```python
import torch

def _softmax(x, axis=-1):
    return torch.softmax(x, dim=axis)
def _gelu(x):
    return torch.nn.functional.gelu(x, approximate="tanh")
def _silu(x):
    return torch.nn.functional.silu(x)
def _sigmoid(x):
    return torch.sigmoid(x)
def _layernorm(x, axis=-1, eps=1e-5):
    m = x.mean(dim=axis, keepdim=True)
    v = x.var(dim=axis, keepdim=True, unbiased=False)
    return (x - m) / torch.sqrt(v + eps)
def _scatter_add(tgt, idx, src, axis=0):
    return tgt.clone().index_add_(axis, idx, src)
def _segment_sum(src, seg, n):
    out = torch.zeros((n,) + src.shape[1:], dtype=src.dtype)
    return out.index_add_(0, seg, src)
def _pad(x, pw):
    flat = tuple(v for pair in reversed(pw) for v in pair)
    return torch.nn.functional.pad(x, flat)

kernel_name = 'concat_heads__mha_block_cached__mha_block_cached__near_ridge'

def setup_inputs():

    _w = (lambda a: (lambda t: t.to(torch.bfloat16) if t.is_floating_point() else t)(torch.as_tensor(a)))
    def _A_setup():
        B, Sq, D = 16, 224, 640
        H, Sk, Dh = 32, 1024, 128
        return {
            "x": _w((lambda *s: torch.randn(*s, dtype=torch.bfloat16) * 0.8)(B, Sq, D)),
            "k_cache": _w((lambda *s: torch.randn(*s, dtype=torch.bfloat16) * 0.8)(B, H, Sk, Dh)),
            "v_cache": _w((lambda *s: torch.randn(*s, dtype=torch.bfloat16) * 0.05)(B, H, Sk, Dh)),
            "wq": _w((lambda *s: torch.randn(*s, dtype=torch.bfloat16) * 0.8)(D, H * Dh) * (D ** -0.5)),
            "wo": _w((lambda *s: torch.randn(*s, dtype=torch.bfloat16) * 0.05)(H * Dh, D) * ((H * Dh) ** -0.5)),
        }
    _A_in = _A_setup()

    def _B_setup():
        B, Sq, D = 16, 224, 640
        H, Sk, Dh = 32, 1024, 128
        return {
            "x": _w((lambda *s: torch.randn(*s, dtype=torch.bfloat16) * 0.8)(B, Sq, D)),
            "k_cache": _w((lambda *s: torch.randn(*s, dtype=torch.bfloat16) * 0.8)(B, H, Sk, Dh)),
            "v_cache": _w((lambda *s: torch.randn(*s, dtype=torch.bfloat16) * 0.05)(B, H, Sk, Dh)),
            "wq": _w((lambda *s: torch.randn(*s, dtype=torch.bfloat16) * 0.8)(D, H * Dh) * (D ** -0.5)),
            "wo": _w((lambda *s: torch.randn(*s, dtype=torch.bfloat16) * 0.05)(H * Dh, D) * ((H * Dh) ** -0.5)),
        }
    _B_in = _B_setup()

    return {
        "A_x": _A_in["x"],
        "A_k_cache": _A_in["k_cache"],
        "A_v_cache": _A_in["v_cache"],
        "A_wq": _A_in["wq"],
        "A_wo": _A_in["wo"],
        "B_x": _B_in["x"],
        "B_k_cache": _B_in["k_cache"],
        "B_v_cache": _B_in["v_cache"],
        "B_wq": _B_in["wq"],
        "B_wo": _B_in["wo"],
    }

def reference(A_x, A_k_cache, A_v_cache, A_wq, A_wo, B_x, B_k_cache, B_v_cache, B_wq, B_wo):
    def _A_ref(x, k_cache, v_cache, wq, wo):
        B, Sq, D = x.shape
        H, Dh = 32, 128
        q = (lambda x, *d: x.permute(*d))((x @ wq).reshape(B, Sq, H, Dh), 0, 2, 1, 3)
        scores = torch.einsum("bhqd,bhkd->bhqk", q, k_cache) * (Dh ** -0.5)
        p = _softmax(scores, axis=-1)
        o = torch.einsum("bhqk,bhkd->bhqd", p, v_cache)
        return (lambda x, *d: x.permute(*d))(o, 0, 2, 1, 3).reshape(B, Sq, H * Dh) @ wo

    def _B_ref(x, k_cache, v_cache, wq, wo):
        B, Sq, D = x.shape
        H, Dh = 32, 128
        q = (lambda x, *d: x.permute(*d))((x @ wq).reshape(B, Sq, H, Dh), 0, 2, 1, 3)
        scores = torch.einsum("bhqd,bhkd->bhqk", q, k_cache) * (Dh ** -0.5)
        p = _softmax(scores, axis=-1)
        o = torch.einsum("bhqk,bhkd->bhqd", p, v_cache)
        return (lambda x, *d: x.permute(*d))(o, 0, 2, 1, 3).reshape(B, Sq, H * Dh) @ wo

    a = _A_ref(A_x, A_k_cache, A_v_cache, A_wq, A_wo)
    b = _B_ref(B_x, B_k_cache, B_v_cache, B_wq, B_wo)
    return torch.concatenate([a, b], axis=-1)
```

```cpp
#include <hip/hip_runtime.h>
#include <hip/hip_bf16.h>
#include <stdint.h>

__global__ __launch_bounds__(256, 1)
void fused_attn(const char* xA, const char* kA, const char* vA, const char* wA, const char* uA,
                const char* xB, const char* kB, const char* vB, const char* wB, const char* uB, char* xch, char* out, char* flags) {
  extern __shared__ __attribute__((aligned(16))) char lds[];
  const unsigned wid = __builtin_amdgcn_readfirstlane(threadIdx.x >> 6);
  const unsigned long long ka = (unsigned long long)__builtin_amdgcn_kernarg_segment_ptr();
  asm volatile(
"s_memtime s[64:65]\n"
"s_memrealtime s[66:67]\n"
"s_bfe_u32 s10, %[wg], 0x10002\n"
"s_mul_i32 s40, s10, 40\n"
"s_add_u32 s40, s40, %[kalo]\n"
"s_addc_u32 s41, %[kahi], 0\n"
"s_mov_b32 s78, %[kalo]\n"
"s_mov_b32 s79, %[kahi]\n"
"s_load_dwordx2 s[24:25], s[40:41], 0\n"
"s_load_dwordx2 s[28:29], s[40:41], 24\n"
"s_load_dwordx2 s[6:7], s[78:79], 0x60\n"
"s_load_dwordx2 s[16:17], s[40:41], 8\n"
"s_load_dwordx2 s[20:21], s[40:41], 16\n"
"s_load_dwordx2 s[8:9], s[40:41], 32\n"
"s_load_dwordx4 s[12:15], s[78:79], 0x50\n"
"s_mov_b32 s42, %[wid]\n"
"s_and_b32 s11, %[wg], 1\n"
"s_lshl_b32 s11, s11, 3\n"
"s_lshr_b32 s60, %[wg], 5\n"
"s_add_u32 s11, s11, s60\n"
"s_bfe_u32 s61, %[wg], 0x10001\n"
"s_bfe_u32 s60, %[wg], 0x20003\n"
"s_lshl2_add_u32 s61, s61, s60\n"
"s_getpc_b64 s[80:81]\n"
"LCP_%=:\n"
"s_mov_b32 s82, LEND_%=-LCP_%=\n"
"s_mov_b32 s83, 0x20000\n"
"v_mbcnt_lo_u32_b32 v2, -1, 0\n"
"v_lshlrev_b32 v2, 7, v2\n"
"s_lshr_b32 s60, %[wg], 3\n"
"s_lshl2_add_u32 s60, s60, s42\n"
"s_mul_i32 s60, s60, 640\n"
"s_mov_b64 exec, 31\n"
"buffer_load_dword v3, v2, s[80:83], s60 offen\n"
"s_mov_b64 exec, -1\n"
"s_mov_b32 s19, 0x20000\n"
"s_mov_b32 s23, 0x20000\n"
"s_mov_b32 s27, 0x20000\n"
"s_mov_b32 s31, 0x20000\n"
"s_mov_b32 s35, 0x20000\n"
"s_mov_b32 s39, 0x20000\n"
"s_mov_b32 s26, 286720\n"
"s_lshl_b32 s44, s42, 12\n"
"s_mov_b32 s50, s44\n"
"s_mul_i32 s96, s42, 7168\n"
"s_add_u32 s51, s96, 0x10000\n"
"s_mov_b32 s74, 0x8000\n"
"s_mov_b32 s58, 524288\n"
"s_mov_b32 s47, 0\n"
"s_add_u32 s60, s11, s61\n"
"s_and_b32 s60, s60, 3\n"
"s_mul_i32 s60, s60, 3\n"
"s_lshl_b32 s49, s60, 7\n"
"s_lshl_b32 s48, s60, 19\n"
"v_mbcnt_lo_u32_b32 v4, -1, 0\n"
"v_mbcnt_hi_u32_b32 v4, -1, v4\n"
"v_and_b32 v5, 15, v4\n"
"v_lshrrev_b32 v6, 4, v4\n"
"v_lshrrev_b32 v7, 3, v4\n"
"v_and_b32 v8, 7, v4\n"
"v_lshlrev_b32 v9, 1, v6\n"
"v_xor_b32 v9, v9, v8\n"
"v_lshlrev_b32 v9, 4, v9\n"
"s_mul_i32 s60, s42, 71680\n"
"v_mul_u32_u24 v10, 1280, v7\n"
"v_add3_u32 v247, v10, v9, s60\n"
"v_add_u32 v253, 9216, v247\n"
"v_add_u32 v254, 18432, v247\n"
"v_add_u32 v255, 27648, v247\n"
"v_lshl_add_u32 v240, v7, 8, v9\n"
"v_add_u32 v240, s44, v240\n"
"v_and_b32 v10, 1, v6\n"
"v_xor_b32 v10, v10, v8\n"
"v_lshlrev_b32 v10, 4, v10\n"
"v_lshl_add_u32 v10, v7, 13, v10\n"
"s_lshl_b32 s60, s42, 17\n"
"v_add_u32 v10, s60, v10\n"
"v_add_u32 v224, 1024, v10\n"
"v_add_u32 v225, 128, v10\n"
"v_xor_b32 v10, 64, v10\n"
"v_add_u32 v226, 64512, v10\n"
"v_add_u32 v227, 63616, v10\n"
"s_mul_i32 s40, s11, 286720\n"
"s_lshl_b32 s41, s61, 10\n"
"s_sub_u32 s30, 5243904, s41\n"
"s_sub_u32 s41, s41, 1024\n"
"s_ashr_i32 s60, s41, 31\n"
"s_lshl4_add_u32 s92, s10, s11\n"
"s_lshl_b32 s93, s92, 8\n"
"v_mov_b32 v0, 0\n"
"s_waitcnt lgkmcnt(0)\n"
"s_add_u32 s24, s24, s40\n"
"s_addc_u32 s25, s25, 0\n"
"s_and_b32 s25, s25, 0xffff\n"
"s_add_u32 s28, s28, s41\n"
"s_addc_u32 s29, s29, s60\n"
"s_and_b32 s29, s29, 0xffff\n"
"s_add_u32 s6, s6, s93\n"
"s_addc_u32 s7, s7, 0\n"
"global_load_dword v1, v0, s[6:7] offset:128 sc1\n"
"s_mov_b32 m0, s51\n"
"s_nop 0\n"
"buffer_load_dwordx4 v247, s[24:27], s49 offen lds\n"
"buffer_load_dwordx4 v253, s[24:27], s49 offen offset:1024 lds\n"
"buffer_load_dwordx4 v254, s[24:27], s49 offen offset:2048 lds\n"
"buffer_load_dwordx4 v255, s[24:27], s49 offen offset:3072 lds\n"
"s_add_u32 m0, s51, 4096\n"
"s_add_u32 s40, s49, 40960\n"
"buffer_load_dwordx4 v247, s[24:27], s40 offen lds\n"
"buffer_load_dwordx4 v253, s[24:27], s40 offen offset:1024 lds\n"
"buffer_load_dwordx4 v254, s[24:27], s40 offen offset:2048 lds\n"
"s_add_u32 s49, s49, 128\n"
"s_sub_u32 s41, s49, 1280\n"
"s_min_u32 s49, s49, s41\n"
"s_xor_b32 s51, s51, s74\n"
"s_mov_b32 m0, s50\n"
"s_nop 0\n"
"buffer_load_dwordx4 v224, s[28:31], s48 offen lds\n"
"buffer_load_dwordx4 v225, s[28:31], s48 offen offset:1024 lds\n"
"buffer_load_dwordx4 v226, s[28:31], s48 offen offset:2048 lds\n"
"buffer_load_dwordx4 v227, s[28:31], s48 offen offset:3072 lds\n"
"s_xor_b32 s50, s50, s74\n"
"s_xor_b32 m0, s50, 0xc000\n"
"s_add_u32 s40, s48, 256\n"
"buffer_load_dwordx4 v224, s[28:31], s40 offen lds\n"
"buffer_load_dwordx4 v225, s[28:31], s40 offen offset:1024 lds\n"
"buffer_load_dwordx4 v226, s[28:31], s40 offen offset:2048 lds\n"
"buffer_load_dwordx4 v227, s[28:31], s40 offen offset:3072 lds\n"
"s_cmp_eq_u32 s47, 8\ns_cselect_b32 s41, 524800, s58\n"
"s_add_u32 s48, s48, s41\n"
"s_sub_u32 s41, s48, 5242880\n"
"s_min_u32 s48, s48, s41\n"
"s_mov_b32 m0, s50\n"
"s_nop 0\n"
"buffer_load_dwordx4 v224, s[28:31], s48 offen lds\n"
"buffer_load_dwordx4 v225, s[28:31], s48 offen offset:1024 lds\n"
"buffer_load_dwordx4 v226, s[28:31], s48 offen offset:2048 lds\n"
"buffer_load_dwordx4 v227, s[28:31], s48 offen offset:3072 lds\n"
"s_xor_b32 s50, s50, s74\n"
"s_mov_b32 m0, s51\n"
"s_nop 0\n"
"buffer_load_dwordx4 v247, s[24:27], s49 offen lds\n"
"buffer_load_dwordx4 v253, s[24:27], s49 offen offset:1024 lds\n"
"buffer_load_dwordx4 v254, s[24:27], s49 offen offset:2048 lds\n"
"buffer_load_dwordx4 v255, s[24:27], s49 offen offset:3072 lds\n"
"s_add_u32 m0, s51, 4096\n"
"s_add_u32 s40, s49, 40960\n"
"buffer_load_dwordx4 v247, s[24:27], s40 offen lds\n"
"buffer_load_dwordx4 v253, s[24:27], s40 offen offset:1024 lds\n"
"buffer_load_dwordx4 v254, s[24:27], s40 offen offset:2048 lds\n"
"s_add_u32 s49, s49, 128\n"
"s_sub_u32 s41, s49, 1280\n"
"s_min_u32 s49, s49, s41\n"
"s_xor_b32 s51, s51, s74\n"
"s_and_b32 s40, s42, 1\n"
"s_lshl_b32 s43, s40, 6\n"
"s_mov_b32 s77, 0x80000000\n"
"s_mov_b32 s75, 0x10000\n"
"s_mov_b32 s73, 0xffff0000\n"
"s_mov_b32 s45, 0x3e0293ee\n"
"s_mov_b32 s46, 0x5d800000\n"
"s_mov_b32 s62, 9\n"
"v_lshl_add_u32 v242, v4, 4, s44\n"
"s_lshr_b32 s41, s42, 1\n"
"s_lshl4_add_u32 s76, s41, s40\n"
"s_lshl_b32 s76, s76, 10\n"
"s_add_u32 s76, s76, 0x20000\n"
"s_sub_u32 s76, s76, s44\n"
"v_add_u32 v248, s76, v242\n"
"v_and_b32 v9, 6, v5\n"
"v_xor_b32 v9, v9, v6\n"
"v_lshlrev_b32 v9, 4, v9\n"
"v_lshl_add_u32 v10, v5, 7, v9\n"
"s_mul_i32 s41, s42, 6144\n"
"s_add_u32 s41, s41, 0x10000\n"
"v_add_u32 v249, s41, v10\n"
"v_xor_b32 v250, 64, v249\n"
"s_lshr_b32 s41, s42, 1\n"
"s_lshl_b32 s41, s41, 11\n"
"s_add_u32 s41, s41, 90112\n"
"v_add_u32 v251, s41, v10\n"
"v_xor_b32 v252, 64, v251\n"
"v_and_b32 v11, 8, v5\n"
"v_lshl_add_u32 v10, v11, 7, v10\n"
"s_mul_i32 s41, s40, 8256\n"
"v_xor_b32 v228, s41, v10\n"
"v_xor_b32 v229, 64, v228\n"
"v_xor_b32 v230, 0x2000, v228\n"
"v_xor_b32 v231, 0x2000, v229\n"
"v_lshrrev_b32 v9, 5, v4\n"
"v_lshlrev_b32 v9, 11, v9\n"
"v_bfe_u32 v10, v4, 2, 3\n"
"v_lshl_add_u32 v9, v10, 7, v9\n"
"v_and_b32 v10, 3, v4\n"
"v_lshl_add_u32 v9, v10, 3, v9\n"
"v_bfe_u32 v10, v4, 3, 2\n"
"v_lshl_add_u32 v9, v10, 5, v9\n"
"s_add_u32 s41, s41, 0x10000\n"
"v_xor_b32 v232, s41, v9\n"
"v_xor_b32 v233, 32, v232\n"
"v_xor_b32 v234, 64, v232\n"
"v_xor_b32 v235, 96, v232\n"
"v_xor_b32 v236, 0x2000, v232\n"
"v_xor_b32 v237, 0x2000, v233\n"
"v_xor_b32 v238, 0x2000, v234\n"
"v_xor_b32 v239, 0x2000, v235\n"
"v_lshlrev_b32 v9, 11, v6\n"
"v_bfe_u32 v10, v4, 2, 2\n"
"v_lshl_add_u32 v9, v10, 7, v9\n"
"v_and_b32 v10, 1, v4\n"
"v_lshl_add_u32 v9, v10, 3, v9\n"
"v_bfe_u32 v10, v4, 1, 1\n"
"v_lshl_add_u32 v9, v10, 5, v9\n"
"v_bfe_u32 v10, v4, 3, 1\n"
"v_lshl_add_u32 v9, v10, 4, v9\n"
"v_bfe_u32 v10, v4, 4, 1\n"
"v_lshl_add_u32 v9, v10, 6, v9\n"
"v_xor_b32 v243, s43, v9\n"
"v_xor_b32 v244, 16, v243\n"
"v_xor_b32 v245, 64, v243\n"
"v_xor_b32 v246, 0x50, v243\n"
"s_lshl3_add_u32 s41, s11, s61\n"
"s_lshl_b32 s41, s41, 20\n"
"s_sub_u32 s41, s41, 896\n"
"s_ashr_i32 s60, s41, 31\n"
"s_add_u32 s16, s16, s41\n"
"s_addc_u32 s17, s17, s60\n"
"s_and_b32 s17, s17, 0xffff\n"
"s_mov_b32 s18, 1049472\n"
"s_mov_b64 s[84:85], s[16:17]\n"
"s_mov_b64 s[86:87], s[18:19]\n"
"s_add_u32 s20, s20, s41\n"
"s_addc_u32 s21, s21, s60\n"
"s_and_b32 s21, s21, 0xffff\n"
"s_mov_b32 s22, 1049472\n"
"s_mov_b64 s[88:89], s[20:21]\n"
"s_mov_b64 s[90:91], s[22:23]\n"
"s_lshr_b32 s57, %[wg], 3\n"
"s_mul_i32 s57, s57, 5\n"
"s_and_b32 s57, s57, 15\n"
"s_lshl_b32 s57, s57, 14\n"
"s_mov_b32 s55, 896\n"
"s_add_u32 s56, s57, 896\n"
"s_mov_b32 s59, 16384\n"
"s_mov_b32 s52, -896\n"
"s_mov_b32 s53, 0\n"
"s_mov_b32 s54, -896\n"
"s_mul_i32 s41, s61, 655360\n"
"s_add_u32 s8, s8, s41\n"
"s_addc_u32 s9, s9, 0\n"
"s_and_b32 s9, s9, 0xffff\n"
"s_mul_i32 s41, s92, 2293760\n"
"s_add_u32 s32, s12, s41\n"
"s_addc_u32 s33, s13, 0\n"
"s_and_b32 s33, s33, 0xffff\n"
"s_mov_b32 s34, 2293760\n"
"s_mul_i32 s41, s11, 573440\n"
"s_mul_i32 s60, s10, 1280\n"
"s_sub_u32 s38, 573440, s60\n"
"s_add_u32 s41, s41, s60\n"
"s_add_u32 s36, s14, s41\n"
"s_addc_u32 s37, s15, 0\n"
"s_and_b32 s37, s37, 0xffff\n"
"s_lshl2_add_u32 s93, s61, s42\n"
"s_mul_i32 s94, s61, 7168\n"
"s_lshl_b32 s41, s42, 11\n"
"s_add_u32 s94, s94, s41\n"
"s_add_u32 s94, s94, 229376\n"
"s_add_u32 s95, s94, 1024\n"
"s_lshl_b32 s60, s61, 7\n"
"s_add_u32 s60, s60, s43\n"
"s_mul_i32 s97, s42, 122880\n"
"s_add_u32 s97, s97, s60\n"
"s_lshr_b32 s98, s42, 1\n"
"s_add_u32 s98, s98, 12\n"
"s_mul_i32 s98, s98, 40960\n"
"s_add_u32 s98, s98, s60\n"
"s_and_b32 s41, s61, 3\n"
"s_mul_i32 s99, s41, 3\n"
"s_add_u32 s99, s99, s42\n"
"s_lshr_b32 s41, s41, 1\n"
"s_add_u32 s41, s41, 12\n"
"s_cmp_eq_u32 s42, 3\n"
"s_cselect_b32 s99, s41, s99\n"
"s_mul_i32 s99, s99, 40960\n"
"s_lshr_b32 s41, s61, 2\n"
"s_lshl_b32 s41, s41, 7\n"
"s_add_u32 s99, s99, s41\n"
"s_and_b32 s41, s61, 1\n"
"s_lshl_b32 s41, s41, 6\n"
"s_add_u32 s99, s99, s41\n"
"s_add_u32 s99, s99, 1024\n"
"s_xor_b32 s100, s99, 64\n"
"s_cmp_eq_u32 s42, 3\n"
"s_cselect_b32 s100, 0x40000000, s100\n"
"s_cselect_b32 s95, 0x40000000, s95\n"
"v_mov_b64 v[4:5], 0\n"
"v_mov_b64 v[6:7], 0\n"
"v_mov_b64 v[8:9], 0\n"
"v_mov_b64 v[10:11], 0\n"
"v_mov_b64 v[12:13], 0\n"
"v_mov_b64 v[14:15], 0\n"
"v_mov_b64 v[16:17], 0\n"
"v_mov_b64 v[18:19], 0\n"
"v_mov_b64 v[20:21], 0\n"
"v_mov_b64 v[22:23], 0\n"
"v_mov_b64 v[24:25], 0\n"
"v_mov_b64 v[26:27], 0\n"
"v_mov_b64 v[28:29], 0\n"
"v_mov_b64 v[30:31], 0\n"
"v_mov_b64 v[32:33], 0\n"
"v_mov_b64 v[34:35], 0\n"
"v_mov_b64 v[36:37], 0\n"
"v_mov_b64 v[38:39], 0\n"
"v_mov_b64 v[40:41], 0\n"
"v_mov_b64 v[42:43], 0\n"
"v_mov_b64 v[44:45], 0\n"
"v_mov_b64 v[46:47], 0\n"
"v_mov_b64 v[48:49], 0\n"
"v_mov_b64 v[50:51], 0\n"
"v_mov_b64 v[52:53], 0\n"
"v_mov_b64 v[54:55], 0\n"
"v_mov_b64 v[56:57], 0\n"
"v_mov_b64 v[58:59], 0\n"
"v_mov_b64 v[60:61], 0\n"
"v_mov_b64 v[62:63], 0\n"
"v_mov_b64 v[64:65], 0\n"
"v_mov_b64 v[66:67], 0\n"
"v_mov_b64 v[68:69], 0\n"
"v_mov_b64 v[70:71], 0\n"
"v_mov_b64 v[72:73], 0\n"
"v_mov_b64 v[74:75], 0\n"
"v_mov_b64 v[76:77], 0\n"
"v_mov_b64 v[78:79], 0\n"
"v_mov_b64 v[80:81], 0\n"
"v_mov_b64 v[82:83], 0\n"
"v_mov_b64 v[84:85], 0\n"
"v_mov_b64 v[86:87], 0\n"
"v_mov_b64 v[88:89], 0\n"
"v_mov_b64 v[90:91], 0\n"
"v_mov_b64 v[92:93], 0\n"
"v_mov_b64 v[94:95], 0\n"
"v_mov_b64 v[96:97], 0\n"
"v_mov_b64 v[98:99], 0\n"
"v_mov_b64 v[100:101], 0\n"
"v_mov_b64 v[102:103], 0\n"
"v_mov_b64 v[104:105], 0\n"
"v_mov_b64 v[106:107], 0\n"
"v_mov_b64 v[108:109], 0\n"
"v_mov_b64 v[110:111], 0\n"
"v_mov_b64 v[112:113], 0\n"
"v_mov_b64 v[114:115], 0\n"
"v_mov_b64 v[116:117], 0\n"
"v_mov_b64 v[118:119], 0\n"
"v_mov_b64 v[120:121], 0\n"
"v_mov_b64 v[122:123], 0\n"
"v_mov_b64 v[124:125], 0\n"
"v_mov_b64 v[126:127], 0\n"
"v_mov_b64 v[128:129], 0\n"
"v_mov_b64 v[130:131], 0\n"
"v_mov_b64 v[132:133], 0\n"
"v_mov_b64 v[134:135], 0\n"
"v_mov_b64 v[136:137], 0\n"
"v_mov_b64 v[138:139], 0\n"
"v_mov_b64 v[140:141], 0\n"
"v_mov_b64 v[142:143], 0\n"
"v_mov_b64 v[144:145], 0\n"
"v_mov_b64 v[146:147], 0\n"
"v_mov_b64 v[148:149], 0\n"
"v_mov_b64 v[150:151], 0\n"
"v_mov_b64 v[152:153], 0\n"
"v_mov_b64 v[154:155], 0\n"
"v_mov_b64 v[156:157], 0\n"
"v_mov_b64 v[158:159], 0\n"
"v_mov_b64 v[160:161], 0\n"
"v_mov_b64 v[162:163], 0\n"
"v_mov_b64 v[164:165], 0\n"
"v_mov_b64 v[166:167], 0\n"
"v_mov_b64 v[168:169], 0\n"
"v_mov_b64 v[170:171], 0\n"
"v_mov_b64 v[172:173], 0\n"
"v_mov_b64 v[174:175], 0\n"
"v_mov_b64 v[176:177], 0\n"
"v_mov_b64 v[178:179], 0\n"
"v_mov_b64 v[180:181], 0\n"
"v_mov_b64 v[182:183], 0\n"
"v_mov_b64 v[184:185], 0\n"
"v_mov_b64 v[186:187], 0\n"
"v_mov_b64 v[188:189], 0\n"
"v_mov_b64 v[190:191], 0\n"
"v_mov_b64 v[192:193], 0\n"
"v_mov_b64 v[194:195], 0\n"
"v_mov_b64 v[196:197], 0\n"
"v_mov_b64 v[198:199], 0\n"
"v_mov_b64 v[200:201], 0\n"
"v_mov_b64 v[202:203], 0\n"
"v_mov_b64 v[204:205], 0\n"
"v_mov_b64 v[206:207], 0\n"
"v_mov_b64 v[208:209], 0\n"
"v_mov_b64 v[210:211], 0\n"
"v_mov_b64 v[212:213], 0\n"
"v_mov_b64 v[214:215], 0\n"
"v_mov_b64 v[216:217], 0\n"
"v_mov_b64 v[218:219], 0\n"
"v_mov_b64 v[220:221], 0\n"
"v_mov_b64 v[222:223], 0\n"
"s_waitcnt vmcnt(26)\n"
"v_readfirstlane_b32 s92, v1\n"
"v_mov_b64 v[0:1], 0\n"
"v_mov_b64 v[2:3], 0\n"
"s_nop 2\n"
"s_mul_i32 s60, s92, 0x9e3779b1\n"
"s_add_u32 s60, s60, 0x7f4a7c15\n"
"s_lshl2_add_u32 s63, s11, s42\n"
"s_mul_i32 s63, s63, 10240\n"
"s_waitcnt vmcnt(19)\n"
"s_memtime s[80:81]\n"
"s_memrealtime s[82:83]\n"
"s_waitcnt lgkmcnt(0)\n"
"s_sub_u32 s80, s80, s64\n"
"s_sub_u32 s82, s82, s66\n"
"s_mul_i32 s80, s80, 10\n"
"s_mul_i32 s82, s82, 211\n"
"s_cmp_gt_u32 s80, s82\n"
"s_cselect_b32 s64, 1, 0\n"
"s_cselect_b32 s68, 655360, 0\n"
"s_cmp_eq_u32 s42, 0\n"
"s_cselect_b32 s64, s64, 0\n"
"s_mov_b32 s65, 0\n"
"s_add_u32 s66, s66, 2800\n"
"s_mov_b32 s67, s66\n"
"s_mov_b64 vcc, s[64:65]\n"
"s_waitcnt vmcnt(15)\n"
"s_barrier\n"
"ds_read_b128 a[160:163], v249 offset:0\n"
"ds_read_b128 a[164:167], v250 offset:0\n"
"ds_read_b64_tr_b16 a[96:97], v243 offset:0\n"
"ds_read_b64_tr_b16 a[98:99], v243 offset:512\n"
"ds_read_b64_tr_b16 a[100:101], v243 offset:8192\n"
"ds_read_b64_tr_b16 a[102:103], v243 offset:8704\n"
"ds_read_b64_tr_b16 a[104:105], v244 offset:0\n"
"ds_read_b64_tr_b16 a[106:107], v244 offset:512\n"
"ds_read_b64_tr_b16 a[108:109], v244 offset:8192\n"
"ds_read_b64_tr_b16 a[110:111], v244 offset:8704\n"
"ds_read_b64_tr_b16 a[112:113], v245 offset:0\n"
"ds_read_b64_tr_b16 a[114:115], v245 offset:512\n"
"ds_read_b64_tr_b16 a[116:117], v245 offset:8192\n"
"ds_read_b64_tr_b16 a[118:119], v245 offset:8704\n"
"ds_read_b64_tr_b16 a[120:121], v246 offset:0\n"
"ds_read_b64_tr_b16 a[122:123], v246 offset:512\n"
"ds_read_b64_tr_b16 a[124:125], v246 offset:8192\n"
"ds_read_b64_tr_b16 a[126:127], v246 offset:8704\n"
"ds_read_b64_tr_b16 a[128:129], v243 offset:1024\n"
"ds_read_b64_tr_b16 a[130:131], v243 offset:1536\n"
"ds_read_b64_tr_b16 a[132:133], v243 offset:9216\n"
"ds_read_b64_tr_b16 a[134:135], v243 offset:9728\n"
"ds_read_b64_tr_b16 a[136:137], v244 offset:1024\n"
"ds_read_b64_tr_b16 a[138:139], v244 offset:1536\n"
"ds_read_b64_tr_b16 a[140:141], v244 offset:9216\n"
"ds_read_b64_tr_b16 a[142:143], v244 offset:9728\n"
"ds_read_b64_tr_b16 a[144:145], v245 offset:1024\n"
"ds_read_b64_tr_b16 a[146:147], v245 offset:1536\n"
"ds_read_b64_tr_b16 a[148:149], v245 offset:9216\n"
"ds_read_b64_tr_b16 a[150:151], v245 offset:9728\n"
"ds_read_b64_tr_b16 a[152:153], v246 offset:1024\n"
"ds_read_b64_tr_b16 a[154:155], v246 offset:1536\n"
"ds_read_b64_tr_b16 a[156:157], v246 offset:9216\n"
"ds_read_b64_tr_b16 a[158:159], v246 offset:9728\n"
"ds_read_b128 a[168:171], v249 offset:2048\n"
"ds_read_b128 a[172:175], v250 offset:2048\n"
"ds_read_b128 a[176:179], v249 offset:4096\n"
"ds_read_b128 a[180:183], v250 offset:4096\n"
"ds_read_b128 a[184:187], v251 offset:0\n"
"ds_read_b128 a[188:191], v252 offset:0\n"
"LPL_%=:\n"
"s_waitcnt lgkmcnt(2)\n"
"v_mfma_f32_16x16x32_bf16 v[112:115], a[96:99], a[160:163], v[112:115]\n"
"v_mfma_f32_16x16x32_bf16 v[112:115], a[100:103], a[164:167], v[112:115]\n"
"v_mfma_f32_16x16x32_bf16 v[116:119], a[104:107], a[160:163], v[116:119]\n"
"v_mfma_f32_16x16x32_bf16 v[116:119], a[108:111], a[164:167], v[116:119]\n"
"s_waitcnt vmcnt(11) lgkmcnt(6)\n"
"v_mfma_f32_16x16x32_bf16 v[120:123], a[112:115], a[160:163], v[120:123]\n"
"s_barrier\n"
"ds_read_b64_tr_b16 a[192:193], v243 offset:16384\n"
"v_mfma_f32_16x16x32_bf16 v[120:123], a[116:119], a[164:167], v[120:123]\n"
"s_xor_b32 m0, s50, 0xc000\n"
"ds_read_b64_tr_b16 a[194:195], v243 offset:16896\n"
"v_mfma_f32_16x16x32_bf16 v[124:127], a[120:123], a[160:163], v[124:127]\n"
"s_add_u32 s40, s48, 256\n"
"buffer_load_dwordx4 v224, s[28:31], s40 offen lds\n"
"v_mfma_f32_16x16x32_bf16 v[124:127], a[124:127], a[164:167], v[124:127]\n"
"ds_read_b64_tr_b16 a[196:197], v243 offset:24576\n"
"v_mfma_f32_16x16x32_bf16 v[128:131], a[128:131], a[160:163], v[128:131]\n"
"ds_read_b64_tr_b16 a[198:199], v243 offset:25088\n"
"v_mfma_f32_16x16x32_bf16 v[128:131], a[132:135], a[164:167], v[128:131]\n"
"ds_read_b64_tr_b16 a[200:201], v244 offset:16384\n"
"v_mfma_f32_16x16x32_bf16 v[132:135], a[136:139], a[160:163], v[132:135]\n"
"ds_read_b64_tr_b16 a[202:203], v244 offset:16896\n"
"v_mfma_f32_16x16x32_bf16 v[132:135], a[140:143], a[164:167], v[132:135]\n"
"buffer_load_dwordx4 v225, s[28:31], s40 offen offset:1024 lds\n"
"v_mfma_f32_16x16x32_bf16 v[136:139], a[144:147], a[160:163], v[136:139]\n"
"ds_read_b64_tr_b16 a[204:205], v244 offset:24576\n"
"v_mfma_f32_16x16x32_bf16 v[136:139], a[148:151], a[164:167], v[136:139]\n"
"ds_read_b64_tr_b16 a[206:207], v244 offset:25088\n"
"v_mfma_f32_16x16x32_bf16 v[140:143], a[152:155], a[160:163], v[140:143]\n"
"ds_read_b64_tr_b16 a[208:209], v245 offset:16384\n"
"v_mfma_f32_16x16x32_bf16 v[140:143], a[156:159], a[164:167], v[140:143]\n"
"s_waitcnt lgkmcnt(3)\n"
"ds_read_b64_tr_b16 a[210:211], v245 offset:16896\n"
"v_mfma_f32_16x16x32_bf16 v[172:175], a[152:155], a[168:171], v[172:175]\n"
"s_waitcnt lgkmcnt(4)\n"
"buffer_load_dwordx4 v226, s[28:31], s40 offen offset:2048 lds\n"
"v_mfma_f32_16x16x32_bf16 v[172:175], a[156:159], a[172:175], v[172:175]\n"
"ds_read_b64_tr_b16 a[212:213], v245 offset:24576\n"
"v_mfma_f32_16x16x32_bf16 v[168:171], a[144:147], a[168:171], v[168:171]\n"
"ds_read_b64_tr_b16 a[214:215], v245 offset:25088\n"
"v_mfma_f32_16x16x32_bf16 v[168:171], a[148:151], a[172:175], v[168:171]\n"
"ds_read_b64_tr_b16 a[216:217], v246 offset:16384\n"
"v_mfma_f32_16x16x32_bf16 v[164:167], a[136:139], a[168:171], v[164:167]\n"
"ds_read_b64_tr_b16 a[218:219], v246 offset:16896\n"
"v_mfma_f32_16x16x32_bf16 v[164:167], a[140:143], a[172:175], v[164:167]\n"
"buffer_load_dwordx4 v227, s[28:31], s40 offen offset:3072 lds\n"
"v_mfma_f32_16x16x32_bf16 v[160:163], a[128:131], a[168:171], v[160:163]\n"
"s_cmp_eq_u32 s47, 8\ns_cselect_b32 s41, 524800, s58\n"
"ds_read_b64_tr_b16 a[220:221], v246 offset:24576\n"
"v_mfma_f32_16x16x32_bf16 v[160:163], a[132:135], a[172:175], v[160:163]\n"
"s_add_u32 s48, s48, s41\n"
"ds_read_b64_tr_b16 a[222:223], v246 offset:25088\n"
"v_mfma_f32_16x16x32_bf16 v[156:159], a[120:123], a[168:171], v[156:159]\n"
"s_sub_u32 s41, s48, 5242880\n"
"ds_read_b64_tr_b16 a[224:225], v243 offset:17408\n"
"v_mfma_f32_16x16x32_bf16 v[156:159], a[124:127], a[172:175], v[156:159]\n"
"s_min_u32 s48, s48, s41\n"
"ds_read_b64_tr_b16 a[226:227], v243 offset:17920\n"
"v_mfma_f32_16x16x32_bf16 v[152:155], a[112:115], a[168:171], v[152:155]\n"
"s_mov_b32 m0, s50\n"
"ds_read_b64_tr_b16 a[228:229], v243 offset:25600\n"
"v_mfma_f32_16x16x32_bf16 v[152:155], a[116:119], a[172:175], v[152:155]\n"
"buffer_load_dwordx4 v224, s[28:31], s48 offen lds\n"
"v_mfma_f32_16x16x32_bf16 v[148:151], a[104:107], a[168:171], v[148:151]\n"
"ds_read_b64_tr_b16 a[230:231], v243 offset:26112\n"
"v_mfma_f32_16x16x32_bf16 v[148:151], a[108:111], a[172:175], v[148:151]\n"
"ds_read_b64_tr_b16 a[232:233], v244 offset:17408\n"
"v_mfma_f32_16x16x32_bf16 v[144:147], a[96:99], a[168:171], v[144:147]\n"
"ds_read_b64_tr_b16 a[234:235], v244 offset:17920\n"
"v_xor_b32 v243, s74, v243\n"
"v_mfma_f32_16x16x32_bf16 v[144:147], a[100:103], a[172:175], v[144:147]\n"
"buffer_load_dwordx4 v225, s[28:31], s48 offen offset:1024 lds\n"
"v_mfma_f32_16x16x32_bf16 v[176:179], a[96:99], a[176:179], v[176:179]\n"
"ds_read_b64_tr_b16 a[236:237], v244 offset:25600\n"
"v_xor_b32 v249, s74, v249\n"
"v_mfma_f32_16x16x32_bf16 v[176:179], a[100:103], a[180:183], v[176:179]\n"
"ds_read_b64_tr_b16 a[238:239], v244 offset:26112\n"
"v_xor_b32 v250, s74, v250\n"
"v_mfma_f32_16x16x32_bf16 v[180:183], a[104:107], a[176:179], v[180:183]\n"
"ds_read_b64_tr_b16 a[240:241], v245 offset:17408\n"
"v_xor_b32 v244, s74, v244\n"
"v_mfma_f32_16x16x32_bf16 v[180:183], a[108:111], a[180:183], v[180:183]\n"
"ds_read_b64_tr_b16 a[242:243], v245 offset:17920\n"
"v_xor_b32 v251, s74, v251\n"
"v_mfma_f32_16x16x32_bf16 v[184:187], a[112:115], a[176:179], v[184:187]\n"
"buffer_load_dwordx4 v226, s[28:31], s48 offen offset:2048 lds\n"
"v_mfma_f32_16x16x32_bf16 v[184:187], a[116:119], a[180:183], v[184:187]\n"
"ds_read_b64_tr_b16 a[244:245], v245 offset:25600\n"
"v_xor_b32 v252, s74, v252\n"
"v_mfma_f32_16x16x32_bf16 v[188:191], a[120:123], a[176:179], v[188:191]\n"
"ds_read_b64_tr_b16 a[246:247], v245 offset:26112\n"
"v_xor_b32 v245, s74, v245\n"
"v_mfma_f32_16x16x32_bf16 v[188:191], a[124:127], a[180:183], v[188:191]\n"
"ds_read_b64_tr_b16 a[248:249], v246 offset:17408\n"
"v_mfma_f32_16x16x32_bf16 v[200:203], a[144:147], a[176:179], v[200:203]\n"
"ds_read_b64_tr_b16 a[250:251], v246 offset:17920\n"
"v_mfma_f32_16x16x32_bf16 v[200:203], a[148:151], a[180:183], v[200:203]\n"
"buffer_load_dwordx4 v227, s[28:31], s48 offen offset:3072 lds\n"
"v_mfma_f32_16x16x32_bf16 v[204:207], a[152:155], a[176:179], v[204:207]\n"
"s_xor_b32 s50, s50, s74\n"
"ds_read_b64_tr_b16 a[252:253], v246 offset:25600\n"
"v_mfma_f32_16x16x32_bf16 v[204:207], a[156:159], a[180:183], v[204:207]\n"
"ds_read_b64_tr_b16 a[254:255], v246 offset:26112\n"
"v_xor_b32 v246, s74, v246\n"
"v_mfma_f32_16x16x32_bf16 v[192:195], a[128:131], a[176:179], v[192:195]\n"
"v_mfma_f32_16x16x32_bf16 v[192:195], a[132:135], a[180:183], v[192:195]\n"
"v_mfma_f32_16x16x32_bf16 v[196:199], a[136:139], a[176:179], v[196:199]\n"
"v_mfma_f32_16x16x32_bf16 v[196:199], a[140:143], a[180:183], v[196:199]\n"
"s_waitcnt lgkmcnt(1)\n"
"v_mfma_f32_16x16x32_bf16 v[220:223], a[136:139], a[184:187], v[220:223]\n"
"v_mfma_f32_16x16x32_bf16 v[220:223], a[140:143], a[188:191], v[220:223]\n"
"v_mfma_f32_16x16x32_bf16 v[216:219], a[128:131], a[184:187], v[216:219]\n"
"v_mfma_f32_16x16x32_bf16 v[216:219], a[132:135], a[188:191], v[216:219]\n"
"v_mfma_f32_16x16x32_bf16 v[212:215], a[104:107], a[184:187], v[212:215]\n"
"v_mfma_f32_16x16x32_bf16 v[212:215], a[108:111], a[188:191], v[212:215]\n"
"v_mfma_f32_16x16x32_bf16 v[208:211], a[96:99], a[184:187], v[208:211]\n"
"v_mfma_f32_16x16x32_bf16 v[208:211], a[100:103], a[188:191], v[208:211]\n"
"v_mfma_f32_16x16x32_bf16 v[0:3], a[192:195], a[160:163], v[0:3]\n"
"v_mfma_f32_16x16x32_bf16 v[0:3], a[196:199], a[164:167], v[0:3]\n"
"v_mfma_f32_16x16x32_bf16 v[4:7], a[200:203], a[160:163], v[4:7]\n"
"s_waitcnt lgkmcnt(0)\n"
"v_mfma_f32_16x16x32_bf16 v[4:7], a[204:207], a[164:167], v[4:7]\n"
"s_waitcnt vmcnt(8) lgkmcnt(0)\n"
"v_mfma_f32_16x16x32_bf16 v[8:11], a[208:211], a[160:163], v[8:11]\n"
"s_barrier\n"
"ds_read_b64_tr_b16 a[96:97], v243 offset:0\n"
"v_mfma_f32_16x16x32_bf16 v[8:11], a[212:215], a[164:167], v[8:11]\n"
"s_mov_b32 m0, s51\n"
"ds_read_b64_tr_b16 a[98:99], v243 offset:512\n"
"v_mfma_f32_16x16x32_bf16 v[12:15], a[216:219], a[160:163], v[12:15]\n"
"buffer_load_dwordx4 v247, s[24:27], s49 offen lds\n"
"v_mfma_f32_16x16x32_bf16 v[12:15], a[220:223], a[164:167], v[12:15]\n"
"ds_read_b64_tr_b16 a[100:101], v243 offset:8192\n"
"v_mfma_f32_16x16x32_bf16 v[16:19], a[224:227], a[160:163], v[16:19]\n"
"ds_read_b64_tr_b16 a[102:103], v243 offset:8704\n"
"v_mfma_f32_16x16x32_bf16 v[16:19], a[228:231], a[164:167], v[16:19]\n"
"ds_read_b64_tr_b16 a[104:105], v244 offset:0\n"
"v_mfma_f32_16x16x32_bf16 v[20:23], a[232:235], a[160:163], v[20:23]\n"
"ds_read_b64_tr_b16 a[106:107], v244 offset:512\n"
"v_mfma_f32_16x16x32_bf16 v[20:23], a[236:239], a[164:167], v[20:23]\n"
"buffer_load_dwordx4 v253, s[24:27], s49 offen offset:1024 lds\n"
"v_mfma_f32_16x16x32_bf16 v[24:27], a[240:243], a[160:163], v[24:27]\n"
"ds_read_b64_tr_b16 a[108:109], v244 offset:8192\n"
"v_mfma_f32_16x16x32_bf16 v[24:27], a[244:247], a[164:167], v[24:27]\n"
"ds_read_b64_tr_b16 a[110:111], v244 offset:8704\n"
"v_mfma_f32_16x16x32_bf16 v[28:31], a[248:251], a[160:163], v[28:31]\n"
"ds_read_b128 a[160:163], v249 offset:0\n"
"v_mfma_f32_16x16x32_bf16 v[28:31], a[252:255], a[164:167], v[28:31]\n"
"ds_read_b128 a[164:167], v250 offset:0\n"
"v_mfma_f32_16x16x32_bf16 v[60:63], a[248:251], a[168:171], v[60:63]\n"
"buffer_load_dwordx4 v254, s[24:27], s49 offen offset:2048 lds\n"
"v_mfma_f32_16x16x32_bf16 v[60:63], a[252:255], a[172:175], v[60:63]\n"
"ds_read_b64_tr_b16 a[112:113], v245 offset:0\n"
"v_mfma_f32_16x16x32_bf16 v[56:59], a[240:243], a[168:171], v[56:59]\n"
"ds_read_b64_tr_b16 a[114:115], v245 offset:512\n"
"v_mfma_f32_16x16x32_bf16 v[56:59], a[244:247], a[172:175], v[56:59]\n"
"ds_read_b64_tr_b16 a[116:117], v245 offset:8192\n"
"v_mfma_f32_16x16x32_bf16 v[52:55], a[232:235], a[168:171], v[52:55]\n"
"ds_read_b64_tr_b16 a[118:119], v245 offset:8704\n"
"v_mfma_f32_16x16x32_bf16 v[52:55], a[236:239], a[172:175], v[52:55]\n"
"buffer_load_dwordx4 v255, s[24:27], s49 offen offset:3072 lds\n"
"v_mfma_f32_16x16x32_bf16 v[48:51], a[224:227], a[168:171], v[48:51]\n"
"ds_read_b64_tr_b16 a[120:121], v246 offset:0\n"
"v_mfma_f32_16x16x32_bf16 v[48:51], a[228:231], a[172:175], v[48:51]\n"
"ds_read_b64_tr_b16 a[122:123], v246 offset:512\n"
"v_mfma_f32_16x16x32_bf16 v[44:47], a[216:219], a[168:171], v[44:47]\n"
"ds_read_b64_tr_b16 a[124:125], v246 offset:8192\n"
"v_mfma_f32_16x16x32_bf16 v[44:47], a[220:223], a[172:175], v[44:47]\n"
"s_add_u32 m0, s51, 4096\n"
"ds_read_b64_tr_b16 a[126:127], v246 offset:8704\n"
"v_mfma_f32_16x16x32_bf16 v[40:43], a[208:211], a[168:171], v[40:43]\n"
"s_add_u32 s40, s49, 40960\n"
"buffer_load_dwordx4 v247, s[24:27], s40 offen lds\n"
"v_mfma_f32_16x16x32_bf16 v[40:43], a[212:215], a[172:175], v[40:43]\n"
"ds_read_b64_tr_b16 a[128:129], v243 offset:1024\n"
"v_mfma_f32_16x16x32_bf16 v[36:39], a[200:203], a[168:171], v[36:39]\n"
"ds_read_b64_tr_b16 a[130:131], v243 offset:1536\n"
"v_mfma_f32_16x16x32_bf16 v[36:39], a[204:207], a[172:175], v[36:39]\n"
"ds_read_b64_tr_b16 a[132:133], v243 offset:9216\n"
"v_mfma_f32_16x16x32_bf16 v[32:35], a[192:195], a[168:171], v[32:35]\n"
"ds_read_b64_tr_b16 a[134:135], v243 offset:9728\n"
"v_mfma_f32_16x16x32_bf16 v[32:35], a[196:199], a[172:175], v[32:35]\n"
"buffer_load_dwordx4 v253, s[24:27], s40 offen offset:1024 lds\n"
"v_mfma_f32_16x16x32_bf16 v[64:67], a[192:195], a[176:179], v[64:67]\n"
"ds_read_b64_tr_b16 a[136:137], v244 offset:1024\n"
"v_mfma_f32_16x16x32_bf16 v[64:67], a[196:199], a[180:183], v[64:67]\n"
"ds_read_b64_tr_b16 a[138:139], v244 offset:1536\n"
"v_mfma_f32_16x16x32_bf16 v[68:71], a[200:203], a[176:179], v[68:71]\n"
"ds_read_b64_tr_b16 a[140:141], v244 offset:9216\n"
"v_mfma_f32_16x16x32_bf16 v[68:71], a[204:207], a[180:183], v[68:71]\n"
"ds_read_b64_tr_b16 a[142:143], v244 offset:9728\n"
"v_mfma_f32_16x16x32_bf16 v[72:75], a[208:211], a[176:179], v[72:75]\n"
"buffer_load_dwordx4 v254, s[24:27], s40 offen offset:2048 lds\n"
"v_mfma_f32_16x16x32_bf16 v[72:75], a[212:215], a[180:183], v[72:75]\n"
"s_add_u32 s49, s49, 128\n"
"ds_read_b64_tr_b16 a[144:145], v245 offset:1024\n"
"v_mfma_f32_16x16x32_bf16 v[76:79], a[216:219], a[176:179], v[76:79]\n"
"s_sub_u32 s41, s49, 1280\n"
"ds_read_b64_tr_b16 a[146:147], v245 offset:1536\n"
"v_mfma_f32_16x16x32_bf16 v[76:79], a[220:223], a[180:183], v[76:79]\n"
"s_min_u32 s49, s49, s41\n"
"ds_read_b64_tr_b16 a[148:149], v245 offset:9216\n"
"v_mfma_f32_16x16x32_bf16 v[88:91], a[240:243], a[176:179], v[88:91]\n"
"s_xor_b32 s51, s51, s74\n"
"ds_read_b64_tr_b16 a[150:151], v245 offset:9728\n"
"v_mfma_f32_16x16x32_bf16 v[88:91], a[244:247], a[180:183], v[88:91]\n"
"ds_read_b64_tr_b16 a[152:153], v246 offset:1024\n"
"v_mfma_f32_16x16x32_bf16 v[92:95], a[248:251], a[176:179], v[92:95]\n"
"ds_read_b64_tr_b16 a[154:155], v246 offset:1536\n"
"v_mfma_f32_16x16x32_bf16 v[92:95], a[252:255], a[180:183], v[92:95]\n"
"ds_read_b64_tr_b16 a[156:157], v246 offset:9216\n"
"v_mfma_f32_16x16x32_bf16 v[80:83], a[224:227], a[176:179], v[80:83]\n"
"ds_read_b64_tr_b16 a[158:159], v246 offset:9728\n"
"v_mfma_f32_16x16x32_bf16 v[80:83], a[228:231], a[180:183], v[80:83]\n"
"ds_read_b128 a[168:171], v249 offset:2048\n"
"v_mfma_f32_16x16x32_bf16 v[84:87], a[232:235], a[176:179], v[84:87]\n"
"ds_read_b128 a[172:175], v250 offset:2048\n"
"v_mfma_f32_16x16x32_bf16 v[84:87], a[236:239], a[180:183], v[84:87]\n"
"ds_read_b128 a[176:179], v249 offset:4096\n"
"v_mfma_f32_16x16x32_bf16 v[108:111], a[232:235], a[184:187], v[108:111]\n"
"ds_read_b128 a[180:183], v250 offset:4096\n"
"v_mfma_f32_16x16x32_bf16 v[108:111], a[236:239], a[188:191], v[108:111]\n"
"v_mfma_f32_16x16x32_bf16 v[104:107], a[224:227], a[184:187], v[104:107]\n"
"v_mfma_f32_16x16x32_bf16 v[104:107], a[228:231], a[188:191], v[104:107]\n"
"v_mfma_f32_16x16x32_bf16 v[100:103], a[200:203], a[184:187], v[100:103]\n"
"v_mfma_f32_16x16x32_bf16 v[100:103], a[204:207], a[188:191], v[100:103]\n"
"v_mfma_f32_16x16x32_bf16 v[96:99], a[192:195], a[184:187], v[96:99]\n"
"s_add_u32 s47, s47, 1\n"
"ds_read_b128 a[184:187], v251 offset:0\n"
"v_mfma_f32_16x16x32_bf16 v[96:99], a[196:199], a[188:191], v[96:99]\n"
"ds_read_b128 a[188:191], v252 offset:0\n"
"s_cmp_lg_u32 s47, s62\ns_cbranch_scc1 LPL_%=\n"
"s_cmp_eq_u32 s47, 18\n"
"s_cbranch_scc1 LPT2_%=\n"
"s_waitcnt lgkmcnt(2)\n"
"v_mfma_f32_16x16x32_bf16 v[112:115], a[96:99], a[160:163], v[112:115]\n"
"v_mfma_f32_16x16x32_bf16 v[112:115], a[100:103], a[164:167], v[112:115]\n"
"v_mfma_f32_16x16x32_bf16 v[116:119], a[104:107], a[160:163], v[116:119]\n"
"v_mfma_f32_16x16x32_bf16 v[116:119], a[108:111], a[164:167], v[116:119]\n"
"s_waitcnt vmcnt(11)\n"
"v_mfma_f32_16x16x32_bf16 v[120:123], a[112:115], a[160:163], v[120:123]\n"
"s_barrier\n"
"ds_read_b64_tr_b16 a[232:233], v244 offset:17408\n"
"v_mfma_f32_16x16x32_bf16 v[120:123], a[116:119], a[164:167], v[120:123]\n"
"s_xor_b32 m0, s50, 0xc000\n"
"ds_read_b64_tr_b16 a[234:235], v244 offset:17920\n"
"v_mfma_f32_16x16x32_bf16 v[124:127], a[120:123], a[160:163], v[124:127]\n"
"s_add_u32 s40, s48, 256\n"
"buffer_load_dwordx4 v224, s[28:31], s40 offen lds\n"
"v_mfma_f32_16x16x32_bf16 v[124:127], a[124:127], a[164:167], v[124:127]\n"
"ds_read_b64_tr_b16 a[236:237], v244 offset:25600\n"
"v_mul_f32 v112, s45, v112\n"
"v_mfma_f32_16x16x32_bf16 v[128:131], a[128:131], a[160:163], v[128:131]\n"
"ds_read_b64_tr_b16 a[238:239], v244 offset:26112\n"
"v_mul_f32 v113, s45, v113\n"
"v_mfma_f32_16x16x32_bf16 v[128:131], a[132:135], a[164:167], v[128:131]\n"
"ds_read_b64_tr_b16 a[224:225], v243 offset:17408\n"
"v_mul_f32 v114, s45, v114\n"
"v_mfma_f32_16x16x32_bf16 v[132:135], a[136:139], a[160:163], v[132:135]\n"
"ds_read_b64_tr_b16 a[226:227], v243 offset:17920\n"
"v_mul_f32 v115, s45, v115\n"
"v_mfma_f32_16x16x32_bf16 v[132:135], a[140:143], a[164:167], v[132:135]\n"
"buffer_load_dwordx4 v225, s[28:31], s40 offen offset:1024 lds\n"
"v_mfma_f32_16x16x32_bf16 v[136:139], a[144:147], a[160:163], v[136:139]\n"
"ds_read_b64_tr_b16 a[228:229], v243 offset:25600\n"
"v_mul_f32 v116, s45, v116\n"
"v_mfma_f32_16x16x32_bf16 v[136:139], a[148:151], a[164:167], v[136:139]\n"
"ds_read_b64_tr_b16 a[230:231], v243 offset:26112\n"
"v_mul_f32 v117, s45, v117\n"
"v_mfma_f32_16x16x32_bf16 v[140:143], a[152:155], a[160:163], v[140:143]\n"
"ds_read_b64_tr_b16 a[192:193], v243 offset:16384\n"
"v_mul_f32 v118, s45, v118\n"
"v_mfma_f32_16x16x32_bf16 v[140:143], a[156:159], a[164:167], v[140:143]\n"
"s_waitcnt lgkmcnt(3)\n"
"ds_read_b64_tr_b16 a[194:195], v243 offset:16896\n"
"v_mul_f32 v119, s45, v119\n"
"v_mfma_f32_16x16x32_bf16 v[220:223], a[136:139], a[184:187], v[220:223]\n"
"buffer_load_dwordx4 v226, s[28:31], s40 offen offset:2048 lds\n"
"v_mfma_f32_16x16x32_bf16 v[220:223], a[140:143], a[188:191], v[220:223]\n"
"ds_read_b64_tr_b16 a[196:197], v243 offset:24576\n"
"v_cvt_pk_bf16_f32 v112, v112, v113\n"
"v_mfma_f32_16x16x32_bf16 v[216:219], a[128:131], a[184:187], v[216:219]\n"
"ds_read_b64_tr_b16 a[198:199], v243 offset:25088\n"
"v_cvt_pk_bf16_f32 v113, v114, v115\n"
"v_mfma_f32_16x16x32_bf16 v[216:219], a[132:135], a[188:191], v[216:219]\n"
"ds_read_b64_tr_b16 a[200:201], v244 offset:16384\n"
"v_cvt_pk_bf16_f32 v114, v116, v117\n"
"v_mfma_f32_16x16x32_bf16 v[212:215], a[104:107], a[184:187], v[212:215]\n"
"ds_read_b64_tr_b16 a[202:203], v244 offset:16896\n"
"v_cvt_pk_bf16_f32 v115, v118, v119\n"
"v_mfma_f32_16x16x32_bf16 v[212:215], a[108:111], a[188:191], v[212:215]\n"
"buffer_load_dwordx4 v227, s[28:31], s40 offen offset:3072 lds\n"
"v_mfma_f32_16x16x32_bf16 v[208:211], a[96:99], a[184:187], v[208:211]\n"
"s_cmp_eq_u32 s47, 8\ns_cselect_b32 s41, 524800, s58\n"
"ds_read_b64_tr_b16 a[204:205], v244 offset:24576\n"
"v_mul_f32 v120, s45, v120\n"
"v_mfma_f32_16x16x32_bf16 v[208:211], a[100:103], a[188:191], v[208:211]\n"
"s_add_u32 s48, s48, s41\n"
"ds_read_b64_tr_b16 a[206:207], v244 offset:25088\n"
"v_mul_f32 v121, s45, v121\n"
"v_mfma_f32_16x16x32_bf16 v[172:175], a[152:155], a[168:171], v[172:175]\n"
"s_sub_u32 s41, s48, 5242880\n"
"ds_read_b64_tr_b16 a[208:209], v245 offset:16384\n"
"v_mul_f32 v122, s45, v122\n"
"v_mfma_f32_16x16x32_bf16 v[172:175], a[156:159], a[172:175], v[172:175]\n"
"s_min_u32 s48, s48, s41\n"
"ds_read_b64_tr_b16 a[210:211], v245 offset:16896\n"
"v_mul_f32 v123, s45, v123\n"
"v_mfma_f32_16x16x32_bf16 v[168:171], a[144:147], a[168:171], v[168:171]\n"
"s_mov_b32 m0, s50\n"
"ds_read_b64_tr_b16 a[212:213], v245 offset:24576\n"
"v_mul_f32 v124, s45, v124\n"
"v_mfma_f32_16x16x32_bf16 v[168:171], a[148:151], a[172:175], v[168:171]\n"
"buffer_load_dwordx4 v224, s[28:31], s48 offen lds\n"
"v_mfma_f32_16x16x32_bf16 v[164:167], a[136:139], a[168:171], v[164:167]\n"
"ds_read_b64_tr_b16 a[214:215], v245 offset:25088\n"
"v_mul_f32 v125, s45, v125\n"
"v_mfma_f32_16x16x32_bf16 v[164:167], a[140:143], a[172:175], v[164:167]\n"
"ds_read_b64_tr_b16 a[216:217], v246 offset:16384\n"
"v_mul_f32 v126, s45, v126\n"
"v_mfma_f32_16x16x32_bf16 v[160:163], a[128:131], a[168:171], v[160:163]\n"
"ds_read_b64_tr_b16 a[218:219], v246 offset:16896\n"
"v_xor_b32 v243, s74, v243\n"
"v_mfma_f32_16x16x32_bf16 v[160:163], a[132:135], a[172:175], v[160:163]\n"
"buffer_load_dwordx4 v225, s[28:31], s48 offen offset:1024 lds\n"
"v_mfma_f32_16x16x32_bf16 v[156:159], a[120:123], a[168:171], v[156:159]\n"
"ds_read_b64_tr_b16 a[220:221], v246 offset:24576\n"
"v_xor_b32 v249, s74, v249\n"
"v_mfma_f32_16x16x32_bf16 v[156:159], a[124:127], a[172:175], v[156:159]\n"
"ds_read_b64_tr_b16 a[222:223], v246 offset:25088\n"
"v_xor_b32 v250, s74, v250\n"
"v_mfma_f32_16x16x32_bf16 v[152:155], a[112:115], a[168:171], v[152:155]\n"
"ds_read_b64_tr_b16 a[240:241], v245 offset:17408\n"
"v_xor_b32 v244, s74, v244\n"
"v_mfma_f32_16x16x32_bf16 v[152:155], a[116:119], a[172:175], v[152:155]\n"
"ds_read_b64_tr_b16 a[242:243], v245 offset:17920\n"
"v_mul_f32 v127, s45, v127\n"
"v_mfma_f32_16x16x32_bf16 v[148:151], a[104:107], a[168:171], v[148:151]\n"
"buffer_load_dwordx4 v226, s[28:31], s48 offen offset:2048 lds\n"
"v_mfma_f32_16x16x32_bf16 v[148:151], a[108:111], a[172:175], v[148:151]\n"
"ds_read_b64_tr_b16 a[244:245], v245 offset:25600\n"
"v_cvt_pk_bf16_f32 v120, v120, v121\n"
"v_mfma_f32_16x16x32_bf16 v[144:147], a[96:99], a[168:171], v[144:147]\n"
"ds_read_b64_tr_b16 a[246:247], v245 offset:26112\n"
"v_xor_b32 v245, s74, v245\n"
"v_mfma_f32_16x16x32_bf16 v[144:147], a[100:103], a[172:175], v[144:147]\n"
"ds_read_b64_tr_b16 a[248:249], v246 offset:17408\n"
"v_cvt_pk_bf16_f32 v121, v122, v123\n"
"v_mfma_f32_16x16x32_bf16 v[176:179], a[96:99], a[176:179], v[176:179]\n"
"ds_read_b64_tr_b16 a[250:251], v246 offset:17920\n"
"v_cvt_pk_bf16_f32 v122, v124, v125\n"
"v_mfma_f32_16x16x32_bf16 v[176:179], a[100:103], a[180:183], v[176:179]\n"
"buffer_load_dwordx4 v227, s[28:31], s48 offen offset:3072 lds\n"
"v_mfma_f32_16x16x32_bf16 v[180:183], a[104:107], a[176:179], v[180:183]\n"
"s_xor_b32 s50, s50, s74\n"
"ds_read_b64_tr_b16 a[252:253], v246 offset:25600\n"
"v_cvt_pk_bf16_f32 v123, v126, v127\n"
"v_mfma_f32_16x16x32_bf16 v[180:183], a[108:111], a[180:183], v[180:183]\n"
"ds_read_b64_tr_b16 a[254:255], v246 offset:26112\n"
"v_xor_b32 v246, s74, v246\n"
"v_mfma_f32_16x16x32_bf16 v[184:187], a[112:115], a[176:179], v[184:187]\n"
"v_permlane16_swap_b32 v112, v114\n"
"v_mfma_f32_16x16x32_bf16 v[184:187], a[116:119], a[180:183], v[184:187]\n"
"v_permlane16_swap_b32 v113, v115\n"
"v_mfma_f32_16x16x32_bf16 v[188:191], a[120:123], a[176:179], v[188:191]\n"
"v_permlane16_swap_b32 v120, v122\n"
"v_mfma_f32_16x16x32_bf16 v[188:191], a[124:127], a[180:183], v[188:191]\n"
"v_permlane16_swap_b32 v121, v123\n"
"v_mfma_f32_16x16x32_bf16 v[200:203], a[144:147], a[176:179], v[200:203]\n"
"v_accvgpr_write_b32 a0, v112\n"
"v_accvgpr_write_b32 a1, v113\n"
"v_mfma_f32_16x16x32_bf16 v[200:203], a[148:151], a[180:183], v[200:203]\n"
"v_accvgpr_write_b32 a2, v114\n"
"v_accvgpr_write_b32 a3, v115\n"
"v_mfma_f32_16x16x32_bf16 v[204:207], a[152:155], a[176:179], v[204:207]\n"
"v_accvgpr_write_b32 a4, v120\n"
"v_accvgpr_write_b32 a5, v121\n"
"v_mfma_f32_16x16x32_bf16 v[204:207], a[156:159], a[180:183], v[204:207]\n"
"v_accvgpr_write_b32 a6, v122\n"
"v_accvgpr_write_b32 a7, v123\n"
"v_mfma_f32_16x16x32_bf16 v[192:195], a[128:131], a[176:179], v[192:195]\n"
"v_mul_f32 v128, s45, v128\n"
"v_mul_f32 v129, s45, v129\n"
"v_mfma_f32_16x16x32_bf16 v[192:195], a[132:135], a[180:183], v[192:195]\n"
"v_mul_f32 v130, s45, v130\n"
"v_mul_f32 v131, s45, v131\n"
"v_mfma_f32_16x16x32_bf16 v[196:199], a[136:139], a[176:179], v[196:199]\n"
"v_mul_f32 v132, s45, v132\n"
"v_mul_f32 v133, s45, v133\n"
"v_mfma_f32_16x16x32_bf16 v[196:199], a[140:143], a[180:183], v[196:199]\n"
"v_mul_f32 v134, s45, v134\n"
"v_mul_f32 v135, s45, v135\n"
"v_mfma_f32_16x16x32_bf16 v[108:111], a[232:235], a[184:187], v[108:111]\n"
"v_cvt_pk_bf16_f32 v128, v128, v129\n"
"v_cvt_pk_bf16_f32 v129, v130, v131\n"
"v_mfma_f32_16x16x32_bf16 v[108:111], a[236:239], a[188:191], v[108:111]\n"
"v_cvt_pk_bf16_f32 v130, v132, v133\n"
"v_cvt_pk_bf16_f32 v131, v134, v135\n"
"v_mfma_f32_16x16x32_bf16 v[104:107], a[224:227], a[184:187], v[104:107]\n"
"s_waitcnt lgkmcnt(0)\n"
"v_permlane16_swap_b32 v128, v130\n"
"v_mfma_f32_16x16x32_bf16 v[104:107], a[228:231], a[188:191], v[104:107]\n"
"s_waitcnt vmcnt(8)\n"
"v_permlane16_swap_b32 v129, v131\n"
"v_mfma_f32_16x16x32_bf16 v[100:103], a[200:203], a[184:187], v[100:103]\n"
"s_barrier\n"
"ds_read_b64_tr_b16 a[96:97], v243 offset:0\n"
"v_mfma_f32_16x16x32_bf16 v[100:103], a[204:207], a[188:191], v[100:103]\n"
"s_mov_b32 m0, s51\n"
"ds_read_b64_tr_b16 a[98:99], v243 offset:512\n"
"v_accvgpr_write_b32 a8, v128\n"
"v_mfma_f32_16x16x32_bf16 v[96:99], a[192:195], a[184:187], v[96:99]\n"
"buffer_load_dwordx4 v247, s[24:27], s49 offen lds\n"
"v_mfma_f32_16x16x32_bf16 v[96:99], a[196:199], a[188:191], v[96:99]\n"
"ds_read_b64_tr_b16 a[100:101], v243 offset:8192\n"
"v_accvgpr_write_b32 a9, v129\n"
"v_mfma_f32_16x16x32_bf16 v[0:3], a[192:195], a[160:163], v[0:3]\n"
"ds_read_b64_tr_b16 a[102:103], v243 offset:8704\n"
"v_accvgpr_write_b32 a10, v130\n"
"v_mfma_f32_16x16x32_bf16 v[0:3], a[196:199], a[164:167], v[0:3]\n"
"ds_read_b64_tr_b16 a[104:105], v244 offset:0\n"
"v_accvgpr_write_b32 a11, v131\n"
"v_mfma_f32_16x16x32_bf16 v[4:7], a[200:203], a[160:163], v[4:7]\n"
"ds_read_b64_tr_b16 a[106:107], v244 offset:512\n"
"v_mul_f32 v136, s45, v136\n"
"v_mfma_f32_16x16x32_bf16 v[4:7], a[204:207], a[164:167], v[4:7]\n"
"buffer_load_dwordx4 v253, s[24:27], s49 offen offset:1024 lds\n"
"v_mfma_f32_16x16x32_bf16 v[8:11], a[208:211], a[160:163], v[8:11]\n"
"ds_read_b64_tr_b16 a[108:109], v244 offset:8192\n"
"v_mul_f32 v137, s45, v137\n"
"v_mfma_f32_16x16x32_bf16 v[8:11], a[212:215], a[164:167], v[8:11]\n"
"ds_read_b64_tr_b16 a[110:111], v244 offset:8704\n"
"v_mul_f32 v138, s45, v138\n"
"v_mfma_f32_16x16x32_bf16 v[12:15], a[216:219], a[160:163], v[12:15]\n"
"ds_read_b64_tr_b16 a[112:113], v245 offset:0\n"
"v_mul_f32 v139, s45, v139\n"
"v_mfma_f32_16x16x32_bf16 v[12:15], a[220:223], a[164:167], v[12:15]\n"
"ds_read_b64_tr_b16 a[114:115], v245 offset:512\n"
"v_mul_f32 v140, s45, v140\n"
"v_mfma_f32_16x16x32_bf16 v[16:19], a[224:227], a[160:163], v[16:19]\n"
"buffer_load_dwordx4 v254, s[24:27], s49 offen offset:2048 lds\n"
"v_mfma_f32_16x16x32_bf16 v[16:19], a[228:231], a[164:167], v[16:19]\n"
"ds_read_b64_tr_b16 a[116:117], v245 offset:8192\n"
"v_mul_f32 v141, s45, v141\n"
"v_mfma_f32_16x16x32_bf16 v[20:23], a[232:235], a[160:163], v[20:23]\n"
"ds_read_b64_tr_b16 a[118:119], v245 offset:8704\n"
"v_mul_f32 v142, s45, v142\n"
"v_mfma_f32_16x16x32_bf16 v[20:23], a[236:239], a[164:167], v[20:23]\n"
"ds_read_b64_tr_b16 a[120:121], v246 offset:0\n"
"v_mul_f32 v143, s45, v143\n"
"v_mfma_f32_16x16x32_bf16 v[24:27], a[240:243], a[160:163], v[24:27]\n"
"ds_read_b64_tr_b16 a[122:123], v246 offset:512\n"
"v_cvt_pk_bf16_f32 v136, v136, v137\n"
"v_mfma_f32_16x16x32_bf16 v[24:27], a[244:247], a[164:167], v[24:27]\n"
"buffer_load_dwordx4 v255, s[24:27], s49 offen offset:3072 lds\n"
"v_mfma_f32_16x16x32_bf16 v[28:31], a[248:251], a[160:163], v[28:31]\n"
"ds_read_b128 a[160:163], v249 offset:0\n"
"v_cvt_pk_bf16_f32 v137, v138, v139\n"
"v_mfma_f32_16x16x32_bf16 v[28:31], a[252:255], a[164:167], v[28:31]\n"
"ds_read_b128 a[164:167], v250 offset:0\n"
"v_cvt_pk_bf16_f32 v138, v140, v141\n"
"v_mfma_f32_16x16x32_bf16 v[60:63], a[248:251], a[168:171], v[60:63]\n"
"ds_read_b64_tr_b16 a[124:125], v246 offset:8192\n"
"v_cvt_pk_bf16_f32 v139, v142, v143\n"
"v_mfma_f32_16x16x32_bf16 v[60:63], a[252:255], a[172:175], v[60:63]\n"
"s_add_u32 m0, s51, 4096\n"
"ds_read_b64_tr_b16 a[126:127], v246 offset:8704\n"
"v_mul_f32 v168, s45, v168\n"
"v_mfma_f32_16x16x32_bf16 v[56:59], a[240:243], a[168:171], v[56:59]\n"
"s_add_u32 s40, s49, 40960\n"
"buffer_load_dwordx4 v247, s[24:27], s40 offen lds\n"
"v_mfma_f32_16x16x32_bf16 v[56:59], a[244:247], a[172:175], v[56:59]\n"
"ds_read_b64_tr_b16 a[128:129], v243 offset:1024\n"
"v_mul_f32 v169, s45, v169\n"
"v_mfma_f32_16x16x32_bf16 v[52:55], a[232:235], a[168:171], v[52:55]\n"
"ds_read_b64_tr_b16 a[130:131], v243 offset:1536\n"
"v_mul_f32 v170, s45, v170\n"
"v_mfma_f32_16x16x32_bf16 v[52:55], a[236:239], a[172:175], v[52:55]\n"
"ds_read_b64_tr_b16 a[132:133], v243 offset:9216\n"
"v_mul_f32 v171, s45, v171\n"
"v_mfma_f32_16x16x32_bf16 v[48:51], a[224:227], a[168:171], v[48:51]\n"
"ds_read_b64_tr_b16 a[134:135], v243 offset:9728\n"
"v_mul_f32 v172, s45, v172\n"
"v_mfma_f32_16x16x32_bf16 v[48:51], a[228:231], a[172:175], v[48:51]\n"
"buffer_load_dwordx4 v253, s[24:27], s40 offen offset:1024 lds\n"
"v_mfma_f32_16x16x32_bf16 v[44:47], a[216:219], a[168:171], v[44:47]\n"
"ds_read_b64_tr_b16 a[136:137], v244 offset:1024\n"
"v_mul_f32 v173, s45, v173\n"
"v_mfma_f32_16x16x32_bf16 v[44:47], a[220:223], a[172:175], v[44:47]\n"
"ds_read_b64_tr_b16 a[138:139], v244 offset:1536\n"
"v_mul_f32 v174, s45, v174\n"
"v_mfma_f32_16x16x32_bf16 v[40:43], a[208:211], a[168:171], v[40:43]\n"
"ds_read_b64_tr_b16 a[140:141], v244 offset:9216\n"
"v_mul_f32 v175, s45, v175\n"
"v_mfma_f32_16x16x32_bf16 v[40:43], a[212:215], a[172:175], v[40:43]\n"
"ds_read_b64_tr_b16 a[142:143], v244 offset:9728\n"
"v_cvt_pk_bf16_f32 v168, v168, v169\n"
"v_mfma_f32_16x16x32_bf16 v[36:39], a[200:203], a[168:171], v[36:39]\n"
"buffer_load_dwordx4 v254, s[24:27], s40 offen offset:2048 lds\n"
"v_mfma_f32_16x16x32_bf16 v[36:39], a[204:207], a[172:175], v[36:39]\n"
"s_add_u32 s49, s49, 128\n"
"ds_read_b64_tr_b16 a[144:145], v245 offset:1024\n"
"v_cvt_pk_bf16_f32 v169, v170, v171\n"
"v_mfma_f32_16x16x32_bf16 v[32:35], a[192:195], a[168:171], v[32:35]\n"
"s_sub_u32 s41, s49, 1280\n"
"ds_read_b64_tr_b16 a[146:147], v245 offset:1536\n"
"v_cvt_pk_bf16_f32 v170, v172, v173\n"
"v_mfma_f32_16x16x32_bf16 v[32:35], a[196:199], a[172:175], v[32:35]\n"
"s_min_u32 s49, s49, s41\n"
"ds_read_b64_tr_b16 a[148:149], v245 offset:9216\n"
"v_cvt_pk_bf16_f32 v171, v174, v175\n"
"v_mfma_f32_16x16x32_bf16 v[64:67], a[192:195], a[176:179], v[64:67]\n"
"s_xor_b32 s51, s51, s74\n"
"ds_read_b64_tr_b16 a[150:151], v245 offset:9728\n"
"v_mul_f32 v160, s45, v160\n"
"v_mfma_f32_16x16x32_bf16 v[64:67], a[196:199], a[180:183], v[64:67]\n"
"ds_read_b64_tr_b16 a[152:153], v246 offset:1024\n"
"v_mul_f32 v161, s45, v161\n"
"v_mfma_f32_16x16x32_bf16 v[68:71], a[200:203], a[176:179], v[68:71]\n"
"ds_read_b64_tr_b16 a[154:155], v246 offset:1536\n"
"v_mul_f32 v162, s45, v162\n"
"v_mfma_f32_16x16x32_bf16 v[68:71], a[204:207], a[180:183], v[68:71]\n"
"ds_read_b64_tr_b16 a[156:157], v246 offset:9216\n"
"v_mul_f32 v163, s45, v163\n"
"v_mfma_f32_16x16x32_bf16 v[72:75], a[208:211], a[176:179], v[72:75]\n"
"ds_read_b64_tr_b16 a[158:159], v246 offset:9728\n"
"v_mul_f32 v164, s45, v164\n"
"v_mfma_f32_16x16x32_bf16 v[72:75], a[212:215], a[180:183], v[72:75]\n"
"ds_read_b128 a[168:171], v249 offset:2048\n"
"v_mul_f32 v165, s45, v165\n"
"v_mfma_f32_16x16x32_bf16 v[76:79], a[216:219], a[176:179], v[76:79]\n"
"ds_read_b128 a[172:175], v250 offset:2048\n"
"v_mul_f32 v166, s45, v166\n"
"v_mfma_f32_16x16x32_bf16 v[76:79], a[220:223], a[180:183], v[76:79]\n"
"v_permlane16_swap_b32 v136, v138\n"
"v_mfma_f32_16x16x32_bf16 v[88:91], a[240:243], a[176:179], v[88:91]\n"
"v_permlane16_swap_b32 v137, v139\n"
"v_mfma_f32_16x16x32_bf16 v[88:91], a[244:247], a[180:183], v[88:91]\n"
"v_permlane16_swap_b32 v168, v170\n"
"v_mfma_f32_16x16x32_bf16 v[92:95], a[248:251], a[176:179], v[92:95]\n"
"v_permlane16_swap_b32 v169, v171\n"
"v_mfma_f32_16x16x32_bf16 v[92:95], a[252:255], a[180:183], v[92:95]\n"
"v_accvgpr_write_b32 a12, v136\n"
"v_accvgpr_write_b32 a13, v137\n"
"v_mfma_f32_16x16x32_bf16 v[80:83], a[224:227], a[176:179], v[80:83]\n"
"v_accvgpr_write_b32 a14, v138\n"
"v_accvgpr_write_b32 a15, v139\n"
"v_mfma_f32_16x16x32_bf16 v[80:83], a[228:231], a[180:183], v[80:83]\n"
"v_accvgpr_write_b32 a28, v168\n"
"v_accvgpr_write_b32 a29, v169\n"
"v_mfma_f32_16x16x32_bf16 v[84:87], a[232:235], a[176:179], v[84:87]\n"
"s_add_u32 s47, s47, 1\n"
"ds_read_b128 a[176:179], v249 offset:4096\n"
"v_accvgpr_write_b32 a30, v170\n"
"v_mfma_f32_16x16x32_bf16 v[84:87], a[236:239], a[180:183], v[84:87]\n"
"ds_read_b128 a[180:183], v250 offset:4096\n"
"v_accvgpr_write_b32 a31, v171\n"
"s_waitcnt lgkmcnt(2)\n"
"v_mfma_f32_16x16x32_bf16 v[112:115], a[96:99], a[160:163], 0\n"
"v_mul_f32 v167, s45, v167\n"
"v_cvt_pk_bf16_f32 v160, v160, v161\n"
"v_mfma_f32_16x16x32_bf16 v[112:115], a[100:103], a[164:167], v[112:115]\n"
"v_cvt_pk_bf16_f32 v161, v162, v163\n"
"v_cvt_pk_bf16_f32 v162, v164, v165\n"
"v_mfma_f32_16x16x32_bf16 v[116:119], a[104:107], a[160:163], 0\n"
"s_nop 0\n"
"v_permlane16_swap_b32 v160, v162\n"
"v_mfma_f32_16x16x32_bf16 v[116:119], a[108:111], a[164:167], v[116:119]\n"
"s_waitcnt vmcnt(11)\n"
"v_cvt_pk_bf16_f32 v163, v166, v167\n"
"v_accvgpr_write_b32 a24, v160\n"
"v_mfma_f32_16x16x32_bf16 v[120:123], a[112:115], a[160:163], 0\n"
"s_barrier\n"
"ds_read_b64_tr_b16 a[192:193], v243 offset:16384\n"
"v_mfma_f32_16x16x32_bf16 v[120:123], a[116:119], a[164:167], v[120:123]\n"
"s_xor_b32 m0, s50, 0xc000\n"
"ds_read_b64_tr_b16 a[194:195], v243 offset:16896\n"
"v_accvgpr_write_b32 a26, v162\n"
"v_mfma_f32_16x16x32_bf16 v[124:127], a[120:123], a[160:163], 0\n"
"s_add_u32 s40, s48, 256\n"
"buffer_load_dwordx4 v224, s[28:31], s40 offen lds\n"
"v_mfma_f32_16x16x32_bf16 v[124:127], a[124:127], a[164:167], v[124:127]\n"
"ds_read_b64_tr_b16 a[196:197], v243 offset:24576\n"
"v_mul_f32 v152, s45, v152\n"
"v_mfma_f32_16x16x32_bf16 v[128:131], a[128:131], a[160:163], 0\n"
"ds_read_b64_tr_b16 a[198:199], v243 offset:25088\n"
"v_mul_f32 v153, s45, v153\n"
"v_mfma_f32_16x16x32_bf16 v[128:131], a[132:135], a[164:167], v[128:131]\n"
"ds_read_b64_tr_b16 a[200:201], v244 offset:16384\n"
"v_mul_f32 v154, s45, v154\n"
"v_mfma_f32_16x16x32_bf16 v[132:135], a[136:139], a[160:163], 0\n"
"ds_read_b64_tr_b16 a[202:203], v244 offset:16896\n"
"v_mul_f32 v155, s45, v155\n"
"v_mfma_f32_16x16x32_bf16 v[132:135], a[140:143], a[164:167], v[132:135]\n"
"buffer_load_dwordx4 v225, s[28:31], s40 offen offset:1024 lds\n"
"v_mfma_f32_16x16x32_bf16 v[136:139], a[144:147], a[160:163], 0\n"
"ds_read_b64_tr_b16 a[204:205], v244 offset:24576\n"
"v_mul_f32 v156, s45, v156\n"
"v_mfma_f32_16x16x32_bf16 v[136:139], a[148:151], a[164:167], v[136:139]\n"
"ds_read_b64_tr_b16 a[206:207], v244 offset:25088\n"
"v_mul_f32 v157, s45, v157\n"
"v_mfma_f32_16x16x32_bf16 v[140:143], a[152:155], a[160:163], 0\n"
"ds_read_b64_tr_b16 a[208:209], v245 offset:16384\n"
"v_mul_f32 v158, s45, v158\n"
"v_mfma_f32_16x16x32_bf16 v[140:143], a[156:159], a[164:167], v[140:143]\n"
"ds_read_b64_tr_b16 a[210:211], v245 offset:16896\n"
"v_mul_f32 v159, s45, v159\n"
"v_mfma_f32_16x16x32_bf16 v[172:175], a[152:155], a[168:171], 0\n"
"buffer_load_dwordx4 v226, s[28:31], s40 offen offset:2048 lds\n"
"v_mfma_f32_16x16x32_bf16 v[172:175], a[156:159], a[172:175], v[172:175]\n"
"ds_read_b64_tr_b16 a[212:213], v245 offset:24576\n"
"v_cvt_pk_bf16_f32 v152, v152, v153\n"
"v_mfma_f32_16x16x32_bf16 v[168:171], a[144:147], a[168:171], 0\n"
"ds_read_b64_tr_b16 a[214:215], v245 offset:25088\n"
"v_cvt_pk_bf16_f32 v153, v154, v155\n"
"v_mfma_f32_16x16x32_bf16 v[168:171], a[148:151], a[172:175], v[168:171]\n"
"ds_read_b64_tr_b16 a[216:217], v246 offset:16384\n"
"v_cvt_pk_bf16_f32 v154, v156, v157\n"
"v_mfma_f32_16x16x32_bf16 v[164:167], a[136:139], a[168:171], 0\n"
"v_permlane16_swap_b32 v161, v163\n"
"v_mfma_f32_16x16x32_bf16 v[164:167], a[140:143], a[172:175], v[164:167]\n"
"buffer_load_dwordx4 v227, s[28:31], s40 offen offset:3072 lds\n"
"v_accvgpr_write_b32 a25, v161\n"
"v_accvgpr_write_b32 a27, v163\n"
"v_mfma_f32_16x16x32_bf16 v[160:163], a[128:131], a[168:171], 0\n"
"s_cmp_eq_u32 s47, 8\ns_cselect_b32 s41, 524800, s58\n"
"ds_read_b64_tr_b16 a[218:219], v246 offset:16896\n"
"v_cvt_pk_bf16_f32 v155, v158, v159\n"
"v_mfma_f32_16x16x32_bf16 v[160:163], a[132:135], a[172:175], v[160:163]\n"
"s_add_u32 s48, s48, s41\n"
"v_permlane16_swap_b32 v152, v154\n"
"v_mfma_f32_16x16x32_bf16 v[156:159], a[120:123], a[168:171], 0\n"
"s_sub_u32 s41, s48, 5242880\n"
"v_permlane16_swap_b32 v153, v155\n"
"v_mfma_f32_16x16x32_bf16 v[156:159], a[124:127], a[172:175], v[156:159]\n"
"s_min_u32 s48, s48, s41\n"
"ds_read_b64_tr_b16 a[220:221], v246 offset:24576\n"
"v_accvgpr_write_b32 a20, v152\n"
"v_accvgpr_write_b32 a21, v153\n"
"v_accvgpr_write_b32 a22, v154\n"
"v_accvgpr_write_b32 a23, v155\n"
"v_mfma_f32_16x16x32_bf16 v[152:155], a[112:115], a[168:171], 0\n"
"s_mov_b32 m0, s50\n"
"ds_read_b64_tr_b16 a[222:223], v246 offset:25088\n"
"v_mul_f32 v144, s45, v144\n"
"v_mfma_f32_16x16x32_bf16 v[152:155], a[116:119], a[172:175], v[152:155]\n"
"buffer_load_dwordx4 v224, s[28:31], s48 offen lds\n"
"v_mul_f32 v145, s45, v145\n"
"v_mul_f32 v146, s45, v146\n"
"v_mul_f32 v147, s45, v147\n"
"v_mul_f32 v148, s45, v148\n"
"v_mul_f32 v149, s45, v149\n"
"v_mul_f32 v150, s45, v150\n"
"v_mul_f32 v151, s45, v151\n"
"v_cvt_pk_bf16_f32 v144, v144, v145\n"
"v_cvt_pk_bf16_f32 v145, v146, v147\n"
"v_cvt_pk_bf16_f32 v146, v148, v149\n"
"v_cvt_pk_bf16_f32 v147, v150, v151\n"
"v_mfma_f32_16x16x32_bf16 v[148:151], a[104:107], a[168:171], 0\n"
"v_permlane16_swap_b32 v144, v146\n"
"v_mfma_f32_16x16x32_bf16 v[148:151], a[108:111], a[172:175], v[148:151]\n"
"v_permlane16_swap_b32 v145, v147\n"
"v_accvgpr_write_b32 a16, v144\n"
"v_accvgpr_write_b32 a17, v145\n"
"v_accvgpr_write_b32 a18, v146\n"
"v_accvgpr_write_b32 a19, v147\n"
"v_mfma_f32_16x16x32_bf16 v[144:147], a[96:99], a[168:171], 0\n"
"ds_read_b64_tr_b16 a[224:225], v243 offset:17408\n"
"v_mul_f32 v176, s45, v176\n"
"v_mul_f32 v177, s45, v177\n"
"v_mul_f32 v178, s45, v178\n"
"v_mul_f32 v179, s45, v179\n"
"v_mul_f32 v180, s45, v180\n"
"v_mul_f32 v181, s45, v181\n"
"v_mul_f32 v182, s45, v182\n"
"v_mul_f32 v183, s45, v183\n"
"v_cvt_pk_bf16_f32 v176, v176, v177\n"
"v_cvt_pk_bf16_f32 v177, v178, v179\n"
"v_cvt_pk_bf16_f32 v178, v180, v181\n"
"v_cvt_pk_bf16_f32 v179, v182, v183\n"
"v_mfma_f32_16x16x32_bf16 v[144:147], a[100:103], a[172:175], v[144:147]\n"
"s_waitcnt lgkmcnt(1)\n"
"buffer_load_dwordx4 v225, s[28:31], s48 offen offset:1024 lds\n"
"v_permlane16_swap_b32 v176, v178\n"
"v_permlane16_swap_b32 v177, v179\n"
"v_accvgpr_write_b32 a32, v176\n"
"v_accvgpr_write_b32 a33, v177\n"
"v_accvgpr_write_b32 a34, v178\n"
"v_accvgpr_write_b32 a35, v179\n"
"v_mfma_f32_16x16x32_bf16 v[176:179], a[96:99], a[176:179], 0\n"
"ds_read_b64_tr_b16 a[226:227], v243 offset:17920\n"
"v_xor_b32 v251, s74, v251\n"
"v_mfma_f32_16x16x32_bf16 v[176:179], a[100:103], a[180:183], v[176:179]\n"
"ds_read_b128 a[184:187], v251 offset:0\n"
"v_mul_f32 v216, s45, v216\n"
"v_mul_f32 v217, s45, v217\n"
"v_mul_f32 v218, s45, v218\n"
"v_mul_f32 v219, s45, v219\n"
"v_mul_f32 v220, s45, v220\n"
"v_mul_f32 v221, s45, v221\n"
"v_mul_f32 v222, s45, v222\n"
"v_mul_f32 v223, s45, v223\n"
"v_cvt_pk_bf16_f32 v216, v216, v217\n"
"v_cvt_pk_bf16_f32 v217, v218, v219\n"
"v_cvt_pk_bf16_f32 v218, v220, v221\n"
"v_cvt_pk_bf16_f32 v219, v222, v223\n"
"v_xor_b32 v252, s74, v252\n"
"v_mfma_f32_16x16x32_bf16 v[180:183], a[104:107], a[176:179], 0\n"
"ds_read_b128 a[188:191], v252 offset:0\n"
"v_permlane16_swap_b32 v216, v218\n"
"v_permlane16_swap_b32 v217, v219\n"
"v_mul_f32 v184, s45, v184\n"
"v_mul_f32 v185, s45, v185\n"
"v_mul_f32 v186, s45, v186\n"
"v_mul_f32 v187, s45, v187\n"
"v_mul_f32 v188, s45, v188\n"
"v_mul_f32 v189, s45, v189\n"
"v_mul_f32 v190, s45, v190\n"
"v_mul_f32 v191, s45, v191\n"
"v_cvt_pk_bf16_f32 v184, v184, v185\n"
"v_cvt_pk_bf16_f32 v185, v186, v187\n"
"v_cvt_pk_bf16_f32 v186, v188, v189\n"
"v_cvt_pk_bf16_f32 v187, v190, v191\n"
"v_mfma_f32_16x16x32_bf16 v[180:183], a[108:111], a[180:183], v[180:183]\n"
"ds_write_b128 v248, v[216:219] offset:2048\n"
"v_permlane16_swap_b32 v184, v186\n"
"v_permlane16_swap_b32 v185, v187\n"
"v_accvgpr_write_b32 a36, v184\n"
"v_accvgpr_write_b32 a37, v185\n"
"v_accvgpr_write_b32 a38, v186\n"
"v_accvgpr_write_b32 a39, v187\n"
"v_mfma_f32_16x16x32_bf16 v[184:187], a[112:115], a[176:179], 0\n"
"buffer_load_dwordx4 v226, s[28:31], s48 offen offset:2048 lds\n"
"v_mfma_f32_16x16x32_bf16 v[184:187], a[116:119], a[180:183], v[184:187]\n"
"ds_read_b64_tr_b16 a[228:229], v243 offset:25600\n"
"v_mul_f32 v208, s45, v208\n"
"v_mfma_f32_16x16x32_bf16 v[188:191], a[120:123], a[176:179], 0\n"
"ds_read_b64_tr_b16 a[230:231], v243 offset:26112\n"
"v_mul_f32 v209, s45, v209\n"
"v_mul_f32 v210, s45, v210\n"
"v_mul_f32 v211, s45, v211\n"
"v_mul_f32 v212, s45, v212\n"
"v_mul_f32 v213, s45, v213\n"
"v_mul_f32 v214, s45, v214\n"
"v_mul_f32 v215, s45, v215\n"
"v_cvt_pk_bf16_f32 v208, v208, v209\n"
"v_cvt_pk_bf16_f32 v209, v210, v211\n"
"v_cvt_pk_bf16_f32 v210, v212, v213\n"
"v_cvt_pk_bf16_f32 v211, v214, v215\n"
"v_mul_f32 v200, s45, v200\n"
"v_mul_f32 v201, s45, v201\n"
"v_mul_f32 v202, s45, v202\n"
"v_mul_f32 v203, s45, v203\n"
"v_mul_f32 v204, s45, v204\n"
"v_mul_f32 v205, s45, v205\n"
"v_mul_f32 v206, s45, v206\n"
"v_mul_f32 v207, s45, v207\n"
"v_cvt_pk_bf16_f32 v200, v200, v201\n"
"v_cvt_pk_bf16_f32 v201, v202, v203\n"
"v_cvt_pk_bf16_f32 v202, v204, v205\n"
"v_cvt_pk_bf16_f32 v203, v206, v207\n"
"v_mfma_f32_16x16x32_bf16 v[188:191], a[124:127], a[180:183], v[188:191]\n"
"v_permlane16_swap_b32 v208, v210\n"
"v_permlane16_swap_b32 v209, v211\n"
"v_permlane16_swap_b32 v200, v202\n"
"v_permlane16_swap_b32 v201, v203\n"
"v_accvgpr_write_b32 a44, v200\n"
"v_accvgpr_write_b32 a45, v201\n"
"v_accvgpr_write_b32 a46, v202\n"
"v_accvgpr_write_b32 a47, v203\n"
"v_mfma_f32_16x16x32_bf16 v[200:203], a[144:147], a[176:179], 0\n"
"ds_write_b128 v248, v[208:211] offset:0\n"
"v_mfma_f32_16x16x32_bf16 v[200:203], a[148:151], a[180:183], v[200:203]\n"
"buffer_load_dwordx4 v227, s[28:31], s48 offen offset:3072 lds\n"
"v_mfma_f32_16x16x32_bf16 v[204:207], a[152:155], a[176:179], 0\n"
"s_xor_b32 s50, s50, s74\n"
"ds_read_b64_tr_b16 a[232:233], v244 offset:17408\n"
"v_mul_f32 v192, s45, v192\n"
"v_mul_f32 v193, s45, v193\n"
"v_mul_f32 v194, s45, v194\n"
"v_mul_f32 v195, s45, v195\n"
"v_mul_f32 v196, s45, v196\n"
"v_mul_f32 v197, s45, v197\n"
"v_mul_f32 v198, s45, v198\n"
"v_mul_f32 v199, s45, v199\n"
"v_cvt_pk_bf16_f32 v192, v192, v193\n"
"v_cvt_pk_bf16_f32 v193, v194, v195\n"
"v_cvt_pk_bf16_f32 v194, v196, v197\n"
"v_cvt_pk_bf16_f32 v195, v198, v199\n"
"v_mfma_f32_16x16x32_bf16 v[204:207], a[156:159], a[180:183], v[204:207]\n"
"v_permlane16_swap_b32 v192, v194\n"
"v_permlane16_swap_b32 v193, v195\n"
"v_accvgpr_write_b32 a40, v192\n"
"v_accvgpr_write_b32 a41, v193\n"
"v_accvgpr_write_b32 a42, v194\n"
"v_accvgpr_write_b32 a43, v195\n"
"v_mfma_f32_16x16x32_bf16 v[192:195], a[128:131], a[176:179], 0\n"
"ds_read_b64_tr_b16 a[234:235], v244 offset:17920\n"
"v_mul_f32 v0, s45, v0\n"
"v_mfma_f32_16x16x32_bf16 v[192:195], a[132:135], a[180:183], v[192:195]\n"
"ds_read_b64_tr_b16 a[236:237], v244 offset:25600\n"
"v_mul_f32 v1, s45, v1\n"
"v_mfma_f32_16x16x32_bf16 v[196:199], a[136:139], a[176:179], 0\n"
"ds_read_b64_tr_b16 a[238:239], v244 offset:26112\n"
"v_mul_f32 v2, s45, v2\n"
"v_mfma_f32_16x16x32_bf16 v[196:199], a[140:143], a[180:183], v[196:199]\n"
"s_waitcnt lgkmcnt(3)\n"
"ds_read_b64_tr_b16 a[240:241], v245 offset:17408\n"
"v_mul_f32 v3, s45, v3\n"
"v_mfma_f32_16x16x32_bf16 v[220:223], a[136:139], a[184:187], 0\n"
"ds_read_b64_tr_b16 a[242:243], v245 offset:17920\n"
"v_mul_f32 v4, s45, v4\n"
"v_mfma_f32_16x16x32_bf16 v[220:223], a[140:143], a[188:191], v[220:223]\n"
"ds_read_b64_tr_b16 a[244:245], v245 offset:25600\n"
"v_mul_f32 v5, s45, v5\n"
"v_mfma_f32_16x16x32_bf16 v[216:219], a[128:131], a[184:187], 0\n"
"ds_read_b64_tr_b16 a[246:247], v245 offset:26112\n"
"v_mul_f32 v6, s45, v6\n"
"v_mfma_f32_16x16x32_bf16 v[216:219], a[132:135], a[188:191], v[216:219]\n"
"ds_read_b64_tr_b16 a[248:249], v246 offset:17408\n"
"v_mul_f32 v7, s45, v7\n"
"v_mfma_f32_16x16x32_bf16 v[212:215], a[104:107], a[184:187], 0\n"
"ds_read_b64_tr_b16 a[250:251], v246 offset:17920\n"
"v_cvt_pk_bf16_f32 v0, v0, v1\n"
"v_mfma_f32_16x16x32_bf16 v[212:215], a[108:111], a[188:191], v[212:215]\n"
"ds_read_b64_tr_b16 a[252:253], v246 offset:25600\n"
"v_cvt_pk_bf16_f32 v1, v2, v3\n"
"v_mfma_f32_16x16x32_bf16 v[208:211], a[96:99], a[184:187], 0\n"
"ds_read_b64_tr_b16 a[254:255], v246 offset:26112\n"
"v_cvt_pk_bf16_f32 v2, v4, v5\n"
"v_cvt_pk_bf16_f32 v3, v6, v7\n"
"v_mfma_f32_16x16x32_bf16 v[208:211], a[100:103], a[188:191], v[208:211]\n"
"v_permlane16_swap_b32 v0, v2\n"
"v_permlane16_swap_b32 v1, v3\n"
"v_accvgpr_write_b32 a48, v0\n"
"v_accvgpr_write_b32 a49, v1\n"
"v_accvgpr_write_b32 a50, v2\n"
"v_accvgpr_write_b32 a51, v3\n"
"v_mfma_f32_16x16x32_bf16 v[0:3], a[192:195], a[160:163], 0\n"
"v_mul_f32 v8, s45, v8\n"
"v_mul_f32 v9, s45, v9\n"
"v_mfma_f32_16x16x32_bf16 v[0:3], a[196:199], a[164:167], v[0:3]\n"
"v_mul_f32 v10, s45, v10\n"
"v_mul_f32 v11, s45, v11\n"
"v_mfma_f32_16x16x32_bf16 v[4:7], a[200:203], a[160:163], 0\n"
"v_mul_f32 v12, s45, v12\n"
"v_mul_f32 v13, s45, v13\n"
"v_mul_f32 v14, s45, v14\n"
"v_mul_f32 v15, s45, v15\n"
"v_cvt_pk_bf16_f32 v8, v8, v9\n"
"v_cvt_pk_bf16_f32 v9, v10, v11\n"
"v_cvt_pk_bf16_f32 v10, v12, v13\n"
"v_cvt_pk_bf16_f32 v11, v14, v15\n"
"v_mfma_f32_16x16x32_bf16 v[4:7], a[204:207], a[164:167], v[4:7]\n"
"s_waitcnt vmcnt(8) lgkmcnt(0)\n"
"v_permlane16_swap_b32 v8, v10\n"
"v_permlane16_swap_b32 v9, v11\n"
"v_accvgpr_write_b32 a52, v8\n"
"v_accvgpr_write_b32 a53, v9\n"
"v_accvgpr_write_b32 a54, v10\n"
"v_accvgpr_write_b32 a55, v11\n"
"v_mfma_f32_16x16x32_bf16 v[8:11], a[208:211], a[160:163], 0\n"
"s_barrier\n"
"v_mul_f32 v16, s45, v16\n"
"v_mfma_f32_16x16x32_bf16 v[8:11], a[212:215], a[164:167], v[8:11]\n"
"s_mov_b32 m0, s51\n"
"v_mul_f32 v17, s45, v17\n"
"v_mul_f32 v18, s45, v18\n"
"v_mfma_f32_16x16x32_bf16 v[12:15], a[216:219], a[160:163], 0\n"
"buffer_load_dwordx4 v247, s[24:27], s49 offen lds\n"
"v_mul_f32 v19, s45, v19\n"
"v_mul_f32 v20, s45, v20\n"
"v_mul_f32 v21, s45, v21\n"
"v_mul_f32 v22, s45, v22\n"
"v_mul_f32 v23, s45, v23\n"
"v_cvt_pk_bf16_f32 v16, v16, v17\n"
"v_cvt_pk_bf16_f32 v17, v18, v19\n"
"v_cvt_pk_bf16_f32 v18, v20, v21\n"
"v_cvt_pk_bf16_f32 v19, v22, v23\n"
"v_mfma_f32_16x16x32_bf16 v[12:15], a[220:223], a[164:167], v[12:15]\n"
"v_permlane16_swap_b32 v16, v18\n"
"v_permlane16_swap_b32 v17, v19\n"
"v_accvgpr_write_b32 a56, v16\n"
"v_accvgpr_write_b32 a57, v17\n"
"v_accvgpr_write_b32 a58, v18\n"
"v_accvgpr_write_b32 a59, v19\n"
"v_mfma_f32_16x16x32_bf16 v[16:19], a[224:227], a[160:163], 0\n"
"v_mul_f32 v24, s45, v24\n"
"v_mul_f32 v25, s45, v25\n"
"v_mfma_f32_16x16x32_bf16 v[16:19], a[228:231], a[164:167], v[16:19]\n"
"v_mul_f32 v26, s45, v26\n"
"v_mul_f32 v27, s45, v27\n"
"v_mfma_f32_16x16x32_bf16 v[20:23], a[232:235], a[160:163], 0\n"
"v_mul_f32 v28, s45, v28\n"
"v_mul_f32 v29, s45, v29\n"
"v_mul_f32 v30, s45, v30\n"
"v_mul_f32 v31, s45, v31\n"
"v_cvt_pk_bf16_f32 v24, v24, v25\n"
"v_cvt_pk_bf16_f32 v25, v26, v27\n"
"v_cvt_pk_bf16_f32 v26, v28, v29\n"
"v_cvt_pk_bf16_f32 v27, v30, v31\n"
"v_mfma_f32_16x16x32_bf16 v[20:23], a[236:239], a[164:167], v[20:23]\n"
"buffer_load_dwordx4 v253, s[24:27], s49 offen offset:1024 lds\n"
"v_permlane16_swap_b32 v24, v26\n"
"v_permlane16_swap_b32 v25, v27\n"
"v_accvgpr_write_b32 a60, v24\n"
"v_accvgpr_write_b32 a61, v25\n"
"v_accvgpr_write_b32 a62, v26\n"
"v_accvgpr_write_b32 a63, v27\n"
"v_mfma_f32_16x16x32_bf16 v[24:27], a[240:243], a[160:163], 0\n"
"v_mul_f32 v56, s45, v56\n"
"v_mul_f32 v57, s45, v57\n"
"v_mfma_f32_16x16x32_bf16 v[24:27], a[244:247], a[164:167], v[24:27]\n"
"v_mul_f32 v58, s45, v58\n"
"v_mul_f32 v59, s45, v59\n"
"v_mfma_f32_16x16x32_bf16 v[28:31], a[248:251], a[160:163], 0\n"
"v_mul_f32 v60, s45, v60\n"
"v_mul_f32 v61, s45, v61\n"
"v_mfma_f32_16x16x32_bf16 v[28:31], a[252:255], a[164:167], v[28:31]\n"
"v_mul_f32 v62, s45, v62\n"
"v_mul_f32 v63, s45, v63\n"
"v_cvt_pk_bf16_f32 v56, v56, v57\n"
"v_cvt_pk_bf16_f32 v57, v58, v59\n"
"v_cvt_pk_bf16_f32 v58, v60, v61\n"
"v_cvt_pk_bf16_f32 v59, v62, v63\n"
"v_mfma_f32_16x16x32_bf16 v[60:63], a[248:251], a[168:171], 0\n"
"buffer_load_dwordx4 v254, s[24:27], s49 offen offset:2048 lds\n"
"v_mfma_f32_16x16x32_bf16 v[60:63], a[252:255], a[172:175], v[60:63]\n"
"v_permlane16_swap_b32 v56, v58\n"
"v_permlane16_swap_b32 v57, v59\n"
"v_accvgpr_write_b32 a76, v56\n"
"v_accvgpr_write_b32 a77, v57\n"
"v_accvgpr_write_b32 a78, v58\n"
"v_accvgpr_write_b32 a79, v59\n"
"v_mfma_f32_16x16x32_bf16 v[56:59], a[240:243], a[168:171], 0\n"
"v_mul_f32 v48, s45, v48\n"
"v_mul_f32 v49, s45, v49\n"
"v_mfma_f32_16x16x32_bf16 v[56:59], a[244:247], a[172:175], v[56:59]\n"
"v_mul_f32 v50, s45, v50\n"
"v_mul_f32 v51, s45, v51\n"
"v_mul_f32 v52, s45, v52\n"
"v_mul_f32 v53, s45, v53\n"
"v_mul_f32 v54, s45, v54\n"
"v_mul_f32 v55, s45, v55\n"
"v_cvt_pk_bf16_f32 v48, v48, v49\n"
"v_cvt_pk_bf16_f32 v49, v50, v51\n"
"v_cvt_pk_bf16_f32 v50, v52, v53\n"
"v_cvt_pk_bf16_f32 v51, v54, v55\n"
"v_mfma_f32_16x16x32_bf16 v[52:55], a[232:235], a[168:171], 0\n"
"v_permlane16_swap_b32 v48, v50\n"
"v_mfma_f32_16x16x32_bf16 v[52:55], a[236:239], a[172:175], v[52:55]\n"
"buffer_load_dwordx4 v255, s[24:27], s49 offen offset:3072 lds\n"
"v_permlane16_swap_b32 v49, v51\n"
"v_accvgpr_write_b32 a72, v48\n"
"v_accvgpr_write_b32 a73, v49\n"
"v_accvgpr_write_b32 a74, v50\n"
"v_accvgpr_write_b32 a75, v51\n"
"v_mfma_f32_16x16x32_bf16 v[48:51], a[224:227], a[168:171], 0\n"
"v_mul_f32 v40, s45, v40\n"
"v_mul_f32 v41, s45, v41\n"
"v_mfma_f32_16x16x32_bf16 v[48:51], a[228:231], a[172:175], v[48:51]\n"
"v_mul_f32 v42, s45, v42\n"
"v_mul_f32 v43, s45, v43\n"
"v_mul_f32 v44, s45, v44\n"
"v_mul_f32 v45, s45, v45\n"
"v_mul_f32 v46, s45, v46\n"
"v_mul_f32 v47, s45, v47\n"
"v_cvt_pk_bf16_f32 v40, v40, v41\n"
"v_cvt_pk_bf16_f32 v41, v42, v43\n"
"v_cvt_pk_bf16_f32 v42, v44, v45\n"
"v_cvt_pk_bf16_f32 v43, v46, v47\n"
"v_mfma_f32_16x16x32_bf16 v[44:47], a[216:219], a[168:171], 0\n"
"v_permlane16_swap_b32 v40, v42\n"
"v_mfma_f32_16x16x32_bf16 v[44:47], a[220:223], a[172:175], v[44:47]\n"
"s_add_u32 m0, s51, 4096\n"
"v_permlane16_swap_b32 v41, v43\n"
"v_accvgpr_write_b32 a68, v40\n"
"v_accvgpr_write_b32 a69, v41\n"
"v_accvgpr_write_b32 a70, v42\n"
"v_accvgpr_write_b32 a71, v43\n"
"v_mfma_f32_16x16x32_bf16 v[40:43], a[208:211], a[168:171], 0\n"
"s_add_u32 s40, s49, 40960\n"
"buffer_load_dwordx4 v247, s[24:27], s40 offen lds\n"
"v_mul_f32 v104, s45, v104\n"
"v_mul_f32 v105, s45, v105\n"
"v_mul_f32 v106, s45, v106\n"
"v_mul_f32 v107, s45, v107\n"
"v_mul_f32 v108, s45, v108\n"
"v_mul_f32 v109, s45, v109\n"
"v_mul_f32 v110, s45, v110\n"
"v_mul_f32 v111, s45, v111\n"
"v_cvt_pk_bf16_f32 v104, v104, v105\n"
"v_cvt_pk_bf16_f32 v105, v106, v107\n"
"v_cvt_pk_bf16_f32 v106, v108, v109\n"
"v_cvt_pk_bf16_f32 v107, v110, v111\n"
"v_mfma_f32_16x16x32_bf16 v[40:43], a[212:215], a[172:175], v[40:43]\n"
"v_permlane16_swap_b32 v104, v106\n"
"v_permlane16_swap_b32 v105, v107\n"
"v_mul_f32 v32, s45, v32\n"
"v_mul_f32 v33, s45, v33\n"
"v_mul_f32 v34, s45, v34\n"
"v_mul_f32 v35, s45, v35\n"
"v_mul_f32 v36, s45, v36\n"
"v_mul_f32 v37, s45, v37\n"
"v_mul_f32 v38, s45, v38\n"
"v_mul_f32 v39, s45, v39\n"
"v_cvt_pk_bf16_f32 v32, v32, v33\n"
"v_cvt_pk_bf16_f32 v33, v34, v35\n"
"v_cvt_pk_bf16_f32 v34, v36, v37\n"
"v_cvt_pk_bf16_f32 v35, v38, v39\n"
"v_mfma_f32_16x16x32_bf16 v[36:39], a[200:203], a[168:171], 0\n"
"ds_write_b128 v248, v[104:107] offset:6144\n"
"v_xor_b32 v243, s74, v243\n"
"v_mfma_f32_16x16x32_bf16 v[36:39], a[204:207], a[172:175], v[36:39]\n"
"ds_read_b64_tr_b16 a[96:97], v243 offset:0\n"
"ds_read_b64_tr_b16 a[98:99], v243 offset:512\n"
"v_permlane16_swap_b32 v32, v34\n"
"v_permlane16_swap_b32 v33, v35\n"
"v_accvgpr_write_b32 a64, v32\n"
"v_accvgpr_write_b32 a65, v33\n"
"v_accvgpr_write_b32 a66, v34\n"
"v_accvgpr_write_b32 a67, v35\n"
"v_xor_b32 v249, s74, v249\n"
"v_mfma_f32_16x16x32_bf16 v[32:35], a[192:195], a[168:171], 0\n"
"ds_read_b128 a[160:163], v249 offset:0\n"
"v_mul_f32 v64, s45, v64\n"
"v_mul_f32 v65, s45, v65\n"
"v_mul_f32 v66, s45, v66\n"
"v_mul_f32 v67, s45, v67\n"
"v_mul_f32 v68, s45, v68\n"
"v_mul_f32 v69, s45, v69\n"
"v_mul_f32 v70, s45, v70\n"
"v_mul_f32 v71, s45, v71\n"
"v_cvt_pk_bf16_f32 v64, v64, v65\n"
"v_cvt_pk_bf16_f32 v65, v66, v67\n"
"v_cvt_pk_bf16_f32 v66, v68, v69\n"
"v_cvt_pk_bf16_f32 v67, v70, v71\n"
"v_mfma_f32_16x16x32_bf16 v[32:35], a[196:199], a[172:175], v[32:35]\n"
"buffer_load_dwordx4 v253, s[24:27], s40 offen offset:1024 lds\n"
"v_permlane16_swap_b32 v64, v66\n"
"v_permlane16_swap_b32 v65, v67\n"
"v_accvgpr_write_b32 a80, v64\n"
"v_accvgpr_write_b32 a81, v65\n"
"v_accvgpr_write_b32 a82, v66\n"
"v_accvgpr_write_b32 a83, v67\n"
"v_mfma_f32_16x16x32_bf16 v[64:67], a[192:195], a[176:179], 0\n"
"ds_read_b64_tr_b16 a[100:101], v243 offset:8192\n"
"ds_read_b64_tr_b16 a[102:103], v243 offset:8704\n"
"v_xor_b32 v250, s74, v250\n"
"v_mfma_f32_16x16x32_bf16 v[64:67], a[196:199], a[180:183], v[64:67]\n"
"ds_read_b128 a[164:167], v250 offset:0\n"
"v_mul_f32 v96, s45, v96\n"
"v_mul_f32 v97, s45, v97\n"
"v_mul_f32 v98, s45, v98\n"
"v_mul_f32 v99, s45, v99\n"
"v_mul_f32 v100, s45, v100\n"
"v_mul_f32 v101, s45, v101\n"
"v_mul_f32 v102, s45, v102\n"
"v_mul_f32 v103, s45, v103\n"
"v_cvt_pk_bf16_f32 v96, v96, v97\n"
"v_cvt_pk_bf16_f32 v97, v98, v99\n"
"v_cvt_pk_bf16_f32 v98, v100, v101\n"
"v_cvt_pk_bf16_f32 v99, v102, v103\n"
"v_xor_b32 v244, s74, v244\n"
"v_mfma_f32_16x16x32_bf16 v[68:71], a[200:203], a[176:179], 0\n"
"ds_read_b64_tr_b16 a[104:105], v244 offset:0\n"
"ds_read_b64_tr_b16 a[106:107], v244 offset:512\n"
"v_permlane16_swap_b32 v96, v98\n"
"v_permlane16_swap_b32 v97, v99\n"
"v_mul_f32 v72, s45, v72\n"
"v_mul_f32 v73, s45, v73\n"
"v_mul_f32 v74, s45, v74\n"
"v_mul_f32 v75, s45, v75\n"
"v_mul_f32 v76, s45, v76\n"
"v_mul_f32 v77, s45, v77\n"
"v_mul_f32 v78, s45, v78\n"
"v_mul_f32 v79, s45, v79\n"
"v_cvt_pk_bf16_f32 v72, v72, v73\n"
"v_cvt_pk_bf16_f32 v73, v74, v75\n"
"v_cvt_pk_bf16_f32 v74, v76, v77\n"
"v_cvt_pk_bf16_f32 v75, v78, v79\n"
"v_mfma_f32_16x16x32_bf16 v[68:71], a[204:207], a[180:183], v[68:71]\n"
"ds_write_b128 v248, v[96:99] offset:4096\n"
"v_permlane16_swap_b32 v72, v74\n"
"v_permlane16_swap_b32 v73, v75\n"
"v_accvgpr_write_b32 a84, v72\n"
"v_accvgpr_write_b32 a85, v73\n"
"v_accvgpr_write_b32 a86, v74\n"
"v_accvgpr_write_b32 a87, v75\n"
"v_mfma_f32_16x16x32_bf16 v[72:75], a[208:211], a[176:179], 0\n"
"buffer_load_dwordx4 v254, s[24:27], s40 offen offset:2048 lds\n"
"v_mfma_f32_16x16x32_bf16 v[72:75], a[212:215], a[180:183], v[72:75]\n"
"s_add_u32 s49, s49, 128\n"
"ds_read_b64_tr_b16 a[108:109], v244 offset:8192\n"
"ds_read_b64_tr_b16 a[110:111], v244 offset:8704\n"
"v_xor_b32 v245, s74, v245\n"
"v_mfma_f32_16x16x32_bf16 v[76:79], a[216:219], a[176:179], 0\n"
"s_sub_u32 s41, s49, 1280\n"
"ds_read_b64_tr_b16 a[112:113], v245 offset:0\n"
"ds_read_b64_tr_b16 a[114:115], v245 offset:512\n"
"v_mul_f32 v88, s45, v88\n"
"v_mul_f32 v89, s45, v89\n"
"v_mul_f32 v90, s45, v90\n"
"v_mul_f32 v91, s45, v91\n"
"v_mul_f32 v92, s45, v92\n"
"v_mul_f32 v93, s45, v93\n"
"v_mul_f32 v94, s45, v94\n"
"v_mul_f32 v95, s45, v95\n"
"v_cvt_pk_bf16_f32 v88, v88, v89\n"
"v_cvt_pk_bf16_f32 v89, v90, v91\n"
"v_cvt_pk_bf16_f32 v90, v92, v93\n"
"v_cvt_pk_bf16_f32 v91, v94, v95\n"
"v_mfma_f32_16x16x32_bf16 v[76:79], a[220:223], a[180:183], v[76:79]\n"
"s_min_u32 s49, s49, s41\n"
"ds_read_b64_tr_b16 a[116:117], v245 offset:8192\n"
"ds_read_b64_tr_b16 a[118:119], v245 offset:8704\n"
"v_permlane16_swap_b32 v88, v90\n"
"v_permlane16_swap_b32 v89, v91\n"
"v_accvgpr_write_b32 a92, v88\n"
"v_accvgpr_write_b32 a93, v89\n"
"v_accvgpr_write_b32 a94, v90\n"
"v_accvgpr_write_b32 a95, v91\n"
"v_xor_b32 v246, s74, v246\n"
"v_mfma_f32_16x16x32_bf16 v[88:91], a[240:243], a[176:179], 0\n"
"s_xor_b32 s51, s51, s74\n"
"ds_read_b64_tr_b16 a[120:121], v246 offset:0\n"
"ds_read_b64_tr_b16 a[122:123], v246 offset:512\n"
"v_mfma_f32_16x16x32_bf16 v[88:91], a[244:247], a[180:183], v[88:91]\n"
"ds_read_b64_tr_b16 a[124:125], v246 offset:8192\n"
"ds_read_b64_tr_b16 a[126:127], v246 offset:8704\n"
"v_mfma_f32_16x16x32_bf16 v[92:95], a[248:251], a[176:179], 0\n"
"ds_read_b64_tr_b16 a[128:129], v243 offset:1024\n"
"ds_read_b64_tr_b16 a[130:131], v243 offset:1536\n"
"v_mul_f32 v80, s45, v80\n"
"v_mul_f32 v81, s45, v81\n"
"v_mul_f32 v82, s45, v82\n"
"v_mul_f32 v83, s45, v83\n"
"v_mul_f32 v84, s45, v84\n"
"v_mul_f32 v85, s45, v85\n"
"v_mul_f32 v86, s45, v86\n"
"v_mul_f32 v87, s45, v87\n"
"v_cvt_pk_bf16_f32 v80, v80, v81\n"
"v_cvt_pk_bf16_f32 v81, v82, v83\n"
"v_cvt_pk_bf16_f32 v82, v84, v85\n"
"v_cvt_pk_bf16_f32 v83, v86, v87\n"
"v_mfma_f32_16x16x32_bf16 v[92:95], a[252:255], a[180:183], v[92:95]\n"
"ds_read_b64_tr_b16 a[132:133], v243 offset:9216\n"
"ds_read_b64_tr_b16 a[134:135], v243 offset:9728\n"
"v_permlane16_swap_b32 v80, v82\n"
"v_permlane16_swap_b32 v81, v83\n"
"v_accvgpr_write_b32 a88, v80\n"
"v_accvgpr_write_b32 a89, v81\n"
"v_accvgpr_write_b32 a90, v82\n"
"v_accvgpr_write_b32 a91, v83\n"
"v_mfma_f32_16x16x32_bf16 v[80:83], a[224:227], a[176:179], 0\n"
"ds_read_b64_tr_b16 a[136:137], v244 offset:1024\n"
"ds_read_b64_tr_b16 a[138:139], v244 offset:1536\n"
"v_mfma_f32_16x16x32_bf16 v[80:83], a[228:231], a[180:183], v[80:83]\n"
"ds_read_b64_tr_b16 a[140:141], v244 offset:9216\n"
"ds_read_b64_tr_b16 a[142:143], v244 offset:9728\n"
"v_mfma_f32_16x16x32_bf16 v[84:87], a[232:235], a[176:179], 0\n"
"ds_read_b64_tr_b16 a[144:145], v245 offset:1024\n"
"ds_read_b64_tr_b16 a[146:147], v245 offset:1536\n"
"v_mfma_f32_16x16x32_bf16 v[84:87], a[236:239], a[180:183], v[84:87]\n"
"ds_read_b64_tr_b16 a[148:149], v245 offset:9216\n"
"ds_read_b64_tr_b16 a[150:151], v245 offset:9728\n"
"v_mfma_f32_16x16x32_bf16 v[108:111], a[232:235], a[184:187], 0\n"
"ds_read_b64_tr_b16 a[152:153], v246 offset:1024\n"
"ds_read_b64_tr_b16 a[154:155], v246 offset:1536\n"
"v_mfma_f32_16x16x32_bf16 v[108:111], a[236:239], a[188:191], v[108:111]\n"
"ds_read_b64_tr_b16 a[156:157], v246 offset:9216\n"
"ds_read_b64_tr_b16 a[158:159], v246 offset:9728\n"
"v_mfma_f32_16x16x32_bf16 v[104:107], a[224:227], a[184:187], 0\n"
"ds_read_b128 a[168:171], v249 offset:2048\n"
"v_xor_b32 v251, s74, v251\n"
"v_mfma_f32_16x16x32_bf16 v[104:107], a[228:231], a[188:191], v[104:107]\n"
"ds_read_b128 a[172:175], v250 offset:2048\n"
"v_xor_b32 v252, s74, v252\n"
"v_mfma_f32_16x16x32_bf16 v[100:103], a[200:203], a[184:187], 0\n"
"ds_read_b128 a[176:179], v249 offset:4096\n"
"v_mfma_f32_16x16x32_bf16 v[100:103], a[204:207], a[188:191], v[100:103]\n"
"ds_read_b128 a[180:183], v250 offset:4096\n"
"v_mfma_f32_16x16x32_bf16 v[96:99], a[192:195], a[184:187], 0\n"
"s_add_u32 s47, s47, 1\n"
"ds_read_b128 a[184:187], v251 offset:0\n"
"v_mfma_f32_16x16x32_bf16 v[96:99], a[196:199], a[188:191], v[96:99]\n"
"ds_read_b128 a[188:191], v252 offset:0\n"
"s_mov_b32 s62, 18\n"
"s_branch LPL_%=\n"
"LPT2_%=:\n"
"s_waitcnt lgkmcnt(2)\n"
"v_mfma_f32_16x16x32_bf16 v[112:115], a[96:99], a[160:163], v[112:115]\n"
"v_bfe_i32 v241, v242, 4, 1\n"
"v_and_b32 v241, 0xaaaaaaaa, v241\n"
"v_mfma_f32_16x16x32_bf16 v[112:115], a[100:103], a[164:167], v[112:115]\n"
"v_or_b32 v241, 0x44444444, v241\n"
"v_bfe_u32 v247, v242, 4, 2\n"
"v_mfma_f32_16x16x32_bf16 v[116:119], a[104:107], a[160:163], v[116:119]\n"
"v_mul_u32_u24 v247, 20, v247\n"
"v_mfma_f32_16x16x32_bf16 v[116:119], a[108:111], a[164:167], v[116:119]\n"
"s_waitcnt vmcnt(11)\n"
"v_mfma_f32_16x16x32_bf16 v[120:123], a[112:115], a[160:163], v[120:123]\n"
"s_barrier\n"
"ds_read_b64_tr_b16 a[192:193], v243 offset:16384\n"
"v_mfma_f32_16x16x32_bf16 v[120:123], a[116:119], a[164:167], v[120:123]\n"
"s_xor_b32 m0, s50, 0xc000\n"
"ds_read_b64_tr_b16 a[194:195], v243 offset:16896\n"
"v_mfma_f32_16x16x32_bf16 v[124:127], a[120:123], a[160:163], v[124:127]\n"
"s_add_u32 s40, s48, 256\n"
"buffer_load_dwordx4 v224, s[28:31], s40 offen lds\n"
"v_mfma_f32_16x16x32_bf16 v[124:127], a[124:127], a[164:167], v[124:127]\n"
"ds_read_b64_tr_b16 a[196:197], v243 offset:24576\n"
"v_mfma_f32_16x16x32_bf16 v[128:131], a[128:131], a[160:163], v[128:131]\n"
"ds_read_b64_tr_b16 a[198:199], v243 offset:25088\n"
"v_mfma_f32_16x16x32_bf16 v[128:131], a[132:135], a[164:167], v[128:131]\n"
"ds_read_b64_tr_b16 a[200:201], v244 offset:16384\n"
"v_mfma_f32_16x16x32_bf16 v[132:135], a[136:139], a[160:163], v[132:135]\n"
"ds_read_b64_tr_b16 a[202:203], v244 offset:16896\n"
"v_mfma_f32_16x16x32_bf16 v[132:135], a[140:143], a[164:167], v[132:135]\n"
"ds_read_b64_tr_b16 a[204:205], v244 offset:24576\n"
"v_mfma_f32_16x16x32_bf16 v[136:139], a[144:147], a[160:163], v[136:139]\n"
"buffer_load_dwordx4 v225, s[28:31], s40 offen offset:1024 lds\n"
"v_mfma_f32_16x16x32_bf16 v[136:139], a[148:151], a[164:167], v[136:139]\n"
"ds_read_b64_tr_b16 a[206:207], v244 offset:25088\n"
"v_mfma_f32_16x16x32_bf16 v[140:143], a[152:155], a[160:163], v[140:143]\n"
"ds_read_b64_tr_b16 a[208:209], v245 offset:16384\n"
"v_mfma_f32_16x16x32_bf16 v[140:143], a[156:159], a[164:167], v[140:143]\n"
"ds_read_b64_tr_b16 a[210:211], v245 offset:16896\n"
"v_mfma_f32_16x16x32_bf16 v[172:175], a[152:155], a[168:171], v[172:175]\n"
"ds_read_b64_tr_b16 a[212:213], v245 offset:24576\n"
"v_mfma_f32_16x16x32_bf16 v[172:175], a[156:159], a[172:175], v[172:175]\n"
"ds_read_b64_tr_b16 a[214:215], v245 offset:25088\n"
"v_mfma_f32_16x16x32_bf16 v[168:171], a[144:147], a[168:171], v[168:171]\n"
"ds_read_b64_tr_b16 a[216:217], v246 offset:16384\n"
"v_mfma_f32_16x16x32_bf16 v[168:171], a[148:151], a[172:175], v[168:171]\n"
"buffer_load_dwordx4 v226, s[28:31], s40 offen offset:2048 lds\n"
"v_mfma_f32_16x16x32_bf16 v[164:167], a[136:139], a[168:171], v[164:167]\n"
"ds_read_b64_tr_b16 a[218:219], v246 offset:16896\n"
"v_mfma_f32_16x16x32_bf16 v[164:167], a[140:143], a[172:175], v[164:167]\n"
"ds_read_b64_tr_b16 a[220:221], v246 offset:24576\n"
"v_mfma_f32_16x16x32_bf16 v[160:163], a[128:131], a[168:171], v[160:163]\n"
"ds_read_b64_tr_b16 a[222:223], v246 offset:25088\n"
"v_mfma_f32_16x16x32_bf16 v[160:163], a[132:135], a[172:175], v[160:163]\n"
"ds_read_b64_tr_b16 a[224:225], v243 offset:17408\n"
"v_mfma_f32_16x16x32_bf16 v[156:159], a[120:123], a[168:171], v[156:159]\n"
"ds_read_b64_tr_b16 a[226:227], v243 offset:17920\n"
"v_mfma_f32_16x16x32_bf16 v[156:159], a[124:127], a[172:175], v[156:159]\n"
"buffer_load_dwordx4 v227, s[28:31], s40 offen offset:3072 lds\n"
"v_mfma_f32_16x16x32_bf16 v[152:155], a[112:115], a[168:171], v[152:155]\n"
"s_mov_b32 s58, 245760\n"
"ds_read_b64_tr_b16 a[228:229], v243 offset:25600\n"
"v_mfma_f32_16x16x32_bf16 v[152:155], a[116:119], a[172:175], v[152:155]\n"
"s_mov_b32 s47, 0\n"
"ds_read_b64_tr_b16 a[230:231], v243 offset:26112\n"
"v_mfma_f32_16x16x32_bf16 v[148:151], a[104:107], a[168:171], v[148:151]\n"
"ds_read_b64_tr_b16 a[232:233], v244 offset:17408\n"
"v_mfma_f32_16x16x32_bf16 v[148:151], a[108:111], a[172:175], v[148:151]\n"
"ds_read_b64_tr_b16 a[234:235], v244 offset:17920\n"
"v_mfma_f32_16x16x32_bf16 v[144:147], a[96:99], a[168:171], v[144:147]\n"
"ds_read_b64_tr_b16 a[236:237], v244 offset:25600\n"
"v_xor_b32 v243, s74, v243\n"
"v_mfma_f32_16x16x32_bf16 v[144:147], a[100:103], a[172:175], v[144:147]\n"
"s_mov_b32 m0, s44\n"
"ds_read_b64_tr_b16 a[238:239], v244 offset:26112\n"
"v_xor_b32 v249, s74, v249\n"
"v_mfma_f32_16x16x32_bf16 v[176:179], a[96:99], a[176:179], v[176:179]\n"
"buffer_load_dwordx4 v240, s[16:19], s56 offen nt lds\n"
"v_mfma_f32_16x16x32_bf16 v[176:179], a[100:103], a[180:183], v[176:179]\n"
"ds_read_b64_tr_b16 a[240:241], v245 offset:17408\n"
"v_xor_b32 v250, s74, v250\n"
"v_mfma_f32_16x16x32_bf16 v[180:183], a[104:107], a[176:179], v[180:183]\n"
"ds_read_b64_tr_b16 a[242:243], v245 offset:17920\n"
"v_xor_b32 v244, s74, v244\n"
"v_mfma_f32_16x16x32_bf16 v[180:183], a[108:111], a[180:183], v[180:183]\n"
"ds_read_b64_tr_b16 a[244:245], v245 offset:25600\n"
"v_xor_b32 v251, s74, v251\n"
"v_mfma_f32_16x16x32_bf16 v[184:187], a[112:115], a[176:179], v[184:187]\n"
"ds_read_b64_tr_b16 a[246:247], v245 offset:26112\n"
"v_xor_b32 v245, s74, v245\n"
"v_mfma_f32_16x16x32_bf16 v[184:187], a[116:119], a[180:183], v[184:187]\n"
"ds_read_b64_tr_b16 a[248:249], v246 offset:17408\n"
"v_xor_b32 v252, s74, v252\n"
"v_mfma_f32_16x16x32_bf16 v[188:191], a[120:123], a[176:179], v[188:191]\n"
"ds_read_b64_tr_b16 a[250:251], v246 offset:17920\n"
"v_mfma_f32_16x16x32_bf16 v[188:191], a[124:127], a[180:183], v[188:191]\n"
"s_add_u32 s40, s56, s52\n"
"buffer_load_dwordx4 v240, s[16:19], s40 offen offset:1024 nt lds\n"
"v_mfma_f32_16x16x32_bf16 v[200:203], a[144:147], a[176:179], v[200:203]\n"
"ds_read_b64_tr_b16 a[252:253], v246 offset:25600\n"
"v_mfma_f32_16x16x32_bf16 v[200:203], a[148:151], a[180:183], v[200:203]\n"
"ds_read_b64_tr_b16 a[254:255], v246 offset:26112\n"
"v_xor_b32 v246, s74, v246\n"
"v_mfma_f32_16x16x32_bf16 v[204:207], a[152:155], a[176:179], v[204:207]\n"
"v_mfma_f32_16x16x32_bf16 v[204:207], a[156:159], a[180:183], v[204:207]\n"
"v_mfma_f32_16x16x32_bf16 v[192:195], a[128:131], a[176:179], v[192:195]\n"
"v_mfma_f32_16x16x32_bf16 v[192:195], a[132:135], a[180:183], v[192:195]\n"
"s_add_u32 s40, s56, s53\n"
"buffer_load_dwordx4 v240, s[16:19], s40 offen offset:2048 nt lds\n"
"v_mfma_f32_16x16x32_bf16 v[196:199], a[136:139], a[176:179], v[196:199]\n"
"v_mfma_f32_16x16x32_bf16 v[196:199], a[140:143], a[180:183], v[196:199]\n"
"s_waitcnt lgkmcnt(0)\n"
"v_mfma_f32_16x16x32_bf16 v[220:223], a[136:139], a[184:187], v[220:223]\n"
"v_mfma_f32_16x16x32_bf16 v[220:223], a[140:143], a[188:191], v[220:223]\n"
"v_mfma_f32_16x16x32_bf16 v[216:219], a[128:131], a[184:187], v[216:219]\n"
"v_mfma_f32_16x16x32_bf16 v[216:219], a[132:135], a[188:191], v[216:219]\n"
"v_mfma_f32_16x16x32_bf16 v[212:215], a[104:107], a[184:187], v[212:215]\n"
"s_add_u32 s40, s56, s54\n"
"buffer_load_dwordx4 v240, s[16:19], s40 offen offset:3072 nt lds\n"
"v_mfma_f32_16x16x32_bf16 v[212:215], a[108:111], a[188:191], v[212:215]\n"
"v_mfma_f32_16x16x32_bf16 v[208:211], a[96:99], a[184:187], v[208:211]\n"
"v_mfma_f32_16x16x32_bf16 v[208:211], a[100:103], a[188:191], v[208:211]\n"
"v_mfma_f32_16x16x32_bf16 v[0:3], a[192:195], a[160:163], v[0:3]\n"
"v_mfma_f32_16x16x32_bf16 v[0:3], a[196:199], a[164:167], v[0:3]\n"
"v_mfma_f32_16x16x32_bf16 v[4:7], a[200:203], a[160:163], v[4:7]\n"
"v_mfma_f32_16x16x32_bf16 v[4:7], a[204:207], a[164:167], v[4:7]\n"
"s_waitcnt vmcnt(8)\n"
"v_mfma_f32_16x16x32_bf16 v[8:11], a[208:211], a[160:163], v[8:11]\n"
"s_barrier\n"
"ds_read_b64_tr_b16 a[96:97], v243 offset:0\n"
"v_mfma_f32_16x16x32_bf16 v[8:11], a[212:215], a[164:167], v[8:11]\n"
"s_add_u32 m0, s44, 65536\n"
"ds_read_b64_tr_b16 a[98:99], v243 offset:512\n"
"v_mfma_f32_16x16x32_bf16 v[12:15], a[216:219], a[160:163], v[12:15]\n"
"buffer_load_dwordx4 v240, s[20:23], s56 offen nt lds\n"
"v_mfma_f32_16x16x32_bf16 v[12:15], a[220:223], a[164:167], v[12:15]\n"
"ds_read_b64_tr_b16 a[100:101], v243 offset:8192\n"
"v_mfma_f32_16x16x32_bf16 v[16:19], a[224:227], a[160:163], v[16:19]\n"
"ds_read_b64_tr_b16 a[102:103], v243 offset:8704\n"
"v_mfma_f32_16x16x32_bf16 v[16:19], a[228:231], a[164:167], v[16:19]\n"
"ds_read_b64_tr_b16 a[104:105], v244 offset:0\n"
"v_mfma_f32_16x16x32_bf16 v[20:23], a[232:235], a[160:163], v[20:23]\n"
"ds_read_b64_tr_b16 a[106:107], v244 offset:512\n"
"v_mfma_f32_16x16x32_bf16 v[20:23], a[236:239], a[164:167], v[20:23]\n"
"ds_read_b64_tr_b16 a[108:109], v244 offset:8192\n"
"v_mfma_f32_16x16x32_bf16 v[24:27], a[240:243], a[160:163], v[24:27]\n"
"s_add_u32 s40, s56, s52\n"
"buffer_load_dwordx4 v240, s[20:23], s40 offen offset:1024 nt lds\n"
"v_mfma_f32_16x16x32_bf16 v[24:27], a[244:247], a[164:167], v[24:27]\n"
"ds_read_b64_tr_b16 a[110:111], v244 offset:8704\n"
"v_mfma_f32_16x16x32_bf16 v[28:31], a[248:251], a[160:163], v[28:31]\n"
"ds_read_b128 a[160:163], v249 offset:0\n"
"v_mfma_f32_16x16x32_bf16 v[28:31], a[252:255], a[164:167], v[28:31]\n"
"ds_read_b128 a[164:167], v250 offset:0\n"
"v_mfma_f32_16x16x32_bf16 v[60:63], a[248:251], a[168:171], v[60:63]\n"
"ds_read_b64_tr_b16 a[112:113], v245 offset:0\n"
"v_mfma_f32_16x16x32_bf16 v[60:63], a[252:255], a[172:175], v[60:63]\n"
"ds_read_b64_tr_b16 a[114:115], v245 offset:512\n"
"v_mfma_f32_16x16x32_bf16 v[56:59], a[240:243], a[168:171], v[56:59]\n"
"ds_read_b64_tr_b16 a[116:117], v245 offset:8192\n"
"v_mfma_f32_16x16x32_bf16 v[56:59], a[244:247], a[172:175], v[56:59]\n"
"s_add_u32 s40, s56, s53\n"
"buffer_load_dwordx4 v240, s[20:23], s40 offen offset:2048 nt lds\n"
"v_mfma_f32_16x16x32_bf16 v[52:55], a[232:235], a[168:171], v[52:55]\n"
"ds_read_b64_tr_b16 a[118:119], v245 offset:8704\n"
"v_mfma_f32_16x16x32_bf16 v[52:55], a[236:239], a[172:175], v[52:55]\n"
"ds_read_b64_tr_b16 a[120:121], v246 offset:0\n"
"v_mfma_f32_16x16x32_bf16 v[48:51], a[224:227], a[168:171], v[48:51]\n"
"ds_read_b64_tr_b16 a[122:123], v246 offset:512\n"
"v_mfma_f32_16x16x32_bf16 v[48:51], a[228:231], a[172:175], v[48:51]\n"
"ds_read_b64_tr_b16 a[124:125], v246 offset:8192\n"
"v_mfma_f32_16x16x32_bf16 v[44:47], a[216:219], a[168:171], v[44:47]\n"
"ds_read_b64_tr_b16 a[126:127], v246 offset:8704\n"
"v_mfma_f32_16x16x32_bf16 v[44:47], a[220:223], a[172:175], v[44:47]\n"
"s_add_u32 s40, s56, s54\n"
"buffer_load_dwordx4 v240, s[20:23], s40 offen offset:3072 nt lds\n"
"v_mfma_f32_16x16x32_bf16 v[40:43], a[208:211], a[168:171], v[40:43]\n"
"s_add_u32 s55, s55, s59\n"
"ds_read_b64_tr_b16 a[128:129], v243 offset:1024\n"
"v_mfma_f32_16x16x32_bf16 v[40:43], a[212:215], a[172:175], v[40:43]\n"
"s_add_u32 s41, s55, s57\n"
"ds_read_b64_tr_b16 a[130:131], v243 offset:1536\n"
"v_mfma_f32_16x16x32_bf16 v[36:39], a[200:203], a[168:171], v[36:39]\n"
"s_and_b32 s41, s41, s58\n"
"ds_read_b64_tr_b16 a[132:133], v243 offset:9216\n"
"v_mfma_f32_16x16x32_bf16 v[36:39], a[204:207], a[172:175], v[36:39]\n"
"s_andn2_b32 s56, s55, s58\n"
"ds_read_b64_tr_b16 a[134:135], v243 offset:9728\n"
"v_mfma_f32_16x16x32_bf16 v[32:35], a[192:195], a[168:171], v[32:35]\n"
"s_or_b32 s56, s56, s41\n"
"ds_read_b64_tr_b16 a[136:137], v244 offset:1024\n"
"v_mfma_f32_16x16x32_bf16 v[32:35], a[196:199], a[172:175], v[32:35]\n"
"s_add_u32 m0, s44, 16384\n"
"ds_read_b64_tr_b16 a[138:139], v244 offset:1536\n"
"v_mfma_f32_16x16x32_bf16 v[64:67], a[192:195], a[176:179], v[64:67]\n"
"buffer_load_dwordx4 v240, s[16:19], s56 offen nt lds\n"
"v_mfma_f32_16x16x32_bf16 v[64:67], a[196:199], a[180:183], v[64:67]\n"
"ds_read_b64_tr_b16 a[140:141], v244 offset:9216\n"
"v_mfma_f32_16x16x32_bf16 v[68:71], a[200:203], a[176:179], v[68:71]\n"
"ds_read_b64_tr_b16 a[142:143], v244 offset:9728\n"
"v_mfma_f32_16x16x32_bf16 v[68:71], a[204:207], a[180:183], v[68:71]\n"
"ds_read_b64_tr_b16 a[144:145], v245 offset:1024\n"
"v_mfma_f32_16x16x32_bf16 v[72:75], a[208:211], a[176:179], v[72:75]\n"
"ds_read_b64_tr_b16 a[146:147], v245 offset:1536\n"
"v_mfma_f32_16x16x32_bf16 v[72:75], a[212:215], a[180:183], v[72:75]\n"
"ds_read_b64_tr_b16 a[148:149], v245 offset:9216\n"
"v_mfma_f32_16x16x32_bf16 v[76:79], a[216:219], a[176:179], v[76:79]\n"
"ds_read_b64_tr_b16 a[150:151], v245 offset:9728\n"
"v_mfma_f32_16x16x32_bf16 v[76:79], a[220:223], a[180:183], v[76:79]\n"
"s_add_u32 s40, s56, s52\n"
"buffer_load_dwordx4 v240, s[16:19], s40 offen offset:1024 nt lds\n"
"v_mfma_f32_16x16x32_bf16 v[88:91], a[240:243], a[176:179], v[88:91]\n"
"ds_read_b64_tr_b16 a[152:153], v246 offset:1024\n"
"v_mfma_f32_16x16x32_bf16 v[88:91], a[244:247], a[180:183], v[88:91]\n"
"ds_read_b64_tr_b16 a[154:155], v246 offset:1536\n"
"v_mfma_f32_16x16x32_bf16 v[92:95], a[248:251], a[176:179], v[92:95]\n"
"ds_read_b64_tr_b16 a[156:157], v246 offset:9216\n"
"v_mfma_f32_16x16x32_bf16 v[92:95], a[252:255], a[180:183], v[92:95]\n"
"ds_read_b64_tr_b16 a[158:159], v246 offset:9728\n"
"v_mfma_f32_16x16x32_bf16 v[80:83], a[224:227], a[176:179], v[80:83]\n"
"ds_read_b128 a[168:171], v249 offset:2048\n"
"v_mfma_f32_16x16x32_bf16 v[80:83], a[228:231], a[180:183], v[80:83]\n"
"s_add_u32 s40, s56, s53\n"
"buffer_load_dwordx4 v240, s[16:19], s40 offen offset:2048 nt lds\n"
"v_mfma_f32_16x16x32_bf16 v[84:87], a[232:235], a[176:179], v[84:87]\n"
"ds_read_b128 a[172:175], v250 offset:2048\n"
"v_mfma_f32_16x16x32_bf16 v[84:87], a[236:239], a[180:183], v[84:87]\n"
"ds_read_b128 a[176:179], v249 offset:4096\n"
"v_and_b32 v249, 0x20, v242\n"
"v_mfma_f32_16x16x32_bf16 v[108:111], a[232:235], a[184:187], v[108:111]\n"
"ds_read_b128 a[180:183], v250 offset:4096\n"
"v_add_u32 v249, -1, v249\n"
"v_mfma_f32_16x16x32_bf16 v[108:111], a[236:239], a[188:191], v[108:111]\n"
"v_ashrrev_i32 v249, 31, v249\n"
"v_and_b32 v249, 0x3f803f80, v249\n"
"v_mfma_f32_16x16x32_bf16 v[104:107], a[224:227], a[184:187], v[104:107]\n"
"v_mfma_f32_16x16x32_bf16 v[104:107], a[228:231], a[188:191], v[104:107]\n"
"v_mfma_f32_16x16x32_bf16 v[100:103], a[200:203], a[184:187], v[100:103]\n"
"s_add_u32 s40, s56, s54\n"
"s_mov_b64 s[24:25], s[8:9]\n"
"buffer_load_dwordx4 v240, s[16:19], s40 offen offset:3072 nt lds\n"
"v_mfma_f32_16x16x32_bf16 v[100:103], a[204:207], a[188:191], v[100:103]\n"
"s_mov_b32 s26, s68\n"
"v_mfma_f32_16x16x32_bf16 v[96:99], a[192:195], a[184:187], v[96:99]\n"
"v_and_b32 v254, 0x3f0, v242\n"
"ds_read_b128 a[184:187], v251 offset:0\n"
"v_mfma_f32_16x16x32_bf16 v[96:99], a[196:199], a[188:191], v[96:99]\n"
"v_lshlrev_b32 v254, 3, v254\n"
"ds_read_b128 a[188:191], v252 offset:0\n"
"s_waitcnt lgkmcnt(2)\n"
"v_mfma_f32_16x16x32_bf16 v[112:115], a[96:99], a[160:163], v[112:115]\n"
"v_mfma_f32_16x16x32_bf16 v[112:115], a[100:103], a[164:167], v[112:115]\n"
"v_mfma_f32_16x16x32_bf16 v[116:119], a[104:107], a[160:163], v[116:119]\n"
"v_mfma_f32_16x16x32_bf16 v[116:119], a[108:111], a[164:167], v[116:119]\n"
"s_waitcnt vmcnt(12)\n"
"v_mfma_f32_16x16x32_bf16 v[120:123], a[112:115], a[160:163], v[120:123]\n"
"s_barrier\n"
"ds_read_b64_tr_b16 a[232:233], v244 offset:17408\n"
"v_mfma_f32_16x16x32_bf16 v[120:123], a[116:119], a[164:167], v[120:123]\n"
"s_add_u32 m0, s44, 81920\n"
"ds_read_b64_tr_b16 a[234:235], v244 offset:17920\n"
"v_mfma_f32_16x16x32_bf16 v[124:127], a[120:123], a[160:163], v[124:127]\n"
"buffer_load_dwordx4 v240, s[20:23], s56 offen nt lds\n"
"v_mfma_f32_16x16x32_bf16 v[124:127], a[124:127], a[164:167], v[124:127]\n"
"ds_read_b64_tr_b16 a[236:237], v244 offset:25600\n"
"v_mul_f32 v112, s45, v112\n"
"v_mfma_f32_16x16x32_bf16 v[128:131], a[128:131], a[160:163], v[128:131]\n"
"ds_read_b64_tr_b16 a[238:239], v244 offset:26112\n"
"v_mul_f32 v113, s45, v113\n"
"v_mfma_f32_16x16x32_bf16 v[128:131], a[132:135], a[164:167], v[128:131]\n"
"ds_read_b64_tr_b16 a[224:225], v243 offset:17408\n"
"v_mul_f32 v114, s45, v114\n"
"v_mfma_f32_16x16x32_bf16 v[132:135], a[136:139], a[160:163], v[132:135]\n"
"ds_read_b64_tr_b16 a[226:227], v243 offset:17920\n"
"v_mul_f32 v115, s45, v115\n"
"v_mfma_f32_16x16x32_bf16 v[132:135], a[140:143], a[164:167], v[132:135]\n"
"ds_read_b64_tr_b16 a[228:229], v243 offset:25600\n"
"v_mul_f32 v120, s45, v120\n"
"v_mfma_f32_16x16x32_bf16 v[136:139], a[144:147], a[160:163], v[136:139]\n"
"ds_read_b64_tr_b16 a[230:231], v243 offset:26112\n"
"v_mul_f32 v121, s45, v121\n"
"v_mfma_f32_16x16x32_bf16 v[136:139], a[148:151], a[164:167], v[136:139]\n"
"ds_read_b64_tr_b16 a[192:193], v243 offset:16384\n"
"v_mul_f32 v122, s45, v122\n"
"v_mfma_f32_16x16x32_bf16 v[140:143], a[152:155], a[160:163], v[140:143]\n"
"ds_read_b64_tr_b16 a[194:195], v243 offset:16896\n"
"v_mul_f32 v123, s45, v123\n"
"v_mfma_f32_16x16x32_bf16 v[140:143], a[156:159], a[164:167], v[140:143]\n"
"s_waitcnt lgkmcnt(4)\n"
"ds_read_b64_tr_b16 a[196:197], v243 offset:24576\n"
"v_mul_f32 v124, s45, v124\n"
"v_mfma_f32_16x16x32_bf16 v[220:223], a[136:139], a[184:187], v[220:223]\n"
"s_add_u32 s40, s56, s52\n"
"buffer_load_dwordx4 v240, s[20:23], s40 offen offset:1024 nt lds\n"
"v_mfma_f32_16x16x32_bf16 v[220:223], a[140:143], a[188:191], v[220:223]\n"
"ds_read_b64_tr_b16 a[198:199], v243 offset:25088\n"
"v_mul_f32 v125, s45, v125\n"
"v_mfma_f32_16x16x32_bf16 v[216:219], a[128:131], a[184:187], v[216:219]\n"
"ds_read_b64_tr_b16 a[200:201], v244 offset:16384\n"
"v_mul_f32 v126, s45, v126\n"
"v_mfma_f32_16x16x32_bf16 v[216:219], a[132:135], a[188:191], v[216:219]\n"
"ds_read_b64_tr_b16 a[202:203], v244 offset:16896\n"
"v_mul_f32 v127, s45, v127\n"
"v_mfma_f32_16x16x32_bf16 v[212:215], a[104:107], a[184:187], v[212:215]\n"
"ds_read_b64_tr_b16 a[204:205], v244 offset:24576\n"
"v_cvt_pk_bf16_f32 v120, v120, v121\n"
"v_mfma_f32_16x16x32_bf16 v[212:215], a[108:111], a[188:191], v[212:215]\n"
"ds_read_b64_tr_b16 a[206:207], v244 offset:25088\n"
"v_cvt_pk_bf16_f32 v121, v122, v123\n"
"v_mfma_f32_16x16x32_bf16 v[208:211], a[96:99], a[184:187], v[208:211]\n"
"ds_read_b64_tr_b16 a[208:209], v245 offset:16384\n"
"v_cvt_pk_bf16_f32 v122, v124, v125\n"
"v_mfma_f32_16x16x32_bf16 v[208:211], a[100:103], a[188:191], v[208:211]\n"
"ds_read_b64_tr_b16 a[210:211], v245 offset:16896\n"
"v_mul_f32 v220, s45, v220\n"
"v_mfma_f32_16x16x32_bf16 v[172:175], a[152:155], a[168:171], v[172:175]\n"
"ds_read_b64_tr_b16 a[212:213], v245 offset:24576\n"
"v_mul_f32 v221, s45, v221\n"
"v_mfma_f32_16x16x32_bf16 v[172:175], a[156:159], a[172:175], v[172:175]\n"
"ds_read_b64_tr_b16 a[214:215], v245 offset:25088\n"
"v_mul_f32 v216, s45, v216\n"
"v_mfma_f32_16x16x32_bf16 v[168:171], a[144:147], a[168:171], v[168:171]\n"
"s_add_u32 s40, s56, s53\n"
"buffer_load_dwordx4 v240, s[20:23], s40 offen offset:2048 nt lds\n"
"v_mfma_f32_16x16x32_bf16 v[168:171], a[148:151], a[172:175], v[168:171]\n"
"ds_read_b64_tr_b16 a[216:217], v246 offset:16384\n"
"v_mul_f32 v212, s45, v212\n"
"v_mfma_f32_16x16x32_bf16 v[164:167], a[136:139], a[168:171], v[164:167]\n"
"ds_read_b64_tr_b16 a[218:219], v246 offset:16896\n"
"v_mul_f32 v213, s45, v213\n"
"v_mfma_f32_16x16x32_bf16 v[164:167], a[140:143], a[172:175], v[164:167]\n"
"ds_read_b64_tr_b16 a[220:221], v246 offset:24576\n"
"v_mul_f32 v208, s45, v208\n"
"v_mfma_f32_16x16x32_bf16 v[160:163], a[128:131], a[168:171], v[160:163]\n"
"ds_read_b64_tr_b16 a[222:223], v246 offset:25088\n"
"v_mul_f32 v209, s45, v209\n"
"v_mfma_f32_16x16x32_bf16 v[160:163], a[132:135], a[172:175], v[160:163]\n"
"ds_read_b64_tr_b16 a[240:241], v245 offset:17408\n"
"v_mul_f32 v210, s45, v210\n"
"v_mfma_f32_16x16x32_bf16 v[156:159], a[120:123], a[168:171], v[156:159]\n"
"ds_read_b64_tr_b16 a[242:243], v245 offset:17920\n"
"v_mul_f32 v211, s45, v211\n"
"v_mfma_f32_16x16x32_bf16 v[156:159], a[124:127], a[172:175], v[156:159]\n"
"ds_read_b64_tr_b16 a[244:245], v245 offset:25600\n"
"v_mul_f32 v214, s45, v214\n"
"v_mfma_f32_16x16x32_bf16 v[152:155], a[112:115], a[168:171], v[152:155]\n"
"ds_read_b64_tr_b16 a[246:247], v245 offset:26112\n"
"v_mul_f32 v215, s45, v215\n"
"v_mfma_f32_16x16x32_bf16 v[152:155], a[116:119], a[172:175], v[152:155]\n"
"ds_read_b64_tr_b16 a[248:249], v246 offset:17408\n"
"v_cvt_pk_bf16_f32 v208, v208, v209\n"
"v_mfma_f32_16x16x32_bf16 v[148:151], a[104:107], a[168:171], v[148:151]\n"
"ds_read_b64_tr_b16 a[250:251], v246 offset:17920\n"
"v_cvt_pk_bf16_f32 v209, v210, v211\n"
"v_mfma_f32_16x16x32_bf16 v[148:151], a[108:111], a[172:175], v[148:151]\n"
"s_add_u32 s40, s56, s54\n"
"buffer_load_dwordx4 v240, s[20:23], s40 offen offset:3072 nt lds\n"
"v_mfma_f32_16x16x32_bf16 v[144:147], a[96:99], a[168:171], v[144:147]\n"
"s_add_u32 s55, s55, s59\n"
"ds_read_b64_tr_b16 a[252:253], v246 offset:25600\n"
"v_cvt_pk_bf16_f32 v210, v212, v213\n"
"v_mfma_f32_16x16x32_bf16 v[144:147], a[100:103], a[172:175], v[144:147]\n"
"s_add_u32 s41, s55, s57\n"
"ds_read_b64_tr_b16 a[254:255], v246 offset:26112\n"
"v_cvt_pk_bf16_f32 v211, v214, v215\n"
"v_mfma_f32_16x16x32_bf16 v[176:179], a[96:99], a[176:179], v[176:179]\n"
"s_and_b32 s41, s41, s58\n"
"v_permlane16_swap_b32 v208, v210\n"
"v_mfma_f32_16x16x32_bf16 v[176:179], a[100:103], a[180:183], v[176:179]\n"
"s_andn2_b32 s56, s55, s58\n"
"v_permlane16_swap_b32 v209, v211\n"
"v_mfma_f32_16x16x32_bf16 v[180:183], a[104:107], a[176:179], v[180:183]\n"
"s_or_b32 s56, s56, s41\n"
"ds_write_b128 v248, v[208:211] offset:8192\n"
"v_mfma_f32_16x16x32_bf16 v[180:183], a[108:111], a[180:183], v[180:183]\n"
"v_permlane16_swap_b32 v120, v122\n"
"v_mfma_f32_16x16x32_bf16 v[184:187], a[112:115], a[176:179], v[184:187]\n"
"v_mul_f32 v217, s45, v217\n"
"v_mul_f32 v218, s45, v218\n"
"v_mfma_f32_16x16x32_bf16 v[184:187], a[116:119], a[180:183], v[184:187]\n"
"v_mul_f32 v219, s45, v219\n"
"v_mul_f32 v222, s45, v222\n"
"v_mfma_f32_16x16x32_bf16 v[188:191], a[120:123], a[176:179], v[188:191]\n"
"s_add_u32 m0, s44, 32768\n"
"v_mul_f32 v223, s45, v223\n"
"v_cvt_pk_bf16_f32 v216, v216, v217\n"
"v_mfma_f32_16x16x32_bf16 v[188:191], a[124:127], a[180:183], v[188:191]\n"
"buffer_load_dwordx4 v240, s[16:19], s56 offen nt lds\n"
"v_mfma_f32_16x16x32_bf16 v[200:203], a[144:147], a[176:179], v[200:203]\n"
"v_cvt_pk_bf16_f32 v217, v218, v219\n"
"v_cvt_pk_bf16_f32 v218, v220, v221\n"
"v_mfma_f32_16x16x32_bf16 v[200:203], a[148:151], a[180:183], v[200:203]\n"
"s_nop 0\n"
"v_permlane16_swap_b32 v216, v218\n"
"v_mfma_f32_16x16x32_bf16 v[204:207], a[152:155], a[176:179], v[204:207]\n"
"v_cvt_pk_bf16_f32 v219, v222, v223\n"
"v_mul_f32 v176, s45, v176\n"
"v_mfma_f32_16x16x32_bf16 v[204:207], a[156:159], a[180:183], v[204:207]\n"
"v_permlane16_swap_b32 v217, v219\n"
"v_mfma_f32_16x16x32_bf16 v[192:195], a[128:131], a[176:179], v[192:195]\n"
"ds_write_b128 v248, v[216:219] offset:10240\n"
"v_mfma_f32_16x16x32_bf16 v[192:195], a[132:135], a[180:183], v[192:195]\n"
"v_mul_f32 v177, s45, v177\n"
"v_mul_f32 v178, s45, v178\n"
"v_mfma_f32_16x16x32_bf16 v[196:199], a[136:139], a[176:179], v[196:199]\n"
"v_mul_f32 v179, s45, v179\n"
"v_mul_f32 v180, s45, v180\n"
"v_mfma_f32_16x16x32_bf16 v[196:199], a[140:143], a[180:183], v[196:199]\n"
"v_mul_f32 v200, s45, v200\n"
"v_mul_f32 v201, s45, v201\n"
"v_mfma_f32_16x16x32_bf16 v[108:111], a[232:235], a[184:187], v[108:111]\n"
"v_mul_f32 v202, s45, v202\n"
"v_mul_f32 v203, s45, v203\n"
"v_mfma_f32_16x16x32_bf16 v[108:111], a[236:239], a[188:191], v[108:111]\n"
"v_mul_f32 v206, s45, v206\n"
"v_mul_f32 v207, s45, v207\n"
"v_mfma_f32_16x16x32_bf16 v[104:107], a[224:227], a[184:187], v[104:107]\n"
"s_waitcnt lgkmcnt(0)\n"
"s_add_u32 s40, s56, s52\n"
"buffer_load_dwordx4 v240, s[16:19], s40 offen offset:1024 nt lds\n"
"v_mfma_f32_16x16x32_bf16 v[104:107], a[228:231], a[188:191], v[104:107]\n"
"s_waitcnt vmcnt(14)\n"
"v_cvt_pk_bf16_f32 v200, v200, v201\n"
"v_cvt_pk_bf16_f32 v201, v202, v203\n"
"v_mfma_f32_16x16x32_bf16 v[100:103], a[200:203], a[184:187], v[100:103]\n"
"s_barrier\n"
"v_cvt_pk_bf16_f32 v203, v206, v207\n"
"v_mfma_f32_16x16x32_bf16 v[100:103], a[204:207], a[188:191], v[100:103]\n"
"s_nop 0\n"
"v_permlane16_swap_b32 v201, v203\n"
"v_mfma_f32_16x16x32_bf16 v[96:99], a[192:195], a[184:187], v[96:99]\n"
"v_bfe_u32 v206, v242, 8, 2\n"
"v_lshl_add_u32 v206, v206, 4, s43\n"
"v_mfma_f32_16x16x32_bf16 v[96:99], a[196:199], a[188:191], v[96:99]\n"
"ds_read_b128 v[208:211], v206 offset:0\n"
"v_xor_b32 v207, 64, v206\n"
"v_mfma_f32_16x16x32_bf16 v[0:3], a[192:195], a[160:163], v[0:3]\n"
"ds_read_b128 v[212:215], v207 offset:0\n"
"v_mul_f32 v181, s45, v181\n"
"v_mfma_f32_16x16x32_bf16 v[0:3], a[196:199], a[164:167], v[0:3]\n"
"ds_read_b128 v[216:219], v206 offset:1024\n"
"v_mul_f32 v182, s45, v182\n"
"v_mfma_f32_16x16x32_bf16 v[4:7], a[200:203], a[160:163], v[4:7]\n"
"ds_read_b128 v[220:223], v207 offset:1024\n"
"v_mul_f32 v183, s45, v183\n"
"v_mfma_f32_16x16x32_bf16 v[4:7], a[204:207], a[164:167], v[4:7]\n"
"v_cvt_pk_bf16_f32 v176, v176, v177\n"
"v_cvt_pk_bf16_f32 v177, v178, v179\n"
"v_mfma_f32_16x16x32_bf16 v[8:11], a[208:211], a[160:163], v[8:11]\n"
"s_add_u32 s40, s56, s53\n"
"buffer_load_dwordx4 v240, s[16:19], s40 offen offset:2048 nt lds\n"
"v_mfma_f32_16x16x32_bf16 v[8:11], a[212:215], a[164:167], v[8:11]\n"
"v_cvt_pk_bf16_f32 v178, v180, v181\n"
"v_cvt_pk_bf16_f32 v179, v182, v183\n"
"v_mfma_f32_16x16x32_bf16 v[12:15], a[216:219], a[160:163], v[12:15]\n"
"v_permlane16_swap_b32 v176, v178\n"
"v_mfma_f32_16x16x32_bf16 v[12:15], a[220:223], a[164:167], v[12:15]\n"
"v_permlane16_swap_b32 v177, v179\n"
"v_mfma_f32_16x16x32_bf16 v[16:19], a[224:227], a[160:163], v[16:19]\n"
"v_mul_f32 v184, s45, v184\n"
"v_mul_f32 v185, s45, v185\n"
"v_mfma_f32_16x16x32_bf16 v[16:19], a[228:231], a[164:167], v[16:19]\n"
"v_mul_f32 v186, s45, v186\n"
"v_mul_f32 v187, s45, v187\n"
"v_mfma_f32_16x16x32_bf16 v[20:23], a[232:235], a[160:163], v[20:23]\n"
"v_mul_f32 v188, s45, v188\n"
"v_mul_f32 v189, s45, v189\n"
"v_mfma_f32_16x16x32_bf16 v[20:23], a[236:239], a[164:167], v[20:23]\n"
"v_mul_f32 v190, s45, v190\n"
"v_mul_f32 v191, s45, v191\n"
"v_mfma_f32_16x16x32_bf16 v[24:27], a[240:243], a[160:163], v[24:27]\n"
"v_cvt_pk_bf16_f32 v184, v184, v185\n"
"v_cvt_pk_bf16_f32 v185, v186, v187\n"
"v_mfma_f32_16x16x32_bf16 v[24:27], a[244:247], a[164:167], v[24:27]\n"
"v_cvt_pk_bf16_f32 v186, v188, v189\n"
"v_cvt_pk_bf16_f32 v187, v190, v191\n"
"v_mfma_f32_16x16x32_bf16 v[28:31], a[248:251], a[160:163], v[28:31]\n"
"s_add_u32 s40, s56, s54\n"
"buffer_load_dwordx4 v240, s[16:19], s40 offen offset:3072 nt lds\n"
"v_mfma_f32_16x16x32_bf16 v[28:31], a[252:255], a[164:167], v[28:31]\n"
"v_permlane16_swap_b32 v184, v186\n"
"v_mfma_f32_16x16x32_bf16 v[60:63], a[248:251], a[168:171], v[60:63]\n"
"v_permlane16_swap_b32 v185, v187\n"
"v_mfma_f32_16x16x32_bf16 v[60:63], a[252:255], a[172:175], v[60:63]\n"
"v_accvgpr_write_b32 a132, v184\n"
"v_accvgpr_write_b32 a133, v185\n"
"v_mfma_f32_16x16x32_bf16 v[56:59], a[240:243], a[168:171], v[56:59]\n"
"v_accvgpr_write_b32 a134, v186\n"
"v_accvgpr_write_b32 a135, v187\n"
"v_mfma_f32_16x16x32_bf16 v[56:59], a[244:247], a[172:175], v[56:59]\n"
"v_xor_b32 v207, 0x400, v248\n"
"v_mul_f32 v192, s45, v192\n"
"v_mfma_f32_16x16x32_bf16 v[52:55], a[232:235], a[168:171], v[52:55]\n"
"v_mul_f32 v193, s45, v193\n"
"v_mul_f32 v194, s45, v194\n"
"v_mfma_f32_16x16x32_bf16 v[52:55], a[236:239], a[172:175], v[52:55]\n"
"v_mul_f32 v195, s45, v195\n"
"v_mul_f32 v196, s45, v196\n"
"v_mfma_f32_16x16x32_bf16 v[48:51], a[224:227], a[168:171], v[48:51]\n"
"v_mul_f32 v197, s45, v197\n"
"v_mul_f32 v198, s45, v198\n"
"v_mfma_f32_16x16x32_bf16 v[48:51], a[228:231], a[172:175], v[48:51]\n"
"v_mul_f32 v199, s45, v199\n"
"v_cvt_pk_bf16_f32 v192, v192, v193\n"
"v_mfma_f32_16x16x32_bf16 v[44:47], a[216:219], a[168:171], v[44:47]\n"
"v_cvt_pk_bf16_f32 v193, v194, v195\n"
"v_cvt_pk_bf16_f32 v194, v196, v197\n"
"v_mfma_f32_16x16x32_bf16 v[44:47], a[220:223], a[172:175], v[44:47]\n"
"buffer_load_dword v253, v254, s[24:27], s63 offen\n"
"v_mfma_f32_16x16x32_bf16 v[40:43], a[208:211], a[168:171], v[40:43]\n"
"v_permlane16_swap_b32 v192, v194\n"
"v_mfma_f32_16x16x32_bf16 v[40:43], a[212:215], a[172:175], v[40:43]\n"
"v_cvt_pk_bf16_f32 v195, v198, v199\n"
"v_accvgpr_write_b32 a136, v192\n"
"v_mfma_f32_16x16x32_bf16 v[36:39], a[200:203], a[168:171], v[36:39]\n"
"v_permlane16_swap_b32 v193, v195\n"
"v_mfma_f32_16x16x32_bf16 v[36:39], a[204:207], a[172:175], v[36:39]\n"
"v_accvgpr_write_b32 a137, v193\n"
"v_accvgpr_write_b32 a138, v194\n"
"v_mfma_f32_16x16x32_bf16 v[32:35], a[192:195], a[168:171], v[32:35]\n"
"v_accvgpr_write_b32 a139, v195\n"
"v_cvt_pk_bf16_f32 v123, v126, v127\n"
"v_mfma_f32_16x16x32_bf16 v[32:35], a[196:199], a[172:175], v[32:35]\n"
"s_nop 0\n"
"v_permlane16_swap_b32 v121, v123\n"
"v_mfma_f32_16x16x32_bf16 v[64:67], a[192:195], a[176:179], v[64:67]\n"
"ds_read_b128 a[192:195], v248 offset:0\n"
"v_mul_f32 v128, s45, v128\n"
"v_mfma_f32_16x16x32_bf16 v[64:67], a[196:199], a[180:183], v[64:67]\n"
"ds_read_b128 a[196:199], v207 offset:0\n"
"v_mul_f32 v129, s45, v129\n"
"v_mfma_f32_16x16x32_bf16 v[68:71], a[200:203], a[176:179], v[68:71]\n"
"ds_read_b128 a[200:203], v248 offset:2048\n"
"v_mul_f32 v130, s45, v130\n"
"v_mfma_f32_16x16x32_bf16 v[68:71], a[204:207], a[180:183], v[68:71]\n"
"buffer_load_dword v253, v254, s[24:27], s63 offen offset:2048\n"
"v_mfma_f32_16x16x32_bf16 v[72:75], a[208:211], a[176:179], v[72:75]\n"
"ds_read_b128 a[204:207], v207 offset:2048\n"
"v_mul_f32 v131, s45, v131\n"
"v_mfma_f32_16x16x32_bf16 v[72:75], a[212:215], a[180:183], v[72:75]\n"
"v_mul_f32 v132, s45, v132\n"
"v_mul_f32 v133, s45, v133\n"
"v_mfma_f32_16x16x32_bf16 v[76:79], a[216:219], a[176:179], v[76:79]\n"
"v_mul_f32 v134, s45, v134\n"
"v_mul_f32 v135, s45, v135\n"
"v_mfma_f32_16x16x32_bf16 v[76:79], a[220:223], a[180:183], v[76:79]\n"
"v_cvt_pk_bf16_f32 v128, v128, v129\n"
"v_cvt_pk_bf16_f32 v129, v130, v131\n"
"v_mfma_f32_16x16x32_bf16 v[88:91], a[240:243], a[176:179], v[88:91]\n"
"v_cvt_pk_bf16_f32 v130, v132, v133\n"
"v_cvt_pk_bf16_f32 v131, v134, v135\n"
"v_mfma_f32_16x16x32_bf16 v[88:91], a[244:247], a[180:183], v[88:91]\n"
"v_permlane16_swap_b32 v128, v130\n"
"v_mfma_f32_16x16x32_bf16 v[92:95], a[248:251], a[176:179], v[92:95]\n"
"v_permlane16_swap_b32 v129, v131\n"
"v_mfma_f32_16x16x32_bf16 v[92:95], a[252:255], a[180:183], v[92:95]\n"
"v_accvgpr_write_b32 a104, v128\n"
"v_accvgpr_write_b32 a105, v129\n"
"v_mfma_f32_16x16x32_bf16 v[80:83], a[224:227], a[176:179], v[80:83]\n"
"v_accvgpr_write_b32 a106, v130\n"
"v_accvgpr_write_b32 a107, v131\n"
"v_mfma_f32_16x16x32_bf16 v[80:83], a[228:231], a[180:183], v[80:83]\n"
"v_mul_f32 v136, s45, v136\n"
"v_mul_f32 v137, s45, v137\n"
"v_mfma_f32_16x16x32_bf16 v[84:87], a[232:235], a[176:179], v[84:87]\n"
"v_mul_f32 v138, s45, v138\n"
"v_mul_f32 v139, s45, v139\n"
"v_mfma_f32_16x16x32_bf16 v[84:87], a[236:239], a[180:183], v[84:87]\n"
"v_mul_f32 v140, s45, v140\n"
"v_mul_f32 v141, s45, v141\n"
"s_waitcnt lgkmcnt(0)\n"
"v_mfma_f32_16x16x32_bf16 v[180:183], v[208:211], a[0:3], 0\n"
"v_mul_f32 v142, s45, v142\n"
"v_mul_f32 v143, s45, v143\n"
"v_mfma_f32_16x16x32_bf16 v[180:183], v[212:215], a[4:7], v[180:183]\n"
"v_cvt_pk_bf16_f32 v136, v136, v137\n"
"v_cvt_pk_bf16_f32 v137, v138, v139\n"
"v_mfma_f32_16x16x32_bf16 v[180:183], v[216:219], a[8:11], v[180:183]\n"
"v_cvt_pk_bf16_f32 v138, v140, v141\n"
"v_cvt_pk_bf16_f32 v139, v142, v143\n"
"v_mfma_f32_16x16x32_bf16 v[180:183], v[220:223], a[12:15], v[180:183]\n"
"v_permlane16_swap_b32 v136, v138\n"
"v_mfma_f32_16x16x32_bf16 v[184:187], v[208:211], a[16:19], 0\n"
"v_permlane16_swap_b32 v137, v139\n"
"v_mfma_f32_16x16x32_bf16 v[184:187], v[212:215], a[20:23], v[184:187]\n"
"v_accvgpr_write_b32 a108, v136\n"
"v_accvgpr_write_b32 a109, v137\n"
"v_mfma_f32_16x16x32_bf16 v[184:187], v[216:219], a[24:27], v[184:187]\n"
"v_accvgpr_write_b32 a110, v138\n"
"v_accvgpr_write_b32 a111, v139\n"
"v_mfma_f32_16x16x32_bf16 v[184:187], v[220:223], a[28:31], v[184:187]\n"
"v_mul_f32 v144, s45, v144\n"
"v_mul_f32 v145, s45, v145\n"
"v_mfma_f32_16x16x32_bf16 v[188:191], v[208:211], a[32:35], 0\n"
"v_mul_f32 v146, s45, v146\n"
"v_mul_f32 v147, s45, v147\n"
"v_mfma_f32_16x16x32_bf16 v[188:191], v[212:215], a[36:39], v[188:191]\n"
"v_xor_b32 v180, s77, v180\n"
"v_xor_b32 v181, s77, v181\n"
"v_mfma_f32_16x16x32_bf16 v[188:191], v[216:219], a[40:43], v[188:191]\n"
"v_xor_b32 v182, s77, v182\n"
"v_xor_b32 v183, s77, v183\n"
"v_mfma_f32_16x16x32_bf16 v[188:191], v[220:223], a[44:47], v[188:191]\n"
"v_mul_f32 v148, s45, v148\n"
"v_mul_f32 v149, s45, v149\n"
"v_mfma_f32_16x16x32_bf16 v[192:195], v[208:211], a[192:195], 0\n"
"ds_read_b128 v[208:211], v228 offset:0\n"
"v_mul_f32 v150, s45, v150\n"
"v_mfma_f32_16x16x32_bf16 v[192:195], v[212:215], a[196:199], v[192:195]\n"
"ds_read_b128 v[212:215], v229 offset:0\n"
"v_xor_b32 v184, s77, v184\n"
"v_mfma_f32_16x16x32_bf16 v[192:195], v[216:219], a[200:203], v[192:195]\n"
"ds_read_b128 v[216:219], v228 offset:1024\n"
"v_xor_b32 v185, s77, v185\n"
"v_mfma_f32_16x16x32_bf16 v[192:195], v[220:223], a[204:207], v[192:195]\n"
"s_waitcnt lgkmcnt(2)\n"
"ds_read_b128 v[220:223], v229 offset:1024\n"
"v_xor_b32 v186, s77, v186\n"
"v_mfma_f32_16x16x32_bf16 v[124:127], v[208:211], a[0:3], v[180:183]\n"
"s_waitcnt lgkmcnt(2)\n"
"v_xor_b32 v187, s77, v187\n"
"v_mul_f32 v151, s45, v151\n"
"v_mfma_f32_16x16x32_bf16 v[124:127], v[212:215], a[4:7], v[124:127]\n"
"s_waitcnt lgkmcnt(1)\n"
"v_xor_b32 v188, s77, v188\n"
"v_xor_b32 v189, s77, v189\n"
"v_mfma_f32_16x16x32_bf16 v[124:127], v[216:219], a[8:11], v[124:127]\n"
"s_waitcnt lgkmcnt(0)\n"
"v_xor_b32 v190, s77, v190\n"
"v_xor_b32 v191, s77, v191\n"
"v_mfma_f32_16x16x32_bf16 v[124:127], v[220:223], a[12:15], v[124:127]\n"
"v_cvt_pk_bf16_f32 v144, v144, v145\n"
"v_cvt_pk_bf16_f32 v145, v146, v147\n"
"v_mfma_f32_16x16x32_bf16 v[128:131], v[216:219], a[24:27], v[184:187]\n"
"v_cvt_pk_bf16_f32 v146, v148, v149\n"
"v_cvt_pk_bf16_f32 v147, v150, v151\n"
"v_mfma_f32_16x16x32_bf16 v[128:131], v[220:223], a[28:31], v[128:131]\n"
"v_xor_b32 v192, s77, v192\n"
"v_xor_b32 v193, s77, v193\n"
"v_mfma_f32_16x16x32_bf16 v[128:131], v[208:211], a[16:19], v[128:131]\n"
"v_xor_b32 v194, s77, v194\n"
"v_xor_b32 v195, s77, v195\n"
"v_mfma_f32_16x16x32_bf16 v[128:131], v[212:215], a[20:23], v[128:131]\n"
"v_permlane16_swap_b32 v144, v146\n"
"v_mfma_f32_16x16x32_bf16 v[132:135], v[208:211], a[32:35], v[188:191]\n"
"v_permlane16_swap_b32 v145, v147\n"
"v_mfma_f32_16x16x32_bf16 v[132:135], v[212:215], a[36:39], v[132:135]\n"
"v_exp_f32 v124, v124\n"
"v_mfma_f32_16x16x32_bf16 v[132:135], v[216:219], a[40:43], v[132:135]\n"
"v_exp_f32 v125, v125\n"
"v_mfma_f32_16x16x32_bf16 v[132:135], v[220:223], a[44:47], v[132:135]\n"
"v_exp_f32 v126, v126\n"
"v_mfma_f32_16x16x32_bf16 v[136:139], v[216:219], a[200:203], v[192:195]\n"
"ds_read_b128 v[216:219], v228 offset:5120\n"
"v_accvgpr_write_b32 a112, v144\n"
"v_mfma_f32_16x16x32_bf16 v[136:139], v[220:223], a[204:207], v[136:139]\n"
"ds_read_b128 v[220:223], v229 offset:5120\n"
"v_accvgpr_write_b32 a113, v145\n"
"v_mfma_f32_16x16x32_bf16 v[136:139], v[208:211], a[192:195], v[136:139]\n"
"ds_read_b128 v[208:211], v228 offset:4096\n"
"v_accvgpr_write_b32 a114, v146\n"
"v_mfma_f32_16x16x32_bf16 v[136:139], v[212:215], a[196:199], v[136:139]\n"
"s_waitcnt lgkmcnt(2)\n"
"ds_read_b128 v[212:215], v229 offset:4096\n"
"v_accvgpr_write_b32 a115, v147\n"
"v_mfma_f32_16x16x32_bf16 v[140:143], v[216:219], a[8:11], v[180:183]\n"
"s_waitcnt lgkmcnt(2)\n"
"v_exp_f32 v127, v127\n"
"v_mfma_f32_16x16x32_bf16 v[140:143], v[220:223], a[12:15], v[140:143]\n"
"s_waitcnt lgkmcnt(1)\n"
"v_exp_f32 v128, v128\n"
"v_mfma_f32_16x16x32_bf16 v[140:143], v[208:211], a[0:3], v[140:143]\n"
"s_waitcnt lgkmcnt(0)\n"
"v_exp_f32 v129, v129\n"
"v_mfma_f32_16x16x32_bf16 v[140:143], v[212:215], a[4:7], v[140:143]\n"
"v_exp_f32 v130, v130\n"
"v_mfma_f32_16x16x32_bf16 v[144:147], v[208:211], a[16:19], v[184:187]\n"
"v_mul_f32 v152, s45, v152\n"
"v_mul_f32 v153, s45, v153\n"
"v_mfma_f32_16x16x32_bf16 v[144:147], v[212:215], a[20:23], v[144:147]\n"
"v_mul_f32 v154, s45, v154\n"
"v_mul_f32 v155, s45, v155\n"
"v_mfma_f32_16x16x32_bf16 v[144:147], v[216:219], a[24:27], v[144:147]\n"
"v_mul_f32 v156, s45, v156\n"
"v_mul_f32 v157, s45, v157\n"
"v_mfma_f32_16x16x32_bf16 v[144:147], v[220:223], a[28:31], v[144:147]\n"
"v_mul_f32 v158, s45, v158\n"
"v_mul_f32 v159, s45, v159\n"
"v_mfma_f32_16x16x32_bf16 v[148:151], v[216:219], a[40:43], v[188:191]\n"
"v_exp_f32 v131, v131\n"
"v_mfma_f32_16x16x32_bf16 v[148:151], v[220:223], a[44:47], v[148:151]\n"
"v_cvt_pk_bf16_f32 v152, v152, v153\n"
"v_cvt_pk_bf16_f32 v153, v154, v155\n"
"v_mfma_f32_16x16x32_bf16 v[148:151], v[208:211], a[32:35], v[148:151]\n"
"v_cvt_pk_bf16_f32 v154, v156, v157\n"
"v_cvt_pk_bf16_f32 v155, v158, v159\n"
"v_mfma_f32_16x16x32_bf16 v[148:151], v[212:215], a[36:39], v[148:151]\n"
"v_permlane16_swap_b32 v152, v154\n"
"v_permlane16_swap_b32 v153, v155\n"
"v_accvgpr_write_b32 a116, v152\n"
"v_accvgpr_write_b32 a117, v153\n"
"v_cvt_pk_bf16_f32 v152, v124, v125\n"
"v_cvt_pk_bf16_f32 v153, v126, v127\n"
"v_mfma_f32_16x16x32_bf16 v[124:127], v[208:211], a[192:195], v[192:195]\n"
"ds_read_b128 v[208:211], v230 offset:0\n"
"v_mul_f32 v160, s45, v160\n"
"v_mfma_f32_16x16x32_bf16 v[124:127], v[212:215], a[196:199], v[124:127]\n"
"ds_read_b128 v[212:215], v231 offset:0\n"
"v_mul_f32 v161, s45, v161\n"
"v_mfma_f32_16x16x32_bf16 v[124:127], v[216:219], a[200:203], v[124:127]\n"
"ds_read_b128 v[216:219], v230 offset:1024\n"
"v_mul_f32 v162, s45, v162\n"
"v_mul_f32 v163, s45, v163\n"
"v_mul_f32 v164, s45, v164\n"
"v_mul_f32 v165, s45, v165\n"
"v_mul_f32 v166, s45, v166\n"
"v_mul_f32 v167, s45, v167\n"
"v_cvt_pk_bf16_f32 v160, v160, v161\n"
"v_cvt_pk_bf16_f32 v161, v162, v163\n"
"v_cvt_pk_bf16_f32 v162, v164, v165\n"
"v_cvt_pk_bf16_f32 v163, v166, v167\n"
"v_mfma_f32_16x16x32_bf16 v[124:127], v[220:223], a[204:207], v[124:127]\n"
"s_waitcnt lgkmcnt(2)\n"
"ds_read_b128 v[220:223], v231 offset:1024\n"
"v_permlane16_swap_b32 v160, v162\n"
"v_permlane16_swap_b32 v161, v163\n"
"v_accvgpr_write_b32 a120, v160\n"
"v_accvgpr_write_b32 a121, v161\n"
"v_cvt_pk_bf16_f32 v160, v128, v129\n"
"v_cvt_pk_bf16_f32 v161, v130, v131\n"
"v_mfma_f32_16x16x32_bf16 v[128:131], v[208:211], a[0:3], v[180:183]\n"
"s_waitcnt lgkmcnt(2)\n"
"v_exp_f32 v132, v132\n"
"v_mfma_f32_16x16x32_bf16 v[128:131], v[212:215], a[4:7], v[128:131]\n"
"s_waitcnt lgkmcnt(1)\n"
"v_exp_f32 v133, v133\n"
"v_mfma_f32_16x16x32_bf16 v[128:131], v[216:219], a[8:11], v[128:131]\n"
"s_waitcnt lgkmcnt(0)\n"
"v_mul_f32 v168, s45, v168\n"
"v_mul_f32 v169, s45, v169\n"
"v_mul_f32 v170, s45, v170\n"
"v_mul_f32 v171, s45, v171\n"
"v_mul_f32 v172, s45, v172\n"
"v_mul_f32 v173, s45, v173\n"
"v_mul_f32 v174, s45, v174\n"
"v_mul_f32 v175, s45, v175\n"
"v_cvt_pk_bf16_f32 v168, v168, v169\n"
"v_cvt_pk_bf16_f32 v169, v170, v171\n"
"v_cvt_pk_bf16_f32 v170, v172, v173\n"
"v_cvt_pk_bf16_f32 v171, v174, v175\n"
"v_exp_f32 v134, v134\n"
"v_exp_f32 v135, v135\n"
"v_mfma_f32_16x16x32_bf16 v[128:131], v[220:223], a[12:15], v[128:131]\n"
"v_permlane16_swap_b32 v168, v170\n"
"v_permlane16_swap_b32 v169, v171\n"
"v_accvgpr_write_b32 a124, v168\n"
"v_accvgpr_write_b32 a125, v169\n"
"v_cvt_pk_bf16_f32 v168, v132, v133\n"
"v_cvt_pk_bf16_f32 v169, v134, v135\n"
"v_mfma_f32_16x16x32_bf16 v[132:135], v[216:219], a[24:27], v[184:187]\n"
"v_exp_f32 v136, v136\n"
"v_mfma_f32_16x16x32_bf16 v[132:135], v[220:223], a[28:31], v[132:135]\n"
"v_exp_f32 v137, v137\n"
"v_mfma_f32_16x16x32_bf16 v[132:135], v[208:211], a[16:19], v[132:135]\n"
"v_exp_f32 v138, v138\n"
"v_exp_f32 v139, v139\n"
"v_mfma_f32_16x16x32_bf16 v[132:135], v[212:215], a[20:23], v[132:135]\n"
"v_accvgpr_write_b32 a128, v176\n"
"v_accvgpr_write_b32 a129, v177\n"
"v_cvt_pk_bf16_f32 v176, v136, v137\n"
"v_cvt_pk_bf16_f32 v177, v138, v139\n"
"v_mfma_f32_16x16x32_bf16 v[136:139], v[208:211], a[32:35], v[188:191]\n"
"ds_read_b128 v[208:211], v230 offset:4096\n"
"v_accvgpr_write_b32 a118, v154\n"
"v_mfma_f32_16x16x32_bf16 v[136:139], v[212:215], a[36:39], v[136:139]\n"
"ds_read_b128 v[212:215], v231 offset:4096\n"
"v_accvgpr_write_b32 a119, v155\n"
"v_mfma_f32_16x16x32_bf16 v[136:139], v[216:219], a[40:43], v[136:139]\n"
"ds_read_b128 v[216:219], v230 offset:5120\n"
"v_exp_f32 v140, v140\n"
"v_exp_f32 v141, v141\n"
"v_exp_f32 v142, v142\n"
"v_exp_f32 v143, v143\n"
"v_mfma_f32_16x16x32_bf16 v[136:139], v[220:223], a[44:47], v[136:139]\n"
"s_waitcnt lgkmcnt(2)\n"
"ds_read_b128 v[220:223], v231 offset:5120\n"
"v_cvt_pk_bf16_f32 v154, v140, v141\n"
"v_cvt_pk_bf16_f32 v155, v142, v143\n"
"v_mfma_f32_16x16x32_bf16 v[140:143], v[208:211], a[0:3], v[180:183]\n"
"s_waitcnt lgkmcnt(2)\n"
"v_exp_f32 v144, v144\n"
"v_mfma_f32_16x16x32_bf16 v[140:143], v[212:215], a[4:7], v[140:143]\n"
"s_waitcnt lgkmcnt(1)\n"
"v_exp_f32 v145, v145\n"
"v_mfma_f32_16x16x32_bf16 v[140:143], v[216:219], a[8:11], v[140:143]\n"
"s_waitcnt lgkmcnt(0)\n"
"v_exp_f32 v146, v146\n"
"v_exp_f32 v147, v147\n"
"v_mfma_f32_16x16x32_bf16 v[140:143], v[220:223], a[12:15], v[140:143]\n"
"v_accvgpr_write_b32 a122, v162\n"
"v_accvgpr_write_b32 a123, v163\n"
"v_cvt_pk_bf16_f32 v162, v144, v145\n"
"v_cvt_pk_bf16_f32 v163, v146, v147\n"
"v_mfma_f32_16x16x32_bf16 v[144:147], v[216:219], a[24:27], v[184:187]\n"
"v_mul_f32 v96, s45, v96\n"
"v_mul_f32 v97, s45, v97\n"
"v_mul_f32 v98, s45, v98\n"
"v_mul_f32 v99, s45, v99\n"
"v_mul_f32 v100, s45, v100\n"
"v_mul_f32 v101, s45, v101\n"
"v_mul_f32 v102, s45, v102\n"
"v_mul_f32 v103, s45, v103\n"
"v_cvt_pk_bf16_f32 v96, v96, v97\n"
"v_cvt_pk_bf16_f32 v97, v98, v99\n"
"v_cvt_pk_bf16_f32 v98, v100, v101\n"
"v_cvt_pk_bf16_f32 v99, v102, v103\n"
"v_mfma_f32_16x16x32_bf16 v[144:147], v[220:223], a[28:31], v[144:147]\n"
"v_permlane16_swap_b32 v96, v98\n"
"v_permlane16_swap_b32 v97, v99\n"
"v_mul_f32 v104, s45, v104\n"
"v_mul_f32 v105, s45, v105\n"
"v_mul_f32 v106, s45, v106\n"
"v_mul_f32 v107, s45, v107\n"
"v_mul_f32 v108, s45, v108\n"
"v_mul_f32 v109, s45, v109\n"
"v_mul_f32 v110, s45, v110\n"
"v_mul_f32 v111, s45, v111\n"
"v_cvt_pk_bf16_f32 v104, v104, v105\n"
"v_cvt_pk_bf16_f32 v105, v106, v107\n"
"v_cvt_pk_bf16_f32 v106, v108, v109\n"
"v_cvt_pk_bf16_f32 v107, v110, v111\n"
"v_mfma_f32_16x16x32_bf16 v[144:147], v[208:211], a[16:19], v[144:147]\n"
"ds_write_b128 v248, v[96:99] offset:12288\n"
"v_permlane16_swap_b32 v104, v106\n"
"v_permlane16_swap_b32 v105, v107\n"
"v_exp_f32 v148, v148\n"
"v_exp_f32 v149, v149\n"
"v_exp_f32 v150, v150\n"
"v_exp_f32 v151, v151\n"
"v_mfma_f32_16x16x32_bf16 v[144:147], v[212:215], a[20:23], v[144:147]\n"
"ds_write_b128 v248, v[104:107] offset:14336\n"
"v_accvgpr_write_b32 a126, v170\n"
"v_accvgpr_write_b32 a127, v171\n"
"v_cvt_pk_bf16_f32 v170, v148, v149\n"
"v_cvt_pk_bf16_f32 v171, v150, v151\n"
"v_mfma_f32_16x16x32_bf16 v[148:151], v[208:211], a[32:35], v[188:191]\n"
"v_exp_f32 v124, v124\n"
"v_mfma_f32_16x16x32_bf16 v[148:151], v[212:215], a[36:39], v[148:151]\n"
"s_cbranch_vccnz LPCE0_%=\nLPKE0_%=:\n"
"v_accvgpr_write_b32 a130, v178\n"
"v_mfma_f32_16x16x32_bf16 v[148:151], v[216:219], a[40:43], v[148:151]\n"
"v_exp_f32 v125, v125\n"
"v_mfma_f32_16x16x32_bf16 v[148:151], v[220:223], a[44:47], v[148:151]\n"
"v_exp_f32 v126, v126\n"
"s_waitcnt vmcnt(10) lgkmcnt(0)\n"
"v_exp_f32 v127, v127\n"
"s_barrier\n"
"ds_read_b128 v[208:211], v228 offset:16384\n"
"ds_read_b128 v[212:215], v229 offset:16384\n"
"ds_read_b128 v[216:219], v228 offset:17408\n"
"ds_read_b128 v[220:223], v229 offset:17408\n"
"v_accvgpr_write_b32 a131, v179\n"
"v_cvt_pk_bf16_f32 v178, v124, v125\n"
"v_cvt_pk_bf16_f32 v179, v126, v127\n"
"s_waitcnt lgkmcnt(3)\n"
"v_mfma_f32_16x16x32_bf16 v[124:127], v[208:211], a[0:3], v[180:183]\n"
"s_add_u32 m0, s44, 98304\n"
"v_exp_f32 v128, v128\n"
"s_waitcnt lgkmcnt(2)\n"
"v_mfma_f32_16x16x32_bf16 v[124:127], v[212:215], a[4:7], v[124:127]\n"
"buffer_load_dwordx4 v240, s[20:23], s56 offen nt lds\n"
"s_waitcnt lgkmcnt(1)\n"
"v_mfma_f32_16x16x32_bf16 v[124:127], v[216:219], a[8:11], v[124:127]\n"
"s_waitcnt lgkmcnt(0)\n"
"v_exp_f32 v129, v129\n"
"v_exp_f32 v130, v130\n"
"v_exp_f32 v131, v131\n"
"v_mfma_f32_16x16x32_bf16 v[124:127], v[220:223], a[12:15], v[124:127]\n"
"v_cvt_pk_bf16_f32 v156, v128, v129\n"
"v_cvt_pk_bf16_f32 v157, v130, v131\n"
"v_mfma_f32_16x16x32_bf16 v[128:131], v[216:219], a[24:27], v[184:187]\n"
"v_exp_f32 v132, v132\n"
"v_mfma_f32_16x16x32_bf16 v[128:131], v[220:223], a[28:31], v[128:131]\n"
"v_exp_f32 v133, v133\n"
"v_mfma_f32_16x16x32_bf16 v[128:131], v[208:211], a[16:19], v[128:131]\n"
"v_exp_f32 v134, v134\n"
"v_exp_f32 v135, v135\n"
"v_mfma_f32_16x16x32_bf16 v[128:131], v[212:215], a[20:23], v[128:131]\n"
"v_cvt_pk_bf16_f32 v164, v132, v133\n"
"v_cvt_pk_bf16_f32 v165, v134, v135\n"
"v_mfma_f32_16x16x32_bf16 v[132:135], v[208:211], a[32:35], v[188:191]\n"
"v_exp_f32 v136, v136\n"
"v_mfma_f32_16x16x32_bf16 v[132:135], v[212:215], a[36:39], v[132:135]\n"
"v_exp_f32 v137, v137\n"
"v_mfma_f32_16x16x32_bf16 v[132:135], v[216:219], a[40:43], v[132:135]\n"
"ds_read_b64_tr_b16 a[244:245], v234 offset:0\n"
"ds_read_b64_tr_b16 a[246:247], v234 offset:4096\n"
"v_exp_f32 v138, v138\n"
"v_exp_f32 v139, v139\n"
"v_mfma_f32_16x16x32_bf16 v[132:135], v[220:223], a[44:47], v[132:135]\n"
"v_cvt_pk_bf16_f32 v172, v136, v137\n"
"v_cvt_pk_bf16_f32 v173, v138, v139\n"
"v_mfma_f32_16x16x32_bf16 v[136:139], v[216:219], a[200:203], v[192:195]\n"
"ds_read_b64_tr_b16 a[248:249], v238 offset:0\n"
"ds_read_b64_tr_b16 a[250:251], v238 offset:4096\n"
"v_mfma_f32_16x16x32_bf16 v[136:139], v[220:223], a[204:207], v[136:139]\n"
"s_add_u32 s40, s56, s52\n"
"buffer_load_dwordx4 v240, s[20:23], s40 offen offset:1024 nt lds\n"
"v_mfma_f32_16x16x32_bf16 v[136:139], v[208:211], a[192:195], v[136:139]\n"
"v_exp_f32 v140, v140\n"
"v_mfma_f32_16x16x32_bf16 v[136:139], v[212:215], a[196:199], v[136:139]\n"
"s_waitcnt lgkmcnt(2)\n"
"v_mul_f32 v8, s45, v8\n"
"v_mul_f32 v9, s45, v9\n"
"v_mul_f32 v10, s45, v10\n"
"v_mul_f32 v11, s45, v11\n"
"v_mul_f32 v12, s45, v12\n"
"v_mul_f32 v13, s45, v13\n"
"v_mul_f32 v14, s45, v14\n"
"v_mul_f32 v15, s45, v15\n"
"v_cvt_pk_bf16_f32 v8, v8, v9\n"
"v_cvt_pk_bf16_f32 v9, v10, v11\n"
"v_cvt_pk_bf16_f32 v10, v12, v13\n"
"v_cvt_pk_bf16_f32 v11, v14, v15\n"
"v_exp_f32 v141, v141\n"
"v_exp_f32 v142, v142\n"
"v_exp_f32 v143, v143\n"
"v_mfma_f32_16x16x32_bf16 a[216:219], v[176:179], a[244:247], 0\n"
"v_permlane16_swap_b32 v8, v10\n"
"v_permlane16_swap_b32 v9, v11\n"
"v_accvgpr_write_b32 a148, v8\n"
"v_accvgpr_write_b32 a149, v9\n"
"v_accvgpr_write_b32 a150, v10\n"
"v_accvgpr_write_b32 a151, v11\n"
"v_cvt_pk_bf16_f32 v158, v140, v141\n"
"v_cvt_pk_bf16_f32 v159, v142, v143\n"
"v_mfma_f32_16x16x32_bf16 v[8:11], v[152:155], a[244:247], 0\n"
"s_waitcnt lgkmcnt(0)\n"
"ds_read_b64_tr_b16 v[224:225], v239 offset:0\n"
"ds_read_b64_tr_b16 v[226:227], v239 offset:4096\n"
"v_mul_f32 v40, s45, v40\n"
"v_mul_f32 v41, s45, v41\n"
"v_mul_f32 v42, s45, v42\n"
"v_mul_f32 v43, s45, v43\n"
"v_mul_f32 v44, s45, v44\n"
"v_mul_f32 v45, s45, v45\n"
"v_mul_f32 v46, s45, v46\n"
"v_mul_f32 v47, s45, v47\n"
"v_cvt_pk_bf16_f32 v40, v40, v41\n"
"v_cvt_pk_bf16_f32 v41, v42, v43\n"
"v_cvt_pk_bf16_f32 v42, v44, v45\n"
"v_cvt_pk_bf16_f32 v43, v46, v47\n"
"v_exp_f32 v144, v144\n"
"v_exp_f32 v145, v145\n"
"v_exp_f32 v146, v146\n"
"v_exp_f32 v147, v147\n"
"v_mfma_f32_16x16x32_bf16 v[8:11], v[156:159], a[248:251], v[8:11]\n"
"ds_read_b64_tr_b16 a[252:253], v235 offset:0\n"
"ds_read_b64_tr_b16 a[254:255], v235 offset:4096\n"
"v_permlane16_swap_b32 v40, v42\n"
"v_permlane16_swap_b32 v41, v43\n"
"v_accvgpr_write_b32 a164, v40\n"
"v_accvgpr_write_b32 a165, v41\n"
"v_accvgpr_write_b32 a166, v42\n"
"v_accvgpr_write_b32 a167, v43\n"
"v_cvt_pk_bf16_f32 v166, v144, v145\n"
"v_cvt_pk_bf16_f32 v167, v146, v147\n"
"v_mfma_f32_16x16x32_bf16 v[40:43], v[160:163], a[244:247], 0\n"
"v_mul_f32 v72, s45, v72\n"
"v_mul_f32 v73, s45, v73\n"
"v_mul_f32 v74, s45, v74\n"
"v_mul_f32 v75, s45, v75\n"
"v_mul_f32 v76, s45, v76\n"
"v_mul_f32 v77, s45, v77\n"
"v_mul_f32 v78, s45, v78\n"
"v_mul_f32 v79, s45, v79\n"
"v_cvt_pk_bf16_f32 v72, v72, v73\n"
"v_cvt_pk_bf16_f32 v73, v74, v75\n"
"v_cvt_pk_bf16_f32 v74, v76, v77\n"
"v_cvt_pk_bf16_f32 v75, v78, v79\n"
"v_exp_f32 v148, v148\n"
"v_exp_f32 v149, v149\n"
"v_exp_f32 v150, v150\n"
"v_exp_f32 v151, v151\n"
"v_mfma_f32_16x16x32_bf16 v[40:43], v[164:167], a[248:251], v[40:43]\n"
"v_permlane16_swap_b32 v72, v74\n"
"v_permlane16_swap_b32 v73, v75\n"
"v_accvgpr_write_b32 a180, v72\n"
"v_accvgpr_write_b32 a181, v73\n"
"v_accvgpr_write_b32 a182, v74\n"
"v_accvgpr_write_b32 a183, v75\n"
"v_cvt_pk_bf16_f32 v174, v148, v149\n"
"v_cvt_pk_bf16_f32 v175, v150, v151\n"
"v_mfma_f32_16x16x32_bf16 v[72:75], v[168:171], a[244:247], 0\n"
"ds_read_b128 v[216:219], v228 offset:21504\n"
"v_mul_f32 v204, s45, v204\n"
"v_mfma_f32_16x16x32_bf16 v[72:75], v[172:175], a[248:251], v[72:75]\n"
"s_waitcnt lgkmcnt(3)\n"
"ds_read_b128 v[220:223], v229 offset:21504\n"
"v_mul_f32 v205, s45, v205\n"
"v_mfma_f32_16x16x32_bf16 v[76:79], v[172:175], v[224:227], 0\n"
"s_waitcnt lgkmcnt(2)\n"
"ds_read_b128 v[208:211], v228 offset:20480\n"
"v_cvt_pk_bf16_f32 v202, v204, v205\n"
"v_mfma_f32_16x16x32_bf16 v[76:79], v[168:171], a[252:255], v[76:79]\n"
"ds_read_b128 v[212:215], v229 offset:20480\n"
"v_accvgpr_write_b32 a240, v249\n"
"v_mfma_f32_16x16x32_bf16 v[44:47], v[164:167], v[224:227], 0\n"
"s_add_u32 s40, s56, s53\n"
"buffer_load_dwordx4 v240, s[20:23], s40 offen offset:2048 nt lds\n"
"v_mfma_f32_16x16x32_bf16 v[44:47], v[160:163], a[252:255], v[44:47]\n"
"ds_read_b64_tr_b16 a[244:245], v234 offset:1024\n"
"v_accvgpr_write_b32 a241, v249\n"
"v_mfma_f32_16x16x32_bf16 v[12:15], v[156:159], v[224:227], 0\n"
"ds_read_b64_tr_b16 a[246:247], v234 offset:5120\n"
"v_accvgpr_write_b32 a242, v249\n"
"v_mfma_f32_16x16x32_bf16 v[12:15], v[152:155], a[252:255], v[12:15]\n"
"v_accvgpr_write_b32 a243, v249\n"
"v_mov_b32 v196, 0\n"
"v_mov_b32 v197, 0\n"
"v_mov_b32 v198, 0\n"
"v_mov_b32 v199, 0\n"
"v_mfma_f32_16x16x32_bf16 a[220:223], v[176:179], a[252:255], 0\n"
"v_permlane16_swap_b32 v200, v202\n"
"v_accvgpr_write_b32 a142, v202\n"
"v_accvgpr_write_b32 a143, v203\n"
"v_mov_b32 v202, 0\n"
"v_mov_b32 v203, 0\n"
"v_mov_b32 v204, 0\n"
"v_mov_b32 v205, 0\n"
"v_smfmac_f32_16x16x64_bf16 v[196:199], a[240:243], v[152:159], v241\n"
"ds_read_b64_tr_b16 a[248:249], v238 offset:1024\n"
"v_mul_f32 v24, s45, v24\n"
"v_mfma_f32_16x16x32_bf16 v[202:205], a[240:243], v[176:179], v[202:205]\n"
"s_waitcnt lgkmcnt(2)\n"
"ds_read_b64_tr_b16 a[250:251], v238 offset:5120\n"
"v_mul_f32 v25, s45, v25\n"
"v_mfma_f32_16x16x32_bf16 v[140:143], v[216:219], a[8:11], v[180:183]\n"
"ds_read_b64_tr_b16 v[224:225], v239 offset:1024\n"
"v_mul_f32 v26, s45, v26\n"
"v_mfma_f32_16x16x32_bf16 v[140:143], v[220:223], a[12:15], v[140:143]\n"
"ds_read_b64_tr_b16 v[226:227], v239 offset:5120\n"
"v_mul_f32 v27, s45, v27\n"
"v_mfma_f32_16x16x32_bf16 v[140:143], v[208:211], a[0:3], v[140:143]\n"
"ds_read_b64_tr_b16 a[252:253], v235 offset:1024\n"
"v_mul_f32 v28, s45, v28\n"
"v_mfma_f32_16x16x32_bf16 v[140:143], v[212:215], a[4:7], v[140:143]\n"
"ds_read_b64_tr_b16 a[254:255], v235 offset:5120\n"
"v_mul_f32 v29, s45, v29\n"
"v_mfma_f32_16x16x32_bf16 v[144:147], v[208:211], a[16:19], v[184:187]\n"
"v_exp_f32 v124, v124\n"
"v_mfma_f32_16x16x32_bf16 v[144:147], v[212:215], a[20:23], v[144:147]\n"
"v_exp_f32 v125, v125\n"
"v_mfma_f32_16x16x32_bf16 v[144:147], v[216:219], a[24:27], v[144:147]\n"
"s_add_u32 s40, s56, s54\n"
"buffer_load_dwordx4 v240, s[20:23], s40 offen offset:3072 nt lds\n"
"v_mfma_f32_16x16x32_bf16 v[144:147], v[220:223], a[28:31], v[144:147]\n"
"s_add_u32 s55, s55, s59\n"
"v_exp_f32 v126, v126\n"
"v_mfma_f32_16x16x32_bf16 v[148:151], v[216:219], a[40:43], v[188:191]\n"
"s_add_u32 s41, s55, s57\n"
"v_exp_f32 v127, v127\n"
"v_mfma_f32_16x16x32_bf16 v[148:151], v[220:223], a[44:47], v[148:151]\n"
"s_and_b32 s41, s41, s58\n"
"v_cvt_pk_bf16_f32 v96, v124, v125\n"
"v_cvt_pk_bf16_f32 v97, v126, v127\n"
"v_mfma_f32_16x16x32_bf16 v[148:151], v[208:211], a[32:35], v[148:151]\n"
"s_andn2_b32 s56, s55, s58\n"
"v_mul_f32 v30, s45, v30\n"
"v_mul_f32 v31, s45, v31\n"
"v_mfma_f32_16x16x32_bf16 v[148:151], v[212:215], a[36:39], v[148:151]\n"
"s_or_b32 s56, s56, s41\n"
"v_cvt_pk_bf16_f32 v24, v24, v25\n"
"v_cvt_pk_bf16_f32 v25, v26, v27\n"
"v_mfma_f32_16x16x32_bf16 v[124:127], v[208:211], a[192:195], v[192:195]\n"
"ds_read_b128 v[208:211], v230 offset:16384\n"
"v_cvt_pk_bf16_f32 v26, v28, v29\n"
"v_mfma_f32_16x16x32_bf16 v[124:127], v[212:215], a[196:199], v[124:127]\n"
"ds_read_b128 v[212:215], v231 offset:16384\n"
"v_cvt_pk_bf16_f32 v27, v30, v31\n"
"v_mfma_f32_16x16x32_bf16 v[124:127], v[216:219], a[200:203], v[124:127]\n"
"v_permlane16_swap_b32 v24, v26\n"
"v_mfma_f32_16x16x32_bf16 v[124:127], v[220:223], a[204:207], v[124:127]\n"
"s_waitcnt lgkmcnt(2)\n"
"v_permlane16_swap_b32 v25, v27\n"
"v_mfma_f32_16x16x32_bf16 a[232:235], v[176:179], a[244:247], 0\n"
"ds_read_b128 v[216:219], v230 offset:17408\n"
"v_accvgpr_write_b32 a156, v24\n"
"v_accvgpr_write_b32 a157, v25\n"
"v_accvgpr_write_b32 a158, v26\n"
"v_accvgpr_write_b32 a159, v27\n"
"v_mfma_f32_16x16x32_bf16 v[24:27], v[152:155], a[244:247], 0\n"
"s_add_u32 m0, s44, 49152\n"
"ds_read_b128 v[220:223], v231 offset:17408\n"
"v_mul_f32 v56, s45, v56\n"
"v_mul_f32 v57, s45, v57\n"
"v_mul_f32 v58, s45, v58\n"
"v_mul_f32 v59, s45, v59\n"
"v_mul_f32 v60, s45, v60\n"
"v_mul_f32 v61, s45, v61\n"
"v_mul_f32 v62, s45, v62\n"
"v_mul_f32 v63, s45, v63\n"
"v_cvt_pk_bf16_f32 v56, v56, v57\n"
"v_cvt_pk_bf16_f32 v57, v58, v59\n"
"v_cvt_pk_bf16_f32 v58, v60, v61\n"
"v_cvt_pk_bf16_f32 v59, v62, v63\n"
"v_mfma_f32_16x16x32_bf16 v[24:27], v[156:159], a[248:251], v[24:27]\n"
"buffer_load_dwordx4 v240, s[16:19], s56 offen nt lds\n"
"v_permlane16_swap_b32 v56, v58\n"
"v_permlane16_swap_b32 v57, v59\n"
"v_accvgpr_write_b32 a172, v56\n"
"v_accvgpr_write_b32 a173, v57\n"
"v_accvgpr_write_b32 a174, v58\n"
"v_accvgpr_write_b32 a175, v59\n"
"v_mfma_f32_16x16x32_bf16 v[56:59], v[160:163], a[244:247], 0\n"
"v_mul_f32 v88, s45, v88\n"
"v_mul_f32 v89, s45, v89\n"
"v_mul_f32 v90, s45, v90\n"
"v_mul_f32 v91, s45, v91\n"
"v_mul_f32 v92, s45, v92\n"
"v_mul_f32 v93, s45, v93\n"
"v_mul_f32 v94, s45, v94\n"
"v_mul_f32 v95, s45, v95\n"
"v_cvt_pk_bf16_f32 v88, v88, v89\n"
"v_cvt_pk_bf16_f32 v89, v90, v91\n"
"v_cvt_pk_bf16_f32 v90, v92, v93\n"
"v_cvt_pk_bf16_f32 v91, v94, v95\n"
"v_mfma_f32_16x16x32_bf16 v[56:59], v[164:167], a[248:251], v[56:59]\n"
"v_permlane16_swap_b32 v88, v90\n"
"v_permlane16_swap_b32 v89, v91\n"
"v_accvgpr_write_b32 a188, v88\n"
"v_accvgpr_write_b32 a189, v89\n"
"v_accvgpr_write_b32 a190, v90\n"
"v_accvgpr_write_b32 a191, v91\n"
"v_mfma_f32_16x16x32_bf16 v[88:91], v[168:171], a[244:247], 0\n"
"v_exp_f32 v128, v128\n"
"v_mfma_f32_16x16x32_bf16 v[88:91], v[172:175], a[248:251], v[88:91]\n"
"v_accvgpr_write_b32 a140, v200\n"
"v_accvgpr_write_b32 a141, v201\n"
"v_mfma_f32_16x16x32_bf16 v[92:95], v[172:175], v[224:227], 0\n"
"v_mov_b32 v200, 0\n"
"v_mov_b32 v201, 0\n"
"v_mfma_f32_16x16x32_bf16 v[92:95], v[168:171], a[252:255], v[92:95]\n"
"v_exp_f32 v129, v129\n"
"v_mfma_f32_16x16x32_bf16 v[60:63], v[164:167], v[224:227], 0\n"
"v_exp_f32 v130, v130\n"
"v_mfma_f32_16x16x32_bf16 v[60:63], v[160:163], a[252:255], v[60:63]\n"
"v_exp_f32 v131, v131\n"
"v_mfma_f32_16x16x32_bf16 v[28:31], v[156:159], v[224:227], 0\n"
"v_cvt_pk_bf16_f32 v104, v128, v129\n"
"v_cvt_pk_bf16_f32 v105, v130, v131\n"
"v_mfma_f32_16x16x32_bf16 v[28:31], v[152:155], a[252:255], v[28:31]\n"
"v_exp_f32 v132, v132\n"
"v_mfma_f32_16x16x32_bf16 a[236:239], v[176:179], a[252:255], 0\n"
"v_exp_f32 v133, v133\n"
"v_smfmac_f32_16x16x64_bf16 v[198:201], a[240:243], v[160:167], v241\n"
"s_waitcnt lgkmcnt(0)\n"
"s_add_u32 s40, s56, s52\n"
"buffer_load_dwordx4 v240, s[16:19], s40 offen offset:1024 nt lds\n"
"v_mfma_f32_16x16x32_bf16 v[128:131], v[208:211], a[0:3], v[180:183]\n"
"v_exp_f32 v134, v134\n"
"v_mfma_f32_16x16x32_bf16 v[128:131], v[212:215], a[4:7], v[128:131]\n"
"v_exp_f32 v135, v135\n"
"v_mfma_f32_16x16x32_bf16 v[128:131], v[216:219], a[8:11], v[128:131]\n"
"v_mul_f32 v116, s45, v116\n"
"v_mul_f32 v117, s45, v117\n"
"v_mul_f32 v118, s45, v118\n"
"v_mul_f32 v119, s45, v119\n"
"v_cvt_pk_bf16_f32 v112, v112, v113\n"
"v_cvt_pk_bf16_f32 v113, v114, v115\n"
"v_cvt_pk_bf16_f32 v114, v116, v117\n"
"v_cvt_pk_bf16_f32 v115, v118, v119\n"
"v_mfma_f32_16x16x32_bf16 v[128:131], v[220:223], a[12:15], v[128:131]\n"
"v_permlane16_swap_b32 v112, v114\n"
"v_permlane16_swap_b32 v113, v115\n"
"v_accvgpr_write_b32 a96, v112\n"
"v_accvgpr_write_b32 a97, v113\n"
"v_cvt_pk_bf16_f32 v112, v132, v133\n"
"v_cvt_pk_bf16_f32 v113, v134, v135\n"
"v_mfma_f32_16x16x32_bf16 v[132:135], v[216:219], a[24:27], v[184:187]\n"
"v_exp_f32 v136, v136\n"
"v_mfma_f32_16x16x32_bf16 v[132:135], v[220:223], a[28:31], v[132:135]\n"
"v_exp_f32 v137, v137\n"
"v_mfma_f32_16x16x32_bf16 v[132:135], v[208:211], a[16:19], v[132:135]\n"
"ds_read_b64_tr_b16 a[244:245], v232 offset:0\n"
"ds_read_b64_tr_b16 a[246:247], v232 offset:4096\n"
"v_exp_f32 v138, v138\n"
"v_exp_f32 v139, v139\n"
"v_mfma_f32_16x16x32_bf16 v[132:135], v[212:215], a[20:23], v[132:135]\n"
"ds_read_b64_tr_b16 a[248:249], v236 offset:0\n"
"v_accvgpr_write_b32 a100, v120\n"
"v_accvgpr_write_b32 a101, v121\n"
"v_cvt_pk_bf16_f32 v120, v136, v137\n"
"v_cvt_pk_bf16_f32 v121, v138, v139\n"
"v_mfma_f32_16x16x32_bf16 v[136:139], v[208:211], a[32:35], v[188:191]\n"
"ds_read_b64_tr_b16 a[250:251], v236 offset:4096\n"
"v_mul_f32 v0, s45, v0\n"
"v_mfma_f32_16x16x32_bf16 v[136:139], v[212:215], a[36:39], v[136:139]\n"
"ds_read_b64_tr_b16 v[224:225], v237 offset:0\n"
"v_mul_f32 v1, s45, v1\n"
"v_mfma_f32_16x16x32_bf16 v[136:139], v[216:219], a[40:43], v[136:139]\n"
"ds_read_b64_tr_b16 v[226:227], v237 offset:4096\n"
"v_mul_f32 v2, s45, v2\n"
"v_mfma_f32_16x16x32_bf16 v[136:139], v[220:223], a[44:47], v[136:139]\n"
"s_waitcnt lgkmcnt(4)\n"
"ds_read_b64_tr_b16 a[252:253], v233 offset:0\n"
"v_mul_f32 v3, s45, v3\n"
"v_mul_f32 v4, s45, v4\n"
"v_mul_f32 v5, s45, v5\n"
"v_mul_f32 v6, s45, v6\n"
"v_mul_f32 v7, s45, v7\n"
"v_cvt_pk_bf16_f32 v0, v0, v1\n"
"v_cvt_pk_bf16_f32 v1, v2, v3\n"
"v_cvt_pk_bf16_f32 v2, v4, v5\n"
"v_cvt_pk_bf16_f32 v3, v6, v7\n"
"v_mfma_f32_16x16x32_bf16 a[208:211], v[176:179], a[244:247], 0\n"
"s_add_u32 s40, s56, s53\n"
"buffer_load_dwordx4 v240, s[16:19], s40 offen offset:2048 nt lds\n"
"v_permlane16_swap_b32 v0, v2\n"
"v_permlane16_swap_b32 v1, v3\n"
"v_accvgpr_write_b32 a144, v0\n"
"v_accvgpr_write_b32 a145, v1\n"
"v_accvgpr_write_b32 a146, v2\n"
"v_accvgpr_write_b32 a147, v3\n"
"v_mfma_f32_16x16x32_bf16 v[0:3], v[152:155], a[244:247], 0\n"
"s_waitcnt lgkmcnt(3)\n"
"v_mul_f32 v32, s45, v32\n"
"v_mul_f32 v33, s45, v33\n"
"v_mul_f32 v34, s45, v34\n"
"v_mul_f32 v35, s45, v35\n"
"v_mul_f32 v36, s45, v36\n"
"v_mul_f32 v37, s45, v37\n"
"v_mul_f32 v38, s45, v38\n"
"v_mul_f32 v39, s45, v39\n"
"v_cvt_pk_bf16_f32 v32, v32, v33\n"
"v_cvt_pk_bf16_f32 v33, v34, v35\n"
"v_cvt_pk_bf16_f32 v34, v36, v37\n"
"v_cvt_pk_bf16_f32 v35, v38, v39\n"
"v_mfma_f32_16x16x32_bf16 v[0:3], v[156:159], a[248:251], v[0:3]\n"
"ds_read_b64_tr_b16 a[254:255], v233 offset:4096\n"
"v_permlane16_swap_b32 v32, v34\n"
"v_permlane16_swap_b32 v33, v35\n"
"v_accvgpr_write_b32 a160, v32\n"
"v_accvgpr_write_b32 a161, v33\n"
"v_accvgpr_write_b32 a162, v34\n"
"v_accvgpr_write_b32 a163, v35\n"
"v_mfma_f32_16x16x32_bf16 v[32:35], v[160:163], a[244:247], 0\n"
"v_mul_f32 v64, s45, v64\n"
"v_mul_f32 v65, s45, v65\n"
"v_mul_f32 v66, s45, v66\n"
"v_mul_f32 v67, s45, v67\n"
"v_mul_f32 v68, s45, v68\n"
"v_mul_f32 v69, s45, v69\n"
"v_mul_f32 v70, s45, v70\n"
"v_mul_f32 v71, s45, v71\n"
"v_cvt_pk_bf16_f32 v64, v64, v65\n"
"v_cvt_pk_bf16_f32 v65, v66, v67\n"
"v_cvt_pk_bf16_f32 v66, v68, v69\n"
"v_cvt_pk_bf16_f32 v67, v70, v71\n"
"v_mfma_f32_16x16x32_bf16 v[32:35], v[164:167], a[248:251], v[32:35]\n"
"v_permlane16_swap_b32 v64, v66\n"
"v_permlane16_swap_b32 v65, v67\n"
"v_accvgpr_write_b32 a176, v64\n"
"v_accvgpr_write_b32 a177, v65\n"
"v_accvgpr_write_b32 a178, v66\n"
"v_accvgpr_write_b32 a179, v67\n"
"v_mfma_f32_16x16x32_bf16 v[64:67], v[168:171], a[244:247], 0\n"
"v_exp_f32 v140, v140\n"
"v_mfma_f32_16x16x32_bf16 v[64:67], v[172:175], a[248:251], v[64:67]\n"
"s_waitcnt lgkmcnt(1)\n"
"v_exp_f32 v141, v141\n"
"v_mfma_f32_16x16x32_bf16 v[68:71], v[172:175], v[224:227], 0\n"
"s_waitcnt lgkmcnt(0)\n"
"v_exp_f32 v142, v142\n"
"v_mfma_f32_16x16x32_bf16 v[68:71], v[168:171], a[252:255], v[68:71]\n"
"ds_read_b128 v[208:211], v230 offset:20480\n"
"v_cvt_pk_bf16_f32 v98, v140, v141\n"
"v_mfma_f32_16x16x32_bf16 v[36:39], v[164:167], v[224:227], 0\n"
"ds_read_b128 v[212:215], v231 offset:20480\n"
"v_accvgpr_write_b32 a98, v114\n"
"v_mfma_f32_16x16x32_bf16 v[36:39], v[160:163], a[252:255], v[36:39]\n"
"ds_read_b128 v[216:219], v230 offset:21504\n"
"v_accvgpr_write_b32 a99, v115\n"
"v_mfma_f32_16x16x32_bf16 v[4:7], v[156:159], v[224:227], 0\n"
"ds_read_b128 v[220:223], v231 offset:21504\n"
"v_mul_f32 v16, s45, v16\n"
"v_mfma_f32_16x16x32_bf16 v[4:7], v[152:155], a[252:255], v[4:7]\n"
"s_add_u32 s40, s56, s54\n"
"buffer_load_dwordx4 v240, s[16:19], s40 offen offset:3072 nt lds\n"
"v_mfma_f32_16x16x32_bf16 a[212:215], v[176:179], a[252:255], 0\n"
"v_exp_f32 v143, v143\n"
"v_smfmac_f32_16x16x64_bf16 v[200:203], a[240:243], v[168:175], v241\n"
"s_waitcnt lgkmcnt(2)\n"
"v_cvt_pk_bf16_f32 v99, v142, v143\n"
"v_mul_f32 v17, s45, v17\n"
"v_mfma_f32_16x16x32_bf16 v[140:143], v[208:211], a[0:3], v[180:183]\n"
"v_exp_f32 v144, v144\n"
"v_mfma_f32_16x16x32_bf16 v[140:143], v[212:215], a[4:7], v[140:143]\n"
"s_waitcnt lgkmcnt(0)\n"
"v_exp_f32 v145, v145\n"
"v_mfma_f32_16x16x32_bf16 v[140:143], v[216:219], a[8:11], v[140:143]\n"
"v_exp_f32 v146, v146\n"
"v_exp_f32 v147, v147\n"
"v_mfma_f32_16x16x32_bf16 v[140:143], v[220:223], a[12:15], v[140:143]\n"
"v_cvt_pk_bf16_f32 v106, v144, v145\n"
"v_cvt_pk_bf16_f32 v107, v146, v147\n"
"v_mfma_f32_16x16x32_bf16 v[144:147], v[216:219], a[24:27], v[184:187]\n"
"v_exp_f32 v148, v148\n"
"v_mfma_f32_16x16x32_bf16 v[144:147], v[220:223], a[28:31], v[144:147]\n"
"v_exp_f32 v149, v149\n"
"v_mfma_f32_16x16x32_bf16 v[144:147], v[208:211], a[16:19], v[144:147]\n"
"ds_read_b64_tr_b16 a[244:245], v232 offset:1024\n"
"ds_read_b64_tr_b16 a[246:247], v232 offset:5120\n"
"v_exp_f32 v150, v150\n"
"v_exp_f32 v151, v151\n"
"v_mfma_f32_16x16x32_bf16 v[144:147], v[212:215], a[20:23], v[144:147]\n"
"v_cvt_pk_bf16_f32 v114, v148, v149\n"
"v_cvt_pk_bf16_f32 v115, v150, v151\n"
"v_mfma_f32_16x16x32_bf16 v[148:151], v[208:211], a[32:35], v[188:191]\n"
"ds_read_b64_tr_b16 a[248:249], v236 offset:1024\n"
"ds_read_b64_tr_b16 a[250:251], v236 offset:5120\n"
"v_mfma_f32_16x16x32_bf16 v[148:151], v[212:215], a[36:39], v[148:151]\n"
"s_cbranch_vccnz LPCE1_%=\nLPKE1_%=:\n"
"ds_read_b64_tr_b16 v[224:225], v237 offset:1024\n"
"v_mfma_f32_16x16x32_bf16 v[148:151], v[216:219], a[40:43], v[148:151]\n"
"v_mul_f32 v18, s45, v18\n"
"v_mul_f32 v19, s45, v19\n"
"v_mfma_f32_16x16x32_bf16 v[148:151], v[220:223], a[44:47], v[148:151]\n"
"v_mul_f32 v20, s45, v20\n"
"v_mul_f32 v21, s45, v21\n"
"v_mul_f32 v22, s45, v22\n"
"v_mul_f32 v23, s45, v23\n"
"v_cvt_pk_bf16_f32 v16, v16, v17\n"
"v_cvt_pk_bf16_f32 v17, v18, v19\n"
"v_cvt_pk_bf16_f32 v18, v20, v21\n"
"v_cvt_pk_bf16_f32 v19, v22, v23\n"
"s_waitcnt lgkmcnt(3)\n"
"v_mfma_f32_16x16x32_bf16 a[224:227], v[176:179], a[244:247], 0\n"
"s_waitcnt vmcnt(10)\n"
"v_permlane16_swap_b32 v16, v18\n"
"v_permlane16_swap_b32 v17, v19\n"
"v_accvgpr_write_b32 a152, v16\n"
"v_accvgpr_write_b32 a153, v17\n"
"v_accvgpr_write_b32 a154, v18\n"
"v_accvgpr_write_b32 a155, v19\n"
"v_mfma_f32_16x16x32_bf16 v[16:19], v[152:155], a[244:247], 0\n"
"s_barrier\n"
"ds_read_b64_tr_b16 v[226:227], v237 offset:5120\n"
"v_mul_f32 v48, s45, v48\n"
"v_mul_f32 v49, s45, v49\n"
"v_mul_f32 v50, s45, v50\n"
"v_mul_f32 v51, s45, v51\n"
"v_mul_f32 v52, s45, v52\n"
"v_mul_f32 v53, s45, v53\n"
"v_mul_f32 v54, s45, v54\n"
"v_mul_f32 v55, s45, v55\n"
"v_cvt_pk_bf16_f32 v48, v48, v49\n"
"v_cvt_pk_bf16_f32 v49, v50, v51\n"
"v_cvt_pk_bf16_f32 v50, v52, v53\n"
"v_cvt_pk_bf16_f32 v51, v54, v55\n"
"s_waitcnt lgkmcnt(2)\n"
"v_mfma_f32_16x16x32_bf16 v[16:19], v[156:159], a[248:251], v[16:19]\n"
"ds_read_b64_tr_b16 a[252:253], v233 offset:1024\n"
"ds_read_b64_tr_b16 a[254:255], v233 offset:5120\n"
"v_permlane16_swap_b32 v48, v50\n"
"v_permlane16_swap_b32 v49, v51\n"
"v_accvgpr_write_b32 a168, v48\n"
"v_accvgpr_write_b32 a169, v49\n"
"v_accvgpr_write_b32 a170, v50\n"
"v_accvgpr_write_b32 a171, v51\n"
"v_mfma_f32_16x16x32_bf16 v[48:51], v[160:163], a[244:247], 0\n"
"v_mul_f32 v80, s45, v80\n"
"v_mul_f32 v81, s45, v81\n"
"v_mul_f32 v82, s45, v82\n"
"v_mul_f32 v83, s45, v83\n"
"v_mul_f32 v84, s45, v84\n"
"v_mul_f32 v85, s45, v85\n"
"v_mul_f32 v86, s45, v86\n"
"v_mul_f32 v87, s45, v87\n"
"v_cvt_pk_bf16_f32 v80, v80, v81\n"
"v_cvt_pk_bf16_f32 v81, v82, v83\n"
"v_cvt_pk_bf16_f32 v82, v84, v85\n"
"v_cvt_pk_bf16_f32 v83, v86, v87\n"
"v_mfma_f32_16x16x32_bf16 v[48:51], v[164:167], a[248:251], v[48:51]\n"
"v_permlane16_swap_b32 v80, v82\n"
"v_permlane16_swap_b32 v81, v83\n"
"v_accvgpr_write_b32 a184, v80\n"
"v_accvgpr_write_b32 a185, v81\n"
"v_accvgpr_write_b32 a186, v82\n"
"v_accvgpr_write_b32 a187, v83\n"
"v_mfma_f32_16x16x32_bf16 v[80:83], v[168:171], a[244:247], 0\n"
"v_exp_f32 v124, v124\n"
"v_mfma_f32_16x16x32_bf16 v[80:83], v[172:175], a[248:251], v[80:83]\n"
"s_waitcnt lgkmcnt(2)\n"
"v_exp_f32 v125, v125\n"
"v_mfma_f32_16x16x32_bf16 v[84:87], v[172:175], v[224:227], 0\n"
"s_waitcnt lgkmcnt(0)\n"
"v_exp_f32 v126, v126\n"
"v_mfma_f32_16x16x32_bf16 v[84:87], v[168:171], a[252:255], v[84:87]\n"
"ds_read_b128 v[208:211], v228 offset:32768\n"
"v_accvgpr_write_b32 a102, v122\n"
"v_mfma_f32_16x16x32_bf16 v[52:55], v[164:167], v[224:227], 0\n"
"ds_read_b128 v[212:215], v229 offset:32768\n"
"v_accvgpr_write_b32 a103, v123\n"
"v_mfma_f32_16x16x32_bf16 v[52:55], v[160:163], a[252:255], v[52:55]\n"
"ds_read_b128 v[216:219], v228 offset:33792\n"
"v_bfe_u32 v246, v242, 8, 2\n"
"v_mfma_f32_16x16x32_bf16 v[20:23], v[156:159], v[224:227], 0\n"
"ds_read_b128 v[220:223], v229 offset:33792\n"
"v_lshlrev_b32 v246, 6, v246\n"
"v_mfma_f32_16x16x32_bf16 v[20:23], v[152:155], a[252:255], v[20:23]\n"
"ds_read_b64_tr_b16 a[244:245], v234 offset:16384\n"
"v_exp_f32 v127, v127\n"
"v_exp_f32 v128, v128\n"
"v_exp_f32 v129, v129\n"
"v_exp_f32 v130, v130\n"
"v_exp_f32 v131, v131\n"
"v_exp_f32 v132, v132\n"
"v_exp_f32 v133, v133\n"
"v_exp_f32 v134, v134\n"
"v_exp_f32 v135, v135\n"
"v_exp_f32 v136, v136\n"
"v_exp_f32 v137, v137\n"
"v_exp_f32 v138, v138\n"
"v_exp_f32 v139, v139\n"
"v_exp_f32 v140, v140\n"
"v_exp_f32 v141, v141\n"
"v_exp_f32 v142, v142\n"
"v_exp_f32 v143, v143\n"
"v_exp_f32 v144, v144\n"
"v_exp_f32 v145, v145\n"
"v_exp_f32 v146, v146\n"
"v_exp_f32 v147, v147\n"
"v_exp_f32 v148, v148\n"
"v_exp_f32 v149, v149\n"
"v_mfma_f32_16x16x32_bf16 a[228:231], v[176:179], a[252:255], 0\n"
"ds_read_b64_tr_b16 a[246:247], v234 offset:20480\n"
"v_xor_b32 v246, v246, v242\n"
"v_bfe_u32 v249, v242, 6, 4\n"
"v_xor_b32 v247, v247, v249\n"
"v_lshl_add_u32 v247, v247, 4, s44\n"
"v_cvt_pk_bf16_f32 v122, v124, v125\n"
"v_cvt_pk_bf16_f32 v123, v126, v127\n"
"v_cvt_pk_bf16_f32 v100, v128, v129\n"
"v_cvt_pk_bf16_f32 v101, v130, v131\n"
"v_cvt_pk_bf16_f32 v108, v132, v133\n"
"v_cvt_pk_bf16_f32 v109, v134, v135\n"
"v_cvt_pk_bf16_f32 v116, v136, v137\n"
"v_cvt_pk_bf16_f32 v117, v138, v139\n"
"v_cvt_pk_bf16_f32 v102, v140, v141\n"
"v_cvt_pk_bf16_f32 v103, v142, v143\n"
"v_cvt_pk_bf16_f32 v110, v144, v145\n"
"v_cvt_pk_bf16_f32 v111, v146, v147\n"
"v_cvt_pk_bf16_f32 v118, v148, v149\n"
"s_branch LAY2_%=\n"
"LAL_%=:\n"
"s_waitcnt lgkmcnt(0)\n"
"v_mfma_f32_16x16x32_bf16 a[224:227], v[176:179], a[244:247], a[224:227]\n"
"s_waitcnt vmcnt(8)\n"
"v_exp_f32 v142, v142\n"
"v_mfma_f32_16x16x32_bf16 v[16:19], v[152:155], a[244:247], v[16:19]\n"
"s_barrier\n"
"ds_read_b128 v[208:211], v228 offset:0\n"
"v_mfma_f32_16x16x32_bf16 v[16:19], v[156:159], a[248:251], v[16:19]\n"
"ds_read_b128 v[212:215], v229 offset:0\n"
"v_cvt_pk_bf16_f32 v102, v140, v141\n"
"v_mfma_f32_16x16x32_bf16 v[48:51], v[160:163], a[244:247], v[48:51]\n"
"v_exp_f32 v143, v143\n"
"v_mfma_f32_16x16x32_bf16 v[48:51], v[164:167], a[248:251], v[48:51]\n"
"v_cvt_pk_bf16_f32 v103, v142, v143\n"
"v_mfma_f32_16x16x32_bf16 v[80:83], v[168:171], a[244:247], v[80:83]\n"
"ds_read_b128 v[216:219], v228 offset:1024\n"
"v_exp_f32 v144, v144\n"
"v_mfma_f32_16x16x32_bf16 v[80:83], v[172:175], a[248:251], v[80:83]\n"
"v_exp_f32 v145, v145\n"
"v_mfma_f32_16x16x32_bf16 v[84:87], v[172:175], v[224:227], v[84:87]\n"
"v_cvt_pk_bf16_f32 v110, v144, v145\n"
"v_mfma_f32_16x16x32_bf16 v[84:87], v[168:171], a[252:255], v[84:87]\n"
"ds_read_b128 v[220:223], v229 offset:1024\n"
"v_exp_f32 v146, v146\n"
"v_mfma_f32_16x16x32_bf16 v[52:55], v[164:167], v[224:227], v[52:55]\n"
"v_exp_f32 v147, v147\n"
"v_mfma_f32_16x16x32_bf16 v[52:55], v[160:163], a[252:255], v[52:55]\n"
"ds_read_b64_tr_b16 a[244:245], v234 offset:49152\n"
"v_cvt_pk_bf16_f32 v111, v146, v147\n"
"v_mfma_f32_16x16x32_bf16 v[20:23], v[156:159], v[224:227], v[20:23]\n"
"v_exp_f32 v148, v148\n"
"v_mfma_f32_16x16x32_bf16 v[20:23], v[152:155], a[252:255], v[20:23]\n"
"v_exp_f32 v149, v149\n"
"v_mfma_f32_16x16x32_bf16 a[228:231], v[176:179], a[252:255], a[228:231]\n"
"ds_read_b64_tr_b16 a[246:247], v234 offset:53248\n"
"v_cvt_pk_bf16_f32 v118, v148, v149\n"
"s_waitcnt lgkmcnt(2)\n"
"v_mfma_f32_16x16x32_bf16 v[124:127], v[208:211], a[0:3], v[180:183]\n"
"s_add_u32 m0, s44, 81920\n"
"v_exp_f32 v150, v150\n"
"v_mfma_f32_16x16x32_bf16 v[124:127], v[212:215], a[4:7], v[124:127]\n"
"buffer_load_dwordx4 v240, s[20:23], s56 offen nt lds\n"
"v_mfma_f32_16x16x32_bf16 v[124:127], v[216:219], a[8:11], v[124:127]\n"
"v_exp_f32 v151, v151\n"
"v_mfma_f32_16x16x32_bf16 v[124:127], v[220:223], a[12:15], v[124:127]\n"
"ds_read_b64_tr_b16 a[248:249], v238 offset:49152\n"
"v_cvt_pk_bf16_f32 v119, v150, v151\n"
"v_mfma_f32_16x16x32_bf16 v[128:131], v[216:219], a[24:27], v[184:187]\n"
"ds_read_b64_tr_b16 a[250:251], v238 offset:53248\n"
"v_mfma_f32_16x16x32_bf16 v[128:131], v[220:223], a[28:31], v[128:131]\n"
"ds_read_b64_tr_b16 v[224:225], v239 offset:49152\n"
"v_mfma_f32_16x16x32_bf16 v[128:131], v[208:211], a[16:19], v[128:131]\n"
"ds_read_b64_tr_b16 v[226:227], v239 offset:53248\n"
"v_mfma_f32_16x16x32_bf16 v[128:131], v[212:215], a[20:23], v[128:131]\n"
"ds_read_b64_tr_b16 a[252:253], v235 offset:49152\n"
"v_mfma_f32_16x16x32_bf16 v[132:135], v[208:211], a[32:35], v[188:191]\n"
"ds_read_b64_tr_b16 a[254:255], v235 offset:53248\n"
"v_mfma_f32_16x16x32_bf16 v[132:135], v[212:215], a[36:39], v[132:135]\n"
"v_exp_f32 v124, v124\n"
"v_mfma_f32_16x16x32_bf16 v[132:135], v[216:219], a[40:43], v[132:135]\n"
"v_exp_f32 v125, v125\n"
"v_mfma_f32_16x16x32_bf16 v[132:135], v[220:223], a[44:47], v[132:135]\n"
"v_cvt_pk_bf16_f32 v152, v124, v125\n"
"v_mfma_f32_16x16x32_bf16 v[136:139], v[216:219], a[200:203], v[192:195]\n"
"v_exp_f32 v126, v126\n"
"v_mfma_f32_16x16x32_bf16 v[136:139], v[220:223], a[204:207], v[136:139]\n"
"s_add_u32 s40, s56, s52\n"
"buffer_load_dwordx4 v240, s[20:23], s40 offen offset:1024 nt lds\n"
"v_mfma_f32_16x16x32_bf16 v[136:139], v[208:211], a[192:195], v[136:139]\n"
"v_exp_f32 v127, v127\n"
"v_mfma_f32_16x16x32_bf16 v[136:139], v[212:215], a[196:199], v[136:139]\n"
"s_waitcnt lgkmcnt(0)\n"
"ds_read_b128 v[216:219], v228 offset:5120\n"
"v_cvt_pk_bf16_f32 v153, v126, v127\n"
"v_mfma_f32_16x16x32_bf16 a[216:219], v[120:123], a[244:247], a[216:219]\n"
"v_exp_f32 v128, v128\n"
"v_mfma_f32_16x16x32_bf16 v[8:11], v[96:99], a[244:247], v[8:11]\n"
"v_exp_f32 v129, v129\n"
"v_mfma_f32_16x16x32_bf16 v[8:11], v[100:103], a[248:251], v[8:11]\n"
"ds_read_b128 v[220:223], v229 offset:5120\n"
"v_cvt_pk_bf16_f32 v160, v128, v129\n"
"v_mfma_f32_16x16x32_bf16 v[40:43], v[104:107], a[244:247], v[40:43]\n"
"v_exp_f32 v130, v130\n"
"v_mfma_f32_16x16x32_bf16 v[40:43], v[108:111], a[248:251], v[40:43]\n"
"v_exp_f32 v131, v131\n"
"v_mfma_f32_16x16x32_bf16 v[72:75], v[112:115], a[244:247], v[72:75]\n"
"ds_read_b128 v[208:211], v228 offset:4096\n"
"v_cvt_pk_bf16_f32 v161, v130, v131\n"
"v_mfma_f32_16x16x32_bf16 v[72:75], v[116:119], a[248:251], v[72:75]\n"
"v_exp_f32 v132, v132\n"
"v_mfma_f32_16x16x32_bf16 v[76:79], v[116:119], v[224:227], v[76:79]\n"
"v_exp_f32 v133, v133\n"
"v_mfma_f32_16x16x32_bf16 v[76:79], v[112:115], a[252:255], v[76:79]\n"
"ds_read_b128 v[212:215], v229 offset:4096\n"
"ds_read_b64_tr_b16 a[244:245], v234 offset:50176\n"
"v_cvt_pk_bf16_f32 v168, v132, v133\n"
"v_mfma_f32_16x16x32_bf16 v[44:47], v[108:111], v[224:227], v[44:47]\n"
"s_add_u32 s40, s56, s53\n"
"buffer_load_dwordx4 v240, s[20:23], s40 offen offset:2048 nt lds\n"
"v_mfma_f32_16x16x32_bf16 v[44:47], v[104:107], a[252:255], v[44:47]\n"
"v_exp_f32 v134, v134\n"
"v_mfma_f32_16x16x32_bf16 v[12:15], v[100:103], v[224:227], v[12:15]\n"
"v_exp_f32 v135, v135\n"
"v_mfma_f32_16x16x32_bf16 v[12:15], v[96:99], a[252:255], v[12:15]\n"
"ds_read_b64_tr_b16 a[246:247], v234 offset:54272\n"
"v_cvt_pk_bf16_f32 v169, v134, v135\n"
"v_mfma_f32_16x16x32_bf16 a[220:223], v[120:123], a[252:255], a[220:223]\n"
"v_exp_f32 v136, v136\n"
"v_smfmac_f32_16x16x64_bf16 v[196:199], a[240:243], v[96:103], v241\n"
"v_exp_f32 v137, v137\n"
"v_mfma_f32_16x16x32_bf16 v[202:205], a[240:243], v[120:123], v[202:205]\n"
"s_waitcnt lgkmcnt(1)\n"
"ds_read_b64_tr_b16 a[248:249], v238 offset:50176\n"
"v_cvt_pk_bf16_f32 v176, v136, v137\n"
"v_mfma_f32_16x16x32_bf16 v[140:143], v[216:219], a[8:11], v[180:183]\n"
"v_exp_f32 v138, v138\n"
"v_mfma_f32_16x16x32_bf16 v[140:143], v[220:223], a[12:15], v[140:143]\n"
"v_exp_f32 v139, v139\n"
"v_mfma_f32_16x16x32_bf16 v[140:143], v[208:211], a[0:3], v[140:143]\n"
"ds_read_b64_tr_b16 a[250:251], v238 offset:54272\n"
"v_cvt_pk_bf16_f32 v177, v138, v139\n"
"v_mfma_f32_16x16x32_bf16 v[140:143], v[212:215], a[4:7], v[140:143]\n"
"ds_read_b64_tr_b16 v[224:225], v239 offset:50176\n"
"v_mfma_f32_16x16x32_bf16 v[144:147], v[208:211], a[16:19], v[184:187]\n"
"ds_read_b64_tr_b16 v[226:227], v239 offset:54272\n"
"v_mfma_f32_16x16x32_bf16 v[144:147], v[212:215], a[20:23], v[144:147]\n"
"ds_read_b64_tr_b16 a[252:253], v235 offset:50176\n"
"v_mfma_f32_16x16x32_bf16 v[144:147], v[216:219], a[24:27], v[144:147]\n"
"s_add_u32 s40, s56, s54\n"
"buffer_load_dwordx4 v240, s[20:23], s40 offen offset:3072 nt lds\n"
"v_mfma_f32_16x16x32_bf16 v[144:147], v[220:223], a[28:31], v[144:147]\n"
"s_add_u32 s55, s55, s59\n"
"ds_read_b64_tr_b16 a[254:255], v235 offset:54272\n"
"v_mfma_f32_16x16x32_bf16 v[148:151], v[216:219], a[40:43], v[188:191]\n"
"s_add_u32 s41, s55, s57\n"
"v_mfma_f32_16x16x32_bf16 v[148:151], v[220:223], a[44:47], v[148:151]\n"
"s_and_b32 s41, s41, s58\n"
"v_exp_f32 v140, v140\n"
"v_mfma_f32_16x16x32_bf16 v[148:151], v[208:211], a[32:35], v[148:151]\n"
"s_andn2_b32 s56, s55, s58\n"
"v_exp_f32 v141, v141\n"
"v_mfma_f32_16x16x32_bf16 v[148:151], v[212:215], a[36:39], v[148:151]\n"
"s_or_b32 s56, s56, s41\n"
"v_exp_f32 v142, v142\n"
"v_mfma_f32_16x16x32_bf16 v[124:127], v[208:211], a[192:195], v[192:195]\n"
"ds_read_b128 v[208:211], v230 offset:0\n"
"v_cvt_pk_bf16_f32 v154, v140, v141\n"
"v_mfma_f32_16x16x32_bf16 v[124:127], v[212:215], a[196:199], v[124:127]\n"
"v_exp_f32 v143, v143\n"
"v_mfma_f32_16x16x32_bf16 v[124:127], v[216:219], a[200:203], v[124:127]\n"
"ds_read_b128 v[212:215], v231 offset:0\n"
"v_cvt_pk_bf16_f32 v155, v142, v143\n"
"v_mfma_f32_16x16x32_bf16 v[124:127], v[220:223], a[204:207], v[124:127]\n"
"s_waitcnt lgkmcnt(2)\n"
"v_exp_f32 v144, v144\n"
"v_mfma_f32_16x16x32_bf16 a[232:235], v[120:123], a[244:247], a[232:235]\n"
"v_exp_f32 v145, v145\n"
"v_mfma_f32_16x16x32_bf16 v[24:27], v[96:99], a[244:247], v[24:27]\n"
"s_add_u32 m0, s44, 32768\n"
"ds_read_b128 v[216:219], v230 offset:1024\n"
"v_cvt_pk_bf16_f32 v162, v144, v145\n"
"v_mfma_f32_16x16x32_bf16 v[24:27], v[100:103], a[248:251], v[24:27]\n"
"buffer_load_dwordx4 v240, s[16:19], s56 offen nt lds\n"
"v_mfma_f32_16x16x32_bf16 v[56:59], v[104:107], a[244:247], v[56:59]\n"
"v_exp_f32 v146, v146\n"
"v_mfma_f32_16x16x32_bf16 v[56:59], v[108:111], a[248:251], v[56:59]\n"
"v_exp_f32 v147, v147\n"
"v_mfma_f32_16x16x32_bf16 v[88:91], v[112:115], a[244:247], v[88:91]\n"
"ds_read_b128 v[220:223], v231 offset:1024\n"
"v_cvt_pk_bf16_f32 v163, v146, v147\n"
"v_mfma_f32_16x16x32_bf16 v[88:91], v[116:119], a[248:251], v[88:91]\n"
"v_exp_f32 v148, v148\n"
"v_mfma_f32_16x16x32_bf16 v[92:95], v[116:119], v[224:227], v[92:95]\n"
"v_exp_f32 v149, v149\n"
"v_mfma_f32_16x16x32_bf16 v[92:95], v[112:115], a[252:255], v[92:95]\n"
"ds_read_b64_tr_b16 a[244:245], v232 offset:49152\n"
"v_cvt_pk_bf16_f32 v170, v148, v149\n"
"v_mfma_f32_16x16x32_bf16 v[60:63], v[108:111], v[224:227], v[60:63]\n"
"v_exp_f32 v150, v150\n"
"v_mfma_f32_16x16x32_bf16 v[60:63], v[104:107], a[252:255], v[60:63]\n"
"v_exp_f32 v151, v151\n"
"v_mfma_f32_16x16x32_bf16 v[28:31], v[100:103], v[224:227], v[28:31]\n"
"ds_read_b64_tr_b16 a[246:247], v232 offset:53248\n"
"v_cvt_pk_bf16_f32 v171, v150, v151\n"
"v_mfma_f32_16x16x32_bf16 v[28:31], v[96:99], a[252:255], v[28:31]\n"
"v_exp_f32 v124, v124\n"
"v_mfma_f32_16x16x32_bf16 a[236:239], v[120:123], a[252:255], a[236:239]\n"
"v_exp_f32 v125, v125\n"
"v_smfmac_f32_16x16x64_bf16 v[198:201], a[240:243], v[104:111], v241\n"
"s_waitcnt lgkmcnt(1)\n"
"s_add_u32 s40, s56, s52\n"
"buffer_load_dwordx4 v240, s[16:19], s40 offen offset:1024 nt lds\n"
"v_mfma_f32_16x16x32_bf16 v[128:131], v[208:211], a[0:3], v[180:183]\n"
"ds_read_b64_tr_b16 a[248:249], v236 offset:49152\n"
"v_cvt_pk_bf16_f32 v178, v124, v125\n"
"v_mfma_f32_16x16x32_bf16 v[128:131], v[212:215], a[4:7], v[128:131]\n"
"v_exp_f32 v126, v126\n"
"v_mfma_f32_16x16x32_bf16 v[128:131], v[216:219], a[8:11], v[128:131]\n"
"v_exp_f32 v127, v127\n"
"v_mfma_f32_16x16x32_bf16 v[128:131], v[220:223], a[12:15], v[128:131]\n"
"ds_read_b64_tr_b16 a[250:251], v236 offset:53248\n"
"v_cvt_pk_bf16_f32 v179, v126, v127\n"
"v_mfma_f32_16x16x32_bf16 v[132:135], v[216:219], a[24:27], v[184:187]\n"
"ds_read_b64_tr_b16 v[224:225], v237 offset:49152\n"
"v_mfma_f32_16x16x32_bf16 v[132:135], v[220:223], a[28:31], v[132:135]\n"
"ds_read_b64_tr_b16 v[226:227], v237 offset:53248\n"
"v_mfma_f32_16x16x32_bf16 v[132:135], v[208:211], a[16:19], v[132:135]\n"
"ds_read_b64_tr_b16 a[252:253], v233 offset:49152\n"
"v_mfma_f32_16x16x32_bf16 v[132:135], v[212:215], a[20:23], v[132:135]\n"
"ds_read_b64_tr_b16 a[254:255], v233 offset:53248\n"
"v_mfma_f32_16x16x32_bf16 v[136:139], v[208:211], a[32:35], v[188:191]\n"
"ds_read_b128 v[208:211], v230 offset:4096\n"
"v_mfma_f32_16x16x32_bf16 v[136:139], v[212:215], a[36:39], v[136:139]\n"
"v_exp_f32 v128, v128\n"
"v_mfma_f32_16x16x32_bf16 v[136:139], v[216:219], a[40:43], v[136:139]\n"
"v_exp_f32 v129, v129\n"
"v_mfma_f32_16x16x32_bf16 v[136:139], v[220:223], a[44:47], v[136:139]\n"
"s_waitcnt lgkmcnt(2)\n"
"ds_read_b128 v[212:215], v231 offset:4096\n"
"v_cvt_pk_bf16_f32 v156, v128, v129\n"
"v_mfma_f32_16x16x32_bf16 a[208:211], v[120:123], a[244:247], a[208:211]\n"
"s_add_u32 s40, s56, s53\n"
"buffer_load_dwordx4 v240, s[16:19], s40 offen offset:2048 nt lds\n"
"v_mfma_f32_16x16x32_bf16 v[0:3], v[96:99], a[244:247], v[0:3]\n"
"v_exp_f32 v130, v130\n"
"v_mfma_f32_16x16x32_bf16 v[0:3], v[100:103], a[248:251], v[0:3]\n"
"v_exp_f32 v131, v131\n"
"v_mfma_f32_16x16x32_bf16 v[32:35], v[104:107], a[244:247], v[32:35]\n"
"ds_read_b128 v[216:219], v230 offset:5120\n"
"v_cvt_pk_bf16_f32 v157, v130, v131\n"
"v_mfma_f32_16x16x32_bf16 v[32:35], v[108:111], a[248:251], v[32:35]\n"
"v_exp_f32 v132, v132\n"
"v_mfma_f32_16x16x32_bf16 v[64:67], v[112:115], a[244:247], v[64:67]\n"
"v_exp_f32 v133, v133\n"
"v_mfma_f32_16x16x32_bf16 v[64:67], v[116:119], a[248:251], v[64:67]\n"
"ds_read_b128 v[220:223], v231 offset:5120\n"
"v_cvt_pk_bf16_f32 v164, v132, v133\n"
"v_mfma_f32_16x16x32_bf16 v[68:71], v[116:119], v[224:227], v[68:71]\n"
"s_waitcnt lgkmcnt(2)\n"
"v_exp_f32 v134, v134\n"
"v_mfma_f32_16x16x32_bf16 v[68:71], v[112:115], a[252:255], v[68:71]\n"
"v_exp_f32 v135, v135\n"
"v_mfma_f32_16x16x32_bf16 v[36:39], v[108:111], v[224:227], v[36:39]\n"
"ds_read_b64_tr_b16 a[244:245], v232 offset:50176\n"
"v_cvt_pk_bf16_f32 v165, v134, v135\n"
"v_mfma_f32_16x16x32_bf16 v[36:39], v[104:107], a[252:255], v[36:39]\n"
"v_exp_f32 v136, v136\n"
"v_mfma_f32_16x16x32_bf16 v[4:7], v[100:103], v[224:227], v[4:7]\n"
"v_exp_f32 v137, v137\n"
"v_mfma_f32_16x16x32_bf16 v[4:7], v[96:99], a[252:255], v[4:7]\n"
"s_add_u32 s40, s56, s54\n"
"buffer_load_dwordx4 v240, s[16:19], s40 offen offset:3072 nt lds\n"
"v_mfma_f32_16x16x32_bf16 a[212:215], v[120:123], a[252:255], a[212:215]\n"
"ds_read_b64_tr_b16 a[246:247], v232 offset:54272\n"
"v_cvt_pk_bf16_f32 v172, v136, v137\n"
"v_smfmac_f32_16x16x64_bf16 v[200:203], a[240:243], v[112:119], v241\n"
"v_exp_f32 v138, v138\n"
"v_mfma_f32_16x16x32_bf16 v[140:143], v[208:211], a[0:3], v[180:183]\n"
"v_exp_f32 v139, v139\n"
"v_mfma_f32_16x16x32_bf16 v[140:143], v[212:215], a[4:7], v[140:143]\n"
"s_waitcnt lgkmcnt(1)\n"
"ds_read_b64_tr_b16 a[248:249], v236 offset:50176\n"
"v_cvt_pk_bf16_f32 v173, v138, v139\n"
"v_mfma_f32_16x16x32_bf16 v[140:143], v[216:219], a[8:11], v[140:143]\n"
"ds_read_b64_tr_b16 a[250:251], v236 offset:54272\n"
"v_mfma_f32_16x16x32_bf16 v[140:143], v[220:223], a[12:15], v[140:143]\n"
"ds_read_b64_tr_b16 v[224:225], v237 offset:50176\n"
"v_mfma_f32_16x16x32_bf16 v[144:147], v[216:219], a[24:27], v[184:187]\n"
"ds_read_b64_tr_b16 v[226:227], v237 offset:54272\n"
"v_mfma_f32_16x16x32_bf16 v[144:147], v[220:223], a[28:31], v[144:147]\n"
"ds_read_b64_tr_b16 a[252:253], v233 offset:50176\n"
"v_mfma_f32_16x16x32_bf16 v[144:147], v[208:211], a[16:19], v[144:147]\n"
"ds_read_b64_tr_b16 a[254:255], v233 offset:54272\n"
"v_mfma_f32_16x16x32_bf16 v[144:147], v[212:215], a[20:23], v[144:147]\n"
"v_mfma_f32_16x16x32_bf16 v[148:151], v[208:211], a[32:35], v[188:191]\n"
"v_mfma_f32_16x16x32_bf16 v[148:151], v[212:215], a[36:39], v[148:151]\n"
"s_cbranch_vccnz LPCL0_%=\nLPKL0_%=:\n"
"v_mfma_f32_16x16x32_bf16 v[148:151], v[216:219], a[40:43], v[148:151]\n"
"v_exp_f32 v140, v140\n"
"v_mfma_f32_16x16x32_bf16 v[148:151], v[220:223], a[44:47], v[148:151]\n"
"v_exp_f32 v141, v141\n"
"s_waitcnt lgkmcnt(0)\n"
"v_mfma_f32_16x16x32_bf16 a[224:227], v[120:123], a[244:247], a[224:227]\n"
"s_waitcnt vmcnt(8)\n"
"v_exp_f32 v142, v142\n"
"v_mfma_f32_16x16x32_bf16 v[16:19], v[96:99], a[244:247], v[16:19]\n"
"s_barrier\n"
"ds_read_b128 v[208:211], v228 offset:16384\n"
"v_mfma_f32_16x16x32_bf16 v[16:19], v[100:103], a[248:251], v[16:19]\n"
"ds_read_b128 v[212:215], v229 offset:16384\n"
"v_cvt_pk_bf16_f32 v158, v140, v141\n"
"v_mfma_f32_16x16x32_bf16 v[48:51], v[104:107], a[244:247], v[48:51]\n"
"v_exp_f32 v143, v143\n"
"v_mfma_f32_16x16x32_bf16 v[48:51], v[108:111], a[248:251], v[48:51]\n"
"v_cvt_pk_bf16_f32 v159, v142, v143\n"
"v_mfma_f32_16x16x32_bf16 v[80:83], v[112:115], a[244:247], v[80:83]\n"
"ds_read_b128 v[216:219], v228 offset:17408\n"
"v_exp_f32 v144, v144\n"
"v_mfma_f32_16x16x32_bf16 v[80:83], v[116:119], a[248:251], v[80:83]\n"
"v_exp_f32 v145, v145\n"
"v_mfma_f32_16x16x32_bf16 v[84:87], v[116:119], v[224:227], v[84:87]\n"
"v_cvt_pk_bf16_f32 v166, v144, v145\n"
"v_mfma_f32_16x16x32_bf16 v[84:87], v[112:115], a[252:255], v[84:87]\n"
"ds_read_b128 v[220:223], v229 offset:17408\n"
"v_exp_f32 v146, v146\n"
"v_mfma_f32_16x16x32_bf16 v[52:55], v[108:111], v[224:227], v[52:55]\n"
"v_exp_f32 v147, v147\n"
"v_mfma_f32_16x16x32_bf16 v[52:55], v[104:107], a[252:255], v[52:55]\n"
"ds_read_b64_tr_b16 a[244:245], v234 offset:0\n"
"v_cvt_pk_bf16_f32 v167, v146, v147\n"
"v_mfma_f32_16x16x32_bf16 v[20:23], v[100:103], v[224:227], v[20:23]\n"
"v_exp_f32 v148, v148\n"
"v_mfma_f32_16x16x32_bf16 v[20:23], v[96:99], a[252:255], v[20:23]\n"
"v_exp_f32 v149, v149\n"
"v_mfma_f32_16x16x32_bf16 a[228:231], v[120:123], a[252:255], a[228:231]\n"
"ds_read_b64_tr_b16 a[246:247], v234 offset:4096\n"
"v_cvt_pk_bf16_f32 v174, v148, v149\n"
"s_waitcnt lgkmcnt(2)\n"
"v_mfma_f32_16x16x32_bf16 v[124:127], v[208:211], a[0:3], v[180:183]\n"
"s_add_u32 m0, s44, 98304\n"
"v_exp_f32 v150, v150\n"
"v_mfma_f32_16x16x32_bf16 v[124:127], v[212:215], a[4:7], v[124:127]\n"
"buffer_load_dwordx4 v240, s[20:23], s56 offen nt lds\n"
"v_mfma_f32_16x16x32_bf16 v[124:127], v[216:219], a[8:11], v[124:127]\n"
"v_exp_f32 v151, v151\n"
"v_mfma_f32_16x16x32_bf16 v[124:127], v[220:223], a[12:15], v[124:127]\n"
"ds_read_b64_tr_b16 a[248:249], v238 offset:0\n"
"v_cvt_pk_bf16_f32 v175, v150, v151\n"
"v_mfma_f32_16x16x32_bf16 v[128:131], v[216:219], a[24:27], v[184:187]\n"
"ds_read_b64_tr_b16 a[250:251], v238 offset:4096\n"
"v_mfma_f32_16x16x32_bf16 v[128:131], v[220:223], a[28:31], v[128:131]\n"
"ds_read_b64_tr_b16 v[224:225], v239 offset:0\n"
"v_mfma_f32_16x16x32_bf16 v[128:131], v[208:211], a[16:19], v[128:131]\n"
"ds_read_b64_tr_b16 v[226:227], v239 offset:4096\n"
"v_mfma_f32_16x16x32_bf16 v[128:131], v[212:215], a[20:23], v[128:131]\n"
"ds_read_b64_tr_b16 a[252:253], v235 offset:0\n"
"v_mfma_f32_16x16x32_bf16 v[132:135], v[208:211], a[32:35], v[188:191]\n"
"ds_read_b64_tr_b16 a[254:255], v235 offset:4096\n"
"v_mfma_f32_16x16x32_bf16 v[132:135], v[212:215], a[36:39], v[132:135]\n"
"v_exp_f32 v124, v124\n"
"v_mfma_f32_16x16x32_bf16 v[132:135], v[216:219], a[40:43], v[132:135]\n"
"v_exp_f32 v125, v125\n"
"v_mfma_f32_16x16x32_bf16 v[132:135], v[220:223], a[44:47], v[132:135]\n"
"v_cvt_pk_bf16_f32 v96, v124, v125\n"
"v_mfma_f32_16x16x32_bf16 v[136:139], v[216:219], a[200:203], v[192:195]\n"
"v_exp_f32 v126, v126\n"
"v_mfma_f32_16x16x32_bf16 v[136:139], v[220:223], a[204:207], v[136:139]\n"
"s_add_u32 s40, s56, s52\n"
"buffer_load_dwordx4 v240, s[20:23], s40 offen offset:1024 nt lds\n"
"v_mfma_f32_16x16x32_bf16 v[136:139], v[208:211], a[192:195], v[136:139]\n"
"v_exp_f32 v127, v127\n"
"v_mfma_f32_16x16x32_bf16 v[136:139], v[212:215], a[196:199], v[136:139]\n"
"s_waitcnt lgkmcnt(0)\n"
"ds_read_b128 v[216:219], v228 offset:21504\n"
"v_cvt_pk_bf16_f32 v97, v126, v127\n"
"v_mfma_f32_16x16x32_bf16 a[216:219], v[176:179], a[244:247], a[216:219]\n"
"v_exp_f32 v128, v128\n"
"v_mfma_f32_16x16x32_bf16 v[8:11], v[152:155], a[244:247], v[8:11]\n"
"v_exp_f32 v129, v129\n"
"v_mfma_f32_16x16x32_bf16 v[8:11], v[156:159], a[248:251], v[8:11]\n"
"ds_read_b128 v[220:223], v229 offset:21504\n"
"v_cvt_pk_bf16_f32 v104, v128, v129\n"
"v_mfma_f32_16x16x32_bf16 v[40:43], v[160:163], a[244:247], v[40:43]\n"
"v_exp_f32 v130, v130\n"
"v_mfma_f32_16x16x32_bf16 v[40:43], v[164:167], a[248:251], v[40:43]\n"
"v_exp_f32 v131, v131\n"
"v_mfma_f32_16x16x32_bf16 v[72:75], v[168:171], a[244:247], v[72:75]\n"
"ds_read_b128 v[208:211], v228 offset:20480\n"
"v_cvt_pk_bf16_f32 v105, v130, v131\n"
"v_mfma_f32_16x16x32_bf16 v[72:75], v[172:175], a[248:251], v[72:75]\n"
"v_exp_f32 v132, v132\n"
"v_mfma_f32_16x16x32_bf16 v[76:79], v[172:175], v[224:227], v[76:79]\n"
"v_exp_f32 v133, v133\n"
"v_mfma_f32_16x16x32_bf16 v[76:79], v[168:171], a[252:255], v[76:79]\n"
"ds_read_b128 v[212:215], v229 offset:20480\n"
"ds_read_b64_tr_b16 a[244:245], v234 offset:1024\n"
"v_cvt_pk_bf16_f32 v112, v132, v133\n"
"v_mfma_f32_16x16x32_bf16 v[44:47], v[164:167], v[224:227], v[44:47]\n"
"s_add_u32 s40, s56, s53\n"
"buffer_load_dwordx4 v240, s[20:23], s40 offen offset:2048 nt lds\n"
"v_mfma_f32_16x16x32_bf16 v[44:47], v[160:163], a[252:255], v[44:47]\n"
"v_exp_f32 v134, v134\n"
"v_mfma_f32_16x16x32_bf16 v[12:15], v[156:159], v[224:227], v[12:15]\n"
"v_exp_f32 v135, v135\n"
"v_mfma_f32_16x16x32_bf16 v[12:15], v[152:155], a[252:255], v[12:15]\n"
"ds_read_b64_tr_b16 a[246:247], v234 offset:5120\n"
"v_cvt_pk_bf16_f32 v113, v134, v135\n"
"v_mfma_f32_16x16x32_bf16 a[220:223], v[176:179], a[252:255], a[220:223]\n"
"v_exp_f32 v136, v136\n"
"v_smfmac_f32_16x16x64_bf16 v[196:199], a[240:243], v[152:159], v241\n"
"v_exp_f32 v137, v137\n"
"v_mfma_f32_16x16x32_bf16 v[202:205], a[240:243], v[176:179], v[202:205]\n"
"s_waitcnt lgkmcnt(1)\n"
"ds_read_b64_tr_b16 a[248:249], v238 offset:1024\n"
"v_cvt_pk_bf16_f32 v120, v136, v137\n"
"v_mfma_f32_16x16x32_bf16 v[140:143], v[216:219], a[8:11], v[180:183]\n"
"v_exp_f32 v138, v138\n"
"v_mfma_f32_16x16x32_bf16 v[140:143], v[220:223], a[12:15], v[140:143]\n"
"v_exp_f32 v139, v139\n"
"v_mfma_f32_16x16x32_bf16 v[140:143], v[208:211], a[0:3], v[140:143]\n"
"ds_read_b64_tr_b16 a[250:251], v238 offset:5120\n"
"v_cvt_pk_bf16_f32 v121, v138, v139\n"
"v_mfma_f32_16x16x32_bf16 v[140:143], v[212:215], a[4:7], v[140:143]\n"
"ds_read_b64_tr_b16 v[224:225], v239 offset:1024\n"
"v_mfma_f32_16x16x32_bf16 v[144:147], v[208:211], a[16:19], v[184:187]\n"
"ds_read_b64_tr_b16 v[226:227], v239 offset:5120\n"
"v_mfma_f32_16x16x32_bf16 v[144:147], v[212:215], a[20:23], v[144:147]\n"
"ds_read_b64_tr_b16 a[252:253], v235 offset:1024\n"
"v_mfma_f32_16x16x32_bf16 v[144:147], v[216:219], a[24:27], v[144:147]\n"
"s_add_u32 s40, s56, s54\n"
"buffer_load_dwordx4 v240, s[20:23], s40 offen offset:3072 nt lds\n"
"v_mfma_f32_16x16x32_bf16 v[144:147], v[220:223], a[28:31], v[144:147]\n"
"s_add_u32 s55, s55, s59\n"
"ds_read_b64_tr_b16 a[254:255], v235 offset:5120\n"
"v_mfma_f32_16x16x32_bf16 v[148:151], v[216:219], a[40:43], v[188:191]\n"
"s_add_u32 s41, s55, s57\n"
"v_mfma_f32_16x16x32_bf16 v[148:151], v[220:223], a[44:47], v[148:151]\n"
"s_and_b32 s41, s41, s58\n"
"v_exp_f32 v140, v140\n"
"v_mfma_f32_16x16x32_bf16 v[148:151], v[208:211], a[32:35], v[148:151]\n"
"s_andn2_b32 s56, s55, s58\n"
"v_exp_f32 v141, v141\n"
"v_mfma_f32_16x16x32_bf16 v[148:151], v[212:215], a[36:39], v[148:151]\n"
"s_or_b32 s56, s56, s41\n"
"v_exp_f32 v142, v142\n"
"v_mfma_f32_16x16x32_bf16 v[124:127], v[208:211], a[192:195], v[192:195]\n"
"ds_read_b128 v[208:211], v230 offset:16384\n"
"v_cvt_pk_bf16_f32 v98, v140, v141\n"
"v_mfma_f32_16x16x32_bf16 v[124:127], v[212:215], a[196:199], v[124:127]\n"
"v_exp_f32 v143, v143\n"
"v_mfma_f32_16x16x32_bf16 v[124:127], v[216:219], a[200:203], v[124:127]\n"
"ds_read_b128 v[212:215], v231 offset:16384\n"
"v_cvt_pk_bf16_f32 v99, v142, v143\n"
"v_mfma_f32_16x16x32_bf16 v[124:127], v[220:223], a[204:207], v[124:127]\n"
"s_waitcnt lgkmcnt(2)\n"
"v_exp_f32 v144, v144\n"
"v_mfma_f32_16x16x32_bf16 a[232:235], v[176:179], a[244:247], a[232:235]\n"
"v_exp_f32 v145, v145\n"
"v_mfma_f32_16x16x32_bf16 v[24:27], v[152:155], a[244:247], v[24:27]\n"
"s_add_u32 m0, s44, 49152\n"
"ds_read_b128 v[216:219], v230 offset:17408\n"
"v_cvt_pk_bf16_f32 v106, v144, v145\n"
"v_mfma_f32_16x16x32_bf16 v[24:27], v[156:159], a[248:251], v[24:27]\n"
"buffer_load_dwordx4 v240, s[16:19], s56 offen nt lds\n"
"v_mfma_f32_16x16x32_bf16 v[56:59], v[160:163], a[244:247], v[56:59]\n"
"v_exp_f32 v146, v146\n"
"v_mfma_f32_16x16x32_bf16 v[56:59], v[164:167], a[248:251], v[56:59]\n"
"v_exp_f32 v147, v147\n"
"v_mfma_f32_16x16x32_bf16 v[88:91], v[168:171], a[244:247], v[88:91]\n"
"ds_read_b128 v[220:223], v231 offset:17408\n"
"v_cvt_pk_bf16_f32 v107, v146, v147\n"
"v_mfma_f32_16x16x32_bf16 v[88:91], v[172:175], a[248:251], v[88:91]\n"
"v_exp_f32 v148, v148\n"
"v_mfma_f32_16x16x32_bf16 v[92:95], v[172:175], v[224:227], v[92:95]\n"
"v_exp_f32 v149, v149\n"
"v_mfma_f32_16x16x32_bf16 v[92:95], v[168:171], a[252:255], v[92:95]\n"
"ds_read_b64_tr_b16 a[244:245], v232 offset:0\n"
"v_cvt_pk_bf16_f32 v114, v148, v149\n"
"v_mfma_f32_16x16x32_bf16 v[60:63], v[164:167], v[224:227], v[60:63]\n"
"v_exp_f32 v150, v150\n"
"v_mfma_f32_16x16x32_bf16 v[60:63], v[160:163], a[252:255], v[60:63]\n"
"v_exp_f32 v151, v151\n"
"v_mfma_f32_16x16x32_bf16 v[28:31], v[156:159], v[224:227], v[28:31]\n"
"ds_read_b64_tr_b16 a[246:247], v232 offset:4096\n"
"v_cvt_pk_bf16_f32 v115, v150, v151\n"
"v_mfma_f32_16x16x32_bf16 v[28:31], v[152:155], a[252:255], v[28:31]\n"
"v_exp_f32 v124, v124\n"
"v_mfma_f32_16x16x32_bf16 a[236:239], v[176:179], a[252:255], a[236:239]\n"
"v_exp_f32 v125, v125\n"
"v_smfmac_f32_16x16x64_bf16 v[198:201], a[240:243], v[160:167], v241\n"
"s_waitcnt lgkmcnt(1)\n"
"s_add_u32 s40, s56, s52\n"
"buffer_load_dwordx4 v240, s[16:19], s40 offen offset:1024 nt lds\n"
"v_mfma_f32_16x16x32_bf16 v[128:131], v[208:211], a[0:3], v[180:183]\n"
"ds_read_b64_tr_b16 a[248:249], v236 offset:0\n"
"v_cvt_pk_bf16_f32 v122, v124, v125\n"
"v_mfma_f32_16x16x32_bf16 v[128:131], v[212:215], a[4:7], v[128:131]\n"
"v_exp_f32 v126, v126\n"
"v_mfma_f32_16x16x32_bf16 v[128:131], v[216:219], a[8:11], v[128:131]\n"
"v_exp_f32 v127, v127\n"
"v_mfma_f32_16x16x32_bf16 v[128:131], v[220:223], a[12:15], v[128:131]\n"
"ds_read_b64_tr_b16 a[250:251], v236 offset:4096\n"
"v_cvt_pk_bf16_f32 v123, v126, v127\n"
"v_mfma_f32_16x16x32_bf16 v[132:135], v[216:219], a[24:27], v[184:187]\n"
"ds_read_b64_tr_b16 v[224:225], v237 offset:0\n"
"v_mfma_f32_16x16x32_bf16 v[132:135], v[220:223], a[28:31], v[132:135]\n"
"ds_read_b64_tr_b16 v[226:227], v237 offset:4096\n"
"v_mfma_f32_16x16x32_bf16 v[132:135], v[208:211], a[16:19], v[132:135]\n"
"ds_read_b64_tr_b16 a[252:253], v233 offset:0\n"
"v_mfma_f32_16x16x32_bf16 v[132:135], v[212:215], a[20:23], v[132:135]\n"
"ds_read_b64_tr_b16 a[254:255], v233 offset:4096\n"
"v_mfma_f32_16x16x32_bf16 v[136:139], v[208:211], a[32:35], v[188:191]\n"
"ds_read_b128 v[208:211], v230 offset:20480\n"
"v_mfma_f32_16x16x32_bf16 v[136:139], v[212:215], a[36:39], v[136:139]\n"
"v_exp_f32 v128, v128\n"
"v_mfma_f32_16x16x32_bf16 v[136:139], v[216:219], a[40:43], v[136:139]\n"
"v_exp_f32 v129, v129\n"
"v_mfma_f32_16x16x32_bf16 v[136:139], v[220:223], a[44:47], v[136:139]\n"
"s_waitcnt lgkmcnt(2)\n"
"ds_read_b128 v[212:215], v231 offset:20480\n"
"v_cvt_pk_bf16_f32 v100, v128, v129\n"
"v_mfma_f32_16x16x32_bf16 a[208:211], v[176:179], a[244:247], a[208:211]\n"
"s_add_u32 s40, s56, s53\n"
"buffer_load_dwordx4 v240, s[16:19], s40 offen offset:2048 nt lds\n"
"v_mfma_f32_16x16x32_bf16 v[0:3], v[152:155], a[244:247], v[0:3]\n"
"v_exp_f32 v130, v130\n"
"v_mfma_f32_16x16x32_bf16 v[0:3], v[156:159], a[248:251], v[0:3]\n"
"v_exp_f32 v131, v131\n"
"v_mfma_f32_16x16x32_bf16 v[32:35], v[160:163], a[244:247], v[32:35]\n"
"ds_read_b128 v[216:219], v230 offset:21504\n"
"v_cvt_pk_bf16_f32 v101, v130, v131\n"
"v_mfma_f32_16x16x32_bf16 v[32:35], v[164:167], a[248:251], v[32:35]\n"
"v_exp_f32 v132, v132\n"
"v_mfma_f32_16x16x32_bf16 v[64:67], v[168:171], a[244:247], v[64:67]\n"
"v_exp_f32 v133, v133\n"
"v_mfma_f32_16x16x32_bf16 v[64:67], v[172:175], a[248:251], v[64:67]\n"
"ds_read_b128 v[220:223], v231 offset:21504\n"
"v_cvt_pk_bf16_f32 v108, v132, v133\n"
"v_mfma_f32_16x16x32_bf16 v[68:71], v[172:175], v[224:227], v[68:71]\n"
"s_waitcnt lgkmcnt(2)\n"
"v_exp_f32 v134, v134\n"
"v_mfma_f32_16x16x32_bf16 v[68:71], v[168:171], a[252:255], v[68:71]\n"
"v_exp_f32 v135, v135\n"
"v_mfma_f32_16x16x32_bf16 v[36:39], v[164:167], v[224:227], v[36:39]\n"
"ds_read_b64_tr_b16 a[244:245], v232 offset:1024\n"
"v_cvt_pk_bf16_f32 v109, v134, v135\n"
"v_mfma_f32_16x16x32_bf16 v[36:39], v[160:163], a[252:255], v[36:39]\n"
"v_exp_f32 v136, v136\n"
"v_mfma_f32_16x16x32_bf16 v[4:7], v[156:159], v[224:227], v[4:7]\n"
"v_exp_f32 v137, v137\n"
"v_mfma_f32_16x16x32_bf16 v[4:7], v[152:155], a[252:255], v[4:7]\n"
"s_add_u32 s40, s56, s54\n"
"buffer_load_dwordx4 v240, s[16:19], s40 offen offset:3072 nt lds\n"
"v_mfma_f32_16x16x32_bf16 a[212:215], v[176:179], a[252:255], a[212:215]\n"
"ds_read_b64_tr_b16 a[246:247], v232 offset:5120\n"
"v_cvt_pk_bf16_f32 v116, v136, v137\n"
"v_smfmac_f32_16x16x64_bf16 v[200:203], a[240:243], v[168:175], v241\n"
"v_exp_f32 v138, v138\n"
"v_mfma_f32_16x16x32_bf16 v[140:143], v[208:211], a[0:3], v[180:183]\n"
"v_exp_f32 v139, v139\n"
"v_mfma_f32_16x16x32_bf16 v[140:143], v[212:215], a[4:7], v[140:143]\n"
"s_waitcnt lgkmcnt(1)\n"
"ds_read_b64_tr_b16 a[248:249], v236 offset:1024\n"
"v_cvt_pk_bf16_f32 v117, v138, v139\n"
"v_mfma_f32_16x16x32_bf16 v[140:143], v[216:219], a[8:11], v[140:143]\n"
"ds_read_b64_tr_b16 a[250:251], v236 offset:5120\n"
"v_mfma_f32_16x16x32_bf16 v[140:143], v[220:223], a[12:15], v[140:143]\n"
"ds_read_b64_tr_b16 v[224:225], v237 offset:1024\n"
"v_mfma_f32_16x16x32_bf16 v[144:147], v[216:219], a[24:27], v[184:187]\n"
"ds_read_b64_tr_b16 v[226:227], v237 offset:5120\n"
"v_mfma_f32_16x16x32_bf16 v[144:147], v[220:223], a[28:31], v[144:147]\n"
"ds_read_b64_tr_b16 a[252:253], v233 offset:1024\n"
"v_mfma_f32_16x16x32_bf16 v[144:147], v[208:211], a[16:19], v[144:147]\n"
"ds_read_b64_tr_b16 a[254:255], v233 offset:5120\n"
"v_mfma_f32_16x16x32_bf16 v[144:147], v[212:215], a[20:23], v[144:147]\n"
"v_mfma_f32_16x16x32_bf16 v[148:151], v[208:211], a[32:35], v[188:191]\n"
"v_mfma_f32_16x16x32_bf16 v[148:151], v[212:215], a[36:39], v[148:151]\n"
"s_cbranch_vccnz LPCL1_%=\nLPKL1_%=:\n"
"v_mfma_f32_16x16x32_bf16 v[148:151], v[216:219], a[40:43], v[148:151]\n"
"v_exp_f32 v140, v140\n"
"v_mfma_f32_16x16x32_bf16 v[148:151], v[220:223], a[44:47], v[148:151]\n"
"v_exp_f32 v141, v141\n"
"s_waitcnt lgkmcnt(0)\n"
"v_mfma_f32_16x16x32_bf16 a[224:227], v[176:179], a[244:247], a[224:227]\n"
"s_waitcnt vmcnt(8)\n"
"v_exp_f32 v142, v142\n"
"v_mfma_f32_16x16x32_bf16 v[16:19], v[152:155], a[244:247], v[16:19]\n"
"s_barrier\n"
"ds_read_b128 v[208:211], v228 offset:32768\n"
"v_mfma_f32_16x16x32_bf16 v[16:19], v[156:159], a[248:251], v[16:19]\n"
"ds_read_b128 v[212:215], v229 offset:32768\n"
"v_cvt_pk_bf16_f32 v102, v140, v141\n"
"v_mfma_f32_16x16x32_bf16 v[48:51], v[160:163], a[244:247], v[48:51]\n"
"v_exp_f32 v143, v143\n"
"v_mfma_f32_16x16x32_bf16 v[48:51], v[164:167], a[248:251], v[48:51]\n"
"v_cvt_pk_bf16_f32 v103, v142, v143\n"
"v_mfma_f32_16x16x32_bf16 v[80:83], v[168:171], a[244:247], v[80:83]\n"
"ds_read_b128 v[216:219], v228 offset:33792\n"
"v_exp_f32 v144, v144\n"
"v_mfma_f32_16x16x32_bf16 v[80:83], v[172:175], a[248:251], v[80:83]\n"
"v_exp_f32 v145, v145\n"
"v_mfma_f32_16x16x32_bf16 v[84:87], v[172:175], v[224:227], v[84:87]\n"
"v_cvt_pk_bf16_f32 v110, v144, v145\n"
"v_mfma_f32_16x16x32_bf16 v[84:87], v[168:171], a[252:255], v[84:87]\n"
"ds_read_b128 v[220:223], v229 offset:33792\n"
"v_exp_f32 v146, v146\n"
"v_mfma_f32_16x16x32_bf16 v[52:55], v[164:167], v[224:227], v[52:55]\n"
"v_exp_f32 v147, v147\n"
"v_mfma_f32_16x16x32_bf16 v[52:55], v[160:163], a[252:255], v[52:55]\n"
"ds_read_b64_tr_b16 a[244:245], v234 offset:16384\n"
"v_cvt_pk_bf16_f32 v111, v146, v147\n"
"v_mfma_f32_16x16x32_bf16 v[20:23], v[156:159], v[224:227], v[20:23]\n"
"v_exp_f32 v148, v148\n"
"v_mfma_f32_16x16x32_bf16 v[20:23], v[152:155], a[252:255], v[20:23]\n"
"v_exp_f32 v149, v149\n"
"v_mfma_f32_16x16x32_bf16 a[228:231], v[176:179], a[252:255], a[228:231]\n"
"ds_read_b64_tr_b16 a[246:247], v234 offset:20480\n"
"v_cvt_pk_bf16_f32 v118, v148, v149\n"
"LAY2_%=:\n"
"s_waitcnt lgkmcnt(2)\n"
"v_mfma_f32_16x16x32_bf16 v[124:127], v[208:211], a[0:3], v[180:183]\n"
"s_waitcnt lgkmcnt(4)\n"
"s_add_u32 m0, s44, 114688\n"
"v_exp_f32 v150, v150\n"
"v_mfma_f32_16x16x32_bf16 v[124:127], v[212:215], a[4:7], v[124:127]\n"
"s_waitcnt lgkmcnt(3)\n"
"buffer_load_dwordx4 v240, s[20:23], s56 offen nt lds\n"
"v_mfma_f32_16x16x32_bf16 v[124:127], v[216:219], a[8:11], v[124:127]\n"
"s_waitcnt lgkmcnt(2)\n"
"v_exp_f32 v151, v151\n"
"v_mfma_f32_16x16x32_bf16 v[124:127], v[220:223], a[12:15], v[124:127]\n"
"ds_read_b64_tr_b16 a[248:249], v238 offset:16384\n"
"v_cvt_pk_bf16_f32 v119, v150, v151\n"
"v_mfma_f32_16x16x32_bf16 v[128:131], v[216:219], a[24:27], v[184:187]\n"
"ds_read_b64_tr_b16 a[250:251], v238 offset:20480\n"
"v_mfma_f32_16x16x32_bf16 v[128:131], v[220:223], a[28:31], v[128:131]\n"
"ds_read_b64_tr_b16 v[224:225], v239 offset:16384\n"
"v_mfma_f32_16x16x32_bf16 v[128:131], v[208:211], a[16:19], v[128:131]\n"
"ds_read_b64_tr_b16 v[226:227], v239 offset:20480\n"
"v_mfma_f32_16x16x32_bf16 v[128:131], v[212:215], a[20:23], v[128:131]\n"
"ds_read_b64_tr_b16 a[252:253], v235 offset:16384\n"
"v_mfma_f32_16x16x32_bf16 v[132:135], v[208:211], a[32:35], v[188:191]\n"
"ds_read_b64_tr_b16 a[254:255], v235 offset:20480\n"
"v_mfma_f32_16x16x32_bf16 v[132:135], v[212:215], a[36:39], v[132:135]\n"
"v_exp_f32 v124, v124\n"
"v_mfma_f32_16x16x32_bf16 v[132:135], v[216:219], a[40:43], v[132:135]\n"
"v_exp_f32 v125, v125\n"
"v_mfma_f32_16x16x32_bf16 v[132:135], v[220:223], a[44:47], v[132:135]\n"
"v_cvt_pk_bf16_f32 v152, v124, v125\n"
"v_mfma_f32_16x16x32_bf16 v[136:139], v[216:219], a[200:203], v[192:195]\n"
"v_exp_f32 v126, v126\n"
"v_mfma_f32_16x16x32_bf16 v[136:139], v[220:223], a[204:207], v[136:139]\n"
"s_add_u32 s40, s56, s52\n"
"buffer_load_dwordx4 v240, s[20:23], s40 offen offset:1024 nt lds\n"
"v_mfma_f32_16x16x32_bf16 v[136:139], v[208:211], a[192:195], v[136:139]\n"
"v_exp_f32 v127, v127\n"
"v_mfma_f32_16x16x32_bf16 v[136:139], v[212:215], a[196:199], v[136:139]\n"
"s_waitcnt lgkmcnt(0)\n"
"ds_read_b128 v[216:219], v228 offset:37888\n"
"v_cvt_pk_bf16_f32 v153, v126, v127\n"
"v_mfma_f32_16x16x32_bf16 a[216:219], v[120:123], a[244:247], a[216:219]\n"
"v_exp_f32 v128, v128\n"
"v_mfma_f32_16x16x32_bf16 v[8:11], v[96:99], a[244:247], v[8:11]\n"
"v_exp_f32 v129, v129\n"
"v_mfma_f32_16x16x32_bf16 v[8:11], v[100:103], a[248:251], v[8:11]\n"
"ds_read_b128 v[220:223], v229 offset:37888\n"
"v_cvt_pk_bf16_f32 v160, v128, v129\n"
"v_mfma_f32_16x16x32_bf16 v[40:43], v[104:107], a[244:247], v[40:43]\n"
"v_exp_f32 v130, v130\n"
"v_mfma_f32_16x16x32_bf16 v[40:43], v[108:111], a[248:251], v[40:43]\n"
"v_exp_f32 v131, v131\n"
"v_mfma_f32_16x16x32_bf16 v[72:75], v[112:115], a[244:247], v[72:75]\n"
"ds_read_b128 v[208:211], v228 offset:36864\n"
"v_cvt_pk_bf16_f32 v161, v130, v131\n"
"v_mfma_f32_16x16x32_bf16 v[72:75], v[116:119], a[248:251], v[72:75]\n"
"v_exp_f32 v132, v132\n"
"v_mfma_f32_16x16x32_bf16 v[76:79], v[116:119], v[224:227], v[76:79]\n"
"v_exp_f32 v133, v133\n"
"v_mfma_f32_16x16x32_bf16 v[76:79], v[112:115], a[252:255], v[76:79]\n"
"ds_read_b128 v[212:215], v229 offset:36864\n"
"ds_read_b64_tr_b16 a[244:245], v234 offset:17408\n"
"v_cvt_pk_bf16_f32 v168, v132, v133\n"
"v_mfma_f32_16x16x32_bf16 v[44:47], v[108:111], v[224:227], v[44:47]\n"
"s_add_u32 s40, s56, s53\n"
"buffer_load_dwordx4 v240, s[20:23], s40 offen offset:2048 nt lds\n"
"v_mfma_f32_16x16x32_bf16 v[44:47], v[104:107], a[252:255], v[44:47]\n"
"v_exp_f32 v134, v134\n"
"v_mfma_f32_16x16x32_bf16 v[12:15], v[100:103], v[224:227], v[12:15]\n"
"v_exp_f32 v135, v135\n"
"v_mfma_f32_16x16x32_bf16 v[12:15], v[96:99], a[252:255], v[12:15]\n"
"ds_read_b64_tr_b16 a[246:247], v234 offset:21504\n"
"v_cvt_pk_bf16_f32 v169, v134, v135\n"
"v_mfma_f32_16x16x32_bf16 a[220:223], v[120:123], a[252:255], a[220:223]\n"
"v_exp_f32 v136, v136\n"
"v_smfmac_f32_16x16x64_bf16 v[196:199], a[240:243], v[96:103], v241\n"
"v_exp_f32 v137, v137\n"
"v_mfma_f32_16x16x32_bf16 v[202:205], a[240:243], v[120:123], v[202:205]\n"
"s_waitcnt lgkmcnt(1)\n"
"ds_read_b64_tr_b16 a[248:249], v238 offset:17408\n"
"v_cvt_pk_bf16_f32 v176, v136, v137\n"
"v_mfma_f32_16x16x32_bf16 v[140:143], v[216:219], a[8:11], v[180:183]\n"
"v_exp_f32 v138, v138\n"
"v_mfma_f32_16x16x32_bf16 v[140:143], v[220:223], a[12:15], v[140:143]\n"
"v_exp_f32 v139, v139\n"
"v_mfma_f32_16x16x32_bf16 v[140:143], v[208:211], a[0:3], v[140:143]\n"
"ds_read_b64_tr_b16 a[250:251], v238 offset:21504\n"
"v_cvt_pk_bf16_f32 v177, v138, v139\n"
"v_mfma_f32_16x16x32_bf16 v[140:143], v[212:215], a[4:7], v[140:143]\n"
"ds_read_b64_tr_b16 v[224:225], v239 offset:17408\n"
"v_mfma_f32_16x16x32_bf16 v[144:147], v[208:211], a[16:19], v[184:187]\n"
"ds_read_b64_tr_b16 v[226:227], v239 offset:21504\n"
"v_mfma_f32_16x16x32_bf16 v[144:147], v[212:215], a[20:23], v[144:147]\n"
"ds_read_b64_tr_b16 a[252:253], v235 offset:17408\n"
"v_mfma_f32_16x16x32_bf16 v[144:147], v[216:219], a[24:27], v[144:147]\n"
"s_add_u32 s40, s56, s54\n"
"buffer_load_dwordx4 v240, s[20:23], s40 offen offset:3072 nt lds\n"
"v_mfma_f32_16x16x32_bf16 v[144:147], v[220:223], a[28:31], v[144:147]\n"
"s_add_u32 s55, s55, s59\n"
"ds_read_b64_tr_b16 a[254:255], v235 offset:21504\n"
"v_mfma_f32_16x16x32_bf16 v[148:151], v[216:219], a[40:43], v[188:191]\n"
"s_add_u32 s41, s55, s57\n"
"v_mfma_f32_16x16x32_bf16 v[148:151], v[220:223], a[44:47], v[148:151]\n"
"s_and_b32 s41, s41, s58\n"
"v_exp_f32 v140, v140\n"
"v_mfma_f32_16x16x32_bf16 v[148:151], v[208:211], a[32:35], v[148:151]\n"
"s_andn2_b32 s56, s55, s58\n"
"v_exp_f32 v141, v141\n"
"v_mfma_f32_16x16x32_bf16 v[148:151], v[212:215], a[36:39], v[148:151]\n"
"s_or_b32 s56, s56, s41\n"
"v_exp_f32 v142, v142\n"
"v_mfma_f32_16x16x32_bf16 v[124:127], v[208:211], a[192:195], v[192:195]\n"
"s_cmp_ge_u32 s47, 15\ns_cbranch_scc1 LSW_%=\nLSWK_%=:\n"
"ds_read_b128 v[208:211], v230 offset:32768\n"
"v_cvt_pk_bf16_f32 v154, v140, v141\n"
"v_mfma_f32_16x16x32_bf16 v[124:127], v[212:215], a[196:199], v[124:127]\n"
"v_exp_f32 v143, v143\n"
"v_mfma_f32_16x16x32_bf16 v[124:127], v[216:219], a[200:203], v[124:127]\n"
"ds_read_b128 v[212:215], v231 offset:32768\n"
"v_cvt_pk_bf16_f32 v155, v142, v143\n"
"v_mfma_f32_16x16x32_bf16 v[124:127], v[220:223], a[204:207], v[124:127]\n"
"s_waitcnt lgkmcnt(2)\n"
"v_exp_f32 v144, v144\n"
"v_mfma_f32_16x16x32_bf16 a[232:235], v[120:123], a[244:247], a[232:235]\n"
"v_exp_f32 v145, v145\n"
"v_mfma_f32_16x16x32_bf16 v[24:27], v[96:99], a[244:247], v[24:27]\n"
"s_mov_b32 m0, s44\n"
"ds_read_b128 v[216:219], v230 offset:33792\n"
"v_cvt_pk_bf16_f32 v162, v144, v145\n"
"v_mfma_f32_16x16x32_bf16 v[24:27], v[100:103], a[248:251], v[24:27]\n"
"buffer_load_dwordx4 v240, s[16:19], s56 offen nt lds\n"
"v_mfma_f32_16x16x32_bf16 v[56:59], v[104:107], a[244:247], v[56:59]\n"
"v_exp_f32 v146, v146\n"
"v_mfma_f32_16x16x32_bf16 v[56:59], v[108:111], a[248:251], v[56:59]\n"
"v_exp_f32 v147, v147\n"
"v_mfma_f32_16x16x32_bf16 v[88:91], v[112:115], a[244:247], v[88:91]\n"
"ds_read_b128 v[220:223], v231 offset:33792\n"
"v_cvt_pk_bf16_f32 v163, v146, v147\n"
"v_mfma_f32_16x16x32_bf16 v[88:91], v[116:119], a[248:251], v[88:91]\n"
"v_exp_f32 v148, v148\n"
"v_mfma_f32_16x16x32_bf16 v[92:95], v[116:119], v[224:227], v[92:95]\n"
"v_exp_f32 v149, v149\n"
"v_mfma_f32_16x16x32_bf16 v[92:95], v[112:115], a[252:255], v[92:95]\n"
"ds_read_b64_tr_b16 a[244:245], v232 offset:16384\n"
"v_cvt_pk_bf16_f32 v170, v148, v149\n"
"v_mfma_f32_16x16x32_bf16 v[60:63], v[108:111], v[224:227], v[60:63]\n"
"v_exp_f32 v150, v150\n"
"v_mfma_f32_16x16x32_bf16 v[60:63], v[104:107], a[252:255], v[60:63]\n"
"v_exp_f32 v151, v151\n"
"v_mfma_f32_16x16x32_bf16 v[28:31], v[100:103], v[224:227], v[28:31]\n"
"ds_read_b64_tr_b16 a[246:247], v232 offset:20480\n"
"v_cvt_pk_bf16_f32 v171, v150, v151\n"
"v_mfma_f32_16x16x32_bf16 v[28:31], v[96:99], a[252:255], v[28:31]\n"
"v_exp_f32 v124, v124\n"
"v_mfma_f32_16x16x32_bf16 a[236:239], v[120:123], a[252:255], a[236:239]\n"
"v_exp_f32 v125, v125\n"
"v_smfmac_f32_16x16x64_bf16 v[198:201], a[240:243], v[104:111], v241\n"
"s_waitcnt lgkmcnt(1)\n"
"s_add_u32 s40, s56, s52\n"
"buffer_load_dwordx4 v240, s[16:19], s40 offen offset:1024 nt lds\n"
"v_mfma_f32_16x16x32_bf16 v[128:131], v[208:211], a[0:3], v[180:183]\n"
"ds_read_b64_tr_b16 a[248:249], v236 offset:16384\n"
"v_cvt_pk_bf16_f32 v178, v124, v125\n"
"v_mfma_f32_16x16x32_bf16 v[128:131], v[212:215], a[4:7], v[128:131]\n"
"v_exp_f32 v126, v126\n"
"v_mfma_f32_16x16x32_bf16 v[128:131], v[216:219], a[8:11], v[128:131]\n"
"v_exp_f32 v127, v127\n"
"v_mfma_f32_16x16x32_bf16 v[128:131], v[220:223], a[12:15], v[128:131]\n"
"ds_read_b64_tr_b16 a[250:251], v236 offset:20480\n"
"v_cvt_pk_bf16_f32 v179, v126, v127\n"
"v_mfma_f32_16x16x32_bf16 v[132:135], v[216:219], a[24:27], v[184:187]\n"
"ds_read_b64_tr_b16 v[224:225], v237 offset:16384\n"
"v_mfma_f32_16x16x32_bf16 v[132:135], v[220:223], a[28:31], v[132:135]\n"
"ds_read_b64_tr_b16 v[226:227], v237 offset:20480\n"
"v_mfma_f32_16x16x32_bf16 v[132:135], v[208:211], a[16:19], v[132:135]\n"
"ds_read_b64_tr_b16 a[252:253], v233 offset:16384\n"
"v_mfma_f32_16x16x32_bf16 v[132:135], v[212:215], a[20:23], v[132:135]\n"
"ds_read_b64_tr_b16 a[254:255], v233 offset:20480\n"
"v_mfma_f32_16x16x32_bf16 v[136:139], v[208:211], a[32:35], v[188:191]\n"
"ds_read_b128 v[208:211], v230 offset:36864\n"
"v_mfma_f32_16x16x32_bf16 v[136:139], v[212:215], a[36:39], v[136:139]\n"
"v_exp_f32 v128, v128\n"
"v_mfma_f32_16x16x32_bf16 v[136:139], v[216:219], a[40:43], v[136:139]\n"
"v_exp_f32 v129, v129\n"
"v_mfma_f32_16x16x32_bf16 v[136:139], v[220:223], a[44:47], v[136:139]\n"
"s_waitcnt lgkmcnt(2)\n"
"ds_read_b128 v[212:215], v231 offset:36864\n"
"v_cvt_pk_bf16_f32 v156, v128, v129\n"
"v_mfma_f32_16x16x32_bf16 a[208:211], v[120:123], a[244:247], a[208:211]\n"
"s_add_u32 s40, s56, s53\n"
"buffer_load_dwordx4 v240, s[16:19], s40 offen offset:2048 nt lds\n"
"v_mfma_f32_16x16x32_bf16 v[0:3], v[96:99], a[244:247], v[0:3]\n"
"v_exp_f32 v130, v130\n"
"v_mfma_f32_16x16x32_bf16 v[0:3], v[100:103], a[248:251], v[0:3]\n"
"v_exp_f32 v131, v131\n"
"v_mfma_f32_16x16x32_bf16 v[32:35], v[104:107], a[244:247], v[32:35]\n"
"ds_read_b128 v[216:219], v230 offset:37888\n"
"v_cvt_pk_bf16_f32 v157, v130, v131\n"
"v_mfma_f32_16x16x32_bf16 v[32:35], v[108:111], a[248:251], v[32:35]\n"
"v_exp_f32 v132, v132\n"
"v_mfma_f32_16x16x32_bf16 v[64:67], v[112:115], a[244:247], v[64:67]\n"
"v_exp_f32 v133, v133\n"
"v_mfma_f32_16x16x32_bf16 v[64:67], v[116:119], a[248:251], v[64:67]\n"
"ds_read_b128 v[220:223], v231 offset:37888\n"
"v_cvt_pk_bf16_f32 v164, v132, v133\n"
"v_mfma_f32_16x16x32_bf16 v[68:71], v[116:119], v[224:227], v[68:71]\n"
"s_waitcnt lgkmcnt(2)\n"
"v_exp_f32 v134, v134\n"
"v_mfma_f32_16x16x32_bf16 v[68:71], v[112:115], a[252:255], v[68:71]\n"
"v_exp_f32 v135, v135\n"
"v_mfma_f32_16x16x32_bf16 v[36:39], v[108:111], v[224:227], v[36:39]\n"
"ds_read_b64_tr_b16 a[244:245], v232 offset:17408\n"
"v_cvt_pk_bf16_f32 v165, v134, v135\n"
"v_mfma_f32_16x16x32_bf16 v[36:39], v[104:107], a[252:255], v[36:39]\n"
"v_exp_f32 v136, v136\n"
"v_mfma_f32_16x16x32_bf16 v[4:7], v[100:103], v[224:227], v[4:7]\n"
"v_exp_f32 v137, v137\n"
"v_mfma_f32_16x16x32_bf16 v[4:7], v[96:99], a[252:255], v[4:7]\n"
"s_add_u32 s40, s56, s54\n"
"buffer_load_dwordx4 v240, s[16:19], s40 offen offset:3072 nt lds\n"
"v_mfma_f32_16x16x32_bf16 a[212:215], v[120:123], a[252:255], a[212:215]\n"
"ds_read_b64_tr_b16 a[246:247], v232 offset:21504\n"
"v_cvt_pk_bf16_f32 v172, v136, v137\n"
"v_smfmac_f32_16x16x64_bf16 v[200:203], a[240:243], v[112:119], v241\n"
"v_exp_f32 v138, v138\n"
"v_mfma_f32_16x16x32_bf16 v[140:143], v[208:211], a[0:3], v[180:183]\n"
"v_exp_f32 v139, v139\n"
"v_mfma_f32_16x16x32_bf16 v[140:143], v[212:215], a[4:7], v[140:143]\n"
"s_waitcnt lgkmcnt(1)\n"
"ds_read_b64_tr_b16 a[248:249], v236 offset:17408\n"
"v_cvt_pk_bf16_f32 v173, v138, v139\n"
"v_mfma_f32_16x16x32_bf16 v[140:143], v[216:219], a[8:11], v[140:143]\n"
"ds_read_b64_tr_b16 a[250:251], v236 offset:21504\n"
"v_mfma_f32_16x16x32_bf16 v[140:143], v[220:223], a[12:15], v[140:143]\n"
"ds_read_b64_tr_b16 v[224:225], v237 offset:17408\n"
"v_mfma_f32_16x16x32_bf16 v[144:147], v[216:219], a[24:27], v[184:187]\n"
"ds_read_b64_tr_b16 v[226:227], v237 offset:21504\n"
"v_mfma_f32_16x16x32_bf16 v[144:147], v[220:223], a[28:31], v[144:147]\n"
"ds_read_b64_tr_b16 a[252:253], v233 offset:17408\n"
"v_mfma_f32_16x16x32_bf16 v[144:147], v[208:211], a[16:19], v[144:147]\n"
"ds_read_b64_tr_b16 a[254:255], v233 offset:21504\n"
"v_mfma_f32_16x16x32_bf16 v[144:147], v[212:215], a[20:23], v[144:147]\n"
"v_mfma_f32_16x16x32_bf16 v[148:151], v[208:211], a[32:35], v[188:191]\n"
"v_mfma_f32_16x16x32_bf16 v[148:151], v[212:215], a[36:39], v[148:151]\n"
"s_cbranch_vccnz LPCL2_%=\nLPKL2_%=:\n"
"v_mfma_f32_16x16x32_bf16 v[148:151], v[216:219], a[40:43], v[148:151]\n"
"v_exp_f32 v140, v140\n"
"v_mfma_f32_16x16x32_bf16 v[148:151], v[220:223], a[44:47], v[148:151]\n"
"v_exp_f32 v141, v141\n"
"s_waitcnt lgkmcnt(0)\n"
"v_mfma_f32_16x16x32_bf16 a[224:227], v[120:123], a[244:247], a[224:227]\n"
"s_waitcnt vmcnt(8)\n"
"v_exp_f32 v142, v142\n"
"v_mfma_f32_16x16x32_bf16 v[16:19], v[96:99], a[244:247], v[16:19]\n"
"s_barrier\n"
"ds_read_b128 v[208:211], v228 offset:49152\n"
"v_mfma_f32_16x16x32_bf16 v[16:19], v[100:103], a[248:251], v[16:19]\n"
"ds_read_b128 v[212:215], v229 offset:49152\n"
"v_cvt_pk_bf16_f32 v158, v140, v141\n"
"v_mfma_f32_16x16x32_bf16 v[48:51], v[104:107], a[244:247], v[48:51]\n"
"v_exp_f32 v143, v143\n"
"v_mfma_f32_16x16x32_bf16 v[48:51], v[108:111], a[248:251], v[48:51]\n"
"v_cvt_pk_bf16_f32 v159, v142, v143\n"
"v_mfma_f32_16x16x32_bf16 v[80:83], v[112:115], a[244:247], v[80:83]\n"
"ds_read_b128 v[216:219], v228 offset:50176\n"
"v_exp_f32 v144, v144\n"
"v_mfma_f32_16x16x32_bf16 v[80:83], v[116:119], a[248:251], v[80:83]\n"
"v_exp_f32 v145, v145\n"
"v_mfma_f32_16x16x32_bf16 v[84:87], v[116:119], v[224:227], v[84:87]\n"
"v_cvt_pk_bf16_f32 v166, v144, v145\n"
"v_mfma_f32_16x16x32_bf16 v[84:87], v[112:115], a[252:255], v[84:87]\n"
"ds_read_b128 v[220:223], v229 offset:50176\n"
"v_exp_f32 v146, v146\n"
"v_mfma_f32_16x16x32_bf16 v[52:55], v[108:111], v[224:227], v[52:55]\n"
"v_exp_f32 v147, v147\n"
"v_mfma_f32_16x16x32_bf16 v[52:55], v[104:107], a[252:255], v[52:55]\n"
"ds_read_b64_tr_b16 a[244:245], v234 offset:32768\n"
"v_cvt_pk_bf16_f32 v167, v146, v147\n"
"v_mfma_f32_16x16x32_bf16 v[20:23], v[100:103], v[224:227], v[20:23]\n"
"v_exp_f32 v148, v148\n"
"v_mfma_f32_16x16x32_bf16 v[20:23], v[96:99], a[252:255], v[20:23]\n"
"v_exp_f32 v149, v149\n"
"v_mfma_f32_16x16x32_bf16 a[228:231], v[120:123], a[252:255], a[228:231]\n"
"ds_read_b64_tr_b16 a[246:247], v234 offset:36864\n"
"v_cvt_pk_bf16_f32 v174, v148, v149\n"
"s_waitcnt lgkmcnt(2)\n"
"v_mfma_f32_16x16x32_bf16 v[124:127], v[208:211], a[0:3], v[180:183]\n"
"s_add_u32 m0, s44, 65536\n"
"v_exp_f32 v150, v150\n"
"v_mfma_f32_16x16x32_bf16 v[124:127], v[212:215], a[4:7], v[124:127]\n"
"buffer_load_dwordx4 v240, s[20:23], s56 offen nt lds\n"
"v_mfma_f32_16x16x32_bf16 v[124:127], v[216:219], a[8:11], v[124:127]\n"
"v_exp_f32 v151, v151\n"
"v_mfma_f32_16x16x32_bf16 v[124:127], v[220:223], a[12:15], v[124:127]\n"
"ds_read_b64_tr_b16 a[248:249], v238 offset:32768\n"
"v_cvt_pk_bf16_f32 v175, v150, v151\n"
"v_mfma_f32_16x16x32_bf16 v[128:131], v[216:219], a[24:27], v[184:187]\n"
"ds_read_b64_tr_b16 a[250:251], v238 offset:36864\n"
"v_mfma_f32_16x16x32_bf16 v[128:131], v[220:223], a[28:31], v[128:131]\n"
"ds_read_b64_tr_b16 v[224:225], v239 offset:32768\n"
"v_mfma_f32_16x16x32_bf16 v[128:131], v[208:211], a[16:19], v[128:131]\n"
"ds_read_b64_tr_b16 v[226:227], v239 offset:36864\n"
"v_mfma_f32_16x16x32_bf16 v[128:131], v[212:215], a[20:23], v[128:131]\n"
"ds_read_b64_tr_b16 a[252:253], v235 offset:32768\n"
"v_mfma_f32_16x16x32_bf16 v[132:135], v[208:211], a[32:35], v[188:191]\n"
"ds_read_b64_tr_b16 a[254:255], v235 offset:36864\n"
"v_mfma_f32_16x16x32_bf16 v[132:135], v[212:215], a[36:39], v[132:135]\n"
"v_exp_f32 v124, v124\n"
"v_mfma_f32_16x16x32_bf16 v[132:135], v[216:219], a[40:43], v[132:135]\n"
"v_exp_f32 v125, v125\n"
"v_mfma_f32_16x16x32_bf16 v[132:135], v[220:223], a[44:47], v[132:135]\n"
"v_cvt_pk_bf16_f32 v96, v124, v125\n"
"v_mfma_f32_16x16x32_bf16 v[136:139], v[216:219], a[200:203], v[192:195]\n"
"v_exp_f32 v126, v126\n"
"v_mfma_f32_16x16x32_bf16 v[136:139], v[220:223], a[204:207], v[136:139]\n"
"s_add_u32 s40, s56, s52\n"
"buffer_load_dwordx4 v240, s[20:23], s40 offen offset:1024 nt lds\n"
"v_mfma_f32_16x16x32_bf16 v[136:139], v[208:211], a[192:195], v[136:139]\n"
"v_exp_f32 v127, v127\n"
"v_mfma_f32_16x16x32_bf16 v[136:139], v[212:215], a[196:199], v[136:139]\n"
"s_waitcnt lgkmcnt(0)\n"
"ds_read_b128 v[216:219], v228 offset:54272\n"
"v_cvt_pk_bf16_f32 v97, v126, v127\n"
"v_mfma_f32_16x16x32_bf16 a[216:219], v[176:179], a[244:247], a[216:219]\n"
"v_exp_f32 v128, v128\n"
"v_mfma_f32_16x16x32_bf16 v[8:11], v[152:155], a[244:247], v[8:11]\n"
"v_exp_f32 v129, v129\n"
"v_mfma_f32_16x16x32_bf16 v[8:11], v[156:159], a[248:251], v[8:11]\n"
"ds_read_b128 v[220:223], v229 offset:54272\n"
"v_cvt_pk_bf16_f32 v104, v128, v129\n"
"v_mfma_f32_16x16x32_bf16 v[40:43], v[160:163], a[244:247], v[40:43]\n"
"v_exp_f32 v130, v130\n"
"v_mfma_f32_16x16x32_bf16 v[40:43], v[164:167], a[248:251], v[40:43]\n"
"v_exp_f32 v131, v131\n"
"v_mfma_f32_16x16x32_bf16 v[72:75], v[168:171], a[244:247], v[72:75]\n"
"ds_read_b128 v[208:211], v228 offset:53248\n"
"v_cvt_pk_bf16_f32 v105, v130, v131\n"
"v_mfma_f32_16x16x32_bf16 v[72:75], v[172:175], a[248:251], v[72:75]\n"
"v_exp_f32 v132, v132\n"
"v_mfma_f32_16x16x32_bf16 v[76:79], v[172:175], v[224:227], v[76:79]\n"
"v_exp_f32 v133, v133\n"
"v_mfma_f32_16x16x32_bf16 v[76:79], v[168:171], a[252:255], v[76:79]\n"
"ds_read_b128 v[212:215], v229 offset:53248\n"
"ds_read_b64_tr_b16 a[244:245], v234 offset:33792\n"
"v_cvt_pk_bf16_f32 v112, v132, v133\n"
"v_mfma_f32_16x16x32_bf16 v[44:47], v[164:167], v[224:227], v[44:47]\n"
"s_add_u32 s40, s56, s53\n"
"buffer_load_dwordx4 v240, s[20:23], s40 offen offset:2048 nt lds\n"
"v_mfma_f32_16x16x32_bf16 v[44:47], v[160:163], a[252:255], v[44:47]\n"
"v_exp_f32 v134, v134\n"
"v_mfma_f32_16x16x32_bf16 v[12:15], v[156:159], v[224:227], v[12:15]\n"
"v_exp_f32 v135, v135\n"
"v_mfma_f32_16x16x32_bf16 v[12:15], v[152:155], a[252:255], v[12:15]\n"
"ds_read_b64_tr_b16 a[246:247], v234 offset:37888\n"
"v_cvt_pk_bf16_f32 v113, v134, v135\n"
"v_mfma_f32_16x16x32_bf16 a[220:223], v[176:179], a[252:255], a[220:223]\n"
"v_exp_f32 v136, v136\n"
"v_smfmac_f32_16x16x64_bf16 v[196:199], a[240:243], v[152:159], v241\n"
"v_exp_f32 v137, v137\n"
"v_mfma_f32_16x16x32_bf16 v[202:205], a[240:243], v[176:179], v[202:205]\n"
"s_waitcnt lgkmcnt(1)\n"
"ds_read_b64_tr_b16 a[248:249], v238 offset:33792\n"
"v_cvt_pk_bf16_f32 v120, v136, v137\n"
"v_mfma_f32_16x16x32_bf16 v[140:143], v[216:219], a[8:11], v[180:183]\n"
"v_exp_f32 v138, v138\n"
"v_mfma_f32_16x16x32_bf16 v[140:143], v[220:223], a[12:15], v[140:143]\n"
"v_exp_f32 v139, v139\n"
"v_mfma_f32_16x16x32_bf16 v[140:143], v[208:211], a[0:3], v[140:143]\n"
"ds_read_b64_tr_b16 a[250:251], v238 offset:37888\n"
"v_cvt_pk_bf16_f32 v121, v138, v139\n"
"v_mfma_f32_16x16x32_bf16 v[140:143], v[212:215], a[4:7], v[140:143]\n"
"ds_read_b64_tr_b16 v[224:225], v239 offset:33792\n"
"v_mfma_f32_16x16x32_bf16 v[144:147], v[208:211], a[16:19], v[184:187]\n"
"ds_read_b64_tr_b16 v[226:227], v239 offset:37888\n"
"v_mfma_f32_16x16x32_bf16 v[144:147], v[212:215], a[20:23], v[144:147]\n"
"ds_read_b64_tr_b16 a[252:253], v235 offset:33792\n"
"v_mfma_f32_16x16x32_bf16 v[144:147], v[216:219], a[24:27], v[144:147]\n"
"s_add_u32 s40, s56, s54\n"
"buffer_load_dwordx4 v240, s[20:23], s40 offen offset:3072 nt lds\n"
"v_mfma_f32_16x16x32_bf16 v[144:147], v[220:223], a[28:31], v[144:147]\n"
"s_add_u32 s55, s55, s59\n"
"ds_read_b64_tr_b16 a[254:255], v235 offset:37888\n"
"v_mfma_f32_16x16x32_bf16 v[148:151], v[216:219], a[40:43], v[188:191]\n"
"s_add_u32 s41, s55, s57\n"
"v_mfma_f32_16x16x32_bf16 v[148:151], v[220:223], a[44:47], v[148:151]\n"
"s_and_b32 s41, s41, s58\n"
"v_exp_f32 v140, v140\n"
"v_mfma_f32_16x16x32_bf16 v[148:151], v[208:211], a[32:35], v[148:151]\n"
"s_andn2_b32 s56, s55, s58\n"
"v_exp_f32 v141, v141\n"
"v_mfma_f32_16x16x32_bf16 v[148:151], v[212:215], a[36:39], v[148:151]\n"
"s_or_b32 s56, s56, s41\n"
"v_exp_f32 v142, v142\n"
"v_mfma_f32_16x16x32_bf16 v[124:127], v[208:211], a[192:195], v[192:195]\n"
"ds_read_b128 v[208:211], v230 offset:49152\n"
"v_cvt_pk_bf16_f32 v98, v140, v141\n"
"v_mfma_f32_16x16x32_bf16 v[124:127], v[212:215], a[196:199], v[124:127]\n"
"v_exp_f32 v143, v143\n"
"v_mfma_f32_16x16x32_bf16 v[124:127], v[216:219], a[200:203], v[124:127]\n"
"ds_read_b128 v[212:215], v231 offset:49152\n"
"v_cvt_pk_bf16_f32 v99, v142, v143\n"
"v_mfma_f32_16x16x32_bf16 v[124:127], v[220:223], a[204:207], v[124:127]\n"
"s_waitcnt lgkmcnt(2)\n"
"v_exp_f32 v144, v144\n"
"v_mfma_f32_16x16x32_bf16 a[232:235], v[176:179], a[244:247], a[232:235]\n"
"v_exp_f32 v145, v145\n"
"v_mfma_f32_16x16x32_bf16 v[24:27], v[152:155], a[244:247], v[24:27]\n"
"s_add_u32 m0, s44, 16384\n"
"ds_read_b128 v[216:219], v230 offset:50176\n"
"v_cvt_pk_bf16_f32 v106, v144, v145\n"
"v_mfma_f32_16x16x32_bf16 v[24:27], v[156:159], a[248:251], v[24:27]\n"
"buffer_load_dwordx4 v240, s[16:19], s56 offen nt lds\n"
"v_mfma_f32_16x16x32_bf16 v[56:59], v[160:163], a[244:247], v[56:59]\n"
"v_exp_f32 v146, v146\n"
"v_mfma_f32_16x16x32_bf16 v[56:59], v[164:167], a[248:251], v[56:59]\n"
"v_exp_f32 v147, v147\n"
"v_mfma_f32_16x16x32_bf16 v[88:91], v[168:171], a[244:247], v[88:91]\n"
"ds_read_b128 v[220:223], v231 offset:50176\n"
"v_cvt_pk_bf16_f32 v107, v146, v147\n"
"v_mfma_f32_16x16x32_bf16 v[88:91], v[172:175], a[248:251], v[88:91]\n"
"v_exp_f32 v148, v148\n"
"v_mfma_f32_16x16x32_bf16 v[92:95], v[172:175], v[224:227], v[92:95]\n"
"v_exp_f32 v149, v149\n"
"v_mfma_f32_16x16x32_bf16 v[92:95], v[168:171], a[252:255], v[92:95]\n"
"ds_read_b64_tr_b16 a[244:245], v232 offset:32768\n"
"v_cvt_pk_bf16_f32 v114, v148, v149\n"
"v_mfma_f32_16x16x32_bf16 v[60:63], v[164:167], v[224:227], v[60:63]\n"
"v_exp_f32 v150, v150\n"
"v_mfma_f32_16x16x32_bf16 v[60:63], v[160:163], a[252:255], v[60:63]\n"
"v_exp_f32 v151, v151\n"
"v_mfma_f32_16x16x32_bf16 v[28:31], v[156:159], v[224:227], v[28:31]\n"
"ds_read_b64_tr_b16 a[246:247], v232 offset:36864\n"
"v_cvt_pk_bf16_f32 v115, v150, v151\n"
"v_mfma_f32_16x16x32_bf16 v[28:31], v[152:155], a[252:255], v[28:31]\n"
"v_exp_f32 v124, v124\n"
"v_mfma_f32_16x16x32_bf16 a[236:239], v[176:179], a[252:255], a[236:239]\n"
"v_exp_f32 v125, v125\n"
"v_smfmac_f32_16x16x64_bf16 v[198:201], a[240:243], v[160:167], v241\n"
"s_waitcnt lgkmcnt(1)\n"
"s_add_u32 s40, s56, s52\n"
"buffer_load_dwordx4 v240, s[16:19], s40 offen offset:1024 nt lds\n"
"v_mfma_f32_16x16x32_bf16 v[128:131], v[208:211], a[0:3], v[180:183]\n"
"ds_read_b64_tr_b16 a[248:249], v236 offset:32768\n"
"v_cvt_pk_bf16_f32 v122, v124, v125\n"
"v_mfma_f32_16x16x32_bf16 v[128:131], v[212:215], a[4:7], v[128:131]\n"
"v_exp_f32 v126, v126\n"
"v_mfma_f32_16x16x32_bf16 v[128:131], v[216:219], a[8:11], v[128:131]\n"
"v_exp_f32 v127, v127\n"
"v_mfma_f32_16x16x32_bf16 v[128:131], v[220:223], a[12:15], v[128:131]\n"
"ds_read_b64_tr_b16 a[250:251], v236 offset:36864\n"
"v_cvt_pk_bf16_f32 v123, v126, v127\n"
"v_mfma_f32_16x16x32_bf16 v[132:135], v[216:219], a[24:27], v[184:187]\n"
"ds_read_b64_tr_b16 v[224:225], v237 offset:32768\n"
"v_mfma_f32_16x16x32_bf16 v[132:135], v[220:223], a[28:31], v[132:135]\n"
"ds_read_b64_tr_b16 v[226:227], v237 offset:36864\n"
"v_mfma_f32_16x16x32_bf16 v[132:135], v[208:211], a[16:19], v[132:135]\n"
"ds_read_b64_tr_b16 a[252:253], v233 offset:32768\n"
"v_mfma_f32_16x16x32_bf16 v[132:135], v[212:215], a[20:23], v[132:135]\n"
"ds_read_b64_tr_b16 a[254:255], v233 offset:36864\n"
"v_mfma_f32_16x16x32_bf16 v[136:139], v[208:211], a[32:35], v[188:191]\n"
"ds_read_b128 v[208:211], v230 offset:53248\n"
"v_mfma_f32_16x16x32_bf16 v[136:139], v[212:215], a[36:39], v[136:139]\n"
"v_exp_f32 v128, v128\n"
"v_mfma_f32_16x16x32_bf16 v[136:139], v[216:219], a[40:43], v[136:139]\n"
"v_exp_f32 v129, v129\n"
"v_mfma_f32_16x16x32_bf16 v[136:139], v[220:223], a[44:47], v[136:139]\n"
"s_waitcnt lgkmcnt(2)\n"
"ds_read_b128 v[212:215], v231 offset:53248\n"
"v_cvt_pk_bf16_f32 v100, v128, v129\n"
"v_mfma_f32_16x16x32_bf16 a[208:211], v[176:179], a[244:247], a[208:211]\n"
"s_add_u32 s40, s56, s53\n"
"buffer_load_dwordx4 v240, s[16:19], s40 offen offset:2048 nt lds\n"
"v_mfma_f32_16x16x32_bf16 v[0:3], v[152:155], a[244:247], v[0:3]\n"
"v_exp_f32 v130, v130\n"
"v_mfma_f32_16x16x32_bf16 v[0:3], v[156:159], a[248:251], v[0:3]\n"
"v_exp_f32 v131, v131\n"
"v_mfma_f32_16x16x32_bf16 v[32:35], v[160:163], a[244:247], v[32:35]\n"
"ds_read_b128 v[216:219], v230 offset:54272\n"
"v_cvt_pk_bf16_f32 v101, v130, v131\n"
"v_mfma_f32_16x16x32_bf16 v[32:35], v[164:167], a[248:251], v[32:35]\n"
"v_exp_f32 v132, v132\n"
"v_mfma_f32_16x16x32_bf16 v[64:67], v[168:171], a[244:247], v[64:67]\n"
"v_exp_f32 v133, v133\n"
"v_mfma_f32_16x16x32_bf16 v[64:67], v[172:175], a[248:251], v[64:67]\n"
"ds_read_b128 v[220:223], v231 offset:54272\n"
"v_cvt_pk_bf16_f32 v108, v132, v133\n"
"v_mfma_f32_16x16x32_bf16 v[68:71], v[172:175], v[224:227], v[68:71]\n"
"s_waitcnt lgkmcnt(2)\n"
"v_exp_f32 v134, v134\n"
"v_mfma_f32_16x16x32_bf16 v[68:71], v[168:171], a[252:255], v[68:71]\n"
"v_exp_f32 v135, v135\n"
"v_mfma_f32_16x16x32_bf16 v[36:39], v[164:167], v[224:227], v[36:39]\n"
"ds_read_b64_tr_b16 a[244:245], v232 offset:33792\n"
"v_cvt_pk_bf16_f32 v109, v134, v135\n"
"v_mfma_f32_16x16x32_bf16 v[36:39], v[160:163], a[252:255], v[36:39]\n"
"v_exp_f32 v136, v136\n"
"v_mfma_f32_16x16x32_bf16 v[4:7], v[156:159], v[224:227], v[4:7]\n"
"v_exp_f32 v137, v137\n"
"v_mfma_f32_16x16x32_bf16 v[4:7], v[152:155], a[252:255], v[4:7]\n"
"s_add_u32 s40, s56, s54\n"
"buffer_load_dwordx4 v240, s[16:19], s40 offen offset:3072 nt lds\n"
"v_mfma_f32_16x16x32_bf16 a[212:215], v[176:179], a[252:255], a[212:215]\n"
"ds_read_b64_tr_b16 a[246:247], v232 offset:37888\n"
"v_cvt_pk_bf16_f32 v116, v136, v137\n"
"v_smfmac_f32_16x16x64_bf16 v[200:203], a[240:243], v[168:175], v241\n"
"v_exp_f32 v138, v138\n"
"v_mfma_f32_16x16x32_bf16 v[140:143], v[208:211], a[0:3], v[180:183]\n"
"v_exp_f32 v139, v139\n"
"v_mfma_f32_16x16x32_bf16 v[140:143], v[212:215], a[4:7], v[140:143]\n"
"s_waitcnt lgkmcnt(1)\n"
"ds_read_b64_tr_b16 a[248:249], v236 offset:33792\n"
"v_cvt_pk_bf16_f32 v117, v138, v139\n"
"v_mfma_f32_16x16x32_bf16 v[140:143], v[216:219], a[8:11], v[140:143]\n"
"ds_read_b64_tr_b16 a[250:251], v236 offset:37888\n"
"v_mfma_f32_16x16x32_bf16 v[140:143], v[220:223], a[12:15], v[140:143]\n"
"ds_read_b64_tr_b16 v[224:225], v237 offset:33792\n"
"v_mfma_f32_16x16x32_bf16 v[144:147], v[216:219], a[24:27], v[184:187]\n"
"ds_read_b64_tr_b16 v[226:227], v237 offset:37888\n"
"v_mfma_f32_16x16x32_bf16 v[144:147], v[220:223], a[28:31], v[144:147]\n"
"ds_read_b64_tr_b16 a[252:253], v233 offset:33792\n"
"v_mfma_f32_16x16x32_bf16 v[144:147], v[208:211], a[16:19], v[144:147]\n"
"ds_read_b64_tr_b16 a[254:255], v233 offset:37888\n"
"v_mfma_f32_16x16x32_bf16 v[144:147], v[212:215], a[20:23], v[144:147]\n"
"v_mfma_f32_16x16x32_bf16 v[148:151], v[208:211], a[32:35], v[188:191]\n"
"v_mfma_f32_16x16x32_bf16 v[148:151], v[212:215], a[36:39], v[148:151]\n"
"s_cbranch_vccnz LPCL3_%=\nLPKL3_%=:\n"
"v_mfma_f32_16x16x32_bf16 v[148:151], v[216:219], a[40:43], v[148:151]\n"
"s_add_u32 s47, s47, 1\n"
"v_exp_f32 v140, v140\n"
"v_mfma_f32_16x16x32_bf16 v[148:151], v[220:223], a[44:47], v[148:151]\n"
"v_exp_f32 v141, v141\n"
"s_and_b32 s40, s47, 3\ns_cbranch_scc1 LAL_%=\n"
"s_cmp_eq_u32 s47, 8\n"
"s_cbranch_scc1 LTH1_%=\n"
"s_cmp_eq_u32 s47, 12\n"
"s_cbranch_scc1 LTH2_%=\n"
"s_cmp_eq_u32 s47, 16\n"
"s_cbranch_scc1 LAX_%=\n"
"LTH0_%=:\n"
"s_waitcnt lgkmcnt(0)\n"
"v_mfma_f32_16x16x32_bf16 a[224:227], v[176:179], a[244:247], a[224:227]\n"
"s_waitcnt vmcnt(8)\n"
"v_exp_f32 v142, v142\n"
"v_mfma_f32_16x16x32_bf16 v[16:19], v[152:155], a[244:247], v[16:19]\n"
"s_barrier\n"
"ds_read_b128 v[208:211], v228 offset:0\n"
"v_mfma_f32_16x16x32_bf16 v[16:19], v[156:159], a[248:251], v[16:19]\n"
"ds_read_b128 v[212:215], v229 offset:0\n"
"v_cvt_pk_bf16_f32 v102, v140, v141\n"
"v_mfma_f32_16x16x32_bf16 v[48:51], v[160:163], a[244:247], v[48:51]\n"
"ds_read_b128 v[216:219], v228 offset:1024\n"
"v_cvt_f32_u32 v254, s43\n"
"v_mfma_f32_16x16x32_bf16 v[48:51], v[164:167], a[248:251], v[48:51]\n"
"ds_read_b128 v[220:223], v229 offset:1024\n"
"v_and_b32 v255, 0x3f0, v242\n"
"v_mfma_f32_16x16x32_bf16 v[80:83], v[168:171], a[244:247], v[80:83]\n"
"v_exp_f32 v143, v143\n"
"v_mfma_f32_16x16x32_bf16 v[80:83], v[172:175], a[248:251], v[80:83]\n"
"v_exp_f32 v148, v148\n"
"v_mfma_f32_16x16x32_bf16 v[84:87], v[172:175], v[224:227], v[84:87]\n"
"v_exp_f32 v149, v149\n"
"v_mfma_f32_16x16x32_bf16 v[84:87], v[168:171], a[252:255], v[84:87]\n"
"ds_read_b64_tr_b16 a[244:245], v234 offset:49152\n"
"ds_read_b64_tr_b16 a[246:247], v234 offset:53248\n"
"v_mfma_f32_16x16x32_bf16 v[52:55], v[164:167], v[224:227], v[52:55]\n"
"v_exp_f32 v150, v150\n"
"v_mfma_f32_16x16x32_bf16 v[52:55], v[160:163], a[252:255], v[52:55]\n"
"v_exp_f32 v151, v151\n"
"v_mfma_f32_16x16x32_bf16 v[20:23], v[156:159], v[224:227], v[20:23]\n"
"ds_read_b64_tr_b16 a[248:249], v238 offset:49152\n"
"ds_read_b64_tr_b16 a[250:251], v238 offset:53248\n"
"v_mfma_f32_16x16x32_bf16 v[20:23], v[152:155], a[252:255], v[20:23]\n"
"v_cvt_pk_bf16_f32 v103, v142, v143\n"
"v_cvt_pk_bf16_f32 v118, v148, v149\n"
"v_mfma_f32_16x16x32_bf16 a[228:231], v[176:179], a[252:255], a[228:231]\n"
"v_exp_f32 v144, v144\n"
"s_waitcnt lgkmcnt(2)\n"
"v_mfma_f32_16x16x32_bf16 a[216:219], v[120:123], a[244:247], a[216:219]\n"
"s_add_u32 m0, s44, 81920\n"
"v_exp_f32 v145, v145\n"
"v_smfmac_f32_16x16x64_bf16 v[196:199], a[240:243], v[96:103], v241\n"
"buffer_load_dwordx4 v240, s[20:23], s56 offen nt lds\n"
"v_mfma_f32_16x16x32_bf16 v[8:11], v[96:99], a[244:247], v[8:11]\n"
"s_waitcnt lgkmcnt(0)\n"
"v_cvt_pk_bf16_f32 v119, v150, v151\n"
"v_cvt_pk_bf16_f32 v110, v144, v145\n"
"v_mfma_f32_16x16x32_bf16 v[8:11], v[100:103], a[248:251], v[8:11]\n"
"v_exp_f32 v146, v146\n"
"v_exp_f32 v147, v147\n"
"v_smfmac_f32_16x16x64_bf16 v[200:203], a[240:243], v[112:119], v241\n"
"ds_read_b64_tr_b16 v[224:225], v239 offset:49152\n"
"v_cvt_pk_bf16_f32 v111, v146, v147\n"
"v_mfma_f32_16x16x32_bf16 v[40:43], v[104:107], a[244:247], v[40:43]\n"
"ds_read_b64_tr_b16 v[226:227], v239 offset:53248\n"
"v_lshrrev_b32 v255, 1, v255\n"
"v_mfma_f32_16x16x32_bf16 v[40:43], v[108:111], a[248:251], v[40:43]\n"
"ds_read_b64_tr_b16 a[252:253], v235 offset:49152\n"
"ds_read_b64_tr_b16 a[254:255], v235 offset:53248\n"
"v_smfmac_f32_16x16x64_bf16 v[198:201], a[240:243], v[104:111], v241\n"
"v_add_f32 v196, v196, v197\n"
"v_sub_u32 v255, v242, v255\n"
"v_mfma_f32_16x16x32_bf16 v[72:75], v[112:115], a[244:247], v[72:75]\n"
"ds_read_b64_tr_b16 a[244:245], v234 offset:50176\n"
"v_xor_b32 v250, 0x1000, v255\n"
"v_mfma_f32_16x16x32_bf16 v[72:75], v[116:119], a[248:251], v[72:75]\n"
"ds_read_b64_tr_b16 a[246:247], v234 offset:54272\n"
"v_mfma_f32_16x16x32_bf16 v[202:205], a[240:243], v[120:123], v[202:205]\n"
"s_waitcnt lgkmcnt(4)\n"
"ds_read_b64_tr_b16 a[248:249], v238 offset:50176\n"
"v_mfma_f32_16x16x32_bf16 v[76:79], v[116:119], v[224:227], v[76:79]\n"
"s_waitcnt lgkmcnt(3)\n"
"ds_read_b64_tr_b16 a[250:251], v238 offset:54272\n"
"v_mfma_f32_16x16x32_bf16 v[76:79], v[112:115], a[252:255], v[76:79]\n"
"v_mfma_f32_16x16x32_bf16 v[44:47], v[108:111], v[224:227], v[44:47]\n"
"v_add_f32 v198, v198, v199\n"
"v_add_f32 v200, v200, v201\n"
"v_mfma_f32_16x16x32_bf16 v[44:47], v[104:107], a[252:255], v[44:47]\n"
"v_add_f32 v249, v196, v198\n"
"v_add_f32 v249, v249, v200\n"
"v_mfma_f32_16x16x32_bf16 v[12:15], v[100:103], v[224:227], v[12:15]\n"
"s_add_u32 s40, s56, s52\n"
"buffer_load_dwordx4 v240, s[20:23], s40 offen offset:1024 nt lds\n"
"v_mfma_f32_16x16x32_bf16 v[12:15], v[96:99], a[252:255], v[12:15]\n"
"ds_read_b64_tr_b16 v[224:225], v239 offset:50176\n"
"v_add_f32 v254, v254, v202\n"
"v_min3_f32 v254, v254, v196, v198\n"
"v_min_f32 v254, v254, v200\n"
"v_rcp_f32 v254, v254\n"
"v_mfma_f32_16x16x32_bf16 a[220:223], v[120:123], a[252:255], a[220:223]\n"
"ds_read_b64_tr_b16 v[226:227], v239 offset:54272\n"
"v_add_f32 v249, v249, v202\n"
"v_fmac_f32 v249, 0x53800000, v254\n"
"v_cmp_nge_f32 vcc, s46, v249\n"
"s_cbranch_vccnz LSLOW_%=\nLBACK0_%=:\ns_mov_b64 vcc, s[64:65]\n"
"s_waitcnt lgkmcnt(2)\n"
"v_mfma_f32_16x16x32_bf16 a[232:235], v[120:123], a[244:247], a[232:235]\n"
"v_xor_b32 v207, 0x400, v248\n"
"v_bfe_u32 v206, v242, 8, 2\n"
"v_lshl_add_u32 v206, v206, 4, s43\n"
"v_mfma_f32_16x16x32_bf16 v[24:27], v[96:99], a[244:247], v[24:27]\n"
"ds_read_b128 a[196:199], v207 offset:4096\n"
"ds_read_b128 a[204:207], v207 offset:6144\n"
"ds_read_b128 v[152:155], v206 offset:0\n"
"v_xor_b32 v207, 64, v206\n"
"v_mfma_f32_16x16x32_bf16 v[24:27], v[100:103], a[248:251], v[24:27]\n"
"ds_read_b128 v[156:159], v207 offset:0\n"
"v_accvgpr_mov_b32 a0, a48\n"
"v_mfma_f32_16x16x32_bf16 v[56:59], v[104:107], a[244:247], v[56:59]\n"
"ds_read_b128 v[160:163], v206 offset:1024\n"
"v_accvgpr_mov_b32 a1, a49\n"
"v_mfma_f32_16x16x32_bf16 v[56:59], v[108:111], a[248:251], v[56:59]\n"
"ds_read_b128 v[164:167], v207 offset:1024\n"
"v_accvgpr_mov_b32 a2, a50\n"
"v_mfma_f32_16x16x32_bf16 v[88:91], v[112:115], a[244:247], v[88:91]\n"
"v_accvgpr_mov_b32 a3, a51\n"
"v_accvgpr_mov_b32 a4, a52\n"
"v_mfma_f32_16x16x32_bf16 v[88:91], v[116:119], a[248:251], v[88:91]\n"
"s_waitcnt lgkmcnt(3)\n"
"v_accvgpr_mov_b32 a5, a53\n"
"v_accvgpr_mov_b32 a6, a54\n"
"v_accvgpr_mov_b32 a7, a55\n"
"v_mfma_f32_16x16x32_bf16 v[180:183], v[152:155], a[0:3], 0\n"
"s_waitcnt lgkmcnt(2)\n"
"v_accvgpr_mov_b32 a8, a56\n"
"v_accvgpr_mov_b32 a9, a57\n"
"v_accvgpr_mov_b32 a10, a58\n"
"v_accvgpr_mov_b32 a11, a59\n"
"v_mfma_f32_16x16x32_bf16 v[180:183], v[156:159], a[4:7], v[180:183]\n"
"s_waitcnt lgkmcnt(1)\n"
"v_accvgpr_mov_b32 a12, a60\n"
"v_accvgpr_mov_b32 a13, a61\n"
"v_accvgpr_mov_b32 a14, a62\n"
"v_accvgpr_mov_b32 a15, a63\n"
"v_mfma_f32_16x16x32_bf16 v[180:183], v[160:163], a[8:11], v[180:183]\n"
"s_waitcnt lgkmcnt(0)\n"
"v_accvgpr_mov_b32 a16, a64\n"
"v_accvgpr_mov_b32 a17, a65\n"
"v_accvgpr_mov_b32 a18, a66\n"
"v_accvgpr_mov_b32 a19, a67\n"
"v_mfma_f32_16x16x32_bf16 v[180:183], v[164:167], a[12:15], v[180:183]\n"
"v_accvgpr_mov_b32 a20, a68\n"
"v_accvgpr_mov_b32 a21, a69\n"
"v_accvgpr_mov_b32 a22, a70\n"
"v_accvgpr_mov_b32 a23, a71\n"
"v_mfma_f32_16x16x32_bf16 v[184:187], v[152:155], a[16:19], 0\n"
"v_accvgpr_mov_b32 a24, a72\n"
"v_accvgpr_mov_b32 a25, a73\n"
"v_accvgpr_mov_b32 a26, a74\n"
"v_accvgpr_mov_b32 a27, a75\n"
"v_mfma_f32_16x16x32_bf16 v[184:187], v[156:159], a[20:23], v[184:187]\n"
"s_add_u32 s40, s56, s53\n"
"buffer_load_dwordx4 v240, s[20:23], s40 offen offset:2048 nt lds\n"
"v_accvgpr_mov_b32 a28, a76\n"
"v_accvgpr_mov_b32 a29, a77\n"
"v_accvgpr_mov_b32 a30, a78\n"
"v_accvgpr_mov_b32 a31, a79\n"
"v_mfma_f32_16x16x32_bf16 v[184:187], v[160:163], a[24:27], v[184:187]\n"
"ds_read_b128 a[192:195], v248 offset:4096\n"
"v_accvgpr_mov_b32 a32, a80\n"
"v_accvgpr_mov_b32 a33, a81\n"
"v_accvgpr_mov_b32 a34, a82\n"
"v_accvgpr_mov_b32 a35, a83\n"
"v_mfma_f32_16x16x32_bf16 v[184:187], v[164:167], a[28:31], v[184:187]\n"
"v_accvgpr_mov_b32 a36, a84\n"
"v_accvgpr_mov_b32 a37, a85\n"
"v_accvgpr_mov_b32 a38, a86\n"
"v_accvgpr_mov_b32 a39, a87\n"
"v_mfma_f32_16x16x32_bf16 v[188:191], v[152:155], a[32:35], 0\n"
"ds_read_b128 a[200:203], v248 offset:6144\n"
"v_accvgpr_mov_b32 a40, a88\n"
"v_accvgpr_mov_b32 a41, a89\n"
"v_accvgpr_mov_b32 a42, a90\n"
"v_accvgpr_mov_b32 a43, a91\n"
"v_mfma_f32_16x16x32_bf16 v[188:191], v[156:159], a[36:39], v[188:191]\n"
"v_accvgpr_mov_b32 a44, a92\n"
"v_accvgpr_mov_b32 a45, a93\n"
"v_accvgpr_mov_b32 a46, a94\n"
"v_accvgpr_mov_b32 a47, a95\n"
"v_mfma_f32_16x16x32_bf16 v[188:191], v[160:163], a[40:43], v[188:191]\n"
"v_mov_b32 v252, v192\n"
"v_xor_b32 v180, s77, v180\n"
"v_mfma_f32_16x16x32_bf16 v[188:191], v[164:167], a[44:47], v[188:191]\n"
"s_waitcnt lgkmcnt(1)\n"
"v_xor_b32 v181, s77, v181\n"
"v_xor_b32 v182, s77, v182\n"
"v_mfma_f32_16x16x32_bf16 v[192:195], v[152:155], a[192:195], 0\n"
"v_rcp_f32 v243, v196\n"
"v_mfma_f32_16x16x32_bf16 v[192:195], v[156:159], a[196:199], v[192:195]\n"
"s_waitcnt lgkmcnt(0)\n"
"v_xor_b32 v183, s77, v183\n"
"v_xor_b32 v184, s77, v184\n"
"v_mfma_f32_16x16x32_bf16 v[192:195], v[160:163], a[200:203], v[192:195]\n"
"v_xor_b32 v185, s77, v185\n"
"v_xor_b32 v186, s77, v186\n"
"v_mfma_f32_16x16x32_bf16 v[192:195], v[164:167], a[204:207], v[192:195]\n"
"v_rcp_f32 v244, v198\n"
"v_mfma_f32_16x16x32_bf16 v[124:127], v[208:211], a[0:3], v[180:183]\n"
"v_rcp_f32 v245, v200\n"
"v_mfma_f32_16x16x32_bf16 v[124:127], v[212:215], a[4:7], v[124:127]\n"
"ds_read_b64_tr_b16 a[252:253], v235 offset:50176\n"
"v_xor_b32 v187, s77, v187\n"
"v_mfma_f32_16x16x32_bf16 v[124:127], v[216:219], a[8:11], v[124:127]\n"
"ds_read_b64_tr_b16 a[254:255], v235 offset:54272\n"
"v_xor_b32 v188, s77, v188\n"
"v_mfma_f32_16x16x32_bf16 v[124:127], v[220:223], a[12:15], v[124:127]\n"
"s_add_u32 s40, s56, s54\n"
"buffer_load_dwordx4 v240, s[20:23], s40 offen offset:3072 nt lds\n"
"v_mfma_f32_16x16x32_bf16 v[128:131], v[216:219], a[24:27], v[184:187]\n"
"s_add_u32 s55, s55, s59\n"
"ds_read_b64_tr_b16 a[244:245], v232 offset:49152\n"
"v_xor_b32 v189, s77, v189\n"
"v_mfma_f32_16x16x32_bf16 v[128:131], v[220:223], a[28:31], v[128:131]\n"
"s_add_u32 s41, s55, s57\n"
"ds_read_b64_tr_b16 a[246:247], v232 offset:53248\n"
"v_xor_b32 v190, s77, v190\n"
"v_mfma_f32_16x16x32_bf16 v[128:131], v[208:211], a[16:19], v[128:131]\n"
"s_and_b32 s41, s41, s58\n"
"ds_read_b64_tr_b16 a[248:249], v236 offset:49152\n"
"v_xor_b32 v191, s77, v191\n"
"v_mfma_f32_16x16x32_bf16 v[128:131], v[212:215], a[20:23], v[128:131]\n"
"s_andn2_b32 s56, s55, s58\n"
"ds_read_b64_tr_b16 a[250:251], v236 offset:53248\n"
"v_xor_b32 v192, s77, v192\n"
"v_mfma_f32_16x16x32_bf16 v[132:135], v[208:211], a[32:35], v[188:191]\n"
"s_or_b32 s56, s56, s41\n"
"v_xor_b32 v193, s77, v193\n"
"v_xor_b32 v194, s77, v194\n"
"v_mfma_f32_16x16x32_bf16 v[132:135], v[212:215], a[36:39], v[132:135]\n"
"v_xor_b32 v195, s77, v195\n"
"v_mfma_f32_16x16x32_bf16 v[132:135], v[216:219], a[40:43], v[132:135]\n"
"v_mfma_f32_16x16x32_bf16 v[132:135], v[220:223], a[44:47], v[132:135]\n"
"v_mfma_f32_16x16x32_bf16 v[136:139], v[216:219], a[200:203], v[192:195]\n"
"ds_read_b128 v[216:219], v228 offset:5120\n"
"v_mfma_f32_16x16x32_bf16 v[136:139], v[220:223], a[204:207], v[136:139]\n"
"ds_read_b128 v[220:223], v229 offset:5120\n"
"v_mfma_f32_16x16x32_bf16 v[136:139], v[208:211], a[192:195], v[136:139]\n"
"ds_read_b128 v[208:211], v228 offset:4096\n"
"v_mfma_f32_16x16x32_bf16 v[136:139], v[212:215], a[196:199], v[136:139]\n"
"ds_read_b128 v[212:215], v229 offset:4096\n"
"s_branch LTC_%=\n"
"LTH1_%=:\n"
"s_waitcnt lgkmcnt(0)\n"
"v_mfma_f32_16x16x32_bf16 a[224:227], v[176:179], a[244:247], a[224:227]\n"
"s_waitcnt vmcnt(8)\n"
"v_exp_f32 v142, v142\n"
"v_mfma_f32_16x16x32_bf16 v[16:19], v[152:155], a[244:247], v[16:19]\n"
"s_barrier\n"
"ds_read_b128 v[208:211], v228 offset:0\n"
"v_mfma_f32_16x16x32_bf16 v[16:19], v[156:159], a[248:251], v[16:19]\n"
"ds_read_b128 v[212:215], v229 offset:0\n"
"v_cvt_pk_bf16_f32 v102, v140, v141\n"
"v_mfma_f32_16x16x32_bf16 v[48:51], v[160:163], a[244:247], v[48:51]\n"
"ds_read_b128 v[216:219], v228 offset:1024\n"
"v_cvt_f32_u32 v254, s43\n"
"v_mfma_f32_16x16x32_bf16 v[48:51], v[164:167], a[248:251], v[48:51]\n"
"ds_read_b128 v[220:223], v229 offset:1024\n"
"v_and_b32 v255, 0x3f0, v242\n"
"v_mfma_f32_16x16x32_bf16 v[80:83], v[168:171], a[244:247], v[80:83]\n"
"v_exp_f32 v143, v143\n"
"v_mfma_f32_16x16x32_bf16 v[80:83], v[172:175], a[248:251], v[80:83]\n"
"v_exp_f32 v148, v148\n"
"v_mfma_f32_16x16x32_bf16 v[84:87], v[172:175], v[224:227], v[84:87]\n"
"v_exp_f32 v149, v149\n"
"v_mfma_f32_16x16x32_bf16 v[84:87], v[168:171], a[252:255], v[84:87]\n"
"ds_read_b64_tr_b16 a[244:245], v234 offset:49152\n"
"ds_read_b64_tr_b16 a[246:247], v234 offset:53248\n"
"v_mfma_f32_16x16x32_bf16 v[52:55], v[164:167], v[224:227], v[52:55]\n"
"v_exp_f32 v150, v150\n"
"v_mfma_f32_16x16x32_bf16 v[52:55], v[160:163], a[252:255], v[52:55]\n"
"v_exp_f32 v151, v151\n"
"v_mfma_f32_16x16x32_bf16 v[20:23], v[156:159], v[224:227], v[20:23]\n"
"ds_read_b64_tr_b16 a[248:249], v238 offset:49152\n"
"ds_read_b64_tr_b16 a[250:251], v238 offset:53248\n"
"v_mfma_f32_16x16x32_bf16 v[20:23], v[152:155], a[252:255], v[20:23]\n"
"v_cvt_pk_bf16_f32 v103, v142, v143\n"
"v_cvt_pk_bf16_f32 v118, v148, v149\n"
"v_mfma_f32_16x16x32_bf16 a[228:231], v[176:179], a[252:255], a[228:231]\n"
"v_exp_f32 v144, v144\n"
"s_waitcnt lgkmcnt(2)\n"
"v_mfma_f32_16x16x32_bf16 a[216:219], v[120:123], a[244:247], a[216:219]\n"
"s_add_u32 m0, s44, 81920\n"
"v_exp_f32 v145, v145\n"
"v_smfmac_f32_16x16x64_bf16 v[196:199], a[240:243], v[96:103], v241\n"
"buffer_load_dwordx4 v240, s[20:23], s56 offen nt lds\n"
"v_mfma_f32_16x16x32_bf16 v[8:11], v[96:99], a[244:247], v[8:11]\n"
"s_waitcnt lgkmcnt(0)\n"
"v_cvt_pk_bf16_f32 v119, v150, v151\n"
"v_cvt_pk_bf16_f32 v110, v144, v145\n"
"v_mfma_f32_16x16x32_bf16 v[8:11], v[100:103], a[248:251], v[8:11]\n"
"v_exp_f32 v146, v146\n"
"v_exp_f32 v147, v147\n"
"v_smfmac_f32_16x16x64_bf16 v[200:203], a[240:243], v[112:119], v241\n"
"ds_read_b64_tr_b16 v[224:225], v239 offset:49152\n"
"v_cvt_pk_bf16_f32 v111, v146, v147\n"
"v_mfma_f32_16x16x32_bf16 v[40:43], v[104:107], a[244:247], v[40:43]\n"
"ds_read_b64_tr_b16 v[226:227], v239 offset:53248\n"
"v_lshrrev_b32 v255, 1, v255\n"
"v_mfma_f32_16x16x32_bf16 v[40:43], v[108:111], a[248:251], v[40:43]\n"
"ds_read_b64_tr_b16 a[252:253], v235 offset:49152\n"
"ds_read_b64_tr_b16 a[254:255], v235 offset:53248\n"
"v_smfmac_f32_16x16x64_bf16 v[198:201], a[240:243], v[104:111], v241\n"
"v_add_f32 v196, v196, v197\n"
"v_sub_u32 v255, v242, v255\n"
"v_mfma_f32_16x16x32_bf16 v[72:75], v[112:115], a[244:247], v[72:75]\n"
"ds_read_b64_tr_b16 a[244:245], v234 offset:50176\n"
"v_xor_b32 v250, 0x1000, v255\n"
"v_mfma_f32_16x16x32_bf16 v[72:75], v[116:119], a[248:251], v[72:75]\n"
"ds_read_b64_tr_b16 a[246:247], v234 offset:54272\n"
"v_mfma_f32_16x16x32_bf16 v[202:205], a[240:243], v[120:123], v[202:205]\n"
"s_waitcnt lgkmcnt(4)\n"
"ds_read_b64_tr_b16 a[248:249], v238 offset:50176\n"
"v_mfma_f32_16x16x32_bf16 v[76:79], v[116:119], v[224:227], v[76:79]\n"
"s_waitcnt lgkmcnt(3)\n"
"ds_read_b64_tr_b16 a[250:251], v238 offset:54272\n"
"v_mfma_f32_16x16x32_bf16 v[76:79], v[112:115], a[252:255], v[76:79]\n"
"v_mfma_f32_16x16x32_bf16 v[44:47], v[108:111], v[224:227], v[44:47]\n"
"v_add_f32 v198, v198, v199\n"
"v_add_f32 v200, v200, v201\n"
"v_mfma_f32_16x16x32_bf16 v[44:47], v[104:107], a[252:255], v[44:47]\n"
"v_add_f32 v249, v196, v198\n"
"v_add_f32 v249, v249, v200\n"
"v_mfma_f32_16x16x32_bf16 v[12:15], v[100:103], v[224:227], v[12:15]\n"
"s_add_u32 s40, s56, s52\n"
"buffer_load_dwordx4 v240, s[20:23], s40 offen offset:1024 nt lds\n"
"v_mfma_f32_16x16x32_bf16 v[12:15], v[96:99], a[252:255], v[12:15]\n"
"ds_read_b64_tr_b16 v[224:225], v239 offset:50176\n"
"v_add_f32 v254, v254, v202\n"
"v_min3_f32 v254, v254, v196, v198\n"
"v_min_f32 v254, v254, v200\n"
"v_rcp_f32 v254, v254\n"
"v_mfma_f32_16x16x32_bf16 a[220:223], v[120:123], a[252:255], a[220:223]\n"
"ds_read_b64_tr_b16 v[226:227], v239 offset:54272\n"
"v_add_f32 v249, v249, v202\n"
"v_fmac_f32 v249, 0x53800000, v254\n"
"v_cmp_nge_f32 vcc, s46, v249\n"
"s_cbranch_vccnz LSLOW_%=\nLBACK1_%=:\ns_mov_b64 vcc, s[64:65]\n"
"s_waitcnt lgkmcnt(2)\n"
"v_mfma_f32_16x16x32_bf16 a[232:235], v[120:123], a[244:247], a[232:235]\n"
"v_xor_b32 v207, 0x400, v248\n"
"v_bfe_u32 v206, v242, 8, 2\n"
"v_lshl_add_u32 v206, v206, 4, s43\n"
"v_mfma_f32_16x16x32_bf16 v[24:27], v[96:99], a[244:247], v[24:27]\n"
"ds_read_b128 a[196:199], v207 offset:4096\n"
"ds_read_b128 a[204:207], v207 offset:6144\n"
"ds_read_b128 v[152:155], v206 offset:0\n"
"v_xor_b32 v207, 64, v206\n"
"v_mfma_f32_16x16x32_bf16 v[24:27], v[100:103], a[248:251], v[24:27]\n"
"ds_read_b128 v[156:159], v207 offset:0\n"
"v_accvgpr_mov_b32 a0, a96\n"
"v_mfma_f32_16x16x32_bf16 v[56:59], v[104:107], a[244:247], v[56:59]\n"
"ds_read_b128 v[160:163], v206 offset:1024\n"
"v_accvgpr_mov_b32 a1, a97\n"
"v_mfma_f32_16x16x32_bf16 v[56:59], v[108:111], a[248:251], v[56:59]\n"
"ds_read_b128 v[164:167], v207 offset:1024\n"
"v_accvgpr_mov_b32 a2, a98\n"
"v_mfma_f32_16x16x32_bf16 v[88:91], v[112:115], a[244:247], v[88:91]\n"
"v_accvgpr_mov_b32 a3, a99\n"
"v_accvgpr_mov_b32 a4, a100\n"
"v_mfma_f32_16x16x32_bf16 v[88:91], v[116:119], a[248:251], v[88:91]\n"
"s_waitcnt lgkmcnt(3)\n"
"v_accvgpr_mov_b32 a5, a101\n"
"v_accvgpr_mov_b32 a6, a102\n"
"v_accvgpr_mov_b32 a7, a103\n"
"v_mfma_f32_16x16x32_bf16 v[180:183], v[152:155], a[0:3], 0\n"
"s_waitcnt lgkmcnt(2)\n"
"v_accvgpr_mov_b32 a8, a104\n"
"v_accvgpr_mov_b32 a9, a105\n"
"v_accvgpr_mov_b32 a10, a106\n"
"v_accvgpr_mov_b32 a11, a107\n"
"v_mfma_f32_16x16x32_bf16 v[180:183], v[156:159], a[4:7], v[180:183]\n"
"s_waitcnt lgkmcnt(1)\n"
"v_accvgpr_mov_b32 a12, a108\n"
"v_accvgpr_mov_b32 a13, a109\n"
"v_accvgpr_mov_b32 a14, a110\n"
"v_accvgpr_mov_b32 a15, a111\n"
"v_mfma_f32_16x16x32_bf16 v[180:183], v[160:163], a[8:11], v[180:183]\n"
"s_waitcnt lgkmcnt(0)\n"
"v_accvgpr_mov_b32 a16, a112\n"
"v_accvgpr_mov_b32 a17, a113\n"
"v_accvgpr_mov_b32 a18, a114\n"
"v_accvgpr_mov_b32 a19, a115\n"
"v_mfma_f32_16x16x32_bf16 v[180:183], v[164:167], a[12:15], v[180:183]\n"
"v_accvgpr_mov_b32 a20, a116\n"
"v_accvgpr_mov_b32 a21, a117\n"
"v_accvgpr_mov_b32 a22, a118\n"
"v_accvgpr_mov_b32 a23, a119\n"
"v_mfma_f32_16x16x32_bf16 v[184:187], v[152:155], a[16:19], 0\n"
"v_accvgpr_mov_b32 a24, a120\n"
"v_accvgpr_mov_b32 a25, a121\n"
"v_accvgpr_mov_b32 a26, a122\n"
"v_accvgpr_mov_b32 a27, a123\n"
"v_mfma_f32_16x16x32_bf16 v[184:187], v[156:159], a[20:23], v[184:187]\n"
"s_add_u32 s40, s56, s53\n"
"buffer_load_dwordx4 v240, s[20:23], s40 offen offset:2048 nt lds\n"
"v_accvgpr_mov_b32 a28, a124\n"
"v_accvgpr_mov_b32 a29, a125\n"
"v_accvgpr_mov_b32 a30, a126\n"
"v_accvgpr_mov_b32 a31, a127\n"
"v_mfma_f32_16x16x32_bf16 v[184:187], v[160:163], a[24:27], v[184:187]\n"
"ds_read_b128 a[192:195], v248 offset:4096\n"
"v_accvgpr_mov_b32 a32, a128\n"
"v_accvgpr_mov_b32 a33, a129\n"
"v_accvgpr_mov_b32 a34, a130\n"
"v_accvgpr_mov_b32 a35, a131\n"
"v_mfma_f32_16x16x32_bf16 v[184:187], v[164:167], a[28:31], v[184:187]\n"
"v_accvgpr_mov_b32 a36, a132\n"
"v_accvgpr_mov_b32 a37, a133\n"
"v_accvgpr_mov_b32 a38, a134\n"
"v_accvgpr_mov_b32 a39, a135\n"
"v_mfma_f32_16x16x32_bf16 v[188:191], v[152:155], a[32:35], 0\n"
"ds_read_b128 a[200:203], v248 offset:6144\n"
"v_accvgpr_mov_b32 a40, a136\n"
"v_accvgpr_mov_b32 a41, a137\n"
"v_accvgpr_mov_b32 a42, a138\n"
"v_accvgpr_mov_b32 a43, a139\n"
"v_mfma_f32_16x16x32_bf16 v[188:191], v[156:159], a[36:39], v[188:191]\n"
"v_accvgpr_mov_b32 a44, a140\n"
"v_accvgpr_mov_b32 a45, a141\n"
"v_accvgpr_mov_b32 a46, a142\n"
"v_accvgpr_mov_b32 a47, a143\n"
"v_mfma_f32_16x16x32_bf16 v[188:191], v[160:163], a[40:43], v[188:191]\n"
"v_mov_b32 v252, v192\n"
"v_xor_b32 v180, s77, v180\n"
"v_mfma_f32_16x16x32_bf16 v[188:191], v[164:167], a[44:47], v[188:191]\n"
"s_waitcnt lgkmcnt(1)\n"
"v_xor_b32 v181, s77, v181\n"
"v_xor_b32 v182, s77, v182\n"
"v_mfma_f32_16x16x32_bf16 v[192:195], v[152:155], a[192:195], 0\n"
"v_rcp_f32 v243, v196\n"
"v_mfma_f32_16x16x32_bf16 v[192:195], v[156:159], a[196:199], v[192:195]\n"
"s_waitcnt lgkmcnt(0)\n"
"v_xor_b32 v183, s77, v183\n"
"v_xor_b32 v184, s77, v184\n"
"v_mfma_f32_16x16x32_bf16 v[192:195], v[160:163], a[200:203], v[192:195]\n"
"v_xor_b32 v185, s77, v185\n"
"v_xor_b32 v186, s77, v186\n"
"v_mfma_f32_16x16x32_bf16 v[192:195], v[164:167], a[204:207], v[192:195]\n"
"v_rcp_f32 v244, v198\n"
"v_mfma_f32_16x16x32_bf16 v[124:127], v[208:211], a[0:3], v[180:183]\n"
"v_rcp_f32 v245, v200\n"
"v_mfma_f32_16x16x32_bf16 v[124:127], v[212:215], a[4:7], v[124:127]\n"
"ds_read_b64_tr_b16 a[252:253], v235 offset:50176\n"
"v_xor_b32 v187, s77, v187\n"
"v_mfma_f32_16x16x32_bf16 v[124:127], v[216:219], a[8:11], v[124:127]\n"
"ds_read_b64_tr_b16 a[254:255], v235 offset:54272\n"
"v_xor_b32 v188, s77, v188\n"
"v_mfma_f32_16x16x32_bf16 v[124:127], v[220:223], a[12:15], v[124:127]\n"
"s_add_u32 s40, s56, s54\n"
"buffer_load_dwordx4 v240, s[20:23], s40 offen offset:3072 nt lds\n"
"v_mfma_f32_16x16x32_bf16 v[128:131], v[216:219], a[24:27], v[184:187]\n"
"s_add_u32 s55, s55, s59\n"
"ds_read_b64_tr_b16 a[244:245], v232 offset:49152\n"
"v_xor_b32 v189, s77, v189\n"
"v_mfma_f32_16x16x32_bf16 v[128:131], v[220:223], a[28:31], v[128:131]\n"
"s_add_u32 s41, s55, s57\n"
"ds_read_b64_tr_b16 a[246:247], v232 offset:53248\n"
"v_xor_b32 v190, s77, v190\n"
"v_mfma_f32_16x16x32_bf16 v[128:131], v[208:211], a[16:19], v[128:131]\n"
"s_and_b32 s41, s41, s58\n"
"ds_read_b64_tr_b16 a[248:249], v236 offset:49152\n"
"v_xor_b32 v191, s77, v191\n"
"v_mfma_f32_16x16x32_bf16 v[128:131], v[212:215], a[20:23], v[128:131]\n"
"s_andn2_b32 s56, s55, s58\n"
"ds_read_b64_tr_b16 a[250:251], v236 offset:53248\n"
"v_xor_b32 v192, s77, v192\n"
"v_mfma_f32_16x16x32_bf16 v[132:135], v[208:211], a[32:35], v[188:191]\n"
"s_or_b32 s56, s56, s41\n"
"v_xor_b32 v193, s77, v193\n"
"v_xor_b32 v194, s77, v194\n"
"v_mfma_f32_16x16x32_bf16 v[132:135], v[212:215], a[36:39], v[132:135]\n"
"v_xor_b32 v195, s77, v195\n"
"v_accvgpr_mov_b32 a100, a52\n"
"v_mfma_f32_16x16x32_bf16 v[132:135], v[216:219], a[40:43], v[132:135]\n"
"v_accvgpr_mov_b32 a101, a53\n"
"v_accvgpr_mov_b32 a102, a54\n"
"v_mfma_f32_16x16x32_bf16 v[132:135], v[220:223], a[44:47], v[132:135]\n"
"v_accvgpr_mov_b32 a103, a55\n"
"v_accvgpr_mov_b32 a116, a68\n"
"v_mfma_f32_16x16x32_bf16 v[136:139], v[216:219], a[200:203], v[192:195]\n"
"ds_read_b128 v[216:219], v228 offset:5120\n"
"v_accvgpr_mov_b32 a117, a69\n"
"v_mfma_f32_16x16x32_bf16 v[136:139], v[220:223], a[204:207], v[136:139]\n"
"ds_read_b128 v[220:223], v229 offset:5120\n"
"v_accvgpr_mov_b32 a118, a70\n"
"v_mfma_f32_16x16x32_bf16 v[136:139], v[208:211], a[192:195], v[136:139]\n"
"ds_read_b128 v[208:211], v228 offset:4096\n"
"v_accvgpr_mov_b32 a119, a71\n"
"v_mfma_f32_16x16x32_bf16 v[136:139], v[212:215], a[196:199], v[136:139]\n"
"ds_read_b128 v[212:215], v229 offset:4096\n"
"v_accvgpr_mov_b32 a132, a84\n"
"v_accvgpr_mov_b32 a133, a85\n"
"v_accvgpr_mov_b32 a134, a86\n"
"v_accvgpr_mov_b32 a135, a87\n"
"v_accvgpr_mov_b32 a108, a60\n"
"v_accvgpr_mov_b32 a109, a61\n"
"v_accvgpr_mov_b32 a110, a62\n"
"v_accvgpr_mov_b32 a111, a63\n"
"v_accvgpr_mov_b32 a124, a76\n"
"v_accvgpr_mov_b32 a125, a77\n"
"v_accvgpr_mov_b32 a126, a78\n"
"v_accvgpr_mov_b32 a127, a79\n"
"v_accvgpr_mov_b32 a140, a92\n"
"v_accvgpr_mov_b32 a141, a93\n"
"v_accvgpr_mov_b32 a142, a94\n"
"v_accvgpr_mov_b32 a143, a95\n"
"v_accvgpr_mov_b32 a96, a48\n"
"v_accvgpr_mov_b32 a97, a49\n"
"v_accvgpr_mov_b32 a98, a50\n"
"v_accvgpr_mov_b32 a99, a51\n"
"v_accvgpr_mov_b32 a112, a64\n"
"v_accvgpr_mov_b32 a113, a65\n"
"v_accvgpr_mov_b32 a114, a66\n"
"v_accvgpr_mov_b32 a115, a67\n"
"v_accvgpr_mov_b32 a128, a80\n"
"v_accvgpr_mov_b32 a129, a81\n"
"v_accvgpr_mov_b32 a130, a82\n"
"v_accvgpr_mov_b32 a131, a83\n"
"v_accvgpr_mov_b32 a104, a56\n"
"v_accvgpr_mov_b32 a105, a57\n"
"v_accvgpr_mov_b32 a106, a58\n"
"v_accvgpr_mov_b32 a107, a59\n"
"v_accvgpr_mov_b32 a120, a72\n"
"v_accvgpr_mov_b32 a121, a73\n"
"v_accvgpr_mov_b32 a122, a74\n"
"v_accvgpr_mov_b32 a123, a75\n"
"v_accvgpr_mov_b32 a136, a88\n"
"v_accvgpr_mov_b32 a137, a89\n"
"v_accvgpr_mov_b32 a138, a90\n"
"v_accvgpr_mov_b32 a139, a91\n"
"s_branch LTC_%=\n"
"LTH2_%=:\n"
"s_waitcnt lgkmcnt(0)\n"
"v_mfma_f32_16x16x32_bf16 a[224:227], v[176:179], a[244:247], a[224:227]\n"
"s_waitcnt vmcnt(8)\n"
"v_exp_f32 v142, v142\n"
"v_mfma_f32_16x16x32_bf16 v[16:19], v[152:155], a[244:247], v[16:19]\n"
"s_barrier\n"
"ds_read_b128 v[208:211], v228 offset:0\n"
"v_mfma_f32_16x16x32_bf16 v[16:19], v[156:159], a[248:251], v[16:19]\n"
"ds_read_b128 v[212:215], v229 offset:0\n"
"v_cvt_pk_bf16_f32 v102, v140, v141\n"
"v_mfma_f32_16x16x32_bf16 v[48:51], v[160:163], a[244:247], v[48:51]\n"
"ds_read_b128 v[216:219], v228 offset:1024\n"
"v_cvt_f32_u32 v254, s43\n"
"v_mfma_f32_16x16x32_bf16 v[48:51], v[164:167], a[248:251], v[48:51]\n"
"ds_read_b128 v[220:223], v229 offset:1024\n"
"v_and_b32 v255, 0x3f0, v242\n"
"v_mfma_f32_16x16x32_bf16 v[80:83], v[168:171], a[244:247], v[80:83]\n"
"v_exp_f32 v143, v143\n"
"v_mfma_f32_16x16x32_bf16 v[80:83], v[172:175], a[248:251], v[80:83]\n"
"v_exp_f32 v148, v148\n"
"v_mfma_f32_16x16x32_bf16 v[84:87], v[172:175], v[224:227], v[84:87]\n"
"v_exp_f32 v149, v149\n"
"v_mfma_f32_16x16x32_bf16 v[84:87], v[168:171], a[252:255], v[84:87]\n"
"ds_read_b64_tr_b16 a[244:245], v234 offset:49152\n"
"ds_read_b64_tr_b16 a[246:247], v234 offset:53248\n"
"v_mfma_f32_16x16x32_bf16 v[52:55], v[164:167], v[224:227], v[52:55]\n"
"v_exp_f32 v150, v150\n"
"v_mfma_f32_16x16x32_bf16 v[52:55], v[160:163], a[252:255], v[52:55]\n"
"v_exp_f32 v151, v151\n"
"v_mfma_f32_16x16x32_bf16 v[20:23], v[156:159], v[224:227], v[20:23]\n"
"ds_read_b64_tr_b16 a[248:249], v238 offset:49152\n"
"ds_read_b64_tr_b16 a[250:251], v238 offset:53248\n"
"v_mfma_f32_16x16x32_bf16 v[20:23], v[152:155], a[252:255], v[20:23]\n"
"v_cvt_pk_bf16_f32 v103, v142, v143\n"
"v_cvt_pk_bf16_f32 v118, v148, v149\n"
"v_mfma_f32_16x16x32_bf16 a[228:231], v[176:179], a[252:255], a[228:231]\n"
"v_exp_f32 v144, v144\n"
"s_waitcnt lgkmcnt(2)\n"
"v_mfma_f32_16x16x32_bf16 a[216:219], v[120:123], a[244:247], a[216:219]\n"
"s_add_u32 m0, s44, 81920\n"
"v_exp_f32 v145, v145\n"
"v_smfmac_f32_16x16x64_bf16 v[196:199], a[240:243], v[96:103], v241\n"
"buffer_load_dwordx4 v240, s[20:23], s56 offen nt lds\n"
"v_mfma_f32_16x16x32_bf16 v[8:11], v[96:99], a[244:247], v[8:11]\n"
"s_waitcnt lgkmcnt(0)\n"
"v_cvt_pk_bf16_f32 v119, v150, v151\n"
"v_cvt_pk_bf16_f32 v110, v144, v145\n"
"v_mfma_f32_16x16x32_bf16 v[8:11], v[100:103], a[248:251], v[8:11]\n"
"v_exp_f32 v146, v146\n"
"v_exp_f32 v147, v147\n"
"v_smfmac_f32_16x16x64_bf16 v[200:203], a[240:243], v[112:119], v241\n"
"ds_read_b64_tr_b16 v[224:225], v239 offset:49152\n"
"v_cvt_pk_bf16_f32 v111, v146, v147\n"
"v_mfma_f32_16x16x32_bf16 v[40:43], v[104:107], a[244:247], v[40:43]\n"
"ds_read_b64_tr_b16 v[226:227], v239 offset:53248\n"
"v_lshrrev_b32 v255, 1, v255\n"
"v_mfma_f32_16x16x32_bf16 v[40:43], v[108:111], a[248:251], v[40:43]\n"
"ds_read_b64_tr_b16 a[252:253], v235 offset:49152\n"
"ds_read_b64_tr_b16 a[254:255], v235 offset:53248\n"
"v_smfmac_f32_16x16x64_bf16 v[198:201], a[240:243], v[104:111], v241\n"
"v_add_f32 v196, v196, v197\n"
"v_sub_u32 v255, v242, v255\n"
"v_mfma_f32_16x16x32_bf16 v[72:75], v[112:115], a[244:247], v[72:75]\n"
"ds_read_b64_tr_b16 a[244:245], v234 offset:50176\n"
"v_xor_b32 v250, 0x1000, v255\n"
"v_mfma_f32_16x16x32_bf16 v[72:75], v[116:119], a[248:251], v[72:75]\n"
"ds_read_b64_tr_b16 a[246:247], v234 offset:54272\n"
"v_mfma_f32_16x16x32_bf16 v[202:205], a[240:243], v[120:123], v[202:205]\n"
"s_waitcnt lgkmcnt(4)\n"
"ds_read_b64_tr_b16 a[248:249], v238 offset:50176\n"
"v_mfma_f32_16x16x32_bf16 v[76:79], v[116:119], v[224:227], v[76:79]\n"
"s_waitcnt lgkmcnt(3)\n"
"ds_read_b64_tr_b16 a[250:251], v238 offset:54272\n"
"v_mfma_f32_16x16x32_bf16 v[76:79], v[112:115], a[252:255], v[76:79]\n"
"v_mfma_f32_16x16x32_bf16 v[44:47], v[108:111], v[224:227], v[44:47]\n"
"v_add_f32 v198, v198, v199\n"
"v_add_f32 v200, v200, v201\n"
"v_mfma_f32_16x16x32_bf16 v[44:47], v[104:107], a[252:255], v[44:47]\n"
"v_add_f32 v249, v196, v198\n"
"v_add_f32 v249, v249, v200\n"
"v_mfma_f32_16x16x32_bf16 v[12:15], v[100:103], v[224:227], v[12:15]\n"
"s_add_u32 s40, s56, s52\n"
"buffer_load_dwordx4 v240, s[20:23], s40 offen offset:1024 nt lds\n"
"v_mfma_f32_16x16x32_bf16 v[12:15], v[96:99], a[252:255], v[12:15]\n"
"ds_read_b64_tr_b16 v[224:225], v239 offset:50176\n"
"v_add_f32 v254, v254, v202\n"
"v_min3_f32 v254, v254, v196, v198\n"
"v_min_f32 v254, v254, v200\n"
"v_rcp_f32 v254, v254\n"
"v_mfma_f32_16x16x32_bf16 a[220:223], v[120:123], a[252:255], a[220:223]\n"
"ds_read_b64_tr_b16 v[226:227], v239 offset:54272\n"
"v_add_f32 v249, v249, v202\n"
"v_fmac_f32 v249, 0x53800000, v254\n"
"v_cmp_nge_f32 vcc, s46, v249\n"
"s_cbranch_vccnz LSLOW_%=\nLBACK2_%=:\ns_mov_b64 vcc, s[64:65]\n"
"s_waitcnt lgkmcnt(2)\n"
"v_mfma_f32_16x16x32_bf16 a[232:235], v[120:123], a[244:247], a[232:235]\n"
"v_xor_b32 v207, 0x400, v248\n"
"v_bfe_u32 v206, v242, 8, 2\n"
"v_lshl_add_u32 v206, v206, 4, s43\n"
"v_mfma_f32_16x16x32_bf16 v[24:27], v[96:99], a[244:247], v[24:27]\n"
"ds_read_b128 a[196:199], v207 offset:4096\n"
"ds_read_b128 a[204:207], v207 offset:6144\n"
"ds_read_b128 v[152:155], v206 offset:0\n"
"v_xor_b32 v207, 64, v206\n"
"v_mfma_f32_16x16x32_bf16 v[24:27], v[100:103], a[248:251], v[24:27]\n"
"ds_read_b128 v[156:159], v207 offset:0\n"
"v_accvgpr_mov_b32 a0, a144\n"
"v_mfma_f32_16x16x32_bf16 v[56:59], v[104:107], a[244:247], v[56:59]\n"
"ds_read_b128 v[160:163], v206 offset:1024\n"
"v_accvgpr_mov_b32 a1, a145\n"
"v_mfma_f32_16x16x32_bf16 v[56:59], v[108:111], a[248:251], v[56:59]\n"
"ds_read_b128 v[164:167], v207 offset:1024\n"
"v_accvgpr_mov_b32 a2, a146\n"
"v_mfma_f32_16x16x32_bf16 v[88:91], v[112:115], a[244:247], v[88:91]\n"
"v_accvgpr_mov_b32 a3, a147\n"
"v_accvgpr_mov_b32 a4, a148\n"
"v_mfma_f32_16x16x32_bf16 v[88:91], v[116:119], a[248:251], v[88:91]\n"
"s_waitcnt lgkmcnt(3)\n"
"v_accvgpr_mov_b32 a5, a149\n"
"v_accvgpr_mov_b32 a6, a150\n"
"v_accvgpr_mov_b32 a7, a151\n"
"v_mfma_f32_16x16x32_bf16 v[180:183], v[152:155], a[0:3], 0\n"
"s_waitcnt lgkmcnt(2)\n"
"v_accvgpr_mov_b32 a8, a152\n"
"v_accvgpr_mov_b32 a9, a153\n"
"v_accvgpr_mov_b32 a10, a154\n"
"v_accvgpr_mov_b32 a11, a155\n"
"v_mfma_f32_16x16x32_bf16 v[180:183], v[156:159], a[4:7], v[180:183]\n"
"s_waitcnt lgkmcnt(1)\n"
"v_accvgpr_mov_b32 a12, a156\n"
"v_accvgpr_mov_b32 a13, a157\n"
"v_accvgpr_mov_b32 a14, a158\n"
"v_accvgpr_mov_b32 a15, a159\n"
"v_mfma_f32_16x16x32_bf16 v[180:183], v[160:163], a[8:11], v[180:183]\n"
"s_waitcnt lgkmcnt(0)\n"
"v_accvgpr_mov_b32 a16, a160\n"
"v_accvgpr_mov_b32 a17, a161\n"
"v_accvgpr_mov_b32 a18, a162\n"
"v_accvgpr_mov_b32 a19, a163\n"
"v_mfma_f32_16x16x32_bf16 v[180:183], v[164:167], a[12:15], v[180:183]\n"
"v_accvgpr_mov_b32 a20, a164\n"
"v_accvgpr_mov_b32 a21, a165\n"
"v_accvgpr_mov_b32 a22, a166\n"
"v_accvgpr_mov_b32 a23, a167\n"
"v_mfma_f32_16x16x32_bf16 v[184:187], v[152:155], a[16:19], 0\n"
"v_accvgpr_mov_b32 a24, a168\n"
"v_accvgpr_mov_b32 a25, a169\n"
"v_accvgpr_mov_b32 a26, a170\n"
"v_accvgpr_mov_b32 a27, a171\n"
"v_mfma_f32_16x16x32_bf16 v[184:187], v[156:159], a[20:23], v[184:187]\n"
"s_add_u32 s40, s56, s53\n"
"buffer_load_dwordx4 v240, s[20:23], s40 offen offset:2048 nt lds\n"
"v_accvgpr_mov_b32 a28, a172\n"
"v_accvgpr_mov_b32 a29, a173\n"
"v_accvgpr_mov_b32 a30, a174\n"
"v_accvgpr_mov_b32 a31, a175\n"
"v_mfma_f32_16x16x32_bf16 v[184:187], v[160:163], a[24:27], v[184:187]\n"
"ds_read_b128 a[192:195], v248 offset:4096\n"
"v_accvgpr_mov_b32 a32, a176\n"
"v_accvgpr_mov_b32 a33, a177\n"
"v_accvgpr_mov_b32 a34, a178\n"
"v_accvgpr_mov_b32 a35, a179\n"
"v_mfma_f32_16x16x32_bf16 v[184:187], v[164:167], a[28:31], v[184:187]\n"
"v_accvgpr_mov_b32 a36, a180\n"
"v_accvgpr_mov_b32 a37, a181\n"
"v_accvgpr_mov_b32 a38, a182\n"
"v_accvgpr_mov_b32 a39, a183\n"
"v_mfma_f32_16x16x32_bf16 v[188:191], v[152:155], a[32:35], 0\n"
"ds_read_b128 a[200:203], v248 offset:6144\n"
"v_accvgpr_mov_b32 a40, a184\n"
"v_accvgpr_mov_b32 a41, a185\n"
"v_accvgpr_mov_b32 a42, a186\n"
"v_accvgpr_mov_b32 a43, a187\n"
"v_mfma_f32_16x16x32_bf16 v[188:191], v[156:159], a[36:39], v[188:191]\n"
"v_accvgpr_mov_b32 a44, a188\n"
"v_accvgpr_mov_b32 a45, a189\n"
"v_accvgpr_mov_b32 a46, a190\n"
"v_accvgpr_mov_b32 a47, a191\n"
"v_mfma_f32_16x16x32_bf16 v[188:191], v[160:163], a[40:43], v[188:191]\n"
"v_mov_b32 v252, v192\n"
"v_xor_b32 v180, s77, v180\n"
"v_mfma_f32_16x16x32_bf16 v[188:191], v[164:167], a[44:47], v[188:191]\n"
"s_waitcnt lgkmcnt(1)\n"
"v_xor_b32 v181, s77, v181\n"
"v_xor_b32 v182, s77, v182\n"
"v_mfma_f32_16x16x32_bf16 v[192:195], v[152:155], a[192:195], 0\n"
"v_rcp_f32 v243, v196\n"
"v_mfma_f32_16x16x32_bf16 v[192:195], v[156:159], a[196:199], v[192:195]\n"
"s_waitcnt lgkmcnt(0)\n"
"v_xor_b32 v183, s77, v183\n"
"v_xor_b32 v184, s77, v184\n"
"v_mfma_f32_16x16x32_bf16 v[192:195], v[160:163], a[200:203], v[192:195]\n"
"v_xor_b32 v185, s77, v185\n"
"v_xor_b32 v186, s77, v186\n"
"v_mfma_f32_16x16x32_bf16 v[192:195], v[164:167], a[204:207], v[192:195]\n"
"v_rcp_f32 v244, v198\n"
"v_mfma_f32_16x16x32_bf16 v[124:127], v[208:211], a[0:3], v[180:183]\n"
"v_rcp_f32 v245, v200\n"
"v_mfma_f32_16x16x32_bf16 v[124:127], v[212:215], a[4:7], v[124:127]\n"
"ds_read_b64_tr_b16 a[252:253], v235 offset:50176\n"
"v_xor_b32 v187, s77, v187\n"
"v_mfma_f32_16x16x32_bf16 v[124:127], v[216:219], a[8:11], v[124:127]\n"
"ds_read_b64_tr_b16 a[254:255], v235 offset:54272\n"
"v_xor_b32 v188, s77, v188\n"
"v_mfma_f32_16x16x32_bf16 v[124:127], v[220:223], a[12:15], v[124:127]\n"
"s_add_u32 s40, s56, s54\n"
"buffer_load_dwordx4 v240, s[20:23], s40 offen offset:3072 nt lds\n"
"v_mfma_f32_16x16x32_bf16 v[128:131], v[216:219], a[24:27], v[184:187]\n"
"s_add_u32 s55, s55, s59\n"
"ds_read_b64_tr_b16 a[244:245], v232 offset:49152\n"
"v_xor_b32 v189, s77, v189\n"
"v_mfma_f32_16x16x32_bf16 v[128:131], v[220:223], a[28:31], v[128:131]\n"
"s_add_u32 s41, s55, s57\n"
"ds_read_b64_tr_b16 a[246:247], v232 offset:53248\n"
"v_xor_b32 v190, s77, v190\n"
"v_mfma_f32_16x16x32_bf16 v[128:131], v[208:211], a[16:19], v[128:131]\n"
"s_and_b32 s41, s41, s58\n"
"ds_read_b64_tr_b16 a[248:249], v236 offset:49152\n"
"v_xor_b32 v191, s77, v191\n"
"v_mfma_f32_16x16x32_bf16 v[128:131], v[212:215], a[20:23], v[128:131]\n"
"s_andn2_b32 s56, s55, s58\n"
"ds_read_b64_tr_b16 a[250:251], v236 offset:53248\n"
"v_xor_b32 v192, s77, v192\n"
"v_mfma_f32_16x16x32_bf16 v[132:135], v[208:211], a[32:35], v[188:191]\n"
"s_or_b32 s56, s56, s41\n"
"v_xor_b32 v193, s77, v193\n"
"v_xor_b32 v194, s77, v194\n"
"v_mfma_f32_16x16x32_bf16 v[132:135], v[212:215], a[36:39], v[132:135]\n"
"v_xor_b32 v195, s77, v195\n"
"v_accvgpr_mov_b32 a148, a52\n"
"v_mfma_f32_16x16x32_bf16 v[132:135], v[216:219], a[40:43], v[132:135]\n"
"v_accvgpr_mov_b32 a149, a53\n"
"v_accvgpr_mov_b32 a150, a54\n"
"v_mfma_f32_16x16x32_bf16 v[132:135], v[220:223], a[44:47], v[132:135]\n"
"v_accvgpr_mov_b32 a151, a55\n"
"v_accvgpr_mov_b32 a164, a68\n"
"v_mfma_f32_16x16x32_bf16 v[136:139], v[216:219], a[200:203], v[192:195]\n"
"ds_read_b128 v[216:219], v228 offset:5120\n"
"v_accvgpr_mov_b32 a165, a69\n"
"v_mfma_f32_16x16x32_bf16 v[136:139], v[220:223], a[204:207], v[136:139]\n"
"ds_read_b128 v[220:223], v229 offset:5120\n"
"v_accvgpr_mov_b32 a166, a70\n"
"v_mfma_f32_16x16x32_bf16 v[136:139], v[208:211], a[192:195], v[136:139]\n"
"ds_read_b128 v[208:211], v228 offset:4096\n"
"v_accvgpr_mov_b32 a167, a71\n"
"v_mfma_f32_16x16x32_bf16 v[136:139], v[212:215], a[196:199], v[136:139]\n"
"ds_read_b128 v[212:215], v229 offset:4096\n"
"v_accvgpr_mov_b32 a180, a84\n"
"v_accvgpr_mov_b32 a181, a85\n"
"v_accvgpr_mov_b32 a182, a86\n"
"v_accvgpr_mov_b32 a183, a87\n"
"v_accvgpr_mov_b32 a156, a60\n"
"v_accvgpr_mov_b32 a157, a61\n"
"v_accvgpr_mov_b32 a158, a62\n"
"v_accvgpr_mov_b32 a159, a63\n"
"v_accvgpr_mov_b32 a172, a76\n"
"v_accvgpr_mov_b32 a173, a77\n"
"v_accvgpr_mov_b32 a174, a78\n"
"v_accvgpr_mov_b32 a175, a79\n"
"v_accvgpr_mov_b32 a188, a92\n"
"v_accvgpr_mov_b32 a189, a93\n"
"v_accvgpr_mov_b32 a190, a94\n"
"v_accvgpr_mov_b32 a191, a95\n"
"v_accvgpr_mov_b32 a144, a48\n"
"v_accvgpr_mov_b32 a145, a49\n"
"v_accvgpr_mov_b32 a146, a50\n"
"v_accvgpr_mov_b32 a147, a51\n"
"v_accvgpr_mov_b32 a160, a64\n"
"v_accvgpr_mov_b32 a161, a65\n"
"v_accvgpr_mov_b32 a162, a66\n"
"v_accvgpr_mov_b32 a163, a67\n"
"v_accvgpr_mov_b32 a176, a80\n"
"v_accvgpr_mov_b32 a177, a81\n"
"v_accvgpr_mov_b32 a178, a82\n"
"v_accvgpr_mov_b32 a179, a83\n"
"v_accvgpr_mov_b32 a152, a56\n"
"v_accvgpr_mov_b32 a153, a57\n"
"v_accvgpr_mov_b32 a154, a58\n"
"v_accvgpr_mov_b32 a155, a59\n"
"v_accvgpr_mov_b32 a168, a72\n"
"v_accvgpr_mov_b32 a169, a73\n"
"v_accvgpr_mov_b32 a170, a74\n"
"v_accvgpr_mov_b32 a171, a75\n"
"v_accvgpr_mov_b32 a184, a88\n"
"v_accvgpr_mov_b32 a185, a89\n"
"v_accvgpr_mov_b32 a186, a90\n"
"v_accvgpr_mov_b32 a187, a91\n"
"LTC_%=:\n"
"v_mfma_f32_16x16x32_bf16 v[92:95], v[116:119], v[224:227], v[92:95]\n"
"s_waitcnt lgkmcnt(4)\n"
"v_exp_f32 v124, v124\n"
"v_mfma_f32_16x16x32_bf16 v[92:95], v[112:115], a[252:255], v[92:95]\n"
"s_add_u32 m0, s44, 32768\n"
"v_exp_f32 v125, v125\n"
"v_mfma_f32_16x16x32_bf16 v[60:63], v[108:111], v[224:227], v[60:63]\n"
"buffer_load_dwordx4 v240, s[16:19], s56 offen nt lds\n"
"v_mfma_f32_16x16x32_bf16 v[60:63], v[104:107], a[252:255], v[60:63]\n"
"v_exp_f32 v126, v126\n"
"v_mfma_f32_16x16x32_bf16 v[28:31], v[100:103], v[224:227], v[28:31]\n"
"ds_read_b64_tr_b16 v[224:225], v237 offset:49152\n"
"v_cvt_pk_bf16_f32 v152, v124, v125\n"
"v_mfma_f32_16x16x32_bf16 v[28:31], v[96:99], a[252:255], v[28:31]\n"
"ds_read_b64_tr_b16 v[226:227], v237 offset:53248\n"
"v_mov_b32 v253, v202\n"
"v_mfma_f32_16x16x32_bf16 a[236:239], v[120:123], a[252:255], a[236:239]\n"
"s_waitcnt lgkmcnt(2)\n"
"ds_write_b64 v250, v[252:253] offset:52224\n"
"v_mfma_f32_16x16x32_bf16 v[140:143], v[216:219], a[8:11], v[180:183]\n"
"ds_read_b64_tr_b16 a[252:253], v233 offset:49152\n"
"v_xor_b32 v254, 0x11000, v242\n"
"v_mfma_f32_16x16x32_bf16 v[140:143], v[220:223], a[12:15], v[140:143]\n"
"ds_write_b128 v254, a[216:219] offset:32768\n"
"v_mfma_f32_16x16x32_bf16 v[140:143], v[208:211], a[0:3], v[140:143]\n"
"ds_write_b128 v254, a[220:223] offset:33792\n"
"v_mfma_f32_16x16x32_bf16 v[140:143], v[212:215], a[4:7], v[140:143]\n"
"ds_write_b128 v254, a[232:235] offset:34816\n"
"v_mfma_f32_16x16x32_bf16 v[144:147], v[208:211], a[16:19], v[184:187]\n"
"ds_read_b64_tr_b16 a[254:255], v233 offset:53248\n"
"v_mov_b32_dpp v243, v243 row_ror:12 row_mask:0x2 bank_mask:0xf\n"
"v_mfma_f32_16x16x32_bf16 v[144:147], v[212:215], a[20:23], v[144:147]\n"
"ds_write_b128 v254, a[236:239] offset:35840\n"
"v_mfma_f32_16x16x32_bf16 v[144:147], v[216:219], a[24:27], v[144:147]\n"
"v_exp_f32 v127, v127\n"
"v_mfma_f32_16x16x32_bf16 v[144:147], v[220:223], a[28:31], v[144:147]\n"
"v_exp_f32 v128, v128\n"
"v_mfma_f32_16x16x32_bf16 v[148:151], v[216:219], a[40:43], v[188:191]\n"
"v_exp_f32 v129, v129\n"
"v_mfma_f32_16x16x32_bf16 v[148:151], v[220:223], a[44:47], v[148:151]\n"
"s_add_u32 s40, s56, s52\n"
"buffer_load_dwordx4 v240, s[16:19], s40 offen offset:1024 nt lds\n"
"v_mfma_f32_16x16x32_bf16 v[148:151], v[208:211], a[32:35], v[148:151]\n"
"v_exp_f32 v130, v130\n"
"v_mfma_f32_16x16x32_bf16 v[148:151], v[212:215], a[36:39], v[148:151]\n"
"v_cvt_pk_bf16_f32 v153, v126, v127\n"
"v_cvt_pk_bf16_f32 v160, v128, v129\n"
"v_mfma_f32_16x16x32_bf16 v[124:127], v[208:211], a[192:195], v[192:195]\n"
"ds_read_b128 v[208:211], v230 offset:0\n"
"v_mov_b32_dpp v243, v243 row_ror:8 row_mask:0x4 bank_mask:0xf\n"
"v_mfma_f32_16x16x32_bf16 v[124:127], v[212:215], a[196:199], v[124:127]\n"
"ds_read_b128 v[212:215], v231 offset:0\n"
"v_mov_b32_dpp v244, v244 row_ror:12 row_mask:0x2 bank_mask:0xf\n"
"v_mfma_f32_16x16x32_bf16 v[124:127], v[216:219], a[200:203], v[124:127]\n"
"ds_read_b128 v[216:219], v230 offset:1024\n"
"v_mov_b32_dpp v243, v243 row_ror:4 row_mask:0x8 bank_mask:0xf\n"
"v_mfma_f32_16x16x32_bf16 v[124:127], v[220:223], a[204:207], v[124:127]\n"
"v_exp_f32 v131, v131\n"
"v_mfma_f32_16x16x32_bf16 a[208:211], v[120:123], a[244:247], a[208:211]\n"
"ds_read_b128 v[220:223], v231 offset:1024\n"
"v_cvt_pk_bf16_f32 v161, v130, v131\n"
"v_mfma_f32_16x16x32_bf16 v[0:3], v[96:99], a[244:247], v[0:3]\n"
"v_exp_f32 v132, v132\n"
"v_mfma_f32_16x16x32_bf16 v[0:3], v[100:103], a[248:251], v[0:3]\n"
"v_exp_f32 v133, v133\n"
"v_mfma_f32_16x16x32_bf16 v[32:35], v[104:107], a[244:247], v[32:35]\n"
"v_exp_f32 v134, v134\n"
"v_mfma_f32_16x16x32_bf16 v[32:35], v[108:111], a[248:251], v[32:35]\n"
"v_exp_f32 v135, v135\n"
"v_mfma_f32_16x16x32_bf16 v[64:67], v[112:115], a[244:247], v[64:67]\n"
"ds_read_b64_tr_b16 a[244:245], v232 offset:50176\n"
"v_cvt_pk_bf16_f32 v168, v132, v133\n"
"v_mfma_f32_16x16x32_bf16 v[64:67], v[116:119], a[248:251], v[64:67]\n"
"s_waitcnt lgkmcnt(1)\n"
"ds_read_b64_tr_b16 a[246:247], v232 offset:54272\n"
"v_cvt_pk_bf16_f32 v169, v134, v135\n"
"v_mfma_f32_16x16x32_bf16 v[128:131], v[208:211], a[0:3], v[180:183]\n"
"v_exp_f32 v136, v136\n"
"v_mfma_f32_16x16x32_bf16 v[128:131], v[212:215], a[4:7], v[128:131]\n"
"s_add_u32 s40, s56, s53\n"
"buffer_load_dwordx4 v240, s[16:19], s40 offen offset:2048 nt lds\n"
"v_mfma_f32_16x16x32_bf16 v[128:131], v[216:219], a[8:11], v[128:131]\n"
"v_exp_f32 v137, v137\n"
"v_mfma_f32_16x16x32_bf16 v[128:131], v[220:223], a[12:15], v[128:131]\n"
"v_exp_f32 v138, v138\n"
"v_mfma_f32_16x16x32_bf16 v[132:135], v[216:219], a[24:27], v[184:187]\n"
"ds_read_b64_tr_b16 a[248:249], v236 offset:50176\n"
"v_cvt_pk_bf16_f32 v176, v136, v137\n"
"v_mfma_f32_16x16x32_bf16 v[132:135], v[220:223], a[28:31], v[132:135]\n"
"v_exp_f32 v139, v139\n"
"v_mfma_f32_16x16x32_bf16 v[132:135], v[208:211], a[16:19], v[132:135]\n"
"v_exp_f32 v140, v140\n"
"v_mfma_f32_16x16x32_bf16 v[132:135], v[212:215], a[20:23], v[132:135]\n"
"ds_read_b64_tr_b16 a[250:251], v236 offset:54272\n"
"v_cvt_pk_bf16_f32 v177, v138, v139\n"
"v_mfma_f32_16x16x32_bf16 v[136:139], v[208:211], a[32:35], v[188:191]\n"
"v_exp_f32 v141, v141\n"
"v_mfma_f32_16x16x32_bf16 v[136:139], v[212:215], a[36:39], v[136:139]\n"
"ds_read_b128 v[208:211], v230 offset:4096\n"
"v_cvt_pk_bf16_f32 v154, v140, v141\n"
"v_mfma_f32_16x16x32_bf16 v[136:139], v[216:219], a[40:43], v[136:139]\n"
"v_exp_f32 v142, v142\n"
"v_mfma_f32_16x16x32_bf16 v[136:139], v[220:223], a[44:47], v[136:139]\n"
"v_exp_f32 v143, v143\n"
"v_mfma_f32_16x16x32_bf16 v[68:71], v[116:119], v[224:227], v[68:71]\n"
"ds_read_b128 v[212:215], v231 offset:4096\n"
"v_cvt_pk_bf16_f32 v155, v142, v143\n"
"v_mfma_f32_16x16x32_bf16 v[68:71], v[112:115], a[252:255], v[68:71]\n"
"v_exp_f32 v144, v144\n"
"v_mfma_f32_16x16x32_bf16 v[36:39], v[108:111], v[224:227], v[36:39]\n"
"ds_read_b128 v[216:219], v230 offset:5120\n"
"v_mov_b32_dpp v245, v245 row_ror:12 row_mask:0x2 bank_mask:0xf\n"
"v_mfma_f32_16x16x32_bf16 v[36:39], v[104:107], a[252:255], v[36:39]\n"
"s_add_u32 s40, s56, s54\n"
"buffer_load_dwordx4 v240, s[16:19], s40 offen offset:3072 nt lds\n"
"v_mfma_f32_16x16x32_bf16 v[4:7], v[100:103], v[224:227], v[4:7]\n"
"ds_read_b128 v[220:223], v231 offset:5120\n"
"v_mov_b32_dpp v244, v244 row_ror:8 row_mask:0x4 bank_mask:0xf\n"
"v_mfma_f32_16x16x32_bf16 v[4:7], v[96:99], a[252:255], v[4:7]\n"
"v_exp_f32 v145, v145\n"
"v_mfma_f32_16x16x32_bf16 a[212:215], v[120:123], a[252:255], a[212:215]\n"
"s_waitcnt lgkmcnt(2)\n"
"v_exp_f32 v146, v146\n"
"v_mfma_f32_16x16x32_bf16 v[140:143], v[208:211], a[0:3], v[180:183]\n"
"v_exp_f32 v147, v147\n"
"v_mfma_f32_16x16x32_bf16 v[140:143], v[212:215], a[4:7], v[140:143]\n"
"s_waitcnt lgkmcnt(1)\n"
"v_cvt_pk_bf16_f32 v162, v144, v145\n"
"v_cvt_pk_bf16_f32 v163, v146, v147\n"
"v_mfma_f32_16x16x32_bf16 v[140:143], v[216:219], a[8:11], v[140:143]\n"
"s_waitcnt lgkmcnt(0)\n"
"v_exp_f32 v148, v148\n"
"v_mfma_f32_16x16x32_bf16 v[140:143], v[220:223], a[12:15], v[140:143]\n"
"v_exp_f32 v149, v149\n"
"v_mfma_f32_16x16x32_bf16 v[144:147], v[216:219], a[24:27], v[184:187]\n"
"v_exp_f32 v150, v150\n"
"v_mfma_f32_16x16x32_bf16 v[144:147], v[220:223], a[28:31], v[144:147]\n"
"v_exp_f32 v151, v151\n"
"v_mfma_f32_16x16x32_bf16 v[144:147], v[208:211], a[16:19], v[144:147]\n"
"ds_read_b64_tr_b16 a[252:253], v233 offset:50176\n"
"ds_read_b64_tr_b16 a[254:255], v233 offset:54272\n"
"v_mfma_f32_16x16x32_bf16 v[144:147], v[212:215], a[20:23], v[144:147]\n"
"ds_read_b64_tr_b16 v[224:225], v237 offset:50176\n"
"ds_read_b64_tr_b16 v[226:227], v237 offset:54272\n"
"v_cvt_pk_bf16_f32 v170, v148, v149\n"
"v_cvt_pk_bf16_f32 v171, v150, v151\n"
"v_mfma_f32_16x16x32_bf16 v[148:151], v[208:211], a[32:35], v[188:191]\n"
"v_mul_f32_dpp v8, v243, v8 row_newbcast:0 row_mask:0xf bank_mask:0xf\n"
"v_mul_f32_dpp v9, v243, v9 row_newbcast:1 row_mask:0xf bank_mask:0xf\n"
"v_mfma_f32_16x16x32_bf16 v[148:151], v[212:215], a[36:39], v[148:151]\n"
"s_cbranch_vccnz LPCT0_%=\nLPKT0_%=:\n"
"v_mul_f32_dpp v10, v243, v10 row_newbcast:2 row_mask:0xf bank_mask:0xf\n"
"v_mfma_f32_16x16x32_bf16 v[148:151], v[216:219], a[40:43], v[148:151]\n"
"v_mul_f32_dpp v11, v243, v11 row_newbcast:3 row_mask:0xf bank_mask:0xf\n"
"v_mul_f32_dpp v12, v243, v12 row_newbcast:0 row_mask:0xf bank_mask:0xf\n"
"v_mfma_f32_16x16x32_bf16 v[148:151], v[220:223], a[44:47], v[148:151]\n"
"v_mul_f32_dpp v13, v243, v13 row_newbcast:1 row_mask:0xf bank_mask:0xf\n"
"v_mul_f32_dpp v14, v243, v14 row_newbcast:2 row_mask:0xf bank_mask:0xf\n"
"v_mul_f32_dpp v15, v243, v15 row_newbcast:3 row_mask:0xf bank_mask:0xf\n"
"v_cvt_pk_bf16_f32 v8, v8, v9\n"
"v_cvt_pk_bf16_f32 v9, v10, v11\n"
"v_cvt_pk_bf16_f32 v10, v12, v13\n"
"v_cvt_pk_bf16_f32 v11, v14, v15\n"
"v_mfma_f32_16x16x32_bf16 a[224:227], v[120:123], a[244:247], a[224:227]\n"
"s_waitcnt vmcnt(8) lgkmcnt(0)\n"
"ds_write_b128 v246, v[8:11] offset:49152\n"
"v_mov_b32_dpp v245, v245 row_ror:8 row_mask:0x4 bank_mask:0xf\n"
"v_mov_b32_dpp v244, v244 row_ror:4 row_mask:0x8 bank_mask:0xf\n"
"v_mfma_f32_16x16x32_bf16 v[16:19], v[96:99], a[244:247], v[16:19]\n"
"s_barrier\n"
"ds_read_b64_tr_b16 a[52:53], v247 offset:49152\n"
"v_mov_b32_dpp v245, v245 row_ror:4 row_mask:0x8 bank_mask:0xf\n"
"v_mul_f32_dpp v40, v244, v40 row_newbcast:0 row_mask:0xf bank_mask:0xf\n"
"v_mul_f32_dpp v41, v244, v41 row_newbcast:1 row_mask:0xf bank_mask:0xf\n"
"v_mul_f32_dpp v42, v244, v42 row_newbcast:2 row_mask:0xf bank_mask:0xf\n"
"v_mul_f32_dpp v43, v244, v43 row_newbcast:3 row_mask:0xf bank_mask:0xf\n"
"v_mul_f32_dpp v44, v244, v44 row_newbcast:0 row_mask:0xf bank_mask:0xf\n"
"v_mul_f32_dpp v45, v244, v45 row_newbcast:1 row_mask:0xf bank_mask:0xf\n"
"v_mul_f32_dpp v46, v244, v46 row_newbcast:2 row_mask:0xf bank_mask:0xf\n"
"v_mul_f32_dpp v47, v244, v47 row_newbcast:3 row_mask:0xf bank_mask:0xf\n"
"v_cvt_pk_bf16_f32 v40, v40, v41\n"
"v_cvt_pk_bf16_f32 v41, v42, v43\n"
"v_cvt_pk_bf16_f32 v42, v44, v45\n"
"v_cvt_pk_bf16_f32 v43, v46, v47\n"
"v_mfma_f32_16x16x32_bf16 v[16:19], v[100:103], a[248:251], v[16:19]\n"
"ds_write_b128 v246, v[40:43] offset:50176\n"
"v_mul_f32_dpp v72, v245, v72 row_newbcast:0 row_mask:0xf bank_mask:0xf\n"
"v_mul_f32_dpp v73, v245, v73 row_newbcast:1 row_mask:0xf bank_mask:0xf\n"
"v_mul_f32_dpp v74, v245, v74 row_newbcast:2 row_mask:0xf bank_mask:0xf\n"
"v_mul_f32_dpp v75, v245, v75 row_newbcast:3 row_mask:0xf bank_mask:0xf\n"
"v_mul_f32_dpp v76, v245, v76 row_newbcast:0 row_mask:0xf bank_mask:0xf\n"
"v_mul_f32_dpp v77, v245, v77 row_newbcast:1 row_mask:0xf bank_mask:0xf\n"
"v_mul_f32_dpp v78, v245, v78 row_newbcast:2 row_mask:0xf bank_mask:0xf\n"
"v_mul_f32_dpp v79, v245, v79 row_newbcast:3 row_mask:0xf bank_mask:0xf\n"
"v_cvt_pk_bf16_f32 v72, v72, v73\n"
"v_cvt_pk_bf16_f32 v73, v74, v75\n"
"v_cvt_pk_bf16_f32 v74, v76, v77\n"
"v_cvt_pk_bf16_f32 v75, v78, v79\n"
"v_mfma_f32_16x16x32_bf16 v[48:51], v[104:107], a[244:247], v[48:51]\n"
"ds_write_b128 v246, v[72:75] offset:51200\n"
"v_mfma_f32_16x16x32_bf16 v[48:51], v[108:111], a[248:251], v[48:51]\n"
"v_mul_f32_dpp v24, v243, v24 row_newbcast:0 row_mask:0xf bank_mask:0xf\n"
"v_mul_f32_dpp v25, v243, v25 row_newbcast:1 row_mask:0xf bank_mask:0xf\n"
"v_mfma_f32_16x16x32_bf16 v[80:83], v[112:115], a[244:247], v[80:83]\n"
"ds_read_b64_tr_b16 a[54:55], v247 offset:49160\n"
"ds_read_b64_tr_b16 a[68:69], v247 offset:50176\n"
"v_mfma_f32_16x16x32_bf16 v[80:83], v[116:119], a[248:251], v[80:83]\n"
"ds_read_b64_tr_b16 a[70:71], v247 offset:50184\n"
"ds_read_b64_tr_b16 a[84:85], v247 offset:51200\n"
"v_mul_f32_dpp v26, v243, v26 row_newbcast:2 row_mask:0xf bank_mask:0xf\n"
"v_mul_f32_dpp v27, v243, v27 row_newbcast:3 row_mask:0xf bank_mask:0xf\n"
"v_mul_f32_dpp v28, v243, v28 row_newbcast:0 row_mask:0xf bank_mask:0xf\n"
"v_mul_f32_dpp v29, v243, v29 row_newbcast:1 row_mask:0xf bank_mask:0xf\n"
"v_mul_f32_dpp v30, v243, v30 row_newbcast:2 row_mask:0xf bank_mask:0xf\n"
"v_mul_f32_dpp v31, v243, v31 row_newbcast:3 row_mask:0xf bank_mask:0xf\n"
"v_cvt_pk_bf16_f32 v24, v24, v25\n"
"v_cvt_pk_bf16_f32 v25, v26, v27\n"
"v_cvt_pk_bf16_f32 v26, v28, v29\n"
"v_cvt_pk_bf16_f32 v27, v30, v31\n"
"v_mfma_f32_16x16x32_bf16 v[84:87], v[116:119], v[224:227], v[84:87]\n"
"ds_write_b128 v246, v[24:27] offset:49152\n"
"v_mul_f32_dpp v56, v244, v56 row_newbcast:0 row_mask:0xf bank_mask:0xf\n"
"v_mul_f32_dpp v57, v244, v57 row_newbcast:1 row_mask:0xf bank_mask:0xf\n"
"v_mul_f32_dpp v58, v244, v58 row_newbcast:2 row_mask:0xf bank_mask:0xf\n"
"v_mul_f32_dpp v59, v244, v59 row_newbcast:3 row_mask:0xf bank_mask:0xf\n"
"v_mul_f32_dpp v60, v244, v60 row_newbcast:0 row_mask:0xf bank_mask:0xf\n"
"v_mul_f32_dpp v61, v244, v61 row_newbcast:1 row_mask:0xf bank_mask:0xf\n"
"v_mul_f32_dpp v62, v244, v62 row_newbcast:2 row_mask:0xf bank_mask:0xf\n"
"v_mul_f32_dpp v63, v244, v63 row_newbcast:3 row_mask:0xf bank_mask:0xf\n"
"v_cvt_pk_bf16_f32 v56, v56, v57\n"
"v_cvt_pk_bf16_f32 v57, v58, v59\n"
"v_cvt_pk_bf16_f32 v58, v60, v61\n"
"v_cvt_pk_bf16_f32 v59, v62, v63\n"
"v_mfma_f32_16x16x32_bf16 v[84:87], v[112:115], a[252:255], v[84:87]\n"
"ds_read_b64_tr_b16 a[86:87], v247 offset:51208\n"
"ds_write_b128 v246, v[56:59] offset:50176\n"
"ds_read_b128 v[208:211], v228 offset:16384\n"
"v_mul_f32_dpp v88, v245, v88 row_newbcast:0 row_mask:0xf bank_mask:0xf\n"
"v_mul_f32_dpp v89, v245, v89 row_newbcast:1 row_mask:0xf bank_mask:0xf\n"
"v_mul_f32_dpp v90, v245, v90 row_newbcast:2 row_mask:0xf bank_mask:0xf\n"
"v_mul_f32_dpp v91, v245, v91 row_newbcast:3 row_mask:0xf bank_mask:0xf\n"
"v_mul_f32_dpp v92, v245, v92 row_newbcast:0 row_mask:0xf bank_mask:0xf\n"
"v_mul_f32_dpp v93, v245, v93 row_newbcast:1 row_mask:0xf bank_mask:0xf\n"
"v_mul_f32_dpp v94, v245, v94 row_newbcast:2 row_mask:0xf bank_mask:0xf\n"
"v_mul_f32_dpp v95, v245, v95 row_newbcast:3 row_mask:0xf bank_mask:0xf\n"
"v_cvt_pk_bf16_f32 v88, v88, v89\n"
"v_cvt_pk_bf16_f32 v89, v90, v91\n"
"v_cvt_pk_bf16_f32 v90, v92, v93\n"
"v_cvt_pk_bf16_f32 v91, v94, v95\n"
"v_mfma_f32_16x16x32_bf16 v[52:55], v[108:111], v[224:227], v[52:55]\n"
"ds_write_b128 v246, v[88:91] offset:51200\n"
"ds_read_b128 v[212:215], v229 offset:16384\n"
"v_mfma_f32_16x16x32_bf16 v[52:55], v[104:107], a[252:255], v[52:55]\n"
"ds_read_b64_tr_b16 a[60:61], v247 offset:49152\n"
"ds_read_b128 v[216:219], v228 offset:17408\n"
"v_mfma_f32_16x16x32_bf16 v[20:23], v[100:103], v[224:227], v[20:23]\n"
"ds_read_b64_tr_b16 a[62:63], v247 offset:49160\n"
"ds_read_b64_tr_b16 a[76:77], v247 offset:50176\n"
"ds_read_b128 v[220:223], v229 offset:17408\n"
"v_mfma_f32_16x16x32_bf16 v[20:23], v[96:99], a[252:255], v[20:23]\n"
"ds_read_b64_tr_b16 a[78:79], v247 offset:50184\n"
"ds_read_b64_tr_b16 a[92:93], v247 offset:51200\n"
"v_exp_f32 v124, v124\n"
"v_exp_f32 v125, v125\n"
"v_exp_f32 v126, v126\n"
"v_exp_f32 v127, v127\n"
"v_mul_f32_dpp v0, v243, v0 row_newbcast:0 row_mask:0xf bank_mask:0xf\n"
"v_mul_f32_dpp v1, v243, v1 row_newbcast:1 row_mask:0xf bank_mask:0xf\n"
"v_mul_f32_dpp v2, v243, v2 row_newbcast:2 row_mask:0xf bank_mask:0xf\n"
"v_mul_f32_dpp v3, v243, v3 row_newbcast:3 row_mask:0xf bank_mask:0xf\n"
"v_mul_f32_dpp v4, v243, v4 row_newbcast:0 row_mask:0xf bank_mask:0xf\n"
"v_mul_f32_dpp v5, v243, v5 row_newbcast:1 row_mask:0xf bank_mask:0xf\n"
"v_mul_f32_dpp v6, v243, v6 row_newbcast:2 row_mask:0xf bank_mask:0xf\n"
"v_mul_f32_dpp v7, v243, v7 row_newbcast:3 row_mask:0xf bank_mask:0xf\n"
"v_cvt_pk_bf16_f32 v0, v0, v1\n"
"v_cvt_pk_bf16_f32 v1, v2, v3\n"
"v_cvt_pk_bf16_f32 v2, v4, v5\n"
"v_cvt_pk_bf16_f32 v3, v6, v7\n"
"v_mfma_f32_16x16x32_bf16 a[228:231], v[120:123], a[252:255], a[228:231]\n"
"ds_write_b128 v246, v[0:3] offset:49152\n"
"v_cvt_pk_bf16_f32 v178, v124, v125\n"
"v_cvt_pk_bf16_f32 v179, v126, v127\n"
"v_mul_f32_dpp v32, v244, v32 row_newbcast:0 row_mask:0xf bank_mask:0xf\n"
"v_mul_f32_dpp v33, v244, v33 row_newbcast:1 row_mask:0xf bank_mask:0xf\n"
"v_mul_f32_dpp v34, v244, v34 row_newbcast:2 row_mask:0xf bank_mask:0xf\n"
"v_mul_f32_dpp v35, v244, v35 row_newbcast:3 row_mask:0xf bank_mask:0xf\n"
"v_mul_f32_dpp v36, v244, v36 row_newbcast:0 row_mask:0xf bank_mask:0xf\n"
"v_mul_f32_dpp v37, v244, v37 row_newbcast:1 row_mask:0xf bank_mask:0xf\n"
"v_mul_f32_dpp v38, v244, v38 row_newbcast:2 row_mask:0xf bank_mask:0xf\n"
"v_mul_f32_dpp v39, v244, v39 row_newbcast:3 row_mask:0xf bank_mask:0xf\n"
"v_cvt_pk_bf16_f32 v32, v32, v33\n"
"v_cvt_pk_bf16_f32 v33, v34, v35\n"
"v_cvt_pk_bf16_f32 v34, v36, v37\n"
"v_cvt_pk_bf16_f32 v35, v38, v39\n"
"s_waitcnt lgkmcnt(10)\n"
"v_mfma_f32_16x16x32_bf16 v[124:127], v[208:211], a[0:3], v[180:183]\n"
"s_waitcnt lgkmcnt(8)\n"
"ds_read_b64_tr_b16 a[94:95], v247 offset:51208\n"
"ds_write_b128 v246, v[32:35] offset:50176\n"
"v_mul_f32_dpp v64, v245, v64 row_newbcast:0 row_mask:0xf bank_mask:0xf\n"
"v_mul_f32_dpp v65, v245, v65 row_newbcast:1 row_mask:0xf bank_mask:0xf\n"
"v_mul_f32_dpp v66, v245, v66 row_newbcast:2 row_mask:0xf bank_mask:0xf\n"
"v_mul_f32_dpp v67, v245, v67 row_newbcast:3 row_mask:0xf bank_mask:0xf\n"
"v_mul_f32_dpp v68, v245, v68 row_newbcast:0 row_mask:0xf bank_mask:0xf\n"
"v_mul_f32_dpp v69, v245, v69 row_newbcast:1 row_mask:0xf bank_mask:0xf\n"
"v_mul_f32_dpp v70, v245, v70 row_newbcast:2 row_mask:0xf bank_mask:0xf\n"
"v_mul_f32_dpp v71, v245, v71 row_newbcast:3 row_mask:0xf bank_mask:0xf\n"
"v_cvt_pk_bf16_f32 v64, v64, v65\n"
"v_cvt_pk_bf16_f32 v65, v66, v67\n"
"v_cvt_pk_bf16_f32 v66, v68, v69\n"
"v_cvt_pk_bf16_f32 v67, v70, v71\n"
"v_mfma_f32_16x16x32_bf16 v[124:127], v[212:215], a[4:7], v[124:127]\n"
"s_waitcnt lgkmcnt(8)\n"
"ds_write_b128 v246, v[64:67] offset:51200\n"
"v_mfma_f32_16x16x32_bf16 v[124:127], v[216:219], a[8:11], v[124:127]\n"
"s_waitcnt lgkmcnt(6)\n"
"ds_read_b64_tr_b16 a[48:49], v247 offset:49152\n"
"v_exp_f32 v128, v128\n"
"v_exp_f32 v129, v129\n"
"v_exp_f32 v130, v130\n"
"v_exp_f32 v131, v131\n"
"v_mfma_f32_16x16x32_bf16 v[124:127], v[220:223], a[12:15], v[124:127]\n"
"ds_read_b64_tr_b16 a[50:51], v247 offset:49160\n"
"ds_read_b64_tr_b16 a[64:65], v247 offset:50176\n"
"v_cvt_pk_bf16_f32 v156, v128, v129\n"
"v_cvt_pk_bf16_f32 v157, v130, v131\n"
"v_mfma_f32_16x16x32_bf16 v[128:131], v[216:219], a[24:27], v[184:187]\n"
"ds_read_b64_tr_b16 a[66:67], v247 offset:50184\n"
"ds_read_b64_tr_b16 a[80:81], v247 offset:51200\n"
"v_mfma_f32_16x16x32_bf16 v[128:131], v[220:223], a[28:31], v[128:131]\n"
"ds_read_b64_tr_b16 a[82:83], v247 offset:51208\n"
"ds_read_b64 v[250:251], v255 offset:52224\n"
"v_xor_b32 v254, 0x1000, v254\n"
"v_mfma_f32_16x16x32_bf16 v[128:131], v[208:211], a[16:19], v[128:131]\n"
"ds_read_b128 v[24:27], v254 offset:32768\n"
"v_exp_f32 v132, v132\n"
"v_exp_f32 v133, v133\n"
"v_exp_f32 v134, v134\n"
"v_exp_f32 v135, v135\n"
"v_mfma_f32_16x16x32_bf16 v[128:131], v[212:215], a[20:23], v[128:131]\n"
"ds_read_b128 v[28:31], v254 offset:33792\n"
"v_cvt_pk_bf16_f32 v164, v132, v133\n"
"v_cvt_pk_bf16_f32 v165, v134, v135\n"
"v_mfma_f32_16x16x32_bf16 v[132:135], v[208:211], a[32:35], v[188:191]\n"
"ds_read_b128 v[56:59], v254 offset:34816\n"
"v_mov_b32 v196, 0\n"
"v_mfma_f32_16x16x32_bf16 v[132:135], v[212:215], a[36:39], v[132:135]\n"
"ds_read_b128 v[60:63], v254 offset:35840\n"
"v_mov_b32 v197, 0\n"
"v_mfma_f32_16x16x32_bf16 v[132:135], v[216:219], a[40:43], v[132:135]\n"
"ds_read_b64_tr_b16 a[244:245], v234 offset:0\n"
"ds_read_b64_tr_b16 a[246:247], v234 offset:4096\n"
"v_exp_f32 v136, v136\n"
"v_exp_f32 v137, v137\n"
"v_exp_f32 v138, v138\n"
"v_exp_f32 v139, v139\n"
"v_mfma_f32_16x16x32_bf16 v[132:135], v[220:223], a[44:47], v[132:135]\n"
"v_cvt_pk_bf16_f32 v172, v136, v137\n"
"v_cvt_pk_bf16_f32 v173, v138, v139\n"
"v_mfma_f32_16x16x32_bf16 v[136:139], v[216:219], a[200:203], v[192:195]\n"
"ds_read_b64_tr_b16 a[248:249], v238 offset:0\n"
"ds_read_b64_tr_b16 a[250:251], v238 offset:4096\n"
"v_mfma_f32_16x16x32_bf16 v[136:139], v[220:223], a[204:207], v[136:139]\n"
"v_exp_f32 v140, v140\n"
"v_mfma_f32_16x16x32_bf16 v[136:139], v[208:211], a[192:195], v[136:139]\n"
"v_exp_f32 v141, v141\n"
"v_mfma_f32_16x16x32_bf16 v[136:139], v[212:215], a[196:199], v[136:139]\n"
"s_waitcnt lgkmcnt(2)\n"
"v_exp_f32 v142, v142\n"
"v_exp_f32 v143, v143\n"
"v_mfma_f32_16x16x32_bf16 a[216:219], v[176:179], a[244:247], 0\n"
"s_add_u32 m0, s44, 98304\n"
"ds_read_b64_tr_b16 v[224:225], v239 offset:0\n"
"ds_read_b64_tr_b16 v[226:227], v239 offset:4096\n"
"v_cvt_pk_bf16_f32 v158, v140, v141\n"
"v_cvt_pk_bf16_f32 v159, v142, v143\n"
"v_mfma_f32_16x16x32_bf16 v[8:11], v[152:155], a[244:247], 0\n"
"s_waitcnt lgkmcnt(2)\n"
"buffer_load_dwordx4 v240, s[20:23], s56 offen nt lds\n"
"v_exp_f32 v144, v144\n"
"v_exp_f32 v145, v145\n"
"v_exp_f32 v146, v146\n"
"v_exp_f32 v147, v147\n"
"v_mfma_f32_16x16x32_bf16 v[8:11], v[156:159], a[248:251], v[8:11]\n"
"ds_read_b64_tr_b16 a[252:253], v235 offset:0\n"
"ds_read_b64_tr_b16 a[254:255], v235 offset:4096\n"
"v_cvt_pk_bf16_f32 v166, v144, v145\n"
"v_cvt_pk_bf16_f32 v167, v146, v147\n"
"v_mfma_f32_16x16x32_bf16 v[40:43], v[160:163], a[244:247], 0\n"
"v_exp_f32 v148, v148\n"
"v_exp_f32 v149, v149\n"
"v_exp_f32 v150, v150\n"
"v_exp_f32 v151, v151\n"
"v_mfma_f32_16x16x32_bf16 v[40:43], v[164:167], a[248:251], v[40:43]\n"
"ds_read_b128 v[216:219], v228 offset:21504\n"
"v_cvt_pk_bf16_f32 v174, v148, v149\n"
"v_cvt_pk_bf16_f32 v175, v150, v151\n"
"v_mfma_f32_16x16x32_bf16 v[72:75], v[168:171], a[244:247], 0\n"
"ds_read_b128 v[220:223], v229 offset:21504\n"
"v_mov_b32 v198, 0\n"
"v_mfma_f32_16x16x32_bf16 v[72:75], v[172:175], a[248:251], v[72:75]\n"
"s_waitcnt lgkmcnt(4)\n"
"ds_read_b128 v[208:211], v228 offset:20480\n"
"v_mov_b32 v199, 0\n"
"v_mfma_f32_16x16x32_bf16 v[76:79], v[172:175], v[224:227], 0\n"
"s_waitcnt lgkmcnt(3)\n"
"ds_read_b128 v[212:215], v229 offset:20480\n"
"v_mov_b32 v202, 0\n"
"v_mfma_f32_16x16x32_bf16 v[76:79], v[168:171], a[252:255], v[76:79]\n"
"ds_read_b64_tr_b16 a[244:245], v234 offset:1024\n"
"v_mov_b32 v203, 0\n"
"v_mfma_f32_16x16x32_bf16 v[44:47], v[164:167], v[224:227], 0\n"
"ds_read_b64_tr_b16 a[246:247], v234 offset:5120\n"
"v_min_f32 v249, v252, v250\n"
"v_mfma_f32_16x16x32_bf16 v[44:47], v[160:163], a[252:255], v[44:47]\n"
"ds_read_b64_tr_b16 a[248:249], v238 offset:1024\n"
"v_sub_f32 v252, v249, v252\n"
"v_mfma_f32_16x16x32_bf16 v[12:15], v[156:159], v[224:227], 0\n"
"s_add_u32 s40, s56, s52\n"
"buffer_load_dwordx4 v240, s[20:23], s40 offen offset:1024 nt lds\n"
"v_mfma_f32_16x16x32_bf16 v[12:15], v[152:155], a[252:255], v[12:15]\n"
"v_exp_f32 v252, v252\n"
"v_mfma_f32_16x16x32_bf16 a[220:223], v[176:179], a[252:255], 0\n"
"ds_read_b64_tr_b16 a[250:251], v238 offset:5120\n"
"v_sub_f32 v250, v249, v250\n"
"v_smfmac_f32_16x16x64_bf16 v[196:199], a[240:243], v[152:159], v241\n"
"v_exp_f32 v250, v250\n"
"v_mfma_f32_16x16x32_bf16 v[202:205], a[240:243], v[176:179], v[202:205]\n"
"s_waitcnt lgkmcnt(1)\n"
"ds_read_b64_tr_b16 v[224:225], v239 offset:1024\n"
"v_mul_f32 v253, v253, v252\n"
"v_mfma_f32_16x16x32_bf16 v[140:143], v[216:219], a[8:11], v[180:183]\n"
"ds_read_b64_tr_b16 v[226:227], v239 offset:5120\n"
"v_fmac_f32 v253, v251, v250\n"
"v_mfma_f32_16x16x32_bf16 v[140:143], v[220:223], a[12:15], v[140:143]\n"
"v_rcp_f32 v253, v253\n"
"v_mfma_f32_16x16x32_bf16 v[140:143], v[208:211], a[0:3], v[140:143]\n"
"ds_read_b64_tr_b16 a[252:253], v235 offset:1024\n"
"v_mul_f32 v250, v250, v253\n"
"v_mfma_f32_16x16x32_bf16 v[140:143], v[212:215], a[4:7], v[140:143]\n"
"ds_read_b64_tr_b16 a[254:255], v235 offset:5120\n"
"v_mul_f32 v252, v252, v253\n"
"v_mfma_f32_16x16x32_bf16 v[144:147], v[208:211], a[16:19], v[184:187]\n"
"v_mov_b32_dpp v250, v250 row_ror:12 row_mask:0x2 bank_mask:0xf\n"
"v_mov_b32_dpp v252, v252 row_ror:12 row_mask:0x2 bank_mask:0xf\n"
"v_mfma_f32_16x16x32_bf16 v[144:147], v[212:215], a[20:23], v[144:147]\n"
"s_add_u32 s40, s56, s53\n"
"buffer_load_dwordx4 v240, s[20:23], s40 offen offset:2048 nt lds\n"
"v_mfma_f32_16x16x32_bf16 v[144:147], v[216:219], a[24:27], v[144:147]\n"
"v_exp_f32 v124, v124\n"
"v_mfma_f32_16x16x32_bf16 v[144:147], v[220:223], a[28:31], v[144:147]\n"
"v_exp_f32 v125, v125\n"
"v_mfma_f32_16x16x32_bf16 v[148:151], v[216:219], a[40:43], v[188:191]\n"
"v_exp_f32 v126, v126\n"
"v_mfma_f32_16x16x32_bf16 v[148:151], v[220:223], a[44:47], v[148:151]\n"
"v_mov_b32_dpp v250, v250 row_ror:8 row_mask:0x4 bank_mask:0xf\n"
"v_mov_b32_dpp v252, v252 row_ror:8 row_mask:0x4 bank_mask:0xf\n"
"v_mfma_f32_16x16x32_bf16 v[148:151], v[208:211], a[32:35], v[148:151]\n"
"v_mov_b32_dpp v250, v250 row_ror:4 row_mask:0x8 bank_mask:0xf\n"
"v_exp_f32 v127, v127\n"
"v_mfma_f32_16x16x32_bf16 v[148:151], v[212:215], a[36:39], v[148:151]\n"
"v_mov_b32_dpp v252, v252 row_ror:4 row_mask:0x8 bank_mask:0xf\n"
"v_accvgpr_read_b32 v249, a208\n"
"v_mul_f32_dpp v24, v250, v24 row_newbcast:0 row_mask:0xf bank_mask:0xf\n"
"v_accvgpr_read_b32 v206, a209\n"
"v_mul_f32_dpp v25, v250, v25 row_newbcast:1 row_mask:0xf bank_mask:0xf\n"
"v_accvgpr_read_b32 v253, a210\n"
"v_mul_f32_dpp v26, v250, v26 row_newbcast:2 row_mask:0xf bank_mask:0xf\n"
"v_accvgpr_read_b32 v251, a211\n"
"v_mul_f32_dpp v27, v250, v27 row_newbcast:3 row_mask:0xf bank_mask:0xf\n"
"v_cvt_pk_bf16_f32 v96, v124, v125\n"
"v_cvt_pk_bf16_f32 v97, v126, v127\n"
"v_mfma_f32_16x16x32_bf16 v[124:127], v[208:211], a[192:195], v[192:195]\n"
"ds_read_b128 v[208:211], v230 offset:16384\n"
"v_fmac_f32_dpp v24, v252, v249 row_newbcast:0 row_mask:0xf bank_mask:0xf\n"
"v_fmac_f32_dpp v25, v252, v206 row_newbcast:1 row_mask:0xf bank_mask:0xf\n"
"v_fmac_f32_dpp v26, v252, v253 row_newbcast:2 row_mask:0xf bank_mask:0xf\n"
"v_fmac_f32_dpp v27, v252, v251 row_newbcast:3 row_mask:0xf bank_mask:0xf\n"
"v_accvgpr_read_b32 v249, a212\n"
"v_mul_f32_dpp v28, v250, v28 row_newbcast:0 row_mask:0xf bank_mask:0xf\n"
"v_accvgpr_read_b32 v206, a213\n"
"v_mul_f32_dpp v29, v250, v29 row_newbcast:1 row_mask:0xf bank_mask:0xf\n"
"v_accvgpr_read_b32 v253, a214\n"
"v_mul_f32_dpp v30, v250, v30 row_newbcast:2 row_mask:0xf bank_mask:0xf\n"
"v_accvgpr_read_b32 v251, a215\n"
"v_mul_f32_dpp v31, v250, v31 row_newbcast:3 row_mask:0xf bank_mask:0xf\n"
"v_mfma_f32_16x16x32_bf16 v[124:127], v[212:215], a[196:199], v[124:127]\n"
"ds_read_b128 v[212:215], v231 offset:16384\n"
"v_fmac_f32_dpp v28, v252, v249 row_newbcast:0 row_mask:0xf bank_mask:0xf\n"
"v_fmac_f32_dpp v29, v252, v206 row_newbcast:1 row_mask:0xf bank_mask:0xf\n"
"v_fmac_f32_dpp v30, v252, v253 row_newbcast:2 row_mask:0xf bank_mask:0xf\n"
"v_fmac_f32_dpp v31, v252, v251 row_newbcast:3 row_mask:0xf bank_mask:0xf\n"
"v_cvt_pk_bf16_f32 v24, v24, v25\n"
"v_cvt_pk_bf16_f32 v25, v26, v27\n"
"v_cvt_pk_bf16_f32 v26, v28, v29\n"
"v_cvt_pk_bf16_f32 v27, v30, v31\n"
"v_bfe_u32 v207, v242, 8, 2\n"
"v_lshlrev_b32 v207, 6, v207\n"
"v_xor_b32 v207, v207, v248\n"
"v_mfma_f32_16x16x32_bf16 v[124:127], v[216:219], a[200:203], v[124:127]\n"
"ds_write_b128 v207, v[24:27] offset:0\n"
"v_accvgpr_read_b32 v249, a224\n"
"v_mul_f32_dpp v56, v250, v56 row_newbcast:0 row_mask:0xf bank_mask:0xf\n"
"v_accvgpr_read_b32 v206, a225\n"
"v_mul_f32_dpp v57, v250, v57 row_newbcast:1 row_mask:0xf bank_mask:0xf\n"
"v_accvgpr_read_b32 v253, a226\n"
"v_mul_f32_dpp v58, v250, v58 row_newbcast:2 row_mask:0xf bank_mask:0xf\n"
"v_accvgpr_read_b32 v251, a227\n"
"v_mul_f32_dpp v59, v250, v59 row_newbcast:3 row_mask:0xf bank_mask:0xf\n"
"v_mfma_f32_16x16x32_bf16 v[124:127], v[220:223], a[204:207], v[124:127]\n"
"ds_read_b128 v[216:219], v230 offset:17408\n"
"v_fmac_f32_dpp v56, v252, v249 row_newbcast:0 row_mask:0xf bank_mask:0xf\n"
"v_fmac_f32_dpp v57, v252, v206 row_newbcast:1 row_mask:0xf bank_mask:0xf\n"
"v_fmac_f32_dpp v58, v252, v253 row_newbcast:2 row_mask:0xf bank_mask:0xf\n"
"v_fmac_f32_dpp v59, v252, v251 row_newbcast:3 row_mask:0xf bank_mask:0xf\n"
"v_accvgpr_read_b32 v249, a228\n"
"v_mul_f32_dpp v60, v250, v60 row_newbcast:0 row_mask:0xf bank_mask:0xf\n"
"v_accvgpr_read_b32 v206, a229\n"
"v_mul_f32_dpp v61, v250, v61 row_newbcast:1 row_mask:0xf bank_mask:0xf\n"
"v_accvgpr_read_b32 v253, a230\n"
"v_mul_f32_dpp v62, v250, v62 row_newbcast:2 row_mask:0xf bank_mask:0xf\n"
"v_accvgpr_read_b32 v251, a231\n"
"v_mul_f32_dpp v63, v250, v63 row_newbcast:3 row_mask:0xf bank_mask:0xf\n"
"v_mfma_f32_16x16x32_bf16 a[232:235], v[176:179], a[244:247], 0\n"
"s_add_u32 s40, s56, s54\n"
"buffer_load_dwordx4 v240, s[20:23], s40 offen offset:3072 nt lds\n"
"v_fmac_f32_dpp v60, v252, v249 row_newbcast:0 row_mask:0xf bank_mask:0xf\n"
"v_fmac_f32_dpp v61, v252, v206 row_newbcast:1 row_mask:0xf bank_mask:0xf\n"
"v_fmac_f32_dpp v62, v252, v253 row_newbcast:2 row_mask:0xf bank_mask:0xf\n"
"v_fmac_f32_dpp v63, v252, v251 row_newbcast:3 row_mask:0xf bank_mask:0xf\n"
"v_cvt_pk_bf16_f32 v56, v56, v57\n"
"v_cvt_pk_bf16_f32 v57, v58, v59\n"
"v_cvt_pk_bf16_f32 v58, v60, v61\n"
"v_cvt_pk_bf16_f32 v59, v62, v63\n"
"v_mfma_f32_16x16x32_bf16 v[24:27], v[152:155], a[244:247], 0\n"
"s_waitcnt lgkmcnt(3)\n"
"s_add_u32 s55, s55, s59\n"
"ds_write_b128 v207, v[56:59] offset:2048\n"
"v_mfma_f32_16x16x32_bf16 v[24:27], v[156:159], a[248:251], v[24:27]\n"
"s_add_u32 s41, s55, s57\n"
"v_exp_f32 v128, v128\n"
"v_mfma_f32_16x16x32_bf16 v[56:59], v[160:163], a[244:247], 0\n"
"s_and_b32 s41, s41, s58\n"
"ds_read_b128 v[220:223], v231 offset:17408\n"
"v_mov_b32 v200, 0\n"
"v_mfma_f32_16x16x32_bf16 v[56:59], v[164:167], a[248:251], v[56:59]\n"
"s_andn2_b32 s56, s55, s58\n"
"v_exp_f32 v129, v129\n"
"v_mfma_f32_16x16x32_bf16 v[88:91], v[168:171], a[244:247], 0\n"
"s_or_b32 s56, s56, s41\n"
"ds_read_b64_tr_b16 a[244:245], v232 offset:0\n"
"v_mov_b32 v201, 0\n"
"v_mfma_f32_16x16x32_bf16 v[88:91], v[172:175], a[248:251], v[88:91]\n"
"v_exp_f32 v130, v130\n"
"v_mfma_f32_16x16x32_bf16 v[92:95], v[172:175], v[224:227], 0\n"
"v_exp_f32 v131, v131\n"
"v_mfma_f32_16x16x32_bf16 v[92:95], v[168:171], a[252:255], v[92:95]\n"
"ds_read_b64_tr_b16 a[246:247], v232 offset:4096\n"
"v_cvt_pk_bf16_f32 v104, v128, v129\n"
"v_mfma_f32_16x16x32_bf16 v[60:63], v[164:167], v[224:227], 0\n"
"s_add_u32 m0, s44, 49152\n"
"v_exp_f32 v132, v132\n"
"v_mfma_f32_16x16x32_bf16 v[60:63], v[160:163], a[252:255], v[60:63]\n"
"buffer_load_dwordx4 v240, s[16:19], s56 offen nt lds\n"
"v_mfma_f32_16x16x32_bf16 v[28:31], v[156:159], v[224:227], 0\n"
"v_exp_f32 v133, v133\n"
"v_mfma_f32_16x16x32_bf16 v[28:31], v[152:155], a[252:255], v[28:31]\n"
"v_exp_f32 v134, v134\n"
"v_mfma_f32_16x16x32_bf16 a[236:239], v[176:179], a[252:255], 0\n"
"ds_read_b64_tr_b16 a[248:249], v236 offset:0\n"
"v_cvt_pk_bf16_f32 v105, v130, v131\n"
"v_smfmac_f32_16x16x64_bf16 v[198:201], a[240:243], v[160:167], v241\n"
"v_exp_f32 v135, v135\n"
"v_mfma_f32_16x16x32_bf16 v[128:131], v[208:211], a[0:3], v[180:183]\n"
"s_waitcnt lgkmcnt(1)\n"
"v_cvt_pk_bf16_f32 v112, v132, v133\n"
"v_cvt_pk_bf16_f32 v113, v134, v135\n"
"v_mfma_f32_16x16x32_bf16 v[128:131], v[212:215], a[4:7], v[128:131]\n"
"v_exp_f32 v136, v136\n"
"v_mfma_f32_16x16x32_bf16 v[128:131], v[216:219], a[8:11], v[128:131]\n"
"v_exp_f32 v137, v137\n"
"v_mfma_f32_16x16x32_bf16 v[128:131], v[220:223], a[12:15], v[128:131]\n"
"v_exp_f32 v138, v138\n"
"v_mfma_f32_16x16x32_bf16 v[132:135], v[216:219], a[24:27], v[184:187]\n"
"ds_read_b64_tr_b16 a[250:251], v236 offset:4096\n"
"v_cvt_pk_bf16_f32 v120, v136, v137\n"
"v_mfma_f32_16x16x32_bf16 v[132:135], v[220:223], a[28:31], v[132:135]\n"
"s_add_u32 s40, s56, s52\n"
"buffer_load_dwordx4 v240, s[16:19], s40 offen offset:1024 nt lds\n"
"v_mfma_f32_16x16x32_bf16 v[132:135], v[208:211], a[16:19], v[132:135]\n"
"v_exp_f32 v139, v139\n"
"v_mfma_f32_16x16x32_bf16 v[132:135], v[212:215], a[20:23], v[132:135]\n"
"ds_read_b64_tr_b16 v[224:225], v237 offset:0\n"
"v_cvt_pk_bf16_f32 v121, v138, v139\n"
"v_mfma_f32_16x16x32_bf16 v[136:139], v[208:211], a[32:35], v[188:191]\n"
"ds_read_b64_tr_b16 v[226:227], v237 offset:4096\n"
"v_xor_b32 v246, s75, v246\n"
"v_mfma_f32_16x16x32_bf16 v[136:139], v[212:215], a[36:39], v[136:139]\n"
"ds_read_b64_tr_b16 a[252:253], v233 offset:0\n"
"v_mul_f32_dpp v16, v243, v16 row_newbcast:0 row_mask:0xf bank_mask:0xf\n"
"v_mfma_f32_16x16x32_bf16 v[136:139], v[216:219], a[40:43], v[136:139]\n"
"ds_read_b64_tr_b16 a[254:255], v233 offset:4096\n"
"v_mul_f32_dpp v17, v243, v17 row_newbcast:1 row_mask:0xf bank_mask:0xf\n"
"v_mfma_f32_16x16x32_bf16 v[136:139], v[220:223], a[44:47], v[136:139]\n"
"ds_read_b128 v[208:211], v230 offset:20480\n"
"v_mul_f32_dpp v18, v243, v18 row_newbcast:2 row_mask:0xf bank_mask:0xf\n"
"v_mfma_f32_16x16x32_bf16 a[208:211], v[176:179], a[244:247], 0\n"
"ds_read_b128 v[212:215], v231 offset:20480\n"
"v_mul_f32_dpp v19, v243, v19 row_newbcast:3 row_mask:0xf bank_mask:0xf\n"
"v_mfma_f32_16x16x32_bf16 v[0:3], v[152:155], a[244:247], 0\n"
"s_waitcnt lgkmcnt(4)\n"
"v_exp_f32 v140, v140\n"
"v_mfma_f32_16x16x32_bf16 v[0:3], v[156:159], a[248:251], v[0:3]\n"
"ds_read_b128 v[216:219], v230 offset:21504\n"
"v_mul_f32_dpp v20, v243, v20 row_newbcast:0 row_mask:0xf bank_mask:0xf\n"
"v_mfma_f32_16x16x32_bf16 v[32:35], v[160:163], a[244:247], 0\n"
"v_exp_f32 v141, v141\n"
"v_mfma_f32_16x16x32_bf16 v[32:35], v[164:167], a[248:251], v[32:35]\n"
"s_add_u32 s40, s56, s53\n"
"buffer_load_dwordx4 v240, s[16:19], s40 offen offset:2048 nt lds\n"
"v_mfma_f32_16x16x32_bf16 v[64:67], v[168:171], a[244:247], 0\n"
"v_exp_f32 v142, v142\n"
"v_mfma_f32_16x16x32_bf16 v[64:67], v[172:175], a[248:251], v[64:67]\n"
"v_exp_f32 v143, v143\n"
"v_mfma_f32_16x16x32_bf16 v[68:71], v[172:175], v[224:227], 0\n"
"s_waitcnt lgkmcnt(0)\n"
"v_exp_f32 v144, v144\n"
"v_mfma_f32_16x16x32_bf16 v[68:71], v[168:171], a[252:255], v[68:71]\n"
"v_mul_f32_dpp v21, v243, v21 row_newbcast:1 row_mask:0xf bank_mask:0xf\n"
"v_mul_f32_dpp v22, v243, v22 row_newbcast:2 row_mask:0xf bank_mask:0xf\n"
"v_mfma_f32_16x16x32_bf16 v[36:39], v[164:167], v[224:227], 0\n"
"v_mul_f32_dpp v23, v243, v23 row_newbcast:3 row_mask:0xf bank_mask:0xf\n"
"v_cvt_pk_bf16_f32 v16, v16, v17\n"
"v_mfma_f32_16x16x32_bf16 v[36:39], v[160:163], a[252:255], v[36:39]\n"
"v_cvt_pk_bf16_f32 v17, v18, v19\n"
"v_cvt_pk_bf16_f32 v18, v20, v21\n"
"v_mfma_f32_16x16x32_bf16 v[4:7], v[156:159], v[224:227], 0\n"
"v_cvt_pk_bf16_f32 v19, v22, v23\n"
"v_cvt_pk_bf16_f32 v98, v140, v141\n"
"v_mfma_f32_16x16x32_bf16 v[4:7], v[152:155], a[252:255], v[4:7]\n"
"ds_read_b128 v[220:223], v231 offset:21504\n"
"v_cvt_pk_bf16_f32 v99, v142, v143\n"
"v_mfma_f32_16x16x32_bf16 a[212:215], v[176:179], a[252:255], 0\n"
"v_mul_f32_dpp v48, v244, v48 row_newbcast:0 row_mask:0xf bank_mask:0xf\n"
"v_mul_f32_dpp v49, v244, v49 row_newbcast:1 row_mask:0xf bank_mask:0xf\n"
"v_smfmac_f32_16x16x64_bf16 v[200:203], a[240:243], v[168:175], v241\n"
"s_add_u32 s40, s56, s54\n"
"buffer_load_dwordx4 v240, s[16:19], s40 offen offset:3072 nt lds\n"
"v_mfma_f32_16x16x32_bf16 v[140:143], v[208:211], a[0:3], v[180:183]\n"
"ds_write_b128 v246, v[16:19] offset:49152\n"
"v_mul_f32_dpp v50, v244, v50 row_newbcast:2 row_mask:0xf bank_mask:0xf\n"
"v_mul_f32_dpp v51, v244, v51 row_newbcast:3 row_mask:0xf bank_mask:0xf\n"
"v_mul_f32_dpp v52, v244, v52 row_newbcast:0 row_mask:0xf bank_mask:0xf\n"
"v_mul_f32_dpp v53, v244, v53 row_newbcast:1 row_mask:0xf bank_mask:0xf\n"
"v_mul_f32_dpp v54, v244, v54 row_newbcast:2 row_mask:0xf bank_mask:0xf\n"
"v_mul_f32_dpp v55, v244, v55 row_newbcast:3 row_mask:0xf bank_mask:0xf\n"
"v_cvt_pk_bf16_f32 v48, v48, v49\n"
"v_cvt_pk_bf16_f32 v49, v50, v51\n"
"v_cvt_pk_bf16_f32 v50, v52, v53\n"
"v_cvt_pk_bf16_f32 v51, v54, v55\n"
"v_mfma_f32_16x16x32_bf16 v[140:143], v[212:215], a[4:7], v[140:143]\n"
"ds_write_b128 v246, v[48:51] offset:50176\n"
"v_mul_f32_dpp v80, v245, v80 row_newbcast:0 row_mask:0xf bank_mask:0xf\n"
"v_mul_f32_dpp v81, v245, v81 row_newbcast:1 row_mask:0xf bank_mask:0xf\n"
"v_mul_f32_dpp v82, v245, v82 row_newbcast:2 row_mask:0xf bank_mask:0xf\n"
"v_mul_f32_dpp v83, v245, v83 row_newbcast:3 row_mask:0xf bank_mask:0xf\n"
"v_mul_f32_dpp v84, v245, v84 row_newbcast:0 row_mask:0xf bank_mask:0xf\n"
"v_mul_f32_dpp v85, v245, v85 row_newbcast:1 row_mask:0xf bank_mask:0xf\n"
"v_mul_f32_dpp v86, v245, v86 row_newbcast:2 row_mask:0xf bank_mask:0xf\n"
"v_mul_f32_dpp v87, v245, v87 row_newbcast:3 row_mask:0xf bank_mask:0xf\n"
"v_cvt_pk_bf16_f32 v80, v80, v81\n"
"v_cvt_pk_bf16_f32 v81, v82, v83\n"
"v_cvt_pk_bf16_f32 v82, v84, v85\n"
"v_cvt_pk_bf16_f32 v83, v86, v87\n"
"v_mfma_f32_16x16x32_bf16 v[140:143], v[216:219], a[8:11], v[140:143]\n"
"s_waitcnt lgkmcnt(2)\n"
"ds_write_b128 v246, v[80:83] offset:51200\n"
"v_xor_b32 v247, s75, v247\n"
"v_exp_f32 v145, v145\n"
"v_exp_f32 v146, v146\n"
"v_exp_f32 v147, v147\n"
"v_mfma_f32_16x16x32_bf16 v[140:143], v[220:223], a[12:15], v[140:143]\n"
"ds_read_b64_tr_b16 a[56:57], v247 offset:49152\n"
"ds_read_b64_tr_b16 a[58:59], v247 offset:49160\n"
"v_cvt_pk_bf16_f32 v106, v144, v145\n"
"v_cvt_pk_bf16_f32 v107, v146, v147\n"
"v_mfma_f32_16x16x32_bf16 v[144:147], v[216:219], a[24:27], v[184:187]\n"
"ds_read_b64_tr_b16 a[72:73], v247 offset:50176\n"
"ds_read_b64_tr_b16 a[74:75], v247 offset:50184\n"
"v_mfma_f32_16x16x32_bf16 v[144:147], v[220:223], a[28:31], v[144:147]\n"
"ds_read_b64_tr_b16 a[88:89], v247 offset:51200\n"
"ds_read_b64_tr_b16 a[90:91], v247 offset:51208\n"
"v_mfma_f32_16x16x32_bf16 v[144:147], v[208:211], a[16:19], v[144:147]\n"
"ds_read_b64_tr_b16 a[244:245], v232 offset:1024\n"
"ds_read_b64_tr_b16 a[246:247], v232 offset:5120\n"
"v_exp_f32 v148, v148\n"
"v_exp_f32 v149, v149\n"
"v_exp_f32 v150, v150\n"
"v_exp_f32 v151, v151\n"
"v_mfma_f32_16x16x32_bf16 v[144:147], v[212:215], a[20:23], v[144:147]\n"
"v_cvt_pk_bf16_f32 v114, v148, v149\n"
"v_cvt_pk_bf16_f32 v115, v150, v151\n"
"v_mfma_f32_16x16x32_bf16 v[148:151], v[208:211], a[32:35], v[188:191]\n"
"ds_read_b64_tr_b16 a[248:249], v236 offset:1024\n"
"ds_read_b64_tr_b16 a[250:251], v236 offset:5120\n"
"v_mfma_f32_16x16x32_bf16 v[148:151], v[212:215], a[36:39], v[148:151]\n"
"s_cbranch_vccnz LPCT1_%=\nLPKT1_%=:\n"
"ds_read_b64_tr_b16 v[224:225], v237 offset:1024\n"
"v_mfma_f32_16x16x32_bf16 v[148:151], v[216:219], a[40:43], v[148:151]\n"
"v_exp_f32 v124, v124\n"
"v_mfma_f32_16x16x32_bf16 v[148:151], v[220:223], a[44:47], v[148:151]\n"
"s_waitcnt lgkmcnt(3)\n"
"v_exp_f32 v125, v125\n"
"v_mfma_f32_16x16x32_bf16 a[224:227], v[176:179], a[244:247], 0\n"
"s_waitcnt vmcnt(8)\n"
"v_exp_f32 v126, v126\n"
"v_mfma_f32_16x16x32_bf16 v[16:19], v[152:155], a[244:247], 0\n"
"s_barrier\n"
"ds_read_b64_tr_b16 v[226:227], v237 offset:5120\n"
"s_waitcnt lgkmcnt(2)\n"
"v_mfma_f32_16x16x32_bf16 v[16:19], v[156:159], a[248:251], v[16:19]\n"
"ds_read_b64_tr_b16 a[252:253], v233 offset:1024\n"
"ds_read_b64_tr_b16 a[254:255], v233 offset:5120\n"
"v_mfma_f32_16x16x32_bf16 v[48:51], v[160:163], a[244:247], 0\n"
"v_exp_f32 v127, v127\n"
"v_mfma_f32_16x16x32_bf16 v[48:51], v[164:167], a[248:251], v[48:51]\n"
"v_exp_f32 v128, v128\n"
"v_mfma_f32_16x16x32_bf16 v[80:83], v[168:171], a[244:247], 0\n"
"v_exp_f32 v129, v129\n"
"v_mfma_f32_16x16x32_bf16 v[80:83], v[172:175], a[248:251], v[80:83]\n"
"s_waitcnt lgkmcnt(2)\n"
"v_exp_f32 v130, v130\n"
"v_mfma_f32_16x16x32_bf16 v[84:87], v[172:175], v[224:227], 0\n"
"s_waitcnt lgkmcnt(0)\n"
"v_exp_f32 v131, v131\n"
"v_mfma_f32_16x16x32_bf16 v[84:87], v[168:171], a[252:255], v[84:87]\n"
"ds_read_b128 v[208:211], v228 offset:32768\n"
"v_xor_b32 v246, s75, v246\n"
"v_mfma_f32_16x16x32_bf16 v[52:55], v[164:167], v[224:227], 0\n"
"ds_read_b128 v[212:215], v229 offset:32768\n"
"v_xor_b32 v247, s75, v247\n"
"v_mfma_f32_16x16x32_bf16 v[52:55], v[160:163], a[252:255], v[52:55]\n"
"ds_read_b128 v[216:219], v228 offset:33792\n"
"v_cvt_pk_bf16_f32 v122, v124, v125\n"
"v_mfma_f32_16x16x32_bf16 v[20:23], v[156:159], v[224:227], 0\n"
"ds_read_b128 v[220:223], v229 offset:33792\n"
"v_cvt_pk_bf16_f32 v123, v126, v127\n"
"v_mfma_f32_16x16x32_bf16 v[20:23], v[152:155], a[252:255], v[20:23]\n"
"ds_read_b64_tr_b16 a[244:245], v234 offset:16384\n"
"v_exp_f32 v132, v132\n"
"v_exp_f32 v133, v133\n"
"v_exp_f32 v134, v134\n"
"v_exp_f32 v135, v135\n"
"v_exp_f32 v136, v136\n"
"v_exp_f32 v137, v137\n"
"v_exp_f32 v138, v138\n"
"v_exp_f32 v139, v139\n"
"v_exp_f32 v140, v140\n"
"v_exp_f32 v141, v141\n"
"v_exp_f32 v142, v142\n"
"v_exp_f32 v143, v143\n"
"v_exp_f32 v144, v144\n"
"v_exp_f32 v145, v145\n"
"v_exp_f32 v146, v146\n"
"v_exp_f32 v147, v147\n"
"v_exp_f32 v148, v148\n"
"v_exp_f32 v149, v149\n"
"v_mfma_f32_16x16x32_bf16 a[228:231], v[176:179], a[252:255], 0\n"
"ds_read_b64_tr_b16 a[246:247], v234 offset:20480\n"
"v_cvt_pk_bf16_f32 v100, v128, v129\n"
"v_cvt_pk_bf16_f32 v101, v130, v131\n"
"v_cvt_pk_bf16_f32 v108, v132, v133\n"
"v_cvt_pk_bf16_f32 v109, v134, v135\n"
"v_cvt_pk_bf16_f32 v116, v136, v137\n"
"v_cvt_pk_bf16_f32 v117, v138, v139\n"
"v_cvt_pk_bf16_f32 v102, v140, v141\n"
"v_cvt_pk_bf16_f32 v103, v142, v143\n"
"v_cvt_pk_bf16_f32 v110, v144, v145\n"
"v_cvt_pk_bf16_f32 v111, v146, v147\n"
"v_cvt_pk_bf16_f32 v118, v148, v149\n"
"v_add_u32 v248, 0x1000, v248\n"
"s_branch LAY2_%=\n"
"LAX_%=:\n"
"s_waitcnt lgkmcnt(0)\n"
"v_mfma_f32_16x16x32_bf16 a[224:227], v[176:179], a[244:247], a[224:227]\n"
"s_waitcnt vmcnt(12)\n"
"v_exp_f32 v142, v142\n"
"v_mfma_f32_16x16x32_bf16 v[16:19], v[152:155], a[244:247], v[16:19]\n"
"s_barrier\n"
"v_cvt_pk_bf16_f32 v102, v140, v141\n"
"v_mfma_f32_16x16x32_bf16 v[16:19], v[156:159], a[248:251], v[16:19]\n"
"v_exp_f32 v143, v143\n"
"v_mfma_f32_16x16x32_bf16 v[48:51], v[160:163], a[244:247], v[48:51]\n"
"v_exp_f32 v144, v144\n"
"v_mfma_f32_16x16x32_bf16 v[48:51], v[164:167], a[248:251], v[48:51]\n"
"v_exp_f32 v145, v145\n"
"v_mfma_f32_16x16x32_bf16 v[80:83], v[168:171], a[244:247], v[80:83]\n"
"ds_read_b64_tr_b16 a[244:245], v234 offset:49152\n"
"v_cvt_pk_bf16_f32 v103, v142, v143\n"
"v_mfma_f32_16x16x32_bf16 v[80:83], v[172:175], a[248:251], v[80:83]\n"
"ds_read_b64_tr_b16 a[246:247], v234 offset:53248\n"
"v_cvt_pk_bf16_f32 v110, v144, v145\n"
"v_mfma_f32_16x16x32_bf16 v[84:87], v[172:175], v[224:227], v[84:87]\n"
"ds_read_b64_tr_b16 a[248:249], v238 offset:49152\n"
"v_cvt_f32_u32 v254, s43\n"
"v_mfma_f32_16x16x32_bf16 v[84:87], v[168:171], a[252:255], v[84:87]\n"
"v_exp_f32 v148, v148\n"
"v_mfma_f32_16x16x32_bf16 v[52:55], v[164:167], v[224:227], v[52:55]\n"
"ds_read_b64_tr_b16 a[250:251], v238 offset:53248\n"
"v_and_b32 v255, 0x3f0, v242\n"
"v_mfma_f32_16x16x32_bf16 v[52:55], v[160:163], a[252:255], v[52:55]\n"
"v_exp_f32 v149, v149\n"
"v_mfma_f32_16x16x32_bf16 v[20:23], v[156:159], v[224:227], v[20:23]\n"
"v_exp_f32 v150, v150\n"
"v_mfma_f32_16x16x32_bf16 v[20:23], v[152:155], a[252:255], v[20:23]\n"
"ds_read_b64_tr_b16 v[224:225], v239 offset:49152\n"
"v_cvt_pk_bf16_f32 v118, v148, v149\n"
"v_mfma_f32_16x16x32_bf16 a[228:231], v[176:179], a[252:255], a[228:231]\n"
"v_exp_f32 v151, v151\n"
"s_waitcnt lgkmcnt(2)\n"
"v_mfma_f32_16x16x32_bf16 a[216:219], v[120:123], a[244:247], a[216:219]\n"
"s_mov_b64 s[28:29], s[8:9]\n"
"v_exp_f32 v146, v146\n"
"v_smfmac_f32_16x16x64_bf16 v[196:199], a[240:243], v[96:103], v241\n"
"s_mov_b32 s30, 655360\n"
"ds_read_b64_tr_b16 v[226:227], v239 offset:53248\n"
"v_cvt_pk_bf16_f32 v119, v150, v151\n"
"v_mfma_f32_16x16x32_bf16 v[8:11], v[96:99], a[244:247], v[8:11]\n"
"s_waitcnt lgkmcnt(2)\n"
"s_mov_b32 s71, s44\n"
"v_exp_f32 v147, v147\n"
"v_mfma_f32_16x16x32_bf16 v[8:11], v[100:103], a[248:251], v[8:11]\n"
"s_mov_b32 s70, 1024\n"
"ds_read_b64_tr_b16 a[252:253], v235 offset:49152\n"
"v_cvt_pk_bf16_f32 v111, v146, v147\n"
"v_smfmac_f32_16x16x64_bf16 v[200:203], a[240:243], v[112:119], v241\n"
"ds_read_b64_tr_b16 a[254:255], v235 offset:53248\n"
"v_lshrrev_b32 v255, 1, v255\n"
"v_mfma_f32_16x16x32_bf16 v[40:43], v[104:107], a[244:247], v[40:43]\n"
"v_sub_u32 v255, v242, v255\n"
"v_xor_b32 v250, 0x1000, v255\n"
"v_mfma_f32_16x16x32_bf16 v[40:43], v[108:111], a[248:251], v[40:43]\n"
"s_add_u32 m0, s71, 32768\n"
"v_smfmac_f32_16x16x64_bf16 v[198:201], a[240:243], v[104:111], v241\n"
"s_add_u32 s41, s70, 327680\n"
"buffer_load_dwordx4 v240, s[28:31], s41 offen lds\n"
"v_mfma_f32_16x16x32_bf16 v[72:75], v[112:115], a[244:247], v[72:75]\n"
"ds_read_b64_tr_b16 a[244:245], v234 offset:50176\n"
"v_add_f32 v196, v196, v197\n"
"v_mfma_f32_16x16x32_bf16 v[72:75], v[116:119], a[248:251], v[72:75]\n"
"ds_read_b64_tr_b16 a[246:247], v234 offset:54272\n"
"v_mov_b32 v241, s60\n"
"v_mfma_f32_16x16x32_bf16 v[202:205], a[240:243], v[120:123], v[202:205]\n"
"s_waitcnt lgkmcnt(2)\n"
"ds_read_b64_tr_b16 a[248:249], v238 offset:50176\n"
"v_mfma_f32_16x16x32_bf16 v[76:79], v[116:119], v[224:227], v[76:79]\n"
"s_add_u32 s40, s41, s52\n"
"buffer_load_dwordx4 v240, s[28:31], s40 offen offset:1024 lds\n"
"v_mfma_f32_16x16x32_bf16 v[76:79], v[112:115], a[252:255], v[76:79]\n"
"ds_read_b64_tr_b16 a[250:251], v238 offset:54272\n"
"v_mfma_f32_16x16x32_bf16 v[44:47], v[108:111], v[224:227], v[44:47]\n"
"v_add_f32 v198, v198, v199\n"
"v_add_f32 v200, v200, v201\n"
"v_mfma_f32_16x16x32_bf16 v[44:47], v[104:107], a[252:255], v[44:47]\n"
"v_add_f32 v249, v196, v198\n"
"v_add_f32 v249, v249, v200\n"
"v_mfma_f32_16x16x32_bf16 v[12:15], v[100:103], v[224:227], v[12:15]\n"
"ds_read_b64_tr_b16 v[224:225], v239 offset:50176\n"
"v_mfma_f32_16x16x32_bf16 v[12:15], v[96:99], a[252:255], v[12:15]\n"
"s_add_u32 s40, s41, s53\n"
"buffer_load_dwordx4 v240, s[28:31], s40 offen offset:2048 lds\n"
"v_add_f32 v254, v254, v202\n"
"v_min3_f32 v254, v254, v196, v198\n"
"v_min_f32 v254, v254, v200\n"
"v_rcp_f32 v254, v254\n"
"v_mfma_f32_16x16x32_bf16 a[220:223], v[120:123], a[252:255], a[220:223]\n"
"ds_read_b64_tr_b16 v[226:227], v239 offset:54272\n"
"v_add_f32 v249, v249, v202\n"
"v_fmac_f32 v249, 0x53800000, v254\n"
"v_cmp_nge_f32 vcc, s46, v249\n"
"s_cbranch_vccnz LSLOW_%=\nLBACK3_%=:\ns_mov_b64 vcc, s[64:65]\n"
"s_waitcnt lgkmcnt(2)\n"
"v_mfma_f32_16x16x32_bf16 a[232:235], v[120:123], a[244:247], a[232:235]\n"
"v_rcp_f32 v243, v196\n"
"v_mfma_f32_16x16x32_bf16 v[24:27], v[96:99], a[244:247], v[24:27]\n"
"v_rcp_f32 v244, v198\n"
"v_mfma_f32_16x16x32_bf16 v[24:27], v[100:103], a[248:251], v[24:27]\n"
"ds_read_b64_tr_b16 a[252:253], v235 offset:50176\n"
"ds_read_b64_tr_b16 a[254:255], v235 offset:54272\n"
"v_mfma_f32_16x16x32_bf16 v[56:59], v[104:107], a[244:247], v[56:59]\n"
"s_add_u32 s40, s41, s54\n"
"buffer_load_dwordx4 v240, s[28:31], s40 offen offset:3072 lds\n"
"v_mfma_f32_16x16x32_bf16 v[56:59], v[108:111], a[248:251], v[56:59]\n"
"s_add_u32 s71, s44, s75\n"
"v_rcp_f32 v245, v200\n"
"v_mfma_f32_16x16x32_bf16 v[88:91], v[112:115], a[244:247], v[88:91]\n"
"s_mov_b32 s70, 1152\n"
"ds_read_b64_tr_b16 a[244:245], v232 offset:49152\n"
"v_mov_b32 v252, v192\n"
"v_mfma_f32_16x16x32_bf16 v[88:91], v[116:119], a[248:251], v[88:91]\n"
"s_waitcnt lgkmcnt(3)\n"
"ds_read_b64_tr_b16 a[246:247], v232 offset:53248\n"
"v_mov_b32 v253, v202\n"
"v_mfma_f32_16x16x32_bf16 v[92:95], v[116:119], v[224:227], v[92:95]\n"
"s_waitcnt lgkmcnt(2)\n"
"s_add_u32 m0, s71, 16384\n"
"ds_read_b64_tr_b16 a[248:249], v236 offset:49152\n"
"v_xor_b32 v254, 0x11000, v242\n"
"v_mfma_f32_16x16x32_bf16 v[92:95], v[112:115], a[252:255], v[92:95]\n"
"s_add_u32 s41, s70, 163840\n"
"buffer_load_dwordx4 v240, s[28:31], s41 offen lds\n"
"v_mfma_f32_16x16x32_bf16 v[60:63], v[108:111], v[224:227], v[60:63]\n"
"ds_read_b64_tr_b16 a[250:251], v236 offset:53248\n"
"v_bfe_u32 v249, v242, 9, 1\n"
"v_mfma_f32_16x16x32_bf16 v[60:63], v[104:107], a[252:255], v[60:63]\n"
"ds_write_b64 v250, v[252:253] offset:52224\n"
"v_mfma_f32_16x16x32_bf16 v[28:31], v[100:103], v[224:227], v[28:31]\n"
"ds_read_b64_tr_b16 v[224:225], v237 offset:49152\n"
"v_lshl_add_u32 v249, v249, 4, s43\n"
"v_mfma_f32_16x16x32_bf16 v[28:31], v[96:99], a[252:255], v[28:31]\n"
"s_add_u32 s40, s41, s52\n"
"buffer_load_dwordx4 v240, s[28:31], s40 offen offset:1024 lds\n"
"v_mfma_f32_16x16x32_bf16 a[236:239], v[120:123], a[252:255], a[236:239]\n"
"s_waitcnt lgkmcnt(3)\n"
"ds_read_b64_tr_b16 v[226:227], v237 offset:53248\n"
"v_lshl_add_u32 v249, v249, 6, s75\n"
"v_mfma_f32_16x16x32_bf16 a[208:211], v[120:123], a[244:247], a[208:211]\n"
"ds_read_b64_tr_b16 a[252:253], v233 offset:49152\n"
"v_sub_u32 v228, v232, v249\n"
"v_mfma_f32_16x16x32_bf16 v[0:3], v[96:99], a[244:247], v[0:3]\n"
"s_waitcnt lgkmcnt(3)\n"
"ds_read_b64_tr_b16 a[254:255], v233 offset:53248\n"
"v_mov_b32_dpp v243, v243 row_ror:12 row_mask:0x2 bank_mask:0xf\n"
"v_mfma_f32_16x16x32_bf16 v[0:3], v[100:103], a[248:251], v[0:3]\n"
"v_mov_b32_dpp v244, v244 row_ror:12 row_mask:0x2 bank_mask:0xf\n"
"v_mov_b32_dpp v243, v243 row_ror:8 row_mask:0x4 bank_mask:0xf\n"
"v_mfma_f32_16x16x32_bf16 v[32:35], v[104:107], a[244:247], v[32:35]\n"
"s_add_u32 s40, s41, s53\n"
"buffer_load_dwordx4 v240, s[28:31], s40 offen offset:2048 lds\n"
"v_mfma_f32_16x16x32_bf16 v[32:35], v[108:111], a[248:251], v[32:35]\n"
"ds_write_b128 v254, a[216:219] offset:32768\n"
"v_mfma_f32_16x16x32_bf16 v[64:67], v[112:115], a[244:247], v[64:67]\n"
"ds_write_b128 v254, a[220:223] offset:33792\n"
"v_mfma_f32_16x16x32_bf16 v[64:67], v[116:119], a[248:251], v[64:67]\n"
"s_waitcnt lgkmcnt(2)\n"
"ds_write_b128 v254, a[232:235] offset:34816\n"
"v_mfma_f32_16x16x32_bf16 v[68:71], v[116:119], v[224:227], v[68:71]\n"
"ds_write_b128 v254, a[236:239] offset:35840\n"
"v_mfma_f32_16x16x32_bf16 v[68:71], v[112:115], a[252:255], v[68:71]\n"
"s_add_u32 s40, s41, s54\n"
"buffer_load_dwordx4 v240, s[28:31], s40 offen offset:3072 lds\n"
"ds_read_b64_tr_b16 a[244:245], v232 offset:50176\n"
"ds_read_b64_tr_b16 a[246:247], v232 offset:54272\n"
"v_mfma_f32_16x16x32_bf16 v[36:39], v[108:111], v[224:227], v[36:39]\n"
"ds_read_b64_tr_b16 a[248:249], v236 offset:50176\n"
"ds_read_b64_tr_b16 a[250:251], v236 offset:54272\n"
"v_mfma_f32_16x16x32_bf16 v[36:39], v[104:107], a[252:255], v[36:39]\n"
"v_xor_b32 v232, 0x1000, v228\n"
"v_mov_b32_dpp v245, v245 row_ror:12 row_mask:0x2 bank_mask:0xf\n"
"v_mfma_f32_16x16x32_bf16 v[4:7], v[100:103], v[224:227], v[4:7]\n"
"ds_read_b64_tr_b16 v[224:225], v237 offset:50176\n"
"ds_read_b64_tr_b16 v[226:227], v237 offset:54272\n"
"v_mfma_f32_16x16x32_bf16 v[4:7], v[96:99], a[252:255], v[4:7]\n"
"v_mov_b32_dpp v244, v244 row_ror:8 row_mask:0x4 bank_mask:0xf\n"
"v_mov_b32_dpp v243, v243 row_ror:4 row_mask:0x8 bank_mask:0xf\n"
"v_mfma_f32_16x16x32_bf16 a[212:215], v[120:123], a[252:255], a[212:215]\n"
"ds_read_b64_tr_b16 a[252:253], v233 offset:50176\n"
"ds_read_b64_tr_b16 a[254:255], v233 offset:54272\n"
"s_waitcnt lgkmcnt(6)\n"
"v_mfma_f32_16x16x32_bf16 a[224:227], v[120:123], a[244:247], a[224:227]\n"
"s_waitcnt vmcnt(8) lgkmcnt(0)\n"
"v_mov_b32_dpp v245, v245 row_ror:8 row_mask:0x4 bank_mask:0xf\n"
"v_mov_b32_dpp v244, v244 row_ror:4 row_mask:0x8 bank_mask:0xf\n"
"v_mfma_f32_16x16x32_bf16 v[16:19], v[96:99], a[244:247], v[16:19]\n"
"s_barrier\n"
"ds_read_b64_tr_b16 v[208:209], v228 offset:16384\n"
"v_mfma_f32_16x16x32_bf16 v[16:19], v[100:103], a[248:251], v[16:19]\n"
"ds_read_b64_tr_b16 v[210:211], v228 offset:18432\n"
"ds_read_b64_tr_b16 v[212:213], v232 offset:16384\n"
"v_mfma_f32_16x16x32_bf16 v[48:51], v[104:107], a[244:247], v[48:51]\n"
"ds_read_b64_tr_b16 v[214:215], v232 offset:18432\n"
"ds_read_b64_tr_b16 v[216:217], v228 offset:24576\n"
"v_mfma_f32_16x16x32_bf16 v[48:51], v[108:111], a[248:251], v[48:51]\n"
"ds_read_b64_tr_b16 v[218:219], v228 offset:26624\n"
"ds_read_b64_tr_b16 v[220:221], v232 offset:24576\n"
"v_mul_f32_dpp v8, v243, v8 row_newbcast:0 row_mask:0xf bank_mask:0xf\n"
"v_mul_f32_dpp v9, v243, v9 row_newbcast:1 row_mask:0xf bank_mask:0xf\n"
"v_mul_f32_dpp v10, v243, v10 row_newbcast:2 row_mask:0xf bank_mask:0xf\n"
"v_mul_f32_dpp v11, v243, v11 row_newbcast:3 row_mask:0xf bank_mask:0xf\n"
"v_mul_f32_dpp v12, v243, v12 row_newbcast:0 row_mask:0xf bank_mask:0xf\n"
"v_mul_f32_dpp v13, v243, v13 row_newbcast:1 row_mask:0xf bank_mask:0xf\n"
"v_mul_f32_dpp v14, v243, v14 row_newbcast:2 row_mask:0xf bank_mask:0xf\n"
"v_mul_f32_dpp v15, v243, v15 row_newbcast:3 row_mask:0xf bank_mask:0xf\n"
"v_cvt_pk_bf16_f32 v8, v8, v9\n"
"v_cvt_pk_bf16_f32 v9, v10, v11\n"
"v_cvt_pk_bf16_f32 v10, v12, v13\n"
"v_cvt_pk_bf16_f32 v11, v14, v15\n"
"v_mfma_f32_16x16x32_bf16 v[80:83], v[112:115], a[244:247], v[80:83]\n"
"ds_write_b128 v246, v[8:11] offset:49152\n"
"v_mov_b32_dpp v245, v245 row_ror:4 row_mask:0x8 bank_mask:0xf\n"
"v_mul_f32_dpp v40, v244, v40 row_newbcast:0 row_mask:0xf bank_mask:0xf\n"
"v_mul_f32_dpp v41, v244, v41 row_newbcast:1 row_mask:0xf bank_mask:0xf\n"
"v_mul_f32_dpp v42, v244, v42 row_newbcast:2 row_mask:0xf bank_mask:0xf\n"
"v_mul_f32_dpp v43, v244, v43 row_newbcast:3 row_mask:0xf bank_mask:0xf\n"
"v_mul_f32_dpp v44, v244, v44 row_newbcast:0 row_mask:0xf bank_mask:0xf\n"
"v_mul_f32_dpp v45, v244, v45 row_newbcast:1 row_mask:0xf bank_mask:0xf\n"
"v_mul_f32_dpp v46, v244, v46 row_newbcast:2 row_mask:0xf bank_mask:0xf\n"
"v_mul_f32_dpp v47, v244, v47 row_newbcast:3 row_mask:0xf bank_mask:0xf\n"
"v_cvt_pk_bf16_f32 v40, v40, v41\n"
"v_cvt_pk_bf16_f32 v41, v42, v43\n"
"v_cvt_pk_bf16_f32 v42, v44, v45\n"
"v_cvt_pk_bf16_f32 v43, v46, v47\n"
"v_mfma_f32_16x16x32_bf16 v[80:83], v[116:119], a[248:251], v[80:83]\n"
"ds_write_b128 v246, v[40:43] offset:50176\n"
"v_mul_f32_dpp v72, v245, v72 row_newbcast:0 row_mask:0xf bank_mask:0xf\n"
"v_mul_f32_dpp v73, v245, v73 row_newbcast:1 row_mask:0xf bank_mask:0xf\n"
"v_mul_f32_dpp v74, v245, v74 row_newbcast:2 row_mask:0xf bank_mask:0xf\n"
"v_mul_f32_dpp v75, v245, v75 row_newbcast:3 row_mask:0xf bank_mask:0xf\n"
"v_mul_f32_dpp v76, v245, v76 row_newbcast:0 row_mask:0xf bank_mask:0xf\n"
"v_mul_f32_dpp v77, v245, v77 row_newbcast:1 row_mask:0xf bank_mask:0xf\n"
"v_mul_f32_dpp v78, v245, v78 row_newbcast:2 row_mask:0xf bank_mask:0xf\n"
"v_mul_f32_dpp v79, v245, v79 row_newbcast:3 row_mask:0xf bank_mask:0xf\n"
"v_cvt_pk_bf16_f32 v72, v72, v73\n"
"v_cvt_pk_bf16_f32 v73, v74, v75\n"
"v_cvt_pk_bf16_f32 v74, v76, v77\n"
"v_cvt_pk_bf16_f32 v75, v78, v79\n"
"v_mfma_f32_16x16x32_bf16 v[84:87], v[116:119], v[224:227], v[84:87]\n"
"ds_write_b128 v246, v[72:75] offset:51200\n"
"v_mfma_f32_16x16x32_bf16 v[84:87], v[112:115], a[252:255], v[84:87]\n"
"ds_read_b64_tr_b16 a[4:5], v247 offset:49152\n"
"ds_read_b64_tr_b16 v[180:181], v228 offset:0\n"
"ds_read_b64_tr_b16 v[182:183], v228 offset:2048\n"
"ds_read_b64_tr_b16 v[222:223], v232 offset:26624\n"
"v_mfma_f32_16x16x32_bf16 v[52:55], v[108:111], v[224:227], v[52:55]\n"
"ds_read_b64_tr_b16 a[6:7], v247 offset:49160\n"
"ds_read_b64_tr_b16 a[20:21], v247 offset:50176\n"
"ds_read_b64_tr_b16 v[184:185], v232 offset:0\n"
"ds_read_b64_tr_b16 v[186:187], v232 offset:2048\n"
"v_mfma_f32_16x16x32_bf16 v[52:55], v[104:107], a[252:255], v[52:55]\n"
"ds_read_b64_tr_b16 a[22:23], v247 offset:50184\n"
"ds_read_b64_tr_b16 a[36:37], v247 offset:51200\n"
"ds_read_b64_tr_b16 v[188:189], v228 offset:8192\n"
"ds_read_b64_tr_b16 v[190:191], v228 offset:10240\n"
"v_mul_f32_dpp v24, v243, v24 row_newbcast:0 row_mask:0xf bank_mask:0xf\n"
"v_mul_f32_dpp v25, v243, v25 row_newbcast:1 row_mask:0xf bank_mask:0xf\n"
"v_mul_f32_dpp v26, v243, v26 row_newbcast:2 row_mask:0xf bank_mask:0xf\n"
"v_mul_f32_dpp v27, v243, v27 row_newbcast:3 row_mask:0xf bank_mask:0xf\n"
"v_mul_f32_dpp v28, v243, v28 row_newbcast:0 row_mask:0xf bank_mask:0xf\n"
"v_mul_f32_dpp v29, v243, v29 row_newbcast:1 row_mask:0xf bank_mask:0xf\n"
"v_mul_f32_dpp v30, v243, v30 row_newbcast:2 row_mask:0xf bank_mask:0xf\n"
"v_mul_f32_dpp v31, v243, v31 row_newbcast:3 row_mask:0xf bank_mask:0xf\n"
"v_cvt_pk_bf16_f32 v24, v24, v25\n"
"v_cvt_pk_bf16_f32 v25, v26, v27\n"
"v_cvt_pk_bf16_f32 v26, v28, v29\n"
"v_cvt_pk_bf16_f32 v27, v30, v31\n"
"v_mfma_f32_16x16x32_bf16 v[20:23], v[100:103], v[224:227], v[20:23]\n"
"ds_write_b128 v246, v[24:27] offset:49152\n"
"ds_read_b64_tr_b16 v[192:193], v232 offset:8192\n"
"ds_read_b64_tr_b16 v[194:195], v232 offset:10240\n"
"v_mul_f32_dpp v56, v244, v56 row_newbcast:0 row_mask:0xf bank_mask:0xf\n"
"v_mul_f32_dpp v57, v244, v57 row_newbcast:1 row_mask:0xf bank_mask:0xf\n"
"v_mul_f32_dpp v58, v244, v58 row_newbcast:2 row_mask:0xf bank_mask:0xf\n"
"v_mul_f32_dpp v59, v244, v59 row_newbcast:3 row_mask:0xf bank_mask:0xf\n"
"v_mul_f32_dpp v60, v244, v60 row_newbcast:0 row_mask:0xf bank_mask:0xf\n"
"v_mul_f32_dpp v61, v244, v61 row_newbcast:1 row_mask:0xf bank_mask:0xf\n"
"v_mul_f32_dpp v62, v244, v62 row_newbcast:2 row_mask:0xf bank_mask:0xf\n"
"v_mul_f32_dpp v63, v244, v63 row_newbcast:3 row_mask:0xf bank_mask:0xf\n"
"v_cvt_pk_bf16_f32 v56, v56, v57\n"
"v_cvt_pk_bf16_f32 v57, v58, v59\n"
"v_cvt_pk_bf16_f32 v58, v60, v61\n"
"v_cvt_pk_bf16_f32 v59, v62, v63\n"
"v_mfma_f32_16x16x32_bf16 v[20:23], v[96:99], a[252:255], v[20:23]\n"
"ds_read_b64_tr_b16 a[38:39], v247 offset:51208\n"
"ds_write_b128 v246, v[56:59] offset:50176\n"
"v_mul_f32_dpp v88, v245, v88 row_newbcast:0 row_mask:0xf bank_mask:0xf\n"
"v_mul_f32_dpp v89, v245, v89 row_newbcast:1 row_mask:0xf bank_mask:0xf\n"
"v_mul_f32_dpp v90, v245, v90 row_newbcast:2 row_mask:0xf bank_mask:0xf\n"
"v_mul_f32_dpp v91, v245, v91 row_newbcast:3 row_mask:0xf bank_mask:0xf\n"
"v_mul_f32_dpp v92, v245, v92 row_newbcast:0 row_mask:0xf bank_mask:0xf\n"
"v_mul_f32_dpp v93, v245, v93 row_newbcast:1 row_mask:0xf bank_mask:0xf\n"
"v_mul_f32_dpp v94, v245, v94 row_newbcast:2 row_mask:0xf bank_mask:0xf\n"
"v_mul_f32_dpp v95, v245, v95 row_newbcast:3 row_mask:0xf bank_mask:0xf\n"
"v_cvt_pk_bf16_f32 v88, v88, v89\n"
"v_cvt_pk_bf16_f32 v89, v90, v91\n"
"v_cvt_pk_bf16_f32 v90, v92, v93\n"
"v_cvt_pk_bf16_f32 v91, v94, v95\n"
"v_mfma_f32_16x16x32_bf16 a[228:231], v[120:123], a[252:255], a[228:231]\n"
"ds_write_b128 v246, v[88:91] offset:51200\n"
"s_waitcnt lgkmcnt(14)\n"
"v_mfma_f32_16x16x32_bf16 v[124:127], v[180:183], a[96:99], 0\n"
"s_waitcnt lgkmcnt(10)\n"
"ds_read_b64_tr_b16 a[12:13], v247 offset:49152\n"
"v_mul_f32_dpp v0, v243, v0 row_newbcast:0 row_mask:0xf bank_mask:0xf\n"
"v_mfma_f32_16x16x32_bf16 v[124:127], v[184:187], a[100:103], v[124:127]\n"
"s_waitcnt lgkmcnt(7)\n"
"ds_read_b64_tr_b16 a[14:15], v247 offset:49160\n"
"ds_read_b64_tr_b16 a[28:29], v247 offset:50176\n"
"v_mfma_f32_16x16x32_bf16 v[124:127], v[188:191], a[104:107], v[124:127]\n"
"s_waitcnt lgkmcnt(6)\n"
"ds_read_b64_tr_b16 a[30:31], v247 offset:50184\n"
"ds_read_b64_tr_b16 a[44:45], v247 offset:51200\n"
"v_mul_f32_dpp v1, v243, v1 row_newbcast:1 row_mask:0xf bank_mask:0xf\n"
"v_mul_f32_dpp v2, v243, v2 row_newbcast:2 row_mask:0xf bank_mask:0xf\n"
"v_mul_f32_dpp v3, v243, v3 row_newbcast:3 row_mask:0xf bank_mask:0xf\n"
"v_mul_f32_dpp v4, v243, v4 row_newbcast:0 row_mask:0xf bank_mask:0xf\n"
"v_mul_f32_dpp v5, v243, v5 row_newbcast:1 row_mask:0xf bank_mask:0xf\n"
"v_mul_f32_dpp v6, v243, v6 row_newbcast:2 row_mask:0xf bank_mask:0xf\n"
"v_mul_f32_dpp v7, v243, v7 row_newbcast:3 row_mask:0xf bank_mask:0xf\n"
"v_cvt_pk_bf16_f32 v0, v0, v1\n"
"v_cvt_pk_bf16_f32 v1, v2, v3\n"
"v_cvt_pk_bf16_f32 v2, v4, v5\n"
"v_cvt_pk_bf16_f32 v3, v6, v7\n"
"v_mfma_f32_16x16x32_bf16 v[124:127], v[192:195], a[108:111], v[124:127]\n"
"ds_write_b128 v246, v[0:3] offset:49152\n"
"v_mul_f32_dpp v32, v244, v32 row_newbcast:0 row_mask:0xf bank_mask:0xf\n"
"v_mul_f32_dpp v33, v244, v33 row_newbcast:1 row_mask:0xf bank_mask:0xf\n"
"v_mul_f32_dpp v34, v244, v34 row_newbcast:2 row_mask:0xf bank_mask:0xf\n"
"v_mul_f32_dpp v35, v244, v35 row_newbcast:3 row_mask:0xf bank_mask:0xf\n"
"v_mul_f32_dpp v36, v244, v36 row_newbcast:0 row_mask:0xf bank_mask:0xf\n"
"v_mul_f32_dpp v37, v244, v37 row_newbcast:1 row_mask:0xf bank_mask:0xf\n"
"v_mul_f32_dpp v38, v244, v38 row_newbcast:2 row_mask:0xf bank_mask:0xf\n"
"v_mul_f32_dpp v39, v244, v39 row_newbcast:3 row_mask:0xf bank_mask:0xf\n"
"v_cvt_pk_bf16_f32 v32, v32, v33\n"
"v_cvt_pk_bf16_f32 v33, v34, v35\n"
"v_cvt_pk_bf16_f32 v34, v36, v37\n"
"v_cvt_pk_bf16_f32 v35, v38, v39\n"
"v_mfma_f32_16x16x32_bf16 v[124:127], v[208:211], a[144:147], v[124:127]\n"
"ds_read_b64_tr_b16 a[46:47], v247 offset:51208\n"
"ds_write_b128 v246, v[32:35] offset:50176\n"
"v_mul_f32_dpp v64, v245, v64 row_newbcast:0 row_mask:0xf bank_mask:0xf\n"
"v_mul_f32_dpp v65, v245, v65 row_newbcast:1 row_mask:0xf bank_mask:0xf\n"
"v_mul_f32_dpp v66, v245, v66 row_newbcast:2 row_mask:0xf bank_mask:0xf\n"
"v_mul_f32_dpp v67, v245, v67 row_newbcast:3 row_mask:0xf bank_mask:0xf\n"
"v_mul_f32_dpp v68, v245, v68 row_newbcast:0 row_mask:0xf bank_mask:0xf\n"
"v_mul_f32_dpp v69, v245, v69 row_newbcast:1 row_mask:0xf bank_mask:0xf\n"
"v_mul_f32_dpp v70, v245, v70 row_newbcast:2 row_mask:0xf bank_mask:0xf\n"
"v_mul_f32_dpp v71, v245, v71 row_newbcast:3 row_mask:0xf bank_mask:0xf\n"
"v_cvt_pk_bf16_f32 v64, v64, v65\n"
"v_cvt_pk_bf16_f32 v65, v66, v67\n"
"v_cvt_pk_bf16_f32 v66, v68, v69\n"
"v_cvt_pk_bf16_f32 v67, v70, v71\n"
"v_mfma_f32_16x16x32_bf16 v[124:127], v[212:215], a[148:151], v[124:127]\n"
"ds_write_b128 v246, v[64:67] offset:51200\n"
"v_mfma_f32_16x16x32_bf16 v[124:127], v[216:219], a[152:155], v[124:127]\n"
"ds_read_b64_tr_b16 a[0:1], v247 offset:49152\n"
"v_xor_b32 v254, 0x1000, v254\n"
"v_mfma_f32_16x16x32_bf16 v[124:127], v[220:223], a[156:159], v[124:127]\n"
"ds_read_b64_tr_b16 a[2:3], v247 offset:49160\n"
"ds_read_b64_tr_b16 a[16:17], v247 offset:50176\n"
"v_mfma_f32_16x16x32_bf16 v[140:143], v[220:223], a[172:175], 0\n"
"ds_read_b64_tr_b16 a[18:19], v247 offset:50184\n"
"ds_read_b64_tr_b16 a[32:33], v247 offset:51200\n"
"v_mfma_f32_16x16x32_bf16 v[140:143], v[216:219], a[168:171], v[140:143]\n"
"ds_read_b64_tr_b16 a[34:35], v247 offset:51208\n"
"ds_read_b64 v[250:251], v255 offset:52224\n"
"v_mfma_f32_16x16x32_bf16 v[140:143], v[212:215], a[164:167], v[140:143]\n"
"ds_read_b128 v[24:27], v254 offset:32768\n"
"v_add_u32 v236, s76, v247\n"
"v_mfma_f32_16x16x32_bf16 v[140:143], v[208:211], a[160:163], v[140:143]\n"
"ds_read_b128 v[28:31], v254 offset:33792\n"
"v_xor_b32 v237, 0x400, v236\n"
"v_mfma_f32_16x16x32_bf16 v[140:143], v[192:195], a[124:127], v[140:143]\n"
"ds_read_b128 v[56:59], v254 offset:34816\n"
"v_sub_u32 v229, v233, v249\n"
"v_mfma_f32_16x16x32_bf16 v[140:143], v[188:191], a[120:123], v[140:143]\n"
"ds_read_b128 v[60:63], v254 offset:35840\n"
"v_xor_b32 v233, 0x1000, v229\n"
"v_mfma_f32_16x16x32_bf16 v[140:143], v[184:187], a[116:119], v[140:143]\n"
"ds_read_b64_tr_b16 a[252:253], v237 offset:6144\n"
"ds_read_b64_tr_b16 a[254:255], v237 offset:6152\n"
"v_mfma_f32_16x16x32_bf16 v[140:143], v[180:183], a[112:115], v[140:143]\n"
"ds_read_b64_tr_b16 a[248:249], v236 offset:6144\n"
"ds_read_b64_tr_b16 a[250:251], v236 offset:6152\n"
"v_mfma_f32_16x16x32_bf16 v[156:159], v[180:183], a[128:131], 0\n"
"ds_read_b64_tr_b16 a[244:245], v237 offset:4096\n"
"ds_read_b64_tr_b16 a[246:247], v237 offset:4104\n"
"v_mfma_f32_16x16x32_bf16 v[156:159], v[184:187], a[132:135], v[156:159]\n"
"ds_read_b64_tr_b16 a[240:241], v236 offset:4096\n"
"ds_read_b64_tr_b16 a[242:243], v236 offset:4104\n"
"v_mfma_f32_16x16x32_bf16 v[156:159], v[188:191], a[136:139], v[156:159]\n"
"ds_read_b64_tr_b16 a[204:205], v237 offset:2048\n"
"ds_read_b64_tr_b16 a[206:207], v237 offset:2056\n"
"v_mfma_f32_16x16x32_bf16 v[156:159], v[192:195], a[140:143], v[156:159]\n"
"s_waitcnt lgkmcnt(8)\n"
"ds_read_b64_tr_b16 a[200:201], v236 offset:2048\n"
"ds_read_b64_tr_b16 a[202:203], v236 offset:2056\n"
"v_mfma_f32_16x16x32_bf16 v[156:159], v[208:211], a[176:179], v[156:159]\n"
"s_add_u32 m0, s44, 49152\n"
"ds_read_b64_tr_b16 a[196:197], v237 offset:0\n"
"ds_read_b64_tr_b16 a[198:199], v237 offset:8\n"
"v_mfma_f32_16x16x32_bf16 v[156:159], v[212:215], a[180:183], v[156:159]\n"
"s_mov_b32 s41, 492544\n"
"buffer_load_dwordx4 v240, s[28:31], s41 offen lds\n"
"v_mfma_f32_16x16x32_bf16 v[156:159], v[216:219], a[184:187], v[156:159]\n"
"ds_read_b64_tr_b16 a[192:193], v236 offset:0\n"
"ds_read_b64_tr_b16 a[194:195], v236 offset:8\n"
"v_mfma_f32_16x16x32_bf16 v[156:159], v[220:223], a[188:191], v[156:159]\n"
"ds_read_b64_tr_b16 v[96:97], v229 offset:0\n"
"ds_read_b64_tr_b16 v[98:99], v229 offset:2048\n"
"v_mfma_f32_16x16x32_bf16 v[172:175], v[220:223], a[252:255], 0\n"
"s_waitcnt lgkmcnt(6)\n"
"s_add_u32 s40, s41, s52\n"
"buffer_load_dwordx4 v240, s[28:31], s40 offen offset:1024 lds\n"
"v_mfma_f32_16x16x32_bf16 v[172:175], v[216:219], a[248:251], v[172:175]\n"
"ds_read_b64_tr_b16 v[100:101], v233 offset:0\n"
"ds_read_b64_tr_b16 v[102:103], v233 offset:2048\n"
"v_mfma_f32_16x16x32_bf16 v[172:175], v[212:215], a[244:247], v[172:175]\n"
"ds_read_b64_tr_b16 v[104:105], v229 offset:8192\n"
"ds_read_b64_tr_b16 v[106:107], v229 offset:10240\n"
"v_mfma_f32_16x16x32_bf16 v[172:175], v[208:211], a[240:243], v[172:175]\n"
"s_add_u32 s40, s41, s53\n"
"buffer_load_dwordx4 v240, s[28:31], s40 offen offset:2048 lds\n"
"v_mfma_f32_16x16x32_bf16 v[172:175], v[192:195], a[204:207], v[172:175]\n"
"ds_read_b64_tr_b16 v[108:109], v233 offset:8192\n"
"ds_read_b64_tr_b16 v[110:111], v233 offset:10240\n"
"v_mfma_f32_16x16x32_bf16 v[172:175], v[188:191], a[200:203], v[172:175]\n"
"s_waitcnt lgkmcnt(6)\n"
"ds_read_b64_tr_b16 v[112:113], v229 offset:16384\n"
"ds_read_b64_tr_b16 v[114:115], v229 offset:18432\n"
"v_mfma_f32_16x16x32_bf16 v[172:175], v[184:187], a[196:199], v[172:175]\n"
"s_add_u32 s40, s41, s54\n"
"buffer_load_dwordx4 v240, s[28:31], s40 offen offset:3072 lds\n"
"v_mfma_f32_16x16x32_bf16 v[172:175], v[180:183], a[192:195], v[172:175]\n"
"ds_read_b64_tr_b16 v[116:117], v233 offset:16384\n"
"ds_read_b64_tr_b16 v[118:119], v233 offset:18432\n"
"v_mfma_f32_16x16x32_bf16 v[128:131], v[96:99], a[96:99], 0\n"
"s_waitcnt lgkmcnt(6)\n"
"ds_read_b64_tr_b16 v[120:121], v229 offset:24576\n"
"ds_read_b64_tr_b16 v[122:123], v229 offset:26624\n"
"v_mfma_f32_16x16x32_bf16 v[128:131], v[100:103], a[100:103], v[128:131]\n"
"ds_read_b64_tr_b16 v[224:225], v233 offset:24576\n"
"ds_read_b64_tr_b16 v[226:227], v233 offset:26624\n"
"v_mfma_f32_16x16x32_bf16 v[128:131], v[104:107], a[104:107], v[128:131]\n"
"s_waitcnt lgkmcnt(6)\n"
"ds_read_b64_tr_b16 a[236:237], v237 offset:10240\n"
"v_sub_u32 v230, v234, v249\n"
"v_mfma_f32_16x16x32_bf16 v[128:131], v[108:111], a[108:111], v[128:131]\n"
"ds_read_b64_tr_b16 v[180:181], v230 offset:0\n"
"v_xor_b32 v234, 0x1000, v230\n"
"v_mfma_f32_16x16x32_bf16 v[128:131], v[112:115], a[144:147], v[128:131]\n"
"s_waitcnt lgkmcnt(6)\n"
"ds_read_b64_tr_b16 v[182:183], v230 offset:2048\n"
"v_sub_u32 v231, v235, v249\n"
"v_mfma_f32_16x16x32_bf16 v[128:131], v[116:119], a[148:151], v[128:131]\n"
"s_waitcnt lgkmcnt(5)\n"
"ds_read_b64_tr_b16 v[184:185], v234 offset:0\n"
"v_min_f32 v249, v252, v250\n"
"v_mfma_f32_16x16x32_bf16 v[128:131], v[120:123], a[152:155], v[128:131]\n"
"s_waitcnt lgkmcnt(4)\n"
"ds_read_b64_tr_b16 v[186:187], v234 offset:2048\n"
"v_sub_f32 v252, v249, v252\n"
"v_mfma_f32_16x16x32_bf16 v[128:131], v[224:227], a[156:159], v[128:131]\n"
"v_exp_f32 v252, v252\n"
"v_mfma_f32_16x16x32_bf16 v[144:147], v[224:227], a[172:175], 0\n"
"ds_read_b64_tr_b16 v[188:189], v230 offset:8192\n"
"v_sub_f32 v250, v249, v250\n"
"v_mfma_f32_16x16x32_bf16 v[144:147], v[120:123], a[168:171], v[144:147]\n"
"v_exp_f32 v250, v250\n"
"v_mfma_f32_16x16x32_bf16 v[144:147], v[116:119], a[164:167], v[144:147]\n"
"ds_read_b64_tr_b16 v[190:191], v230 offset:10240\n"
"v_mul_f32 v253, v253, v252\n"
"v_mfma_f32_16x16x32_bf16 v[144:147], v[112:115], a[160:163], v[144:147]\n"
"ds_read_b64_tr_b16 v[192:193], v234 offset:8192\n"
"v_fmac_f32 v253, v251, v250\n"
"v_mfma_f32_16x16x32_bf16 v[144:147], v[108:111], a[124:127], v[144:147]\n"
"v_rcp_f32 v253, v253\n"
"v_mfma_f32_16x16x32_bf16 v[144:147], v[104:107], a[120:123], v[144:147]\n"
"ds_read_b64_tr_b16 v[194:195], v234 offset:10240\n"
"v_mul_f32 v250, v250, v253\n"
"v_mfma_f32_16x16x32_bf16 v[144:147], v[100:103], a[116:119], v[144:147]\n"
"ds_read_b64_tr_b16 v[208:209], v230 offset:16384\n"
"v_mul_f32 v252, v252, v253\n"
"v_mfma_f32_16x16x32_bf16 v[144:147], v[96:99], a[112:115], v[144:147]\n"
"ds_read_b64_tr_b16 v[210:211], v230 offset:18432\n"
"v_mov_b32_dpp v250, v250 row_ror:12 row_mask:0x2 bank_mask:0xf\n"
"v_mfma_f32_16x16x32_bf16 v[160:163], v[96:99], a[128:131], 0\n"
"ds_read_b64_tr_b16 v[212:213], v234 offset:16384\n"
"v_mov_b32_dpp v252, v252 row_ror:12 row_mask:0x2 bank_mask:0xf\n"
"v_mfma_f32_16x16x32_bf16 v[160:163], v[100:103], a[132:135], v[160:163]\n"
"ds_read_b64_tr_b16 v[214:215], v234 offset:18432\n"
"v_mov_b32_dpp v250, v250 row_ror:8 row_mask:0x4 bank_mask:0xf\n"
"v_mfma_f32_16x16x32_bf16 v[160:163], v[104:107], a[136:139], v[160:163]\n"
"ds_read_b64_tr_b16 v[216:217], v230 offset:24576\n"
"v_mov_b32_dpp v252, v252 row_ror:8 row_mask:0x4 bank_mask:0xf\n"
"v_mfma_f32_16x16x32_bf16 v[160:163], v[108:111], a[140:143], v[160:163]\n"
"ds_read_b64_tr_b16 v[218:219], v230 offset:26624\n"
"v_mov_b32_dpp v250, v250 row_ror:4 row_mask:0x8 bank_mask:0xf\n"
"v_mfma_f32_16x16x32_bf16 v[160:163], v[112:115], a[176:179], v[160:163]\n"
"ds_read_b64_tr_b16 v[220:221], v234 offset:24576\n"
"v_mov_b32_dpp v252, v252 row_ror:4 row_mask:0x8 bank_mask:0xf\n"
"v_mfma_f32_16x16x32_bf16 v[160:163], v[116:119], a[180:183], v[160:163]\n"
"ds_read_b64_tr_b16 v[222:223], v234 offset:26624\n"
"v_accvgpr_read_b32 v249, a208\n"
"v_mfma_f32_16x16x32_bf16 v[160:163], v[120:123], a[184:187], v[160:163]\n"
"ds_read_b64_tr_b16 a[238:239], v237 offset:10248\n"
"v_mul_f32_dpp v24, v250, v24 row_newbcast:0 row_mask:0xf bank_mask:0xf\n"
"v_mfma_f32_16x16x32_bf16 v[160:163], v[224:227], a[188:191], v[160:163]\n"
"ds_read_b64_tr_b16 a[232:233], v236 offset:10240\n"
"v_accvgpr_read_b32 v206, a209\n"
"v_mfma_f32_16x16x32_bf16 v[176:179], v[224:227], a[252:255], 0\n"
"ds_read_b64_tr_b16 a[234:235], v236 offset:10248\n"
"v_mul_f32_dpp v25, v250, v25 row_newbcast:1 row_mask:0xf bank_mask:0xf\n"
"v_mfma_f32_16x16x32_bf16 v[176:179], v[120:123], a[248:251], v[176:179]\n"
"ds_read_b64_tr_b16 v[120:121], v231 offset:24576\n"
"v_accvgpr_read_b32 v253, a210\n"
"v_mfma_f32_16x16x32_bf16 v[176:179], v[116:119], a[244:247], v[176:179]\n"
"ds_read_b64_tr_b16 v[122:123], v231 offset:26624\n"
"v_mul_f32_dpp v26, v250, v26 row_newbcast:2 row_mask:0xf bank_mask:0xf\n"
"v_mfma_f32_16x16x32_bf16 v[176:179], v[112:115], a[240:243], v[176:179]\n"
"ds_read_b64_tr_b16 v[112:113], v231 offset:16384\n"
"v_accvgpr_read_b32 v251, a211\n"
"v_mfma_f32_16x16x32_bf16 v[176:179], v[108:111], a[204:207], v[176:179]\n"
"ds_read_b64_tr_b16 v[114:115], v231 offset:18432\n"
"v_mul_f32_dpp v27, v250, v27 row_newbcast:3 row_mask:0xf bank_mask:0xf\n"
"v_mfma_f32_16x16x32_bf16 v[176:179], v[104:107], a[200:203], v[176:179]\n"
"ds_read_b64_tr_b16 v[104:105], v231 offset:8192\n"
"v_fmac_f32_dpp v24, v252, v249 row_newbcast:0 row_mask:0xf bank_mask:0xf\n"
"v_mfma_f32_16x16x32_bf16 v[176:179], v[100:103], a[196:199], v[176:179]\n"
"ds_read_b64_tr_b16 v[106:107], v231 offset:10240\n"
"v_fmac_f32_dpp v25, v252, v206 row_newbcast:1 row_mask:0xf bank_mask:0xf\n"
"v_mfma_f32_16x16x32_bf16 v[176:179], v[96:99], a[192:195], v[176:179]\n"
"s_waitcnt lgkmcnt(4)\n"
"ds_read_b64_tr_b16 v[96:97], v231 offset:0\n"
"v_fmac_f32_dpp v26, v252, v253 row_newbcast:2 row_mask:0xf bank_mask:0xf\n"
"v_mfma_f32_16x16x32_bf16 v[132:135], v[180:183], a[96:99], 0\n"
"s_mov_b32 s47, 0\n"
"ds_read_b64_tr_b16 v[98:99], v231 offset:2048\n"
"v_fmac_f32_dpp v27, v252, v251 row_newbcast:3 row_mask:0xf bank_mask:0xf\n"
"v_mfma_f32_16x16x32_bf16 v[132:135], v[184:187], a[100:103], v[132:135]\n"
"s_mul_i32 s16, s61, 286720\n"
"ds_read_b64_tr_b16 a[220:221], v237 offset:8192\n"
"v_accvgpr_read_b32 v249, a212\n"
"v_mfma_f32_16x16x32_bf16 v[132:135], v[188:191], a[104:107], v[132:135]\n"
"s_add_u32 s16, s16, 229376\n"
"ds_read_b64_tr_b16 a[222:223], v237 offset:8200\n"
"v_mul_f32_dpp v28, v250, v28 row_newbcast:0 row_mask:0xf bank_mask:0xf\n"
"v_mfma_f32_16x16x32_bf16 v[132:135], v[192:195], a[108:111], v[132:135]\n"
"s_add_u32 s17, s16, 4096\n"
"ds_read_b64_tr_b16 a[216:217], v236 offset:8192\n"
"v_accvgpr_read_b32 v206, a213\n"
"v_mfma_f32_16x16x32_bf16 v[132:135], v[208:211], a[144:147], v[132:135]\n"
"s_add_u32 s72, s61, 3\n"
"ds_read_b64_tr_b16 a[218:219], v236 offset:8200\n"
"v_mul_f32_dpp v29, v250, v29 row_newbcast:1 row_mask:0xf bank_mask:0xf\n"
"v_mfma_f32_16x16x32_bf16 v[132:135], v[212:215], a[148:151], v[132:135]\n"
"s_add_u32 s21, s60, -1\n"
"v_accvgpr_read_b32 v253, a214\n"
"v_mul_f32_dpp v30, v250, v30 row_newbcast:2 row_mask:0xf bank_mask:0xf\n"
"v_mfma_f32_16x16x32_bf16 v[132:135], v[216:219], a[152:155], v[132:135]\n"
"s_sub_u32 s72, s72, 1\n"
"v_accvgpr_read_b32 v251, a215\n"
"v_mul_f32_dpp v31, v250, v31 row_newbcast:3 row_mask:0xf bank_mask:0xf\n"
"v_mfma_f32_16x16x32_bf16 v[132:135], v[220:223], a[156:159], v[132:135]\n"
"s_and_b32 s72, s72, 7\n"
"v_fmac_f32_dpp v28, v252, v249 row_newbcast:0 row_mask:0xf bank_mask:0xf\n"
"v_fmac_f32_dpp v29, v252, v206 row_newbcast:1 row_mask:0xf bank_mask:0xf\n"
"v_mfma_f32_16x16x32_bf16 v[148:151], v[220:223], a[172:175], 0\n"
"s_mul_i32 s20, s72, 286720\n"
"v_fmac_f32_dpp v30, v252, v253 row_newbcast:2 row_mask:0xf bank_mask:0xf\n"
"v_fmac_f32_dpp v31, v252, v251 row_newbcast:3 row_mask:0xf bank_mask:0xf\n"
"v_mfma_f32_16x16x32_bf16 v[148:151], v[216:219], a[168:171], v[148:151]\n"
"s_mul_i32 s18, s61, 28672\n"
"v_accvgpr_read_b32 v249, a224\n"
"v_mul_f32_dpp v56, v250, v56 row_newbcast:0 row_mask:0xf bank_mask:0xf\n"
"v_mfma_f32_16x16x32_bf16 v[148:151], v[212:215], a[164:167], v[148:151]\n"
"s_add_u32 s18, s18, s20\n"
"v_accvgpr_read_b32 v206, a225\n"
"v_mul_f32_dpp v57, v250, v57 row_newbcast:1 row_mask:0xf bank_mask:0xf\n"
"v_mfma_f32_16x16x32_bf16 v[148:151], v[208:211], a[160:163], v[148:151]\n"
"s_cmp_ge_u32 s47, 3\ns_cselect_b32 s18, s18, s77\n"
"v_accvgpr_read_b32 v253, a226\n"
"v_mul_f32_dpp v58, v250, v58 row_newbcast:2 row_mask:0xf bank_mask:0xf\n"
"v_mfma_f32_16x16x32_bf16 v[148:151], v[192:195], a[124:127], v[148:151]\n"
"s_add_u32 s19, s18, 4096\n"
"v_accvgpr_read_b32 v251, a227\n"
"v_mul_f32_dpp v59, v250, v59 row_newbcast:3 row_mask:0xf bank_mask:0xf\n"
"v_mfma_f32_16x16x32_bf16 v[148:151], v[188:191], a[120:123], v[148:151]\n"
"s_cmp_ge_u32 s47, 2\ns_cselect_b32 s20, s20, s77\ns_cselect_b32 s69, 15, -1\n"
"v_cvt_pk_bf16_f32 v24, v24, v25\n"
"v_cvt_pk_bf16_f32 v25, v26, v27\n"
"v_mfma_f32_16x16x32_bf16 v[148:151], v[184:187], a[116:119], v[148:151]\n"
"s_add_u32 s21, s21, 1\n"
"v_cvt_pk_bf16_f32 v26, v28, v29\n"
"v_cvt_pk_bf16_f32 v27, v30, v31\n"
"v_mfma_f32_16x16x32_bf16 v[148:151], v[180:183], a[112:115], v[148:151]\n"
"v_bfe_u32 v207, v242, 8, 2\n"
"v_lshlrev_b32 v207, 6, v207\n"
"v_xor_b32 v207, v207, v248\n"
"v_fmac_f32_dpp v56, v252, v249 row_newbcast:0 row_mask:0xf bank_mask:0xf\n"
"v_fmac_f32_dpp v57, v252, v206 row_newbcast:1 row_mask:0xf bank_mask:0xf\n"
"v_fmac_f32_dpp v58, v252, v253 row_newbcast:2 row_mask:0xf bank_mask:0xf\n"
"v_fmac_f32_dpp v59, v252, v251 row_newbcast:3 row_mask:0xf bank_mask:0xf\n"
"v_accvgpr_read_b32 v249, a228\n"
"v_mul_f32_dpp v60, v250, v60 row_newbcast:0 row_mask:0xf bank_mask:0xf\n"
"v_accvgpr_read_b32 v206, a229\n"
"v_mul_f32_dpp v61, v250, v61 row_newbcast:1 row_mask:0xf bank_mask:0xf\n"
"v_accvgpr_read_b32 v253, a230\n"
"v_mul_f32_dpp v62, v250, v62 row_newbcast:2 row_mask:0xf bank_mask:0xf\n"
"v_accvgpr_read_b32 v251, a231\n"
"v_mul_f32_dpp v63, v250, v63 row_newbcast:3 row_mask:0xf bank_mask:0xf\n"
"v_mfma_f32_16x16x32_bf16 v[164:167], v[180:183], a[128:131], 0\n"
"ds_write_b128 v207, v[24:27] offset:0\n"
"v_fmac_f32_dpp v60, v252, v249 row_newbcast:0 row_mask:0xf bank_mask:0xf\n"
"v_fmac_f32_dpp v61, v252, v206 row_newbcast:1 row_mask:0xf bank_mask:0xf\n"
"v_fmac_f32_dpp v62, v252, v253 row_newbcast:2 row_mask:0xf bank_mask:0xf\n"
"v_fmac_f32_dpp v63, v252, v251 row_newbcast:3 row_mask:0xf bank_mask:0xf\n"
"v_cvt_pk_bf16_f32 v56, v56, v57\n"
"v_cvt_pk_bf16_f32 v57, v58, v59\n"
"v_cvt_pk_bf16_f32 v58, v60, v61\n"
"v_cvt_pk_bf16_f32 v59, v62, v63\n"
"v_mfma_f32_16x16x32_bf16 v[164:167], v[184:187], a[132:135], v[164:167]\n"
"ds_write_b128 v207, v[56:59] offset:2048\n"
"v_xor_b32 v235, 0x1000, v231\n"
"v_mfma_f32_16x16x32_bf16 v[164:167], v[188:191], a[136:139], v[164:167]\n"
"ds_read_b64_tr_b16 v[100:101], v235 offset:0\n"
"v_xor_b32 v246, s75, v246\n"
"v_mfma_f32_16x16x32_bf16 v[164:167], v[192:195], a[140:143], v[164:167]\n"
"ds_read_b64_tr_b16 v[102:103], v235 offset:2048\n"
"v_mul_f32_dpp v16, v243, v16 row_newbcast:0 row_mask:0xf bank_mask:0xf\n"
"v_mfma_f32_16x16x32_bf16 v[164:167], v[208:211], a[176:179], v[164:167]\n"
"ds_read_b64_tr_b16 v[108:109], v235 offset:8192\n"
"v_mul_f32_dpp v17, v243, v17 row_newbcast:1 row_mask:0xf bank_mask:0xf\n"
"v_mfma_f32_16x16x32_bf16 v[164:167], v[212:215], a[180:183], v[164:167]\n"
"ds_read_b64_tr_b16 v[110:111], v235 offset:10240\n"
"v_mul_f32_dpp v18, v243, v18 row_newbcast:2 row_mask:0xf bank_mask:0xf\n"
"v_mfma_f32_16x16x32_bf16 v[164:167], v[216:219], a[184:187], v[164:167]\n"
"ds_read_b64_tr_b16 v[116:117], v235 offset:16384\n"
"v_mul_f32_dpp v19, v243, v19 row_newbcast:3 row_mask:0xf bank_mask:0xf\n"
"v_mfma_f32_16x16x32_bf16 v[164:167], v[220:223], a[188:191], v[164:167]\n"
"s_waitcnt lgkmcnt(4)\n"
"ds_read_b64_tr_b16 v[118:119], v235 offset:18432\n"
"v_mul_f32_dpp v20, v243, v20 row_newbcast:0 row_mask:0xf bank_mask:0xf\n"
"v_mfma_f32_16x16x32_bf16 v[136:139], v[96:99], a[96:99], 0\n"
"s_waitcnt lgkmcnt(4)\n"
"s_waitcnt vmcnt(0)\n"
"ds_read_b64_tr_b16 v[224:225], v235 offset:24576\n"
"v_mul_f32_dpp v21, v243, v21 row_newbcast:1 row_mask:0xf bank_mask:0xf\n"
"v_mfma_f32_16x16x32_bf16 v[136:139], v[100:103], a[100:103], v[136:139]\n"
"s_barrier\n"
"ds_read_b64_tr_b16 v[226:227], v235 offset:26624\n"
"v_mfma_f32_16x16x32_bf16 v[136:139], v[104:107], a[104:107], v[136:139]\n"
"s_waitcnt lgkmcnt(4)\n"
"ds_read_b64_tr_b16 v[180:181], v228 offset:32768\n"
"v_mul_f32_dpp v22, v243, v22 row_newbcast:2 row_mask:0xf bank_mask:0xf\n"
"v_mfma_f32_16x16x32_bf16 v[136:139], v[108:111], a[108:111], v[136:139]\n"
"ds_read_b64_tr_b16 v[182:183], v228 offset:34816\n"
"v_mul_f32_dpp v23, v243, v23 row_newbcast:3 row_mask:0xf bank_mask:0xf\n"
"v_mfma_f32_16x16x32_bf16 v[136:139], v[112:115], a[144:147], v[136:139]\n"
"s_waitcnt lgkmcnt(4)\n"
"ds_read_b64_tr_b16 v[184:185], v232 offset:32768\n"
"v_cvt_pk_bf16_f32 v16, v16, v17\n"
"v_mfma_f32_16x16x32_bf16 v[136:139], v[116:119], a[148:151], v[136:139]\n"
"ds_read_b64_tr_b16 v[186:187], v232 offset:34816\n"
"v_cvt_pk_bf16_f32 v17, v18, v19\n"
"v_mfma_f32_16x16x32_bf16 v[136:139], v[120:123], a[152:155], v[136:139]\n"
"s_waitcnt lgkmcnt(4)\n"
"ds_read_b64_tr_b16 v[188:189], v228 offset:40960\n"
"v_cvt_pk_bf16_f32 v18, v20, v21\n"
"v_mfma_f32_16x16x32_bf16 v[136:139], v[224:227], a[156:159], v[136:139]\n"
"ds_read_b64_tr_b16 v[190:191], v228 offset:43008\n"
"v_cvt_pk_bf16_f32 v19, v22, v23\n"
"v_mfma_f32_16x16x32_bf16 v[152:155], v[224:227], a[172:175], 0\n"
"ds_write_b128 v246, v[16:19] offset:49152\n"
"v_mul_f32_dpp v48, v244, v48 row_newbcast:0 row_mask:0xf bank_mask:0xf\n"
"v_mul_f32_dpp v49, v244, v49 row_newbcast:1 row_mask:0xf bank_mask:0xf\n"
"v_mul_f32_dpp v50, v244, v50 row_newbcast:2 row_mask:0xf bank_mask:0xf\n"
"v_mul_f32_dpp v51, v244, v51 row_newbcast:3 row_mask:0xf bank_mask:0xf\n"
"v_mul_f32_dpp v52, v244, v52 row_newbcast:0 row_mask:0xf bank_mask:0xf\n"
"v_mul_f32_dpp v53, v244, v53 row_newbcast:1 row_mask:0xf bank_mask:0xf\n"
"v_mul_f32_dpp v54, v244, v54 row_newbcast:2 row_mask:0xf bank_mask:0xf\n"
"v_mul_f32_dpp v55, v244, v55 row_newbcast:3 row_mask:0xf bank_mask:0xf\n"
"v_cvt_pk_bf16_f32 v48, v48, v49\n"
"v_cvt_pk_bf16_f32 v49, v50, v51\n"
"v_cvt_pk_bf16_f32 v50, v52, v53\n"
"v_cvt_pk_bf16_f32 v51, v54, v55\n"
"v_mfma_f32_16x16x32_bf16 v[152:155], v[120:123], a[168:171], v[152:155]\n"
"ds_write_b128 v246, v[48:51] offset:50176\n"
"v_mul_f32_dpp v80, v245, v80 row_newbcast:0 row_mask:0xf bank_mask:0xf\n"
"v_mul_f32_dpp v81, v245, v81 row_newbcast:1 row_mask:0xf bank_mask:0xf\n"
"v_mul_f32_dpp v82, v245, v82 row_newbcast:2 row_mask:0xf bank_mask:0xf\n"
"v_mul_f32_dpp v83, v245, v83 row_newbcast:3 row_mask:0xf bank_mask:0xf\n"
"v_mul_f32_dpp v84, v245, v84 row_newbcast:0 row_mask:0xf bank_mask:0xf\n"
"v_mul_f32_dpp v85, v245, v85 row_newbcast:1 row_mask:0xf bank_mask:0xf\n"
"v_mul_f32_dpp v86, v245, v86 row_newbcast:2 row_mask:0xf bank_mask:0xf\n"
"v_mul_f32_dpp v87, v245, v87 row_newbcast:3 row_mask:0xf bank_mask:0xf\n"
"v_cvt_pk_bf16_f32 v80, v80, v81\n"
"v_cvt_pk_bf16_f32 v81, v82, v83\n"
"v_cvt_pk_bf16_f32 v82, v84, v85\n"
"v_cvt_pk_bf16_f32 v83, v86, v87\n"
"v_mfma_f32_16x16x32_bf16 v[152:155], v[116:119], a[164:167], v[152:155]\n"
"ds_write_b128 v246, v[80:83] offset:51200\n"
"v_xor_b32 v247, s75, v247\n"
"v_mfma_f32_16x16x32_bf16 v[152:155], v[112:115], a[160:163], v[152:155]\n"
"ds_read_b64_tr_b16 a[8:9], v247 offset:49152\n"
"ds_read_b64_tr_b16 a[10:11], v247 offset:49160\n"
"v_mfma_f32_16x16x32_bf16 v[152:155], v[108:111], a[124:127], v[152:155]\n"
"ds_read_b64_tr_b16 a[24:25], v247 offset:50176\n"
"ds_read_b64_tr_b16 a[26:27], v247 offset:50184\n"
"v_mfma_f32_16x16x32_bf16 v[152:155], v[104:107], a[120:123], v[152:155]\n"
"ds_read_b64_tr_b16 a[40:41], v247 offset:51200\n"
"ds_read_b64_tr_b16 a[42:43], v247 offset:51208\n"
"v_mfma_f32_16x16x32_bf16 v[152:155], v[100:103], a[116:119], v[152:155]\n"
"ds_read_b64_tr_b16 v[192:193], v232 offset:40960\n"
"v_and_b32 v243, 0x3f0, v242\n"
"v_mfma_f32_16x16x32_bf16 v[152:155], v[96:99], a[112:115], v[152:155]\n"
"ds_read_b64_tr_b16 v[194:195], v232 offset:43008\n"
"v_mov_b32 v196, 0\n"
"v_mfma_f32_16x16x32_bf16 v[168:171], v[96:99], a[128:131], 0\n"
"ds_read_b64_tr_b16 v[208:209], v228 offset:49152\n"
"v_mov_b32 v197, 0\n"
"v_mfma_f32_16x16x32_bf16 v[168:171], v[100:103], a[132:135], v[168:171]\n"
"ds_read_b64_tr_b16 v[210:211], v228 offset:51200\n"
"v_mov_b32 v198, 0\n"
"v_mfma_f32_16x16x32_bf16 v[168:171], v[104:107], a[136:139], v[168:171]\n"
"ds_read_b64_tr_b16 v[212:213], v232 offset:49152\n"
"v_mov_b32 v199, 0\n"
"v_mfma_f32_16x16x32_bf16 v[168:171], v[108:111], a[140:143], v[168:171]\n"
"s_waitcnt lgkmcnt(4)\n"
"ds_read_b64_tr_b16 v[214:215], v232 offset:51200\n"
"v_mov_b32 v200, 0\n"
"v_mfma_f32_16x16x32_bf16 v[168:171], v[112:115], a[176:179], v[168:171]\n"
"ds_read_b64_tr_b16 v[216:217], v228 offset:57344\n"
"v_mov_b32 v201, 0\n"
"v_mfma_f32_16x16x32_bf16 v[168:171], v[116:119], a[180:183], v[168:171]\n"
"ds_read_b64_tr_b16 v[218:219], v228 offset:59392\n"
"v_xor_b32 v228, s75, v228\n"
"v_mfma_f32_16x16x32_bf16 v[168:171], v[120:123], a[184:187], v[168:171]\n"
"s_add_u32 m0, s71, 32768\n"
"ds_read_b64_tr_b16 v[220:221], v232 offset:57344\n"
"v_mov_b32 v202, 0\n"
"v_mfma_f32_16x16x32_bf16 v[168:171], v[224:227], a[188:191], v[168:171]\n"
"s_add_u32 s41, s70, 327680\n"
"buffer_load_dwordx4 v240, s[28:31], s41 offen lds\n"
"v_mfma_f32_16x16x32_bf16 v[124:127], v[180:183], a[48:51], v[124:127]\n"
"ds_read_b64_tr_b16 v[222:223], v232 offset:59392\n"
"v_xor_b32 v232, s75, v232\n"
"v_mfma_f32_16x16x32_bf16 v[124:127], v[184:187], a[52:55], v[124:127]\n"
"ds_read_b64_tr_b16 a[228:229], v237 offset:14336\n"
"v_mov_b32 v203, 0\n"
"v_mfma_f32_16x16x32_bf16 v[124:127], v[188:191], a[56:59], v[124:127]\n"
"s_waitcnt lgkmcnt(3)\n"
"ds_read_b64_tr_b16 a[230:231], v237 offset:14344\n"
"v_xor_b32 v246, s75, v246\n"
"v_mfma_f32_16x16x32_bf16 v[124:127], v[192:195], a[60:63], v[124:127]\n"
"s_add_u32 s40, s41, s52\n"
"buffer_load_dwordx4 v240, s[28:31], s40 offen offset:1024 lds\n"
"v_mfma_f32_16x16x32_bf16 v[124:127], v[208:211], a[0:3], v[124:127]\n"
"ds_read_b64_tr_b16 a[224:225], v236 offset:14336\n"
"v_xor_b32 v247, s75, v247\n"
"v_mfma_f32_16x16x32_bf16 v[124:127], v[212:215], a[4:7], v[124:127]\n"
"ds_read_b64_tr_b16 a[226:227], v236 offset:14344\n"
"v_bfe_u32 v254, v242, 4, 4\n"
"v_mfma_f32_16x16x32_bf16 v[124:127], v[216:219], a[8:11], v[124:127]\n"
"s_waitcnt lgkmcnt(3)\n"
"ds_read_b64_tr_b16 a[212:213], v237 offset:12288\n"
"v_mul_u32_u24 v254, 2560, v254\n"
"v_mfma_f32_16x16x32_bf16 v[124:127], v[220:223], a[12:15], v[124:127]\n"
"s_add_u32 s40, s41, s53\n"
"buffer_load_dwordx4 v240, s[28:31], s40 offen offset:2048 lds\n"
"v_mfma_f32_16x16x32_bf16 v[140:143], v[220:223], a[28:31], v[140:143]\n"
"ds_read_b64_tr_b16 a[214:215], v237 offset:12296\n"
"v_add_u32 v237, s95, v243\n"
"v_mfma_f32_16x16x32_bf16 v[140:143], v[216:219], a[24:27], v[140:143]\n"
"ds_read_b64_tr_b16 a[208:209], v236 offset:12288\n"
"v_bfe_u32 v255, v242, 8, 1\n"
"v_mfma_f32_16x16x32_bf16 v[140:143], v[212:215], a[20:23], v[140:143]\n"
"ds_read_b64_tr_b16 a[210:211], v236 offset:12296\n"
"v_add_u32 v236, s94, v243\n"
"v_mfma_f32_16x16x32_bf16 v[140:143], v[208:211], a[16:19], v[140:143]\n"
"ds_read_b64_tr_b16 v[96:97], v229 offset:32768\n"
"v_add_u32 v243, s96, v243\n"
"v_mfma_f32_16x16x32_bf16 v[140:143], v[192:195], a[76:79], v[140:143]\n"
"s_add_u32 s40, s41, s54\n"
"buffer_load_dwordx4 v240, s[28:31], s40 offen offset:3072 lds\n"
"v_mfma_f32_16x16x32_bf16 v[140:143], v[188:191], a[72:75], v[140:143]\n"
"ds_read_b64_tr_b16 v[98:99], v229 offset:34816\n"
"v_cvt_pk_bf16_f32 v124, v124, v125\n"
"v_mfma_f32_16x16x32_bf16 v[140:143], v[184:187], a[68:71], v[140:143]\n"
"ds_read_b64_tr_b16 v[100:101], v233 offset:32768\n"
"v_cvt_pk_bf16_f32 v125, v126, v127\n"
"v_mfma_f32_16x16x32_bf16 v[140:143], v[180:183], a[64:67], v[140:143]\n"
"s_add_u32 m0, s71, 49152\n"
"ds_read_b64_tr_b16 v[102:103], v233 offset:34816\n"
"v_lshl_add_u32 v254, v255, 5, v254\n"
"v_mfma_f32_16x16x32_bf16 v[156:159], v[180:183], a[80:83], v[156:159]\n"
"s_add_u32 s41, s70, 491520\n"
"buffer_load_dwordx4 v240, s[28:31], s41 offen lds\n"
"v_mfma_f32_16x16x32_bf16 v[156:159], v[184:187], a[84:87], v[156:159]\n"
"ds_read_b64_tr_b16 v[104:105], v229 offset:40960\n"
"v_bfe_u32 v255, v242, 9, 1\n"
"v_mfma_f32_16x16x32_bf16 v[156:159], v[188:191], a[88:91], v[156:159]\n"
"ds_read_b64_tr_b16 v[106:107], v229 offset:43008\n"
"v_lshl_add_u32 v254, v255, 4, v254\n"
"v_mfma_f32_16x16x32_bf16 v[156:159], v[192:195], a[92:95], v[156:159]\n"
"ds_read_b64_tr_b16 v[108:109], v233 offset:40960\n"
"v_add_u32 v238, s97, v254\n"
"v_mfma_f32_16x16x32_bf16 v[156:159], v[208:211], a[32:35], v[156:159]\n"
"s_add_u32 s40, s41, s52\n"
"buffer_load_dwordx4 v240, s[28:31], s40 offen offset:1024 lds\n"
"v_mfma_f32_16x16x32_bf16 v[156:159], v[212:215], a[36:39], v[156:159]\n"
"ds_read_b64_tr_b16 v[110:111], v233 offset:43008\n"
"v_cvt_pk_bf16_f32 v140, v140, v141\n"
"v_mfma_f32_16x16x32_bf16 v[156:159], v[216:219], a[40:43], v[156:159]\n"
"ds_read_b64_tr_b16 v[112:113], v229 offset:49152\n"
"v_cvt_pk_bf16_f32 v141, v142, v143\n"
"v_mfma_f32_16x16x32_bf16 v[156:159], v[220:223], a[44:47], v[156:159]\n"
"s_waitcnt lgkmcnt(3)\n"
"ds_read_b64_tr_b16 v[114:115], v229 offset:51200\n"
"v_xor_b32 v239, 64, v238\n"
"v_mfma_f32_16x16x32_bf16 v[172:175], v[220:223], a[228:231], v[172:175]\n"
"ds_read_b64_tr_b16 v[116:117], v233 offset:49152\n"
"v_add_u32 v252, s98, v254\n"
"v_mfma_f32_16x16x32_bf16 v[172:175], v[216:219], a[224:227], v[172:175]\n"
"s_add_u32 s40, s41, s53\n"
"buffer_load_dwordx4 v240, s[28:31], s40 offen offset:2048 lds\n"
"v_mfma_f32_16x16x32_bf16 v[172:175], v[212:215], a[212:215], v[172:175]\n"
"ds_read_b64_tr_b16 v[118:119], v233 offset:51200\n"
"v_add_u32 v253, s99, v254\n"
"v_mfma_f32_16x16x32_bf16 v[172:175], v[208:211], a[208:211], v[172:175]\n"
"ds_read_b64_tr_b16 v[120:121], v229 offset:57344\n"
"v_add_u32 v254, s100, v254\n"
"v_mfma_f32_16x16x32_bf16 v[172:175], v[192:195], a[236:239], v[172:175]\n"
"ds_read_b64_tr_b16 v[122:123], v229 offset:59392\n"
"v_xor_b32 v229, s75, v229\n"
"v_mfma_f32_16x16x32_bf16 v[172:175], v[188:191], a[232:235], v[172:175]\n"
"s_add_u32 s40, s41, s54\n"
"buffer_load_dwordx4 v240, s[28:31], s40 offen offset:3072 lds\n"
"v_mfma_f32_16x16x32_bf16 v[172:175], v[184:187], a[220:223], v[172:175]\n"
"s_mov_b32 s62, 258048\n"
"ds_read_b64_tr_b16 v[224:225], v233 offset:57344\n"
"v_cvt_pk_bf16_f32 v156, v156, v157\n"
"v_mfma_f32_16x16x32_bf16 v[172:175], v[180:183], a[216:219], v[172:175]\n"
"s_add_u32 s70, s61, s47\n"
"ds_read_b64_tr_b16 v[226:227], v233 offset:59392\n"
"v_cvt_pk_bf16_f32 v157, v158, v159\n"
"v_mfma_f32_16x16x32_bf16 v[128:131], v[96:99], a[48:51], v[128:131]\n"
"s_add_u32 s70, s70, 1\n"
"ds_read_b64_tr_b16 v[180:181], v230 offset:32768\n"
"v_xor_b32 v233, s75, v233\n"
"v_mfma_f32_16x16x32_bf16 v[128:131], v[100:103], a[52:55], v[128:131]\n"
"s_and_b32 s70, s70, 7\n"
"ds_read_b64_tr_b16 v[182:183], v230 offset:34816\n"
"v_mov_b32 v0, 0\n"
"v_mfma_f32_16x16x32_bf16 v[128:131], v[104:107], a[56:59], v[128:131]\n"
"s_waitcnt lgkmcnt(4)\n"
"s_lshl_b32 s70, s70, 7\n"
"ds_read_b64_tr_b16 v[184:185], v234 offset:32768\n"
"v_mov_b32 v1, 0\n"
"v_mfma_f32_16x16x32_bf16 v[128:131], v[108:111], a[60:63], v[128:131]\n"
"s_xor_b32 s71, s71, s75\n"
"ds_read_b64_tr_b16 v[186:187], v234 offset:34816\n"
"v_mov_b32 v2, 0\n"
"v_mfma_f32_16x16x32_bf16 v[128:131], v[112:115], a[0:3], v[128:131]\n"
"v_lshlrev_b32_e64 v245, 2, s72\n"
"ds_read_b64_tr_b16 v[188:189], v230 offset:40960\n"
"v_mov_b32 v3, 0\n"
"v_mfma_f32_16x16x32_bf16 v[128:131], v[116:119], a[4:7], v[128:131]\n"
"global_load_dword v244, v245, s[6:7] sc1\n"
"v_mfma_f32_16x16x32_bf16 v[128:131], v[120:123], a[8:11], v[128:131]\n"
"s_waitcnt lgkmcnt(3)\n"
"ds_read_b64_tr_b16 v[190:191], v230 offset:43008\n"
"v_mov_b32 v4, 0\n"
"v_mfma_f32_16x16x32_bf16 v[128:131], v[224:227], a[12:15], v[128:131]\n"
"ds_read_b64_tr_b16 v[192:193], v234 offset:40960\n"
"v_mov_b32 v5, 0\n"
"v_mfma_f32_16x16x32_bf16 v[144:147], v[224:227], a[28:31], v[144:147]\n"
"ds_read_b64_tr_b16 v[194:195], v234 offset:43008\n"
"v_mov_b32 v6, 0\n"
"v_mfma_f32_16x16x32_bf16 v[144:147], v[120:123], a[24:27], v[144:147]\n"
"ds_read_b64_tr_b16 v[208:209], v230 offset:49152\n"
"v_mov_b32 v7, 0\n"
"v_mfma_f32_16x16x32_bf16 v[144:147], v[116:119], a[20:23], v[144:147]\n"
"ds_read_b64_tr_b16 v[210:211], v230 offset:51200\n"
"v_mov_b32 v8, 0\n"
"v_mfma_f32_16x16x32_bf16 v[144:147], v[112:115], a[16:19], v[144:147]\n"
"ds_read_b64_tr_b16 v[212:213], v234 offset:49152\n"
"v_mov_b32 v9, 0\n"
"v_mfma_f32_16x16x32_bf16 v[144:147], v[108:111], a[76:79], v[144:147]\n"
"ds_read_b64_tr_b16 v[214:215], v234 offset:51200\n"
"v_mov_b32 v10, 0\n"
"v_mfma_f32_16x16x32_bf16 v[144:147], v[104:107], a[72:75], v[144:147]\n"
"ds_read_b64_tr_b16 v[216:217], v230 offset:57344\n"
"v_cvt_pk_bf16_f32 v126, v128, v129\n"
"v_mfma_f32_16x16x32_bf16 v[144:147], v[100:103], a[68:71], v[144:147]\n"
"ds_read_b64_tr_b16 v[218:219], v230 offset:59392\n"
"v_cvt_pk_bf16_f32 v127, v130, v131\n"
"v_mfma_f32_16x16x32_bf16 v[144:147], v[96:99], a[64:67], v[144:147]\n"
"ds_read_b64_tr_b16 v[220:221], v234 offset:57344\n"
"v_mov_b32 v11, 0\n"
"v_mfma_f32_16x16x32_bf16 v[160:163], v[96:99], a[80:83], v[160:163]\n"
"ds_read_b64_tr_b16 v[222:223], v234 offset:59392\n"
"v_mov_b32 v12, 0\n"
"v_mfma_f32_16x16x32_bf16 v[160:163], v[100:103], a[84:87], v[160:163]\n"
"buffer_store_dwordx4 v[124:127], v243, s[32:35], s16 offen sc1\n"
"v_mov_b32 v13, 0\n"
"v_mfma_f32_16x16x32_bf16 v[160:163], v[104:107], a[88:91], v[160:163]\n"
"v_mov_b32 v14, 0\n"
"v_mov_b32 v15, 0\n"
"v_mfma_f32_16x16x32_bf16 v[160:163], v[108:111], a[92:95], v[160:163]\n"
"v_mov_b32 v16, 0\n"
"v_mov_b32 v17, 0\n"
"v_mfma_f32_16x16x32_bf16 v[160:163], v[112:115], a[32:35], v[160:163]\n"
"v_mov_b32 v18, 0\n"
"v_mov_b32 v19, 0\n"
"v_mfma_f32_16x16x32_bf16 v[160:163], v[116:119], a[36:39], v[160:163]\n"
"v_cvt_pk_bf16_f32 v142, v144, v145\n"
"v_cvt_pk_bf16_f32 v143, v146, v147\n"
"v_mfma_f32_16x16x32_bf16 v[160:163], v[120:123], a[40:43], v[160:163]\n"
"v_mov_b32 v20, 0\n"
"v_mov_b32 v21, 0\n"
"v_mfma_f32_16x16x32_bf16 v[160:163], v[224:227], a[44:47], v[160:163]\n"
"v_mov_b32 v22, 0\n"
"v_mov_b32 v23, 0\n"
"v_mfma_f32_16x16x32_bf16 v[176:179], v[224:227], a[228:231], v[176:179]\n"
"ds_read_b64_tr_b16 v[224:225], v235 offset:57344\n"
"v_mov_b32 v24, 0\n"
"v_mfma_f32_16x16x32_bf16 v[176:179], v[120:123], a[224:227], v[176:179]\n"
"ds_read_b64_tr_b16 v[120:121], v231 offset:57344\n"
"v_mov_b32 v25, 0\n"
"v_mfma_f32_16x16x32_bf16 v[176:179], v[116:119], a[212:215], v[176:179]\n"
"ds_read_b64_tr_b16 v[116:117], v235 offset:49152\n"
"v_mov_b32 v26, 0\n"
"v_mfma_f32_16x16x32_bf16 v[176:179], v[112:115], a[208:211], v[176:179]\n"
"ds_read_b64_tr_b16 v[112:113], v231 offset:49152\n"
"v_mov_b32 v27, 0\n"
"v_mfma_f32_16x16x32_bf16 v[176:179], v[108:111], a[236:239], v[176:179]\n"
"ds_read_b64_tr_b16 v[108:109], v235 offset:40960\n"
"v_mov_b32 v28, 0\n"
"v_mfma_f32_16x16x32_bf16 v[176:179], v[104:107], a[232:235], v[176:179]\n"
"ds_read_b64_tr_b16 v[104:105], v231 offset:40960\n"
"v_cvt_pk_bf16_f32 v158, v160, v161\n"
"v_mfma_f32_16x16x32_bf16 v[176:179], v[100:103], a[220:223], v[176:179]\n"
"ds_read_b64_tr_b16 v[100:101], v235 offset:32768\n"
"v_cvt_pk_bf16_f32 v159, v162, v163\n"
"v_mfma_f32_16x16x32_bf16 v[176:179], v[96:99], a[216:219], v[176:179]\n"
"ds_read_b64_tr_b16 v[96:97], v231 offset:32768\n"
"v_mov_b32 v29, 0\n"
"v_mfma_f32_16x16x32_bf16 v[132:135], v[180:183], a[48:51], v[132:135]\n"
"s_waitcnt lgkmcnt(4)\n"
"ds_read_b64_tr_b16 v[98:99], v231 offset:34816\n"
"v_mov_b32 v30, 0\n"
"v_mfma_f32_16x16x32_bf16 v[132:135], v[184:187], a[52:55], v[132:135]\n"
"ds_read_b64_tr_b16 v[102:103], v235 offset:34816\n"
"v_mov_b32 v31, 0\n"
"v_mfma_f32_16x16x32_bf16 v[132:135], v[188:191], a[56:59], v[132:135]\n"
"ds_read_b64_tr_b16 v[106:107], v231 offset:43008\n"
"v_mov_b32 v32, 0\n"
"v_mfma_f32_16x16x32_bf16 v[132:135], v[192:195], a[60:63], v[132:135]\n"
"ds_read_b64_tr_b16 v[110:111], v235 offset:43008\n"
"v_mov_b32 v33, 0\n"
"v_mfma_f32_16x16x32_bf16 v[132:135], v[208:211], a[0:3], v[132:135]\n"
"ds_read_b64_tr_b16 v[114:115], v231 offset:51200\n"
"v_mov_b32 v34, 0\n"
"v_mfma_f32_16x16x32_bf16 v[132:135], v[212:215], a[4:7], v[132:135]\n"
"ds_read_b64_tr_b16 v[118:119], v235 offset:51200\n"
"v_mov_b32 v35, 0\n"
"v_mfma_f32_16x16x32_bf16 v[132:135], v[216:219], a[8:11], v[132:135]\n"
"ds_read_b64_tr_b16 v[122:123], v231 offset:59392\n"
"v_mov_b32 v36, 0\n"
"v_mfma_f32_16x16x32_bf16 v[132:135], v[220:223], a[12:15], v[132:135]\n"
"buffer_store_dwordx4 v[140:143], v243, s[32:35], s16 offen offset:2048 sc1\n"
"v_mov_b32 v37, 0\n"
"v_mfma_f32_16x16x32_bf16 v[148:151], v[220:223], a[28:31], v[148:151]\n"
"ds_read_b64_tr_b16 v[226:227], v235 offset:59392\n"
"v_mov_b32 v38, 0\n"
"v_mfma_f32_16x16x32_bf16 v[148:151], v[216:219], a[24:27], v[148:151]\n"
"v_mov_b32 v39, 0\n"
"v_mov_b32 v40, 0\n"
"v_mfma_f32_16x16x32_bf16 v[148:151], v[212:215], a[20:23], v[148:151]\n"
"v_mov_b32 v41, 0\n"
"v_mov_b32 v42, 0\n"
"v_mfma_f32_16x16x32_bf16 v[148:151], v[208:211], a[16:19], v[148:151]\n"
"v_mov_b32 v43, 0\n"
"v_mov_b32 v44, 0\n"
"v_mfma_f32_16x16x32_bf16 v[148:151], v[192:195], a[76:79], v[148:151]\n"
"v_mov_b32 v45, 0\n"
"v_mov_b32 v46, 0\n"
"v_mfma_f32_16x16x32_bf16 v[148:151], v[188:191], a[72:75], v[148:151]\n"
"v_mov_b32 v47, 0\n"
"v_mov_b32 v48, 0\n"
"v_mfma_f32_16x16x32_bf16 v[148:151], v[184:187], a[68:71], v[148:151]\n"
"v_mov_b32 v49, 0\n"
"v_mov_b32 v50, 0\n"
"v_mfma_f32_16x16x32_bf16 v[148:151], v[180:183], a[64:67], v[148:151]\n"
"v_mov_b32 v51, 0\n"
"v_mov_b32 v52, 0\n"
"v_mfma_f32_16x16x32_bf16 v[164:167], v[180:183], a[80:83], v[164:167]\n"
"v_mov_b32 v53, 0\n"
"v_mov_b32 v54, 0\n"
"v_mfma_f32_16x16x32_bf16 v[164:167], v[184:187], a[84:87], v[164:167]\n"
"v_mov_b32 v55, 0\n"
"v_mov_b32 v56, 0\n"
"v_mfma_f32_16x16x32_bf16 v[164:167], v[188:191], a[88:91], v[164:167]\n"
"v_mov_b32 v57, 0\n"
"v_mov_b32 v58, 0\n"
"v_mfma_f32_16x16x32_bf16 v[164:167], v[192:195], a[92:95], v[164:167]\n"
"v_mov_b32 v59, 0\n"
"v_mov_b32 v60, 0\n"
"v_mfma_f32_16x16x32_bf16 v[164:167], v[208:211], a[32:35], v[164:167]\n"
"v_mov_b32 v61, 0\n"
"v_mov_b32 v62, 0\n"
"v_mfma_f32_16x16x32_bf16 v[164:167], v[212:215], a[36:39], v[164:167]\n"
"v_mov_b32 v63, 0\n"
"v_mov_b32 v64, 0\n"
"v_mfma_f32_16x16x32_bf16 v[164:167], v[216:219], a[40:43], v[164:167]\n"
"v_mov_b32 v65, 0\n"
"v_mov_b32 v66, 0\n"
"v_mfma_f32_16x16x32_bf16 v[164:167], v[220:223], a[44:47], v[164:167]\n"
"s_waitcnt lgkmcnt(0)\n"
"v_mov_b32 v67, 0\n"
"v_mov_b32 v68, 0\n"
"v_mfma_f32_16x16x32_bf16 v[136:139], v[96:99], a[48:51], v[136:139]\n"
"v_mov_b32 v69, 0\n"
"v_mov_b32 v70, 0\n"
"v_mfma_f32_16x16x32_bf16 v[136:139], v[100:103], a[52:55], v[136:139]\n"
"s_barrier\n"
"ds_read_b64_tr_b16 v[180:181], v228 offset:0\n"
"v_mfma_f32_16x16x32_bf16 v[136:139], v[104:107], a[56:59], v[136:139]\n"
"ds_read_b64_tr_b16 v[182:183], v228 offset:2048\n"
"v_mov_b32 v71, 0\n"
"v_mfma_f32_16x16x32_bf16 v[136:139], v[108:111], a[60:63], v[136:139]\n"
"ds_read_b64_tr_b16 v[184:185], v232 offset:0\n"
"v_mov_b32 v92, 0\n"
"v_mfma_f32_16x16x32_bf16 v[136:139], v[112:115], a[0:3], v[136:139]\n"
"ds_read_b64_tr_b16 v[186:187], v232 offset:2048\n"
"v_mov_b32 v93, 0\n"
"v_mfma_f32_16x16x32_bf16 v[136:139], v[116:119], a[4:7], v[136:139]\n"
"buffer_store_dwordx4 v[156:159], v243, s[32:35], s17 offen sc1\n"
"v_mov_b32 v94, 0\n"
"v_mfma_f32_16x16x32_bf16 v[136:139], v[120:123], a[8:11], v[136:139]\n"
"ds_read_b64_tr_b16 v[188:189], v228 offset:8192\n"
"v_mov_b32 v95, 0\n"
"v_mfma_f32_16x16x32_bf16 v[136:139], v[224:227], a[12:15], v[136:139]\n"
"ds_read_b64_tr_b16 v[190:191], v228 offset:10240\n"
"v_mov_b32 v196, 0\n"
"v_mfma_f32_16x16x32_bf16 v[152:155], v[224:227], a[28:31], v[152:155]\n"
"ds_read_b64_tr_b16 v[192:193], v232 offset:8192\n"
"v_mov_b32 v197, 0\n"
"v_mfma_f32_16x16x32_bf16 v[152:155], v[120:123], a[24:27], v[152:155]\n"
"ds_read_b64_tr_b16 v[194:195], v232 offset:10240\n"
"v_mov_b32 v198, 0\n"
"v_mfma_f32_16x16x32_bf16 v[152:155], v[116:119], a[20:23], v[152:155]\n"
"ds_read_b64_tr_b16 v[208:209], v228 offset:16384\n"
"v_mov_b32 v199, 0\n"
"v_mfma_f32_16x16x32_bf16 v[152:155], v[112:115], a[16:19], v[152:155]\n"
"ds_read_b64_tr_b16 v[210:211], v228 offset:18432\n"
"v_mov_b32 v200, 0\n"
"v_mfma_f32_16x16x32_bf16 v[152:155], v[108:111], a[76:79], v[152:155]\n"
"ds_read_b64_tr_b16 v[212:213], v232 offset:16384\n"
"v_mov_b32 v201, 0\n"
"v_mfma_f32_16x16x32_bf16 v[152:155], v[104:107], a[72:75], v[152:155]\n"
"ds_read_b64_tr_b16 v[214:215], v232 offset:18432\n"
"v_mov_b32 v202, 0\n"
"v_mfma_f32_16x16x32_bf16 v[152:155], v[100:103], a[68:71], v[152:155]\n"
"ds_read_b64_tr_b16 v[216:217], v228 offset:24576\n"
"v_mov_b32 v203, 0\n"
"v_mfma_f32_16x16x32_bf16 v[152:155], v[96:99], a[64:67], v[152:155]\n"
"ds_read_b64_tr_b16 v[218:219], v228 offset:26624\n"
"v_mov_b32 v204, 0\n"
"v_mfma_f32_16x16x32_bf16 v[168:171], v[96:99], a[80:83], v[168:171]\n"
"ds_read_b64_tr_b16 v[220:221], v232 offset:24576\n"
"v_mov_b32 v205, 0\n"
"v_mfma_f32_16x16x32_bf16 v[168:171], v[100:103], a[84:87], v[168:171]\n"
"ds_read_b64_tr_b16 v[222:223], v232 offset:26624\n"
"v_mov_b32 v206, 0\n"
"v_mfma_f32_16x16x32_bf16 v[168:171], v[104:107], a[88:91], v[168:171]\n"
"ds_read_b64_tr_b16 v[96:97], v229 offset:0\n"
"v_mov_b32 v207, 0\n"
"v_mfma_f32_16x16x32_bf16 v[168:171], v[108:111], a[92:95], v[168:171]\n"
"ds_read_b64_tr_b16 v[98:99], v229 offset:2048\n"
"v_cvt_pk_bf16_f32 v172, v172, v173\n"
"v_mfma_f32_16x16x32_bf16 v[168:171], v[112:115], a[32:35], v[168:171]\n"
"ds_read_b64_tr_b16 v[100:101], v233 offset:0\n"
"v_cvt_pk_bf16_f32 v173, v174, v175\n"
"v_mfma_f32_16x16x32_bf16 v[168:171], v[116:119], a[36:39], v[168:171]\n"
"ds_read_b64_tr_b16 v[102:103], v233 offset:2048\n"
"v_cvt_pk_bf16_f32 v174, v176, v177\n"
"v_mfma_f32_16x16x32_bf16 v[168:171], v[120:123], a[40:43], v[168:171]\n"
"s_add_u32 s47, s47, 1\n"
"v_cvt_pk_bf16_f32 v175, v178, v179\n"
"v_cvt_pk_bf16_f32 v132, v132, v133\n"
"v_mfma_f32_16x16x32_bf16 v[168:171], v[224:227], a[44:47], v[168:171]\n"
"v_cvt_pk_bf16_f32 v133, v134, v135\n"
"v_cvt_pk_bf16_f32 v148, v148, v149\n"
"v_cvt_pk_bf16_f32 v149, v150, v151\n"
"v_cvt_pk_bf16_f32 v164, v164, v165\n"
"v_cvt_pk_bf16_f32 v165, v166, v167\n"
"v_xor_b32 v230, s75, v230\n"
"v_xor_b32 v234, s75, v234\n"
"LGL_%=:\n"
"s_waitcnt lgkmcnt(3)\n"
"v_mfma_f32_16x16x32_bf16 v[124:127], v[180:183], a[96:99], 0\n"
"s_mov_b32 m0, s71\n"
"ds_read_b64_tr_b16 v[104:105], v229 offset:8192\n"
"v_cvt_pk_bf16_f32 v134, v136, v137\n"
"v_mfma_f32_16x16x32_bf16 v[124:127], v[184:187], a[100:103], v[124:127]\n"
"s_add_u32 s41, s70, 0\n"
"buffer_load_dwordx4 v240, s[28:31], s41 offen lds\n"
"v_mfma_f32_16x16x32_bf16 v[124:127], v[188:191], a[104:107], v[124:127]\n"
"ds_read_b64_tr_b16 v[106:107], v229 offset:10240\n"
"v_cvt_pk_bf16_f32 v135, v138, v139\n"
"v_mfma_f32_16x16x32_bf16 v[124:127], v[192:195], a[108:111], v[124:127]\n"
"buffer_store_dwordx4 v[172:175], v243, s[32:35], s17 offen offset:2048 sc1\n"
"v_cvt_pk_bf16_f32 v150, v152, v153\n"
"v_mfma_f32_16x16x32_bf16 v[124:127], v[208:211], a[144:147], v[124:127]\n"
"ds_read_b64_tr_b16 v[108:109], v233 offset:8192\n"
"v_cvt_pk_bf16_f32 v151, v154, v155\n"
"v_mfma_f32_16x16x32_bf16 v[124:127], v[212:215], a[148:151], v[124:127]\n"
"s_waitcnt vmcnt(13)\n"
"ds_read_b64_tr_b16 v[110:111], v233 offset:10240\n"
"v_cvt_pk_bf16_f32 v166, v168, v169\n"
"v_mfma_f32_16x16x32_bf16 v[124:127], v[216:219], a[152:155], v[124:127]\n"
"s_waitcnt lgkmcnt(3)\n"
"ds_read_b64_tr_b16 v[112:113], v229 offset:16384\n"
"v_cvt_pk_bf16_f32 v167, v170, v171\n"
"v_mfma_f32_16x16x32_bf16 v[124:127], v[220:223], a[156:159], v[124:127]\n"
"ds_read_b64_tr_b16 v[114:115], v229 offset:18432\n"
"v_xor_b32 v231, s75, v231\n"
"v_mfma_f32_16x16x32_bf16 v[140:143], v[220:223], a[172:175], 0\n"
"ds_read_b64_tr_b16 v[116:117], v233 offset:16384\n"
"v_xor_b32 v235, s75, v235\n"
"v_mfma_f32_16x16x32_bf16 v[140:143], v[216:219], a[168:171], v[140:143]\n"
"s_add_u32 s40, s41, s52\n"
"buffer_load_dwordx4 v240, s[28:31], s40 offen offset:1024 lds\n"
"v_mfma_f32_16x16x32_bf16 v[140:143], v[212:215], a[164:167], v[140:143]\n"
"ds_read_b64_tr_b16 v[118:119], v233 offset:18432\n"
"v_add_u32 v241, 1, v241\n"
"v_mfma_f32_16x16x32_bf16 v[140:143], v[208:211], a[160:163], v[140:143]\n"
"ds_read_b64_tr_b16 v[120:121], v229 offset:24576\n"
"v_lshlrev_b32 v248, 16, v92\n"
"v_mfma_f32_16x16x32_bf16 v[140:143], v[192:195], a[124:127], v[140:143]\n"
"ds_read_b64_tr_b16 v[122:123], v229 offset:26624\n"
"v_and_b32 v92, s73, v92\n"
"v_mfma_f32_16x16x32_bf16 v[140:143], v[188:191], a[120:123], v[140:143]\n"
"ds_read_b64_tr_b16 v[224:225], v233 offset:24576\n"
"v_add_f32 v41, v41, v92\n"
"v_mfma_f32_16x16x32_bf16 v[140:143], v[184:187], a[116:119], v[140:143]\n"
"ds_read_b64_tr_b16 v[226:227], v233 offset:26624\n"
"v_lshlrev_b32 v249, 16, v93\n"
"v_mfma_f32_16x16x32_bf16 v[140:143], v[180:183], a[112:115], v[140:143]\n"
"v_and_b32 v93, s73, v93\n"
"v_add_f32 v43, v43, v93\n"
"v_mfma_f32_16x16x32_bf16 v[156:159], v[180:183], a[128:131], 0\n"
"v_lshlrev_b32 v250, 16, v94\n"
"v_and_b32 v94, s73, v94\n"
"v_mfma_f32_16x16x32_bf16 v[156:159], v[184:187], a[132:135], v[156:159]\n"
"s_add_u32 s40, s41, s53\n"
"buffer_load_dwordx4 v240, s[28:31], s40 offen offset:2048 lds\n"
"v_mfma_f32_16x16x32_bf16 v[156:159], v[188:191], a[136:139], v[156:159]\n"
"v_add_f32 v45, v45, v94\n"
"v_lshlrev_b32 v251, 16, v95\n"
"v_mfma_f32_16x16x32_bf16 v[156:159], v[192:195], a[140:143], v[156:159]\n"
"v_and_b32 v95, s73, v95\n"
"v_add_f32 v47, v47, v95\n"
"v_mfma_f32_16x16x32_bf16 v[156:159], v[208:211], a[176:179], v[156:159]\n"
"v_add_f32 v40, v40, v248\n"
"v_add_f32 v42, v42, v249\n"
"v_mfma_f32_16x16x32_bf16 v[156:159], v[212:215], a[180:183], v[156:159]\n"
"v_add_f32 v44, v44, v250\n"
"v_add_f32 v46, v46, v251\n"
"v_mfma_f32_16x16x32_bf16 v[156:159], v[216:219], a[184:187], v[156:159]\n"
"v_mfma_f32_16x16x32_bf16 v[156:159], v[220:223], a[188:191], v[156:159]\n"
"s_waitcnt vmcnt(12)\n"
"v_lshlrev_b32 v246, 16, v196\n"
"v_and_b32 v196, s73, v196\n"
"v_mfma_f32_16x16x32_bf16 v[172:175], v[220:223], a[252:255], 0\n"
"ds_read_b64_tr_b16 v[220:221], v234 offset:24576\n"
"v_add_f32 v49, v49, v196\n"
"v_mfma_f32_16x16x32_bf16 v[172:175], v[216:219], a[248:251], v[172:175]\n"
"buffer_store_dwordx4 v[132:135], v243, s[32:35], s16 offen offset:1024 sc1\n"
"v_lshlrev_b32 v247, 16, v197\n"
"v_mfma_f32_16x16x32_bf16 v[172:175], v[212:215], a[244:247], v[172:175]\n"
"ds_read_b64_tr_b16 v[212:213], v234 offset:16384\n"
"v_and_b32 v197, s73, v197\n"
"v_mfma_f32_16x16x32_bf16 v[172:175], v[208:211], a[240:243], v[172:175]\n"
"ds_read_b64_tr_b16 v[208:209], v230 offset:16384\n"
"v_add_f32 v51, v51, v197\n"
"v_mfma_f32_16x16x32_bf16 v[172:175], v[192:195], a[204:207], v[172:175]\n"
"ds_read_b64_tr_b16 v[192:193], v234 offset:8192\n"
"v_lshlrev_b32 v248, 16, v198\n"
"v_mfma_f32_16x16x32_bf16 v[172:175], v[188:191], a[200:203], v[172:175]\n"
"ds_read_b64_tr_b16 v[188:189], v230 offset:8192\n"
"v_and_b32 v198, s73, v198\n"
"v_mfma_f32_16x16x32_bf16 v[172:175], v[184:187], a[196:199], v[172:175]\n"
"ds_read_b64_tr_b16 v[184:185], v234 offset:0\n"
"v_add_f32 v53, v53, v198\n"
"v_mfma_f32_16x16x32_bf16 v[172:175], v[180:183], a[192:195], v[172:175]\n"
"s_waitcnt lgkmcnt(4)\n"
"s_add_u32 s40, s41, s54\n"
"buffer_load_dwordx4 v240, s[28:31], s40 offen offset:3072 lds\n"
"v_mfma_f32_16x16x32_bf16 v[128:131], v[96:99], a[96:99], 0\n"
"ds_read_b64_tr_b16 v[180:181], v230 offset:0\n"
"v_lshlrev_b32 v249, 16, v199\n"
"v_mfma_f32_16x16x32_bf16 v[128:131], v[100:103], a[100:103], v[128:131]\n"
"s_waitcnt lgkmcnt(3)\n"
"ds_read_b64_tr_b16 v[182:183], v230 offset:2048\n"
"v_and_b32 v199, s73, v199\n"
"v_mfma_f32_16x16x32_bf16 v[128:131], v[104:107], a[104:107], v[128:131]\n"
"ds_read_b64_tr_b16 v[186:187], v234 offset:2048\n"
"v_add_f32 v55, v55, v199\n"
"v_mfma_f32_16x16x32_bf16 v[128:131], v[108:111], a[108:111], v[128:131]\n"
"ds_read_b64_tr_b16 v[190:191], v230 offset:10240\n"
"v_add_f32 v48, v48, v246\n"
"v_mfma_f32_16x16x32_bf16 v[128:131], v[112:115], a[144:147], v[128:131]\n"
"s_waitcnt vmcnt(7)\n"
"ds_read_b64_tr_b16 v[194:195], v234 offset:10240\n"
"v_add_f32 v50, v50, v247\n"
"v_mfma_f32_16x16x32_bf16 v[128:131], v[116:119], a[148:151], v[128:131]\n"
"v_subrev_u32 v244, s21, v244\n"
"ds_read_b64_tr_b16 v[210:211], v230 offset:18432\n"
"v_mfma_f32_16x16x32_bf16 v[128:131], v[120:123], a[152:155], v[128:131]\n"
"v_cmp_lt_u32 vcc, s69, v244\n"
"ds_read_b64_tr_b16 v[214:215], v234 offset:18432\n"
"v_mfma_f32_16x16x32_bf16 v[128:131], v[224:227], a[156:159], v[128:131]\n"
"ds_read_b64_tr_b16 v[216:217], v230 offset:24576\n"
"s_cbranch_vccnz LGP_%=\nLGPK_%=:\n"
"v_mfma_f32_16x16x32_bf16 v[144:147], v[224:227], a[172:175], 0\n"
"s_sub_u32 s72, s72, 1\n"
"ds_read_b64_tr_b16 v[218:219], v230 offset:26624\n"
"v_add_f32 v52, v52, v248\n"
"v_mfma_f32_16x16x32_bf16 v[144:147], v[120:123], a[168:171], v[144:147]\n"
"s_and_b32 s72, s72, 7\n"
"ds_read_b64_tr_b16 v[222:223], v234 offset:26624\n"
"v_mfma_f32_16x16x32_bf16 v[144:147], v[116:119], a[164:167], v[144:147]\n"
"v_lshlrev_b32 v250, 16, v200\n"
"v_and_b32 v200, s73, v200\n"
"v_mfma_f32_16x16x32_bf16 v[144:147], v[112:115], a[160:163], v[144:147]\n"
"buffer_load_dwordx4 v[72:75], v243, s[32:35], s18 offen sc1\n"
"v_mfma_f32_16x16x32_bf16 v[144:147], v[108:111], a[124:127], v[144:147]\n"
"v_add_f32 v57, v57, v200\n"
"v_lshlrev_b32 v251, 16, v201\n"
"v_mfma_f32_16x16x32_bf16 v[144:147], v[104:107], a[120:123], v[144:147]\n"
"v_and_b32 v201, s73, v201\n"
"v_add_f32 v59, v59, v201\n"
"v_mfma_f32_16x16x32_bf16 v[144:147], v[100:103], a[116:119], v[144:147]\n"
"v_lshlrev_b32 v246, 16, v202\n"
"v_and_b32 v202, s73, v202\n"
"v_mfma_f32_16x16x32_bf16 v[144:147], v[96:99], a[112:115], v[144:147]\n"
"buffer_store_dwordx4 v[148:151], v243, s[32:35], s16 offen offset:3072 sc1\n"
"v_add_f32 v61, v61, v202\n"
"v_mfma_f32_16x16x32_bf16 v[160:163], v[96:99], a[128:131], 0\n"
"v_lshlrev_b32 v247, 16, v203\n"
"v_and_b32 v203, s73, v203\n"
"v_mfma_f32_16x16x32_bf16 v[160:163], v[100:103], a[132:135], v[160:163]\n"
"v_add_f32 v54, v54, v249\n"
"v_add_f32 v63, v63, v203\n"
"v_mfma_f32_16x16x32_bf16 v[160:163], v[104:107], a[136:139], v[160:163]\n"
"s_add_u32 m0, s71, 16384\n"
"v_add_f32 v56, v56, v250\n"
"v_add_f32 v58, v58, v251\n"
"v_mfma_f32_16x16x32_bf16 v[160:163], v[108:111], a[140:143], v[160:163]\n"
"s_add_u32 s41, s70, 163840\n"
"buffer_load_dwordx4 v240, s[28:31], s41 offen lds\n"
"v_mfma_f32_16x16x32_bf16 v[160:163], v[112:115], a[176:179], v[160:163]\n"
"v_add_f32 v60, v60, v246\n"
"v_add_f32 v62, v62, v247\n"
"v_mfma_f32_16x16x32_bf16 v[160:163], v[116:119], a[180:183], v[160:163]\n"
"v_mfma_f32_16x16x32_bf16 v[160:163], v[120:123], a[184:187], v[160:163]\n"
"v_mfma_f32_16x16x32_bf16 v[160:163], v[224:227], a[188:191], v[160:163]\n"
"v_mfma_f32_16x16x32_bf16 v[176:179], v[224:227], a[252:255], 0\n"
"ds_read_b64_tr_b16 v[224:225], v235 offset:24576\n"
"v_mfma_f32_16x16x32_bf16 v[176:179], v[120:123], a[248:251], v[176:179]\n"
"s_add_u32 s40, s41, s52\n"
"buffer_load_dwordx4 v240, s[28:31], s40 offen offset:1024 lds\n"
"v_mfma_f32_16x16x32_bf16 v[176:179], v[116:119], a[244:247], v[176:179]\n"
"ds_read_b64_tr_b16 v[116:117], v235 offset:16384\n"
"v_mfma_f32_16x16x32_bf16 v[176:179], v[112:115], a[240:243], v[176:179]\n"
"ds_read_b64_tr_b16 v[112:113], v231 offset:16384\n"
"v_mfma_f32_16x16x32_bf16 v[176:179], v[108:111], a[204:207], v[176:179]\n"
"ds_read_b64_tr_b16 v[108:109], v235 offset:8192\n"
"v_mfma_f32_16x16x32_bf16 v[176:179], v[104:107], a[200:203], v[176:179]\n"
"buffer_load_dwordx4 v[76:79], v243, s[32:35], s18 offen offset:1024 sc1\n"
"v_mfma_f32_16x16x32_bf16 v[176:179], v[100:103], a[196:199], v[176:179]\n"
"ds_read_b64_tr_b16 v[100:101], v235 offset:0\n"
"v_lshlrev_b32 v248, 16, v204\n"
"v_mfma_f32_16x16x32_bf16 v[176:179], v[96:99], a[192:195], v[176:179]\n"
"s_waitcnt lgkmcnt(3)\n"
"ds_read_b64_tr_b16 v[96:97], v231 offset:0\n"
"v_and_b32 v204, s73, v204\n"
"v_mfma_f32_16x16x32_bf16 v[132:135], v[180:183], a[96:99], 0\n"
"ds_read_b64_tr_b16 v[98:99], v231 offset:2048\n"
"v_add_f32 v65, v65, v204\n"
"v_mfma_f32_16x16x32_bf16 v[132:135], v[184:187], a[100:103], v[132:135]\n"
"s_add_u32 s40, s41, s53\n"
"buffer_load_dwordx4 v240, s[28:31], s40 offen offset:2048 lds\n"
"v_mfma_f32_16x16x32_bf16 v[132:135], v[188:191], a[104:107], v[132:135]\n"
"ds_read_b64_tr_b16 v[102:103], v235 offset:2048\n"
"v_lshlrev_b32 v249, 16, v205\n"
"v_mfma_f32_16x16x32_bf16 v[132:135], v[192:195], a[108:111], v[132:135]\n"
"ds_read_b64_tr_b16 v[104:105], v231 offset:8192\n"
"v_and_b32 v205, s73, v205\n"
"v_mfma_f32_16x16x32_bf16 v[132:135], v[208:211], a[144:147], v[132:135]\n"
"ds_read_b64_tr_b16 v[106:107], v231 offset:10240\n"
"v_add_f32 v67, v67, v205\n"
"v_mfma_f32_16x16x32_bf16 v[132:135], v[212:215], a[148:151], v[132:135]\n"
"buffer_store_dwordx4 v[164:167], v243, s[32:35], s17 offen offset:1024 sc1\n"
"v_lshlrev_b32 v250, 16, v206\n"
"v_mfma_f32_16x16x32_bf16 v[132:135], v[216:219], a[152:155], v[132:135]\n"
"ds_read_b64_tr_b16 v[110:111], v235 offset:10240\n"
"v_and_b32 v206, s73, v206\n"
"v_mfma_f32_16x16x32_bf16 v[132:135], v[220:223], a[156:159], v[132:135]\n"
"ds_read_b64_tr_b16 v[114:115], v231 offset:18432\n"
"v_add_f32 v69, v69, v206\n"
"v_mfma_f32_16x16x32_bf16 v[148:151], v[220:223], a[172:175], 0\n"
"ds_read_b64_tr_b16 v[118:119], v235 offset:18432\n"
"v_lshlrev_b32 v251, 16, v207\n"
"v_mfma_f32_16x16x32_bf16 v[148:151], v[216:219], a[168:171], v[148:151]\n"
"ds_read_b64_tr_b16 v[120:121], v231 offset:24576\n"
"v_and_b32 v207, s73, v207\n"
"v_mfma_f32_16x16x32_bf16 v[148:151], v[212:215], a[164:167], v[148:151]\n"
"ds_read_b64_tr_b16 v[122:123], v231 offset:26624\n"
"v_add_f32 v71, v71, v207\n"
"v_mfma_f32_16x16x32_bf16 v[148:151], v[208:211], a[160:163], v[148:151]\n"
"s_add_u32 s40, s41, s54\n"
"buffer_load_dwordx4 v240, s[28:31], s40 offen offset:3072 lds\n"
"v_mfma_f32_16x16x32_bf16 v[148:151], v[192:195], a[124:127], v[148:151]\n"
"ds_read_b64_tr_b16 v[226:227], v235 offset:26624\n"
"v_add_f32 v64, v64, v248\n"
"v_mfma_f32_16x16x32_bf16 v[148:151], v[188:191], a[120:123], v[148:151]\n"
"v_add_f32 v66, v66, v249\n"
"v_add_f32 v68, v68, v250\n"
"v_mfma_f32_16x16x32_bf16 v[148:151], v[184:187], a[116:119], v[148:151]\n"
"v_add_f32 v70, v70, v251\n"
"v_mfma_f32_16x16x32_bf16 v[148:151], v[180:183], a[112:115], v[148:151]\n"
"buffer_load_dwordx4 v[80:83], v243, s[32:35], s18 offen offset:2048 sc1\n"
"v_mfma_f32_16x16x32_bf16 v[164:167], v[180:183], a[128:131], 0\n"
"v_mfma_f32_16x16x32_bf16 v[164:167], v[184:187], a[132:135], v[164:167]\n"
"v_mfma_f32_16x16x32_bf16 v[164:167], v[188:191], a[136:139], v[164:167]\n"
"v_mfma_f32_16x16x32_bf16 v[164:167], v[192:195], a[140:143], v[164:167]\n"
"v_mfma_f32_16x16x32_bf16 v[164:167], v[208:211], a[176:179], v[164:167]\n"
"v_mfma_f32_16x16x32_bf16 v[164:167], v[212:215], a[180:183], v[164:167]\n"
"v_mfma_f32_16x16x32_bf16 v[164:167], v[216:219], a[184:187], v[164:167]\n"
"v_mfma_f32_16x16x32_bf16 v[164:167], v[220:223], a[188:191], v[164:167]\n"
"s_waitcnt lgkmcnt(0)\n"
"v_mfma_f32_16x16x32_bf16 v[136:139], v[96:99], a[96:99], 0\n"
"v_mfma_f32_16x16x32_bf16 v[136:139], v[100:103], a[100:103], v[136:139]\n"
"s_barrier\n"
"ds_read_b64_tr_b16 v[180:181], v228 offset:32768\n"
"v_mfma_f32_16x16x32_bf16 v[136:139], v[104:107], a[104:107], v[136:139]\n"
"ds_read_b64_tr_b16 v[182:183], v228 offset:34816\n"
"v_mfma_f32_16x16x32_bf16 v[136:139], v[108:111], a[108:111], v[136:139]\n"
"ds_read_b64_tr_b16 v[184:185], v232 offset:32768\n"
"v_mfma_f32_16x16x32_bf16 v[136:139], v[112:115], a[144:147], v[136:139]\n"
"ds_read_b64_tr_b16 v[186:187], v232 offset:34816\n"
"v_mfma_f32_16x16x32_bf16 v[136:139], v[116:119], a[148:151], v[136:139]\n"
"ds_read_b64_tr_b16 v[188:189], v228 offset:40960\n"
"v_mfma_f32_16x16x32_bf16 v[136:139], v[120:123], a[152:155], v[136:139]\n"
"ds_read_b64_tr_b16 v[190:191], v228 offset:43008\n"
"v_mfma_f32_16x16x32_bf16 v[136:139], v[224:227], a[156:159], v[136:139]\n"
"ds_read_b64_tr_b16 v[192:193], v232 offset:40960\n"
"v_mfma_f32_16x16x32_bf16 v[152:155], v[224:227], a[172:175], 0\n"
"ds_read_b64_tr_b16 v[194:195], v232 offset:43008\n"
"v_mfma_f32_16x16x32_bf16 v[152:155], v[120:123], a[168:171], v[152:155]\n"
"buffer_load_dwordx4 v[84:87], v243, s[32:35], s18 offen offset:3072 sc1\n"
"v_mfma_f32_16x16x32_bf16 v[152:155], v[116:119], a[164:167], v[152:155]\n"
"ds_read_b64_tr_b16 v[208:209], v228 offset:49152\n"
"v_mfma_f32_16x16x32_bf16 v[152:155], v[112:115], a[160:163], v[152:155]\n"
"ds_read_b64_tr_b16 v[210:211], v228 offset:51200\n"
"v_mfma_f32_16x16x32_bf16 v[152:155], v[108:111], a[124:127], v[152:155]\n"
"ds_read_b64_tr_b16 v[212:213], v232 offset:49152\n"
"v_mfma_f32_16x16x32_bf16 v[152:155], v[104:107], a[120:123], v[152:155]\n"
"ds_read_b64_tr_b16 v[214:215], v232 offset:51200\n"
"v_mfma_f32_16x16x32_bf16 v[152:155], v[100:103], a[116:119], v[152:155]\n"
"ds_read_b64_tr_b16 v[216:217], v228 offset:57344\n"
"v_mfma_f32_16x16x32_bf16 v[152:155], v[96:99], a[112:115], v[152:155]\n"
"ds_read_b64_tr_b16 v[218:219], v228 offset:59392\n"
"v_xor_b32 v228, s75, v228\n"
"v_mfma_f32_16x16x32_bf16 v[168:171], v[96:99], a[128:131], 0\n"
"ds_read_b64_tr_b16 v[220:221], v232 offset:57344\n"
"v_mfma_f32_16x16x32_bf16 v[168:171], v[100:103], a[132:135], v[168:171]\n"
"ds_read_b64_tr_b16 v[222:223], v232 offset:59392\n"
"v_xor_b32 v232, s75, v232\n"
"v_mfma_f32_16x16x32_bf16 v[168:171], v[104:107], a[136:139], v[168:171]\n"
"s_add_u32 m0, s71, 32768\n"
"ds_read_b64_tr_b16 v[96:97], v229 offset:32768\n"
"v_mfma_f32_16x16x32_bf16 v[168:171], v[108:111], a[140:143], v[168:171]\n"
"s_add_u32 s41, s70, 327680\n"
"buffer_load_dwordx4 v240, s[28:31], s41 offen lds\n"
"v_mfma_f32_16x16x32_bf16 v[168:171], v[112:115], a[176:179], v[168:171]\n"
"ds_read_b64_tr_b16 v[98:99], v229 offset:34816\n"
"v_mfma_f32_16x16x32_bf16 v[168:171], v[116:119], a[180:183], v[168:171]\n"
"ds_read_b64_tr_b16 v[100:101], v233 offset:32768\n"
"v_mfma_f32_16x16x32_bf16 v[168:171], v[120:123], a[184:187], v[168:171]\n"
"ds_read_b64_tr_b16 v[102:103], v233 offset:34816\n"
"v_mfma_f32_16x16x32_bf16 v[168:171], v[224:227], a[188:191], v[168:171]\n"
"s_waitcnt lgkmcnt(3)\n"
"ds_read_b64_tr_b16 v[104:105], v229 offset:40960\n"
"v_mfma_f32_16x16x32_bf16 v[124:127], v[180:183], a[48:51], v[124:127]\n"
"ds_read_b64_tr_b16 v[106:107], v229 offset:43008\n"
"v_mfma_f32_16x16x32_bf16 v[124:127], v[184:187], a[52:55], v[124:127]\n"
"s_add_u32 s40, s41, s52\n"
"buffer_load_dwordx4 v240, s[28:31], s40 offen offset:1024 lds\n"
"v_mfma_f32_16x16x32_bf16 v[124:127], v[188:191], a[56:59], v[124:127]\n"
"ds_read_b64_tr_b16 v[108:109], v233 offset:40960\n"
"v_mfma_f32_16x16x32_bf16 v[124:127], v[192:195], a[60:63], v[124:127]\n"
"ds_read_b64_tr_b16 v[110:111], v233 offset:43008\n"
"v_mfma_f32_16x16x32_bf16 v[124:127], v[208:211], a[0:3], v[124:127]\n"
"ds_read_b64_tr_b16 v[112:113], v229 offset:49152\n"
"v_mfma_f32_16x16x32_bf16 v[124:127], v[212:215], a[4:7], v[124:127]\n"
"buffer_load_dwordx4 v[88:91], v243, s[32:35], s19 offen sc1\n"
"v_mfma_f32_16x16x32_bf16 v[124:127], v[216:219], a[8:11], v[124:127]\n"
"ds_read_b64_tr_b16 v[114:115], v229 offset:51200\n"
"v_mfma_f32_16x16x32_bf16 v[124:127], v[220:223], a[12:15], v[124:127]\n"
"ds_read_b64_tr_b16 v[116:117], v233 offset:49152\n"
"v_mfma_f32_16x16x32_bf16 v[140:143], v[220:223], a[28:31], v[140:143]\n"
"ds_read_b64_tr_b16 v[118:119], v233 offset:51200\n"
"v_mfma_f32_16x16x32_bf16 v[140:143], v[216:219], a[24:27], v[140:143]\n"
"ds_read_b64_tr_b16 v[120:121], v229 offset:57344\n"
"v_mfma_f32_16x16x32_bf16 v[140:143], v[212:215], a[20:23], v[140:143]\n"
"ds_read_b64_tr_b16 v[122:123], v229 offset:59392\n"
"v_xor_b32 v229, s75, v229\n"
"v_mfma_f32_16x16x32_bf16 v[140:143], v[208:211], a[16:19], v[140:143]\n"
"s_add_u32 s40, s41, s53\n"
"buffer_load_dwordx4 v240, s[28:31], s40 offen offset:2048 lds\n"
"v_mfma_f32_16x16x32_bf16 v[140:143], v[192:195], a[76:79], v[140:143]\n"
"ds_read_b64_tr_b16 v[224:225], v233 offset:57344\n"
"v_mfma_f32_16x16x32_bf16 v[140:143], v[188:191], a[72:75], v[140:143]\n"
"ds_read_b64_tr_b16 v[226:227], v233 offset:59392\n"
"v_cvt_pk_bf16_f32 v124, v124, v125\n"
"v_mfma_f32_16x16x32_bf16 v[140:143], v[184:187], a[68:71], v[140:143]\n"
"v_cvt_pk_bf16_f32 v125, v126, v127\n"
"v_xor_b32 v233, s75, v233\n"
"v_mfma_f32_16x16x32_bf16 v[140:143], v[180:183], a[64:67], v[140:143]\n"
"v_mfma_f32_16x16x32_bf16 v[156:159], v[180:183], a[80:83], v[156:159]\n"
"v_mfma_f32_16x16x32_bf16 v[156:159], v[184:187], a[84:87], v[156:159]\n"
"v_mfma_f32_16x16x32_bf16 v[156:159], v[188:191], a[88:91], v[156:159]\n"
"v_mfma_f32_16x16x32_bf16 v[156:159], v[192:195], a[92:95], v[156:159]\n"
"s_add_u32 s40, s41, s54\n"
"buffer_load_dwordx4 v240, s[28:31], s40 offen offset:3072 lds\n"
"v_mfma_f32_16x16x32_bf16 v[156:159], v[208:211], a[32:35], v[156:159]\n"
"v_mfma_f32_16x16x32_bf16 v[156:159], v[212:215], a[36:39], v[156:159]\n"
"s_waitcnt vmcnt(14)\n"
"v_cvt_pk_bf16_f32 v140, v140, v141\n"
"v_cvt_pk_bf16_f32 v141, v142, v143\n"
"v_mfma_f32_16x16x32_bf16 v[156:159], v[216:219], a[40:43], v[156:159]\n"
"s_waitcnt vmcnt(8)\n"
"v_lshlrev_b32 v246, 16, v72\n"
"v_and_b32 v72, s73, v72\n"
"v_mfma_f32_16x16x32_bf16 v[156:159], v[220:223], a[44:47], v[156:159]\n"
"s_barrier\n"
"v_add_f32 v0, v0, v246\n"
"v_mfma_f32_16x16x32_bf16 v[172:175], v[220:223], a[228:231], v[172:175]\n"
"ds_read_b64_tr_b16 v[220:221], v234 offset:57344\n"
"v_add_f32 v1, v1, v72\n"
"v_mfma_f32_16x16x32_bf16 v[172:175], v[216:219], a[224:227], v[172:175]\n"
"ds_read_b64_tr_b16 v[216:217], v230 offset:57344\n"
"s_cmp_eq_u32 s42, 0\ns_cbranch_scc1 LGS_%=\nLGSK_%=:\n"
"v_mfma_f32_16x16x32_bf16 v[172:175], v[212:215], a[212:215], v[172:175]\n"
"ds_read_b64_tr_b16 v[212:213], v234 offset:49152\n"
"v_lshlrev_b32 v247, 16, v73\n"
"v_mfma_f32_16x16x32_bf16 v[172:175], v[208:211], a[208:211], v[172:175]\n"
"s_mul_i32 s16, s61, 286720\n"
"buffer_load_dwordx4 v[92:95], v243, s[32:35], s19 offen offset:1024 sc1\n"
"v_mfma_f32_16x16x32_bf16 v[172:175], v[192:195], a[236:239], v[172:175]\n"
"s_add_u32 s16, s16, s62\n"
"ds_read_b64_tr_b16 v[192:193], v234 offset:40960\n"
"v_and_b32 v73, s73, v73\n"
"v_mfma_f32_16x16x32_bf16 v[172:175], v[188:191], a[232:235], v[172:175]\n"
"s_add_u32 s17, s16, 4096\n"
"ds_read_b64_tr_b16 v[188:189], v230 offset:40960\n"
"v_cvt_pk_bf16_f32 v156, v156, v157\n"
"v_mfma_f32_16x16x32_bf16 v[172:175], v[184:187], a[220:223], v[172:175]\n"
"s_mul_i32 s62, s70, 224\n"
"ds_read_b64_tr_b16 v[184:185], v234 offset:32768\n"
"v_cvt_pk_bf16_f32 v157, v158, v159\n"
"v_mfma_f32_16x16x32_bf16 v[172:175], v[180:183], a[216:219], v[172:175]\n"
"s_waitcnt lgkmcnt(3)\n"
"ds_read_b64_tr_b16 v[180:181], v230 offset:32768\n"
"v_add_f32 v2, v2, v247\n"
"v_mfma_f32_16x16x32_bf16 v[128:131], v[96:99], a[48:51], v[128:131]\n"
"s_add_u32 m0, s71, 49152\n"
"ds_read_b64_tr_b16 v[182:183], v230 offset:34816\n"
"v_add_f32 v3, v3, v73\n"
"v_mfma_f32_16x16x32_bf16 v[128:131], v[100:103], a[52:55], v[128:131]\n"
"s_add_u32 s41, s70, 491520\n"
"buffer_load_dwordx4 v240, s[28:31], s41 offen lds\n"
"v_mfma_f32_16x16x32_bf16 v[128:131], v[104:107], a[56:59], v[128:131]\n"
"ds_read_b64_tr_b16 v[186:187], v234 offset:34816\n"
"v_lshlrev_b32 v248, 16, v74\n"
"v_mfma_f32_16x16x32_bf16 v[128:131], v[108:111], a[60:63], v[128:131]\n"
"ds_read_b64_tr_b16 v[190:191], v230 offset:43008\n"
"v_and_b32 v74, s73, v74\n"
"v_mfma_f32_16x16x32_bf16 v[128:131], v[112:115], a[0:3], v[128:131]\n"
"ds_read_b64_tr_b16 v[194:195], v234 offset:43008\n"
"v_add_f32 v4, v4, v248\n"
"v_mfma_f32_16x16x32_bf16 v[128:131], v[116:119], a[4:7], v[128:131]\n"
"ds_read_b64_tr_b16 v[208:209], v230 offset:49152\n"
"v_add_f32 v5, v5, v74\n"
"v_mfma_f32_16x16x32_bf16 v[128:131], v[120:123], a[8:11], v[128:131]\n"
"ds_read_b64_tr_b16 v[210:211], v230 offset:51200\n"
"v_lshlrev_b32 v249, 16, v75\n"
"v_mfma_f32_16x16x32_bf16 v[128:131], v[224:227], a[12:15], v[128:131]\n"
"s_add_u32 s40, s41, s52\n"
"buffer_load_dwordx4 v240, s[28:31], s40 offen offset:1024 lds\n"
"v_mfma_f32_16x16x32_bf16 v[144:147], v[224:227], a[28:31], v[144:147]\n"
"ds_read_b64_tr_b16 v[214:215], v234 offset:51200\n"
"v_and_b32 v75, s73, v75\n"
"v_mfma_f32_16x16x32_bf16 v[144:147], v[120:123], a[24:27], v[144:147]\n"
"ds_read_b64_tr_b16 v[218:219], v230 offset:59392\n"
"v_add_f32 v6, v6, v249\n"
"v_mfma_f32_16x16x32_bf16 v[144:147], v[116:119], a[20:23], v[144:147]\n"
"ds_read_b64_tr_b16 v[222:223], v234 offset:59392\n"
"v_add_f32 v7, v7, v75\n"
"v_mfma_f32_16x16x32_bf16 v[144:147], v[112:115], a[16:19], v[144:147]\n"
"v_lshlrev_b32 v250, 16, v76\n"
"v_and_b32 v76, s73, v76\n"
"v_mfma_f32_16x16x32_bf16 v[144:147], v[108:111], a[76:79], v[144:147]\n"
"v_add_f32 v8, v8, v250\n"
"v_add_f32 v9, v9, v76\n"
"v_mfma_f32_16x16x32_bf16 v[144:147], v[104:107], a[72:75], v[144:147]\n"
"buffer_load_dwordx4 v[196:199], v243, s[32:35], s19 offen offset:2048 sc1\n"
"v_mfma_f32_16x16x32_bf16 v[144:147], v[100:103], a[68:71], v[144:147]\n"
"v_cvt_pk_bf16_f32 v126, v128, v129\n"
"v_cvt_pk_bf16_f32 v127, v130, v131\n"
"v_mfma_f32_16x16x32_bf16 v[144:147], v[96:99], a[64:67], v[144:147]\n"
"v_lshlrev_b32 v251, 16, v77\n"
"v_and_b32 v77, s73, v77\n"
"v_mfma_f32_16x16x32_bf16 v[160:163], v[96:99], a[80:83], v[160:163]\n"
"v_add_f32 v10, v10, v251\n"
"v_add_f32 v11, v11, v77\n"
"v_mfma_f32_16x16x32_bf16 v[160:163], v[100:103], a[84:87], v[160:163]\n"
"buffer_store_dwordx4 v[124:127], v243, s[32:35], s16 offen sc1\n"
"v_lshlrev_b32 v246, 16, v78\n"
"v_mfma_f32_16x16x32_bf16 v[160:163], v[104:107], a[88:91], v[160:163]\n"
"v_and_b32 v78, s73, v78\n"
"v_add_f32 v12, v12, v246\n"
"v_mfma_f32_16x16x32_bf16 v[160:163], v[108:111], a[92:95], v[160:163]\n"
"v_add_f32 v13, v13, v78\n"
"v_lshlrev_b32 v247, 16, v79\n"
"v_mfma_f32_16x16x32_bf16 v[160:163], v[112:115], a[32:35], v[160:163]\n"
"v_and_b32 v79, s73, v79\n"
"v_add_f32 v14, v14, v247\n"
"v_mfma_f32_16x16x32_bf16 v[160:163], v[116:119], a[36:39], v[160:163]\n"
"v_cvt_pk_bf16_f32 v142, v144, v145\n"
"v_cvt_pk_bf16_f32 v143, v146, v147\n"
"v_mfma_f32_16x16x32_bf16 v[160:163], v[120:123], a[40:43], v[160:163]\n"
"v_cvt_pk_bf16_f32 v172, v172, v173\n"
"v_add_f32 v15, v15, v79\n"
"v_mfma_f32_16x16x32_bf16 v[160:163], v[224:227], a[44:47], v[160:163]\n"
"s_add_u32 s40, s41, s53\n"
"buffer_load_dwordx4 v240, s[28:31], s40 offen offset:2048 lds\n"
"v_mfma_f32_16x16x32_bf16 v[176:179], v[224:227], a[228:231], v[176:179]\n"
"ds_read_b64_tr_b16 v[224:225], v235 offset:57344\n"
"v_cvt_pk_bf16_f32 v173, v174, v175\n"
"v_mfma_f32_16x16x32_bf16 v[176:179], v[120:123], a[224:227], v[176:179]\n"
"s_waitcnt vmcnt(12)\n"
"ds_read_b64_tr_b16 v[120:121], v231 offset:57344\n"
"v_lshlrev_b32 v248, 16, v80\n"
"v_mfma_f32_16x16x32_bf16 v[176:179], v[116:119], a[212:215], v[176:179]\n"
"ds_read_b64_tr_b16 v[116:117], v235 offset:49152\n"
"v_and_b32 v80, s73, v80\n"
"v_mfma_f32_16x16x32_bf16 v[176:179], v[112:115], a[208:211], v[176:179]\n"
"ds_read_b64_tr_b16 v[112:113], v231 offset:49152\n"
"v_add_f32 v16, v16, v248\n"
"v_mfma_f32_16x16x32_bf16 v[176:179], v[108:111], a[236:239], v[176:179]\n"
"ds_read_b64_tr_b16 v[108:109], v235 offset:40960\n"
"v_add_f32 v17, v17, v80\n"
"v_mfma_f32_16x16x32_bf16 v[176:179], v[104:107], a[232:235], v[176:179]\n"
"ds_read_b64_tr_b16 v[104:105], v231 offset:40960\n"
"v_cvt_pk_bf16_f32 v158, v160, v161\n"
"v_mfma_f32_16x16x32_bf16 v[176:179], v[100:103], a[220:223], v[176:179]\n"
"ds_read_b64_tr_b16 v[100:101], v235 offset:32768\n"
"v_cvt_pk_bf16_f32 v159, v162, v163\n"
"v_mfma_f32_16x16x32_bf16 v[176:179], v[96:99], a[216:219], v[176:179]\n"
"s_waitcnt lgkmcnt(4)\n"
"buffer_load_dwordx4 v[200:203], v236, s[32:35], s20 offen sc1\n"
"v_mfma_f32_16x16x32_bf16 v[132:135], v[180:183], a[48:51], v[132:135]\n"
"ds_read_b64_tr_b16 v[96:97], v231 offset:32768\n"
"v_lshlrev_b32 v249, 16, v81\n"
"v_mfma_f32_16x16x32_bf16 v[132:135], v[184:187], a[52:55], v[132:135]\n"
"ds_read_b64_tr_b16 v[98:99], v231 offset:34816\n"
"v_and_b32 v81, s73, v81\n"
"v_mfma_f32_16x16x32_bf16 v[132:135], v[188:191], a[56:59], v[132:135]\n"
"ds_read_b64_tr_b16 v[102:103], v235 offset:34816\n"
"v_add_f32 v18, v18, v249\n"
"v_mfma_f32_16x16x32_bf16 v[132:135], v[192:195], a[60:63], v[132:135]\n"
"s_add_u32 s40, s41, s54\n"
"buffer_load_dwordx4 v240, s[28:31], s40 offen offset:3072 lds\n"
"v_mfma_f32_16x16x32_bf16 v[132:135], v[208:211], a[0:3], v[132:135]\n"
"s_add_u32 s70, s61, s47\n"
"ds_read_b64_tr_b16 v[106:107], v231 offset:43008\n"
"v_add_f32 v19, v19, v81\n"
"v_mfma_f32_16x16x32_bf16 v[132:135], v[212:215], a[4:7], v[132:135]\n"
"s_add_u32 s70, s70, 1\n"
"ds_read_b64_tr_b16 v[110:111], v235 offset:43008\n"
"v_lshlrev_b32 v250, 16, v82\n"
"v_mfma_f32_16x16x32_bf16 v[132:135], v[216:219], a[8:11], v[132:135]\n"
"s_and_b32 s70, s70, 7\n"
"ds_read_b64_tr_b16 v[114:115], v231 offset:51200\n"
"v_and_b32 v82, s73, v82\n"
"v_mfma_f32_16x16x32_bf16 v[132:135], v[220:223], a[12:15], v[132:135]\n"
"s_lshl_b32 s70, s70, 7\n"
"buffer_store_dwordx4 v[140:143], v243, s[32:35], s16 offen offset:2048 sc1\n"
"v_add_f32 v20, v20, v250\n"
"v_mfma_f32_16x16x32_bf16 v[148:151], v[220:223], a[28:31], v[148:151]\n"
"s_xor_b32 s71, s71, s75\n"
"ds_read_b64_tr_b16 v[118:119], v235 offset:51200\n"
"v_add_f32 v21, v21, v82\n"
"v_mfma_f32_16x16x32_bf16 v[148:151], v[216:219], a[24:27], v[148:151]\n"
"ds_read_b64_tr_b16 v[122:123], v231 offset:59392\n"
"v_lshlrev_b32 v251, 16, v83\n"
"v_mfma_f32_16x16x32_bf16 v[148:151], v[212:215], a[20:23], v[148:151]\n"
"ds_read_b64_tr_b16 v[226:227], v235 offset:59392\n"
"v_and_b32 v83, s73, v83\n"
"v_mfma_f32_16x16x32_bf16 v[148:151], v[208:211], a[16:19], v[148:151]\n"
"s_waitcnt vmcnt(14)\n"
"v_add_f32 v22, v22, v251\n"
"v_add_f32 v23, v23, v83\n"
"v_mfma_f32_16x16x32_bf16 v[148:151], v[192:195], a[76:79], v[148:151]\n"
"v_lshlrev_b32 v246, 16, v84\n"
"v_and_b32 v84, s73, v84\n"
"v_mfma_f32_16x16x32_bf16 v[148:151], v[188:191], a[72:75], v[148:151]\n"
"v_add_f32 v24, v24, v246\n"
"v_add_f32 v25, v25, v84\n"
"v_mfma_f32_16x16x32_bf16 v[148:151], v[184:187], a[68:71], v[148:151]\n"
"v_lshlrev_b32 v247, 16, v85\n"
"v_and_b32 v85, s73, v85\n"
"v_mfma_f32_16x16x32_bf16 v[148:151], v[180:183], a[64:67], v[148:151]\n"
"v_add_f32 v26, v26, v247\n"
"v_add_f32 v27, v27, v85\n"
"v_mfma_f32_16x16x32_bf16 v[164:167], v[180:183], a[80:83], v[164:167]\n"
"v_lshlrev_b32 v248, 16, v86\n"
"v_and_b32 v86, s73, v86\n"
"v_mfma_f32_16x16x32_bf16 v[164:167], v[184:187], a[84:87], v[164:167]\n"
"v_add_f32 v28, v28, v248\n"
"v_add_f32 v29, v29, v86\n"
"v_mfma_f32_16x16x32_bf16 v[164:167], v[188:191], a[88:91], v[164:167]\n"
"v_lshlrev_b32 v249, 16, v87\n"
"v_and_b32 v87, s73, v87\n"
"v_mfma_f32_16x16x32_bf16 v[164:167], v[192:195], a[92:95], v[164:167]\n"
"buffer_load_dwordx4 v[204:207], v237, s[32:35], s20 offen sc1\n"
"v_mfma_f32_16x16x32_bf16 v[164:167], v[208:211], a[32:35], v[164:167]\n"
"s_mul_i32 s20, s72, 286720\n"
"v_add_f32 v30, v30, v249\n"
"v_add_f32 v31, v31, v87\n"
"v_mfma_f32_16x16x32_bf16 v[164:167], v[212:215], a[36:39], v[164:167]\n"
"s_mul_i32 s18, s61, 28672\n"
"v_cvt_pk_bf16_f32 v174, v176, v177\n"
"v_cvt_pk_bf16_f32 v175, v178, v179\n"
"v_mfma_f32_16x16x32_bf16 v[164:167], v[216:219], a[40:43], v[164:167]\n"
"s_add_u32 s18, s18, s20\n"
"v_cvt_pk_bf16_f32 v132, v132, v133\n"
"v_cvt_pk_bf16_f32 v133, v134, v135\n"
"v_mfma_f32_16x16x32_bf16 v[164:167], v[220:223], a[44:47], v[164:167]\n"
"s_waitcnt lgkmcnt(0)\n"
"s_cmp_ge_u32 s47, 3\ns_cselect_b32 s18, s18, s77\n"
"v_cvt_pk_bf16_f32 v148, v148, v149\n"
"v_cvt_pk_bf16_f32 v149, v150, v151\n"
"v_mfma_f32_16x16x32_bf16 v[136:139], v[96:99], a[48:51], v[136:139]\n"
"s_add_u32 s19, s18, 4096\n"
"v_xor_b32 v230, s75, v230\n"
"v_mfma_f32_16x16x32_bf16 v[136:139], v[100:103], a[52:55], v[136:139]\n"
"s_barrier\n"
"s_cmp_ge_u32 s47, 2\ns_cselect_b32 s20, s20, s77\ns_cselect_b32 s69, 15, -1\n"
"v_mfma_f32_16x16x32_bf16 v[136:139], v[104:107], a[56:59], v[136:139]\n"
"s_add_u32 s21, s21, 1\n"
"v_lshlrev_b32_e64 v245, 2, s72\n"
"ds_read_b64_tr_b16 v[180:181], v228 offset:0\n"
"v_mfma_f32_16x16x32_bf16 v[136:139], v[108:111], a[60:63], v[136:139]\n"
"global_load_dword v244, v245, s[6:7] sc1\n"
"v_mfma_f32_16x16x32_bf16 v[136:139], v[112:115], a[0:3], v[136:139]\n"
"ds_read_b64_tr_b16 v[182:183], v228 offset:2048\n"
"v_xor_b32 v234, s75, v234\n"
"v_mfma_f32_16x16x32_bf16 v[136:139], v[116:119], a[4:7], v[136:139]\n"
"buffer_store_dwordx4 v[156:159], v243, s[32:35], s17 offen sc1\n"
"v_cvt_pk_bf16_f32 v164, v164, v165\n"
"v_mfma_f32_16x16x32_bf16 v[136:139], v[120:123], a[8:11], v[136:139]\n"
"ds_read_b64_tr_b16 v[184:185], v232 offset:0\n"
"v_cvt_pk_bf16_f32 v165, v166, v167\n"
"v_mfma_f32_16x16x32_bf16 v[136:139], v[224:227], a[12:15], v[136:139]\n"
"s_waitcnt vmcnt(14)\n"
"ds_read_b64_tr_b16 v[186:187], v232 offset:2048\n"
"v_lshlrev_b32 v250, 16, v88\n"
"v_mfma_f32_16x16x32_bf16 v[152:155], v[224:227], a[28:31], v[152:155]\n"
"ds_read_b64_tr_b16 v[188:189], v228 offset:8192\n"
"v_and_b32 v88, s73, v88\n"
"v_mfma_f32_16x16x32_bf16 v[152:155], v[120:123], a[24:27], v[152:155]\n"
"ds_read_b64_tr_b16 v[190:191], v228 offset:10240\n"
"v_add_f32 v32, v32, v250\n"
"v_mfma_f32_16x16x32_bf16 v[152:155], v[116:119], a[20:23], v[152:155]\n"
"ds_read_b64_tr_b16 v[192:193], v232 offset:8192\n"
"v_add_f32 v33, v33, v88\n"
"v_mfma_f32_16x16x32_bf16 v[152:155], v[112:115], a[16:19], v[152:155]\n"
"ds_read_b64_tr_b16 v[194:195], v232 offset:10240\n"
"v_lshlrev_b32 v251, 16, v89\n"
"v_mfma_f32_16x16x32_bf16 v[152:155], v[108:111], a[76:79], v[152:155]\n"
"ds_read_b64_tr_b16 v[208:209], v228 offset:16384\n"
"v_and_b32 v89, s73, v89\n"
"v_mfma_f32_16x16x32_bf16 v[152:155], v[104:107], a[72:75], v[152:155]\n"
"ds_read_b64_tr_b16 v[210:211], v228 offset:18432\n"
"v_add_f32 v34, v34, v251\n"
"v_mfma_f32_16x16x32_bf16 v[152:155], v[100:103], a[68:71], v[152:155]\n"
"ds_read_b64_tr_b16 v[212:213], v232 offset:16384\n"
"v_add_f32 v35, v35, v89\n"
"v_mfma_f32_16x16x32_bf16 v[152:155], v[96:99], a[64:67], v[152:155]\n"
"ds_read_b64_tr_b16 v[214:215], v232 offset:18432\n"
"v_lshlrev_b32 v246, 16, v90\n"
"v_mfma_f32_16x16x32_bf16 v[168:171], v[96:99], a[80:83], v[168:171]\n"
"ds_read_b64_tr_b16 v[216:217], v228 offset:24576\n"
"v_and_b32 v90, s73, v90\n"
"v_mfma_f32_16x16x32_bf16 v[168:171], v[100:103], a[84:87], v[168:171]\n"
"ds_read_b64_tr_b16 v[218:219], v228 offset:26624\n"
"v_add_f32 v36, v36, v246\n"
"v_mfma_f32_16x16x32_bf16 v[168:171], v[104:107], a[88:91], v[168:171]\n"
"ds_read_b64_tr_b16 v[220:221], v232 offset:24576\n"
"v_add_f32 v37, v37, v90\n"
"v_mfma_f32_16x16x32_bf16 v[168:171], v[108:111], a[92:95], v[168:171]\n"
"ds_read_b64_tr_b16 v[222:223], v232 offset:26624\n"
"v_lshlrev_b32 v247, 16, v91\n"
"v_mfma_f32_16x16x32_bf16 v[168:171], v[112:115], a[32:35], v[168:171]\n"
"ds_read_b64_tr_b16 v[96:97], v229 offset:0\n"
"v_and_b32 v91, s73, v91\n"
"v_mfma_f32_16x16x32_bf16 v[168:171], v[116:119], a[36:39], v[168:171]\n"
"ds_read_b64_tr_b16 v[98:99], v229 offset:2048\n"
"v_add_f32 v38, v38, v247\n"
"v_mfma_f32_16x16x32_bf16 v[168:171], v[120:123], a[40:43], v[168:171]\n"
"s_add_u32 s47, s47, 1\n"
"ds_read_b64_tr_b16 v[100:101], v233 offset:0\n"
"v_add_f32 v39, v39, v91\n"
"v_mfma_f32_16x16x32_bf16 v[168:171], v[224:227], a[44:47], v[168:171]\n"
"ds_read_b64_tr_b16 v[102:103], v233 offset:2048\n"
"s_cmp_lg_u32 s47, 9\ns_cbranch_scc1 LGL_%=\n"
"s_waitcnt lgkmcnt(5)\n"
"v_mfma_f32_16x16x32_bf16 v[124:127], v[180:183], a[96:99], 0\n"
"ds_read_b64_tr_b16 v[104:105], v229 offset:8192\n"
"v_cvt_pk_bf16_f32 v134, v136, v137\n"
"v_mfma_f32_16x16x32_bf16 v[124:127], v[184:187], a[100:103], v[124:127]\n"
"ds_read_b64_tr_b16 v[106:107], v229 offset:10240\n"
"v_cvt_pk_bf16_f32 v135, v138, v139\n"
"v_mfma_f32_16x16x32_bf16 v[124:127], v[188:191], a[104:107], v[124:127]\n"
"ds_read_b64_tr_b16 v[108:109], v233 offset:8192\n"
"v_cvt_pk_bf16_f32 v150, v152, v153\n"
"v_mfma_f32_16x16x32_bf16 v[124:127], v[192:195], a[108:111], v[124:127]\n"
"buffer_store_dwordx4 v[172:175], v243, s[32:35], s17 offen offset:2048 sc1\n"
"v_cvt_pk_bf16_f32 v151, v154, v155\n"
"v_mfma_f32_16x16x32_bf16 v[124:127], v[208:211], a[144:147], v[124:127]\n"
"ds_read_b64_tr_b16 v[110:111], v233 offset:10240\n"
"v_xor_b32 v231, s75, v231\n"
"v_mfma_f32_16x16x32_bf16 v[124:127], v[212:215], a[148:151], v[124:127]\n"
"s_waitcnt vmcnt(12)\n"
"ds_read_b64_tr_b16 v[112:113], v229 offset:16384\n"
"v_cvt_pk_bf16_f32 v166, v168, v169\n"
"v_mfma_f32_16x16x32_bf16 v[124:127], v[216:219], a[152:155], v[124:127]\n"
"s_waitcnt lgkmcnt(3)\n"
"ds_read_b64_tr_b16 v[114:115], v229 offset:18432\n"
"v_cvt_pk_bf16_f32 v167, v170, v171\n"
"v_mfma_f32_16x16x32_bf16 v[124:127], v[220:223], a[156:159], v[124:127]\n"
"buffer_store_dwordx4 v[132:135], v243, s[32:35], s16 offen offset:1024 sc1\n"
"v_xor_b32 v235, s75, v235\n"
"v_mfma_f32_16x16x32_bf16 v[140:143], v[220:223], a[172:175], 0\n"
"ds_read_b64_tr_b16 v[116:117], v233 offset:16384\n"
"v_lshlrev_b32 v248, 16, v92\n"
"v_mfma_f32_16x16x32_bf16 v[140:143], v[216:219], a[168:171], v[140:143]\n"
"ds_read_b64_tr_b16 v[118:119], v233 offset:18432\n"
"v_and_b32 v92, s73, v92\n"
"v_mfma_f32_16x16x32_bf16 v[140:143], v[212:215], a[164:167], v[140:143]\n"
"ds_read_b64_tr_b16 v[120:121], v229 offset:24576\n"
"v_add_f32 v41, v41, v92\n"
"v_mfma_f32_16x16x32_bf16 v[140:143], v[208:211], a[160:163], v[140:143]\n"
"buffer_store_dwordx4 v[148:151], v243, s[32:35], s16 offen offset:3072 sc1\n"
"v_lshlrev_b32 v249, 16, v93\n"
"v_mfma_f32_16x16x32_bf16 v[140:143], v[192:195], a[124:127], v[140:143]\n"
"ds_read_b64_tr_b16 v[122:123], v229 offset:26624\n"
"v_and_b32 v93, s73, v93\n"
"v_mfma_f32_16x16x32_bf16 v[140:143], v[188:191], a[120:123], v[140:143]\n"
"ds_read_b64_tr_b16 v[224:225], v233 offset:24576\n"
"v_add_f32 v43, v43, v93\n"
"v_mfma_f32_16x16x32_bf16 v[140:143], v[184:187], a[116:119], v[140:143]\n"
"ds_read_b64_tr_b16 v[226:227], v233 offset:26624\n"
"v_lshlrev_b32 v250, 16, v94\n"
"v_mfma_f32_16x16x32_bf16 v[140:143], v[180:183], a[112:115], v[140:143]\n"
"buffer_store_dwordx4 v[164:167], v243, s[32:35], s17 offen offset:1024 sc1\n"
"v_and_b32 v94, s73, v94\n"
"v_mfma_f32_16x16x32_bf16 v[156:159], v[180:183], a[128:131], 0\n"
"v_add_f32 v45, v45, v94\n"
"v_lshlrev_b32 v251, 16, v95\n"
"v_mfma_f32_16x16x32_bf16 v[156:159], v[184:187], a[132:135], v[156:159]\n"
"v_and_b32 v95, s73, v95\n"
"v_add_f32 v47, v47, v95\n"
"v_mfma_f32_16x16x32_bf16 v[156:159], v[188:191], a[136:139], v[156:159]\n"
"v_add_f32 v40, v40, v248\n"
"v_add_f32 v42, v42, v249\n"
"v_mfma_f32_16x16x32_bf16 v[156:159], v[192:195], a[140:143], v[156:159]\n"
"v_add_u32 v241, 1, v241\n"
"v_add_f32 v44, v44, v250\n"
"v_mfma_f32_16x16x32_bf16 v[156:159], v[208:211], a[176:179], v[156:159]\n"
"v_add_f32 v46, v46, v251\n"
"v_mfma_f32_16x16x32_bf16 v[156:159], v[212:215], a[180:183], v[156:159]\n"
"v_mfma_f32_16x16x32_bf16 v[156:159], v[216:219], a[184:187], v[156:159]\n"
"v_mfma_f32_16x16x32_bf16 v[156:159], v[220:223], a[188:191], v[156:159]\n"
"s_waitcnt vmcnt(12)\n"
"v_lshlrev_b32 v246, 16, v196\n"
"v_and_b32 v196, s73, v196\n"
"v_mfma_f32_16x16x32_bf16 v[172:175], v[220:223], a[252:255], 0\n"
"ds_read_b64_tr_b16 v[220:221], v234 offset:24576\n"
"v_add_f32 v49, v49, v196\n"
"v_mfma_f32_16x16x32_bf16 v[172:175], v[216:219], a[248:251], v[172:175]\n"
"ds_read_b64_tr_b16 v[216:217], v230 offset:24576\n"
"v_lshlrev_b32 v247, 16, v197\n"
"v_mfma_f32_16x16x32_bf16 v[172:175], v[212:215], a[244:247], v[172:175]\n"
"ds_read_b64_tr_b16 v[212:213], v234 offset:16384\n"
"v_and_b32 v197, s73, v197\n"
"v_mfma_f32_16x16x32_bf16 v[172:175], v[208:211], a[240:243], v[172:175]\n"
"ds_read_b64_tr_b16 v[208:209], v230 offset:16384\n"
"v_add_f32 v51, v51, v197\n"
"v_mfma_f32_16x16x32_bf16 v[172:175], v[192:195], a[204:207], v[172:175]\n"
"ds_read_b64_tr_b16 v[192:193], v234 offset:8192\n"
"v_lshlrev_b32 v248, 16, v198\n"
"v_mfma_f32_16x16x32_bf16 v[172:175], v[188:191], a[200:203], v[172:175]\n"
"ds_read_b64_tr_b16 v[188:189], v230 offset:8192\n"
"v_and_b32 v198, s73, v198\n"
"v_mfma_f32_16x16x32_bf16 v[172:175], v[184:187], a[196:199], v[172:175]\n"
"ds_read_b64_tr_b16 v[184:185], v234 offset:0\n"
"v_add_f32 v53, v53, v198\n"
"v_mfma_f32_16x16x32_bf16 v[172:175], v[180:183], a[192:195], v[172:175]\n"
"ds_read_b64_tr_b16 v[180:181], v230 offset:0\n"
"v_lshlrev_b32 v249, 16, v199\n"
"v_mfma_f32_16x16x32_bf16 v[128:131], v[96:99], a[96:99], 0\n"
"ds_read_b64_tr_b16 v[182:183], v230 offset:2048\n"
"v_and_b32 v199, s73, v199\n"
"v_mfma_f32_16x16x32_bf16 v[128:131], v[100:103], a[100:103], v[128:131]\n"
"ds_read_b64_tr_b16 v[186:187], v234 offset:2048\n"
"v_add_f32 v55, v55, v199\n"
"v_mfma_f32_16x16x32_bf16 v[128:131], v[104:107], a[104:107], v[128:131]\n"
"s_waitcnt lgkmcnt(4)\n"
"ds_read_b64_tr_b16 v[190:191], v230 offset:10240\n"
"v_add_f32 v48, v48, v246\n"
"v_mfma_f32_16x16x32_bf16 v[128:131], v[108:111], a[108:111], v[128:131]\n"
"ds_read_b64_tr_b16 v[194:195], v234 offset:10240\n"
"v_add_f32 v50, v50, v247\n"
"v_mfma_f32_16x16x32_bf16 v[128:131], v[112:115], a[144:147], v[128:131]\n"
"s_waitcnt vmcnt(5)\n"
"ds_read_b64_tr_b16 v[210:211], v230 offset:18432\n"
"v_add_f32 v52, v52, v248\n"
"v_mfma_f32_16x16x32_bf16 v[128:131], v[116:119], a[148:151], v[128:131]\n"
"v_subrev_u32 v244, s21, v244\n"
"ds_read_b64_tr_b16 v[214:215], v234 offset:18432\n"
"v_mfma_f32_16x16x32_bf16 v[128:131], v[120:123], a[152:155], v[128:131]\n"
"v_cmp_lt_u32 vcc, s69, v244\n"
"ds_read_b64_tr_b16 v[218:219], v230 offset:26624\n"
"v_mfma_f32_16x16x32_bf16 v[128:131], v[224:227], a[156:159], v[128:131]\n"
"ds_read_b64_tr_b16 v[222:223], v234 offset:26624\n"
"s_cbranch_vccnz LGQ_%=\nLGQK_%=:\n"
"v_mfma_f32_16x16x32_bf16 v[144:147], v[224:227], a[172:175], 0\n"
"v_add_f32 v54, v54, v249\n"
"v_mfma_f32_16x16x32_bf16 v[144:147], v[120:123], a[168:171], v[144:147]\n"
"v_lshlrev_b32 v250, 16, v200\n"
"v_and_b32 v200, s73, v200\n"
"v_mfma_f32_16x16x32_bf16 v[144:147], v[116:119], a[164:167], v[144:147]\n"
"v_add_f32 v57, v57, v200\n"
"v_lshlrev_b32 v251, 16, v201\n"
"v_mfma_f32_16x16x32_bf16 v[144:147], v[112:115], a[160:163], v[144:147]\n"
"v_and_b32 v201, s73, v201\n"
"v_add_f32 v59, v59, v201\n"
"v_mfma_f32_16x16x32_bf16 v[144:147], v[108:111], a[124:127], v[144:147]\n"
"v_lshlrev_b32 v246, 16, v202\n"
"v_and_b32 v202, s73, v202\n"
"v_mfma_f32_16x16x32_bf16 v[144:147], v[104:107], a[120:123], v[144:147]\n"
"v_add_f32 v61, v61, v202\n"
"v_lshlrev_b32 v247, 16, v203\n"
"v_mfma_f32_16x16x32_bf16 v[144:147], v[100:103], a[116:119], v[144:147]\n"
"v_and_b32 v203, s73, v203\n"
"v_add_f32 v63, v63, v203\n"
"v_mfma_f32_16x16x32_bf16 v[144:147], v[96:99], a[112:115], v[144:147]\n"
"v_add_f32 v56, v56, v250\n"
"v_add_f32 v58, v58, v251\n"
"v_mfma_f32_16x16x32_bf16 v[160:163], v[96:99], a[128:131], 0\n"
"v_add_f32 v60, v60, v246\n"
"v_add_f32 v62, v62, v247\n"
"v_mfma_f32_16x16x32_bf16 v[160:163], v[100:103], a[132:135], v[160:163]\n"
"v_mfma_f32_16x16x32_bf16 v[160:163], v[104:107], a[136:139], v[160:163]\n"
"v_mfma_f32_16x16x32_bf16 v[160:163], v[108:111], a[140:143], v[160:163]\n"
"v_mfma_f32_16x16x32_bf16 v[160:163], v[112:115], a[176:179], v[160:163]\n"
"v_mfma_f32_16x16x32_bf16 v[160:163], v[116:119], a[180:183], v[160:163]\n"
"v_mfma_f32_16x16x32_bf16 v[160:163], v[120:123], a[184:187], v[160:163]\n"
"v_mfma_f32_16x16x32_bf16 v[160:163], v[224:227], a[188:191], v[160:163]\n"
"v_mfma_f32_16x16x32_bf16 v[176:179], v[224:227], a[252:255], 0\n"
"ds_read_b64_tr_b16 v[224:225], v235 offset:24576\n"
"v_mfma_f32_16x16x32_bf16 v[176:179], v[120:123], a[248:251], v[176:179]\n"
"ds_read_b64_tr_b16 v[120:121], v231 offset:24576\n"
"v_mfma_f32_16x16x32_bf16 v[176:179], v[116:119], a[244:247], v[176:179]\n"
"ds_read_b64_tr_b16 v[116:117], v235 offset:16384\n"
"v_mfma_f32_16x16x32_bf16 v[176:179], v[112:115], a[240:243], v[176:179]\n"
"ds_read_b64_tr_b16 v[112:113], v231 offset:16384\n"
"v_mfma_f32_16x16x32_bf16 v[176:179], v[108:111], a[204:207], v[176:179]\n"
"ds_read_b64_tr_b16 v[108:109], v235 offset:8192\n"
"v_mfma_f32_16x16x32_bf16 v[176:179], v[104:107], a[200:203], v[176:179]\n"
"ds_read_b64_tr_b16 v[104:105], v231 offset:8192\n"
"v_lshlrev_b32 v248, 16, v204\n"
"v_mfma_f32_16x16x32_bf16 v[176:179], v[100:103], a[196:199], v[176:179]\n"
"ds_read_b64_tr_b16 v[100:101], v235 offset:0\n"
"v_and_b32 v204, s73, v204\n"
"v_mfma_f32_16x16x32_bf16 v[176:179], v[96:99], a[192:195], v[176:179]\n"
"s_waitcnt lgkmcnt(4)\n"
"ds_read_b64_tr_b16 v[96:97], v231 offset:0\n"
"v_add_f32 v65, v65, v204\n"
"v_mfma_f32_16x16x32_bf16 v[132:135], v[180:183], a[96:99], 0\n"
"ds_read_b64_tr_b16 v[98:99], v231 offset:2048\n"
"v_lshlrev_b32 v249, 16, v205\n"
"v_mfma_f32_16x16x32_bf16 v[132:135], v[184:187], a[100:103], v[132:135]\n"
"ds_read_b64_tr_b16 v[102:103], v235 offset:2048\n"
"v_and_b32 v205, s73, v205\n"
"v_mfma_f32_16x16x32_bf16 v[132:135], v[188:191], a[104:107], v[132:135]\n"
"ds_read_b64_tr_b16 v[106:107], v231 offset:10240\n"
"v_add_f32 v67, v67, v205\n"
"v_mfma_f32_16x16x32_bf16 v[132:135], v[192:195], a[108:111], v[132:135]\n"
"ds_read_b64_tr_b16 v[110:111], v235 offset:10240\n"
"v_lshlrev_b32 v250, 16, v206\n"
"v_mfma_f32_16x16x32_bf16 v[132:135], v[208:211], a[144:147], v[132:135]\n"
"ds_read_b64_tr_b16 v[114:115], v231 offset:18432\n"
"v_and_b32 v206, s73, v206\n"
"v_mfma_f32_16x16x32_bf16 v[132:135], v[212:215], a[148:151], v[132:135]\n"
"ds_read_b64_tr_b16 v[118:119], v235 offset:18432\n"
"v_add_f32 v69, v69, v206\n"
"v_mfma_f32_16x16x32_bf16 v[132:135], v[216:219], a[152:155], v[132:135]\n"
"ds_read_b64_tr_b16 v[122:123], v231 offset:26624\n"
"v_lshlrev_b32 v251, 16, v207\n"
"v_mfma_f32_16x16x32_bf16 v[132:135], v[220:223], a[156:159], v[132:135]\n"
"ds_read_b64_tr_b16 v[226:227], v235 offset:26624\n"
"v_and_b32 v207, s73, v207\n"
"v_mfma_f32_16x16x32_bf16 v[148:151], v[220:223], a[172:175], 0\n"
"v_add_f32 v71, v71, v207\n"
"v_add_f32 v64, v64, v248\n"
"v_mfma_f32_16x16x32_bf16 v[148:151], v[216:219], a[168:171], v[148:151]\n"
"v_add_f32 v66, v66, v249\n"
"v_add_f32 v68, v68, v250\n"
"v_mfma_f32_16x16x32_bf16 v[148:151], v[212:215], a[164:167], v[148:151]\n"
"v_add_f32 v70, v70, v251\n"
"v_mfma_f32_16x16x32_bf16 v[148:151], v[208:211], a[160:163], v[148:151]\n"
"buffer_load_dwordx4 v[72:75], v243, s[32:35], s18 offen sc1\n"
"v_mfma_f32_16x16x32_bf16 v[148:151], v[192:195], a[124:127], v[148:151]\n"
"v_mfma_f32_16x16x32_bf16 v[148:151], v[188:191], a[120:123], v[148:151]\n"
"v_mfma_f32_16x16x32_bf16 v[148:151], v[184:187], a[116:119], v[148:151]\n"
"v_mfma_f32_16x16x32_bf16 v[148:151], v[180:183], a[112:115], v[148:151]\n"
"buffer_load_dwordx4 v[76:79], v243, s[32:35], s18 offen offset:1024 sc1\n"
"v_mfma_f32_16x16x32_bf16 v[164:167], v[180:183], a[128:131], 0\n"
"v_mfma_f32_16x16x32_bf16 v[164:167], v[184:187], a[132:135], v[164:167]\n"
"v_mfma_f32_16x16x32_bf16 v[164:167], v[188:191], a[136:139], v[164:167]\n"
"v_mfma_f32_16x16x32_bf16 v[164:167], v[192:195], a[140:143], v[164:167]\n"
"buffer_load_dwordx4 v[80:83], v243, s[32:35], s18 offen offset:2048 sc1\n"
"v_mfma_f32_16x16x32_bf16 v[164:167], v[208:211], a[176:179], v[164:167]\n"
"v_mfma_f32_16x16x32_bf16 v[164:167], v[212:215], a[180:183], v[164:167]\n"
"v_mfma_f32_16x16x32_bf16 v[164:167], v[216:219], a[184:187], v[164:167]\n"
"v_mfma_f32_16x16x32_bf16 v[164:167], v[220:223], a[188:191], v[164:167]\n"
"s_waitcnt lgkmcnt(0)\n"
"buffer_load_dwordx4 v[84:87], v243, s[32:35], s18 offen offset:3072 sc1\n"
"v_mfma_f32_16x16x32_bf16 v[136:139], v[96:99], a[96:99], 0\n"
"v_mfma_f32_16x16x32_bf16 v[136:139], v[100:103], a[100:103], v[136:139]\n"
"s_barrier\n"
"ds_read_b64_tr_b16 v[180:181], v228 offset:32768\n"
"v_mfma_f32_16x16x32_bf16 v[136:139], v[104:107], a[104:107], v[136:139]\n"
"ds_read_b64_tr_b16 v[182:183], v228 offset:34816\n"
"v_mfma_f32_16x16x32_bf16 v[136:139], v[108:111], a[108:111], v[136:139]\n"
"buffer_load_dwordx4 v[88:91], v243, s[32:35], s19 offen sc1\n"
"v_mfma_f32_16x16x32_bf16 v[136:139], v[112:115], a[144:147], v[136:139]\n"
"ds_read_b64_tr_b16 v[184:185], v232 offset:32768\n"
"v_mfma_f32_16x16x32_bf16 v[136:139], v[116:119], a[148:151], v[136:139]\n"
"ds_read_b64_tr_b16 v[186:187], v232 offset:34816\n"
"v_mfma_f32_16x16x32_bf16 v[136:139], v[120:123], a[152:155], v[136:139]\n"
"ds_read_b64_tr_b16 v[188:189], v228 offset:40960\n"
"v_mfma_f32_16x16x32_bf16 v[136:139], v[224:227], a[156:159], v[136:139]\n"
"buffer_load_dwordx4 v[92:95], v243, s[32:35], s19 offen offset:1024 sc1\n"
"v_mfma_f32_16x16x32_bf16 v[152:155], v[224:227], a[172:175], 0\n"
"ds_read_b64_tr_b16 v[190:191], v228 offset:43008\n"
"v_mfma_f32_16x16x32_bf16 v[152:155], v[120:123], a[168:171], v[152:155]\n"
"ds_read_b64_tr_b16 v[192:193], v232 offset:40960\n"
"v_mfma_f32_16x16x32_bf16 v[152:155], v[116:119], a[164:167], v[152:155]\n"
"s_waitcnt vmcnt(6)\n"
"ds_read_b64_tr_b16 v[194:195], v232 offset:43008\n"
"v_mfma_f32_16x16x32_bf16 v[152:155], v[112:115], a[160:163], v[152:155]\n"
"s_barrier\n"
"ds_read_b64_tr_b16 v[208:209], v228 offset:49152\n"
"v_mfma_f32_16x16x32_bf16 v[152:155], v[108:111], a[124:127], v[152:155]\n"
"ds_read_b64_tr_b16 v[210:211], v228 offset:51200\n"
"v_mfma_f32_16x16x32_bf16 v[152:155], v[104:107], a[120:123], v[152:155]\n"
"buffer_load_dwordx4 v[196:199], v243, s[32:35], s19 offen offset:2048 sc1\n"
"s_cmp_eq_u32 s42, 0\ns_cbranch_scc1 LGT_%=\nLGTK_%=:\n"
"v_mfma_f32_16x16x32_bf16 v[152:155], v[100:103], a[116:119], v[152:155]\n"
"ds_read_b64_tr_b16 v[212:213], v232 offset:49152\n"
"v_mfma_f32_16x16x32_bf16 v[152:155], v[96:99], a[112:115], v[152:155]\n"
"ds_read_b64_tr_b16 v[214:215], v232 offset:51200\n"
"v_mfma_f32_16x16x32_bf16 v[168:171], v[96:99], a[128:131], 0\n"
"ds_read_b64_tr_b16 v[216:217], v228 offset:57344\n"
"v_mfma_f32_16x16x32_bf16 v[168:171], v[100:103], a[132:135], v[168:171]\n"
"buffer_load_dwordx4 v[200:203], v236, s[32:35], s20 offen sc1\n"
"v_mfma_f32_16x16x32_bf16 v[168:171], v[104:107], a[136:139], v[168:171]\n"
"ds_read_b64_tr_b16 v[218:219], v228 offset:59392\n"
"v_mfma_f32_16x16x32_bf16 v[168:171], v[108:111], a[140:143], v[168:171]\n"
"ds_read_b64_tr_b16 v[220:221], v232 offset:57344\n"
"v_mfma_f32_16x16x32_bf16 v[168:171], v[112:115], a[176:179], v[168:171]\n"
"ds_read_b64_tr_b16 v[222:223], v232 offset:59392\n"
"v_mfma_f32_16x16x32_bf16 v[168:171], v[116:119], a[180:183], v[168:171]\n"
"buffer_load_dwordx4 v[204:207], v237, s[32:35], s20 offen sc1\n"
"v_mfma_f32_16x16x32_bf16 v[168:171], v[120:123], a[184:187], v[168:171]\n"
"s_sub_u32 s72, s72, 1\n"
"ds_read_b64_tr_b16 v[96:97], v229 offset:32768\n"
"v_mfma_f32_16x16x32_bf16 v[168:171], v[224:227], a[188:191], v[168:171]\n"
"s_waitcnt lgkmcnt(3)\n"
"s_and_b32 s72, s72, 7\n"
"ds_read_b64_tr_b16 v[98:99], v229 offset:34816\n"
"v_mfma_f32_16x16x32_bf16 v[124:127], v[180:183], a[48:51], v[124:127]\n"
"s_mul_i32 s20, s72, 286720\n"
"ds_read_b64_tr_b16 v[100:101], v233 offset:32768\n"
"v_mfma_f32_16x16x32_bf16 v[124:127], v[184:187], a[52:55], v[124:127]\n"
"s_mul_i32 s18, s61, 28672\n"
"ds_read_b64_tr_b16 v[102:103], v233 offset:34816\n"
"v_mfma_f32_16x16x32_bf16 v[124:127], v[188:191], a[56:59], v[124:127]\n"
"s_add_u32 s18, s18, s20\n"
"ds_read_b64_tr_b16 v[104:105], v229 offset:40960\n"
"v_mfma_f32_16x16x32_bf16 v[124:127], v[192:195], a[60:63], v[124:127]\n"
"s_cmp_ge_u32 s47, 3\ns_cselect_b32 s18, s18, s77\n"
"ds_read_b64_tr_b16 v[106:107], v229 offset:43008\n"
"v_mfma_f32_16x16x32_bf16 v[124:127], v[208:211], a[0:3], v[124:127]\n"
"s_add_u32 s19, s18, 4096\n"
"ds_read_b64_tr_b16 v[108:109], v233 offset:40960\n"
"v_mfma_f32_16x16x32_bf16 v[124:127], v[212:215], a[4:7], v[124:127]\n"
"s_cmp_ge_u32 s47, 2\ns_cselect_b32 s20, s20, s77\ns_cselect_b32 s69, 15, -1\n"
"ds_read_b64_tr_b16 v[110:111], v233 offset:43008\n"
"v_mfma_f32_16x16x32_bf16 v[124:127], v[216:219], a[8:11], v[124:127]\n"
"s_waitcnt lgkmcnt(4)\n"
"s_add_u32 s21, s21, 1\n"
"ds_read_b64_tr_b16 v[112:113], v229 offset:49152\n"
"v_mfma_f32_16x16x32_bf16 v[124:127], v[220:223], a[12:15], v[124:127]\n"
"ds_read_b64_tr_b16 v[114:115], v229 offset:51200\n"
"v_mfma_f32_16x16x32_bf16 v[140:143], v[220:223], a[28:31], v[140:143]\n"
"ds_read_b64_tr_b16 v[116:117], v233 offset:49152\n"
"v_mfma_f32_16x16x32_bf16 v[140:143], v[216:219], a[24:27], v[140:143]\n"
"ds_read_b64_tr_b16 v[118:119], v233 offset:51200\n"
"v_mfma_f32_16x16x32_bf16 v[140:143], v[212:215], a[20:23], v[140:143]\n"
"ds_read_b64_tr_b16 v[120:121], v229 offset:57344\n"
"v_mfma_f32_16x16x32_bf16 v[140:143], v[208:211], a[16:19], v[140:143]\n"
"ds_read_b64_tr_b16 v[122:123], v229 offset:59392\n"
"v_mfma_f32_16x16x32_bf16 v[140:143], v[192:195], a[76:79], v[140:143]\n"
"ds_read_b64_tr_b16 v[224:225], v233 offset:57344\n"
"v_mfma_f32_16x16x32_bf16 v[140:143], v[188:191], a[72:75], v[140:143]\n"
"ds_read_b64_tr_b16 v[226:227], v233 offset:59392\n"
"v_add_f32 v0, v0, v124\n"
"v_mfma_f32_16x16x32_bf16 v[140:143], v[184:187], a[68:71], v[140:143]\n"
"v_add_f32 v1, v1, v125\n"
"v_add_f32 v2, v2, v126\n"
"v_mfma_f32_16x16x32_bf16 v[140:143], v[180:183], a[64:67], v[140:143]\n"
"v_add_f32 v3, v3, v127\n"
"v_mfma_f32_16x16x32_bf16 v[156:159], v[180:183], a[80:83], v[156:159]\n"
"v_mfma_f32_16x16x32_bf16 v[156:159], v[184:187], a[84:87], v[156:159]\n"
"v_mfma_f32_16x16x32_bf16 v[156:159], v[188:191], a[88:91], v[156:159]\n"
"v_mfma_f32_16x16x32_bf16 v[156:159], v[192:195], a[92:95], v[156:159]\n"
"v_mfma_f32_16x16x32_bf16 v[156:159], v[208:211], a[32:35], v[156:159]\n"
"v_mfma_f32_16x16x32_bf16 v[156:159], v[212:215], a[36:39], v[156:159]\n"
"s_nop 0\n"
"v_add_f32 v16, v16, v140\n"
"v_add_f32 v17, v17, v141\n"
"v_mfma_f32_16x16x32_bf16 v[156:159], v[216:219], a[40:43], v[156:159]\n"
"v_add_f32 v18, v18, v142\n"
"v_add_f32 v19, v19, v143\n"
"v_mfma_f32_16x16x32_bf16 v[156:159], v[220:223], a[44:47], v[156:159]\n"
"v_mfma_f32_16x16x32_bf16 v[172:175], v[220:223], a[228:231], v[172:175]\n"
"ds_read_b64_tr_b16 v[220:221], v234 offset:57344\n"
"v_mfma_f32_16x16x32_bf16 v[172:175], v[216:219], a[224:227], v[172:175]\n"
"ds_read_b64_tr_b16 v[216:217], v230 offset:57344\n"
"v_mfma_f32_16x16x32_bf16 v[172:175], v[212:215], a[212:215], v[172:175]\n"
"ds_read_b64_tr_b16 v[212:213], v234 offset:49152\n"
"v_mfma_f32_16x16x32_bf16 v[172:175], v[208:211], a[208:211], v[172:175]\n"
"ds_read_b64_tr_b16 v[208:209], v230 offset:49152\n"
"v_mfma_f32_16x16x32_bf16 v[172:175], v[192:195], a[236:239], v[172:175]\n"
"ds_read_b64_tr_b16 v[192:193], v234 offset:40960\n"
"v_mfma_f32_16x16x32_bf16 v[172:175], v[188:191], a[232:235], v[172:175]\n"
"ds_read_b64_tr_b16 v[188:189], v230 offset:40960\n"
"v_add_f32 v32, v32, v156\n"
"v_mfma_f32_16x16x32_bf16 v[172:175], v[184:187], a[220:223], v[172:175]\n"
"ds_read_b64_tr_b16 v[184:185], v234 offset:32768\n"
"v_add_f32 v33, v33, v157\n"
"v_mfma_f32_16x16x32_bf16 v[172:175], v[180:183], a[216:219], v[172:175]\n"
"ds_read_b64_tr_b16 v[180:181], v230 offset:32768\n"
"v_add_f32 v34, v34, v158\n"
"v_mfma_f32_16x16x32_bf16 v[128:131], v[96:99], a[48:51], v[128:131]\n"
"ds_read_b64_tr_b16 v[182:183], v230 offset:34816\n"
"v_add_f32 v35, v35, v159\n"
"v_mfma_f32_16x16x32_bf16 v[128:131], v[100:103], a[52:55], v[128:131]\n"
"s_waitcnt lgkmcnt(4)\n"
"ds_read_b64_tr_b16 v[186:187], v234 offset:34816\n"
"v_mfma_f32_16x16x32_bf16 v[128:131], v[104:107], a[56:59], v[128:131]\n"
"ds_read_b64_tr_b16 v[190:191], v230 offset:43008\n"
"v_mfma_f32_16x16x32_bf16 v[128:131], v[108:111], a[60:63], v[128:131]\n"
"ds_read_b64_tr_b16 v[194:195], v234 offset:43008\n"
"v_mfma_f32_16x16x32_bf16 v[128:131], v[112:115], a[0:3], v[128:131]\n"
"ds_read_b64_tr_b16 v[210:211], v230 offset:51200\n"
"v_mfma_f32_16x16x32_bf16 v[128:131], v[116:119], a[4:7], v[128:131]\n"
"ds_read_b64_tr_b16 v[214:215], v234 offset:51200\n"
"v_add_f32 v48, v48, v172\n"
"v_mfma_f32_16x16x32_bf16 v[128:131], v[120:123], a[8:11], v[128:131]\n"
"ds_read_b64_tr_b16 v[218:219], v230 offset:59392\n"
"v_add_f32 v49, v49, v173\n"
"v_mfma_f32_16x16x32_bf16 v[128:131], v[224:227], a[12:15], v[128:131]\n"
"ds_read_b64_tr_b16 v[222:223], v234 offset:59392\n"
"v_add_f32 v50, v50, v174\n"
"v_mfma_f32_16x16x32_bf16 v[144:147], v[224:227], a[28:31], v[144:147]\n"
"v_add_f32 v51, v51, v175\n"
"v_mfma_f32_16x16x32_bf16 v[144:147], v[120:123], a[24:27], v[144:147]\n"
"v_mfma_f32_16x16x32_bf16 v[144:147], v[116:119], a[20:23], v[144:147]\n"
"v_mfma_f32_16x16x32_bf16 v[144:147], v[112:115], a[16:19], v[144:147]\n"
"v_mfma_f32_16x16x32_bf16 v[144:147], v[108:111], a[76:79], v[144:147]\n"
"v_mfma_f32_16x16x32_bf16 v[144:147], v[104:107], a[72:75], v[144:147]\n"
"v_add_f32 v4, v4, v128\n"
"v_add_f32 v5, v5, v129\n"
"v_mfma_f32_16x16x32_bf16 v[144:147], v[100:103], a[68:71], v[144:147]\n"
"v_add_f32 v6, v6, v130\n"
"v_add_f32 v7, v7, v131\n"
"v_mfma_f32_16x16x32_bf16 v[144:147], v[96:99], a[64:67], v[144:147]\n"
"v_mfma_f32_16x16x32_bf16 v[160:163], v[96:99], a[80:83], v[160:163]\n"
"v_mfma_f32_16x16x32_bf16 v[160:163], v[100:103], a[84:87], v[160:163]\n"
"v_mfma_f32_16x16x32_bf16 v[160:163], v[104:107], a[88:91], v[160:163]\n"
"v_mfma_f32_16x16x32_bf16 v[160:163], v[108:111], a[92:95], v[160:163]\n"
"v_mfma_f32_16x16x32_bf16 v[160:163], v[112:115], a[32:35], v[160:163]\n"
"v_mfma_f32_16x16x32_bf16 v[160:163], v[116:119], a[36:39], v[160:163]\n"
"s_waitcnt vmcnt(8)\n"
"s_nop 0\n"
"v_add_f32 v20, v20, v144\n"
"v_add_f32 v21, v21, v145\n"
"v_mfma_f32_16x16x32_bf16 v[160:163], v[120:123], a[40:43], v[160:163]\n"
"v_add_f32 v22, v22, v146\n"
"v_add_f32 v23, v23, v147\n"
"v_mfma_f32_16x16x32_bf16 v[160:163], v[224:227], a[44:47], v[160:163]\n"
"v_lshlrev_b32 v246, 16, v72\n"
"v_and_b32 v72, s73, v72\n"
"v_mfma_f32_16x16x32_bf16 v[176:179], v[224:227], a[228:231], v[176:179]\n"
"ds_read_b64_tr_b16 v[224:225], v235 offset:57344\n"
"v_add_f32 v0, v0, v246\n"
"v_mfma_f32_16x16x32_bf16 v[176:179], v[120:123], a[224:227], v[176:179]\n"
"s_waitcnt vmcnt(7)\n"
"ds_read_b64_tr_b16 v[120:121], v231 offset:57344\n"
"v_add_f32 v1, v1, v72\n"
"v_mfma_f32_16x16x32_bf16 v[176:179], v[116:119], a[212:215], v[176:179]\n"
"ds_read_b64_tr_b16 v[116:117], v235 offset:49152\n"
"v_lshlrev_b32 v247, 16, v73\n"
"v_mfma_f32_16x16x32_bf16 v[176:179], v[112:115], a[208:211], v[176:179]\n"
"ds_read_b64_tr_b16 v[112:113], v231 offset:49152\n"
"v_and_b32 v73, s73, v73\n"
"v_mfma_f32_16x16x32_bf16 v[176:179], v[108:111], a[236:239], v[176:179]\n"
"ds_read_b64_tr_b16 v[108:109], v235 offset:40960\n"
"v_add_f32 v2, v2, v247\n"
"v_mfma_f32_16x16x32_bf16 v[176:179], v[104:107], a[232:235], v[176:179]\n"
"s_waitcnt vmcnt(6)\n"
"ds_read_b64_tr_b16 v[104:105], v231 offset:40960\n"
"v_add_f32 v3, v3, v73\n"
"v_mfma_f32_16x16x32_bf16 v[176:179], v[100:103], a[220:223], v[176:179]\n"
"ds_read_b64_tr_b16 v[100:101], v235 offset:32768\n"
"v_lshlrev_b32 v248, 16, v74\n"
"v_mfma_f32_16x16x32_bf16 v[176:179], v[96:99], a[216:219], v[176:179]\n"
"s_waitcnt lgkmcnt(4)\n"
"ds_read_b64_tr_b16 v[96:97], v231 offset:32768\n"
"v_and_b32 v74, s73, v74\n"
"v_mfma_f32_16x16x32_bf16 v[132:135], v[180:183], a[48:51], v[132:135]\n"
"ds_read_b64_tr_b16 v[98:99], v231 offset:34816\n"
"v_add_f32 v4, v4, v248\n"
"v_mfma_f32_16x16x32_bf16 v[132:135], v[184:187], a[52:55], v[132:135]\n"
"s_waitcnt vmcnt(5)\n"
"ds_read_b64_tr_b16 v[102:103], v235 offset:34816\n"
"v_add_f32 v5, v5, v74\n"
"v_mfma_f32_16x16x32_bf16 v[132:135], v[188:191], a[56:59], v[132:135]\n"
"v_lshlrev_b32_e64 v245, 2, s72\n"
"ds_read_b64_tr_b16 v[106:107], v231 offset:43008\n"
"v_lshlrev_b32 v249, 16, v75\n"
"v_mfma_f32_16x16x32_bf16 v[132:135], v[192:195], a[60:63], v[132:135]\n"
"global_load_dword v244, v245, s[6:7] sc1\n"
"v_mfma_f32_16x16x32_bf16 v[132:135], v[208:211], a[0:3], v[132:135]\n"
"ds_read_b64_tr_b16 v[110:111], v235 offset:43008\n"
"v_and_b32 v75, s73, v75\n"
"v_mfma_f32_16x16x32_bf16 v[132:135], v[212:215], a[4:7], v[132:135]\n"
"s_waitcnt vmcnt(5)\n"
"ds_read_b64_tr_b16 v[114:115], v231 offset:51200\n"
"v_add_f32 v6, v6, v249\n"
"v_mfma_f32_16x16x32_bf16 v[132:135], v[216:219], a[8:11], v[132:135]\n"
"ds_read_b64_tr_b16 v[118:119], v235 offset:51200\n"
"v_add_f32 v7, v7, v75\n"
"v_mfma_f32_16x16x32_bf16 v[132:135], v[220:223], a[12:15], v[132:135]\n"
"ds_read_b64_tr_b16 v[122:123], v231 offset:59392\n"
"v_add_f32 v36, v36, v160\n"
"v_mfma_f32_16x16x32_bf16 v[148:151], v[220:223], a[28:31], v[148:151]\n"
"ds_read_b64_tr_b16 v[226:227], v235 offset:59392\n"
"v_add_f32 v37, v37, v161\n"
"v_mfma_f32_16x16x32_bf16 v[148:151], v[216:219], a[24:27], v[148:151]\n"
"s_waitcnt vmcnt(4)\n"
"v_add_f32 v38, v38, v162\n"
"v_add_f32 v39, v39, v163\n"
"v_mfma_f32_16x16x32_bf16 v[148:151], v[212:215], a[20:23], v[148:151]\n"
"v_lshlrev_b32 v250, 16, v76\n"
"v_and_b32 v76, s73, v76\n"
"v_mfma_f32_16x16x32_bf16 v[148:151], v[208:211], a[16:19], v[148:151]\n"
"v_add_f32 v8, v8, v250\n"
"v_add_f32 v9, v9, v76\n"
"v_mfma_f32_16x16x32_bf16 v[148:151], v[192:195], a[76:79], v[148:151]\n"
"v_lshlrev_b32 v251, 16, v77\n"
"v_and_b32 v77, s73, v77\n"
"v_mfma_f32_16x16x32_bf16 v[148:151], v[188:191], a[72:75], v[148:151]\n"
"v_add_f32 v10, v10, v251\n"
"v_add_f32 v11, v11, v77\n"
"v_mfma_f32_16x16x32_bf16 v[148:151], v[184:187], a[68:71], v[148:151]\n"
"v_lshlrev_b32 v246, 16, v78\n"
"v_and_b32 v78, s73, v78\n"
"v_mfma_f32_16x16x32_bf16 v[148:151], v[180:183], a[64:67], v[148:151]\n"
"s_waitcnt vmcnt(3)\n"
"v_add_f32 v12, v12, v246\n"
"v_add_f32 v13, v13, v78\n"
"v_mfma_f32_16x16x32_bf16 v[164:167], v[180:183], a[80:83], v[164:167]\n"
"v_lshlrev_b32 v247, 16, v79\n"
"v_and_b32 v79, s73, v79\n"
"v_mfma_f32_16x16x32_bf16 v[164:167], v[184:187], a[84:87], v[164:167]\n"
"v_add_f32 v14, v14, v247\n"
"v_add_f32 v15, v15, v79\n"
"v_mfma_f32_16x16x32_bf16 v[164:167], v[188:191], a[88:91], v[164:167]\n"
"v_lshlrev_b32 v248, 16, v80\n"
"v_and_b32 v80, s73, v80\n"
"v_mfma_f32_16x16x32_bf16 v[164:167], v[192:195], a[92:95], v[164:167]\n"
"s_waitcnt vmcnt(2)\n"
"v_add_f32 v16, v16, v248\n"
"v_add_f32 v17, v17, v80\n"
"v_mfma_f32_16x16x32_bf16 v[164:167], v[208:211], a[32:35], v[164:167]\n"
"v_lshlrev_b32 v249, 16, v81\n"
"v_and_b32 v81, s73, v81\n"
"v_mfma_f32_16x16x32_bf16 v[164:167], v[212:215], a[36:39], v[164:167]\n"
"v_add_f32 v18, v18, v249\n"
"v_add_f32 v19, v19, v81\n"
"v_mfma_f32_16x16x32_bf16 v[164:167], v[216:219], a[40:43], v[164:167]\n"
"v_lshlrev_b32 v250, 16, v82\n"
"v_and_b32 v82, s73, v82\n"
"v_mfma_f32_16x16x32_bf16 v[164:167], v[220:223], a[44:47], v[164:167]\n"
"s_waitcnt lgkmcnt(0)\n"
"s_waitcnt vmcnt(1)\n"
"v_add_f32 v20, v20, v250\n"
"v_add_f32 v21, v21, v82\n"
"v_mfma_f32_16x16x32_bf16 v[136:139], v[96:99], a[48:51], v[136:139]\n"
"v_lshlrev_b32 v251, 16, v83\n"
"v_and_b32 v83, s73, v83\n"
"v_mfma_f32_16x16x32_bf16 v[136:139], v[100:103], a[52:55], v[136:139]\n"
"v_add_f32 v22, v22, v251\n"
"v_add_f32 v23, v23, v83\n"
"v_mfma_f32_16x16x32_bf16 v[136:139], v[104:107], a[56:59], v[136:139]\n"
"v_add_f32 v52, v52, v176\n"
"v_add_f32 v53, v53, v177\n"
"v_mfma_f32_16x16x32_bf16 v[136:139], v[108:111], a[60:63], v[136:139]\n"
"v_add_f32 v54, v54, v178\n"
"v_add_f32 v55, v55, v179\n"
"v_mfma_f32_16x16x32_bf16 v[136:139], v[112:115], a[0:3], v[136:139]\n"
"v_lshlrev_b32 v246, 16, v84\n"
"v_and_b32 v84, s73, v84\n"
"v_mfma_f32_16x16x32_bf16 v[136:139], v[116:119], a[4:7], v[136:139]\n"
"v_add_f32 v24, v24, v246\n"
"v_add_f32 v25, v25, v84\n"
"v_mfma_f32_16x16x32_bf16 v[136:139], v[120:123], a[8:11], v[136:139]\n"
"v_lshlrev_b32 v247, 16, v85\n"
"v_and_b32 v85, s73, v85\n"
"v_mfma_f32_16x16x32_bf16 v[136:139], v[224:227], a[12:15], v[136:139]\n"
"v_add_f32 v26, v26, v247\n"
"v_add_f32 v27, v27, v85\n"
"v_mfma_f32_16x16x32_bf16 v[152:155], v[224:227], a[28:31], v[152:155]\n"
"v_lshlrev_b32 v248, 16, v86\n"
"v_and_b32 v86, s73, v86\n"
"v_mfma_f32_16x16x32_bf16 v[152:155], v[120:123], a[24:27], v[152:155]\n"
"v_add_f32 v28, v28, v248\n"
"v_add_f32 v29, v29, v86\n"
"v_mfma_f32_16x16x32_bf16 v[152:155], v[116:119], a[20:23], v[152:155]\n"
"v_lshlrev_b32 v249, 16, v87\n"
"v_and_b32 v87, s73, v87\n"
"v_mfma_f32_16x16x32_bf16 v[152:155], v[112:115], a[16:19], v[152:155]\n"
"v_add_f32 v30, v30, v249\n"
"v_add_f32 v31, v31, v87\n"
"v_mfma_f32_16x16x32_bf16 v[152:155], v[108:111], a[76:79], v[152:155]\n"
"v_lshlrev_b32 v250, 16, v88\n"
"v_and_b32 v88, s73, v88\n"
"v_mfma_f32_16x16x32_bf16 v[152:155], v[104:107], a[72:75], v[152:155]\n"
"v_add_f32 v32, v32, v250\n"
"v_add_f32 v33, v33, v88\n"
"v_mfma_f32_16x16x32_bf16 v[152:155], v[100:103], a[68:71], v[152:155]\n"
"v_lshlrev_b32 v251, 16, v89\n"
"v_and_b32 v89, s73, v89\n"
"v_mfma_f32_16x16x32_bf16 v[152:155], v[96:99], a[64:67], v[152:155]\n"
"v_add_f32 v34, v34, v251\n"
"v_add_f32 v35, v35, v89\n"
"v_mfma_f32_16x16x32_bf16 v[168:171], v[96:99], a[80:83], v[168:171]\n"
"v_lshlrev_b32 v246, 16, v90\n"
"v_and_b32 v90, s73, v90\n"
"v_mfma_f32_16x16x32_bf16 v[168:171], v[100:103], a[84:87], v[168:171]\n"
"v_add_f32 v36, v36, v246\n"
"v_add_f32 v37, v37, v90\n"
"v_mfma_f32_16x16x32_bf16 v[168:171], v[104:107], a[88:91], v[168:171]\n"
"v_lshlrev_b32 v247, 16, v91\n"
"v_and_b32 v91, s73, v91\n"
"v_mfma_f32_16x16x32_bf16 v[168:171], v[108:111], a[92:95], v[168:171]\n"
"v_add_f32 v38, v38, v247\n"
"v_add_f32 v39, v39, v91\n"
"v_mfma_f32_16x16x32_bf16 v[168:171], v[112:115], a[32:35], v[168:171]\n"
"v_lshlrev_b32 v248, 16, v92\n"
"v_and_b32 v92, s73, v92\n"
"v_mfma_f32_16x16x32_bf16 v[168:171], v[116:119], a[36:39], v[168:171]\n"
"v_add_f32 v40, v40, v248\n"
"v_add_f32 v41, v41, v92\n"
"v_mfma_f32_16x16x32_bf16 v[168:171], v[120:123], a[40:43], v[168:171]\n"
"v_lshlrev_b32 v249, 16, v93\n"
"v_and_b32 v93, s73, v93\n"
"v_mfma_f32_16x16x32_bf16 v[168:171], v[224:227], a[44:47], v[168:171]\n"
"v_add_f32 v42, v42, v249\n"
"v_add_f32 v43, v43, v93\n"
"s_waitcnt vmcnt(0)\n"
"v_subrev_u32 v244, s21, v244\n"
"v_cmp_lt_u32 vcc, s69, v244\n"
"v_lshlrev_b32 v250, 16, v94\n"
"v_and_b32 v94, s73, v94\n"
"v_add_f32 v44, v44, v250\n"
"v_add_f32 v45, v45, v94\n"
"v_lshlrev_b32 v251, 16, v95\n"
"v_and_b32 v95, s73, v95\n"
"v_add_f32 v46, v46, v251\n"
"v_add_f32 v47, v47, v95\n"
"v_lshlrev_b32 v246, 16, v196\n"
"v_and_b32 v196, s73, v196\n"
"v_add_f32 v48, v48, v246\n"
"v_add_f32 v49, v49, v196\n"
"v_lshlrev_b32 v247, 16, v197\n"
"v_and_b32 v197, s73, v197\n"
"v_add_f32 v50, v50, v247\n"
"v_add_f32 v51, v51, v197\n"
"v_lshlrev_b32 v248, 16, v198\n"
"v_and_b32 v198, s73, v198\n"
"v_add_f32 v52, v52, v248\n"
"v_add_f32 v53, v53, v198\n"
"v_lshlrev_b32 v249, 16, v199\n"
"v_and_b32 v199, s73, v199\n"
"v_add_f32 v54, v54, v249\n"
"v_add_f32 v55, v55, v199\n"
"v_lshlrev_b32 v250, 16, v200\n"
"v_and_b32 v200, s73, v200\n"
"v_add_f32 v56, v56, v250\n"
"v_add_f32 v57, v57, v200\n"
"v_lshlrev_b32 v251, 16, v201\n"
"v_and_b32 v201, s73, v201\n"
"v_add_f32 v58, v58, v251\n"
"v_add_f32 v59, v59, v201\n"
"v_lshlrev_b32 v246, 16, v202\n"
"v_and_b32 v202, s73, v202\n"
"v_add_f32 v61, v61, v202\n"
"v_lshlrev_b32 v247, 16, v203\n"
"v_and_b32 v203, s73, v203\n"
"v_add_f32 v63, v63, v203\n"
"v_lshlrev_b32 v248, 16, v204\n"
"v_and_b32 v204, s73, v204\n"
"v_add_f32 v65, v65, v204\n"
"v_lshlrev_b32 v249, 16, v205\n"
"v_and_b32 v205, s73, v205\n"
"v_add_f32 v67, v67, v205\n"
"v_lshlrev_b32 v250, 16, v206\n"
"v_and_b32 v206, s73, v206\n"
"v_add_f32 v69, v69, v206\n"
"v_lshlrev_b32 v251, 16, v207\n"
"v_and_b32 v207, s73, v207\n"
"v_add_f32 v71, v71, v207\n"
"s_cbranch_vccnz LGR_%=\nLGRK_%=:\n"
"buffer_load_dwordx4 v[72:75], v243, s[32:35], s18 offen sc1\n"
"buffer_load_dwordx4 v[76:79], v243, s[32:35], s18 offen offset:1024 sc1\n"
"buffer_load_dwordx4 v[80:83], v243, s[32:35], s18 offen offset:2048 sc1\n"
"buffer_load_dwordx4 v[84:87], v243, s[32:35], s18 offen offset:3072 sc1\n"
"buffer_load_dwordx4 v[88:91], v243, s[32:35], s19 offen sc1\n"
"buffer_load_dwordx4 v[92:95], v243, s[32:35], s19 offen offset:1024 sc1\n"
"buffer_load_dwordx4 v[196:199], v243, s[32:35], s19 offen offset:2048 sc1\n"
"buffer_load_dwordx4 v[200:203], v236, s[32:35], s20 offen sc1\n"
"buffer_load_dwordx4 v[204:207], v237, s[32:35], s20 offen sc1\n"
"s_cmp_eq_u32 s93, 0\ns_cbranch_scc1 LEP_%=\nLEPK_%=:\n"
"v_add_f32 v60, v60, v246\n"
"v_add_f32 v62, v62, v247\n"
"v_add_f32 v64, v64, v248\n"
"v_add_f32 v66, v66, v249\n"
"s_waitcnt vmcnt(8)\n"
"v_add_f32 v8, v8, v132\n"
"v_add_f32 v9, v9, v133\n"
"v_add_f32 v10, v10, v134\n"
"v_add_f32 v11, v11, v135\n"
"v_add_f32 v68, v68, v250\n"
"v_add_f32 v70, v70, v251\n"
"v_add_f32 v12, v12, v136\n"
"v_add_f32 v13, v13, v137\n"
"v_add_f32 v14, v14, v138\n"
"v_add_f32 v15, v15, v139\n"
"v_lshlrev_b32 v246, 16, v72\n"
"v_and_b32 v72, s73, v72\n"
"v_add_f32 v0, v0, v246\n"
"v_add_f32 v1, v1, v72\n"
"v_lshlrev_b32 v247, 16, v73\n"
"v_and_b32 v73, s73, v73\n"
"v_add_f32 v2, v2, v247\n"
"v_add_f32 v3, v3, v73\n"
"v_lshlrev_b32 v248, 16, v74\n"
"v_and_b32 v74, s73, v74\n"
"v_add_f32 v4, v4, v248\n"
"v_add_f32 v5, v5, v74\n"
"v_lshlrev_b32 v249, 16, v75\n"
"v_and_b32 v75, s73, v75\n"
"v_add_f32 v6, v6, v249\n"
"v_add_f32 v7, v7, v75\n"
"v_cvt_pk_bf16_f32 v0, v0, v1\n"
"v_cvt_pk_bf16_f32 v1, v2, v3\n"
"v_cvt_pk_bf16_f32 v2, v4, v5\n"
"v_cvt_pk_bf16_f32 v3, v6, v7\n"
"s_waitcnt vmcnt(7)\n"
"v_permlane16_swap_b32 v0, v2\n"
"v_permlane16_swap_b32 v1, v3\n"
"v_lshlrev_b32 v250, 16, v76\n"
"v_and_b32 v76, s73, v76\n"
"v_add_f32 v8, v8, v250\n"
"v_add_f32 v9, v9, v76\n"
"v_lshlrev_b32 v251, 16, v77\n"
"v_and_b32 v77, s73, v77\n"
"v_add_f32 v10, v10, v251\n"
"v_add_f32 v11, v11, v77\n"
"v_lshlrev_b32 v246, 16, v78\n"
"v_and_b32 v78, s73, v78\n"
"v_add_f32 v12, v12, v246\n"
"v_add_f32 v13, v13, v78\n"
"v_lshlrev_b32 v247, 16, v79\n"
"v_and_b32 v79, s73, v79\n"
"v_add_f32 v14, v14, v247\n"
"v_add_f32 v15, v15, v79\n"
"v_cvt_pk_bf16_f32 v8, v8, v9\n"
"v_cvt_pk_bf16_f32 v9, v10, v11\n"
"v_cvt_pk_bf16_f32 v10, v12, v13\n"
"v_cvt_pk_bf16_f32 v11, v14, v15\n"
"s_waitcnt vmcnt(6)\n"
"s_mov_b32 s40, 0\n"
"buffer_store_dwordx4 v[0:3], v238, s[36:39], s40 offen\n"
"v_add_f32 v24, v24, v148\n"
"v_add_f32 v25, v25, v149\n"
"v_add_f32 v26, v26, v150\n"
"v_add_f32 v27, v27, v151\n"
"v_add_f32 v28, v28, v152\n"
"v_add_f32 v29, v29, v153\n"
"v_add_f32 v30, v30, v154\n"
"v_add_f32 v31, v31, v155\n"
"v_permlane16_swap_b32 v8, v10\n"
"v_permlane16_swap_b32 v9, v11\n"
"v_lshlrev_b32 v248, 16, v80\n"
"v_and_b32 v80, s73, v80\n"
"v_add_f32 v16, v16, v248\n"
"v_add_f32 v17, v17, v80\n"
"v_lshlrev_b32 v249, 16, v81\n"
"v_and_b32 v81, s73, v81\n"
"v_add_f32 v18, v18, v249\n"
"v_add_f32 v19, v19, v81\n"
"v_lshlrev_b32 v250, 16, v82\n"
"v_and_b32 v82, s73, v82\n"
"v_add_f32 v20, v20, v250\n"
"v_add_f32 v21, v21, v82\n"
"v_lshlrev_b32 v251, 16, v83\n"
"v_and_b32 v83, s73, v83\n"
"v_add_f32 v22, v22, v251\n"
"v_add_f32 v23, v23, v83\n"
"v_cvt_pk_bf16_f32 v16, v16, v17\n"
"v_cvt_pk_bf16_f32 v17, v18, v19\n"
"v_cvt_pk_bf16_f32 v18, v20, v21\n"
"v_cvt_pk_bf16_f32 v19, v22, v23\n"
"s_waitcnt vmcnt(6)\n"
"s_mov_b32 s40, 0\n"
"buffer_store_dwordx4 v[8:11], v239, s[36:39], s40 offen\n"
"v_permlane16_swap_b32 v16, v18\n"
"v_permlane16_swap_b32 v17, v19\n"
"v_lshlrev_b32 v246, 16, v84\n"
"v_and_b32 v84, s73, v84\n"
"v_add_f32 v24, v24, v246\n"
"v_add_f32 v25, v25, v84\n"
"v_lshlrev_b32 v247, 16, v85\n"
"v_and_b32 v85, s73, v85\n"
"v_add_f32 v26, v26, v247\n"
"v_add_f32 v27, v27, v85\n"
"v_lshlrev_b32 v248, 16, v86\n"
"v_and_b32 v86, s73, v86\n"
"v_add_f32 v28, v28, v248\n"
"v_add_f32 v29, v29, v86\n"
"v_lshlrev_b32 v249, 16, v87\n"
"v_and_b32 v87, s73, v87\n"
"v_add_f32 v30, v30, v249\n"
"v_add_f32 v31, v31, v87\n"
"v_cvt_pk_bf16_f32 v24, v24, v25\n"
"v_cvt_pk_bf16_f32 v25, v26, v27\n"
"v_cvt_pk_bf16_f32 v26, v28, v29\n"
"v_cvt_pk_bf16_f32 v27, v30, v31\n"
"s_waitcnt vmcnt(6)\n"
"s_mov_b32 s40, 40960\n"
"buffer_store_dwordx4 v[16:19], v238, s[36:39], s40 offen\n"
"v_add_f32 v40, v40, v164\n"
"v_add_f32 v41, v41, v165\n"
"v_add_f32 v42, v42, v166\n"
"v_add_f32 v43, v43, v167\n"
"v_add_f32 v44, v44, v168\n"
"v_add_f32 v45, v45, v169\n"
"v_add_f32 v46, v46, v170\n"
"v_add_f32 v47, v47, v171\n"
"v_permlane16_swap_b32 v24, v26\n"
"v_permlane16_swap_b32 v25, v27\n"
"v_lshlrev_b32 v250, 16, v88\n"
"v_and_b32 v88, s73, v88\n"
"v_add_f32 v32, v32, v250\n"
"v_add_f32 v33, v33, v88\n"
"v_lshlrev_b32 v251, 16, v89\n"
"v_and_b32 v89, s73, v89\n"
"v_add_f32 v34, v34, v251\n"
"v_add_f32 v35, v35, v89\n"
"v_lshlrev_b32 v246, 16, v90\n"
"v_and_b32 v90, s73, v90\n"
"v_add_f32 v36, v36, v246\n"
"v_add_f32 v37, v37, v90\n"
"v_lshlrev_b32 v247, 16, v91\n"
"v_and_b32 v91, s73, v91\n"
"v_add_f32 v38, v38, v247\n"
"v_add_f32 v39, v39, v91\n"
"v_cvt_pk_bf16_f32 v32, v32, v33\n"
"v_cvt_pk_bf16_f32 v33, v34, v35\n"
"v_cvt_pk_bf16_f32 v34, v36, v37\n"
"v_cvt_pk_bf16_f32 v35, v38, v39\n"
"s_waitcnt vmcnt(6)\n"
"s_mov_b32 s40, 40960\n"
"buffer_store_dwordx4 v[24:27], v239, s[36:39], s40 offen\n"
"v_permlane16_swap_b32 v32, v34\n"
"v_permlane16_swap_b32 v33, v35\n"
"v_lshlrev_b32 v248, 16, v92\n"
"v_and_b32 v92, s73, v92\n"
"v_add_f32 v40, v40, v248\n"
"v_add_f32 v41, v41, v92\n"
"v_lshlrev_b32 v249, 16, v93\n"
"v_and_b32 v93, s73, v93\n"
"v_add_f32 v42, v42, v249\n"
"v_add_f32 v43, v43, v93\n"
"v_lshlrev_b32 v250, 16, v94\n"
"v_and_b32 v94, s73, v94\n"
"v_add_f32 v44, v44, v250\n"
"v_add_f32 v45, v45, v94\n"
"v_lshlrev_b32 v251, 16, v95\n"
"v_and_b32 v95, s73, v95\n"
"v_add_f32 v46, v46, v251\n"
"v_add_f32 v47, v47, v95\n"
"v_cvt_pk_bf16_f32 v40, v40, v41\n"
"v_cvt_pk_bf16_f32 v41, v42, v43\n"
"v_cvt_pk_bf16_f32 v42, v44, v45\n"
"v_cvt_pk_bf16_f32 v43, v46, v47\n"
"s_waitcnt vmcnt(6)\n"
"s_mov_b32 s40, 81920\n"
"buffer_store_dwordx4 v[32:35], v238, s[36:39], s40 offen\n"
"v_permlane16_swap_b32 v40, v42\n"
"v_permlane16_swap_b32 v41, v43\n"
"v_lshlrev_b32 v246, 16, v196\n"
"v_and_b32 v196, s73, v196\n"
"v_add_f32 v48, v48, v246\n"
"v_add_f32 v49, v49, v196\n"
"v_lshlrev_b32 v247, 16, v197\n"
"v_and_b32 v197, s73, v197\n"
"v_add_f32 v50, v50, v247\n"
"v_add_f32 v51, v51, v197\n"
"v_lshlrev_b32 v248, 16, v198\n"
"v_and_b32 v198, s73, v198\n"
"v_add_f32 v52, v52, v248\n"
"v_add_f32 v53, v53, v198\n"
"v_lshlrev_b32 v249, 16, v199\n"
"v_and_b32 v199, s73, v199\n"
"v_add_f32 v54, v54, v249\n"
"v_add_f32 v55, v55, v199\n"
"v_cvt_pk_bf16_f32 v48, v48, v49\n"
"v_cvt_pk_bf16_f32 v49, v50, v51\n"
"v_cvt_pk_bf16_f32 v50, v52, v53\n"
"v_cvt_pk_bf16_f32 v51, v54, v55\n"
"s_waitcnt vmcnt(6)\n"
"s_mov_b32 s40, 81920\n"
"buffer_store_dwordx4 v[40:43], v239, s[36:39], s40 offen\n"
"v_permlane16_swap_b32 v48, v50\n"
"v_permlane16_swap_b32 v49, v51\n"
"v_lshlrev_b32 v250, 16, v200\n"
"v_and_b32 v200, s73, v200\n"
"v_add_f32 v56, v56, v250\n"
"v_add_f32 v57, v57, v200\n"
"v_lshlrev_b32 v251, 16, v201\n"
"v_and_b32 v201, s73, v201\n"
"v_add_f32 v58, v58, v251\n"
"v_add_f32 v59, v59, v201\n"
"v_lshlrev_b32 v246, 16, v202\n"
"v_and_b32 v202, s73, v202\n"
"v_add_f32 v60, v60, v246\n"
"v_add_f32 v61, v61, v202\n"
"v_lshlrev_b32 v247, 16, v203\n"
"v_and_b32 v203, s73, v203\n"
"v_add_f32 v62, v62, v247\n"
"v_add_f32 v63, v63, v203\n"
"v_cvt_pk_bf16_f32 v56, v56, v57\n"
"v_cvt_pk_bf16_f32 v57, v58, v59\n"
"v_cvt_pk_bf16_f32 v58, v60, v61\n"
"v_cvt_pk_bf16_f32 v59, v62, v63\n"
"s_waitcnt vmcnt(6)\n"
"s_mov_b32 s40, 0\n"
"buffer_store_dwordx4 v[48:51], v252, s[36:39], s40 offen\n"
"v_permlane16_swap_b32 v56, v58\n"
"v_permlane16_swap_b32 v57, v59\n"
"v_lshlrev_b32 v248, 16, v204\n"
"v_and_b32 v204, s73, v204\n"
"v_add_f32 v64, v64, v248\n"
"v_add_f32 v65, v65, v204\n"
"v_lshlrev_b32 v249, 16, v205\n"
"v_and_b32 v205, s73, v205\n"
"v_add_f32 v66, v66, v249\n"
"v_add_f32 v67, v67, v205\n"
"v_lshlrev_b32 v250, 16, v206\n"
"v_and_b32 v206, s73, v206\n"
"v_add_f32 v68, v68, v250\n"
"v_add_f32 v69, v69, v206\n"
"v_lshlrev_b32 v251, 16, v207\n"
"v_and_b32 v207, s73, v207\n"
"v_add_f32 v70, v70, v251\n"
"v_add_f32 v71, v71, v207\n"
"v_cvt_pk_bf16_f32 v64, v64, v65\n"
"v_cvt_pk_bf16_f32 v65, v66, v67\n"
"v_cvt_pk_bf16_f32 v66, v68, v69\n"
"v_cvt_pk_bf16_f32 v67, v70, v71\n"
"s_mov_b32 s40, 0\n"
"buffer_store_dwordx4 v[56:59], v253, s[36:39], s40 offen\n"
"v_permlane16_swap_b32 v64, v66\n"
"v_permlane16_swap_b32 v65, v67\n"
"s_mov_b32 s40, 0\n"
"buffer_store_dwordx4 v[64:67], v254, s[36:39], s40 offen\n"
"s_branch LFIN_%=\n"
"LSW_%=:\n"
"s_add_u32 s16, s8, 1024\n"
"s_addc_u32 s17, s9, 0\n"
"s_mov_b32 s18, 654336\n"
"s_add_u32 s20, s8, 1152\n"
"s_addc_u32 s21, s9, 0\n"
"s_mov_b32 s22, 654208\n"
"s_mov_b32 s56, 0\n"
"s_mov_b32 s55, 0\n"
"s_mov_b32 s58, 0\n"
"s_mov_b32 s59, 163840\n"
"s_mov_b32 s52, 9216\n"
"s_mov_b32 s53, 18432\n"
"s_mov_b32 s54, 27648\n"
"v_lshrrev_b32 v206, 7, v242\n"
"v_mul_u32_u24 v206, 1280, v206\n"
"v_and_b32 v240, 0xff, v240\n"
"v_add_u32 v240, v206, v240\n"
"s_branch LSWK_%=\n"
"LGP_%=:\n"
"s_mov_b32 s80, 0x40000\n"
"LGPa_%=:\n"
"s_sleep 4\n"
"global_load_dword v244, v245, s[6:7] sc1\n"
"s_sub_u32 s80, s80, 1\n"
"s_cmp_eq_u32 s80, 0\n"
"s_waitcnt vmcnt(0)\n"
"s_cbranch_scc1 LGPK_%=\n"
"v_subrev_u32 v244, s21, v244\n"
"v_cmp_lt_u32 vcc, s69, v244\n"
"s_nop 4\n"
"s_cbranch_vccnz LGPa_%=\n"
"s_branch LGPK_%=\n"
"LGQ_%=:\n"
"s_mov_b32 s80, 0x40000\n"
"LGQa_%=:\n"
"s_sleep 4\n"
"global_load_dword v244, v245, s[6:7] sc1\n"
"s_sub_u32 s80, s80, 1\n"
"s_cmp_eq_u32 s80, 0\n"
"s_waitcnt vmcnt(0)\n"
"s_cbranch_scc1 LGQK_%=\n"
"v_subrev_u32 v244, s21, v244\n"
"v_cmp_lt_u32 vcc, s69, v244\n"
"s_nop 4\n"
"s_cbranch_vccnz LGQa_%=\n"
"s_branch LGQK_%=\n"
"LGR_%=:\n"
"s_mov_b32 s80, 0x40000\n"
"LGRa_%=:\n"
"s_sleep 4\n"
"global_load_dword v244, v245, s[6:7] sc1\n"
"s_sub_u32 s80, s80, 1\n"
"s_cmp_eq_u32 s80, 0\n"
"s_waitcnt vmcnt(0)\n"
"s_cbranch_scc1 LGRK_%=\n"
"v_subrev_u32 v244, s21, v244\n"
"v_cmp_lt_u32 vcc, s69, v244\n"
"s_nop 4\n"
"s_cbranch_vccnz LGRa_%=\n"
"s_branch LGRK_%=\n"
"LGS_%=:\n"
"v_mov_b32 v255, s93\n"
"s_mov_b64 exec, 1\n"
"global_store_dword v255, v241, s[6:7] sc1\n"
"s_mov_b64 exec, -1\n"
"s_branch LGSK_%=\n"
"LGT_%=:\n"
"v_mov_b32 v255, s93\n"
"s_mov_b64 exec, 1\n"
"global_store_dword v255, v241, s[6:7] sc1\n"
"s_mov_b64 exec, -1\n"
"s_branch LGTK_%=\n"
"LEP_%=:\n"
"s_add_u32 s92, s92, 1\n"
"v_mov_b32 v244, s92\n"
"v_mov_b32 v255, 0\n"
"s_mov_b64 exec, 1\n"
"global_store_dword v255, v244, s[6:7] offset:128 sc1\n"
"s_mov_b64 exec, -1\n"
"s_branch LEPK_%=\n"
"LPCE0_%=:\n"
"s_memrealtime s[80:81]\n"
"s_sub_u32 s82, s67, s66\n"
"s_cmp_gt_i32 s82, 0\n"
"s_cselect_b32 s83, s67, s66\n"
"s_waitcnt lgkmcnt(0)\n"
"s_sub_u32 s82, s83, s80\n"
"s_cmp_gt_i32 s82, 3\n"
"s_cbranch_scc0 LPEE0_%=\n"
"LPDE0_%=:\n"
"s_sleep 1\n"
"s_memrealtime s[80:81]\n"
"s_waitcnt lgkmcnt(0)\n"
"s_sub_u32 s82, s83, s80\n"
"s_cmp_gt_i32 s82, 3\n"
"s_cbranch_scc1 LPDE0_%=\n"
"LPEE0_%=:\n"
"s_add_u32 s66, s66, 132\n"
"s_add_u32 s67, s80, 128\n"
"s_branch LPKE0_%=\n"
"LPCE1_%=:\n"
"s_memrealtime s[80:81]\n"
"s_sub_u32 s82, s67, s66\n"
"s_cmp_gt_i32 s82, 0\n"
"s_cselect_b32 s83, s67, s66\n"
"s_waitcnt lgkmcnt(0)\n"
"s_sub_u32 s82, s83, s80\n"
"s_cmp_gt_i32 s82, 3\n"
"s_cbranch_scc0 LPEE1_%=\n"
"LPDE1_%=:\n"
"s_sleep 1\n"
"s_memrealtime s[80:81]\n"
"s_waitcnt lgkmcnt(0)\n"
"s_sub_u32 s82, s83, s80\n"
"s_cmp_gt_i32 s82, 3\n"
"s_cbranch_scc1 LPDE1_%=\n"
"LPEE1_%=:\n"
"s_add_u32 s66, s66, 132\n"
"s_add_u32 s67, s80, 128\n"
"s_branch LPKE1_%=\n"
"LPCT0_%=:\n"
"s_memrealtime s[80:81]\n"
"s_sub_u32 s82, s67, s66\n"
"s_cmp_gt_i32 s82, 0\n"
"s_cselect_b32 s83, s67, s66\n"
"s_waitcnt lgkmcnt(0)\n"
"s_sub_u32 s82, s83, s80\n"
"s_cmp_gt_i32 s82, 3\n"
"s_cbranch_scc0 LPET0_%=\n"
"LPDT0_%=:\n"
"s_sleep 1\n"
"s_memrealtime s[80:81]\n"
"s_waitcnt lgkmcnt(0)\n"
"s_sub_u32 s82, s83, s80\n"
"s_cmp_gt_i32 s82, 3\n"
"s_cbranch_scc1 LPDT0_%=\n"
"LPET0_%=:\n"
"s_add_u32 s66, s66, 132\n"
"s_add_u32 s67, s80, 128\n"
"s_branch LPKT0_%=\n"
"LPCT1_%=:\n"
"s_memrealtime s[80:81]\n"
"s_sub_u32 s82, s67, s66\n"
"s_cmp_gt_i32 s82, 0\n"
"s_cselect_b32 s83, s67, s66\n"
"s_waitcnt lgkmcnt(0)\n"
"s_sub_u32 s82, s83, s80\n"
"s_cmp_gt_i32 s82, 3\n"
"s_cbranch_scc0 LPET1_%=\n"
"LPDT1_%=:\n"
"s_sleep 1\n"
"s_memrealtime s[80:81]\n"
"s_waitcnt lgkmcnt(0)\n"
"s_sub_u32 s82, s83, s80\n"
"s_cmp_gt_i32 s82, 3\n"
"s_cbranch_scc1 LPDT1_%=\n"
"LPET1_%=:\n"
"s_add_u32 s66, s66, 132\n"
"s_add_u32 s67, s80, 128\n"
"s_branch LPKT1_%=\n"
"LPCL0_%=:\n"
"s_memrealtime s[80:81]\n"
"s_sub_u32 s82, s67, s66\n"
"s_cmp_gt_i32 s82, 0\n"
"s_cselect_b32 s83, s67, s66\n"
"s_waitcnt lgkmcnt(0)\n"
"s_sub_u32 s82, s83, s80\n"
"s_cmp_gt_i32 s82, 3\n"
"s_cbranch_scc0 LPEL0_%=\n"
"LPDL0_%=:\n"
"s_sleep 1\n"
"s_memrealtime s[80:81]\n"
"s_waitcnt lgkmcnt(0)\n"
"s_sub_u32 s82, s83, s80\n"
"s_cmp_gt_i32 s82, 3\n"
"s_cbranch_scc1 LPDL0_%=\n"
"LPEL0_%=:\n"
"s_add_u32 s66, s66, 132\n"
"s_add_u32 s67, s80, 128\n"
"s_branch LPKL0_%=\n"
"LPCL1_%=:\n"
"s_memrealtime s[80:81]\n"
"s_sub_u32 s82, s67, s66\n"
"s_cmp_gt_i32 s82, 0\n"
"s_cselect_b32 s83, s67, s66\n"
"s_waitcnt lgkmcnt(0)\n"
"s_sub_u32 s82, s83, s80\n"
"s_cmp_gt_i32 s82, 3\n"
"s_cbranch_scc0 LPEL1_%=\n"
"LPDL1_%=:\n"
"s_sleep 1\n"
"s_memrealtime s[80:81]\n"
"s_waitcnt lgkmcnt(0)\n"
"s_sub_u32 s82, s83, s80\n"
"s_cmp_gt_i32 s82, 3\n"
"s_cbranch_scc1 LPDL1_%=\n"
"LPEL1_%=:\n"
"s_add_u32 s66, s66, 132\n"
"s_add_u32 s67, s80, 128\n"
"s_branch LPKL1_%=\n"
"LPCL2_%=:\n"
"s_memrealtime s[80:81]\n"
"s_sub_u32 s82, s67, s66\n"
"s_cmp_gt_i32 s82, 0\n"
"s_cselect_b32 s83, s67, s66\n"
"s_waitcnt lgkmcnt(0)\n"
"s_sub_u32 s82, s83, s80\n"
"s_cmp_gt_i32 s82, 3\n"
"s_cbranch_scc0 LPEL2_%=\n"
"LPDL2_%=:\n"
"s_sleep 1\n"
"s_memrealtime s[80:81]\n"
"s_waitcnt lgkmcnt(0)\n"
"s_sub_u32 s82, s83, s80\n"
"s_cmp_gt_i32 s82, 3\n"
"s_cbranch_scc1 LPDL2_%=\n"
"LPEL2_%=:\n"
"s_add_u32 s66, s66, 132\n"
"s_add_u32 s67, s80, 128\n"
"s_branch LPKL2_%=\n"
"LPCL3_%=:\n"
"s_memrealtime s[80:81]\n"
"s_sub_u32 s82, s67, s66\n"
"s_cmp_gt_i32 s82, 0\n"
"s_cselect_b32 s83, s67, s66\n"
"s_waitcnt lgkmcnt(0)\n"
"s_sub_u32 s82, s83, s80\n"
"s_cmp_gt_i32 s82, 3\n"
"s_cbranch_scc0 LPEL3_%=\n"
"LPDL3_%=:\n"
"s_sleep 1\n"
"s_memrealtime s[80:81]\n"
"s_waitcnt lgkmcnt(0)\n"
"s_sub_u32 s82, s83, s80\n"
"s_cmp_gt_i32 s82, 3\n"
"s_cbranch_scc1 LPDL3_%=\n"
"LPEL3_%=:\n"
"s_add_u32 s66, s66, 132\n"
"s_add_u32 s67, s80, 128\n"
"s_branch LPKL3_%=\n"
"LEND_%=:\n"
"LSLOW_%=:\n"
"s_waitcnt vmcnt(0) lgkmcnt(0)\n"
"s_nop 9\n"
"v_bfe_u32 v148, v242, 4, 4\n"
"v_bfe_u32 v149, v242, 8, 2\n"
"v_lshlrev_b32 v144, 8, v148\n"
"v_lshl_add_u32 v144, v149, 4, v144\n"
"v_add_u32 v144, s43, v144\n"
"v_xor_b32 v145, 64, v144\n"
"v_lshlrev_b32 v146, 1, v148\n"
"v_lshl_add_u32 v146, v149, 10, v146\n"
"v_add_u32 v146, s43, v146\n"
"v_xor_b32 v147, 64, v146\n"
"v_mov_b32 v152, 0xff7fffff\n"
"v_mov_b32 v155, 0\n"
"v_mov_b32 v0, 0\n"
"v_mov_b32 v1, 0\n"
"v_mov_b32 v2, 0\n"
"v_mov_b32 v3, 0\n"
"v_mov_b32 v4, 0\n"
"v_mov_b32 v5, 0\n"
"v_mov_b32 v6, 0\n"
"v_mov_b32 v7, 0\n"
"v_mov_b32 v8, 0\n"
"v_mov_b32 v9, 0\n"
"v_mov_b32 v10, 0\n"
"v_mov_b32 v11, 0\n"
"v_mov_b32 v12, 0\n"
"v_mov_b32 v13, 0\n"
"v_mov_b32 v14, 0\n"
"v_mov_b32 v15, 0\n"
"v_mov_b32 v16, 0\n"
"v_mov_b32 v17, 0\n"
"v_mov_b32 v18, 0\n"
"v_mov_b32 v19, 0\n"
"v_mov_b32 v20, 0\n"
"v_mov_b32 v21, 0\n"
"v_mov_b32 v22, 0\n"
"v_mov_b32 v23, 0\n"
"v_mov_b32 v24, 0\n"
"v_mov_b32 v25, 0\n"
"v_mov_b32 v26, 0\n"
"v_mov_b32 v27, 0\n"
"v_mov_b32 v28, 0\n"
"v_mov_b32 v29, 0\n"
"v_mov_b32 v30, 0\n"
"v_mov_b32 v31, 0\n"
"v_mov_b32 v156, 0xff7fffff\n"
"v_mov_b32 v159, 0\n"
"v_mov_b32 v32, 0\n"
"v_mov_b32 v33, 0\n"
"v_mov_b32 v34, 0\n"
"v_mov_b32 v35, 0\n"
"v_mov_b32 v36, 0\n"
"v_mov_b32 v37, 0\n"
"v_mov_b32 v38, 0\n"
"v_mov_b32 v39, 0\n"
"v_mov_b32 v40, 0\n"
"v_mov_b32 v41, 0\n"
"v_mov_b32 v42, 0\n"
"v_mov_b32 v43, 0\n"
"v_mov_b32 v44, 0\n"
"v_mov_b32 v45, 0\n"
"v_mov_b32 v46, 0\n"
"v_mov_b32 v47, 0\n"
"v_mov_b32 v48, 0\n"
"v_mov_b32 v49, 0\n"
"v_mov_b32 v50, 0\n"
"v_mov_b32 v51, 0\n"
"v_mov_b32 v52, 0\n"
"v_mov_b32 v53, 0\n"
"v_mov_b32 v54, 0\n"
"v_mov_b32 v55, 0\n"
"v_mov_b32 v56, 0\n"
"v_mov_b32 v57, 0\n"
"v_mov_b32 v58, 0\n"
"v_mov_b32 v59, 0\n"
"v_mov_b32 v60, 0\n"
"v_mov_b32 v61, 0\n"
"v_mov_b32 v62, 0\n"
"v_mov_b32 v63, 0\n"
"v_mov_b32 v160, 0xff7fffff\n"
"v_mov_b32 v163, 0\n"
"v_mov_b32 v64, 0\n"
"v_mov_b32 v65, 0\n"
"v_mov_b32 v66, 0\n"
"v_mov_b32 v67, 0\n"
"v_mov_b32 v68, 0\n"
"v_mov_b32 v69, 0\n"
"v_mov_b32 v70, 0\n"
"v_mov_b32 v71, 0\n"
"v_mov_b32 v72, 0\n"
"v_mov_b32 v73, 0\n"
"v_mov_b32 v74, 0\n"
"v_mov_b32 v75, 0\n"
"v_mov_b32 v76, 0\n"
"v_mov_b32 v77, 0\n"
"v_mov_b32 v78, 0\n"
"v_mov_b32 v79, 0\n"
"v_mov_b32 v80, 0\n"
"v_mov_b32 v81, 0\n"
"v_mov_b32 v82, 0\n"
"v_mov_b32 v83, 0\n"
"v_mov_b32 v84, 0\n"
"v_mov_b32 v85, 0\n"
"v_mov_b32 v86, 0\n"
"v_mov_b32 v87, 0\n"
"v_mov_b32 v88, 0\n"
"v_mov_b32 v89, 0\n"
"v_mov_b32 v90, 0\n"
"v_mov_b32 v91, 0\n"
"v_mov_b32 v92, 0\n"
"v_mov_b32 v93, 0\n"
"v_mov_b32 v94, 0\n"
"v_mov_b32 v95, 0\n"
"v_mov_b32 v164, 0xff7fffff\n"
"v_mov_b32 v167, 0\n"
"v_accvgpr_write_b32 a208, 0\n"
"v_accvgpr_write_b32 a209, 0\n"
"v_accvgpr_write_b32 a210, 0\n"
"v_accvgpr_write_b32 a211, 0\n"
"v_accvgpr_write_b32 a212, 0\n"
"v_accvgpr_write_b32 a213, 0\n"
"v_accvgpr_write_b32 a214, 0\n"
"v_accvgpr_write_b32 a215, 0\n"
"v_accvgpr_write_b32 a216, 0\n"
"v_accvgpr_write_b32 a217, 0\n"
"v_accvgpr_write_b32 a218, 0\n"
"v_accvgpr_write_b32 a219, 0\n"
"v_accvgpr_write_b32 a220, 0\n"
"v_accvgpr_write_b32 a221, 0\n"
"v_accvgpr_write_b32 a222, 0\n"
"v_accvgpr_write_b32 a223, 0\n"
"v_accvgpr_write_b32 a224, 0\n"
"v_accvgpr_write_b32 a225, 0\n"
"v_accvgpr_write_b32 a226, 0\n"
"v_accvgpr_write_b32 a227, 0\n"
"v_accvgpr_write_b32 a228, 0\n"
"v_accvgpr_write_b32 a229, 0\n"
"v_accvgpr_write_b32 a230, 0\n"
"v_accvgpr_write_b32 a231, 0\n"
"v_accvgpr_write_b32 a232, 0\n"
"v_accvgpr_write_b32 a233, 0\n"
"v_accvgpr_write_b32 a234, 0\n"
"v_accvgpr_write_b32 a235, 0\n"
"v_accvgpr_write_b32 a236, 0\n"
"v_accvgpr_write_b32 a237, 0\n"
"v_accvgpr_write_b32 a238, 0\n"
"v_accvgpr_write_b32 a239, 0\n"
"s_lshl_b32 s80, s47, 16\n"
"s_sub_u32 s80, s80, 261248\n"
"s_add_u32 s81, s80, 4096\n"
"s_mov_b32 s82, 0\n"
"LSLOOP_%=:\n"
"s_lshl_b32 s83, s82, 6\n"
"s_and_b32 s83, s83, 64\n"
"buffer_load_dwordx4 v[128:131], v144, s[84:87], s80 offen offset:0\n"
"buffer_load_dwordx4 v[132:135], v145, s[84:87], s80 offen offset:0\n"
"buffer_load_dwordx4 v[136:139], v144, s[84:87], s80 offen offset:128\n"
"buffer_load_dwordx4 v[140:143], v145, s[84:87], s80 offen offset:128\n"
"s_waitcnt vmcnt(0)\n"
"v_mfma_f32_16x16x32_bf16 v[96:99], v[128:131], a[0:3], 0\n"
"v_mfma_f32_16x16x32_bf16 v[96:99], v[132:135], a[4:7], v[96:99]\n"
"v_mfma_f32_16x16x32_bf16 v[96:99], v[136:139], a[8:11], v[96:99]\n"
"v_mfma_f32_16x16x32_bf16 v[96:99], v[140:143], a[12:15], v[96:99]\n"
"v_mfma_f32_16x16x32_bf16 v[104:107], v[128:131], a[16:19], 0\n"
"v_mfma_f32_16x16x32_bf16 v[104:107], v[132:135], a[20:23], v[104:107]\n"
"v_mfma_f32_16x16x32_bf16 v[104:107], v[136:139], a[24:27], v[104:107]\n"
"v_mfma_f32_16x16x32_bf16 v[104:107], v[140:143], a[28:31], v[104:107]\n"
"v_mfma_f32_16x16x32_bf16 v[112:115], v[128:131], a[32:35], 0\n"
"v_mfma_f32_16x16x32_bf16 v[112:115], v[132:135], a[36:39], v[112:115]\n"
"v_mfma_f32_16x16x32_bf16 v[112:115], v[136:139], a[40:43], v[112:115]\n"
"v_mfma_f32_16x16x32_bf16 v[112:115], v[140:143], a[44:47], v[112:115]\n"
"v_mfma_f32_16x16x32_bf16 v[120:123], v[128:131], a[192:195], 0\n"
"v_mfma_f32_16x16x32_bf16 v[120:123], v[132:135], a[196:199], v[120:123]\n"
"v_mfma_f32_16x16x32_bf16 v[120:123], v[136:139], a[200:203], v[120:123]\n"
"v_mfma_f32_16x16x32_bf16 v[120:123], v[140:143], a[204:207], v[120:123]\n"
"s_nop 7\n"
"buffer_load_dwordx4 v[128:131], v144, s[84:87], s81 offen offset:0\n"
"buffer_load_dwordx4 v[132:135], v145, s[84:87], s81 offen offset:0\n"
"buffer_load_dwordx4 v[136:139], v144, s[84:87], s81 offen offset:128\n"
"buffer_load_dwordx4 v[140:143], v145, s[84:87], s81 offen offset:128\n"
"s_waitcnt vmcnt(0)\n"
"v_mfma_f32_16x16x32_bf16 v[100:103], v[128:131], a[0:3], 0\n"
"v_mfma_f32_16x16x32_bf16 v[100:103], v[132:135], a[4:7], v[100:103]\n"
"v_mfma_f32_16x16x32_bf16 v[100:103], v[136:139], a[8:11], v[100:103]\n"
"v_mfma_f32_16x16x32_bf16 v[100:103], v[140:143], a[12:15], v[100:103]\n"
"v_mfma_f32_16x16x32_bf16 v[108:111], v[128:131], a[16:19], 0\n"
"v_mfma_f32_16x16x32_bf16 v[108:111], v[132:135], a[20:23], v[108:111]\n"
"v_mfma_f32_16x16x32_bf16 v[108:111], v[136:139], a[24:27], v[108:111]\n"
"v_mfma_f32_16x16x32_bf16 v[108:111], v[140:143], a[28:31], v[108:111]\n"
"v_mfma_f32_16x16x32_bf16 v[116:119], v[128:131], a[32:35], 0\n"
"v_mfma_f32_16x16x32_bf16 v[116:119], v[132:135], a[36:39], v[116:119]\n"
"v_mfma_f32_16x16x32_bf16 v[116:119], v[136:139], a[40:43], v[116:119]\n"
"v_mfma_f32_16x16x32_bf16 v[116:119], v[140:143], a[44:47], v[116:119]\n"
"v_mfma_f32_16x16x32_bf16 v[124:127], v[128:131], a[192:195], 0\n"
"v_mfma_f32_16x16x32_bf16 v[124:127], v[132:135], a[196:199], v[124:127]\n"
"v_mfma_f32_16x16x32_bf16 v[124:127], v[136:139], a[200:203], v[124:127]\n"
"v_mfma_f32_16x16x32_bf16 v[124:127], v[140:143], a[204:207], v[124:127]\n"
"s_nop 7\n"
"v_max3_f32 v153, v96, v97, v98\n"
"v_max3_f32 v153, v153, v99, v100\n"
"v_max3_f32 v153, v153, v101, v102\n"
"v_max3_f32 v153, v153, v103, v152\n"
"v_mov_b32 v154, v153\n"
"s_nop 1\n"
"v_permlane32_swap_b32 v153, v154\n"
"v_max_f32 v153, v153, v154\n"
"v_mov_b32 v154, v153\n"
"s_nop 1\n"
"v_permlane16_swap_b32 v153, v154\n"
"v_max_f32 v153, v153, v154\n"
"v_sub_f32 v154, v152, v153\n"
"v_exp_f32 v154, v154\n"
"v_mov_b32 v152, v153\n"
"v_sub_f32 v96, v96, v152\n"
"v_sub_f32 v97, v97, v152\n"
"v_sub_f32 v98, v98, v152\n"
"v_sub_f32 v99, v99, v152\n"
"v_sub_f32 v100, v100, v152\n"
"v_sub_f32 v101, v101, v152\n"
"v_sub_f32 v102, v102, v152\n"
"v_sub_f32 v103, v103, v152\n"
"v_exp_f32 v96, v96\n"
"v_exp_f32 v97, v97\n"
"v_exp_f32 v98, v98\n"
"v_exp_f32 v99, v99\n"
"v_exp_f32 v100, v100\n"
"v_exp_f32 v101, v101\n"
"v_exp_f32 v102, v102\n"
"v_exp_f32 v103, v103\n"
"v_mul_f32 v155, v155, v154\n"
"v_add_f32 v155, v155, v96\n"
"v_add_f32 v155, v155, v97\n"
"v_add_f32 v155, v155, v98\n"
"v_add_f32 v155, v155, v99\n"
"v_add_f32 v155, v155, v100\n"
"v_add_f32 v155, v155, v101\n"
"v_add_f32 v155, v155, v102\n"
"v_add_f32 v155, v155, v103\n"
"v_cvt_pk_bf16_f32 v96, v96, v97\n"
"v_cvt_pk_bf16_f32 v97, v98, v99\n"
"v_cvt_pk_bf16_f32 v98, v100, v101\n"
"v_cvt_pk_bf16_f32 v99, v102, v103\n"
"v_mov_b32_dpp v154, v154 row_ror:12 row_mask:0x2 bank_mask:0xf\n"
"s_nop 1\n"
"v_mov_b32_dpp v154, v154 row_ror:8 row_mask:0x4 bank_mask:0xf\n"
"s_nop 1\n"
"v_mov_b32_dpp v154, v154 row_ror:4 row_mask:0x8 bank_mask:0xf\n"
"s_nop 1\n"
"v_mul_f32_dpp v0, v154, v0 row_newbcast:0 row_mask:0xf bank_mask:0xf\n"
"v_mul_f32_dpp v1, v154, v1 row_newbcast:1 row_mask:0xf bank_mask:0xf\n"
"v_mul_f32_dpp v2, v154, v2 row_newbcast:2 row_mask:0xf bank_mask:0xf\n"
"v_mul_f32_dpp v3, v154, v3 row_newbcast:3 row_mask:0xf bank_mask:0xf\n"
"v_mul_f32_dpp v4, v154, v4 row_newbcast:0 row_mask:0xf bank_mask:0xf\n"
"v_mul_f32_dpp v5, v154, v5 row_newbcast:1 row_mask:0xf bank_mask:0xf\n"
"v_mul_f32_dpp v6, v154, v6 row_newbcast:2 row_mask:0xf bank_mask:0xf\n"
"v_mul_f32_dpp v7, v154, v7 row_newbcast:3 row_mask:0xf bank_mask:0xf\n"
"v_mul_f32_dpp v8, v154, v8 row_newbcast:0 row_mask:0xf bank_mask:0xf\n"
"v_mul_f32_dpp v9, v154, v9 row_newbcast:1 row_mask:0xf bank_mask:0xf\n"
"v_mul_f32_dpp v10, v154, v10 row_newbcast:2 row_mask:0xf bank_mask:0xf\n"
"v_mul_f32_dpp v11, v154, v11 row_newbcast:3 row_mask:0xf bank_mask:0xf\n"
"v_mul_f32_dpp v12, v154, v12 row_newbcast:0 row_mask:0xf bank_mask:0xf\n"
"v_mul_f32_dpp v13, v154, v13 row_newbcast:1 row_mask:0xf bank_mask:0xf\n"
"v_mul_f32_dpp v14, v154, v14 row_newbcast:2 row_mask:0xf bank_mask:0xf\n"
"v_mul_f32_dpp v15, v154, v15 row_newbcast:3 row_mask:0xf bank_mask:0xf\n"
"v_mul_f32_dpp v16, v154, v16 row_newbcast:0 row_mask:0xf bank_mask:0xf\n"
"v_mul_f32_dpp v17, v154, v17 row_newbcast:1 row_mask:0xf bank_mask:0xf\n"
"v_mul_f32_dpp v18, v154, v18 row_newbcast:2 row_mask:0xf bank_mask:0xf\n"
"v_mul_f32_dpp v19, v154, v19 row_newbcast:3 row_mask:0xf bank_mask:0xf\n"
"v_mul_f32_dpp v20, v154, v20 row_newbcast:0 row_mask:0xf bank_mask:0xf\n"
"v_mul_f32_dpp v21, v154, v21 row_newbcast:1 row_mask:0xf bank_mask:0xf\n"
"v_mul_f32_dpp v22, v154, v22 row_newbcast:2 row_mask:0xf bank_mask:0xf\n"
"v_mul_f32_dpp v23, v154, v23 row_newbcast:3 row_mask:0xf bank_mask:0xf\n"
"v_mul_f32_dpp v24, v154, v24 row_newbcast:0 row_mask:0xf bank_mask:0xf\n"
"v_mul_f32_dpp v25, v154, v25 row_newbcast:1 row_mask:0xf bank_mask:0xf\n"
"v_mul_f32_dpp v26, v154, v26 row_newbcast:2 row_mask:0xf bank_mask:0xf\n"
"v_mul_f32_dpp v27, v154, v27 row_newbcast:3 row_mask:0xf bank_mask:0xf\n"
"v_mul_f32_dpp v28, v154, v28 row_newbcast:0 row_mask:0xf bank_mask:0xf\n"
"v_mul_f32_dpp v29, v154, v29 row_newbcast:1 row_mask:0xf bank_mask:0xf\n"
"v_mul_f32_dpp v30, v154, v30 row_newbcast:2 row_mask:0xf bank_mask:0xf\n"
"v_mul_f32_dpp v31, v154, v31 row_newbcast:3 row_mask:0xf bank_mask:0xf\n"
"v_max3_f32 v157, v104, v105, v106\n"
"v_max3_f32 v157, v157, v107, v108\n"
"v_max3_f32 v157, v157, v109, v110\n"
"v_max3_f32 v157, v157, v111, v156\n"
"v_mov_b32 v158, v157\n"
"s_nop 1\n"
"v_permlane32_swap_b32 v157, v158\n"
"v_max_f32 v157, v157, v158\n"
"v_mov_b32 v158, v157\n"
"s_nop 1\n"
"v_permlane16_swap_b32 v157, v158\n"
"v_max_f32 v157, v157, v158\n"
"v_sub_f32 v158, v156, v157\n"
"v_exp_f32 v158, v158\n"
"v_mov_b32 v156, v157\n"
"v_sub_f32 v104, v104, v156\n"
"v_sub_f32 v105, v105, v156\n"
"v_sub_f32 v106, v106, v156\n"
"v_sub_f32 v107, v107, v156\n"
"v_sub_f32 v108, v108, v156\n"
"v_sub_f32 v109, v109, v156\n"
"v_sub_f32 v110, v110, v156\n"
"v_sub_f32 v111, v111, v156\n"
"v_exp_f32 v104, v104\n"
"v_exp_f32 v105, v105\n"
"v_exp_f32 v106, v106\n"
"v_exp_f32 v107, v107\n"
"v_exp_f32 v108, v108\n"
"v_exp_f32 v109, v109\n"
"v_exp_f32 v110, v110\n"
"v_exp_f32 v111, v111\n"
"v_mul_f32 v159, v159, v158\n"
"v_add_f32 v159, v159, v104\n"
"v_add_f32 v159, v159, v105\n"
"v_add_f32 v159, v159, v106\n"
"v_add_f32 v159, v159, v107\n"
"v_add_f32 v159, v159, v108\n"
"v_add_f32 v159, v159, v109\n"
"v_add_f32 v159, v159, v110\n"
"v_add_f32 v159, v159, v111\n"
"v_cvt_pk_bf16_f32 v104, v104, v105\n"
"v_cvt_pk_bf16_f32 v105, v106, v107\n"
"v_cvt_pk_bf16_f32 v106, v108, v109\n"
"v_cvt_pk_bf16_f32 v107, v110, v111\n"
"v_mov_b32_dpp v158, v158 row_ror:12 row_mask:0x2 bank_mask:0xf\n"
"s_nop 1\n"
"v_mov_b32_dpp v158, v158 row_ror:8 row_mask:0x4 bank_mask:0xf\n"
"s_nop 1\n"
"v_mov_b32_dpp v158, v158 row_ror:4 row_mask:0x8 bank_mask:0xf\n"
"s_nop 1\n"
"v_mul_f32_dpp v32, v158, v32 row_newbcast:0 row_mask:0xf bank_mask:0xf\n"
"v_mul_f32_dpp v33, v158, v33 row_newbcast:1 row_mask:0xf bank_mask:0xf\n"
"v_mul_f32_dpp v34, v158, v34 row_newbcast:2 row_mask:0xf bank_mask:0xf\n"
"v_mul_f32_dpp v35, v158, v35 row_newbcast:3 row_mask:0xf bank_mask:0xf\n"
"v_mul_f32_dpp v36, v158, v36 row_newbcast:0 row_mask:0xf bank_mask:0xf\n"
"v_mul_f32_dpp v37, v158, v37 row_newbcast:1 row_mask:0xf bank_mask:0xf\n"
"v_mul_f32_dpp v38, v158, v38 row_newbcast:2 row_mask:0xf bank_mask:0xf\n"
"v_mul_f32_dpp v39, v158, v39 row_newbcast:3 row_mask:0xf bank_mask:0xf\n"
"v_mul_f32_dpp v40, v158, v40 row_newbcast:0 row_mask:0xf bank_mask:0xf\n"
"v_mul_f32_dpp v41, v158, v41 row_newbcast:1 row_mask:0xf bank_mask:0xf\n"
"v_mul_f32_dpp v42, v158, v42 row_newbcast:2 row_mask:0xf bank_mask:0xf\n"
"v_mul_f32_dpp v43, v158, v43 row_newbcast:3 row_mask:0xf bank_mask:0xf\n"
"v_mul_f32_dpp v44, v158, v44 row_newbcast:0 row_mask:0xf bank_mask:0xf\n"
"v_mul_f32_dpp v45, v158, v45 row_newbcast:1 row_mask:0xf bank_mask:0xf\n"
"v_mul_f32_dpp v46, v158, v46 row_newbcast:2 row_mask:0xf bank_mask:0xf\n"
"v_mul_f32_dpp v47, v158, v47 row_newbcast:3 row_mask:0xf bank_mask:0xf\n"
"v_mul_f32_dpp v48, v158, v48 row_newbcast:0 row_mask:0xf bank_mask:0xf\n"
"v_mul_f32_dpp v49, v158, v49 row_newbcast:1 row_mask:0xf bank_mask:0xf\n"
"v_mul_f32_dpp v50, v158, v50 row_newbcast:2 row_mask:0xf bank_mask:0xf\n"
"v_mul_f32_dpp v51, v158, v51 row_newbcast:3 row_mask:0xf bank_mask:0xf\n"
"v_mul_f32_dpp v52, v158, v52 row_newbcast:0 row_mask:0xf bank_mask:0xf\n"
"v_mul_f32_dpp v53, v158, v53 row_newbcast:1 row_mask:0xf bank_mask:0xf\n"
"v_mul_f32_dpp v54, v158, v54 row_newbcast:2 row_mask:0xf bank_mask:0xf\n"
"v_mul_f32_dpp v55, v158, v55 row_newbcast:3 row_mask:0xf bank_mask:0xf\n"
"v_mul_f32_dpp v56, v158, v56 row_newbcast:0 row_mask:0xf bank_mask:0xf\n"
"v_mul_f32_dpp v57, v158, v57 row_newbcast:1 row_mask:0xf bank_mask:0xf\n"
"v_mul_f32_dpp v58, v158, v58 row_newbcast:2 row_mask:0xf bank_mask:0xf\n"
"v_mul_f32_dpp v59, v158, v59 row_newbcast:3 row_mask:0xf bank_mask:0xf\n"
"v_mul_f32_dpp v60, v158, v60 row_newbcast:0 row_mask:0xf bank_mask:0xf\n"
"v_mul_f32_dpp v61, v158, v61 row_newbcast:1 row_mask:0xf bank_mask:0xf\n"
"v_mul_f32_dpp v62, v158, v62 row_newbcast:2 row_mask:0xf bank_mask:0xf\n"
"v_mul_f32_dpp v63, v158, v63 row_newbcast:3 row_mask:0xf bank_mask:0xf\n"
"v_max3_f32 v161, v112, v113, v114\n"
"v_max3_f32 v161, v161, v115, v116\n"
"v_max3_f32 v161, v161, v117, v118\n"
"v_max3_f32 v161, v161, v119, v160\n"
"v_mov_b32 v162, v161\n"
"s_nop 1\n"
"v_permlane32_swap_b32 v161, v162\n"
"v_max_f32 v161, v161, v162\n"
"v_mov_b32 v162, v161\n"
"s_nop 1\n"
"v_permlane16_swap_b32 v161, v162\n"
"v_max_f32 v161, v161, v162\n"
"v_sub_f32 v162, v160, v161\n"
"v_exp_f32 v162, v162\n"
"v_mov_b32 v160, v161\n"
"v_sub_f32 v112, v112, v160\n"
"v_sub_f32 v113, v113, v160\n"
"v_sub_f32 v114, v114, v160\n"
"v_sub_f32 v115, v115, v160\n"
"v_sub_f32 v116, v116, v160\n"
"v_sub_f32 v117, v117, v160\n"
"v_sub_f32 v118, v118, v160\n"
"v_sub_f32 v119, v119, v160\n"
"v_exp_f32 v112, v112\n"
"v_exp_f32 v113, v113\n"
"v_exp_f32 v114, v114\n"
"v_exp_f32 v115, v115\n"
"v_exp_f32 v116, v116\n"
"v_exp_f32 v117, v117\n"
"v_exp_f32 v118, v118\n"
"v_exp_f32 v119, v119\n"
"v_mul_f32 v163, v163, v162\n"
"v_add_f32 v163, v163, v112\n"
"v_add_f32 v163, v163, v113\n"
"v_add_f32 v163, v163, v114\n"
"v_add_f32 v163, v163, v115\n"
"v_add_f32 v163, v163, v116\n"
"v_add_f32 v163, v163, v117\n"
"v_add_f32 v163, v163, v118\n"
"v_add_f32 v163, v163, v119\n"
"v_cvt_pk_bf16_f32 v112, v112, v113\n"
"v_cvt_pk_bf16_f32 v113, v114, v115\n"
"v_cvt_pk_bf16_f32 v114, v116, v117\n"
"v_cvt_pk_bf16_f32 v115, v118, v119\n"
"v_mov_b32_dpp v162, v162 row_ror:12 row_mask:0x2 bank_mask:0xf\n"
"s_nop 1\n"
"v_mov_b32_dpp v162, v162 row_ror:8 row_mask:0x4 bank_mask:0xf\n"
"s_nop 1\n"
"v_mov_b32_dpp v162, v162 row_ror:4 row_mask:0x8 bank_mask:0xf\n"
"s_nop 1\n"
"v_mul_f32_dpp v64, v162, v64 row_newbcast:0 row_mask:0xf bank_mask:0xf\n"
"v_mul_f32_dpp v65, v162, v65 row_newbcast:1 row_mask:0xf bank_mask:0xf\n"
"v_mul_f32_dpp v66, v162, v66 row_newbcast:2 row_mask:0xf bank_mask:0xf\n"
"v_mul_f32_dpp v67, v162, v67 row_newbcast:3 row_mask:0xf bank_mask:0xf\n"
"v_mul_f32_dpp v68, v162, v68 row_newbcast:0 row_mask:0xf bank_mask:0xf\n"
"v_mul_f32_dpp v69, v162, v69 row_newbcast:1 row_mask:0xf bank_mask:0xf\n"
"v_mul_f32_dpp v70, v162, v70 row_newbcast:2 row_mask:0xf bank_mask:0xf\n"
"v_mul_f32_dpp v71, v162, v71 row_newbcast:3 row_mask:0xf bank_mask:0xf\n"
"v_mul_f32_dpp v72, v162, v72 row_newbcast:0 row_mask:0xf bank_mask:0xf\n"
"v_mul_f32_dpp v73, v162, v73 row_newbcast:1 row_mask:0xf bank_mask:0xf\n"
"v_mul_f32_dpp v74, v162, v74 row_newbcast:2 row_mask:0xf bank_mask:0xf\n"
"v_mul_f32_dpp v75, v162, v75 row_newbcast:3 row_mask:0xf bank_mask:0xf\n"
"v_mul_f32_dpp v76, v162, v76 row_newbcast:0 row_mask:0xf bank_mask:0xf\n"
"v_mul_f32_dpp v77, v162, v77 row_newbcast:1 row_mask:0xf bank_mask:0xf\n"
"v_mul_f32_dpp v78, v162, v78 row_newbcast:2 row_mask:0xf bank_mask:0xf\n"
"v_mul_f32_dpp v79, v162, v79 row_newbcast:3 row_mask:0xf bank_mask:0xf\n"
"v_mul_f32_dpp v80, v162, v80 row_newbcast:0 row_mask:0xf bank_mask:0xf\n"
"v_mul_f32_dpp v81, v162, v81 row_newbcast:1 row_mask:0xf bank_mask:0xf\n"
"v_mul_f32_dpp v82, v162, v82 row_newbcast:2 row_mask:0xf bank_mask:0xf\n"
"v_mul_f32_dpp v83, v162, v83 row_newbcast:3 row_mask:0xf bank_mask:0xf\n"
"v_mul_f32_dpp v84, v162, v84 row_newbcast:0 row_mask:0xf bank_mask:0xf\n"
"v_mul_f32_dpp v85, v162, v85 row_newbcast:1 row_mask:0xf bank_mask:0xf\n"
"v_mul_f32_dpp v86, v162, v86 row_newbcast:2 row_mask:0xf bank_mask:0xf\n"
"v_mul_f32_dpp v87, v162, v87 row_newbcast:3 row_mask:0xf bank_mask:0xf\n"
"v_mul_f32_dpp v88, v162, v88 row_newbcast:0 row_mask:0xf bank_mask:0xf\n"
"v_mul_f32_dpp v89, v162, v89 row_newbcast:1 row_mask:0xf bank_mask:0xf\n"
"v_mul_f32_dpp v90, v162, v90 row_newbcast:2 row_mask:0xf bank_mask:0xf\n"
"v_mul_f32_dpp v91, v162, v91 row_newbcast:3 row_mask:0xf bank_mask:0xf\n"
"v_mul_f32_dpp v92, v162, v92 row_newbcast:0 row_mask:0xf bank_mask:0xf\n"
"v_mul_f32_dpp v93, v162, v93 row_newbcast:1 row_mask:0xf bank_mask:0xf\n"
"v_mul_f32_dpp v94, v162, v94 row_newbcast:2 row_mask:0xf bank_mask:0xf\n"
"v_mul_f32_dpp v95, v162, v95 row_newbcast:3 row_mask:0xf bank_mask:0xf\n"
"s_cmp_lg_u32 s83, s43\n"
"s_cbranch_scc1 LSK3A_%=\n"
"v_max3_f32 v165, v120, v121, v122\n"
"v_max3_f32 v165, v165, v123, v124\n"
"v_max3_f32 v165, v165, v125, v126\n"
"v_max3_f32 v165, v165, v127, v164\n"
"v_mov_b32 v166, v165\n"
"s_nop 1\n"
"v_permlane32_swap_b32 v165, v166\n"
"v_max_f32 v165, v165, v166\n"
"v_mov_b32 v166, v165\n"
"s_nop 1\n"
"v_permlane16_swap_b32 v165, v166\n"
"v_max_f32 v165, v165, v166\n"
"v_sub_f32 v166, v164, v165\n"
"v_exp_f32 v166, v166\n"
"v_mov_b32 v164, v165\n"
"v_sub_f32 v120, v120, v164\n"
"v_sub_f32 v121, v121, v164\n"
"v_sub_f32 v122, v122, v164\n"
"v_sub_f32 v123, v123, v164\n"
"v_sub_f32 v124, v124, v164\n"
"v_sub_f32 v125, v125, v164\n"
"v_sub_f32 v126, v126, v164\n"
"v_sub_f32 v127, v127, v164\n"
"v_exp_f32 v120, v120\n"
"v_exp_f32 v121, v121\n"
"v_exp_f32 v122, v122\n"
"v_exp_f32 v123, v123\n"
"v_exp_f32 v124, v124\n"
"v_exp_f32 v125, v125\n"
"v_exp_f32 v126, v126\n"
"v_exp_f32 v127, v127\n"
"v_mul_f32 v167, v167, v166\n"
"v_add_f32 v167, v167, v120\n"
"v_add_f32 v167, v167, v121\n"
"v_add_f32 v167, v167, v122\n"
"v_add_f32 v167, v167, v123\n"
"v_add_f32 v167, v167, v124\n"
"v_add_f32 v167, v167, v125\n"
"v_add_f32 v167, v167, v126\n"
"v_add_f32 v167, v167, v127\n"
"v_cvt_pk_bf16_f32 v120, v120, v121\n"
"v_cvt_pk_bf16_f32 v121, v122, v123\n"
"v_cvt_pk_bf16_f32 v122, v124, v125\n"
"v_cvt_pk_bf16_f32 v123, v126, v127\n"
"v_mov_b32_dpp v166, v166 row_ror:12 row_mask:0x2 bank_mask:0xf\n"
"s_nop 1\n"
"v_mov_b32_dpp v166, v166 row_ror:8 row_mask:0x4 bank_mask:0xf\n"
"s_nop 1\n"
"v_mov_b32_dpp v166, v166 row_ror:4 row_mask:0x8 bank_mask:0xf\n"
"v_accvgpr_read_b32 v124, a208\n"
"s_nop 1\n"
"v_mul_f32_dpp v124, v166, v124 row_newbcast:0 row_mask:0xf bank_mask:0xf\n"
"v_accvgpr_write_b32 a208, v124\n"
"v_accvgpr_read_b32 v125, a209\n"
"s_nop 1\n"
"v_mul_f32_dpp v125, v166, v125 row_newbcast:1 row_mask:0xf bank_mask:0xf\n"
"v_accvgpr_write_b32 a209, v125\n"
"v_accvgpr_read_b32 v126, a210\n"
"s_nop 1\n"
"v_mul_f32_dpp v126, v166, v126 row_newbcast:2 row_mask:0xf bank_mask:0xf\n"
"v_accvgpr_write_b32 a210, v126\n"
"v_accvgpr_read_b32 v127, a211\n"
"s_nop 1\n"
"v_mul_f32_dpp v127, v166, v127 row_newbcast:3 row_mask:0xf bank_mask:0xf\n"
"v_accvgpr_write_b32 a211, v127\n"
"v_accvgpr_read_b32 v124, a212\n"
"s_nop 1\n"
"v_mul_f32_dpp v124, v166, v124 row_newbcast:0 row_mask:0xf bank_mask:0xf\n"
"v_accvgpr_write_b32 a212, v124\n"
"v_accvgpr_read_b32 v125, a213\n"
"s_nop 1\n"
"v_mul_f32_dpp v125, v166, v125 row_newbcast:1 row_mask:0xf bank_mask:0xf\n"
"v_accvgpr_write_b32 a213, v125\n"
"v_accvgpr_read_b32 v126, a214\n"
"s_nop 1\n"
"v_mul_f32_dpp v126, v166, v126 row_newbcast:2 row_mask:0xf bank_mask:0xf\n"
"v_accvgpr_write_b32 a214, v126\n"
"v_accvgpr_read_b32 v127, a215\n"
"s_nop 1\n"
"v_mul_f32_dpp v127, v166, v127 row_newbcast:3 row_mask:0xf bank_mask:0xf\n"
"v_accvgpr_write_b32 a215, v127\n"
"v_accvgpr_read_b32 v124, a216\n"
"s_nop 1\n"
"v_mul_f32_dpp v124, v166, v124 row_newbcast:0 row_mask:0xf bank_mask:0xf\n"
"v_accvgpr_write_b32 a216, v124\n"
"v_accvgpr_read_b32 v125, a217\n"
"s_nop 1\n"
"v_mul_f32_dpp v125, v166, v125 row_newbcast:1 row_mask:0xf bank_mask:0xf\n"
"v_accvgpr_write_b32 a217, v125\n"
"v_accvgpr_read_b32 v126, a218\n"
"s_nop 1\n"
"v_mul_f32_dpp v126, v166, v126 row_newbcast:2 row_mask:0xf bank_mask:0xf\n"
"v_accvgpr_write_b32 a218, v126\n"
"v_accvgpr_read_b32 v127, a219\n"
"s_nop 1\n"
"v_mul_f32_dpp v127, v166, v127 row_newbcast:3 row_mask:0xf bank_mask:0xf\n"
"v_accvgpr_write_b32 a219, v127\n"
"v_accvgpr_read_b32 v124, a220\n"
"s_nop 1\n"
"v_mul_f32_dpp v124, v166, v124 row_newbcast:0 row_mask:0xf bank_mask:0xf\n"
"v_accvgpr_write_b32 a220, v124\n"
"v_accvgpr_read_b32 v125, a221\n"
"s_nop 1\n"
"v_mul_f32_dpp v125, v166, v125 row_newbcast:1 row_mask:0xf bank_mask:0xf\n"
"v_accvgpr_write_b32 a221, v125\n"
"v_accvgpr_read_b32 v126, a222\n"
"s_nop 1\n"
"v_mul_f32_dpp v126, v166, v126 row_newbcast:2 row_mask:0xf bank_mask:0xf\n"
"v_accvgpr_write_b32 a222, v126\n"
"v_accvgpr_read_b32 v127, a223\n"
"s_nop 1\n"
"v_mul_f32_dpp v127, v166, v127 row_newbcast:3 row_mask:0xf bank_mask:0xf\n"
"v_accvgpr_write_b32 a223, v127\n"
"v_accvgpr_read_b32 v124, a224\n"
"s_nop 1\n"
"v_mul_f32_dpp v124, v166, v124 row_newbcast:0 row_mask:0xf bank_mask:0xf\n"
"v_accvgpr_write_b32 a224, v124\n"
"v_accvgpr_read_b32 v125, a225\n"
"s_nop 1\n"
"v_mul_f32_dpp v125, v166, v125 row_newbcast:1 row_mask:0xf bank_mask:0xf\n"
"v_accvgpr_write_b32 a225, v125\n"
"v_accvgpr_read_b32 v126, a226\n"
"s_nop 1\n"
"v_mul_f32_dpp v126, v166, v126 row_newbcast:2 row_mask:0xf bank_mask:0xf\n"
"v_accvgpr_write_b32 a226, v126\n"
"v_accvgpr_read_b32 v127, a227\n"
"s_nop 1\n"
"v_mul_f32_dpp v127, v166, v127 row_newbcast:3 row_mask:0xf bank_mask:0xf\n"
"v_accvgpr_write_b32 a227, v127\n"
"v_accvgpr_read_b32 v124, a228\n"
"s_nop 1\n"
"v_mul_f32_dpp v124, v166, v124 row_newbcast:0 row_mask:0xf bank_mask:0xf\n"
"v_accvgpr_write_b32 a228, v124\n"
"v_accvgpr_read_b32 v125, a229\n"
"s_nop 1\n"
"v_mul_f32_dpp v125, v166, v125 row_newbcast:1 row_mask:0xf bank_mask:0xf\n"
"v_accvgpr_write_b32 a229, v125\n"
"v_accvgpr_read_b32 v126, a230\n"
"s_nop 1\n"
"v_mul_f32_dpp v126, v166, v126 row_newbcast:2 row_mask:0xf bank_mask:0xf\n"
"v_accvgpr_write_b32 a230, v126\n"
"v_accvgpr_read_b32 v127, a231\n"
"s_nop 1\n"
"v_mul_f32_dpp v127, v166, v127 row_newbcast:3 row_mask:0xf bank_mask:0xf\n"
"v_accvgpr_write_b32 a231, v127\n"
"v_accvgpr_read_b32 v124, a232\n"
"s_nop 1\n"
"v_mul_f32_dpp v124, v166, v124 row_newbcast:0 row_mask:0xf bank_mask:0xf\n"
"v_accvgpr_write_b32 a232, v124\n"
"v_accvgpr_read_b32 v125, a233\n"
"s_nop 1\n"
"v_mul_f32_dpp v125, v166, v125 row_newbcast:1 row_mask:0xf bank_mask:0xf\n"
"v_accvgpr_write_b32 a233, v125\n"
"v_accvgpr_read_b32 v126, a234\n"
"s_nop 1\n"
"v_mul_f32_dpp v126, v166, v126 row_newbcast:2 row_mask:0xf bank_mask:0xf\n"
"v_accvgpr_write_b32 a234, v126\n"
"v_accvgpr_read_b32 v127, a235\n"
"s_nop 1\n"
"v_mul_f32_dpp v127, v166, v127 row_newbcast:3 row_mask:0xf bank_mask:0xf\n"
"v_accvgpr_write_b32 a235, v127\n"
"v_accvgpr_read_b32 v124, a236\n"
"s_nop 1\n"
"v_mul_f32_dpp v124, v166, v124 row_newbcast:0 row_mask:0xf bank_mask:0xf\n"
"v_accvgpr_write_b32 a236, v124\n"
"v_accvgpr_read_b32 v125, a237\n"
"s_nop 1\n"
"v_mul_f32_dpp v125, v166, v125 row_newbcast:1 row_mask:0xf bank_mask:0xf\n"
"v_accvgpr_write_b32 a237, v125\n"
"v_accvgpr_read_b32 v126, a238\n"
"s_nop 1\n"
"v_mul_f32_dpp v126, v166, v126 row_newbcast:2 row_mask:0xf bank_mask:0xf\n"
"v_accvgpr_write_b32 a238, v126\n"
"v_accvgpr_read_b32 v127, a239\n"
"s_nop 1\n"
"v_mul_f32_dpp v127, v166, v127 row_newbcast:3 row_mask:0xf bank_mask:0xf\n"
"v_accvgpr_write_b32 a239, v127\n"
"LSK3A_%=:\n"
"s_nop 3\n"
"buffer_load_ushort v128, v146, s[88:91], s80 offen offset:0\n"
"buffer_load_ushort v129, v146, s[88:91], s80 offen offset:256\n"
"buffer_load_ushort v130, v146, s[88:91], s80 offen offset:512\n"
"buffer_load_ushort v131, v146, s[88:91], s80 offen offset:768\n"
"buffer_load_ushort v132, v146, s[88:91], s81 offen offset:0\n"
"buffer_load_ushort v133, v146, s[88:91], s81 offen offset:256\n"
"buffer_load_ushort v134, v146, s[88:91], s81 offen offset:512\n"
"buffer_load_ushort v135, v146, s[88:91], s81 offen offset:768\n"
"buffer_load_ushort v136, v146, s[88:91], s80 offen offset:32\n"
"buffer_load_ushort v137, v146, s[88:91], s80 offen offset:288\n"
"buffer_load_ushort v138, v146, s[88:91], s80 offen offset:544\n"
"buffer_load_ushort v139, v146, s[88:91], s80 offen offset:800\n"
"buffer_load_ushort v140, v146, s[88:91], s81 offen offset:32\n"
"buffer_load_ushort v141, v146, s[88:91], s81 offen offset:288\n"
"buffer_load_ushort v142, v146, s[88:91], s81 offen offset:544\n"
"buffer_load_ushort v143, v146, s[88:91], s81 offen offset:800\n"
"s_waitcnt vmcnt(0)\n"
"v_lshl_or_b32 v128, v129, 16, v128\n"
"v_lshl_or_b32 v129, v131, 16, v130\n"
"v_lshl_or_b32 v130, v133, 16, v132\n"
"v_lshl_or_b32 v131, v135, 16, v134\n"
"v_lshl_or_b32 v136, v137, 16, v136\n"
"v_lshl_or_b32 v137, v139, 16, v138\n"
"v_lshl_or_b32 v138, v141, 16, v140\n"
"v_lshl_or_b32 v139, v143, 16, v142\n"
"v_mfma_f32_16x16x32_bf16 v[0:3], v[96:99], v[128:131], v[0:3]\n"
"s_nop 0\n"
"v_mfma_f32_16x16x32_bf16 v[4:7], v[96:99], v[136:139], v[4:7]\n"
"v_mfma_f32_16x16x32_bf16 v[32:35], v[104:107], v[128:131], v[32:35]\n"
"v_mfma_f32_16x16x32_bf16 v[36:39], v[104:107], v[136:139], v[36:39]\n"
"v_mfma_f32_16x16x32_bf16 v[64:67], v[112:115], v[128:131], v[64:67]\n"
"v_mfma_f32_16x16x32_bf16 v[68:71], v[112:115], v[136:139], v[68:71]\n"
"s_cmp_lg_u32 s83, s43\n"
"s_cbranch_scc1 LSK3B0_%=\n"
"v_mfma_f32_16x16x32_bf16 a[208:211], v[120:123], v[128:131], a[208:211]\n"
"v_mfma_f32_16x16x32_bf16 a[212:215], v[120:123], v[136:139], a[212:215]\n"
"LSK3B0_%=:\n"
"s_nop 3\n"
"buffer_load_ushort v128, v147, s[88:91], s80 offen offset:0\n"
"buffer_load_ushort v129, v147, s[88:91], s80 offen offset:256\n"
"buffer_load_ushort v130, v147, s[88:91], s80 offen offset:512\n"
"buffer_load_ushort v131, v147, s[88:91], s80 offen offset:768\n"
"buffer_load_ushort v132, v147, s[88:91], s81 offen offset:0\n"
"buffer_load_ushort v133, v147, s[88:91], s81 offen offset:256\n"
"buffer_load_ushort v134, v147, s[88:91], s81 offen offset:512\n"
"buffer_load_ushort v135, v147, s[88:91], s81 offen offset:768\n"
"buffer_load_ushort v136, v147, s[88:91], s80 offen offset:32\n"
"buffer_load_ushort v137, v147, s[88:91], s80 offen offset:288\n"
"buffer_load_ushort v138, v147, s[88:91], s80 offen offset:544\n"
"buffer_load_ushort v139, v147, s[88:91], s80 offen offset:800\n"
"buffer_load_ushort v140, v147, s[88:91], s81 offen offset:32\n"
"buffer_load_ushort v141, v147, s[88:91], s81 offen offset:288\n"
"buffer_load_ushort v142, v147, s[88:91], s81 offen offset:544\n"
"buffer_load_ushort v143, v147, s[88:91], s81 offen offset:800\n"
"s_waitcnt vmcnt(0)\n"
"v_lshl_or_b32 v128, v129, 16, v128\n"
"v_lshl_or_b32 v129, v131, 16, v130\n"
"v_lshl_or_b32 v130, v133, 16, v132\n"
"v_lshl_or_b32 v131, v135, 16, v134\n"
"v_lshl_or_b32 v136, v137, 16, v136\n"
"v_lshl_or_b32 v137, v139, 16, v138\n"
"v_lshl_or_b32 v138, v141, 16, v140\n"
"v_lshl_or_b32 v139, v143, 16, v142\n"
"v_mfma_f32_16x16x32_bf16 v[8:11], v[96:99], v[128:131], v[8:11]\n"
"s_nop 0\n"
"v_mfma_f32_16x16x32_bf16 v[12:15], v[96:99], v[136:139], v[12:15]\n"
"v_mfma_f32_16x16x32_bf16 v[40:43], v[104:107], v[128:131], v[40:43]\n"
"v_mfma_f32_16x16x32_bf16 v[44:47], v[104:107], v[136:139], v[44:47]\n"
"v_mfma_f32_16x16x32_bf16 v[72:75], v[112:115], v[128:131], v[72:75]\n"
"v_mfma_f32_16x16x32_bf16 v[76:79], v[112:115], v[136:139], v[76:79]\n"
"s_cmp_lg_u32 s83, s43\n"
"s_cbranch_scc1 LSK3B1_%=\n"
"v_mfma_f32_16x16x32_bf16 a[216:219], v[120:123], v[128:131], a[216:219]\n"
"v_mfma_f32_16x16x32_bf16 a[220:223], v[120:123], v[136:139], a[220:223]\n"
"LSK3B1_%=:\n"
"s_nop 3\n"
"buffer_load_ushort v128, v146, s[88:91], s80 offen offset:128\n"
"buffer_load_ushort v129, v146, s[88:91], s80 offen offset:384\n"
"buffer_load_ushort v130, v146, s[88:91], s80 offen offset:640\n"
"buffer_load_ushort v131, v146, s[88:91], s80 offen offset:896\n"
"buffer_load_ushort v132, v146, s[88:91], s81 offen offset:128\n"
"buffer_load_ushort v133, v146, s[88:91], s81 offen offset:384\n"
"buffer_load_ushort v134, v146, s[88:91], s81 offen offset:640\n"
"buffer_load_ushort v135, v146, s[88:91], s81 offen offset:896\n"
"buffer_load_ushort v136, v146, s[88:91], s80 offen offset:160\n"
"buffer_load_ushort v137, v146, s[88:91], s80 offen offset:416\n"
"buffer_load_ushort v138, v146, s[88:91], s80 offen offset:672\n"
"buffer_load_ushort v139, v146, s[88:91], s80 offen offset:928\n"
"buffer_load_ushort v140, v146, s[88:91], s81 offen offset:160\n"
"buffer_load_ushort v141, v146, s[88:91], s81 offen offset:416\n"
"buffer_load_ushort v142, v146, s[88:91], s81 offen offset:672\n"
"buffer_load_ushort v143, v146, s[88:91], s81 offen offset:928\n"
"s_waitcnt vmcnt(0)\n"
"v_lshl_or_b32 v128, v129, 16, v128\n"
"v_lshl_or_b32 v129, v131, 16, v130\n"
"v_lshl_or_b32 v130, v133, 16, v132\n"
"v_lshl_or_b32 v131, v135, 16, v134\n"
"v_lshl_or_b32 v136, v137, 16, v136\n"
"v_lshl_or_b32 v137, v139, 16, v138\n"
"v_lshl_or_b32 v138, v141, 16, v140\n"
"v_lshl_or_b32 v139, v143, 16, v142\n"
"v_mfma_f32_16x16x32_bf16 v[16:19], v[96:99], v[128:131], v[16:19]\n"
"s_nop 0\n"
"v_mfma_f32_16x16x32_bf16 v[20:23], v[96:99], v[136:139], v[20:23]\n"
"v_mfma_f32_16x16x32_bf16 v[48:51], v[104:107], v[128:131], v[48:51]\n"
"v_mfma_f32_16x16x32_bf16 v[52:55], v[104:107], v[136:139], v[52:55]\n"
"v_mfma_f32_16x16x32_bf16 v[80:83], v[112:115], v[128:131], v[80:83]\n"
"v_mfma_f32_16x16x32_bf16 v[84:87], v[112:115], v[136:139], v[84:87]\n"
"s_cmp_lg_u32 s83, s43\n"
"s_cbranch_scc1 LSK3B2_%=\n"
"v_mfma_f32_16x16x32_bf16 a[224:227], v[120:123], v[128:131], a[224:227]\n"
"v_mfma_f32_16x16x32_bf16 a[228:231], v[120:123], v[136:139], a[228:231]\n"
"LSK3B2_%=:\n"
"s_nop 3\n"
"buffer_load_ushort v128, v147, s[88:91], s80 offen offset:128\n"
"buffer_load_ushort v129, v147, s[88:91], s80 offen offset:384\n"
"buffer_load_ushort v130, v147, s[88:91], s80 offen offset:640\n"
"buffer_load_ushort v131, v147, s[88:91], s80 offen offset:896\n"
"buffer_load_ushort v132, v147, s[88:91], s81 offen offset:128\n"
"buffer_load_ushort v133, v147, s[88:91], s81 offen offset:384\n"
"buffer_load_ushort v134, v147, s[88:91], s81 offen offset:640\n"
"buffer_load_ushort v135, v147, s[88:91], s81 offen offset:896\n"
"buffer_load_ushort v136, v147, s[88:91], s80 offen offset:160\n"
"buffer_load_ushort v137, v147, s[88:91], s80 offen offset:416\n"
"buffer_load_ushort v138, v147, s[88:91], s80 offen offset:672\n"
"buffer_load_ushort v139, v147, s[88:91], s80 offen offset:928\n"
"buffer_load_ushort v140, v147, s[88:91], s81 offen offset:160\n"
"buffer_load_ushort v141, v147, s[88:91], s81 offen offset:416\n"
"buffer_load_ushort v142, v147, s[88:91], s81 offen offset:672\n"
"buffer_load_ushort v143, v147, s[88:91], s81 offen offset:928\n"
"s_waitcnt vmcnt(0)\n"
"v_lshl_or_b32 v128, v129, 16, v128\n"
"v_lshl_or_b32 v129, v131, 16, v130\n"
"v_lshl_or_b32 v130, v133, 16, v132\n"
"v_lshl_or_b32 v131, v135, 16, v134\n"
"v_lshl_or_b32 v136, v137, 16, v136\n"
"v_lshl_or_b32 v137, v139, 16, v138\n"
"v_lshl_or_b32 v138, v141, 16, v140\n"
"v_lshl_or_b32 v139, v143, 16, v142\n"
"v_mfma_f32_16x16x32_bf16 v[24:27], v[96:99], v[128:131], v[24:27]\n"
"s_nop 0\n"
"v_mfma_f32_16x16x32_bf16 v[28:31], v[96:99], v[136:139], v[28:31]\n"
"v_mfma_f32_16x16x32_bf16 v[56:59], v[104:107], v[128:131], v[56:59]\n"
"v_mfma_f32_16x16x32_bf16 v[60:63], v[104:107], v[136:139], v[60:63]\n"
"v_mfma_f32_16x16x32_bf16 v[88:91], v[112:115], v[128:131], v[88:91]\n"
"v_mfma_f32_16x16x32_bf16 v[92:95], v[112:115], v[136:139], v[92:95]\n"
"s_cmp_lg_u32 s83, s43\n"
"s_cbranch_scc1 LSK3B3_%=\n"
"v_mfma_f32_16x16x32_bf16 a[232:235], v[120:123], v[128:131], a[232:235]\n"
"v_mfma_f32_16x16x32_bf16 a[236:239], v[120:123], v[136:139], a[236:239]\n"
"LSK3B3_%=:\n"
"s_nop 3\n"
"s_nop 9\n"
"s_add_u32 s80, s80, 8192\n"
"s_add_u32 s81, s81, 8192\n"
"s_add_u32 s82, s82, 1\n"
"s_cmp_lt_u32 s82, 32\n"
"s_cbranch_scc1 LSLOOP_%=\n"
"v_mov_b32 v154, v155\n"
"s_nop 1\n"
"v_permlane32_swap_b32 v155, v154\n"
"v_add_f32 v155, v155, v154\n"
"v_mov_b32 v154, v155\n"
"s_nop 1\n"
"v_permlane16_swap_b32 v155, v154\n"
"v_add_f32 v155, v155, v154\n"
"v_mov_b32 v196, v155\n"
"v_mov_b32 v158, v159\n"
"s_nop 1\n"
"v_permlane32_swap_b32 v159, v158\n"
"v_add_f32 v159, v159, v158\n"
"v_mov_b32 v158, v159\n"
"s_nop 1\n"
"v_permlane16_swap_b32 v159, v158\n"
"v_add_f32 v159, v159, v158\n"
"v_mov_b32 v198, v159\n"
"v_mov_b32 v162, v163\n"
"s_nop 1\n"
"v_permlane32_swap_b32 v163, v162\n"
"v_add_f32 v163, v163, v162\n"
"v_mov_b32 v162, v163\n"
"s_nop 1\n"
"v_permlane16_swap_b32 v163, v162\n"
"v_add_f32 v163, v163, v162\n"
"v_mov_b32 v200, v163\n"
"v_mov_b32 v166, v167\n"
"s_nop 1\n"
"v_permlane32_swap_b32 v167, v166\n"
"v_add_f32 v167, v167, v166\n"
"v_mov_b32 v166, v167\n"
"s_nop 1\n"
"v_permlane16_swap_b32 v167, v166\n"
"v_add_f32 v167, v167, v166\n"
"v_mov_b32 v202, v167\n"
"v_sub_f32 v192, 0, v164\n"
"v_mov_b32 v96, 0\n"
"v_mov_b32 v97, 0\n"
"v_mov_b32 v98, 0\n"
"v_mov_b32 v99, 0\n"
"v_mov_b32 v100, 0\n"
"v_mov_b32 v101, 0\n"
"v_mov_b32 v102, 0\n"
"v_mov_b32 v103, 0\n"
"v_mov_b32 v104, 0\n"
"v_mov_b32 v105, 0\n"
"v_mov_b32 v106, 0\n"
"v_mov_b32 v107, 0\n"
"v_mov_b32 v108, 0\n"
"v_mov_b32 v109, 0\n"
"v_mov_b32 v110, 0\n"
"v_mov_b32 v111, 0\n"
"v_mov_b32 v112, 0\n"
"v_mov_b32 v113, 0\n"
"v_mov_b32 v114, 0\n"
"v_mov_b32 v115, 0\n"
"v_mov_b32 v116, 0\n"
"v_mov_b32 v117, 0\n"
"v_mov_b32 v118, 0\n"
"v_mov_b32 v119, 0\n"
"v_mov_b32 v120, 0\n"
"v_mov_b32 v121, 0\n"
"v_mov_b32 v122, 0\n"
"v_mov_b32 v123, 0\n"
"v_mov_b32 v204, 0\n"
"v_mov_b32 v205, 0\n"
"s_nop 3\n"
"s_cmp_eq_u32 s47, 4\n"
"s_cbranch_scc1 LBACK0_%=\n"
"s_cmp_eq_u32 s47, 8\n"
"s_cbranch_scc1 LBACK1_%=\n"
"s_cmp_eq_u32 s47, 12\n"
"s_cbranch_scc1 LBACK2_%=\n"
"s_branch LBACK3_%=\n"
"LFIN_%=:\n"
    :: [kalo]"s"((unsigned)ka), [kahi]"s"((unsigned)(ka >> 32)), [wg]"s"(blockIdx.x), [wid]"s"(wid)
    : "v0","v1","v2","v3","v4","v5","v6","v7","v8","v9","v10","v11","v12","v13","v14","v15","v16","v17","v18","v19","v20","v21","v22","v23","v24","v25","v26","v27","v28","v29","v30","v31","v32","v33","v34","v35","v36","v37","v38","v39","v40","v41","v42","v43","v44","v45","v46","v47","v48","v49","v50","v51","v52","v53","v54","v55","v56","v57","v58","v59","v60","v61","v62","v63","v64","v65","v66","v67","v68","v69","v70","v71","v72","v73","v74","v75","v76","v77","v78","v79","v80","v81","v82","v83","v84","v85","v86","v87","v88","v89","v90","v91","v92","v93","v94","v95","v96","v97","v98","v99","v100","v101","v102","v103","v104","v105","v106","v107","v108","v109","v110","v111","v112","v113","v114","v115","v116","v117","v118","v119","v120","v121","v122","v123","v124","v125","v126","v127","v128","v129","v130","v131","v132","v133","v134","v135","v136","v137","v138","v139","v140","v141","v142","v143","v144","v145","v146","v147","v148","v149","v150","v151","v152","v153","v154","v155","v156","v157","v158","v159","v160","v161","v162","v163","v164","v165","v166","v167","v168","v169","v170","v171","v172","v173","v174","v175","v176","v177","v178","v179","v180","v181","v182","v183","v184","v185","v186","v187","v188","v189","v190","v191","v192","v193","v194","v195","v196","v197","v198","v199","v200","v201","v202","v203","v204","v205","v206","v207","v208","v209","v210","v211","v212","v213","v214","v215","v216","v217","v218","v219","v220","v221","v222","v223","v224","v225","v226","v227","v228","v229","v230","v231","v232","v233","v234","v235","v236","v237","v238","v239","v240","v241","v242","v243","v244","v245","v246","v247","v248","v249","v250","v251","v252","v253","v254","v255","a0","a1","a2","a3","a4","a5","a6","a7","a8","a9","a10","a11","a12","a13","a14","a15","a16","a17","a18","a19","a20","a21","a22","a23","a24","a25","a26","a27","a28","a29","a30","a31","a32","a33","a34","a35","a36","a37","a38","a39","a40","a41","a42","a43","a44","a45","a46","a47","a48","a49","a50","a51","a52","a53","a54","a55","a56","a57","a58","a59","a60","a61","a62","a63","a64","a65","a66","a67","a68","a69","a70","a71","a72","a73","a74","a75","a76","a77","a78","a79","a80","a81","a82","a83","a84","a85","a86","a87","a88","a89","a90","a91","a92","a93","a94","a95","a96","a97","a98","a99","a100","a101","a102","a103","a104","a105","a106","a107","a108","a109","a110","a111","a112","a113","a114","a115","a116","a117","a118","a119","a120","a121","a122","a123","a124","a125","a126","a127","a128","a129","a130","a131","a132","a133","a134","a135","a136","a137","a138","a139","a140","a141","a142","a143","a144","a145","a146","a147","a148","a149","a150","a151","a152","a153","a154","a155","a156","a157","a158","a159","a160","a161","a162","a163","a164","a165","a166","a167","a168","a169","a170","a171","a172","a173","a174","a175","a176","a177","a178","a179","a180","a181","a182","a183","a184","a185","a186","a187","a188","a189","a190","a191","a192","a193","a194","a195","a196","a197","a198","a199","a200","a201","a202","a203","a204","a205","a206","a207","a208","a209","a210","a211","a212","a213","a214","a215","a216","a217","a218","a219","a220","a221","a222","a223","a224","a225","a226","a227","a228","a229","a230","a231","a232","a233","a234","a235","a236","a237","a238","a239","a240","a241","a242","a243","a244","a245","a246","a247","a248","a249","a250","a251","a252","a253","a254","a255","s6","s7","s8","s9","s10","s11","s12","s13","s14","s15","s16","s17","s18","s19","s20","s21","s22","s23","s24","s25","s26","s27","s28","s29","s30","s31","s32","s33","s34","s35","s36","s37","s38","s39","s40","s41","s42","s43","s44","s45","s46","s47","s48","s49","s50","s51","s52","s53","s54","s55","s56","s57","s58","s59","s60","s61","s62","s63","s64","s65","s66","s67","s68","s69","s70","s71","s72","s73","s74","s75","s76","s77","s78","s79","s80","s81","s82","s83","s84","s85","s86","s87","s88","s89","s90","s91","s92","s93","s94","s95","s96","s97","s98","s99","s100","vcc","scc","m0","memory");
}
extern "C" void kernel_launch(void* const* d_in, const int* in_sizes, int n_in, void* d_out, int out_size, void* d_ws, size_t ws_size, hipStream_t stream) {
  char* xch = (char*)d_ws + ((ws_size - (size_t)75497472) & ~(size_t)0xfffff);
  (void)hipFuncSetAttribute((const void*)fused_attn, hipFuncAttributeMaxDynamicSharedMemorySize, 163840);
  hipLaunchKernelGGL(fused_attn, dim3(256), dim3(256), 163840, stream,
                     (const char*)d_in[0], (const char*)d_in[1], (const char*)d_in[2], (const char*)d_in[3], (const char*)d_in[4],
                     (const char*)d_in[5], (const char*)d_in[6], (const char*)d_in[7], (const char*)d_in[8], (const char*)d_in[9], xch, (char*)d_out, (char*)d_ws);
}
```

```cpp
#include <hip/hip_runtime.h>
#include <hip/hip_bf16.h>
#include <stdint.h>

__global__ __launch_bounds__(256, 1)
void fused_attn(const char* xA, const char* kA, const char* vA, const char* wA, const char* uA,
                const char* xB, const char* kB, const char* vB, const char* wB, const char* uB, char* xch, char* out, char* flags) {
  extern __shared__ __attribute__((aligned(16))) char lds[];
  const unsigned wid = __builtin_amdgcn_readfirstlane(threadIdx.x >> 6);
  const unsigned long long ka = (unsigned long long)__builtin_amdgcn_kernarg_segment_ptr();
  asm volatile(
"s_memtime s[64:65]\n"
"s_memrealtime s[66:67]\n"
"s_bfe_u32 s10, %[wg], 0x10002\n"
"s_mul_i32 s40, s10, 40\n"
"s_add_u32 s40, s40, %[kalo]\n"
"s_addc_u32 s41, %[kahi], 0\n"
"s_mov_b32 s78, %[kalo]\n"
"s_mov_b32 s79, %[kahi]\n"
"s_load_dwordx2 s[24:25], s[40:41], 0\n"
"s_load_dwordx2 s[28:29], s[40:41], 24\n"
"s_load_dwordx2 s[6:7], s[78:79], 0x60\n"
"s_load_dwordx2 s[16:17], s[40:41], 8\n"
"s_load_dwordx2 s[20:21], s[40:41], 16\n"
"s_load_dwordx2 s[8:9], s[40:41], 32\n"
"s_load_dwordx4 s[12:15], s[78:79], 0x50\n"
"s_mov_b32 s42, %[wid]\n"
"s_and_b32 s11, %[wg], 1\n"
"s_lshl_b32 s11, s11, 3\n"
"s_lshr_b32 s60, %[wg], 5\n"
"s_add_u32 s11, s11, s60\n"
"s_bfe_u32 s61, %[wg], 0x10001\n"
"s_bfe_u32 s60, %[wg], 0x20003\n"
"s_lshl2_add_u32 s61, s61, s60\n"
"s_getpc_b64 s[80:81]\n"
"LCP_%=:\n"
"s_mov_b32 s82, LEND_%=-LCP_%=\n"
"s_mov_b32 s83, 0x20000\n"
"v_mbcnt_lo_u32_b32 v2, -1, 0\n"
"v_lshlrev_b32 v2, 7, v2\n"
"s_lshr_b32 s60, %[wg], 3\n"
"s_lshl2_add_u32 s60, s60, s42\n"
"s_mul_i32 s60, s60, 640\n"
"s_mov_b64 exec, 31\n"
"buffer_load_dword v3, v2, s[80:83], s60 offen\n"
"s_mov_b64 exec, -1\n"
"s_mov_b32 s19, 0x20000\n"
"s_mov_b32 s23, 0x20000\n"
"s_mov_b32 s27, 0x20000\n"
"s_mov_b32 s31, 0x20000\n"
"s_mov_b32 s35, 0x20000\n"
"s_mov_b32 s39, 0x20000\n"
"s_mov_b32 s26, 286720\n"
"s_lshl_b32 s44, s42, 12\n"
"s_mov_b32 s50, s44\n"
"s_mul_i32 s96, s42, 7168\n"
"s_add_u32 s51, s96, 0x10000\n"
"s_mov_b32 s74, 0x8000\n"
"s_mov_b32 s58, 524288\n"
"s_mov_b32 s47, 0\n"
"s_add_u32 s60, s11, s61\n"
"s_and_b32 s60, s60, 0\n"
"s_mul_i32 s60, s60, 3\n"
"s_lshl_b32 s49, s60, 7\n"
"s_lshl_b32 s48, s60, 19\n"
"v_mbcnt_lo_u32_b32 v4, -1, 0\n"
"v_mbcnt_hi_u32_b32 v4, -1, v4\n"
"v_and_b32 v5, 15, v4\n"
"v_lshrrev_b32 v6, 4, v4\n"
"v_lshrrev_b32 v7, 3, v4\n"
"v_and_b32 v8, 7, v4\n"
"v_lshlrev_b32 v9, 1, v6\n"
"v_xor_b32 v9, v9, v8\n"
"v_lshlrev_b32 v9, 4, v9\n"
"s_mul_i32 s60, s42, 71680\n"
"v_mul_u32_u24 v10, 1280, v7\n"
"v_add3_u32 v247, v10, v9, s60\n"
"v_add_u32 v253, 9216, v247\n"
"v_add_u32 v254, 18432, v247\n"
"v_add_u32 v255, 27648, v247\n"
"v_lshl_add_u32 v240, v7, 8, v9\n"
"v_add_u32 v240, s44, v240\n"
"v_and_b32 v10, 1, v6\n"
"v_xor_b32 v10, v10, v8\n"
"v_lshlrev_b32 v10, 4, v10\n"
"v_lshl_add_u32 v10, v7, 13, v10\n"
"s_lshl_b32 s60, s42, 17\n"
"v_add_u32 v10, s60, v10\n"
"v_add_u32 v224, 1024, v10\n"
"v_add_u32 v225, 128, v10\n"
"v_xor_b32 v10, 64, v10\n"
"v_add_u32 v226, 64512, v10\n"
"v_add_u32 v227, 63616, v10\n"
"s_mul_i32 s40, s11, 286720\n"
"s_lshl_b32 s41, s61, 10\n"
"s_sub_u32 s30, 5243904, s41\n"
"s_sub_u32 s41, s41, 1024\n"
"s_ashr_i32 s60, s41, 31\n"
"s_lshl4_add_u32 s92, s10, s11\n"
"s_lshl_b32 s93, s92, 8\n"
"v_mov_b32 v0, 0\n"
"s_waitcnt lgkmcnt(0)\n"
"s_add_u32 s24, s24, s40\n"
"s_addc_u32 s25, s25, 0\n"
"s_and_b32 s25, s25, 0xffff\n"
"s_add_u32 s28, s28, s41\n"
"s_addc_u32 s29, s29, s60\n"
"s_and_b32 s29, s29, 0xffff\n"
"s_add_u32 s6, s6, s93\n"
"s_addc_u32 s7, s7, 0\n"
"global_load_dword v1, v0, s[6:7] offset:128 sc1\n"
"s_mov_b32 m0, s51\n"
"s_nop 0\n"
"buffer_load_dwordx4 v247, s[24:27], s49 offen lds\n"
"buffer_load_dwordx4 v253, s[24:27], s49 offen offset:1024 lds\n"
"buffer_load_dwordx4 v254, s[24:27], s49 offen offset:2048 lds\n"
"buffer_load_dwordx4 v255, s[24:27], s49 offen offset:3072 lds\n"
"s_add_u32 m0, s51, 4096\n"
"s_add_u32 s40, s49, 40960\n"
"buffer_load_dwordx4 v247, s[24:27], s40 offen lds\n"
"buffer_load_dwordx4 v253, s[24:27], s40 offen offset:1024 lds\n"
"buffer_load_dwordx4 v254, s[24:27], s40 offen offset:2048 lds\n"
"s_add_u32 s49, s49, 128\n"
"s_sub_u32 s41, s49, 1280\n"
"s_min_u32 s49, s49, s41\n"
"s_xor_b32 s51, s51, s74\n"
"s_mov_b32 m0, s50\n"
"s_nop 0\n"
"buffer_load_dwordx4 v224, s[28:31], s48 offen lds\n"
"buffer_load_dwordx4 v225, s[28:31], s48 offen offset:1024 lds\n"
"buffer_load_dwordx4 v226, s[28:31], s48 offen offset:2048 lds\n"
"buffer_load_dwordx4 v227, s[28:31], s48 offen offset:3072 lds\n"
"s_xor_b32 s50, s50, s74\n"
"s_xor_b32 m0, s50, 0xc000\n"
"s_add_u32 s40, s48, 256\n"
"buffer_load_dwordx4 v224, s[28:31], s40 offen lds\n"
"buffer_load_dwordx4 v225, s[28:31], s40 offen offset:1024 lds\n"
"buffer_load_dwordx4 v226, s[28:31], s40 offen offset:2048 lds\n"
"buffer_load_dwordx4 v227, s[28:31], s40 offen offset:3072 lds\n"
"s_cmp_eq_u32 s47, 8\ns_cselect_b32 s41, 524800, s58\n"
"s_add_u32 s48, s48, s41\n"
"s_sub_u32 s41, s48, 5242880\n"
"s_min_u32 s48, s48, s41\n"
"s_mov_b32 m0, s50\n"
"s_nop 0\n"
"buffer_load_dwordx4 v224, s[28:31], s48 offen lds\n"
"buffer_load_dwordx4 v225, s[28:31], s48 offen offset:1024 lds\n"
"buffer_load_dwordx4 v226, s[28:31], s48 offen offset:2048 lds\n"
"buffer_load_dwordx4 v227, s[28:31], s48 offen offset:3072 lds\n"
"s_xor_b32 s50, s50, s74\n"
"s_mov_b32 m0, s51\n"
"s_nop 0\n"
"buffer_load_dwordx4 v247, s[24:27], s49 offen lds\n"
"buffer_load_dwordx4 v253, s[24:27], s49 offen offset:1024 lds\n"
"buffer_load_dwordx4 v254, s[24:27], s49 offen offset:2048 lds\n"
"buffer_load_dwordx4 v255, s[24:27], s49 offen offset:3072 lds\n"
"s_add_u32 m0, s51, 4096\n"
"s_add_u32 s40, s49, 40960\n"
"buffer_load_dwordx4 v247, s[24:27], s40 offen lds\n"
"buffer_load_dwordx4 v253, s[24:27], s40 offen offset:1024 lds\n"
"buffer_load_dwordx4 v254, s[24:27], s40 offen offset:2048 lds\n"
"s_add_u32 s49, s49, 128\n"
"s_sub_u32 s41, s49, 1280\n"
"s_min_u32 s49, s49, s41\n"
"s_xor_b32 s51, s51, s74\n"
"s_and_b32 s40, s42, 1\n"
"s_lshl_b32 s43, s40, 6\n"
"s_mov_b32 s77, 0x80000000\n"
"s_mov_b32 s75, 0x10000\n"
"s_mov_b32 s73, 0xffff0000\n"
"s_mov_b32 s45, 0x3e0293ee\n"
"s_mov_b32 s46, 0x5d800000\n"
"s_mov_b32 s62, 9\n"
"v_lshl_add_u32 v242, v4, 4, s44\n"
"s_lshr_b32 s41, s42, 1\n"
"s_lshl4_add_u32 s76, s41, s40\n"
"s_lshl_b32 s76, s76, 10\n"
"s_add_u32 s76, s76, 0x20000\n"
"s_sub_u32 s76, s76, s44\n"
"v_add_u32 v248, s76, v242\n"
"v_and_b32 v9, 6, v5\n"
"v_xor_b32 v9, v9, v6\n"
"v_lshlrev_b32 v9, 4, v9\n"
"v_lshl_add_u32 v10, v5, 7, v9\n"
"s_mul_i32 s41, s42, 6144\n"
"s_add_u32 s41, s41, 0x10000\n"
"v_add_u32 v249, s41, v10\n"
"v_xor_b32 v250, 64, v249\n"
"s_lshr_b32 s41, s42, 1\n"
"s_lshl_b32 s41, s41, 11\n"
"s_add_u32 s41, s41, 90112\n"
"v_add_u32 v251, s41, v10\n"
"v_xor_b32 v252, 64, v251\n"
"v_and_b32 v11, 8, v5\n"
"v_lshl_add_u32 v10, v11, 7, v10\n"
"s_mul_i32 s41, s40, 8256\n"
"v_xor_b32 v228, s41, v10\n"
"v_xor_b32 v229, 64, v228\n"
"v_xor_b32 v230, 0x2000, v228\n"
"v_xor_b32 v231, 0x2000, v229\n"
"v_lshrrev_b32 v9, 5, v4\n"
"v_lshlrev_b32 v9, 11, v9\n"
"v_bfe_u32 v10, v4, 2, 3\n"
"v_lshl_add_u32 v9, v10, 7, v9\n"
"v_and_b32 v10, 3, v4\n"
"v_lshl_add_u32 v9, v10, 3, v9\n"
"v_bfe_u32 v10, v4, 3, 2\n"
"v_lshl_add_u32 v9, v10, 5, v9\n"
"s_add_u32 s41, s41, 0x10000\n"
"v_xor_b32 v232, s41, v9\n"
"v_xor_b32 v233, 32, v232\n"
"v_xor_b32 v234, 64, v232\n"
"v_xor_b32 v235, 96, v232\n"
"v_xor_b32 v236, 0x2000, v232\n"
"v_xor_b32 v237, 0x2000, v233\n"
"v_xor_b32 v238, 0x2000, v234\n"
"v_xor_b32 v239, 0x2000, v235\n"
"v_lshlrev_b32 v9, 11, v6\n"
"v_bfe_u32 v10, v4, 2, 2\n"
"v_lshl_add_u32 v9, v10, 7, v9\n"
"v_and_b32 v10, 1, v4\n"
"v_lshl_add_u32 v9, v10, 3, v9\n"
"v_bfe_u32 v10, v4, 1, 1\n"
"v_lshl_add_u32 v9, v10, 5, v9\n"
"v_bfe_u32 v10, v4, 3, 1\n"
"v_lshl_add_u32 v9, v10, 4, v9\n"
"v_bfe_u32 v10, v4, 4, 1\n"
"v_lshl_add_u32 v9, v10, 6, v9\n"
"v_xor_b32 v243, s43, v9\n"
"v_xor_b32 v244, 16, v243\n"
"v_xor_b32 v245, 64, v243\n"
"v_xor_b32 v246, 0x50, v243\n"
"s_lshl3_add_u32 s41, s11, s61\n"
"s_lshl_b32 s41, s41, 20\n"
"s_sub_u32 s41, s41, 896\n"
"s_ashr_i32 s60, s41, 31\n"
"s_add_u32 s16, s16, s41\n"
"s_addc_u32 s17, s17, s60\n"
"s_and_b32 s17, s17, 0xffff\n"
"s_mov_b32 s18, 1049472\n"
"s_mov_b64 s[84:85], s[16:17]\n"
"s_mov_b64 s[86:87], s[18:19]\n"
"s_add_u32 s20, s20, s41\n"
"s_addc_u32 s21, s21, s60\n"
"s_and_b32 s21, s21, 0xffff\n"
"s_mov_b32 s22, 1049472\n"
"s_mov_b64 s[88:89], s[20:21]\n"
"s_mov_b64 s[90:91], s[22:23]\n"
"s_lshr_b32 s57, %[wg], 3\n"
"s_mul_i32 s57, s57, 5\n"
"s_and_b32 s57, s57, 15\n"
"s_lshl_b32 s57, s57, 14\n"
"s_mov_b32 s55, 896\n"
"s_add_u32 s56, s57, 896\n"
"s_mov_b32 s59, 16384\n"
"s_mov_b32 s52, -896\n"
"s_mov_b32 s53, 0\n"
"s_mov_b32 s54, -896\n"
"s_mul_i32 s41, s61, 655360\n"
"s_add_u32 s8, s8, s41\n"
"s_addc_u32 s9, s9, 0\n"
"s_and_b32 s9, s9, 0xffff\n"
"s_mul_i32 s41, s92, 2293760\n"
"s_add_u32 s32, s12, s41\n"
"s_addc_u32 s33, s13, 0\n"
"s_and_b32 s33, s33, 0xffff\n"
"s_mov_b32 s34, 2293760\n"
"s_mul_i32 s41, s11, 573440\n"
"s_mul_i32 s60, s10, 1280\n"
"s_sub_u32 s38, 573440, s60\n"
"s_add_u32 s41, s41, s60\n"
"s_add_u32 s36, s14, s41\n"
"s_addc_u32 s37, s15, 0\n"
"s_and_b32 s37, s37, 0xffff\n"
"s_lshl2_add_u32 s93, s61, s42\n"
"s_mul_i32 s94, s61, 7168\n"
"s_lshl_b32 s41, s42, 11\n"
"s_add_u32 s94, s94, s41\n"
"s_add_u32 s94, s94, 229376\n"
"s_add_u32 s95, s94, 1024\n"
"s_lshl_b32 s60, s61, 7\n"
"s_add_u32 s60, s60, s43\n"
"s_mul_i32 s97, s42, 122880\n"
"s_add_u32 s97, s97, s60\n"
"s_lshr_b32 s98, s42, 1\n"
"s_add_u32 s98, s98, 12\n"
"s_mul_i32 s98, s98, 40960\n"
"s_add_u32 s98, s98, s60\n"
"s_and_b32 s41, s61, 3\n"
"s_mul_i32 s99, s41, 3\n"
"s_add_u32 s99, s99, s42\n"
"s_lshr_b32 s41, s41, 1\n"
"s_add_u32 s41, s41, 12\n"
"s_cmp_eq_u32 s42, 3\n"
"s_cselect_b32 s99, s41, s99\n"
"s_mul_i32 s99, s99, 40960\n"
"s_lshr_b32 s41, s61, 2\n"
"s_lshl_b32 s41, s41, 7\n"
"s_add_u32 s99, s99, s41\n"
"s_and_b32 s41, s61, 1\n"
"s_lshl_b32 s41, s41, 6\n"
"s_add_u32 s99, s99, s41\n"
"s_add_u32 s99, s99, 1024\n"
"s_xor_b32 s100, s99, 64\n"
"s_cmp_eq_u32 s42, 3\n"
"s_cselect_b32 s100, 0x40000000, s100\n"
"s_cselect_b32 s95, 0x40000000, s95\n"
"v_mov_b64 v[4:5], 0\n"
"v_mov_b64 v[6:7], 0\n"
"v_mov_b64 v[8:9], 0\n"
"v_mov_b64 v[10:11], 0\n"
"v_mov_b64 v[12:13], 0\n"
"v_mov_b64 v[14:15], 0\n"
"v_mov_b64 v[16:17], 0\n"
"v_mov_b64 v[18:19], 0\n"
"v_mov_b64 v[20:21], 0\n"
"v_mov_b64 v[22:23], 0\n"
"v_mov_b64 v[24:25], 0\n"
"v_mov_b64 v[26:27], 0\n"
"v_mov_b64 v[28:29], 0\n"
"v_mov_b64 v[30:31], 0\n"
"v_mov_b64 v[32:33], 0\n"
"v_mov_b64 v[34:35], 0\n"
"v_mov_b64 v[36:37], 0\n"
"v_mov_b64 v[38:39], 0\n"
"v_mov_b64 v[40:41], 0\n"
"v_mov_b64 v[42:43], 0\n"
"v_mov_b64 v[44:45], 0\n"
"v_mov_b64 v[46:47], 0\n"
"v_mov_b64 v[48:49], 0\n"
"v_mov_b64 v[50:51], 0\n"
"v_mov_b64 v[52:53], 0\n"
"v_mov_b64 v[54:55], 0\n"
"v_mov_b64 v[56:57], 0\n"
"v_mov_b64 v[58:59], 0\n"
"v_mov_b64 v[60:61], 0\n"
"v_mov_b64 v[62:63], 0\n"
"v_mov_b64 v[64:65], 0\n"
"v_mov_b64 v[66:67], 0\n"
"v_mov_b64 v[68:69], 0\n"
"v_mov_b64 v[70:71], 0\n"
"v_mov_b64 v[72:73], 0\n"
"v_mov_b64 v[74:75], 0\n"
"v_mov_b64 v[76:77], 0\n"
"v_mov_b64 v[78:79], 0\n"
"v_mov_b64 v[80:81], 0\n"
"v_mov_b64 v[82:83], 0\n"
"v_mov_b64 v[84:85], 0\n"
"v_mov_b64 v[86:87], 0\n"
"v_mov_b64 v[88:89], 0\n"
"v_mov_b64 v[90:91], 0\n"
"v_mov_b64 v[92:93], 0\n"
"v_mov_b64 v[94:95], 0\n"
"v_mov_b64 v[96:97], 0\n"
"v_mov_b64 v[98:99], 0\n"
"v_mov_b64 v[100:101], 0\n"
"v_mov_b64 v[102:103], 0\n"
"v_mov_b64 v[104:105], 0\n"
"v_mov_b64 v[106:107], 0\n"
"v_mov_b64 v[108:109], 0\n"
"v_mov_b64 v[110:111], 0\n"
"v_mov_b64 v[112:113], 0\n"
"v_mov_b64 v[114:115], 0\n"
"v_mov_b64 v[116:117], 0\n"
"v_mov_b64 v[118:119], 0\n"
"v_mov_b64 v[120:121], 0\n"
"v_mov_b64 v[122:123], 0\n"
"v_mov_b64 v[124:125], 0\n"
"v_mov_b64 v[126:127], 0\n"
"v_mov_b64 v[128:129], 0\n"
"v_mov_b64 v[130:131], 0\n"
"v_mov_b64 v[132:133], 0\n"
"v_mov_b64 v[134:135], 0\n"
"v_mov_b64 v[136:137], 0\n"
"v_mov_b64 v[138:139], 0\n"
"v_mov_b64 v[140:141], 0\n"
"v_mov_b64 v[142:143], 0\n"
"v_mov_b64 v[144:145], 0\n"
"v_mov_b64 v[146:147], 0\n"
"v_mov_b64 v[148:149], 0\n"
"v_mov_b64 v[150:151], 0\n"
"v_mov_b64 v[152:153], 0\n"
"v_mov_b64 v[154:155], 0\n"
"v_mov_b64 v[156:157], 0\n"
"v_mov_b64 v[158:159], 0\n"
"v_mov_b64 v[160:161], 0\n"
"v_mov_b64 v[162:163], 0\n"
"v_mov_b64 v[164:165], 0\n"
"v_mov_b64 v[166:167], 0\n"
"v_mov_b64 v[168:169], 0\n"
"v_mov_b64 v[170:171], 0\n"
"v_mov_b64 v[172:173], 0\n"
"v_mov_b64 v[174:175], 0\n"
"v_mov_b64 v[176:177], 0\n"
"v_mov_b64 v[178:179], 0\n"
"v_mov_b64 v[180:181], 0\n"
"v_mov_b64 v[182:183], 0\n"
"v_mov_b64 v[184:185], 0\n"
"v_mov_b64 v[186:187], 0\n"
"v_mov_b64 v[188:189], 0\n"
"v_mov_b64 v[190:191], 0\n"
"v_mov_b64 v[192:193], 0\n"
"v_mov_b64 v[194:195], 0\n"
"v_mov_b64 v[196:197], 0\n"
"v_mov_b64 v[198:199], 0\n"
"v_mov_b64 v[200:201], 0\n"
"v_mov_b64 v[202:203], 0\n"
"v_mov_b64 v[204:205], 0\n"
"v_mov_b64 v[206:207], 0\n"
"v_mov_b64 v[208:209], 0\n"
"v_mov_b64 v[210:211], 0\n"
"v_mov_b64 v[212:213], 0\n"
"v_mov_b64 v[214:215], 0\n"
"v_mov_b64 v[216:217], 0\n"
"v_mov_b64 v[218:219], 0\n"
"v_mov_b64 v[220:221], 0\n"
"v_mov_b64 v[222:223], 0\n"
"s_waitcnt vmcnt(26)\n"
"v_readfirstlane_b32 s92, v1\n"
"v_mov_b64 v[0:1], 0\n"
"v_mov_b64 v[2:3], 0\n"
"s_nop 2\n"
"s_mul_i32 s60, s92, 0x9e3779b1\n"
"s_add_u32 s60, s60, 0x7f4a7c15\n"
"s_lshl2_add_u32 s63, s11, s42\n"
"s_mul_i32 s63, s63, 10240\n"
"s_waitcnt vmcnt(19)\n"
"s_memtime s[80:81]\n"
"s_memrealtime s[82:83]\n"
"s_waitcnt lgkmcnt(0)\n"
"s_sub_u32 s80, s80, s64\n"
"s_sub_u32 s82, s82, s66\n"
"s_mul_i32 s80, s80, 10\n"
"s_mul_i32 s82, s82, 211\n"
"s_cmp_gt_u32 s80, s82\n"
"s_cselect_b32 s64, 1, 0\n"
"s_cselect_b32 s68, 655360, 0\n"
"s_cmp_eq_u32 s42, 0\n"
"s_cselect_b32 s64, s64, 0\n"
"s_mov_b32 s65, 0\n"
"s_add_u32 s66, s66, 2800\n"
"s_mov_b32 s67, s66\n"
"s_mov_b64 vcc, s[64:65]\n"
"s_waitcnt vmcnt(15)\n"
"s_barrier\n"
"ds_read_b128 a[160:163], v249 offset:0\n"
"ds_read_b128 a[164:167], v250 offset:0\n"
"ds_read_b64_tr_b16 a[96:97], v243 offset:0\n"
"ds_read_b64_tr_b16 a[98:99], v243 offset:512\n"
"ds_read_b64_tr_b16 a[100:101], v243 offset:8192\n"
"ds_read_b64_tr_b16 a[102:103], v243 offset:8704\n"
"ds_read_b64_tr_b16 a[104:105], v244 offset:0\n"
"ds_read_b64_tr_b16 a[106:107], v244 offset:512\n"
"ds_read_b64_tr_b16 a[108:109], v244 offset:8192\n"
"ds_read_b64_tr_b16 a[110:111], v244 offset:8704\n"
"ds_read_b64_tr_b16 a[112:113], v245 offset:0\n"
"ds_read_b64_tr_b16 a[114:115], v245 offset:512\n"
"ds_read_b64_tr_b16 a[116:117], v245 offset:8192\n"
"ds_read_b64_tr_b16 a[118:119], v245 offset:8704\n"
"ds_read_b64_tr_b16 a[120:121], v246 offset:0\n"
"ds_read_b64_tr_b16 a[122:123], v246 offset:512\n"
"ds_read_b64_tr_b16 a[124:125], v246 offset:8192\n"
"ds_read_b64_tr_b16 a[126:127], v246 offset:8704\n"
"ds_read_b64_tr_b16 a[128:129], v243 offset:1024\n"
"ds_read_b64_tr_b16 a[130:131], v243 offset:1536\n"
"ds_read_b64_tr_b16 a[132:133], v243 offset:9216\n"
"ds_read_b64_tr_b16 a[134:135], v243 offset:9728\n"
"ds_read_b64_tr_b16 a[136:137], v244 offset:1024\n"
"ds_read_b64_tr_b16 a[138:139], v244 offset:1536\n"
"ds_read_b64_tr_b16 a[140:141], v244 offset:9216\n"
"ds_read_b64_tr_b16 a[142:143], v244 offset:9728\n"
"ds_read_b64_tr_b16 a[144:145], v245 offset:1024\n"
"ds_read_b64_tr_b16 a[146:147], v245 offset:1536\n"
"ds_read_b64_tr_b16 a[148:149], v245 offset:9216\n"
"ds_read_b64_tr_b16 a[150:151], v245 offset:9728\n"
"ds_read_b64_tr_b16 a[152:153], v246 offset:1024\n"
"ds_read_b64_tr_b16 a[154:155], v246 offset:1536\n"
"ds_read_b64_tr_b16 a[156:157], v246 offset:9216\n"
"ds_read_b64_tr_b16 a[158:159], v246 offset:9728\n"
"ds_read_b128 a[168:171], v249 offset:2048\n"
"ds_read_b128 a[172:175], v250 offset:2048\n"
"ds_read_b128 a[176:179], v249 offset:4096\n"
"ds_read_b128 a[180:183], v250 offset:4096\n"
"ds_read_b128 a[184:187], v251 offset:0\n"
"ds_read_b128 a[188:191], v252 offset:0\n"
"LPL_%=:\n"
"s_waitcnt lgkmcnt(4)\n"
"v_mfma_f32_16x16x32_bf16 v[112:115], a[96:99], a[160:163], v[112:115]\n"
"v_mfma_f32_16x16x32_bf16 v[112:115], a[100:103], a[164:167], v[112:115]\n"
"v_mfma_f32_16x16x32_bf16 v[116:119], a[104:107], a[160:163], v[116:119]\n"
"v_mfma_f32_16x16x32_bf16 v[116:119], a[108:111], a[164:167], v[116:119]\n"
"s_waitcnt vmcnt(11) lgkmcnt(4)\n"
"v_mfma_f32_16x16x32_bf16 v[120:123], a[112:115], a[160:163], v[120:123]\n"
"s_barrier\n"
"v_mfma_f32_16x16x32_bf16 v[120:123], a[116:119], a[164:167], v[120:123]\n"
"s_add_u32 s40, s48, 256\n"
"s_xor_b32 m0, s50, 0xc000\n"
"ds_read_b64_tr_b16 a[192:193], v243 offset:16384\n"
"v_mfma_f32_16x16x32_bf16 v[124:127], a[120:123], a[160:163], v[124:127]\n"
"buffer_load_dwordx4 v224, s[28:31], s40 offen lds\n"
"v_mfma_f32_16x16x32_bf16 v[124:127], a[124:127], a[164:167], v[124:127]\n"
"ds_read_b64_tr_b16 a[194:195], v243 offset:16896\n"
"v_mfma_f32_16x16x32_bf16 v[128:131], a[128:131], a[160:163], v[128:131]\n"
"ds_read_b64_tr_b16 a[196:197], v243 offset:24576\n"
"v_mfma_f32_16x16x32_bf16 v[128:131], a[132:135], a[164:167], v[128:131]\n"
"ds_read_b64_tr_b16 a[198:199], v243 offset:25088\n"
"v_mfma_f32_16x16x32_bf16 v[132:135], a[136:139], a[160:163], v[132:135]\n"
"ds_read_b64_tr_b16 a[200:201], v244 offset:16384\n"
"v_mfma_f32_16x16x32_bf16 v[132:135], a[140:143], a[164:167], v[132:135]\n"
"buffer_load_dwordx4 v225, s[28:31], s40 offen offset:1024 lds\n"
"v_mfma_f32_16x16x32_bf16 v[136:139], a[144:147], a[160:163], v[136:139]\n"
"ds_read_b64_tr_b16 a[202:203], v244 offset:16896\n"
"v_mfma_f32_16x16x32_bf16 v[136:139], a[148:151], a[164:167], v[136:139]\n"
"ds_read_b64_tr_b16 a[204:205], v244 offset:24576\n"
"v_mfma_f32_16x16x32_bf16 v[140:143], a[152:155], a[160:163], v[140:143]\n"
"ds_read_b64_tr_b16 a[206:207], v244 offset:25088\n"
"v_mfma_f32_16x16x32_bf16 v[140:143], a[156:159], a[164:167], v[140:143]\n"
"s_waitcnt lgkmcnt(6)\n"
"ds_read_b64_tr_b16 a[208:209], v245 offset:16384\n"
"v_mfma_f32_16x16x32_bf16 v[172:175], a[152:155], a[168:171], v[172:175]\n"
"buffer_load_dwordx4 v226, s[28:31], s40 offen offset:2048 lds\n"
"v_mfma_f32_16x16x32_bf16 v[172:175], a[156:159], a[172:175], v[172:175]\n"
"ds_read_b64_tr_b16 a[210:211], v245 offset:16896\n"
"v_mfma_f32_16x16x32_bf16 v[168:171], a[144:147], a[168:171], v[168:171]\n"
"ds_read_b64_tr_b16 a[212:213], v245 offset:24576\n"
"v_mfma_f32_16x16x32_bf16 v[168:171], a[148:151], a[172:175], v[168:171]\n"
"ds_read_b64_tr_b16 a[214:215], v245 offset:25088\n"
"v_mfma_f32_16x16x32_bf16 v[164:167], a[136:139], a[168:171], v[164:167]\n"
"ds_read_b64_tr_b16 a[216:217], v246 offset:16384\n"
"v_mfma_f32_16x16x32_bf16 v[164:167], a[140:143], a[172:175], v[164:167]\n"
"buffer_load_dwordx4 v227, s[28:31], s40 offen offset:3072 lds\n"
"v_mfma_f32_16x16x32_bf16 v[160:163], a[128:131], a[168:171], v[160:163]\n"
"s_cmp_eq_u32 s47, 8\ns_cselect_b32 s41, 524800, s58\n"
"ds_read_b64_tr_b16 a[218:219], v246 offset:16896\n"
"v_mfma_f32_16x16x32_bf16 v[160:163], a[132:135], a[172:175], v[160:163]\n"
"s_add_u32 s48, s48, s41\n"
"ds_read_b64_tr_b16 a[220:221], v246 offset:24576\n"
"v_mfma_f32_16x16x32_bf16 v[156:159], a[120:123], a[168:171], v[156:159]\n"
"s_sub_u32 s41, s48, 5242880\n"
"ds_read_b64_tr_b16 a[222:223], v246 offset:25088\n"
"v_mfma_f32_16x16x32_bf16 v[156:159], a[124:127], a[172:175], v[156:159]\n"
"s_min_u32 s48, s48, s41\n"
"ds_read_b64_tr_b16 a[224:225], v243 offset:17408\n"
"v_mfma_f32_16x16x32_bf16 v[152:155], a[112:115], a[168:171], v[152:155]\n"
"s_mov_b32 m0, s50\n"
"ds_read_b64_tr_b16 a[226:227], v243 offset:17920\n"
"v_mfma_f32_16x16x32_bf16 v[152:155], a[116:119], a[172:175], v[152:155]\n"
"buffer_load_dwordx4 v224, s[28:31], s48 offen lds\n"
"v_mfma_f32_16x16x32_bf16 v[148:151], a[104:107], a[168:171], v[148:151]\n"
"ds_read_b64_tr_b16 a[228:229], v243 offset:25600\n"
"v_mfma_f32_16x16x32_bf16 v[148:151], a[108:111], a[172:175], v[148:151]\n"
"ds_read_b64_tr_b16 a[230:231], v243 offset:26112\n"
"v_mfma_f32_16x16x32_bf16 v[144:147], a[96:99], a[168:171], v[144:147]\n"
"ds_read_b64_tr_b16 a[232:233], v244 offset:17408\n"
"v_xor_b32 v243, s74, v243\n"
"v_mfma_f32_16x16x32_bf16 v[144:147], a[100:103], a[172:175], v[144:147]\n"
"s_waitcnt lgkmcnt(6)\n"
"buffer_load_dwordx4 v225, s[28:31], s48 offen offset:1024 lds\n"
"v_mfma_f32_16x16x32_bf16 v[176:179], a[96:99], a[176:179], v[176:179]\n"
"ds_read_b64_tr_b16 a[234:235], v244 offset:17920\n"
"v_xor_b32 v249, s74, v249\n"
"v_mfma_f32_16x16x32_bf16 v[176:179], a[100:103], a[180:183], v[176:179]\n"
"ds_read_b64_tr_b16 a[236:237], v244 offset:25600\n"
"v_xor_b32 v250, s74, v250\n"
"v_mfma_f32_16x16x32_bf16 v[180:183], a[104:107], a[176:179], v[180:183]\n"
"ds_read_b64_tr_b16 a[238:239], v244 offset:26112\n"
"v_xor_b32 v244, s74, v244\n"
"v_mfma_f32_16x16x32_bf16 v[180:183], a[108:111], a[180:183], v[180:183]\n"
"ds_read_b64_tr_b16 a[240:241], v245 offset:17408\n"
"v_xor_b32 v251, s74, v251\n"
"v_mfma_f32_16x16x32_bf16 v[184:187], a[112:115], a[176:179], v[184:187]\n"
"buffer_load_dwordx4 v226, s[28:31], s48 offen offset:2048 lds\n"
"v_mfma_f32_16x16x32_bf16 v[184:187], a[116:119], a[180:183], v[184:187]\n"
"ds_read_b64_tr_b16 a[242:243], v245 offset:17920\n"
"v_xor_b32 v252, s74, v252\n"
"v_mfma_f32_16x16x32_bf16 v[188:191], a[120:123], a[176:179], v[188:191]\n"
"ds_read_b64_tr_b16 a[244:245], v245 offset:25600\n"
"v_mfma_f32_16x16x32_bf16 v[188:191], a[124:127], a[180:183], v[188:191]\n"
"ds_read_b64_tr_b16 a[246:247], v245 offset:26112\n"
"v_xor_b32 v245, s74, v245\n"
"v_mfma_f32_16x16x32_bf16 v[200:203], a[144:147], a[176:179], v[200:203]\n"
"ds_read_b64_tr_b16 a[248:249], v246 offset:17408\n"
"v_mfma_f32_16x16x32_bf16 v[200:203], a[148:151], a[180:183], v[200:203]\n"
"buffer_load_dwordx4 v227, s[28:31], s48 offen offset:3072 lds\n"
"v_mfma_f32_16x16x32_bf16 v[204:207], a[152:155], a[176:179], v[204:207]\n"
"s_xor_b32 s50, s50, s74\n"
"ds_read_b64_tr_b16 a[250:251], v246 offset:17920\n"
"v_mfma_f32_16x16x32_bf16 v[204:207], a[156:159], a[180:183], v[204:207]\n"
"ds_read_b64_tr_b16 a[252:253], v246 offset:25600\n"
"v_mfma_f32_16x16x32_bf16 v[192:195], a[128:131], a[176:179], v[192:195]\n"
"ds_read_b64_tr_b16 a[254:255], v246 offset:26112\n"
"v_xor_b32 v246, s74, v246\n"
"v_mfma_f32_16x16x32_bf16 v[192:195], a[132:135], a[180:183], v[192:195]\n"
"v_mfma_f32_16x16x32_bf16 v[196:199], a[136:139], a[176:179], v[196:199]\n"
"v_mfma_f32_16x16x32_bf16 v[196:199], a[140:143], a[180:183], v[196:199]\n"
"v_mfma_f32_16x16x32_bf16 v[220:223], a[136:139], a[184:187], v[220:223]\n"
"v_mfma_f32_16x16x32_bf16 v[220:223], a[140:143], a[188:191], v[220:223]\n"
"v_mfma_f32_16x16x32_bf16 v[216:219], a[128:131], a[184:187], v[216:219]\n"
"v_mfma_f32_16x16x32_bf16 v[216:219], a[132:135], a[188:191], v[216:219]\n"
"v_mfma_f32_16x16x32_bf16 v[212:215], a[104:107], a[184:187], v[212:215]\n"
"v_mfma_f32_16x16x32_bf16 v[212:215], a[108:111], a[188:191], v[212:215]\n"
"v_mfma_f32_16x16x32_bf16 v[208:211], a[96:99], a[184:187], v[208:211]\n"
"v_mfma_f32_16x16x32_bf16 v[208:211], a[100:103], a[188:191], v[208:211]\n"
"v_mfma_f32_16x16x32_bf16 v[0:3], a[192:195], a[160:163], v[0:3]\n"
"s_waitcnt lgkmcnt(0)\n"
"v_mfma_f32_16x16x32_bf16 v[0:3], a[196:199], a[164:167], v[0:3]\n"
"v_mfma_f32_16x16x32_bf16 v[4:7], a[200:203], a[160:163], v[4:7]\n"
"v_mfma_f32_16x16x32_bf16 v[4:7], a[204:207], a[164:167], v[4:7]\n"
"s_waitcnt vmcnt(8) lgkmcnt(0)\n"
"v_mfma_f32_16x16x32_bf16 v[8:11], a[208:211], a[160:163], v[8:11]\n"
"s_barrier\n"
"v_mfma_f32_16x16x32_bf16 v[8:11], a[212:215], a[164:167], v[8:11]\n"
"s_mov_b32 m0, s51\n"
"ds_read_b64_tr_b16 a[96:97], v243 offset:0\n"
"v_mfma_f32_16x16x32_bf16 v[12:15], a[216:219], a[160:163], v[12:15]\n"
"buffer_load_dwordx4 v247, s[24:27], s49 offen lds\n"
"v_mfma_f32_16x16x32_bf16 v[12:15], a[220:223], a[164:167], v[12:15]\n"
"ds_read_b64_tr_b16 a[98:99], v243 offset:512\n"
"v_mfma_f32_16x16x32_bf16 v[16:19], a[224:227], a[160:163], v[16:19]\n"
"ds_read_b64_tr_b16 a[100:101], v243 offset:8192\n"
"v_mfma_f32_16x16x32_bf16 v[16:19], a[228:231], a[164:167], v[16:19]\n"
"ds_read_b64_tr_b16 a[102:103], v243 offset:8704\n"
"v_mfma_f32_16x16x32_bf16 v[20:23], a[232:235], a[160:163], v[20:23]\n"
"ds_read_b64_tr_b16 a[104:105], v244 offset:0\n"
"v_mfma_f32_16x16x32_bf16 v[20:23], a[236:239], a[164:167], v[20:23]\n"
"buffer_load_dwordx4 v253, s[24:27], s49 offen offset:1024 lds\n"
"v_mfma_f32_16x16x32_bf16 v[24:27], a[240:243], a[160:163], v[24:27]\n"
"ds_read_b64_tr_b16 a[106:107], v244 offset:512\n"
"v_mfma_f32_16x16x32_bf16 v[24:27], a[244:247], a[164:167], v[24:27]\n"
"ds_read_b64_tr_b16 a[108:109], v244 offset:8192\n"
"v_mfma_f32_16x16x32_bf16 v[28:31], a[248:251], a[160:163], v[28:31]\n"
"ds_read_b128 a[160:163], v249 offset:0\n"
"v_mfma_f32_16x16x32_bf16 v[28:31], a[252:255], a[164:167], v[28:31]\n"
"ds_read_b128 a[164:167], v250 offset:0\n"
"v_mfma_f32_16x16x32_bf16 v[60:63], a[248:251], a[168:171], v[60:63]\n"
"buffer_load_dwordx4 v254, s[24:27], s49 offen offset:2048 lds\n"
"v_mfma_f32_16x16x32_bf16 v[60:63], a[252:255], a[172:175], v[60:63]\n"
"ds_read_b64_tr_b16 a[110:111], v244 offset:8704\n"
"v_mfma_f32_16x16x32_bf16 v[56:59], a[240:243], a[168:171], v[56:59]\n"
"ds_read_b64_tr_b16 a[112:113], v245 offset:0\n"
"v_mfma_f32_16x16x32_bf16 v[56:59], a[244:247], a[172:175], v[56:59]\n"
"ds_read_b64_tr_b16 a[114:115], v245 offset:512\n"
"v_mfma_f32_16x16x32_bf16 v[52:55], a[232:235], a[168:171], v[52:55]\n"
"ds_read_b64_tr_b16 a[116:117], v245 offset:8192\n"
"v_mfma_f32_16x16x32_bf16 v[52:55], a[236:239], a[172:175], v[52:55]\n"
"buffer_load_dwordx4 v255, s[24:27], s49 offen offset:3072 lds\n"
"v_mfma_f32_16x16x32_bf16 v[48:51], a[224:227], a[168:171], v[48:51]\n"
"ds_read_b64_tr_b16 a[118:119], v245 offset:8704\n"
"v_mfma_f32_16x16x32_bf16 v[48:51], a[228:231], a[172:175], v[48:51]\n"
"ds_read_b64_tr_b16 a[120:121], v246 offset:0\n"
"v_mfma_f32_16x16x32_bf16 v[44:47], a[216:219], a[168:171], v[44:47]\n"
"s_add_u32 s40, s49, 40960\n"
"ds_read_b64_tr_b16 a[122:123], v246 offset:512\n"
"v_mfma_f32_16x16x32_bf16 v[44:47], a[220:223], a[172:175], v[44:47]\n"
"s_add_u32 m0, s51, 4096\n"
"ds_read_b64_tr_b16 a[124:125], v246 offset:8192\n"
"v_mfma_f32_16x16x32_bf16 v[40:43], a[208:211], a[168:171], v[40:43]\n"
"buffer_load_dwordx4 v247, s[24:27], s40 offen lds\n"
"v_mfma_f32_16x16x32_bf16 v[40:43], a[212:215], a[172:175], v[40:43]\n"
"ds_read_b64_tr_b16 a[126:127], v246 offset:8704\n"
"v_mfma_f32_16x16x32_bf16 v[36:39], a[200:203], a[168:171], v[36:39]\n"
"ds_read_b64_tr_b16 a[128:129], v243 offset:1024\n"
"v_mfma_f32_16x16x32_bf16 v[36:39], a[204:207], a[172:175], v[36:39]\n"
"ds_read_b64_tr_b16 a[130:131], v243 offset:1536\n"
"v_mfma_f32_16x16x32_bf16 v[32:35], a[192:195], a[168:171], v[32:35]\n"
"ds_read_b64_tr_b16 a[132:133], v243 offset:9216\n"
"v_mfma_f32_16x16x32_bf16 v[32:35], a[196:199], a[172:175], v[32:35]\n"
"buffer_load_dwordx4 v253, s[24:27], s40 offen offset:1024 lds\n"
"v_mfma_f32_16x16x32_bf16 v[64:67], a[192:195], a[176:179], v[64:67]\n"
"ds_read_b64_tr_b16 a[134:135], v243 offset:9728\n"
"v_mfma_f32_16x16x32_bf16 v[64:67], a[196:199], a[180:183], v[64:67]\n"
"ds_read_b64_tr_b16 a[136:137], v244 offset:1024\n"
"v_mfma_f32_16x16x32_bf16 v[68:71], a[200:203], a[176:179], v[68:71]\n"
"ds_read_b64_tr_b16 a[138:139], v244 offset:1536\n"
"v_mfma_f32_16x16x32_bf16 v[68:71], a[204:207], a[180:183], v[68:71]\n"
"ds_read_b64_tr_b16 a[140:141], v244 offset:9216\n"
"v_mfma_f32_16x16x32_bf16 v[72:75], a[208:211], a[176:179], v[72:75]\n"
"buffer_load_dwordx4 v254, s[24:27], s40 offen offset:2048 lds\n"
"v_mfma_f32_16x16x32_bf16 v[72:75], a[212:215], a[180:183], v[72:75]\n"
"s_add_u32 s49, s49, 128\n"
"ds_read_b64_tr_b16 a[142:143], v244 offset:9728\n"
"v_mfma_f32_16x16x32_bf16 v[76:79], a[216:219], a[176:179], v[76:79]\n"
"s_sub_u32 s41, s49, 1280\n"
"ds_read_b64_tr_b16 a[144:145], v245 offset:1024\n"
"v_mfma_f32_16x16x32_bf16 v[76:79], a[220:223], a[180:183], v[76:79]\n"
"s_min_u32 s49, s49, s41\n"
"ds_read_b64_tr_b16 a[146:147], v245 offset:1536\n"
"v_mfma_f32_16x16x32_bf16 v[88:91], a[240:243], a[176:179], v[88:91]\n"
"s_xor_b32 s51, s51, s74\n"
"ds_read_b64_tr_b16 a[148:149], v245 offset:9216\n"
"v_mfma_f32_16x16x32_bf16 v[88:91], a[244:247], a[180:183], v[88:91]\n"
"ds_read_b64_tr_b16 a[150:151], v245 offset:9728\n"
"v_mfma_f32_16x16x32_bf16 v[92:95], a[248:251], a[176:179], v[92:95]\n"
"ds_read_b64_tr_b16 a[152:153], v246 offset:1024\n"
"v_mfma_f32_16x16x32_bf16 v[92:95], a[252:255], a[180:183], v[92:95]\n"
"ds_read_b64_tr_b16 a[154:155], v246 offset:1536\n"
"v_mfma_f32_16x16x32_bf16 v[80:83], a[224:227], a[176:179], v[80:83]\n"
"ds_read_b64_tr_b16 a[156:157], v246 offset:9216\n"
"v_mfma_f32_16x16x32_bf16 v[80:83], a[228:231], a[180:183], v[80:83]\n"
"ds_read_b64_tr_b16 a[158:159], v246 offset:9728\n"
"v_mfma_f32_16x16x32_bf16 v[84:87], a[232:235], a[176:179], v[84:87]\n"
"ds_read_b128 a[168:171], v249 offset:2048\n"
"v_mfma_f32_16x16x32_bf16 v[84:87], a[236:239], a[180:183], v[84:87]\n"
"ds_read_b128 a[172:175], v250 offset:2048\n"
"v_mfma_f32_16x16x32_bf16 v[108:111], a[232:235], a[184:187], v[108:111]\n"
"ds_read_b128 a[176:179], v249 offset:4096\n"
"v_mfma_f32_16x16x32_bf16 v[108:111], a[236:239], a[188:191], v[108:111]\n"
"ds_read_b128 a[180:183], v250 offset:4096\n"
"v_mfma_f32_16x16x32_bf16 v[104:107], a[224:227], a[184:187], v[104:107]\n"
"v_mfma_f32_16x16x32_bf16 v[104:107], a[228:231], a[188:191], v[104:107]\n"
"v_mfma_f32_16x16x32_bf16 v[100:103], a[200:203], a[184:187], v[100:103]\n"
"v_mfma_f32_16x16x32_bf16 v[100:103], a[204:207], a[188:191], v[100:103]\n"
"v_mfma_f32_16x16x32_bf16 v[96:99], a[192:195], a[184:187], v[96:99]\n"
"s_add_u32 s47, s47, 1\n"
"ds_read_b128 a[184:187], v251 offset:0\n"
"v_mfma_f32_16x16x32_bf16 v[96:99], a[196:199], a[188:191], v[96:99]\n"
"s_cmp_lg_u32 s47, s62\n"
"ds_read_b128 a[188:191], v252 offset:0\n"
"s_cbranch_scc1 LPL_%=\n"
"s_cmp_eq_u32 s47, 18\n"
"s_cbranch_scc1 LPT2_%=\n"
"s_waitcnt lgkmcnt(4)\n"
"v_mfma_f32_16x16x32_bf16 v[112:115], a[96:99], a[160:163], v[112:115]\n"
"v_mfma_f32_16x16x32_bf16 v[112:115], a[100:103], a[164:167], v[112:115]\n"
"v_mfma_f32_16x16x32_bf16 v[116:119], a[104:107], a[160:163], v[116:119]\n"
"v_mfma_f32_16x16x32_bf16 v[116:119], a[108:111], a[164:167], v[116:119]\n"
"s_waitcnt vmcnt(11)\n"
"v_mfma_f32_16x16x32_bf16 v[120:123], a[112:115], a[160:163], v[120:123]\n"
"s_barrier\n"
"v_mfma_f32_16x16x32_bf16 v[120:123], a[116:119], a[164:167], v[120:123]\n"
"s_add_u32 s40, s48, 256\n"
"s_xor_b32 m0, s50, 0xc000\n"
"ds_read_b64_tr_b16 a[232:233], v244 offset:17408\n"
"v_mfma_f32_16x16x32_bf16 v[124:127], a[120:123], a[160:163], v[124:127]\n"
"buffer_load_dwordx4 v224, s[28:31], s40 offen lds\n"
"v_mfma_f32_16x16x32_bf16 v[124:127], a[124:127], a[164:167], v[124:127]\n"
"ds_read_b64_tr_b16 a[234:235], v244 offset:17920\n"
"v_mul_f32 v112, s45, v112\n"
"v_mfma_f32_16x16x32_bf16 v[128:131], a[128:131], a[160:163], v[128:131]\n"
"ds_read_b64_tr_b16 a[236:237], v244 offset:25600\n"
"v_mul_f32 v113, s45, v113\n"
"v_mfma_f32_16x16x32_bf16 v[128:131], a[132:135], a[164:167], v[128:131]\n"
"ds_read_b64_tr_b16 a[238:239], v244 offset:26112\n"
"v_mul_f32 v114, s45, v114\n"
"v_mfma_f32_16x16x32_bf16 v[132:135], a[136:139], a[160:163], v[132:135]\n"
"ds_read_b64_tr_b16 a[224:225], v243 offset:17408\n"
"v_mul_f32 v115, s45, v115\n"
"v_mfma_f32_16x16x32_bf16 v[132:135], a[140:143], a[164:167], v[132:135]\n"
"buffer_load_dwordx4 v225, s[28:31], s40 offen offset:1024 lds\n"
"v_mfma_f32_16x16x32_bf16 v[136:139], a[144:147], a[160:163], v[136:139]\n"
"ds_read_b64_tr_b16 a[226:227], v243 offset:17920\n"
"v_mul_f32 v116, s45, v116\n"
"v_mfma_f32_16x16x32_bf16 v[136:139], a[148:151], a[164:167], v[136:139]\n"
"ds_read_b64_tr_b16 a[228:229], v243 offset:25600\n"
"v_mul_f32 v117, s45, v117\n"
"v_mfma_f32_16x16x32_bf16 v[140:143], a[152:155], a[160:163], v[140:143]\n"
"ds_read_b64_tr_b16 a[230:231], v243 offset:26112\n"
"v_mul_f32 v118, s45, v118\n"
"v_mfma_f32_16x16x32_bf16 v[140:143], a[156:159], a[164:167], v[140:143]\n"
"s_waitcnt lgkmcnt(6)\n"
"ds_read_b64_tr_b16 a[192:193], v243 offset:16384\n"
"v_mul_f32 v119, s45, v119\n"
"v_mfma_f32_16x16x32_bf16 v[220:223], a[136:139], a[184:187], v[220:223]\n"
"buffer_load_dwordx4 v226, s[28:31], s40 offen offset:2048 lds\n"
"v_mfma_f32_16x16x32_bf16 v[220:223], a[140:143], a[188:191], v[220:223]\n"
"ds_read_b64_tr_b16 a[194:195], v243 offset:16896\n"
"v_cvt_pk_bf16_f32 v112, v112, v113\n"
"v_mfma_f32_16x16x32_bf16 v[216:219], a[128:131], a[184:187], v[216:219]\n"
"ds_read_b64_tr_b16 a[196:197], v243 offset:24576\n"
"v_cvt_pk_bf16_f32 v113, v114, v115\n"
"v_mfma_f32_16x16x32_bf16 v[216:219], a[132:135], a[188:191], v[216:219]\n"
"ds_read_b64_tr_b16 a[198:199], v243 offset:25088\n"
"v_cvt_pk_bf16_f32 v114, v116, v117\n"
"v_mfma_f32_16x16x32_bf16 v[212:215], a[104:107], a[184:187], v[212:215]\n"
"ds_read_b64_tr_b16 a[200:201], v244 offset:16384\n"
"v_cvt_pk_bf16_f32 v115, v118, v119\n"
"v_mfma_f32_16x16x32_bf16 v[212:215], a[108:111], a[188:191], v[212:215]\n"
"buffer_load_dwordx4 v227, s[28:31], s40 offen offset:3072 lds\n"
"v_mfma_f32_16x16x32_bf16 v[208:211], a[96:99], a[184:187], v[208:211]\n"
"s_cmp_eq_u32 s47, 8\ns_cselect_b32 s41, 524800, s58\n"
"ds_read_b64_tr_b16 a[202:203], v244 offset:16896\n"
"v_mul_f32 v120, s45, v120\n"
"v_mfma_f32_16x16x32_bf16 v[208:211], a[100:103], a[188:191], v[208:211]\n"
"s_add_u32 s48, s48, s41\n"
"ds_read_b64_tr_b16 a[204:205], v244 offset:24576\n"
"v_mul_f32 v121, s45, v121\n"
"v_mfma_f32_16x16x32_bf16 v[172:175], a[152:155], a[168:171], v[172:175]\n"
"s_sub_u32 s41, s48, 5242880\n"
"ds_read_b64_tr_b16 a[206:207], v244 offset:25088\n"
"v_mul_f32 v122, s45, v122\n"
"v_mfma_f32_16x16x32_bf16 v[172:175], a[156:159], a[172:175], v[172:175]\n"
"s_min_u32 s48, s48, s41\n"
"ds_read_b64_tr_b16 a[208:209], v245 offset:16384\n"
"v_mul_f32 v123, s45, v123\n"
"v_mfma_f32_16x16x32_bf16 v[168:171], a[144:147], a[168:171], v[168:171]\n"
"s_mov_b32 m0, s50\n"
"ds_read_b64_tr_b16 a[210:211], v245 offset:16896\n"
"v_mul_f32 v124, s45, v124\n"
"v_mfma_f32_16x16x32_bf16 v[168:171], a[148:151], a[172:175], v[168:171]\n"
"buffer_load_dwordx4 v224, s[28:31], s48 offen lds\n"
"v_mfma_f32_16x16x32_bf16 v[164:167], a[136:139], a[168:171], v[164:167]\n"
"ds_read_b64_tr_b16 a[212:213], v245 offset:24576\n"
"v_mul_f32 v125, s45, v125\n"
"v_mfma_f32_16x16x32_bf16 v[164:167], a[140:143], a[172:175], v[164:167]\n"
"ds_read_b64_tr_b16 a[214:215], v245 offset:25088\n"
"v_mul_f32 v126, s45, v126\n"
"v_mfma_f32_16x16x32_bf16 v[160:163], a[128:131], a[168:171], v[160:163]\n"
"ds_read_b64_tr_b16 a[216:217], v246 offset:16384\n"
"v_xor_b32 v243, s74, v243\n"
"v_mfma_f32_16x16x32_bf16 v[160:163], a[132:135], a[172:175], v[160:163]\n"
"buffer_load_dwordx4 v225, s[28:31], s48 offen offset:1024 lds\n"
"v_mfma_f32_16x16x32_bf16 v[156:159], a[120:123], a[168:171], v[156:159]\n"
"ds_read_b64_tr_b16 a[218:219], v246 offset:16896\n"
"v_xor_b32 v249, s74, v249\n"
"v_mfma_f32_16x16x32_bf16 v[156:159], a[124:127], a[172:175], v[156:159]\n"
"ds_read_b64_tr_b16 a[220:221], v246 offset:24576\n"
"v_xor_b32 v250, s74, v250\n"
"v_mfma_f32_16x16x32_bf16 v[152:155], a[112:115], a[168:171], v[152:155]\n"
"ds_read_b64_tr_b16 a[222:223], v246 offset:25088\n"
"v_xor_b32 v244, s74, v244\n"
"v_mfma_f32_16x16x32_bf16 v[152:155], a[116:119], a[172:175], v[152:155]\n"
"ds_read_b64_tr_b16 a[240:241], v245 offset:17408\n"
"v_mul_f32 v127, s45, v127\n"
"v_mfma_f32_16x16x32_bf16 v[148:151], a[104:107], a[168:171], v[148:151]\n"
"buffer_load_dwordx4 v226, s[28:31], s48 offen offset:2048 lds\n"
"v_mfma_f32_16x16x32_bf16 v[148:151], a[108:111], a[172:175], v[148:151]\n"
"ds_read_b64_tr_b16 a[242:243], v245 offset:17920\n"
"v_cvt_pk_bf16_f32 v120, v120, v121\n"
"v_mfma_f32_16x16x32_bf16 v[144:147], a[96:99], a[168:171], v[144:147]\n"
"ds_read_b64_tr_b16 a[244:245], v245 offset:25600\n"
"v_cvt_pk_bf16_f32 v121, v122, v123\n"
"v_mfma_f32_16x16x32_bf16 v[144:147], a[100:103], a[172:175], v[144:147]\n"
"ds_read_b64_tr_b16 a[246:247], v245 offset:26112\n"
"v_xor_b32 v245, s74, v245\n"
"v_mfma_f32_16x16x32_bf16 v[176:179], a[96:99], a[176:179], v[176:179]\n"
"ds_read_b64_tr_b16 a[248:249], v246 offset:17408\n"
"v_cvt_pk_bf16_f32 v122, v124, v125\n"
"v_mfma_f32_16x16x32_bf16 v[176:179], a[100:103], a[180:183], v[176:179]\n"
"buffer_load_dwordx4 v227, s[28:31], s48 offen offset:3072 lds\n"
"v_mfma_f32_16x16x32_bf16 v[180:183], a[104:107], a[176:179], v[180:183]\n"
"s_xor_b32 s50, s50, s74\n"
"ds_read_b64_tr_b16 a[250:251], v246 offset:17920\n"
"v_cvt_pk_bf16_f32 v123, v126, v127\n"
"v_mfma_f32_16x16x32_bf16 v[180:183], a[108:111], a[180:183], v[180:183]\n"
"ds_read_b64_tr_b16 a[252:253], v246 offset:25600\n"
"v_mul_f32 v128, s45, v128\n"
"v_mfma_f32_16x16x32_bf16 v[184:187], a[112:115], a[176:179], v[184:187]\n"
"ds_read_b64_tr_b16 a[254:255], v246 offset:26112\n"
"v_xor_b32 v246, s74, v246\n"
"v_mfma_f32_16x16x32_bf16 v[184:187], a[116:119], a[180:183], v[184:187]\n"
"v_permlane16_swap_b32 v112, v114\n"
"v_mfma_f32_16x16x32_bf16 v[188:191], a[120:123], a[176:179], v[188:191]\n"
"v_permlane16_swap_b32 v113, v115\n"
"v_mfma_f32_16x16x32_bf16 v[188:191], a[124:127], a[180:183], v[188:191]\n"
"v_permlane16_swap_b32 v120, v122\n"
"v_mfma_f32_16x16x32_bf16 v[200:203], a[144:147], a[176:179], v[200:203]\n"
"v_permlane16_swap_b32 v121, v123\n"
"v_mfma_f32_16x16x32_bf16 v[200:203], a[148:151], a[180:183], v[200:203]\n"
"v_accvgpr_write_b32 a0, v112\n"
"v_accvgpr_write_b32 a1, v113\n"
"v_mfma_f32_16x16x32_bf16 v[204:207], a[152:155], a[176:179], v[204:207]\n"
"v_accvgpr_write_b32 a2, v114\n"
"v_accvgpr_write_b32 a3, v115\n"
"v_mfma_f32_16x16x32_bf16 v[204:207], a[156:159], a[180:183], v[204:207]\n"
"v_accvgpr_write_b32 a4, v120\n"
"v_accvgpr_write_b32 a5, v121\n"
"v_mfma_f32_16x16x32_bf16 v[192:195], a[128:131], a[176:179], v[192:195]\n"
"v_accvgpr_write_b32 a6, v122\n"
"v_accvgpr_write_b32 a7, v123\n"
"v_mfma_f32_16x16x32_bf16 v[192:195], a[132:135], a[180:183], v[192:195]\n"
"v_mul_f32 v129, s45, v129\n"
"v_mul_f32 v130, s45, v130\n"
"v_mfma_f32_16x16x32_bf16 v[196:199], a[136:139], a[176:179], v[196:199]\n"
"v_mul_f32 v131, s45, v131\n"
"v_mul_f32 v132, s45, v132\n"
"v_mfma_f32_16x16x32_bf16 v[196:199], a[140:143], a[180:183], v[196:199]\n"
"v_mul_f32 v133, s45, v133\n"
"v_mul_f32 v134, s45, v134\n"
"v_mfma_f32_16x16x32_bf16 v[108:111], a[232:235], a[184:187], v[108:111]\n"
"s_waitcnt lgkmcnt(0)\n"
"v_mul_f32 v135, s45, v135\n"
"v_cvt_pk_bf16_f32 v128, v128, v129\n"
"v_mfma_f32_16x16x32_bf16 v[108:111], a[236:239], a[188:191], v[108:111]\n"
"v_cvt_pk_bf16_f32 v129, v130, v131\n"
"v_cvt_pk_bf16_f32 v130, v132, v133\n"
"v_mfma_f32_16x16x32_bf16 v[104:107], a[224:227], a[184:187], v[104:107]\n"
"s_nop 0\n"
"v_permlane16_swap_b32 v128, v130\n"
"v_mfma_f32_16x16x32_bf16 v[104:107], a[228:231], a[188:191], v[104:107]\n"
"s_waitcnt vmcnt(8)\n"
"v_cvt_pk_bf16_f32 v131, v134, v135\n"
"v_accvgpr_write_b32 a8, v128\n"
"v_mfma_f32_16x16x32_bf16 v[100:103], a[200:203], a[184:187], v[100:103]\n"
"s_barrier\n"
"v_mfma_f32_16x16x32_bf16 v[100:103], a[204:207], a[188:191], v[100:103]\n"
"s_mov_b32 m0, s51\n"
"ds_read_b64_tr_b16 a[96:97], v243 offset:0\n"
"v_accvgpr_write_b32 a10, v130\n"
"v_mfma_f32_16x16x32_bf16 v[96:99], a[192:195], a[184:187], v[96:99]\n"
"buffer_load_dwordx4 v247, s[24:27], s49 offen lds\n"
"v_mfma_f32_16x16x32_bf16 v[96:99], a[196:199], a[188:191], v[96:99]\n"
"ds_read_b64_tr_b16 a[98:99], v243 offset:512\n"
"v_mul_f32 v136, s45, v136\n"
"v_mfma_f32_16x16x32_bf16 v[0:3], a[192:195], a[160:163], v[0:3]\n"
"ds_read_b64_tr_b16 a[100:101], v243 offset:8192\n"
"v_mul_f32 v137, s45, v137\n"
"v_mfma_f32_16x16x32_bf16 v[0:3], a[196:199], a[164:167], v[0:3]\n"
"ds_read_b64_tr_b16 a[102:103], v243 offset:8704\n"
"v_mul_f32 v138, s45, v138\n"
"v_mfma_f32_16x16x32_bf16 v[4:7], a[200:203], a[160:163], v[4:7]\n"
"ds_read_b64_tr_b16 a[104:105], v244 offset:0\n"
"v_mul_f32 v139, s45, v139\n"
"v_mfma_f32_16x16x32_bf16 v[4:7], a[204:207], a[164:167], v[4:7]\n"
"buffer_load_dwordx4 v253, s[24:27], s49 offen offset:1024 lds\n"
"v_mfma_f32_16x16x32_bf16 v[8:11], a[208:211], a[160:163], v[8:11]\n"
"ds_read_b64_tr_b16 a[106:107], v244 offset:512\n"
"v_mul_f32 v140, s45, v140\n"
"v_mfma_f32_16x16x32_bf16 v[8:11], a[212:215], a[164:167], v[8:11]\n"
"ds_read_b64_tr_b16 a[108:109], v244 offset:8192\n"
"v_mul_f32 v141, s45, v141\n"
"v_mfma_f32_16x16x32_bf16 v[12:15], a[216:219], a[160:163], v[12:15]\n"
"ds_read_b64_tr_b16 a[110:111], v244 offset:8704\n"
"v_mul_f32 v142, s45, v142\n"
"v_mfma_f32_16x16x32_bf16 v[12:15], a[220:223], a[164:167], v[12:15]\n"
"ds_read_b64_tr_b16 a[112:113], v245 offset:0\n"
"v_mul_f32 v143, s45, v143\n"
"v_mfma_f32_16x16x32_bf16 v[16:19], a[224:227], a[160:163], v[16:19]\n"
"buffer_load_dwordx4 v254, s[24:27], s49 offen offset:2048 lds\n"
"v_mfma_f32_16x16x32_bf16 v[16:19], a[228:231], a[164:167], v[16:19]\n"
"ds_read_b64_tr_b16 a[114:115], v245 offset:512\n"
"v_cvt_pk_bf16_f32 v136, v136, v137\n"
"v_mfma_f32_16x16x32_bf16 v[20:23], a[232:235], a[160:163], v[20:23]\n"
"ds_read_b64_tr_b16 a[116:117], v245 offset:8192\n"
"v_cvt_pk_bf16_f32 v137, v138, v139\n"
"v_mfma_f32_16x16x32_bf16 v[20:23], a[236:239], a[164:167], v[20:23]\n"
"ds_read_b64_tr_b16 a[118:119], v245 offset:8704\n"
"v_cvt_pk_bf16_f32 v138, v140, v141\n"
"v_mfma_f32_16x16x32_bf16 v[24:27], a[240:243], a[160:163], v[24:27]\n"
"ds_read_b64_tr_b16 a[120:121], v246 offset:0\n"
"v_cvt_pk_bf16_f32 v139, v142, v143\n"
"v_mfma_f32_16x16x32_bf16 v[24:27], a[244:247], a[164:167], v[24:27]\n"
"buffer_load_dwordx4 v255, s[24:27], s49 offen offset:3072 lds\n"
"v_mfma_f32_16x16x32_bf16 v[28:31], a[248:251], a[160:163], v[28:31]\n"
"ds_read_b128 a[160:163], v249 offset:0\n"
"v_mul_f32 v168, s45, v168\n"
"v_mfma_f32_16x16x32_bf16 v[28:31], a[252:255], a[164:167], v[28:31]\n"
"ds_read_b128 a[164:167], v250 offset:0\n"
"v_mul_f32 v169, s45, v169\n"
"v_mfma_f32_16x16x32_bf16 v[60:63], a[248:251], a[168:171], v[60:63]\n"
"s_add_u32 s40, s49, 40960\n"
"ds_read_b64_tr_b16 a[122:123], v246 offset:512\n"
"v_mul_f32 v170, s45, v170\n"
"v_mfma_f32_16x16x32_bf16 v[60:63], a[252:255], a[172:175], v[60:63]\n"
"s_add_u32 m0, s51, 4096\n"
"ds_read_b64_tr_b16 a[124:125], v246 offset:8192\n"
"v_mul_f32 v171, s45, v171\n"
"v_mfma_f32_16x16x32_bf16 v[56:59], a[240:243], a[168:171], v[56:59]\n"
"buffer_load_dwordx4 v247, s[24:27], s40 offen lds\n"
"v_mfma_f32_16x16x32_bf16 v[56:59], a[244:247], a[172:175], v[56:59]\n"
"ds_read_b64_tr_b16 a[126:127], v246 offset:8704\n"
"v_mul_f32 v172, s45, v172\n"
"v_mfma_f32_16x16x32_bf16 v[52:55], a[232:235], a[168:171], v[52:55]\n"
"ds_read_b64_tr_b16 a[128:129], v243 offset:1024\n"
"v_mul_f32 v173, s45, v173\n"
"v_mfma_f32_16x16x32_bf16 v[52:55], a[236:239], a[172:175], v[52:55]\n"
"ds_read_b64_tr_b16 a[130:131], v243 offset:1536\n"
"v_mul_f32 v174, s45, v174\n"
"v_mfma_f32_16x16x32_bf16 v[48:51], a[224:227], a[168:171], v[48:51]\n"
"ds_read_b64_tr_b16 a[132:133], v243 offset:9216\n"
"v_mul_f32 v175, s45, v175\n"
"v_mfma_f32_16x16x32_bf16 v[48:51], a[228:231], a[172:175], v[48:51]\n"
"buffer_load_dwordx4 v253, s[24:27], s40 offen offset:1024 lds\n"
"v_mfma_f32_16x16x32_bf16 v[44:47], a[216:219], a[168:171], v[44:47]\n"
"ds_read_b64_tr_b16 a[134:135], v243 offset:9728\n"
"v_cvt_pk_bf16_f32 v168, v168, v169\n"
"v_mfma_f32_16x16x32_bf16 v[44:47], a[220:223], a[172:175], v[44:47]\n"
"ds_read_b64_tr_b16 a[136:137], v244 offset:1024\n"
"v_cvt_pk_bf16_f32 v169, v170, v171\n"
"v_mfma_f32_16x16x32_bf16 v[40:43], a[208:211], a[168:171], v[40:43]\n"
"ds_read_b64_tr_b16 a[138:139], v244 offset:1536\n"
"v_cvt_pk_bf16_f32 v170, v172, v173\n"
"v_mfma_f32_16x16x32_bf16 v[40:43], a[212:215], a[172:175], v[40:43]\n"
"ds_read_b64_tr_b16 a[140:141], v244 offset:9216\n"
"v_cvt_pk_bf16_f32 v171, v174, v175\n"
"v_mfma_f32_16x16x32_bf16 v[36:39], a[200:203], a[168:171], v[36:39]\n"
"buffer_load_dwordx4 v254, s[24:27], s40 offen offset:2048 lds\n"
"v_mfma_f32_16x16x32_bf16 v[36:39], a[204:207], a[172:175], v[36:39]\n"
"s_add_u32 s49, s49, 128\n"
"ds_read_b64_tr_b16 a[142:143], v244 offset:9728\n"
"v_mul_f32 v160, s45, v160\n"
"v_mfma_f32_16x16x32_bf16 v[32:35], a[192:195], a[168:171], v[32:35]\n"
"s_sub_u32 s41, s49, 1280\n"
"ds_read_b64_tr_b16 a[144:145], v245 offset:1024\n"
"v_mul_f32 v161, s45, v161\n"
"v_mfma_f32_16x16x32_bf16 v[32:35], a[196:199], a[172:175], v[32:35]\n"
"s_min_u32 s49, s49, s41\n"
"ds_read_b64_tr_b16 a[146:147], v245 offset:1536\n"
"v_mul_f32 v162, s45, v162\n"
"v_mfma_f32_16x16x32_bf16 v[64:67], a[192:195], a[176:179], v[64:67]\n"
"s_xor_b32 s51, s51, s74\n"
"ds_read_b64_tr_b16 a[148:149], v245 offset:9216\n"
"v_mul_f32 v163, s45, v163\n"
"v_mfma_f32_16x16x32_bf16 v[64:67], a[196:199], a[180:183], v[64:67]\n"
"ds_read_b64_tr_b16 a[150:151], v245 offset:9728\n"
"v_mul_f32 v164, s45, v164\n"
"v_mfma_f32_16x16x32_bf16 v[68:71], a[200:203], a[176:179], v[68:71]\n"
"ds_read_b64_tr_b16 a[152:153], v246 offset:1024\n"
"v_mul_f32 v165, s45, v165\n"
"v_mfma_f32_16x16x32_bf16 v[68:71], a[204:207], a[180:183], v[68:71]\n"
"ds_read_b64_tr_b16 a[154:155], v246 offset:1536\n"
"v_mul_f32 v166, s45, v166\n"
"v_mfma_f32_16x16x32_bf16 v[72:75], a[208:211], a[176:179], v[72:75]\n"
"ds_read_b64_tr_b16 a[156:157], v246 offset:9216\n"
"v_mul_f32 v167, s45, v167\n"
"v_mfma_f32_16x16x32_bf16 v[72:75], a[212:215], a[180:183], v[72:75]\n"
"ds_read_b64_tr_b16 a[158:159], v246 offset:9728\n"
"v_cvt_pk_bf16_f32 v160, v160, v161\n"
"v_mfma_f32_16x16x32_bf16 v[76:79], a[216:219], a[176:179], v[76:79]\n"
"ds_read_b128 a[168:171], v249 offset:2048\n"
"v_cvt_pk_bf16_f32 v161, v162, v163\n"
"v_mfma_f32_16x16x32_bf16 v[76:79], a[220:223], a[180:183], v[76:79]\n"
"ds_read_b128 a[172:175], v250 offset:2048\n"
"v_cvt_pk_bf16_f32 v162, v164, v165\n"
"v_mfma_f32_16x16x32_bf16 v[88:91], a[240:243], a[176:179], v[88:91]\n"
"v_permlane16_swap_b32 v129, v131\n"
"v_mfma_f32_16x16x32_bf16 v[88:91], a[244:247], a[180:183], v[88:91]\n"
"v_permlane16_swap_b32 v136, v138\n"
"v_mfma_f32_16x16x32_bf16 v[92:95], a[248:251], a[176:179], v[92:95]\n"
"v_permlane16_swap_b32 v137, v139\n"
"v_mfma_f32_16x16x32_bf16 v[92:95], a[252:255], a[180:183], v[92:95]\n"
"v_permlane16_swap_b32 v168, v170\n"
"v_mfma_f32_16x16x32_bf16 v[80:83], a[224:227], a[176:179], v[80:83]\n"
"v_permlane16_swap_b32 v169, v171\n"
"v_mfma_f32_16x16x32_bf16 v[80:83], a[228:231], a[180:183], v[80:83]\n"
"v_permlane16_swap_b32 v160, v162\n"
"v_mfma_f32_16x16x32_bf16 v[84:87], a[232:235], a[176:179], v[84:87]\n"
"s_add_u32 s47, s47, 1\n"
"ds_read_b128 a[176:179], v249 offset:4096\n"
"v_accvgpr_write_b32 a9, v129\n"
"v_mfma_f32_16x16x32_bf16 v[84:87], a[236:239], a[180:183], v[84:87]\n"
"ds_read_b128 a[180:183], v250 offset:4096\n"
"v_accvgpr_write_b32 a11, v131\n"
"s_waitcnt lgkmcnt(2)\n"
"v_mfma_f32_16x16x32_bf16 v[112:115], a[96:99], a[160:163], 0\n"
"v_accvgpr_write_b32 a12, v136\n"
"v_accvgpr_write_b32 a13, v137\n"
"v_mfma_f32_16x16x32_bf16 v[112:115], a[100:103], a[164:167], v[112:115]\n"
"v_accvgpr_write_b32 a14, v138\n"
"v_accvgpr_write_b32 a15, v139\n"
"v_mfma_f32_16x16x32_bf16 v[116:119], a[104:107], a[160:163], 0\n"
"v_accvgpr_write_b32 a28, v168\n"
"v_accvgpr_write_b32 a29, v169\n"
"v_mfma_f32_16x16x32_bf16 v[116:119], a[108:111], a[164:167], v[116:119]\n"
"s_waitcnt vmcnt(11)\n"
"v_accvgpr_write_b32 a30, v170\n"
"v_accvgpr_write_b32 a31, v171\n"
"v_mfma_f32_16x16x32_bf16 v[120:123], a[112:115], a[160:163], 0\n"
"s_barrier\n"
"v_mfma_f32_16x16x32_bf16 v[120:123], a[116:119], a[164:167], v[120:123]\n"
"s_add_u32 s40, s48, 256\n"
"s_xor_b32 m0, s50, 0xc000\n"
"ds_read_b64_tr_b16 a[192:193], v243 offset:16384\n"
"v_mfma_f32_16x16x32_bf16 v[124:127], a[120:123], a[160:163], 0\n"
"buffer_load_dwordx4 v224, s[28:31], s40 offen lds\n"
"v_mfma_f32_16x16x32_bf16 v[124:127], a[124:127], a[164:167], v[124:127]\n"
"ds_read_b64_tr_b16 a[194:195], v243 offset:16896\n"
"v_cvt_pk_bf16_f32 v163, v166, v167\n"
"v_mfma_f32_16x16x32_bf16 v[128:131], a[128:131], a[160:163], 0\n"
"ds_read_b64_tr_b16 a[196:197], v243 offset:24576\n"
"v_accvgpr_write_b32 a24, v160\n"
"v_mfma_f32_16x16x32_bf16 v[128:131], a[132:135], a[164:167], v[128:131]\n"
"ds_read_b64_tr_b16 a[198:199], v243 offset:25088\n"
"v_accvgpr_write_b32 a26, v162\n"
"v_mfma_f32_16x16x32_bf16 v[132:135], a[136:139], a[160:163], 0\n"
"ds_read_b64_tr_b16 a[200:201], v244 offset:16384\n"
"v_mul_f32 v152, s45, v152\n"
"v_mfma_f32_16x16x32_bf16 v[132:135], a[140:143], a[164:167], v[132:135]\n"
"buffer_load_dwordx4 v225, s[28:31], s40 offen offset:1024 lds\n"
"v_mfma_f32_16x16x32_bf16 v[136:139], a[144:147], a[160:163], 0\n"
"ds_read_b64_tr_b16 a[202:203], v244 offset:16896\n"
"v_mul_f32 v153, s45, v153\n"
"v_mfma_f32_16x16x32_bf16 v[136:139], a[148:151], a[164:167], v[136:139]\n"
"ds_read_b64_tr_b16 a[204:205], v244 offset:24576\n"
"v_mul_f32 v154, s45, v154\n"
"v_mfma_f32_16x16x32_bf16 v[140:143], a[152:155], a[160:163], 0\n"
"ds_read_b64_tr_b16 a[206:207], v244 offset:25088\n"
"v_mul_f32 v155, s45, v155\n"
"v_mfma_f32_16x16x32_bf16 v[140:143], a[156:159], a[164:167], v[140:143]\n"
"ds_read_b64_tr_b16 a[208:209], v245 offset:16384\n"
"v_mul_f32 v156, s45, v156\n"
"v_mfma_f32_16x16x32_bf16 v[172:175], a[152:155], a[168:171], 0\n"
"buffer_load_dwordx4 v226, s[28:31], s40 offen offset:2048 lds\n"
"v_mfma_f32_16x16x32_bf16 v[172:175], a[156:159], a[172:175], v[172:175]\n"
"ds_read_b64_tr_b16 a[210:211], v245 offset:16896\n"
"v_mul_f32 v157, s45, v157\n"
"v_mfma_f32_16x16x32_bf16 v[168:171], a[144:147], a[168:171], 0\n"
"ds_read_b64_tr_b16 a[212:213], v245 offset:24576\n"
"v_mul_f32 v158, s45, v158\n"
"v_mfma_f32_16x16x32_bf16 v[168:171], a[148:151], a[172:175], v[168:171]\n"
"ds_read_b64_tr_b16 a[214:215], v245 offset:25088\n"
"v_mul_f32 v159, s45, v159\n"
"v_mfma_f32_16x16x32_bf16 v[164:167], a[136:139], a[168:171], 0\n"
"v_permlane16_swap_b32 v161, v163\n"
"v_mfma_f32_16x16x32_bf16 v[164:167], a[140:143], a[172:175], v[164:167]\n"
"buffer_load_dwordx4 v227, s[28:31], s40 offen offset:3072 lds\n"
"v_accvgpr_write_b32 a25, v161\n"
"v_accvgpr_write_b32 a27, v163\n"
"v_mfma_f32_16x16x32_bf16 v[160:163], a[128:131], a[168:171], 0\n"
"s_cmp_eq_u32 s47, 8\ns_cselect_b32 s41, 524800, s58\n"
"ds_read_b64_tr_b16 a[216:217], v246 offset:16384\n"
"v_cvt_pk_bf16_f32 v152, v152, v153\n"
"v_mfma_f32_16x16x32_bf16 v[160:163], a[132:135], a[172:175], v[160:163]\n"
"s_add_u32 s48, s48, s41\n"
"ds_read_b64_tr_b16 a[218:219], v246 offset:16896\n"
"v_cvt_pk_bf16_f32 v153, v154, v155\n"
"v_cvt_pk_bf16_f32 v154, v156, v157\n"
"v_cvt_pk_bf16_f32 v155, v158, v159\n"
"v_mfma_f32_16x16x32_bf16 v[156:159], a[120:123], a[168:171], 0\n"
"s_sub_u32 s41, s48, 5242880\n"
"v_permlane16_swap_b32 v152, v154\n"
"v_mfma_f32_16x16x32_bf16 v[156:159], a[124:127], a[172:175], v[156:159]\n"
"s_min_u32 s48, s48, s41\n"
"v_permlane16_swap_b32 v153, v155\n"
"v_accvgpr_write_b32 a20, v152\n"
"v_accvgpr_write_b32 a21, v153\n"
"v_accvgpr_write_b32 a22, v154\n"
"v_accvgpr_write_b32 a23, v155\n"
"v_mfma_f32_16x16x32_bf16 v[152:155], a[112:115], a[168:171], 0\n"
"s_mov_b32 m0, s50\n"
"ds_read_b64_tr_b16 a[220:221], v246 offset:24576\n"
"v_mul_f32 v144, s45, v144\n"
"v_mfma_f32_16x16x32_bf16 v[152:155], a[116:119], a[172:175], v[152:155]\n"
"buffer_load_dwordx4 v224, s[28:31], s48 offen lds\n"
"v_mul_f32 v145, s45, v145\n"
"v_mul_f32 v146, s45, v146\n"
"v_mul_f32 v147, s45, v147\n"
"v_mul_f32 v148, s45, v148\n"
"v_mul_f32 v149, s45, v149\n"
"v_mul_f32 v150, s45, v150\n"
"v_mul_f32 v151, s45, v151\n"
"v_cvt_pk_bf16_f32 v144, v144, v145\n"
"v_cvt_pk_bf16_f32 v145, v146, v147\n"
"v_cvt_pk_bf16_f32 v146, v148, v149\n"
"v_cvt_pk_bf16_f32 v147, v150, v151\n"
"v_mfma_f32_16x16x32_bf16 v[148:151], a[104:107], a[168:171], 0\n"
"v_permlane16_swap_b32 v144, v146\n"
"v_mfma_f32_16x16x32_bf16 v[148:151], a[108:111], a[172:175], v[148:151]\n"
"v_permlane16_swap_b32 v145, v147\n"
"v_accvgpr_write_b32 a16, v144\n"
"v_accvgpr_write_b32 a17, v145\n"
"v_accvgpr_write_b32 a18, v146\n"
"v_accvgpr_write_b32 a19, v147\n"
"v_mfma_f32_16x16x32_bf16 v[144:147], a[96:99], a[168:171], 0\n"
"ds_read_b64_tr_b16 a[222:223], v246 offset:25088\n"
"v_mul_f32 v176, s45, v176\n"
"v_mul_f32 v177, s45, v177\n"
"v_mul_f32 v178, s45, v178\n"
"v_mul_f32 v179, s45, v179\n"
"v_mul_f32 v180, s45, v180\n"
"v_mul_f32 v181, s45, v181\n"
"v_mul_f32 v182, s45, v182\n"
"v_mul_f32 v183, s45, v183\n"
"v_cvt_pk_bf16_f32 v176, v176, v177\n"
"v_cvt_pk_bf16_f32 v177, v178, v179\n"
"v_cvt_pk_bf16_f32 v178, v180, v181\n"
"v_cvt_pk_bf16_f32 v179, v182, v183\n"
"v_mfma_f32_16x16x32_bf16 v[144:147], a[100:103], a[172:175], v[144:147]\n"
"s_waitcnt lgkmcnt(2)\n"
"buffer_load_dwordx4 v225, s[28:31], s48 offen offset:1024 lds\n"
"v_permlane16_swap_b32 v176, v178\n"
"v_permlane16_swap_b32 v177, v179\n"
"v_accvgpr_write_b32 a32, v176\n"
"v_accvgpr_write_b32 a33, v177\n"
"v_accvgpr_write_b32 a34, v178\n"
"v_accvgpr_write_b32 a35, v179\n"
"v_mfma_f32_16x16x32_bf16 v[176:179], a[96:99], a[176:179], 0\n"
"ds_read_b64_tr_b16 a[224:225], v243 offset:17408\n"
"v_xor_b32 v251, s74, v251\n"
"v_mfma_f32_16x16x32_bf16 v[176:179], a[100:103], a[180:183], v[176:179]\n"
"ds_read_b128 a[184:187], v251 offset:0\n"
"v_mul_f32 v216, s45, v216\n"
"v_mul_f32 v217, s45, v217\n"
"v_mul_f32 v218, s45, v218\n"
"v_mul_f32 v219, s45, v219\n"
"v_mul_f32 v220, s45, v220\n"
"v_mul_f32 v221, s45, v221\n"
"v_mul_f32 v222, s45, v222\n"
"v_mul_f32 v223, s45, v223\n"
"v_cvt_pk_bf16_f32 v216, v216, v217\n"
"v_cvt_pk_bf16_f32 v217, v218, v219\n"
"v_cvt_pk_bf16_f32 v218, v220, v221\n"
"v_cvt_pk_bf16_f32 v219, v222, v223\n"
"v_xor_b32 v252, s74, v252\n"
"v_mfma_f32_16x16x32_bf16 v[180:183], a[104:107], a[176:179], 0\n"
"ds_read_b128 a[188:191], v252 offset:0\n"
"v_permlane16_swap_b32 v216, v218\n"
"v_permlane16_swap_b32 v217, v219\n"
"v_mul_f32 v184, s45, v184\n"
"v_mul_f32 v185, s45, v185\n"
"v_mul_f32 v186, s45, v186\n"
"v_mul_f32 v187, s45, v187\n"
"v_mul_f32 v188, s45, v188\n"
"v_mul_f32 v189, s45, v189\n"
"v_mul_f32 v190, s45, v190\n"
"v_mul_f32 v191, s45, v191\n"
"v_cvt_pk_bf16_f32 v184, v184, v185\n"
"v_cvt_pk_bf16_f32 v185, v186, v187\n"
"v_cvt_pk_bf16_f32 v186, v188, v189\n"
"v_cvt_pk_bf16_f32 v187, v190, v191\n"
"v_mfma_f32_16x16x32_bf16 v[180:183], a[108:111], a[180:183], v[180:183]\n"
"ds_write_b128 v248, v[216:219] offset:2048\n"
"v_permlane16_swap_b32 v184, v186\n"
"v_permlane16_swap_b32 v185, v187\n"
"v_accvgpr_write_b32 a36, v184\n"
"v_accvgpr_write_b32 a37, v185\n"
"v_accvgpr_write_b32 a38, v186\n"
"v_accvgpr_write_b32 a39, v187\n"
"v_mfma_f32_16x16x32_bf16 v[184:187], a[112:115], a[176:179], 0\n"
"buffer_load_dwordx4 v226, s[28:31], s48 offen offset:2048 lds\n"
"v_mfma_f32_16x16x32_bf16 v[184:187], a[116:119], a[180:183], v[184:187]\n"
"ds_read_b64_tr_b16 a[226:227], v243 offset:17920\n"
"v_mul_f32 v208, s45, v208\n"
"v_mfma_f32_16x16x32_bf16 v[188:191], a[120:123], a[176:179], 0\n"
"ds_read_b64_tr_b16 a[228:229], v243 offset:25600\n"
"v_mul_f32 v209, s45, v209\n"
"v_mul_f32 v210, s45, v210\n"
"v_mul_f32 v211, s45, v211\n"
"v_mul_f32 v212, s45, v212\n"
"v_mul_f32 v213, s45, v213\n"
"v_mul_f32 v214, s45, v214\n"
"v_mul_f32 v215, s45, v215\n"
"v_cvt_pk_bf16_f32 v208, v208, v209\n"
"v_cvt_pk_bf16_f32 v209, v210, v211\n"
"v_cvt_pk_bf16_f32 v210, v212, v213\n"
"v_cvt_pk_bf16_f32 v211, v214, v215\n"
"v_mul_f32 v200, s45, v200\n"
"v_mul_f32 v201, s45, v201\n"
"v_mul_f32 v202, s45, v202\n"
"v_mul_f32 v203, s45, v203\n"
"v_mul_f32 v204, s45, v204\n"
"v_mul_f32 v205, s45, v205\n"
"v_mul_f32 v206, s45, v206\n"
"v_mul_f32 v207, s45, v207\n"
"v_cvt_pk_bf16_f32 v200, v200, v201\n"
"v_cvt_pk_bf16_f32 v201, v202, v203\n"
"v_cvt_pk_bf16_f32 v202, v204, v205\n"
"v_cvt_pk_bf16_f32 v203, v206, v207\n"
"v_mfma_f32_16x16x32_bf16 v[188:191], a[124:127], a[180:183], v[188:191]\n"
"v_permlane16_swap_b32 v208, v210\n"
"v_permlane16_swap_b32 v209, v211\n"
"v_permlane16_swap_b32 v200, v202\n"
"v_permlane16_swap_b32 v201, v203\n"
"v_accvgpr_write_b32 a44, v200\n"
"v_accvgpr_write_b32 a45, v201\n"
"v_accvgpr_write_b32 a46, v202\n"
"v_accvgpr_write_b32 a47, v203\n"
"v_mfma_f32_16x16x32_bf16 v[200:203], a[144:147], a[176:179], 0\n"
"ds_write_b128 v248, v[208:211] offset:0\n"
"v_mfma_f32_16x16x32_bf16 v[200:203], a[148:151], a[180:183], v[200:203]\n"
"buffer_load_dwordx4 v227, s[28:31], s48 offen offset:3072 lds\n"
"v_mfma_f32_16x16x32_bf16 v[204:207], a[152:155], a[176:179], 0\n"
"s_xor_b32 s50, s50, s74\n"
"ds_read_b64_tr_b16 a[230:231], v243 offset:26112\n"
"v_mul_f32 v192, s45, v192\n"
"v_mul_f32 v193, s45, v193\n"
"v_mul_f32 v194, s45, v194\n"
"v_mul_f32 v195, s45, v195\n"
"v_mul_f32 v196, s45, v196\n"
"v_mul_f32 v197, s45, v197\n"
"v_mul_f32 v198, s45, v198\n"
"v_mul_f32 v199, s45, v199\n"
"v_cvt_pk_bf16_f32 v192, v192, v193\n"
"v_cvt_pk_bf16_f32 v193, v194, v195\n"
"v_cvt_pk_bf16_f32 v194, v196, v197\n"
"v_cvt_pk_bf16_f32 v195, v198, v199\n"
"v_mfma_f32_16x16x32_bf16 v[204:207], a[156:159], a[180:183], v[204:207]\n"
"v_permlane16_swap_b32 v192, v194\n"
"v_permlane16_swap_b32 v193, v195\n"
"v_accvgpr_write_b32 a40, v192\n"
"v_accvgpr_write_b32 a41, v193\n"
"v_accvgpr_write_b32 a42, v194\n"
"v_accvgpr_write_b32 a43, v195\n"
"v_mfma_f32_16x16x32_bf16 v[192:195], a[128:131], a[176:179], 0\n"
"ds_read_b64_tr_b16 a[232:233], v244 offset:17408\n"
"v_mul_f32 v0, s45, v0\n"
"v_mfma_f32_16x16x32_bf16 v[192:195], a[132:135], a[180:183], v[192:195]\n"
"ds_read_b64_tr_b16 a[234:235], v244 offset:17920\n"
"v_mul_f32 v1, s45, v1\n"
"v_mfma_f32_16x16x32_bf16 v[196:199], a[136:139], a[176:179], 0\n"
"ds_read_b64_tr_b16 a[236:237], v244 offset:25600\n"
"v_mul_f32 v2, s45, v2\n"
"v_mfma_f32_16x16x32_bf16 v[196:199], a[140:143], a[180:183], v[196:199]\n"
"s_waitcnt lgkmcnt(5)\n"
"ds_read_b64_tr_b16 a[238:239], v244 offset:26112\n"
"v_mul_f32 v3, s45, v3\n"
"v_mfma_f32_16x16x32_bf16 v[220:223], a[136:139], a[184:187], 0\n"
"ds_read_b64_tr_b16 a[240:241], v245 offset:17408\n"
"v_mul_f32 v4, s45, v4\n"
"v_mfma_f32_16x16x32_bf16 v[220:223], a[140:143], a[188:191], v[220:223]\n"
"ds_read_b64_tr_b16 a[242:243], v245 offset:17920\n"
"v_mul_f32 v5, s45, v5\n"
"v_mfma_f32_16x16x32_bf16 v[216:219], a[128:131], a[184:187], 0\n"
"ds_read_b64_tr_b16 a[244:245], v245 offset:25600\n"
"v_mul_f32 v6, s45, v6\n"
"v_mfma_f32_16x16x32_bf16 v[216:219], a[132:135], a[188:191], v[216:219]\n"
"ds_read_b64_tr_b16 a[246:247], v245 offset:26112\n"
"v_mul_f32 v7, s45, v7\n"
"v_mfma_f32_16x16x32_bf16 v[212:215], a[104:107], a[184:187], 0\n"
"ds_read_b64_tr_b16 a[248:249], v246 offset:17408\n"
"v_cvt_pk_bf16_f32 v0, v0, v1\n"
"v_mfma_f32_16x16x32_bf16 v[212:215], a[108:111], a[188:191], v[212:215]\n"
"ds_read_b64_tr_b16 a[250:251], v246 offset:17920\n"
"v_cvt_pk_bf16_f32 v1, v2, v3\n"
"v_mfma_f32_16x16x32_bf16 v[208:211], a[96:99], a[184:187], 0\n"
"ds_read_b64_tr_b16 a[252:253], v246 offset:25600\n"
"ds_read_b64_tr_b16 a[254:255], v246 offset:26112\n"
"v_cvt_pk_bf16_f32 v2, v4, v5\n"
"v_cvt_pk_bf16_f32 v3, v6, v7\n"
"v_mfma_f32_16x16x32_bf16 v[208:211], a[100:103], a[188:191], v[208:211]\n"
"v_permlane16_swap_b32 v0, v2\n"
"v_permlane16_swap_b32 v1, v3\n"
"v_accvgpr_write_b32 a48, v0\n"
"v_accvgpr_write_b32 a49, v1\n"
"v_accvgpr_write_b32 a50, v2\n"
"v_accvgpr_write_b32 a51, v3\n"
"v_mfma_f32_16x16x32_bf16 v[0:3], a[192:195], a[160:163], 0\n"
"v_mul_f32 v8, s45, v8\n"
"v_mul_f32 v9, s45, v9\n"
"v_mfma_f32_16x16x32_bf16 v[0:3], a[196:199], a[164:167], v[0:3]\n"
"v_mul_f32 v10, s45, v10\n"
"v_mul_f32 v11, s45, v11\n"
"v_mfma_f32_16x16x32_bf16 v[4:7], a[200:203], a[160:163], 0\n"
"v_mul_f32 v12, s45, v12\n"
"v_mul_f32 v13, s45, v13\n"
"v_mul_f32 v14, s45, v14\n"
"v_mul_f32 v15, s45, v15\n"
"v_cvt_pk_bf16_f32 v8, v8, v9\n"
"v_cvt_pk_bf16_f32 v9, v10, v11\n"
"v_cvt_pk_bf16_f32 v10, v12, v13\n"
"v_cvt_pk_bf16_f32 v11, v14, v15\n"
"v_mfma_f32_16x16x32_bf16 v[4:7], a[204:207], a[164:167], v[4:7]\n"
"s_waitcnt vmcnt(8) lgkmcnt(0)\n"
"v_permlane16_swap_b32 v8, v10\n"
"v_permlane16_swap_b32 v9, v11\n"
"v_accvgpr_write_b32 a52, v8\n"
"v_accvgpr_write_b32 a53, v9\n"
"v_accvgpr_write_b32 a54, v10\n"
"v_accvgpr_write_b32 a55, v11\n"
"v_mfma_f32_16x16x32_bf16 v[8:11], a[208:211], a[160:163], 0\n"
"s_barrier\n"
"v_mfma_f32_16x16x32_bf16 v[8:11], a[212:215], a[164:167], v[8:11]\n"
"s_mov_b32 m0, s51\n"
"v_mul_f32 v16, s45, v16\n"
"v_mul_f32 v17, s45, v17\n"
"v_mfma_f32_16x16x32_bf16 v[12:15], a[216:219], a[160:163], 0\n"
"buffer_load_dwordx4 v247, s[24:27], s49 offen lds\n"
"v_mul_f32 v18, s45, v18\n"
"v_mul_f32 v19, s45, v19\n"
"v_mul_f32 v20, s45, v20\n"
"v_mul_f32 v21, s45, v21\n"
"v_mul_f32 v22, s45, v22\n"
"v_mul_f32 v23, s45, v23\n"
"v_cvt_pk_bf16_f32 v16, v16, v17\n"
"v_cvt_pk_bf16_f32 v17, v18, v19\n"
"v_cvt_pk_bf16_f32 v18, v20, v21\n"
"v_cvt_pk_bf16_f32 v19, v22, v23\n"
"v_mfma_f32_16x16x32_bf16 v[12:15], a[220:223], a[164:167], v[12:15]\n"
"v_permlane16_swap_b32 v16, v18\n"
"v_permlane16_swap_b32 v17, v19\n"
"v_accvgpr_write_b32 a56, v16\n"
"v_accvgpr_write_b32 a57, v17\n"
"v_accvgpr_write_b32 a58, v18\n"
"v_accvgpr_write_b32 a59, v19\n"
"v_mfma_f32_16x16x32_bf16 v[16:19], a[224:227], a[160:163], 0\n"
"v_mul_f32 v24, s45, v24\n"
"v_mul_f32 v25, s45, v25\n"
"v_mfma_f32_16x16x32_bf16 v[16:19], a[228:231], a[164:167], v[16:19]\n"
"v_mul_f32 v26, s45, v26\n"
"v_mul_f32 v27, s45, v27\n"
"v_mfma_f32_16x16x32_bf16 v[20:23], a[232:235], a[160:163], 0\n"
"v_mul_f32 v28, s45, v28\n"
"v_mul_f32 v29, s45, v29\n"
"v_mul_f32 v30, s45, v30\n"
"v_mul_f32 v31, s45, v31\n"
"v_cvt_pk_bf16_f32 v24, v24, v25\n"
"v_cvt_pk_bf16_f32 v25, v26, v27\n"
"v_cvt_pk_bf16_f32 v26, v28, v29\n"
"v_cvt_pk_bf16_f32 v27, v30, v31\n"
"v_mfma_f32_16x16x32_bf16 v[20:23], a[236:239], a[164:167], v[20:23]\n"
"buffer_load_dwordx4 v253, s[24:27], s49 offen offset:1024 lds\n"
"v_permlane16_swap_b32 v24, v26\n"
"v_permlane16_swap_b32 v25, v27\n"
"v_accvgpr_write_b32 a60, v24\n"
"v_accvgpr_write_b32 a61, v25\n"
"v_accvgpr_write_b32 a62, v26\n"
"v_accvgpr_write_b32 a63, v27\n"
"v_mfma_f32_16x16x32_bf16 v[24:27], a[240:243], a[160:163], 0\n"
"v_mul_f32 v56, s45, v56\n"
"v_mul_f32 v57, s45, v57\n"
"v_mfma_f32_16x16x32_bf16 v[24:27], a[244:247], a[164:167], v[24:27]\n"
"v_mul_f32 v58, s45, v58\n"
"v_mul_f32 v59, s45, v59\n"
"v_mfma_f32_16x16x32_bf16 v[28:31], a[248:251], a[160:163], 0\n"
"v_mul_f32 v60, s45, v60\n"
"v_mul_f32 v61, s45, v61\n"
"v_mfma_f32_16x16x32_bf16 v[28:31], a[252:255], a[164:167], v[28:31]\n"
"v_mul_f32 v62, s45, v62\n"
"v_mul_f32 v63, s45, v63\n"
"v_cvt_pk_bf16_f32 v56, v56, v57\n"
"v_cvt_pk_bf16_f32 v57, v58, v59\n"
"v_cvt_pk_bf16_f32 v58, v60, v61\n"
"v_cvt_pk_bf16_f32 v59, v62, v63\n"
"v_mfma_f32_16x16x32_bf16 v[60:63], a[248:251], a[168:171], 0\n"
"buffer_load_dwordx4 v254, s[24:27], s49 offen offset:2048 lds\n"
"v_mfma_f32_16x16x32_bf16 v[60:63], a[252:255], a[172:175], v[60:63]\n"
"v_permlane16_swap_b32 v56, v58\n"
"v_permlane16_swap_b32 v57, v59\n"
"v_accvgpr_write_b32 a76, v56\n"
"v_accvgpr_write_b32 a77, v57\n"
"v_accvgpr_write_b32 a78, v58\n"
"v_accvgpr_write_b32 a79, v59\n"
"v_mfma_f32_16x16x32_bf16 v[56:59], a[240:243], a[168:171], 0\n"
"v_mul_f32 v48, s45, v48\n"
"v_mul_f32 v49, s45, v49\n"
"v_mfma_f32_16x16x32_bf16 v[56:59], a[244:247], a[172:175], v[56:59]\n"
"v_mul_f32 v50, s45, v50\n"
"v_mul_f32 v51, s45, v51\n"
"v_mul_f32 v52, s45, v52\n"
"v_mul_f32 v53, s45, v53\n"
"v_mul_f32 v54, s45, v54\n"
"v_mul_f32 v55, s45, v55\n"
"v_cvt_pk_bf16_f32 v48, v48, v49\n"
"v_cvt_pk_bf16_f32 v49, v50, v51\n"
"v_cvt_pk_bf16_f32 v50, v52, v53\n"
"v_cvt_pk_bf16_f32 v51, v54, v55\n"
"v_mfma_f32_16x16x32_bf16 v[52:55], a[232:235], a[168:171], 0\n"
"v_permlane16_swap_b32 v48, v50\n"
"v_mfma_f32_16x16x32_bf16 v[52:55], a[236:239], a[172:175], v[52:55]\n"
"buffer_load_dwordx4 v255, s[24:27], s49 offen offset:3072 lds\n"
"v_permlane16_swap_b32 v49, v51\n"
"v_accvgpr_write_b32 a72, v48\n"
"v_accvgpr_write_b32 a73, v49\n"
"v_accvgpr_write_b32 a74, v50\n"
"v_accvgpr_write_b32 a75, v51\n"
"v_mfma_f32_16x16x32_bf16 v[48:51], a[224:227], a[168:171], 0\n"
"v_mul_f32 v40, s45, v40\n"
"v_mul_f32 v41, s45, v41\n"
"v_mfma_f32_16x16x32_bf16 v[48:51], a[228:231], a[172:175], v[48:51]\n"
"v_mul_f32 v42, s45, v42\n"
"v_mul_f32 v43, s45, v43\n"
"v_mul_f32 v44, s45, v44\n"
"v_mul_f32 v45, s45, v45\n"
"v_mul_f32 v46, s45, v46\n"
"v_mul_f32 v47, s45, v47\n"
"v_cvt_pk_bf16_f32 v40, v40, v41\n"
"v_cvt_pk_bf16_f32 v41, v42, v43\n"
"v_cvt_pk_bf16_f32 v42, v44, v45\n"
"v_cvt_pk_bf16_f32 v43, v46, v47\n"
"v_mfma_f32_16x16x32_bf16 v[44:47], a[216:219], a[168:171], 0\n"
"s_add_u32 s40, s49, 40960\n"
"v_permlane16_swap_b32 v40, v42\n"
"v_mfma_f32_16x16x32_bf16 v[44:47], a[220:223], a[172:175], v[44:47]\n"
"s_add_u32 m0, s51, 4096\n"
"v_permlane16_swap_b32 v41, v43\n"
"v_accvgpr_write_b32 a68, v40\n"
"v_accvgpr_write_b32 a69, v41\n"
"v_accvgpr_write_b32 a70, v42\n"
"v_accvgpr_write_b32 a71, v43\n"
"v_mfma_f32_16x16x32_bf16 v[40:43], a[208:211], a[168:171], 0\n"
"buffer_load_dwordx4 v247, s[24:27], s40 offen lds\n"
"v_mul_f32 v104, s45, v104\n"
"v_mul_f32 v105, s45, v105\n"
"v_mul_f32 v106, s45, v106\n"
"v_mul_f32 v107, s45, v107\n"
"v_mul_f32 v108, s45, v108\n"
"v_mul_f32 v109, s45, v109\n"
"v_mul_f32 v110, s45, v110\n"
"v_mul_f32 v111, s45, v111\n"
"v_cvt_pk_bf16_f32 v104, v104, v105\n"
"v_cvt_pk_bf16_f32 v105, v106, v107\n"
"v_cvt_pk_bf16_f32 v106, v108, v109\n"
"v_cvt_pk_bf16_f32 v107, v110, v111\n"
"v_mfma_f32_16x16x32_bf16 v[40:43], a[212:215], a[172:175], v[40:43]\n"
"v_permlane16_swap_b32 v104, v106\n"
"v_permlane16_swap_b32 v105, v107\n"
"v_mul_f32 v32, s45, v32\n"
"v_mul_f32 v33, s45, v33\n"
"v_mul_f32 v34, s45, v34\n"
"v_mul_f32 v35, s45, v35\n"
"v_mul_f32 v36, s45, v36\n"
"v_mul_f32 v37, s45, v37\n"
"v_mul_f32 v38, s45, v38\n"
"v_mul_f32 v39, s45, v39\n"
"v_cvt_pk_bf16_f32 v32, v32, v33\n"
"v_cvt_pk_bf16_f32 v33, v34, v35\n"
"v_cvt_pk_bf16_f32 v34, v36, v37\n"
"v_cvt_pk_bf16_f32 v35, v38, v39\n"
"v_mfma_f32_16x16x32_bf16 v[36:39], a[200:203], a[168:171], 0\n"
"ds_write_b128 v248, v[104:107] offset:6144\n"
"v_xor_b32 v243, s74, v243\n"
"v_mfma_f32_16x16x32_bf16 v[36:39], a[204:207], a[172:175], v[36:39]\n"
"ds_read_b64_tr_b16 a[96:97], v243 offset:0\n"
"ds_read_b64_tr_b16 a[98:99], v243 offset:512\n"
"v_permlane16_swap_b32 v32, v34\n"
"v_permlane16_swap_b32 v33, v35\n"
"v_accvgpr_write_b32 a64, v32\n"
"v_accvgpr_write_b32 a65, v33\n"
"v_accvgpr_write_b32 a66, v34\n"
"v_accvgpr_write_b32 a67, v35\n"
"v_xor_b32 v249, s74, v249\n"
"v_mfma_f32_16x16x32_bf16 v[32:35], a[192:195], a[168:171], 0\n"
"ds_read_b128 a[160:163], v249 offset:0\n"
"v_mul_f32 v64, s45, v64\n"
"v_mul_f32 v65, s45, v65\n"
"v_mul_f32 v66, s45, v66\n"
"v_mul_f32 v67, s45, v67\n"
"v_mul_f32 v68, s45, v68\n"
"v_mul_f32 v69, s45, v69\n"
"v_mul_f32 v70, s45, v70\n"
"v_mul_f32 v71, s45, v71\n"
"v_cvt_pk_bf16_f32 v64, v64, v65\n"
"v_cvt_pk_bf16_f32 v65, v66, v67\n"
"v_cvt_pk_bf16_f32 v66, v68, v69\n"
"v_cvt_pk_bf16_f32 v67, v70, v71\n"
"v_mfma_f32_16x16x32_bf16 v[32:35], a[196:199], a[172:175], v[32:35]\n"
"buffer_load_dwordx4 v253, s[24:27], s40 offen offset:1024 lds\n"
"v_permlane16_swap_b32 v64, v66\n"
"v_permlane16_swap_b32 v65, v67\n"
"v_accvgpr_write_b32 a80, v64\n"
"v_accvgpr_write_b32 a81, v65\n"
"v_accvgpr_write_b32 a82, v66\n"
"v_accvgpr_write_b32 a83, v67\n"
"v_mfma_f32_16x16x32_bf16 v[64:67], a[192:195], a[176:179], 0\n"
"ds_read_b64_tr_b16 a[100:101], v243 offset:8192\n"
"ds_read_b64_tr_b16 a[102:103], v243 offset:8704\n"
"v_xor_b32 v250, s74, v250\n"
"v_mfma_f32_16x16x32_bf16 v[64:67], a[196:199], a[180:183], v[64:67]\n"
"ds_read_b128 a[164:167], v250 offset:0\n"
"v_mul_f32 v96, s45, v96\n"
"v_mul_f32 v97, s45, v97\n"
"v_mul_f32 v98, s45, v98\n"
"v_mul_f32 v99, s45, v99\n"
"v_mul_f32 v100, s45, v100\n"
"v_mul_f32 v101, s45, v101\n"
"v_mul_f32 v102, s45, v102\n"
"v_mul_f32 v103, s45, v103\n"
"v_cvt_pk_bf16_f32 v96, v96, v97\n"
"v_cvt_pk_bf16_f32 v97, v98, v99\n"
"v_cvt_pk_bf16_f32 v98, v100, v101\n"
"v_cvt_pk_bf16_f32 v99, v102, v103\n"
"v_xor_b32 v244, s74, v244\n"
"v_mfma_f32_16x16x32_bf16 v[68:71], a[200:203], a[176:179], 0\n"
"ds_read_b64_tr_b16 a[104:105], v244 offset:0\n"
"ds_read_b64_tr_b16 a[106:107], v244 offset:512\n"
"v_permlane16_swap_b32 v96, v98\n"
"v_permlane16_swap_b32 v97, v99\n"
"v_mul_f32 v72, s45, v72\n"
"v_mul_f32 v73, s45, v73\n"
"v_mul_f32 v74, s45, v74\n"
"v_mul_f32 v75, s45, v75\n"
"v_mul_f32 v76, s45, v76\n"
"v_mul_f32 v77, s45, v77\n"
"v_mul_f32 v78, s45, v78\n"
"v_mul_f32 v79, s45, v79\n"
"v_cvt_pk_bf16_f32 v72, v72, v73\n"
"v_cvt_pk_bf16_f32 v73, v74, v75\n"
"v_cvt_pk_bf16_f32 v74, v76, v77\n"
"v_cvt_pk_bf16_f32 v75, v78, v79\n"
"v_mfma_f32_16x16x32_bf16 v[68:71], a[204:207], a[180:183], v[68:71]\n"
"ds_write_b128 v248, v[96:99] offset:4096\n"
"v_permlane16_swap_b32 v72, v74\n"
"v_permlane16_swap_b32 v73, v75\n"
"v_accvgpr_write_b32 a84, v72\n"
"v_accvgpr_write_b32 a85, v73\n"
"v_accvgpr_write_b32 a86, v74\n"
"v_accvgpr_write_b32 a87, v75\n"
"v_mfma_f32_16x16x32_bf16 v[72:75], a[208:211], a[176:179], 0\n"
"buffer_load_dwordx4 v254, s[24:27], s40 offen offset:2048 lds\n"
"v_mfma_f32_16x16x32_bf16 v[72:75], a[212:215], a[180:183], v[72:75]\n"
"s_add_u32 s49, s49, 128\n"
"ds_read_b64_tr_b16 a[108:109], v244 offset:8192\n"
"ds_read_b64_tr_b16 a[110:111], v244 offset:8704\n"
"v_xor_b32 v245, s74, v245\n"
"v_mfma_f32_16x16x32_bf16 v[76:79], a[216:219], a[176:179], 0\n"
"s_sub_u32 s41, s49, 1280\n"
"ds_read_b64_tr_b16 a[112:113], v245 offset:0\n"
"ds_read_b64_tr_b16 a[114:115], v245 offset:512\n"
"v_mul_f32 v88, s45, v88\n"
"v_mul_f32 v89, s45, v89\n"
"v_mul_f32 v90, s45, v90\n"
"v_mul_f32 v91, s45, v91\n"
"v_mul_f32 v92, s45, v92\n"
"v_mul_f32 v93, s45, v93\n"
"v_mul_f32 v94, s45, v94\n"
"v_mul_f32 v95, s45, v95\n"
"v_cvt_pk_bf16_f32 v88, v88, v89\n"
"v_cvt_pk_bf16_f32 v89, v90, v91\n"
"v_cvt_pk_bf16_f32 v90, v92, v93\n"
"v_cvt_pk_bf16_f32 v91, v94, v95\n"
"v_mfma_f32_16x16x32_bf16 v[76:79], a[220:223], a[180:183], v[76:79]\n"
"s_min_u32 s49, s49, s41\n"
"ds_read_b64_tr_b16 a[116:117], v245 offset:8192\n"
"ds_read_b64_tr_b16 a[118:119], v245 offset:8704\n"
"v_permlane16_swap_b32 v88, v90\n"
"v_permlane16_swap_b32 v89, v91\n"
"v_accvgpr_write_b32 a92, v88\n"
"v_accvgpr_write_b32 a93, v89\n"
"v_accvgpr_write_b32 a94, v90\n"
"v_accvgpr_write_b32 a95, v91\n"
"v_xor_b32 v246, s74, v246\n"
"v_mfma_f32_16x16x32_bf16 v[88:91], a[240:243], a[176:179], 0\n"
"s_xor_b32 s51, s51, s74\n"
"ds_read_b64_tr_b16 a[120:121], v246 offset:0\n"
"ds_read_b64_tr_b16 a[122:123], v246 offset:512\n"
"v_mfma_f32_16x16x32_bf16 v[88:91], a[244:247], a[180:183], v[88:91]\n"
"ds_read_b64_tr_b16 a[124:125], v246 offset:8192\n"
"ds_read_b64_tr_b16 a[126:127], v246 offset:8704\n"
"v_mfma_f32_16x16x32_bf16 v[92:95], a[248:251], a[176:179], 0\n"
"ds_read_b64_tr_b16 a[128:129], v243 offset:1024\n"
"ds_read_b64_tr_b16 a[130:131], v243 offset:1536\n"
"v_mul_f32 v80, s45, v80\n"
"v_mul_f32 v81, s45, v81\n"
"v_mul_f32 v82, s45, v82\n"
"v_mul_f32 v83, s45, v83\n"
"v_mul_f32 v84, s45, v84\n"
"v_mul_f32 v85, s45, v85\n"
"v_mul_f32 v86, s45, v86\n"
"v_mul_f32 v87, s45, v87\n"
"v_cvt_pk_bf16_f32 v80, v80, v81\n"
"v_cvt_pk_bf16_f32 v81, v82, v83\n"
"v_cvt_pk_bf16_f32 v82, v84, v85\n"
"v_cvt_pk_bf16_f32 v83, v86, v87\n"
"v_mfma_f32_16x16x32_bf16 v[92:95], a[252:255], a[180:183], v[92:95]\n"
"ds_read_b64_tr_b16 a[132:133], v243 offset:9216\n"
"ds_read_b64_tr_b16 a[134:135], v243 offset:9728\n"
"v_permlane16_swap_b32 v80, v82\n"
"v_permlane16_swap_b32 v81, v83\n"
"v_accvgpr_write_b32 a88, v80\n"
"v_accvgpr_write_b32 a89, v81\n"
"v_accvgpr_write_b32 a90, v82\n"
"v_accvgpr_write_b32 a91, v83\n"
"v_mfma_f32_16x16x32_bf16 v[80:83], a[224:227], a[176:179], 0\n"
"ds_read_b64_tr_b16 a[136:137], v244 offset:1024\n"
"ds_read_b64_tr_b16 a[138:139], v244 offset:1536\n"
"v_mfma_f32_16x16x32_bf16 v[80:83], a[228:231], a[180:183], v[80:83]\n"
"ds_read_b64_tr_b16 a[140:141], v244 offset:9216\n"
"ds_read_b64_tr_b16 a[142:143], v244 offset:9728\n"
"v_mfma_f32_16x16x32_bf16 v[84:87], a[232:235], a[176:179], 0\n"
"ds_read_b64_tr_b16 a[144:145], v245 offset:1024\n"
"ds_read_b64_tr_b16 a[146:147], v245 offset:1536\n"
"v_mfma_f32_16x16x32_bf16 v[84:87], a[236:239], a[180:183], v[84:87]\n"
"ds_read_b64_tr_b16 a[148:149], v245 offset:9216\n"
"ds_read_b64_tr_b16 a[150:151], v245 offset:9728\n"
"v_mfma_f32_16x16x32_bf16 v[108:111], a[232:235], a[184:187], 0\n"
"ds_read_b64_tr_b16 a[152:153], v246 offset:1024\n"
"ds_read_b64_tr_b16 a[154:155], v246 offset:1536\n"
"v_mfma_f32_16x16x32_bf16 v[108:111], a[236:239], a[188:191], v[108:111]\n"
"ds_read_b64_tr_b16 a[156:157], v246 offset:9216\n"
"ds_read_b64_tr_b16 a[158:159], v246 offset:9728\n"
"v_mfma_f32_16x16x32_bf16 v[104:107], a[224:227], a[184:187], 0\n"
"ds_read_b128 a[168:171], v249 offset:2048\n"
"v_xor_b32 v251, s74, v251\n"
"v_mfma_f32_16x16x32_bf16 v[104:107], a[228:231], a[188:191], v[104:107]\n"
"ds_read_b128 a[172:175], v250 offset:2048\n"
"v_xor_b32 v252, s74, v252\n"
"v_mfma_f32_16x16x32_bf16 v[100:103], a[200:203], a[184:187], 0\n"
"ds_read_b128 a[176:179], v249 offset:4096\n"
"v_mfma_f32_16x16x32_bf16 v[100:103], a[204:207], a[188:191], v[100:103]\n"
"ds_read_b128 a[180:183], v250 offset:4096\n"
"v_mfma_f32_16x16x32_bf16 v[96:99], a[192:195], a[184:187], 0\n"
"s_add_u32 s47, s47, 1\n"
"ds_read_b128 a[184:187], v251 offset:0\n"
"v_mfma_f32_16x16x32_bf16 v[96:99], a[196:199], a[188:191], v[96:99]\n"
"ds_read_b128 a[188:191], v252 offset:0\n"
"s_mov_b32 s62, 18\n"
"s_branch LPL_%=\n"
"LPT2_%=:\n"
"s_waitcnt lgkmcnt(4)\n"
"v_mfma_f32_16x16x32_bf16 v[112:115], a[96:99], a[160:163], v[112:115]\n"
"v_bfe_i32 v241, v242, 4, 1\n"
"v_and_b32 v241, 0xaaaaaaaa, v241\n"
"v_mfma_f32_16x16x32_bf16 v[112:115], a[100:103], a[164:167], v[112:115]\n"
"v_or_b32 v241, 0x44444444, v241\n"
"v_bfe_u32 v247, v242, 4, 2\n"
"v_mfma_f32_16x16x32_bf16 v[116:119], a[104:107], a[160:163], v[116:119]\n"
"v_mul_u32_u24 v247, 20, v247\n"
"v_mfma_f32_16x16x32_bf16 v[116:119], a[108:111], a[164:167], v[116:119]\n"
"s_waitcnt vmcnt(11)\n"
"v_mfma_f32_16x16x32_bf16 v[120:123], a[112:115], a[160:163], v[120:123]\n"
"s_barrier\n"
"v_mfma_f32_16x16x32_bf16 v[120:123], a[116:119], a[164:167], v[120:123]\n"
"s_add_u32 s40, s48, 256\n"
"s_xor_b32 m0, s50, 0xc000\n"
"ds_read_b64_tr_b16 a[192:193], v243 offset:16384\n"
"v_mfma_f32_16x16x32_bf16 v[124:127], a[120:123], a[160:163], v[124:127]\n"
"buffer_load_dwordx4 v224, s[28:31], s40 offen lds\n"
"v_mfma_f32_16x16x32_bf16 v[124:127], a[124:127], a[164:167], v[124:127]\n"
"ds_read_b64_tr_b16 a[194:195], v243 offset:16896\n"
"v_mfma_f32_16x16x32_bf16 v[128:131], a[128:131], a[160:163], v[128:131]\n"
"ds_read_b64_tr_b16 a[196:197], v243 offset:24576\n"
"v_mfma_f32_16x16x32_bf16 v[128:131], a[132:135], a[164:167], v[128:131]\n"
"ds_read_b64_tr_b16 a[198:199], v243 offset:25088\n"
"v_mfma_f32_16x16x32_bf16 v[132:135], a[136:139], a[160:163], v[132:135]\n"
"ds_read_b64_tr_b16 a[200:201], v244 offset:16384\n"
"v_mfma_f32_16x16x32_bf16 v[132:135], a[140:143], a[164:167], v[132:135]\n"
"ds_read_b64_tr_b16 a[202:203], v244 offset:16896\n"
"v_mfma_f32_16x16x32_bf16 v[136:139], a[144:147], a[160:163], v[136:139]\n"
"buffer_load_dwordx4 v225, s[28:31], s40 offen offset:1024 lds\n"
"v_mfma_f32_16x16x32_bf16 v[136:139], a[148:151], a[164:167], v[136:139]\n"
"ds_read_b64_tr_b16 a[204:205], v244 offset:24576\n"
"v_mfma_f32_16x16x32_bf16 v[140:143], a[152:155], a[160:163], v[140:143]\n"
"ds_read_b64_tr_b16 a[206:207], v244 offset:25088\n"
"v_mfma_f32_16x16x32_bf16 v[140:143], a[156:159], a[164:167], v[140:143]\n"
"ds_read_b64_tr_b16 a[208:209], v245 offset:16384\n"
"v_mfma_f32_16x16x32_bf16 v[172:175], a[152:155], a[168:171], v[172:175]\n"
"ds_read_b64_tr_b16 a[210:211], v245 offset:16896\n"
"v_mfma_f32_16x16x32_bf16 v[172:175], a[156:159], a[172:175], v[172:175]\n"
"ds_read_b64_tr_b16 a[212:213], v245 offset:24576\n"
"v_mfma_f32_16x16x32_bf16 v[168:171], a[144:147], a[168:171], v[168:171]\n"
"ds_read_b64_tr_b16 a[214:215], v245 offset:25088\n"
"v_mfma_f32_16x16x32_bf16 v[168:171], a[148:151], a[172:175], v[168:171]\n"
"buffer_load_dwordx4 v226, s[28:31], s40 offen offset:2048 lds\n"
"v_mfma_f32_16x16x32_bf16 v[164:167], a[136:139], a[168:171], v[164:167]\n"
"ds_read_b64_tr_b16 a[216:217], v246 offset:16384\n"
"v_mfma_f32_16x16x32_bf16 v[164:167], a[140:143], a[172:175], v[164:167]\n"
"ds_read_b64_tr_b16 a[218:219], v246 offset:16896\n"
"v_mfma_f32_16x16x32_bf16 v[160:163], a[128:131], a[168:171], v[160:163]\n"
"ds_read_b64_tr_b16 a[220:221], v246 offset:24576\n"
"v_mfma_f32_16x16x32_bf16 v[160:163], a[132:135], a[172:175], v[160:163]\n"
"ds_read_b64_tr_b16 a[222:223], v246 offset:25088\n"
"v_mfma_f32_16x16x32_bf16 v[156:159], a[120:123], a[168:171], v[156:159]\n"
"ds_read_b64_tr_b16 a[224:225], v243 offset:17408\n"
"v_mfma_f32_16x16x32_bf16 v[156:159], a[124:127], a[172:175], v[156:159]\n"
"buffer_load_dwordx4 v227, s[28:31], s40 offen offset:3072 lds\n"
"v_mfma_f32_16x16x32_bf16 v[152:155], a[112:115], a[168:171], v[152:155]\n"
"s_mov_b32 s58, 245760\n"
"ds_read_b64_tr_b16 a[226:227], v243 offset:17920\n"
"v_mfma_f32_16x16x32_bf16 v[152:155], a[116:119], a[172:175], v[152:155]\n"
"s_mov_b32 s47, 0\n"
"ds_read_b64_tr_b16 a[228:229], v243 offset:25600\n"
"v_mfma_f32_16x16x32_bf16 v[148:151], a[104:107], a[168:171], v[148:151]\n"
"ds_read_b64_tr_b16 a[230:231], v243 offset:26112\n"
"v_mfma_f32_16x16x32_bf16 v[148:151], a[108:111], a[172:175], v[148:151]\n"
"ds_read_b64_tr_b16 a[232:233], v244 offset:17408\n"
"v_mfma_f32_16x16x32_bf16 v[144:147], a[96:99], a[168:171], v[144:147]\n"
"ds_read_b64_tr_b16 a[234:235], v244 offset:17920\n"
"v_xor_b32 v243, s74, v243\n"
"v_mfma_f32_16x16x32_bf16 v[144:147], a[100:103], a[172:175], v[144:147]\n"
"s_waitcnt lgkmcnt(6)\n"
"s_mov_b32 m0, s44\n"
"ds_read_b64_tr_b16 a[236:237], v244 offset:25600\n"
"v_xor_b32 v249, s74, v249\n"
"v_mfma_f32_16x16x32_bf16 v[176:179], a[96:99], a[176:179], v[176:179]\n"
"buffer_load_dwordx4 v240, s[16:19], s56 offen nt lds\n"
"v_mfma_f32_16x16x32_bf16 v[176:179], a[100:103], a[180:183], v[176:179]\n"
"ds_read_b64_tr_b16 a[238:239], v244 offset:26112\n"
"v_xor_b32 v250, s74, v250\n"
"v_mfma_f32_16x16x32_bf16 v[180:183], a[104:107], a[176:179], v[180:183]\n"
"ds_read_b64_tr_b16 a[240:241], v245 offset:17408\n"
"v_xor_b32 v244, s74, v244\n"
"v_mfma_f32_16x16x32_bf16 v[180:183], a[108:111], a[180:183], v[180:183]\n"
"ds_read_b64_tr_b16 a[242:243], v245 offset:17920\n"
"v_xor_b32 v251, s74, v251\n"
"v_mfma_f32_16x16x32_bf16 v[184:187], a[112:115], a[176:179], v[184:187]\n"
"ds_read_b64_tr_b16 a[244:245], v245 offset:25600\n"
"v_xor_b32 v252, s74, v252\n"
"v_mfma_f32_16x16x32_bf16 v[184:187], a[116:119], a[180:183], v[184:187]\n"
"ds_read_b64_tr_b16 a[246:247], v245 offset:26112\n"
"v_xor_b32 v245, s74, v245\n"
"v_mfma_f32_16x16x32_bf16 v[188:191], a[120:123], a[176:179], v[188:191]\n"
"s_add_u32 s40, s56, s52\n"
"ds_read_b64_tr_b16 a[248:249], v246 offset:17408\n"
"v_mfma_f32_16x16x32_bf16 v[188:191], a[124:127], a[180:183], v[188:191]\n"
"buffer_load_dwordx4 v240, s[16:19], s40 offen offset:1024 nt lds\n"
"v_mfma_f32_16x16x32_bf16 v[200:203], a[144:147], a[176:179], v[200:203]\n"
"ds_read_b64_tr_b16 a[250:251], v246 offset:17920\n"
"v_mfma_f32_16x16x32_bf16 v[200:203], a[148:151], a[180:183], v[200:203]\n"
"ds_read_b64_tr_b16 a[252:253], v246 offset:25600\n"
"v_mfma_f32_16x16x32_bf16 v[204:207], a[152:155], a[176:179], v[204:207]\n"
"ds_read_b64_tr_b16 a[254:255], v246 offset:26112\n"
"v_xor_b32 v246, s74, v246\n"
"v_mfma_f32_16x16x32_bf16 v[204:207], a[156:159], a[180:183], v[204:207]\n"
"v_mfma_f32_16x16x32_bf16 v[192:195], a[128:131], a[176:179], v[192:195]\n"
"s_add_u32 s40, s56, s53\n"
"v_mfma_f32_16x16x32_bf16 v[192:195], a[132:135], a[180:183], v[192:195]\n"
"buffer_load_dwordx4 v240, s[16:19], s40 offen offset:2048 nt lds\n"
"v_mfma_f32_16x16x32_bf16 v[196:199], a[136:139], a[176:179], v[196:199]\n"
"v_mfma_f32_16x16x32_bf16 v[196:199], a[140:143], a[180:183], v[196:199]\n"
"v_mfma_f32_16x16x32_bf16 v[220:223], a[136:139], a[184:187], v[220:223]\n"
"v_mfma_f32_16x16x32_bf16 v[220:223], a[140:143], a[188:191], v[220:223]\n"
"v_mfma_f32_16x16x32_bf16 v[216:219], a[128:131], a[184:187], v[216:219]\n"
"v_mfma_f32_16x16x32_bf16 v[216:219], a[132:135], a[188:191], v[216:219]\n"
"s_add_u32 s40, s56, s54\n"
"v_mfma_f32_16x16x32_bf16 v[212:215], a[104:107], a[184:187], v[212:215]\n"
"buffer_load_dwordx4 v240, s[16:19], s40 offen offset:3072 nt lds\n"
"v_mfma_f32_16x16x32_bf16 v[212:215], a[108:111], a[188:191], v[212:215]\n"
"v_mfma_f32_16x16x32_bf16 v[208:211], a[96:99], a[184:187], v[208:211]\n"
"v_mfma_f32_16x16x32_bf16 v[208:211], a[100:103], a[188:191], v[208:211]\n"
"v_mfma_f32_16x16x32_bf16 v[0:3], a[192:195], a[160:163], v[0:3]\n"
"v_mfma_f32_16x16x32_bf16 v[0:3], a[196:199], a[164:167], v[0:3]\n"
"v_mfma_f32_16x16x32_bf16 v[4:7], a[200:203], a[160:163], v[4:7]\n"
"v_mfma_f32_16x16x32_bf16 v[4:7], a[204:207], a[164:167], v[4:7]\n"
"s_waitcnt vmcnt(8) lgkmcnt(0)\n"
"v_mfma_f32_16x16x32_bf16 v[8:11], a[208:211], a[160:163], v[8:11]\n"
"s_barrier\n"
"v_mfma_f32_16x16x32_bf16 v[8:11], a[212:215], a[164:167], v[8:11]\n"
"s_add_u32 m0, s44, 65536\n"
"ds_read_b64_tr_b16 a[96:97], v243 offset:0\n"
"v_mfma_f32_16x16x32_bf16 v[12:15], a[216:219], a[160:163], v[12:15]\n"
"buffer_load_dwordx4 v240, s[20:23], s56 offen nt lds\n"
"v_mfma_f32_16x16x32_bf16 v[12:15], a[220:223], a[164:167], v[12:15]\n"
"ds_read_b64_tr_b16 a[98:99], v243 offset:512\n"
"v_mfma_f32_16x16x32_bf16 v[16:19], a[224:227], a[160:163], v[16:19]\n"
"ds_read_b64_tr_b16 a[100:101], v243 offset:8192\n"
"v_mfma_f32_16x16x32_bf16 v[16:19], a[228:231], a[164:167], v[16:19]\n"
"ds_read_b64_tr_b16 a[102:103], v243 offset:8704\n"
"v_mfma_f32_16x16x32_bf16 v[20:23], a[232:235], a[160:163], v[20:23]\n"
"ds_read_b64_tr_b16 a[104:105], v244 offset:0\n"
"v_mfma_f32_16x16x32_bf16 v[20:23], a[236:239], a[164:167], v[20:23]\n"
"s_add_u32 s40, s56, s52\n"
"ds_read_b64_tr_b16 a[106:107], v244 offset:512\n"
"v_mfma_f32_16x16x32_bf16 v[24:27], a[240:243], a[160:163], v[24:27]\n"
"buffer_load_dwordx4 v240, s[20:23], s40 offen offset:1024 nt lds\n"
"v_mfma_f32_16x16x32_bf16 v[24:27], a[244:247], a[164:167], v[24:27]\n"
"ds_read_b64_tr_b16 a[108:109], v244 offset:8192\n"
"v_mfma_f32_16x16x32_bf16 v[28:31], a[248:251], a[160:163], v[28:31]\n"
"ds_read_b128 a[160:163], v249 offset:0\n"
"v_mfma_f32_16x16x32_bf16 v[28:31], a[252:255], a[164:167], v[28:31]\n"
"ds_read_b128 a[164:167], v250 offset:0\n"
"v_mfma_f32_16x16x32_bf16 v[60:63], a[248:251], a[168:171], v[60:63]\n"
"ds_read_b64_tr_b16 a[110:111], v244 offset:8704\n"
"v_mfma_f32_16x16x32_bf16 v[60:63], a[252:255], a[172:175], v[60:63]\n"
"ds_read_b64_tr_b16 a[112:113], v245 offset:0\n"
"v_mfma_f32_16x16x32_bf16 v[56:59], a[240:243], a[168:171], v[56:59]\n"
"s_add_u32 s40, s56, s53\n"
"ds_read_b64_tr_b16 a[114:115], v245 offset:512\n"
"v_mfma_f32_16x16x32_bf16 v[56:59], a[244:247], a[172:175], v[56:59]\n"
"buffer_load_dwordx4 v240, s[20:23], s40 offen offset:2048 nt lds\n"
"v_mfma_f32_16x16x32_bf16 v[52:55], a[232:235], a[168:171], v[52:55]\n"
"ds_read_b64_tr_b16 a[116:117], v245 offset:8192\n"
"v_mfma_f32_16x16x32_bf16 v[52:55], a[236:239], a[172:175], v[52:55]\n"
"ds_read_b64_tr_b16 a[118:119], v245 offset:8704\n"
"v_mfma_f32_16x16x32_bf16 v[48:51], a[224:227], a[168:171], v[48:51]\n"
"ds_read_b64_tr_b16 a[120:121], v246 offset:0\n"
"v_mfma_f32_16x16x32_bf16 v[48:51], a[228:231], a[172:175], v[48:51]\n"
"ds_read_b64_tr_b16 a[122:123], v246 offset:512\n"
"v_mfma_f32_16x16x32_bf16 v[44:47], a[216:219], a[168:171], v[44:47]\n"
"s_add_u32 s40, s56, s54\n"
"ds_read_b64_tr_b16 a[124:125], v246 offset:8192\n"
"v_mfma_f32_16x16x32_bf16 v[44:47], a[220:223], a[172:175], v[44:47]\n"
"buffer_load_dwordx4 v240, s[20:23], s40 offen offset:3072 nt lds\n"
"v_mfma_f32_16x16x32_bf16 v[40:43], a[208:211], a[168:171], v[40:43]\n"
"s_add_u32 s55, s55, s59\n"
"ds_read_b64_tr_b16 a[126:127], v246 offset:8704\n"
"v_mfma_f32_16x16x32_bf16 v[40:43], a[212:215], a[172:175], v[40:43]\n"
"s_add_u32 s41, s55, s57\n"
"ds_read_b64_tr_b16 a[128:129], v243 offset:1024\n"
"v_mfma_f32_16x16x32_bf16 v[36:39], a[200:203], a[168:171], v[36:39]\n"
"s_and_b32 s41, s41, s58\n"
"ds_read_b64_tr_b16 a[130:131], v243 offset:1536\n"
"v_mfma_f32_16x16x32_bf16 v[36:39], a[204:207], a[172:175], v[36:39]\n"
"s_andn2_b32 s56, s55, s58\n"
"ds_read_b64_tr_b16 a[132:133], v243 offset:9216\n"
"v_mfma_f32_16x16x32_bf16 v[32:35], a[192:195], a[168:171], v[32:35]\n"
"s_or_b32 s56, s56, s41\n"
"ds_read_b64_tr_b16 a[134:135], v243 offset:9728\n"
"v_mfma_f32_16x16x32_bf16 v[32:35], a[196:199], a[172:175], v[32:35]\n"
"s_add_u32 m0, s44, 16384\n"
"ds_read_b64_tr_b16 a[136:137], v244 offset:1024\n"
"v_mfma_f32_16x16x32_bf16 v[64:67], a[192:195], a[176:179], v[64:67]\n"
"buffer_load_dwordx4 v240, s[16:19], s56 offen nt lds\n"
"v_mfma_f32_16x16x32_bf16 v[64:67], a[196:199], a[180:183], v[64:67]\n"
"ds_read_b64_tr_b16 a[138:139], v244 offset:1536\n"
"v_mfma_f32_16x16x32_bf16 v[68:71], a[200:203], a[176:179], v[68:71]\n"
"ds_read_b64_tr_b16 a[140:141], v244 offset:9216\n"
"v_mfma_f32_16x16x32_bf16 v[68:71], a[204:207], a[180:183], v[68:71]\n"
"ds_read_b64_tr_b16 a[142:143], v244 offset:9728\n"
"v_mfma_f32_16x16x32_bf16 v[72:75], a[208:211], a[176:179], v[72:75]\n"
"ds_read_b64_tr_b16 a[144:145], v245 offset:1024\n"
"v_mfma_f32_16x16x32_bf16 v[72:75], a[212:215], a[180:183], v[72:75]\n"
"ds_read_b64_tr_b16 a[146:147], v245 offset:1536\n"
"v_mfma_f32_16x16x32_bf16 v[76:79], a[216:219], a[176:179], v[76:79]\n"
"s_add_u32 s40, s56, s52\n"
"ds_read_b64_tr_b16 a[148:149], v245 offset:9216\n"
"v_mfma_f32_16x16x32_bf16 v[76:79], a[220:223], a[180:183], v[76:79]\n"
"buffer_load_dwordx4 v240, s[16:19], s40 offen offset:1024 nt lds\n"
"v_mfma_f32_16x16x32_bf16 v[88:91], a[240:243], a[176:179], v[88:91]\n"
"ds_read_b64_tr_b16 a[150:151], v245 offset:9728\n"
"v_mfma_f32_16x16x32_bf16 v[88:91], a[244:247], a[180:183], v[88:91]\n"
"ds_read_b64_tr_b16 a[152:153], v246 offset:1024\n"
"v_mfma_f32_16x16x32_bf16 v[92:95], a[248:251], a[176:179], v[92:95]\n"
"ds_read_b64_tr_b16 a[154:155], v246 offset:1536\n"
"v_mfma_f32_16x16x32_bf16 v[92:95], a[252:255], a[180:183], v[92:95]\n"
"ds_read_b64_tr_b16 a[156:157], v246 offset:9216\n"
"v_mfma_f32_16x16x32_bf16 v[80:83], a[224:227], a[176:179], v[80:83]\n"
"s_add_u32 s40, s56, s53\n"
"ds_read_b64_tr_b16 a[158:159], v246 offset:9728\n"
"v_mfma_f32_16x16x32_bf16 v[80:83], a[228:231], a[180:183], v[80:83]\n"
"buffer_load_dwordx4 v240, s[16:19], s40 offen offset:2048 nt lds\n"
"v_mfma_f32_16x16x32_bf16 v[84:87], a[232:235], a[176:179], v[84:87]\n"
"ds_read_b128 a[168:171], v249 offset:2048\n"
"v_mfma_f32_16x16x32_bf16 v[84:87], a[236:239], a[180:183], v[84:87]\n"
"ds_read_b128 a[172:175], v250 offset:2048\n"
"v_mfma_f32_16x16x32_bf16 v[108:111], a[232:235], a[184:187], v[108:111]\n"
"ds_read_b128 a[176:179], v249 offset:4096\n"
"v_and_b32 v249, 0x20, v242\n"
"v_mfma_f32_16x16x32_bf16 v[108:111], a[236:239], a[188:191], v[108:111]\n"
"ds_read_b128 a[180:183], v250 offset:4096\n"
"v_add_u32 v249, -1, v249\n"
"v_mfma_f32_16x16x32_bf16 v[104:107], a[224:227], a[184:187], v[104:107]\n"
"v_ashrrev_i32 v249, 31, v249\n"
"v_and_b32 v249, 0x3f803f80, v249\n"
"v_mfma_f32_16x16x32_bf16 v[104:107], a[228:231], a[188:191], v[104:107]\n"
"s_add_u32 s40, s56, s54\n"
"v_mfma_f32_16x16x32_bf16 v[100:103], a[200:203], a[184:187], v[100:103]\n"
"s_mov_b64 s[24:25], s[8:9]\n"
"buffer_load_dwordx4 v240, s[16:19], s40 offen offset:3072 nt lds\n"
"v_mfma_f32_16x16x32_bf16 v[100:103], a[204:207], a[188:191], v[100:103]\n"
"s_mov_b32 s26, s68\n"
"v_mfma_f32_16x16x32_bf16 v[96:99], a[192:195], a[184:187], v[96:99]\n"
"v_and_b32 v254, 0x3f0, v242\n"
"ds_read_b128 a[184:187], v251 offset:0\n"
"v_mfma_f32_16x16x32_bf16 v[96:99], a[196:199], a[188:191], v[96:99]\n"
"v_lshlrev_b32 v254, 3, v254\n"
"ds_read_b128 a[188:191], v252 offset:0\n"
"s_waitcnt lgkmcnt(4)\n"
"v_mfma_f32_16x16x32_bf16 v[112:115], a[96:99], a[160:163], v[112:115]\n"
"v_mfma_f32_16x16x32_bf16 v[112:115], a[100:103], a[164:167], v[112:115]\n"
"v_mfma_f32_16x16x32_bf16 v[116:119], a[104:107], a[160:163], v[116:119]\n"
"v_mfma_f32_16x16x32_bf16 v[116:119], a[108:111], a[164:167], v[116:119]\n"
"s_waitcnt vmcnt(12)\n"
"v_mfma_f32_16x16x32_bf16 v[120:123], a[112:115], a[160:163], v[120:123]\n"
"s_barrier\n"
"v_mfma_f32_16x16x32_bf16 v[120:123], a[116:119], a[164:167], v[120:123]\n"
"s_add_u32 m0, s44, 81920\n"
"ds_read_b64_tr_b16 a[232:233], v244 offset:17408\n"
"v_mfma_f32_16x16x32_bf16 v[124:127], a[120:123], a[160:163], v[124:127]\n"
"buffer_load_dwordx4 v240, s[20:23], s56 offen nt lds\n"
"v_mfma_f32_16x16x32_bf16 v[124:127], a[124:127], a[164:167], v[124:127]\n"
"ds_read_b64_tr_b16 a[234:235], v244 offset:17920\n"
"v_mul_f32 v112, s45, v112\n"
"v_mfma_f32_16x16x32_bf16 v[128:131], a[128:131], a[160:163], v[128:131]\n"
"ds_read_b64_tr_b16 a[236:237], v244 offset:25600\n"
"v_mul_f32 v113, s45, v113\n"
"v_mfma_f32_16x16x32_bf16 v[128:131], a[132:135], a[164:167], v[128:131]\n"
"ds_read_b64_tr_b16 a[238:239], v244 offset:26112\n"
"v_mul_f32 v114, s45, v114\n"
"v_mfma_f32_16x16x32_bf16 v[132:135], a[136:139], a[160:163], v[132:135]\n"
"ds_read_b64_tr_b16 a[224:225], v243 offset:17408\n"
"v_mul_f32 v115, s45, v115\n"
"v_mfma_f32_16x16x32_bf16 v[132:135], a[140:143], a[164:167], v[132:135]\n"
"ds_read_b64_tr_b16 a[226:227], v243 offset:17920\n"
"v_mul_f32 v120, s45, v120\n"
"v_mfma_f32_16x16x32_bf16 v[136:139], a[144:147], a[160:163], v[136:139]\n"
"ds_read_b64_tr_b16 a[228:229], v243 offset:25600\n"
"v_mul_f32 v121, s45, v121\n"
"v_mfma_f32_16x16x32_bf16 v[136:139], a[148:151], a[164:167], v[136:139]\n"
"ds_read_b64_tr_b16 a[230:231], v243 offset:26112\n"
"v_mul_f32 v122, s45, v122\n"
"v_mfma_f32_16x16x32_bf16 v[140:143], a[152:155], a[160:163], v[140:143]\n"
"ds_read_b64_tr_b16 a[192:193], v243 offset:16384\n"
"v_mul_f32 v123, s45, v123\n"
"v_mfma_f32_16x16x32_bf16 v[140:143], a[156:159], a[164:167], v[140:143]\n"
"s_waitcnt lgkmcnt(7)\n"
"s_add_u32 s40, s56, s52\n"
"ds_read_b64_tr_b16 a[194:195], v243 offset:16896\n"
"v_mul_f32 v124, s45, v124\n"
"v_mfma_f32_16x16x32_bf16 v[220:223], a[136:139], a[184:187], v[220:223]\n"
"buffer_load_dwordx4 v240, s[20:23], s40 offen offset:1024 nt lds\n"
"v_mfma_f32_16x16x32_bf16 v[220:223], a[140:143], a[188:191], v[220:223]\n"
"ds_read_b64_tr_b16 a[196:197], v243 offset:24576\n"
"v_mul_f32 v125, s45, v125\n"
"v_mfma_f32_16x16x32_bf16 v[216:219], a[128:131], a[184:187], v[216:219]\n"
"ds_read_b64_tr_b16 a[198:199], v243 offset:25088\n"
"v_mul_f32 v126, s45, v126\n"
"v_mfma_f32_16x16x32_bf16 v[216:219], a[132:135], a[188:191], v[216:219]\n"
"ds_read_b64_tr_b16 a[200:201], v244 offset:16384\n"
"v_mul_f32 v127, s45, v127\n"
"v_mfma_f32_16x16x32_bf16 v[212:215], a[104:107], a[184:187], v[212:215]\n"
"ds_read_b64_tr_b16 a[202:203], v244 offset:16896\n"
"v_cvt_pk_bf16_f32 v120, v120, v121\n"
"v_mfma_f32_16x16x32_bf16 v[212:215], a[108:111], a[188:191], v[212:215]\n"
"ds_read_b64_tr_b16 a[204:205], v244 offset:24576\n"
"v_cvt_pk_bf16_f32 v121, v122, v123\n"
"v_mfma_f32_16x16x32_bf16 v[208:211], a[96:99], a[184:187], v[208:211]\n"
"ds_read_b64_tr_b16 a[206:207], v244 offset:25088\n"
"v_cvt_pk_bf16_f32 v122, v124, v125\n"
"v_mfma_f32_16x16x32_bf16 v[208:211], a[100:103], a[188:191], v[208:211]\n"
"ds_read_b64_tr_b16 a[208:209], v245 offset:16384\n"
"v_mul_f32 v220, s45, v220\n"
"v_mfma_f32_16x16x32_bf16 v[172:175], a[152:155], a[168:171], v[172:175]\n"
"ds_read_b64_tr_b16 a[210:211], v245 offset:16896\n"
"v_mul_f32 v221, s45, v221\n"
"v_mfma_f32_16x16x32_bf16 v[172:175], a[156:159], a[172:175], v[172:175]\n"
"s_add_u32 s40, s56, s53\n"
"ds_read_b64_tr_b16 a[212:213], v245 offset:24576\n"
"v_mul_f32 v216, s45, v216\n"
"v_mfma_f32_16x16x32_bf16 v[168:171], a[144:147], a[168:171], v[168:171]\n"
"buffer_load_dwordx4 v240, s[20:23], s40 offen offset:2048 nt lds\n"
"v_mfma_f32_16x16x32_bf16 v[168:171], a[148:151], a[172:175], v[168:171]\n"
"ds_read_b64_tr_b16 a[214:215], v245 offset:25088\n"
"v_mul_f32 v212, s45, v212\n"
"v_mfma_f32_16x16x32_bf16 v[164:167], a[136:139], a[168:171], v[164:167]\n"
"ds_read_b64_tr_b16 a[216:217], v246 offset:16384\n"
"v_mul_f32 v213, s45, v213\n"
"v_mfma_f32_16x16x32_bf16 v[164:167], a[140:143], a[172:175], v[164:167]\n"
"ds_read_b64_tr_b16 a[218:219], v246 offset:16896\n"
"v_mul_f32 v208, s45, v208\n"
"v_mfma_f32_16x16x32_bf16 v[160:163], a[128:131], a[168:171], v[160:163]\n"
"ds_read_b64_tr_b16 a[220:221], v246 offset:24576\n"
"v_mul_f32 v209, s45, v209\n"
"v_mfma_f32_16x16x32_bf16 v[160:163], a[132:135], a[172:175], v[160:163]\n"
"ds_read_b64_tr_b16 a[222:223], v246 offset:25088\n"
"v_mul_f32 v210, s45, v210\n"
"v_mfma_f32_16x16x32_bf16 v[156:159], a[120:123], a[168:171], v[156:159]\n"
"ds_read_b64_tr_b16 a[240:241], v245 offset:17408\n"
"v_mul_f32 v211, s45, v211\n"
"v_mfma_f32_16x16x32_bf16 v[156:159], a[124:127], a[172:175], v[156:159]\n"
"ds_read_b64_tr_b16 a[242:243], v245 offset:17920\n"
"v_mul_f32 v214, s45, v214\n"
"v_mfma_f32_16x16x32_bf16 v[152:155], a[112:115], a[168:171], v[152:155]\n"
"ds_read_b64_tr_b16 a[244:245], v245 offset:25600\n"
"v_mul_f32 v215, s45, v215\n"
"v_mfma_f32_16x16x32_bf16 v[152:155], a[116:119], a[172:175], v[152:155]\n"
"ds_read_b64_tr_b16 a[246:247], v245 offset:26112\n"
"v_cvt_pk_bf16_f32 v208, v208, v209\n"
"v_mfma_f32_16x16x32_bf16 v[148:151], a[104:107], a[168:171], v[148:151]\n"
"s_add_u32 s40, s56, s54\n"
"ds_read_b64_tr_b16 a[248:249], v246 offset:17408\n"
"v_cvt_pk_bf16_f32 v209, v210, v211\n"
"v_mfma_f32_16x16x32_bf16 v[148:151], a[108:111], a[172:175], v[148:151]\n"
"buffer_load_dwordx4 v240, s[20:23], s40 offen offset:3072 nt lds\n"
"v_mfma_f32_16x16x32_bf16 v[144:147], a[96:99], a[168:171], v[144:147]\n"
"s_add_u32 s55, s55, s59\n"
"ds_read_b64_tr_b16 a[250:251], v246 offset:17920\n"
"v_cvt_pk_bf16_f32 v210, v212, v213\n"
"v_mfma_f32_16x16x32_bf16 v[144:147], a[100:103], a[172:175], v[144:147]\n"
"s_add_u32 s41, s55, s57\n"
"ds_read_b64_tr_b16 a[252:253], v246 offset:25600\n"
"v_cvt_pk_bf16_f32 v211, v214, v215\n"
"v_mfma_f32_16x16x32_bf16 v[176:179], a[96:99], a[176:179], v[176:179]\n"
"s_and_b32 s41, s41, s58\n"
"ds_read_b64_tr_b16 a[254:255], v246 offset:26112\n"
"v_mul_f32 v217, s45, v217\n"
"v_mfma_f32_16x16x32_bf16 v[176:179], a[100:103], a[180:183], v[176:179]\n"
"s_andn2_b32 s56, s55, s58\n"
"v_permlane16_swap_b32 v208, v210\n"
"v_mfma_f32_16x16x32_bf16 v[180:183], a[104:107], a[176:179], v[180:183]\n"
"s_or_b32 s56, s56, s41\n"
"v_permlane16_swap_b32 v209, v211\n"
"v_mfma_f32_16x16x32_bf16 v[180:183], a[108:111], a[180:183], v[180:183]\n"
"ds_write_b128 v248, v[208:211] offset:8192\n"
"v_mfma_f32_16x16x32_bf16 v[184:187], a[112:115], a[176:179], v[184:187]\n"
"v_permlane16_swap_b32 v120, v122\n"
"v_mfma_f32_16x16x32_bf16 v[184:187], a[116:119], a[180:183], v[184:187]\n"
"v_mul_f32 v218, s45, v218\n"
"v_mul_f32 v219, s45, v219\n"
"v_mfma_f32_16x16x32_bf16 v[188:191], a[120:123], a[176:179], v[188:191]\n"
"s_add_u32 m0, s44, 32768\n"
"v_mul_f32 v222, s45, v222\n"
"v_mul_f32 v223, s45, v223\n"
"v_mfma_f32_16x16x32_bf16 v[188:191], a[124:127], a[180:183], v[188:191]\n"
"buffer_load_dwordx4 v240, s[16:19], s56 offen nt lds\n"
"v_mfma_f32_16x16x32_bf16 v[200:203], a[144:147], a[176:179], v[200:203]\n"
"v_cvt_pk_bf16_f32 v216, v216, v217\n"
"v_cvt_pk_bf16_f32 v217, v218, v219\n"
"v_mfma_f32_16x16x32_bf16 v[200:203], a[148:151], a[180:183], v[200:203]\n"
"v_cvt_pk_bf16_f32 v218, v220, v221\n"
"v_cvt_pk_bf16_f32 v219, v222, v223\n"
"v_mfma_f32_16x16x32_bf16 v[204:207], a[152:155], a[176:179], v[204:207]\n"
"v_permlane16_swap_b32 v216, v218\n"
"v_mfma_f32_16x16x32_bf16 v[204:207], a[156:159], a[180:183], v[204:207]\n"
"v_permlane16_swap_b32 v217, v219\n"
"v_mfma_f32_16x16x32_bf16 v[192:195], a[128:131], a[176:179], v[192:195]\n"
"ds_write_b128 v248, v[216:219] offset:10240\n"
"v_mfma_f32_16x16x32_bf16 v[192:195], a[132:135], a[180:183], v[192:195]\n"
"v_mul_f32 v176, s45, v176\n"
"v_mul_f32 v177, s45, v177\n"
"v_mfma_f32_16x16x32_bf16 v[196:199], a[136:139], a[176:179], v[196:199]\n"
"v_mul_f32 v178, s45, v178\n"
"v_mul_f32 v179, s45, v179\n"
"v_mfma_f32_16x16x32_bf16 v[196:199], a[140:143], a[180:183], v[196:199]\n"
"v_mul_f32 v200, s45, v200\n"
"v_mul_f32 v201, s45, v201\n"
"v_mfma_f32_16x16x32_bf16 v[108:111], a[232:235], a[184:187], v[108:111]\n"
"s_waitcnt lgkmcnt(1)\n"
"v_mul_f32 v202, s45, v202\n"
"v_mul_f32 v203, s45, v203\n"
"v_mfma_f32_16x16x32_bf16 v[108:111], a[236:239], a[188:191], v[108:111]\n"
"s_add_u32 s40, s56, s52\n"
"v_mul_f32 v206, s45, v206\n"
"v_mul_f32 v207, s45, v207\n"
"v_mfma_f32_16x16x32_bf16 v[104:107], a[224:227], a[184:187], v[104:107]\n"
"buffer_load_dwordx4 v240, s[16:19], s40 offen offset:1024 nt lds\n"
"v_mfma_f32_16x16x32_bf16 v[104:107], a[228:231], a[188:191], v[104:107]\n"
"s_waitcnt vmcnt(14)\n"
"v_cvt_pk_bf16_f32 v200, v200, v201\n"
"v_cvt_pk_bf16_f32 v201, v202, v203\n"
"v_mfma_f32_16x16x32_bf16 v[100:103], a[200:203], a[184:187], v[100:103]\n"
"s_barrier\n"
"v_mfma_f32_16x16x32_bf16 v[100:103], a[204:207], a[188:191], v[100:103]\n"
"v_cvt_pk_bf16_f32 v203, v206, v207\n"
"v_bfe_u32 v206, v242, 8, 2\n"
"v_mfma_f32_16x16x32_bf16 v[96:99], a[192:195], a[184:187], v[96:99]\n"
"v_permlane16_swap_b32 v201, v203\n"
"v_mfma_f32_16x16x32_bf16 v[96:99], a[196:199], a[188:191], v[96:99]\n"
"v_lshl_add_u32 v206, v206, 4, s43\n"
"v_xor_b32 v207, 64, v206\n"
"v_mfma_f32_16x16x32_bf16 v[0:3], a[192:195], a[160:163], v[0:3]\n"
"ds_read_b128 v[208:211], v206 offset:0\n"
"v_mul_f32 v180, s45, v180\n"
"v_mfma_f32_16x16x32_bf16 v[0:3], a[196:199], a[164:167], v[0:3]\n"
"ds_read_b128 v[212:215], v207 offset:0\n"
"v_mul_f32 v181, s45, v181\n"
"v_mfma_f32_16x16x32_bf16 v[4:7], a[200:203], a[160:163], v[4:7]\n"
"ds_read_b128 v[216:219], v206 offset:1024\n"
"v_mul_f32 v182, s45, v182\n"
"v_mfma_f32_16x16x32_bf16 v[4:7], a[204:207], a[164:167], v[4:7]\n"
"s_add_u32 s40, s56, s53\n"
"ds_read_b128 v[220:223], v207 offset:1024\n"
"v_mul_f32 v183, s45, v183\n"
"v_mfma_f32_16x16x32_bf16 v[8:11], a[208:211], a[160:163], v[8:11]\n"
"buffer_load_dwordx4 v240, s[16:19], s40 offen offset:2048 nt lds\n"
"v_mfma_f32_16x16x32_bf16 v[8:11], a[212:215], a[164:167], v[8:11]\n"
"v_cvt_pk_bf16_f32 v176, v176, v177\n"
"v_cvt_pk_bf16_f32 v177, v178, v179\n"
"v_mfma_f32_16x16x32_bf16 v[12:15], a[216:219], a[160:163], v[12:15]\n"
"v_cvt_pk_bf16_f32 v178, v180, v181\n"
"v_cvt_pk_bf16_f32 v179, v182, v183\n"
"v_mfma_f32_16x16x32_bf16 v[12:15], a[220:223], a[164:167], v[12:15]\n"
"v_permlane16_swap_b32 v176, v178\n"
"v_mfma_f32_16x16x32_bf16 v[16:19], a[224:227], a[160:163], v[16:19]\n"
"v_permlane16_swap_b32 v177, v179\n"
"v_mfma_f32_16x16x32_bf16 v[16:19], a[228:231], a[164:167], v[16:19]\n"
"v_mul_f32 v184, s45, v184\n"
"v_mul_f32 v185, s45, v185\n"
"v_mfma_f32_16x16x32_bf16 v[20:23], a[232:235], a[160:163], v[20:23]\n"
"v_mul_f32 v186, s45, v186\n"
"v_mul_f32 v187, s45, v187\n"
"v_mfma_f32_16x16x32_bf16 v[20:23], a[236:239], a[164:167], v[20:23]\n"
"v_mul_f32 v188, s45, v188\n"
"v_mul_f32 v189, s45, v189\n"
"v_mfma_f32_16x16x32_bf16 v[24:27], a[240:243], a[160:163], v[24:27]\n"
"v_mul_f32 v190, s45, v190\n"
"v_mul_f32 v191, s45, v191\n"
"v_mfma_f32_16x16x32_bf16 v[24:27], a[244:247], a[164:167], v[24:27]\n"
"s_add_u32 s40, s56, s54\n"
"v_cvt_pk_bf16_f32 v184, v184, v185\n"
"v_cvt_pk_bf16_f32 v185, v186, v187\n"
"v_mfma_f32_16x16x32_bf16 v[28:31], a[248:251], a[160:163], v[28:31]\n"
"buffer_load_dwordx4 v240, s[16:19], s40 offen offset:3072 nt lds\n"
"v_mfma_f32_16x16x32_bf16 v[28:31], a[252:255], a[164:167], v[28:31]\n"
"v_cvt_pk_bf16_f32 v186, v188, v189\n"
"v_cvt_pk_bf16_f32 v187, v190, v191\n"
"v_mfma_f32_16x16x32_bf16 v[60:63], a[248:251], a[168:171], v[60:63]\n"
"v_permlane16_swap_b32 v184, v186\n"
"v_mfma_f32_16x16x32_bf16 v[60:63], a[252:255], a[172:175], v[60:63]\n"
"v_permlane16_swap_b32 v185, v187\n"
"v_mfma_f32_16x16x32_bf16 v[56:59], a[240:243], a[168:171], v[56:59]\n"
"v_accvgpr_write_b32 a132, v184\n"
"v_accvgpr_write_b32 a133, v185\n"
"v_mfma_f32_16x16x32_bf16 v[56:59], a[244:247], a[172:175], v[56:59]\n"
"v_accvgpr_write_b32 a134, v186\n"
"v_accvgpr_write_b32 a135, v187\n"
"v_mfma_f32_16x16x32_bf16 v[52:55], a[232:235], a[168:171], v[52:55]\n"
"v_xor_b32 v207, 0x400, v248\n"
"v_mul_f32 v192, s45, v192\n"
"v_mfma_f32_16x16x32_bf16 v[52:55], a[236:239], a[172:175], v[52:55]\n"
"v_mul_f32 v193, s45, v193\n"
"v_mul_f32 v194, s45, v194\n"
"v_mfma_f32_16x16x32_bf16 v[48:51], a[224:227], a[168:171], v[48:51]\n"
"v_mul_f32 v195, s45, v195\n"
"v_mul_f32 v196, s45, v196\n"
"v_mfma_f32_16x16x32_bf16 v[48:51], a[228:231], a[172:175], v[48:51]\n"
"v_mul_f32 v197, s45, v197\n"
"v_mul_f32 v198, s45, v198\n"
"v_mfma_f32_16x16x32_bf16 v[44:47], a[216:219], a[168:171], v[44:47]\n"
"v_mul_f32 v199, s45, v199\n"
"v_cvt_pk_bf16_f32 v192, v192, v193\n"
"v_mfma_f32_16x16x32_bf16 v[44:47], a[220:223], a[172:175], v[44:47]\n"
"buffer_load_dword v253, v254, s[24:27], s63 offen\n"
"v_mfma_f32_16x16x32_bf16 v[40:43], a[208:211], a[168:171], v[40:43]\n"
"v_cvt_pk_bf16_f32 v193, v194, v195\n"
"v_cvt_pk_bf16_f32 v194, v196, v197\n"
"v_mfma_f32_16x16x32_bf16 v[40:43], a[212:215], a[172:175], v[40:43]\n"
"s_nop 0\n"
"v_permlane16_swap_b32 v192, v194\n"
"v_mfma_f32_16x16x32_bf16 v[36:39], a[200:203], a[168:171], v[36:39]\n"
"v_cvt_pk_bf16_f32 v195, v198, v199\n"
"v_accvgpr_write_b32 a136, v192\n"
"v_mfma_f32_16x16x32_bf16 v[36:39], a[204:207], a[172:175], v[36:39]\n"
"v_permlane16_swap_b32 v193, v195\n"
"v_mfma_f32_16x16x32_bf16 v[32:35], a[192:195], a[168:171], v[32:35]\n"
"v_accvgpr_write_b32 a137, v193\n"
"v_accvgpr_write_b32 a138, v194\n"
"v_mfma_f32_16x16x32_bf16 v[32:35], a[196:199], a[172:175], v[32:35]\n"
"v_accvgpr_write_b32 a139, v195\n"
"v_cvt_pk_bf16_f32 v123, v126, v127\n"
"v_mfma_f32_16x16x32_bf16 v[64:67], a[192:195], a[176:179], v[64:67]\n"
"ds_read_b128 a[192:195], v248 offset:0\n"
"v_mul_f32 v128, s45, v128\n"
"v_mfma_f32_16x16x32_bf16 v[64:67], a[196:199], a[180:183], v[64:67]\n"
"ds_read_b128 a[196:199], v207 offset:0\n"
"v_mul_f32 v129, s45, v129\n"
"v_mfma_f32_16x16x32_bf16 v[68:71], a[200:203], a[176:179], v[68:71]\n"
"ds_read_b128 a[200:203], v248 offset:2048\n"
"v_mul_f32 v130, s45, v130\n"
"v_mfma_f32_16x16x32_bf16 v[68:71], a[204:207], a[180:183], v[68:71]\n"
"buffer_load_dword v253, v254, s[24:27], s63 offen offset:2048\n"
"v_mfma_f32_16x16x32_bf16 v[72:75], a[208:211], a[176:179], v[72:75]\n"
"ds_read_b128 a[204:207], v207 offset:2048\n"
"v_mul_f32 v131, s45, v131\n"
"v_mfma_f32_16x16x32_bf16 v[72:75], a[212:215], a[180:183], v[72:75]\n"
"v_permlane16_swap_b32 v121, v123\n"
"v_mfma_f32_16x16x32_bf16 v[76:79], a[216:219], a[176:179], v[76:79]\n"
"v_mul_f32 v132, s45, v132\n"
"v_mul_f32 v133, s45, v133\n"
"v_mfma_f32_16x16x32_bf16 v[76:79], a[220:223], a[180:183], v[76:79]\n"
"v_mul_f32 v134, s45, v134\n"
"v_mul_f32 v135, s45, v135\n"
"v_mfma_f32_16x16x32_bf16 v[88:91], a[240:243], a[176:179], v[88:91]\n"
"v_cvt_pk_bf16_f32 v128, v128, v129\n"
"v_cvt_pk_bf16_f32 v129, v130, v131\n"
"v_mfma_f32_16x16x32_bf16 v[88:91], a[244:247], a[180:183], v[88:91]\n"
"v_cvt_pk_bf16_f32 v130, v132, v133\n"
"v_cvt_pk_bf16_f32 v131, v134, v135\n"
"v_mfma_f32_16x16x32_bf16 v[92:95], a[248:251], a[176:179], v[92:95]\n"
"v_permlane16_swap_b32 v128, v130\n"
"v_mfma_f32_16x16x32_bf16 v[92:95], a[252:255], a[180:183], v[92:95]\n"
"v_permlane16_swap_b32 v129, v131\n"
"v_mfma_f32_16x16x32_bf16 v[80:83], a[224:227], a[176:179], v[80:83]\n"
"v_accvgpr_write_b32 a104, v128\n"
"v_accvgpr_write_b32 a105, v129\n"
"v_mfma_f32_16x16x32_bf16 v[80:83], a[228:231], a[180:183], v[80:83]\n"
"v_accvgpr_write_b32 a106, v130\n"
"v_accvgpr_write_b32 a107, v131\n"
"v_mfma_f32_16x16x32_bf16 v[84:87], a[232:235], a[176:179], v[84:87]\n"
"v_mul_f32 v136, s45, v136\n"
"v_mul_f32 v137, s45, v137\n"
"v_mfma_f32_16x16x32_bf16 v[84:87], a[236:239], a[180:183], v[84:87]\n"
"v_mul_f32 v138, s45, v138\n"
"v_mul_f32 v139, s45, v139\n"
"s_waitcnt lgkmcnt(0)\n"
"v_mfma_f32_16x16x32_bf16 v[180:183], v[208:211], a[0:3], 0\n"
"v_mul_f32 v140, s45, v140\n"
"v_mul_f32 v141, s45, v141\n"
"v_mfma_f32_16x16x32_bf16 v[180:183], v[212:215], a[4:7], v[180:183]\n"
"v_mul_f32 v142, s45, v142\n"
"v_mul_f32 v143, s45, v143\n"
"v_mfma_f32_16x16x32_bf16 v[180:183], v[216:219], a[8:11], v[180:183]\n"
"v_cvt_pk_bf16_f32 v136, v136, v137\n"
"v_cvt_pk_bf16_f32 v137, v138, v139\n"
"v_mfma_f32_16x16x32_bf16 v[180:183], v[220:223], a[12:15], v[180:183]\n"
"v_cvt_pk_bf16_f32 v138, v140, v141\n"
"v_cvt_pk_bf16_f32 v139, v142, v143\n"
"v_mfma_f32_16x16x32_bf16 v[184:187], v[208:211], a[16:19], 0\n"
"v_permlane16_swap_b32 v136, v138\n"
"v_mfma_f32_16x16x32_bf16 v[184:187], v[212:215], a[20:23], v[184:187]\n"
"v_permlane16_swap_b32 v137, v139\n"
"v_mfma_f32_16x16x32_bf16 v[184:187], v[216:219], a[24:27], v[184:187]\n"
"v_accvgpr_write_b32 a108, v136\n"
"v_accvgpr_write_b32 a109, v137\n"
"v_mfma_f32_16x16x32_bf16 v[184:187], v[220:223], a[28:31], v[184:187]\n"
"v_accvgpr_write_b32 a110, v138\n"
"v_accvgpr_write_b32 a111, v139\n"
"v_mfma_f32_16x16x32_bf16 v[188:191], v[208:211], a[32:35], 0\n"
"v_mul_f32 v144, s45, v144\n"
"v_mul_f32 v145, s45, v145\n"
"v_mfma_f32_16x16x32_bf16 v[188:191], v[212:215], a[36:39], v[188:191]\n"
"v_xor_b32 v180, s77, v180\n"
"v_xor_b32 v181, s77, v181\n"
"v_mfma_f32_16x16x32_bf16 v[188:191], v[216:219], a[40:43], v[188:191]\n"
"v_xor_b32 v182, s77, v182\n"
"v_xor_b32 v183, s77, v183\n"
"v_mfma_f32_16x16x32_bf16 v[188:191], v[220:223], a[44:47], v[188:191]\n"
"v_mul_f32 v146, s45, v146\n"
"v_mul_f32 v147, s45, v147\n"
"v_mfma_f32_16x16x32_bf16 v[192:195], v[208:211], a[192:195], 0\n"
"ds_read_b128 v[208:211], v228 offset:0\n"
"v_mul_f32 v148, s45, v148\n"
"v_mfma_f32_16x16x32_bf16 v[192:195], v[212:215], a[196:199], v[192:195]\n"
"ds_read_b128 v[212:215], v229 offset:0\n"
"v_xor_b32 v184, s77, v184\n"
"v_mfma_f32_16x16x32_bf16 v[192:195], v[216:219], a[200:203], v[192:195]\n"
"ds_read_b128 v[216:219], v228 offset:1024\n"
"v_xor_b32 v185, s77, v185\n"
"v_mfma_f32_16x16x32_bf16 v[192:195], v[220:223], a[204:207], v[192:195]\n"
"s_waitcnt lgkmcnt(2)\n"
"ds_read_b128 v[220:223], v229 offset:1024\n"
"v_xor_b32 v186, s77, v186\n"
"v_mfma_f32_16x16x32_bf16 v[124:127], v[208:211], a[0:3], v[180:183]\n"
"s_waitcnt lgkmcnt(2)\n"
"v_xor_b32 v187, s77, v187\n"
"v_mul_f32 v149, s45, v149\n"
"v_mfma_f32_16x16x32_bf16 v[124:127], v[212:215], a[4:7], v[124:127]\n"
"s_waitcnt lgkmcnt(1)\n"
"v_xor_b32 v188, s77, v188\n"
"v_xor_b32 v189, s77, v189\n"
"v_mfma_f32_16x16x32_bf16 v[124:127], v[216:219], a[8:11], v[124:127]\n"
"s_waitcnt lgkmcnt(0)\n"
"v_xor_b32 v190, s77, v190\n"
"v_xor_b32 v191, s77, v191\n"
"v_mfma_f32_16x16x32_bf16 v[124:127], v[220:223], a[12:15], v[124:127]\n"
"v_mul_f32 v150, s45, v150\n"
"v_mul_f32 v151, s45, v151\n"
"v_mfma_f32_16x16x32_bf16 v[128:131], v[216:219], a[24:27], v[184:187]\n"
"v_cvt_pk_bf16_f32 v144, v144, v145\n"
"v_cvt_pk_bf16_f32 v145, v146, v147\n"
"v_mfma_f32_16x16x32_bf16 v[128:131], v[220:223], a[28:31], v[128:131]\n"
"v_xor_b32 v192, s77, v192\n"
"v_xor_b32 v193, s77, v193\n"
"v_mfma_f32_16x16x32_bf16 v[128:131], v[208:211], a[16:19], v[128:131]\n"
"v_xor_b32 v194, s77, v194\n"
"v_xor_b32 v195, s77, v195\n"
"v_mfma_f32_16x16x32_bf16 v[128:131], v[212:215], a[20:23], v[128:131]\n"
"v_cvt_pk_bf16_f32 v146, v148, v149\n"
"v_cvt_pk_bf16_f32 v147, v150, v151\n"
"v_mfma_f32_16x16x32_bf16 v[132:135], v[208:211], a[32:35], v[188:191]\n"
"v_permlane16_swap_b32 v144, v146\n"
"v_mfma_f32_16x16x32_bf16 v[132:135], v[212:215], a[36:39], v[132:135]\n"
"v_permlane16_swap_b32 v145, v147\n"
"v_mfma_f32_16x16x32_bf16 v[132:135], v[216:219], a[40:43], v[132:135]\n"
"v_exp_f32 v124, v124\n"
"v_mfma_f32_16x16x32_bf16 v[132:135], v[220:223], a[44:47], v[132:135]\n"
"v_exp_f32 v125, v125\n"
"v_mfma_f32_16x16x32_bf16 v[136:139], v[216:219], a[200:203], v[192:195]\n"
"ds_read_b128 v[216:219], v228 offset:5120\n"
"v_accvgpr_write_b32 a112, v144\n"
"v_mfma_f32_16x16x32_bf16 v[136:139], v[220:223], a[204:207], v[136:139]\n"
"ds_read_b128 v[220:223], v229 offset:5120\n"
"v_accvgpr_write_b32 a113, v145\n"
"v_mfma_f32_16x16x32_bf16 v[136:139], v[208:211], a[192:195], v[136:139]\n"
"ds_read_b128 v[208:211], v228 offset:4096\n"
"v_accvgpr_write_b32 a114, v146\n"
"v_mfma_f32_16x16x32_bf16 v[136:139], v[212:215], a[196:199], v[136:139]\n"
"s_waitcnt lgkmcnt(2)\n"
"ds_read_b128 v[212:215], v229 offset:4096\n"
"v_accvgpr_write_b32 a115, v147\n"
"v_mfma_f32_16x16x32_bf16 v[140:143], v[216:219], a[8:11], v[180:183]\n"
"s_waitcnt lgkmcnt(2)\n"
"v_exp_f32 v126, v126\n"
"v_mfma_f32_16x16x32_bf16 v[140:143], v[220:223], a[12:15], v[140:143]\n"
"s_waitcnt lgkmcnt(1)\n"
"v_exp_f32 v127, v127\n"
"v_mfma_f32_16x16x32_bf16 v[140:143], v[208:211], a[0:3], v[140:143]\n"
"s_waitcnt lgkmcnt(0)\n"
"v_exp_f32 v128, v128\n"
"v_mfma_f32_16x16x32_bf16 v[140:143], v[212:215], a[4:7], v[140:143]\n"
"v_exp_f32 v129, v129\n"
"v_mfma_f32_16x16x32_bf16 v[144:147], v[208:211], a[16:19], v[184:187]\n"
"v_mul_f32 v152, s45, v152\n"
"v_mul_f32 v153, s45, v153\n"
"v_mfma_f32_16x16x32_bf16 v[144:147], v[212:215], a[20:23], v[144:147]\n"
"v_mul_f32 v154, s45, v154\n"
"v_mul_f32 v155, s45, v155\n"
"v_mfma_f32_16x16x32_bf16 v[144:147], v[216:219], a[24:27], v[144:147]\n"
"v_mul_f32 v156, s45, v156\n"
"v_mul_f32 v157, s45, v157\n"
"v_mfma_f32_16x16x32_bf16 v[144:147], v[220:223], a[28:31], v[144:147]\n"
"v_mul_f32 v158, s45, v158\n"
"v_mul_f32 v159, s45, v159\n"
"v_mfma_f32_16x16x32_bf16 v[148:151], v[216:219], a[40:43], v[188:191]\n"
"v_exp_f32 v130, v130\n"
"v_mfma_f32_16x16x32_bf16 v[148:151], v[220:223], a[44:47], v[148:151]\n"
"v_exp_f32 v131, v131\n"
"v_mfma_f32_16x16x32_bf16 v[148:151], v[208:211], a[32:35], v[148:151]\n"
"v_cvt_pk_bf16_f32 v152, v152, v153\n"
"v_cvt_pk_bf16_f32 v153, v154, v155\n"
"v_cvt_pk_bf16_f32 v154, v156, v157\n"
"v_cvt_pk_bf16_f32 v155, v158, v159\n"
"v_mfma_f32_16x16x32_bf16 v[148:151], v[212:215], a[36:39], v[148:151]\n"
"v_permlane16_swap_b32 v152, v154\n"
"v_permlane16_swap_b32 v153, v155\n"
"v_accvgpr_write_b32 a116, v152\n"
"v_accvgpr_write_b32 a117, v153\n"
"v_cvt_pk_bf16_f32 v152, v124, v125\n"
"v_cvt_pk_bf16_f32 v153, v126, v127\n"
"v_mfma_f32_16x16x32_bf16 v[124:127], v[208:211], a[192:195], v[192:195]\n"
"ds_read_b128 v[208:211], v230 offset:0\n"
"v_mul_f32 v160, s45, v160\n"
"v_mfma_f32_16x16x32_bf16 v[124:127], v[212:215], a[196:199], v[124:127]\n"
"ds_read_b128 v[212:215], v231 offset:0\n"
"v_mul_f32 v161, s45, v161\n"
"v_mfma_f32_16x16x32_bf16 v[124:127], v[216:219], a[200:203], v[124:127]\n"
"ds_read_b128 v[216:219], v230 offset:1024\n"
"v_mul_f32 v162, s45, v162\n"
"v_mul_f32 v163, s45, v163\n"
"v_mul_f32 v164, s45, v164\n"
"v_mul_f32 v165, s45, v165\n"
"v_mul_f32 v166, s45, v166\n"
"v_mul_f32 v167, s45, v167\n"
"v_cvt_pk_bf16_f32 v160, v160, v161\n"
"v_cvt_pk_bf16_f32 v161, v162, v163\n"
"v_cvt_pk_bf16_f32 v162, v164, v165\n"
"v_cvt_pk_bf16_f32 v163, v166, v167\n"
"v_mfma_f32_16x16x32_bf16 v[124:127], v[220:223], a[204:207], v[124:127]\n"
"s_waitcnt lgkmcnt(2)\n"
"ds_read_b128 v[220:223], v231 offset:1024\n"
"v_permlane16_swap_b32 v160, v162\n"
"v_permlane16_swap_b32 v161, v163\n"
"v_accvgpr_write_b32 a120, v160\n"
"v_accvgpr_write_b32 a121, v161\n"
"v_cvt_pk_bf16_f32 v160, v128, v129\n"
"v_cvt_pk_bf16_f32 v161, v130, v131\n"
"v_mfma_f32_16x16x32_bf16 v[128:131], v[208:211], a[0:3], v[180:183]\n"
"s_waitcnt lgkmcnt(2)\n"
"v_exp_f32 v132, v132\n"
"v_mfma_f32_16x16x32_bf16 v[128:131], v[212:215], a[4:7], v[128:131]\n"
"s_waitcnt lgkmcnt(1)\n"
"v_exp_f32 v133, v133\n"
"v_mfma_f32_16x16x32_bf16 v[128:131], v[216:219], a[8:11], v[128:131]\n"
"s_waitcnt lgkmcnt(0)\n"
"v_mul_f32 v168, s45, v168\n"
"v_mul_f32 v169, s45, v169\n"
"v_mul_f32 v170, s45, v170\n"
"v_mul_f32 v171, s45, v171\n"
"v_mul_f32 v172, s45, v172\n"
"v_mul_f32 v173, s45, v173\n"
"v_mul_f32 v174, s45, v174\n"
"v_mul_f32 v175, s45, v175\n"
"v_cvt_pk_bf16_f32 v168, v168, v169\n"
"v_cvt_pk_bf16_f32 v169, v170, v171\n"
"v_cvt_pk_bf16_f32 v170, v172, v173\n"
"v_cvt_pk_bf16_f32 v171, v174, v175\n"
"v_exp_f32 v134, v134\n"
"v_exp_f32 v135, v135\n"
"v_mfma_f32_16x16x32_bf16 v[128:131], v[220:223], a[12:15], v[128:131]\n"
"v_permlane16_swap_b32 v168, v170\n"
"v_permlane16_swap_b32 v169, v171\n"
"v_accvgpr_write_b32 a124, v168\n"
"v_accvgpr_write_b32 a125, v169\n"
"v_cvt_pk_bf16_f32 v168, v132, v133\n"
"v_cvt_pk_bf16_f32 v169, v134, v135\n"
"v_mfma_f32_16x16x32_bf16 v[132:135], v[216:219], a[24:27], v[184:187]\n"
"v_exp_f32 v136, v136\n"
"v_mfma_f32_16x16x32_bf16 v[132:135], v[220:223], a[28:31], v[132:135]\n"
"v_exp_f32 v137, v137\n"
"v_mfma_f32_16x16x32_bf16 v[132:135], v[208:211], a[16:19], v[132:135]\n"
"v_exp_f32 v138, v138\n"
"v_exp_f32 v139, v139\n"
"v_mfma_f32_16x16x32_bf16 v[132:135], v[212:215], a[20:23], v[132:135]\n"
"v_accvgpr_write_b32 a128, v176\n"
"v_accvgpr_write_b32 a129, v177\n"
"v_cvt_pk_bf16_f32 v176, v136, v137\n"
"v_cvt_pk_bf16_f32 v177, v138, v139\n"
"v_mfma_f32_16x16x32_bf16 v[136:139], v[208:211], a[32:35], v[188:191]\n"
"ds_read_b128 v[208:211], v230 offset:4096\n"
"v_accvgpr_write_b32 a118, v154\n"
"v_mfma_f32_16x16x32_bf16 v[136:139], v[212:215], a[36:39], v[136:139]\n"
"ds_read_b128 v[212:215], v231 offset:4096\n"
"v_accvgpr_write_b32 a119, v155\n"
"v_mfma_f32_16x16x32_bf16 v[136:139], v[216:219], a[40:43], v[136:139]\n"
"ds_read_b128 v[216:219], v230 offset:5120\n"
"v_exp_f32 v140, v140\n"
"v_exp_f32 v141, v141\n"
"v_exp_f32 v142, v142\n"
"v_exp_f32 v143, v143\n"
"v_mfma_f32_16x16x32_bf16 v[136:139], v[220:223], a[44:47], v[136:139]\n"
"s_waitcnt lgkmcnt(2)\n"
"ds_read_b128 v[220:223], v231 offset:5120\n"
"v_cvt_pk_bf16_f32 v154, v140, v141\n"
"v_cvt_pk_bf16_f32 v155, v142, v143\n"
"v_mfma_f32_16x16x32_bf16 v[140:143], v[208:211], a[0:3], v[180:183]\n"
"s_waitcnt lgkmcnt(2)\n"
"v_exp_f32 v144, v144\n"
"v_mfma_f32_16x16x32_bf16 v[140:143], v[212:215], a[4:7], v[140:143]\n"
"s_waitcnt lgkmcnt(1)\n"
"v_exp_f32 v145, v145\n"
"v_mfma_f32_16x16x32_bf16 v[140:143], v[216:219], a[8:11], v[140:143]\n"
"s_waitcnt lgkmcnt(0)\n"
"v_exp_f32 v146, v146\n"
"v_exp_f32 v147, v147\n"
"v_mfma_f32_16x16x32_bf16 v[140:143], v[220:223], a[12:15], v[140:143]\n"
"v_accvgpr_write_b32 a122, v162\n"
"v_accvgpr_write_b32 a123, v163\n"
"v_cvt_pk_bf16_f32 v162, v144, v145\n"
"v_cvt_pk_bf16_f32 v163, v146, v147\n"
"v_mfma_f32_16x16x32_bf16 v[144:147], v[216:219], a[24:27], v[184:187]\n"
"v_mul_f32 v96, s45, v96\n"
"v_mul_f32 v97, s45, v97\n"
"v_mul_f32 v98, s45, v98\n"
"v_mul_f32 v99, s45, v99\n"
"v_mul_f32 v100, s45, v100\n"
"v_mul_f32 v101, s45, v101\n"
"v_mul_f32 v102, s45, v102\n"
"v_mul_f32 v103, s45, v103\n"
"v_cvt_pk_bf16_f32 v96, v96, v97\n"
"v_cvt_pk_bf16_f32 v97, v98, v99\n"
"v_cvt_pk_bf16_f32 v98, v100, v101\n"
"v_cvt_pk_bf16_f32 v99, v102, v103\n"
"v_mfma_f32_16x16x32_bf16 v[144:147], v[220:223], a[28:31], v[144:147]\n"
"v_permlane16_swap_b32 v96, v98\n"
"v_permlane16_swap_b32 v97, v99\n"
"v_mul_f32 v104, s45, v104\n"
"v_mul_f32 v105, s45, v105\n"
"v_mul_f32 v106, s45, v106\n"
"v_mul_f32 v107, s45, v107\n"
"v_mul_f32 v108, s45, v108\n"
"v_mul_f32 v109, s45, v109\n"
"v_mul_f32 v110, s45, v110\n"
"v_mul_f32 v111, s45, v111\n"
"v_cvt_pk_bf16_f32 v104, v104, v105\n"
"v_cvt_pk_bf16_f32 v105, v106, v107\n"
"v_cvt_pk_bf16_f32 v106, v108, v109\n"
"v_cvt_pk_bf16_f32 v107, v110, v111\n"
"v_mfma_f32_16x16x32_bf16 v[144:147], v[208:211], a[16:19], v[144:147]\n"
"ds_write_b128 v248, v[96:99] offset:12288\n"
"v_permlane16_swap_b32 v104, v106\n"
"v_permlane16_swap_b32 v105, v107\n"
"v_exp_f32 v148, v148\n"
"v_exp_f32 v149, v149\n"
"v_exp_f32 v150, v150\n"
"v_exp_f32 v151, v151\n"
"v_mfma_f32_16x16x32_bf16 v[144:147], v[212:215], a[20:23], v[144:147]\n"
"ds_write_b128 v248, v[104:107] offset:14336\n"
"v_accvgpr_write_b32 a126, v170\n"
"v_accvgpr_write_b32 a127, v171\n"
"v_cvt_pk_bf16_f32 v170, v148, v149\n"
"v_cvt_pk_bf16_f32 v171, v150, v151\n"
"v_mfma_f32_16x16x32_bf16 v[148:151], v[208:211], a[32:35], v[188:191]\n"
"v_exp_f32 v124, v124\n"
"v_mfma_f32_16x16x32_bf16 v[148:151], v[212:215], a[36:39], v[148:151]\n"
"s_cbranch_vccnz LPCE0_%=\nLPKE0_%=:\n"
"v_accvgpr_write_b32 a130, v178\n"
"v_mfma_f32_16x16x32_bf16 v[148:151], v[216:219], a[40:43], v[148:151]\n"
"v_exp_f32 v125, v125\n"
"v_mfma_f32_16x16x32_bf16 v[148:151], v[220:223], a[44:47], v[148:151]\n"
"v_exp_f32 v126, v126\n"
"s_waitcnt vmcnt(10) lgkmcnt(0)\n"
"v_exp_f32 v127, v127\n"
"s_barrier\n"
"ds_read_b128 v[208:211], v228 offset:16384\n"
"ds_read_b128 v[212:215], v229 offset:16384\n"
"ds_read_b128 v[216:219], v228 offset:17408\n"
"ds_read_b128 v[220:223], v229 offset:17408\n"
"v_accvgpr_write_b32 a131, v179\n"
"v_cvt_pk_bf16_f32 v178, v124, v125\n"
"v_cvt_pk_bf16_f32 v179, v126, v127\n"
"s_waitcnt lgkmcnt(3)\n"
"v_mfma_f32_16x16x32_bf16 v[124:127], v[208:211], a[0:3], v[180:183]\n"
"s_add_u32 m0, s44, 98304\n"
"v_exp_f32 v128, v128\n"
"s_waitcnt lgkmcnt(2)\n"
"v_mfma_f32_16x16x32_bf16 v[124:127], v[212:215], a[4:7], v[124:127]\n"
"buffer_load_dwordx4 v240, s[20:23], s56 offen nt lds\n"
"s_waitcnt lgkmcnt(1)\n"
"v_mfma_f32_16x16x32_bf16 v[124:127], v[216:219], a[8:11], v[124:127]\n"
"s_waitcnt lgkmcnt(0)\n"
"v_exp_f32 v129, v129\n"
"v_exp_f32 v130, v130\n"
"v_exp_f32 v131, v131\n"
"v_mfma_f32_16x16x32_bf16 v[124:127], v[220:223], a[12:15], v[124:127]\n"
"v_cvt_pk_bf16_f32 v156, v128, v129\n"
"v_cvt_pk_bf16_f32 v157, v130, v131\n"
"v_mfma_f32_16x16x32_bf16 v[128:131], v[216:219], a[24:27], v[184:187]\n"
"v_exp_f32 v132, v132\n"
"v_mfma_f32_16x16x32_bf16 v[128:131], v[220:223], a[28:31], v[128:131]\n"
"v_exp_f32 v133, v133\n"
"v_mfma_f32_16x16x32_bf16 v[128:131], v[208:211], a[16:19], v[128:131]\n"
"v_exp_f32 v134, v134\n"
"v_exp_f32 v135, v135\n"
"v_mfma_f32_16x16x32_bf16 v[128:131], v[212:215], a[20:23], v[128:131]\n"
"v_cvt_pk_bf16_f32 v164, v132, v133\n"
"v_cvt_pk_bf16_f32 v165, v134, v135\n"
"v_mfma_f32_16x16x32_bf16 v[132:135], v[208:211], a[32:35], v[188:191]\n"
"v_exp_f32 v136, v136\n"
"v_mfma_f32_16x16x32_bf16 v[132:135], v[212:215], a[36:39], v[132:135]\n"
"v_exp_f32 v137, v137\n"
"v_mfma_f32_16x16x32_bf16 v[132:135], v[216:219], a[40:43], v[132:135]\n"
"ds_read_b64_tr_b16 a[244:245], v234 offset:0\n"
"ds_read_b64_tr_b16 a[246:247], v234 offset:4096\n"
"v_exp_f32 v138, v138\n"
"v_exp_f32 v139, v139\n"
"v_mfma_f32_16x16x32_bf16 v[132:135], v[220:223], a[44:47], v[132:135]\n"
"v_cvt_pk_bf16_f32 v172, v136, v137\n"
"v_cvt_pk_bf16_f32 v173, v138, v139\n"
"v_mfma_f32_16x16x32_bf16 v[136:139], v[216:219], a[200:203], v[192:195]\n"
"s_add_u32 s40, s56, s52\n"
"ds_read_b64_tr_b16 a[248:249], v238 offset:0\n"
"ds_read_b64_tr_b16 a[250:251], v238 offset:4096\n"
"v_mfma_f32_16x16x32_bf16 v[136:139], v[220:223], a[204:207], v[136:139]\n"
"buffer_load_dwordx4 v240, s[20:23], s40 offen offset:1024 nt lds\n"
"v_mfma_f32_16x16x32_bf16 v[136:139], v[208:211], a[192:195], v[136:139]\n"
"v_exp_f32 v140, v140\n"
"v_mfma_f32_16x16x32_bf16 v[136:139], v[212:215], a[196:199], v[136:139]\n"
"s_waitcnt lgkmcnt(2)\n"
"v_mul_f32 v8, s45, v8\n"
"v_mul_f32 v9, s45, v9\n"
"v_mul_f32 v10, s45, v10\n"
"v_mul_f32 v11, s45, v11\n"
"v_mul_f32 v12, s45, v12\n"
"v_mul_f32 v13, s45, v13\n"
"v_mul_f32 v14, s45, v14\n"
"v_mul_f32 v15, s45, v15\n"
"v_cvt_pk_bf16_f32 v8, v8, v9\n"
"v_cvt_pk_bf16_f32 v9, v10, v11\n"
"v_cvt_pk_bf16_f32 v10, v12, v13\n"
"v_cvt_pk_bf16_f32 v11, v14, v15\n"
"v_exp_f32 v141, v141\n"
"v_exp_f32 v142, v142\n"
"v_exp_f32 v143, v143\n"
"v_mfma_f32_16x16x32_bf16 a[216:219], v[176:179], a[244:247], 0\n"
"v_permlane16_swap_b32 v8, v10\n"
"v_permlane16_swap_b32 v9, v11\n"
"v_accvgpr_write_b32 a148, v8\n"
"v_accvgpr_write_b32 a149, v9\n"
"v_accvgpr_write_b32 a150, v10\n"
"v_accvgpr_write_b32 a151, v11\n"
"v_cvt_pk_bf16_f32 v158, v140, v141\n"
"v_cvt_pk_bf16_f32 v159, v142, v143\n"
"v_mfma_f32_16x16x32_bf16 v[8:11], v[152:155], a[244:247], 0\n"
"s_waitcnt lgkmcnt(0)\n"
"ds_read_b64_tr_b16 v[224:225], v239 offset:0\n"
"ds_read_b64_tr_b16 v[226:227], v239 offset:4096\n"
"v_mul_f32 v40, s45, v40\n"
"v_mul_f32 v41, s45, v41\n"
"v_mul_f32 v42, s45, v42\n"
"v_mul_f32 v43, s45, v43\n"
"v_mul_f32 v44, s45, v44\n"
"v_mul_f32 v45, s45, v45\n"
"v_mul_f32 v46, s45, v46\n"
"v_mul_f32 v47, s45, v47\n"
"v_cvt_pk_bf16_f32 v40, v40, v41\n"
"v_cvt_pk_bf16_f32 v41, v42, v43\n"
"v_cvt_pk_bf16_f32 v42, v44, v45\n"
"v_cvt_pk_bf16_f32 v43, v46, v47\n"
"v_exp_f32 v144, v144\n"
"v_exp_f32 v145, v145\n"
"v_exp_f32 v146, v146\n"
"v_exp_f32 v147, v147\n"
"v_mfma_f32_16x16x32_bf16 v[8:11], v[156:159], a[248:251], v[8:11]\n"
"ds_read_b64_tr_b16 a[252:253], v235 offset:0\n"
"ds_read_b64_tr_b16 a[254:255], v235 offset:4096\n"
"v_permlane16_swap_b32 v40, v42\n"
"v_permlane16_swap_b32 v41, v43\n"
"v_accvgpr_write_b32 a164, v40\n"
"v_accvgpr_write_b32 a165, v41\n"
"v_accvgpr_write_b32 a166, v42\n"
"v_accvgpr_write_b32 a167, v43\n"
"v_cvt_pk_bf16_f32 v166, v144, v145\n"
"v_cvt_pk_bf16_f32 v167, v146, v147\n"
"v_mfma_f32_16x16x32_bf16 v[40:43], v[160:163], a[244:247], 0\n"
"v_mul_f32 v72, s45, v72\n"
"v_mul_f32 v73, s45, v73\n"
"v_mul_f32 v74, s45, v74\n"
"v_mul_f32 v75, s45, v75\n"
"v_mul_f32 v76, s45, v76\n"
"v_mul_f32 v77, s45, v77\n"
"v_mul_f32 v78, s45, v78\n"
"v_mul_f32 v79, s45, v79\n"
"v_cvt_pk_bf16_f32 v72, v72, v73\n"
"v_cvt_pk_bf16_f32 v73, v74, v75\n"
"v_cvt_pk_bf16_f32 v74, v76, v77\n"
"v_cvt_pk_bf16_f32 v75, v78, v79\n"
"v_exp_f32 v148, v148\n"
"v_exp_f32 v149, v149\n"
"v_exp_f32 v150, v150\n"
"v_exp_f32 v151, v151\n"
"v_mfma_f32_16x16x32_bf16 v[40:43], v[164:167], a[248:251], v[40:43]\n"
"v_permlane16_swap_b32 v72, v74\n"
"v_permlane16_swap_b32 v73, v75\n"
"v_accvgpr_write_b32 a180, v72\n"
"v_accvgpr_write_b32 a181, v73\n"
"v_accvgpr_write_b32 a182, v74\n"
"v_accvgpr_write_b32 a183, v75\n"
"v_cvt_pk_bf16_f32 v174, v148, v149\n"
"v_cvt_pk_bf16_f32 v175, v150, v151\n"
"v_mfma_f32_16x16x32_bf16 v[72:75], v[168:171], a[244:247], 0\n"
"ds_read_b128 v[216:219], v228 offset:21504\n"
"v_mul_f32 v204, s45, v204\n"
"v_mfma_f32_16x16x32_bf16 v[72:75], v[172:175], a[248:251], v[72:75]\n"
"s_waitcnt lgkmcnt(3)\n"
"ds_read_b128 v[220:223], v229 offset:21504\n"
"v_mul_f32 v205, s45, v205\n"
"v_mfma_f32_16x16x32_bf16 v[76:79], v[172:175], v[224:227], 0\n"
"s_waitcnt lgkmcnt(2)\n"
"ds_read_b128 v[208:211], v228 offset:20480\n"
"v_cvt_pk_bf16_f32 v202, v204, v205\n"
"v_mfma_f32_16x16x32_bf16 v[76:79], v[168:171], a[252:255], v[76:79]\n"
"s_add_u32 s40, s56, s53\n"
"ds_read_b128 v[212:215], v229 offset:20480\n"
"v_accvgpr_write_b32 a240, v249\n"
"v_mfma_f32_16x16x32_bf16 v[44:47], v[164:167], v[224:227], 0\n"
"buffer_load_dwordx4 v240, s[20:23], s40 offen offset:2048 nt lds\n"
"v_mfma_f32_16x16x32_bf16 v[44:47], v[160:163], a[252:255], v[44:47]\n"
"ds_read_b64_tr_b16 a[244:245], v234 offset:1024\n"
"v_accvgpr_write_b32 a241, v249\n"
"v_mfma_f32_16x16x32_bf16 v[12:15], v[156:159], v[224:227], 0\n"
"ds_read_b64_tr_b16 a[246:247], v234 offset:5120\n"
"v_accvgpr_write_b32 a242, v249\n"
"v_mfma_f32_16x16x32_bf16 v[12:15], v[152:155], a[252:255], v[12:15]\n"
"v_accvgpr_write_b32 a243, v249\n"
"v_mov_b32 v196, 0\n"
"v_mov_b32 v197, 0\n"
"v_mov_b32 v198, 0\n"
"v_mov_b32 v199, 0\n"
"v_mfma_f32_16x16x32_bf16 a[220:223], v[176:179], a[252:255], 0\n"
"v_permlane16_swap_b32 v200, v202\n"
"v_accvgpr_write_b32 a142, v202\n"
"v_accvgpr_write_b32 a143, v203\n"
"v_mov_b32 v202, 0\n"
"v_mov_b32 v203, 0\n"
"v_mov_b32 v204, 0\n"
"v_mov_b32 v205, 0\n"
"v_smfmac_f32_16x16x64_bf16 v[196:199], a[240:243], v[152:159], v241\n"
"ds_read_b64_tr_b16 a[248:249], v238 offset:1024\n"
"v_mul_f32 v24, s45, v24\n"
"v_mfma_f32_16x16x32_bf16 v[202:205], a[240:243], v[176:179], v[202:205]\n"
"s_waitcnt lgkmcnt(4)\n"
"ds_read_b64_tr_b16 a[250:251], v238 offset:5120\n"
"v_mul_f32 v25, s45, v25\n"
"v_mfma_f32_16x16x32_bf16 v[140:143], v[216:219], a[8:11], v[180:183]\n"
"ds_read_b64_tr_b16 v[224:225], v239 offset:1024\n"
"v_mul_f32 v26, s45, v26\n"
"v_mfma_f32_16x16x32_bf16 v[140:143], v[220:223], a[12:15], v[140:143]\n"
"ds_read_b64_tr_b16 v[226:227], v239 offset:5120\n"
"v_mul_f32 v27, s45, v27\n"
"v_mfma_f32_16x16x32_bf16 v[140:143], v[208:211], a[0:3], v[140:143]\n"
"s_waitcnt lgkmcnt(5)\n"
"ds_read_b64_tr_b16 a[252:253], v235 offset:1024\n"
"v_mul_f32 v28, s45, v28\n"
"v_mfma_f32_16x16x32_bf16 v[140:143], v[212:215], a[4:7], v[140:143]\n"
"ds_read_b64_tr_b16 a[254:255], v235 offset:5120\n"
"v_mul_f32 v29, s45, v29\n"
"v_mfma_f32_16x16x32_bf16 v[144:147], v[208:211], a[16:19], v[184:187]\n"
"v_exp_f32 v124, v124\n"
"v_mfma_f32_16x16x32_bf16 v[144:147], v[212:215], a[20:23], v[144:147]\n"
"s_add_u32 s40, s56, s54\n"
"v_exp_f32 v125, v125\n"
"v_mfma_f32_16x16x32_bf16 v[144:147], v[216:219], a[24:27], v[144:147]\n"
"buffer_load_dwordx4 v240, s[20:23], s40 offen offset:3072 nt lds\n"
"v_mfma_f32_16x16x32_bf16 v[144:147], v[220:223], a[28:31], v[144:147]\n"
"s_add_u32 s55, s55, s59\n"
"v_exp_f32 v126, v126\n"
"v_mfma_f32_16x16x32_bf16 v[148:151], v[216:219], a[40:43], v[188:191]\n"
"s_add_u32 s41, s55, s57\n"
"v_exp_f32 v127, v127\n"
"v_mfma_f32_16x16x32_bf16 v[148:151], v[220:223], a[44:47], v[148:151]\n"
"s_and_b32 s41, s41, s58\n"
"v_cvt_pk_bf16_f32 v96, v124, v125\n"
"v_cvt_pk_bf16_f32 v97, v126, v127\n"
"v_mfma_f32_16x16x32_bf16 v[148:151], v[208:211], a[32:35], v[148:151]\n"
"s_andn2_b32 s56, s55, s58\n"
"v_mul_f32 v30, s45, v30\n"
"v_mul_f32 v31, s45, v31\n"
"v_mfma_f32_16x16x32_bf16 v[148:151], v[212:215], a[36:39], v[148:151]\n"
"s_or_b32 s56, s56, s41\n"
"v_cvt_pk_bf16_f32 v24, v24, v25\n"
"v_cvt_pk_bf16_f32 v25, v26, v27\n"
"v_mfma_f32_16x16x32_bf16 v[124:127], v[208:211], a[192:195], v[192:195]\n"
"ds_read_b128 v[208:211], v230 offset:16384\n"
"v_cvt_pk_bf16_f32 v26, v28, v29\n"
"v_mfma_f32_16x16x32_bf16 v[124:127], v[212:215], a[196:199], v[124:127]\n"
"ds_read_b128 v[212:215], v231 offset:16384\n"
"v_cvt_pk_bf16_f32 v27, v30, v31\n"
"v_mfma_f32_16x16x32_bf16 v[124:127], v[216:219], a[200:203], v[124:127]\n"
"v_permlane16_swap_b32 v24, v26\n"
"v_mfma_f32_16x16x32_bf16 v[124:127], v[220:223], a[204:207], v[124:127]\n"
"s_waitcnt lgkmcnt(2)\n"
"v_permlane16_swap_b32 v25, v27\n"
"v_mfma_f32_16x16x32_bf16 a[232:235], v[176:179], a[244:247], 0\n"
"ds_read_b128 v[216:219], v230 offset:17408\n"
"v_accvgpr_write_b32 a156, v24\n"
"v_accvgpr_write_b32 a157, v25\n"
"v_accvgpr_write_b32 a158, v26\n"
"v_accvgpr_write_b32 a159, v27\n"
"v_mfma_f32_16x16x32_bf16 v[24:27], v[152:155], a[244:247], 0\n"
"s_add_u32 m0, s44, 49152\n"
"ds_read_b128 v[220:223], v231 offset:17408\n"
"v_mul_f32 v56, s45, v56\n"
"v_mul_f32 v57, s45, v57\n"
"v_mul_f32 v58, s45, v58\n"
"v_mul_f32 v59, s45, v59\n"
"v_mul_f32 v60, s45, v60\n"
"v_mul_f32 v61, s45, v61\n"
"v_mul_f32 v62, s45, v62\n"
"v_mul_f32 v63, s45, v63\n"
"v_cvt_pk_bf16_f32 v56, v56, v57\n"
"v_cvt_pk_bf16_f32 v57, v58, v59\n"
"v_cvt_pk_bf16_f32 v58, v60, v61\n"
"v_cvt_pk_bf16_f32 v59, v62, v63\n"
"v_mfma_f32_16x16x32_bf16 v[24:27], v[156:159], a[248:251], v[24:27]\n"
"buffer_load_dwordx4 v240, s[16:19], s56 offen nt lds\n"
"v_permlane16_swap_b32 v56, v58\n"
"v_permlane16_swap_b32 v57, v59\n"
"v_accvgpr_write_b32 a172, v56\n"
"v_accvgpr_write_b32 a173, v57\n"
"v_accvgpr_write_b32 a174, v58\n"
"v_accvgpr_write_b32 a175, v59\n"
"v_mfma_f32_16x16x32_bf16 v[56:59], v[160:163], a[244:247], 0\n"
"v_mul_f32 v88, s45, v88\n"
"v_mul_f32 v89, s45, v89\n"
"v_mul_f32 v90, s45, v90\n"
"v_mul_f32 v91, s45, v91\n"
"v_mul_f32 v92, s45, v92\n"
"v_mul_f32 v93, s45, v93\n"
"v_mul_f32 v94, s45, v94\n"
"v_mul_f32 v95, s45, v95\n"
"v_cvt_pk_bf16_f32 v88, v88, v89\n"
"v_cvt_pk_bf16_f32 v89, v90, v91\n"
"v_cvt_pk_bf16_f32 v90, v92, v93\n"
"v_cvt_pk_bf16_f32 v91, v94, v95\n"
"v_mfma_f32_16x16x32_bf16 v[56:59], v[164:167], a[248:251], v[56:59]\n"
"v_permlane16_swap_b32 v88, v90\n"
"v_permlane16_swap_b32 v89, v91\n"
"v_accvgpr_write_b32 a188, v88\n"
"v_accvgpr_write_b32 a189, v89\n"
"v_accvgpr_write_b32 a190, v90\n"
"v_accvgpr_write_b32 a191, v91\n"
"v_mfma_f32_16x16x32_bf16 v[88:91], v[168:171], a[244:247], 0\n"
"v_exp_f32 v128, v128\n"
"v_mfma_f32_16x16x32_bf16 v[88:91], v[172:175], a[248:251], v[88:91]\n"
"v_accvgpr_write_b32 a140, v200\n"
"v_accvgpr_write_b32 a141, v201\n"
"v_mfma_f32_16x16x32_bf16 v[92:95], v[172:175], v[224:227], 0\n"
"v_mov_b32 v200, 0\n"
"v_mov_b32 v201, 0\n"
"v_mfma_f32_16x16x32_bf16 v[92:95], v[168:171], a[252:255], v[92:95]\n"
"v_exp_f32 v129, v129\n"
"v_mfma_f32_16x16x32_bf16 v[60:63], v[164:167], v[224:227], 0\n"
"v_exp_f32 v130, v130\n"
"v_mfma_f32_16x16x32_bf16 v[60:63], v[160:163], a[252:255], v[60:63]\n"
"v_exp_f32 v131, v131\n"
"v_mfma_f32_16x16x32_bf16 v[28:31], v[156:159], v[224:227], 0\n"
"v_cvt_pk_bf16_f32 v104, v128, v129\n"
"v_cvt_pk_bf16_f32 v105, v130, v131\n"
"v_mfma_f32_16x16x32_bf16 v[28:31], v[152:155], a[252:255], v[28:31]\n"
"v_exp_f32 v132, v132\n"
"v_mfma_f32_16x16x32_bf16 a[236:239], v[176:179], a[252:255], 0\n"
"v_exp_f32 v133, v133\n"
"v_smfmac_f32_16x16x64_bf16 v[198:201], a[240:243], v[160:167], v241\n"
"s_waitcnt lgkmcnt(0)\n"
"v_exp_f32 v134, v134\n"
"v_mfma_f32_16x16x32_bf16 v[128:131], v[208:211], a[0:3], v[180:183]\n"
"s_add_u32 s40, s56, s52\n"
"v_exp_f32 v135, v135\n"
"v_mfma_f32_16x16x32_bf16 v[128:131], v[212:215], a[4:7], v[128:131]\n"
"buffer_load_dwordx4 v240, s[16:19], s40 offen offset:1024 nt lds\n"
"v_mfma_f32_16x16x32_bf16 v[128:131], v[216:219], a[8:11], v[128:131]\n"
"v_mul_f32 v116, s45, v116\n"
"v_mul_f32 v117, s45, v117\n"
"v_mul_f32 v118, s45, v118\n"
"v_mul_f32 v119, s45, v119\n"
"v_cvt_pk_bf16_f32 v112, v112, v113\n"
"v_cvt_pk_bf16_f32 v113, v114, v115\n"
"v_cvt_pk_bf16_f32 v114, v116, v117\n"
"v_cvt_pk_bf16_f32 v115, v118, v119\n"
"v_mfma_f32_16x16x32_bf16 v[128:131], v[220:223], a[12:15], v[128:131]\n"
"v_permlane16_swap_b32 v112, v114\n"
"v_permlane16_swap_b32 v113, v115\n"
"v_accvgpr_write_b32 a96, v112\n"
"v_accvgpr_write_b32 a97, v113\n"
"v_cvt_pk_bf16_f32 v112, v132, v133\n"
"v_cvt_pk_bf16_f32 v113, v134, v135\n"
"v_mfma_f32_16x16x32_bf16 v[132:135], v[216:219], a[24:27], v[184:187]\n"
"v_exp_f32 v136, v136\n"
"v_mfma_f32_16x16x32_bf16 v[132:135], v[220:223], a[28:31], v[132:135]\n"
"v_exp_f32 v137, v137\n"
"v_mfma_f32_16x16x32_bf16 v[132:135], v[208:211], a[16:19], v[132:135]\n"
"ds_read_b64_tr_b16 a[244:245], v232 offset:0\n"
"ds_read_b64_tr_b16 a[246:247], v232 offset:4096\n"
"v_exp_f32 v138, v138\n"
"v_exp_f32 v139, v139\n"
"v_mfma_f32_16x16x32_bf16 v[132:135], v[212:215], a[20:23], v[132:135]\n"
"ds_read_b64_tr_b16 a[248:249], v236 offset:0\n"
"v_accvgpr_write_b32 a100, v120\n"
"v_accvgpr_write_b32 a101, v121\n"
"v_cvt_pk_bf16_f32 v120, v136, v137\n"
"v_cvt_pk_bf16_f32 v121, v138, v139\n"
"v_mfma_f32_16x16x32_bf16 v[136:139], v[208:211], a[32:35], v[188:191]\n"
"ds_read_b64_tr_b16 a[250:251], v236 offset:4096\n"
"v_mul_f32 v0, s45, v0\n"
"v_mfma_f32_16x16x32_bf16 v[136:139], v[212:215], a[36:39], v[136:139]\n"
"ds_read_b64_tr_b16 v[224:225], v237 offset:0\n"
"v_mul_f32 v1, s45, v1\n"
"v_mfma_f32_16x16x32_bf16 v[136:139], v[216:219], a[40:43], v[136:139]\n"
"ds_read_b64_tr_b16 v[226:227], v237 offset:4096\n"
"v_mul_f32 v2, s45, v2\n"
"v_mfma_f32_16x16x32_bf16 v[136:139], v[220:223], a[44:47], v[136:139]\n"
"s_waitcnt lgkmcnt(4)\n"
"ds_read_b64_tr_b16 a[252:253], v233 offset:0\n"
"v_mul_f32 v3, s45, v3\n"
"v_mul_f32 v4, s45, v4\n"
"v_mul_f32 v5, s45, v5\n"
"v_mul_f32 v6, s45, v6\n"
"v_mul_f32 v7, s45, v7\n"
"v_cvt_pk_bf16_f32 v0, v0, v1\n"
"v_cvt_pk_bf16_f32 v1, v2, v3\n"
"v_cvt_pk_bf16_f32 v2, v4, v5\n"
"v_cvt_pk_bf16_f32 v3, v6, v7\n"
"v_mfma_f32_16x16x32_bf16 a[208:211], v[176:179], a[244:247], 0\n"
"s_add_u32 s40, s56, s53\n"
"v_permlane16_swap_b32 v0, v2\n"
"v_permlane16_swap_b32 v1, v3\n"
"v_accvgpr_write_b32 a144, v0\n"
"v_accvgpr_write_b32 a145, v1\n"
"v_accvgpr_write_b32 a146, v2\n"
"v_accvgpr_write_b32 a147, v3\n"
"v_mfma_f32_16x16x32_bf16 v[0:3], v[152:155], a[244:247], 0\n"
"s_waitcnt lgkmcnt(3)\n"
"buffer_load_dwordx4 v240, s[16:19], s40 offen offset:2048 nt lds\n"
"v_mul_f32 v32, s45, v32\n"
"v_mul_f32 v33, s45, v33\n"
"v_mul_f32 v34, s45, v34\n"
"v_mul_f32 v35, s45, v35\n"
"v_mul_f32 v36, s45, v36\n"
"v_mul_f32 v37, s45, v37\n"
"v_mul_f32 v38, s45, v38\n"
"v_mul_f32 v39, s45, v39\n"
"v_cvt_pk_bf16_f32 v32, v32, v33\n"
"v_cvt_pk_bf16_f32 v33, v34, v35\n"
"v_cvt_pk_bf16_f32 v34, v36, v37\n"
"v_cvt_pk_bf16_f32 v35, v38, v39\n"
"v_mfma_f32_16x16x32_bf16 v[0:3], v[156:159], a[248:251], v[0:3]\n"
"ds_read_b64_tr_b16 a[254:255], v233 offset:4096\n"
"v_permlane16_swap_b32 v32, v34\n"
"v_permlane16_swap_b32 v33, v35\n"
"v_accvgpr_write_b32 a160, v32\n"
"v_accvgpr_write_b32 a161, v33\n"
"v_accvgpr_write_b32 a162, v34\n"
"v_accvgpr_write_b32 a163, v35\n"
"v_mfma_f32_16x16x32_bf16 v[32:35], v[160:163], a[244:247], 0\n"
"v_mul_f32 v64, s45, v64\n"
"v_mul_f32 v65, s45, v65\n"
"v_mul_f32 v66, s45, v66\n"
"v_mul_f32 v67, s45, v67\n"
"v_mul_f32 v68, s45, v68\n"
"v_mul_f32 v69, s45, v69\n"
"v_mul_f32 v70, s45, v70\n"
"v_mul_f32 v71, s45, v71\n"
"v_cvt_pk_bf16_f32 v64, v64, v65\n"
"v_cvt_pk_bf16_f32 v65, v66, v67\n"
"v_cvt_pk_bf16_f32 v66, v68, v69\n"
"v_cvt_pk_bf16_f32 v67, v70, v71\n"
"v_mfma_f32_16x16x32_bf16 v[32:35], v[164:167], a[248:251], v[32:35]\n"
"v_permlane16_swap_b32 v64, v66\n"
"v_permlane16_swap_b32 v65, v67\n"
"v_accvgpr_write_b32 a176, v64\n"
"v_accvgpr_write_b32 a177, v65\n"
"v_accvgpr_write_b32 a178, v66\n"
"v_accvgpr_write_b32 a179, v67\n"
"v_mfma_f32_16x16x32_bf16 v[64:67], v[168:171], a[244:247], 0\n"
"v_exp_f32 v140, v140\n"
"v_mfma_f32_16x16x32_bf16 v[64:67], v[172:175], a[248:251], v[64:67]\n"
"s_waitcnt lgkmcnt(2)\n"
"v_exp_f32 v141, v141\n"
"v_mfma_f32_16x16x32_bf16 v[68:71], v[172:175], v[224:227], 0\n"
"s_waitcnt lgkmcnt(0)\n"
"v_exp_f32 v142, v142\n"
"v_mfma_f32_16x16x32_bf16 v[68:71], v[168:171], a[252:255], v[68:71]\n"
"ds_read_b128 v[208:211], v230 offset:20480\n"
"v_cvt_pk_bf16_f32 v98, v140, v141\n"
"v_mfma_f32_16x16x32_bf16 v[36:39], v[164:167], v[224:227], 0\n"
"ds_read_b128 v[212:215], v231 offset:20480\n"
"v_accvgpr_write_b32 a98, v114\n"
"v_mfma_f32_16x16x32_bf16 v[36:39], v[160:163], a[252:255], v[36:39]\n"
"ds_read_b128 v[216:219], v230 offset:21504\n"
"v_accvgpr_write_b32 a99, v115\n"
"v_mfma_f32_16x16x32_bf16 v[4:7], v[156:159], v[224:227], 0\n"
"s_add_u32 s40, s56, s54\n"
"ds_read_b128 v[220:223], v231 offset:21504\n"
"v_mul_f32 v16, s45, v16\n"
"v_mfma_f32_16x16x32_bf16 v[4:7], v[152:155], a[252:255], v[4:7]\n"
"buffer_load_dwordx4 v240, s[16:19], s40 offen offset:3072 nt lds\n"
"v_mfma_f32_16x16x32_bf16 a[212:215], v[176:179], a[252:255], 0\n"
"v_exp_f32 v143, v143\n"
"v_smfmac_f32_16x16x64_bf16 v[200:203], a[240:243], v[168:175], v241\n"
"s_waitcnt lgkmcnt(3)\n"
"v_cvt_pk_bf16_f32 v99, v142, v143\n"
"v_mul_f32 v17, s45, v17\n"
"v_mfma_f32_16x16x32_bf16 v[140:143], v[208:211], a[0:3], v[180:183]\n"
"s_waitcnt lgkmcnt(2)\n"
"v_exp_f32 v144, v144\n"
"v_mfma_f32_16x16x32_bf16 v[140:143], v[212:215], a[4:7], v[140:143]\n"
"s_waitcnt lgkmcnt(1)\n"
"v_exp_f32 v145, v145\n"
"v_mfma_f32_16x16x32_bf16 v[140:143], v[216:219], a[8:11], v[140:143]\n"
"s_waitcnt lgkmcnt(0)\n"
"v_exp_f32 v146, v146\n"
"v_exp_f32 v147, v147\n"
"v_mfma_f32_16x16x32_bf16 v[140:143], v[220:223], a[12:15], v[140:143]\n"
"v_cvt_pk_bf16_f32 v106, v144, v145\n"
"v_cvt_pk_bf16_f32 v107, v146, v147\n"
"v_mfma_f32_16x16x32_bf16 v[144:147], v[216:219], a[24:27], v[184:187]\n"
"v_exp_f32 v148, v148\n"
"v_mfma_f32_16x16x32_bf16 v[144:147], v[220:223], a[28:31], v[144:147]\n"
"v_exp_f32 v149, v149\n"
"v_mfma_f32_16x16x32_bf16 v[144:147], v[208:211], a[16:19], v[144:147]\n"
"ds_read_b64_tr_b16 a[244:245], v232 offset:1024\n"
"ds_read_b64_tr_b16 a[246:247], v232 offset:5120\n"
"v_exp_f32 v150, v150\n"
"v_exp_f32 v151, v151\n"
"v_mfma_f32_16x16x32_bf16 v[144:147], v[212:215], a[20:23], v[144:147]\n"
"v_cvt_pk_bf16_f32 v114, v148, v149\n"
"v_cvt_pk_bf16_f32 v115, v150, v151\n"
"v_mfma_f32_16x16x32_bf16 v[148:151], v[208:211], a[32:35], v[188:191]\n"
"ds_read_b64_tr_b16 a[248:249], v236 offset:1024\n"
"ds_read_b64_tr_b16 a[250:251], v236 offset:5120\n"
"v_mfma_f32_16x16x32_bf16 v[148:151], v[212:215], a[36:39], v[148:151]\n"
"s_cbranch_vccnz LPCE1_%=\nLPKE1_%=:\n"
"ds_read_b64_tr_b16 v[224:225], v237 offset:1024\n"
"v_mfma_f32_16x16x32_bf16 v[148:151], v[216:219], a[40:43], v[148:151]\n"
"v_mul_f32 v18, s45, v18\n"
"v_mul_f32 v19, s45, v19\n"
"v_mfma_f32_16x16x32_bf16 v[148:151], v[220:223], a[44:47], v[148:151]\n"
"v_mul_f32 v20, s45, v20\n"
"v_mul_f32 v21, s45, v21\n"
"v_mul_f32 v22, s45, v22\n"
"v_mul_f32 v23, s45, v23\n"
"v_cvt_pk_bf16_f32 v16, v16, v17\n"
"v_cvt_pk_bf16_f32 v17, v18, v19\n"
"v_cvt_pk_bf16_f32 v18, v20, v21\n"
"v_cvt_pk_bf16_f32 v19, v22, v23\n"
"s_waitcnt lgkmcnt(3)\n"
"v_mfma_f32_16x16x32_bf16 a[224:227], v[176:179], a[244:247], 0\n"
"s_waitcnt vmcnt(10)\n"
"ds_read_b64_tr_b16 v[226:227], v237 offset:5120\n"
"v_permlane16_swap_b32 v16, v18\n"
"v_permlane16_swap_b32 v17, v19\n"
"v_accvgpr_write_b32 a152, v16\n"
"v_accvgpr_write_b32 a153, v17\n"
"v_accvgpr_write_b32 a154, v18\n"
"v_accvgpr_write_b32 a155, v19\n"
"v_mfma_f32_16x16x32_bf16 v[16:19], v[152:155], a[244:247], 0\n"
"s_barrier\n"
"v_mul_f32 v48, s45, v48\n"
"v_mul_f32 v49, s45, v49\n"
"v_mul_f32 v50, s45, v50\n"
"v_mul_f32 v51, s45, v51\n"
"v_mul_f32 v52, s45, v52\n"
"v_mul_f32 v53, s45, v53\n"
"v_mul_f32 v54, s45, v54\n"
"v_mul_f32 v55, s45, v55\n"
"v_cvt_pk_bf16_f32 v48, v48, v49\n"
"v_cvt_pk_bf16_f32 v49, v50, v51\n"
"v_cvt_pk_bf16_f32 v50, v52, v53\n"
"v_cvt_pk_bf16_f32 v51, v54, v55\n"
"s_waitcnt lgkmcnt(2)\n"
"v_mfma_f32_16x16x32_bf16 v[16:19], v[156:159], a[248:251], v[16:19]\n"
"ds_read_b64_tr_b16 a[252:253], v233 offset:1024\n"
"ds_read_b64_tr_b16 a[254:255], v233 offset:5120\n"
"v_permlane16_swap_b32 v48, v50\n"
"v_permlane16_swap_b32 v49, v51\n"
"v_accvgpr_write_b32 a168, v48\n"
"v_accvgpr_write_b32 a169, v49\n"
"v_accvgpr_write_b32 a170, v50\n"
"v_accvgpr_write_b32 a171, v51\n"
"v_mfma_f32_16x16x32_bf16 v[48:51], v[160:163], a[244:247], 0\n"
"v_mul_f32 v80, s45, v80\n"
"v_mul_f32 v81, s45, v81\n"
"v_mul_f32 v82, s45, v82\n"
"v_mul_f32 v83, s45, v83\n"
"v_mul_f32 v84, s45, v84\n"
"v_mul_f32 v85, s45, v85\n"
"v_mul_f32 v86, s45, v86\n"
"v_mul_f32 v87, s45, v87\n"
"v_cvt_pk_bf16_f32 v80, v80, v81\n"
"v_cvt_pk_bf16_f32 v81, v82, v83\n"
"v_cvt_pk_bf16_f32 v82, v84, v85\n"
"v_cvt_pk_bf16_f32 v83, v86, v87\n"
"v_mfma_f32_16x16x32_bf16 v[48:51], v[164:167], a[248:251], v[48:51]\n"
"v_permlane16_swap_b32 v80, v82\n"
"v_permlane16_swap_b32 v81, v83\n"
"v_accvgpr_write_b32 a184, v80\n"
"v_accvgpr_write_b32 a185, v81\n"
"v_accvgpr_write_b32 a186, v82\n"
"v_accvgpr_write_b32 a187, v83\n"
"v_mfma_f32_16x16x32_bf16 v[80:83], v[168:171], a[244:247], 0\n"
"v_exp_f32 v124, v124\n"
"v_mfma_f32_16x16x32_bf16 v[80:83], v[172:175], a[248:251], v[80:83]\n"
"s_waitcnt lgkmcnt(2)\n"
"v_exp_f32 v125, v125\n"
"v_mfma_f32_16x16x32_bf16 v[84:87], v[172:175], v[224:227], 0\n"
"s_waitcnt lgkmcnt(0)\n"
"ds_read_b128 v[208:211], v228 offset:32768\n"
"v_accvgpr_write_b32 a102, v122\n"
"v_mfma_f32_16x16x32_bf16 v[84:87], v[168:171], a[252:255], v[84:87]\n"
"ds_read_b128 v[212:215], v229 offset:32768\n"
"v_accvgpr_write_b32 a103, v123\n"
"v_mfma_f32_16x16x32_bf16 v[52:55], v[164:167], v[224:227], 0\n"
"ds_read_b128 v[216:219], v228 offset:33792\n"
"v_bfe_u32 v246, v242, 8, 2\n"
"v_mfma_f32_16x16x32_bf16 v[52:55], v[160:163], a[252:255], v[52:55]\n"
"ds_read_b128 v[220:223], v229 offset:33792\n"
"v_lshlrev_b32 v246, 6, v246\n"
"v_mfma_f32_16x16x32_bf16 v[20:23], v[156:159], v[224:227], 0\n"
"ds_read_b64_tr_b16 a[246:247], v234 offset:20480\n"
"v_xor_b32 v246, v246, v242\n"
"v_mfma_f32_16x16x32_bf16 v[20:23], v[152:155], a[252:255], v[20:23]\n"
"ds_read_b64_tr_b16 a[248:249], v238 offset:16384\n"
"ds_read_b64_tr_b16 a[250:251], v238 offset:20480\n"
"v_exp_f32 v126, v126\n"
"v_exp_f32 v127, v127\n"
"v_exp_f32 v130, v130\n"
"v_exp_f32 v131, v131\n"
"v_exp_f32 v132, v132\n"
"v_exp_f32 v133, v133\n"
"v_exp_f32 v136, v136\n"
"v_exp_f32 v137, v137\n"
"v_exp_f32 v138, v138\n"
"v_exp_f32 v139, v139\n"
"v_mfma_f32_16x16x32_bf16 a[228:231], v[176:179], a[252:255], 0\n"
"ds_read_b64_tr_b16 v[226:227], v239 offset:20480\n"
"v_bfe_u32 v249, v242, 6, 4\n"
"v_xor_b32 v247, v247, v249\n"
"v_lshl_add_u32 v247, v247, 4, s44\n"
"v_cvt_pk_bf16_f32 v122, v124, v125\n"
"v_cvt_pk_bf16_f32 v123, v126, v127\n"
"v_exp_f32 v128, v128\n"
"v_exp_f32 v129, v129\n"
"v_cvt_pk_bf16_f32 v101, v130, v131\n"
"v_cvt_pk_bf16_f32 v108, v132, v133\n"
"v_exp_f32 v134, v134\n"
"v_exp_f32 v135, v135\n"
"v_cvt_pk_bf16_f32 v116, v136, v137\n"
"v_cvt_pk_bf16_f32 v117, v138, v139\n"
"v_exp_f32 v140, v140\n"
"v_exp_f32 v142, v142\n"
"v_exp_f32 v144, v144\n"
"v_exp_f32 v145, v145\n"
"v_exp_f32 v147, v147\n"
"v_exp_f32 v150, v150\n"
"v_exp_f32 v151, v151\n"
"s_branch LAY2_%=\n"
"LAL_%=:\n"
"s_waitcnt lgkmcnt(0)\n"
"v_mfma_f32_16x16x32_bf16 a[224:227], v[176:179], a[244:247], a[224:227]\n"
"s_waitcnt vmcnt(8)\n"
"v_cvt_pk_bf16_f32 v101, v130, v131\n"
"v_cvt_pk_bf16_f32 v117, v138, v139\n"
"v_mfma_f32_16x16x32_bf16 v[16:19], v[152:155], a[244:247], v[16:19]\n"
"s_barrier\n"
"v_mfma_f32_16x16x32_bf16 v[16:19], v[156:159], a[248:251], v[16:19]\n"
"ds_read_b128 v[216:219], v228 offset:1024\n"
"v_cvt_pk_bf16_f32 v108, v132, v133\n"
"v_mfma_f32_16x16x32_bf16 v[48:51], v[160:163], a[244:247], v[48:51]\n"
"ds_read_b128 v[212:215], v229 offset:0\n"
"v_cvt_pk_bf16_f32 v116, v136, v137\n"
"v_mfma_f32_16x16x32_bf16 v[48:51], v[164:167], a[248:251], v[48:51]\n"
"ds_read_b128 v[208:211], v228 offset:0\n"
"ds_read_b128 v[220:223], v229 offset:1024\n"
"v_mfma_f32_16x16x32_bf16 v[80:83], v[168:171], a[244:247], v[80:83]\n"
"v_exp_f32 v147, v147\n"
"v_mfma_f32_16x16x32_bf16 v[80:83], v[172:175], a[248:251], v[80:83]\n"
"ds_read_b64_tr_b16 a[246:247], v234 offset:53248\n"
"v_mfma_f32_16x16x32_bf16 v[84:87], v[172:175], v[224:227], v[84:87]\n"
"v_exp_f32 v145, v145\n"
"v_mfma_f32_16x16x32_bf16 v[84:87], v[168:171], a[252:255], v[84:87]\n"
"v_exp_f32 v151, v151\n"
"v_mfma_f32_16x16x32_bf16 v[52:55], v[164:167], v[224:227], v[52:55]\n"
"v_exp_f32 v129, v129\n"
"v_mfma_f32_16x16x32_bf16 v[52:55], v[160:163], a[252:255], v[52:55]\n"
"v_exp_f32 v144, v144\n"
"v_mfma_f32_16x16x32_bf16 v[20:23], v[156:159], v[224:227], v[20:23]\n"
"ds_read_b64_tr_b16 a[248:249], v238 offset:49152\n"
"ds_read_b64_tr_b16 v[226:227], v239 offset:53248\n"
"v_mfma_f32_16x16x32_bf16 v[20:23], v[152:155], a[252:255], v[20:23]\n"
"v_exp_f32 v150, v150\n"
"v_mfma_f32_16x16x32_bf16 a[228:231], v[176:179], a[252:255], a[228:231]\n"
"ds_read_b64_tr_b16 a[250:251], v238 offset:53248\n"
"s_waitcnt lgkmcnt(4)\n"
"v_mfma_f32_16x16x32_bf16 v[124:127], v[208:211], a[0:3], v[180:183]\n"
"s_add_u32 m0, s44, 81920\n"
"v_mfma_f32_16x16x32_bf16 v[124:127], v[212:215], a[4:7], v[124:127]\n"
"buffer_load_dwordx4 v240, s[20:23], s56 offen nt lds\n"
"v_mfma_f32_16x16x32_bf16 v[124:127], v[216:219], a[8:11], v[124:127]\n"
"v_exp_f32 v148, v148\n"
"v_mfma_f32_16x16x32_bf16 v[124:127], v[220:223], a[12:15], v[124:127]\n"
"v_cvt_pk_bf16_f32 v100, v128, v129\n"
"v_cvt_pk_bf16_f32 v109, v134, v135\n"
"v_mfma_f32_16x16x32_bf16 v[128:131], v[216:219], a[24:27], v[184:187]\n"
"v_exp_f32 v149, v149\n"
"v_mfma_f32_16x16x32_bf16 v[128:131], v[220:223], a[28:31], v[128:131]\n"
"v_exp_f32 v141, v141\n"
"v_mfma_f32_16x16x32_bf16 v[128:131], v[208:211], a[16:19], v[128:131]\n"
"ds_read_b64_tr_b16 a[244:245], v234 offset:49152\n"
"ds_read_b64_tr_b16 v[224:225], v239 offset:49152\n"
"v_mfma_f32_16x16x32_bf16 v[128:131], v[212:215], a[20:23], v[128:131]\n"
"ds_read_b64_tr_b16 a[252:253], v235 offset:49152\n"
"ds_read_b64_tr_b16 a[254:255], v235 offset:53248\n"
"v_mfma_f32_16x16x32_bf16 v[132:135], v[208:211], a[32:35], v[188:191]\n"
"v_cvt_pk_bf16_f32 v102, v140, v141\n"
"v_cvt_pk_bf16_f32 v119, v150, v151\n"
"v_mfma_f32_16x16x32_bf16 v[132:135], v[212:215], a[36:39], v[132:135]\n"
"v_exp_f32 v143, v143\n"
"v_mfma_f32_16x16x32_bf16 v[132:135], v[216:219], a[40:43], v[132:135]\n"
"v_exp_f32 v146, v146\n"
"v_mfma_f32_16x16x32_bf16 v[132:135], v[220:223], a[44:47], v[132:135]\n"
"v_exp_f32 v126, v126\n"
"v_mfma_f32_16x16x32_bf16 v[136:139], v[216:219], a[200:203], v[192:195]\n"
"s_add_u32 s40, s56, s52\n"
"v_cvt_pk_bf16_f32 v110, v144, v145\n"
"v_mfma_f32_16x16x32_bf16 v[136:139], v[220:223], a[204:207], v[136:139]\n"
"buffer_load_dwordx4 v240, s[20:23], s40 offen offset:1024 nt lds\n"
"v_mfma_f32_16x16x32_bf16 v[136:139], v[208:211], a[192:195], v[136:139]\n"
"v_cvt_pk_bf16_f32 v103, v142, v143\n"
"v_mfma_f32_16x16x32_bf16 v[136:139], v[212:215], a[196:199], v[136:139]\n"
"s_waitcnt lgkmcnt(0)\n"
"v_cvt_pk_bf16_f32 v111, v146, v147\n"
"v_mfma_f32_16x16x32_bf16 a[216:219], v[120:123], a[244:247], a[216:219]\n"
"v_exp_f32 v124, v124\n"
"v_mfma_f32_16x16x32_bf16 v[8:11], v[96:99], a[244:247], v[8:11]\n"
"v_exp_f32 v127, v127\n"
"v_mfma_f32_16x16x32_bf16 v[8:11], v[100:103], a[248:251], v[8:11]\n"
"v_exp_f32 v132, v132\n"
"v_mfma_f32_16x16x32_bf16 v[40:43], v[104:107], a[244:247], v[40:43]\n"
"v_exp_f32 v125, v125\n"
"v_mfma_f32_16x16x32_bf16 v[40:43], v[108:111], a[248:251], v[40:43]\n"
"ds_read_b128 v[208:211], v228 offset:4096\n"
"v_cvt_pk_bf16_f32 v118, v148, v149\n"
"v_mfma_f32_16x16x32_bf16 v[72:75], v[112:115], a[244:247], v[72:75]\n"
"ds_read_b128 v[220:223], v229 offset:5120\n"
"v_cvt_pk_bf16_f32 v153, v126, v127\n"
"v_mfma_f32_16x16x32_bf16 v[72:75], v[116:119], a[248:251], v[72:75]\n"
"v_mfma_f32_16x16x32_bf16 v[76:79], v[116:119], v[224:227], v[76:79]\n"
"ds_read_b128 v[216:219], v228 offset:5120\n"
"ds_read_b128 v[212:215], v229 offset:4096\n"
"v_mfma_f32_16x16x32_bf16 v[76:79], v[112:115], a[252:255], v[76:79]\n"
"s_add_u32 s40, s56, s53\n"
"v_mfma_f32_16x16x32_bf16 v[44:47], v[108:111], v[224:227], v[44:47]\n"
"buffer_load_dwordx4 v240, s[20:23], s40 offen offset:2048 nt lds\n"
"v_mfma_f32_16x16x32_bf16 v[44:47], v[104:107], a[252:255], v[44:47]\n"
"v_exp_f32 v129, v129\n"
"v_mfma_f32_16x16x32_bf16 v[12:15], v[100:103], v[224:227], v[12:15]\n"
"v_mfma_f32_16x16x32_bf16 v[12:15], v[96:99], a[252:255], v[12:15]\n"
"v_exp_f32 v131, v131\n"
"v_mfma_f32_16x16x32_bf16 a[220:223], v[120:123], a[252:255], a[220:223]\n"
"v_exp_f32 v130, v130\n"
"v_smfmac_f32_16x16x64_bf16 v[196:199], a[240:243], v[96:103], v241\n"
"ds_read_b64_tr_b16 a[252:253], v235 offset:50176\n"
"v_cvt_pk_bf16_f32 v152, v124, v125\n"
"v_mfma_f32_16x16x32_bf16 v[202:205], a[240:243], v[120:123], v[202:205]\n"
"s_waitcnt lgkmcnt(1)\n"
"ds_read_b64_tr_b16 a[244:245], v234 offset:50176\n"
"ds_read_b64_tr_b16 a[246:247], v234 offset:54272\n"
"v_mfma_f32_16x16x32_bf16 v[140:143], v[216:219], a[8:11], v[180:183]\n"
"ds_read_b64_tr_b16 a[250:251], v238 offset:54272\n"
"v_cvt_pk_bf16_f32 v161, v130, v131\n"
"v_mfma_f32_16x16x32_bf16 v[140:143], v[220:223], a[12:15], v[140:143]\n"
"v_exp_f32 v128, v128\n"
"v_mfma_f32_16x16x32_bf16 v[140:143], v[208:211], a[0:3], v[140:143]\n"
"v_exp_f32 v139, v139\n"
"v_mfma_f32_16x16x32_bf16 v[140:143], v[212:215], a[4:7], v[140:143]\n"
"ds_read_b64_tr_b16 a[254:255], v235 offset:54272\n"
"ds_read_b64_tr_b16 v[226:227], v239 offset:54272\n"
"v_mfma_f32_16x16x32_bf16 v[144:147], v[208:211], a[16:19], v[184:187]\n"
"ds_read_b64_tr_b16 a[248:249], v238 offset:50176\n"
"v_mfma_f32_16x16x32_bf16 v[144:147], v[212:215], a[20:23], v[144:147]\n"
"s_add_u32 s40, s56, s54\n"
"ds_read_b64_tr_b16 v[224:225], v239 offset:50176\n"
"v_mfma_f32_16x16x32_bf16 v[144:147], v[216:219], a[24:27], v[144:147]\n"
"buffer_load_dwordx4 v240, s[20:23], s40 offen offset:3072 nt lds\n"
"v_mfma_f32_16x16x32_bf16 v[144:147], v[220:223], a[28:31], v[144:147]\n"
"s_add_u32 s55, s55, s59\n"
"v_exp_f32 v136, v136\n"
"v_mfma_f32_16x16x32_bf16 v[148:151], v[216:219], a[40:43], v[188:191]\n"
"s_add_u32 s41, s55, s57\n"
"v_exp_f32 v133, v133\n"
"v_mfma_f32_16x16x32_bf16 v[148:151], v[220:223], a[44:47], v[148:151]\n"
"s_and_b32 s41, s41, s58\n"
"v_exp_f32 v143, v143\n"
"v_mfma_f32_16x16x32_bf16 v[148:151], v[208:211], a[32:35], v[148:151]\n"
"s_andn2_b32 s56, s55, s58\n"
"v_exp_f32 v137, v137\n"
"v_mfma_f32_16x16x32_bf16 v[148:151], v[212:215], a[36:39], v[148:151]\n"
"s_or_b32 s56, s56, s41\n"
"v_exp_f32 v140, v140\n"
"v_mfma_f32_16x16x32_bf16 v[124:127], v[208:211], a[192:195], v[192:195]\n"
"v_exp_f32 v135, v135\n"
"v_mfma_f32_16x16x32_bf16 v[124:127], v[212:215], a[196:199], v[124:127]\n"
"v_exp_f32 v138, v138\n"
"v_mfma_f32_16x16x32_bf16 v[124:127], v[216:219], a[200:203], v[124:127]\n"
"v_exp_f32 v144, v144\n"
"v_mfma_f32_16x16x32_bf16 v[124:127], v[220:223], a[204:207], v[124:127]\n"
"s_waitcnt lgkmcnt(0)\n"
"ds_read_b128 v[208:211], v230 offset:0\n"
"v_mfma_f32_16x16x32_bf16 a[232:235], v[120:123], a[244:247], a[232:235]\n"
"ds_read_b128 v[212:215], v231 offset:0\n"
"ds_read_b128 v[220:223], v231 offset:1024\n"
"v_mfma_f32_16x16x32_bf16 v[24:27], v[96:99], a[244:247], v[24:27]\n"
"s_add_u32 m0, s44, 32768\n"
"ds_read_b128 v[216:219], v230 offset:1024\n"
"v_mfma_f32_16x16x32_bf16 v[24:27], v[100:103], a[248:251], v[24:27]\n"
"buffer_load_dwordx4 v240, s[16:19], s56 offen nt lds\n"
"v_mfma_f32_16x16x32_bf16 v[56:59], v[104:107], a[244:247], v[56:59]\n"
"v_cvt_pk_bf16_f32 v177, v138, v139\n"
"v_mfma_f32_16x16x32_bf16 v[56:59], v[108:111], a[248:251], v[56:59]\n"
"v_exp_f32 v148, v148\n"
"v_mfma_f32_16x16x32_bf16 v[88:91], v[112:115], a[244:247], v[88:91]\n"
"v_exp_f32 v126, v126\n"
"v_mfma_f32_16x16x32_bf16 v[88:91], v[116:119], a[248:251], v[88:91]\n"
"v_exp_f32 v134, v134\n"
"v_mfma_f32_16x16x32_bf16 v[92:95], v[116:119], v[224:227], v[92:95]\n"
"v_exp_f32 v124, v124\n"
"v_mfma_f32_16x16x32_bf16 v[92:95], v[112:115], a[252:255], v[92:95]\n"
"v_exp_f32 v150, v150\n"
"v_mfma_f32_16x16x32_bf16 v[60:63], v[108:111], v[224:227], v[60:63]\n"
"v_cvt_pk_bf16_f32 v176, v136, v137\n"
"v_mfma_f32_16x16x32_bf16 v[60:63], v[104:107], a[252:255], v[60:63]\n"
"ds_read_b64_tr_b16 a[246:247], v232 offset:53248\n"
"ds_read_b64_tr_b16 a[248:249], v236 offset:49152\n"
"v_mfma_f32_16x16x32_bf16 v[28:31], v[100:103], v[224:227], v[28:31]\n"
"ds_read_b64_tr_b16 a[244:245], v232 offset:49152\n"
"ds_read_b64_tr_b16 v[224:225], v237 offset:49152\n"
"v_mfma_f32_16x16x32_bf16 v[28:31], v[96:99], a[252:255], v[28:31]\n"
"v_exp_f32 v146, v146\n"
"v_mfma_f32_16x16x32_bf16 a[236:239], v[120:123], a[252:255], a[236:239]\n"
"ds_read_b64_tr_b16 v[226:227], v237 offset:53248\n"
"v_cvt_pk_bf16_f32 v160, v128, v129\n"
"v_smfmac_f32_16x16x64_bf16 v[198:201], a[240:243], v[104:111], v241\n"
"s_waitcnt lgkmcnt(5)\n"
"ds_read_b64_tr_b16 a[250:251], v236 offset:53248\n"
"ds_read_b64_tr_b16 a[254:255], v233 offset:53248\n"
"v_mfma_f32_16x16x32_bf16 v[128:131], v[208:211], a[0:3], v[180:183]\n"
"s_add_u32 s40, s56, s52\n"
"ds_read_b64_tr_b16 a[252:253], v233 offset:49152\n"
"v_mfma_f32_16x16x32_bf16 v[128:131], v[212:215], a[4:7], v[128:131]\n"
"buffer_load_dwordx4 v240, s[16:19], s40 offen offset:1024 nt lds\n"
"v_mfma_f32_16x16x32_bf16 v[128:131], v[216:219], a[8:11], v[128:131]\n"
"v_exp_f32 v125, v125\n"
"v_mfma_f32_16x16x32_bf16 v[128:131], v[220:223], a[12:15], v[128:131]\n"
"v_cvt_pk_bf16_f32 v168, v132, v133\n"
"v_cvt_pk_bf16_f32 v169, v134, v135\n"
"v_mfma_f32_16x16x32_bf16 v[132:135], v[216:219], a[24:27], v[184:187]\n"
"v_exp_f32 v145, v145\n"
"v_mfma_f32_16x16x32_bf16 v[132:135], v[220:223], a[28:31], v[132:135]\n"
"v_exp_f32 v142, v142\n"
"v_mfma_f32_16x16x32_bf16 v[132:135], v[208:211], a[16:19], v[132:135]\n"
"v_exp_f32 v127, v127\n"
"v_mfma_f32_16x16x32_bf16 v[132:135], v[212:215], a[20:23], v[132:135]\n"
"v_exp_f32 v151, v151\n"
"v_mfma_f32_16x16x32_bf16 v[136:139], v[208:211], a[32:35], v[188:191]\n"
"v_exp_f32 v141, v141\n"
"v_mfma_f32_16x16x32_bf16 v[136:139], v[212:215], a[36:39], v[136:139]\n"
"v_exp_f32 v149, v149\n"
"v_mfma_f32_16x16x32_bf16 v[136:139], v[216:219], a[40:43], v[136:139]\n"
"v_exp_f32 v147, v147\n"
"v_mfma_f32_16x16x32_bf16 v[136:139], v[220:223], a[44:47], v[136:139]\n"
"s_waitcnt lgkmcnt(0)\n"
"ds_read_b128 v[212:215], v231 offset:4096\n"
"v_cvt_pk_bf16_f32 v162, v144, v145\n"
"v_mfma_f32_16x16x32_bf16 a[208:211], v[120:123], a[244:247], a[208:211]\n"
"s_add_u32 s40, s56, s53\n"
"ds_read_b128 v[216:219], v230 offset:5120\n"
"v_mfma_f32_16x16x32_bf16 v[0:3], v[96:99], a[244:247], v[0:3]\n"
"buffer_load_dwordx4 v240, s[16:19], s40 offen offset:2048 nt lds\n"
"v_mfma_f32_16x16x32_bf16 v[0:3], v[100:103], a[248:251], v[0:3]\n"
"v_cvt_pk_bf16_f32 v163, v146, v147\n"
"v_cvt_pk_bf16_f32 v171, v150, v151\n"
"v_mfma_f32_16x16x32_bf16 v[32:35], v[104:107], a[244:247], v[32:35]\n"
"v_cvt_pk_bf16_f32 v154, v140, v141\n"
"v_cvt_pk_bf16_f32 v178, v124, v125\n"
"v_mfma_f32_16x16x32_bf16 v[32:35], v[108:111], a[248:251], v[32:35]\n"
"v_exp_f32 v128, v128\n"
"v_mfma_f32_16x16x32_bf16 v[64:67], v[112:115], a[244:247], v[64:67]\n"
"v_exp_f32 v139, v139\n"
"v_mfma_f32_16x16x32_bf16 v[64:67], v[116:119], a[248:251], v[64:67]\n"
"ds_read_b128 v[208:211], v230 offset:4096\n"
"ds_read_b128 v[220:223], v231 offset:5120\n"
"v_mfma_f32_16x16x32_bf16 v[68:71], v[116:119], v[224:227], v[68:71]\n"
"v_mfma_f32_16x16x32_bf16 v[68:71], v[112:115], a[252:255], v[68:71]\n"
"ds_read_b64_tr_b16 a[248:249], v236 offset:50176\n"
"v_mfma_f32_16x16x32_bf16 v[36:39], v[108:111], v[224:227], v[36:39]\n"
"ds_read_b64_tr_b16 a[244:245], v232 offset:50176\n"
"v_cvt_pk_bf16_f32 v155, v142, v143\n"
"v_mfma_f32_16x16x32_bf16 v[36:39], v[104:107], a[252:255], v[36:39]\n"
"ds_read_b64_tr_b16 a[250:251], v236 offset:54272\n"
"v_cvt_pk_bf16_f32 v179, v126, v127\n"
"v_mfma_f32_16x16x32_bf16 v[4:7], v[100:103], v[224:227], v[4:7]\n"
"s_add_u32 s40, s56, s54\n"
"ds_read_b64_tr_b16 a[246:247], v232 offset:54272\n"
"v_mfma_f32_16x16x32_bf16 v[4:7], v[96:99], a[252:255], v[4:7]\n"
"buffer_load_dwordx4 v240, s[16:19], s40 offen offset:3072 nt lds\n"
"v_mfma_f32_16x16x32_bf16 a[212:215], v[120:123], a[252:255], a[212:215]\n"
"v_exp_f32 v135, v135\n"
"v_smfmac_f32_16x16x64_bf16 v[200:203], a[240:243], v[112:119], v241\n"
"s_waitcnt lgkmcnt(4)\n"
"v_exp_f32 v134, v134\n"
"v_mfma_f32_16x16x32_bf16 v[140:143], v[208:211], a[0:3], v[180:183]\n"
"v_exp_f32 v138, v138\n"
"v_mfma_f32_16x16x32_bf16 v[140:143], v[212:215], a[4:7], v[140:143]\n"
"v_cvt_pk_bf16_f32 v170, v148, v149\n"
"v_mfma_f32_16x16x32_bf16 v[140:143], v[216:219], a[8:11], v[140:143]\n"
"ds_read_b64_tr_b16 a[254:255], v233 offset:54272\n"
"ds_read_b64_tr_b16 v[226:227], v237 offset:54272\n"
"v_mfma_f32_16x16x32_bf16 v[140:143], v[220:223], a[12:15], v[140:143]\n"
"ds_read_b64_tr_b16 a[252:253], v233 offset:50176\n"
"ds_read_b64_tr_b16 v[224:225], v237 offset:50176\n"
"v_mfma_f32_16x16x32_bf16 v[144:147], v[216:219], a[24:27], v[184:187]\n"
"v_exp_f32 v132, v132\n"
"v_mfma_f32_16x16x32_bf16 v[144:147], v[220:223], a[28:31], v[144:147]\n"
"v_exp_f32 v130, v130\n"
"v_mfma_f32_16x16x32_bf16 v[144:147], v[208:211], a[16:19], v[144:147]\n"
"v_exp_f32 v131, v131\n"
"v_mfma_f32_16x16x32_bf16 v[144:147], v[212:215], a[20:23], v[144:147]\n"
"v_exp_f32 v133, v133\n"
"v_mfma_f32_16x16x32_bf16 v[148:151], v[208:211], a[32:35], v[188:191]\n"
"v_exp_f32 v137, v137\n"
"v_mfma_f32_16x16x32_bf16 v[148:151], v[212:215], a[36:39], v[148:151]\n"
"s_cbranch_vccnz LPCL0_%=\nLPKL0_%=:\n"
"v_exp_f32 v142, v142\n"
"v_mfma_f32_16x16x32_bf16 v[148:151], v[216:219], a[40:43], v[148:151]\n"
"v_exp_f32 v140, v140\n"
"v_mfma_f32_16x16x32_bf16 v[148:151], v[220:223], a[44:47], v[148:151]\n"
"v_exp_f32 v136, v136\n"
"s_waitcnt lgkmcnt(0)\n"
"v_mfma_f32_16x16x32_bf16 a[224:227], v[120:123], a[244:247], a[224:227]\n"
"s_waitcnt vmcnt(8)\n"
"v_cvt_pk_bf16_f32 v157, v130, v131\n"
"v_cvt_pk_bf16_f32 v173, v138, v139\n"
"v_mfma_f32_16x16x32_bf16 v[16:19], v[96:99], a[244:247], v[16:19]\n"
"s_barrier\n"
"v_mfma_f32_16x16x32_bf16 v[16:19], v[100:103], a[248:251], v[16:19]\n"
"ds_read_b128 v[216:219], v228 offset:17408\n"
"v_cvt_pk_bf16_f32 v164, v132, v133\n"
"v_mfma_f32_16x16x32_bf16 v[48:51], v[104:107], a[244:247], v[48:51]\n"
"ds_read_b128 v[212:215], v229 offset:16384\n"
"v_cvt_pk_bf16_f32 v172, v136, v137\n"
"v_mfma_f32_16x16x32_bf16 v[48:51], v[108:111], a[248:251], v[48:51]\n"
"ds_read_b128 v[208:211], v228 offset:16384\n"
"ds_read_b128 v[220:223], v229 offset:17408\n"
"v_mfma_f32_16x16x32_bf16 v[80:83], v[112:115], a[244:247], v[80:83]\n"
"v_exp_f32 v147, v147\n"
"v_mfma_f32_16x16x32_bf16 v[80:83], v[116:119], a[248:251], v[80:83]\n"
"ds_read_b64_tr_b16 a[246:247], v234 offset:4096\n"
"v_mfma_f32_16x16x32_bf16 v[84:87], v[116:119], v[224:227], v[84:87]\n"
"v_exp_f32 v145, v145\n"
"v_mfma_f32_16x16x32_bf16 v[84:87], v[112:115], a[252:255], v[84:87]\n"
"v_exp_f32 v151, v151\n"
"v_mfma_f32_16x16x32_bf16 v[52:55], v[108:111], v[224:227], v[52:55]\n"
"v_exp_f32 v129, v129\n"
"v_mfma_f32_16x16x32_bf16 v[52:55], v[104:107], a[252:255], v[52:55]\n"
"v_exp_f32 v144, v144\n"
"v_mfma_f32_16x16x32_bf16 v[20:23], v[100:103], v[224:227], v[20:23]\n"
"ds_read_b64_tr_b16 a[248:249], v238 offset:0\n"
"ds_read_b64_tr_b16 v[226:227], v239 offset:4096\n"
"v_mfma_f32_16x16x32_bf16 v[20:23], v[96:99], a[252:255], v[20:23]\n"
"v_exp_f32 v150, v150\n"
"v_mfma_f32_16x16x32_bf16 a[228:231], v[120:123], a[252:255], a[228:231]\n"
"ds_read_b64_tr_b16 a[250:251], v238 offset:4096\n"
"s_waitcnt lgkmcnt(4)\n"
"v_mfma_f32_16x16x32_bf16 v[124:127], v[208:211], a[0:3], v[180:183]\n"
"s_add_u32 m0, s44, 98304\n"
"v_mfma_f32_16x16x32_bf16 v[124:127], v[212:215], a[4:7], v[124:127]\n"
"buffer_load_dwordx4 v240, s[20:23], s56 offen nt lds\n"
"v_mfma_f32_16x16x32_bf16 v[124:127], v[216:219], a[8:11], v[124:127]\n"
"v_exp_f32 v148, v148\n"
"v_mfma_f32_16x16x32_bf16 v[124:127], v[220:223], a[12:15], v[124:127]\n"
"v_cvt_pk_bf16_f32 v156, v128, v129\n"
"v_cvt_pk_bf16_f32 v165, v134, v135\n"
"v_mfma_f32_16x16x32_bf16 v[128:131], v[216:219], a[24:27], v[184:187]\n"
"v_exp_f32 v149, v149\n"
"v_mfma_f32_16x16x32_bf16 v[128:131], v[220:223], a[28:31], v[128:131]\n"
"v_exp_f32 v141, v141\n"
"v_mfma_f32_16x16x32_bf16 v[128:131], v[208:211], a[16:19], v[128:131]\n"
"ds_read_b64_tr_b16 a[244:245], v234 offset:0\n"
"ds_read_b64_tr_b16 v[224:225], v239 offset:0\n"
"v_mfma_f32_16x16x32_bf16 v[128:131], v[212:215], a[20:23], v[128:131]\n"
"ds_read_b64_tr_b16 a[252:253], v235 offset:0\n"
"ds_read_b64_tr_b16 a[254:255], v235 offset:4096\n"
"v_mfma_f32_16x16x32_bf16 v[132:135], v[208:211], a[32:35], v[188:191]\n"
"v_cvt_pk_bf16_f32 v158, v140, v141\n"
"v_cvt_pk_bf16_f32 v175, v150, v151\n"
"v_mfma_f32_16x16x32_bf16 v[132:135], v[212:215], a[36:39], v[132:135]\n"
"v_exp_f32 v143, v143\n"
"v_mfma_f32_16x16x32_bf16 v[132:135], v[216:219], a[40:43], v[132:135]\n"
"v_exp_f32 v146, v146\n"
"v_mfma_f32_16x16x32_bf16 v[132:135], v[220:223], a[44:47], v[132:135]\n"
"v_exp_f32 v126, v126\n"
"v_mfma_f32_16x16x32_bf16 v[136:139], v[216:219], a[200:203], v[192:195]\n"
"s_add_u32 s40, s56, s52\n"
"v_cvt_pk_bf16_f32 v166, v144, v145\n"
"v_mfma_f32_16x16x32_bf16 v[136:139], v[220:223], a[204:207], v[136:139]\n"
"buffer_load_dwordx4 v240, s[20:23], s40 offen offset:1024 nt lds\n"
"v_mfma_f32_16x16x32_bf16 v[136:139], v[208:211], a[192:195], v[136:139]\n"
"v_cvt_pk_bf16_f32 v159, v142, v143\n"
"v_mfma_f32_16x16x32_bf16 v[136:139], v[212:215], a[196:199], v[136:139]\n"
"s_waitcnt lgkmcnt(0)\n"
"v_cvt_pk_bf16_f32 v167, v146, v147\n"
"v_mfma_f32_16x16x32_bf16 a[216:219], v[176:179], a[244:247], a[216:219]\n"
"v_exp_f32 v124, v124\n"
"v_mfma_f32_16x16x32_bf16 v[8:11], v[152:155], a[244:247], v[8:11]\n"
"v_exp_f32 v127, v127\n"
"v_mfma_f32_16x16x32_bf16 v[8:11], v[156:159], a[248:251], v[8:11]\n"
"v_exp_f32 v132, v132\n"
"v_mfma_f32_16x16x32_bf16 v[40:43], v[160:163], a[244:247], v[40:43]\n"
"v_exp_f32 v125, v125\n"
"v_mfma_f32_16x16x32_bf16 v[40:43], v[164:167], a[248:251], v[40:43]\n"
"ds_read_b128 v[208:211], v228 offset:20480\n"
"v_cvt_pk_bf16_f32 v174, v148, v149\n"
"v_mfma_f32_16x16x32_bf16 v[72:75], v[168:171], a[244:247], v[72:75]\n"
"ds_read_b128 v[220:223], v229 offset:21504\n"
"v_cvt_pk_bf16_f32 v97, v126, v127\n"
"v_mfma_f32_16x16x32_bf16 v[72:75], v[172:175], a[248:251], v[72:75]\n"
"v_mfma_f32_16x16x32_bf16 v[76:79], v[172:175], v[224:227], v[76:79]\n"
"ds_read_b128 v[216:219], v228 offset:21504\n"
"ds_read_b128 v[212:215], v229 offset:20480\n"
"v_mfma_f32_16x16x32_bf16 v[76:79], v[168:171], a[252:255], v[76:79]\n"
"s_add_u32 s40, s56, s53\n"
"v_mfma_f32_16x16x32_bf16 v[44:47], v[164:167], v[224:227], v[44:47]\n"
"buffer_load_dwordx4 v240, s[20:23], s40 offen offset:2048 nt lds\n"
"v_mfma_f32_16x16x32_bf16 v[44:47], v[160:163], a[252:255], v[44:47]\n"
"v_exp_f32 v129, v129\n"
"v_mfma_f32_16x16x32_bf16 v[12:15], v[156:159], v[224:227], v[12:15]\n"
"v_mfma_f32_16x16x32_bf16 v[12:15], v[152:155], a[252:255], v[12:15]\n"
"v_exp_f32 v131, v131\n"
"v_mfma_f32_16x16x32_bf16 a[220:223], v[176:179], a[252:255], a[220:223]\n"
"v_exp_f32 v130, v130\n"
"v_smfmac_f32_16x16x64_bf16 v[196:199], a[240:243], v[152:159], v241\n"
"ds_read_b64_tr_b16 a[252:253], v235 offset:1024\n"
"v_cvt_pk_bf16_f32 v96, v124, v125\n"
"v_mfma_f32_16x16x32_bf16 v[202:205], a[240:243], v[176:179], v[202:205]\n"
"s_waitcnt lgkmcnt(1)\n"
"ds_read_b64_tr_b16 a[244:245], v234 offset:1024\n"
"ds_read_b64_tr_b16 a[246:247], v234 offset:5120\n"
"v_mfma_f32_16x16x32_bf16 v[140:143], v[216:219], a[8:11], v[180:183]\n"
"ds_read_b64_tr_b16 a[250:251], v238 offset:5120\n"
"v_cvt_pk_bf16_f32 v105, v130, v131\n"
"v_mfma_f32_16x16x32_bf16 v[140:143], v[220:223], a[12:15], v[140:143]\n"
"v_exp_f32 v128, v128\n"
"v_mfma_f32_16x16x32_bf16 v[140:143], v[208:211], a[0:3], v[140:143]\n"
"v_exp_f32 v139, v139\n"
"v_mfma_f32_16x16x32_bf16 v[140:143], v[212:215], a[4:7], v[140:143]\n"
"ds_read_b64_tr_b16 a[254:255], v235 offset:5120\n"
"ds_read_b64_tr_b16 v[226:227], v239 offset:5120\n"
"v_mfma_f32_16x16x32_bf16 v[144:147], v[208:211], a[16:19], v[184:187]\n"
"ds_read_b64_tr_b16 a[248:249], v238 offset:1024\n"
"v_mfma_f32_16x16x32_bf16 v[144:147], v[212:215], a[20:23], v[144:147]\n"
"s_add_u32 s40, s56, s54\n"
"ds_read_b64_tr_b16 v[224:225], v239 offset:1024\n"
"v_mfma_f32_16x16x32_bf16 v[144:147], v[216:219], a[24:27], v[144:147]\n"
"buffer_load_dwordx4 v240, s[20:23], s40 offen offset:3072 nt lds\n"
"v_mfma_f32_16x16x32_bf16 v[144:147], v[220:223], a[28:31], v[144:147]\n"
"s_add_u32 s55, s55, s59\n"
"v_exp_f32 v136, v136\n"
"v_mfma_f32_16x16x32_bf16 v[148:151], v[216:219], a[40:43], v[188:191]\n"
"s_add_u32 s41, s55, s57\n"
"v_exp_f32 v133, v133\n"
"v_mfma_f32_16x16x32_bf16 v[148:151], v[220:223], a[44:47], v[148:151]\n"
"s_and_b32 s41, s41, s58\n"
"v_exp_f32 v143, v143\n"
"v_mfma_f32_16x16x32_bf16 v[148:151], v[208:211], a[32:35], v[148:151]\n"
"s_andn2_b32 s56, s55, s58\n"
"v_exp_f32 v137, v137\n"
"v_mfma_f32_16x16x32_bf16 v[148:151], v[212:215], a[36:39], v[148:151]\n"
"s_or_b32 s56, s56, s41\n"
"v_exp_f32 v140, v140\n"
"v_mfma_f32_16x16x32_bf16 v[124:127], v[208:211], a[192:195], v[192:195]\n"
"v_exp_f32 v135, v135\n"
"v_mfma_f32_16x16x32_bf16 v[124:127], v[212:215], a[196:199], v[124:127]\n"
"v_exp_f32 v138, v138\n"
"v_mfma_f32_16x16x32_bf16 v[124:127], v[216:219], a[200:203], v[124:127]\n"
"v_exp_f32 v144, v144\n"
"v_mfma_f32_16x16x32_bf16 v[124:127], v[220:223], a[204:207], v[124:127]\n"
"s_waitcnt lgkmcnt(0)\n"
"ds_read_b128 v[208:211], v230 offset:16384\n"
"v_mfma_f32_16x16x32_bf16 a[232:235], v[176:179], a[244:247], a[232:235]\n"
"ds_read_b128 v[212:215], v231 offset:16384\n"
"ds_read_b128 v[220:223], v231 offset:17408\n"
"v_mfma_f32_16x16x32_bf16 v[24:27], v[152:155], a[244:247], v[24:27]\n"
"s_add_u32 m0, s44, 49152\n"
"ds_read_b128 v[216:219], v230 offset:17408\n"
"v_mfma_f32_16x16x32_bf16 v[24:27], v[156:159], a[248:251], v[24:27]\n"
"buffer_load_dwordx4 v240, s[16:19], s56 offen nt lds\n"
"v_mfma_f32_16x16x32_bf16 v[56:59], v[160:163], a[244:247], v[56:59]\n"
"v_cvt_pk_bf16_f32 v121, v138, v139\n"
"v_mfma_f32_16x16x32_bf16 v[56:59], v[164:167], a[248:251], v[56:59]\n"
"v_exp_f32 v148, v148\n"
"v_mfma_f32_16x16x32_bf16 v[88:91], v[168:171], a[244:247], v[88:91]\n"
"v_exp_f32 v126, v126\n"
"v_mfma_f32_16x16x32_bf16 v[88:91], v[172:175], a[248:251], v[88:91]\n"
"v_exp_f32 v134, v134\n"
"v_mfma_f32_16x16x32_bf16 v[92:95], v[172:175], v[224:227], v[92:95]\n"
"v_exp_f32 v124, v124\n"
"v_mfma_f32_16x16x32_bf16 v[92:95], v[168:171], a[252:255], v[92:95]\n"
"v_exp_f32 v150, v150\n"
"v_mfma_f32_16x16x32_bf16 v[60:63], v[164:167], v[224:227], v[60:63]\n"
"v_cvt_pk_bf16_f32 v120, v136, v137\n"
"v_mfma_f32_16x16x32_bf16 v[60:63], v[160:163], a[252:255], v[60:63]\n"
"ds_read_b64_tr_b16 a[246:247], v232 offset:4096\n"
"ds_read_b64_tr_b16 a[248:249], v236 offset:0\n"
"v_mfma_f32_16x16x32_bf16 v[28:31], v[156:159], v[224:227], v[28:31]\n"
"ds_read_b64_tr_b16 a[244:245], v232 offset:0\n"
"ds_read_b64_tr_b16 v[224:225], v237 offset:0\n"
"v_mfma_f32_16x16x32_bf16 v[28:31], v[152:155], a[252:255], v[28:31]\n"
"v_exp_f32 v146, v146\n"
"v_mfma_f32_16x16x32_bf16 a[236:239], v[176:179], a[252:255], a[236:239]\n"
"ds_read_b64_tr_b16 v[226:227], v237 offset:4096\n"
"v_cvt_pk_bf16_f32 v104, v128, v129\n"
"v_smfmac_f32_16x16x64_bf16 v[198:201], a[240:243], v[160:167], v241\n"
"s_waitcnt lgkmcnt(5)\n"
"ds_read_b64_tr_b16 a[250:251], v236 offset:4096\n"
"ds_read_b64_tr_b16 a[254:255], v233 offset:4096\n"
"v_mfma_f32_16x16x32_bf16 v[128:131], v[208:211], a[0:3], v[180:183]\n"
"s_add_u32 s40, s56, s52\n"
"ds_read_b64_tr_b16 a[252:253], v233 offset:0\n"
"v_mfma_f32_16x16x32_bf16 v[128:131], v[212:215], a[4:7], v[128:131]\n"
"buffer_load_dwordx4 v240, s[16:19], s40 offen offset:1024 nt lds\n"
"v_mfma_f32_16x16x32_bf16 v[128:131], v[216:219], a[8:11], v[128:131]\n"
"v_exp_f32 v125, v125\n"
"v_mfma_f32_16x16x32_bf16 v[128:131], v[220:223], a[12:15], v[128:131]\n"
"v_cvt_pk_bf16_f32 v112, v132, v133\n"
"v_cvt_pk_bf16_f32 v113, v134, v135\n"
"v_mfma_f32_16x16x32_bf16 v[132:135], v[216:219], a[24:27], v[184:187]\n"
"v_exp_f32 v145, v145\n"
"v_mfma_f32_16x16x32_bf16 v[132:135], v[220:223], a[28:31], v[132:135]\n"
"v_exp_f32 v142, v142\n"
"v_mfma_f32_16x16x32_bf16 v[132:135], v[208:211], a[16:19], v[132:135]\n"
"v_exp_f32 v127, v127\n"
"v_mfma_f32_16x16x32_bf16 v[132:135], v[212:215], a[20:23], v[132:135]\n"
"v_exp_f32 v151, v151\n"
"v_mfma_f32_16x16x32_bf16 v[136:139], v[208:211], a[32:35], v[188:191]\n"
"v_exp_f32 v141, v141\n"
"v_mfma_f32_16x16x32_bf16 v[136:139], v[212:215], a[36:39], v[136:139]\n"
"v_exp_f32 v149, v149\n"
"v_mfma_f32_16x16x32_bf16 v[136:139], v[216:219], a[40:43], v[136:139]\n"
"v_exp_f32 v147, v147\n"
"v_mfma_f32_16x16x32_bf16 v[136:139], v[220:223], a[44:47], v[136:139]\n"
"s_waitcnt lgkmcnt(0)\n"
"ds_read_b128 v[212:215], v231 offset:20480\n"
"v_cvt_pk_bf16_f32 v106, v144, v145\n"
"v_mfma_f32_16x16x32_bf16 a[208:211], v[176:179], a[244:247], a[208:211]\n"
"s_add_u32 s40, s56, s53\n"
"ds_read_b128 v[216:219], v230 offset:21504\n"
"v_mfma_f32_16x16x32_bf16 v[0:3], v[152:155], a[244:247], v[0:3]\n"
"buffer_load_dwordx4 v240, s[16:19], s40 offen offset:2048 nt lds\n"
"v_mfma_f32_16x16x32_bf16 v[0:3], v[156:159], a[248:251], v[0:3]\n"
"v_cvt_pk_bf16_f32 v107, v146, v147\n"
"v_cvt_pk_bf16_f32 v115, v150, v151\n"
"v_mfma_f32_16x16x32_bf16 v[32:35], v[160:163], a[244:247], v[32:35]\n"
"v_cvt_pk_bf16_f32 v98, v140, v141\n"
"v_cvt_pk_bf16_f32 v122, v124, v125\n"
"v_mfma_f32_16x16x32_bf16 v[32:35], v[164:167], a[248:251], v[32:35]\n"
"v_exp_f32 v128, v128\n"
"v_mfma_f32_16x16x32_bf16 v[64:67], v[168:171], a[244:247], v[64:67]\n"
"v_exp_f32 v139, v139\n"
"v_mfma_f32_16x16x32_bf16 v[64:67], v[172:175], a[248:251], v[64:67]\n"
"ds_read_b128 v[208:211], v230 offset:20480\n"
"ds_read_b128 v[220:223], v231 offset:21504\n"
"v_mfma_f32_16x16x32_bf16 v[68:71], v[172:175], v[224:227], v[68:71]\n"
"v_mfma_f32_16x16x32_bf16 v[68:71], v[168:171], a[252:255], v[68:71]\n"
"ds_read_b64_tr_b16 a[248:249], v236 offset:1024\n"
"v_mfma_f32_16x16x32_bf16 v[36:39], v[164:167], v[224:227], v[36:39]\n"
"ds_read_b64_tr_b16 a[244:245], v232 offset:1024\n"
"v_cvt_pk_bf16_f32 v99, v142, v143\n"
"v_mfma_f32_16x16x32_bf16 v[36:39], v[160:163], a[252:255], v[36:39]\n"
"ds_read_b64_tr_b16 a[250:251], v236 offset:5120\n"
"v_cvt_pk_bf16_f32 v123, v126, v127\n"
"v_mfma_f32_16x16x32_bf16 v[4:7], v[156:159], v[224:227], v[4:7]\n"
"s_add_u32 s40, s56, s54\n"
"ds_read_b64_tr_b16 a[246:247], v232 offset:5120\n"
"v_mfma_f32_16x16x32_bf16 v[4:7], v[152:155], a[252:255], v[4:7]\n"
"buffer_load_dwordx4 v240, s[16:19], s40 offen offset:3072 nt lds\n"
"v_mfma_f32_16x16x32_bf16 a[212:215], v[176:179], a[252:255], a[212:215]\n"
"v_exp_f32 v135, v135\n"
"v_smfmac_f32_16x16x64_bf16 v[200:203], a[240:243], v[168:175], v241\n"
"s_waitcnt lgkmcnt(4)\n"
"v_exp_f32 v134, v134\n"
"v_mfma_f32_16x16x32_bf16 v[140:143], v[208:211], a[0:3], v[180:183]\n"
"v_exp_f32 v138, v138\n"
"v_mfma_f32_16x16x32_bf16 v[140:143], v[212:215], a[4:7], v[140:143]\n"
"v_cvt_pk_bf16_f32 v114, v148, v149\n"
"v_mfma_f32_16x16x32_bf16 v[140:143], v[216:219], a[8:11], v[140:143]\n"
"ds_read_b64_tr_b16 a[254:255], v233 offset:5120\n"
"ds_read_b64_tr_b16 v[226:227], v237 offset:5120\n"
"v_mfma_f32_16x16x32_bf16 v[140:143], v[220:223], a[12:15], v[140:143]\n"
"ds_read_b64_tr_b16 a[252:253], v233 offset:1024\n"
"ds_read_b64_tr_b16 v[224:225], v237 offset:1024\n"
"v_mfma_f32_16x16x32_bf16 v[144:147], v[216:219], a[24:27], v[184:187]\n"
"v_exp_f32 v132, v132\n"
"v_mfma_f32_16x16x32_bf16 v[144:147], v[220:223], a[28:31], v[144:147]\n"
"v_exp_f32 v130, v130\n"
"v_mfma_f32_16x16x32_bf16 v[144:147], v[208:211], a[16:19], v[144:147]\n"
"v_exp_f32 v131, v131\n"
"v_mfma_f32_16x16x32_bf16 v[144:147], v[212:215], a[20:23], v[144:147]\n"
"v_exp_f32 v133, v133\n"
"v_mfma_f32_16x16x32_bf16 v[148:151], v[208:211], a[32:35], v[188:191]\n"
"v_exp_f32 v137, v137\n"
"v_mfma_f32_16x16x32_bf16 v[148:151], v[212:215], a[36:39], v[148:151]\n"
"s_cbranch_vccnz LPCL1_%=\nLPKL1_%=:\n"
"v_exp_f32 v142, v142\n"
"v_mfma_f32_16x16x32_bf16 v[148:151], v[216:219], a[40:43], v[148:151]\n"
"v_exp_f32 v140, v140\n"
"v_mfma_f32_16x16x32_bf16 v[148:151], v[220:223], a[44:47], v[148:151]\n"
"v_exp_f32 v136, v136\n"
"s_waitcnt lgkmcnt(0)\n"
"v_mfma_f32_16x16x32_bf16 a[224:227], v[176:179], a[244:247], a[224:227]\n"
"s_waitcnt vmcnt(8)\n"
"v_cvt_pk_bf16_f32 v101, v130, v131\n"
"v_cvt_pk_bf16_f32 v117, v138, v139\n"
"v_mfma_f32_16x16x32_bf16 v[16:19], v[152:155], a[244:247], v[16:19]\n"
"s_barrier\n"
"v_mfma_f32_16x16x32_bf16 v[16:19], v[156:159], a[248:251], v[16:19]\n"
"ds_read_b128 v[216:219], v228 offset:33792\n"
"v_cvt_pk_bf16_f32 v108, v132, v133\n"
"v_mfma_f32_16x16x32_bf16 v[48:51], v[160:163], a[244:247], v[48:51]\n"
"ds_read_b128 v[212:215], v229 offset:32768\n"
"v_cvt_pk_bf16_f32 v116, v136, v137\n"
"v_mfma_f32_16x16x32_bf16 v[48:51], v[164:167], a[248:251], v[48:51]\n"
"ds_read_b128 v[208:211], v228 offset:32768\n"
"ds_read_b128 v[220:223], v229 offset:33792\n"
"v_mfma_f32_16x16x32_bf16 v[80:83], v[168:171], a[244:247], v[80:83]\n"
"v_exp_f32 v147, v147\n"
"v_mfma_f32_16x16x32_bf16 v[80:83], v[172:175], a[248:251], v[80:83]\n"
"ds_read_b64_tr_b16 a[246:247], v234 offset:20480\n"
"v_mfma_f32_16x16x32_bf16 v[84:87], v[172:175], v[224:227], v[84:87]\n"
"v_exp_f32 v145, v145\n"
"v_mfma_f32_16x16x32_bf16 v[84:87], v[168:171], a[252:255], v[84:87]\n"
"v_exp_f32 v151, v151\n"
"v_mfma_f32_16x16x32_bf16 v[52:55], v[164:167], v[224:227], v[52:55]\n"
"v_exp_f32 v129, v129\n"
"v_mfma_f32_16x16x32_bf16 v[52:55], v[160:163], a[252:255], v[52:55]\n"
"v_exp_f32 v144, v144\n"
"v_mfma_f32_16x16x32_bf16 v[20:23], v[156:159], v[224:227], v[20:23]\n"
"ds_read_b64_tr_b16 a[248:249], v238 offset:16384\n"
"ds_read_b64_tr_b16 v[226:227], v239 offset:20480\n"
"v_mfma_f32_16x16x32_bf16 v[20:23], v[152:155], a[252:255], v[20:23]\n"
"v_exp_f32 v150, v150\n"
"v_mfma_f32_16x16x32_bf16 a[228:231], v[176:179], a[252:255], a[228:231]\n"
"ds_read_b64_tr_b16 a[250:251], v238 offset:20480\n"
"LAY2_%=:\n"
"s_waitcnt lgkmcnt(4)\n"
"v_mfma_f32_16x16x32_bf16 v[124:127], v[208:211], a[0:3], v[180:183]\n"
"s_waitcnt lgkmcnt(6)\n"
"s_add_u32 m0, s44, 114688\n"
"v_mfma_f32_16x16x32_bf16 v[124:127], v[212:215], a[4:7], v[124:127]\n"
"s_waitcnt lgkmcnt(5)\n"
"buffer_load_dwordx4 v240, s[20:23], s56 offen nt lds\n"
"v_mfma_f32_16x16x32_bf16 v[124:127], v[216:219], a[8:11], v[124:127]\n"
"s_waitcnt lgkmcnt(4)\n"
"v_exp_f32 v148, v148\n"
"v_mfma_f32_16x16x32_bf16 v[124:127], v[220:223], a[12:15], v[124:127]\n"
"v_cvt_pk_bf16_f32 v100, v128, v129\n"
"v_cvt_pk_bf16_f32 v109, v134, v135\n"
"v_mfma_f32_16x16x32_bf16 v[128:131], v[216:219], a[24:27], v[184:187]\n"
"v_exp_f32 v149, v149\n"
"v_mfma_f32_16x16x32_bf16 v[128:131], v[220:223], a[28:31], v[128:131]\n"
"v_exp_f32 v141, v141\n"
"v_mfma_f32_16x16x32_bf16 v[128:131], v[208:211], a[16:19], v[128:131]\n"
"ds_read_b64_tr_b16 a[244:245], v234 offset:16384\n"
"ds_read_b64_tr_b16 v[224:225], v239 offset:16384\n"
"v_mfma_f32_16x16x32_bf16 v[128:131], v[212:215], a[20:23], v[128:131]\n"
"ds_read_b64_tr_b16 a[252:253], v235 offset:16384\n"
"ds_read_b64_tr_b16 a[254:255], v235 offset:20480\n"
"v_mfma_f32_16x16x32_bf16 v[132:135], v[208:211], a[32:35], v[188:191]\n"
"v_cvt_pk_bf16_f32 v102, v140, v141\n"
"v_cvt_pk_bf16_f32 v119, v150, v151\n"
"v_mfma_f32_16x16x32_bf16 v[132:135], v[212:215], a[36:39], v[132:135]\n"
"v_exp_f32 v143, v143\n"
"v_mfma_f32_16x16x32_bf16 v[132:135], v[216:219], a[40:43], v[132:135]\n"
"v_exp_f32 v146, v146\n"
"v_mfma_f32_16x16x32_bf16 v[132:135], v[220:223], a[44:47], v[132:135]\n"
"v_exp_f32 v126, v126\n"
"v_mfma_f32_16x16x32_bf16 v[136:139], v[216:219], a[200:203], v[192:195]\n"
"s_add_u32 s40, s56, s52\n"
"v_cvt_pk_bf16_f32 v110, v144, v145\n"
"v_mfma_f32_16x16x32_bf16 v[136:139], v[220:223], a[204:207], v[136:139]\n"
"buffer_load_dwordx4 v240, s[20:23], s40 offen offset:1024 nt lds\n"
"v_mfma_f32_16x16x32_bf16 v[136:139], v[208:211], a[192:195], v[136:139]\n"
"v_cvt_pk_bf16_f32 v103, v142, v143\n"
"v_mfma_f32_16x16x32_bf16 v[136:139], v[212:215], a[196:199], v[136:139]\n"
"s_waitcnt lgkmcnt(0)\n"
"v_cvt_pk_bf16_f32 v111, v146, v147\n"
"v_mfma_f32_16x16x32_bf16 a[216:219], v[120:123], a[244:247], a[216:219]\n"
"v_exp_f32 v124, v124\n"
"v_mfma_f32_16x16x32_bf16 v[8:11], v[96:99], a[244:247], v[8:11]\n"
"v_exp_f32 v127, v127\n"
"v_mfma_f32_16x16x32_bf16 v[8:11], v[100:103], a[248:251], v[8:11]\n"
"v_exp_f32 v132, v132\n"
"v_mfma_f32_16x16x32_bf16 v[40:43], v[104:107], a[244:247], v[40:43]\n"
"v_exp_f32 v125, v125\n"
"v_mfma_f32_16x16x32_bf16 v[40:43], v[108:111], a[248:251], v[40:43]\n"
"ds_read_b128 v[208:211], v228 offset:36864\n"
"v_cvt_pk_bf16_f32 v118, v148, v149\n"
"v_mfma_f32_16x16x32_bf16 v[72:75], v[112:115], a[244:247], v[72:75]\n"
"ds_read_b128 v[220:223], v229 offset:37888\n"
"v_cvt_pk_bf16_f32 v153, v126, v127\n"
"v_mfma_f32_16x16x32_bf16 v[72:75], v[116:119], a[248:251], v[72:75]\n"
"v_mfma_f32_16x16x32_bf16 v[76:79], v[116:119], v[224:227], v[76:79]\n"
"ds_read_b128 v[216:219], v228 offset:37888\n"
"ds_read_b128 v[212:215], v229 offset:36864\n"
"v_mfma_f32_16x16x32_bf16 v[76:79], v[112:115], a[252:255], v[76:79]\n"
"s_add_u32 s40, s56, s53\n"
"v_mfma_f32_16x16x32_bf16 v[44:47], v[108:111], v[224:227], v[44:47]\n"
"buffer_load_dwordx4 v240, s[20:23], s40 offen offset:2048 nt lds\n"
"v_mfma_f32_16x16x32_bf16 v[44:47], v[104:107], a[252:255], v[44:47]\n"
"v_exp_f32 v129, v129\n"
"v_mfma_f32_16x16x32_bf16 v[12:15], v[100:103], v[224:227], v[12:15]\n"
"v_mfma_f32_16x16x32_bf16 v[12:15], v[96:99], a[252:255], v[12:15]\n"
"v_exp_f32 v131, v131\n"
"v_mfma_f32_16x16x32_bf16 a[220:223], v[120:123], a[252:255], a[220:223]\n"
"v_exp_f32 v130, v130\n"
"v_smfmac_f32_16x16x64_bf16 v[196:199], a[240:243], v[96:103], v241\n"
"ds_read_b64_tr_b16 a[252:253], v235 offset:17408\n"
"v_cvt_pk_bf16_f32 v152, v124, v125\n"
"v_mfma_f32_16x16x32_bf16 v[202:205], a[240:243], v[120:123], v[202:205]\n"
"s_waitcnt lgkmcnt(1)\n"
"ds_read_b64_tr_b16 a[244:245], v234 offset:17408\n"
"ds_read_b64_tr_b16 a[246:247], v234 offset:21504\n"
"v_mfma_f32_16x16x32_bf16 v[140:143], v[216:219], a[8:11], v[180:183]\n"
"ds_read_b64_tr_b16 a[250:251], v238 offset:21504\n"
"v_cvt_pk_bf16_f32 v161, v130, v131\n"
"v_mfma_f32_16x16x32_bf16 v[140:143], v[220:223], a[12:15], v[140:143]\n"
"v_exp_f32 v128, v128\n"
"v_mfma_f32_16x16x32_bf16 v[140:143], v[208:211], a[0:3], v[140:143]\n"
"v_exp_f32 v139, v139\n"
"v_mfma_f32_16x16x32_bf16 v[140:143], v[212:215], a[4:7], v[140:143]\n"
"ds_read_b64_tr_b16 a[254:255], v235 offset:21504\n"
"ds_read_b64_tr_b16 v[226:227], v239 offset:21504\n"
"v_mfma_f32_16x16x32_bf16 v[144:147], v[208:211], a[16:19], v[184:187]\n"
"ds_read_b64_tr_b16 a[248:249], v238 offset:17408\n"
"v_mfma_f32_16x16x32_bf16 v[144:147], v[212:215], a[20:23], v[144:147]\n"
"s_add_u32 s40, s56, s54\n"
"ds_read_b64_tr_b16 v[224:225], v239 offset:17408\n"
"v_mfma_f32_16x16x32_bf16 v[144:147], v[216:219], a[24:27], v[144:147]\n"
"buffer_load_dwordx4 v240, s[20:23], s40 offen offset:3072 nt lds\n"
"v_mfma_f32_16x16x32_bf16 v[144:147], v[220:223], a[28:31], v[144:147]\n"
"s_add_u32 s55, s55, s59\n"
"v_exp_f32 v136, v136\n"
"v_mfma_f32_16x16x32_bf16 v[148:151], v[216:219], a[40:43], v[188:191]\n"
"s_add_u32 s41, s55, s57\n"
"v_exp_f32 v133, v133\n"
"v_mfma_f32_16x16x32_bf16 v[148:151], v[220:223], a[44:47], v[148:151]\n"
"s_and_b32 s41, s41, s58\n"
"v_exp_f32 v143, v143\n"
"v_mfma_f32_16x16x32_bf16 v[148:151], v[208:211], a[32:35], v[148:151]\n"
"s_andn2_b32 s56, s55, s58\n"
"v_exp_f32 v137, v137\n"
"v_mfma_f32_16x16x32_bf16 v[148:151], v[212:215], a[36:39], v[148:151]\n"
"s_or_b32 s56, s56, s41\n"
"v_exp_f32 v140, v140\n"
"v_mfma_f32_16x16x32_bf16 v[124:127], v[208:211], a[192:195], v[192:195]\n"
"s_cmp_ge_u32 s47, 15\n"
"v_exp_f32 v135, v135\n"
"v_mfma_f32_16x16x32_bf16 v[124:127], v[212:215], a[196:199], v[124:127]\n"
"s_cbranch_scc1 LSW_%=\nLSWK_%=:\n"
"v_exp_f32 v138, v138\n"
"v_mfma_f32_16x16x32_bf16 v[124:127], v[216:219], a[200:203], v[124:127]\n"
"v_exp_f32 v144, v144\n"
"v_mfma_f32_16x16x32_bf16 v[124:127], v[220:223], a[204:207], v[124:127]\n"
"s_waitcnt lgkmcnt(0)\n"
"ds_read_b128 v[208:211], v230 offset:32768\n"
"v_mfma_f32_16x16x32_bf16 a[232:235], v[120:123], a[244:247], a[232:235]\n"
"ds_read_b128 v[212:215], v231 offset:32768\n"
"ds_read_b128 v[220:223], v231 offset:33792\n"
"v_mfma_f32_16x16x32_bf16 v[24:27], v[96:99], a[244:247], v[24:27]\n"
"s_mov_b32 m0, s44\n"
"ds_read_b128 v[216:219], v230 offset:33792\n"
"v_mfma_f32_16x16x32_bf16 v[24:27], v[100:103], a[248:251], v[24:27]\n"
"buffer_load_dwordx4 v240, s[16:19], s56 offen nt lds\n"
"v_mfma_f32_16x16x32_bf16 v[56:59], v[104:107], a[244:247], v[56:59]\n"
"v_cvt_pk_bf16_f32 v177, v138, v139\n"
"v_mfma_f32_16x16x32_bf16 v[56:59], v[108:111], a[248:251], v[56:59]\n"
"v_exp_f32 v148, v148\n"
"v_mfma_f32_16x16x32_bf16 v[88:91], v[112:115], a[244:247], v[88:91]\n"
"v_exp_f32 v126, v126\n"
"v_mfma_f32_16x16x32_bf16 v[88:91], v[116:119], a[248:251], v[88:91]\n"
"v_exp_f32 v134, v134\n"
"v_mfma_f32_16x16x32_bf16 v[92:95], v[116:119], v[224:227], v[92:95]\n"
"v_exp_f32 v124, v124\n"
"v_mfma_f32_16x16x32_bf16 v[92:95], v[112:115], a[252:255], v[92:95]\n"
"v_exp_f32 v150, v150\n"
"v_mfma_f32_16x16x32_bf16 v[60:63], v[108:111], v[224:227], v[60:63]\n"
"v_cvt_pk_bf16_f32 v176, v136, v137\n"
"v_mfma_f32_16x16x32_bf16 v[60:63], v[104:107], a[252:255], v[60:63]\n"
"ds_read_b64_tr_b16 a[246:247], v232 offset:20480\n"
"ds_read_b64_tr_b16 a[248:249], v236 offset:16384\n"
"v_mfma_f32_16x16x32_bf16 v[28:31], v[100:103], v[224:227], v[28:31]\n"
"ds_read_b64_tr_b16 a[244:245], v232 offset:16384\n"
"ds_read_b64_tr_b16 v[224:225], v237 offset:16384\n"
"v_mfma_f32_16x16x32_bf16 v[28:31], v[96:99], a[252:255], v[28:31]\n"
"v_exp_f32 v146, v146\n"
"v_mfma_f32_16x16x32_bf16 a[236:239], v[120:123], a[252:255], a[236:239]\n"
"ds_read_b64_tr_b16 v[226:227], v237 offset:20480\n"
"v_cvt_pk_bf16_f32 v160, v128, v129\n"
"v_smfmac_f32_16x16x64_bf16 v[198:201], a[240:243], v[104:111], v241\n"
"s_waitcnt lgkmcnt(5)\n"
"ds_read_b64_tr_b16 a[250:251], v236 offset:20480\n"
"ds_read_b64_tr_b16 a[254:255], v233 offset:20480\n"
"v_mfma_f32_16x16x32_bf16 v[128:131], v[208:211], a[0:3], v[180:183]\n"
"s_add_u32 s40, s56, s52\n"
"ds_read_b64_tr_b16 a[252:253], v233 offset:16384\n"
"v_mfma_f32_16x16x32_bf16 v[128:131], v[212:215], a[4:7], v[128:131]\n"
"buffer_load_dwordx4 v240, s[16:19], s40 offen offset:1024 nt lds\n"
"v_mfma_f32_16x16x32_bf16 v[128:131], v[216:219], a[8:11], v[128:131]\n"
"v_exp_f32 v125, v125\n"
"v_mfma_f32_16x16x32_bf16 v[128:131], v[220:223], a[12:15], v[128:131]\n"
"v_cvt_pk_bf16_f32 v168, v132, v133\n"
"v_cvt_pk_bf16_f32 v169, v134, v135\n"
"v_mfma_f32_16x16x32_bf16 v[132:135], v[216:219], a[24:27], v[184:187]\n"
"v_exp_f32 v145, v145\n"
"v_mfma_f32_16x16x32_bf16 v[132:135], v[220:223], a[28:31], v[132:135]\n"
"v_exp_f32 v142, v142\n"
"v_mfma_f32_16x16x32_bf16 v[132:135], v[208:211], a[16:19], v[132:135]\n"
"v_exp_f32 v127, v127\n"
"v_mfma_f32_16x16x32_bf16 v[132:135], v[212:215], a[20:23], v[132:135]\n"
"v_exp_f32 v151, v151\n"
"v_mfma_f32_16x16x32_bf16 v[136:139], v[208:211], a[32:35], v[188:191]\n"
"v_exp_f32 v141, v141\n"
"v_mfma_f32_16x16x32_bf16 v[136:139], v[212:215], a[36:39], v[136:139]\n"
"v_exp_f32 v149, v149\n"
"v_mfma_f32_16x16x32_bf16 v[136:139], v[216:219], a[40:43], v[136:139]\n"
"v_exp_f32 v147, v147\n"
"v_mfma_f32_16x16x32_bf16 v[136:139], v[220:223], a[44:47], v[136:139]\n"
"s_waitcnt lgkmcnt(0)\n"
"ds_read_b128 v[212:215], v231 offset:36864\n"
"v_cvt_pk_bf16_f32 v162, v144, v145\n"
"v_mfma_f32_16x16x32_bf16 a[208:211], v[120:123], a[244:247], a[208:211]\n"
"s_add_u32 s40, s56, s53\n"
"ds_read_b128 v[216:219], v230 offset:37888\n"
"v_mfma_f32_16x16x32_bf16 v[0:3], v[96:99], a[244:247], v[0:3]\n"
"buffer_load_dwordx4 v240, s[16:19], s40 offen offset:2048 nt lds\n"
"v_mfma_f32_16x16x32_bf16 v[0:3], v[100:103], a[248:251], v[0:3]\n"
"v_cvt_pk_bf16_f32 v163, v146, v147\n"
"v_cvt_pk_bf16_f32 v171, v150, v151\n"
"v_mfma_f32_16x16x32_bf16 v[32:35], v[104:107], a[244:247], v[32:35]\n"
"v_cvt_pk_bf16_f32 v154, v140, v141\n"
"v_cvt_pk_bf16_f32 v178, v124, v125\n"
"v_mfma_f32_16x16x32_bf16 v[32:35], v[108:111], a[248:251], v[32:35]\n"
"v_exp_f32 v128, v128\n"
"v_mfma_f32_16x16x32_bf16 v[64:67], v[112:115], a[244:247], v[64:67]\n"
"v_exp_f32 v139, v139\n"
"v_mfma_f32_16x16x32_bf16 v[64:67], v[116:119], a[248:251], v[64:67]\n"
"ds_read_b128 v[208:211], v230 offset:36864\n"
"ds_read_b128 v[220:223], v231 offset:37888\n"
"v_mfma_f32_16x16x32_bf16 v[68:71], v[116:119], v[224:227], v[68:71]\n"
"v_mfma_f32_16x16x32_bf16 v[68:71], v[112:115], a[252:255], v[68:71]\n"
"ds_read_b64_tr_b16 a[248:249], v236 offset:17408\n"
"v_mfma_f32_16x16x32_bf16 v[36:39], v[108:111], v[224:227], v[36:39]\n"
"ds_read_b64_tr_b16 a[244:245], v232 offset:17408\n"
"v_cvt_pk_bf16_f32 v155, v142, v143\n"
"v_mfma_f32_16x16x32_bf16 v[36:39], v[104:107], a[252:255], v[36:39]\n"
"ds_read_b64_tr_b16 a[250:251], v236 offset:21504\n"
"v_cvt_pk_bf16_f32 v179, v126, v127\n"
"v_mfma_f32_16x16x32_bf16 v[4:7], v[100:103], v[224:227], v[4:7]\n"
"s_add_u32 s40, s56, s54\n"
"ds_read_b64_tr_b16 a[246:247], v232 offset:21504\n"
"v_mfma_f32_16x16x32_bf16 v[4:7], v[96:99], a[252:255], v[4:7]\n"
"buffer_load_dwordx4 v240, s[16:19], s40 offen offset:3072 nt lds\n"
"v_mfma_f32_16x16x32_bf16 a[212:215], v[120:123], a[252:255], a[212:215]\n"
"v_exp_f32 v135, v135\n"
"v_smfmac_f32_16x16x64_bf16 v[200:203], a[240:243], v[112:119], v241\n"
"s_waitcnt lgkmcnt(4)\n"
"v_exp_f32 v134, v134\n"
"v_mfma_f32_16x16x32_bf16 v[140:143], v[208:211], a[0:3], v[180:183]\n"
"v_exp_f32 v138, v138\n"
"v_mfma_f32_16x16x32_bf16 v[140:143], v[212:215], a[4:7], v[140:143]\n"
"v_cvt_pk_bf16_f32 v170, v148, v149\n"
"v_mfma_f32_16x16x32_bf16 v[140:143], v[216:219], a[8:11], v[140:143]\n"
"ds_read_b64_tr_b16 a[254:255], v233 offset:21504\n"
"ds_read_b64_tr_b16 v[226:227], v237 offset:21504\n"
"v_mfma_f32_16x16x32_bf16 v[140:143], v[220:223], a[12:15], v[140:143]\n"
"ds_read_b64_tr_b16 a[252:253], v233 offset:17408\n"
"ds_read_b64_tr_b16 v[224:225], v237 offset:17408\n"
"v_mfma_f32_16x16x32_bf16 v[144:147], v[216:219], a[24:27], v[184:187]\n"
"v_exp_f32 v132, v132\n"
"v_mfma_f32_16x16x32_bf16 v[144:147], v[220:223], a[28:31], v[144:147]\n"
"v_exp_f32 v130, v130\n"
"v_mfma_f32_16x16x32_bf16 v[144:147], v[208:211], a[16:19], v[144:147]\n"
"v_exp_f32 v131, v131\n"
"v_mfma_f32_16x16x32_bf16 v[144:147], v[212:215], a[20:23], v[144:147]\n"
"v_exp_f32 v133, v133\n"
"v_mfma_f32_16x16x32_bf16 v[148:151], v[208:211], a[32:35], v[188:191]\n"
"v_exp_f32 v137, v137\n"
"v_mfma_f32_16x16x32_bf16 v[148:151], v[212:215], a[36:39], v[148:151]\n"
"s_cbranch_vccnz LPCL2_%=\nLPKL2_%=:\n"
"v_exp_f32 v142, v142\n"
"v_mfma_f32_16x16x32_bf16 v[148:151], v[216:219], a[40:43], v[148:151]\n"
"v_exp_f32 v140, v140\n"
"v_mfma_f32_16x16x32_bf16 v[148:151], v[220:223], a[44:47], v[148:151]\n"
"v_exp_f32 v136, v136\n"
"s_waitcnt lgkmcnt(0)\n"
"v_mfma_f32_16x16x32_bf16 a[224:227], v[120:123], a[244:247], a[224:227]\n"
"s_waitcnt vmcnt(8)\n"
"v_cvt_pk_bf16_f32 v157, v130, v131\n"
"v_cvt_pk_bf16_f32 v173, v138, v139\n"
"v_mfma_f32_16x16x32_bf16 v[16:19], v[96:99], a[244:247], v[16:19]\n"
"s_barrier\n"
"v_mfma_f32_16x16x32_bf16 v[16:19], v[100:103], a[248:251], v[16:19]\n"
"ds_read_b128 v[216:219], v228 offset:50176\n"
"v_cvt_pk_bf16_f32 v164, v132, v133\n"
"v_mfma_f32_16x16x32_bf16 v[48:51], v[104:107], a[244:247], v[48:51]\n"
"ds_read_b128 v[212:215], v229 offset:49152\n"
"v_cvt_pk_bf16_f32 v172, v136, v137\n"
"v_mfma_f32_16x16x32_bf16 v[48:51], v[108:111], a[248:251], v[48:51]\n"
"ds_read_b128 v[208:211], v228 offset:49152\n"
"ds_read_b128 v[220:223], v229 offset:50176\n"
"v_mfma_f32_16x16x32_bf16 v[80:83], v[112:115], a[244:247], v[80:83]\n"
"v_exp_f32 v147, v147\n"
"v_mfma_f32_16x16x32_bf16 v[80:83], v[116:119], a[248:251], v[80:83]\n"
"ds_read_b64_tr_b16 a[246:247], v234 offset:36864\n"
"v_mfma_f32_16x16x32_bf16 v[84:87], v[116:119], v[224:227], v[84:87]\n"
"v_exp_f32 v145, v145\n"
"v_mfma_f32_16x16x32_bf16 v[84:87], v[112:115], a[252:255], v[84:87]\n"
"v_exp_f32 v151, v151\n"
"v_mfma_f32_16x16x32_bf16 v[52:55], v[108:111], v[224:227], v[52:55]\n"
"v_exp_f32 v129, v129\n"
"v_mfma_f32_16x16x32_bf16 v[52:55], v[104:107], a[252:255], v[52:55]\n"
"v_exp_f32 v144, v144\n"
"v_mfma_f32_16x16x32_bf16 v[20:23], v[100:103], v[224:227], v[20:23]\n"
"ds_read_b64_tr_b16 a[248:249], v238 offset:32768\n"
"ds_read_b64_tr_b16 v[226:227], v239 offset:36864\n"
"v_mfma_f32_16x16x32_bf16 v[20:23], v[96:99], a[252:255], v[20:23]\n"
"v_exp_f32 v150, v150\n"
"v_mfma_f32_16x16x32_bf16 a[228:231], v[120:123], a[252:255], a[228:231]\n"
"ds_read_b64_tr_b16 a[250:251], v238 offset:36864\n"
"s_waitcnt lgkmcnt(4)\n"
"v_mfma_f32_16x16x32_bf16 v[124:127], v[208:211], a[0:3], v[180:183]\n"
"s_add_u32 m0, s44, 65536\n"
"v_mfma_f32_16x16x32_bf16 v[124:127], v[212:215], a[4:7], v[124:127]\n"
"buffer_load_dwordx4 v240, s[20:23], s56 offen nt lds\n"
"v_mfma_f32_16x16x32_bf16 v[124:127], v[216:219], a[8:11], v[124:127]\n"
"v_exp_f32 v148, v148\n"
"v_mfma_f32_16x16x32_bf16 v[124:127], v[220:223], a[12:15], v[124:127]\n"
"v_cvt_pk_bf16_f32 v156, v128, v129\n"
"v_cvt_pk_bf16_f32 v165, v134, v135\n"
"v_mfma_f32_16x16x32_bf16 v[128:131], v[216:219], a[24:27], v[184:187]\n"
"v_exp_f32 v149, v149\n"
"v_mfma_f32_16x16x32_bf16 v[128:131], v[220:223], a[28:31], v[128:131]\n"
"v_exp_f32 v141, v141\n"
"v_mfma_f32_16x16x32_bf16 v[128:131], v[208:211], a[16:19], v[128:131]\n"
"ds_read_b64_tr_b16 a[244:245], v234 offset:32768\n"
"ds_read_b64_tr_b16 v[224:225], v239 offset:32768\n"
"v_mfma_f32_16x16x32_bf16 v[128:131], v[212:215], a[20:23], v[128:131]\n"
"ds_read_b64_tr_b16 a[252:253], v235 offset:32768\n"
"ds_read_b64_tr_b16 a[254:255], v235 offset:36864\n"
"v_mfma_f32_16x16x32_bf16 v[132:135], v[208:211], a[32:35], v[188:191]\n"
"v_cvt_pk_bf16_f32 v158, v140, v141\n"
"v_cvt_pk_bf16_f32 v175, v150, v151\n"
"v_mfma_f32_16x16x32_bf16 v[132:135], v[212:215], a[36:39], v[132:135]\n"
"v_exp_f32 v143, v143\n"
"v_mfma_f32_16x16x32_bf16 v[132:135], v[216:219], a[40:43], v[132:135]\n"
"v_exp_f32 v146, v146\n"
"v_mfma_f32_16x16x32_bf16 v[132:135], v[220:223], a[44:47], v[132:135]\n"
"v_exp_f32 v126, v126\n"
"v_mfma_f32_16x16x32_bf16 v[136:139], v[216:219], a[200:203], v[192:195]\n"
"s_add_u32 s40, s56, s52\n"
"v_cvt_pk_bf16_f32 v166, v144, v145\n"
"v_mfma_f32_16x16x32_bf16 v[136:139], v[220:223], a[204:207], v[136:139]\n"
"buffer_load_dwordx4 v240, s[20:23], s40 offen offset:1024 nt lds\n"
"v_mfma_f32_16x16x32_bf16 v[136:139], v[208:211], a[192:195], v[136:139]\n"
"v_cvt_pk_bf16_f32 v159, v142, v143\n"
"v_mfma_f32_16x16x32_bf16 v[136:139], v[212:215], a[196:199], v[136:139]\n"
"s_waitcnt lgkmcnt(0)\n"
"v_cvt_pk_bf16_f32 v167, v146, v147\n"
"v_mfma_f32_16x16x32_bf16 a[216:219], v[176:179], a[244:247], a[216:219]\n"
"v_exp_f32 v124, v124\n"
"v_mfma_f32_16x16x32_bf16 v[8:11], v[152:155], a[244:247], v[8:11]\n"
"v_exp_f32 v127, v127\n"
"v_mfma_f32_16x16x32_bf16 v[8:11], v[156:159], a[248:251], v[8:11]\n"
"v_exp_f32 v132, v132\n"
"v_mfma_f32_16x16x32_bf16 v[40:43], v[160:163], a[244:247], v[40:43]\n"
"v_exp_f32 v125, v125\n"
"v_mfma_f32_16x16x32_bf16 v[40:43], v[164:167], a[248:251], v[40:43]\n"
"ds_read_b128 v[208:211], v228 offset:53248\n"
"v_cvt_pk_bf16_f32 v174, v148, v149\n"
"v_mfma_f32_16x16x32_bf16 v[72:75], v[168:171], a[244:247], v[72:75]\n"
"ds_read_b128 v[220:223], v229 offset:54272\n"
"v_cvt_pk_bf16_f32 v97, v126, v127\n"
"v_mfma_f32_16x16x32_bf16 v[72:75], v[172:175], a[248:251], v[72:75]\n"
"v_mfma_f32_16x16x32_bf16 v[76:79], v[172:175], v[224:227], v[76:79]\n"
"ds_read_b128 v[216:219], v228 offset:54272\n"
"ds_read_b128 v[212:215], v229 offset:53248\n"
"v_mfma_f32_16x16x32_bf16 v[76:79], v[168:171], a[252:255], v[76:79]\n"
"s_add_u32 s40, s56, s53\n"
"v_mfma_f32_16x16x32_bf16 v[44:47], v[164:167], v[224:227], v[44:47]\n"
"buffer_load_dwordx4 v240, s[20:23], s40 offen offset:2048 nt lds\n"
"v_mfma_f32_16x16x32_bf16 v[44:47], v[160:163], a[252:255], v[44:47]\n"
"v_exp_f32 v129, v129\n"
"v_mfma_f32_16x16x32_bf16 v[12:15], v[156:159], v[224:227], v[12:15]\n"
"v_mfma_f32_16x16x32_bf16 v[12:15], v[152:155], a[252:255], v[12:15]\n"
"v_exp_f32 v131, v131\n"
"v_mfma_f32_16x16x32_bf16 a[220:223], v[176:179], a[252:255], a[220:223]\n"
"v_exp_f32 v130, v130\n"
"v_smfmac_f32_16x16x64_bf16 v[196:199], a[240:243], v[152:159], v241\n"
"ds_read_b64_tr_b16 a[252:253], v235 offset:33792\n"
"v_cvt_pk_bf16_f32 v96, v124, v125\n"
"v_mfma_f32_16x16x32_bf16 v[202:205], a[240:243], v[176:179], v[202:205]\n"
"s_waitcnt lgkmcnt(1)\n"
"ds_read_b64_tr_b16 a[244:245], v234 offset:33792\n"
"ds_read_b64_tr_b16 a[246:247], v234 offset:37888\n"
"v_mfma_f32_16x16x32_bf16 v[140:143], v[216:219], a[8:11], v[180:183]\n"
"ds_read_b64_tr_b16 a[250:251], v238 offset:37888\n"
"v_cvt_pk_bf16_f32 v105, v130, v131\n"
"v_mfma_f32_16x16x32_bf16 v[140:143], v[220:223], a[12:15], v[140:143]\n"
"v_exp_f32 v128, v128\n"
"v_mfma_f32_16x16x32_bf16 v[140:143], v[208:211], a[0:3], v[140:143]\n"
"v_exp_f32 v139, v139\n"
"v_mfma_f32_16x16x32_bf16 v[140:143], v[212:215], a[4:7], v[140:143]\n"
"ds_read_b64_tr_b16 a[254:255], v235 offset:37888\n"
"ds_read_b64_tr_b16 v[226:227], v239 offset:37888\n"
"v_mfma_f32_16x16x32_bf16 v[144:147], v[208:211], a[16:19], v[184:187]\n"
"ds_read_b64_tr_b16 a[248:249], v238 offset:33792\n"
"v_mfma_f32_16x16x32_bf16 v[144:147], v[212:215], a[20:23], v[144:147]\n"
"s_add_u32 s40, s56, s54\n"
"ds_read_b64_tr_b16 v[224:225], v239 offset:33792\n"
"v_mfma_f32_16x16x32_bf16 v[144:147], v[216:219], a[24:27], v[144:147]\n"
"buffer_load_dwordx4 v240, s[20:23], s40 offen offset:3072 nt lds\n"
"v_mfma_f32_16x16x32_bf16 v[144:147], v[220:223], a[28:31], v[144:147]\n"
"s_add_u32 s55, s55, s59\n"
"v_exp_f32 v136, v136\n"
"v_mfma_f32_16x16x32_bf16 v[148:151], v[216:219], a[40:43], v[188:191]\n"
"s_add_u32 s41, s55, s57\n"
"v_exp_f32 v133, v133\n"
"v_mfma_f32_16x16x32_bf16 v[148:151], v[220:223], a[44:47], v[148:151]\n"
"s_and_b32 s41, s41, s58\n"
"v_exp_f32 v143, v143\n"
"v_mfma_f32_16x16x32_bf16 v[148:151], v[208:211], a[32:35], v[148:151]\n"
"s_andn2_b32 s56, s55, s58\n"
"v_exp_f32 v137, v137\n"
"v_mfma_f32_16x16x32_bf16 v[148:151], v[212:215], a[36:39], v[148:151]\n"
"s_or_b32 s56, s56, s41\n"
"v_exp_f32 v140, v140\n"
"v_mfma_f32_16x16x32_bf16 v[124:127], v[208:211], a[192:195], v[192:195]\n"
"v_exp_f32 v135, v135\n"
"v_mfma_f32_16x16x32_bf16 v[124:127], v[212:215], a[196:199], v[124:127]\n"
"v_exp_f32 v138, v138\n"
"v_mfma_f32_16x16x32_bf16 v[124:127], v[216:219], a[200:203], v[124:127]\n"
"v_exp_f32 v144, v144\n"
"v_mfma_f32_16x16x32_bf16 v[124:127], v[220:223], a[204:207], v[124:127]\n"
"s_waitcnt lgkmcnt(0)\n"
"ds_read_b128 v[208:211], v230 offset:49152\n"
"v_mfma_f32_16x16x32_bf16 a[232:235], v[176:179], a[244:247], a[232:235]\n"
"ds_read_b128 v[212:215], v231 offset:49152\n"
"ds_read_b128 v[220:223], v231 offset:50176\n"
"v_mfma_f32_16x16x32_bf16 v[24:27], v[152:155], a[244:247], v[24:27]\n"
"s_add_u32 m0, s44, 16384\n"
"ds_read_b128 v[216:219], v230 offset:50176\n"
"v_mfma_f32_16x16x32_bf16 v[24:27], v[156:159], a[248:251], v[24:27]\n"
"buffer_load_dwordx4 v240, s[16:19], s56 offen nt lds\n"
"v_mfma_f32_16x16x32_bf16 v[56:59], v[160:163], a[244:247], v[56:59]\n"
"v_cvt_pk_bf16_f32 v121, v138, v139\n"
"v_mfma_f32_16x16x32_bf16 v[56:59], v[164:167], a[248:251], v[56:59]\n"
"v_exp_f32 v148, v148\n"
"v_mfma_f32_16x16x32_bf16 v[88:91], v[168:171], a[244:247], v[88:91]\n"
"v_exp_f32 v126, v126\n"
"v_mfma_f32_16x16x32_bf16 v[88:91], v[172:175], a[248:251], v[88:91]\n"
"v_exp_f32 v134, v134\n"
"v_mfma_f32_16x16x32_bf16 v[92:95], v[172:175], v[224:227], v[92:95]\n"
"v_exp_f32 v124, v124\n"
"v_mfma_f32_16x16x32_bf16 v[92:95], v[168:171], a[252:255], v[92:95]\n"
"v_exp_f32 v150, v150\n"
"v_mfma_f32_16x16x32_bf16 v[60:63], v[164:167], v[224:227], v[60:63]\n"
"v_cvt_pk_bf16_f32 v120, v136, v137\n"
"v_mfma_f32_16x16x32_bf16 v[60:63], v[160:163], a[252:255], v[60:63]\n"
"ds_read_b64_tr_b16 a[246:247], v232 offset:36864\n"
"ds_read_b64_tr_b16 a[248:249], v236 offset:32768\n"
"v_mfma_f32_16x16x32_bf16 v[28:31], v[156:159], v[224:227], v[28:31]\n"
"ds_read_b64_tr_b16 a[244:245], v232 offset:32768\n"
"ds_read_b64_tr_b16 v[224:225], v237 offset:32768\n"
"v_mfma_f32_16x16x32_bf16 v[28:31], v[152:155], a[252:255], v[28:31]\n"
"v_exp_f32 v146, v146\n"
"v_mfma_f32_16x16x32_bf16 a[236:239], v[176:179], a[252:255], a[236:239]\n"
"ds_read_b64_tr_b16 v[226:227], v237 offset:36864\n"
"v_cvt_pk_bf16_f32 v104, v128, v129\n"
"v_smfmac_f32_16x16x64_bf16 v[198:201], a[240:243], v[160:167], v241\n"
"s_waitcnt lgkmcnt(5)\n"
"ds_read_b64_tr_b16 a[250:251], v236 offset:36864\n"
"ds_read_b64_tr_b16 a[254:255], v233 offset:36864\n"
"v_mfma_f32_16x16x32_bf16 v[128:131], v[208:211], a[0:3], v[180:183]\n"
"s_add_u32 s40, s56, s52\n"
"ds_read_b64_tr_b16 a[252:253], v233 offset:32768\n"
"v_mfma_f32_16x16x32_bf16 v[128:131], v[212:215], a[4:7], v[128:131]\n"
"buffer_load_dwordx4 v240, s[16:19], s40 offen offset:1024 nt lds\n"
"v_mfma_f32_16x16x32_bf16 v[128:131], v[216:219], a[8:11], v[128:131]\n"
"v_exp_f32 v125, v125\n"
"v_mfma_f32_16x16x32_bf16 v[128:131], v[220:223], a[12:15], v[128:131]\n"
"v_cvt_pk_bf16_f32 v112, v132, v133\n"
"v_cvt_pk_bf16_f32 v113, v134, v135\n"
"v_mfma_f32_16x16x32_bf16 v[132:135], v[216:219], a[24:27], v[184:187]\n"
"v_exp_f32 v145, v145\n"
"v_mfma_f32_16x16x32_bf16 v[132:135], v[220:223], a[28:31], v[132:135]\n"
"v_exp_f32 v142, v142\n"
"v_mfma_f32_16x16x32_bf16 v[132:135], v[208:211], a[16:19], v[132:135]\n"
"v_exp_f32 v127, v127\n"
"v_mfma_f32_16x16x32_bf16 v[132:135], v[212:215], a[20:23], v[132:135]\n"
"v_exp_f32 v151, v151\n"
"v_mfma_f32_16x16x32_bf16 v[136:139], v[208:211], a[32:35], v[188:191]\n"
"v_exp_f32 v141, v141\n"
"v_mfma_f32_16x16x32_bf16 v[136:139], v[212:215], a[36:39], v[136:139]\n"
"v_exp_f32 v149, v149\n"
"v_mfma_f32_16x16x32_bf16 v[136:139], v[216:219], a[40:43], v[136:139]\n"
"v_exp_f32 v147, v147\n"
"v_mfma_f32_16x16x32_bf16 v[136:139], v[220:223], a[44:47], v[136:139]\n"
"s_waitcnt lgkmcnt(0)\n"
"ds_read_b128 v[212:215], v231 offset:53248\n"
"v_cvt_pk_bf16_f32 v106, v144, v145\n"
"v_mfma_f32_16x16x32_bf16 a[208:211], v[176:179], a[244:247], a[208:211]\n"
"s_add_u32 s40, s56, s53\n"
"ds_read_b128 v[216:219], v230 offset:54272\n"
"v_mfma_f32_16x16x32_bf16 v[0:3], v[152:155], a[244:247], v[0:3]\n"
"buffer_load_dwordx4 v240, s[16:19], s40 offen offset:2048 nt lds\n"
"v_mfma_f32_16x16x32_bf16 v[0:3], v[156:159], a[248:251], v[0:3]\n"
"v_cvt_pk_bf16_f32 v107, v146, v147\n"
"v_cvt_pk_bf16_f32 v115, v150, v151\n"
"v_mfma_f32_16x16x32_bf16 v[32:35], v[160:163], a[244:247], v[32:35]\n"
"v_cvt_pk_bf16_f32 v98, v140, v141\n"
"v_cvt_pk_bf16_f32 v122, v124, v125\n"
"v_mfma_f32_16x16x32_bf16 v[32:35], v[164:167], a[248:251], v[32:35]\n"
"v_exp_f32 v128, v128\n"
"v_mfma_f32_16x16x32_bf16 v[64:67], v[168:171], a[244:247], v[64:67]\n"
"v_exp_f32 v139, v139\n"
"v_mfma_f32_16x16x32_bf16 v[64:67], v[172:175], a[248:251], v[64:67]\n"
"ds_read_b128 v[208:211], v230 offset:53248\n"
"ds_read_b128 v[220:223], v231 offset:54272\n"
"v_mfma_f32_16x16x32_bf16 v[68:71], v[172:175], v[224:227], v[68:71]\n"
"v_mfma_f32_16x16x32_bf16 v[68:71], v[168:171], a[252:255], v[68:71]\n"
"ds_read_b64_tr_b16 a[248:249], v236 offset:33792\n"
"v_mfma_f32_16x16x32_bf16 v[36:39], v[164:167], v[224:227], v[36:39]\n"
"ds_read_b64_tr_b16 a[244:245], v232 offset:33792\n"
"v_cvt_pk_bf16_f32 v99, v142, v143\n"
"v_mfma_f32_16x16x32_bf16 v[36:39], v[160:163], a[252:255], v[36:39]\n"
"ds_read_b64_tr_b16 a[250:251], v236 offset:37888\n"
"v_cvt_pk_bf16_f32 v123, v126, v127\n"
"v_mfma_f32_16x16x32_bf16 v[4:7], v[156:159], v[224:227], v[4:7]\n"
"s_add_u32 s40, s56, s54\n"
"ds_read_b64_tr_b16 a[246:247], v232 offset:37888\n"
"v_mfma_f32_16x16x32_bf16 v[4:7], v[152:155], a[252:255], v[4:7]\n"
"buffer_load_dwordx4 v240, s[16:19], s40 offen offset:3072 nt lds\n"
"v_mfma_f32_16x16x32_bf16 a[212:215], v[176:179], a[252:255], a[212:215]\n"
"v_exp_f32 v135, v135\n"
"v_smfmac_f32_16x16x64_bf16 v[200:203], a[240:243], v[168:175], v241\n"
"s_waitcnt lgkmcnt(4)\n"
"v_exp_f32 v134, v134\n"
"v_mfma_f32_16x16x32_bf16 v[140:143], v[208:211], a[0:3], v[180:183]\n"
"v_exp_f32 v138, v138\n"
"v_mfma_f32_16x16x32_bf16 v[140:143], v[212:215], a[4:7], v[140:143]\n"
"v_cvt_pk_bf16_f32 v114, v148, v149\n"
"v_mfma_f32_16x16x32_bf16 v[140:143], v[216:219], a[8:11], v[140:143]\n"
"ds_read_b64_tr_b16 a[254:255], v233 offset:37888\n"
"ds_read_b64_tr_b16 v[226:227], v237 offset:37888\n"
"v_mfma_f32_16x16x32_bf16 v[140:143], v[220:223], a[12:15], v[140:143]\n"
"ds_read_b64_tr_b16 a[252:253], v233 offset:33792\n"
"ds_read_b64_tr_b16 v[224:225], v237 offset:33792\n"
"v_mfma_f32_16x16x32_bf16 v[144:147], v[216:219], a[24:27], v[184:187]\n"
"v_exp_f32 v132, v132\n"
"v_mfma_f32_16x16x32_bf16 v[144:147], v[220:223], a[28:31], v[144:147]\n"
"v_exp_f32 v130, v130\n"
"v_mfma_f32_16x16x32_bf16 v[144:147], v[208:211], a[16:19], v[144:147]\n"
"v_exp_f32 v131, v131\n"
"v_mfma_f32_16x16x32_bf16 v[144:147], v[212:215], a[20:23], v[144:147]\n"
"v_exp_f32 v133, v133\n"
"v_mfma_f32_16x16x32_bf16 v[148:151], v[208:211], a[32:35], v[188:191]\n"
"v_exp_f32 v137, v137\n"
"v_mfma_f32_16x16x32_bf16 v[148:151], v[212:215], a[36:39], v[148:151]\n"
"s_cbranch_vccnz LPCL3_%=\nLPKL3_%=:\n"
"v_exp_f32 v142, v142\n"
"v_mfma_f32_16x16x32_bf16 v[148:151], v[216:219], a[40:43], v[148:151]\n"
"s_add_u32 s47, s47, 1\n"
"v_exp_f32 v140, v140\n"
"v_mfma_f32_16x16x32_bf16 v[148:151], v[220:223], a[44:47], v[148:151]\n"
"s_and_b32 s40, s47, 3\n"
"v_exp_f32 v136, v136\n"
"s_cbranch_scc1 LAL_%=\n"
"s_cmp_eq_u32 s47, 8\n"
"s_cbranch_scc1 LTH1_%=\n"
"s_cmp_eq_u32 s47, 12\n"
"s_cbranch_scc1 LTH2_%=\n"
"s_cmp_eq_u32 s47, 16\n"
"s_cbranch_scc1 LAX_%=\n"
"LTH0_%=:\n"
"s_waitcnt lgkmcnt(0)\n"
"v_mfma_f32_16x16x32_bf16 a[224:227], v[176:179], a[244:247], a[224:227]\n"
"s_waitcnt vmcnt(8)\n"
"v_exp_f32 v129, v129\n"
"v_mfma_f32_16x16x32_bf16 v[16:19], v[152:155], a[244:247], v[16:19]\n"
"s_barrier\n"
"v_mfma_f32_16x16x32_bf16 v[16:19], v[156:159], a[248:251], v[16:19]\n"
"ds_read_b128 v[208:211], v228 offset:0\n"
"v_cvt_pk_bf16_f32 v100, v128, v129\n"
"v_mfma_f32_16x16x32_bf16 v[48:51], v[160:163], a[244:247], v[48:51]\n"
"ds_read_b128 v[212:215], v229 offset:0\n"
"v_cvt_pk_bf16_f32 v101, v130, v131\n"
"v_mfma_f32_16x16x32_bf16 v[48:51], v[164:167], a[248:251], v[48:51]\n"
"ds_read_b128 v[216:219], v228 offset:1024\n"
"v_cvt_pk_bf16_f32 v116, v136, v137\n"
"v_mfma_f32_16x16x32_bf16 v[80:83], v[168:171], a[244:247], v[80:83]\n"
"v_exp_f32 v141, v141\n"
"v_mfma_f32_16x16x32_bf16 v[80:83], v[172:175], a[248:251], v[80:83]\n"
"v_exp_f32 v143, v143\n"
"v_mfma_f32_16x16x32_bf16 v[84:87], v[172:175], v[224:227], v[84:87]\n"
"v_cvt_pk_bf16_f32 v102, v140, v141\n"
"v_cvt_pk_bf16_f32 v103, v142, v143\n"
"v_mfma_f32_16x16x32_bf16 v[84:87], v[168:171], a[252:255], v[84:87]\n"
"ds_read_b64_tr_b16 a[244:245], v234 offset:49152\n"
"ds_read_b64_tr_b16 a[246:247], v234 offset:53248\n"
"v_mfma_f32_16x16x32_bf16 v[52:55], v[164:167], v[224:227], v[52:55]\n"
"v_exp_f32 v148, v148\n"
"v_mfma_f32_16x16x32_bf16 v[52:55], v[160:163], a[252:255], v[52:55]\n"
"v_exp_f32 v149, v149\n"
"v_mfma_f32_16x16x32_bf16 v[20:23], v[156:159], v[224:227], v[20:23]\n"
"ds_read_b64_tr_b16 a[248:249], v238 offset:49152\n"
"ds_read_b64_tr_b16 a[250:251], v238 offset:53248\n"
"v_mfma_f32_16x16x32_bf16 v[20:23], v[152:155], a[252:255], v[20:23]\n"
"v_exp_f32 v150, v150\n"
"v_mfma_f32_16x16x32_bf16 a[228:231], v[176:179], a[252:255], a[228:231]\n"
"v_exp_f32 v151, v151\n"
"s_waitcnt lgkmcnt(2)\n"
"v_mfma_f32_16x16x32_bf16 a[216:219], v[120:123], a[244:247], a[216:219]\n"
"s_add_u32 m0, s44, 81920\n"
"v_cvt_pk_bf16_f32 v117, v138, v139\n"
"v_cvt_pk_bf16_f32 v118, v148, v149\n"
"v_smfmac_f32_16x16x64_bf16 v[196:199], a[240:243], v[96:103], v241\n"
"buffer_load_dwordx4 v240, s[20:23], s56 offen nt lds\n"
"v_mfma_f32_16x16x32_bf16 v[8:11], v[96:99], a[244:247], v[8:11]\n"
"s_waitcnt lgkmcnt(0)\n"
"v_cvt_pk_bf16_f32 v119, v150, v151\n"
"v_cvt_pk_bf16_f32 v108, v132, v133\n"
"v_mfma_f32_16x16x32_bf16 v[8:11], v[100:103], a[248:251], v[8:11]\n"
"v_exp_f32 v144, v144\n"
"v_exp_f32 v145, v145\n"
"v_exp_f32 v146, v146\n"
"v_exp_f32 v147, v147\n"
"v_smfmac_f32_16x16x64_bf16 v[200:203], a[240:243], v[112:119], v241\n"
"ds_read_b64_tr_b16 v[224:225], v239 offset:49152\n"
"v_cvt_pk_bf16_f32 v109, v134, v135\n"
"v_cvt_pk_bf16_f32 v110, v144, v145\n"
"v_cvt_pk_bf16_f32 v111, v146, v147\n"
"v_mfma_f32_16x16x32_bf16 v[40:43], v[104:107], a[244:247], v[40:43]\n"
"ds_read_b64_tr_b16 v[226:227], v239 offset:53248\n"
"v_cvt_f32_u32 v254, s43\n"
"v_mfma_f32_16x16x32_bf16 v[40:43], v[108:111], a[248:251], v[40:43]\n"
"ds_read_b64_tr_b16 a[252:253], v235 offset:49152\n"
"ds_read_b64_tr_b16 a[254:255], v235 offset:53248\n"
"v_smfmac_f32_16x16x64_bf16 v[198:201], a[240:243], v[104:111], v241\n"
"ds_read_b128 v[220:223], v229 offset:1024\n"
"v_add_f32 v196, v196, v197\n"
"v_mfma_f32_16x16x32_bf16 v[72:75], v[112:115], a[244:247], v[72:75]\n"
"ds_read_b64_tr_b16 a[244:245], v234 offset:50176\n"
"v_and_b32 v255, 0x3f0, v242\n"
"v_mfma_f32_16x16x32_bf16 v[72:75], v[116:119], a[248:251], v[72:75]\n"
"ds_read_b64_tr_b16 a[246:247], v234 offset:54272\n"
"v_lshrrev_b32 v255, 1, v255\n"
"v_mfma_f32_16x16x32_bf16 v[202:205], a[240:243], v[120:123], v[202:205]\n"
"s_waitcnt lgkmcnt(5)\n"
"ds_read_b64_tr_b16 a[248:249], v238 offset:50176\n"
"v_sub_u32 v255, v242, v255\n"
"v_mfma_f32_16x16x32_bf16 v[76:79], v[116:119], v[224:227], v[76:79]\n"
"s_waitcnt lgkmcnt(4)\n"
"ds_read_b64_tr_b16 a[250:251], v238 offset:54272\n"
"v_xor_b32 v250, 0x1000, v255\n"
"v_mfma_f32_16x16x32_bf16 v[76:79], v[112:115], a[252:255], v[76:79]\n"
"v_mfma_f32_16x16x32_bf16 v[44:47], v[108:111], v[224:227], v[44:47]\n"
"v_add_f32 v198, v198, v199\n"
"v_add_f32 v200, v200, v201\n"
"v_mfma_f32_16x16x32_bf16 v[44:47], v[104:107], a[252:255], v[44:47]\n"
"s_add_u32 s40, s56, s52\n"
"v_add_f32 v249, v196, v198\n"
"v_add_f32 v249, v249, v200\n"
"v_mfma_f32_16x16x32_bf16 v[12:15], v[100:103], v[224:227], v[12:15]\n"
"buffer_load_dwordx4 v240, s[20:23], s40 offen offset:1024 nt lds\n"
"v_mfma_f32_16x16x32_bf16 v[12:15], v[96:99], a[252:255], v[12:15]\n"
"ds_read_b64_tr_b16 v[224:225], v239 offset:50176\n"
"v_add_f32 v254, v254, v202\n"
"v_min3_f32 v254, v254, v196, v198\n"
"v_min_f32 v254, v254, v200\n"
"v_rcp_f32 v254, v254\n"
"v_mfma_f32_16x16x32_bf16 a[220:223], v[120:123], a[252:255], a[220:223]\n"
"ds_read_b64_tr_b16 v[226:227], v239 offset:54272\n"
"v_add_f32 v249, v249, v202\n"
"v_fmac_f32 v249, 0x53800000, v254\n"
"v_cmp_nge_f32 vcc, s46, v249\n"
"s_cbranch_vccnz LSLOW_%=\nLBACK0_%=:\ns_mov_b64 vcc, s[64:65]\n"
"s_waitcnt lgkmcnt(4)\n"
"v_mfma_f32_16x16x32_bf16 a[232:235], v[120:123], a[244:247], a[232:235]\n"
"v_xor_b32 v207, 0x400, v248\n"
"v_bfe_u32 v206, v242, 8, 2\n"
"v_lshl_add_u32 v206, v206, 4, s43\n"
"v_mfma_f32_16x16x32_bf16 v[24:27], v[96:99], a[244:247], v[24:27]\n"
"s_waitcnt lgkmcnt(2)\n"
"ds_read_b128 a[196:199], v207 offset:4096\n"
"ds_read_b128 a[204:207], v207 offset:6144\n"
"ds_read_b128 v[152:155], v206 offset:0\n"
"v_xor_b32 v207, 64, v206\n"
"v_mfma_f32_16x16x32_bf16 v[24:27], v[100:103], a[248:251], v[24:27]\n"
"ds_read_b128 v[156:159], v207 offset:0\n"
"v_accvgpr_mov_b32 a0, a48\n"
"v_mfma_f32_16x16x32_bf16 v[56:59], v[104:107], a[244:247], v[56:59]\n"
"ds_read_b128 v[160:163], v206 offset:1024\n"
"v_accvgpr_mov_b32 a1, a49\n"
"v_mfma_f32_16x16x32_bf16 v[56:59], v[108:111], a[248:251], v[56:59]\n"
"ds_read_b128 v[164:167], v207 offset:1024\n"
"v_accvgpr_mov_b32 a2, a50\n"
"v_mfma_f32_16x16x32_bf16 v[88:91], v[112:115], a[244:247], v[88:91]\n"
"v_accvgpr_mov_b32 a3, a51\n"
"v_accvgpr_mov_b32 a4, a52\n"
"v_mfma_f32_16x16x32_bf16 v[88:91], v[116:119], a[248:251], v[88:91]\n"
"s_waitcnt lgkmcnt(3)\n"
"v_accvgpr_mov_b32 a5, a53\n"
"v_accvgpr_mov_b32 a6, a54\n"
"v_accvgpr_mov_b32 a7, a55\n"
"v_mfma_f32_16x16x32_bf16 v[180:183], v[152:155], a[0:3], 0\n"
"s_waitcnt lgkmcnt(2)\n"
"v_accvgpr_mov_b32 a8, a56\n"
"v_accvgpr_mov_b32 a9, a57\n"
"v_accvgpr_mov_b32 a10, a58\n"
"v_accvgpr_mov_b32 a11, a59\n"
"v_mfma_f32_16x16x32_bf16 v[180:183], v[156:159], a[4:7], v[180:183]\n"
"s_waitcnt lgkmcnt(1)\n"
"v_accvgpr_mov_b32 a12, a60\n"
"v_accvgpr_mov_b32 a13, a61\n"
"v_accvgpr_mov_b32 a14, a62\n"
"v_accvgpr_mov_b32 a15, a63\n"
"v_mfma_f32_16x16x32_bf16 v[180:183], v[160:163], a[8:11], v[180:183]\n"
"s_waitcnt lgkmcnt(0)\n"
"v_accvgpr_mov_b32 a16, a64\n"
"v_accvgpr_mov_b32 a17, a65\n"
"v_accvgpr_mov_b32 a18, a66\n"
"v_accvgpr_mov_b32 a19, a67\n"
"v_mfma_f32_16x16x32_bf16 v[180:183], v[164:167], a[12:15], v[180:183]\n"
"v_accvgpr_mov_b32 a20, a68\n"
"v_accvgpr_mov_b32 a21, a69\n"
"v_accvgpr_mov_b32 a22, a70\n"
"v_accvgpr_mov_b32 a23, a71\n"
"v_mfma_f32_16x16x32_bf16 v[184:187], v[152:155], a[16:19], 0\n"
"s_add_u32 s40, s56, s53\n"
"v_accvgpr_mov_b32 a24, a72\n"
"v_accvgpr_mov_b32 a25, a73\n"
"v_accvgpr_mov_b32 a26, a74\n"
"v_accvgpr_mov_b32 a27, a75\n"
"v_mfma_f32_16x16x32_bf16 v[184:187], v[156:159], a[20:23], v[184:187]\n"
"buffer_load_dwordx4 v240, s[20:23], s40 offen offset:2048 nt lds\n"
"v_accvgpr_mov_b32 a28, a76\n"
"v_accvgpr_mov_b32 a29, a77\n"
"v_accvgpr_mov_b32 a30, a78\n"
"v_accvgpr_mov_b32 a31, a79\n"
"v_mfma_f32_16x16x32_bf16 v[184:187], v[160:163], a[24:27], v[184:187]\n"
"ds_read_b128 a[192:195], v248 offset:4096\n"
"v_accvgpr_mov_b32 a32, a80\n"
"v_accvgpr_mov_b32 a33, a81\n"
"v_accvgpr_mov_b32 a34, a82\n"
"v_accvgpr_mov_b32 a35, a83\n"
"v_mfma_f32_16x16x32_bf16 v[184:187], v[164:167], a[28:31], v[184:187]\n"
"v_accvgpr_mov_b32 a36, a84\n"
"v_accvgpr_mov_b32 a37, a85\n"
"v_accvgpr_mov_b32 a38, a86\n"
"v_accvgpr_mov_b32 a39, a87\n"
"v_mfma_f32_16x16x32_bf16 v[188:191], v[152:155], a[32:35], 0\n"
"ds_read_b128 a[200:203], v248 offset:6144\n"
"v_accvgpr_mov_b32 a40, a88\n"
"v_accvgpr_mov_b32 a41, a89\n"
"v_accvgpr_mov_b32 a42, a90\n"
"v_accvgpr_mov_b32 a43, a91\n"
"v_mfma_f32_16x16x32_bf16 v[188:191], v[156:159], a[36:39], v[188:191]\n"
"v_accvgpr_mov_b32 a44, a92\n"
"v_accvgpr_mov_b32 a45, a93\n"
"v_accvgpr_mov_b32 a46, a94\n"
"v_accvgpr_mov_b32 a47, a95\n"
"v_mfma_f32_16x16x32_bf16 v[188:191], v[160:163], a[40:43], v[188:191]\n"
"v_mov_b32 v252, v192\n"
"v_xor_b32 v180, s77, v180\n"
"v_mfma_f32_16x16x32_bf16 v[188:191], v[164:167], a[44:47], v[188:191]\n"
"s_waitcnt lgkmcnt(1)\n"
"v_xor_b32 v181, s77, v181\n"
"v_xor_b32 v182, s77, v182\n"
"v_mfma_f32_16x16x32_bf16 v[192:195], v[152:155], a[192:195], 0\n"
"v_rcp_f32 v243, v196\n"
"v_mfma_f32_16x16x32_bf16 v[192:195], v[156:159], a[196:199], v[192:195]\n"
"s_waitcnt lgkmcnt(0)\n"
"v_xor_b32 v183, s77, v183\n"
"v_xor_b32 v184, s77, v184\n"
"v_mfma_f32_16x16x32_bf16 v[192:195], v[160:163], a[200:203], v[192:195]\n"
"v_xor_b32 v185, s77, v185\n"
"v_xor_b32 v186, s77, v186\n"
"v_mfma_f32_16x16x32_bf16 v[192:195], v[164:167], a[204:207], v[192:195]\n"
"v_rcp_f32 v244, v198\n"
"v_mfma_f32_16x16x32_bf16 v[124:127], v[208:211], a[0:3], v[180:183]\n"
"v_rcp_f32 v245, v200\n"
"v_mfma_f32_16x16x32_bf16 v[124:127], v[212:215], a[4:7], v[124:127]\n"
"ds_read_b64_tr_b16 a[252:253], v235 offset:50176\n"
"v_xor_b32 v187, s77, v187\n"
"v_mfma_f32_16x16x32_bf16 v[124:127], v[216:219], a[8:11], v[124:127]\n"
"s_add_u32 s40, s56, s54\n"
"ds_read_b64_tr_b16 a[254:255], v235 offset:54272\n"
"v_xor_b32 v188, s77, v188\n"
"v_mfma_f32_16x16x32_bf16 v[124:127], v[220:223], a[12:15], v[124:127]\n"
"buffer_load_dwordx4 v240, s[20:23], s40 offen offset:3072 nt lds\n"
"v_mfma_f32_16x16x32_bf16 v[128:131], v[216:219], a[24:27], v[184:187]\n"
"s_add_u32 s55, s55, s59\n"
"ds_read_b64_tr_b16 a[244:245], v232 offset:49152\n"
"v_xor_b32 v189, s77, v189\n"
"v_mfma_f32_16x16x32_bf16 v[128:131], v[220:223], a[28:31], v[128:131]\n"
"s_add_u32 s41, s55, s57\n"
"ds_read_b64_tr_b16 a[246:247], v232 offset:53248\n"
"v_xor_b32 v190, s77, v190\n"
"v_mfma_f32_16x16x32_bf16 v[128:131], v[208:211], a[16:19], v[128:131]\n"
"s_and_b32 s41, s41, s58\n"
"ds_read_b64_tr_b16 a[248:249], v236 offset:49152\n"
"v_xor_b32 v191, s77, v191\n"
"v_mfma_f32_16x16x32_bf16 v[128:131], v[212:215], a[20:23], v[128:131]\n"
"s_andn2_b32 s56, s55, s58\n"
"ds_read_b64_tr_b16 a[250:251], v236 offset:53248\n"
"v_xor_b32 v192, s77, v192\n"
"v_mfma_f32_16x16x32_bf16 v[132:135], v[208:211], a[32:35], v[188:191]\n"
"s_or_b32 s56, s56, s41\n"
"v_xor_b32 v193, s77, v193\n"
"v_xor_b32 v194, s77, v194\n"
"v_mfma_f32_16x16x32_bf16 v[132:135], v[212:215], a[36:39], v[132:135]\n"
"v_xor_b32 v195, s77, v195\n"
"v_mfma_f32_16x16x32_bf16 v[132:135], v[216:219], a[40:43], v[132:135]\n"
"v_mfma_f32_16x16x32_bf16 v[132:135], v[220:223], a[44:47], v[132:135]\n"
"v_mfma_f32_16x16x32_bf16 v[136:139], v[216:219], a[200:203], v[192:195]\n"
"ds_read_b128 v[216:219], v228 offset:5120\n"
"v_mfma_f32_16x16x32_bf16 v[136:139], v[220:223], a[204:207], v[136:139]\n"
"ds_read_b128 v[220:223], v229 offset:5120\n"
"v_mfma_f32_16x16x32_bf16 v[136:139], v[208:211], a[192:195], v[136:139]\n"
"ds_read_b128 v[208:211], v228 offset:4096\n"
"v_mfma_f32_16x16x32_bf16 v[136:139], v[212:215], a[196:199], v[136:139]\n"
"ds_read_b128 v[212:215], v229 offset:4096\n"
"s_branch LTC_%=\n"
"LTH1_%=:\n"
"s_waitcnt lgkmcnt(0)\n"
"v_mfma_f32_16x16x32_bf16 a[224:227], v[176:179], a[244:247], a[224:227]\n"
"s_waitcnt vmcnt(8)\n"
"v_exp_f32 v129, v129\n"
"v_mfma_f32_16x16x32_bf16 v[16:19], v[152:155], a[244:247], v[16:19]\n"
"s_barrier\n"
"v_mfma_f32_16x16x32_bf16 v[16:19], v[156:159], a[248:251], v[16:19]\n"
"ds_read_b128 v[208:211], v228 offset:0\n"
"v_cvt_pk_bf16_f32 v100, v128, v129\n"
"v_mfma_f32_16x16x32_bf16 v[48:51], v[160:163], a[244:247], v[48:51]\n"
"ds_read_b128 v[212:215], v229 offset:0\n"
"v_cvt_pk_bf16_f32 v101, v130, v131\n"
"v_mfma_f32_16x16x32_bf16 v[48:51], v[164:167], a[248:251], v[48:51]\n"
"ds_read_b128 v[216:219], v228 offset:1024\n"
"v_cvt_pk_bf16_f32 v116, v136, v137\n"
"v_mfma_f32_16x16x32_bf16 v[80:83], v[168:171], a[244:247], v[80:83]\n"
"v_exp_f32 v141, v141\n"
"v_mfma_f32_16x16x32_bf16 v[80:83], v[172:175], a[248:251], v[80:83]\n"
"v_exp_f32 v143, v143\n"
"v_mfma_f32_16x16x32_bf16 v[84:87], v[172:175], v[224:227], v[84:87]\n"
"v_cvt_pk_bf16_f32 v102, v140, v141\n"
"v_cvt_pk_bf16_f32 v103, v142, v143\n"
"v_mfma_f32_16x16x32_bf16 v[84:87], v[168:171], a[252:255], v[84:87]\n"
"ds_read_b64_tr_b16 a[244:245], v234 offset:49152\n"
"ds_read_b64_tr_b16 a[246:247], v234 offset:53248\n"
"v_mfma_f32_16x16x32_bf16 v[52:55], v[164:167], v[224:227], v[52:55]\n"
"v_exp_f32 v148, v148\n"
"v_mfma_f32_16x16x32_bf16 v[52:55], v[160:163], a[252:255], v[52:55]\n"
"v_exp_f32 v149, v149\n"
"v_mfma_f32_16x16x32_bf16 v[20:23], v[156:159], v[224:227], v[20:23]\n"
"ds_read_b64_tr_b16 a[248:249], v238 offset:49152\n"
"ds_read_b64_tr_b16 a[250:251], v238 offset:53248\n"
"v_mfma_f32_16x16x32_bf16 v[20:23], v[152:155], a[252:255], v[20:23]\n"
"v_exp_f32 v150, v150\n"
"v_mfma_f32_16x16x32_bf16 a[228:231], v[176:179], a[252:255], a[228:231]\n"
"v_exp_f32 v151, v151\n"
"s_waitcnt lgkmcnt(2)\n"
"v_mfma_f32_16x16x32_bf16 a[216:219], v[120:123], a[244:247], a[216:219]\n"
"s_add_u32 m0, s44, 81920\n"
"v_cvt_pk_bf16_f32 v117, v138, v139\n"
"v_cvt_pk_bf16_f32 v118, v148, v149\n"
"v_smfmac_f32_16x16x64_bf16 v[196:199], a[240:243], v[96:103], v241\n"
"buffer_load_dwordx4 v240, s[20:23], s56 offen nt lds\n"
"v_mfma_f32_16x16x32_bf16 v[8:11], v[96:99], a[244:247], v[8:11]\n"
"s_waitcnt lgkmcnt(0)\n"
"v_cvt_pk_bf16_f32 v119, v150, v151\n"
"v_cvt_pk_bf16_f32 v108, v132, v133\n"
"v_mfma_f32_16x16x32_bf16 v[8:11], v[100:103], a[248:251], v[8:11]\n"
"v_exp_f32 v144, v144\n"
"v_exp_f32 v145, v145\n"
"v_exp_f32 v146, v146\n"
"v_exp_f32 v147, v147\n"
"v_smfmac_f32_16x16x64_bf16 v[200:203], a[240:243], v[112:119], v241\n"
"ds_read_b64_tr_b16 v[224:225], v239 offset:49152\n"
"v_cvt_pk_bf16_f32 v109, v134, v135\n"
"v_cvt_pk_bf16_f32 v110, v144, v145\n"
"v_cvt_pk_bf16_f32 v111, v146, v147\n"
"v_mfma_f32_16x16x32_bf16 v[40:43], v[104:107], a[244:247], v[40:43]\n"
"ds_read_b64_tr_b16 v[226:227], v239 offset:53248\n"
"v_cvt_f32_u32 v254, s43\n"
"v_mfma_f32_16x16x32_bf16 v[40:43], v[108:111], a[248:251], v[40:43]\n"
"ds_read_b64_tr_b16 a[252:253], v235 offset:49152\n"
"ds_read_b64_tr_b16 a[254:255], v235 offset:53248\n"
"v_smfmac_f32_16x16x64_bf16 v[198:201], a[240:243], v[104:111], v241\n"
"ds_read_b128 v[220:223], v229 offset:1024\n"
"v_add_f32 v196, v196, v197\n"
"v_mfma_f32_16x16x32_bf16 v[72:75], v[112:115], a[244:247], v[72:75]\n"
"ds_read_b64_tr_b16 a[244:245], v234 offset:50176\n"
"v_and_b32 v255, 0x3f0, v242\n"
"v_mfma_f32_16x16x32_bf16 v[72:75], v[116:119], a[248:251], v[72:75]\n"
"ds_read_b64_tr_b16 a[246:247], v234 offset:54272\n"
"v_lshrrev_b32 v255, 1, v255\n"
"v_mfma_f32_16x16x32_bf16 v[202:205], a[240:243], v[120:123], v[202:205]\n"
"s_waitcnt lgkmcnt(5)\n"
"ds_read_b64_tr_b16 a[248:249], v238 offset:50176\n"
"v_sub_u32 v255, v242, v255\n"
"v_mfma_f32_16x16x32_bf16 v[76:79], v[116:119], v[224:227], v[76:79]\n"
"s_waitcnt lgkmcnt(4)\n"
"ds_read_b64_tr_b16 a[250:251], v238 offset:54272\n"
"v_xor_b32 v250, 0x1000, v255\n"
"v_mfma_f32_16x16x32_bf16 v[76:79], v[112:115], a[252:255], v[76:79]\n"
"v_mfma_f32_16x16x32_bf16 v[44:47], v[108:111], v[224:227], v[44:47]\n"
"v_add_f32 v198, v198, v199\n"
"v_add_f32 v200, v200, v201\n"
"v_mfma_f32_16x16x32_bf16 v[44:47], v[104:107], a[252:255], v[44:47]\n"
"s_add_u32 s40, s56, s52\n"
"v_add_f32 v249, v196, v198\n"
"v_add_f32 v249, v249, v200\n"
"v_mfma_f32_16x16x32_bf16 v[12:15], v[100:103], v[224:227], v[12:15]\n"
"buffer_load_dwordx4 v240, s[20:23], s40 offen offset:1024 nt lds\n"
"v_mfma_f32_16x16x32_bf16 v[12:15], v[96:99], a[252:255], v[12:15]\n"
"ds_read_b64_tr_b16 v[224:225], v239 offset:50176\n"
"v_add_f32 v254, v254, v202\n"
"v_min3_f32 v254, v254, v196, v198\n"
"v_min_f32 v254, v254, v200\n"
"v_rcp_f32 v254, v254\n"
"v_mfma_f32_16x16x32_bf16 a[220:223], v[120:123], a[252:255], a[220:223]\n"
"ds_read_b64_tr_b16 v[226:227], v239 offset:54272\n"
"v_add_f32 v249, v249, v202\n"
"v_fmac_f32 v249, 0x53800000, v254\n"
"v_cmp_nge_f32 vcc, s46, v249\n"
"s_cbranch_vccnz LSLOW_%=\nLBACK1_%=:\ns_mov_b64 vcc, s[64:65]\n"
"s_waitcnt lgkmcnt(4)\n"
"v_mfma_f32_16x16x32_bf16 a[232:235], v[120:123], a[244:247], a[232:235]\n"
"v_xor_b32 v207, 0x400, v248\n"
"v_bfe_u32 v206, v242, 8, 2\n"
"v_lshl_add_u32 v206, v206, 4, s43\n"
"v_mfma_f32_16x16x32_bf16 v[24:27], v[96:99], a[244:247], v[24:27]\n"
"s_waitcnt lgkmcnt(2)\n"
"ds_read_b128 a[196:199], v207 offset:4096\n"
"ds_read_b128 a[204:207], v207 offset:6144\n"
"ds_read_b128 v[152:155], v206 offset:0\n"
"v_xor_b32 v207, 64, v206\n"
"v_mfma_f32_16x16x32_bf16 v[24:27], v[100:103], a[248:251], v[24:27]\n"
"ds_read_b128 v[156:159], v207 offset:0\n"
"v_accvgpr_mov_b32 a0, a96\n"
"v_mfma_f32_16x16x32_bf16 v[56:59], v[104:107], a[244:247], v[56:59]\n"
"ds_read_b128 v[160:163], v206 offset:1024\n"
"v_accvgpr_mov_b32 a1, a97\n"
"v_mfma_f32_16x16x32_bf16 v[56:59], v[108:111], a[248:251], v[56:59]\n"
"ds_read_b128 v[164:167], v207 offset:1024\n"
"v_accvgpr_mov_b32 a2, a98\n"
"v_mfma_f32_16x16x32_bf16 v[88:91], v[112:115], a[244:247], v[88:91]\n"
"v_accvgpr_mov_b32 a3, a99\n"
"v_accvgpr_mov_b32 a4, a100\n"
"v_mfma_f32_16x16x32_bf16 v[88:91], v[116:119], a[248:251], v[88:91]\n"
"s_waitcnt lgkmcnt(3)\n"
"v_accvgpr_mov_b32 a5, a101\n"
"v_accvgpr_mov_b32 a6, a102\n"
"v_accvgpr_mov_b32 a7, a103\n"
"v_mfma_f32_16x16x32_bf16 v[180:183], v[152:155], a[0:3], 0\n"
"s_waitcnt lgkmcnt(2)\n"
"v_accvgpr_mov_b32 a8, a104\n"
"v_accvgpr_mov_b32 a9, a105\n"
"v_accvgpr_mov_b32 a10, a106\n"
"v_accvgpr_mov_b32 a11, a107\n"
"v_mfma_f32_16x16x32_bf16 v[180:183], v[156:159], a[4:7], v[180:183]\n"
"s_waitcnt lgkmcnt(1)\n"
"v_accvgpr_mov_b32 a12, a108\n"
"v_accvgpr_mov_b32 a13, a109\n"
"v_accvgpr_mov_b32 a14, a110\n"
"v_accvgpr_mov_b32 a15, a111\n"
"v_mfma_f32_16x16x32_bf16 v[180:183], v[160:163], a[8:11], v[180:183]\n"
"s_waitcnt lgkmcnt(0)\n"
"v_accvgpr_mov_b32 a16, a112\n"
"v_accvgpr_mov_b32 a17, a113\n"
"v_accvgpr_mov_b32 a18, a114\n"
"v_accvgpr_mov_b32 a19, a115\n"
"v_mfma_f32_16x16x32_bf16 v[180:183], v[164:167], a[12:15], v[180:183]\n"
"v_accvgpr_mov_b32 a20, a116\n"
"v_accvgpr_mov_b32 a21, a117\n"
"v_accvgpr_mov_b32 a22, a118\n"
"v_accvgpr_mov_b32 a23, a119\n"
"v_mfma_f32_16x16x32_bf16 v[184:187], v[152:155], a[16:19], 0\n"
"s_add_u32 s40, s56, s53\n"
"v_accvgpr_mov_b32 a24, a120\n"
"v_accvgpr_mov_b32 a25, a121\n"
"v_accvgpr_mov_b32 a26, a122\n"
"v_accvgpr_mov_b32 a27, a123\n"
"v_mfma_f32_16x16x32_bf16 v[184:187], v[156:159], a[20:23], v[184:187]\n"
"buffer_load_dwordx4 v240, s[20:23], s40 offen offset:2048 nt lds\n"
"v_accvgpr_mov_b32 a28, a124\n"
"v_accvgpr_mov_b32 a29, a125\n"
"v_accvgpr_mov_b32 a30, a126\n"
"v_accvgpr_mov_b32 a31, a127\n"
"v_mfma_f32_16x16x32_bf16 v[184:187], v[160:163], a[24:27], v[184:187]\n"
"ds_read_b128 a[192:195], v248 offset:4096\n"
"v_accvgpr_mov_b32 a32, a128\n"
"v_accvgpr_mov_b32 a33, a129\n"
"v_accvgpr_mov_b32 a34, a130\n"
"v_accvgpr_mov_b32 a35, a131\n"
"v_mfma_f32_16x16x32_bf16 v[184:187], v[164:167], a[28:31], v[184:187]\n"
"v_accvgpr_mov_b32 a36, a132\n"
"v_accvgpr_mov_b32 a37, a133\n"
"v_accvgpr_mov_b32 a38, a134\n"
"v_accvgpr_mov_b32 a39, a135\n"
"v_mfma_f32_16x16x32_bf16 v[188:191], v[152:155], a[32:35], 0\n"
"ds_read_b128 a[200:203], v248 offset:6144\n"
"v_accvgpr_mov_b32 a40, a136\n"
"v_accvgpr_mov_b32 a41, a137\n"
"v_accvgpr_mov_b32 a42, a138\n"
"v_accvgpr_mov_b32 a43, a139\n"
"v_mfma_f32_16x16x32_bf16 v[188:191], v[156:159], a[36:39], v[188:191]\n"
"v_accvgpr_mov_b32 a44, a140\n"
"v_accvgpr_mov_b32 a45, a141\n"
"v_accvgpr_mov_b32 a46, a142\n"
"v_accvgpr_mov_b32 a47, a143\n"
"v_mfma_f32_16x16x32_bf16 v[188:191], v[160:163], a[40:43], v[188:191]\n"
"v_mov_b32 v252, v192\n"
"v_xor_b32 v180, s77, v180\n"
"v_mfma_f32_16x16x32_bf16 v[188:191], v[164:167], a[44:47], v[188:191]\n"
"s_waitcnt lgkmcnt(1)\n"
"v_xor_b32 v181, s77, v181\n"
"v_xor_b32 v182, s77, v182\n"
"v_mfma_f32_16x16x32_bf16 v[192:195], v[152:155], a[192:195], 0\n"
"v_rcp_f32 v243, v196\n"
"v_mfma_f32_16x16x32_bf16 v[192:195], v[156:159], a[196:199], v[192:195]\n"
"s_waitcnt lgkmcnt(0)\n"
"v_xor_b32 v183, s77, v183\n"
"v_xor_b32 v184, s77, v184\n"
"v_mfma_f32_16x16x32_bf16 v[192:195], v[160:163], a[200:203], v[192:195]\n"
"v_xor_b32 v185, s77, v185\n"
"v_xor_b32 v186, s77, v186\n"
"v_mfma_f32_16x16x32_bf16 v[192:195], v[164:167], a[204:207], v[192:195]\n"
"v_rcp_f32 v244, v198\n"
"v_mfma_f32_16x16x32_bf16 v[124:127], v[208:211], a[0:3], v[180:183]\n"
"v_rcp_f32 v245, v200\n"
"v_mfma_f32_16x16x32_bf16 v[124:127], v[212:215], a[4:7], v[124:127]\n"
"ds_read_b64_tr_b16 a[252:253], v235 offset:50176\n"
"v_xor_b32 v187, s77, v187\n"
"v_mfma_f32_16x16x32_bf16 v[124:127], v[216:219], a[8:11], v[124:127]\n"
"s_add_u32 s40, s56, s54\n"
"ds_read_b64_tr_b16 a[254:255], v235 offset:54272\n"
"v_xor_b32 v188, s77, v188\n"
"v_mfma_f32_16x16x32_bf16 v[124:127], v[220:223], a[12:15], v[124:127]\n"
"buffer_load_dwordx4 v240, s[20:23], s40 offen offset:3072 nt lds\n"
"v_mfma_f32_16x16x32_bf16 v[128:131], v[216:219], a[24:27], v[184:187]\n"
"s_add_u32 s55, s55, s59\n"
"ds_read_b64_tr_b16 a[244:245], v232 offset:49152\n"
"v_xor_b32 v189, s77, v189\n"
"v_mfma_f32_16x16x32_bf16 v[128:131], v[220:223], a[28:31], v[128:131]\n"
"s_add_u32 s41, s55, s57\n"
"ds_read_b64_tr_b16 a[246:247], v232 offset:53248\n"
"v_xor_b32 v190, s77, v190\n"
"v_mfma_f32_16x16x32_bf16 v[128:131], v[208:211], a[16:19], v[128:131]\n"
"s_and_b32 s41, s41, s58\n"
"ds_read_b64_tr_b16 a[248:249], v236 offset:49152\n"
"v_xor_b32 v191, s77, v191\n"
"v_mfma_f32_16x16x32_bf16 v[128:131], v[212:215], a[20:23], v[128:131]\n"
"s_andn2_b32 s56, s55, s58\n"
"ds_read_b64_tr_b16 a[250:251], v236 offset:53248\n"
"v_xor_b32 v192, s77, v192\n"
"v_mfma_f32_16x16x32_bf16 v[132:135], v[208:211], a[32:35], v[188:191]\n"
"s_or_b32 s56, s56, s41\n"
"v_xor_b32 v193, s77, v193\n"
"v_xor_b32 v194, s77, v194\n"
"v_mfma_f32_16x16x32_bf16 v[132:135], v[212:215], a[36:39], v[132:135]\n"
"v_xor_b32 v195, s77, v195\n"
"v_accvgpr_mov_b32 a100, a52\n"
"v_mfma_f32_16x16x32_bf16 v[132:135], v[216:219], a[40:43], v[132:135]\n"
"v_accvgpr_mov_b32 a101, a53\n"
"v_accvgpr_mov_b32 a102, a54\n"
"v_mfma_f32_16x16x32_bf16 v[132:135], v[220:223], a[44:47], v[132:135]\n"
"v_accvgpr_mov_b32 a103, a55\n"
"v_accvgpr_mov_b32 a116, a68\n"
"v_mfma_f32_16x16x32_bf16 v[136:139], v[216:219], a[200:203], v[192:195]\n"
"ds_read_b128 v[216:219], v228 offset:5120\n"
"v_accvgpr_mov_b32 a117, a69\n"
"v_mfma_f32_16x16x32_bf16 v[136:139], v[220:223], a[204:207], v[136:139]\n"
"ds_read_b128 v[220:223], v229 offset:5120\n"
"v_accvgpr_mov_b32 a118, a70\n"
"v_mfma_f32_16x16x32_bf16 v[136:139], v[208:211], a[192:195], v[136:139]\n"
"ds_read_b128 v[208:211], v228 offset:4096\n"
"v_accvgpr_mov_b32 a119, a71\n"
"v_mfma_f32_16x16x32_bf16 v[136:139], v[212:215], a[196:199], v[136:139]\n"
"ds_read_b128 v[212:215], v229 offset:4096\n"
"v_accvgpr_mov_b32 a132, a84\n"
"v_accvgpr_mov_b32 a133, a85\n"
"v_accvgpr_mov_b32 a134, a86\n"
"v_accvgpr_mov_b32 a135, a87\n"
"v_accvgpr_mov_b32 a108, a60\n"
"v_accvgpr_mov_b32 a109, a61\n"
"v_accvgpr_mov_b32 a110, a62\n"
"v_accvgpr_mov_b32 a111, a63\n"
"v_accvgpr_mov_b32 a124, a76\n"
"v_accvgpr_mov_b32 a125, a77\n"
"v_accvgpr_mov_b32 a126, a78\n"
"v_accvgpr_mov_b32 a127, a79\n"
"v_accvgpr_mov_b32 a140, a92\n"
"v_accvgpr_mov_b32 a141, a93\n"
"v_accvgpr_mov_b32 a142, a94\n"
"v_accvgpr_mov_b32 a143, a95\n"
"v_accvgpr_mov_b32 a96, a48\n"
"v_accvgpr_mov_b32 a97, a49\n"
"v_accvgpr_mov_b32 a98, a50\n"
"v_accvgpr_mov_b32 a99, a51\n"
"v_accvgpr_mov_b32 a112, a64\n"
"v_accvgpr_mov_b32 a113, a65\n"
"v_accvgpr_mov_b32 a114, a66\n"
"v_accvgpr_mov_b32 a115, a67\n"
"v_accvgpr_mov_b32 a128, a80\n"
"v_accvgpr_mov_b32 a129, a81\n"
"v_accvgpr_mov_b32 a130, a82\n"
"v_accvgpr_mov_b32 a131, a83\n"
"v_accvgpr_mov_b32 a104, a56\n"
"v_accvgpr_mov_b32 a105, a57\n"
"v_accvgpr_mov_b32 a106, a58\n"
"v_accvgpr_mov_b32 a107, a59\n"
"v_accvgpr_mov_b32 a120, a72\n"
"v_accvgpr_mov_b32 a121, a73\n"
"v_accvgpr_mov_b32 a122, a74\n"
"v_accvgpr_mov_b32 a123, a75\n"
"v_accvgpr_mov_b32 a136, a88\n"
"v_accvgpr_mov_b32 a137, a89\n"
"v_accvgpr_mov_b32 a138, a90\n"
"v_accvgpr_mov_b32 a139, a91\n"
"s_branch LTC_%=\n"
"LTH2_%=:\n"
"s_waitcnt lgkmcnt(0)\n"
"v_mfma_f32_16x16x32_bf16 a[224:227], v[176:179], a[244:247], a[224:227]\n"
"s_waitcnt vmcnt(8)\n"
"v_exp_f32 v129, v129\n"
"v_mfma_f32_16x16x32_bf16 v[16:19], v[152:155], a[244:247], v[16:19]\n"
"s_barrier\n"
"v_mfma_f32_16x16x32_bf16 v[16:19], v[156:159], a[248:251], v[16:19]\n"
"ds_read_b128 v[208:211], v228 offset:0\n"
"v_cvt_pk_bf16_f32 v100, v128, v129\n"
"v_mfma_f32_16x16x32_bf16 v[48:51], v[160:163], a[244:247], v[48:51]\n"
"ds_read_b128 v[212:215], v229 offset:0\n"
"v_cvt_pk_bf16_f32 v101, v130, v131\n"
"v_mfma_f32_16x16x32_bf16 v[48:51], v[164:167], a[248:251], v[48:51]\n"
"ds_read_b128 v[216:219], v228 offset:1024\n"
"v_cvt_pk_bf16_f32 v116, v136, v137\n"
"v_mfma_f32_16x16x32_bf16 v[80:83], v[168:171], a[244:247], v[80:83]\n"
"v_exp_f32 v141, v141\n"
"v_mfma_f32_16x16x32_bf16 v[80:83], v[172:175], a[248:251], v[80:83]\n"
"v_exp_f32 v143, v143\n"
"v_mfma_f32_16x16x32_bf16 v[84:87], v[172:175], v[224:227], v[84:87]\n"
"v_cvt_pk_bf16_f32 v102, v140, v141\n"
"v_cvt_pk_bf16_f32 v103, v142, v143\n"
"v_mfma_f32_16x16x32_bf16 v[84:87], v[168:171], a[252:255], v[84:87]\n"
"ds_read_b64_tr_b16 a[244:245], v234 offset:49152\n"
"ds_read_b64_tr_b16 a[246:247], v234 offset:53248\n"
"v_mfma_f32_16x16x32_bf16 v[52:55], v[164:167], v[224:227], v[52:55]\n"
"v_exp_f32 v148, v148\n"
"v_mfma_f32_16x16x32_bf16 v[52:55], v[160:163], a[252:255], v[52:55]\n"
"v_exp_f32 v149, v149\n"
"v_mfma_f32_16x16x32_bf16 v[20:23], v[156:159], v[224:227], v[20:23]\n"
"ds_read_b64_tr_b16 a[248:249], v238 offset:49152\n"
"ds_read_b64_tr_b16 a[250:251], v238 offset:53248\n"
"v_mfma_f32_16x16x32_bf16 v[20:23], v[152:155], a[252:255], v[20:23]\n"
"v_exp_f32 v150, v150\n"
"v_mfma_f32_16x16x32_bf16 a[228:231], v[176:179], a[252:255], a[228:231]\n"
"v_exp_f32 v151, v151\n"
"s_waitcnt lgkmcnt(2)\n"
"v_mfma_f32_16x16x32_bf16 a[216:219], v[120:123], a[244:247], a[216:219]\n"
"s_add_u32 m0, s44, 81920\n"
"v_cvt_pk_bf16_f32 v117, v138, v139\n"
"v_cvt_pk_bf16_f32 v118, v148, v149\n"
"v_smfmac_f32_16x16x64_bf16 v[196:199], a[240:243], v[96:103], v241\n"
"buffer_load_dwordx4 v240, s[20:23], s56 offen nt lds\n"
"v_mfma_f32_16x16x32_bf16 v[8:11], v[96:99], a[244:247], v[8:11]\n"
"s_waitcnt lgkmcnt(0)\n"
"v_cvt_pk_bf16_f32 v119, v150, v151\n"
"v_cvt_pk_bf16_f32 v108, v132, v133\n"
"v_mfma_f32_16x16x32_bf16 v[8:11], v[100:103], a[248:251], v[8:11]\n"
"v_exp_f32 v144, v144\n"
"v_exp_f32 v145, v145\n"
"v_exp_f32 v146, v146\n"
"v_exp_f32 v147, v147\n"
"v_smfmac_f32_16x16x64_bf16 v[200:203], a[240:243], v[112:119], v241\n"
"ds_read_b64_tr_b16 v[224:225], v239 offset:49152\n"
"v_cvt_pk_bf16_f32 v109, v134, v135\n"
"v_cvt_pk_bf16_f32 v110, v144, v145\n"
"v_cvt_pk_bf16_f32 v111, v146, v147\n"
"v_mfma_f32_16x16x32_bf16 v[40:43], v[104:107], a[244:247], v[40:43]\n"
"ds_read_b64_tr_b16 v[226:227], v239 offset:53248\n"
"v_cvt_f32_u32 v254, s43\n"
"v_mfma_f32_16x16x32_bf16 v[40:43], v[108:111], a[248:251], v[40:43]\n"
"ds_read_b64_tr_b16 a[252:253], v235 offset:49152\n"
"ds_read_b64_tr_b16 a[254:255], v235 offset:53248\n"
"v_smfmac_f32_16x16x64_bf16 v[198:201], a[240:243], v[104:111], v241\n"
"ds_read_b128 v[220:223], v229 offset:1024\n"
"v_add_f32 v196, v196, v197\n"
"v_mfma_f32_16x16x32_bf16 v[72:75], v[112:115], a[244:247], v[72:75]\n"
"ds_read_b64_tr_b16 a[244:245], v234 offset:50176\n"
"v_and_b32 v255, 0x3f0, v242\n"
"v_mfma_f32_16x16x32_bf16 v[72:75], v[116:119], a[248:251], v[72:75]\n"
"ds_read_b64_tr_b16 a[246:247], v234 offset:54272\n"
"v_lshrrev_b32 v255, 1, v255\n"
"v_mfma_f32_16x16x32_bf16 v[202:205], a[240:243], v[120:123], v[202:205]\n"
"s_waitcnt lgkmcnt(5)\n"
"ds_read_b64_tr_b16 a[248:249], v238 offset:50176\n"
"v_sub_u32 v255, v242, v255\n"
"v_mfma_f32_16x16x32_bf16 v[76:79], v[116:119], v[224:227], v[76:79]\n"
"s_waitcnt lgkmcnt(4)\n"
"ds_read_b64_tr_b16 a[250:251], v238 offset:54272\n"
"v_xor_b32 v250, 0x1000, v255\n"
"v_mfma_f32_16x16x32_bf16 v[76:79], v[112:115], a[252:255], v[76:79]\n"
"v_mfma_f32_16x16x32_bf16 v[44:47], v[108:111], v[224:227], v[44:47]\n"
"v_add_f32 v198, v198, v199\n"
"v_add_f32 v200, v200, v201\n"
"v_mfma_f32_16x16x32_bf16 v[44:47], v[104:107], a[252:255], v[44:47]\n"
"s_add_u32 s40, s56, s52\n"
"v_add_f32 v249, v196, v198\n"
"v_add_f32 v249, v249, v200\n"
"v_mfma_f32_16x16x32_bf16 v[12:15], v[100:103], v[224:227], v[12:15]\n"
"buffer_load_dwordx4 v240, s[20:23], s40 offen offset:1024 nt lds\n"
"v_mfma_f32_16x16x32_bf16 v[12:15], v[96:99], a[252:255], v[12:15]\n"
"ds_read_b64_tr_b16 v[224:225], v239 offset:50176\n"
"v_add_f32 v254, v254, v202\n"
"v_min3_f32 v254, v254, v196, v198\n"
"v_min_f32 v254, v254, v200\n"
"v_rcp_f32 v254, v254\n"
"v_mfma_f32_16x16x32_bf16 a[220:223], v[120:123], a[252:255], a[220:223]\n"
"ds_read_b64_tr_b16 v[226:227], v239 offset:54272\n"
"v_add_f32 v249, v249, v202\n"
"v_fmac_f32 v249, 0x53800000, v254\n"
"v_cmp_nge_f32 vcc, s46, v249\n"
"s_cbranch_vccnz LSLOW_%=\nLBACK2_%=:\ns_mov_b64 vcc, s[64:65]\n"
"s_waitcnt lgkmcnt(4)\n"
"v_mfma_f32_16x16x32_bf16 a[232:235], v[120:123], a[244:247], a[232:235]\n"
"v_xor_b32 v207, 0x400, v248\n"
"v_bfe_u32 v206, v242, 8, 2\n"
"v_lshl_add_u32 v206, v206, 4, s43\n"
"v_mfma_f32_16x16x32_bf16 v[24:27], v[96:99], a[244:247], v[24:27]\n"
"s_waitcnt lgkmcnt(2)\n"
"ds_read_b128 a[196:199], v207 offset:4096\n"
"ds_read_b128 a[204:207], v207 offset:6144\n"
"ds_read_b128 v[152:155], v206 offset:0\n"
"v_xor_b32 v207, 64, v206\n"
"v_mfma_f32_16x16x32_bf16 v[24:27], v[100:103], a[248:251], v[24:27]\n"
"ds_read_b128 v[156:159], v207 offset:0\n"
"v_accvgpr_mov_b32 a0, a144\n"
"v_mfma_f32_16x16x32_bf16 v[56:59], v[104:107], a[244:247], v[56:59]\n"
"ds_read_b128 v[160:163], v206 offset:1024\n"
"v_accvgpr_mov_b32 a1, a145\n"
"v_mfma_f32_16x16x32_bf16 v[56:59], v[108:111], a[248:251], v[56:59]\n"
"ds_read_b128 v[164:167], v207 offset:1024\n"
"v_accvgpr_mov_b32 a2, a146\n"
"v_mfma_f32_16x16x32_bf16 v[88:91], v[112:115], a[244:247], v[88:91]\n"
"v_accvgpr_mov_b32 a3, a147\n"
"v_accvgpr_mov_b32 a4, a148\n"
"v_mfma_f32_16x16x32_bf16 v[88:91], v[116:119], a[248:251], v[88:91]\n"
"s_waitcnt lgkmcnt(3)\n"
"v_accvgpr_mov_b32 a5, a149\n"
"v_accvgpr_mov_b32 a6, a150\n"
"v_accvgpr_mov_b32 a7, a151\n"
"v_mfma_f32_16x16x32_bf16 v[180:183], v[152:155], a[0:3], 0\n"
"s_waitcnt lgkmcnt(2)\n"
"v_accvgpr_mov_b32 a8, a152\n"
"v_accvgpr_mov_b32 a9, a153\n"
"v_accvgpr_mov_b32 a10, a154\n"
"v_accvgpr_mov_b32 a11, a155\n"
"v_mfma_f32_16x16x32_bf16 v[180:183], v[156:159], a[4:7], v[180:183]\n"
"s_waitcnt lgkmcnt(1)\n"
"v_accvgpr_mov_b32 a12, a156\n"
"v_accvgpr_mov_b32 a13, a157\n"
"v_accvgpr_mov_b32 a14, a158\n"
"v_accvgpr_mov_b32 a15, a159\n"
"v_mfma_f32_16x16x32_bf16 v[180:183], v[160:163], a[8:11], v[180:183]\n"
"s_waitcnt lgkmcnt(0)\n"
"v_accvgpr_mov_b32 a16, a160\n"
"v_accvgpr_mov_b32 a17, a161\n"
"v_accvgpr_mov_b32 a18, a162\n"
"v_accvgpr_mov_b32 a19, a163\n"
"v_mfma_f32_16x16x32_bf16 v[180:183], v[164:167], a[12:15], v[180:183]\n"
"v_accvgpr_mov_b32 a20, a164\n"
"v_accvgpr_mov_b32 a21, a165\n"
"v_accvgpr_mov_b32 a22, a166\n"
"v_accvgpr_mov_b32 a23, a167\n"
"v_mfma_f32_16x16x32_bf16 v[184:187], v[152:155], a[16:19], 0\n"
"s_add_u32 s40, s56, s53\n"
"v_accvgpr_mov_b32 a24, a168\n"
"v_accvgpr_mov_b32 a25, a169\n"
"v_accvgpr_mov_b32 a26, a170\n"
"v_accvgpr_mov_b32 a27, a171\n"
"v_mfma_f32_16x16x32_bf16 v[184:187], v[156:159], a[20:23], v[184:187]\n"
"buffer_load_dwordx4 v240, s[20:23], s40 offen offset:2048 nt lds\n"
"v_accvgpr_mov_b32 a28, a172\n"
"v_accvgpr_mov_b32 a29, a173\n"
"v_accvgpr_mov_b32 a30, a174\n"
"v_accvgpr_mov_b32 a31, a175\n"
"v_mfma_f32_16x16x32_bf16 v[184:187], v[160:163], a[24:27], v[184:187]\n"
"ds_read_b128 a[192:195], v248 offset:4096\n"
"v_accvgpr_mov_b32 a32, a176\n"
"v_accvgpr_mov_b32 a33, a177\n"
"v_accvgpr_mov_b32 a34, a178\n"
"v_accvgpr_mov_b32 a35, a179\n"
"v_mfma_f32_16x16x32_bf16 v[184:187], v[164:167], a[28:31], v[184:187]\n"
"v_accvgpr_mov_b32 a36, a180\n"
"v_accvgpr_mov_b32 a37, a181\n"
"v_accvgpr_mov_b32 a38, a182\n"
"v_accvgpr_mov_b32 a39, a183\n"
"v_mfma_f32_16x16x32_bf16 v[188:191], v[152:155], a[32:35], 0\n"
"ds_read_b128 a[200:203], v248 offset:6144\n"
"v_accvgpr_mov_b32 a40, a184\n"
"v_accvgpr_mov_b32 a41, a185\n"
"v_accvgpr_mov_b32 a42, a186\n"
"v_accvgpr_mov_b32 a43, a187\n"
"v_mfma_f32_16x16x32_bf16 v[188:191], v[156:159], a[36:39], v[188:191]\n"
"v_accvgpr_mov_b32 a44, a188\n"
"v_accvgpr_mov_b32 a45, a189\n"
"v_accvgpr_mov_b32 a46, a190\n"
"v_accvgpr_mov_b32 a47, a191\n"
"v_mfma_f32_16x16x32_bf16 v[188:191], v[160:163], a[40:43], v[188:191]\n"
"v_mov_b32 v252, v192\n"
"v_xor_b32 v180, s77, v180\n"
"v_mfma_f32_16x16x32_bf16 v[188:191], v[164:167], a[44:47], v[188:191]\n"
"s_waitcnt lgkmcnt(1)\n"
"v_xor_b32 v181, s77, v181\n"
"v_xor_b32 v182, s77, v182\n"
"v_mfma_f32_16x16x32_bf16 v[192:195], v[152:155], a[192:195], 0\n"
"v_rcp_f32 v243, v196\n"
"v_mfma_f32_16x16x32_bf16 v[192:195], v[156:159], a[196:199], v[192:195]\n"
"s_waitcnt lgkmcnt(0)\n"
"v_xor_b32 v183, s77, v183\n"
"v_xor_b32 v184, s77, v184\n"
"v_mfma_f32_16x16x32_bf16 v[192:195], v[160:163], a[200:203], v[192:195]\n"
"v_xor_b32 v185, s77, v185\n"
"v_xor_b32 v186, s77, v186\n"
"v_mfma_f32_16x16x32_bf16 v[192:195], v[164:167], a[204:207], v[192:195]\n"
"v_rcp_f32 v244, v198\n"
"v_mfma_f32_16x16x32_bf16 v[124:127], v[208:211], a[0:3], v[180:183]\n"
"v_rcp_f32 v245, v200\n"
"v_mfma_f32_16x16x32_bf16 v[124:127], v[212:215], a[4:7], v[124:127]\n"
"ds_read_b64_tr_b16 a[252:253], v235 offset:50176\n"
"v_xor_b32 v187, s77, v187\n"
"v_mfma_f32_16x16x32_bf16 v[124:127], v[216:219], a[8:11], v[124:127]\n"
"s_add_u32 s40, s56, s54\n"
"ds_read_b64_tr_b16 a[254:255], v235 offset:54272\n"
"v_xor_b32 v188, s77, v188\n"
"v_mfma_f32_16x16x32_bf16 v[124:127], v[220:223], a[12:15], v[124:127]\n"
"buffer_load_dwordx4 v240, s[20:23], s40 offen offset:3072 nt lds\n"
"v_mfma_f32_16x16x32_bf16 v[128:131], v[216:219], a[24:27], v[184:187]\n"
"s_add_u32 s55, s55, s59\n"
"ds_read_b64_tr_b16 a[244:245], v232 offset:49152\n"
"v_xor_b32 v189, s77, v189\n"
"v_mfma_f32_16x16x32_bf16 v[128:131], v[220:223], a[28:31], v[128:131]\n"
"s_add_u32 s41, s55, s57\n"
"ds_read_b64_tr_b16 a[246:247], v232 offset:53248\n"
"v_xor_b32 v190, s77, v190\n"
"v_mfma_f32_16x16x32_bf16 v[128:131], v[208:211], a[16:19], v[128:131]\n"
"s_and_b32 s41, s41, s58\n"
"ds_read_b64_tr_b16 a[248:249], v236 offset:49152\n"
"v_xor_b32 v191, s77, v191\n"
"v_mfma_f32_16x16x32_bf16 v[128:131], v[212:215], a[20:23], v[128:131]\n"
"s_andn2_b32 s56, s55, s58\n"
"ds_read_b64_tr_b16 a[250:251], v236 offset:53248\n"
"v_xor_b32 v192, s77, v192\n"
"v_mfma_f32_16x16x32_bf16 v[132:135], v[208:211], a[32:35], v[188:191]\n"
"s_or_b32 s56, s56, s41\n"
"v_xor_b32 v193, s77, v193\n"
"v_xor_b32 v194, s77, v194\n"
"v_mfma_f32_16x16x32_bf16 v[132:135], v[212:215], a[36:39], v[132:135]\n"
"v_xor_b32 v195, s77, v195\n"
"v_accvgpr_mov_b32 a148, a52\n"
"v_mfma_f32_16x16x32_bf16 v[132:135], v[216:219], a[40:43], v[132:135]\n"
"v_accvgpr_mov_b32 a149, a53\n"
"v_accvgpr_mov_b32 a150, a54\n"
"v_mfma_f32_16x16x32_bf16 v[132:135], v[220:223], a[44:47], v[132:135]\n"
"v_accvgpr_mov_b32 a151, a55\n"
"v_accvgpr_mov_b32 a164, a68\n"
"v_mfma_f32_16x16x32_bf16 v[136:139], v[216:219], a[200:203], v[192:195]\n"
"ds_read_b128 v[216:219], v228 offset:5120\n"
"v_accvgpr_mov_b32 a165, a69\n"
"v_mfma_f32_16x16x32_bf16 v[136:139], v[220:223], a[204:207], v[136:139]\n"
"ds_read_b128 v[220:223], v229 offset:5120\n"
"v_accvgpr_mov_b32 a166, a70\n"
"v_mfma_f32_16x16x32_bf16 v[136:139], v[208:211], a[192:195], v[136:139]\n"
"ds_read_b128 v[208:211], v228 offset:4096\n"
"v_accvgpr_mov_b32 a167, a71\n"
"v_mfma_f32_16x16x32_bf16 v[136:139], v[212:215], a[196:199], v[136:139]\n"
"ds_read_b128 v[212:215], v229 offset:4096\n"
"v_accvgpr_mov_b32 a180, a84\n"
"v_accvgpr_mov_b32 a181, a85\n"
"v_accvgpr_mov_b32 a182, a86\n"
"v_accvgpr_mov_b32 a183, a87\n"
"v_accvgpr_mov_b32 a156, a60\n"
"v_accvgpr_mov_b32 a157, a61\n"
"v_accvgpr_mov_b32 a158, a62\n"
"v_accvgpr_mov_b32 a159, a63\n"
"v_accvgpr_mov_b32 a172, a76\n"
"v_accvgpr_mov_b32 a173, a77\n"
"v_accvgpr_mov_b32 a174, a78\n"
"v_accvgpr_mov_b32 a175, a79\n"
"v_accvgpr_mov_b32 a188, a92\n"
"v_accvgpr_mov_b32 a189, a93\n"
"v_accvgpr_mov_b32 a190, a94\n"
"v_accvgpr_mov_b32 a191, a95\n"
"v_accvgpr_mov_b32 a144, a48\n"
"v_accvgpr_mov_b32 a145, a49\n"
"v_accvgpr_mov_b32 a146, a50\n"
"v_accvgpr_mov_b32 a147, a51\n"
"v_accvgpr_mov_b32 a160, a64\n"
"v_accvgpr_mov_b32 a161, a65\n"
"v_accvgpr_mov_b32 a162, a66\n"
"v_accvgpr_mov_b32 a163, a67\n"
"v_accvgpr_mov_b32 a176, a80\n"
"v_accvgpr_mov_b32 a177, a81\n"
"v_accvgpr_mov_b32 a178, a82\n"
"v_accvgpr_mov_b32 a179, a83\n"
"v_accvgpr_mov_b32 a152, a56\n"
"v_accvgpr_mov_b32 a153, a57\n"
"v_accvgpr_mov_b32 a154, a58\n"
"v_accvgpr_mov_b32 a155, a59\n"
"v_accvgpr_mov_b32 a168, a72\n"
"v_accvgpr_mov_b32 a169, a73\n"
"v_accvgpr_mov_b32 a170, a74\n"
"v_accvgpr_mov_b32 a171, a75\n"
"v_accvgpr_mov_b32 a184, a88\n"
"v_accvgpr_mov_b32 a185, a89\n"
"v_accvgpr_mov_b32 a186, a90\n"
"v_accvgpr_mov_b32 a187, a91\n"
"LTC_%=:\n"
"v_mfma_f32_16x16x32_bf16 v[92:95], v[116:119], v[224:227], v[92:95]\n"
"s_waitcnt lgkmcnt(4)\n"
"v_exp_f32 v124, v124\n"
"v_mfma_f32_16x16x32_bf16 v[92:95], v[112:115], a[252:255], v[92:95]\n"
"s_add_u32 m0, s44, 32768\n"
"v_exp_f32 v125, v125\n"
"v_mfma_f32_16x16x32_bf16 v[60:63], v[108:111], v[224:227], v[60:63]\n"
"buffer_load_dwordx4 v240, s[16:19], s56 offen nt lds\n"
"v_mfma_f32_16x16x32_bf16 v[60:63], v[104:107], a[252:255], v[60:63]\n"
"v_exp_f32 v126, v126\n"
"v_mfma_f32_16x16x32_bf16 v[28:31], v[100:103], v[224:227], v[28:31]\n"
"ds_read_b64_tr_b16 v[224:225], v237 offset:49152\n"
"v_cvt_pk_bf16_f32 v152, v124, v125\n"
"v_mfma_f32_16x16x32_bf16 v[28:31], v[96:99], a[252:255], v[28:31]\n"
"ds_read_b64_tr_b16 v[226:227], v237 offset:53248\n"
"v_mov_b32 v253, v202\n"
"v_mfma_f32_16x16x32_bf16 a[236:239], v[120:123], a[252:255], a[236:239]\n"
"s_waitcnt lgkmcnt(3)\n"
"ds_write_b64 v250, v[252:253] offset:52224\n"
"v_mfma_f32_16x16x32_bf16 v[140:143], v[216:219], a[8:11], v[180:183]\n"
"ds_read_b64_tr_b16 a[252:253], v233 offset:49152\n"
"v_xor_b32 v254, 0x11000, v242\n"
"v_mfma_f32_16x16x32_bf16 v[140:143], v[220:223], a[12:15], v[140:143]\n"
"ds_write_b128 v254, a[216:219] offset:32768\n"
"v_mfma_f32_16x16x32_bf16 v[140:143], v[208:211], a[0:3], v[140:143]\n"
"s_waitcnt lgkmcnt(5)\n"
"ds_write_b128 v254, a[220:223] offset:33792\n"
"v_mfma_f32_16x16x32_bf16 v[140:143], v[212:215], a[4:7], v[140:143]\n"
"ds_write_b128 v254, a[232:235] offset:34816\n"
"v_mfma_f32_16x16x32_bf16 v[144:147], v[208:211], a[16:19], v[184:187]\n"
"ds_read_b64_tr_b16 a[254:255], v233 offset:53248\n"
"v_mov_b32_dpp v243, v243 row_ror:12 row_mask:0x2 bank_mask:0xf\n"
"v_mfma_f32_16x16x32_bf16 v[144:147], v[212:215], a[20:23], v[144:147]\n"
"ds_write_b128 v254, a[236:239] offset:35840\n"
"v_mfma_f32_16x16x32_bf16 v[144:147], v[216:219], a[24:27], v[144:147]\n"
"v_exp_f32 v127, v127\n"
"v_mfma_f32_16x16x32_bf16 v[144:147], v[220:223], a[28:31], v[144:147]\n"
"v_exp_f32 v128, v128\n"
"v_mfma_f32_16x16x32_bf16 v[148:151], v[216:219], a[40:43], v[188:191]\n"
"s_add_u32 s40, s56, s52\n"
"v_exp_f32 v129, v129\n"
"v_mfma_f32_16x16x32_bf16 v[148:151], v[220:223], a[44:47], v[148:151]\n"
"buffer_load_dwordx4 v240, s[16:19], s40 offen offset:1024 nt lds\n"
"v_mfma_f32_16x16x32_bf16 v[148:151], v[208:211], a[32:35], v[148:151]\n"
"v_exp_f32 v130, v130\n"
"v_mfma_f32_16x16x32_bf16 v[148:151], v[212:215], a[36:39], v[148:151]\n"
"v_cvt_pk_bf16_f32 v153, v126, v127\n"
"v_cvt_pk_bf16_f32 v160, v128, v129\n"
"v_mfma_f32_16x16x32_bf16 v[124:127], v[208:211], a[192:195], v[192:195]\n"
"ds_read_b128 v[208:211], v230 offset:0\n"
"v_mov_b32_dpp v243, v243 row_ror:8 row_mask:0x4 bank_mask:0xf\n"
"v_mfma_f32_16x16x32_bf16 v[124:127], v[212:215], a[196:199], v[124:127]\n"
"ds_read_b128 v[212:215], v231 offset:0\n"
"v_mov_b32_dpp v244, v244 row_ror:12 row_mask:0x2 bank_mask:0xf\n"
"v_mfma_f32_16x16x32_bf16 v[124:127], v[216:219], a[200:203], v[124:127]\n"
"ds_read_b128 v[216:219], v230 offset:1024\n"
"v_mov_b32_dpp v243, v243 row_ror:4 row_mask:0x8 bank_mask:0xf\n"
"v_mfma_f32_16x16x32_bf16 v[124:127], v[220:223], a[204:207], v[124:127]\n"
"v_exp_f32 v131, v131\n"
"v_mfma_f32_16x16x32_bf16 a[208:211], v[120:123], a[244:247], a[208:211]\n"
"ds_read_b128 v[220:223], v231 offset:1024\n"
"v_cvt_pk_bf16_f32 v161, v130, v131\n"
"v_mfma_f32_16x16x32_bf16 v[0:3], v[96:99], a[244:247], v[0:3]\n"
"v_exp_f32 v132, v132\n"
"v_mfma_f32_16x16x32_bf16 v[0:3], v[100:103], a[248:251], v[0:3]\n"
"v_exp_f32 v133, v133\n"
"v_mfma_f32_16x16x32_bf16 v[32:35], v[104:107], a[244:247], v[32:35]\n"
"v_exp_f32 v134, v134\n"
"v_mfma_f32_16x16x32_bf16 v[32:35], v[108:111], a[248:251], v[32:35]\n"
"v_exp_f32 v135, v135\n"
"v_mfma_f32_16x16x32_bf16 v[64:67], v[112:115], a[244:247], v[64:67]\n"
"ds_read_b64_tr_b16 a[244:245], v232 offset:50176\n"
"v_cvt_pk_bf16_f32 v168, v132, v133\n"
"v_mfma_f32_16x16x32_bf16 v[64:67], v[116:119], a[248:251], v[64:67]\n"
"s_waitcnt lgkmcnt(2)\n"
"ds_read_b64_tr_b16 a[246:247], v232 offset:54272\n"
"v_cvt_pk_bf16_f32 v169, v134, v135\n"
"v_mfma_f32_16x16x32_bf16 v[128:131], v[208:211], a[0:3], v[180:183]\n"
"s_add_u32 s40, s56, s53\n"
"v_exp_f32 v136, v136\n"
"v_mfma_f32_16x16x32_bf16 v[128:131], v[212:215], a[4:7], v[128:131]\n"
"buffer_load_dwordx4 v240, s[16:19], s40 offen offset:2048 nt lds\n"
"v_mfma_f32_16x16x32_bf16 v[128:131], v[216:219], a[8:11], v[128:131]\n"
"s_waitcnt lgkmcnt(2)\n"
"v_exp_f32 v137, v137\n"
"v_mfma_f32_16x16x32_bf16 v[128:131], v[220:223], a[12:15], v[128:131]\n"
"v_exp_f32 v138, v138\n"
"v_mfma_f32_16x16x32_bf16 v[132:135], v[216:219], a[24:27], v[184:187]\n"
"ds_read_b64_tr_b16 a[248:249], v236 offset:50176\n"
"v_cvt_pk_bf16_f32 v176, v136, v137\n"
"v_mfma_f32_16x16x32_bf16 v[132:135], v[220:223], a[28:31], v[132:135]\n"
"v_exp_f32 v139, v139\n"
"v_mfma_f32_16x16x32_bf16 v[132:135], v[208:211], a[16:19], v[132:135]\n"
"v_exp_f32 v140, v140\n"
"v_mfma_f32_16x16x32_bf16 v[132:135], v[212:215], a[20:23], v[132:135]\n"
"ds_read_b64_tr_b16 a[250:251], v236 offset:54272\n"
"v_cvt_pk_bf16_f32 v177, v138, v139\n"
"v_mfma_f32_16x16x32_bf16 v[136:139], v[208:211], a[32:35], v[188:191]\n"
"v_exp_f32 v141, v141\n"
"v_mfma_f32_16x16x32_bf16 v[136:139], v[212:215], a[36:39], v[136:139]\n"
"ds_read_b128 v[208:211], v230 offset:4096\n"
"v_cvt_pk_bf16_f32 v154, v140, v141\n"
"v_mfma_f32_16x16x32_bf16 v[136:139], v[216:219], a[40:43], v[136:139]\n"
"v_exp_f32 v142, v142\n"
"v_mfma_f32_16x16x32_bf16 v[136:139], v[220:223], a[44:47], v[136:139]\n"
"v_exp_f32 v143, v143\n"
"v_mfma_f32_16x16x32_bf16 v[68:71], v[116:119], v[224:227], v[68:71]\n"
"ds_read_b128 v[212:215], v231 offset:4096\n"
"v_cvt_pk_bf16_f32 v155, v142, v143\n"
"v_mfma_f32_16x16x32_bf16 v[68:71], v[112:115], a[252:255], v[68:71]\n"
"v_exp_f32 v144, v144\n"
"v_mfma_f32_16x16x32_bf16 v[36:39], v[108:111], v[224:227], v[36:39]\n"
"s_add_u32 s40, s56, s54\n"
"ds_read_b128 v[216:219], v230 offset:5120\n"
"v_mov_b32_dpp v245, v245 row_ror:12 row_mask:0x2 bank_mask:0xf\n"
"v_mfma_f32_16x16x32_bf16 v[36:39], v[104:107], a[252:255], v[36:39]\n"
"buffer_load_dwordx4 v240, s[16:19], s40 offen offset:3072 nt lds\n"
"v_mfma_f32_16x16x32_bf16 v[4:7], v[100:103], v[224:227], v[4:7]\n"
"ds_read_b128 v[220:223], v231 offset:5120\n"
"v_mov_b32_dpp v244, v244 row_ror:8 row_mask:0x4 bank_mask:0xf\n"
"v_mfma_f32_16x16x32_bf16 v[4:7], v[96:99], a[252:255], v[4:7]\n"
"v_exp_f32 v145, v145\n"
"v_mfma_f32_16x16x32_bf16 a[212:215], v[120:123], a[252:255], a[212:215]\n"
"s_waitcnt lgkmcnt(3)\n"
"v_exp_f32 v146, v146\n"
"v_mfma_f32_16x16x32_bf16 v[140:143], v[208:211], a[0:3], v[180:183]\n"
"s_waitcnt lgkmcnt(2)\n"
"v_exp_f32 v147, v147\n"
"v_mfma_f32_16x16x32_bf16 v[140:143], v[212:215], a[4:7], v[140:143]\n"
"s_waitcnt lgkmcnt(1)\n"
"v_cvt_pk_bf16_f32 v162, v144, v145\n"
"v_cvt_pk_bf16_f32 v163, v146, v147\n"
"v_mfma_f32_16x16x32_bf16 v[140:143], v[216:219], a[8:11], v[140:143]\n"
"s_waitcnt lgkmcnt(0)\n"
"v_exp_f32 v148, v148\n"
"v_mfma_f32_16x16x32_bf16 v[140:143], v[220:223], a[12:15], v[140:143]\n"
"v_exp_f32 v149, v149\n"
"v_mfma_f32_16x16x32_bf16 v[144:147], v[216:219], a[24:27], v[184:187]\n"
"v_exp_f32 v150, v150\n"
"v_mfma_f32_16x16x32_bf16 v[144:147], v[220:223], a[28:31], v[144:147]\n"
"v_exp_f32 v151, v151\n"
"v_mfma_f32_16x16x32_bf16 v[144:147], v[208:211], a[16:19], v[144:147]\n"
"ds_read_b64_tr_b16 a[252:253], v233 offset:50176\n"
"ds_read_b64_tr_b16 a[254:255], v233 offset:54272\n"
"v_mfma_f32_16x16x32_bf16 v[144:147], v[212:215], a[20:23], v[144:147]\n"
"ds_read_b64_tr_b16 v[224:225], v237 offset:50176\n"
"ds_read_b64_tr_b16 v[226:227], v237 offset:54272\n"
"v_cvt_pk_bf16_f32 v170, v148, v149\n"
"v_cvt_pk_bf16_f32 v171, v150, v151\n"
"v_mfma_f32_16x16x32_bf16 v[148:151], v[208:211], a[32:35], v[188:191]\n"
"v_mul_f32_dpp v8, v243, v8 row_newbcast:0 row_mask:0xf bank_mask:0xf\n"
"v_mul_f32_dpp v9, v243, v9 row_newbcast:1 row_mask:0xf bank_mask:0xf\n"
"v_mfma_f32_16x16x32_bf16 v[148:151], v[212:215], a[36:39], v[148:151]\n"
"s_cbranch_vccnz LPCT0_%=\nLPKT0_%=:\n"
"v_mul_f32_dpp v10, v243, v10 row_newbcast:2 row_mask:0xf bank_mask:0xf\n"
"v_mfma_f32_16x16x32_bf16 v[148:151], v[216:219], a[40:43], v[148:151]\n"
"v_mul_f32_dpp v11, v243, v11 row_newbcast:3 row_mask:0xf bank_mask:0xf\n"
"v_mul_f32_dpp v12, v243, v12 row_newbcast:0 row_mask:0xf bank_mask:0xf\n"
"v_mfma_f32_16x16x32_bf16 v[148:151], v[220:223], a[44:47], v[148:151]\n"
"v_mul_f32_dpp v13, v243, v13 row_newbcast:1 row_mask:0xf bank_mask:0xf\n"
"v_mul_f32_dpp v14, v243, v14 row_newbcast:2 row_mask:0xf bank_mask:0xf\n"
"v_mul_f32_dpp v15, v243, v15 row_newbcast:3 row_mask:0xf bank_mask:0xf\n"
"v_cvt_pk_bf16_f32 v8, v8, v9\n"
"v_cvt_pk_bf16_f32 v9, v10, v11\n"
"v_cvt_pk_bf16_f32 v10, v12, v13\n"
"v_cvt_pk_bf16_f32 v11, v14, v15\n"
"v_mfma_f32_16x16x32_bf16 a[224:227], v[120:123], a[244:247], a[224:227]\n"
"s_waitcnt vmcnt(8) lgkmcnt(0)\n"
"ds_write_b128 v246, v[8:11] offset:49152\n"
"v_mov_b32_dpp v245, v245 row_ror:8 row_mask:0x4 bank_mask:0xf\n"
"v_mov_b32_dpp v244, v244 row_ror:4 row_mask:0x8 bank_mask:0xf\n"
"v_mfma_f32_16x16x32_bf16 v[16:19], v[96:99], a[244:247], v[16:19]\n"
"s_barrier\n"
"v_mov_b32_dpp v245, v245 row_ror:4 row_mask:0x8 bank_mask:0xf\n"
"v_mul_f32_dpp v40, v244, v40 row_newbcast:0 row_mask:0xf bank_mask:0xf\n"
"v_mul_f32_dpp v41, v244, v41 row_newbcast:1 row_mask:0xf bank_mask:0xf\n"
"v_mul_f32_dpp v42, v244, v42 row_newbcast:2 row_mask:0xf bank_mask:0xf\n"
"v_mul_f32_dpp v43, v244, v43 row_newbcast:3 row_mask:0xf bank_mask:0xf\n"
"v_mul_f32_dpp v44, v244, v44 row_newbcast:0 row_mask:0xf bank_mask:0xf\n"
"v_mul_f32_dpp v45, v244, v45 row_newbcast:1 row_mask:0xf bank_mask:0xf\n"
"v_mul_f32_dpp v46, v244, v46 row_newbcast:2 row_mask:0xf bank_mask:0xf\n"
"v_mul_f32_dpp v47, v244, v47 row_newbcast:3 row_mask:0xf bank_mask:0xf\n"
"v_cvt_pk_bf16_f32 v40, v40, v41\n"
"v_cvt_pk_bf16_f32 v41, v42, v43\n"
"v_cvt_pk_bf16_f32 v42, v44, v45\n"
"v_cvt_pk_bf16_f32 v43, v46, v47\n"
"v_mfma_f32_16x16x32_bf16 v[16:19], v[100:103], a[248:251], v[16:19]\n"
"ds_write_b128 v246, v[40:43] offset:50176\n"
"v_mul_f32_dpp v72, v245, v72 row_newbcast:0 row_mask:0xf bank_mask:0xf\n"
"v_mul_f32_dpp v73, v245, v73 row_newbcast:1 row_mask:0xf bank_mask:0xf\n"
"v_mul_f32_dpp v74, v245, v74 row_newbcast:2 row_mask:0xf bank_mask:0xf\n"
"v_mul_f32_dpp v75, v245, v75 row_newbcast:3 row_mask:0xf bank_mask:0xf\n"
"v_mul_f32_dpp v76, v245, v76 row_newbcast:0 row_mask:0xf bank_mask:0xf\n"
"v_mul_f32_dpp v77, v245, v77 row_newbcast:1 row_mask:0xf bank_mask:0xf\n"
"v_mul_f32_dpp v78, v245, v78 row_newbcast:2 row_mask:0xf bank_mask:0xf\n"
"v_mul_f32_dpp v79, v245, v79 row_newbcast:3 row_mask:0xf bank_mask:0xf\n"
"v_cvt_pk_bf16_f32 v72, v72, v73\n"
"v_cvt_pk_bf16_f32 v73, v74, v75\n"
"v_cvt_pk_bf16_f32 v74, v76, v77\n"
"v_cvt_pk_bf16_f32 v75, v78, v79\n"
"v_mfma_f32_16x16x32_bf16 v[48:51], v[104:107], a[244:247], v[48:51]\n"
"ds_write_b128 v246, v[72:75] offset:51200\n"
"v_mfma_f32_16x16x32_bf16 v[48:51], v[108:111], a[248:251], v[48:51]\n"
"ds_read_b64_tr_b16 a[52:53], v247 offset:49152\n"
"v_mul_f32_dpp v24, v243, v24 row_newbcast:0 row_mask:0xf bank_mask:0xf\n"
"v_mfma_f32_16x16x32_bf16 v[80:83], v[112:115], a[244:247], v[80:83]\n"
"ds_read_b64_tr_b16 a[54:55], v247 offset:49160\n"
"ds_read_b64_tr_b16 a[68:69], v247 offset:50176\n"
"v_mfma_f32_16x16x32_bf16 v[80:83], v[116:119], a[248:251], v[80:83]\n"
"ds_read_b64_tr_b16 a[70:71], v247 offset:50184\n"
"ds_read_b64_tr_b16 a[84:85], v247 offset:51200\n"
"v_mul_f32_dpp v25, v243, v25 row_newbcast:1 row_mask:0xf bank_mask:0xf\n"
"v_mul_f32_dpp v26, v243, v26 row_newbcast:2 row_mask:0xf bank_mask:0xf\n"
"v_mul_f32_dpp v27, v243, v27 row_newbcast:3 row_mask:0xf bank_mask:0xf\n"
"v_mul_f32_dpp v28, v243, v28 row_newbcast:0 row_mask:0xf bank_mask:0xf\n"
"v_mul_f32_dpp v29, v243, v29 row_newbcast:1 row_mask:0xf bank_mask:0xf\n"
"v_mul_f32_dpp v30, v243, v30 row_newbcast:2 row_mask:0xf bank_mask:0xf\n"
"v_mul_f32_dpp v31, v243, v31 row_newbcast:3 row_mask:0xf bank_mask:0xf\n"
"v_cvt_pk_bf16_f32 v24, v24, v25\n"
"v_cvt_pk_bf16_f32 v25, v26, v27\n"
"v_cvt_pk_bf16_f32 v26, v28, v29\n"
"v_cvt_pk_bf16_f32 v27, v30, v31\n"
"v_mfma_f32_16x16x32_bf16 v[84:87], v[116:119], v[224:227], v[84:87]\n"
"ds_write_b128 v246, v[24:27] offset:49152\n"
"v_mul_f32_dpp v56, v244, v56 row_newbcast:0 row_mask:0xf bank_mask:0xf\n"
"v_mul_f32_dpp v57, v244, v57 row_newbcast:1 row_mask:0xf bank_mask:0xf\n"
"v_mul_f32_dpp v58, v244, v58 row_newbcast:2 row_mask:0xf bank_mask:0xf\n"
"v_mul_f32_dpp v59, v244, v59 row_newbcast:3 row_mask:0xf bank_mask:0xf\n"
"v_mul_f32_dpp v60, v244, v60 row_newbcast:0 row_mask:0xf bank_mask:0xf\n"
"v_mul_f32_dpp v61, v244, v61 row_newbcast:1 row_mask:0xf bank_mask:0xf\n"
"v_mul_f32_dpp v62, v244, v62 row_newbcast:2 row_mask:0xf bank_mask:0xf\n"
"v_mul_f32_dpp v63, v244, v63 row_newbcast:3 row_mask:0xf bank_mask:0xf\n"
"v_cvt_pk_bf16_f32 v56, v56, v57\n"
"v_cvt_pk_bf16_f32 v57, v58, v59\n"
"v_cvt_pk_bf16_f32 v58, v60, v61\n"
"v_cvt_pk_bf16_f32 v59, v62, v63\n"
"v_mfma_f32_16x16x32_bf16 v[84:87], v[112:115], a[252:255], v[84:87]\n"
"ds_read_b64_tr_b16 a[86:87], v247 offset:51208\n"
"ds_write_b128 v246, v[56:59] offset:50176\n"
"ds_read_b128 v[208:211], v228 offset:16384\n"
"v_mul_f32_dpp v88, v245, v88 row_newbcast:0 row_mask:0xf bank_mask:0xf\n"
"v_mul_f32_dpp v89, v245, v89 row_newbcast:1 row_mask:0xf bank_mask:0xf\n"
"v_mul_f32_dpp v90, v245, v90 row_newbcast:2 row_mask:0xf bank_mask:0xf\n"
"v_mul_f32_dpp v91, v245, v91 row_newbcast:3 row_mask:0xf bank_mask:0xf\n"
"v_mul_f32_dpp v92, v245, v92 row_newbcast:0 row_mask:0xf bank_mask:0xf\n"
"v_mul_f32_dpp v93, v245, v93 row_newbcast:1 row_mask:0xf bank_mask:0xf\n"
"v_mul_f32_dpp v94, v245, v94 row_newbcast:2 row_mask:0xf bank_mask:0xf\n"
"v_mul_f32_dpp v95, v245, v95 row_newbcast:3 row_mask:0xf bank_mask:0xf\n"
"v_cvt_pk_bf16_f32 v88, v88, v89\n"
"v_cvt_pk_bf16_f32 v89, v90, v91\n"
"v_cvt_pk_bf16_f32 v90, v92, v93\n"
"v_cvt_pk_bf16_f32 v91, v94, v95\n"
"v_mfma_f32_16x16x32_bf16 v[52:55], v[108:111], v[224:227], v[52:55]\n"
"ds_write_b128 v246, v[88:91] offset:51200\n"
"ds_read_b128 v[212:215], v229 offset:16384\n"
"v_mfma_f32_16x16x32_bf16 v[52:55], v[104:107], a[252:255], v[52:55]\n"
"ds_read_b64_tr_b16 a[60:61], v247 offset:49152\n"
"ds_read_b128 v[216:219], v228 offset:17408\n"
"v_mfma_f32_16x16x32_bf16 v[20:23], v[100:103], v[224:227], v[20:23]\n"
"ds_read_b64_tr_b16 a[62:63], v247 offset:49160\n"
"ds_read_b64_tr_b16 a[76:77], v247 offset:50176\n"
"ds_read_b128 v[220:223], v229 offset:17408\n"
"v_mfma_f32_16x16x32_bf16 v[20:23], v[96:99], a[252:255], v[20:23]\n"
"ds_read_b64_tr_b16 a[78:79], v247 offset:50184\n"
"ds_read_b64_tr_b16 a[92:93], v247 offset:51200\n"
"v_exp_f32 v124, v124\n"
"v_exp_f32 v125, v125\n"
"v_exp_f32 v126, v126\n"
"v_exp_f32 v127, v127\n"
"v_mul_f32_dpp v0, v243, v0 row_newbcast:0 row_mask:0xf bank_mask:0xf\n"
"v_mul_f32_dpp v1, v243, v1 row_newbcast:1 row_mask:0xf bank_mask:0xf\n"
"v_mul_f32_dpp v2, v243, v2 row_newbcast:2 row_mask:0xf bank_mask:0xf\n"
"v_mul_f32_dpp v3, v243, v3 row_newbcast:3 row_mask:0xf bank_mask:0xf\n"
"v_mul_f32_dpp v4, v243, v4 row_newbcast:0 row_mask:0xf bank_mask:0xf\n"
"v_mul_f32_dpp v5, v243, v5 row_newbcast:1 row_mask:0xf bank_mask:0xf\n"
"v_mul_f32_dpp v6, v243, v6 row_newbcast:2 row_mask:0xf bank_mask:0xf\n"
"v_mul_f32_dpp v7, v243, v7 row_newbcast:3 row_mask:0xf bank_mask:0xf\n"
"v_cvt_pk_bf16_f32 v0, v0, v1\n"
"v_cvt_pk_bf16_f32 v1, v2, v3\n"
"v_cvt_pk_bf16_f32 v2, v4, v5\n"
"v_cvt_pk_bf16_f32 v3, v6, v7\n"
"v_mfma_f32_16x16x32_bf16 a[228:231], v[120:123], a[252:255], a[228:231]\n"
"ds_write_b128 v246, v[0:3] offset:49152\n"
"v_cvt_pk_bf16_f32 v178, v124, v125\n"
"v_cvt_pk_bf16_f32 v179, v126, v127\n"
"v_mul_f32_dpp v32, v244, v32 row_newbcast:0 row_mask:0xf bank_mask:0xf\n"
"v_mul_f32_dpp v33, v244, v33 row_newbcast:1 row_mask:0xf bank_mask:0xf\n"
"v_mul_f32_dpp v34, v244, v34 row_newbcast:2 row_mask:0xf bank_mask:0xf\n"
"v_mul_f32_dpp v35, v244, v35 row_newbcast:3 row_mask:0xf bank_mask:0xf\n"
"v_mul_f32_dpp v36, v244, v36 row_newbcast:0 row_mask:0xf bank_mask:0xf\n"
"v_mul_f32_dpp v37, v244, v37 row_newbcast:1 row_mask:0xf bank_mask:0xf\n"
"v_mul_f32_dpp v38, v244, v38 row_newbcast:2 row_mask:0xf bank_mask:0xf\n"
"v_mul_f32_dpp v39, v244, v39 row_newbcast:3 row_mask:0xf bank_mask:0xf\n"
"v_cvt_pk_bf16_f32 v32, v32, v33\n"
"v_cvt_pk_bf16_f32 v33, v34, v35\n"
"v_cvt_pk_bf16_f32 v34, v36, v37\n"
"v_cvt_pk_bf16_f32 v35, v38, v39\n"
"s_waitcnt lgkmcnt(10)\n"
"v_mfma_f32_16x16x32_bf16 v[124:127], v[208:211], a[0:3], v[180:183]\n"
"s_waitcnt lgkmcnt(8)\n"
"ds_read_b64_tr_b16 a[94:95], v247 offset:51208\n"
"ds_write_b128 v246, v[32:35] offset:50176\n"
"v_mul_f32_dpp v64, v245, v64 row_newbcast:0 row_mask:0xf bank_mask:0xf\n"
"v_mul_f32_dpp v65, v245, v65 row_newbcast:1 row_mask:0xf bank_mask:0xf\n"
"v_mul_f32_dpp v66, v245, v66 row_newbcast:2 row_mask:0xf bank_mask:0xf\n"
"v_mul_f32_dpp v67, v245, v67 row_newbcast:3 row_mask:0xf bank_mask:0xf\n"
"v_mul_f32_dpp v68, v245, v68 row_newbcast:0 row_mask:0xf bank_mask:0xf\n"
"v_mul_f32_dpp v69, v245, v69 row_newbcast:1 row_mask:0xf bank_mask:0xf\n"
"v_mul_f32_dpp v70, v245, v70 row_newbcast:2 row_mask:0xf bank_mask:0xf\n"
"v_mul_f32_dpp v71, v245, v71 row_newbcast:3 row_mask:0xf bank_mask:0xf\n"
"v_cvt_pk_bf16_f32 v64, v64, v65\n"
"v_cvt_pk_bf16_f32 v65, v66, v67\n"
"v_cvt_pk_bf16_f32 v66, v68, v69\n"
"v_cvt_pk_bf16_f32 v67, v70, v71\n"
"v_mfma_f32_16x16x32_bf16 v[124:127], v[212:215], a[4:7], v[124:127]\n"
"s_waitcnt lgkmcnt(8)\n"
"ds_write_b128 v246, v[64:67] offset:51200\n"
"v_mfma_f32_16x16x32_bf16 v[124:127], v[216:219], a[8:11], v[124:127]\n"
"s_waitcnt lgkmcnt(6)\n"
"ds_read_b64_tr_b16 a[48:49], v247 offset:49152\n"
"v_exp_f32 v128, v128\n"
"v_exp_f32 v129, v129\n"
"v_exp_f32 v130, v130\n"
"v_exp_f32 v131, v131\n"
"v_mfma_f32_16x16x32_bf16 v[124:127], v[220:223], a[12:15], v[124:127]\n"
"ds_read_b64_tr_b16 a[50:51], v247 offset:49160\n"
"ds_read_b64_tr_b16 a[64:65], v247 offset:50176\n"
"v_cvt_pk_bf16_f32 v156, v128, v129\n"
"v_cvt_pk_bf16_f32 v157, v130, v131\n"
"v_mfma_f32_16x16x32_bf16 v[128:131], v[216:219], a[24:27], v[184:187]\n"
"ds_read_b64_tr_b16 a[66:67], v247 offset:50184\n"
"ds_read_b64_tr_b16 a[80:81], v247 offset:51200\n"
"v_mfma_f32_16x16x32_bf16 v[128:131], v[220:223], a[28:31], v[128:131]\n"
"ds_read_b64_tr_b16 a[82:83], v247 offset:51208\n"
"ds_read_b64 v[250:251], v255 offset:52224\n"
"v_xor_b32 v254, 0x1000, v254\n"
"v_mfma_f32_16x16x32_bf16 v[128:131], v[208:211], a[16:19], v[128:131]\n"
"ds_read_b128 v[24:27], v254 offset:32768\n"
"v_exp_f32 v132, v132\n"
"v_exp_f32 v133, v133\n"
"v_exp_f32 v134, v134\n"
"v_exp_f32 v135, v135\n"
"v_mfma_f32_16x16x32_bf16 v[128:131], v[212:215], a[20:23], v[128:131]\n"
"ds_read_b128 v[28:31], v254 offset:33792\n"
"v_cvt_pk_bf16_f32 v164, v132, v133\n"
"v_cvt_pk_bf16_f32 v165, v134, v135\n"
"v_mfma_f32_16x16x32_bf16 v[132:135], v[208:211], a[32:35], v[188:191]\n"
"ds_read_b128 v[56:59], v254 offset:34816\n"
"v_mov_b64 v[196:197], 0\n"
"v_mfma_f32_16x16x32_bf16 v[132:135], v[212:215], a[36:39], v[132:135]\n"
"ds_read_b128 v[60:63], v254 offset:35840\n"
"v_mov_b64 v[198:199], 0\n"
"v_mfma_f32_16x16x32_bf16 v[132:135], v[216:219], a[40:43], v[132:135]\n"
"ds_read_b64_tr_b16 a[244:245], v234 offset:0\n"
"ds_read_b64_tr_b16 a[246:247], v234 offset:4096\n"
"v_exp_f32 v136, v136\n"
"v_exp_f32 v137, v137\n"
"v_exp_f32 v138, v138\n"
"v_exp_f32 v139, v139\n"
"v_mfma_f32_16x16x32_bf16 v[132:135], v[220:223], a[44:47], v[132:135]\n"
"v_cvt_pk_bf16_f32 v172, v136, v137\n"
"v_cvt_pk_bf16_f32 v173, v138, v139\n"
"v_mfma_f32_16x16x32_bf16 v[136:139], v[216:219], a[200:203], v[192:195]\n"
"ds_read_b64_tr_b16 a[248:249], v238 offset:0\n"
"ds_read_b64_tr_b16 a[250:251], v238 offset:4096\n"
"v_mfma_f32_16x16x32_bf16 v[136:139], v[220:223], a[204:207], v[136:139]\n"
"v_exp_f32 v140, v140\n"
"v_mfma_f32_16x16x32_bf16 v[136:139], v[208:211], a[192:195], v[136:139]\n"
"v_exp_f32 v141, v141\n"
"v_mfma_f32_16x16x32_bf16 v[136:139], v[212:215], a[196:199], v[136:139]\n"
"s_waitcnt lgkmcnt(2)\n"
"ds_read_b64_tr_b16 v[224:225], v239 offset:0\n"
"v_exp_f32 v142, v142\n"
"v_exp_f32 v143, v143\n"
"v_mfma_f32_16x16x32_bf16 a[216:219], v[176:179], a[244:247], 0\n"
"s_add_u32 m0, s44, 98304\n"
"ds_read_b64_tr_b16 v[226:227], v239 offset:4096\n"
"v_cvt_pk_bf16_f32 v158, v140, v141\n"
"v_cvt_pk_bf16_f32 v159, v142, v143\n"
"v_mfma_f32_16x16x32_bf16 v[8:11], v[152:155], a[244:247], 0\n"
"s_waitcnt lgkmcnt(2)\n"
"buffer_load_dwordx4 v240, s[20:23], s56 offen nt lds\n"
"v_exp_f32 v144, v144\n"
"v_exp_f32 v145, v145\n"
"v_exp_f32 v146, v146\n"
"v_exp_f32 v147, v147\n"
"v_mfma_f32_16x16x32_bf16 v[8:11], v[156:159], a[248:251], v[8:11]\n"
"ds_read_b64_tr_b16 a[252:253], v235 offset:0\n"
"ds_read_b64_tr_b16 a[254:255], v235 offset:4096\n"
"v_cvt_pk_bf16_f32 v166, v144, v145\n"
"v_cvt_pk_bf16_f32 v167, v146, v147\n"
"v_mfma_f32_16x16x32_bf16 v[40:43], v[160:163], a[244:247], 0\n"
"v_exp_f32 v148, v148\n"
"v_exp_f32 v149, v149\n"
"v_exp_f32 v150, v150\n"
"v_exp_f32 v151, v151\n"
"v_mfma_f32_16x16x32_bf16 v[40:43], v[164:167], a[248:251], v[40:43]\n"
"ds_read_b128 v[216:219], v228 offset:21504\n"
"v_cvt_pk_bf16_f32 v174, v148, v149\n"
"v_cvt_pk_bf16_f32 v175, v150, v151\n"
"v_mfma_f32_16x16x32_bf16 v[72:75], v[168:171], a[244:247], 0\n"
"ds_read_b128 v[220:223], v229 offset:21504\n"
"v_mov_b64 v[202:203], 0\n"
"v_mfma_f32_16x16x32_bf16 v[72:75], v[172:175], a[248:251], v[72:75]\n"
"s_waitcnt lgkmcnt(4)\n"
"ds_read_b128 v[208:211], v228 offset:20480\n"
"v_min_f32 v249, v252, v250\n"
"v_mfma_f32_16x16x32_bf16 v[76:79], v[172:175], v[224:227], 0\n"
"s_waitcnt lgkmcnt(3)\n"
"ds_read_b128 v[212:215], v229 offset:20480\n"
"v_sub_f32 v252, v249, v252\n"
"v_mfma_f32_16x16x32_bf16 v[76:79], v[168:171], a[252:255], v[76:79]\n"
"v_exp_f32 v252, v252\n"
"v_mfma_f32_16x16x32_bf16 v[44:47], v[164:167], v[224:227], 0\n"
"ds_read_b64_tr_b16 a[244:245], v234 offset:1024\n"
"v_sub_f32 v250, v249, v250\n"
"v_mfma_f32_16x16x32_bf16 v[44:47], v[160:163], a[252:255], v[44:47]\n"
"s_add_u32 s40, s56, s52\n"
"v_exp_f32 v250, v250\n"
"v_mfma_f32_16x16x32_bf16 v[12:15], v[156:159], v[224:227], 0\n"
"buffer_load_dwordx4 v240, s[20:23], s40 offen offset:1024 nt lds\n"
"v_mfma_f32_16x16x32_bf16 v[12:15], v[152:155], a[252:255], v[12:15]\n"
"ds_read_b64_tr_b16 a[246:247], v234 offset:5120\n"
"v_mul_f32 v253, v253, v252\n"
"v_mfma_f32_16x16x32_bf16 a[220:223], v[176:179], a[252:255], 0\n"
"ds_read_b64_tr_b16 a[248:249], v238 offset:1024\n"
"v_fmac_f32 v253, v251, v250\n"
"v_smfmac_f32_16x16x64_bf16 v[196:199], a[240:243], v[152:159], v241\n"
"v_rcp_f32 v253, v253\n"
"v_mfma_f32_16x16x32_bf16 v[202:205], a[240:243], v[176:179], v[202:205]\n"
"s_waitcnt lgkmcnt(3)\n"
"ds_read_b64_tr_b16 a[250:251], v238 offset:5120\n"
"v_mul_f32 v250, v250, v253\n"
"v_mfma_f32_16x16x32_bf16 v[140:143], v[216:219], a[8:11], v[180:183]\n"
"ds_read_b64_tr_b16 v[224:225], v239 offset:1024\n"
"v_mul_f32 v252, v252, v253\n"
"v_mfma_f32_16x16x32_bf16 v[140:143], v[220:223], a[12:15], v[140:143]\n"
"ds_read_b64_tr_b16 v[226:227], v239 offset:5120\n"
"v_mov_b32_dpp v250, v250 row_ror:12 row_mask:0x2 bank_mask:0xf\n"
"v_mfma_f32_16x16x32_bf16 v[140:143], v[208:211], a[0:3], v[140:143]\n"
"ds_read_b64_tr_b16 a[252:253], v235 offset:1024\n"
"v_mov_b32_dpp v252, v252 row_ror:12 row_mask:0x2 bank_mask:0xf\n"
"v_mfma_f32_16x16x32_bf16 v[140:143], v[212:215], a[4:7], v[140:143]\n"
"ds_read_b64_tr_b16 a[254:255], v235 offset:5120\n"
"v_mov_b32_dpp v250, v250 row_ror:8 row_mask:0x4 bank_mask:0xf\n"
"v_mfma_f32_16x16x32_bf16 v[144:147], v[208:211], a[16:19], v[184:187]\n"
"s_add_u32 s40, s56, s53\n"
"v_exp_f32 v124, v124\n"
"v_mfma_f32_16x16x32_bf16 v[144:147], v[212:215], a[20:23], v[144:147]\n"
"buffer_load_dwordx4 v240, s[20:23], s40 offen offset:2048 nt lds\n"
"v_mfma_f32_16x16x32_bf16 v[144:147], v[216:219], a[24:27], v[144:147]\n"
"v_exp_f32 v125, v125\n"
"v_mfma_f32_16x16x32_bf16 v[144:147], v[220:223], a[28:31], v[144:147]\n"
"v_exp_f32 v126, v126\n"
"v_mfma_f32_16x16x32_bf16 v[148:151], v[216:219], a[40:43], v[188:191]\n"
"v_exp_f32 v127, v127\n"
"v_mfma_f32_16x16x32_bf16 v[148:151], v[220:223], a[44:47], v[148:151]\n"
"v_mov_b32_dpp v252, v252 row_ror:8 row_mask:0x4 bank_mask:0xf\n"
"v_mov_b32_dpp v250, v250 row_ror:4 row_mask:0x8 bank_mask:0xf\n"
"v_mfma_f32_16x16x32_bf16 v[148:151], v[208:211], a[32:35], v[148:151]\n"
"v_mov_b32_dpp v252, v252 row_ror:4 row_mask:0x8 bank_mask:0xf\n"
"v_accvgpr_read_b32 v249, a208\n"
"v_mfma_f32_16x16x32_bf16 v[148:151], v[212:215], a[36:39], v[148:151]\n"
"v_mul_f32_dpp v24, v250, v24 row_newbcast:0 row_mask:0xf bank_mask:0xf\n"
"v_accvgpr_read_b32 v206, a209\n"
"v_mul_f32_dpp v25, v250, v25 row_newbcast:1 row_mask:0xf bank_mask:0xf\n"
"v_accvgpr_read_b32 v253, a210\n"
"v_mul_f32_dpp v26, v250, v26 row_newbcast:2 row_mask:0xf bank_mask:0xf\n"
"v_accvgpr_read_b32 v251, a211\n"
"v_mul_f32_dpp v27, v250, v27 row_newbcast:3 row_mask:0xf bank_mask:0xf\n"
"v_cvt_pk_bf16_f32 v96, v124, v125\n"
"v_cvt_pk_bf16_f32 v97, v126, v127\n"
"v_mfma_f32_16x16x32_bf16 v[124:127], v[208:211], a[192:195], v[192:195]\n"
"ds_read_b128 v[208:211], v230 offset:16384\n"
"v_fmac_f32_dpp v24, v252, v249 row_newbcast:0 row_mask:0xf bank_mask:0xf\n"
"v_fmac_f32_dpp v25, v252, v206 row_newbcast:1 row_mask:0xf bank_mask:0xf\n"
"v_fmac_f32_dpp v26, v252, v253 row_newbcast:2 row_mask:0xf bank_mask:0xf\n"
"v_fmac_f32_dpp v27, v252, v251 row_newbcast:3 row_mask:0xf bank_mask:0xf\n"
"v_accvgpr_read_b32 v249, a212\n"
"v_mul_f32_dpp v28, v250, v28 row_newbcast:0 row_mask:0xf bank_mask:0xf\n"
"v_accvgpr_read_b32 v206, a213\n"
"v_mul_f32_dpp v29, v250, v29 row_newbcast:1 row_mask:0xf bank_mask:0xf\n"
"v_accvgpr_read_b32 v253, a214\n"
"v_mul_f32_dpp v30, v250, v30 row_newbcast:2 row_mask:0xf bank_mask:0xf\n"
"v_accvgpr_read_b32 v251, a215\n"
"v_mul_f32_dpp v31, v250, v31 row_newbcast:3 row_mask:0xf bank_mask:0xf\n"
"v_mfma_f32_16x16x32_bf16 v[124:127], v[212:215], a[196:199], v[124:127]\n"
"ds_read_b128 v[212:215], v231 offset:16384\n"
"v_fmac_f32_dpp v28, v252, v249 row_newbcast:0 row_mask:0xf bank_mask:0xf\n"
"v_fmac_f32_dpp v29, v252, v206 row_newbcast:1 row_mask:0xf bank_mask:0xf\n"
"v_fmac_f32_dpp v30, v252, v253 row_newbcast:2 row_mask:0xf bank_mask:0xf\n"
"v_fmac_f32_dpp v31, v252, v251 row_newbcast:3 row_mask:0xf bank_mask:0xf\n"
"v_cvt_pk_bf16_f32 v24, v24, v25\n"
"v_cvt_pk_bf16_f32 v25, v26, v27\n"
"v_cvt_pk_bf16_f32 v26, v28, v29\n"
"v_cvt_pk_bf16_f32 v27, v30, v31\n"
"v_bfe_u32 v207, v242, 8, 2\n"
"v_lshlrev_b32 v207, 6, v207\n"
"v_xor_b32 v207, v207, v248\n"
"v_accvgpr_read_b32 v249, a224\n"
"v_mul_f32_dpp v56, v250, v56 row_newbcast:0 row_mask:0xf bank_mask:0xf\n"
"v_accvgpr_read_b32 v206, a225\n"
"v_mul_f32_dpp v57, v250, v57 row_newbcast:1 row_mask:0xf bank_mask:0xf\n"
"v_accvgpr_read_b32 v253, a226\n"
"v_mul_f32_dpp v58, v250, v58 row_newbcast:2 row_mask:0xf bank_mask:0xf\n"
"v_accvgpr_read_b32 v251, a227\n"
"v_mul_f32_dpp v59, v250, v59 row_newbcast:3 row_mask:0xf bank_mask:0xf\n"
"v_mfma_f32_16x16x32_bf16 v[124:127], v[216:219], a[200:203], v[124:127]\n"
"ds_write_b128 v207, v[24:27] offset:0\n"
"v_fmac_f32_dpp v56, v252, v249 row_newbcast:0 row_mask:0xf bank_mask:0xf\n"
"v_fmac_f32_dpp v57, v252, v206 row_newbcast:1 row_mask:0xf bank_mask:0xf\n"
"v_fmac_f32_dpp v58, v252, v253 row_newbcast:2 row_mask:0xf bank_mask:0xf\n"
"v_fmac_f32_dpp v59, v252, v251 row_newbcast:3 row_mask:0xf bank_mask:0xf\n"
"v_accvgpr_read_b32 v249, a228\n"
"v_mul_f32_dpp v60, v250, v60 row_newbcast:0 row_mask:0xf bank_mask:0xf\n"
"v_accvgpr_read_b32 v206, a229\n"
"v_mul_f32_dpp v61, v250, v61 row_newbcast:1 row_mask:0xf bank_mask:0xf\n"
"v_accvgpr_read_b32 v253, a230\n"
"v_mul_f32_dpp v62, v250, v62 row_newbcast:2 row_mask:0xf bank_mask:0xf\n"
"v_accvgpr_read_b32 v251, a231\n"
"v_mul_f32_dpp v63, v250, v63 row_newbcast:3 row_mask:0xf bank_mask:0xf\n"
"v_mfma_f32_16x16x32_bf16 v[124:127], v[220:223], a[204:207], v[124:127]\n"
"s_waitcnt lgkmcnt(3)\n"
"ds_read_b128 v[216:219], v230 offset:17408\n"
"v_fmac_f32_dpp v60, v252, v249 row_newbcast:0 row_mask:0xf bank_mask:0xf\n"
"v_fmac_f32_dpp v61, v252, v206 row_newbcast:1 row_mask:0xf bank_mask:0xf\n"
"v_fmac_f32_dpp v62, v252, v253 row_newbcast:2 row_mask:0xf bank_mask:0xf\n"
"v_fmac_f32_dpp v63, v252, v251 row_newbcast:3 row_mask:0xf bank_mask:0xf\n"
"v_cvt_pk_bf16_f32 v56, v56, v57\n"
"v_cvt_pk_bf16_f32 v57, v58, v59\n"
"v_cvt_pk_bf16_f32 v58, v60, v61\n"
"v_cvt_pk_bf16_f32 v59, v62, v63\n"
"v_mfma_f32_16x16x32_bf16 a[232:235], v[176:179], a[244:247], 0\n"
"s_add_u32 s40, s56, s54\n"
"ds_write_b128 v207, v[56:59] offset:2048\n"
"v_mfma_f32_16x16x32_bf16 v[24:27], v[152:155], a[244:247], 0\n"
"buffer_load_dwordx4 v240, s[20:23], s40 offen offset:3072 nt lds\n"
"v_mfma_f32_16x16x32_bf16 v[24:27], v[156:159], a[248:251], v[24:27]\n"
"s_add_u32 s55, s55, s59\n"
"v_exp_f32 v128, v128\n"
"v_mfma_f32_16x16x32_bf16 v[56:59], v[160:163], a[244:247], 0\n"
"s_add_u32 s41, s55, s57\n"
"ds_read_b128 v[220:223], v231 offset:17408\n"
"v_mov_b64 v[200:201], 0\n"
"v_mfma_f32_16x16x32_bf16 v[56:59], v[164:167], a[248:251], v[56:59]\n"
"s_and_b32 s41, s41, s58\n"
"v_exp_f32 v129, v129\n"
"v_mfma_f32_16x16x32_bf16 v[88:91], v[168:171], a[244:247], 0\n"
"s_andn2_b32 s56, s55, s58\n"
"ds_read_b64_tr_b16 a[244:245], v232 offset:0\n"
"v_cvt_pk_bf16_f32 v104, v128, v129\n"
"v_mfma_f32_16x16x32_bf16 v[88:91], v[172:175], a[248:251], v[88:91]\n"
"s_or_b32 s56, s56, s41\n"
"v_exp_f32 v130, v130\n"
"v_mfma_f32_16x16x32_bf16 v[92:95], v[172:175], v[224:227], 0\n"
"v_exp_f32 v131, v131\n"
"v_mfma_f32_16x16x32_bf16 v[92:95], v[168:171], a[252:255], v[92:95]\n"
"ds_read_b64_tr_b16 a[246:247], v232 offset:4096\n"
"v_cvt_pk_bf16_f32 v105, v130, v131\n"
"v_mfma_f32_16x16x32_bf16 v[60:63], v[164:167], v[224:227], 0\n"
"s_add_u32 m0, s44, 49152\n"
"v_exp_f32 v132, v132\n"
"v_mfma_f32_16x16x32_bf16 v[60:63], v[160:163], a[252:255], v[60:63]\n"
"buffer_load_dwordx4 v240, s[16:19], s56 offen nt lds\n"
"v_mfma_f32_16x16x32_bf16 v[28:31], v[156:159], v[224:227], 0\n"
"v_exp_f32 v133, v133\n"
"v_mfma_f32_16x16x32_bf16 v[28:31], v[152:155], a[252:255], v[28:31]\n"
"v_exp_f32 v134, v134\n"
"v_mfma_f32_16x16x32_bf16 a[236:239], v[176:179], a[252:255], 0\n"
"ds_read_b64_tr_b16 a[248:249], v236 offset:0\n"
"v_cvt_pk_bf16_f32 v112, v132, v133\n"
"v_smfmac_f32_16x16x64_bf16 v[198:201], a[240:243], v[160:167], v241\n"
"s_waitcnt lgkmcnt(2)\n"
"v_exp_f32 v135, v135\n"
"v_mfma_f32_16x16x32_bf16 v[128:131], v[208:211], a[0:3], v[180:183]\n"
"v_exp_f32 v136, v136\n"
"v_mfma_f32_16x16x32_bf16 v[128:131], v[212:215], a[4:7], v[128:131]\n"
"ds_read_b64_tr_b16 a[250:251], v236 offset:4096\n"
"v_cvt_pk_bf16_f32 v113, v134, v135\n"
"v_mfma_f32_16x16x32_bf16 v[128:131], v[216:219], a[8:11], v[128:131]\n"
"v_exp_f32 v137, v137\n"
"v_mfma_f32_16x16x32_bf16 v[128:131], v[220:223], a[12:15], v[128:131]\n"
"v_exp_f32 v138, v138\n"
"v_mfma_f32_16x16x32_bf16 v[132:135], v[216:219], a[24:27], v[184:187]\n"
"s_add_u32 s40, s56, s52\n"
"ds_read_b64_tr_b16 v[224:225], v237 offset:0\n"
"v_cvt_pk_bf16_f32 v120, v136, v137\n"
"v_mfma_f32_16x16x32_bf16 v[132:135], v[220:223], a[28:31], v[132:135]\n"
"buffer_load_dwordx4 v240, s[16:19], s40 offen offset:1024 nt lds\n"
"v_mfma_f32_16x16x32_bf16 v[132:135], v[208:211], a[16:19], v[132:135]\n"
"v_exp_f32 v139, v139\n"
"v_mfma_f32_16x16x32_bf16 v[132:135], v[212:215], a[20:23], v[132:135]\n"
"ds_read_b64_tr_b16 v[226:227], v237 offset:4096\n"
"v_cvt_pk_bf16_f32 v121, v138, v139\n"
"v_mfma_f32_16x16x32_bf16 v[136:139], v[208:211], a[32:35], v[188:191]\n"
"ds_read_b64_tr_b16 a[252:253], v233 offset:0\n"
"v_xor_b32 v246, s75, v246\n"
"v_mfma_f32_16x16x32_bf16 v[136:139], v[212:215], a[36:39], v[136:139]\n"
"ds_read_b64_tr_b16 a[254:255], v233 offset:4096\n"
"v_mul_f32_dpp v16, v243, v16 row_newbcast:0 row_mask:0xf bank_mask:0xf\n"
"v_mfma_f32_16x16x32_bf16 v[136:139], v[216:219], a[40:43], v[136:139]\n"
"ds_read_b128 v[208:211], v230 offset:20480\n"
"v_mul_f32_dpp v17, v243, v17 row_newbcast:1 row_mask:0xf bank_mask:0xf\n"
"v_mfma_f32_16x16x32_bf16 v[136:139], v[220:223], a[44:47], v[136:139]\n"
"s_waitcnt lgkmcnt(5)\n"
"ds_read_b128 v[212:215], v231 offset:20480\n"
"v_mul_f32_dpp v18, v243, v18 row_newbcast:2 row_mask:0xf bank_mask:0xf\n"
"v_mfma_f32_16x16x32_bf16 a[208:211], v[176:179], a[244:247], 0\n"
"ds_read_b128 v[216:219], v230 offset:21504\n"
"v_mul_f32_dpp v19, v243, v19 row_newbcast:3 row_mask:0xf bank_mask:0xf\n"
"v_mfma_f32_16x16x32_bf16 v[0:3], v[152:155], a[244:247], 0\n"
"ds_read_b128 v[220:223], v231 offset:21504\n"
"v_mul_f32_dpp v20, v243, v20 row_newbcast:0 row_mask:0xf bank_mask:0xf\n"
"v_mfma_f32_16x16x32_bf16 v[0:3], v[156:159], a[248:251], v[0:3]\n"
"v_exp_f32 v140, v140\n"
"v_mfma_f32_16x16x32_bf16 v[32:35], v[160:163], a[244:247], 0\n"
"s_add_u32 s40, s56, s53\n"
"v_exp_f32 v141, v141\n"
"v_mfma_f32_16x16x32_bf16 v[32:35], v[164:167], a[248:251], v[32:35]\n"
"buffer_load_dwordx4 v240, s[16:19], s40 offen offset:2048 nt lds\n"
"v_mfma_f32_16x16x32_bf16 v[64:67], v[168:171], a[244:247], 0\n"
"v_exp_f32 v142, v142\n"
"v_mfma_f32_16x16x32_bf16 v[64:67], v[172:175], a[248:251], v[64:67]\n"
"s_waitcnt lgkmcnt(3)\n"
"v_exp_f32 v143, v143\n"
"v_mfma_f32_16x16x32_bf16 v[68:71], v[172:175], v[224:227], 0\n"
"v_mul_f32_dpp v21, v243, v21 row_newbcast:1 row_mask:0xf bank_mask:0xf\n"
"v_mul_f32_dpp v22, v243, v22 row_newbcast:2 row_mask:0xf bank_mask:0xf\n"
"v_mfma_f32_16x16x32_bf16 v[68:71], v[168:171], a[252:255], v[68:71]\n"
"v_mul_f32_dpp v23, v243, v23 row_newbcast:3 row_mask:0xf bank_mask:0xf\n"
"v_cvt_pk_bf16_f32 v16, v16, v17\n"
"v_mfma_f32_16x16x32_bf16 v[36:39], v[164:167], v[224:227], 0\n"
"v_cvt_pk_bf16_f32 v17, v18, v19\n"
"v_cvt_pk_bf16_f32 v18, v20, v21\n"
"v_mfma_f32_16x16x32_bf16 v[36:39], v[160:163], a[252:255], v[36:39]\n"
"v_cvt_pk_bf16_f32 v19, v22, v23\n"
"v_mul_f32_dpp v48, v244, v48 row_newbcast:0 row_mask:0xf bank_mask:0xf\n"
"v_mfma_f32_16x16x32_bf16 v[4:7], v[156:159], v[224:227], 0\n"
"ds_write_b128 v246, v[16:19] offset:49152\n"
"v_mfma_f32_16x16x32_bf16 v[4:7], v[152:155], a[252:255], v[4:7]\n"
"v_mul_f32_dpp v49, v244, v49 row_newbcast:1 row_mask:0xf bank_mask:0xf\n"
"v_mul_f32_dpp v50, v244, v50 row_newbcast:2 row_mask:0xf bank_mask:0xf\n"
"v_mfma_f32_16x16x32_bf16 a[212:215], v[176:179], a[252:255], 0\n"
"v_mul_f32_dpp v51, v244, v51 row_newbcast:3 row_mask:0xf bank_mask:0xf\n"
"v_mul_f32_dpp v52, v244, v52 row_newbcast:0 row_mask:0xf bank_mask:0xf\n"
"v_smfmac_f32_16x16x64_bf16 v[200:203], a[240:243], v[168:175], v241\n"
"v_mul_f32_dpp v53, v244, v53 row_newbcast:1 row_mask:0xf bank_mask:0xf\n"
"v_mul_f32_dpp v54, v244, v54 row_newbcast:2 row_mask:0xf bank_mask:0xf\n"
"v_mul_f32_dpp v55, v244, v55 row_newbcast:3 row_mask:0xf bank_mask:0xf\n"
"v_cvt_pk_bf16_f32 v48, v48, v49\n"
"v_cvt_pk_bf16_f32 v49, v50, v51\n"
"v_cvt_pk_bf16_f32 v50, v52, v53\n"
"v_cvt_pk_bf16_f32 v51, v54, v55\n"
"v_cvt_pk_bf16_f32 v98, v140, v141\n"
"v_cvt_pk_bf16_f32 v99, v142, v143\n"
"v_mfma_f32_16x16x32_bf16 v[140:143], v[208:211], a[0:3], v[180:183]\n"
"s_waitcnt lgkmcnt(1)\n"
"s_add_u32 s40, s56, s54\n"
"ds_write_b128 v246, v[48:51] offset:50176\n"
"v_mfma_f32_16x16x32_bf16 v[140:143], v[212:215], a[4:7], v[140:143]\n"
"buffer_load_dwordx4 v240, s[16:19], s40 offen offset:3072 nt lds\n"
"v_mul_f32_dpp v80, v245, v80 row_newbcast:0 row_mask:0xf bank_mask:0xf\n"
"v_mul_f32_dpp v81, v245, v81 row_newbcast:1 row_mask:0xf bank_mask:0xf\n"
"v_mul_f32_dpp v82, v245, v82 row_newbcast:2 row_mask:0xf bank_mask:0xf\n"
"v_mul_f32_dpp v83, v245, v83 row_newbcast:3 row_mask:0xf bank_mask:0xf\n"
"v_mul_f32_dpp v84, v245, v84 row_newbcast:0 row_mask:0xf bank_mask:0xf\n"
"v_mul_f32_dpp v85, v245, v85 row_newbcast:1 row_mask:0xf bank_mask:0xf\n"
"v_mul_f32_dpp v86, v245, v86 row_newbcast:2 row_mask:0xf bank_mask:0xf\n"
"v_mul_f32_dpp v87, v245, v87 row_newbcast:3 row_mask:0xf bank_mask:0xf\n"
"v_cvt_pk_bf16_f32 v80, v80, v81\n"
"v_cvt_pk_bf16_f32 v81, v82, v83\n"
"v_cvt_pk_bf16_f32 v82, v84, v85\n"
"v_cvt_pk_bf16_f32 v83, v86, v87\n"
"v_mfma_f32_16x16x32_bf16 v[140:143], v[216:219], a[8:11], v[140:143]\n"
"ds_write_b128 v246, v[80:83] offset:51200\n"
"v_xor_b32 v247, s75, v247\n"
"v_exp_f32 v144, v144\n"
"v_exp_f32 v145, v145\n"
"v_exp_f32 v146, v146\n"
"v_exp_f32 v147, v147\n"
"v_mfma_f32_16x16x32_bf16 v[140:143], v[220:223], a[12:15], v[140:143]\n"
"ds_read_b64_tr_b16 a[56:57], v247 offset:49152\n"
"ds_read_b64_tr_b16 a[58:59], v247 offset:49160\n"
"v_cvt_pk_bf16_f32 v106, v144, v145\n"
"v_cvt_pk_bf16_f32 v107, v146, v147\n"
"v_mfma_f32_16x16x32_bf16 v[144:147], v[216:219], a[24:27], v[184:187]\n"
"ds_read_b64_tr_b16 a[72:73], v247 offset:50176\n"
"ds_read_b64_tr_b16 a[74:75], v247 offset:50184\n"
"v_mfma_f32_16x16x32_bf16 v[144:147], v[220:223], a[28:31], v[144:147]\n"
"ds_read_b64_tr_b16 a[88:89], v247 offset:51200\n"
"ds_read_b64_tr_b16 a[90:91], v247 offset:51208\n"
"v_mfma_f32_16x16x32_bf16 v[144:147], v[208:211], a[16:19], v[144:147]\n"
"ds_read_b64_tr_b16 a[244:245], v232 offset:1024\n"
"ds_read_b64_tr_b16 a[246:247], v232 offset:5120\n"
"v_exp_f32 v148, v148\n"
"v_exp_f32 v149, v149\n"
"v_exp_f32 v150, v150\n"
"v_exp_f32 v151, v151\n"
"v_mfma_f32_16x16x32_bf16 v[144:147], v[212:215], a[20:23], v[144:147]\n"
"v_cvt_pk_bf16_f32 v114, v148, v149\n"
"v_cvt_pk_bf16_f32 v115, v150, v151\n"
"v_mfma_f32_16x16x32_bf16 v[148:151], v[208:211], a[32:35], v[188:191]\n"
"ds_read_b64_tr_b16 a[248:249], v236 offset:1024\n"
"ds_read_b64_tr_b16 a[250:251], v236 offset:5120\n"
"v_mfma_f32_16x16x32_bf16 v[148:151], v[212:215], a[36:39], v[148:151]\n"
"s_cbranch_vccnz LPCT1_%=\nLPKT1_%=:\n"
"ds_read_b64_tr_b16 v[224:225], v237 offset:1024\n"
"v_mfma_f32_16x16x32_bf16 v[148:151], v[216:219], a[40:43], v[148:151]\n"
"v_exp_f32 v124, v124\n"
"v_mfma_f32_16x16x32_bf16 v[148:151], v[220:223], a[44:47], v[148:151]\n"
"s_waitcnt lgkmcnt(5)\n"
"ds_read_b64_tr_b16 v[226:227], v237 offset:5120\n"
"s_waitcnt lgkmcnt(4)\n"
"v_mfma_f32_16x16x32_bf16 a[224:227], v[176:179], a[244:247], 0\n"
"s_waitcnt vmcnt(8)\n"
"v_exp_f32 v125, v125\n"
"v_mfma_f32_16x16x32_bf16 v[16:19], v[152:155], a[244:247], 0\n"
"s_barrier\n"
"s_waitcnt lgkmcnt(2)\n"
"v_mfma_f32_16x16x32_bf16 v[16:19], v[156:159], a[248:251], v[16:19]\n"
"ds_read_b64_tr_b16 a[252:253], v233 offset:1024\n"
"ds_read_b64_tr_b16 a[254:255], v233 offset:5120\n"
"v_mfma_f32_16x16x32_bf16 v[48:51], v[160:163], a[244:247], 0\n"
"v_exp_f32 v126, v126\n"
"v_mfma_f32_16x16x32_bf16 v[48:51], v[164:167], a[248:251], v[48:51]\n"
"v_exp_f32 v127, v127\n"
"v_mfma_f32_16x16x32_bf16 v[80:83], v[168:171], a[244:247], 0\n"
"v_exp_f32 v130, v130\n"
"v_mfma_f32_16x16x32_bf16 v[80:83], v[172:175], a[248:251], v[80:83]\n"
"s_waitcnt lgkmcnt(2)\n"
"v_exp_f32 v131, v131\n"
"v_mfma_f32_16x16x32_bf16 v[84:87], v[172:175], v[224:227], 0\n"
"s_waitcnt lgkmcnt(0)\n"
"ds_read_b128 v[208:211], v228 offset:32768\n"
"v_xor_b32 v246, s75, v246\n"
"v_mfma_f32_16x16x32_bf16 v[84:87], v[168:171], a[252:255], v[84:87]\n"
"ds_read_b128 v[212:215], v229 offset:32768\n"
"v_xor_b32 v247, s75, v247\n"
"v_mfma_f32_16x16x32_bf16 v[52:55], v[164:167], v[224:227], 0\n"
"ds_read_b128 v[216:219], v228 offset:33792\n"
"v_cvt_pk_bf16_f32 v122, v124, v125\n"
"v_mfma_f32_16x16x32_bf16 v[52:55], v[160:163], a[252:255], v[52:55]\n"
"ds_read_b128 v[220:223], v229 offset:33792\n"
"v_cvt_pk_bf16_f32 v123, v126, v127\n"
"v_mfma_f32_16x16x32_bf16 v[20:23], v[156:159], v[224:227], 0\n"
"ds_read_b64_tr_b16 a[246:247], v234 offset:20480\n"
"v_cvt_pk_bf16_f32 v101, v130, v131\n"
"v_mfma_f32_16x16x32_bf16 v[20:23], v[152:155], a[252:255], v[20:23]\n"
"ds_read_b64_tr_b16 a[248:249], v238 offset:16384\n"
"ds_read_b64_tr_b16 a[250:251], v238 offset:20480\n"
"v_exp_f32 v132, v132\n"
"v_exp_f32 v133, v133\n"
"v_exp_f32 v136, v136\n"
"v_exp_f32 v137, v137\n"
"v_exp_f32 v138, v138\n"
"v_exp_f32 v139, v139\n"
"v_mfma_f32_16x16x32_bf16 a[228:231], v[176:179], a[252:255], 0\n"
"ds_read_b64_tr_b16 v[226:227], v239 offset:20480\n"
"v_exp_f32 v128, v128\n"
"v_exp_f32 v129, v129\n"
"v_cvt_pk_bf16_f32 v108, v132, v133\n"
"v_exp_f32 v134, v134\n"
"v_exp_f32 v135, v135\n"
"v_cvt_pk_bf16_f32 v116, v136, v137\n"
"v_cvt_pk_bf16_f32 v117, v138, v139\n"
"v_exp_f32 v140, v140\n"
"v_exp_f32 v142, v142\n"
"v_exp_f32 v144, v144\n"
"v_exp_f32 v145, v145\n"
"v_exp_f32 v147, v147\n"
"v_exp_f32 v150, v150\n"
"v_exp_f32 v151, v151\n"
"v_add_u32 v248, 0x1000, v248\n"
"s_branch LAY2_%=\n"
"LAX_%=:\n"
"s_waitcnt lgkmcnt(0)\n"
"v_mfma_f32_16x16x32_bf16 a[224:227], v[176:179], a[244:247], a[224:227]\n"
"s_waitcnt vmcnt(12)\n"
"v_exp_f32 v129, v129\n"
"v_mfma_f32_16x16x32_bf16 v[16:19], v[152:155], a[244:247], v[16:19]\n"
"s_barrier\n"
"v_mfma_f32_16x16x32_bf16 v[16:19], v[156:159], a[248:251], v[16:19]\n"
"v_exp_f32 v141, v141\n"
"v_mfma_f32_16x16x32_bf16 v[48:51], v[160:163], a[244:247], v[48:51]\n"
"v_exp_f32 v143, v143\n"
"v_mfma_f32_16x16x32_bf16 v[48:51], v[164:167], a[248:251], v[48:51]\n"
"v_exp_f32 v144, v144\n"
"v_mfma_f32_16x16x32_bf16 v[80:83], v[168:171], a[244:247], v[80:83]\n"
"ds_read_b64_tr_b16 a[244:245], v234 offset:49152\n"
"v_cvt_pk_bf16_f32 v100, v128, v129\n"
"v_mfma_f32_16x16x32_bf16 v[80:83], v[172:175], a[248:251], v[80:83]\n"
"v_exp_f32 v148, v148\n"
"v_mfma_f32_16x16x32_bf16 v[84:87], v[172:175], v[224:227], v[84:87]\n"
"ds_read_b64_tr_b16 a[246:247], v234 offset:53248\n"
"v_cvt_pk_bf16_f32 v101, v130, v131\n"
"v_mfma_f32_16x16x32_bf16 v[84:87], v[168:171], a[252:255], v[84:87]\n"
"v_cvt_pk_bf16_f32 v102, v140, v141\n"
"v_cvt_pk_bf16_f32 v103, v142, v143\n"
"v_mfma_f32_16x16x32_bf16 v[52:55], v[164:167], v[224:227], v[52:55]\n"
"v_exp_f32 v149, v149\n"
"v_mfma_f32_16x16x32_bf16 v[52:55], v[160:163], a[252:255], v[52:55]\n"
"v_exp_f32 v150, v150\n"
"v_mfma_f32_16x16x32_bf16 v[20:23], v[156:159], v[224:227], v[20:23]\n"
"ds_read_b64_tr_b16 a[248:249], v238 offset:49152\n"
"ds_read_b64_tr_b16 a[250:251], v238 offset:53248\n"
"v_mfma_f32_16x16x32_bf16 v[20:23], v[152:155], a[252:255], v[20:23]\n"
"v_exp_f32 v151, v151\n"
"v_mfma_f32_16x16x32_bf16 a[228:231], v[176:179], a[252:255], a[228:231]\n"
"v_cvt_pk_bf16_f32 v116, v136, v137\n"
"v_cvt_pk_bf16_f32 v117, v138, v139\n"
"s_waitcnt lgkmcnt(2)\n"
"v_mfma_f32_16x16x32_bf16 a[216:219], v[120:123], a[244:247], a[216:219]\n"
"s_mov_b64 s[28:29], s[8:9]\n"
"v_cvt_pk_bf16_f32 v118, v148, v149\n"
"v_cvt_pk_bf16_f32 v119, v150, v151\n"
"v_smfmac_f32_16x16x64_bf16 v[196:199], a[240:243], v[96:103], v241\n"
"s_mov_b32 s30, 655360\n"
"v_exp_f32 v145, v145\n"
"v_mfma_f32_16x16x32_bf16 v[8:11], v[96:99], a[244:247], v[8:11]\n"
"s_waitcnt lgkmcnt(0)\n"
"s_mov_b32 s71, s44\n"
"v_exp_f32 v146, v146\n"
"v_mfma_f32_16x16x32_bf16 v[8:11], v[100:103], a[248:251], v[8:11]\n"
"s_mov_b32 s70, 1024\n"
"v_exp_f32 v147, v147\n"
"v_smfmac_f32_16x16x64_bf16 v[200:203], a[240:243], v[112:119], v241\n"
"ds_read_b64_tr_b16 v[224:225], v239 offset:49152\n"
"v_cvt_pk_bf16_f32 v108, v132, v133\n"
"v_cvt_pk_bf16_f32 v109, v134, v135\n"
"v_cvt_pk_bf16_f32 v110, v144, v145\n"
"v_cvt_pk_bf16_f32 v111, v146, v147\n"
"v_mfma_f32_16x16x32_bf16 v[40:43], v[104:107], a[244:247], v[40:43]\n"
"ds_read_b64_tr_b16 a[252:253], v235 offset:49152\n"
"ds_read_b64_tr_b16 v[226:227], v239 offset:53248\n"
"v_mfma_f32_16x16x32_bf16 v[40:43], v[108:111], a[248:251], v[40:43]\n"
"s_add_u32 s41, s70, 327680\n"
"s_add_u32 m0, s71, 32768\n"
"ds_read_b64_tr_b16 a[254:255], v235 offset:53248\n"
"v_smfmac_f32_16x16x64_bf16 v[198:201], a[240:243], v[104:111], v241\n"
"buffer_load_dwordx4 v240, s[28:31], s41 offen lds\n"
"v_mfma_f32_16x16x32_bf16 v[72:75], v[112:115], a[244:247], v[72:75]\n"
"ds_read_b64_tr_b16 a[244:245], v234 offset:50176\n"
"v_add_f32 v196, v196, v197\n"
"v_mfma_f32_16x16x32_bf16 v[72:75], v[116:119], a[248:251], v[72:75]\n"
"ds_read_b64_tr_b16 a[246:247], v234 offset:54272\n"
"v_cvt_f32_u32 v254, s43\n"
"v_mfma_f32_16x16x32_bf16 v[202:205], a[240:243], v[120:123], v[202:205]\n"
"s_waitcnt lgkmcnt(3)\n"
"s_add_u32 s40, s41, s52\n"
"ds_read_b64_tr_b16 a[248:249], v238 offset:50176\n"
"v_and_b32 v255, 0x3f0, v242\n"
"v_mfma_f32_16x16x32_bf16 v[76:79], v[116:119], v[224:227], v[76:79]\n"
"s_waitcnt lgkmcnt(3)\n"
"buffer_load_dwordx4 v240, s[28:31], s40 offen offset:1024 lds\n"
"v_mfma_f32_16x16x32_bf16 v[76:79], v[112:115], a[252:255], v[76:79]\n"
"ds_read_b64_tr_b16 a[250:251], v238 offset:54272\n"
"v_lshrrev_b32 v255, 1, v255\n"
"v_mfma_f32_16x16x32_bf16 v[44:47], v[108:111], v[224:227], v[44:47]\n"
"v_add_f32 v198, v198, v199\n"
"v_add_f32 v200, v200, v201\n"
"v_mfma_f32_16x16x32_bf16 v[44:47], v[104:107], a[252:255], v[44:47]\n"
"v_add_f32 v249, v196, v198\n"
"v_add_f32 v249, v249, v200\n"
"v_mfma_f32_16x16x32_bf16 v[12:15], v[100:103], v[224:227], v[12:15]\n"
"s_add_u32 s40, s41, s53\n"
"ds_read_b64_tr_b16 v[224:225], v239 offset:50176\n"
"v_sub_u32 v255, v242, v255\n"
"v_mfma_f32_16x16x32_bf16 v[12:15], v[96:99], a[252:255], v[12:15]\n"
"buffer_load_dwordx4 v240, s[28:31], s40 offen offset:2048 lds\n"
"v_add_f32 v254, v254, v202\n"
"v_min3_f32 v254, v254, v196, v198\n"
"v_min_f32 v254, v254, v200\n"
"v_rcp_f32 v254, v254\n"
"v_mfma_f32_16x16x32_bf16 a[220:223], v[120:123], a[252:255], a[220:223]\n"
"ds_read_b64_tr_b16 v[226:227], v239 offset:54272\n"
"v_add_f32 v249, v249, v202\n"
"v_fmac_f32 v249, 0x53800000, v254\n"
"v_cmp_nge_f32 vcc, s46, v249\n"
"s_cbranch_vccnz LSLOW_%=\nLBACK3_%=:\ns_mov_b64 vcc, s[64:65]\n"
"s_waitcnt lgkmcnt(4)\n"
"v_mfma_f32_16x16x32_bf16 a[232:235], v[120:123], a[244:247], a[232:235]\n"
"v_rcp_f32 v243, v196\n"
"v_mfma_f32_16x16x32_bf16 v[24:27], v[96:99], a[244:247], v[24:27]\n"
"s_waitcnt lgkmcnt(2)\n"
"v_rcp_f32 v244, v198\n"
"v_mfma_f32_16x16x32_bf16 v[24:27], v[100:103], a[248:251], v[24:27]\n"
"s_add_u32 s40, s41, s54\n"
"ds_read_b64_tr_b16 a[252:253], v235 offset:50176\n"
"ds_read_b64_tr_b16 a[254:255], v235 offset:54272\n"
"v_mfma_f32_16x16x32_bf16 v[56:59], v[104:107], a[244:247], v[56:59]\n"
"buffer_load_dwordx4 v240, s[28:31], s40 offen offset:3072 lds\n"
"v_mfma_f32_16x16x32_bf16 v[56:59], v[108:111], a[248:251], v[56:59]\n"
"s_add_u32 s71, s44, s75\n"
"v_rcp_f32 v245, v200\n"
"v_mfma_f32_16x16x32_bf16 v[88:91], v[112:115], a[244:247], v[88:91]\n"
"s_mov_b32 s70, 1152\n"
"ds_read_b64_tr_b16 a[244:245], v232 offset:49152\n"
"v_mov_b32 v252, v192\n"
"v_mfma_f32_16x16x32_bf16 v[88:91], v[116:119], a[248:251], v[88:91]\n"
"s_waitcnt lgkmcnt(3)\n"
"s_add_u32 s41, s70, 163840\n"
"ds_read_b64_tr_b16 a[246:247], v232 offset:53248\n"
"v_mov_b32 v253, v202\n"
"v_mfma_f32_16x16x32_bf16 v[92:95], v[116:119], v[224:227], v[92:95]\n"
"s_waitcnt lgkmcnt(2)\n"
"s_add_u32 m0, s71, 16384\n"
"ds_read_b64_tr_b16 a[248:249], v236 offset:49152\n"
"v_xor_b32 v250, 0x1000, v255\n"
"v_mfma_f32_16x16x32_bf16 v[92:95], v[112:115], a[252:255], v[92:95]\n"
"buffer_load_dwordx4 v240, s[28:31], s41 offen lds\n"
"v_mfma_f32_16x16x32_bf16 v[60:63], v[108:111], v[224:227], v[60:63]\n"
"ds_read_b64_tr_b16 a[250:251], v236 offset:53248\n"
"v_xor_b32 v254, 0x11000, v242\n"
"v_mfma_f32_16x16x32_bf16 v[60:63], v[104:107], a[252:255], v[60:63]\n"
"ds_write_b64 v250, v[252:253] offset:52224\n"
"v_mfma_f32_16x16x32_bf16 v[28:31], v[100:103], v[224:227], v[28:31]\n"
"s_add_u32 s40, s41, s52\n"
"ds_read_b64_tr_b16 v[224:225], v237 offset:49152\n"
"v_mov_b32_dpp v243, v243 row_ror:12 row_mask:0x2 bank_mask:0xf\n"
"v_mfma_f32_16x16x32_bf16 v[28:31], v[96:99], a[252:255], v[28:31]\n"
"buffer_load_dwordx4 v240, s[28:31], s40 offen offset:1024 lds\n"
"v_mfma_f32_16x16x32_bf16 a[236:239], v[120:123], a[252:255], a[236:239]\n"
"s_waitcnt lgkmcnt(4)\n"
"ds_read_b64_tr_b16 v[226:227], v237 offset:53248\n"
"v_bfe_u32 v249, v242, 9, 1\n"
"v_mfma_f32_16x16x32_bf16 a[208:211], v[120:123], a[244:247], a[208:211]\n"
"ds_read_b64_tr_b16 a[252:253], v233 offset:49152\n"
"v_lshl_add_u32 v249, v249, 4, s43\n"
"v_mfma_f32_16x16x32_bf16 v[0:3], v[96:99], a[244:247], v[0:3]\n"
"s_waitcnt lgkmcnt(4)\n"
"ds_read_b64_tr_b16 a[254:255], v233 offset:53248\n"
"v_lshl_add_u32 v249, v249, 6, s75\n"
"v_mfma_f32_16x16x32_bf16 v[0:3], v[100:103], a[248:251], v[0:3]\n"
"s_add_u32 s40, s41, s53\n"
"v_sub_u32 v228, v232, v249\n"
"v_mov_b32_dpp v244, v244 row_ror:12 row_mask:0x2 bank_mask:0xf\n"
"v_mfma_f32_16x16x32_bf16 v[32:35], v[104:107], a[244:247], v[32:35]\n"
"buffer_load_dwordx4 v240, s[28:31], s40 offen offset:2048 lds\n"
"v_mfma_f32_16x16x32_bf16 v[32:35], v[108:111], a[248:251], v[32:35]\n"
"ds_write_b128 v254, a[216:219] offset:32768\n"
"v_mfma_f32_16x16x32_bf16 v[64:67], v[112:115], a[244:247], v[64:67]\n"
"ds_write_b128 v254, a[220:223] offset:33792\n"
"v_mfma_f32_16x16x32_bf16 v[64:67], v[116:119], a[248:251], v[64:67]\n"
"s_waitcnt lgkmcnt(4)\n"
"ds_write_b128 v254, a[232:235] offset:34816\n"
"v_mfma_f32_16x16x32_bf16 v[68:71], v[116:119], v[224:227], v[68:71]\n"
"s_waitcnt lgkmcnt(3)\n"
"s_add_u32 s40, s41, s54\n"
"ds_write_b128 v254, a[236:239] offset:35840\n"
"v_mfma_f32_16x16x32_bf16 v[68:71], v[112:115], a[252:255], v[68:71]\n"
"buffer_load_dwordx4 v240, s[28:31], s40 offen offset:3072 lds\n"
"ds_read_b64_tr_b16 a[244:245], v232 offset:50176\n"
"ds_read_b64_tr_b16 a[246:247], v232 offset:54272\n"
"v_mfma_f32_16x16x32_bf16 v[36:39], v[108:111], v[224:227], v[36:39]\n"
"ds_read_b64_tr_b16 a[248:249], v236 offset:50176\n"
"ds_read_b64_tr_b16 a[250:251], v236 offset:54272\n"
"v_mfma_f32_16x16x32_bf16 v[36:39], v[104:107], a[252:255], v[36:39]\n"
"v_xor_b32 v232, 0x1000, v228\n"
"v_mov_b32_dpp v243, v243 row_ror:8 row_mask:0x4 bank_mask:0xf\n"
"v_mfma_f32_16x16x32_bf16 v[4:7], v[100:103], v[224:227], v[4:7]\n"
"ds_read_b64_tr_b16 v[224:225], v237 offset:50176\n"
"ds_read_b64_tr_b16 v[226:227], v237 offset:54272\n"
"v_mfma_f32_16x16x32_bf16 v[4:7], v[96:99], a[252:255], v[4:7]\n"
"v_mov_b32_dpp v245, v245 row_ror:12 row_mask:0x2 bank_mask:0xf\n"
"v_mov_b32_dpp v244, v244 row_ror:8 row_mask:0x4 bank_mask:0xf\n"
"v_mfma_f32_16x16x32_bf16 a[212:215], v[120:123], a[252:255], a[212:215]\n"
"ds_read_b64_tr_b16 a[252:253], v233 offset:50176\n"
"ds_read_b64_tr_b16 a[254:255], v233 offset:54272\n"
"s_waitcnt lgkmcnt(6)\n"
"v_mfma_f32_16x16x32_bf16 a[224:227], v[120:123], a[244:247], a[224:227]\n"
"s_waitcnt vmcnt(8) lgkmcnt(0)\n"
"v_mov_b32_dpp v243, v243 row_ror:4 row_mask:0x8 bank_mask:0xf\n"
"v_mov_b32_dpp v245, v245 row_ror:8 row_mask:0x4 bank_mask:0xf\n"
"v_mfma_f32_16x16x32_bf16 v[16:19], v[96:99], a[244:247], v[16:19]\n"
"s_barrier\n"
"v_mfma_f32_16x16x32_bf16 v[16:19], v[100:103], a[248:251], v[16:19]\n"
"ds_read_b64_tr_b16 v[212:213], v232 offset:16384\n"
"ds_read_b64_tr_b16 v[214:215], v232 offset:18432\n"
"v_mfma_f32_16x16x32_bf16 v[48:51], v[104:107], a[244:247], v[48:51]\n"
"ds_read_b64_tr_b16 v[216:217], v228 offset:24576\n"
"ds_read_b64_tr_b16 v[218:219], v228 offset:26624\n"
"v_mfma_f32_16x16x32_bf16 v[48:51], v[108:111], a[248:251], v[48:51]\n"
"ds_read_b64_tr_b16 v[220:221], v232 offset:24576\n"
"ds_read_b64_tr_b16 v[222:223], v232 offset:26624\n"
"v_mov_b32_dpp v244, v244 row_ror:4 row_mask:0x8 bank_mask:0xf\n"
"v_mul_f32_dpp v8, v243, v8 row_newbcast:0 row_mask:0xf bank_mask:0xf\n"
"v_mul_f32_dpp v9, v243, v9 row_newbcast:1 row_mask:0xf bank_mask:0xf\n"
"v_mul_f32_dpp v10, v243, v10 row_newbcast:2 row_mask:0xf bank_mask:0xf\n"
"v_mul_f32_dpp v11, v243, v11 row_newbcast:3 row_mask:0xf bank_mask:0xf\n"
"v_mul_f32_dpp v12, v243, v12 row_newbcast:0 row_mask:0xf bank_mask:0xf\n"
"v_mul_f32_dpp v13, v243, v13 row_newbcast:1 row_mask:0xf bank_mask:0xf\n"
"v_mul_f32_dpp v14, v243, v14 row_newbcast:2 row_mask:0xf bank_mask:0xf\n"
"v_mul_f32_dpp v15, v243, v15 row_newbcast:3 row_mask:0xf bank_mask:0xf\n"
"v_cvt_pk_bf16_f32 v8, v8, v9\n"
"v_cvt_pk_bf16_f32 v9, v10, v11\n"
"v_cvt_pk_bf16_f32 v10, v12, v13\n"
"v_cvt_pk_bf16_f32 v11, v14, v15\n"
"v_mfma_f32_16x16x32_bf16 v[80:83], v[112:115], a[244:247], v[80:83]\n"
"ds_write_b128 v246, v[8:11] offset:49152\n"
"v_mov_b32_dpp v245, v245 row_ror:4 row_mask:0x8 bank_mask:0xf\n"
"v_mul_f32_dpp v40, v244, v40 row_newbcast:0 row_mask:0xf bank_mask:0xf\n"
"v_mul_f32_dpp v41, v244, v41 row_newbcast:1 row_mask:0xf bank_mask:0xf\n"
"v_mul_f32_dpp v42, v244, v42 row_newbcast:2 row_mask:0xf bank_mask:0xf\n"
"v_mul_f32_dpp v43, v244, v43 row_newbcast:3 row_mask:0xf bank_mask:0xf\n"
"v_mul_f32_dpp v44, v244, v44 row_newbcast:0 row_mask:0xf bank_mask:0xf\n"
"v_mul_f32_dpp v45, v244, v45 row_newbcast:1 row_mask:0xf bank_mask:0xf\n"
"v_mul_f32_dpp v46, v244, v46 row_newbcast:2 row_mask:0xf bank_mask:0xf\n"
"v_mul_f32_dpp v47, v244, v47 row_newbcast:3 row_mask:0xf bank_mask:0xf\n"
"v_cvt_pk_bf16_f32 v40, v40, v41\n"
"v_cvt_pk_bf16_f32 v41, v42, v43\n"
"v_cvt_pk_bf16_f32 v42, v44, v45\n"
"v_cvt_pk_bf16_f32 v43, v46, v47\n"
"v_mfma_f32_16x16x32_bf16 v[80:83], v[116:119], a[248:251], v[80:83]\n"
"ds_write_b128 v246, v[40:43] offset:50176\n"
"v_mul_f32_dpp v72, v245, v72 row_newbcast:0 row_mask:0xf bank_mask:0xf\n"
"v_mul_f32_dpp v73, v245, v73 row_newbcast:1 row_mask:0xf bank_mask:0xf\n"
"v_mul_f32_dpp v74, v245, v74 row_newbcast:2 row_mask:0xf bank_mask:0xf\n"
"v_mul_f32_dpp v75, v245, v75 row_newbcast:3 row_mask:0xf bank_mask:0xf\n"
"v_mul_f32_dpp v76, v245, v76 row_newbcast:0 row_mask:0xf bank_mask:0xf\n"
"v_mul_f32_dpp v77, v245, v77 row_newbcast:1 row_mask:0xf bank_mask:0xf\n"
"v_mul_f32_dpp v78, v245, v78 row_newbcast:2 row_mask:0xf bank_mask:0xf\n"
"v_mul_f32_dpp v79, v245, v79 row_newbcast:3 row_mask:0xf bank_mask:0xf\n"
"v_cvt_pk_bf16_f32 v72, v72, v73\n"
"v_cvt_pk_bf16_f32 v73, v74, v75\n"
"v_cvt_pk_bf16_f32 v74, v76, v77\n"
"v_cvt_pk_bf16_f32 v75, v78, v79\n"
"v_mfma_f32_16x16x32_bf16 v[84:87], v[116:119], v[224:227], v[84:87]\n"
"ds_write_b128 v246, v[72:75] offset:51200\n"
"v_mfma_f32_16x16x32_bf16 v[84:87], v[112:115], a[252:255], v[84:87]\n"
"ds_read_b64_tr_b16 a[4:5], v247 offset:49152\n"
"ds_read_b64_tr_b16 a[6:7], v247 offset:49160\n"
"ds_read_b64_tr_b16 v[180:181], v228 offset:0\n"
"ds_read_b64_tr_b16 v[182:183], v228 offset:2048\n"
"v_mfma_f32_16x16x32_bf16 v[52:55], v[108:111], v[224:227], v[52:55]\n"
"ds_read_b64_tr_b16 a[20:21], v247 offset:50176\n"
"ds_read_b64_tr_b16 a[22:23], v247 offset:50184\n"
"ds_read_b64_tr_b16 v[184:185], v232 offset:0\n"
"ds_read_b64_tr_b16 v[186:187], v232 offset:2048\n"
"v_mfma_f32_16x16x32_bf16 v[52:55], v[104:107], a[252:255], v[52:55]\n"
"ds_read_b64_tr_b16 a[36:37], v247 offset:51200\n"
"ds_read_b64_tr_b16 a[38:39], v247 offset:51208\n"
"ds_read_b64_tr_b16 v[188:189], v228 offset:8192\n"
"ds_read_b64_tr_b16 v[190:191], v228 offset:10240\n"
"v_mul_f32_dpp v24, v243, v24 row_newbcast:0 row_mask:0xf bank_mask:0xf\n"
"v_mul_f32_dpp v25, v243, v25 row_newbcast:1 row_mask:0xf bank_mask:0xf\n"
"v_mul_f32_dpp v26, v243, v26 row_newbcast:2 row_mask:0xf bank_mask:0xf\n"
"v_mul_f32_dpp v27, v243, v27 row_newbcast:3 row_mask:0xf bank_mask:0xf\n"
"v_mul_f32_dpp v28, v243, v28 row_newbcast:0 row_mask:0xf bank_mask:0xf\n"
"v_mul_f32_dpp v29, v243, v29 row_newbcast:1 row_mask:0xf bank_mask:0xf\n"
"v_mul_f32_dpp v30, v243, v30 row_newbcast:2 row_mask:0xf bank_mask:0xf\n"
"v_mul_f32_dpp v31, v243, v31 row_newbcast:3 row_mask:0xf bank_mask:0xf\n"
"v_cvt_pk_bf16_f32 v24, v24, v25\n"
"v_cvt_pk_bf16_f32 v25, v26, v27\n"
"v_cvt_pk_bf16_f32 v26, v28, v29\n"
"v_cvt_pk_bf16_f32 v27, v30, v31\n"
"v_mfma_f32_16x16x32_bf16 v[20:23], v[100:103], v[224:227], v[20:23]\n"
"ds_write_b128 v246, v[24:27] offset:49152\n"
"ds_read_b64_tr_b16 v[192:193], v232 offset:8192\n"
"ds_read_b64_tr_b16 v[194:195], v232 offset:10240\n"
"v_mul_f32_dpp v56, v244, v56 row_newbcast:0 row_mask:0xf bank_mask:0xf\n"
"v_mul_f32_dpp v57, v244, v57 row_newbcast:1 row_mask:0xf bank_mask:0xf\n"
"v_mul_f32_dpp v58, v244, v58 row_newbcast:2 row_mask:0xf bank_mask:0xf\n"
"v_mul_f32_dpp v59, v244, v59 row_newbcast:3 row_mask:0xf bank_mask:0xf\n"
"v_mul_f32_dpp v60, v244, v60 row_newbcast:0 row_mask:0xf bank_mask:0xf\n"
"v_mul_f32_dpp v61, v244, v61 row_newbcast:1 row_mask:0xf bank_mask:0xf\n"
"v_mul_f32_dpp v62, v244, v62 row_newbcast:2 row_mask:0xf bank_mask:0xf\n"
"v_mul_f32_dpp v63, v244, v63 row_newbcast:3 row_mask:0xf bank_mask:0xf\n"
"v_cvt_pk_bf16_f32 v56, v56, v57\n"
"v_cvt_pk_bf16_f32 v57, v58, v59\n"
"v_cvt_pk_bf16_f32 v58, v60, v61\n"
"v_cvt_pk_bf16_f32 v59, v62, v63\n"
"v_mfma_f32_16x16x32_bf16 v[20:23], v[96:99], a[252:255], v[20:23]\n"
"ds_write_b128 v246, v[56:59] offset:50176\n"
"ds_read_b64_tr_b16 v[208:209], v228 offset:16384\n"
"ds_read_b64_tr_b16 v[210:211], v228 offset:18432\n"
"v_mul_f32_dpp v88, v245, v88 row_newbcast:0 row_mask:0xf bank_mask:0xf\n"
"v_mul_f32_dpp v89, v245, v89 row_newbcast:1 row_mask:0xf bank_mask:0xf\n"
"v_mul_f32_dpp v90, v245, v90 row_newbcast:2 row_mask:0xf bank_mask:0xf\n"
"v_mul_f32_dpp v91, v245, v91 row_newbcast:3 row_mask:0xf bank_mask:0xf\n"
"v_mul_f32_dpp v92, v245, v92 row_newbcast:0 row_mask:0xf bank_mask:0xf\n"
"v_mul_f32_dpp v93, v245, v93 row_newbcast:1 row_mask:0xf bank_mask:0xf\n"
"v_mul_f32_dpp v94, v245, v94 row_newbcast:2 row_mask:0xf bank_mask:0xf\n"
"v_mul_f32_dpp v95, v245, v95 row_newbcast:3 row_mask:0xf bank_mask:0xf\n"
"v_cvt_pk_bf16_f32 v88, v88, v89\n"
"v_cvt_pk_bf16_f32 v89, v90, v91\n"
"v_cvt_pk_bf16_f32 v90, v92, v93\n"
"v_cvt_pk_bf16_f32 v91, v94, v95\n"
"v_mfma_f32_16x16x32_bf16 a[228:231], v[120:123], a[252:255], a[228:231]\n"
"ds_write_b128 v246, v[88:91] offset:51200\n"
"s_waitcnt lgkmcnt(15)\n"
"v_mfma_f32_16x16x32_bf16 v[124:127], v[180:183], a[96:99], 0\n"
"s_waitcnt lgkmcnt(11)\n"
"ds_read_b64_tr_b16 a[12:13], v247 offset:49152\n"
"ds_read_b64_tr_b16 a[14:15], v247 offset:49160\n"
"v_mfma_f32_16x16x32_bf16 v[124:127], v[184:187], a[100:103], v[124:127]\n"
"s_waitcnt lgkmcnt(9)\n"
"ds_read_b64_tr_b16 a[28:29], v247 offset:50176\n"
"ds_read_b64_tr_b16 a[30:31], v247 offset:50184\n"
"v_mfma_f32_16x16x32_bf16 v[124:127], v[188:191], a[104:107], v[124:127]\n"
"s_waitcnt lgkmcnt(8)\n"
"ds_read_b64_tr_b16 a[44:45], v247 offset:51200\n"
"ds_read_b64_tr_b16 a[46:47], v247 offset:51208\n"
"v_mul_f32_dpp v0, v243, v0 row_newbcast:0 row_mask:0xf bank_mask:0xf\n"
"v_mul_f32_dpp v1, v243, v1 row_newbcast:1 row_mask:0xf bank_mask:0xf\n"
"v_mul_f32_dpp v2, v243, v2 row_newbcast:2 row_mask:0xf bank_mask:0xf\n"
"v_mul_f32_dpp v3, v243, v3 row_newbcast:3 row_mask:0xf bank_mask:0xf\n"
"v_mul_f32_dpp v4, v243, v4 row_newbcast:0 row_mask:0xf bank_mask:0xf\n"
"v_mul_f32_dpp v5, v243, v5 row_newbcast:1 row_mask:0xf bank_mask:0xf\n"
"v_mul_f32_dpp v6, v243, v6 row_newbcast:2 row_mask:0xf bank_mask:0xf\n"
"v_mul_f32_dpp v7, v243, v7 row_newbcast:3 row_mask:0xf bank_mask:0xf\n"
"v_cvt_pk_bf16_f32 v0, v0, v1\n"
"v_cvt_pk_bf16_f32 v1, v2, v3\n"
"v_cvt_pk_bf16_f32 v2, v4, v5\n"
"v_cvt_pk_bf16_f32 v3, v6, v7\n"
"v_mfma_f32_16x16x32_bf16 v[124:127], v[192:195], a[108:111], v[124:127]\n"
"s_waitcnt lgkmcnt(7)\n"
"ds_write_b128 v246, v[0:3] offset:49152\n"
"v_mul_f32_dpp v32, v244, v32 row_newbcast:0 row_mask:0xf bank_mask:0xf\n"
"v_mul_f32_dpp v33, v244, v33 row_newbcast:1 row_mask:0xf bank_mask:0xf\n"
"v_mul_f32_dpp v34, v244, v34 row_newbcast:2 row_mask:0xf bank_mask:0xf\n"
"v_mul_f32_dpp v35, v244, v35 row_newbcast:3 row_mask:0xf bank_mask:0xf\n"
"v_mul_f32_dpp v36, v244, v36 row_newbcast:0 row_mask:0xf bank_mask:0xf\n"
"v_mul_f32_dpp v37, v244, v37 row_newbcast:1 row_mask:0xf bank_mask:0xf\n"
"v_mul_f32_dpp v38, v244, v38 row_newbcast:2 row_mask:0xf bank_mask:0xf\n"
"v_mul_f32_dpp v39, v244, v39 row_newbcast:3 row_mask:0xf bank_mask:0xf\n"
"v_cvt_pk_bf16_f32 v32, v32, v33\n"
"v_cvt_pk_bf16_f32 v33, v34, v35\n"
"v_cvt_pk_bf16_f32 v34, v36, v37\n"
"v_cvt_pk_bf16_f32 v35, v38, v39\n"
"v_mfma_f32_16x16x32_bf16 v[124:127], v[208:211], a[144:147], v[124:127]\n"
"ds_write_b128 v246, v[32:35] offset:50176\n"
"v_mul_f32_dpp v64, v245, v64 row_newbcast:0 row_mask:0xf bank_mask:0xf\n"
"v_mul_f32_dpp v65, v245, v65 row_newbcast:1 row_mask:0xf bank_mask:0xf\n"
"v_mul_f32_dpp v66, v245, v66 row_newbcast:2 row_mask:0xf bank_mask:0xf\n"
"v_mul_f32_dpp v67, v245, v67 row_newbcast:3 row_mask:0xf bank_mask:0xf\n"
"v_mul_f32_dpp v68, v245, v68 row_newbcast:0 row_mask:0xf bank_mask:0xf\n"
"v_mul_f32_dpp v69, v245, v69 row_newbcast:1 row_mask:0xf bank_mask:0xf\n"
"v_mul_f32_dpp v70, v245, v70 row_newbcast:2 row_mask:0xf bank_mask:0xf\n"
"v_mul_f32_dpp v71, v245, v71 row_newbcast:3 row_mask:0xf bank_mask:0xf\n"
"v_cvt_pk_bf16_f32 v64, v64, v65\n"
"v_cvt_pk_bf16_f32 v65, v66, v67\n"
"v_cvt_pk_bf16_f32 v66, v68, v69\n"
"v_cvt_pk_bf16_f32 v67, v70, v71\n"
"v_mfma_f32_16x16x32_bf16 v[124:127], v[212:215], a[148:151], v[124:127]\n"
"ds_write_b128 v246, v[64:67] offset:51200\n"
"v_mfma_f32_16x16x32_bf16 v[124:127], v[216:219], a[152:155], v[124:127]\n"
"ds_read_b64_tr_b16 a[0:1], v247 offset:49152\n"
"ds_read_b64_tr_b16 a[2:3], v247 offset:49160\n"
"v_mfma_f32_16x16x32_bf16 v[124:127], v[220:223], a[156:159], v[124:127]\n"
"ds_read_b64_tr_b16 a[16:17], v247 offset:50176\n"
"ds_read_b64_tr_b16 a[18:19], v247 offset:50184\n"
"v_mfma_f32_16x16x32_bf16 v[140:143], v[220:223], a[172:175], 0\n"
"ds_read_b64_tr_b16 a[32:33], v247 offset:51200\n"
"ds_read_b64_tr_b16 a[34:35], v247 offset:51208\n"
"v_xor_b32 v254, 0x1000, v254\n"
"v_mfma_f32_16x16x32_bf16 v[140:143], v[216:219], a[168:171], v[140:143]\n"
"ds_read_b128 v[24:27], v254 offset:32768\n"
"v_add_u32 v236, s76, v247\n"
"v_mfma_f32_16x16x32_bf16 v[140:143], v[212:215], a[164:167], v[140:143]\n"
"ds_read_b128 v[28:31], v254 offset:33792\n"
"v_xor_b32 v237, 0x400, v236\n"
"v_mfma_f32_16x16x32_bf16 v[140:143], v[208:211], a[160:163], v[140:143]\n"
"ds_read_b128 v[56:59], v254 offset:34816\n"
"v_sub_u32 v229, v233, v249\n"
"v_mfma_f32_16x16x32_bf16 v[140:143], v[192:195], a[124:127], v[140:143]\n"
"ds_read_b128 v[60:63], v254 offset:35840\n"
"v_xor_b32 v233, 0x1000, v229\n"
"v_mfma_f32_16x16x32_bf16 v[140:143], v[188:191], a[120:123], v[140:143]\n"
"ds_read_b64 v[250:251], v255 offset:52224\n"
"ds_read_b64_tr_b16 a[252:253], v237 offset:6144\n"
"v_mfma_f32_16x16x32_bf16 v[140:143], v[184:187], a[116:119], v[140:143]\n"
"ds_read_b64_tr_b16 a[248:249], v236 offset:6144\n"
"ds_read_b64_tr_b16 a[254:255], v237 offset:6152\n"
"v_mfma_f32_16x16x32_bf16 v[140:143], v[180:183], a[112:115], v[140:143]\n"
"ds_read_b64_tr_b16 a[244:245], v237 offset:4096\n"
"ds_read_b64_tr_b16 a[250:251], v236 offset:6152\n"
"v_mfma_f32_16x16x32_bf16 v[156:159], v[180:183], a[128:131], 0\n"
"ds_read_b64_tr_b16 a[240:241], v236 offset:4096\n"
"ds_read_b64_tr_b16 a[246:247], v237 offset:4104\n"
"v_mfma_f32_16x16x32_bf16 v[156:159], v[184:187], a[132:135], v[156:159]\n"
"ds_read_b64_tr_b16 a[204:205], v237 offset:2048\n"
"ds_read_b64_tr_b16 a[242:243], v236 offset:4104\n"
"v_mfma_f32_16x16x32_bf16 v[156:159], v[188:191], a[136:139], v[156:159]\n"
"ds_read_b64_tr_b16 a[200:201], v236 offset:2048\n"
"ds_read_b64_tr_b16 a[206:207], v237 offset:2056\n"
"v_mfma_f32_16x16x32_bf16 v[156:159], v[192:195], a[140:143], v[156:159]\n"
"s_waitcnt lgkmcnt(11)\n"
"ds_read_b64_tr_b16 a[196:197], v237 offset:0\n"
"ds_read_b64_tr_b16 a[202:203], v236 offset:2056\n"
"v_mfma_f32_16x16x32_bf16 v[156:159], v[208:211], a[176:179], v[156:159]\n"
"s_mov_b32 s41, 492544\n"
"s_add_u32 m0, s44, 49152\n"
"ds_read_b64_tr_b16 a[198:199], v237 offset:8\n"
"v_mfma_f32_16x16x32_bf16 v[156:159], v[212:215], a[180:183], v[156:159]\n"
"buffer_load_dwordx4 v240, s[28:31], s41 offen lds\n"
"v_mfma_f32_16x16x32_bf16 v[156:159], v[216:219], a[184:187], v[156:159]\n"
"ds_read_b64_tr_b16 a[192:193], v236 offset:0\n"
"ds_read_b64_tr_b16 a[194:195], v236 offset:8\n"
"v_mfma_f32_16x16x32_bf16 v[156:159], v[220:223], a[188:191], v[156:159]\n"
"s_waitcnt lgkmcnt(11)\n"
"s_add_u32 s40, s41, s52\n"
"ds_read_b64_tr_b16 v[96:97], v229 offset:0\n"
"ds_read_b64_tr_b16 v[98:99], v229 offset:2048\n"
"v_mfma_f32_16x16x32_bf16 v[172:175], v[220:223], a[252:255], 0\n"
"buffer_load_dwordx4 v240, s[28:31], s40 offen offset:1024 lds\n"
"v_mfma_f32_16x16x32_bf16 v[172:175], v[216:219], a[248:251], v[172:175]\n"
"s_waitcnt lgkmcnt(9)\n"
"ds_read_b64_tr_b16 v[100:101], v233 offset:0\n"
"ds_read_b64_tr_b16 v[102:103], v233 offset:2048\n"
"v_mfma_f32_16x16x32_bf16 v[172:175], v[212:215], a[244:247], v[172:175]\n"
"s_add_u32 s40, s41, s53\n"
"ds_read_b64_tr_b16 v[104:105], v229 offset:8192\n"
"ds_read_b64_tr_b16 v[106:107], v229 offset:10240\n"
"v_mfma_f32_16x16x32_bf16 v[172:175], v[208:211], a[240:243], v[172:175]\n"
"s_waitcnt lgkmcnt(9)\n"
"buffer_load_dwordx4 v240, s[28:31], s40 offen offset:2048 lds\n"
"v_mfma_f32_16x16x32_bf16 v[172:175], v[192:195], a[204:207], v[172:175]\n"
"ds_read_b64_tr_b16 v[108:109], v233 offset:8192\n"
"ds_read_b64_tr_b16 v[110:111], v233 offset:10240\n"
"v_mfma_f32_16x16x32_bf16 v[172:175], v[188:191], a[200:203], v[172:175]\n"
"s_waitcnt lgkmcnt(10)\n"
"s_add_u32 s40, s41, s54\n"
"ds_read_b64_tr_b16 v[112:113], v229 offset:16384\n"
"ds_read_b64_tr_b16 v[114:115], v229 offset:18432\n"
"v_mfma_f32_16x16x32_bf16 v[172:175], v[184:187], a[196:199], v[172:175]\n"
"s_waitcnt lgkmcnt(10)\n"
"buffer_load_dwordx4 v240, s[28:31], s40 offen offset:3072 lds\n"
"v_mfma_f32_16x16x32_bf16 v[172:175], v[180:183], a[192:195], v[172:175]\n"
"s_waitcnt lgkmcnt(8)\n"
"ds_read_b64_tr_b16 v[116:117], v233 offset:16384\n"
"ds_read_b64_tr_b16 v[118:119], v233 offset:18432\n"
"v_mfma_f32_16x16x32_bf16 v[128:131], v[96:99], a[96:99], 0\n"
"s_waitcnt lgkmcnt(8)\n"
"ds_read_b64_tr_b16 v[120:121], v229 offset:24576\n"
"ds_read_b64_tr_b16 v[122:123], v229 offset:26624\n"
"v_mfma_f32_16x16x32_bf16 v[128:131], v[100:103], a[100:103], v[128:131]\n"
"s_waitcnt lgkmcnt(8)\n"
"ds_read_b64_tr_b16 v[224:225], v233 offset:24576\n"
"ds_read_b64_tr_b16 v[226:227], v233 offset:26624\n"
"v_mfma_f32_16x16x32_bf16 v[128:131], v[104:107], a[104:107], v[128:131]\n"
"s_waitcnt lgkmcnt(8)\n"
"ds_read_b64_tr_b16 a[236:237], v237 offset:10240\n"
"v_sub_u32 v230, v234, v249\n"
"v_mfma_f32_16x16x32_bf16 v[128:131], v[108:111], a[108:111], v[128:131]\n"
"s_waitcnt lgkmcnt(7)\n"
"ds_read_b64_tr_b16 v[180:181], v230 offset:0\n"
"v_xor_b32 v234, 0x1000, v230\n"
"v_mfma_f32_16x16x32_bf16 v[128:131], v[112:115], a[144:147], v[128:131]\n"
"s_waitcnt lgkmcnt(6)\n"
"ds_read_b64_tr_b16 v[182:183], v230 offset:2048\n"
"v_sub_u32 v231, v235, v249\n"
"v_mfma_f32_16x16x32_bf16 v[128:131], v[116:119], a[148:151], v[128:131]\n"
"s_waitcnt lgkmcnt(5)\n"
"ds_read_b64_tr_b16 v[184:185], v234 offset:0\n"
"v_min_f32 v249, v252, v250\n"
"v_mfma_f32_16x16x32_bf16 v[128:131], v[120:123], a[152:155], v[128:131]\n"
"s_waitcnt lgkmcnt(4)\n"
"ds_read_b64_tr_b16 v[186:187], v234 offset:2048\n"
"v_sub_f32 v252, v249, v252\n"
"v_mfma_f32_16x16x32_bf16 v[128:131], v[224:227], a[156:159], v[128:131]\n"
"v_exp_f32 v252, v252\n"
"v_mfma_f32_16x16x32_bf16 v[144:147], v[224:227], a[172:175], 0\n"
"ds_read_b64_tr_b16 v[188:189], v230 offset:8192\n"
"v_sub_f32 v250, v249, v250\n"
"v_mfma_f32_16x16x32_bf16 v[144:147], v[120:123], a[168:171], v[144:147]\n"
"v_exp_f32 v250, v250\n"
"v_mfma_f32_16x16x32_bf16 v[144:147], v[116:119], a[164:167], v[144:147]\n"
"ds_read_b64_tr_b16 v[190:191], v230 offset:10240\n"
"v_mul_f32 v253, v253, v252\n"
"v_mfma_f32_16x16x32_bf16 v[144:147], v[112:115], a[160:163], v[144:147]\n"
"ds_read_b64_tr_b16 v[192:193], v234 offset:8192\n"
"v_fmac_f32 v253, v251, v250\n"
"v_mfma_f32_16x16x32_bf16 v[144:147], v[108:111], a[124:127], v[144:147]\n"
"v_rcp_f32 v253, v253\n"
"v_mfma_f32_16x16x32_bf16 v[144:147], v[104:107], a[120:123], v[144:147]\n"
"ds_read_b64_tr_b16 v[194:195], v234 offset:10240\n"
"v_mul_f32 v250, v250, v253\n"
"v_mfma_f32_16x16x32_bf16 v[144:147], v[100:103], a[116:119], v[144:147]\n"
"ds_read_b64_tr_b16 v[208:209], v230 offset:16384\n"
"v_mul_f32 v252, v252, v253\n"
"v_mfma_f32_16x16x32_bf16 v[144:147], v[96:99], a[112:115], v[144:147]\n"
"ds_read_b64_tr_b16 v[210:211], v230 offset:18432\n"
"v_mov_b32_dpp v250, v250 row_ror:12 row_mask:0x2 bank_mask:0xf\n"
"v_mfma_f32_16x16x32_bf16 v[160:163], v[96:99], a[128:131], 0\n"
"ds_read_b64_tr_b16 v[212:213], v234 offset:16384\n"
"v_mov_b32_dpp v252, v252 row_ror:12 row_mask:0x2 bank_mask:0xf\n"
"v_mfma_f32_16x16x32_bf16 v[160:163], v[100:103], a[132:135], v[160:163]\n"
"ds_read_b64_tr_b16 v[214:215], v234 offset:18432\n"
"v_mov_b32_dpp v250, v250 row_ror:8 row_mask:0x4 bank_mask:0xf\n"
"v_mfma_f32_16x16x32_bf16 v[160:163], v[104:107], a[136:139], v[160:163]\n"
"ds_read_b64_tr_b16 v[216:217], v230 offset:24576\n"
"v_mov_b32_dpp v252, v252 row_ror:8 row_mask:0x4 bank_mask:0xf\n"
"v_mfma_f32_16x16x32_bf16 v[160:163], v[108:111], a[140:143], v[160:163]\n"
"ds_read_b64_tr_b16 v[218:219], v230 offset:26624\n"
"v_mov_b32_dpp v250, v250 row_ror:4 row_mask:0x8 bank_mask:0xf\n"
"v_mfma_f32_16x16x32_bf16 v[160:163], v[112:115], a[176:179], v[160:163]\n"
"ds_read_b64_tr_b16 v[220:221], v234 offset:24576\n"
"v_mov_b32_dpp v252, v252 row_ror:4 row_mask:0x8 bank_mask:0xf\n"
"v_mfma_f32_16x16x32_bf16 v[160:163], v[116:119], a[180:183], v[160:163]\n"
"ds_read_b64_tr_b16 v[222:223], v234 offset:26624\n"
"v_accvgpr_read_b32 v249, a208\n"
"v_mfma_f32_16x16x32_bf16 v[160:163], v[120:123], a[184:187], v[160:163]\n"
"ds_read_b64_tr_b16 a[238:239], v237 offset:10248\n"
"v_mul_f32_dpp v24, v250, v24 row_newbcast:0 row_mask:0xf bank_mask:0xf\n"
"v_mfma_f32_16x16x32_bf16 v[160:163], v[224:227], a[188:191], v[160:163]\n"
"ds_read_b64_tr_b16 a[232:233], v236 offset:10240\n"
"v_accvgpr_read_b32 v206, a209\n"
"v_mfma_f32_16x16x32_bf16 v[176:179], v[224:227], a[252:255], 0\n"
"ds_read_b64_tr_b16 a[234:235], v236 offset:10248\n"
"v_mul_f32_dpp v25, v250, v25 row_newbcast:1 row_mask:0xf bank_mask:0xf\n"
"v_mfma_f32_16x16x32_bf16 v[176:179], v[120:123], a[248:251], v[176:179]\n"
"ds_read_b64_tr_b16 v[120:121], v231 offset:24576\n"
"v_accvgpr_read_b32 v253, a210\n"
"v_mfma_f32_16x16x32_bf16 v[176:179], v[116:119], a[244:247], v[176:179]\n"
"ds_read_b64_tr_b16 v[122:123], v231 offset:26624\n"
"v_mul_f32_dpp v26, v250, v26 row_newbcast:2 row_mask:0xf bank_mask:0xf\n"
"v_mfma_f32_16x16x32_bf16 v[176:179], v[112:115], a[240:243], v[176:179]\n"
"ds_read_b64_tr_b16 v[112:113], v231 offset:16384\n"
"v_accvgpr_read_b32 v251, a211\n"
"v_mfma_f32_16x16x32_bf16 v[176:179], v[108:111], a[204:207], v[176:179]\n"
"ds_read_b64_tr_b16 v[114:115], v231 offset:18432\n"
"v_mul_f32_dpp v27, v250, v27 row_newbcast:3 row_mask:0xf bank_mask:0xf\n"
"v_mfma_f32_16x16x32_bf16 v[176:179], v[104:107], a[200:203], v[176:179]\n"
"ds_read_b64_tr_b16 v[104:105], v231 offset:8192\n"
"v_fmac_f32_dpp v24, v252, v249 row_newbcast:0 row_mask:0xf bank_mask:0xf\n"
"v_mfma_f32_16x16x32_bf16 v[176:179], v[100:103], a[196:199], v[176:179]\n"
"ds_read_b64_tr_b16 v[106:107], v231 offset:10240\n"
"v_fmac_f32_dpp v25, v252, v206 row_newbcast:1 row_mask:0xf bank_mask:0xf\n"
"v_mfma_f32_16x16x32_bf16 v[176:179], v[96:99], a[192:195], v[176:179]\n"
"s_waitcnt lgkmcnt(7)\n"
"ds_read_b64_tr_b16 v[96:97], v231 offset:0\n"
"v_fmac_f32_dpp v26, v252, v253 row_newbcast:2 row_mask:0xf bank_mask:0xf\n"
"v_mfma_f32_16x16x32_bf16 v[132:135], v[180:183], a[96:99], 0\n"
"s_mov_b32 s47, 0\n"
"ds_read_b64_tr_b16 v[98:99], v231 offset:2048\n"
"v_fmac_f32_dpp v27, v252, v251 row_newbcast:3 row_mask:0xf bank_mask:0xf\n"
"v_mfma_f32_16x16x32_bf16 v[132:135], v[184:187], a[100:103], v[132:135]\n"
"s_mul_i32 s16, s61, 286720\n"
"ds_read_b64_tr_b16 a[220:221], v237 offset:8192\n"
"v_accvgpr_read_b32 v249, a212\n"
"v_mfma_f32_16x16x32_bf16 v[132:135], v[188:191], a[104:107], v[132:135]\n"
"s_add_u32 s16, s16, 229376\n"
"ds_read_b64_tr_b16 a[222:223], v237 offset:8200\n"
"v_mul_f32_dpp v28, v250, v28 row_newbcast:0 row_mask:0xf bank_mask:0xf\n"
"v_mfma_f32_16x16x32_bf16 v[132:135], v[192:195], a[108:111], v[132:135]\n"
"s_add_u32 s17, s16, 4096\n"
"ds_read_b64_tr_b16 a[216:217], v236 offset:8192\n"
"v_accvgpr_read_b32 v206, a213\n"
"v_mfma_f32_16x16x32_bf16 v[132:135], v[208:211], a[144:147], v[132:135]\n"
"s_add_u32 s72, s61, 3\n"
"ds_read_b64_tr_b16 a[218:219], v236 offset:8200\n"
"v_mul_f32_dpp v29, v250, v29 row_newbcast:1 row_mask:0xf bank_mask:0xf\n"
"v_mfma_f32_16x16x32_bf16 v[132:135], v[212:215], a[148:151], v[132:135]\n"
"s_add_u32 s21, s60, -1\n"
"v_accvgpr_read_b32 v253, a214\n"
"v_mul_f32_dpp v30, v250, v30 row_newbcast:2 row_mask:0xf bank_mask:0xf\n"
"v_mfma_f32_16x16x32_bf16 v[132:135], v[216:219], a[152:155], v[132:135]\n"
"s_sub_u32 s72, s72, 1\n"
"v_accvgpr_read_b32 v251, a215\n"
"v_mul_f32_dpp v31, v250, v31 row_newbcast:3 row_mask:0xf bank_mask:0xf\n"
"v_mfma_f32_16x16x32_bf16 v[132:135], v[220:223], a[156:159], v[132:135]\n"
"s_and_b32 s72, s72, 7\n"
"v_fmac_f32_dpp v28, v252, v249 row_newbcast:0 row_mask:0xf bank_mask:0xf\n"
"v_fmac_f32_dpp v29, v252, v206 row_newbcast:1 row_mask:0xf bank_mask:0xf\n"
"v_mfma_f32_16x16x32_bf16 v[148:151], v[220:223], a[172:175], 0\n"
"s_mul_i32 s20, s72, 286720\n"
"v_fmac_f32_dpp v30, v252, v253 row_newbcast:2 row_mask:0xf bank_mask:0xf\n"
"v_fmac_f32_dpp v31, v252, v251 row_newbcast:3 row_mask:0xf bank_mask:0xf\n"
"v_mfma_f32_16x16x32_bf16 v[148:151], v[216:219], a[168:171], v[148:151]\n"
"s_mul_i32 s18, s61, 28672\n"
"v_accvgpr_read_b32 v249, a224\n"
"v_mul_f32_dpp v56, v250, v56 row_newbcast:0 row_mask:0xf bank_mask:0xf\n"
"v_mfma_f32_16x16x32_bf16 v[148:151], v[212:215], a[164:167], v[148:151]\n"
"s_add_u32 s18, s18, s20\n"
"v_accvgpr_read_b32 v206, a225\n"
"v_mul_f32_dpp v57, v250, v57 row_newbcast:1 row_mask:0xf bank_mask:0xf\n"
"v_mfma_f32_16x16x32_bf16 v[148:151], v[208:211], a[160:163], v[148:151]\n"
"s_cmp_ge_u32 s47, 3\ns_cselect_b32 s18, s18, s77\n"
"v_accvgpr_read_b32 v253, a226\n"
"v_mul_f32_dpp v58, v250, v58 row_newbcast:2 row_mask:0xf bank_mask:0xf\n"
"v_mfma_f32_16x16x32_bf16 v[148:151], v[192:195], a[124:127], v[148:151]\n"
"s_add_u32 s19, s18, 4096\n"
"v_accvgpr_read_b32 v251, a227\n"
"v_mul_f32_dpp v59, v250, v59 row_newbcast:3 row_mask:0xf bank_mask:0xf\n"
"v_mfma_f32_16x16x32_bf16 v[148:151], v[188:191], a[120:123], v[148:151]\n"
"s_cmp_ge_u32 s47, 2\ns_cselect_b32 s20, s20, s77\ns_cselect_b32 s69, 15, -1\n"
"v_cvt_pk_bf16_f32 v24, v24, v25\n"
"v_cvt_pk_bf16_f32 v25, v26, v27\n"
"v_mfma_f32_16x16x32_bf16 v[148:151], v[184:187], a[116:119], v[148:151]\n"
"s_add_u32 s21, s21, 1\n"
"v_cvt_pk_bf16_f32 v26, v28, v29\n"
"v_cvt_pk_bf16_f32 v27, v30, v31\n"
"v_mfma_f32_16x16x32_bf16 v[148:151], v[180:183], a[112:115], v[148:151]\n"
"v_bfe_u32 v207, v242, 8, 2\n"
"v_lshlrev_b32 v207, 6, v207\n"
"v_xor_b32 v207, v207, v248\n"
"v_fmac_f32_dpp v56, v252, v249 row_newbcast:0 row_mask:0xf bank_mask:0xf\n"
"v_fmac_f32_dpp v57, v252, v206 row_newbcast:1 row_mask:0xf bank_mask:0xf\n"
"v_fmac_f32_dpp v58, v252, v253 row_newbcast:2 row_mask:0xf bank_mask:0xf\n"
"v_fmac_f32_dpp v59, v252, v251 row_newbcast:3 row_mask:0xf bank_mask:0xf\n"
"v_accvgpr_read_b32 v249, a228\n"
"v_mul_f32_dpp v60, v250, v60 row_newbcast:0 row_mask:0xf bank_mask:0xf\n"
"v_accvgpr_read_b32 v206, a229\n"
"v_mul_f32_dpp v61, v250, v61 row_newbcast:1 row_mask:0xf bank_mask:0xf\n"
"v_accvgpr_read_b32 v253, a230\n"
"v_mul_f32_dpp v62, v250, v62 row_newbcast:2 row_mask:0xf bank_mask:0xf\n"
"v_accvgpr_read_b32 v251, a231\n"
"v_mul_f32_dpp v63, v250, v63 row_newbcast:3 row_mask:0xf bank_mask:0xf\n"
"v_mfma_f32_16x16x32_bf16 v[164:167], v[180:183], a[128:131], 0\n"
"ds_write_b128 v207, v[24:27] offset:0\n"
"v_fmac_f32_dpp v60, v252, v249 row_newbcast:0 row_mask:0xf bank_mask:0xf\n"
"v_fmac_f32_dpp v61, v252, v206 row_newbcast:1 row_mask:0xf bank_mask:0xf\n"
"v_fmac_f32_dpp v62, v252, v253 row_newbcast:2 row_mask:0xf bank_mask:0xf\n"
"v_fmac_f32_dpp v63, v252, v251 row_newbcast:3 row_mask:0xf bank_mask:0xf\n"
"v_cvt_pk_bf16_f32 v56, v56, v57\n"
"v_cvt_pk_bf16_f32 v57, v58, v59\n"
"v_cvt_pk_bf16_f32 v58, v60, v61\n"
"v_cvt_pk_bf16_f32 v59, v62, v63\n"
"v_mfma_f32_16x16x32_bf16 v[164:167], v[184:187], a[132:135], v[164:167]\n"
"ds_write_b128 v207, v[56:59] offset:2048\n"
"v_xor_b32 v235, 0x1000, v231\n"
"v_mfma_f32_16x16x32_bf16 v[164:167], v[188:191], a[136:139], v[164:167]\n"
"ds_read_b64_tr_b16 v[100:101], v235 offset:0\n"
"ds_read_b64_tr_b16 v[102:103], v235 offset:2048\n"
"v_mfma_f32_16x16x32_bf16 v[164:167], v[192:195], a[140:143], v[164:167]\n"
"ds_read_b64_tr_b16 v[108:109], v235 offset:8192\n"
"v_xor_b32 v246, s75, v246\n"
"v_mfma_f32_16x16x32_bf16 v[164:167], v[208:211], a[176:179], v[164:167]\n"
"ds_read_b64_tr_b16 v[110:111], v235 offset:10240\n"
"v_mul_f32_dpp v16, v243, v16 row_newbcast:0 row_mask:0xf bank_mask:0xf\n"
"v_mfma_f32_16x16x32_bf16 v[164:167], v[212:215], a[180:183], v[164:167]\n"
"ds_read_b64_tr_b16 v[116:117], v235 offset:16384\n"
"v_mul_f32_dpp v17, v243, v17 row_newbcast:1 row_mask:0xf bank_mask:0xf\n"
"v_mfma_f32_16x16x32_bf16 v[164:167], v[216:219], a[184:187], v[164:167]\n"
"ds_read_b64_tr_b16 v[118:119], v235 offset:18432\n"
"v_mul_f32_dpp v18, v243, v18 row_newbcast:2 row_mask:0xf bank_mask:0xf\n"
"v_mfma_f32_16x16x32_bf16 v[164:167], v[220:223], a[188:191], v[164:167]\n"
"s_waitcnt lgkmcnt(8)\n"
"ds_read_b64_tr_b16 v[224:225], v235 offset:24576\n"
"v_mul_f32_dpp v19, v243, v19 row_newbcast:3 row_mask:0xf bank_mask:0xf\n"
"v_mfma_f32_16x16x32_bf16 v[136:139], v[96:99], a[96:99], 0\n"
"s_waitcnt lgkmcnt(5)\n"
"s_waitcnt vmcnt(0) lgkmcnt(7)\n"
"ds_read_b64_tr_b16 v[226:227], v235 offset:26624\n"
"v_mul_f32_dpp v20, v243, v20 row_newbcast:0 row_mask:0xf bank_mask:0xf\n"
"v_mfma_f32_16x16x32_bf16 v[136:139], v[100:103], a[100:103], v[136:139]\n"
"s_barrier\n"
"v_mfma_f32_16x16x32_bf16 v[136:139], v[104:107], a[104:107], v[136:139]\n"
"s_waitcnt lgkmcnt(4)\n"
"ds_read_b64_tr_b16 v[180:181], v228 offset:32768\n"
"v_mul_f32_dpp v21, v243, v21 row_newbcast:1 row_mask:0xf bank_mask:0xf\n"
"v_mfma_f32_16x16x32_bf16 v[136:139], v[108:111], a[108:111], v[136:139]\n"
"ds_read_b64_tr_b16 v[182:183], v228 offset:34816\n"
"v_mul_f32_dpp v22, v243, v22 row_newbcast:2 row_mask:0xf bank_mask:0xf\n"
"v_mfma_f32_16x16x32_bf16 v[136:139], v[112:115], a[144:147], v[136:139]\n"
"s_waitcnt lgkmcnt(4)\n"
"ds_read_b64_tr_b16 v[184:185], v232 offset:32768\n"
"v_mul_f32_dpp v23, v243, v23 row_newbcast:3 row_mask:0xf bank_mask:0xf\n"
"v_mfma_f32_16x16x32_bf16 v[136:139], v[116:119], a[148:151], v[136:139]\n"
"ds_read_b64_tr_b16 v[186:187], v232 offset:34816\n"
"v_cvt_pk_bf16_f32 v16, v16, v17\n"
"v_mfma_f32_16x16x32_bf16 v[136:139], v[120:123], a[152:155], v[136:139]\n"
"s_waitcnt lgkmcnt(4)\n"
"ds_read_b64_tr_b16 v[188:189], v228 offset:40960\n"
"v_cvt_pk_bf16_f32 v17, v18, v19\n"
"v_mfma_f32_16x16x32_bf16 v[136:139], v[224:227], a[156:159], v[136:139]\n"
"ds_read_b64_tr_b16 v[190:191], v228 offset:43008\n"
"v_cvt_pk_bf16_f32 v18, v20, v21\n"
"v_cvt_pk_bf16_f32 v19, v22, v23\n"
"v_mfma_f32_16x16x32_bf16 v[152:155], v[224:227], a[172:175], 0\n"
"ds_write_b128 v246, v[16:19] offset:49152\n"
"v_mul_f32_dpp v48, v244, v48 row_newbcast:0 row_mask:0xf bank_mask:0xf\n"
"v_mul_f32_dpp v49, v244, v49 row_newbcast:1 row_mask:0xf bank_mask:0xf\n"
"v_mul_f32_dpp v50, v244, v50 row_newbcast:2 row_mask:0xf bank_mask:0xf\n"
"v_mul_f32_dpp v51, v244, v51 row_newbcast:3 row_mask:0xf bank_mask:0xf\n"
"v_mul_f32_dpp v52, v244, v52 row_newbcast:0 row_mask:0xf bank_mask:0xf\n"
"v_mul_f32_dpp v53, v244, v53 row_newbcast:1 row_mask:0xf bank_mask:0xf\n"
"v_mul_f32_dpp v54, v244, v54 row_newbcast:2 row_mask:0xf bank_mask:0xf\n"
"v_mul_f32_dpp v55, v244, v55 row_newbcast:3 row_mask:0xf bank_mask:0xf\n"
"v_cvt_pk_bf16_f32 v48, v48, v49\n"
"v_cvt_pk_bf16_f32 v49, v50, v51\n"
"v_cvt_pk_bf16_f32 v50, v52, v53\n"
"v_cvt_pk_bf16_f32 v51, v54, v55\n"
"v_mfma_f32_16x16x32_bf16 v[152:155], v[120:123], a[168:171], v[152:155]\n"
"ds_write_b128 v246, v[48:51] offset:50176\n"
"v_mul_f32_dpp v80, v245, v80 row_newbcast:0 row_mask:0xf bank_mask:0xf\n"
"v_mul_f32_dpp v81, v245, v81 row_newbcast:1 row_mask:0xf bank_mask:0xf\n"
"v_mul_f32_dpp v82, v245, v82 row_newbcast:2 row_mask:0xf bank_mask:0xf\n"
"v_mul_f32_dpp v83, v245, v83 row_newbcast:3 row_mask:0xf bank_mask:0xf\n"
"v_mul_f32_dpp v84, v245, v84 row_newbcast:0 row_mask:0xf bank_mask:0xf\n"
"v_mul_f32_dpp v85, v245, v85 row_newbcast:1 row_mask:0xf bank_mask:0xf\n"
"v_mul_f32_dpp v86, v245, v86 row_newbcast:2 row_mask:0xf bank_mask:0xf\n"
"v_mul_f32_dpp v87, v245, v87 row_newbcast:3 row_mask:0xf bank_mask:0xf\n"
"v_cvt_pk_bf16_f32 v80, v80, v81\n"
"v_cvt_pk_bf16_f32 v81, v82, v83\n"
"v_cvt_pk_bf16_f32 v82, v84, v85\n"
"v_cvt_pk_bf16_f32 v83, v86, v87\n"
"v_mfma_f32_16x16x32_bf16 v[152:155], v[116:119], a[164:167], v[152:155]\n"
"ds_write_b128 v246, v[80:83] offset:51200\n"
"v_xor_b32 v247, s75, v247\n"
"v_mfma_f32_16x16x32_bf16 v[152:155], v[112:115], a[160:163], v[152:155]\n"
"ds_read_b64_tr_b16 a[8:9], v247 offset:49152\n"
"ds_read_b64_tr_b16 a[10:11], v247 offset:49160\n"
"v_mfma_f32_16x16x32_bf16 v[152:155], v[108:111], a[124:127], v[152:155]\n"
"ds_read_b64_tr_b16 a[24:25], v247 offset:50176\n"
"ds_read_b64_tr_b16 a[26:27], v247 offset:50184\n"
"v_mfma_f32_16x16x32_bf16 v[152:155], v[104:107], a[120:123], v[152:155]\n"
"ds_read_b64_tr_b16 a[40:41], v247 offset:51200\n"
"ds_read_b64_tr_b16 a[42:43], v247 offset:51208\n"
"v_mfma_f32_16x16x32_bf16 v[152:155], v[100:103], a[116:119], v[152:155]\n"
"ds_read_b64_tr_b16 v[192:193], v232 offset:40960\n"
"v_and_b32 v243, 0x3f0, v242\n"
"v_mfma_f32_16x16x32_bf16 v[152:155], v[96:99], a[112:115], v[152:155]\n"
"ds_read_b64_tr_b16 v[194:195], v232 offset:43008\n"
"v_mov_b64 v[196:197], 0\n"
"v_mfma_f32_16x16x32_bf16 v[168:171], v[96:99], a[128:131], 0\n"
"ds_read_b64_tr_b16 v[208:209], v228 offset:49152\n"
"v_mov_b64 v[198:199], 0\n"
"v_mfma_f32_16x16x32_bf16 v[168:171], v[100:103], a[132:135], v[168:171]\n"
"ds_read_b64_tr_b16 v[210:211], v228 offset:51200\n"
"v_mov_b64 v[200:201], 0\n"
"v_mfma_f32_16x16x32_bf16 v[168:171], v[104:107], a[136:139], v[168:171]\n"
"ds_read_b64_tr_b16 v[212:213], v232 offset:49152\n"
"v_mov_b64 v[202:203], 0\n"
"v_mfma_f32_16x16x32_bf16 v[168:171], v[108:111], a[140:143], v[168:171]\n"
"s_waitcnt lgkmcnt(5)\n"
"ds_read_b64_tr_b16 v[214:215], v232 offset:51200\n"
"v_xor_b32 v246, s75, v246\n"
"v_mfma_f32_16x16x32_bf16 v[168:171], v[112:115], a[176:179], v[168:171]\n"
"ds_read_b64_tr_b16 v[216:217], v228 offset:57344\n"
"v_xor_b32 v247, s75, v247\n"
"v_mfma_f32_16x16x32_bf16 v[168:171], v[116:119], a[180:183], v[168:171]\n"
"ds_read_b64_tr_b16 v[218:219], v228 offset:59392\n"
"v_xor_b32 v228, s75, v228\n"
"v_mfma_f32_16x16x32_bf16 v[168:171], v[120:123], a[184:187], v[168:171]\n"
"s_add_u32 s41, s70, 327680\n"
"s_add_u32 m0, s71, 32768\n"
"ds_read_b64_tr_b16 v[220:221], v232 offset:57344\n"
"v_mfma_f32_16x16x32_bf16 v[168:171], v[224:227], a[188:191], v[168:171]\n"
"buffer_load_dwordx4 v240, s[28:31], s41 offen lds\n"
"v_mfma_f32_16x16x32_bf16 v[124:127], v[180:183], a[48:51], v[124:127]\n"
"ds_read_b64_tr_b16 v[222:223], v232 offset:59392\n"
"v_xor_b32 v232, s75, v232\n"
"v_mfma_f32_16x16x32_bf16 v[124:127], v[184:187], a[52:55], v[124:127]\n"
"ds_read_b64_tr_b16 a[228:229], v237 offset:14336\n"
"v_mov_b32 v241, s60\n"
"v_mfma_f32_16x16x32_bf16 v[124:127], v[188:191], a[56:59], v[124:127]\n"
"s_waitcnt lgkmcnt(6)\n"
"s_add_u32 s40, s41, s52\n"
"ds_read_b64_tr_b16 a[230:231], v237 offset:14344\n"
"v_bfe_u32 v254, v242, 4, 4\n"
"v_mfma_f32_16x16x32_bf16 v[124:127], v[192:195], a[60:63], v[124:127]\n"
"buffer_load_dwordx4 v240, s[28:31], s40 offen offset:1024 lds\n"
"v_mfma_f32_16x16x32_bf16 v[124:127], v[208:211], a[0:3], v[124:127]\n"
"s_waitcnt lgkmcnt(5)\n"
"ds_read_b64_tr_b16 a[224:225], v236 offset:14336\n"
"v_mul_u32_u24 v254, 2560, v254\n"
"v_mfma_f32_16x16x32_bf16 v[124:127], v[212:215], a[4:7], v[124:127]\n"
"s_waitcnt lgkmcnt(5)\n"
"ds_read_b64_tr_b16 a[226:227], v236 offset:14344\n"
"v_bfe_u32 v255, v242, 8, 1\n"
"v_mfma_f32_16x16x32_bf16 v[124:127], v[216:219], a[8:11], v[124:127]\n"
"s_waitcnt lgkmcnt(4)\n"
"s_add_u32 s40, s41, s53\n"
"ds_read_b64_tr_b16 a[212:213], v237 offset:12288\n"
"v_lshl_add_u32 v254, v255, 5, v254\n"
"v_mfma_f32_16x16x32_bf16 v[124:127], v[220:223], a[12:15], v[124:127]\n"
"buffer_load_dwordx4 v240, s[28:31], s40 offen offset:2048 lds\n"
"v_mfma_f32_16x16x32_bf16 v[140:143], v[220:223], a[28:31], v[140:143]\n"
"ds_read_b64_tr_b16 a[214:215], v237 offset:12296\n"
"v_add_u32 v237, s95, v243\n"
"v_mfma_f32_16x16x32_bf16 v[140:143], v[216:219], a[24:27], v[140:143]\n"
"ds_read_b64_tr_b16 a[208:209], v236 offset:12288\n"
"v_bfe_u32 v255, v242, 9, 1\n"
"v_mfma_f32_16x16x32_bf16 v[140:143], v[212:215], a[20:23], v[140:143]\n"
"ds_read_b64_tr_b16 a[210:211], v236 offset:12296\n"
"v_add_u32 v236, s94, v243\n"
"v_mfma_f32_16x16x32_bf16 v[140:143], v[208:211], a[16:19], v[140:143]\n"
"s_add_u32 s40, s41, s54\n"
"ds_read_b64_tr_b16 v[96:97], v229 offset:32768\n"
"v_add_u32 v243, s96, v243\n"
"v_mfma_f32_16x16x32_bf16 v[140:143], v[192:195], a[76:79], v[140:143]\n"
"buffer_load_dwordx4 v240, s[28:31], s40 offen offset:3072 lds\n"
"v_mfma_f32_16x16x32_bf16 v[140:143], v[188:191], a[72:75], v[140:143]\n"
"ds_read_b64_tr_b16 v[98:99], v229 offset:34816\n"
"v_cvt_pk_bf16_f32 v124, v124, v125\n"
"v_mfma_f32_16x16x32_bf16 v[140:143], v[184:187], a[68:71], v[140:143]\n"
"s_add_u32 s41, s70, 491520\n"
"ds_read_b64_tr_b16 v[100:101], v233 offset:32768\n"
"v_cvt_pk_bf16_f32 v125, v126, v127\n"
"v_mfma_f32_16x16x32_bf16 v[140:143], v[180:183], a[64:67], v[140:143]\n"
"s_add_u32 m0, s71, 49152\n"
"ds_read_b64_tr_b16 v[102:103], v233 offset:34816\n"
"v_lshl_add_u32 v254, v255, 4, v254\n"
"v_mfma_f32_16x16x32_bf16 v[156:159], v[180:183], a[80:83], v[156:159]\n"
"buffer_load_dwordx4 v240, s[28:31], s41 offen lds\n"
"v_mfma_f32_16x16x32_bf16 v[156:159], v[184:187], a[84:87], v[156:159]\n"
"ds_read_b64_tr_b16 v[104:105], v229 offset:40960\n"
"v_add_u32 v238, s97, v254\n"
"v_mfma_f32_16x16x32_bf16 v[156:159], v[188:191], a[88:91], v[156:159]\n"
"ds_read_b64_tr_b16 v[106:107], v229 offset:43008\n"
"v_xor_b32 v239, 64, v238\n"
"v_mfma_f32_16x16x32_bf16 v[156:159], v[192:195], a[92:95], v[156:159]\n"
"s_add_u32 s40, s41, s52\n"
"ds_read_b64_tr_b16 v[108:109], v233 offset:40960\n"
"v_add_u32 v252, s98, v254\n"
"v_mfma_f32_16x16x32_bf16 v[156:159], v[208:211], a[32:35], v[156:159]\n"
"buffer_load_dwordx4 v240, s[28:31], s40 offen offset:1024 lds\n"
"v_mfma_f32_16x16x32_bf16 v[156:159], v[212:215], a[36:39], v[156:159]\n"
"ds_read_b64_tr_b16 v[110:111], v233 offset:43008\n"
"v_cvt_pk_bf16_f32 v140, v140, v141\n"
"v_mfma_f32_16x16x32_bf16 v[156:159], v[216:219], a[40:43], v[156:159]\n"
"ds_read_b64_tr_b16 v[112:113], v229 offset:49152\n"
"v_cvt_pk_bf16_f32 v141, v142, v143\n"
"v_mfma_f32_16x16x32_bf16 v[156:159], v[220:223], a[44:47], v[156:159]\n"
"s_waitcnt lgkmcnt(5)\n"
"ds_read_b64_tr_b16 v[114:115], v229 offset:51200\n"
"v_add_u32 v253, s99, v254\n"
"v_mfma_f32_16x16x32_bf16 v[172:175], v[220:223], a[228:231], v[172:175]\n"
"s_add_u32 s40, s41, s53\n"
"ds_read_b64_tr_b16 v[116:117], v233 offset:49152\n"
"v_add_u32 v254, s100, v254\n"
"v_mfma_f32_16x16x32_bf16 v[172:175], v[216:219], a[224:227], v[172:175]\n"
"buffer_load_dwordx4 v240, s[28:31], s40 offen offset:2048 lds\n"
"v_mfma_f32_16x16x32_bf16 v[172:175], v[212:215], a[212:215], v[172:175]\n"
"ds_read_b64_tr_b16 v[118:119], v233 offset:51200\n"
"v_mov_b32 v0, 0\n"
"v_mfma_f32_16x16x32_bf16 v[172:175], v[208:211], a[208:211], v[172:175]\n"
"ds_read_b64_tr_b16 v[120:121], v229 offset:57344\n"
"v_mov_b32 v1, 0\n"
"v_mfma_f32_16x16x32_bf16 v[172:175], v[192:195], a[236:239], v[172:175]\n"
"s_add_u32 s40, s41, s54\n"
"ds_read_b64_tr_b16 v[122:123], v229 offset:59392\n"
"v_xor_b32 v229, s75, v229\n"
"v_mfma_f32_16x16x32_bf16 v[172:175], v[188:191], a[232:235], v[172:175]\n"
"buffer_load_dwordx4 v240, s[28:31], s40 offen offset:3072 lds\n"
"v_mfma_f32_16x16x32_bf16 v[172:175], v[184:187], a[220:223], v[172:175]\n"
"s_mov_b32 s62, 258048\n"
"ds_read_b64_tr_b16 v[224:225], v233 offset:57344\n"
"v_cvt_pk_bf16_f32 v156, v156, v157\n"
"v_mfma_f32_16x16x32_bf16 v[172:175], v[180:183], a[216:219], v[172:175]\n"
"s_add_u32 s70, s61, s47\n"
"ds_read_b64_tr_b16 v[226:227], v233 offset:59392\n"
"v_cvt_pk_bf16_f32 v157, v158, v159\n"
"v_mfma_f32_16x16x32_bf16 v[128:131], v[96:99], a[48:51], v[128:131]\n"
"s_add_u32 s70, s70, 1\n"
"ds_read_b64_tr_b16 v[180:181], v230 offset:32768\n"
"v_xor_b32 v233, s75, v233\n"
"v_mfma_f32_16x16x32_bf16 v[128:131], v[100:103], a[52:55], v[128:131]\n"
"s_waitcnt lgkmcnt(6)\n"
"s_and_b32 s70, s70, 7\n"
"ds_read_b64_tr_b16 v[182:183], v230 offset:34816\n"
"v_mov_b32 v2, 0\n"
"v_mfma_f32_16x16x32_bf16 v[128:131], v[104:107], a[56:59], v[128:131]\n"
"s_lshl_b32 s70, s70, 7\n"
"ds_read_b64_tr_b16 v[184:185], v234 offset:32768\n"
"v_mov_b32 v3, 0\n"
"v_mfma_f32_16x16x32_bf16 v[128:131], v[108:111], a[60:63], v[128:131]\n"
"s_xor_b32 s71, s71, s75\n"
"ds_read_b64_tr_b16 v[186:187], v234 offset:34816\n"
"v_mov_b32 v4, 0\n"
"v_mfma_f32_16x16x32_bf16 v[128:131], v[112:115], a[0:3], v[128:131]\n"
"s_waitcnt lgkmcnt(6)\n"
"v_lshlrev_b32_e64 v245, 2, s72\n"
"ds_read_b64_tr_b16 v[188:189], v230 offset:40960\n"
"v_mov_b32 v5, 0\n"
"v_mfma_f32_16x16x32_bf16 v[128:131], v[116:119], a[4:7], v[128:131]\n"
"global_load_dword v244, v245, s[6:7] sc1\n"
"v_mfma_f32_16x16x32_bf16 v[128:131], v[120:123], a[8:11], v[128:131]\n"
"s_waitcnt lgkmcnt(5)\n"
"ds_read_b64_tr_b16 v[190:191], v230 offset:43008\n"
"v_mov_b32 v6, 0\n"
"v_mfma_f32_16x16x32_bf16 v[128:131], v[224:227], a[12:15], v[128:131]\n"
"ds_read_b64_tr_b16 v[192:193], v234 offset:40960\n"
"v_mov_b32 v7, 0\n"
"v_mfma_f32_16x16x32_bf16 v[144:147], v[224:227], a[28:31], v[144:147]\n"
"ds_read_b64_tr_b16 v[194:195], v234 offset:43008\n"
"v_mov_b32 v8, 0\n"
"v_mfma_f32_16x16x32_bf16 v[144:147], v[120:123], a[24:27], v[144:147]\n"
"ds_read_b64_tr_b16 v[208:209], v230 offset:49152\n"
"v_mov_b32 v9, 0\n"
"v_mfma_f32_16x16x32_bf16 v[144:147], v[116:119], a[20:23], v[144:147]\n"
"ds_read_b64_tr_b16 v[210:211], v230 offset:51200\n"
"v_mov_b32 v10, 0\n"
"v_mfma_f32_16x16x32_bf16 v[144:147], v[112:115], a[16:19], v[144:147]\n"
"ds_read_b64_tr_b16 v[212:213], v234 offset:49152\n"
"v_mov_b32 v11, 0\n"
"v_mfma_f32_16x16x32_bf16 v[144:147], v[108:111], a[76:79], v[144:147]\n"
"ds_read_b64_tr_b16 v[214:215], v234 offset:51200\n"
"v_mov_b32 v12, 0\n"
"v_mfma_f32_16x16x32_bf16 v[144:147], v[104:107], a[72:75], v[144:147]\n"
"ds_read_b64_tr_b16 v[216:217], v230 offset:57344\n"
"v_cvt_pk_bf16_f32 v126, v128, v129\n"
"v_mfma_f32_16x16x32_bf16 v[144:147], v[100:103], a[68:71], v[144:147]\n"
"ds_read_b64_tr_b16 v[218:219], v230 offset:59392\n"
"v_cvt_pk_bf16_f32 v127, v130, v131\n"
"v_mfma_f32_16x16x32_bf16 v[144:147], v[96:99], a[64:67], v[144:147]\n"
"ds_read_b64_tr_b16 v[220:221], v234 offset:57344\n"
"v_mov_b32 v13, 0\n"
"v_mfma_f32_16x16x32_bf16 v[160:163], v[96:99], a[80:83], v[160:163]\n"
"ds_read_b64_tr_b16 v[222:223], v234 offset:59392\n"
"v_mov_b32 v14, 0\n"
"v_mfma_f32_16x16x32_bf16 v[160:163], v[100:103], a[84:87], v[160:163]\n"
"buffer_store_dwordx4 v[124:127], v243, s[32:35], s16 offen sc1\n"
"v_mov_b32 v15, 0\n"
"v_mfma_f32_16x16x32_bf16 v[160:163], v[104:107], a[88:91], v[160:163]\n"
"v_mov_b32 v16, 0\n"
"v_mov_b32 v17, 0\n"
"v_mfma_f32_16x16x32_bf16 v[160:163], v[108:111], a[92:95], v[160:163]\n"
"v_mov_b32 v18, 0\n"
"v_mov_b32 v19, 0\n"
"v_mfma_f32_16x16x32_bf16 v[160:163], v[112:115], a[32:35], v[160:163]\n"
"v_mov_b32 v20, 0\n"
"v_mov_b32 v21, 0\n"
"v_mfma_f32_16x16x32_bf16 v[160:163], v[116:119], a[36:39], v[160:163]\n"
"v_cvt_pk_bf16_f32 v142, v144, v145\n"
"v_cvt_pk_bf16_f32 v143, v146, v147\n"
"v_mfma_f32_16x16x32_bf16 v[160:163], v[120:123], a[40:43], v[160:163]\n"
"v_mov_b32 v22, 0\n"
"v_mov_b32 v23, 0\n"
"v_mfma_f32_16x16x32_bf16 v[160:163], v[224:227], a[44:47], v[160:163]\n"
"v_mov_b32 v24, 0\n"
"v_mov_b32 v25, 0\n"
"v_mfma_f32_16x16x32_bf16 v[176:179], v[224:227], a[228:231], v[176:179]\n"
"ds_read_b64_tr_b16 v[224:225], v235 offset:57344\n"
"v_mov_b32 v26, 0\n"
"v_mfma_f32_16x16x32_bf16 v[176:179], v[120:123], a[224:227], v[176:179]\n"
"ds_read_b64_tr_b16 v[120:121], v231 offset:57344\n"
"v_mov_b32 v27, 0\n"
"v_mfma_f32_16x16x32_bf16 v[176:179], v[116:119], a[212:215], v[176:179]\n"
"ds_read_b64_tr_b16 v[116:117], v235 offset:49152\n"
"v_mov_b32 v28, 0\n"
"v_mfma_f32_16x16x32_bf16 v[176:179], v[112:115], a[208:211], v[176:179]\n"
"ds_read_b64_tr_b16 v[112:113], v231 offset:49152\n"
"v_mov_b32 v29, 0\n"
"v_mfma_f32_16x16x32_bf16 v[176:179], v[108:111], a[236:239], v[176:179]\n"
"ds_read_b64_tr_b16 v[108:109], v235 offset:40960\n"
"v_mov_b32 v30, 0\n"
"v_mfma_f32_16x16x32_bf16 v[176:179], v[104:107], a[232:235], v[176:179]\n"
"ds_read_b64_tr_b16 v[104:105], v231 offset:40960\n"
"v_cvt_pk_bf16_f32 v158, v160, v161\n"
"v_mfma_f32_16x16x32_bf16 v[176:179], v[100:103], a[220:223], v[176:179]\n"
"ds_read_b64_tr_b16 v[100:101], v235 offset:32768\n"
"v_cvt_pk_bf16_f32 v159, v162, v163\n"
"v_mfma_f32_16x16x32_bf16 v[176:179], v[96:99], a[216:219], v[176:179]\n"
"s_waitcnt lgkmcnt(7)\n"
"ds_read_b64_tr_b16 v[96:97], v231 offset:32768\n"
"v_mov_b32 v31, 0\n"
"v_mfma_f32_16x16x32_bf16 v[132:135], v[180:183], a[48:51], v[132:135]\n"
"ds_read_b64_tr_b16 v[98:99], v231 offset:34816\n"
"v_mov_b32 v32, 0\n"
"v_mfma_f32_16x16x32_bf16 v[132:135], v[184:187], a[52:55], v[132:135]\n"
"ds_read_b64_tr_b16 v[102:103], v235 offset:34816\n"
"v_mov_b32 v33, 0\n"
"v_mfma_f32_16x16x32_bf16 v[132:135], v[188:191], a[56:59], v[132:135]\n"
"ds_read_b64_tr_b16 v[106:107], v231 offset:43008\n"
"v_mov_b32 v34, 0\n"
"v_mfma_f32_16x16x32_bf16 v[132:135], v[192:195], a[60:63], v[132:135]\n"
"ds_read_b64_tr_b16 v[110:111], v235 offset:43008\n"
"v_mov_b32 v35, 0\n"
"v_mfma_f32_16x16x32_bf16 v[132:135], v[208:211], a[0:3], v[132:135]\n"
"ds_read_b64_tr_b16 v[114:115], v231 offset:51200\n"
"v_mov_b32 v36, 0\n"
"v_mfma_f32_16x16x32_bf16 v[132:135], v[212:215], a[4:7], v[132:135]\n"
"ds_read_b64_tr_b16 v[118:119], v235 offset:51200\n"
"v_mov_b32 v37, 0\n"
"v_mfma_f32_16x16x32_bf16 v[132:135], v[216:219], a[8:11], v[132:135]\n"
"ds_read_b64_tr_b16 v[122:123], v231 offset:59392\n"
"v_mov_b32 v38, 0\n"
"v_mfma_f32_16x16x32_bf16 v[132:135], v[220:223], a[12:15], v[132:135]\n"
"buffer_store_dwordx4 v[140:143], v243, s[32:35], s16 offen offset:2048 sc1\n"
"v_mov_b32 v39, 0\n"
"v_mfma_f32_16x16x32_bf16 v[148:151], v[220:223], a[28:31], v[148:151]\n"
"ds_read_b64_tr_b16 v[226:227], v235 offset:59392\n"
"v_mov_b32 v40, 0\n"
"v_mfma_f32_16x16x32_bf16 v[148:151], v[216:219], a[24:27], v[148:151]\n"
"v_mov_b32 v41, 0\n"
"v_mov_b32 v42, 0\n"
"v_mfma_f32_16x16x32_bf16 v[148:151], v[212:215], a[20:23], v[148:151]\n"
"v_mov_b32 v43, 0\n"
"v_mov_b32 v44, 0\n"
"v_mfma_f32_16x16x32_bf16 v[148:151], v[208:211], a[16:19], v[148:151]\n"
"v_mov_b32 v45, 0\n"
"v_mov_b32 v46, 0\n"
"v_mfma_f32_16x16x32_bf16 v[148:151], v[192:195], a[76:79], v[148:151]\n"
"v_mov_b32 v47, 0\n"
"v_mov_b32 v48, 0\n"
"v_mfma_f32_16x16x32_bf16 v[148:151], v[188:191], a[72:75], v[148:151]\n"
"v_mov_b32 v49, 0\n"
"v_mov_b32 v50, 0\n"
"v_mfma_f32_16x16x32_bf16 v[148:151], v[184:187], a[68:71], v[148:151]\n"
"v_mov_b32 v51, 0\n"
"v_mov_b32 v52, 0\n"
"v_mfma_f32_16x16x32_bf16 v[148:151], v[180:183], a[64:67], v[148:151]\n"
"v_mov_b32 v53, 0\n"
"v_mov_b32 v54, 0\n"
"v_mfma_f32_16x16x32_bf16 v[164:167], v[180:183], a[80:83], v[164:167]\n"
"v_mov_b32 v55, 0\n"
"v_mov_b32 v56, 0\n"
"v_mfma_f32_16x16x32_bf16 v[164:167], v[184:187], a[84:87], v[164:167]\n"
"v_mov_b32 v57, 0\n"
"v_mov_b32 v58, 0\n"
"v_mfma_f32_16x16x32_bf16 v[164:167], v[188:191], a[88:91], v[164:167]\n"
"v_mov_b32 v59, 0\n"
"v_mov_b32 v60, 0\n"
"v_mfma_f32_16x16x32_bf16 v[164:167], v[192:195], a[92:95], v[164:167]\n"
"v_mov_b32 v61, 0\n"
"v_mov_b32 v62, 0\n"
"v_mfma_f32_16x16x32_bf16 v[164:167], v[208:211], a[32:35], v[164:167]\n"
"v_mov_b32 v63, 0\n"
"v_mov_b32 v64, 0\n"
"v_mfma_f32_16x16x32_bf16 v[164:167], v[212:215], a[36:39], v[164:167]\n"
"v_mov_b32 v65, 0\n"
"v_mov_b32 v66, 0\n"
"v_mfma_f32_16x16x32_bf16 v[164:167], v[216:219], a[40:43], v[164:167]\n"
"v_mov_b32 v67, 0\n"
"v_mov_b32 v68, 0\n"
"v_mfma_f32_16x16x32_bf16 v[164:167], v[220:223], a[44:47], v[164:167]\n"
"s_waitcnt lgkmcnt(0)\n"
"v_mov_b32 v69, 0\n"
"v_mov_b32 v70, 0\n"
"v_mfma_f32_16x16x32_bf16 v[136:139], v[96:99], a[48:51], v[136:139]\n"
"v_mov_b32 v71, 0\n"
"v_mov_b32 v92, 0\n"
"v_mfma_f32_16x16x32_bf16 v[136:139], v[100:103], a[52:55], v[136:139]\n"
"s_barrier\n"
"v_mfma_f32_16x16x32_bf16 v[136:139], v[104:107], a[56:59], v[136:139]\n"
"ds_read_b64_tr_b16 v[180:181], v228 offset:0\n"
"v_mov_b32 v93, 0\n"
"v_mfma_f32_16x16x32_bf16 v[136:139], v[108:111], a[60:63], v[136:139]\n"
"ds_read_b64_tr_b16 v[182:183], v228 offset:2048\n"
"v_mov_b32 v94, 0\n"
"v_mfma_f32_16x16x32_bf16 v[136:139], v[112:115], a[0:3], v[136:139]\n"
"ds_read_b64_tr_b16 v[184:185], v232 offset:0\n"
"v_mov_b32 v95, 0\n"
"v_mfma_f32_16x16x32_bf16 v[136:139], v[116:119], a[4:7], v[136:139]\n"
"buffer_store_dwordx4 v[156:159], v243, s[32:35], s17 offen sc1\n"
"v_mov_b32 v196, 0\n"
"v_mfma_f32_16x16x32_bf16 v[136:139], v[120:123], a[8:11], v[136:139]\n"
"ds_read_b64_tr_b16 v[186:187], v232 offset:2048\n"
"v_mov_b32 v197, 0\n"
"v_mfma_f32_16x16x32_bf16 v[136:139], v[224:227], a[12:15], v[136:139]\n"
"ds_read_b64_tr_b16 v[188:189], v228 offset:8192\n"
"v_mov_b32 v198, 0\n"
"v_mfma_f32_16x16x32_bf16 v[152:155], v[224:227], a[28:31], v[152:155]\n"
"ds_read_b64_tr_b16 v[190:191], v228 offset:10240\n"
"v_mov_b32 v199, 0\n"
"v_mfma_f32_16x16x32_bf16 v[152:155], v[120:123], a[24:27], v[152:155]\n"
"ds_read_b64_tr_b16 v[192:193], v232 offset:8192\n"
"v_mov_b32 v200, 0\n"
"v_mfma_f32_16x16x32_bf16 v[152:155], v[116:119], a[20:23], v[152:155]\n"
"ds_read_b64_tr_b16 v[194:195], v232 offset:10240\n"
"v_mov_b32 v201, 0\n"
"v_mfma_f32_16x16x32_bf16 v[152:155], v[112:115], a[16:19], v[152:155]\n"
"ds_read_b64_tr_b16 v[208:209], v228 offset:16384\n"
"v_mov_b32 v202, 0\n"
"v_mfma_f32_16x16x32_bf16 v[152:155], v[108:111], a[76:79], v[152:155]\n"
"ds_read_b64_tr_b16 v[210:211], v228 offset:18432\n"
"v_mov_b32 v203, 0\n"
"v_mfma_f32_16x16x32_bf16 v[152:155], v[104:107], a[72:75], v[152:155]\n"
"ds_read_b64_tr_b16 v[212:213], v232 offset:16384\n"
"v_mov_b32 v204, 0\n"
"v_mfma_f32_16x16x32_bf16 v[152:155], v[100:103], a[68:71], v[152:155]\n"
"ds_read_b64_tr_b16 v[214:215], v232 offset:18432\n"
"v_mov_b32 v205, 0\n"
"v_mfma_f32_16x16x32_bf16 v[152:155], v[96:99], a[64:67], v[152:155]\n"
"ds_read_b64_tr_b16 v[216:217], v228 offset:24576\n"
"v_mov_b32 v206, 0\n"
"v_mfma_f32_16x16x32_bf16 v[168:171], v[96:99], a[80:83], v[168:171]\n"
"ds_read_b64_tr_b16 v[218:219], v228 offset:26624\n"
"v_mov_b32 v207, 0\n"
"v_mfma_f32_16x16x32_bf16 v[168:171], v[100:103], a[84:87], v[168:171]\n"
"ds_read_b64_tr_b16 v[220:221], v232 offset:24576\n"
"v_cvt_pk_bf16_f32 v172, v172, v173\n"
"v_mfma_f32_16x16x32_bf16 v[168:171], v[104:107], a[88:91], v[168:171]\n"
"ds_read_b64_tr_b16 v[222:223], v232 offset:26624\n"
"v_cvt_pk_bf16_f32 v173, v174, v175\n"
"v_mfma_f32_16x16x32_bf16 v[168:171], v[108:111], a[92:95], v[168:171]\n"
"ds_read_b64_tr_b16 v[96:97], v229 offset:0\n"
"v_cvt_pk_bf16_f32 v174, v176, v177\n"
"v_mfma_f32_16x16x32_bf16 v[168:171], v[112:115], a[32:35], v[168:171]\n"
"ds_read_b64_tr_b16 v[98:99], v229 offset:2048\n"
"v_cvt_pk_bf16_f32 v175, v178, v179\n"
"v_mfma_f32_16x16x32_bf16 v[168:171], v[116:119], a[36:39], v[168:171]\n"
"ds_read_b64_tr_b16 v[100:101], v233 offset:0\n"
"v_cvt_pk_bf16_f32 v132, v132, v133\n"
"v_mfma_f32_16x16x32_bf16 v[168:171], v[120:123], a[40:43], v[168:171]\n"
"s_add_u32 s47, s47, 1\n"
"ds_read_b64_tr_b16 v[102:103], v233 offset:2048\n"
"v_cvt_pk_bf16_f32 v133, v134, v135\n"
"v_mfma_f32_16x16x32_bf16 v[168:171], v[224:227], a[44:47], v[168:171]\n"
"v_cvt_pk_bf16_f32 v148, v148, v149\n"
"v_cvt_pk_bf16_f32 v149, v150, v151\n"
"v_xor_b32 v230, s75, v230\n"
"v_xor_b32 v234, s75, v234\n"
"LGL_%=:\n"
"s_waitcnt lgkmcnt(7)\n"
"v_mfma_f32_16x16x32_bf16 v[124:127], v[180:183], a[96:99], 0\n"
"s_add_u32 s41, s70, 0\n"
"ds_read_b64_tr_b16 v[104:105], v229 offset:8192\n"
"v_cvt_pk_bf16_f32 v134, v136, v137\n"
"v_mfma_f32_16x16x32_bf16 v[124:127], v[184:187], a[100:103], v[124:127]\n"
"s_mov_b32 m0, s71\n"
"ds_read_b64_tr_b16 v[106:107], v229 offset:10240\n"
"v_cvt_pk_bf16_f32 v135, v138, v139\n"
"v_mfma_f32_16x16x32_bf16 v[124:127], v[188:191], a[104:107], v[124:127]\n"
"buffer_load_dwordx4 v240, s[28:31], s41 offen lds\n"
"v_mfma_f32_16x16x32_bf16 v[124:127], v[192:195], a[108:111], v[124:127]\n"
"buffer_store_dwordx4 v[172:175], v243, s[32:35], s17 offen offset:2048 sc1\n"
"v_cvt_pk_bf16_f32 v150, v152, v153\n"
"v_mfma_f32_16x16x32_bf16 v[124:127], v[208:211], a[144:147], v[124:127]\n"
"ds_read_b64_tr_b16 v[108:109], v233 offset:8192\n"
"v_cvt_pk_bf16_f32 v151, v154, v155\n"
"v_mfma_f32_16x16x32_bf16 v[124:127], v[212:215], a[148:151], v[124:127]\n"
"s_waitcnt vmcnt(13) lgkmcnt(4)\n"
"ds_read_b64_tr_b16 v[110:111], v233 offset:10240\n"
"v_cvt_pk_bf16_f32 v164, v164, v165\n"
"v_mfma_f32_16x16x32_bf16 v[124:127], v[216:219], a[152:155], v[124:127]\n"
"ds_read_b64_tr_b16 v[112:113], v229 offset:16384\n"
"v_cvt_pk_bf16_f32 v165, v166, v167\n"
"v_mfma_f32_16x16x32_bf16 v[124:127], v[220:223], a[156:159], v[124:127]\n"
"ds_read_b64_tr_b16 v[114:115], v229 offset:18432\n"
"v_cvt_pk_bf16_f32 v166, v168, v169\n"
"v_mfma_f32_16x16x32_bf16 v[140:143], v[220:223], a[172:175], 0\n"
"s_add_u32 s40, s41, s52\n"
"ds_read_b64_tr_b16 v[116:117], v233 offset:16384\n"
"v_cvt_pk_bf16_f32 v167, v170, v171\n"
"v_mfma_f32_16x16x32_bf16 v[140:143], v[216:219], a[168:171], v[140:143]\n"
"buffer_load_dwordx4 v240, s[28:31], s40 offen offset:1024 lds\n"
"v_mfma_f32_16x16x32_bf16 v[140:143], v[212:215], a[164:167], v[140:143]\n"
"ds_read_b64_tr_b16 v[118:119], v233 offset:18432\n"
"v_xor_b32 v231, s75, v231\n"
"v_mfma_f32_16x16x32_bf16 v[140:143], v[208:211], a[160:163], v[140:143]\n"
"ds_read_b64_tr_b16 v[120:121], v229 offset:24576\n"
"v_xor_b32 v235, s75, v235\n"
"v_mfma_f32_16x16x32_bf16 v[140:143], v[192:195], a[124:127], v[140:143]\n"
"ds_read_b64_tr_b16 v[122:123], v229 offset:26624\n"
"v_add_u32 v241, 1, v241\n"
"v_mfma_f32_16x16x32_bf16 v[140:143], v[188:191], a[120:123], v[140:143]\n"
"ds_read_b64_tr_b16 v[224:225], v233 offset:24576\n"
"v_lshlrev_b32 v248, 16, v92\n"
"v_mfma_f32_16x16x32_bf16 v[140:143], v[184:187], a[116:119], v[140:143]\n"
"ds_read_b64_tr_b16 v[226:227], v233 offset:26624\n"
"v_and_b32 v92, s73, v92\n"
"v_mfma_f32_16x16x32_bf16 v[140:143], v[180:183], a[112:115], v[140:143]\n"
"v_add_f32 v41, v41, v92\n"
"v_lshlrev_b32 v249, 16, v93\n"
"v_mfma_f32_16x16x32_bf16 v[156:159], v[180:183], a[128:131], 0\n"
"s_add_u32 s40, s41, s53\n"
"v_and_b32 v93, s73, v93\n"
"v_add_f32 v43, v43, v93\n"
"v_mfma_f32_16x16x32_bf16 v[156:159], v[184:187], a[132:135], v[156:159]\n"
"buffer_load_dwordx4 v240, s[28:31], s40 offen offset:2048 lds\n"
"v_mfma_f32_16x16x32_bf16 v[156:159], v[188:191], a[136:139], v[156:159]\n"
"v_lshlrev_b32 v250, 16, v94\n"
"v_and_b32 v94, s73, v94\n"
"v_mfma_f32_16x16x32_bf16 v[156:159], v[192:195], a[140:143], v[156:159]\n"
"v_add_f32 v45, v45, v94\n"
"v_lshlrev_b32 v251, 16, v95\n"
"v_mfma_f32_16x16x32_bf16 v[156:159], v[208:211], a[176:179], v[156:159]\n"
"v_and_b32 v95, s73, v95\n"
"v_add_f32 v47, v47, v95\n"
"v_mfma_f32_16x16x32_bf16 v[156:159], v[212:215], a[180:183], v[156:159]\n"
"v_add_f32 v40, v40, v248\n"
"v_add_f32 v42, v42, v249\n"
"v_mfma_f32_16x16x32_bf16 v[156:159], v[216:219], a[184:187], v[156:159]\n"
"v_add_f32 v44, v44, v250\n"
"v_add_f32 v46, v46, v251\n"
"v_mfma_f32_16x16x32_bf16 v[156:159], v[220:223], a[188:191], v[156:159]\n"
"s_waitcnt vmcnt(12)\n"
"v_lshlrev_b32 v246, 16, v196\n"
"v_and_b32 v196, s73, v196\n"
"v_mfma_f32_16x16x32_bf16 v[172:175], v[220:223], a[252:255], 0\n"
"ds_read_b64_tr_b16 v[220:221], v234 offset:24576\n"
"v_add_f32 v49, v49, v196\n"
"v_mfma_f32_16x16x32_bf16 v[172:175], v[216:219], a[248:251], v[172:175]\n"
"buffer_store_dwordx4 v[132:135], v243, s[32:35], s16 offen offset:1024 sc1\n"
"v_lshlrev_b32 v247, 16, v197\n"
"v_mfma_f32_16x16x32_bf16 v[172:175], v[212:215], a[244:247], v[172:175]\n"
"ds_read_b64_tr_b16 v[212:213], v234 offset:16384\n"
"v_and_b32 v197, s73, v197\n"
"v_mfma_f32_16x16x32_bf16 v[172:175], v[208:211], a[240:243], v[172:175]\n"
"ds_read_b64_tr_b16 v[208:209], v230 offset:16384\n"
"v_add_f32 v51, v51, v197\n"
"v_mfma_f32_16x16x32_bf16 v[172:175], v[192:195], a[204:207], v[172:175]\n"
"ds_read_b64_tr_b16 v[192:193], v234 offset:8192\n"
"v_lshlrev_b32 v248, 16, v198\n"
"v_mfma_f32_16x16x32_bf16 v[172:175], v[188:191], a[200:203], v[172:175]\n"
"ds_read_b64_tr_b16 v[188:189], v230 offset:8192\n"
"v_and_b32 v198, s73, v198\n"
"v_mfma_f32_16x16x32_bf16 v[172:175], v[184:187], a[196:199], v[172:175]\n"
"s_add_u32 s40, s41, s54\n"
"ds_read_b64_tr_b16 v[184:185], v234 offset:0\n"
"v_add_f32 v53, v53, v198\n"
"v_mfma_f32_16x16x32_bf16 v[172:175], v[180:183], a[192:195], v[172:175]\n"
"buffer_load_dwordx4 v240, s[28:31], s40 offen offset:3072 lds\n"
"v_mfma_f32_16x16x32_bf16 v[128:131], v[96:99], a[96:99], 0\n"
"s_waitcnt lgkmcnt(5)\n"
"ds_read_b64_tr_b16 v[180:181], v230 offset:0\n"
"v_lshlrev_b32 v249, 16, v199\n"
"v_mfma_f32_16x16x32_bf16 v[128:131], v[100:103], a[100:103], v[128:131]\n"
"ds_read_b64_tr_b16 v[182:183], v230 offset:2048\n"
"v_and_b32 v199, s73, v199\n"
"v_mfma_f32_16x16x32_bf16 v[128:131], v[104:107], a[104:107], v[128:131]\n"
"ds_read_b64_tr_b16 v[186:187], v234 offset:2048\n"
"v_add_f32 v55, v55, v199\n"
"v_mfma_f32_16x16x32_bf16 v[128:131], v[108:111], a[108:111], v[128:131]\n"
"ds_read_b64_tr_b16 v[190:191], v230 offset:10240\n"
"v_add_f32 v48, v48, v246\n"
"v_mfma_f32_16x16x32_bf16 v[128:131], v[112:115], a[144:147], v[128:131]\n"
"s_waitcnt vmcnt(7)\n"
"ds_read_b64_tr_b16 v[194:195], v234 offset:10240\n"
"v_add_f32 v50, v50, v247\n"
"v_mfma_f32_16x16x32_bf16 v[128:131], v[116:119], a[148:151], v[128:131]\n"
"v_subrev_u32 v244, s21, v244\n"
"ds_read_b64_tr_b16 v[210:211], v230 offset:18432\n"
"v_mfma_f32_16x16x32_bf16 v[128:131], v[120:123], a[152:155], v[128:131]\n"
"v_cmp_lt_u32 vcc, s69, v244\n"
"ds_read_b64_tr_b16 v[214:215], v234 offset:18432\n"
"v_mfma_f32_16x16x32_bf16 v[128:131], v[224:227], a[156:159], v[128:131]\n"
"ds_read_b64_tr_b16 v[216:217], v230 offset:24576\n"
"s_cbranch_vccnz LGP_%=\nLGPK_%=:\n"
"v_mfma_f32_16x16x32_bf16 v[144:147], v[224:227], a[172:175], 0\n"
"s_sub_u32 s72, s72, 1\n"
"ds_read_b64_tr_b16 v[218:219], v230 offset:26624\n"
"v_add_f32 v52, v52, v248\n"
"v_mfma_f32_16x16x32_bf16 v[144:147], v[120:123], a[168:171], v[144:147]\n"
"s_and_b32 s72, s72, 7\n"
"ds_read_b64_tr_b16 v[222:223], v234 offset:26624\n"
"v_mfma_f32_16x16x32_bf16 v[144:147], v[116:119], a[164:167], v[144:147]\n"
"v_lshlrev_b32 v250, 16, v200\n"
"v_and_b32 v200, s73, v200\n"
"v_mfma_f32_16x16x32_bf16 v[144:147], v[112:115], a[160:163], v[144:147]\n"
"buffer_load_dwordx4 v[72:75], v243, s[32:35], s18 offen sc1\n"
"v_mfma_f32_16x16x32_bf16 v[144:147], v[108:111], a[124:127], v[144:147]\n"
"v_add_f32 v57, v57, v200\n"
"v_lshlrev_b32 v251, 16, v201\n"
"v_mfma_f32_16x16x32_bf16 v[144:147], v[104:107], a[120:123], v[144:147]\n"
"v_and_b32 v201, s73, v201\n"
"v_add_f32 v59, v59, v201\n"
"v_mfma_f32_16x16x32_bf16 v[144:147], v[100:103], a[116:119], v[144:147]\n"
"v_lshlrev_b32 v246, 16, v202\n"
"v_and_b32 v202, s73, v202\n"
"v_mfma_f32_16x16x32_bf16 v[144:147], v[96:99], a[112:115], v[144:147]\n"
"buffer_store_dwordx4 v[148:151], v243, s[32:35], s16 offen offset:3072 sc1\n"
"v_add_f32 v61, v61, v202\n"
"v_mfma_f32_16x16x32_bf16 v[160:163], v[96:99], a[128:131], 0\n"
"v_lshlrev_b32 v247, 16, v203\n"
"v_and_b32 v203, s73, v203\n"
"v_mfma_f32_16x16x32_bf16 v[160:163], v[100:103], a[132:135], v[160:163]\n"
"s_add_u32 s41, s70, 163840\n"
"v_add_f32 v54, v54, v249\n"
"v_add_f32 v63, v63, v203\n"
"v_mfma_f32_16x16x32_bf16 v[160:163], v[104:107], a[136:139], v[160:163]\n"
"s_add_u32 m0, s71, 16384\n"
"v_add_f32 v56, v56, v250\n"
"v_add_f32 v58, v58, v251\n"
"v_mfma_f32_16x16x32_bf16 v[160:163], v[108:111], a[140:143], v[160:163]\n"
"buffer_load_dwordx4 v240, s[28:31], s41 offen lds\n"
"v_mfma_f32_16x16x32_bf16 v[160:163], v[112:115], a[176:179], v[160:163]\n"
"v_add_f32 v60, v60, v246\n"
"v_add_f32 v62, v62, v247\n"
"v_mfma_f32_16x16x32_bf16 v[160:163], v[116:119], a[180:183], v[160:163]\n"
"v_mfma_f32_16x16x32_bf16 v[160:163], v[120:123], a[184:187], v[160:163]\n"
"v_mfma_f32_16x16x32_bf16 v[160:163], v[224:227], a[188:191], v[160:163]\n"
"v_mfma_f32_16x16x32_bf16 v[176:179], v[224:227], a[252:255], 0\n"
"s_add_u32 s40, s41, s52\n"
"ds_read_b64_tr_b16 v[224:225], v235 offset:24576\n"
"v_mfma_f32_16x16x32_bf16 v[176:179], v[120:123], a[248:251], v[176:179]\n"
"buffer_load_dwordx4 v240, s[28:31], s40 offen offset:1024 lds\n"
"v_mfma_f32_16x16x32_bf16 v[176:179], v[116:119], a[244:247], v[176:179]\n"
"ds_read_b64_tr_b16 v[116:117], v235 offset:16384\n"
"v_mfma_f32_16x16x32_bf16 v[176:179], v[112:115], a[240:243], v[176:179]\n"
"ds_read_b64_tr_b16 v[112:113], v231 offset:16384\n"
"v_mfma_f32_16x16x32_bf16 v[176:179], v[108:111], a[204:207], v[176:179]\n"
"ds_read_b64_tr_b16 v[108:109], v235 offset:8192\n"
"v_mfma_f32_16x16x32_bf16 v[176:179], v[104:107], a[200:203], v[176:179]\n"
"buffer_load_dwordx4 v[76:79], v243, s[32:35], s18 offen offset:1024 sc1\n"
"v_mfma_f32_16x16x32_bf16 v[176:179], v[100:103], a[196:199], v[176:179]\n"
"ds_read_b64_tr_b16 v[100:101], v235 offset:0\n"
"v_lshlrev_b32 v248, 16, v204\n"
"v_mfma_f32_16x16x32_bf16 v[176:179], v[96:99], a[192:195], v[176:179]\n"
"s_waitcnt lgkmcnt(5)\n"
"ds_read_b64_tr_b16 v[96:97], v231 offset:0\n"
"v_and_b32 v204, s73, v204\n"
"v_mfma_f32_16x16x32_bf16 v[132:135], v[180:183], a[96:99], 0\n"
"s_add_u32 s40, s41, s53\n"
"ds_read_b64_tr_b16 v[98:99], v231 offset:2048\n"
"v_add_f32 v65, v65, v204\n"
"v_mfma_f32_16x16x32_bf16 v[132:135], v[184:187], a[100:103], v[132:135]\n"
"buffer_load_dwordx4 v240, s[28:31], s40 offen offset:2048 lds\n"
"v_mfma_f32_16x16x32_bf16 v[132:135], v[188:191], a[104:107], v[132:135]\n"
"ds_read_b64_tr_b16 v[102:103], v235 offset:2048\n"
"v_lshlrev_b32 v249, 16, v205\n"
"v_mfma_f32_16x16x32_bf16 v[132:135], v[192:195], a[108:111], v[132:135]\n"
"ds_read_b64_tr_b16 v[104:105], v231 offset:8192\n"
"v_and_b32 v205, s73, v205\n"
"v_mfma_f32_16x16x32_bf16 v[132:135], v[208:211], a[144:147], v[132:135]\n"
"ds_read_b64_tr_b16 v[106:107], v231 offset:10240\n"
"v_add_f32 v67, v67, v205\n"
"v_mfma_f32_16x16x32_bf16 v[132:135], v[212:215], a[148:151], v[132:135]\n"
"buffer_store_dwordx4 v[164:167], v243, s[32:35], s17 offen offset:1024 sc1\n"
"v_lshlrev_b32 v250, 16, v206\n"
"v_mfma_f32_16x16x32_bf16 v[132:135], v[216:219], a[152:155], v[132:135]\n"
"ds_read_b64_tr_b16 v[110:111], v235 offset:10240\n"
"v_and_b32 v206, s73, v206\n"
"v_mfma_f32_16x16x32_bf16 v[132:135], v[220:223], a[156:159], v[132:135]\n"
"ds_read_b64_tr_b16 v[114:115], v231 offset:18432\n"
"v_add_f32 v69, v69, v206\n"
"v_mfma_f32_16x16x32_bf16 v[148:151], v[220:223], a[172:175], 0\n"
"ds_read_b64_tr_b16 v[118:119], v235 offset:18432\n"
"v_lshlrev_b32 v251, 16, v207\n"
"v_mfma_f32_16x16x32_bf16 v[148:151], v[216:219], a[168:171], v[148:151]\n"
"ds_read_b64_tr_b16 v[120:121], v231 offset:24576\n"
"v_and_b32 v207, s73, v207\n"
"v_mfma_f32_16x16x32_bf16 v[148:151], v[212:215], a[164:167], v[148:151]\n"
"s_add_u32 s40, s41, s54\n"
"ds_read_b64_tr_b16 v[122:123], v231 offset:26624\n"
"v_add_f32 v71, v71, v207\n"
"v_mfma_f32_16x16x32_bf16 v[148:151], v[208:211], a[160:163], v[148:151]\n"
"buffer_load_dwordx4 v240, s[28:31], s40 offen offset:3072 lds\n"
"v_mfma_f32_16x16x32_bf16 v[148:151], v[192:195], a[124:127], v[148:151]\n"
"ds_read_b64_tr_b16 v[226:227], v235 offset:26624\n"
"v_add_f32 v64, v64, v248\n"
"v_mfma_f32_16x16x32_bf16 v[148:151], v[188:191], a[120:123], v[148:151]\n"
"v_add_f32 v66, v66, v249\n"
"v_add_f32 v68, v68, v250\n"
"v_mfma_f32_16x16x32_bf16 v[148:151], v[184:187], a[116:119], v[148:151]\n"
"v_add_f32 v70, v70, v251\n"
"v_mfma_f32_16x16x32_bf16 v[148:151], v[180:183], a[112:115], v[148:151]\n"
"buffer_load_dwordx4 v[80:83], v243, s[32:35], s18 offen offset:2048 sc1\n"
"v_mfma_f32_16x16x32_bf16 v[164:167], v[180:183], a[128:131], 0\n"
"v_mfma_f32_16x16x32_bf16 v[164:167], v[184:187], a[132:135], v[164:167]\n"
"v_mfma_f32_16x16x32_bf16 v[164:167], v[188:191], a[136:139], v[164:167]\n"
"v_mfma_f32_16x16x32_bf16 v[164:167], v[192:195], a[140:143], v[164:167]\n"
"v_mfma_f32_16x16x32_bf16 v[164:167], v[208:211], a[176:179], v[164:167]\n"
"v_mfma_f32_16x16x32_bf16 v[164:167], v[212:215], a[180:183], v[164:167]\n"
"v_mfma_f32_16x16x32_bf16 v[164:167], v[216:219], a[184:187], v[164:167]\n"
"v_mfma_f32_16x16x32_bf16 v[164:167], v[220:223], a[188:191], v[164:167]\n"
"s_waitcnt lgkmcnt(0)\n"
"v_mfma_f32_16x16x32_bf16 v[136:139], v[96:99], a[96:99], 0\n"
"v_mfma_f32_16x16x32_bf16 v[136:139], v[100:103], a[100:103], v[136:139]\n"
"s_barrier\n"
"v_mfma_f32_16x16x32_bf16 v[136:139], v[104:107], a[104:107], v[136:139]\n"
"ds_read_b64_tr_b16 v[180:181], v228 offset:32768\n"
"v_mfma_f32_16x16x32_bf16 v[136:139], v[108:111], a[108:111], v[136:139]\n"
"ds_read_b64_tr_b16 v[182:183], v228 offset:34816\n"
"v_mfma_f32_16x16x32_bf16 v[136:139], v[112:115], a[144:147], v[136:139]\n"
"ds_read_b64_tr_b16 v[184:185], v232 offset:32768\n"
"v_mfma_f32_16x16x32_bf16 v[136:139], v[116:119], a[148:151], v[136:139]\n"
"ds_read_b64_tr_b16 v[186:187], v232 offset:34816\n"
"v_mfma_f32_16x16x32_bf16 v[136:139], v[120:123], a[152:155], v[136:139]\n"
"ds_read_b64_tr_b16 v[188:189], v228 offset:40960\n"
"v_mfma_f32_16x16x32_bf16 v[136:139], v[224:227], a[156:159], v[136:139]\n"
"ds_read_b64_tr_b16 v[190:191], v228 offset:43008\n"
"v_mfma_f32_16x16x32_bf16 v[152:155], v[224:227], a[172:175], 0\n"
"ds_read_b64_tr_b16 v[192:193], v232 offset:40960\n"
"v_mfma_f32_16x16x32_bf16 v[152:155], v[120:123], a[168:171], v[152:155]\n"
"buffer_load_dwordx4 v[84:87], v243, s[32:35], s18 offen offset:3072 sc1\n"
"v_mfma_f32_16x16x32_bf16 v[152:155], v[116:119], a[164:167], v[152:155]\n"
"ds_read_b64_tr_b16 v[194:195], v232 offset:43008\n"
"v_mfma_f32_16x16x32_bf16 v[152:155], v[112:115], a[160:163], v[152:155]\n"
"ds_read_b64_tr_b16 v[208:209], v228 offset:49152\n"
"v_mfma_f32_16x16x32_bf16 v[152:155], v[108:111], a[124:127], v[152:155]\n"
"ds_read_b64_tr_b16 v[210:211], v228 offset:51200\n"
"v_mfma_f32_16x16x32_bf16 v[152:155], v[104:107], a[120:123], v[152:155]\n"
"ds_read_b64_tr_b16 v[212:213], v232 offset:49152\n"
"v_mfma_f32_16x16x32_bf16 v[152:155], v[100:103], a[116:119], v[152:155]\n"
"ds_read_b64_tr_b16 v[214:215], v232 offset:51200\n"
"v_mfma_f32_16x16x32_bf16 v[152:155], v[96:99], a[112:115], v[152:155]\n"
"ds_read_b64_tr_b16 v[216:217], v228 offset:57344\n"
"v_mfma_f32_16x16x32_bf16 v[168:171], v[96:99], a[128:131], 0\n"
"ds_read_b64_tr_b16 v[218:219], v228 offset:59392\n"
"v_xor_b32 v228, s75, v228\n"
"v_mfma_f32_16x16x32_bf16 v[168:171], v[100:103], a[132:135], v[168:171]\n"
"s_add_u32 s41, s70, 327680\n"
"ds_read_b64_tr_b16 v[220:221], v232 offset:57344\n"
"v_mfma_f32_16x16x32_bf16 v[168:171], v[104:107], a[136:139], v[168:171]\n"
"s_add_u32 m0, s71, 32768\n"
"ds_read_b64_tr_b16 v[222:223], v232 offset:59392\n"
"v_xor_b32 v232, s75, v232\n"
"v_mfma_f32_16x16x32_bf16 v[168:171], v[108:111], a[140:143], v[168:171]\n"
"buffer_load_dwordx4 v240, s[28:31], s41 offen lds\n"
"v_mfma_f32_16x16x32_bf16 v[168:171], v[112:115], a[176:179], v[168:171]\n"
"ds_read_b64_tr_b16 v[96:97], v229 offset:32768\n"
"v_mfma_f32_16x16x32_bf16 v[168:171], v[116:119], a[180:183], v[168:171]\n"
"ds_read_b64_tr_b16 v[98:99], v229 offset:34816\n"
"v_mfma_f32_16x16x32_bf16 v[168:171], v[120:123], a[184:187], v[168:171]\n"
"ds_read_b64_tr_b16 v[100:101], v233 offset:32768\n"
"v_mfma_f32_16x16x32_bf16 v[168:171], v[224:227], a[188:191], v[168:171]\n"
"s_waitcnt lgkmcnt(6)\n"
"ds_read_b64_tr_b16 v[102:103], v233 offset:34816\n"
"v_mfma_f32_16x16x32_bf16 v[124:127], v[180:183], a[48:51], v[124:127]\n"
"s_add_u32 s40, s41, s52\n"
"ds_read_b64_tr_b16 v[104:105], v229 offset:40960\n"
"v_mfma_f32_16x16x32_bf16 v[124:127], v[184:187], a[52:55], v[124:127]\n"
"buffer_load_dwordx4 v240, s[28:31], s40 offen offset:1024 lds\n"
"v_mfma_f32_16x16x32_bf16 v[124:127], v[188:191], a[56:59], v[124:127]\n"
"ds_read_b64_tr_b16 v[106:107], v229 offset:43008\n"
"v_mfma_f32_16x16x32_bf16 v[124:127], v[192:195], a[60:63], v[124:127]\n"
"ds_read_b64_tr_b16 v[108:109], v233 offset:40960\n"
"v_mfma_f32_16x16x32_bf16 v[124:127], v[208:211], a[0:3], v[124:127]\n"
"ds_read_b64_tr_b16 v[110:111], v233 offset:43008\n"
"v_mfma_f32_16x16x32_bf16 v[124:127], v[212:215], a[4:7], v[124:127]\n"
"s_waitcnt lgkmcnt(6)\n"
"buffer_load_dwordx4 v[88:91], v243, s[32:35], s19 offen sc1\n"
"v_mfma_f32_16x16x32_bf16 v[124:127], v[216:219], a[8:11], v[124:127]\n"
"ds_read_b64_tr_b16 v[112:113], v229 offset:49152\n"
"v_mfma_f32_16x16x32_bf16 v[124:127], v[220:223], a[12:15], v[124:127]\n"
"ds_read_b64_tr_b16 v[114:115], v229 offset:51200\n"
"v_mfma_f32_16x16x32_bf16 v[140:143], v[220:223], a[28:31], v[140:143]\n"
"ds_read_b64_tr_b16 v[116:117], v233 offset:49152\n"
"v_mfma_f32_16x16x32_bf16 v[140:143], v[216:219], a[24:27], v[140:143]\n"
"ds_read_b64_tr_b16 v[118:119], v233 offset:51200\n"
"v_mfma_f32_16x16x32_bf16 v[140:143], v[212:215], a[20:23], v[140:143]\n"
"s_add_u32 s40, s41, s53\n"
"ds_read_b64_tr_b16 v[120:121], v229 offset:57344\n"
"v_mfma_f32_16x16x32_bf16 v[140:143], v[208:211], a[16:19], v[140:143]\n"
"buffer_load_dwordx4 v240, s[28:31], s40 offen offset:2048 lds\n"
"v_mfma_f32_16x16x32_bf16 v[140:143], v[192:195], a[76:79], v[140:143]\n"
"ds_read_b64_tr_b16 v[122:123], v229 offset:59392\n"
"v_xor_b32 v229, s75, v229\n"
"v_mfma_f32_16x16x32_bf16 v[140:143], v[188:191], a[72:75], v[140:143]\n"
"ds_read_b64_tr_b16 v[224:225], v233 offset:57344\n"
"v_cvt_pk_bf16_f32 v124, v124, v125\n"
"v_mfma_f32_16x16x32_bf16 v[140:143], v[184:187], a[68:71], v[140:143]\n"
"ds_read_b64_tr_b16 v[226:227], v233 offset:59392\n"
"v_cvt_pk_bf16_f32 v125, v126, v127\n"
"v_mfma_f32_16x16x32_bf16 v[140:143], v[180:183], a[64:67], v[140:143]\n"
"v_xor_b32 v233, s75, v233\n"
"v_mfma_f32_16x16x32_bf16 v[156:159], v[180:183], a[80:83], v[156:159]\n"
"v_mfma_f32_16x16x32_bf16 v[156:159], v[184:187], a[84:87], v[156:159]\n"
"v_mfma_f32_16x16x32_bf16 v[156:159], v[188:191], a[88:91], v[156:159]\n"
"s_add_u32 s40, s41, s54\n"
"v_mfma_f32_16x16x32_bf16 v[156:159], v[192:195], a[92:95], v[156:159]\n"
"buffer_load_dwordx4 v240, s[28:31], s40 offen offset:3072 lds\n"
"v_mfma_f32_16x16x32_bf16 v[156:159], v[208:211], a[32:35], v[156:159]\n"
"v_mfma_f32_16x16x32_bf16 v[156:159], v[212:215], a[36:39], v[156:159]\n"
"s_waitcnt vmcnt(14)\n"
"v_cvt_pk_bf16_f32 v140, v140, v141\n"
"v_cvt_pk_bf16_f32 v141, v142, v143\n"
"v_mfma_f32_16x16x32_bf16 v[156:159], v[216:219], a[40:43], v[156:159]\n"
"s_waitcnt vmcnt(8)\n"
"v_lshlrev_b32 v246, 16, v72\n"
"v_and_b32 v72, s73, v72\n"
"v_mfma_f32_16x16x32_bf16 v[156:159], v[220:223], a[44:47], v[156:159]\n"
"s_barrier\n"
"v_mfma_f32_16x16x32_bf16 v[172:175], v[220:223], a[228:231], v[172:175]\n"
"ds_read_b64_tr_b16 v[220:221], v234 offset:57344\n"
"s_cmp_eq_u32 s42, 0\n"
"v_mfma_f32_16x16x32_bf16 v[172:175], v[216:219], a[224:227], v[172:175]\n"
"s_cbranch_scc1 LGS_%=\nLGSK_%=:\n"
"ds_read_b64_tr_b16 v[216:217], v230 offset:57344\n"
"v_mfma_f32_16x16x32_bf16 v[172:175], v[212:215], a[212:215], v[172:175]\n"
"ds_read_b64_tr_b16 v[212:213], v234 offset:49152\n"
"v_add_f32 v0, v0, v246\n"
"v_mfma_f32_16x16x32_bf16 v[172:175], v[208:211], a[208:211], v[172:175]\n"
"s_mul_i32 s16, s61, 286720\n"
"buffer_load_dwordx4 v[92:95], v243, s[32:35], s19 offen offset:1024 sc1\n"
"v_mfma_f32_16x16x32_bf16 v[172:175], v[192:195], a[236:239], v[172:175]\n"
"s_add_u32 s16, s16, s62\n"
"ds_read_b64_tr_b16 v[192:193], v234 offset:40960\n"
"v_add_f32 v1, v1, v72\n"
"v_mfma_f32_16x16x32_bf16 v[172:175], v[188:191], a[232:235], v[172:175]\n"
"s_add_u32 s17, s16, 4096\n"
"ds_read_b64_tr_b16 v[188:189], v230 offset:40960\n"
"v_cvt_pk_bf16_f32 v156, v156, v157\n"
"v_mfma_f32_16x16x32_bf16 v[172:175], v[184:187], a[220:223], v[172:175]\n"
"s_mul_i32 s62, s70, 224\n"
"ds_read_b64_tr_b16 v[184:185], v234 offset:32768\n"
"v_cvt_pk_bf16_f32 v157, v158, v159\n"
"v_mfma_f32_16x16x32_bf16 v[172:175], v[180:183], a[216:219], v[172:175]\n"
"s_add_u32 s41, s70, 491520\n"
"ds_read_b64_tr_b16 v[180:181], v230 offset:32768\n"
"v_lshlrev_b32 v247, 16, v73\n"
"v_mfma_f32_16x16x32_bf16 v[128:131], v[96:99], a[48:51], v[128:131]\n"
"s_waitcnt lgkmcnt(6)\n"
"s_add_u32 m0, s71, 49152\n"
"ds_read_b64_tr_b16 v[182:183], v230 offset:34816\n"
"v_and_b32 v73, s73, v73\n"
"v_mfma_f32_16x16x32_bf16 v[128:131], v[100:103], a[52:55], v[128:131]\n"
"buffer_load_dwordx4 v240, s[28:31], s41 offen lds\n"
"v_mfma_f32_16x16x32_bf16 v[128:131], v[104:107], a[56:59], v[128:131]\n"
"ds_read_b64_tr_b16 v[186:187], v234 offset:34816\n"
"v_add_f32 v2, v2, v247\n"
"v_mfma_f32_16x16x32_bf16 v[128:131], v[108:111], a[60:63], v[128:131]\n"
"ds_read_b64_tr_b16 v[190:191], v230 offset:43008\n"
"v_add_f32 v3, v3, v73\n"
"v_mfma_f32_16x16x32_bf16 v[128:131], v[112:115], a[0:3], v[128:131]\n"
"ds_read_b64_tr_b16 v[194:195], v234 offset:43008\n"
"v_lshlrev_b32 v248, 16, v74\n"
"v_mfma_f32_16x16x32_bf16 v[128:131], v[116:119], a[4:7], v[128:131]\n"
"ds_read_b64_tr_b16 v[208:209], v230 offset:49152\n"
"v_and_b32 v74, s73, v74\n"
"v_mfma_f32_16x16x32_bf16 v[128:131], v[120:123], a[8:11], v[128:131]\n"
"s_add_u32 s40, s41, s52\n"
"ds_read_b64_tr_b16 v[210:211], v230 offset:51200\n"
"v_add_f32 v4, v4, v248\n"
"v_mfma_f32_16x16x32_bf16 v[128:131], v[224:227], a[12:15], v[128:131]\n"
"buffer_load_dwordx4 v240, s[28:31], s40 offen offset:1024 lds\n"
"v_mfma_f32_16x16x32_bf16 v[144:147], v[224:227], a[28:31], v[144:147]\n"
"ds_read_b64_tr_b16 v[214:215], v234 offset:51200\n"
"v_add_f32 v5, v5, v74\n"
"v_mfma_f32_16x16x32_bf16 v[144:147], v[120:123], a[24:27], v[144:147]\n"
"ds_read_b64_tr_b16 v[218:219], v230 offset:59392\n"
"v_lshlrev_b32 v249, 16, v75\n"
"v_mfma_f32_16x16x32_bf16 v[144:147], v[116:119], a[20:23], v[144:147]\n"
"ds_read_b64_tr_b16 v[222:223], v234 offset:59392\n"
"v_and_b32 v75, s73, v75\n"
"v_mfma_f32_16x16x32_bf16 v[144:147], v[112:115], a[16:19], v[144:147]\n"
"v_add_f32 v6, v6, v249\n"
"v_add_f32 v7, v7, v75\n"
"v_mfma_f32_16x16x32_bf16 v[144:147], v[108:111], a[76:79], v[144:147]\n"
"v_lshlrev_b32 v250, 16, v76\n"
"v_and_b32 v76, s73, v76\n"
"v_mfma_f32_16x16x32_bf16 v[144:147], v[104:107], a[72:75], v[144:147]\n"
"buffer_load_dwordx4 v[196:199], v243, s[32:35], s19 offen offset:2048 sc1\n"
"v_mfma_f32_16x16x32_bf16 v[144:147], v[100:103], a[68:71], v[144:147]\n"
"v_cvt_pk_bf16_f32 v126, v128, v129\n"
"v_cvt_pk_bf16_f32 v127, v130, v131\n"
"v_mfma_f32_16x16x32_bf16 v[144:147], v[96:99], a[64:67], v[144:147]\n"
"v_add_f32 v8, v8, v250\n"
"v_add_f32 v9, v9, v76\n"
"v_mfma_f32_16x16x32_bf16 v[160:163], v[96:99], a[80:83], v[160:163]\n"
"v_lshlrev_b32 v251, 16, v77\n"
"v_and_b32 v77, s73, v77\n"
"v_mfma_f32_16x16x32_bf16 v[160:163], v[100:103], a[84:87], v[160:163]\n"
"buffer_store_dwordx4 v[124:127], v243, s[32:35], s16 offen sc1\n"
"v_add_f32 v10, v10, v251\n"
"v_mfma_f32_16x16x32_bf16 v[160:163], v[104:107], a[88:91], v[160:163]\n"
"v_add_f32 v11, v11, v77\n"
"v_lshlrev_b32 v246, 16, v78\n"
"v_mfma_f32_16x16x32_bf16 v[160:163], v[108:111], a[92:95], v[160:163]\n"
"v_and_b32 v78, s73, v78\n"
"v_add_f32 v12, v12, v246\n"
"v_mfma_f32_16x16x32_bf16 v[160:163], v[112:115], a[32:35], v[160:163]\n"
"v_add_f32 v13, v13, v78\n"
"v_lshlrev_b32 v247, 16, v79\n"
"v_mfma_f32_16x16x32_bf16 v[160:163], v[116:119], a[36:39], v[160:163]\n"
"v_cvt_pk_bf16_f32 v142, v144, v145\n"
"v_cvt_pk_bf16_f32 v143, v146, v147\n"
"v_mfma_f32_16x16x32_bf16 v[160:163], v[120:123], a[40:43], v[160:163]\n"
"s_add_u32 s40, s41, s53\n"
"v_and_b32 v79, s73, v79\n"
"v_add_f32 v14, v14, v247\n"
"v_mfma_f32_16x16x32_bf16 v[160:163], v[224:227], a[44:47], v[160:163]\n"
"buffer_load_dwordx4 v240, s[28:31], s40 offen offset:2048 lds\n"
"v_mfma_f32_16x16x32_bf16 v[176:179], v[224:227], a[228:231], v[176:179]\n"
"ds_read_b64_tr_b16 v[224:225], v235 offset:57344\n"
"v_add_f32 v15, v15, v79\n"
"v_mfma_f32_16x16x32_bf16 v[176:179], v[120:123], a[224:227], v[176:179]\n"
"s_waitcnt vmcnt(12)\n"
"ds_read_b64_tr_b16 v[120:121], v231 offset:57344\n"
"v_lshlrev_b32 v248, 16, v80\n"
"v_mfma_f32_16x16x32_bf16 v[176:179], v[116:119], a[212:215], v[176:179]\n"
"ds_read_b64_tr_b16 v[116:117], v235 offset:49152\n"
"v_and_b32 v80, s73, v80\n"
"v_mfma_f32_16x16x32_bf16 v[176:179], v[112:115], a[208:211], v[176:179]\n"
"ds_read_b64_tr_b16 v[112:113], v231 offset:49152\n"
"v_add_f32 v16, v16, v248\n"
"v_mfma_f32_16x16x32_bf16 v[176:179], v[108:111], a[236:239], v[176:179]\n"
"ds_read_b64_tr_b16 v[108:109], v235 offset:40960\n"
"v_add_f32 v17, v17, v80\n"
"v_mfma_f32_16x16x32_bf16 v[176:179], v[104:107], a[232:235], v[176:179]\n"
"ds_read_b64_tr_b16 v[104:105], v231 offset:40960\n"
"v_cvt_pk_bf16_f32 v158, v160, v161\n"
"v_mfma_f32_16x16x32_bf16 v[176:179], v[100:103], a[220:223], v[176:179]\n"
"ds_read_b64_tr_b16 v[100:101], v235 offset:32768\n"
"v_cvt_pk_bf16_f32 v159, v162, v163\n"
"v_mfma_f32_16x16x32_bf16 v[176:179], v[96:99], a[216:219], v[176:179]\n"
"s_waitcnt lgkmcnt(7)\n"
"buffer_load_dwordx4 v[200:203], v236, s[32:35], s20 offen sc1\n"
"v_mfma_f32_16x16x32_bf16 v[132:135], v[180:183], a[48:51], v[132:135]\n"
"ds_read_b64_tr_b16 v[96:97], v231 offset:32768\n"
"v_lshlrev_b32 v249, 16, v81\n"
"v_mfma_f32_16x16x32_bf16 v[132:135], v[184:187], a[52:55], v[132:135]\n"
"ds_read_b64_tr_b16 v[98:99], v231 offset:34816\n"
"v_and_b32 v81, s73, v81\n"
"v_mfma_f32_16x16x32_bf16 v[132:135], v[188:191], a[56:59], v[132:135]\n"
"s_add_u32 s40, s41, s54\n"
"ds_read_b64_tr_b16 v[102:103], v235 offset:34816\n"
"v_add_f32 v18, v18, v249\n"
"v_mfma_f32_16x16x32_bf16 v[132:135], v[192:195], a[60:63], v[132:135]\n"
"buffer_load_dwordx4 v240, s[28:31], s40 offen offset:3072 lds\n"
"v_mfma_f32_16x16x32_bf16 v[132:135], v[208:211], a[0:3], v[132:135]\n"
"s_add_u32 s70, s61, s47\n"
"ds_read_b64_tr_b16 v[106:107], v231 offset:43008\n"
"v_add_f32 v19, v19, v81\n"
"v_mfma_f32_16x16x32_bf16 v[132:135], v[212:215], a[4:7], v[132:135]\n"
"s_add_u32 s70, s70, 1\n"
"ds_read_b64_tr_b16 v[110:111], v235 offset:43008\n"
"v_lshlrev_b32 v250, 16, v82\n"
"v_mfma_f32_16x16x32_bf16 v[132:135], v[216:219], a[8:11], v[132:135]\n"
"s_and_b32 s70, s70, 7\n"
"ds_read_b64_tr_b16 v[114:115], v231 offset:51200\n"
"v_and_b32 v82, s73, v82\n"
"v_mfma_f32_16x16x32_bf16 v[132:135], v[220:223], a[12:15], v[132:135]\n"
"s_lshl_b32 s70, s70, 7\n"
"buffer_store_dwordx4 v[140:143], v243, s[32:35], s16 offen offset:2048 sc1\n"
"v_add_f32 v20, v20, v250\n"
"v_mfma_f32_16x16x32_bf16 v[148:151], v[220:223], a[28:31], v[148:151]\n"
"s_xor_b32 s71, s71, s75\n"
"ds_read_b64_tr_b16 v[118:119], v235 offset:51200\n"
"v_add_f32 v21, v21, v82\n"
"v_mfma_f32_16x16x32_bf16 v[148:151], v[216:219], a[24:27], v[148:151]\n"
"ds_read_b64_tr_b16 v[122:123], v231 offset:59392\n"
"v_lshlrev_b32 v251, 16, v83\n"
"v_mfma_f32_16x16x32_bf16 v[148:151], v[212:215], a[20:23], v[148:151]\n"
"ds_read_b64_tr_b16 v[226:227], v235 offset:59392\n"
"v_and_b32 v83, s73, v83\n"
"v_mfma_f32_16x16x32_bf16 v[148:151], v[208:211], a[16:19], v[148:151]\n"
"s_waitcnt vmcnt(14)\n"
"v_add_f32 v22, v22, v251\n"
"v_add_f32 v23, v23, v83\n"
"v_mfma_f32_16x16x32_bf16 v[148:151], v[192:195], a[76:79], v[148:151]\n"
"v_lshlrev_b32 v246, 16, v84\n"
"v_and_b32 v84, s73, v84\n"
"v_mfma_f32_16x16x32_bf16 v[148:151], v[188:191], a[72:75], v[148:151]\n"
"v_add_f32 v24, v24, v246\n"
"v_add_f32 v25, v25, v84\n"
"v_mfma_f32_16x16x32_bf16 v[148:151], v[184:187], a[68:71], v[148:151]\n"
"v_lshlrev_b32 v247, 16, v85\n"
"v_and_b32 v85, s73, v85\n"
"v_mfma_f32_16x16x32_bf16 v[148:151], v[180:183], a[64:67], v[148:151]\n"
"v_add_f32 v26, v26, v247\n"
"v_add_f32 v27, v27, v85\n"
"v_mfma_f32_16x16x32_bf16 v[164:167], v[180:183], a[80:83], v[164:167]\n"
"v_lshlrev_b32 v248, 16, v86\n"
"v_and_b32 v86, s73, v86\n"
"v_mfma_f32_16x16x32_bf16 v[164:167], v[184:187], a[84:87], v[164:167]\n"
"v_add_f32 v28, v28, v248\n"
"v_add_f32 v29, v29, v86\n"
"v_mfma_f32_16x16x32_bf16 v[164:167], v[188:191], a[88:91], v[164:167]\n"
"v_lshlrev_b32 v249, 16, v87\n"
"v_and_b32 v87, s73, v87\n"
"v_mfma_f32_16x16x32_bf16 v[164:167], v[192:195], a[92:95], v[164:167]\n"
"buffer_load_dwordx4 v[204:207], v237, s[32:35], s20 offen sc1\n"
"v_mfma_f32_16x16x32_bf16 v[164:167], v[208:211], a[32:35], v[164:167]\n"
"s_mul_i32 s20, s72, 286720\n"
"v_add_f32 v30, v30, v249\n"
"v_add_f32 v31, v31, v87\n"
"v_mfma_f32_16x16x32_bf16 v[164:167], v[212:215], a[36:39], v[164:167]\n"
"s_mul_i32 s18, s61, 28672\n"
"v_cvt_pk_bf16_f32 v172, v172, v173\n"
"v_cvt_pk_bf16_f32 v173, v174, v175\n"
"v_mfma_f32_16x16x32_bf16 v[164:167], v[216:219], a[40:43], v[164:167]\n"
"s_add_u32 s18, s18, s20\n"
"v_cvt_pk_bf16_f32 v174, v176, v177\n"
"v_cvt_pk_bf16_f32 v175, v178, v179\n"
"v_mfma_f32_16x16x32_bf16 v[164:167], v[220:223], a[44:47], v[164:167]\n"
"s_waitcnt lgkmcnt(0)\n"
"s_cmp_ge_u32 s47, 3\ns_cselect_b32 s18, s18, s77\n"
"v_cvt_pk_bf16_f32 v132, v132, v133\n"
"v_cvt_pk_bf16_f32 v133, v134, v135\n"
"v_mfma_f32_16x16x32_bf16 v[136:139], v[96:99], a[48:51], v[136:139]\n"
"s_add_u32 s19, s18, 4096\n"
"v_cvt_pk_bf16_f32 v148, v148, v149\n"
"v_mfma_f32_16x16x32_bf16 v[136:139], v[100:103], a[52:55], v[136:139]\n"
"s_barrier\n"
"s_cmp_ge_u32 s47, 2\ns_cselect_b32 s20, s20, s77\ns_cselect_b32 s69, 15, -1\n"
"v_mfma_f32_16x16x32_bf16 v[136:139], v[104:107], a[56:59], v[136:139]\n"
"s_add_u32 s21, s21, 1\n"
"v_lshlrev_b32_e64 v245, 2, s72\n"
"ds_read_b64_tr_b16 v[180:181], v228 offset:0\n"
"v_mfma_f32_16x16x32_bf16 v[136:139], v[108:111], a[60:63], v[136:139]\n"
"global_load_dword v244, v245, s[6:7] sc1\n"
"v_mfma_f32_16x16x32_bf16 v[136:139], v[112:115], a[0:3], v[136:139]\n"
"ds_read_b64_tr_b16 v[182:183], v228 offset:2048\n"
"v_cvt_pk_bf16_f32 v149, v150, v151\n"
"v_mfma_f32_16x16x32_bf16 v[136:139], v[116:119], a[4:7], v[136:139]\n"
"buffer_store_dwordx4 v[156:159], v243, s[32:35], s17 offen sc1\n"
"v_xor_b32 v230, s75, v230\n"
"v_mfma_f32_16x16x32_bf16 v[136:139], v[120:123], a[8:11], v[136:139]\n"
"ds_read_b64_tr_b16 v[184:185], v232 offset:0\n"
"v_xor_b32 v234, s75, v234\n"
"v_mfma_f32_16x16x32_bf16 v[136:139], v[224:227], a[12:15], v[136:139]\n"
"s_waitcnt vmcnt(14)\n"
"ds_read_b64_tr_b16 v[186:187], v232 offset:2048\n"
"v_lshlrev_b32 v250, 16, v88\n"
"v_mfma_f32_16x16x32_bf16 v[152:155], v[224:227], a[28:31], v[152:155]\n"
"ds_read_b64_tr_b16 v[188:189], v228 offset:8192\n"
"v_and_b32 v88, s73, v88\n"
"v_mfma_f32_16x16x32_bf16 v[152:155], v[120:123], a[24:27], v[152:155]\n"
"ds_read_b64_tr_b16 v[190:191], v228 offset:10240\n"
"v_add_f32 v32, v32, v250\n"
"v_mfma_f32_16x16x32_bf16 v[152:155], v[116:119], a[20:23], v[152:155]\n"
"ds_read_b64_tr_b16 v[192:193], v232 offset:8192\n"
"v_add_f32 v33, v33, v88\n"
"v_mfma_f32_16x16x32_bf16 v[152:155], v[112:115], a[16:19], v[152:155]\n"
"ds_read_b64_tr_b16 v[194:195], v232 offset:10240\n"
"v_lshlrev_b32 v251, 16, v89\n"
"v_mfma_f32_16x16x32_bf16 v[152:155], v[108:111], a[76:79], v[152:155]\n"
"ds_read_b64_tr_b16 v[208:209], v228 offset:16384\n"
"v_and_b32 v89, s73, v89\n"
"v_mfma_f32_16x16x32_bf16 v[152:155], v[104:107], a[72:75], v[152:155]\n"
"ds_read_b64_tr_b16 v[210:211], v228 offset:18432\n"
"v_add_f32 v34, v34, v251\n"
"v_mfma_f32_16x16x32_bf16 v[152:155], v[100:103], a[68:71], v[152:155]\n"
"ds_read_b64_tr_b16 v[212:213], v232 offset:16384\n"
"v_add_f32 v35, v35, v89\n"
"v_mfma_f32_16x16x32_bf16 v[152:155], v[96:99], a[64:67], v[152:155]\n"
"ds_read_b64_tr_b16 v[214:215], v232 offset:18432\n"
"v_lshlrev_b32 v246, 16, v90\n"
"v_mfma_f32_16x16x32_bf16 v[168:171], v[96:99], a[80:83], v[168:171]\n"
"ds_read_b64_tr_b16 v[216:217], v228 offset:24576\n"
"v_and_b32 v90, s73, v90\n"
"v_mfma_f32_16x16x32_bf16 v[168:171], v[100:103], a[84:87], v[168:171]\n"
"ds_read_b64_tr_b16 v[218:219], v228 offset:26624\n"
"v_add_f32 v36, v36, v246\n"
"v_mfma_f32_16x16x32_bf16 v[168:171], v[104:107], a[88:91], v[168:171]\n"
"ds_read_b64_tr_b16 v[220:221], v232 offset:24576\n"
"v_add_f32 v37, v37, v90\n"
"v_mfma_f32_16x16x32_bf16 v[168:171], v[108:111], a[92:95], v[168:171]\n"
"ds_read_b64_tr_b16 v[222:223], v232 offset:26624\n"
"v_lshlrev_b32 v247, 16, v91\n"
"v_mfma_f32_16x16x32_bf16 v[168:171], v[112:115], a[32:35], v[168:171]\n"
"ds_read_b64_tr_b16 v[96:97], v229 offset:0\n"
"v_and_b32 v91, s73, v91\n"
"v_mfma_f32_16x16x32_bf16 v[168:171], v[116:119], a[36:39], v[168:171]\n"
"ds_read_b64_tr_b16 v[98:99], v229 offset:2048\n"
"v_add_f32 v38, v38, v247\n"
"v_mfma_f32_16x16x32_bf16 v[168:171], v[120:123], a[40:43], v[168:171]\n"
"s_add_u32 s47, s47, 1\n"
"ds_read_b64_tr_b16 v[100:101], v233 offset:0\n"
"v_add_f32 v39, v39, v91\n"
"v_mfma_f32_16x16x32_bf16 v[168:171], v[224:227], a[44:47], v[168:171]\n"
"s_cmp_lg_u32 s47, 9\n"
"ds_read_b64_tr_b16 v[102:103], v233 offset:2048\n"
"s_cbranch_scc1 LGL_%=\n"
"s_waitcnt lgkmcnt(8)\n"
"v_mfma_f32_16x16x32_bf16 v[0:3], v[180:183], a[96:99], v[0:3]\n"
"ds_read_b64_tr_b16 v[104:105], v229 offset:8192\n"
"v_cvt_pk_bf16_f32 v134, v136, v137\n"
"v_mfma_f32_16x16x32_bf16 v[0:3], v[184:187], a[100:103], v[0:3]\n"
"ds_read_b64_tr_b16 v[106:107], v229 offset:10240\n"
"v_cvt_pk_bf16_f32 v135, v138, v139\n"
"v_mfma_f32_16x16x32_bf16 v[0:3], v[188:191], a[104:107], v[0:3]\n"
"ds_read_b64_tr_b16 v[108:109], v233 offset:8192\n"
"v_cvt_pk_bf16_f32 v150, v152, v153\n"
"v_mfma_f32_16x16x32_bf16 v[0:3], v[192:195], a[108:111], v[0:3]\n"
"buffer_store_dwordx4 v[172:175], v243, s[32:35], s17 offen offset:2048 sc1\n"
"v_cvt_pk_bf16_f32 v151, v154, v155\n"
"v_mfma_f32_16x16x32_bf16 v[0:3], v[208:211], a[144:147], v[0:3]\n"
"ds_read_b64_tr_b16 v[110:111], v233 offset:10240\n"
"v_cvt_pk_bf16_f32 v164, v164, v165\n"
"v_mfma_f32_16x16x32_bf16 v[0:3], v[212:215], a[148:151], v[0:3]\n"
"s_waitcnt lgkmcnt(6)\n"
"s_waitcnt vmcnt(12)\n"
"ds_read_b64_tr_b16 v[112:113], v229 offset:16384\n"
"v_cvt_pk_bf16_f32 v165, v166, v167\n"
"v_mfma_f32_16x16x32_bf16 v[0:3], v[216:219], a[152:155], v[0:3]\n"
"ds_read_b64_tr_b16 v[114:115], v229 offset:18432\n"
"v_cvt_pk_bf16_f32 v166, v168, v169\n"
"v_mfma_f32_16x16x32_bf16 v[0:3], v[220:223], a[156:159], v[0:3]\n"
"buffer_store_dwordx4 v[132:135], v243, s[32:35], s16 offen offset:1024 sc1\n"
"v_cvt_pk_bf16_f32 v167, v170, v171\n"
"v_mfma_f32_16x16x32_bf16 v[16:19], v[220:223], a[172:175], v[16:19]\n"
"ds_read_b64_tr_b16 v[116:117], v233 offset:16384\n"
"v_lshlrev_b32 v248, 16, v92\n"
"v_mfma_f32_16x16x32_bf16 v[16:19], v[216:219], a[168:171], v[16:19]\n"
"ds_read_b64_tr_b16 v[118:119], v233 offset:18432\n"
"v_add_f32 v40, v40, v248\n"
"v_mfma_f32_16x16x32_bf16 v[16:19], v[212:215], a[164:167], v[16:19]\n"
"ds_read_b64_tr_b16 v[120:121], v229 offset:24576\n"
"v_lshlrev_b32 v249, 16, v93\n"
"v_mfma_f32_16x16x32_bf16 v[16:19], v[208:211], a[160:163], v[16:19]\n"
"buffer_store_dwordx4 v[148:151], v243, s[32:35], s16 offen offset:3072 sc1\n"
"v_add_f32 v42, v42, v249\n"
"v_mfma_f32_16x16x32_bf16 v[16:19], v[192:195], a[124:127], v[16:19]\n"
"ds_read_b64_tr_b16 v[122:123], v229 offset:26624\n"
"v_and_b32 v92, s73, v92\n"
"v_mfma_f32_16x16x32_bf16 v[16:19], v[188:191], a[120:123], v[16:19]\n"
"ds_read_b64_tr_b16 v[224:225], v233 offset:24576\n"
"v_add_f32 v41, v41, v92\n"
"v_mfma_f32_16x16x32_bf16 v[16:19], v[184:187], a[116:119], v[16:19]\n"
"ds_read_b64_tr_b16 v[226:227], v233 offset:26624\n"
"v_and_b32 v93, s73, v93\n"
"v_mfma_f32_16x16x32_bf16 v[16:19], v[180:183], a[112:115], v[16:19]\n"
"buffer_store_dwordx4 v[164:167], v243, s[32:35], s17 offen offset:1024 sc1\n"
"v_add_f32 v43, v43, v93\n"
"v_mfma_f32_16x16x32_bf16 v[32:35], v[180:183], a[128:131], v[32:35]\n"
"v_lshlrev_b32 v250, 16, v94\n"
"v_and_b32 v94, s73, v94\n"
"v_mfma_f32_16x16x32_bf16 v[32:35], v[184:187], a[132:135], v[32:35]\n"
"v_add_f32 v45, v45, v94\n"
"v_lshlrev_b32 v251, 16, v95\n"
"v_mfma_f32_16x16x32_bf16 v[32:35], v[188:191], a[136:139], v[32:35]\n"
"v_and_b32 v95, s73, v95\n"
"v_add_f32 v47, v47, v95\n"
"v_mfma_f32_16x16x32_bf16 v[32:35], v[192:195], a[140:143], v[32:35]\n"
"v_add_f32 v44, v44, v250\n"
"v_add_f32 v46, v46, v251\n"
"v_mfma_f32_16x16x32_bf16 v[32:35], v[208:211], a[176:179], v[32:35]\n"
"v_add_u32 v241, 1, v241\n"
"v_xor_b32 v231, s75, v231\n"
"v_mfma_f32_16x16x32_bf16 v[32:35], v[212:215], a[180:183], v[32:35]\n"
"v_xor_b32 v235, s75, v235\n"
"v_bfe_u32 v160, v242, 6, 2\n"
"v_mfma_f32_16x16x32_bf16 v[32:35], v[216:219], a[184:187], v[32:35]\n"
"v_bfe_u32 v161, v242, 8, 2\n"
"v_xor_b32 v160, v160, v161\n"
"v_mfma_f32_16x16x32_bf16 v[32:35], v[220:223], a[188:191], v[32:35]\n"
"s_waitcnt vmcnt(12)\n"
"v_lshlrev_b32 v246, 16, v196\n"
"v_and_b32 v196, s73, v196\n"
"v_add_f32 v48, v48, v246\n"
"v_add_f32 v49, v49, v196\n"
"v_lshlrev_b32 v247, 16, v197\n"
"v_and_b32 v197, s73, v197\n"
"v_add_f32 v50, v50, v247\n"
"v_add_f32 v51, v51, v197\n"
"s_nop 1\n"
"v_mfma_f32_16x16x32_bf16 v[48:51], v[220:223], a[252:255], v[48:51]\n"
"ds_read_b64_tr_b16 v[220:221], v234 offset:24576\n"
"v_lshlrev_b32 v248, 16, v198\n"
"v_mfma_f32_16x16x32_bf16 v[48:51], v[216:219], a[248:251], v[48:51]\n"
"ds_read_b64_tr_b16 v[216:217], v230 offset:24576\n"
"v_and_b32 v198, s73, v198\n"
"v_mfma_f32_16x16x32_bf16 v[48:51], v[212:215], a[244:247], v[48:51]\n"
"ds_read_b64_tr_b16 v[212:213], v234 offset:16384\n"
"v_add_f32 v52, v52, v248\n"
"v_mfma_f32_16x16x32_bf16 v[48:51], v[208:211], a[240:243], v[48:51]\n"
"ds_read_b64_tr_b16 v[208:209], v230 offset:16384\n"
"v_add_f32 v53, v53, v198\n"
"v_mfma_f32_16x16x32_bf16 v[48:51], v[192:195], a[204:207], v[48:51]\n"
"ds_read_b64_tr_b16 v[192:193], v234 offset:8192\n"
"v_lshlrev_b32 v249, 16, v199\n"
"v_mfma_f32_16x16x32_bf16 v[48:51], v[188:191], a[200:203], v[48:51]\n"
"ds_read_b64_tr_b16 v[188:189], v230 offset:8192\n"
"v_and_b32 v199, s73, v199\n"
"v_mfma_f32_16x16x32_bf16 v[48:51], v[184:187], a[196:199], v[48:51]\n"
"ds_read_b64_tr_b16 v[184:185], v234 offset:0\n"
"v_add_f32 v54, v54, v249\n"
"v_mfma_f32_16x16x32_bf16 v[48:51], v[180:183], a[192:195], v[48:51]\n"
"ds_read_b64_tr_b16 v[180:181], v230 offset:0\n"
"v_add_f32 v55, v55, v199\n"
"v_mfma_f32_16x16x32_bf16 v[4:7], v[96:99], a[96:99], v[4:7]\n"
"s_waitcnt lgkmcnt(7)\n"
"ds_read_b64_tr_b16 v[182:183], v230 offset:2048\n"
"v_add_u32 v160, -1, v160\n"
"v_mfma_f32_16x16x32_bf16 v[4:7], v[100:103], a[100:103], v[4:7]\n"
"ds_read_b64_tr_b16 v[186:187], v234 offset:2048\n"
"v_ashrrev_i32 v160, 31, v160\n"
"v_mfma_f32_16x16x32_bf16 v[4:7], v[104:107], a[104:107], v[4:7]\n"
"ds_read_b64_tr_b16 v[190:191], v230 offset:10240\n"
"v_and_b32 v160, 0x3f80, v160\n"
"v_mfma_f32_16x16x32_bf16 v[4:7], v[108:111], a[108:111], v[4:7]\n"
"ds_read_b64_tr_b16 v[194:195], v234 offset:10240\n"
"v_and_b32 v161, 16, v242\n"
"v_mfma_f32_16x16x32_bf16 v[4:7], v[112:115], a[144:147], v[4:7]\n"
"s_waitcnt vmcnt(5)\n"
"ds_read_b64_tr_b16 v[210:211], v230 offset:18432\n"
"v_lshlrev_b32 v160, v161, v160\n"
"v_mfma_f32_16x16x32_bf16 v[4:7], v[116:119], a[148:151], v[4:7]\n"
"v_subrev_u32 v244, s21, v244\n"
"ds_read_b64_tr_b16 v[214:215], v234 offset:18432\n"
"v_mfma_f32_16x16x32_bf16 v[4:7], v[120:123], a[152:155], v[4:7]\n"
"v_cmp_lt_u32 vcc, s69, v244\n"
"ds_read_b64_tr_b16 v[218:219], v230 offset:26624\n"
"v_mfma_f32_16x16x32_bf16 v[4:7], v[224:227], a[156:159], v[4:7]\n"
"ds_read_b64_tr_b16 v[222:223], v234 offset:26624\n"
"s_cbranch_vccnz LGQ_%=\nLGQK_%=:\n"
"v_mfma_f32_16x16x32_bf16 v[20:23], v[224:227], a[172:175], v[20:23]\n"
"v_bfe_i32 v161, v242, 5, 1\n"
"v_and_b32 v163, v160, v161\n"
"v_mfma_f32_16x16x32_bf16 v[20:23], v[120:123], a[168:171], v[20:23]\n"
"v_lshlrev_b32 v250, 16, v200\n"
"v_and_b32 v200, s73, v200\n"
"v_mfma_f32_16x16x32_bf16 v[20:23], v[116:119], a[164:167], v[20:23]\n"
"v_add_f32 v57, v57, v200\n"
"v_lshlrev_b32 v251, 16, v201\n"
"v_mfma_f32_16x16x32_bf16 v[20:23], v[112:115], a[160:163], v[20:23]\n"
"buffer_load_dwordx4 v[72:75], v243, s[32:35], s18 offen sc1\n"
"v_mfma_f32_16x16x32_bf16 v[20:23], v[108:111], a[124:127], v[20:23]\n"
"v_and_b32 v201, s73, v201\n"
"v_add_f32 v59, v59, v201\n"
"v_mfma_f32_16x16x32_bf16 v[20:23], v[104:107], a[120:123], v[20:23]\n"
"v_lshlrev_b32 v246, 16, v202\n"
"v_and_b32 v202, s73, v202\n"
"v_mfma_f32_16x16x32_bf16 v[20:23], v[100:103], a[116:119], v[20:23]\n"
"v_add_f32 v61, v61, v202\n"
"v_lshlrev_b32 v247, 16, v203\n"
"v_mfma_f32_16x16x32_bf16 v[20:23], v[96:99], a[112:115], v[20:23]\n"
"buffer_load_dwordx4 v[80:83], v243, s[32:35], s18 offen offset:2048 sc1\n"
"v_mfma_f32_16x16x32_bf16 v[36:39], v[96:99], a[128:131], v[36:39]\n"
"v_and_b32 v203, s73, v203\n"
"v_add_f32 v63, v63, v203\n"
"v_mfma_f32_16x16x32_bf16 v[36:39], v[100:103], a[132:135], v[36:39]\n"
"v_add_f32 v56, v56, v250\n"
"v_add_f32 v58, v58, v251\n"
"v_mfma_f32_16x16x32_bf16 v[36:39], v[104:107], a[136:139], v[36:39]\n"
"v_add_f32 v60, v60, v246\n"
"v_add_f32 v62, v62, v247\n"
"v_mfma_f32_16x16x32_bf16 v[36:39], v[108:111], a[140:143], v[36:39]\n"
"buffer_load_dwordx4 v[88:91], v243, s[32:35], s19 offen sc1\n"
"v_mfma_f32_16x16x32_bf16 v[36:39], v[112:115], a[176:179], v[36:39]\n"
"v_xor_b32 v162, v160, v163\n"
"v_mov_b32 v164, 0\n"
"v_mfma_f32_16x16x32_bf16 v[36:39], v[116:119], a[180:183], v[36:39]\n"
"v_mov_b32 v165, 0\n"
"v_mov_b32 v160, 0\n"
"v_mfma_f32_16x16x32_bf16 v[36:39], v[120:123], a[184:187], v[36:39]\n"
"v_mov_b32 v161, 0\n"
"v_mfma_f32_16x16x32_bf16 v[36:39], v[224:227], a[188:191], v[36:39]\n"
"buffer_load_dwordx4 v[196:199], v243, s[32:35], s19 offen offset:2048 sc1\n"
"v_mfma_f32_16x16x32_bf16 v[52:55], v[224:227], a[252:255], v[52:55]\n"
"ds_read_b64_tr_b16 v[224:225], v235 offset:24576\n"
"v_mfma_f32_16x16x32_bf16 v[52:55], v[120:123], a[248:251], v[52:55]\n"
"ds_read_b64_tr_b16 v[120:121], v231 offset:24576\n"
"v_mfma_f32_16x16x32_bf16 v[52:55], v[116:119], a[244:247], v[52:55]\n"
"ds_read_b64_tr_b16 v[116:117], v235 offset:16384\n"
"v_mfma_f32_16x16x32_bf16 v[52:55], v[112:115], a[240:243], v[52:55]\n"
"buffer_load_dwordx4 v[76:79], v243, s[32:35], s18 offen offset:1024 sc1\n"
"v_mfma_f32_16x16x32_bf16 v[52:55], v[108:111], a[204:207], v[52:55]\n"
"ds_read_b64_tr_b16 v[108:109], v235 offset:8192\n"
"v_mfma_f32_16x16x32_bf16 v[52:55], v[104:107], a[200:203], v[52:55]\n"
"ds_read_b64_tr_b16 v[104:105], v231 offset:8192\n"
"v_lshlrev_b32 v248, 16, v204\n"
"v_mfma_f32_16x16x32_bf16 v[52:55], v[100:103], a[196:199], v[52:55]\n"
"ds_read_b64_tr_b16 v[100:101], v235 offset:0\n"
"v_and_b32 v204, s73, v204\n"
"v_mfma_f32_16x16x32_bf16 v[52:55], v[96:99], a[192:195], v[52:55]\n"
"s_waitcnt lgkmcnt(6)\n"
"buffer_load_dwordx4 v[84:87], v243, s[32:35], s18 offen offset:3072 sc1\n"
"v_mfma_f32_16x16x32_bf16 v[8:11], v[180:183], a[96:99], v[8:11]\n"
"ds_read_b64_tr_b16 v[96:97], v231 offset:0\n"
"v_add_f32 v65, v65, v204\n"
"v_mfma_f32_16x16x32_bf16 v[8:11], v[184:187], a[100:103], v[8:11]\n"
"ds_read_b64_tr_b16 v[98:99], v231 offset:2048\n"
"v_lshlrev_b32 v249, 16, v205\n"
"v_mfma_f32_16x16x32_bf16 v[8:11], v[188:191], a[104:107], v[8:11]\n"
"ds_read_b64_tr_b16 v[102:103], v235 offset:2048\n"
"v_and_b32 v205, s73, v205\n"
"v_mfma_f32_16x16x32_bf16 v[8:11], v[192:195], a[108:111], v[8:11]\n"
"buffer_load_dwordx4 v[92:95], v243, s[32:35], s19 offen offset:1024 sc1\n"
"v_mfma_f32_16x16x32_bf16 v[8:11], v[208:211], a[144:147], v[8:11]\n"
"ds_read_b64_tr_b16 v[106:107], v231 offset:10240\n"
"v_add_f32 v67, v67, v205\n"
"v_mfma_f32_16x16x32_bf16 v[8:11], v[212:215], a[148:151], v[8:11]\n"
"ds_read_b64_tr_b16 v[110:111], v235 offset:10240\n"
"v_lshlrev_b32 v250, 16, v206\n"
"v_mfma_f32_16x16x32_bf16 v[8:11], v[216:219], a[152:155], v[8:11]\n"
"ds_read_b64_tr_b16 v[112:113], v231 offset:16384\n"
"v_and_b32 v206, s73, v206\n"
"v_mfma_f32_16x16x32_bf16 v[8:11], v[220:223], a[156:159], v[8:11]\n"
"buffer_load_dwordx4 v[200:203], v236, s[32:35], s20 offen sc1\n"
"v_mfma_f32_16x16x32_bf16 v[24:27], v[220:223], a[172:175], v[24:27]\n"
"ds_read_b64_tr_b16 v[114:115], v231 offset:18432\n"
"v_add_f32 v69, v69, v206\n"
"v_mfma_f32_16x16x32_bf16 v[24:27], v[216:219], a[168:171], v[24:27]\n"
"ds_read_b64_tr_b16 v[118:119], v235 offset:18432\n"
"v_lshlrev_b32 v251, 16, v207\n"
"v_mfma_f32_16x16x32_bf16 v[24:27], v[212:215], a[164:167], v[24:27]\n"
"s_waitcnt vmcnt(8)\n"
"ds_read_b64_tr_b16 v[122:123], v231 offset:26624\n"
"v_and_b32 v207, s73, v207\n"
"v_add_f32 v71, v71, v207\n"
"v_mfma_f32_16x16x32_bf16 v[24:27], v[208:211], a[160:163], v[24:27]\n"
"s_barrier\n"
"buffer_load_dwordx4 v[204:207], v237, s[32:35], s20 offen sc1\n"
"v_mfma_f32_16x16x32_bf16 v[24:27], v[192:195], a[124:127], v[24:27]\n"
"s_sub_u32 s72, s72, 1\n"
"ds_read_b64_tr_b16 v[226:227], v235 offset:26624\n"
"s_cmp_eq_u32 s42, 0\n"
"v_mfma_f32_16x16x32_bf16 v[24:27], v[188:191], a[120:123], v[24:27]\n"
"s_cbranch_scc1 LGT_%=\nLGTK_%=:\n"
"s_and_b32 s72, s72, 7\n"
"v_mfma_f32_16x16x32_bf16 v[24:27], v[184:187], a[116:119], v[24:27]\n"
"s_mul_i32 s20, s72, 286720\n"
"v_add_f32 v64, v64, v248\n"
"v_add_f32 v66, v66, v249\n"
"v_mfma_f32_16x16x32_bf16 v[24:27], v[180:183], a[112:115], v[24:27]\n"
"s_mul_i32 s18, s61, 28672\n"
"v_add_f32 v68, v68, v250\n"
"v_add_f32 v70, v70, v251\n"
"v_mfma_f32_16x16x32_bf16 v[40:43], v[180:183], a[128:131], v[40:43]\n"
"s_add_u32 s18, s18, s20\n"
"v_mfma_f32_16x16x32_bf16 v[40:43], v[184:187], a[132:135], v[40:43]\n"
"s_cmp_ge_u32 s47, 3\ns_cselect_b32 s18, s18, s77\n"
"v_mfma_f32_16x16x32_bf16 v[40:43], v[188:191], a[136:139], v[40:43]\n"
"s_add_u32 s19, s18, 4096\n"
"v_mfma_f32_16x16x32_bf16 v[40:43], v[192:195], a[140:143], v[40:43]\n"
"s_cmp_ge_u32 s47, 2\ns_cselect_b32 s20, s20, s77\ns_cselect_b32 s69, 15, -1\n"
"v_mfma_f32_16x16x32_bf16 v[40:43], v[208:211], a[176:179], v[40:43]\n"
"s_add_u32 s21, s21, 1\n"
"v_mfma_f32_16x16x32_bf16 v[40:43], v[212:215], a[180:183], v[40:43]\n"
"s_mov_b32 s52, 40960\n"
"v_mfma_f32_16x16x32_bf16 v[40:43], v[216:219], a[184:187], v[40:43]\n"
"s_mov_b32 s53, 81920\n"
"v_mfma_f32_16x16x32_bf16 v[40:43], v[220:223], a[188:191], v[40:43]\n"
"s_waitcnt lgkmcnt(0)\n"
"v_mfma_f32_16x16x32_bf16 v[12:15], v[96:99], a[96:99], v[12:15]\n"
"v_mfma_f32_16x16x32_bf16 v[12:15], v[100:103], a[100:103], v[12:15]\n"
"s_barrier\n"
"v_mfma_f32_16x16x32_bf16 v[12:15], v[104:107], a[104:107], v[12:15]\n"
"ds_read_b64_tr_b16 v[180:181], v228 offset:32768\n"
"v_mfma_f32_16x16x32_bf16 v[12:15], v[108:111], a[108:111], v[12:15]\n"
"ds_read_b64_tr_b16 v[182:183], v228 offset:34816\n"
"v_mfma_f32_16x16x32_bf16 v[12:15], v[112:115], a[144:147], v[12:15]\n"
"ds_read_b64_tr_b16 v[184:185], v232 offset:32768\n"
"v_mfma_f32_16x16x32_bf16 v[12:15], v[116:119], a[148:151], v[12:15]\n"
"ds_read_b64_tr_b16 v[186:187], v232 offset:34816\n"
"v_mfma_f32_16x16x32_bf16 v[12:15], v[120:123], a[152:155], v[12:15]\n"
"ds_read_b64_tr_b16 v[188:189], v228 offset:40960\n"
"v_mfma_f32_16x16x32_bf16 v[12:15], v[224:227], a[156:159], v[12:15]\n"
"ds_read_b64_tr_b16 v[190:191], v228 offset:43008\n"
"v_mfma_f32_16x16x32_bf16 v[28:31], v[224:227], a[172:175], v[28:31]\n"
"ds_read_b64_tr_b16 v[192:193], v232 offset:40960\n"
"v_mfma_f32_16x16x32_bf16 v[28:31], v[120:123], a[168:171], v[28:31]\n"
"ds_read_b64_tr_b16 v[194:195], v232 offset:43008\n"
"v_mfma_f32_16x16x32_bf16 v[28:31], v[116:119], a[164:167], v[28:31]\n"
"ds_read_b64_tr_b16 v[208:209], v228 offset:49152\n"
"v_mfma_f32_16x16x32_bf16 v[28:31], v[112:115], a[160:163], v[28:31]\n"
"ds_read_b64_tr_b16 v[210:211], v228 offset:51200\n"
"v_mfma_f32_16x16x32_bf16 v[28:31], v[108:111], a[124:127], v[28:31]\n"
"ds_read_b64_tr_b16 v[212:213], v232 offset:49152\n"
"v_mfma_f32_16x16x32_bf16 v[28:31], v[104:107], a[120:123], v[28:31]\n"
"ds_read_b64_tr_b16 v[214:215], v232 offset:51200\n"
"v_mfma_f32_16x16x32_bf16 v[28:31], v[100:103], a[116:119], v[28:31]\n"
"ds_read_b64_tr_b16 v[216:217], v228 offset:57344\n"
"v_mfma_f32_16x16x32_bf16 v[28:31], v[96:99], a[112:115], v[28:31]\n"
"ds_read_b64_tr_b16 v[218:219], v228 offset:59392\n"
"v_mfma_f32_16x16x32_bf16 v[44:47], v[96:99], a[128:131], v[44:47]\n"
"ds_read_b64_tr_b16 v[220:221], v232 offset:57344\n"
"v_mfma_f32_16x16x32_bf16 v[44:47], v[100:103], a[132:135], v[44:47]\n"
"ds_read_b64_tr_b16 v[222:223], v232 offset:59392\n"
"v_mfma_f32_16x16x32_bf16 v[44:47], v[104:107], a[136:139], v[44:47]\n"
"ds_read_b64_tr_b16 v[96:97], v229 offset:32768\n"
"v_mfma_f32_16x16x32_bf16 v[44:47], v[108:111], a[140:143], v[44:47]\n"
"s_waitcnt vmcnt(8)\n"
"ds_read_b64_tr_b16 v[98:99], v229 offset:34816\n"
"v_lshlrev_b32 v246, 16, v72\n"
"v_mfma_f32_16x16x32_bf16 v[44:47], v[112:115], a[176:179], v[44:47]\n"
"v_lshlrev_b32_e64 v245, 2, s72\n"
"ds_read_b64_tr_b16 v[100:101], v233 offset:32768\n"
"v_and_b32 v72, s73, v72\n"
"v_mfma_f32_16x16x32_bf16 v[44:47], v[116:119], a[180:183], v[44:47]\n"
"global_load_dword v244, v245, s[6:7] sc1\n"
"v_mfma_f32_16x16x32_bf16 v[44:47], v[120:123], a[184:187], v[44:47]\n"
"ds_read_b64_tr_b16 v[102:103], v233 offset:34816\n"
"v_add_f32 v0, v0, v246\n"
"v_add_f32 v1, v1, v72\n"
"v_lshlrev_b32 v247, 16, v73\n"
"v_and_b32 v73, s73, v73\n"
"v_add_f32 v2, v2, v247\n"
"v_add_f32 v3, v3, v73\n"
"v_mfma_f32_16x16x32_bf16 v[44:47], v[224:227], a[188:191], v[44:47]\n"
"s_waitcnt lgkmcnt(6)\n"
"s_waitcnt vmcnt(8)\n"
"ds_read_b64_tr_b16 v[104:105], v229 offset:40960\n"
"v_lshlrev_b32 v248, 16, v74\n"
"v_mfma_f32_16x16x32_bf16 v[0:3], v[180:183], a[48:51], v[0:3]\n"
"ds_read_b64_tr_b16 v[106:107], v229 offset:43008\n"
"v_add_f32 v4, v4, v248\n"
"v_mfma_f32_16x16x32_bf16 v[0:3], v[184:187], a[52:55], v[0:3]\n"
"ds_read_b64_tr_b16 v[108:109], v233 offset:40960\n"
"v_lshlrev_b32 v249, 16, v75\n"
"v_mfma_f32_16x16x32_bf16 v[0:3], v[188:191], a[56:59], v[0:3]\n"
"ds_read_b64_tr_b16 v[110:111], v233 offset:43008\n"
"v_add_f32 v6, v6, v249\n"
"v_mfma_f32_16x16x32_bf16 v[0:3], v[192:195], a[60:63], v[0:3]\n"
"s_waitcnt vmcnt(7)\n"
"ds_read_b64_tr_b16 v[112:113], v229 offset:49152\n"
"v_lshlrev_b32 v248, 16, v80\n"
"v_mfma_f32_16x16x32_bf16 v[0:3], v[208:211], a[0:3], v[0:3]\n"
"ds_read_b64_tr_b16 v[114:115], v229 offset:51200\n"
"v_and_b32 v80, s73, v80\n"
"v_mfma_f32_16x16x32_bf16 v[0:3], v[212:215], a[4:7], v[0:3]\n"
"ds_read_b64_tr_b16 v[116:117], v233 offset:49152\n"
"v_add_f32 v16, v16, v248\n"
"v_mfma_f32_16x16x32_bf16 v[0:3], v[216:219], a[8:11], v[0:3]\n"
"s_waitcnt lgkmcnt(7)\n"
"ds_read_b64_tr_b16 v[118:119], v233 offset:51200\n"
"v_add_f32 v17, v17, v80\n"
"v_lshlrev_b32 v249, 16, v81\n"
"v_and_b32 v81, s73, v81\n"
"v_add_f32 v18, v18, v249\n"
"v_add_f32 v19, v19, v81\n"
"v_mfma_f32_16x16x32_bf16 v[0:3], v[220:223], a[12:15], v[0:3]\n"
"s_waitcnt vmcnt(6)\n"
"ds_read_b64_tr_b16 v[120:121], v229 offset:57344\n"
"v_lshlrev_b32 v250, 16, v82\n"
"v_mfma_f32_16x16x32_bf16 v[16:19], v[220:223], a[28:31], v[16:19]\n"
"ds_read_b64_tr_b16 v[122:123], v229 offset:59392\n"
"v_add_f32 v20, v20, v250\n"
"v_mfma_f32_16x16x32_bf16 v[16:19], v[216:219], a[24:27], v[16:19]\n"
"ds_read_b64_tr_b16 v[224:225], v233 offset:57344\n"
"v_lshlrev_b32 v251, 16, v83\n"
"v_mfma_f32_16x16x32_bf16 v[16:19], v[212:215], a[20:23], v[16:19]\n"
"ds_read_b64_tr_b16 v[226:227], v233 offset:59392\n"
"v_add_f32 v22, v22, v251\n"
"v_mfma_f32_16x16x32_bf16 v[16:19], v[208:211], a[16:19], v[16:19]\n"
"s_waitcnt vmcnt(5)\n"
"v_lshlrev_b32 v250, 16, v88\n"
"v_and_b32 v88, s73, v88\n"
"v_mfma_f32_16x16x32_bf16 v[16:19], v[192:195], a[76:79], v[16:19]\n"
"v_add_f32 v32, v32, v250\n"
"v_add_f32 v33, v33, v88\n"
"v_mfma_f32_16x16x32_bf16 v[16:19], v[188:191], a[72:75], v[16:19]\n"
"v_lshlrev_b32 v251, 16, v89\n"
"v_and_b32 v89, s73, v89\n"
"v_mfma_f32_16x16x32_bf16 v[16:19], v[184:187], a[68:71], v[16:19]\n"
"v_add_f32 v34, v34, v251\n"
"v_add_f32 v35, v35, v89\n"
"v_mfma_f32_16x16x32_bf16 v[16:19], v[180:183], a[64:67], v[16:19]\n"
"s_waitcnt vmcnt(4)\n"
"v_lshlrev_b32 v246, 16, v90\n"
"v_add_f32 v36, v36, v246\n"
"v_mfma_f32_16x16x32_bf16 v[32:35], v[180:183], a[80:83], v[32:35]\n"
"v_lshlrev_b32 v247, 16, v91\n"
"v_add_f32 v38, v38, v247\n"
"v_mfma_f32_16x16x32_bf16 v[32:35], v[184:187], a[84:87], v[32:35]\n"
"v_lshlrev_b32 v246, 16, v196\n"
"v_and_b32 v196, s73, v196\n"
"v_mfma_f32_16x16x32_bf16 v[32:35], v[188:191], a[88:91], v[32:35]\n"
"v_add_f32 v48, v48, v246\n"
"v_add_f32 v49, v49, v196\n"
"v_mfma_f32_16x16x32_bf16 v[32:35], v[192:195], a[92:95], v[32:35]\n"
"s_waitcnt vmcnt(3)\n"
"v_lshlrev_b32 v247, 16, v197\n"
"v_and_b32 v197, s73, v197\n"
"v_mfma_f32_16x16x32_bf16 v[32:35], v[208:211], a[32:35], v[32:35]\n"
"v_add_f32 v50, v50, v247\n"
"v_add_f32 v51, v51, v197\n"
"v_mfma_f32_16x16x32_bf16 v[32:35], v[212:215], a[36:39], v[32:35]\n"
"v_and_b32 v74, s73, v74\n"
"v_add_f32 v5, v5, v74\n"
"v_mfma_f32_16x16x32_bf16 v[32:35], v[216:219], a[40:43], v[32:35]\n"
"v_and_b32 v75, s73, v75\n"
"v_add_f32 v7, v7, v75\n"
"v_mfma_f32_16x16x32_bf16 v[32:35], v[220:223], a[44:47], v[32:35]\n"
"s_waitcnt vmcnt(2)\n"
"v_and_b32 v82, s73, v82\n"
"v_add_f32 v21, v21, v82\n"
"v_mfma_f32_16x16x32_bf16 v[48:51], v[220:223], a[228:231], v[48:51]\n"
"s_waitcnt vmcnt(0)\n"
"ds_read_b64_tr_b16 v[220:221], v234 offset:57344\n"
"v_and_b32 v83, s73, v83\n"
"v_mfma_f32_16x16x32_bf16 v[48:51], v[216:219], a[224:227], v[48:51]\n"
"v_subrev_u32 v244, s21, v244\n"
"ds_read_b64_tr_b16 v[216:217], v230 offset:57344\n"
"v_mfma_f32_16x16x32_bf16 v[48:51], v[212:215], a[212:215], v[48:51]\n"
"v_cmp_lt_u32 vcc, s69, v244\n"
"ds_read_b64_tr_b16 v[212:213], v234 offset:49152\n"
"v_mfma_f32_16x16x32_bf16 v[48:51], v[208:211], a[208:211], v[48:51]\n"
"ds_read_b64_tr_b16 v[208:209], v230 offset:49152\n"
"s_cbranch_vccnz LGR_%=\nLGRK_%=:\n"
"v_mfma_f32_16x16x32_bf16 v[48:51], v[192:195], a[236:239], v[48:51]\n"
"ds_read_b64_tr_b16 v[192:193], v234 offset:40960\n"
"s_cmp_eq_u32 s93, 0\ns_cbranch_scc1 LEP_%=\nLEPK_%=:\n"
"v_mfma_f32_16x16x32_bf16 v[48:51], v[188:191], a[232:235], v[48:51]\n"
"buffer_load_dwordx4 v[152:155], v236, s[32:35], s20 offen sc1\n"
"v_mfma_f32_16x16x32_bf16 v[48:51], v[184:187], a[220:223], v[48:51]\n"
"ds_read_b64_tr_b16 v[184:185], v234 offset:32768\n"
"v_add_f32 v23, v23, v83\n"
"v_mfma_f32_16x16x32_bf16 v[48:51], v[180:183], a[216:219], v[48:51]\n"
"ds_read_b64_tr_b16 v[180:181], v230 offset:32768\n"
"v_and_b32 v90, s73, v90\n"
"v_mfma_f32_16x16x32_bf16 v[4:7], v[96:99], a[48:51], v[4:7]\n"
"buffer_load_dwordx4 v[156:159], v237, s[32:35], s20 offen sc1\n"
"v_mfma_f32_16x16x32_bf16 v[4:7], v[100:103], a[52:55], v[4:7]\n"
"s_waitcnt lgkmcnt(5)\n"
"ds_read_b64_tr_b16 v[182:183], v230 offset:34816\n"
"v_add_f32 v37, v37, v90\n"
"v_mfma_f32_16x16x32_bf16 v[4:7], v[104:107], a[56:59], v[4:7]\n"
"ds_read_b64_tr_b16 v[186:187], v234 offset:34816\n"
"v_and_b32 v91, s73, v91\n"
"v_mfma_f32_16x16x32_bf16 v[4:7], v[108:111], a[60:63], v[4:7]\n"
"buffer_load_dwordx4 v[124:127], v243, s[32:35], s18 offen sc1\n"
"v_mfma_f32_16x16x32_bf16 v[4:7], v[112:115], a[0:3], v[4:7]\n"
"ds_read_b64_tr_b16 v[188:189], v230 offset:40960\n"
"v_add_f32 v39, v39, v91\n"
"v_mfma_f32_16x16x32_bf16 v[4:7], v[116:119], a[4:7], v[4:7]\n"
"ds_read_b64_tr_b16 v[190:191], v230 offset:43008\n"
"v_lshlrev_b32 v248, 16, v198\n"
"v_mfma_f32_16x16x32_bf16 v[4:7], v[120:123], a[8:11], v[4:7]\n"
"buffer_load_dwordx4 v[132:135], v243, s[32:35], s18 offen offset:2048 sc1\n"
"v_mfma_f32_16x16x32_bf16 v[4:7], v[224:227], a[12:15], v[4:7]\n"
"ds_read_b64_tr_b16 v[194:195], v234 offset:43008\n"
"v_and_b32 v198, s73, v198\n"
"v_mfma_f32_16x16x32_bf16 v[20:23], v[224:227], a[28:31], v[20:23]\n"
"ds_read_b64_tr_b16 v[210:211], v230 offset:51200\n"
"v_add_f32 v52, v52, v248\n"
"v_mfma_f32_16x16x32_bf16 v[20:23], v[120:123], a[24:27], v[20:23]\n"
"buffer_load_dwordx4 v[140:143], v243, s[32:35], s19 offen sc1\n"
"v_mfma_f32_16x16x32_bf16 v[20:23], v[116:119], a[20:23], v[20:23]\n"
"ds_read_b64_tr_b16 v[214:215], v234 offset:51200\n"
"v_add_f32 v53, v53, v198\n"
"v_mfma_f32_16x16x32_bf16 v[20:23], v[112:115], a[16:19], v[20:23]\n"
"ds_read_b64_tr_b16 v[218:219], v230 offset:59392\n"
"v_lshlrev_b32 v249, 16, v199\n"
"v_mfma_f32_16x16x32_bf16 v[20:23], v[108:111], a[76:79], v[20:23]\n"
"buffer_load_dwordx4 v[148:151], v243, s[32:35], s19 offen offset:2048 sc1\n"
"v_mfma_f32_16x16x32_bf16 v[20:23], v[104:107], a[72:75], v[20:23]\n"
"ds_read_b64_tr_b16 v[222:223], v234 offset:59392\n"
"v_and_b32 v199, s73, v199\n"
"v_mfma_f32_16x16x32_bf16 v[20:23], v[100:103], a[68:71], v[20:23]\n"
"v_add_f32 v54, v54, v249\n"
"v_add_f32 v55, v55, v199\n"
"v_mfma_f32_16x16x32_bf16 v[20:23], v[96:99], a[64:67], v[20:23]\n"
"buffer_load_dwordx4 v[128:131], v243, s[32:35], s18 offen offset:1024 sc1\n"
"v_mfma_f32_16x16x32_bf16 v[36:39], v[96:99], a[80:83], v[36:39]\n"
"v_lshlrev_b32 v250, 16, v76\n"
"v_and_b32 v76, s73, v76\n"
"v_mfma_f32_16x16x32_bf16 v[36:39], v[100:103], a[84:87], v[36:39]\n"
"v_add_f32 v8, v8, v250\n"
"v_add_f32 v9, v9, v76\n"
"v_mfma_f32_16x16x32_bf16 v[36:39], v[104:107], a[88:91], v[36:39]\n"
"buffer_load_dwordx4 v[136:139], v243, s[32:35], s18 offen offset:3072 sc1\n"
"v_mfma_f32_16x16x32_bf16 v[36:39], v[108:111], a[92:95], v[36:39]\n"
"v_lshlrev_b32 v251, 16, v77\n"
"v_and_b32 v77, s73, v77\n"
"v_mfma_f32_16x16x32_bf16 v[36:39], v[112:115], a[32:35], v[36:39]\n"
"v_add_f32 v10, v10, v251\n"
"v_add_f32 v11, v11, v77\n"
"v_mfma_f32_16x16x32_bf16 v[36:39], v[116:119], a[36:39], v[36:39]\n"
"buffer_load_dwordx4 v[144:147], v243, s[32:35], s19 offen offset:1024 sc1\n"
"v_mfma_f32_16x16x32_bf16 v[36:39], v[120:123], a[40:43], v[36:39]\n"
"v_lshlrev_b32 v246, 16, v78\n"
"v_add_f32 v12, v12, v246\n"
"v_mfma_f32_16x16x32_bf16 v[36:39], v[224:227], a[44:47], v[36:39]\n"
"v_lshlrev_b32 v247, 16, v79\n"
"v_add_f32 v14, v14, v247\n"
"v_mfma_f32_16x16x32_bf16 v[52:55], v[224:227], a[228:231], v[52:55]\n"
"ds_read_b64_tr_b16 v[224:225], v235 offset:57344\n"
"v_lshlrev_b32 v246, 16, v84\n"
"v_mfma_f32_16x16x32_bf16 v[52:55], v[120:123], a[224:227], v[52:55]\n"
"ds_read_b64_tr_b16 v[120:121], v231 offset:57344\n"
"v_and_b32 v84, s73, v84\n"
"v_mfma_f32_16x16x32_bf16 v[52:55], v[116:119], a[212:215], v[52:55]\n"
"ds_read_b64_tr_b16 v[116:117], v235 offset:49152\n"
"v_add_f32 v24, v24, v246\n"
"v_mfma_f32_16x16x32_bf16 v[52:55], v[112:115], a[208:211], v[52:55]\n"
"ds_read_b64_tr_b16 v[112:113], v231 offset:49152\n"
"v_add_f32 v25, v25, v84\n"
"v_mfma_f32_16x16x32_bf16 v[52:55], v[108:111], a[236:239], v[52:55]\n"
"ds_read_b64_tr_b16 v[108:109], v235 offset:40960\n"
"v_lshlrev_b32 v247, 16, v85\n"
"v_mfma_f32_16x16x32_bf16 v[52:55], v[104:107], a[232:235], v[52:55]\n"
"ds_read_b64_tr_b16 v[104:105], v231 offset:40960\n"
"v_and_b32 v85, s73, v85\n"
"v_mfma_f32_16x16x32_bf16 v[52:55], v[100:103], a[220:223], v[52:55]\n"
"ds_read_b64_tr_b16 v[100:101], v235 offset:32768\n"
"v_add_f32 v26, v26, v247\n"
"v_mfma_f32_16x16x32_bf16 v[52:55], v[96:99], a[216:219], v[52:55]\n"
"s_waitcnt lgkmcnt(7)\n"
"ds_read_b64_tr_b16 v[96:97], v231 offset:32768\n"
"v_add_f32 v27, v27, v85\n"
"v_mfma_f32_16x16x32_bf16 v[8:11], v[180:183], a[48:51], v[8:11]\n"
"ds_read_b64_tr_b16 v[98:99], v231 offset:34816\n"
"v_lshlrev_b32 v248, 16, v86\n"
"v_mfma_f32_16x16x32_bf16 v[8:11], v[184:187], a[52:55], v[8:11]\n"
"ds_read_b64_tr_b16 v[102:103], v235 offset:34816\n"
"v_add_f32 v28, v28, v248\n"
"v_mfma_f32_16x16x32_bf16 v[8:11], v[188:191], a[56:59], v[8:11]\n"
"ds_read_b64_tr_b16 v[106:107], v231 offset:43008\n"
"v_lshlrev_b32 v249, 16, v87\n"
"v_mfma_f32_16x16x32_bf16 v[8:11], v[192:195], a[60:63], v[8:11]\n"
"ds_read_b64_tr_b16 v[110:111], v235 offset:43008\n"
"v_add_f32 v30, v30, v249\n"
"v_mfma_f32_16x16x32_bf16 v[8:11], v[208:211], a[0:3], v[8:11]\n"
"ds_read_b64_tr_b16 v[114:115], v231 offset:51200\n"
"v_lshlrev_b32 v248, 16, v92\n"
"v_mfma_f32_16x16x32_bf16 v[8:11], v[212:215], a[4:7], v[8:11]\n"
"ds_read_b64_tr_b16 v[118:119], v235 offset:51200\n"
"v_and_b32 v92, s73, v92\n"
"v_mfma_f32_16x16x32_bf16 v[8:11], v[216:219], a[8:11], v[8:11]\n"
"ds_read_b64_tr_b16 v[122:123], v231 offset:59392\n"
"v_add_f32 v40, v40, v248\n"
"v_mfma_f32_16x16x32_bf16 v[8:11], v[220:223], a[12:15], v[8:11]\n"
"ds_read_b64_tr_b16 v[226:227], v235 offset:59392\n"
"v_add_f32 v41, v41, v92\n"
"v_mfma_f32_16x16x32_bf16 v[24:27], v[220:223], a[28:31], v[24:27]\n"
"v_lshlrev_b32 v249, 16, v93\n"
"v_and_b32 v93, s73, v93\n"
"v_mfma_f32_16x16x32_bf16 v[24:27], v[216:219], a[24:27], v[24:27]\n"
"v_add_f32 v42, v42, v249\n"
"v_add_f32 v43, v43, v93\n"
"v_mfma_f32_16x16x32_bf16 v[24:27], v[212:215], a[20:23], v[24:27]\n"
"v_and_b32 v78, s73, v78\n"
"v_add_f32 v13, v13, v78\n"
"v_mfma_f32_16x16x32_bf16 v[24:27], v[208:211], a[16:19], v[24:27]\n"
"v_and_b32 v79, s73, v79\n"
"v_add_f32 v15, v15, v79\n"
"v_mfma_f32_16x16x32_bf16 v[24:27], v[192:195], a[76:79], v[24:27]\n"
"v_lshlrev_b32 v250, 16, v94\n"
"v_add_f32 v44, v44, v250\n"
"v_mfma_f32_16x16x32_bf16 v[24:27], v[188:191], a[72:75], v[24:27]\n"
"v_lshlrev_b32 v251, 16, v95\n"
"v_add_f32 v46, v46, v251\n"
"v_mfma_f32_16x16x32_bf16 v[24:27], v[184:187], a[68:71], v[24:27]\n"
"v_lshlrev_b32 v250, 16, v200\n"
"v_and_b32 v200, s73, v200\n"
"v_mfma_f32_16x16x32_bf16 v[24:27], v[180:183], a[64:67], v[24:27]\n"
"v_add_f32 v56, v56, v250\n"
"v_add_f32 v57, v57, v200\n"
"v_mfma_f32_16x16x32_bf16 v[40:43], v[180:183], a[80:83], v[40:43]\n"
"v_lshlrev_b32 v251, 16, v201\n"
"v_and_b32 v201, s73, v201\n"
"v_mfma_f32_16x16x32_bf16 v[40:43], v[184:187], a[84:87], v[40:43]\n"
"v_add_f32 v58, v58, v251\n"
"v_add_f32 v59, v59, v201\n"
"v_mfma_f32_16x16x32_bf16 v[40:43], v[188:191], a[88:91], v[40:43]\n"
"v_lshlrev_b32 v246, 16, v202\n"
"v_and_b32 v202, s73, v202\n"
"v_mfma_f32_16x16x32_bf16 v[40:43], v[192:195], a[92:95], v[40:43]\n"
"v_add_f32 v60, v60, v246\n"
"v_add_f32 v61, v61, v202\n"
"v_mfma_f32_16x16x32_bf16 v[40:43], v[208:211], a[32:35], v[40:43]\n"
"v_lshlrev_b32 v247, 16, v203\n"
"v_and_b32 v203, s73, v203\n"
"v_mfma_f32_16x16x32_bf16 v[40:43], v[212:215], a[36:39], v[40:43]\n"
"v_add_f32 v62, v62, v247\n"
"v_add_f32 v63, v63, v203\n"
"v_mfma_f32_16x16x32_bf16 v[40:43], v[216:219], a[40:43], v[40:43]\n"
"v_lshlrev_b32 v248, 16, v204\n"
"v_and_b32 v204, s73, v204\n"
"v_mfma_f32_16x16x32_bf16 v[40:43], v[220:223], a[44:47], v[40:43]\n"
"s_waitcnt lgkmcnt(0)\n"
"v_add_f32 v64, v64, v248\n"
"v_add_f32 v65, v65, v204\n"
"v_mfma_f32_16x16x32_bf16 v[12:15], v[96:99], a[48:51], v[12:15]\n"
"v_lshlrev_b32 v249, 16, v205\n"
"v_and_b32 v205, s73, v205\n"
"v_mfma_f32_16x16x32_bf16 v[12:15], v[100:103], a[52:55], v[12:15]\n"
"v_add_f32 v66, v66, v249\n"
"v_add_f32 v67, v67, v205\n"
"v_mfma_f32_16x16x32_bf16 v[12:15], v[104:107], a[56:59], v[12:15]\n"
"v_lshlrev_b32 v250, 16, v206\n"
"v_and_b32 v206, s73, v206\n"
"v_mfma_f32_16x16x32_bf16 v[12:15], v[108:111], a[60:63], v[12:15]\n"
"v_add_f32 v68, v68, v250\n"
"v_add_f32 v69, v69, v206\n"
"v_mfma_f32_16x16x32_bf16 v[12:15], v[112:115], a[0:3], v[12:15]\n"
"v_lshlrev_b32 v251, 16, v207\n"
"v_and_b32 v207, s73, v207\n"
"v_mfma_f32_16x16x32_bf16 v[12:15], v[116:119], a[4:7], v[12:15]\n"
"v_add_f32 v70, v70, v251\n"
"v_add_f32 v71, v71, v207\n"
"v_mfma_f32_16x16x32_bf16 v[12:15], v[120:123], a[8:11], v[12:15]\n"
"v_and_b32 v86, s73, v86\n"
"v_add_f32 v29, v29, v86\n"
"v_mfma_f32_16x16x32_bf16 v[12:15], v[224:227], a[12:15], v[12:15]\n"
"v_and_b32 v87, s73, v87\n"
"s_waitcnt vmcnt(8)\n"
"v_mfma_f32_16x16x32_bf16 v[56:59], v[162:165], v[152:155], v[56:59]\n"
"v_add_f32 v31, v31, v87\n"
"v_and_b32 v94, s73, v94\n"
"v_mfma_f32_16x16x32_bf16 v[60:63], v[160:163], v[152:155], v[60:63]\n"
"v_add_f32 v45, v45, v94\n"
"s_waitcnt vmcnt(7)\n"
"v_mfma_f32_16x16x32_bf16 v[64:67], v[162:165], v[156:159], v[64:67]\n"
"v_and_b32 v95, s73, v95\n"
"v_add_f32 v47, v47, v95\n"
"v_mfma_f32_16x16x32_bf16 v[68:71], v[160:163], v[156:159], v[68:71]\n"
"v_mfma_f32_16x16x32_bf16 v[28:31], v[224:227], a[28:31], v[28:31]\n"
"v_mfma_f32_16x16x32_bf16 v[28:31], v[120:123], a[24:27], v[28:31]\n"
"v_mfma_f32_16x16x32_bf16 v[28:31], v[116:119], a[20:23], v[28:31]\n"
"v_cvt_pk_bf16_f32 v56, v56, v57\n"
"v_cvt_pk_bf16_f32 v57, v58, v59\n"
"v_mfma_f32_16x16x32_bf16 v[28:31], v[112:115], a[16:19], v[28:31]\n"
"v_cvt_pk_bf16_f32 v58, v60, v61\n"
"v_cvt_pk_bf16_f32 v59, v62, v63\n"
"v_mfma_f32_16x16x32_bf16 v[28:31], v[108:111], a[76:79], v[28:31]\n"
"v_permlane16_swap_b32 v56, v58\n"
"v_mfma_f32_16x16x32_bf16 v[28:31], v[104:107], a[72:75], v[28:31]\n"
"v_permlane16_swap_b32 v57, v59\n"
"v_mfma_f32_16x16x32_bf16 v[28:31], v[100:103], a[68:71], v[28:31]\n"
"buffer_store_dwordx4 v[56:59], v253, s[36:39], 0 offen\n"
"v_cvt_pk_bf16_f32 v64, v64, v65\n"
"v_mfma_f32_16x16x32_bf16 v[28:31], v[96:99], a[64:67], v[28:31]\n"
"v_cvt_pk_bf16_f32 v65, v66, v67\n"
"s_waitcnt vmcnt(7)\n"
"v_mfma_f32_16x16x32_bf16 v[0:3], v[162:165], v[124:127], v[0:3]\n"
"v_cvt_pk_bf16_f32 v66, v68, v69\n"
"v_cvt_pk_bf16_f32 v67, v70, v71\n"
"v_mfma_f32_16x16x32_bf16 v[4:7], v[160:163], v[124:127], v[4:7]\n"
"v_permlane16_swap_b32 v64, v66\n"
"s_waitcnt vmcnt(6)\n"
"v_mfma_f32_16x16x32_bf16 v[16:19], v[162:165], v[132:135], v[16:19]\n"
"v_permlane16_swap_b32 v65, v67\n"
"v_mfma_f32_16x16x32_bf16 v[20:23], v[160:163], v[132:135], v[20:23]\n"
"buffer_store_dwordx4 v[64:67], v254, s[36:39], 0 offen\n"
"v_mfma_f32_16x16x32_bf16 v[44:47], v[96:99], a[80:83], v[44:47]\n"
"v_mfma_f32_16x16x32_bf16 v[44:47], v[100:103], a[84:87], v[44:47]\n"
"v_mfma_f32_16x16x32_bf16 v[44:47], v[104:107], a[88:91], v[44:47]\n"
"v_cvt_pk_bf16_f32 v0, v0, v1\n"
"v_cvt_pk_bf16_f32 v1, v2, v3\n"
"v_mfma_f32_16x16x32_bf16 v[44:47], v[108:111], a[92:95], v[44:47]\n"
"v_cvt_pk_bf16_f32 v2, v4, v5\n"
"v_cvt_pk_bf16_f32 v3, v6, v7\n"
"v_mfma_f32_16x16x32_bf16 v[44:47], v[112:115], a[32:35], v[44:47]\n"
"v_permlane16_swap_b32 v0, v2\n"
"v_mfma_f32_16x16x32_bf16 v[44:47], v[116:119], a[36:39], v[44:47]\n"
"v_permlane16_swap_b32 v1, v3\n"
"v_mfma_f32_16x16x32_bf16 v[44:47], v[120:123], a[40:43], v[44:47]\n"
"buffer_store_dwordx4 v[0:3], v238, s[36:39], 0 offen\n"
"v_cvt_pk_bf16_f32 v16, v16, v17\n"
"v_mfma_f32_16x16x32_bf16 v[44:47], v[224:227], a[44:47], v[44:47]\n"
"v_cvt_pk_bf16_f32 v17, v18, v19\n"
"s_waitcnt vmcnt(7)\n"
"v_mfma_f32_16x16x32_bf16 v[32:35], v[162:165], v[140:143], v[32:35]\n"
"v_cvt_pk_bf16_f32 v18, v20, v21\n"
"v_cvt_pk_bf16_f32 v19, v22, v23\n"
"v_mfma_f32_16x16x32_bf16 v[36:39], v[160:163], v[140:143], v[36:39]\n"
"v_permlane16_swap_b32 v16, v18\n"
"s_waitcnt vmcnt(6)\n"
"v_mfma_f32_16x16x32_bf16 v[48:51], v[162:165], v[148:151], v[48:51]\n"
"v_permlane16_swap_b32 v17, v19\n"
"v_mfma_f32_16x16x32_bf16 v[52:55], v[160:163], v[148:151], v[52:55]\n"
"s_waitcnt vmcnt(5)\n"
"v_mfma_f32_16x16x32_bf16 v[8:11], v[162:165], v[128:131], v[8:11]\n"
"buffer_store_dwordx4 v[16:19], v238, s[36:39], s52 offen\n"
"v_mfma_f32_16x16x32_bf16 v[12:15], v[160:163], v[128:131], v[12:15]\n"
"s_waitcnt vmcnt(5)\n"
"v_mfma_f32_16x16x32_bf16 v[24:27], v[162:165], v[136:139], v[24:27]\n"
"v_cvt_pk_bf16_f32 v32, v32, v33\n"
"v_cvt_pk_bf16_f32 v33, v34, v35\n"
"v_mfma_f32_16x16x32_bf16 v[28:31], v[160:163], v[136:139], v[28:31]\n"
"v_cvt_pk_bf16_f32 v34, v36, v37\n"
"v_cvt_pk_bf16_f32 v35, v38, v39\n"
"s_waitcnt vmcnt(4)\n"
"v_mfma_f32_16x16x32_bf16 v[40:43], v[162:165], v[144:147], v[40:43]\n"
"v_permlane16_swap_b32 v32, v34\n"
"v_permlane16_swap_b32 v33, v35\n"
"v_cvt_pk_bf16_f32 v48, v48, v49\n"
"v_cvt_pk_bf16_f32 v49, v50, v51\n"
"v_mfma_f32_16x16x32_bf16 v[44:47], v[160:163], v[144:147], v[44:47]\n"
"buffer_store_dwordx4 v[32:35], v238, s[36:39], s53 offen\n"
"v_cvt_pk_bf16_f32 v50, v52, v53\n"
"v_cvt_pk_bf16_f32 v51, v54, v55\n"
"s_nop 0\n"
"v_permlane16_swap_b32 v48, v50\n"
"v_permlane16_swap_b32 v49, v51\n"
"buffer_store_dwordx4 v[48:51], v252, s[36:39], 0 offen\n"
"v_cvt_pk_bf16_f32 v8, v8, v9\n"
"v_cvt_pk_bf16_f32 v9, v10, v11\n"
"v_cvt_pk_bf16_f32 v10, v12, v13\n"
"v_cvt_pk_bf16_f32 v11, v14, v15\n"
"s_nop 0\n"
"v_permlane16_swap_b32 v8, v10\n"
"v_permlane16_swap_b32 v9, v11\n"
"buffer_store_dwordx4 v[8:11], v239, s[36:39], 0 offen\n"
"v_cvt_pk_bf16_f32 v24, v24, v25\n"
"v_cvt_pk_bf16_f32 v25, v26, v27\n"
"v_cvt_pk_bf16_f32 v26, v28, v29\n"
"v_cvt_pk_bf16_f32 v27, v30, v31\n"
"s_nop 0\n"
"v_permlane16_swap_b32 v24, v26\n"
"v_permlane16_swap_b32 v25, v27\n"
"buffer_store_dwordx4 v[24:27], v239, s[36:39], s52 offen\n"
"v_cvt_pk_bf16_f32 v40, v40, v41\n"
"v_cvt_pk_bf16_f32 v41, v42, v43\n"
"v_cvt_pk_bf16_f32 v42, v44, v45\n"
"v_cvt_pk_bf16_f32 v43, v46, v47\n"
"s_nop 0\n"
"v_permlane16_swap_b32 v40, v42\n"
"v_permlane16_swap_b32 v41, v43\n"
"buffer_store_dwordx4 v[40:43], v239, s[36:39], s53 offen\n"
"s_branch LFIN_%=\n"
"LSW_%=:\n"
"s_add_u32 s16, s8, 1024\n"
"s_addc_u32 s17, s9, 0\n"
"s_mov_b32 s18, 654336\n"
"s_add_u32 s20, s8, 1152\n"
"s_addc_u32 s21, s9, 0\n"
"s_mov_b32 s22, 654208\n"
"s_mov_b32 s56, 0\n"
"s_mov_b32 s55, 0\n"
"s_mov_b32 s58, 0\n"
"s_mov_b32 s59, 163840\n"
"s_mov_b32 s52, 9216\n"
"s_mov_b32 s53, 18432\n"
"s_mov_b32 s54, 27648\n"
"v_lshrrev_b32 v206, 7, v242\n"
"v_mul_u32_u24 v206, 1280, v206\n"
"v_and_b32 v240, 0xff, v240\n"
"v_add_u32 v240, v206, v240\n"
"s_branch LSWK_%=\n"
"LGP_%=:\n"
"s_mov_b32 s80, 0x40000\n"
"LGPa_%=:\n"
"s_sleep 4\n"
"global_load_dword v244, v245, s[6:7] sc1\n"
"s_sub_u32 s80, s80, 1\n"
"s_cmp_eq_u32 s80, 0\n"
"s_waitcnt vmcnt(0)\n"
"s_cbranch_scc1 LGPK_%=\n"
"v_subrev_u32 v244, s21, v244\n"
"v_cmp_lt_u32 vcc, s69, v244\n"
"s_nop 4\n"
"s_cbranch_vccnz LGPa_%=\n"
"s_branch LGPK_%=\n"
"LGQ_%=:\n"
"s_mov_b32 s80, 0x40000\n"
"LGQa_%=:\n"
"s_sleep 4\n"
"global_load_dword v244, v245, s[6:7] sc1\n"
"s_sub_u32 s80, s80, 1\n"
"s_cmp_eq_u32 s80, 0\n"
"s_waitcnt vmcnt(0)\n"
"s_cbranch_scc1 LGQK_%=\n"
"v_subrev_u32 v244, s21, v244\n"
"v_cmp_lt_u32 vcc, s69, v244\n"
"s_nop 4\n"
"s_cbranch_vccnz LGQa_%=\n"
"s_branch LGQK_%=\n"
"LGR_%=:\n"
"s_mov_b32 s80, 0x40000\n"
"LGRa_%=:\n"
"s_sleep 4\n"
"global_load_dword v244, v245, s[6:7] sc1\n"
"s_sub_u32 s80, s80, 1\n"
"s_cmp_eq_u32 s80, 0\n"
"s_waitcnt vmcnt(0)\n"
"s_cbranch_scc1 LGRK_%=\n"
"v_subrev_u32 v244, s21, v244\n"
"v_cmp_lt_u32 vcc, s69, v244\n"
"s_nop 4\n"
"s_cbranch_vccnz LGRa_%=\n"
"s_branch LGRK_%=\n"
"LGS_%=:\n"
"v_mov_b32 v255, s93\n"
"s_mov_b64 exec, 1\n"
"global_store_dword v255, v241, s[6:7] sc1\n"
"s_mov_b64 exec, -1\n"
"s_branch LGSK_%=\n"
"LGT_%=:\n"
"v_mov_b32 v255, s93\n"
"s_mov_b64 exec, 1\n"
"global_store_dword v255, v241, s[6:7] sc1\n"
"s_mov_b64 exec, -1\n"
"s_branch LGTK_%=\n"
"LEP_%=:\n"
"s_add_u32 s92, s92, 1\n"
"v_mov_b32 v244, s92\n"
"v_mov_b32 v255, 0\n"
"s_mov_b64 exec, 1\n"
"global_store_dword v255, v244, s[6:7] offset:128 sc1\n"
"s_mov_b64 exec, -1\n"
"s_branch LEPK_%=\n"
"LPCE0_%=:\n"
"s_memrealtime s[80:81]\n"
"s_sub_u32 s82, s67, s66\n"
"s_cmp_gt_i32 s82, 0\n"
"s_cselect_b32 s83, s67, s66\n"
"s_waitcnt lgkmcnt(0)\n"
"s_sub_u32 s82, s83, s80\n"
"s_cmp_gt_i32 s82, 3\n"
"s_cbranch_scc0 LPEE0_%=\n"
"LPDE0_%=:\n"
"s_sleep 1\n"
"s_memrealtime s[80:81]\n"
"s_waitcnt lgkmcnt(0)\n"
"s_sub_u32 s82, s83, s80\n"
"s_cmp_gt_i32 s82, 3\n"
"s_cbranch_scc1 LPDE0_%=\n"
"LPEE0_%=:\n"
"s_add_u32 s66, s66, 132\n"
"s_add_u32 s67, s80, 128\n"
"s_branch LPKE0_%=\n"
"LPCE1_%=:\n"
"s_memrealtime s[80:81]\n"
"s_sub_u32 s82, s67, s66\n"
"s_cmp_gt_i32 s82, 0\n"
"s_cselect_b32 s83, s67, s66\n"
"s_waitcnt lgkmcnt(0)\n"
"s_sub_u32 s82, s83, s80\n"
"s_cmp_gt_i32 s82, 3\n"
"s_cbranch_scc0 LPEE1_%=\n"
"LPDE1_%=:\n"
"s_sleep 1\n"
"s_memrealtime s[80:81]\n"
"s_waitcnt lgkmcnt(0)\n"
"s_sub_u32 s82, s83, s80\n"
"s_cmp_gt_i32 s82, 3\n"
"s_cbranch_scc1 LPDE1_%=\n"
"LPEE1_%=:\n"
"s_add_u32 s66, s66, 132\n"
"s_add_u32 s67, s80, 128\n"
"s_branch LPKE1_%=\n"
"LPCT0_%=:\n"
"s_memrealtime s[80:81]\n"
"s_sub_u32 s82, s67, s66\n"
"s_cmp_gt_i32 s82, 0\n"
"s_cselect_b32 s83, s67, s66\n"
"s_waitcnt lgkmcnt(0)\n"
"s_sub_u32 s82, s83, s80\n"
"s_cmp_gt_i32 s82, 3\n"
"s_cbranch_scc0 LPET0_%=\n"
"LPDT0_%=:\n"
"s_sleep 1\n"
"s_memrealtime s[80:81]\n"
"s_waitcnt lgkmcnt(0)\n"
"s_sub_u32 s82, s83, s80\n"
"s_cmp_gt_i32 s82, 3\n"
"s_cbranch_scc1 LPDT0_%=\n"
"LPET0_%=:\n"
"s_add_u32 s66, s66, 132\n"
"s_add_u32 s67, s80, 128\n"
"s_branch LPKT0_%=\n"
"LPCT1_%=:\n"
"s_memrealtime s[80:81]\n"
"s_sub_u32 s82, s67, s66\n"
"s_cmp_gt_i32 s82, 0\n"
"s_cselect_b32 s83, s67, s66\n"
"s_waitcnt lgkmcnt(0)\n"
"s_sub_u32 s82, s83, s80\n"
"s_cmp_gt_i32 s82, 3\n"
"s_cbranch_scc0 LPET1_%=\n"
"LPDT1_%=:\n"
"s_sleep 1\n"
"s_memrealtime s[80:81]\n"
"s_waitcnt lgkmcnt(0)\n"
"s_sub_u32 s82, s83, s80\n"
"s_cmp_gt_i32 s82, 3\n"
"s_cbranch_scc1 LPDT1_%=\n"
"LPET1_%=:\n"
"s_add_u32 s66, s66, 132\n"
"s_add_u32 s67, s80, 128\n"
"s_branch LPKT1_%=\n"
"LPCL0_%=:\n"
"s_memrealtime s[80:81]\n"
"s_sub_u32 s82, s67, s66\n"
"s_cmp_gt_i32 s82, 0\n"
"s_cselect_b32 s83, s67, s66\n"
"s_waitcnt lgkmcnt(0)\n"
"s_sub_u32 s82, s83, s80\n"
"s_cmp_gt_i32 s82, 3\n"
"s_cbranch_scc0 LPEL0_%=\n"
"LPDL0_%=:\n"
"s_sleep 1\n"
"s_memrealtime s[80:81]\n"
"s_waitcnt lgkmcnt(0)\n"
"s_sub_u32 s82, s83, s80\n"
"s_cmp_gt_i32 s82, 3\n"
"s_cbranch_scc1 LPDL0_%=\n"
"LPEL0_%=:\n"
"s_add_u32 s66, s66, 132\n"
"s_add_u32 s67, s80, 128\n"
"s_branch LPKL0_%=\n"
"LPCL1_%=:\n"
"s_memrealtime s[80:81]\n"
"s_sub_u32 s82, s67, s66\n"
"s_cmp_gt_i32 s82, 0\n"
"s_cselect_b32 s83, s67, s66\n"
"s_waitcnt lgkmcnt(0)\n"
"s_sub_u32 s82, s83, s80\n"
"s_cmp_gt_i32 s82, 3\n"
"s_cbranch_scc0 LPEL1_%=\n"
"LPDL1_%=:\n"
"s_sleep 1\n"
"s_memrealtime s[80:81]\n"
"s_waitcnt lgkmcnt(0)\n"
"s_sub_u32 s82, s83, s80\n"
"s_cmp_gt_i32 s82, 3\n"
"s_cbranch_scc1 LPDL1_%=\n"
"LPEL1_%=:\n"
"s_add_u32 s66, s66, 132\n"
"s_add_u32 s67, s80, 128\n"
"s_branch LPKL1_%=\n"
"LPCL2_%=:\n"
"s_memrealtime s[80:81]\n"
"s_sub_u32 s82, s67, s66\n"
"s_cmp_gt_i32 s82, 0\n"
"s_cselect_b32 s83, s67, s66\n"
"s_waitcnt lgkmcnt(0)\n"
"s_sub_u32 s82, s83, s80\n"
"s_cmp_gt_i32 s82, 3\n"
"s_cbranch_scc0 LPEL2_%=\n"
"LPDL2_%=:\n"
"s_sleep 1\n"
"s_memrealtime s[80:81]\n"
"s_waitcnt lgkmcnt(0)\n"
"s_sub_u32 s82, s83, s80\n"
"s_cmp_gt_i32 s82, 3\n"
"s_cbranch_scc1 LPDL2_%=\n"
"LPEL2_%=:\n"
"s_add_u32 s66, s66, 132\n"
"s_add_u32 s67, s80, 128\n"
"s_branch LPKL2_%=\n"
"LPCL3_%=:\n"
"s_memrealtime s[80:81]\n"
"s_sub_u32 s82, s67, s66\n"
"s_cmp_gt_i32 s82, 0\n"
"s_cselect_b32 s83, s67, s66\n"
"s_waitcnt lgkmcnt(0)\n"
"s_sub_u32 s82, s83, s80\n"
"s_cmp_gt_i32 s82, 3\n"
"s_cbranch_scc0 LPEL3_%=\n"
"LPDL3_%=:\n"
"s_sleep 1\n"
"s_memrealtime s[80:81]\n"
"s_waitcnt lgkmcnt(0)\n"
"s_sub_u32 s82, s83, s80\n"
"s_cmp_gt_i32 s82, 3\n"
"s_cbranch_scc1 LPDL3_%=\n"
"LPEL3_%=:\n"
"s_add_u32 s66, s66, 132\n"
"s_add_u32 s67, s80, 128\n"
"s_branch LPKL3_%=\n"
"LEND_%=:\n"
"LSLOW_%=:\n"
"s_waitcnt vmcnt(0) lgkmcnt(0)\n"
"s_nop 9\n"
"v_bfe_u32 v148, v242, 4, 4\n"
"v_bfe_u32 v149, v242, 8, 2\n"
"v_lshlrev_b32 v144, 8, v148\n"
"v_lshl_add_u32 v144, v149, 4, v144\n"
"v_add_u32 v144, s43, v144\n"
"v_xor_b32 v145, 64, v144\n"
"v_lshlrev_b32 v146, 1, v148\n"
"v_lshl_add_u32 v146, v149, 10, v146\n"
"v_add_u32 v146, s43, v146\n"
"v_xor_b32 v147, 64, v146\n"
"v_mov_b32 v152, 0xff7fffff\n"
"v_mov_b32 v155, 0\n"
"v_mov_b32 v0, 0\n"
"v_mov_b32 v1, 0\n"
"v_mov_b32 v2, 0\n"
"v_mov_b32 v3, 0\n"
"v_mov_b32 v4, 0\n"
"v_mov_b32 v5, 0\n"
"v_mov_b32 v6, 0\n"
"v_mov_b32 v7, 0\n"
"v_mov_b32 v8, 0\n"
"v_mov_b32 v9, 0\n"
"v_mov_b32 v10, 0\n"
"v_mov_b32 v11, 0\n"
"v_mov_b32 v12, 0\n"
"v_mov_b32 v13, 0\n"
"v_mov_b32 v14, 0\n"
"v_mov_b32 v15, 0\n"
"v_mov_b32 v16, 0\n"
"v_mov_b32 v17, 0\n"
"v_mov_b32 v18, 0\n"
"v_mov_b32 v19, 0\n"
"v_mov_b32 v20, 0\n"
"v_mov_b32 v21, 0\n"
"v_mov_b32 v22, 0\n"
"v_mov_b32 v23, 0\n"
"v_mov_b32 v24, 0\n"
"v_mov_b32 v25, 0\n"
"v_mov_b32 v26, 0\n"
"v_mov_b32 v27, 0\n"
"v_mov_b32 v28, 0\n"
"v_mov_b32 v29, 0\n"
"v_mov_b32 v30, 0\n"
"v_mov_b32 v31, 0\n"
"v_mov_b32 v156, 0xff7fffff\n"
"v_mov_b32 v159, 0\n"
"v_mov_b32 v32, 0\n"
"v_mov_b32 v33, 0\n"
"v_mov_b32 v34, 0\n"
"v_mov_b32 v35, 0\n"
"v_mov_b32 v36, 0\n"
"v_mov_b32 v37, 0\n"
"v_mov_b32 v38, 0\n"
"v_mov_b32 v39, 0\n"
"v_mov_b32 v40, 0\n"
"v_mov_b32 v41, 0\n"
"v_mov_b32 v42, 0\n"
"v_mov_b32 v43, 0\n"
"v_mov_b32 v44, 0\n"
"v_mov_b32 v45, 0\n"
"v_mov_b32 v46, 0\n"
"v_mov_b32 v47, 0\n"
"v_mov_b32 v48, 0\n"
"v_mov_b32 v49, 0\n"
"v_mov_b32 v50, 0\n"
"v_mov_b32 v51, 0\n"
"v_mov_b32 v52, 0\n"
"v_mov_b32 v53, 0\n"
"v_mov_b32 v54, 0\n"
"v_mov_b32 v55, 0\n"
"v_mov_b32 v56, 0\n"
"v_mov_b32 v57, 0\n"
"v_mov_b32 v58, 0\n"
"v_mov_b32 v59, 0\n"
"v_mov_b32 v60, 0\n"
"v_mov_b32 v61, 0\n"
"v_mov_b32 v62, 0\n"
"v_mov_b32 v63, 0\n"
"v_mov_b32 v160, 0xff7fffff\n"
"v_mov_b32 v163, 0\n"
"v_mov_b32 v64, 0\n"
"v_mov_b32 v65, 0\n"
"v_mov_b32 v66, 0\n"
"v_mov_b32 v67, 0\n"
"v_mov_b32 v68, 0\n"
"v_mov_b32 v69, 0\n"
"v_mov_b32 v70, 0\n"
"v_mov_b32 v71, 0\n"
"v_mov_b32 v72, 0\n"
"v_mov_b32 v73, 0\n"
"v_mov_b32 v74, 0\n"
"v_mov_b32 v75, 0\n"
"v_mov_b32 v76, 0\n"
"v_mov_b32 v77, 0\n"
"v_mov_b32 v78, 0\n"
"v_mov_b32 v79, 0\n"
"v_mov_b32 v80, 0\n"
"v_mov_b32 v81, 0\n"
"v_mov_b32 v82, 0\n"
"v_mov_b32 v83, 0\n"
"v_mov_b32 v84, 0\n"
"v_mov_b32 v85, 0\n"
"v_mov_b32 v86, 0\n"
"v_mov_b32 v87, 0\n"
"v_mov_b32 v88, 0\n"
"v_mov_b32 v89, 0\n"
"v_mov_b32 v90, 0\n"
"v_mov_b32 v91, 0\n"
"v_mov_b32 v92, 0\n"
"v_mov_b32 v93, 0\n"
"v_mov_b32 v94, 0\n"
"v_mov_b32 v95, 0\n"
"v_mov_b32 v164, 0xff7fffff\n"
"v_mov_b32 v167, 0\n"
"v_accvgpr_write_b32 a208, 0\n"
"v_accvgpr_write_b32 a209, 0\n"
"v_accvgpr_write_b32 a210, 0\n"
"v_accvgpr_write_b32 a211, 0\n"
"v_accvgpr_write_b32 a212, 0\n"
"v_accvgpr_write_b32 a213, 0\n"
"v_accvgpr_write_b32 a214, 0\n"
"v_accvgpr_write_b32 a215, 0\n"
"v_accvgpr_write_b32 a216, 0\n"
"v_accvgpr_write_b32 a217, 0\n"
"v_accvgpr_write_b32 a218, 0\n"
"v_accvgpr_write_b32 a219, 0\n"
"v_accvgpr_write_b32 a220, 0\n"
"v_accvgpr_write_b32 a221, 0\n"
"v_accvgpr_write_b32 a222, 0\n"
"v_accvgpr_write_b32 a223, 0\n"
"v_accvgpr_write_b32 a224, 0\n"
"v_accvgpr_write_b32 a225, 0\n"
"v_accvgpr_write_b32 a226, 0\n"
"v_accvgpr_write_b32 a227, 0\n"
"v_accvgpr_write_b32 a228, 0\n"
"v_accvgpr_write_b32 a229, 0\n"
"v_accvgpr_write_b32 a230, 0\n"
"v_accvgpr_write_b32 a231, 0\n"
"v_accvgpr_write_b32 a232, 0\n"
"v_accvgpr_write_b32 a233, 0\n"
"v_accvgpr_write_b32 a234, 0\n"
"v_accvgpr_write_b32 a235, 0\n"
"v_accvgpr_write_b32 a236, 0\n"
"v_accvgpr_write_b32 a237, 0\n"
"v_accvgpr_write_b32 a238, 0\n"
"v_accvgpr_write_b32 a239, 0\n"
"s_lshl_b32 s80, s47, 16\n"
"s_sub_u32 s80, s80, 261248\n"
"s_add_u32 s81, s80, 4096\n"
"s_mov_b32 s82, 0\n"
"LSLOOP_%=:\n"
"s_lshl_b32 s83, s82, 6\n"
"s_and_b32 s83, s83, 64\n"
"buffer_load_dwordx4 v[128:131], v144, s[84:87], s80 offen offset:0\n"
"buffer_load_dwordx4 v[132:135], v145, s[84:87], s80 offen offset:0\n"
"buffer_load_dwordx4 v[136:139], v144, s[84:87], s80 offen offset:128\n"
"buffer_load_dwordx4 v[140:143], v145, s[84:87], s80 offen offset:128\n"
"s_waitcnt vmcnt(0)\n"
"v_mfma_f32_16x16x32_bf16 v[96:99], v[128:131], a[0:3], 0\n"
"v_mfma_f32_16x16x32_bf16 v[96:99], v[132:135], a[4:7], v[96:99]\n"
"v_mfma_f32_16x16x32_bf16 v[96:99], v[136:139], a[8:11], v[96:99]\n"
"v_mfma_f32_16x16x32_bf16 v[96:99], v[140:143], a[12:15], v[96:99]\n"
"v_mfma_f32_16x16x32_bf16 v[104:107], v[128:131], a[16:19], 0\n"
"v_mfma_f32_16x16x32_bf16 v[104:107], v[132:135], a[20:23], v[104:107]\n"
"v_mfma_f32_16x16x32_bf16 v[104:107], v[136:139], a[24:27], v[104:107]\n"
"v_mfma_f32_16x16x32_bf16 v[104:107], v[140:143], a[28:31], v[104:107]\n"
"v_mfma_f32_16x16x32_bf16 v[112:115], v[128:131], a[32:35], 0\n"
"v_mfma_f32_16x16x32_bf16 v[112:115], v[132:135], a[36:39], v[112:115]\n"
"v_mfma_f32_16x16x32_bf16 v[112:115], v[136:139], a[40:43], v[112:115]\n"
"v_mfma_f32_16x16x32_bf16 v[112:115], v[140:143], a[44:47], v[112:115]\n"
"v_mfma_f32_16x16x32_bf16 v[120:123], v[128:131], a[192:195], 0\n"
"v_mfma_f32_16x16x32_bf16 v[120:123], v[132:135], a[196:199], v[120:123]\n"
"v_mfma_f32_16x16x32_bf16 v[120:123], v[136:139], a[200:203], v[120:123]\n"
"v_mfma_f32_16x16x32_bf16 v[120:123], v[140:143], a[204:207], v[120:123]\n"
"s_nop 7\n"
"buffer_load_dwordx4 v[128:131], v144, s[84:87], s81 offen offset:0\n"
"buffer_load_dwordx4 v[132:135], v145, s[84:87], s81 offen offset:0\n"
"buffer_load_dwordx4 v[136:139], v144, s[84:87], s81 offen offset:128\n"
"buffer_load_dwordx4 v[140:143], v145, s[84:87], s81 offen offset:128\n"
"s_waitcnt vmcnt(0)\n"
"v_mfma_f32_16x16x32_bf16 v[100:103], v[128:131], a[0:3], 0\n"
"v_mfma_f32_16x16x32_bf16 v[100:103], v[132:135], a[4:7], v[100:103]\n"
"v_mfma_f32_16x16x32_bf16 v[100:103], v[136:139], a[8:11], v[100:103]\n"
"v_mfma_f32_16x16x32_bf16 v[100:103], v[140:143], a[12:15], v[100:103]\n"
"v_mfma_f32_16x16x32_bf16 v[108:111], v[128:131], a[16:19], 0\n"
"v_mfma_f32_16x16x32_bf16 v[108:111], v[132:135], a[20:23], v[108:111]\n"
"v_mfma_f32_16x16x32_bf16 v[108:111], v[136:139], a[24:27], v[108:111]\n"
"v_mfma_f32_16x16x32_bf16 v[108:111], v[140:143], a[28:31], v[108:111]\n"
"v_mfma_f32_16x16x32_bf16 v[116:119], v[128:131], a[32:35], 0\n"
"v_mfma_f32_16x16x32_bf16 v[116:119], v[132:135], a[36:39], v[116:119]\n"
"v_mfma_f32_16x16x32_bf16 v[116:119], v[136:139], a[40:43], v[116:119]\n"
"v_mfma_f32_16x16x32_bf16 v[116:119], v[140:143], a[44:47], v[116:119]\n"
"v_mfma_f32_16x16x32_bf16 v[124:127], v[128:131], a[192:195], 0\n"
"v_mfma_f32_16x16x32_bf16 v[124:127], v[132:135], a[196:199], v[124:127]\n"
"v_mfma_f32_16x16x32_bf16 v[124:127], v[136:139], a[200:203], v[124:127]\n"
"v_mfma_f32_16x16x32_bf16 v[124:127], v[140:143], a[204:207], v[124:127]\n"
"s_nop 7\n"
"v_max3_f32 v153, v96, v97, v98\n"
"v_max3_f32 v153, v153, v99, v100\n"
"v_max3_f32 v153, v153, v101, v102\n"
"v_max3_f32 v153, v153, v103, v152\n"
"v_mov_b32 v154, v153\n"
"s_nop 1\n"
"v_permlane32_swap_b32 v153, v154\n"
"v_max_f32 v153, v153, v154\n"
"v_mov_b32 v154, v153\n"
"s_nop 1\n"
"v_permlane16_swap_b32 v153, v154\n"
"v_max_f32 v153, v153, v154\n"
"v_sub_f32 v154, v152, v153\n"
"v_exp_f32 v154, v154\n"
"v_mov_b32 v152, v153\n"
"v_sub_f32 v96, v96, v152\n"
"v_sub_f32 v97, v97, v152\n"
"v_sub_f32 v98, v98, v152\n"
"v_sub_f32 v99, v99, v152\n"
"v_sub_f32 v100, v100, v152\n"
"v_sub_f32 v101, v101, v152\n"
"v_sub_f32 v102, v102, v152\n"
"v_sub_f32 v103, v103, v152\n"
"v_exp_f32 v96, v96\n"
"v_exp_f32 v97, v97\n"
"v_exp_f32 v98, v98\n"
"v_exp_f32 v99, v99\n"
"v_exp_f32 v100, v100\n"
"v_exp_f32 v101, v101\n"
"v_exp_f32 v102, v102\n"
"v_exp_f32 v103, v103\n"
"v_mul_f32 v155, v155, v154\n"
"v_add_f32 v155, v155, v96\n"
"v_add_f32 v155, v155, v97\n"
"v_add_f32 v155, v155, v98\n"
"v_add_f32 v155, v155, v99\n"
"v_add_f32 v155, v155, v100\n"
"v_add_f32 v155, v155, v101\n"
"v_add_f32 v155, v155, v102\n"
"v_add_f32 v155, v155, v103\n"
"v_cvt_pk_bf16_f32 v96, v96, v97\n"
"v_cvt_pk_bf16_f32 v97, v98, v99\n"
"v_cvt_pk_bf16_f32 v98, v100, v101\n"
"v_cvt_pk_bf16_f32 v99, v102, v103\n"
"v_mov_b32_dpp v154, v154 row_ror:12 row_mask:0x2 bank_mask:0xf\n"
"s_nop 1\n"
"v_mov_b32_dpp v154, v154 row_ror:8 row_mask:0x4 bank_mask:0xf\n"
"s_nop 1\n"
"v_mov_b32_dpp v154, v154 row_ror:4 row_mask:0x8 bank_mask:0xf\n"
"s_nop 1\n"
"v_mul_f32_dpp v0, v154, v0 row_newbcast:0 row_mask:0xf bank_mask:0xf\n"
"v_mul_f32_dpp v1, v154, v1 row_newbcast:1 row_mask:0xf bank_mask:0xf\n"
"v_mul_f32_dpp v2, v154, v2 row_newbcast:2 row_mask:0xf bank_mask:0xf\n"
"v_mul_f32_dpp v3, v154, v3 row_newbcast:3 row_mask:0xf bank_mask:0xf\n"
"v_mul_f32_dpp v4, v154, v4 row_newbcast:0 row_mask:0xf bank_mask:0xf\n"
"v_mul_f32_dpp v5, v154, v5 row_newbcast:1 row_mask:0xf bank_mask:0xf\n"
"v_mul_f32_dpp v6, v154, v6 row_newbcast:2 row_mask:0xf bank_mask:0xf\n"
"v_mul_f32_dpp v7, v154, v7 row_newbcast:3 row_mask:0xf bank_mask:0xf\n"
"v_mul_f32_dpp v8, v154, v8 row_newbcast:0 row_mask:0xf bank_mask:0xf\n"
"v_mul_f32_dpp v9, v154, v9 row_newbcast:1 row_mask:0xf bank_mask:0xf\n"
"v_mul_f32_dpp v10, v154, v10 row_newbcast:2 row_mask:0xf bank_mask:0xf\n"
"v_mul_f32_dpp v11, v154, v11 row_newbcast:3 row_mask:0xf bank_mask:0xf\n"
"v_mul_f32_dpp v12, v154, v12 row_newbcast:0 row_mask:0xf bank_mask:0xf\n"
"v_mul_f32_dpp v13, v154, v13 row_newbcast:1 row_mask:0xf bank_mask:0xf\n"
"v_mul_f32_dpp v14, v154, v14 row_newbcast:2 row_mask:0xf bank_mask:0xf\n"
"v_mul_f32_dpp v15, v154, v15 row_newbcast:3 row_mask:0xf bank_mask:0xf\n"
"v_mul_f32_dpp v16, v154, v16 row_newbcast:0 row_mask:0xf bank_mask:0xf\n"
"v_mul_f32_dpp v17, v154, v17 row_newbcast:1 row_mask:0xf bank_mask:0xf\n"
"v_mul_f32_dpp v18, v154, v18 row_newbcast:2 row_mask:0xf bank_mask:0xf\n"
"v_mul_f32_dpp v19, v154, v19 row_newbcast:3 row_mask:0xf bank_mask:0xf\n"
"v_mul_f32_dpp v20, v154, v20 row_newbcast:0 row_mask:0xf bank_mask:0xf\n"
"v_mul_f32_dpp v21, v154, v21 row_newbcast:1 row_mask:0xf bank_mask:0xf\n"
"v_mul_f32_dpp v22, v154, v22 row_newbcast:2 row_mask:0xf bank_mask:0xf\n"
"v_mul_f32_dpp v23, v154, v23 row_newbcast:3 row_mask:0xf bank_mask:0xf\n"
"v_mul_f32_dpp v24, v154, v24 row_newbcast:0 row_mask:0xf bank_mask:0xf\n"
"v_mul_f32_dpp v25, v154, v25 row_newbcast:1 row_mask:0xf bank_mask:0xf\n"
"v_mul_f32_dpp v26, v154, v26 row_newbcast:2 row_mask:0xf bank_mask:0xf\n"
"v_mul_f32_dpp v27, v154, v27 row_newbcast:3 row_mask:0xf bank_mask:0xf\n"
"v_mul_f32_dpp v28, v154, v28 row_newbcast:0 row_mask:0xf bank_mask:0xf\n"
"v_mul_f32_dpp v29, v154, v29 row_newbcast:1 row_mask:0xf bank_mask:0xf\n"
"v_mul_f32_dpp v30, v154, v30 row_newbcast:2 row_mask:0xf bank_mask:0xf\n"
"v_mul_f32_dpp v31, v154, v31 row_newbcast:3 row_mask:0xf bank_mask:0xf\n"
"v_max3_f32 v157, v104, v105, v106\n"
"v_max3_f32 v157, v157, v107, v108\n"
"v_max3_f32 v157, v157, v109, v110\n"
"v_max3_f32 v157, v157, v111, v156\n"
"v_mov_b32 v158, v157\n"
"s_nop 1\n"
"v_permlane32_swap_b32 v157, v158\n"
"v_max_f32 v157, v157, v158\n"
"v_mov_b32 v158, v157\n"
"s_nop 1\n"
"v_permlane16_swap_b32 v157, v158\n"
"v_max_f32 v157, v157, v158\n"
"v_sub_f32 v158, v156, v157\n"
"v_exp_f32 v158, v158\n"
"v_mov_b32 v156, v157\n"
"v_sub_f32 v104, v104, v156\n"
"v_sub_f32 v105, v105, v156\n"
"v_sub_f32 v106, v106, v156\n"
"v_sub_f32 v107, v107, v156\n"
"v_sub_f32 v108, v108, v156\n"
"v_sub_f32 v109, v109, v156\n"
"v_sub_f32 v110, v110, v156\n"
"v_sub_f32 v111, v111, v156\n"
"v_exp_f32 v104, v104\n"
"v_exp_f32 v105, v105\n"
"v_exp_f32 v106, v106\n"
"v_exp_f32 v107, v107\n"
"v_exp_f32 v108, v108\n"
"v_exp_f32 v109, v109\n"
"v_exp_f32 v110, v110\n"
"v_exp_f32 v111, v111\n"
"v_mul_f32 v159, v159, v158\n"
"v_add_f32 v159, v159, v104\n"
"v_add_f32 v159, v159, v105\n"
"v_add_f32 v159, v159, v106\n"
"v_add_f32 v159, v159, v107\n"
"v_add_f32 v159, v159, v108\n"
"v_add_f32 v159, v159, v109\n"
"v_add_f32 v159, v159, v110\n"
"v_add_f32 v159, v159, v111\n"
"v_cvt_pk_bf16_f32 v104, v104, v105\n"
"v_cvt_pk_bf16_f32 v105, v106, v107\n"
"v_cvt_pk_bf16_f32 v106, v108, v109\n"
"v_cvt_pk_bf16_f32 v107, v110, v111\n"
"v_mov_b32_dpp v158, v158 row_ror:12 row_mask:0x2 bank_mask:0xf\n"
"s_nop 1\n"
"v_mov_b32_dpp v158, v158 row_ror:8 row_mask:0x4 bank_mask:0xf\n"
"s_nop 1\n"
"v_mov_b32_dpp v158, v158 row_ror:4 row_mask:0x8 bank_mask:0xf\n"
"s_nop 1\n"
"v_mul_f32_dpp v32, v158, v32 row_newbcast:0 row_mask:0xf bank_mask:0xf\n"
"v_mul_f32_dpp v33, v158, v33 row_newbcast:1 row_mask:0xf bank_mask:0xf\n"
"v_mul_f32_dpp v34, v158, v34 row_newbcast:2 row_mask:0xf bank_mask:0xf\n"
"v_mul_f32_dpp v35, v158, v35 row_newbcast:3 row_mask:0xf bank_mask:0xf\n"
"v_mul_f32_dpp v36, v158, v36 row_newbcast:0 row_mask:0xf bank_mask:0xf\n"
"v_mul_f32_dpp v37, v158, v37 row_newbcast:1 row_mask:0xf bank_mask:0xf\n"
"v_mul_f32_dpp v38, v158, v38 row_newbcast:2 row_mask:0xf bank_mask:0xf\n"
"v_mul_f32_dpp v39, v158, v39 row_newbcast:3 row_mask:0xf bank_mask:0xf\n"
"v_mul_f32_dpp v40, v158, v40 row_newbcast:0 row_mask:0xf bank_mask:0xf\n"
"v_mul_f32_dpp v41, v158, v41 row_newbcast:1 row_mask:0xf bank_mask:0xf\n"
"v_mul_f32_dpp v42, v158, v42 row_newbcast:2 row_mask:0xf bank_mask:0xf\n"
"v_mul_f32_dpp v43, v158, v43 row_newbcast:3 row_mask:0xf bank_mask:0xf\n"
"v_mul_f32_dpp v44, v158, v44 row_newbcast:0 row_mask:0xf bank_mask:0xf\n"
"v_mul_f32_dpp v45, v158, v45 row_newbcast:1 row_mask:0xf bank_mask:0xf\n"
"v_mul_f32_dpp v46, v158, v46 row_newbcast:2 row_mask:0xf bank_mask:0xf\n"
"v_mul_f32_dpp v47, v158, v47 row_newbcast:3 row_mask:0xf bank_mask:0xf\n"
"v_mul_f32_dpp v48, v158, v48 row_newbcast:0 row_mask:0xf bank_mask:0xf\n"
"v_mul_f32_dpp v49, v158, v49 row_newbcast:1 row_mask:0xf bank_mask:0xf\n"
"v_mul_f32_dpp v50, v158, v50 row_newbcast:2 row_mask:0xf bank_mask:0xf\n"
"v_mul_f32_dpp v51, v158, v51 row_newbcast:3 row_mask:0xf bank_mask:0xf\n"
"v_mul_f32_dpp v52, v158, v52 row_newbcast:0 row_mask:0xf bank_mask:0xf\n"
"v_mul_f32_dpp v53, v158, v53 row_newbcast:1 row_mask:0xf bank_mask:0xf\n"
"v_mul_f32_dpp v54, v158, v54 row_newbcast:2 row_mask:0xf bank_mask:0xf\n"
"v_mul_f32_dpp v55, v158, v55 row_newbcast:3 row_mask:0xf bank_mask:0xf\n"
"v_mul_f32_dpp v56, v158, v56 row_newbcast:0 row_mask:0xf bank_mask:0xf\n"
"v_mul_f32_dpp v57, v158, v57 row_newbcast:1 row_mask:0xf bank_mask:0xf\n"
"v_mul_f32_dpp v58, v158, v58 row_newbcast:2 row_mask:0xf bank_mask:0xf\n"
"v_mul_f32_dpp v59, v158, v59 row_newbcast:3 row_mask:0xf bank_mask:0xf\n"
"v_mul_f32_dpp v60, v158, v60 row_newbcast:0 row_mask:0xf bank_mask:0xf\n"
"v_mul_f32_dpp v61, v158, v61 row_newbcast:1 row_mask:0xf bank_mask:0xf\n"
"v_mul_f32_dpp v62, v158, v62 row_newbcast:2 row_mask:0xf bank_mask:0xf\n"
"v_mul_f32_dpp v63, v158, v63 row_newbcast:3 row_mask:0xf bank_mask:0xf\n"
"v_max3_f32 v161, v112, v113, v114\n"
"v_max3_f32 v161, v161, v115, v116\n"
"v_max3_f32 v161, v161, v117, v118\n"
"v_max3_f32 v161, v161, v119, v160\n"
"v_mov_b32 v162, v161\n"
"s_nop 1\n"
"v_permlane32_swap_b32 v161, v162\n"
"v_max_f32 v161, v161, v162\n"
"v_mov_b32 v162, v161\n"
"s_nop 1\n"
"v_permlane16_swap_b32 v161, v162\n"
"v_max_f32 v161, v161, v162\n"
"v_sub_f32 v162, v160, v161\n"
"v_exp_f32 v162, v162\n"
"v_mov_b32 v160, v161\n"
"v_sub_f32 v112, v112, v160\n"
"v_sub_f32 v113, v113, v160\n"
"v_sub_f32 v114, v114, v160\n"
"v_sub_f32 v115, v115, v160\n"
"v_sub_f32 v116, v116, v160\n"
"v_sub_f32 v117, v117, v160\n"
"v_sub_f32 v118, v118, v160\n"
"v_sub_f32 v119, v119, v160\n"
"v_exp_f32 v112, v112\n"
"v_exp_f32 v113, v113\n"
"v_exp_f32 v114, v114\n"
"v_exp_f32 v115, v115\n"
"v_exp_f32 v116, v116\n"
"v_exp_f32 v117, v117\n"
"v_exp_f32 v118, v118\n"
"v_exp_f32 v119, v119\n"
"v_mul_f32 v163, v163, v162\n"
"v_add_f32 v163, v163, v112\n"
"v_add_f32 v163, v163, v113\n"
"v_add_f32 v163, v163, v114\n"
"v_add_f32 v163, v163, v115\n"
"v_add_f32 v163, v163, v116\n"
"v_add_f32 v163, v163, v117\n"
"v_add_f32 v163, v163, v118\n"
"v_add_f32 v163, v163, v119\n"
"v_cvt_pk_bf16_f32 v112, v112, v113\n"
"v_cvt_pk_bf16_f32 v113, v114, v115\n"
"v_cvt_pk_bf16_f32 v114, v116, v117\n"
"v_cvt_pk_bf16_f32 v115, v118, v119\n"
"v_mov_b32_dpp v162, v162 row_ror:12 row_mask:0x2 bank_mask:0xf\n"
"s_nop 1\n"
"v_mov_b32_dpp v162, v162 row_ror:8 row_mask:0x4 bank_mask:0xf\n"
"s_nop 1\n"
"v_mov_b32_dpp v162, v162 row_ror:4 row_mask:0x8 bank_mask:0xf\n"
"s_nop 1\n"
"v_mul_f32_dpp v64, v162, v64 row_newbcast:0 row_mask:0xf bank_mask:0xf\n"
"v_mul_f32_dpp v65, v162, v65 row_newbcast:1 row_mask:0xf bank_mask:0xf\n"
"v_mul_f32_dpp v66, v162, v66 row_newbcast:2 row_mask:0xf bank_mask:0xf\n"
"v_mul_f32_dpp v67, v162, v67 row_newbcast:3 row_mask:0xf bank_mask:0xf\n"
"v_mul_f32_dpp v68, v162, v68 row_newbcast:0 row_mask:0xf bank_mask:0xf\n"
"v_mul_f32_dpp v69, v162, v69 row_newbcast:1 row_mask:0xf bank_mask:0xf\n"
"v_mul_f32_dpp v70, v162, v70 row_newbcast:2 row_mask:0xf bank_mask:0xf\n"
"v_mul_f32_dpp v71, v162, v71 row_newbcast:3 row_mask:0xf bank_mask:0xf\n"
"v_mul_f32_dpp v72, v162, v72 row_newbcast:0 row_mask:0xf bank_mask:0xf\n"
"v_mul_f32_dpp v73, v162, v73 row_newbcast:1 row_mask:0xf bank_mask:0xf\n"
"v_mul_f32_dpp v74, v162, v74 row_newbcast:2 row_mask:0xf bank_mask:0xf\n"
"v_mul_f32_dpp v75, v162, v75 row_newbcast:3 row_mask:0xf bank_mask:0xf\n"
"v_mul_f32_dpp v76, v162, v76 row_newbcast:0 row_mask:0xf bank_mask:0xf\n"
"v_mul_f32_dpp v77, v162, v77 row_newbcast:1 row_mask:0xf bank_mask:0xf\n"
"v_mul_f32_dpp v78, v162, v78 row_newbcast:2 row_mask:0xf bank_mask:0xf\n"
"v_mul_f32_dpp v79, v162, v79 row_newbcast:3 row_mask:0xf bank_mask:0xf\n"
"v_mul_f32_dpp v80, v162, v80 row_newbcast:0 row_mask:0xf bank_mask:0xf\n"
"v_mul_f32_dpp v81, v162, v81 row_newbcast:1 row_mask:0xf bank_mask:0xf\n"
"v_mul_f32_dpp v82, v162, v82 row_newbcast:2 row_mask:0xf bank_mask:0xf\n"
"v_mul_f32_dpp v83, v162, v83 row_newbcast:3 row_mask:0xf bank_mask:0xf\n"
"v_mul_f32_dpp v84, v162, v84 row_newbcast:0 row_mask:0xf bank_mask:0xf\n"
"v_mul_f32_dpp v85, v162, v85 row_newbcast:1 row_mask:0xf bank_mask:0xf\n"
"v_mul_f32_dpp v86, v162, v86 row_newbcast:2 row_mask:0xf bank_mask:0xf\n"
"v_mul_f32_dpp v87, v162, v87 row_newbcast:3 row_mask:0xf bank_mask:0xf\n"
"v_mul_f32_dpp v88, v162, v88 row_newbcast:0 row_mask:0xf bank_mask:0xf\n"
"v_mul_f32_dpp v89, v162, v89 row_newbcast:1 row_mask:0xf bank_mask:0xf\n"
"v_mul_f32_dpp v90, v162, v90 row_newbcast:2 row_mask:0xf bank_mask:0xf\n"
"v_mul_f32_dpp v91, v162, v91 row_newbcast:3 row_mask:0xf bank_mask:0xf\n"
"v_mul_f32_dpp v92, v162, v92 row_newbcast:0 row_mask:0xf bank_mask:0xf\n"
"v_mul_f32_dpp v93, v162, v93 row_newbcast:1 row_mask:0xf bank_mask:0xf\n"
"v_mul_f32_dpp v94, v162, v94 row_newbcast:2 row_mask:0xf bank_mask:0xf\n"
"v_mul_f32_dpp v95, v162, v95 row_newbcast:3 row_mask:0xf bank_mask:0xf\n"
"s_cmp_lg_u32 s83, s43\n"
"s_cbranch_scc1 LSK3A_%=\n"
"v_max3_f32 v165, v120, v121, v122\n"
"v_max3_f32 v165, v165, v123, v124\n"
"v_max3_f32 v165, v165, v125, v126\n"
"v_max3_f32 v165, v165, v127, v164\n"
"v_mov_b32 v166, v165\n"
"s_nop 1\n"
"v_permlane32_swap_b32 v165, v166\n"
"v_max_f32 v165, v165, v166\n"
"v_mov_b32 v166, v165\n"
"s_nop 1\n"
"v_permlane16_swap_b32 v165, v166\n"
"v_max_f32 v165, v165, v166\n"
"v_sub_f32 v166, v164, v165\n"
"v_exp_f32 v166, v166\n"
"v_mov_b32 v164, v165\n"
"v_sub_f32 v120, v120, v164\n"
"v_sub_f32 v121, v121, v164\n"
"v_sub_f32 v122, v122, v164\n"
"v_sub_f32 v123, v123, v164\n"
"v_sub_f32 v124, v124, v164\n"
"v_sub_f32 v125, v125, v164\n"
"v_sub_f32 v126, v126, v164\n"
"v_sub_f32 v127, v127, v164\n"
"v_exp_f32 v120, v120\n"
"v_exp_f32 v121, v121\n"
"v_exp_f32 v122, v122\n"
"v_exp_f32 v123, v123\n"
"v_exp_f32 v124, v124\n"
"v_exp_f32 v125, v125\n"
"v_exp_f32 v126, v126\n"
"v_exp_f32 v127, v127\n"
"v_mul_f32 v167, v167, v166\n"
"v_add_f32 v167, v167, v120\n"
"v_add_f32 v167, v167, v121\n"
"v_add_f32 v167, v167, v122\n"
"v_add_f32 v167, v167, v123\n"
"v_add_f32 v167, v167, v124\n"
"v_add_f32 v167, v167, v125\n"
"v_add_f32 v167, v167, v126\n"
"v_add_f32 v167, v167, v127\n"
"v_cvt_pk_bf16_f32 v120, v120, v121\n"
"v_cvt_pk_bf16_f32 v121, v122, v123\n"
"v_cvt_pk_bf16_f32 v122, v124, v125\n"
"v_cvt_pk_bf16_f32 v123, v126, v127\n"
"v_mov_b32_dpp v166, v166 row_ror:12 row_mask:0x2 bank_mask:0xf\n"
"s_nop 1\n"
"v_mov_b32_dpp v166, v166 row_ror:8 row_mask:0x4 bank_mask:0xf\n"
"s_nop 1\n"
"v_mov_b32_dpp v166, v166 row_ror:4 row_mask:0x8 bank_mask:0xf\n"
"v_accvgpr_read_b32 v124, a208\n"
"s_nop 1\n"
"v_mul_f32_dpp v124, v166, v124 row_newbcast:0 row_mask:0xf bank_mask:0xf\n"
"v_accvgpr_write_b32 a208, v124\n"
"v_accvgpr_read_b32 v125, a209\n"
"s_nop 1\n"
"v_mul_f32_dpp v125, v166, v125 row_newbcast:1 row_mask:0xf bank_mask:0xf\n"
"v_accvgpr_write_b32 a209, v125\n"
"v_accvgpr_read_b32 v126, a210\n"
"s_nop 1\n"
"v_mul_f32_dpp v126, v166, v126 row_newbcast:2 row_mask:0xf bank_mask:0xf\n"
"v_accvgpr_write_b32 a210, v126\n"
"v_accvgpr_read_b32 v127, a211\n"
"s_nop 1\n"
"v_mul_f32_dpp v127, v166, v127 row_newbcast:3 row_mask:0xf bank_mask:0xf\n"
"v_accvgpr_write_b32 a211, v127\n"
"v_accvgpr_read_b32 v124, a212\n"
"s_nop 1\n"
"v_mul_f32_dpp v124, v166, v124 row_newbcast:0 row_mask:0xf bank_mask:0xf\n"
"v_accvgpr_write_b32 a212, v124\n"
"v_accvgpr_read_b32 v125, a213\n"
"s_nop 1\n"
"v_mul_f32_dpp v125, v166, v125 row_newbcast:1 row_mask:0xf bank_mask:0xf\n"
"v_accvgpr_write_b32 a213, v125\n"
"v_accvgpr_read_b32 v126, a214\n"
"s_nop 1\n"
"v_mul_f32_dpp v126, v166, v126 row_newbcast:2 row_mask:0xf bank_mask:0xf\n"
"v_accvgpr_write_b32 a214, v126\n"
"v_accvgpr_read_b32 v127, a215\n"
"s_nop 1\n"
"v_mul_f32_dpp v127, v166, v127 row_newbcast:3 row_mask:0xf bank_mask:0xf\n"
"v_accvgpr_write_b32 a215, v127\n"
"v_accvgpr_read_b32 v124, a216\n"
"s_nop 1\n"
"v_mul_f32_dpp v124, v166, v124 row_newbcast:0 row_mask:0xf bank_mask:0xf\n"
"v_accvgpr_write_b32 a216, v124\n"
"v_accvgpr_read_b32 v125, a217\n"
"s_nop 1\n"
"v_mul_f32_dpp v125, v166, v125 row_newbcast:1 row_mask:0xf bank_mask:0xf\n"
"v_accvgpr_write_b32 a217, v125\n"
"v_accvgpr_read_b32 v126, a218\n"
"s_nop 1\n"
"v_mul_f32_dpp v126, v166, v126 row_newbcast:2 row_mask:0xf bank_mask:0xf\n"
"v_accvgpr_write_b32 a218, v126\n"
"v_accvgpr_read_b32 v127, a219\n"
"s_nop 1\n"
"v_mul_f32_dpp v127, v166, v127 row_newbcast:3 row_mask:0xf bank_mask:0xf\n"
"v_accvgpr_write_b32 a219, v127\n"
"v_accvgpr_read_b32 v124, a220\n"
"s_nop 1\n"
"v_mul_f32_dpp v124, v166, v124 row_newbcast:0 row_mask:0xf bank_mask:0xf\n"
"v_accvgpr_write_b32 a220, v124\n"
"v_accvgpr_read_b32 v125, a221\n"
"s_nop 1\n"
"v_mul_f32_dpp v125, v166, v125 row_newbcast:1 row_mask:0xf bank_mask:0xf\n"
"v_accvgpr_write_b32 a221, v125\n"
"v_accvgpr_read_b32 v126, a222\n"
"s_nop 1\n"
"v_mul_f32_dpp v126, v166, v126 row_newbcast:2 row_mask:0xf bank_mask:0xf\n"
"v_accvgpr_write_b32 a222, v126\n"
"v_accvgpr_read_b32 v127, a223\n"
"s_nop 1\n"
"v_mul_f32_dpp v127, v166, v127 row_newbcast:3 row_mask:0xf bank_mask:0xf\n"
"v_accvgpr_write_b32 a223, v127\n"
"v_accvgpr_read_b32 v124, a224\n"
"s_nop 1\n"
"v_mul_f32_dpp v124, v166, v124 row_newbcast:0 row_mask:0xf bank_mask:0xf\n"
"v_accvgpr_write_b32 a224, v124\n"
"v_accvgpr_read_b32 v125, a225\n"
"s_nop 1\n"
"v_mul_f32_dpp v125, v166, v125 row_newbcast:1 row_mask:0xf bank_mask:0xf\n"
"v_accvgpr_write_b32 a225, v125\n"
"v_accvgpr_read_b32 v126, a226\n"
"s_nop 1\n"
"v_mul_f32_dpp v126, v166, v126 row_newbcast:2 row_mask:0xf bank_mask:0xf\n"
"v_accvgpr_write_b32 a226, v126\n"
"v_accvgpr_read_b32 v127, a227\n"
"s_nop 1\n"
"v_mul_f32_dpp v127, v166, v127 row_newbcast:3 row_mask:0xf bank_mask:0xf\n"
"v_accvgpr_write_b32 a227, v127\n"
"v_accvgpr_read_b32 v124, a228\n"
"s_nop 1\n"
"v_mul_f32_dpp v124, v166, v124 row_newbcast:0 row_mask:0xf bank_mask:0xf\n"
"v_accvgpr_write_b32 a228, v124\n"
"v_accvgpr_read_b32 v125, a229\n"
"s_nop 1\n"
"v_mul_f32_dpp v125, v166, v125 row_newbcast:1 row_mask:0xf bank_mask:0xf\n"
"v_accvgpr_write_b32 a229, v125\n"
"v_accvgpr_read_b32 v126, a230\n"
"s_nop 1\n"
"v_mul_f32_dpp v126, v166, v126 row_newbcast:2 row_mask:0xf bank_mask:0xf\n"
"v_accvgpr_write_b32 a230, v126\n"
"v_accvgpr_read_b32 v127, a231\n"
"s_nop 1\n"
"v_mul_f32_dpp v127, v166, v127 row_newbcast:3 row_mask:0xf bank_mask:0xf\n"
"v_accvgpr_write_b32 a231, v127\n"
"v_accvgpr_read_b32 v124, a232\n"
"s_nop 1\n"
"v_mul_f32_dpp v124, v166, v124 row_newbcast:0 row_mask:0xf bank_mask:0xf\n"
"v_accvgpr_write_b32 a232, v124\n"
"v_accvgpr_read_b32 v125, a233\n"
"s_nop 1\n"
"v_mul_f32_dpp v125, v166, v125 row_newbcast:1 row_mask:0xf bank_mask:0xf\n"
"v_accvgpr_write_b32 a233, v125\n"
"v_accvgpr_read_b32 v126, a234\n"
"s_nop 1\n"
"v_mul_f32_dpp v126, v166, v126 row_newbcast:2 row_mask:0xf bank_mask:0xf\n"
"v_accvgpr_write_b32 a234, v126\n"
"v_accvgpr_read_b32 v127, a235\n"
"s_nop 1\n"
"v_mul_f32_dpp v127, v166, v127 row_newbcast:3 row_mask:0xf bank_mask:0xf\n"
"v_accvgpr_write_b32 a235, v127\n"
"v_accvgpr_read_b32 v124, a236\n"
"s_nop 1\n"
"v_mul_f32_dpp v124, v166, v124 row_newbcast:0 row_mask:0xf bank_mask:0xf\n"
"v_accvgpr_write_b32 a236, v124\n"
"v_accvgpr_read_b32 v125, a237\n"
"s_nop 1\n"
"v_mul_f32_dpp v125, v166, v125 row_newbcast:1 row_mask:0xf bank_mask:0xf\n"
"v_accvgpr_write_b32 a237, v125\n"
"v_accvgpr_read_b32 v126, a238\n"
"s_nop 1\n"
"v_mul_f32_dpp v126, v166, v126 row_newbcast:2 row_mask:0xf bank_mask:0xf\n"
"v_accvgpr_write_b32 a238, v126\n"
"v_accvgpr_read_b32 v127, a239\n"
"s_nop 1\n"
"v_mul_f32_dpp v127, v166, v127 row_newbcast:3 row_mask:0xf bank_mask:0xf\n"
"v_accvgpr_write_b32 a239, v127\n"
"LSK3A_%=:\n"
"s_nop 3\n"
"buffer_load_ushort v128, v146, s[88:91], s80 offen offset:0\n"
"buffer_load_ushort v129, v146, s[88:91], s80 offen offset:256\n"
"buffer_load_ushort v130, v146, s[88:91], s80 offen offset:512\n"
"buffer_load_ushort v131, v146, s[88:91], s80 offen offset:768\n"
"buffer_load_ushort v132, v146, s[88:91], s81 offen offset:0\n"
"buffer_load_ushort v133, v146, s[88:91], s81 offen offset:256\n"
"buffer_load_ushort v134, v146, s[88:91], s81 offen offset:512\n"
"buffer_load_ushort v135, v146, s[88:91], s81 offen offset:768\n"
"buffer_load_ushort v136, v146, s[88:91], s80 offen offset:32\n"
"buffer_load_ushort v137, v146, s[88:91], s80 offen offset:288\n"
"buffer_load_ushort v138, v146, s[88:91], s80 offen offset:544\n"
"buffer_load_ushort v139, v146, s[88:91], s80 offen offset:800\n"
"buffer_load_ushort v140, v146, s[88:91], s81 offen offset:32\n"
"buffer_load_ushort v141, v146, s[88:91], s81 offen offset:288\n"
"buffer_load_ushort v142, v146, s[88:91], s81 offen offset:544\n"
"buffer_load_ushort v143, v146, s[88:91], s81 offen offset:800\n"
"s_waitcnt vmcnt(0)\n"
"v_lshl_or_b32 v128, v129, 16, v128\n"
"v_lshl_or_b32 v129, v131, 16, v130\n"
"v_lshl_or_b32 v130, v133, 16, v132\n"
"v_lshl_or_b32 v131, v135, 16, v134\n"
"v_lshl_or_b32 v136, v137, 16, v136\n"
"v_lshl_or_b32 v137, v139, 16, v138\n"
"v_lshl_or_b32 v138, v141, 16, v140\n"
"v_lshl_or_b32 v139, v143, 16, v142\n"
"v_mfma_f32_16x16x32_bf16 v[0:3], v[96:99], v[128:131], v[0:3]\n"
"s_nop 0\n"
"v_mfma_f32_16x16x32_bf16 v[4:7], v[96:99], v[136:139], v[4:7]\n"
"v_mfma_f32_16x16x32_bf16 v[32:35], v[104:107], v[128:131], v[32:35]\n"
"v_mfma_f32_16x16x32_bf16 v[36:39], v[104:107], v[136:139], v[36:39]\n"
"v_mfma_f32_16x16x32_bf16 v[64:67], v[112:115], v[128:131], v[64:67]\n"
"v_mfma_f32_16x16x32_bf16 v[68:71], v[112:115], v[136:139], v[68:71]\n"
"s_cmp_lg_u32 s83, s43\n"
"s_cbranch_scc1 LSK3B0_%=\n"
"v_mfma_f32_16x16x32_bf16 a[208:211], v[120:123], v[128:131], a[208:211]\n"
"v_mfma_f32_16x16x32_bf16 a[212:215], v[120:123], v[136:139], a[212:215]\n"
"LSK3B0_%=:\n"
"s_nop 3\n"
"buffer_load_ushort v128, v147, s[88:91], s80 offen offset:0\n"
"buffer_load_ushort v129, v147, s[88:91], s80 offen offset:256\n"
"buffer_load_ushort v130, v147, s[88:91], s80 offen offset:512\n"
"buffer_load_ushort v131, v147, s[88:91], s80 offen offset:768\n"
"buffer_load_ushort v132, v147, s[88:91], s81 offen offset:0\n"
"buffer_load_ushort v133, v147, s[88:91], s81 offen offset:256\n"
"buffer_load_ushort v134, v147, s[88:91], s81 offen offset:512\n"
"buffer_load_ushort v135, v147, s[88:91], s81 offen offset:768\n"
"buffer_load_ushort v136, v147, s[88:91], s80 offen offset:32\n"
"buffer_load_ushort v137, v147, s[88:91], s80 offen offset:288\n"
"buffer_load_ushort v138, v147, s[88:91], s80 offen offset:544\n"
"buffer_load_ushort v139, v147, s[88:91], s80 offen offset:800\n"
"buffer_load_ushort v140, v147, s[88:91], s81 offen offset:32\n"
"buffer_load_ushort v141, v147, s[88:91], s81 offen offset:288\n"
"buffer_load_ushort v142, v147, s[88:91], s81 offen offset:544\n"
"buffer_load_ushort v143, v147, s[88:91], s81 offen offset:800\n"
"s_waitcnt vmcnt(0)\n"
"v_lshl_or_b32 v128, v129, 16, v128\n"
"v_lshl_or_b32 v129, v131, 16, v130\n"
"v_lshl_or_b32 v130, v133, 16, v132\n"
"v_lshl_or_b32 v131, v135, 16, v134\n"
"v_lshl_or_b32 v136, v137, 16, v136\n"
"v_lshl_or_b32 v137, v139, 16, v138\n"
"v_lshl_or_b32 v138, v141, 16, v140\n"
"v_lshl_or_b32 v139, v143, 16, v142\n"
"v_mfma_f32_16x16x32_bf16 v[8:11], v[96:99], v[128:131], v[8:11]\n"
"s_nop 0\n"
"v_mfma_f32_16x16x32_bf16 v[12:15], v[96:99], v[136:139], v[12:15]\n"
"v_mfma_f32_16x16x32_bf16 v[40:43], v[104:107], v[128:131], v[40:43]\n"
"v_mfma_f32_16x16x32_bf16 v[44:47], v[104:107], v[136:139], v[44:47]\n"
"v_mfma_f32_16x16x32_bf16 v[72:75], v[112:115], v[128:131], v[72:75]\n"
"v_mfma_f32_16x16x32_bf16 v[76:79], v[112:115], v[136:139], v[76:79]\n"
"s_cmp_lg_u32 s83, s43\n"
"s_cbranch_scc1 LSK3B1_%=\n"
"v_mfma_f32_16x16x32_bf16 a[216:219], v[120:123], v[128:131], a[216:219]\n"
"v_mfma_f32_16x16x32_bf16 a[220:223], v[120:123], v[136:139], a[220:223]\n"
"LSK3B1_%=:\n"
"s_nop 3\n"
"buffer_load_ushort v128, v146, s[88:91], s80 offen offset:128\n"
"buffer_load_ushort v129, v146, s[88:91], s80 offen offset:384\n"
"buffer_load_ushort v130, v146, s[88:91], s80 offen offset:640\n"
"buffer_load_ushort v131, v146, s[88:91], s80 offen offset:896\n"
"buffer_load_ushort v132, v146, s[88:91], s81 offen offset:128\n"
"buffer_load_ushort v133, v146, s[88:91], s81 offen offset:384\n"
"buffer_load_ushort v134, v146, s[88:91], s81 offen offset:640\n"
"buffer_load_ushort v135, v146, s[88:91], s81 offen offset:896\n"
"buffer_load_ushort v136, v146, s[88:91], s80 offen offset:160\n"
"buffer_load_ushort v137, v146, s[88:91], s80 offen offset:416\n"
"buffer_load_ushort v138, v146, s[88:91], s80 offen offset:672\n"
"buffer_load_ushort v139, v146, s[88:91], s80 offen offset:928\n"
"buffer_load_ushort v140, v146, s[88:91], s81 offen offset:160\n"
"buffer_load_ushort v141, v146, s[88:91], s81 offen offset:416\n"
"buffer_load_ushort v142, v146, s[88:91], s81 offen offset:672\n"
"buffer_load_ushort v143, v146, s[88:91], s81 offen offset:928\n"
"s_waitcnt vmcnt(0)\n"
"v_lshl_or_b32 v128, v129, 16, v128\n"
"v_lshl_or_b32 v129, v131, 16, v130\n"
"v_lshl_or_b32 v130, v133, 16, v132\n"
"v_lshl_or_b32 v131, v135, 16, v134\n"
"v_lshl_or_b32 v136, v137, 16, v136\n"
"v_lshl_or_b32 v137, v139, 16, v138\n"
"v_lshl_or_b32 v138, v141, 16, v140\n"
"v_lshl_or_b32 v139, v143, 16, v142\n"
"v_mfma_f32_16x16x32_bf16 v[16:19], v[96:99], v[128:131], v[16:19]\n"
"s_nop 0\n"
"v_mfma_f32_16x16x32_bf16 v[20:23], v[96:99], v[136:139], v[20:23]\n"
"v_mfma_f32_16x16x32_bf16 v[48:51], v[104:107], v[128:131], v[48:51]\n"
"v_mfma_f32_16x16x32_bf16 v[52:55], v[104:107], v[136:139], v[52:55]\n"
"v_mfma_f32_16x16x32_bf16 v[80:83], v[112:115], v[128:131], v[80:83]\n"
"v_mfma_f32_16x16x32_bf16 v[84:87], v[112:115], v[136:139], v[84:87]\n"
"s_cmp_lg_u32 s83, s43\n"
"s_cbranch_scc1 LSK3B2_%=\n"
"v_mfma_f32_16x16x32_bf16 a[224:227], v[120:123], v[128:131], a[224:227]\n"
"v_mfma_f32_16x16x32_bf16 a[228:231], v[120:123], v[136:139], a[228:231]\n"
"LSK3B2_%=:\n"
"s_nop 3\n"
"buffer_load_ushort v128, v147, s[88:91], s80 offen offset:128\n"
"buffer_load_ushort v129, v147, s[88:91], s80 offen offset:384\n"
"buffer_load_ushort v130, v147, s[88:91], s80 offen offset:640\n"
"buffer_load_ushort v131, v147, s[88:91], s80 offen offset:896\n"
"buffer_load_ushort v132, v147, s[88:91], s81 offen offset:128\n"
"buffer_load_ushort v133, v147, s[88:91], s81 offen offset:384\n"
"buffer_load_ushort v134, v147, s[88:91], s81 offen offset:640\n"
"buffer_load_ushort v135, v147, s[88:91], s81 offen offset:896\n"
"buffer_load_ushort v136, v147, s[88:91], s80 offen offset:160\n"
"buffer_load_ushort v137, v147, s[88:91], s80 offen offset:416\n"
"buffer_load_ushort v138, v147, s[88:91], s80 offen offset:672\n"
"buffer_load_ushort v139, v147, s[88:91], s80 offen offset:928\n"
"buffer_load_ushort v140, v147, s[88:91], s81 offen offset:160\n"
"buffer_load_ushort v141, v147, s[88:91], s81 offen offset:416\n"
"buffer_load_ushort v142, v147, s[88:91], s81 offen offset:672\n"
"buffer_load_ushort v143, v147, s[88:91], s81 offen offset:928\n"
"s_waitcnt vmcnt(0)\n"
"v_lshl_or_b32 v128, v129, 16, v128\n"
"v_lshl_or_b32 v129, v131, 16, v130\n"
"v_lshl_or_b32 v130, v133, 16, v132\n"
"v_lshl_or_b32 v131, v135, 16, v134\n"
"v_lshl_or_b32 v136, v137, 16, v136\n"
"v_lshl_or_b32 v137, v139, 16, v138\n"
"v_lshl_or_b32 v138, v141, 16, v140\n"
"v_lshl_or_b32 v139, v143, 16, v142\n"
"v_mfma_f32_16x16x32_bf16 v[24:27], v[96:99], v[128:131], v[24:27]\n"
"s_nop 0\n"
"v_mfma_f32_16x16x32_bf16 v[28:31], v[96:99], v[136:139], v[28:31]\n"
"v_mfma_f32_16x16x32_bf16 v[56:59], v[104:107], v[128:131], v[56:59]\n"
"v_mfma_f32_16x16x32_bf16 v[60:63], v[104:107], v[136:139], v[60:63]\n"
"v_mfma_f32_16x16x32_bf16 v[88:91], v[112:115], v[128:131], v[88:91]\n"
"v_mfma_f32_16x16x32_bf16 v[92:95], v[112:115], v[136:139], v[92:95]\n"
"s_cmp_lg_u32 s83, s43\n"
"s_cbranch_scc1 LSK3B3_%=\n"
"v_mfma_f32_16x16x32_bf16 a[232:235], v[120:123], v[128:131], a[232:235]\n"
"v_mfma_f32_16x16x32_bf16 a[236:239], v[120:123], v[136:139], a[236:239]\n"
"LSK3B3_%=:\n"
"s_nop 3\n"
"s_nop 9\n"
"s_add_u32 s80, s80, 8192\n"
"s_add_u32 s81, s81, 8192\n"
"s_add_u32 s82, s82, 1\n"
"s_cmp_lt_u32 s82, 32\n"
"s_cbranch_scc1 LSLOOP_%=\n"
"v_mov_b32 v154, v155\n"
"s_nop 1\n"
"v_permlane32_swap_b32 v155, v154\n"
"v_add_f32 v155, v155, v154\n"
"v_mov_b32 v154, v155\n"
"s_nop 1\n"
"v_permlane16_swap_b32 v155, v154\n"
"v_add_f32 v155, v155, v154\n"
"v_mov_b32 v196, v155\n"
"v_mov_b32 v158, v159\n"
"s_nop 1\n"
"v_permlane32_swap_b32 v159, v158\n"
"v_add_f32 v159, v159, v158\n"
"v_mov_b32 v158, v159\n"
"s_nop 1\n"
"v_permlane16_swap_b32 v159, v158\n"
"v_add_f32 v159, v159, v158\n"
"v_mov_b32 v198, v159\n"
"v_mov_b32 v162, v163\n"
"s_nop 1\n"
"v_permlane32_swap_b32 v163, v162\n"
"v_add_f32 v163, v163, v162\n"
"v_mov_b32 v162, v163\n"
"s_nop 1\n"
"v_permlane16_swap_b32 v163, v162\n"
"v_add_f32 v163, v163, v162\n"
"v_mov_b32 v200, v163\n"
"v_mov_b32 v166, v167\n"
"s_nop 1\n"
"v_permlane32_swap_b32 v167, v166\n"
"v_add_f32 v167, v167, v166\n"
"v_mov_b32 v166, v167\n"
"s_nop 1\n"
"v_permlane16_swap_b32 v167, v166\n"
"v_add_f32 v167, v167, v166\n"
"v_mov_b32 v202, v167\n"
"v_sub_f32 v192, 0, v164\n"
"v_mov_b32 v96, 0\n"
"v_mov_b32 v97, 0\n"
"v_mov_b32 v98, 0\n"
"v_mov_b32 v99, 0\n"
"v_mov_b32 v100, 0\n"
"v_mov_b32 v101, 0\n"
"v_mov_b32 v102, 0\n"
"v_mov_b32 v103, 0\n"
"v_mov_b32 v104, 0\n"
"v_mov_b32 v105, 0\n"
"v_mov_b32 v106, 0\n"
"v_mov_b32 v107, 0\n"
"v_mov_b32 v108, 0\n"
"v_mov_b32 v109, 0\n"
"v_mov_b32 v110, 0\n"
"v_mov_b32 v111, 0\n"
"v_mov_b32 v112, 0\n"
"v_mov_b32 v113, 0\n"
"v_mov_b32 v114, 0\n"
"v_mov_b32 v115, 0\n"
"v_mov_b32 v116, 0\n"
"v_mov_b32 v117, 0\n"
"v_mov_b32 v118, 0\n"
"v_mov_b32 v119, 0\n"
"v_mov_b32 v120, 0\n"
"v_mov_b32 v121, 0\n"
"v_mov_b32 v122, 0\n"
"v_mov_b32 v123, 0\n"
"v_mov_b32 v204, 0\n"
"v_mov_b32 v205, 0\n"
"s_nop 3\n"
"s_cmp_eq_u32 s47, 4\n"
"s_cbranch_scc1 LBACK0_%=\n"
"s_cmp_eq_u32 s47, 8\n"
"s_cbranch_scc1 LBACK1_%=\n"
"s_cmp_eq_u32 s47, 12\n"
"s_cbranch_scc1 LBACK2_%=\n"
"s_branch LBACK3_%=\n"
"LFIN_%=:\n"
    :: [kalo]"s"((unsigned)ka), [kahi]"s"((unsigned)(ka >> 32)), [wg]"s"(blockIdx.x), [wid]"s"(wid)
    : "v0","v1","v2","v3","v4","v5","v6","v7","v8","v9","v10","v11","v12","v13","v14","v15","v16","v17","v18","v19","v20","v21","v22","v23","v24","v25","v26","v27","v28","v29","v30","v31","v32","v33","v34","v35","v36","v37","v38","v39","v40","v41","v42","v43","v44","v45","v46","v47","v48","v49","v50","v51","v52","v53","v54","v55","v56","v57","v58","v59","v60","v61","v62","v63","v64","v65","v66","v67","v68","v69","v70","v71","v72","v73","v74","v75","v76","v77","v78","v79","v80","v81","v82","v83","v84","v85","v86","v87","v88","v89","v90","v91","v92","v93","v94","v95","v96","v97","v98","v99","v100","v101","v102","v103","v104","v105","v106","v107","v108","v109","v110","v111","v112","v113","v114","v115","v116","v117","v118","v119","v120","v121","v122","v123","v124","v125","v126","v127","v128","v129","v130","v131","v132","v133","v134","v135","v136","v137","v138","v139","v140","v141","v142","v143","v144","v145","v146","v147","v148","v149","v150","v151","v152","v153","v154","v155","v156","v157","v158","v159","v160","v161","v162","v163","v164","v165","v166","v167","v168","v169","v170","v171","v172","v173","v174","v175","v176","v177","v178","v179","v180","v181","v182","v183","v184","v185","v186","v187","v188","v189","v190","v191","v192","v193","v194","v195","v196","v197","v198","v199","v200","v201","v202","v203","v204","v205","v206","v207","v208","v209","v210","v211","v212","v213","v214","v215","v216","v217","v218","v219","v220","v221","v222","v223","v224","v225","v226","v227","v228","v229","v230","v231","v232","v233","v234","v235","v236","v237","v238","v239","v240","v241","v242","v243","v244","v245","v246","v247","v248","v249","v250","v251","v252","v253","v254","v255","a0","a1","a2","a3","a4","a5","a6","a7","a8","a9","a10","a11","a12","a13","a14","a15","a16","a17","a18","a19","a20","a21","a22","a23","a24","a25","a26","a27","a28","a29","a30","a31","a32","a33","a34","a35","a36","a37","a38","a39","a40","a41","a42","a43","a44","a45","a46","a47","a48","a49","a50","a51","a52","a53","a54","a55","a56","a57","a58","a59","a60","a61","a62","a63","a64","a65","a66","a67","a68","a69","a70","a71","a72","a73","a74","a75","a76","a77","a78","a79","a80","a81","a82","a83","a84","a85","a86","a87","a88","a89","a90","a91","a92","a93","a94","a95","a96","a97","a98","a99","a100","a101","a102","a103","a104","a105","a106","a107","a108","a109","a110","a111","a112","a113","a114","a115","a116","a117","a118","a119","a120","a121","a122","a123","a124","a125","a126","a127","a128","a129","a130","a131","a132","a133","a134","a135","a136","a137","a138","a139","a140","a141","a142","a143","a144","a145","a146","a147","a148","a149","a150","a151","a152","a153","a154","a155","a156","a157","a158","a159","a160","a161","a162","a163","a164","a165","a166","a167","a168","a169","a170","a171","a172","a173","a174","a175","a176","a177","a178","a179","a180","a181","a182","a183","a184","a185","a186","a187","a188","a189","a190","a191","a192","a193","a194","a195","a196","a197","a198","a199","a200","a201","a202","a203","a204","a205","a206","a207","a208","a209","a210","a211","a212","a213","a214","a215","a216","a217","a218","a219","a220","a221","a222","a223","a224","a225","a226","a227","a228","a229","a230","a231","a232","a233","a234","a235","a236","a237","a238","a239","a240","a241","a242","a243","a244","a245","a246","a247","a248","a249","a250","a251","a252","a253","a254","a255","s6","s7","s8","s9","s10","s11","s12","s13","s14","s15","s16","s17","s18","s19","s20","s21","s22","s23","s24","s25","s26","s27","s28","s29","s30","s31","s32","s33","s34","s35","s36","s37","s38","s39","s40","s41","s42","s43","s44","s45","s46","s47","s48","s49","s50","s51","s52","s53","s54","s55","s56","s57","s58","s59","s60","s61","s62","s63","s64","s65","s66","s67","s68","s69","s70","s71","s72","s73","s74","s75","s76","s77","s78","s79","s80","s81","s82","s83","s84","s85","s86","s87","s88","s89","s90","s91","s92","s93","s94","s95","s96","s97","s98","s99","s100","vcc","scc","m0","memory");
}
extern "C" void kernel_launch(void* const* d_in, const int* in_sizes, int n_in, void* d_out, int out_size, void* d_ws, size_t ws_size, hipStream_t stream) {
  char* xch = (char*)d_ws + ((ws_size - (size_t)75497472) & ~(size_t)0xfffff);
  (void)hipFuncSetAttribute((const void*)fused_attn, hipFuncAttributeMaxDynamicSharedMemorySize, 163840);
  hipLaunchKernelGGL(fused_attn, dim3(256), dim3(256), 163840, stream,
                     (const char*)d_in[0], (const char*)d_in[1], (const char*)d_in[2], (const char*)d_in[3], (const char*)d_in[4],
                     (const char*)d_in[5], (const char*)d_in[6], (const char*)d_in[7], (const char*)d_in[8], (const char*)d_in[9], xch, (char*)d_out, (char*)d_ws);
}
```

```cpp
#include <hip/hip_runtime.h>
#include <hip/hip_bf16.h>
#include <stdint.h>

__global__ __launch_bounds__(256, 1)
void fused_attn(const char* xA, const char* kA, const char* vA, const char* wA, const char* uA,
                const char* xB, const char* kB, const char* vB, const char* wB, const char* uB, char* xch, char* out, char* flags) {
  extern __shared__ __attribute__((aligned(16))) char lds[];
  const unsigned wid = __builtin_amdgcn_readfirstlane(threadIdx.x >> 6);
  const unsigned long long ka = (unsigned long long)__builtin_amdgcn_kernarg_segment_ptr();
  asm volatile(
"s_memtime s[64:65]\n"
"s_memrealtime s[66:67]\n"
"s_bfe_u32 s10, %[wg], 0x10002\n"
"s_mul_i32 s40, s10, 40\n"
"s_add_u32 s40, s40, %[kalo]\n"
"s_addc_u32 s41, %[kahi], 0\n"
"s_mov_b32 s78, %[kalo]\n"
"s_mov_b32 s79, %[kahi]\n"
"s_load_dwordx2 s[24:25], s[40:41], 0\n"
"s_load_dwordx2 s[28:29], s[40:41], 24\n"
"s_load_dwordx2 s[6:7], s[78:79], 0x60\n"
"s_load_dwordx2 s[16:17], s[40:41], 8\n"
"s_load_dwordx2 s[20:21], s[40:41], 16\n"
"s_load_dwordx2 s[8:9], s[40:41], 32\n"
"s_load_dwordx4 s[12:15], s[78:79], 0x50\n"
"s_mov_b32 s42, %[wid]\n"
"s_and_b32 s11, %[wg], 1\n"
"s_lshl_b32 s11, s11, 3\n"
"s_lshr_b32 s60, %[wg], 5\n"
"s_add_u32 s11, s11, s60\n"
"s_bfe_u32 s61, %[wg], 0x10001\n"
"s_bfe_u32 s60, %[wg], 0x20003\n"
"s_lshl2_add_u32 s61, s61, s60\n"
"s_getpc_b64 s[80:81]\n"
"LCP_%=:\n"
"s_mov_b32 s82, LEND_%=-LCP_%=\n"
"s_mov_b32 s83, 0x20000\n"
"v_mbcnt_lo_u32_b32 v2, -1, 0\n"
"v_lshlrev_b32 v2, 7, v2\n"
"s_lshr_b32 s60, %[wg], 3\n"
"s_lshl2_add_u32 s60, s60, s42\n"
"s_mul_i32 s60, s60, 640\n"
"s_mov_b64 exec, 31\n"
"buffer_load_dword v3, v2, s[80:83], s60 offen\n"
"s_mov_b64 exec, -1\n"
"s_mov_b32 s19, 0x20000\n"
"s_mov_b32 s23, 0x20000\n"
"s_mov_b32 s27, 0x20000\n"
"s_mov_b32 s31, 0x20000\n"
"s_mov_b32 s35, 0x20000\n"
"s_mov_b32 s39, 0x20000\n"
"s_mov_b32 s26, 286720\n"
"s_lshl_b32 s44, s42, 12\n"
"s_mov_b32 s50, s44\n"
"s_mul_i32 s96, s42, 7168\n"
"s_add_u32 s51, s96, 0x10000\n"
"s_mov_b32 s74, 0x8000\n"
"s_mov_b32 s58, 524288\n"
"s_mov_b32 s47, 0\n"
"s_add_u32 s60, s11, s61\n"
"s_and_b32 s60, s60, 0\n"
"s_mul_i32 s60, s60, 3\n"
"s_lshl_b32 s49, s60, 7\n"
"s_lshl_b32 s48, s60, 19\n"
"v_mbcnt_lo_u32_b32 v4, -1, 0\n"
"v_mbcnt_hi_u32_b32 v4, -1, v4\n"
"v_and_b32 v5, 15, v4\n"
"v_lshrrev_b32 v6, 4, v4\n"
"v_lshrrev_b32 v7, 3, v4\n"
"v_and_b32 v8, 7, v4\n"
"v_lshlrev_b32 v9, 1, v6\n"
"v_xor_b32 v9, v9, v8\n"
"v_lshlrev_b32 v9, 4, v9\n"
"s_mul_i32 s60, s42, 71680\n"
"v_mul_u32_u24 v10, 1280, v7\n"
"v_add3_u32 v247, v10, v9, s60\n"
"v_add_u32 v253, 9216, v247\n"
"v_add_u32 v254, 18432, v247\n"
"v_add_u32 v255, 27648, v247\n"
"v_lshl_add_u32 v240, v7, 8, v9\n"
"v_add_u32 v240, s44, v240\n"
"v_and_b32 v10, 1, v6\n"
"v_xor_b32 v10, v10, v8\n"
"v_lshlrev_b32 v10, 4, v10\n"
"v_lshl_add_u32 v10, v7, 13, v10\n"
"s_lshl_b32 s60, s42, 17\n"
"v_add_u32 v10, s60, v10\n"
"v_add_u32 v224, 1024, v10\n"
"v_add_u32 v225, 128, v10\n"
"v_xor_b32 v10, 64, v10\n"
"v_add_u32 v226, 64512, v10\n"
"v_add_u32 v227, 63616, v10\n"
"s_mul_i32 s40, s11, 286720\n"
"s_lshl_b32 s41, s61, 10\n"
"s_sub_u32 s30, 5243904, s41\n"
"s_sub_u32 s41, s41, 1024\n"
"s_ashr_i32 s60, s41, 31\n"
"s_lshl4_add_u32 s92, s10, s11\n"
"s_lshl_b32 s93, s92, 8\n"
"v_mov_b32 v0, 0\n"
"s_waitcnt lgkmcnt(0)\n"
"s_add_u32 s24, s24, s40\n"
"s_addc_u32 s25, s25, 0\n"
"s_and_b32 s25, s25, 0xffff\n"
"s_add_u32 s28, s28, s41\n"
"s_addc_u32 s29, s29, s60\n"
"s_and_b32 s29, s29, 0xffff\n"
"s_add_u32 s6, s6, s93\n"
"s_addc_u32 s7, s7, 0\n"
"global_load_dword v1, v0, s[6:7] offset:128 sc1\n"
"s_mov_b32 m0, s51\n"
"s_nop 0\n"
"buffer_load_dwordx4 v247, s[24:27], s49 offen lds\n"
"buffer_load_dwordx4 v253, s[24:27], s49 offen offset:1024 lds\n"
"buffer_load_dwordx4 v254, s[24:27], s49 offen offset:2048 lds\n"
"buffer_load_dwordx4 v255, s[24:27], s49 offen offset:3072 lds\n"
"s_add_u32 m0, s51, 4096\n"
"s_add_u32 s40, s49, 40960\n"
"buffer_load_dwordx4 v247, s[24:27], s40 offen lds\n"
"buffer_load_dwordx4 v253, s[24:27], s40 offen offset:1024 lds\n"
"buffer_load_dwordx4 v254, s[24:27], s40 offen offset:2048 lds\n"
"s_add_u32 s49, s49, 128\n"
"s_sub_u32 s41, s49, 1280\n"
"s_min_u32 s49, s49, s41\n"
"s_xor_b32 s51, s51, s74\n"
"s_mov_b32 m0, s50\n"
"s_nop 0\n"
"buffer_load_dwordx4 v224, s[28:31], s48 offen lds\n"
"buffer_load_dwordx4 v225, s[28:31], s48 offen offset:1024 lds\n"
"buffer_load_dwordx4 v226, s[28:31], s48 offen offset:2048 lds\n"
"buffer_load_dwordx4 v227, s[28:31], s48 offen offset:3072 lds\n"
"s_xor_b32 s50, s50, s74\n"
"s_lshl2_add_u32 s63, s11, s42\n"
"s_mul_i32 s63, s63, 10240\n"
"s_and_b32 s78, s42, 1\n"
"s_lshl_b32 s43, s78, 6\n"
"s_mov_b32 s77, 0x80000000\n"
"s_mov_b32 s75, 0x10000\n"
"s_mov_b32 s73, 0xffff0000\n"
"s_mov_b32 s45, 0x3e0293ee\n"
"s_mov_b32 s46, 0x5d800000\n"
"s_mov_b32 s62, 9\n"
"v_lshl_add_u32 v242, v4, 4, s44\n"
"s_lshr_b32 s79, s42, 1\n"
"s_lshl4_add_u32 s76, s79, s78\n"
"s_lshl_b32 s76, s76, 10\n"
"s_add_u32 s76, s76, 0x20000\n"
"s_sub_u32 s76, s76, s44\n"
"v_add_u32 v248, s76, v242\n"
"v_and_b32 v9, 6, v5\n"
"v_xor_b32 v9, v9, v6\n"
"v_lshlrev_b32 v9, 4, v9\n"
"s_xor_b32 m0, s50, 0xc000\n"
"s_add_u32 s40, s48, 256\n"
"buffer_load_dwordx4 v224, s[28:31], s40 offen lds\n"
"v_lshl_add_u32 v10, v5, 7, v9\n"
"s_mul_i32 s79, s42, 6144\n"
"s_add_u32 s79, s79, 0x10000\n"
"v_add_u32 v249, s79, v10\n"
"v_xor_b32 v250, 64, v249\n"
"s_lshr_b32 s79, s42, 1\n"
"s_lshl_b32 s79, s79, 11\n"
"s_add_u32 s79, s79, 90112\n"
"v_add_u32 v251, s79, v10\n"
"v_xor_b32 v252, 64, v251\n"
"v_and_b32 v11, 8, v5\n"
"v_lshl_add_u32 v10, v11, 7, v10\n"
"s_mul_i32 s79, s78, 8256\n"
"v_xor_b32 v228, s79, v10\n"
"buffer_load_dwordx4 v225, s[28:31], s40 offen offset:1024 lds\n"
"v_xor_b32 v229, 64, v228\n"
"v_xor_b32 v230, 0x2000, v228\n"
"v_xor_b32 v231, 0x2000, v229\n"
"v_lshrrev_b32 v9, 5, v4\n"
"v_lshlrev_b32 v9, 11, v9\n"
"v_bfe_u32 v10, v4, 2, 3\n"
"v_lshl_add_u32 v9, v10, 7, v9\n"
"v_and_b32 v10, 3, v4\n"
"v_lshl_add_u32 v9, v10, 3, v9\n"
"v_bfe_u32 v10, v4, 3, 2\n"
"v_lshl_add_u32 v9, v10, 5, v9\n"
"s_add_u32 s79, s79, 0x10000\n"
"v_xor_b32 v232, s79, v9\n"
"v_xor_b32 v233, 32, v232\n"
"buffer_load_dwordx4 v226, s[28:31], s40 offen offset:2048 lds\n"
"v_xor_b32 v234, 64, v232\n"
"v_xor_b32 v235, 96, v232\n"
"v_xor_b32 v236, 0x2000, v232\n"
"v_xor_b32 v237, 0x2000, v233\n"
"v_xor_b32 v238, 0x2000, v234\n"
"v_xor_b32 v239, 0x2000, v235\n"
"v_lshlrev_b32 v9, 11, v6\n"
"v_bfe_u32 v10, v4, 2, 2\n"
"v_lshl_add_u32 v9, v10, 7, v9\n"
"v_and_b32 v10, 1, v4\n"
"v_lshl_add_u32 v9, v10, 3, v9\n"
"v_bfe_u32 v10, v4, 1, 1\n"
"v_lshl_add_u32 v9, v10, 5, v9\n"
"v_bfe_u32 v10, v4, 3, 1\n"
"buffer_load_dwordx4 v227, s[28:31], s40 offen offset:3072 lds\n"
"s_cmp_eq_u32 s47, 8\ns_cselect_b32 s41, 524800, s58\n"
"s_add_u32 s48, s48, s41\n"
"s_sub_u32 s41, s48, 5242880\n"
"s_min_u32 s48, s48, s41\n"
"v_lshl_add_u32 v9, v10, 4, v9\n"
"v_bfe_u32 v10, v4, 4, 1\n"
"v_lshl_add_u32 v9, v10, 6, v9\n"
"v_xor_b32 v243, s43, v9\n"
"v_xor_b32 v244, 16, v243\n"
"v_xor_b32 v245, 64, v243\n"
"v_xor_b32 v246, 0x50, v243\n"
"s_lshl3_add_u32 s79, s11, s61\n"
"s_lshl_b32 s79, s79, 20\n"
"s_sub_u32 s79, s79, 896\n"
"s_ashr_i32 s60, s79, 31\n"
"s_add_u32 s16, s16, s79\n"
"s_addc_u32 s17, s17, s60\n"
"s_and_b32 s17, s17, 0xffff\n"
"s_mov_b32 m0, s50\n"
"s_nop 0\n"
"buffer_load_dwordx4 v224, s[28:31], s48 offen lds\n"
"s_mov_b32 s18, 1049472\n"
"s_mov_b64 s[84:85], s[16:17]\n"
"s_mov_b64 s[86:87], s[18:19]\n"
"s_add_u32 s20, s20, s79\n"
"s_addc_u32 s21, s21, s60\n"
"s_and_b32 s21, s21, 0xffff\n"
"s_mov_b32 s22, 1049472\n"
"s_mov_b64 s[88:89], s[20:21]\n"
"s_mov_b64 s[90:91], s[22:23]\n"
"s_lshr_b32 s57, %[wg], 3\n"
"s_mul_i32 s57, s57, 5\n"
"s_and_b32 s57, s57, 15\n"
"s_lshl_b32 s57, s57, 14\n"
"s_mov_b32 s55, 896\n"
"buffer_load_dwordx4 v225, s[28:31], s48 offen offset:1024 lds\n"
"s_add_u32 s56, s57, 896\n"
"s_mov_b32 s59, 16384\n"
"s_mov_b32 s52, -896\n"
"s_mov_b32 s53, 0\n"
"s_mov_b32 s54, -896\n"
"s_mul_i32 s79, s61, 655360\n"
"s_add_u32 s8, s8, s79\n"
"s_addc_u32 s9, s9, 0\n"
"s_and_b32 s9, s9, 0xffff\n"
"s_mul_i32 s79, s92, 2293760\n"
"s_add_u32 s32, s12, s79\n"
"s_addc_u32 s33, s13, 0\n"
"s_and_b32 s33, s33, 0xffff\n"
"s_mov_b32 s34, 2293760\n"
"buffer_load_dwordx4 v226, s[28:31], s48 offen offset:2048 lds\n"
"s_mul_i32 s79, s11, 573440\n"
"s_mul_i32 s60, s10, 1280\n"
"s_sub_u32 s38, 573440, s60\n"
"s_add_u32 s79, s79, s60\n"
"s_add_u32 s36, s14, s79\n"
"s_addc_u32 s37, s15, 0\n"
"s_and_b32 s37, s37, 0xffff\n"
"s_lshl2_add_u32 s93, s61, s42\n"
"s_lshl_b32 s94, s93, 10\n"
"s_add_u32 s94, s94, 229376\n"
"s_add_u32 s95, s61, 1\n"
"s_and_b32 s95, s95, 7\n"
"s_lshl_b32 s95, s95, 7\n"
"s_add_u32 s100, s61, 2\n"
"buffer_load_dwordx4 v227, s[28:31], s48 offen offset:3072 lds\n"
"s_xor_b32 s50, s50, s74\n"
"s_and_b32 s100, s100, 7\n"
"s_lshl_b32 s100, s100, 7\n"
"s_lshl_b32 s60, s61, 7\n"
"s_add_u32 s60, s60, s43\n"
"s_mul_i32 s97, s42, 122880\n"
"s_add_u32 s97, s97, s60\n"
"s_lshr_b32 s98, s42, 1\n"
"s_add_u32 s98, s98, 12\n"
"s_mul_i32 s98, s98, 40960\n"
"s_add_u32 s98, s98, s60\n"
"s_mul_i32 s60, s93, 37\n"
"s_lshr_b32 s60, s60, 8\n"
"s_mul_i32 s79, s60, 7\n"
"s_sub_u32 s79, s93, s79\n"
"s_mov_b32 m0, s51\n"
"s_nop 0\n"
"buffer_load_dwordx4 v247, s[24:27], s49 offen lds\n"
"s_mul_i32 s99, s60, 3\n"
"s_lshr_b32 s78, s79, 1\n"
"s_add_u32 s99, s99, s78\n"
"s_lshr_b32 s78, s60, 1\n"
"s_add_u32 s78, s78, 12\n"
"s_cmp_eq_u32 s79, 6\n"
"s_cselect_b32 s99, s78, s99\n"
"s_mul_i32 s99, s99, 40960\n"
"s_xor_b32 s79, s79, s60\n"
"s_and_b32 s79, s79, 1\n"
"s_lshl_b32 s79, s79, 6\n"
"s_add_u32 s99, s99, s79\n"
"s_add_u32 s99, s99, 1024\n"
"s_cmp_eq_u32 s61, 7\n"
"s_cselect_b32 s99, 0x40000000, s99\n"
"s_cselect_b32 s94, 0x40000000, s94\n"
"buffer_load_dwordx4 v253, s[24:27], s49 offen offset:1024 lds\n"
"v_mov_b64 v[4:5], 0\n"
"v_mov_b64 v[6:7], 0\n"
"v_mov_b64 v[8:9], 0\n"
"v_mov_b64 v[10:11], 0\n"
"v_mov_b64 v[12:13], 0\n"
"v_mov_b64 v[14:15], 0\n"
"v_mov_b64 v[16:17], 0\n"
"v_mov_b64 v[18:19], 0\n"
"v_mov_b64 v[20:21], 0\n"
"v_mov_b64 v[22:23], 0\n"
"v_mov_b64 v[24:25], 0\n"
"v_mov_b64 v[26:27], 0\n"
"buffer_load_dwordx4 v254, s[24:27], s49 offen offset:2048 lds\n"
"v_mov_b64 v[28:29], 0\n"
"v_mov_b64 v[30:31], 0\n"
"v_mov_b64 v[32:33], 0\n"
"v_mov_b64 v[34:35], 0\n"
"v_mov_b64 v[36:37], 0\n"
"v_mov_b64 v[38:39], 0\n"
"v_mov_b64 v[40:41], 0\n"
"v_mov_b64 v[42:43], 0\n"
"v_mov_b64 v[44:45], 0\n"
"v_mov_b64 v[46:47], 0\n"
"v_mov_b64 v[48:49], 0\n"
"v_mov_b64 v[50:51], 0\n"
"v_mov_b64 v[52:53], 0\n"
"v_mov_b64 v[54:55], 0\n"
"buffer_load_dwordx4 v255, s[24:27], s49 offen offset:3072 lds\n"
"v_mov_b64 v[56:57], 0\n"
"v_mov_b64 v[58:59], 0\n"
"v_mov_b64 v[60:61], 0\n"
"v_mov_b64 v[62:63], 0\n"
"v_mov_b64 v[64:65], 0\n"
"v_mov_b64 v[66:67], 0\n"
"v_mov_b64 v[68:69], 0\n"
"v_mov_b64 v[70:71], 0\n"
"v_mov_b64 v[72:73], 0\n"
"v_mov_b64 v[74:75], 0\n"
"v_mov_b64 v[76:77], 0\n"
"v_mov_b64 v[78:79], 0\n"
"v_mov_b64 v[80:81], 0\n"
"v_mov_b64 v[82:83], 0\n"
"s_add_u32 m0, s51, 4096\n"
"s_add_u32 s40, s49, 40960\n"
"buffer_load_dwordx4 v247, s[24:27], s40 offen lds\n"
"v_mov_b64 v[84:85], 0\n"
"v_mov_b64 v[86:87], 0\n"
"v_mov_b64 v[88:89], 0\n"
"v_mov_b64 v[90:91], 0\n"
"v_mov_b64 v[92:93], 0\n"
"v_mov_b64 v[94:95], 0\n"
"v_mov_b64 v[96:97], 0\n"
"v_mov_b64 v[98:99], 0\n"
"v_mov_b64 v[100:101], 0\n"
"v_mov_b64 v[102:103], 0\n"
"v_mov_b64 v[104:105], 0\n"
"v_mov_b64 v[106:107], 0\n"
"v_mov_b64 v[108:109], 0\n"
"v_mov_b64 v[110:111], 0\n"
"buffer_load_dwordx4 v253, s[24:27], s40 offen offset:1024 lds\n"
"v_mov_b64 v[112:113], 0\n"
"v_mov_b64 v[114:115], 0\n"
"v_mov_b64 v[116:117], 0\n"
"v_mov_b64 v[118:119], 0\n"
"v_mov_b64 v[120:121], 0\n"
"v_mov_b64 v[122:123], 0\n"
"v_mov_b64 v[124:125], 0\n"
"v_mov_b64 v[126:127], 0\n"
"v_mov_b64 v[128:129], 0\n"
"v_mov_b64 v[130:131], 0\n"
"v_mov_b64 v[132:133], 0\n"
"v_mov_b64 v[134:135], 0\n"
"v_mov_b64 v[136:137], 0\n"
"v_mov_b64 v[138:139], 0\n"
"buffer_load_dwordx4 v254, s[24:27], s40 offen offset:2048 lds\n"
"s_add_u32 s49, s49, 128\n"
"s_sub_u32 s41, s49, 1280\n"
"s_min_u32 s49, s49, s41\n"
"s_xor_b32 s51, s51, s74\n"
"v_mov_b64 v[140:141], 0\n"
"v_mov_b64 v[142:143], 0\n"
"v_mov_b64 v[144:145], 0\n"
"v_mov_b64 v[146:147], 0\n"
"v_mov_b64 v[148:149], 0\n"
"v_mov_b64 v[150:151], 0\n"
"v_mov_b64 v[152:153], 0\n"
"v_mov_b64 v[154:155], 0\n"
"v_mov_b64 v[156:157], 0\n"
"v_mov_b64 v[158:159], 0\n"
"v_mov_b64 v[160:161], 0\n"
"v_mov_b64 v[162:163], 0\n"
"v_mov_b64 v[164:165], 0\n"
"v_mov_b64 v[166:167], 0\n"
"v_mov_b64 v[168:169], 0\n"
"v_mov_b64 v[170:171], 0\n"
"v_mov_b64 v[172:173], 0\n"
"v_mov_b64 v[174:175], 0\n"
"s_memtime s[80:81]\n"
"s_memrealtime s[82:83]\n"
"v_mov_b64 v[176:177], 0\n"
"v_mov_b64 v[178:179], 0\n"
"v_mov_b64 v[180:181], 0\n"
"v_mov_b64 v[182:183], 0\n"
"v_mov_b64 v[184:185], 0\n"
"v_mov_b64 v[186:187], 0\n"
"v_mov_b64 v[188:189], 0\n"
"v_mov_b64 v[190:191], 0\n"
"v_mov_b64 v[192:193], 0\n"
"v_mov_b64 v[194:195], 0\n"
"v_mov_b64 v[196:197], 0\n"
"v_mov_b64 v[198:199], 0\n"
"v_mov_b64 v[200:201], 0\n"
"v_mov_b64 v[202:203], 0\n"
"v_mov_b64 v[204:205], 0\n"
"v_mov_b64 v[206:207], 0\n"
"v_mov_b64 v[208:209], 0\n"
"v_mov_b64 v[210:211], 0\n"
"v_mov_b64 v[212:213], 0\n"
"v_mov_b64 v[214:215], 0\n"
"v_mov_b64 v[216:217], 0\n"
"v_mov_b64 v[218:219], 0\n"
"v_mov_b64 v[220:221], 0\n"
"v_mov_b64 v[222:223], 0\n"
"s_waitcnt vmcnt(26)\n"
"v_readfirstlane_b32 s92, v1\n"
"v_mov_b64 v[0:1], 0\n"
"v_mov_b64 v[2:3], 0\n"
"s_waitcnt lgkmcnt(0)\n"
"s_sub_u32 s80, s80, s64\n"
"s_sub_u32 s82, s82, s66\n"
"s_mul_i32 s80, s80, 10\n"
"s_mul_i32 s82, s82, 211\n"
"s_cmp_gt_u32 s80, s82\n"
"s_cselect_b32 s64, 1, 0\n"
"s_cselect_b32 s68, 655360, 0\n"
"s_cmp_eq_u32 s42, 0\n"
"s_cselect_b32 s64, s64, 0\n"
"s_mov_b32 s65, 0\n"
"s_add_u32 s66, s66, 2800\n"
"s_mov_b32 s67, s66\n"
"s_mov_b32 s72, 122\n"
"s_mov_b64 vcc, s[64:65]\n"
"s_nop 2\n"
"s_mul_i32 s60, s92, 0x9e3779b1\n"
"s_add_u32 s60, s60, 0x7f4a7c15\n"
"s_waitcnt vmcnt(15)\n"
"s_barrier\n"
"ds_read_b128 a[160:163], v249 offset:0\n"
"ds_read_b128 a[164:167], v250 offset:0\n"
"ds_read_b64_tr_b16 a[96:97], v243 offset:0\n"
"ds_read_b64_tr_b16 a[98:99], v243 offset:512\n"
"ds_read_b64_tr_b16 a[100:101], v243 offset:8192\n"
"ds_read_b64_tr_b16 a[102:103], v243 offset:8704\n"
"ds_read_b64_tr_b16 a[104:105], v244 offset:0\n"
"ds_read_b64_tr_b16 a[106:107], v244 offset:512\n"
"ds_read_b64_tr_b16 a[108:109], v244 offset:8192\n"
"ds_read_b64_tr_b16 a[110:111], v244 offset:8704\n"
"ds_read_b64_tr_b16 a[112:113], v245 offset:0\n"
"ds_read_b64_tr_b16 a[114:115], v245 offset:512\n"
"ds_read_b64_tr_b16 a[116:117], v245 offset:8192\n"
"ds_read_b64_tr_b16 a[118:119], v245 offset:8704\n"
"ds_read_b64_tr_b16 a[120:121], v246 offset:0\n"
"ds_read_b64_tr_b16 a[122:123], v246 offset:512\n"
"ds_read_b64_tr_b16 a[124:125], v246 offset:8192\n"
"ds_read_b64_tr_b16 a[126:127], v246 offset:8704\n"
"ds_read_b64_tr_b16 a[128:129], v243 offset:1024\n"
"ds_read_b64_tr_b16 a[130:131], v243 offset:1536\n"
"ds_read_b64_tr_b16 a[132:133], v243 offset:9216\n"
"ds_read_b64_tr_b16 a[134:135], v243 offset:9728\n"
"ds_read_b64_tr_b16 a[136:137], v244 offset:1024\n"
"ds_read_b64_tr_b16 a[138:139], v244 offset:1536\n"
"ds_read_b64_tr_b16 a[140:141], v244 offset:9216\n"
"ds_read_b64_tr_b16 a[142:143], v244 offset:9728\n"
"ds_read_b64_tr_b16 a[144:145], v245 offset:1024\n"
"ds_read_b64_tr_b16 a[146:147], v245 offset:1536\n"
"ds_read_b64_tr_b16 a[148:149], v245 offset:9216\n"
"ds_read_b64_tr_b16 a[150:151], v245 offset:9728\n"
"ds_read_b64_tr_b16 a[152:153], v246 offset:1024\n"
"ds_read_b64_tr_b16 a[154:155], v246 offset:1536\n"
"ds_read_b64_tr_b16 a[156:157], v246 offset:9216\n"
"ds_read_b64_tr_b16 a[158:159], v246 offset:9728\n"
"ds_read_b128 a[168:171], v249 offset:2048\n"
"ds_read_b128 a[172:175], v250 offset:2048\n"
"ds_read_b128 a[176:179], v249 offset:4096\n"
"ds_read_b128 a[180:183], v250 offset:4096\n"
"ds_read_b128 a[184:187], v251 offset:0\n"
"ds_read_b128 a[188:191], v252 offset:0\n"
"LPL_%=:\n"
"s_waitcnt lgkmcnt(15)\n"
"v_mfma_f32_16x16x32_bf16 v[112:115], a[96:99], a[160:163], v[112:115]\n"
"v_mfma_f32_16x16x32_bf16 v[112:115], a[100:103], a[164:167], v[112:115]\n"
"v_mfma_f32_16x16x32_bf16 v[116:119], a[104:107], a[160:163], v[116:119]\n"
"s_waitcnt vmcnt(11) lgkmcnt(6)\n"
"v_mfma_f32_16x16x32_bf16 v[116:119], a[108:111], a[164:167], v[116:119]\n"
"s_barrier\n"
"v_mfma_f32_16x16x32_bf16 v[120:123], a[112:115], a[160:163], v[120:123]\n"
"s_add_u32 s40, s48, 256\n"
"s_xor_b32 m0, s50, 0xc000\n"
"ds_read_b64_tr_b16 a[192:193], v243 offset:16384\n"
"v_mfma_f32_16x16x32_bf16 v[120:123], a[116:119], a[164:167], v[120:123]\n"
"buffer_load_dwordx4 v224, s[28:31], s40 offen lds\n"
"v_mfma_f32_16x16x32_bf16 v[124:127], a[120:123], a[160:163], v[124:127]\n"
"ds_read_b64_tr_b16 a[194:195], v243 offset:16896\n"
"v_mfma_f32_16x16x32_bf16 v[124:127], a[124:127], a[164:167], v[124:127]\n"
"ds_read_b64_tr_b16 a[196:197], v243 offset:24576\n"
"v_mfma_f32_16x16x32_bf16 v[128:131], a[128:131], a[160:163], v[128:131]\n"
"ds_read_b64_tr_b16 a[198:199], v243 offset:25088\n"
"v_mfma_f32_16x16x32_bf16 v[128:131], a[132:135], a[164:167], v[128:131]\n"
"ds_read_b64_tr_b16 a[200:201], v244 offset:16384\n"
"v_mfma_f32_16x16x32_bf16 v[132:135], a[136:139], a[160:163], v[132:135]\n"
"ds_read_b64_tr_b16 a[202:203], v244 offset:16896\n"
"v_mfma_f32_16x16x32_bf16 v[132:135], a[140:143], a[164:167], v[132:135]\n"
"buffer_load_dwordx4 v225, s[28:31], s40 offen offset:1024 lds\n"
"v_mfma_f32_16x16x32_bf16 v[136:139], a[144:147], a[160:163], v[136:139]\n"
"ds_read_b64_tr_b16 a[204:205], v244 offset:24576\n"
"v_mfma_f32_16x16x32_bf16 v[136:139], a[148:151], a[164:167], v[136:139]\n"
"ds_read_b64_tr_b16 a[206:207], v244 offset:25088\n"
"v_mfma_f32_16x16x32_bf16 v[140:143], a[152:155], a[160:163], v[140:143]\n"
"ds_read_b64_tr_b16 a[208:209], v245 offset:16384\n"
"v_mfma_f32_16x16x32_bf16 v[140:143], a[156:159], a[164:167], v[140:143]\n"
"s_waitcnt lgkmcnt(6)\n"
"buffer_load_dwordx4 v226, s[28:31], s40 offen offset:2048 lds\n"
"v_mfma_f32_16x16x32_bf16 v[172:175], a[152:155], a[168:171], v[172:175]\n"
"s_waitcnt lgkmcnt(5)\n"
"ds_read_b64_tr_b16 a[210:211], v245 offset:16896\n"
"v_mfma_f32_16x16x32_bf16 v[172:175], a[156:159], a[172:175], v[172:175]\n"
"ds_read_b64_tr_b16 a[212:213], v245 offset:24576\n"
"v_mfma_f32_16x16x32_bf16 v[168:171], a[144:147], a[168:171], v[168:171]\n"
"ds_read_b64_tr_b16 a[214:215], v245 offset:25088\n"
"v_mfma_f32_16x16x32_bf16 v[168:171], a[148:151], a[172:175], v[168:171]\n"
"ds_read_b64_tr_b16 a[216:217], v246 offset:16384\n"
"v_mfma_f32_16x16x32_bf16 v[164:167], a[136:139], a[168:171], v[164:167]\n"
"ds_read_b64_tr_b16 a[218:219], v246 offset:16896\n"
"v_mfma_f32_16x16x32_bf16 v[164:167], a[140:143], a[172:175], v[164:167]\n"
"buffer_load_dwordx4 v227, s[28:31], s40 offen offset:3072 lds\n"
"v_mfma_f32_16x16x32_bf16 v[160:163], a[128:131], a[168:171], v[160:163]\n"
"s_cmp_eq_u32 s47, 8\ns_cselect_b32 s41, 524800, s58\n"
"ds_read_b64_tr_b16 a[220:221], v246 offset:24576\n"
"v_mfma_f32_16x16x32_bf16 v[160:163], a[132:135], a[172:175], v[160:163]\n"
"s_add_u32 s48, s48, s41\n"
"ds_read_b64_tr_b16 a[222:223], v246 offset:25088\n"
"v_mfma_f32_16x16x32_bf16 v[156:159], a[120:123], a[168:171], v[156:159]\n"
"s_sub_u32 s41, s48, 5242880\n"
"ds_read_b64_tr_b16 a[224:225], v243 offset:17408\n"
"v_mfma_f32_16x16x32_bf16 v[156:159], a[124:127], a[172:175], v[156:159]\n"
"s_min_u32 s48, s48, s41\n"
"ds_read_b64_tr_b16 a[226:227], v243 offset:17920\n"
"v_mfma_f32_16x16x32_bf16 v[152:155], a[112:115], a[168:171], v[152:155]\n"
"s_mov_b32 m0, s50\n"
"ds_read_b64_tr_b16 a[228:229], v243 offset:25600\n"
"v_mfma_f32_16x16x32_bf16 v[152:155], a[116:119], a[172:175], v[152:155]\n"
"buffer_load_dwordx4 v224, s[28:31], s48 offen lds\n"
"v_mfma_f32_16x16x32_bf16 v[148:151], a[104:107], a[168:171], v[148:151]\n"
"ds_read_b64_tr_b16 a[230:231], v243 offset:26112\n"
"v_mfma_f32_16x16x32_bf16 v[148:151], a[108:111], a[172:175], v[148:151]\n"
"ds_read_b64_tr_b16 a[232:233], v244 offset:17408\n"
"v_xor_b32 v243, s74, v243\n"
"v_mfma_f32_16x16x32_bf16 v[144:147], a[96:99], a[168:171], v[144:147]\n"
"ds_read_b64_tr_b16 a[234:235], v244 offset:17920\n"
"v_xor_b32 v249, s74, v249\n"
"v_mfma_f32_16x16x32_bf16 v[144:147], a[100:103], a[172:175], v[144:147]\n"
"buffer_load_dwordx4 v225, s[28:31], s48 offen offset:1024 lds\n"
"v_mfma_f32_16x16x32_bf16 v[176:179], a[96:99], a[176:179], v[176:179]\n"
"s_waitcnt lgkmcnt(5)\n"
"ds_read_b64_tr_b16 a[236:237], v244 offset:25600\n"
"v_xor_b32 v250, s74, v250\n"
"v_mfma_f32_16x16x32_bf16 v[176:179], a[100:103], a[180:183], v[176:179]\n"
"ds_read_b64_tr_b16 a[238:239], v244 offset:26112\n"
"v_xor_b32 v244, s74, v244\n"
"v_mfma_f32_16x16x32_bf16 v[180:183], a[104:107], a[176:179], v[180:183]\n"
"ds_read_b64_tr_b16 a[240:241], v245 offset:17408\n"
"v_xor_b32 v251, s74, v251\n"
"v_mfma_f32_16x16x32_bf16 v[180:183], a[108:111], a[180:183], v[180:183]\n"
"buffer_load_dwordx4 v226, s[28:31], s48 offen offset:2048 lds\n"
"v_mfma_f32_16x16x32_bf16 v[184:187], a[112:115], a[176:179], v[184:187]\n"
"ds_read_b64_tr_b16 a[242:243], v245 offset:17920\n"
"v_xor_b32 v252, s74, v252\n"
"v_mfma_f32_16x16x32_bf16 v[184:187], a[116:119], a[180:183], v[184:187]\n"
"ds_read_b64_tr_b16 a[244:245], v245 offset:25600\n"
"v_mfma_f32_16x16x32_bf16 v[188:191], a[120:123], a[176:179], v[188:191]\n"
"ds_read_b64_tr_b16 a[246:247], v245 offset:26112\n"
"v_xor_b32 v245, s74, v245\n"
"v_mfma_f32_16x16x32_bf16 v[188:191], a[124:127], a[180:183], v[188:191]\n"
"ds_read_b64_tr_b16 a[248:249], v246 offset:17408\n"
"v_mfma_f32_16x16x32_bf16 v[200:203], a[144:147], a[176:179], v[200:203]\n"
"ds_read_b64_tr_b16 a[250:251], v246 offset:17920\n"
"v_mfma_f32_16x16x32_bf16 v[200:203], a[148:151], a[180:183], v[200:203]\n"
"buffer_load_dwordx4 v227, s[28:31], s48 offen offset:3072 lds\n"
"v_mfma_f32_16x16x32_bf16 v[204:207], a[152:155], a[176:179], v[204:207]\n"
"s_xor_b32 s50, s50, s74\n"
"ds_read_b64_tr_b16 a[252:253], v246 offset:25600\n"
"v_mfma_f32_16x16x32_bf16 v[204:207], a[156:159], a[180:183], v[204:207]\n"
"ds_read_b64_tr_b16 a[254:255], v246 offset:26112\n"
"v_xor_b32 v246, s74, v246\n"
"v_mfma_f32_16x16x32_bf16 v[192:195], a[128:131], a[176:179], v[192:195]\n"
"v_mfma_f32_16x16x32_bf16 v[192:195], a[132:135], a[180:183], v[192:195]\n"
"v_mfma_f32_16x16x32_bf16 v[196:199], a[136:139], a[176:179], v[196:199]\n"
"v_mfma_f32_16x16x32_bf16 v[196:199], a[140:143], a[180:183], v[196:199]\n"
"v_mfma_f32_16x16x32_bf16 v[220:223], a[136:139], a[184:187], v[220:223]\n"
"v_mfma_f32_16x16x32_bf16 v[220:223], a[140:143], a[188:191], v[220:223]\n"
"v_mfma_f32_16x16x32_bf16 v[216:219], a[128:131], a[184:187], v[216:219]\n"
"v_mfma_f32_16x16x32_bf16 v[216:219], a[132:135], a[188:191], v[216:219]\n"
"v_mfma_f32_16x16x32_bf16 v[212:215], a[104:107], a[184:187], v[212:215]\n"
"v_mfma_f32_16x16x32_bf16 v[212:215], a[108:111], a[188:191], v[212:215]\n"
"v_mfma_f32_16x16x32_bf16 v[208:211], a[96:99], a[184:187], v[208:211]\n"
"v_mfma_f32_16x16x32_bf16 v[208:211], a[100:103], a[188:191], v[208:211]\n"
"v_mfma_f32_16x16x32_bf16 v[0:3], a[192:195], a[160:163], v[0:3]\n"
"s_waitcnt lgkmcnt(0)\n"
"v_mfma_f32_16x16x32_bf16 v[0:3], a[196:199], a[164:167], v[0:3]\n"
"s_waitcnt lgkmcnt(0)\n"
"v_mfma_f32_16x16x32_bf16 v[4:7], a[200:203], a[160:163], v[4:7]\n"
"s_waitcnt vmcnt(8) lgkmcnt(0)\n"
"v_mfma_f32_16x16x32_bf16 v[4:7], a[204:207], a[164:167], v[4:7]\n"
"s_barrier\n"
"v_mfma_f32_16x16x32_bf16 v[8:11], a[208:211], a[160:163], v[8:11]\n"
"s_mov_b32 m0, s51\n"
"ds_read_b64_tr_b16 a[96:97], v243 offset:0\n"
"v_mfma_f32_16x16x32_bf16 v[8:11], a[212:215], a[164:167], v[8:11]\n"
"buffer_load_dwordx4 v247, s[24:27], s49 offen lds\n"
"v_mfma_f32_16x16x32_bf16 v[12:15], a[216:219], a[160:163], v[12:15]\n"
"ds_read_b64_tr_b16 a[98:99], v243 offset:512\n"
"v_mfma_f32_16x16x32_bf16 v[12:15], a[220:223], a[164:167], v[12:15]\n"
"ds_read_b64_tr_b16 a[100:101], v243 offset:8192\n"
"v_mfma_f32_16x16x32_bf16 v[16:19], a[224:227], a[160:163], v[16:19]\n"
"ds_read_b64_tr_b16 a[102:103], v243 offset:8704\n"
"v_mfma_f32_16x16x32_bf16 v[16:19], a[228:231], a[164:167], v[16:19]\n"
"ds_read_b64_tr_b16 a[104:105], v244 offset:0\n"
"v_mfma_f32_16x16x32_bf16 v[20:23], a[232:235], a[160:163], v[20:23]\n"
"ds_read_b64_tr_b16 a[106:107], v244 offset:512\n"
"v_mfma_f32_16x16x32_bf16 v[20:23], a[236:239], a[164:167], v[20:23]\n"
"buffer_load_dwordx4 v253, s[24:27], s49 offen offset:1024 lds\n"
"v_mfma_f32_16x16x32_bf16 v[24:27], a[240:243], a[160:163], v[24:27]\n"
"ds_read_b64_tr_b16 a[108:109], v244 offset:8192\n"
"v_mfma_f32_16x16x32_bf16 v[24:27], a[244:247], a[164:167], v[24:27]\n"
"ds_read_b64_tr_b16 a[110:111], v244 offset:8704\n"
"v_mfma_f32_16x16x32_bf16 v[28:31], a[248:251], a[160:163], v[28:31]\n"
"ds_read_b128 a[160:163], v249 offset:0\n"
"v_mfma_f32_16x16x32_bf16 v[28:31], a[252:255], a[164:167], v[28:31]\n"
"buffer_load_dwordx4 v254, s[24:27], s49 offen offset:2048 lds\n"
"v_mfma_f32_16x16x32_bf16 v[60:63], a[248:251], a[168:171], v[60:63]\n"
"ds_read_b128 a[164:167], v250 offset:0\n"
"v_mfma_f32_16x16x32_bf16 v[60:63], a[252:255], a[172:175], v[60:63]\n"
"ds_read_b64_tr_b16 a[112:113], v245 offset:0\n"
"v_mfma_f32_16x16x32_bf16 v[56:59], a[240:243], a[168:171], v[56:59]\n"
"ds_read_b64_tr_b16 a[114:115], v245 offset:512\n"
"v_mfma_f32_16x16x32_bf16 v[56:59], a[244:247], a[172:175], v[56:59]\n"
"ds_read_b64_tr_b16 a[116:117], v245 offset:8192\n"
"v_mfma_f32_16x16x32_bf16 v[52:55], a[232:235], a[168:171], v[52:55]\n"
"ds_read_b64_tr_b16 a[118:119], v245 offset:8704\n"
"v_mfma_f32_16x16x32_bf16 v[52:55], a[236:239], a[172:175], v[52:55]\n"
"buffer_load_dwordx4 v255, s[24:27], s49 offen offset:3072 lds\n"
"v_mfma_f32_16x16x32_bf16 v[48:51], a[224:227], a[168:171], v[48:51]\n"
"ds_read_b64_tr_b16 a[120:121], v246 offset:0\n"
"v_mfma_f32_16x16x32_bf16 v[48:51], a[228:231], a[172:175], v[48:51]\n"
"s_add_u32 s40, s49, 40960\n"
"ds_read_b64_tr_b16 a[122:123], v246 offset:512\n"
"v_mfma_f32_16x16x32_bf16 v[44:47], a[216:219], a[168:171], v[44:47]\n"
"s_add_u32 m0, s51, 4096\n"
"ds_read_b64_tr_b16 a[124:125], v246 offset:8192\n"
"v_mfma_f32_16x16x32_bf16 v[44:47], a[220:223], a[172:175], v[44:47]\n"
"buffer_load_dwordx4 v247, s[24:27], s40 offen lds\n"
"v_mfma_f32_16x16x32_bf16 v[40:43], a[208:211], a[168:171], v[40:43]\n"
"ds_read_b64_tr_b16 a[126:127], v246 offset:8704\n"
"v_mfma_f32_16x16x32_bf16 v[40:43], a[212:215], a[172:175], v[40:43]\n"
"ds_read_b64_tr_b16 a[128:129], v243 offset:1024\n"
"v_mfma_f32_16x16x32_bf16 v[36:39], a[200:203], a[168:171], v[36:39]\n"
"ds_read_b64_tr_b16 a[130:131], v243 offset:1536\n"
"v_mfma_f32_16x16x32_bf16 v[36:39], a[204:207], a[172:175], v[36:39]\n"
"ds_read_b64_tr_b16 a[132:133], v243 offset:9216\n"
"v_mfma_f32_16x16x32_bf16 v[32:35], a[192:195], a[168:171], v[32:35]\n"
"ds_read_b64_tr_b16 a[134:135], v243 offset:9728\n"
"v_mfma_f32_16x16x32_bf16 v[32:35], a[196:199], a[172:175], v[32:35]\n"
"buffer_load_dwordx4 v253, s[24:27], s40 offen offset:1024 lds\n"
"v_mfma_f32_16x16x32_bf16 v[64:67], a[192:195], a[176:179], v[64:67]\n"
"ds_read_b64_tr_b16 a[136:137], v244 offset:1024\n"
"v_mfma_f32_16x16x32_bf16 v[64:67], a[196:199], a[180:183], v[64:67]\n"
"ds_read_b64_tr_b16 a[138:139], v244 offset:1536\n"
"v_mfma_f32_16x16x32_bf16 v[68:71], a[200:203], a[176:179], v[68:71]\n"
"ds_read_b64_tr_b16 a[140:141], v244 offset:9216\n"
"v_mfma_f32_16x16x32_bf16 v[68:71], a[204:207], a[180:183], v[68:71]\n"
"buffer_load_dwordx4 v254, s[24:27], s40 offen offset:2048 lds\n"
"v_mfma_f32_16x16x32_bf16 v[72:75], a[208:211], a[176:179], v[72:75]\n"
"s_add_u32 s49, s49, 128\n"
"ds_read_b64_tr_b16 a[142:143], v244 offset:9728\n"
"v_mfma_f32_16x16x32_bf16 v[72:75], a[212:215], a[180:183], v[72:75]\n"
"s_sub_u32 s41, s49, 1280\n"
"ds_read_b64_tr_b16 a[144:145], v245 offset:1024\n"
"v_mfma_f32_16x16x32_bf16 v[76:79], a[216:219], a[176:179], v[76:79]\n"
"s_min_u32 s49, s49, s41\n"
"ds_read_b64_tr_b16 a[146:147], v245 offset:1536\n"
"v_mfma_f32_16x16x32_bf16 v[76:79], a[220:223], a[180:183], v[76:79]\n"
"s_xor_b32 s51, s51, s74\n"
"ds_read_b64_tr_b16 a[148:149], v245 offset:9216\n"
"v_mfma_f32_16x16x32_bf16 v[88:91], a[240:243], a[176:179], v[88:91]\n"
"ds_read_b64_tr_b16 a[150:151], v245 offset:9728\n"
"v_mfma_f32_16x16x32_bf16 v[88:91], a[244:247], a[180:183], v[88:91]\n"
"ds_read_b64_tr_b16 a[152:153], v246 offset:1024\n"
"v_mfma_f32_16x16x32_bf16 v[92:95], a[248:251], a[176:179], v[92:95]\n"
"ds_read_b64_tr_b16 a[154:155], v246 offset:1536\n"
"v_mfma_f32_16x16x32_bf16 v[92:95], a[252:255], a[180:183], v[92:95]\n"
"ds_read_b64_tr_b16 a[156:157], v246 offset:9216\n"
"v_mfma_f32_16x16x32_bf16 v[80:83], a[224:227], a[176:179], v[80:83]\n"
"ds_read_b64_tr_b16 a[158:159], v246 offset:9728\n"
"v_mfma_f32_16x16x32_bf16 v[80:83], a[228:231], a[180:183], v[80:83]\n"
"ds_read_b128 a[168:171], v249 offset:2048\n"
"v_mfma_f32_16x16x32_bf16 v[84:87], a[232:235], a[176:179], v[84:87]\n"
"ds_read_b128 a[172:175], v250 offset:2048\n"
"v_mfma_f32_16x16x32_bf16 v[84:87], a[236:239], a[180:183], v[84:87]\n"
"ds_read_b128 a[176:179], v249 offset:4096\n"
"v_mfma_f32_16x16x32_bf16 v[108:111], a[232:235], a[184:187], v[108:111]\n"
"ds_read_b128 a[180:183], v250 offset:4096\n"
"v_mfma_f32_16x16x32_bf16 v[108:111], a[236:239], a[188:191], v[108:111]\n"
"v_mfma_f32_16x16x32_bf16 v[104:107], a[224:227], a[184:187], v[104:107]\n"
"v_mfma_f32_16x16x32_bf16 v[104:107], a[228:231], a[188:191], v[104:107]\n"
"v_mfma_f32_16x16x32_bf16 v[100:103], a[200:203], a[184:187], v[100:103]\n"
"v_mfma_f32_16x16x32_bf16 v[100:103], a[204:207], a[188:191], v[100:103]\n"
"v_mfma_f32_16x16x32_bf16 v[96:99], a[192:195], a[184:187], v[96:99]\n"
"s_add_u32 s47, s47, 1\n"
"ds_read_b128 a[184:187], v251 offset:0\n"
"v_mfma_f32_16x16x32_bf16 v[96:99], a[196:199], a[188:191], v[96:99]\n"
"s_waitcnt lgkmcnt(2)\n"
"s_cmp_lg_u32 s47, s62\n"
"ds_read_b128 a[188:191], v252 offset:0\n"
"s_cbranch_scc1 LPL_%=\n"
"s_cmp_eq_u32 s47, 18\n"
"s_cbranch_scc1 LPT2_%=\n"
"v_mfma_f32_16x16x32_bf16 v[112:115], a[96:99], a[160:163], v[112:115]\n"
"v_mfma_f32_16x16x32_bf16 v[112:115], a[100:103], a[164:167], v[112:115]\n"
"v_mfma_f32_16x16x32_bf16 v[116:119], a[104:107], a[160:163], v[116:119]\n"
"s_waitcnt vmcnt(11)\n"
"v_mfma_f32_16x16x32_bf16 v[116:119], a[108:111], a[164:167], v[116:119]\n"
"s_barrier\n"
"v_mfma_f32_16x16x32_bf16 v[120:123], a[112:115], a[160:163], v[120:123]\n"
"s_add_u32 s40, s48, 256\n"
"s_xor_b32 m0, s50, 0xc000\n"
"ds_read_b64_tr_b16 a[228:229], v243 offset:25600\n"
"v_mfma_f32_16x16x32_bf16 v[120:123], a[116:119], a[164:167], v[120:123]\n"
"buffer_load_dwordx4 v224, s[28:31], s40 offen lds\n"
"v_mfma_f32_16x16x32_bf16 v[124:127], a[120:123], a[160:163], v[124:127]\n"
"v_mul_f32 v114, s45, v114\n"
"v_mul_f32 v117, s45, v117\n"
"v_mfma_f32_16x16x32_bf16 v[124:127], a[124:127], a[164:167], v[124:127]\n"
"ds_read_b64_tr_b16 a[236:237], v244 offset:25600\n"
"v_mul_f32 v113, s45, v113\n"
"v_mfma_f32_16x16x32_bf16 v[128:131], a[128:131], a[160:163], v[128:131]\n"
"ds_read_b64_tr_b16 a[206:207], v244 offset:25088\n"
"ds_read_b64_tr_b16 a[242:243], v245 offset:17920\n"
"v_mfma_f32_16x16x32_bf16 v[128:131], a[132:135], a[164:167], v[128:131]\n"
"ds_read_b64_tr_b16 a[196:197], v243 offset:24576\n"
"ds_read_b64_tr_b16 a[218:219], v246 offset:16896\n"
"v_mfma_f32_16x16x32_bf16 v[132:135], a[136:139], a[160:163], v[132:135]\n"
"ds_read_b64_tr_b16 a[240:241], v245 offset:17408\n"
"ds_read_b64_tr_b16 a[244:245], v245 offset:25600\n"
"v_mfma_f32_16x16x32_bf16 v[132:135], a[140:143], a[164:167], v[132:135]\n"
"buffer_load_dwordx4 v225, s[28:31], s40 offen offset:1024 lds\n"
"v_mfma_f32_16x16x32_bf16 v[136:139], a[144:147], a[160:163], v[136:139]\n"
"ds_read_b64_tr_b16 a[210:211], v245 offset:16896\n"
"v_mul_f32 v120, s45, v120\n"
"v_mfma_f32_16x16x32_bf16 v[136:139], a[148:151], a[164:167], v[136:139]\n"
"ds_read_b64_tr_b16 a[248:249], v246 offset:17408\n"
"ds_read_b64_tr_b16 a[250:251], v246 offset:17920\n"
"v_mul_f32 v112, s45, v112\n"
"v_mul_f32 v121, s45, v121\n"
"v_mfma_f32_16x16x32_bf16 v[140:143], a[152:155], a[160:163], v[140:143]\n"
"ds_read_b64_tr_b16 a[226:227], v243 offset:17920\n"
"v_mul_f32 v126, s45, v126\n"
"v_mfma_f32_16x16x32_bf16 v[140:143], a[156:159], a[164:167], v[140:143]\n"
"s_waitcnt lgkmcnt(10)\n"
"buffer_load_dwordx4 v226, s[28:31], s40 offen offset:2048 lds\n"
"v_mfma_f32_16x16x32_bf16 v[220:223], a[136:139], a[184:187], v[220:223]\n"
"v_cvt_pk_bf16_f32 v112, v112, v113\n"
"v_mul_f32 v122, s45, v122\n"
"v_mfma_f32_16x16x32_bf16 v[220:223], a[140:143], a[188:191], v[220:223]\n"
"v_mul_f32 v119, s45, v119\n"
"v_mul_f32 v135, s45, v135\n"
"v_mfma_f32_16x16x32_bf16 v[216:219], a[128:131], a[184:187], v[216:219]\n"
"ds_read_b64_tr_b16 a[214:215], v245 offset:25088\n"
"v_mul_f32 v124, s45, v124\n"
"v_mfma_f32_16x16x32_bf16 v[216:219], a[132:135], a[188:191], v[216:219]\n"
"ds_read_b64_tr_b16 a[194:195], v243 offset:16896\n"
"ds_read_b64_tr_b16 a[234:235], v244 offset:17920\n"
"v_mfma_f32_16x16x32_bf16 v[212:215], a[104:107], a[184:187], v[212:215]\n"
"ds_read_b64_tr_b16 a[212:213], v245 offset:24576\n"
"v_mul_f32 v115, s45, v115\n"
"v_mfma_f32_16x16x32_bf16 v[212:215], a[108:111], a[188:191], v[212:215]\n"
"buffer_load_dwordx4 v227, s[28:31], s40 offen offset:3072 lds\n"
"v_mfma_f32_16x16x32_bf16 v[208:211], a[96:99], a[184:187], v[208:211]\n"
"s_cmp_eq_u32 s47, 8\ns_cselect_b32 s41, 524800, s58\n"
"v_mul_f32 v130, s45, v130\n"
"v_mul_f32 v131, s45, v131\n"
"v_mfma_f32_16x16x32_bf16 v[208:211], a[100:103], a[188:191], v[208:211]\n"
"s_add_u32 s48, s48, s41\n"
"v_mul_f32 v118, s45, v118\n"
"v_mul_f32 v141, s45, v141\n"
"v_mfma_f32_16x16x32_bf16 v[172:175], a[152:155], a[168:171], v[172:175]\n"
"s_sub_u32 s41, s48, 5242880\n"
"ds_read_b64_tr_b16 a[254:255], v246 offset:26112\n"
"v_mul_f32 v139, s45, v139\n"
"v_mfma_f32_16x16x32_bf16 v[172:175], a[156:159], a[172:175], v[172:175]\n"
"s_min_u32 s48, s48, s41\n"
"ds_read_b64_tr_b16 a[232:233], v244 offset:17408\n"
"v_mul_f32 v214, s45, v214\n"
"v_mfma_f32_16x16x32_bf16 v[168:171], a[144:147], a[168:171], v[168:171]\n"
"s_mov_b32 m0, s50\n"
"ds_read_b64_tr_b16 a[216:217], v246 offset:16384\n"
"ds_read_b64_tr_b16 a[252:253], v246 offset:25600\n"
"v_mfma_f32_16x16x32_bf16 v[168:171], a[148:151], a[172:175], v[168:171]\n"
"buffer_load_dwordx4 v224, s[28:31], s48 offen lds\n"
"v_mfma_f32_16x16x32_bf16 v[164:167], a[136:139], a[168:171], v[164:167]\n"
"v_cvt_pk_bf16_f32 v113, v114, v115\n"
"v_mul_f32 v123, s45, v123\n"
"v_mfma_f32_16x16x32_bf16 v[164:167], a[140:143], a[172:175], v[164:167]\n"
"ds_read_b64_tr_b16 a[230:231], v243 offset:26112\n"
"v_mul_f32 v209, s45, v209\n"
"v_mfma_f32_16x16x32_bf16 v[160:163], a[128:131], a[168:171], v[160:163]\n"
"ds_read_b64_tr_b16 a[204:205], v244 offset:24576\n"
"v_mul_f32 v129, s45, v129\n"
"v_mfma_f32_16x16x32_bf16 v[160:163], a[132:135], a[172:175], v[160:163]\n"
"buffer_load_dwordx4 v225, s[28:31], s48 offen offset:1024 lds\n"
"v_mfma_f32_16x16x32_bf16 v[156:159], a[120:123], a[168:171], v[156:159]\n"
"v_mul_f32 v138, s45, v138\n"
"v_mul_f32 v169, s45, v169\n"
"v_mfma_f32_16x16x32_bf16 v[156:159], a[124:127], a[172:175], v[156:159]\n"
"s_mov_b64 s[80:81], s[8:9]\n"
"v_mul_f32 v125, s45, v125\n"
"v_mul_f32 v171, s45, v171\n"
"v_mfma_f32_16x16x32_bf16 v[152:155], a[112:115], a[168:171], v[152:155]\n"
"s_mov_b32 s82, s68\n"
"ds_read_b64_tr_b16 a[198:199], v243 offset:25088\n"
"ds_read_b64_tr_b16 a[208:209], v245 offset:16384\n"
"v_mfma_f32_16x16x32_bf16 v[152:155], a[116:119], a[172:175], v[152:155]\n"
"s_mov_b32 s83, 131072\n"
"buffer_load_dwordx4 v226, s[28:31], s48 offen offset:2048 lds\n"
"v_mul_f32 v116, s45, v116\n"
"v_cvt_pk_bf16_f32 v115, v118, v119\n"
"v_mul_f32 v174, s45, v174\n"
"v_xor_b32 v252, s74, v252\n"
"v_mfma_f32_16x16x32_bf16 v[148:151], a[104:107], a[168:171], v[148:151]\n"
"v_and_b32 v241, 0x3f0, v242\n"
"v_mul_f32 v128, s45, v128\n"
"v_mul_f32 v168, s45, v168\n"
"v_mfma_f32_16x16x32_bf16 v[148:151], a[108:111], a[172:175], v[148:151]\n"
"v_mul_u32_u24 v241, 10, v241\n"
"v_cvt_pk_bf16_f32 v120, v120, v121\n"
"v_mul_f32 v136, s45, v136\n"
"v_mfma_f32_16x16x32_bf16 v[144:147], a[96:99], a[168:171], v[144:147]\n"
"ds_read_b64_tr_b16 a[222:223], v246 offset:25088\n"
"v_mul_f32 v170, s45, v170\n"
"v_mfma_f32_16x16x32_bf16 v[144:147], a[100:103], a[172:175], v[144:147]\n"
"ds_read_b64_tr_b16 a[238:239], v244 offset:26112\n"
"v_xor_b32 v251, s74, v251\n"
"v_mfma_f32_16x16x32_bf16 v[176:179], a[96:99], a[176:179], v[176:179]\n"
"v_mul_f32 v218, s45, v218\n"
"v_mul_f32 v157, s45, v157\n"
"v_mfma_f32_16x16x32_bf16 v[176:179], a[100:103], a[180:183], v[176:179]\n"
"buffer_load_dwordx4 v227, s[28:31], s48 offen offset:3072 lds\n"
"v_mfma_f32_16x16x32_bf16 v[180:183], a[104:107], a[176:179], v[180:183]\n"
"s_xor_b32 s50, s50, s74\n"
"v_mul_f32 v150, s45, v150\n"
"v_mul_f32 v166, s45, v166\n"
"v_mfma_f32_16x16x32_bf16 v[180:183], a[108:111], a[180:183], v[180:183]\n"
"v_mul_f32 v153, s45, v153\n"
"v_mul_f32 v154, s45, v154\n"
"v_mfma_f32_16x16x32_bf16 v[184:187], a[112:115], a[176:179], v[184:187]\n"
"ds_read_b64_tr_b16 a[224:225], v243 offset:17408\n"
"v_mul_f32 v222, s45, v222\n"
"v_mfma_f32_16x16x32_bf16 v[184:187], a[116:119], a[180:183], v[184:187]\n"
"ds_read_b64_tr_b16 a[220:221], v246 offset:24576\n"
"v_mul_f32 v127, s45, v127\n"
"v_mfma_f32_16x16x32_bf16 v[188:191], a[120:123], a[176:179], v[188:191]\n"
"ds_read_b64_tr_b16 a[200:201], v244 offset:16384\n"
"ds_read_b64_tr_b16 a[246:247], v245 offset:26112\n"
"v_mfma_f32_16x16x32_bf16 v[188:191], a[124:127], a[180:183], v[188:191]\n"
"ds_read_b64_tr_b16 a[202:203], v244 offset:16896\n"
"v_cvt_pk_bf16_f32 v121, v122, v123\n"
"v_mul_f32 v145, s45, v145\n"
"v_mul_f32 v146, s45, v146\n"
"v_mul_f32 v155, s45, v155\n"
"v_mul_f32 v156, s45, v156\n"
"v_mul_f32 v158, s45, v158\n"
"v_mul_f32 v160, s45, v160\n"
"v_mul_f32 v179, s45, v179\n"
"v_xor_b32 v246, s74, v246\n"
"v_mfma_f32_16x16x32_bf16 v[200:203], a[144:147], a[176:179], v[200:203]\n"
"v_mul_f32 v148, s45, v148\n"
"v_mul_f32 v152, s45, v152\n"
"v_mfma_f32_16x16x32_bf16 v[200:203], a[148:151], a[180:183], v[200:203]\n"
"ds_read_b64_tr_b16 a[192:193], v243 offset:16384\n"
"v_mul_f32 v159, s45, v159\n"
"v_mfma_f32_16x16x32_bf16 v[204:207], a[152:155], a[176:179], v[204:207]\n"
"v_cvt_pk_bf16_f32 v168, v168, v169\n"
"v_xor_b32 v243, s74, v243\n"
"v_mfma_f32_16x16x32_bf16 v[204:207], a[156:159], a[180:183], v[204:207]\n"
"v_mul_f32 v142, s45, v142\n"
"v_mul_f32 v144, s45, v144\n"
"v_mfma_f32_16x16x32_bf16 v[192:195], a[128:131], a[176:179], v[192:195]\n"
"v_permlane16_swap_b32 v113, v115\n"
"v_mfma_f32_16x16x32_bf16 v[192:195], a[132:135], a[180:183], v[192:195]\n"
"v_mul_f32 v211, s45, v211\n"
"v_mul_f32 v203, s45, v203\n"
"v_mfma_f32_16x16x32_bf16 v[196:199], a[136:139], a[176:179], v[196:199]\n"
"v_mul_f32 v143, s45, v143\n"
"v_cvt_pk_bf16_f32 v152, v152, v153\n"
"v_mfma_f32_16x16x32_bf16 v[196:199], a[140:143], a[180:183], v[196:199]\n"
"s_waitcnt lgkmcnt(1)\n"
"v_mul_f32 v164, s45, v164\n"
"v_xor_b32 v244, s74, v244\n"
"v_mfma_f32_16x16x32_bf16 v[108:111], a[232:235], a[184:187], v[108:111]\n"
"v_mul_f32 v223, s45, v223\n"
"v_mul_f32 v173, s45, v173\n"
"v_mfma_f32_16x16x32_bf16 v[108:111], a[236:239], a[188:191], v[108:111]\n"
"v_cvt_pk_bf16_f32 v144, v144, v145\n"
"v_mul_f32 v202, s45, v202\n"
"v_mfma_f32_16x16x32_bf16 v[104:107], a[224:227], a[184:187], v[104:107]\n"
"s_waitcnt vmcnt(8) lgkmcnt(0)\n"
"v_mul_f32 v140, s45, v140\n"
"v_cvt_pk_bf16_f32 v153, v154, v155\n"
"v_mfma_f32_16x16x32_bf16 v[104:107], a[228:231], a[188:191], v[104:107]\n"
"s_barrier\n"
"ds_read_b64_tr_b16 a[140:141], v244 offset:9216\n"
"ds_read_b64_tr_b16 a[152:153], v246 offset:1024\n"
"v_cvt_pk_bf16_f32 v114, v116, v117\n"
"v_mul_f32 v217, s45, v217\n"
"v_mul_f32 v161, s45, v161\n"
"v_mul_f32 v177, s45, v177\n"
"v_mul_f32 v180, s45, v180\n"
"v_xor_b32 v245, s74, v245\n"
"v_mfma_f32_16x16x32_bf16 v[100:103], a[200:203], a[184:187], v[100:103]\n"
"s_mov_b32 m0, s51\n"
"v_permlane16_swap_b32 v112, v114\n"
"v_mfma_f32_16x16x32_bf16 v[100:103], a[204:207], a[188:191], v[100:103]\n"
"buffer_load_dwordx4 v247, s[24:27], s49 offen lds\n"
"v_mul_f32 v132, s45, v132\n"
"v_mul_f32 v134, s45, v134\n"
"v_mul_f32 v210, s45, v210\n"
"v_mul_f32 v149, s45, v149\n"
"v_mul_f32 v175, s45, v175\n"
"v_mul_f32 v176, s45, v176\n"
"v_mul_f32 v178, s45, v178\n"
"v_mul_f32 v184, s45, v184\n"
"v_mfma_f32_16x16x32_bf16 v[96:99], a[192:195], a[184:187], v[96:99]\n"
"v_cvt_pk_bf16_f32 v169, v170, v171\n"
"v_mul_f32 v194, s45, v194\n"
"v_mfma_f32_16x16x32_bf16 v[96:99], a[196:199], a[188:191], v[96:99]\n"
"v_mul_f32 v208, s45, v208\n"
"v_mul_f32 v105, s45, v105\n"
"v_mfma_f32_16x16x32_bf16 v[0:3], a[192:195], a[160:163], v[0:3]\n"
"ds_read_b64_tr_b16 a[108:109], v244 offset:8192\n"
"ds_read_b64_tr_b16 a[122:123], v246 offset:512\n"
"v_mfma_f32_16x16x32_bf16 v[0:3], a[196:199], a[164:167], v[0:3]\n"
"ds_read_b64_tr_b16 a[112:113], v245 offset:0\n"
"ds_read_b64_tr_b16 a[120:121], v246 offset:0\n"
"v_mfma_f32_16x16x32_bf16 v[4:7], a[200:203], a[160:163], v[4:7]\n"
"ds_read_b64_tr_b16 a[144:145], v245 offset:1024\n"
"ds_read_b64_tr_b16 a[158:159], v246 offset:9728\n"
"v_mfma_f32_16x16x32_bf16 v[4:7], a[204:207], a[164:167], v[4:7]\n"
"buffer_load_dwordx4 v253, s[24:27], s49 offen offset:1024 lds\n"
"v_mul_f32 v221, s45, v221\n"
"v_mul_f32 v147, s45, v147\n"
"v_cvt_pk_bf16_f32 v171, v174, v175\n"
"v_mul_f32 v185, s45, v185\n"
"v_mul_f32 v187, s45, v187\n"
"v_mul_f32 v201, s45, v201\n"
"v_mul_f32 v207, s45, v207\n"
"v_mul_f32 v104, s45, v104\n"
"v_mfma_f32_16x16x32_bf16 v[8:11], a[208:211], a[160:163], v[8:11]\n"
"v_cvt_pk_bf16_f32 v145, v146, v147\n"
"v_mul_f32 v172, s45, v172\n"
"v_mfma_f32_16x16x32_bf16 v[8:11], a[212:215], a[164:167], v[8:11]\n"
"v_cvt_pk_bf16_f32 v128, v128, v129\n"
"v_cvt_pk_bf16_f32 v155, v158, v159\n"
"v_mfma_f32_16x16x32_bf16 v[12:15], a[216:219], a[160:163], v[12:15]\n"
"ds_read_b64_tr_b16 a[128:129], v243 offset:1024\n"
"ds_read_b128 a[184:187], v251 offset:0\n"
"v_mfma_f32_16x16x32_bf16 v[12:15], a[220:223], a[164:167], v[12:15]\n"
"buffer_load_dwordx4 v254, s[24:27], s49 offen offset:2048 lds\n"
"v_cvt_pk_bf16_f32 v122, v124, v125\n"
"v_permlane16_swap_b32 v169, v171\n"
"v_mul_f32 v183, s45, v183\n"
"v_cvt_pk_bf16_f32 v184, v184, v185\n"
"v_mul_f32 v197, s45, v197\n"
"v_mul_f32 v199, s45, v199\n"
"v_mul_f32 v111, s45, v111\n"
"v_mfma_f32_16x16x32_bf16 v[16:19], a[224:227], a[160:163], v[16:19]\n"
"v_mul_f32 v137, s45, v137\n"
"v_mul_f32 v7, s45, v7\n"
"v_mfma_f32_16x16x32_bf16 v[16:19], a[228:231], a[164:167], v[16:19]\n"
"v_mul_f32 v193, s45, v193\n"
"v_mul_f32 v0, s45, v0\n"
"v_mfma_f32_16x16x32_bf16 v[20:23], a[232:235], a[160:163], v[20:23]\n"
"ds_read_b64_tr_b16 a[154:155], v246 offset:1536\n"
"ds_read_b64_tr_b16 a[156:157], v246 offset:9216\n"
"v_mfma_f32_16x16x32_bf16 v[20:23], a[236:239], a[164:167], v[20:23]\n"
"ds_read_b64_tr_b16 a[124:125], v246 offset:8192\n"
"ds_read_b64_tr_b16 a[136:137], v244 offset:1024\n"
"v_cvt_pk_bf16_f32 v123, v126, v127\n"
"v_mul_f32 v213, s45, v213\n"
"v_mul_f32 v162, s45, v162\n"
"v_mul_f32 v163, s45, v163\n"
"v_mul_f32 v165, s45, v165\n"
"v_cvt_pk_bf16_f32 v176, v176, v177\n"
"v_mul_f32 v188, s45, v188\n"
"v_mul_f32 v11, s45, v11\n"
"v_mfma_f32_16x16x32_bf16 v[24:27], a[240:243], a[160:163], v[24:27]\n"
"ds_read_b64_tr_b16 a[126:127], v246 offset:8704\n"
"v_mul_f32 v16, s45, v16\n"
"v_mfma_f32_16x16x32_bf16 v[24:27], a[244:247], a[164:167], v[24:27]\n"
"buffer_load_dwordx4 v255, s[24:27], s49 offen offset:3072 lds\n"
"v_mul_f32 v133, s45, v133\n"
"v_cvt_pk_bf16_f32 v129, v130, v131\n"
"v_cvt_pk_bf16_f32 v136, v136, v137\n"
"v_cvt_pk_bf16_f32 v208, v208, v209\n"
"v_cvt_pk_bf16_f32 v146, v148, v149\n"
"v_mul_f32 v181, s45, v181\n"
"v_mul_f32 v110, s45, v110\n"
"v_mul_f32 v6, s45, v6\n"
"v_mfma_f32_16x16x32_bf16 v[28:31], a[248:251], a[160:163], v[28:31]\n"
"v_cvt_pk_bf16_f32 v160, v160, v161\n"
"v_mul_f32 v13, s45, v13\n"
"v_mfma_f32_16x16x32_bf16 v[28:31], a[252:255], a[164:167], v[28:31]\n"
"s_add_u32 s40, s49, 40960\n"
"v_cvt_pk_bf16_f32 v137, v138, v139\n"
"v_mul_f32 v106, s45, v106\n"
"v_mfma_f32_16x16x32_bf16 v[60:63], a[248:251], a[168:171], v[60:63]\n"
"s_add_u32 m0, s51, 4096\n"
"ds_read_b64_tr_b16 a[132:133], v243 offset:9216\n"
"ds_read_b64_tr_b16 a[134:135], v243 offset:9728\n"
"v_mfma_f32_16x16x32_bf16 v[60:63], a[252:255], a[172:175], v[60:63]\n"
"buffer_load_dwordx4 v247, s[24:27], s40 offen lds\n"
"v_cvt_pk_bf16_f32 v130, v132, v133\n"
"v_cvt_pk_bf16_f32 v139, v142, v143\n"
"v_mul_f32 v219, s45, v219\n"
"v_mul_f32 v151, s45, v151\n"
"v_cvt_pk_bf16_f32 v177, v178, v179\n"
"v_xor_b32 v249, s74, v249\n"
"v_mfma_f32_16x16x32_bf16 v[56:59], a[240:243], a[168:171], v[56:59]\n"
"v_mul_f32 v182, s45, v182\n"
"v_mul_f32 v204, s45, v204\n"
"v_mfma_f32_16x16x32_bf16 v[56:59], a[244:247], a[172:175], v[56:59]\n"
"v_cvt_pk_bf16_f32 v161, v162, v163\n"
"v_mul_f32 v107, s45, v107\n"
"v_mfma_f32_16x16x32_bf16 v[52:55], a[232:235], a[168:171], v[52:55]\n"
"ds_read_b64_tr_b16 a[102:103], v243 offset:8704\n"
"v_cvt_pk_bf16_f32 v131, v134, v135\n"
"v_mfma_f32_16x16x32_bf16 v[52:55], a[236:239], a[172:175], v[52:55]\n"
"ds_read_b64_tr_b16 a[110:111], v244 offset:8704\n"
"ds_read_b128 a[160:163], v249 offset:0\n"
"v_mul_f32 v220, s45, v220\n"
"v_permlane16_swap_b32 v153, v155\n"
"v_mul_f32 v167, s45, v167\n"
"v_mul_f32 v195, s45, v195\n"
"v_mul_f32 v200, s45, v200\n"
"v_mul_f32 v1, s45, v1\n"
"v_mfma_f32_16x16x32_bf16 v[48:51], a[224:227], a[168:171], v[48:51]\n"
"v_mul_f32 v3, s45, v3\n"
"v_mul_f32 v30, s45, v30\n"
"v_mfma_f32_16x16x32_bf16 v[48:51], a[228:231], a[172:175], v[48:51]\n"
"buffer_load_dwordx4 v253, s[24:27], s40 offen offset:1024 lds\n"
"v_cvt_pk_bf16_f32 v163, v166, v167\n"
"v_accvgpr_write_b32 a31, v171\n"
"v_cvt_pk_bf16_f32 v179, v182, v183\n"
"v_mul_f32 v186, s45, v186\n"
"v_mul_f32 v189, s45, v189\n"
"v_cvt_pk_bf16_f32 v185, v186, v187\n"
"v_mul_f32 v196, s45, v196\n"
"v_mul_f32 v206, s45, v206\n"
"v_mfma_f32_16x16x32_bf16 v[44:47], a[216:219], a[168:171], v[44:47]\n"
"v_cvt_pk_bf16_f32 v162, v164, v165\n"
"v_mul_f32 v9, s45, v9\n"
"v_mfma_f32_16x16x32_bf16 v[44:47], a[220:223], a[172:175], v[44:47]\n"
"v_permlane16_swap_b32 v129, v131\n"
"v_permlane16_swap_b32 v137, v139\n"
"v_permlane16_swap_b32 v160, v162\n"
"v_cvt_pk_bf16_f32 v178, v180, v181\n"
"v_mul_f32 v198, s45, v198\n"
"v_mul_f32 v8, s45, v8\n"
"v_mul_f32 v29, s45, v29\n"
"v_mfma_f32_16x16x32_bf16 v[40:43], a[208:211], a[168:171], v[40:43]\n"
"ds_read_b64_tr_b16 a[98:99], v243 offset:512\n"
"v_mul_f32 v28, s45, v28\n"
"v_mfma_f32_16x16x32_bf16 v[40:43], a[212:215], a[172:175], v[40:43]\n"
"buffer_load_dwordx4 v254, s[24:27], s40 offen offset:2048 lds\n"
"v_permlane16_swap_b32 v120, v122\n"
"v_permlane16_swap_b32 v144, v146\n"
"v_permlane16_swap_b32 v161, v163\n"
"v_mul_f32 v17, s45, v17\n"
"v_mul_f32 v20, s45, v20\n"
"v_mfma_f32_16x16x32_bf16 v[36:39], a[200:203], a[168:171], v[36:39]\n"
"s_add_u32 s49, s49, 128\n"
"v_cvt_pk_bf16_f32 v170, v172, v173\n"
"v_mul_f32 v22, s45, v22\n"
"v_mfma_f32_16x16x32_bf16 v[36:39], a[204:207], a[172:175], v[36:39]\n"
"s_sub_u32 s41, s49, 1280\n"
"v_accvgpr_write_b32 a9, v129\n"
"v_cvt_pk_bf16_f32 v186, v188, v189\n"
"v_mfma_f32_16x16x32_bf16 v[32:35], a[192:195], a[168:171], v[32:35]\n"
"s_min_u32 s49, s49, s41\n"
"ds_read_b64_tr_b16 a[150:151], v245 offset:9728\n"
"v_cvt_pk_bf16_f32 v138, v140, v141\n"
"v_mfma_f32_16x16x32_bf16 v[32:35], a[196:199], a[172:175], v[32:35]\n"
"s_xor_b32 s51, s51, s74\n"
"ds_read_b64_tr_b16 a[142:143], v244 offset:9728\n"
"ds_read_b64_tr_b16 a[148:149], v245 offset:9216\n"
"v_cvt_pk_bf16_f32 v147, v150, v151\n"
"v_accvgpr_write_b32 a23, v155\n"
"v_accvgpr_write_b32 a26, v162\n"
"v_accvgpr_write_b32 a29, v169\n"
"v_mul_f32 v98, s45, v98\n"
"v_mul_f32 v101, s45, v101\n"
"v_mul_f32 v109, s45, v109\n"
"v_mul_f32 v10, s45, v10\n"
"v_mfma_f32_16x16x32_bf16 v[64:67], a[192:195], a[176:179], v[64:67]\n"
"s_mov_b32 m0, 139264\n"
"ds_read_b128 a[168:171], v249 offset:2048\n"
"v_mul_f32 v49, s45, v49\n"
"v_mfma_f32_16x16x32_bf16 v[64:67], a[196:199], a[180:183], v[64:67]\n"
"buffer_load_dword v241, s[80:83], s63 offen lds\n"
"v_permlane16_swap_b32 v176, v178\n"
"v_permlane16_swap_b32 v184, v186\n"
"v_mul_f32 v18, s45, v18\n"
"v_mul_f32 v26, s45, v26\n"
"v_mfma_f32_16x16x32_bf16 v[68:71], a[200:203], a[176:179], v[68:71]\n"
"v_cvt_pk_bf16_f32 v154, v156, v157\n"
"v_mul_f32 v15, s45, v15\n"
"v_mfma_f32_16x16x32_bf16 v[68:71], a[204:207], a[180:183], v[68:71]\n"
"v_permlane16_swap_b32 v121, v123\n"
"v_permlane16_swap_b32 v136, v138\n"
"v_permlane16_swap_b32 v168, v170\n"
"v_mul_f32 v102, s45, v102\n"
"v_cvt_pk_bf16_f32 v104, v104, v105\n"
"v_mul_f32 v24, s45, v24\n"
"v_mul_f32 v59, s45, v59\n"
"v_mfma_f32_16x16x32_bf16 v[72:75], a[208:211], a[176:179], v[72:75]\n"
"ds_read_b64_tr_b16 a[114:115], v245 offset:512\n"
"ds_read_b64_tr_b16 a[146:147], v245 offset:1536\n"
"v_mfma_f32_16x16x32_bf16 v[72:75], a[212:215], a[180:183], v[72:75]\n"
"buffer_load_dword v241, s[80:83], s63 offen offset:80 lds\n"
"v_accvgpr_write_b32 a0, v112\n"
"v_accvgpr_write_b32 a4, v120\n"
"v_accvgpr_write_b32 a6, v122\n"
"v_permlane16_swap_b32 v128, v130\n"
"v_xor_b32 v250, s74, v250\n"
"v_mul_f32 v44, s45, v44\n"
"v_mfma_f32_16x16x32_bf16 v[76:79], a[216:219], a[176:179], v[76:79]\n"
"v_cvt_pk_bf16_f32 v0, v0, v1\n"
"v_mul_f32 v27, s45, v27\n"
"v_mfma_f32_16x16x32_bf16 v[76:79], a[220:223], a[180:183], v[76:79]\n"
"v_accvgpr_write_b32 a12, v136\n"
"v_mul_f32 v216, s45, v216\n"
"v_mfma_f32_16x16x32_bf16 v[88:91], a[240:243], a[176:179], v[88:91]\n"
"ds_read_b64_tr_b16 a[104:105], v244 offset:0\n"
"ds_read_b64_tr_b16 a[138:139], v244 offset:1536\n"
"v_mfma_f32_16x16x32_bf16 v[88:91], a[244:247], a[180:183], v[88:91]\n"
"ds_read_b128 a[164:167], v250 offset:0\n"
"v_accvgpr_write_b32 a1, v113\n"
"v_accvgpr_write_b32 a3, v115\n"
"v_accvgpr_write_b32 a13, v137\n"
"v_accvgpr_write_b32 a14, v138\n"
"v_mul_f32 v190, s45, v190\n"
"v_mul_f32 v191, s45, v191\n"
"v_cvt_pk_bf16_f32 v200, v200, v201\n"
"v_mul_f32 v62, s45, v62\n"
"v_mfma_f32_16x16x32_bf16 v[92:95], a[248:251], a[176:179], v[92:95]\n"
"ds_read_b64_tr_b16 a[96:97], v243 offset:0\n"
"v_accvgpr_write_b32 a2, v114\n"
"v_mfma_f32_16x16x32_bf16 v[92:95], a[252:255], a[180:183], v[92:95]\n"
"ds_read_b64_tr_b16 a[100:101], v243 offset:8192\n"
"ds_read_b64_tr_b16 a[116:117], v245 offset:8192\n"
"v_accvgpr_write_b32 a7, v123\n"
"v_cvt_pk_bf16_f32 v8, v8, v9\n"
"v_mul_f32 v19, s45, v19\n"
"v_mul_f32 v50, s45, v50\n"
"v_mul_f32 v57, s45, v57\n"
"v_mul_f32 v72, s45, v72\n"
"v_mul_f32 v73, s45, v73\n"
"v_mul_f32 v74, s45, v74\n"
"v_mfma_f32_16x16x32_bf16 v[80:83], a[224:227], a[176:179], v[80:83]\n"
"ds_read_b64_tr_b16 a[106:107], v244 offset:512\n"
"v_mul_f32 v14, s45, v14\n"
"v_mfma_f32_16x16x32_bf16 v[80:83], a[228:231], a[180:183], v[80:83]\n"
"ds_read_b64_tr_b16 a[118:119], v245 offset:8704\n"
"ds_read_b64_tr_b16 a[130:131], v243 offset:1536\n"
"v_cvt_pk_bf16_f32 v216, v216, v217\n"
"v_permlane16_swap_b32 v145, v147\n"
"v_accvgpr_write_b32 a34, v178\n"
"v_cvt_pk_bf16_f32 v201, v202, v203\n"
"v_mul_f32 v2, s45, v2\n"
"v_mul_f32 v23, s45, v23\n"
"v_cvt_pk_bf16_f32 v16, v16, v17\n"
"v_mfma_f32_16x16x32_bf16 v[84:87], a[232:235], a[176:179], v[84:87]\n"
"s_add_u32 s47, s47, 1\n"
"v_mul_f32 v108, s45, v108\n"
"v_mul_f32 v64, s45, v64\n"
"v_mfma_f32_16x16x32_bf16 v[84:87], a[236:239], a[180:183], v[84:87]\n"
"s_waitcnt lgkmcnt(5)\n"
"ds_read_b128 a[172:175], v250 offset:2048\n"
"v_accvgpr_write_b32 a8, v128\n"
"v_accvgpr_write_b32 a24, v160\n"
"v_mul_f32 v205, s45, v205\n"
"v_mul_f32 v4, s45, v4\n"
"v_mul_f32 v25, s45, v25\n"
"v_mul_f32 v39, s45, v39\n"
"v_mul_f32 v45, s45, v45\n"
"v_mul_f32 v52, s45, v52\n"
"v_mul_f32 v63, s45, v63\n"
"v_mfma_f32_16x16x32_bf16 v[112:115], a[96:99], a[160:163], 0\n"
"s_waitcnt lgkmcnt(5)\n"
"v_accvgpr_write_b32 a11, v131\n"
"v_accvgpr_write_b32 a18, v146\n"
"v_mfma_f32_16x16x32_bf16 v[112:115], a[100:103], a[164:167], v[112:115]\n"
"s_waitcnt lgkmcnt(3)\n"
"ds_read_b128 a[176:179], v249 offset:4096\n"
"v_accvgpr_write_b32 a5, v121\n"
"v_accvgpr_write_b32 a10, v130\n"
"v_permlane16_swap_b32 v177, v179\n"
"v_cvt_pk_bf16_f32 v203, v206, v207\n"
"v_mul_f32 v97, s45, v97\n"
"v_cvt_pk_bf16_f32 v1, v2, v3\n"
"v_mul_f32 v12, s45, v12\n"
"v_mul_f32 v48, s45, v48\n"
"v_mfma_f32_16x16x32_bf16 v[116:119], a[104:107], a[160:163], 0\n"
"s_waitcnt vmcnt(13) lgkmcnt(2)\n"
"v_mul_f32 v99, s45, v99\n"
"v_mul_f32 v56, s45, v56\n"
"v_mfma_f32_16x16x32_bf16 v[116:119], a[108:111], a[164:167], v[116:119]\n"
"s_barrier\n"
"ds_read_b128 a[180:183], v250 offset:4096\n"
"v_accvgpr_write_b32 a28, v168\n"
"v_accvgpr_write_b32 a38, v186\n"
"v_cvt_pk_bf16_f32 v202, v204, v205\n"
"v_mul_f32 v42, s45, v42\n"
"v_mul_f32 v51, s45, v51\n"
"v_mul_f32 v60, s45, v60\n"
"v_mul_f32 v66, s45, v66\n"
"v_mfma_f32_16x16x32_bf16 v[120:123], a[112:115], a[160:163], 0\n"
"s_add_u32 s40, s48, 256\n"
"s_xor_b32 m0, s50, 0xc000\n"
"ds_read_b64_tr_b16 a[222:223], v246 offset:25088\n"
"ds_read_b64_tr_b16 a[224:225], v243 offset:17408\n"
"v_mul_f32 v215, s45, v215\n"
"v_mul_f32 v32, s45, v32\n"
"v_mul_f32 v54, s45, v54\n"
"v_mul_f32 v58, s45, v58\n"
"v_mul_f32 v79, s45, v79\n"
"v_mul_f32 v87, s45, v87\n"
"v_mfma_f32_16x16x32_bf16 v[120:123], a[116:119], a[164:167], v[120:123]\n"
"buffer_load_dwordx4 v224, s[28:31], s40 offen lds\n"
"v_accvgpr_write_b32 a15, v139\n"
"v_accvgpr_write_b32 a17, v145\n"
"v_cvt_pk_bf16_f32 v187, v190, v191\n"
"v_mul_f32 v86, s45, v86\n"
"v_mfma_f32_16x16x32_bf16 v[124:127], a[120:123], a[160:163], 0\n"
"v_mul_f32 v5, s45, v5\n"
"v_mul_f32 v36, s45, v36\n"
"v_mfma_f32_16x16x32_bf16 v[124:127], a[124:127], a[164:167], v[124:127]\n"
"v_cvt_pk_bf16_f32 v209, v210, v211\n"
"v_cvt_pk_bf16_f32 v217, v218, v219\n"
"v_permlane16_swap_b32 v201, v203\n"
"v_mul_f32 v33, s45, v33\n"
"v_cvt_pk_bf16_f32 v56, v56, v57\n"
"v_cvt_pk_bf16_f32 v57, v58, v59\n"
"v_mul_f32 v65, s45, v65\n"
"v_mul_f32 v75, s45, v75\n"
"v_mul_f32 v84, s45, v84\n"
"v_mfma_f32_16x16x32_bf16 v[128:131], a[128:131], a[160:163], 0\n"
"ds_read_b64_tr_b16 a[240:241], v245 offset:17408\n"
"v_mul_f32 v53, s45, v53\n"
"v_mfma_f32_16x16x32_bf16 v[128:131], a[132:135], a[164:167], v[128:131]\n"
"ds_read_b64_tr_b16 a[202:203], v244 offset:16896\n"
"ds_read_b64_tr_b16 a[220:221], v246 offset:24576\n"
"v_accvgpr_write_b32 a21, v153\n"
"v_accvgpr_write_b32 a25, v161\n"
"v_accvgpr_write_b32 a30, v170\n"
"v_mul_f32 v192, s45, v192\n"
"v_mul_f32 v21, s45, v21\n"
"v_mul_f32 v35, s45, v35\n"
"v_mul_f32 v47, s45, v47\n"
"v_mul_f32 v71, s45, v71\n"
"v_mfma_f32_16x16x32_bf16 v[132:135], a[136:139], a[160:163], 0\n"
"ds_read_b64_tr_b16 a[234:235], v244 offset:17920\n"
"v_accvgpr_write_b32 a33, v177\n"
"v_mfma_f32_16x16x32_bf16 v[132:135], a[140:143], a[164:167], v[132:135]\n"
"buffer_load_dwordx4 v225, s[28:31], s40 offen offset:1024 lds\n"
"v_mul_f32 v212, s45, v212\n"
"v_accvgpr_write_b32 a32, v176\n"
"v_cvt_pk_bf16_f32 v192, v192, v193\n"
"v_cvt_pk_bf16_f32 v105, v106, v107\n"
"v_cvt_pk_bf16_f32 v24, v24, v25\n"
"v_mul_f32 v34, s45, v34\n"
"v_mul_f32 v61, s45, v61\n"
"v_mfma_f32_16x16x32_bf16 v[136:139], a[144:147], a[160:163], 0\n"
"v_accvgpr_write_b32 a16, v144\n"
"v_cvt_pk_bf16_f32 v193, v194, v195\n"
"v_mfma_f32_16x16x32_bf16 v[136:139], a[148:151], a[164:167], v[136:139]\n"
"ds_read_b64_tr_b16 a[232:233], v244 offset:17408\n"
"ds_read_b64_tr_b16 a[238:239], v244 offset:26112\n"
"ds_read_b64_tr_b16 a[252:253], v246 offset:25600\n"
"v_cvt_pk_bf16_f32 v211, v214, v215\n"
"v_permlane16_swap_b32 v152, v154\n"
"v_accvgpr_write_b32 a27, v163\n"
"v_cvt_pk_bf16_f32 v17, v18, v19\n"
"v_mul_f32 v88, s45, v88\n"
"v_mfma_f32_16x16x32_bf16 v[140:143], a[152:155], a[160:163], 0\n"
"ds_read_b64_tr_b16 a[254:255], v246 offset:26112\n"
"v_mul_f32 v81, s45, v81\n"
"v_mfma_f32_16x16x32_bf16 v[140:143], a[156:159], a[164:167], v[140:143]\n"
"buffer_load_dwordx4 v226, s[28:31], s40 offen offset:2048 lds\n"
"v_cvt_pk_bf16_f32 v219, v222, v223\n"
"v_accvgpr_write_b32 a19, v147\n"
"v_permlane16_swap_b32 v185, v187\n"
"v_cvt_pk_bf16_f32 v3, v6, v7\n"
"v_cvt_pk_bf16_f32 v32, v32, v33\n"
"v_mul_f32 v68, s45, v68\n"
"v_mul_f32 v89, s45, v89\n"
"v_mfma_f32_16x16x32_bf16 v[172:175], a[152:155], a[168:171], 0\n"
"s_waitcnt lgkmcnt(7)\n"
"v_accvgpr_write_b32 a35, v179\n"
"v_cvt_pk_bf16_f32 v25, v26, v27\n"
"v_mfma_f32_16x16x32_bf16 v[172:175], a[156:159], a[172:175], v[172:175]\n"
"ds_read_b64_tr_b16 a[218:219], v246 offset:16896\n"
"ds_read_b64_tr_b16 a[230:231], v243 offset:26112\n"
"v_accvgpr_write_b32 a22, v154\n"
"v_accvgpr_write_b32 a47, v203\n"
"v_mul_f32 v96, s45, v96\n"
"v_cvt_pk_bf16_f32 v26, v28, v29\n"
"v_cvt_pk_bf16_f32 v48, v48, v49\n"
"v_mul_f32 v69, s45, v69\n"
"v_mul_f32 v77, s45, v77\n"
"v_mul_f32 v94, s45, v94\n"
"v_mfma_f32_16x16x32_bf16 v[168:171], a[144:147], a[168:171], 0\n"
"ds_read_b128 a[188:191], v252 offset:0\n"
"v_accvgpr_write_b32 a37, v185\n"
"v_mfma_f32_16x16x32_bf16 v[168:171], a[148:151], a[172:175], v[168:171]\n"
"ds_read_b64_tr_b16 a[194:195], v243 offset:16896\n"
"ds_read_b64_tr_b16 a[214:215], v245 offset:25088\n"
"v_accvgpr_write_b32 a20, v152\n"
"v_cvt_pk_bf16_f32 v2, v4, v5\n"
"v_cvt_pk_bf16_f32 v33, v34, v35\n"
"v_mul_f32 v40, s45, v40\n"
"v_mul_f32 v46, s45, v46\n"
"v_cvt_pk_bf16_f32 v59, v62, v63\n"
"v_mul_f32 v67, s45, v67\n"
"v_mul_f32 v93, s45, v93\n"
"v_mfma_f32_16x16x32_bf16 v[164:167], a[136:139], a[168:171], 0\n"
"ds_read_b64_tr_b16 a[204:205], v244 offset:24576\n"
"ds_read_b64_tr_b16 a[244:245], v245 offset:25600\n"
"v_mfma_f32_16x16x32_bf16 v[164:167], a[140:143], a[172:175], v[164:167]\n"
"buffer_load_dwordx4 v227, s[28:31], s40 offen offset:3072 lds\n"
"v_permlane16_swap_b32 v217, v219\n"
"v_accvgpr_write_b32 a36, v184\n"
"v_cvt_pk_bf16_f32 v195, v198, v199\n"
"v_cvt_pk_bf16_f32 v9, v10, v11\n"
"v_cvt_pk_bf16_f32 v64, v64, v65\n"
"v_mul_f32 v91, s45, v91\n"
"v_cvt_pk_bf16_f32 v88, v88, v89\n"
"v_mfma_f32_16x16x32_bf16 v[160:163], a[128:131], a[168:171], 0\n"
"s_cmp_eq_u32 s47, 8\ns_cselect_b32 s41, 524800, s58\n"
"v_mul_f32 v37, s45, v37\n"
"v_cvt_pk_bf16_f32 v58, v60, v61\n"
"v_mfma_f32_16x16x32_bf16 v[160:163], a[132:135], a[172:175], v[160:163]\n"
"s_add_u32 s48, s48, s41\n"
"ds_read_b64_tr_b16 a[192:193], v243 offset:16384\n"
"ds_read_b64_tr_b16 a[210:211], v245 offset:16896\n"
"v_accvgpr_write_b32 a39, v187\n"
"v_permlane16_swap_b32 v200, v202\n"
"v_accvgpr_write_b32 a44, v200\n"
"v_accvgpr_write_b32 a45, v201\n"
"v_permlane16_swap_b32 v0, v2\n"
"v_mul_f32 v43, s45, v43\n"
"v_mfma_f32_16x16x32_bf16 v[156:159], a[120:123], a[168:171], 0\n"
"s_sub_u32 s41, s48, 5242880\n"
"ds_read_b64_tr_b16 a[198:199], v243 offset:25088\n"
"v_cvt_pk_bf16_f32 v210, v212, v213\n"
"v_mfma_f32_16x16x32_bf16 v[156:159], a[124:127], a[172:175], v[156:159]\n"
"s_min_u32 s48, s48, s41\n"
"ds_read_b64_tr_b16 a[206:207], v244 offset:25088\n"
"ds_read_b64_tr_b16 a[208:209], v245 offset:16384\n"
"v_cvt_pk_bf16_f32 v107, v110, v111\n"
"v_cvt_pk_bf16_f32 v11, v14, v15\n"
"v_mul_f32 v55, s45, v55\n"
"v_mul_f32 v70, s45, v70\n"
"v_cvt_pk_bf16_f32 v65, v66, v67\n"
"v_cvt_pk_bf16_f32 v72, v72, v73\n"
"v_mul_f32 v82, s45, v82\n"
"v_mfma_f32_16x16x32_bf16 v[152:155], a[112:115], a[168:171], 0\n"
"s_mov_b32 m0, s50\n"
"ds_read_b64_tr_b16 a[228:229], v243 offset:25600\n"
"v_cvt_pk_bf16_f32 v19, v22, v23\n"
"v_mfma_f32_16x16x32_bf16 v[152:155], a[116:119], a[172:175], v[152:155]\n"
"buffer_load_dwordx4 v224, s[28:31], s48 offen lds\n"
"v_cvt_pk_bf16_f32 v218, v220, v221\n"
"v_cvt_pk_bf16_f32 v18, v20, v21\n"
"v_permlane16_swap_b32 v24, v26\n"
"v_mul_f32 v41, s45, v41\n"
"v_mul_f32 v83, s45, v83\n"
"v_mul_f32 v90, s45, v90\n"
"v_mfma_f32_16x16x32_bf16 v[148:151], a[104:107], a[168:171], 0\n"
"v_permlane16_swap_b32 v193, v195\n"
"v_mfma_f32_16x16x32_bf16 v[148:151], a[108:111], a[172:175], v[148:151]\n"
"v_permlane16_swap_b32 v216, v218\n"
"v_cvt_pk_bf16_f32 v194, v196, v197\n"
"v_permlane16_swap_b32 v1, v3\n"
"v_permlane16_swap_b32 v9, v11\n"
"v_permlane16_swap_b32 v17, v19\n"
"v_mul_f32 v95, s45, v95\n"
"v_mfma_f32_16x16x32_bf16 v[144:147], a[96:99], a[168:171], 0\n"
"ds_read_b64_tr_b16 a[212:213], v245 offset:24576\n"
"v_mul_f32 v31, s45, v31\n"
"v_mfma_f32_16x16x32_bf16 v[144:147], a[100:103], a[172:175], v[144:147]\n"
"buffer_load_dwordx4 v225, s[28:31], s48 offen offset:1024 lds\n"
"v_permlane16_swap_b32 v192, v194\n"
"v_accvgpr_write_b32 a48, v0\n"
"v_permlane16_swap_b32 v57, v59\n"
"v_accvgpr_write_b32 a77, v57\n"
"v_cvt_pk_bf16_f32 v66, v68, v69\n"
"v_xor_b32 v250, s74, v250\n"
"v_mfma_f32_16x16x32_bf16 v[176:179], a[96:99], a[176:179], 0\n"
"v_mul_f32 v80, s45, v80\n"
"v_cvt_pk_bf16_f32 v89, v90, v91\n"
"v_mfma_f32_16x16x32_bf16 v[176:179], a[100:103], a[180:183], v[176:179]\n"
"v_accvgpr_write_b32 a40, v192\n"
"v_accvgpr_write_b32 a51, v3\n"
"v_permlane16_swap_b32 v16, v18\n"
"v_mul_f32 v38, s45, v38\n"
"v_cvt_pk_bf16_f32 v34, v36, v37\n"
"v_cvt_pk_bf16_f32 v49, v50, v51\n"
"v_permlane16_swap_b32 v56, v58\n"
"v_cvt_pk_bf16_f32 v80, v80, v81\n"
"v_mfma_f32_16x16x32_bf16 v[180:183], a[104:107], a[176:179], 0\n"
"ds_read_b64_tr_b16 a[226:227], v243 offset:17920\n"
"v_cvt_pk_bf16_f32 v27, v30, v31\n"
"v_mfma_f32_16x16x32_bf16 v[180:183], a[108:111], a[180:183], v[180:183]\n"
"buffer_load_dwordx4 v226, s[28:31], s48 offen offset:2048 lds\n"
"v_permlane16_swap_b32 v209, v211\n"
"v_accvgpr_write_b32 a43, v195\n"
"v_accvgpr_write_b32 a50, v2\n"
"v_accvgpr_write_b32 a53, v9\n"
"v_permlane16_swap_b32 v25, v27\n"
"v_mfma_f32_16x16x32_bf16 v[184:187], a[112:115], a[176:179], 0\n"
"v_accvgpr_write_b32 a41, v193\n"
"v_accvgpr_write_b32 a60, v24\n"
"v_mfma_f32_16x16x32_bf16 v[184:187], a[116:119], a[180:183], v[184:187]\n"
"v_accvgpr_write_b32 a46, v202\n"
"v_cvt_pk_bf16_f32 v35, v38, v39\n"
"v_mfma_f32_16x16x32_bf16 v[188:191], a[120:123], a[176:179], 0\n"
"v_cvt_pk_bf16_f32 v73, v74, v75\n"
"v_cvt_pk_bf16_f32 v91, v94, v95\n"
"v_mfma_f32_16x16x32_bf16 v[188:191], a[124:127], a[180:183], v[188:191]\n"
"ds_write_b128 v248, v[216:219] offset:2048\n"
"v_cvt_pk_bf16_f32 v96, v96, v97\n"
"v_permlane16_swap_b32 v105, v107\n"
"v_cvt_pk_bf16_f32 v10, v12, v13\n"
"v_accvgpr_write_b32 a59, v19\n"
"v_mfma_f32_16x16x32_bf16 v[200:203], a[144:147], a[176:179], 0\n"
"v_accvgpr_write_b32 a61, v25\n"
"v_cvt_pk_bf16_f32 v67, v70, v71\n"
"v_mfma_f32_16x16x32_bf16 v[200:203], a[148:151], a[180:183], v[200:203]\n"
"buffer_load_dwordx4 v227, s[28:31], s48 offen offset:3072 lds\n"
"v_permlane16_swap_b32 v208, v210\n"
"v_accvgpr_write_b32 a42, v194\n"
"v_permlane16_swap_b32 v8, v10\n"
"v_accvgpr_write_b32 a54, v10\n"
"v_accvgpr_write_b32 a63, v27\n"
"v_cvt_pk_bf16_f32 v51, v54, v55\n"
"v_mfma_f32_16x16x32_bf16 v[204:207], a[152:155], a[176:179], 0\n"
"s_xor_b32 s50, s50, s74\n"
"v_cvt_pk_bf16_f32 v40, v40, v41\n"
"v_cvt_pk_bf16_f32 v81, v82, v83\n"
"v_mfma_f32_16x16x32_bf16 v[204:207], a[156:159], a[180:183], v[204:207]\n"
"v_accvgpr_write_b32 a57, v17\n"
"v_mul_f32 v76, s45, v76\n"
"v_mfma_f32_16x16x32_bf16 v[192:195], a[128:131], a[176:179], 0\n"
"ds_read_b64_tr_b16 a[248:249], v246 offset:17408\n"
"v_accvgpr_write_b32 a49, v1\n"
"v_mfma_f32_16x16x32_bf16 v[192:195], a[132:135], a[180:183], v[192:195]\n"
"ds_write_b128 v248, v[208:211] offset:0\n"
"v_cvt_pk_bf16_f32 v106, v108, v109\n"
"v_accvgpr_write_b32 a62, v26\n"
"v_cvt_pk_bf16_f32 v50, v52, v53\n"
"v_mfma_f32_16x16x32_bf16 v[196:199], a[136:139], a[176:179], 0\n"
"v_permlane16_swap_b32 v33, v35\n"
"v_mfma_f32_16x16x32_bf16 v[196:199], a[140:143], a[180:183], v[196:199]\n"
"ds_read_b64_tr_b16 a[242:243], v245 offset:17920\n"
"ds_read_b64_tr_b16 a[246:247], v245 offset:26112\n"
"v_mfma_f32_16x16x32_bf16 v[220:223], a[136:139], a[184:187], 0\n"
"s_waitcnt lgkmcnt(4)\n"
"ds_read_b64_tr_b16 a[200:201], v244 offset:16384\n"
"v_cvt_pk_bf16_f32 v41, v42, v43\n"
"v_mfma_f32_16x16x32_bf16 v[220:223], a[140:143], a[188:191], v[220:223]\n"
"ds_read_b64_tr_b16 a[196:197], v243 offset:24576\n"
"v_mul_f32 v103, s45, v103\n"
"v_mfma_f32_16x16x32_bf16 v[216:219], a[128:131], a[184:187], 0\n"
"ds_read_b64_tr_b16 a[236:237], v244 offset:25600\n"
"v_cvt_pk_bf16_f32 v74, v76, v77\n"
"v_mfma_f32_16x16x32_bf16 v[216:219], a[132:135], a[188:191], v[216:219]\n"
"ds_read_b64_tr_b16 a[216:217], v246 offset:16384\n"
"ds_read_b64_tr_b16 a[250:251], v246 offset:17920\n"
"v_mfma_f32_16x16x32_bf16 v[212:215], a[104:107], a[184:187], 0\n"
"v_mul_f32 v100, s45, v100\n"
"v_accvgpr_write_b32 a55, v11\n"
"v_mfma_f32_16x16x32_bf16 v[212:215], a[108:111], a[188:191], v[212:215]\n"
"v_mul_f32 v92, s45, v92\n"
"v_xor_b32 v246, s74, v246\n"
"v_mfma_f32_16x16x32_bf16 v[208:211], a[96:99], a[184:187], 0\n"
"v_mul_f32 v85, s45, v85\n"
"v_cvt_pk_bf16_f32 v82, v84, v85\n"
"v_mfma_f32_16x16x32_bf16 v[208:211], a[100:103], a[188:191], v[208:211]\n"
"v_cvt_pk_bf16_f32 v42, v44, v45\n"
"v_xor_b32 v243, s74, v243\n"
"v_mfma_f32_16x16x32_bf16 v[0:3], a[192:195], a[160:163], 0\n"
"s_waitcnt lgkmcnt(3)\n"
"v_accvgpr_write_b32 a52, v8\n"
"v_accvgpr_write_b32 a76, v56\n"
"v_mfma_f32_16x16x32_bf16 v[0:3], a[196:199], a[164:167], v[0:3]\n"
"v_cvt_pk_bf16_f32 v97, v98, v99\n"
"v_permlane16_swap_b32 v40, v42\n"
"v_permlane16_swap_b32 v49, v51\n"
"v_permlane16_swap_b32 v65, v67\n"
"v_permlane16_swap_b32 v89, v91\n"
"v_xor_b32 v245, s74, v245\n"
"v_mfma_f32_16x16x32_bf16 v[4:7], a[200:203], a[160:163], 0\n"
"s_waitcnt vmcnt(10) lgkmcnt(0)\n"
"v_cvt_pk_bf16_f32 v43, v46, v47\n"
"v_accvgpr_write_b32 a78, v58\n"
"v_mfma_f32_16x16x32_bf16 v[4:7], a[204:207], a[164:167], v[4:7]\n"
"s_barrier\n"
"ds_read_b64_tr_b16 a[126:127], v246 offset:8704\n"
"v_accvgpr_write_b32 a58, v18\n"
"v_permlane16_swap_b32 v32, v34\n"
"v_permlane16_swap_b32 v41, v43\n"
"v_accvgpr_write_b32 a95, v91\n"
"v_mfma_f32_16x16x32_bf16 v[8:11], a[208:211], a[160:163], 0\n"
"s_mov_b32 m0, s51\n"
"v_accvgpr_write_b32 a56, v16\n"
"v_accvgpr_write_b32 a75, v51\n"
"v_mfma_f32_16x16x32_bf16 v[8:11], a[212:215], a[164:167], v[8:11]\n"
"buffer_load_dwordx4 v247, s[24:27], s49 offen lds\n"
"v_accvgpr_write_b32 a65, v33\n"
"v_accvgpr_write_b32 a66, v34\n"
"v_permlane16_swap_b32 v48, v50\n"
"v_accvgpr_write_b32 a72, v48\n"
"v_accvgpr_write_b32 a79, v59\n"
"v_cvt_pk_bf16_f32 v83, v86, v87\n"
"v_mfma_f32_16x16x32_bf16 v[12:15], a[216:219], a[160:163], 0\n"
"v_permlane16_swap_b32 v104, v106\n"
"v_mfma_f32_16x16x32_bf16 v[12:15], a[220:223], a[164:167], v[12:15]\n"
"v_cvt_pk_bf16_f32 v98, v100, v101\n"
"v_accvgpr_write_b32 a69, v41\n"
"v_mfma_f32_16x16x32_bf16 v[16:19], a[224:227], a[160:163], 0\n"
"ds_read_b64_tr_b16 a[148:149], v245 offset:9216\n"
"v_cvt_pk_bf16_f32 v90, v92, v93\n"
"v_mfma_f32_16x16x32_bf16 v[16:19], a[228:231], a[164:167], v[16:19]\n"
"v_accvgpr_write_b32 a68, v40\n"
"v_accvgpr_write_b32 a73, v49\n"
"v_mfma_f32_16x16x32_bf16 v[20:23], a[232:235], a[160:163], 0\n"
"ds_read_b64_tr_b16 a[122:123], v246 offset:512\n"
"ds_read_b64_tr_b16 a[130:131], v243 offset:1536\n"
"v_mfma_f32_16x16x32_bf16 v[20:23], a[236:239], a[164:167], v[20:23]\n"
"buffer_load_dwordx4 v253, s[24:27], s49 offen offset:1024 lds\n"
"v_mfma_f32_16x16x32_bf16 v[24:27], a[240:243], a[160:163], 0\n"
"v_mul_f32 v78, s45, v78\n"
"v_xor_b32 v244, s74, v244\n"
"v_mfma_f32_16x16x32_bf16 v[24:27], a[244:247], a[164:167], v[24:27]\n"
"ds_write_b128 v248, v[104:107] offset:6144\n"
"v_accvgpr_write_b32 a83, v67\n"
"v_permlane16_swap_b32 v72, v74\n"
"v_permlane16_swap_b32 v81, v83\n"
"v_mfma_f32_16x16x32_bf16 v[28:31], a[248:251], a[160:163], 0\n"
"ds_read_b64_tr_b16 a[102:103], v243 offset:8704\n"
"v_accvgpr_write_b32 a71, v43\n"
"v_mfma_f32_16x16x32_bf16 v[28:31], a[252:255], a[164:167], v[28:31]\n"
"buffer_load_dwordx4 v254, s[24:27], s49 offen offset:2048 lds\n"
"v_mfma_f32_16x16x32_bf16 v[60:63], a[248:251], a[168:171], 0\n"
"v_permlane16_swap_b32 v64, v66\n"
"v_mfma_f32_16x16x32_bf16 v[60:63], a[252:255], a[172:175], v[60:63]\n"
"v_accvgpr_write_b32 a74, v50\n"
"v_cvt_pk_bf16_f32 v75, v78, v79\n"
"v_mfma_f32_16x16x32_bf16 v[56:59], a[240:243], a[168:171], 0\n"
"ds_read_b64_tr_b16 a[128:129], v243 offset:1024\n"
"v_accvgpr_write_b32 a64, v32\n"
"v_mfma_f32_16x16x32_bf16 v[56:59], a[244:247], a[172:175], v[56:59]\n"
"ds_read_b64_tr_b16 a[124:125], v246 offset:8192\n"
"ds_read_b64_tr_b16 a[152:153], v246 offset:1024\n"
"v_mfma_f32_16x16x32_bf16 v[52:55], a[232:235], a[168:171], 0\n"
"ds_read_b128 a[164:167], v250 offset:0\n"
"v_accvgpr_write_b32 a70, v42\n"
"v_mfma_f32_16x16x32_bf16 v[52:55], a[236:239], a[172:175], v[52:55]\n"
"buffer_load_dwordx4 v255, s[24:27], s49 offen offset:3072 lds\n"
"v_permlane16_swap_b32 v96, v98\n"
"v_accvgpr_write_b32 a67, v35\n"
"v_accvgpr_write_b32 a80, v64\n"
"v_permlane16_swap_b32 v80, v82\n"
"v_permlane16_swap_b32 v88, v90\n"
"v_mfma_f32_16x16x32_bf16 v[48:51], a[224:227], a[168:171], 0\n"
"v_accvgpr_write_b32 a81, v65\n"
"v_accvgpr_write_b32 a82, v66\n"
"v_mfma_f32_16x16x32_bf16 v[48:51], a[228:231], a[172:175], v[48:51]\n"
"s_add_u32 s40, s49, 40960\n"
"v_accvgpr_write_b32 a84, v72\n"
"v_accvgpr_write_b32 a93, v89\n"
"v_mfma_f32_16x16x32_bf16 v[44:47], a[216:219], a[168:171], 0\n"
"s_add_u32 m0, s51, 4096\n"
"ds_read_b64_tr_b16 a[112:113], v245 offset:0\n"
"v_accvgpr_write_b32 a91, v83\n"
"v_mfma_f32_16x16x32_bf16 v[44:47], a[220:223], a[172:175], v[44:47]\n"
"buffer_load_dwordx4 v247, s[24:27], s40 offen lds\n"
"v_mfma_f32_16x16x32_bf16 v[40:43], a[208:211], a[168:171], 0\n"
"v_permlane16_swap_b32 v73, v75\n"
"v_mfma_f32_16x16x32_bf16 v[40:43], a[212:215], a[172:175], v[40:43]\n"
"ds_read_b64_tr_b16 a[100:101], v243 offset:8192\n"
"v_accvgpr_write_b32 a85, v73\n"
"v_mfma_f32_16x16x32_bf16 v[36:39], a[200:203], a[168:171], 0\n"
"ds_read_b64_tr_b16 a[120:121], v246 offset:0\n"
"v_accvgpr_write_b32 a87, v75\n"
"v_mfma_f32_16x16x32_bf16 v[36:39], a[204:207], a[172:175], v[36:39]\n"
"ds_read_b64_tr_b16 a[110:111], v244 offset:8704\n"
"ds_read_b64_tr_b16 a[138:139], v244 offset:1536\n"
"v_mfma_f32_16x16x32_bf16 v[32:35], a[192:195], a[168:171], 0\n"
"ds_read_b64_tr_b16 a[132:133], v243 offset:9216\n"
"ds_read_b64_tr_b16 a[144:145], v245 offset:1024\n"
"v_mfma_f32_16x16x32_bf16 v[32:35], a[196:199], a[172:175], v[32:35]\n"
"buffer_load_dwordx4 v253, s[24:27], s40 offen offset:1024 lds\n"
"v_mfma_f32_16x16x32_bf16 v[64:67], a[192:195], a[176:179], 0\n"
"v_cvt_pk_bf16_f32 v99, v102, v103\n"
"v_accvgpr_write_b32 a86, v74\n"
"v_mfma_f32_16x16x32_bf16 v[64:67], a[196:199], a[180:183], v[64:67]\n"
"v_accvgpr_write_b32 a92, v88\n"
"v_xor_b32 v249, s74, v249\n"
"v_mfma_f32_16x16x32_bf16 v[68:71], a[200:203], a[176:179], 0\n"
"ds_read_b64_tr_b16 a[96:97], v243 offset:0\n"
"ds_read_b64_tr_b16 a[142:143], v244 offset:9728\n"
"v_mfma_f32_16x16x32_bf16 v[68:71], a[204:207], a[180:183], v[68:71]\n"
"buffer_load_dwordx4 v254, s[24:27], s40 offen offset:2048 lds\n"
"v_mfma_f32_16x16x32_bf16 v[72:75], a[208:211], a[176:179], 0\n"
"s_add_u32 s49, s49, 128\n"
"v_permlane16_swap_b32 v97, v99\n"
"v_mfma_f32_16x16x32_bf16 v[72:75], a[212:215], a[180:183], v[72:75]\n"
"s_sub_u32 s41, s49, 1280\n"
"ds_write_b128 v248, v[96:99] offset:4096\n"
"v_accvgpr_write_b32 a89, v81\n"
"v_accvgpr_write_b32 a94, v90\n"
"v_mfma_f32_16x16x32_bf16 v[76:79], a[216:219], a[176:179], 0\n"
"s_min_u32 s49, s49, s41\n"
"v_accvgpr_write_b32 a88, v80\n"
"v_accvgpr_write_b32 a90, v82\n"
"v_mfma_f32_16x16x32_bf16 v[76:79], a[220:223], a[180:183], v[76:79]\n"
"s_xor_b32 s51, s51, s74\n"
"ds_read_b64_tr_b16 a[106:107], v244 offset:512\n"
"ds_read_b128 a[160:163], v249 offset:0\n"
"v_mfma_f32_16x16x32_bf16 v[88:91], a[240:243], a[176:179], 0\n"
"ds_read_b64_tr_b16 a[98:99], v243 offset:512\n"
"ds_read_b64_tr_b16 a[156:157], v246 offset:9216\n"
"v_mfma_f32_16x16x32_bf16 v[88:91], a[244:247], a[180:183], v[88:91]\n"
"ds_read_b64_tr_b16 a[118:119], v245 offset:8704\n"
"ds_read_b64_tr_b16 a[154:155], v246 offset:1536\n"
"v_mfma_f32_16x16x32_bf16 v[92:95], a[248:251], a[176:179], 0\n"
"ds_read_b64_tr_b16 a[140:141], v244 offset:9216\n"
"ds_read_b64_tr_b16 a[150:151], v245 offset:9728\n"
"v_mfma_f32_16x16x32_bf16 v[92:95], a[252:255], a[180:183], v[92:95]\n"
"ds_read_b64_tr_b16 a[134:135], v243 offset:9728\n"
"ds_read_b128 a[168:171], v249 offset:2048\n"
"v_mfma_f32_16x16x32_bf16 v[80:83], a[224:227], a[176:179], 0\n"
"ds_read_b64_tr_b16 a[114:115], v245 offset:512\n"
"ds_read_b64_tr_b16 a[158:159], v246 offset:9728\n"
"v_mfma_f32_16x16x32_bf16 v[80:83], a[228:231], a[180:183], v[80:83]\n"
"ds_read_b64_tr_b16 a[104:105], v244 offset:0\n"
"ds_read_b64_tr_b16 a[108:109], v244 offset:8192\n"
"v_mfma_f32_16x16x32_bf16 v[84:87], a[232:235], a[176:179], 0\n"
"ds_read_b64_tr_b16 a[136:137], v244 offset:1024\n"
"ds_read_b64_tr_b16 a[146:147], v245 offset:1536\n"
"v_mfma_f32_16x16x32_bf16 v[84:87], a[236:239], a[180:183], v[84:87]\n"
"ds_read_b64_tr_b16 a[116:117], v245 offset:8192\n"
"ds_read_b128 a[176:179], v249 offset:4096\n"
"v_mfma_f32_16x16x32_bf16 v[108:111], a[232:235], a[184:187], 0\n"
"ds_read_b128 a[172:175], v250 offset:2048\n"
"ds_read_b128 a[180:183], v250 offset:4096\n"
"v_mfma_f32_16x16x32_bf16 v[108:111], a[236:239], a[188:191], v[108:111]\n"
"v_xor_b32 v252, s74, v252\n"
"v_mfma_f32_16x16x32_bf16 v[104:107], a[224:227], a[184:187], 0\n"
"v_mfma_f32_16x16x32_bf16 v[104:107], a[228:231], a[188:191], v[104:107]\n"
"v_mfma_f32_16x16x32_bf16 v[100:103], a[200:203], a[184:187], 0\n"
"v_xor_b32 v251, s74, v251\n"
"v_mfma_f32_16x16x32_bf16 v[100:103], a[204:207], a[188:191], v[100:103]\n"
"v_mfma_f32_16x16x32_bf16 v[96:99], a[192:195], a[184:187], 0\n"
"s_add_u32 s47, s47, 1\n"
"ds_read_b128 a[184:187], v251 offset:0\n"
"v_mfma_f32_16x16x32_bf16 v[96:99], a[196:199], a[188:191], v[96:99]\n"
"s_waitcnt lgkmcnt(3)\n"
"ds_read_b128 a[188:191], v252 offset:0\n"
"s_mov_b32 s62, 18\n"
"s_branch LPL_%=\n"
"LPT2_%=:\n"
"v_mfma_f32_16x16x32_bf16 v[112:115], a[96:99], a[160:163], v[112:115]\n"
"v_mfma_f32_16x16x32_bf16 v[112:115], a[100:103], a[164:167], v[112:115]\n"
"v_mfma_f32_16x16x32_bf16 v[116:119], a[104:107], a[160:163], v[116:119]\n"
"s_waitcnt vmcnt(11)\n"
"v_mfma_f32_16x16x32_bf16 v[116:119], a[108:111], a[164:167], v[116:119]\n"
"s_barrier\n"
"v_mfma_f32_16x16x32_bf16 v[120:123], a[112:115], a[160:163], v[120:123]\n"
"s_add_u32 s40, s48, 256\n"
"s_xor_b32 m0, s50, 0xc000\n"
"ds_read_b64_tr_b16 a[222:223], v246 offset:25088\n"
"v_mfma_f32_16x16x32_bf16 v[120:123], a[116:119], a[164:167], v[120:123]\n"
"buffer_load_dwordx4 v224, s[28:31], s40 offen lds\n"
"v_mfma_f32_16x16x32_bf16 v[124:127], a[120:123], a[160:163], v[124:127]\n"
"v_mfma_f32_16x16x32_bf16 v[124:127], a[124:127], a[164:167], v[124:127]\n"
"v_mfma_f32_16x16x32_bf16 v[128:131], a[128:131], a[160:163], v[128:131]\n"
"ds_read_b64_tr_b16 a[216:217], v246 offset:16384\n"
"v_mfma_f32_16x16x32_bf16 v[128:131], a[132:135], a[164:167], v[128:131]\n"
"ds_read_b64_tr_b16 a[244:245], v245 offset:25600\n"
"v_mfma_f32_16x16x32_bf16 v[132:135], a[136:139], a[160:163], v[132:135]\n"
"ds_read_b64_tr_b16 a[194:195], v243 offset:16896\n"
"ds_read_b64_tr_b16 a[246:247], v245 offset:26112\n"
"v_mfma_f32_16x16x32_bf16 v[132:135], a[140:143], a[164:167], v[132:135]\n"
"buffer_load_dwordx4 v225, s[28:31], s40 offen offset:1024 lds\n"
"v_mfma_f32_16x16x32_bf16 v[136:139], a[144:147], a[160:163], v[136:139]\n"
"v_mfma_f32_16x16x32_bf16 v[136:139], a[148:151], a[164:167], v[136:139]\n"
"v_bfe_i32 v241, v242, 4, 1\n"
"v_mfma_f32_16x16x32_bf16 v[140:143], a[152:155], a[160:163], v[140:143]\n"
"ds_read_b64_tr_b16 a[202:203], v244 offset:16896\n"
"ds_read_b64_tr_b16 a[242:243], v245 offset:17920\n"
"v_mfma_f32_16x16x32_bf16 v[140:143], a[156:159], a[164:167], v[140:143]\n"
"ds_read_b64_tr_b16 a[240:241], v245 offset:17408\n"
"v_mfma_f32_16x16x32_bf16 v[172:175], a[152:155], a[168:171], v[172:175]\n"
"ds_read_b64_tr_b16 a[210:211], v245 offset:16896\n"
"v_and_b32 v241, 0xaaaaaaaa, v241\n"
"v_mfma_f32_16x16x32_bf16 v[172:175], a[156:159], a[172:175], v[172:175]\n"
"ds_read_b64_tr_b16 a[234:235], v244 offset:17920\n"
"v_mfma_f32_16x16x32_bf16 v[168:171], a[144:147], a[168:171], v[168:171]\n"
"ds_read_b64_tr_b16 a[208:209], v245 offset:16384\n"
"ds_read_b64_tr_b16 a[236:237], v244 offset:25600\n"
"v_mfma_f32_16x16x32_bf16 v[168:171], a[148:151], a[172:175], v[168:171]\n"
"buffer_load_dwordx4 v226, s[28:31], s40 offen offset:2048 lds\n"
"v_mfma_f32_16x16x32_bf16 v[164:167], a[136:139], a[168:171], v[164:167]\n"
"v_mfma_f32_16x16x32_bf16 v[164:167], a[140:143], a[172:175], v[164:167]\n"
"v_mfma_f32_16x16x32_bf16 v[160:163], a[128:131], a[168:171], v[160:163]\n"
"ds_read_b64_tr_b16 a[214:215], v245 offset:25088\n"
"v_mfma_f32_16x16x32_bf16 v[160:163], a[132:135], a[172:175], v[160:163]\n"
"ds_read_b64_tr_b16 a[192:193], v243 offset:16384\n"
"v_mfma_f32_16x16x32_bf16 v[156:159], a[120:123], a[168:171], v[156:159]\n"
"ds_read_b64_tr_b16 a[204:205], v244 offset:24576\n"
"v_mfma_f32_16x16x32_bf16 v[156:159], a[124:127], a[172:175], v[156:159]\n"
"buffer_load_dwordx4 v227, s[28:31], s40 offen offset:3072 lds\n"
"v_mfma_f32_16x16x32_bf16 v[152:155], a[112:115], a[168:171], v[152:155]\n"
"s_mov_b32 s58, 245760\n"
"v_mfma_f32_16x16x32_bf16 v[152:155], a[116:119], a[172:175], v[152:155]\n"
"s_mov_b32 s47, 0\n"
"v_mfma_f32_16x16x32_bf16 v[148:151], a[104:107], a[168:171], v[148:151]\n"
"ds_read_b64_tr_b16 a[232:233], v244 offset:17408\n"
"v_mfma_f32_16x16x32_bf16 v[148:151], a[108:111], a[172:175], v[148:151]\n"
"ds_read_b64_tr_b16 a[218:219], v246 offset:16896\n"
"v_mfma_f32_16x16x32_bf16 v[144:147], a[96:99], a[168:171], v[144:147]\n"
"s_mov_b32 m0, s44\n"
"ds_read_b64_tr_b16 a[226:227], v243 offset:17920\n"
"ds_read_b64_tr_b16 a[250:251], v246 offset:17920\n"
"v_mfma_f32_16x16x32_bf16 v[144:147], a[100:103], a[172:175], v[144:147]\n"
"buffer_load_dwordx4 v240, s[16:19], s56 offen nt lds\n"
"v_mfma_f32_16x16x32_bf16 v[176:179], a[96:99], a[176:179], v[176:179]\n"
"s_waitcnt lgkmcnt(4)\n"
"v_mfma_f32_16x16x32_bf16 v[176:179], a[100:103], a[180:183], v[176:179]\n"
"v_mfma_f32_16x16x32_bf16 v[180:183], a[104:107], a[176:179], v[180:183]\n"
"ds_read_b64_tr_b16 a[254:255], v246 offset:26112\n"
"v_mfma_f32_16x16x32_bf16 v[180:183], a[108:111], a[180:183], v[180:183]\n"
"ds_read_b64_tr_b16 a[230:231], v243 offset:26112\n"
"v_mfma_f32_16x16x32_bf16 v[184:187], a[112:115], a[176:179], v[184:187]\n"
"ds_read_b64_tr_b16 a[238:239], v244 offset:26112\n"
"v_mfma_f32_16x16x32_bf16 v[184:187], a[116:119], a[180:183], v[184:187]\n"
"ds_read_b64_tr_b16 a[196:197], v243 offset:24576\n"
"v_mfma_f32_16x16x32_bf16 v[188:191], a[120:123], a[176:179], v[188:191]\n"
"s_add_u32 s40, s56, s52\n"
"ds_read_b64_tr_b16 a[220:221], v246 offset:24576\n"
"ds_read_b64_tr_b16 a[228:229], v243 offset:25600\n"
"v_mfma_f32_16x16x32_bf16 v[188:191], a[124:127], a[180:183], v[188:191]\n"
"buffer_load_dwordx4 v240, s[16:19], s40 offen offset:1024 nt lds\n"
"v_mfma_f32_16x16x32_bf16 v[200:203], a[144:147], a[176:179], v[200:203]\n"
"v_mfma_f32_16x16x32_bf16 v[200:203], a[148:151], a[180:183], v[200:203]\n"
"v_mfma_f32_16x16x32_bf16 v[204:207], a[152:155], a[176:179], v[204:207]\n"
"ds_read_b64_tr_b16 a[212:213], v245 offset:24576\n"
"v_mfma_f32_16x16x32_bf16 v[204:207], a[156:159], a[180:183], v[204:207]\n"
"ds_read_b64_tr_b16 a[224:225], v243 offset:17408\n"
"v_mfma_f32_16x16x32_bf16 v[192:195], a[128:131], a[176:179], v[192:195]\n"
"s_add_u32 s40, s56, s53\n"
"ds_read_b64_tr_b16 a[198:199], v243 offset:25088\n"
"ds_read_b64_tr_b16 a[248:249], v246 offset:17408\n"
"v_mfma_f32_16x16x32_bf16 v[192:195], a[132:135], a[180:183], v[192:195]\n"
"buffer_load_dwordx4 v240, s[16:19], s40 offen offset:2048 nt lds\n"
"v_mfma_f32_16x16x32_bf16 v[196:199], a[136:139], a[176:179], v[196:199]\n"
"v_xor_b32 v245, s74, v245\n"
"v_mfma_f32_16x16x32_bf16 v[196:199], a[140:143], a[180:183], v[196:199]\n"
"v_xor_b32 v249, s74, v249\n"
"v_mfma_f32_16x16x32_bf16 v[220:223], a[136:139], a[184:187], v[220:223]\n"
"ds_read_b64_tr_b16 a[206:207], v244 offset:25088\n"
"v_mfma_f32_16x16x32_bf16 v[220:223], a[140:143], a[188:191], v[220:223]\n"
"ds_read_b64_tr_b16 a[200:201], v244 offset:16384\n"
"v_mfma_f32_16x16x32_bf16 v[216:219], a[128:131], a[184:187], v[216:219]\n"
"s_add_u32 s40, s56, s54\n"
"ds_read_b64_tr_b16 a[252:253], v246 offset:25600\n"
"v_mfma_f32_16x16x32_bf16 v[216:219], a[132:135], a[188:191], v[216:219]\n"
"buffer_load_dwordx4 v240, s[16:19], s40 offen offset:3072 nt lds\n"
"v_mfma_f32_16x16x32_bf16 v[212:215], a[104:107], a[184:187], v[212:215]\n"
"v_mfma_f32_16x16x32_bf16 v[212:215], a[108:111], a[188:191], v[212:215]\n"
"v_xor_b32 v243, s74, v243\n"
"v_mfma_f32_16x16x32_bf16 v[208:211], a[96:99], a[184:187], v[208:211]\n"
"v_mfma_f32_16x16x32_bf16 v[208:211], a[100:103], a[188:191], v[208:211]\n"
"v_mfma_f32_16x16x32_bf16 v[0:3], a[192:195], a[160:163], v[0:3]\n"
"s_waitcnt lgkmcnt(2)\n"
"v_mfma_f32_16x16x32_bf16 v[0:3], a[196:199], a[164:167], v[0:3]\n"
"s_waitcnt lgkmcnt(1)\n"
"v_xor_b32 v246, s74, v246\n"
"v_mfma_f32_16x16x32_bf16 v[4:7], a[200:203], a[160:163], v[4:7]\n"
"s_waitcnt vmcnt(8) lgkmcnt(0)\n"
"v_mfma_f32_16x16x32_bf16 v[4:7], a[204:207], a[164:167], v[4:7]\n"
"s_barrier\n"
"v_mfma_f32_16x16x32_bf16 v[8:11], a[208:211], a[160:163], v[8:11]\n"
"s_add_u32 m0, s44, 65536\n"
"ds_read_b64_tr_b16 a[100:101], v243 offset:8192\n"
"ds_read_b64_tr_b16 a[116:117], v245 offset:8192\n"
"v_mfma_f32_16x16x32_bf16 v[8:11], a[212:215], a[164:167], v[8:11]\n"
"buffer_load_dwordx4 v240, s[20:23], s56 offen nt lds\n"
"v_mfma_f32_16x16x32_bf16 v[12:15], a[216:219], a[160:163], v[12:15]\n"
"v_mfma_f32_16x16x32_bf16 v[12:15], a[220:223], a[164:167], v[12:15]\n"
"v_xor_b32 v244, s74, v244\n"
"v_mfma_f32_16x16x32_bf16 v[16:19], a[224:227], a[160:163], v[16:19]\n"
"ds_read_b64_tr_b16 a[98:99], v243 offset:512\n"
"ds_read_b64_tr_b16 a[132:133], v243 offset:9216\n"
"v_mfma_f32_16x16x32_bf16 v[16:19], a[228:231], a[164:167], v[16:19]\n"
"ds_read_b64_tr_b16 a[126:127], v246 offset:8704\n"
"v_mfma_f32_16x16x32_bf16 v[20:23], a[232:235], a[160:163], v[20:23]\n"
"s_add_u32 s40, s56, s52\n"
"ds_read_b64_tr_b16 a[112:113], v245 offset:0\n"
"ds_read_b64_tr_b16 a[148:149], v245 offset:9216\n"
"v_mfma_f32_16x16x32_bf16 v[20:23], a[236:239], a[164:167], v[20:23]\n"
"buffer_load_dwordx4 v240, s[20:23], s40 offen offset:1024 nt lds\n"
"v_mfma_f32_16x16x32_bf16 v[24:27], a[240:243], a[160:163], v[24:27]\n"
"v_bfe_u32 v247, v242, 4, 2\n"
"v_mfma_f32_16x16x32_bf16 v[24:27], a[244:247], a[164:167], v[24:27]\n"
"v_mfma_f32_16x16x32_bf16 v[28:31], a[248:251], a[160:163], v[28:31]\n"
"ds_read_b64_tr_b16 a[158:159], v246 offset:9728\n"
"v_mfma_f32_16x16x32_bf16 v[28:31], a[252:255], a[164:167], v[28:31]\n"
"ds_read_b64_tr_b16 a[114:115], v245 offset:512\n"
"v_xor_b32 v250, s74, v250\n"
"v_mfma_f32_16x16x32_bf16 v[60:63], a[248:251], a[168:171], v[60:63]\n"
"ds_read_b64_tr_b16 a[104:105], v244 offset:0\n"
"v_mfma_f32_16x16x32_bf16 v[60:63], a[252:255], a[172:175], v[60:63]\n"
"ds_read_b64_tr_b16 a[130:131], v243 offset:1536\n"
"v_mfma_f32_16x16x32_bf16 v[56:59], a[240:243], a[168:171], v[56:59]\n"
"s_add_u32 s40, s56, s53\n"
"ds_read_b128 a[164:167], v250 offset:0\n"
"v_mfma_f32_16x16x32_bf16 v[56:59], a[244:247], a[172:175], v[56:59]\n"
"buffer_load_dwordx4 v240, s[20:23], s40 offen offset:2048 nt lds\n"
"v_mfma_f32_16x16x32_bf16 v[52:55], a[232:235], a[168:171], v[52:55]\n"
"v_mfma_f32_16x16x32_bf16 v[52:55], a[236:239], a[172:175], v[52:55]\n"
"v_mfma_f32_16x16x32_bf16 v[48:51], a[224:227], a[168:171], v[48:51]\n"
"ds_read_b64_tr_b16 a[136:137], v244 offset:1024\n"
"v_mfma_f32_16x16x32_bf16 v[48:51], a[228:231], a[172:175], v[48:51]\n"
"ds_read_b64_tr_b16 a[122:123], v246 offset:512\n"
"v_mfma_f32_16x16x32_bf16 v[44:47], a[216:219], a[168:171], v[44:47]\n"
"s_add_u32 s40, s56, s54\n"
"ds_read_b64_tr_b16 a[106:107], v244 offset:512\n"
"ds_read_b64_tr_b16 a[146:147], v245 offset:1536\n"
"v_mfma_f32_16x16x32_bf16 v[44:47], a[220:223], a[172:175], v[44:47]\n"
"buffer_load_dwordx4 v240, s[20:23], s40 offen offset:3072 nt lds\n"
"v_mfma_f32_16x16x32_bf16 v[40:43], a[208:211], a[168:171], v[40:43]\n"
"s_add_u32 s55, s55, s59\n"
"v_mfma_f32_16x16x32_bf16 v[40:43], a[212:215], a[172:175], v[40:43]\n"
"s_add_u32 s41, s55, s57\n"
"v_xor_b32 v251, s74, v251\n"
"v_mfma_f32_16x16x32_bf16 v[36:39], a[200:203], a[168:171], v[36:39]\n"
"s_and_b32 s41, s41, s58\n"
"ds_read_b64_tr_b16 a[124:125], v246 offset:8192\n"
"ds_read_b64_tr_b16 a[140:141], v244 offset:9216\n"
"v_mfma_f32_16x16x32_bf16 v[36:39], a[204:207], a[172:175], v[36:39]\n"
"s_andn2_b32 s56, s55, s58\n"
"ds_read_b64_tr_b16 a[110:111], v244 offset:8704\n"
"v_mfma_f32_16x16x32_bf16 v[32:35], a[192:195], a[168:171], v[32:35]\n"
"s_or_b32 s56, s56, s41\n"
"ds_read_b64_tr_b16 a[144:145], v245 offset:1024\n"
"v_mfma_f32_16x16x32_bf16 v[32:35], a[196:199], a[172:175], v[32:35]\n"
"ds_read_b64_tr_b16 a[156:157], v246 offset:9216\n"
"v_or_b32 v241, 0x44444444, v241\n"
"v_mfma_f32_16x16x32_bf16 v[64:67], a[192:195], a[176:179], v[64:67]\n"
"s_add_u32 m0, s44, 16384\n"
"ds_read_b64_tr_b16 a[134:135], v243 offset:9728\n"
"ds_read_b64_tr_b16 a[150:151], v245 offset:9728\n"
"v_mfma_f32_16x16x32_bf16 v[64:67], a[196:199], a[180:183], v[64:67]\n"
"buffer_load_dwordx4 v240, s[16:19], s56 offen nt lds\n"
"v_mfma_f32_16x16x32_bf16 v[68:71], a[200:203], a[176:179], v[68:71]\n"
"v_mfma_f32_16x16x32_bf16 v[68:71], a[204:207], a[180:183], v[68:71]\n"
"v_mfma_f32_16x16x32_bf16 v[72:75], a[208:211], a[176:179], v[72:75]\n"
"ds_read_b128 a[168:171], v249 offset:2048\n"
"v_mfma_f32_16x16x32_bf16 v[72:75], a[212:215], a[180:183], v[72:75]\n"
"ds_read_b64_tr_b16 a[108:109], v244 offset:8192\n"
"v_xor_b32 v252, s74, v252\n"
"v_mfma_f32_16x16x32_bf16 v[76:79], a[216:219], a[176:179], v[76:79]\n"
"s_add_u32 s40, s56, s52\n"
"ds_read_b64_tr_b16 a[102:103], v243 offset:8704\n"
"ds_read_b64_tr_b16 a[128:129], v243 offset:1024\n"
"v_mfma_f32_16x16x32_bf16 v[76:79], a[220:223], a[180:183], v[76:79]\n"
"buffer_load_dwordx4 v240, s[16:19], s40 offen offset:1024 nt lds\n"
"v_mfma_f32_16x16x32_bf16 v[88:91], a[240:243], a[176:179], v[88:91]\n"
"v_mfma_f32_16x16x32_bf16 v[88:91], a[244:247], a[180:183], v[88:91]\n"
"v_mfma_f32_16x16x32_bf16 v[92:95], a[248:251], a[176:179], v[92:95]\n"
"ds_read_b64_tr_b16 a[96:97], v243 offset:0\n"
"v_mul_u32_u24 v247, 20, v247\n"
"v_mfma_f32_16x16x32_bf16 v[92:95], a[252:255], a[180:183], v[92:95]\n"
"ds_read_b128 a[160:163], v249 offset:0\n"
"v_mfma_f32_16x16x32_bf16 v[80:83], a[224:227], a[176:179], v[80:83]\n"
"s_add_u32 s40, s56, s53\n"
"ds_read_b64_tr_b16 a[154:155], v246 offset:1536\n"
"v_mfma_f32_16x16x32_bf16 v[80:83], a[228:231], a[180:183], v[80:83]\n"
"buffer_load_dwordx4 v240, s[16:19], s40 offen offset:2048 nt lds\n"
"v_mfma_f32_16x16x32_bf16 v[84:87], a[232:235], a[176:179], v[84:87]\n"
"v_mfma_f32_16x16x32_bf16 v[84:87], a[236:239], a[180:183], v[84:87]\n"
"v_mfma_f32_16x16x32_bf16 v[108:111], a[232:235], a[184:187], v[108:111]\n"
"ds_read_b64_tr_b16 a[118:119], v245 offset:8704\n"
"ds_read_b64_tr_b16 a[120:121], v246 offset:0\n"
"v_mfma_f32_16x16x32_bf16 v[108:111], a[236:239], a[188:191], v[108:111]\n"
"ds_read_b64_tr_b16 a[142:143], v244 offset:9728\n"
"v_mfma_f32_16x16x32_bf16 v[104:107], a[224:227], a[184:187], v[104:107]\n"
"s_add_u32 s40, s56, s54\n"
"ds_read_b64_tr_b16 a[138:139], v244 offset:1536\n"
"ds_read_b64_tr_b16 a[152:153], v246 offset:1024\n"
"v_mfma_f32_16x16x32_bf16 v[104:107], a[228:231], a[188:191], v[104:107]\n"
"buffer_load_dwordx4 v240, s[16:19], s40 offen offset:3072 nt lds\n"
"v_mfma_f32_16x16x32_bf16 v[100:103], a[200:203], a[184:187], v[100:103]\n"
"v_mfma_f32_16x16x32_bf16 v[100:103], a[204:207], a[188:191], v[100:103]\n"
"v_mfma_f32_16x16x32_bf16 v[96:99], a[192:195], a[184:187], v[96:99]\n"
"ds_read_b128 a[180:183], v250 offset:4096\n"
"v_mfma_f32_16x16x32_bf16 v[96:99], a[196:199], a[188:191], v[96:99]\n"
"s_waitcnt lgkmcnt(6)\n"
"v_mfma_f32_16x16x32_bf16 v[112:115], a[96:99], a[160:163], v[112:115]\n"
"ds_read_b128 a[184:187], v251 offset:0\n"
"v_mfma_f32_16x16x32_bf16 v[112:115], a[100:103], a[164:167], v[112:115]\n"
"ds_read_b128 a[176:179], v249 offset:4096\n"
"v_mfma_f32_16x16x32_bf16 v[116:119], a[104:107], a[160:163], v[116:119]\n"
"s_waitcnt vmcnt(12) lgkmcnt(3)\n"
"ds_read_b128 a[172:175], v250 offset:2048\n"
"v_and_b32 v249, 0x20, v242\n"
"v_mfma_f32_16x16x32_bf16 v[116:119], a[108:111], a[164:167], v[116:119]\n"
"s_barrier\n"
"v_mfma_f32_16x16x32_bf16 v[120:123], a[112:115], a[160:163], v[120:123]\n"
"s_add_u32 m0, s44, 81920\n"
"ds_read_b64_tr_b16 a[196:197], v243 offset:24576\n"
"ds_read_b64_tr_b16 a[252:253], v246 offset:25600\n"
"v_mfma_f32_16x16x32_bf16 v[120:123], a[116:119], a[164:167], v[120:123]\n"
"buffer_load_dwordx4 v240, s[20:23], s56 offen nt lds\n"
"v_mfma_f32_16x16x32_bf16 v[124:127], a[120:123], a[160:163], v[124:127]\n"
"v_add_u32 v249, -1, v249\n"
"v_ashrrev_i32 v249, 31, v249\n"
"v_mfma_f32_16x16x32_bf16 v[124:127], a[124:127], a[164:167], v[124:127]\n"
"v_mul_f32 v114, s45, v114\n"
"v_mfma_f32_16x16x32_bf16 v[128:131], a[128:131], a[160:163], v[128:131]\n"
"ds_read_b128 a[188:191], v252 offset:0\n"
"v_mul_f32 v121, s45, v121\n"
"v_mfma_f32_16x16x32_bf16 v[128:131], a[132:135], a[164:167], v[128:131]\n"
"ds_read_b64_tr_b16 a[198:199], v243 offset:25088\n"
"v_mul_f32 v123, s45, v123\n"
"v_mfma_f32_16x16x32_bf16 v[132:135], a[136:139], a[160:163], v[132:135]\n"
"ds_read_b64_tr_b16 a[224:225], v243 offset:17408\n"
"v_mfma_f32_16x16x32_bf16 v[132:135], a[140:143], a[164:167], v[132:135]\n"
"ds_read_b64_tr_b16 a[200:201], v244 offset:16384\n"
"ds_read_b64_tr_b16 a[240:241], v245 offset:17408\n"
"v_mfma_f32_16x16x32_bf16 v[136:139], a[144:147], a[160:163], v[136:139]\n"
"ds_read_b64_tr_b16 a[234:235], v244 offset:17920\n"
"v_mul_f32 v130, s45, v130\n"
"v_mfma_f32_16x16x32_bf16 v[136:139], a[148:151], a[164:167], v[136:139]\n"
"ds_read_b64_tr_b16 a[232:233], v244 offset:17408\n"
"ds_read_b64_tr_b16 a[244:245], v245 offset:25600\n"
"v_mfma_f32_16x16x32_bf16 v[140:143], a[152:155], a[160:163], v[140:143]\n"
"ds_read_b64_tr_b16 a[192:193], v243 offset:16384\n"
"v_mul_f32 v132, s45, v132\n"
"v_mfma_f32_16x16x32_bf16 v[140:143], a[156:159], a[164:167], v[140:143]\n"
"s_waitcnt lgkmcnt(9)\n"
"ds_read_b64_tr_b16 a[222:223], v246 offset:25088\n"
"v_mul_f32 v124, s45, v124\n"
"v_mfma_f32_16x16x32_bf16 v[220:223], a[136:139], a[184:187], v[220:223]\n"
"s_waitcnt lgkmcnt(9)\n"
"ds_read_b64_tr_b16 a[242:243], v245 offset:17920\n"
"v_mul_f32 v133, s45, v133\n"
"v_mfma_f32_16x16x32_bf16 v[220:223], a[140:143], a[188:191], v[220:223]\n"
"ds_read_b64_tr_b16 a[236:237], v244 offset:25600\n"
"v_mul_f32 v120, s45, v120\n"
"v_mfma_f32_16x16x32_bf16 v[216:219], a[128:131], a[184:187], v[216:219]\n"
"s_add_u32 s40, s56, s52\n"
"ds_read_b64_tr_b16 a[212:213], v245 offset:24576\n"
"ds_read_b64_tr_b16 a[220:221], v246 offset:24576\n"
"v_mfma_f32_16x16x32_bf16 v[216:219], a[132:135], a[188:191], v[216:219]\n"
"buffer_load_dwordx4 v240, s[20:23], s40 offen offset:1024 nt lds\n"
"v_mfma_f32_16x16x32_bf16 v[212:215], a[104:107], a[184:187], v[212:215]\n"
"v_mul_f32 v115, s45, v115\n"
"v_mul_f32 v128, s45, v128\n"
"v_mfma_f32_16x16x32_bf16 v[212:215], a[108:111], a[188:191], v[212:215]\n"
"v_mul_f32 v129, s45, v129\n"
"v_mul_f32 v222, s45, v222\n"
"v_mfma_f32_16x16x32_bf16 v[208:211], a[96:99], a[184:187], v[208:211]\n"
"ds_read_b64_tr_b16 a[206:207], v244 offset:25088\n"
"v_mul_f32 v131, s45, v131\n"
"v_mfma_f32_16x16x32_bf16 v[208:211], a[100:103], a[188:191], v[208:211]\n"
"ds_read_b64_tr_b16 a[230:231], v243 offset:26112\n"
"v_mul_f32 v216, s45, v216\n"
"v_mfma_f32_16x16x32_bf16 v[172:175], a[152:155], a[168:171], v[172:175]\n"
"ds_read_b64_tr_b16 a[210:211], v245 offset:16896\n"
"v_mul_f32 v217, s45, v217\n"
"v_mfma_f32_16x16x32_bf16 v[172:175], a[156:159], a[172:175], v[172:175]\n"
"ds_read_b64_tr_b16 a[246:247], v245 offset:26112\n"
"v_mul_f32 v218, s45, v218\n"
"v_mfma_f32_16x16x32_bf16 v[168:171], a[144:147], a[168:171], v[168:171]\n"
"ds_read_b64_tr_b16 a[218:219], v246 offset:16896\n"
"v_mul_f32 v219, s45, v219\n"
"v_mfma_f32_16x16x32_bf16 v[168:171], a[148:151], a[172:175], v[168:171]\n"
"v_mul_f32 v211, s45, v211\n"
"v_and_b32 v249, 0x3f803f80, v249\n"
"v_mfma_f32_16x16x32_bf16 v[164:167], a[136:139], a[168:171], v[164:167]\n"
"v_mul_f32 v214, s45, v214\n"
"v_cvt_pk_bf16_f32 v216, v216, v217\n"
"v_mfma_f32_16x16x32_bf16 v[164:167], a[140:143], a[172:175], v[164:167]\n"
"ds_read_b64_tr_b16 a[228:229], v243 offset:25600\n"
"v_mul_f32 v210, s45, v210\n"
"v_mfma_f32_16x16x32_bf16 v[160:163], a[128:131], a[168:171], v[160:163]\n"
"s_add_u32 s40, s56, s53\n"
"ds_read_b64_tr_b16 a[194:195], v243 offset:16896\n"
"v_mul_f32 v209, s45, v209\n"
"v_mfma_f32_16x16x32_bf16 v[160:163], a[132:135], a[172:175], v[160:163]\n"
"buffer_load_dwordx4 v240, s[20:23], s40 offen offset:2048 nt lds\n"
"v_mfma_f32_16x16x32_bf16 v[156:159], a[120:123], a[168:171], v[156:159]\n"
"v_mul_f32 v223, s45, v223\n"
"v_mul_f32 v165, s45, v165\n"
"v_mfma_f32_16x16x32_bf16 v[156:159], a[124:127], a[172:175], v[156:159]\n"
"v_mul_f32 v169, s45, v169\n"
"v_mul_f32 v175, s45, v175\n"
"v_mfma_f32_16x16x32_bf16 v[152:155], a[112:115], a[168:171], v[152:155]\n"
"ds_read_b64_tr_b16 a[250:251], v246 offset:17920\n"
"v_mul_f32 v208, s45, v208\n"
"v_mfma_f32_16x16x32_bf16 v[152:155], a[116:119], a[172:175], v[152:155]\n"
"ds_read_b64_tr_b16 a[254:255], v246 offset:26112\n"
"v_mul_f32 v174, s45, v174\n"
"v_mfma_f32_16x16x32_bf16 v[148:151], a[104:107], a[168:171], v[148:151]\n"
"ds_read_b64_tr_b16 a[226:227], v243 offset:17920\n"
"v_mul_f32 v221, s45, v221\n"
"v_mfma_f32_16x16x32_bf16 v[148:151], a[108:111], a[172:175], v[148:151]\n"
"ds_read_b64_tr_b16 a[214:215], v245 offset:25088\n"
"v_cvt_pk_bf16_f32 v120, v120, v121\n"
"v_mfma_f32_16x16x32_bf16 v[144:147], a[96:99], a[168:171], v[144:147]\n"
"ds_read_b64_tr_b16 a[238:239], v244 offset:26112\n"
"v_mul_f32 v162, s45, v162\n"
"v_mfma_f32_16x16x32_bf16 v[144:147], a[100:103], a[172:175], v[144:147]\n"
"v_mul_f32 v122, s45, v122\n"
"v_mul_f32 v212, s45, v212\n"
"v_mfma_f32_16x16x32_bf16 v[176:179], a[96:99], a[176:179], v[176:179]\n"
"v_mul_f32 v135, s45, v135\n"
"v_mul_f32 v159, s45, v159\n"
"v_mfma_f32_16x16x32_bf16 v[176:179], a[100:103], a[180:183], v[176:179]\n"
"v_cvt_pk_bf16_f32 v208, v208, v209\n"
"v_mul_f32 v158, s45, v158\n"
"v_mfma_f32_16x16x32_bf16 v[180:183], a[104:107], a[176:179], v[180:183]\n"
"v_mul_f32 v215, s45, v215\n"
"v_mul_f32 v156, s45, v156\n"
"v_mfma_f32_16x16x32_bf16 v[180:183], a[108:111], a[180:183], v[180:183]\n"
"ds_read_b64_tr_b16 a[208:209], v245 offset:16384\n"
"v_mul_f32 v148, s45, v148\n"
"v_mfma_f32_16x16x32_bf16 v[184:187], a[112:115], a[176:179], v[184:187]\n"
"s_add_u32 s40, s56, s54\n"
"ds_read_b64_tr_b16 a[204:205], v244 offset:24576\n"
"v_mul_f32 v166, s45, v166\n"
"v_mfma_f32_16x16x32_bf16 v[184:187], a[116:119], a[180:183], v[184:187]\n"
"buffer_load_dwordx4 v240, s[20:23], s40 offen offset:3072 nt lds\n"
"v_mfma_f32_16x16x32_bf16 v[188:191], a[120:123], a[176:179], v[188:191]\n"
"s_add_u32 s55, s55, s59\n"
"v_cvt_pk_bf16_f32 v209, v210, v211\n"
"v_mul_f32 v149, s45, v149\n"
"v_mfma_f32_16x16x32_bf16 v[188:191], a[124:127], a[180:183], v[188:191]\n"
"s_add_u32 s41, s55, s57\n"
"v_mul_f32 v136, s45, v136\n"
"v_mul_f32 v161, s45, v161\n"
"v_mfma_f32_16x16x32_bf16 v[200:203], a[144:147], a[176:179], v[200:203]\n"
"s_and_b32 s41, s41, s58\n"
"ds_read_b64_tr_b16 a[248:249], v246 offset:17408\n"
"v_mul_f32 v119, s45, v119\n"
"v_mfma_f32_16x16x32_bf16 v[200:203], a[148:151], a[180:183], v[200:203]\n"
"s_andn2_b32 s56, s55, s58\n"
"ds_read_b64_tr_b16 a[216:217], v246 offset:16384\n"
"v_mul_f32 v178, s45, v178\n"
"v_mfma_f32_16x16x32_bf16 v[204:207], a[152:155], a[176:179], v[204:207]\n"
"s_or_b32 s56, s56, s41\n"
"v_mul_f32 v170, s45, v170\n"
"v_mul_f32 v183, s45, v183\n"
"v_mfma_f32_16x16x32_bf16 v[204:207], a[156:159], a[180:183], v[204:207]\n"
"ds_read_b64_tr_b16 a[202:203], v244 offset:16896\n"
"v_cvt_pk_bf16_f32 v128, v128, v129\n"
"v_mfma_f32_16x16x32_bf16 v[192:195], a[128:131], a[176:179], v[192:195]\n"
"v_mul_f32 v190, s45, v190\n"
"v_mul_f32 v203, s45, v203\n"
"v_mfma_f32_16x16x32_bf16 v[192:195], a[132:135], a[180:183], v[192:195]\n"
"v_mul_f32 v154, s45, v154\n"
"v_mul_f32 v160, s45, v160\n"
"v_mfma_f32_16x16x32_bf16 v[196:199], a[136:139], a[176:179], v[196:199]\n"
"v_mul_f32 v220, s45, v220\n"
"v_mul_f32 v184, s45, v184\n"
"v_mfma_f32_16x16x32_bf16 v[196:199], a[140:143], a[180:183], v[196:199]\n"
"s_waitcnt lgkmcnt(3)\n"
"v_mul_f32 v153, s45, v153\n"
"v_mul_f32 v171, s45, v171\n"
"v_mfma_f32_16x16x32_bf16 v[108:111], a[232:235], a[184:187], v[108:111]\n"
"s_add_u32 m0, s44, 32768\n"
"v_mul_f32 v188, s45, v188\n"
"v_mul_f32 v191, s45, v191\n"
"v_mfma_f32_16x16x32_bf16 v[108:111], a[236:239], a[188:191], v[108:111]\n"
"buffer_load_dwordx4 v240, s[16:19], s56 offen nt lds\n"
"v_mul_f32 v137, s45, v137\n"
"v_mul_f32 v213, s45, v213\n"
"v_cvt_pk_bf16_f32 v217, v218, v219\n"
"v_mul_f32 v152, s45, v152\n"
"v_cvt_pk_bf16_f32 v160, v160, v161\n"
"v_mul_f32 v187, s45, v187\n"
"v_mul_f32 v195, s45, v195\n"
"v_mfma_f32_16x16x32_bf16 v[104:107], a[224:227], a[184:187], v[104:107]\n"
"s_waitcnt vmcnt(13) lgkmcnt(0)\n"
"v_cvt_pk_bf16_f32 v210, v212, v213\n"
"v_mul_f32 v177, s45, v177\n"
"v_mfma_f32_16x16x32_bf16 v[104:107], a[228:231], a[188:191], v[104:107]\n"
"s_barrier\n"
"v_mul_f32 v113, s45, v113\n"
"v_mul_f32 v155, s45, v155\n"
"v_mul_f32 v157, s45, v157\n"
"v_mul_f32 v176, s45, v176\n"
"v_mul_f32 v185, s45, v185\n"
"v_mul_f32 v189, s45, v189\n"
"v_mfma_f32_16x16x32_bf16 v[100:103], a[200:203], a[184:187], v[100:103]\n"
"v_mul_f32 v200, s45, v200\n"
"v_mul_f32 v202, s45, v202\n"
"v_mfma_f32_16x16x32_bf16 v[100:103], a[204:207], a[188:191], v[100:103]\n"
"v_cvt_pk_bf16_f32 v152, v152, v153\n"
"v_mul_f32 v207, s45, v207\n"
"v_mfma_f32_16x16x32_bf16 v[96:99], a[192:195], a[184:187], v[96:99]\n"
"v_mul_f32 v164, s45, v164\n"
"v_mul_f32 v201, s45, v201\n"
"v_mfma_f32_16x16x32_bf16 v[96:99], a[196:199], a[188:191], v[96:99]\n"
"v_mul_f32 v181, s45, v181\n"
"v_mul_f32 v196, s45, v196\n"
"v_mfma_f32_16x16x32_bf16 v[0:3], a[192:195], a[160:163], v[0:3]\n"
"v_mul_f32 v145, s45, v145\n"
"v_mul_f32 v168, s45, v168\n"
"v_mfma_f32_16x16x32_bf16 v[0:3], a[196:199], a[164:167], v[0:3]\n"
"v_mul_f32 v142, s45, v142\n"
"v_cvt_pk_bf16_f32 v168, v168, v169\n"
"v_mfma_f32_16x16x32_bf16 v[4:7], a[200:203], a[160:163], v[4:7]\n"
"v_mul_f32 v186, s45, v186\n"
"v_mul_f32 v192, s45, v192\n"
"v_mfma_f32_16x16x32_bf16 v[4:7], a[204:207], a[164:167], v[4:7]\n"
"v_cvt_pk_bf16_f32 v153, v154, v155\n"
"v_cvt_pk_bf16_f32 v169, v170, v171\n"
"v_cvt_pk_bf16_f32 v184, v184, v185\n"
"v_mul_f32 v193, s45, v193\n"
"v_mul_f32 v206, s45, v206\n"
"v_cvt_pk_bf16_f32 v200, v200, v201\n"
"v_cvt_pk_bf16_f32 v201, v202, v203\n"
"v_mul_f32 v104, s45, v104\n"
"v_mfma_f32_16x16x32_bf16 v[8:11], a[208:211], a[160:163], v[8:11]\n"
"v_mul_f32 v179, s45, v179\n"
"v_mul_f32 v182, s45, v182\n"
"v_mfma_f32_16x16x32_bf16 v[8:11], a[212:215], a[164:167], v[8:11]\n"
"v_cvt_pk_bf16_f32 v176, v176, v177\n"
"v_mul_f32 v194, s45, v194\n"
"v_mfma_f32_16x16x32_bf16 v[12:15], a[216:219], a[160:163], v[12:15]\n"
"s_add_u32 s40, s56, s52\n"
"v_mul_f32 v163, s45, v163\n"
"v_cvt_pk_bf16_f32 v161, v162, v163\n"
"v_mfma_f32_16x16x32_bf16 v[12:15], a[220:223], a[164:167], v[12:15]\n"
"buffer_load_dwordx4 v240, s[16:19], s40 offen offset:1024 nt lds\n"
"v_mul_f32 v134, s45, v134\n"
"v_cvt_pk_bf16_f32 v129, v130, v131\n"
"v_mul_f32 v144, s45, v144\n"
"v_mul_f32 v180, s45, v180\n"
"v_cvt_pk_bf16_f32 v203, v206, v207\n"
"v_bfe_u32 v206, v242, 8, 2\n"
"v_lshl_add_u32 v206, v206, 4, s43\n"
"v_mfma_f32_16x16x32_bf16 v[16:19], a[224:227], a[160:163], v[16:19]\n"
"v_cvt_pk_bf16_f32 v218, v220, v221\n"
"v_cvt_pk_bf16_f32 v185, v186, v187\n"
"v_mfma_f32_16x16x32_bf16 v[16:19], a[228:231], a[164:167], v[16:19]\n"
"v_cvt_pk_bf16_f32 v219, v222, v223\n"
"v_cvt_pk_bf16_f32 v154, v156, v157\n"
"v_mfma_f32_16x16x32_bf16 v[20:23], a[232:235], a[160:163], v[20:23]\n"
"v_mul_f32 v140, s45, v140\n"
"v_mul_f32 v197, s45, v197\n"
"v_mfma_f32_16x16x32_bf16 v[20:23], a[236:239], a[164:167], v[20:23]\n"
"v_cvt_pk_bf16_f32 v211, v214, v215\n"
"v_mul_f32 v1, s45, v1\n"
"v_mfma_f32_16x16x32_bf16 v[24:27], a[240:243], a[160:163], v[24:27]\n"
"v_permlane16_swap_b32 v216, v218\n"
"v_mfma_f32_16x16x32_bf16 v[24:27], a[244:247], a[164:167], v[24:27]\n"
"v_cvt_pk_bf16_f32 v171, v174, v175\n"
"v_mul_f32 v111, s45, v111\n"
"v_mfma_f32_16x16x32_bf16 v[28:31], a[248:251], a[160:163], v[28:31]\n"
"v_cvt_pk_bf16_f32 v177, v178, v179\n"
"v_mul_f32 v199, s45, v199\n"
"v_mfma_f32_16x16x32_bf16 v[28:31], a[252:255], a[164:167], v[28:31]\n"
"v_mul_f32 v172, s45, v172\n"
"v_xor_b32 v207, 64, v206\n"
"v_mfma_f32_16x16x32_bf16 v[60:63], a[248:251], a[168:171], v[60:63]\n"
"v_mul_f32 v198, s45, v198\n"
"v_mul_f32 v97, s45, v97\n"
"v_mfma_f32_16x16x32_bf16 v[60:63], a[252:255], a[172:175], v[60:63]\n"
"v_cvt_pk_bf16_f32 v187, v190, v191\n"
"v_mul_f32 v105, s45, v105\n"
"v_mfma_f32_16x16x32_bf16 v[56:59], a[240:243], a[168:171], v[56:59]\n"
"s_add_u32 s40, s56, s53\n"
"v_permlane16_swap_b32 v209, v211\n"
"v_mfma_f32_16x16x32_bf16 v[56:59], a[244:247], a[172:175], v[56:59]\n"
"buffer_load_dwordx4 v240, s[16:19], s40 offen offset:2048 nt lds\n"
"v_cvt_pk_bf16_f32 v121, v122, v123\n"
"v_permlane16_swap_b32 v208, v210\n"
"v_permlane16_swap_b32 v152, v154\n"
"v_cvt_pk_bf16_f32 v162, v164, v165\n"
"v_mul_f32 v98, s45, v98\n"
"v_mfma_f32_16x16x32_bf16 v[52:55], a[232:235], a[168:171], v[52:55]\n"
"v_cvt_pk_bf16_f32 v130, v132, v133\n"
"v_cvt_pk_bf16_f32 v131, v134, v135\n"
"v_mfma_f32_16x16x32_bf16 v[52:55], a[236:239], a[172:175], v[52:55]\n"
"v_permlane16_swap_b32 v217, v219\n"
"v_mfma_f32_16x16x32_bf16 v[48:51], a[224:227], a[168:171], v[48:51]\n"
"v_cvt_pk_bf16_f32 v192, v192, v193\n"
"v_mul_f32 v205, s45, v205\n"
"v_mfma_f32_16x16x32_bf16 v[48:51], a[228:231], a[172:175], v[48:51]\n"
"ds_write_b128 v248, v[208:211] offset:8192\n"
"v_permlane16_swap_b32 v185, v187\n"
"v_mul_f32 v5, s45, v5\n"
"v_mfma_f32_16x16x32_bf16 v[44:47], a[216:219], a[168:171], v[44:47]\n"
"v_mul_f32 v112, s45, v112\n"
"v_cvt_pk_bf16_f32 v179, v182, v183\n"
"v_mfma_f32_16x16x32_bf16 v[44:47], a[220:223], a[172:175], v[44:47]\n"
"v_mul_f32 v204, s45, v204\n"
"v_mul_f32 v25, s45, v25\n"
"v_mfma_f32_16x16x32_bf16 v[40:43], a[208:211], a[168:171], v[40:43]\n"
"v_mul_f32 v173, s45, v173\n"
"v_mul_f32 v15, s45, v15\n"
"v_mfma_f32_16x16x32_bf16 v[40:43], a[212:215], a[172:175], v[40:43]\n"
"ds_read_b128 v[212:215], v207 offset:0\n"
"v_mul_f32 v109, s45, v109\n"
"v_mfma_f32_16x16x32_bf16 v[36:39], a[200:203], a[168:171], v[36:39]\n"
"ds_read_b128 v[208:211], v206 offset:0\n"
"v_mul_f32 v147, s45, v147\n"
"v_mfma_f32_16x16x32_bf16 v[36:39], a[204:207], a[172:175], v[36:39]\n"
"v_accvgpr_write_b32 a116, v152\n"
"v_cvt_pk_bf16_f32 v186, v188, v189\n"
"v_mfma_f32_16x16x32_bf16 v[32:35], a[192:195], a[168:171], v[32:35]\n"
"v_mul_f32 v117, s45, v117\n"
"v_cvt_pk_bf16_f32 v104, v104, v105\n"
"v_mfma_f32_16x16x32_bf16 v[32:35], a[196:199], a[172:175], v[32:35]\n"
"v_mul_f32 v126, s45, v126\n"
"v_mul_f32 v151, s45, v151\n"
"v_mfma_f32_16x16x32_bf16 v[64:67], a[192:195], a[176:179], v[64:67]\n"
"s_add_u32 s40, s56, s54\n"
"v_permlane16_swap_b32 v177, v179\n"
"v_mfma_f32_16x16x32_bf16 v[64:67], a[196:199], a[180:183], v[64:67]\n"
"buffer_load_dwordx4 v240, s[16:19], s40 offen offset:3072 nt lds\n"
"v_cvt_pk_bf16_f32 v136, v136, v137\n"
"v_mul_f32 v146, s45, v146\n"
"v_permlane16_swap_b32 v160, v162\n"
"v_cvt_pk_bf16_f32 v170, v172, v173\n"
"v_accvgpr_write_b32 a133, v185\n"
"v_mul_f32 v47, s45, v47\n"
"v_mul_f32 v53, s45, v53\n"
"v_mfma_f32_16x16x32_bf16 v[68:71], a[200:203], a[176:179], v[68:71]\n"
"v_permlane16_swap_b32 v168, v170\n"
"v_mfma_f32_16x16x32_bf16 v[68:71], a[204:207], a[180:183], v[68:71]\n"
"v_mul_f32 v127, s45, v127\n"
"v_permlane16_swap_b32 v129, v131\n"
"v_cvt_pk_bf16_f32 v155, v158, v159\n"
"v_cvt_pk_bf16_f32 v193, v194, v195\n"
"v_cvt_pk_bf16_f32 v202, v204, v205\n"
"v_permlane16_swap_b32 v201, v203\n"
"v_mul_f32 v17, s45, v17\n"
"v_mul_f32 v40, s45, v40\n"
"v_mfma_f32_16x16x32_bf16 v[72:75], a[208:211], a[176:179], v[72:75]\n"
"ds_read_b128 v[220:223], v207 offset:1024\n"
"v_mul_f32 v42, s45, v42\n"
"v_mfma_f32_16x16x32_bf16 v[72:75], a[212:215], a[180:183], v[72:75]\n"
"v_accvgpr_write_b32 a118, v154\n"
"v_accvgpr_write_b32 a122, v162\n"
"v_cvt_pk_bf16_f32 v178, v180, v181\n"
"v_permlane16_swap_b32 v184, v186\n"
"v_accvgpr_write_b32 a132, v184\n"
"v_accvgpr_write_b32 a135, v187\n"
"v_mul_f32 v96, s45, v96\n"
"v_mul_f32 v3, s45, v3\n"
"v_mfma_f32_16x16x32_bf16 v[76:79], a[216:219], a[176:179], v[76:79]\n"
"v_mul_f32 v150, s45, v150\n"
"v_cvt_pk_bf16_f32 v194, v196, v197\n"
"v_mfma_f32_16x16x32_bf16 v[76:79], a[220:223], a[180:183], v[76:79]\n"
"ds_write_b128 v248, v[216:219] offset:10240\n"
"v_permlane16_swap_b32 v128, v130\n"
"v_mul_f32 v167, s45, v167\n"
"v_accvgpr_write_b32 a120, v160\n"
"v_mul_f32 v99, s45, v99\n"
"v_mfma_f32_16x16x32_bf16 v[88:91], a[240:243], a[176:179], v[88:91]\n"
"v_cvt_pk_bf16_f32 v163, v166, v167\n"
"v_mul_f32 v108, s45, v108\n"
"v_mfma_f32_16x16x32_bf16 v[88:91], a[244:247], a[180:183], v[88:91]\n"
"ds_read_b128 v[216:219], v206 offset:1024\n"
"v_mul_f32 v143, s45, v143\n"
"v_accvgpr_write_b32 a131, v179\n"
"v_cvt_pk_bf16_f32 v195, v198, v199\n"
"v_permlane16_swap_b32 v192, v194\n"
"v_mul_f32 v72, s45, v72\n"
"v_xor_b32 v207, 0x400, v248\n"
"v_mov_b32 v197, 0\n"
"v_mfma_f32_16x16x32_bf16 v[92:95], a[248:251], a[176:179], v[92:95]\n"
"v_permlane16_swap_b32 v161, v163\n"
"v_mfma_f32_16x16x32_bf16 v[92:95], a[252:255], a[180:183], v[92:95]\n"
"v_mul_f32 v116, s45, v116\n"
"v_cvt_pk_bf16_f32 v144, v144, v145\n"
"v_permlane16_swap_b32 v153, v155\n"
"v_accvgpr_write_b32 a121, v161\n"
"v_accvgpr_write_b32 a126, v170\n"
"v_mul_f32 v78, s45, v78\n"
"v_mul_f32 v79, s45, v79\n"
"v_mfma_f32_16x16x32_bf16 v[80:83], a[224:227], a[176:179], v[80:83]\n"
"ds_read_b128 a[196:199], v207 offset:0\n"
"v_accvgpr_write_b32 a134, v186\n"
"v_mfma_f32_16x16x32_bf16 v[80:83], a[228:231], a[180:183], v[80:83]\n"
"ds_read_b128 a[192:195], v248 offset:0\n"
"v_cvt_pk_bf16_f32 v112, v112, v113\n"
"v_mul_f32 v138, s45, v138\n"
"v_cvt_pk_bf16_f32 v145, v146, v147\n"
"v_permlane16_swap_b32 v176, v178\n"
"v_mul_f32 v107, s45, v107\n"
"v_mul_f32 v110, s45, v110\n"
"v_mul_f32 v45, s45, v45\n"
"v_mul_f32 v57, s45, v57\n"
"v_mfma_f32_16x16x32_bf16 v[84:87], a[232:235], a[176:179], v[84:87]\n"
"v_permlane16_swap_b32 v169, v171\n"
"v_mfma_f32_16x16x32_bf16 v[84:87], a[236:239], a[180:183], v[84:87]\n"
"s_waitcnt lgkmcnt(3)\n"
"ds_read_b128 a[204:207], v207 offset:2048\n"
"v_accvgpr_write_b32 a130, v178\n"
"v_mfma_f32_16x16x32_bf16 v[180:183], v[208:211], a[0:3], 0\n"
"v_permlane16_swap_b32 v193, v195\n"
"v_mfma_f32_16x16x32_bf16 v[180:183], v[212:215], a[4:7], v[180:183]\n"
"s_waitcnt lgkmcnt(3)\n"
"ds_read_b128 a[200:203], v248 offset:2048\n"
"v_mul_f32 v141, s45, v141\n"
"v_mfma_f32_16x16x32_bf16 v[180:183], v[216:219], a[8:11], v[180:183]\n"
"v_cvt_pk_bf16_f32 v96, v96, v97\n"
"v_accvgpr_write_b32 a242, v249\n"
"v_mfma_f32_16x16x32_bf16 v[180:183], v[220:223], a[12:15], v[180:183]\n"
"v_mul_f32 v103, s45, v103\n"
"v_mul_f32 v31, s45, v31\n"
"v_mfma_f32_16x16x32_bf16 v[184:187], v[208:211], a[16:19], 0\n"
"v_cvt_pk_bf16_f32 v147, v150, v151\n"
"v_accvgpr_write_b32 a138, v194\n"
"v_mfma_f32_16x16x32_bf16 v[184:187], v[212:215], a[20:23], v[184:187]\n"
"v_mul_f32 v139, s45, v139\n"
"v_accvgpr_write_b32 a139, v195\n"
"v_mfma_f32_16x16x32_bf16 v[184:187], v[216:219], a[24:27], v[184:187]\n"
"v_accvgpr_write_b32 a117, v153\n"
"v_mul_f32 v106, s45, v106\n"
"v_mfma_f32_16x16x32_bf16 v[184:187], v[220:223], a[28:31], v[184:187]\n"
"v_cvt_pk_bf16_f32 v137, v138, v139\n"
"v_accvgpr_write_b32 a124, v168\n"
"v_accvgpr_write_b32 a136, v192\n"
"v_accvgpr_write_b32 a137, v193\n"
"v_permlane16_swap_b32 v200, v202\n"
"v_mul_f32 v11, s45, v11\n"
"v_mul_f32 v27, s45, v27\n"
"v_mul_f32 v89, s45, v89\n"
"v_xor_b32 v180, s77, v180\n"
"v_mfma_f32_16x16x32_bf16 v[188:191], v[208:211], a[32:35], 0\n"
"v_accvgpr_write_b32 a119, v155\n"
"v_mul_f32 v93, s45, v93\n"
"v_mfma_f32_16x16x32_bf16 v[188:191], v[212:215], a[36:39], v[188:191]\n"
"v_mul_f32 v75, s45, v75\n"
"v_mov_b32 v204, 0\n"
"v_mfma_f32_16x16x32_bf16 v[188:191], v[216:219], a[40:43], v[188:191]\n"
"v_mul_f32 v73, s45, v73\n"
"v_xor_b32 v184, s77, v184\n"
"v_mfma_f32_16x16x32_bf16 v[188:191], v[220:223], a[44:47], v[188:191]\n"
"s_waitcnt lgkmcnt(0)\n"
"v_cvt_pk_bf16_f32 v146, v148, v149\n"
"v_accvgpr_write_b32 a127, v171\n"
"v_mul_f32 v0, s45, v0\n"
"v_mul_f32 v10, s45, v10\n"
"v_mul_f32 v24, s45, v24\n"
"v_mul_f32 v46, s45, v46\n"
"v_mul_f32 v49, s45, v49\n"
"v_xor_b32 v181, s77, v181\n"
"v_xor_b32 v185, s77, v185\n"
"v_xor_b32 v187, s77, v187\n"
"v_mfma_f32_16x16x32_bf16 v[192:195], v[208:211], a[192:195], 0\n"
"ds_read_b128 v[208:211], v228 offset:0\n"
"v_xor_b32 v183, s77, v183\n"
"v_mfma_f32_16x16x32_bf16 v[192:195], v[212:215], a[196:199], v[192:195]\n"
"ds_read_b128 v[212:215], v229 offset:0\n"
"v_xor_b32 v182, s77, v182\n"
"v_mfma_f32_16x16x32_bf16 v[192:195], v[216:219], a[200:203], v[192:195]\n"
"ds_read_b128 v[216:219], v228 offset:1024\n"
"v_accvgpr_write_b32 a123, v163\n"
"v_mfma_f32_16x16x32_bf16 v[192:195], v[220:223], a[204:207], v[192:195]\n"
"s_waitcnt lgkmcnt(2)\n"
"ds_read_b128 v[220:223], v229 offset:1024\n"
"v_cvt_pk_bf16_f32 v139, v142, v143\n"
"v_permlane16_swap_b32 v145, v147\n"
"v_accvgpr_write_b32 a140, v200\n"
"v_accvgpr_write_b32 a143, v203\n"
"v_cvt_pk_bf16_f32 v97, v98, v99\n"
"v_mul_f32 v74, s45, v74\n"
"v_xor_b32 v188, s77, v188\n"
"v_xor_b32 v190, s77, v190\n"
"v_mfma_f32_16x16x32_bf16 v[152:155], v[208:211], a[0:3], v[180:183]\n"
"s_waitcnt lgkmcnt(2)\n"
"v_mul_f32 v125, s45, v125\n"
"v_mul_f32 v26, s45, v26\n"
"v_mfma_f32_16x16x32_bf16 v[152:155], v[212:215], a[4:7], v[152:155]\n"
"s_waitcnt lgkmcnt(1)\n"
"v_mul_f32 v81, s45, v81\n"
"v_xor_b32 v186, s77, v186\n"
"v_mfma_f32_16x16x32_bf16 v[152:155], v[216:219], a[8:11], v[152:155]\n"
"s_waitcnt lgkmcnt(0)\n"
"v_accvgpr_write_b32 a141, v201\n"
"v_mul_f32 v41, s45, v41\n"
"v_mfma_f32_16x16x32_bf16 v[152:155], v[220:223], a[12:15], v[152:155]\n"
"v_cvt_pk_bf16_f32 v113, v114, v115\n"
"v_cvt_pk_bf16_f32 v138, v140, v141\n"
"v_accvgpr_write_b32 a125, v169\n"
"v_cvt_pk_bf16_f32 v105, v106, v107\n"
"v_mul_f32 v28, s45, v28\n"
"v_mul_f32 v30, s45, v30\n"
"v_cvt_pk_bf16_f32 v24, v24, v25\n"
"v_cvt_pk_bf16_f32 v72, v72, v73\n"
"v_xor_b32 v191, s77, v191\n"
"v_xor_b32 v194, s77, v194\n"
"v_mfma_f32_16x16x32_bf16 v[156:159], v[216:219], a[24:27], v[184:187]\n"
"v_mul_f32 v90, s45, v90\n"
"v_xor_b32 v193, s77, v193\n"
"v_mfma_f32_16x16x32_bf16 v[156:159], v[220:223], a[28:31], v[156:159]\n"
"v_accvgpr_write_b32 a241, v249\n"
"v_xor_b32 v195, s77, v195\n"
"v_mfma_f32_16x16x32_bf16 v[156:159], v[208:211], a[16:19], v[156:159]\n"
"v_mul_f32 v43, s45, v43\n"
"v_xor_b32 v189, s77, v189\n"
"v_mfma_f32_16x16x32_bf16 v[156:159], v[212:215], a[20:23], v[156:159]\n"
"v_permlane16_swap_b32 v137, v139\n"
"v_mfma_f32_16x16x32_bf16 v[160:163], v[208:211], a[32:35], v[188:191]\n"
"v_mul_f32 v100, s45, v100\n"
"v_mul_f32 v44, s45, v44\n"
"v_mfma_f32_16x16x32_bf16 v[160:163], v[212:215], a[36:39], v[160:163]\n"
"v_mul_f32 v8, s45, v8\n"
"v_xor_b32 v192, s77, v192\n"
"v_mfma_f32_16x16x32_bf16 v[160:163], v[216:219], a[40:43], v[160:163]\n"
"v_exp_f32 v154, v154\n"
"v_mfma_f32_16x16x32_bf16 v[160:163], v[220:223], a[44:47], v[160:163]\n"
"v_permlane16_swap_b32 v136, v138\n"
"v_accvgpr_write_b32 a115, v147\n"
"v_mul_f32 v76, s45, v76\n"
"v_mul_f32 v88, s45, v88\n"
"v_exp_f32 v155, v155\n"
"v_exp_f32 v157, v157\n"
"v_exp_f32 v159, v159\n"
"v_mfma_f32_16x16x32_bf16 v[164:167], v[216:219], a[200:203], v[192:195]\n"
"ds_read_b128 v[216:219], v228 offset:5120\n"
"v_mul_f32 v101, s45, v101\n"
"v_mfma_f32_16x16x32_bf16 v[164:167], v[220:223], a[204:207], v[164:167]\n"
"ds_read_b128 v[220:223], v229 offset:5120\n"
"v_mul_f32 v65, s45, v65\n"
"v_mfma_f32_16x16x32_bf16 v[164:167], v[208:211], a[192:195], v[164:167]\n"
"ds_read_b128 v[208:211], v228 offset:4096\n"
"v_accvgpr_write_b32 a104, v128\n"
"v_accvgpr_write_b32 a108, v136\n"
"v_accvgpr_write_b32 a128, v176\n"
"v_mul_f32 v13, s45, v13\n"
"v_cvt_pk_bf16_f32 v40, v40, v41\n"
"v_mul_f32 v52, s45, v52\n"
"v_exp_f32 v152, v152\n"
"v_exp_f32 v156, v156\n"
"v_mfma_f32_16x16x32_bf16 v[164:167], v[212:215], a[196:199], v[164:167]\n"
"s_waitcnt lgkmcnt(2)\n"
"ds_read_b128 v[212:215], v229 offset:4096\n"
"v_accvgpr_write_b32 a105, v129\n"
"v_accvgpr_write_b32 a113, v145\n"
"v_accvgpr_write_b32 a129, v177\n"
"v_mul_f32 v102, s45, v102\n"
"v_cvt_pk_bf16_f32 v107, v110, v111\n"
"v_mul_f32 v32, s45, v32\n"
"v_mul_f32 v35, s45, v35\n"
"v_exp_f32 v161, v161\n"
"v_mfma_f32_16x16x32_bf16 v[168:171], v[216:219], a[8:11], v[180:183]\n"
"s_waitcnt lgkmcnt(2)\n"
"v_cvt_pk_bf16_f32 v106, v108, v109\n"
"v_accvgpr_write_b32 a243, v249\n"
"v_mfma_f32_16x16x32_bf16 v[168:171], v[220:223], a[12:15], v[168:171]\n"
"s_waitcnt lgkmcnt(1)\n"
"v_exp_f32 v153, v153\n"
"v_mfma_f32_16x16x32_bf16 v[168:171], v[208:211], a[0:3], v[168:171]\n"
"s_waitcnt lgkmcnt(0)\n"
"v_exp_f32 v163, v163\n"
"v_mfma_f32_16x16x32_bf16 v[168:171], v[212:215], a[4:7], v[168:171]\n"
"v_mul_f32 v118, s45, v118\n"
"v_permlane16_swap_b32 v144, v146\n"
"v_cvt_pk_bf16_f32 v99, v102, v103\n"
"v_mul_f32 v4, s45, v4\n"
"v_mul_f32 v91, s45, v91\n"
"v_exp_f32 v158, v158\n"
"v_exp_f32 v165, v165\n"
"v_mfma_f32_16x16x32_bf16 v[172:175], v[208:211], a[16:19], v[184:187]\n"
"v_mul_f32 v92, s45, v92\n"
"v_cvt_pk_bf16_f32 v129, v154, v155\n"
"v_mfma_f32_16x16x32_bf16 v[172:175], v[212:215], a[20:23], v[172:175]\n"
"v_cvt_pk_bf16_f32 v41, v42, v43\n"
"v_cvt_pk_bf16_f32 v42, v44, v45\n"
"v_cvt_pk_bf16_f32 v43, v46, v47\n"
"v_mul_f32 v77, s45, v77\n"
"v_mov_b32 v196, 0\n"
"v_cvt_pk_bf16_f32 v128, v152, v153\n"
"v_exp_f32 v160, v160\n"
"v_exp_f32 v162, v162\n"
"v_mfma_f32_16x16x32_bf16 v[172:175], v[216:219], a[24:27], v[172:175]\n"
"v_mul_f32 v48, s45, v48\n"
"v_mul_f32 v51, s45, v51\n"
"v_mfma_f32_16x16x32_bf16 v[172:175], v[220:223], a[28:31], v[172:175]\n"
"v_accvgpr_write_b32 a106, v130\n"
"v_accvgpr_write_b32 a109, v137\n"
"v_accvgpr_write_b32 a112, v144\n"
"v_permlane16_swap_b32 v105, v107\n"
"v_mul_f32 v9, s45, v9\n"
"v_mul_f32 v14, s45, v14\n"
"v_mul_f32 v95, s45, v95\n"
"v_accvgpr_write_b32 a240, v249\n"
"v_cvt_pk_bf16_f32 v136, v156, v157\n"
"v_mfma_f32_16x16x32_bf16 v[176:179], v[216:219], a[40:43], v[188:191]\n"
"v_mul_f32 v19, s45, v19\n"
"v_mul_f32 v66, s45, v66\n"
"v_mfma_f32_16x16x32_bf16 v[176:179], v[220:223], a[44:47], v[176:179]\n"
"v_exp_f32 v167, v167\n"
"v_mfma_f32_16x16x32_bf16 v[176:179], v[208:211], a[32:35], v[176:179]\n"
"v_mul_f32 v2, s45, v2\n"
"v_cvt_pk_bf16_f32 v137, v158, v159\n"
"v_mfma_f32_16x16x32_bf16 v[176:179], v[212:215], a[36:39], v[176:179]\n"
"v_cvt_pk_bf16_f32 v122, v124, v125\n"
"v_accvgpr_write_b32 a142, v202\n"
"v_cvt_pk_bf16_f32 v0, v0, v1\n"
"v_mul_f32 v33, s45, v33\n"
"v_mul_f32 v50, s45, v50\n"
"v_mul_f32 v94, s45, v94\n"
"v_exp_f32 v169, v169\n"
"v_exp_f32 v171, v171\n"
"v_mfma_f32_16x16x32_bf16 v[152:155], v[208:211], a[192:195], v[192:195]\n"
"ds_read_b128 v[208:211], v230 offset:0\n"
"v_cvt_pk_bf16_f32 v123, v126, v127\n"
"v_mfma_f32_16x16x32_bf16 v[152:155], v[212:215], a[196:199], v[152:155]\n"
"ds_read_b128 v[212:215], v231 offset:0\n"
"v_mul_f32 v69, s45, v69\n"
"v_mfma_f32_16x16x32_bf16 v[152:155], v[216:219], a[200:203], v[152:155]\n"
"ds_read_b128 v[216:219], v230 offset:1024\n"
"v_mul_f32 v56, s45, v56\n"
"v_mul_f32 v58, s45, v58\n"
"v_cvt_pk_bf16_f32 v88, v88, v89\n"
"v_cvt_pk_bf16_f32 v144, v160, v161\n"
"v_cvt_pk_bf16_f32 v145, v162, v163\n"
"v_exp_f32 v168, v168\n"
"v_exp_f32 v172, v172\n"
"v_mfma_f32_16x16x32_bf16 v[152:155], v[220:223], a[204:207], v[152:155]\n"
"s_waitcnt lgkmcnt(2)\n"
"ds_read_b128 v[220:223], v231 offset:1024\n"
"v_cvt_pk_bf16_f32 v98, v100, v101\n"
"v_permlane16_swap_b32 v40, v42\n"
"v_cvt_pk_bf16_f32 v48, v48, v49\n"
"v_exp_f32 v164, v164\n"
"v_cvt_pk_bf16_f32 v130, v168, v169\n"
"v_exp_f32 v170, v170\n"
"v_mfma_f32_16x16x32_bf16 v[156:159], v[208:211], a[0:3], v[180:183]\n"
"s_waitcnt lgkmcnt(2)\n"
"v_mul_f32 v18, s45, v18\n"
"v_cvt_pk_bf16_f32 v25, v26, v27\n"
"v_mfma_f32_16x16x32_bf16 v[156:159], v[212:215], a[4:7], v[156:159]\n"
"s_waitcnt lgkmcnt(1)\n"
"v_cvt_pk_bf16_f32 v114, v116, v117\n"
"v_mul_f32 v12, s45, v12\n"
"v_mfma_f32_16x16x32_bf16 v[156:159], v[216:219], a[8:11], v[156:159]\n"
"s_waitcnt lgkmcnt(0)\n"
"v_exp_f32 v173, v173\n"
"v_mfma_f32_16x16x32_bf16 v[156:159], v[220:223], a[12:15], v[156:159]\n"
"v_permlane16_swap_b32 v97, v99\n"
"v_cvt_pk_bf16_f32 v8, v8, v9\n"
"v_cvt_pk_bf16_f32 v9, v10, v11\n"
"v_permlane16_swap_b32 v41, v43\n"
"v_cvt_pk_bf16_f32 v49, v50, v51\n"
"v_cvt_pk_bf16_f32 v73, v74, v75\n"
"v_exp_f32 v174, v174\n"
"v_mfma_f32_16x16x32_bf16 v[160:163], v[216:219], a[24:27], v[184:187]\n"
"v_accvgpr_write_b32 a110, v138\n"
"v_accvgpr_write_b32 a114, v146\n"
"v_permlane16_swap_b32 v96, v98\n"
"v_cvt_pk_bf16_f32 v11, v14, v15\n"
"v_cvt_pk_bf16_f32 v74, v76, v77\n"
"v_exp_f32 v166, v166\n"
"v_exp_f32 v177, v177\n"
"v_mfma_f32_16x16x32_bf16 v[160:163], v[220:223], a[28:31], v[160:163]\n"
"v_mul_f32 v85, s45, v85\n"
"v_cvt_pk_bf16_f32 v138, v172, v173\n"
"v_mfma_f32_16x16x32_bf16 v[160:163], v[208:211], a[16:19], v[160:163]\n"
"v_mul_f32 v34, s45, v34\n"
"v_cvt_pk_bf16_f32 v56, v56, v57\n"
"v_mfma_f32_16x16x32_bf16 v[160:163], v[212:215], a[20:23], v[160:163]\n"
"v_accvgpr_write_b32 a107, v131\n"
"v_mul_f32 v64, s45, v64\n"
"v_cvt_pk_bf16_f32 v124, v164, v165\n"
"v_cvt_pk_bf16_f32 v125, v166, v167\n"
"v_exp_f32 v175, v175\n"
"v_exp_f32 v179, v179\n"
"v_exp_f32 v158, v158\n"
"v_mfma_f32_16x16x32_bf16 v[164:167], v[208:211], a[32:35], v[188:191]\n"
"ds_read_b128 v[208:211], v230 offset:4096\n"
"v_accvgpr_write_b32 a111, v139\n"
"v_cvt_pk_bf16_f32 v10, v12, v13\n"
"v_mul_f32 v29, s45, v29\n"
"v_permlane16_swap_b32 v72, v74\n"
"v_exp_f32 v178, v178\n"
"v_exp_f32 v156, v156\n"
"v_mfma_f32_16x16x32_bf16 v[164:167], v[212:215], a[36:39], v[164:167]\n"
"ds_read_b128 v[212:215], v231 offset:4096\n"
"v_mul_f32 v7, s45, v7\n"
"v_mfma_f32_16x16x32_bf16 v[164:167], v[216:219], a[40:43], v[164:167]\n"
"ds_read_b128 v[216:219], v230 offset:5120\n"
"v_cvt_pk_bf16_f32 v131, v170, v171\n"
"v_mfma_f32_16x16x32_bf16 v[164:167], v[220:223], a[44:47], v[164:167]\n"
"s_waitcnt lgkmcnt(2)\n"
"ds_read_b128 v[220:223], v231 offset:5120\n"
"v_permlane16_swap_b32 v104, v106\n"
"v_cvt_pk_bf16_f32 v75, v78, v79\n"
"v_exp_f32 v154, v154\n"
"v_exp_f32 v159, v159\n"
"v_exp_f32 v160, v160\n"
"v_mfma_f32_16x16x32_bf16 v[168:171], v[208:211], a[0:3], v[180:183]\n"
"s_waitcnt lgkmcnt(2)\n"
"v_exp_f32 v162, v162\n"
"v_mfma_f32_16x16x32_bf16 v[168:171], v[212:215], a[4:7], v[168:171]\n"
"s_waitcnt lgkmcnt(1)\n"
"v_exp_f32 v157, v157\n"
"v_mfma_f32_16x16x32_bf16 v[168:171], v[216:219], a[8:11], v[168:171]\n"
"s_waitcnt lgkmcnt(0)\n"
"ds_write_b128 v248, v[96:99] offset:12288\n"
"v_cvt_pk_bf16_f32 v89, v90, v91\n"
"v_exp_f32 v176, v176\n"
"v_exp_f32 v161, v161\n"
"v_exp_f32 v166, v166\n"
"v_mfma_f32_16x16x32_bf16 v[168:171], v[220:223], a[12:15], v[168:171]\n"
"v_permlane16_swap_b32 v121, v123\n"
"v_mul_f32 v60, s45, v60\n"
"v_cvt_pk_bf16_f32 v139, v174, v175\n"
"v_exp_f32 v153, v153\n"
"v_exp_f32 v155, v155\n"
"v_exp_f32 v167, v167\n"
"v_mfma_f32_16x16x32_bf16 v[172:175], v[216:219], a[24:27], v[184:187]\n"
"v_mov_b32 v200, 0\n"
"v_cvt_pk_bf16_f32 v147, v178, v179\n"
"v_mfma_f32_16x16x32_bf16 v[172:175], v[220:223], a[28:31], v[172:175]\n"
"v_exp_f32 v152, v152\n"
"v_mfma_f32_16x16x32_bf16 v[172:175], v[208:211], a[16:19], v[172:175]\n"
"v_cvt_pk_bf16_f32 v146, v176, v177\n"
"v_cvt_pk_bf16_f32 v133, v158, v159\n"
"v_mfma_f32_16x16x32_bf16 v[172:175], v[212:215], a[20:23], v[172:175]\n"
"s_cbranch_vccnz LPCE0_%=\nLPKE0_%=:\n"
"ds_write_b128 v248, v[104:107] offset:14336\n"
"v_accvgpr_write_b32 a182, v74\n"
"v_cvt_pk_bf16_f32 v127, v154, v155\n"
"v_exp_f32 v163, v163\n"
"v_mfma_f32_16x16x32_bf16 v[176:179], v[208:211], a[32:35], v[188:191]\n"
"v_cvt_pk_bf16_f32 v126, v152, v153\n"
"v_cvt_pk_bf16_f32 v140, v160, v161\n"
"v_mfma_f32_16x16x32_bf16 v[176:179], v[212:215], a[36:39], v[176:179]\n"
"v_mul_f32 v16, s45, v16\n"
"v_cvt_pk_bf16_f32 v64, v64, v65\n"
"v_mfma_f32_16x16x32_bf16 v[176:179], v[216:219], a[40:43], v[176:179]\n"
"v_cvt_pk_bf16_f32 v132, v156, v157\n"
"v_cvt_pk_bf16_f32 v149, v166, v167\n"
"v_mfma_f32_16x16x32_bf16 v[176:179], v[220:223], a[44:47], v[176:179]\n"
"v_permlane16_swap_b32 v8, v10\n"
"v_permlane16_swap_b32 v9, v11\n"
"v_accvgpr_write_b32 a150, v10\n"
"v_accvgpr_write_b32 a164, v40\n"
"v_exp_f32 v165, v165\n"
"v_exp_f32 v169, v169\n"
"s_waitcnt vmcnt(8) lgkmcnt(0)\n"
"s_barrier\n"
"ds_read_b128 v[208:211], v228 offset:16384\n"
"ds_read_b128 v[212:215], v229 offset:16384\n"
"ds_read_b128 v[216:219], v228 offset:17408\n"
"ds_read_b128 v[220:223], v229 offset:17408\n"
"v_accvgpr_write_b32 a180, v72\n"
"s_waitcnt lgkmcnt(3)\n"
"v_mfma_f32_16x16x32_bf16 v[152:155], v[208:211], a[0:3], v[180:183]\n"
"v_exp_f32 v164, v164\n"
"s_waitcnt lgkmcnt(2)\n"
"v_mfma_f32_16x16x32_bf16 v[152:155], v[212:215], a[4:7], v[152:155]\n"
"ds_read_b64_tr_b16 a[248:249], v238 offset:0\n"
"v_mul_f32 v23, s45, v23\n"
"s_waitcnt lgkmcnt(2)\n"
"v_mfma_f32_16x16x32_bf16 v[152:155], v[216:219], a[8:11], v[152:155]\n"
"s_waitcnt lgkmcnt(1)\n"
"s_add_u32 m0, s44, 98304\n"
"ds_read_b64_tr_b16 v[226:227], v239 offset:4096\n"
"v_cvt_pk_bf16_f32 v141, v162, v163\n"
"v_mfma_f32_16x16x32_bf16 v[152:155], v[220:223], a[12:15], v[152:155]\n"
"buffer_load_dwordx4 v240, s[20:23], s56 offen nt lds\n"
"v_accvgpr_write_b32 a149, v9\n"
"v_accvgpr_write_b32 a166, v42\n"
"v_exp_f32 v170, v170\n"
"v_exp_f32 v177, v177\n"
"v_exp_f32 v179, v179\n"
"v_mfma_f32_16x16x32_bf16 v[156:159], v[216:219], a[24:27], v[184:187]\n"
"v_exp_f32 v171, v171\n"
"v_mfma_f32_16x16x32_bf16 v[156:159], v[220:223], a[28:31], v[156:159]\n"
"v_cvt_pk_bf16_f32 v27, v30, v31\n"
"v_mul_f32 v62, s45, v62\n"
"v_mfma_f32_16x16x32_bf16 v[156:159], v[208:211], a[16:19], v[156:159]\n"
"ds_read_b64_tr_b16 a[244:245], v234 offset:0\n"
"v_mul_f32 v67, s45, v67\n"
"v_mfma_f32_16x16x32_bf16 v[156:159], v[212:215], a[20:23], v[156:159]\n"
"ds_read_b64_tr_b16 a[250:251], v238 offset:4096\n"
"v_cvt_pk_bf16_f32 v1, v2, v3\n"
"v_cvt_pk_bf16_f32 v26, v28, v29\n"
"v_mul_f32 v61, s45, v61\n"
"v_mul_f32 v87, s45, v87\n"
"v_cvt_pk_bf16_f32 v135, v170, v171\n"
"v_exp_f32 v175, v175\n"
"v_exp_f32 v178, v178\n"
"v_mfma_f32_16x16x32_bf16 v[160:163], v[208:211], a[32:35], v[188:191]\n"
"ds_read_b64_tr_b16 v[224:225], v239 offset:0\n"
"v_accvgpr_write_b32 a151, v11\n"
"v_mul_f32 v54, s45, v54\n"
"v_permlane16_swap_b32 v73, v75\n"
"v_mov_b32 v199, 0\n"
"v_exp_f32 v168, v168\n"
"v_exp_f32 v172, v172\n"
"v_exp_f32 v174, v174\n"
"v_mfma_f32_16x16x32_bf16 v[160:163], v[212:215], a[36:39], v[160:163]\n"
"v_cvt_pk_bf16_f32 v148, v164, v165\n"
"v_cvt_pk_bf16_f32 v143, v174, v175\n"
"v_mfma_f32_16x16x32_bf16 v[160:163], v[216:219], a[40:43], v[160:163]\n"
"v_mul_f32 v37, s45, v37\n"
"v_mul_f32 v59, s45, v59\n"
"v_mfma_f32_16x16x32_bf16 v[160:163], v[220:223], a[44:47], v[160:163]\n"
"ds_read_b64_tr_b16 a[246:247], v234 offset:4096\n"
"v_permlane16_swap_b32 v112, v114\n"
"v_mul_f32 v6, s45, v6\n"
"v_accvgpr_write_b32 a148, v8\n"
"v_cvt_pk_bf16_f32 v16, v16, v17\n"
"v_accvgpr_write_b32 a181, v73\n"
"v_exp_f32 v173, v173\n"
"v_exp_f32 v176, v176\n"
"v_mfma_f32_16x16x32_bf16 v[164:167], v[216:219], a[200:203], v[192:195]\n"
"v_exp_f32 v158, v158\n"
"v_mfma_f32_16x16x32_bf16 v[164:167], v[220:223], a[204:207], v[164:167]\n"
"ds_read_b64_tr_b16 a[254:255], v235 offset:4096\n"
"v_accvgpr_write_b32 a167, v43\n"
"v_mfma_f32_16x16x32_bf16 v[164:167], v[208:211], a[192:195], v[164:167]\n"
"s_add_u32 s40, s56, s52\n"
"ds_read_b64_tr_b16 a[252:253], v235 offset:0\n"
"v_cvt_pk_bf16_f32 v134, v168, v169\n"
"v_mfma_f32_16x16x32_bf16 v[164:167], v[212:215], a[196:199], v[164:167]\n"
"s_waitcnt lgkmcnt(2)\n"
"buffer_load_dwordx4 v240, s[20:23], s40 offen offset:1024 nt lds\n"
"v_cvt_pk_bf16_f32 v115, v118, v119\n"
"v_permlane16_swap_b32 v25, v27\n"
"v_mul_f32 v63, s45, v63\n"
"v_cvt_pk_bf16_f32 v91, v94, v95\n"
"v_cvt_pk_bf16_f32 v151, v178, v179\n"
"v_exp_f32 v162, v162\n"
"v_mfma_f32_16x16x32_bf16 a[216:219], v[124:127], a[244:247], 0\n"
"v_permlane16_swap_b32 v113, v115\n"
"v_mul_f32 v36, s45, v36\n"
"v_mul_f32 v39, s45, v39\n"
"v_accvgpr_write_b32 a183, v75\n"
"v_mul_f32 v83, s45, v83\n"
"v_cvt_pk_bf16_f32 v142, v172, v173\n"
"v_cvt_pk_bf16_f32 v150, v176, v177\n"
"v_exp_f32 v163, v163\n"
"v_mfma_f32_16x16x32_bf16 v[8:11], v[128:131], a[244:247], 0\n"
"v_mul_f32 v38, s45, v38\n"
"v_mov_b32 v201, 0\n"
"v_mfma_f32_16x16x32_bf16 v[8:11], v[132:135], a[248:251], v[8:11]\n"
"ds_read_b128 v[220:223], v229 offset:21504\n"
"v_accvgpr_write_b32 a165, v41\n"
"v_mfma_f32_16x16x32_bf16 v[40:43], v[136:139], a[244:247], 0\n"
"v_cvt_pk_bf16_f32 v32, v32, v33\n"
"v_mov_b32 v205, 0\n"
"v_mfma_f32_16x16x32_bf16 v[40:43], v[140:143], a[248:251], v[40:43]\n"
"v_exp_f32 v157, v157\n"
"v_mfma_f32_16x16x32_bf16 v[72:75], v[144:147], a[244:247], 0\n"
"ds_read_b128 v[212:215], v229 offset:20480\n"
"v_cvt_pk_bf16_f32 v33, v34, v35\n"
"v_mfma_f32_16x16x32_bf16 v[72:75], v[148:151], a[248:251], v[72:75]\n"
"v_cvt_pk_bf16_f32 v2, v4, v5\n"
"v_mov_b32 v202, 0\n"
"v_mfma_f32_16x16x32_bf16 v[76:79], v[148:151], v[224:227], 0\n"
"s_waitcnt lgkmcnt(2)\n"
"ds_read_b128 v[208:211], v228 offset:20480\n"
"v_cvt_pk_bf16_f32 v3, v6, v7\n"
"v_mfma_f32_16x16x32_bf16 v[76:79], v[144:147], a[252:255], v[76:79]\n"
"ds_read_b128 v[216:219], v228 offset:21504\n"
"v_mul_f32 v68, s45, v68\n"
"v_mul_f32 v80, s45, v80\n"
"v_mul_f32 v84, s45, v84\n"
"v_mov_b32 v198, 0\n"
"v_mov_b32 v203, 0\n"
"v_exp_f32 v153, v153\n"
"v_exp_f32 v156, v156\n"
"v_mfma_f32_16x16x32_bf16 v[44:47], v[140:143], v[224:227], 0\n"
"s_add_u32 s40, s56, s53\n"
"v_exp_f32 v152, v152\n"
"v_mfma_f32_16x16x32_bf16 v[44:47], v[136:139], a[252:255], v[44:47]\n"
"buffer_load_dwordx4 v240, s[20:23], s40 offen offset:2048 nt lds\n"
"v_accvgpr_write_b32 a97, v113\n"
"v_permlane16_swap_b32 v24, v26\n"
"v_accvgpr_write_b32 a159, v27\n"
"v_cvt_pk_bf16_f32 v35, v38, v39\n"
"v_cvt_pk_bf16_f32 v90, v92, v93\n"
"v_exp_f32 v167, v167\n"
"v_mfma_f32_16x16x32_bf16 v[12:15], v[132:135], v[224:227], 0\n"
"v_mul_f32 v55, s45, v55\n"
"v_mul_f32 v86, s45, v86\n"
"v_mfma_f32_16x16x32_bf16 v[12:15], v[128:131], a[252:255], v[12:15]\n"
"v_permlane16_swap_b32 v0, v2\n"
"v_cvt_pk_bf16_f32 v57, v58, v59\n"
"v_cvt_pk_bf16_f32 v59, v62, v63\n"
"v_permlane16_swap_b32 v88, v90\n"
"v_cvt_pk_bf16_f32 v96, v152, v153\n"
"v_exp_f32 v155, v155\n"
"v_cvt_pk_bf16_f32 v113, v162, v163\n"
"v_mfma_f32_16x16x32_bf16 a[220:223], v[124:127], a[252:255], 0\n"
"ds_read_b64_tr_b16 v[224:225], v239 offset:1024\n"
"v_cvt_pk_bf16_f32 v58, v60, v61\n"
"v_smfmac_f32_16x16x64_bf16 v[196:199], a[240:243], v[128:135], v241\n"
"ds_read_b64_tr_b16 a[252:253], v235 offset:1024\n"
"v_accvgpr_write_b32 a101, v121\n"
"v_mfma_f32_16x16x32_bf16 v[202:205], a[240:243], v[124:127], v[202:205]\n"
"s_waitcnt lgkmcnt(2)\n"
"v_permlane16_swap_b32 v1, v3\n"
"v_accvgpr_write_b32 a156, v24\n"
"v_permlane16_swap_b32 v33, v35\n"
"v_cvt_pk_bf16_f32 v50, v52, v53\n"
"v_permlane16_swap_b32 v56, v58\n"
"v_cvt_pk_bf16_f32 v65, v66, v67\n"
"v_permlane16_swap_b32 v89, v91\n"
"v_mfma_f32_16x16x32_bf16 v[168:171], v[216:219], a[8:11], v[180:183]\n"
"v_permlane16_swap_b32 v57, v59\n"
"v_mfma_f32_16x16x32_bf16 v[168:171], v[220:223], a[12:15], v[168:171]\n"
"ds_read_b64_tr_b16 a[246:247], v234 offset:5120\n"
"v_cvt_pk_bf16_f32 v80, v80, v81\n"
"v_mfma_f32_16x16x32_bf16 v[168:171], v[208:211], a[0:3], v[168:171]\n"
"v_exp_f32 v154, v154\n"
"v_mfma_f32_16x16x32_bf16 v[168:171], v[212:215], a[4:7], v[168:171]\n"
"ds_read_b64_tr_b16 v[226:227], v239 offset:5120\n"
"v_cvt_pk_bf16_f32 v97, v154, v155\n"
"v_mfma_f32_16x16x32_bf16 v[172:175], v[208:211], a[16:19], v[184:187]\n"
"ds_read_b64_tr_b16 a[250:251], v238 offset:5120\n"
"v_cvt_pk_bf16_f32 v104, v156, v157\n"
"v_mfma_f32_16x16x32_bf16 v[172:175], v[212:215], a[20:23], v[172:175]\n"
"ds_read_b64_tr_b16 a[248:249], v238 offset:1024\n"
"v_accvgpr_write_b32 a190, v90\n"
"v_mfma_f32_16x16x32_bf16 v[172:175], v[216:219], a[24:27], v[172:175]\n"
"s_add_u32 s40, s56, s54\n"
"ds_read_b64_tr_b16 a[244:245], v234 offset:1024\n"
"v_mul_f32 v22, s45, v22\n"
"v_mfma_f32_16x16x32_bf16 v[172:175], v[220:223], a[28:31], v[172:175]\n"
"buffer_load_dwordx4 v240, s[20:23], s40 offen offset:3072 nt lds\n"
"v_cvt_pk_bf16_f32 v17, v18, v19\n"
"v_accvgpr_write_b32 a158, v26\n"
"v_accvgpr_write_b32 a175, v59\n"
"v_cvt_pk_bf16_f32 v66, v68, v69\n"
"v_exp_f32 v161, v161\n"
"v_exp_f32 v169, v169\n"
"v_mfma_f32_16x16x32_bf16 v[176:179], v[216:219], a[40:43], v[188:191]\n"
"s_add_u32 s55, s55, s59\n"
"v_mul_f32 v71, s45, v71\n"
"v_accvgpr_write_b32 a191, v91\n"
"v_mfma_f32_16x16x32_bf16 v[176:179], v[220:223], a[44:47], v[176:179]\n"
"s_add_u32 s41, s55, s57\n"
"v_cvt_pk_bf16_f32 v34, v36, v37\n"
"v_accvgpr_write_b32 a172, v56\n"
"v_mfma_f32_16x16x32_bf16 v[176:179], v[208:211], a[32:35], v[176:179]\n"
"s_and_b32 s41, s41, s58\n"
"ds_read_b64_tr_b16 a[254:255], v235 offset:5120\n"
"v_accvgpr_write_b32 a188, v88\n"
"v_mfma_f32_16x16x32_bf16 v[176:179], v[212:215], a[36:39], v[176:179]\n"
"s_andn2_b32 s56, s55, s58\n"
"v_mul_f32 v21, s45, v21\n"
"v_accvgpr_write_b32 a174, v58\n"
"v_mfma_f32_16x16x32_bf16 v[152:155], v[208:211], a[192:195], v[192:195]\n"
"s_or_b32 s56, s56, s41\n"
"v_exp_f32 v159, v159\n"
"v_mfma_f32_16x16x32_bf16 v[152:155], v[212:215], a[196:199], v[152:155]\n"
"ds_read_b128 v[212:215], v231 offset:16384\n"
"v_mul_f32 v70, s45, v70\n"
"v_mfma_f32_16x16x32_bf16 v[152:155], v[216:219], a[200:203], v[152:155]\n"
"v_accvgpr_write_b32 a146, v2\n"
"v_cvt_pk_bf16_f32 v67, v70, v71\n"
"v_mfma_f32_16x16x32_bf16 v[152:155], v[220:223], a[204:207], v[152:155]\n"
"s_waitcnt lgkmcnt(2)\n"
"v_exp_f32 v171, v171\n"
"v_mfma_f32_16x16x32_bf16 a[232:235], v[124:127], a[244:247], 0\n"
"v_accvgpr_write_b32 a96, v112\n"
"v_permlane16_swap_b32 v120, v122\n"
"v_accvgpr_write_b32 a157, v25\n"
"v_permlane16_swap_b32 v32, v34\n"
"v_accvgpr_write_b32 a161, v33\n"
"v_accvgpr_write_b32 a189, v89\n"
"v_cvt_pk_bf16_f32 v105, v158, v159\n"
"v_mfma_f32_16x16x32_bf16 v[24:27], v[128:131], a[244:247], 0\n"
"s_add_u32 m0, s44, 49152\n"
"ds_read_b128 v[216:219], v230 offset:17408\n"
"v_accvgpr_write_b32 a173, v57\n"
"v_mfma_f32_16x16x32_bf16 v[24:27], v[132:135], a[248:251], v[24:27]\n"
"buffer_load_dwordx4 v240, s[16:19], s56 offen nt lds\n"
"v_accvgpr_write_b32 a144, v0\n"
"v_permlane16_swap_b32 v65, v67\n"
"v_exp_f32 v165, v165\n"
"v_exp_f32 v166, v166\n"
"v_mfma_f32_16x16x32_bf16 v[56:59], v[136:139], a[244:247], 0\n"
"v_mul_f32 v20, s45, v20\n"
"v_mul_f32 v82, s45, v82\n"
"v_mfma_f32_16x16x32_bf16 v[56:59], v[140:143], a[248:251], v[56:59]\n"
"v_exp_f32 v160, v160\n"
"v_mfma_f32_16x16x32_bf16 v[88:91], v[144:147], a[244:247], 0\n"
"v_accvgpr_write_b32 a179, v67\n"
"v_bfe_u32 v246, v242, 8, 2\n"
"v_mfma_f32_16x16x32_bf16 v[88:91], v[148:151], a[248:251], v[88:91]\n"
"ds_read_b64_tr_b16 a[248:249], v236 offset:0\n"
"v_cvt_pk_bf16_f32 v19, v22, v23\n"
"v_mfma_f32_16x16x32_bf16 v[92:95], v[148:151], v[224:227], 0\n"
"s_waitcnt lgkmcnt(2)\n"
"ds_read_b128 v[208:211], v230 offset:16384\n"
"v_accvgpr_write_b32 a145, v1\n"
"v_mfma_f32_16x16x32_bf16 v[92:95], v[144:147], a[252:255], v[92:95]\n"
"v_cvt_pk_bf16_f32 v81, v82, v83\n"
"v_cvt_pk_bf16_f32 v112, v160, v161\n"
"v_mfma_f32_16x16x32_bf16 v[60:63], v[140:143], v[224:227], 0\n"
"v_permlane16_swap_b32 v48, v50\n"
"v_mfma_f32_16x16x32_bf16 v[60:63], v[136:139], a[252:255], v[60:63]\n"
"ds_read_b128 v[220:223], v231 offset:17408\n"
"v_cvt_pk_bf16_f32 v18, v20, v21\n"
"v_permlane16_swap_b32 v17, v19\n"
"v_exp_f32 v168, v168\n"
"v_exp_f32 v176, v176\n"
"v_exp_f32 v177, v177\n"
"v_mfma_f32_16x16x32_bf16 v[28:31], v[132:135], v[224:227], 0\n"
"v_exp_f32 v164, v164\n"
"v_mfma_f32_16x16x32_bf16 v[28:31], v[128:131], a[252:255], v[28:31]\n"
"ds_read_b64_tr_b16 a[244:245], v232 offset:0\n"
"v_accvgpr_write_b32 a147, v3\n"
"v_permlane16_swap_b32 v64, v66\n"
"v_bfe_u32 v249, v242, 6, 4\n"
"v_exp_f32 v170, v170\n"
"v_exp_f32 v173, v173\n"
"v_mfma_f32_16x16x32_bf16 a[236:239], v[124:127], a[252:255], 0\n"
"s_add_u32 s40, s56, s52\n"
"v_accvgpr_write_b32 a162, v34\n"
"v_accvgpr_write_b32 a163, v35\n"
"v_smfmac_f32_16x16x64_bf16 v[198:201], a[240:243], v[136:143], v241\n"
"s_waitcnt lgkmcnt(2)\n"
"buffer_load_dwordx4 v240, s[16:19], s40 offen offset:1024 nt lds\n"
"v_mfma_f32_16x16x32_bf16 v[156:159], v[208:211], a[0:3], v[180:183]\n"
"v_accvgpr_write_b32 a99, v115\n"
"v_accvgpr_write_b32 a177, v65\n"
"v_mfma_f32_16x16x32_bf16 v[156:159], v[212:215], a[4:7], v[156:159]\n"
"v_accvgpr_write_b32 a100, v120\n"
"v_cvt_pk_bf16_f32 v121, v166, v167\n"
"v_mfma_f32_16x16x32_bf16 v[156:159], v[216:219], a[8:11], v[156:159]\n"
"s_waitcnt lgkmcnt(1)\n"
"ds_read_b64_tr_b16 a[246:247], v232 offset:4096\n"
"v_accvgpr_write_b32 a98, v114\n"
"v_mfma_f32_16x16x32_bf16 v[156:159], v[220:223], a[12:15], v[156:159]\n"
"ds_read_b64_tr_b16 v[226:227], v237 offset:4096\n"
"v_cvt_pk_bf16_f32 v120, v164, v165\n"
"v_mfma_f32_16x16x32_bf16 v[160:163], v[216:219], a[24:27], v[184:187]\n"
"v_exp_f32 v172, v172\n"
"v_mfma_f32_16x16x32_bf16 v[160:163], v[220:223], a[28:31], v[160:163]\n"
"v_cvt_pk_bf16_f32 v51, v54, v55\n"
"v_lshlrev_b32 v246, 6, v246\n"
"v_mfma_f32_16x16x32_bf16 v[160:163], v[208:211], a[16:19], v[160:163]\n"
"ds_read_b64_tr_b16 a[250:251], v236 offset:4096\n"
"ds_read_b64_tr_b16 a[254:255], v233 offset:4096\n"
"v_mfma_f32_16x16x32_bf16 v[160:163], v[212:215], a[20:23], v[160:163]\n"
"ds_read_b64_tr_b16 v[224:225], v237 offset:0\n"
"v_cvt_pk_bf16_f32 v98, v168, v169\n"
"v_mfma_f32_16x16x32_bf16 v[164:167], v[208:211], a[32:35], v[188:191]\n"
"ds_read_b64_tr_b16 a[252:253], v233 offset:0\n"
"v_cvt_pk_bf16_f32 v83, v86, v87\n"
"v_mfma_f32_16x16x32_bf16 v[164:167], v[212:215], a[36:39], v[164:167]\n"
"v_exp_f32 v175, v175\n"
"v_mfma_f32_16x16x32_bf16 v[164:167], v[216:219], a[40:43], v[164:167]\n"
"ds_read_b128 v[208:211], v230 offset:20480\n"
"v_cvt_pk_bf16_f32 v82, v84, v85\n"
"v_mfma_f32_16x16x32_bf16 v[164:167], v[220:223], a[44:47], v[164:167]\n"
"s_waitcnt lgkmcnt(5)\n"
"v_exp_f32 v178, v178\n"
"v_mfma_f32_16x16x32_bf16 a[208:211], v[124:127], a[244:247], 0\n"
"ds_read_b128 v[220:223], v231 offset:21504\n"
"v_accvgpr_write_b32 a176, v64\n"
"v_permlane16_swap_b32 v81, v83\n"
"v_xor_b32 v246, v246, v242\n"
"v_cvt_pk_bf16_f32 v99, v170, v171\n"
"v_exp_f32 v174, v174\n"
"v_exp_f32 v158, v158\n"
"v_mfma_f32_16x16x32_bf16 v[0:3], v[128:131], a[244:247], 0\n"
"s_waitcnt lgkmcnt(5)\n"
"s_add_u32 s40, s56, s53\n"
"v_accvgpr_write_b32 a153, v17\n"
"v_accvgpr_write_b32 a160, v32\n"
"v_mfma_f32_16x16x32_bf16 v[0:3], v[132:135], a[248:251], v[0:3]\n"
"buffer_load_dwordx4 v240, s[16:19], s40 offen offset:2048 nt lds\n"
"v_mfma_f32_16x16x32_bf16 v[32:35], v[136:139], a[244:247], 0\n"
"v_permlane16_swap_b32 v16, v18\n"
"v_mfma_f32_16x16x32_bf16 v[32:35], v[140:143], a[248:251], v[32:35]\n"
"v_accvgpr_write_b32 a152, v16\n"
"v_accvgpr_write_b32 a178, v66\n"
"v_mfma_f32_16x16x32_bf16 v[64:67], v[144:147], a[244:247], 0\n"
"v_permlane16_swap_b32 v49, v51\n"
"v_mfma_f32_16x16x32_bf16 v[64:67], v[148:151], a[248:251], v[64:67]\n"
"s_waitcnt lgkmcnt(1)\n"
"ds_read_b128 v[212:215], v231 offset:20480\n"
"v_accvgpr_write_b32 a187, v83\n"
"v_mfma_f32_16x16x32_bf16 v[68:71], v[148:151], v[224:227], 0\n"
"ds_read_b128 v[216:219], v230 offset:21504\n"
"v_accvgpr_write_b32 a154, v18\n"
"v_mfma_f32_16x16x32_bf16 v[68:71], v[144:147], a[252:255], v[68:71]\n"
"v_exp_f32 v161, v161\n"
"v_mfma_f32_16x16x32_bf16 v[36:39], v[140:143], v[224:227], 0\n"
"ds_read_b64_tr_b16 a[246:247], v232 offset:5120\n"
"v_cvt_pk_bf16_f32 v107, v174, v175\n"
"v_mfma_f32_16x16x32_bf16 v[36:39], v[136:139], a[252:255], v[36:39]\n"
"ds_read_b64_tr_b16 a[244:245], v232 offset:1024\n"
"v_cvt_pk_bf16_f32 v106, v172, v173\n"
"v_exp_f32 v154, v154\n"
"v_exp_f32 v156, v156\n"
"v_exp_f32 v160, v160\n"
"v_mfma_f32_16x16x32_bf16 v[4:7], v[132:135], v[224:227], 0\n"
"s_add_u32 s40, s56, s54\n"
"v_permlane16_swap_b32 v80, v82\n"
"v_mfma_f32_16x16x32_bf16 v[4:7], v[128:131], a[252:255], v[4:7]\n"
"buffer_load_dwordx4 v240, s[16:19], s40 offen offset:3072 nt lds\n"
"v_mfma_f32_16x16x32_bf16 a[212:215], v[124:127], a[252:255], 0\n"
"v_exp_f32 v179, v179\n"
"v_smfmac_f32_16x16x64_bf16 v[200:203], a[240:243], v[144:151], v241\n"
"v_accvgpr_write_b32 a170, v50\n"
"v_cvt_pk_bf16_f32 v115, v178, v179\n"
"v_mfma_f32_16x16x32_bf16 v[168:171], v[208:211], a[0:3], v[180:183]\n"
"s_waitcnt lgkmcnt(2)\n"
"ds_read_b64_tr_b16 a[250:251], v236 offset:5120\n"
"v_cvt_pk_bf16_f32 v114, v176, v177\n"
"v_mfma_f32_16x16x32_bf16 v[168:171], v[212:215], a[4:7], v[168:171]\n"
"v_exp_f32 v164, v164\n"
"v_mfma_f32_16x16x32_bf16 v[168:171], v[216:219], a[8:11], v[168:171]\n"
"ds_read_b64_tr_b16 a[248:249], v236 offset:1024\n"
"ds_read_b64_tr_b16 v[224:225], v237 offset:1024\n"
"v_mfma_f32_16x16x32_bf16 v[168:171], v[220:223], a[12:15], v[168:171]\n"
"v_exp_f32 v162, v162\n"
"v_mfma_f32_16x16x32_bf16 v[172:175], v[216:219], a[24:27], v[184:187]\n"
"v_exp_f32 v165, v165\n"
"v_mfma_f32_16x16x32_bf16 v[172:175], v[220:223], a[28:31], v[172:175]\n"
"v_exp_f32 v153, v153\n"
"v_mfma_f32_16x16x32_bf16 v[172:175], v[208:211], a[16:19], v[172:175]\n"
"v_accvgpr_write_b32 a168, v48\n"
"v_accvgpr_write_b32 a186, v82\n"
"v_mfma_f32_16x16x32_bf16 v[172:175], v[212:215], a[20:23], v[172:175]\n"
"s_cbranch_vccnz LPCE1_%=\nLPKE1_%=:\n"
"ds_read_b64_tr_b16 v[226:227], v237 offset:5120\n"
"v_mfma_f32_16x16x32_bf16 v[176:179], v[208:211], a[32:35], v[188:191]\n"
"v_accvgpr_write_b32 a169, v49\n"
"v_accvgpr_write_b32 a171, v51\n"
"v_mfma_f32_16x16x32_bf16 v[176:179], v[212:215], a[36:39], v[176:179]\n"
"ds_read_b64_tr_b16 a[252:253], v233 offset:1024\n"
"v_accvgpr_write_b32 a155, v19\n"
"v_mfma_f32_16x16x32_bf16 v[176:179], v[216:219], a[40:43], v[176:179]\n"
"v_exp_f32 v166, v166\n"
"v_mfma_f32_16x16x32_bf16 v[176:179], v[220:223], a[44:47], v[176:179]\n"
"s_waitcnt lgkmcnt(2)\n"
"v_accvgpr_write_b32 a185, v81\n"
"v_cvt_pk_bf16_f32 v116, v164, v165\n"
"v_mfma_f32_16x16x32_bf16 a[224:227], v[124:127], a[244:247], 0\n"
"v_exp_f32 v172, v172\n"
"v_mfma_f32_16x16x32_bf16 v[16:19], v[128:131], a[244:247], 0\n"
"s_waitcnt vmcnt(8)\n"
"ds_read_b64_tr_b16 a[254:255], v233 offset:5120\n"
"v_accvgpr_write_b32 a184, v80\n"
"v_mfma_f32_16x16x32_bf16 v[16:19], v[132:135], a[248:251], v[16:19]\n"
"s_barrier\n"
"ds_read_b128 v[208:211], v228 offset:32768\n"
"v_accvgpr_write_b32 a102, v122\n"
"v_xor_b32 v247, v247, v249\n"
"v_exp_f32 v155, v155\n"
"v_exp_f32 v157, v157\n"
"v_exp_f32 v169, v169\n"
"v_mfma_f32_16x16x32_bf16 v[48:51], v[136:139], a[244:247], 0\n"
"ds_read_b128 v[212:215], v229 offset:32768\n"
"v_cvt_pk_bf16_f32 v108, v160, v161\n"
"v_mfma_f32_16x16x32_bf16 v[48:51], v[140:143], a[248:251], v[48:51]\n"
"ds_read_b128 v[216:219], v228 offset:33792\n"
"ds_read_b128 v[220:223], v229 offset:33792\n"
"v_mfma_f32_16x16x32_bf16 v[80:83], v[144:147], a[244:247], 0\n"
"ds_read_b64_tr_b16 a[244:245], v234 offset:16384\n"
"v_lshl_add_u32 v247, v247, 4, s44\n"
"v_mfma_f32_16x16x32_bf16 v[80:83], v[148:151], a[248:251], v[80:83]\n"
"s_waitcnt lgkmcnt(6)\n"
"ds_read_b64_tr_b16 a[246:247], v234 offset:20480\n"
"ds_read_b64_tr_b16 a[248:249], v238 offset:16384\n"
"ds_read_b64_tr_b16 a[250:251], v238 offset:20480\n"
"v_accvgpr_write_b32 a103, v123\n"
"v_cvt_pk_bf16_f32 v100, v156, v157\n"
"v_exp_f32 v159, v159\n"
"v_exp_f32 v167, v167\n"
"v_exp_f32 v175, v175\n"
"v_mfma_f32_16x16x32_bf16 v[84:87], v[148:151], v[224:227], 0\n"
"s_waitcnt lgkmcnt(8)\n"
"v_exp_f32 v163, v163\n"
"v_mfma_f32_16x16x32_bf16 v[84:87], v[144:147], a[252:255], v[84:87]\n"
"v_exp_f32 v173, v173\n"
"v_mfma_f32_16x16x32_bf16 v[52:55], v[140:143], v[224:227], 0\n"
"v_cvt_pk_bf16_f32 v123, v154, v155\n"
"v_cvt_pk_bf16_f32 v117, v166, v167\n"
"v_mfma_f32_16x16x32_bf16 v[52:55], v[136:139], a[252:255], v[52:55]\n"
"v_exp_f32 v152, v152\n"
"v_mfma_f32_16x16x32_bf16 v[20:23], v[132:135], v[224:227], 0\n"
"ds_read_b64_tr_b16 v[224:225], v239 offset:16384\n"
"v_cvt_pk_bf16_f32 v101, v158, v159\n"
"v_mfma_f32_16x16x32_bf16 v[20:23], v[128:131], a[252:255], v[20:23]\n"
"v_cvt_pk_bf16_f32 v122, v152, v153\n"
"v_cvt_pk_bf16_f32 v109, v162, v163\n"
"v_mfma_f32_16x16x32_bf16 a[228:231], v[124:127], a[252:255], 0\n"
"s_waitcnt lgkmcnt(4)\n"
"ds_read_b64_tr_b16 a[254:255], v235 offset:20480\n"
"s_branch LAY2_%=\n"
"LAL_%=:\n"
"v_mfma_f32_16x16x32_bf16 a[224:227], v[124:127], a[244:247], a[224:227]\n"
"v_exp_f32 v169, v169\n"
"v_mfma_f32_16x16x32_bf16 v[16:19], v[128:131], a[244:247], v[16:19]\n"
"s_waitcnt vmcnt(8)\n"
"v_exp_f32 v172, v172\n"
"v_mfma_f32_16x16x32_bf16 v[16:19], v[132:135], a[248:251], v[16:19]\n"
"s_barrier\n"
"ds_read_b128 v[216:219], v228 offset:1024\n"
"v_mfma_f32_16x16x32_bf16 v[48:51], v[136:139], a[244:247], v[48:51]\n"
"ds_read_b128 v[212:215], v229 offset:0\n"
"v_cvt_pk_bf16_f32 v108, v160, v161\n"
"v_mfma_f32_16x16x32_bf16 v[48:51], v[140:143], a[248:251], v[48:51]\n"
"ds_read_b128 v[208:211], v228 offset:0\n"
"v_cvt_pk_bf16_f32 v100, v156, v157\n"
"v_mfma_f32_16x16x32_bf16 v[80:83], v[144:147], a[244:247], v[80:83]\n"
"ds_read_b128 v[220:223], v229 offset:1024\n"
"v_cvt_pk_bf16_f32 v117, v166, v167\n"
"v_mfma_f32_16x16x32_bf16 v[80:83], v[148:151], a[248:251], v[80:83]\n"
"ds_read_b64_tr_b16 a[244:245], v234 offset:49152\n"
"v_cvt_pk_bf16_f32 v109, v162, v163\n"
"v_mfma_f32_16x16x32_bf16 v[84:87], v[148:151], v[224:227], v[84:87]\n"
"ds_read_b64_tr_b16 a[248:249], v238 offset:49152\n"
"v_mfma_f32_16x16x32_bf16 v[84:87], v[144:147], a[252:255], v[84:87]\n"
"ds_read_b64_tr_b16 a[246:247], v234 offset:53248\n"
"v_mfma_f32_16x16x32_bf16 v[52:55], v[140:143], v[224:227], v[52:55]\n"
"ds_read_b64_tr_b16 a[250:251], v238 offset:53248\n"
"v_mfma_f32_16x16x32_bf16 v[52:55], v[136:139], a[252:255], v[52:55]\n"
"v_exp_f32 v175, v175\n"
"v_mfma_f32_16x16x32_bf16 v[20:23], v[132:135], v[224:227], v[20:23]\n"
"ds_read_b64_tr_b16 v[224:225], v239 offset:49152\n"
"v_mfma_f32_16x16x32_bf16 v[20:23], v[128:131], a[252:255], v[20:23]\n"
"v_exp_f32 v173, v173\n"
"v_mfma_f32_16x16x32_bf16 a[228:231], v[124:127], a[252:255], a[228:231]\n"
"s_waitcnt lgkmcnt(5)\n"
"ds_read_b64_tr_b16 a[254:255], v235 offset:53248\n"
"v_cvt_pk_bf16_f32 v116, v164, v165\n"
"v_mfma_f32_16x16x32_bf16 v[152:155], v[208:211], a[0:3], v[180:183]\n"
"v_exp_f32 v174, v174\n"
"v_mfma_f32_16x16x32_bf16 v[152:155], v[212:215], a[4:7], v[152:155]\n"
"ds_read_b64_tr_b16 a[252:253], v235 offset:49152\n"
"v_cvt_pk_bf16_f32 v111, v174, v175\n"
"v_mfma_f32_16x16x32_bf16 v[152:155], v[216:219], a[8:11], v[152:155]\n"
"s_add_u32 m0, s44, 81920\n"
"ds_read_b64_tr_b16 v[226:227], v239 offset:53248\n"
"v_mfma_f32_16x16x32_bf16 v[152:155], v[220:223], a[12:15], v[152:155]\n"
"buffer_load_dwordx4 v240, s[20:23], s56 offen nt lds\n"
"v_mfma_f32_16x16x32_bf16 v[156:159], v[216:219], a[24:27], v[184:187]\n"
"v_exp_f32 v171, v171\n"
"v_mfma_f32_16x16x32_bf16 v[156:159], v[220:223], a[28:31], v[156:159]\n"
"v_exp_f32 v168, v168\n"
"v_mfma_f32_16x16x32_bf16 v[156:159], v[208:211], a[16:19], v[156:159]\n"
"v_exp_f32 v176, v176\n"
"v_mfma_f32_16x16x32_bf16 v[156:159], v[212:215], a[20:23], v[156:159]\n"
"v_exp_f32 v177, v177\n"
"v_mfma_f32_16x16x32_bf16 v[160:163], v[208:211], a[32:35], v[188:191]\n"
"v_exp_f32 v179, v179\n"
"v_mfma_f32_16x16x32_bf16 v[160:163], v[212:215], a[36:39], v[160:163]\n"
"v_exp_f32 v170, v170\n"
"v_mfma_f32_16x16x32_bf16 v[160:163], v[216:219], a[40:43], v[160:163]\n"
"v_exp_f32 v153, v153\n"
"v_mfma_f32_16x16x32_bf16 v[160:163], v[220:223], a[44:47], v[160:163]\n"
"v_cvt_pk_bf16_f32 v102, v168, v169\n"
"v_mfma_f32_16x16x32_bf16 v[164:167], v[216:219], a[200:203], v[192:195]\n"
"ds_read_b128 v[216:219], v228 offset:5120\n"
"v_cvt_pk_bf16_f32 v103, v170, v171\n"
"v_mfma_f32_16x16x32_bf16 v[164:167], v[220:223], a[204:207], v[164:167]\n"
"v_cvt_pk_bf16_f32 v110, v172, v173\n"
"v_cvt_pk_bf16_f32 v118, v176, v177\n"
"v_mfma_f32_16x16x32_bf16 v[164:167], v[208:211], a[192:195], v[164:167]\n"
"s_add_u32 s40, s56, s52\n"
"ds_read_b128 v[208:211], v228 offset:4096\n"
"v_mfma_f32_16x16x32_bf16 v[164:167], v[212:215], a[196:199], v[164:167]\n"
"s_waitcnt lgkmcnt(2)\n"
"buffer_load_dwordx4 v240, s[20:23], s40 offen offset:1024 nt lds\n"
"v_mfma_f32_16x16x32_bf16 a[216:219], v[120:123], a[244:247], a[216:219]\n"
"v_exp_f32 v155, v155\n"
"v_mfma_f32_16x16x32_bf16 v[8:11], v[96:99], a[244:247], v[8:11]\n"
"v_exp_f32 v161, v161\n"
"v_mfma_f32_16x16x32_bf16 v[8:11], v[100:103], a[248:251], v[8:11]\n"
"v_exp_f32 v163, v163\n"
"v_mfma_f32_16x16x32_bf16 v[40:43], v[104:107], a[244:247], v[40:43]\n"
"v_exp_f32 v178, v178\n"
"v_mfma_f32_16x16x32_bf16 v[40:43], v[108:111], a[248:251], v[40:43]\n"
"ds_read_b128 v[220:223], v229 offset:5120\n"
"v_cvt_pk_bf16_f32 v119, v178, v179\n"
"v_mfma_f32_16x16x32_bf16 v[72:75], v[112:115], a[244:247], v[72:75]\n"
"ds_read_b64_tr_b16 a[244:245], v234 offset:50176\n"
"v_mfma_f32_16x16x32_bf16 v[72:75], v[116:119], a[248:251], v[72:75]\n"
"v_exp_f32 v158, v158\n"
"v_mfma_f32_16x16x32_bf16 v[76:79], v[116:119], v[224:227], v[76:79]\n"
"ds_read_b128 v[212:215], v229 offset:4096\n"
"v_mfma_f32_16x16x32_bf16 v[76:79], v[112:115], a[252:255], v[76:79]\n"
"v_exp_f32 v160, v160\n"
"v_mfma_f32_16x16x32_bf16 v[44:47], v[108:111], v[224:227], v[44:47]\n"
"s_add_u32 s40, s56, s53\n"
"ds_read_b64_tr_b16 a[248:249], v238 offset:50176\n"
"v_mfma_f32_16x16x32_bf16 v[44:47], v[104:107], a[252:255], v[44:47]\n"
"buffer_load_dwordx4 v240, s[20:23], s40 offen offset:2048 nt lds\n"
"v_mfma_f32_16x16x32_bf16 v[12:15], v[100:103], v[224:227], v[12:15]\n"
"v_exp_f32 v165, v165\n"
"v_mfma_f32_16x16x32_bf16 v[12:15], v[96:99], a[252:255], v[12:15]\n"
"v_exp_f32 v159, v159\n"
"v_mfma_f32_16x16x32_bf16 a[220:223], v[120:123], a[252:255], a[220:223]\n"
"v_exp_f32 v152, v152\n"
"v_smfmac_f32_16x16x64_bf16 v[196:199], a[240:243], v[96:103], v241\n"
"v_mfma_f32_16x16x32_bf16 v[202:205], a[240:243], v[120:123], v[202:205]\n"
"s_waitcnt lgkmcnt(1)\n"
"ds_read_b64_tr_b16 v[224:225], v239 offset:50176\n"
"v_cvt_pk_bf16_f32 v128, v152, v153\n"
"v_mfma_f32_16x16x32_bf16 v[168:171], v[216:219], a[8:11], v[180:183]\n"
"v_exp_f32 v162, v162\n"
"v_mfma_f32_16x16x32_bf16 v[168:171], v[220:223], a[12:15], v[168:171]\n"
"ds_read_b64_tr_b16 a[250:251], v238 offset:54272\n"
"v_mfma_f32_16x16x32_bf16 v[168:171], v[208:211], a[0:3], v[168:171]\n"
"ds_read_b64_tr_b16 a[252:253], v235 offset:50176\n"
"v_mfma_f32_16x16x32_bf16 v[168:171], v[212:215], a[4:7], v[168:171]\n"
"v_exp_f32 v154, v154\n"
"v_mfma_f32_16x16x32_bf16 v[172:175], v[208:211], a[16:19], v[184:187]\n"
"ds_read_b64_tr_b16 v[226:227], v239 offset:54272\n"
"v_mfma_f32_16x16x32_bf16 v[172:175], v[212:215], a[20:23], v[172:175]\n"
"ds_read_b64_tr_b16 a[246:247], v234 offset:54272\n"
"v_cvt_pk_bf16_f32 v129, v154, v155\n"
"v_mfma_f32_16x16x32_bf16 v[172:175], v[216:219], a[24:27], v[172:175]\n"
"s_add_u32 s40, s56, s54\n"
"ds_read_b64_tr_b16 a[254:255], v235 offset:54272\n"
"v_mfma_f32_16x16x32_bf16 v[172:175], v[220:223], a[28:31], v[172:175]\n"
"buffer_load_dwordx4 v240, s[20:23], s40 offen offset:3072 nt lds\n"
"v_mfma_f32_16x16x32_bf16 v[176:179], v[216:219], a[40:43], v[188:191]\n"
"s_add_u32 s55, s55, s59\n"
"v_exp_f32 v157, v157\n"
"v_mfma_f32_16x16x32_bf16 v[176:179], v[220:223], a[44:47], v[176:179]\n"
"s_add_u32 s41, s55, s57\n"
"v_exp_f32 v170, v170\n"
"v_mfma_f32_16x16x32_bf16 v[176:179], v[208:211], a[32:35], v[176:179]\n"
"s_and_b32 s41, s41, s58\n"
"v_exp_f32 v156, v156\n"
"v_mfma_f32_16x16x32_bf16 v[176:179], v[212:215], a[36:39], v[176:179]\n"
"s_andn2_b32 s56, s55, s58\n"
"v_exp_f32 v166, v166\n"
"v_mfma_f32_16x16x32_bf16 v[152:155], v[208:211], a[192:195], v[192:195]\n"
"s_or_b32 s56, s56, s41\n"
"ds_read_b128 v[208:211], v230 offset:0\n"
"v_mfma_f32_16x16x32_bf16 v[152:155], v[212:215], a[196:199], v[152:155]\n"
"v_exp_f32 v174, v174\n"
"v_mfma_f32_16x16x32_bf16 v[152:155], v[216:219], a[200:203], v[152:155]\n"
"ds_read_b128 v[212:215], v231 offset:0\n"
"v_mfma_f32_16x16x32_bf16 v[152:155], v[220:223], a[204:207], v[152:155]\n"
"s_waitcnt lgkmcnt(2)\n"
"ds_read_b128 v[216:219], v230 offset:1024\n"
"v_cvt_pk_bf16_f32 v137, v158, v159\n"
"v_mfma_f32_16x16x32_bf16 a[232:235], v[120:123], a[244:247], a[232:235]\n"
"v_exp_f32 v164, v164\n"
"v_mfma_f32_16x16x32_bf16 v[24:27], v[96:99], a[244:247], v[24:27]\n"
"s_add_u32 m0, s44, 32768\n"
"ds_read_b128 v[220:223], v231 offset:1024\n"
"v_mfma_f32_16x16x32_bf16 v[24:27], v[100:103], a[248:251], v[24:27]\n"
"buffer_load_dwordx4 v240, s[16:19], s56 offen nt lds\n"
"v_mfma_f32_16x16x32_bf16 v[56:59], v[104:107], a[244:247], v[56:59]\n"
"v_exp_f32 v167, v167\n"
"v_mfma_f32_16x16x32_bf16 v[56:59], v[108:111], a[248:251], v[56:59]\n"
"v_exp_f32 v176, v176\n"
"v_mfma_f32_16x16x32_bf16 v[88:91], v[112:115], a[244:247], v[88:91]\n"
"v_exp_f32 v153, v153\n"
"v_mfma_f32_16x16x32_bf16 v[88:91], v[116:119], a[248:251], v[88:91]\n"
"v_exp_f32 v173, v173\n"
"v_mfma_f32_16x16x32_bf16 v[92:95], v[116:119], v[224:227], v[92:95]\n"
"ds_read_b64_tr_b16 a[250:251], v236 offset:53248\n"
"v_cvt_pk_bf16_f32 v125, v166, v167\n"
"v_mfma_f32_16x16x32_bf16 v[92:95], v[112:115], a[252:255], v[92:95]\n"
"ds_read_b64_tr_b16 a[244:245], v232 offset:49152\n"
"v_cvt_pk_bf16_f32 v144, v160, v161\n"
"v_mfma_f32_16x16x32_bf16 v[60:63], v[108:111], v[224:227], v[60:63]\n"
"ds_read_b64_tr_b16 a[248:249], v236 offset:49152\n"
"v_cvt_pk_bf16_f32 v124, v164, v165\n"
"v_mfma_f32_16x16x32_bf16 v[60:63], v[104:107], a[252:255], v[60:63]\n"
"ds_read_b64_tr_b16 a[246:247], v232 offset:53248\n"
"v_cvt_pk_bf16_f32 v136, v156, v157\n"
"v_mfma_f32_16x16x32_bf16 v[28:31], v[100:103], v[224:227], v[28:31]\n"
"ds_read_b64_tr_b16 v[226:227], v237 offset:53248\n"
"v_cvt_pk_bf16_f32 v145, v162, v163\n"
"v_mfma_f32_16x16x32_bf16 v[28:31], v[96:99], a[252:255], v[28:31]\n"
"ds_read_b64_tr_b16 v[224:225], v237 offset:49152\n"
"v_mfma_f32_16x16x32_bf16 a[236:239], v[120:123], a[252:255], a[236:239]\n"
"s_add_u32 s40, s56, s52\n"
"ds_read_b64_tr_b16 a[252:253], v233 offset:49152\n"
"v_smfmac_f32_16x16x64_bf16 v[198:201], a[240:243], v[104:111], v241\n"
"s_waitcnt lgkmcnt(7)\n"
"buffer_load_dwordx4 v240, s[16:19], s40 offen offset:1024 nt lds\n"
"v_mfma_f32_16x16x32_bf16 v[156:159], v[208:211], a[0:3], v[180:183]\n"
"v_exp_f32 v169, v169\n"
"v_mfma_f32_16x16x32_bf16 v[156:159], v[212:215], a[4:7], v[156:159]\n"
"v_exp_f32 v168, v168\n"
"v_mfma_f32_16x16x32_bf16 v[156:159], v[216:219], a[8:11], v[156:159]\n"
"v_exp_f32 v175, v175\n"
"v_mfma_f32_16x16x32_bf16 v[156:159], v[220:223], a[12:15], v[156:159]\n"
"v_exp_f32 v172, v172\n"
"v_mfma_f32_16x16x32_bf16 v[160:163], v[216:219], a[24:27], v[184:187]\n"
"ds_read_b64_tr_b16 a[254:255], v233 offset:53248\n"
"v_mfma_f32_16x16x32_bf16 v[160:163], v[220:223], a[28:31], v[160:163]\n"
"v_exp_f32 v155, v155\n"
"v_mfma_f32_16x16x32_bf16 v[160:163], v[208:211], a[16:19], v[160:163]\n"
"v_exp_f32 v177, v177\n"
"v_mfma_f32_16x16x32_bf16 v[160:163], v[212:215], a[20:23], v[160:163]\n"
"v_exp_f32 v179, v179\n"
"v_mfma_f32_16x16x32_bf16 v[164:167], v[208:211], a[32:35], v[188:191]\n"
"ds_read_b128 v[208:211], v230 offset:4096\n"
"v_cvt_pk_bf16_f32 v139, v174, v175\n"
"v_mfma_f32_16x16x32_bf16 v[164:167], v[212:215], a[36:39], v[164:167]\n"
"v_exp_f32 v171, v171\n"
"v_mfma_f32_16x16x32_bf16 v[164:167], v[216:219], a[40:43], v[164:167]\n"
"ds_read_b128 v[212:215], v231 offset:4096\n"
"v_cvt_pk_bf16_f32 v138, v172, v173\n"
"v_mfma_f32_16x16x32_bf16 v[164:167], v[220:223], a[44:47], v[164:167]\n"
"s_waitcnt lgkmcnt(3)\n"
"ds_read_b128 v[220:223], v231 offset:5120\n"
"v_cvt_pk_bf16_f32 v130, v168, v169\n"
"v_mfma_f32_16x16x32_bf16 a[208:211], v[120:123], a[244:247], a[208:211]\n"
"v_exp_f32 v154, v154\n"
"v_mfma_f32_16x16x32_bf16 v[0:3], v[96:99], a[244:247], v[0:3]\n"
"s_add_u32 s40, s56, s53\n"
"ds_read_b128 v[216:219], v230 offset:5120\n"
"v_mfma_f32_16x16x32_bf16 v[0:3], v[100:103], a[248:251], v[0:3]\n"
"buffer_load_dwordx4 v240, s[16:19], s40 offen offset:2048 nt lds\n"
"v_mfma_f32_16x16x32_bf16 v[32:35], v[104:107], a[244:247], v[32:35]\n"
"v_exp_f32 v160, v160\n"
"v_mfma_f32_16x16x32_bf16 v[32:35], v[108:111], a[248:251], v[32:35]\n"
"v_exp_f32 v178, v178\n"
"v_mfma_f32_16x16x32_bf16 v[64:67], v[112:115], a[244:247], v[64:67]\n"
"v_exp_f32 v152, v152\n"
"v_mfma_f32_16x16x32_bf16 v[64:67], v[116:119], a[248:251], v[64:67]\n"
"v_exp_f32 v161, v161\n"
"v_mfma_f32_16x16x32_bf16 v[68:71], v[116:119], v[224:227], v[68:71]\n"
"s_waitcnt lgkmcnt(1)\n"
"ds_read_b64_tr_b16 a[244:245], v232 offset:50176\n"
"v_cvt_pk_bf16_f32 v147, v178, v179\n"
"v_mfma_f32_16x16x32_bf16 v[68:71], v[112:115], a[252:255], v[68:71]\n"
"ds_read_b64_tr_b16 a[248:249], v236 offset:50176\n"
"v_cvt_pk_bf16_f32 v146, v176, v177\n"
"v_mfma_f32_16x16x32_bf16 v[36:39], v[108:111], v[224:227], v[36:39]\n"
"ds_read_b64_tr_b16 a[250:251], v236 offset:54272\n"
"v_cvt_pk_bf16_f32 v131, v170, v171\n"
"v_mfma_f32_16x16x32_bf16 v[36:39], v[104:107], a[252:255], v[36:39]\n"
"v_exp_f32 v163, v163\n"
"v_mfma_f32_16x16x32_bf16 v[4:7], v[100:103], v[224:227], v[4:7]\n"
"s_add_u32 s40, s56, s54\n"
"ds_read_b64_tr_b16 a[246:247], v232 offset:54272\n"
"v_mfma_f32_16x16x32_bf16 v[4:7], v[96:99], a[252:255], v[4:7]\n"
"buffer_load_dwordx4 v240, s[16:19], s40 offen offset:3072 nt lds\n"
"v_mfma_f32_16x16x32_bf16 a[212:215], v[120:123], a[252:255], a[212:215]\n"
"v_exp_f32 v167, v167\n"
"v_smfmac_f32_16x16x64_bf16 v[200:203], a[240:243], v[112:119], v241\n"
"v_cvt_pk_bf16_f32 v126, v152, v153\n"
"v_mfma_f32_16x16x32_bf16 v[168:171], v[208:211], a[0:3], v[180:183]\n"
"v_exp_f32 v159, v159\n"
"v_mfma_f32_16x16x32_bf16 v[168:171], v[212:215], a[4:7], v[168:171]\n"
"s_waitcnt lgkmcnt(2)\n"
"v_exp_f32 v165, v165\n"
"v_mfma_f32_16x16x32_bf16 v[168:171], v[216:219], a[8:11], v[168:171]\n"
"ds_read_b64_tr_b16 a[252:253], v233 offset:50176\n"
"ds_read_b64_tr_b16 v[224:225], v237 offset:50176\n"
"v_mfma_f32_16x16x32_bf16 v[168:171], v[220:223], a[12:15], v[168:171]\n"
"ds_read_b64_tr_b16 a[254:255], v233 offset:54272\n"
"ds_read_b64_tr_b16 v[226:227], v237 offset:54272\n"
"v_mfma_f32_16x16x32_bf16 v[172:175], v[216:219], a[24:27], v[184:187]\n"
"v_exp_f32 v166, v166\n"
"v_mfma_f32_16x16x32_bf16 v[172:175], v[220:223], a[28:31], v[172:175]\n"
"v_exp_f32 v164, v164\n"
"v_mfma_f32_16x16x32_bf16 v[172:175], v[208:211], a[16:19], v[172:175]\n"
"v_exp_f32 v157, v157\n"
"v_mfma_f32_16x16x32_bf16 v[172:175], v[212:215], a[20:23], v[172:175]\n"
"s_cbranch_vccnz LPCL0_%=\nLPKL0_%=:\n"
"v_cvt_pk_bf16_f32 v127, v154, v155\n"
"v_mfma_f32_16x16x32_bf16 v[176:179], v[208:211], a[32:35], v[188:191]\n"
"v_exp_f32 v158, v158\n"
"v_mfma_f32_16x16x32_bf16 v[176:179], v[212:215], a[36:39], v[176:179]\n"
"v_exp_f32 v162, v162\n"
"v_mfma_f32_16x16x32_bf16 v[176:179], v[216:219], a[40:43], v[176:179]\n"
"v_exp_f32 v156, v156\n"
"v_mfma_f32_16x16x32_bf16 v[176:179], v[220:223], a[44:47], v[176:179]\n"
"s_waitcnt lgkmcnt(0)\n"
"v_cvt_pk_bf16_f32 v133, v158, v159\n"
"v_mfma_f32_16x16x32_bf16 a[224:227], v[120:123], a[244:247], a[224:227]\n"
"v_exp_f32 v169, v169\n"
"v_mfma_f32_16x16x32_bf16 v[16:19], v[96:99], a[244:247], v[16:19]\n"
"s_waitcnt vmcnt(8)\n"
"v_exp_f32 v172, v172\n"
"v_mfma_f32_16x16x32_bf16 v[16:19], v[100:103], a[248:251], v[16:19]\n"
"s_barrier\n"
"ds_read_b128 v[216:219], v228 offset:17408\n"
"v_mfma_f32_16x16x32_bf16 v[48:51], v[104:107], a[244:247], v[48:51]\n"
"ds_read_b128 v[212:215], v229 offset:16384\n"
"v_cvt_pk_bf16_f32 v140, v160, v161\n"
"v_mfma_f32_16x16x32_bf16 v[48:51], v[108:111], a[248:251], v[48:51]\n"
"ds_read_b128 v[208:211], v228 offset:16384\n"
"v_cvt_pk_bf16_f32 v132, v156, v157\n"
"v_mfma_f32_16x16x32_bf16 v[80:83], v[112:115], a[244:247], v[80:83]\n"
"ds_read_b128 v[220:223], v229 offset:17408\n"
"v_cvt_pk_bf16_f32 v149, v166, v167\n"
"v_mfma_f32_16x16x32_bf16 v[80:83], v[116:119], a[248:251], v[80:83]\n"
"ds_read_b64_tr_b16 a[244:245], v234 offset:0\n"
"v_cvt_pk_bf16_f32 v141, v162, v163\n"
"v_mfma_f32_16x16x32_bf16 v[84:87], v[116:119], v[224:227], v[84:87]\n"
"ds_read_b64_tr_b16 a[248:249], v238 offset:0\n"
"v_mfma_f32_16x16x32_bf16 v[84:87], v[112:115], a[252:255], v[84:87]\n"
"ds_read_b64_tr_b16 a[246:247], v234 offset:4096\n"
"v_mfma_f32_16x16x32_bf16 v[52:55], v[108:111], v[224:227], v[52:55]\n"
"ds_read_b64_tr_b16 a[250:251], v238 offset:4096\n"
"v_mfma_f32_16x16x32_bf16 v[52:55], v[104:107], a[252:255], v[52:55]\n"
"v_exp_f32 v175, v175\n"
"v_mfma_f32_16x16x32_bf16 v[20:23], v[100:103], v[224:227], v[20:23]\n"
"ds_read_b64_tr_b16 v[224:225], v239 offset:0\n"
"v_mfma_f32_16x16x32_bf16 v[20:23], v[96:99], a[252:255], v[20:23]\n"
"v_exp_f32 v173, v173\n"
"v_mfma_f32_16x16x32_bf16 a[228:231], v[120:123], a[252:255], a[228:231]\n"
"s_waitcnt lgkmcnt(5)\n"
"ds_read_b64_tr_b16 a[254:255], v235 offset:4096\n"
"v_cvt_pk_bf16_f32 v148, v164, v165\n"
"v_mfma_f32_16x16x32_bf16 v[152:155], v[208:211], a[0:3], v[180:183]\n"
"v_exp_f32 v174, v174\n"
"v_mfma_f32_16x16x32_bf16 v[152:155], v[212:215], a[4:7], v[152:155]\n"
"ds_read_b64_tr_b16 a[252:253], v235 offset:0\n"
"v_cvt_pk_bf16_f32 v143, v174, v175\n"
"v_mfma_f32_16x16x32_bf16 v[152:155], v[216:219], a[8:11], v[152:155]\n"
"s_add_u32 m0, s44, 98304\n"
"ds_read_b64_tr_b16 v[226:227], v239 offset:4096\n"
"v_mfma_f32_16x16x32_bf16 v[152:155], v[220:223], a[12:15], v[152:155]\n"
"buffer_load_dwordx4 v240, s[20:23], s56 offen nt lds\n"
"v_mfma_f32_16x16x32_bf16 v[156:159], v[216:219], a[24:27], v[184:187]\n"
"v_exp_f32 v171, v171\n"
"v_mfma_f32_16x16x32_bf16 v[156:159], v[220:223], a[28:31], v[156:159]\n"
"v_exp_f32 v168, v168\n"
"v_mfma_f32_16x16x32_bf16 v[156:159], v[208:211], a[16:19], v[156:159]\n"
"v_exp_f32 v176, v176\n"
"v_mfma_f32_16x16x32_bf16 v[156:159], v[212:215], a[20:23], v[156:159]\n"
"v_exp_f32 v177, v177\n"
"v_mfma_f32_16x16x32_bf16 v[160:163], v[208:211], a[32:35], v[188:191]\n"
"v_exp_f32 v179, v179\n"
"v_mfma_f32_16x16x32_bf16 v[160:163], v[212:215], a[36:39], v[160:163]\n"
"v_exp_f32 v170, v170\n"
"v_mfma_f32_16x16x32_bf16 v[160:163], v[216:219], a[40:43], v[160:163]\n"
"v_exp_f32 v153, v153\n"
"v_mfma_f32_16x16x32_bf16 v[160:163], v[220:223], a[44:47], v[160:163]\n"
"v_cvt_pk_bf16_f32 v134, v168, v169\n"
"v_mfma_f32_16x16x32_bf16 v[164:167], v[216:219], a[200:203], v[192:195]\n"
"ds_read_b128 v[216:219], v228 offset:21504\n"
"v_cvt_pk_bf16_f32 v135, v170, v171\n"
"v_mfma_f32_16x16x32_bf16 v[164:167], v[220:223], a[204:207], v[164:167]\n"
"v_cvt_pk_bf16_f32 v142, v172, v173\n"
"v_cvt_pk_bf16_f32 v150, v176, v177\n"
"v_mfma_f32_16x16x32_bf16 v[164:167], v[208:211], a[192:195], v[164:167]\n"
"s_add_u32 s40, s56, s52\n"
"ds_read_b128 v[208:211], v228 offset:20480\n"
"v_mfma_f32_16x16x32_bf16 v[164:167], v[212:215], a[196:199], v[164:167]\n"
"s_waitcnt lgkmcnt(2)\n"
"buffer_load_dwordx4 v240, s[20:23], s40 offen offset:1024 nt lds\n"
"v_mfma_f32_16x16x32_bf16 a[216:219], v[124:127], a[244:247], a[216:219]\n"
"v_exp_f32 v155, v155\n"
"v_mfma_f32_16x16x32_bf16 v[8:11], v[128:131], a[244:247], v[8:11]\n"
"v_exp_f32 v161, v161\n"
"v_mfma_f32_16x16x32_bf16 v[8:11], v[132:135], a[248:251], v[8:11]\n"
"v_exp_f32 v163, v163\n"
"v_mfma_f32_16x16x32_bf16 v[40:43], v[136:139], a[244:247], v[40:43]\n"
"v_exp_f32 v178, v178\n"
"v_mfma_f32_16x16x32_bf16 v[40:43], v[140:143], a[248:251], v[40:43]\n"
"ds_read_b128 v[220:223], v229 offset:21504\n"
"v_cvt_pk_bf16_f32 v151, v178, v179\n"
"v_mfma_f32_16x16x32_bf16 v[72:75], v[144:147], a[244:247], v[72:75]\n"
"ds_read_b64_tr_b16 a[244:245], v234 offset:1024\n"
"v_mfma_f32_16x16x32_bf16 v[72:75], v[148:151], a[248:251], v[72:75]\n"
"v_exp_f32 v158, v158\n"
"v_mfma_f32_16x16x32_bf16 v[76:79], v[148:151], v[224:227], v[76:79]\n"
"ds_read_b128 v[212:215], v229 offset:20480\n"
"v_mfma_f32_16x16x32_bf16 v[76:79], v[144:147], a[252:255], v[76:79]\n"
"v_exp_f32 v160, v160\n"
"v_mfma_f32_16x16x32_bf16 v[44:47], v[140:143], v[224:227], v[44:47]\n"
"s_add_u32 s40, s56, s53\n"
"ds_read_b64_tr_b16 a[248:249], v238 offset:1024\n"
"v_mfma_f32_16x16x32_bf16 v[44:47], v[136:139], a[252:255], v[44:47]\n"
"buffer_load_dwordx4 v240, s[20:23], s40 offen offset:2048 nt lds\n"
"v_mfma_f32_16x16x32_bf16 v[12:15], v[132:135], v[224:227], v[12:15]\n"
"v_exp_f32 v165, v165\n"
"v_mfma_f32_16x16x32_bf16 v[12:15], v[128:131], a[252:255], v[12:15]\n"
"v_exp_f32 v159, v159\n"
"v_mfma_f32_16x16x32_bf16 a[220:223], v[124:127], a[252:255], a[220:223]\n"
"v_exp_f32 v152, v152\n"
"v_smfmac_f32_16x16x64_bf16 v[196:199], a[240:243], v[128:135], v241\n"
"v_mfma_f32_16x16x32_bf16 v[202:205], a[240:243], v[124:127], v[202:205]\n"
"s_waitcnt lgkmcnt(1)\n"
"ds_read_b64_tr_b16 v[224:225], v239 offset:1024\n"
"v_cvt_pk_bf16_f32 v96, v152, v153\n"
"v_mfma_f32_16x16x32_bf16 v[168:171], v[216:219], a[8:11], v[180:183]\n"
"v_exp_f32 v162, v162\n"
"v_mfma_f32_16x16x32_bf16 v[168:171], v[220:223], a[12:15], v[168:171]\n"
"ds_read_b64_tr_b16 a[250:251], v238 offset:5120\n"
"v_mfma_f32_16x16x32_bf16 v[168:171], v[208:211], a[0:3], v[168:171]\n"
"ds_read_b64_tr_b16 a[252:253], v235 offset:1024\n"
"v_mfma_f32_16x16x32_bf16 v[168:171], v[212:215], a[4:7], v[168:171]\n"
"v_exp_f32 v154, v154\n"
"v_mfma_f32_16x16x32_bf16 v[172:175], v[208:211], a[16:19], v[184:187]\n"
"ds_read_b64_tr_b16 v[226:227], v239 offset:5120\n"
"v_mfma_f32_16x16x32_bf16 v[172:175], v[212:215], a[20:23], v[172:175]\n"
"ds_read_b64_tr_b16 a[246:247], v234 offset:5120\n"
"v_cvt_pk_bf16_f32 v97, v154, v155\n"
"v_mfma_f32_16x16x32_bf16 v[172:175], v[216:219], a[24:27], v[172:175]\n"
"s_add_u32 s40, s56, s54\n"
"ds_read_b64_tr_b16 a[254:255], v235 offset:5120\n"
"v_mfma_f32_16x16x32_bf16 v[172:175], v[220:223], a[28:31], v[172:175]\n"
"buffer_load_dwordx4 v240, s[20:23], s40 offen offset:3072 nt lds\n"
"v_mfma_f32_16x16x32_bf16 v[176:179], v[216:219], a[40:43], v[188:191]\n"
"s_add_u32 s55, s55, s59\n"
"v_exp_f32 v157, v157\n"
"v_mfma_f32_16x16x32_bf16 v[176:179], v[220:223], a[44:47], v[176:179]\n"
"s_add_u32 s41, s55, s57\n"
"v_exp_f32 v170, v170\n"
"v_mfma_f32_16x16x32_bf16 v[176:179], v[208:211], a[32:35], v[176:179]\n"
"s_and_b32 s41, s41, s58\n"
"v_exp_f32 v156, v156\n"
"v_mfma_f32_16x16x32_bf16 v[176:179], v[212:215], a[36:39], v[176:179]\n"
"s_andn2_b32 s56, s55, s58\n"
"v_exp_f32 v166, v166\n"
"v_mfma_f32_16x16x32_bf16 v[152:155], v[208:211], a[192:195], v[192:195]\n"
"s_or_b32 s56, s56, s41\n"
"ds_read_b128 v[208:211], v230 offset:16384\n"
"v_mfma_f32_16x16x32_bf16 v[152:155], v[212:215], a[196:199], v[152:155]\n"
"v_exp_f32 v174, v174\n"
"v_mfma_f32_16x16x32_bf16 v[152:155], v[216:219], a[200:203], v[152:155]\n"
"ds_read_b128 v[212:215], v231 offset:16384\n"
"v_mfma_f32_16x16x32_bf16 v[152:155], v[220:223], a[204:207], v[152:155]\n"
"s_waitcnt lgkmcnt(2)\n"
"ds_read_b128 v[216:219], v230 offset:17408\n"
"v_cvt_pk_bf16_f32 v105, v158, v159\n"
"v_mfma_f32_16x16x32_bf16 a[232:235], v[124:127], a[244:247], a[232:235]\n"
"v_exp_f32 v164, v164\n"
"v_mfma_f32_16x16x32_bf16 v[24:27], v[128:131], a[244:247], v[24:27]\n"
"s_add_u32 m0, s44, 49152\n"
"ds_read_b128 v[220:223], v231 offset:17408\n"
"v_mfma_f32_16x16x32_bf16 v[24:27], v[132:135], a[248:251], v[24:27]\n"
"buffer_load_dwordx4 v240, s[16:19], s56 offen nt lds\n"
"v_mfma_f32_16x16x32_bf16 v[56:59], v[136:139], a[244:247], v[56:59]\n"
"v_exp_f32 v167, v167\n"
"v_mfma_f32_16x16x32_bf16 v[56:59], v[140:143], a[248:251], v[56:59]\n"
"v_exp_f32 v176, v176\n"
"v_mfma_f32_16x16x32_bf16 v[88:91], v[144:147], a[244:247], v[88:91]\n"
"v_exp_f32 v153, v153\n"
"v_mfma_f32_16x16x32_bf16 v[88:91], v[148:151], a[248:251], v[88:91]\n"
"v_exp_f32 v173, v173\n"
"v_mfma_f32_16x16x32_bf16 v[92:95], v[148:151], v[224:227], v[92:95]\n"
"ds_read_b64_tr_b16 a[250:251], v236 offset:4096\n"
"v_cvt_pk_bf16_f32 v121, v166, v167\n"
"v_mfma_f32_16x16x32_bf16 v[92:95], v[144:147], a[252:255], v[92:95]\n"
"ds_read_b64_tr_b16 a[244:245], v232 offset:0\n"
"v_cvt_pk_bf16_f32 v112, v160, v161\n"
"v_mfma_f32_16x16x32_bf16 v[60:63], v[140:143], v[224:227], v[60:63]\n"
"ds_read_b64_tr_b16 a[248:249], v236 offset:0\n"
"v_cvt_pk_bf16_f32 v120, v164, v165\n"
"v_mfma_f32_16x16x32_bf16 v[60:63], v[136:139], a[252:255], v[60:63]\n"
"ds_read_b64_tr_b16 a[246:247], v232 offset:4096\n"
"v_cvt_pk_bf16_f32 v104, v156, v157\n"
"v_mfma_f32_16x16x32_bf16 v[28:31], v[132:135], v[224:227], v[28:31]\n"
"ds_read_b64_tr_b16 v[226:227], v237 offset:4096\n"
"v_cvt_pk_bf16_f32 v113, v162, v163\n"
"v_mfma_f32_16x16x32_bf16 v[28:31], v[128:131], a[252:255], v[28:31]\n"
"ds_read_b64_tr_b16 v[224:225], v237 offset:0\n"
"v_mfma_f32_16x16x32_bf16 a[236:239], v[124:127], a[252:255], a[236:239]\n"
"s_add_u32 s40, s56, s52\n"
"ds_read_b64_tr_b16 a[252:253], v233 offset:0\n"
"v_smfmac_f32_16x16x64_bf16 v[198:201], a[240:243], v[136:143], v241\n"
"s_waitcnt lgkmcnt(7)\n"
"buffer_load_dwordx4 v240, s[16:19], s40 offen offset:1024 nt lds\n"
"v_mfma_f32_16x16x32_bf16 v[156:159], v[208:211], a[0:3], v[180:183]\n"
"v_exp_f32 v169, v169\n"
"v_mfma_f32_16x16x32_bf16 v[156:159], v[212:215], a[4:7], v[156:159]\n"
"v_exp_f32 v168, v168\n"
"v_mfma_f32_16x16x32_bf16 v[156:159], v[216:219], a[8:11], v[156:159]\n"
"v_exp_f32 v175, v175\n"
"v_mfma_f32_16x16x32_bf16 v[156:159], v[220:223], a[12:15], v[156:159]\n"
"v_exp_f32 v172, v172\n"
"v_mfma_f32_16x16x32_bf16 v[160:163], v[216:219], a[24:27], v[184:187]\n"
"ds_read_b64_tr_b16 a[254:255], v233 offset:4096\n"
"v_mfma_f32_16x16x32_bf16 v[160:163], v[220:223], a[28:31], v[160:163]\n"
"v_exp_f32 v155, v155\n"
"v_mfma_f32_16x16x32_bf16 v[160:163], v[208:211], a[16:19], v[160:163]\n"
"v_exp_f32 v177, v177\n"
"v_mfma_f32_16x16x32_bf16 v[160:163], v[212:215], a[20:23], v[160:163]\n"
"v_exp_f32 v179, v179\n"
"v_mfma_f32_16x16x32_bf16 v[164:167], v[208:211], a[32:35], v[188:191]\n"
"ds_read_b128 v[208:211], v230 offset:20480\n"
"v_cvt_pk_bf16_f32 v107, v174, v175\n"
"v_mfma_f32_16x16x32_bf16 v[164:167], v[212:215], a[36:39], v[164:167]\n"
"v_exp_f32 v171, v171\n"
"v_mfma_f32_16x16x32_bf16 v[164:167], v[216:219], a[40:43], v[164:167]\n"
"ds_read_b128 v[212:215], v231 offset:20480\n"
"v_cvt_pk_bf16_f32 v106, v172, v173\n"
"v_mfma_f32_16x16x32_bf16 v[164:167], v[220:223], a[44:47], v[164:167]\n"
"s_waitcnt lgkmcnt(3)\n"
"ds_read_b128 v[220:223], v231 offset:21504\n"
"v_cvt_pk_bf16_f32 v98, v168, v169\n"
"v_mfma_f32_16x16x32_bf16 a[208:211], v[124:127], a[244:247], a[208:211]\n"
"v_exp_f32 v154, v154\n"
"v_mfma_f32_16x16x32_bf16 v[0:3], v[128:131], a[244:247], v[0:3]\n"
"s_add_u32 s40, s56, s53\n"
"ds_read_b128 v[216:219], v230 offset:21504\n"
"v_mfma_f32_16x16x32_bf16 v[0:3], v[132:135], a[248:251], v[0:3]\n"
"buffer_load_dwordx4 v240, s[16:19], s40 offen offset:2048 nt lds\n"
"v_mfma_f32_16x16x32_bf16 v[32:35], v[136:139], a[244:247], v[32:35]\n"
"v_exp_f32 v160, v160\n"
"v_mfma_f32_16x16x32_bf16 v[32:35], v[140:143], a[248:251], v[32:35]\n"
"v_exp_f32 v178, v178\n"
"v_mfma_f32_16x16x32_bf16 v[64:67], v[144:147], a[244:247], v[64:67]\n"
"v_exp_f32 v152, v152\n"
"v_mfma_f32_16x16x32_bf16 v[64:67], v[148:151], a[248:251], v[64:67]\n"
"v_exp_f32 v161, v161\n"
"v_mfma_f32_16x16x32_bf16 v[68:71], v[148:151], v[224:227], v[68:71]\n"
"s_waitcnt lgkmcnt(1)\n"
"ds_read_b64_tr_b16 a[244:245], v232 offset:1024\n"
"v_cvt_pk_bf16_f32 v115, v178, v179\n"
"v_mfma_f32_16x16x32_bf16 v[68:71], v[144:147], a[252:255], v[68:71]\n"
"ds_read_b64_tr_b16 a[248:249], v236 offset:1024\n"
"v_cvt_pk_bf16_f32 v114, v176, v177\n"
"v_mfma_f32_16x16x32_bf16 v[36:39], v[140:143], v[224:227], v[36:39]\n"
"ds_read_b64_tr_b16 a[250:251], v236 offset:5120\n"
"v_cvt_pk_bf16_f32 v99, v170, v171\n"
"v_mfma_f32_16x16x32_bf16 v[36:39], v[136:139], a[252:255], v[36:39]\n"
"v_exp_f32 v163, v163\n"
"v_mfma_f32_16x16x32_bf16 v[4:7], v[132:135], v[224:227], v[4:7]\n"
"s_add_u32 s40, s56, s54\n"
"ds_read_b64_tr_b16 a[246:247], v232 offset:5120\n"
"v_mfma_f32_16x16x32_bf16 v[4:7], v[128:131], a[252:255], v[4:7]\n"
"buffer_load_dwordx4 v240, s[16:19], s40 offen offset:3072 nt lds\n"
"v_mfma_f32_16x16x32_bf16 a[212:215], v[124:127], a[252:255], a[212:215]\n"
"v_exp_f32 v167, v167\n"
"v_smfmac_f32_16x16x64_bf16 v[200:203], a[240:243], v[144:151], v241\n"
"v_cvt_pk_bf16_f32 v122, v152, v153\n"
"v_mfma_f32_16x16x32_bf16 v[168:171], v[208:211], a[0:3], v[180:183]\n"
"v_exp_f32 v159, v159\n"
"v_mfma_f32_16x16x32_bf16 v[168:171], v[212:215], a[4:7], v[168:171]\n"
"s_waitcnt lgkmcnt(2)\n"
"v_exp_f32 v165, v165\n"
"v_mfma_f32_16x16x32_bf16 v[168:171], v[216:219], a[8:11], v[168:171]\n"
"ds_read_b64_tr_b16 a[252:253], v233 offset:1024\n"
"ds_read_b64_tr_b16 v[224:225], v237 offset:1024\n"
"v_mfma_f32_16x16x32_bf16 v[168:171], v[220:223], a[12:15], v[168:171]\n"
"ds_read_b64_tr_b16 a[254:255], v233 offset:5120\n"
"ds_read_b64_tr_b16 v[226:227], v237 offset:5120\n"
"v_mfma_f32_16x16x32_bf16 v[172:175], v[216:219], a[24:27], v[184:187]\n"
"v_exp_f32 v166, v166\n"
"v_mfma_f32_16x16x32_bf16 v[172:175], v[220:223], a[28:31], v[172:175]\n"
"v_exp_f32 v164, v164\n"
"v_mfma_f32_16x16x32_bf16 v[172:175], v[208:211], a[16:19], v[172:175]\n"
"v_exp_f32 v157, v157\n"
"v_mfma_f32_16x16x32_bf16 v[172:175], v[212:215], a[20:23], v[172:175]\n"
"s_cbranch_vccnz LPCL1_%=\nLPKL1_%=:\n"
"v_cvt_pk_bf16_f32 v123, v154, v155\n"
"v_mfma_f32_16x16x32_bf16 v[176:179], v[208:211], a[32:35], v[188:191]\n"
"v_exp_f32 v158, v158\n"
"v_mfma_f32_16x16x32_bf16 v[176:179], v[212:215], a[36:39], v[176:179]\n"
"v_exp_f32 v162, v162\n"
"v_mfma_f32_16x16x32_bf16 v[176:179], v[216:219], a[40:43], v[176:179]\n"
"v_exp_f32 v156, v156\n"
"v_mfma_f32_16x16x32_bf16 v[176:179], v[220:223], a[44:47], v[176:179]\n"
"s_waitcnt lgkmcnt(0)\n"
"v_cvt_pk_bf16_f32 v101, v158, v159\n"
"v_mfma_f32_16x16x32_bf16 a[224:227], v[124:127], a[244:247], a[224:227]\n"
"v_exp_f32 v169, v169\n"
"v_mfma_f32_16x16x32_bf16 v[16:19], v[128:131], a[244:247], v[16:19]\n"
"s_waitcnt vmcnt(8)\n"
"v_exp_f32 v172, v172\n"
"v_mfma_f32_16x16x32_bf16 v[16:19], v[132:135], a[248:251], v[16:19]\n"
"s_barrier\n"
"ds_read_b128 v[216:219], v228 offset:33792\n"
"v_mfma_f32_16x16x32_bf16 v[48:51], v[136:139], a[244:247], v[48:51]\n"
"ds_read_b128 v[212:215], v229 offset:32768\n"
"v_cvt_pk_bf16_f32 v108, v160, v161\n"
"v_mfma_f32_16x16x32_bf16 v[48:51], v[140:143], a[248:251], v[48:51]\n"
"ds_read_b128 v[208:211], v228 offset:32768\n"
"v_cvt_pk_bf16_f32 v100, v156, v157\n"
"v_mfma_f32_16x16x32_bf16 v[80:83], v[144:147], a[244:247], v[80:83]\n"
"ds_read_b128 v[220:223], v229 offset:33792\n"
"v_cvt_pk_bf16_f32 v117, v166, v167\n"
"v_mfma_f32_16x16x32_bf16 v[80:83], v[148:151], a[248:251], v[80:83]\n"
"ds_read_b64_tr_b16 a[244:245], v234 offset:16384\n"
"v_cvt_pk_bf16_f32 v109, v162, v163\n"
"v_mfma_f32_16x16x32_bf16 v[84:87], v[148:151], v[224:227], v[84:87]\n"
"ds_read_b64_tr_b16 a[248:249], v238 offset:16384\n"
"v_mfma_f32_16x16x32_bf16 v[84:87], v[144:147], a[252:255], v[84:87]\n"
"ds_read_b64_tr_b16 a[246:247], v234 offset:20480\n"
"v_mfma_f32_16x16x32_bf16 v[52:55], v[140:143], v[224:227], v[52:55]\n"
"ds_read_b64_tr_b16 a[250:251], v238 offset:20480\n"
"v_mfma_f32_16x16x32_bf16 v[52:55], v[136:139], a[252:255], v[52:55]\n"
"v_exp_f32 v175, v175\n"
"v_mfma_f32_16x16x32_bf16 v[20:23], v[132:135], v[224:227], v[20:23]\n"
"ds_read_b64_tr_b16 v[224:225], v239 offset:16384\n"
"v_mfma_f32_16x16x32_bf16 v[20:23], v[128:131], a[252:255], v[20:23]\n"
"v_exp_f32 v173, v173\n"
"v_mfma_f32_16x16x32_bf16 a[228:231], v[124:127], a[252:255], a[228:231]\n"
"s_waitcnt lgkmcnt(5)\n"
"ds_read_b64_tr_b16 a[254:255], v235 offset:20480\n"
"v_cvt_pk_bf16_f32 v116, v164, v165\n"
"LAY2_%=:\n"
"v_mfma_f32_16x16x32_bf16 v[152:155], v[208:211], a[0:3], v[180:183]\n"
"v_exp_f32 v174, v174\n"
"v_mfma_f32_16x16x32_bf16 v[152:155], v[212:215], a[4:7], v[152:155]\n"
"ds_read_b64_tr_b16 a[252:253], v235 offset:16384\n"
"v_cvt_pk_bf16_f32 v111, v174, v175\n"
"v_mfma_f32_16x16x32_bf16 v[152:155], v[216:219], a[8:11], v[152:155]\n"
"s_add_u32 m0, s44, 114688\n"
"ds_read_b64_tr_b16 v[226:227], v239 offset:20480\n"
"v_mfma_f32_16x16x32_bf16 v[152:155], v[220:223], a[12:15], v[152:155]\n"
"buffer_load_dwordx4 v240, s[20:23], s56 offen nt lds\n"
"v_mfma_f32_16x16x32_bf16 v[156:159], v[216:219], a[24:27], v[184:187]\n"
"v_exp_f32 v171, v171\n"
"v_mfma_f32_16x16x32_bf16 v[156:159], v[220:223], a[28:31], v[156:159]\n"
"v_exp_f32 v168, v168\n"
"v_mfma_f32_16x16x32_bf16 v[156:159], v[208:211], a[16:19], v[156:159]\n"
"v_exp_f32 v176, v176\n"
"v_mfma_f32_16x16x32_bf16 v[156:159], v[212:215], a[20:23], v[156:159]\n"
"v_exp_f32 v177, v177\n"
"v_mfma_f32_16x16x32_bf16 v[160:163], v[208:211], a[32:35], v[188:191]\n"
"v_exp_f32 v179, v179\n"
"v_mfma_f32_16x16x32_bf16 v[160:163], v[212:215], a[36:39], v[160:163]\n"
"v_exp_f32 v170, v170\n"
"v_mfma_f32_16x16x32_bf16 v[160:163], v[216:219], a[40:43], v[160:163]\n"
"v_exp_f32 v153, v153\n"
"v_mfma_f32_16x16x32_bf16 v[160:163], v[220:223], a[44:47], v[160:163]\n"
"v_cvt_pk_bf16_f32 v102, v168, v169\n"
"v_mfma_f32_16x16x32_bf16 v[164:167], v[216:219], a[200:203], v[192:195]\n"
"ds_read_b128 v[216:219], v228 offset:37888\n"
"v_cvt_pk_bf16_f32 v103, v170, v171\n"
"v_mfma_f32_16x16x32_bf16 v[164:167], v[220:223], a[204:207], v[164:167]\n"
"v_cvt_pk_bf16_f32 v110, v172, v173\n"
"v_cvt_pk_bf16_f32 v118, v176, v177\n"
"v_mfma_f32_16x16x32_bf16 v[164:167], v[208:211], a[192:195], v[164:167]\n"
"s_add_u32 s40, s56, s52\n"
"ds_read_b128 v[208:211], v228 offset:36864\n"
"v_mfma_f32_16x16x32_bf16 v[164:167], v[212:215], a[196:199], v[164:167]\n"
"s_waitcnt lgkmcnt(2)\n"
"buffer_load_dwordx4 v240, s[20:23], s40 offen offset:1024 nt lds\n"
"v_mfma_f32_16x16x32_bf16 a[216:219], v[120:123], a[244:247], a[216:219]\n"
"v_exp_f32 v155, v155\n"
"v_mfma_f32_16x16x32_bf16 v[8:11], v[96:99], a[244:247], v[8:11]\n"
"s_waitcnt lgkmcnt(2)\n"
"v_exp_f32 v161, v161\n"
"v_mfma_f32_16x16x32_bf16 v[8:11], v[100:103], a[248:251], v[8:11]\n"
"v_exp_f32 v163, v163\n"
"v_mfma_f32_16x16x32_bf16 v[40:43], v[104:107], a[244:247], v[40:43]\n"
"v_exp_f32 v178, v178\n"
"v_mfma_f32_16x16x32_bf16 v[40:43], v[108:111], a[248:251], v[40:43]\n"
"ds_read_b128 v[220:223], v229 offset:37888\n"
"v_cvt_pk_bf16_f32 v119, v178, v179\n"
"v_mfma_f32_16x16x32_bf16 v[72:75], v[112:115], a[244:247], v[72:75]\n"
"ds_read_b64_tr_b16 a[244:245], v234 offset:17408\n"
"v_mfma_f32_16x16x32_bf16 v[72:75], v[116:119], a[248:251], v[72:75]\n"
"v_exp_f32 v158, v158\n"
"v_mfma_f32_16x16x32_bf16 v[76:79], v[116:119], v[224:227], v[76:79]\n"
"ds_read_b128 v[212:215], v229 offset:36864\n"
"v_mfma_f32_16x16x32_bf16 v[76:79], v[112:115], a[252:255], v[76:79]\n"
"v_exp_f32 v160, v160\n"
"v_mfma_f32_16x16x32_bf16 v[44:47], v[108:111], v[224:227], v[44:47]\n"
"s_add_u32 s40, s56, s53\n"
"ds_read_b64_tr_b16 a[248:249], v238 offset:17408\n"
"v_mfma_f32_16x16x32_bf16 v[44:47], v[104:107], a[252:255], v[44:47]\n"
"buffer_load_dwordx4 v240, s[20:23], s40 offen offset:2048 nt lds\n"
"v_mfma_f32_16x16x32_bf16 v[12:15], v[100:103], v[224:227], v[12:15]\n"
"v_exp_f32 v165, v165\n"
"v_mfma_f32_16x16x32_bf16 v[12:15], v[96:99], a[252:255], v[12:15]\n"
"v_exp_f32 v159, v159\n"
"v_mfma_f32_16x16x32_bf16 a[220:223], v[120:123], a[252:255], a[220:223]\n"
"v_exp_f32 v152, v152\n"
"v_smfmac_f32_16x16x64_bf16 v[196:199], a[240:243], v[96:103], v241\n"
"v_mfma_f32_16x16x32_bf16 v[202:205], a[240:243], v[120:123], v[202:205]\n"
"s_waitcnt lgkmcnt(1)\n"
"ds_read_b64_tr_b16 v[224:225], v239 offset:17408\n"
"v_cvt_pk_bf16_f32 v128, v152, v153\n"
"v_mfma_f32_16x16x32_bf16 v[168:171], v[216:219], a[8:11], v[180:183]\n"
"v_exp_f32 v162, v162\n"
"v_mfma_f32_16x16x32_bf16 v[168:171], v[220:223], a[12:15], v[168:171]\n"
"ds_read_b64_tr_b16 a[250:251], v238 offset:21504\n"
"v_mfma_f32_16x16x32_bf16 v[168:171], v[208:211], a[0:3], v[168:171]\n"
"ds_read_b64_tr_b16 a[252:253], v235 offset:17408\n"
"v_mfma_f32_16x16x32_bf16 v[168:171], v[212:215], a[4:7], v[168:171]\n"
"v_exp_f32 v154, v154\n"
"v_mfma_f32_16x16x32_bf16 v[172:175], v[208:211], a[16:19], v[184:187]\n"
"ds_read_b64_tr_b16 v[226:227], v239 offset:21504\n"
"v_mfma_f32_16x16x32_bf16 v[172:175], v[212:215], a[20:23], v[172:175]\n"
"ds_read_b64_tr_b16 a[246:247], v234 offset:21504\n"
"v_cvt_pk_bf16_f32 v129, v154, v155\n"
"v_mfma_f32_16x16x32_bf16 v[172:175], v[216:219], a[24:27], v[172:175]\n"
"s_add_u32 s40, s56, s54\n"
"ds_read_b64_tr_b16 a[254:255], v235 offset:21504\n"
"v_mfma_f32_16x16x32_bf16 v[172:175], v[220:223], a[28:31], v[172:175]\n"
"buffer_load_dwordx4 v240, s[20:23], s40 offen offset:3072 nt lds\n"
"v_mfma_f32_16x16x32_bf16 v[176:179], v[216:219], a[40:43], v[188:191]\n"
"s_add_u32 s55, s55, s59\n"
"v_exp_f32 v157, v157\n"
"v_mfma_f32_16x16x32_bf16 v[176:179], v[220:223], a[44:47], v[176:179]\n"
"s_add_u32 s41, s55, s57\n"
"v_exp_f32 v170, v170\n"
"v_mfma_f32_16x16x32_bf16 v[176:179], v[208:211], a[32:35], v[176:179]\n"
"s_and_b32 s41, s41, s58\n"
"v_exp_f32 v156, v156\n"
"v_mfma_f32_16x16x32_bf16 v[176:179], v[212:215], a[36:39], v[176:179]\n"
"s_andn2_b32 s56, s55, s58\n"
"v_exp_f32 v166, v166\n"
"v_mfma_f32_16x16x32_bf16 v[152:155], v[208:211], a[192:195], v[192:195]\n"
"s_or_b32 s56, s56, s41\n"
"ds_read_b128 v[208:211], v230 offset:32768\n"
"v_mfma_f32_16x16x32_bf16 v[152:155], v[212:215], a[196:199], v[152:155]\n"
"s_cmp_ge_u32 s47, 15\n"
"v_exp_f32 v174, v174\n"
"v_mfma_f32_16x16x32_bf16 v[152:155], v[216:219], a[200:203], v[152:155]\n"
"s_cbranch_scc1 LSW_%=\nLSWK_%=:\n"
"ds_read_b128 v[212:215], v231 offset:32768\n"
"v_mfma_f32_16x16x32_bf16 v[152:155], v[220:223], a[204:207], v[152:155]\n"
"s_waitcnt lgkmcnt(2)\n"
"ds_read_b128 v[216:219], v230 offset:33792\n"
"v_cvt_pk_bf16_f32 v137, v158, v159\n"
"v_mfma_f32_16x16x32_bf16 a[232:235], v[120:123], a[244:247], a[232:235]\n"
"v_exp_f32 v164, v164\n"
"v_mfma_f32_16x16x32_bf16 v[24:27], v[96:99], a[244:247], v[24:27]\n"
"s_mov_b32 m0, s44\n"
"ds_read_b128 v[220:223], v231 offset:33792\n"
"v_mfma_f32_16x16x32_bf16 v[24:27], v[100:103], a[248:251], v[24:27]\n"
"buffer_load_dwordx4 v240, s[16:19], s56 offen nt lds\n"
"v_mfma_f32_16x16x32_bf16 v[56:59], v[104:107], a[244:247], v[56:59]\n"
"v_exp_f32 v167, v167\n"
"v_mfma_f32_16x16x32_bf16 v[56:59], v[108:111], a[248:251], v[56:59]\n"
"v_exp_f32 v176, v176\n"
"v_mfma_f32_16x16x32_bf16 v[88:91], v[112:115], a[244:247], v[88:91]\n"
"v_exp_f32 v153, v153\n"
"v_mfma_f32_16x16x32_bf16 v[88:91], v[116:119], a[248:251], v[88:91]\n"
"v_exp_f32 v173, v173\n"
"v_mfma_f32_16x16x32_bf16 v[92:95], v[116:119], v[224:227], v[92:95]\n"
"ds_read_b64_tr_b16 a[250:251], v236 offset:20480\n"
"v_cvt_pk_bf16_f32 v125, v166, v167\n"
"v_mfma_f32_16x16x32_bf16 v[92:95], v[112:115], a[252:255], v[92:95]\n"
"ds_read_b64_tr_b16 a[244:245], v232 offset:16384\n"
"v_cvt_pk_bf16_f32 v144, v160, v161\n"
"v_mfma_f32_16x16x32_bf16 v[60:63], v[108:111], v[224:227], v[60:63]\n"
"ds_read_b64_tr_b16 a[248:249], v236 offset:16384\n"
"v_cvt_pk_bf16_f32 v124, v164, v165\n"
"v_mfma_f32_16x16x32_bf16 v[60:63], v[104:107], a[252:255], v[60:63]\n"
"ds_read_b64_tr_b16 a[246:247], v232 offset:20480\n"
"v_cvt_pk_bf16_f32 v136, v156, v157\n"
"v_mfma_f32_16x16x32_bf16 v[28:31], v[100:103], v[224:227], v[28:31]\n"
"ds_read_b64_tr_b16 v[226:227], v237 offset:20480\n"
"v_cvt_pk_bf16_f32 v145, v162, v163\n"
"v_mfma_f32_16x16x32_bf16 v[28:31], v[96:99], a[252:255], v[28:31]\n"
"ds_read_b64_tr_b16 v[224:225], v237 offset:16384\n"
"v_mfma_f32_16x16x32_bf16 a[236:239], v[120:123], a[252:255], a[236:239]\n"
"s_add_u32 s40, s56, s52\n"
"ds_read_b64_tr_b16 a[252:253], v233 offset:16384\n"
"v_smfmac_f32_16x16x64_bf16 v[198:201], a[240:243], v[104:111], v241\n"
"s_waitcnt lgkmcnt(7)\n"
"buffer_load_dwordx4 v240, s[16:19], s40 offen offset:1024 nt lds\n"
"v_mfma_f32_16x16x32_bf16 v[156:159], v[208:211], a[0:3], v[180:183]\n"
"v_exp_f32 v169, v169\n"
"v_mfma_f32_16x16x32_bf16 v[156:159], v[212:215], a[4:7], v[156:159]\n"
"v_exp_f32 v168, v168\n"
"v_mfma_f32_16x16x32_bf16 v[156:159], v[216:219], a[8:11], v[156:159]\n"
"v_exp_f32 v175, v175\n"
"v_mfma_f32_16x16x32_bf16 v[156:159], v[220:223], a[12:15], v[156:159]\n"
"v_exp_f32 v172, v172\n"
"v_mfma_f32_16x16x32_bf16 v[160:163], v[216:219], a[24:27], v[184:187]\n"
"ds_read_b64_tr_b16 a[254:255], v233 offset:20480\n"
"v_mfma_f32_16x16x32_bf16 v[160:163], v[220:223], a[28:31], v[160:163]\n"
"v_exp_f32 v155, v155\n"
"v_mfma_f32_16x16x32_bf16 v[160:163], v[208:211], a[16:19], v[160:163]\n"
"v_exp_f32 v177, v177\n"
"v_mfma_f32_16x16x32_bf16 v[160:163], v[212:215], a[20:23], v[160:163]\n"
"v_exp_f32 v179, v179\n"
"v_mfma_f32_16x16x32_bf16 v[164:167], v[208:211], a[32:35], v[188:191]\n"
"ds_read_b128 v[208:211], v230 offset:36864\n"
"v_cvt_pk_bf16_f32 v139, v174, v175\n"
"v_mfma_f32_16x16x32_bf16 v[164:167], v[212:215], a[36:39], v[164:167]\n"
"v_exp_f32 v171, v171\n"
"v_mfma_f32_16x16x32_bf16 v[164:167], v[216:219], a[40:43], v[164:167]\n"
"ds_read_b128 v[212:215], v231 offset:36864\n"
"v_cvt_pk_bf16_f32 v138, v172, v173\n"
"v_mfma_f32_16x16x32_bf16 v[164:167], v[220:223], a[44:47], v[164:167]\n"
"s_waitcnt lgkmcnt(3)\n"
"ds_read_b128 v[220:223], v231 offset:37888\n"
"v_cvt_pk_bf16_f32 v130, v168, v169\n"
"v_mfma_f32_16x16x32_bf16 a[208:211], v[120:123], a[244:247], a[208:211]\n"
"v_exp_f32 v154, v154\n"
"v_mfma_f32_16x16x32_bf16 v[0:3], v[96:99], a[244:247], v[0:3]\n"
"s_add_u32 s40, s56, s53\n"
"ds_read_b128 v[216:219], v230 offset:37888\n"
"v_mfma_f32_16x16x32_bf16 v[0:3], v[100:103], a[248:251], v[0:3]\n"
"buffer_load_dwordx4 v240, s[16:19], s40 offen offset:2048 nt lds\n"
"v_mfma_f32_16x16x32_bf16 v[32:35], v[104:107], a[244:247], v[32:35]\n"
"v_exp_f32 v160, v160\n"
"v_mfma_f32_16x16x32_bf16 v[32:35], v[108:111], a[248:251], v[32:35]\n"
"v_exp_f32 v178, v178\n"
"v_mfma_f32_16x16x32_bf16 v[64:67], v[112:115], a[244:247], v[64:67]\n"
"v_exp_f32 v152, v152\n"
"v_mfma_f32_16x16x32_bf16 v[64:67], v[116:119], a[248:251], v[64:67]\n"
"v_exp_f32 v161, v161\n"
"v_mfma_f32_16x16x32_bf16 v[68:71], v[116:119], v[224:227], v[68:71]\n"
"s_waitcnt lgkmcnt(1)\n"
"ds_read_b64_tr_b16 a[244:245], v232 offset:17408\n"
"v_cvt_pk_bf16_f32 v147, v178, v179\n"
"v_mfma_f32_16x16x32_bf16 v[68:71], v[112:115], a[252:255], v[68:71]\n"
"ds_read_b64_tr_b16 a[248:249], v236 offset:17408\n"
"v_cvt_pk_bf16_f32 v146, v176, v177\n"
"v_mfma_f32_16x16x32_bf16 v[36:39], v[108:111], v[224:227], v[36:39]\n"
"ds_read_b64_tr_b16 a[250:251], v236 offset:21504\n"
"v_cvt_pk_bf16_f32 v131, v170, v171\n"
"v_mfma_f32_16x16x32_bf16 v[36:39], v[104:107], a[252:255], v[36:39]\n"
"v_exp_f32 v163, v163\n"
"v_mfma_f32_16x16x32_bf16 v[4:7], v[100:103], v[224:227], v[4:7]\n"
"s_add_u32 s40, s56, s54\n"
"ds_read_b64_tr_b16 a[246:247], v232 offset:21504\n"
"v_mfma_f32_16x16x32_bf16 v[4:7], v[96:99], a[252:255], v[4:7]\n"
"buffer_load_dwordx4 v240, s[16:19], s40 offen offset:3072 nt lds\n"
"v_mfma_f32_16x16x32_bf16 a[212:215], v[120:123], a[252:255], a[212:215]\n"
"v_exp_f32 v167, v167\n"
"v_smfmac_f32_16x16x64_bf16 v[200:203], a[240:243], v[112:119], v241\n"
"v_cvt_pk_bf16_f32 v126, v152, v153\n"
"v_mfma_f32_16x16x32_bf16 v[168:171], v[208:211], a[0:3], v[180:183]\n"
"v_exp_f32 v159, v159\n"
"v_mfma_f32_16x16x32_bf16 v[168:171], v[212:215], a[4:7], v[168:171]\n"
"s_waitcnt lgkmcnt(2)\n"
"v_exp_f32 v165, v165\n"
"v_mfma_f32_16x16x32_bf16 v[168:171], v[216:219], a[8:11], v[168:171]\n"
"ds_read_b64_tr_b16 a[252:253], v233 offset:17408\n"
"ds_read_b64_tr_b16 v[224:225], v237 offset:17408\n"
"v_mfma_f32_16x16x32_bf16 v[168:171], v[220:223], a[12:15], v[168:171]\n"
"ds_read_b64_tr_b16 a[254:255], v233 offset:21504\n"
"ds_read_b64_tr_b16 v[226:227], v237 offset:21504\n"
"v_mfma_f32_16x16x32_bf16 v[172:175], v[216:219], a[24:27], v[184:187]\n"
"v_exp_f32 v166, v166\n"
"v_mfma_f32_16x16x32_bf16 v[172:175], v[220:223], a[28:31], v[172:175]\n"
"v_exp_f32 v164, v164\n"
"v_mfma_f32_16x16x32_bf16 v[172:175], v[208:211], a[16:19], v[172:175]\n"
"v_exp_f32 v157, v157\n"
"v_mfma_f32_16x16x32_bf16 v[172:175], v[212:215], a[20:23], v[172:175]\n"
"s_cbranch_vccnz LPCL2_%=\nLPKL2_%=:\n"
"v_cvt_pk_bf16_f32 v127, v154, v155\n"
"v_mfma_f32_16x16x32_bf16 v[176:179], v[208:211], a[32:35], v[188:191]\n"
"v_exp_f32 v158, v158\n"
"v_mfma_f32_16x16x32_bf16 v[176:179], v[212:215], a[36:39], v[176:179]\n"
"v_exp_f32 v162, v162\n"
"v_mfma_f32_16x16x32_bf16 v[176:179], v[216:219], a[40:43], v[176:179]\n"
"v_exp_f32 v156, v156\n"
"v_mfma_f32_16x16x32_bf16 v[176:179], v[220:223], a[44:47], v[176:179]\n"
"s_waitcnt lgkmcnt(0)\n"
"v_cvt_pk_bf16_f32 v133, v158, v159\n"
"v_mfma_f32_16x16x32_bf16 a[224:227], v[120:123], a[244:247], a[224:227]\n"
"v_exp_f32 v169, v169\n"
"v_mfma_f32_16x16x32_bf16 v[16:19], v[96:99], a[244:247], v[16:19]\n"
"s_waitcnt vmcnt(8)\n"
"v_exp_f32 v172, v172\n"
"v_mfma_f32_16x16x32_bf16 v[16:19], v[100:103], a[248:251], v[16:19]\n"
"s_barrier\n"
"ds_read_b128 v[216:219], v228 offset:50176\n"
"v_mfma_f32_16x16x32_bf16 v[48:51], v[104:107], a[244:247], v[48:51]\n"
"ds_read_b128 v[212:215], v229 offset:49152\n"
"v_cvt_pk_bf16_f32 v140, v160, v161\n"
"v_mfma_f32_16x16x32_bf16 v[48:51], v[108:111], a[248:251], v[48:51]\n"
"ds_read_b128 v[208:211], v228 offset:49152\n"
"v_cvt_pk_bf16_f32 v132, v156, v157\n"
"v_mfma_f32_16x16x32_bf16 v[80:83], v[112:115], a[244:247], v[80:83]\n"
"ds_read_b128 v[220:223], v229 offset:50176\n"
"v_cvt_pk_bf16_f32 v149, v166, v167\n"
"v_mfma_f32_16x16x32_bf16 v[80:83], v[116:119], a[248:251], v[80:83]\n"
"ds_read_b64_tr_b16 a[244:245], v234 offset:32768\n"
"v_cvt_pk_bf16_f32 v141, v162, v163\n"
"v_mfma_f32_16x16x32_bf16 v[84:87], v[116:119], v[224:227], v[84:87]\n"
"ds_read_b64_tr_b16 a[248:249], v238 offset:32768\n"
"v_mfma_f32_16x16x32_bf16 v[84:87], v[112:115], a[252:255], v[84:87]\n"
"ds_read_b64_tr_b16 a[246:247], v234 offset:36864\n"
"v_mfma_f32_16x16x32_bf16 v[52:55], v[108:111], v[224:227], v[52:55]\n"
"ds_read_b64_tr_b16 a[250:251], v238 offset:36864\n"
"v_mfma_f32_16x16x32_bf16 v[52:55], v[104:107], a[252:255], v[52:55]\n"
"v_exp_f32 v175, v175\n"
"v_mfma_f32_16x16x32_bf16 v[20:23], v[100:103], v[224:227], v[20:23]\n"
"ds_read_b64_tr_b16 v[224:225], v239 offset:32768\n"
"v_mfma_f32_16x16x32_bf16 v[20:23], v[96:99], a[252:255], v[20:23]\n"
"v_exp_f32 v173, v173\n"
"v_mfma_f32_16x16x32_bf16 a[228:231], v[120:123], a[252:255], a[228:231]\n"
"s_waitcnt lgkmcnt(5)\n"
"ds_read_b64_tr_b16 a[254:255], v235 offset:36864\n"
"v_cvt_pk_bf16_f32 v148, v164, v165\n"
"v_mfma_f32_16x16x32_bf16 v[152:155], v[208:211], a[0:3], v[180:183]\n"
"v_exp_f32 v174, v174\n"
"v_mfma_f32_16x16x32_bf16 v[152:155], v[212:215], a[4:7], v[152:155]\n"
"ds_read_b64_tr_b16 a[252:253], v235 offset:32768\n"
"v_cvt_pk_bf16_f32 v143, v174, v175\n"
"v_mfma_f32_16x16x32_bf16 v[152:155], v[216:219], a[8:11], v[152:155]\n"
"s_add_u32 m0, s44, 65536\n"
"ds_read_b64_tr_b16 v[226:227], v239 offset:36864\n"
"v_mfma_f32_16x16x32_bf16 v[152:155], v[220:223], a[12:15], v[152:155]\n"
"buffer_load_dwordx4 v240, s[20:23], s56 offen nt lds\n"
"v_mfma_f32_16x16x32_bf16 v[156:159], v[216:219], a[24:27], v[184:187]\n"
"v_exp_f32 v171, v171\n"
"v_mfma_f32_16x16x32_bf16 v[156:159], v[220:223], a[28:31], v[156:159]\n"
"v_exp_f32 v168, v168\n"
"v_mfma_f32_16x16x32_bf16 v[156:159], v[208:211], a[16:19], v[156:159]\n"
"v_exp_f32 v176, v176\n"
"v_mfma_f32_16x16x32_bf16 v[156:159], v[212:215], a[20:23], v[156:159]\n"
"v_exp_f32 v177, v177\n"
"v_mfma_f32_16x16x32_bf16 v[160:163], v[208:211], a[32:35], v[188:191]\n"
"v_exp_f32 v179, v179\n"
"v_mfma_f32_16x16x32_bf16 v[160:163], v[212:215], a[36:39], v[160:163]\n"
"v_exp_f32 v170, v170\n"
"v_mfma_f32_16x16x32_bf16 v[160:163], v[216:219], a[40:43], v[160:163]\n"
"v_exp_f32 v153, v153\n"
"v_mfma_f32_16x16x32_bf16 v[160:163], v[220:223], a[44:47], v[160:163]\n"
"v_cvt_pk_bf16_f32 v134, v168, v169\n"
"v_mfma_f32_16x16x32_bf16 v[164:167], v[216:219], a[200:203], v[192:195]\n"
"ds_read_b128 v[216:219], v228 offset:54272\n"
"v_cvt_pk_bf16_f32 v135, v170, v171\n"
"v_mfma_f32_16x16x32_bf16 v[164:167], v[220:223], a[204:207], v[164:167]\n"
"v_cvt_pk_bf16_f32 v142, v172, v173\n"
"v_cvt_pk_bf16_f32 v150, v176, v177\n"
"v_mfma_f32_16x16x32_bf16 v[164:167], v[208:211], a[192:195], v[164:167]\n"
"s_add_u32 s40, s56, s52\n"
"ds_read_b128 v[208:211], v228 offset:53248\n"
"v_mfma_f32_16x16x32_bf16 v[164:167], v[212:215], a[196:199], v[164:167]\n"
"s_waitcnt lgkmcnt(2)\n"
"buffer_load_dwordx4 v240, s[20:23], s40 offen offset:1024 nt lds\n"
"v_mfma_f32_16x16x32_bf16 a[216:219], v[124:127], a[244:247], a[216:219]\n"
"v_exp_f32 v155, v155\n"
"v_mfma_f32_16x16x32_bf16 v[8:11], v[128:131], a[244:247], v[8:11]\n"
"v_exp_f32 v161, v161\n"
"v_mfma_f32_16x16x32_bf16 v[8:11], v[132:135], a[248:251], v[8:11]\n"
"v_exp_f32 v163, v163\n"
"v_mfma_f32_16x16x32_bf16 v[40:43], v[136:139], a[244:247], v[40:43]\n"
"v_exp_f32 v178, v178\n"
"v_mfma_f32_16x16x32_bf16 v[40:43], v[140:143], a[248:251], v[40:43]\n"
"ds_read_b128 v[220:223], v229 offset:54272\n"
"v_cvt_pk_bf16_f32 v151, v178, v179\n"
"v_mfma_f32_16x16x32_bf16 v[72:75], v[144:147], a[244:247], v[72:75]\n"
"ds_read_b64_tr_b16 a[244:245], v234 offset:33792\n"
"v_mfma_f32_16x16x32_bf16 v[72:75], v[148:151], a[248:251], v[72:75]\n"
"v_exp_f32 v158, v158\n"
"v_mfma_f32_16x16x32_bf16 v[76:79], v[148:151], v[224:227], v[76:79]\n"
"ds_read_b128 v[212:215], v229 offset:53248\n"
"v_mfma_f32_16x16x32_bf16 v[76:79], v[144:147], a[252:255], v[76:79]\n"
"v_exp_f32 v160, v160\n"
"v_mfma_f32_16x16x32_bf16 v[44:47], v[140:143], v[224:227], v[44:47]\n"
"s_add_u32 s40, s56, s53\n"
"ds_read_b64_tr_b16 a[248:249], v238 offset:33792\n"
"v_mfma_f32_16x16x32_bf16 v[44:47], v[136:139], a[252:255], v[44:47]\n"
"buffer_load_dwordx4 v240, s[20:23], s40 offen offset:2048 nt lds\n"
"v_mfma_f32_16x16x32_bf16 v[12:15], v[132:135], v[224:227], v[12:15]\n"
"v_exp_f32 v165, v165\n"
"v_mfma_f32_16x16x32_bf16 v[12:15], v[128:131], a[252:255], v[12:15]\n"
"v_exp_f32 v159, v159\n"
"v_mfma_f32_16x16x32_bf16 a[220:223], v[124:127], a[252:255], a[220:223]\n"
"v_exp_f32 v152, v152\n"
"v_smfmac_f32_16x16x64_bf16 v[196:199], a[240:243], v[128:135], v241\n"
"v_mfma_f32_16x16x32_bf16 v[202:205], a[240:243], v[124:127], v[202:205]\n"
"s_waitcnt lgkmcnt(1)\n"
"ds_read_b64_tr_b16 v[224:225], v239 offset:33792\n"
"v_cvt_pk_bf16_f32 v96, v152, v153\n"
"v_mfma_f32_16x16x32_bf16 v[168:171], v[216:219], a[8:11], v[180:183]\n"
"v_exp_f32 v162, v162\n"
"v_mfma_f32_16x16x32_bf16 v[168:171], v[220:223], a[12:15], v[168:171]\n"
"ds_read_b64_tr_b16 a[250:251], v238 offset:37888\n"
"v_mfma_f32_16x16x32_bf16 v[168:171], v[208:211], a[0:3], v[168:171]\n"
"ds_read_b64_tr_b16 a[252:253], v235 offset:33792\n"
"v_mfma_f32_16x16x32_bf16 v[168:171], v[212:215], a[4:7], v[168:171]\n"
"v_exp_f32 v154, v154\n"
"v_mfma_f32_16x16x32_bf16 v[172:175], v[208:211], a[16:19], v[184:187]\n"
"ds_read_b64_tr_b16 v[226:227], v239 offset:37888\n"
"v_mfma_f32_16x16x32_bf16 v[172:175], v[212:215], a[20:23], v[172:175]\n"
"ds_read_b64_tr_b16 a[246:247], v234 offset:37888\n"
"v_cvt_pk_bf16_f32 v97, v154, v155\n"
"v_mfma_f32_16x16x32_bf16 v[172:175], v[216:219], a[24:27], v[172:175]\n"
"s_add_u32 s40, s56, s54\n"
"ds_read_b64_tr_b16 a[254:255], v235 offset:37888\n"
"v_mfma_f32_16x16x32_bf16 v[172:175], v[220:223], a[28:31], v[172:175]\n"
"buffer_load_dwordx4 v240, s[20:23], s40 offen offset:3072 nt lds\n"
"v_mfma_f32_16x16x32_bf16 v[176:179], v[216:219], a[40:43], v[188:191]\n"
"s_add_u32 s55, s55, s59\n"
"v_exp_f32 v157, v157\n"
"v_mfma_f32_16x16x32_bf16 v[176:179], v[220:223], a[44:47], v[176:179]\n"
"s_add_u32 s41, s55, s57\n"
"v_exp_f32 v170, v170\n"
"v_mfma_f32_16x16x32_bf16 v[176:179], v[208:211], a[32:35], v[176:179]\n"
"s_and_b32 s41, s41, s58\n"
"v_exp_f32 v156, v156\n"
"v_mfma_f32_16x16x32_bf16 v[176:179], v[212:215], a[36:39], v[176:179]\n"
"s_andn2_b32 s56, s55, s58\n"
"v_exp_f32 v166, v166\n"
"v_mfma_f32_16x16x32_bf16 v[152:155], v[208:211], a[192:195], v[192:195]\n"
"s_or_b32 s56, s56, s41\n"
"ds_read_b128 v[208:211], v230 offset:49152\n"
"v_mfma_f32_16x16x32_bf16 v[152:155], v[212:215], a[196:199], v[152:155]\n"
"v_exp_f32 v174, v174\n"
"v_mfma_f32_16x16x32_bf16 v[152:155], v[216:219], a[200:203], v[152:155]\n"
"ds_read_b128 v[212:215], v231 offset:49152\n"
"v_mfma_f32_16x16x32_bf16 v[152:155], v[220:223], a[204:207], v[152:155]\n"
"s_waitcnt lgkmcnt(2)\n"
"ds_read_b128 v[216:219], v230 offset:50176\n"
"v_cvt_pk_bf16_f32 v105, v158, v159\n"
"v_mfma_f32_16x16x32_bf16 a[232:235], v[124:127], a[244:247], a[232:235]\n"
"v_exp_f32 v164, v164\n"
"v_mfma_f32_16x16x32_bf16 v[24:27], v[128:131], a[244:247], v[24:27]\n"
"s_add_u32 m0, s44, 16384\n"
"ds_read_b128 v[220:223], v231 offset:50176\n"
"v_mfma_f32_16x16x32_bf16 v[24:27], v[132:135], a[248:251], v[24:27]\n"
"buffer_load_dwordx4 v240, s[16:19], s56 offen nt lds\n"
"v_mfma_f32_16x16x32_bf16 v[56:59], v[136:139], a[244:247], v[56:59]\n"
"v_exp_f32 v167, v167\n"
"v_mfma_f32_16x16x32_bf16 v[56:59], v[140:143], a[248:251], v[56:59]\n"
"v_exp_f32 v176, v176\n"
"v_mfma_f32_16x16x32_bf16 v[88:91], v[144:147], a[244:247], v[88:91]\n"
"v_exp_f32 v153, v153\n"
"v_mfma_f32_16x16x32_bf16 v[88:91], v[148:151], a[248:251], v[88:91]\n"
"v_exp_f32 v173, v173\n"
"v_mfma_f32_16x16x32_bf16 v[92:95], v[148:151], v[224:227], v[92:95]\n"
"ds_read_b64_tr_b16 a[250:251], v236 offset:36864\n"
"v_cvt_pk_bf16_f32 v121, v166, v167\n"
"v_mfma_f32_16x16x32_bf16 v[92:95], v[144:147], a[252:255], v[92:95]\n"
"ds_read_b64_tr_b16 a[244:245], v232 offset:32768\n"
"v_cvt_pk_bf16_f32 v112, v160, v161\n"
"v_mfma_f32_16x16x32_bf16 v[60:63], v[140:143], v[224:227], v[60:63]\n"
"ds_read_b64_tr_b16 a[248:249], v236 offset:32768\n"
"v_cvt_pk_bf16_f32 v120, v164, v165\n"
"v_mfma_f32_16x16x32_bf16 v[60:63], v[136:139], a[252:255], v[60:63]\n"
"ds_read_b64_tr_b16 a[246:247], v232 offset:36864\n"
"v_cvt_pk_bf16_f32 v104, v156, v157\n"
"v_mfma_f32_16x16x32_bf16 v[28:31], v[132:135], v[224:227], v[28:31]\n"
"ds_read_b64_tr_b16 v[226:227], v237 offset:36864\n"
"v_cvt_pk_bf16_f32 v113, v162, v163\n"
"v_mfma_f32_16x16x32_bf16 v[28:31], v[128:131], a[252:255], v[28:31]\n"
"ds_read_b64_tr_b16 v[224:225], v237 offset:32768\n"
"v_mfma_f32_16x16x32_bf16 a[236:239], v[124:127], a[252:255], a[236:239]\n"
"s_add_u32 s40, s56, s52\n"
"ds_read_b64_tr_b16 a[252:253], v233 offset:32768\n"
"v_smfmac_f32_16x16x64_bf16 v[198:201], a[240:243], v[136:143], v241\n"
"s_waitcnt lgkmcnt(7)\n"
"buffer_load_dwordx4 v240, s[16:19], s40 offen offset:1024 nt lds\n"
"v_mfma_f32_16x16x32_bf16 v[156:159], v[208:211], a[0:3], v[180:183]\n"
"v_exp_f32 v169, v169\n"
"v_mfma_f32_16x16x32_bf16 v[156:159], v[212:215], a[4:7], v[156:159]\n"
"v_exp_f32 v168, v168\n"
"v_mfma_f32_16x16x32_bf16 v[156:159], v[216:219], a[8:11], v[156:159]\n"
"v_exp_f32 v175, v175\n"
"v_mfma_f32_16x16x32_bf16 v[156:159], v[220:223], a[12:15], v[156:159]\n"
"v_exp_f32 v172, v172\n"
"v_mfma_f32_16x16x32_bf16 v[160:163], v[216:219], a[24:27], v[184:187]\n"
"ds_read_b64_tr_b16 a[254:255], v233 offset:36864\n"
"v_mfma_f32_16x16x32_bf16 v[160:163], v[220:223], a[28:31], v[160:163]\n"
"v_exp_f32 v155, v155\n"
"v_mfma_f32_16x16x32_bf16 v[160:163], v[208:211], a[16:19], v[160:163]\n"
"v_exp_f32 v177, v177\n"
"v_mfma_f32_16x16x32_bf16 v[160:163], v[212:215], a[20:23], v[160:163]\n"
"v_exp_f32 v179, v179\n"
"v_mfma_f32_16x16x32_bf16 v[164:167], v[208:211], a[32:35], v[188:191]\n"
"ds_read_b128 v[208:211], v230 offset:53248\n"
"v_cvt_pk_bf16_f32 v107, v174, v175\n"
"v_mfma_f32_16x16x32_bf16 v[164:167], v[212:215], a[36:39], v[164:167]\n"
"v_exp_f32 v171, v171\n"
"v_mfma_f32_16x16x32_bf16 v[164:167], v[216:219], a[40:43], v[164:167]\n"
"ds_read_b128 v[212:215], v231 offset:53248\n"
"v_cvt_pk_bf16_f32 v106, v172, v173\n"
"v_mfma_f32_16x16x32_bf16 v[164:167], v[220:223], a[44:47], v[164:167]\n"
"s_waitcnt lgkmcnt(3)\n"
"ds_read_b128 v[220:223], v231 offset:54272\n"
"v_cvt_pk_bf16_f32 v98, v168, v169\n"
"v_mfma_f32_16x16x32_bf16 a[208:211], v[124:127], a[244:247], a[208:211]\n"
"v_exp_f32 v154, v154\n"
"v_mfma_f32_16x16x32_bf16 v[0:3], v[128:131], a[244:247], v[0:3]\n"
"s_add_u32 s40, s56, s53\n"
"ds_read_b128 v[216:219], v230 offset:54272\n"
"v_mfma_f32_16x16x32_bf16 v[0:3], v[132:135], a[248:251], v[0:3]\n"
"buffer_load_dwordx4 v240, s[16:19], s40 offen offset:2048 nt lds\n"
"v_mfma_f32_16x16x32_bf16 v[32:35], v[136:139], a[244:247], v[32:35]\n"
"v_exp_f32 v160, v160\n"
"v_mfma_f32_16x16x32_bf16 v[32:35], v[140:143], a[248:251], v[32:35]\n"
"v_exp_f32 v178, v178\n"
"v_mfma_f32_16x16x32_bf16 v[64:67], v[144:147], a[244:247], v[64:67]\n"
"v_exp_f32 v152, v152\n"
"v_mfma_f32_16x16x32_bf16 v[64:67], v[148:151], a[248:251], v[64:67]\n"
"v_exp_f32 v161, v161\n"
"v_mfma_f32_16x16x32_bf16 v[68:71], v[148:151], v[224:227], v[68:71]\n"
"s_waitcnt lgkmcnt(1)\n"
"ds_read_b64_tr_b16 a[244:245], v232 offset:33792\n"
"v_cvt_pk_bf16_f32 v115, v178, v179\n"
"v_mfma_f32_16x16x32_bf16 v[68:71], v[144:147], a[252:255], v[68:71]\n"
"ds_read_b64_tr_b16 a[248:249], v236 offset:33792\n"
"v_cvt_pk_bf16_f32 v114, v176, v177\n"
"v_mfma_f32_16x16x32_bf16 v[36:39], v[140:143], v[224:227], v[36:39]\n"
"ds_read_b64_tr_b16 a[250:251], v236 offset:37888\n"
"v_cvt_pk_bf16_f32 v99, v170, v171\n"
"v_mfma_f32_16x16x32_bf16 v[36:39], v[136:139], a[252:255], v[36:39]\n"
"v_exp_f32 v163, v163\n"
"v_mfma_f32_16x16x32_bf16 v[4:7], v[132:135], v[224:227], v[4:7]\n"
"s_add_u32 s40, s56, s54\n"
"ds_read_b64_tr_b16 a[246:247], v232 offset:37888\n"
"v_mfma_f32_16x16x32_bf16 v[4:7], v[128:131], a[252:255], v[4:7]\n"
"buffer_load_dwordx4 v240, s[16:19], s40 offen offset:3072 nt lds\n"
"v_mfma_f32_16x16x32_bf16 a[212:215], v[124:127], a[252:255], a[212:215]\n"
"v_exp_f32 v167, v167\n"
"v_smfmac_f32_16x16x64_bf16 v[200:203], a[240:243], v[144:151], v241\n"
"v_cvt_pk_bf16_f32 v122, v152, v153\n"
"v_mfma_f32_16x16x32_bf16 v[168:171], v[208:211], a[0:3], v[180:183]\n"
"v_exp_f32 v159, v159\n"
"v_mfma_f32_16x16x32_bf16 v[168:171], v[212:215], a[4:7], v[168:171]\n"
"s_waitcnt lgkmcnt(2)\n"
"v_exp_f32 v165, v165\n"
"v_mfma_f32_16x16x32_bf16 v[168:171], v[216:219], a[8:11], v[168:171]\n"
"ds_read_b64_tr_b16 a[252:253], v233 offset:33792\n"
"ds_read_b64_tr_b16 v[224:225], v237 offset:33792\n"
"v_mfma_f32_16x16x32_bf16 v[168:171], v[220:223], a[12:15], v[168:171]\n"
"ds_read_b64_tr_b16 a[254:255], v233 offset:37888\n"
"ds_read_b64_tr_b16 v[226:227], v237 offset:37888\n"
"v_mfma_f32_16x16x32_bf16 v[172:175], v[216:219], a[24:27], v[184:187]\n"
"v_exp_f32 v166, v166\n"
"v_mfma_f32_16x16x32_bf16 v[172:175], v[220:223], a[28:31], v[172:175]\n"
"v_exp_f32 v164, v164\n"
"v_mfma_f32_16x16x32_bf16 v[172:175], v[208:211], a[16:19], v[172:175]\n"
"v_exp_f32 v157, v157\n"
"v_mfma_f32_16x16x32_bf16 v[172:175], v[212:215], a[20:23], v[172:175]\n"
"s_cbranch_vccnz LPCL3_%=\nLPKL3_%=:\n"
"v_cvt_pk_bf16_f32 v123, v154, v155\n"
"v_mfma_f32_16x16x32_bf16 v[176:179], v[208:211], a[32:35], v[188:191]\n"
"s_add_u32 s47, s47, 1\n"
"v_exp_f32 v158, v158\n"
"v_mfma_f32_16x16x32_bf16 v[176:179], v[212:215], a[36:39], v[176:179]\n"
"v_exp_f32 v162, v162\n"
"v_mfma_f32_16x16x32_bf16 v[176:179], v[216:219], a[40:43], v[176:179]\n"
"s_and_b32 s40, s47, 3\n"
"v_exp_f32 v156, v156\n"
"v_mfma_f32_16x16x32_bf16 v[176:179], v[220:223], a[44:47], v[176:179]\n"
"s_waitcnt lgkmcnt(0)\n"
"v_cvt_pk_bf16_f32 v101, v158, v159\n"
"s_cbranch_scc1 LAL_%=\n"
"s_cmp_eq_u32 s47, 8\n"
"s_cbranch_scc1 LTH1_%=\n"
"s_cmp_eq_u32 s47, 12\n"
"s_cbranch_scc1 LTH2_%=\n"
"s_cmp_eq_u32 s47, 16\n"
"s_cbranch_scc1 LAX_%=\n"
"LTH0_%=:\n"
"v_mfma_f32_16x16x32_bf16 a[224:227], v[124:127], a[244:247], a[224:227]\n"
"v_exp_f32 v171, v171\n"
"v_mfma_f32_16x16x32_bf16 v[16:19], v[128:131], a[244:247], v[16:19]\n"
"s_waitcnt vmcnt(8)\n"
"v_exp_f32 v168, v168\n"
"v_mfma_f32_16x16x32_bf16 v[16:19], v[132:135], a[248:251], v[16:19]\n"
"s_barrier\n"
"v_mfma_f32_16x16x32_bf16 v[48:51], v[136:139], a[244:247], v[48:51]\n"
"v_exp_f32 v179, v179\n"
"v_mfma_f32_16x16x32_bf16 v[48:51], v[140:143], a[248:251], v[48:51]\n"
"v_exp_f32 v169, v169\n"
"v_mfma_f32_16x16x32_bf16 v[80:83], v[144:147], a[244:247], v[80:83]\n"
"ds_read_b64_tr_b16 a[244:245], v234 offset:49152\n"
"v_cvt_pk_bf16_f32 v102, v168, v169\n"
"v_mfma_f32_16x16x32_bf16 v[80:83], v[148:151], a[248:251], v[80:83]\n"
"ds_read_b64_tr_b16 a[246:247], v234 offset:53248\n"
"ds_read_b64_tr_b16 a[250:251], v238 offset:53248\n"
"v_mfma_f32_16x16x32_bf16 v[84:87], v[148:151], v[224:227], v[84:87]\n"
"v_cvt_pk_bf16_f32 v108, v160, v161\n"
"v_cvt_pk_bf16_f32 v109, v162, v163\n"
"v_mfma_f32_16x16x32_bf16 v[84:87], v[144:147], a[252:255], v[84:87]\n"
"v_cvt_pk_bf16_f32 v117, v166, v167\n"
"v_exp_f32 v172, v172\n"
"v_exp_f32 v173, v173\n"
"v_exp_f32 v176, v176\n"
"v_exp_f32 v177, v177\n"
"v_exp_f32 v178, v178\n"
"v_mfma_f32_16x16x32_bf16 v[52:55], v[140:143], v[224:227], v[52:55]\n"
"ds_read_b64_tr_b16 a[248:249], v238 offset:49152\n"
"v_cvt_pk_bf16_f32 v100, v156, v157\n"
"v_mfma_f32_16x16x32_bf16 v[52:55], v[136:139], a[252:255], v[52:55]\n"
"v_exp_f32 v170, v170\n"
"v_mfma_f32_16x16x32_bf16 v[20:23], v[132:135], v[224:227], v[20:23]\n"
"ds_read_b64_tr_b16 v[226:227], v239 offset:53248\n"
"v_cvt_pk_bf16_f32 v110, v172, v173\n"
"v_mfma_f32_16x16x32_bf16 v[20:23], v[128:131], a[252:255], v[20:23]\n"
"v_exp_f32 v175, v175\n"
"v_mfma_f32_16x16x32_bf16 a[228:231], v[124:127], a[252:255], a[228:231]\n"
"s_waitcnt lgkmcnt(3)\n"
"ds_read_b64_tr_b16 v[224:225], v239 offset:49152\n"
"v_cvt_pk_bf16_f32 v103, v170, v171\n"
"v_mfma_f32_16x16x32_bf16 a[216:219], v[120:123], a[244:247], a[216:219]\n"
"s_add_u32 m0, s44, 81920\n"
"ds_read_b64_tr_b16 a[254:255], v235 offset:53248\n"
"v_cvt_pk_bf16_f32 v118, v176, v177\n"
"v_smfmac_f32_16x16x64_bf16 v[196:199], a[240:243], v[96:103], v241\n"
"buffer_load_dwordx4 v240, s[20:23], s56 offen nt lds\n"
"v_mfma_f32_16x16x32_bf16 v[8:11], v[96:99], a[244:247], v[8:11]\n"
"s_waitcnt lgkmcnt(3)\n"
"v_cvt_pk_bf16_f32 v116, v164, v165\n"
"v_cvt_pk_bf16_f32 v119, v178, v179\n"
"v_mfma_f32_16x16x32_bf16 v[8:11], v[100:103], a[248:251], v[8:11]\n"
"v_exp_f32 v174, v174\n"
"v_smfmac_f32_16x16x64_bf16 v[200:203], a[240:243], v[112:119], v241\n"
"ds_read_b64_tr_b16 a[252:253], v235 offset:49152\n"
"v_cvt_pk_bf16_f32 v111, v174, v175\n"
"v_mfma_f32_16x16x32_bf16 v[40:43], v[104:107], a[244:247], v[40:43]\n"
"ds_read_b128 v[212:215], v229 offset:0\n"
"v_add_f32 v196, v196, v197\n"
"v_mfma_f32_16x16x32_bf16 v[40:43], v[108:111], a[248:251], v[40:43]\n"
"ds_read_b128 v[208:211], v228 offset:0\n"
"v_and_b32 v255, 0x3f0, v242\n"
"v_smfmac_f32_16x16x64_bf16 v[198:201], a[240:243], v[104:111], v241\n"
"ds_read_b128 v[220:223], v229 offset:1024\n"
"v_mfma_f32_16x16x32_bf16 v[72:75], v[112:115], a[244:247], v[72:75]\n"
"ds_read_b64_tr_b16 a[244:245], v234 offset:50176\n"
"v_lshrrev_b32 v255, 1, v255\n"
"v_mfma_f32_16x16x32_bf16 v[72:75], v[116:119], a[248:251], v[72:75]\n"
"ds_read_b64_tr_b16 a[248:249], v238 offset:50176\n"
"v_cvt_f32_u32 v254, s43\n"
"v_mfma_f32_16x16x32_bf16 v[202:205], a[240:243], v[120:123], v[202:205]\n"
"s_waitcnt lgkmcnt(6)\n"
"ds_read_b64_tr_b16 a[246:247], v234 offset:54272\n"
"v_add_f32 v198, v198, v199\n"
"v_mfma_f32_16x16x32_bf16 v[76:79], v[116:119], v[224:227], v[76:79]\n"
"s_waitcnt lgkmcnt(6)\n"
"ds_read_b64_tr_b16 a[250:251], v238 offset:54272\n"
"v_add_f32 v200, v200, v201\n"
"v_mfma_f32_16x16x32_bf16 v[76:79], v[112:115], a[252:255], v[76:79]\n"
"v_add_f32 v249, v196, v198\n"
"v_add_f32 v249, v249, v200\n"
"v_mfma_f32_16x16x32_bf16 v[44:47], v[108:111], v[224:227], v[44:47]\n"
"v_add_f32 v254, v254, v202\n"
"v_min3_f32 v254, v254, v196, v198\n"
"v_mfma_f32_16x16x32_bf16 v[44:47], v[104:107], a[252:255], v[44:47]\n"
"v_min_f32 v254, v254, v200\n"
"v_rcp_f32 v254, v254\n"
"v_mfma_f32_16x16x32_bf16 v[12:15], v[100:103], v[224:227], v[12:15]\n"
"s_add_u32 s40, s56, s52\n"
"v_add_f32 v249, v249, v202\n"
"v_fmac_f32 v249, 0x53800000, v254\n"
"v_mfma_f32_16x16x32_bf16 v[12:15], v[96:99], a[252:255], v[12:15]\n"
"buffer_load_dwordx4 v240, s[20:23], s40 offen offset:1024 nt lds\n"
"v_mfma_f32_16x16x32_bf16 a[220:223], v[120:123], a[252:255], a[220:223]\n"
"s_waitcnt lgkmcnt(1)\n"
"v_cmp_nge_f32 vcc, s46, v249\n"
"s_cbranch_vccnz LSLOW_%=\nLBACK0_%=:\ns_mov_b64 vcc, s[64:65]\n"
"v_mfma_f32_16x16x32_bf16 a[232:235], v[120:123], a[244:247], a[232:235]\n"
"v_accvgpr_mov_b32 a5, a53\n"
"v_accvgpr_mov_b32 a8, a56\n"
"v_accvgpr_mov_b32 a10, a58\n"
"v_accvgpr_mov_b32 a34, a82\n"
"v_accvgpr_mov_b32 a38, a86\n"
"v_accvgpr_mov_b32 a46, a94\n"
"v_xor_b32 v207, 0x400, v248\n"
"v_bfe_u32 v206, v242, 8, 2\n"
"v_lshl_add_u32 v206, v206, 4, s43\n"
"v_mfma_f32_16x16x32_bf16 v[24:27], v[96:99], a[244:247], v[24:27]\n"
"s_waitcnt lgkmcnt(0)\n"
"ds_read_b128 a[204:207], v207 offset:6144\n"
"ds_read_b128 v[132:135], v206 offset:1024\n"
"v_mfma_f32_16x16x32_bf16 v[24:27], v[100:103], a[248:251], v[24:27]\n"
"ds_read_b128 v[124:127], v206 offset:0\n"
"v_accvgpr_mov_b32 a13, a61\n"
"v_accvgpr_mov_b32 a14, a62\n"
"v_accvgpr_mov_b32 a16, a64\n"
"v_accvgpr_mov_b32 a19, a67\n"
"v_accvgpr_mov_b32 a27, a75\n"
"v_mfma_f32_16x16x32_bf16 v[56:59], v[104:107], a[244:247], v[56:59]\n"
"ds_read_b128 a[196:199], v207 offset:4096\n"
"v_xor_b32 v207, 64, v206\n"
"v_mfma_f32_16x16x32_bf16 v[56:59], v[108:111], a[248:251], v[56:59]\n"
"ds_read_b128 v[128:131], v207 offset:0\n"
"v_accvgpr_mov_b32 a2, a50\n"
"v_accvgpr_mov_b32 a3, a51\n"
"v_accvgpr_mov_b32 a9, a57\n"
"v_accvgpr_mov_b32 a22, a70\n"
"v_accvgpr_mov_b32 a30, a78\n"
"v_accvgpr_mov_b32 a35, a83\n"
"v_accvgpr_mov_b32 a37, a85\n"
"v_accvgpr_mov_b32 a44, a92\n"
"v_mfma_f32_16x16x32_bf16 v[88:91], v[112:115], a[244:247], v[88:91]\n"
"ds_read_b128 v[136:139], v207 offset:1024\n"
"v_accvgpr_mov_b32 a0, a48\n"
"v_accvgpr_mov_b32 a1, a49\n"
"v_accvgpr_mov_b32 a7, a55\n"
"v_accvgpr_mov_b32 a20, a68\n"
"v_accvgpr_mov_b32 a23, a71\n"
"v_accvgpr_mov_b32 a31, a79\n"
"v_accvgpr_mov_b32 a45, a93\n"
"v_accvgpr_mov_b32 a47, a95\n"
"v_mfma_f32_16x16x32_bf16 v[88:91], v[116:119], a[248:251], v[88:91]\n"
"v_rcp_f32 v244, v198\n"
"v_accvgpr_mov_b32 a4, a52\n"
"v_accvgpr_mov_b32 a6, a54\n"
"v_accvgpr_mov_b32 a11, a59\n"
"v_accvgpr_mov_b32 a15, a63\n"
"v_accvgpr_mov_b32 a26, a74\n"
"v_accvgpr_mov_b32 a29, a77\n"
"v_accvgpr_mov_b32 a40, a88\n"
"s_waitcnt lgkmcnt(3)\n"
"v_mfma_f32_16x16x32_bf16 v[180:183], v[124:127], a[0:3], 0\n"
"ds_read_b128 v[216:219], v228 offset:1024\n"
"v_accvgpr_mov_b32 a12, a60\n"
"v_accvgpr_mov_b32 a17, a65\n"
"v_accvgpr_mov_b32 a18, a66\n"
"v_accvgpr_mov_b32 a21, a69\n"
"v_accvgpr_mov_b32 a25, a73\n"
"v_accvgpr_mov_b32 a42, a90\n"
"s_waitcnt lgkmcnt(2)\n"
"v_mfma_f32_16x16x32_bf16 v[180:183], v[128:131], a[4:7], v[180:183]\n"
"v_accvgpr_mov_b32 a33, a81\n"
"v_mfma_f32_16x16x32_bf16 v[180:183], v[132:135], a[8:11], v[180:183]\n"
"s_waitcnt lgkmcnt(1)\n"
"ds_read_b128 a[200:203], v248 offset:6144\n"
"v_mov_b32_dpp v244, v244 row_ror:12 row_mask:0x2 bank_mask:0xf\n"
"v_mfma_f32_16x16x32_bf16 v[180:183], v[136:139], a[12:15], v[180:183]\n"
"v_rcp_f32 v243, v196\n"
"v_accvgpr_mov_b32 a28, a76\n"
"v_accvgpr_mov_b32 a32, a80\n"
"v_accvgpr_mov_b32 a36, a84\n"
"v_accvgpr_mov_b32 a39, a87\n"
"v_mfma_f32_16x16x32_bf16 v[184:187], v[124:127], a[16:19], 0\n"
"v_accvgpr_mov_b32 a24, a72\n"
"v_mfma_f32_16x16x32_bf16 v[184:187], v[128:131], a[20:23], v[184:187]\n"
"ds_read_b64_tr_b16 v[224:225], v239 offset:50176\n"
"v_mov_b32_dpp v243, v243 row_ror:12 row_mask:0x2 bank_mask:0xf\n"
"v_mfma_f32_16x16x32_bf16 v[184:187], v[132:135], a[24:27], v[184:187]\n"
"s_add_u32 s40, s56, s53\n"
"ds_read_b128 a[192:195], v248 offset:4096\n"
"v_mov_b32 v252, v192\n"
"v_accvgpr_mov_b32 a43, a91\n"
"v_xor_b32 v182, s77, v182\n"
"v_mfma_f32_16x16x32_bf16 v[184:187], v[136:139], a[28:31], v[184:187]\n"
"buffer_load_dwordx4 v240, s[20:23], s40 offen offset:2048 nt lds\n"
"v_rcp_f32 v245, v200\n"
"v_accvgpr_mov_b32 a41, a89\n"
"v_xor_b32 v180, s77, v180\n"
"v_mfma_f32_16x16x32_bf16 v[188:191], v[124:127], a[32:35], 0\n"
"v_mov_b32_dpp v245, v245 row_ror:12 row_mask:0x2 bank_mask:0xf\n"
"v_xor_b32 v183, s77, v183\n"
"v_mfma_f32_16x16x32_bf16 v[188:191], v[128:131], a[36:39], v[188:191]\n"
"v_mov_b32_dpp v243, v243 row_ror:8 row_mask:0x4 bank_mask:0xf\n"
"v_xor_b32 v254, 0x11000, v242\n"
"v_mfma_f32_16x16x32_bf16 v[188:191], v[132:135], a[40:43], v[188:191]\n"
"v_mov_b64 v[196:197], 0\n"
"v_xor_b32 v186, s77, v186\n"
"v_mfma_f32_16x16x32_bf16 v[188:191], v[136:139], a[44:47], v[188:191]\n"
"v_xor_b32 v181, s77, v181\n"
"s_waitcnt lgkmcnt(0)\n"
"v_mfma_f32_16x16x32_bf16 v[192:195], v[124:127], a[192:195], 0\n"
"ds_read_b64_tr_b16 a[246:247], v232 offset:53248\n"
"v_xor_b32 v184, s77, v184\n"
"v_mfma_f32_16x16x32_bf16 v[192:195], v[128:131], a[196:199], v[192:195]\n"
"v_mov_b32_dpp v243, v243 row_ror:4 row_mask:0x8 bank_mask:0xf\n"
"v_mfma_f32_16x16x32_bf16 v[192:195], v[132:135], a[200:203], v[192:195]\n"
"v_xor_b32 v185, s77, v185\n"
"v_xor_b32 v187, s77, v187\n"
"v_mfma_f32_16x16x32_bf16 v[192:195], v[136:139], a[204:207], v[192:195]\n"
"ds_read_b64_tr_b16 a[252:253], v235 offset:50176\n"
"v_mov_b32_dpp v245, v245 row_ror:8 row_mask:0x4 bank_mask:0xf\n"
"v_mfma_f32_16x16x32_bf16 v[152:155], v[208:211], a[0:3], v[180:183]\n"
"v_mfma_f32_16x16x32_bf16 v[152:155], v[212:215], a[4:7], v[152:155]\n"
"v_mfma_f32_16x16x32_bf16 v[152:155], v[216:219], a[8:11], v[152:155]\n"
"s_add_u32 s40, s56, s54\n"
"ds_read_b64_tr_b16 a[250:251], v236 offset:53248\n"
"v_mfma_f32_16x16x32_bf16 v[152:155], v[220:223], a[12:15], v[152:155]\n"
"buffer_load_dwordx4 v240, s[20:23], s40 offen offset:3072 nt lds\n"
"v_mov_b32_dpp v244, v244 row_ror:8 row_mask:0x4 bank_mask:0xf\n"
"v_xor_b32 v188, s77, v188\n"
"v_xor_b32 v189, s77, v189\n"
"v_xor_b32 v190, s77, v190\n"
"v_mfma_f32_16x16x32_bf16 v[156:159], v[216:219], a[24:27], v[184:187]\n"
"s_add_u32 s55, s55, s59\n"
"v_mov_b32_dpp v244, v244 row_ror:4 row_mask:0x8 bank_mask:0xf\n"
"v_xor_b32 v191, s77, v191\n"
"v_mfma_f32_16x16x32_bf16 v[156:159], v[220:223], a[28:31], v[156:159]\n"
"s_add_u32 s41, s55, s57\n"
"v_xor_b32 v194, s77, v194\n"
"v_mfma_f32_16x16x32_bf16 v[156:159], v[208:211], a[16:19], v[156:159]\n"
"s_and_b32 s41, s41, s58\n"
"ds_read_b64_tr_b16 a[244:245], v232 offset:49152\n"
"v_xor_b32 v195, s77, v195\n"
"v_mfma_f32_16x16x32_bf16 v[156:159], v[212:215], a[20:23], v[156:159]\n"
"s_andn2_b32 s56, s55, s58\n"
"ds_read_b64_tr_b16 v[226:227], v239 offset:54272\n"
"v_mov_b32_dpp v245, v245 row_ror:4 row_mask:0x8 bank_mask:0xf\n"
"v_xor_b32 v192, s77, v192\n"
"v_exp_f32 v154, v154\n"
"v_mfma_f32_16x16x32_bf16 v[160:163], v[208:211], a[32:35], v[188:191]\n"
"s_or_b32 s56, s56, s41\n"
"v_mfma_f32_16x16x32_bf16 v[160:163], v[212:215], a[36:39], v[160:163]\n"
"v_sub_u32 v255, v242, v255\n"
"v_xor_b32 v193, s77, v193\n"
"v_mfma_f32_16x16x32_bf16 v[160:163], v[216:219], a[40:43], v[160:163]\n"
"v_mfma_f32_16x16x32_bf16 v[160:163], v[220:223], a[44:47], v[160:163]\n"
"ds_read_b64_tr_b16 a[254:255], v235 offset:54272\n"
"v_mul_f32_dpp v40, v244, v40 row_newbcast:0 row_mask:0xf bank_mask:0xf\n"
"v_mul_f32_dpp v72, v245, v72 row_newbcast:0 row_mask:0xf bank_mask:0xf\n"
"v_exp_f32 v156, v156\n"
"v_exp_f32 v158, v158\n"
"v_mfma_f32_16x16x32_bf16 v[164:167], v[216:219], a[200:203], v[192:195]\n"
"v_mfma_f32_16x16x32_bf16 v[164:167], v[220:223], a[204:207], v[164:167]\n"
"v_mul_f32_dpp v8, v243, v8 row_newbcast:0 row_mask:0xf bank_mask:0xf\n"
"v_mfma_f32_16x16x32_bf16 v[164:167], v[208:211], a[192:195], v[164:167]\n"
"ds_read_b128 v[208:211], v228 offset:4096\n"
"v_mul_f32_dpp v76, v245, v76 row_newbcast:0 row_mask:0xf bank_mask:0xf\n"
"v_mfma_f32_16x16x32_bf16 v[164:167], v[212:215], a[196:199], v[164:167]\n"
"ds_read_b128 v[216:219], v228 offset:5120\n"
"v_mul_f32_dpp v44, v244, v44 row_newbcast:0 row_mask:0xf bank_mask:0xf\n"
"v_exp_f32 v163, v163\n"
"s_branch LTC_%=\n"
"LTH1_%=:\n"
"v_mfma_f32_16x16x32_bf16 a[224:227], v[124:127], a[244:247], a[224:227]\n"
"v_exp_f32 v171, v171\n"
"v_mfma_f32_16x16x32_bf16 v[16:19], v[128:131], a[244:247], v[16:19]\n"
"s_waitcnt vmcnt(8)\n"
"v_exp_f32 v168, v168\n"
"v_mfma_f32_16x16x32_bf16 v[16:19], v[132:135], a[248:251], v[16:19]\n"
"s_barrier\n"
"v_mfma_f32_16x16x32_bf16 v[48:51], v[136:139], a[244:247], v[48:51]\n"
"v_exp_f32 v179, v179\n"
"v_mfma_f32_16x16x32_bf16 v[48:51], v[140:143], a[248:251], v[48:51]\n"
"v_exp_f32 v169, v169\n"
"v_mfma_f32_16x16x32_bf16 v[80:83], v[144:147], a[244:247], v[80:83]\n"
"ds_read_b64_tr_b16 a[244:245], v234 offset:49152\n"
"v_cvt_pk_bf16_f32 v102, v168, v169\n"
"v_mfma_f32_16x16x32_bf16 v[80:83], v[148:151], a[248:251], v[80:83]\n"
"ds_read_b64_tr_b16 a[246:247], v234 offset:53248\n"
"ds_read_b64_tr_b16 a[250:251], v238 offset:53248\n"
"v_mfma_f32_16x16x32_bf16 v[84:87], v[148:151], v[224:227], v[84:87]\n"
"v_cvt_pk_bf16_f32 v108, v160, v161\n"
"v_cvt_pk_bf16_f32 v109, v162, v163\n"
"v_mfma_f32_16x16x32_bf16 v[84:87], v[144:147], a[252:255], v[84:87]\n"
"v_cvt_pk_bf16_f32 v117, v166, v167\n"
"v_exp_f32 v172, v172\n"
"v_exp_f32 v173, v173\n"
"v_exp_f32 v176, v176\n"
"v_exp_f32 v177, v177\n"
"v_exp_f32 v178, v178\n"
"v_mfma_f32_16x16x32_bf16 v[52:55], v[140:143], v[224:227], v[52:55]\n"
"ds_read_b64_tr_b16 a[248:249], v238 offset:49152\n"
"v_cvt_pk_bf16_f32 v100, v156, v157\n"
"v_mfma_f32_16x16x32_bf16 v[52:55], v[136:139], a[252:255], v[52:55]\n"
"v_exp_f32 v170, v170\n"
"v_mfma_f32_16x16x32_bf16 v[20:23], v[132:135], v[224:227], v[20:23]\n"
"ds_read_b64_tr_b16 v[226:227], v239 offset:53248\n"
"v_cvt_pk_bf16_f32 v110, v172, v173\n"
"v_mfma_f32_16x16x32_bf16 v[20:23], v[128:131], a[252:255], v[20:23]\n"
"v_exp_f32 v175, v175\n"
"v_mfma_f32_16x16x32_bf16 a[228:231], v[124:127], a[252:255], a[228:231]\n"
"s_waitcnt lgkmcnt(3)\n"
"ds_read_b64_tr_b16 v[224:225], v239 offset:49152\n"
"v_cvt_pk_bf16_f32 v103, v170, v171\n"
"v_mfma_f32_16x16x32_bf16 a[216:219], v[120:123], a[244:247], a[216:219]\n"
"s_add_u32 m0, s44, 81920\n"
"ds_read_b64_tr_b16 a[254:255], v235 offset:53248\n"
"v_cvt_pk_bf16_f32 v118, v176, v177\n"
"v_smfmac_f32_16x16x64_bf16 v[196:199], a[240:243], v[96:103], v241\n"
"buffer_load_dwordx4 v240, s[20:23], s56 offen nt lds\n"
"v_mfma_f32_16x16x32_bf16 v[8:11], v[96:99], a[244:247], v[8:11]\n"
"s_waitcnt lgkmcnt(3)\n"
"v_cvt_pk_bf16_f32 v116, v164, v165\n"
"v_cvt_pk_bf16_f32 v119, v178, v179\n"
"v_mfma_f32_16x16x32_bf16 v[8:11], v[100:103], a[248:251], v[8:11]\n"
"v_exp_f32 v174, v174\n"
"v_smfmac_f32_16x16x64_bf16 v[200:203], a[240:243], v[112:119], v241\n"
"ds_read_b64_tr_b16 a[252:253], v235 offset:49152\n"
"v_cvt_pk_bf16_f32 v111, v174, v175\n"
"v_mfma_f32_16x16x32_bf16 v[40:43], v[104:107], a[244:247], v[40:43]\n"
"ds_read_b128 v[212:215], v229 offset:0\n"
"v_add_f32 v196, v196, v197\n"
"v_mfma_f32_16x16x32_bf16 v[40:43], v[108:111], a[248:251], v[40:43]\n"
"ds_read_b128 v[208:211], v228 offset:0\n"
"v_and_b32 v255, 0x3f0, v242\n"
"v_smfmac_f32_16x16x64_bf16 v[198:201], a[240:243], v[104:111], v241\n"
"ds_read_b128 v[220:223], v229 offset:1024\n"
"v_mfma_f32_16x16x32_bf16 v[72:75], v[112:115], a[244:247], v[72:75]\n"
"ds_read_b64_tr_b16 a[244:245], v234 offset:50176\n"
"v_lshrrev_b32 v255, 1, v255\n"
"v_mfma_f32_16x16x32_bf16 v[72:75], v[116:119], a[248:251], v[72:75]\n"
"ds_read_b64_tr_b16 a[248:249], v238 offset:50176\n"
"v_cvt_f32_u32 v254, s43\n"
"v_mfma_f32_16x16x32_bf16 v[202:205], a[240:243], v[120:123], v[202:205]\n"
"s_waitcnt lgkmcnt(6)\n"
"ds_read_b64_tr_b16 a[246:247], v234 offset:54272\n"
"v_add_f32 v198, v198, v199\n"
"v_mfma_f32_16x16x32_bf16 v[76:79], v[116:119], v[224:227], v[76:79]\n"
"s_waitcnt lgkmcnt(6)\n"
"ds_read_b64_tr_b16 a[250:251], v238 offset:54272\n"
"v_add_f32 v200, v200, v201\n"
"v_mfma_f32_16x16x32_bf16 v[76:79], v[112:115], a[252:255], v[76:79]\n"
"v_add_f32 v249, v196, v198\n"
"v_add_f32 v249, v249, v200\n"
"v_mfma_f32_16x16x32_bf16 v[44:47], v[108:111], v[224:227], v[44:47]\n"
"v_add_f32 v254, v254, v202\n"
"v_min3_f32 v254, v254, v196, v198\n"
"v_mfma_f32_16x16x32_bf16 v[44:47], v[104:107], a[252:255], v[44:47]\n"
"v_min_f32 v254, v254, v200\n"
"v_rcp_f32 v254, v254\n"
"v_mfma_f32_16x16x32_bf16 v[12:15], v[100:103], v[224:227], v[12:15]\n"
"s_add_u32 s40, s56, s52\n"
"v_add_f32 v249, v249, v202\n"
"v_fmac_f32 v249, 0x53800000, v254\n"
"v_mfma_f32_16x16x32_bf16 v[12:15], v[96:99], a[252:255], v[12:15]\n"
"buffer_load_dwordx4 v240, s[20:23], s40 offen offset:1024 nt lds\n"
"v_mfma_f32_16x16x32_bf16 a[220:223], v[120:123], a[252:255], a[220:223]\n"
"s_waitcnt lgkmcnt(1)\n"
"v_cmp_nge_f32 vcc, s46, v249\n"
"s_cbranch_vccnz LSLOW_%=\nLBACK1_%=:\ns_mov_b64 vcc, s[64:65]\n"
"v_mfma_f32_16x16x32_bf16 a[232:235], v[120:123], a[244:247], a[232:235]\n"
"v_accvgpr_mov_b32 a5, a101\n"
"v_accvgpr_mov_b32 a8, a104\n"
"v_accvgpr_mov_b32 a10, a106\n"
"v_accvgpr_mov_b32 a34, a130\n"
"v_accvgpr_mov_b32 a38, a134\n"
"v_accvgpr_mov_b32 a46, a142\n"
"v_xor_b32 v207, 0x400, v248\n"
"v_bfe_u32 v206, v242, 8, 2\n"
"v_lshl_add_u32 v206, v206, 4, s43\n"
"v_mfma_f32_16x16x32_bf16 v[24:27], v[96:99], a[244:247], v[24:27]\n"
"s_waitcnt lgkmcnt(0)\n"
"ds_read_b128 a[204:207], v207 offset:6144\n"
"ds_read_b128 v[132:135], v206 offset:1024\n"
"v_mfma_f32_16x16x32_bf16 v[24:27], v[100:103], a[248:251], v[24:27]\n"
"ds_read_b128 v[124:127], v206 offset:0\n"
"v_accvgpr_mov_b32 a13, a109\n"
"v_accvgpr_mov_b32 a14, a110\n"
"v_accvgpr_mov_b32 a16, a112\n"
"v_accvgpr_mov_b32 a19, a115\n"
"v_accvgpr_mov_b32 a27, a123\n"
"v_mfma_f32_16x16x32_bf16 v[56:59], v[104:107], a[244:247], v[56:59]\n"
"ds_read_b128 a[196:199], v207 offset:4096\n"
"v_xor_b32 v207, 64, v206\n"
"v_mfma_f32_16x16x32_bf16 v[56:59], v[108:111], a[248:251], v[56:59]\n"
"ds_read_b128 v[128:131], v207 offset:0\n"
"v_accvgpr_mov_b32 a2, a98\n"
"v_accvgpr_mov_b32 a3, a99\n"
"v_accvgpr_mov_b32 a9, a105\n"
"v_accvgpr_mov_b32 a22, a118\n"
"v_accvgpr_mov_b32 a30, a126\n"
"v_accvgpr_mov_b32 a35, a131\n"
"v_accvgpr_mov_b32 a37, a133\n"
"v_accvgpr_mov_b32 a44, a140\n"
"v_mfma_f32_16x16x32_bf16 v[88:91], v[112:115], a[244:247], v[88:91]\n"
"ds_read_b128 v[136:139], v207 offset:1024\n"
"v_accvgpr_mov_b32 a0, a96\n"
"v_accvgpr_mov_b32 a1, a97\n"
"v_accvgpr_mov_b32 a7, a103\n"
"v_accvgpr_mov_b32 a20, a116\n"
"v_accvgpr_mov_b32 a23, a119\n"
"v_accvgpr_mov_b32 a31, a127\n"
"v_accvgpr_mov_b32 a45, a141\n"
"v_accvgpr_mov_b32 a47, a143\n"
"v_accvgpr_mov_b32 a116, a68\n"
"v_mfma_f32_16x16x32_bf16 v[88:91], v[116:119], a[248:251], v[88:91]\n"
"s_waitcnt lgkmcnt(3)\n"
"v_rcp_f32 v244, v198\n"
"v_accvgpr_mov_b32 a4, a100\n"
"v_accvgpr_mov_b32 a6, a102\n"
"v_accvgpr_mov_b32 a11, a107\n"
"v_accvgpr_mov_b32 a15, a111\n"
"v_accvgpr_mov_b32 a26, a122\n"
"v_accvgpr_mov_b32 a29, a125\n"
"v_accvgpr_mov_b32 a40, a136\n"
"v_accvgpr_mov_b32 a130, a82\n"
"v_mfma_f32_16x16x32_bf16 v[180:183], v[124:127], a[0:3], 0\n"
"s_waitcnt lgkmcnt(1)\n"
"ds_read_b128 v[216:219], v228 offset:1024\n"
"v_accvgpr_mov_b32 a12, a108\n"
"v_accvgpr_mov_b32 a17, a113\n"
"v_accvgpr_mov_b32 a18, a114\n"
"v_accvgpr_mov_b32 a21, a117\n"
"v_accvgpr_mov_b32 a25, a121\n"
"v_accvgpr_mov_b32 a42, a138\n"
"v_accvgpr_mov_b32 a115, a67\n"
"v_accvgpr_mov_b32 a122, a74\n"
"v_mfma_f32_16x16x32_bf16 v[180:183], v[128:131], a[4:7], v[180:183]\n"
"v_accvgpr_mov_b32 a33, a129\n"
"v_accvgpr_mov_b32 a125, a77\n"
"v_mfma_f32_16x16x32_bf16 v[180:183], v[132:135], a[8:11], v[180:183]\n"
"s_waitcnt lgkmcnt(1)\n"
"ds_read_b128 a[200:203], v248 offset:6144\n"
"v_mov_b32_dpp v244, v244 row_ror:12 row_mask:0x2 bank_mask:0xf\n"
"v_mfma_f32_16x16x32_bf16 v[180:183], v[136:139], a[12:15], v[180:183]\n"
"v_rcp_f32 v243, v196\n"
"v_accvgpr_mov_b32 a28, a124\n"
"v_accvgpr_mov_b32 a32, a128\n"
"v_accvgpr_mov_b32 a36, a132\n"
"v_accvgpr_mov_b32 a39, a135\n"
"v_accvgpr_mov_b32 a119, a71\n"
"v_accvgpr_mov_b32 a110, a62\n"
"v_accvgpr_mov_b32 a121, a73\n"
"v_accvgpr_mov_b32 a136, a88\n"
"v_mfma_f32_16x16x32_bf16 v[184:187], v[124:127], a[16:19], 0\n"
"v_accvgpr_mov_b32 a24, a120\n"
"v_accvgpr_mov_b32 a126, a78\n"
"v_mfma_f32_16x16x32_bf16 v[184:187], v[128:131], a[20:23], v[184:187]\n"
"ds_read_b64_tr_b16 v[224:225], v239 offset:50176\n"
"v_mov_b32_dpp v243, v243 row_ror:12 row_mask:0x2 bank_mask:0xf\n"
"v_mfma_f32_16x16x32_bf16 v[184:187], v[132:135], a[24:27], v[184:187]\n"
"s_add_u32 s40, s56, s53\n"
"ds_read_b128 a[192:195], v248 offset:4096\n"
"v_mov_b32 v252, v192\n"
"v_accvgpr_mov_b32 a43, a139\n"
"v_accvgpr_mov_b32 a133, a85\n"
"v_accvgpr_mov_b32 a111, a63\n"
"v_accvgpr_mov_b32 a99, a51\n"
"v_accvgpr_mov_b32 a131, a83\n"
"v_accvgpr_mov_b32 a106, a58\n"
"v_xor_b32 v182, s77, v182\n"
"v_mfma_f32_16x16x32_bf16 v[184:187], v[136:139], a[28:31], v[184:187]\n"
"buffer_load_dwordx4 v240, s[20:23], s40 offen offset:2048 nt lds\n"
"v_rcp_f32 v245, v200\n"
"v_accvgpr_mov_b32 a41, a137\n"
"v_accvgpr_mov_b32 a134, a86\n"
"v_accvgpr_mov_b32 a142, a94\n"
"v_accvgpr_mov_b32 a112, a64\n"
"v_accvgpr_mov_b32 a113, a65\n"
"v_xor_b32 v180, s77, v180\n"
"v_mfma_f32_16x16x32_bf16 v[188:191], v[124:127], a[32:35], 0\n"
"v_mov_b32_dpp v245, v245 row_ror:12 row_mask:0x2 bank_mask:0xf\n"
"v_xor_b32 v183, s77, v183\n"
"v_mfma_f32_16x16x32_bf16 v[188:191], v[128:131], a[36:39], v[188:191]\n"
"v_mov_b32_dpp v243, v243 row_ror:8 row_mask:0x4 bank_mask:0xf\n"
"v_xor_b32 v254, 0x11000, v242\n"
"v_mfma_f32_16x16x32_bf16 v[188:191], v[132:135], a[40:43], v[188:191]\n"
"v_mov_b64 v[196:197], 0\n"
"v_xor_b32 v186, s77, v186\n"
"v_mfma_f32_16x16x32_bf16 v[188:191], v[136:139], a[44:47], v[188:191]\n"
"s_waitcnt lgkmcnt(0)\n"
"v_accvgpr_mov_b32 a141, a93\n"
"v_xor_b32 v181, s77, v181\n"
"v_mfma_f32_16x16x32_bf16 v[192:195], v[124:127], a[192:195], 0\n"
"ds_read_b64_tr_b16 a[246:247], v232 offset:53248\n"
"v_xor_b32 v184, s77, v184\n"
"v_mfma_f32_16x16x32_bf16 v[192:195], v[128:131], a[196:199], v[192:195]\n"
"v_mov_b32_dpp v243, v243 row_ror:4 row_mask:0x8 bank_mask:0xf\n"
"v_accvgpr_mov_b32 a124, a76\n"
"v_mfma_f32_16x16x32_bf16 v[192:195], v[132:135], a[200:203], v[192:195]\n"
"v_xor_b32 v185, s77, v185\n"
"v_xor_b32 v187, s77, v187\n"
"v_mfma_f32_16x16x32_bf16 v[192:195], v[136:139], a[204:207], v[192:195]\n"
"ds_read_b64_tr_b16 a[252:253], v235 offset:50176\n"
"v_mov_b32_dpp v245, v245 row_ror:8 row_mask:0x4 bank_mask:0xf\n"
"v_mfma_f32_16x16x32_bf16 v[152:155], v[208:211], a[0:3], v[180:183]\n"
"v_accvgpr_mov_b32 a135, a87\n"
"v_accvgpr_mov_b32 a127, a79\n"
"v_mfma_f32_16x16x32_bf16 v[152:155], v[212:215], a[4:7], v[152:155]\n"
"v_accvgpr_mov_b32 a100, a52\n"
"v_accvgpr_mov_b32 a102, a54\n"
"v_mfma_f32_16x16x32_bf16 v[152:155], v[216:219], a[8:11], v[152:155]\n"
"s_add_u32 s40, s56, s54\n"
"ds_read_b64_tr_b16 a[250:251], v236 offset:53248\n"
"v_accvgpr_mov_b32 a97, a49\n"
"v_mfma_f32_16x16x32_bf16 v[152:155], v[220:223], a[12:15], v[152:155]\n"
"buffer_load_dwordx4 v240, s[20:23], s40 offen offset:3072 nt lds\n"
"v_mov_b32_dpp v244, v244 row_ror:8 row_mask:0x4 bank_mask:0xf\n"
"v_accvgpr_mov_b32 a108, a60\n"
"v_accvgpr_mov_b32 a120, a72\n"
"v_accvgpr_mov_b32 a137, a89\n"
"v_accvgpr_mov_b32 a138, a90\n"
"v_xor_b32 v188, s77, v188\n"
"v_xor_b32 v189, s77, v189\n"
"v_xor_b32 v190, s77, v190\n"
"v_mfma_f32_16x16x32_bf16 v[156:159], v[216:219], a[24:27], v[184:187]\n"
"s_add_u32 s55, s55, s59\n"
"v_mov_b32_dpp v244, v244 row_ror:4 row_mask:0x8 bank_mask:0xf\n"
"v_xor_b32 v191, s77, v191\n"
"v_mfma_f32_16x16x32_bf16 v[156:159], v[220:223], a[28:31], v[156:159]\n"
"s_add_u32 s41, s55, s57\n"
"v_accvgpr_mov_b32 a132, a84\n"
"v_xor_b32 v194, s77, v194\n"
"v_mfma_f32_16x16x32_bf16 v[156:159], v[208:211], a[16:19], v[156:159]\n"
"s_and_b32 s41, s41, s58\n"
"ds_read_b64_tr_b16 a[244:245], v232 offset:49152\n"
"v_xor_b32 v195, s77, v195\n"
"v_mfma_f32_16x16x32_bf16 v[156:159], v[212:215], a[20:23], v[156:159]\n"
"s_andn2_b32 s56, s55, s58\n"
"ds_read_b64_tr_b16 v[226:227], v239 offset:54272\n"
"v_mov_b32_dpp v245, v245 row_ror:4 row_mask:0x8 bank_mask:0xf\n"
"v_accvgpr_mov_b32 a98, a50\n"
"v_accvgpr_mov_b32 a114, a66\n"
"v_accvgpr_mov_b32 a107, a59\n"
"v_accvgpr_mov_b32 a139, a91\n"
"v_xor_b32 v192, s77, v192\n"
"v_exp_f32 v154, v154\n"
"v_mfma_f32_16x16x32_bf16 v[160:163], v[208:211], a[32:35], v[188:191]\n"
"s_or_b32 s56, s56, s41\n"
"v_accvgpr_mov_b32 a109, a61\n"
"v_accvgpr_mov_b32 a104, a56\n"
"v_mfma_f32_16x16x32_bf16 v[160:163], v[212:215], a[36:39], v[160:163]\n"
"v_sub_u32 v255, v242, v255\n"
"v_xor_b32 v193, s77, v193\n"
"v_mfma_f32_16x16x32_bf16 v[160:163], v[216:219], a[40:43], v[160:163]\n"
"v_accvgpr_mov_b32 a103, a55\n"
"v_accvgpr_mov_b32 a96, a48\n"
"v_mfma_f32_16x16x32_bf16 v[160:163], v[220:223], a[44:47], v[160:163]\n"
"ds_read_b64_tr_b16 a[254:255], v235 offset:54272\n"
"v_accvgpr_mov_b32 a117, a69\n"
"v_accvgpr_mov_b32 a129, a81\n"
"v_accvgpr_mov_b32 a105, a57\n"
"v_mul_f32_dpp v40, v244, v40 row_newbcast:0 row_mask:0xf bank_mask:0xf\n"
"v_mul_f32_dpp v72, v245, v72 row_newbcast:0 row_mask:0xf bank_mask:0xf\n"
"v_exp_f32 v156, v156\n"
"v_exp_f32 v158, v158\n"
"v_mfma_f32_16x16x32_bf16 v[164:167], v[216:219], a[200:203], v[192:195]\n"
"v_accvgpr_mov_b32 a118, a70\n"
"v_accvgpr_mov_b32 a140, a92\n"
"v_mfma_f32_16x16x32_bf16 v[164:167], v[220:223], a[204:207], v[164:167]\n"
"v_accvgpr_mov_b32 a143, a95\n"
"v_mul_f32_dpp v8, v243, v8 row_newbcast:0 row_mask:0xf bank_mask:0xf\n"
"v_mfma_f32_16x16x32_bf16 v[164:167], v[208:211], a[192:195], v[164:167]\n"
"ds_read_b128 v[208:211], v228 offset:4096\n"
"v_mul_f32_dpp v76, v245, v76 row_newbcast:0 row_mask:0xf bank_mask:0xf\n"
"v_mfma_f32_16x16x32_bf16 v[164:167], v[212:215], a[196:199], v[164:167]\n"
"s_waitcnt lgkmcnt(2)\n"
"ds_read_b128 v[216:219], v228 offset:5120\n"
"v_accvgpr_mov_b32 a101, a53\n"
"v_accvgpr_mov_b32 a128, a80\n"
"v_accvgpr_mov_b32 a123, a75\n"
"v_mul_f32_dpp v44, v244, v44 row_newbcast:0 row_mask:0xf bank_mask:0xf\n"
"v_exp_f32 v163, v163\n"
"s_branch LTC_%=\n"
"LTH2_%=:\n"
"v_mfma_f32_16x16x32_bf16 a[224:227], v[124:127], a[244:247], a[224:227]\n"
"v_exp_f32 v171, v171\n"
"v_mfma_f32_16x16x32_bf16 v[16:19], v[128:131], a[244:247], v[16:19]\n"
"s_waitcnt vmcnt(8)\n"
"v_exp_f32 v168, v168\n"
"v_mfma_f32_16x16x32_bf16 v[16:19], v[132:135], a[248:251], v[16:19]\n"
"s_barrier\n"
"v_mfma_f32_16x16x32_bf16 v[48:51], v[136:139], a[244:247], v[48:51]\n"
"v_exp_f32 v179, v179\n"
"v_mfma_f32_16x16x32_bf16 v[48:51], v[140:143], a[248:251], v[48:51]\n"
"v_exp_f32 v169, v169\n"
"v_mfma_f32_16x16x32_bf16 v[80:83], v[144:147], a[244:247], v[80:83]\n"
"ds_read_b64_tr_b16 a[244:245], v234 offset:49152\n"
"v_cvt_pk_bf16_f32 v102, v168, v169\n"
"v_mfma_f32_16x16x32_bf16 v[80:83], v[148:151], a[248:251], v[80:83]\n"
"ds_read_b64_tr_b16 a[246:247], v234 offset:53248\n"
"ds_read_b64_tr_b16 a[250:251], v238 offset:53248\n"
"v_mfma_f32_16x16x32_bf16 v[84:87], v[148:151], v[224:227], v[84:87]\n"
"v_cvt_pk_bf16_f32 v108, v160, v161\n"
"v_cvt_pk_bf16_f32 v109, v162, v163\n"
"v_mfma_f32_16x16x32_bf16 v[84:87], v[144:147], a[252:255], v[84:87]\n"
"v_cvt_pk_bf16_f32 v117, v166, v167\n"
"v_exp_f32 v172, v172\n"
"v_exp_f32 v173, v173\n"
"v_exp_f32 v176, v176\n"
"v_exp_f32 v177, v177\n"
"v_exp_f32 v178, v178\n"
"v_mfma_f32_16x16x32_bf16 v[52:55], v[140:143], v[224:227], v[52:55]\n"
"ds_read_b64_tr_b16 a[248:249], v238 offset:49152\n"
"v_cvt_pk_bf16_f32 v100, v156, v157\n"
"v_mfma_f32_16x16x32_bf16 v[52:55], v[136:139], a[252:255], v[52:55]\n"
"v_exp_f32 v170, v170\n"
"v_mfma_f32_16x16x32_bf16 v[20:23], v[132:135], v[224:227], v[20:23]\n"
"ds_read_b64_tr_b16 v[226:227], v239 offset:53248\n"
"v_cvt_pk_bf16_f32 v110, v172, v173\n"
"v_mfma_f32_16x16x32_bf16 v[20:23], v[128:131], a[252:255], v[20:23]\n"
"v_exp_f32 v175, v175\n"
"v_mfma_f32_16x16x32_bf16 a[228:231], v[124:127], a[252:255], a[228:231]\n"
"s_waitcnt lgkmcnt(3)\n"
"ds_read_b64_tr_b16 v[224:225], v239 offset:49152\n"
"v_cvt_pk_bf16_f32 v103, v170, v171\n"
"v_mfma_f32_16x16x32_bf16 a[216:219], v[120:123], a[244:247], a[216:219]\n"
"s_add_u32 m0, s44, 81920\n"
"ds_read_b64_tr_b16 a[254:255], v235 offset:53248\n"
"v_cvt_pk_bf16_f32 v118, v176, v177\n"
"v_smfmac_f32_16x16x64_bf16 v[196:199], a[240:243], v[96:103], v241\n"
"buffer_load_dwordx4 v240, s[20:23], s56 offen nt lds\n"
"v_mfma_f32_16x16x32_bf16 v[8:11], v[96:99], a[244:247], v[8:11]\n"
"s_waitcnt lgkmcnt(3)\n"
"v_cvt_pk_bf16_f32 v116, v164, v165\n"
"v_cvt_pk_bf16_f32 v119, v178, v179\n"
"v_mfma_f32_16x16x32_bf16 v[8:11], v[100:103], a[248:251], v[8:11]\n"
"v_exp_f32 v174, v174\n"
"v_smfmac_f32_16x16x64_bf16 v[200:203], a[240:243], v[112:119], v241\n"
"ds_read_b64_tr_b16 a[252:253], v235 offset:49152\n"
"v_cvt_pk_bf16_f32 v111, v174, v175\n"
"v_mfma_f32_16x16x32_bf16 v[40:43], v[104:107], a[244:247], v[40:43]\n"
"ds_read_b128 v[212:215], v229 offset:0\n"
"v_add_f32 v196, v196, v197\n"
"v_mfma_f32_16x16x32_bf16 v[40:43], v[108:111], a[248:251], v[40:43]\n"
"ds_read_b128 v[208:211], v228 offset:0\n"
"v_and_b32 v255, 0x3f0, v242\n"
"v_smfmac_f32_16x16x64_bf16 v[198:201], a[240:243], v[104:111], v241\n"
"ds_read_b128 v[220:223], v229 offset:1024\n"
"v_mfma_f32_16x16x32_bf16 v[72:75], v[112:115], a[244:247], v[72:75]\n"
"ds_read_b64_tr_b16 a[244:245], v234 offset:50176\n"
"v_lshrrev_b32 v255, 1, v255\n"
"v_mfma_f32_16x16x32_bf16 v[72:75], v[116:119], a[248:251], v[72:75]\n"
"ds_read_b64_tr_b16 a[248:249], v238 offset:50176\n"
"v_cvt_f32_u32 v254, s43\n"
"v_mfma_f32_16x16x32_bf16 v[202:205], a[240:243], v[120:123], v[202:205]\n"
"s_waitcnt lgkmcnt(6)\n"
"ds_read_b64_tr_b16 a[246:247], v234 offset:54272\n"
"v_add_f32 v198, v198, v199\n"
"v_mfma_f32_16x16x32_bf16 v[76:79], v[116:119], v[224:227], v[76:79]\n"
"s_waitcnt lgkmcnt(6)\n"
"ds_read_b64_tr_b16 a[250:251], v238 offset:54272\n"
"v_add_f32 v200, v200, v201\n"
"v_mfma_f32_16x16x32_bf16 v[76:79], v[112:115], a[252:255], v[76:79]\n"
"v_add_f32 v249, v196, v198\n"
"v_add_f32 v249, v249, v200\n"
"v_mfma_f32_16x16x32_bf16 v[44:47], v[108:111], v[224:227], v[44:47]\n"
"v_add_f32 v254, v254, v202\n"
"v_min3_f32 v254, v254, v196, v198\n"
"v_mfma_f32_16x16x32_bf16 v[44:47], v[104:107], a[252:255], v[44:47]\n"
"v_min_f32 v254, v254, v200\n"
"v_rcp_f32 v254, v254\n"
"v_mfma_f32_16x16x32_bf16 v[12:15], v[100:103], v[224:227], v[12:15]\n"
"s_add_u32 s40, s56, s52\n"
"v_add_f32 v249, v249, v202\n"
"v_fmac_f32 v249, 0x53800000, v254\n"
"v_mfma_f32_16x16x32_bf16 v[12:15], v[96:99], a[252:255], v[12:15]\n"
"buffer_load_dwordx4 v240, s[20:23], s40 offen offset:1024 nt lds\n"
"v_mfma_f32_16x16x32_bf16 a[220:223], v[120:123], a[252:255], a[220:223]\n"
"s_waitcnt lgkmcnt(1)\n"
"v_cmp_nge_f32 vcc, s46, v249\n"
"s_cbranch_vccnz LSLOW_%=\nLBACK2_%=:\ns_mov_b64 vcc, s[64:65]\n"
"v_mfma_f32_16x16x32_bf16 a[232:235], v[120:123], a[244:247], a[232:235]\n"
"v_accvgpr_mov_b32 a5, a149\n"
"v_accvgpr_mov_b32 a8, a152\n"
"v_accvgpr_mov_b32 a10, a154\n"
"v_accvgpr_mov_b32 a34, a178\n"
"v_accvgpr_mov_b32 a38, a182\n"
"v_accvgpr_mov_b32 a46, a190\n"
"v_xor_b32 v207, 0x400, v248\n"
"v_bfe_u32 v206, v242, 8, 2\n"
"v_lshl_add_u32 v206, v206, 4, s43\n"
"v_mfma_f32_16x16x32_bf16 v[24:27], v[96:99], a[244:247], v[24:27]\n"
"s_waitcnt lgkmcnt(0)\n"
"ds_read_b128 a[204:207], v207 offset:6144\n"
"ds_read_b128 v[132:135], v206 offset:1024\n"
"v_mfma_f32_16x16x32_bf16 v[24:27], v[100:103], a[248:251], v[24:27]\n"
"ds_read_b128 v[124:127], v206 offset:0\n"
"v_accvgpr_mov_b32 a13, a157\n"
"v_accvgpr_mov_b32 a14, a158\n"
"v_accvgpr_mov_b32 a16, a160\n"
"v_accvgpr_mov_b32 a19, a163\n"
"v_accvgpr_mov_b32 a27, a171\n"
"v_mfma_f32_16x16x32_bf16 v[56:59], v[104:107], a[244:247], v[56:59]\n"
"ds_read_b128 a[196:199], v207 offset:4096\n"
"v_xor_b32 v207, 64, v206\n"
"v_mfma_f32_16x16x32_bf16 v[56:59], v[108:111], a[248:251], v[56:59]\n"
"ds_read_b128 v[128:131], v207 offset:0\n"
"v_accvgpr_mov_b32 a2, a146\n"
"v_accvgpr_mov_b32 a3, a147\n"
"v_accvgpr_mov_b32 a9, a153\n"
"v_accvgpr_mov_b32 a22, a166\n"
"v_accvgpr_mov_b32 a30, a174\n"
"v_accvgpr_mov_b32 a35, a179\n"
"v_accvgpr_mov_b32 a37, a181\n"
"v_accvgpr_mov_b32 a44, a188\n"
"v_mfma_f32_16x16x32_bf16 v[88:91], v[112:115], a[244:247], v[88:91]\n"
"ds_read_b128 v[136:139], v207 offset:1024\n"
"v_accvgpr_mov_b32 a0, a144\n"
"v_accvgpr_mov_b32 a1, a145\n"
"v_accvgpr_mov_b32 a7, a151\n"
"v_accvgpr_mov_b32 a20, a164\n"
"v_accvgpr_mov_b32 a23, a167\n"
"v_accvgpr_mov_b32 a31, a175\n"
"v_accvgpr_mov_b32 a45, a189\n"
"v_accvgpr_mov_b32 a47, a191\n"
"v_accvgpr_mov_b32 a164, a68\n"
"v_mfma_f32_16x16x32_bf16 v[88:91], v[116:119], a[248:251], v[88:91]\n"
"s_waitcnt lgkmcnt(3)\n"
"v_rcp_f32 v244, v198\n"
"v_accvgpr_mov_b32 a4, a148\n"
"v_accvgpr_mov_b32 a6, a150\n"
"v_accvgpr_mov_b32 a11, a155\n"
"v_accvgpr_mov_b32 a15, a159\n"
"v_accvgpr_mov_b32 a26, a170\n"
"v_accvgpr_mov_b32 a29, a173\n"
"v_accvgpr_mov_b32 a40, a184\n"
"v_accvgpr_mov_b32 a178, a82\n"
"v_mfma_f32_16x16x32_bf16 v[180:183], v[124:127], a[0:3], 0\n"
"s_waitcnt lgkmcnt(1)\n"
"ds_read_b128 v[216:219], v228 offset:1024\n"
"v_accvgpr_mov_b32 a12, a156\n"
"v_accvgpr_mov_b32 a17, a161\n"
"v_accvgpr_mov_b32 a18, a162\n"
"v_accvgpr_mov_b32 a21, a165\n"
"v_accvgpr_mov_b32 a25, a169\n"
"v_accvgpr_mov_b32 a42, a186\n"
"v_accvgpr_mov_b32 a163, a67\n"
"v_accvgpr_mov_b32 a170, a74\n"
"v_mfma_f32_16x16x32_bf16 v[180:183], v[128:131], a[4:7], v[180:183]\n"
"v_accvgpr_mov_b32 a33, a177\n"
"v_accvgpr_mov_b32 a173, a77\n"
"v_mfma_f32_16x16x32_bf16 v[180:183], v[132:135], a[8:11], v[180:183]\n"
"s_waitcnt lgkmcnt(1)\n"
"ds_read_b128 a[200:203], v248 offset:6144\n"
"v_mov_b32_dpp v244, v244 row_ror:12 row_mask:0x2 bank_mask:0xf\n"
"v_mfma_f32_16x16x32_bf16 v[180:183], v[136:139], a[12:15], v[180:183]\n"
"v_rcp_f32 v243, v196\n"
"v_accvgpr_mov_b32 a28, a172\n"
"v_accvgpr_mov_b32 a32, a176\n"
"v_accvgpr_mov_b32 a36, a180\n"
"v_accvgpr_mov_b32 a39, a183\n"
"v_accvgpr_mov_b32 a167, a71\n"
"v_accvgpr_mov_b32 a158, a62\n"
"v_accvgpr_mov_b32 a169, a73\n"
"v_accvgpr_mov_b32 a184, a88\n"
"v_mfma_f32_16x16x32_bf16 v[184:187], v[124:127], a[16:19], 0\n"
"v_accvgpr_mov_b32 a24, a168\n"
"v_accvgpr_mov_b32 a174, a78\n"
"v_mfma_f32_16x16x32_bf16 v[184:187], v[128:131], a[20:23], v[184:187]\n"
"ds_read_b64_tr_b16 v[224:225], v239 offset:50176\n"
"v_mov_b32_dpp v243, v243 row_ror:12 row_mask:0x2 bank_mask:0xf\n"
"v_mfma_f32_16x16x32_bf16 v[184:187], v[132:135], a[24:27], v[184:187]\n"
"s_add_u32 s40, s56, s53\n"
"ds_read_b128 a[192:195], v248 offset:4096\n"
"v_mov_b32 v252, v192\n"
"v_accvgpr_mov_b32 a43, a187\n"
"v_accvgpr_mov_b32 a181, a85\n"
"v_accvgpr_mov_b32 a159, a63\n"
"v_accvgpr_mov_b32 a147, a51\n"
"v_accvgpr_mov_b32 a179, a83\n"
"v_accvgpr_mov_b32 a154, a58\n"
"v_xor_b32 v182, s77, v182\n"
"v_mfma_f32_16x16x32_bf16 v[184:187], v[136:139], a[28:31], v[184:187]\n"
"buffer_load_dwordx4 v240, s[20:23], s40 offen offset:2048 nt lds\n"
"v_rcp_f32 v245, v200\n"
"v_accvgpr_mov_b32 a41, a185\n"
"v_accvgpr_mov_b32 a182, a86\n"
"v_accvgpr_mov_b32 a190, a94\n"
"v_accvgpr_mov_b32 a160, a64\n"
"v_accvgpr_mov_b32 a161, a65\n"
"v_xor_b32 v180, s77, v180\n"
"v_mfma_f32_16x16x32_bf16 v[188:191], v[124:127], a[32:35], 0\n"
"v_mov_b32_dpp v245, v245 row_ror:12 row_mask:0x2 bank_mask:0xf\n"
"v_xor_b32 v183, s77, v183\n"
"v_mfma_f32_16x16x32_bf16 v[188:191], v[128:131], a[36:39], v[188:191]\n"
"v_mov_b32_dpp v243, v243 row_ror:8 row_mask:0x4 bank_mask:0xf\n"
"v_xor_b32 v254, 0x11000, v242\n"
"v_mfma_f32_16x16x32_bf16 v[188:191], v[132:135], a[40:43], v[188:191]\n"
"v_mov_b64 v[196:197], 0\n"
"v_xor_b32 v186, s77, v186\n"
"v_mfma_f32_16x16x32_bf16 v[188:191], v[136:139], a[44:47], v[188:191]\n"
"s_waitcnt lgkmcnt(0)\n"
"v_accvgpr_mov_b32 a189, a93\n"
"v_xor_b32 v181, s77, v181\n"
"v_mfma_f32_16x16x32_bf16 v[192:195], v[124:127], a[192:195], 0\n"
"ds_read_b64_tr_b16 a[246:247], v232 offset:53248\n"
"v_xor_b32 v184, s77, v184\n"
"v_mfma_f32_16x16x32_bf16 v[192:195], v[128:131], a[196:199], v[192:195]\n"
"v_mov_b32_dpp v243, v243 row_ror:4 row_mask:0x8 bank_mask:0xf\n"
"v_accvgpr_mov_b32 a172, a76\n"
"v_mfma_f32_16x16x32_bf16 v[192:195], v[132:135], a[200:203], v[192:195]\n"
"v_xor_b32 v185, s77, v185\n"
"v_xor_b32 v187, s77, v187\n"
"v_mfma_f32_16x16x32_bf16 v[192:195], v[136:139], a[204:207], v[192:195]\n"
"ds_read_b64_tr_b16 a[252:253], v235 offset:50176\n"
"v_mov_b32_dpp v245, v245 row_ror:8 row_mask:0x4 bank_mask:0xf\n"
"v_mfma_f32_16x16x32_bf16 v[152:155], v[208:211], a[0:3], v[180:183]\n"
"v_accvgpr_mov_b32 a183, a87\n"
"v_accvgpr_mov_b32 a175, a79\n"
"v_mfma_f32_16x16x32_bf16 v[152:155], v[212:215], a[4:7], v[152:155]\n"
"v_accvgpr_mov_b32 a148, a52\n"
"v_accvgpr_mov_b32 a150, a54\n"
"v_mfma_f32_16x16x32_bf16 v[152:155], v[216:219], a[8:11], v[152:155]\n"
"s_add_u32 s40, s56, s54\n"
"ds_read_b64_tr_b16 a[250:251], v236 offset:53248\n"
"v_accvgpr_mov_b32 a145, a49\n"
"v_mfma_f32_16x16x32_bf16 v[152:155], v[220:223], a[12:15], v[152:155]\n"
"buffer_load_dwordx4 v240, s[20:23], s40 offen offset:3072 nt lds\n"
"v_mov_b32_dpp v244, v244 row_ror:8 row_mask:0x4 bank_mask:0xf\n"
"v_accvgpr_mov_b32 a156, a60\n"
"v_accvgpr_mov_b32 a168, a72\n"
"v_accvgpr_mov_b32 a185, a89\n"
"v_accvgpr_mov_b32 a186, a90\n"
"v_xor_b32 v188, s77, v188\n"
"v_xor_b32 v189, s77, v189\n"
"v_xor_b32 v190, s77, v190\n"
"v_mfma_f32_16x16x32_bf16 v[156:159], v[216:219], a[24:27], v[184:187]\n"
"s_add_u32 s55, s55, s59\n"
"v_mov_b32_dpp v244, v244 row_ror:4 row_mask:0x8 bank_mask:0xf\n"
"v_xor_b32 v191, s77, v191\n"
"v_mfma_f32_16x16x32_bf16 v[156:159], v[220:223], a[28:31], v[156:159]\n"
"s_add_u32 s41, s55, s57\n"
"v_accvgpr_mov_b32 a180, a84\n"
"v_xor_b32 v194, s77, v194\n"
"v_mfma_f32_16x16x32_bf16 v[156:159], v[208:211], a[16:19], v[156:159]\n"
"s_and_b32 s41, s41, s58\n"
"ds_read_b64_tr_b16 a[244:245], v232 offset:49152\n"
"v_xor_b32 v195, s77, v195\n"
"v_mfma_f32_16x16x32_bf16 v[156:159], v[212:215], a[20:23], v[156:159]\n"
"s_andn2_b32 s56, s55, s58\n"
"ds_read_b64_tr_b16 v[226:227], v239 offset:54272\n"
"v_mov_b32_dpp v245, v245 row_ror:4 row_mask:0x8 bank_mask:0xf\n"
"v_accvgpr_mov_b32 a146, a50\n"
"v_accvgpr_mov_b32 a162, a66\n"
"v_accvgpr_mov_b32 a155, a59\n"
"v_accvgpr_mov_b32 a187, a91\n"
"v_xor_b32 v192, s77, v192\n"
"v_exp_f32 v154, v154\n"
"v_mfma_f32_16x16x32_bf16 v[160:163], v[208:211], a[32:35], v[188:191]\n"
"s_or_b32 s56, s56, s41\n"
"v_accvgpr_mov_b32 a157, a61\n"
"v_accvgpr_mov_b32 a152, a56\n"
"v_mfma_f32_16x16x32_bf16 v[160:163], v[212:215], a[36:39], v[160:163]\n"
"v_sub_u32 v255, v242, v255\n"
"v_xor_b32 v193, s77, v193\n"
"v_mfma_f32_16x16x32_bf16 v[160:163], v[216:219], a[40:43], v[160:163]\n"
"v_accvgpr_mov_b32 a151, a55\n"
"v_accvgpr_mov_b32 a144, a48\n"
"v_mfma_f32_16x16x32_bf16 v[160:163], v[220:223], a[44:47], v[160:163]\n"
"ds_read_b64_tr_b16 a[254:255], v235 offset:54272\n"
"v_accvgpr_mov_b32 a165, a69\n"
"v_accvgpr_mov_b32 a177, a81\n"
"v_accvgpr_mov_b32 a153, a57\n"
"v_mul_f32_dpp v40, v244, v40 row_newbcast:0 row_mask:0xf bank_mask:0xf\n"
"v_mul_f32_dpp v72, v245, v72 row_newbcast:0 row_mask:0xf bank_mask:0xf\n"
"v_exp_f32 v156, v156\n"
"v_exp_f32 v158, v158\n"
"v_mfma_f32_16x16x32_bf16 v[164:167], v[216:219], a[200:203], v[192:195]\n"
"v_accvgpr_mov_b32 a166, a70\n"
"v_accvgpr_mov_b32 a188, a92\n"
"v_mfma_f32_16x16x32_bf16 v[164:167], v[220:223], a[204:207], v[164:167]\n"
"v_accvgpr_mov_b32 a191, a95\n"
"v_mul_f32_dpp v8, v243, v8 row_newbcast:0 row_mask:0xf bank_mask:0xf\n"
"v_mfma_f32_16x16x32_bf16 v[164:167], v[208:211], a[192:195], v[164:167]\n"
"ds_read_b128 v[208:211], v228 offset:4096\n"
"v_mul_f32_dpp v76, v245, v76 row_newbcast:0 row_mask:0xf bank_mask:0xf\n"
"v_mfma_f32_16x16x32_bf16 v[164:167], v[212:215], a[196:199], v[164:167]\n"
"s_waitcnt lgkmcnt(2)\n"
"ds_read_b128 v[216:219], v228 offset:5120\n"
"v_accvgpr_mov_b32 a149, a53\n"
"v_accvgpr_mov_b32 a176, a80\n"
"v_accvgpr_mov_b32 a171, a75\n"
"v_mul_f32_dpp v44, v244, v44 row_newbcast:0 row_mask:0xf bank_mask:0xf\n"
"v_exp_f32 v163, v163\n"
"LTC_%=:\n"
"s_waitcnt lgkmcnt(3)\n"
"v_mfma_f32_16x16x32_bf16 v[92:95], v[116:119], v[224:227], v[92:95]\n"
"s_waitcnt lgkmcnt(2)\n"
"ds_read_b128 v[220:223], v229 offset:5120\n"
"v_mul_f32_dpp v46, v244, v46 row_newbcast:2 row_mask:0xf bank_mask:0xf\n"
"v_mfma_f32_16x16x32_bf16 v[92:95], v[112:115], a[252:255], v[92:95]\n"
"ds_read_b128 v[212:215], v229 offset:4096\n"
"v_exp_f32 v152, v152\n"
"v_exp_f32 v162, v162\n"
"v_bfe_u32 v207, v242, 8, 2\n"
"v_mfma_f32_16x16x32_bf16 v[60:63], v[108:111], v[224:227], v[60:63]\n"
"s_add_u32 m0, s44, 32768\n"
"v_xor_b32 v250, 0x1000, v255\n"
"v_mul_f32_dpp v9, v243, v9 row_newbcast:1 row_mask:0xf bank_mask:0xf\n"
"v_mfma_f32_16x16x32_bf16 v[60:63], v[104:107], a[252:255], v[60:63]\n"
"buffer_load_dwordx4 v240, s[16:19], s56 offen nt lds\n"
"v_mfma_f32_16x16x32_bf16 v[28:31], v[100:103], v[224:227], v[28:31]\n"
"v_exp_f32 v166, v166\n"
"v_mfma_f32_16x16x32_bf16 v[28:31], v[96:99], a[252:255], v[28:31]\n"
"v_mul_f32_dpp v11, v243, v11 row_newbcast:3 row_mask:0xf bank_mask:0xf\n"
"v_mul_f32_dpp v56, v244, v56 row_newbcast:0 row_mask:0xf bank_mask:0xf\n"
"v_mfma_f32_16x16x32_bf16 a[236:239], v[120:123], a[252:255], a[236:239]\n"
"s_waitcnt lgkmcnt(2)\n"
"ds_write_b128 v254, a[216:219] offset:32768\n"
"v_mul_f32_dpp v14, v243, v14 row_newbcast:2 row_mask:0xf bank_mask:0xf\n"
"v_cvt_pk_bf16_f32 v8, v8, v9\n"
"v_exp_f32 v155, v155\n"
"v_mul_f32_dpp v59, v244, v59 row_newbcast:3 row_mask:0xf bank_mask:0xf\n"
"v_mfma_f32_16x16x32_bf16 v[168:171], v[216:219], a[8:11], v[180:183]\n"
"s_waitcnt lgkmcnt(2)\n"
"v_exp_f32 v160, v160\n"
"v_mfma_f32_16x16x32_bf16 v[168:171], v[220:223], a[12:15], v[168:171]\n"
"v_exp_f32 v153, v153\n"
"v_mfma_f32_16x16x32_bf16 v[168:171], v[208:211], a[0:3], v[168:171]\n"
"s_waitcnt lgkmcnt(1)\n"
"ds_read_b64_tr_b16 v[224:225], v237 offset:49152\n"
"v_mul_f32_dpp v58, v244, v58 row_newbcast:2 row_mask:0xf bank_mask:0xf\n"
"v_mfma_f32_16x16x32_bf16 v[168:171], v[212:215], a[4:7], v[168:171]\n"
"v_mul_f32_dpp v13, v243, v13 row_newbcast:1 row_mask:0xf bank_mask:0xf\n"
"v_mul_f32_dpp v41, v244, v41 row_newbcast:1 row_mask:0xf bank_mask:0xf\n"
"v_mfma_f32_16x16x32_bf16 v[172:175], v[208:211], a[16:19], v[184:187]\n"
"v_mul_f32_dpp v63, v244, v63 row_newbcast:3 row_mask:0xf bank_mask:0xf\n"
"v_mul_f32_dpp v94, v245, v94 row_newbcast:2 row_mask:0xf bank_mask:0xf\n"
"v_mfma_f32_16x16x32_bf16 v[172:175], v[212:215], a[20:23], v[172:175]\n"
"ds_read_b64_tr_b16 a[254:255], v233 offset:53248\n"
"v_mov_b32 v253, v202\n"
"v_mfma_f32_16x16x32_bf16 v[172:175], v[216:219], a[24:27], v[172:175]\n"
"v_cvt_pk_bf16_f32 v40, v40, v41\n"
"v_mul_f32_dpp v90, v245, v90 row_newbcast:2 row_mask:0xf bank_mask:0xf\n"
"v_mfma_f32_16x16x32_bf16 v[172:175], v[220:223], a[28:31], v[172:175]\n"
"ds_write_b64 v250, v[252:253] offset:52224\n"
"v_cvt_pk_bf16_f32 v128, v152, v153\n"
"v_cvt_pk_bf16_f32 v129, v154, v155\n"
"v_exp_f32 v159, v159\n"
"v_exp_f32 v165, v165\n"
"v_mul_f32_dpp v24, v243, v24 row_newbcast:0 row_mask:0xf bank_mask:0xf\n"
"v_mfma_f32_16x16x32_bf16 v[176:179], v[216:219], a[40:43], v[188:191]\n"
"v_mul_f32_dpp v10, v243, v10 row_newbcast:2 row_mask:0xf bank_mask:0xf\n"
"v_mul_f32_dpp v73, v245, v73 row_newbcast:1 row_mask:0xf bank_mask:0xf\n"
"v_mfma_f32_16x16x32_bf16 v[176:179], v[220:223], a[44:47], v[176:179]\n"
"v_mul_f32_dpp v43, v244, v43 row_newbcast:3 row_mask:0xf bank_mask:0xf\n"
"v_mul_f32_dpp v75, v245, v75 row_newbcast:3 row_mask:0xf bank_mask:0xf\n"
"v_mfma_f32_16x16x32_bf16 v[176:179], v[208:211], a[32:35], v[176:179]\n"
"s_add_u32 s40, s56, s52\n"
"ds_read_b64_tr_b16 a[248:249], v236 offset:49152\n"
"v_mul_f32_dpp v74, v245, v74 row_newbcast:2 row_mask:0xf bank_mask:0xf\n"
"v_mfma_f32_16x16x32_bf16 v[176:179], v[212:215], a[36:39], v[176:179]\n"
"buffer_load_dwordx4 v240, s[16:19], s40 offen offset:1024 nt lds\n"
"v_mul_f32_dpp v12, v243, v12 row_newbcast:0 row_mask:0xf bank_mask:0xf\n"
"v_exp_f32 v161, v161\n"
"v_mul_f32_dpp v25, v243, v25 row_newbcast:1 row_mask:0xf bank_mask:0xf\n"
"v_mul_f32_dpp v26, v243, v26 row_newbcast:2 row_mask:0xf bank_mask:0xf\n"
"v_mul_f32_dpp v88, v245, v88 row_newbcast:0 row_mask:0xf bank_mask:0xf\n"
"v_exp_f32 v172, v172\n"
"v_mfma_f32_16x16x32_bf16 v[152:155], v[208:211], a[192:195], v[192:195]\n"
"v_exp_f32 v171, v171\n"
"v_mfma_f32_16x16x32_bf16 v[152:155], v[212:215], a[196:199], v[152:155]\n"
"v_cvt_pk_bf16_f32 v9, v10, v11\n"
"v_mul_f32_dpp v42, v244, v42 row_newbcast:2 row_mask:0xf bank_mask:0xf\n"
"v_mfma_f32_16x16x32_bf16 v[152:155], v[216:219], a[200:203], v[152:155]\n"
"ds_read_b128 v[208:211], v230 offset:0\n"
"v_cvt_pk_bf16_f32 v144, v160, v161\n"
"v_mfma_f32_16x16x32_bf16 v[152:155], v[220:223], a[204:207], v[152:155]\n"
"ds_read_b128 v[216:219], v230 offset:1024\n"
"v_cvt_pk_bf16_f32 v41, v42, v43\n"
"v_cvt_pk_bf16_f32 v145, v162, v163\n"
"v_mul_f32_dpp v30, v243, v30 row_newbcast:2 row_mask:0xf bank_mask:0xf\n"
"v_exp_f32 v170, v170\n"
"v_exp_f32 v174, v174\n"
"v_mfma_f32_16x16x32_bf16 a[208:211], v[120:123], a[244:247], a[208:211]\n"
"v_mul_f32_dpp v77, v245, v77 row_newbcast:1 row_mask:0xf bank_mask:0xf\n"
"v_mul_f32_dpp v29, v243, v29 row_newbcast:1 row_mask:0xf bank_mask:0xf\n"
"v_mfma_f32_16x16x32_bf16 v[0:3], v[96:99], a[244:247], v[0:3]\n"
"s_waitcnt lgkmcnt(2)\n"
"ds_read_b128 v[220:223], v231 offset:1024\n"
"v_cvt_pk_bf16_f32 v72, v72, v73\n"
"v_mfma_f32_16x16x32_bf16 v[0:3], v[100:103], a[248:251], v[0:3]\n"
"ds_read_b128 v[212:215], v231 offset:0\n"
"v_mul_f32_dpp v45, v244, v45 row_newbcast:1 row_mask:0xf bank_mask:0xf\n"
"v_mul_f32_dpp v78, v245, v78 row_newbcast:2 row_mask:0xf bank_mask:0xf\n"
"v_exp_f32 v164, v164\n"
"v_mul_f32_dpp v93, v245, v93 row_newbcast:1 row_mask:0xf bank_mask:0xf\n"
"v_exp_f32 v176, v176\n"
"v_exp_f32 v153, v153\n"
"v_mfma_f32_16x16x32_bf16 v[32:35], v[104:107], a[244:247], v[32:35]\n"
"v_cvt_pk_bf16_f32 v137, v158, v159\n"
"v_accvgpr_read_b32 v206, a209\n"
"v_mfma_f32_16x16x32_bf16 v[32:35], v[108:111], a[248:251], v[32:35]\n"
"ds_read_b64_tr_b16 a[252:253], v233 offset:49152\n"
"v_mul_f32_dpp v79, v245, v79 row_newbcast:3 row_mask:0xf bank_mask:0xf\n"
"v_exp_f32 v157, v157\n"
"v_exp_f32 v167, v167\n"
"v_mul_f32_dpp v62, v244, v62 row_newbcast:2 row_mask:0xf bank_mask:0xf\n"
"v_mul_f32_dpp v92, v245, v92 row_newbcast:0 row_mask:0xf bank_mask:0xf\n"
"v_exp_f32 v169, v169\n"
"v_mfma_f32_16x16x32_bf16 v[64:67], v[112:115], a[244:247], v[64:67]\n"
"ds_read_b64_tr_b16 v[226:227], v237 offset:53248\n"
"v_cvt_pk_bf16_f32 v136, v156, v157\n"
"v_mfma_f32_16x16x32_bf16 v[64:67], v[116:119], a[248:251], v[64:67]\n"
"s_waitcnt lgkmcnt(5)\n"
"ds_write_b128 v254, a[236:239] offset:35840\n"
"v_mul_f32_dpp v57, v244, v57 row_newbcast:1 row_mask:0xf bank_mask:0xf\n"
"v_cvt_pk_bf16_f32 v56, v56, v57\n"
"v_exp_f32 v168, v168\n"
"v_cvt_pk_bf16_f32 v131, v170, v171\n"
"v_mfma_f32_16x16x32_bf16 v[156:159], v[208:211], a[0:3], v[180:183]\n"
"s_waitcnt lgkmcnt(3)\n"
"v_mul_f32_dpp v15, v243, v15 row_newbcast:3 row_mask:0xf bank_mask:0xf\n"
"v_cvt_pk_bf16_f32 v10, v12, v13\n"
"v_mfma_f32_16x16x32_bf16 v[156:159], v[212:215], a[4:7], v[156:159]\n"
"v_mul_f32_dpp v28, v243, v28 row_newbcast:0 row_mask:0xf bank_mask:0xf\n"
"v_mul_f32_dpp v3, v243, v3 row_newbcast:3 row_mask:0xf bank_mask:0xf\n"
"v_mfma_f32_16x16x32_bf16 v[156:159], v[216:219], a[8:11], v[156:159]\n"
"s_add_u32 s40, s56, s53\n"
"ds_read_b64_tr_b16 a[244:245], v232 offset:50176\n"
"v_mul_f32_dpp v89, v245, v89 row_newbcast:1 row_mask:0xf bank_mask:0xf\n"
"v_mfma_f32_16x16x32_bf16 v[156:159], v[220:223], a[12:15], v[156:159]\n"
"buffer_load_dwordx4 v240, s[16:19], s40 offen offset:2048 nt lds\n"
"v_cvt_pk_bf16_f32 v73, v74, v75\n"
"v_cvt_pk_bf16_f32 v124, v164, v165\n"
"v_cvt_pk_bf16_f32 v24, v24, v25\n"
"v_exp_f32 v173, v173\n"
"v_exp_f32 v175, v175\n"
"v_mul_f32_dpp v33, v244, v33 row_newbcast:1 row_mask:0xf bank_mask:0xf\n"
"v_mfma_f32_16x16x32_bf16 v[160:163], v[216:219], a[24:27], v[184:187]\n"
"v_cvt_pk_bf16_f32 v11, v14, v15\n"
"v_cvt_pk_bf16_f32 v57, v58, v59\n"
"v_mfma_f32_16x16x32_bf16 v[160:163], v[220:223], a[28:31], v[160:163]\n"
"v_cvt_pk_bf16_f32 v130, v168, v169\n"
"v_mul_f32_dpp v0, v243, v0 row_newbcast:0 row_mask:0xf bank_mask:0xf\n"
"v_mfma_f32_16x16x32_bf16 v[160:163], v[208:211], a[16:19], v[160:163]\n"
"v_cvt_pk_bf16_f32 v125, v166, v167\n"
"v_mul_f32_dpp v27, v243, v27 row_newbcast:3 row_mask:0xf bank_mask:0xf\n"
"v_mfma_f32_16x16x32_bf16 v[160:163], v[212:215], a[20:23], v[160:163]\n"
"ds_write_b128 v254, a[220:223] offset:33792\n"
"v_mul_f32_dpp v47, v244, v47 row_newbcast:3 row_mask:0xf bank_mask:0xf\n"
"v_cvt_pk_bf16_f32 v42, v44, v45\n"
"v_exp_f32 v155, v155\n"
"v_mul_f32_dpp v34, v244, v34 row_newbcast:2 row_mask:0xf bank_mask:0xf\n"
"v_mfma_f32_16x16x32_bf16 v[164:167], v[208:211], a[32:35], v[188:191]\n"
"ds_read_b64_tr_b16 a[246:247], v232 offset:54272\n"
"v_mul_f32_dpp v35, v244, v35 row_newbcast:3 row_mask:0xf bank_mask:0xf\n"
"v_mfma_f32_16x16x32_bf16 v[164:167], v[212:215], a[36:39], v[164:167]\n"
"ds_read_b128 v[208:211], v230 offset:4096\n"
"v_mul_f32_dpp v60, v244, v60 row_newbcast:0 row_mask:0xf bank_mask:0xf\n"
"v_mfma_f32_16x16x32_bf16 v[164:167], v[216:219], a[40:43], v[164:167]\n"
"v_mul_f32_dpp v91, v245, v91 row_newbcast:3 row_mask:0xf bank_mask:0xf\n"
"v_mul_f32_dpp v64, v245, v64 row_newbcast:0 row_mask:0xf bank_mask:0xf\n"
"v_mfma_f32_16x16x32_bf16 v[164:167], v[220:223], a[44:47], v[164:167]\n"
"s_waitcnt lgkmcnt(3)\n"
"ds_write_b128 v254, a[232:235] offset:34816\n"
"v_cvt_pk_bf16_f32 v75, v78, v79\n"
"v_cvt_pk_bf16_f32 v88, v88, v89\n"
"v_exp_f32 v158, v158\n"
"v_mfma_f32_16x16x32_bf16 v[68:71], v[116:119], v[224:227], v[68:71]\n"
"ds_read_b128 v[220:223], v231 offset:5120\n"
"v_cvt_pk_bf16_f32 v74, v76, v77\n"
"v_mfma_f32_16x16x32_bf16 v[68:71], v[112:115], a[252:255], v[68:71]\n"
"ds_read_b128 v[212:215], v231 offset:4096\n"
"v_mul_f32_dpp v32, v244, v32 row_newbcast:0 row_mask:0xf bank_mask:0xf\n"
"v_mfma_f32_16x16x32_bf16 v[36:39], v[108:111], v[224:227], v[36:39]\n"
"s_add_u32 s40, s56, s54\n"
"ds_read_b128 v[216:219], v230 offset:5120\n"
"v_mul_f32_dpp v61, v244, v61 row_newbcast:1 row_mask:0xf bank_mask:0xf\n"
"v_mfma_f32_16x16x32_bf16 v[36:39], v[104:107], a[252:255], v[36:39]\n"
"buffer_load_dwordx4 v240, s[16:19], s40 offen offset:3072 nt lds\n"
"v_cvt_pk_bf16_f32 v43, v46, v47\n"
"v_mul_f32_dpp v31, v243, v31 row_newbcast:3 row_mask:0xf bank_mask:0xf\n"
"v_cvt_pk_bf16_f32 v59, v62, v63\n"
"v_cvt_pk_bf16_f32 v138, v172, v173\n"
"v_exp_f32 v177, v177\n"
"v_exp_f32 v152, v152\n"
"v_mfma_f32_16x16x32_bf16 v[4:7], v[100:103], v[224:227], v[4:7]\n"
"v_cvt_pk_bf16_f32 v25, v26, v27\n"
"v_mul_f32_dpp v1, v243, v1 row_newbcast:1 row_mask:0xf bank_mask:0xf\n"
"v_mfma_f32_16x16x32_bf16 v[4:7], v[96:99], a[252:255], v[4:7]\n"
"ds_write_b128 v246, v[8:11] offset:49152\n"
"v_cvt_pk_bf16_f32 v146, v176, v177\n"
"v_exp_f32 v179, v179\n"
"v_exp_f32 v159, v159\n"
"v_mfma_f32_16x16x32_bf16 a[212:215], v[120:123], a[252:255], a[212:215]\n"
"s_waitcnt lgkmcnt(5)\n"
"ds_write_b128 v246, v[40:43] offset:50176\n"
"v_cvt_pk_bf16_f32 v26, v28, v29\n"
"v_exp_f32 v178, v178\n"
"v_mul_f32_dpp v2, v243, v2 row_newbcast:2 row_mask:0xf bank_mask:0xf\n"
"v_lshlrev_b32 v207, 6, v207\n"
"v_mfma_f32_16x16x32_bf16 v[168:171], v[208:211], a[0:3], v[180:183]\n"
"s_waitcnt lgkmcnt(3)\n"
"ds_read_b64_tr_b16 a[70:71], v247 offset:50184\n"
"ds_read_b64_tr_b16 a[250:251], v236 offset:54272\n"
"v_mfma_f32_16x16x32_bf16 v[168:171], v[212:215], a[4:7], v[168:171]\n"
"s_waitcnt lgkmcnt(4)\n"
"v_cvt_pk_bf16_f32 v89, v90, v91\n"
"v_mul_f32_dpp v67, v245, v67 row_newbcast:3 row_mask:0xf bank_mask:0xf\n"
"v_mfma_f32_16x16x32_bf16 v[168:171], v[216:219], a[8:11], v[168:171]\n"
"ds_read_b64_tr_b16 a[68:69], v247 offset:50176\n"
"ds_read_b64_tr_b16 a[248:249], v236 offset:50176\n"
"v_mfma_f32_16x16x32_bf16 v[168:171], v[220:223], a[12:15], v[168:171]\n"
"ds_write_b128 v246, v[72:75] offset:51200\n"
"v_cvt_pk_bf16_f32 v58, v60, v61\n"
"v_cvt_pk_bf16_f32 v139, v174, v175\n"
"v_cvt_pk_bf16_f32 v0, v0, v1\n"
"v_cvt_pk_bf16_f32 v32, v32, v33\n"
"v_mfma_f32_16x16x32_bf16 v[172:175], v[216:219], a[24:27], v[184:187]\n"
"v_exp_f32 v157, v157\n"
"v_mfma_f32_16x16x32_bf16 v[172:175], v[220:223], a[28:31], v[172:175]\n"
"v_cvt_pk_bf16_f32 v27, v30, v31\n"
"v_cvt_pk_bf16_f32 v1, v2, v3\n"
"v_mfma_f32_16x16x32_bf16 v[172:175], v[208:211], a[16:19], v[172:175]\n"
"ds_read_b64_tr_b16 a[86:87], v247 offset:51208\n"
"ds_read_b64_tr_b16 a[252:253], v233 offset:50176\n"
"v_mfma_f32_16x16x32_bf16 v[172:175], v[212:215], a[20:23], v[172:175]\n"
"s_cbranch_vccnz LPCT0_%=\nLPKT0_%=:\n"
"ds_read_b64_tr_b16 v[224:225], v237 offset:50176\n"
"ds_read_b64_tr_b16 v[226:227], v237 offset:54272\n"
"v_cvt_pk_bf16_f32 v147, v178, v179\n"
"v_exp_f32 v154, v154\n"
"v_exp_f32 v162, v162\n"
"v_mul_f32_dpp v39, v244, v39 row_newbcast:3 row_mask:0xf bank_mask:0xf\n"
"v_mfma_f32_16x16x32_bf16 v[176:179], v[208:211], a[32:35], v[188:191]\n"
"ds_read_b64_tr_b16 a[254:255], v233 offset:54272\n"
"v_xor_b32 v254, 0x1000, v254\n"
"v_mfma_f32_16x16x32_bf16 v[176:179], v[212:215], a[36:39], v[176:179]\n"
"ds_read_b64_tr_b16 a[52:53], v247 offset:49152\n"
"v_mul_f32_dpp v38, v244, v38 row_newbcast:2 row_mask:0xf bank_mask:0xf\n"
"v_mfma_f32_16x16x32_bf16 v[176:179], v[216:219], a[40:43], v[176:179]\n"
"ds_write_b128 v246, v[56:59] offset:50176\n"
"v_exp_f32 v166, v166\n"
"v_mul_f32_dpp v68, v245, v68 row_newbcast:0 row_mask:0xf bank_mask:0xf\n"
"v_exp_f32 v172, v172\n"
"v_mfma_f32_16x16x32_bf16 v[176:179], v[220:223], a[44:47], v[176:179]\n"
"ds_read_b64_tr_b16 a[54:55], v247 offset:49160\n"
"ds_read_b64_tr_b16 a[78:79], v247 offset:50184\n"
"v_mfma_f32_16x16x32_bf16 a[224:227], v[120:123], a[244:247], a[224:227]\n"
"ds_write_b128 v246, v[24:27] offset:49152\n"
"v_cvt_pk_bf16_f32 v90, v92, v93\n"
"v_cvt_pk_bf16_f32 v126, v152, v153\n"
"v_exp_f32 v156, v156\n"
"v_mul_f32_dpp v71, v245, v71 row_newbcast:3 row_mask:0xf bank_mask:0xf\n"
"v_mfma_f32_16x16x32_bf16 v[16:19], v[96:99], a[244:247], v[16:19]\n"
"s_waitcnt vmcnt(8) lgkmcnt(5)\n"
"v_cvt_pk_bf16_f32 v33, v34, v35\n"
"v_mul_f32_dpp v65, v245, v65 row_newbcast:1 row_mask:0xf bank_mask:0xf\n"
"v_mfma_f32_16x16x32_bf16 v[16:19], v[100:103], a[248:251], v[16:19]\n"
"s_barrier\n"
"ds_read_b128 v[208:211], v228 offset:16384\n"
"v_exp_f32 v164, v164\n"
"v_cvt_pk_bf16_f32 v35, v38, v39\n"
"v_exp_f32 v170, v170\n"
"v_exp_f32 v177, v177\n"
"v_mfma_f32_16x16x32_bf16 v[48:51], v[104:107], a[244:247], v[48:51]\n"
"ds_read_b128 v[24:27], v254 offset:32768\n"
"v_cvt_pk_bf16_f32 v127, v154, v155\n"
"v_mfma_f32_16x16x32_bf16 v[48:51], v[108:111], a[248:251], v[48:51]\n"
"ds_read_b64_tr_b16 a[76:77], v247 offset:50176\n"
"ds_read_b64 v[250:251], v255 offset:52224\n"
"v_mul_f32_dpp v95, v245, v95 row_newbcast:3 row_mask:0xf bank_mask:0xf\n"
"v_cvt_pk_bf16_f32 v133, v158, v159\n"
"v_mul_f32_dpp v7, v243, v7 row_newbcast:3 row_mask:0xf bank_mask:0xf\n"
"v_mul_f32_dpp v36, v244, v36 row_newbcast:0 row_mask:0xf bank_mask:0xf\n"
"v_mul_f32_dpp v70, v245, v70 row_newbcast:2 row_mask:0xf bank_mask:0xf\n"
"v_xor_b32 v207, v207, v248\n"
"v_mfma_f32_16x16x32_bf16 v[80:83], v[112:115], a[244:247], v[80:83]\n"
"ds_read_b128 v[220:223], v229 offset:17408\n"
"v_mul_f32_dpp v6, v243, v6 row_newbcast:2 row_mask:0xf bank_mask:0xf\n"
"v_mfma_f32_16x16x32_bf16 v[80:83], v[116:119], a[248:251], v[80:83]\n"
"ds_read_b128 v[212:215], v229 offset:16384\n"
"v_exp_f32 v161, v161\n"
"v_mul_f32_dpp v5, v243, v5 row_newbcast:1 row_mask:0xf bank_mask:0xf\n"
"v_mul_f32_dpp v37, v244, v37 row_newbcast:1 row_mask:0xf bank_mask:0xf\n"
"v_mul_f32_dpp v66, v245, v66 row_newbcast:2 row_mask:0xf bank_mask:0xf\n"
"v_mul_f32_dpp v69, v245, v69 row_newbcast:1 row_mask:0xf bank_mask:0xf\n"
"v_cvt_pk_bf16_f32 v64, v64, v65\n"
"v_cvt_pk_bf16_f32 v65, v66, v67\n"
"v_mfma_f32_16x16x32_bf16 v[84:87], v[116:119], v[224:227], v[84:87]\n"
"ds_read_b64_tr_b16 a[84:85], v247 offset:51200\n"
"v_mul_f32_dpp v49, v244, v49 row_newbcast:1 row_mask:0xf bank_mask:0xf\n"
"v_mfma_f32_16x16x32_bf16 v[84:87], v[112:115], a[252:255], v[84:87]\n"
"ds_read_b128 v[28:31], v254 offset:33792\n"
"v_cvt_pk_bf16_f32 v91, v94, v95\n"
"v_mfma_f32_16x16x32_bf16 v[52:55], v[108:111], v[224:227], v[52:55]\n"
"ds_read_b128 v[60:63], v254 offset:35840\n"
"v_cvt_pk_bf16_f32 v132, v156, v157\n"
"v_mfma_f32_16x16x32_bf16 v[52:55], v[104:107], a[252:255], v[52:55]\n"
"s_waitcnt lgkmcnt(5)\n"
"ds_write_b128 v246, v[88:91] offset:51200\n"
"v_mul_f32_dpp v4, v243, v4 row_newbcast:0 row_mask:0xf bank_mask:0xf\n"
"v_cvt_pk_bf16_f32 v66, v68, v69\n"
"v_exp_f32 v173, v173\n"
"v_min_f32 v249, v252, v250\n"
"v_mfma_f32_16x16x32_bf16 v[20:23], v[100:103], v[224:227], v[20:23]\n"
"ds_read_b128 v[216:219], v228 offset:17408\n"
"v_cvt_pk_bf16_f32 v3, v6, v7\n"
"v_mfma_f32_16x16x32_bf16 v[20:23], v[96:99], a[252:255], v[20:23]\n"
"ds_read_b64_tr_b16 a[60:61], v247 offset:49152\n"
"ds_read_b64_tr_b16 a[92:93], v247 offset:51200\n"
"v_exp_f32 v160, v160\n"
"v_exp_f32 v165, v165\n"
"v_cvt_pk_bf16_f32 v2, v4, v5\n"
"v_sub_f32 v250, v249, v250\n"
"v_mul_f32_dpp v18, v243, v18 row_newbcast:2 row_mask:0xf bank_mask:0xf\n"
"v_mfma_f32_16x16x32_bf16 a[228:231], v[120:123], a[252:255], a[228:231]\n"
"ds_read_b64_tr_b16 a[94:95], v247 offset:51208\n"
"ds_read_b64_tr_b16 a[246:247], v234 offset:4096\n"
"v_exp_f32 v163, v163\n"
"v_cvt_pk_bf16_f32 v34, v36, v37\n"
"v_exp_f32 v168, v168\n"
"v_exp_f32 v250, v250\n"
"v_mul_f32_dpp v85, v245, v85 row_newbcast:1 row_mask:0xf bank_mask:0xf\n"
"v_mfma_f32_16x16x32_bf16 v[152:155], v[208:211], a[0:3], v[180:183]\n"
"s_waitcnt lgkmcnt(9)\n"
"ds_read_b64_tr_b16 a[62:63], v247 offset:49160\n"
"v_mul_f32_dpp v80, v245, v80 row_newbcast:0 row_mask:0xf bank_mask:0xf\n"
"v_mfma_f32_16x16x32_bf16 v[152:155], v[212:215], a[4:7], v[152:155]\n"
"s_waitcnt lgkmcnt(5)\n"
"ds_write_b128 v246, v[32:35] offset:50176\n"
"v_cvt_pk_bf16_f32 v141, v162, v163\n"
"v_exp_f32 v167, v167\n"
"v_cvt_pk_bf16_f32 v67, v70, v71\n"
"v_sub_f32 v252, v249, v252\n"
"v_mfma_f32_16x16x32_bf16 v[152:155], v[216:219], a[8:11], v[152:155]\n"
"ds_write_b128 v246, v[0:3] offset:49152\n"
"v_cvt_pk_bf16_f32 v148, v164, v165\n"
"v_exp_f32 v171, v171\n"
"v_accvgpr_read_b32 v249, a208\n"
"v_mul_f32_dpp v16, v243, v16 row_newbcast:0 row_mask:0xf bank_mask:0xf\n"
"v_mfma_f32_16x16x32_bf16 v[152:155], v[220:223], a[12:15], v[152:155]\n"
"ds_write_b128 v246, v[64:67] offset:51200\n"
"v_cvt_pk_bf16_f32 v140, v160, v161\n"
"v_cvt_pk_bf16_f32 v149, v166, v167\n"
"v_exp_f32 v252, v252\n"
"v_mul_f32_dpp v48, v244, v48 row_newbcast:0 row_mask:0xf bank_mask:0xf\n"
"v_mfma_f32_16x16x32_bf16 v[156:159], v[216:219], a[24:27], v[184:187]\n"
"ds_read_b128 v[56:59], v254 offset:34816\n"
"v_mul_f32 v253, v253, v252\n"
"v_mfma_f32_16x16x32_bf16 v[156:159], v[220:223], a[28:31], v[156:159]\n"
"ds_read_b64_tr_b16 a[48:49], v247 offset:49152\n"
"ds_read_b64_tr_b16 a[82:83], v247 offset:51208\n"
"v_mfma_f32_16x16x32_bf16 v[156:159], v[208:211], a[16:19], v[156:159]\n"
"ds_read_b64_tr_b16 a[64:65], v247 offset:50176\n"
"v_fmac_f32 v253, v251, v250\n"
"v_mfma_f32_16x16x32_bf16 v[156:159], v[212:215], a[20:23], v[156:159]\n"
"ds_read_b64_tr_b16 a[50:51], v247 offset:49160\n"
"ds_read_b64_tr_b16 a[66:67], v247 offset:50184\n"
"ds_read_b64_tr_b16 a[80:81], v247 offset:51200\n"
"v_exp_f32 v169, v169\n"
"v_exp_f32 v174, v174\n"
"v_rcp_f32 v253, v253\n"
"v_mfma_f32_16x16x32_bf16 v[160:163], v[208:211], a[32:35], v[188:191]\n"
"v_mul_f32 v252, v252, v253\n"
"v_mul_f32_dpp v83, v245, v83 row_newbcast:3 row_mask:0xf bank_mask:0xf\n"
"v_mfma_f32_16x16x32_bf16 v[160:163], v[212:215], a[36:39], v[160:163]\n"
"ds_read_b64_tr_b16 a[244:245], v234 offset:0\n"
"ds_read_b64_tr_b16 a[250:251], v238 offset:4096\n"
"v_mfma_f32_16x16x32_bf16 v[160:163], v[216:219], a[40:43], v[160:163]\n"
"v_exp_f32 v178, v178\n"
"v_mfma_f32_16x16x32_bf16 v[160:163], v[220:223], a[44:47], v[160:163]\n"
"ds_read_b64_tr_b16 a[248:249], v238 offset:0\n"
"ds_read_b64_tr_b16 a[254:255], v235 offset:4096\n"
"v_mov_b64 v[198:199], 0\n"
"v_cvt_pk_bf16_f32 v134, v168, v169\n"
"v_exp_f32 v175, v175\n"
"v_exp_f32 v176, v176\n"
"v_mul_f32 v250, v250, v253\n"
"v_mov_b32_dpp v252, v252 row_ror:12 row_mask:0x2 bank_mask:0xf\n"
"v_mfma_f32_16x16x32_bf16 v[164:167], v[216:219], a[200:203], v[192:195]\n"
"v_cvt_pk_bf16_f32 v142, v172, v173\n"
"v_mul_f32_dpp v23, v243, v23 row_newbcast:3 row_mask:0xf bank_mask:0xf\n"
"v_mfma_f32_16x16x32_bf16 v[164:167], v[220:223], a[204:207], v[164:167]\n"
"ds_read_b64_tr_b16 v[224:225], v239 offset:0\n"
"ds_read_b64_tr_b16 v[226:227], v239 offset:4096\n"
"v_mfma_f32_16x16x32_bf16 v[164:167], v[208:211], a[192:195], v[164:167]\n"
"v_cvt_pk_bf16_f32 v150, v176, v177\n"
"v_mul_f32_dpp v82, v245, v82 row_newbcast:2 row_mask:0xf bank_mask:0xf\n"
"v_mfma_f32_16x16x32_bf16 v[164:167], v[212:215], a[196:199], v[164:167]\n"
"s_waitcnt lgkmcnt(5)\n"
"ds_read_b128 v[220:223], v229 offset:21504\n"
"v_mov_b64 v[200:201], 0\n"
"v_cvt_pk_bf16_f32 v135, v170, v171\n"
"v_cvt_pk_bf16_f32 v143, v174, v175\n"
"v_exp_f32 v179, v179\n"
"v_mov_b32_dpp v250, v250 row_ror:12 row_mask:0x2 bank_mask:0xf\n"
"v_xor_b32 v246, s75, v246\n"
"v_mul_f32_dpp v21, v243, v21 row_newbcast:1 row_mask:0xf bank_mask:0xf\n"
"v_mfma_f32_16x16x32_bf16 a[216:219], v[124:127], a[244:247], 0\n"
"v_mov_b32_dpp v252, v252 row_ror:8 row_mask:0x4 bank_mask:0xf\n"
"v_mul_f32_dpp v84, v245, v84 row_newbcast:0 row_mask:0xf bank_mask:0xf\n"
"v_mfma_f32_16x16x32_bf16 v[8:11], v[128:131], a[244:247], 0\n"
"s_waitcnt lgkmcnt(4)\n"
"s_add_u32 m0, s44, 98304\n"
"ds_read_b64_tr_b16 a[252:253], v235 offset:0\n"
"v_mov_b64 v[202:203], 0\n"
"v_mfma_f32_16x16x32_bf16 v[8:11], v[132:135], a[248:251], v[8:11]\n"
"buffer_load_dwordx4 v240, s[20:23], s56 offen nt lds\n"
"v_cvt_pk_bf16_f32 v151, v178, v179\n"
"v_mov_b32_dpp v250, v250 row_ror:8 row_mask:0x4 bank_mask:0xf\n"
"v_mov_b32_dpp v252, v252 row_ror:4 row_mask:0x8 bank_mask:0xf\n"
"v_accvgpr_read_b32 v251, a211\n"
"v_mul_f32_dpp v87, v245, v87 row_newbcast:3 row_mask:0xf bank_mask:0xf\n"
"v_exp_f32 v163, v163\n"
"v_mfma_f32_16x16x32_bf16 v[40:43], v[136:139], a[244:247], 0\n"
"v_mov_b32_dpp v250, v250 row_ror:4 row_mask:0x8 bank_mask:0xf\n"
"v_add_u32 v248, 0x1000, v248\n"
"v_mfma_f32_16x16x32_bf16 v[40:43], v[140:143], a[248:251], v[40:43]\n"
"v_mul_f32_dpp v25, v250, v25 row_newbcast:1 row_mask:0xf bank_mask:0xf\n"
"v_accvgpr_read_b32 v253, a210\n"
"v_mul_f32_dpp v26, v250, v26 row_newbcast:2 row_mask:0xf bank_mask:0xf\n"
"v_mul_f32_dpp v28, v250, v28 row_newbcast:0 row_mask:0xf bank_mask:0xf\n"
"v_mul_f32_dpp v81, v245, v81 row_newbcast:1 row_mask:0xf bank_mask:0xf\n"
"v_mul_f32_dpp v62, v250, v62 row_newbcast:2 row_mask:0xf bank_mask:0xf\n"
"v_exp_f32 v153, v153\n"
"v_exp_f32 v155, v155\n"
"v_mfma_f32_16x16x32_bf16 v[72:75], v[144:147], a[244:247], 0\n"
"v_exp_f32 v160, v160\n"
"v_mfma_f32_16x16x32_bf16 v[72:75], v[148:151], a[248:251], v[72:75]\n"
"s_waitcnt lgkmcnt(2)\n"
"ds_read_b128 v[208:211], v228 offset:20480\n"
"v_mul_f32_dpp v24, v250, v24 row_newbcast:0 row_mask:0xf bank_mask:0xf\n"
"v_mfma_f32_16x16x32_bf16 v[76:79], v[148:151], v[224:227], 0\n"
"s_waitcnt lgkmcnt(1)\n"
"v_exp_f32 v154, v154\n"
"v_mfma_f32_16x16x32_bf16 v[76:79], v[144:147], a[252:255], v[76:79]\n"
"ds_read_b128 v[212:215], v229 offset:20480\n"
"v_mul_f32_dpp v27, v250, v27 row_newbcast:3 row_mask:0xf bank_mask:0xf\n"
"v_fmac_f32_dpp v24, v252, v249 row_newbcast:0 row_mask:0xf bank_mask:0xf\n"
"v_fmac_f32_dpp v25, v252, v206 row_newbcast:1 row_mask:0xf bank_mask:0xf\n"
"v_accvgpr_read_b32 v249, a212\n"
"v_cvt_pk_bf16_f32 v24, v24, v25\n"
"v_mul_f32_dpp v51, v244, v51 row_newbcast:3 row_mask:0xf bank_mask:0xf\n"
"v_exp_f32 v162, v162\n"
"v_mfma_f32_16x16x32_bf16 v[44:47], v[140:143], v[224:227], 0\n"
"v_mul_f32_dpp v22, v243, v22 row_newbcast:2 row_mask:0xf bank_mask:0xf\n"
"v_mul_f32_dpp v56, v250, v56 row_newbcast:0 row_mask:0xf bank_mask:0xf\n"
"v_mfma_f32_16x16x32_bf16 v[44:47], v[136:139], a[252:255], v[44:47]\n"
"ds_read_b128 v[216:219], v228 offset:21504\n"
"v_mul_f32_dpp v29, v250, v29 row_newbcast:1 row_mask:0xf bank_mask:0xf\n"
"v_mfma_f32_16x16x32_bf16 v[12:15], v[132:135], v[224:227], 0\n"
"s_add_u32 s40, s56, s52\n"
"v_fmac_f32_dpp v27, v252, v251 row_newbcast:3 row_mask:0xf bank_mask:0xf\n"
"v_mul_f32_dpp v50, v244, v50 row_newbcast:2 row_mask:0xf bank_mask:0xf\n"
"v_mfma_f32_16x16x32_bf16 v[12:15], v[128:131], a[252:255], v[12:15]\n"
"buffer_load_dwordx4 v240, s[20:23], s40 offen offset:1024 nt lds\n"
"v_accvgpr_read_b32 v206, a213\n"
"v_mul_f32_dpp v30, v250, v30 row_newbcast:2 row_mask:0xf bank_mask:0xf\n"
"v_accvgpr_read_b32 v251, a215\n"
"v_fmac_f32_dpp v28, v252, v249 row_newbcast:0 row_mask:0xf bank_mask:0xf\n"
"v_mul_f32_dpp v19, v243, v19 row_newbcast:3 row_mask:0xf bank_mask:0xf\n"
"v_mul_f32_dpp v57, v250, v57 row_newbcast:1 row_mask:0xf bank_mask:0xf\n"
"v_mul_f32_dpp v61, v250, v61 row_newbcast:1 row_mask:0xf bank_mask:0xf\n"
"v_mfma_f32_16x16x32_bf16 a[220:223], v[124:127], a[252:255], 0\n"
"v_fmac_f32_dpp v26, v252, v253 row_newbcast:2 row_mask:0xf bank_mask:0xf\n"
"v_accvgpr_read_b32 v253, a214\n"
"v_mul_f32_dpp v31, v250, v31 row_newbcast:3 row_mask:0xf bank_mask:0xf\n"
"v_fmac_f32_dpp v29, v252, v206 row_newbcast:1 row_mask:0xf bank_mask:0xf\n"
"v_cvt_pk_bf16_f32 v25, v26, v27\n"
"v_mul_f32_dpp v55, v244, v55 row_newbcast:3 row_mask:0xf bank_mask:0xf\n"
"v_accvgpr_read_b32 v249, a224\n"
"v_mul_f32_dpp v63, v250, v63 row_newbcast:3 row_mask:0xf bank_mask:0xf\n"
"v_exp_f32 v165, v165\n"
"v_smfmac_f32_16x16x64_bf16 v[196:199], a[240:243], v[128:135], v241\n"
"v_accvgpr_read_b32 v206, a225\n"
"v_mul_f32_dpp v59, v250, v59 row_newbcast:3 row_mask:0xf bank_mask:0xf\n"
"v_mfma_f32_16x16x32_bf16 v[202:205], a[240:243], v[124:127], v[202:205]\n"
"s_waitcnt lgkmcnt(0)\n"
"ds_read_b64_tr_b16 a[248:249], v238 offset:1024\n"
"ds_read_b64_tr_b16 v[224:225], v239 offset:1024\n"
"v_fmac_f32_dpp v30, v252, v253 row_newbcast:2 row_mask:0xf bank_mask:0xf\n"
"v_exp_f32 v152, v152\n"
"v_cvt_pk_bf16_f32 v97, v154, v155\n"
"v_exp_f32 v158, v158\n"
"v_exp_f32 v166, v166\n"
"v_mfma_f32_16x16x32_bf16 v[168:171], v[216:219], a[8:11], v[180:183]\n"
"v_fmac_f32_dpp v31, v252, v251 row_newbcast:3 row_mask:0xf bank_mask:0xf\n"
"v_mul_f32_dpp v60, v250, v60 row_newbcast:0 row_mask:0xf bank_mask:0xf\n"
"v_mfma_f32_16x16x32_bf16 v[168:171], v[220:223], a[12:15], v[168:171]\n"
"v_mul_f32_dpp v58, v250, v58 row_newbcast:2 row_mask:0xf bank_mask:0xf\n"
"v_cvt_pk_bf16_f32 v96, v152, v153\n"
"v_mfma_f32_16x16x32_bf16 v[168:171], v[208:211], a[0:3], v[168:171]\n"
"v_cvt_pk_bf16_f32 v27, v30, v31\n"
"v_fmac_f32_dpp v56, v252, v249 row_newbcast:0 row_mask:0xf bank_mask:0xf\n"
"v_mfma_f32_16x16x32_bf16 v[168:171], v[212:215], a[4:7], v[168:171]\n"
"ds_read_b64_tr_b16 v[226:227], v239 offset:5120\n"
"v_cvt_pk_bf16_f32 v48, v48, v49\n"
"v_accvgpr_read_b32 v251, a227\n"
"v_accvgpr_read_b32 v249, a228\n"
"v_exp_f32 v156, v156\n"
"v_exp_f32 v161, v161\n"
"v_mfma_f32_16x16x32_bf16 v[172:175], v[208:211], a[16:19], v[184:187]\n"
"ds_read_b64_tr_b16 a[244:245], v234 offset:1024\n"
"v_xor_b32 v247, s75, v247\n"
"v_mfma_f32_16x16x32_bf16 v[172:175], v[212:215], a[20:23], v[172:175]\n"
"ds_read_b64_tr_b16 a[246:247], v234 offset:5120\n"
"v_cvt_pk_bf16_f32 v26, v28, v29\n"
"v_mfma_f32_16x16x32_bf16 v[172:175], v[216:219], a[24:27], v[172:175]\n"
"s_add_u32 s40, s56, s53\n"
"ds_read_b64_tr_b16 a[250:251], v238 offset:5120\n"
"v_fmac_f32_dpp v60, v252, v249 row_newbcast:0 row_mask:0xf bank_mask:0xf\n"
"v_mfma_f32_16x16x32_bf16 v[172:175], v[220:223], a[28:31], v[172:175]\n"
"buffer_load_dwordx4 v240, s[20:23], s40 offen offset:2048 nt lds\n"
"v_accvgpr_read_b32 v253, a226\n"
"v_fmac_f32_dpp v57, v252, v206 row_newbcast:1 row_mask:0xf bank_mask:0xf\n"
"v_fmac_f32_dpp v59, v252, v251 row_newbcast:3 row_mask:0xf bank_mask:0xf\n"
"v_accvgpr_read_b32 v206, a229\n"
"v_accvgpr_read_b32 v251, a231\n"
"v_cvt_pk_bf16_f32 v56, v56, v57\n"
"v_exp_f32 v171, v171\n"
"v_mfma_f32_16x16x32_bf16 v[176:179], v[216:219], a[40:43], v[188:191]\n"
"v_mul_f32_dpp v17, v243, v17 row_newbcast:1 row_mask:0xf bank_mask:0xf\n"
"v_fmac_f32_dpp v63, v252, v251 row_newbcast:3 row_mask:0xf bank_mask:0xf\n"
"v_mfma_f32_16x16x32_bf16 v[176:179], v[220:223], a[44:47], v[176:179]\n"
"v_fmac_f32_dpp v58, v252, v253 row_newbcast:2 row_mask:0xf bank_mask:0xf\n"
"v_accvgpr_read_b32 v253, a230\n"
"v_mfma_f32_16x16x32_bf16 v[176:179], v[208:211], a[32:35], v[176:179]\n"
"ds_read_b64_tr_b16 a[254:255], v235 offset:5120\n"
"v_fmac_f32_dpp v62, v252, v253 row_newbcast:2 row_mask:0xf bank_mask:0xf\n"
"v_mfma_f32_16x16x32_bf16 v[176:179], v[212:215], a[36:39], v[176:179]\n"
"ds_write_b128 v207, v[24:27] offset:0\n"
"v_mul_f32_dpp v53, v244, v53 row_newbcast:1 row_mask:0xf bank_mask:0xf\n"
"v_cvt_pk_bf16_f32 v57, v58, v59\n"
"v_cvt_pk_bf16_f32 v59, v62, v63\n"
"v_exp_f32 v157, v157\n"
"v_mfma_f32_16x16x32_bf16 v[152:155], v[208:211], a[192:195], v[192:195]\n"
"v_fmac_f32_dpp v61, v252, v206 row_newbcast:1 row_mask:0xf bank_mask:0xf\n"
"v_cvt_pk_bf16_f32 v58, v60, v61\n"
"v_mfma_f32_16x16x32_bf16 v[152:155], v[212:215], a[196:199], v[152:155]\n"
"ds_write_b128 v207, v[56:59] offset:2048\n"
"v_cvt_pk_bf16_f32 v49, v50, v51\n"
"v_cvt_pk_bf16_f32 v80, v80, v81\n"
"v_exp_f32 v164, v164\n"
"v_mfma_f32_16x16x32_bf16 v[152:155], v[216:219], a[200:203], v[152:155]\n"
"ds_read_b64_tr_b16 a[252:253], v235 offset:1024\n"
"v_cvt_pk_bf16_f32 v81, v82, v83\n"
"v_mfma_f32_16x16x32_bf16 v[152:155], v[220:223], a[204:207], v[152:155]\n"
"s_waitcnt lgkmcnt(4)\n"
"ds_read_b128 v[208:211], v230 offset:16384\n"
"v_mul_f32_dpp v20, v243, v20 row_newbcast:0 row_mask:0xf bank_mask:0xf\n"
"v_mfma_f32_16x16x32_bf16 a[232:235], v[124:127], a[244:247], 0\n"
"v_exp_f32 v170, v170\n"
"v_mfma_f32_16x16x32_bf16 v[24:27], v[128:131], a[244:247], 0\n"
"s_add_u32 s40, s56, s54\n"
"v_exp_f32 v159, v159\n"
"v_mfma_f32_16x16x32_bf16 v[24:27], v[132:135], a[248:251], v[24:27]\n"
"buffer_load_dwordx4 v240, s[20:23], s40 offen offset:3072 nt lds\n"
"v_cvt_pk_bf16_f32 v16, v16, v17\n"
"v_cvt_pk_bf16_f32 v113, v162, v163\n"
"v_exp_f32 v168, v168\n"
"v_exp_f32 v174, v174\n"
"v_exp_f32 v175, v175\n"
"v_mfma_f32_16x16x32_bf16 v[56:59], v[136:139], a[244:247], 0\n"
"s_add_u32 s55, s55, s59\n"
"v_exp_f32 v173, v173\n"
"v_mfma_f32_16x16x32_bf16 v[56:59], v[140:143], a[248:251], v[56:59]\n"
"s_add_u32 s41, s55, s57\n"
"v_exp_f32 v167, v167\n"
"v_mfma_f32_16x16x32_bf16 v[88:91], v[144:147], a[244:247], 0\n"
"s_and_b32 s41, s41, s58\n"
"ds_read_b128 v[216:219], v230 offset:17408\n"
"v_cvt_pk_bf16_f32 v104, v156, v157\n"
"v_mfma_f32_16x16x32_bf16 v[88:91], v[148:151], a[248:251], v[88:91]\n"
"s_andn2_b32 s56, s55, s58\n"
"ds_read_b128 v[220:223], v231 offset:17408\n"
"v_cvt_pk_bf16_f32 v105, v158, v159\n"
"v_mfma_f32_16x16x32_bf16 v[92:95], v[148:151], v[224:227], 0\n"
"s_waitcnt lgkmcnt(2)\n"
"s_or_b32 s56, s56, s41\n"
"v_cvt_pk_bf16_f32 v82, v84, v85\n"
"v_cvt_pk_bf16_f32 v120, v164, v165\n"
"v_mfma_f32_16x16x32_bf16 v[92:95], v[144:147], a[252:255], v[92:95]\n"
"ds_read_b128 v[212:215], v231 offset:16384\n"
"v_mul_f32_dpp v52, v244, v52 row_newbcast:0 row_mask:0xf bank_mask:0xf\n"
"v_mfma_f32_16x16x32_bf16 v[60:63], v[140:143], v[224:227], 0\n"
"s_add_u32 m0, s44, 49152\n"
"v_cvt_pk_bf16_f32 v112, v160, v161\n"
"v_cvt_pk_bf16_f32 v121, v166, v167\n"
"v_mfma_f32_16x16x32_bf16 v[60:63], v[136:139], a[252:255], v[60:63]\n"
"buffer_load_dwordx4 v240, s[16:19], s56 offen nt lds\n"
"v_mfma_f32_16x16x32_bf16 v[28:31], v[132:135], v[224:227], 0\n"
"v_exp_f32 v179, v179\n"
"v_mfma_f32_16x16x32_bf16 v[28:31], v[128:131], a[252:255], v[28:31]\n"
"v_exp_f32 v172, v172\n"
"v_mfma_f32_16x16x32_bf16 a[236:239], v[124:127], a[252:255], 0\n"
"ds_read_b64_tr_b16 a[250:251], v236 offset:4096\n"
"ds_read_b64_tr_b16 v[226:227], v237 offset:4096\n"
"v_smfmac_f32_16x16x64_bf16 v[198:201], a[240:243], v[136:143], v241\n"
"ds_read_b64_tr_b16 a[254:255], v233 offset:4096\n"
"v_mul_f32_dpp v54, v244, v54 row_newbcast:2 row_mask:0xf bank_mask:0xf\n"
"v_mfma_f32_16x16x32_bf16 v[156:159], v[208:211], a[0:3], v[180:183]\n"
"s_waitcnt lgkmcnt(3)\n"
"v_exp_f32 v176, v176\n"
"v_mfma_f32_16x16x32_bf16 v[156:159], v[212:215], a[4:7], v[156:159]\n"
"v_exp_f32 v178, v178\n"
"v_mfma_f32_16x16x32_bf16 v[156:159], v[216:219], a[8:11], v[156:159]\n"
"ds_read_b64_tr_b16 a[244:245], v232 offset:0\n"
"ds_read_b64_tr_b16 a[246:247], v232 offset:4096\n"
"v_mfma_f32_16x16x32_bf16 v[156:159], v[220:223], a[12:15], v[156:159]\n"
"v_exp_f32 v169, v169\n"
"v_mfma_f32_16x16x32_bf16 v[160:163], v[216:219], a[24:27], v[184:187]\n"
"ds_read_b64_tr_b16 a[248:249], v236 offset:0\n"
"v_cvt_pk_bf16_f32 v51, v54, v55\n"
"v_mfma_f32_16x16x32_bf16 v[160:163], v[220:223], a[28:31], v[160:163]\n"
"v_cvt_pk_bf16_f32 v17, v18, v19\n"
"v_cvt_pk_bf16_f32 v18, v20, v21\n"
"v_mfma_f32_16x16x32_bf16 v[160:163], v[208:211], a[16:19], v[160:163]\n"
"s_add_u32 s40, s56, s52\n"
"ds_read_b64_tr_b16 v[224:225], v237 offset:0\n"
"v_mul_f32_dpp v86, v245, v86 row_newbcast:2 row_mask:0xf bank_mask:0xf\n"
"v_mfma_f32_16x16x32_bf16 v[160:163], v[212:215], a[20:23], v[160:163]\n"
"buffer_load_dwordx4 v240, s[16:19], s40 offen offset:1024 nt lds\n"
"v_mfma_f32_16x16x32_bf16 v[164:167], v[208:211], a[32:35], v[188:191]\n"
"v_exp_f32 v156, v156\n"
"v_mfma_f32_16x16x32_bf16 v[164:167], v[212:215], a[36:39], v[164:167]\n"
"v_exp_f32 v158, v158\n"
"v_mfma_f32_16x16x32_bf16 v[164:167], v[216:219], a[40:43], v[164:167]\n"
"ds_read_b64_tr_b16 a[252:253], v233 offset:0\n"
"ds_read_b128 v[208:211], v230 offset:20480\n"
"v_mfma_f32_16x16x32_bf16 v[164:167], v[220:223], a[44:47], v[164:167]\n"
"s_waitcnt lgkmcnt(4)\n"
"v_cvt_pk_bf16_f32 v83, v86, v87\n"
"v_cvt_pk_bf16_f32 v107, v174, v175\n"
"v_mfma_f32_16x16x32_bf16 a[208:211], v[124:127], a[244:247], 0\n"
"ds_read_b128 v[220:223], v231 offset:21504\n"
"v_cvt_pk_bf16_f32 v98, v168, v169\n"
"v_mfma_f32_16x16x32_bf16 v[0:3], v[128:131], a[244:247], 0\n"
"s_waitcnt lgkmcnt(4)\n"
"v_exp_f32 v177, v177\n"
"v_mfma_f32_16x16x32_bf16 v[0:3], v[132:135], a[248:251], v[0:3]\n"
"ds_read_b128 v[212:215], v231 offset:20480\n"
"v_cvt_pk_bf16_f32 v99, v170, v171\n"
"v_mfma_f32_16x16x32_bf16 v[32:35], v[136:139], a[244:247], 0\n"
"s_add_u32 s40, s56, s53\n"
"v_cvt_pk_bf16_f32 v19, v22, v23\n"
"v_cvt_pk_bf16_f32 v50, v52, v53\n"
"v_mfma_f32_16x16x32_bf16 v[32:35], v[140:143], a[248:251], v[32:35]\n"
"buffer_load_dwordx4 v240, s[16:19], s40 offen offset:2048 nt lds\n"
"v_mfma_f32_16x16x32_bf16 v[64:67], v[144:147], a[244:247], 0\n"
"v_exp_f32 v163, v163\n"
"v_mfma_f32_16x16x32_bf16 v[64:67], v[148:151], a[248:251], v[64:67]\n"
"s_waitcnt lgkmcnt(2)\n"
"ds_write_b128 v246, v[48:51] offset:50176\n"
"v_cvt_pk_bf16_f32 v106, v172, v173\n"
"v_exp_f32 v153, v153\n"
"v_exp_f32 v164, v164\n"
"v_mfma_f32_16x16x32_bf16 v[68:71], v[148:151], v[224:227], 0\n"
"v_exp_f32 v162, v162\n"
"v_mfma_f32_16x16x32_bf16 v[68:71], v[144:147], a[252:255], v[68:71]\n"
"ds_write_b128 v246, v[16:19] offset:49152\n"
"v_exp_f32 v152, v152\n"
"v_exp_f32 v154, v154\n"
"v_mfma_f32_16x16x32_bf16 v[36:39], v[140:143], v[224:227], 0\n"
"ds_read_b64_tr_b16 a[56:57], v247 offset:49152\n"
"ds_read_b64_tr_b16 a[58:59], v247 offset:49160\n"
"v_mfma_f32_16x16x32_bf16 v[36:39], v[136:139], a[252:255], v[36:39]\n"
"ds_read_b128 v[216:219], v230 offset:21504\n"
"ds_read_b64_tr_b16 a[244:245], v232 offset:1024\n"
"v_mfma_f32_16x16x32_bf16 v[4:7], v[132:135], v[224:227], 0\n"
"ds_read_b64_tr_b16 a[72:73], v247 offset:50176\n"
"ds_read_b64_tr_b16 a[246:247], v232 offset:5120\n"
"v_mfma_f32_16x16x32_bf16 v[4:7], v[128:131], a[252:255], v[4:7]\n"
"ds_read_b64_tr_b16 a[74:75], v247 offset:50184\n"
"ds_read_b64_tr_b16 a[250:251], v236 offset:5120\n"
"v_mfma_f32_16x16x32_bf16 a[212:215], v[124:127], a[252:255], 0\n"
"s_add_u32 s40, s56, s54\n"
"ds_write_b128 v246, v[80:83] offset:51200\n"
"v_exp_f32 v160, v160\n"
"v_smfmac_f32_16x16x64_bf16 v[200:203], a[240:243], v[144:151], v241\n"
"buffer_load_dwordx4 v240, s[16:19], s40 offen offset:3072 nt lds\n"
"v_mfma_f32_16x16x32_bf16 v[168:171], v[208:211], a[0:3], v[180:183]\n"
"s_waitcnt lgkmcnt(10)\n"
"v_exp_f32 v161, v161\n"
"v_mfma_f32_16x16x32_bf16 v[168:171], v[212:215], a[4:7], v[168:171]\n"
"s_waitcnt lgkmcnt(6)\n"
"ds_read_b64_tr_b16 a[88:89], v247 offset:51200\n"
"ds_read_b64_tr_b16 a[254:255], v233 offset:5120\n"
"v_mfma_f32_16x16x32_bf16 v[168:171], v[216:219], a[8:11], v[168:171]\n"
"ds_read_b64_tr_b16 a[90:91], v247 offset:51208\n"
"v_cvt_pk_bf16_f32 v114, v176, v177\n"
"v_mfma_f32_16x16x32_bf16 v[168:171], v[220:223], a[12:15], v[168:171]\n"
"v_exp_f32 v165, v165\n"
"v_mfma_f32_16x16x32_bf16 v[172:175], v[216:219], a[24:27], v[184:187]\n"
"ds_read_b64_tr_b16 a[248:249], v236 offset:1024\n"
"v_cvt_pk_bf16_f32 v115, v178, v179\n"
"v_mfma_f32_16x16x32_bf16 v[172:175], v[220:223], a[28:31], v[172:175]\n"
"v_exp_f32 v155, v155\n"
"v_mfma_f32_16x16x32_bf16 v[172:175], v[208:211], a[16:19], v[172:175]\n"
"v_exp_f32 v166, v166\n"
"v_mfma_f32_16x16x32_bf16 v[172:175], v[212:215], a[20:23], v[172:175]\n"
"s_cbranch_vccnz LPCT1_%=\nLPKT1_%=:\n"
"v_xor_b32 v246, s75, v246\n"
"v_mfma_f32_16x16x32_bf16 v[176:179], v[208:211], a[32:35], v[188:191]\n"
"ds_read_b64_tr_b16 a[252:253], v233 offset:1024\n"
"v_cvt_pk_bf16_f32 v109, v162, v163\n"
"v_mfma_f32_16x16x32_bf16 v[176:179], v[212:215], a[36:39], v[176:179]\n"
"v_exp_f32 v157, v157\n"
"v_mfma_f32_16x16x32_bf16 v[176:179], v[216:219], a[40:43], v[176:179]\n"
"ds_read_b64_tr_b16 v[224:225], v237 offset:1024\n"
"v_cvt_pk_bf16_f32 v100, v156, v157\n"
"v_mfma_f32_16x16x32_bf16 v[176:179], v[220:223], a[44:47], v[176:179]\n"
"s_waitcnt lgkmcnt(3)\n"
"ds_read_b64_tr_b16 v[226:227], v237 offset:5120\n"
"v_mfma_f32_16x16x32_bf16 a[224:227], v[124:127], a[244:247], 0\n"
"v_cvt_pk_bf16_f32 v122, v152, v153\n"
"v_cvt_pk_bf16_f32 v108, v160, v161\n"
"v_mfma_f32_16x16x32_bf16 v[16:19], v[128:131], a[244:247], 0\n"
"s_waitcnt vmcnt(8) lgkmcnt(3)\n"
"v_xor_b32 v247, s75, v247\n"
"v_cvt_pk_bf16_f32 v123, v154, v155\n"
"v_mfma_f32_16x16x32_bf16 v[16:19], v[132:135], a[248:251], v[16:19]\n"
"s_barrier\n"
"ds_read_b128 v[208:211], v228 offset:32768\n"
"ds_read_b128 v[216:219], v228 offset:33792\n"
"v_exp_f32 v159, v159\n"
"v_mfma_f32_16x16x32_bf16 v[48:51], v[136:139], a[244:247], 0\n"
"v_exp_f32 v167, v167\n"
"v_mfma_f32_16x16x32_bf16 v[48:51], v[140:143], a[248:251], v[48:51]\n"
"ds_read_b128 v[212:215], v229 offset:32768\n"
"ds_read_b128 v[220:223], v229 offset:33792\n"
"v_mfma_f32_16x16x32_bf16 v[80:83], v[144:147], a[244:247], 0\n"
"ds_read_b64_tr_b16 a[244:245], v234 offset:16384\n"
"ds_read_b64_tr_b16 a[246:247], v234 offset:20480\n"
"v_mfma_f32_16x16x32_bf16 v[80:83], v[148:151], a[248:251], v[80:83]\n"
"s_waitcnt lgkmcnt(6)\n"
"ds_read_b64_tr_b16 a[248:249], v238 offset:16384\n"
"ds_read_b64_tr_b16 a[250:251], v238 offset:20480\n"
"v_mfma_f32_16x16x32_bf16 v[84:87], v[148:151], v[224:227], 0\n"
"v_exp_f32 v175, v175\n"
"v_mfma_f32_16x16x32_bf16 v[84:87], v[144:147], a[252:255], v[84:87]\n"
"v_exp_f32 v173, v173\n"
"v_mfma_f32_16x16x32_bf16 v[52:55], v[140:143], v[224:227], 0\n"
"v_exp_f32 v169, v169\n"
"v_mfma_f32_16x16x32_bf16 v[52:55], v[136:139], a[252:255], v[52:55]\n"
"v_cvt_pk_bf16_f32 v101, v158, v159\n"
"v_cvt_pk_bf16_f32 v116, v164, v165\n"
"v_mfma_f32_16x16x32_bf16 v[20:23], v[132:135], v[224:227], 0\n"
"ds_read_b64_tr_b16 v[224:225], v239 offset:16384\n"
"v_cvt_pk_bf16_f32 v117, v166, v167\n"
"v_mfma_f32_16x16x32_bf16 v[20:23], v[128:131], a[252:255], v[20:23]\n"
"v_exp_f32 v172, v172\n"
"v_mfma_f32_16x16x32_bf16 a[228:231], v[124:127], a[252:255], 0\n"
"s_waitcnt lgkmcnt(3)\n"
"ds_read_b64_tr_b16 a[254:255], v235 offset:20480\n"
"s_branch LAY2_%=\n"
"LAX_%=:\n"
"v_mfma_f32_16x16x32_bf16 a[224:227], v[124:127], a[244:247], a[224:227]\n"
"v_exp_f32 v171, v171\n"
"v_mfma_f32_16x16x32_bf16 v[16:19], v[128:131], a[244:247], v[16:19]\n"
"s_waitcnt vmcnt(12)\n"
"v_exp_f32 v168, v168\n"
"v_mfma_f32_16x16x32_bf16 v[16:19], v[132:135], a[248:251], v[16:19]\n"
"s_barrier\n"
"v_mfma_f32_16x16x32_bf16 v[48:51], v[136:139], a[244:247], v[48:51]\n"
"v_exp_f32 v175, v175\n"
"v_mfma_f32_16x16x32_bf16 v[48:51], v[140:143], a[248:251], v[48:51]\n"
"v_exp_f32 v170, v170\n"
"v_mfma_f32_16x16x32_bf16 v[80:83], v[144:147], a[244:247], v[80:83]\n"
"ds_read_b64_tr_b16 a[246:247], v234 offset:53248\n"
"v_cvt_pk_bf16_f32 v108, v160, v161\n"
"v_mfma_f32_16x16x32_bf16 v[80:83], v[148:151], a[248:251], v[80:83]\n"
"ds_read_b64_tr_b16 a[244:245], v234 offset:49152\n"
"ds_read_b64_tr_b16 a[250:251], v238 offset:53248\n"
"v_exp_f32 v169, v169\n"
"v_exp_f32 v172, v172\n"
"v_exp_f32 v174, v174\n"
"v_exp_f32 v177, v177\n"
"v_mfma_f32_16x16x32_bf16 v[84:87], v[148:151], v[224:227], v[84:87]\n"
"ds_read_b64_tr_b16 a[248:249], v238 offset:49152\n"
"v_cvt_pk_bf16_f32 v103, v170, v171\n"
"v_mfma_f32_16x16x32_bf16 v[84:87], v[144:147], a[252:255], v[84:87]\n"
"v_cvt_pk_bf16_f32 v100, v156, v157\n"
"v_cvt_pk_bf16_f32 v117, v166, v167\n"
"v_mfma_f32_16x16x32_bf16 v[52:55], v[140:143], v[224:227], v[52:55]\n"
"v_cvt_pk_bf16_f32 v116, v164, v165\n"
"v_cvt_pk_bf16_f32 v102, v168, v169\n"
"v_mfma_f32_16x16x32_bf16 v[52:55], v[136:139], a[252:255], v[52:55]\n"
"v_exp_f32 v178, v178\n"
"v_mfma_f32_16x16x32_bf16 v[20:23], v[132:135], v[224:227], v[20:23]\n"
"v_exp_f32 v179, v179\n"
"v_mfma_f32_16x16x32_bf16 v[20:23], v[128:131], a[252:255], v[20:23]\n"
"v_exp_f32 v176, v176\n"
"v_mfma_f32_16x16x32_bf16 a[228:231], v[124:127], a[252:255], a[228:231]\n"
"s_waitcnt lgkmcnt(2)\n"
"v_exp_f32 v173, v173\n"
"v_mfma_f32_16x16x32_bf16 a[216:219], v[120:123], a[244:247], a[216:219]\n"
"s_mov_b64 s[28:29], s[8:9]\n"
"ds_read_b64_tr_b16 v[224:225], v239 offset:49152\n"
"v_cvt_pk_bf16_f32 v119, v178, v179\n"
"v_smfmac_f32_16x16x64_bf16 v[196:199], a[240:243], v[96:103], v241\n"
"s_mov_b32 s30, 655360\n"
"ds_read_b64_tr_b16 a[252:253], v235 offset:49152\n"
"v_cvt_pk_bf16_f32 v110, v172, v173\n"
"v_mfma_f32_16x16x32_bf16 v[8:11], v[96:99], a[244:247], v[8:11]\n"
"s_waitcnt lgkmcnt(2)\n"
"s_mov_b32 s71, s44\n"
"ds_read_b64_tr_b16 v[226:227], v239 offset:53248\n"
"v_cvt_pk_bf16_f32 v118, v176, v177\n"
"v_mfma_f32_16x16x32_bf16 v[8:11], v[100:103], a[248:251], v[8:11]\n"
"s_mov_b32 s70, s95\n"
"ds_read_b64_tr_b16 a[254:255], v235 offset:53248\n"
"v_and_b32 v255, 0x3f0, v242\n"
"v_smfmac_f32_16x16x64_bf16 v[200:203], a[240:243], v[112:119], v241\n"
"v_cvt_pk_bf16_f32 v109, v162, v163\n"
"v_cvt_pk_bf16_f32 v111, v174, v175\n"
"v_mfma_f32_16x16x32_bf16 v[40:43], v[104:107], a[244:247], v[40:43]\n"
"v_lshrrev_b32 v255, 1, v255\n"
"v_sub_u32 v255, v242, v255\n"
"v_mfma_f32_16x16x32_bf16 v[40:43], v[108:111], a[248:251], v[40:43]\n"
"s_add_u32 s41, s70, 327680\n"
"s_add_u32 m0, s71, 32768\n"
"v_xor_b32 v250, 0x1000, v255\n"
"v_smfmac_f32_16x16x64_bf16 v[198:201], a[240:243], v[104:111], v241\n"
"buffer_load_dwordx4 v240, s[28:31], s41 offen lds\n"
"v_mfma_f32_16x16x32_bf16 v[72:75], v[112:115], a[244:247], v[72:75]\n"
"v_add_f32 v196, v196, v197\n"
"v_cvt_f32_u32 v254, s43\n"
"v_mfma_f32_16x16x32_bf16 v[72:75], v[116:119], a[248:251], v[72:75]\n"
"ds_read_b64_tr_b16 a[244:245], v234 offset:50176\n"
"ds_read_b64_tr_b16 a[250:251], v238 offset:54272\n"
"v_mfma_f32_16x16x32_bf16 v[202:205], a[240:243], v[120:123], v[202:205]\n"
"s_waitcnt lgkmcnt(3)\n"
"ds_read_b64_tr_b16 a[246:247], v234 offset:54272\n"
"v_add_f32 v198, v198, v199\n"
"v_mfma_f32_16x16x32_bf16 v[76:79], v[116:119], v[224:227], v[76:79]\n"
"s_waitcnt lgkmcnt(3)\n"
"s_add_u32 s40, s41, s52\n"
"ds_read_b64_tr_b16 a[248:249], v238 offset:50176\n"
"v_add_f32 v249, v196, v198\n"
"v_mfma_f32_16x16x32_bf16 v[76:79], v[112:115], a[252:255], v[76:79]\n"
"buffer_load_dwordx4 v240, s[28:31], s40 offen offset:1024 lds\n"
"v_mfma_f32_16x16x32_bf16 v[44:47], v[108:111], v[224:227], v[44:47]\n"
"v_add_f32 v254, v254, v202\n"
"v_min3_f32 v254, v254, v196, v198\n"
"v_mfma_f32_16x16x32_bf16 v[44:47], v[104:107], a[252:255], v[44:47]\n"
"v_add_f32 v200, v200, v201\n"
"v_min_f32 v254, v254, v200\n"
"v_rcp_f32 v254, v254\n"
"v_mfma_f32_16x16x32_bf16 v[12:15], v[100:103], v[224:227], v[12:15]\n"
"s_add_u32 s40, s41, s53\n"
"ds_read_b64_tr_b16 v[224:225], v239 offset:50176\n"
"v_add_f32 v249, v249, v200\n"
"v_mfma_f32_16x16x32_bf16 v[12:15], v[96:99], a[252:255], v[12:15]\n"
"buffer_load_dwordx4 v240, s[28:31], s40 offen offset:2048 lds\n"
"v_mfma_f32_16x16x32_bf16 a[220:223], v[120:123], a[252:255], a[220:223]\n"
"s_waitcnt lgkmcnt(2)\n"
"v_add_f32 v249, v249, v202\n"
"v_fmac_f32 v249, 0x53800000, v254\n"
"v_cmp_nge_f32 vcc, s46, v249\n"
"s_cbranch_vccnz LSLOW_%=\nLBACK3_%=:\ns_mov_b64 vcc, s[64:65]\n"
"v_mfma_f32_16x16x32_bf16 a[232:235], v[120:123], a[244:247], a[232:235]\n"
"ds_read_b64_tr_b16 a[252:253], v235 offset:50176\n"
"ds_read_b64_tr_b16 a[254:255], v235 offset:54272\n"
"ds_read_b64_tr_b16 v[226:227], v239 offset:54272\n"
"v_rcp_f32 v244, v198\n"
"v_rcp_f32 v245, v200\n"
"v_mfma_f32_16x16x32_bf16 v[24:27], v[96:99], a[244:247], v[24:27]\n"
"s_waitcnt lgkmcnt(4)\n"
"v_mov_b32_dpp v244, v244 row_ror:12 row_mask:0x2 bank_mask:0xf\n"
"v_xor_b32 v254, 0x11000, v242\n"
"v_mfma_f32_16x16x32_bf16 v[24:27], v[100:103], a[248:251], v[24:27]\n"
"ds_write_b128 v254, a[216:219] offset:32768\n"
"v_rcp_f32 v243, v196\n"
"v_mfma_f32_16x16x32_bf16 v[56:59], v[104:107], a[244:247], v[56:59]\n"
"s_add_u32 s40, s41, s54\n"
"v_mov_b32_dpp v243, v243 row_ror:12 row_mask:0x2 bank_mask:0xf\n"
"v_mov_b32_dpp v245, v245 row_ror:12 row_mask:0x2 bank_mask:0xf\n"
"v_mfma_f32_16x16x32_bf16 v[56:59], v[108:111], a[248:251], v[56:59]\n"
"buffer_load_dwordx4 v240, s[28:31], s40 offen offset:3072 lds\n"
"v_mfma_f32_16x16x32_bf16 v[88:91], v[112:115], a[244:247], v[88:91]\n"
"s_add_u32 s71, s44, s75\n"
"v_mov_b32_dpp v244, v244 row_ror:8 row_mask:0x4 bank_mask:0xf\n"
"v_mov_b32_dpp v245, v245 row_ror:8 row_mask:0x4 bank_mask:0xf\n"
"v_mfma_f32_16x16x32_bf16 v[88:91], v[116:119], a[248:251], v[88:91]\n"
"s_waitcnt lgkmcnt(1)\n"
"s_mov_b32 s70, s100\n"
"ds_read_b64_tr_b16 a[244:245], v232 offset:49152\n"
"ds_read_b64_tr_b16 a[248:249], v236 offset:49152\n"
"ds_read_b64_tr_b16 a[250:251], v236 offset:53248\n"
"v_mov_b32_dpp v243, v243 row_ror:8 row_mask:0x4 bank_mask:0xf\n"
"v_mov_b32_dpp v244, v244 row_ror:4 row_mask:0x8 bank_mask:0xf\n"
"v_mov_b32_dpp v245, v245 row_ror:4 row_mask:0x8 bank_mask:0xf\n"
"v_mfma_f32_16x16x32_bf16 v[92:95], v[116:119], v[224:227], v[92:95]\n"
"s_add_u32 s41, s70, 163840\n"
"s_add_u32 m0, s71, 16384\n"
"ds_read_b64_tr_b16 a[246:247], v232 offset:53248\n"
"v_mfma_f32_16x16x32_bf16 v[92:95], v[112:115], a[252:255], v[92:95]\n"
"buffer_load_dwordx4 v240, s[28:31], s41 offen lds\n"
"v_mfma_f32_16x16x32_bf16 v[60:63], v[108:111], v[224:227], v[60:63]\n"
"v_mov_b32_dpp v243, v243 row_ror:4 row_mask:0x8 bank_mask:0xf\n"
"v_bfe_u32 v249, v242, 9, 1\n"
"v_mfma_f32_16x16x32_bf16 v[60:63], v[104:107], a[252:255], v[60:63]\n"
"ds_write_b128 v254, a[232:235] offset:34816\n"
"v_lshl_add_u32 v249, v249, 4, s43\n"
"v_mul_f32_dpp v42, v244, v42 row_newbcast:2 row_mask:0xf bank_mask:0xf\n"
"v_mul_f32_dpp v72, v245, v72 row_newbcast:0 row_mask:0xf bank_mask:0xf\n"
"v_mfma_f32_16x16x32_bf16 v[28:31], v[100:103], v[224:227], v[28:31]\n"
"s_add_u32 s40, s41, s52\n"
"v_mov_b32 v253, v202\n"
"v_mul_f32_dpp v12, v243, v12 row_newbcast:0 row_mask:0xf bank_mask:0xf\n"
"v_mfma_f32_16x16x32_bf16 v[28:31], v[96:99], a[252:255], v[28:31]\n"
"buffer_load_dwordx4 v240, s[28:31], s40 offen offset:1024 lds\n"
"v_lshl_add_u32 v249, v249, 6, s75\n"
"v_mul_f32_dpp v40, v244, v40 row_newbcast:0 row_mask:0xf bank_mask:0xf\n"
"v_mul_f32_dpp v43, v244, v43 row_newbcast:3 row_mask:0xf bank_mask:0xf\n"
"v_mul_f32_dpp v75, v245, v75 row_newbcast:3 row_mask:0xf bank_mask:0xf\n"
"v_mul_f32_dpp v93, v245, v93 row_newbcast:1 row_mask:0xf bank_mask:0xf\n"
"v_mfma_f32_16x16x32_bf16 a[236:239], v[120:123], a[252:255], a[236:239]\n"
"s_waitcnt lgkmcnt(1)\n"
"v_mul_f32_dpp v74, v245, v74 row_newbcast:2 row_mask:0xf bank_mask:0xf\n"
"v_mul_f32_dpp v24, v243, v24 row_newbcast:0 row_mask:0xf bank_mask:0xf\n"
"v_mfma_f32_16x16x32_bf16 a[208:211], v[120:123], a[244:247], a[208:211]\n"
"ds_read_b64_tr_b16 a[252:253], v233 offset:49152\n"
"ds_read_b64_tr_b16 a[254:255], v233 offset:53248\n"
"ds_read_b64_tr_b16 v[224:225], v237 offset:49152\n"
"v_mul_f32_dpp v41, v244, v41 row_newbcast:1 row_mask:0xf bank_mask:0xf\n"
"v_mul_f32_dpp v44, v244, v44 row_newbcast:0 row_mask:0xf bank_mask:0xf\n"
"v_mul_f32_dpp v45, v244, v45 row_newbcast:1 row_mask:0xf bank_mask:0xf\n"
"v_mul_f32_dpp v46, v244, v46 row_newbcast:2 row_mask:0xf bank_mask:0xf\n"
"v_mul_f32_dpp v78, v245, v78 row_newbcast:2 row_mask:0xf bank_mask:0xf\n"
"v_mul_f32_dpp v59, v244, v59 row_newbcast:3 row_mask:0xf bank_mask:0xf\n"
"v_mul_f32_dpp v61, v244, v61 row_newbcast:1 row_mask:0xf bank_mask:0xf\n"
"v_mfma_f32_16x16x32_bf16 v[0:3], v[96:99], a[244:247], v[0:3]\n"
"ds_read_b64_tr_b16 v[226:227], v237 offset:53248\n"
"v_mul_f32_dpp v14, v243, v14 row_newbcast:2 row_mask:0xf bank_mask:0xf\n"
"v_mfma_f32_16x16x32_bf16 v[0:3], v[100:103], a[248:251], v[0:3]\n"
"ds_write_b128 v254, a[220:223] offset:33792\n"
"v_mov_b32 v252, v192\n"
"v_mul_f32_dpp v8, v243, v8 row_newbcast:0 row_mask:0xf bank_mask:0xf\n"
"v_mul_f32_dpp v9, v243, v9 row_newbcast:1 row_mask:0xf bank_mask:0xf\n"
"v_mul_f32_dpp v10, v243, v10 row_newbcast:2 row_mask:0xf bank_mask:0xf\n"
"v_cvt_pk_bf16_f32 v40, v40, v41\n"
"v_mfma_f32_16x16x32_bf16 v[32:35], v[104:107], a[244:247], v[32:35]\n"
"s_add_u32 s40, s41, s53\n"
"ds_write_b128 v254, a[236:239] offset:35840\n"
"v_sub_u32 v228, v232, v249\n"
"v_mul_f32_dpp v15, v243, v15 row_newbcast:3 row_mask:0xf bank_mask:0xf\n"
"v_mul_f32_dpp v25, v243, v25 row_newbcast:1 row_mask:0xf bank_mask:0xf\n"
"v_mul_f32_dpp v27, v243, v27 row_newbcast:3 row_mask:0xf bank_mask:0xf\n"
"v_mfma_f32_16x16x32_bf16 v[32:35], v[108:111], a[248:251], v[32:35]\n"
"buffer_load_dwordx4 v240, s[28:31], s40 offen offset:2048 lds\n"
"v_cvt_pk_bf16_f32 v41, v42, v43\n"
"v_mul_f32_dpp v73, v245, v73 row_newbcast:1 row_mask:0xf bank_mask:0xf\n"
"v_mul_f32_dpp v79, v245, v79 row_newbcast:3 row_mask:0xf bank_mask:0xf\n"
"v_mul_f32_dpp v57, v244, v57 row_newbcast:1 row_mask:0xf bank_mask:0xf\n"
"v_mul_f32_dpp v58, v244, v58 row_newbcast:2 row_mask:0xf bank_mask:0xf\n"
"v_mul_f32_dpp v60, v244, v60 row_newbcast:0 row_mask:0xf bank_mask:0xf\n"
"v_mfma_f32_16x16x32_bf16 v[64:67], v[112:115], a[244:247], v[64:67]\n"
"v_mul_f32_dpp v0, v243, v0 row_newbcast:0 row_mask:0xf bank_mask:0xf\n"
"v_mul_f32_dpp v2, v243, v2 row_newbcast:2 row_mask:0xf bank_mask:0xf\n"
"v_mfma_f32_16x16x32_bf16 v[64:67], v[116:119], a[248:251], v[64:67]\n"
"s_waitcnt lgkmcnt(2)\n"
"ds_read_b64_tr_b16 a[244:245], v232 offset:50176\n"
"ds_read_b64_tr_b16 a[246:247], v232 offset:54272\n"
"ds_read_b64_tr_b16 a[250:251], v236 offset:54272\n"
"v_sub_u32 v230, v234, v249\n"
"v_mul_f32_dpp v11, v243, v11 row_newbcast:3 row_mask:0xf bank_mask:0xf\n"
"v_cvt_pk_bf16_f32 v8, v8, v9\n"
"v_mul_f32_dpp v89, v245, v89 row_newbcast:1 row_mask:0xf bank_mask:0xf\n"
"v_xor_b32 v234, 0x1000, v230\n"
"v_mfma_f32_16x16x32_bf16 v[68:71], v[116:119], v[224:227], v[68:71]\n"
"s_add_u32 s40, s41, s54\n"
"ds_write_b64 v250, v[252:253] offset:52224\n"
"v_sub_u32 v231, v235, v249\n"
"v_cvt_pk_bf16_f32 v9, v10, v11\n"
"v_cvt_pk_bf16_f32 v11, v14, v15\n"
"v_cvt_pk_bf16_f32 v24, v24, v25\n"
"v_mul_f32_dpp v56, v244, v56 row_newbcast:0 row_mask:0xf bank_mask:0xf\n"
"v_mul_f32_dpp v94, v245, v94 row_newbcast:2 row_mask:0xf bank_mask:0xf\n"
"v_mfma_f32_16x16x32_bf16 v[68:71], v[112:115], a[252:255], v[68:71]\n"
"buffer_load_dwordx4 v240, s[28:31], s40 offen offset:3072 lds\n"
"v_sub_u32 v229, v233, v249\n"
"v_mul_f32_dpp v13, v243, v13 row_newbcast:1 row_mask:0xf bank_mask:0xf\n"
"v_cvt_pk_bf16_f32 v10, v12, v13\n"
"v_mul_f32_dpp v47, v244, v47 row_newbcast:3 row_mask:0xf bank_mask:0xf\n"
"v_mul_f32_dpp v26, v243, v26 row_newbcast:2 row_mask:0xf bank_mask:0xf\n"
"v_mul_f32_dpp v30, v243, v30 row_newbcast:2 row_mask:0xf bank_mask:0xf\n"
"v_cvt_pk_bf16_f32 v56, v56, v57\n"
"v_xor_b32 v232, 0x1000, v228\n"
"v_mfma_f32_16x16x32_bf16 v[36:39], v[108:111], v[224:227], v[36:39]\n"
"v_cvt_pk_bf16_f32 v72, v72, v73\n"
"v_mul_f32_dpp v88, v245, v88 row_newbcast:0 row_mask:0xf bank_mask:0xf\n"
"v_mfma_f32_16x16x32_bf16 v[36:39], v[104:107], a[252:255], v[36:39]\n"
"ds_write_b128 v246, v[8:11] offset:49152\n"
"v_cvt_pk_bf16_f32 v43, v46, v47\n"
"v_cvt_pk_bf16_f32 v73, v74, v75\n"
"v_mul_f32_dpp v28, v243, v28 row_newbcast:0 row_mask:0xf bank_mask:0xf\n"
"v_mul_f32_dpp v90, v245, v90 row_newbcast:2 row_mask:0xf bank_mask:0xf\n"
"v_cvt_pk_bf16_f32 v88, v88, v89\n"
"v_mfma_f32_16x16x32_bf16 v[4:7], v[100:103], v[224:227], v[4:7]\n"
"ds_read_b64_tr_b16 a[248:249], v236 offset:50176\n"
"ds_read_b64_tr_b16 v[224:225], v237 offset:50176\n"
"ds_read_b64_tr_b16 v[226:227], v237 offset:54272\n"
"v_mul_f32_dpp v76, v245, v76 row_newbcast:0 row_mask:0xf bank_mask:0xf\n"
"v_mul_f32_dpp v29, v243, v29 row_newbcast:1 row_mask:0xf bank_mask:0xf\n"
"v_mul_f32_dpp v31, v243, v31 row_newbcast:3 row_mask:0xf bank_mask:0xf\n"
"v_mul_f32_dpp v63, v244, v63 row_newbcast:3 row_mask:0xf bank_mask:0xf\n"
"v_mul_f32_dpp v3, v243, v3 row_newbcast:3 row_mask:0xf bank_mask:0xf\n"
"v_mfma_f32_16x16x32_bf16 v[4:7], v[96:99], a[252:255], v[4:7]\n"
"ds_read_b64_tr_b16 a[4:5], v247 offset:49152\n"
"v_mul_f32_dpp v95, v245, v95 row_newbcast:3 row_mask:0xf bank_mask:0xf\n"
"v_mfma_f32_16x16x32_bf16 a[212:215], v[120:123], a[252:255], a[212:215]\n"
"s_waitcnt lgkmcnt(7)\n"
"ds_read_b64_tr_b16 a[6:7], v247 offset:49160\n"
"ds_read_b64_tr_b16 a[252:253], v233 offset:50176\n"
"ds_read_b64_tr_b16 a[254:255], v233 offset:54272\n"
"v_cvt_pk_bf16_f32 v42, v44, v45\n"
"v_cvt_pk_bf16_f32 v57, v58, v59\n"
"v_mul_f32_dpp v64, v245, v64 row_newbcast:0 row_mask:0xf bank_mask:0xf\n"
"v_xor_b32 v254, 0x1000, v254\n"
"v_mfma_f32_16x16x32_bf16 a[224:227], v[120:123], a[244:247], a[224:227]\n"
"ds_write_b128 v246, v[40:43] offset:50176\n"
"v_cvt_pk_bf16_f32 v75, v78, v79\n"
"v_cvt_pk_bf16_f32 v58, v60, v61\n"
"v_mul_f32_dpp v91, v245, v91 row_newbcast:3 row_mask:0xf bank_mask:0xf\n"
"v_add_u32 v236, s76, v247\n"
"v_xor_b32 v237, 0x400, v236\n"
"v_mfma_f32_16x16x32_bf16 v[16:19], v[96:99], a[244:247], v[16:19]\n"
"s_waitcnt vmcnt(8) lgkmcnt(1)\n"
"v_mul_f32_dpp v32, v244, v32 row_newbcast:0 row_mask:0xf bank_mask:0xf\n"
"v_mul_f32_dpp v34, v244, v34 row_newbcast:2 row_mask:0xf bank_mask:0xf\n"
"v_mfma_f32_16x16x32_bf16 v[16:19], v[100:103], a[248:251], v[16:19]\n"
"s_barrier\n"
"ds_read_b64_tr_b16 v[190:191], v228 offset:10240\n"
"ds_read_b64_tr_b16 v[194:195], v232 offset:10240\n"
"ds_read_b64_tr_b16 v[210:211], v228 offset:18432\n"
"ds_read_b64_tr_b16 v[212:213], v232 offset:16384\n"
"ds_read_b64_tr_b16 v[218:219], v228 offset:26624\n"
"v_cvt_pk_bf16_f32 v25, v26, v27\n"
"v_cvt_pk_bf16_f32 v89, v90, v91\n"
"v_mul_f32_dpp v65, v245, v65 row_newbcast:1 row_mask:0xf bank_mask:0xf\n"
"v_mfma_f32_16x16x32_bf16 v[48:51], v[104:107], a[244:247], v[48:51]\n"
"ds_read_b64_tr_b16 a[194:195], v236 offset:8\n"
"v_mul_f32_dpp v77, v245, v77 row_newbcast:1 row_mask:0xf bank_mask:0xf\n"
"v_mfma_f32_16x16x32_bf16 v[48:51], v[108:111], a[248:251], v[48:51]\n"
"ds_read_b64_tr_b16 a[22:23], v247 offset:50184\n"
"ds_read_b64 v[250:251], v255 offset:52224\n"
"ds_read_b64_tr_b16 v[180:181], v228 offset:0\n"
"ds_read_b64_tr_b16 v[186:187], v232 offset:2048\n"
"v_cvt_pk_bf16_f32 v26, v28, v29\n"
"v_mul_f32_dpp v62, v244, v62 row_newbcast:2 row_mask:0xf bank_mask:0xf\n"
"v_mul_f32_dpp v5, v243, v5 row_newbcast:1 row_mask:0xf bank_mask:0xf\n"
"v_mul_f32_dpp v35, v244, v35 row_newbcast:3 row_mask:0xf bank_mask:0xf\n"
"v_mul_f32_dpp v37, v244, v37 row_newbcast:1 row_mask:0xf bank_mask:0xf\n"
"v_mfma_f32_16x16x32_bf16 v[80:83], v[112:115], a[244:247], v[80:83]\n"
"ds_read_b64_tr_b16 v[188:189], v228 offset:8192\n"
"ds_read_b64_tr_b16 v[220:221], v232 offset:24576\n"
"v_mfma_f32_16x16x32_bf16 v[80:83], v[116:119], a[248:251], v[80:83]\n"
"ds_read_b64_tr_b16 v[182:183], v228 offset:2048\n"
"ds_read_b64_tr_b16 v[184:185], v232 offset:0\n"
"ds_read_b64_tr_b16 v[208:209], v228 offset:16384\n"
"ds_read_b64_tr_b16 v[214:215], v232 offset:18432\n"
"v_cvt_pk_bf16_f32 v74, v76, v77\n"
"v_mul_f32_dpp v92, v245, v92 row_newbcast:0 row_mask:0xf bank_mask:0xf\n"
"v_cvt_pk_bf16_f32 v64, v64, v65\n"
"v_xor_b32 v235, 0x1000, v231\n"
"v_mfma_f32_16x16x32_bf16 v[84:87], v[116:119], v[224:227], v[84:87]\n"
"ds_write_b128 v246, v[72:75] offset:51200\n"
"v_cvt_pk_bf16_f32 v27, v30, v31\n"
"v_cvt_pk_bf16_f32 v59, v62, v63\n"
"v_cvt_pk_bf16_f32 v90, v92, v93\n"
"v_mul_f32_dpp v7, v243, v7 row_newbcast:3 row_mask:0xf bank_mask:0xf\n"
"v_mul_f32_dpp v67, v245, v67 row_newbcast:3 row_mask:0xf bank_mask:0xf\n"
"v_mfma_f32_16x16x32_bf16 v[84:87], v[112:115], a[252:255], v[84:87]\n"
"s_waitcnt lgkmcnt(9)\n"
"ds_read_b64_tr_b16 a[38:39], v247 offset:51208\n"
"ds_read_b64_tr_b16 v[192:193], v232 offset:8192\n"
"ds_read_b64_tr_b16 a[204:205], v237 offset:2048\n"
"ds_read_b64_tr_b16 a[248:249], v236 offset:6144\n"
"v_mul_f32_dpp v1, v243, v1 row_newbcast:1 row_mask:0xf bank_mask:0xf\n"
"v_mul_f32_dpp v33, v244, v33 row_newbcast:1 row_mask:0xf bank_mask:0xf\n"
"v_mul_f32_dpp v36, v244, v36 row_newbcast:0 row_mask:0xf bank_mask:0xf\n"
"v_min_f32 v249, v252, v250\n"
"v_mfma_f32_16x16x32_bf16 v[52:55], v[108:111], v[224:227], v[52:55]\n"
"ds_read_b64_tr_b16 v[216:217], v228 offset:24576\n"
"ds_read_b64_tr_b16 v[114:115], v229 offset:18432\n"
"v_mfma_f32_16x16x32_bf16 v[52:55], v[104:107], a[252:255], v[52:55]\n"
"ds_read_b64_tr_b16 a[36:37], v247 offset:51200\n"
"ds_read_b64_tr_b16 v[222:223], v232 offset:26624\n"
"ds_read_b64_tr_b16 v[106:107], v229 offset:10240\n"
"ds_read_b64_tr_b16 a[250:251], v236 offset:6152\n"
"v_cvt_pk_bf16_f32 v91, v94, v95\n"
"v_mul_f32_dpp v38, v244, v38 row_newbcast:2 row_mask:0xf bank_mask:0xf\n"
"v_mul_f32_dpp v71, v245, v71 row_newbcast:3 row_mask:0xf bank_mask:0xf\n"
"v_sub_f32 v252, v249, v252\n"
"v_sub_f32 v250, v249, v250\n"
"v_mfma_f32_16x16x32_bf16 v[20:23], v[100:103], v[224:227], v[20:23]\n"
"ds_read_b64_tr_b16 a[20:21], v247 offset:50176\n"
"ds_read_b64_tr_b16 a[242:243], v236 offset:4104\n"
"v_mfma_f32_16x16x32_bf16 v[20:23], v[96:99], a[252:255], v[20:23]\n"
"ds_write_b128 v246, v[56:59] offset:50176\n"
"v_mul_f32_dpp v4, v243, v4 row_newbcast:0 row_mask:0xf bank_mask:0xf\n"
"v_mul_f32_dpp v68, v245, v68 row_newbcast:0 row_mask:0xf bank_mask:0xf\n"
"v_mul_f32_dpp v69, v245, v69 row_newbcast:1 row_mask:0xf bank_mask:0xf\n"
"v_mul_f32_dpp v17, v243, v17 row_newbcast:1 row_mask:0xf bank_mask:0xf\n"
"v_mfma_f32_16x16x32_bf16 a[228:231], v[120:123], a[252:255], a[228:231]\n"
"s_waitcnt lgkmcnt(15)\n"
"ds_write_b128 v246, v[24:27] offset:49152\n"
"v_cvt_pk_bf16_f32 v0, v0, v1\n"
"v_mul_f32_dpp v66, v245, v66 row_newbcast:2 row_mask:0xf bank_mask:0xf\n"
"v_mul_f32_dpp v70, v245, v70 row_newbcast:2 row_mask:0xf bank_mask:0xf\n"
"v_accvgpr_read_b32 v249, a208\n"
"v_mfma_f32_16x16x32_bf16 v[124:127], v[180:183], a[96:99], 0\n"
"ds_write_b128 v246, v[88:91] offset:51200\n"
"v_cvt_pk_bf16_f32 v1, v2, v3\n"
"v_cvt_pk_bf16_f32 v65, v66, v67\n"
"v_xor_b32 v233, 0x1000, v229\n"
"v_mul_f32_dpp v82, v245, v82 row_newbcast:2 row_mask:0xf bank_mask:0xf\n"
"v_mfma_f32_16x16x32_bf16 v[124:127], v[184:187], a[100:103], v[124:127]\n"
"ds_read_b64_tr_b16 a[44:45], v247 offset:51200\n"
"ds_read_b128 v[24:27], v254 offset:32768\n"
"v_mfma_f32_16x16x32_bf16 v[124:127], v[188:191], a[104:107], v[124:127]\n"
"s_waitcnt lgkmcnt(13)\n"
"ds_read_b64_tr_b16 a[14:15], v247 offset:49160\n"
"ds_read_b64_tr_b16 a[30:31], v247 offset:50184\n"
"ds_read_b64_tr_b16 v[110:111], v233 offset:10240\n"
"ds_read_b64_tr_b16 v[112:113], v229 offset:16384\n"
"v_mul_f32_dpp v6, v243, v6 row_newbcast:2 row_mask:0xf bank_mask:0xf\n"
"v_mul_f32_dpp v39, v244, v39 row_newbcast:3 row_mask:0xf bank_mask:0xf\n"
"v_cvt_pk_bf16_f32 v32, v32, v33\n"
"v_cvt_pk_bf16_f32 v33, v34, v35\n"
"v_cvt_pk_bf16_f32 v34, v36, v37\n"
"v_cvt_pk_bf16_f32 v35, v38, v39\n"
"v_mfma_f32_16x16x32_bf16 v[124:127], v[192:195], a[108:111], v[124:127]\n"
"ds_read_b64_tr_b16 a[12:13], v247 offset:49152\n"
"ds_read_b64_tr_b16 a[28:29], v247 offset:50176\n"
"v_mfma_f32_16x16x32_bf16 v[124:127], v[208:211], a[144:147], v[124:127]\n"
"ds_write_b128 v246, v[32:35] offset:50176\n"
"v_cvt_pk_bf16_f32 v2, v4, v5\n"
"v_cvt_pk_bf16_f32 v3, v6, v7\n"
"v_cvt_pk_bf16_f32 v66, v68, v69\n"
"v_mul_f32_dpp v49, v244, v49 row_newbcast:1 row_mask:0xf bank_mask:0xf\n"
"v_mul_f32_dpp v53, v244, v53 row_newbcast:1 row_mask:0xf bank_mask:0xf\n"
"v_mfma_f32_16x16x32_bf16 v[124:127], v[212:215], a[148:151], v[124:127]\n"
"s_waitcnt lgkmcnt(12)\n"
"ds_write_b128 v246, v[0:3] offset:49152\n"
"v_cvt_pk_bf16_f32 v67, v70, v71\n"
"v_exp_f32 v252, v252\n"
"v_exp_f32 v250, v250\n"
"v_mfma_f32_16x16x32_bf16 v[124:127], v[216:219], a[152:155], v[124:127]\n"
"ds_read_b64_tr_b16 a[46:47], v247 offset:51208\n"
"ds_read_b128 v[56:59], v254 offset:34816\n"
"v_mfma_f32_16x16x32_bf16 v[124:127], v[220:223], a[156:159], v[124:127]\n"
"ds_write_b128 v246, v[64:67] offset:51200\n"
"v_accvgpr_read_b32 v206, a209\n"
"v_mul_f32_dpp v23, v243, v23 row_newbcast:3 row_mask:0xf bank_mask:0xf\n"
"v_mul_f32_dpp v52, v244, v52 row_newbcast:0 row_mask:0xf bank_mask:0xf\n"
"v_mul_f32_dpp v81, v245, v81 row_newbcast:1 row_mask:0xf bank_mask:0xf\n"
"v_mfma_f32_16x16x32_bf16 v[140:143], v[220:223], a[172:175], 0\n"
"ds_read_b64_tr_b16 a[16:17], v247 offset:50176\n"
"ds_read_b64_tr_b16 a[18:19], v247 offset:50184\n"
"v_mfma_f32_16x16x32_bf16 v[140:143], v[216:219], a[168:171], v[140:143]\n"
"ds_read_b64_tr_b16 a[34:35], v247 offset:51208\n"
"ds_read_b128 v[28:31], v254 offset:33792\n"
"v_mfma_f32_16x16x32_bf16 v[140:143], v[212:215], a[164:167], v[140:143]\n"
"ds_read_b64_tr_b16 a[32:33], v247 offset:51200\n"
"ds_read_b128 v[60:63], v254 offset:35840\n"
"v_mfma_f32_16x16x32_bf16 v[140:143], v[208:211], a[160:163], v[140:143]\n"
"ds_read_b64_tr_b16 a[0:1], v247 offset:49152\n"
"ds_read_b64_tr_b16 a[2:3], v247 offset:49160\n"
"v_mfma_f32_16x16x32_bf16 v[140:143], v[192:195], a[124:127], v[140:143]\n"
"ds_read_b64_tr_b16 a[200:201], v236 offset:2048\n"
"ds_read_b64_tr_b16 v[100:101], v233 offset:0\n"
"v_mfma_f32_16x16x32_bf16 v[140:143], v[188:191], a[120:123], v[140:143]\n"
"ds_read_b64_tr_b16 a[252:253], v237 offset:6144\n"
"ds_read_b64_tr_b16 a[254:255], v237 offset:6152\n"
"v_mfma_f32_16x16x32_bf16 v[140:143], v[184:187], a[116:119], v[140:143]\n"
"ds_read_b64_tr_b16 a[244:245], v237 offset:4096\n"
"ds_read_b64_tr_b16 a[246:247], v237 offset:4104\n"
"v_mfma_f32_16x16x32_bf16 v[140:143], v[180:183], a[112:115], v[140:143]\n"
"ds_read_b64_tr_b16 a[206:207], v237 offset:2056\n"
"ds_read_b64_tr_b16 v[108:109], v233 offset:8192\n"
"v_mfma_f32_16x16x32_bf16 v[156:159], v[180:183], a[128:131], 0\n"
"ds_read_b64_tr_b16 v[104:105], v229 offset:8192\n"
"v_mul_f32_dpp v22, v243, v22 row_newbcast:2 row_mask:0xf bank_mask:0xf\n"
"v_mfma_f32_16x16x32_bf16 v[156:159], v[184:187], a[132:135], v[156:159]\n"
"ds_read_b64_tr_b16 a[196:197], v237 offset:0\n"
"ds_read_b64_tr_b16 a[202:203], v236 offset:2056\n"
"v_mfma_f32_16x16x32_bf16 v[156:159], v[188:191], a[136:139], v[156:159]\n"
"ds_read_b64_tr_b16 a[240:241], v236 offset:4096\n"
"v_mul_f32_dpp v84, v245, v84 row_newbcast:0 row_mask:0xf bank_mask:0xf\n"
"v_mfma_f32_16x16x32_bf16 v[156:159], v[192:195], a[140:143], v[156:159]\n"
"s_waitcnt lgkmcnt(12)\n"
"ds_read_b64_tr_b16 v[98:99], v229 offset:2048\n"
"ds_read_b64_tr_b16 v[116:117], v233 offset:16384\n"
"v_mfma_f32_16x16x32_bf16 v[156:159], v[208:211], a[176:179], v[156:159]\n"
"ds_read_b64_tr_b16 v[96:97], v229 offset:0\n"
"v_mul_f32_dpp v83, v245, v83 row_newbcast:3 row_mask:0xf bank_mask:0xf\n"
"v_mfma_f32_16x16x32_bf16 v[156:159], v[212:215], a[180:183], v[156:159]\n"
"s_add_u32 s41, s95, 491520\n"
"ds_read_b64_tr_b16 a[198:199], v237 offset:8\n"
"ds_read_b64_tr_b16 v[226:227], v233 offset:26624\n"
"v_mfma_f32_16x16x32_bf16 v[156:159], v[216:219], a[184:187], v[156:159]\n"
"s_add_u32 m0, s44, 49152\n"
"ds_read_b64_tr_b16 a[192:193], v236 offset:0\n"
"ds_read_b64_tr_b16 v[122:123], v229 offset:26624\n"
"v_mfma_f32_16x16x32_bf16 v[156:159], v[220:223], a[188:191], v[156:159]\n"
"s_waitcnt lgkmcnt(11)\n"
"buffer_load_dwordx4 v240, s[28:31], s41 offen lds\n"
"v_mfma_f32_16x16x32_bf16 v[172:175], v[220:223], a[252:255], 0\n"
"s_add_u32 s40, s41, s52\n"
"ds_read_b64_tr_b16 v[102:103], v233 offset:2048\n"
"ds_read_b64_tr_b16 v[118:119], v233 offset:18432\n"
"v_mfma_f32_16x16x32_bf16 v[172:175], v[216:219], a[248:251], v[172:175]\n"
"buffer_load_dwordx4 v240, s[28:31], s40 offen offset:1024 lds\n"
"v_mfma_f32_16x16x32_bf16 v[172:175], v[212:215], a[244:247], v[172:175]\n"
"s_waitcnt lgkmcnt(9)\n"
"s_add_u32 s40, s41, s53\n"
"v_mul_f32 v253, v253, v252\n"
"v_mul_f32_dpp v54, v244, v54 row_newbcast:2 row_mask:0xf bank_mask:0xf\n"
"v_mfma_f32_16x16x32_bf16 v[172:175], v[208:211], a[240:243], v[172:175]\n"
"buffer_load_dwordx4 v240, s[28:31], s40 offen offset:2048 lds\n"
"v_mfma_f32_16x16x32_bf16 v[172:175], v[192:195], a[204:207], v[172:175]\n"
"v_bfe_u32 v207, v242, 8, 2\n"
"v_mul_f32_dpp v85, v245, v85 row_newbcast:1 row_mask:0xf bank_mask:0xf\n"
"v_mfma_f32_16x16x32_bf16 v[172:175], v[188:191], a[200:203], v[172:175]\n"
"s_waitcnt lgkmcnt(4)\n"
"v_fmac_f32 v253, v251, v250\n"
"v_accvgpr_read_b32 v251, a211\n"
"v_mfma_f32_16x16x32_bf16 v[172:175], v[184:187], a[196:199], v[172:175]\n"
"s_waitcnt lgkmcnt(2)\n"
"s_add_u32 s40, s41, s54\n"
"ds_read_b64_tr_b16 v[120:121], v229 offset:24576\n"
"ds_read_b64_tr_b16 v[224:225], v233 offset:24576\n"
"v_mfma_f32_16x16x32_bf16 v[172:175], v[180:183], a[192:195], v[172:175]\n"
"buffer_load_dwordx4 v240, s[28:31], s40 offen offset:3072 lds\n"
"v_mfma_f32_16x16x32_bf16 v[128:131], v[96:99], a[96:99], 0\n"
"s_waitcnt lgkmcnt(2)\n"
"v_rcp_f32 v253, v253\n"
"v_mfma_f32_16x16x32_bf16 v[128:131], v[100:103], a[100:103], v[128:131]\n"
"ds_read_b64_tr_b16 a[238:239], v237 offset:10248\n"
"v_mul_f32 v250, v250, v253\n"
"v_mfma_f32_16x16x32_bf16 v[128:131], v[104:107], a[104:107], v[128:131]\n"
"ds_read_b64_tr_b16 v[214:215], v234 offset:18432\n"
"v_mul_f32 v252, v252, v253\n"
"v_mfma_f32_16x16x32_bf16 v[128:131], v[108:111], a[108:111], v[128:131]\n"
"ds_read_b64_tr_b16 v[188:189], v230 offset:8192\n"
"ds_read_b64_tr_b16 v[220:221], v234 offset:24576\n"
"v_mfma_f32_16x16x32_bf16 v[128:131], v[112:115], a[144:147], v[128:131]\n"
"ds_read_b64_tr_b16 v[192:193], v234 offset:8192\n"
"v_mov_b32_dpp v250, v250 row_ror:12 row_mask:0x2 bank_mask:0xf\n"
"v_mfma_f32_16x16x32_bf16 v[128:131], v[116:119], a[148:151], v[128:131]\n"
"s_waitcnt lgkmcnt(6)\n"
"v_lshlrev_b32 v207, 6, v207\n"
"v_mul_f32_dpp v48, v244, v48 row_newbcast:0 row_mask:0xf bank_mask:0xf\n"
"v_mfma_f32_16x16x32_bf16 v[128:131], v[120:123], a[152:155], v[128:131]\n"
"s_waitcnt lgkmcnt(5)\n"
"v_mov_b32_dpp v252, v252 row_ror:12 row_mask:0x2 bank_mask:0xf\n"
"v_mov_b32_dpp v250, v250 row_ror:8 row_mask:0x4 bank_mask:0xf\n"
"v_mfma_f32_16x16x32_bf16 v[128:131], v[224:227], a[156:159], v[128:131]\n"
"ds_read_b64_tr_b16 v[208:209], v230 offset:16384\n"
"v_mov_b32_dpp v250, v250 row_ror:4 row_mask:0x8 bank_mask:0xf\n"
"v_mfma_f32_16x16x32_bf16 v[144:147], v[224:227], a[172:175], 0\n"
"v_mov_b32_dpp v252, v252 row_ror:8 row_mask:0x4 bank_mask:0xf\n"
"v_mul_f32_dpp v31, v250, v31 row_newbcast:3 row_mask:0xf bank_mask:0xf\n"
"v_mfma_f32_16x16x32_bf16 v[144:147], v[120:123], a[168:171], v[144:147]\n"
"ds_read_b64_tr_b16 v[186:187], v234 offset:2048\n"
"v_mov_b32_dpp v252, v252 row_ror:4 row_mask:0x8 bank_mask:0xf\n"
"v_mfma_f32_16x16x32_bf16 v[144:147], v[116:119], a[164:167], v[144:147]\n"
"v_mul_f32_dpp v24, v250, v24 row_newbcast:0 row_mask:0xf bank_mask:0xf\n"
"v_mul_f32_dpp v30, v250, v30 row_newbcast:2 row_mask:0xf bank_mask:0xf\n"
"v_mfma_f32_16x16x32_bf16 v[144:147], v[112:115], a[160:163], v[144:147]\n"
"ds_read_b64_tr_b16 a[220:221], v237 offset:8192\n"
"v_mul_f32_dpp v59, v250, v59 row_newbcast:3 row_mask:0xf bank_mask:0xf\n"
"v_mfma_f32_16x16x32_bf16 v[144:147], v[108:111], a[124:127], v[144:147]\n"
"v_accvgpr_read_b32 v253, a210\n"
"v_fmac_f32_dpp v24, v252, v249 row_newbcast:0 row_mask:0xf bank_mask:0xf\n"
"v_mfma_f32_16x16x32_bf16 v[144:147], v[104:107], a[120:123], v[144:147]\n"
"v_mul_f32_dpp v26, v250, v26 row_newbcast:2 row_mask:0xf bank_mask:0xf\n"
"v_cvt_pk_bf16_f32 v48, v48, v49\n"
"v_mfma_f32_16x16x32_bf16 v[144:147], v[100:103], a[116:119], v[144:147]\n"
"ds_read_b64_tr_b16 v[194:195], v234 offset:10240\n"
"v_mul_f32_dpp v60, v250, v60 row_newbcast:0 row_mask:0xf bank_mask:0xf\n"
"v_mfma_f32_16x16x32_bf16 v[144:147], v[96:99], a[112:115], v[144:147]\n"
"ds_read_b64_tr_b16 v[184:185], v234 offset:0\n"
"v_mul_f32_dpp v25, v250, v25 row_newbcast:1 row_mask:0xf bank_mask:0xf\n"
"v_fmac_f32_dpp v26, v252, v253 row_newbcast:2 row_mask:0xf bank_mask:0xf\n"
"v_mul_f32_dpp v28, v250, v28 row_newbcast:0 row_mask:0xf bank_mask:0xf\n"
"v_accvgpr_read_b32 v253, a214\n"
"v_mul_f32_dpp v61, v250, v61 row_newbcast:1 row_mask:0xf bank_mask:0xf\n"
"v_mfma_f32_16x16x32_bf16 v[160:163], v[96:99], a[128:131], 0\n"
"ds_read_b64_tr_b16 v[210:211], v230 offset:18432\n"
"v_mul_f32_dpp v80, v245, v80 row_newbcast:0 row_mask:0xf bank_mask:0xf\n"
"v_mfma_f32_16x16x32_bf16 v[160:163], v[100:103], a[132:135], v[160:163]\n"
"v_fmac_f32_dpp v25, v252, v206 row_newbcast:1 row_mask:0xf bank_mask:0xf\n"
"v_mul_f32_dpp v57, v250, v57 row_newbcast:1 row_mask:0xf bank_mask:0xf\n"
"v_mfma_f32_16x16x32_bf16 v[160:163], v[104:107], a[136:139], v[160:163]\n"
"ds_read_b64_tr_b16 v[190:191], v230 offset:10240\n"
"v_accvgpr_read_b32 v249, a212\n"
"v_mfma_f32_16x16x32_bf16 v[160:163], v[108:111], a[140:143], v[160:163]\n"
"ds_read_b64_tr_b16 v[212:213], v234 offset:16384\n"
"v_xor_b32 v207, v207, v248\n"
"v_mfma_f32_16x16x32_bf16 v[160:163], v[112:115], a[176:179], v[160:163]\n"
"ds_read_b64_tr_b16 v[180:181], v230 offset:0\n"
"ds_read_b64_tr_b16 v[218:219], v230 offset:26624\n"
"v_mfma_f32_16x16x32_bf16 v[160:163], v[116:119], a[180:183], v[160:163]\n"
"v_fmac_f32_dpp v28, v252, v249 row_newbcast:0 row_mask:0xf bank_mask:0xf\n"
"v_accvgpr_read_b32 v249, a224\n"
"v_mfma_f32_16x16x32_bf16 v[160:163], v[120:123], a[184:187], v[160:163]\n"
"ds_read_b64_tr_b16 v[216:217], v230 offset:24576\n"
"v_accvgpr_read_b32 v206, a213\n"
"v_mfma_f32_16x16x32_bf16 v[160:163], v[224:227], a[188:191], v[160:163]\n"
"ds_read_b64_tr_b16 v[182:183], v230 offset:2048\n"
"v_cvt_pk_bf16_f32 v24, v24, v25\n"
"v_mfma_f32_16x16x32_bf16 v[176:179], v[224:227], a[252:255], 0\n"
"v_mul_f32_dpp v56, v250, v56 row_newbcast:0 row_mask:0xf bank_mask:0xf\n"
"v_mul_f32_dpp v63, v250, v63 row_newbcast:3 row_mask:0xf bank_mask:0xf\n"
"v_mfma_f32_16x16x32_bf16 v[176:179], v[120:123], a[248:251], v[176:179]\n"
"ds_read_b64_tr_b16 v[222:223], v234 offset:26624\n"
"v_fmac_f32_dpp v30, v252, v253 row_newbcast:2 row_mask:0xf bank_mask:0xf\n"
"v_mfma_f32_16x16x32_bf16 v[176:179], v[116:119], a[244:247], v[176:179]\n"
"v_mul_f32_dpp v29, v250, v29 row_newbcast:1 row_mask:0xf bank_mask:0xf\n"
"v_mul_f32_dpp v58, v250, v58 row_newbcast:2 row_mask:0xf bank_mask:0xf\n"
"v_mfma_f32_16x16x32_bf16 v[176:179], v[112:115], a[240:243], v[176:179]\n"
"v_mul_f32_dpp v27, v250, v27 row_newbcast:3 row_mask:0xf bank_mask:0xf\n"
"v_fmac_f32_dpp v29, v252, v206 row_newbcast:1 row_mask:0xf bank_mask:0xf\n"
"v_mfma_f32_16x16x32_bf16 v[176:179], v[108:111], a[204:207], v[176:179]\n"
"ds_read_b64_tr_b16 a[234:235], v236 offset:10248\n"
"v_fmac_f32_dpp v56, v252, v249 row_newbcast:0 row_mask:0xf bank_mask:0xf\n"
"v_mfma_f32_16x16x32_bf16 v[176:179], v[104:107], a[200:203], v[176:179]\n"
"v_mul_f32_dpp v87, v245, v87 row_newbcast:3 row_mask:0xf bank_mask:0xf\n"
"v_accvgpr_read_b32 v206, a225\n"
"v_mfma_f32_16x16x32_bf16 v[176:179], v[100:103], a[196:199], v[176:179]\n"
"v_mul_f32_dpp v20, v243, v20 row_newbcast:0 row_mask:0xf bank_mask:0xf\n"
"v_mul_f32_dpp v51, v244, v51 row_newbcast:3 row_mask:0xf bank_mask:0xf\n"
"v_mfma_f32_16x16x32_bf16 v[176:179], v[96:99], a[192:195], v[176:179]\n"
"s_waitcnt lgkmcnt(2)\n"
"ds_read_b64_tr_b16 v[108:109], v235 offset:8192\n"
"v_accvgpr_read_b32 v253, a226\n"
"v_mfma_f32_16x16x32_bf16 v[132:135], v[180:183], a[96:99], 0\n"
"s_mov_b32 s47, 0\n"
"ds_read_b64_tr_b16 v[122:123], v231 offset:26624\n"
"v_fmac_f32_dpp v27, v252, v251 row_newbcast:3 row_mask:0xf bank_mask:0xf\n"
"v_mfma_f32_16x16x32_bf16 v[132:135], v[184:187], a[100:103], v[132:135]\n"
"s_mul_i32 s16, s61, 1280\n"
"ds_read_b64_tr_b16 v[100:101], v235 offset:0\n"
"v_accvgpr_read_b32 v251, a215\n"
"v_mfma_f32_16x16x32_bf16 v[132:135], v[188:191], a[104:107], v[132:135]\n"
"s_add_u32 s16, s16, s95\n"
"ds_read_b64_tr_b16 a[218:219], v236 offset:8200\n"
"ds_read_b64_tr_b16 v[118:119], v235 offset:18432\n"
"v_mfma_f32_16x16x32_bf16 v[132:135], v[192:195], a[108:111], v[132:135]\n"
"s_mul_i32 s16, s16, 224\n"
"v_fmac_f32_dpp v57, v252, v206 row_newbcast:1 row_mask:0xf bank_mask:0xf\n"
"v_accvgpr_read_b32 v249, a228\n"
"v_mfma_f32_16x16x32_bf16 v[132:135], v[208:211], a[144:147], v[132:135]\n"
"s_add_u32 s17, s16, 4096\n"
"ds_read_b64_tr_b16 v[112:113], v231 offset:16384\n"
"v_cvt_pk_bf16_f32 v25, v26, v27\n"
"v_mfma_f32_16x16x32_bf16 v[132:135], v[212:215], a[148:151], v[132:135]\n"
"s_mov_b32 s72, s61\n"
"ds_read_b64_tr_b16 v[110:111], v235 offset:10240\n"
"v_fmac_f32_dpp v31, v252, v251 row_newbcast:3 row_mask:0xf bank_mask:0xf\n"
"v_mfma_f32_16x16x32_bf16 v[132:135], v[216:219], a[152:155], v[132:135]\n"
"s_waitcnt lgkmcnt(7)\n"
"s_mov_b32 s21, s60\n"
"v_cvt_pk_bf16_f32 v27, v30, v31\n"
"v_fmac_f32_dpp v58, v252, v253 row_newbcast:2 row_mask:0xf bank_mask:0xf\n"
"v_mfma_f32_16x16x32_bf16 v[132:135], v[220:223], a[156:159], v[132:135]\n"
"s_mov_b32 s18, s77\n"
"ds_read_b64_tr_b16 v[102:103], v235 offset:2048\n"
"ds_read_b64_tr_b16 v[104:105], v231 offset:8192\n"
"ds_read_b64_tr_b16 v[106:107], v231 offset:10240\n"
"v_accvgpr_read_b32 v206, a229\n"
"v_mul_f32_dpp v62, v250, v62 row_newbcast:2 row_mask:0xf bank_mask:0xf\n"
"v_fmac_f32_dpp v60, v252, v249 row_newbcast:0 row_mask:0xf bank_mask:0xf\n"
"v_cvt_pk_bf16_f32 v56, v56, v57\n"
"v_mfma_f32_16x16x32_bf16 v[148:151], v[220:223], a[172:175], 0\n"
"s_mov_b32 s19, s77\n"
"v_cvt_pk_bf16_f32 v26, v28, v29\n"
"v_fmac_f32_dpp v61, v252, v206 row_newbcast:1 row_mask:0xf bank_mask:0xf\n"
"v_mfma_f32_16x16x32_bf16 v[148:151], v[216:219], a[168:171], v[148:151]\n"
"s_mov_b32 s69, -1\n"
"ds_read_b64_tr_b16 a[216:217], v236 offset:8192\n"
"v_accvgpr_read_b32 v251, a227\n"
"v_mfma_f32_16x16x32_bf16 v[148:151], v[212:215], a[164:167], v[148:151]\n"
"ds_read_b64_tr_b16 v[96:97], v231 offset:0\n"
"v_accvgpr_read_b32 v253, a230\n"
"v_mfma_f32_16x16x32_bf16 v[148:151], v[208:211], a[160:163], v[148:151]\n"
"ds_read_b64_tr_b16 v[98:99], v231 offset:2048\n"
"v_fmac_f32_dpp v59, v252, v251 row_newbcast:3 row_mask:0xf bank_mask:0xf\n"
"v_mfma_f32_16x16x32_bf16 v[148:151], v[192:195], a[124:127], v[148:151]\n"
"v_mul_f32_dpp v86, v245, v86 row_newbcast:2 row_mask:0xf bank_mask:0xf\n"
"v_accvgpr_read_b32 v251, a231\n"
"v_mfma_f32_16x16x32_bf16 v[148:151], v[188:191], a[120:123], v[148:151]\n"
"ds_write_b128 v207, v[24:27] offset:0\n"
"v_cvt_pk_bf16_f32 v80, v80, v81\n"
"v_fmac_f32_dpp v62, v252, v253 row_newbcast:2 row_mask:0xf bank_mask:0xf\n"
"v_fmac_f32_dpp v63, v252, v251 row_newbcast:3 row_mask:0xf bank_mask:0xf\n"
"v_cvt_pk_bf16_f32 v57, v58, v59\n"
"v_mfma_f32_16x16x32_bf16 v[148:151], v[184:187], a[116:119], v[148:151]\n"
"v_cvt_pk_bf16_f32 v58, v60, v61\n"
"v_cvt_pk_bf16_f32 v59, v62, v63\n"
"v_mfma_f32_16x16x32_bf16 v[148:151], v[180:183], a[112:115], v[148:151]\n"
"ds_write_b128 v207, v[56:59] offset:2048\n"
"v_xor_b32 v247, s75, v247\n"
"v_mfma_f32_16x16x32_bf16 v[164:167], v[180:183], a[128:131], 0\n"
"v_mul_f32_dpp v55, v244, v55 row_newbcast:3 row_mask:0xf bank_mask:0xf\n"
"v_cvt_pk_bf16_f32 v81, v82, v83\n"
"v_mfma_f32_16x16x32_bf16 v[164:167], v[184:187], a[132:135], v[164:167]\n"
"ds_read_b64_tr_b16 v[120:121], v231 offset:24576\n"
"v_mul_f32_dpp v18, v243, v18 row_newbcast:2 row_mask:0xf bank_mask:0xf\n"
"v_mfma_f32_16x16x32_bf16 v[164:167], v[188:191], a[136:139], v[164:167]\n"
"ds_read_b64_tr_b16 v[116:117], v235 offset:16384\n"
"v_mul_f32_dpp v50, v244, v50 row_newbcast:2 row_mask:0xf bank_mask:0xf\n"
"v_mfma_f32_16x16x32_bf16 v[164:167], v[192:195], a[140:143], v[164:167]\n"
"v_mul_f32_dpp v16, v243, v16 row_newbcast:0 row_mask:0xf bank_mask:0xf\n"
"v_cvt_pk_bf16_f32 v83, v86, v87\n"
"v_mfma_f32_16x16x32_bf16 v[164:167], v[208:211], a[176:179], v[164:167]\n"
"ds_read_b64_tr_b16 v[226:227], v235 offset:26624\n"
"v_cvt_pk_bf16_f32 v82, v84, v85\n"
"v_mfma_f32_16x16x32_bf16 v[164:167], v[212:215], a[180:183], v[164:167]\n"
"ds_read_b64_tr_b16 v[114:115], v231 offset:18432\n"
"ds_read_b64_tr_b16 v[224:225], v235 offset:24576\n"
"v_mfma_f32_16x16x32_bf16 v[164:167], v[216:219], a[184:187], v[164:167]\n"
"s_waitcnt vmcnt(0) lgkmcnt(5)\n"
"v_xor_b32 v246, s75, v246\n"
"v_mul_f32_dpp v19, v243, v19 row_newbcast:3 row_mask:0xf bank_mask:0xf\n"
"v_mfma_f32_16x16x32_bf16 v[164:167], v[220:223], a[188:191], v[164:167]\n"
"s_barrier\n"
"ds_write_b128 v246, v[80:83] offset:51200\n"
"v_mfma_f32_16x16x32_bf16 v[136:139], v[96:99], a[96:99], 0\n"
"v_cvt_pk_bf16_f32 v49, v50, v51\n"
"v_cvt_pk_bf16_f32 v50, v52, v53\n"
"v_mfma_f32_16x16x32_bf16 v[136:139], v[100:103], a[100:103], v[136:139]\n"
"ds_read_b64_tr_b16 a[214:215], v237 offset:12296\n"
"v_cvt_pk_bf16_f32 v51, v54, v55\n"
"v_mfma_f32_16x16x32_bf16 v[136:139], v[104:107], a[104:107], v[136:139]\n"
"ds_read_b64_tr_b16 a[42:43], v247 offset:51208\n"
"v_cvt_pk_bf16_f32 v16, v16, v17\n"
"v_mfma_f32_16x16x32_bf16 v[136:139], v[108:111], a[108:111], v[136:139]\n"
"s_waitcnt lgkmcnt(4)\n"
"ds_write_b128 v246, v[48:51] offset:50176\n"
"v_mul_f32_dpp v21, v243, v21 row_newbcast:1 row_mask:0xf bank_mask:0xf\n"
"v_mfma_f32_16x16x32_bf16 v[136:139], v[112:115], a[144:147], v[136:139]\n"
"v_cvt_pk_bf16_f32 v17, v18, v19\n"
"v_cvt_pk_bf16_f32 v18, v20, v21\n"
"v_mfma_f32_16x16x32_bf16 v[136:139], v[116:119], a[148:151], v[136:139]\n"
"ds_read_b64_tr_b16 a[224:225], v236 offset:14336\n"
"ds_read_b64_tr_b16 v[188:189], v228 offset:40960\n"
"v_mfma_f32_16x16x32_bf16 v[136:139], v[120:123], a[152:155], v[136:139]\n"
"s_waitcnt lgkmcnt(6)\n"
"ds_read_b64_tr_b16 a[40:41], v247 offset:51200\n"
"v_cvt_pk_bf16_f32 v19, v22, v23\n"
"v_mfma_f32_16x16x32_bf16 v[136:139], v[224:227], a[156:159], v[136:139]\n"
"ds_write_b128 v246, v[16:19] offset:49152\n"
"v_mfma_f32_16x16x32_bf16 v[152:155], v[224:227], a[172:175], 0\n"
"v_mfma_f32_16x16x32_bf16 v[152:155], v[120:123], a[168:171], v[152:155]\n"
"ds_read_b64_tr_b16 a[8:9], v247 offset:49152\n"
"ds_read_b64_tr_b16 a[10:11], v247 offset:49160\n"
"v_mfma_f32_16x16x32_bf16 v[152:155], v[116:119], a[164:167], v[152:155]\n"
"ds_read_b64_tr_b16 a[24:25], v247 offset:50176\n"
"ds_read_b64_tr_b16 a[26:27], v247 offset:50184\n"
"v_mfma_f32_16x16x32_bf16 v[152:155], v[112:115], a[160:163], v[152:155]\n"
"ds_read_b64_tr_b16 v[208:209], v228 offset:49152\n"
"ds_read_b64_tr_b16 v[212:213], v232 offset:49152\n"
"v_mfma_f32_16x16x32_bf16 v[152:155], v[108:111], a[124:127], v[152:155]\n"
"ds_read_b64_tr_b16 a[208:209], v236 offset:12288\n"
"v_mov_b64 v[10:11], 0\n"
"v_mfma_f32_16x16x32_bf16 v[152:155], v[104:107], a[120:123], v[152:155]\n"
"ds_read_b64_tr_b16 v[180:181], v228 offset:32768\n"
"ds_read_b64_tr_b16 v[182:183], v228 offset:34816\n"
"v_mfma_f32_16x16x32_bf16 v[152:155], v[100:103], a[116:119], v[152:155]\n"
"ds_read_b64_tr_b16 v[194:195], v232 offset:43008\n"
"v_and_b32 v243, 0x3f0, v242\n"
"v_mfma_f32_16x16x32_bf16 v[152:155], v[96:99], a[112:115], v[152:155]\n"
"ds_read_b64_tr_b16 v[184:185], v232 offset:32768\n"
"ds_read_b64_tr_b16 v[210:211], v228 offset:51200\n"
"v_mfma_f32_16x16x32_bf16 v[168:171], v[96:99], a[128:131], 0\n"
"ds_read_b64_tr_b16 a[226:227], v236 offset:14344\n"
"ds_read_b64_tr_b16 v[186:187], v232 offset:34816\n"
"v_mfma_f32_16x16x32_bf16 v[168:171], v[100:103], a[132:135], v[168:171]\n"
"ds_read_b64_tr_b16 v[190:191], v228 offset:43008\n"
"ds_read_b64_tr_b16 v[222:223], v232 offset:59392\n"
"v_mfma_f32_16x16x32_bf16 v[168:171], v[104:107], a[136:139], v[168:171]\n"
"ds_read_b64_tr_b16 v[192:193], v232 offset:40960\n"
"v_bfe_u32 v254, v242, 4, 4\n"
"v_mfma_f32_16x16x32_bf16 v[168:171], v[108:111], a[140:143], v[168:171]\n"
"s_waitcnt lgkmcnt(11)\n"
"ds_read_b64_tr_b16 v[214:215], v232 offset:51200\n"
"ds_read_b64_tr_b16 v[216:217], v228 offset:57344\n"
"v_mfma_f32_16x16x32_bf16 v[168:171], v[112:115], a[176:179], v[168:171]\n"
"ds_read_b64_tr_b16 v[100:101], v233 offset:32768\n"
"v_mov_b64 v[38:39], 0\n"
"v_mfma_f32_16x16x32_bf16 v[168:171], v[116:119], a[180:183], v[168:171]\n"
"ds_read_b64_tr_b16 v[218:219], v228 offset:59392\n"
"ds_read_b64_tr_b16 v[110:111], v233 offset:43008\n"
"v_mfma_f32_16x16x32_bf16 v[168:171], v[120:123], a[184:187], v[168:171]\n"
"s_add_u32 s41, s70, 327680\n"
"s_add_u32 m0, s71, 32768\n"
"ds_read_b64_tr_b16 v[220:221], v232 offset:57344\n"
"v_mfma_f32_16x16x32_bf16 v[168:171], v[224:227], a[188:191], v[168:171]\n"
"s_waitcnt lgkmcnt(11)\n"
"buffer_load_dwordx4 v240, s[28:31], s41 offen lds\n"
"v_mfma_f32_16x16x32_bf16 v[124:127], v[180:183], a[48:51], v[124:127]\n"
"s_waitcnt lgkmcnt(9)\n"
"v_mfma_f32_16x16x32_bf16 v[124:127], v[184:187], a[52:55], v[124:127]\n"
"s_waitcnt lgkmcnt(7)\n"
"v_mfma_f32_16x16x32_bf16 v[124:127], v[188:191], a[56:59], v[124:127]\n"
"s_waitcnt lgkmcnt(6)\n"
"s_add_u32 s40, s41, s52\n"
"ds_read_b64_tr_b16 a[222:223], v237 offset:8200\n"
"ds_read_b64_tr_b16 v[104:105], v229 offset:40960\n"
"v_mfma_f32_16x16x32_bf16 v[124:127], v[192:195], a[60:63], v[124:127]\n"
"buffer_load_dwordx4 v240, s[28:31], s40 offen offset:1024 lds\n"
"v_mfma_f32_16x16x32_bf16 v[124:127], v[208:211], a[0:3], v[124:127]\n"
"s_waitcnt lgkmcnt(5)\n"
"v_mov_b64 v[6:7], 0\n"
"v_mfma_f32_16x16x32_bf16 v[124:127], v[212:215], a[4:7], v[124:127]\n"
"s_waitcnt lgkmcnt(3)\n"
"v_mov_b64 v[202:203], 0\n"
"v_mfma_f32_16x16x32_bf16 v[124:127], v[216:219], a[8:11], v[124:127]\n"
"s_waitcnt lgkmcnt(2)\n"
"s_add_u32 s40, s41, s53\n"
"ds_read_b64_tr_b16 a[212:213], v237 offset:12288\n"
"ds_read_b64_tr_b16 v[226:227], v233 offset:59392\n"
"v_mfma_f32_16x16x32_bf16 v[124:127], v[220:223], a[12:15], v[124:127]\n"
"buffer_load_dwordx4 v240, s[28:31], s40 offen offset:2048 lds\n"
"v_mfma_f32_16x16x32_bf16 v[140:143], v[220:223], a[28:31], v[140:143]\n"
"v_mov_b64 v[44:45], 0\n"
"v_mfma_f32_16x16x32_bf16 v[140:143], v[216:219], a[24:27], v[140:143]\n"
"v_mov_b64 v[34:35], 0\n"
"v_mfma_f32_16x16x32_bf16 v[140:143], v[212:215], a[20:23], v[140:143]\n"
"ds_read_b64_tr_b16 a[210:211], v236 offset:12296\n"
"ds_read_b64_tr_b16 v[116:117], v233 offset:49152\n"
"v_mfma_f32_16x16x32_bf16 v[140:143], v[208:211], a[16:19], v[140:143]\n"
"ds_read_b64_tr_b16 a[236:237], v237 offset:10240\n"
"ds_read_b64_tr_b16 v[106:107], v229 offset:43008\n"
"v_mfma_f32_16x16x32_bf16 v[140:143], v[192:195], a[76:79], v[140:143]\n"
"ds_read_b64_tr_b16 v[98:99], v229 offset:34816\n"
"ds_read_b64_tr_b16 v[122:123], v229 offset:59392\n"
"v_mfma_f32_16x16x32_bf16 v[140:143], v[188:191], a[72:75], v[140:143]\n"
"ds_read_b64_tr_b16 a[230:231], v237 offset:14344\n"
"v_mfma_f32_16x16x32_bf16 v[140:143], v[184:187], a[68:71], v[140:143]\n"
"s_add_u32 s40, s41, s54\n"
"ds_read_b64_tr_b16 a[228:229], v237 offset:14336\n"
"ds_read_b64_tr_b16 v[114:115], v229 offset:51200\n"
"v_mfma_f32_16x16x32_bf16 v[140:143], v[180:183], a[64:67], v[140:143]\n"
"buffer_load_dwordx4 v240, s[28:31], s40 offen offset:3072 lds\n"
"v_mfma_f32_16x16x32_bf16 v[156:159], v[180:183], a[80:83], v[156:159]\n"
"s_add_u32 s41, s70, 491520\n"
"s_add_u32 m0, s71, 49152\n"
"v_bfe_u32 v255, v242, 8, 1\n"
"v_mfma_f32_16x16x32_bf16 v[156:159], v[184:187], a[84:87], v[156:159]\n"
"buffer_load_dwordx4 v240, s[28:31], s41 offen lds\n"
"v_mfma_f32_16x16x32_bf16 v[156:159], v[188:191], a[88:91], v[156:159]\n"
"v_mov_b32 v241, s60\n"
"v_mfma_f32_16x16x32_bf16 v[156:159], v[192:195], a[92:95], v[156:159]\n"
"ds_read_b64_tr_b16 a[232:233], v236 offset:10240\n"
"ds_read_b64_tr_b16 v[120:121], v229 offset:57344\n"
"v_mfma_f32_16x16x32_bf16 v[156:159], v[208:211], a[32:35], v[156:159]\n"
"ds_read_b64_tr_b16 v[112:113], v229 offset:49152\n"
"v_mfma_f32_16x16x32_bf16 v[156:159], v[212:215], a[36:39], v[156:159]\n"
"ds_read_b64_tr_b16 v[96:97], v229 offset:32768\n"
"ds_read_b64_tr_b16 v[118:119], v233 offset:51200\n"
"v_mfma_f32_16x16x32_bf16 v[156:159], v[216:219], a[40:43], v[156:159]\n"
"s_add_u32 s40, s41, s52\n"
"ds_read_b64_tr_b16 v[102:103], v233 offset:34816\n"
"ds_read_b64_tr_b16 v[108:109], v233 offset:40960\n"
"v_mfma_f32_16x16x32_bf16 v[156:159], v[220:223], a[44:47], v[156:159]\n"
"s_waitcnt lgkmcnt(7)\n"
"buffer_load_dwordx4 v240, s[28:31], s40 offen offset:1024 lds\n"
"v_mfma_f32_16x16x32_bf16 v[172:175], v[220:223], a[228:231], v[172:175]\n"
"s_add_u32 s40, s41, s53\n"
"v_mfma_f32_16x16x32_bf16 v[172:175], v[216:219], a[224:227], v[172:175]\n"
"buffer_load_dwordx4 v240, s[28:31], s40 offen offset:2048 lds\n"
"v_mfma_f32_16x16x32_bf16 v[172:175], v[212:215], a[212:215], v[172:175]\n"
"s_add_u32 s40, s41, s54\n"
"v_mov_b64 v[30:31], 0\n"
"v_mfma_f32_16x16x32_bf16 v[172:175], v[208:211], a[208:211], v[172:175]\n"
"buffer_load_dwordx4 v240, s[28:31], s40 offen offset:3072 lds\n"
"v_mfma_f32_16x16x32_bf16 v[172:175], v[192:195], a[236:239], v[172:175]\n"
"s_waitcnt lgkmcnt(4)\n"
"s_mul_i32 s62, s70, 224\n"
"v_mfma_f32_16x16x32_bf16 v[172:175], v[188:191], a[232:235], v[172:175]\n"
"s_add_u32 s20, s47, 3\n"
"v_mul_u32_u24 v254, 2560, v254\n"
"v_lshl_add_u32 v254, v255, 5, v254\n"
"v_mfma_f32_16x16x32_bf16 v[172:175], v[184:187], a[220:223], v[172:175]\n"
"s_add_u32 s70, s61, s20\n"
"ds_read_b64_tr_b16 v[224:225], v233 offset:57344\n"
"v_mfma_f32_16x16x32_bf16 v[172:175], v[180:183], a[216:219], v[172:175]\n"
"s_waitcnt lgkmcnt(1)\n"
"s_and_b32 s70, s70, 7\n"
"v_mfma_f32_16x16x32_bf16 v[128:131], v[96:99], a[48:51], v[128:131]\n"
"s_cmp_ge_u32 s47, 5\ns_cselect_b32 s70, s20, s70\n"
"ds_read_b64_tr_b16 v[182:183], v230 offset:34816\n"
"v_mov_b64 v[4:5], 0\n"
"v_mfma_f32_16x16x32_bf16 v[128:131], v[100:103], a[52:55], v[128:131]\n"
"s_cmp_eq_u32 s47, 7\ns_cselect_b32 s70, s61, s70\n"
"v_mov_b64 v[16:17], 0\n"
"v_mfma_f32_16x16x32_bf16 v[128:131], v[104:107], a[56:59], v[128:131]\n"
"s_lshl_b32 s70, s70, 7\n"
"ds_read_b64_tr_b16 v[180:181], v230 offset:32768\n"
"v_xor_b32 v232, s75, v232\n"
"v_mfma_f32_16x16x32_bf16 v[128:131], v[108:111], a[60:63], v[128:131]\n"
"s_xor_b32 s71, s71, s75\n"
"v_mov_b64 v[48:49], 0\n"
"v_mfma_f32_16x16x32_bf16 v[128:131], v[112:115], a[0:3], v[128:131]\n"
"v_lshlrev_b32_e64 v245, 2, s72\n"
"ds_read_b64_tr_b16 v[186:187], v234 offset:34816\n"
"v_mfma_f32_16x16x32_bf16 v[128:131], v[116:119], a[4:7], v[128:131]\n"
"global_load_dword v244, v245, s[6:7] sc1\n"
"v_mfma_f32_16x16x32_bf16 v[128:131], v[120:123], a[8:11], v[128:131]\n"
"s_waitcnt lgkmcnt(3)\n"
"v_mfma_f32_16x16x32_bf16 v[128:131], v[224:227], a[12:15], v[128:131]\n"
"v_cvt_pk_bf16_f32 v124, v124, v125\n"
"v_mfma_f32_16x16x32_bf16 v[144:147], v[224:227], a[28:31], v[144:147]\n"
"ds_read_b64_tr_b16 v[194:195], v234 offset:43008\n"
"v_mfma_f32_16x16x32_bf16 v[144:147], v[120:123], a[24:27], v[144:147]\n"
"v_mfma_f32_16x16x32_bf16 v[144:147], v[116:119], a[20:23], v[144:147]\n"
"ds_read_b64_tr_b16 v[218:219], v230 offset:59392\n"
"v_cvt_pk_bf16_f32 v140, v140, v141\n"
"v_mfma_f32_16x16x32_bf16 v[144:147], v[112:115], a[16:19], v[144:147]\n"
"ds_read_b64_tr_b16 v[188:189], v230 offset:40960\n"
"v_add_u32 v236, s94, v243\n"
"v_mfma_f32_16x16x32_bf16 v[144:147], v[108:111], a[76:79], v[144:147]\n"
"v_cvt_pk_bf16_f32 v156, v156, v157\n"
"v_mfma_f32_16x16x32_bf16 v[144:147], v[104:107], a[72:75], v[144:147]\n"
"ds_read_b64_tr_b16 v[210:211], v230 offset:51200\n"
"v_mov_b64 v[14:15], 0\n"
"v_mfma_f32_16x16x32_bf16 v[144:147], v[100:103], a[68:71], v[144:147]\n"
"v_mov_b64 v[24:25], 0\n"
"v_cvt_pk_bf16_f32 v125, v126, v127\n"
"v_mfma_f32_16x16x32_bf16 v[144:147], v[96:99], a[64:67], v[144:147]\n"
"ds_read_b64_tr_b16 v[214:215], v234 offset:51200\n"
"v_mfma_f32_16x16x32_bf16 v[160:163], v[96:99], a[80:83], v[160:163]\n"
"v_mfma_f32_16x16x32_bf16 v[160:163], v[100:103], a[84:87], v[160:163]\n"
"ds_read_b64_tr_b16 v[208:209], v230 offset:49152\n"
"v_mfma_f32_16x16x32_bf16 v[160:163], v[104:107], a[88:91], v[160:163]\n"
"v_cvt_pk_bf16_f32 v126, v128, v129\n"
"v_mfma_f32_16x16x32_bf16 v[160:163], v[108:111], a[92:95], v[160:163]\n"
"ds_read_b64_tr_b16 v[222:223], v234 offset:59392\n"
"v_cvt_pk_bf16_f32 v127, v130, v131\n"
"v_mfma_f32_16x16x32_bf16 v[160:163], v[112:115], a[32:35], v[160:163]\n"
"ds_read_b64_tr_b16 v[216:217], v230 offset:57344\n"
"v_add_u32 v243, s96, v243\n"
"v_mfma_f32_16x16x32_bf16 v[160:163], v[116:119], a[36:39], v[160:163]\n"
"ds_read_b64_tr_b16 v[190:191], v230 offset:43008\n"
"v_xor_b32 v247, s75, v247\n"
"v_mfma_f32_16x16x32_bf16 v[160:163], v[120:123], a[40:43], v[160:163]\n"
"v_mfma_f32_16x16x32_bf16 v[160:163], v[224:227], a[44:47], v[160:163]\n"
"ds_read_b64_tr_b16 v[184:185], v234 offset:32768\n"
"buffer_store_dwordx4 v[124:127], v243, s[32:35], s16 offen sc0 sc1\n"
"v_mfma_f32_16x16x32_bf16 v[176:179], v[224:227], a[228:231], v[176:179]\n"
"v_mfma_f32_16x16x32_bf16 v[176:179], v[120:123], a[224:227], v[176:179]\n"
"ds_read_b64_tr_b16 v[192:193], v234 offset:40960\n"
"v_mov_b64 v[0:1], 0\n"
"v_mfma_f32_16x16x32_bf16 v[176:179], v[116:119], a[212:215], v[176:179]\n"
"ds_read_b64_tr_b16 v[212:213], v234 offset:49152\n"
"v_cvt_pk_bf16_f32 v141, v142, v143\n"
"v_mfma_f32_16x16x32_bf16 v[176:179], v[112:115], a[208:211], v[176:179]\n"
"v_mov_b64 v[26:27], 0\n"
"v_mfma_f32_16x16x32_bf16 v[176:179], v[108:111], a[236:239], v[176:179]\n"
"ds_read_b64_tr_b16 v[112:113], v231 offset:49152\n"
"v_mfma_f32_16x16x32_bf16 v[176:179], v[104:107], a[232:235], v[176:179]\n"
"ds_read_b64_tr_b16 v[220:221], v234 offset:57344\n"
"v_bfe_u32 v255, v242, 9, 1\n"
"v_mfma_f32_16x16x32_bf16 v[176:179], v[100:103], a[220:223], v[176:179]\n"
"v_mov_b64 v[40:41], 0\n"
"v_mfma_f32_16x16x32_bf16 v[176:179], v[96:99], a[216:219], v[176:179]\n"
"s_waitcnt lgkmcnt(4)\n"
"ds_read_b64_tr_b16 v[118:119], v235 offset:51200\n"
"v_mov_b64 v[50:51], 0\n"
"v_mfma_f32_16x16x32_bf16 v[132:135], v[180:183], a[48:51], v[132:135]\n"
"v_xor_b32 v228, s75, v228\n"
"v_mfma_f32_16x16x32_bf16 v[132:135], v[184:187], a[52:55], v[132:135]\n"
"ds_read_b64_tr_b16 v[100:101], v235 offset:32768\n"
"v_cvt_pk_bf16_f32 v142, v144, v145\n"
"v_mfma_f32_16x16x32_bf16 v[132:135], v[188:191], a[56:59], v[132:135]\n"
"s_waitcnt lgkmcnt(4)\n"
"v_mov_b64 v[46:47], 0\n"
"v_cvt_pk_bf16_f32 v143, v146, v147\n"
"v_mfma_f32_16x16x32_bf16 v[132:135], v[192:195], a[60:63], v[132:135]\n"
"ds_read_b64_tr_b16 v[104:105], v231 offset:40960\n"
"v_mov_b64 v[18:19], 0\n"
"v_mfma_f32_16x16x32_bf16 v[132:135], v[208:211], a[0:3], v[132:135]\n"
"v_mfma_f32_16x16x32_bf16 v[132:135], v[212:215], a[4:7], v[132:135]\n"
"ds_read_b64_tr_b16 v[98:99], v231 offset:34816\n"
"v_mfma_f32_16x16x32_bf16 v[132:135], v[216:219], a[8:11], v[132:135]\n"
"s_waitcnt lgkmcnt(4)\n"
"ds_read_b64_tr_b16 v[96:97], v231 offset:32768\n"
"v_mov_b64 v[2:3], 0\n"
"v_mfma_f32_16x16x32_bf16 v[132:135], v[220:223], a[12:15], v[132:135]\n"
"buffer_store_dwordx4 v[140:143], v243, s[32:35], s16 offen offset:2048 sc0 sc1\n"
"ds_read_b64_tr_b16 v[110:111], v235 offset:43008\n"
"v_mfma_f32_16x16x32_bf16 v[148:151], v[220:223], a[28:31], v[148:151]\n"
"v_mov_b64 v[198:199], 0\n"
"v_mov_b64 v[20:21], 0\n"
"v_mfma_f32_16x16x32_bf16 v[148:151], v[216:219], a[24:27], v[148:151]\n"
"ds_read_b64_tr_b16 v[120:121], v231 offset:57344\n"
"v_mfma_f32_16x16x32_bf16 v[148:151], v[212:215], a[20:23], v[148:151]\n"
"ds_read_b64_tr_b16 v[114:115], v231 offset:51200\n"
"v_mov_b64 v[36:37], 0\n"
"v_mfma_f32_16x16x32_bf16 v[148:151], v[208:211], a[16:19], v[148:151]\n"
"ds_read_b64_tr_b16 v[102:103], v235 offset:34816\n"
"v_cvt_pk_bf16_f32 v157, v158, v159\n"
"v_mfma_f32_16x16x32_bf16 v[148:151], v[192:195], a[76:79], v[148:151]\n"
"v_mfma_f32_16x16x32_bf16 v[148:151], v[188:191], a[72:75], v[148:151]\n"
"ds_read_b64_tr_b16 v[108:109], v235 offset:40960\n"
"v_lshl_add_u32 v254, v255, 4, v254\n"
"v_mfma_f32_16x16x32_bf16 v[148:151], v[184:187], a[68:71], v[148:151]\n"
"v_mov_b64 v[42:43], 0\n"
"v_mfma_f32_16x16x32_bf16 v[148:151], v[180:183], a[64:67], v[148:151]\n"
"ds_read_b64_tr_b16 v[122:123], v231 offset:59392\n"
"v_add_u32 v238, s97, v254\n"
"v_mfma_f32_16x16x32_bf16 v[164:167], v[180:183], a[80:83], v[164:167]\n"
"ds_read_b64_tr_b16 v[106:107], v231 offset:43008\n"
"v_cvt_pk_bf16_f32 v158, v160, v161\n"
"v_mfma_f32_16x16x32_bf16 v[164:167], v[184:187], a[84:87], v[164:167]\n"
"v_mfma_f32_16x16x32_bf16 v[164:167], v[188:191], a[88:91], v[164:167]\n"
"ds_read_b64_tr_b16 v[226:227], v235 offset:59392\n"
"v_mov_b64 v[32:33], 0\n"
"v_mfma_f32_16x16x32_bf16 v[164:167], v[192:195], a[92:95], v[164:167]\n"
"v_mov_b64 v[196:197], 0\n"
"v_mfma_f32_16x16x32_bf16 v[164:167], v[208:211], a[32:35], v[164:167]\n"
"ds_read_b64_tr_b16 v[116:117], v235 offset:49152\n"
"v_cvt_pk_bf16_f32 v159, v162, v163\n"
"v_mfma_f32_16x16x32_bf16 v[164:167], v[212:215], a[36:39], v[164:167]\n"
"v_mfma_f32_16x16x32_bf16 v[164:167], v[216:219], a[40:43], v[164:167]\n"
"ds_read_b64_tr_b16 v[224:225], v235 offset:57344\n"
"v_mfma_f32_16x16x32_bf16 v[164:167], v[220:223], a[44:47], v[164:167]\n"
"s_barrier\n"
"s_waitcnt lgkmcnt(4)\n"
"v_mfma_f32_16x16x32_bf16 v[136:139], v[96:99], a[48:51], v[136:139]\n"
"ds_read_b64_tr_b16 v[186:187], v232 offset:2048\n"
"v_mfma_f32_16x16x32_bf16 v[136:139], v[100:103], a[52:55], v[136:139]\n"
"s_waitcnt lgkmcnt(4)\n"
"ds_read_b64_tr_b16 v[184:185], v232 offset:0\n"
"v_mov_b64 v[22:23], 0\n"
"v_mfma_f32_16x16x32_bf16 v[136:139], v[104:107], a[56:59], v[136:139]\n"
"ds_read_b64_tr_b16 v[190:191], v228 offset:10240\n"
"v_mov_b64 v[200:201], 0\n"
"v_mfma_f32_16x16x32_bf16 v[136:139], v[108:111], a[60:63], v[136:139]\n"
"ds_read_b64_tr_b16 v[182:183], v228 offset:2048\n"
"v_mfma_f32_16x16x32_bf16 v[136:139], v[112:115], a[0:3], v[136:139]\n"
"s_waitcnt lgkmcnt(5)\n"
"ds_read_b64_tr_b16 v[212:213], v232 offset:16384\n"
"v_mfma_f32_16x16x32_bf16 v[136:139], v[116:119], a[4:7], v[136:139]\n"
"ds_read_b64_tr_b16 v[192:193], v232 offset:8192\n"
"v_mov_b64 v[54:55], 0\n"
"v_mfma_f32_16x16x32_bf16 v[136:139], v[120:123], a[8:11], v[136:139]\n"
"s_waitcnt lgkmcnt(6)\n"
"ds_read_b64_tr_b16 v[220:221], v232 offset:24576\n"
"v_mfma_f32_16x16x32_bf16 v[136:139], v[224:227], a[12:15], v[136:139]\n"
"buffer_store_dwordx4 v[156:159], v243, s[32:35], s17 offen sc0 sc1\n"
"ds_read_b64_tr_b16 v[216:217], v228 offset:24576\n"
"v_mfma_f32_16x16x32_bf16 v[152:155], v[224:227], a[28:31], v[152:155]\n"
"ds_read_b64_tr_b16 v[222:223], v232 offset:26624\n"
"v_mfma_f32_16x16x32_bf16 v[152:155], v[120:123], a[24:27], v[152:155]\n"
"ds_read_b64_tr_b16 v[194:195], v232 offset:10240\n"
"v_mfma_f32_16x16x32_bf16 v[152:155], v[116:119], a[20:23], v[152:155]\n"
"ds_read_b64_tr_b16 v[214:215], v232 offset:18432\n"
"v_mfma_f32_16x16x32_bf16 v[152:155], v[112:115], a[16:19], v[152:155]\n"
"ds_read_b64_tr_b16 v[208:209], v228 offset:16384\n"
"v_mfma_f32_16x16x32_bf16 v[152:155], v[108:111], a[76:79], v[152:155]\n"
"ds_read_b64_tr_b16 v[210:211], v228 offset:18432\n"
"v_mfma_f32_16x16x32_bf16 v[152:155], v[104:107], a[72:75], v[152:155]\n"
"ds_read_b64_tr_b16 v[180:181], v228 offset:0\n"
"v_mov_b64 v[28:29], 0\n"
"v_mfma_f32_16x16x32_bf16 v[152:155], v[100:103], a[68:71], v[152:155]\n"
"ds_read_b64_tr_b16 v[188:189], v228 offset:8192\n"
"v_mfma_f32_16x16x32_bf16 v[152:155], v[96:99], a[64:67], v[152:155]\n"
"ds_read_b64_tr_b16 v[218:219], v228 offset:26624\n"
"v_mfma_f32_16x16x32_bf16 v[168:171], v[96:99], a[80:83], v[168:171]\n"
"v_add_u32 v252, s98, v254\n"
"v_mov_b64 v[52:53], 0\n"
"v_mfma_f32_16x16x32_bf16 v[168:171], v[100:103], a[84:87], v[168:171]\n"
"v_xor_b32 v246, s75, v246\n"
"v_mfma_f32_16x16x32_bf16 v[168:171], v[104:107], a[88:91], v[168:171]\n"
"v_mfma_f32_16x16x32_bf16 v[168:171], v[108:111], a[92:95], v[168:171]\n"
"v_mfma_f32_16x16x32_bf16 v[168:171], v[112:115], a[32:35], v[168:171]\n"
"v_mfma_f32_16x16x32_bf16 v[168:171], v[116:119], a[36:39], v[168:171]\n"
"v_mfma_f32_16x16x32_bf16 v[168:171], v[120:123], a[40:43], v[168:171]\n"
"s_add_u32 s47, s47, 1\n"
"v_xor_b32 v239, 64, v238\n"
"v_mov_b64 v[12:13], 0\n"
"v_mfma_f32_16x16x32_bf16 v[168:171], v[224:227], a[44:47], v[168:171]\n"
"s_waitcnt lgkmcnt(0)\n"
"v_add_u32 v253, s99, v254\n"
"v_mov_b64 v[8:9], 0\n"
"LGL_%=:\n"
"v_mfma_f32_16x16x32_bf16 v[124:127], v[180:183], a[96:99], 0\n"
"v_cvt_pk_bf16_f32 v172, v172, v173\n"
"v_cvt_pk_bf16_f32 v173, v174, v175\n"
"v_mfma_f32_16x16x32_bf16 v[124:127], v[184:187], a[100:103], v[124:127]\n"
"v_cvt_pk_bf16_f32 v174, v176, v177\n"
"v_cvt_pk_bf16_f32 v175, v178, v179\n"
"v_mfma_f32_16x16x32_bf16 v[124:127], v[188:191], a[104:107], v[124:127]\n"
"v_cvt_pk_bf16_f32 v132, v132, v133\n"
"v_cvt_pk_bf16_f32 v133, v134, v135\n"
"v_mfma_f32_16x16x32_bf16 v[124:127], v[192:195], a[108:111], v[124:127]\n"
"v_cvt_pk_bf16_f32 v134, v136, v137\n"
"v_cvt_pk_bf16_f32 v135, v138, v139\n"
"v_mfma_f32_16x16x32_bf16 v[124:127], v[208:211], a[144:147], v[124:127]\n"
"v_xor_b32 v229, s75, v229\n"
"v_xor_b32 v233, s75, v233\n"
"v_mfma_f32_16x16x32_bf16 v[124:127], v[212:215], a[148:151], v[124:127]\n"
"s_waitcnt lgkmcnt(0)\n"
"ds_read_b64_tr_b16 v[96:97], v229 offset:0\n"
"v_cvt_pk_bf16_f32 v148, v148, v149\n"
"v_mfma_f32_16x16x32_bf16 v[124:127], v[216:219], a[152:155], v[124:127]\n"
"ds_read_b64_tr_b16 v[98:99], v229 offset:2048\n"
"v_cvt_pk_bf16_f32 v149, v150, v151\n"
"v_mfma_f32_16x16x32_bf16 v[124:127], v[220:223], a[156:159], v[124:127]\n"
"buffer_store_dwordx4 v[172:175], v243, s[32:35], s17 offen offset:2048 sc0 sc1\n"
"v_cvt_pk_bf16_f32 v150, v152, v153\n"
"v_mfma_f32_16x16x32_bf16 v[140:143], v[220:223], a[172:175], 0\n"
"ds_read_b64_tr_b16 v[100:101], v233 offset:0\n"
"v_cvt_pk_bf16_f32 v151, v154, v155\n"
"v_mfma_f32_16x16x32_bf16 v[140:143], v[216:219], a[168:171], v[140:143]\n"
"ds_read_b64_tr_b16 v[102:103], v233 offset:2048\n"
"v_xor_b32 v230, s75, v230\n"
"v_mfma_f32_16x16x32_bf16 v[140:143], v[212:215], a[164:167], v[140:143]\n"
"ds_read_b64_tr_b16 v[104:105], v229 offset:8192\n"
"v_xor_b32 v234, s75, v234\n"
"v_mfma_f32_16x16x32_bf16 v[140:143], v[208:211], a[160:163], v[140:143]\n"
"ds_read_b64_tr_b16 v[106:107], v229 offset:10240\n"
"v_cvt_pk_bf16_f32 v164, v164, v165\n"
"v_mfma_f32_16x16x32_bf16 v[140:143], v[192:195], a[124:127], v[140:143]\n"
"ds_read_b64_tr_b16 v[108:109], v233 offset:8192\n"
"v_cvt_pk_bf16_f32 v165, v166, v167\n"
"v_mfma_f32_16x16x32_bf16 v[140:143], v[188:191], a[120:123], v[140:143]\n"
"s_add_u32 s41, s70, 0\n"
"ds_read_b64_tr_b16 v[110:111], v233 offset:10240\n"
"v_cvt_pk_bf16_f32 v166, v168, v169\n"
"v_mfma_f32_16x16x32_bf16 v[140:143], v[184:187], a[116:119], v[140:143]\n"
"s_mov_b32 m0, s71\n"
"ds_read_b64_tr_b16 v[112:113], v229 offset:16384\n"
"v_cvt_pk_bf16_f32 v167, v170, v171\n"
"v_mfma_f32_16x16x32_bf16 v[140:143], v[180:183], a[112:115], v[140:143]\n"
"buffer_load_dwordx4 v240, s[28:31], s41 offen lds\n"
"v_mfma_f32_16x16x32_bf16 v[156:159], v[180:183], a[128:131], 0\n"
"ds_read_b64_tr_b16 v[114:115], v229 offset:18432\n"
"v_xor_b32 v231, s75, v231\n"
"v_mfma_f32_16x16x32_bf16 v[156:159], v[184:187], a[132:135], v[156:159]\n"
"ds_read_b64_tr_b16 v[116:117], v233 offset:16384\n"
"v_xor_b32 v235, s75, v235\n"
"v_mfma_f32_16x16x32_bf16 v[156:159], v[188:191], a[136:139], v[156:159]\n"
"ds_read_b64_tr_b16 v[118:119], v233 offset:18432\n"
"v_add_u32 v241, 1, v241\n"
"v_mfma_f32_16x16x32_bf16 v[156:159], v[192:195], a[140:143], v[156:159]\n"
"ds_read_b64_tr_b16 v[120:121], v229 offset:24576\n"
"v_mfma_f32_16x16x32_bf16 v[156:159], v[208:211], a[176:179], v[156:159]\n"
"ds_read_b64_tr_b16 v[122:123], v229 offset:26624\n"
"v_mfma_f32_16x16x32_bf16 v[156:159], v[212:215], a[180:183], v[156:159]\n"
"ds_read_b64_tr_b16 v[224:225], v233 offset:24576\n"
"v_mfma_f32_16x16x32_bf16 v[156:159], v[216:219], a[184:187], v[156:159]\n"
"ds_read_b64_tr_b16 v[226:227], v233 offset:26624\n"
"v_mfma_f32_16x16x32_bf16 v[156:159], v[220:223], a[188:191], v[156:159]\n"
"buffer_store_dwordx4 v[132:135], v243, s[32:35], s16 offen offset:1024 sc0 sc1\n"
"v_mfma_f32_16x16x32_bf16 v[172:175], v[220:223], a[252:255], 0\n"
"ds_read_b64_tr_b16 v[220:221], v234 offset:24576\n"
"v_mfma_f32_16x16x32_bf16 v[172:175], v[216:219], a[248:251], v[172:175]\n"
"ds_read_b64_tr_b16 v[216:217], v230 offset:24576\n"
"v_mfma_f32_16x16x32_bf16 v[172:175], v[212:215], a[244:247], v[172:175]\n"
"ds_read_b64_tr_b16 v[212:213], v234 offset:16384\n"
"v_mfma_f32_16x16x32_bf16 v[172:175], v[208:211], a[240:243], v[172:175]\n"
"ds_read_b64_tr_b16 v[208:209], v230 offset:16384\n"
"v_mfma_f32_16x16x32_bf16 v[172:175], v[192:195], a[204:207], v[172:175]\n"
"ds_read_b64_tr_b16 v[192:193], v234 offset:8192\n"
"v_mfma_f32_16x16x32_bf16 v[172:175], v[188:191], a[200:203], v[172:175]\n"
"ds_read_b64_tr_b16 v[188:189], v230 offset:8192\n"
"v_mfma_f32_16x16x32_bf16 v[172:175], v[184:187], a[196:199], v[172:175]\n"
"s_add_u32 s40, s41, s52\n"
"ds_read_b64_tr_b16 v[184:185], v234 offset:0\n"
"v_mfma_f32_16x16x32_bf16 v[172:175], v[180:183], a[192:195], v[172:175]\n"
"s_waitcnt lgkmcnt(7)\n"
"buffer_load_dwordx4 v240, s[28:31], s40 offen offset:1024 lds\n"
"v_mfma_f32_16x16x32_bf16 v[128:131], v[96:99], a[96:99], 0\n"
"ds_read_b64_tr_b16 v[180:181], v230 offset:0\n"
"v_mfma_f32_16x16x32_bf16 v[128:131], v[100:103], a[100:103], v[128:131]\n"
"ds_read_b64_tr_b16 v[182:183], v230 offset:2048\n"
"v_mfma_f32_16x16x32_bf16 v[128:131], v[104:107], a[104:107], v[128:131]\n"
"ds_read_b64_tr_b16 v[186:187], v234 offset:2048\n"
"v_mfma_f32_16x16x32_bf16 v[128:131], v[108:111], a[108:111], v[128:131]\n"
"ds_read_b64_tr_b16 v[190:191], v230 offset:10240\n"
"v_mfma_f32_16x16x32_bf16 v[128:131], v[112:115], a[144:147], v[128:131]\n"
"s_waitcnt vmcnt(6)\n"
"ds_read_b64_tr_b16 v[194:195], v234 offset:10240\n"
"v_mfma_f32_16x16x32_bf16 v[128:131], v[116:119], a[148:151], v[128:131]\n"
"v_subrev_u32 v244, s21, v244\n"
"ds_read_b64_tr_b16 v[210:211], v230 offset:18432\n"
"v_mfma_f32_16x16x32_bf16 v[128:131], v[120:123], a[152:155], v[128:131]\n"
"v_cmp_lt_u32 vcc, s69, v244\n"
"ds_read_b64_tr_b16 v[214:215], v234 offset:18432\n"
"v_mfma_f32_16x16x32_bf16 v[128:131], v[224:227], a[156:159], v[128:131]\n"
"ds_read_b64_tr_b16 v[218:219], v230 offset:26624\n"
"s_cbranch_vccnz LGP_%=\nLGPK_%=:\n"
"v_mfma_f32_16x16x32_bf16 v[144:147], v[224:227], a[172:175], 0\n"
"s_sub_u32 s72, s72, 1\n"
"ds_read_b64_tr_b16 v[222:223], v234 offset:26624\n"
"v_mfma_f32_16x16x32_bf16 v[144:147], v[120:123], a[168:171], v[144:147]\n"
"s_and_b32 s72, s72, 7\n"
"v_mfma_f32_16x16x32_bf16 v[144:147], v[116:119], a[164:167], v[144:147]\n"
"v_mfma_f32_16x16x32_bf16 v[144:147], v[112:115], a[160:163], v[144:147]\n"
"buffer_load_dwordx4 v[72:75], v243, s[32:35], s18 offen sc1\n"
"v_mfma_f32_16x16x32_bf16 v[144:147], v[108:111], a[124:127], v[144:147]\n"
"v_mfma_f32_16x16x32_bf16 v[144:147], v[104:107], a[120:123], v[144:147]\n"
"v_mfma_f32_16x16x32_bf16 v[144:147], v[100:103], a[116:119], v[144:147]\n"
"v_mfma_f32_16x16x32_bf16 v[144:147], v[96:99], a[112:115], v[144:147]\n"
"buffer_store_dwordx4 v[148:151], v243, s[32:35], s16 offen offset:3072 sc0 sc1\n"
"v_mfma_f32_16x16x32_bf16 v[160:163], v[96:99], a[128:131], 0\n"
"v_mfma_f32_16x16x32_bf16 v[160:163], v[100:103], a[132:135], v[160:163]\n"
"v_mfma_f32_16x16x32_bf16 v[160:163], v[104:107], a[136:139], v[160:163]\n"
"v_mfma_f32_16x16x32_bf16 v[160:163], v[108:111], a[140:143], v[160:163]\n"
"v_mfma_f32_16x16x32_bf16 v[160:163], v[112:115], a[176:179], v[160:163]\n"
"v_mfma_f32_16x16x32_bf16 v[160:163], v[116:119], a[180:183], v[160:163]\n"
"v_mfma_f32_16x16x32_bf16 v[160:163], v[120:123], a[184:187], v[160:163]\n"
"s_add_u32 s40, s41, s53\n"
"v_mfma_f32_16x16x32_bf16 v[160:163], v[224:227], a[188:191], v[160:163]\n"
"buffer_load_dwordx4 v240, s[28:31], s40 offen offset:2048 lds\n"
"v_mfma_f32_16x16x32_bf16 v[176:179], v[224:227], a[252:255], 0\n"
"ds_read_b64_tr_b16 v[224:225], v235 offset:24576\n"
"v_mfma_f32_16x16x32_bf16 v[176:179], v[120:123], a[248:251], v[176:179]\n"
"ds_read_b64_tr_b16 v[120:121], v231 offset:24576\n"
"v_mfma_f32_16x16x32_bf16 v[176:179], v[116:119], a[244:247], v[176:179]\n"
"ds_read_b64_tr_b16 v[116:117], v235 offset:16384\n"
"v_mfma_f32_16x16x32_bf16 v[176:179], v[112:115], a[240:243], v[176:179]\n"
"buffer_load_dwordx4 v[76:79], v243, s[32:35], s18 offen offset:1024 sc1\n"
"v_mfma_f32_16x16x32_bf16 v[176:179], v[108:111], a[204:207], v[176:179]\n"
"ds_read_b64_tr_b16 v[108:109], v235 offset:8192\n"
"v_mfma_f32_16x16x32_bf16 v[176:179], v[104:107], a[200:203], v[176:179]\n"
"ds_read_b64_tr_b16 v[104:105], v231 offset:8192\n"
"v_mfma_f32_16x16x32_bf16 v[176:179], v[100:103], a[196:199], v[176:179]\n"
"s_add_u32 s40, s41, s54\n"
"ds_read_b64_tr_b16 v[100:101], v235 offset:0\n"
"v_mfma_f32_16x16x32_bf16 v[176:179], v[96:99], a[192:195], v[176:179]\n"
"s_waitcnt lgkmcnt(6)\n"
"buffer_load_dwordx4 v240, s[28:31], s40 offen offset:3072 lds\n"
"v_mfma_f32_16x16x32_bf16 v[132:135], v[180:183], a[96:99], 0\n"
"ds_read_b64_tr_b16 v[96:97], v231 offset:0\n"
"v_mfma_f32_16x16x32_bf16 v[132:135], v[184:187], a[100:103], v[132:135]\n"
"ds_read_b64_tr_b16 v[98:99], v231 offset:2048\n"
"v_mfma_f32_16x16x32_bf16 v[132:135], v[188:191], a[104:107], v[132:135]\n"
"ds_read_b64_tr_b16 v[102:103], v235 offset:2048\n"
"v_mfma_f32_16x16x32_bf16 v[132:135], v[192:195], a[108:111], v[132:135]\n"
"ds_read_b64_tr_b16 v[106:107], v231 offset:10240\n"
"v_mfma_f32_16x16x32_bf16 v[132:135], v[208:211], a[144:147], v[132:135]\n"
"ds_read_b64_tr_b16 v[110:111], v235 offset:10240\n"
"v_mfma_f32_16x16x32_bf16 v[132:135], v[212:215], a[148:151], v[132:135]\n"
"ds_read_b64_tr_b16 v[112:113], v231 offset:16384\n"
"v_mfma_f32_16x16x32_bf16 v[132:135], v[216:219], a[152:155], v[132:135]\n"
"ds_read_b64_tr_b16 v[114:115], v231 offset:18432\n"
"v_mfma_f32_16x16x32_bf16 v[132:135], v[220:223], a[156:159], v[132:135]\n"
"buffer_store_dwordx4 v[164:167], v243, s[32:35], s17 offen offset:1024 sc0 sc1\n"
"v_mfma_f32_16x16x32_bf16 v[148:151], v[220:223], a[172:175], 0\n"
"ds_read_b64_tr_b16 v[118:119], v235 offset:18432\n"
"v_mfma_f32_16x16x32_bf16 v[148:151], v[216:219], a[168:171], v[148:151]\n"
"ds_read_b64_tr_b16 v[122:123], v231 offset:26624\n"
"v_mfma_f32_16x16x32_bf16 v[148:151], v[212:215], a[164:167], v[148:151]\n"
"ds_read_b64_tr_b16 v[226:227], v235 offset:26624\n"
"v_mfma_f32_16x16x32_bf16 v[148:151], v[208:211], a[160:163], v[148:151]\n"
"buffer_load_dwordx4 v[80:83], v243, s[32:35], s18 offen offset:2048 sc1\n"
"v_mfma_f32_16x16x32_bf16 v[148:151], v[192:195], a[124:127], v[148:151]\n"
"v_mfma_f32_16x16x32_bf16 v[148:151], v[188:191], a[120:123], v[148:151]\n"
"s_add_u32 s41, s70, 163840\n"
"v_mfma_f32_16x16x32_bf16 v[148:151], v[184:187], a[116:119], v[148:151]\n"
"s_add_u32 m0, s71, 16384\n"
"v_mfma_f32_16x16x32_bf16 v[148:151], v[180:183], a[112:115], v[148:151]\n"
"buffer_load_dwordx4 v240, s[28:31], s41 offen lds\n"
"v_mfma_f32_16x16x32_bf16 v[164:167], v[180:183], a[128:131], 0\n"
"v_mfma_f32_16x16x32_bf16 v[164:167], v[184:187], a[132:135], v[164:167]\n"
"v_mfma_f32_16x16x32_bf16 v[164:167], v[188:191], a[136:139], v[164:167]\n"
"v_mfma_f32_16x16x32_bf16 v[164:167], v[192:195], a[140:143], v[164:167]\n"
"v_mfma_f32_16x16x32_bf16 v[164:167], v[208:211], a[176:179], v[164:167]\n"
"v_mfma_f32_16x16x32_bf16 v[164:167], v[212:215], a[180:183], v[164:167]\n"
"v_mfma_f32_16x16x32_bf16 v[164:167], v[216:219], a[184:187], v[164:167]\n"
"s_waitcnt vmcnt(12)\n"
"v_mfma_f32_16x16x32_bf16 v[164:167], v[220:223], a[188:191], v[164:167]\n"
"s_barrier\n"
"s_waitcnt lgkmcnt(0)\n"
"v_mfma_f32_16x16x32_bf16 v[136:139], v[96:99], a[96:99], 0\n"
"ds_read_b64_tr_b16 v[180:181], v228 offset:32768\n"
"v_mfma_f32_16x16x32_bf16 v[136:139], v[100:103], a[100:103], v[136:139]\n"
"ds_read_b64_tr_b16 v[182:183], v228 offset:34816\n"
"v_mfma_f32_16x16x32_bf16 v[136:139], v[104:107], a[104:107], v[136:139]\n"
"ds_read_b64_tr_b16 v[184:185], v232 offset:32768\n"
"v_mfma_f32_16x16x32_bf16 v[136:139], v[108:111], a[108:111], v[136:139]\n"
"buffer_load_dwordx4 v[84:87], v243, s[32:35], s18 offen offset:3072 sc1\n"
"v_mfma_f32_16x16x32_bf16 v[136:139], v[112:115], a[144:147], v[136:139]\n"
"ds_read_b64_tr_b16 v[186:187], v232 offset:34816\n"
"v_mfma_f32_16x16x32_bf16 v[136:139], v[116:119], a[148:151], v[136:139]\n"
"ds_read_b64_tr_b16 v[188:189], v228 offset:40960\n"
"v_mfma_f32_16x16x32_bf16 v[136:139], v[120:123], a[152:155], v[136:139]\n"
"s_add_u32 s40, s41, s52\n"
"ds_read_b64_tr_b16 v[190:191], v228 offset:43008\n"
"v_mfma_f32_16x16x32_bf16 v[136:139], v[224:227], a[156:159], v[136:139]\n"
"buffer_load_dwordx4 v240, s[28:31], s40 offen offset:1024 lds\n"
"v_mfma_f32_16x16x32_bf16 v[152:155], v[224:227], a[172:175], 0\n"
"ds_read_b64_tr_b16 v[192:193], v232 offset:40960\n"
"v_mfma_f32_16x16x32_bf16 v[152:155], v[120:123], a[168:171], v[152:155]\n"
"ds_read_b64_tr_b16 v[194:195], v232 offset:43008\n"
"v_mfma_f32_16x16x32_bf16 v[152:155], v[116:119], a[164:167], v[152:155]\n"
"ds_read_b64_tr_b16 v[208:209], v228 offset:49152\n"
"v_mfma_f32_16x16x32_bf16 v[152:155], v[112:115], a[160:163], v[152:155]\n"
"ds_read_b64_tr_b16 v[210:211], v228 offset:51200\n"
"v_mfma_f32_16x16x32_bf16 v[152:155], v[108:111], a[124:127], v[152:155]\n"
"ds_read_b64_tr_b16 v[212:213], v232 offset:49152\n"
"v_mfma_f32_16x16x32_bf16 v[152:155], v[104:107], a[120:123], v[152:155]\n"
"ds_read_b64_tr_b16 v[214:215], v232 offset:51200\n"
"v_mfma_f32_16x16x32_bf16 v[152:155], v[100:103], a[116:119], v[152:155]\n"
"s_add_u32 s40, s41, s53\n"
"ds_read_b64_tr_b16 v[216:217], v228 offset:57344\n"
"v_mfma_f32_16x16x32_bf16 v[152:155], v[96:99], a[112:115], v[152:155]\n"
"buffer_load_dwordx4 v240, s[28:31], s40 offen offset:2048 lds\n"
"v_mfma_f32_16x16x32_bf16 v[168:171], v[96:99], a[128:131], 0\n"
"ds_read_b64_tr_b16 v[218:219], v228 offset:59392\n"
"v_xor_b32 v228, s75, v228\n"
"v_mfma_f32_16x16x32_bf16 v[168:171], v[100:103], a[132:135], v[168:171]\n"
"ds_read_b64_tr_b16 v[220:221], v232 offset:57344\n"
"v_mfma_f32_16x16x32_bf16 v[168:171], v[104:107], a[136:139], v[168:171]\n"
"ds_read_b64_tr_b16 v[222:223], v232 offset:59392\n"
"v_xor_b32 v232, s75, v232\n"
"v_mfma_f32_16x16x32_bf16 v[168:171], v[108:111], a[140:143], v[168:171]\n"
"buffer_load_dwordx4 v[88:91], v243, s[32:35], s19 offen sc1\n"
"v_mfma_f32_16x16x32_bf16 v[168:171], v[112:115], a[176:179], v[168:171]\n"
"ds_read_b64_tr_b16 v[96:97], v229 offset:32768\n"
"v_mfma_f32_16x16x32_bf16 v[168:171], v[116:119], a[180:183], v[168:171]\n"
"ds_read_b64_tr_b16 v[98:99], v229 offset:34816\n"
"v_mfma_f32_16x16x32_bf16 v[168:171], v[120:123], a[184:187], v[168:171]\n"
"s_add_u32 s40, s41, s54\n"
"ds_read_b64_tr_b16 v[100:101], v233 offset:32768\n"
"v_mfma_f32_16x16x32_bf16 v[168:171], v[224:227], a[188:191], v[168:171]\n"
"s_waitcnt lgkmcnt(6)\n"
"buffer_load_dwordx4 v240, s[28:31], s40 offen offset:3072 lds\n"
"v_mfma_f32_16x16x32_bf16 v[124:127], v[180:183], a[48:51], v[124:127]\n"
"ds_read_b64_tr_b16 v[102:103], v233 offset:34816\n"
"v_mfma_f32_16x16x32_bf16 v[124:127], v[184:187], a[52:55], v[124:127]\n"
"s_waitcnt vmcnt(12)\n"
"ds_read_b64_tr_b16 v[104:105], v229 offset:40960\n"
"v_lshlrev_b32 v246, 16, v72\n"
"v_mfma_f32_16x16x32_bf16 v[124:127], v[188:191], a[56:59], v[124:127]\n"
"ds_read_b64_tr_b16 v[106:107], v229 offset:43008\n"
"v_and_b32 v72, s73, v72\n"
"v_mfma_f32_16x16x32_bf16 v[124:127], v[192:195], a[60:63], v[124:127]\n"
"ds_read_b64_tr_b16 v[108:109], v233 offset:40960\n"
"v_add_f32 v0, v0, v246\n"
"v_mfma_f32_16x16x32_bf16 v[124:127], v[208:211], a[0:3], v[124:127]\n"
"ds_read_b64_tr_b16 v[110:111], v233 offset:43008\n"
"v_add_f32 v1, v1, v72\n"
"v_mfma_f32_16x16x32_bf16 v[124:127], v[212:215], a[4:7], v[124:127]\n"
"s_waitcnt lgkmcnt(6)\n"
"s_add_u32 s41, s70, 327680\n"
"ds_read_b64_tr_b16 v[112:113], v229 offset:49152\n"
"v_lshlrev_b32 v247, 16, v73\n"
"v_mfma_f32_16x16x32_bf16 v[124:127], v[216:219], a[8:11], v[124:127]\n"
"s_add_u32 m0, s71, 32768\n"
"ds_read_b64_tr_b16 v[114:115], v229 offset:51200\n"
"v_and_b32 v73, s73, v73\n"
"v_mfma_f32_16x16x32_bf16 v[124:127], v[220:223], a[12:15], v[124:127]\n"
"buffer_load_dwordx4 v240, s[28:31], s41 offen lds\n"
"v_mfma_f32_16x16x32_bf16 v[140:143], v[220:223], a[28:31], v[140:143]\n"
"ds_read_b64_tr_b16 v[116:117], v233 offset:49152\n"
"v_add_f32 v2, v2, v247\n"
"v_mfma_f32_16x16x32_bf16 v[140:143], v[216:219], a[24:27], v[140:143]\n"
"ds_read_b64_tr_b16 v[118:119], v233 offset:51200\n"
"v_add_f32 v3, v3, v73\n"
"v_mfma_f32_16x16x32_bf16 v[140:143], v[212:215], a[20:23], v[140:143]\n"
"ds_read_b64_tr_b16 v[120:121], v229 offset:57344\n"
"v_lshlrev_b32 v248, 16, v74\n"
"v_mfma_f32_16x16x32_bf16 v[140:143], v[208:211], a[16:19], v[140:143]\n"
"buffer_load_dwordx4 v[92:95], v243, s[32:35], s19 offen offset:1024 sc1\n"
"v_mfma_f32_16x16x32_bf16 v[140:143], v[192:195], a[76:79], v[140:143]\n"
"ds_read_b64_tr_b16 v[122:123], v229 offset:59392\n"
"v_and_b32 v74, s73, v74\n"
"v_mfma_f32_16x16x32_bf16 v[140:143], v[188:191], a[72:75], v[140:143]\n"
"ds_read_b64_tr_b16 v[224:225], v233 offset:57344\n"
"v_cvt_pk_bf16_f32 v124, v124, v125\n"
"v_mfma_f32_16x16x32_bf16 v[140:143], v[184:187], a[68:71], v[140:143]\n"
"s_add_u32 s40, s41, s52\n"
"ds_read_b64_tr_b16 v[226:227], v233 offset:59392\n"
"v_cvt_pk_bf16_f32 v125, v126, v127\n"
"v_mfma_f32_16x16x32_bf16 v[140:143], v[180:183], a[64:67], v[140:143]\n"
"buffer_load_dwordx4 v240, s[28:31], s40 offen offset:1024 lds\n"
"v_mfma_f32_16x16x32_bf16 v[156:159], v[180:183], a[80:83], v[156:159]\n"
"v_add_f32 v4, v4, v248\n"
"v_add_f32 v5, v5, v74\n"
"v_mfma_f32_16x16x32_bf16 v[156:159], v[184:187], a[84:87], v[156:159]\n"
"s_waitcnt vmcnt(12)\n"
"v_lshlrev_b32 v249, 16, v75\n"
"v_and_b32 v75, s73, v75\n"
"v_mfma_f32_16x16x32_bf16 v[156:159], v[188:191], a[88:91], v[156:159]\n"
"v_add_f32 v6, v6, v249\n"
"v_add_f32 v7, v7, v75\n"
"v_mfma_f32_16x16x32_bf16 v[156:159], v[192:195], a[92:95], v[156:159]\n"
"v_lshlrev_b32 v250, 16, v76\n"
"v_and_b32 v76, s73, v76\n"
"v_mfma_f32_16x16x32_bf16 v[156:159], v[208:211], a[32:35], v[156:159]\n"
"v_add_f32 v8, v8, v250\n"
"v_add_f32 v9, v9, v76\n"
"v_mfma_f32_16x16x32_bf16 v[156:159], v[212:215], a[36:39], v[156:159]\n"
"v_cvt_pk_bf16_f32 v140, v140, v141\n"
"v_cvt_pk_bf16_f32 v141, v142, v143\n"
"v_mfma_f32_16x16x32_bf16 v[156:159], v[216:219], a[40:43], v[156:159]\n"
"s_waitcnt vmcnt(10)\n"
"v_lshlrev_b32 v251, 16, v77\n"
"v_and_b32 v77, s73, v77\n"
"v_mfma_f32_16x16x32_bf16 v[156:159], v[220:223], a[44:47], v[156:159]\n"
"s_barrier\n"
"v_mfma_f32_16x16x32_bf16 v[172:175], v[220:223], a[228:231], v[172:175]\n"
"ds_read_b64_tr_b16 v[220:221], v234 offset:57344\n"
"s_cmp_eq_u32 s42, 0\n"
"v_mfma_f32_16x16x32_bf16 v[172:175], v[216:219], a[224:227], v[172:175]\n"
"s_cbranch_scc1 LGS_%=\nLGSK_%=:\n"
"ds_read_b64_tr_b16 v[216:217], v230 offset:57344\n"
"v_mfma_f32_16x16x32_bf16 v[172:175], v[212:215], a[212:215], v[172:175]\n"
"ds_read_b64_tr_b16 v[212:213], v234 offset:49152\n"
"v_add_f32 v10, v10, v251\n"
"v_mfma_f32_16x16x32_bf16 v[172:175], v[208:211], a[208:211], v[172:175]\n"
"s_mul_i32 s16, s61, 286720\n"
"buffer_load_dwordx4 v[196:199], v243, s[32:35], s19 offen offset:2048 sc1\n"
"v_mfma_f32_16x16x32_bf16 v[172:175], v[192:195], a[236:239], v[172:175]\n"
"s_add_u32 s16, s16, s62\n"
"s_mul_i32 s18, s72, 10\n"
"ds_read_b64_tr_b16 v[192:193], v234 offset:40960\n"
"v_mfma_f32_16x16x32_bf16 v[172:175], v[188:191], a[232:235], v[172:175]\n"
"s_add_u32 s17, s16, 4096\n"
"s_add_u32 s18, s18, s61\n"
"ds_read_b64_tr_b16 v[188:189], v230 offset:40960\n"
"v_mfma_f32_16x16x32_bf16 v[172:175], v[184:187], a[220:223], v[172:175]\n"
"s_add_u32 s40, s41, s53\n"
"s_mul_i32 s62, s70, 224\n"
"s_mul_i32 s18, s18, 28672\n"
"v_mfma_f32_16x16x32_bf16 v[172:175], v[180:183], a[216:219], v[172:175]\n"
"s_add_u32 s19, s18, 4096\n"
"buffer_load_dwordx4 v240, s[28:31], s40 offen offset:2048 lds\n"
"v_mfma_f32_16x16x32_bf16 v[128:131], v[96:99], a[48:51], v[128:131]\n"
"s_waitcnt lgkmcnt(4)\n"
"s_mov_b32 s69, 15\n"
"ds_read_b64_tr_b16 v[180:181], v230 offset:32768\n"
"v_cvt_pk_bf16_f32 v156, v156, v157\n"
"v_mfma_f32_16x16x32_bf16 v[128:131], v[100:103], a[52:55], v[128:131]\n"
"s_waitcnt vmcnt(11)\n"
"s_add_u32 s21, s21, 1\n"
"ds_read_b64_tr_b16 v[182:183], v230 offset:34816\n"
"v_mfma_f32_16x16x32_bf16 v[128:131], v[104:107], a[56:59], v[128:131]\n"
"ds_read_b64_tr_b16 v[184:185], v234 offset:32768\n"
"v_cvt_pk_bf16_f32 v157, v158, v159\n"
"v_mfma_f32_16x16x32_bf16 v[128:131], v[108:111], a[60:63], v[128:131]\n"
"ds_read_b64_tr_b16 v[186:187], v234 offset:34816\n"
"v_add_f32 v11, v11, v77\n"
"v_mfma_f32_16x16x32_bf16 v[128:131], v[112:115], a[0:3], v[128:131]\n"
"ds_read_b64_tr_b16 v[190:191], v230 offset:43008\n"
"v_lshlrev_b32 v246, 16, v78\n"
"v_mfma_f32_16x16x32_bf16 v[128:131], v[116:119], a[4:7], v[128:131]\n"
"ds_read_b64_tr_b16 v[194:195], v234 offset:43008\n"
"v_and_b32 v78, s73, v78\n"
"v_mfma_f32_16x16x32_bf16 v[128:131], v[120:123], a[8:11], v[128:131]\n"
"s_add_u32 s40, s41, s54\n"
"ds_read_b64_tr_b16 v[208:209], v230 offset:49152\n"
"v_add_f32 v12, v12, v246\n"
"v_mfma_f32_16x16x32_bf16 v[128:131], v[224:227], a[12:15], v[128:131]\n"
"buffer_load_dwordx4 v240, s[28:31], s40 offen offset:3072 lds\n"
"v_mfma_f32_16x16x32_bf16 v[144:147], v[224:227], a[28:31], v[144:147]\n"
"ds_read_b64_tr_b16 v[210:211], v230 offset:51200\n"
"v_add_f32 v13, v13, v78\n"
"v_mfma_f32_16x16x32_bf16 v[144:147], v[120:123], a[24:27], v[144:147]\n"
"ds_read_b64_tr_b16 v[214:215], v234 offset:51200\n"
"v_lshlrev_b32 v247, 16, v79\n"
"v_mfma_f32_16x16x32_bf16 v[144:147], v[116:119], a[20:23], v[144:147]\n"
"ds_read_b64_tr_b16 v[218:219], v230 offset:59392\n"
"v_and_b32 v79, s73, v79\n"
"v_mfma_f32_16x16x32_bf16 v[144:147], v[112:115], a[16:19], v[144:147]\n"
"ds_read_b64_tr_b16 v[222:223], v234 offset:59392\n"
"v_add_f32 v14, v14, v247\n"
"v_mfma_f32_16x16x32_bf16 v[144:147], v[108:111], a[76:79], v[144:147]\n"
"v_add_f32 v15, v15, v79\n"
"v_lshlrev_b32 v248, 16, v80\n"
"v_mfma_f32_16x16x32_bf16 v[144:147], v[104:107], a[72:75], v[144:147]\n"
"s_add_u32 s41, s70, 491520\n"
"v_cvt_pk_bf16_f32 v126, v128, v129\n"
"v_cvt_pk_bf16_f32 v127, v130, v131\n"
"v_mfma_f32_16x16x32_bf16 v[144:147], v[100:103], a[68:71], v[144:147]\n"
"s_add_u32 m0, s71, 49152\n"
"v_and_b32 v80, s73, v80\n"
"v_add_f32 v16, v16, v248\n"
"v_mfma_f32_16x16x32_bf16 v[144:147], v[96:99], a[64:67], v[144:147]\n"
"buffer_load_dwordx4 v240, s[28:31], s41 offen lds\n"
"v_mfma_f32_16x16x32_bf16 v[160:163], v[96:99], a[80:83], v[160:163]\n"
"s_add_u32 s20, s47, 3\n"
"v_add_f32 v17, v17, v80\n"
"v_lshlrev_b32 v249, 16, v81\n"
"v_mfma_f32_16x16x32_bf16 v[160:163], v[100:103], a[84:87], v[160:163]\n"
"s_waitcnt vmcnt(11)\n"
"s_add_u32 s70, s61, s20\n"
"v_and_b32 v81, s73, v81\n"
"v_mfma_f32_16x16x32_bf16 v[160:163], v[104:107], a[88:91], v[160:163]\n"
"s_and_b32 s70, s70, 7\n"
"v_add_f32 v18, v18, v249\n"
"v_add_f32 v19, v19, v81\n"
"v_mfma_f32_16x16x32_bf16 v[160:163], v[108:111], a[92:95], v[160:163]\n"
"s_cmp_ge_u32 s47, 5\ns_cselect_b32 s70, s20, s70\n"
"v_lshlrev_b32 v250, 16, v82\n"
"v_and_b32 v82, s73, v82\n"
"v_mfma_f32_16x16x32_bf16 v[160:163], v[112:115], a[32:35], v[160:163]\n"
"s_cmp_eq_u32 s47, 7\ns_cselect_b32 s70, s61, s70\n"
"v_add_f32 v20, v20, v250\n"
"v_add_f32 v21, v21, v82\n"
"v_mfma_f32_16x16x32_bf16 v[160:163], v[116:119], a[36:39], v[160:163]\n"
"s_lshl_b32 s70, s70, 7\n"
"v_cvt_pk_bf16_f32 v142, v144, v145\n"
"v_cvt_pk_bf16_f32 v143, v146, v147\n"
"v_mfma_f32_16x16x32_bf16 v[160:163], v[120:123], a[40:43], v[160:163]\n"
"s_xor_b32 s71, s71, s75\n"
"v_lshlrev_b32 v251, 16, v83\n"
"v_and_b32 v83, s73, v83\n"
"v_mfma_f32_16x16x32_bf16 v[160:163], v[224:227], a[44:47], v[160:163]\n"
"buffer_store_dwordx4 v[124:127], v243, s[32:35], s16 offen sc0 sc1\n"
"v_add_f32 v22, v22, v251\n"
"v_mfma_f32_16x16x32_bf16 v[176:179], v[224:227], a[228:231], v[176:179]\n"
"ds_read_b64_tr_b16 v[224:225], v235 offset:57344\n"
"v_add_f32 v23, v23, v83\n"
"v_mfma_f32_16x16x32_bf16 v[176:179], v[120:123], a[224:227], v[176:179]\n"
"ds_read_b64_tr_b16 v[120:121], v231 offset:57344\n"
"v_lshlrev_b32 v246, 16, v84\n"
"v_mfma_f32_16x16x32_bf16 v[176:179], v[116:119], a[212:215], v[176:179]\n"
"ds_read_b64_tr_b16 v[116:117], v235 offset:49152\n"
"v_and_b32 v84, s73, v84\n"
"v_mfma_f32_16x16x32_bf16 v[176:179], v[112:115], a[208:211], v[176:179]\n"
"ds_read_b64_tr_b16 v[112:113], v231 offset:49152\n"
"v_add_f32 v24, v24, v246\n"
"v_mfma_f32_16x16x32_bf16 v[176:179], v[108:111], a[236:239], v[176:179]\n"
"ds_read_b64_tr_b16 v[108:109], v235 offset:40960\n"
"v_add_f32 v25, v25, v84\n"
"v_mfma_f32_16x16x32_bf16 v[176:179], v[104:107], a[232:235], v[176:179]\n"
"ds_read_b64_tr_b16 v[104:105], v231 offset:40960\n"
"v_cvt_pk_bf16_f32 v158, v160, v161\n"
"v_mfma_f32_16x16x32_bf16 v[176:179], v[100:103], a[220:223], v[176:179]\n"
"s_add_u32 s40, s41, s52\n"
"ds_read_b64_tr_b16 v[100:101], v235 offset:32768\n"
"v_cvt_pk_bf16_f32 v159, v162, v163\n"
"v_mfma_f32_16x16x32_bf16 v[176:179], v[96:99], a[216:219], v[176:179]\n"
"s_waitcnt lgkmcnt(7)\n"
"buffer_load_dwordx4 v240, s[28:31], s40 offen offset:1024 lds\n"
"v_mfma_f32_16x16x32_bf16 v[132:135], v[180:183], a[48:51], v[132:135]\n"
"ds_read_b64_tr_b16 v[96:97], v231 offset:32768\n"
"v_lshlrev_b32 v247, 16, v85\n"
"v_mfma_f32_16x16x32_bf16 v[132:135], v[184:187], a[52:55], v[132:135]\n"
"s_waitcnt vmcnt(10)\n"
"ds_read_b64_tr_b16 v[98:99], v231 offset:34816\n"
"v_and_b32 v85, s73, v85\n"
"v_mfma_f32_16x16x32_bf16 v[132:135], v[188:191], a[56:59], v[132:135]\n"
"ds_read_b64_tr_b16 v[102:103], v235 offset:34816\n"
"v_add_f32 v26, v26, v247\n"
"v_mfma_f32_16x16x32_bf16 v[132:135], v[192:195], a[60:63], v[132:135]\n"
"ds_read_b64_tr_b16 v[106:107], v231 offset:43008\n"
"v_add_f32 v27, v27, v85\n"
"v_mfma_f32_16x16x32_bf16 v[132:135], v[208:211], a[0:3], v[132:135]\n"
"ds_read_b64_tr_b16 v[110:111], v235 offset:43008\n"
"v_lshlrev_b32 v248, 16, v86\n"
"v_mfma_f32_16x16x32_bf16 v[132:135], v[212:215], a[4:7], v[132:135]\n"
"ds_read_b64_tr_b16 v[114:115], v231 offset:51200\n"
"v_and_b32 v86, s73, v86\n"
"v_mfma_f32_16x16x32_bf16 v[132:135], v[216:219], a[8:11], v[132:135]\n"
"ds_read_b64_tr_b16 v[118:119], v235 offset:51200\n"
"v_add_f32 v28, v28, v248\n"
"v_mfma_f32_16x16x32_bf16 v[132:135], v[220:223], a[12:15], v[132:135]\n"
"buffer_store_dwordx4 v[140:143], v243, s[32:35], s16 offen offset:2048 sc0 sc1\n"
"v_add_f32 v29, v29, v86\n"
"v_mfma_f32_16x16x32_bf16 v[148:151], v[220:223], a[28:31], v[148:151]\n"
"ds_read_b64_tr_b16 v[122:123], v231 offset:59392\n"
"v_lshlrev_b32 v249, 16, v87\n"
"v_mfma_f32_16x16x32_bf16 v[148:151], v[216:219], a[24:27], v[148:151]\n"
"ds_read_b64_tr_b16 v[226:227], v235 offset:59392\n"
"v_and_b32 v87, s73, v87\n"
"v_mfma_f32_16x16x32_bf16 v[148:151], v[212:215], a[20:23], v[148:151]\n"
"v_add_f32 v30, v30, v249\n"
"v_add_f32 v31, v31, v87\n"
"v_mfma_f32_16x16x32_bf16 v[148:151], v[208:211], a[16:19], v[148:151]\n"
"v_lshlrev_b32 v250, 16, v88\n"
"v_and_b32 v88, s73, v88\n"
"v_mfma_f32_16x16x32_bf16 v[148:151], v[192:195], a[76:79], v[148:151]\n"
"v_add_f32 v32, v32, v250\n"
"v_add_f32 v33, v33, v88\n"
"v_mfma_f32_16x16x32_bf16 v[148:151], v[188:191], a[72:75], v[148:151]\n"
"v_lshlrev_b32 v251, 16, v89\n"
"v_and_b32 v89, s73, v89\n"
"v_mfma_f32_16x16x32_bf16 v[148:151], v[184:187], a[68:71], v[148:151]\n"
"s_add_u32 s40, s41, s53\n"
"v_add_f32 v34, v34, v251\n"
"v_add_f32 v35, v35, v89\n"
"v_mfma_f32_16x16x32_bf16 v[148:151], v[180:183], a[64:67], v[148:151]\n"
"buffer_load_dwordx4 v240, s[28:31], s40 offen offset:2048 lds\n"
"v_mfma_f32_16x16x32_bf16 v[164:167], v[180:183], a[80:83], v[164:167]\n"
"v_lshlrev_b32 v246, 16, v90\n"
"v_and_b32 v90, s73, v90\n"
"v_mfma_f32_16x16x32_bf16 v[164:167], v[184:187], a[84:87], v[164:167]\n"
"s_waitcnt vmcnt(9)\n"
"v_add_f32 v36, v36, v246\n"
"v_add_f32 v37, v37, v90\n"
"v_mfma_f32_16x16x32_bf16 v[164:167], v[188:191], a[88:91], v[164:167]\n"
"v_lshlrev_b32 v247, 16, v91\n"
"v_and_b32 v91, s73, v91\n"
"v_mfma_f32_16x16x32_bf16 v[164:167], v[192:195], a[92:95], v[164:167]\n"
"v_add_f32 v38, v38, v247\n"
"v_add_f32 v39, v39, v91\n"
"v_mfma_f32_16x16x32_bf16 v[164:167], v[208:211], a[32:35], v[164:167]\n"
"v_lshlrev_b32 v248, 16, v92\n"
"v_and_b32 v92, s73, v92\n"
"v_mfma_f32_16x16x32_bf16 v[164:167], v[212:215], a[36:39], v[164:167]\n"
"v_add_f32 v40, v40, v248\n"
"v_add_f32 v41, v41, v92\n"
"v_mfma_f32_16x16x32_bf16 v[164:167], v[216:219], a[40:43], v[164:167]\n"
"v_lshlrev_b32 v249, 16, v93\n"
"v_and_b32 v93, s73, v93\n"
"v_mfma_f32_16x16x32_bf16 v[164:167], v[220:223], a[44:47], v[164:167]\n"
"s_barrier\n"
"s_waitcnt lgkmcnt(0)\n"
"v_mfma_f32_16x16x32_bf16 v[136:139], v[96:99], a[48:51], v[136:139]\n"
"ds_read_b64_tr_b16 v[180:181], v228 offset:0\n"
"v_add_f32 v42, v42, v249\n"
"v_mfma_f32_16x16x32_bf16 v[136:139], v[100:103], a[52:55], v[136:139]\n"
"ds_read_b64_tr_b16 v[182:183], v228 offset:2048\n"
"v_add_f32 v43, v43, v93\n"
"v_mfma_f32_16x16x32_bf16 v[136:139], v[104:107], a[56:59], v[136:139]\n"
"v_lshlrev_b32_e64 v245, 2, s72\n"
"ds_read_b64_tr_b16 v[184:185], v232 offset:0\n"
"v_lshlrev_b32 v250, 16, v94\n"
"v_mfma_f32_16x16x32_bf16 v[136:139], v[108:111], a[60:63], v[136:139]\n"
"global_load_dword v244, v245, s[6:7] sc1\n"
"v_mfma_f32_16x16x32_bf16 v[136:139], v[112:115], a[0:3], v[136:139]\n"
"ds_read_b64_tr_b16 v[186:187], v232 offset:2048\n"
"v_and_b32 v94, s73, v94\n"
"v_mfma_f32_16x16x32_bf16 v[136:139], v[116:119], a[4:7], v[136:139]\n"
"ds_read_b64_tr_b16 v[188:189], v228 offset:8192\n"
"v_add_f32 v44, v44, v250\n"
"v_mfma_f32_16x16x32_bf16 v[136:139], v[120:123], a[8:11], v[136:139]\n"
"ds_read_b64_tr_b16 v[190:191], v228 offset:10240\n"
"v_add_f32 v45, v45, v94\n"
"v_mfma_f32_16x16x32_bf16 v[136:139], v[224:227], a[12:15], v[136:139]\n"
"buffer_store_dwordx4 v[156:159], v243, s[32:35], s17 offen sc0 sc1\n"
"v_lshlrev_b32 v251, 16, v95\n"
"v_mfma_f32_16x16x32_bf16 v[152:155], v[224:227], a[28:31], v[152:155]\n"
"ds_read_b64_tr_b16 v[192:193], v232 offset:8192\n"
"v_and_b32 v95, s73, v95\n"
"v_mfma_f32_16x16x32_bf16 v[152:155], v[120:123], a[24:27], v[152:155]\n"
"s_waitcnt vmcnt(9)\n"
"ds_read_b64_tr_b16 v[194:195], v232 offset:10240\n"
"v_add_f32 v46, v46, v251\n"
"v_mfma_f32_16x16x32_bf16 v[152:155], v[116:119], a[20:23], v[152:155]\n"
"ds_read_b64_tr_b16 v[208:209], v228 offset:16384\n"
"v_add_f32 v47, v47, v95\n"
"v_mfma_f32_16x16x32_bf16 v[152:155], v[112:115], a[16:19], v[152:155]\n"
"ds_read_b64_tr_b16 v[210:211], v228 offset:18432\n"
"v_lshlrev_b32 v250, 16, v196\n"
"v_mfma_f32_16x16x32_bf16 v[152:155], v[108:111], a[76:79], v[152:155]\n"
"ds_read_b64_tr_b16 v[212:213], v232 offset:16384\n"
"v_and_b32 v196, s73, v196\n"
"v_mfma_f32_16x16x32_bf16 v[152:155], v[104:107], a[72:75], v[152:155]\n"
"ds_read_b64_tr_b16 v[214:215], v232 offset:18432\n"
"v_add_f32 v48, v48, v250\n"
"v_mfma_f32_16x16x32_bf16 v[152:155], v[100:103], a[68:71], v[152:155]\n"
"s_add_u32 s40, s41, s54\n"
"ds_read_b64_tr_b16 v[216:217], v228 offset:24576\n"
"v_add_f32 v49, v49, v196\n"
"v_mfma_f32_16x16x32_bf16 v[152:155], v[96:99], a[64:67], v[152:155]\n"
"buffer_load_dwordx4 v240, s[28:31], s40 offen offset:3072 lds\n"
"v_mfma_f32_16x16x32_bf16 v[168:171], v[96:99], a[80:83], v[168:171]\n"
"ds_read_b64_tr_b16 v[218:219], v228 offset:26624\n"
"v_lshlrev_b32 v251, 16, v197\n"
"v_mfma_f32_16x16x32_bf16 v[168:171], v[100:103], a[84:87], v[168:171]\n"
"ds_read_b64_tr_b16 v[220:221], v232 offset:24576\n"
"v_and_b32 v197, s73, v197\n"
"v_mfma_f32_16x16x32_bf16 v[168:171], v[104:107], a[88:91], v[168:171]\n"
"ds_read_b64_tr_b16 v[222:223], v232 offset:26624\n"
"v_add_f32 v50, v50, v251\n"
"v_mfma_f32_16x16x32_bf16 v[168:171], v[108:111], a[92:95], v[168:171]\n"
"v_add_f32 v51, v51, v197\n"
"v_lshlrev_b32 v246, 16, v198\n"
"v_mfma_f32_16x16x32_bf16 v[168:171], v[112:115], a[32:35], v[168:171]\n"
"v_and_b32 v198, s73, v198\n"
"v_add_f32 v52, v52, v246\n"
"v_mfma_f32_16x16x32_bf16 v[168:171], v[116:119], a[36:39], v[168:171]\n"
"v_add_f32 v53, v53, v198\n"
"v_lshlrev_b32 v247, 16, v199\n"
"v_mfma_f32_16x16x32_bf16 v[168:171], v[120:123], a[40:43], v[168:171]\n"
"s_add_u32 s47, s47, 1\n"
"v_and_b32 v199, s73, v199\n"
"v_add_f32 v54, v54, v247\n"
"v_mfma_f32_16x16x32_bf16 v[168:171], v[224:227], a[44:47], v[168:171]\n"
"s_waitcnt lgkmcnt(3)\n"
"s_cmp_lg_u32 s47, 9\n"
"v_add_f32 v55, v55, v199\n"
"s_cbranch_scc1 LGL_%=\n"
"v_mfma_f32_16x16x32_bf16 v[0:3], v[180:183], a[96:99], v[0:3]\n"
"v_xor_b32 v229, s75, v229\n"
"v_xor_b32 v233, s75, v233\n"
"v_mfma_f32_16x16x32_bf16 v[0:3], v[184:187], a[100:103], v[0:3]\n"
"ds_read_b64_tr_b16 v[224:225], v233 offset:24576\n"
"v_cvt_pk_bf16_f32 v172, v172, v173\n"
"v_mfma_f32_16x16x32_bf16 v[0:3], v[188:191], a[104:107], v[0:3]\n"
"v_bfe_u32 v245, v242, 2, 5\n"
"ds_read_b64_tr_b16 v[108:109], v233 offset:8192\n"
"v_cvt_pk_bf16_f32 v132, v132, v133\n"
"v_mfma_f32_16x16x32_bf16 v[0:3], v[192:195], a[108:111], v[0:3]\n"
"global_load_dword v244, v245, s[6:7] sc1\n"
"v_mfma_f32_16x16x32_bf16 v[0:3], v[208:211], a[144:147], v[0:3]\n"
"v_cvt_pk_bf16_f32 v173, v174, v175\n"
"v_cvt_pk_bf16_f32 v133, v134, v135\n"
"v_mfma_f32_16x16x32_bf16 v[0:3], v[212:215], a[148:151], v[0:3]\n"
"s_waitcnt lgkmcnt(2)\n"
"v_cvt_pk_bf16_f32 v174, v176, v177\n"
"v_cvt_pk_bf16_f32 v135, v138, v139\n"
"v_mfma_f32_16x16x32_bf16 v[0:3], v[216:219], a[152:155], v[0:3]\n"
"ds_read_b64_tr_b16 v[122:123], v229 offset:26624\n"
"v_cvt_pk_bf16_f32 v175, v178, v179\n"
"v_mfma_f32_16x16x32_bf16 v[0:3], v[220:223], a[156:159], v[0:3]\n"
"buffer_store_dwordx4 v[172:175], v243, s[32:35], s17 offen offset:2048 sc0 sc1\n"
"v_cvt_pk_bf16_f32 v134, v136, v137\n"
"v_mfma_f32_16x16x32_bf16 v[16:19], v[220:223], a[172:175], v[16:19]\n"
"buffer_store_dwordx4 v[132:135], v243, s[32:35], s16 offen offset:1024 sc0 sc1\n"
"ds_read_b64_tr_b16 v[116:117], v233 offset:16384\n"
"v_mfma_f32_16x16x32_bf16 v[16:19], v[216:219], a[168:171], v[16:19]\n"
"ds_read_b64_tr_b16 v[100:101], v233 offset:0\n"
"v_cvt_pk_bf16_f32 v148, v148, v149\n"
"v_mfma_f32_16x16x32_bf16 v[16:19], v[212:215], a[164:167], v[16:19]\n"
"v_cvt_pk_bf16_f32 v149, v150, v151\n"
"v_cvt_pk_bf16_f32 v151, v154, v155\n"
"v_mfma_f32_16x16x32_bf16 v[16:19], v[208:211], a[160:163], v[16:19]\n"
"ds_read_b64_tr_b16 v[96:97], v229 offset:0\n"
"v_cvt_pk_bf16_f32 v164, v164, v165\n"
"v_mfma_f32_16x16x32_bf16 v[16:19], v[192:195], a[124:127], v[16:19]\n"
"v_cvt_pk_bf16_f32 v165, v166, v167\n"
"v_cvt_pk_bf16_f32 v150, v152, v153\n"
"v_mfma_f32_16x16x32_bf16 v[16:19], v[188:191], a[120:123], v[16:19]\n"
"ds_read_b64_tr_b16 v[226:227], v233 offset:26624\n"
"v_cvt_pk_bf16_f32 v167, v170, v171\n"
"v_mfma_f32_16x16x32_bf16 v[16:19], v[184:187], a[116:119], v[16:19]\n"
"ds_read_b64_tr_b16 v[118:119], v233 offset:18432\n"
"v_bfe_u32 v160, v242, 6, 2\n"
"v_mfma_f32_16x16x32_bf16 v[16:19], v[180:183], a[112:115], v[16:19]\n"
"buffer_store_dwordx4 v[148:151], v243, s[32:35], s16 offen offset:3072 sc0 sc1\n"
"v_cvt_pk_bf16_f32 v166, v168, v169\n"
"v_mfma_f32_16x16x32_bf16 v[32:35], v[180:183], a[128:131], v[32:35]\n"
"buffer_store_dwordx4 v[164:167], v243, s[32:35], s17 offen offset:1024 sc0 sc1\n"
"ds_read_b64_tr_b16 v[106:107], v229 offset:10240\n"
"v_mfma_f32_16x16x32_bf16 v[32:35], v[184:187], a[132:135], v[32:35]\n"
"ds_read_b64_tr_b16 v[102:103], v233 offset:2048\n"
"v_xor_b32 v234, s75, v234\n"
"v_mfma_f32_16x16x32_bf16 v[32:35], v[188:191], a[136:139], v[32:35]\n"
"ds_read_b64_tr_b16 v[104:105], v229 offset:8192\n"
"v_mfma_f32_16x16x32_bf16 v[32:35], v[192:195], a[140:143], v[32:35]\n"
"v_xor_b32 v230, s75, v230\n"
"v_bfe_u32 v161, v242, 8, 2\n"
"v_mfma_f32_16x16x32_bf16 v[32:35], v[208:211], a[176:179], v[32:35]\n"
"ds_read_b64_tr_b16 v[98:99], v229 offset:2048\n"
"v_mfma_f32_16x16x32_bf16 v[32:35], v[212:215], a[180:183], v[32:35]\n"
"v_mfma_f32_16x16x32_bf16 v[32:35], v[216:219], a[184:187], v[32:35]\n"
"ds_read_b64_tr_b16 v[114:115], v229 offset:18432\n"
"v_xor_b32 v160, v160, v161\n"
"v_mfma_f32_16x16x32_bf16 v[32:35], v[220:223], a[188:191], v[32:35]\n"
"ds_read_b64_tr_b16 v[112:113], v229 offset:16384\n"
"v_mfma_f32_16x16x32_bf16 v[48:51], v[220:223], a[252:255], v[48:51]\n"
"ds_read_b64_tr_b16 v[120:121], v229 offset:24576\n"
"v_mfma_f32_16x16x32_bf16 v[48:51], v[216:219], a[248:251], v[48:51]\n"
"ds_read_b64_tr_b16 v[110:111], v233 offset:10240\n"
"v_xor_b32 v231, s75, v231\n"
"v_mfma_f32_16x16x32_bf16 v[48:51], v[212:215], a[244:247], v[48:51]\n"
"ds_read_b64_tr_b16 v[222:223], v234 offset:26624\n"
"v_and_b32 v161, 16, v242\n"
"v_mfma_f32_16x16x32_bf16 v[48:51], v[208:211], a[240:243], v[48:51]\n"
"ds_read_b64_tr_b16 v[216:217], v230 offset:24576\n"
"v_mfma_f32_16x16x32_bf16 v[48:51], v[192:195], a[204:207], v[48:51]\n"
"ds_read_b64_tr_b16 v[220:221], v234 offset:24576\n"
"v_mfma_f32_16x16x32_bf16 v[48:51], v[188:191], a[200:203], v[48:51]\n"
"ds_read_b64_tr_b16 v[218:219], v230 offset:26624\n"
"v_xor_b32 v235, s75, v235\n"
"v_mfma_f32_16x16x32_bf16 v[48:51], v[184:187], a[196:199], v[48:51]\n"
"ds_read_b64_tr_b16 v[192:193], v234 offset:8192\n"
"v_mfma_f32_16x16x32_bf16 v[48:51], v[180:183], a[192:195], v[48:51]\n"
"s_waitcnt lgkmcnt(7)\n"
"ds_read_b64_tr_b16 v[214:215], v234 offset:18432\n"
"v_mfma_f32_16x16x32_bf16 v[4:7], v[96:99], a[96:99], v[4:7]\n"
"ds_read_b64_tr_b16 v[194:195], v234 offset:10240\n"
"v_mfma_f32_16x16x32_bf16 v[4:7], v[100:103], a[100:103], v[4:7]\n"
"ds_read_b64_tr_b16 v[180:181], v230 offset:0\n"
"v_mfma_f32_16x16x32_bf16 v[4:7], v[104:107], a[104:107], v[4:7]\n"
"s_waitcnt lgkmcnt(7)\n"
"ds_read_b64_tr_b16 v[186:187], v234 offset:2048\n"
"v_mfma_f32_16x16x32_bf16 v[4:7], v[108:111], a[108:111], v[4:7]\n"
"ds_read_b64_tr_b16 v[184:185], v234 offset:0\n"
"v_mfma_f32_16x16x32_bf16 v[4:7], v[112:115], a[144:147], v[4:7]\n"
"s_waitcnt vmcnt(4)\n"
"ds_read_b64_tr_b16 v[182:183], v230 offset:2048\n"
"v_mfma_f32_16x16x32_bf16 v[4:7], v[116:119], a[148:151], v[4:7]\n"
"v_subrev_u32 v244, s21, v244\n"
"v_mfma_f32_16x16x32_bf16 v[4:7], v[120:123], a[152:155], v[4:7]\n"
"v_cmp_lt_u32 vcc, s69, v244\n"
"ds_read_b64_tr_b16 v[190:191], v230 offset:10240\n"
"v_mfma_f32_16x16x32_bf16 v[4:7], v[224:227], a[156:159], v[4:7]\n"
"ds_read_b64_tr_b16 v[188:189], v230 offset:8192\n"
"s_cbranch_vccnz LGQ_%=\nLGQK_%=:\n"
"v_mfma_f32_16x16x32_bf16 v[20:23], v[224:227], a[172:175], v[20:23]\n"
"s_mov_b32 s20, 0\n"
"ds_read_b64_tr_b16 v[210:211], v230 offset:18432\n"
"v_mfma_f32_16x16x32_bf16 v[20:23], v[120:123], a[168:171], v[20:23]\n"
"buffer_load_dwordx4 v[124:127], v236, s[32:35], s20 offen sc1\n"
"v_mfma_f32_16x16x32_bf16 v[20:23], v[116:119], a[164:167], v[20:23]\n"
"v_add_u32 v160, -1, v160\n"
"v_mfma_f32_16x16x32_bf16 v[20:23], v[112:115], a[160:163], v[20:23]\n"
"s_mov_b32 s20, 286720\n"
"v_mfma_f32_16x16x32_bf16 v[20:23], v[108:111], a[124:127], v[20:23]\n"
"buffer_load_dwordx4 v[128:131], v236, s[32:35], s20 offen sc1\n"
"v_mfma_f32_16x16x32_bf16 v[20:23], v[104:107], a[120:123], v[20:23]\n"
"v_mfma_f32_16x16x32_bf16 v[20:23], v[100:103], a[116:119], v[20:23]\n"
"s_mov_b32 s20, 573440\n"
"v_ashrrev_i32 v160, 31, v160\n"
"v_and_b32 v160, 0x3f80, v160\n"
"v_mfma_f32_16x16x32_bf16 v[20:23], v[96:99], a[112:115], v[20:23]\n"
"buffer_load_dwordx4 v[132:135], v236, s[32:35], s20 offen sc1\n"
"v_mfma_f32_16x16x32_bf16 v[36:39], v[96:99], a[128:131], v[36:39]\n"
"v_add_u32 v241, 1, v241\n"
"v_mfma_f32_16x16x32_bf16 v[36:39], v[100:103], a[132:135], v[36:39]\n"
"s_mov_b32 s20, 860160\n"
"v_mov_b32 v165, 0\n"
"v_mfma_f32_16x16x32_bf16 v[36:39], v[104:107], a[136:139], v[36:39]\n"
"buffer_load_dwordx4 v[136:139], v236, s[32:35], s20 offen sc1\n"
"v_mfma_f32_16x16x32_bf16 v[36:39], v[108:111], a[140:143], v[36:39]\n"
"v_lshlrev_b32 v160, v161, v160\n"
"v_mfma_f32_16x16x32_bf16 v[36:39], v[112:115], a[176:179], v[36:39]\n"
"s_mov_b32 s20, 1146880\n"
"v_mfma_f32_16x16x32_bf16 v[36:39], v[116:119], a[180:183], v[36:39]\n"
"buffer_load_dwordx4 v[140:143], v236, s[32:35], s20 offen sc1\n"
"v_mfma_f32_16x16x32_bf16 v[36:39], v[120:123], a[184:187], v[36:39]\n"
"v_mfma_f32_16x16x32_bf16 v[36:39], v[224:227], a[188:191], v[36:39]\n"
"s_mov_b32 s20, 1433600\n"
"v_mfma_f32_16x16x32_bf16 v[52:55], v[224:227], a[252:255], v[52:55]\n"
"buffer_load_dwordx4 v[144:147], v236, s[32:35], s20 offen sc1\n"
"v_mfma_f32_16x16x32_bf16 v[52:55], v[120:123], a[248:251], v[52:55]\n"
"v_bfe_i32 v161, v242, 5, 1\n"
"v_mfma_f32_16x16x32_bf16 v[52:55], v[116:119], a[244:247], v[52:55]\n"
"s_mov_b32 s20, 1720320\n"
"ds_read_b64_tr_b16 v[208:209], v230 offset:16384\n"
"ds_read_b64_tr_b16 v[212:213], v234 offset:16384\n"
"v_mfma_f32_16x16x32_bf16 v[52:55], v[112:115], a[240:243], v[52:55]\n"
"buffer_load_dwordx4 v[148:151], v236, s[32:35], s20 offen sc1\n"
"v_mfma_f32_16x16x32_bf16 v[52:55], v[108:111], a[204:207], v[52:55]\n"
"s_mov_b32 s20, 2007040\n"
"v_and_b32 v163, v160, v161\n"
"v_mfma_f32_16x16x32_bf16 v[52:55], v[104:107], a[200:203], v[52:55]\n"
"buffer_load_dwordx4 v[152:155], v236, s[32:35], s20 offen sc1\n"
"v_mfma_f32_16x16x32_bf16 v[52:55], v[100:103], a[196:199], v[52:55]\n"
"s_waitcnt vmcnt(8)\n"
"s_add_u32 s21, s21, 1\n"
"v_xor_b32 v162, v160, v163\n"
"v_mfma_f32_16x16x32_bf16 v[52:55], v[96:99], a[192:195], v[52:55]\n"
"s_barrier\n"
"s_mov_b32 s52, 40960\n"
"s_waitcnt lgkmcnt(2)\n"
"v_mfma_f32_16x16x32_bf16 v[8:11], v[180:183], a[96:99], v[8:11]\n"
"s_mov_b32 s53, 81920\n"
"ds_read_b64_tr_b16 v[120:121], v231 offset:24576\n"
"s_cmp_eq_u32 s42, 0\n"
"v_mfma_f32_16x16x32_bf16 v[8:11], v[184:187], a[100:103], v[8:11]\n"
"s_cbranch_scc1 LGT_%=\nLGTK_%=:\n"
"ds_read_b64_tr_b16 v[100:101], v235 offset:0\n"
"v_mfma_f32_16x16x32_bf16 v[8:11], v[188:191], a[104:107], v[8:11]\n"
"v_mfma_f32_16x16x32_bf16 v[8:11], v[192:195], a[108:111], v[8:11]\n"
"s_waitcnt lgkmcnt(2)\n"
"ds_read_b64_tr_b16 v[118:119], v235 offset:18432\n"
"v_mfma_f32_16x16x32_bf16 v[8:11], v[208:211], a[144:147], v[8:11]\n"
"ds_read_b64_tr_b16 v[96:97], v231 offset:0\n"
"v_mfma_f32_16x16x32_bf16 v[8:11], v[212:215], a[148:151], v[8:11]\n"
"ds_read_b64_tr_b16 v[112:113], v231 offset:16384\n"
"v_mfma_f32_16x16x32_bf16 v[8:11], v[216:219], a[152:155], v[8:11]\n"
"v_mfma_f32_16x16x32_bf16 v[8:11], v[220:223], a[156:159], v[8:11]\n"
"ds_read_b64_tr_b16 v[106:107], v231 offset:10240\n"
"v_mfma_f32_16x16x32_bf16 v[24:27], v[220:223], a[172:175], v[24:27]\n"
"v_mfma_f32_16x16x32_bf16 v[24:27], v[216:219], a[168:171], v[24:27]\n"
"ds_read_b64_tr_b16 v[110:111], v235 offset:10240\n"
"v_mfma_f32_16x16x32_bf16 v[24:27], v[212:215], a[164:167], v[24:27]\n"
"v_mfma_f32_16x16x32_bf16 v[24:27], v[208:211], a[160:163], v[24:27]\n"
"ds_read_b64_tr_b16 v[102:103], v235 offset:2048\n"
"v_mfma_f32_16x16x32_bf16 v[24:27], v[192:195], a[124:127], v[24:27]\n"
"ds_read_b64_tr_b16 v[116:117], v235 offset:16384\n"
"v_mfma_f32_16x16x32_bf16 v[24:27], v[188:191], a[120:123], v[24:27]\n"
"ds_read_b64_tr_b16 v[104:105], v231 offset:8192\n"
"v_mfma_f32_16x16x32_bf16 v[24:27], v[184:187], a[116:119], v[24:27]\n"
"v_mov_b32 v160, 0\n"
"v_mov_b32 v164, 0\n"
"v_mfma_f32_16x16x32_bf16 v[24:27], v[180:183], a[112:115], v[24:27]\n"
"ds_read_b64_tr_b16 v[98:99], v231 offset:2048\n"
"v_mfma_f32_16x16x32_bf16 v[40:43], v[180:183], a[128:131], v[40:43]\n"
"ds_read_b64_tr_b16 v[108:109], v235 offset:8192\n"
"v_mfma_f32_16x16x32_bf16 v[40:43], v[184:187], a[132:135], v[40:43]\n"
"ds_read_b64_tr_b16 v[226:227], v235 offset:26624\n"
"v_mfma_f32_16x16x32_bf16 v[40:43], v[188:191], a[136:139], v[40:43]\n"
"v_mfma_f32_16x16x32_bf16 v[40:43], v[192:195], a[140:143], v[40:43]\n"
"ds_read_b64_tr_b16 v[114:115], v231 offset:18432\n"
"v_mfma_f32_16x16x32_bf16 v[40:43], v[208:211], a[176:179], v[40:43]\n"
"ds_read_b64_tr_b16 v[122:123], v231 offset:26624\n"
"v_mfma_f32_16x16x32_bf16 v[40:43], v[212:215], a[180:183], v[40:43]\n"
"v_mfma_f32_16x16x32_bf16 v[40:43], v[216:219], a[184:187], v[40:43]\n"
"ds_read_b64_tr_b16 v[224:225], v235 offset:24576\n"
"v_mfma_f32_16x16x32_bf16 v[40:43], v[220:223], a[188:191], v[40:43]\n"
"s_barrier\n"
"s_waitcnt lgkmcnt(5)\n"
"v_mfma_f32_16x16x32_bf16 v[12:15], v[96:99], a[96:99], v[12:15]\n"
"ds_read_b64_tr_b16 v[180:181], v228 offset:32768\n"
"ds_read_b64_tr_b16 v[214:215], v232 offset:51200\n"
"v_mfma_f32_16x16x32_bf16 v[12:15], v[100:103], a[100:103], v[12:15]\n"
"ds_read_b64_tr_b16 v[186:187], v232 offset:34816\n"
"ds_read_b64_tr_b16 v[220:221], v232 offset:57344\n"
"v_mfma_f32_16x16x32_bf16 v[12:15], v[104:107], a[104:107], v[12:15]\n"
"s_waitcnt lgkmcnt(7)\n"
"ds_read_b64_tr_b16 v[194:195], v232 offset:43008\n"
"ds_read_b64_tr_b16 v[216:217], v228 offset:57344\n"
"v_mfma_f32_16x16x32_bf16 v[12:15], v[108:111], a[108:111], v[12:15]\n"
"s_waitcnt lgkmcnt(8)\n"
"ds_read_b64_tr_b16 v[188:189], v228 offset:40960\n"
"ds_read_b64_tr_b16 v[210:211], v228 offset:51200\n"
"v_mfma_f32_16x16x32_bf16 v[12:15], v[112:115], a[144:147], v[12:15]\n"
"ds_read_b64_tr_b16 v[212:213], v232 offset:49152\n"
"v_mfma_f32_16x16x32_bf16 v[12:15], v[116:119], a[148:151], v[12:15]\n"
"s_waitcnt lgkmcnt(10)\n"
"ds_read_b64_tr_b16 v[190:191], v228 offset:43008\n"
"ds_read_b64_tr_b16 v[208:209], v228 offset:49152\n"
"v_mfma_f32_16x16x32_bf16 v[12:15], v[120:123], a[152:155], v[12:15]\n"
"s_waitcnt lgkmcnt(11)\n"
"ds_read_b64_tr_b16 v[182:183], v228 offset:34816\n"
"ds_read_b64_tr_b16 v[192:193], v232 offset:40960\n"
"v_mfma_f32_16x16x32_bf16 v[12:15], v[224:227], a[156:159], v[12:15]\n"
"global_load_dword v244, v245, s[6:7] sc1\n"
"v_mfma_f32_16x16x32_bf16 v[28:31], v[224:227], a[172:175], v[28:31]\n"
"v_mfma_f32_16x16x32_bf16 v[28:31], v[120:123], a[168:171], v[28:31]\n"
"s_waitcnt vmcnt(8)\n"
"v_and_b32 v57, s73, v124\n"
"v_and_b32 v59, s73, v125\n"
"v_mfma_f32_16x16x32_bf16 v[28:31], v[116:119], a[164:167], v[28:31]\n"
"v_lshlrev_b32 v58, 16, v125\n"
"v_lshlrev_b32 v60, 16, v126\n"
"v_mfma_f32_16x16x32_bf16 v[28:31], v[112:115], a[160:163], v[28:31]\n"
"ds_read_b64_tr_b16 v[184:185], v232 offset:32768\n"
"v_lshlrev_b32 v62, 16, v127\n"
"v_mfma_f32_16x16x32_bf16 v[28:31], v[108:111], a[124:127], v[28:31]\n"
"s_waitcnt vmcnt(7)\n"
"ds_read_b64_tr_b16 v[222:223], v232 offset:59392\n"
"v_lshlrev_b32 v56, 16, v124\n"
"v_mfma_f32_16x16x32_bf16 v[28:31], v[104:107], a[120:123], v[28:31]\n"
"ds_read_b64_tr_b16 v[218:219], v228 offset:59392\n"
"v_lshlrev_b32 v248, 16, v128\n"
"v_mfma_f32_16x16x32_bf16 v[28:31], v[100:103], a[116:119], v[28:31]\n"
"v_add_f32 v56, v56, v248\n"
"v_lshlrev_b32 v251, 16, v131\n"
"v_mfma_f32_16x16x32_bf16 v[28:31], v[96:99], a[112:115], v[28:31]\n"
"s_waitcnt vmcnt(6)\n"
"v_lshlrev_b32 v249, 16, v129\n"
"v_lshlrev_b32 v250, 16, v130\n"
"v_mfma_f32_16x16x32_bf16 v[44:47], v[96:99], a[128:131], v[44:47]\n"
"v_add_f32 v58, v58, v249\n"
"v_lshlrev_b32 v247, 16, v133\n"
"v_mfma_f32_16x16x32_bf16 v[44:47], v[100:103], a[132:135], v[44:47]\n"
"v_and_b32 v63, s73, v127\n"
"v_lshlrev_b32 v246, 16, v132\n"
"v_mfma_f32_16x16x32_bf16 v[44:47], v[104:107], a[136:139], v[44:47]\n"
"s_waitcnt vmcnt(5)\n"
"ds_read_b64_tr_b16 v[102:103], v233 offset:34816\n"
"v_add_f32 v60, v60, v250\n"
"v_mfma_f32_16x16x32_bf16 v[44:47], v[108:111], a[140:143], v[44:47]\n"
"v_add_f32 v56, v56, v246\n"
"v_lshlrev_b32 v249, 16, v135\n"
"v_mfma_f32_16x16x32_bf16 v[44:47], v[112:115], a[176:179], v[44:47]\n"
"ds_read_b64_tr_b16 v[96:97], v229 offset:32768\n"
"ds_read_b64_tr_b16 v[110:111], v233 offset:43008\n"
"v_mfma_f32_16x16x32_bf16 v[44:47], v[116:119], a[180:183], v[44:47]\n"
"s_waitcnt vmcnt(4)\n"
"v_and_b32 v129, s73, v129\n"
"v_lshlrev_b32 v250, 16, v136\n"
"v_mfma_f32_16x16x32_bf16 v[44:47], v[120:123], a[184:187], v[44:47]\n"
"v_lshlrev_b32 v248, 16, v134\n"
"v_lshlrev_b32 v246, 16, v138\n"
"v_mfma_f32_16x16x32_bf16 v[44:47], v[224:227], a[188:191], v[44:47]\n"
"s_waitcnt lgkmcnt(3)\n"
"v_and_b32 v136, s73, v136\n"
"v_and_b32 v138, s73, v138\n"
"v_mfma_f32_16x16x32_bf16 v[0:3], v[180:183], a[48:51], v[0:3]\n"
"s_waitcnt vmcnt(3)\n"
"ds_read_b64_tr_b16 v[226:227], v233 offset:59392\n"
"v_and_b32 v128, s73, v128\n"
"v_mfma_f32_16x16x32_bf16 v[0:3], v[184:187], a[52:55], v[0:3]\n"
"ds_read_b64_tr_b16 v[100:101], v233 offset:32768\n"
"v_and_b32 v61, s73, v126\n"
"v_mfma_f32_16x16x32_bf16 v[0:3], v[188:191], a[56:59], v[0:3]\n"
"v_add_f32 v62, v62, v251\n"
"v_add_f32 v56, v56, v250\n"
"v_mfma_f32_16x16x32_bf16 v[0:3], v[192:195], a[60:63], v[0:3]\n"
"s_waitcnt vmcnt(2)\n"
"ds_read_b64_tr_b16 v[108:109], v233 offset:40960\n"
"v_add_f32 v60, v60, v248\n"
"v_mfma_f32_16x16x32_bf16 v[0:3], v[208:211], a[0:3], v[0:3]\n"
"v_add_f32 v57, v57, v128\n"
"v_lshlrev_b32 v248, 16, v140\n"
"v_mfma_f32_16x16x32_bf16 v[0:3], v[212:215], a[4:7], v[0:3]\n"
"s_waitcnt vmcnt(1)\n"
"ds_read_b64_tr_b16 v[114:115], v229 offset:51200\n"
"v_lshlrev_b32 v251, 16, v137\n"
"v_mfma_f32_16x16x32_bf16 v[0:3], v[216:219], a[8:11], v[0:3]\n"
"v_add_f32 v58, v58, v247\n"
"v_and_b32 v135, s73, v135\n"
"v_mfma_f32_16x16x32_bf16 v[0:3], v[220:223], a[12:15], v[0:3]\n"
"ds_read_b64_tr_b16 v[116:117], v233 offset:49152\n"
"v_and_b32 v132, s73, v132\n"
"v_and_b32 v133, s73, v133\n"
"v_and_b32 v137, s73, v137\n"
"v_add_f32 v58, v58, v251\n"
"v_add_f32 v60, v60, v246\n"
"v_lshlrev_b32 v250, 16, v142\n"
"v_mfma_f32_16x16x32_bf16 v[16:19], v[220:223], a[28:31], v[16:19]\n"
"ds_read_b64_tr_b16 v[224:225], v233 offset:57344\n"
"v_lshlrev_b32 v247, 16, v139\n"
"v_mfma_f32_16x16x32_bf16 v[16:19], v[216:219], a[24:27], v[16:19]\n"
"ds_read_b64_tr_b16 v[120:121], v229 offset:57344\n"
"v_add_f32 v56, v56, v248\n"
"v_mfma_f32_16x16x32_bf16 v[16:19], v[212:215], a[20:23], v[16:19]\n"
"ds_read_b64_tr_b16 v[106:107], v229 offset:43008\n"
"ds_read_b64_tr_b16 v[112:113], v229 offset:49152\n"
"v_mfma_f32_16x16x32_bf16 v[16:19], v[208:211], a[16:19], v[16:19]\n"
"ds_read_b64_tr_b16 v[122:123], v229 offset:59392\n"
"v_lshlrev_b32 v246, 16, v144\n"
"v_mfma_f32_16x16x32_bf16 v[16:19], v[192:195], a[76:79], v[16:19]\n"
"s_waitcnt vmcnt(0)\n"
"v_add_f32 v62, v62, v249\n"
"v_add_f32 v60, v60, v250\n"
"v_mfma_f32_16x16x32_bf16 v[16:19], v[188:191], a[72:75], v[16:19]\n"
"v_subrev_u32 v244, s21, v244\n"
"ds_read_b64_tr_b16 v[98:99], v229 offset:34816\n"
"v_mfma_f32_16x16x32_bf16 v[16:19], v[184:187], a[68:71], v[16:19]\n"
"v_cmp_lt_u32 vcc, s69, v244\n"
"v_add_f32 v59, v59, v129\n"
"v_mfma_f32_16x16x32_bf16 v[16:19], v[180:183], a[64:67], v[16:19]\n"
"ds_read_b64_tr_b16 v[118:119], v233 offset:51200\n"
"v_and_b32 v130, s73, v130\n"
"v_add_f32 v61, v61, v130\n"
"v_add_f32 v57, v57, v132\n"
"v_and_b32 v134, s73, v134\n"
"v_lshlrev_b32 v249, 16, v141\n"
"v_add_f32 v56, v56, v246\n"
"v_lshlrev_b32 v248, 16, v146\n"
"v_cvt_pk_bf16_f32 v0, v0, v1\n"
"s_cbranch_vccnz LGR_%=\nLGRK_%=:\n"
"v_mfma_f32_16x16x32_bf16 v[32:35], v[180:183], a[80:83], v[32:35]\n"
"v_add_f32 v59, v59, v133\n"
"s_cmp_eq_u32 s93, 0\ns_cbranch_scc1 LEP_%=\nLEPK_%=:\n"
"v_mfma_f32_16x16x32_bf16 v[32:35], v[184:187], a[84:87], v[32:35]\n"
"s_mov_b32 s20, 28672\n"
"ds_read_b64_tr_b16 v[104:105], v229 offset:40960\n"
"v_lshlrev_b32 v250, 16, v148\n"
"v_mfma_f32_16x16x32_bf16 v[32:35], v[188:191], a[88:91], v[32:35]\n"
"buffer_load_dwordx4 v[72:75], v236, s[32:35], s20 offen sc1\n"
"v_mfma_f32_16x16x32_bf16 v[32:35], v[192:195], a[92:95], v[32:35]\n"
"v_add_f32 v60, v60, v248\n"
"v_add_f32 v56, v56, v250\n"
"v_mfma_f32_16x16x32_bf16 v[32:35], v[208:211], a[32:35], v[32:35]\n"
"s_mov_b32 s20, 315392\n"
"v_and_b32 v139, s73, v139\n"
"v_lshlrev_b32 v251, 16, v143\n"
"v_mfma_f32_16x16x32_bf16 v[32:35], v[212:215], a[36:39], v[32:35]\n"
"buffer_load_dwordx4 v[76:79], v236, s[32:35], s20 offen sc1\n"
"v_mfma_f32_16x16x32_bf16 v[32:35], v[216:219], a[40:43], v[32:35]\n"
"v_add_f32 v62, v62, v247\n"
"v_lshlrev_b32 v246, 16, v150\n"
"v_mfma_f32_16x16x32_bf16 v[32:35], v[220:223], a[44:47], v[32:35]\n"
"s_mov_b32 s20, 602112\n"
"v_add_f32 v58, v58, v249\n"
"v_lshlrev_b32 v247, 16, v145\n"
"v_mfma_f32_16x16x32_bf16 v[48:51], v[220:223], a[228:231], v[48:51]\n"
"buffer_load_dwordx4 v[80:83], v236, s[32:35], s20 offen sc1\n"
"v_mfma_f32_16x16x32_bf16 v[48:51], v[216:219], a[224:227], v[48:51]\n"
"v_add_f32 v61, v61, v134\n"
"v_add_f32 v60, v60, v246\n"
"v_mfma_f32_16x16x32_bf16 v[48:51], v[212:215], a[212:215], v[48:51]\n"
"s_mov_b32 s20, 888832\n"
"v_and_b32 v140, s73, v140\n"
"v_and_b32 v144, s73, v144\n"
"v_mfma_f32_16x16x32_bf16 v[48:51], v[208:211], a[208:211], v[48:51]\n"
"buffer_load_dwordx4 v[84:87], v236, s[32:35], s20 offen sc1\n"
"v_mfma_f32_16x16x32_bf16 v[48:51], v[192:195], a[236:239], v[48:51]\n"
"v_lshlrev_b32 v249, 16, v147\n"
"v_cvt_pk_bf16_f32 v16, v16, v17\n"
"v_mfma_f32_16x16x32_bf16 v[48:51], v[188:191], a[232:235], v[48:51]\n"
"s_mov_b32 s20, 1175552\n"
"v_add_f32 v61, v61, v138\n"
"v_add_f32 v62, v62, v251\n"
"v_mfma_f32_16x16x32_bf16 v[48:51], v[184:187], a[220:223], v[48:51]\n"
"buffer_load_dwordx4 v[88:91], v236, s[32:35], s20 offen sc1\n"
"v_mfma_f32_16x16x32_bf16 v[48:51], v[180:183], a[216:219], v[48:51]\n"
"s_waitcnt lgkmcnt(0)\n"
"v_add_f32 v59, v59, v137\n"
"v_add_f32 v62, v62, v249\n"
"v_mfma_f32_16x16x32_bf16 v[4:7], v[96:99], a[48:51], v[4:7]\n"
"s_mov_b32 s20, 1462272\n"
"v_and_b32 v131, s73, v131\n"
"v_cvt_pk_bf16_f32 v1, v2, v3\n"
"v_mfma_f32_16x16x32_bf16 v[4:7], v[100:103], a[52:55], v[4:7]\n"
"buffer_load_dwordx4 v[92:95], v236, s[32:35], s20 offen sc1\n"
"v_mfma_f32_16x16x32_bf16 v[4:7], v[104:107], a[56:59], v[4:7]\n"
"v_and_b32 v142, s73, v142\n"
"v_add_f32 v58, v58, v247\n"
"v_mfma_f32_16x16x32_bf16 v[4:7], v[108:111], a[60:63], v[4:7]\n"
"s_mov_b32 s20, 1748992\n"
"v_add_f32 v63, v63, v131\n"
"v_and_b32 v150, s73, v150\n"
"v_mfma_f32_16x16x32_bf16 v[4:7], v[112:115], a[0:3], v[4:7]\n"
"buffer_load_dwordx4 v[196:199], v236, s[32:35], s20 offen sc1\n"
"v_mfma_f32_16x16x32_bf16 v[4:7], v[116:119], a[4:7], v[4:7]\n"
"v_and_b32 v145, s73, v145\n"
"v_and_b32 v148, s73, v148\n"
"v_mfma_f32_16x16x32_bf16 v[4:7], v[120:123], a[8:11], v[4:7]\n"
"s_mov_b32 s20, 2035712\n"
"ds_read_b64_tr_b16 v[186:187], v234 offset:34816\n"
"v_cvt_pk_bf16_f32 v17, v18, v19\n"
"v_mfma_f32_16x16x32_bf16 v[4:7], v[224:227], a[12:15], v[4:7]\n"
"buffer_load_dwordx4 v[200:203], v236, s[32:35], s20 offen sc1\n"
"v_mfma_f32_16x16x32_bf16 v[20:23], v[224:227], a[28:31], v[20:23]\n"
"v_and_b32 v147, s73, v147\n"
"v_lshlrev_b32 v249, 16, v153\n"
"v_mfma_f32_16x16x32_bf16 v[20:23], v[120:123], a[24:27], v[20:23]\n"
"v_add_f32 v63, v63, v135\n"
"v_add_f32 v63, v63, v139\n"
"v_mfma_f32_16x16x32_bf16 v[20:23], v[116:119], a[20:23], v[20:23]\n"
"ds_read_b64_tr_b16 v[210:211], v230 offset:51200\n"
"v_cvt_pk_bf16_f32 v32, v32, v33\n"
"v_mfma_f32_16x16x32_bf16 v[20:23], v[112:115], a[16:19], v[20:23]\n"
"v_add_f32 v57, v57, v136\n"
"v_lshlrev_b32 v248, 16, v152\n"
"v_mfma_f32_16x16x32_bf16 v[20:23], v[108:111], a[76:79], v[20:23]\n"
"ds_read_b64_tr_b16 v[216:217], v230 offset:57344\n"
"v_add_f32 v57, v57, v140\n"
"v_mfma_f32_16x16x32_bf16 v[20:23], v[104:107], a[72:75], v[20:23]\n"
"ds_read_b64_tr_b16 v[180:181], v230 offset:32768\n"
"v_cvt_pk_bf16_f32 v2, v4, v5\n"
"v_mfma_f32_16x16x32_bf16 v[20:23], v[100:103], a[68:71], v[20:23]\n"
"v_add_f32 v56, v56, v248\n"
"v_cvt_pk_bf16_f32 v3, v6, v7\n"
"v_mfma_f32_16x16x32_bf16 v[20:23], v[96:99], a[64:67], v[20:23]\n"
"ds_read_b64_tr_b16 v[188:189], v230 offset:40960\n"
"ds_read_b64_tr_b16 v[212:213], v234 offset:49152\n"
"v_and_b32 v143, s73, v143\n"
"v_add_f32 v63, v63, v143\n"
"v_lshlrev_b32 v251, 16, v149\n"
"v_and_b32 v149, s73, v149\n"
"v_permlane16_swap_b32 v0, v2\n"
"v_permlane16_swap_b32 v1, v3\n"
"v_mfma_f32_16x16x32_bf16 v[36:39], v[96:99], a[80:83], v[36:39]\n"
"v_and_b32 v152, s73, v152\n"
"v_lshlrev_b32 v250, 16, v154\n"
"v_mfma_f32_16x16x32_bf16 v[36:39], v[100:103], a[84:87], v[36:39]\n"
"v_add_f32 v57, v57, v144\n"
"v_and_b32 v154, s73, v154\n"
"v_mfma_f32_16x16x32_bf16 v[36:39], v[104:107], a[88:91], v[36:39]\n"
"ds_read_b64_tr_b16 v[182:183], v230 offset:34816\n"
"ds_read_b64_tr_b16 v[218:219], v230 offset:59392\n"
"buffer_store_dwordx4 v[0:3], v238, s[36:39], 0 offen sc0 sc1\n"
"v_and_b32 v141, s73, v141\n"
"v_add_f32 v61, v61, v142\n"
"v_and_b32 v146, s73, v146\n"
"v_cvt_pk_bf16_f32 v18, v20, v21\n"
"v_mfma_f32_16x16x32_bf16 v[36:39], v[108:111], a[92:95], v[36:39]\n"
"ds_read_b64_tr_b16 v[184:185], v234 offset:32768\n"
"v_lshlrev_b32 v247, 16, v151\n"
"v_mfma_f32_16x16x32_bf16 v[36:39], v[112:115], a[32:35], v[36:39]\n"
"ds_read_b64_tr_b16 v[220:221], v234 offset:57344\n"
"v_add_f32 v60, v60, v250\n"
"v_mfma_f32_16x16x32_bf16 v[36:39], v[116:119], a[36:39], v[36:39]\n"
"v_add_f32 v59, v59, v141\n"
"v_add_f32 v61, v61, v146\n"
"v_mfma_f32_16x16x32_bf16 v[36:39], v[120:123], a[40:43], v[36:39]\n"
"ds_read_b64_tr_b16 v[208:209], v230 offset:49152\n"
"v_add_f32 v63, v63, v147\n"
"v_mfma_f32_16x16x32_bf16 v[36:39], v[224:227], a[44:47], v[36:39]\n"
"ds_read_b64_tr_b16 v[190:191], v230 offset:43008\n"
"v_add_f32 v57, v57, v148\n"
"v_add_f32 v58, v58, v251\n"
"v_and_b32 v151, s73, v151\n"
"v_add_f32 v62, v62, v247\n"
"v_lshlrev_b32 v251, 16, v155\n"
"v_and_b32 v155, s73, v155\n"
"v_add_f32 v62, v62, v251\n"
"v_cvt_pk_bf16_f32 v19, v22, v23\n"
"v_permlane16_swap_b32 v16, v18\n"
"v_mfma_f32_16x16x32_bf16 v[52:55], v[224:227], a[228:231], v[52:55]\n"
"ds_read_b64_tr_b16 v[192:193], v234 offset:40960\n"
"v_and_b32 v153, s73, v153\n"
"v_mfma_f32_16x16x32_bf16 v[52:55], v[120:123], a[224:227], v[52:55]\n"
"v_add_f32 v63, v63, v151\n"
"v_add_f32 v57, v57, v152\n"
"v_mfma_f32_16x16x32_bf16 v[52:55], v[116:119], a[212:215], v[52:55]\n"
"v_permlane16_swap_b32 v17, v19\n"
"v_mfma_f32_16x16x32_bf16 v[52:55], v[112:115], a[208:211], v[52:55]\n"
"ds_read_b64_tr_b16 v[222:223], v234 offset:59392\n"
"v_add_f32 v59, v59, v145\n"
"v_mfma_f32_16x16x32_bf16 v[52:55], v[108:111], a[236:239], v[52:55]\n"
"ds_read_b64_tr_b16 v[214:215], v234 offset:51200\n"
"v_add_f32 v63, v63, v155\n"
"v_mfma_f32_16x16x32_bf16 v[52:55], v[104:107], a[232:235], v[52:55]\n"
"ds_read_b64_tr_b16 v[194:195], v234 offset:43008\n"
"buffer_store_dwordx4 v[16:19], v238, s[36:39], s52 offen sc0 sc1\n"
"v_add_f32 v59, v59, v149\n"
"v_add_f32 v61, v61, v150\n"
"v_add_f32 v58, v58, v249\n"
"v_add_f32 v59, v59, v153\n"
"v_cvt_pk_bf16_f32 v56, v56, v57\n"
"v_mfma_f32_16x16x32_bf16 v[52:55], v[100:103], a[220:223], v[52:55]\n"
"v_add_f32 v61, v61, v154\n"
"v_cvt_pk_bf16_f32 v57, v58, v59\n"
"v_mfma_f32_16x16x32_bf16 v[52:55], v[96:99], a[216:219], v[52:55]\n"
"s_waitcnt lgkmcnt(4)\n"
"v_cvt_pk_bf16_f32 v58, v60, v61\n"
"v_cvt_pk_bf16_f32 v59, v62, v63\n"
"v_mfma_f32_16x16x32_bf16 v[8:11], v[180:183], a[48:51], v[8:11]\n"
"s_nop 0\n"
"v_permlane16_swap_b32 v57, v59\n"
"v_mfma_f32_16x16x32_bf16 v[8:11], v[184:187], a[52:55], v[8:11]\n"
"v_permlane16_swap_b32 v56, v58\n"
"v_mfma_f32_16x16x32_bf16 v[8:11], v[188:191], a[56:59], v[8:11]\n"
"s_waitcnt lgkmcnt(0)\n"
"buffer_store_dwordx4 v[56:59], v253, s[36:39], 0 offen sc0 sc1\n"
"v_cvt_pk_bf16_f32 v33, v34, v35\n"
"v_cvt_pk_bf16_f32 v34, v36, v37\n"
"v_cvt_pk_bf16_f32 v48, v48, v49\n"
"v_mfma_f32_16x16x32_bf16 v[8:11], v[192:195], a[60:63], v[8:11]\n"
"v_permlane16_swap_b32 v32, v34\n"
"v_mfma_f32_16x16x32_bf16 v[8:11], v[208:211], a[0:3], v[8:11]\n"
"ds_read_b64_tr_b16 v[100:101], v235 offset:32768\n"
"v_cvt_pk_bf16_f32 v35, v38, v39\n"
"v_mfma_f32_16x16x32_bf16 v[8:11], v[212:215], a[4:7], v[8:11]\n"
"s_nop 0\n"
"v_permlane16_swap_b32 v33, v35\n"
"v_mfma_f32_16x16x32_bf16 v[8:11], v[216:219], a[8:11], v[8:11]\n"
"ds_read_b64_tr_b16 v[122:123], v231 offset:59392\n"
"v_cvt_pk_bf16_f32 v49, v50, v51\n"
"v_mfma_f32_16x16x32_bf16 v[8:11], v[220:223], a[12:15], v[8:11]\n"
"buffer_store_dwordx4 v[32:35], v238, s[36:39], s53 offen sc0 sc1\n"
"ds_read_b64_tr_b16 v[102:103], v235 offset:34816\n"
"v_mfma_f32_16x16x32_bf16 v[24:27], v[220:223], a[28:31], v[24:27]\n"
"ds_read_b64_tr_b16 v[96:97], v231 offset:32768\n"
"v_cvt_pk_bf16_f32 v50, v52, v53\n"
"v_mfma_f32_16x16x32_bf16 v[24:27], v[216:219], a[24:27], v[24:27]\n"
"s_nop 0\n"
"v_permlane16_swap_b32 v48, v50\n"
"v_mfma_f32_16x16x32_bf16 v[24:27], v[212:215], a[20:23], v[24:27]\n"
"ds_read_b64_tr_b16 v[112:113], v231 offset:49152\n"
"v_cvt_pk_bf16_f32 v51, v54, v55\n"
"v_mfma_f32_16x16x32_bf16 v[24:27], v[208:211], a[16:19], v[24:27]\n"
"s_nop 0\n"
"v_permlane16_swap_b32 v49, v51\n"
"v_mfma_f32_16x16x32_bf16 v[24:27], v[192:195], a[76:79], v[24:27]\n"
"ds_read_b64_tr_b16 v[120:121], v231 offset:57344\n"
"v_mfma_f32_16x16x32_bf16 v[24:27], v[188:191], a[72:75], v[24:27]\n"
"buffer_store_dwordx4 v[48:51], v252, s[36:39], 0 offen sc0 sc1\n"
"ds_read_b64_tr_b16 v[106:107], v231 offset:43008\n"
"v_mfma_f32_16x16x32_bf16 v[24:27], v[184:187], a[68:71], v[24:27]\n"
"ds_read_b64_tr_b16 v[104:105], v231 offset:40960\n"
"v_mfma_f32_16x16x32_bf16 v[24:27], v[180:183], a[64:67], v[24:27]\n"
"ds_read_b64_tr_b16 v[98:99], v231 offset:34816\n"
"v_cvt_pk_bf16_f32 v8, v8, v9\n"
"v_mfma_f32_16x16x32_bf16 v[40:43], v[180:183], a[80:83], v[40:43]\n"
"ds_read_b64_tr_b16 v[108:109], v235 offset:40960\n"
"v_mfma_f32_16x16x32_bf16 v[40:43], v[184:187], a[84:87], v[40:43]\n"
"ds_read_b64_tr_b16 v[118:119], v235 offset:51200\n"
"v_mfma_f32_16x16x32_bf16 v[40:43], v[188:191], a[88:91], v[40:43]\n"
"ds_read_b64_tr_b16 v[110:111], v235 offset:43008\n"
"v_mfma_f32_16x16x32_bf16 v[40:43], v[192:195], a[92:95], v[40:43]\n"
"ds_read_b64_tr_b16 v[114:115], v231 offset:51200\n"
"v_mfma_f32_16x16x32_bf16 v[40:43], v[208:211], a[32:35], v[40:43]\n"
"ds_read_b64_tr_b16 v[116:117], v235 offset:49152\n"
"v_mfma_f32_16x16x32_bf16 v[40:43], v[212:215], a[36:39], v[40:43]\n"
"ds_read_b64_tr_b16 v[224:225], v235 offset:57344\n"
"v_mov_b32 v161, 0\n"
"v_mfma_f32_16x16x32_bf16 v[40:43], v[216:219], a[40:43], v[40:43]\n"
"ds_read_b64_tr_b16 v[226:227], v235 offset:59392\n"
"v_mfma_f32_16x16x32_bf16 v[40:43], v[220:223], a[44:47], v[40:43]\n"
"s_waitcnt lgkmcnt(7)\n"
"v_cvt_pk_bf16_f32 v9, v10, v11\n"
"v_mfma_f32_16x16x32_bf16 v[12:15], v[96:99], a[48:51], v[12:15]\n"
"v_mfma_f32_16x16x32_bf16 v[12:15], v[100:103], a[52:55], v[12:15]\n"
"v_cvt_pk_bf16_f32 v24, v24, v25\n"
"v_mfma_f32_16x16x32_bf16 v[12:15], v[104:107], a[56:59], v[12:15]\n"
"s_waitcnt lgkmcnt(4)\n"
"v_mfma_f32_16x16x32_bf16 v[12:15], v[108:111], a[60:63], v[12:15]\n"
"s_waitcnt lgkmcnt(3)\n"
"v_mfma_f32_16x16x32_bf16 v[12:15], v[112:115], a[0:3], v[12:15]\n"
"s_waitcnt lgkmcnt(2)\n"
"v_mfma_f32_16x16x32_bf16 v[12:15], v[116:119], a[4:7], v[12:15]\n"
"v_mfma_f32_16x16x32_bf16 v[12:15], v[120:123], a[8:11], v[12:15]\n"
"s_waitcnt lgkmcnt(0)\n"
"v_mfma_f32_16x16x32_bf16 v[12:15], v[224:227], a[12:15], v[12:15]\n"
"s_waitcnt vmcnt(5)\n"
"v_mfma_f32_16x16x32_bf16 v[64:67], v[162:165], v[72:75], 0\n"
"v_cvt_pk_bf16_f32 v40, v40, v41\n"
"v_mfma_f32_16x16x32_bf16 v[64:67], v[162:165], v[76:79], v[64:67]\n"
"v_mfma_f32_16x16x32_bf16 v[64:67], v[162:165], v[80:83], v[64:67]\n"
"s_nop 2\n"
"v_cvt_pk_bf16_f32 v10, v12, v13\n"
"v_cvt_pk_bf16_f32 v11, v14, v15\n"
"v_mfma_f32_16x16x32_bf16 v[64:67], v[162:165], v[84:87], v[64:67]\n"
"v_mfma_f32_16x16x32_bf16 v[64:67], v[162:165], v[88:91], v[64:67]\n"
"v_permlane16_swap_b32 v9, v11\n"
"v_mfma_f32_16x16x32_bf16 v[64:67], v[162:165], v[92:95], v[64:67]\n"
"v_permlane16_swap_b32 v8, v10\n"
"v_mfma_f32_16x16x32_bf16 v[64:67], v[162:165], v[196:199], v[64:67]\n"
"v_mfma_f32_16x16x32_bf16 v[64:67], v[162:165], v[200:203], v[64:67]\n"
"buffer_store_dwordx4 v[8:11], v239, s[36:39], 0 offen sc0 sc1\n"
"v_mfma_f32_16x16x32_bf16 v[68:71], v[160:163], v[72:75], 0\n"
"v_mfma_f32_16x16x32_bf16 v[68:71], v[160:163], v[76:79], v[68:71]\n"
"v_cvt_pk_bf16_f32 v41, v42, v43\n"
"v_mfma_f32_16x16x32_bf16 v[68:71], v[160:163], v[80:83], v[68:71]\n"
"v_mfma_f32_16x16x32_bf16 v[68:71], v[160:163], v[84:87], v[68:71]\n"
"v_mfma_f32_16x16x32_bf16 v[68:71], v[160:163], v[88:91], v[68:71]\n"
"s_nop 0\n"
"v_cvt_pk_bf16_f32 v64, v64, v65\n"
"v_cvt_pk_bf16_f32 v65, v66, v67\n"
"v_mfma_f32_16x16x32_bf16 v[68:71], v[160:163], v[92:95], v[68:71]\n"
"v_mfma_f32_16x16x32_bf16 v[68:71], v[160:163], v[196:199], v[68:71]\n"
"v_mfma_f32_16x16x32_bf16 v[68:71], v[160:163], v[200:203], v[68:71]\n"
"v_mfma_f32_16x16x32_bf16 v[28:31], v[224:227], a[28:31], v[28:31]\n"
"v_cvt_pk_bf16_f32 v25, v26, v27\n"
"v_mfma_f32_16x16x32_bf16 v[28:31], v[120:123], a[24:27], v[28:31]\n"
"v_mfma_f32_16x16x32_bf16 v[28:31], v[116:119], a[20:23], v[28:31]\n"
"s_nop 3\n"
"v_cvt_pk_bf16_f32 v66, v68, v69\n"
"v_cvt_pk_bf16_f32 v67, v70, v71\n"
"v_mfma_f32_16x16x32_bf16 v[28:31], v[112:115], a[16:19], v[28:31]\n"
"s_nop 0\n"
"v_permlane16_swap_b32 v65, v67\n"
"v_mfma_f32_16x16x32_bf16 v[28:31], v[108:111], a[76:79], v[28:31]\n"
"v_mfma_f32_16x16x32_bf16 v[28:31], v[104:107], a[72:75], v[28:31]\n"
"v_permlane16_swap_b32 v64, v66\n"
"v_mfma_f32_16x16x32_bf16 v[28:31], v[100:103], a[68:71], v[28:31]\n"
"v_mfma_f32_16x16x32_bf16 v[28:31], v[96:99], a[64:67], v[28:31]\n"
"buffer_store_dwordx4 v[64:67], v253, s[36:39], 0 offen offset:128 sc0 sc1\n"
"v_mfma_f32_16x16x32_bf16 v[44:47], v[96:99], a[80:83], v[44:47]\n"
"v_mfma_f32_16x16x32_bf16 v[44:47], v[100:103], a[84:87], v[44:47]\n"
"v_mfma_f32_16x16x32_bf16 v[44:47], v[104:107], a[88:91], v[44:47]\n"
"v_mfma_f32_16x16x32_bf16 v[44:47], v[108:111], a[92:95], v[44:47]\n"
"s_nop 2\n"
"v_cvt_pk_bf16_f32 v26, v28, v29\n"
"v_cvt_pk_bf16_f32 v27, v30, v31\n"
"v_mfma_f32_16x16x32_bf16 v[44:47], v[112:115], a[32:35], v[44:47]\n"
"s_nop 0\n"
"v_permlane16_swap_b32 v25, v27\n"
"v_mfma_f32_16x16x32_bf16 v[44:47], v[116:119], a[36:39], v[44:47]\n"
"v_permlane16_swap_b32 v24, v26\n"
"v_mfma_f32_16x16x32_bf16 v[44:47], v[120:123], a[40:43], v[44:47]\n"
"v_mfma_f32_16x16x32_bf16 v[44:47], v[224:227], a[44:47], v[44:47]\n"
"buffer_store_dwordx4 v[24:27], v239, s[36:39], s52 offen sc0 sc1\n"
"s_nop 6\n"
"v_cvt_pk_bf16_f32 v42, v44, v45\n"
"v_cvt_pk_bf16_f32 v43, v46, v47\n"
"s_nop 0\n"
"v_permlane16_swap_b32 v40, v42\n"
"v_permlane16_swap_b32 v41, v43\n"
"buffer_store_dwordx4 v[40:43], v239, s[36:39], s53 offen sc0 sc1\n"
"s_branch LFIN_%=\n"
"LSW_%=:\n"
"s_add_u32 s16, s8, s95\n"
"s_addc_u32 s17, s9, 0\n"
"s_sub_u32 s18, 655360, s95\n"
"s_add_u32 s20, s8, s100\n"
"s_addc_u32 s21, s9, 0\n"
"s_sub_u32 s22, 655360, s100\n"
"s_mov_b32 s56, 0\n"
"s_mov_b32 s55, 0\n"
"s_mov_b32 s58, 0\n"
"s_mov_b32 s59, 163840\n"
"s_mov_b32 s52, 9216\n"
"s_mov_b32 s53, 18432\n"
"s_mov_b32 s54, 27648\n"
"v_lshrrev_b32 v206, 7, v242\n"
"v_mul_u32_u24 v206, 1280, v206\n"
"v_and_b32 v240, 0xff, v240\n"
"v_add_u32 v240, v206, v240\n"
"s_branch LSWK_%=\n"
"LGP_%=:\n"
"s_mov_b32 s80, 0x40000\n"
"LGPa_%=:\n"
"s_sleep 4\n"
"global_load_dword v244, v245, s[6:7] sc1\n"
"s_sub_u32 s80, s80, 1\n"
"s_cmp_eq_u32 s80, 0\n"
"s_waitcnt vmcnt(0)\n"
"s_cbranch_scc1 LGPK_%=\n"
"v_subrev_u32 v244, s21, v244\n"
"v_cmp_lt_u32 vcc, s69, v244\n"
"s_nop 4\n"
"s_cbranch_vccnz LGPa_%=\n"
"s_branch LGPK_%=\n"
"LGQ_%=:\n"
"s_mov_b32 s80, 0x40000\n"
"LGQa_%=:\n"
"s_sleep 4\n"
"global_load_dword v244, v245, s[6:7] sc1\n"
"s_sub_u32 s80, s80, 1\n"
"s_cmp_eq_u32 s80, 0\n"
"s_waitcnt vmcnt(0)\n"
"s_cbranch_scc1 LGQK_%=\n"
"v_subrev_u32 v244, s21, v244\n"
"v_cmp_lt_u32 vcc, s69, v244\n"
"s_nop 4\n"
"s_cbranch_vccnz LGQa_%=\n"
"s_branch LGQK_%=\n"
"LGR_%=:\n"
"s_mov_b32 s80, 0x40000\n"
"LGRa_%=:\n"
"s_sleep 4\n"
"global_load_dword v244, v245, s[6:7] sc1\n"
"s_sub_u32 s80, s80, 1\n"
"s_cmp_eq_u32 s80, 0\n"
"s_waitcnt vmcnt(0)\n"
"s_cbranch_scc1 LGRK_%=\n"
"v_subrev_u32 v244, s21, v244\n"
"v_cmp_lt_u32 vcc, s69, v244\n"
"s_nop 4\n"
"s_cbranch_vccnz LGRa_%=\n"
"s_branch LGRK_%=\n"
"LGS_%=:\n"
"v_mov_b32 v255, s93\n"
"s_mov_b64 exec, 1\n"
"global_store_dword v255, v241, s[6:7] sc1\n"
"s_mov_b64 exec, -1\n"
"s_branch LGSK_%=\n"
"LGT_%=:\n"
"v_mov_b32 v255, s93\n"
"s_mov_b64 exec, 1\n"
"global_store_dword v255, v241, s[6:7] sc1\n"
"s_mov_b64 exec, -1\n"
"s_branch LGTK_%=\n"
"LEP_%=:\n"
"s_add_u32 s92, s92, 1\n"
"v_mov_b32 v244, s92\n"
"v_mov_b32 v255, 0\n"
"s_mov_b64 exec, 1\n"
"global_store_dword v255, v244, s[6:7] offset:128 sc1\n"
"s_mov_b64 exec, -1\n"
"s_branch LEPK_%=\n"
"LPCE0_%=:\n"
"s_memrealtime s[80:81]\n"
"s_sub_u32 s82, s67, s66\n"
"s_cmp_gt_i32 s82, 0\n"
"s_cselect_b32 s83, s67, s66\n"
"s_waitcnt lgkmcnt(0)\n"
"s_sub_u32 s82, s83, s80\n"
"s_cmp_gt_i32 s82, 3\n"
"s_cbranch_scc0 LPEE0_%=\n"
"LPDE0_%=:\n"
"s_sleep 1\n"
"s_memrealtime s[80:81]\n"
"s_waitcnt lgkmcnt(0)\n"
"s_sub_u32 s82, s83, s80\n"
"s_cmp_gt_i32 s82, 3\n"
"s_cbranch_scc1 LPDE0_%=\n"
"LPEE0_%=:\n"
"s_add_u32 s66, s66, 126\n"
"s_add_u32 s67, s80, s72\n"
"s_sub_u32 s72, s72, 0\n"
"s_max_u32 s72, s72, 122\n"
"s_branch LPKE0_%=\n"
"LPCE1_%=:\n"
"s_memrealtime s[80:81]\n"
"s_sub_u32 s82, s67, s66\n"
"s_cmp_gt_i32 s82, 0\n"
"s_cselect_b32 s83, s67, s66\n"
"s_waitcnt lgkmcnt(0)\n"
"s_sub_u32 s82, s83, s80\n"
"s_cmp_gt_i32 s82, 3\n"
"s_cbranch_scc0 LPEE1_%=\n"
"LPDE1_%=:\n"
"s_sleep 1\n"
"s_memrealtime s[80:81]\n"
"s_waitcnt lgkmcnt(0)\n"
"s_sub_u32 s82, s83, s80\n"
"s_cmp_gt_i32 s82, 3\n"
"s_cbranch_scc1 LPDE1_%=\n"
"LPEE1_%=:\n"
"s_add_u32 s66, s66, 126\n"
"s_add_u32 s67, s80, s72\n"
"s_sub_u32 s72, s72, 0\n"
"s_max_u32 s72, s72, 122\n"
"s_branch LPKE1_%=\n"
"LPCT0_%=:\n"
"s_memrealtime s[80:81]\n"
"s_sub_u32 s82, s67, s66\n"
"s_cmp_gt_i32 s82, 0\n"
"s_cselect_b32 s83, s67, s66\n"
"s_waitcnt lgkmcnt(0)\n"
"s_sub_u32 s82, s83, s80\n"
"s_cmp_gt_i32 s82, 3\n"
"s_cbranch_scc0 LPET0_%=\n"
"LPDT0_%=:\n"
"s_sleep 1\n"
"s_memrealtime s[80:81]\n"
"s_waitcnt lgkmcnt(0)\n"
"s_sub_u32 s82, s83, s80\n"
"s_cmp_gt_i32 s82, 3\n"
"s_cbranch_scc1 LPDT0_%=\n"
"LPET0_%=:\n"
"s_add_u32 s66, s66, 126\n"
"s_add_u32 s67, s80, s72\n"
"s_sub_u32 s72, s72, 0\n"
"s_max_u32 s72, s72, 122\n"
"s_branch LPKT0_%=\n"
"LPCT1_%=:\n"
"s_memrealtime s[80:81]\n"
"s_sub_u32 s82, s67, s66\n"
"s_cmp_gt_i32 s82, 0\n"
"s_cselect_b32 s83, s67, s66\n"
"s_waitcnt lgkmcnt(0)\n"
"s_sub_u32 s82, s83, s80\n"
"s_cmp_gt_i32 s82, 3\n"
"s_cbranch_scc0 LPET1_%=\n"
"LPDT1_%=:\n"
"s_sleep 1\n"
"s_memrealtime s[80:81]\n"
"s_waitcnt lgkmcnt(0)\n"
"s_sub_u32 s82, s83, s80\n"
"s_cmp_gt_i32 s82, 3\n"
"s_cbranch_scc1 LPDT1_%=\n"
"LPET1_%=:\n"
"s_add_u32 s66, s66, 126\n"
"s_add_u32 s67, s80, s72\n"
"s_sub_u32 s72, s72, 0\n"
"s_max_u32 s72, s72, 122\n"
"s_branch LPKT1_%=\n"
"LPCL0_%=:\n"
"s_memrealtime s[80:81]\n"
"s_sub_u32 s82, s67, s66\n"
"s_cmp_gt_i32 s82, 0\n"
"s_cselect_b32 s83, s67, s66\n"
"s_waitcnt lgkmcnt(0)\n"
"s_sub_u32 s82, s83, s80\n"
"s_cmp_gt_i32 s82, 3\n"
"s_cbranch_scc0 LPEL0_%=\n"
"LPDL0_%=:\n"
"s_sleep 1\n"
"s_memrealtime s[80:81]\n"
"s_waitcnt lgkmcnt(0)\n"
"s_sub_u32 s82, s83, s80\n"
"s_cmp_gt_i32 s82, 3\n"
"s_cbranch_scc1 LPDL0_%=\n"
"LPEL0_%=:\n"
"s_add_u32 s66, s66, 126\n"
"s_add_u32 s67, s80, s72\n"
"s_sub_u32 s72, s72, 0\n"
"s_max_u32 s72, s72, 122\n"
"s_branch LPKL0_%=\n"
"LPCL1_%=:\n"
"s_memrealtime s[80:81]\n"
"s_sub_u32 s82, s67, s66\n"
"s_cmp_gt_i32 s82, 0\n"
"s_cselect_b32 s83, s67, s66\n"
"s_waitcnt lgkmcnt(0)\n"
"s_sub_u32 s82, s83, s80\n"
"s_cmp_gt_i32 s82, 3\n"
"s_cbranch_scc0 LPEL1_%=\n"
"LPDL1_%=:\n"
"s_sleep 1\n"
"s_memrealtime s[80:81]\n"
"s_waitcnt lgkmcnt(0)\n"
"s_sub_u32 s82, s83, s80\n"
"s_cmp_gt_i32 s82, 3\n"
"s_cbranch_scc1 LPDL1_%=\n"
"LPEL1_%=:\n"
"s_add_u32 s66, s66, 126\n"
"s_add_u32 s67, s80, s72\n"
"s_sub_u32 s72, s72, 0\n"
"s_max_u32 s72, s72, 122\n"
"s_branch LPKL1_%=\n"
"LPCL2_%=:\n"
"s_memrealtime s[80:81]\n"
"s_sub_u32 s82, s67, s66\n"
"s_cmp_gt_i32 s82, 0\n"
"s_cselect_b32 s83, s67, s66\n"
"s_waitcnt lgkmcnt(0)\n"
"s_sub_u32 s82, s83, s80\n"
"s_cmp_gt_i32 s82, 3\n"
"s_cbranch_scc0 LPEL2_%=\n"
"LPDL2_%=:\n"
"s_sleep 1\n"
"s_memrealtime s[80:81]\n"
"s_waitcnt lgkmcnt(0)\n"
"s_sub_u32 s82, s83, s80\n"
"s_cmp_gt_i32 s82, 3\n"
"s_cbranch_scc1 LPDL2_%=\n"
"LPEL2_%=:\n"
"s_add_u32 s66, s66, 126\n"
"s_add_u32 s67, s80, s72\n"
"s_sub_u32 s72, s72, 0\n"
"s_max_u32 s72, s72, 122\n"
"s_branch LPKL2_%=\n"
"LPCL3_%=:\n"
"s_memrealtime s[80:81]\n"
"s_sub_u32 s82, s67, s66\n"
"s_cmp_gt_i32 s82, 0\n"
"s_cselect_b32 s83, s67, s66\n"
"s_waitcnt lgkmcnt(0)\n"
"s_sub_u32 s82, s83, s80\n"
"s_cmp_gt_i32 s82, 3\n"
"s_cbranch_scc0 LPEL3_%=\n"
"LPDL3_%=:\n"
"s_sleep 1\n"
"s_memrealtime s[80:81]\n"
"s_waitcnt lgkmcnt(0)\n"
"s_sub_u32 s82, s83, s80\n"
"s_cmp_gt_i32 s82, 3\n"
"s_cbranch_scc1 LPDL3_%=\n"
"LPEL3_%=:\n"
"s_add_u32 s66, s66, 126\n"
"s_add_u32 s67, s80, s72\n"
"s_sub_u32 s72, s72, 0\n"
"s_max_u32 s72, s72, 122\n"
"s_branch LPKL3_%=\n"
"LEND_%=:\n"
"LSLOW_%=:\n"
"s_waitcnt vmcnt(0) lgkmcnt(0)\n"
"s_nop 9\n"
"v_bfe_u32 v148, v242, 4, 4\n"
"v_bfe_u32 v149, v242, 8, 2\n"
"v_lshlrev_b32 v144, 8, v148\n"
"v_lshl_add_u32 v144, v149, 4, v144\n"
"v_add_u32 v144, s43, v144\n"
"v_xor_b32 v145, 64, v144\n"
"v_lshlrev_b32 v146, 1, v148\n"
"v_lshl_add_u32 v146, v149, 10, v146\n"
"v_add_u32 v146, s43, v146\n"
"v_xor_b32 v147, 64, v146\n"
"v_mov_b32 v152, 0xff7fffff\n"
"v_mov_b32 v155, 0\n"
"v_mov_b32 v0, 0\n"
"v_mov_b32 v1, 0\n"
"v_mov_b32 v2, 0\n"
"v_mov_b32 v3, 0\n"
"v_mov_b32 v4, 0\n"
"v_mov_b32 v5, 0\n"
"v_mov_b32 v6, 0\n"
"v_mov_b32 v7, 0\n"
"v_mov_b32 v8, 0\n"
"v_mov_b32 v9, 0\n"
"v_mov_b32 v10, 0\n"
"v_mov_b32 v11, 0\n"
"v_mov_b32 v12, 0\n"
"v_mov_b32 v13, 0\n"
"v_mov_b32 v14, 0\n"
"v_mov_b32 v15, 0\n"
"v_mov_b32 v16, 0\n"
"v_mov_b32 v17, 0\n"
"v_mov_b32 v18, 0\n"
"v_mov_b32 v19, 0\n"
"v_mov_b32 v20, 0\n"
"v_mov_b32 v21, 0\n"
"v_mov_b32 v22, 0\n"
"v_mov_b32 v23, 0\n"
"v_mov_b32 v24, 0\n"
"v_mov_b32 v25, 0\n"
"v_mov_b32 v26, 0\n"
"v_mov_b32 v27, 0\n"
"v_mov_b32 v28, 0\n"
"v_mov_b32 v29, 0\n"
"v_mov_b32 v30, 0\n"
"v_mov_b32 v31, 0\n"
"v_mov_b32 v156, 0xff7fffff\n"
"v_mov_b32 v159, 0\n"
"v_mov_b32 v32, 0\n"
"v_mov_b32 v33, 0\n"
"v_mov_b32 v34, 0\n"
"v_mov_b32 v35, 0\n"
"v_mov_b32 v36, 0\n"
"v_mov_b32 v37, 0\n"
"v_mov_b32 v38, 0\n"
"v_mov_b32 v39, 0\n"
"v_mov_b32 v40, 0\n"
"v_mov_b32 v41, 0\n"
"v_mov_b32 v42, 0\n"
"v_mov_b32 v43, 0\n"
"v_mov_b32 v44, 0\n"
"v_mov_b32 v45, 0\n"
"v_mov_b32 v46, 0\n"
"v_mov_b32 v47, 0\n"
"v_mov_b32 v48, 0\n"
"v_mov_b32 v49, 0\n"
"v_mov_b32 v50, 0\n"
"v_mov_b32 v51, 0\n"
"v_mov_b32 v52, 0\n"
"v_mov_b32 v53, 0\n"
"v_mov_b32 v54, 0\n"
"v_mov_b32 v55, 0\n"
"v_mov_b32 v56, 0\n"
"v_mov_b32 v57, 0\n"
"v_mov_b32 v58, 0\n"
"v_mov_b32 v59, 0\n"
"v_mov_b32 v60, 0\n"
"v_mov_b32 v61, 0\n"
"v_mov_b32 v62, 0\n"
"v_mov_b32 v63, 0\n"
"v_mov_b32 v160, 0xff7fffff\n"
"v_mov_b32 v163, 0\n"
"v_mov_b32 v64, 0\n"
"v_mov_b32 v65, 0\n"
"v_mov_b32 v66, 0\n"
"v_mov_b32 v67, 0\n"
"v_mov_b32 v68, 0\n"
"v_mov_b32 v69, 0\n"
"v_mov_b32 v70, 0\n"
"v_mov_b32 v71, 0\n"
"v_mov_b32 v72, 0\n"
"v_mov_b32 v73, 0\n"
"v_mov_b32 v74, 0\n"
"v_mov_b32 v75, 0\n"
"v_mov_b32 v76, 0\n"
"v_mov_b32 v77, 0\n"
"v_mov_b32 v78, 0\n"
"v_mov_b32 v79, 0\n"
"v_mov_b32 v80, 0\n"
"v_mov_b32 v81, 0\n"
"v_mov_b32 v82, 0\n"
"v_mov_b32 v83, 0\n"
"v_mov_b32 v84, 0\n"
"v_mov_b32 v85, 0\n"
"v_mov_b32 v86, 0\n"
"v_mov_b32 v87, 0\n"
"v_mov_b32 v88, 0\n"
"v_mov_b32 v89, 0\n"
"v_mov_b32 v90, 0\n"
"v_mov_b32 v91, 0\n"
"v_mov_b32 v92, 0\n"
"v_mov_b32 v93, 0\n"
"v_mov_b32 v94, 0\n"
"v_mov_b32 v95, 0\n"
"v_mov_b32 v164, 0xff7fffff\n"
"v_mov_b32 v167, 0\n"
"v_accvgpr_write_b32 a208, 0\n"
"v_accvgpr_write_b32 a209, 0\n"
"v_accvgpr_write_b32 a210, 0\n"
"v_accvgpr_write_b32 a211, 0\n"
"v_accvgpr_write_b32 a212, 0\n"
"v_accvgpr_write_b32 a213, 0\n"
"v_accvgpr_write_b32 a214, 0\n"
"v_accvgpr_write_b32 a215, 0\n"
"v_accvgpr_write_b32 a216, 0\n"
"v_accvgpr_write_b32 a217, 0\n"
"v_accvgpr_write_b32 a218, 0\n"
"v_accvgpr_write_b32 a219, 0\n"
"v_accvgpr_write_b32 a220, 0\n"
"v_accvgpr_write_b32 a221, 0\n"
"v_accvgpr_write_b32 a222, 0\n"
"v_accvgpr_write_b32 a223, 0\n"
"v_accvgpr_write_b32 a224, 0\n"
"v_accvgpr_write_b32 a225, 0\n"
"v_accvgpr_write_b32 a226, 0\n"
"v_accvgpr_write_b32 a227, 0\n"
"v_accvgpr_write_b32 a228, 0\n"
"v_accvgpr_write_b32 a229, 0\n"
"v_accvgpr_write_b32 a230, 0\n"
"v_accvgpr_write_b32 a231, 0\n"
"v_accvgpr_write_b32 a232, 0\n"
"v_accvgpr_write_b32 a233, 0\n"
"v_accvgpr_write_b32 a234, 0\n"
"v_accvgpr_write_b32 a235, 0\n"
"v_accvgpr_write_b32 a236, 0\n"
"v_accvgpr_write_b32 a237, 0\n"
"v_accvgpr_write_b32 a238, 0\n"
"v_accvgpr_write_b32 a239, 0\n"
"s_lshl_b32 s80, s47, 16\n"
"s_sub_u32 s80, s80, 261248\n"
"s_add_u32 s81, s80, 4096\n"
"s_mov_b32 s82, 0\n"
"LSLOOP_%=:\n"
"s_lshl_b32 s83, s82, 6\n"
"s_and_b32 s83, s83, 64\n"
"buffer_load_dwordx4 v[128:131], v144, s[84:87], s80 offen offset:0\n"
"buffer_load_dwordx4 v[132:135], v145, s[84:87], s80 offen offset:0\n"
"buffer_load_dwordx4 v[136:139], v144, s[84:87], s80 offen offset:128\n"
"buffer_load_dwordx4 v[140:143], v145, s[84:87], s80 offen offset:128\n"
"s_waitcnt vmcnt(0)\n"
"v_mfma_f32_16x16x32_bf16 v[96:99], v[128:131], a[0:3], 0\n"
"v_mfma_f32_16x16x32_bf16 v[96:99], v[132:135], a[4:7], v[96:99]\n"
"v_mfma_f32_16x16x32_bf16 v[96:99], v[136:139], a[8:11], v[96:99]\n"
"v_mfma_f32_16x16x32_bf16 v[96:99], v[140:143], a[12:15], v[96:99]\n"
"v_mfma_f32_16x16x32_bf16 v[104:107], v[128:131], a[16:19], 0\n"
"v_mfma_f32_16x16x32_bf16 v[104:107], v[132:135], a[20:23], v[104:107]\n"
"v_mfma_f32_16x16x32_bf16 v[104:107], v[136:139], a[24:27], v[104:107]\n"
"v_mfma_f32_16x16x32_bf16 v[104:107], v[140:143], a[28:31], v[104:107]\n"
"v_mfma_f32_16x16x32_bf16 v[112:115], v[128:131], a[32:35], 0\n"
"v_mfma_f32_16x16x32_bf16 v[112:115], v[132:135], a[36:39], v[112:115]\n"
"v_mfma_f32_16x16x32_bf16 v[112:115], v[136:139], a[40:43], v[112:115]\n"
"v_mfma_f32_16x16x32_bf16 v[112:115], v[140:143], a[44:47], v[112:115]\n"
"v_mfma_f32_16x16x32_bf16 v[120:123], v[128:131], a[192:195], 0\n"
"v_mfma_f32_16x16x32_bf16 v[120:123], v[132:135], a[196:199], v[120:123]\n"
"v_mfma_f32_16x16x32_bf16 v[120:123], v[136:139], a[200:203], v[120:123]\n"
"v_mfma_f32_16x16x32_bf16 v[120:123], v[140:143], a[204:207], v[120:123]\n"
"s_nop 7\n"
"buffer_load_dwordx4 v[128:131], v144, s[84:87], s81 offen offset:0\n"
"buffer_load_dwordx4 v[132:135], v145, s[84:87], s81 offen offset:0\n"
"buffer_load_dwordx4 v[136:139], v144, s[84:87], s81 offen offset:128\n"
"buffer_load_dwordx4 v[140:143], v145, s[84:87], s81 offen offset:128\n"
"s_waitcnt vmcnt(0)\n"
"v_mfma_f32_16x16x32_bf16 v[100:103], v[128:131], a[0:3], 0\n"
"v_mfma_f32_16x16x32_bf16 v[100:103], v[132:135], a[4:7], v[100:103]\n"
"v_mfma_f32_16x16x32_bf16 v[100:103], v[136:139], a[8:11], v[100:103]\n"
"v_mfma_f32_16x16x32_bf16 v[100:103], v[140:143], a[12:15], v[100:103]\n"
"v_mfma_f32_16x16x32_bf16 v[108:111], v[128:131], a[16:19], 0\n"
"v_mfma_f32_16x16x32_bf16 v[108:111], v[132:135], a[20:23], v[108:111]\n"
"v_mfma_f32_16x16x32_bf16 v[108:111], v[136:139], a[24:27], v[108:111]\n"
"v_mfma_f32_16x16x32_bf16 v[108:111], v[140:143], a[28:31], v[108:111]\n"
"v_mfma_f32_16x16x32_bf16 v[116:119], v[128:131], a[32:35], 0\n"
"v_mfma_f32_16x16x32_bf16 v[116:119], v[132:135], a[36:39], v[116:119]\n"
"v_mfma_f32_16x16x32_bf16 v[116:119], v[136:139], a[40:43], v[116:119]\n"
"v_mfma_f32_16x16x32_bf16 v[116:119], v[140:143], a[44:47], v[116:119]\n"
"v_mfma_f32_16x16x32_bf16 v[124:127], v[128:131], a[192:195], 0\n"
"v_mfma_f32_16x16x32_bf16 v[124:127], v[132:135], a[196:199], v[124:127]\n"
"v_mfma_f32_16x16x32_bf16 v[124:127], v[136:139], a[200:203], v[124:127]\n"
"v_mfma_f32_16x16x32_bf16 v[124:127], v[140:143], a[204:207], v[124:127]\n"
"s_nop 7\n"
"v_max3_f32 v153, v96, v97, v98\n"
"v_max3_f32 v153, v153, v99, v100\n"
"v_max3_f32 v153, v153, v101, v102\n"
"v_max3_f32 v153, v153, v103, v152\n"
"v_mov_b32 v154, v153\n"
"s_nop 1\n"
"v_permlane32_swap_b32 v153, v154\n"
"v_max_f32 v153, v153, v154\n"
"v_mov_b32 v154, v153\n"
"s_nop 1\n"
"v_permlane16_swap_b32 v153, v154\n"
"v_max_f32 v153, v153, v154\n"
"v_sub_f32 v154, v152, v153\n"
"v_exp_f32 v154, v154\n"
"v_mov_b32 v152, v153\n"
"v_sub_f32 v96, v96, v152\n"
"v_sub_f32 v97, v97, v152\n"
"v_sub_f32 v98, v98, v152\n"
"v_sub_f32 v99, v99, v152\n"
"v_sub_f32 v100, v100, v152\n"
"v_sub_f32 v101, v101, v152\n"
"v_sub_f32 v102, v102, v152\n"
"v_sub_f32 v103, v103, v152\n"
"v_exp_f32 v96, v96\n"
"v_exp_f32 v97, v97\n"
"v_exp_f32 v98, v98\n"
"v_exp_f32 v99, v99\n"
"v_exp_f32 v100, v100\n"
"v_exp_f32 v101, v101\n"
"v_exp_f32 v102, v102\n"
"v_exp_f32 v103, v103\n"
"v_mul_f32 v155, v155, v154\n"
"v_add_f32 v155, v155, v96\n"
"v_add_f32 v155, v155, v97\n"
"v_add_f32 v155, v155, v98\n"
"v_add_f32 v155, v155, v99\n"
"v_add_f32 v155, v155, v100\n"
"v_add_f32 v155, v155, v101\n"
"v_add_f32 v155, v155, v102\n"
"v_add_f32 v155, v155, v103\n"
"v_cvt_pk_bf16_f32 v96, v96, v97\n"
"v_cvt_pk_bf16_f32 v97, v98, v99\n"
"v_cvt_pk_bf16_f32 v98, v100, v101\n"
"v_cvt_pk_bf16_f32 v99, v102, v103\n"
"v_mov_b32_dpp v154, v154 row_ror:12 row_mask:0x2 bank_mask:0xf\n"
"s_nop 1\n"
"v_mov_b32_dpp v154, v154 row_ror:8 row_mask:0x4 bank_mask:0xf\n"
"s_nop 1\n"
"v_mov_b32_dpp v154, v154 row_ror:4 row_mask:0x8 bank_mask:0xf\n"
"s_nop 1\n"
"v_mul_f32_dpp v0, v154, v0 row_newbcast:0 row_mask:0xf bank_mask:0xf\n"
"v_mul_f32_dpp v1, v154, v1 row_newbcast:1 row_mask:0xf bank_mask:0xf\n"
"v_mul_f32_dpp v2, v154, v2 row_newbcast:2 row_mask:0xf bank_mask:0xf\n"
"v_mul_f32_dpp v3, v154, v3 row_newbcast:3 row_mask:0xf bank_mask:0xf\n"
"v_mul_f32_dpp v4, v154, v4 row_newbcast:0 row_mask:0xf bank_mask:0xf\n"
"v_mul_f32_dpp v5, v154, v5 row_newbcast:1 row_mask:0xf bank_mask:0xf\n"
"v_mul_f32_dpp v6, v154, v6 row_newbcast:2 row_mask:0xf bank_mask:0xf\n"
"v_mul_f32_dpp v7, v154, v7 row_newbcast:3 row_mask:0xf bank_mask:0xf\n"
"v_mul_f32_dpp v8, v154, v8 row_newbcast:0 row_mask:0xf bank_mask:0xf\n"
"v_mul_f32_dpp v9, v154, v9 row_newbcast:1 row_mask:0xf bank_mask:0xf\n"
"v_mul_f32_dpp v10, v154, v10 row_newbcast:2 row_mask:0xf bank_mask:0xf\n"
"v_mul_f32_dpp v11, v154, v11 row_newbcast:3 row_mask:0xf bank_mask:0xf\n"
"v_mul_f32_dpp v12, v154, v12 row_newbcast:0 row_mask:0xf bank_mask:0xf\n"
"v_mul_f32_dpp v13, v154, v13 row_newbcast:1 row_mask:0xf bank_mask:0xf\n"
"v_mul_f32_dpp v14, v154, v14 row_newbcast:2 row_mask:0xf bank_mask:0xf\n"
"v_mul_f32_dpp v15, v154, v15 row_newbcast:3 row_mask:0xf bank_mask:0xf\n"
"v_mul_f32_dpp v16, v154, v16 row_newbcast:0 row_mask:0xf bank_mask:0xf\n"
"v_mul_f32_dpp v17, v154, v17 row_newbcast:1 row_mask:0xf bank_mask:0xf\n"
"v_mul_f32_dpp v18, v154, v18 row_newbcast:2 row_mask:0xf bank_mask:0xf\n"
"v_mul_f32_dpp v19, v154, v19 row_newbcast:3 row_mask:0xf bank_mask:0xf\n"
"v_mul_f32_dpp v20, v154, v20 row_newbcast:0 row_mask:0xf bank_mask:0xf\n"
"v_mul_f32_dpp v21, v154, v21 row_newbcast:1 row_mask:0xf bank_mask:0xf\n"
"v_mul_f32_dpp v22, v154, v22 row_newbcast:2 row_mask:0xf bank_mask:0xf\n"
"v_mul_f32_dpp v23, v154, v23 row_newbcast:3 row_mask:0xf bank_mask:0xf\n"
"v_mul_f32_dpp v24, v154, v24 row_newbcast:0 row_mask:0xf bank_mask:0xf\n"
"v_mul_f32_dpp v25, v154, v25 row_newbcast:1 row_mask:0xf bank_mask:0xf\n"
"v_mul_f32_dpp v26, v154, v26 row_newbcast:2 row_mask:0xf bank_mask:0xf\n"
"v_mul_f32_dpp v27, v154, v27 row_newbcast:3 row_mask:0xf bank_mask:0xf\n"
"v_mul_f32_dpp v28, v154, v28 row_newbcast:0 row_mask:0xf bank_mask:0xf\n"
"v_mul_f32_dpp v29, v154, v29 row_newbcast:1 row_mask:0xf bank_mask:0xf\n"
"v_mul_f32_dpp v30, v154, v30 row_newbcast:2 row_mask:0xf bank_mask:0xf\n"
"v_mul_f32_dpp v31, v154, v31 row_newbcast:3 row_mask:0xf bank_mask:0xf\n"
"v_max3_f32 v157, v104, v105, v106\n"
"v_max3_f32 v157, v157, v107, v108\n"
"v_max3_f32 v157, v157, v109, v110\n"
"v_max3_f32 v157, v157, v111, v156\n"
"v_mov_b32 v158, v157\n"
"s_nop 1\n"
"v_permlane32_swap_b32 v157, v158\n"
"v_max_f32 v157, v157, v158\n"
"v_mov_b32 v158, v157\n"
"s_nop 1\n"
"v_permlane16_swap_b32 v157, v158\n"
"v_max_f32 v157, v157, v158\n"
"v_sub_f32 v158, v156, v157\n"
"v_exp_f32 v158, v158\n"
"v_mov_b32 v156, v157\n"
"v_sub_f32 v104, v104, v156\n"
"v_sub_f32 v105, v105, v156\n"
"v_sub_f32 v106, v106, v156\n"
"v_sub_f32 v107, v107, v156\n"
"v_sub_f32 v108, v108, v156\n"
"v_sub_f32 v109, v109, v156\n"
"v_sub_f32 v110, v110, v156\n"
"v_sub_f32 v111, v111, v156\n"
"v_exp_f32 v104, v104\n"
"v_exp_f32 v105, v105\n"
"v_exp_f32 v106, v106\n"
"v_exp_f32 v107, v107\n"
"v_exp_f32 v108, v108\n"
"v_exp_f32 v109, v109\n"
"v_exp_f32 v110, v110\n"
"v_exp_f32 v111, v111\n"
"v_mul_f32 v159, v159, v158\n"
"v_add_f32 v159, v159, v104\n"
"v_add_f32 v159, v159, v105\n"
"v_add_f32 v159, v159, v106\n"
"v_add_f32 v159, v159, v107\n"
"v_add_f32 v159, v159, v108\n"
"v_add_f32 v159, v159, v109\n"
"v_add_f32 v159, v159, v110\n"
"v_add_f32 v159, v159, v111\n"
"v_cvt_pk_bf16_f32 v104, v104, v105\n"
"v_cvt_pk_bf16_f32 v105, v106, v107\n"
"v_cvt_pk_bf16_f32 v106, v108, v109\n"
"v_cvt_pk_bf16_f32 v107, v110, v111\n"
"v_mov_b32_dpp v158, v158 row_ror:12 row_mask:0x2 bank_mask:0xf\n"
"s_nop 1\n"
"v_mov_b32_dpp v158, v158 row_ror:8 row_mask:0x4 bank_mask:0xf\n"
"s_nop 1\n"
"v_mov_b32_dpp v158, v158 row_ror:4 row_mask:0x8 bank_mask:0xf\n"
"s_nop 1\n"
"v_mul_f32_dpp v32, v158, v32 row_newbcast:0 row_mask:0xf bank_mask:0xf\n"
"v_mul_f32_dpp v33, v158, v33 row_newbcast:1 row_mask:0xf bank_mask:0xf\n"
"v_mul_f32_dpp v34, v158, v34 row_newbcast:2 row_mask:0xf bank_mask:0xf\n"
"v_mul_f32_dpp v35, v158, v35 row_newbcast:3 row_mask:0xf bank_mask:0xf\n"
"v_mul_f32_dpp v36, v158, v36 row_newbcast:0 row_mask:0xf bank_mask:0xf\n"
"v_mul_f32_dpp v37, v158, v37 row_newbcast:1 row_mask:0xf bank_mask:0xf\n"
"v_mul_f32_dpp v38, v158, v38 row_newbcast:2 row_mask:0xf bank_mask:0xf\n"
"v_mul_f32_dpp v39, v158, v39 row_newbcast:3 row_mask:0xf bank_mask:0xf\n"
"v_mul_f32_dpp v40, v158, v40 row_newbcast:0 row_mask:0xf bank_mask:0xf\n"
"v_mul_f32_dpp v41, v158, v41 row_newbcast:1 row_mask:0xf bank_mask:0xf\n"
"v_mul_f32_dpp v42, v158, v42 row_newbcast:2 row_mask:0xf bank_mask:0xf\n"
"v_mul_f32_dpp v43, v158, v43 row_newbcast:3 row_mask:0xf bank_mask:0xf\n"
"v_mul_f32_dpp v44, v158, v44 row_newbcast:0 row_mask:0xf bank_mask:0xf\n"
"v_mul_f32_dpp v45, v158, v45 row_newbcast:1 row_mask:0xf bank_mask:0xf\n"
"v_mul_f32_dpp v46, v158, v46 row_newbcast:2 row_mask:0xf bank_mask:0xf\n"
"v_mul_f32_dpp v47, v158, v47 row_newbcast:3 row_mask:0xf bank_mask:0xf\n"
"v_mul_f32_dpp v48, v158, v48 row_newbcast:0 row_mask:0xf bank_mask:0xf\n"
"v_mul_f32_dpp v49, v158, v49 row_newbcast:1 row_mask:0xf bank_mask:0xf\n"
"v_mul_f32_dpp v50, v158, v50 row_newbcast:2 row_mask:0xf bank_mask:0xf\n"
"v_mul_f32_dpp v51, v158, v51 row_newbcast:3 row_mask:0xf bank_mask:0xf\n"
"v_mul_f32_dpp v52, v158, v52 row_newbcast:0 row_mask:0xf bank_mask:0xf\n"
"v_mul_f32_dpp v53, v158, v53 row_newbcast:1 row_mask:0xf bank_mask:0xf\n"
"v_mul_f32_dpp v54, v158, v54 row_newbcast:2 row_mask:0xf bank_mask:0xf\n"
"v_mul_f32_dpp v55, v158, v55 row_newbcast:3 row_mask:0xf bank_mask:0xf\n"
"v_mul_f32_dpp v56, v158, v56 row_newbcast:0 row_mask:0xf bank_mask:0xf\n"
"v_mul_f32_dpp v57, v158, v57 row_newbcast:1 row_mask:0xf bank_mask:0xf\n"
"v_mul_f32_dpp v58, v158, v58 row_newbcast:2 row_mask:0xf bank_mask:0xf\n"
"v_mul_f32_dpp v59, v158, v59 row_newbcast:3 row_mask:0xf bank_mask:0xf\n"
"v_mul_f32_dpp v60, v158, v60 row_newbcast:0 row_mask:0xf bank_mask:0xf\n"
"v_mul_f32_dpp v61, v158, v61 row_newbcast:1 row_mask:0xf bank_mask:0xf\n"
"v_mul_f32_dpp v62, v158, v62 row_newbcast:2 row_mask:0xf bank_mask:0xf\n"
"v_mul_f32_dpp v63, v158, v63 row_newbcast:3 row_mask:0xf bank_mask:0xf\n"
"v_max3_f32 v161, v112, v113, v114\n"
"v_max3_f32 v161, v161, v115, v116\n"
"v_max3_f32 v161, v161, v117, v118\n"
"v_max3_f32 v161, v161, v119, v160\n"
"v_mov_b32 v162, v161\n"
"s_nop 1\n"
"v_permlane32_swap_b32 v161, v162\n"
"v_max_f32 v161, v161, v162\n"
"v_mov_b32 v162, v161\n"
"s_nop 1\n"
"v_permlane16_swap_b32 v161, v162\n"
"v_max_f32 v161, v161, v162\n"
"v_sub_f32 v162, v160, v161\n"
"v_exp_f32 v162, v162\n"
"v_mov_b32 v160, v161\n"
"v_sub_f32 v112, v112, v160\n"
"v_sub_f32 v113, v113, v160\n"
"v_sub_f32 v114, v114, v160\n"
"v_sub_f32 v115, v115, v160\n"
"v_sub_f32 v116, v116, v160\n"
"v_sub_f32 v117, v117, v160\n"
"v_sub_f32 v118, v118, v160\n"
"v_sub_f32 v119, v119, v160\n"
"v_exp_f32 v112, v112\n"
"v_exp_f32 v113, v113\n"
"v_exp_f32 v114, v114\n"
"v_exp_f32 v115, v115\n"
"v_exp_f32 v116, v116\n"
"v_exp_f32 v117, v117\n"
"v_exp_f32 v118, v118\n"
"v_exp_f32 v119, v119\n"
"v_mul_f32 v163, v163, v162\n"
"v_add_f32 v163, v163, v112\n"
"v_add_f32 v163, v163, v113\n"
"v_add_f32 v163, v163, v114\n"
"v_add_f32 v163, v163, v115\n"
"v_add_f32 v163, v163, v116\n"
"v_add_f32 v163, v163, v117\n"
"v_add_f32 v163, v163, v118\n"
"v_add_f32 v163, v163, v119\n"
"v_cvt_pk_bf16_f32 v112, v112, v113\n"
"v_cvt_pk_bf16_f32 v113, v114, v115\n"
"v_cvt_pk_bf16_f32 v114, v116, v117\n"
"v_cvt_pk_bf16_f32 v115, v118, v119\n"
"v_mov_b32_dpp v162, v162 row_ror:12 row_mask:0x2 bank_mask:0xf\n"
"s_nop 1\n"
"v_mov_b32_dpp v162, v162 row_ror:8 row_mask:0x4 bank_mask:0xf\n"
"s_nop 1\n"
"v_mov_b32_dpp v162, v162 row_ror:4 row_mask:0x8 bank_mask:0xf\n"
"s_nop 1\n"
"v_mul_f32_dpp v64, v162, v64 row_newbcast:0 row_mask:0xf bank_mask:0xf\n"
"v_mul_f32_dpp v65, v162, v65 row_newbcast:1 row_mask:0xf bank_mask:0xf\n"
"v_mul_f32_dpp v66, v162, v66 row_newbcast:2 row_mask:0xf bank_mask:0xf\n"
"v_mul_f32_dpp v67, v162, v67 row_newbcast:3 row_mask:0xf bank_mask:0xf\n"
"v_mul_f32_dpp v68, v162, v68 row_newbcast:0 row_mask:0xf bank_mask:0xf\n"
"v_mul_f32_dpp v69, v162, v69 row_newbcast:1 row_mask:0xf bank_mask:0xf\n"
"v_mul_f32_dpp v70, v162, v70 row_newbcast:2 row_mask:0xf bank_mask:0xf\n"
"v_mul_f32_dpp v71, v162, v71 row_newbcast:3 row_mask:0xf bank_mask:0xf\n"
"v_mul_f32_dpp v72, v162, v72 row_newbcast:0 row_mask:0xf bank_mask:0xf\n"
"v_mul_f32_dpp v73, v162, v73 row_newbcast:1 row_mask:0xf bank_mask:0xf\n"
"v_mul_f32_dpp v74, v162, v74 row_newbcast:2 row_mask:0xf bank_mask:0xf\n"
"v_mul_f32_dpp v75, v162, v75 row_newbcast:3 row_mask:0xf bank_mask:0xf\n"
"v_mul_f32_dpp v76, v162, v76 row_newbcast:0 row_mask:0xf bank_mask:0xf\n"
"v_mul_f32_dpp v77, v162, v77 row_newbcast:1 row_mask:0xf bank_mask:0xf\n"
"v_mul_f32_dpp v78, v162, v78 row_newbcast:2 row_mask:0xf bank_mask:0xf\n"
"v_mul_f32_dpp v79, v162, v79 row_newbcast:3 row_mask:0xf bank_mask:0xf\n"
"v_mul_f32_dpp v80, v162, v80 row_newbcast:0 row_mask:0xf bank_mask:0xf\n"
"v_mul_f32_dpp v81, v162, v81 row_newbcast:1 row_mask:0xf bank_mask:0xf\n"
"v_mul_f32_dpp v82, v162, v82 row_newbcast:2 row_mask:0xf bank_mask:0xf\n"
"v_mul_f32_dpp v83, v162, v83 row_newbcast:3 row_mask:0xf bank_mask:0xf\n"
"v_mul_f32_dpp v84, v162, v84 row_newbcast:0 row_mask:0xf bank_mask:0xf\n"
"v_mul_f32_dpp v85, v162, v85 row_newbcast:1 row_mask:0xf bank_mask:0xf\n"
"v_mul_f32_dpp v86, v162, v86 row_newbcast:2 row_mask:0xf bank_mask:0xf\n"
"v_mul_f32_dpp v87, v162, v87 row_newbcast:3 row_mask:0xf bank_mask:0xf\n"
"v_mul_f32_dpp v88, v162, v88 row_newbcast:0 row_mask:0xf bank_mask:0xf\n"
"v_mul_f32_dpp v89, v162, v89 row_newbcast:1 row_mask:0xf bank_mask:0xf\n"
"v_mul_f32_dpp v90, v162, v90 row_newbcast:2 row_mask:0xf bank_mask:0xf\n"
"v_mul_f32_dpp v91, v162, v91 row_newbcast:3 row_mask:0xf bank_mask:0xf\n"
"v_mul_f32_dpp v92, v162, v92 row_newbcast:0 row_mask:0xf bank_mask:0xf\n"
"v_mul_f32_dpp v93, v162, v93 row_newbcast:1 row_mask:0xf bank_mask:0xf\n"
"v_mul_f32_dpp v94, v162, v94 row_newbcast:2 row_mask:0xf bank_mask:0xf\n"
"v_mul_f32_dpp v95, v162, v95 row_newbcast:3 row_mask:0xf bank_mask:0xf\n"
"s_cmp_lg_u32 s83, s43\n"
"s_cbranch_scc1 LSK3A_%=\n"
"v_max3_f32 v165, v120, v121, v122\n"
"v_max3_f32 v165, v165, v123, v124\n"
"v_max3_f32 v165, v165, v125, v126\n"
"v_max3_f32 v165, v165, v127, v164\n"
"v_mov_b32 v166, v165\n"
"s_nop 1\n"
"v_permlane32_swap_b32 v165, v166\n"
"v_max_f32 v165, v165, v166\n"
"v_mov_b32 v166, v165\n"
"s_nop 1\n"
"v_permlane16_swap_b32 v165, v166\n"
"v_max_f32 v165, v165, v166\n"
"v_sub_f32 v166, v164, v165\n"
"v_exp_f32 v166, v166\n"
"v_mov_b32 v164, v165\n"
"v_sub_f32 v120, v120, v164\n"
"v_sub_f32 v121, v121, v164\n"
"v_sub_f32 v122, v122, v164\n"
"v_sub_f32 v123, v123, v164\n"
"v_sub_f32 v124, v124, v164\n"
"v_sub_f32 v125, v125, v164\n"
"v_sub_f32 v126, v126, v164\n"
"v_sub_f32 v127, v127, v164\n"
"v_exp_f32 v120, v120\n"
"v_exp_f32 v121, v121\n"
"v_exp_f32 v122, v122\n"
"v_exp_f32 v123, v123\n"
"v_exp_f32 v124, v124\n"
"v_exp_f32 v125, v125\n"
"v_exp_f32 v126, v126\n"
"v_exp_f32 v127, v127\n"
"v_mul_f32 v167, v167, v166\n"
"v_add_f32 v167, v167, v120\n"
"v_add_f32 v167, v167, v121\n"
"v_add_f32 v167, v167, v122\n"
"v_add_f32 v167, v167, v123\n"
"v_add_f32 v167, v167, v124\n"
"v_add_f32 v167, v167, v125\n"
"v_add_f32 v167, v167, v126\n"
"v_add_f32 v167, v167, v127\n"
"v_cvt_pk_bf16_f32 v120, v120, v121\n"
"v_cvt_pk_bf16_f32 v121, v122, v123\n"
"v_cvt_pk_bf16_f32 v122, v124, v125\n"
"v_cvt_pk_bf16_f32 v123, v126, v127\n"
"v_mov_b32_dpp v166, v166 row_ror:12 row_mask:0x2 bank_mask:0xf\n"
"s_nop 1\n"
"v_mov_b32_dpp v166, v166 row_ror:8 row_mask:0x4 bank_mask:0xf\n"
"s_nop 1\n"
"v_mov_b32_dpp v166, v166 row_ror:4 row_mask:0x8 bank_mask:0xf\n"
"v_accvgpr_read_b32 v124, a208\n"
"s_nop 1\n"
"v_mul_f32_dpp v124, v166, v124 row_newbcast:0 row_mask:0xf bank_mask:0xf\n"
"v_accvgpr_write_b32 a208, v124\n"
"v_accvgpr_read_b32 v125, a209\n"
"s_nop 1\n"
"v_mul_f32_dpp v125, v166, v125 row_newbcast:1 row_mask:0xf bank_mask:0xf\n"
"v_accvgpr_write_b32 a209, v125\n"
"v_accvgpr_read_b32 v126, a210\n"
"s_nop 1\n"
"v_mul_f32_dpp v126, v166, v126 row_newbcast:2 row_mask:0xf bank_mask:0xf\n"
"v_accvgpr_write_b32 a210, v126\n"
"v_accvgpr_read_b32 v127, a211\n"
"s_nop 1\n"
"v_mul_f32_dpp v127, v166, v127 row_newbcast:3 row_mask:0xf bank_mask:0xf\n"
"v_accvgpr_write_b32 a211, v127\n"
"v_accvgpr_read_b32 v124, a212\n"
"s_nop 1\n"
"v_mul_f32_dpp v124, v166, v124 row_newbcast:0 row_mask:0xf bank_mask:0xf\n"
"v_accvgpr_write_b32 a212, v124\n"
"v_accvgpr_read_b32 v125, a213\n"
"s_nop 1\n"
"v_mul_f32_dpp v125, v166, v125 row_newbcast:1 row_mask:0xf bank_mask:0xf\n"
"v_accvgpr_write_b32 a213, v125\n"
"v_accvgpr_read_b32 v126, a214\n"
"s_nop 1\n"
"v_mul_f32_dpp v126, v166, v126 row_newbcast:2 row_mask:0xf bank_mask:0xf\n"
"v_accvgpr_write_b32 a214, v126\n"
"v_accvgpr_read_b32 v127, a215\n"
"s_nop 1\n"
"v_mul_f32_dpp v127, v166, v127 row_newbcast:3 row_mask:0xf bank_mask:0xf\n"
"v_accvgpr_write_b32 a215, v127\n"
"v_accvgpr_read_b32 v124, a216\n"
"s_nop 1\n"
"v_mul_f32_dpp v124, v166, v124 row_newbcast:0 row_mask:0xf bank_mask:0xf\n"
"v_accvgpr_write_b32 a216, v124\n"
"v_accvgpr_read_b32 v125, a217\n"
"s_nop 1\n"
"v_mul_f32_dpp v125, v166, v125 row_newbcast:1 row_mask:0xf bank_mask:0xf\n"
"v_accvgpr_write_b32 a217, v125\n"
"v_accvgpr_read_b32 v126, a218\n"
"s_nop 1\n"
"v_mul_f32_dpp v126, v166, v126 row_newbcast:2 row_mask:0xf bank_mask:0xf\n"
"v_accvgpr_write_b32 a218, v126\n"
"v_accvgpr_read_b32 v127, a219\n"
"s_nop 1\n"
"v_mul_f32_dpp v127, v166, v127 row_newbcast:3 row_mask:0xf bank_mask:0xf\n"
"v_accvgpr_write_b32 a219, v127\n"
"v_accvgpr_read_b32 v124, a220\n"
"s_nop 1\n"
"v_mul_f32_dpp v124, v166, v124 row_newbcast:0 row_mask:0xf bank_mask:0xf\n"
"v_accvgpr_write_b32 a220, v124\n"
"v_accvgpr_read_b32 v125, a221\n"
"s_nop 1\n"
"v_mul_f32_dpp v125, v166, v125 row_newbcast:1 row_mask:0xf bank_mask:0xf\n"
"v_accvgpr_write_b32 a221, v125\n"
"v_accvgpr_read_b32 v126, a222\n"
"s_nop 1\n"
"v_mul_f32_dpp v126, v166, v126 row_newbcast:2 row_mask:0xf bank_mask:0xf\n"
"v_accvgpr_write_b32 a222, v126\n"
"v_accvgpr_read_b32 v127, a223\n"
"s_nop 1\n"
"v_mul_f32_dpp v127, v166, v127 row_newbcast:3 row_mask:0xf bank_mask:0xf\n"
"v_accvgpr_write_b32 a223, v127\n"
"v_accvgpr_read_b32 v124, a224\n"
"s_nop 1\n"
"v_mul_f32_dpp v124, v166, v124 row_newbcast:0 row_mask:0xf bank_mask:0xf\n"
"v_accvgpr_write_b32 a224, v124\n"
"v_accvgpr_read_b32 v125, a225\n"
"s_nop 1\n"
"v_mul_f32_dpp v125, v166, v125 row_newbcast:1 row_mask:0xf bank_mask:0xf\n"
"v_accvgpr_write_b32 a225, v125\n"
"v_accvgpr_read_b32 v126, a226\n"
"s_nop 1\n"
"v_mul_f32_dpp v126, v166, v126 row_newbcast:2 row_mask:0xf bank_mask:0xf\n"
"v_accvgpr_write_b32 a226, v126\n"
"v_accvgpr_read_b32 v127, a227\n"
"s_nop 1\n"
"v_mul_f32_dpp v127, v166, v127 row_newbcast:3 row_mask:0xf bank_mask:0xf\n"
"v_accvgpr_write_b32 a227, v127\n"
"v_accvgpr_read_b32 v124, a228\n"
"s_nop 1\n"
"v_mul_f32_dpp v124, v166, v124 row_newbcast:0 row_mask:0xf bank_mask:0xf\n"
"v_accvgpr_write_b32 a228, v124\n"
"v_accvgpr_read_b32 v125, a229\n"
"s_nop 1\n"
"v_mul_f32_dpp v125, v166, v125 row_newbcast:1 row_mask:0xf bank_mask:0xf\n"
"v_accvgpr_write_b32 a229, v125\n"
"v_accvgpr_read_b32 v126, a230\n"
"s_nop 1\n"
"v_mul_f32_dpp v126, v166, v126 row_newbcast:2 row_mask:0xf bank_mask:0xf\n"
"v_accvgpr_write_b32 a230, v126\n"
"v_accvgpr_read_b32 v127, a231\n"
"s_nop 1\n"
"v_mul_f32_dpp v127, v166, v127 row_newbcast:3 row_mask:0xf bank_mask:0xf\n"
"v_accvgpr_write_b32 a231, v127\n"
"v_accvgpr_read_b32 v124, a232\n"
"s_nop 1\n"
"v_mul_f32_dpp v124, v166, v124 row_newbcast:0 row_mask:0xf bank_mask:0xf\n"
"v_accvgpr_write_b32 a232, v124\n"
"v_accvgpr_read_b32 v125, a233\n"
"s_nop 1\n"
"v_mul_f32_dpp v125, v166, v125 row_newbcast:1 row_mask:0xf bank_mask:0xf\n"
"v_accvgpr_write_b32 a233, v125\n"
"v_accvgpr_read_b32 v126, a234\n"
"s_nop 1\n"
"v_mul_f32_dpp v126, v166, v126 row_newbcast:2 row_mask:0xf bank_mask:0xf\n"
"v_accvgpr_write_b32 a234, v126\n"
"v_accvgpr_read_b32 v127, a235\n"
"s_nop 1\n"
"v_mul_f32_dpp v127, v166, v127 row_newbcast:3 row_mask:0xf bank_mask:0xf\n"
"v_accvgpr_write_b32 a235, v127\n"
"v_accvgpr_read_b32 v124, a236\n"
"s_nop 1\n"
"v_mul_f32_dpp v124, v166, v124 row_newbcast:0 row_mask:0xf bank_mask:0xf\n"
"v_accvgpr_write_b32 a236, v124\n"
"v_accvgpr_read_b32 v125, a237\n"
"s_nop 1\n"
"v_mul_f32_dpp v125, v166, v125 row_newbcast:1 row_mask:0xf bank_mask:0xf\n"
"v_accvgpr_write_b32 a237, v125\n"
"v_accvgpr_read_b32 v126, a238\n"
"s_nop 1\n"
"v_mul_f32_dpp v126, v166, v126 row_newbcast:2 row_mask:0xf bank_mask:0xf\n"
"v_accvgpr_write_b32 a238, v126\n"
"v_accvgpr_read_b32 v127, a239\n"
"s_nop 1\n"
"v_mul_f32_dpp v127, v166, v127 row_newbcast:3 row_mask:0xf bank_mask:0xf\n"
"v_accvgpr_write_b32 a239, v127\n"
"LSK3A_%=:\n"
"s_nop 3\n"
"buffer_load_ushort v128, v146, s[88:91], s80 offen offset:0\n"
"buffer_load_ushort v129, v146, s[88:91], s80 offen offset:256\n"
"buffer_load_ushort v130, v146, s[88:91], s80 offen offset:512\n"
"buffer_load_ushort v131, v146, s[88:91], s80 offen offset:768\n"
"buffer_load_ushort v132, v146, s[88:91], s81 offen offset:0\n"
"buffer_load_ushort v133, v146, s[88:91], s81 offen offset:256\n"
"buffer_load_ushort v134, v146, s[88:91], s81 offen offset:512\n"
"buffer_load_ushort v135, v146, s[88:91], s81 offen offset:768\n"
"buffer_load_ushort v136, v146, s[88:91], s80 offen offset:32\n"
"buffer_load_ushort v137, v146, s[88:91], s80 offen offset:288\n"
"buffer_load_ushort v138, v146, s[88:91], s80 offen offset:544\n"
"buffer_load_ushort v139, v146, s[88:91], s80 offen offset:800\n"
"buffer_load_ushort v140, v146, s[88:91], s81 offen offset:32\n"
"buffer_load_ushort v141, v146, s[88:91], s81 offen offset:288\n"
"buffer_load_ushort v142, v146, s[88:91], s81 offen offset:544\n"
"buffer_load_ushort v143, v146, s[88:91], s81 offen offset:800\n"
"s_waitcnt vmcnt(0)\n"
"v_lshl_or_b32 v128, v129, 16, v128\n"
"v_lshl_or_b32 v129, v131, 16, v130\n"
"v_lshl_or_b32 v130, v133, 16, v132\n"
"v_lshl_or_b32 v131, v135, 16, v134\n"
"v_lshl_or_b32 v136, v137, 16, v136\n"
"v_lshl_or_b32 v137, v139, 16, v138\n"
"v_lshl_or_b32 v138, v141, 16, v140\n"
"v_lshl_or_b32 v139, v143, 16, v142\n"
"v_mfma_f32_16x16x32_bf16 v[0:3], v[96:99], v[128:131], v[0:3]\n"
"s_nop 0\n"
"v_mfma_f32_16x16x32_bf16 v[4:7], v[96:99], v[136:139], v[4:7]\n"
"v_mfma_f32_16x16x32_bf16 v[32:35], v[104:107], v[128:131], v[32:35]\n"
"v_mfma_f32_16x16x32_bf16 v[36:39], v[104:107], v[136:139], v[36:39]\n"
"v_mfma_f32_16x16x32_bf16 v[64:67], v[112:115], v[128:131], v[64:67]\n"
"v_mfma_f32_16x16x32_bf16 v[68:71], v[112:115], v[136:139], v[68:71]\n"
"s_cmp_lg_u32 s83, s43\n"
"s_cbranch_scc1 LSK3B0_%=\n"
"v_mfma_f32_16x16x32_bf16 a[208:211], v[120:123], v[128:131], a[208:211]\n"
"v_mfma_f32_16x16x32_bf16 a[212:215], v[120:123], v[136:139], a[212:215]\n"
"LSK3B0_%=:\n"
"s_nop 3\n"
"buffer_load_ushort v128, v147, s[88:91], s80 offen offset:0\n"
"buffer_load_ushort v129, v147, s[88:91], s80 offen offset:256\n"
"buffer_load_ushort v130, v147, s[88:91], s80 offen offset:512\n"
"buffer_load_ushort v131, v147, s[88:91], s80 offen offset:768\n"
"buffer_load_ushort v132, v147, s[88:91], s81 offen offset:0\n"
"buffer_load_ushort v133, v147, s[88:91], s81 offen offset:256\n"
"buffer_load_ushort v134, v147, s[88:91], s81 offen offset:512\n"
"buffer_load_ushort v135, v147, s[88:91], s81 offen offset:768\n"
"buffer_load_ushort v136, v147, s[88:91], s80 offen offset:32\n"
"buffer_load_ushort v137, v147, s[88:91], s80 offen offset:288\n"
"buffer_load_ushort v138, v147, s[88:91], s80 offen offset:544\n"
"buffer_load_ushort v139, v147, s[88:91], s80 offen offset:800\n"
"buffer_load_ushort v140, v147, s[88:91], s81 offen offset:32\n"
"buffer_load_ushort v141, v147, s[88:91], s81 offen offset:288\n"
"buffer_load_ushort v142, v147, s[88:91], s81 offen offset:544\n"
"buffer_load_ushort v143, v147, s[88:91], s81 offen offset:800\n"
"s_waitcnt vmcnt(0)\n"
"v_lshl_or_b32 v128, v129, 16, v128\n"
"v_lshl_or_b32 v129, v131, 16, v130\n"
"v_lshl_or_b32 v130, v133, 16, v132\n"
"v_lshl_or_b32 v131, v135, 16, v134\n"
"v_lshl_or_b32 v136, v137, 16, v136\n"
"v_lshl_or_b32 v137, v139, 16, v138\n"
"v_lshl_or_b32 v138, v141, 16, v140\n"
"v_lshl_or_b32 v139, v143, 16, v142\n"
"v_mfma_f32_16x16x32_bf16 v[8:11], v[96:99], v[128:131], v[8:11]\n"
"s_nop 0\n"
"v_mfma_f32_16x16x32_bf16 v[12:15], v[96:99], v[136:139], v[12:15]\n"
"v_mfma_f32_16x16x32_bf16 v[40:43], v[104:107], v[128:131], v[40:43]\n"
"v_mfma_f32_16x16x32_bf16 v[44:47], v[104:107], v[136:139], v[44:47]\n"
"v_mfma_f32_16x16x32_bf16 v[72:75], v[112:115], v[128:131], v[72:75]\n"
"v_mfma_f32_16x16x32_bf16 v[76:79], v[112:115], v[136:139], v[76:79]\n"
"s_cmp_lg_u32 s83, s43\n"
"s_cbranch_scc1 LSK3B1_%=\n"
"v_mfma_f32_16x16x32_bf16 a[216:219], v[120:123], v[128:131], a[216:219]\n"
"v_mfma_f32_16x16x32_bf16 a[220:223], v[120:123], v[136:139], a[220:223]\n"
"LSK3B1_%=:\n"
"s_nop 3\n"
"buffer_load_ushort v128, v146, s[88:91], s80 offen offset:128\n"
"buffer_load_ushort v129, v146, s[88:91], s80 offen offset:384\n"
"buffer_load_ushort v130, v146, s[88:91], s80 offen offset:640\n"
"buffer_load_ushort v131, v146, s[88:91], s80 offen offset:896\n"
"buffer_load_ushort v132, v146, s[88:91], s81 offen offset:128\n"
"buffer_load_ushort v133, v146, s[88:91], s81 offen offset:384\n"
"buffer_load_ushort v134, v146, s[88:91], s81 offen offset:640\n"
"buffer_load_ushort v135, v146, s[88:91], s81 offen offset:896\n"
"buffer_load_ushort v136, v146, s[88:91], s80 offen offset:160\n"
"buffer_load_ushort v137, v146, s[88:91], s80 offen offset:416\n"
"buffer_load_ushort v138, v146, s[88:91], s80 offen offset:672\n"
"buffer_load_ushort v139, v146, s[88:91], s80 offen offset:928\n"
"buffer_load_ushort v140, v146, s[88:91], s81 offen offset:160\n"
"buffer_load_ushort v141, v146, s[88:91], s81 offen offset:416\n"
"buffer_load_ushort v142, v146, s[88:91], s81 offen offset:672\n"
"buffer_load_ushort v143, v146, s[88:91], s81 offen offset:928\n"
"s_waitcnt vmcnt(0)\n"
"v_lshl_or_b32 v128, v129, 16, v128\n"
"v_lshl_or_b32 v129, v131, 16, v130\n"
"v_lshl_or_b32 v130, v133, 16, v132\n"
"v_lshl_or_b32 v131, v135, 16, v134\n"
"v_lshl_or_b32 v136, v137, 16, v136\n"
"v_lshl_or_b32 v137, v139, 16, v138\n"
"v_lshl_or_b32 v138, v141, 16, v140\n"
"v_lshl_or_b32 v139, v143, 16, v142\n"
"v_mfma_f32_16x16x32_bf16 v[16:19], v[96:99], v[128:131], v[16:19]\n"
"s_nop 0\n"
"v_mfma_f32_16x16x32_bf16 v[20:23], v[96:99], v[136:139], v[20:23]\n"
"v_mfma_f32_16x16x32_bf16 v[48:51], v[104:107], v[128:131], v[48:51]\n"
"v_mfma_f32_16x16x32_bf16 v[52:55], v[104:107], v[136:139], v[52:55]\n"
"v_mfma_f32_16x16x32_bf16 v[80:83], v[112:115], v[128:131], v[80:83]\n"
"v_mfma_f32_16x16x32_bf16 v[84:87], v[112:115], v[136:139], v[84:87]\n"
"s_cmp_lg_u32 s83, s43\n"
"s_cbranch_scc1 LSK3B2_%=\n"
"v_mfma_f32_16x16x32_bf16 a[224:227], v[120:123], v[128:131], a[224:227]\n"
"v_mfma_f32_16x16x32_bf16 a[228:231], v[120:123], v[136:139], a[228:231]\n"
"LSK3B2_%=:\n"
"s_nop 3\n"
"buffer_load_ushort v128, v147, s[88:91], s80 offen offset:128\n"
"buffer_load_ushort v129, v147, s[88:91], s80 offen offset:384\n"
"buffer_load_ushort v130, v147, s[88:91], s80 offen offset:640\n"
"buffer_load_ushort v131, v147, s[88:91], s80 offen offset:896\n"
"buffer_load_ushort v132, v147, s[88:91], s81 offen offset:128\n"
"buffer_load_ushort v133, v147, s[88:91], s81 offen offset:384\n"
"buffer_load_ushort v134, v147, s[88:91], s81 offen offset:640\n"
"buffer_load_ushort v135, v147, s[88:91], s81 offen offset:896\n"
"buffer_load_ushort v136, v147, s[88:91], s80 offen offset:160\n"
"buffer_load_ushort v137, v147, s[88:91], s80 offen offset:416\n"
"buffer_load_ushort v138, v147, s[88:91], s80 offen offset:672\n"
"buffer_load_ushort v139, v147, s[88:91], s80 offen offset:928\n"
"buffer_load_ushort v140, v147, s[88:91], s81 offen offset:160\n"
"buffer_load_ushort v141, v147, s[88:91], s81 offen offset:416\n"
"buffer_load_ushort v142, v147, s[88:91], s81 offen offset:672\n"
"buffer_load_ushort v143, v147, s[88:91], s81 offen offset:928\n"
"s_waitcnt vmcnt(0)\n"
"v_lshl_or_b32 v128, v129, 16, v128\n"
"v_lshl_or_b32 v129, v131, 16, v130\n"
"v_lshl_or_b32 v130, v133, 16, v132\n"
"v_lshl_or_b32 v131, v135, 16, v134\n"
"v_lshl_or_b32 v136, v137, 16, v136\n"
"v_lshl_or_b32 v137, v139, 16, v138\n"
"v_lshl_or_b32 v138, v141, 16, v140\n"
"v_lshl_or_b32 v139, v143, 16, v142\n"
"v_mfma_f32_16x16x32_bf16 v[24:27], v[96:99], v[128:131], v[24:27]\n"
"s_nop 0\n"
"v_mfma_f32_16x16x32_bf16 v[28:31], v[96:99], v[136:139], v[28:31]\n"
"v_mfma_f32_16x16x32_bf16 v[56:59], v[104:107], v[128:131], v[56:59]\n"
"v_mfma_f32_16x16x32_bf16 v[60:63], v[104:107], v[136:139], v[60:63]\n"
"v_mfma_f32_16x16x32_bf16 v[88:91], v[112:115], v[128:131], v[88:91]\n"
"v_mfma_f32_16x16x32_bf16 v[92:95], v[112:115], v[136:139], v[92:95]\n"
"s_cmp_lg_u32 s83, s43\n"
"s_cbranch_scc1 LSK3B3_%=\n"
"v_mfma_f32_16x16x32_bf16 a[232:235], v[120:123], v[128:131], a[232:235]\n"
"v_mfma_f32_16x16x32_bf16 a[236:239], v[120:123], v[136:139], a[236:239]\n"
"LSK3B3_%=:\n"
"s_nop 3\n"
"s_nop 9\n"
"s_add_u32 s80, s80, 8192\n"
"s_add_u32 s81, s81, 8192\n"
"s_add_u32 s82, s82, 1\n"
"s_cmp_lt_u32 s82, 32\n"
"s_cbranch_scc1 LSLOOP_%=\n"
"v_mov_b32 v154, v155\n"
"s_nop 1\n"
"v_permlane32_swap_b32 v155, v154\n"
"v_add_f32 v155, v155, v154\n"
"v_mov_b32 v154, v155\n"
"s_nop 1\n"
"v_permlane16_swap_b32 v155, v154\n"
"v_add_f32 v155, v155, v154\n"
"v_mov_b32 v196, v155\n"
"v_mov_b32 v158, v159\n"
"s_nop 1\n"
"v_permlane32_swap_b32 v159, v158\n"
"v_add_f32 v159, v159, v158\n"
"v_mov_b32 v158, v159\n"
"s_nop 1\n"
"v_permlane16_swap_b32 v159, v158\n"
"v_add_f32 v159, v159, v158\n"
"v_mov_b32 v198, v159\n"
"v_mov_b32 v162, v163\n"
"s_nop 1\n"
"v_permlane32_swap_b32 v163, v162\n"
"v_add_f32 v163, v163, v162\n"
"v_mov_b32 v162, v163\n"
"s_nop 1\n"
"v_permlane16_swap_b32 v163, v162\n"
"v_add_f32 v163, v163, v162\n"
"v_mov_b32 v200, v163\n"
"v_mov_b32 v166, v167\n"
"s_nop 1\n"
"v_permlane32_swap_b32 v167, v166\n"
"v_add_f32 v167, v167, v166\n"
"v_mov_b32 v166, v167\n"
"s_nop 1\n"
"v_permlane16_swap_b32 v167, v166\n"
"v_add_f32 v167, v167, v166\n"
"v_mov_b32 v202, v167\n"
"v_sub_f32 v192, 0, v164\n"
"v_mov_b32 v96, 0\n"
"v_mov_b32 v97, 0\n"
"v_mov_b32 v98, 0\n"
"v_mov_b32 v99, 0\n"
"v_mov_b32 v100, 0\n"
"v_mov_b32 v101, 0\n"
"v_mov_b32 v102, 0\n"
"v_mov_b32 v103, 0\n"
"v_mov_b32 v104, 0\n"
"v_mov_b32 v105, 0\n"
"v_mov_b32 v106, 0\n"
"v_mov_b32 v107, 0\n"
"v_mov_b32 v108, 0\n"
"v_mov_b32 v109, 0\n"
"v_mov_b32 v110, 0\n"
"v_mov_b32 v111, 0\n"
"v_mov_b32 v112, 0\n"
"v_mov_b32 v113, 0\n"
"v_mov_b32 v114, 0\n"
"v_mov_b32 v115, 0\n"
"v_mov_b32 v116, 0\n"
"v_mov_b32 v117, 0\n"
"v_mov_b32 v118, 0\n"
"v_mov_b32 v119, 0\n"
"v_mov_b32 v120, 0\n"
"v_mov_b32 v121, 0\n"
"v_mov_b32 v122, 0\n"
"v_mov_b32 v123, 0\n"
"v_mov_b32 v204, 0\n"
"v_mov_b32 v205, 0\n"
"s_nop 3\n"
"s_cmp_eq_u32 s47, 4\n"
"s_cbranch_scc1 LBACK0_%=\n"
"s_cmp_eq_u32 s47, 8\n"
"s_cbranch_scc1 LBACK1_%=\n"
"s_cmp_eq_u32 s47, 12\n"
"s_cbranch_scc1 LBACK2_%=\n"
"s_branch LBACK3_%=\n"
"LFIN_%=:\n"
    :: [kalo]"s"((unsigned)ka), [kahi]"s"((unsigned)(ka >> 32)), [wg]"s"(blockIdx.x), [wid]"s"(wid)
    : "v0","v1","v2","v3","v4","v5","v6","v7","v8","v9","v10","v11","v12","v13","v14","v15","v16","v17","v18","v19","v20","v21","v22","v23","v24","v25","v26","v27","v28","v29","v30","v31","v32","v33","v34","v35","v36","v37","v38","v39","v40","v41","v42","v43","v44","v45","v46","v47","v48","v49","v50","v51","v52","v53","v54","v55","v56","v57","v58","v59","v60","v61","v62","v63","v64","v65","v66","v67","v68","v69","v70","v71","v72","v73","v74","v75","v76","v77","v78","v79","v80","v81","v82","v83","v84","v85","v86","v87","v88","v89","v90","v91","v92","v93","v94","v95","v96","v97","v98","v99","v100","v101","v102","v103","v104","v105","v106","v107","v108","v109","v110","v111","v112","v113","v114","v115","v116","v117","v118","v119","v120","v121","v122","v123","v124","v125","v126","v127","v128","v129","v130","v131","v132","v133","v134","v135","v136","v137","v138","v139","v140","v141","v142","v143","v144","v145","v146","v147","v148","v149","v150","v151","v152","v153","v154","v155","v156","v157","v158","v159","v160","v161","v162","v163","v164","v165","v166","v167","v168","v169","v170","v171","v172","v173","v174","v175","v176","v177","v178","v179","v180","v181","v182","v183","v184","v185","v186","v187","v188","v189","v190","v191","v192","v193","v194","v195","v196","v197","v198","v199","v200","v201","v202","v203","v204","v205","v206","v207","v208","v209","v210","v211","v212","v213","v214","v215","v216","v217","v218","v219","v220","v221","v222","v223","v224","v225","v226","v227","v228","v229","v230","v231","v232","v233","v234","v235","v236","v237","v238","v239","v240","v241","v242","v243","v244","v245","v246","v247","v248","v249","v250","v251","v252","v253","v254","v255","a0","a1","a2","a3","a4","a5","a6","a7","a8","a9","a10","a11","a12","a13","a14","a15","a16","a17","a18","a19","a20","a21","a22","a23","a24","a25","a26","a27","a28","a29","a30","a31","a32","a33","a34","a35","a36","a37","a38","a39","a40","a41","a42","a43","a44","a45","a46","a47","a48","a49","a50","a51","a52","a53","a54","a55","a56","a57","a58","a59","a60","a61","a62","a63","a64","a65","a66","a67","a68","a69","a70","a71","a72","a73","a74","a75","a76","a77","a78","a79","a80","a81","a82","a83","a84","a85","a86","a87","a88","a89","a90","a91","a92","a93","a94","a95","a96","a97","a98","a99","a100","a101","a102","a103","a104","a105","a106","a107","a108","a109","a110","a111","a112","a113","a114","a115","a116","a117","a118","a119","a120","a121","a122","a123","a124","a125","a126","a127","a128","a129","a130","a131","a132","a133","a134","a135","a136","a137","a138","a139","a140","a141","a142","a143","a144","a145","a146","a147","a148","a149","a150","a151","a152","a153","a154","a155","a156","a157","a158","a159","a160","a161","a162","a163","a164","a165","a166","a167","a168","a169","a170","a171","a172","a173","a174","a175","a176","a177","a178","a179","a180","a181","a182","a183","a184","a185","a186","a187","a188","a189","a190","a191","a192","a193","a194","a195","a196","a197","a198","a199","a200","a201","a202","a203","a204","a205","a206","a207","a208","a209","a210","a211","a212","a213","a214","a215","a216","a217","a218","a219","a220","a221","a222","a223","a224","a225","a226","a227","a228","a229","a230","a231","a232","a233","a234","a235","a236","a237","a238","a239","a240","a241","a242","a243","a244","a245","a246","a247","a248","a249","a250","a251","a252","a253","a254","a255","s6","s7","s8","s9","s10","s11","s12","s13","s14","s15","s16","s17","s18","s19","s20","s21","s22","s23","s24","s25","s26","s27","s28","s29","s30","s31","s32","s33","s34","s35","s36","s37","s38","s39","s40","s41","s42","s43","s44","s45","s46","s47","s48","s49","s50","s51","s52","s53","s54","s55","s56","s57","s58","s59","s60","s61","s62","s63","s64","s65","s66","s67","s68","s69","s70","s71","s72","s73","s74","s75","s76","s77","s78","s79","s80","s81","s82","s83","s84","s85","s86","s87","s88","s89","s90","s91","s92","s93","s94","s95","s96","s97","s98","s99","s100","vcc","scc","m0","memory");
}
extern "C" void kernel_launch(void* const* d_in, const int* in_sizes, int n_in, void* d_out, int out_size, void* d_ws, size_t ws_size, hipStream_t stream) {
  char* xch = (char*)d_ws + ((ws_size - (size_t)75497472) & ~(size_t)0xfffff);
  (void)hipFuncSetAttribute((const void*)fused_attn, hipFuncAttributeMaxDynamicSharedMemorySize, 163840);
  hipLaunchKernelGGL(fused_attn, dim3(256), dim3(256), 163840, stream,
                     (const char*)d_in[0], (const char*)d_in[1], (const char*)d_in[2], (const char*)d_in[3], (const char*)d_in[4],
                     (const char*)d_in[5], (const char*)d_in[6], (const char*)d_in[7], (const char*)d_in[8], (const char*)d_in[9], xch, (char*)d_out, (char*)d_ws);
}
```

```cpp
#include <hip/hip_runtime.h>
#include <hip/hip_bf16.h>
#include <stdint.h>

__global__ __launch_bounds__(256, 1)
void fused_attn(const char* xA, const char* kA, const char* vA, const char* wA, const char* uA,
                const char* xB, const char* kB, const char* vB, const char* wB, const char* uB, char* xch, char* out, char* flags) {
  extern __shared__ __attribute__((aligned(16))) char lds[];
  const unsigned wid = __builtin_amdgcn_readfirstlane(threadIdx.x >> 6);
  const unsigned long long ka = (unsigned long long)__builtin_amdgcn_kernarg_segment_ptr();
  asm volatile(
"s_memtime s[64:65]\n"
"s_memrealtime s[66:67]\n"
"s_bfe_u32 s10, %[wg], 0x10002\n"
"s_mul_i32 s40, s10, 40\n"
"s_add_u32 s40, s40, %[kalo]\n"
"s_addc_u32 s41, %[kahi], 0\n"
"s_mov_b32 s78, %[kalo]\n"
"s_mov_b32 s79, %[kahi]\n"
"s_load_dwordx2 s[24:25], s[40:41], 0\n"
"s_load_dwordx2 s[28:29], s[40:41], 24\n"
"s_load_dwordx2 s[6:7], s[78:79], 0x60\n"
"s_load_dwordx2 s[16:17], s[40:41], 8\n"
"s_load_dwordx2 s[20:21], s[40:41], 16\n"
"s_load_dwordx2 s[8:9], s[40:41], 32\n"
"s_load_dwordx4 s[12:15], s[78:79], 0x50\n"
"s_mov_b32 s42, %[wid]\n"
"s_and_b32 s11, %[wg], 1\n"
"s_lshl_b32 s11, s11, 3\n"
"s_lshr_b32 s60, %[wg], 5\n"
"s_add_u32 s11, s11, s60\n"
"s_bfe_u32 s61, %[wg], 0x10001\n"
"s_bfe_u32 s60, %[wg], 0x20003\n"
"s_lshl2_add_u32 s61, s61, s60\n"
"s_getpc_b64 s[80:81]\n"
"LCP_%=:\n"
"s_mov_b32 s82, LEND_%=-LCP_%=\n"
"s_mov_b32 s83, 0x20000\n"
"v_mbcnt_lo_u32_b32 v2, -1, 0\n"
"v_lshlrev_b32 v2, 7, v2\n"
"s_lshr_b32 s60, %[wg], 3\n"
"s_lshl2_add_u32 s60, s60, s42\n"
"s_mul_i32 s60, s60, 640\n"
"s_mov_b64 exec, 31\n"
"buffer_load_dword v3, v2, s[80:83], s60 offen\n"
"s_mov_b64 exec, -1\n"
"s_mov_b32 s19, 0x20000\n"
"s_mov_b32 s23, 0x20000\n"
"s_mov_b32 s27, 0x20000\n"
"s_mov_b32 s31, 0x20000\n"
"s_mov_b32 s35, 0x20000\n"
"s_mov_b32 s39, 0x20000\n"
"s_mov_b32 s26, 286720\n"
"s_lshl_b32 s44, s42, 12\n"
"s_mov_b32 s50, s44\n"
"s_mul_i32 s96, s42, 7168\n"
"s_add_u32 s51, s96, 0x10000\n"
"s_mov_b32 s74, 0x8000\n"
"s_mov_b32 s58, 524288\n"
"s_mov_b32 s47, 0\n"
"s_add_u32 s60, s11, s61\n"
"s_and_b32 s60, s60, 0\n"
"s_mul_i32 s60, s60, 3\n"
"s_lshl_b32 s49, s60, 7\n"
"s_lshl_b32 s48, s60, 19\n"
"v_mbcnt_lo_u32_b32 v4, -1, 0\n"
"v_mbcnt_hi_u32_b32 v4, -1, v4\n"
"v_and_b32 v5, 15, v4\n"
"v_lshrrev_b32 v6, 4, v4\n"
"v_lshrrev_b32 v7, 3, v4\n"
"v_and_b32 v8, 7, v4\n"
"v_lshlrev_b32 v9, 1, v6\n"
"v_xor_b32 v9, v9, v8\n"
"v_lshlrev_b32 v9, 4, v9\n"
"s_mul_i32 s60, s42, 71680\n"
"v_mul_u32_u24 v10, 1280, v7\n"
"v_add3_u32 v247, v10, v9, s60\n"
"v_add_u32 v253, 9216, v247\n"
"v_add_u32 v254, 18432, v247\n"
"v_add_u32 v255, 27648, v247\n"
"v_lshl_add_u32 v240, v7, 8, v9\n"
"v_add_u32 v240, s44, v240\n"
"v_and_b32 v10, 1, v6\n"
"v_xor_b32 v10, v10, v8\n"
"v_lshlrev_b32 v10, 4, v10\n"
"v_lshl_add_u32 v10, v7, 13, v10\n"
"s_lshl_b32 s60, s42, 17\n"
"v_add_u32 v10, s60, v10\n"
"v_add_u32 v224, 1024, v10\n"
"v_add_u32 v225, 128, v10\n"
"v_xor_b32 v10, 64, v10\n"
"v_add_u32 v226, 64512, v10\n"
"v_add_u32 v227, 63616, v10\n"
"s_mul_i32 s40, s11, 286720\n"
"s_lshl_b32 s41, s61, 10\n"
"s_sub_u32 s30, 5243904, s41\n"
"s_sub_u32 s41, s41, 1024\n"
"s_ashr_i32 s60, s41, 31\n"
"s_lshl4_add_u32 s92, s10, s11\n"
"s_lshl_b32 s93, s92, 8\n"
"v_mov_b32 v0, 0\n"
"s_waitcnt lgkmcnt(0)\n"
"s_add_u32 s24, s24, s40\n"
"s_addc_u32 s25, s25, 0\n"
"s_and_b32 s25, s25, 0xffff\n"
"s_add_u32 s28, s28, s41\n"
"s_addc_u32 s29, s29, s60\n"
"s_and_b32 s29, s29, 0xffff\n"
"s_add_u32 s6, s6, s93\n"
"s_addc_u32 s7, s7, 0\n"
"global_load_dword v1, v0, s[6:7] offset:128 sc1\n"
"s_mov_b32 m0, s51\n"
"s_nop 0\n"
"buffer_load_dwordx4 v247, s[24:27], s49 offen lds\n"
"buffer_load_dwordx4 v253, s[24:27], s49 offen offset:1024 lds\n"
"buffer_load_dwordx4 v254, s[24:27], s49 offen offset:2048 lds\n"
"buffer_load_dwordx4 v255, s[24:27], s49 offen offset:3072 lds\n"
"s_add_u32 m0, s51, 4096\n"
"s_add_u32 s40, s49, 40960\n"
"buffer_load_dwordx4 v247, s[24:27], s40 offen lds\n"
"buffer_load_dwordx4 v253, s[24:27], s40 offen offset:1024 lds\n"
"buffer_load_dwordx4 v254, s[24:27], s40 offen offset:2048 lds\n"
"s_add_u32 s49, s49, 128\n"
"s_sub_u32 s41, s49, 1280\n"
"s_min_u32 s49, s49, s41\n"
"s_xor_b32 s51, s51, s74\n"
"s_mov_b32 m0, s50\n"
"s_nop 0\n"
"buffer_load_dwordx4 v224, s[28:31], s48 offen lds\n"
"buffer_load_dwordx4 v225, s[28:31], s48 offen offset:1024 lds\n"
"buffer_load_dwordx4 v226, s[28:31], s48 offen offset:2048 lds\n"
"buffer_load_dwordx4 v227, s[28:31], s48 offen offset:3072 lds\n"
"s_xor_b32 s50, s50, s74\n"
"s_lshl2_add_u32 s63, s11, s42\n"
"s_mul_i32 s63, s63, 10240\n"
"s_and_b32 s78, s42, 1\n"
"s_lshl_b32 s43, s78, 6\n"
"s_mov_b32 s77, 0x80000000\n"
"s_mov_b32 s75, 0x10000\n"
"s_mov_b32 s73, 0xffff0000\n"
"s_mov_b32 s46, 0x3e0293ee\n"
"s_mov_b32 s45, 0x5d800000\n"
"s_mov_b32 s62, 9\n"
"v_lshl_add_u32 v242, v4, 4, s44\n"
"s_lshr_b32 s79, s42, 1\n"
"s_lshl4_add_u32 s76, s79, s78\n"
"s_lshl_b32 s76, s76, 10\n"
"s_add_u32 s76, s76, 0x20000\n"
"s_sub_u32 s76, s76, s44\n"
"v_add_u32 v248, s76, v242\n"
"v_and_b32 v9, 6, v5\n"
"v_xor_b32 v9, v9, v6\n"
"v_lshlrev_b32 v9, 4, v9\n"
"s_xor_b32 m0, s50, 0xc000\n"
"s_add_u32 s40, s48, 256\n"
"buffer_load_dwordx4 v224, s[28:31], s40 offen lds\n"
"v_lshl_add_u32 v10, v5, 7, v9\n"
"s_mul_i32 s79, s42, 6144\n"
"s_add_u32 s79, s79, 0x10000\n"
"v_add_u32 v249, s79, v10\n"
"v_xor_b32 v250, 64, v249\n"
"s_lshr_b32 s79, s42, 1\n"
"s_lshl_b32 s79, s79, 11\n"
"s_add_u32 s79, s79, 90112\n"
"v_add_u32 v251, s79, v10\n"
"v_xor_b32 v252, 64, v251\n"
"v_and_b32 v11, 8, v5\n"
"v_lshl_add_u32 v10, v11, 7, v10\n"
"s_mul_i32 s79, s78, 8256\n"
"v_xor_b32 v228, s79, v10\n"
"buffer_load_dwordx4 v225, s[28:31], s40 offen offset:1024 lds\n"
"v_xor_b32 v229, 64, v228\n"
"v_xor_b32 v230, 0x2000, v228\n"
"v_xor_b32 v231, 0x2000, v229\n"
"v_lshrrev_b32 v9, 5, v4\n"
"v_lshlrev_b32 v9, 11, v9\n"
"v_bfe_u32 v10, v4, 2, 3\n"
"v_lshl_add_u32 v9, v10, 7, v9\n"
"v_and_b32 v10, 3, v4\n"
"v_lshl_add_u32 v9, v10, 3, v9\n"
"v_bfe_u32 v10, v4, 3, 2\n"
"v_lshl_add_u32 v9, v10, 5, v9\n"
"s_add_u32 s79, s79, 0x10000\n"
"v_xor_b32 v232, s79, v9\n"
"v_xor_b32 v233, 32, v232\n"
"buffer_load_dwordx4 v226, s[28:31], s40 offen offset:2048 lds\n"
"v_xor_b32 v234, 64, v232\n"
"v_xor_b32 v235, 96, v232\n"
"v_xor_b32 v236, 0x2000, v232\n"
"v_xor_b32 v237, 0x2000, v233\n"
"v_xor_b32 v238, 0x2000, v234\n"
"v_xor_b32 v239, 0x2000, v235\n"
"v_lshlrev_b32 v9, 11, v6\n"
"v_bfe_u32 v10, v4, 2, 2\n"
"v_lshl_add_u32 v9, v10, 7, v9\n"
"v_and_b32 v10, 1, v4\n"
"v_lshl_add_u32 v9, v10, 3, v9\n"
"v_bfe_u32 v10, v4, 1, 1\n"
"v_lshl_add_u32 v9, v10, 5, v9\n"
"v_bfe_u32 v10, v4, 3, 1\n"
"buffer_load_dwordx4 v227, s[28:31], s40 offen offset:3072 lds\n"
"s_cmp_eq_u32 s47, 8\ns_cselect_b32 s41, 524800, s58\n"
"s_add_u32 s48, s48, s41\n"
"s_sub_u32 s41, s48, 5242880\n"
"s_min_u32 s48, s48, s41\n"
"v_lshl_add_u32 v9, v10, 4, v9\n"
"v_bfe_u32 v10, v4, 4, 1\n"
"v_lshl_add_u32 v9, v10, 6, v9\n"
"v_xor_b32 v243, s43, v9\n"
"v_xor_b32 v244, 16, v243\n"
"v_xor_b32 v245, 64, v243\n"
"v_xor_b32 v246, 0x50, v243\n"
"s_lshl3_add_u32 s79, s11, s61\n"
"s_lshl_b32 s79, s79, 20\n"
"s_sub_u32 s79, s79, 896\n"
"s_ashr_i32 s60, s79, 31\n"
"s_add_u32 s16, s16, s79\n"
"s_addc_u32 s17, s17, s60\n"
"s_and_b32 s17, s17, 0xffff\n"
"s_mov_b32 m0, s50\n"
"s_nop 0\n"
"buffer_load_dwordx4 v224, s[28:31], s48 offen lds\n"
"s_mov_b32 s18, 1049472\n"
"s_mov_b64 s[84:85], s[16:17]\n"
"s_mov_b64 s[86:87], s[18:19]\n"
"s_add_u32 s20, s20, s79\n"
"s_addc_u32 s21, s21, s60\n"
"s_and_b32 s21, s21, 0xffff\n"
"s_mov_b32 s22, 1049472\n"
"s_mov_b64 s[88:89], s[20:21]\n"
"s_mov_b64 s[90:91], s[22:23]\n"
"s_lshr_b32 s57, %[wg], 3\n"
"s_mul_i32 s57, s57, 5\n"
"s_and_b32 s57, s57, 15\n"
"s_lshl_b32 s57, s57, 14\n"
"s_mov_b32 s55, 896\n"
"buffer_load_dwordx4 v225, s[28:31], s48 offen offset:1024 lds\n"
"s_add_u32 s56, s57, 896\n"
"s_mov_b32 s59, 16384\n"
"s_mov_b32 s52, -896\n"
"s_mov_b32 s53, 0\n"
"s_mov_b32 s54, -896\n"
"s_mul_i32 s79, s61, 655360\n"
"s_add_u32 s8, s8, s79\n"
"s_addc_u32 s9, s9, 0\n"
"s_and_b32 s9, s9, 0xffff\n"
"s_mul_i32 s79, s92, 2293760\n"
"s_add_u32 s32, s12, s79\n"
"s_addc_u32 s33, s13, 0\n"
"s_and_b32 s33, s33, 0xffff\n"
"s_mov_b32 s34, 2293760\n"
"buffer_load_dwordx4 v226, s[28:31], s48 offen offset:2048 lds\n"
"s_mul_i32 s79, s11, 573440\n"
"s_mul_i32 s60, s10, 1280\n"
"s_sub_u32 s38, 573440, s60\n"
"s_add_u32 s79, s79, s60\n"
"s_add_u32 s36, s14, s79\n"
"s_addc_u32 s37, s15, 0\n"
"s_and_b32 s37, s37, 0xffff\n"
"s_lshl2_add_u32 s93, s61, s42\n"
"s_lshl_b32 s94, s93, 10\n"
"s_add_u32 s94, s94, 229376\n"
"s_add_u32 s95, s61, 1\n"
"s_and_b32 s95, s95, 7\n"
"s_lshl_b32 s95, s95, 7\n"
"s_add_u32 s100, s61, 2\n"
"buffer_load_dwordx4 v227, s[28:31], s48 offen offset:3072 lds\n"
"s_xor_b32 s50, s50, s74\n"
"s_and_b32 s100, s100, 7\n"
"s_lshl_b32 s100, s100, 7\n"
"s_lshl_b32 s60, s61, 7\n"
"s_add_u32 s60, s60, s43\n"
"s_mul_i32 s97, s42, 122880\n"
"s_add_u32 s97, s97, s60\n"
"s_lshr_b32 s98, s42, 1\n"
"s_add_u32 s98, s98, 12\n"
"s_mul_i32 s98, s98, 40960\n"
"s_add_u32 s98, s98, s60\n"
"s_mul_i32 s60, s93, 37\n"
"s_lshr_b32 s60, s60, 8\n"
"s_mul_i32 s79, s60, 7\n"
"s_sub_u32 s79, s93, s79\n"
"s_mov_b32 m0, s51\n"
"s_nop 0\n"
"buffer_load_dwordx4 v247, s[24:27], s49 offen lds\n"
"s_mul_i32 s99, s60, 3\n"
"s_lshr_b32 s78, s79, 1\n"
"s_add_u32 s99, s99, s78\n"
"s_lshr_b32 s78, s60, 1\n"
"s_add_u32 s78, s78, 12\n"
"s_cmp_eq_u32 s79, 6\n"
"s_cselect_b32 s99, s78, s99\n"
"s_mul_i32 s99, s99, 40960\n"
"s_xor_b32 s79, s79, s60\n"
"s_and_b32 s79, s79, 1\n"
"s_lshl_b32 s79, s79, 6\n"
"s_add_u32 s99, s99, s79\n"
"s_add_u32 s99, s99, 1024\n"
"s_cmp_eq_u32 s61, 7\n"
"s_cselect_b32 s99, 0x40000000, s99\n"
"s_cselect_b32 s94, 0x40000000, s94\n"
"buffer_load_dwordx4 v253, s[24:27], s49 offen offset:1024 lds\n"
"v_mov_b64 v[4:5], 0\n"
"v_mov_b64 v[6:7], 0\n"
"v_mov_b64 v[8:9], 0\n"
"v_mov_b64 v[10:11], 0\n"
"v_mov_b64 v[12:13], 0\n"
"v_mov_b64 v[14:15], 0\n"
"v_mov_b64 v[16:17], 0\n"
"v_mov_b64 v[18:19], 0\n"
"v_mov_b64 v[20:21], 0\n"
"v_mov_b64 v[22:23], 0\n"
"v_mov_b64 v[24:25], 0\n"
"v_mov_b64 v[26:27], 0\n"
"buffer_load_dwordx4 v254, s[24:27], s49 offen offset:2048 lds\n"
"v_mov_b64 v[28:29], 0\n"
"v_mov_b64 v[30:31], 0\n"
"v_mov_b64 v[32:33], 0\n"
"v_mov_b64 v[34:35], 0\n"
"v_mov_b64 v[36:37], 0\n"
"v_mov_b64 v[38:39], 0\n"
"v_mov_b64 v[40:41], 0\n"
"v_mov_b64 v[42:43], 0\n"
"v_mov_b64 v[44:45], 0\n"
"v_mov_b64 v[46:47], 0\n"
"v_mov_b64 v[48:49], 0\n"
"v_mov_b64 v[50:51], 0\n"
"v_mov_b64 v[52:53], 0\n"
"v_mov_b64 v[54:55], 0\n"
"buffer_load_dwordx4 v255, s[24:27], s49 offen offset:3072 lds\n"
"v_mov_b64 v[56:57], 0\n"
"v_mov_b64 v[58:59], 0\n"
"v_mov_b64 v[60:61], 0\n"
"v_mov_b64 v[62:63], 0\n"
"v_mov_b64 v[64:65], 0\n"
"v_mov_b64 v[66:67], 0\n"
"v_mov_b64 v[68:69], 0\n"
"v_mov_b64 v[70:71], 0\n"
"v_mov_b64 v[72:73], 0\n"
"v_mov_b64 v[74:75], 0\n"
"v_mov_b64 v[76:77], 0\n"
"v_mov_b64 v[78:79], 0\n"
"v_mov_b64 v[80:81], 0\n"
"v_mov_b64 v[82:83], 0\n"
"s_add_u32 m0, s51, 4096\n"
"s_add_u32 s40, s49, 40960\n"
"buffer_load_dwordx4 v247, s[24:27], s40 offen lds\n"
"v_mov_b64 v[84:85], 0\n"
"v_mov_b64 v[86:87], 0\n"
"v_mov_b64 v[88:89], 0\n"
"v_mov_b64 v[90:91], 0\n"
"v_mov_b64 v[92:93], 0\n"
"v_mov_b64 v[94:95], 0\n"
"v_mov_b64 v[96:97], 0\n"
"v_mov_b64 v[98:99], 0\n"
"v_mov_b64 v[100:101], 0\n"
"v_mov_b64 v[102:103], 0\n"
"v_mov_b64 v[104:105], 0\n"
"v_mov_b64 v[106:107], 0\n"
"v_mov_b64 v[108:109], 0\n"
"v_mov_b64 v[110:111], 0\n"
"buffer_load_dwordx4 v253, s[24:27], s40 offen offset:1024 lds\n"
"v_mov_b64 v[112:113], 0\n"
"v_mov_b64 v[114:115], 0\n"
"v_mov_b64 v[116:117], 0\n"
"v_mov_b64 v[118:119], 0\n"
"v_mov_b64 v[120:121], 0\n"
"v_mov_b64 v[122:123], 0\n"
"v_mov_b64 v[124:125], 0\n"
"v_mov_b64 v[126:127], 0\n"
"v_mov_b64 v[128:129], 0\n"
"v_mov_b64 v[130:131], 0\n"
"v_mov_b64 v[132:133], 0\n"
"v_mov_b64 v[134:135], 0\n"
"v_mov_b64 v[136:137], 0\n"
"v_mov_b64 v[138:139], 0\n"
"buffer_load_dwordx4 v254, s[24:27], s40 offen offset:2048 lds\n"
"s_add_u32 s49, s49, 128\n"
"s_sub_u32 s41, s49, 1280\n"
"s_min_u32 s49, s49, s41\n"
"s_xor_b32 s51, s51, s74\n"
"v_mov_b64 v[140:141], 0\n"
"v_mov_b64 v[142:143], 0\n"
"v_mov_b64 v[144:145], 0\n"
"v_mov_b64 v[146:147], 0\n"
"v_mov_b64 v[148:149], 0\n"
"v_mov_b64 v[150:151], 0\n"
"v_mov_b64 v[152:153], 0\n"
"v_mov_b64 v[154:155], 0\n"
"v_mov_b64 v[156:157], 0\n"
"v_mov_b64 v[158:159], 0\n"
"v_mov_b64 v[160:161], 0\n"
"v_mov_b64 v[162:163], 0\n"
"v_mov_b64 v[164:165], 0\n"
"v_mov_b64 v[166:167], 0\n"
"v_mov_b64 v[168:169], 0\n"
"v_mov_b64 v[170:171], 0\n"
"v_mov_b64 v[172:173], 0\n"
"v_mov_b64 v[174:175], 0\n"
"s_memtime s[80:81]\n"
"s_memrealtime s[82:83]\n"
"v_mov_b64 v[176:177], 0\n"
"v_mov_b64 v[178:179], 0\n"
"v_mov_b64 v[180:181], 0\n"
"v_mov_b64 v[182:183], 0\n"
"v_mov_b64 v[184:185], 0\n"
"v_mov_b64 v[186:187], 0\n"
"v_mov_b64 v[188:189], 0\n"
"v_mov_b64 v[190:191], 0\n"
"v_mov_b64 v[192:193], 0\n"
"v_mov_b64 v[194:195], 0\n"
"v_mov_b64 v[196:197], 0\n"
"v_mov_b64 v[198:199], 0\n"
"v_mov_b64 v[200:201], 0\n"
"v_mov_b64 v[202:203], 0\n"
"v_mov_b64 v[204:205], 0\n"
"v_mov_b64 v[206:207], 0\n"
"v_mov_b64 v[208:209], 0\n"
"v_mov_b64 v[210:211], 0\n"
"v_mov_b64 v[212:213], 0\n"
"v_mov_b64 v[214:215], 0\n"
"v_mov_b64 v[216:217], 0\n"
"v_mov_b64 v[218:219], 0\n"
"v_mov_b64 v[220:221], 0\n"
"v_mov_b64 v[222:223], 0\n"
"s_waitcnt vmcnt(26)\n"
"v_readfirstlane_b32 s92, v1\n"
"v_mov_b64 v[0:1], 0\n"
"v_mov_b64 v[2:3], 0\n"
"s_waitcnt lgkmcnt(0)\n"
"s_sub_u32 s80, s80, s64\n"
"s_sub_u32 s82, s82, s66\n"
"s_mul_i32 s80, s80, 10\n"
"s_mul_i32 s82, s82, 211\n"
"s_cmp_gt_u32 s80, s82\n"
"s_cselect_b32 s64, 1, 0\n"
"s_cselect_b32 s68, 655360, 0\n"
"s_cmp_eq_u32 s42, 0\n"
"s_cselect_b32 s64, s64, 0\n"
"s_mov_b32 s65, 0\n"
"s_add_u32 s66, s66, 2800\n"
"s_mov_b32 s67, s66\n"
"s_mov_b32 s72, 122\n"
"s_mov_b64 vcc, s[64:65]\n"
"s_nop 2\n"
"s_mul_i32 s60, s92, 0x9e3779b1\n"
"s_add_u32 s60, s60, 0x7f4a7c15\n"
"s_waitcnt vmcnt(15)\n"
"s_barrier\n"
"ds_read_b128 a[160:163], v249 offset:0\n"
"ds_read_b128 a[164:167], v250 offset:0\n"
"ds_read_b64_tr_b16 a[96:97], v243 offset:0\n"
"ds_read_b64_tr_b16 a[98:99], v243 offset:512\n"
"ds_read_b64_tr_b16 a[100:101], v243 offset:8192\n"
"ds_read_b64_tr_b16 a[102:103], v243 offset:8704\n"
"ds_read_b64_tr_b16 a[104:105], v244 offset:0\n"
"ds_read_b64_tr_b16 a[106:107], v244 offset:512\n"
"ds_read_b64_tr_b16 a[108:109], v244 offset:8192\n"
"ds_read_b64_tr_b16 a[110:111], v244 offset:8704\n"
"ds_read_b64_tr_b16 a[112:113], v245 offset:0\n"
"ds_read_b64_tr_b16 a[114:115], v245 offset:512\n"
"ds_read_b64_tr_b16 a[116:117], v245 offset:8192\n"
"ds_read_b64_tr_b16 a[118:119], v245 offset:8704\n"
"ds_read_b64_tr_b16 a[120:121], v246 offset:0\n"
"ds_read_b64_tr_b16 a[122:123], v246 offset:512\n"
"ds_read_b64_tr_b16 a[124:125], v246 offset:8192\n"
"ds_read_b64_tr_b16 a[126:127], v246 offset:8704\n"
"ds_read_b64_tr_b16 a[128:129], v243 offset:1024\n"
"ds_read_b64_tr_b16 a[130:131], v243 offset:1536\n"
"ds_read_b64_tr_b16 a[132:133], v243 offset:9216\n"
"ds_read_b64_tr_b16 a[134:135], v243 offset:9728\n"
"ds_read_b64_tr_b16 a[136:137], v244 offset:1024\n"
"ds_read_b64_tr_b16 a[138:139], v244 offset:1536\n"
"ds_read_b64_tr_b16 a[140:141], v244 offset:9216\n"
"ds_read_b64_tr_b16 a[142:143], v244 offset:9728\n"
"ds_read_b64_tr_b16 a[144:145], v245 offset:1024\n"
"ds_read_b64_tr_b16 a[146:147], v245 offset:1536\n"
"ds_read_b64_tr_b16 a[148:149], v245 offset:9216\n"
"ds_read_b64_tr_b16 a[150:151], v245 offset:9728\n"
"ds_read_b64_tr_b16 a[152:153], v246 offset:1024\n"
"ds_read_b64_tr_b16 a[154:155], v246 offset:1536\n"
"ds_read_b64_tr_b16 a[156:157], v246 offset:9216\n"
"ds_read_b64_tr_b16 a[158:159], v246 offset:9728\n"
"ds_read_b128 a[168:171], v249 offset:2048\n"
"ds_read_b128 a[172:175], v250 offset:2048\n"
"ds_read_b128 a[176:179], v249 offset:4096\n"
"ds_read_b128 a[180:183], v250 offset:4096\n"
"ds_read_b128 a[184:187], v251 offset:0\n"
"ds_read_b128 a[188:191], v252 offset:0\n"
"LPL_%=:\n"
"s_waitcnt lgkmcnt(15)\n"
"v_mfma_f32_16x16x32_bf16 v[112:115], a[96:99], a[160:163], v[112:115]\n"
"v_mfma_f32_16x16x32_bf16 v[112:115], a[100:103], a[164:167], v[112:115]\n"
"v_mfma_f32_16x16x32_bf16 v[116:119], a[104:107], a[160:163], v[116:119]\n"
"s_waitcnt vmcnt(11) lgkmcnt(5)\n"
"v_mfma_f32_16x16x32_bf16 v[116:119], a[108:111], a[164:167], v[116:119]\n"
"s_barrier\n"
"v_mfma_f32_16x16x32_bf16 v[120:123], a[112:115], a[160:163], v[120:123]\n"
"s_add_u32 s40, s48, 256\n"
"s_xor_b32 m0, s50, 0xc000\n"
"ds_read_b64_tr_b16 a[192:193], v243 offset:16384\n"
"v_mfma_f32_16x16x32_bf16 v[120:123], a[116:119], a[164:167], v[120:123]\n"
"buffer_load_dwordx4 v224, s[28:31], s40 offen lds\n"
"v_mfma_f32_16x16x32_bf16 v[124:127], a[120:123], a[160:163], v[124:127]\n"
"ds_read_b64_tr_b16 a[194:195], v243 offset:16896\n"
"v_mfma_f32_16x16x32_bf16 v[124:127], a[124:127], a[164:167], v[124:127]\n"
"ds_read_b64_tr_b16 a[196:197], v243 offset:24576\n"
"v_mfma_f32_16x16x32_bf16 v[128:131], a[128:131], a[160:163], v[128:131]\n"
"ds_read_b64_tr_b16 a[198:199], v243 offset:25088\n"
"v_mfma_f32_16x16x32_bf16 v[128:131], a[132:135], a[164:167], v[128:131]\n"
"ds_read_b64_tr_b16 a[200:201], v244 offset:16384\n"
"v_mfma_f32_16x16x32_bf16 v[132:135], a[136:139], a[160:163], v[132:135]\n"
"ds_read_b64_tr_b16 a[202:203], v244 offset:16896\n"
"v_mfma_f32_16x16x32_bf16 v[132:135], a[140:143], a[164:167], v[132:135]\n"
"buffer_load_dwordx4 v225, s[28:31], s40 offen offset:1024 lds\n"
"v_mfma_f32_16x16x32_bf16 v[136:139], a[144:147], a[160:163], v[136:139]\n"
"ds_read_b64_tr_b16 a[204:205], v244 offset:24576\n"
"v_mfma_f32_16x16x32_bf16 v[136:139], a[148:151], a[164:167], v[136:139]\n"
"ds_read_b64_tr_b16 a[206:207], v244 offset:25088\n"
"v_mfma_f32_16x16x32_bf16 v[140:143], a[152:155], a[160:163], v[140:143]\n"
"ds_read_b64_tr_b16 a[208:209], v245 offset:16384\n"
"v_mfma_f32_16x16x32_bf16 v[140:143], a[156:159], a[164:167], v[140:143]\n"
"s_waitcnt lgkmcnt(6)\n"
"ds_read_b64_tr_b16 a[210:211], v245 offset:16896\n"
"v_mfma_f32_16x16x32_bf16 v[172:175], a[152:155], a[168:171], v[172:175]\n"
"s_waitcnt lgkmcnt(6)\n"
"ds_read_b64_tr_b16 a[212:213], v245 offset:24576\n"
"v_mfma_f32_16x16x32_bf16 v[172:175], a[156:159], a[172:175], v[172:175]\n"
"buffer_load_dwordx4 v226, s[28:31], s40 offen offset:2048 lds\n"
"v_mfma_f32_16x16x32_bf16 v[168:171], a[144:147], a[168:171], v[168:171]\n"
"ds_read_b64_tr_b16 a[214:215], v245 offset:25088\n"
"v_mfma_f32_16x16x32_bf16 v[168:171], a[148:151], a[172:175], v[168:171]\n"
"ds_read_b64_tr_b16 a[216:217], v246 offset:16384\n"
"v_mfma_f32_16x16x32_bf16 v[164:167], a[136:139], a[168:171], v[164:167]\n"
"ds_read_b64_tr_b16 a[218:219], v246 offset:16896\n"
"v_mfma_f32_16x16x32_bf16 v[164:167], a[140:143], a[172:175], v[164:167]\n"
"ds_read_b64_tr_b16 a[220:221], v246 offset:24576\n"
"v_mfma_f32_16x16x32_bf16 v[160:163], a[128:131], a[168:171], v[160:163]\n"
"ds_read_b64_tr_b16 a[222:223], v246 offset:25088\n"
"v_mfma_f32_16x16x32_bf16 v[160:163], a[132:135], a[172:175], v[160:163]\n"
"buffer_load_dwordx4 v227, s[28:31], s40 offen offset:3072 lds\n"
"v_mfma_f32_16x16x32_bf16 v[156:159], a[120:123], a[168:171], v[156:159]\n"
"s_cmp_eq_u32 s47, 8\ns_cselect_b32 s41, 524800, s58\n"
"ds_read_b64_tr_b16 a[224:225], v243 offset:17408\n"
"v_mfma_f32_16x16x32_bf16 v[156:159], a[124:127], a[172:175], v[156:159]\n"
"s_add_u32 s48, s48, s41\n"
"ds_read_b64_tr_b16 a[226:227], v243 offset:17920\n"
"v_mfma_f32_16x16x32_bf16 v[152:155], a[112:115], a[168:171], v[152:155]\n"
"s_sub_u32 s41, s48, 5242880\n"
"ds_read_b64_tr_b16 a[228:229], v243 offset:25600\n"
"v_mfma_f32_16x16x32_bf16 v[152:155], a[116:119], a[172:175], v[152:155]\n"
"s_min_u32 s48, s48, s41\n"
"ds_read_b64_tr_b16 a[230:231], v243 offset:26112\n"
"v_mfma_f32_16x16x32_bf16 v[148:151], a[104:107], a[168:171], v[148:151]\n"
"s_mov_b32 m0, s50\n"
"ds_read_b64_tr_b16 a[232:233], v244 offset:17408\n"
"v_mfma_f32_16x16x32_bf16 v[148:151], a[108:111], a[172:175], v[148:151]\n"
"buffer_load_dwordx4 v224, s[28:31], s48 offen lds\n"
"v_mfma_f32_16x16x32_bf16 v[144:147], a[96:99], a[168:171], v[144:147]\n"
"ds_read_b64_tr_b16 a[234:235], v244 offset:17920\n"
"v_xor_b32 v243, s74, v243\n"
"v_mfma_f32_16x16x32_bf16 v[144:147], a[100:103], a[172:175], v[144:147]\n"
"ds_read_b64_tr_b16 a[236:237], v244 offset:25600\n"
"v_xor_b32 v249, s74, v249\n"
"v_mfma_f32_16x16x32_bf16 v[176:179], a[96:99], a[176:179], v[176:179]\n"
"s_waitcnt lgkmcnt(6)\n"
"ds_read_b64_tr_b16 a[238:239], v244 offset:26112\n"
"v_xor_b32 v250, s74, v250\n"
"v_mfma_f32_16x16x32_bf16 v[176:179], a[100:103], a[180:183], v[176:179]\n"
"ds_read_b64_tr_b16 a[240:241], v245 offset:17408\n"
"v_xor_b32 v244, s74, v244\n"
"v_mfma_f32_16x16x32_bf16 v[180:183], a[104:107], a[176:179], v[180:183]\n"
"ds_read_b64_tr_b16 a[242:243], v245 offset:17920\n"
"v_xor_b32 v251, s74, v251\n"
"v_mfma_f32_16x16x32_bf16 v[180:183], a[108:111], a[180:183], v[180:183]\n"
"buffer_load_dwordx4 v225, s[28:31], s48 offen offset:1024 lds\n"
"v_mfma_f32_16x16x32_bf16 v[184:187], a[112:115], a[176:179], v[184:187]\n"
"ds_read_b64_tr_b16 a[244:245], v245 offset:25600\n"
"v_xor_b32 v252, s74, v252\n"
"v_mfma_f32_16x16x32_bf16 v[184:187], a[116:119], a[180:183], v[184:187]\n"
"ds_read_b64_tr_b16 a[246:247], v245 offset:26112\n"
"v_xor_b32 v245, s74, v245\n"
"v_mfma_f32_16x16x32_bf16 v[188:191], a[120:123], a[176:179], v[188:191]\n"
"ds_read_b64_tr_b16 a[248:249], v246 offset:17408\n"
"v_mfma_f32_16x16x32_bf16 v[188:191], a[124:127], a[180:183], v[188:191]\n"
"ds_read_b64_tr_b16 a[250:251], v246 offset:17920\n"
"v_mfma_f32_16x16x32_bf16 v[200:203], a[144:147], a[176:179], v[200:203]\n"
"ds_read_b64_tr_b16 a[252:253], v246 offset:25600\n"
"v_mfma_f32_16x16x32_bf16 v[200:203], a[148:151], a[180:183], v[200:203]\n"
"buffer_load_dwordx4 v226, s[28:31], s48 offen offset:2048 lds\n"
"v_mfma_f32_16x16x32_bf16 v[204:207], a[152:155], a[176:179], v[204:207]\n"
"ds_read_b64_tr_b16 a[254:255], v246 offset:26112\n"
"v_xor_b32 v246, s74, v246\n"
"v_mfma_f32_16x16x32_bf16 v[204:207], a[156:159], a[180:183], v[204:207]\n"
"v_mfma_f32_16x16x32_bf16 v[192:195], a[128:131], a[176:179], v[192:195]\n"
"v_mfma_f32_16x16x32_bf16 v[192:195], a[132:135], a[180:183], v[192:195]\n"
"v_mfma_f32_16x16x32_bf16 v[196:199], a[136:139], a[176:179], v[196:199]\n"
"v_mfma_f32_16x16x32_bf16 v[196:199], a[140:143], a[180:183], v[196:199]\n"
"buffer_load_dwordx4 v227, s[28:31], s48 offen offset:3072 lds\n"
"v_mfma_f32_16x16x32_bf16 v[220:223], a[136:139], a[184:187], v[220:223]\n"
"s_xor_b32 s50, s50, s74\n"
"v_mfma_f32_16x16x32_bf16 v[220:223], a[140:143], a[188:191], v[220:223]\n"
"v_mfma_f32_16x16x32_bf16 v[216:219], a[128:131], a[184:187], v[216:219]\n"
"v_mfma_f32_16x16x32_bf16 v[216:219], a[132:135], a[188:191], v[216:219]\n"
"v_mfma_f32_16x16x32_bf16 v[212:215], a[104:107], a[184:187], v[212:215]\n"
"v_mfma_f32_16x16x32_bf16 v[212:215], a[108:111], a[188:191], v[212:215]\n"
"v_mfma_f32_16x16x32_bf16 v[208:211], a[96:99], a[184:187], v[208:211]\n"
"v_mfma_f32_16x16x32_bf16 v[208:211], a[100:103], a[188:191], v[208:211]\n"
"v_mfma_f32_16x16x32_bf16 v[0:3], a[192:195], a[160:163], v[0:3]\n"
"s_waitcnt lgkmcnt(0)\n"
"v_mfma_f32_16x16x32_bf16 v[0:3], a[196:199], a[164:167], v[0:3]\n"
"s_waitcnt lgkmcnt(0)\n"
"v_mfma_f32_16x16x32_bf16 v[4:7], a[200:203], a[160:163], v[4:7]\n"
"s_waitcnt vmcnt(8) lgkmcnt(0)\n"
"v_mfma_f32_16x16x32_bf16 v[4:7], a[204:207], a[164:167], v[4:7]\n"
"s_barrier\n"
"v_mfma_f32_16x16x32_bf16 v[8:11], a[208:211], a[160:163], v[8:11]\n"
"s_mov_b32 m0, s51\n"
"ds_read_b64_tr_b16 a[96:97], v243 offset:0\n"
"v_mfma_f32_16x16x32_bf16 v[8:11], a[212:215], a[164:167], v[8:11]\n"
"buffer_load_dwordx4 v247, s[24:27], s49 offen lds\n"
"v_mfma_f32_16x16x32_bf16 v[12:15], a[216:219], a[160:163], v[12:15]\n"
"ds_read_b64_tr_b16 a[98:99], v243 offset:512\n"
"v_mfma_f32_16x16x32_bf16 v[12:15], a[220:223], a[164:167], v[12:15]\n"
"ds_read_b64_tr_b16 a[100:101], v243 offset:8192\n"
"v_mfma_f32_16x16x32_bf16 v[16:19], a[224:227], a[160:163], v[16:19]\n"
"ds_read_b64_tr_b16 a[102:103], v243 offset:8704\n"
"v_mfma_f32_16x16x32_bf16 v[16:19], a[228:231], a[164:167], v[16:19]\n"
"ds_read_b64_tr_b16 a[104:105], v244 offset:0\n"
"v_mfma_f32_16x16x32_bf16 v[20:23], a[232:235], a[160:163], v[20:23]\n"
"ds_read_b64_tr_b16 a[106:107], v244 offset:512\n"
"v_mfma_f32_16x16x32_bf16 v[20:23], a[236:239], a[164:167], v[20:23]\n"
"buffer_load_dwordx4 v253, s[24:27], s49 offen offset:1024 lds\n"
"v_mfma_f32_16x16x32_bf16 v[24:27], a[240:243], a[160:163], v[24:27]\n"
"ds_read_b64_tr_b16 a[108:109], v244 offset:8192\n"
"v_mfma_f32_16x16x32_bf16 v[24:27], a[244:247], a[164:167], v[24:27]\n"
"ds_read_b64_tr_b16 a[110:111], v244 offset:8704\n"
"v_mfma_f32_16x16x32_bf16 v[28:31], a[248:251], a[160:163], v[28:31]\n"
"ds_read_b128 a[160:163], v249 offset:0\n"
"v_mfma_f32_16x16x32_bf16 v[28:31], a[252:255], a[164:167], v[28:31]\n"
"ds_read_b128 a[164:167], v250 offset:0\n"
"v_mfma_f32_16x16x32_bf16 v[60:63], a[248:251], a[168:171], v[60:63]\n"
"ds_read_b64_tr_b16 a[112:113], v245 offset:0\n"
"v_mfma_f32_16x16x32_bf16 v[60:63], a[252:255], a[172:175], v[60:63]\n"
"buffer_load_dwordx4 v254, s[24:27], s49 offen offset:2048 lds\n"
"v_mfma_f32_16x16x32_bf16 v[56:59], a[240:243], a[168:171], v[56:59]\n"
"ds_read_b64_tr_b16 a[114:115], v245 offset:512\n"
"v_mfma_f32_16x16x32_bf16 v[56:59], a[244:247], a[172:175], v[56:59]\n"
"ds_read_b64_tr_b16 a[116:117], v245 offset:8192\n"
"v_mfma_f32_16x16x32_bf16 v[52:55], a[232:235], a[168:171], v[52:55]\n"
"ds_read_b64_tr_b16 a[118:119], v245 offset:8704\n"
"v_mfma_f32_16x16x32_bf16 v[52:55], a[236:239], a[172:175], v[52:55]\n"
"ds_read_b64_tr_b16 a[120:121], v246 offset:0\n"
"v_mfma_f32_16x16x32_bf16 v[48:51], a[224:227], a[168:171], v[48:51]\n"
"ds_read_b64_tr_b16 a[122:123], v246 offset:512\n"
"v_mfma_f32_16x16x32_bf16 v[48:51], a[228:231], a[172:175], v[48:51]\n"
"buffer_load_dwordx4 v255, s[24:27], s49 offen offset:3072 lds\n"
"v_mfma_f32_16x16x32_bf16 v[44:47], a[216:219], a[168:171], v[44:47]\n"
"ds_read_b64_tr_b16 a[124:125], v246 offset:8192\n"
"v_mfma_f32_16x16x32_bf16 v[44:47], a[220:223], a[172:175], v[44:47]\n"
"ds_read_b64_tr_b16 a[126:127], v246 offset:8704\n"
"v_mfma_f32_16x16x32_bf16 v[40:43], a[208:211], a[168:171], v[40:43]\n"
"ds_read_b64_tr_b16 a[128:129], v243 offset:1024\n"
"v_mfma_f32_16x16x32_bf16 v[40:43], a[212:215], a[172:175], v[40:43]\n"
"s_add_u32 s40, s49, 40960\n"
"ds_read_b64_tr_b16 a[130:131], v243 offset:1536\n"
"v_mfma_f32_16x16x32_bf16 v[36:39], a[200:203], a[168:171], v[36:39]\n"
"s_add_u32 m0, s51, 4096\n"
"ds_read_b64_tr_b16 a[132:133], v243 offset:9216\n"
"v_mfma_f32_16x16x32_bf16 v[36:39], a[204:207], a[172:175], v[36:39]\n"
"buffer_load_dwordx4 v247, s[24:27], s40 offen lds\n"
"v_mfma_f32_16x16x32_bf16 v[32:35], a[192:195], a[168:171], v[32:35]\n"
"ds_read_b64_tr_b16 a[134:135], v243 offset:9728\n"
"v_mfma_f32_16x16x32_bf16 v[32:35], a[196:199], a[172:175], v[32:35]\n"
"ds_read_b64_tr_b16 a[136:137], v244 offset:1024\n"
"v_mfma_f32_16x16x32_bf16 v[64:67], a[192:195], a[176:179], v[64:67]\n"
"ds_read_b64_tr_b16 a[138:139], v244 offset:1536\n"
"v_mfma_f32_16x16x32_bf16 v[64:67], a[196:199], a[180:183], v[64:67]\n"
"ds_read_b64_tr_b16 a[140:141], v244 offset:9216\n"
"v_mfma_f32_16x16x32_bf16 v[68:71], a[200:203], a[176:179], v[68:71]\n"
"ds_read_b64_tr_b16 a[142:143], v244 offset:9728\n"
"v_mfma_f32_16x16x32_bf16 v[68:71], a[204:207], a[180:183], v[68:71]\n"
"buffer_load_dwordx4 v253, s[24:27], s40 offen offset:1024 lds\n"
"v_mfma_f32_16x16x32_bf16 v[72:75], a[208:211], a[176:179], v[72:75]\n"
"ds_read_b64_tr_b16 a[144:145], v245 offset:1024\n"
"v_mfma_f32_16x16x32_bf16 v[72:75], a[212:215], a[180:183], v[72:75]\n"
"ds_read_b64_tr_b16 a[146:147], v245 offset:1536\n"
"v_mfma_f32_16x16x32_bf16 v[76:79], a[216:219], a[176:179], v[76:79]\n"
"ds_read_b64_tr_b16 a[148:149], v245 offset:9216\n"
"v_mfma_f32_16x16x32_bf16 v[76:79], a[220:223], a[180:183], v[76:79]\n"
"ds_read_b64_tr_b16 a[150:151], v245 offset:9728\n"
"v_mfma_f32_16x16x32_bf16 v[88:91], a[240:243], a[176:179], v[88:91]\n"
"ds_read_b64_tr_b16 a[152:153], v246 offset:1024\n"
"v_mfma_f32_16x16x32_bf16 v[88:91], a[244:247], a[180:183], v[88:91]\n"
"buffer_load_dwordx4 v254, s[24:27], s40 offen offset:2048 lds\n"
"v_mfma_f32_16x16x32_bf16 v[92:95], a[248:251], a[176:179], v[92:95]\n"
"s_add_u32 s49, s49, 128\n"
"ds_read_b64_tr_b16 a[154:155], v246 offset:1536\n"
"v_mfma_f32_16x16x32_bf16 v[92:95], a[252:255], a[180:183], v[92:95]\n"
"s_sub_u32 s41, s49, 1280\n"
"ds_read_b64_tr_b16 a[156:157], v246 offset:9216\n"
"v_mfma_f32_16x16x32_bf16 v[80:83], a[224:227], a[176:179], v[80:83]\n"
"s_min_u32 s49, s49, s41\n"
"ds_read_b64_tr_b16 a[158:159], v246 offset:9728\n"
"v_mfma_f32_16x16x32_bf16 v[80:83], a[228:231], a[180:183], v[80:83]\n"
"s_xor_b32 s51, s51, s74\n"
"ds_read_b128 a[168:171], v249 offset:2048\n"
"v_mfma_f32_16x16x32_bf16 v[84:87], a[232:235], a[176:179], v[84:87]\n"
"ds_read_b128 a[172:175], v250 offset:2048\n"
"v_mfma_f32_16x16x32_bf16 v[84:87], a[236:239], a[180:183], v[84:87]\n"
"ds_read_b128 a[176:179], v249 offset:4096\n"
"v_mfma_f32_16x16x32_bf16 v[108:111], a[232:235], a[184:187], v[108:111]\n"
"ds_read_b128 a[180:183], v250 offset:4096\n"
"v_mfma_f32_16x16x32_bf16 v[108:111], a[236:239], a[188:191], v[108:111]\n"
"v_mfma_f32_16x16x32_bf16 v[104:107], a[224:227], a[184:187], v[104:107]\n"
"v_mfma_f32_16x16x32_bf16 v[104:107], a[228:231], a[188:191], v[104:107]\n"
"v_mfma_f32_16x16x32_bf16 v[100:103], a[200:203], a[184:187], v[100:103]\n"
"v_mfma_f32_16x16x32_bf16 v[100:103], a[204:207], a[188:191], v[100:103]\n"
"v_mfma_f32_16x16x32_bf16 v[96:99], a[192:195], a[184:187], v[96:99]\n"
"s_add_u32 s47, s47, 1\n"
"ds_read_b128 a[184:187], v251 offset:0\n"
"v_mfma_f32_16x16x32_bf16 v[96:99], a[196:199], a[188:191], v[96:99]\n"
"s_waitcnt lgkmcnt(2)\n"
"s_cmp_lg_u32 s47, s62\n"
"ds_read_b128 a[188:191], v252 offset:0\n"
"s_cbranch_scc1 LPL_%=\n"
"s_cmp_eq_u32 s47, 18\n"
"s_cbranch_scc1 LPT2_%=\n"
"v_mfma_f32_16x16x32_bf16 v[112:115], a[96:99], a[160:163], v[112:115]\n"
"v_mfma_f32_16x16x32_bf16 v[112:115], a[100:103], a[164:167], v[112:115]\n"
"v_mfma_f32_16x16x32_bf16 v[116:119], a[104:107], a[160:163], v[116:119]\n"
"s_waitcnt vmcnt(11)\n"
"v_mfma_f32_16x16x32_bf16 v[116:119], a[108:111], a[164:167], v[116:119]\n"
"s_barrier\n"
"v_mfma_f32_16x16x32_bf16 v[120:123], a[112:115], a[160:163], v[120:123]\n"
"s_add_u32 s40, s48, 256\n"
"s_xor_b32 m0, s50, 0xc000\n"
"ds_read_b64_tr_b16 a[232:233], v244 offset:17408\n"
"v_mfma_f32_16x16x32_bf16 v[120:123], a[116:119], a[164:167], v[120:123]\n"
"buffer_load_dwordx4 v224, s[28:31], s40 offen lds\n"
"v_mfma_f32_16x16x32_bf16 v[124:127], a[120:123], a[160:163], v[124:127]\n"
"ds_read_b64_tr_b16 a[234:235], v244 offset:17920\n"
"v_mfma_f32_16x16x32_bf16 v[124:127], a[124:127], a[164:167], v[124:127]\n"
"ds_read_b64_tr_b16 a[236:237], v244 offset:25600\n"
"v_mfma_f32_16x16x32_bf16 v[128:131], a[128:131], a[160:163], v[128:131]\n"
"ds_read_b64_tr_b16 a[238:239], v244 offset:26112\n"
"v_mfma_f32_16x16x32_bf16 v[128:131], a[132:135], a[164:167], v[128:131]\n"
"ds_read_b64_tr_b16 a[224:225], v243 offset:17408\n"
"v_mfma_f32_16x16x32_bf16 v[132:135], a[136:139], a[160:163], v[132:135]\n"
"ds_read_b64_tr_b16 a[226:227], v243 offset:17920\n"
"v_mfma_f32_16x16x32_bf16 v[132:135], a[140:143], a[164:167], v[132:135]\n"
"buffer_load_dwordx4 v225, s[28:31], s40 offen offset:1024 lds\n"
"v_mfma_f32_16x16x32_bf16 v[136:139], a[144:147], a[160:163], v[136:139]\n"
"ds_read_b64_tr_b16 a[192:193], v243 offset:16384\n"
"v_mfma_f32_16x16x32_bf16 v[136:139], a[148:151], a[164:167], v[136:139]\n"
"ds_read_b64_tr_b16 a[194:195], v243 offset:16896\n"
"v_mfma_f32_16x16x32_bf16 v[140:143], a[152:155], a[160:163], v[140:143]\n"
"ds_read_b64_tr_b16 a[196:197], v243 offset:24576\n"
"v_mfma_f32_16x16x32_bf16 v[140:143], a[156:159], a[164:167], v[140:143]\n"
"s_waitcnt lgkmcnt(6)\n"
"ds_read_b64_tr_b16 a[198:199], v243 offset:25088\n"
"v_mfma_f32_16x16x32_bf16 v[220:223], a[136:139], a[184:187], v[220:223]\n"
"ds_read_b64_tr_b16 a[200:201], v244 offset:16384\n"
"v_mfma_f32_16x16x32_bf16 v[220:223], a[140:143], a[188:191], v[220:223]\n"
"buffer_load_dwordx4 v226, s[28:31], s40 offen offset:2048 lds\n"
"v_mfma_f32_16x16x32_bf16 v[216:219], a[128:131], a[184:187], v[216:219]\n"
"ds_read_b64_tr_b16 a[202:203], v244 offset:16896\n"
"v_mfma_f32_16x16x32_bf16 v[216:219], a[132:135], a[188:191], v[216:219]\n"
"ds_read_b64_tr_b16 a[204:205], v244 offset:24576\n"
"v_mfma_f32_16x16x32_bf16 v[212:215], a[104:107], a[184:187], v[212:215]\n"
"ds_read_b64_tr_b16 a[206:207], v244 offset:25088\n"
"v_mfma_f32_16x16x32_bf16 v[212:215], a[108:111], a[188:191], v[212:215]\n"
"v_pk_mul_f32 v[112:113], v[112:113], s[46:47] op_sel_hi:[1,0]\nv_pk_mul_f32 v[114:115], v[114:115], s[46:47] op_sel_hi:[1,0]\nv_pk_mul_f32 v[116:117], v[116:117], s[46:47] op_sel_hi:[1,0]\nv_pk_mul_f32 v[118:119], v[118:119], s[46:47] op_sel_hi:[1,0]\nv_pk_mul_f32 v[120:121], v[120:121], s[46:47] op_sel_hi:[1,0]\nv_pk_mul_f32 v[122:123], v[122:123], s[46:47] op_sel_hi:[1,0]\nv_pk_mul_f32 v[124:125], v[124:125], s[46:47] op_sel_hi:[1,0]\nv_pk_mul_f32 v[126:127], v[126:127], s[46:47] op_sel_hi:[1,0]\nv_pk_mul_f32 v[128:129], v[128:129], s[46:47] op_sel_hi:[1,0]\nv_pk_mul_f32 v[130:131], v[130:131], s[46:47] op_sel_hi:[1,0]\nv_pk_mul_f32 v[132:133], v[132:133], s[46:47] op_sel_hi:[1,0]\nv_pk_mul_f32 v[134:135], v[134:135], s[46:47] op_sel_hi:[1,0]\nv_pk_mul_f32 v[136:137], v[136:137], s[46:47] op_sel_hi:[1,0]\nv_pk_mul_f32 v[138:139], v[138:139], s[46:47] op_sel_hi:[1,0]\nv_pk_mul_f32 v[140:141], v[140:141], s[46:47] op_sel_hi:[1,0]\nv_pk_mul_f32 v[142:143], v[142:143], s[46:47] op_sel_hi:[1,0]\n"
"v_mfma_f32_16x16x32_bf16 v[208:211], a[96:99], a[184:187], v[208:211]\n"
"ds_read_b64_tr_b16 a[208:209], v245 offset:16384\n"
"v_cvt_pk_bf16_f32 v112, v112, v113\n"
"v_mfma_f32_16x16x32_bf16 v[208:211], a[100:103], a[188:191], v[208:211]\n"
"buffer_load_dwordx4 v227, s[28:31], s40 offen offset:3072 lds\n"
"v_mfma_f32_16x16x32_bf16 v[172:175], a[152:155], a[168:171], v[172:175]\n"
"s_cmp_eq_u32 s47, 8\ns_cselect_b32 s41, 524800, s58\n"
"ds_read_b64_tr_b16 a[210:211], v245 offset:16896\n"
"v_cvt_pk_bf16_f32 v113, v114, v115\n"
"v_mfma_f32_16x16x32_bf16 v[172:175], a[156:159], a[172:175], v[172:175]\n"
"s_add_u32 s48, s48, s41\n"
"ds_read_b64_tr_b16 a[212:213], v245 offset:24576\n"
"v_cvt_pk_bf16_f32 v114, v116, v117\n"
"v_mfma_f32_16x16x32_bf16 v[168:171], a[144:147], a[168:171], v[168:171]\n"
"s_sub_u32 s41, s48, 5242880\n"
"ds_read_b64_tr_b16 a[214:215], v245 offset:25088\n"
"v_cvt_pk_bf16_f32 v115, v118, v119\n"
"v_mfma_f32_16x16x32_bf16 v[168:171], a[148:151], a[172:175], v[168:171]\n"
"s_min_u32 s48, s48, s41\n"
"ds_read_b64_tr_b16 a[216:217], v246 offset:16384\n"
"v_cvt_pk_bf16_f32 v120, v120, v121\n"
"v_mfma_f32_16x16x32_bf16 v[164:167], a[136:139], a[168:171], v[164:167]\n"
"s_mov_b32 m0, s50\n"
"ds_read_b64_tr_b16 a[218:219], v246 offset:16896\n"
"v_cvt_pk_bf16_f32 v121, v122, v123\n"
"v_mfma_f32_16x16x32_bf16 v[164:167], a[140:143], a[172:175], v[164:167]\n"
"buffer_load_dwordx4 v224, s[28:31], s48 offen lds\n"
"v_mfma_f32_16x16x32_bf16 v[160:163], a[128:131], a[168:171], v[160:163]\n"
"ds_read_b64_tr_b16 a[220:221], v246 offset:24576\n"
"v_xor_b32 v249, s74, v249\n"
"v_mfma_f32_16x16x32_bf16 v[160:163], a[132:135], a[172:175], v[160:163]\n"
"ds_read_b64_tr_b16 a[222:223], v246 offset:25088\n"
"v_xor_b32 v250, s74, v250\n"
"v_mfma_f32_16x16x32_bf16 v[156:159], a[120:123], a[168:171], v[156:159]\n"
"ds_read_b64_tr_b16 a[228:229], v243 offset:25600\n"
"v_xor_b32 v244, s74, v244\n"
"v_mfma_f32_16x16x32_bf16 v[156:159], a[124:127], a[172:175], v[156:159]\n"
"s_mov_b64 s[80:81], s[8:9]\n"
"ds_read_b64_tr_b16 a[230:231], v243 offset:26112\n"
"v_xor_b32 v243, s74, v243\n"
"v_mfma_f32_16x16x32_bf16 v[152:155], a[112:115], a[168:171], v[152:155]\n"
"s_mov_b32 s82, s68\n"
"ds_read_b64_tr_b16 a[240:241], v245 offset:17408\n"
"v_cvt_pk_bf16_f32 v122, v124, v125\n"
"v_mfma_f32_16x16x32_bf16 v[152:155], a[116:119], a[172:175], v[152:155]\n"
"s_mov_b32 s83, 131072\n"
"buffer_load_dwordx4 v225, s[28:31], s48 offen offset:1024 lds\n"
"v_mfma_f32_16x16x32_bf16 v[148:151], a[104:107], a[168:171], v[148:151]\n"
"v_and_b32 v241, 0x3f0, v242\n"
"ds_read_b64_tr_b16 a[242:243], v245 offset:17920\n"
"v_cvt_pk_bf16_f32 v123, v126, v127\n"
"v_mfma_f32_16x16x32_bf16 v[148:151], a[108:111], a[172:175], v[148:151]\n"
"v_mul_u32_u24 v241, 10, v241\n"
"ds_read_b64_tr_b16 a[244:245], v245 offset:25600\n"
"v_cvt_pk_bf16_f32 v128, v128, v129\n"
"v_mfma_f32_16x16x32_bf16 v[144:147], a[96:99], a[168:171], v[144:147]\n"
"ds_read_b64_tr_b16 a[246:247], v245 offset:26112\n"
"v_xor_b32 v245, s74, v245\n"
"v_mfma_f32_16x16x32_bf16 v[144:147], a[100:103], a[172:175], v[144:147]\n"
"ds_read_b64_tr_b16 a[248:249], v246 offset:17408\n"
"v_cvt_pk_bf16_f32 v129, v130, v131\n"
"v_mfma_f32_16x16x32_bf16 v[176:179], a[96:99], a[176:179], v[176:179]\n"
"ds_read_b64_tr_b16 a[250:251], v246 offset:17920\n"
"v_cvt_pk_bf16_f32 v130, v132, v133\n"
"v_mfma_f32_16x16x32_bf16 v[176:179], a[100:103], a[180:183], v[176:179]\n"
"buffer_load_dwordx4 v226, s[28:31], s48 offen offset:2048 lds\n"
"v_mfma_f32_16x16x32_bf16 v[180:183], a[104:107], a[176:179], v[180:183]\n"
"ds_read_b64_tr_b16 a[252:253], v246 offset:25600\n"
"v_cvt_pk_bf16_f32 v131, v134, v135\n"
"v_mfma_f32_16x16x32_bf16 v[180:183], a[108:111], a[180:183], v[180:183]\n"
"ds_read_b64_tr_b16 a[254:255], v246 offset:26112\n"
"v_xor_b32 v246, s74, v246\n"
"v_mfma_f32_16x16x32_bf16 v[184:187], a[112:115], a[176:179], v[184:187]\n"
"v_permlane16_swap_b32 v112, v114\n"
"v_mfma_f32_16x16x32_bf16 v[184:187], a[116:119], a[180:183], v[184:187]\n"
"v_permlane16_swap_b32 v113, v115\n"
"v_mfma_f32_16x16x32_bf16 v[188:191], a[120:123], a[176:179], v[188:191]\n"
"v_permlane16_swap_b32 v120, v122\n"
"v_mfma_f32_16x16x32_bf16 v[188:191], a[124:127], a[180:183], v[188:191]\n"
"buffer_load_dwordx4 v227, s[28:31], s48 offen offset:3072 lds\n"
"v_mfma_f32_16x16x32_bf16 v[200:203], a[144:147], a[176:179], v[200:203]\n"
"s_xor_b32 s50, s50, s74\n"
"v_permlane16_swap_b32 v121, v123\n"
"v_mfma_f32_16x16x32_bf16 v[200:203], a[148:151], a[180:183], v[200:203]\n"
"v_permlane16_swap_b32 v128, v130\n"
"v_mfma_f32_16x16x32_bf16 v[204:207], a[152:155], a[176:179], v[204:207]\n"
"v_permlane16_swap_b32 v129, v131\n"
"v_mfma_f32_16x16x32_bf16 v[204:207], a[156:159], a[180:183], v[204:207]\n"
"v_pk_mul_f32 v[144:145], v[144:145], s[46:47] op_sel_hi:[1,0]\nv_pk_mul_f32 v[146:147], v[146:147], s[46:47] op_sel_hi:[1,0]\nv_pk_mul_f32 v[148:149], v[148:149], s[46:47] op_sel_hi:[1,0]\nv_pk_mul_f32 v[150:151], v[150:151], s[46:47] op_sel_hi:[1,0]\nv_pk_mul_f32 v[152:153], v[152:153], s[46:47] op_sel_hi:[1,0]\nv_pk_mul_f32 v[154:155], v[154:155], s[46:47] op_sel_hi:[1,0]\nv_pk_mul_f32 v[156:157], v[156:157], s[46:47] op_sel_hi:[1,0]\nv_pk_mul_f32 v[158:159], v[158:159], s[46:47] op_sel_hi:[1,0]\nv_pk_mul_f32 v[160:161], v[160:161], s[46:47] op_sel_hi:[1,0]\nv_pk_mul_f32 v[162:163], v[162:163], s[46:47] op_sel_hi:[1,0]\nv_pk_mul_f32 v[164:165], v[164:165], s[46:47] op_sel_hi:[1,0]\nv_pk_mul_f32 v[166:167], v[166:167], s[46:47] op_sel_hi:[1,0]\nv_pk_mul_f32 v[168:169], v[168:169], s[46:47] op_sel_hi:[1,0]\nv_pk_mul_f32 v[170:171], v[170:171], s[46:47] op_sel_hi:[1,0]\nv_pk_mul_f32 v[172:173], v[172:173], s[46:47] op_sel_hi:[1,0]\nv_pk_mul_f32 v[174:175], v[174:175], s[46:47] op_sel_hi:[1,0]\n"
"v_mfma_f32_16x16x32_bf16 v[192:195], a[128:131], a[176:179], v[192:195]\n"
"v_pk_mul_f32 v[208:209], v[208:209], s[46:47] op_sel_hi:[1,0]\nv_pk_mul_f32 v[210:211], v[210:211], s[46:47] op_sel_hi:[1,0]\nv_pk_mul_f32 v[212:213], v[212:213], s[46:47] op_sel_hi:[1,0]\nv_pk_mul_f32 v[214:215], v[214:215], s[46:47] op_sel_hi:[1,0]\nv_pk_mul_f32 v[216:217], v[216:217], s[46:47] op_sel_hi:[1,0]\nv_pk_mul_f32 v[218:219], v[218:219], s[46:47] op_sel_hi:[1,0]\nv_pk_mul_f32 v[220:221], v[220:221], s[46:47] op_sel_hi:[1,0]\nv_pk_mul_f32 v[222:223], v[222:223], s[46:47] op_sel_hi:[1,0]\n"
"v_mfma_f32_16x16x32_bf16 v[192:195], a[132:135], a[180:183], v[192:195]\n"
"v_accvgpr_write_b32 a0, v112\n"
"v_accvgpr_write_b32 a1, v113\n"
"v_mfma_f32_16x16x32_bf16 v[196:199], a[136:139], a[176:179], v[196:199]\n"
"v_accvgpr_write_b32 a2, v114\n"
"v_accvgpr_write_b32 a3, v115\n"
"v_mfma_f32_16x16x32_bf16 v[196:199], a[140:143], a[180:183], v[196:199]\n"
"v_accvgpr_write_b32 a4, v120\n"
"v_accvgpr_write_b32 a5, v121\n"
"v_mfma_f32_16x16x32_bf16 v[108:111], a[232:235], a[184:187], v[108:111]\n"
"s_waitcnt lgkmcnt(0)\n"
"v_accvgpr_write_b32 a6, v122\n"
"v_accvgpr_write_b32 a7, v123\n"
"v_mfma_f32_16x16x32_bf16 v[108:111], a[236:239], a[188:191], v[108:111]\n"
"v_accvgpr_write_b32 a8, v128\n"
"v_accvgpr_write_b32 a9, v129\n"
"v_mfma_f32_16x16x32_bf16 v[104:107], a[224:227], a[184:187], v[104:107]\n"
"s_waitcnt vmcnt(8)\n"
"v_accvgpr_write_b32 a10, v130\n"
"v_accvgpr_write_b32 a11, v131\n"
"v_mfma_f32_16x16x32_bf16 v[104:107], a[228:231], a[188:191], v[104:107]\n"
"s_barrier\n"
"v_mfma_f32_16x16x32_bf16 v[100:103], a[200:203], a[184:187], v[100:103]\n"
"s_mov_b32 m0, s51\n"
"ds_read_b64_tr_b16 a[96:97], v243 offset:0\n"
"v_cvt_pk_bf16_f32 v136, v136, v137\n"
"v_mfma_f32_16x16x32_bf16 v[100:103], a[204:207], a[188:191], v[100:103]\n"
"buffer_load_dwordx4 v247, s[24:27], s49 offen lds\n"
"v_mfma_f32_16x16x32_bf16 v[96:99], a[192:195], a[184:187], v[96:99]\n"
"ds_read_b64_tr_b16 a[98:99], v243 offset:512\n"
"v_cvt_pk_bf16_f32 v137, v138, v139\n"
"v_mfma_f32_16x16x32_bf16 v[96:99], a[196:199], a[188:191], v[96:99]\n"
"ds_read_b64_tr_b16 a[100:101], v243 offset:8192\n"
"v_cvt_pk_bf16_f32 v138, v140, v141\n"
"v_mfma_f32_16x16x32_bf16 v[0:3], a[192:195], a[160:163], v[0:3]\n"
"ds_read_b64_tr_b16 a[102:103], v243 offset:8704\n"
"v_cvt_pk_bf16_f32 v139, v142, v143\n"
"v_mfma_f32_16x16x32_bf16 v[0:3], a[196:199], a[164:167], v[0:3]\n"
"ds_read_b64_tr_b16 a[104:105], v244 offset:0\n"
"v_cvt_pk_bf16_f32 v168, v168, v169\n"
"v_mfma_f32_16x16x32_bf16 v[4:7], a[200:203], a[160:163], v[4:7]\n"
"ds_read_b64_tr_b16 a[106:107], v244 offset:512\n"
"v_cvt_pk_bf16_f32 v169, v170, v171\n"
"v_mfma_f32_16x16x32_bf16 v[4:7], a[204:207], a[164:167], v[4:7]\n"
"buffer_load_dwordx4 v253, s[24:27], s49 offen offset:1024 lds\n"
"v_mfma_f32_16x16x32_bf16 v[8:11], a[208:211], a[160:163], v[8:11]\n"
"ds_read_b64_tr_b16 a[108:109], v244 offset:8192\n"
"v_cvt_pk_bf16_f32 v170, v172, v173\n"
"v_mfma_f32_16x16x32_bf16 v[8:11], a[212:215], a[164:167], v[8:11]\n"
"ds_read_b64_tr_b16 a[110:111], v244 offset:8704\n"
"v_cvt_pk_bf16_f32 v171, v174, v175\n"
"v_mfma_f32_16x16x32_bf16 v[12:15], a[216:219], a[160:163], v[12:15]\n"
"ds_read_b64_tr_b16 a[112:113], v245 offset:0\n"
"v_cvt_pk_bf16_f32 v160, v160, v161\n"
"v_mfma_f32_16x16x32_bf16 v[12:15], a[220:223], a[164:167], v[12:15]\n"
"buffer_load_dwordx4 v254, s[24:27], s49 offen offset:2048 lds\n"
"v_mfma_f32_16x16x32_bf16 v[16:19], a[224:227], a[160:163], v[16:19]\n"
"ds_read_b64_tr_b16 a[114:115], v245 offset:512\n"
"v_cvt_pk_bf16_f32 v161, v162, v163\n"
"v_mfma_f32_16x16x32_bf16 v[16:19], a[228:231], a[164:167], v[16:19]\n"
"ds_read_b64_tr_b16 a[116:117], v245 offset:8192\n"
"v_cvt_pk_bf16_f32 v162, v164, v165\n"
"v_mfma_f32_16x16x32_bf16 v[20:23], a[232:235], a[160:163], v[20:23]\n"
"ds_read_b64_tr_b16 a[118:119], v245 offset:8704\n"
"v_cvt_pk_bf16_f32 v163, v166, v167\n"
"v_mfma_f32_16x16x32_bf16 v[20:23], a[236:239], a[164:167], v[20:23]\n"
"ds_read_b64_tr_b16 a[120:121], v246 offset:0\n"
"v_cvt_pk_bf16_f32 v152, v152, v153\n"
"v_mfma_f32_16x16x32_bf16 v[24:27], a[240:243], a[160:163], v[24:27]\n"
"ds_read_b64_tr_b16 a[122:123], v246 offset:512\n"
"v_cvt_pk_bf16_f32 v153, v154, v155\n"
"v_mfma_f32_16x16x32_bf16 v[24:27], a[244:247], a[164:167], v[24:27]\n"
"buffer_load_dwordx4 v255, s[24:27], s49 offen offset:3072 lds\n"
"v_mfma_f32_16x16x32_bf16 v[28:31], a[248:251], a[160:163], v[28:31]\n"
"ds_read_b128 a[160:163], v249 offset:0\n"
"v_cvt_pk_bf16_f32 v154, v156, v157\n"
"v_mfma_f32_16x16x32_bf16 v[28:31], a[252:255], a[164:167], v[28:31]\n"
"ds_read_b128 a[164:167], v250 offset:0\n"
"v_cvt_pk_bf16_f32 v155, v158, v159\n"
"v_mfma_f32_16x16x32_bf16 v[60:63], a[248:251], a[168:171], v[60:63]\n"
"ds_read_b64_tr_b16 a[124:125], v246 offset:8192\n"
"v_cvt_pk_bf16_f32 v144, v144, v145\n"
"v_mfma_f32_16x16x32_bf16 v[60:63], a[252:255], a[172:175], v[60:63]\n"
"s_add_u32 s40, s49, 40960\n"
"ds_read_b64_tr_b16 a[126:127], v246 offset:8704\n"
"v_cvt_pk_bf16_f32 v145, v146, v147\n"
"v_mfma_f32_16x16x32_bf16 v[56:59], a[240:243], a[168:171], v[56:59]\n"
"s_add_u32 m0, s51, 4096\n"
"ds_read_b64_tr_b16 a[128:129], v243 offset:1024\n"
"v_cvt_pk_bf16_f32 v146, v148, v149\n"
"v_mfma_f32_16x16x32_bf16 v[56:59], a[244:247], a[172:175], v[56:59]\n"
"buffer_load_dwordx4 v247, s[24:27], s40 offen lds\n"
"v_mfma_f32_16x16x32_bf16 v[52:55], a[232:235], a[168:171], v[52:55]\n"
"ds_read_b64_tr_b16 a[130:131], v243 offset:1536\n"
"v_cvt_pk_bf16_f32 v147, v150, v151\n"
"v_mfma_f32_16x16x32_bf16 v[52:55], a[236:239], a[172:175], v[52:55]\n"
"ds_read_b64_tr_b16 a[132:133], v243 offset:9216\n"
"v_xor_b32 v251, s74, v251\n"
"v_mfma_f32_16x16x32_bf16 v[48:51], a[224:227], a[168:171], v[48:51]\n"
"ds_read_b64_tr_b16 a[134:135], v243 offset:9728\n"
"v_cvt_pk_bf16_f32 v216, v216, v217\n"
"v_mfma_f32_16x16x32_bf16 v[48:51], a[228:231], a[172:175], v[48:51]\n"
"ds_read_b64_tr_b16 a[136:137], v244 offset:1024\n"
"v_cvt_pk_bf16_f32 v217, v218, v219\n"
"v_mfma_f32_16x16x32_bf16 v[44:47], a[216:219], a[168:171], v[44:47]\n"
"ds_read_b64_tr_b16 a[138:139], v244 offset:1536\n"
"v_cvt_pk_bf16_f32 v218, v220, v221\n"
"v_mfma_f32_16x16x32_bf16 v[44:47], a[220:223], a[172:175], v[44:47]\n"
"buffer_load_dwordx4 v253, s[24:27], s40 offen offset:1024 lds\n"
"v_mfma_f32_16x16x32_bf16 v[40:43], a[208:211], a[168:171], v[40:43]\n"
"ds_read_b64_tr_b16 a[140:141], v244 offset:9216\n"
"v_cvt_pk_bf16_f32 v219, v222, v223\n"
"v_mfma_f32_16x16x32_bf16 v[40:43], a[212:215], a[172:175], v[40:43]\n"
"ds_read_b64_tr_b16 a[142:143], v244 offset:9728\n"
"v_xor_b32 v252, s74, v252\n"
"v_mfma_f32_16x16x32_bf16 v[36:39], a[200:203], a[168:171], v[36:39]\n"
"ds_read_b64_tr_b16 a[144:145], v245 offset:1024\n"
"v_cvt_pk_bf16_f32 v208, v208, v209\n"
"v_mfma_f32_16x16x32_bf16 v[36:39], a[204:207], a[172:175], v[36:39]\n"
"buffer_load_dwordx4 v254, s[24:27], s40 offen offset:2048 lds\n"
"v_mfma_f32_16x16x32_bf16 v[32:35], a[192:195], a[168:171], v[32:35]\n"
"s_add_u32 s49, s49, 128\n"
"ds_read_b64_tr_b16 a[146:147], v245 offset:1536\n"
"v_cvt_pk_bf16_f32 v209, v210, v211\n"
"v_mfma_f32_16x16x32_bf16 v[32:35], a[196:199], a[172:175], v[32:35]\n"
"s_sub_u32 s41, s49, 1280\n"
"ds_read_b64_tr_b16 a[148:149], v245 offset:9216\n"
"v_cvt_pk_bf16_f32 v210, v212, v213\n"
"v_mfma_f32_16x16x32_bf16 v[64:67], a[192:195], a[176:179], v[64:67]\n"
"s_min_u32 s49, s49, s41\n"
"ds_read_b64_tr_b16 a[150:151], v245 offset:9728\n"
"v_cvt_pk_bf16_f32 v211, v214, v215\n"
"v_mfma_f32_16x16x32_bf16 v[64:67], a[196:199], a[180:183], v[64:67]\n"
"s_xor_b32 s51, s51, s74\n"
"v_permlane16_swap_b32 v136, v138\n"
"v_mfma_f32_16x16x32_bf16 v[68:71], a[200:203], a[176:179], v[68:71]\n"
"s_mov_b32 m0, 139264\n"
"ds_read_b64_tr_b16 a[152:153], v246 offset:1024\n"
"v_accvgpr_write_b32 a12, v136\n"
"v_mfma_f32_16x16x32_bf16 v[68:71], a[204:207], a[180:183], v[68:71]\n"
"buffer_load_dword v241, s[80:83], s63 offen lds\n"
"v_mfma_f32_16x16x32_bf16 v[72:75], a[208:211], a[176:179], v[72:75]\n"
"ds_read_b64_tr_b16 a[154:155], v246 offset:1536\n"
"v_accvgpr_write_b32 a14, v138\n"
"v_mfma_f32_16x16x32_bf16 v[72:75], a[212:215], a[180:183], v[72:75]\n"
"v_permlane16_swap_b32 v137, v139\n"
"v_mfma_f32_16x16x32_bf16 v[76:79], a[216:219], a[176:179], v[76:79]\n"
"ds_read_b64_tr_b16 a[156:157], v246 offset:9216\n"
"v_accvgpr_write_b32 a13, v137\n"
"v_mfma_f32_16x16x32_bf16 v[76:79], a[220:223], a[180:183], v[76:79]\n"
"ds_read_b64_tr_b16 a[158:159], v246 offset:9728\n"
"v_accvgpr_write_b32 a15, v139\n"
"v_mfma_f32_16x16x32_bf16 v[88:91], a[240:243], a[176:179], v[88:91]\n"
"v_permlane16_swap_b32 v168, v170\n"
"v_mfma_f32_16x16x32_bf16 v[88:91], a[244:247], a[180:183], v[88:91]\n"
"buffer_load_dword v241, s[80:83], s63 offen offset:80 lds\n"
"v_mfma_f32_16x16x32_bf16 v[92:95], a[248:251], a[176:179], v[92:95]\n"
"ds_read_b128 a[168:171], v249 offset:2048\n"
"v_accvgpr_write_b32 a28, v168\n"
"v_mfma_f32_16x16x32_bf16 v[92:95], a[252:255], a[180:183], v[92:95]\n"
"ds_read_b128 a[172:175], v250 offset:2048\n"
"v_accvgpr_write_b32 a30, v170\n"
"v_mfma_f32_16x16x32_bf16 v[80:83], a[224:227], a[176:179], v[80:83]\n"
"v_permlane16_swap_b32 v169, v171\n"
"v_mfma_f32_16x16x32_bf16 v[80:83], a[228:231], a[180:183], v[80:83]\n"
"ds_read_b128 a[184:187], v251 offset:0\n"
"v_accvgpr_write_b32 a29, v169\n"
"v_mfma_f32_16x16x32_bf16 v[84:87], a[232:235], a[176:179], v[84:87]\n"
"s_add_u32 s47, s47, 1\n"
"ds_read_b128 a[176:179], v249 offset:4096\n"
"v_accvgpr_write_b32 a31, v171\n"
"v_mfma_f32_16x16x32_bf16 v[84:87], a[236:239], a[180:183], v[84:87]\n"
"s_waitcnt lgkmcnt(4)\n"
"v_permlane16_swap_b32 v160, v162\n"
"v_mfma_f32_16x16x32_bf16 v[112:115], a[96:99], a[160:163], 0\n"
"ds_read_b128 a[180:183], v250 offset:4096\n"
"v_accvgpr_write_b32 a24, v160\n"
"v_mfma_f32_16x16x32_bf16 v[112:115], a[100:103], a[164:167], v[112:115]\n"
"ds_read_b128 a[188:191], v252 offset:0\n"
"v_accvgpr_write_b32 a26, v162\n"
"v_mfma_f32_16x16x32_bf16 v[116:119], a[104:107], a[160:163], 0\n"
"s_waitcnt vmcnt(13)\n"
"v_permlane16_swap_b32 v161, v163\n"
"v_mfma_f32_16x16x32_bf16 v[116:119], a[108:111], a[164:167], v[116:119]\n"
"s_barrier\n"
"v_mfma_f32_16x16x32_bf16 v[120:123], a[112:115], a[160:163], 0\n"
"s_add_u32 s40, s48, 256\n"
"s_xor_b32 m0, s50, 0xc000\n"
"ds_read_b64_tr_b16 a[192:193], v243 offset:16384\n"
"v_mfma_f32_16x16x32_bf16 v[120:123], a[116:119], a[164:167], v[120:123]\n"
"buffer_load_dwordx4 v224, s[28:31], s40 offen lds\n"
"v_mfma_f32_16x16x32_bf16 v[124:127], a[120:123], a[160:163], 0\n"
"ds_read_b64_tr_b16 a[194:195], v243 offset:16896\n"
"v_accvgpr_write_b32 a25, v161\n"
"v_mfma_f32_16x16x32_bf16 v[124:127], a[124:127], a[164:167], v[124:127]\n"
"ds_read_b64_tr_b16 a[196:197], v243 offset:24576\n"
"v_accvgpr_write_b32 a27, v163\n"
"v_mfma_f32_16x16x32_bf16 v[128:131], a[128:131], a[160:163], 0\n"
"v_permlane16_swap_b32 v152, v154\n"
"v_mfma_f32_16x16x32_bf16 v[128:131], a[132:135], a[164:167], v[128:131]\n"
"ds_read_b64_tr_b16 a[198:199], v243 offset:25088\n"
"v_accvgpr_write_b32 a20, v152\n"
"v_mfma_f32_16x16x32_bf16 v[132:135], a[136:139], a[160:163], 0\n"
"ds_read_b64_tr_b16 a[200:201], v244 offset:16384\n"
"v_accvgpr_write_b32 a22, v154\n"
"v_mfma_f32_16x16x32_bf16 v[132:135], a[140:143], a[164:167], v[132:135]\n"
"buffer_load_dwordx4 v225, s[28:31], s40 offen offset:1024 lds\n"
"v_mfma_f32_16x16x32_bf16 v[136:139], a[144:147], a[160:163], 0\n"
"v_permlane16_swap_b32 v153, v155\n"
"v_mfma_f32_16x16x32_bf16 v[136:139], a[148:151], a[164:167], v[136:139]\n"
"ds_read_b64_tr_b16 a[202:203], v244 offset:16896\n"
"v_accvgpr_write_b32 a21, v153\n"
"v_mfma_f32_16x16x32_bf16 v[140:143], a[152:155], a[160:163], 0\n"
"ds_read_b64_tr_b16 a[204:205], v244 offset:24576\n"
"v_accvgpr_write_b32 a23, v155\n"
"v_mfma_f32_16x16x32_bf16 v[140:143], a[156:159], a[164:167], v[140:143]\n"
"s_waitcnt lgkmcnt(4)\n"
"v_permlane16_swap_b32 v144, v146\n"
"v_mfma_f32_16x16x32_bf16 v[172:175], a[152:155], a[168:171], 0\n"
"ds_read_b64_tr_b16 a[206:207], v244 offset:25088\n"
"v_accvgpr_write_b32 a16, v144\n"
"v_mfma_f32_16x16x32_bf16 v[172:175], a[156:159], a[172:175], v[172:175]\n"
"buffer_load_dwordx4 v226, s[28:31], s40 offen offset:2048 lds\n"
"v_mfma_f32_16x16x32_bf16 v[168:171], a[144:147], a[168:171], 0\n"
"ds_read_b64_tr_b16 a[208:209], v245 offset:16384\n"
"v_accvgpr_write_b32 a18, v146\n"
"v_mfma_f32_16x16x32_bf16 v[168:171], a[148:151], a[172:175], v[168:171]\n"
"v_permlane16_swap_b32 v145, v147\n"
"v_mfma_f32_16x16x32_bf16 v[164:167], a[136:139], a[168:171], 0\n"
"ds_read_b64_tr_b16 a[210:211], v245 offset:16896\n"
"v_accvgpr_write_b32 a17, v145\n"
"v_mfma_f32_16x16x32_bf16 v[164:167], a[140:143], a[172:175], v[164:167]\n"
"ds_read_b64_tr_b16 a[212:213], v245 offset:24576\n"
"v_accvgpr_write_b32 a19, v147\n"
"v_mfma_f32_16x16x32_bf16 v[160:163], a[128:131], a[168:171], 0\n"
"v_pk_mul_f32 v[176:177], v[176:177], s[46:47] op_sel_hi:[1,0]\nv_pk_mul_f32 v[178:179], v[178:179], s[46:47] op_sel_hi:[1,0]\nv_pk_mul_f32 v[180:181], v[180:181], s[46:47] op_sel_hi:[1,0]\nv_pk_mul_f32 v[182:183], v[182:183], s[46:47] op_sel_hi:[1,0]\nv_pk_mul_f32 v[184:185], v[184:185], s[46:47] op_sel_hi:[1,0]\nv_pk_mul_f32 v[186:187], v[186:187], s[46:47] op_sel_hi:[1,0]\nv_pk_mul_f32 v[188:189], v[188:189], s[46:47] op_sel_hi:[1,0]\nv_pk_mul_f32 v[190:191], v[190:191], s[46:47] op_sel_hi:[1,0]\nv_pk_mul_f32 v[192:193], v[192:193], s[46:47] op_sel_hi:[1,0]\nv_pk_mul_f32 v[194:195], v[194:195], s[46:47] op_sel_hi:[1,0]\nv_pk_mul_f32 v[196:197], v[196:197], s[46:47] op_sel_hi:[1,0]\nv_pk_mul_f32 v[198:199], v[198:199], s[46:47] op_sel_hi:[1,0]\nv_pk_mul_f32 v[200:201], v[200:201], s[46:47] op_sel_hi:[1,0]\nv_pk_mul_f32 v[202:203], v[202:203], s[46:47] op_sel_hi:[1,0]\nv_pk_mul_f32 v[204:205], v[204:205], s[46:47] op_sel_hi:[1,0]\nv_pk_mul_f32 v[206:207], v[206:207], s[46:47] op_sel_hi:[1,0]\n"
"v_mfma_f32_16x16x32_bf16 v[160:163], a[132:135], a[172:175], v[160:163]\n"
"buffer_load_dwordx4 v227, s[28:31], s40 offen offset:3072 lds\n"
"v_mfma_f32_16x16x32_bf16 v[156:159], a[120:123], a[168:171], 0\n"
"s_cmp_eq_u32 s47, 8\ns_cselect_b32 s41, 524800, s58\n"
"ds_read_b64_tr_b16 a[214:215], v245 offset:25088\n"
"v_cvt_pk_bf16_f32 v176, v176, v177\n"
"v_mfma_f32_16x16x32_bf16 v[156:159], a[124:127], a[172:175], v[156:159]\n"
"s_add_u32 s48, s48, s41\n"
"ds_read_b64_tr_b16 a[216:217], v246 offset:16384\n"
"v_cvt_pk_bf16_f32 v177, v178, v179\n"
"v_mfma_f32_16x16x32_bf16 v[152:155], a[112:115], a[168:171], 0\n"
"s_sub_u32 s41, s48, 5242880\n"
"ds_read_b64_tr_b16 a[218:219], v246 offset:16896\n"
"v_cvt_pk_bf16_f32 v178, v180, v181\n"
"v_mfma_f32_16x16x32_bf16 v[152:155], a[116:119], a[172:175], v[152:155]\n"
"s_min_u32 s48, s48, s41\n"
"ds_read_b64_tr_b16 a[220:221], v246 offset:24576\n"
"v_cvt_pk_bf16_f32 v179, v182, v183\n"
"v_mfma_f32_16x16x32_bf16 v[148:151], a[104:107], a[168:171], 0\n"
"s_mov_b32 m0, s50\n"
"v_permlane16_swap_b32 v176, v178\n"
"v_mfma_f32_16x16x32_bf16 v[148:151], a[108:111], a[172:175], v[148:151]\n"
"buffer_load_dwordx4 v224, s[28:31], s48 offen lds\n"
"v_mfma_f32_16x16x32_bf16 v[144:147], a[96:99], a[168:171], 0\n"
"v_permlane16_swap_b32 v177, v179\n"
"v_mfma_f32_16x16x32_bf16 v[144:147], a[100:103], a[172:175], v[144:147]\n"
"v_accvgpr_write_b32 a32, v176\n"
"v_accvgpr_write_b32 a33, v177\n"
"v_accvgpr_write_b32 a34, v178\n"
"v_accvgpr_write_b32 a35, v179\n"
"v_mfma_f32_16x16x32_bf16 v[176:179], a[96:99], a[176:179], 0\n"
"v_permlane16_swap_b32 v216, v218\n"
"v_permlane16_swap_b32 v217, v219\n"
"v_mfma_f32_16x16x32_bf16 v[176:179], a[100:103], a[180:183], v[176:179]\n"
"ds_write_b128 v248, v[216:219] offset:2048\n"
"v_mfma_f32_16x16x32_bf16 v[180:183], a[104:107], a[176:179], 0\n"
"v_cvt_pk_bf16_f32 v184, v184, v185\n"
"v_cvt_pk_bf16_f32 v185, v186, v187\n"
"v_cvt_pk_bf16_f32 v186, v188, v189\n"
"v_cvt_pk_bf16_f32 v187, v190, v191\n"
"v_mfma_f32_16x16x32_bf16 v[180:183], a[108:111], a[180:183], v[180:183]\n"
"buffer_load_dwordx4 v225, s[28:31], s48 offen offset:1024 lds\n"
"v_permlane16_swap_b32 v184, v186\n"
"v_permlane16_swap_b32 v185, v187\n"
"v_accvgpr_write_b32 a36, v184\n"
"v_accvgpr_write_b32 a37, v185\n"
"v_accvgpr_write_b32 a38, v186\n"
"v_accvgpr_write_b32 a39, v187\n"
"v_mfma_f32_16x16x32_bf16 v[184:187], a[112:115], a[176:179], 0\n"
"v_permlane16_swap_b32 v208, v210\n"
"v_mfma_f32_16x16x32_bf16 v[184:187], a[116:119], a[180:183], v[184:187]\n"
"v_permlane16_swap_b32 v209, v211\n"
"v_mfma_f32_16x16x32_bf16 v[188:191], a[120:123], a[176:179], 0\n"
"ds_write_b128 v248, v[208:211] offset:0\n"
"v_cvt_pk_bf16_f32 v200, v200, v201\n"
"v_cvt_pk_bf16_f32 v201, v202, v203\n"
"v_cvt_pk_bf16_f32 v202, v204, v205\n"
"v_cvt_pk_bf16_f32 v203, v206, v207\n"
"v_mfma_f32_16x16x32_bf16 v[188:191], a[124:127], a[180:183], v[188:191]\n"
"v_permlane16_swap_b32 v200, v202\n"
"v_permlane16_swap_b32 v201, v203\n"
"v_accvgpr_write_b32 a44, v200\n"
"v_accvgpr_write_b32 a45, v201\n"
"v_accvgpr_write_b32 a46, v202\n"
"v_accvgpr_write_b32 a47, v203\n"
"v_mfma_f32_16x16x32_bf16 v[200:203], a[144:147], a[176:179], 0\n"
"ds_read_b64_tr_b16 a[222:223], v246 offset:25088\n"
"v_cvt_pk_bf16_f32 v192, v192, v193\n"
"v_mfma_f32_16x16x32_bf16 v[200:203], a[148:151], a[180:183], v[200:203]\n"
"buffer_load_dwordx4 v226, s[28:31], s48 offen offset:2048 lds\n"
"v_mfma_f32_16x16x32_bf16 v[204:207], a[152:155], a[176:179], 0\n"
"ds_read_b64_tr_b16 a[224:225], v243 offset:17408\n"
"v_cvt_pk_bf16_f32 v193, v194, v195\n"
"v_cvt_pk_bf16_f32 v194, v196, v197\n"
"v_cvt_pk_bf16_f32 v195, v198, v199\n"
"v_mfma_f32_16x16x32_bf16 v[204:207], a[156:159], a[180:183], v[204:207]\n"
"v_permlane16_swap_b32 v192, v194\n"
"v_permlane16_swap_b32 v193, v195\n"
"v_accvgpr_write_b32 a40, v192\n"
"v_accvgpr_write_b32 a41, v193\n"
"v_accvgpr_write_b32 a42, v194\n"
"v_accvgpr_write_b32 a43, v195\n"
"v_mfma_f32_16x16x32_bf16 v[192:195], a[128:131], a[176:179], 0\n"
"ds_read_b64_tr_b16 a[226:227], v243 offset:17920\n"
"v_xor_b32 v249, s74, v249\n"
"v_mfma_f32_16x16x32_bf16 v[192:195], a[132:135], a[180:183], v[192:195]\n"
"ds_read_b64_tr_b16 a[228:229], v243 offset:25600\n"
"v_xor_b32 v250, s74, v250\n"
"v_mfma_f32_16x16x32_bf16 v[196:199], a[136:139], a[176:179], 0\n"
"ds_read_b64_tr_b16 a[230:231], v243 offset:26112\n"
"v_xor_b32 v243, s74, v243\n"
"v_mfma_f32_16x16x32_bf16 v[196:199], a[140:143], a[180:183], v[196:199]\n"
"buffer_load_dwordx4 v227, s[28:31], s48 offen offset:3072 lds\n"
"v_mfma_f32_16x16x32_bf16 v[220:223], a[136:139], a[184:187], 0\n"
"s_xor_b32 s50, s50, s74\n"
"ds_read_b64_tr_b16 a[232:233], v244 offset:17408\n"
"ds_read_b64_tr_b16 a[234:235], v244 offset:17920\n"
"v_mfma_f32_16x16x32_bf16 v[220:223], a[140:143], a[188:191], v[220:223]\n"
"ds_read_b64_tr_b16 a[236:237], v244 offset:25600\n"
"ds_read_b64_tr_b16 a[238:239], v244 offset:26112\n"
"v_mfma_f32_16x16x32_bf16 v[216:219], a[128:131], a[184:187], 0\n"
"ds_read_b64_tr_b16 a[240:241], v245 offset:17408\n"
"ds_read_b64_tr_b16 a[242:243], v245 offset:17920\n"
"v_mfma_f32_16x16x32_bf16 v[216:219], a[132:135], a[188:191], v[216:219]\n"
"ds_read_b64_tr_b16 a[244:245], v245 offset:25600\n"
"ds_read_b64_tr_b16 a[246:247], v245 offset:26112\n"
"v_mfma_f32_16x16x32_bf16 v[212:215], a[104:107], a[184:187], 0\n"
"ds_read_b64_tr_b16 a[248:249], v246 offset:17408\n"
"ds_read_b64_tr_b16 a[250:251], v246 offset:17920\n"
"v_mfma_f32_16x16x32_bf16 v[212:215], a[108:111], a[188:191], v[212:215]\n"
"ds_read_b64_tr_b16 a[252:253], v246 offset:25600\n"
"ds_read_b64_tr_b16 a[254:255], v246 offset:26112\n"
"v_mfma_f32_16x16x32_bf16 v[208:211], a[96:99], a[184:187], 0\n"
"v_pk_mul_f32 v[0:1], v[0:1], s[46:47] op_sel_hi:[1,0]\nv_pk_mul_f32 v[2:3], v[2:3], s[46:47] op_sel_hi:[1,0]\nv_pk_mul_f32 v[4:5], v[4:5], s[46:47] op_sel_hi:[1,0]\nv_pk_mul_f32 v[6:7], v[6:7], s[46:47] op_sel_hi:[1,0]\nv_pk_mul_f32 v[8:9], v[8:9], s[46:47] op_sel_hi:[1,0]\nv_pk_mul_f32 v[10:11], v[10:11], s[46:47] op_sel_hi:[1,0]\nv_pk_mul_f32 v[12:13], v[12:13], s[46:47] op_sel_hi:[1,0]\nv_pk_mul_f32 v[14:15], v[14:15], s[46:47] op_sel_hi:[1,0]\nv_pk_mul_f32 v[16:17], v[16:17], s[46:47] op_sel_hi:[1,0]\nv_pk_mul_f32 v[18:19], v[18:19], s[46:47] op_sel_hi:[1,0]\nv_pk_mul_f32 v[20:21], v[20:21], s[46:47] op_sel_hi:[1,0]\nv_pk_mul_f32 v[22:23], v[22:23], s[46:47] op_sel_hi:[1,0]\nv_pk_mul_f32 v[24:25], v[24:25], s[46:47] op_sel_hi:[1,0]\nv_pk_mul_f32 v[26:27], v[26:27], s[46:47] op_sel_hi:[1,0]\nv_pk_mul_f32 v[28:29], v[28:29], s[46:47] op_sel_hi:[1,0]\nv_pk_mul_f32 v[30:31], v[30:31], s[46:47] op_sel_hi:[1,0]\n"
"v_cvt_pk_bf16_f32 v0, v0, v1\n"
"v_cvt_pk_bf16_f32 v1, v2, v3\n"
"v_cvt_pk_bf16_f32 v2, v4, v5\n"
"v_cvt_pk_bf16_f32 v3, v6, v7\n"
"v_mfma_f32_16x16x32_bf16 v[208:211], a[100:103], a[188:191], v[208:211]\n"
"v_permlane16_swap_b32 v0, v2\n"
"v_permlane16_swap_b32 v1, v3\n"
"v_accvgpr_write_b32 a48, v0\n"
"v_accvgpr_write_b32 a49, v1\n"
"v_accvgpr_write_b32 a50, v2\n"
"v_accvgpr_write_b32 a51, v3\n"
"v_mfma_f32_16x16x32_bf16 v[0:3], a[192:195], a[160:163], 0\n"
"s_waitcnt lgkmcnt(10)\n"
"v_cvt_pk_bf16_f32 v8, v8, v9\n"
"v_cvt_pk_bf16_f32 v9, v10, v11\n"
"v_mfma_f32_16x16x32_bf16 v[0:3], a[196:199], a[164:167], v[0:3]\n"
"v_cvt_pk_bf16_f32 v10, v12, v13\n"
"v_cvt_pk_bf16_f32 v11, v14, v15\n"
"v_mfma_f32_16x16x32_bf16 v[4:7], a[200:203], a[160:163], 0\n"
"s_waitcnt vmcnt(10) lgkmcnt(0)\n"
"v_permlane16_swap_b32 v8, v10\n"
"v_mfma_f32_16x16x32_bf16 v[4:7], a[204:207], a[164:167], v[4:7]\n"
"s_barrier\n"
"v_permlane16_swap_b32 v9, v11\n"
"v_accvgpr_write_b32 a52, v8\n"
"v_accvgpr_write_b32 a53, v9\n"
"v_accvgpr_write_b32 a54, v10\n"
"v_accvgpr_write_b32 a55, v11\n"
"v_mfma_f32_16x16x32_bf16 v[8:11], a[208:211], a[160:163], 0\n"
"s_mov_b32 m0, s51\n"
"ds_read_b64_tr_b16 a[96:97], v243 offset:0\n"
"v_cvt_pk_bf16_f32 v16, v16, v17\n"
"v_mfma_f32_16x16x32_bf16 v[8:11], a[212:215], a[164:167], v[8:11]\n"
"buffer_load_dwordx4 v247, s[24:27], s49 offen lds\n"
"v_mfma_f32_16x16x32_bf16 v[12:15], a[216:219], a[160:163], 0\n"
"ds_read_b64_tr_b16 a[98:99], v243 offset:512\n"
"v_cvt_pk_bf16_f32 v17, v18, v19\n"
"v_cvt_pk_bf16_f32 v18, v20, v21\n"
"v_cvt_pk_bf16_f32 v19, v22, v23\n"
"v_mfma_f32_16x16x32_bf16 v[12:15], a[220:223], a[164:167], v[12:15]\n"
"v_permlane16_swap_b32 v16, v18\n"
"v_permlane16_swap_b32 v17, v19\n"
"v_accvgpr_write_b32 a56, v16\n"
"v_accvgpr_write_b32 a57, v17\n"
"v_accvgpr_write_b32 a58, v18\n"
"v_accvgpr_write_b32 a59, v19\n"
"v_mfma_f32_16x16x32_bf16 v[16:19], a[224:227], a[160:163], 0\n"
"ds_read_b64_tr_b16 a[100:101], v243 offset:8192\n"
"v_cvt_pk_bf16_f32 v24, v24, v25\n"
"v_mfma_f32_16x16x32_bf16 v[16:19], a[228:231], a[164:167], v[16:19]\n"
"ds_read_b64_tr_b16 a[102:103], v243 offset:8704\n"
"v_cvt_pk_bf16_f32 v25, v26, v27\n"
"v_mfma_f32_16x16x32_bf16 v[20:23], a[232:235], a[160:163], 0\n"
"ds_read_b64_tr_b16 a[128:129], v243 offset:1024\n"
"v_cvt_pk_bf16_f32 v26, v28, v29\n"
"v_cvt_pk_bf16_f32 v27, v30, v31\n"
"v_mfma_f32_16x16x32_bf16 v[20:23], a[236:239], a[164:167], v[20:23]\n"
"buffer_load_dwordx4 v253, s[24:27], s49 offen offset:1024 lds\n"
"v_permlane16_swap_b32 v24, v26\n"
"v_permlane16_swap_b32 v25, v27\n"
"v_accvgpr_write_b32 a60, v24\n"
"v_accvgpr_write_b32 a61, v25\n"
"v_accvgpr_write_b32 a62, v26\n"
"v_accvgpr_write_b32 a63, v27\n"
"v_mfma_f32_16x16x32_bf16 v[24:27], a[240:243], a[160:163], 0\n"
"ds_read_b64_tr_b16 a[130:131], v243 offset:1536\n"
"v_xor_b32 v244, s74, v244\n"
"v_mfma_f32_16x16x32_bf16 v[24:27], a[244:247], a[164:167], v[24:27]\n"
"ds_read_b64_tr_b16 a[104:105], v244 offset:0\n"
"v_xor_b32 v245, s74, v245\n"
"v_mfma_f32_16x16x32_bf16 v[28:31], a[248:251], a[160:163], 0\n"
"v_pk_mul_f32 v[32:33], v[32:33], s[46:47] op_sel_hi:[1,0]\nv_pk_mul_f32 v[34:35], v[34:35], s[46:47] op_sel_hi:[1,0]\nv_pk_mul_f32 v[36:37], v[36:37], s[46:47] op_sel_hi:[1,0]\nv_pk_mul_f32 v[38:39], v[38:39], s[46:47] op_sel_hi:[1,0]\nv_pk_mul_f32 v[40:41], v[40:41], s[46:47] op_sel_hi:[1,0]\nv_pk_mul_f32 v[42:43], v[42:43], s[46:47] op_sel_hi:[1,0]\nv_pk_mul_f32 v[44:45], v[44:45], s[46:47] op_sel_hi:[1,0]\nv_pk_mul_f32 v[46:47], v[46:47], s[46:47] op_sel_hi:[1,0]\nv_pk_mul_f32 v[48:49], v[48:49], s[46:47] op_sel_hi:[1,0]\nv_pk_mul_f32 v[50:51], v[50:51], s[46:47] op_sel_hi:[1,0]\nv_pk_mul_f32 v[52:53], v[52:53], s[46:47] op_sel_hi:[1,0]\nv_pk_mul_f32 v[54:55], v[54:55], s[46:47] op_sel_hi:[1,0]\nv_pk_mul_f32 v[56:57], v[56:57], s[46:47] op_sel_hi:[1,0]\nv_pk_mul_f32 v[58:59], v[58:59], s[46:47] op_sel_hi:[1,0]\nv_pk_mul_f32 v[60:61], v[60:61], s[46:47] op_sel_hi:[1,0]\nv_pk_mul_f32 v[62:63], v[62:63], s[46:47] op_sel_hi:[1,0]\n"
"v_mfma_f32_16x16x32_bf16 v[28:31], a[252:255], a[164:167], v[28:31]\n"
"ds_read_b128 a[160:163], v249 offset:0\n"
"v_cvt_pk_bf16_f32 v56, v56, v57\n"
"v_cvt_pk_bf16_f32 v57, v58, v59\n"
"v_cvt_pk_bf16_f32 v58, v60, v61\n"
"v_cvt_pk_bf16_f32 v59, v62, v63\n"
"v_mfma_f32_16x16x32_bf16 v[60:63], a[248:251], a[168:171], 0\n"
"v_permlane16_swap_b32 v56, v58\n"
"v_mfma_f32_16x16x32_bf16 v[60:63], a[252:255], a[172:175], v[60:63]\n"
"buffer_load_dwordx4 v254, s[24:27], s49 offen offset:2048 lds\n"
"v_permlane16_swap_b32 v57, v59\n"
"v_accvgpr_write_b32 a76, v56\n"
"v_accvgpr_write_b32 a77, v57\n"
"v_accvgpr_write_b32 a78, v58\n"
"v_accvgpr_write_b32 a79, v59\n"
"v_mfma_f32_16x16x32_bf16 v[56:59], a[240:243], a[168:171], 0\n"
"ds_read_b128 a[164:167], v250 offset:0\n"
"v_cvt_pk_bf16_f32 v48, v48, v49\n"
"v_mfma_f32_16x16x32_bf16 v[56:59], a[244:247], a[172:175], v[56:59]\n"
"ds_read_b64_tr_b16 a[106:107], v244 offset:512\n"
"v_cvt_pk_bf16_f32 v49, v50, v51\n"
"v_cvt_pk_bf16_f32 v50, v52, v53\n"
"v_cvt_pk_bf16_f32 v51, v54, v55\n"
"v_mfma_f32_16x16x32_bf16 v[52:55], a[232:235], a[168:171], 0\n"
"v_permlane16_swap_b32 v48, v50\n"
"v_mfma_f32_16x16x32_bf16 v[52:55], a[236:239], a[172:175], v[52:55]\n"
"v_permlane16_swap_b32 v49, v51\n"
"v_accvgpr_write_b32 a72, v48\n"
"v_accvgpr_write_b32 a73, v49\n"
"v_accvgpr_write_b32 a74, v50\n"
"v_accvgpr_write_b32 a75, v51\n"
"v_mfma_f32_16x16x32_bf16 v[48:51], a[224:227], a[168:171], 0\n"
"ds_read_b64_tr_b16 a[108:109], v244 offset:8192\n"
"v_cvt_pk_bf16_f32 v40, v40, v41\n"
"v_mfma_f32_16x16x32_bf16 v[48:51], a[228:231], a[172:175], v[48:51]\n"
"buffer_load_dwordx4 v255, s[24:27], s49 offen offset:3072 lds\n"
"v_cvt_pk_bf16_f32 v41, v42, v43\n"
"v_cvt_pk_bf16_f32 v42, v44, v45\n"
"v_cvt_pk_bf16_f32 v43, v46, v47\n"
"v_mfma_f32_16x16x32_bf16 v[44:47], a[216:219], a[168:171], 0\n"
"v_pk_mul_f32 v[96:97], v[96:97], s[46:47] op_sel_hi:[1,0]\nv_pk_mul_f32 v[98:99], v[98:99], s[46:47] op_sel_hi:[1,0]\nv_pk_mul_f32 v[100:101], v[100:101], s[46:47] op_sel_hi:[1,0]\nv_pk_mul_f32 v[102:103], v[102:103], s[46:47] op_sel_hi:[1,0]\nv_pk_mul_f32 v[104:105], v[104:105], s[46:47] op_sel_hi:[1,0]\nv_pk_mul_f32 v[106:107], v[106:107], s[46:47] op_sel_hi:[1,0]\nv_pk_mul_f32 v[108:109], v[108:109], s[46:47] op_sel_hi:[1,0]\nv_pk_mul_f32 v[110:111], v[110:111], s[46:47] op_sel_hi:[1,0]\n"
"v_mfma_f32_16x16x32_bf16 v[44:47], a[220:223], a[172:175], v[44:47]\n"
"v_cvt_pk_bf16_f32 v104, v104, v105\n"
"v_cvt_pk_bf16_f32 v105, v106, v107\n"
"v_cvt_pk_bf16_f32 v106, v108, v109\n"
"v_cvt_pk_bf16_f32 v107, v110, v111\n"
"v_permlane16_swap_b32 v40, v42\n"
"v_permlane16_swap_b32 v41, v43\n"
"v_accvgpr_write_b32 a68, v40\n"
"v_accvgpr_write_b32 a69, v41\n"
"v_accvgpr_write_b32 a70, v42\n"
"v_accvgpr_write_b32 a71, v43\n"
"v_mfma_f32_16x16x32_bf16 v[40:43], a[208:211], a[168:171], 0\n"
"v_permlane16_swap_b32 v104, v106\n"
"v_permlane16_swap_b32 v105, v107\n"
"v_mfma_f32_16x16x32_bf16 v[40:43], a[212:215], a[172:175], v[40:43]\n"
"s_add_u32 s40, s49, 40960\n"
"ds_write_b128 v248, v[104:107] offset:6144\n"
"v_cvt_pk_bf16_f32 v32, v32, v33\n"
"v_cvt_pk_bf16_f32 v33, v34, v35\n"
"v_cvt_pk_bf16_f32 v34, v36, v37\n"
"v_cvt_pk_bf16_f32 v35, v38, v39\n"
"v_mfma_f32_16x16x32_bf16 v[36:39], a[200:203], a[168:171], 0\n"
"s_add_u32 m0, s51, 4096\n"
"v_permlane16_swap_b32 v32, v34\n"
"v_mfma_f32_16x16x32_bf16 v[36:39], a[204:207], a[172:175], v[36:39]\n"
"buffer_load_dwordx4 v247, s[24:27], s40 offen lds\n"
"v_permlane16_swap_b32 v33, v35\n"
"v_accvgpr_write_b32 a64, v32\n"
"v_accvgpr_write_b32 a65, v33\n"
"v_accvgpr_write_b32 a66, v34\n"
"v_accvgpr_write_b32 a67, v35\n"
"v_mfma_f32_16x16x32_bf16 v[32:35], a[192:195], a[168:171], 0\n"
"v_pk_mul_f32 v[64:65], v[64:65], s[46:47] op_sel_hi:[1,0]\nv_pk_mul_f32 v[66:67], v[66:67], s[46:47] op_sel_hi:[1,0]\nv_pk_mul_f32 v[68:69], v[68:69], s[46:47] op_sel_hi:[1,0]\nv_pk_mul_f32 v[70:71], v[70:71], s[46:47] op_sel_hi:[1,0]\nv_pk_mul_f32 v[72:73], v[72:73], s[46:47] op_sel_hi:[1,0]\nv_pk_mul_f32 v[74:75], v[74:75], s[46:47] op_sel_hi:[1,0]\nv_pk_mul_f32 v[76:77], v[76:77], s[46:47] op_sel_hi:[1,0]\nv_pk_mul_f32 v[78:79], v[78:79], s[46:47] op_sel_hi:[1,0]\nv_pk_mul_f32 v[80:81], v[80:81], s[46:47] op_sel_hi:[1,0]\nv_pk_mul_f32 v[82:83], v[82:83], s[46:47] op_sel_hi:[1,0]\nv_pk_mul_f32 v[84:85], v[84:85], s[46:47] op_sel_hi:[1,0]\nv_pk_mul_f32 v[86:87], v[86:87], s[46:47] op_sel_hi:[1,0]\nv_pk_mul_f32 v[88:89], v[88:89], s[46:47] op_sel_hi:[1,0]\nv_pk_mul_f32 v[90:91], v[90:91], s[46:47] op_sel_hi:[1,0]\nv_pk_mul_f32 v[92:93], v[92:93], s[46:47] op_sel_hi:[1,0]\nv_pk_mul_f32 v[94:95], v[94:95], s[46:47] op_sel_hi:[1,0]\n"
"v_cvt_pk_bf16_f32 v64, v64, v65\n"
"v_cvt_pk_bf16_f32 v65, v66, v67\n"
"v_cvt_pk_bf16_f32 v66, v68, v69\n"
"v_cvt_pk_bf16_f32 v67, v70, v71\n"
"v_mfma_f32_16x16x32_bf16 v[32:35], a[196:199], a[172:175], v[32:35]\n"
"v_permlane16_swap_b32 v64, v66\n"
"v_permlane16_swap_b32 v65, v67\n"
"v_accvgpr_write_b32 a80, v64\n"
"v_accvgpr_write_b32 a81, v65\n"
"v_accvgpr_write_b32 a82, v66\n"
"v_accvgpr_write_b32 a83, v67\n"
"v_mfma_f32_16x16x32_bf16 v[64:67], a[192:195], a[176:179], 0\n"
"ds_read_b64_tr_b16 a[110:111], v244 offset:8704\n"
"v_cvt_pk_bf16_f32 v96, v96, v97\n"
"v_cvt_pk_bf16_f32 v97, v98, v99\n"
"v_cvt_pk_bf16_f32 v98, v100, v101\n"
"v_cvt_pk_bf16_f32 v99, v102, v103\n"
"v_mfma_f32_16x16x32_bf16 v[64:67], a[196:199], a[180:183], v[64:67]\n"
"v_permlane16_swap_b32 v96, v98\n"
"v_permlane16_swap_b32 v97, v99\n"
"v_mfma_f32_16x16x32_bf16 v[68:71], a[200:203], a[176:179], 0\n"
"ds_write_b128 v248, v[96:99] offset:4096\n"
"v_cvt_pk_bf16_f32 v72, v72, v73\n"
"v_cvt_pk_bf16_f32 v73, v74, v75\n"
"v_cvt_pk_bf16_f32 v74, v76, v77\n"
"v_cvt_pk_bf16_f32 v75, v78, v79\n"
"v_mfma_f32_16x16x32_bf16 v[68:71], a[204:207], a[180:183], v[68:71]\n"
"buffer_load_dwordx4 v253, s[24:27], s40 offen offset:1024 lds\n"
"v_permlane16_swap_b32 v72, v74\n"
"v_permlane16_swap_b32 v73, v75\n"
"v_accvgpr_write_b32 a84, v72\n"
"v_accvgpr_write_b32 a85, v73\n"
"v_accvgpr_write_b32 a86, v74\n"
"v_accvgpr_write_b32 a87, v75\n"
"v_mfma_f32_16x16x32_bf16 v[72:75], a[208:211], a[176:179], 0\n"
"ds_read_b64_tr_b16 a[112:113], v245 offset:0\n"
"v_cvt_pk_bf16_f32 v88, v88, v89\n"
"v_mfma_f32_16x16x32_bf16 v[72:75], a[212:215], a[180:183], v[72:75]\n"
"ds_read_b64_tr_b16 a[114:115], v245 offset:512\n"
"v_cvt_pk_bf16_f32 v89, v90, v91\n"
"v_mfma_f32_16x16x32_bf16 v[76:79], a[216:219], a[176:179], 0\n"
"ds_read_b64_tr_b16 a[116:117], v245 offset:8192\n"
"ds_read_b64_tr_b16 a[118:119], v245 offset:8704\n"
"v_cvt_pk_bf16_f32 v90, v92, v93\n"
"v_cvt_pk_bf16_f32 v91, v94, v95\n"
"v_xor_b32 v246, s74, v246\n"
"v_mfma_f32_16x16x32_bf16 v[76:79], a[220:223], a[180:183], v[76:79]\n"
"ds_read_b64_tr_b16 a[120:121], v246 offset:0\n"
"ds_read_b64_tr_b16 a[122:123], v246 offset:512\n"
"v_permlane16_swap_b32 v88, v90\n"
"v_permlane16_swap_b32 v89, v91\n"
"v_accvgpr_write_b32 a92, v88\n"
"v_accvgpr_write_b32 a93, v89\n"
"v_accvgpr_write_b32 a94, v90\n"
"v_accvgpr_write_b32 a95, v91\n"
"v_mfma_f32_16x16x32_bf16 v[88:91], a[240:243], a[176:179], 0\n"
"ds_read_b64_tr_b16 a[124:125], v246 offset:8192\n"
"ds_read_b64_tr_b16 a[126:127], v246 offset:8704\n"
"v_mfma_f32_16x16x32_bf16 v[88:91], a[244:247], a[180:183], v[88:91]\n"
"buffer_load_dwordx4 v254, s[24:27], s40 offen offset:2048 lds\n"
"v_mfma_f32_16x16x32_bf16 v[92:95], a[248:251], a[176:179], 0\n"
"s_add_u32 s49, s49, 128\n"
"ds_read_b64_tr_b16 a[132:133], v243 offset:9216\n"
"v_cvt_pk_bf16_f32 v80, v80, v81\n"
"v_cvt_pk_bf16_f32 v81, v82, v83\n"
"v_cvt_pk_bf16_f32 v82, v84, v85\n"
"v_cvt_pk_bf16_f32 v83, v86, v87\n"
"v_mfma_f32_16x16x32_bf16 v[92:95], a[252:255], a[180:183], v[92:95]\n"
"s_sub_u32 s41, s49, 1280\n"
"ds_read_b64_tr_b16 a[134:135], v243 offset:9728\n"
"v_permlane16_swap_b32 v80, v82\n"
"v_permlane16_swap_b32 v81, v83\n"
"v_accvgpr_write_b32 a88, v80\n"
"v_accvgpr_write_b32 a89, v81\n"
"v_accvgpr_write_b32 a90, v82\n"
"v_accvgpr_write_b32 a91, v83\n"
"v_mfma_f32_16x16x32_bf16 v[80:83], a[224:227], a[176:179], 0\n"
"s_min_u32 s49, s49, s41\n"
"ds_read_b64_tr_b16 a[136:137], v244 offset:1024\n"
"ds_read_b64_tr_b16 a[138:139], v244 offset:1536\n"
"v_mfma_f32_16x16x32_bf16 v[80:83], a[228:231], a[180:183], v[80:83]\n"
"s_xor_b32 s51, s51, s74\n"
"ds_read_b64_tr_b16 a[140:141], v244 offset:9216\n"
"ds_read_b64_tr_b16 a[142:143], v244 offset:9728\n"
"v_mfma_f32_16x16x32_bf16 v[84:87], a[232:235], a[176:179], 0\n"
"ds_read_b64_tr_b16 a[144:145], v245 offset:1024\n"
"ds_read_b64_tr_b16 a[146:147], v245 offset:1536\n"
"v_mfma_f32_16x16x32_bf16 v[84:87], a[236:239], a[180:183], v[84:87]\n"
"ds_read_b64_tr_b16 a[148:149], v245 offset:9216\n"
"ds_read_b64_tr_b16 a[150:151], v245 offset:9728\n"
"v_mfma_f32_16x16x32_bf16 v[108:111], a[232:235], a[184:187], 0\n"
"ds_read_b64_tr_b16 a[152:153], v246 offset:1024\n"
"ds_read_b64_tr_b16 a[154:155], v246 offset:1536\n"
"v_mfma_f32_16x16x32_bf16 v[108:111], a[236:239], a[188:191], v[108:111]\n"
"ds_read_b64_tr_b16 a[156:157], v246 offset:9216\n"
"ds_read_b64_tr_b16 a[158:159], v246 offset:9728\n"
"v_mfma_f32_16x16x32_bf16 v[104:107], a[224:227], a[184:187], 0\n"
"ds_read_b128 a[168:171], v249 offset:2048\n"
"v_xor_b32 v251, s74, v251\n"
"v_mfma_f32_16x16x32_bf16 v[104:107], a[228:231], a[188:191], v[104:107]\n"
"ds_read_b128 a[172:175], v250 offset:2048\n"
"v_xor_b32 v252, s74, v252\n"
"v_mfma_f32_16x16x32_bf16 v[100:103], a[200:203], a[184:187], 0\n"
"ds_read_b128 a[176:179], v249 offset:4096\n"
"v_mfma_f32_16x16x32_bf16 v[100:103], a[204:207], a[188:191], v[100:103]\n"
"ds_read_b128 a[180:183], v250 offset:4096\n"
"v_mfma_f32_16x16x32_bf16 v[96:99], a[192:195], a[184:187], 0\n"
"s_add_u32 s47, s47, 1\n"
"ds_read_b128 a[184:187], v251 offset:0\n"
"v_mfma_f32_16x16x32_bf16 v[96:99], a[196:199], a[188:191], v[96:99]\n"
"s_waitcnt lgkmcnt(9)\n"
"ds_read_b128 a[188:191], v252 offset:0\n"
"s_mov_b32 s62, 18\n"
"s_branch LPL_%=\n"
"LPT2_%=:\n"
"v_mfma_f32_16x16x32_bf16 v[112:115], a[96:99], a[160:163], v[112:115]\n"
"v_bfe_i32 v241, v242, 4, 1\n"
"v_and_b32 v241, 0xaaaaaaaa, v241\n"
"v_mfma_f32_16x16x32_bf16 v[112:115], a[100:103], a[164:167], v[112:115]\n"
"v_or_b32 v241, 0x44444444, v241\n"
"v_bfe_u32 v247, v242, 4, 2\n"
"v_mfma_f32_16x16x32_bf16 v[116:119], a[104:107], a[160:163], v[116:119]\n"
"s_waitcnt vmcnt(11)\n"
"v_mul_u32_u24 v247, 20, v247\n"
"v_mfma_f32_16x16x32_bf16 v[116:119], a[108:111], a[164:167], v[116:119]\n"
"s_barrier\n"
"v_mfma_f32_16x16x32_bf16 v[120:123], a[112:115], a[160:163], v[120:123]\n"
"s_add_u32 s40, s48, 256\n"
"s_xor_b32 m0, s50, 0xc000\n"
"ds_read_b64_tr_b16 a[192:193], v243 offset:16384\n"
"v_mfma_f32_16x16x32_bf16 v[120:123], a[116:119], a[164:167], v[120:123]\n"
"buffer_load_dwordx4 v224, s[28:31], s40 offen lds\n"
"v_mfma_f32_16x16x32_bf16 v[124:127], a[120:123], a[160:163], v[124:127]\n"
"ds_read_b64_tr_b16 a[194:195], v243 offset:16896\n"
"v_mfma_f32_16x16x32_bf16 v[124:127], a[124:127], a[164:167], v[124:127]\n"
"ds_read_b64_tr_b16 a[196:197], v243 offset:24576\n"
"v_mfma_f32_16x16x32_bf16 v[128:131], a[128:131], a[160:163], v[128:131]\n"
"ds_read_b64_tr_b16 a[198:199], v243 offset:25088\n"
"v_mfma_f32_16x16x32_bf16 v[128:131], a[132:135], a[164:167], v[128:131]\n"
"ds_read_b64_tr_b16 a[200:201], v244 offset:16384\n"
"v_mfma_f32_16x16x32_bf16 v[132:135], a[136:139], a[160:163], v[132:135]\n"
"ds_read_b64_tr_b16 a[202:203], v244 offset:16896\n"
"v_mfma_f32_16x16x32_bf16 v[132:135], a[140:143], a[164:167], v[132:135]\n"
"buffer_load_dwordx4 v225, s[28:31], s40 offen offset:1024 lds\n"
"v_mfma_f32_16x16x32_bf16 v[136:139], a[144:147], a[160:163], v[136:139]\n"
"ds_read_b64_tr_b16 a[204:205], v244 offset:24576\n"
"v_mfma_f32_16x16x32_bf16 v[136:139], a[148:151], a[164:167], v[136:139]\n"
"ds_read_b64_tr_b16 a[206:207], v244 offset:25088\n"
"v_mfma_f32_16x16x32_bf16 v[140:143], a[152:155], a[160:163], v[140:143]\n"
"ds_read_b64_tr_b16 a[208:209], v245 offset:16384\n"
"v_mfma_f32_16x16x32_bf16 v[140:143], a[156:159], a[164:167], v[140:143]\n"
"ds_read_b64_tr_b16 a[210:211], v245 offset:16896\n"
"v_mfma_f32_16x16x32_bf16 v[172:175], a[152:155], a[168:171], v[172:175]\n"
"ds_read_b64_tr_b16 a[212:213], v245 offset:24576\n"
"v_mfma_f32_16x16x32_bf16 v[172:175], a[156:159], a[172:175], v[172:175]\n"
"ds_read_b64_tr_b16 a[214:215], v245 offset:25088\n"
"v_mfma_f32_16x16x32_bf16 v[168:171], a[144:147], a[168:171], v[168:171]\n"
"ds_read_b64_tr_b16 a[216:217], v246 offset:16384\n"
"v_mfma_f32_16x16x32_bf16 v[168:171], a[148:151], a[172:175], v[168:171]\n"
"buffer_load_dwordx4 v226, s[28:31], s40 offen offset:2048 lds\n"
"v_mfma_f32_16x16x32_bf16 v[164:167], a[136:139], a[168:171], v[164:167]\n"
"ds_read_b64_tr_b16 a[218:219], v246 offset:16896\n"
"v_mfma_f32_16x16x32_bf16 v[164:167], a[140:143], a[172:175], v[164:167]\n"
"ds_read_b64_tr_b16 a[220:221], v246 offset:24576\n"
"v_mfma_f32_16x16x32_bf16 v[160:163], a[128:131], a[168:171], v[160:163]\n"
"ds_read_b64_tr_b16 a[222:223], v246 offset:25088\n"
"v_mfma_f32_16x16x32_bf16 v[160:163], a[132:135], a[172:175], v[160:163]\n"
"ds_read_b64_tr_b16 a[224:225], v243 offset:17408\n"
"v_mfma_f32_16x16x32_bf16 v[156:159], a[120:123], a[168:171], v[156:159]\n"
"ds_read_b64_tr_b16 a[226:227], v243 offset:17920\n"
"v_mfma_f32_16x16x32_bf16 v[156:159], a[124:127], a[172:175], v[156:159]\n"
"buffer_load_dwordx4 v227, s[28:31], s40 offen offset:3072 lds\n"
"v_mfma_f32_16x16x32_bf16 v[152:155], a[112:115], a[168:171], v[152:155]\n"
"s_mov_b32 s58, 245760\n"
"ds_read_b64_tr_b16 a[228:229], v243 offset:25600\n"
"v_mfma_f32_16x16x32_bf16 v[152:155], a[116:119], a[172:175], v[152:155]\n"
"s_mov_b32 s47, 0\n"
"ds_read_b64_tr_b16 a[230:231], v243 offset:26112\n"
"v_mfma_f32_16x16x32_bf16 v[148:151], a[104:107], a[168:171], v[148:151]\n"
"ds_read_b64_tr_b16 a[232:233], v244 offset:17408\n"
"v_mfma_f32_16x16x32_bf16 v[148:151], a[108:111], a[172:175], v[148:151]\n"
"ds_read_b64_tr_b16 a[234:235], v244 offset:17920\n"
"v_xor_b32 v243, s74, v243\n"
"v_mfma_f32_16x16x32_bf16 v[144:147], a[96:99], a[168:171], v[144:147]\n"
"s_mov_b32 m0, s44\n"
"ds_read_b64_tr_b16 a[236:237], v244 offset:25600\n"
"v_xor_b32 v249, s74, v249\n"
"v_mfma_f32_16x16x32_bf16 v[144:147], a[100:103], a[172:175], v[144:147]\n"
"buffer_load_dwordx4 v240, s[16:19], s56 offen nt lds\n"
"v_mfma_f32_16x16x32_bf16 v[176:179], a[96:99], a[176:179], v[176:179]\n"
"s_waitcnt lgkmcnt(5)\n"
"ds_read_b64_tr_b16 a[238:239], v244 offset:26112\n"
"v_xor_b32 v250, s74, v250\n"
"v_mfma_f32_16x16x32_bf16 v[176:179], a[100:103], a[180:183], v[176:179]\n"
"ds_read_b64_tr_b16 a[240:241], v245 offset:17408\n"
"v_xor_b32 v244, s74, v244\n"
"v_mfma_f32_16x16x32_bf16 v[180:183], a[104:107], a[176:179], v[180:183]\n"
"ds_read_b64_tr_b16 a[242:243], v245 offset:17920\n"
"v_xor_b32 v251, s74, v251\n"
"v_mfma_f32_16x16x32_bf16 v[180:183], a[108:111], a[180:183], v[180:183]\n"
"ds_read_b64_tr_b16 a[244:245], v245 offset:25600\n"
"v_xor_b32 v252, s74, v252\n"
"v_mfma_f32_16x16x32_bf16 v[184:187], a[112:115], a[176:179], v[184:187]\n"
"ds_read_b64_tr_b16 a[246:247], v245 offset:26112\n"
"v_xor_b32 v245, s74, v245\n"
"v_mfma_f32_16x16x32_bf16 v[184:187], a[116:119], a[180:183], v[184:187]\n"
"ds_read_b64_tr_b16 a[248:249], v246 offset:17408\n"
"v_mfma_f32_16x16x32_bf16 v[188:191], a[120:123], a[176:179], v[188:191]\n"
"s_add_u32 s40, s56, s52\n"
"ds_read_b64_tr_b16 a[250:251], v246 offset:17920\n"
"v_mfma_f32_16x16x32_bf16 v[188:191], a[124:127], a[180:183], v[188:191]\n"
"buffer_load_dwordx4 v240, s[16:19], s40 offen offset:1024 nt lds\n"
"v_mfma_f32_16x16x32_bf16 v[200:203], a[144:147], a[176:179], v[200:203]\n"
"ds_read_b64_tr_b16 a[252:253], v246 offset:25600\n"
"v_mfma_f32_16x16x32_bf16 v[200:203], a[148:151], a[180:183], v[200:203]\n"
"ds_read_b64_tr_b16 a[254:255], v246 offset:26112\n"
"v_xor_b32 v246, s74, v246\n"
"v_mfma_f32_16x16x32_bf16 v[204:207], a[152:155], a[176:179], v[204:207]\n"
"v_mfma_f32_16x16x32_bf16 v[204:207], a[156:159], a[180:183], v[204:207]\n"
"v_mfma_f32_16x16x32_bf16 v[192:195], a[128:131], a[176:179], v[192:195]\n"
"s_add_u32 s40, s56, s53\n"
"v_mfma_f32_16x16x32_bf16 v[192:195], a[132:135], a[180:183], v[192:195]\n"
"buffer_load_dwordx4 v240, s[16:19], s40 offen offset:2048 nt lds\n"
"v_mfma_f32_16x16x32_bf16 v[196:199], a[136:139], a[176:179], v[196:199]\n"
"v_mfma_f32_16x16x32_bf16 v[196:199], a[140:143], a[180:183], v[196:199]\n"
"v_mfma_f32_16x16x32_bf16 v[220:223], a[136:139], a[184:187], v[220:223]\n"
"v_mfma_f32_16x16x32_bf16 v[220:223], a[140:143], a[188:191], v[220:223]\n"
"v_mfma_f32_16x16x32_bf16 v[216:219], a[128:131], a[184:187], v[216:219]\n"
"s_add_u32 s40, s56, s54\n"
"v_mfma_f32_16x16x32_bf16 v[216:219], a[132:135], a[188:191], v[216:219]\n"
"buffer_load_dwordx4 v240, s[16:19], s40 offen offset:3072 nt lds\n"
"v_mfma_f32_16x16x32_bf16 v[212:215], a[104:107], a[184:187], v[212:215]\n"
"v_mfma_f32_16x16x32_bf16 v[212:215], a[108:111], a[188:191], v[212:215]\n"
"v_mfma_f32_16x16x32_bf16 v[208:211], a[96:99], a[184:187], v[208:211]\n"
"v_mfma_f32_16x16x32_bf16 v[208:211], a[100:103], a[188:191], v[208:211]\n"
"v_mfma_f32_16x16x32_bf16 v[0:3], a[192:195], a[160:163], v[0:3]\n"
"v_mfma_f32_16x16x32_bf16 v[0:3], a[196:199], a[164:167], v[0:3]\n"
"v_mfma_f32_16x16x32_bf16 v[4:7], a[200:203], a[160:163], v[4:7]\n"
"s_waitcnt vmcnt(8) lgkmcnt(0)\n"
"v_mfma_f32_16x16x32_bf16 v[4:7], a[204:207], a[164:167], v[4:7]\n"
"s_barrier\n"
"v_mfma_f32_16x16x32_bf16 v[8:11], a[208:211], a[160:163], v[8:11]\n"
"s_add_u32 m0, s44, 65536\n"
"ds_read_b64_tr_b16 a[96:97], v243 offset:0\n"
"v_mfma_f32_16x16x32_bf16 v[8:11], a[212:215], a[164:167], v[8:11]\n"
"buffer_load_dwordx4 v240, s[20:23], s56 offen nt lds\n"
"v_mfma_f32_16x16x32_bf16 v[12:15], a[216:219], a[160:163], v[12:15]\n"
"ds_read_b64_tr_b16 a[98:99], v243 offset:512\n"
"v_mfma_f32_16x16x32_bf16 v[12:15], a[220:223], a[164:167], v[12:15]\n"
"ds_read_b64_tr_b16 a[100:101], v243 offset:8192\n"
"v_mfma_f32_16x16x32_bf16 v[16:19], a[224:227], a[160:163], v[16:19]\n"
"ds_read_b64_tr_b16 a[102:103], v243 offset:8704\n"
"v_mfma_f32_16x16x32_bf16 v[16:19], a[228:231], a[164:167], v[16:19]\n"
"ds_read_b64_tr_b16 a[104:105], v244 offset:0\n"
"v_mfma_f32_16x16x32_bf16 v[20:23], a[232:235], a[160:163], v[20:23]\n"
"s_add_u32 s40, s56, s52\n"
"ds_read_b64_tr_b16 a[106:107], v244 offset:512\n"
"v_mfma_f32_16x16x32_bf16 v[20:23], a[236:239], a[164:167], v[20:23]\n"
"buffer_load_dwordx4 v240, s[20:23], s40 offen offset:1024 nt lds\n"
"v_mfma_f32_16x16x32_bf16 v[24:27], a[240:243], a[160:163], v[24:27]\n"
"ds_read_b64_tr_b16 a[108:109], v244 offset:8192\n"
"v_mfma_f32_16x16x32_bf16 v[24:27], a[244:247], a[164:167], v[24:27]\n"
"ds_read_b64_tr_b16 a[110:111], v244 offset:8704\n"
"v_mfma_f32_16x16x32_bf16 v[28:31], a[248:251], a[160:163], v[28:31]\n"
"ds_read_b128 a[160:163], v249 offset:0\n"
"v_mfma_f32_16x16x32_bf16 v[28:31], a[252:255], a[164:167], v[28:31]\n"
"ds_read_b128 a[164:167], v250 offset:0\n"
"v_mfma_f32_16x16x32_bf16 v[60:63], a[248:251], a[168:171], v[60:63]\n"
"ds_read_b64_tr_b16 a[112:113], v245 offset:0\n"
"v_mfma_f32_16x16x32_bf16 v[60:63], a[252:255], a[172:175], v[60:63]\n"
"ds_read_b64_tr_b16 a[114:115], v245 offset:512\n"
"v_mfma_f32_16x16x32_bf16 v[56:59], a[240:243], a[168:171], v[56:59]\n"
"s_add_u32 s40, s56, s53\n"
"ds_read_b64_tr_b16 a[116:117], v245 offset:8192\n"
"v_mfma_f32_16x16x32_bf16 v[56:59], a[244:247], a[172:175], v[56:59]\n"
"buffer_load_dwordx4 v240, s[20:23], s40 offen offset:2048 nt lds\n"
"v_mfma_f32_16x16x32_bf16 v[52:55], a[232:235], a[168:171], v[52:55]\n"
"ds_read_b64_tr_b16 a[118:119], v245 offset:8704\n"
"v_mfma_f32_16x16x32_bf16 v[52:55], a[236:239], a[172:175], v[52:55]\n"
"ds_read_b64_tr_b16 a[120:121], v246 offset:0\n"
"v_mfma_f32_16x16x32_bf16 v[48:51], a[224:227], a[168:171], v[48:51]\n"
"ds_read_b64_tr_b16 a[122:123], v246 offset:512\n"
"v_mfma_f32_16x16x32_bf16 v[48:51], a[228:231], a[172:175], v[48:51]\n"
"ds_read_b64_tr_b16 a[124:125], v246 offset:8192\n"
"v_mfma_f32_16x16x32_bf16 v[44:47], a[216:219], a[168:171], v[44:47]\n"
"s_add_u32 s40, s56, s54\n"
"ds_read_b64_tr_b16 a[126:127], v246 offset:8704\n"
"v_mfma_f32_16x16x32_bf16 v[44:47], a[220:223], a[172:175], v[44:47]\n"
"buffer_load_dwordx4 v240, s[20:23], s40 offen offset:3072 nt lds\n"
"v_mfma_f32_16x16x32_bf16 v[40:43], a[208:211], a[168:171], v[40:43]\n"
"s_add_u32 s55, s55, s59\n"
"ds_read_b64_tr_b16 a[128:129], v243 offset:1024\n"
"v_mfma_f32_16x16x32_bf16 v[40:43], a[212:215], a[172:175], v[40:43]\n"
"s_add_u32 s41, s55, s57\n"
"ds_read_b64_tr_b16 a[130:131], v243 offset:1536\n"
"v_mfma_f32_16x16x32_bf16 v[36:39], a[200:203], a[168:171], v[36:39]\n"
"s_and_b32 s41, s41, s58\n"
"ds_read_b64_tr_b16 a[132:133], v243 offset:9216\n"
"v_mfma_f32_16x16x32_bf16 v[36:39], a[204:207], a[172:175], v[36:39]\n"
"s_andn2_b32 s56, s55, s58\n"
"ds_read_b64_tr_b16 a[134:135], v243 offset:9728\n"
"v_mfma_f32_16x16x32_bf16 v[32:35], a[192:195], a[168:171], v[32:35]\n"
"s_or_b32 s56, s56, s41\n"
"ds_read_b64_tr_b16 a[136:137], v244 offset:1024\n"
"v_mfma_f32_16x16x32_bf16 v[32:35], a[196:199], a[172:175], v[32:35]\n"
"ds_read_b64_tr_b16 a[138:139], v244 offset:1536\n"
"v_mfma_f32_16x16x32_bf16 v[64:67], a[192:195], a[176:179], v[64:67]\n"
"s_add_u32 m0, s44, 16384\n"
"ds_read_b64_tr_b16 a[140:141], v244 offset:9216\n"
"v_mfma_f32_16x16x32_bf16 v[64:67], a[196:199], a[180:183], v[64:67]\n"
"buffer_load_dwordx4 v240, s[16:19], s56 offen nt lds\n"
"v_mfma_f32_16x16x32_bf16 v[68:71], a[200:203], a[176:179], v[68:71]\n"
"ds_read_b64_tr_b16 a[142:143], v244 offset:9728\n"
"v_mfma_f32_16x16x32_bf16 v[68:71], a[204:207], a[180:183], v[68:71]\n"
"ds_read_b64_tr_b16 a[144:145], v245 offset:1024\n"
"v_mfma_f32_16x16x32_bf16 v[72:75], a[208:211], a[176:179], v[72:75]\n"
"ds_read_b64_tr_b16 a[146:147], v245 offset:1536\n"
"v_mfma_f32_16x16x32_bf16 v[72:75], a[212:215], a[180:183], v[72:75]\n"
"ds_read_b64_tr_b16 a[148:149], v245 offset:9216\n"
"v_mfma_f32_16x16x32_bf16 v[76:79], a[216:219], a[176:179], v[76:79]\n"
"s_add_u32 s40, s56, s52\n"
"ds_read_b64_tr_b16 a[150:151], v245 offset:9728\n"
"v_mfma_f32_16x16x32_bf16 v[76:79], a[220:223], a[180:183], v[76:79]\n"
"buffer_load_dwordx4 v240, s[16:19], s40 offen offset:1024 nt lds\n"
"v_mfma_f32_16x16x32_bf16 v[88:91], a[240:243], a[176:179], v[88:91]\n"
"ds_read_b64_tr_b16 a[152:153], v246 offset:1024\n"
"v_mfma_f32_16x16x32_bf16 v[88:91], a[244:247], a[180:183], v[88:91]\n"
"ds_read_b64_tr_b16 a[154:155], v246 offset:1536\n"
"v_mfma_f32_16x16x32_bf16 v[92:95], a[248:251], a[176:179], v[92:95]\n"
"ds_read_b64_tr_b16 a[156:157], v246 offset:9216\n"
"v_mfma_f32_16x16x32_bf16 v[92:95], a[252:255], a[180:183], v[92:95]\n"
"ds_read_b64_tr_b16 a[158:159], v246 offset:9728\n"
"v_mfma_f32_16x16x32_bf16 v[80:83], a[224:227], a[176:179], v[80:83]\n"
"s_add_u32 s40, s56, s53\n"
"ds_read_b128 a[168:171], v249 offset:2048\n"
"v_mfma_f32_16x16x32_bf16 v[80:83], a[228:231], a[180:183], v[80:83]\n"
"buffer_load_dwordx4 v240, s[16:19], s40 offen offset:2048 nt lds\n"
"v_mfma_f32_16x16x32_bf16 v[84:87], a[232:235], a[176:179], v[84:87]\n"
"ds_read_b128 a[172:175], v250 offset:2048\n"
"v_mfma_f32_16x16x32_bf16 v[84:87], a[236:239], a[180:183], v[84:87]\n"
"ds_read_b128 a[176:179], v249 offset:4096\n"
"v_and_b32 v249, 0x20, v242\n"
"v_mfma_f32_16x16x32_bf16 v[108:111], a[232:235], a[184:187], v[108:111]\n"
"ds_read_b128 a[180:183], v250 offset:4096\n"
"v_add_u32 v249, -1, v249\n"
"v_mfma_f32_16x16x32_bf16 v[108:111], a[236:239], a[188:191], v[108:111]\n"
"v_ashrrev_i32 v249, 31, v249\n"
"v_and_b32 v249, 0x3f803f80, v249\n"
"v_mfma_f32_16x16x32_bf16 v[104:107], a[224:227], a[184:187], v[104:107]\n"
"s_add_u32 s40, s56, s54\n"
"v_mfma_f32_16x16x32_bf16 v[104:107], a[228:231], a[188:191], v[104:107]\n"
"buffer_load_dwordx4 v240, s[16:19], s40 offen offset:3072 nt lds\n"
"v_mfma_f32_16x16x32_bf16 v[100:103], a[200:203], a[184:187], v[100:103]\n"
"v_mfma_f32_16x16x32_bf16 v[100:103], a[204:207], a[188:191], v[100:103]\n"
"v_mfma_f32_16x16x32_bf16 v[96:99], a[192:195], a[184:187], v[96:99]\n"
"ds_read_b128 a[184:187], v251 offset:0\n"
"v_mfma_f32_16x16x32_bf16 v[96:99], a[196:199], a[188:191], v[96:99]\n"
"s_waitcnt lgkmcnt(2)\n"
"ds_read_b128 a[188:191], v252 offset:0\n"
"v_mfma_f32_16x16x32_bf16 v[112:115], a[96:99], a[160:163], v[112:115]\n"
"v_mfma_f32_16x16x32_bf16 v[112:115], a[100:103], a[164:167], v[112:115]\n"
"v_mfma_f32_16x16x32_bf16 v[116:119], a[104:107], a[160:163], v[116:119]\n"
"s_waitcnt vmcnt(12)\n"
"v_mfma_f32_16x16x32_bf16 v[116:119], a[108:111], a[164:167], v[116:119]\n"
"s_barrier\n"
"v_mfma_f32_16x16x32_bf16 v[120:123], a[112:115], a[160:163], v[120:123]\n"
"s_add_u32 m0, s44, 81920\n"
"ds_read_b64_tr_b16 a[232:233], v244 offset:17408\n"
"v_mfma_f32_16x16x32_bf16 v[120:123], a[116:119], a[164:167], v[120:123]\n"
"buffer_load_dwordx4 v240, s[20:23], s56 offen nt lds\n"
"v_mfma_f32_16x16x32_bf16 v[124:127], a[120:123], a[160:163], v[124:127]\n"
"ds_read_b64_tr_b16 a[234:235], v244 offset:17920\n"
"v_mfma_f32_16x16x32_bf16 v[124:127], a[124:127], a[164:167], v[124:127]\n"
"ds_read_b64_tr_b16 a[236:237], v244 offset:25600\n"
"v_mfma_f32_16x16x32_bf16 v[128:131], a[128:131], a[160:163], v[128:131]\n"
"ds_read_b64_tr_b16 a[238:239], v244 offset:26112\n"
"v_mfma_f32_16x16x32_bf16 v[128:131], a[132:135], a[164:167], v[128:131]\n"
"ds_read_b64_tr_b16 a[224:225], v243 offset:17408\n"
"v_mfma_f32_16x16x32_bf16 v[132:135], a[136:139], a[160:163], v[132:135]\n"
"ds_read_b64_tr_b16 a[226:227], v243 offset:17920\n"
"v_mfma_f32_16x16x32_bf16 v[132:135], a[140:143], a[164:167], v[132:135]\n"
"ds_read_b64_tr_b16 a[192:193], v243 offset:16384\n"
"v_mfma_f32_16x16x32_bf16 v[136:139], a[144:147], a[160:163], v[136:139]\n"
"ds_read_b64_tr_b16 a[194:195], v243 offset:16896\n"
"v_mfma_f32_16x16x32_bf16 v[136:139], a[148:151], a[164:167], v[136:139]\n"
"ds_read_b64_tr_b16 a[196:197], v243 offset:24576\n"
"v_mfma_f32_16x16x32_bf16 v[140:143], a[152:155], a[160:163], v[140:143]\n"
"ds_read_b64_tr_b16 a[198:199], v243 offset:25088\n"
"v_mfma_f32_16x16x32_bf16 v[140:143], a[156:159], a[164:167], v[140:143]\n"
"s_waitcnt lgkmcnt(7)\n"
"ds_read_b64_tr_b16 a[200:201], v244 offset:16384\n"
"v_mfma_f32_16x16x32_bf16 v[220:223], a[136:139], a[184:187], v[220:223]\n"
"ds_read_b64_tr_b16 a[202:203], v244 offset:16896\n"
"v_mfma_f32_16x16x32_bf16 v[220:223], a[140:143], a[188:191], v[220:223]\n"
"ds_read_b64_tr_b16 a[204:205], v244 offset:24576\n"
"v_mfma_f32_16x16x32_bf16 v[216:219], a[128:131], a[184:187], v[216:219]\n"
"s_add_u32 s40, s56, s52\n"
"ds_read_b64_tr_b16 a[206:207], v244 offset:25088\n"
"v_mfma_f32_16x16x32_bf16 v[216:219], a[132:135], a[188:191], v[216:219]\n"
"buffer_load_dwordx4 v240, s[20:23], s40 offen offset:1024 nt lds\n"
"v_mfma_f32_16x16x32_bf16 v[212:215], a[104:107], a[184:187], v[212:215]\n"
"ds_read_b64_tr_b16 a[208:209], v245 offset:16384\n"
"v_mfma_f32_16x16x32_bf16 v[212:215], a[108:111], a[188:191], v[212:215]\n"
"v_pk_mul_f32 v[112:113], v[112:113], s[46:47] op_sel_hi:[1,0]\nv_pk_mul_f32 v[114:115], v[114:115], s[46:47] op_sel_hi:[1,0]\nv_pk_mul_f32 v[116:117], v[116:117], s[46:47] op_sel_hi:[1,0]\nv_pk_mul_f32 v[118:119], v[118:119], s[46:47] op_sel_hi:[1,0]\nv_pk_mul_f32 v[120:121], v[120:121], s[46:47] op_sel_hi:[1,0]\nv_pk_mul_f32 v[122:123], v[122:123], s[46:47] op_sel_hi:[1,0]\nv_pk_mul_f32 v[124:125], v[124:125], s[46:47] op_sel_hi:[1,0]\nv_pk_mul_f32 v[126:127], v[126:127], s[46:47] op_sel_hi:[1,0]\nv_pk_mul_f32 v[128:129], v[128:129], s[46:47] op_sel_hi:[1,0]\nv_pk_mul_f32 v[130:131], v[130:131], s[46:47] op_sel_hi:[1,0]\nv_pk_mul_f32 v[132:133], v[132:133], s[46:47] op_sel_hi:[1,0]\nv_pk_mul_f32 v[134:135], v[134:135], s[46:47] op_sel_hi:[1,0]\nv_pk_mul_f32 v[136:137], v[136:137], s[46:47] op_sel_hi:[1,0]\nv_pk_mul_f32 v[138:139], v[138:139], s[46:47] op_sel_hi:[1,0]\nv_pk_mul_f32 v[140:141], v[140:141], s[46:47] op_sel_hi:[1,0]\nv_pk_mul_f32 v[142:143], v[142:143], s[46:47] op_sel_hi:[1,0]\n"
"v_mfma_f32_16x16x32_bf16 v[208:211], a[96:99], a[184:187], v[208:211]\n"
"ds_read_b64_tr_b16 a[210:211], v245 offset:16896\n"
"v_cvt_pk_bf16_f32 v128, v128, v129\n"
"v_mfma_f32_16x16x32_bf16 v[208:211], a[100:103], a[188:191], v[208:211]\n"
"ds_read_b64_tr_b16 a[212:213], v245 offset:24576\n"
"v_cvt_pk_bf16_f32 v129, v130, v131\n"
"v_mfma_f32_16x16x32_bf16 v[172:175], a[152:155], a[168:171], v[172:175]\n"
"ds_read_b64_tr_b16 a[214:215], v245 offset:25088\n"
"v_cvt_pk_bf16_f32 v130, v132, v133\n"
"v_mfma_f32_16x16x32_bf16 v[172:175], a[156:159], a[172:175], v[172:175]\n"
"ds_read_b64_tr_b16 a[216:217], v246 offset:16384\n"
"v_cvt_pk_bf16_f32 v131, v134, v135\n"
"v_mfma_f32_16x16x32_bf16 v[168:171], a[144:147], a[168:171], v[168:171]\n"
"ds_read_b64_tr_b16 a[218:219], v246 offset:16896\n"
"v_cvt_pk_bf16_f32 v136, v136, v137\n"
"v_mfma_f32_16x16x32_bf16 v[168:171], a[148:151], a[172:175], v[168:171]\n"
"ds_read_b64_tr_b16 a[220:221], v246 offset:24576\n"
"v_cvt_pk_bf16_f32 v137, v138, v139\n"
"v_mfma_f32_16x16x32_bf16 v[164:167], a[136:139], a[168:171], v[164:167]\n"
"ds_read_b64_tr_b16 a[222:223], v246 offset:25088\n"
"v_cvt_pk_bf16_f32 v138, v140, v141\n"
"v_mfma_f32_16x16x32_bf16 v[164:167], a[140:143], a[172:175], v[164:167]\n"
"ds_read_b64_tr_b16 a[228:229], v243 offset:25600\n"
"v_cvt_pk_bf16_f32 v139, v142, v143\n"
"v_mfma_f32_16x16x32_bf16 v[160:163], a[128:131], a[168:171], v[160:163]\n"
"s_add_u32 s40, s56, s53\n"
"ds_read_b64_tr_b16 a[230:231], v243 offset:26112\n"
"v_cvt_pk_bf16_f32 v120, v120, v121\n"
"v_mfma_f32_16x16x32_bf16 v[160:163], a[132:135], a[172:175], v[160:163]\n"
"buffer_load_dwordx4 v240, s[20:23], s40 offen offset:2048 nt lds\n"
"v_mfma_f32_16x16x32_bf16 v[156:159], a[120:123], a[168:171], v[156:159]\n"
"ds_read_b64_tr_b16 a[240:241], v245 offset:17408\n"
"v_cvt_pk_bf16_f32 v121, v122, v123\n"
"v_mfma_f32_16x16x32_bf16 v[156:159], a[124:127], a[172:175], v[156:159]\n"
"ds_read_b64_tr_b16 a[242:243], v245 offset:17920\n"
"v_cvt_pk_bf16_f32 v122, v124, v125\n"
"v_mfma_f32_16x16x32_bf16 v[152:155], a[112:115], a[168:171], v[152:155]\n"
"ds_read_b64_tr_b16 a[244:245], v245 offset:25600\n"
"v_cvt_pk_bf16_f32 v123, v126, v127\n"
"v_mfma_f32_16x16x32_bf16 v[152:155], a[116:119], a[172:175], v[152:155]\n"
"ds_read_b64_tr_b16 a[246:247], v245 offset:26112\n"
"v_cvt_pk_bf16_f32 v112, v112, v113\n"
"v_mfma_f32_16x16x32_bf16 v[148:151], a[104:107], a[168:171], v[148:151]\n"
"ds_read_b64_tr_b16 a[248:249], v246 offset:17408\n"
"v_cvt_pk_bf16_f32 v113, v114, v115\n"
"v_mfma_f32_16x16x32_bf16 v[148:151], a[108:111], a[172:175], v[148:151]\n"
"ds_read_b64_tr_b16 a[250:251], v246 offset:17920\n"
"v_cvt_pk_bf16_f32 v114, v116, v117\n"
"v_mfma_f32_16x16x32_bf16 v[144:147], a[96:99], a[168:171], v[144:147]\n"
"ds_read_b64_tr_b16 a[252:253], v246 offset:25600\n"
"v_cvt_pk_bf16_f32 v115, v118, v119\n"
"v_mfma_f32_16x16x32_bf16 v[144:147], a[100:103], a[172:175], v[144:147]\n"
"v_pk_mul_f32 v[208:209], v[208:209], s[46:47] op_sel_hi:[1,0]\nv_pk_mul_f32 v[210:211], v[210:211], s[46:47] op_sel_hi:[1,0]\nv_pk_mul_f32 v[212:213], v[212:213], s[46:47] op_sel_hi:[1,0]\nv_pk_mul_f32 v[214:215], v[214:215], s[46:47] op_sel_hi:[1,0]\nv_pk_mul_f32 v[216:217], v[216:217], s[46:47] op_sel_hi:[1,0]\nv_pk_mul_f32 v[218:219], v[218:219], s[46:47] op_sel_hi:[1,0]\nv_pk_mul_f32 v[220:221], v[220:221], s[46:47] op_sel_hi:[1,0]\nv_pk_mul_f32 v[222:223], v[222:223], s[46:47] op_sel_hi:[1,0]\n"
"v_mfma_f32_16x16x32_bf16 v[176:179], a[96:99], a[176:179], v[176:179]\n"
"ds_read_b64_tr_b16 a[254:255], v246 offset:26112\n"
"v_cvt_pk_bf16_f32 v208, v208, v209\n"
"v_mfma_f32_16x16x32_bf16 v[176:179], a[100:103], a[180:183], v[176:179]\n"
"v_permlane16_swap_b32 v128, v130\n"
"v_mfma_f32_16x16x32_bf16 v[180:183], a[104:107], a[176:179], v[180:183]\n"
"v_permlane16_swap_b32 v129, v131\n"
"v_mfma_f32_16x16x32_bf16 v[180:183], a[108:111], a[180:183], v[180:183]\n"
"v_permlane16_swap_b32 v136, v138\n"
"v_mfma_f32_16x16x32_bf16 v[184:187], a[112:115], a[176:179], v[184:187]\n"
"s_add_u32 s40, s56, s54\n"
"v_permlane16_swap_b32 v137, v139\n"
"v_mfma_f32_16x16x32_bf16 v[184:187], a[116:119], a[180:183], v[184:187]\n"
"buffer_load_dwordx4 v240, s[20:23], s40 offen offset:3072 nt lds\n"
"v_mfma_f32_16x16x32_bf16 v[188:191], a[120:123], a[176:179], v[188:191]\n"
"s_add_u32 s55, s55, s59\n"
"v_pk_mul_f32 v[144:145], v[144:145], s[46:47] op_sel_hi:[1,0]\nv_pk_mul_f32 v[146:147], v[146:147], s[46:47] op_sel_hi:[1,0]\nv_pk_mul_f32 v[148:149], v[148:149], s[46:47] op_sel_hi:[1,0]\nv_pk_mul_f32 v[150:151], v[150:151], s[46:47] op_sel_hi:[1,0]\nv_pk_mul_f32 v[152:153], v[152:153], s[46:47] op_sel_hi:[1,0]\nv_pk_mul_f32 v[154:155], v[154:155], s[46:47] op_sel_hi:[1,0]\nv_pk_mul_f32 v[156:157], v[156:157], s[46:47] op_sel_hi:[1,0]\nv_pk_mul_f32 v[158:159], v[158:159], s[46:47] op_sel_hi:[1,0]\nv_pk_mul_f32 v[160:161], v[160:161], s[46:47] op_sel_hi:[1,0]\nv_pk_mul_f32 v[162:163], v[162:163], s[46:47] op_sel_hi:[1,0]\nv_pk_mul_f32 v[164:165], v[164:165], s[46:47] op_sel_hi:[1,0]\nv_pk_mul_f32 v[166:167], v[166:167], s[46:47] op_sel_hi:[1,0]\nv_pk_mul_f32 v[168:169], v[168:169], s[46:47] op_sel_hi:[1,0]\nv_pk_mul_f32 v[170:171], v[170:171], s[46:47] op_sel_hi:[1,0]\nv_pk_mul_f32 v[172:173], v[172:173], s[46:47] op_sel_hi:[1,0]\nv_pk_mul_f32 v[174:175], v[174:175], s[46:47] op_sel_hi:[1,0]\n"
"v_mfma_f32_16x16x32_bf16 v[188:191], a[124:127], a[180:183], v[188:191]\n"
"s_add_u32 s41, s55, s57\n"
"v_permlane16_swap_b32 v112, v114\n"
"v_mfma_f32_16x16x32_bf16 v[200:203], a[144:147], a[176:179], v[200:203]\n"
"s_and_b32 s41, s41, s58\n"
"v_permlane16_swap_b32 v113, v115\n"
"v_mfma_f32_16x16x32_bf16 v[200:203], a[148:151], a[180:183], v[200:203]\n"
"s_andn2_b32 s56, s55, s58\n"
"v_permlane16_swap_b32 v120, v122\n"
"v_mfma_f32_16x16x32_bf16 v[204:207], a[152:155], a[176:179], v[204:207]\n"
"s_or_b32 s56, s56, s41\n"
"v_permlane16_swap_b32 v121, v123\n"
"v_mfma_f32_16x16x32_bf16 v[204:207], a[156:159], a[180:183], v[204:207]\n"
"v_cvt_pk_bf16_f32 v209, v210, v211\n"
"v_cvt_pk_bf16_f32 v210, v212, v213\n"
"v_mfma_f32_16x16x32_bf16 v[192:195], a[128:131], a[176:179], v[192:195]\n"
"s_nop 0\n"
"v_permlane16_swap_b32 v208, v210\n"
"v_mfma_f32_16x16x32_bf16 v[192:195], a[132:135], a[180:183], v[192:195]\n"
"v_cvt_pk_bf16_f32 v211, v214, v215\n"
"v_cvt_pk_bf16_f32 v216, v216, v217\n"
"v_mfma_f32_16x16x32_bf16 v[196:199], a[136:139], a[176:179], v[196:199]\n"
"v_permlane16_swap_b32 v209, v211\n"
"v_mfma_f32_16x16x32_bf16 v[196:199], a[140:143], a[180:183], v[196:199]\n"
"ds_write_b128 v248, v[208:211] offset:8192\n"
"v_mfma_f32_16x16x32_bf16 v[108:111], a[232:235], a[184:187], v[108:111]\n"
"s_waitcnt lgkmcnt(1)\n"
"s_add_u32 m0, s44, 32768\n"
"v_cvt_pk_bf16_f32 v217, v218, v219\n"
"v_cvt_pk_bf16_f32 v218, v220, v221\n"
"v_mfma_f32_16x16x32_bf16 v[108:111], a[236:239], a[188:191], v[108:111]\n"
"buffer_load_dwordx4 v240, s[16:19], s56 offen nt lds\n"
"v_mfma_f32_16x16x32_bf16 v[104:107], a[224:227], a[184:187], v[104:107]\n"
"s_waitcnt vmcnt(13)\n"
"v_permlane16_swap_b32 v216, v218\n"
"v_mfma_f32_16x16x32_bf16 v[104:107], a[228:231], a[188:191], v[104:107]\n"
"s_barrier\n"
"v_mfma_f32_16x16x32_bf16 v[100:103], a[200:203], a[184:187], v[100:103]\n"
"v_cvt_pk_bf16_f32 v219, v222, v223\n"
"v_cvt_pk_bf16_f32 v152, v152, v153\n"
"v_mfma_f32_16x16x32_bf16 v[100:103], a[204:207], a[188:191], v[100:103]\n"
"v_pk_mul_f32 v[176:177], v[176:177], s[46:47] op_sel_hi:[1,0]\nv_pk_mul_f32 v[178:179], v[178:179], s[46:47] op_sel_hi:[1,0]\nv_pk_mul_f32 v[180:181], v[180:181], s[46:47] op_sel_hi:[1,0]\nv_pk_mul_f32 v[182:183], v[182:183], s[46:47] op_sel_hi:[1,0]\nv_pk_mul_f32 v[184:185], v[184:185], s[46:47] op_sel_hi:[1,0]\nv_pk_mul_f32 v[186:187], v[186:187], s[46:47] op_sel_hi:[1,0]\nv_pk_mul_f32 v[188:189], v[188:189], s[46:47] op_sel_hi:[1,0]\nv_pk_mul_f32 v[190:191], v[190:191], s[46:47] op_sel_hi:[1,0]\nv_pk_mul_f32 v[192:193], v[192:193], s[46:47] op_sel_hi:[1,0]\nv_pk_mul_f32 v[194:195], v[194:195], s[46:47] op_sel_hi:[1,0]\nv_pk_mul_f32 v[196:197], v[196:197], s[46:47] op_sel_hi:[1,0]\nv_pk_mul_f32 v[198:199], v[198:199], s[46:47] op_sel_hi:[1,0]\nv_pk_mul_f32 v[200:201], v[200:201], s[46:47] op_sel_hi:[1,0]\nv_pk_mul_f32 v[202:203], v[202:203], s[46:47] op_sel_hi:[1,0]\nv_pk_mul_f32 v[204:205], v[204:205], s[46:47] op_sel_hi:[1,0]\nv_pk_mul_f32 v[206:207], v[206:207], s[46:47] op_sel_hi:[1,0]\n"
"v_mfma_f32_16x16x32_bf16 v[96:99], a[192:195], a[184:187], v[96:99]\n"
"v_permlane16_swap_b32 v217, v219\n"
"v_mfma_f32_16x16x32_bf16 v[96:99], a[196:199], a[188:191], v[96:99]\n"
"ds_write_b128 v248, v[216:219] offset:10240\n"
"v_mfma_f32_16x16x32_bf16 v[0:3], a[192:195], a[160:163], v[0:3]\n"
"v_cvt_pk_bf16_f32 v200, v200, v201\n"
"v_cvt_pk_bf16_f32 v201, v202, v203\n"
"v_mfma_f32_16x16x32_bf16 v[0:3], a[196:199], a[164:167], v[0:3]\n"
"v_cvt_pk_bf16_f32 v203, v206, v207\n"
"v_bfe_u32 v206, v242, 8, 2\n"
"v_mfma_f32_16x16x32_bf16 v[4:7], a[200:203], a[160:163], v[4:7]\n"
"v_permlane16_swap_b32 v201, v203\n"
"v_mfma_f32_16x16x32_bf16 v[4:7], a[204:207], a[164:167], v[4:7]\n"
"v_lshl_add_u32 v206, v206, 4, s43\n"
"v_xor_b32 v207, 64, v206\n"
"v_mfma_f32_16x16x32_bf16 v[8:11], a[208:211], a[160:163], v[8:11]\n"
"ds_read_b128 v[208:211], v206 offset:0\n"
"v_cvt_pk_bf16_f32 v176, v176, v177\n"
"v_mfma_f32_16x16x32_bf16 v[8:11], a[212:215], a[164:167], v[8:11]\n"
"ds_read_b128 v[212:215], v207 offset:0\n"
"v_cvt_pk_bf16_f32 v177, v178, v179\n"
"v_mfma_f32_16x16x32_bf16 v[12:15], a[216:219], a[160:163], v[12:15]\n"
"s_add_u32 s40, s56, s52\n"
"ds_read_b128 v[216:219], v206 offset:1024\n"
"v_cvt_pk_bf16_f32 v178, v180, v181\n"
"v_mfma_f32_16x16x32_bf16 v[12:15], a[220:223], a[164:167], v[12:15]\n"
"buffer_load_dwordx4 v240, s[16:19], s40 offen offset:1024 nt lds\n"
"v_mfma_f32_16x16x32_bf16 v[16:19], a[224:227], a[160:163], v[16:19]\n"
"ds_read_b128 v[220:223], v207 offset:1024\n"
"v_cvt_pk_bf16_f32 v179, v182, v183\n"
"v_mfma_f32_16x16x32_bf16 v[16:19], a[228:231], a[164:167], v[16:19]\n"
"v_permlane16_swap_b32 v176, v178\n"
"v_mfma_f32_16x16x32_bf16 v[20:23], a[232:235], a[160:163], v[20:23]\n"
"v_permlane16_swap_b32 v177, v179\n"
"v_mfma_f32_16x16x32_bf16 v[20:23], a[236:239], a[164:167], v[20:23]\n"
"v_pk_mul_f32 v[96:97], v[96:97], s[46:47] op_sel_hi:[1,0]\nv_pk_mul_f32 v[98:99], v[98:99], s[46:47] op_sel_hi:[1,0]\nv_pk_mul_f32 v[100:101], v[100:101], s[46:47] op_sel_hi:[1,0]\nv_pk_mul_f32 v[102:103], v[102:103], s[46:47] op_sel_hi:[1,0]\nv_pk_mul_f32 v[104:105], v[104:105], s[46:47] op_sel_hi:[1,0]\nv_pk_mul_f32 v[106:107], v[106:107], s[46:47] op_sel_hi:[1,0]\nv_pk_mul_f32 v[108:109], v[108:109], s[46:47] op_sel_hi:[1,0]\nv_pk_mul_f32 v[110:111], v[110:111], s[46:47] op_sel_hi:[1,0]\n"
"v_mfma_f32_16x16x32_bf16 v[24:27], a[240:243], a[160:163], v[24:27]\n"
"v_cvt_pk_bf16_f32 v184, v184, v185\n"
"v_cvt_pk_bf16_f32 v185, v186, v187\n"
"v_mfma_f32_16x16x32_bf16 v[24:27], a[244:247], a[164:167], v[24:27]\n"
"v_cvt_pk_bf16_f32 v186, v188, v189\n"
"v_cvt_pk_bf16_f32 v187, v190, v191\n"
"v_mfma_f32_16x16x32_bf16 v[28:31], a[248:251], a[160:163], v[28:31]\n"
"v_permlane16_swap_b32 v184, v186\n"
"v_mfma_f32_16x16x32_bf16 v[28:31], a[252:255], a[164:167], v[28:31]\n"
"v_permlane16_swap_b32 v185, v187\n"
"v_mfma_f32_16x16x32_bf16 v[60:63], a[248:251], a[168:171], v[60:63]\n"
"v_accvgpr_write_b32 a132, v184\n"
"v_accvgpr_write_b32 a133, v185\n"
"v_mfma_f32_16x16x32_bf16 v[60:63], a[252:255], a[172:175], v[60:63]\n"
"v_accvgpr_write_b32 a134, v186\n"
"v_accvgpr_write_b32 a135, v187\n"
"v_mfma_f32_16x16x32_bf16 v[56:59], a[240:243], a[168:171], v[56:59]\n"
"s_add_u32 s40, s56, s53\n"
"v_xor_b32 v207, 0x400, v248\n"
"v_cvt_pk_bf16_f32 v192, v192, v193\n"
"v_mfma_f32_16x16x32_bf16 v[56:59], a[244:247], a[172:175], v[56:59]\n"
"buffer_load_dwordx4 v240, s[16:19], s40 offen offset:2048 nt lds\n"
"v_mfma_f32_16x16x32_bf16 v[52:55], a[232:235], a[168:171], v[52:55]\n"
"v_cvt_pk_bf16_f32 v193, v194, v195\n"
"v_cvt_pk_bf16_f32 v194, v196, v197\n"
"v_mfma_f32_16x16x32_bf16 v[52:55], a[236:239], a[172:175], v[52:55]\n"
"s_nop 0\n"
"v_permlane16_swap_b32 v192, v194\n"
"v_mfma_f32_16x16x32_bf16 v[48:51], a[224:227], a[168:171], v[48:51]\n"
"v_pk_mul_f32 v[0:1], v[0:1], s[46:47] op_sel_hi:[1,0]\nv_pk_mul_f32 v[2:3], v[2:3], s[46:47] op_sel_hi:[1,0]\nv_pk_mul_f32 v[4:5], v[4:5], s[46:47] op_sel_hi:[1,0]\nv_pk_mul_f32 v[6:7], v[6:7], s[46:47] op_sel_hi:[1,0]\nv_pk_mul_f32 v[8:9], v[8:9], s[46:47] op_sel_hi:[1,0]\nv_pk_mul_f32 v[10:11], v[10:11], s[46:47] op_sel_hi:[1,0]\nv_pk_mul_f32 v[12:13], v[12:13], s[46:47] op_sel_hi:[1,0]\nv_pk_mul_f32 v[14:15], v[14:15], s[46:47] op_sel_hi:[1,0]\nv_pk_mul_f32 v[16:17], v[16:17], s[46:47] op_sel_hi:[1,0]\nv_pk_mul_f32 v[18:19], v[18:19], s[46:47] op_sel_hi:[1,0]\nv_pk_mul_f32 v[20:21], v[20:21], s[46:47] op_sel_hi:[1,0]\nv_pk_mul_f32 v[22:23], v[22:23], s[46:47] op_sel_hi:[1,0]\nv_pk_mul_f32 v[24:25], v[24:25], s[46:47] op_sel_hi:[1,0]\nv_pk_mul_f32 v[26:27], v[26:27], s[46:47] op_sel_hi:[1,0]\nv_pk_mul_f32 v[28:29], v[28:29], s[46:47] op_sel_hi:[1,0]\nv_pk_mul_f32 v[30:31], v[30:31], s[46:47] op_sel_hi:[1,0]\n"
"v_mfma_f32_16x16x32_bf16 v[48:51], a[228:231], a[172:175], v[48:51]\n"
"v_cvt_pk_bf16_f32 v195, v198, v199\n"
"v_accvgpr_write_b32 a136, v192\n"
"v_mfma_f32_16x16x32_bf16 v[44:47], a[216:219], a[168:171], v[44:47]\n"
"v_permlane16_swap_b32 v193, v195\n"
"v_mfma_f32_16x16x32_bf16 v[44:47], a[220:223], a[172:175], v[44:47]\n"
"v_accvgpr_write_b32 a137, v193\n"
"v_accvgpr_write_b32 a138, v194\n"
"v_mfma_f32_16x16x32_bf16 v[40:43], a[208:211], a[168:171], v[40:43]\n"
"v_accvgpr_write_b32 a139, v195\n"
"v_cvt_pk_bf16_f32 v153, v154, v155\n"
"v_mfma_f32_16x16x32_bf16 v[40:43], a[212:215], a[172:175], v[40:43]\n"
"v_cvt_pk_bf16_f32 v154, v156, v157\n"
"v_cvt_pk_bf16_f32 v155, v158, v159\n"
"v_mfma_f32_16x16x32_bf16 v[36:39], a[200:203], a[168:171], v[36:39]\n"
"v_permlane16_swap_b32 v152, v154\n"
"v_mfma_f32_16x16x32_bf16 v[36:39], a[204:207], a[172:175], v[36:39]\n"
"v_permlane16_swap_b32 v153, v155\n"
"v_mfma_f32_16x16x32_bf16 v[32:35], a[192:195], a[168:171], v[32:35]\n"
"v_accvgpr_write_b32 a116, v152\n"
"v_accvgpr_write_b32 a117, v153\n"
"v_mfma_f32_16x16x32_bf16 v[32:35], a[196:199], a[172:175], v[32:35]\n"
"v_accvgpr_write_b32 a118, v154\n"
"v_accvgpr_write_b32 a119, v155\n"
"v_mfma_f32_16x16x32_bf16 v[64:67], a[192:195], a[176:179], v[64:67]\n"
"s_add_u32 s40, s56, s54\n"
"ds_read_b128 a[192:195], v248 offset:0\n"
"v_cvt_pk_bf16_f32 v160, v160, v161\n"
"v_mfma_f32_16x16x32_bf16 v[64:67], a[196:199], a[180:183], v[64:67]\n"
"buffer_load_dwordx4 v240, s[16:19], s40 offen offset:3072 nt lds\n"
"v_mfma_f32_16x16x32_bf16 v[68:71], a[200:203], a[176:179], v[68:71]\n"
"ds_read_b128 a[196:199], v207 offset:0\n"
"v_cvt_pk_bf16_f32 v161, v162, v163\n"
"v_mfma_f32_16x16x32_bf16 v[68:71], a[204:207], a[180:183], v[68:71]\n"
"ds_read_b128 a[200:203], v248 offset:2048\n"
"v_cvt_pk_bf16_f32 v162, v164, v165\n"
"v_mfma_f32_16x16x32_bf16 v[72:75], a[208:211], a[176:179], v[72:75]\n"
"ds_read_b128 a[204:207], v207 offset:2048\n"
"v_cvt_pk_bf16_f32 v163, v166, v167\n"
"v_mfma_f32_16x16x32_bf16 v[72:75], a[212:215], a[180:183], v[72:75]\n"
"v_permlane16_swap_b32 v160, v162\n"
"v_mfma_f32_16x16x32_bf16 v[76:79], a[216:219], a[176:179], v[76:79]\n"
"v_permlane16_swap_b32 v161, v163\n"
"v_mfma_f32_16x16x32_bf16 v[76:79], a[220:223], a[180:183], v[76:79]\n"
"v_pk_mul_f32 v[32:33], v[32:33], s[46:47] op_sel_hi:[1,0]\nv_pk_mul_f32 v[34:35], v[34:35], s[46:47] op_sel_hi:[1,0]\nv_pk_mul_f32 v[36:37], v[36:37], s[46:47] op_sel_hi:[1,0]\nv_pk_mul_f32 v[38:39], v[38:39], s[46:47] op_sel_hi:[1,0]\nv_pk_mul_f32 v[40:41], v[40:41], s[46:47] op_sel_hi:[1,0]\nv_pk_mul_f32 v[42:43], v[42:43], s[46:47] op_sel_hi:[1,0]\nv_pk_mul_f32 v[44:45], v[44:45], s[46:47] op_sel_hi:[1,0]\nv_pk_mul_f32 v[46:47], v[46:47], s[46:47] op_sel_hi:[1,0]\nv_pk_mul_f32 v[48:49], v[48:49], s[46:47] op_sel_hi:[1,0]\nv_pk_mul_f32 v[50:51], v[50:51], s[46:47] op_sel_hi:[1,0]\nv_pk_mul_f32 v[52:53], v[52:53], s[46:47] op_sel_hi:[1,0]\nv_pk_mul_f32 v[54:55], v[54:55], s[46:47] op_sel_hi:[1,0]\nv_pk_mul_f32 v[56:57], v[56:57], s[46:47] op_sel_hi:[1,0]\nv_pk_mul_f32 v[58:59], v[58:59], s[46:47] op_sel_hi:[1,0]\nv_pk_mul_f32 v[60:61], v[60:61], s[46:47] op_sel_hi:[1,0]\nv_pk_mul_f32 v[62:63], v[62:63], s[46:47] op_sel_hi:[1,0]\n"
"v_mfma_f32_16x16x32_bf16 v[88:91], a[240:243], a[176:179], v[88:91]\n"
"v_accvgpr_write_b32 a120, v160\n"
"v_accvgpr_write_b32 a121, v161\n"
"v_mfma_f32_16x16x32_bf16 v[88:91], a[244:247], a[180:183], v[88:91]\n"
"v_accvgpr_write_b32 a122, v162\n"
"v_accvgpr_write_b32 a123, v163\n"
"v_mfma_f32_16x16x32_bf16 v[92:95], a[248:251], a[176:179], v[92:95]\n"
"v_cvt_pk_bf16_f32 v168, v168, v169\n"
"v_cvt_pk_bf16_f32 v169, v170, v171\n"
"v_mfma_f32_16x16x32_bf16 v[92:95], a[252:255], a[180:183], v[92:95]\n"
"v_cvt_pk_bf16_f32 v170, v172, v173\n"
"v_cvt_pk_bf16_f32 v171, v174, v175\n"
"v_mfma_f32_16x16x32_bf16 v[80:83], a[224:227], a[176:179], v[80:83]\n"
"v_permlane16_swap_b32 v168, v170\n"
"v_mfma_f32_16x16x32_bf16 v[80:83], a[228:231], a[180:183], v[80:83]\n"
"v_permlane16_swap_b32 v169, v171\n"
"v_mfma_f32_16x16x32_bf16 v[84:87], a[232:235], a[176:179], v[84:87]\n"
"v_accvgpr_write_b32 a124, v168\n"
"v_accvgpr_write_b32 a125, v169\n"
"v_mfma_f32_16x16x32_bf16 v[84:87], a[236:239], a[180:183], v[84:87]\n"
"s_waitcnt lgkmcnt(0)\n"
"v_accvgpr_write_b32 a126, v170\n"
"v_accvgpr_write_b32 a127, v171\n"
"v_mfma_f32_16x16x32_bf16 v[180:183], v[208:211], a[0:3], 0\n"
"v_accvgpr_write_b32 a128, v176\n"
"v_accvgpr_write_b32 a129, v177\n"
"v_mfma_f32_16x16x32_bf16 v[180:183], v[212:215], a[4:7], v[180:183]\n"
"v_accvgpr_write_b32 a130, v178\n"
"v_accvgpr_write_b32 a131, v179\n"
"v_mfma_f32_16x16x32_bf16 v[180:183], v[216:219], a[8:11], v[180:183]\n"
"v_accvgpr_write_b32 a104, v128\n"
"v_accvgpr_write_b32 a105, v129\n"
"v_mfma_f32_16x16x32_bf16 v[180:183], v[220:223], a[12:15], v[180:183]\n"
"v_accvgpr_write_b32 a108, v136\n"
"v_accvgpr_write_b32 a109, v137\n"
"v_mfma_f32_16x16x32_bf16 v[184:187], v[208:211], a[16:19], 0\n"
"v_cvt_pk_bf16_f32 v144, v144, v145\n"
"v_cvt_pk_bf16_f32 v145, v146, v147\n"
"v_mfma_f32_16x16x32_bf16 v[184:187], v[212:215], a[20:23], v[184:187]\n"
"v_pk_mul_f32 v[64:65], v[64:65], s[46:47] op_sel_hi:[1,0]\nv_pk_mul_f32 v[66:67], v[66:67], s[46:47] op_sel_hi:[1,0]\nv_pk_mul_f32 v[68:69], v[68:69], s[46:47] op_sel_hi:[1,0]\nv_pk_mul_f32 v[70:71], v[70:71], s[46:47] op_sel_hi:[1,0]\nv_pk_mul_f32 v[72:73], v[72:73], s[46:47] op_sel_hi:[1,0]\nv_pk_mul_f32 v[74:75], v[74:75], s[46:47] op_sel_hi:[1,0]\nv_pk_mul_f32 v[76:77], v[76:77], s[46:47] op_sel_hi:[1,0]\nv_pk_mul_f32 v[78:79], v[78:79], s[46:47] op_sel_hi:[1,0]\nv_pk_mul_f32 v[80:81], v[80:81], s[46:47] op_sel_hi:[1,0]\nv_pk_mul_f32 v[82:83], v[82:83], s[46:47] op_sel_hi:[1,0]\nv_pk_mul_f32 v[84:85], v[84:85], s[46:47] op_sel_hi:[1,0]\nv_pk_mul_f32 v[86:87], v[86:87], s[46:47] op_sel_hi:[1,0]\nv_pk_mul_f32 v[88:89], v[88:89], s[46:47] op_sel_hi:[1,0]\nv_pk_mul_f32 v[90:91], v[90:91], s[46:47] op_sel_hi:[1,0]\nv_pk_mul_f32 v[92:93], v[92:93], s[46:47] op_sel_hi:[1,0]\nv_pk_mul_f32 v[94:95], v[94:95], s[46:47] op_sel_hi:[1,0]\n"
"v_mfma_f32_16x16x32_bf16 v[184:187], v[216:219], a[24:27], v[184:187]\n"
"v_cvt_pk_bf16_f32 v146, v148, v149\n"
"v_cvt_pk_bf16_f32 v147, v150, v151\n"
"v_mfma_f32_16x16x32_bf16 v[184:187], v[220:223], a[28:31], v[184:187]\n"
"v_permlane16_swap_b32 v144, v146\n"
"v_mfma_f32_16x16x32_bf16 v[188:191], v[208:211], a[32:35], 0\n"
"v_permlane16_swap_b32 v145, v147\n"
"v_mfma_f32_16x16x32_bf16 v[188:191], v[212:215], a[36:39], v[188:191]\n"
"v_xor_b32 v180, s77, v180\n"
"v_xor_b32 v181, s77, v181\n"
"v_mfma_f32_16x16x32_bf16 v[188:191], v[216:219], a[40:43], v[188:191]\n"
"v_xor_b32 v182, s77, v182\n"
"v_xor_b32 v183, s77, v183\n"
"v_mfma_f32_16x16x32_bf16 v[188:191], v[220:223], a[44:47], v[188:191]\n"
"v_accvgpr_write_b32 a112, v144\n"
"v_accvgpr_write_b32 a113, v145\n"
"v_mfma_f32_16x16x32_bf16 v[192:195], v[208:211], a[192:195], 0\n"
"ds_read_b128 v[208:211], v228 offset:0\n"
"v_accvgpr_write_b32 a106, v130\n"
"v_mfma_f32_16x16x32_bf16 v[192:195], v[212:215], a[196:199], v[192:195]\n"
"ds_read_b128 v[212:215], v229 offset:0\n"
"v_xor_b32 v184, s77, v184\n"
"v_mfma_f32_16x16x32_bf16 v[192:195], v[216:219], a[200:203], v[192:195]\n"
"ds_read_b128 v[216:219], v228 offset:1024\n"
"v_xor_b32 v185, s77, v185\n"
"v_mfma_f32_16x16x32_bf16 v[192:195], v[220:223], a[204:207], v[192:195]\n"
"s_waitcnt lgkmcnt(2)\n"
"ds_read_b128 v[220:223], v229 offset:1024\n"
"v_xor_b32 v186, s77, v186\n"
"v_mfma_f32_16x16x32_bf16 v[152:155], v[208:211], a[0:3], v[180:183]\n"
"s_waitcnt lgkmcnt(2)\n"
"v_xor_b32 v187, s77, v187\n"
"v_accvgpr_write_b32 a107, v131\n"
"v_mfma_f32_16x16x32_bf16 v[152:155], v[212:215], a[4:7], v[152:155]\n"
"s_waitcnt lgkmcnt(1)\n"
"v_xor_b32 v188, s77, v188\n"
"v_xor_b32 v189, s77, v189\n"
"v_mfma_f32_16x16x32_bf16 v[152:155], v[216:219], a[8:11], v[152:155]\n"
"s_waitcnt lgkmcnt(0)\n"
"v_xor_b32 v190, s77, v190\n"
"v_xor_b32 v191, s77, v191\n"
"v_mfma_f32_16x16x32_bf16 v[152:155], v[220:223], a[12:15], v[152:155]\n"
"v_accvgpr_write_b32 a110, v138\n"
"v_accvgpr_write_b32 a111, v139\n"
"v_mfma_f32_16x16x32_bf16 v[156:159], v[216:219], a[24:27], v[184:187]\n"
"v_cvt_pk_bf16_f32 v96, v96, v97\n"
"v_cvt_pk_bf16_f32 v97, v98, v99\n"
"v_mfma_f32_16x16x32_bf16 v[156:159], v[220:223], a[28:31], v[156:159]\n"
"v_xor_b32 v192, s77, v192\n"
"v_xor_b32 v193, s77, v193\n"
"v_mfma_f32_16x16x32_bf16 v[156:159], v[208:211], a[16:19], v[156:159]\n"
"v_xor_b32 v194, s77, v194\n"
"v_xor_b32 v195, s77, v195\n"
"v_mfma_f32_16x16x32_bf16 v[156:159], v[212:215], a[20:23], v[156:159]\n"
"v_cvt_pk_bf16_f32 v98, v100, v101\n"
"v_cvt_pk_bf16_f32 v99, v102, v103\n"
"v_mfma_f32_16x16x32_bf16 v[160:163], v[208:211], a[32:35], v[188:191]\n"
"v_permlane16_swap_b32 v96, v98\n"
"v_mfma_f32_16x16x32_bf16 v[160:163], v[212:215], a[36:39], v[160:163]\n"
"v_exp_f32 v152, v152\n"
"v_mfma_f32_16x16x32_bf16 v[160:163], v[216:219], a[40:43], v[160:163]\n"
"v_exp_f32 v153, v153\n"
"v_mfma_f32_16x16x32_bf16 v[160:163], v[220:223], a[44:47], v[160:163]\n"
"v_exp_f32 v154, v154\n"
"v_mfma_f32_16x16x32_bf16 v[164:167], v[216:219], a[200:203], v[192:195]\n"
"ds_read_b128 v[216:219], v228 offset:5120\n"
"v_cvt_pk_bf16_f32 v128, v152, v153\n"
"v_mfma_f32_16x16x32_bf16 v[164:167], v[220:223], a[204:207], v[164:167]\n"
"ds_read_b128 v[220:223], v229 offset:5120\n"
"v_cvt_pk_bf16_f32 v104, v104, v105\n"
"v_mfma_f32_16x16x32_bf16 v[164:167], v[208:211], a[192:195], v[164:167]\n"
"ds_read_b128 v[208:211], v228 offset:4096\n"
"v_cvt_pk_bf16_f32 v105, v106, v107\n"
"v_mfma_f32_16x16x32_bf16 v[164:167], v[212:215], a[196:199], v[164:167]\n"
"s_waitcnt lgkmcnt(2)\n"
"ds_read_b128 v[212:215], v229 offset:4096\n"
"v_cvt_pk_bf16_f32 v106, v108, v109\n"
"v_mfma_f32_16x16x32_bf16 v[168:171], v[216:219], a[8:11], v[180:183]\n"
"s_waitcnt lgkmcnt(2)\n"
"v_exp_f32 v155, v155\n"
"v_mfma_f32_16x16x32_bf16 v[168:171], v[220:223], a[12:15], v[168:171]\n"
"s_waitcnt lgkmcnt(1)\n"
"v_exp_f32 v156, v156\n"
"v_mfma_f32_16x16x32_bf16 v[168:171], v[208:211], a[0:3], v[168:171]\n"
"s_waitcnt lgkmcnt(0)\n"
"v_exp_f32 v157, v157\n"
"v_mfma_f32_16x16x32_bf16 v[168:171], v[212:215], a[4:7], v[168:171]\n"
"v_exp_f32 v158, v158\n"
"v_mfma_f32_16x16x32_bf16 v[172:175], v[208:211], a[16:19], v[184:187]\n"
"v_exp_f32 v159, v159\n"
"v_mfma_f32_16x16x32_bf16 v[172:175], v[212:215], a[20:23], v[172:175]\n"
"v_exp_f32 v160, v160\n"
"v_mfma_f32_16x16x32_bf16 v[172:175], v[216:219], a[24:27], v[172:175]\n"
"v_exp_f32 v161, v161\n"
"v_mfma_f32_16x16x32_bf16 v[172:175], v[220:223], a[28:31], v[172:175]\n"
"v_exp_f32 v162, v162\n"
"v_mfma_f32_16x16x32_bf16 v[176:179], v[216:219], a[40:43], v[188:191]\n"
"v_exp_f32 v163, v163\n"
"v_mfma_f32_16x16x32_bf16 v[176:179], v[220:223], a[44:47], v[176:179]\n"
"v_cvt_pk_bf16_f32 v129, v154, v155\n"
"v_cvt_pk_bf16_f32 v136, v156, v157\n"
"v_mfma_f32_16x16x32_bf16 v[176:179], v[208:211], a[32:35], v[176:179]\n"
"v_exp_f32 v164, v164\n"
"v_mfma_f32_16x16x32_bf16 v[176:179], v[212:215], a[36:39], v[176:179]\n"
"v_exp_f32 v165, v165\n"
"v_mfma_f32_16x16x32_bf16 v[152:155], v[208:211], a[192:195], v[192:195]\n"
"ds_read_b128 v[208:211], v230 offset:0\n"
"v_cvt_pk_bf16_f32 v137, v158, v159\n"
"v_mfma_f32_16x16x32_bf16 v[152:155], v[212:215], a[196:199], v[152:155]\n"
"ds_read_b128 v[212:215], v231 offset:0\n"
"v_cvt_pk_bf16_f32 v144, v160, v161\n"
"v_mfma_f32_16x16x32_bf16 v[152:155], v[216:219], a[200:203], v[152:155]\n"
"ds_read_b128 v[216:219], v230 offset:1024\n"
"v_cvt_pk_bf16_f32 v145, v162, v163\n"
"v_mfma_f32_16x16x32_bf16 v[152:155], v[220:223], a[204:207], v[152:155]\n"
"s_waitcnt lgkmcnt(2)\n"
"ds_read_b128 v[220:223], v231 offset:1024\n"
"v_cvt_pk_bf16_f32 v124, v164, v165\n"
"v_mfma_f32_16x16x32_bf16 v[156:159], v[208:211], a[0:3], v[180:183]\n"
"s_waitcnt lgkmcnt(2)\n"
"v_exp_f32 v166, v166\n"
"v_mfma_f32_16x16x32_bf16 v[156:159], v[212:215], a[4:7], v[156:159]\n"
"s_waitcnt lgkmcnt(1)\n"
"v_exp_f32 v167, v167\n"
"v_mfma_f32_16x16x32_bf16 v[156:159], v[216:219], a[8:11], v[156:159]\n"
"s_waitcnt lgkmcnt(0)\n"
"v_exp_f32 v168, v168\n"
"v_mfma_f32_16x16x32_bf16 v[156:159], v[220:223], a[12:15], v[156:159]\n"
"v_exp_f32 v169, v169\n"
"v_mfma_f32_16x16x32_bf16 v[160:163], v[216:219], a[24:27], v[184:187]\n"
"v_exp_f32 v170, v170\n"
"v_mfma_f32_16x16x32_bf16 v[160:163], v[220:223], a[28:31], v[160:163]\n"
"v_exp_f32 v171, v171\n"
"v_mfma_f32_16x16x32_bf16 v[160:163], v[208:211], a[16:19], v[160:163]\n"
"v_cvt_pk_bf16_f32 v125, v166, v167\n"
"v_cvt_pk_bf16_f32 v130, v168, v169\n"
"v_mfma_f32_16x16x32_bf16 v[160:163], v[212:215], a[20:23], v[160:163]\n"
"v_exp_f32 v172, v172\n"
"v_mfma_f32_16x16x32_bf16 v[164:167], v[208:211], a[32:35], v[188:191]\n"
"ds_read_b128 v[208:211], v230 offset:4096\n"
"v_cvt_pk_bf16_f32 v131, v170, v171\n"
"v_mfma_f32_16x16x32_bf16 v[164:167], v[212:215], a[36:39], v[164:167]\n"
"ds_read_b128 v[212:215], v231 offset:4096\n"
"v_cvt_pk_bf16_f32 v107, v110, v111\n"
"v_mfma_f32_16x16x32_bf16 v[164:167], v[216:219], a[40:43], v[164:167]\n"
"ds_read_b128 v[216:219], v230 offset:5120\n"
"v_accvgpr_write_b32 a114, v146\n"
"v_mfma_f32_16x16x32_bf16 v[164:167], v[220:223], a[44:47], v[164:167]\n"
"s_waitcnt lgkmcnt(2)\n"
"ds_read_b128 v[220:223], v231 offset:5120\n"
"v_accvgpr_write_b32 a115, v147\n"
"v_mfma_f32_16x16x32_bf16 v[168:171], v[208:211], a[0:3], v[180:183]\n"
"s_waitcnt lgkmcnt(2)\n"
"v_exp_f32 v173, v173\n"
"v_mfma_f32_16x16x32_bf16 v[168:171], v[212:215], a[4:7], v[168:171]\n"
"s_waitcnt lgkmcnt(1)\n"
"v_exp_f32 v174, v174\n"
"v_mfma_f32_16x16x32_bf16 v[168:171], v[216:219], a[8:11], v[168:171]\n"
"s_waitcnt lgkmcnt(0)\n"
"v_exp_f32 v175, v175\n"
"v_mfma_f32_16x16x32_bf16 v[168:171], v[220:223], a[12:15], v[168:171]\n"
"v_cvt_pk_bf16_f32 v138, v172, v173\n"
"v_cvt_pk_bf16_f32 v139, v174, v175\n"
"v_mfma_f32_16x16x32_bf16 v[172:175], v[216:219], a[24:27], v[184:187]\n"
"v_permlane16_swap_b32 v97, v99\n"
"v_mfma_f32_16x16x32_bf16 v[172:175], v[220:223], a[28:31], v[172:175]\n"
"ds_write_b128 v248, v[96:99] offset:12288\n"
"v_permlane16_swap_b32 v104, v106\n"
"v_permlane16_swap_b32 v105, v107\n"
"v_mfma_f32_16x16x32_bf16 v[172:175], v[208:211], a[16:19], v[172:175]\n"
"ds_write_b128 v248, v[104:107] offset:14336\n"
"v_exp_f32 v176, v176\n"
"v_exp_f32 v177, v177\n"
"v_exp_f32 v178, v178\n"
"v_exp_f32 v179, v179\n"
"v_mfma_f32_16x16x32_bf16 v[172:175], v[212:215], a[20:23], v[172:175]\n"
"s_cbranch_vccnz LPCE0_%=\nLPKE0_%=:\n"
"v_cvt_pk_bf16_f32 v146, v176, v177\n"
"v_cvt_pk_bf16_f32 v147, v178, v179\n"
"v_mfma_f32_16x16x32_bf16 v[176:179], v[208:211], a[32:35], v[188:191]\n"
"v_exp_f32 v152, v152\n"
"v_mfma_f32_16x16x32_bf16 v[176:179], v[212:215], a[36:39], v[176:179]\n"
"v_exp_f32 v153, v153\n"
"v_mfma_f32_16x16x32_bf16 v[176:179], v[216:219], a[40:43], v[176:179]\n"
"v_exp_f32 v154, v154\n"
"v_mfma_f32_16x16x32_bf16 v[176:179], v[220:223], a[44:47], v[176:179]\n"
"v_exp_f32 v155, v155\n"
"s_waitcnt vmcnt(8) lgkmcnt(0)\n"
"s_barrier\n"
"ds_read_b128 v[208:211], v228 offset:16384\n"
"ds_read_b128 v[212:215], v229 offset:16384\n"
"ds_read_b128 v[216:219], v228 offset:17408\n"
"ds_read_b128 v[220:223], v229 offset:17408\n"
"v_cvt_pk_bf16_f32 v126, v152, v153\n"
"v_cvt_pk_bf16_f32 v127, v154, v155\n"
"s_waitcnt lgkmcnt(3)\n"
"v_mfma_f32_16x16x32_bf16 v[152:155], v[208:211], a[0:3], v[180:183]\n"
"v_exp_f32 v156, v156\n"
"s_waitcnt lgkmcnt(2)\n"
"v_mfma_f32_16x16x32_bf16 v[152:155], v[212:215], a[4:7], v[152:155]\n"
"v_exp_f32 v157, v157\n"
"s_waitcnt lgkmcnt(1)\n"
"v_mfma_f32_16x16x32_bf16 v[152:155], v[216:219], a[8:11], v[152:155]\n"
"s_waitcnt lgkmcnt(0)\n"
"s_add_u32 m0, s44, 98304\n"
"v_exp_f32 v158, v158\n"
"v_exp_f32 v159, v159\n"
"v_mfma_f32_16x16x32_bf16 v[152:155], v[220:223], a[12:15], v[152:155]\n"
"buffer_load_dwordx4 v240, s[20:23], s56 offen nt lds\n"
"v_cvt_pk_bf16_f32 v132, v156, v157\n"
"v_cvt_pk_bf16_f32 v133, v158, v159\n"
"v_mfma_f32_16x16x32_bf16 v[156:159], v[216:219], a[24:27], v[184:187]\n"
"v_exp_f32 v160, v160\n"
"v_mfma_f32_16x16x32_bf16 v[156:159], v[220:223], a[28:31], v[156:159]\n"
"v_exp_f32 v161, v161\n"
"v_mfma_f32_16x16x32_bf16 v[156:159], v[208:211], a[16:19], v[156:159]\n"
"v_exp_f32 v162, v162\n"
"v_exp_f32 v163, v163\n"
"v_mfma_f32_16x16x32_bf16 v[156:159], v[212:215], a[20:23], v[156:159]\n"
"v_cvt_pk_bf16_f32 v140, v160, v161\n"
"v_cvt_pk_bf16_f32 v141, v162, v163\n"
"v_mfma_f32_16x16x32_bf16 v[160:163], v[208:211], a[32:35], v[188:191]\n"
"v_exp_f32 v164, v164\n"
"v_mfma_f32_16x16x32_bf16 v[160:163], v[212:215], a[36:39], v[160:163]\n"
"v_exp_f32 v165, v165\n"
"v_mfma_f32_16x16x32_bf16 v[160:163], v[216:219], a[40:43], v[160:163]\n"
"ds_read_b64_tr_b16 a[244:245], v234 offset:0\n"
"ds_read_b64_tr_b16 a[246:247], v234 offset:4096\n"
"v_exp_f32 v166, v166\n"
"v_exp_f32 v167, v167\n"
"v_mfma_f32_16x16x32_bf16 v[160:163], v[220:223], a[44:47], v[160:163]\n"
"v_cvt_pk_bf16_f32 v148, v164, v165\n"
"v_cvt_pk_bf16_f32 v149, v166, v167\n"
"v_mfma_f32_16x16x32_bf16 v[164:167], v[216:219], a[200:203], v[192:195]\n"
"ds_read_b64_tr_b16 a[248:249], v238 offset:0\n"
"ds_read_b64_tr_b16 a[250:251], v238 offset:4096\n"
"v_mfma_f32_16x16x32_bf16 v[164:167], v[220:223], a[204:207], v[164:167]\n"
"v_exp_f32 v168, v168\n"
"v_mfma_f32_16x16x32_bf16 v[164:167], v[208:211], a[192:195], v[164:167]\n"
"s_add_u32 s40, s56, s52\n"
"v_exp_f32 v169, v169\n"
"v_mfma_f32_16x16x32_bf16 v[164:167], v[212:215], a[196:199], v[164:167]\n"
"s_waitcnt lgkmcnt(2)\n"
"buffer_load_dwordx4 v240, s[20:23], s40 offen offset:1024 nt lds\n"
"v_cvt_pk_bf16_f32 v8, v8, v9\n"
"v_cvt_pk_bf16_f32 v9, v10, v11\n"
"v_cvt_pk_bf16_f32 v10, v12, v13\n"
"v_cvt_pk_bf16_f32 v11, v14, v15\n"
"v_exp_f32 v170, v170\n"
"v_exp_f32 v171, v171\n"
"v_mfma_f32_16x16x32_bf16 a[216:219], v[124:127], a[244:247], 0\n"
"v_permlane16_swap_b32 v8, v10\n"
"v_permlane16_swap_b32 v9, v11\n"
"v_accvgpr_write_b32 a148, v8\n"
"v_accvgpr_write_b32 a149, v9\n"
"v_accvgpr_write_b32 a150, v10\n"
"v_accvgpr_write_b32 a151, v11\n"
"v_cvt_pk_bf16_f32 v134, v168, v169\n"
"v_cvt_pk_bf16_f32 v135, v170, v171\n"
"v_mfma_f32_16x16x32_bf16 v[8:11], v[128:131], a[244:247], 0\n"
"s_waitcnt lgkmcnt(0)\n"
"ds_read_b64_tr_b16 v[224:225], v239 offset:0\n"
"ds_read_b64_tr_b16 v[226:227], v239 offset:4096\n"
"v_cvt_pk_bf16_f32 v40, v40, v41\n"
"v_cvt_pk_bf16_f32 v41, v42, v43\n"
"v_cvt_pk_bf16_f32 v42, v44, v45\n"
"v_cvt_pk_bf16_f32 v43, v46, v47\n"
"v_exp_f32 v172, v172\n"
"v_exp_f32 v173, v173\n"
"v_exp_f32 v174, v174\n"
"v_exp_f32 v175, v175\n"
"v_mfma_f32_16x16x32_bf16 v[8:11], v[132:135], a[248:251], v[8:11]\n"
"ds_read_b64_tr_b16 a[252:253], v235 offset:0\n"
"ds_read_b64_tr_b16 a[254:255], v235 offset:4096\n"
"v_permlane16_swap_b32 v40, v42\n"
"v_permlane16_swap_b32 v41, v43\n"
"v_accvgpr_write_b32 a164, v40\n"
"v_accvgpr_write_b32 a165, v41\n"
"v_accvgpr_write_b32 a166, v42\n"
"v_accvgpr_write_b32 a167, v43\n"
"v_cvt_pk_bf16_f32 v142, v172, v173\n"
"v_cvt_pk_bf16_f32 v143, v174, v175\n"
"v_mfma_f32_16x16x32_bf16 v[40:43], v[136:139], a[244:247], 0\n"
"v_cvt_pk_bf16_f32 v72, v72, v73\n"
"v_cvt_pk_bf16_f32 v73, v74, v75\n"
"v_cvt_pk_bf16_f32 v74, v76, v77\n"
"v_cvt_pk_bf16_f32 v75, v78, v79\n"
"v_exp_f32 v176, v176\n"
"v_exp_f32 v177, v177\n"
"v_exp_f32 v178, v178\n"
"v_exp_f32 v179, v179\n"
"v_mfma_f32_16x16x32_bf16 v[40:43], v[140:143], a[248:251], v[40:43]\n"
"v_permlane16_swap_b32 v72, v74\n"
"v_permlane16_swap_b32 v73, v75\n"
"v_accvgpr_write_b32 a180, v72\n"
"v_accvgpr_write_b32 a181, v73\n"
"v_accvgpr_write_b32 a182, v74\n"
"v_accvgpr_write_b32 a183, v75\n"
"v_cvt_pk_bf16_f32 v150, v176, v177\n"
"v_cvt_pk_bf16_f32 v151, v178, v179\n"
"v_mfma_f32_16x16x32_bf16 v[72:75], v[144:147], a[244:247], 0\n"
"ds_read_b128 v[216:219], v228 offset:21504\n"
"v_cvt_pk_bf16_f32 v202, v204, v205\n"
"v_mfma_f32_16x16x32_bf16 v[72:75], v[148:151], a[248:251], v[72:75]\n"
"s_waitcnt lgkmcnt(3)\n"
"ds_read_b128 v[220:223], v229 offset:21504\n"
"v_accvgpr_write_b32 a240, v249\n"
"v_mfma_f32_16x16x32_bf16 v[76:79], v[148:151], v[224:227], 0\n"
"s_waitcnt lgkmcnt(2)\n"
"ds_read_b128 v[208:211], v228 offset:20480\n"
"v_accvgpr_write_b32 a241, v249\n"
"v_mfma_f32_16x16x32_bf16 v[76:79], v[144:147], a[252:255], v[76:79]\n"
"ds_read_b128 v[212:215], v229 offset:20480\n"
"v_accvgpr_write_b32 a242, v249\n"
"v_mfma_f32_16x16x32_bf16 v[44:47], v[140:143], v[224:227], 0\n"
"s_add_u32 s40, s56, s53\n"
"ds_read_b64_tr_b16 a[244:245], v234 offset:1024\n"
"v_accvgpr_write_b32 a243, v249\n"
"v_mfma_f32_16x16x32_bf16 v[44:47], v[136:139], a[252:255], v[44:47]\n"
"buffer_load_dwordx4 v240, s[20:23], s40 offen offset:2048 nt lds\n"
"v_mfma_f32_16x16x32_bf16 v[12:15], v[132:135], v[224:227], 0\n"
"ds_read_b64_tr_b16 a[246:247], v234 offset:5120\n"
"v_mov_b32 v196, 0\n"
"v_mfma_f32_16x16x32_bf16 v[12:15], v[128:131], a[252:255], v[12:15]\n"
"v_mov_b32 v197, 0\n"
"v_mov_b32 v198, 0\n"
"v_mov_b32 v199, 0\n"
"v_mfma_f32_16x16x32_bf16 a[220:223], v[124:127], a[252:255], 0\n"
"v_permlane16_swap_b32 v200, v202\n"
"v_accvgpr_write_b32 a142, v202\n"
"v_accvgpr_write_b32 a143, v203\n"
"v_mov_b32 v202, 0\n"
"v_mov_b32 v203, 0\n"
"v_mov_b32 v204, 0\n"
"v_mov_b32 v205, 0\n"
"v_smfmac_f32_16x16x64_bf16 v[196:199], a[240:243], v[128:135], v241\n"
"ds_read_b64_tr_b16 a[248:249], v238 offset:1024\n"
"v_cvt_pk_bf16_f32 v24, v24, v25\n"
"v_mfma_f32_16x16x32_bf16 v[202:205], a[240:243], v[124:127], v[202:205]\n"
"s_waitcnt lgkmcnt(4)\n"
"ds_read_b64_tr_b16 a[250:251], v238 offset:5120\n"
"v_cvt_pk_bf16_f32 v25, v26, v27\n"
"v_mfma_f32_16x16x32_bf16 v[168:171], v[216:219], a[8:11], v[180:183]\n"
"ds_read_b64_tr_b16 v[224:225], v239 offset:1024\n"
"v_cvt_pk_bf16_f32 v26, v28, v29\n"
"v_mfma_f32_16x16x32_bf16 v[168:171], v[220:223], a[12:15], v[168:171]\n"
"ds_read_b64_tr_b16 v[226:227], v239 offset:5120\n"
"v_cvt_pk_bf16_f32 v27, v30, v31\n"
"v_mfma_f32_16x16x32_bf16 v[168:171], v[208:211], a[0:3], v[168:171]\n"
"s_waitcnt lgkmcnt(5)\n"
"ds_read_b64_tr_b16 a[252:253], v235 offset:1024\n"
"v_cvt_pk_bf16_f32 v56, v56, v57\n"
"v_mfma_f32_16x16x32_bf16 v[168:171], v[212:215], a[4:7], v[168:171]\n"
"ds_read_b64_tr_b16 a[254:255], v235 offset:5120\n"
"v_cvt_pk_bf16_f32 v57, v58, v59\n"
"v_mfma_f32_16x16x32_bf16 v[172:175], v[208:211], a[16:19], v[184:187]\n"
"v_exp_f32 v152, v152\n"
"v_mfma_f32_16x16x32_bf16 v[172:175], v[212:215], a[20:23], v[172:175]\n"
"v_exp_f32 v153, v153\n"
"v_mfma_f32_16x16x32_bf16 v[172:175], v[216:219], a[24:27], v[172:175]\n"
"s_add_u32 s40, s56, s54\n"
"v_exp_f32 v154, v154\n"
"v_mfma_f32_16x16x32_bf16 v[172:175], v[220:223], a[28:31], v[172:175]\n"
"buffer_load_dwordx4 v240, s[20:23], s40 offen offset:3072 nt lds\n"
"v_mfma_f32_16x16x32_bf16 v[176:179], v[216:219], a[40:43], v[188:191]\n"
"s_add_u32 s55, s55, s59\n"
"v_exp_f32 v155, v155\n"
"v_mfma_f32_16x16x32_bf16 v[176:179], v[220:223], a[44:47], v[176:179]\n"
"s_add_u32 s41, s55, s57\n"
"v_cvt_pk_bf16_f32 v96, v152, v153\n"
"v_cvt_pk_bf16_f32 v97, v154, v155\n"
"v_mfma_f32_16x16x32_bf16 v[176:179], v[208:211], a[32:35], v[176:179]\n"
"s_and_b32 s41, s41, s58\n"
"v_permlane16_swap_b32 v24, v26\n"
"v_mfma_f32_16x16x32_bf16 v[176:179], v[212:215], a[36:39], v[176:179]\n"
"s_andn2_b32 s56, s55, s58\n"
"v_permlane16_swap_b32 v25, v27\n"
"v_mfma_f32_16x16x32_bf16 v[152:155], v[208:211], a[192:195], v[192:195]\n"
"s_or_b32 s56, s56, s41\n"
"ds_read_b128 v[208:211], v230 offset:16384\n"
"v_accvgpr_write_b32 a156, v24\n"
"v_mfma_f32_16x16x32_bf16 v[152:155], v[212:215], a[196:199], v[152:155]\n"
"ds_read_b128 v[212:215], v231 offset:16384\n"
"v_accvgpr_write_b32 a157, v25\n"
"v_mfma_f32_16x16x32_bf16 v[152:155], v[216:219], a[200:203], v[152:155]\n"
"ds_read_b128 v[216:219], v230 offset:17408\n"
"v_accvgpr_write_b32 a158, v26\n"
"v_mfma_f32_16x16x32_bf16 v[152:155], v[220:223], a[204:207], v[152:155]\n"
"s_waitcnt lgkmcnt(3)\n"
"ds_read_b128 v[220:223], v231 offset:17408\n"
"v_accvgpr_write_b32 a159, v27\n"
"v_mfma_f32_16x16x32_bf16 a[232:235], v[124:127], a[244:247], 0\n"
"v_cvt_pk_bf16_f32 v58, v60, v61\n"
"v_cvt_pk_bf16_f32 v59, v62, v63\n"
"v_mfma_f32_16x16x32_bf16 v[24:27], v[128:131], a[244:247], 0\n"
"s_add_u32 m0, s44, 49152\n"
"v_permlane16_swap_b32 v56, v58\n"
"v_mfma_f32_16x16x32_bf16 v[24:27], v[132:135], a[248:251], v[24:27]\n"
"buffer_load_dwordx4 v240, s[16:19], s56 offen nt lds\n"
"v_permlane16_swap_b32 v57, v59\n"
"v_accvgpr_write_b32 a172, v56\n"
"v_accvgpr_write_b32 a173, v57\n"
"v_accvgpr_write_b32 a174, v58\n"
"v_accvgpr_write_b32 a175, v59\n"
"v_mfma_f32_16x16x32_bf16 v[56:59], v[136:139], a[244:247], 0\n"
"v_cvt_pk_bf16_f32 v88, v88, v89\n"
"v_cvt_pk_bf16_f32 v89, v90, v91\n"
"v_cvt_pk_bf16_f32 v90, v92, v93\n"
"v_cvt_pk_bf16_f32 v91, v94, v95\n"
"v_mfma_f32_16x16x32_bf16 v[56:59], v[140:143], a[248:251], v[56:59]\n"
"v_permlane16_swap_b32 v88, v90\n"
"v_permlane16_swap_b32 v89, v91\n"
"v_accvgpr_write_b32 a188, v88\n"
"v_accvgpr_write_b32 a189, v89\n"
"v_accvgpr_write_b32 a190, v90\n"
"v_accvgpr_write_b32 a191, v91\n"
"v_mfma_f32_16x16x32_bf16 v[88:91], v[144:147], a[244:247], 0\n"
"v_exp_f32 v156, v156\n"
"v_mfma_f32_16x16x32_bf16 v[88:91], v[148:151], a[248:251], v[88:91]\n"
"v_accvgpr_write_b32 a140, v200\n"
"v_accvgpr_write_b32 a141, v201\n"
"v_mfma_f32_16x16x32_bf16 v[92:95], v[148:151], v[224:227], 0\n"
"v_mov_b32 v200, 0\n"
"v_mov_b32 v201, 0\n"
"v_mfma_f32_16x16x32_bf16 v[92:95], v[144:147], a[252:255], v[92:95]\n"
"v_exp_f32 v157, v157\n"
"v_mfma_f32_16x16x32_bf16 v[60:63], v[140:143], v[224:227], 0\n"
"v_exp_f32 v158, v158\n"
"v_mfma_f32_16x16x32_bf16 v[60:63], v[136:139], a[252:255], v[60:63]\n"
"v_exp_f32 v159, v159\n"
"v_mfma_f32_16x16x32_bf16 v[28:31], v[132:135], v[224:227], 0\n"
"v_cvt_pk_bf16_f32 v104, v156, v157\n"
"v_cvt_pk_bf16_f32 v105, v158, v159\n"
"v_mfma_f32_16x16x32_bf16 v[28:31], v[128:131], a[252:255], v[28:31]\n"
"v_exp_f32 v160, v160\n"
"v_mfma_f32_16x16x32_bf16 a[236:239], v[124:127], a[252:255], 0\n"
"s_add_u32 s40, s56, s52\n"
"v_exp_f32 v161, v161\n"
"v_smfmac_f32_16x16x64_bf16 v[198:201], a[240:243], v[136:143], v241\n"
"s_waitcnt lgkmcnt(0)\n"
"buffer_load_dwordx4 v240, s[16:19], s40 offen offset:1024 nt lds\n"
"v_mfma_f32_16x16x32_bf16 v[156:159], v[208:211], a[0:3], v[180:183]\n"
"v_exp_f32 v162, v162\n"
"v_mfma_f32_16x16x32_bf16 v[156:159], v[212:215], a[4:7], v[156:159]\n"
"v_exp_f32 v163, v163\n"
"v_mfma_f32_16x16x32_bf16 v[156:159], v[216:219], a[8:11], v[156:159]\n"
"v_accvgpr_write_b32 a96, v112\n"
"v_accvgpr_write_b32 a97, v113\n"
"v_mfma_f32_16x16x32_bf16 v[156:159], v[220:223], a[12:15], v[156:159]\n"
"v_cvt_pk_bf16_f32 v112, v160, v161\n"
"v_cvt_pk_bf16_f32 v113, v162, v163\n"
"v_mfma_f32_16x16x32_bf16 v[160:163], v[216:219], a[24:27], v[184:187]\n"
"v_exp_f32 v164, v164\n"
"v_mfma_f32_16x16x32_bf16 v[160:163], v[220:223], a[28:31], v[160:163]\n"
"v_exp_f32 v165, v165\n"
"v_mfma_f32_16x16x32_bf16 v[160:163], v[208:211], a[16:19], v[160:163]\n"
"ds_read_b64_tr_b16 a[244:245], v232 offset:0\n"
"ds_read_b64_tr_b16 a[246:247], v232 offset:4096\n"
"v_exp_f32 v166, v166\n"
"v_exp_f32 v167, v167\n"
"v_mfma_f32_16x16x32_bf16 v[160:163], v[212:215], a[20:23], v[160:163]\n"
"ds_read_b64_tr_b16 a[248:249], v236 offset:0\n"
"v_accvgpr_write_b32 a100, v120\n"
"v_accvgpr_write_b32 a101, v121\n"
"v_cvt_pk_bf16_f32 v120, v164, v165\n"
"v_cvt_pk_bf16_f32 v121, v166, v167\n"
"v_mfma_f32_16x16x32_bf16 v[164:167], v[208:211], a[32:35], v[188:191]\n"
"ds_read_b64_tr_b16 a[250:251], v236 offset:4096\n"
"v_cvt_pk_bf16_f32 v0, v0, v1\n"
"v_mfma_f32_16x16x32_bf16 v[164:167], v[212:215], a[36:39], v[164:167]\n"
"ds_read_b64_tr_b16 v[224:225], v237 offset:0\n"
"v_cvt_pk_bf16_f32 v1, v2, v3\n"
"v_mfma_f32_16x16x32_bf16 v[164:167], v[216:219], a[40:43], v[164:167]\n"
"ds_read_b64_tr_b16 v[226:227], v237 offset:4096\n"
"v_cvt_pk_bf16_f32 v2, v4, v5\n"
"v_mfma_f32_16x16x32_bf16 v[164:167], v[220:223], a[44:47], v[164:167]\n"
"s_waitcnt lgkmcnt(4)\n"
"v_cvt_pk_bf16_f32 v3, v6, v7\n"
"v_cvt_pk_bf16_f32 v32, v32, v33\n"
"v_mfma_f32_16x16x32_bf16 a[208:211], v[124:127], a[244:247], 0\n"
"v_permlane16_swap_b32 v0, v2\n"
"v_permlane16_swap_b32 v1, v3\n"
"v_accvgpr_write_b32 a144, v0\n"
"v_accvgpr_write_b32 a145, v1\n"
"v_accvgpr_write_b32 a146, v2\n"
"v_accvgpr_write_b32 a147, v3\n"
"v_mfma_f32_16x16x32_bf16 v[0:3], v[128:131], a[244:247], 0\n"
"s_waitcnt lgkmcnt(2)\n"
"s_add_u32 s40, s56, s53\n"
"ds_read_b64_tr_b16 a[252:253], v233 offset:0\n"
"ds_read_b64_tr_b16 a[254:255], v233 offset:4096\n"
"v_cvt_pk_bf16_f32 v33, v34, v35\n"
"v_cvt_pk_bf16_f32 v34, v36, v37\n"
"v_cvt_pk_bf16_f32 v35, v38, v39\n"
"v_mfma_f32_16x16x32_bf16 v[0:3], v[132:135], a[248:251], v[0:3]\n"
"buffer_load_dwordx4 v240, s[16:19], s40 offen offset:2048 nt lds\n"
"v_permlane16_swap_b32 v32, v34\n"
"v_permlane16_swap_b32 v33, v35\n"
"v_accvgpr_write_b32 a160, v32\n"
"v_accvgpr_write_b32 a161, v33\n"
"v_accvgpr_write_b32 a162, v34\n"
"v_accvgpr_write_b32 a163, v35\n"
"v_mfma_f32_16x16x32_bf16 v[32:35], v[136:139], a[244:247], 0\n"
"v_cvt_pk_bf16_f32 v64, v64, v65\n"
"v_cvt_pk_bf16_f32 v65, v66, v67\n"
"v_cvt_pk_bf16_f32 v66, v68, v69\n"
"v_cvt_pk_bf16_f32 v67, v70, v71\n"
"v_mfma_f32_16x16x32_bf16 v[32:35], v[140:143], a[248:251], v[32:35]\n"
"v_permlane16_swap_b32 v64, v66\n"
"v_permlane16_swap_b32 v65, v67\n"
"v_accvgpr_write_b32 a176, v64\n"
"v_accvgpr_write_b32 a177, v65\n"
"v_accvgpr_write_b32 a178, v66\n"
"v_accvgpr_write_b32 a179, v67\n"
"v_mfma_f32_16x16x32_bf16 v[64:67], v[144:147], a[244:247], 0\n"
"v_exp_f32 v168, v168\n"
"v_mfma_f32_16x16x32_bf16 v[64:67], v[148:151], a[248:251], v[64:67]\n"
"s_waitcnt lgkmcnt(2)\n"
"v_exp_f32 v169, v169\n"
"v_mfma_f32_16x16x32_bf16 v[68:71], v[148:151], v[224:227], 0\n"
"s_waitcnt lgkmcnt(0)\n"
"v_exp_f32 v170, v170\n"
"v_mfma_f32_16x16x32_bf16 v[68:71], v[144:147], a[252:255], v[68:71]\n"
"ds_read_b128 v[208:211], v230 offset:20480\n"
"v_cvt_pk_bf16_f32 v98, v168, v169\n"
"v_mfma_f32_16x16x32_bf16 v[36:39], v[140:143], v[224:227], 0\n"
"ds_read_b128 v[212:215], v231 offset:20480\n"
"v_accvgpr_write_b32 a98, v114\n"
"v_mfma_f32_16x16x32_bf16 v[36:39], v[136:139], a[252:255], v[36:39]\n"
"ds_read_b128 v[216:219], v230 offset:21504\n"
"v_accvgpr_write_b32 a99, v115\n"
"v_mfma_f32_16x16x32_bf16 v[4:7], v[132:135], v[224:227], 0\n"
"s_add_u32 s40, s56, s54\n"
"ds_read_b128 v[220:223], v231 offset:21504\n"
"v_cvt_pk_bf16_f32 v16, v16, v17\n"
"v_mfma_f32_16x16x32_bf16 v[4:7], v[128:131], a[252:255], v[4:7]\n"
"buffer_load_dwordx4 v240, s[16:19], s40 offen offset:3072 nt lds\n"
"v_mfma_f32_16x16x32_bf16 a[212:215], v[124:127], a[252:255], 0\n"
"v_exp_f32 v171, v171\n"
"v_smfmac_f32_16x16x64_bf16 v[200:203], a[240:243], v[144:151], v241\n"
"s_waitcnt lgkmcnt(3)\n"
"v_cvt_pk_bf16_f32 v99, v170, v171\n"
"v_cvt_pk_bf16_f32 v17, v18, v19\n"
"v_mfma_f32_16x16x32_bf16 v[168:171], v[208:211], a[0:3], v[180:183]\n"
"s_waitcnt lgkmcnt(2)\n"
"v_exp_f32 v172, v172\n"
"v_mfma_f32_16x16x32_bf16 v[168:171], v[212:215], a[4:7], v[168:171]\n"
"s_waitcnt lgkmcnt(1)\n"
"v_exp_f32 v173, v173\n"
"v_mfma_f32_16x16x32_bf16 v[168:171], v[216:219], a[8:11], v[168:171]\n"
"s_waitcnt lgkmcnt(0)\n"
"v_exp_f32 v174, v174\n"
"v_exp_f32 v175, v175\n"
"v_mfma_f32_16x16x32_bf16 v[168:171], v[220:223], a[12:15], v[168:171]\n"
"v_cvt_pk_bf16_f32 v106, v172, v173\n"
"v_cvt_pk_bf16_f32 v107, v174, v175\n"
"v_mfma_f32_16x16x32_bf16 v[172:175], v[216:219], a[24:27], v[184:187]\n"
"v_exp_f32 v176, v176\n"
"v_mfma_f32_16x16x32_bf16 v[172:175], v[220:223], a[28:31], v[172:175]\n"
"v_exp_f32 v177, v177\n"
"v_mfma_f32_16x16x32_bf16 v[172:175], v[208:211], a[16:19], v[172:175]\n"
"ds_read_b64_tr_b16 a[244:245], v232 offset:1024\n"
"ds_read_b64_tr_b16 a[246:247], v232 offset:5120\n"
"v_exp_f32 v178, v178\n"
"v_exp_f32 v179, v179\n"
"v_mfma_f32_16x16x32_bf16 v[172:175], v[212:215], a[20:23], v[172:175]\n"
"s_cbranch_vccnz LPCE1_%=\nLPKE1_%=:\n"
"ds_read_b64_tr_b16 a[248:249], v236 offset:1024\n"
"v_cvt_pk_bf16_f32 v114, v176, v177\n"
"v_cvt_pk_bf16_f32 v115, v178, v179\n"
"v_mfma_f32_16x16x32_bf16 v[176:179], v[208:211], a[32:35], v[188:191]\n"
"ds_read_b64_tr_b16 a[250:251], v236 offset:5120\n"
"v_cvt_pk_bf16_f32 v18, v20, v21\n"
"v_mfma_f32_16x16x32_bf16 v[176:179], v[212:215], a[36:39], v[176:179]\n"
"s_nop 0\n"
"v_permlane16_swap_b32 v16, v18\n"
"v_mfma_f32_16x16x32_bf16 v[176:179], v[216:219], a[40:43], v[176:179]\n"
"v_cvt_pk_bf16_f32 v19, v22, v23\n"
"v_accvgpr_write_b32 a152, v16\n"
"v_mfma_f32_16x16x32_bf16 v[176:179], v[220:223], a[44:47], v[176:179]\n"
"s_waitcnt lgkmcnt(2)\n"
"v_permlane16_swap_b32 v17, v19\n"
"v_mfma_f32_16x16x32_bf16 a[224:227], v[124:127], a[244:247], 0\n"
"ds_read_b64_tr_b16 v[224:225], v237 offset:1024\n"
"ds_read_b64_tr_b16 v[226:227], v237 offset:5120\n"
"v_accvgpr_write_b32 a153, v17\n"
"v_accvgpr_write_b32 a154, v18\n"
"v_accvgpr_write_b32 a155, v19\n"
"v_mfma_f32_16x16x32_bf16 v[16:19], v[128:131], a[244:247], 0\n"
"s_waitcnt lgkmcnt(2)\n"
"s_waitcnt vmcnt(8)\n"
"ds_read_b64_tr_b16 a[252:253], v233 offset:1024\n"
"ds_read_b64_tr_b16 a[254:255], v233 offset:5120\n"
"v_cvt_pk_bf16_f32 v48, v48, v49\n"
"v_cvt_pk_bf16_f32 v49, v50, v51\n"
"v_cvt_pk_bf16_f32 v50, v52, v53\n"
"v_cvt_pk_bf16_f32 v51, v54, v55\n"
"v_mfma_f32_16x16x32_bf16 v[16:19], v[132:135], a[248:251], v[16:19]\n"
"s_barrier\n"
"v_permlane16_swap_b32 v48, v50\n"
"v_permlane16_swap_b32 v49, v51\n"
"v_accvgpr_write_b32 a168, v48\n"
"v_accvgpr_write_b32 a169, v49\n"
"v_accvgpr_write_b32 a170, v50\n"
"v_accvgpr_write_b32 a171, v51\n"
"v_mfma_f32_16x16x32_bf16 v[48:51], v[136:139], a[244:247], 0\n"
"v_cvt_pk_bf16_f32 v80, v80, v81\n"
"v_cvt_pk_bf16_f32 v81, v82, v83\n"
"v_cvt_pk_bf16_f32 v82, v84, v85\n"
"v_cvt_pk_bf16_f32 v83, v86, v87\n"
"v_mfma_f32_16x16x32_bf16 v[48:51], v[140:143], a[248:251], v[48:51]\n"
"v_permlane16_swap_b32 v80, v82\n"
"v_permlane16_swap_b32 v81, v83\n"
"v_accvgpr_write_b32 a184, v80\n"
"v_accvgpr_write_b32 a185, v81\n"
"v_accvgpr_write_b32 a186, v82\n"
"v_accvgpr_write_b32 a187, v83\n"
"v_mfma_f32_16x16x32_bf16 v[80:83], v[144:147], a[244:247], 0\n"
"v_exp_f32 v152, v152\n"
"v_mfma_f32_16x16x32_bf16 v[80:83], v[148:151], a[248:251], v[80:83]\n"
"s_waitcnt lgkmcnt(2)\n"
"v_exp_f32 v153, v153\n"
"v_mfma_f32_16x16x32_bf16 v[84:87], v[148:151], v[224:227], 0\n"
"s_waitcnt lgkmcnt(0)\n"
"ds_read_b128 v[208:211], v228 offset:32768\n"
"v_accvgpr_write_b32 a102, v122\n"
"v_mfma_f32_16x16x32_bf16 v[84:87], v[144:147], a[252:255], v[84:87]\n"
"ds_read_b128 v[212:215], v229 offset:32768\n"
"v_accvgpr_write_b32 a103, v123\n"
"v_mfma_f32_16x16x32_bf16 v[52:55], v[140:143], v[224:227], 0\n"
"ds_read_b128 v[216:219], v228 offset:33792\n"
"v_bfe_u32 v246, v242, 8, 2\n"
"v_mfma_f32_16x16x32_bf16 v[52:55], v[136:139], a[252:255], v[52:55]\n"
"ds_read_b128 v[220:223], v229 offset:33792\n"
"v_lshlrev_b32 v246, 6, v246\n"
"v_mfma_f32_16x16x32_bf16 v[20:23], v[132:135], v[224:227], 0\n"
"ds_read_b64_tr_b16 a[244:245], v234 offset:16384\n"
"ds_read_b64_tr_b16 a[246:247], v234 offset:20480\n"
"v_mfma_f32_16x16x32_bf16 v[20:23], v[128:131], a[252:255], v[20:23]\n"
"ds_read_b64_tr_b16 a[248:249], v238 offset:16384\n"
"ds_read_b64_tr_b16 a[250:251], v238 offset:20480\n"
"v_exp_f32 v154, v154\n"
"v_exp_f32 v155, v155\n"
"v_exp_f32 v156, v156\n"
"v_exp_f32 v157, v157\n"
"v_exp_f32 v158, v158\n"
"v_exp_f32 v159, v159\n"
"v_exp_f32 v160, v160\n"
"v_exp_f32 v161, v161\n"
"v_exp_f32 v162, v162\n"
"v_exp_f32 v163, v163\n"
"v_exp_f32 v164, v164\n"
"v_exp_f32 v165, v165\n"
"v_exp_f32 v166, v166\n"
"v_exp_f32 v167, v167\n"
"v_mfma_f32_16x16x32_bf16 a[228:231], v[124:127], a[252:255], 0\n"
"s_waitcnt lgkmcnt(7)\n"
"ds_read_b64_tr_b16 a[254:255], v235 offset:20480\n"
"ds_read_b64_tr_b16 v[224:225], v239 offset:16384\n"
"v_xor_b32 v246, v246, v242\n"
"v_bfe_u32 v249, v242, 6, 4\n"
"v_xor_b32 v247, v247, v249\n"
"v_lshl_add_u32 v247, v247, 4, s44\n"
"v_cvt_pk_bf16_f32 v122, v152, v153\n"
"v_cvt_pk_bf16_f32 v123, v154, v155\n"
"v_cvt_pk_bf16_f32 v100, v156, v157\n"
"v_cvt_pk_bf16_f32 v101, v158, v159\n"
"v_cvt_pk_bf16_f32 v108, v160, v161\n"
"v_cvt_pk_bf16_f32 v109, v162, v163\n"
"v_cvt_pk_bf16_f32 v116, v164, v165\n"
"v_cvt_pk_bf16_f32 v117, v166, v167\n"
"v_exp_f32 v169, v169\n"
"v_exp_f32 v172, v172\n"
"v_exp_f32 v173, v173\n"
"v_exp_f32 v175, v175\n"
"s_branch LAY2_%=\n"
"LAL_%=:\n"
"v_mfma_f32_16x16x32_bf16 a[224:227], v[124:127], a[244:247], a[224:227]\n"
"v_exp_f32 v169, v169\n"
"v_mfma_f32_16x16x32_bf16 v[16:19], v[128:131], a[244:247], v[16:19]\n"
"s_waitcnt vmcnt(8)\n"
"v_exp_f32 v172, v172\n"
"v_mfma_f32_16x16x32_bf16 v[16:19], v[132:135], a[248:251], v[16:19]\n"
"s_barrier\n"
"ds_read_b128 v[216:219], v228 offset:1024\n"
"v_mfma_f32_16x16x32_bf16 v[48:51], v[136:139], a[244:247], v[48:51]\n"
"ds_read_b128 v[212:215], v229 offset:0\n"
"v_cvt_pk_bf16_f32 v108, v160, v161\n"
"v_mfma_f32_16x16x32_bf16 v[48:51], v[140:143], a[248:251], v[48:51]\n"
"ds_read_b128 v[208:211], v228 offset:0\n"
"v_cvt_pk_bf16_f32 v100, v156, v157\n"
"v_mfma_f32_16x16x32_bf16 v[80:83], v[144:147], a[244:247], v[80:83]\n"
"ds_read_b128 v[220:223], v229 offset:1024\n"
"v_cvt_pk_bf16_f32 v117, v166, v167\n"
"v_mfma_f32_16x16x32_bf16 v[80:83], v[148:151], a[248:251], v[80:83]\n"
"ds_read_b64_tr_b16 a[244:245], v234 offset:49152\n"
"v_cvt_pk_bf16_f32 v109, v162, v163\n"
"v_mfma_f32_16x16x32_bf16 v[84:87], v[148:151], v[224:227], v[84:87]\n"
"ds_read_b64_tr_b16 a[248:249], v238 offset:49152\n"
"v_mfma_f32_16x16x32_bf16 v[84:87], v[144:147], a[252:255], v[84:87]\n"
"ds_read_b64_tr_b16 a[246:247], v234 offset:53248\n"
"v_mfma_f32_16x16x32_bf16 v[52:55], v[140:143], v[224:227], v[52:55]\n"
"ds_read_b64_tr_b16 a[250:251], v238 offset:53248\n"
"v_mfma_f32_16x16x32_bf16 v[52:55], v[136:139], a[252:255], v[52:55]\n"
"v_exp_f32 v175, v175\n"
"v_mfma_f32_16x16x32_bf16 v[20:23], v[132:135], v[224:227], v[20:23]\n"
"ds_read_b64_tr_b16 v[224:225], v239 offset:49152\n"
"v_mfma_f32_16x16x32_bf16 v[20:23], v[128:131], a[252:255], v[20:23]\n"
"v_exp_f32 v173, v173\n"
"v_mfma_f32_16x16x32_bf16 a[228:231], v[124:127], a[252:255], a[228:231]\n"
"s_waitcnt lgkmcnt(5)\n"
"ds_read_b64_tr_b16 a[254:255], v235 offset:53248\n"
"v_cvt_pk_bf16_f32 v116, v164, v165\n"
"v_mfma_f32_16x16x32_bf16 v[152:155], v[208:211], a[0:3], v[180:183]\n"
"v_exp_f32 v174, v174\n"
"v_mfma_f32_16x16x32_bf16 v[152:155], v[212:215], a[4:7], v[152:155]\n"
"ds_read_b64_tr_b16 a[252:253], v235 offset:49152\n"
"v_cvt_pk_bf16_f32 v111, v174, v175\n"
"v_mfma_f32_16x16x32_bf16 v[152:155], v[216:219], a[8:11], v[152:155]\n"
"s_add_u32 m0, s44, 81920\n"
"ds_read_b64_tr_b16 v[226:227], v239 offset:53248\n"
"v_mfma_f32_16x16x32_bf16 v[152:155], v[220:223], a[12:15], v[152:155]\n"
"buffer_load_dwordx4 v240, s[20:23], s56 offen nt lds\n"
"v_mfma_f32_16x16x32_bf16 v[156:159], v[216:219], a[24:27], v[184:187]\n"
"v_exp_f32 v171, v171\n"
"v_mfma_f32_16x16x32_bf16 v[156:159], v[220:223], a[28:31], v[156:159]\n"
"v_exp_f32 v168, v168\n"
"v_mfma_f32_16x16x32_bf16 v[156:159], v[208:211], a[16:19], v[156:159]\n"
"v_exp_f32 v176, v176\n"
"v_mfma_f32_16x16x32_bf16 v[156:159], v[212:215], a[20:23], v[156:159]\n"
"v_exp_f32 v177, v177\n"
"v_mfma_f32_16x16x32_bf16 v[160:163], v[208:211], a[32:35], v[188:191]\n"
"v_exp_f32 v179, v179\n"
"v_mfma_f32_16x16x32_bf16 v[160:163], v[212:215], a[36:39], v[160:163]\n"
"v_exp_f32 v170, v170\n"
"v_mfma_f32_16x16x32_bf16 v[160:163], v[216:219], a[40:43], v[160:163]\n"
"v_exp_f32 v153, v153\n"
"v_mfma_f32_16x16x32_bf16 v[160:163], v[220:223], a[44:47], v[160:163]\n"
"v_cvt_pk_bf16_f32 v102, v168, v169\n"
"v_mfma_f32_16x16x32_bf16 v[164:167], v[216:219], a[200:203], v[192:195]\n"
"ds_read_b128 v[216:219], v228 offset:5120\n"
"v_cvt_pk_bf16_f32 v103, v170, v171\n"
"v_mfma_f32_16x16x32_bf16 v[164:167], v[220:223], a[204:207], v[164:167]\n"
"v_cvt_pk_bf16_f32 v110, v172, v173\n"
"v_cvt_pk_bf16_f32 v118, v176, v177\n"
"v_mfma_f32_16x16x32_bf16 v[164:167], v[208:211], a[192:195], v[164:167]\n"
"s_add_u32 s40, s56, s52\n"
"ds_read_b128 v[208:211], v228 offset:4096\n"
"v_mfma_f32_16x16x32_bf16 v[164:167], v[212:215], a[196:199], v[164:167]\n"
"s_waitcnt lgkmcnt(2)\n"
"buffer_load_dwordx4 v240, s[20:23], s40 offen offset:1024 nt lds\n"
"v_mfma_f32_16x16x32_bf16 a[216:219], v[120:123], a[244:247], a[216:219]\n"
"v_exp_f32 v155, v155\n"
"v_mfma_f32_16x16x32_bf16 v[8:11], v[96:99], a[244:247], v[8:11]\n"
"v_exp_f32 v161, v161\n"
"v_mfma_f32_16x16x32_bf16 v[8:11], v[100:103], a[248:251], v[8:11]\n"
"v_exp_f32 v163, v163\n"
"v_mfma_f32_16x16x32_bf16 v[40:43], v[104:107], a[244:247], v[40:43]\n"
"v_exp_f32 v178, v178\n"
"v_mfma_f32_16x16x32_bf16 v[40:43], v[108:111], a[248:251], v[40:43]\n"
"ds_read_b128 v[220:223], v229 offset:5120\n"
"v_cvt_pk_bf16_f32 v119, v178, v179\n"
"v_mfma_f32_16x16x32_bf16 v[72:75], v[112:115], a[244:247], v[72:75]\n"
"ds_read_b64_tr_b16 a[244:245], v234 offset:50176\n"
"v_mfma_f32_16x16x32_bf16 v[72:75], v[116:119], a[248:251], v[72:75]\n"
"v_exp_f32 v158, v158\n"
"v_mfma_f32_16x16x32_bf16 v[76:79], v[116:119], v[224:227], v[76:79]\n"
"ds_read_b128 v[212:215], v229 offset:4096\n"
"v_mfma_f32_16x16x32_bf16 v[76:79], v[112:115], a[252:255], v[76:79]\n"
"v_exp_f32 v160, v160\n"
"v_mfma_f32_16x16x32_bf16 v[44:47], v[108:111], v[224:227], v[44:47]\n"
"s_add_u32 s40, s56, s53\n"
"ds_read_b64_tr_b16 a[248:249], v238 offset:50176\n"
"v_mfma_f32_16x16x32_bf16 v[44:47], v[104:107], a[252:255], v[44:47]\n"
"buffer_load_dwordx4 v240, s[20:23], s40 offen offset:2048 nt lds\n"
"v_mfma_f32_16x16x32_bf16 v[12:15], v[100:103], v[224:227], v[12:15]\n"
"v_exp_f32 v165, v165\n"
"v_mfma_f32_16x16x32_bf16 v[12:15], v[96:99], a[252:255], v[12:15]\n"
"v_exp_f32 v159, v159\n"
"v_mfma_f32_16x16x32_bf16 a[220:223], v[120:123], a[252:255], a[220:223]\n"
"v_exp_f32 v152, v152\n"
"v_smfmac_f32_16x16x64_bf16 v[196:199], a[240:243], v[96:103], v241\n"
"v_mfma_f32_16x16x32_bf16 v[202:205], a[240:243], v[120:123], v[202:205]\n"
"s_waitcnt lgkmcnt(1)\n"
"ds_read_b64_tr_b16 v[224:225], v239 offset:50176\n"
"v_cvt_pk_bf16_f32 v128, v152, v153\n"
"v_mfma_f32_16x16x32_bf16 v[168:171], v[216:219], a[8:11], v[180:183]\n"
"v_exp_f32 v162, v162\n"
"v_mfma_f32_16x16x32_bf16 v[168:171], v[220:223], a[12:15], v[168:171]\n"
"ds_read_b64_tr_b16 a[250:251], v238 offset:54272\n"
"v_mfma_f32_16x16x32_bf16 v[168:171], v[208:211], a[0:3], v[168:171]\n"
"ds_read_b64_tr_b16 a[252:253], v235 offset:50176\n"
"v_mfma_f32_16x16x32_bf16 v[168:171], v[212:215], a[4:7], v[168:171]\n"
"v_exp_f32 v154, v154\n"
"v_mfma_f32_16x16x32_bf16 v[172:175], v[208:211], a[16:19], v[184:187]\n"
"ds_read_b64_tr_b16 v[226:227], v239 offset:54272\n"
"v_mfma_f32_16x16x32_bf16 v[172:175], v[212:215], a[20:23], v[172:175]\n"
"ds_read_b64_tr_b16 a[246:247], v234 offset:54272\n"
"v_cvt_pk_bf16_f32 v129, v154, v155\n"
"v_mfma_f32_16x16x32_bf16 v[172:175], v[216:219], a[24:27], v[172:175]\n"
"s_add_u32 s40, s56, s54\n"
"ds_read_b64_tr_b16 a[254:255], v235 offset:54272\n"
"v_mfma_f32_16x16x32_bf16 v[172:175], v[220:223], a[28:31], v[172:175]\n"
"buffer_load_dwordx4 v240, s[20:23], s40 offen offset:3072 nt lds\n"
"v_mfma_f32_16x16x32_bf16 v[176:179], v[216:219], a[40:43], v[188:191]\n"
"s_add_u32 s55, s55, s59\n"
"v_exp_f32 v157, v157\n"
"v_mfma_f32_16x16x32_bf16 v[176:179], v[220:223], a[44:47], v[176:179]\n"
"s_add_u32 s41, s55, s57\n"
"v_exp_f32 v170, v170\n"
"v_mfma_f32_16x16x32_bf16 v[176:179], v[208:211], a[32:35], v[176:179]\n"
"s_and_b32 s41, s41, s58\n"
"v_exp_f32 v156, v156\n"
"v_mfma_f32_16x16x32_bf16 v[176:179], v[212:215], a[36:39], v[176:179]\n"
"s_andn2_b32 s56, s55, s58\n"
"v_exp_f32 v166, v166\n"
"v_mfma_f32_16x16x32_bf16 v[152:155], v[208:211], a[192:195], v[192:195]\n"
"s_or_b32 s56, s56, s41\n"
"ds_read_b128 v[208:211], v230 offset:0\n"
"v_mfma_f32_16x16x32_bf16 v[152:155], v[212:215], a[196:199], v[152:155]\n"
"v_exp_f32 v174, v174\n"
"v_mfma_f32_16x16x32_bf16 v[152:155], v[216:219], a[200:203], v[152:155]\n"
"ds_read_b128 v[212:215], v231 offset:0\n"
"v_mfma_f32_16x16x32_bf16 v[152:155], v[220:223], a[204:207], v[152:155]\n"
"s_waitcnt lgkmcnt(2)\n"
"ds_read_b128 v[216:219], v230 offset:1024\n"
"v_cvt_pk_bf16_f32 v137, v158, v159\n"
"v_mfma_f32_16x16x32_bf16 a[232:235], v[120:123], a[244:247], a[232:235]\n"
"v_exp_f32 v164, v164\n"
"v_mfma_f32_16x16x32_bf16 v[24:27], v[96:99], a[244:247], v[24:27]\n"
"s_add_u32 m0, s44, 32768\n"
"ds_read_b128 v[220:223], v231 offset:1024\n"
"v_mfma_f32_16x16x32_bf16 v[24:27], v[100:103], a[248:251], v[24:27]\n"
"buffer_load_dwordx4 v240, s[16:19], s56 offen nt lds\n"
"v_mfma_f32_16x16x32_bf16 v[56:59], v[104:107], a[244:247], v[56:59]\n"
"v_exp_f32 v167, v167\n"
"v_mfma_f32_16x16x32_bf16 v[56:59], v[108:111], a[248:251], v[56:59]\n"
"v_exp_f32 v176, v176\n"
"v_mfma_f32_16x16x32_bf16 v[88:91], v[112:115], a[244:247], v[88:91]\n"
"v_exp_f32 v153, v153\n"
"v_mfma_f32_16x16x32_bf16 v[88:91], v[116:119], a[248:251], v[88:91]\n"
"v_exp_f32 v173, v173\n"
"v_mfma_f32_16x16x32_bf16 v[92:95], v[116:119], v[224:227], v[92:95]\n"
"ds_read_b64_tr_b16 a[250:251], v236 offset:53248\n"
"v_cvt_pk_bf16_f32 v125, v166, v167\n"
"v_mfma_f32_16x16x32_bf16 v[92:95], v[112:115], a[252:255], v[92:95]\n"
"ds_read_b64_tr_b16 a[244:245], v232 offset:49152\n"
"v_cvt_pk_bf16_f32 v144, v160, v161\n"
"v_mfma_f32_16x16x32_bf16 v[60:63], v[108:111], v[224:227], v[60:63]\n"
"ds_read_b64_tr_b16 a[248:249], v236 offset:49152\n"
"v_cvt_pk_bf16_f32 v124, v164, v165\n"
"v_mfma_f32_16x16x32_bf16 v[60:63], v[104:107], a[252:255], v[60:63]\n"
"ds_read_b64_tr_b16 a[246:247], v232 offset:53248\n"
"v_cvt_pk_bf16_f32 v136, v156, v157\n"
"v_mfma_f32_16x16x32_bf16 v[28:31], v[100:103], v[224:227], v[28:31]\n"
"ds_read_b64_tr_b16 v[226:227], v237 offset:53248\n"
"v_cvt_pk_bf16_f32 v145, v162, v163\n"
"v_mfma_f32_16x16x32_bf16 v[28:31], v[96:99], a[252:255], v[28:31]\n"
"ds_read_b64_tr_b16 v[224:225], v237 offset:49152\n"
"v_mfma_f32_16x16x32_bf16 a[236:239], v[120:123], a[252:255], a[236:239]\n"
"s_add_u32 s40, s56, s52\n"
"ds_read_b64_tr_b16 a[252:253], v233 offset:49152\n"
"v_smfmac_f32_16x16x64_bf16 v[198:201], a[240:243], v[104:111], v241\n"
"s_waitcnt lgkmcnt(7)\n"
"buffer_load_dwordx4 v240, s[16:19], s40 offen offset:1024 nt lds\n"
"v_mfma_f32_16x16x32_bf16 v[156:159], v[208:211], a[0:3], v[180:183]\n"
"v_exp_f32 v169, v169\n"
"v_mfma_f32_16x16x32_bf16 v[156:159], v[212:215], a[4:7], v[156:159]\n"
"v_exp_f32 v168, v168\n"
"v_mfma_f32_16x16x32_bf16 v[156:159], v[216:219], a[8:11], v[156:159]\n"
"v_exp_f32 v175, v175\n"
"v_mfma_f32_16x16x32_bf16 v[156:159], v[220:223], a[12:15], v[156:159]\n"
"v_exp_f32 v172, v172\n"
"v_mfma_f32_16x16x32_bf16 v[160:163], v[216:219], a[24:27], v[184:187]\n"
"ds_read_b64_tr_b16 a[254:255], v233 offset:53248\n"
"v_mfma_f32_16x16x32_bf16 v[160:163], v[220:223], a[28:31], v[160:163]\n"
"v_exp_f32 v155, v155\n"
"v_mfma_f32_16x16x32_bf16 v[160:163], v[208:211], a[16:19], v[160:163]\n"
"v_exp_f32 v177, v177\n"
"v_mfma_f32_16x16x32_bf16 v[160:163], v[212:215], a[20:23], v[160:163]\n"
"v_exp_f32 v179, v179\n"
"v_mfma_f32_16x16x32_bf16 v[164:167], v[208:211], a[32:35], v[188:191]\n"
"ds_read_b128 v[208:211], v230 offset:4096\n"
"v_cvt_pk_bf16_f32 v139, v174, v175\n"
"v_mfma_f32_16x16x32_bf16 v[164:167], v[212:215], a[36:39], v[164:167]\n"
"v_exp_f32 v171, v171\n"
"v_mfma_f32_16x16x32_bf16 v[164:167], v[216:219], a[40:43], v[164:167]\n"
"ds_read_b128 v[212:215], v231 offset:4096\n"
"v_cvt_pk_bf16_f32 v138, v172, v173\n"
"v_mfma_f32_16x16x32_bf16 v[164:167], v[220:223], a[44:47], v[164:167]\n"
"s_waitcnt lgkmcnt(3)\n"
"ds_read_b128 v[220:223], v231 offset:5120\n"
"v_cvt_pk_bf16_f32 v130, v168, v169\n"
"v_mfma_f32_16x16x32_bf16 a[208:211], v[120:123], a[244:247], a[208:211]\n"
"v_exp_f32 v154, v154\n"
"v_mfma_f32_16x16x32_bf16 v[0:3], v[96:99], a[244:247], v[0:3]\n"
"s_add_u32 s40, s56, s53\n"
"ds_read_b128 v[216:219], v230 offset:5120\n"
"v_mfma_f32_16x16x32_bf16 v[0:3], v[100:103], a[248:251], v[0:3]\n"
"buffer_load_dwordx4 v240, s[16:19], s40 offen offset:2048 nt lds\n"
"v_mfma_f32_16x16x32_bf16 v[32:35], v[104:107], a[244:247], v[32:35]\n"
"v_exp_f32 v160, v160\n"
"v_mfma_f32_16x16x32_bf16 v[32:35], v[108:111], a[248:251], v[32:35]\n"
"v_exp_f32 v178, v178\n"
"v_mfma_f32_16x16x32_bf16 v[64:67], v[112:115], a[244:247], v[64:67]\n"
"v_exp_f32 v152, v152\n"
"v_mfma_f32_16x16x32_bf16 v[64:67], v[116:119], a[248:251], v[64:67]\n"
"v_exp_f32 v161, v161\n"
"v_mfma_f32_16x16x32_bf16 v[68:71], v[116:119], v[224:227], v[68:71]\n"
"s_waitcnt lgkmcnt(1)\n"
"ds_read_b64_tr_b16 a[244:245], v232 offset:50176\n"
"v_cvt_pk_bf16_f32 v147, v178, v179\n"
"v_mfma_f32_16x16x32_bf16 v[68:71], v[112:115], a[252:255], v[68:71]\n"
"ds_read_b64_tr_b16 a[248:249], v236 offset:50176\n"
"v_cvt_pk_bf16_f32 v146, v176, v177\n"
"v_mfma_f32_16x16x32_bf16 v[36:39], v[108:111], v[224:227], v[36:39]\n"
"ds_read_b64_tr_b16 a[250:251], v236 offset:54272\n"
"v_cvt_pk_bf16_f32 v131, v170, v171\n"
"v_mfma_f32_16x16x32_bf16 v[36:39], v[104:107], a[252:255], v[36:39]\n"
"v_exp_f32 v163, v163\n"
"v_mfma_f32_16x16x32_bf16 v[4:7], v[100:103], v[224:227], v[4:7]\n"
"s_add_u32 s40, s56, s54\n"
"ds_read_b64_tr_b16 a[246:247], v232 offset:54272\n"
"v_mfma_f32_16x16x32_bf16 v[4:7], v[96:99], a[252:255], v[4:7]\n"
"buffer_load_dwordx4 v240, s[16:19], s40 offen offset:3072 nt lds\n"
"v_mfma_f32_16x16x32_bf16 a[212:215], v[120:123], a[252:255], a[212:215]\n"
"v_exp_f32 v167, v167\n"
"v_smfmac_f32_16x16x64_bf16 v[200:203], a[240:243], v[112:119], v241\n"
"v_cvt_pk_bf16_f32 v126, v152, v153\n"
"v_mfma_f32_16x16x32_bf16 v[168:171], v[208:211], a[0:3], v[180:183]\n"
"v_exp_f32 v159, v159\n"
"v_mfma_f32_16x16x32_bf16 v[168:171], v[212:215], a[4:7], v[168:171]\n"
"s_waitcnt lgkmcnt(2)\n"
"v_exp_f32 v165, v165\n"
"v_mfma_f32_16x16x32_bf16 v[168:171], v[216:219], a[8:11], v[168:171]\n"
"ds_read_b64_tr_b16 a[252:253], v233 offset:50176\n"
"ds_read_b64_tr_b16 v[224:225], v237 offset:50176\n"
"v_mfma_f32_16x16x32_bf16 v[168:171], v[220:223], a[12:15], v[168:171]\n"
"ds_read_b64_tr_b16 a[254:255], v233 offset:54272\n"
"ds_read_b64_tr_b16 v[226:227], v237 offset:54272\n"
"v_mfma_f32_16x16x32_bf16 v[172:175], v[216:219], a[24:27], v[184:187]\n"
"v_exp_f32 v166, v166\n"
"v_mfma_f32_16x16x32_bf16 v[172:175], v[220:223], a[28:31], v[172:175]\n"
"v_exp_f32 v164, v164\n"
"v_mfma_f32_16x16x32_bf16 v[172:175], v[208:211], a[16:19], v[172:175]\n"
"v_exp_f32 v157, v157\n"
"v_mfma_f32_16x16x32_bf16 v[172:175], v[212:215], a[20:23], v[172:175]\n"
"s_cbranch_vccnz LPCL0_%=\nLPKL0_%=:\n"
"v_cvt_pk_bf16_f32 v127, v154, v155\n"
"v_mfma_f32_16x16x32_bf16 v[176:179], v[208:211], a[32:35], v[188:191]\n"
"v_exp_f32 v158, v158\n"
"v_mfma_f32_16x16x32_bf16 v[176:179], v[212:215], a[36:39], v[176:179]\n"
"v_exp_f32 v162, v162\n"
"v_mfma_f32_16x16x32_bf16 v[176:179], v[216:219], a[40:43], v[176:179]\n"
"v_exp_f32 v156, v156\n"
"v_mfma_f32_16x16x32_bf16 v[176:179], v[220:223], a[44:47], v[176:179]\n"
"s_waitcnt lgkmcnt(0)\n"
"v_cvt_pk_bf16_f32 v133, v158, v159\n"
"v_mfma_f32_16x16x32_bf16 a[224:227], v[120:123], a[244:247], a[224:227]\n"
"v_exp_f32 v169, v169\n"
"v_mfma_f32_16x16x32_bf16 v[16:19], v[96:99], a[244:247], v[16:19]\n"
"s_waitcnt vmcnt(8)\n"
"v_exp_f32 v172, v172\n"
"v_mfma_f32_16x16x32_bf16 v[16:19], v[100:103], a[248:251], v[16:19]\n"
"s_barrier\n"
"ds_read_b128 v[216:219], v228 offset:17408\n"
"v_mfma_f32_16x16x32_bf16 v[48:51], v[104:107], a[244:247], v[48:51]\n"
"ds_read_b128 v[212:215], v229 offset:16384\n"
"v_cvt_pk_bf16_f32 v140, v160, v161\n"
"v_mfma_f32_16x16x32_bf16 v[48:51], v[108:111], a[248:251], v[48:51]\n"
"ds_read_b128 v[208:211], v228 offset:16384\n"
"v_cvt_pk_bf16_f32 v132, v156, v157\n"
"v_mfma_f32_16x16x32_bf16 v[80:83], v[112:115], a[244:247], v[80:83]\n"
"ds_read_b128 v[220:223], v229 offset:17408\n"
"v_cvt_pk_bf16_f32 v149, v166, v167\n"
"v_mfma_f32_16x16x32_bf16 v[80:83], v[116:119], a[248:251], v[80:83]\n"
"ds_read_b64_tr_b16 a[244:245], v234 offset:0\n"
"v_cvt_pk_bf16_f32 v141, v162, v163\n"
"v_mfma_f32_16x16x32_bf16 v[84:87], v[116:119], v[224:227], v[84:87]\n"
"ds_read_b64_tr_b16 a[248:249], v238 offset:0\n"
"v_mfma_f32_16x16x32_bf16 v[84:87], v[112:115], a[252:255], v[84:87]\n"
"ds_read_b64_tr_b16 a[246:247], v234 offset:4096\n"
"v_mfma_f32_16x16x32_bf16 v[52:55], v[108:111], v[224:227], v[52:55]\n"
"ds_read_b64_tr_b16 a[250:251], v238 offset:4096\n"
"v_mfma_f32_16x16x32_bf16 v[52:55], v[104:107], a[252:255], v[52:55]\n"
"v_exp_f32 v175, v175\n"
"v_mfma_f32_16x16x32_bf16 v[20:23], v[100:103], v[224:227], v[20:23]\n"
"ds_read_b64_tr_b16 v[224:225], v239 offset:0\n"
"v_mfma_f32_16x16x32_bf16 v[20:23], v[96:99], a[252:255], v[20:23]\n"
"v_exp_f32 v173, v173\n"
"v_mfma_f32_16x16x32_bf16 a[228:231], v[120:123], a[252:255], a[228:231]\n"
"s_waitcnt lgkmcnt(5)\n"
"ds_read_b64_tr_b16 a[254:255], v235 offset:4096\n"
"v_cvt_pk_bf16_f32 v148, v164, v165\n"
"v_mfma_f32_16x16x32_bf16 v[152:155], v[208:211], a[0:3], v[180:183]\n"
"v_exp_f32 v174, v174\n"
"v_mfma_f32_16x16x32_bf16 v[152:155], v[212:215], a[4:7], v[152:155]\n"
"ds_read_b64_tr_b16 a[252:253], v235 offset:0\n"
"v_cvt_pk_bf16_f32 v143, v174, v175\n"
"v_mfma_f32_16x16x32_bf16 v[152:155], v[216:219], a[8:11], v[152:155]\n"
"s_add_u32 m0, s44, 98304\n"
"ds_read_b64_tr_b16 v[226:227], v239 offset:4096\n"
"v_mfma_f32_16x16x32_bf16 v[152:155], v[220:223], a[12:15], v[152:155]\n"
"buffer_load_dwordx4 v240, s[20:23], s56 offen nt lds\n"
"v_mfma_f32_16x16x32_bf16 v[156:159], v[216:219], a[24:27], v[184:187]\n"
"v_exp_f32 v171, v171\n"
"v_mfma_f32_16x16x32_bf16 v[156:159], v[220:223], a[28:31], v[156:159]\n"
"v_exp_f32 v168, v168\n"
"v_mfma_f32_16x16x32_bf16 v[156:159], v[208:211], a[16:19], v[156:159]\n"
"v_exp_f32 v176, v176\n"
"v_mfma_f32_16x16x32_bf16 v[156:159], v[212:215], a[20:23], v[156:159]\n"
"v_exp_f32 v177, v177\n"
"v_mfma_f32_16x16x32_bf16 v[160:163], v[208:211], a[32:35], v[188:191]\n"
"v_exp_f32 v179, v179\n"
"v_mfma_f32_16x16x32_bf16 v[160:163], v[212:215], a[36:39], v[160:163]\n"
"v_exp_f32 v170, v170\n"
"v_mfma_f32_16x16x32_bf16 v[160:163], v[216:219], a[40:43], v[160:163]\n"
"v_exp_f32 v153, v153\n"
"v_mfma_f32_16x16x32_bf16 v[160:163], v[220:223], a[44:47], v[160:163]\n"
"v_cvt_pk_bf16_f32 v134, v168, v169\n"
"v_mfma_f32_16x16x32_bf16 v[164:167], v[216:219], a[200:203], v[192:195]\n"
"ds_read_b128 v[216:219], v228 offset:21504\n"
"v_cvt_pk_bf16_f32 v135, v170, v171\n"
"v_mfma_f32_16x16x32_bf16 v[164:167], v[220:223], a[204:207], v[164:167]\n"
"v_cvt_pk_bf16_f32 v142, v172, v173\n"
"v_cvt_pk_bf16_f32 v150, v176, v177\n"
"v_mfma_f32_16x16x32_bf16 v[164:167], v[208:211], a[192:195], v[164:167]\n"
"s_add_u32 s40, s56, s52\n"
"ds_read_b128 v[208:211], v228 offset:20480\n"
"v_mfma_f32_16x16x32_bf16 v[164:167], v[212:215], a[196:199], v[164:167]\n"
"s_waitcnt lgkmcnt(2)\n"
"buffer_load_dwordx4 v240, s[20:23], s40 offen offset:1024 nt lds\n"
"v_mfma_f32_16x16x32_bf16 a[216:219], v[124:127], a[244:247], a[216:219]\n"
"v_exp_f32 v155, v155\n"
"v_mfma_f32_16x16x32_bf16 v[8:11], v[128:131], a[244:247], v[8:11]\n"
"v_exp_f32 v161, v161\n"
"v_mfma_f32_16x16x32_bf16 v[8:11], v[132:135], a[248:251], v[8:11]\n"
"v_exp_f32 v163, v163\n"
"v_mfma_f32_16x16x32_bf16 v[40:43], v[136:139], a[244:247], v[40:43]\n"
"v_exp_f32 v178, v178\n"
"v_mfma_f32_16x16x32_bf16 v[40:43], v[140:143], a[248:251], v[40:43]\n"
"ds_read_b128 v[220:223], v229 offset:21504\n"
"v_cvt_pk_bf16_f32 v151, v178, v179\n"
"v_mfma_f32_16x16x32_bf16 v[72:75], v[144:147], a[244:247], v[72:75]\n"
"ds_read_b64_tr_b16 a[244:245], v234 offset:1024\n"
"v_mfma_f32_16x16x32_bf16 v[72:75], v[148:151], a[248:251], v[72:75]\n"
"v_exp_f32 v158, v158\n"
"v_mfma_f32_16x16x32_bf16 v[76:79], v[148:151], v[224:227], v[76:79]\n"
"ds_read_b128 v[212:215], v229 offset:20480\n"
"v_mfma_f32_16x16x32_bf16 v[76:79], v[144:147], a[252:255], v[76:79]\n"
"v_exp_f32 v160, v160\n"
"v_mfma_f32_16x16x32_bf16 v[44:47], v[140:143], v[224:227], v[44:47]\n"
"s_add_u32 s40, s56, s53\n"
"ds_read_b64_tr_b16 a[248:249], v238 offset:1024\n"
"v_mfma_f32_16x16x32_bf16 v[44:47], v[136:139], a[252:255], v[44:47]\n"
"buffer_load_dwordx4 v240, s[20:23], s40 offen offset:2048 nt lds\n"
"v_mfma_f32_16x16x32_bf16 v[12:15], v[132:135], v[224:227], v[12:15]\n"
"v_exp_f32 v165, v165\n"
"v_mfma_f32_16x16x32_bf16 v[12:15], v[128:131], a[252:255], v[12:15]\n"
"v_exp_f32 v159, v159\n"
"v_mfma_f32_16x16x32_bf16 a[220:223], v[124:127], a[252:255], a[220:223]\n"
"v_exp_f32 v152, v152\n"
"v_smfmac_f32_16x16x64_bf16 v[196:199], a[240:243], v[128:135], v241\n"
"v_mfma_f32_16x16x32_bf16 v[202:205], a[240:243], v[124:127], v[202:205]\n"
"s_waitcnt lgkmcnt(1)\n"
"ds_read_b64_tr_b16 v[224:225], v239 offset:1024\n"
"v_cvt_pk_bf16_f32 v96, v152, v153\n"
"v_mfma_f32_16x16x32_bf16 v[168:171], v[216:219], a[8:11], v[180:183]\n"
"v_exp_f32 v162, v162\n"
"v_mfma_f32_16x16x32_bf16 v[168:171], v[220:223], a[12:15], v[168:171]\n"
"ds_read_b64_tr_b16 a[250:251], v238 offset:5120\n"
"v_mfma_f32_16x16x32_bf16 v[168:171], v[208:211], a[0:3], v[168:171]\n"
"ds_read_b64_tr_b16 a[252:253], v235 offset:1024\n"
"v_mfma_f32_16x16x32_bf16 v[168:171], v[212:215], a[4:7], v[168:171]\n"
"v_exp_f32 v154, v154\n"
"v_mfma_f32_16x16x32_bf16 v[172:175], v[208:211], a[16:19], v[184:187]\n"
"ds_read_b64_tr_b16 v[226:227], v239 offset:5120\n"
"v_mfma_f32_16x16x32_bf16 v[172:175], v[212:215], a[20:23], v[172:175]\n"
"ds_read_b64_tr_b16 a[246:247], v234 offset:5120\n"
"v_cvt_pk_bf16_f32 v97, v154, v155\n"
"v_mfma_f32_16x16x32_bf16 v[172:175], v[216:219], a[24:27], v[172:175]\n"
"s_add_u32 s40, s56, s54\n"
"ds_read_b64_tr_b16 a[254:255], v235 offset:5120\n"
"v_mfma_f32_16x16x32_bf16 v[172:175], v[220:223], a[28:31], v[172:175]\n"
"buffer_load_dwordx4 v240, s[20:23], s40 offen offset:3072 nt lds\n"
"v_mfma_f32_16x16x32_bf16 v[176:179], v[216:219], a[40:43], v[188:191]\n"
"s_add_u32 s55, s55, s59\n"
"v_exp_f32 v157, v157\n"
"v_mfma_f32_16x16x32_bf16 v[176:179], v[220:223], a[44:47], v[176:179]\n"
"s_add_u32 s41, s55, s57\n"
"v_exp_f32 v170, v170\n"
"v_mfma_f32_16x16x32_bf16 v[176:179], v[208:211], a[32:35], v[176:179]\n"
"s_and_b32 s41, s41, s58\n"
"v_exp_f32 v156, v156\n"
"v_mfma_f32_16x16x32_bf16 v[176:179], v[212:215], a[36:39], v[176:179]\n"
"s_andn2_b32 s56, s55, s58\n"
"v_exp_f32 v166, v166\n"
"v_mfma_f32_16x16x32_bf16 v[152:155], v[208:211], a[192:195], v[192:195]\n"
"s_or_b32 s56, s56, s41\n"
"ds_read_b128 v[208:211], v230 offset:16384\n"
"v_mfma_f32_16x16x32_bf16 v[152:155], v[212:215], a[196:199], v[152:155]\n"
"v_exp_f32 v174, v174\n"
"v_mfma_f32_16x16x32_bf16 v[152:155], v[216:219], a[200:203], v[152:155]\n"
"ds_read_b128 v[212:215], v231 offset:16384\n"
"v_mfma_f32_16x16x32_bf16 v[152:155], v[220:223], a[204:207], v[152:155]\n"
"s_waitcnt lgkmcnt(2)\n"
"ds_read_b128 v[216:219], v230 offset:17408\n"
"v_cvt_pk_bf16_f32 v105, v158, v159\n"
"v_mfma_f32_16x16x32_bf16 a[232:235], v[124:127], a[244:247], a[232:235]\n"
"v_exp_f32 v164, v164\n"
"v_mfma_f32_16x16x32_bf16 v[24:27], v[128:131], a[244:247], v[24:27]\n"
"s_add_u32 m0, s44, 49152\n"
"ds_read_b128 v[220:223], v231 offset:17408\n"
"v_mfma_f32_16x16x32_bf16 v[24:27], v[132:135], a[248:251], v[24:27]\n"
"buffer_load_dwordx4 v240, s[16:19], s56 offen nt lds\n"
"v_mfma_f32_16x16x32_bf16 v[56:59], v[136:139], a[244:247], v[56:59]\n"
"v_exp_f32 v167, v167\n"
"v_mfma_f32_16x16x32_bf16 v[56:59], v[140:143], a[248:251], v[56:59]\n"
"v_exp_f32 v176, v176\n"
"v_mfma_f32_16x16x32_bf16 v[88:91], v[144:147], a[244:247], v[88:91]\n"
"v_exp_f32 v153, v153\n"
"v_mfma_f32_16x16x32_bf16 v[88:91], v[148:151], a[248:251], v[88:91]\n"
"v_exp_f32 v173, v173\n"
"v_mfma_f32_16x16x32_bf16 v[92:95], v[148:151], v[224:227], v[92:95]\n"
"ds_read_b64_tr_b16 a[250:251], v236 offset:4096\n"
"v_cvt_pk_bf16_f32 v121, v166, v167\n"
"v_mfma_f32_16x16x32_bf16 v[92:95], v[144:147], a[252:255], v[92:95]\n"
"ds_read_b64_tr_b16 a[244:245], v232 offset:0\n"
"v_cvt_pk_bf16_f32 v112, v160, v161\n"
"v_mfma_f32_16x16x32_bf16 v[60:63], v[140:143], v[224:227], v[60:63]\n"
"ds_read_b64_tr_b16 a[248:249], v236 offset:0\n"
"v_cvt_pk_bf16_f32 v120, v164, v165\n"
"v_mfma_f32_16x16x32_bf16 v[60:63], v[136:139], a[252:255], v[60:63]\n"
"ds_read_b64_tr_b16 a[246:247], v232 offset:4096\n"
"v_cvt_pk_bf16_f32 v104, v156, v157\n"
"v_mfma_f32_16x16x32_bf16 v[28:31], v[132:135], v[224:227], v[28:31]\n"
"ds_read_b64_tr_b16 v[226:227], v237 offset:4096\n"
"v_cvt_pk_bf16_f32 v113, v162, v163\n"
"v_mfma_f32_16x16x32_bf16 v[28:31], v[128:131], a[252:255], v[28:31]\n"
"ds_read_b64_tr_b16 v[224:225], v237 offset:0\n"
"v_mfma_f32_16x16x32_bf16 a[236:239], v[124:127], a[252:255], a[236:239]\n"
"s_add_u32 s40, s56, s52\n"
"ds_read_b64_tr_b16 a[252:253], v233 offset:0\n"
"v_smfmac_f32_16x16x64_bf16 v[198:201], a[240:243], v[136:143], v241\n"
"s_waitcnt lgkmcnt(7)\n"
"buffer_load_dwordx4 v240, s[16:19], s40 offen offset:1024 nt lds\n"
"v_mfma_f32_16x16x32_bf16 v[156:159], v[208:211], a[0:3], v[180:183]\n"
"v_exp_f32 v169, v169\n"
"v_mfma_f32_16x16x32_bf16 v[156:159], v[212:215], a[4:7], v[156:159]\n"
"v_exp_f32 v168, v168\n"
"v_mfma_f32_16x16x32_bf16 v[156:159], v[216:219], a[8:11], v[156:159]\n"
"v_exp_f32 v175, v175\n"
"v_mfma_f32_16x16x32_bf16 v[156:159], v[220:223], a[12:15], v[156:159]\n"
"v_exp_f32 v172, v172\n"
"v_mfma_f32_16x16x32_bf16 v[160:163], v[216:219], a[24:27], v[184:187]\n"
"ds_read_b64_tr_b16 a[254:255], v233 offset:4096\n"
"v_mfma_f32_16x16x32_bf16 v[160:163], v[220:223], a[28:31], v[160:163]\n"
"v_exp_f32 v155, v155\n"
"v_mfma_f32_16x16x32_bf16 v[160:163], v[208:211], a[16:19], v[160:163]\n"
"v_exp_f32 v177, v177\n"
"v_mfma_f32_16x16x32_bf16 v[160:163], v[212:215], a[20:23], v[160:163]\n"
"v_exp_f32 v179, v179\n"
"v_mfma_f32_16x16x32_bf16 v[164:167], v[208:211], a[32:35], v[188:191]\n"
"ds_read_b128 v[208:211], v230 offset:20480\n"
"v_cvt_pk_bf16_f32 v107, v174, v175\n"
"v_mfma_f32_16x16x32_bf16 v[164:167], v[212:215], a[36:39], v[164:167]\n"
"v_exp_f32 v171, v171\n"
"v_mfma_f32_16x16x32_bf16 v[164:167], v[216:219], a[40:43], v[164:167]\n"
"ds_read_b128 v[212:215], v231 offset:20480\n"
"v_cvt_pk_bf16_f32 v106, v172, v173\n"
"v_mfma_f32_16x16x32_bf16 v[164:167], v[220:223], a[44:47], v[164:167]\n"
"s_waitcnt lgkmcnt(3)\n"
"ds_read_b128 v[220:223], v231 offset:21504\n"
"v_cvt_pk_bf16_f32 v98, v168, v169\n"
"v_mfma_f32_16x16x32_bf16 a[208:211], v[124:127], a[244:247], a[208:211]\n"
"v_exp_f32 v154, v154\n"
"v_mfma_f32_16x16x32_bf16 v[0:3], v[128:131], a[244:247], v[0:3]\n"
"s_add_u32 s40, s56, s53\n"
"ds_read_b128 v[216:219], v230 offset:21504\n"
"v_mfma_f32_16x16x32_bf16 v[0:3], v[132:135], a[248:251], v[0:3]\n"
"buffer_load_dwordx4 v240, s[16:19], s40 offen offset:2048 nt lds\n"
"v_mfma_f32_16x16x32_bf16 v[32:35], v[136:139], a[244:247], v[32:35]\n"
"v_exp_f32 v160, v160\n"
"v_mfma_f32_16x16x32_bf16 v[32:35], v[140:143], a[248:251], v[32:35]\n"
"v_exp_f32 v178, v178\n"
"v_mfma_f32_16x16x32_bf16 v[64:67], v[144:147], a[244:247], v[64:67]\n"
"v_exp_f32 v152, v152\n"
"v_mfma_f32_16x16x32_bf16 v[64:67], v[148:151], a[248:251], v[64:67]\n"
"v_exp_f32 v161, v161\n"
"v_mfma_f32_16x16x32_bf16 v[68:71], v[148:151], v[224:227], v[68:71]\n"
"s_waitcnt lgkmcnt(1)\n"
"ds_read_b64_tr_b16 a[244:245], v232 offset:1024\n"
"v_cvt_pk_bf16_f32 v115, v178, v179\n"
"v_mfma_f32_16x16x32_bf16 v[68:71], v[144:147], a[252:255], v[68:71]\n"
"ds_read_b64_tr_b16 a[248:249], v236 offset:1024\n"
"v_cvt_pk_bf16_f32 v114, v176, v177\n"
"v_mfma_f32_16x16x32_bf16 v[36:39], v[140:143], v[224:227], v[36:39]\n"
"ds_read_b64_tr_b16 a[250:251], v236 offset:5120\n"
"v_cvt_pk_bf16_f32 v99, v170, v171\n"
"v_mfma_f32_16x16x32_bf16 v[36:39], v[136:139], a[252:255], v[36:39]\n"
"v_exp_f32 v163, v163\n"
"v_mfma_f32_16x16x32_bf16 v[4:7], v[132:135], v[224:227], v[4:7]\n"
"s_add_u32 s40, s56, s54\n"
"ds_read_b64_tr_b16 a[246:247], v232 offset:5120\n"
"v_mfma_f32_16x16x32_bf16 v[4:7], v[128:131], a[252:255], v[4:7]\n"
"buffer_load_dwordx4 v240, s[16:19], s40 offen offset:3072 nt lds\n"
"v_mfma_f32_16x16x32_bf16 a[212:215], v[124:127], a[252:255], a[212:215]\n"
"v_exp_f32 v167, v167\n"
"v_smfmac_f32_16x16x64_bf16 v[200:203], a[240:243], v[144:151], v241\n"
"v_cvt_pk_bf16_f32 v122, v152, v153\n"
"v_mfma_f32_16x16x32_bf16 v[168:171], v[208:211], a[0:3], v[180:183]\n"
"v_exp_f32 v159, v159\n"
"v_mfma_f32_16x16x32_bf16 v[168:171], v[212:215], a[4:7], v[168:171]\n"
"s_waitcnt lgkmcnt(2)\n"
"v_exp_f32 v165, v165\n"
"v_mfma_f32_16x16x32_bf16 v[168:171], v[216:219], a[8:11], v[168:171]\n"
"ds_read_b64_tr_b16 a[252:253], v233 offset:1024\n"
"ds_read_b64_tr_b16 v[224:225], v237 offset:1024\n"
"v_mfma_f32_16x16x32_bf16 v[168:171], v[220:223], a[12:15], v[168:171]\n"
"ds_read_b64_tr_b16 a[254:255], v233 offset:5120\n"
"ds_read_b64_tr_b16 v[226:227], v237 offset:5120\n"
"v_mfma_f32_16x16x32_bf16 v[172:175], v[216:219], a[24:27], v[184:187]\n"
"v_exp_f32 v166, v166\n"
"v_mfma_f32_16x16x32_bf16 v[172:175], v[220:223], a[28:31], v[172:175]\n"
"v_exp_f32 v164, v164\n"
"v_mfma_f32_16x16x32_bf16 v[172:175], v[208:211], a[16:19], v[172:175]\n"
"v_exp_f32 v157, v157\n"
"v_mfma_f32_16x16x32_bf16 v[172:175], v[212:215], a[20:23], v[172:175]\n"
"s_cbranch_vccnz LPCL1_%=\nLPKL1_%=:\n"
"v_cvt_pk_bf16_f32 v123, v154, v155\n"
"v_mfma_f32_16x16x32_bf16 v[176:179], v[208:211], a[32:35], v[188:191]\n"
"v_exp_f32 v158, v158\n"
"v_mfma_f32_16x16x32_bf16 v[176:179], v[212:215], a[36:39], v[176:179]\n"
"v_exp_f32 v162, v162\n"
"v_mfma_f32_16x16x32_bf16 v[176:179], v[216:219], a[40:43], v[176:179]\n"
"v_exp_f32 v156, v156\n"
"v_mfma_f32_16x16x32_bf16 v[176:179], v[220:223], a[44:47], v[176:179]\n"
"s_waitcnt lgkmcnt(0)\n"
"v_cvt_pk_bf16_f32 v101, v158, v159\n"
"v_mfma_f32_16x16x32_bf16 a[224:227], v[124:127], a[244:247], a[224:227]\n"
"v_exp_f32 v169, v169\n"
"v_mfma_f32_16x16x32_bf16 v[16:19], v[128:131], a[244:247], v[16:19]\n"
"s_waitcnt vmcnt(8)\n"
"v_exp_f32 v172, v172\n"
"v_mfma_f32_16x16x32_bf16 v[16:19], v[132:135], a[248:251], v[16:19]\n"
"s_barrier\n"
"ds_read_b128 v[216:219], v228 offset:33792\n"
"v_mfma_f32_16x16x32_bf16 v[48:51], v[136:139], a[244:247], v[48:51]\n"
"ds_read_b128 v[212:215], v229 offset:32768\n"
"v_cvt_pk_bf16_f32 v108, v160, v161\n"
"v_mfma_f32_16x16x32_bf16 v[48:51], v[140:143], a[248:251], v[48:51]\n"
"ds_read_b128 v[208:211], v228 offset:32768\n"
"v_cvt_pk_bf16_f32 v100, v156, v157\n"
"v_mfma_f32_16x16x32_bf16 v[80:83], v[144:147], a[244:247], v[80:83]\n"
"ds_read_b128 v[220:223], v229 offset:33792\n"
"v_cvt_pk_bf16_f32 v117, v166, v167\n"
"v_mfma_f32_16x16x32_bf16 v[80:83], v[148:151], a[248:251], v[80:83]\n"
"ds_read_b64_tr_b16 a[244:245], v234 offset:16384\n"
"v_cvt_pk_bf16_f32 v109, v162, v163\n"
"v_mfma_f32_16x16x32_bf16 v[84:87], v[148:151], v[224:227], v[84:87]\n"
"ds_read_b64_tr_b16 a[248:249], v238 offset:16384\n"
"v_mfma_f32_16x16x32_bf16 v[84:87], v[144:147], a[252:255], v[84:87]\n"
"ds_read_b64_tr_b16 a[246:247], v234 offset:20480\n"
"v_mfma_f32_16x16x32_bf16 v[52:55], v[140:143], v[224:227], v[52:55]\n"
"ds_read_b64_tr_b16 a[250:251], v238 offset:20480\n"
"v_mfma_f32_16x16x32_bf16 v[52:55], v[136:139], a[252:255], v[52:55]\n"
"v_exp_f32 v175, v175\n"
"v_mfma_f32_16x16x32_bf16 v[20:23], v[132:135], v[224:227], v[20:23]\n"
"ds_read_b64_tr_b16 v[224:225], v239 offset:16384\n"
"v_mfma_f32_16x16x32_bf16 v[20:23], v[128:131], a[252:255], v[20:23]\n"
"v_exp_f32 v173, v173\n"
"v_mfma_f32_16x16x32_bf16 a[228:231], v[124:127], a[252:255], a[228:231]\n"
"s_waitcnt lgkmcnt(5)\n"
"ds_read_b64_tr_b16 a[254:255], v235 offset:20480\n"
"v_cvt_pk_bf16_f32 v116, v164, v165\n"
"LAY2_%=:\n"
"v_mfma_f32_16x16x32_bf16 v[152:155], v[208:211], a[0:3], v[180:183]\n"
"s_waitcnt lgkmcnt(8)\n"
"v_exp_f32 v174, v174\n"
"v_mfma_f32_16x16x32_bf16 v[152:155], v[212:215], a[4:7], v[152:155]\n"
"s_waitcnt lgkmcnt(7)\n"
"ds_read_b64_tr_b16 a[252:253], v235 offset:16384\n"
"v_cvt_pk_bf16_f32 v111, v174, v175\n"
"v_mfma_f32_16x16x32_bf16 v[152:155], v[216:219], a[8:11], v[152:155]\n"
"s_waitcnt lgkmcnt(7)\n"
"s_add_u32 m0, s44, 114688\n"
"ds_read_b64_tr_b16 v[226:227], v239 offset:20480\n"
"v_mfma_f32_16x16x32_bf16 v[152:155], v[220:223], a[12:15], v[152:155]\n"
"buffer_load_dwordx4 v240, s[20:23], s56 offen nt lds\n"
"v_mfma_f32_16x16x32_bf16 v[156:159], v[216:219], a[24:27], v[184:187]\n"
"v_exp_f32 v171, v171\n"
"v_mfma_f32_16x16x32_bf16 v[156:159], v[220:223], a[28:31], v[156:159]\n"
"v_exp_f32 v168, v168\n"
"v_mfma_f32_16x16x32_bf16 v[156:159], v[208:211], a[16:19], v[156:159]\n"
"v_exp_f32 v176, v176\n"
"v_mfma_f32_16x16x32_bf16 v[156:159], v[212:215], a[20:23], v[156:159]\n"
"v_exp_f32 v177, v177\n"
"v_mfma_f32_16x16x32_bf16 v[160:163], v[208:211], a[32:35], v[188:191]\n"
"v_exp_f32 v179, v179\n"
"v_mfma_f32_16x16x32_bf16 v[160:163], v[212:215], a[36:39], v[160:163]\n"
"v_exp_f32 v170, v170\n"
"v_mfma_f32_16x16x32_bf16 v[160:163], v[216:219], a[40:43], v[160:163]\n"
"v_exp_f32 v153, v153\n"
"v_mfma_f32_16x16x32_bf16 v[160:163], v[220:223], a[44:47], v[160:163]\n"
"v_cvt_pk_bf16_f32 v102, v168, v169\n"
"v_mfma_f32_16x16x32_bf16 v[164:167], v[216:219], a[200:203], v[192:195]\n"
"ds_read_b128 v[216:219], v228 offset:37888\n"
"v_cvt_pk_bf16_f32 v103, v170, v171\n"
"v_mfma_f32_16x16x32_bf16 v[164:167], v[220:223], a[204:207], v[164:167]\n"
"v_cvt_pk_bf16_f32 v110, v172, v173\n"
"v_cvt_pk_bf16_f32 v118, v176, v177\n"
"v_mfma_f32_16x16x32_bf16 v[164:167], v[208:211], a[192:195], v[164:167]\n"
"s_add_u32 s40, s56, s52\n"
"ds_read_b128 v[208:211], v228 offset:36864\n"
"v_mfma_f32_16x16x32_bf16 v[164:167], v[212:215], a[196:199], v[164:167]\n"
"s_waitcnt lgkmcnt(2)\n"
"buffer_load_dwordx4 v240, s[20:23], s40 offen offset:1024 nt lds\n"
"v_mfma_f32_16x16x32_bf16 a[216:219], v[120:123], a[244:247], a[216:219]\n"
"v_exp_f32 v155, v155\n"
"v_mfma_f32_16x16x32_bf16 v[8:11], v[96:99], a[244:247], v[8:11]\n"
"s_waitcnt lgkmcnt(2)\n"
"v_exp_f32 v161, v161\n"
"v_mfma_f32_16x16x32_bf16 v[8:11], v[100:103], a[248:251], v[8:11]\n"
"v_exp_f32 v163, v163\n"
"v_mfma_f32_16x16x32_bf16 v[40:43], v[104:107], a[244:247], v[40:43]\n"
"v_exp_f32 v178, v178\n"
"v_mfma_f32_16x16x32_bf16 v[40:43], v[108:111], a[248:251], v[40:43]\n"
"ds_read_b128 v[220:223], v229 offset:37888\n"
"v_cvt_pk_bf16_f32 v119, v178, v179\n"
"v_mfma_f32_16x16x32_bf16 v[72:75], v[112:115], a[244:247], v[72:75]\n"
"ds_read_b64_tr_b16 a[244:245], v234 offset:17408\n"
"v_mfma_f32_16x16x32_bf16 v[72:75], v[116:119], a[248:251], v[72:75]\n"
"v_exp_f32 v158, v158\n"
"v_mfma_f32_16x16x32_bf16 v[76:79], v[116:119], v[224:227], v[76:79]\n"
"ds_read_b128 v[212:215], v229 offset:36864\n"
"v_mfma_f32_16x16x32_bf16 v[76:79], v[112:115], a[252:255], v[76:79]\n"
"v_exp_f32 v160, v160\n"
"v_mfma_f32_16x16x32_bf16 v[44:47], v[108:111], v[224:227], v[44:47]\n"
"s_add_u32 s40, s56, s53\n"
"ds_read_b64_tr_b16 a[248:249], v238 offset:17408\n"
"v_mfma_f32_16x16x32_bf16 v[44:47], v[104:107], a[252:255], v[44:47]\n"
"buffer_load_dwordx4 v240, s[20:23], s40 offen offset:2048 nt lds\n"
"v_mfma_f32_16x16x32_bf16 v[12:15], v[100:103], v[224:227], v[12:15]\n"
"v_exp_f32 v165, v165\n"
"v_mfma_f32_16x16x32_bf16 v[12:15], v[96:99], a[252:255], v[12:15]\n"
"v_exp_f32 v159, v159\n"
"v_mfma_f32_16x16x32_bf16 a[220:223], v[120:123], a[252:255], a[220:223]\n"
"v_exp_f32 v152, v152\n"
"v_smfmac_f32_16x16x64_bf16 v[196:199], a[240:243], v[96:103], v241\n"
"v_mfma_f32_16x16x32_bf16 v[202:205], a[240:243], v[120:123], v[202:205]\n"
"s_waitcnt lgkmcnt(1)\n"
"ds_read_b64_tr_b16 v[224:225], v239 offset:17408\n"
"v_cvt_pk_bf16_f32 v128, v152, v153\n"
"v_mfma_f32_16x16x32_bf16 v[168:171], v[216:219], a[8:11], v[180:183]\n"
"v_exp_f32 v162, v162\n"
"v_mfma_f32_16x16x32_bf16 v[168:171], v[220:223], a[12:15], v[168:171]\n"
"ds_read_b64_tr_b16 a[250:251], v238 offset:21504\n"
"v_mfma_f32_16x16x32_bf16 v[168:171], v[208:211], a[0:3], v[168:171]\n"
"ds_read_b64_tr_b16 a[252:253], v235 offset:17408\n"
"v_mfma_f32_16x16x32_bf16 v[168:171], v[212:215], a[4:7], v[168:171]\n"
"v_exp_f32 v154, v154\n"
"v_mfma_f32_16x16x32_bf16 v[172:175], v[208:211], a[16:19], v[184:187]\n"
"ds_read_b64_tr_b16 v[226:227], v239 offset:21504\n"
"v_mfma_f32_16x16x32_bf16 v[172:175], v[212:215], a[20:23], v[172:175]\n"
"ds_read_b64_tr_b16 a[246:247], v234 offset:21504\n"
"v_cvt_pk_bf16_f32 v129, v154, v155\n"
"v_mfma_f32_16x16x32_bf16 v[172:175], v[216:219], a[24:27], v[172:175]\n"
"s_add_u32 s40, s56, s54\n"
"ds_read_b64_tr_b16 a[254:255], v235 offset:21504\n"
"v_mfma_f32_16x16x32_bf16 v[172:175], v[220:223], a[28:31], v[172:175]\n"
"buffer_load_dwordx4 v240, s[20:23], s40 offen offset:3072 nt lds\n"
"v_mfma_f32_16x16x32_bf16 v[176:179], v[216:219], a[40:43], v[188:191]\n"
"s_add_u32 s55, s55, s59\n"
"v_exp_f32 v157, v157\n"
"v_mfma_f32_16x16x32_bf16 v[176:179], v[220:223], a[44:47], v[176:179]\n"
"s_add_u32 s41, s55, s57\n"
"v_exp_f32 v170, v170\n"
"v_mfma_f32_16x16x32_bf16 v[176:179], v[208:211], a[32:35], v[176:179]\n"
"s_and_b32 s41, s41, s58\n"
"v_exp_f32 v156, v156\n"
"v_mfma_f32_16x16x32_bf16 v[176:179], v[212:215], a[36:39], v[176:179]\n"
"s_andn2_b32 s56, s55, s58\n"
"v_exp_f32 v166, v166\n"
"v_mfma_f32_16x16x32_bf16 v[152:155], v[208:211], a[192:195], v[192:195]\n"
"s_or_b32 s56, s56, s41\n"
"ds_read_b128 v[208:211], v230 offset:32768\n"
"v_mfma_f32_16x16x32_bf16 v[152:155], v[212:215], a[196:199], v[152:155]\n"
"s_cmp_ge_u32 s47, 15\n"
"v_exp_f32 v174, v174\n"
"v_mfma_f32_16x16x32_bf16 v[152:155], v[216:219], a[200:203], v[152:155]\n"
"s_cbranch_scc1 LSW_%=\nLSWK_%=:\n"
"ds_read_b128 v[212:215], v231 offset:32768\n"
"v_mfma_f32_16x16x32_bf16 v[152:155], v[220:223], a[204:207], v[152:155]\n"
"s_waitcnt lgkmcnt(2)\n"
"ds_read_b128 v[216:219], v230 offset:33792\n"
"v_cvt_pk_bf16_f32 v137, v158, v159\n"
"v_mfma_f32_16x16x32_bf16 a[232:235], v[120:123], a[244:247], a[232:235]\n"
"v_exp_f32 v164, v164\n"
"v_mfma_f32_16x16x32_bf16 v[24:27], v[96:99], a[244:247], v[24:27]\n"
"s_mov_b32 m0, s44\n"
"ds_read_b128 v[220:223], v231 offset:33792\n"
"v_mfma_f32_16x16x32_bf16 v[24:27], v[100:103], a[248:251], v[24:27]\n"
"buffer_load_dwordx4 v240, s[16:19], s56 offen nt lds\n"
"v_mfma_f32_16x16x32_bf16 v[56:59], v[104:107], a[244:247], v[56:59]\n"
"v_exp_f32 v167, v167\n"
"v_mfma_f32_16x16x32_bf16 v[56:59], v[108:111], a[248:251], v[56:59]\n"
"v_exp_f32 v176, v176\n"
"v_mfma_f32_16x16x32_bf16 v[88:91], v[112:115], a[244:247], v[88:91]\n"
"v_exp_f32 v153, v153\n"
"v_mfma_f32_16x16x32_bf16 v[88:91], v[116:119], a[248:251], v[88:91]\n"
"v_exp_f32 v173, v173\n"
"v_mfma_f32_16x16x32_bf16 v[92:95], v[116:119], v[224:227], v[92:95]\n"
"ds_read_b64_tr_b16 a[250:251], v236 offset:20480\n"
"v_cvt_pk_bf16_f32 v125, v166, v167\n"
"v_mfma_f32_16x16x32_bf16 v[92:95], v[112:115], a[252:255], v[92:95]\n"
"ds_read_b64_tr_b16 a[244:245], v232 offset:16384\n"
"v_cvt_pk_bf16_f32 v144, v160, v161\n"
"v_mfma_f32_16x16x32_bf16 v[60:63], v[108:111], v[224:227], v[60:63]\n"
"ds_read_b64_tr_b16 a[248:249], v236 offset:16384\n"
"v_cvt_pk_bf16_f32 v124, v164, v165\n"
"v_mfma_f32_16x16x32_bf16 v[60:63], v[104:107], a[252:255], v[60:63]\n"
"ds_read_b64_tr_b16 a[246:247], v232 offset:20480\n"
"v_cvt_pk_bf16_f32 v136, v156, v157\n"
"v_mfma_f32_16x16x32_bf16 v[28:31], v[100:103], v[224:227], v[28:31]\n"
"ds_read_b64_tr_b16 v[226:227], v237 offset:20480\n"
"v_cvt_pk_bf16_f32 v145, v162, v163\n"
"v_mfma_f32_16x16x32_bf16 v[28:31], v[96:99], a[252:255], v[28:31]\n"
"ds_read_b64_tr_b16 v[224:225], v237 offset:16384\n"
"v_mfma_f32_16x16x32_bf16 a[236:239], v[120:123], a[252:255], a[236:239]\n"
"s_add_u32 s40, s56, s52\n"
"ds_read_b64_tr_b16 a[252:253], v233 offset:16384\n"
"v_smfmac_f32_16x16x64_bf16 v[198:201], a[240:243], v[104:111], v241\n"
"s_waitcnt lgkmcnt(7)\n"
"buffer_load_dwordx4 v240, s[16:19], s40 offen offset:1024 nt lds\n"
"v_mfma_f32_16x16x32_bf16 v[156:159], v[208:211], a[0:3], v[180:183]\n"
"v_exp_f32 v169, v169\n"
"v_mfma_f32_16x16x32_bf16 v[156:159], v[212:215], a[4:7], v[156:159]\n"
"v_exp_f32 v168, v168\n"
"v_mfma_f32_16x16x32_bf16 v[156:159], v[216:219], a[8:11], v[156:159]\n"
"v_exp_f32 v175, v175\n"
"v_mfma_f32_16x16x32_bf16 v[156:159], v[220:223], a[12:15], v[156:159]\n"
"v_exp_f32 v172, v172\n"
"v_mfma_f32_16x16x32_bf16 v[160:163], v[216:219], a[24:27], v[184:187]\n"
"ds_read_b64_tr_b16 a[254:255], v233 offset:20480\n"
"v_mfma_f32_16x16x32_bf16 v[160:163], v[220:223], a[28:31], v[160:163]\n"
"v_exp_f32 v155, v155\n"
"v_mfma_f32_16x16x32_bf16 v[160:163], v[208:211], a[16:19], v[160:163]\n"
"v_exp_f32 v177, v177\n"
"v_mfma_f32_16x16x32_bf16 v[160:163], v[212:215], a[20:23], v[160:163]\n"
"v_exp_f32 v179, v179\n"
"v_mfma_f32_16x16x32_bf16 v[164:167], v[208:211], a[32:35], v[188:191]\n"
"ds_read_b128 v[208:211], v230 offset:36864\n"
"v_cvt_pk_bf16_f32 v139, v174, v175\n"
"v_mfma_f32_16x16x32_bf16 v[164:167], v[212:215], a[36:39], v[164:167]\n"
"v_exp_f32 v171, v171\n"
"v_mfma_f32_16x16x32_bf16 v[164:167], v[216:219], a[40:43], v[164:167]\n"
"ds_read_b128 v[212:215], v231 offset:36864\n"
"v_cvt_pk_bf16_f32 v138, v172, v173\n"
"v_mfma_f32_16x16x32_bf16 v[164:167], v[220:223], a[44:47], v[164:167]\n"
"s_waitcnt lgkmcnt(3)\n"
"ds_read_b128 v[220:223], v231 offset:37888\n"
"v_cvt_pk_bf16_f32 v130, v168, v169\n"
"v_mfma_f32_16x16x32_bf16 a[208:211], v[120:123], a[244:247], a[208:211]\n"
"v_exp_f32 v154, v154\n"
"v_mfma_f32_16x16x32_bf16 v[0:3], v[96:99], a[244:247], v[0:3]\n"
"s_add_u32 s40, s56, s53\n"
"ds_read_b128 v[216:219], v230 offset:37888\n"
"v_mfma_f32_16x16x32_bf16 v[0:3], v[100:103], a[248:251], v[0:3]\n"
"buffer_load_dwordx4 v240, s[16:19], s40 offen offset:2048 nt lds\n"
"v_mfma_f32_16x16x32_bf16 v[32:35], v[104:107], a[244:247], v[32:35]\n"
"v_exp_f32 v160, v160\n"
"v_mfma_f32_16x16x32_bf16 v[32:35], v[108:111], a[248:251], v[32:35]\n"
"v_exp_f32 v178, v178\n"
"v_mfma_f32_16x16x32_bf16 v[64:67], v[112:115], a[244:247], v[64:67]\n"
"v_exp_f32 v152, v152\n"
"v_mfma_f32_16x16x32_bf16 v[64:67], v[116:119], a[248:251], v[64:67]\n"
"v_exp_f32 v161, v161\n"
"v_mfma_f32_16x16x32_bf16 v[68:71], v[116:119], v[224:227], v[68:71]\n"
"s_waitcnt lgkmcnt(1)\n"
"ds_read_b64_tr_b16 a[244:245], v232 offset:17408\n"
"v_cvt_pk_bf16_f32 v147, v178, v179\n"
"v_mfma_f32_16x16x32_bf16 v[68:71], v[112:115], a[252:255], v[68:71]\n"
"ds_read_b64_tr_b16 a[248:249], v236 offset:17408\n"
"v_cvt_pk_bf16_f32 v146, v176, v177\n"
"v_mfma_f32_16x16x32_bf16 v[36:39], v[108:111], v[224:227], v[36:39]\n"
"ds_read_b64_tr_b16 a[250:251], v236 offset:21504\n"
"v_cvt_pk_bf16_f32 v131, v170, v171\n"
"v_mfma_f32_16x16x32_bf16 v[36:39], v[104:107], a[252:255], v[36:39]\n"
"v_exp_f32 v163, v163\n"
"v_mfma_f32_16x16x32_bf16 v[4:7], v[100:103], v[224:227], v[4:7]\n"
"s_add_u32 s40, s56, s54\n"
"ds_read_b64_tr_b16 a[246:247], v232 offset:21504\n"
"v_mfma_f32_16x16x32_bf16 v[4:7], v[96:99], a[252:255], v[4:7]\n"
"buffer_load_dwordx4 v240, s[16:19], s40 offen offset:3072 nt lds\n"
"v_mfma_f32_16x16x32_bf16 a[212:215], v[120:123], a[252:255], a[212:215]\n"
"v_exp_f32 v167, v167\n"
"v_smfmac_f32_16x16x64_bf16 v[200:203], a[240:243], v[112:119], v241\n"
"v_cvt_pk_bf16_f32 v126, v152, v153\n"
"v_mfma_f32_16x16x32_bf16 v[168:171], v[208:211], a[0:3], v[180:183]\n"
"v_exp_f32 v159, v159\n"
"v_mfma_f32_16x16x32_bf16 v[168:171], v[212:215], a[4:7], v[168:171]\n"
"s_waitcnt lgkmcnt(2)\n"
"v_exp_f32 v165, v165\n"
"v_mfma_f32_16x16x32_bf16 v[168:171], v[216:219], a[8:11], v[168:171]\n"
"ds_read_b64_tr_b16 a[252:253], v233 offset:17408\n"
"ds_read_b64_tr_b16 v[224:225], v237 offset:17408\n"
"v_mfma_f32_16x16x32_bf16 v[168:171], v[220:223], a[12:15], v[168:171]\n"
"ds_read_b64_tr_b16 a[254:255], v233 offset:21504\n"
"ds_read_b64_tr_b16 v[226:227], v237 offset:21504\n"
"v_mfma_f32_16x16x32_bf16 v[172:175], v[216:219], a[24:27], v[184:187]\n"
"v_exp_f32 v166, v166\n"
"v_mfma_f32_16x16x32_bf16 v[172:175], v[220:223], a[28:31], v[172:175]\n"
"v_exp_f32 v164, v164\n"
"v_mfma_f32_16x16x32_bf16 v[172:175], v[208:211], a[16:19], v[172:175]\n"
"v_exp_f32 v157, v157\n"
"v_mfma_f32_16x16x32_bf16 v[172:175], v[212:215], a[20:23], v[172:175]\n"
"s_cbranch_vccnz LPCL2_%=\nLPKL2_%=:\n"
"v_cvt_pk_bf16_f32 v127, v154, v155\n"
"v_mfma_f32_16x16x32_bf16 v[176:179], v[208:211], a[32:35], v[188:191]\n"
"v_exp_f32 v158, v158\n"
"v_mfma_f32_16x16x32_bf16 v[176:179], v[212:215], a[36:39], v[176:179]\n"
"v_exp_f32 v162, v162\n"
"v_mfma_f32_16x16x32_bf16 v[176:179], v[216:219], a[40:43], v[176:179]\n"
"v_exp_f32 v156, v156\n"
"v_mfma_f32_16x16x32_bf16 v[176:179], v[220:223], a[44:47], v[176:179]\n"
"s_waitcnt lgkmcnt(0)\n"
"v_cvt_pk_bf16_f32 v133, v158, v159\n"
"v_mfma_f32_16x16x32_bf16 a[224:227], v[120:123], a[244:247], a[224:227]\n"
"v_exp_f32 v169, v169\n"
"v_mfma_f32_16x16x32_bf16 v[16:19], v[96:99], a[244:247], v[16:19]\n"
"s_waitcnt vmcnt(8)\n"
"v_exp_f32 v172, v172\n"
"v_mfma_f32_16x16x32_bf16 v[16:19], v[100:103], a[248:251], v[16:19]\n"
"s_barrier\n"
"ds_read_b128 v[216:219], v228 offset:50176\n"
"v_mfma_f32_16x16x32_bf16 v[48:51], v[104:107], a[244:247], v[48:51]\n"
"ds_read_b128 v[212:215], v229 offset:49152\n"
"v_cvt_pk_bf16_f32 v140, v160, v161\n"
"v_mfma_f32_16x16x32_bf16 v[48:51], v[108:111], a[248:251], v[48:51]\n"
"ds_read_b128 v[208:211], v228 offset:49152\n"
"v_cvt_pk_bf16_f32 v132, v156, v157\n"
"v_mfma_f32_16x16x32_bf16 v[80:83], v[112:115], a[244:247], v[80:83]\n"
"ds_read_b128 v[220:223], v229 offset:50176\n"
"v_cvt_pk_bf16_f32 v149, v166, v167\n"
"v_mfma_f32_16x16x32_bf16 v[80:83], v[116:119], a[248:251], v[80:83]\n"
"ds_read_b64_tr_b16 a[244:245], v234 offset:32768\n"
"v_cvt_pk_bf16_f32 v141, v162, v163\n"
"v_mfma_f32_16x16x32_bf16 v[84:87], v[116:119], v[224:227], v[84:87]\n"
"ds_read_b64_tr_b16 a[248:249], v238 offset:32768\n"
"v_mfma_f32_16x16x32_bf16 v[84:87], v[112:115], a[252:255], v[84:87]\n"
"ds_read_b64_tr_b16 a[246:247], v234 offset:36864\n"
"v_mfma_f32_16x16x32_bf16 v[52:55], v[108:111], v[224:227], v[52:55]\n"
"ds_read_b64_tr_b16 a[250:251], v238 offset:36864\n"
"v_mfma_f32_16x16x32_bf16 v[52:55], v[104:107], a[252:255], v[52:55]\n"
"v_exp_f32 v175, v175\n"
"v_mfma_f32_16x16x32_bf16 v[20:23], v[100:103], v[224:227], v[20:23]\n"
"ds_read_b64_tr_b16 v[224:225], v239 offset:32768\n"
"v_mfma_f32_16x16x32_bf16 v[20:23], v[96:99], a[252:255], v[20:23]\n"
"v_exp_f32 v173, v173\n"
"v_mfma_f32_16x16x32_bf16 a[228:231], v[120:123], a[252:255], a[228:231]\n"
"s_waitcnt lgkmcnt(5)\n"
"ds_read_b64_tr_b16 a[254:255], v235 offset:36864\n"
"v_cvt_pk_bf16_f32 v148, v164, v165\n"
"v_mfma_f32_16x16x32_bf16 v[152:155], v[208:211], a[0:3], v[180:183]\n"
"v_exp_f32 v174, v174\n"
"v_mfma_f32_16x16x32_bf16 v[152:155], v[212:215], a[4:7], v[152:155]\n"
"ds_read_b64_tr_b16 a[252:253], v235 offset:32768\n"
"v_cvt_pk_bf16_f32 v143, v174, v175\n"
"v_mfma_f32_16x16x32_bf16 v[152:155], v[216:219], a[8:11], v[152:155]\n"
"s_add_u32 m0, s44, 65536\n"
"ds_read_b64_tr_b16 v[226:227], v239 offset:36864\n"
"v_mfma_f32_16x16x32_bf16 v[152:155], v[220:223], a[12:15], v[152:155]\n"
"buffer_load_dwordx4 v240, s[20:23], s56 offen nt lds\n"
"v_mfma_f32_16x16x32_bf16 v[156:159], v[216:219], a[24:27], v[184:187]\n"
"v_exp_f32 v171, v171\n"
"v_mfma_f32_16x16x32_bf16 v[156:159], v[220:223], a[28:31], v[156:159]\n"
"v_exp_f32 v168, v168\n"
"v_mfma_f32_16x16x32_bf16 v[156:159], v[208:211], a[16:19], v[156:159]\n"
"v_exp_f32 v176, v176\n"
"v_mfma_f32_16x16x32_bf16 v[156:159], v[212:215], a[20:23], v[156:159]\n"
"v_exp_f32 v177, v177\n"
"v_mfma_f32_16x16x32_bf16 v[160:163], v[208:211], a[32:35], v[188:191]\n"
"v_exp_f32 v179, v179\n"
"v_mfma_f32_16x16x32_bf16 v[160:163], v[212:215], a[36:39], v[160:163]\n"
"v_exp_f32 v170, v170\n"
"v_mfma_f32_16x16x32_bf16 v[160:163], v[216:219], a[40:43], v[160:163]\n"
"v_exp_f32 v153, v153\n"
"v_mfma_f32_16x16x32_bf16 v[160:163], v[220:223], a[44:47], v[160:163]\n"
"v_cvt_pk_bf16_f32 v134, v168, v169\n"
"v_mfma_f32_16x16x32_bf16 v[164:167], v[216:219], a[200:203], v[192:195]\n"
"ds_read_b128 v[216:219], v228 offset:54272\n"
"v_cvt_pk_bf16_f32 v135, v170, v171\n"
"v_mfma_f32_16x16x32_bf16 v[164:167], v[220:223], a[204:207], v[164:167]\n"
"v_cvt_pk_bf16_f32 v142, v172, v173\n"
"v_cvt_pk_bf16_f32 v150, v176, v177\n"
"v_mfma_f32_16x16x32_bf16 v[164:167], v[208:211], a[192:195], v[164:167]\n"
"s_add_u32 s40, s56, s52\n"
"ds_read_b128 v[208:211], v228 offset:53248\n"
"v_mfma_f32_16x16x32_bf16 v[164:167], v[212:215], a[196:199], v[164:167]\n"
"s_waitcnt lgkmcnt(2)\n"
"buffer_load_dwordx4 v240, s[20:23], s40 offen offset:1024 nt lds\n"
"v_mfma_f32_16x16x32_bf16 a[216:219], v[124:127], a[244:247], a[216:219]\n"
"v_exp_f32 v155, v155\n"
"v_mfma_f32_16x16x32_bf16 v[8:11], v[128:131], a[244:247], v[8:11]\n"
"v_exp_f32 v161, v161\n"
"v_mfma_f32_16x16x32_bf16 v[8:11], v[132:135], a[248:251], v[8:11]\n"
"v_exp_f32 v163, v163\n"
"v_mfma_f32_16x16x32_bf16 v[40:43], v[136:139], a[244:247], v[40:43]\n"
"v_exp_f32 v178, v178\n"
"v_mfma_f32_16x16x32_bf16 v[40:43], v[140:143], a[248:251], v[40:43]\n"
"ds_read_b128 v[220:223], v229 offset:54272\n"
"v_cvt_pk_bf16_f32 v151, v178, v179\n"
"v_mfma_f32_16x16x32_bf16 v[72:75], v[144:147], a[244:247], v[72:75]\n"
"ds_read_b64_tr_b16 a[244:245], v234 offset:33792\n"
"v_mfma_f32_16x16x32_bf16 v[72:75], v[148:151], a[248:251], v[72:75]\n"
"v_exp_f32 v158, v158\n"
"v_mfma_f32_16x16x32_bf16 v[76:79], v[148:151], v[224:227], v[76:79]\n"
"ds_read_b128 v[212:215], v229 offset:53248\n"
"v_mfma_f32_16x16x32_bf16 v[76:79], v[144:147], a[252:255], v[76:79]\n"
"v_exp_f32 v160, v160\n"
"v_mfma_f32_16x16x32_bf16 v[44:47], v[140:143], v[224:227], v[44:47]\n"
"s_add_u32 s40, s56, s53\n"
"ds_read_b64_tr_b16 a[248:249], v238 offset:33792\n"
"v_mfma_f32_16x16x32_bf16 v[44:47], v[136:139], a[252:255], v[44:47]\n"
"buffer_load_dwordx4 v240, s[20:23], s40 offen offset:2048 nt lds\n"
"v_mfma_f32_16x16x32_bf16 v[12:15], v[132:135], v[224:227], v[12:15]\n"
"v_exp_f32 v165, v165\n"
"v_mfma_f32_16x16x32_bf16 v[12:15], v[128:131], a[252:255], v[12:15]\n"
"v_exp_f32 v159, v159\n"
"v_mfma_f32_16x16x32_bf16 a[220:223], v[124:127], a[252:255], a[220:223]\n"
"v_exp_f32 v152, v152\n"
"v_smfmac_f32_16x16x64_bf16 v[196:199], a[240:243], v[128:135], v241\n"
"v_mfma_f32_16x16x32_bf16 v[202:205], a[240:243], v[124:127], v[202:205]\n"
"s_waitcnt lgkmcnt(1)\n"
"ds_read_b64_tr_b16 v[224:225], v239 offset:33792\n"
"v_cvt_pk_bf16_f32 v96, v152, v153\n"
"v_mfma_f32_16x16x32_bf16 v[168:171], v[216:219], a[8:11], v[180:183]\n"
"v_exp_f32 v162, v162\n"
"v_mfma_f32_16x16x32_bf16 v[168:171], v[220:223], a[12:15], v[168:171]\n"
"ds_read_b64_tr_b16 a[250:251], v238 offset:37888\n"
"v_mfma_f32_16x16x32_bf16 v[168:171], v[208:211], a[0:3], v[168:171]\n"
"ds_read_b64_tr_b16 a[252:253], v235 offset:33792\n"
"v_mfma_f32_16x16x32_bf16 v[168:171], v[212:215], a[4:7], v[168:171]\n"
"v_exp_f32 v154, v154\n"
"v_mfma_f32_16x16x32_bf16 v[172:175], v[208:211], a[16:19], v[184:187]\n"
"ds_read_b64_tr_b16 v[226:227], v239 offset:37888\n"
"v_mfma_f32_16x16x32_bf16 v[172:175], v[212:215], a[20:23], v[172:175]\n"
"ds_read_b64_tr_b16 a[246:247], v234 offset:37888\n"
"v_cvt_pk_bf16_f32 v97, v154, v155\n"
"v_mfma_f32_16x16x32_bf16 v[172:175], v[216:219], a[24:27], v[172:175]\n"
"s_add_u32 s40, s56, s54\n"
"ds_read_b64_tr_b16 a[254:255], v235 offset:37888\n"
"v_mfma_f32_16x16x32_bf16 v[172:175], v[220:223], a[28:31], v[172:175]\n"
"buffer_load_dwordx4 v240, s[20:23], s40 offen offset:3072 nt lds\n"
"v_mfma_f32_16x16x32_bf16 v[176:179], v[216:219], a[40:43], v[188:191]\n"
"s_add_u32 s55, s55, s59\n"
"v_exp_f32 v157, v157\n"
"v_mfma_f32_16x16x32_bf16 v[176:179], v[220:223], a[44:47], v[176:179]\n"
"s_add_u32 s41, s55, s57\n"
"v_exp_f32 v170, v170\n"
"v_mfma_f32_16x16x32_bf16 v[176:179], v[208:211], a[32:35], v[176:179]\n"
"s_and_b32 s41, s41, s58\n"
"v_exp_f32 v156, v156\n"
"v_mfma_f32_16x16x32_bf16 v[176:179], v[212:215], a[36:39], v[176:179]\n"
"s_andn2_b32 s56, s55, s58\n"
"v_exp_f32 v166, v166\n"
"v_mfma_f32_16x16x32_bf16 v[152:155], v[208:211], a[192:195], v[192:195]\n"
"s_or_b32 s56, s56, s41\n"
"ds_read_b128 v[208:211], v230 offset:49152\n"
"v_mfma_f32_16x16x32_bf16 v[152:155], v[212:215], a[196:199], v[152:155]\n"
"v_exp_f32 v174, v174\n"
"v_mfma_f32_16x16x32_bf16 v[152:155], v[216:219], a[200:203], v[152:155]\n"
"ds_read_b128 v[212:215], v231 offset:49152\n"
"v_mfma_f32_16x16x32_bf16 v[152:155], v[220:223], a[204:207], v[152:155]\n"
"s_waitcnt lgkmcnt(2)\n"
"ds_read_b128 v[216:219], v230 offset:50176\n"
"v_cvt_pk_bf16_f32 v105, v158, v159\n"
"v_mfma_f32_16x16x32_bf16 a[232:235], v[124:127], a[244:247], a[232:235]\n"
"v_exp_f32 v164, v164\n"
"v_mfma_f32_16x16x32_bf16 v[24:27], v[128:131], a[244:247], v[24:27]\n"
"s_add_u32 m0, s44, 16384\n"
"ds_read_b128 v[220:223], v231 offset:50176\n"
"v_mfma_f32_16x16x32_bf16 v[24:27], v[132:135], a[248:251], v[24:27]\n"
"buffer_load_dwordx4 v240, s[16:19], s56 offen nt lds\n"
"v_mfma_f32_16x16x32_bf16 v[56:59], v[136:139], a[244:247], v[56:59]\n"
"v_exp_f32 v167, v167\n"
"v_mfma_f32_16x16x32_bf16 v[56:59], v[140:143], a[248:251], v[56:59]\n"
"v_exp_f32 v176, v176\n"
"v_mfma_f32_16x16x32_bf16 v[88:91], v[144:147], a[244:247], v[88:91]\n"
"v_exp_f32 v153, v153\n"
"v_mfma_f32_16x16x32_bf16 v[88:91], v[148:151], a[248:251], v[88:91]\n"
"v_exp_f32 v173, v173\n"
"v_mfma_f32_16x16x32_bf16 v[92:95], v[148:151], v[224:227], v[92:95]\n"
"ds_read_b64_tr_b16 a[250:251], v236 offset:36864\n"
"v_cvt_pk_bf16_f32 v121, v166, v167\n"
"v_mfma_f32_16x16x32_bf16 v[92:95], v[144:147], a[252:255], v[92:95]\n"
"ds_read_b64_tr_b16 a[244:245], v232 offset:32768\n"
"v_cvt_pk_bf16_f32 v112, v160, v161\n"
"v_mfma_f32_16x16x32_bf16 v[60:63], v[140:143], v[224:227], v[60:63]\n"
"ds_read_b64_tr_b16 a[248:249], v236 offset:32768\n"
"v_cvt_pk_bf16_f32 v120, v164, v165\n"
"v_mfma_f32_16x16x32_bf16 v[60:63], v[136:139], a[252:255], v[60:63]\n"
"ds_read_b64_tr_b16 a[246:247], v232 offset:36864\n"
"v_cvt_pk_bf16_f32 v104, v156, v157\n"
"v_mfma_f32_16x16x32_bf16 v[28:31], v[132:135], v[224:227], v[28:31]\n"
"ds_read_b64_tr_b16 v[226:227], v237 offset:36864\n"
"v_cvt_pk_bf16_f32 v113, v162, v163\n"
"v_mfma_f32_16x16x32_bf16 v[28:31], v[128:131], a[252:255], v[28:31]\n"
"ds_read_b64_tr_b16 v[224:225], v237 offset:32768\n"
"v_mfma_f32_16x16x32_bf16 a[236:239], v[124:127], a[252:255], a[236:239]\n"
"s_add_u32 s40, s56, s52\n"
"ds_read_b64_tr_b16 a[252:253], v233 offset:32768\n"
"v_smfmac_f32_16x16x64_bf16 v[198:201], a[240:243], v[136:143], v241\n"
"s_waitcnt lgkmcnt(7)\n"
"buffer_load_dwordx4 v240, s[16:19], s40 offen offset:1024 nt lds\n"
"v_mfma_f32_16x16x32_bf16 v[156:159], v[208:211], a[0:3], v[180:183]\n"
"v_exp_f32 v169, v169\n"
"v_mfma_f32_16x16x32_bf16 v[156:159], v[212:215], a[4:7], v[156:159]\n"
"v_exp_f32 v168, v168\n"
"v_mfma_f32_16x16x32_bf16 v[156:159], v[216:219], a[8:11], v[156:159]\n"
"v_exp_f32 v175, v175\n"
"v_mfma_f32_16x16x32_bf16 v[156:159], v[220:223], a[12:15], v[156:159]\n"
"v_exp_f32 v172, v172\n"
"v_mfma_f32_16x16x32_bf16 v[160:163], v[216:219], a[24:27], v[184:187]\n"
"ds_read_b64_tr_b16 a[254:255], v233 offset:36864\n"
"v_mfma_f32_16x16x32_bf16 v[160:163], v[220:223], a[28:31], v[160:163]\n"
"v_exp_f32 v155, v155\n"
"v_mfma_f32_16x16x32_bf16 v[160:163], v[208:211], a[16:19], v[160:163]\n"
"v_exp_f32 v177, v177\n"
"v_mfma_f32_16x16x32_bf16 v[160:163], v[212:215], a[20:23], v[160:163]\n"
"v_exp_f32 v179, v179\n"
"v_mfma_f32_16x16x32_bf16 v[164:167], v[208:211], a[32:35], v[188:191]\n"
"ds_read_b128 v[208:211], v230 offset:53248\n"
"v_cvt_pk_bf16_f32 v107, v174, v175\n"
"v_mfma_f32_16x16x32_bf16 v[164:167], v[212:215], a[36:39], v[164:167]\n"
"v_exp_f32 v171, v171\n"
"v_mfma_f32_16x16x32_bf16 v[164:167], v[216:219], a[40:43], v[164:167]\n"
"ds_read_b128 v[212:215], v231 offset:53248\n"
"v_cvt_pk_bf16_f32 v106, v172, v173\n"
"v_mfma_f32_16x16x32_bf16 v[164:167], v[220:223], a[44:47], v[164:167]\n"
"s_waitcnt lgkmcnt(3)\n"
"ds_read_b128 v[220:223], v231 offset:54272\n"
"v_cvt_pk_bf16_f32 v98, v168, v169\n"
"v_mfma_f32_16x16x32_bf16 a[208:211], v[124:127], a[244:247], a[208:211]\n"
"v_exp_f32 v154, v154\n"
"v_mfma_f32_16x16x32_bf16 v[0:3], v[128:131], a[244:247], v[0:3]\n"
"s_add_u32 s40, s56, s53\n"
"ds_read_b128 v[216:219], v230 offset:54272\n"
"v_mfma_f32_16x16x32_bf16 v[0:3], v[132:135], a[248:251], v[0:3]\n"
"buffer_load_dwordx4 v240, s[16:19], s40 offen offset:2048 nt lds\n"
"v_mfma_f32_16x16x32_bf16 v[32:35], v[136:139], a[244:247], v[32:35]\n"
"v_exp_f32 v160, v160\n"
"v_mfma_f32_16x16x32_bf16 v[32:35], v[140:143], a[248:251], v[32:35]\n"
"v_exp_f32 v178, v178\n"
"v_mfma_f32_16x16x32_bf16 v[64:67], v[144:147], a[244:247], v[64:67]\n"
"v_exp_f32 v152, v152\n"
"v_mfma_f32_16x16x32_bf16 v[64:67], v[148:151], a[248:251], v[64:67]\n"
"v_exp_f32 v161, v161\n"
"v_mfma_f32_16x16x32_bf16 v[68:71], v[148:151], v[224:227], v[68:71]\n"
"s_waitcnt lgkmcnt(1)\n"
"ds_read_b64_tr_b16 a[244:245], v232 offset:33792\n"
"v_cvt_pk_bf16_f32 v115, v178, v179\n"
"v_mfma_f32_16x16x32_bf16 v[68:71], v[144:147], a[252:255], v[68:71]\n"
"ds_read_b64_tr_b16 a[248:249], v236 offset:33792\n"
"v_cvt_pk_bf16_f32 v114, v176, v177\n"
"v_mfma_f32_16x16x32_bf16 v[36:39], v[140:143], v[224:227], v[36:39]\n"
"ds_read_b64_tr_b16 a[250:251], v236 offset:37888\n"
"v_cvt_pk_bf16_f32 v99, v170, v171\n"
"v_mfma_f32_16x16x32_bf16 v[36:39], v[136:139], a[252:255], v[36:39]\n"
"v_exp_f32 v163, v163\n"
"v_mfma_f32_16x16x32_bf16 v[4:7], v[132:135], v[224:227], v[4:7]\n"
"s_add_u32 s40, s56, s54\n"
"ds_read_b64_tr_b16 a[246:247], v232 offset:37888\n"
"v_mfma_f32_16x16x32_bf16 v[4:7], v[128:131], a[252:255], v[4:7]\n"
"buffer_load_dwordx4 v240, s[16:19], s40 offen offset:3072 nt lds\n"
"v_mfma_f32_16x16x32_bf16 a[212:215], v[124:127], a[252:255], a[212:215]\n"
"v_exp_f32 v167, v167\n"
"v_smfmac_f32_16x16x64_bf16 v[200:203], a[240:243], v[144:151], v241\n"
"v_cvt_pk_bf16_f32 v122, v152, v153\n"
"v_mfma_f32_16x16x32_bf16 v[168:171], v[208:211], a[0:3], v[180:183]\n"
"v_exp_f32 v159, v159\n"
"v_mfma_f32_16x16x32_bf16 v[168:171], v[212:215], a[4:7], v[168:171]\n"
"s_waitcnt lgkmcnt(2)\n"
"v_exp_f32 v165, v165\n"
"v_mfma_f32_16x16x32_bf16 v[168:171], v[216:219], a[8:11], v[168:171]\n"
"ds_read_b64_tr_b16 a[252:253], v233 offset:33792\n"
"ds_read_b64_tr_b16 v[224:225], v237 offset:33792\n"
"v_mfma_f32_16x16x32_bf16 v[168:171], v[220:223], a[12:15], v[168:171]\n"
"ds_read_b64_tr_b16 a[254:255], v233 offset:37888\n"
"ds_read_b64_tr_b16 v[226:227], v237 offset:37888\n"
"v_mfma_f32_16x16x32_bf16 v[172:175], v[216:219], a[24:27], v[184:187]\n"
"v_exp_f32 v166, v166\n"
"v_mfma_f32_16x16x32_bf16 v[172:175], v[220:223], a[28:31], v[172:175]\n"
"v_exp_f32 v164, v164\n"
"v_mfma_f32_16x16x32_bf16 v[172:175], v[208:211], a[16:19], v[172:175]\n"
"v_exp_f32 v157, v157\n"
"v_mfma_f32_16x16x32_bf16 v[172:175], v[212:215], a[20:23], v[172:175]\n"
"s_cbranch_vccnz LPCL3_%=\nLPKL3_%=:\n"
"v_cvt_pk_bf16_f32 v123, v154, v155\n"
"v_mfma_f32_16x16x32_bf16 v[176:179], v[208:211], a[32:35], v[188:191]\n"
"s_add_u32 s47, s47, 1\n"
"v_exp_f32 v158, v158\n"
"v_mfma_f32_16x16x32_bf16 v[176:179], v[212:215], a[36:39], v[176:179]\n"
"v_exp_f32 v162, v162\n"
"v_mfma_f32_16x16x32_bf16 v[176:179], v[216:219], a[40:43], v[176:179]\n"
"s_and_b32 s40, s47, 3\n"
"v_exp_f32 v156, v156\n"
"v_mfma_f32_16x16x32_bf16 v[176:179], v[220:223], a[44:47], v[176:179]\n"
"s_waitcnt lgkmcnt(0)\n"
"v_cvt_pk_bf16_f32 v101, v158, v159\n"
"s_cbranch_scc1 LAL_%=\n"
"s_cmp_eq_u32 s47, 8\n"
"s_cbranch_scc1 LTH1_%=\n"
"s_cmp_eq_u32 s47, 12\n"
"s_cbranch_scc1 LTH2_%=\n"
"s_cmp_eq_u32 s47, 16\n"
"s_cbranch_scc1 LAX_%=\n"
"LTH0_%=:\n"
"v_mfma_f32_16x16x32_bf16 a[224:227], v[124:127], a[244:247], a[224:227]\n"
"v_exp_f32 v171, v171\n"
"v_mfma_f32_16x16x32_bf16 v[16:19], v[128:131], a[244:247], v[16:19]\n"
"s_waitcnt vmcnt(8)\n"
"v_exp_f32 v168, v168\n"
"v_mfma_f32_16x16x32_bf16 v[16:19], v[132:135], a[248:251], v[16:19]\n"
"s_barrier\n"
"v_mfma_f32_16x16x32_bf16 v[48:51], v[136:139], a[244:247], v[48:51]\n"
"v_exp_f32 v179, v179\n"
"v_mfma_f32_16x16x32_bf16 v[48:51], v[140:143], a[248:251], v[48:51]\n"
"v_exp_f32 v169, v169\n"
"v_mfma_f32_16x16x32_bf16 v[80:83], v[144:147], a[244:247], v[80:83]\n"
"ds_read_b64_tr_b16 a[244:245], v234 offset:49152\n"
"v_cvt_pk_bf16_f32 v102, v168, v169\n"
"v_mfma_f32_16x16x32_bf16 v[80:83], v[148:151], a[248:251], v[80:83]\n"
"ds_read_b64_tr_b16 a[246:247], v234 offset:53248\n"
"ds_read_b64_tr_b16 a[250:251], v238 offset:53248\n"
"v_mfma_f32_16x16x32_bf16 v[84:87], v[148:151], v[224:227], v[84:87]\n"
"v_cvt_pk_bf16_f32 v108, v160, v161\n"
"v_cvt_pk_bf16_f32 v109, v162, v163\n"
"v_mfma_f32_16x16x32_bf16 v[84:87], v[144:147], a[252:255], v[84:87]\n"
"v_cvt_pk_bf16_f32 v117, v166, v167\n"
"v_exp_f32 v172, v172\n"
"v_exp_f32 v173, v173\n"
"v_exp_f32 v176, v176\n"
"v_exp_f32 v177, v177\n"
"v_exp_f32 v178, v178\n"
"v_mfma_f32_16x16x32_bf16 v[52:55], v[140:143], v[224:227], v[52:55]\n"
"ds_read_b64_tr_b16 a[248:249], v238 offset:49152\n"
"v_cvt_pk_bf16_f32 v100, v156, v157\n"
"v_mfma_f32_16x16x32_bf16 v[52:55], v[136:139], a[252:255], v[52:55]\n"
"v_exp_f32 v170, v170\n"
"v_mfma_f32_16x16x32_bf16 v[20:23], v[132:135], v[224:227], v[20:23]\n"
"ds_read_b64_tr_b16 v[226:227], v239 offset:53248\n"
"v_cvt_pk_bf16_f32 v110, v172, v173\n"
"v_mfma_f32_16x16x32_bf16 v[20:23], v[128:131], a[252:255], v[20:23]\n"
"v_exp_f32 v175, v175\n"
"v_mfma_f32_16x16x32_bf16 a[228:231], v[124:127], a[252:255], a[228:231]\n"
"s_waitcnt lgkmcnt(3)\n"
"ds_read_b64_tr_b16 v[224:225], v239 offset:49152\n"
"v_cvt_pk_bf16_f32 v103, v170, v171\n"
"v_mfma_f32_16x16x32_bf16 a[216:219], v[120:123], a[244:247], a[216:219]\n"
"s_add_u32 m0, s44, 81920\n"
"ds_read_b64_tr_b16 a[254:255], v235 offset:53248\n"
"v_cvt_pk_bf16_f32 v118, v176, v177\n"
"v_smfmac_f32_16x16x64_bf16 v[196:199], a[240:243], v[96:103], v241\n"
"buffer_load_dwordx4 v240, s[20:23], s56 offen nt lds\n"
"v_mfma_f32_16x16x32_bf16 v[8:11], v[96:99], a[244:247], v[8:11]\n"
"s_waitcnt lgkmcnt(3)\n"
"v_cvt_pk_bf16_f32 v116, v164, v165\n"
"v_cvt_pk_bf16_f32 v119, v178, v179\n"
"v_mfma_f32_16x16x32_bf16 v[8:11], v[100:103], a[248:251], v[8:11]\n"
"v_exp_f32 v174, v174\n"
"v_smfmac_f32_16x16x64_bf16 v[200:203], a[240:243], v[112:119], v241\n"
"ds_read_b64_tr_b16 a[252:253], v235 offset:49152\n"
"v_cvt_pk_bf16_f32 v111, v174, v175\n"
"v_mfma_f32_16x16x32_bf16 v[40:43], v[104:107], a[244:247], v[40:43]\n"
"ds_read_b128 v[212:215], v229 offset:0\n"
"v_add_f32 v196, v196, v197\n"
"v_mfma_f32_16x16x32_bf16 v[40:43], v[108:111], a[248:251], v[40:43]\n"
"ds_read_b128 v[208:211], v228 offset:0\n"
"v_and_b32 v255, 0x3f0, v242\n"
"v_smfmac_f32_16x16x64_bf16 v[198:201], a[240:243], v[104:111], v241\n"
"ds_read_b128 v[220:223], v229 offset:1024\n"
"v_mfma_f32_16x16x32_bf16 v[72:75], v[112:115], a[244:247], v[72:75]\n"
"ds_read_b64_tr_b16 a[244:245], v234 offset:50176\n"
"v_lshrrev_b32 v255, 1, v255\n"
"v_mfma_f32_16x16x32_bf16 v[72:75], v[116:119], a[248:251], v[72:75]\n"
"ds_read_b64_tr_b16 a[248:249], v238 offset:50176\n"
"v_cvt_f32_u32 v254, s43\n"
"v_mfma_f32_16x16x32_bf16 v[202:205], a[240:243], v[120:123], v[202:205]\n"
"s_waitcnt lgkmcnt(6)\n"
"ds_read_b64_tr_b16 a[246:247], v234 offset:54272\n"
"v_add_f32 v198, v198, v199\n"
"v_mfma_f32_16x16x32_bf16 v[76:79], v[116:119], v[224:227], v[76:79]\n"
"s_waitcnt lgkmcnt(6)\n"
"ds_read_b64_tr_b16 a[250:251], v238 offset:54272\n"
"v_add_f32 v200, v200, v201\n"
"v_mfma_f32_16x16x32_bf16 v[76:79], v[112:115], a[252:255], v[76:79]\n"
"v_add_f32 v249, v196, v198\n"
"v_add_f32 v249, v249, v200\n"
"v_mfma_f32_16x16x32_bf16 v[44:47], v[108:111], v[224:227], v[44:47]\n"
"v_add_f32 v254, v254, v202\n"
"v_min3_f32 v254, v254, v196, v198\n"
"v_mfma_f32_16x16x32_bf16 v[44:47], v[104:107], a[252:255], v[44:47]\n"
"v_min_f32 v254, v254, v200\n"
"v_rcp_f32 v254, v254\n"
"v_mfma_f32_16x16x32_bf16 v[12:15], v[100:103], v[224:227], v[12:15]\n"
"s_add_u32 s40, s56, s52\n"
"v_add_f32 v249, v249, v202\n"
"v_fmac_f32 v249, 0x53800000, v254\n"
"v_mfma_f32_16x16x32_bf16 v[12:15], v[96:99], a[252:255], v[12:15]\n"
"buffer_load_dwordx4 v240, s[20:23], s40 offen offset:1024 nt lds\n"
"v_mfma_f32_16x16x32_bf16 a[220:223], v[120:123], a[252:255], a[220:223]\n"
"s_waitcnt lgkmcnt(1)\n"
"v_cmp_nge_f32 vcc, s45, v249\n"
"s_cbranch_vccnz LSLOW_%=\nLBACK0_%=:\ns_mov_b64 vcc, s[64:65]\n"
"v_mfma_f32_16x16x32_bf16 a[232:235], v[120:123], a[244:247], a[232:235]\n"
"v_accvgpr_mov_b32 a5, a53\n"
"v_accvgpr_mov_b32 a8, a56\n"
"v_accvgpr_mov_b32 a10, a58\n"
"v_accvgpr_mov_b32 a34, a82\n"
"v_accvgpr_mov_b32 a38, a86\n"
"v_accvgpr_mov_b32 a46, a94\n"
"v_xor_b32 v207, 0x400, v248\n"
"v_bfe_u32 v206, v242, 8, 2\n"
"v_lshl_add_u32 v206, v206, 4, s43\n"
"v_mfma_f32_16x16x32_bf16 v[24:27], v[96:99], a[244:247], v[24:27]\n"
"s_waitcnt lgkmcnt(0)\n"
"ds_read_b128 a[204:207], v207 offset:6144\n"
"ds_read_b128 v[132:135], v206 offset:1024\n"
"v_mfma_f32_16x16x32_bf16 v[24:27], v[100:103], a[248:251], v[24:27]\n"
"ds_read_b128 v[124:127], v206 offset:0\n"
"v_accvgpr_mov_b32 a13, a61\n"
"v_accvgpr_mov_b32 a14, a62\n"
"v_accvgpr_mov_b32 a16, a64\n"
"v_accvgpr_mov_b32 a19, a67\n"
"v_accvgpr_mov_b32 a27, a75\n"
"v_mfma_f32_16x16x32_bf16 v[56:59], v[104:107], a[244:247], v[56:59]\n"
"ds_read_b128 a[196:199], v207 offset:4096\n"
"v_xor_b32 v207, 64, v206\n"
"v_mfma_f32_16x16x32_bf16 v[56:59], v[108:111], a[248:251], v[56:59]\n"
"ds_read_b128 v[128:131], v207 offset:0\n"
"v_accvgpr_mov_b32 a2, a50\n"
"v_accvgpr_mov_b32 a3, a51\n"
"v_accvgpr_mov_b32 a9, a57\n"
"v_accvgpr_mov_b32 a22, a70\n"
"v_accvgpr_mov_b32 a30, a78\n"
"v_accvgpr_mov_b32 a35, a83\n"
"v_accvgpr_mov_b32 a37, a85\n"
"v_accvgpr_mov_b32 a44, a92\n"
"v_mfma_f32_16x16x32_bf16 v[88:91], v[112:115], a[244:247], v[88:91]\n"
"ds_read_b128 v[136:139], v207 offset:1024\n"
"v_accvgpr_mov_b32 a0, a48\n"
"v_accvgpr_mov_b32 a1, a49\n"
"v_accvgpr_mov_b32 a7, a55\n"
"v_accvgpr_mov_b32 a20, a68\n"
"v_accvgpr_mov_b32 a23, a71\n"
"v_accvgpr_mov_b32 a31, a79\n"
"v_accvgpr_mov_b32 a45, a93\n"
"v_accvgpr_mov_b32 a47, a95\n"
"v_mfma_f32_16x16x32_bf16 v[88:91], v[116:119], a[248:251], v[88:91]\n"
"v_rcp_f32 v244, v198\n"
"v_accvgpr_mov_b32 a4, a52\n"
"v_accvgpr_mov_b32 a6, a54\n"
"v_accvgpr_mov_b32 a11, a59\n"
"v_accvgpr_mov_b32 a15, a63\n"
"v_accvgpr_mov_b32 a26, a74\n"
"v_accvgpr_mov_b32 a29, a77\n"
"v_accvgpr_mov_b32 a40, a88\n"
"s_waitcnt lgkmcnt(3)\n"
"v_mfma_f32_16x16x32_bf16 v[180:183], v[124:127], a[0:3], 0\n"
"ds_read_b128 v[216:219], v228 offset:1024\n"
"v_accvgpr_mov_b32 a12, a60\n"
"v_accvgpr_mov_b32 a17, a65\n"
"v_accvgpr_mov_b32 a18, a66\n"
"v_accvgpr_mov_b32 a21, a69\n"
"v_accvgpr_mov_b32 a25, a73\n"
"v_accvgpr_mov_b32 a42, a90\n"
"s_waitcnt lgkmcnt(2)\n"
"v_mfma_f32_16x16x32_bf16 v[180:183], v[128:131], a[4:7], v[180:183]\n"
"v_accvgpr_mov_b32 a33, a81\n"
"v_mfma_f32_16x16x32_bf16 v[180:183], v[132:135], a[8:11], v[180:183]\n"
"s_waitcnt lgkmcnt(1)\n"
"ds_read_b128 a[200:203], v248 offset:6144\n"
"v_mov_b32_dpp v244, v244 row_ror:12 row_mask:0x2 bank_mask:0xf\n"
"v_mfma_f32_16x16x32_bf16 v[180:183], v[136:139], a[12:15], v[180:183]\n"
"v_rcp_f32 v243, v196\n"
"v_accvgpr_mov_b32 a28, a76\n"
"v_accvgpr_mov_b32 a32, a80\n"
"v_accvgpr_mov_b32 a36, a84\n"
"v_accvgpr_mov_b32 a39, a87\n"
"v_mfma_f32_16x16x32_bf16 v[184:187], v[124:127], a[16:19], 0\n"
"v_accvgpr_mov_b32 a24, a72\n"
"v_mfma_f32_16x16x32_bf16 v[184:187], v[128:131], a[20:23], v[184:187]\n"
"ds_read_b64_tr_b16 v[224:225], v239 offset:50176\n"
"v_mov_b32_dpp v243, v243 row_ror:12 row_mask:0x2 bank_mask:0xf\n"
"v_mfma_f32_16x16x32_bf16 v[184:187], v[132:135], a[24:27], v[184:187]\n"
"s_add_u32 s40, s56, s53\n"
"ds_read_b128 a[192:195], v248 offset:4096\n"
"v_mov_b32 v252, v192\n"
"v_accvgpr_mov_b32 a43, a91\n"
"v_xor_b32 v182, s77, v182\n"
"v_mfma_f32_16x16x32_bf16 v[184:187], v[136:139], a[28:31], v[184:187]\n"
"buffer_load_dwordx4 v240, s[20:23], s40 offen offset:2048 nt lds\n"
"v_rcp_f32 v245, v200\n"
"v_accvgpr_mov_b32 a41, a89\n"
"v_xor_b32 v180, s77, v180\n"
"v_mfma_f32_16x16x32_bf16 v[188:191], v[124:127], a[32:35], 0\n"
"v_mov_b32_dpp v245, v245 row_ror:12 row_mask:0x2 bank_mask:0xf\n"
"v_xor_b32 v183, s77, v183\n"
"v_mfma_f32_16x16x32_bf16 v[188:191], v[128:131], a[36:39], v[188:191]\n"
"v_mov_b32_dpp v243, v243 row_ror:8 row_mask:0x4 bank_mask:0xf\n"
"v_xor_b32 v254, 0x11000, v242\n"
"v_mfma_f32_16x16x32_bf16 v[188:191], v[132:135], a[40:43], v[188:191]\n"
"v_mov_b64 v[196:197], 0\n"
"v_xor_b32 v186, s77, v186\n"
"v_mfma_f32_16x16x32_bf16 v[188:191], v[136:139], a[44:47], v[188:191]\n"
"v_xor_b32 v181, s77, v181\n"
"s_waitcnt lgkmcnt(0)\n"
"v_mfma_f32_16x16x32_bf16 v[192:195], v[124:127], a[192:195], 0\n"
"ds_read_b64_tr_b16 a[246:247], v232 offset:53248\n"
"v_xor_b32 v184, s77, v184\n"
"v_mfma_f32_16x16x32_bf16 v[192:195], v[128:131], a[196:199], v[192:195]\n"
"v_mov_b32_dpp v243, v243 row_ror:4 row_mask:0x8 bank_mask:0xf\n"
"v_mfma_f32_16x16x32_bf16 v[192:195], v[132:135], a[200:203], v[192:195]\n"
"v_xor_b32 v185, s77, v185\n"
"v_xor_b32 v187, s77, v187\n"
"v_mfma_f32_16x16x32_bf16 v[192:195], v[136:139], a[204:207], v[192:195]\n"
"ds_read_b64_tr_b16 a[252:253], v235 offset:50176\n"
"v_mov_b32_dpp v245, v245 row_ror:8 row_mask:0x4 bank_mask:0xf\n"
"v_mfma_f32_16x16x32_bf16 v[152:155], v[208:211], a[0:3], v[180:183]\n"
"v_mfma_f32_16x16x32_bf16 v[152:155], v[212:215], a[4:7], v[152:155]\n"
"v_mfma_f32_16x16x32_bf16 v[152:155], v[216:219], a[8:11], v[152:155]\n"
"s_add_u32 s40, s56, s54\n"
"ds_read_b64_tr_b16 a[250:251], v236 offset:53248\n"
"v_mfma_f32_16x16x32_bf16 v[152:155], v[220:223], a[12:15], v[152:155]\n"
"buffer_load_dwordx4 v240, s[20:23], s40 offen offset:3072 nt lds\n"
"v_mov_b32_dpp v244, v244 row_ror:8 row_mask:0x4 bank_mask:0xf\n"
"v_xor_b32 v188, s77, v188\n"
"v_xor_b32 v189, s77, v189\n"
"v_xor_b32 v190, s77, v190\n"
"v_mfma_f32_16x16x32_bf16 v[156:159], v[216:219], a[24:27], v[184:187]\n"
"s_add_u32 s55, s55, s59\n"
"v_mov_b32_dpp v244, v244 row_ror:4 row_mask:0x8 bank_mask:0xf\n"
"v_xor_b32 v191, s77, v191\n"
"v_mfma_f32_16x16x32_bf16 v[156:159], v[220:223], a[28:31], v[156:159]\n"
"s_add_u32 s41, s55, s57\n"
"v_xor_b32 v194, s77, v194\n"
"v_mfma_f32_16x16x32_bf16 v[156:159], v[208:211], a[16:19], v[156:159]\n"
"s_and_b32 s41, s41, s58\n"
"ds_read_b64_tr_b16 a[244:245], v232 offset:49152\n"
"v_xor_b32 v195, s77, v195\n"
"v_mfma_f32_16x16x32_bf16 v[156:159], v[212:215], a[20:23], v[156:159]\n"
"s_andn2_b32 s56, s55, s58\n"
"ds_read_b64_tr_b16 v[226:227], v239 offset:54272\n"
"v_mov_b32_dpp v245, v245 row_ror:4 row_mask:0x8 bank_mask:0xf\n"
"v_xor_b32 v192, s77, v192\n"
"v_exp_f32 v154, v154\n"
"v_mfma_f32_16x16x32_bf16 v[160:163], v[208:211], a[32:35], v[188:191]\n"
"s_or_b32 s56, s56, s41\n"
"v_mfma_f32_16x16x32_bf16 v[160:163], v[212:215], a[36:39], v[160:163]\n"
"v_sub_u32 v255, v242, v255\n"
"v_xor_b32 v193, s77, v193\n"
"v_mfma_f32_16x16x32_bf16 v[160:163], v[216:219], a[40:43], v[160:163]\n"
"v_mfma_f32_16x16x32_bf16 v[160:163], v[220:223], a[44:47], v[160:163]\n"
"ds_read_b64_tr_b16 a[254:255], v235 offset:54272\n"
"v_mul_f32_dpp v40, v244, v40 row_newbcast:0 row_mask:0xf bank_mask:0xf\n"
"v_mul_f32_dpp v72, v245, v72 row_newbcast:0 row_mask:0xf bank_mask:0xf\n"
"v_exp_f32 v156, v156\n"
"v_exp_f32 v158, v158\n"
"v_mfma_f32_16x16x32_bf16 v[164:167], v[216:219], a[200:203], v[192:195]\n"
"v_mfma_f32_16x16x32_bf16 v[164:167], v[220:223], a[204:207], v[164:167]\n"
"v_mul_f32_dpp v8, v243, v8 row_newbcast:0 row_mask:0xf bank_mask:0xf\n"
"v_mfma_f32_16x16x32_bf16 v[164:167], v[208:211], a[192:195], v[164:167]\n"
"ds_read_b128 v[208:211], v228 offset:4096\n"
"v_mul_f32_dpp v76, v245, v76 row_newbcast:0 row_mask:0xf bank_mask:0xf\n"
"v_mfma_f32_16x16x32_bf16 v[164:167], v[212:215], a[196:199], v[164:167]\n"
"ds_read_b128 v[216:219], v228 offset:5120\n"
"v_mul_f32_dpp v44, v244, v44 row_newbcast:0 row_mask:0xf bank_mask:0xf\n"
"v_exp_f32 v163, v163\n"
"s_branch LTC_%=\n"
"LTH1_%=:\n"
"v_mfma_f32_16x16x32_bf16 a[224:227], v[124:127], a[244:247], a[224:227]\n"
"v_exp_f32 v171, v171\n"
"v_mfma_f32_16x16x32_bf16 v[16:19], v[128:131], a[244:247], v[16:19]\n"
"s_waitcnt vmcnt(8)\n"
"v_exp_f32 v168, v168\n"
"v_mfma_f32_16x16x32_bf16 v[16:19], v[132:135], a[248:251], v[16:19]\n"
"s_barrier\n"
"v_mfma_f32_16x16x32_bf16 v[48:51], v[136:139], a[244:247], v[48:51]\n"
"v_exp_f32 v179, v179\n"
"v_mfma_f32_16x16x32_bf16 v[48:51], v[140:143], a[248:251], v[48:51]\n"
"v_exp_f32 v169, v169\n"
"v_mfma_f32_16x16x32_bf16 v[80:83], v[144:147], a[244:247], v[80:83]\n"
"ds_read_b64_tr_b16 a[244:245], v234 offset:49152\n"
"v_cvt_pk_bf16_f32 v102, v168, v169\n"
"v_mfma_f32_16x16x32_bf16 v[80:83], v[148:151], a[248:251], v[80:83]\n"
"ds_read_b64_tr_b16 a[246:247], v234 offset:53248\n"
"ds_read_b64_tr_b16 a[250:251], v238 offset:53248\n"
"v_mfma_f32_16x16x32_bf16 v[84:87], v[148:151], v[224:227], v[84:87]\n"
"v_cvt_pk_bf16_f32 v108, v160, v161\n"
"v_cvt_pk_bf16_f32 v109, v162, v163\n"
"v_mfma_f32_16x16x32_bf16 v[84:87], v[144:147], a[252:255], v[84:87]\n"
"v_cvt_pk_bf16_f32 v117, v166, v167\n"
"v_exp_f32 v172, v172\n"
"v_exp_f32 v173, v173\n"
"v_exp_f32 v176, v176\n"
"v_exp_f32 v177, v177\n"
"v_exp_f32 v178, v178\n"
"v_mfma_f32_16x16x32_bf16 v[52:55], v[140:143], v[224:227], v[52:55]\n"
"ds_read_b64_tr_b16 a[248:249], v238 offset:49152\n"
"v_cvt_pk_bf16_f32 v100, v156, v157\n"
"v_mfma_f32_16x16x32_bf16 v[52:55], v[136:139], a[252:255], v[52:55]\n"
"v_exp_f32 v170, v170\n"
"v_mfma_f32_16x16x32_bf16 v[20:23], v[132:135], v[224:227], v[20:23]\n"
"ds_read_b64_tr_b16 v[226:227], v239 offset:53248\n"
"v_cvt_pk_bf16_f32 v110, v172, v173\n"
"v_mfma_f32_16x16x32_bf16 v[20:23], v[128:131], a[252:255], v[20:23]\n"
"v_exp_f32 v175, v175\n"
"v_mfma_f32_16x16x32_bf16 a[228:231], v[124:127], a[252:255], a[228:231]\n"
"s_waitcnt lgkmcnt(3)\n"
"ds_read_b64_tr_b16 v[224:225], v239 offset:49152\n"
"v_cvt_pk_bf16_f32 v103, v170, v171\n"
"v_mfma_f32_16x16x32_bf16 a[216:219], v[120:123], a[244:247], a[216:219]\n"
"s_add_u32 m0, s44, 81920\n"
"ds_read_b64_tr_b16 a[254:255], v235 offset:53248\n"
"v_cvt_pk_bf16_f32 v118, v176, v177\n"
"v_smfmac_f32_16x16x64_bf16 v[196:199], a[240:243], v[96:103], v241\n"
"buffer_load_dwordx4 v240, s[20:23], s56 offen nt lds\n"
"v_mfma_f32_16x16x32_bf16 v[8:11], v[96:99], a[244:247], v[8:11]\n"
"s_waitcnt lgkmcnt(3)\n"
"v_cvt_pk_bf16_f32 v116, v164, v165\n"
"v_cvt_pk_bf16_f32 v119, v178, v179\n"
"v_mfma_f32_16x16x32_bf16 v[8:11], v[100:103], a[248:251], v[8:11]\n"
"v_exp_f32 v174, v174\n"
"v_smfmac_f32_16x16x64_bf16 v[200:203], a[240:243], v[112:119], v241\n"
"ds_read_b64_tr_b16 a[252:253], v235 offset:49152\n"
"v_cvt_pk_bf16_f32 v111, v174, v175\n"
"v_mfma_f32_16x16x32_bf16 v[40:43], v[104:107], a[244:247], v[40:43]\n"
"ds_read_b128 v[212:215], v229 offset:0\n"
"v_add_f32 v196, v196, v197\n"
"v_mfma_f32_16x16x32_bf16 v[40:43], v[108:111], a[248:251], v[40:43]\n"
"ds_read_b128 v[208:211], v228 offset:0\n"
"v_and_b32 v255, 0x3f0, v242\n"
"v_smfmac_f32_16x16x64_bf16 v[198:201], a[240:243], v[104:111], v241\n"
"ds_read_b128 v[220:223], v229 offset:1024\n"
"v_mfma_f32_16x16x32_bf16 v[72:75], v[112:115], a[244:247], v[72:75]\n"
"ds_read_b64_tr_b16 a[244:245], v234 offset:50176\n"
"v_lshrrev_b32 v255, 1, v255\n"
"v_mfma_f32_16x16x32_bf16 v[72:75], v[116:119], a[248:251], v[72:75]\n"
"ds_read_b64_tr_b16 a[248:249], v238 offset:50176\n"
"v_cvt_f32_u32 v254, s43\n"
"v_mfma_f32_16x16x32_bf16 v[202:205], a[240:243], v[120:123], v[202:205]\n"
"s_waitcnt lgkmcnt(6)\n"
"ds_read_b64_tr_b16 a[246:247], v234 offset:54272\n"
"v_add_f32 v198, v198, v199\n"
"v_mfma_f32_16x16x32_bf16 v[76:79], v[116:119], v[224:227], v[76:79]\n"
"s_waitcnt lgkmcnt(6)\n"
"ds_read_b64_tr_b16 a[250:251], v238 offset:54272\n"
"v_add_f32 v200, v200, v201\n"
"v_mfma_f32_16x16x32_bf16 v[76:79], v[112:115], a[252:255], v[76:79]\n"
"v_add_f32 v249, v196, v198\n"
"v_add_f32 v249, v249, v200\n"
"v_mfma_f32_16x16x32_bf16 v[44:47], v[108:111], v[224:227], v[44:47]\n"
"v_add_f32 v254, v254, v202\n"
"v_min3_f32 v254, v254, v196, v198\n"
"v_mfma_f32_16x16x32_bf16 v[44:47], v[104:107], a[252:255], v[44:47]\n"
"v_min_f32 v254, v254, v200\n"
"v_rcp_f32 v254, v254\n"
"v_mfma_f32_16x16x32_bf16 v[12:15], v[100:103], v[224:227], v[12:15]\n"
"s_add_u32 s40, s56, s52\n"
"v_add_f32 v249, v249, v202\n"
"v_fmac_f32 v249, 0x53800000, v254\n"
"v_mfma_f32_16x16x32_bf16 v[12:15], v[96:99], a[252:255], v[12:15]\n"
"buffer_load_dwordx4 v240, s[20:23], s40 offen offset:1024 nt lds\n"
"v_mfma_f32_16x16x32_bf16 a[220:223], v[120:123], a[252:255], a[220:223]\n"
"s_waitcnt lgkmcnt(1)\n"
"v_cmp_nge_f32 vcc, s45, v249\n"
"s_cbranch_vccnz LSLOW_%=\nLBACK1_%=:\ns_mov_b64 vcc, s[64:65]\n"
"v_mfma_f32_16x16x32_bf16 a[232:235], v[120:123], a[244:247], a[232:235]\n"
"v_accvgpr_mov_b32 a5, a101\n"
"v_accvgpr_mov_b32 a8, a104\n"
"v_accvgpr_mov_b32 a10, a106\n"
"v_accvgpr_mov_b32 a34, a130\n"
"v_accvgpr_mov_b32 a38, a134\n"
"v_accvgpr_mov_b32 a46, a142\n"
"v_xor_b32 v207, 0x400, v248\n"
"v_bfe_u32 v206, v242, 8, 2\n"
"v_lshl_add_u32 v206, v206, 4, s43\n"
"v_mfma_f32_16x16x32_bf16 v[24:27], v[96:99], a[244:247], v[24:27]\n"
"s_waitcnt lgkmcnt(0)\n"
"ds_read_b128 a[204:207], v207 offset:6144\n"
"ds_read_b128 v[132:135], v206 offset:1024\n"
"v_mfma_f32_16x16x32_bf16 v[24:27], v[100:103], a[248:251], v[24:27]\n"
"ds_read_b128 v[124:127], v206 offset:0\n"
"v_accvgpr_mov_b32 a13, a109\n"
"v_accvgpr_mov_b32 a14, a110\n"
"v_accvgpr_mov_b32 a16, a112\n"
"v_accvgpr_mov_b32 a19, a115\n"
"v_accvgpr_mov_b32 a27, a123\n"
"v_mfma_f32_16x16x32_bf16 v[56:59], v[104:107], a[244:247], v[56:59]\n"
"ds_read_b128 a[196:199], v207 offset:4096\n"
"v_xor_b32 v207, 64, v206\n"
"v_mfma_f32_16x16x32_bf16 v[56:59], v[108:111], a[248:251], v[56:59]\n"
"ds_read_b128 v[128:131], v207 offset:0\n"
"v_accvgpr_mov_b32 a2, a98\n"
"v_accvgpr_mov_b32 a3, a99\n"
"v_accvgpr_mov_b32 a9, a105\n"
"v_accvgpr_mov_b32 a22, a118\n"
"v_accvgpr_mov_b32 a30, a126\n"
"v_accvgpr_mov_b32 a35, a131\n"
"v_accvgpr_mov_b32 a37, a133\n"
"v_accvgpr_mov_b32 a44, a140\n"
"v_mfma_f32_16x16x32_bf16 v[88:91], v[112:115], a[244:247], v[88:91]\n"
"ds_read_b128 v[136:139], v207 offset:1024\n"
"v_accvgpr_mov_b32 a0, a96\n"
"v_accvgpr_mov_b32 a1, a97\n"
"v_accvgpr_mov_b32 a7, a103\n"
"v_accvgpr_mov_b32 a20, a116\n"
"v_accvgpr_mov_b32 a23, a119\n"
"v_accvgpr_mov_b32 a31, a127\n"
"v_accvgpr_mov_b32 a45, a141\n"
"v_accvgpr_mov_b32 a47, a143\n"
"v_accvgpr_mov_b32 a116, a68\n"
"v_mfma_f32_16x16x32_bf16 v[88:91], v[116:119], a[248:251], v[88:91]\n"
"s_waitcnt lgkmcnt(3)\n"
"v_rcp_f32 v244, v198\n"
"v_accvgpr_mov_b32 a4, a100\n"
"v_accvgpr_mov_b32 a6, a102\n"
"v_accvgpr_mov_b32 a11, a107\n"
"v_accvgpr_mov_b32 a15, a111\n"
"v_accvgpr_mov_b32 a26, a122\n"
"v_accvgpr_mov_b32 a29, a125\n"
"v_accvgpr_mov_b32 a40, a136\n"
"v_accvgpr_mov_b32 a130, a82\n"
"v_mfma_f32_16x16x32_bf16 v[180:183], v[124:127], a[0:3], 0\n"
"s_waitcnt lgkmcnt(1)\n"
"ds_read_b128 v[216:219], v228 offset:1024\n"
"v_accvgpr_mov_b32 a12, a108\n"
"v_accvgpr_mov_b32 a17, a113\n"
"v_accvgpr_mov_b32 a18, a114\n"
"v_accvgpr_mov_b32 a21, a117\n"
"v_accvgpr_mov_b32 a25, a121\n"
"v_accvgpr_mov_b32 a42, a138\n"
"v_accvgpr_mov_b32 a115, a67\n"
"v_accvgpr_mov_b32 a122, a74\n"
"v_mfma_f32_16x16x32_bf16 v[180:183], v[128:131], a[4:7], v[180:183]\n"
"v_accvgpr_mov_b32 a33, a129\n"
"v_accvgpr_mov_b32 a125, a77\n"
"v_mfma_f32_16x16x32_bf16 v[180:183], v[132:135], a[8:11], v[180:183]\n"
"s_waitcnt lgkmcnt(1)\n"
"ds_read_b128 a[200:203], v248 offset:6144\n"
"v_mov_b32_dpp v244, v244 row_ror:12 row_mask:0x2 bank_mask:0xf\n"
"v_mfma_f32_16x16x32_bf16 v[180:183], v[136:139], a[12:15], v[180:183]\n"
"v_rcp_f32 v243, v196\n"
"v_accvgpr_mov_b32 a28, a124\n"
"v_accvgpr_mov_b32 a32, a128\n"
"v_accvgpr_mov_b32 a36, a132\n"
"v_accvgpr_mov_b32 a39, a135\n"
"v_accvgpr_mov_b32 a119, a71\n"
"v_accvgpr_mov_b32 a110, a62\n"
"v_accvgpr_mov_b32 a121, a73\n"
"v_accvgpr_mov_b32 a136, a88\n"
"v_mfma_f32_16x16x32_bf16 v[184:187], v[124:127], a[16:19], 0\n"
"v_accvgpr_mov_b32 a24, a120\n"
"v_accvgpr_mov_b32 a126, a78\n"
"v_mfma_f32_16x16x32_bf16 v[184:187], v[128:131], a[20:23], v[184:187]\n"
"ds_read_b64_tr_b16 v[224:225], v239 offset:50176\n"
"v_mov_b32_dpp v243, v243 row_ror:12 row_mask:0x2 bank_mask:0xf\n"
"v_mfma_f32_16x16x32_bf16 v[184:187], v[132:135], a[24:27], v[184:187]\n"
"s_add_u32 s40, s56, s53\n"
"ds_read_b128 a[192:195], v248 offset:4096\n"
"v_mov_b32 v252, v192\n"
"v_accvgpr_mov_b32 a43, a139\n"
"v_accvgpr_mov_b32 a133, a85\n"
"v_accvgpr_mov_b32 a111, a63\n"
"v_accvgpr_mov_b32 a99, a51\n"
"v_accvgpr_mov_b32 a131, a83\n"
"v_accvgpr_mov_b32 a106, a58\n"
"v_xor_b32 v182, s77, v182\n"
"v_mfma_f32_16x16x32_bf16 v[184:187], v[136:139], a[28:31], v[184:187]\n"
"buffer_load_dwordx4 v240, s[20:23], s40 offen offset:2048 nt lds\n"
"v_rcp_f32 v245, v200\n"
"v_accvgpr_mov_b32 a41, a137\n"
"v_accvgpr_mov_b32 a134, a86\n"
"v_accvgpr_mov_b32 a142, a94\n"
"v_accvgpr_mov_b32 a112, a64\n"
"v_accvgpr_mov_b32 a113, a65\n"
"v_xor_b32 v180, s77, v180\n"
"v_mfma_f32_16x16x32_bf16 v[188:191], v[124:127], a[32:35], 0\n"
"v_mov_b32_dpp v245, v245 row_ror:12 row_mask:0x2 bank_mask:0xf\n"
"v_xor_b32 v183, s77, v183\n"
"v_mfma_f32_16x16x32_bf16 v[188:191], v[128:131], a[36:39], v[188:191]\n"
"v_mov_b32_dpp v243, v243 row_ror:8 row_mask:0x4 bank_mask:0xf\n"
"v_xor_b32 v254, 0x11000, v242\n"
"v_mfma_f32_16x16x32_bf16 v[188:191], v[132:135], a[40:43], v[188:191]\n"
"v_mov_b64 v[196:197], 0\n"
"v_xor_b32 v186, s77, v186\n"
"v_mfma_f32_16x16x32_bf16 v[188:191], v[136:139], a[44:47], v[188:191]\n"
"s_waitcnt lgkmcnt(0)\n"
"v_accvgpr_mov_b32 a141, a93\n"
"v_xor_b32 v181, s77, v181\n"
"v_mfma_f32_16x16x32_bf16 v[192:195], v[124:127], a[192:195], 0\n"
"ds_read_b64_tr_b16 a[246:247], v232 offset:53248\n"
"v_xor_b32 v184, s77, v184\n"
"v_mfma_f32_16x16x32_bf16 v[192:195], v[128:131], a[196:199], v[192:195]\n"
"v_mov_b32_dpp v243, v243 row_ror:4 row_mask:0x8 bank_mask:0xf\n"
"v_accvgpr_mov_b32 a124, a76\n"
"v_mfma_f32_16x16x32_bf16 v[192:195], v[132:135], a[200:203], v[192:195]\n"
"v_xor_b32 v185, s77, v185\n"
"v_xor_b32 v187, s77, v187\n"
"v_mfma_f32_16x16x32_bf16 v[192:195], v[136:139], a[204:207], v[192:195]\n"
"ds_read_b64_tr_b16 a[252:253], v235 offset:50176\n"
"v_mov_b32_dpp v245, v245 row_ror:8 row_mask:0x4 bank_mask:0xf\n"
"v_mfma_f32_16x16x32_bf16 v[152:155], v[208:211], a[0:3], v[180:183]\n"
"v_accvgpr_mov_b32 a135, a87\n"
"v_accvgpr_mov_b32 a127, a79\n"
"v_mfma_f32_16x16x32_bf16 v[152:155], v[212:215], a[4:7], v[152:155]\n"
"v_accvgpr_mov_b32 a100, a52\n"
"v_accvgpr_mov_b32 a102, a54\n"
"v_mfma_f32_16x16x32_bf16 v[152:155], v[216:219], a[8:11], v[152:155]\n"
"s_add_u32 s40, s56, s54\n"
"ds_read_b64_tr_b16 a[250:251], v236 offset:53248\n"
"v_accvgpr_mov_b32 a97, a49\n"
"v_mfma_f32_16x16x32_bf16 v[152:155], v[220:223], a[12:15], v[152:155]\n"
"buffer_load_dwordx4 v240, s[20:23], s40 offen offset:3072 nt lds\n"
"v_mov_b32_dpp v244, v244 row_ror:8 row_mask:0x4 bank_mask:0xf\n"
"v_accvgpr_mov_b32 a108, a60\n"
"v_accvgpr_mov_b32 a120, a72\n"
"v_accvgpr_mov_b32 a137, a89\n"
"v_accvgpr_mov_b32 a138, a90\n"
"v_xor_b32 v188, s77, v188\n"
"v_xor_b32 v189, s77, v189\n"
"v_xor_b32 v190, s77, v190\n"
"v_mfma_f32_16x16x32_bf16 v[156:159], v[216:219], a[24:27], v[184:187]\n"
"s_add_u32 s55, s55, s59\n"
"v_mov_b32_dpp v244, v244 row_ror:4 row_mask:0x8 bank_mask:0xf\n"
"v_xor_b32 v191, s77, v191\n"
"v_mfma_f32_16x16x32_bf16 v[156:159], v[220:223], a[28:31], v[156:159]\n"
"s_add_u32 s41, s55, s57\n"
"v_accvgpr_mov_b32 a132, a84\n"
"v_xor_b32 v194, s77, v194\n"
"v_mfma_f32_16x16x32_bf16 v[156:159], v[208:211], a[16:19], v[156:159]\n"
"s_and_b32 s41, s41, s58\n"
"ds_read_b64_tr_b16 a[244:245], v232 offset:49152\n"
"v_xor_b32 v195, s77, v195\n"
"v_mfma_f32_16x16x32_bf16 v[156:159], v[212:215], a[20:23], v[156:159]\n"
"s_andn2_b32 s56, s55, s58\n"
"ds_read_b64_tr_b16 v[226:227], v239 offset:54272\n"
"v_mov_b32_dpp v245, v245 row_ror:4 row_mask:0x8 bank_mask:0xf\n"
"v_accvgpr_mov_b32 a98, a50\n"
"v_accvgpr_mov_b32 a114, a66\n"
"v_accvgpr_mov_b32 a107, a59\n"
"v_accvgpr_mov_b32 a139, a91\n"
"v_xor_b32 v192, s77, v192\n"
"v_exp_f32 v154, v154\n"
"v_mfma_f32_16x16x32_bf16 v[160:163], v[208:211], a[32:35], v[188:191]\n"
"s_or_b32 s56, s56, s41\n"
"v_accvgpr_mov_b32 a109, a61\n"
"v_accvgpr_mov_b32 a104, a56\n"
"v_mfma_f32_16x16x32_bf16 v[160:163], v[212:215], a[36:39], v[160:163]\n"
"v_sub_u32 v255, v242, v255\n"
"v_xor_b32 v193, s77, v193\n"
"v_mfma_f32_16x16x32_bf16 v[160:163], v[216:219], a[40:43], v[160:163]\n"
"v_accvgpr_mov_b32 a103, a55\n"
"v_accvgpr_mov_b32 a96, a48\n"
"v_mfma_f32_16x16x32_bf16 v[160:163], v[220:223], a[44:47], v[160:163]\n"
"ds_read_b64_tr_b16 a[254:255], v235 offset:54272\n"
"v_accvgpr_mov_b32 a117, a69\n"
"v_accvgpr_mov_b32 a129, a81\n"
"v_accvgpr_mov_b32 a105, a57\n"
"v_mul_f32_dpp v40, v244, v40 row_newbcast:0 row_mask:0xf bank_mask:0xf\n"
"v_mul_f32_dpp v72, v245, v72 row_newbcast:0 row_mask:0xf bank_mask:0xf\n"
"v_exp_f32 v156, v156\n"
"v_exp_f32 v158, v158\n"
"v_mfma_f32_16x16x32_bf16 v[164:167], v[216:219], a[200:203], v[192:195]\n"
"v_accvgpr_mov_b32 a118, a70\n"
"v_accvgpr_mov_b32 a140, a92\n"
"v_mfma_f32_16x16x32_bf16 v[164:167], v[220:223], a[204:207], v[164:167]\n"
"v_accvgpr_mov_b32 a143, a95\n"
"v_mul_f32_dpp v8, v243, v8 row_newbcast:0 row_mask:0xf bank_mask:0xf\n"
"v_mfma_f32_16x16x32_bf16 v[164:167], v[208:211], a[192:195], v[164:167]\n"
"ds_read_b128 v[208:211], v228 offset:4096\n"
"v_mul_f32_dpp v76, v245, v76 row_newbcast:0 row_mask:0xf bank_mask:0xf\n"
"v_mfma_f32_16x16x32_bf16 v[164:167], v[212:215], a[196:199], v[164:167]\n"
"s_waitcnt lgkmcnt(2)\n"
"ds_read_b128 v[216:219], v228 offset:5120\n"
"v_accvgpr_mov_b32 a101, a53\n"
"v_accvgpr_mov_b32 a128, a80\n"
"v_accvgpr_mov_b32 a123, a75\n"
"v_mul_f32_dpp v44, v244, v44 row_newbcast:0 row_mask:0xf bank_mask:0xf\n"
"v_exp_f32 v163, v163\n"
"s_branch LTC_%=\n"
"LTH2_%=:\n"
"v_mfma_f32_16x16x32_bf16 a[224:227], v[124:127], a[244:247], a[224:227]\n"
"v_exp_f32 v171, v171\n"
"v_mfma_f32_16x16x32_bf16 v[16:19], v[128:131], a[244:247], v[16:19]\n"
"s_waitcnt vmcnt(8)\n"
"v_exp_f32 v168, v168\n"
"v_mfma_f32_16x16x32_bf16 v[16:19], v[132:135], a[248:251], v[16:19]\n"
"s_barrier\n"
"v_mfma_f32_16x16x32_bf16 v[48:51], v[136:139], a[244:247], v[48:51]\n"
"v_exp_f32 v179, v179\n"
"v_mfma_f32_16x16x32_bf16 v[48:51], v[140:143], a[248:251], v[48:51]\n"
"v_exp_f32 v169, v169\n"
"v_mfma_f32_16x16x32_bf16 v[80:83], v[144:147], a[244:247], v[80:83]\n"
"ds_read_b64_tr_b16 a[244:245], v234 offset:49152\n"
"v_cvt_pk_bf16_f32 v102, v168, v169\n"
"v_mfma_f32_16x16x32_bf16 v[80:83], v[148:151], a[248:251], v[80:83]\n"
"ds_read_b64_tr_b16 a[246:247], v234 offset:53248\n"
"ds_read_b64_tr_b16 a[250:251], v238 offset:53248\n"
"v_mfma_f32_16x16x32_bf16 v[84:87], v[148:151], v[224:227], v[84:87]\n"
"v_cvt_pk_bf16_f32 v108, v160, v161\n"
"v_cvt_pk_bf16_f32 v109, v162, v163\n"
"v_mfma_f32_16x16x32_bf16 v[84:87], v[144:147], a[252:255], v[84:87]\n"
"v_cvt_pk_bf16_f32 v117, v166, v167\n"
"v_exp_f32 v172, v172\n"
"v_exp_f32 v173, v173\n"
"v_exp_f32 v176, v176\n"
"v_exp_f32 v177, v177\n"
"v_exp_f32 v178, v178\n"
"v_mfma_f32_16x16x32_bf16 v[52:55], v[140:143], v[224:227], v[52:55]\n"
"ds_read_b64_tr_b16 a[248:249], v238 offset:49152\n"
"v_cvt_pk_bf16_f32 v100, v156, v157\n"
"v_mfma_f32_16x16x32_bf16 v[52:55], v[136:139], a[252:255], v[52:55]\n"
"v_exp_f32 v170, v170\n"
"v_mfma_f32_16x16x32_bf16 v[20:23], v[132:135], v[224:227], v[20:23]\n"
"ds_read_b64_tr_b16 v[226:227], v239 offset:53248\n"
"v_cvt_pk_bf16_f32 v110, v172, v173\n"
"v_mfma_f32_16x16x32_bf16 v[20:23], v[128:131], a[252:255], v[20:23]\n"
"v_exp_f32 v175, v175\n"
"v_mfma_f32_16x16x32_bf16 a[228:231], v[124:127], a[252:255], a[228:231]\n"
"s_waitcnt lgkmcnt(3)\n"
"ds_read_b64_tr_b16 v[224:225], v239 offset:49152\n"
"v_cvt_pk_bf16_f32 v103, v170, v171\n"
"v_mfma_f32_16x16x32_bf16 a[216:219], v[120:123], a[244:247], a[216:219]\n"
"s_add_u32 m0, s44, 81920\n"
"ds_read_b64_tr_b16 a[254:255], v235 offset:53248\n"
"v_cvt_pk_bf16_f32 v118, v176, v177\n"
"v_smfmac_f32_16x16x64_bf16 v[196:199], a[240:243], v[96:103], v241\n"
"buffer_load_dwordx4 v240, s[20:23], s56 offen nt lds\n"
"v_mfma_f32_16x16x32_bf16 v[8:11], v[96:99], a[244:247], v[8:11]\n"
"s_waitcnt lgkmcnt(3)\n"
"v_cvt_pk_bf16_f32 v116, v164, v165\n"
"v_cvt_pk_bf16_f32 v119, v178, v179\n"
"v_mfma_f32_16x16x32_bf16 v[8:11], v[100:103], a[248:251], v[8:11]\n"
"v_exp_f32 v174, v174\n"
"v_smfmac_f32_16x16x64_bf16 v[200:203], a[240:243], v[112:119], v241\n"
"ds_read_b64_tr_b16 a[252:253], v235 offset:49152\n"
"v_cvt_pk_bf16_f32 v111, v174, v175\n"
"v_mfma_f32_16x16x32_bf16 v[40:43], v[104:107], a[244:247], v[40:43]\n"
"ds_read_b128 v[212:215], v229 offset:0\n"
"v_add_f32 v196, v196, v197\n"
"v_mfma_f32_16x16x32_bf16 v[40:43], v[108:111], a[248:251], v[40:43]\n"
"ds_read_b128 v[208:211], v228 offset:0\n"
"v_and_b32 v255, 0x3f0, v242\n"
"v_smfmac_f32_16x16x64_bf16 v[198:201], a[240:243], v[104:111], v241\n"
"ds_read_b128 v[220:223], v229 offset:1024\n"
"v_mfma_f32_16x16x32_bf16 v[72:75], v[112:115], a[244:247], v[72:75]\n"
"ds_read_b64_tr_b16 a[244:245], v234 offset:50176\n"
"v_lshrrev_b32 v255, 1, v255\n"
"v_mfma_f32_16x16x32_bf16 v[72:75], v[116:119], a[248:251], v[72:75]\n"
"ds_read_b64_tr_b16 a[248:249], v238 offset:50176\n"
"v_cvt_f32_u32 v254, s43\n"
"v_mfma_f32_16x16x32_bf16 v[202:205], a[240:243], v[120:123], v[202:205]\n"
"s_waitcnt lgkmcnt(6)\n"
"ds_read_b64_tr_b16 a[246:247], v234 offset:54272\n"
"v_add_f32 v198, v198, v199\n"
"v_mfma_f32_16x16x32_bf16 v[76:79], v[116:119], v[224:227], v[76:79]\n"
"s_waitcnt lgkmcnt(6)\n"
"ds_read_b64_tr_b16 a[250:251], v238 offset:54272\n"
"v_add_f32 v200, v200, v201\n"
"v_mfma_f32_16x16x32_bf16 v[76:79], v[112:115], a[252:255], v[76:79]\n"
"v_add_f32 v249, v196, v198\n"
"v_add_f32 v249, v249, v200\n"
"v_mfma_f32_16x16x32_bf16 v[44:47], v[108:111], v[224:227], v[44:47]\n"
"v_add_f32 v254, v254, v202\n"
"v_min3_f32 v254, v254, v196, v198\n"
"v_mfma_f32_16x16x32_bf16 v[44:47], v[104:107], a[252:255], v[44:47]\n"
"v_min_f32 v254, v254, v200\n"
"v_rcp_f32 v254, v254\n"
"v_mfma_f32_16x16x32_bf16 v[12:15], v[100:103], v[224:227], v[12:15]\n"
"s_add_u32 s40, s56, s52\n"
"v_add_f32 v249, v249, v202\n"
"v_fmac_f32 v249, 0x53800000, v254\n"
"v_mfma_f32_16x16x32_bf16 v[12:15], v[96:99], a[252:255], v[12:15]\n"
"buffer_load_dwordx4 v240, s[20:23], s40 offen offset:1024 nt lds\n"
"v_mfma_f32_16x16x32_bf16 a[220:223], v[120:123], a[252:255], a[220:223]\n"
"s_waitcnt lgkmcnt(1)\n"
"v_cmp_nge_f32 vcc, s45, v249\n"
"s_cbranch_vccnz LSLOW_%=\nLBACK2_%=:\ns_mov_b64 vcc, s[64:65]\n"
"v_mfma_f32_16x16x32_bf16 a[232:235], v[120:123], a[244:247], a[232:235]\n"
"v_accvgpr_mov_b32 a5, a149\n"
"v_accvgpr_mov_b32 a8, a152\n"
"v_accvgpr_mov_b32 a10, a154\n"
"v_accvgpr_mov_b32 a34, a178\n"
"v_accvgpr_mov_b32 a38, a182\n"
"v_accvgpr_mov_b32 a46, a190\n"
"v_xor_b32 v207, 0x400, v248\n"
"v_bfe_u32 v206, v242, 8, 2\n"
"v_lshl_add_u32 v206, v206, 4, s43\n"
"v_mfma_f32_16x16x32_bf16 v[24:27], v[96:99], a[244:247], v[24:27]\n"
"s_waitcnt lgkmcnt(0)\n"
"ds_read_b128 a[204:207], v207 offset:6144\n"
"ds_read_b128 v[132:135], v206 offset:1024\n"
"v_mfma_f32_16x16x32_bf16 v[24:27], v[100:103], a[248:251], v[24:27]\n"
"ds_read_b128 v[124:127], v206 offset:0\n"
"v_accvgpr_mov_b32 a13, a157\n"
"v_accvgpr_mov_b32 a14, a158\n"
"v_accvgpr_mov_b32 a16, a160\n"
"v_accvgpr_mov_b32 a19, a163\n"
"v_accvgpr_mov_b32 a27, a171\n"
"v_mfma_f32_16x16x32_bf16 v[56:59], v[104:107], a[244:247], v[56:59]\n"
"ds_read_b128 a[196:199], v207 offset:4096\n"
"v_xor_b32 v207, 64, v206\n"
"v_mfma_f32_16x16x32_bf16 v[56:59], v[108:111], a[248:251], v[56:59]\n"
"ds_read_b128 v[128:131], v207 offset:0\n"
"v_accvgpr_mov_b32 a2, a146\n"
"v_accvgpr_mov_b32 a3, a147\n"
"v_accvgpr_mov_b32 a9, a153\n"
"v_accvgpr_mov_b32 a22, a166\n"
"v_accvgpr_mov_b32 a30, a174\n"
"v_accvgpr_mov_b32 a35, a179\n"
"v_accvgpr_mov_b32 a37, a181\n"
"v_accvgpr_mov_b32 a44, a188\n"
"v_mfma_f32_16x16x32_bf16 v[88:91], v[112:115], a[244:247], v[88:91]\n"
"ds_read_b128 v[136:139], v207 offset:1024\n"
"v_accvgpr_mov_b32 a0, a144\n"
"v_accvgpr_mov_b32 a1, a145\n"
"v_accvgpr_mov_b32 a7, a151\n"
"v_accvgpr_mov_b32 a20, a164\n"
"v_accvgpr_mov_b32 a23, a167\n"
"v_accvgpr_mov_b32 a31, a175\n"
"v_accvgpr_mov_b32 a45, a189\n"
"v_accvgpr_mov_b32 a47, a191\n"
"v_accvgpr_mov_b32 a164, a68\n"
"v_mfma_f32_16x16x32_bf16 v[88:91], v[116:119], a[248:251], v[88:91]\n"
"s_waitcnt lgkmcnt(3)\n"
"v_rcp_f32 v244, v198\n"
"v_accvgpr_mov_b32 a4, a148\n"
"v_accvgpr_mov_b32 a6, a150\n"
"v_accvgpr_mov_b32 a11, a155\n"
"v_accvgpr_mov_b32 a15, a159\n"
"v_accvgpr_mov_b32 a26, a170\n"
"v_accvgpr_mov_b32 a29, a173\n"
"v_accvgpr_mov_b32 a40, a184\n"
"v_accvgpr_mov_b32 a178, a82\n"
"v_mfma_f32_16x16x32_bf16 v[180:183], v[124:127], a[0:3], 0\n"
"s_waitcnt lgkmcnt(1)\n"
"ds_read_b128 v[216:219], v228 offset:1024\n"
"v_accvgpr_mov_b32 a12, a156\n"
"v_accvgpr_mov_b32 a17, a161\n"
"v_accvgpr_mov_b32 a18, a162\n"
"v_accvgpr_mov_b32 a21, a165\n"
"v_accvgpr_mov_b32 a25, a169\n"
"v_accvgpr_mov_b32 a42, a186\n"
"v_accvgpr_mov_b32 a163, a67\n"
"v_accvgpr_mov_b32 a170, a74\n"
"v_mfma_f32_16x16x32_bf16 v[180:183], v[128:131], a[4:7], v[180:183]\n"
"v_accvgpr_mov_b32 a33, a177\n"
"v_accvgpr_mov_b32 a173, a77\n"
"v_mfma_f32_16x16x32_bf16 v[180:183], v[132:135], a[8:11], v[180:183]\n"
"s_waitcnt lgkmcnt(1)\n"
"ds_read_b128 a[200:203], v248 offset:6144\n"
"v_mov_b32_dpp v244, v244 row_ror:12 row_mask:0x2 bank_mask:0xf\n"
"v_mfma_f32_16x16x32_bf16 v[180:183], v[136:139], a[12:15], v[180:183]\n"
"v_rcp_f32 v243, v196\n"
"v_accvgpr_mov_b32 a28, a172\n"
"v_accvgpr_mov_b32 a32, a176\n"
"v_accvgpr_mov_b32 a36, a180\n"
"v_accvgpr_mov_b32 a39, a183\n"
"v_accvgpr_mov_b32 a167, a71\n"
"v_accvgpr_mov_b32 a158, a62\n"
"v_accvgpr_mov_b32 a169, a73\n"
"v_accvgpr_mov_b32 a184, a88\n"
"v_mfma_f32_16x16x32_bf16 v[184:187], v[124:127], a[16:19], 0\n"
"v_accvgpr_mov_b32 a24, a168\n"
"v_accvgpr_mov_b32 a174, a78\n"
"v_mfma_f32_16x16x32_bf16 v[184:187], v[128:131], a[20:23], v[184:187]\n"
"ds_read_b64_tr_b16 v[224:225], v239 offset:50176\n"
"v_mov_b32_dpp v243, v243 row_ror:12 row_mask:0x2 bank_mask:0xf\n"
"v_mfma_f32_16x16x32_bf16 v[184:187], v[132:135], a[24:27], v[184:187]\n"
"s_add_u32 s40, s56, s53\n"
"ds_read_b128 a[192:195], v248 offset:4096\n"
"v_mov_b32 v252, v192\n"
"v_accvgpr_mov_b32 a43, a187\n"
"v_accvgpr_mov_b32 a181, a85\n"
"v_accvgpr_mov_b32 a159, a63\n"
"v_accvgpr_mov_b32 a147, a51\n"
"v_accvgpr_mov_b32 a179, a83\n"
"v_accvgpr_mov_b32 a154, a58\n"
"v_xor_b32 v182, s77, v182\n"
"v_mfma_f32_16x16x32_bf16 v[184:187], v[136:139], a[28:31], v[184:187]\n"
"buffer_load_dwordx4 v240, s[20:23], s40 offen offset:2048 nt lds\n"
"v_rcp_f32 v245, v200\n"
"v_accvgpr_mov_b32 a41, a185\n"
"v_accvgpr_mov_b32 a182, a86\n"
"v_accvgpr_mov_b32 a190, a94\n"
"v_accvgpr_mov_b32 a160, a64\n"
"v_accvgpr_mov_b32 a161, a65\n"
"v_xor_b32 v180, s77, v180\n"
"v_mfma_f32_16x16x32_bf16 v[188:191], v[124:127], a[32:35], 0\n"
"v_mov_b32_dpp v245, v245 row_ror:12 row_mask:0x2 bank_mask:0xf\n"
"v_xor_b32 v183, s77, v183\n"
"v_mfma_f32_16x16x32_bf16 v[188:191], v[128:131], a[36:39], v[188:191]\n"
"v_mov_b32_dpp v243, v243 row_ror:8 row_mask:0x4 bank_mask:0xf\n"
"v_xor_b32 v254, 0x11000, v242\n"
"v_mfma_f32_16x16x32_bf16 v[188:191], v[132:135], a[40:43], v[188:191]\n"
"v_mov_b64 v[196:197], 0\n"
"v_xor_b32 v186, s77, v186\n"
"v_mfma_f32_16x16x32_bf16 v[188:191], v[136:139], a[44:47], v[188:191]\n"
"s_waitcnt lgkmcnt(0)\n"
"v_accvgpr_mov_b32 a189, a93\n"
"v_xor_b32 v181, s77, v181\n"
"v_mfma_f32_16x16x32_bf16 v[192:195], v[124:127], a[192:195], 0\n"
"ds_read_b64_tr_b16 a[246:247], v232 offset:53248\n"
"v_xor_b32 v184, s77, v184\n"
"v_mfma_f32_16x16x32_bf16 v[192:195], v[128:131], a[196:199], v[192:195]\n"
"v_mov_b32_dpp v243, v243 row_ror:4 row_mask:0x8 bank_mask:0xf\n"
"v_accvgpr_mov_b32 a172, a76\n"
"v_mfma_f32_16x16x32_bf16 v[192:195], v[132:135], a[200:203], v[192:195]\n"
"v_xor_b32 v185, s77, v185\n"
"v_xor_b32 v187, s77, v187\n"
"v_mfma_f32_16x16x32_bf16 v[192:195], v[136:139], a[204:207], v[192:195]\n"
"ds_read_b64_tr_b16 a[252:253], v235 offset:50176\n"
"v_mov_b32_dpp v245, v245 row_ror:8 row_mask:0x4 bank_mask:0xf\n"
"v_mfma_f32_16x16x32_bf16 v[152:155], v[208:211], a[0:3], v[180:183]\n"
"v_accvgpr_mov_b32 a183, a87\n"
"v_accvgpr_mov_b32 a175, a79\n"
"v_mfma_f32_16x16x32_bf16 v[152:155], v[212:215], a[4:7], v[152:155]\n"
"v_accvgpr_mov_b32 a148, a52\n"
"v_accvgpr_mov_b32 a150, a54\n"
"v_mfma_f32_16x16x32_bf16 v[152:155], v[216:219], a[8:11], v[152:155]\n"
"s_add_u32 s40, s56, s54\n"
"ds_read_b64_tr_b16 a[250:251], v236 offset:53248\n"
"v_accvgpr_mov_b32 a145, a49\n"
"v_mfma_f32_16x16x32_bf16 v[152:155], v[220:223], a[12:15], v[152:155]\n"
"buffer_load_dwordx4 v240, s[20:23], s40 offen offset:3072 nt lds\n"
"v_mov_b32_dpp v244, v244 row_ror:8 row_mask:0x4 bank_mask:0xf\n"
"v_accvgpr_mov_b32 a156, a60\n"
"v_accvgpr_mov_b32 a168, a72\n"
"v_accvgpr_mov_b32 a185, a89\n"
"v_accvgpr_mov_b32 a186, a90\n"
"v_xor_b32 v188, s77, v188\n"
"v_xor_b32 v189, s77, v189\n"
"v_xor_b32 v190, s77, v190\n"
"v_mfma_f32_16x16x32_bf16 v[156:159], v[216:219], a[24:27], v[184:187]\n"
"s_add_u32 s55, s55, s59\n"
"v_mov_b32_dpp v244, v244 row_ror:4 row_mask:0x8 bank_mask:0xf\n"
"v_xor_b32 v191, s77, v191\n"
"v_mfma_f32_16x16x32_bf16 v[156:159], v[220:223], a[28:31], v[156:159]\n"
"s_add_u32 s41, s55, s57\n"
"v_accvgpr_mov_b32 a180, a84\n"
"v_xor_b32 v194, s77, v194\n"
"v_mfma_f32_16x16x32_bf16 v[156:159], v[208:211], a[16:19], v[156:159]\n"
"s_and_b32 s41, s41, s58\n"
"ds_read_b64_tr_b16 a[244:245], v232 offset:49152\n"
"v_xor_b32 v195, s77, v195\n"
"v_mfma_f32_16x16x32_bf16 v[156:159], v[212:215], a[20:23], v[156:159]\n"
"s_andn2_b32 s56, s55, s58\n"
"ds_read_b64_tr_b16 v[226:227], v239 offset:54272\n"
"v_mov_b32_dpp v245, v245 row_ror:4 row_mask:0x8 bank_mask:0xf\n"
"v_accvgpr_mov_b32 a146, a50\n"
"v_accvgpr_mov_b32 a162, a66\n"
"v_accvgpr_mov_b32 a155, a59\n"
"v_accvgpr_mov_b32 a187, a91\n"
"v_xor_b32 v192, s77, v192\n"
"v_exp_f32 v154, v154\n"
"v_mfma_f32_16x16x32_bf16 v[160:163], v[208:211], a[32:35], v[188:191]\n"
"s_or_b32 s56, s56, s41\n"
"v_accvgpr_mov_b32 a157, a61\n"
"v_accvgpr_mov_b32 a152, a56\n"
"v_mfma_f32_16x16x32_bf16 v[160:163], v[212:215], a[36:39], v[160:163]\n"
"v_sub_u32 v255, v242, v255\n"
"v_xor_b32 v193, s77, v193\n"
"v_mfma_f32_16x16x32_bf16 v[160:163], v[216:219], a[40:43], v[160:163]\n"
"v_accvgpr_mov_b32 a151, a55\n"
"v_accvgpr_mov_b32 a144, a48\n"
"v_mfma_f32_16x16x32_bf16 v[160:163], v[220:223], a[44:47], v[160:163]\n"
"ds_read_b64_tr_b16 a[254:255], v235 offset:54272\n"
"v_accvgpr_mov_b32 a165, a69\n"
"v_accvgpr_mov_b32 a177, a81\n"
"v_accvgpr_mov_b32 a153, a57\n"
"v_mul_f32_dpp v40, v244, v40 row_newbcast:0 row_mask:0xf bank_mask:0xf\n"
"v_mul_f32_dpp v72, v245, v72 row_newbcast:0 row_mask:0xf bank_mask:0xf\n"
"v_exp_f32 v156, v156\n"
"v_exp_f32 v158, v158\n"
"v_mfma_f32_16x16x32_bf16 v[164:167], v[216:219], a[200:203], v[192:195]\n"
"v_accvgpr_mov_b32 a166, a70\n"
"v_accvgpr_mov_b32 a188, a92\n"
"v_mfma_f32_16x16x32_bf16 v[164:167], v[220:223], a[204:207], v[164:167]\n"
"v_accvgpr_mov_b32 a191, a95\n"
"v_mul_f32_dpp v8, v243, v8 row_newbcast:0 row_mask:0xf bank_mask:0xf\n"
"v_mfma_f32_16x16x32_bf16 v[164:167], v[208:211], a[192:195], v[164:167]\n"
"ds_read_b128 v[208:211], v228 offset:4096\n"
"v_mul_f32_dpp v76, v245, v76 row_newbcast:0 row_mask:0xf bank_mask:0xf\n"
"v_mfma_f32_16x16x32_bf16 v[164:167], v[212:215], a[196:199], v[164:167]\n"
"s_waitcnt lgkmcnt(2)\n"
"ds_read_b128 v[216:219], v228 offset:5120\n"
"v_accvgpr_mov_b32 a149, a53\n"
"v_accvgpr_mov_b32 a176, a80\n"
"v_accvgpr_mov_b32 a171, a75\n"
"v_mul_f32_dpp v44, v244, v44 row_newbcast:0 row_mask:0xf bank_mask:0xf\n"
"v_exp_f32 v163, v163\n"
"LTC_%=:\n"
"s_waitcnt lgkmcnt(3)\n"
"v_mfma_f32_16x16x32_bf16 v[92:95], v[116:119], v[224:227], v[92:95]\n"
"s_waitcnt lgkmcnt(2)\n"
"ds_read_b128 v[220:223], v229 offset:5120\n"
"v_mul_f32_dpp v46, v244, v46 row_newbcast:2 row_mask:0xf bank_mask:0xf\n"
"v_mfma_f32_16x16x32_bf16 v[92:95], v[112:115], a[252:255], v[92:95]\n"
"ds_read_b128 v[212:215], v229 offset:4096\n"
"v_exp_f32 v152, v152\n"
"v_exp_f32 v162, v162\n"
"v_bfe_u32 v207, v242, 8, 2\n"
"v_mfma_f32_16x16x32_bf16 v[60:63], v[108:111], v[224:227], v[60:63]\n"
"s_add_u32 m0, s44, 32768\n"
"v_xor_b32 v250, 0x1000, v255\n"
"v_mul_f32_dpp v9, v243, v9 row_newbcast:1 row_mask:0xf bank_mask:0xf\n"
"v_mfma_f32_16x16x32_bf16 v[60:63], v[104:107], a[252:255], v[60:63]\n"
"buffer_load_dwordx4 v240, s[16:19], s56 offen nt lds\n"
"v_mfma_f32_16x16x32_bf16 v[28:31], v[100:103], v[224:227], v[28:31]\n"
"v_exp_f32 v166, v166\n"
"v_mfma_f32_16x16x32_bf16 v[28:31], v[96:99], a[252:255], v[28:31]\n"
"v_mul_f32_dpp v11, v243, v11 row_newbcast:3 row_mask:0xf bank_mask:0xf\n"
"v_mul_f32_dpp v56, v244, v56 row_newbcast:0 row_mask:0xf bank_mask:0xf\n"
"v_mfma_f32_16x16x32_bf16 a[236:239], v[120:123], a[252:255], a[236:239]\n"
"s_waitcnt lgkmcnt(2)\n"
"ds_write_b128 v254, a[216:219] offset:32768\n"
"v_mul_f32_dpp v14, v243, v14 row_newbcast:2 row_mask:0xf bank_mask:0xf\n"
"v_cvt_pk_bf16_f32 v8, v8, v9\n"
"v_exp_f32 v155, v155\n"
"v_mul_f32_dpp v59, v244, v59 row_newbcast:3 row_mask:0xf bank_mask:0xf\n"
"v_mfma_f32_16x16x32_bf16 v[168:171], v[216:219], a[8:11], v[180:183]\n"
"s_waitcnt lgkmcnt(2)\n"
"v_exp_f32 v160, v160\n"
"v_mfma_f32_16x16x32_bf16 v[168:171], v[220:223], a[12:15], v[168:171]\n"
"v_exp_f32 v153, v153\n"
"v_mfma_f32_16x16x32_bf16 v[168:171], v[208:211], a[0:3], v[168:171]\n"
"s_waitcnt lgkmcnt(1)\n"
"ds_read_b64_tr_b16 v[224:225], v237 offset:49152\n"
"v_mul_f32_dpp v58, v244, v58 row_newbcast:2 row_mask:0xf bank_mask:0xf\n"
"v_mfma_f32_16x16x32_bf16 v[168:171], v[212:215], a[4:7], v[168:171]\n"
"v_mul_f32_dpp v13, v243, v13 row_newbcast:1 row_mask:0xf bank_mask:0xf\n"
"v_mul_f32_dpp v41, v244, v41 row_newbcast:1 row_mask:0xf bank_mask:0xf\n"
"v_mfma_f32_16x16x32_bf16 v[172:175], v[208:211], a[16:19], v[184:187]\n"
"v_mul_f32_dpp v63, v244, v63 row_newbcast:3 row_mask:0xf bank_mask:0xf\n"
"v_mul_f32_dpp v94, v245, v94 row_newbcast:2 row_mask:0xf bank_mask:0xf\n"
"v_mfma_f32_16x16x32_bf16 v[172:175], v[212:215], a[20:23], v[172:175]\n"
"ds_read_b64_tr_b16 a[254:255], v233 offset:53248\n"
"v_mov_b32 v253, v202\n"
"v_mfma_f32_16x16x32_bf16 v[172:175], v[216:219], a[24:27], v[172:175]\n"
"v_cvt_pk_bf16_f32 v40, v40, v41\n"
"v_mul_f32_dpp v90, v245, v90 row_newbcast:2 row_mask:0xf bank_mask:0xf\n"
"v_mfma_f32_16x16x32_bf16 v[172:175], v[220:223], a[28:31], v[172:175]\n"
"ds_write_b64 v250, v[252:253] offset:52224\n"
"v_cvt_pk_bf16_f32 v128, v152, v153\n"
"v_cvt_pk_bf16_f32 v129, v154, v155\n"
"v_exp_f32 v159, v159\n"
"v_exp_f32 v165, v165\n"
"v_mul_f32_dpp v24, v243, v24 row_newbcast:0 row_mask:0xf bank_mask:0xf\n"
"v_mfma_f32_16x16x32_bf16 v[176:179], v[216:219], a[40:43], v[188:191]\n"
"v_mul_f32_dpp v10, v243, v10 row_newbcast:2 row_mask:0xf bank_mask:0xf\n"
"v_mul_f32_dpp v73, v245, v73 row_newbcast:1 row_mask:0xf bank_mask:0xf\n"
"v_mfma_f32_16x16x32_bf16 v[176:179], v[220:223], a[44:47], v[176:179]\n"
"v_mul_f32_dpp v43, v244, v43 row_newbcast:3 row_mask:0xf bank_mask:0xf\n"
"v_mul_f32_dpp v75, v245, v75 row_newbcast:3 row_mask:0xf bank_mask:0xf\n"
"v_mfma_f32_16x16x32_bf16 v[176:179], v[208:211], a[32:35], v[176:179]\n"
"s_add_u32 s40, s56, s52\n"
"ds_read_b64_tr_b16 a[248:249], v236 offset:49152\n"
"v_mul_f32_dpp v74, v245, v74 row_newbcast:2 row_mask:0xf bank_mask:0xf\n"
"v_mfma_f32_16x16x32_bf16 v[176:179], v[212:215], a[36:39], v[176:179]\n"
"buffer_load_dwordx4 v240, s[16:19], s40 offen offset:1024 nt lds\n"
"v_mul_f32_dpp v12, v243, v12 row_newbcast:0 row_mask:0xf bank_mask:0xf\n"
"v_exp_f32 v161, v161\n"
"v_mul_f32_dpp v25, v243, v25 row_newbcast:1 row_mask:0xf bank_mask:0xf\n"
"v_mul_f32_dpp v26, v243, v26 row_newbcast:2 row_mask:0xf bank_mask:0xf\n"
"v_mul_f32_dpp v88, v245, v88 row_newbcast:0 row_mask:0xf bank_mask:0xf\n"
"v_exp_f32 v172, v172\n"
"v_mfma_f32_16x16x32_bf16 v[152:155], v[208:211], a[192:195], v[192:195]\n"
"v_exp_f32 v171, v171\n"
"v_mfma_f32_16x16x32_bf16 v[152:155], v[212:215], a[196:199], v[152:155]\n"
"v_cvt_pk_bf16_f32 v9, v10, v11\n"
"v_mul_f32_dpp v42, v244, v42 row_newbcast:2 row_mask:0xf bank_mask:0xf\n"
"v_mfma_f32_16x16x32_bf16 v[152:155], v[216:219], a[200:203], v[152:155]\n"
"ds_read_b128 v[208:211], v230 offset:0\n"
"v_cvt_pk_bf16_f32 v144, v160, v161\n"
"v_mfma_f32_16x16x32_bf16 v[152:155], v[220:223], a[204:207], v[152:155]\n"
"ds_read_b128 v[216:219], v230 offset:1024\n"
"v_cvt_pk_bf16_f32 v41, v42, v43\n"
"v_cvt_pk_bf16_f32 v145, v162, v163\n"
"v_mul_f32_dpp v30, v243, v30 row_newbcast:2 row_mask:0xf bank_mask:0xf\n"
"v_exp_f32 v170, v170\n"
"v_exp_f32 v174, v174\n"
"v_mfma_f32_16x16x32_bf16 a[208:211], v[120:123], a[244:247], a[208:211]\n"
"v_mul_f32_dpp v77, v245, v77 row_newbcast:1 row_mask:0xf bank_mask:0xf\n"
"v_mul_f32_dpp v29, v243, v29 row_newbcast:1 row_mask:0xf bank_mask:0xf\n"
"v_mfma_f32_16x16x32_bf16 v[0:3], v[96:99], a[244:247], v[0:3]\n"
"s_waitcnt lgkmcnt(2)\n"
"ds_read_b128 v[220:223], v231 offset:1024\n"
"v_cvt_pk_bf16_f32 v72, v72, v73\n"
"v_mfma_f32_16x16x32_bf16 v[0:3], v[100:103], a[248:251], v[0:3]\n"
"ds_read_b128 v[212:215], v231 offset:0\n"
"v_mul_f32_dpp v45, v244, v45 row_newbcast:1 row_mask:0xf bank_mask:0xf\n"
"v_mul_f32_dpp v78, v245, v78 row_newbcast:2 row_mask:0xf bank_mask:0xf\n"
"v_exp_f32 v164, v164\n"
"v_mul_f32_dpp v93, v245, v93 row_newbcast:1 row_mask:0xf bank_mask:0xf\n"
"v_exp_f32 v176, v176\n"
"v_exp_f32 v153, v153\n"
"v_mfma_f32_16x16x32_bf16 v[32:35], v[104:107], a[244:247], v[32:35]\n"
"v_cvt_pk_bf16_f32 v137, v158, v159\n"
"v_accvgpr_read_b32 v206, a209\n"
"v_mfma_f32_16x16x32_bf16 v[32:35], v[108:111], a[248:251], v[32:35]\n"
"ds_read_b64_tr_b16 a[252:253], v233 offset:49152\n"
"v_mul_f32_dpp v79, v245, v79 row_newbcast:3 row_mask:0xf bank_mask:0xf\n"
"v_exp_f32 v157, v157\n"
"v_exp_f32 v167, v167\n"
"v_mul_f32_dpp v62, v244, v62 row_newbcast:2 row_mask:0xf bank_mask:0xf\n"
"v_mul_f32_dpp v92, v245, v92 row_newbcast:0 row_mask:0xf bank_mask:0xf\n"
"v_exp_f32 v169, v169\n"
"v_mfma_f32_16x16x32_bf16 v[64:67], v[112:115], a[244:247], v[64:67]\n"
"ds_read_b64_tr_b16 v[226:227], v237 offset:53248\n"
"v_cvt_pk_bf16_f32 v136, v156, v157\n"
"v_mfma_f32_16x16x32_bf16 v[64:67], v[116:119], a[248:251], v[64:67]\n"
"s_waitcnt lgkmcnt(5)\n"
"ds_write_b128 v254, a[236:239] offset:35840\n"
"v_mul_f32_dpp v57, v244, v57 row_newbcast:1 row_mask:0xf bank_mask:0xf\n"
"v_cvt_pk_bf16_f32 v56, v56, v57\n"
"v_exp_f32 v168, v168\n"
"v_cvt_pk_bf16_f32 v131, v170, v171\n"
"v_mfma_f32_16x16x32_bf16 v[156:159], v[208:211], a[0:3], v[180:183]\n"
"s_waitcnt lgkmcnt(3)\n"
"v_mul_f32_dpp v15, v243, v15 row_newbcast:3 row_mask:0xf bank_mask:0xf\n"
"v_cvt_pk_bf16_f32 v10, v12, v13\n"
"v_mfma_f32_16x16x32_bf16 v[156:159], v[212:215], a[4:7], v[156:159]\n"
"v_mul_f32_dpp v28, v243, v28 row_newbcast:0 row_mask:0xf bank_mask:0xf\n"
"v_mul_f32_dpp v3, v243, v3 row_newbcast:3 row_mask:0xf bank_mask:0xf\n"
"v_mfma_f32_16x16x32_bf16 v[156:159], v[216:219], a[8:11], v[156:159]\n"
"s_add_u32 s40, s56, s53\n"
"ds_read_b64_tr_b16 a[244:245], v232 offset:50176\n"
"v_mul_f32_dpp v89, v245, v89 row_newbcast:1 row_mask:0xf bank_mask:0xf\n"
"v_mfma_f32_16x16x32_bf16 v[156:159], v[220:223], a[12:15], v[156:159]\n"
"buffer_load_dwordx4 v240, s[16:19], s40 offen offset:2048 nt lds\n"
"v_cvt_pk_bf16_f32 v73, v74, v75\n"
"v_cvt_pk_bf16_f32 v124, v164, v165\n"
"v_cvt_pk_bf16_f32 v24, v24, v25\n"
"v_exp_f32 v173, v173\n"
"v_exp_f32 v175, v175\n"
"v_mul_f32_dpp v33, v244, v33 row_newbcast:1 row_mask:0xf bank_mask:0xf\n"
"v_mfma_f32_16x16x32_bf16 v[160:163], v[216:219], a[24:27], v[184:187]\n"
"v_cvt_pk_bf16_f32 v11, v14, v15\n"
"v_cvt_pk_bf16_f32 v57, v58, v59\n"
"v_mfma_f32_16x16x32_bf16 v[160:163], v[220:223], a[28:31], v[160:163]\n"
"v_cvt_pk_bf16_f32 v130, v168, v169\n"
"v_mul_f32_dpp v0, v243, v0 row_newbcast:0 row_mask:0xf bank_mask:0xf\n"
"v_mfma_f32_16x16x32_bf16 v[160:163], v[208:211], a[16:19], v[160:163]\n"
"v_cvt_pk_bf16_f32 v125, v166, v167\n"
"v_mul_f32_dpp v27, v243, v27 row_newbcast:3 row_mask:0xf bank_mask:0xf\n"
"v_mfma_f32_16x16x32_bf16 v[160:163], v[212:215], a[20:23], v[160:163]\n"
"ds_write_b128 v254, a[220:223] offset:33792\n"
"v_mul_f32_dpp v47, v244, v47 row_newbcast:3 row_mask:0xf bank_mask:0xf\n"
"v_cvt_pk_bf16_f32 v42, v44, v45\n"
"v_exp_f32 v155, v155\n"
"v_mul_f32_dpp v34, v244, v34 row_newbcast:2 row_mask:0xf bank_mask:0xf\n"
"v_mfma_f32_16x16x32_bf16 v[164:167], v[208:211], a[32:35], v[188:191]\n"
"ds_read_b64_tr_b16 a[246:247], v232 offset:54272\n"
"v_mul_f32_dpp v35, v244, v35 row_newbcast:3 row_mask:0xf bank_mask:0xf\n"
"v_mfma_f32_16x16x32_bf16 v[164:167], v[212:215], a[36:39], v[164:167]\n"
"ds_read_b128 v[208:211], v230 offset:4096\n"
"v_mul_f32_dpp v60, v244, v60 row_newbcast:0 row_mask:0xf bank_mask:0xf\n"
"v_mfma_f32_16x16x32_bf16 v[164:167], v[216:219], a[40:43], v[164:167]\n"
"v_mul_f32_dpp v91, v245, v91 row_newbcast:3 row_mask:0xf bank_mask:0xf\n"
"v_mul_f32_dpp v64, v245, v64 row_newbcast:0 row_mask:0xf bank_mask:0xf\n"
"v_mfma_f32_16x16x32_bf16 v[164:167], v[220:223], a[44:47], v[164:167]\n"
"s_waitcnt lgkmcnt(3)\n"
"ds_write_b128 v254, a[232:235] offset:34816\n"
"v_cvt_pk_bf16_f32 v75, v78, v79\n"
"v_cvt_pk_bf16_f32 v88, v88, v89\n"
"v_exp_f32 v158, v158\n"
"v_mfma_f32_16x16x32_bf16 v[68:71], v[116:119], v[224:227], v[68:71]\n"
"ds_read_b128 v[220:223], v231 offset:5120\n"
"v_cvt_pk_bf16_f32 v74, v76, v77\n"
"v_mfma_f32_16x16x32_bf16 v[68:71], v[112:115], a[252:255], v[68:71]\n"
"ds_read_b128 v[212:215], v231 offset:4096\n"
"v_mul_f32_dpp v32, v244, v32 row_newbcast:0 row_mask:0xf bank_mask:0xf\n"
"v_mfma_f32_16x16x32_bf16 v[36:39], v[108:111], v[224:227], v[36:39]\n"
"s_add_u32 s40, s56, s54\n"
"ds_read_b128 v[216:219], v230 offset:5120\n"
"v_mul_f32_dpp v61, v244, v61 row_newbcast:1 row_mask:0xf bank_mask:0xf\n"
"v_mfma_f32_16x16x32_bf16 v[36:39], v[104:107], a[252:255], v[36:39]\n"
"buffer_load_dwordx4 v240, s[16:19], s40 offen offset:3072 nt lds\n"
"v_cvt_pk_bf16_f32 v43, v46, v47\n"
"v_mul_f32_dpp v31, v243, v31 row_newbcast:3 row_mask:0xf bank_mask:0xf\n"
"v_cvt_pk_bf16_f32 v59, v62, v63\n"
"v_cvt_pk_bf16_f32 v138, v172, v173\n"
"v_exp_f32 v177, v177\n"
"v_exp_f32 v152, v152\n"
"v_mfma_f32_16x16x32_bf16 v[4:7], v[100:103], v[224:227], v[4:7]\n"
"v_cvt_pk_bf16_f32 v25, v26, v27\n"
"v_mul_f32_dpp v1, v243, v1 row_newbcast:1 row_mask:0xf bank_mask:0xf\n"
"v_mfma_f32_16x16x32_bf16 v[4:7], v[96:99], a[252:255], v[4:7]\n"
"ds_write_b128 v246, v[8:11] offset:49152\n"
"v_cvt_pk_bf16_f32 v146, v176, v177\n"
"v_exp_f32 v179, v179\n"
"v_exp_f32 v159, v159\n"
"v_mfma_f32_16x16x32_bf16 a[212:215], v[120:123], a[252:255], a[212:215]\n"
"s_waitcnt lgkmcnt(5)\n"
"ds_write_b128 v246, v[40:43] offset:50176\n"
"v_cvt_pk_bf16_f32 v26, v28, v29\n"
"v_exp_f32 v178, v178\n"
"v_mul_f32_dpp v2, v243, v2 row_newbcast:2 row_mask:0xf bank_mask:0xf\n"
"v_lshlrev_b32 v207, 6, v207\n"
"v_mfma_f32_16x16x32_bf16 v[168:171], v[208:211], a[0:3], v[180:183]\n"
"s_waitcnt lgkmcnt(3)\n"
"ds_read_b64_tr_b16 a[70:71], v247 offset:50184\n"
"ds_read_b64_tr_b16 a[250:251], v236 offset:54272\n"
"v_mfma_f32_16x16x32_bf16 v[168:171], v[212:215], a[4:7], v[168:171]\n"
"s_waitcnt lgkmcnt(4)\n"
"v_cvt_pk_bf16_f32 v89, v90, v91\n"
"v_mul_f32_dpp v67, v245, v67 row_newbcast:3 row_mask:0xf bank_mask:0xf\n"
"v_mfma_f32_16x16x32_bf16 v[168:171], v[216:219], a[8:11], v[168:171]\n"
"ds_read_b64_tr_b16 a[68:69], v247 offset:50176\n"
"ds_read_b64_tr_b16 a[248:249], v236 offset:50176\n"
"v_mfma_f32_16x16x32_bf16 v[168:171], v[220:223], a[12:15], v[168:171]\n"
"ds_write_b128 v246, v[72:75] offset:51200\n"
"v_cvt_pk_bf16_f32 v58, v60, v61\n"
"v_cvt_pk_bf16_f32 v139, v174, v175\n"
"v_cvt_pk_bf16_f32 v0, v0, v1\n"
"v_cvt_pk_bf16_f32 v32, v32, v33\n"
"v_mfma_f32_16x16x32_bf16 v[172:175], v[216:219], a[24:27], v[184:187]\n"
"v_exp_f32 v157, v157\n"
"v_mfma_f32_16x16x32_bf16 v[172:175], v[220:223], a[28:31], v[172:175]\n"
"v_cvt_pk_bf16_f32 v27, v30, v31\n"
"v_cvt_pk_bf16_f32 v1, v2, v3\n"
"v_mfma_f32_16x16x32_bf16 v[172:175], v[208:211], a[16:19], v[172:175]\n"
"ds_read_b64_tr_b16 a[86:87], v247 offset:51208\n"
"ds_read_b64_tr_b16 a[252:253], v233 offset:50176\n"
"v_mfma_f32_16x16x32_bf16 v[172:175], v[212:215], a[20:23], v[172:175]\n"
"s_cbranch_vccnz LPCT0_%=\nLPKT0_%=:\n"
"ds_read_b64_tr_b16 v[224:225], v237 offset:50176\n"
"ds_read_b64_tr_b16 v[226:227], v237 offset:54272\n"
"v_cvt_pk_bf16_f32 v147, v178, v179\n"
"v_exp_f32 v154, v154\n"
"v_exp_f32 v162, v162\n"
"v_mul_f32_dpp v39, v244, v39 row_newbcast:3 row_mask:0xf bank_mask:0xf\n"
"v_mfma_f32_16x16x32_bf16 v[176:179], v[208:211], a[32:35], v[188:191]\n"
"ds_read_b64_tr_b16 a[254:255], v233 offset:54272\n"
"v_xor_b32 v254, 0x1000, v254\n"
"v_mfma_f32_16x16x32_bf16 v[176:179], v[212:215], a[36:39], v[176:179]\n"
"ds_read_b64_tr_b16 a[52:53], v247 offset:49152\n"
"v_mul_f32_dpp v38, v244, v38 row_newbcast:2 row_mask:0xf bank_mask:0xf\n"
"v_mfma_f32_16x16x32_bf16 v[176:179], v[216:219], a[40:43], v[176:179]\n"
"ds_write_b128 v246, v[56:59] offset:50176\n"
"v_exp_f32 v166, v166\n"
"v_mul_f32_dpp v68, v245, v68 row_newbcast:0 row_mask:0xf bank_mask:0xf\n"
"v_exp_f32 v172, v172\n"
"v_mfma_f32_16x16x32_bf16 v[176:179], v[220:223], a[44:47], v[176:179]\n"
"ds_read_b64_tr_b16 a[54:55], v247 offset:49160\n"
"ds_read_b64_tr_b16 a[78:79], v247 offset:50184\n"
"v_mfma_f32_16x16x32_bf16 a[224:227], v[120:123], a[244:247], a[224:227]\n"
"ds_write_b128 v246, v[24:27] offset:49152\n"
"v_cvt_pk_bf16_f32 v90, v92, v93\n"
"v_cvt_pk_bf16_f32 v126, v152, v153\n"
"v_exp_f32 v156, v156\n"
"v_mul_f32_dpp v71, v245, v71 row_newbcast:3 row_mask:0xf bank_mask:0xf\n"
"v_mfma_f32_16x16x32_bf16 v[16:19], v[96:99], a[244:247], v[16:19]\n"
"s_waitcnt vmcnt(8) lgkmcnt(5)\n"
"v_cvt_pk_bf16_f32 v33, v34, v35\n"
"v_mul_f32_dpp v65, v245, v65 row_newbcast:1 row_mask:0xf bank_mask:0xf\n"
"v_mfma_f32_16x16x32_bf16 v[16:19], v[100:103], a[248:251], v[16:19]\n"
"s_barrier\n"
"ds_read_b128 v[208:211], v228 offset:16384\n"
"v_exp_f32 v164, v164\n"
"v_cvt_pk_bf16_f32 v35, v38, v39\n"
"v_exp_f32 v170, v170\n"
"v_exp_f32 v177, v177\n"
"v_mfma_f32_16x16x32_bf16 v[48:51], v[104:107], a[244:247], v[48:51]\n"
"ds_read_b128 v[24:27], v254 offset:32768\n"
"v_cvt_pk_bf16_f32 v127, v154, v155\n"
"v_mfma_f32_16x16x32_bf16 v[48:51], v[108:111], a[248:251], v[48:51]\n"
"ds_read_b64_tr_b16 a[76:77], v247 offset:50176\n"
"ds_read_b64 v[250:251], v255 offset:52224\n"
"v_mul_f32_dpp v95, v245, v95 row_newbcast:3 row_mask:0xf bank_mask:0xf\n"
"v_cvt_pk_bf16_f32 v133, v158, v159\n"
"v_mul_f32_dpp v7, v243, v7 row_newbcast:3 row_mask:0xf bank_mask:0xf\n"
"v_mul_f32_dpp v36, v244, v36 row_newbcast:0 row_mask:0xf bank_mask:0xf\n"
"v_mul_f32_dpp v70, v245, v70 row_newbcast:2 row_mask:0xf bank_mask:0xf\n"
"v_xor_b32 v207, v207, v248\n"
"v_mfma_f32_16x16x32_bf16 v[80:83], v[112:115], a[244:247], v[80:83]\n"
"ds_read_b128 v[220:223], v229 offset:17408\n"
"v_mul_f32_dpp v6, v243, v6 row_newbcast:2 row_mask:0xf bank_mask:0xf\n"
"v_mfma_f32_16x16x32_bf16 v[80:83], v[116:119], a[248:251], v[80:83]\n"
"ds_read_b128 v[212:215], v229 offset:16384\n"
"v_exp_f32 v161, v161\n"
"v_mul_f32_dpp v5, v243, v5 row_newbcast:1 row_mask:0xf bank_mask:0xf\n"
"v_mul_f32_dpp v37, v244, v37 row_newbcast:1 row_mask:0xf bank_mask:0xf\n"
"v_mul_f32_dpp v66, v245, v66 row_newbcast:2 row_mask:0xf bank_mask:0xf\n"
"v_mul_f32_dpp v69, v245, v69 row_newbcast:1 row_mask:0xf bank_mask:0xf\n"
"v_cvt_pk_bf16_f32 v64, v64, v65\n"
"v_cvt_pk_bf16_f32 v65, v66, v67\n"
"v_mfma_f32_16x16x32_bf16 v[84:87], v[116:119], v[224:227], v[84:87]\n"
"ds_read_b64_tr_b16 a[84:85], v247 offset:51200\n"
"v_mul_f32_dpp v49, v244, v49 row_newbcast:1 row_mask:0xf bank_mask:0xf\n"
"v_mfma_f32_16x16x32_bf16 v[84:87], v[112:115], a[252:255], v[84:87]\n"
"ds_read_b128 v[28:31], v254 offset:33792\n"
"v_cvt_pk_bf16_f32 v91, v94, v95\n"
"v_mfma_f32_16x16x32_bf16 v[52:55], v[108:111], v[224:227], v[52:55]\n"
"ds_read_b128 v[60:63], v254 offset:35840\n"
"v_cvt_pk_bf16_f32 v132, v156, v157\n"
"v_mfma_f32_16x16x32_bf16 v[52:55], v[104:107], a[252:255], v[52:55]\n"
"s_waitcnt lgkmcnt(5)\n"
"ds_write_b128 v246, v[88:91] offset:51200\n"
"v_mul_f32_dpp v4, v243, v4 row_newbcast:0 row_mask:0xf bank_mask:0xf\n"
"v_cvt_pk_bf16_f32 v66, v68, v69\n"
"v_exp_f32 v173, v173\n"
"v_min_f32 v249, v252, v250\n"
"v_mfma_f32_16x16x32_bf16 v[20:23], v[100:103], v[224:227], v[20:23]\n"
"ds_read_b128 v[216:219], v228 offset:17408\n"
"v_cvt_pk_bf16_f32 v3, v6, v7\n"
"v_mfma_f32_16x16x32_bf16 v[20:23], v[96:99], a[252:255], v[20:23]\n"
"ds_read_b64_tr_b16 a[60:61], v247 offset:49152\n"
"ds_read_b64_tr_b16 a[92:93], v247 offset:51200\n"
"v_exp_f32 v160, v160\n"
"v_exp_f32 v165, v165\n"
"v_cvt_pk_bf16_f32 v2, v4, v5\n"
"v_sub_f32 v250, v249, v250\n"
"v_mul_f32_dpp v18, v243, v18 row_newbcast:2 row_mask:0xf bank_mask:0xf\n"
"v_mfma_f32_16x16x32_bf16 a[228:231], v[120:123], a[252:255], a[228:231]\n"
"ds_read_b64_tr_b16 a[94:95], v247 offset:51208\n"
"ds_read_b64_tr_b16 a[246:247], v234 offset:4096\n"
"v_exp_f32 v163, v163\n"
"v_cvt_pk_bf16_f32 v34, v36, v37\n"
"v_exp_f32 v168, v168\n"
"v_exp_f32 v250, v250\n"
"v_mul_f32_dpp v85, v245, v85 row_newbcast:1 row_mask:0xf bank_mask:0xf\n"
"v_mfma_f32_16x16x32_bf16 v[152:155], v[208:211], a[0:3], v[180:183]\n"
"s_waitcnt lgkmcnt(9)\n"
"ds_read_b64_tr_b16 a[62:63], v247 offset:49160\n"
"v_mul_f32_dpp v80, v245, v80 row_newbcast:0 row_mask:0xf bank_mask:0xf\n"
"v_mfma_f32_16x16x32_bf16 v[152:155], v[212:215], a[4:7], v[152:155]\n"
"s_waitcnt lgkmcnt(5)\n"
"ds_write_b128 v246, v[32:35] offset:50176\n"
"v_cvt_pk_bf16_f32 v141, v162, v163\n"
"v_exp_f32 v167, v167\n"
"v_cvt_pk_bf16_f32 v67, v70, v71\n"
"v_sub_f32 v252, v249, v252\n"
"v_mfma_f32_16x16x32_bf16 v[152:155], v[216:219], a[8:11], v[152:155]\n"
"ds_write_b128 v246, v[0:3] offset:49152\n"
"v_cvt_pk_bf16_f32 v148, v164, v165\n"
"v_exp_f32 v171, v171\n"
"v_accvgpr_read_b32 v249, a208\n"
"v_mul_f32_dpp v16, v243, v16 row_newbcast:0 row_mask:0xf bank_mask:0xf\n"
"v_mfma_f32_16x16x32_bf16 v[152:155], v[220:223], a[12:15], v[152:155]\n"
"ds_write_b128 v246, v[64:67] offset:51200\n"
"v_cvt_pk_bf16_f32 v140, v160, v161\n"
"v_cvt_pk_bf16_f32 v149, v166, v167\n"
"v_exp_f32 v252, v252\n"
"v_mul_f32_dpp v48, v244, v48 row_newbcast:0 row_mask:0xf bank_mask:0xf\n"
"v_mfma_f32_16x16x32_bf16 v[156:159], v[216:219], a[24:27], v[184:187]\n"
"ds_read_b128 v[56:59], v254 offset:34816\n"
"v_mul_f32 v253, v253, v252\n"
"v_mfma_f32_16x16x32_bf16 v[156:159], v[220:223], a[28:31], v[156:159]\n"
"ds_read_b64_tr_b16 a[48:49], v247 offset:49152\n"
"ds_read_b64_tr_b16 a[82:83], v247 offset:51208\n"
"v_mfma_f32_16x16x32_bf16 v[156:159], v[208:211], a[16:19], v[156:159]\n"
"ds_read_b64_tr_b16 a[64:65], v247 offset:50176\n"
"v_fmac_f32 v253, v251, v250\n"
"v_mfma_f32_16x16x32_bf16 v[156:159], v[212:215], a[20:23], v[156:159]\n"
"ds_read_b64_tr_b16 a[50:51], v247 offset:49160\n"
"ds_read_b64_tr_b16 a[66:67], v247 offset:50184\n"
"ds_read_b64_tr_b16 a[80:81], v247 offset:51200\n"
"v_exp_f32 v169, v169\n"
"v_exp_f32 v174, v174\n"
"v_rcp_f32 v253, v253\n"
"v_mfma_f32_16x16x32_bf16 v[160:163], v[208:211], a[32:35], v[188:191]\n"
"v_mul_f32 v252, v252, v253\n"
"v_mul_f32_dpp v83, v245, v83 row_newbcast:3 row_mask:0xf bank_mask:0xf\n"
"v_mfma_f32_16x16x32_bf16 v[160:163], v[212:215], a[36:39], v[160:163]\n"
"ds_read_b64_tr_b16 a[244:245], v234 offset:0\n"
"ds_read_b64_tr_b16 a[250:251], v238 offset:4096\n"
"v_mfma_f32_16x16x32_bf16 v[160:163], v[216:219], a[40:43], v[160:163]\n"
"v_exp_f32 v178, v178\n"
"v_mfma_f32_16x16x32_bf16 v[160:163], v[220:223], a[44:47], v[160:163]\n"
"ds_read_b64_tr_b16 a[248:249], v238 offset:0\n"
"ds_read_b64_tr_b16 a[254:255], v235 offset:4096\n"
"v_mov_b64 v[198:199], 0\n"
"v_cvt_pk_bf16_f32 v134, v168, v169\n"
"v_exp_f32 v175, v175\n"
"v_exp_f32 v176, v176\n"
"v_mul_f32 v250, v250, v253\n"
"v_mov_b32_dpp v252, v252 row_ror:12 row_mask:0x2 bank_mask:0xf\n"
"v_mfma_f32_16x16x32_bf16 v[164:167], v[216:219], a[200:203], v[192:195]\n"
"v_cvt_pk_bf16_f32 v142, v172, v173\n"
"v_mul_f32_dpp v23, v243, v23 row_newbcast:3 row_mask:0xf bank_mask:0xf\n"
"v_mfma_f32_16x16x32_bf16 v[164:167], v[220:223], a[204:207], v[164:167]\n"
"ds_read_b64_tr_b16 v[224:225], v239 offset:0\n"
"ds_read_b64_tr_b16 v[226:227], v239 offset:4096\n"
"v_mfma_f32_16x16x32_bf16 v[164:167], v[208:211], a[192:195], v[164:167]\n"
"v_cvt_pk_bf16_f32 v150, v176, v177\n"
"v_mul_f32_dpp v82, v245, v82 row_newbcast:2 row_mask:0xf bank_mask:0xf\n"
"v_mfma_f32_16x16x32_bf16 v[164:167], v[212:215], a[196:199], v[164:167]\n"
"s_waitcnt lgkmcnt(5)\n"
"ds_read_b128 v[220:223], v229 offset:21504\n"
"v_mov_b64 v[200:201], 0\n"
"v_cvt_pk_bf16_f32 v135, v170, v171\n"
"v_cvt_pk_bf16_f32 v143, v174, v175\n"
"v_exp_f32 v179, v179\n"
"v_mov_b32_dpp v250, v250 row_ror:12 row_mask:0x2 bank_mask:0xf\n"
"v_xor_b32 v246, s75, v246\n"
"v_mul_f32_dpp v21, v243, v21 row_newbcast:1 row_mask:0xf bank_mask:0xf\n"
"v_mfma_f32_16x16x32_bf16 a[216:219], v[124:127], a[244:247], 0\n"
"v_mov_b32_dpp v252, v252 row_ror:8 row_mask:0x4 bank_mask:0xf\n"
"v_mul_f32_dpp v84, v245, v84 row_newbcast:0 row_mask:0xf bank_mask:0xf\n"
"v_mfma_f32_16x16x32_bf16 v[8:11], v[128:131], a[244:247], 0\n"
"s_waitcnt lgkmcnt(4)\n"
"s_add_u32 m0, s44, 98304\n"
"ds_read_b64_tr_b16 a[252:253], v235 offset:0\n"
"v_mov_b64 v[202:203], 0\n"
"v_mfma_f32_16x16x32_bf16 v[8:11], v[132:135], a[248:251], v[8:11]\n"
"buffer_load_dwordx4 v240, s[20:23], s56 offen nt lds\n"
"v_cvt_pk_bf16_f32 v151, v178, v179\n"
"v_mov_b32_dpp v250, v250 row_ror:8 row_mask:0x4 bank_mask:0xf\n"
"v_mov_b32_dpp v252, v252 row_ror:4 row_mask:0x8 bank_mask:0xf\n"
"v_accvgpr_read_b32 v251, a211\n"
"v_mul_f32_dpp v87, v245, v87 row_newbcast:3 row_mask:0xf bank_mask:0xf\n"
"v_exp_f32 v163, v163\n"
"v_mfma_f32_16x16x32_bf16 v[40:43], v[136:139], a[244:247], 0\n"
"v_mov_b32_dpp v250, v250 row_ror:4 row_mask:0x8 bank_mask:0xf\n"
"v_add_u32 v248, 0x1000, v248\n"
"v_mfma_f32_16x16x32_bf16 v[40:43], v[140:143], a[248:251], v[40:43]\n"
"v_mul_f32_dpp v25, v250, v25 row_newbcast:1 row_mask:0xf bank_mask:0xf\n"
"v_accvgpr_read_b32 v253, a210\n"
"v_mul_f32_dpp v26, v250, v26 row_newbcast:2 row_mask:0xf bank_mask:0xf\n"
"v_mul_f32_dpp v28, v250, v28 row_newbcast:0 row_mask:0xf bank_mask:0xf\n"
"v_mul_f32_dpp v81, v245, v81 row_newbcast:1 row_mask:0xf bank_mask:0xf\n"
"v_mul_f32_dpp v62, v250, v62 row_newbcast:2 row_mask:0xf bank_mask:0xf\n"
"v_exp_f32 v153, v153\n"
"v_exp_f32 v155, v155\n"
"v_mfma_f32_16x16x32_bf16 v[72:75], v[144:147], a[244:247], 0\n"
"v_exp_f32 v160, v160\n"
"v_mfma_f32_16x16x32_bf16 v[72:75], v[148:151], a[248:251], v[72:75]\n"
"s_waitcnt lgkmcnt(2)\n"
"ds_read_b128 v[208:211], v228 offset:20480\n"
"v_mul_f32_dpp v24, v250, v24 row_newbcast:0 row_mask:0xf bank_mask:0xf\n"
"v_mfma_f32_16x16x32_bf16 v[76:79], v[148:151], v[224:227], 0\n"
"s_waitcnt lgkmcnt(1)\n"
"v_exp_f32 v154, v154\n"
"v_mfma_f32_16x16x32_bf16 v[76:79], v[144:147], a[252:255], v[76:79]\n"
"ds_read_b128 v[212:215], v229 offset:20480\n"
"v_mul_f32_dpp v27, v250, v27 row_newbcast:3 row_mask:0xf bank_mask:0xf\n"
"v_fmac_f32_dpp v24, v252, v249 row_newbcast:0 row_mask:0xf bank_mask:0xf\n"
"v_fmac_f32_dpp v25, v252, v206 row_newbcast:1 row_mask:0xf bank_mask:0xf\n"
"v_accvgpr_read_b32 v249, a212\n"
"v_cvt_pk_bf16_f32 v24, v24, v25\n"
"v_mul_f32_dpp v51, v244, v51 row_newbcast:3 row_mask:0xf bank_mask:0xf\n"
"v_exp_f32 v162, v162\n"
"v_mfma_f32_16x16x32_bf16 v[44:47], v[140:143], v[224:227], 0\n"
"v_mul_f32_dpp v22, v243, v22 row_newbcast:2 row_mask:0xf bank_mask:0xf\n"
"v_mul_f32_dpp v56, v250, v56 row_newbcast:0 row_mask:0xf bank_mask:0xf\n"
"v_mfma_f32_16x16x32_bf16 v[44:47], v[136:139], a[252:255], v[44:47]\n"
"ds_read_b128 v[216:219], v228 offset:21504\n"
"v_mul_f32_dpp v29, v250, v29 row_newbcast:1 row_mask:0xf bank_mask:0xf\n"
"v_mfma_f32_16x16x32_bf16 v[12:15], v[132:135], v[224:227], 0\n"
"s_add_u32 s40, s56, s52\n"
"v_fmac_f32_dpp v27, v252, v251 row_newbcast:3 row_mask:0xf bank_mask:0xf\n"
"v_mul_f32_dpp v50, v244, v50 row_newbcast:2 row_mask:0xf bank_mask:0xf\n"
"v_mfma_f32_16x16x32_bf16 v[12:15], v[128:131], a[252:255], v[12:15]\n"
"buffer_load_dwordx4 v240, s[20:23], s40 offen offset:1024 nt lds\n"
"v_accvgpr_read_b32 v206, a213\n"
"v_mul_f32_dpp v30, v250, v30 row_newbcast:2 row_mask:0xf bank_mask:0xf\n"
"v_accvgpr_read_b32 v251, a215\n"
"v_fmac_f32_dpp v28, v252, v249 row_newbcast:0 row_mask:0xf bank_mask:0xf\n"
"v_mul_f32_dpp v19, v243, v19 row_newbcast:3 row_mask:0xf bank_mask:0xf\n"
"v_mul_f32_dpp v57, v250, v57 row_newbcast:1 row_mask:0xf bank_mask:0xf\n"
"v_mul_f32_dpp v61, v250, v61 row_newbcast:1 row_mask:0xf bank_mask:0xf\n"
"v_mfma_f32_16x16x32_bf16 a[220:223], v[124:127], a[252:255], 0\n"
"v_fmac_f32_dpp v26, v252, v253 row_newbcast:2 row_mask:0xf bank_mask:0xf\n"
"v_accvgpr_read_b32 v253, a214\n"
"v_mul_f32_dpp v31, v250, v31 row_newbcast:3 row_mask:0xf bank_mask:0xf\n"
"v_fmac_f32_dpp v29, v252, v206 row_newbcast:1 row_mask:0xf bank_mask:0xf\n"
"v_cvt_pk_bf16_f32 v25, v26, v27\n"
"v_mul_f32_dpp v55, v244, v55 row_newbcast:3 row_mask:0xf bank_mask:0xf\n"
"v_accvgpr_read_b32 v249, a224\n"
"v_mul_f32_dpp v63, v250, v63 row_newbcast:3 row_mask:0xf bank_mask:0xf\n"
"v_exp_f32 v165, v165\n"
"v_smfmac_f32_16x16x64_bf16 v[196:199], a[240:243], v[128:135], v241\n"
"v_accvgpr_read_b32 v206, a225\n"
"v_mul_f32_dpp v59, v250, v59 row_newbcast:3 row_mask:0xf bank_mask:0xf\n"
"v_mfma_f32_16x16x32_bf16 v[202:205], a[240:243], v[124:127], v[202:205]\n"
"s_waitcnt lgkmcnt(0)\n"
"ds_read_b64_tr_b16 a[248:249], v238 offset:1024\n"
"ds_read_b64_tr_b16 v[224:225], v239 offset:1024\n"
"v_fmac_f32_dpp v30, v252, v253 row_newbcast:2 row_mask:0xf bank_mask:0xf\n"
"v_exp_f32 v152, v152\n"
"v_cvt_pk_bf16_f32 v97, v154, v155\n"
"v_exp_f32 v158, v158\n"
"v_exp_f32 v166, v166\n"
"v_mfma_f32_16x16x32_bf16 v[168:171], v[216:219], a[8:11], v[180:183]\n"
"v_fmac_f32_dpp v31, v252, v251 row_newbcast:3 row_mask:0xf bank_mask:0xf\n"
"v_mul_f32_dpp v60, v250, v60 row_newbcast:0 row_mask:0xf bank_mask:0xf\n"
"v_mfma_f32_16x16x32_bf16 v[168:171], v[220:223], a[12:15], v[168:171]\n"
"v_mul_f32_dpp v58, v250, v58 row_newbcast:2 row_mask:0xf bank_mask:0xf\n"
"v_cvt_pk_bf16_f32 v96, v152, v153\n"
"v_mfma_f32_16x16x32_bf16 v[168:171], v[208:211], a[0:3], v[168:171]\n"
"v_cvt_pk_bf16_f32 v27, v30, v31\n"
"v_fmac_f32_dpp v56, v252, v249 row_newbcast:0 row_mask:0xf bank_mask:0xf\n"
"v_mfma_f32_16x16x32_bf16 v[168:171], v[212:215], a[4:7], v[168:171]\n"
"ds_read_b64_tr_b16 v[226:227], v239 offset:5120\n"
"v_cvt_pk_bf16_f32 v48, v48, v49\n"
"v_accvgpr_read_b32 v251, a227\n"
"v_accvgpr_read_b32 v249, a228\n"
"v_exp_f32 v156, v156\n"
"v_exp_f32 v161, v161\n"
"v_mfma_f32_16x16x32_bf16 v[172:175], v[208:211], a[16:19], v[184:187]\n"
"ds_read_b64_tr_b16 a[244:245], v234 offset:1024\n"
"v_xor_b32 v247, s75, v247\n"
"v_mfma_f32_16x16x32_bf16 v[172:175], v[212:215], a[20:23], v[172:175]\n"
"ds_read_b64_tr_b16 a[246:247], v234 offset:5120\n"
"v_cvt_pk_bf16_f32 v26, v28, v29\n"
"v_mfma_f32_16x16x32_bf16 v[172:175], v[216:219], a[24:27], v[172:175]\n"
"s_add_u32 s40, s56, s53\n"
"ds_read_b64_tr_b16 a[250:251], v238 offset:5120\n"
"v_fmac_f32_dpp v60, v252, v249 row_newbcast:0 row_mask:0xf bank_mask:0xf\n"
"v_mfma_f32_16x16x32_bf16 v[172:175], v[220:223], a[28:31], v[172:175]\n"
"buffer_load_dwordx4 v240, s[20:23], s40 offen offset:2048 nt lds\n"
"v_accvgpr_read_b32 v253, a226\n"
"v_fmac_f32_dpp v57, v252, v206 row_newbcast:1 row_mask:0xf bank_mask:0xf\n"
"v_fmac_f32_dpp v59, v252, v251 row_newbcast:3 row_mask:0xf bank_mask:0xf\n"
"v_accvgpr_read_b32 v206, a229\n"
"v_accvgpr_read_b32 v251, a231\n"
"v_cvt_pk_bf16_f32 v56, v56, v57\n"
"v_exp_f32 v171, v171\n"
"v_mfma_f32_16x16x32_bf16 v[176:179], v[216:219], a[40:43], v[188:191]\n"
"v_mul_f32_dpp v17, v243, v17 row_newbcast:1 row_mask:0xf bank_mask:0xf\n"
"v_fmac_f32_dpp v63, v252, v251 row_newbcast:3 row_mask:0xf bank_mask:0xf\n"
"v_mfma_f32_16x16x32_bf16 v[176:179], v[220:223], a[44:47], v[176:179]\n"
"v_fmac_f32_dpp v58, v252, v253 row_newbcast:2 row_mask:0xf bank_mask:0xf\n"
"v_accvgpr_read_b32 v253, a230\n"
"v_mfma_f32_16x16x32_bf16 v[176:179], v[208:211], a[32:35], v[176:179]\n"
"ds_read_b64_tr_b16 a[254:255], v235 offset:5120\n"
"v_fmac_f32_dpp v62, v252, v253 row_newbcast:2 row_mask:0xf bank_mask:0xf\n"
"v_mfma_f32_16x16x32_bf16 v[176:179], v[212:215], a[36:39], v[176:179]\n"
"ds_write_b128 v207, v[24:27] offset:0\n"
"v_mul_f32_dpp v53, v244, v53 row_newbcast:1 row_mask:0xf bank_mask:0xf\n"
"v_cvt_pk_bf16_f32 v57, v58, v59\n"
"v_cvt_pk_bf16_f32 v59, v62, v63\n"
"v_exp_f32 v157, v157\n"
"v_mfma_f32_16x16x32_bf16 v[152:155], v[208:211], a[192:195], v[192:195]\n"
"v_fmac_f32_dpp v61, v252, v206 row_newbcast:1 row_mask:0xf bank_mask:0xf\n"
"v_cvt_pk_bf16_f32 v58, v60, v61\n"
"v_mfma_f32_16x16x32_bf16 v[152:155], v[212:215], a[196:199], v[152:155]\n"
"ds_write_b128 v207, v[56:59] offset:2048\n"
"v_cvt_pk_bf16_f32 v49, v50, v51\n"
"v_cvt_pk_bf16_f32 v80, v80, v81\n"
"v_exp_f32 v164, v164\n"
"v_mfma_f32_16x16x32_bf16 v[152:155], v[216:219], a[200:203], v[152:155]\n"
"ds_read_b64_tr_b16 a[252:253], v235 offset:1024\n"
"v_cvt_pk_bf16_f32 v81, v82, v83\n"
"v_mfma_f32_16x16x32_bf16 v[152:155], v[220:223], a[204:207], v[152:155]\n"
"s_waitcnt lgkmcnt(4)\n"
"ds_read_b128 v[208:211], v230 offset:16384\n"
"v_mul_f32_dpp v20, v243, v20 row_newbcast:0 row_mask:0xf bank_mask:0xf\n"
"v_mfma_f32_16x16x32_bf16 a[232:235], v[124:127], a[244:247], 0\n"
"v_exp_f32 v170, v170\n"
"v_mfma_f32_16x16x32_bf16 v[24:27], v[128:131], a[244:247], 0\n"
"s_add_u32 s40, s56, s54\n"
"v_exp_f32 v159, v159\n"
"v_mfma_f32_16x16x32_bf16 v[24:27], v[132:135], a[248:251], v[24:27]\n"
"buffer_load_dwordx4 v240, s[20:23], s40 offen offset:3072 nt lds\n"
"v_cvt_pk_bf16_f32 v16, v16, v17\n"
"v_cvt_pk_bf16_f32 v113, v162, v163\n"
"v_exp_f32 v168, v168\n"
"v_exp_f32 v174, v174\n"
"v_exp_f32 v175, v175\n"
"v_mfma_f32_16x16x32_bf16 v[56:59], v[136:139], a[244:247], 0\n"
"s_add_u32 s55, s55, s59\n"
"v_exp_f32 v173, v173\n"
"v_mfma_f32_16x16x32_bf16 v[56:59], v[140:143], a[248:251], v[56:59]\n"
"s_add_u32 s41, s55, s57\n"
"v_exp_f32 v167, v167\n"
"v_mfma_f32_16x16x32_bf16 v[88:91], v[144:147], a[244:247], 0\n"
"s_and_b32 s41, s41, s58\n"
"ds_read_b128 v[216:219], v230 offset:17408\n"
"v_cvt_pk_bf16_f32 v104, v156, v157\n"
"v_mfma_f32_16x16x32_bf16 v[88:91], v[148:151], a[248:251], v[88:91]\n"
"s_andn2_b32 s56, s55, s58\n"
"ds_read_b128 v[220:223], v231 offset:17408\n"
"v_cvt_pk_bf16_f32 v105, v158, v159\n"
"v_mfma_f32_16x16x32_bf16 v[92:95], v[148:151], v[224:227], 0\n"
"s_waitcnt lgkmcnt(2)\n"
"s_or_b32 s56, s56, s41\n"
"v_cvt_pk_bf16_f32 v82, v84, v85\n"
"v_cvt_pk_bf16_f32 v120, v164, v165\n"
"v_mfma_f32_16x16x32_bf16 v[92:95], v[144:147], a[252:255], v[92:95]\n"
"ds_read_b128 v[212:215], v231 offset:16384\n"
"v_mul_f32_dpp v52, v244, v52 row_newbcast:0 row_mask:0xf bank_mask:0xf\n"
"v_mfma_f32_16x16x32_bf16 v[60:63], v[140:143], v[224:227], 0\n"
"s_add_u32 m0, s44, 49152\n"
"v_cvt_pk_bf16_f32 v112, v160, v161\n"
"v_cvt_pk_bf16_f32 v121, v166, v167\n"
"v_mfma_f32_16x16x32_bf16 v[60:63], v[136:139], a[252:255], v[60:63]\n"
"buffer_load_dwordx4 v240, s[16:19], s56 offen nt lds\n"
"v_mfma_f32_16x16x32_bf16 v[28:31], v[132:135], v[224:227], 0\n"
"v_exp_f32 v179, v179\n"
"v_mfma_f32_16x16x32_bf16 v[28:31], v[128:131], a[252:255], v[28:31]\n"
"v_exp_f32 v172, v172\n"
"v_mfma_f32_16x16x32_bf16 a[236:239], v[124:127], a[252:255], 0\n"
"ds_read_b64_tr_b16 a[250:251], v236 offset:4096\n"
"ds_read_b64_tr_b16 v[226:227], v237 offset:4096\n"
"v_smfmac_f32_16x16x64_bf16 v[198:201], a[240:243], v[136:143], v241\n"
"ds_read_b64_tr_b16 a[254:255], v233 offset:4096\n"
"v_mul_f32_dpp v54, v244, v54 row_newbcast:2 row_mask:0xf bank_mask:0xf\n"
"v_mfma_f32_16x16x32_bf16 v[156:159], v[208:211], a[0:3], v[180:183]\n"
"s_waitcnt lgkmcnt(3)\n"
"v_exp_f32 v176, v176\n"
"v_mfma_f32_16x16x32_bf16 v[156:159], v[212:215], a[4:7], v[156:159]\n"
"v_exp_f32 v178, v178\n"
"v_mfma_f32_16x16x32_bf16 v[156:159], v[216:219], a[8:11], v[156:159]\n"
"ds_read_b64_tr_b16 a[244:245], v232 offset:0\n"
"ds_read_b64_tr_b16 a[246:247], v232 offset:4096\n"
"v_mfma_f32_16x16x32_bf16 v[156:159], v[220:223], a[12:15], v[156:159]\n"
"v_exp_f32 v169, v169\n"
"v_mfma_f32_16x16x32_bf16 v[160:163], v[216:219], a[24:27], v[184:187]\n"
"ds_read_b64_tr_b16 a[248:249], v236 offset:0\n"
"v_cvt_pk_bf16_f32 v51, v54, v55\n"
"v_mfma_f32_16x16x32_bf16 v[160:163], v[220:223], a[28:31], v[160:163]\n"
"v_cvt_pk_bf16_f32 v17, v18, v19\n"
"v_cvt_pk_bf16_f32 v18, v20, v21\n"
"v_mfma_f32_16x16x32_bf16 v[160:163], v[208:211], a[16:19], v[160:163]\n"
"s_add_u32 s40, s56, s52\n"
"ds_read_b64_tr_b16 v[224:225], v237 offset:0\n"
"v_mul_f32_dpp v86, v245, v86 row_newbcast:2 row_mask:0xf bank_mask:0xf\n"
"v_mfma_f32_16x16x32_bf16 v[160:163], v[212:215], a[20:23], v[160:163]\n"
"buffer_load_dwordx4 v240, s[16:19], s40 offen offset:1024 nt lds\n"
"v_mfma_f32_16x16x32_bf16 v[164:167], v[208:211], a[32:35], v[188:191]\n"
"v_exp_f32 v156, v156\n"
"v_mfma_f32_16x16x32_bf16 v[164:167], v[212:215], a[36:39], v[164:167]\n"
"v_exp_f32 v158, v158\n"
"v_mfma_f32_16x16x32_bf16 v[164:167], v[216:219], a[40:43], v[164:167]\n"
"ds_read_b64_tr_b16 a[252:253], v233 offset:0\n"
"ds_read_b128 v[208:211], v230 offset:20480\n"
"v_mfma_f32_16x16x32_bf16 v[164:167], v[220:223], a[44:47], v[164:167]\n"
"s_waitcnt lgkmcnt(4)\n"
"v_cvt_pk_bf16_f32 v83, v86, v87\n"
"v_cvt_pk_bf16_f32 v107, v174, v175\n"
"v_mfma_f32_16x16x32_bf16 a[208:211], v[124:127], a[244:247], 0\n"
"ds_read_b128 v[220:223], v231 offset:21504\n"
"v_cvt_pk_bf16_f32 v98, v168, v169\n"
"v_mfma_f32_16x16x32_bf16 v[0:3], v[128:131], a[244:247], 0\n"
"s_waitcnt lgkmcnt(4)\n"
"v_exp_f32 v177, v177\n"
"v_mfma_f32_16x16x32_bf16 v[0:3], v[132:135], a[248:251], v[0:3]\n"
"ds_read_b128 v[212:215], v231 offset:20480\n"
"v_cvt_pk_bf16_f32 v99, v170, v171\n"
"v_mfma_f32_16x16x32_bf16 v[32:35], v[136:139], a[244:247], 0\n"
"s_add_u32 s40, s56, s53\n"
"v_cvt_pk_bf16_f32 v19, v22, v23\n"
"v_cvt_pk_bf16_f32 v50, v52, v53\n"
"v_mfma_f32_16x16x32_bf16 v[32:35], v[140:143], a[248:251], v[32:35]\n"
"buffer_load_dwordx4 v240, s[16:19], s40 offen offset:2048 nt lds\n"
"v_mfma_f32_16x16x32_bf16 v[64:67], v[144:147], a[244:247], 0\n"
"v_exp_f32 v163, v163\n"
"v_mfma_f32_16x16x32_bf16 v[64:67], v[148:151], a[248:251], v[64:67]\n"
"s_waitcnt lgkmcnt(2)\n"
"ds_write_b128 v246, v[48:51] offset:50176\n"
"v_cvt_pk_bf16_f32 v106, v172, v173\n"
"v_exp_f32 v153, v153\n"
"v_exp_f32 v164, v164\n"
"v_mfma_f32_16x16x32_bf16 v[68:71], v[148:151], v[224:227], 0\n"
"v_exp_f32 v162, v162\n"
"v_mfma_f32_16x16x32_bf16 v[68:71], v[144:147], a[252:255], v[68:71]\n"
"ds_write_b128 v246, v[16:19] offset:49152\n"
"v_exp_f32 v152, v152\n"
"v_exp_f32 v154, v154\n"
"v_mfma_f32_16x16x32_bf16 v[36:39], v[140:143], v[224:227], 0\n"
"ds_read_b64_tr_b16 a[56:57], v247 offset:49152\n"
"ds_read_b64_tr_b16 a[58:59], v247 offset:49160\n"
"v_mfma_f32_16x16x32_bf16 v[36:39], v[136:139], a[252:255], v[36:39]\n"
"ds_read_b128 v[216:219], v230 offset:21504\n"
"ds_read_b64_tr_b16 a[244:245], v232 offset:1024\n"
"v_mfma_f32_16x16x32_bf16 v[4:7], v[132:135], v[224:227], 0\n"
"ds_read_b64_tr_b16 a[72:73], v247 offset:50176\n"
"ds_read_b64_tr_b16 a[246:247], v232 offset:5120\n"
"v_mfma_f32_16x16x32_bf16 v[4:7], v[128:131], a[252:255], v[4:7]\n"
"ds_read_b64_tr_b16 a[74:75], v247 offset:50184\n"
"ds_read_b64_tr_b16 a[250:251], v236 offset:5120\n"
"v_mfma_f32_16x16x32_bf16 a[212:215], v[124:127], a[252:255], 0\n"
"s_add_u32 s40, s56, s54\n"
"ds_write_b128 v246, v[80:83] offset:51200\n"
"v_exp_f32 v160, v160\n"
"v_smfmac_f32_16x16x64_bf16 v[200:203], a[240:243], v[144:151], v241\n"
"buffer_load_dwordx4 v240, s[16:19], s40 offen offset:3072 nt lds\n"
"v_mfma_f32_16x16x32_bf16 v[168:171], v[208:211], a[0:3], v[180:183]\n"
"s_waitcnt lgkmcnt(10)\n"
"v_exp_f32 v161, v161\n"
"v_mfma_f32_16x16x32_bf16 v[168:171], v[212:215], a[4:7], v[168:171]\n"
"s_waitcnt lgkmcnt(6)\n"
"ds_read_b64_tr_b16 a[88:89], v247 offset:51200\n"
"ds_read_b64_tr_b16 a[254:255], v233 offset:5120\n"
"v_mfma_f32_16x16x32_bf16 v[168:171], v[216:219], a[8:11], v[168:171]\n"
"ds_read_b64_tr_b16 a[90:91], v247 offset:51208\n"
"v_cvt_pk_bf16_f32 v114, v176, v177\n"
"v_mfma_f32_16x16x32_bf16 v[168:171], v[220:223], a[12:15], v[168:171]\n"
"v_exp_f32 v165, v165\n"
"v_mfma_f32_16x16x32_bf16 v[172:175], v[216:219], a[24:27], v[184:187]\n"
"ds_read_b64_tr_b16 a[248:249], v236 offset:1024\n"
"v_cvt_pk_bf16_f32 v115, v178, v179\n"
"v_mfma_f32_16x16x32_bf16 v[172:175], v[220:223], a[28:31], v[172:175]\n"
"v_exp_f32 v155, v155\n"
"v_mfma_f32_16x16x32_bf16 v[172:175], v[208:211], a[16:19], v[172:175]\n"
"v_exp_f32 v166, v166\n"
"v_mfma_f32_16x16x32_bf16 v[172:175], v[212:215], a[20:23], v[172:175]\n"
"s_cbranch_vccnz LPCT1_%=\nLPKT1_%=:\n"
"v_xor_b32 v246, s75, v246\n"
"v_mfma_f32_16x16x32_bf16 v[176:179], v[208:211], a[32:35], v[188:191]\n"
"ds_read_b64_tr_b16 a[252:253], v233 offset:1024\n"
"v_cvt_pk_bf16_f32 v109, v162, v163\n"
"v_mfma_f32_16x16x32_bf16 v[176:179], v[212:215], a[36:39], v[176:179]\n"
"v_exp_f32 v157, v157\n"
"v_mfma_f32_16x16x32_bf16 v[176:179], v[216:219], a[40:43], v[176:179]\n"
"ds_read_b64_tr_b16 v[224:225], v237 offset:1024\n"
"v_cvt_pk_bf16_f32 v100, v156, v157\n"
"v_mfma_f32_16x16x32_bf16 v[176:179], v[220:223], a[44:47], v[176:179]\n"
"s_waitcnt lgkmcnt(3)\n"
"ds_read_b64_tr_b16 v[226:227], v237 offset:5120\n"
"v_mfma_f32_16x16x32_bf16 a[224:227], v[124:127], a[244:247], 0\n"
"v_cvt_pk_bf16_f32 v122, v152, v153\n"
"v_cvt_pk_bf16_f32 v108, v160, v161\n"
"v_mfma_f32_16x16x32_bf16 v[16:19], v[128:131], a[244:247], 0\n"
"s_waitcnt vmcnt(8) lgkmcnt(3)\n"
"v_xor_b32 v247, s75, v247\n"
"v_cvt_pk_bf16_f32 v123, v154, v155\n"
"v_mfma_f32_16x16x32_bf16 v[16:19], v[132:135], a[248:251], v[16:19]\n"
"s_barrier\n"
"ds_read_b128 v[208:211], v228 offset:32768\n"
"ds_read_b128 v[216:219], v228 offset:33792\n"
"v_exp_f32 v159, v159\n"
"v_mfma_f32_16x16x32_bf16 v[48:51], v[136:139], a[244:247], 0\n"
"v_exp_f32 v167, v167\n"
"v_mfma_f32_16x16x32_bf16 v[48:51], v[140:143], a[248:251], v[48:51]\n"
"ds_read_b128 v[212:215], v229 offset:32768\n"
"ds_read_b128 v[220:223], v229 offset:33792\n"
"v_mfma_f32_16x16x32_bf16 v[80:83], v[144:147], a[244:247], 0\n"
"ds_read_b64_tr_b16 a[244:245], v234 offset:16384\n"
"ds_read_b64_tr_b16 a[246:247], v234 offset:20480\n"
"v_mfma_f32_16x16x32_bf16 v[80:83], v[148:151], a[248:251], v[80:83]\n"
"s_waitcnt lgkmcnt(6)\n"
"ds_read_b64_tr_b16 a[248:249], v238 offset:16384\n"
"ds_read_b64_tr_b16 a[250:251], v238 offset:20480\n"
"v_mfma_f32_16x16x32_bf16 v[84:87], v[148:151], v[224:227], 0\n"
"v_exp_f32 v175, v175\n"
"v_mfma_f32_16x16x32_bf16 v[84:87], v[144:147], a[252:255], v[84:87]\n"
"v_exp_f32 v173, v173\n"
"v_mfma_f32_16x16x32_bf16 v[52:55], v[140:143], v[224:227], 0\n"
"v_exp_f32 v169, v169\n"
"v_mfma_f32_16x16x32_bf16 v[52:55], v[136:139], a[252:255], v[52:55]\n"
"v_cvt_pk_bf16_f32 v101, v158, v159\n"
"v_cvt_pk_bf16_f32 v116, v164, v165\n"
"v_mfma_f32_16x16x32_bf16 v[20:23], v[132:135], v[224:227], 0\n"
"ds_read_b64_tr_b16 v[224:225], v239 offset:16384\n"
"v_cvt_pk_bf16_f32 v117, v166, v167\n"
"v_mfma_f32_16x16x32_bf16 v[20:23], v[128:131], a[252:255], v[20:23]\n"
"v_exp_f32 v172, v172\n"
"v_mfma_f32_16x16x32_bf16 a[228:231], v[124:127], a[252:255], 0\n"
"s_waitcnt lgkmcnt(3)\n"
"ds_read_b64_tr_b16 a[254:255], v235 offset:20480\n"
"s_branch LAY2_%=\n"
"LAX_%=:\n"
"v_mfma_f32_16x16x32_bf16 a[224:227], v[124:127], a[244:247], a[224:227]\n"
"v_exp_f32 v169, v169\n"
"v_mfma_f32_16x16x32_bf16 v[16:19], v[128:131], a[244:247], v[16:19]\n"
"s_waitcnt vmcnt(12)\n"
"v_exp_f32 v171, v171\n"
"v_mfma_f32_16x16x32_bf16 v[16:19], v[132:135], a[248:251], v[16:19]\n"
"s_barrier\n"
"v_mfma_f32_16x16x32_bf16 v[48:51], v[136:139], a[244:247], v[48:51]\n"
"v_exp_f32 v168, v168\n"
"v_mfma_f32_16x16x32_bf16 v[48:51], v[140:143], a[248:251], v[48:51]\n"
"v_exp_f32 v170, v170\n"
"v_mfma_f32_16x16x32_bf16 v[80:83], v[144:147], a[244:247], v[80:83]\n"
"ds_read_b64_tr_b16 a[244:245], v234 offset:49152\n"
"ds_read_b64_tr_b16 a[246:247], v234 offset:53248\n"
"v_mfma_f32_16x16x32_bf16 v[80:83], v[148:151], a[248:251], v[80:83]\n"
"v_exp_f32 v177, v177\n"
"v_mfma_f32_16x16x32_bf16 v[84:87], v[148:151], v[224:227], v[84:87]\n"
"ds_read_b64_tr_b16 a[250:251], v238 offset:53248\n"
"v_cvt_pk_bf16_f32 v100, v156, v157\n"
"v_mfma_f32_16x16x32_bf16 v[84:87], v[144:147], a[252:255], v[84:87]\n"
"ds_read_b64_tr_b16 a[248:249], v238 offset:49152\n"
"v_cvt_pk_bf16_f32 v102, v168, v169\n"
"v_cvt_pk_bf16_f32 v103, v170, v171\n"
"v_exp_f32 v172, v172\n"
"v_exp_f32 v173, v173\n"
"v_exp_f32 v179, v179\n"
"v_mfma_f32_16x16x32_bf16 v[52:55], v[140:143], v[224:227], v[52:55]\n"
"v_exp_f32 v176, v176\n"
"v_mfma_f32_16x16x32_bf16 v[52:55], v[136:139], a[252:255], v[52:55]\n"
"v_exp_f32 v174, v174\n"
"v_mfma_f32_16x16x32_bf16 v[20:23], v[132:135], v[224:227], v[20:23]\n"
"ds_read_b64_tr_b16 v[224:225], v239 offset:49152\n"
"v_cvt_pk_bf16_f32 v118, v176, v177\n"
"v_mfma_f32_16x16x32_bf16 v[20:23], v[128:131], a[252:255], v[20:23]\n"
"v_exp_f32 v178, v178\n"
"v_mfma_f32_16x16x32_bf16 a[228:231], v[124:127], a[252:255], a[228:231]\n"
"s_waitcnt lgkmcnt(3)\n"
"v_cvt_pk_bf16_f32 v109, v162, v163\n"
"v_cvt_pk_bf16_f32 v116, v164, v165\n"
"v_mfma_f32_16x16x32_bf16 a[216:219], v[120:123], a[244:247], a[216:219]\n"
"s_mov_b64 s[28:29], s[8:9]\n"
"ds_read_b64_tr_b16 a[254:255], v235 offset:53248\n"
"v_cvt_pk_bf16_f32 v108, v160, v161\n"
"v_smfmac_f32_16x16x64_bf16 v[196:199], a[240:243], v[96:103], v241\n"
"s_mov_b32 s30, 655360\n"
"ds_read_b64_tr_b16 v[226:227], v239 offset:53248\n"
"v_cvt_pk_bf16_f32 v110, v172, v173\n"
"v_mfma_f32_16x16x32_bf16 v[8:11], v[96:99], a[244:247], v[8:11]\n"
"s_waitcnt lgkmcnt(3)\n"
"s_mov_b32 s71, s44\n"
"v_cvt_pk_bf16_f32 v117, v166, v167\n"
"v_cvt_pk_bf16_f32 v119, v178, v179\n"
"v_mfma_f32_16x16x32_bf16 v[8:11], v[100:103], a[248:251], v[8:11]\n"
"s_mov_b32 s70, s95\n"
"v_exp_f32 v175, v175\n"
"v_smfmac_f32_16x16x64_bf16 v[200:203], a[240:243], v[112:119], v241\n"
"ds_read_b64_tr_b16 a[252:253], v235 offset:49152\n"
"v_cvt_pk_bf16_f32 v111, v174, v175\n"
"v_mfma_f32_16x16x32_bf16 v[40:43], v[104:107], a[244:247], v[40:43]\n"
"v_and_b32 v255, 0x3f0, v242\n"
"v_lshrrev_b32 v255, 1, v255\n"
"v_mfma_f32_16x16x32_bf16 v[40:43], v[108:111], a[248:251], v[40:43]\n"
"s_add_u32 s41, s70, 327680\n"
"s_add_u32 m0, s71, 32768\n"
"v_cvt_f32_u32 v254, s43\n"
"v_smfmac_f32_16x16x64_bf16 v[198:201], a[240:243], v[104:111], v241\n"
"buffer_load_dwordx4 v240, s[28:31], s41 offen lds\n"
"v_mfma_f32_16x16x32_bf16 v[72:75], v[112:115], a[244:247], v[72:75]\n"
"v_add_f32 v196, v196, v197\n"
"v_sub_u32 v255, v242, v255\n"
"v_mfma_f32_16x16x32_bf16 v[72:75], v[116:119], a[248:251], v[72:75]\n"
"ds_read_b64_tr_b16 a[246:247], v234 offset:54272\n"
"ds_read_b64_tr_b16 a[248:249], v238 offset:50176\n"
"v_mfma_f32_16x16x32_bf16 v[202:205], a[240:243], v[120:123], v[202:205]\n"
"s_waitcnt lgkmcnt(3)\n"
"ds_read_b64_tr_b16 a[244:245], v234 offset:50176\n"
"ds_read_b64_tr_b16 a[250:251], v238 offset:54272\n"
"v_mfma_f32_16x16x32_bf16 v[76:79], v[116:119], v[224:227], v[76:79]\n"
"s_waitcnt lgkmcnt(4)\n"
"s_add_u32 s40, s41, s52\n"
"v_add_f32 v198, v198, v199\n"
"v_add_f32 v249, v196, v198\n"
"v_mfma_f32_16x16x32_bf16 v[76:79], v[112:115], a[252:255], v[76:79]\n"
"buffer_load_dwordx4 v240, s[28:31], s40 offen offset:1024 lds\n"
"v_mfma_f32_16x16x32_bf16 v[44:47], v[108:111], v[224:227], v[44:47]\n"
"v_add_f32 v200, v200, v201\n"
"v_add_f32 v254, v254, v202\n"
"v_mfma_f32_16x16x32_bf16 v[44:47], v[104:107], a[252:255], v[44:47]\n"
"v_min3_f32 v254, v254, v196, v198\n"
"v_min_f32 v254, v254, v200\n"
"v_mfma_f32_16x16x32_bf16 v[12:15], v[100:103], v[224:227], v[12:15]\n"
"s_add_u32 s40, s41, s53\n"
"v_rcp_f32 v254, v254\n"
"v_mfma_f32_16x16x32_bf16 v[12:15], v[96:99], a[252:255], v[12:15]\n"
"buffer_load_dwordx4 v240, s[28:31], s40 offen offset:2048 lds\n"
"v_mfma_f32_16x16x32_bf16 a[220:223], v[120:123], a[252:255], a[220:223]\n"
"s_waitcnt lgkmcnt(1)\n"
"v_add_f32 v249, v249, v200\n"
"v_add_f32 v249, v249, v202\n"
"v_fmac_f32 v249, 0x53800000, v254\n"
"v_cmp_nge_f32 vcc, s45, v249\n"
"v_xor_b32 v250, 0x1000, v255\n"
"v_xor_b32 v254, 0x11000, v242\n"
"s_cbranch_vccnz LSLOW_%=\nLBACK3_%=:\ns_mov_b64 vcc, s[64:65]\n"
"v_mfma_f32_16x16x32_bf16 a[232:235], v[120:123], a[244:247], a[232:235]\n"
"ds_read_b64_tr_b16 a[254:255], v235 offset:54272\n"
"ds_read_b64_tr_b16 v[224:225], v239 offset:50176\n"
"ds_read_b64_tr_b16 v[226:227], v239 offset:54272\n"
"v_rcp_f32 v243, v196\n"
"v_rcp_f32 v244, v198\n"
"v_mfma_f32_16x16x32_bf16 v[24:27], v[96:99], a[244:247], v[24:27]\n"
"s_waitcnt lgkmcnt(3)\n"
"ds_read_b64_tr_b16 a[252:253], v235 offset:50176\n"
"v_mov_b32_dpp v244, v244 row_ror:12 row_mask:0x2 bank_mask:0xf\n"
"v_mfma_f32_16x16x32_bf16 v[24:27], v[100:103], a[248:251], v[24:27]\n"
"ds_write_b128 v254, a[220:223] offset:33792\n"
"v_rcp_f32 v245, v200\n"
"v_mov_b32_dpp v243, v243 row_ror:12 row_mask:0x2 bank_mask:0xf\n"
"v_mov_b32 v253, v202\n"
"v_mfma_f32_16x16x32_bf16 v[56:59], v[104:107], a[244:247], v[56:59]\n"
"s_add_u32 s40, s41, s54\n"
"v_mov_b32_dpp v243, v243 row_ror:8 row_mask:0x4 bank_mask:0xf\n"
"v_bfe_u32 v249, v242, 9, 1\n"
"v_mfma_f32_16x16x32_bf16 v[56:59], v[108:111], a[248:251], v[56:59]\n"
"buffer_load_dwordx4 v240, s[28:31], s40 offen offset:3072 lds\n"
"v_mfma_f32_16x16x32_bf16 v[88:91], v[112:115], a[244:247], v[88:91]\n"
"s_add_u32 s71, s44, s75\n"
"v_mov_b32_dpp v245, v245 row_ror:12 row_mask:0x2 bank_mask:0xf\n"
"v_mov_b32_dpp v244, v244 row_ror:8 row_mask:0x4 bank_mask:0xf\n"
"v_mfma_f32_16x16x32_bf16 v[88:91], v[116:119], a[248:251], v[88:91]\n"
"s_waitcnt lgkmcnt(2)\n"
"s_mov_b32 s70, s100\n"
"v_mov_b32_dpp v245, v245 row_ror:8 row_mask:0x4 bank_mask:0xf\n"
"v_mov_b32_dpp v243, v243 row_ror:4 row_mask:0x8 bank_mask:0xf\n"
"v_mfma_f32_16x16x32_bf16 v[92:95], v[116:119], v[224:227], v[92:95]\n"
"s_waitcnt lgkmcnt(1)\n"
"s_add_u32 s41, s70, 163840\n"
"s_add_u32 m0, s71, 16384\n"
"ds_read_b64_tr_b16 a[244:245], v232 offset:49152\n"
"v_mfma_f32_16x16x32_bf16 v[92:95], v[112:115], a[252:255], v[92:95]\n"
"buffer_load_dwordx4 v240, s[28:31], s41 offen lds\n"
"ds_read_b64_tr_b16 a[246:247], v232 offset:53248\n"
"ds_read_b64_tr_b16 a[248:249], v236 offset:49152\n"
"ds_read_b64_tr_b16 a[250:251], v236 offset:53248\n"
"v_mov_b32_dpp v244, v244 row_ror:4 row_mask:0x8 bank_mask:0xf\n"
"v_lshl_add_u32 v249, v249, 4, s43\n"
"v_mul_f32_dpp v9, v243, v9 row_newbcast:1 row_mask:0xf bank_mask:0xf\n"
"v_mul_f32_dpp v10, v243, v10 row_newbcast:2 row_mask:0xf bank_mask:0xf\n"
"v_mfma_f32_16x16x32_bf16 v[60:63], v[108:111], v[224:227], v[60:63]\n"
"v_mov_b32_dpp v245, v245 row_ror:4 row_mask:0x8 bank_mask:0xf\n"
"v_mov_b32 v252, v192\n"
"v_mfma_f32_16x16x32_bf16 v[60:63], v[104:107], a[252:255], v[60:63]\n"
"ds_write_b128 v254, a[232:235] offset:34816\n"
"v_mul_f32_dpp v73, v245, v73 row_newbcast:1 row_mask:0xf bank_mask:0xf\n"
"v_mul_f32_dpp v74, v245, v74 row_newbcast:2 row_mask:0xf bank_mask:0xf\n"
"v_mul_f32_dpp v25, v243, v25 row_newbcast:1 row_mask:0xf bank_mask:0xf\n"
"v_mul_f32_dpp v89, v245, v89 row_newbcast:1 row_mask:0xf bank_mask:0xf\n"
"v_bfe_u32 v207, v242, 8, 2\n"
"v_mfma_f32_16x16x32_bf16 v[28:31], v[100:103], v[224:227], v[28:31]\n"
"s_add_u32 s40, s41, s52\n"
"ds_read_b64_tr_b16 v[224:225], v237 offset:49152\n"
"ds_read_b64_tr_b16 v[226:227], v237 offset:53248\n"
"v_mfma_f32_16x16x32_bf16 v[28:31], v[96:99], a[252:255], v[28:31]\n"
"buffer_load_dwordx4 v240, s[28:31], s40 offen offset:1024 lds\n"
"v_lshl_add_u32 v249, v249, 6, s75\n"
"v_mul_f32_dpp v12, v243, v12 row_newbcast:0 row_mask:0xf bank_mask:0xf\n"
"v_mul_f32_dpp v41, v244, v41 row_newbcast:1 row_mask:0xf bank_mask:0xf\n"
"v_mul_f32_dpp v72, v245, v72 row_newbcast:0 row_mask:0xf bank_mask:0xf\n"
"v_mul_f32_dpp v76, v245, v76 row_newbcast:0 row_mask:0xf bank_mask:0xf\n"
"v_cvt_pk_bf16_f32 v72, v72, v73\n"
"v_mfma_f32_16x16x32_bf16 a[236:239], v[120:123], a[252:255], a[236:239]\n"
"s_waitcnt lgkmcnt(5)\n"
"v_mul_f32_dpp v75, v245, v75 row_newbcast:3 row_mask:0xf bank_mask:0xf\n"
"v_mul_f32_dpp v61, v244, v61 row_newbcast:1 row_mask:0xf bank_mask:0xf\n"
"v_mfma_f32_16x16x32_bf16 a[208:211], v[120:123], a[244:247], a[208:211]\n"
"ds_write_b64 v250, v[252:253] offset:52224\n"
"v_sub_u32 v231, v235, v249\n"
"v_mul_f32_dpp v13, v243, v13 row_newbcast:1 row_mask:0xf bank_mask:0xf\n"
"v_mul_f32_dpp v40, v244, v40 row_newbcast:0 row_mask:0xf bank_mask:0xf\n"
"v_mul_f32_dpp v77, v245, v77 row_newbcast:1 row_mask:0xf bank_mask:0xf\n"
"v_mul_f32_dpp v79, v245, v79 row_newbcast:3 row_mask:0xf bank_mask:0xf\n"
"v_cvt_pk_bf16_f32 v73, v74, v75\n"
"v_mul_f32_dpp v27, v243, v27 row_newbcast:3 row_mask:0xf bank_mask:0xf\n"
"v_mfma_f32_16x16x32_bf16 v[0:3], v[96:99], a[244:247], v[0:3]\n"
"s_waitcnt lgkmcnt(4)\n"
"ds_read_b64_tr_b16 a[252:253], v233 offset:49152\n"
"ds_read_b64_tr_b16 a[254:255], v233 offset:53248\n"
"v_mfma_f32_16x16x32_bf16 v[0:3], v[100:103], a[248:251], v[0:3]\n"
"ds_write_b128 v254, a[216:219] offset:32768\n"
"v_mul_f32_dpp v8, v243, v8 row_newbcast:0 row_mask:0xf bank_mask:0xf\n"
"v_mul_f32_dpp v26, v243, v26 row_newbcast:2 row_mask:0xf bank_mask:0xf\n"
"v_mul_f32_dpp v62, v244, v62 row_newbcast:2 row_mask:0xf bank_mask:0xf\n"
"v_mul_f32_dpp v63, v244, v63 row_newbcast:3 row_mask:0xf bank_mask:0xf\n"
"v_mul_f32_dpp v94, v245, v94 row_newbcast:2 row_mask:0xf bank_mask:0xf\n"
"v_mfma_f32_16x16x32_bf16 v[32:35], v[104:107], a[244:247], v[32:35]\n"
"s_add_u32 s40, s41, s53\n"
"v_mul_f32_dpp v78, v245, v78 row_newbcast:2 row_mask:0xf bank_mask:0xf\n"
"v_mul_f32_dpp v28, v243, v28 row_newbcast:0 row_mask:0xf bank_mask:0xf\n"
"v_mfma_f32_16x16x32_bf16 v[32:35], v[108:111], a[248:251], v[32:35]\n"
"buffer_load_dwordx4 v240, s[28:31], s40 offen offset:2048 lds\n"
"v_sub_u32 v229, v233, v249\n"
"v_mul_f32_dpp v15, v243, v15 row_newbcast:3 row_mask:0xf bank_mask:0xf\n"
"v_mul_f32_dpp v43, v244, v43 row_newbcast:3 row_mask:0xf bank_mask:0xf\n"
"v_mul_f32_dpp v45, v244, v45 row_newbcast:1 row_mask:0xf bank_mask:0xf\n"
"v_mul_f32_dpp v57, v244, v57 row_newbcast:1 row_mask:0xf bank_mask:0xf\n"
"v_mul_f32_dpp v60, v244, v60 row_newbcast:0 row_mask:0xf bank_mask:0xf\n"
"v_mul_f32_dpp v88, v245, v88 row_newbcast:0 row_mask:0xf bank_mask:0xf\n"
"v_mfma_f32_16x16x32_bf16 v[64:67], v[112:115], a[244:247], v[64:67]\n"
"v_mul_f32_dpp v46, v244, v46 row_newbcast:2 row_mask:0xf bank_mask:0xf\n"
"v_mul_f32_dpp v58, v244, v58 row_newbcast:2 row_mask:0xf bank_mask:0xf\n"
"v_mfma_f32_16x16x32_bf16 v[64:67], v[116:119], a[248:251], v[64:67]\n"
"s_waitcnt lgkmcnt(4)\n"
"ds_write_b128 v254, a[236:239] offset:35840\n"
"v_mul_f32_dpp v11, v243, v11 row_newbcast:3 row_mask:0xf bank_mask:0xf\n"
"v_cvt_pk_bf16_f32 v8, v8, v9\n"
"v_cvt_pk_bf16_f32 v9, v10, v11\n"
"v_mul_f32_dpp v42, v244, v42 row_newbcast:2 row_mask:0xf bank_mask:0xf\n"
"v_cvt_pk_bf16_f32 v74, v76, v77\n"
"v_mfma_f32_16x16x32_bf16 v[68:71], v[116:119], v[224:227], v[68:71]\n"
"s_waitcnt lgkmcnt(2)\n"
"s_add_u32 s40, s41, s54\n"
"ds_read_b64_tr_b16 a[246:247], v232 offset:54272\n"
"v_mul_f32_dpp v3, v243, v3 row_newbcast:3 row_mask:0xf bank_mask:0xf\n"
"v_mfma_f32_16x16x32_bf16 v[68:71], v[112:115], a[252:255], v[68:71]\n"
"buffer_load_dwordx4 v240, s[28:31], s40 offen offset:3072 lds\n"
"v_mul_f32_dpp v14, v243, v14 row_newbcast:2 row_mask:0xf bank_mask:0xf\n"
"v_mul_f32_dpp v44, v244, v44 row_newbcast:0 row_mask:0xf bank_mask:0xf\n"
"v_cvt_pk_bf16_f32 v40, v40, v41\n"
"v_mul_f32_dpp v93, v245, v93 row_newbcast:1 row_mask:0xf bank_mask:0xf\n"
"v_mfma_f32_16x16x32_bf16 v[36:39], v[108:111], v[224:227], v[36:39]\n"
"v_sub_u32 v228, v232, v249\n"
"v_mul_f32_dpp v91, v245, v91 row_newbcast:3 row_mask:0xf bank_mask:0xf\n"
"v_mfma_f32_16x16x32_bf16 v[36:39], v[104:107], a[252:255], v[36:39]\n"
"ds_read_b64_tr_b16 a[244:245], v232 offset:50176\n"
"ds_read_b64_tr_b16 a[248:249], v236 offset:50176\n"
"ds_read_b64_tr_b16 a[250:251], v236 offset:54272\n"
"v_cvt_pk_bf16_f32 v10, v12, v13\n"
"v_mul_f32_dpp v47, v244, v47 row_newbcast:3 row_mask:0xf bank_mask:0xf\n"
"v_cvt_pk_bf16_f32 v75, v78, v79\n"
"v_mul_f32_dpp v56, v244, v56 row_newbcast:0 row_mask:0xf bank_mask:0xf\n"
"v_mul_f32_dpp v95, v245, v95 row_newbcast:3 row_mask:0xf bank_mask:0xf\n"
"v_cvt_pk_bf16_f32 v88, v88, v89\n"
"v_xor_b32 v232, 0x1000, v228\n"
"v_mfma_f32_16x16x32_bf16 v[4:7], v[100:103], v[224:227], v[4:7]\n"
"ds_read_b64_tr_b16 v[224:225], v237 offset:50176\n"
"ds_read_b64_tr_b16 v[226:227], v237 offset:54272\n"
"v_mfma_f32_16x16x32_bf16 v[4:7], v[96:99], a[252:255], v[4:7]\n"
"ds_write_b128 v246, v[72:75] offset:51200\n"
"v_cvt_pk_bf16_f32 v41, v42, v43\n"
"v_cvt_pk_bf16_f32 v43, v46, v47\n"
"v_mul_f32_dpp v90, v245, v90 row_newbcast:2 row_mask:0xf bank_mask:0xf\n"
"v_mul_f32_dpp v32, v244, v32 row_newbcast:0 row_mask:0xf bank_mask:0xf\n"
"v_mfma_f32_16x16x32_bf16 a[212:215], v[120:123], a[252:255], a[212:215]\n"
"s_waitcnt lgkmcnt(5)\n"
"ds_read_b64_tr_b16 a[36:37], v247 offset:51200\n"
"ds_read_b64_tr_b16 a[252:253], v233 offset:50176\n"
"ds_read_b64_tr_b16 a[254:255], v233 offset:54272\n"
"v_cvt_pk_bf16_f32 v11, v14, v15\n"
"v_cvt_pk_bf16_f32 v42, v44, v45\n"
"v_mul_f32_dpp v59, v244, v59 row_newbcast:3 row_mask:0xf bank_mask:0xf\n"
"v_cvt_pk_bf16_f32 v56, v56, v57\n"
"v_mul_f32_dpp v0, v243, v0 row_newbcast:0 row_mask:0xf bank_mask:0xf\n"
"v_mul_f32_dpp v38, v244, v38 row_newbcast:2 row_mask:0xf bank_mask:0xf\n"
"v_mul_f32_dpp v67, v245, v67 row_newbcast:3 row_mask:0xf bank_mask:0xf\n"
"v_mfma_f32_16x16x32_bf16 a[224:227], v[120:123], a[244:247], a[224:227]\n"
"ds_write_b128 v246, v[40:43] offset:50176\n"
"v_mul_f32_dpp v64, v245, v64 row_newbcast:0 row_mask:0xf bank_mask:0xf\n"
"v_mul_f32_dpp v65, v245, v65 row_newbcast:1 row_mask:0xf bank_mask:0xf\n"
"v_cvt_pk_bf16_f32 v64, v64, v65\n"
"v_xor_b32 v254, 0x1000, v254\n"
"v_add_u32 v236, s76, v247\n"
"v_mfma_f32_16x16x32_bf16 v[16:19], v[96:99], a[244:247], v[16:19]\n"
"s_waitcnt vmcnt(8) lgkmcnt(1)\n"
"ds_write_b128 v246, v[8:11] offset:49152\n"
"v_sub_u32 v230, v234, v249\n"
"v_cvt_pk_bf16_f32 v57, v58, v59\n"
"v_mul_f32_dpp v71, v245, v71 row_newbcast:3 row_mask:0xf bank_mask:0xf\n"
"v_lshlrev_b32 v207, 6, v207\n"
"v_xor_b32 v237, 0x400, v236\n"
"v_mfma_f32_16x16x32_bf16 v[16:19], v[100:103], a[248:251], v[16:19]\n"
"s_barrier\n"
"ds_read_b64 v[250:251], v255 offset:52224\n"
"ds_read_b64_tr_b16 v[182:183], v228 offset:2048\n"
"ds_read_b64_tr_b16 v[222:223], v232 offset:26624\n"
"ds_read_b64_tr_b16 a[242:243], v236 offset:4104\n"
"v_mul_f32_dpp v24, v243, v24 row_newbcast:0 row_mask:0xf bank_mask:0xf\n"
"v_mul_f32_dpp v30, v243, v30 row_newbcast:2 row_mask:0xf bank_mask:0xf\n"
"v_mul_f32_dpp v66, v245, v66 row_newbcast:2 row_mask:0xf bank_mask:0xf\n"
"v_mul_f32_dpp v70, v245, v70 row_newbcast:2 row_mask:0xf bank_mask:0xf\n"
"v_mfma_f32_16x16x32_bf16 v[48:51], v[104:107], a[244:247], v[48:51]\n"
"ds_read_b64_tr_b16 a[38:39], v247 offset:51208\n"
"v_cvt_pk_bf16_f32 v58, v60, v61\n"
"v_mfma_f32_16x16x32_bf16 v[48:51], v[108:111], a[248:251], v[48:51]\n"
"ds_read_b64_tr_b16 a[20:21], v247 offset:50176\n"
"ds_read_b64_tr_b16 v[184:185], v232 offset:0\n"
"ds_read_b64_tr_b16 v[188:189], v228 offset:8192\n"
"ds_read_b64_tr_b16 v[208:209], v228 offset:16384\n"
"v_mul_f32_dpp v29, v243, v29 row_newbcast:1 row_mask:0xf bank_mask:0xf\n"
"v_mul_f32_dpp v2, v243, v2 row_newbcast:2 row_mask:0xf bank_mask:0xf\n"
"v_mul_f32_dpp v5, v243, v5 row_newbcast:1 row_mask:0xf bank_mask:0xf\n"
"v_mul_f32_dpp v33, v244, v33 row_newbcast:1 row_mask:0xf bank_mask:0xf\n"
"v_mul_f32_dpp v34, v244, v34 row_newbcast:2 row_mask:0xf bank_mask:0xf\n"
"v_mul_f32_dpp v39, v244, v39 row_newbcast:3 row_mask:0xf bank_mask:0xf\n"
"v_mfma_f32_16x16x32_bf16 v[80:83], v[112:115], a[244:247], v[80:83]\n"
"ds_read_b64_tr_b16 v[192:193], v232 offset:8192\n"
"ds_read_b64_tr_b16 v[220:221], v232 offset:24576\n"
"v_mfma_f32_16x16x32_bf16 v[80:83], v[116:119], a[248:251], v[80:83]\n"
"ds_read_b64_tr_b16 a[6:7], v247 offset:49160\n"
"ds_read_b64_tr_b16 v[180:181], v228 offset:0\n"
"v_mfma_f32_16x16x32_bf16 v[84:87], v[116:119], v[224:227], v[84:87]\n"
"ds_read_b64_tr_b16 v[194:195], v232 offset:10240\n"
"ds_read_b64_tr_b16 a[198:199], v237 offset:8\n"
"v_mfma_f32_16x16x32_bf16 v[84:87], v[112:115], a[252:255], v[84:87]\n"
"ds_read_b64_tr_b16 v[212:213], v232 offset:16384\n"
"ds_read_b64_tr_b16 v[214:215], v232 offset:18432\n"
"ds_read_b64_tr_b16 v[218:219], v228 offset:26624\n"
"ds_read_b64_tr_b16 a[248:249], v236 offset:6144\n"
"v_cvt_pk_bf16_f32 v24, v24, v25\n"
"v_cvt_pk_bf16_f32 v59, v62, v63\n"
"v_mul_f32_dpp v92, v245, v92 row_newbcast:0 row_mask:0xf bank_mask:0xf\n"
"v_mul_f32_dpp v69, v245, v69 row_newbcast:1 row_mask:0xf bank_mask:0xf\n"
"v_mfma_f32_16x16x32_bf16 v[52:55], v[108:111], v[224:227], v[52:55]\n"
"ds_read_b64_tr_b16 a[4:5], v247 offset:49152\n"
"ds_read_b64_tr_b16 a[22:23], v247 offset:50184\n"
"v_mfma_f32_16x16x32_bf16 v[52:55], v[104:107], a[252:255], v[52:55]\n"
"s_waitcnt lgkmcnt(15)\n"
"ds_read_b64_tr_b16 v[186:187], v232 offset:2048\n"
"ds_read_b64_tr_b16 v[210:211], v228 offset:18432\n"
"ds_read_b64_tr_b16 v[216:217], v228 offset:24576\n"
"v_cvt_pk_bf16_f32 v89, v90, v91\n"
"v_mul_f32_dpp v1, v243, v1 row_newbcast:1 row_mask:0xf bank_mask:0xf\n"
"v_mul_f32_dpp v36, v244, v36 row_newbcast:0 row_mask:0xf bank_mask:0xf\n"
"v_cvt_pk_bf16_f32 v65, v66, v67\n"
"v_min_f32 v249, v252, v250\n"
"v_sub_f32 v252, v249, v252\n"
"v_sub_f32 v250, v249, v250\n"
"v_mfma_f32_16x16x32_bf16 v[20:23], v[100:103], v[224:227], v[20:23]\n"
"ds_write_b128 v246, v[56:59] offset:50176\n"
"v_mul_f32_dpp v31, v243, v31 row_newbcast:3 row_mask:0xf bank_mask:0xf\n"
"v_cvt_pk_bf16_f32 v25, v26, v27\n"
"v_cvt_pk_bf16_f32 v90, v92, v93\n"
"v_mul_f32_dpp v4, v243, v4 row_newbcast:0 row_mask:0xf bank_mask:0xf\n"
"v_mfma_f32_16x16x32_bf16 v[20:23], v[96:99], a[252:255], v[20:23]\n"
"ds_read_b64_tr_b16 a[28:29], v247 offset:50176\n"
"ds_read_b64_tr_b16 a[30:31], v247 offset:50184\n"
"ds_read_b64_tr_b16 v[190:191], v228 offset:10240\n"
"ds_read_b64_tr_b16 a[250:251], v236 offset:6152\n"
"v_cvt_pk_bf16_f32 v91, v94, v95\n"
"v_mul_f32_dpp v7, v243, v7 row_newbcast:3 row_mask:0xf bank_mask:0xf\n"
"v_cvt_pk_bf16_f32 v0, v0, v1\n"
"v_cvt_pk_bf16_f32 v1, v2, v3\n"
"v_mul_f32_dpp v68, v245, v68 row_newbcast:0 row_mask:0xf bank_mask:0xf\n"
"v_mfma_f32_16x16x32_bf16 a[228:231], v[120:123], a[252:255], a[228:231]\n"
"s_waitcnt lgkmcnt(15)\n"
"ds_write_b128 v246, v[88:91] offset:51200\n"
"v_cvt_pk_bf16_f32 v26, v28, v29\n"
"v_cvt_pk_bf16_f32 v27, v30, v31\n"
"v_mul_f32_dpp v6, v243, v6 row_newbcast:2 row_mask:0xf bank_mask:0xf\n"
"v_cvt_pk_bf16_f32 v2, v4, v5\n"
"v_cvt_pk_bf16_f32 v66, v68, v69\n"
"v_mfma_f32_16x16x32_bf16 v[124:127], v[180:183], a[96:99], 0\n"
"s_waitcnt lgkmcnt(8)\n"
"ds_write_b128 v246, v[24:27] offset:49152\n"
"v_cvt_pk_bf16_f32 v3, v6, v7\n"
"v_mul_f32_dpp v35, v244, v35 row_newbcast:3 row_mask:0xf bank_mask:0xf\n"
"v_mul_f32_dpp v37, v244, v37 row_newbcast:1 row_mask:0xf bank_mask:0xf\n"
"v_xor_b32 v207, v207, v248\n"
"v_xor_b32 v234, 0x1000, v230\n"
"v_mfma_f32_16x16x32_bf16 v[124:127], v[184:187], a[100:103], v[124:127]\n"
"s_waitcnt lgkmcnt(3)\n"
"ds_read_b64_tr_b16 a[12:13], v247 offset:49152\n"
"ds_read_b64_tr_b16 a[14:15], v247 offset:49160\n"
"v_mfma_f32_16x16x32_bf16 v[124:127], v[188:191], a[104:107], v[124:127]\n"
"ds_read_b64_tr_b16 a[46:47], v247 offset:51208\n"
"ds_read_b64_tr_b16 a[192:193], v236 offset:0\n"
"v_mfma_f32_16x16x32_bf16 v[124:127], v[192:195], a[108:111], v[124:127]\n"
"ds_write_b128 v246, v[0:3] offset:49152\n"
"v_cvt_pk_bf16_f32 v32, v32, v33\n"
"v_cvt_pk_bf16_f32 v67, v70, v71\n"
"v_exp_f32 v252, v252\n"
"v_mfma_f32_16x16x32_bf16 v[124:127], v[208:211], a[144:147], v[124:127]\n"
"ds_read_b64_tr_b16 a[44:45], v247 offset:51200\n"
"ds_read_b128 v[28:31], v254 offset:33792\n"
"v_mfma_f32_16x16x32_bf16 v[124:127], v[212:215], a[148:151], v[124:127]\n"
"ds_write_b128 v246, v[64:67] offset:51200\n"
"v_cvt_pk_bf16_f32 v33, v34, v35\n"
"v_cvt_pk_bf16_f32 v34, v36, v37\n"
"v_cvt_pk_bf16_f32 v35, v38, v39\n"
"v_mul_f32_dpp v16, v243, v16 row_newbcast:0 row_mask:0xf bank_mask:0xf\n"
"v_mfma_f32_16x16x32_bf16 v[124:127], v[216:219], a[152:155], v[124:127]\n"
"ds_write_b128 v246, v[32:35] offset:50176\n"
"v_exp_f32 v250, v250\n"
"v_xor_b32 v233, 0x1000, v229\n"
"v_mul_f32_dpp v17, v243, v17 row_newbcast:1 row_mask:0xf bank_mask:0xf\n"
"v_mfma_f32_16x16x32_bf16 v[124:127], v[220:223], a[156:159], v[124:127]\n"
"ds_read_b64_tr_b16 a[18:19], v247 offset:50184\n"
"ds_read_b64_tr_b16 a[34:35], v247 offset:51208\n"
"ds_read_b128 v[24:27], v254 offset:32768\n"
"v_accvgpr_read_b32 v249, a208\n"
"v_xor_b32 v235, 0x1000, v231\n"
"v_mul_f32_dpp v18, v243, v18 row_newbcast:2 row_mask:0xf bank_mask:0xf\n"
"v_mfma_f32_16x16x32_bf16 v[140:143], v[220:223], a[172:175], 0\n"
"ds_read_b64_tr_b16 a[0:1], v247 offset:49152\n"
"ds_read_b64_tr_b16 a[2:3], v247 offset:49160\n"
"v_mfma_f32_16x16x32_bf16 v[140:143], v[216:219], a[168:171], v[140:143]\n"
"ds_read_b128 v[56:59], v254 offset:34816\n"
"ds_read_b64_tr_b16 a[194:195], v236 offset:8\n"
"v_mfma_f32_16x16x32_bf16 v[140:143], v[212:215], a[164:167], v[140:143]\n"
"ds_read_b64_tr_b16 a[32:33], v247 offset:51200\n"
"ds_read_b128 v[60:63], v254 offset:35840\n"
"v_mfma_f32_16x16x32_bf16 v[140:143], v[208:211], a[160:163], v[140:143]\n"
"ds_read_b64_tr_b16 a[16:17], v247 offset:50176\n"
"ds_read_b64_tr_b16 a[244:245], v237 offset:4096\n"
"v_mfma_f32_16x16x32_bf16 v[140:143], v[192:195], a[124:127], v[140:143]\n"
"ds_read_b64_tr_b16 v[106:107], v229 offset:10240\n"
"ds_read_b64_tr_b16 a[246:247], v237 offset:4104\n"
"v_mfma_f32_16x16x32_bf16 v[140:143], v[188:191], a[120:123], v[140:143]\n"
"ds_read_b64_tr_b16 a[206:207], v237 offset:2056\n"
"ds_read_b64_tr_b16 a[252:253], v237 offset:6144\n"
"v_mfma_f32_16x16x32_bf16 v[140:143], v[184:187], a[116:119], v[140:143]\n"
"ds_read_b64_tr_b16 a[204:205], v237 offset:2048\n"
"ds_read_b64_tr_b16 a[254:255], v237 offset:6152\n"
"v_mfma_f32_16x16x32_bf16 v[140:143], v[180:183], a[112:115], v[140:143]\n"
"ds_read_b64_tr_b16 v[114:115], v229 offset:18432\n"
"ds_read_b64_tr_b16 v[120:121], v229 offset:24576\n"
"ds_read_b64_tr_b16 a[232:233], v236 offset:10240\n"
"v_mul_f32 v253, v253, v252\n"
"v_mfma_f32_16x16x32_bf16 v[156:159], v[180:183], a[128:131], 0\n"
"ds_read_b64_tr_b16 v[100:101], v233 offset:0\n"
"v_fmac_f32 v253, v251, v250\n"
"v_mfma_f32_16x16x32_bf16 v[156:159], v[184:187], a[132:135], v[156:159]\n"
"ds_read_b64_tr_b16 a[200:201], v236 offset:2048\n"
"ds_read_b64_tr_b16 v[118:119], v233 offset:18432\n"
"v_mfma_f32_16x16x32_bf16 v[156:159], v[188:191], a[136:139], v[156:159]\n"
"ds_read_b64_tr_b16 v[102:103], v233 offset:2048\n"
"ds_read_b64_tr_b16 v[108:109], v233 offset:8192\n"
"v_mfma_f32_16x16x32_bf16 v[156:159], v[192:195], a[140:143], v[156:159]\n"
"s_waitcnt lgkmcnt(14)\n"
"ds_read_b64_tr_b16 a[240:241], v236 offset:4096\n"
"v_mul_f32_dpp v55, v244, v55 row_newbcast:3 row_mask:0xf bank_mask:0xf\n"
"v_mfma_f32_16x16x32_bf16 v[156:159], v[208:211], a[176:179], v[156:159]\n"
"ds_read_b64_tr_b16 a[202:203], v236 offset:2056\n"
"ds_read_b64_tr_b16 v[112:113], v229 offset:16384\n"
"v_mfma_f32_16x16x32_bf16 v[156:159], v[212:215], a[180:183], v[156:159]\n"
"s_add_u32 s41, s95, 491520\n"
"ds_read_b64_tr_b16 v[98:99], v229 offset:2048\n"
"ds_read_b64_tr_b16 v[122:123], v229 offset:26624\n"
"v_mfma_f32_16x16x32_bf16 v[156:159], v[216:219], a[184:187], v[156:159]\n"
"s_add_u32 m0, s44, 49152\n"
"ds_read_b64_tr_b16 a[196:197], v237 offset:0\n"
"ds_read_b64_tr_b16 v[104:105], v229 offset:8192\n"
"v_mfma_f32_16x16x32_bf16 v[156:159], v[220:223], a[188:191], v[156:159]\n"
"s_waitcnt lgkmcnt(12)\n"
"buffer_load_dwordx4 v240, s[28:31], s41 offen lds\n"
"v_mfma_f32_16x16x32_bf16 v[172:175], v[220:223], a[252:255], 0\n"
"s_add_u32 s40, s41, s52\n"
"ds_read_b64_tr_b16 v[96:97], v229 offset:0\n"
"ds_read_b64_tr_b16 v[110:111], v233 offset:10240\n"
"v_mfma_f32_16x16x32_bf16 v[172:175], v[216:219], a[248:251], v[172:175]\n"
"buffer_load_dwordx4 v240, s[28:31], s40 offen offset:1024 lds\n"
"v_mfma_f32_16x16x32_bf16 v[172:175], v[212:215], a[244:247], v[172:175]\n"
"s_waitcnt lgkmcnt(8)\n"
"s_add_u32 s40, s41, s53\n"
"ds_read_b64_tr_b16 v[224:225], v233 offset:24576\n"
"v_mul_f32_dpp v19, v243, v19 row_newbcast:3 row_mask:0xf bank_mask:0xf\n"
"v_mfma_f32_16x16x32_bf16 v[172:175], v[208:211], a[240:243], v[172:175]\n"
"buffer_load_dwordx4 v240, s[28:31], s40 offen offset:2048 lds\n"
"v_mfma_f32_16x16x32_bf16 v[172:175], v[192:195], a[204:207], v[172:175]\n"
"s_waitcnt lgkmcnt(7)\n"
"v_mov_b64 v[200:201], 0\n"
"v_mul_f32_dpp v80, v245, v80 row_newbcast:0 row_mask:0xf bank_mask:0xf\n"
"v_mfma_f32_16x16x32_bf16 v[172:175], v[188:191], a[200:203], v[172:175]\n"
"s_waitcnt lgkmcnt(4)\n"
"ds_read_b64_tr_b16 v[116:117], v233 offset:16384\n"
"ds_read_b64_tr_b16 v[226:227], v233 offset:26624\n"
"v_mfma_f32_16x16x32_bf16 v[172:175], v[184:187], a[196:199], v[172:175]\n"
"s_add_u32 s40, s41, s54\n"
"ds_read_b64_tr_b16 v[216:217], v230 offset:24576\n"
"v_mul_f32_dpp v21, v243, v21 row_newbcast:1 row_mask:0xf bank_mask:0xf\n"
"v_mfma_f32_16x16x32_bf16 v[172:175], v[180:183], a[192:195], v[172:175]\n"
"s_waitcnt lgkmcnt(5)\n"
"buffer_load_dwordx4 v240, s[28:31], s40 offen offset:3072 lds\n"
"v_mfma_f32_16x16x32_bf16 v[128:131], v[96:99], a[96:99], 0\n"
"v_rcp_f32 v253, v253\n"
"v_mfma_f32_16x16x32_bf16 v[128:131], v[100:103], a[100:103], v[128:131]\n"
"v_mul_f32 v250, v250, v253\n"
"v_mul_f32_dpp v22, v243, v22 row_newbcast:2 row_mask:0xf bank_mask:0xf\n"
"v_mfma_f32_16x16x32_bf16 v[128:131], v[104:107], a[104:107], v[128:131]\n"
"s_waitcnt lgkmcnt(3)\n"
"ds_read_b64_tr_b16 v[192:193], v234 offset:8192\n"
"v_mul_f32_dpp v49, v244, v49 row_newbcast:1 row_mask:0xf bank_mask:0xf\n"
"v_mfma_f32_16x16x32_bf16 v[128:131], v[108:111], a[108:111], v[128:131]\n"
"v_mul_f32 v252, v252, v253\n"
"v_mov_b32_dpp v250, v250 row_ror:12 row_mask:0x2 bank_mask:0xf\n"
"v_mfma_f32_16x16x32_bf16 v[128:131], v[112:115], a[144:147], v[128:131]\n"
"s_waitcnt lgkmcnt(3)\n"
"ds_read_b64_tr_b16 v[180:181], v230 offset:0\n"
"v_mov_b32_dpp v252, v252 row_ror:12 row_mask:0x2 bank_mask:0xf\n"
"v_mfma_f32_16x16x32_bf16 v[128:131], v[116:119], a[148:151], v[128:131]\n"
"ds_read_b64_tr_b16 a[234:235], v236 offset:10248\n"
"ds_read_b64_tr_b16 v[220:221], v234 offset:24576\n"
"v_mfma_f32_16x16x32_bf16 v[128:131], v[120:123], a[152:155], v[128:131]\n"
"s_waitcnt lgkmcnt(4)\n"
"v_accvgpr_read_b32 v251, a211\n"
"v_mul_f32_dpp v82, v245, v82 row_newbcast:2 row_mask:0xf bank_mask:0xf\n"
"v_mfma_f32_16x16x32_bf16 v[128:131], v[224:227], a[156:159], v[128:131]\n"
"ds_read_b64_tr_b16 v[182:183], v230 offset:2048\n"
"v_mov_b32_dpp v250, v250 row_ror:8 row_mask:0x4 bank_mask:0xf\n"
"v_mfma_f32_16x16x32_bf16 v[144:147], v[224:227], a[172:175], 0\n"
"ds_read_b64_tr_b16 v[188:189], v230 offset:8192\n"
"v_mul_f32_dpp v54, v244, v54 row_newbcast:2 row_mask:0xf bank_mask:0xf\n"
"v_mfma_f32_16x16x32_bf16 v[144:147], v[120:123], a[168:171], v[144:147]\n"
"v_mov_b32_dpp v252, v252 row_ror:8 row_mask:0x4 bank_mask:0xf\n"
"v_mov_b32_dpp v250, v250 row_ror:4 row_mask:0x8 bank_mask:0xf\n"
"v_mfma_f32_16x16x32_bf16 v[144:147], v[116:119], a[164:167], v[144:147]\n"
"s_nop 0\n"
"v_mul_f32_dpp v24, v250, v24 row_newbcast:0 row_mask:0xf bank_mask:0xf\n"
"v_mul_f32_dpp v51, v244, v51 row_newbcast:3 row_mask:0xf bank_mask:0xf\n"
"v_mfma_f32_16x16x32_bf16 v[144:147], v[112:115], a[160:163], v[144:147]\n"
"ds_read_b64_tr_b16 v[208:209], v230 offset:16384\n"
"v_accvgpr_read_b32 v206, a209\n"
"v_mfma_f32_16x16x32_bf16 v[144:147], v[108:111], a[124:127], v[144:147]\n"
"ds_read_b64_tr_b16 v[210:211], v230 offset:18432\n"
"v_mov_b32_dpp v252, v252 row_ror:4 row_mask:0x8 bank_mask:0xf\n"
"v_mfma_f32_16x16x32_bf16 v[144:147], v[104:107], a[120:123], v[144:147]\n"
"ds_read_b64_tr_b16 v[214:215], v234 offset:18432\n"
"v_mul_f32_dpp v48, v244, v48 row_newbcast:0 row_mask:0xf bank_mask:0xf\n"
"v_mfma_f32_16x16x32_bf16 v[144:147], v[100:103], a[116:119], v[144:147]\n"
"ds_read_b64_tr_b16 v[212:213], v234 offset:16384\n"
"v_mul_f32_dpp v25, v250, v25 row_newbcast:1 row_mask:0xf bank_mask:0xf\n"
"v_mfma_f32_16x16x32_bf16 v[144:147], v[96:99], a[112:115], v[144:147]\n"
"ds_read_b64_tr_b16 v[218:219], v230 offset:26624\n"
"v_accvgpr_read_b32 v253, a210\n"
"v_mul_f32_dpp v26, v250, v26 row_newbcast:2 row_mask:0xf bank_mask:0xf\n"
"v_mul_f32_dpp v27, v250, v27 row_newbcast:3 row_mask:0xf bank_mask:0xf\n"
"v_fmac_f32_dpp v24, v252, v249 row_newbcast:0 row_mask:0xf bank_mask:0xf\n"
"v_fmac_f32_dpp v25, v252, v206 row_newbcast:1 row_mask:0xf bank_mask:0xf\n"
"v_accvgpr_read_b32 v206, a213\n"
"v_mul_f32_dpp v31, v250, v31 row_newbcast:3 row_mask:0xf bank_mask:0xf\n"
"v_mul_f32_dpp v85, v245, v85 row_newbcast:1 row_mask:0xf bank_mask:0xf\n"
"v_mul_f32_dpp v57, v250, v57 row_newbcast:1 row_mask:0xf bank_mask:0xf\n"
"v_mfma_f32_16x16x32_bf16 v[160:163], v[96:99], a[128:131], 0\n"
"ds_read_b64_tr_b16 v[222:223], v234 offset:26624\n"
"v_cvt_pk_bf16_f32 v16, v16, v17\n"
"v_mfma_f32_16x16x32_bf16 v[160:163], v[100:103], a[132:135], v[160:163]\n"
"v_fmac_f32_dpp v27, v252, v251 row_newbcast:3 row_mask:0xf bank_mask:0xf\n"
"v_accvgpr_read_b32 v249, a212\n"
"v_mfma_f32_16x16x32_bf16 v[160:163], v[104:107], a[136:139], v[160:163]\n"
"ds_read_b64_tr_b16 v[194:195], v234 offset:10240\n"
"v_fmac_f32_dpp v26, v252, v253 row_newbcast:2 row_mask:0xf bank_mask:0xf\n"
"v_mfma_f32_16x16x32_bf16 v[160:163], v[108:111], a[140:143], v[160:163]\n"
"ds_read_b64_tr_b16 a[218:219], v236 offset:8200\n"
"v_accvgpr_read_b32 v251, a215\n"
"v_mfma_f32_16x16x32_bf16 v[160:163], v[112:115], a[176:179], v[160:163]\n"
"ds_read_b64_tr_b16 v[184:185], v234 offset:0\n"
"v_cvt_pk_bf16_f32 v24, v24, v25\n"
"v_mfma_f32_16x16x32_bf16 v[160:163], v[116:119], a[180:183], v[160:163]\n"
"v_accvgpr_read_b32 v253, a214\n"
"v_mul_f32_dpp v52, v244, v52 row_newbcast:0 row_mask:0xf bank_mask:0xf\n"
"v_mfma_f32_16x16x32_bf16 v[160:163], v[120:123], a[184:187], v[160:163]\n"
"ds_read_b64_tr_b16 v[186:187], v234 offset:2048\n"
"v_mul_f32_dpp v29, v250, v29 row_newbcast:1 row_mask:0xf bank_mask:0xf\n"
"v_mfma_f32_16x16x32_bf16 v[160:163], v[224:227], a[188:191], v[160:163]\n"
"v_mul_f32_dpp v28, v250, v28 row_newbcast:0 row_mask:0xf bank_mask:0xf\n"
"v_mul_f32_dpp v30, v250, v30 row_newbcast:2 row_mask:0xf bank_mask:0xf\n"
"v_fmac_f32_dpp v29, v252, v206 row_newbcast:1 row_mask:0xf bank_mask:0xf\n"
"v_mul_f32_dpp v20, v243, v20 row_newbcast:0 row_mask:0xf bank_mask:0xf\n"
"v_mul_f32_dpp v58, v250, v58 row_newbcast:2 row_mask:0xf bank_mask:0xf\n"
"v_mul_f32_dpp v62, v250, v62 row_newbcast:2 row_mask:0xf bank_mask:0xf\n"
"v_mfma_f32_16x16x32_bf16 v[176:179], v[224:227], a[252:255], 0\n"
"ds_read_b64_tr_b16 v[190:191], v230 offset:10240\n"
"v_fmac_f32_dpp v28, v252, v249 row_newbcast:0 row_mask:0xf bank_mask:0xf\n"
"v_mfma_f32_16x16x32_bf16 v[176:179], v[120:123], a[248:251], v[176:179]\n"
"v_accvgpr_read_b32 v206, a225\n"
"v_mul_f32_dpp v59, v250, v59 row_newbcast:3 row_mask:0xf bank_mask:0xf\n"
"v_mfma_f32_16x16x32_bf16 v[176:179], v[116:119], a[244:247], v[176:179]\n"
"ds_read_b64_tr_b16 v[224:225], v235 offset:24576\n"
"v_cvt_pk_bf16_f32 v25, v26, v27\n"
"v_mfma_f32_16x16x32_bf16 v[176:179], v[112:115], a[240:243], v[176:179]\n"
"v_fmac_f32_dpp v31, v252, v251 row_newbcast:3 row_mask:0xf bank_mask:0xf\n"
"v_mul_f32_dpp v61, v250, v61 row_newbcast:1 row_mask:0xf bank_mask:0xf\n"
"v_mfma_f32_16x16x32_bf16 v[176:179], v[108:111], a[204:207], v[176:179]\n"
"ds_read_b64_tr_b16 v[120:121], v231 offset:24576\n"
"v_mul_f32_dpp v81, v245, v81 row_newbcast:1 row_mask:0xf bank_mask:0xf\n"
"v_mfma_f32_16x16x32_bf16 v[176:179], v[104:107], a[200:203], v[176:179]\n"
"ds_read_b64_tr_b16 a[222:223], v237 offset:8200\n"
"v_fmac_f32_dpp v57, v252, v206 row_newbcast:1 row_mask:0xf bank_mask:0xf\n"
"v_mfma_f32_16x16x32_bf16 v[176:179], v[100:103], a[196:199], v[176:179]\n"
"v_accvgpr_read_b32 v249, a224\n"
"v_mul_f32_dpp v56, v250, v56 row_newbcast:0 row_mask:0xf bank_mask:0xf\n"
"v_mfma_f32_16x16x32_bf16 v[176:179], v[96:99], a[192:195], v[176:179]\n"
"s_waitcnt lgkmcnt(4)\n"
"ds_read_b64_tr_b16 v[106:107], v231 offset:10240\n"
"v_cvt_pk_bf16_f32 v26, v28, v29\n"
"v_mfma_f32_16x16x32_bf16 v[132:135], v[180:183], a[96:99], 0\n"
"s_mov_b32 s47, 0\n"
"v_fmac_f32_dpp v30, v252, v253 row_newbcast:2 row_mask:0xf bank_mask:0xf\n"
"v_accvgpr_read_b32 v251, a227\n"
"v_mfma_f32_16x16x32_bf16 v[132:135], v[184:187], a[100:103], v[132:135]\n"
"s_waitcnt lgkmcnt(4)\n"
"s_mul_i32 s16, s61, 1280\n"
"ds_read_b64_tr_b16 a[238:239], v237 offset:10248\n"
"v_cvt_pk_bf16_f32 v27, v30, v31\n"
"v_mfma_f32_16x16x32_bf16 v[132:135], v[188:191], a[104:107], v[132:135]\n"
"s_add_u32 s16, s16, s95\n"
"v_mul_f32_dpp v23, v243, v23 row_newbcast:3 row_mask:0xf bank_mask:0xf\n"
"v_fmac_f32_dpp v56, v252, v249 row_newbcast:0 row_mask:0xf bank_mask:0xf\n"
"v_mfma_f32_16x16x32_bf16 v[132:135], v[192:195], a[108:111], v[132:135]\n"
"s_mul_i32 s16, s16, 224\n"
"v_accvgpr_read_b32 v253, a226\n"
"v_mul_f32_dpp v63, v250, v63 row_newbcast:3 row_mask:0xf bank_mask:0xf\n"
"v_mfma_f32_16x16x32_bf16 v[132:135], v[208:211], a[144:147], v[132:135]\n"
"s_add_u32 s17, s16, 4096\n"
"ds_read_b64_tr_b16 v[108:109], v235 offset:8192\n"
"v_fmac_f32_dpp v58, v252, v253 row_newbcast:2 row_mask:0xf bank_mask:0xf\n"
"v_mfma_f32_16x16x32_bf16 v[132:135], v[212:215], a[148:151], v[132:135]\n"
"s_mov_b32 s72, s61\n"
"ds_read_b64_tr_b16 v[118:119], v235 offset:18432\n"
"v_fmac_f32_dpp v59, v252, v251 row_newbcast:3 row_mask:0xf bank_mask:0xf\n"
"v_mfma_f32_16x16x32_bf16 v[132:135], v[216:219], a[152:155], v[132:135]\n"
"s_mov_b32 s21, s60\n"
"ds_read_b64_tr_b16 v[104:105], v231 offset:8192\n"
"v_accvgpr_read_b32 v253, a230\n"
"v_mfma_f32_16x16x32_bf16 v[132:135], v[220:223], a[156:159], v[132:135]\n"
"s_mov_b32 s18, s77\n"
"ds_read_b64_tr_b16 v[100:101], v235 offset:0\n"
"ds_read_b64_tr_b16 v[226:227], v235 offset:26624\n"
"v_mfma_f32_16x16x32_bf16 v[148:151], v[220:223], a[172:175], 0\n"
"s_mov_b32 s19, s77\n"
"v_accvgpr_read_b32 v251, a231\n"
"v_cvt_pk_bf16_f32 v56, v56, v57\n"
"v_mfma_f32_16x16x32_bf16 v[148:151], v[216:219], a[168:171], v[148:151]\n"
"s_mov_b32 s69, -1\n"
"ds_read_b64_tr_b16 v[98:99], v231 offset:2048\n"
"v_cvt_pk_bf16_f32 v17, v18, v19\n"
"v_mfma_f32_16x16x32_bf16 v[148:151], v[212:215], a[164:167], v[148:151]\n"
"ds_read_b64_tr_b16 v[110:111], v235 offset:10240\n"
"v_accvgpr_read_b32 v206, a229\n"
"v_mfma_f32_16x16x32_bf16 v[148:151], v[208:211], a[160:163], v[148:151]\n"
"ds_write_b128 v207, v[24:27] offset:0\n"
"v_accvgpr_read_b32 v249, a228\n"
"v_mul_f32_dpp v60, v250, v60 row_newbcast:0 row_mask:0xf bank_mask:0xf\n"
"v_fmac_f32_dpp v61, v252, v206 row_newbcast:1 row_mask:0xf bank_mask:0xf\n"
"v_fmac_f32_dpp v63, v252, v251 row_newbcast:3 row_mask:0xf bank_mask:0xf\n"
"v_cvt_pk_bf16_f32 v57, v58, v59\n"
"v_mfma_f32_16x16x32_bf16 v[148:151], v[192:195], a[124:127], v[148:151]\n"
"ds_read_b64_tr_b16 v[112:113], v231 offset:16384\n"
"v_fmac_f32_dpp v62, v252, v253 row_newbcast:2 row_mask:0xf bank_mask:0xf\n"
"v_mfma_f32_16x16x32_bf16 v[148:151], v[188:191], a[120:123], v[148:151]\n"
"ds_read_b64_tr_b16 v[96:97], v231 offset:0\n"
"v_fmac_f32_dpp v60, v252, v249 row_newbcast:0 row_mask:0xf bank_mask:0xf\n"
"v_mfma_f32_16x16x32_bf16 v[148:151], v[184:187], a[116:119], v[148:151]\n"
"v_cvt_pk_bf16_f32 v58, v60, v61\n"
"v_cvt_pk_bf16_f32 v59, v62, v63\n"
"v_mfma_f32_16x16x32_bf16 v[148:151], v[180:183], a[112:115], v[148:151]\n"
"ds_write_b128 v207, v[56:59] offset:2048\n"
"v_mul_f32_dpp v83, v245, v83 row_newbcast:3 row_mask:0xf bank_mask:0xf\n"
"v_mfma_f32_16x16x32_bf16 v[164:167], v[180:183], a[128:131], 0\n"
"v_xor_b32 v246, s75, v246\n"
"v_xor_b32 v247, s75, v247\n"
"v_mfma_f32_16x16x32_bf16 v[164:167], v[184:187], a[132:135], v[164:167]\n"
"ds_read_b64_tr_b16 v[114:115], v231 offset:18432\n"
"v_cvt_pk_bf16_f32 v48, v48, v49\n"
"v_mfma_f32_16x16x32_bf16 v[164:167], v[188:191], a[136:139], v[164:167]\n"
"ds_read_b64_tr_b16 v[102:103], v235 offset:2048\n"
"v_mul_f32_dpp v50, v244, v50 row_newbcast:2 row_mask:0xf bank_mask:0xf\n"
"v_mfma_f32_16x16x32_bf16 v[164:167], v[192:195], a[140:143], v[164:167]\n"
"ds_read_b64_tr_b16 a[236:237], v237 offset:10240\n"
"v_cvt_pk_bf16_f32 v19, v22, v23\n"
"v_mfma_f32_16x16x32_bf16 v[164:167], v[208:211], a[176:179], v[164:167]\n"
"ds_read_b64_tr_b16 a[220:221], v237 offset:8192\n"
"v_cvt_pk_bf16_f32 v49, v50, v51\n"
"v_mfma_f32_16x16x32_bf16 v[164:167], v[212:215], a[180:183], v[164:167]\n"
"ds_read_b64_tr_b16 v[116:117], v235 offset:16384\n"
"v_cvt_pk_bf16_f32 v18, v20, v21\n"
"v_mfma_f32_16x16x32_bf16 v[164:167], v[216:219], a[184:187], v[164:167]\n"
"s_waitcnt vmcnt(0) lgkmcnt(5)\n"
"ds_read_b64_tr_b16 v[122:123], v231 offset:26624\n"
"v_mul_f32_dpp v53, v244, v53 row_newbcast:1 row_mask:0xf bank_mask:0xf\n"
"v_mfma_f32_16x16x32_bf16 v[164:167], v[220:223], a[188:191], v[164:167]\n"
"s_barrier\n"
"ds_write_b128 v246, v[16:19] offset:49152\n"
"v_mfma_f32_16x16x32_bf16 v[136:139], v[96:99], a[96:99], 0\n"
"s_waitcnt lgkmcnt(5)\n"
"v_cvt_pk_bf16_f32 v51, v54, v55\n"
"v_cvt_pk_bf16_f32 v80, v80, v81\n"
"v_mfma_f32_16x16x32_bf16 v[136:139], v[100:103], a[100:103], v[136:139]\n"
"ds_read_b64_tr_b16 a[8:9], v247 offset:49152\n"
"v_cvt_pk_bf16_f32 v50, v52, v53\n"
"v_mfma_f32_16x16x32_bf16 v[136:139], v[104:107], a[104:107], v[136:139]\n"
"ds_write_b128 v246, v[48:51] offset:50176\n"
"v_mul_f32_dpp v87, v245, v87 row_newbcast:3 row_mask:0xf bank_mask:0xf\n"
"v_mfma_f32_16x16x32_bf16 v[136:139], v[108:111], a[108:111], v[136:139]\n"
"v_mul_f32_dpp v84, v245, v84 row_newbcast:0 row_mask:0xf bank_mask:0xf\n"
"v_cvt_pk_bf16_f32 v81, v82, v83\n"
"v_mfma_f32_16x16x32_bf16 v[136:139], v[112:115], a[144:147], v[136:139]\n"
"s_waitcnt lgkmcnt(4)\n"
"ds_read_b64_tr_b16 v[190:191], v228 offset:43008\n"
"v_cvt_pk_bf16_f32 v82, v84, v85\n"
"v_mfma_f32_16x16x32_bf16 v[136:139], v[116:119], a[148:151], v[136:139]\n"
"s_waitcnt lgkmcnt(4)\n"
"ds_read_b64_tr_b16 a[26:27], v247 offset:50184\n"
"v_mul_f32_dpp v86, v245, v86 row_newbcast:2 row_mask:0xf bank_mask:0xf\n"
"v_mfma_f32_16x16x32_bf16 v[136:139], v[120:123], a[152:155], v[136:139]\n"
"ds_read_b64_tr_b16 v[184:185], v232 offset:32768\n"
"v_cvt_pk_bf16_f32 v83, v86, v87\n"
"v_mfma_f32_16x16x32_bf16 v[136:139], v[224:227], a[156:159], v[136:139]\n"
"ds_write_b128 v246, v[80:83] offset:51200\n"
"v_mfma_f32_16x16x32_bf16 v[152:155], v[224:227], a[172:175], 0\n"
"v_mov_b64 v[32:33], 0\n"
"v_mfma_f32_16x16x32_bf16 v[152:155], v[120:123], a[168:171], v[152:155]\n"
"ds_read_b64_tr_b16 a[24:25], v247 offset:50176\n"
"ds_read_b64_tr_b16 a[40:41], v247 offset:51200\n"
"v_mfma_f32_16x16x32_bf16 v[152:155], v[116:119], a[164:167], v[152:155]\n"
"ds_read_b64_tr_b16 a[10:11], v247 offset:49160\n"
"ds_read_b64_tr_b16 a[42:43], v247 offset:51208\n"
"v_mfma_f32_16x16x32_bf16 v[152:155], v[112:115], a[160:163], v[152:155]\n"
"ds_read_b64_tr_b16 v[180:181], v228 offset:32768\n"
"ds_read_b64_tr_b16 v[208:209], v228 offset:49152\n"
"v_mfma_f32_16x16x32_bf16 v[152:155], v[108:111], a[124:127], v[152:155]\n"
"ds_read_b64_tr_b16 v[216:217], v228 offset:57344\n"
"v_mfma_f32_16x16x32_bf16 v[152:155], v[104:107], a[120:123], v[152:155]\n"
"ds_read_b64_tr_b16 v[186:187], v232 offset:34816\n"
"ds_read_b64_tr_b16 v[214:215], v232 offset:51200\n"
"v_mfma_f32_16x16x32_bf16 v[152:155], v[100:103], a[116:119], v[152:155]\n"
"ds_read_b64_tr_b16 v[182:183], v228 offset:34816\n"
"ds_read_b64_tr_b16 v[220:221], v232 offset:57344\n"
"v_mfma_f32_16x16x32_bf16 v[152:155], v[96:99], a[112:115], v[152:155]\n"
"ds_read_b64_tr_b16 v[210:211], v228 offset:51200\n"
"v_mov_b64 v[36:37], 0\n"
"v_mfma_f32_16x16x32_bf16 v[168:171], v[96:99], a[128:131], 0\n"
"ds_read_b64_tr_b16 a[230:231], v237 offset:14344\n"
"ds_read_b64_tr_b16 v[188:189], v228 offset:40960\n"
"v_mfma_f32_16x16x32_bf16 v[168:171], v[100:103], a[132:135], v[168:171]\n"
"ds_read_b64_tr_b16 v[192:193], v232 offset:40960\n"
"v_mfma_f32_16x16x32_bf16 v[168:171], v[104:107], a[136:139], v[168:171]\n"
"ds_read_b64_tr_b16 v[194:195], v232 offset:43008\n"
"ds_read_b64_tr_b16 v[106:107], v229 offset:43008\n"
"v_mfma_f32_16x16x32_bf16 v[168:171], v[108:111], a[140:143], v[168:171]\n"
"s_waitcnt lgkmcnt(11)\n"
"ds_read_b64_tr_b16 v[110:111], v233 offset:43008\n"
"v_mov_b64 v[26:27], 0\n"
"v_mfma_f32_16x16x32_bf16 v[168:171], v[112:115], a[176:179], v[168:171]\n"
"ds_read_b64_tr_b16 a[224:225], v236 offset:14336\n"
"ds_read_b64_tr_b16 v[212:213], v232 offset:49152\n"
"v_mfma_f32_16x16x32_bf16 v[168:171], v[116:119], a[180:183], v[168:171]\n"
"ds_read_b64_tr_b16 v[218:219], v228 offset:59392\n"
"ds_read_b64_tr_b16 v[222:223], v232 offset:59392\n"
"v_mfma_f32_16x16x32_bf16 v[168:171], v[120:123], a[184:187], v[168:171]\n"
"s_add_u32 s41, s70, 327680\n"
"s_add_u32 m0, s71, 32768\n"
"ds_read_b64_tr_b16 a[210:211], v236 offset:12296\n"
"v_mfma_f32_16x16x32_bf16 v[168:171], v[224:227], a[188:191], v[168:171]\n"
"s_waitcnt lgkmcnt(11)\n"
"buffer_load_dwordx4 v240, s[28:31], s41 offen lds\n"
"v_mfma_f32_16x16x32_bf16 v[124:127], v[180:183], a[48:51], v[124:127]\n"
"v_xor_b32 v247, s75, v247\n"
"v_mov_b64 v[18:19], 0\n"
"v_mfma_f32_16x16x32_bf16 v[124:127], v[184:187], a[52:55], v[124:127]\n"
"s_waitcnt lgkmcnt(8)\n"
"v_and_b32 v243, 0x3f0, v242\n"
"v_mfma_f32_16x16x32_bf16 v[124:127], v[188:191], a[56:59], v[124:127]\n"
"s_waitcnt lgkmcnt(6)\n"
"s_add_u32 s40, s41, s52\n"
"ds_read_b64_tr_b16 a[228:229], v237 offset:14336\n"
"ds_read_b64_tr_b16 v[120:121], v229 offset:57344\n"
"v_mfma_f32_16x16x32_bf16 v[124:127], v[192:195], a[60:63], v[124:127]\n"
"buffer_load_dwordx4 v240, s[28:31], s40 offen offset:1024 lds\n"
"v_mfma_f32_16x16x32_bf16 v[124:127], v[208:211], a[0:3], v[124:127]\n"
"s_waitcnt lgkmcnt(5)\n"
"v_mfma_f32_16x16x32_bf16 v[124:127], v[212:215], a[4:7], v[124:127]\n"
"s_waitcnt lgkmcnt(3)\n"
"v_mfma_f32_16x16x32_bf16 v[124:127], v[216:219], a[8:11], v[124:127]\n"
"s_add_u32 s40, s41, s53\n"
"ds_read_b64_tr_b16 v[98:99], v229 offset:34816\n"
"ds_read_b64_tr_b16 v[116:117], v233 offset:49152\n"
"v_mfma_f32_16x16x32_bf16 v[124:127], v[220:223], a[12:15], v[124:127]\n"
"buffer_load_dwordx4 v240, s[28:31], s40 offen offset:2048 lds\n"
"v_mfma_f32_16x16x32_bf16 v[140:143], v[220:223], a[28:31], v[140:143]\n"
"v_mfma_f32_16x16x32_bf16 v[140:143], v[216:219], a[24:27], v[140:143]\n"
"ds_read_b64_tr_b16 v[96:97], v229 offset:32768\n"
"ds_read_b64_tr_b16 v[100:101], v233 offset:32768\n"
"v_mfma_f32_16x16x32_bf16 v[140:143], v[212:215], a[20:23], v[140:143]\n"
"ds_read_b64_tr_b16 v[114:115], v229 offset:51200\n"
"v_mfma_f32_16x16x32_bf16 v[140:143], v[208:211], a[16:19], v[140:143]\n"
"ds_read_b64_tr_b16 a[214:215], v237 offset:12296\n"
"ds_read_b64_tr_b16 a[226:227], v236 offset:14344\n"
"v_mfma_f32_16x16x32_bf16 v[140:143], v[192:195], a[76:79], v[140:143]\n"
"ds_read_b64_tr_b16 a[208:209], v236 offset:12288\n"
"v_cvt_pk_bf16_f32 v124, v124, v125\n"
"v_mfma_f32_16x16x32_bf16 v[140:143], v[188:191], a[72:75], v[140:143]\n"
"ds_read_b64_tr_b16 a[216:217], v236 offset:8192\n"
"v_mov_b64 v[0:1], 0\n"
"v_mfma_f32_16x16x32_bf16 v[140:143], v[184:187], a[68:71], v[140:143]\n"
"s_add_u32 s40, s41, s54\n"
"ds_read_b64_tr_b16 a[212:213], v237 offset:12288\n"
"ds_read_b64_tr_b16 v[108:109], v233 offset:40960\n"
"v_mfma_f32_16x16x32_bf16 v[140:143], v[180:183], a[64:67], v[140:143]\n"
"buffer_load_dwordx4 v240, s[28:31], s40 offen offset:3072 lds\n"
"v_mfma_f32_16x16x32_bf16 v[156:159], v[180:183], a[80:83], v[156:159]\n"
"s_add_u32 s41, s70, 491520\n"
"s_add_u32 m0, s71, 49152\n"
"v_mov_b64 v[2:3], 0\n"
"v_mfma_f32_16x16x32_bf16 v[156:159], v[184:187], a[84:87], v[156:159]\n"
"buffer_load_dwordx4 v240, s[28:31], s41 offen lds\n"
"v_mfma_f32_16x16x32_bf16 v[156:159], v[188:191], a[88:91], v[156:159]\n"
"v_mfma_f32_16x16x32_bf16 v[156:159], v[192:195], a[92:95], v[156:159]\n"
"v_bfe_u32 v255, v242, 8, 1\n"
"v_mfma_f32_16x16x32_bf16 v[156:159], v[208:211], a[32:35], v[156:159]\n"
"ds_read_b64_tr_b16 v[102:103], v233 offset:34816\n"
"ds_read_b64_tr_b16 v[122:123], v229 offset:59392\n"
"v_mfma_f32_16x16x32_bf16 v[156:159], v[212:215], a[36:39], v[156:159]\n"
"ds_read_b64_tr_b16 v[104:105], v229 offset:40960\n"
"v_mfma_f32_16x16x32_bf16 v[156:159], v[216:219], a[40:43], v[156:159]\n"
"s_add_u32 s40, s41, s52\n"
"ds_read_b64_tr_b16 v[112:113], v229 offset:49152\n"
"ds_read_b64_tr_b16 v[118:119], v233 offset:51200\n"
"v_mfma_f32_16x16x32_bf16 v[156:159], v[220:223], a[44:47], v[156:159]\n"
"s_waitcnt lgkmcnt(5)\n"
"buffer_load_dwordx4 v240, s[28:31], s40 offen offset:1024 lds\n"
"v_mfma_f32_16x16x32_bf16 v[172:175], v[220:223], a[228:231], v[172:175]\n"
"s_add_u32 s40, s41, s53\n"
"v_mov_b64 v[38:39], 0\n"
"v_cvt_pk_bf16_f32 v125, v126, v127\n"
"v_mfma_f32_16x16x32_bf16 v[172:175], v[216:219], a[224:227], v[172:175]\n"
"buffer_load_dwordx4 v240, s[28:31], s40 offen offset:2048 lds\n"
"v_mfma_f32_16x16x32_bf16 v[172:175], v[212:215], a[212:215], v[172:175]\n"
"s_add_u32 s40, s41, s54\n"
"v_mov_b64 v[202:203], 0\n"
"v_bfe_u32 v254, v242, 4, 4\n"
"v_mfma_f32_16x16x32_bf16 v[172:175], v[208:211], a[208:211], v[172:175]\n"
"buffer_load_dwordx4 v240, s[28:31], s40 offen offset:3072 lds\n"
"v_mfma_f32_16x16x32_bf16 v[172:175], v[192:195], a[236:239], v[172:175]\n"
"s_mul_i32 s62, s70, 224\n"
"v_mfma_f32_16x16x32_bf16 v[172:175], v[188:191], a[232:235], v[172:175]\n"
"s_add_u32 s20, s47, 3\n"
"v_mov_b64 v[40:41], 0\n"
"v_mfma_f32_16x16x32_bf16 v[172:175], v[184:187], a[220:223], v[172:175]\n"
"s_add_u32 s70, s61, s20\n"
"ds_read_b64_tr_b16 v[224:225], v233 offset:57344\n"
"ds_read_b64_tr_b16 v[226:227], v233 offset:59392\n"
"v_mfma_f32_16x16x32_bf16 v[172:175], v[180:183], a[216:219], v[172:175]\n"
"s_and_b32 s70, s70, 7\n"
"v_mfma_f32_16x16x32_bf16 v[128:131], v[96:99], a[48:51], v[128:131]\n"
"s_waitcnt lgkmcnt(2)\n"
"s_cmp_ge_u32 s47, 5\ns_cselect_b32 s70, s20, s70\n"
"ds_read_b64_tr_b16 v[220:221], v234 offset:57344\n"
"v_add_u32 v236, s94, v243\n"
"v_mfma_f32_16x16x32_bf16 v[128:131], v[100:103], a[52:55], v[128:131]\n"
"s_cmp_eq_u32 s47, 7\ns_cselect_b32 s70, s61, s70\n"
"v_cvt_pk_bf16_f32 v140, v140, v141\n"
"v_xor_b32 v232, s75, v232\n"
"v_mfma_f32_16x16x32_bf16 v[128:131], v[104:107], a[56:59], v[128:131]\n"
"s_lshl_b32 s70, s70, 7\n"
"ds_read_b64_tr_b16 v[208:209], v230 offset:49152\n"
"v_bfe_u32 v237, v242, 4, 3\n"
"v_mfma_f32_16x16x32_bf16 v[128:131], v[108:111], a[60:63], v[128:131]\n"
"s_xor_b32 s71, s71, s75\n"
"v_mov_b64 v[34:35], 0\n"
"v_mfma_f32_16x16x32_bf16 v[128:131], v[112:115], a[0:3], v[128:131]\n"
"v_lshlrev_b32_e64 v245, 2, s72\n"
"ds_read_b64_tr_b16 v[186:187], v234 offset:34816\n"
"v_mov_b64 v[14:15], 0\n"
"v_mfma_f32_16x16x32_bf16 v[128:131], v[116:119], a[4:7], v[128:131]\n"
"global_load_dword v244, v245, s[6:7] sc1\n"
"v_mfma_f32_16x16x32_bf16 v[128:131], v[120:123], a[8:11], v[128:131]\n"
"s_waitcnt lgkmcnt(3)\n"
"v_mul_u32_u24 v254, 2560, v254\n"
"v_cvt_pk_bf16_f32 v156, v156, v157\n"
"v_mfma_f32_16x16x32_bf16 v[128:131], v[224:227], a[12:15], v[128:131]\n"
"v_mov_b64 v[6:7], 0\n"
"v_mfma_f32_16x16x32_bf16 v[144:147], v[224:227], a[28:31], v[144:147]\n"
"ds_read_b64_tr_b16 v[182:183], v230 offset:34816\n"
"v_mov_b64 v[20:21], 0\n"
"v_mfma_f32_16x16x32_bf16 v[144:147], v[120:123], a[24:27], v[144:147]\n"
"ds_read_b64_tr_b16 v[210:211], v230 offset:51200\n"
"v_mov_b64 v[10:11], 0\n"
"v_mfma_f32_16x16x32_bf16 v[144:147], v[116:119], a[20:23], v[144:147]\n"
"v_add_u32 v243, s96, v243\n"
"v_mfma_f32_16x16x32_bf16 v[144:147], v[112:115], a[16:19], v[144:147]\n"
"ds_read_b64_tr_b16 v[222:223], v234 offset:59392\n"
"v_mfma_f32_16x16x32_bf16 v[144:147], v[108:111], a[76:79], v[144:147]\n"
"v_mfma_f32_16x16x32_bf16 v[144:147], v[104:107], a[72:75], v[144:147]\n"
"ds_read_b64_tr_b16 v[212:213], v234 offset:49152\n"
"v_mov_b64 v[12:13], 0\n"
"v_mfma_f32_16x16x32_bf16 v[144:147], v[100:103], a[68:71], v[144:147]\n"
"ds_read_b64_tr_b16 v[218:219], v230 offset:59392\n"
"v_mfma_f32_16x16x32_bf16 v[144:147], v[96:99], a[64:67], v[144:147]\n"
"ds_read_b64_tr_b16 v[192:193], v234 offset:40960\n"
"v_mfma_f32_16x16x32_bf16 v[160:163], v[96:99], a[80:83], v[160:163]\n"
"v_mov_b64 v[196:197], 0\n"
"v_mfma_f32_16x16x32_bf16 v[160:163], v[100:103], a[84:87], v[160:163]\n"
"ds_read_b64_tr_b16 v[188:189], v230 offset:40960\n"
"v_cvt_pk_bf16_f32 v127, v130, v131\n"
"v_mfma_f32_16x16x32_bf16 v[160:163], v[104:107], a[88:91], v[160:163]\n"
"v_cvt_pk_bf16_f32 v126, v128, v129\n"
"v_mfma_f32_16x16x32_bf16 v[160:163], v[108:111], a[92:95], v[160:163]\n"
"ds_read_b64_tr_b16 v[194:195], v234 offset:43008\n"
"v_cvt_pk_bf16_f32 v141, v142, v143\n"
"v_mfma_f32_16x16x32_bf16 v[160:163], v[112:115], a[32:35], v[160:163]\n"
"ds_read_b64_tr_b16 v[184:185], v234 offset:32768\n"
"v_mfma_f32_16x16x32_bf16 v[160:163], v[116:119], a[36:39], v[160:163]\n"
"v_mov_b32 v241, s60\n"
"v_mfma_f32_16x16x32_bf16 v[160:163], v[120:123], a[40:43], v[160:163]\n"
"ds_read_b64_tr_b16 v[180:181], v230 offset:32768\n"
"v_mov_b64 v[8:9], 0\n"
"v_mfma_f32_16x16x32_bf16 v[160:163], v[224:227], a[44:47], v[160:163]\n"
"buffer_store_dwordx4 v[124:127], v243, s[32:35], s16 offen sc0 sc1\n"
"v_mfma_f32_16x16x32_bf16 v[176:179], v[224:227], a[228:231], v[176:179]\n"
"ds_read_b64_tr_b16 v[190:191], v230 offset:43008\n"
"v_mov_b64 v[46:47], 0\n"
"v_mfma_f32_16x16x32_bf16 v[176:179], v[120:123], a[224:227], v[176:179]\n"
"v_cvt_pk_bf16_f32 v157, v158, v159\n"
"v_mfma_f32_16x16x32_bf16 v[176:179], v[116:119], a[212:215], v[176:179]\n"
"ds_read_b64_tr_b16 v[216:217], v230 offset:57344\n"
"v_mov_b64 v[52:53], 0\n"
"v_mfma_f32_16x16x32_bf16 v[176:179], v[112:115], a[208:211], v[176:179]\n"
"v_cvt_pk_bf16_f32 v143, v146, v147\n"
"v_mfma_f32_16x16x32_bf16 v[176:179], v[108:111], a[236:239], v[176:179]\n"
"ds_read_b64_tr_b16 v[214:215], v234 offset:51200\n"
"v_mfma_f32_16x16x32_bf16 v[176:179], v[104:107], a[232:235], v[176:179]\n"
"ds_read_b64_tr_b16 v[224:225], v235 offset:57344\n"
"v_mov_b64 v[48:49], 0\n"
"v_mfma_f32_16x16x32_bf16 v[176:179], v[100:103], a[220:223], v[176:179]\n"
"v_cvt_pk_bf16_f32 v142, v144, v145\n"
"v_mfma_f32_16x16x32_bf16 v[176:179], v[96:99], a[216:219], v[176:179]\n"
"s_waitcnt lgkmcnt(4)\n"
"ds_read_b64_tr_b16 v[112:113], v231 offset:49152\n"
"v_mfma_f32_16x16x32_bf16 v[132:135], v[180:183], a[48:51], v[132:135]\n"
"v_mov_b64 v[44:45], 0\n"
"v_mfma_f32_16x16x32_bf16 v[132:135], v[184:187], a[52:55], v[132:135]\n"
"s_waitcnt lgkmcnt(4)\n"
"ds_read_b64_tr_b16 v[110:111], v235 offset:43008\n"
"v_mfma_f32_16x16x32_bf16 v[132:135], v[188:191], a[56:59], v[132:135]\n"
"v_mfma_f32_16x16x32_bf16 v[132:135], v[192:195], a[60:63], v[132:135]\n"
"ds_read_b64_tr_b16 v[102:103], v235 offset:34816\n"
"v_mov_b64 v[4:5], 0\n"
"v_mfma_f32_16x16x32_bf16 v[132:135], v[208:211], a[0:3], v[132:135]\n"
"s_waitcnt lgkmcnt(4)\n"
"ds_read_b64_tr_b16 v[98:99], v231 offset:34816\n"
"v_mfma_f32_16x16x32_bf16 v[132:135], v[212:215], a[4:7], v[132:135]\n"
"v_mfma_f32_16x16x32_bf16 v[132:135], v[216:219], a[8:11], v[132:135]\n"
"ds_read_b64_tr_b16 v[120:121], v231 offset:57344\n"
"v_mfma_f32_16x16x32_bf16 v[132:135], v[220:223], a[12:15], v[132:135]\n"
"buffer_store_dwordx4 v[140:143], v243, s[32:35], s16 offen offset:2048 sc0 sc1\n"
"v_mfma_f32_16x16x32_bf16 v[148:151], v[220:223], a[28:31], v[148:151]\n"
"ds_read_b64_tr_b16 v[96:97], v231 offset:32768\n"
"v_mfma_f32_16x16x32_bf16 v[148:151], v[216:219], a[24:27], v[148:151]\n"
"v_mfma_f32_16x16x32_bf16 v[148:151], v[212:215], a[20:23], v[148:151]\n"
"ds_read_b64_tr_b16 v[106:107], v231 offset:43008\n"
"v_max_u32 v237, 1, v237\n"
"v_mfma_f32_16x16x32_bf16 v[148:151], v[208:211], a[16:19], v[148:151]\n"
"ds_read_b64_tr_b16 v[114:115], v231 offset:51200\n"
"v_mfma_f32_16x16x32_bf16 v[148:151], v[192:195], a[76:79], v[148:151]\n"
"ds_read_b64_tr_b16 v[108:109], v235 offset:40960\n"
"v_mfma_f32_16x16x32_bf16 v[148:151], v[188:191], a[72:75], v[148:151]\n"
"v_lshl_add_u32 v254, v255, 5, v254\n"
"v_cvt_pk_bf16_f32 v158, v160, v161\n"
"v_mfma_f32_16x16x32_bf16 v[148:151], v[184:187], a[68:71], v[148:151]\n"
"ds_read_b64_tr_b16 v[100:101], v235 offset:32768\n"
"v_mfma_f32_16x16x32_bf16 v[148:151], v[180:183], a[64:67], v[148:151]\n"
"v_bfe_u32 v255, v242, 9, 1\n"
"v_mov_b64 v[24:25], 0\n"
"v_mfma_f32_16x16x32_bf16 v[164:167], v[180:183], a[80:83], v[164:167]\n"
"ds_read_b64_tr_b16 v[116:117], v235 offset:49152\n"
"v_mfma_f32_16x16x32_bf16 v[164:167], v[184:187], a[84:87], v[164:167]\n"
"ds_read_b64_tr_b16 v[104:105], v231 offset:40960\n"
"v_xor_b32 v246, s75, v246\n"
"v_mfma_f32_16x16x32_bf16 v[164:167], v[188:191], a[88:91], v[164:167]\n"
"ds_read_b64_tr_b16 v[118:119], v235 offset:51200\n"
"v_mfma_f32_16x16x32_bf16 v[164:167], v[192:195], a[92:95], v[164:167]\n"
"v_lshl_add_u32 v254, v255, 4, v254\n"
"v_cvt_pk_bf16_f32 v159, v162, v163\n"
"v_mfma_f32_16x16x32_bf16 v[164:167], v[208:211], a[32:35], v[164:167]\n"
"ds_read_b64_tr_b16 v[122:123], v231 offset:59392\n"
"v_xor_b32 v228, s75, v228\n"
"v_mfma_f32_16x16x32_bf16 v[164:167], v[212:215], a[36:39], v[164:167]\n"
"v_mov_b64 v[22:23], 0\n"
"v_mfma_f32_16x16x32_bf16 v[164:167], v[216:219], a[40:43], v[164:167]\n"
"ds_read_b64_tr_b16 v[226:227], v235 offset:59392\n"
"v_mfma_f32_16x16x32_bf16 v[164:167], v[220:223], a[44:47], v[164:167]\n"
"s_barrier\n"
"s_waitcnt lgkmcnt(5)\n"
"v_mfma_f32_16x16x32_bf16 v[136:139], v[96:99], a[48:51], v[136:139]\n"
"ds_read_b64_tr_b16 v[194:195], v232 offset:10240\n"
"v_mfma_f32_16x16x32_bf16 v[136:139], v[100:103], a[52:55], v[136:139]\n"
"s_waitcnt lgkmcnt(4)\n"
"ds_read_b64_tr_b16 v[220:221], v232 offset:24576\n"
"v_mfma_f32_16x16x32_bf16 v[136:139], v[104:107], a[56:59], v[136:139]\n"
"ds_read_b64_tr_b16 v[184:185], v232 offset:0\n"
"v_mov_b64 v[54:55], 0\n"
"v_mfma_f32_16x16x32_bf16 v[136:139], v[108:111], a[60:63], v[136:139]\n"
"ds_read_b64_tr_b16 v[186:187], v232 offset:2048\n"
"v_mfma_f32_16x16x32_bf16 v[136:139], v[112:115], a[0:3], v[136:139]\n"
"s_waitcnt lgkmcnt(5)\n"
"ds_read_b64_tr_b16 v[212:213], v232 offset:16384\n"
"v_mfma_f32_16x16x32_bf16 v[136:139], v[116:119], a[4:7], v[136:139]\n"
"ds_read_b64_tr_b16 v[210:211], v228 offset:18432\n"
"v_mov_b64 v[50:51], 0\n"
"v_mfma_f32_16x16x32_bf16 v[136:139], v[120:123], a[8:11], v[136:139]\n"
"s_waitcnt lgkmcnt(6)\n"
"ds_read_b64_tr_b16 v[208:209], v228 offset:16384\n"
"v_mfma_f32_16x16x32_bf16 v[136:139], v[224:227], a[12:15], v[136:139]\n"
"buffer_store_dwordx4 v[156:159], v243, s[32:35], s17 offen sc0 sc1\n"
"ds_read_b64_tr_b16 v[182:183], v228 offset:2048\n"
"v_mfma_f32_16x16x32_bf16 v[152:155], v[224:227], a[28:31], v[152:155]\n"
"ds_read_b64_tr_b16 v[218:219], v228 offset:26624\n"
"v_add_u32 v238, s97, v254\n"
"v_mfma_f32_16x16x32_bf16 v[152:155], v[120:123], a[24:27], v[152:155]\n"
"ds_read_b64_tr_b16 v[216:217], v228 offset:24576\n"
"v_mfma_f32_16x16x32_bf16 v[152:155], v[116:119], a[20:23], v[152:155]\n"
"ds_read_b64_tr_b16 v[190:191], v228 offset:10240\n"
"v_mfma_f32_16x16x32_bf16 v[152:155], v[112:115], a[16:19], v[152:155]\n"
"ds_read_b64_tr_b16 v[192:193], v232 offset:8192\n"
"v_mfma_f32_16x16x32_bf16 v[152:155], v[108:111], a[76:79], v[152:155]\n"
"ds_read_b64_tr_b16 v[214:215], v232 offset:18432\n"
"v_mov_b64 v[16:17], 0\n"
"v_mfma_f32_16x16x32_bf16 v[152:155], v[104:107], a[72:75], v[152:155]\n"
"ds_read_b64_tr_b16 v[222:223], v232 offset:26624\n"
"v_mfma_f32_16x16x32_bf16 v[152:155], v[100:103], a[68:71], v[152:155]\n"
"ds_read_b64_tr_b16 v[188:189], v228 offset:8192\n"
"v_mfma_f32_16x16x32_bf16 v[152:155], v[96:99], a[64:67], v[152:155]\n"
"ds_read_b64_tr_b16 v[180:181], v228 offset:0\n"
"v_add_u32 v253, s99, v254\n"
"v_mfma_f32_16x16x32_bf16 v[168:171], v[96:99], a[80:83], v[168:171]\n"
"v_mov_b64 v[28:29], 0\n"
"v_mov_b64 v[42:43], 0\n"
"v_mfma_f32_16x16x32_bf16 v[168:171], v[100:103], a[84:87], v[168:171]\n"
"v_mfma_f32_16x16x32_bf16 v[168:171], v[104:107], a[88:91], v[168:171]\n"
"v_mov_b64 v[198:199], 0\n"
"v_mov_b64 v[30:31], 0\n"
"v_mfma_f32_16x16x32_bf16 v[168:171], v[108:111], a[92:95], v[168:171]\n"
"v_xor_b32 v239, 64, v238\n"
"v_mfma_f32_16x16x32_bf16 v[168:171], v[112:115], a[32:35], v[168:171]\n"
"v_xor_b32 v237, s61, v237\n"
"v_mfma_f32_16x16x32_bf16 v[168:171], v[116:119], a[36:39], v[168:171]\n"
"v_lshlrev_b32 v237, 2, v237\n"
"v_mfma_f32_16x16x32_bf16 v[168:171], v[120:123], a[40:43], v[168:171]\n"
"s_add_u32 s47, s47, 1\n"
"v_mfma_f32_16x16x32_bf16 v[168:171], v[224:227], a[44:47], v[168:171]\n"
"s_waitcnt lgkmcnt(0)\n"
"v_add_u32 v252, s98, v254\n"
"LGL_%=:\n"
"v_mfma_f32_16x16x32_bf16 v[124:127], v[180:183], a[96:99], 0\n"
"v_cvt_pk_bf16_f32 v172, v172, v173\n"
"v_cvt_pk_bf16_f32 v173, v174, v175\n"
"v_mfma_f32_16x16x32_bf16 v[124:127], v[184:187], a[100:103], v[124:127]\n"
"v_cvt_pk_bf16_f32 v174, v176, v177\n"
"v_cvt_pk_bf16_f32 v175, v178, v179\n"
"v_mfma_f32_16x16x32_bf16 v[124:127], v[188:191], a[104:107], v[124:127]\n"
"v_cvt_pk_bf16_f32 v132, v132, v133\n"
"v_cvt_pk_bf16_f32 v133, v134, v135\n"
"v_mfma_f32_16x16x32_bf16 v[124:127], v[192:195], a[108:111], v[124:127]\n"
"v_cvt_pk_bf16_f32 v134, v136, v137\n"
"v_cvt_pk_bf16_f32 v135, v138, v139\n"
"v_mfma_f32_16x16x32_bf16 v[124:127], v[208:211], a[144:147], v[124:127]\n"
"v_xor_b32 v229, s75, v229\n"
"v_xor_b32 v233, s75, v233\n"
"v_mfma_f32_16x16x32_bf16 v[124:127], v[212:215], a[148:151], v[124:127]\n"
"s_waitcnt lgkmcnt(0)\n"
"ds_read_b64_tr_b16 v[96:97], v229 offset:0\n"
"v_cvt_pk_bf16_f32 v148, v148, v149\n"
"v_mfma_f32_16x16x32_bf16 v[124:127], v[216:219], a[152:155], v[124:127]\n"
"ds_read_b64_tr_b16 v[98:99], v229 offset:2048\n"
"v_cvt_pk_bf16_f32 v149, v150, v151\n"
"v_mfma_f32_16x16x32_bf16 v[124:127], v[220:223], a[156:159], v[124:127]\n"
"buffer_store_dwordx4 v[172:175], v243, s[32:35], s17 offen offset:2048 sc0 sc1\n"
"v_cvt_pk_bf16_f32 v150, v152, v153\n"
"v_mfma_f32_16x16x32_bf16 v[140:143], v[220:223], a[172:175], 0\n"
"ds_read_b64_tr_b16 v[100:101], v233 offset:0\n"
"v_cvt_pk_bf16_f32 v151, v154, v155\n"
"v_mfma_f32_16x16x32_bf16 v[140:143], v[216:219], a[168:171], v[140:143]\n"
"ds_read_b64_tr_b16 v[102:103], v233 offset:2048\n"
"v_xor_b32 v230, s75, v230\n"
"v_mfma_f32_16x16x32_bf16 v[140:143], v[212:215], a[164:167], v[140:143]\n"
"ds_read_b64_tr_b16 v[104:105], v229 offset:8192\n"
"v_xor_b32 v234, s75, v234\n"
"v_mfma_f32_16x16x32_bf16 v[140:143], v[208:211], a[160:163], v[140:143]\n"
"ds_read_b64_tr_b16 v[106:107], v229 offset:10240\n"
"v_cvt_pk_bf16_f32 v164, v164, v165\n"
"v_mfma_f32_16x16x32_bf16 v[140:143], v[192:195], a[124:127], v[140:143]\n"
"ds_read_b64_tr_b16 v[108:109], v233 offset:8192\n"
"v_cvt_pk_bf16_f32 v165, v166, v167\n"
"v_mfma_f32_16x16x32_bf16 v[140:143], v[188:191], a[120:123], v[140:143]\n"
"s_add_u32 s41, s70, 0\n"
"ds_read_b64_tr_b16 v[110:111], v233 offset:10240\n"
"v_cvt_pk_bf16_f32 v166, v168, v169\n"
"v_mfma_f32_16x16x32_bf16 v[140:143], v[184:187], a[116:119], v[140:143]\n"
"s_mov_b32 m0, s71\n"
"ds_read_b64_tr_b16 v[112:113], v229 offset:16384\n"
"v_cvt_pk_bf16_f32 v167, v170, v171\n"
"v_mfma_f32_16x16x32_bf16 v[140:143], v[180:183], a[112:115], v[140:143]\n"
"buffer_load_dwordx4 v240, s[28:31], s41 offen lds\n"
"v_mfma_f32_16x16x32_bf16 v[156:159], v[180:183], a[128:131], 0\n"
"ds_read_b64_tr_b16 v[114:115], v229 offset:18432\n"
"v_xor_b32 v231, s75, v231\n"
"v_mfma_f32_16x16x32_bf16 v[156:159], v[184:187], a[132:135], v[156:159]\n"
"ds_read_b64_tr_b16 v[116:117], v233 offset:16384\n"
"v_xor_b32 v235, s75, v235\n"
"v_mfma_f32_16x16x32_bf16 v[156:159], v[188:191], a[136:139], v[156:159]\n"
"ds_read_b64_tr_b16 v[118:119], v233 offset:18432\n"
"v_add_u32 v241, 1, v241\n"
"v_mfma_f32_16x16x32_bf16 v[156:159], v[192:195], a[140:143], v[156:159]\n"
"ds_read_b64_tr_b16 v[120:121], v229 offset:24576\n"
"v_mfma_f32_16x16x32_bf16 v[156:159], v[208:211], a[176:179], v[156:159]\n"
"ds_read_b64_tr_b16 v[122:123], v229 offset:26624\n"
"v_mfma_f32_16x16x32_bf16 v[156:159], v[212:215], a[180:183], v[156:159]\n"
"ds_read_b64_tr_b16 v[224:225], v233 offset:24576\n"
"v_mfma_f32_16x16x32_bf16 v[156:159], v[216:219], a[184:187], v[156:159]\n"
"ds_read_b64_tr_b16 v[226:227], v233 offset:26624\n"
"v_mfma_f32_16x16x32_bf16 v[156:159], v[220:223], a[188:191], v[156:159]\n"
"buffer_store_dwordx4 v[132:135], v243, s[32:35], s16 offen offset:1024 sc0 sc1\n"
"v_mfma_f32_16x16x32_bf16 v[172:175], v[220:223], a[252:255], 0\n"
"ds_read_b64_tr_b16 v[220:221], v234 offset:24576\n"
"v_mfma_f32_16x16x32_bf16 v[172:175], v[216:219], a[248:251], v[172:175]\n"
"ds_read_b64_tr_b16 v[216:217], v230 offset:24576\n"
"v_mfma_f32_16x16x32_bf16 v[172:175], v[212:215], a[244:247], v[172:175]\n"
"ds_read_b64_tr_b16 v[212:213], v234 offset:16384\n"
"v_mfma_f32_16x16x32_bf16 v[172:175], v[208:211], a[240:243], v[172:175]\n"
"ds_read_b64_tr_b16 v[208:209], v230 offset:16384\n"
"v_mfma_f32_16x16x32_bf16 v[172:175], v[192:195], a[204:207], v[172:175]\n"
"ds_read_b64_tr_b16 v[192:193], v234 offset:8192\n"
"v_mfma_f32_16x16x32_bf16 v[172:175], v[188:191], a[200:203], v[172:175]\n"
"ds_read_b64_tr_b16 v[188:189], v230 offset:8192\n"
"v_mfma_f32_16x16x32_bf16 v[172:175], v[184:187], a[196:199], v[172:175]\n"
"s_add_u32 s40, s41, s52\n"
"ds_read_b64_tr_b16 v[184:185], v234 offset:0\n"
"v_mfma_f32_16x16x32_bf16 v[172:175], v[180:183], a[192:195], v[172:175]\n"
"s_waitcnt lgkmcnt(7)\n"
"buffer_load_dwordx4 v240, s[28:31], s40 offen offset:1024 lds\n"
"v_mfma_f32_16x16x32_bf16 v[128:131], v[96:99], a[96:99], 0\n"
"ds_read_b64_tr_b16 v[180:181], v230 offset:0\n"
"v_mfma_f32_16x16x32_bf16 v[128:131], v[100:103], a[100:103], v[128:131]\n"
"ds_read_b64_tr_b16 v[182:183], v230 offset:2048\n"
"v_mfma_f32_16x16x32_bf16 v[128:131], v[104:107], a[104:107], v[128:131]\n"
"ds_read_b64_tr_b16 v[186:187], v234 offset:2048\n"
"v_mfma_f32_16x16x32_bf16 v[128:131], v[108:111], a[108:111], v[128:131]\n"
"ds_read_b64_tr_b16 v[190:191], v230 offset:10240\n"
"v_mfma_f32_16x16x32_bf16 v[128:131], v[112:115], a[144:147], v[128:131]\n"
"s_waitcnt vmcnt(6)\n"
"ds_read_b64_tr_b16 v[194:195], v234 offset:10240\n"
"v_mfma_f32_16x16x32_bf16 v[128:131], v[116:119], a[148:151], v[128:131]\n"
"v_subrev_u32 v244, s21, v244\n"
"ds_read_b64_tr_b16 v[210:211], v230 offset:18432\n"
"v_mfma_f32_16x16x32_bf16 v[128:131], v[120:123], a[152:155], v[128:131]\n"
"v_cmp_lt_u32 vcc, s69, v244\n"
"ds_read_b64_tr_b16 v[214:215], v234 offset:18432\n"
"v_mfma_f32_16x16x32_bf16 v[128:131], v[224:227], a[156:159], v[128:131]\n"
"ds_read_b64_tr_b16 v[218:219], v230 offset:26624\n"
"s_cbranch_vccnz LGP_%=\nLGPK_%=:\n"
"v_mfma_f32_16x16x32_bf16 v[144:147], v[224:227], a[172:175], 0\n"
"s_sub_u32 s72, s72, 1\n"
"ds_read_b64_tr_b16 v[222:223], v234 offset:26624\n"
"v_mfma_f32_16x16x32_bf16 v[144:147], v[120:123], a[168:171], v[144:147]\n"
"s_and_b32 s72, s72, 7\n"
"v_mfma_f32_16x16x32_bf16 v[144:147], v[116:119], a[164:167], v[144:147]\n"
"v_mfma_f32_16x16x32_bf16 v[144:147], v[112:115], a[160:163], v[144:147]\n"
"buffer_load_dwordx4 v[72:75], v243, s[32:35], s18 offen sc1\n"
"v_mfma_f32_16x16x32_bf16 v[144:147], v[108:111], a[124:127], v[144:147]\n"
"v_mfma_f32_16x16x32_bf16 v[144:147], v[104:107], a[120:123], v[144:147]\n"
"v_mfma_f32_16x16x32_bf16 v[144:147], v[100:103], a[116:119], v[144:147]\n"
"v_mfma_f32_16x16x32_bf16 v[144:147], v[96:99], a[112:115], v[144:147]\n"
"buffer_store_dwordx4 v[148:151], v243, s[32:35], s16 offen offset:3072 sc0 sc1\n"
"v_mfma_f32_16x16x32_bf16 v[160:163], v[96:99], a[128:131], 0\n"
"v_mfma_f32_16x16x32_bf16 v[160:163], v[100:103], a[132:135], v[160:163]\n"
"v_mfma_f32_16x16x32_bf16 v[160:163], v[104:107], a[136:139], v[160:163]\n"
"v_mfma_f32_16x16x32_bf16 v[160:163], v[108:111], a[140:143], v[160:163]\n"
"v_mfma_f32_16x16x32_bf16 v[160:163], v[112:115], a[176:179], v[160:163]\n"
"v_mfma_f32_16x16x32_bf16 v[160:163], v[116:119], a[180:183], v[160:163]\n"
"v_mfma_f32_16x16x32_bf16 v[160:163], v[120:123], a[184:187], v[160:163]\n"
"s_add_u32 s40, s41, s53\n"
"v_mfma_f32_16x16x32_bf16 v[160:163], v[224:227], a[188:191], v[160:163]\n"
"buffer_load_dwordx4 v240, s[28:31], s40 offen offset:2048 lds\n"
"v_mfma_f32_16x16x32_bf16 v[176:179], v[224:227], a[252:255], 0\n"
"ds_read_b64_tr_b16 v[224:225], v235 offset:24576\n"
"v_mfma_f32_16x16x32_bf16 v[176:179], v[120:123], a[248:251], v[176:179]\n"
"ds_read_b64_tr_b16 v[120:121], v231 offset:24576\n"
"v_mfma_f32_16x16x32_bf16 v[176:179], v[116:119], a[244:247], v[176:179]\n"
"ds_read_b64_tr_b16 v[116:117], v235 offset:16384\n"
"v_mfma_f32_16x16x32_bf16 v[176:179], v[112:115], a[240:243], v[176:179]\n"
"buffer_load_dwordx4 v[76:79], v243, s[32:35], s18 offen offset:1024 sc1\n"
"v_mfma_f32_16x16x32_bf16 v[176:179], v[108:111], a[204:207], v[176:179]\n"
"ds_read_b64_tr_b16 v[108:109], v235 offset:8192\n"
"v_mfma_f32_16x16x32_bf16 v[176:179], v[104:107], a[200:203], v[176:179]\n"
"ds_read_b64_tr_b16 v[104:105], v231 offset:8192\n"
"v_mfma_f32_16x16x32_bf16 v[176:179], v[100:103], a[196:199], v[176:179]\n"
"s_add_u32 s40, s41, s54\n"
"ds_read_b64_tr_b16 v[100:101], v235 offset:0\n"
"v_mfma_f32_16x16x32_bf16 v[176:179], v[96:99], a[192:195], v[176:179]\n"
"s_waitcnt lgkmcnt(6)\n"
"buffer_load_dwordx4 v240, s[28:31], s40 offen offset:3072 lds\n"
"v_mfma_f32_16x16x32_bf16 v[132:135], v[180:183], a[96:99], 0\n"
"ds_read_b64_tr_b16 v[96:97], v231 offset:0\n"
"v_mfma_f32_16x16x32_bf16 v[132:135], v[184:187], a[100:103], v[132:135]\n"
"ds_read_b64_tr_b16 v[98:99], v231 offset:2048\n"
"v_mfma_f32_16x16x32_bf16 v[132:135], v[188:191], a[104:107], v[132:135]\n"
"ds_read_b64_tr_b16 v[102:103], v235 offset:2048\n"
"v_mfma_f32_16x16x32_bf16 v[132:135], v[192:195], a[108:111], v[132:135]\n"
"ds_read_b64_tr_b16 v[106:107], v231 offset:10240\n"
"v_mfma_f32_16x16x32_bf16 v[132:135], v[208:211], a[144:147], v[132:135]\n"
"ds_read_b64_tr_b16 v[110:111], v235 offset:10240\n"
"v_mfma_f32_16x16x32_bf16 v[132:135], v[212:215], a[148:151], v[132:135]\n"
"ds_read_b64_tr_b16 v[112:113], v231 offset:16384\n"
"v_mfma_f32_16x16x32_bf16 v[132:135], v[216:219], a[152:155], v[132:135]\n"
"ds_read_b64_tr_b16 v[114:115], v231 offset:18432\n"
"v_mfma_f32_16x16x32_bf16 v[132:135], v[220:223], a[156:159], v[132:135]\n"
"buffer_store_dwordx4 v[164:167], v243, s[32:35], s17 offen offset:1024 sc0 sc1\n"
"v_mfma_f32_16x16x32_bf16 v[148:151], v[220:223], a[172:175], 0\n"
"ds_read_b64_tr_b16 v[118:119], v235 offset:18432\n"
"v_mfma_f32_16x16x32_bf16 v[148:151], v[216:219], a[168:171], v[148:151]\n"
"ds_read_b64_tr_b16 v[122:123], v231 offset:26624\n"
"v_mfma_f32_16x16x32_bf16 v[148:151], v[212:215], a[164:167], v[148:151]\n"
"ds_read_b64_tr_b16 v[226:227], v235 offset:26624\n"
"v_mfma_f32_16x16x32_bf16 v[148:151], v[208:211], a[160:163], v[148:151]\n"
"buffer_load_dwordx4 v[80:83], v243, s[32:35], s18 offen offset:2048 sc1\n"
"v_mfma_f32_16x16x32_bf16 v[148:151], v[192:195], a[124:127], v[148:151]\n"
"v_mfma_f32_16x16x32_bf16 v[148:151], v[188:191], a[120:123], v[148:151]\n"
"s_add_u32 s41, s70, 163840\n"
"v_mfma_f32_16x16x32_bf16 v[148:151], v[184:187], a[116:119], v[148:151]\n"
"s_add_u32 m0, s71, 16384\n"
"v_mfma_f32_16x16x32_bf16 v[148:151], v[180:183], a[112:115], v[148:151]\n"
"buffer_load_dwordx4 v240, s[28:31], s41 offen lds\n"
"v_mfma_f32_16x16x32_bf16 v[164:167], v[180:183], a[128:131], 0\n"
"v_mfma_f32_16x16x32_bf16 v[164:167], v[184:187], a[132:135], v[164:167]\n"
"v_mfma_f32_16x16x32_bf16 v[164:167], v[188:191], a[136:139], v[164:167]\n"
"v_mfma_f32_16x16x32_bf16 v[164:167], v[192:195], a[140:143], v[164:167]\n"
"v_mfma_f32_16x16x32_bf16 v[164:167], v[208:211], a[176:179], v[164:167]\n"
"v_mfma_f32_16x16x32_bf16 v[164:167], v[212:215], a[180:183], v[164:167]\n"
"v_mfma_f32_16x16x32_bf16 v[164:167], v[216:219], a[184:187], v[164:167]\n"
"s_waitcnt vmcnt(12)\n"
"v_mfma_f32_16x16x32_bf16 v[164:167], v[220:223], a[188:191], v[164:167]\n"
"s_barrier\n"
"s_waitcnt lgkmcnt(0)\n"
"v_mfma_f32_16x16x32_bf16 v[136:139], v[96:99], a[96:99], 0\n"
"ds_read_b64_tr_b16 v[180:181], v228 offset:32768\n"
"v_mfma_f32_16x16x32_bf16 v[136:139], v[100:103], a[100:103], v[136:139]\n"
"ds_read_b64_tr_b16 v[182:183], v228 offset:34816\n"
"v_mfma_f32_16x16x32_bf16 v[136:139], v[104:107], a[104:107], v[136:139]\n"
"ds_read_b64_tr_b16 v[184:185], v232 offset:32768\n"
"v_mfma_f32_16x16x32_bf16 v[136:139], v[108:111], a[108:111], v[136:139]\n"
"buffer_load_dwordx4 v[84:87], v243, s[32:35], s18 offen offset:3072 sc1\n"
"v_mfma_f32_16x16x32_bf16 v[136:139], v[112:115], a[144:147], v[136:139]\n"
"ds_read_b64_tr_b16 v[186:187], v232 offset:34816\n"
"v_mfma_f32_16x16x32_bf16 v[136:139], v[116:119], a[148:151], v[136:139]\n"
"ds_read_b64_tr_b16 v[188:189], v228 offset:40960\n"
"v_mfma_f32_16x16x32_bf16 v[136:139], v[120:123], a[152:155], v[136:139]\n"
"s_add_u32 s40, s41, s52\n"
"ds_read_b64_tr_b16 v[190:191], v228 offset:43008\n"
"v_mfma_f32_16x16x32_bf16 v[136:139], v[224:227], a[156:159], v[136:139]\n"
"buffer_load_dwordx4 v240, s[28:31], s40 offen offset:1024 lds\n"
"v_mfma_f32_16x16x32_bf16 v[152:155], v[224:227], a[172:175], 0\n"
"ds_read_b64_tr_b16 v[192:193], v232 offset:40960\n"
"v_mfma_f32_16x16x32_bf16 v[152:155], v[120:123], a[168:171], v[152:155]\n"
"ds_read_b64_tr_b16 v[194:195], v232 offset:43008\n"
"v_mfma_f32_16x16x32_bf16 v[152:155], v[116:119], a[164:167], v[152:155]\n"
"ds_read_b64_tr_b16 v[208:209], v228 offset:49152\n"
"v_mfma_f32_16x16x32_bf16 v[152:155], v[112:115], a[160:163], v[152:155]\n"
"ds_read_b64_tr_b16 v[210:211], v228 offset:51200\n"
"v_mfma_f32_16x16x32_bf16 v[152:155], v[108:111], a[124:127], v[152:155]\n"
"ds_read_b64_tr_b16 v[212:213], v232 offset:49152\n"
"v_mfma_f32_16x16x32_bf16 v[152:155], v[104:107], a[120:123], v[152:155]\n"
"ds_read_b64_tr_b16 v[214:215], v232 offset:51200\n"
"v_mfma_f32_16x16x32_bf16 v[152:155], v[100:103], a[116:119], v[152:155]\n"
"s_add_u32 s40, s41, s53\n"
"ds_read_b64_tr_b16 v[216:217], v228 offset:57344\n"
"v_mfma_f32_16x16x32_bf16 v[152:155], v[96:99], a[112:115], v[152:155]\n"
"buffer_load_dwordx4 v240, s[28:31], s40 offen offset:2048 lds\n"
"v_mfma_f32_16x16x32_bf16 v[168:171], v[96:99], a[128:131], 0\n"
"ds_read_b64_tr_b16 v[218:219], v228 offset:59392\n"
"v_xor_b32 v228, s75, v228\n"
"v_mfma_f32_16x16x32_bf16 v[168:171], v[100:103], a[132:135], v[168:171]\n"
"ds_read_b64_tr_b16 v[220:221], v232 offset:57344\n"
"v_mfma_f32_16x16x32_bf16 v[168:171], v[104:107], a[136:139], v[168:171]\n"
"ds_read_b64_tr_b16 v[222:223], v232 offset:59392\n"
"v_xor_b32 v232, s75, v232\n"
"v_mfma_f32_16x16x32_bf16 v[168:171], v[108:111], a[140:143], v[168:171]\n"
"buffer_load_dwordx4 v[88:91], v243, s[32:35], s19 offen sc1\n"
"v_mfma_f32_16x16x32_bf16 v[168:171], v[112:115], a[176:179], v[168:171]\n"
"ds_read_b64_tr_b16 v[96:97], v229 offset:32768\n"
"v_mfma_f32_16x16x32_bf16 v[168:171], v[116:119], a[180:183], v[168:171]\n"
"ds_read_b64_tr_b16 v[98:99], v229 offset:34816\n"
"v_mfma_f32_16x16x32_bf16 v[168:171], v[120:123], a[184:187], v[168:171]\n"
"s_add_u32 s40, s41, s54\n"
"ds_read_b64_tr_b16 v[100:101], v233 offset:32768\n"
"v_mfma_f32_16x16x32_bf16 v[168:171], v[224:227], a[188:191], v[168:171]\n"
"s_waitcnt lgkmcnt(6)\n"
"buffer_load_dwordx4 v240, s[28:31], s40 offen offset:3072 lds\n"
"v_mfma_f32_16x16x32_bf16 v[124:127], v[180:183], a[48:51], v[124:127]\n"
"ds_read_b64_tr_b16 v[102:103], v233 offset:34816\n"
"v_mfma_f32_16x16x32_bf16 v[124:127], v[184:187], a[52:55], v[124:127]\n"
"s_waitcnt vmcnt(12)\n"
"ds_read_b64_tr_b16 v[104:105], v229 offset:40960\n"
"v_lshlrev_b32 v246, 16, v72\n"
"v_mfma_f32_16x16x32_bf16 v[124:127], v[188:191], a[56:59], v[124:127]\n"
"ds_read_b64_tr_b16 v[106:107], v229 offset:43008\n"
"v_and_b32 v72, s73, v72\n"
"v_mfma_f32_16x16x32_bf16 v[124:127], v[192:195], a[60:63], v[124:127]\n"
"ds_read_b64_tr_b16 v[108:109], v233 offset:40960\n"
"v_add_f32 v0, v0, v246\n"
"v_mfma_f32_16x16x32_bf16 v[124:127], v[208:211], a[0:3], v[124:127]\n"
"ds_read_b64_tr_b16 v[110:111], v233 offset:43008\n"
"v_add_f32 v1, v1, v72\n"
"v_mfma_f32_16x16x32_bf16 v[124:127], v[212:215], a[4:7], v[124:127]\n"
"s_waitcnt lgkmcnt(6)\n"
"s_add_u32 s41, s70, 327680\n"
"ds_read_b64_tr_b16 v[112:113], v229 offset:49152\n"
"v_lshlrev_b32 v247, 16, v73\n"
"v_mfma_f32_16x16x32_bf16 v[124:127], v[216:219], a[8:11], v[124:127]\n"
"s_add_u32 m0, s71, 32768\n"
"ds_read_b64_tr_b16 v[114:115], v229 offset:51200\n"
"v_and_b32 v73, s73, v73\n"
"v_mfma_f32_16x16x32_bf16 v[124:127], v[220:223], a[12:15], v[124:127]\n"
"buffer_load_dwordx4 v240, s[28:31], s41 offen lds\n"
"v_mfma_f32_16x16x32_bf16 v[140:143], v[220:223], a[28:31], v[140:143]\n"
"ds_read_b64_tr_b16 v[116:117], v233 offset:49152\n"
"v_add_f32 v2, v2, v247\n"
"v_mfma_f32_16x16x32_bf16 v[140:143], v[216:219], a[24:27], v[140:143]\n"
"ds_read_b64_tr_b16 v[118:119], v233 offset:51200\n"
"v_add_f32 v3, v3, v73\n"
"v_mfma_f32_16x16x32_bf16 v[140:143], v[212:215], a[20:23], v[140:143]\n"
"ds_read_b64_tr_b16 v[120:121], v229 offset:57344\n"
"v_lshlrev_b32 v248, 16, v74\n"
"v_mfma_f32_16x16x32_bf16 v[140:143], v[208:211], a[16:19], v[140:143]\n"
"buffer_load_dwordx4 v[92:95], v243, s[32:35], s19 offen offset:1024 sc1\n"
"v_mfma_f32_16x16x32_bf16 v[140:143], v[192:195], a[76:79], v[140:143]\n"
"ds_read_b64_tr_b16 v[122:123], v229 offset:59392\n"
"v_and_b32 v74, s73, v74\n"
"v_mfma_f32_16x16x32_bf16 v[140:143], v[188:191], a[72:75], v[140:143]\n"
"ds_read_b64_tr_b16 v[224:225], v233 offset:57344\n"
"v_cvt_pk_bf16_f32 v124, v124, v125\n"
"v_mfma_f32_16x16x32_bf16 v[140:143], v[184:187], a[68:71], v[140:143]\n"
"s_add_u32 s40, s41, s52\n"
"ds_read_b64_tr_b16 v[226:227], v233 offset:59392\n"
"v_cvt_pk_bf16_f32 v125, v126, v127\n"
"v_mfma_f32_16x16x32_bf16 v[140:143], v[180:183], a[64:67], v[140:143]\n"
"buffer_load_dwordx4 v240, s[28:31], s40 offen offset:1024 lds\n"
"v_mfma_f32_16x16x32_bf16 v[156:159], v[180:183], a[80:83], v[156:159]\n"
"v_add_f32 v4, v4, v248\n"
"v_add_f32 v5, v5, v74\n"
"v_mfma_f32_16x16x32_bf16 v[156:159], v[184:187], a[84:87], v[156:159]\n"
"s_waitcnt vmcnt(12)\n"
"v_lshlrev_b32 v249, 16, v75\n"
"v_and_b32 v75, s73, v75\n"
"v_mfma_f32_16x16x32_bf16 v[156:159], v[188:191], a[88:91], v[156:159]\n"
"v_add_f32 v6, v6, v249\n"
"v_add_f32 v7, v7, v75\n"
"v_mfma_f32_16x16x32_bf16 v[156:159], v[192:195], a[92:95], v[156:159]\n"
"v_lshlrev_b32 v250, 16, v76\n"
"v_and_b32 v76, s73, v76\n"
"v_mfma_f32_16x16x32_bf16 v[156:159], v[208:211], a[32:35], v[156:159]\n"
"v_add_f32 v8, v8, v250\n"
"v_add_f32 v9, v9, v76\n"
"v_mfma_f32_16x16x32_bf16 v[156:159], v[212:215], a[36:39], v[156:159]\n"
"v_cvt_pk_bf16_f32 v140, v140, v141\n"
"v_cvt_pk_bf16_f32 v141, v142, v143\n"
"v_mfma_f32_16x16x32_bf16 v[156:159], v[216:219], a[40:43], v[156:159]\n"
"s_waitcnt vmcnt(10)\n"
"v_lshlrev_b32 v251, 16, v77\n"
"v_and_b32 v77, s73, v77\n"
"v_mfma_f32_16x16x32_bf16 v[156:159], v[220:223], a[44:47], v[156:159]\n"
"s_barrier\n"
"v_mfma_f32_16x16x32_bf16 v[172:175], v[220:223], a[228:231], v[172:175]\n"
"ds_read_b64_tr_b16 v[220:221], v234 offset:57344\n"
"s_cmp_eq_u32 s42, 0\n"
"v_mfma_f32_16x16x32_bf16 v[172:175], v[216:219], a[224:227], v[172:175]\n"
"s_cbranch_scc1 LGS_%=\nLGSK_%=:\n"
"ds_read_b64_tr_b16 v[216:217], v230 offset:57344\n"
"v_mfma_f32_16x16x32_bf16 v[172:175], v[212:215], a[212:215], v[172:175]\n"
"ds_read_b64_tr_b16 v[212:213], v234 offset:49152\n"
"v_add_f32 v10, v10, v251\n"
"v_mfma_f32_16x16x32_bf16 v[172:175], v[208:211], a[208:211], v[172:175]\n"
"s_mul_i32 s16, s61, 286720\n"
"buffer_load_dwordx4 v[196:199], v243, s[32:35], s19 offen offset:2048 sc1\n"
"v_mfma_f32_16x16x32_bf16 v[172:175], v[192:195], a[236:239], v[172:175]\n"
"s_add_u32 s16, s16, s62\n"
"s_mul_i32 s18, s72, 10\n"
"ds_read_b64_tr_b16 v[192:193], v234 offset:40960\n"
"v_mfma_f32_16x16x32_bf16 v[172:175], v[188:191], a[232:235], v[172:175]\n"
"s_add_u32 s17, s16, 4096\n"
"s_add_u32 s18, s18, s61\n"
"ds_read_b64_tr_b16 v[188:189], v230 offset:40960\n"
"v_mfma_f32_16x16x32_bf16 v[172:175], v[184:187], a[220:223], v[172:175]\n"
"s_add_u32 s40, s41, s53\n"
"s_mul_i32 s62, s70, 224\n"
"s_mul_i32 s18, s18, 28672\n"
"v_mfma_f32_16x16x32_bf16 v[172:175], v[180:183], a[216:219], v[172:175]\n"
"s_add_u32 s19, s18, 4096\n"
"buffer_load_dwordx4 v240, s[28:31], s40 offen offset:2048 lds\n"
"v_mfma_f32_16x16x32_bf16 v[128:131], v[96:99], a[48:51], v[128:131]\n"
"s_waitcnt lgkmcnt(4)\n"
"s_mov_b32 s69, 15\n"
"ds_read_b64_tr_b16 v[180:181], v230 offset:32768\n"
"v_cvt_pk_bf16_f32 v156, v156, v157\n"
"v_mfma_f32_16x16x32_bf16 v[128:131], v[100:103], a[52:55], v[128:131]\n"
"s_waitcnt vmcnt(11)\n"
"s_add_u32 s21, s21, 1\n"
"ds_read_b64_tr_b16 v[182:183], v230 offset:34816\n"
"v_mfma_f32_16x16x32_bf16 v[128:131], v[104:107], a[56:59], v[128:131]\n"
"ds_read_b64_tr_b16 v[184:185], v234 offset:32768\n"
"v_cvt_pk_bf16_f32 v157, v158, v159\n"
"v_mfma_f32_16x16x32_bf16 v[128:131], v[108:111], a[60:63], v[128:131]\n"
"ds_read_b64_tr_b16 v[186:187], v234 offset:34816\n"
"v_add_f32 v11, v11, v77\n"
"v_mfma_f32_16x16x32_bf16 v[128:131], v[112:115], a[0:3], v[128:131]\n"
"ds_read_b64_tr_b16 v[190:191], v230 offset:43008\n"
"v_lshlrev_b32 v246, 16, v78\n"
"v_mfma_f32_16x16x32_bf16 v[128:131], v[116:119], a[4:7], v[128:131]\n"
"ds_read_b64_tr_b16 v[194:195], v234 offset:43008\n"
"v_and_b32 v78, s73, v78\n"
"v_mfma_f32_16x16x32_bf16 v[128:131], v[120:123], a[8:11], v[128:131]\n"
"s_add_u32 s40, s41, s54\n"
"ds_read_b64_tr_b16 v[208:209], v230 offset:49152\n"
"v_add_f32 v12, v12, v246\n"
"v_mfma_f32_16x16x32_bf16 v[128:131], v[224:227], a[12:15], v[128:131]\n"
"buffer_load_dwordx4 v240, s[28:31], s40 offen offset:3072 lds\n"
"v_mfma_f32_16x16x32_bf16 v[144:147], v[224:227], a[28:31], v[144:147]\n"
"ds_read_b64_tr_b16 v[210:211], v230 offset:51200\n"
"v_add_f32 v13, v13, v78\n"
"v_mfma_f32_16x16x32_bf16 v[144:147], v[120:123], a[24:27], v[144:147]\n"
"ds_read_b64_tr_b16 v[214:215], v234 offset:51200\n"
"v_lshlrev_b32 v247, 16, v79\n"
"v_mfma_f32_16x16x32_bf16 v[144:147], v[116:119], a[20:23], v[144:147]\n"
"ds_read_b64_tr_b16 v[218:219], v230 offset:59392\n"
"v_and_b32 v79, s73, v79\n"
"v_mfma_f32_16x16x32_bf16 v[144:147], v[112:115], a[16:19], v[144:147]\n"
"ds_read_b64_tr_b16 v[222:223], v234 offset:59392\n"
"v_add_f32 v14, v14, v247\n"
"v_mfma_f32_16x16x32_bf16 v[144:147], v[108:111], a[76:79], v[144:147]\n"
"v_add_f32 v15, v15, v79\n"
"v_lshlrev_b32 v248, 16, v80\n"
"v_mfma_f32_16x16x32_bf16 v[144:147], v[104:107], a[72:75], v[144:147]\n"
"s_add_u32 s41, s70, 491520\n"
"v_cvt_pk_bf16_f32 v126, v128, v129\n"
"v_cvt_pk_bf16_f32 v127, v130, v131\n"
"v_mfma_f32_16x16x32_bf16 v[144:147], v[100:103], a[68:71], v[144:147]\n"
"s_add_u32 m0, s71, 49152\n"
"v_and_b32 v80, s73, v80\n"
"v_add_f32 v16, v16, v248\n"
"v_mfma_f32_16x16x32_bf16 v[144:147], v[96:99], a[64:67], v[144:147]\n"
"buffer_load_dwordx4 v240, s[28:31], s41 offen lds\n"
"v_mfma_f32_16x16x32_bf16 v[160:163], v[96:99], a[80:83], v[160:163]\n"
"s_add_u32 s20, s47, 3\n"
"v_add_f32 v17, v17, v80\n"
"v_lshlrev_b32 v249, 16, v81\n"
"v_mfma_f32_16x16x32_bf16 v[160:163], v[100:103], a[84:87], v[160:163]\n"
"s_waitcnt vmcnt(11)\n"
"s_add_u32 s70, s61, s20\n"
"v_and_b32 v81, s73, v81\n"
"v_mfma_f32_16x16x32_bf16 v[160:163], v[104:107], a[88:91], v[160:163]\n"
"s_and_b32 s70, s70, 7\n"
"v_add_f32 v18, v18, v249\n"
"v_add_f32 v19, v19, v81\n"
"v_mfma_f32_16x16x32_bf16 v[160:163], v[108:111], a[92:95], v[160:163]\n"
"s_cmp_ge_u32 s47, 5\ns_cselect_b32 s70, s20, s70\n"
"v_lshlrev_b32 v250, 16, v82\n"
"v_and_b32 v82, s73, v82\n"
"v_mfma_f32_16x16x32_bf16 v[160:163], v[112:115], a[32:35], v[160:163]\n"
"s_cmp_eq_u32 s47, 7\ns_cselect_b32 s70, s61, s70\n"
"v_add_f32 v20, v20, v250\n"
"v_add_f32 v21, v21, v82\n"
"v_mfma_f32_16x16x32_bf16 v[160:163], v[116:119], a[36:39], v[160:163]\n"
"s_lshl_b32 s70, s70, 7\n"
"v_cvt_pk_bf16_f32 v142, v144, v145\n"
"v_cvt_pk_bf16_f32 v143, v146, v147\n"
"v_mfma_f32_16x16x32_bf16 v[160:163], v[120:123], a[40:43], v[160:163]\n"
"s_xor_b32 s71, s71, s75\n"
"v_lshlrev_b32 v251, 16, v83\n"
"v_and_b32 v83, s73, v83\n"
"v_mfma_f32_16x16x32_bf16 v[160:163], v[224:227], a[44:47], v[160:163]\n"
"buffer_store_dwordx4 v[124:127], v243, s[32:35], s16 offen sc0 sc1\n"
"v_add_f32 v22, v22, v251\n"
"v_mfma_f32_16x16x32_bf16 v[176:179], v[224:227], a[228:231], v[176:179]\n"
"ds_read_b64_tr_b16 v[224:225], v235 offset:57344\n"
"v_add_f32 v23, v23, v83\n"
"v_mfma_f32_16x16x32_bf16 v[176:179], v[120:123], a[224:227], v[176:179]\n"
"ds_read_b64_tr_b16 v[120:121], v231 offset:57344\n"
"v_lshlrev_b32 v246, 16, v84\n"
"v_mfma_f32_16x16x32_bf16 v[176:179], v[116:119], a[212:215], v[176:179]\n"
"ds_read_b64_tr_b16 v[116:117], v235 offset:49152\n"
"v_and_b32 v84, s73, v84\n"
"v_mfma_f32_16x16x32_bf16 v[176:179], v[112:115], a[208:211], v[176:179]\n"
"ds_read_b64_tr_b16 v[112:113], v231 offset:49152\n"
"v_add_f32 v24, v24, v246\n"
"v_mfma_f32_16x16x32_bf16 v[176:179], v[108:111], a[236:239], v[176:179]\n"
"ds_read_b64_tr_b16 v[108:109], v235 offset:40960\n"
"v_add_f32 v25, v25, v84\n"
"v_mfma_f32_16x16x32_bf16 v[176:179], v[104:107], a[232:235], v[176:179]\n"
"ds_read_b64_tr_b16 v[104:105], v231 offset:40960\n"
"v_cvt_pk_bf16_f32 v158, v160, v161\n"
"v_mfma_f32_16x16x32_bf16 v[176:179], v[100:103], a[220:223], v[176:179]\n"
"s_add_u32 s40, s41, s52\n"
"ds_read_b64_tr_b16 v[100:101], v235 offset:32768\n"
"v_cvt_pk_bf16_f32 v159, v162, v163\n"
"v_mfma_f32_16x16x32_bf16 v[176:179], v[96:99], a[216:219], v[176:179]\n"
"s_waitcnt lgkmcnt(7)\n"
"buffer_load_dwordx4 v240, s[28:31], s40 offen offset:1024 lds\n"
"v_mfma_f32_16x16x32_bf16 v[132:135], v[180:183], a[48:51], v[132:135]\n"
"ds_read_b64_tr_b16 v[96:97], v231 offset:32768\n"
"v_lshlrev_b32 v247, 16, v85\n"
"v_mfma_f32_16x16x32_bf16 v[132:135], v[184:187], a[52:55], v[132:135]\n"
"s_waitcnt vmcnt(10)\n"
"ds_read_b64_tr_b16 v[98:99], v231 offset:34816\n"
"v_and_b32 v85, s73, v85\n"
"v_mfma_f32_16x16x32_bf16 v[132:135], v[188:191], a[56:59], v[132:135]\n"
"ds_read_b64_tr_b16 v[102:103], v235 offset:34816\n"
"v_add_f32 v26, v26, v247\n"
"v_mfma_f32_16x16x32_bf16 v[132:135], v[192:195], a[60:63], v[132:135]\n"
"ds_read_b64_tr_b16 v[106:107], v231 offset:43008\n"
"v_add_f32 v27, v27, v85\n"
"v_mfma_f32_16x16x32_bf16 v[132:135], v[208:211], a[0:3], v[132:135]\n"
"ds_read_b64_tr_b16 v[110:111], v235 offset:43008\n"
"v_lshlrev_b32 v248, 16, v86\n"
"v_mfma_f32_16x16x32_bf16 v[132:135], v[212:215], a[4:7], v[132:135]\n"
"ds_read_b64_tr_b16 v[114:115], v231 offset:51200\n"
"v_and_b32 v86, s73, v86\n"
"v_mfma_f32_16x16x32_bf16 v[132:135], v[216:219], a[8:11], v[132:135]\n"
"ds_read_b64_tr_b16 v[118:119], v235 offset:51200\n"
"v_add_f32 v28, v28, v248\n"
"v_mfma_f32_16x16x32_bf16 v[132:135], v[220:223], a[12:15], v[132:135]\n"
"buffer_store_dwordx4 v[140:143], v243, s[32:35], s16 offen offset:2048 sc0 sc1\n"
"v_add_f32 v29, v29, v86\n"
"v_mfma_f32_16x16x32_bf16 v[148:151], v[220:223], a[28:31], v[148:151]\n"
"ds_read_b64_tr_b16 v[122:123], v231 offset:59392\n"
"v_lshlrev_b32 v249, 16, v87\n"
"v_mfma_f32_16x16x32_bf16 v[148:151], v[216:219], a[24:27], v[148:151]\n"
"ds_read_b64_tr_b16 v[226:227], v235 offset:59392\n"
"v_and_b32 v87, s73, v87\n"
"v_mfma_f32_16x16x32_bf16 v[148:151], v[212:215], a[20:23], v[148:151]\n"
"v_add_f32 v30, v30, v249\n"
"v_add_f32 v31, v31, v87\n"
"v_mfma_f32_16x16x32_bf16 v[148:151], v[208:211], a[16:19], v[148:151]\n"
"v_lshlrev_b32 v250, 16, v88\n"
"v_and_b32 v88, s73, v88\n"
"v_mfma_f32_16x16x32_bf16 v[148:151], v[192:195], a[76:79], v[148:151]\n"
"v_add_f32 v32, v32, v250\n"
"v_add_f32 v33, v33, v88\n"
"v_mfma_f32_16x16x32_bf16 v[148:151], v[188:191], a[72:75], v[148:151]\n"
"v_lshlrev_b32 v251, 16, v89\n"
"v_and_b32 v89, s73, v89\n"
"v_mfma_f32_16x16x32_bf16 v[148:151], v[184:187], a[68:71], v[148:151]\n"
"s_add_u32 s40, s41, s53\n"
"v_add_f32 v34, v34, v251\n"
"v_add_f32 v35, v35, v89\n"
"v_mfma_f32_16x16x32_bf16 v[148:151], v[180:183], a[64:67], v[148:151]\n"
"buffer_load_dwordx4 v240, s[28:31], s40 offen offset:2048 lds\n"
"v_mfma_f32_16x16x32_bf16 v[164:167], v[180:183], a[80:83], v[164:167]\n"
"v_lshlrev_b32 v246, 16, v90\n"
"v_and_b32 v90, s73, v90\n"
"v_mfma_f32_16x16x32_bf16 v[164:167], v[184:187], a[84:87], v[164:167]\n"
"s_waitcnt vmcnt(9)\n"
"v_add_f32 v36, v36, v246\n"
"v_add_f32 v37, v37, v90\n"
"v_mfma_f32_16x16x32_bf16 v[164:167], v[188:191], a[88:91], v[164:167]\n"
"v_lshlrev_b32 v247, 16, v91\n"
"v_and_b32 v91, s73, v91\n"
"v_mfma_f32_16x16x32_bf16 v[164:167], v[192:195], a[92:95], v[164:167]\n"
"v_add_f32 v38, v38, v247\n"
"v_add_f32 v39, v39, v91\n"
"v_mfma_f32_16x16x32_bf16 v[164:167], v[208:211], a[32:35], v[164:167]\n"
"v_lshlrev_b32 v248, 16, v92\n"
"v_and_b32 v92, s73, v92\n"
"v_mfma_f32_16x16x32_bf16 v[164:167], v[212:215], a[36:39], v[164:167]\n"
"v_add_f32 v40, v40, v248\n"
"v_add_f32 v41, v41, v92\n"
"v_mfma_f32_16x16x32_bf16 v[164:167], v[216:219], a[40:43], v[164:167]\n"
"v_lshlrev_b32 v249, 16, v93\n"
"v_and_b32 v93, s73, v93\n"
"v_mfma_f32_16x16x32_bf16 v[164:167], v[220:223], a[44:47], v[164:167]\n"
"s_barrier\n"
"s_waitcnt lgkmcnt(0)\n"
"v_mfma_f32_16x16x32_bf16 v[136:139], v[96:99], a[48:51], v[136:139]\n"
"ds_read_b64_tr_b16 v[180:181], v228 offset:0\n"
"v_add_f32 v42, v42, v249\n"
"v_mfma_f32_16x16x32_bf16 v[136:139], v[100:103], a[52:55], v[136:139]\n"
"ds_read_b64_tr_b16 v[182:183], v228 offset:2048\n"
"v_add_f32 v43, v43, v93\n"
"v_mfma_f32_16x16x32_bf16 v[136:139], v[104:107], a[56:59], v[136:139]\n"
"v_lshlrev_b32_e64 v245, 2, s72\n"
"ds_read_b64_tr_b16 v[184:185], v232 offset:0\n"
"v_lshlrev_b32 v250, 16, v94\n"
"v_mfma_f32_16x16x32_bf16 v[136:139], v[108:111], a[60:63], v[136:139]\n"
"global_load_dword v244, v245, s[6:7] sc1\n"
"v_mfma_f32_16x16x32_bf16 v[136:139], v[112:115], a[0:3], v[136:139]\n"
"ds_read_b64_tr_b16 v[186:187], v232 offset:2048\n"
"v_and_b32 v94, s73, v94\n"
"v_mfma_f32_16x16x32_bf16 v[136:139], v[116:119], a[4:7], v[136:139]\n"
"ds_read_b64_tr_b16 v[188:189], v228 offset:8192\n"
"v_add_f32 v44, v44, v250\n"
"v_mfma_f32_16x16x32_bf16 v[136:139], v[120:123], a[8:11], v[136:139]\n"
"ds_read_b64_tr_b16 v[190:191], v228 offset:10240\n"
"v_add_f32 v45, v45, v94\n"
"v_mfma_f32_16x16x32_bf16 v[136:139], v[224:227], a[12:15], v[136:139]\n"
"buffer_store_dwordx4 v[156:159], v243, s[32:35], s17 offen sc0 sc1\n"
"v_lshlrev_b32 v251, 16, v95\n"
"v_mfma_f32_16x16x32_bf16 v[152:155], v[224:227], a[28:31], v[152:155]\n"
"ds_read_b64_tr_b16 v[192:193], v232 offset:8192\n"
"v_and_b32 v95, s73, v95\n"
"v_mfma_f32_16x16x32_bf16 v[152:155], v[120:123], a[24:27], v[152:155]\n"
"s_waitcnt vmcnt(9)\n"
"ds_read_b64_tr_b16 v[194:195], v232 offset:10240\n"
"v_add_f32 v46, v46, v251\n"
"v_mfma_f32_16x16x32_bf16 v[152:155], v[116:119], a[20:23], v[152:155]\n"
"ds_read_b64_tr_b16 v[208:209], v228 offset:16384\n"
"v_add_f32 v47, v47, v95\n"
"v_mfma_f32_16x16x32_bf16 v[152:155], v[112:115], a[16:19], v[152:155]\n"
"ds_read_b64_tr_b16 v[210:211], v228 offset:18432\n"
"v_lshlrev_b32 v250, 16, v196\n"
"v_mfma_f32_16x16x32_bf16 v[152:155], v[108:111], a[76:79], v[152:155]\n"
"ds_read_b64_tr_b16 v[212:213], v232 offset:16384\n"
"v_and_b32 v196, s73, v196\n"
"v_mfma_f32_16x16x32_bf16 v[152:155], v[104:107], a[72:75], v[152:155]\n"
"ds_read_b64_tr_b16 v[214:215], v232 offset:18432\n"
"v_add_f32 v48, v48, v250\n"
"v_mfma_f32_16x16x32_bf16 v[152:155], v[100:103], a[68:71], v[152:155]\n"
"s_add_u32 s40, s41, s54\n"
"ds_read_b64_tr_b16 v[216:217], v228 offset:24576\n"
"v_add_f32 v49, v49, v196\n"
"v_mfma_f32_16x16x32_bf16 v[152:155], v[96:99], a[64:67], v[152:155]\n"
"buffer_load_dwordx4 v240, s[28:31], s40 offen offset:3072 lds\n"
"v_mfma_f32_16x16x32_bf16 v[168:171], v[96:99], a[80:83], v[168:171]\n"
"ds_read_b64_tr_b16 v[218:219], v228 offset:26624\n"
"v_lshlrev_b32 v251, 16, v197\n"
"v_mfma_f32_16x16x32_bf16 v[168:171], v[100:103], a[84:87], v[168:171]\n"
"ds_read_b64_tr_b16 v[220:221], v232 offset:24576\n"
"v_and_b32 v197, s73, v197\n"
"v_mfma_f32_16x16x32_bf16 v[168:171], v[104:107], a[88:91], v[168:171]\n"
"ds_read_b64_tr_b16 v[222:223], v232 offset:26624\n"
"v_add_f32 v50, v50, v251\n"
"v_mfma_f32_16x16x32_bf16 v[168:171], v[108:111], a[92:95], v[168:171]\n"
"v_add_f32 v51, v51, v197\n"
"v_lshlrev_b32 v246, 16, v198\n"
"v_mfma_f32_16x16x32_bf16 v[168:171], v[112:115], a[32:35], v[168:171]\n"
"v_and_b32 v198, s73, v198\n"
"v_add_f32 v52, v52, v246\n"
"v_mfma_f32_16x16x32_bf16 v[168:171], v[116:119], a[36:39], v[168:171]\n"
"v_add_f32 v53, v53, v198\n"
"v_lshlrev_b32 v247, 16, v199\n"
"v_mfma_f32_16x16x32_bf16 v[168:171], v[120:123], a[40:43], v[168:171]\n"
"s_add_u32 s47, s47, 1\n"
"v_and_b32 v199, s73, v199\n"
"v_add_f32 v54, v54, v247\n"
"v_mfma_f32_16x16x32_bf16 v[168:171], v[224:227], a[44:47], v[168:171]\n"
"s_waitcnt lgkmcnt(3)\n"
"s_cmp_lg_u32 s47, 9\n"
"v_add_f32 v55, v55, v199\n"
"s_cbranch_scc1 LGL_%=\n"
"v_mfma_f32_16x16x32_bf16 v[0:3], v[180:183], a[96:99], v[0:3]\n"
"v_xor_b32 v229, s75, v229\n"
"v_xor_b32 v233, s75, v233\n"
"v_mfma_f32_16x16x32_bf16 v[0:3], v[184:187], a[100:103], v[0:3]\n"
"ds_read_b64_tr_b16 v[224:225], v233 offset:24576\n"
"v_cvt_pk_bf16_f32 v172, v172, v173\n"
"v_mfma_f32_16x16x32_bf16 v[0:3], v[188:191], a[104:107], v[0:3]\n"
"v_mov_b32 v245, v237\n"
"ds_read_b64_tr_b16 v[108:109], v233 offset:8192\n"
"v_cvt_pk_bf16_f32 v132, v132, v133\n"
"v_mfma_f32_16x16x32_bf16 v[0:3], v[192:195], a[108:111], v[0:3]\n"
"global_load_dword v244, v245, s[6:7] sc1\n"
"v_mfma_f32_16x16x32_bf16 v[0:3], v[208:211], a[144:147], v[0:3]\n"
"v_cvt_pk_bf16_f32 v173, v174, v175\n"
"v_cvt_pk_bf16_f32 v133, v134, v135\n"
"v_mfma_f32_16x16x32_bf16 v[0:3], v[212:215], a[148:151], v[0:3]\n"
"s_waitcnt lgkmcnt(2)\n"
"v_cvt_pk_bf16_f32 v174, v176, v177\n"
"v_cvt_pk_bf16_f32 v135, v138, v139\n"
"v_mfma_f32_16x16x32_bf16 v[0:3], v[216:219], a[152:155], v[0:3]\n"
"ds_read_b64_tr_b16 v[122:123], v229 offset:26624\n"
"v_cvt_pk_bf16_f32 v175, v178, v179\n"
"v_mfma_f32_16x16x32_bf16 v[0:3], v[220:223], a[156:159], v[0:3]\n"
"buffer_store_dwordx4 v[172:175], v243, s[32:35], s17 offen offset:2048 sc0 sc1\n"
"v_cvt_pk_bf16_f32 v134, v136, v137\n"
"v_mfma_f32_16x16x32_bf16 v[16:19], v[220:223], a[172:175], v[16:19]\n"
"buffer_store_dwordx4 v[132:135], v243, s[32:35], s16 offen offset:1024 sc0 sc1\n"
"ds_read_b64_tr_b16 v[116:117], v233 offset:16384\n"
"v_mfma_f32_16x16x32_bf16 v[16:19], v[216:219], a[168:171], v[16:19]\n"
"ds_read_b64_tr_b16 v[100:101], v233 offset:0\n"
"v_cvt_pk_bf16_f32 v148, v148, v149\n"
"v_mfma_f32_16x16x32_bf16 v[16:19], v[212:215], a[164:167], v[16:19]\n"
"v_cvt_pk_bf16_f32 v149, v150, v151\n"
"v_cvt_pk_bf16_f32 v151, v154, v155\n"
"v_mfma_f32_16x16x32_bf16 v[16:19], v[208:211], a[160:163], v[16:19]\n"
"ds_read_b64_tr_b16 v[96:97], v229 offset:0\n"
"v_cvt_pk_bf16_f32 v164, v164, v165\n"
"v_mfma_f32_16x16x32_bf16 v[16:19], v[192:195], a[124:127], v[16:19]\n"
"v_cvt_pk_bf16_f32 v165, v166, v167\n"
"v_cvt_pk_bf16_f32 v150, v152, v153\n"
"v_mfma_f32_16x16x32_bf16 v[16:19], v[188:191], a[120:123], v[16:19]\n"
"ds_read_b64_tr_b16 v[226:227], v233 offset:26624\n"
"v_cvt_pk_bf16_f32 v167, v170, v171\n"
"v_mfma_f32_16x16x32_bf16 v[16:19], v[184:187], a[116:119], v[16:19]\n"
"ds_read_b64_tr_b16 v[118:119], v233 offset:18432\n"
"v_bfe_u32 v160, v242, 6, 2\n"
"v_mfma_f32_16x16x32_bf16 v[16:19], v[180:183], a[112:115], v[16:19]\n"
"buffer_store_dwordx4 v[148:151], v243, s[32:35], s16 offen offset:3072 sc0 sc1\n"
"v_cvt_pk_bf16_f32 v166, v168, v169\n"
"v_mfma_f32_16x16x32_bf16 v[32:35], v[180:183], a[128:131], v[32:35]\n"
"buffer_store_dwordx4 v[164:167], v243, s[32:35], s17 offen offset:1024 sc0 sc1\n"
"ds_read_b64_tr_b16 v[106:107], v229 offset:10240\n"
"v_mfma_f32_16x16x32_bf16 v[32:35], v[184:187], a[132:135], v[32:35]\n"
"ds_read_b64_tr_b16 v[102:103], v233 offset:2048\n"
"v_xor_b32 v234, s75, v234\n"
"v_mfma_f32_16x16x32_bf16 v[32:35], v[188:191], a[136:139], v[32:35]\n"
"ds_read_b64_tr_b16 v[104:105], v229 offset:8192\n"
"v_mfma_f32_16x16x32_bf16 v[32:35], v[192:195], a[140:143], v[32:35]\n"
"v_xor_b32 v230, s75, v230\n"
"v_bfe_u32 v161, v242, 8, 2\n"
"v_mfma_f32_16x16x32_bf16 v[32:35], v[208:211], a[176:179], v[32:35]\n"
"ds_read_b64_tr_b16 v[98:99], v229 offset:2048\n"
"v_mfma_f32_16x16x32_bf16 v[32:35], v[212:215], a[180:183], v[32:35]\n"
"v_mfma_f32_16x16x32_bf16 v[32:35], v[216:219], a[184:187], v[32:35]\n"
"ds_read_b64_tr_b16 v[114:115], v229 offset:18432\n"
"v_xor_b32 v160, v160, v161\n"
"v_mfma_f32_16x16x32_bf16 v[32:35], v[220:223], a[188:191], v[32:35]\n"
"ds_read_b64_tr_b16 v[112:113], v229 offset:16384\n"
"v_mfma_f32_16x16x32_bf16 v[48:51], v[220:223], a[252:255], v[48:51]\n"
"ds_read_b64_tr_b16 v[120:121], v229 offset:24576\n"
"v_mfma_f32_16x16x32_bf16 v[48:51], v[216:219], a[248:251], v[48:51]\n"
"ds_read_b64_tr_b16 v[110:111], v233 offset:10240\n"
"v_xor_b32 v231, s75, v231\n"
"v_mfma_f32_16x16x32_bf16 v[48:51], v[212:215], a[244:247], v[48:51]\n"
"ds_read_b64_tr_b16 v[222:223], v234 offset:26624\n"
"v_and_b32 v161, 16, v242\n"
"v_mfma_f32_16x16x32_bf16 v[48:51], v[208:211], a[240:243], v[48:51]\n"
"ds_read_b64_tr_b16 v[216:217], v230 offset:24576\n"
"v_mfma_f32_16x16x32_bf16 v[48:51], v[192:195], a[204:207], v[48:51]\n"
"ds_read_b64_tr_b16 v[220:221], v234 offset:24576\n"
"v_mfma_f32_16x16x32_bf16 v[48:51], v[188:191], a[200:203], v[48:51]\n"
"ds_read_b64_tr_b16 v[218:219], v230 offset:26624\n"
"v_xor_b32 v235, s75, v235\n"
"v_mfma_f32_16x16x32_bf16 v[48:51], v[184:187], a[196:199], v[48:51]\n"
"ds_read_b64_tr_b16 v[192:193], v234 offset:8192\n"
"v_mfma_f32_16x16x32_bf16 v[48:51], v[180:183], a[192:195], v[48:51]\n"
"s_waitcnt lgkmcnt(7)\n"
"ds_read_b64_tr_b16 v[214:215], v234 offset:18432\n"
"v_mfma_f32_16x16x32_bf16 v[4:7], v[96:99], a[96:99], v[4:7]\n"
"ds_read_b64_tr_b16 v[194:195], v234 offset:10240\n"
"v_mfma_f32_16x16x32_bf16 v[4:7], v[100:103], a[100:103], v[4:7]\n"
"ds_read_b64_tr_b16 v[180:181], v230 offset:0\n"
"v_mfma_f32_16x16x32_bf16 v[4:7], v[104:107], a[104:107], v[4:7]\n"
"s_waitcnt lgkmcnt(7)\n"
"ds_read_b64_tr_b16 v[186:187], v234 offset:2048\n"
"v_mfma_f32_16x16x32_bf16 v[4:7], v[108:111], a[108:111], v[4:7]\n"
"ds_read_b64_tr_b16 v[184:185], v234 offset:0\n"
"v_mfma_f32_16x16x32_bf16 v[4:7], v[112:115], a[144:147], v[4:7]\n"
"s_waitcnt vmcnt(4)\n"
"ds_read_b64_tr_b16 v[182:183], v230 offset:2048\n"
"v_mfma_f32_16x16x32_bf16 v[4:7], v[116:119], a[148:151], v[4:7]\n"
"v_subrev_u32 v244, s21, v244\n"
"v_mfma_f32_16x16x32_bf16 v[4:7], v[120:123], a[152:155], v[4:7]\n"
"v_cmp_lt_u32 vcc, s69, v244\n"
"ds_read_b64_tr_b16 v[190:191], v230 offset:10240\n"
"v_mfma_f32_16x16x32_bf16 v[4:7], v[224:227], a[156:159], v[4:7]\n"
"ds_read_b64_tr_b16 v[188:189], v230 offset:8192\n"
"s_cbranch_vccnz LGQ_%=\nLGQK_%=:\n"
"v_mfma_f32_16x16x32_bf16 v[20:23], v[224:227], a[172:175], v[20:23]\n"
"s_mov_b32 s20, 0\n"
"ds_read_b64_tr_b16 v[210:211], v230 offset:18432\n"
"v_mfma_f32_16x16x32_bf16 v[20:23], v[120:123], a[168:171], v[20:23]\n"
"buffer_load_dwordx4 v[124:127], v236, s[32:35], s20 offen sc1\n"
"v_mfma_f32_16x16x32_bf16 v[20:23], v[116:119], a[164:167], v[20:23]\n"
"v_add_u32 v160, -1, v160\n"
"v_mfma_f32_16x16x32_bf16 v[20:23], v[112:115], a[160:163], v[20:23]\n"
"s_mov_b32 s20, 286720\n"
"v_mfma_f32_16x16x32_bf16 v[20:23], v[108:111], a[124:127], v[20:23]\n"
"buffer_load_dwordx4 v[128:131], v236, s[32:35], s20 offen sc1\n"
"v_mfma_f32_16x16x32_bf16 v[20:23], v[104:107], a[120:123], v[20:23]\n"
"v_mfma_f32_16x16x32_bf16 v[20:23], v[100:103], a[116:119], v[20:23]\n"
"s_mov_b32 s20, 573440\n"
"v_ashrrev_i32 v160, 31, v160\n"
"v_and_b32 v160, 0x3f80, v160\n"
"v_mfma_f32_16x16x32_bf16 v[20:23], v[96:99], a[112:115], v[20:23]\n"
"buffer_load_dwordx4 v[132:135], v236, s[32:35], s20 offen sc1\n"
"v_mfma_f32_16x16x32_bf16 v[36:39], v[96:99], a[128:131], v[36:39]\n"
"v_add_u32 v241, 1, v241\n"
"v_mfma_f32_16x16x32_bf16 v[36:39], v[100:103], a[132:135], v[36:39]\n"
"s_mov_b32 s20, 860160\n"
"v_mov_b32 v165, 0\n"
"v_mfma_f32_16x16x32_bf16 v[36:39], v[104:107], a[136:139], v[36:39]\n"
"buffer_load_dwordx4 v[136:139], v236, s[32:35], s20 offen sc1\n"
"v_mfma_f32_16x16x32_bf16 v[36:39], v[108:111], a[140:143], v[36:39]\n"
"v_lshlrev_b32 v160, v161, v160\n"
"v_mfma_f32_16x16x32_bf16 v[36:39], v[112:115], a[176:179], v[36:39]\n"
"s_mov_b32 s20, 1146880\n"
"v_mfma_f32_16x16x32_bf16 v[36:39], v[116:119], a[180:183], v[36:39]\n"
"buffer_load_dwordx4 v[140:143], v236, s[32:35], s20 offen sc1\n"
"v_mfma_f32_16x16x32_bf16 v[36:39], v[120:123], a[184:187], v[36:39]\n"
"v_mfma_f32_16x16x32_bf16 v[36:39], v[224:227], a[188:191], v[36:39]\n"
"s_mov_b32 s20, 1433600\n"
"v_mfma_f32_16x16x32_bf16 v[52:55], v[224:227], a[252:255], v[52:55]\n"
"buffer_load_dwordx4 v[144:147], v236, s[32:35], s20 offen sc1\n"
"v_mfma_f32_16x16x32_bf16 v[52:55], v[120:123], a[248:251], v[52:55]\n"
"v_bfe_i32 v161, v242, 5, 1\n"
"v_mfma_f32_16x16x32_bf16 v[52:55], v[116:119], a[244:247], v[52:55]\n"
"s_mov_b32 s20, 1720320\n"
"ds_read_b64_tr_b16 v[208:209], v230 offset:16384\n"
"ds_read_b64_tr_b16 v[212:213], v234 offset:16384\n"
"v_mfma_f32_16x16x32_bf16 v[52:55], v[112:115], a[240:243], v[52:55]\n"
"buffer_load_dwordx4 v[148:151], v236, s[32:35], s20 offen sc1\n"
"v_mfma_f32_16x16x32_bf16 v[52:55], v[108:111], a[204:207], v[52:55]\n"
"s_mov_b32 s20, 2007040\n"
"v_and_b32 v163, v160, v161\n"
"v_mfma_f32_16x16x32_bf16 v[52:55], v[104:107], a[200:203], v[52:55]\n"
"buffer_load_dwordx4 v[152:155], v236, s[32:35], s20 offen sc1\n"
"v_mfma_f32_16x16x32_bf16 v[52:55], v[100:103], a[196:199], v[52:55]\n"
"s_waitcnt vmcnt(8)\n"
"s_add_u32 s21, s21, 1\n"
"v_xor_b32 v162, v160, v163\n"
"v_mfma_f32_16x16x32_bf16 v[52:55], v[96:99], a[192:195], v[52:55]\n"
"s_barrier\n"
"s_mov_b32 s52, 40960\n"
"s_waitcnt lgkmcnt(2)\n"
"v_mfma_f32_16x16x32_bf16 v[8:11], v[180:183], a[96:99], v[8:11]\n"
"s_mov_b32 s53, 81920\n"
"ds_read_b64_tr_b16 v[120:121], v231 offset:24576\n"
"s_cmp_eq_u32 s42, 0\n"
"v_mfma_f32_16x16x32_bf16 v[8:11], v[184:187], a[100:103], v[8:11]\n"
"s_cbranch_scc1 LGT_%=\nLGTK_%=:\n"
"ds_read_b64_tr_b16 v[100:101], v235 offset:0\n"
"v_mfma_f32_16x16x32_bf16 v[8:11], v[188:191], a[104:107], v[8:11]\n"
"v_mfma_f32_16x16x32_bf16 v[8:11], v[192:195], a[108:111], v[8:11]\n"
"s_waitcnt lgkmcnt(2)\n"
"ds_read_b64_tr_b16 v[118:119], v235 offset:18432\n"
"v_mfma_f32_16x16x32_bf16 v[8:11], v[208:211], a[144:147], v[8:11]\n"
"ds_read_b64_tr_b16 v[96:97], v231 offset:0\n"
"v_mfma_f32_16x16x32_bf16 v[8:11], v[212:215], a[148:151], v[8:11]\n"
"ds_read_b64_tr_b16 v[112:113], v231 offset:16384\n"
"v_mfma_f32_16x16x32_bf16 v[8:11], v[216:219], a[152:155], v[8:11]\n"
"v_mfma_f32_16x16x32_bf16 v[8:11], v[220:223], a[156:159], v[8:11]\n"
"ds_read_b64_tr_b16 v[106:107], v231 offset:10240\n"
"v_mfma_f32_16x16x32_bf16 v[24:27], v[220:223], a[172:175], v[24:27]\n"
"v_mfma_f32_16x16x32_bf16 v[24:27], v[216:219], a[168:171], v[24:27]\n"
"ds_read_b64_tr_b16 v[110:111], v235 offset:10240\n"
"v_mfma_f32_16x16x32_bf16 v[24:27], v[212:215], a[164:167], v[24:27]\n"
"v_mfma_f32_16x16x32_bf16 v[24:27], v[208:211], a[160:163], v[24:27]\n"
"ds_read_b64_tr_b16 v[102:103], v235 offset:2048\n"
"v_mfma_f32_16x16x32_bf16 v[24:27], v[192:195], a[124:127], v[24:27]\n"
"ds_read_b64_tr_b16 v[116:117], v235 offset:16384\n"
"v_mfma_f32_16x16x32_bf16 v[24:27], v[188:191], a[120:123], v[24:27]\n"
"ds_read_b64_tr_b16 v[104:105], v231 offset:8192\n"
"v_mfma_f32_16x16x32_bf16 v[24:27], v[184:187], a[116:119], v[24:27]\n"
"v_mov_b32 v160, 0\n"
"v_mov_b32 v164, 0\n"
"v_mfma_f32_16x16x32_bf16 v[24:27], v[180:183], a[112:115], v[24:27]\n"
"ds_read_b64_tr_b16 v[98:99], v231 offset:2048\n"
"v_mfma_f32_16x16x32_bf16 v[40:43], v[180:183], a[128:131], v[40:43]\n"
"ds_read_b64_tr_b16 v[108:109], v235 offset:8192\n"
"v_mfma_f32_16x16x32_bf16 v[40:43], v[184:187], a[132:135], v[40:43]\n"
"ds_read_b64_tr_b16 v[226:227], v235 offset:26624\n"
"v_mfma_f32_16x16x32_bf16 v[40:43], v[188:191], a[136:139], v[40:43]\n"
"v_mfma_f32_16x16x32_bf16 v[40:43], v[192:195], a[140:143], v[40:43]\n"
"ds_read_b64_tr_b16 v[114:115], v231 offset:18432\n"
"v_mfma_f32_16x16x32_bf16 v[40:43], v[208:211], a[176:179], v[40:43]\n"
"ds_read_b64_tr_b16 v[122:123], v231 offset:26624\n"
"v_mfma_f32_16x16x32_bf16 v[40:43], v[212:215], a[180:183], v[40:43]\n"
"v_mfma_f32_16x16x32_bf16 v[40:43], v[216:219], a[184:187], v[40:43]\n"
"ds_read_b64_tr_b16 v[224:225], v235 offset:24576\n"
"v_mfma_f32_16x16x32_bf16 v[40:43], v[220:223], a[188:191], v[40:43]\n"
"s_barrier\n"
"s_waitcnt lgkmcnt(5)\n"
"v_mfma_f32_16x16x32_bf16 v[12:15], v[96:99], a[96:99], v[12:15]\n"
"ds_read_b64_tr_b16 v[180:181], v228 offset:32768\n"
"ds_read_b64_tr_b16 v[214:215], v232 offset:51200\n"
"v_mfma_f32_16x16x32_bf16 v[12:15], v[100:103], a[100:103], v[12:15]\n"
"ds_read_b64_tr_b16 v[186:187], v232 offset:34816\n"
"ds_read_b64_tr_b16 v[220:221], v232 offset:57344\n"
"v_mfma_f32_16x16x32_bf16 v[12:15], v[104:107], a[104:107], v[12:15]\n"
"s_waitcnt lgkmcnt(7)\n"
"ds_read_b64_tr_b16 v[194:195], v232 offset:43008\n"
"ds_read_b64_tr_b16 v[216:217], v228 offset:57344\n"
"v_mfma_f32_16x16x32_bf16 v[12:15], v[108:111], a[108:111], v[12:15]\n"
"s_waitcnt lgkmcnt(8)\n"
"ds_read_b64_tr_b16 v[188:189], v228 offset:40960\n"
"ds_read_b64_tr_b16 v[210:211], v228 offset:51200\n"
"v_mfma_f32_16x16x32_bf16 v[12:15], v[112:115], a[144:147], v[12:15]\n"
"ds_read_b64_tr_b16 v[212:213], v232 offset:49152\n"
"v_mfma_f32_16x16x32_bf16 v[12:15], v[116:119], a[148:151], v[12:15]\n"
"s_waitcnt lgkmcnt(10)\n"
"ds_read_b64_tr_b16 v[190:191], v228 offset:43008\n"
"ds_read_b64_tr_b16 v[208:209], v228 offset:49152\n"
"v_mfma_f32_16x16x32_bf16 v[12:15], v[120:123], a[152:155], v[12:15]\n"
"s_waitcnt lgkmcnt(11)\n"
"ds_read_b64_tr_b16 v[182:183], v228 offset:34816\n"
"ds_read_b64_tr_b16 v[192:193], v232 offset:40960\n"
"v_mfma_f32_16x16x32_bf16 v[12:15], v[224:227], a[156:159], v[12:15]\n"
"global_load_dword v244, v245, s[6:7] sc1\n"
"v_mfma_f32_16x16x32_bf16 v[28:31], v[224:227], a[172:175], v[28:31]\n"
"v_mfma_f32_16x16x32_bf16 v[28:31], v[120:123], a[168:171], v[28:31]\n"
"s_waitcnt vmcnt(8)\n"
"v_and_b32 v57, s73, v124\n"
"v_and_b32 v59, s73, v125\n"
"v_mfma_f32_16x16x32_bf16 v[28:31], v[116:119], a[164:167], v[28:31]\n"
"v_lshlrev_b32 v58, 16, v125\n"
"v_lshlrev_b32 v60, 16, v126\n"
"v_mfma_f32_16x16x32_bf16 v[28:31], v[112:115], a[160:163], v[28:31]\n"
"ds_read_b64_tr_b16 v[184:185], v232 offset:32768\n"
"v_lshlrev_b32 v62, 16, v127\n"
"v_mfma_f32_16x16x32_bf16 v[28:31], v[108:111], a[124:127], v[28:31]\n"
"s_waitcnt vmcnt(7)\n"
"ds_read_b64_tr_b16 v[222:223], v232 offset:59392\n"
"v_lshlrev_b32 v56, 16, v124\n"
"v_mfma_f32_16x16x32_bf16 v[28:31], v[104:107], a[120:123], v[28:31]\n"
"ds_read_b64_tr_b16 v[218:219], v228 offset:59392\n"
"v_lshlrev_b32 v248, 16, v128\n"
"v_mfma_f32_16x16x32_bf16 v[28:31], v[100:103], a[116:119], v[28:31]\n"
"v_add_f32 v56, v56, v248\n"
"v_lshlrev_b32 v251, 16, v131\n"
"v_mfma_f32_16x16x32_bf16 v[28:31], v[96:99], a[112:115], v[28:31]\n"
"s_waitcnt vmcnt(6)\n"
"v_lshlrev_b32 v249, 16, v129\n"
"v_lshlrev_b32 v250, 16, v130\n"
"v_mfma_f32_16x16x32_bf16 v[44:47], v[96:99], a[128:131], v[44:47]\n"
"v_add_f32 v58, v58, v249\n"
"v_lshlrev_b32 v247, 16, v133\n"
"v_mfma_f32_16x16x32_bf16 v[44:47], v[100:103], a[132:135], v[44:47]\n"
"v_and_b32 v63, s73, v127\n"
"v_lshlrev_b32 v246, 16, v132\n"
"v_mfma_f32_16x16x32_bf16 v[44:47], v[104:107], a[136:139], v[44:47]\n"
"s_waitcnt vmcnt(5)\n"
"ds_read_b64_tr_b16 v[102:103], v233 offset:34816\n"
"v_add_f32 v60, v60, v250\n"
"v_mfma_f32_16x16x32_bf16 v[44:47], v[108:111], a[140:143], v[44:47]\n"
"v_add_f32 v56, v56, v246\n"
"v_lshlrev_b32 v249, 16, v135\n"
"v_mfma_f32_16x16x32_bf16 v[44:47], v[112:115], a[176:179], v[44:47]\n"
"ds_read_b64_tr_b16 v[96:97], v229 offset:32768\n"
"ds_read_b64_tr_b16 v[110:111], v233 offset:43008\n"
"v_mfma_f32_16x16x32_bf16 v[44:47], v[116:119], a[180:183], v[44:47]\n"
"s_waitcnt vmcnt(4)\n"
"v_and_b32 v129, s73, v129\n"
"v_lshlrev_b32 v250, 16, v136\n"
"v_mfma_f32_16x16x32_bf16 v[44:47], v[120:123], a[184:187], v[44:47]\n"
"v_lshlrev_b32 v248, 16, v134\n"
"v_lshlrev_b32 v246, 16, v138\n"
"v_mfma_f32_16x16x32_bf16 v[44:47], v[224:227], a[188:191], v[44:47]\n"
"s_waitcnt lgkmcnt(3)\n"
"v_and_b32 v136, s73, v136\n"
"v_and_b32 v138, s73, v138\n"
"v_mfma_f32_16x16x32_bf16 v[0:3], v[180:183], a[48:51], v[0:3]\n"
"s_waitcnt vmcnt(3)\n"
"ds_read_b64_tr_b16 v[226:227], v233 offset:59392\n"
"v_and_b32 v128, s73, v128\n"
"v_mfma_f32_16x16x32_bf16 v[0:3], v[184:187], a[52:55], v[0:3]\n"
"ds_read_b64_tr_b16 v[100:101], v233 offset:32768\n"
"v_and_b32 v61, s73, v126\n"
"v_mfma_f32_16x16x32_bf16 v[0:3], v[188:191], a[56:59], v[0:3]\n"
"v_add_f32 v62, v62, v251\n"
"v_add_f32 v56, v56, v250\n"
"v_mfma_f32_16x16x32_bf16 v[0:3], v[192:195], a[60:63], v[0:3]\n"
"s_waitcnt vmcnt(2)\n"
"ds_read_b64_tr_b16 v[108:109], v233 offset:40960\n"
"v_add_f32 v60, v60, v248\n"
"v_mfma_f32_16x16x32_bf16 v[0:3], v[208:211], a[0:3], v[0:3]\n"
"v_add_f32 v57, v57, v128\n"
"v_lshlrev_b32 v248, 16, v140\n"
"v_mfma_f32_16x16x32_bf16 v[0:3], v[212:215], a[4:7], v[0:3]\n"
"s_waitcnt vmcnt(1)\n"
"ds_read_b64_tr_b16 v[114:115], v229 offset:51200\n"
"v_lshlrev_b32 v251, 16, v137\n"
"v_mfma_f32_16x16x32_bf16 v[0:3], v[216:219], a[8:11], v[0:3]\n"
"v_add_f32 v58, v58, v247\n"
"v_and_b32 v135, s73, v135\n"
"v_mfma_f32_16x16x32_bf16 v[0:3], v[220:223], a[12:15], v[0:3]\n"
"ds_read_b64_tr_b16 v[116:117], v233 offset:49152\n"
"v_and_b32 v132, s73, v132\n"
"v_and_b32 v133, s73, v133\n"
"v_and_b32 v137, s73, v137\n"
"v_add_f32 v58, v58, v251\n"
"v_add_f32 v60, v60, v246\n"
"v_lshlrev_b32 v250, 16, v142\n"
"v_mfma_f32_16x16x32_bf16 v[16:19], v[220:223], a[28:31], v[16:19]\n"
"ds_read_b64_tr_b16 v[224:225], v233 offset:57344\n"
"v_lshlrev_b32 v247, 16, v139\n"
"v_mfma_f32_16x16x32_bf16 v[16:19], v[216:219], a[24:27], v[16:19]\n"
"ds_read_b64_tr_b16 v[120:121], v229 offset:57344\n"
"v_add_f32 v56, v56, v248\n"
"v_mfma_f32_16x16x32_bf16 v[16:19], v[212:215], a[20:23], v[16:19]\n"
"ds_read_b64_tr_b16 v[106:107], v229 offset:43008\n"
"ds_read_b64_tr_b16 v[112:113], v229 offset:49152\n"
"v_mfma_f32_16x16x32_bf16 v[16:19], v[208:211], a[16:19], v[16:19]\n"
"ds_read_b64_tr_b16 v[122:123], v229 offset:59392\n"
"v_lshlrev_b32 v246, 16, v144\n"
"v_mfma_f32_16x16x32_bf16 v[16:19], v[192:195], a[76:79], v[16:19]\n"
"s_waitcnt vmcnt(0)\n"
"v_add_f32 v62, v62, v249\n"
"v_add_f32 v60, v60, v250\n"
"v_mfma_f32_16x16x32_bf16 v[16:19], v[188:191], a[72:75], v[16:19]\n"
"v_subrev_u32 v244, s21, v244\n"
"ds_read_b64_tr_b16 v[98:99], v229 offset:34816\n"
"v_mfma_f32_16x16x32_bf16 v[16:19], v[184:187], a[68:71], v[16:19]\n"
"v_cmp_lt_u32 vcc, s69, v244\n"
"v_add_f32 v59, v59, v129\n"
"v_mfma_f32_16x16x32_bf16 v[16:19], v[180:183], a[64:67], v[16:19]\n"
"ds_read_b64_tr_b16 v[118:119], v233 offset:51200\n"
"v_and_b32 v130, s73, v130\n"
"v_add_f32 v61, v61, v130\n"
"v_add_f32 v57, v57, v132\n"
"v_and_b32 v134, s73, v134\n"
"v_lshlrev_b32 v249, 16, v141\n"
"v_add_f32 v56, v56, v246\n"
"v_lshlrev_b32 v248, 16, v146\n"
"v_cvt_pk_bf16_f32 v0, v0, v1\n"
"s_cbranch_vccnz LGR_%=\nLGRK_%=:\n"
"v_mfma_f32_16x16x32_bf16 v[32:35], v[180:183], a[80:83], v[32:35]\n"
"v_add_f32 v59, v59, v133\n"
"s_cmp_eq_u32 s93, 0\ns_cbranch_scc1 LEP_%=\nLEPK_%=:\n"
"v_mfma_f32_16x16x32_bf16 v[32:35], v[184:187], a[84:87], v[32:35]\n"
"s_mov_b32 s20, 28672\n"
"ds_read_b64_tr_b16 v[104:105], v229 offset:40960\n"
"v_lshlrev_b32 v250, 16, v148\n"
"v_mfma_f32_16x16x32_bf16 v[32:35], v[188:191], a[88:91], v[32:35]\n"
"buffer_load_dwordx4 v[72:75], v236, s[32:35], s20 offen sc1\n"
"v_mfma_f32_16x16x32_bf16 v[32:35], v[192:195], a[92:95], v[32:35]\n"
"v_add_f32 v60, v60, v248\n"
"v_add_f32 v56, v56, v250\n"
"v_mfma_f32_16x16x32_bf16 v[32:35], v[208:211], a[32:35], v[32:35]\n"
"s_mov_b32 s20, 315392\n"
"v_and_b32 v139, s73, v139\n"
"v_lshlrev_b32 v251, 16, v143\n"
"v_mfma_f32_16x16x32_bf16 v[32:35], v[212:215], a[36:39], v[32:35]\n"
"buffer_load_dwordx4 v[76:79], v236, s[32:35], s20 offen sc1\n"
"v_mfma_f32_16x16x32_bf16 v[32:35], v[216:219], a[40:43], v[32:35]\n"
"v_add_f32 v62, v62, v247\n"
"v_lshlrev_b32 v246, 16, v150\n"
"v_mfma_f32_16x16x32_bf16 v[32:35], v[220:223], a[44:47], v[32:35]\n"
"s_mov_b32 s20, 602112\n"
"v_add_f32 v58, v58, v249\n"
"v_lshlrev_b32 v247, 16, v145\n"
"v_mfma_f32_16x16x32_bf16 v[48:51], v[220:223], a[228:231], v[48:51]\n"
"buffer_load_dwordx4 v[80:83], v236, s[32:35], s20 offen sc1\n"
"v_mfma_f32_16x16x32_bf16 v[48:51], v[216:219], a[224:227], v[48:51]\n"
"v_add_f32 v61, v61, v134\n"
"v_add_f32 v60, v60, v246\n"
"v_mfma_f32_16x16x32_bf16 v[48:51], v[212:215], a[212:215], v[48:51]\n"
"s_mov_b32 s20, 888832\n"
"v_and_b32 v140, s73, v140\n"
"v_and_b32 v144, s73, v144\n"
"v_mfma_f32_16x16x32_bf16 v[48:51], v[208:211], a[208:211], v[48:51]\n"
"buffer_load_dwordx4 v[84:87], v236, s[32:35], s20 offen sc1\n"
"v_mfma_f32_16x16x32_bf16 v[48:51], v[192:195], a[236:239], v[48:51]\n"
"v_lshlrev_b32 v249, 16, v147\n"
"v_cvt_pk_bf16_f32 v16, v16, v17\n"
"v_mfma_f32_16x16x32_bf16 v[48:51], v[188:191], a[232:235], v[48:51]\n"
"s_mov_b32 s20, 1175552\n"
"v_add_f32 v61, v61, v138\n"
"v_add_f32 v62, v62, v251\n"
"v_mfma_f32_16x16x32_bf16 v[48:51], v[184:187], a[220:223], v[48:51]\n"
"buffer_load_dwordx4 v[88:91], v236, s[32:35], s20 offen sc1\n"
"v_mfma_f32_16x16x32_bf16 v[48:51], v[180:183], a[216:219], v[48:51]\n"
"s_waitcnt lgkmcnt(0)\n"
"v_add_f32 v59, v59, v137\n"
"v_add_f32 v62, v62, v249\n"
"v_mfma_f32_16x16x32_bf16 v[4:7], v[96:99], a[48:51], v[4:7]\n"
"s_mov_b32 s20, 1462272\n"
"v_and_b32 v131, s73, v131\n"
"v_cvt_pk_bf16_f32 v1, v2, v3\n"
"v_mfma_f32_16x16x32_bf16 v[4:7], v[100:103], a[52:55], v[4:7]\n"
"buffer_load_dwordx4 v[92:95], v236, s[32:35], s20 offen sc1\n"
"v_mfma_f32_16x16x32_bf16 v[4:7], v[104:107], a[56:59], v[4:7]\n"
"v_and_b32 v142, s73, v142\n"
"v_add_f32 v58, v58, v247\n"
"v_mfma_f32_16x16x32_bf16 v[4:7], v[108:111], a[60:63], v[4:7]\n"
"s_mov_b32 s20, 1748992\n"
"v_add_f32 v63, v63, v131\n"
"v_and_b32 v150, s73, v150\n"
"v_mfma_f32_16x16x32_bf16 v[4:7], v[112:115], a[0:3], v[4:7]\n"
"buffer_load_dwordx4 v[196:199], v236, s[32:35], s20 offen sc1\n"
"v_mfma_f32_16x16x32_bf16 v[4:7], v[116:119], a[4:7], v[4:7]\n"
"v_and_b32 v145, s73, v145\n"
"v_and_b32 v148, s73, v148\n"
"v_mfma_f32_16x16x32_bf16 v[4:7], v[120:123], a[8:11], v[4:7]\n"
"s_mov_b32 s20, 2035712\n"
"ds_read_b64_tr_b16 v[186:187], v234 offset:34816\n"
"v_cvt_pk_bf16_f32 v17, v18, v19\n"
"v_mfma_f32_16x16x32_bf16 v[4:7], v[224:227], a[12:15], v[4:7]\n"
"buffer_load_dwordx4 v[200:203], v236, s[32:35], s20 offen sc1\n"
"v_mfma_f32_16x16x32_bf16 v[20:23], v[224:227], a[28:31], v[20:23]\n"
"v_and_b32 v147, s73, v147\n"
"v_lshlrev_b32 v249, 16, v153\n"
"v_mfma_f32_16x16x32_bf16 v[20:23], v[120:123], a[24:27], v[20:23]\n"
"v_add_f32 v63, v63, v135\n"
"v_add_f32 v63, v63, v139\n"
"v_mfma_f32_16x16x32_bf16 v[20:23], v[116:119], a[20:23], v[20:23]\n"
"ds_read_b64_tr_b16 v[210:211], v230 offset:51200\n"
"v_cvt_pk_bf16_f32 v32, v32, v33\n"
"v_mfma_f32_16x16x32_bf16 v[20:23], v[112:115], a[16:19], v[20:23]\n"
"v_add_f32 v57, v57, v136\n"
"v_lshlrev_b32 v248, 16, v152\n"
"v_mfma_f32_16x16x32_bf16 v[20:23], v[108:111], a[76:79], v[20:23]\n"
"ds_read_b64_tr_b16 v[216:217], v230 offset:57344\n"
"v_add_f32 v57, v57, v140\n"
"v_mfma_f32_16x16x32_bf16 v[20:23], v[104:107], a[72:75], v[20:23]\n"
"ds_read_b64_tr_b16 v[180:181], v230 offset:32768\n"
"v_cvt_pk_bf16_f32 v2, v4, v5\n"
"v_mfma_f32_16x16x32_bf16 v[20:23], v[100:103], a[68:71], v[20:23]\n"
"v_add_f32 v56, v56, v248\n"
"v_cvt_pk_bf16_f32 v3, v6, v7\n"
"v_mfma_f32_16x16x32_bf16 v[20:23], v[96:99], a[64:67], v[20:23]\n"
"ds_read_b64_tr_b16 v[188:189], v230 offset:40960\n"
"ds_read_b64_tr_b16 v[212:213], v234 offset:49152\n"
"v_and_b32 v143, s73, v143\n"
"v_add_f32 v63, v63, v143\n"
"v_lshlrev_b32 v251, 16, v149\n"
"v_and_b32 v149, s73, v149\n"
"v_permlane16_swap_b32 v0, v2\n"
"v_permlane16_swap_b32 v1, v3\n"
"v_mfma_f32_16x16x32_bf16 v[36:39], v[96:99], a[80:83], v[36:39]\n"
"v_and_b32 v152, s73, v152\n"
"v_lshlrev_b32 v250, 16, v154\n"
"v_mfma_f32_16x16x32_bf16 v[36:39], v[100:103], a[84:87], v[36:39]\n"
"v_add_f32 v57, v57, v144\n"
"v_and_b32 v154, s73, v154\n"
"v_mfma_f32_16x16x32_bf16 v[36:39], v[104:107], a[88:91], v[36:39]\n"
"ds_read_b64_tr_b16 v[182:183], v230 offset:34816\n"
"ds_read_b64_tr_b16 v[218:219], v230 offset:59392\n"
"buffer_store_dwordx4 v[0:3], v238, s[36:39], 0 offen sc0 sc1\n"
"v_and_b32 v141, s73, v141\n"
"v_add_f32 v61, v61, v142\n"
"v_and_b32 v146, s73, v146\n"
"v_cvt_pk_bf16_f32 v18, v20, v21\n"
"v_mfma_f32_16x16x32_bf16 v[36:39], v[108:111], a[92:95], v[36:39]\n"
"ds_read_b64_tr_b16 v[184:185], v234 offset:32768\n"
"v_lshlrev_b32 v247, 16, v151\n"
"v_mfma_f32_16x16x32_bf16 v[36:39], v[112:115], a[32:35], v[36:39]\n"
"ds_read_b64_tr_b16 v[220:221], v234 offset:57344\n"
"v_add_f32 v60, v60, v250\n"
"v_mfma_f32_16x16x32_bf16 v[36:39], v[116:119], a[36:39], v[36:39]\n"
"v_add_f32 v59, v59, v141\n"
"v_add_f32 v61, v61, v146\n"
"v_mfma_f32_16x16x32_bf16 v[36:39], v[120:123], a[40:43], v[36:39]\n"
"ds_read_b64_tr_b16 v[208:209], v230 offset:49152\n"
"v_add_f32 v63, v63, v147\n"
"v_mfma_f32_16x16x32_bf16 v[36:39], v[224:227], a[44:47], v[36:39]\n"
"ds_read_b64_tr_b16 v[190:191], v230 offset:43008\n"
"v_add_f32 v57, v57, v148\n"
"v_add_f32 v58, v58, v251\n"
"v_and_b32 v151, s73, v151\n"
"v_add_f32 v62, v62, v247\n"
"v_lshlrev_b32 v251, 16, v155\n"
"v_and_b32 v155, s73, v155\n"
"v_add_f32 v62, v62, v251\n"
"v_cvt_pk_bf16_f32 v19, v22, v23\n"
"v_permlane16_swap_b32 v16, v18\n"
"v_mfma_f32_16x16x32_bf16 v[52:55], v[224:227], a[228:231], v[52:55]\n"
"ds_read_b64_tr_b16 v[192:193], v234 offset:40960\n"
"v_and_b32 v153, s73, v153\n"
"v_mfma_f32_16x16x32_bf16 v[52:55], v[120:123], a[224:227], v[52:55]\n"
"v_add_f32 v63, v63, v151\n"
"v_add_f32 v57, v57, v152\n"
"v_mfma_f32_16x16x32_bf16 v[52:55], v[116:119], a[212:215], v[52:55]\n"
"v_permlane16_swap_b32 v17, v19\n"
"v_mfma_f32_16x16x32_bf16 v[52:55], v[112:115], a[208:211], v[52:55]\n"
"ds_read_b64_tr_b16 v[222:223], v234 offset:59392\n"
"v_add_f32 v59, v59, v145\n"
"v_mfma_f32_16x16x32_bf16 v[52:55], v[108:111], a[236:239], v[52:55]\n"
"ds_read_b64_tr_b16 v[214:215], v234 offset:51200\n"
"v_add_f32 v63, v63, v155\n"
"v_mfma_f32_16x16x32_bf16 v[52:55], v[104:107], a[232:235], v[52:55]\n"
"ds_read_b64_tr_b16 v[194:195], v234 offset:43008\n"
"buffer_store_dwordx4 v[16:19], v238, s[36:39], s52 offen sc0 sc1\n"
"v_add_f32 v59, v59, v149\n"
"v_add_f32 v61, v61, v150\n"
"v_add_f32 v58, v58, v249\n"
"v_add_f32 v59, v59, v153\n"
"v_cvt_pk_bf16_f32 v56, v56, v57\n"
"v_mfma_f32_16x16x32_bf16 v[52:55], v[100:103], a[220:223], v[52:55]\n"
"v_add_f32 v61, v61, v154\n"
"v_cvt_pk_bf16_f32 v57, v58, v59\n"
"v_mfma_f32_16x16x32_bf16 v[52:55], v[96:99], a[216:219], v[52:55]\n"
"s_waitcnt lgkmcnt(4)\n"
"v_cvt_pk_bf16_f32 v58, v60, v61\n"
"v_cvt_pk_bf16_f32 v59, v62, v63\n"
"v_mfma_f32_16x16x32_bf16 v[8:11], v[180:183], a[48:51], v[8:11]\n"
"s_nop 0\n"
"v_permlane16_swap_b32 v57, v59\n"
"v_mfma_f32_16x16x32_bf16 v[8:11], v[184:187], a[52:55], v[8:11]\n"
"v_permlane16_swap_b32 v56, v58\n"
"v_mfma_f32_16x16x32_bf16 v[8:11], v[188:191], a[56:59], v[8:11]\n"
"s_waitcnt lgkmcnt(0)\n"
"buffer_store_dwordx4 v[56:59], v253, s[36:39], 0 offen sc0 sc1\n"
"v_cvt_pk_bf16_f32 v33, v34, v35\n"
"v_cvt_pk_bf16_f32 v34, v36, v37\n"
"v_cvt_pk_bf16_f32 v48, v48, v49\n"
"v_mfma_f32_16x16x32_bf16 v[8:11], v[192:195], a[60:63], v[8:11]\n"
"v_permlane16_swap_b32 v32, v34\n"
"v_mfma_f32_16x16x32_bf16 v[8:11], v[208:211], a[0:3], v[8:11]\n"
"ds_read_b64_tr_b16 v[100:101], v235 offset:32768\n"
"v_cvt_pk_bf16_f32 v35, v38, v39\n"
"v_mfma_f32_16x16x32_bf16 v[8:11], v[212:215], a[4:7], v[8:11]\n"
"s_nop 0\n"
"v_permlane16_swap_b32 v33, v35\n"
"v_mfma_f32_16x16x32_bf16 v[8:11], v[216:219], a[8:11], v[8:11]\n"
"ds_read_b64_tr_b16 v[122:123], v231 offset:59392\n"
"v_cvt_pk_bf16_f32 v49, v50, v51\n"
"v_mfma_f32_16x16x32_bf16 v[8:11], v[220:223], a[12:15], v[8:11]\n"
"buffer_store_dwordx4 v[32:35], v238, s[36:39], s53 offen sc0 sc1\n"
"ds_read_b64_tr_b16 v[102:103], v235 offset:34816\n"
"v_mfma_f32_16x16x32_bf16 v[24:27], v[220:223], a[28:31], v[24:27]\n"
"ds_read_b64_tr_b16 v[96:97], v231 offset:32768\n"
"v_cvt_pk_bf16_f32 v50, v52, v53\n"
"v_mfma_f32_16x16x32_bf16 v[24:27], v[216:219], a[24:27], v[24:27]\n"
"s_nop 0\n"
"v_permlane16_swap_b32 v48, v50\n"
"v_mfma_f32_16x16x32_bf16 v[24:27], v[212:215], a[20:23], v[24:27]\n"
"ds_read_b64_tr_b16 v[112:113], v231 offset:49152\n"
"v_cvt_pk_bf16_f32 v51, v54, v55\n"
"v_mfma_f32_16x16x32_bf16 v[24:27], v[208:211], a[16:19], v[24:27]\n"
"s_nop 0\n"
"v_permlane16_swap_b32 v49, v51\n"
"v_mfma_f32_16x16x32_bf16 v[24:27], v[192:195], a[76:79], v[24:27]\n"
"ds_read_b64_tr_b16 v[120:121], v231 offset:57344\n"
"v_mfma_f32_16x16x32_bf16 v[24:27], v[188:191], a[72:75], v[24:27]\n"
"buffer_store_dwordx4 v[48:51], v252, s[36:39], 0 offen sc0 sc1\n"
"ds_read_b64_tr_b16 v[106:107], v231 offset:43008\n"
"v_mfma_f32_16x16x32_bf16 v[24:27], v[184:187], a[68:71], v[24:27]\n"
"ds_read_b64_tr_b16 v[104:105], v231 offset:40960\n"
"v_mfma_f32_16x16x32_bf16 v[24:27], v[180:183], a[64:67], v[24:27]\n"
"ds_read_b64_tr_b16 v[98:99], v231 offset:34816\n"
"v_cvt_pk_bf16_f32 v8, v8, v9\n"
"v_mfma_f32_16x16x32_bf16 v[40:43], v[180:183], a[80:83], v[40:43]\n"
"ds_read_b64_tr_b16 v[108:109], v235 offset:40960\n"
"v_mfma_f32_16x16x32_bf16 v[40:43], v[184:187], a[84:87], v[40:43]\n"
"ds_read_b64_tr_b16 v[118:119], v235 offset:51200\n"
"v_mfma_f32_16x16x32_bf16 v[40:43], v[188:191], a[88:91], v[40:43]\n"
"ds_read_b64_tr_b16 v[110:111], v235 offset:43008\n"
"v_mfma_f32_16x16x32_bf16 v[40:43], v[192:195], a[92:95], v[40:43]\n"
"ds_read_b64_tr_b16 v[114:115], v231 offset:51200\n"
"v_mfma_f32_16x16x32_bf16 v[40:43], v[208:211], a[32:35], v[40:43]\n"
"ds_read_b64_tr_b16 v[116:117], v235 offset:49152\n"
"v_mfma_f32_16x16x32_bf16 v[40:43], v[212:215], a[36:39], v[40:43]\n"
"ds_read_b64_tr_b16 v[224:225], v235 offset:57344\n"
"v_mov_b32 v161, 0\n"
"v_mfma_f32_16x16x32_bf16 v[40:43], v[216:219], a[40:43], v[40:43]\n"
"ds_read_b64_tr_b16 v[226:227], v235 offset:59392\n"
"v_mfma_f32_16x16x32_bf16 v[40:43], v[220:223], a[44:47], v[40:43]\n"
"s_waitcnt lgkmcnt(7)\n"
"v_cvt_pk_bf16_f32 v9, v10, v11\n"
"v_mfma_f32_16x16x32_bf16 v[12:15], v[96:99], a[48:51], v[12:15]\n"
"v_mfma_f32_16x16x32_bf16 v[12:15], v[100:103], a[52:55], v[12:15]\n"
"v_cvt_pk_bf16_f32 v24, v24, v25\n"
"v_mfma_f32_16x16x32_bf16 v[12:15], v[104:107], a[56:59], v[12:15]\n"
"s_waitcnt lgkmcnt(4)\n"
"v_mfma_f32_16x16x32_bf16 v[12:15], v[108:111], a[60:63], v[12:15]\n"
"s_waitcnt lgkmcnt(3)\n"
"v_mfma_f32_16x16x32_bf16 v[12:15], v[112:115], a[0:3], v[12:15]\n"
"s_waitcnt lgkmcnt(2)\n"
"v_mfma_f32_16x16x32_bf16 v[12:15], v[116:119], a[4:7], v[12:15]\n"
"v_mfma_f32_16x16x32_bf16 v[12:15], v[120:123], a[8:11], v[12:15]\n"
"s_waitcnt lgkmcnt(0)\n"
"v_mfma_f32_16x16x32_bf16 v[12:15], v[224:227], a[12:15], v[12:15]\n"
"s_waitcnt vmcnt(5)\n"
"v_mfma_f32_16x16x32_bf16 v[64:67], v[162:165], v[72:75], 0\n"
"v_cvt_pk_bf16_f32 v40, v40, v41\n"
"v_mfma_f32_16x16x32_bf16 v[64:67], v[162:165], v[76:79], v[64:67]\n"
"v_mfma_f32_16x16x32_bf16 v[64:67], v[162:165], v[80:83], v[64:67]\n"
"s_nop 2\n"
"v_cvt_pk_bf16_f32 v10, v12, v13\n"
"v_cvt_pk_bf16_f32 v11, v14, v15\n"
"v_mfma_f32_16x16x32_bf16 v[64:67], v[162:165], v[84:87], v[64:67]\n"
"v_mfma_f32_16x16x32_bf16 v[64:67], v[162:165], v[88:91], v[64:67]\n"
"v_permlane16_swap_b32 v9, v11\n"
"v_mfma_f32_16x16x32_bf16 v[64:67], v[162:165], v[92:95], v[64:67]\n"
"v_permlane16_swap_b32 v8, v10\n"
"v_mfma_f32_16x16x32_bf16 v[64:67], v[162:165], v[196:199], v[64:67]\n"
"v_mfma_f32_16x16x32_bf16 v[64:67], v[162:165], v[200:203], v[64:67]\n"
"buffer_store_dwordx4 v[8:11], v239, s[36:39], 0 offen sc0 sc1\n"
"v_mfma_f32_16x16x32_bf16 v[68:71], v[160:163], v[72:75], 0\n"
"v_mfma_f32_16x16x32_bf16 v[68:71], v[160:163], v[76:79], v[68:71]\n"
"v_cvt_pk_bf16_f32 v41, v42, v43\n"
"v_mfma_f32_16x16x32_bf16 v[68:71], v[160:163], v[80:83], v[68:71]\n"
"v_mfma_f32_16x16x32_bf16 v[68:71], v[160:163], v[84:87], v[68:71]\n"
"v_mfma_f32_16x16x32_bf16 v[68:71], v[160:163], v[88:91], v[68:71]\n"
"s_nop 0\n"
"v_cvt_pk_bf16_f32 v64, v64, v65\n"
"v_cvt_pk_bf16_f32 v65, v66, v67\n"
"v_mfma_f32_16x16x32_bf16 v[68:71], v[160:163], v[92:95], v[68:71]\n"
"v_mfma_f32_16x16x32_bf16 v[68:71], v[160:163], v[196:199], v[68:71]\n"
"v_mfma_f32_16x16x32_bf16 v[68:71], v[160:163], v[200:203], v[68:71]\n"
"v_mfma_f32_16x16x32_bf16 v[28:31], v[224:227], a[28:31], v[28:31]\n"
"v_cvt_pk_bf16_f32 v25, v26, v27\n"
"v_mfma_f32_16x16x32_bf16 v[28:31], v[120:123], a[24:27], v[28:31]\n"
"v_mfma_f32_16x16x32_bf16 v[28:31], v[116:119], a[20:23], v[28:31]\n"
"s_nop 3\n"
"v_cvt_pk_bf16_f32 v66, v68, v69\n"
"v_cvt_pk_bf16_f32 v67, v70, v71\n"
"v_mfma_f32_16x16x32_bf16 v[28:31], v[112:115], a[16:19], v[28:31]\n"
"s_nop 0\n"
"v_permlane16_swap_b32 v65, v67\n"
"v_mfma_f32_16x16x32_bf16 v[28:31], v[108:111], a[76:79], v[28:31]\n"
"v_mfma_f32_16x16x32_bf16 v[28:31], v[104:107], a[72:75], v[28:31]\n"
"v_permlane16_swap_b32 v64, v66\n"
"v_mfma_f32_16x16x32_bf16 v[28:31], v[100:103], a[68:71], v[28:31]\n"
"v_mfma_f32_16x16x32_bf16 v[28:31], v[96:99], a[64:67], v[28:31]\n"
"buffer_store_dwordx4 v[64:67], v253, s[36:39], 0 offen offset:128 sc0 sc1\n"
"v_mfma_f32_16x16x32_bf16 v[44:47], v[96:99], a[80:83], v[44:47]\n"
"v_mfma_f32_16x16x32_bf16 v[44:47], v[100:103], a[84:87], v[44:47]\n"
"v_mfma_f32_16x16x32_bf16 v[44:47], v[104:107], a[88:91], v[44:47]\n"
"v_mfma_f32_16x16x32_bf16 v[44:47], v[108:111], a[92:95], v[44:47]\n"
"s_nop 2\n"
"v_cvt_pk_bf16_f32 v26, v28, v29\n"
"v_cvt_pk_bf16_f32 v27, v30, v31\n"
"v_mfma_f32_16x16x32_bf16 v[44:47], v[112:115], a[32:35], v[44:47]\n"
"s_nop 0\n"
"v_permlane16_swap_b32 v25, v27\n"
"v_mfma_f32_16x16x32_bf16 v[44:47], v[116:119], a[36:39], v[44:47]\n"
"v_permlane16_swap_b32 v24, v26\n"
"v_mfma_f32_16x16x32_bf16 v[44:47], v[120:123], a[40:43], v[44:47]\n"
"v_mfma_f32_16x16x32_bf16 v[44:47], v[224:227], a[44:47], v[44:47]\n"
"buffer_store_dwordx4 v[24:27], v239, s[36:39], s52 offen sc0 sc1\n"
"s_nop 6\n"
"v_cvt_pk_bf16_f32 v42, v44, v45\n"
"v_cvt_pk_bf16_f32 v43, v46, v47\n"
"s_nop 0\n"
"v_permlane16_swap_b32 v40, v42\n"
"v_permlane16_swap_b32 v41, v43\n"
"buffer_store_dwordx4 v[40:43], v239, s[36:39], s53 offen sc0 sc1\n"
"s_branch LFIN_%=\n"
"LSW_%=:\n"
"s_add_u32 s16, s8, s95\n"
"s_addc_u32 s17, s9, 0\n"
"s_sub_u32 s18, 655360, s95\n"
"s_add_u32 s20, s8, s100\n"
"s_addc_u32 s21, s9, 0\n"
"s_sub_u32 s22, 655360, s100\n"
"s_mov_b32 s56, 0\n"
"s_mov_b32 s55, 0\n"
"s_mov_b32 s58, 0\n"
"s_mov_b32 s59, 163840\n"
"s_mov_b32 s52, 9216\n"
"s_mov_b32 s53, 18432\n"
"s_mov_b32 s54, 27648\n"
"v_lshrrev_b32 v206, 7, v242\n"
"v_mul_u32_u24 v206, 1280, v206\n"
"v_and_b32 v240, 0xff, v240\n"
"v_add_u32 v240, v206, v240\n"
"s_branch LSWK_%=\n"
"LGP_%=:\n"
"s_mov_b32 s80, 0x40000\n"
"LGPa_%=:\n"
"s_sleep 1\n"
"global_load_dword v244, v245, s[6:7] sc1\n"
"s_sub_u32 s80, s80, 1\n"
"s_cmp_eq_u32 s80, 0\n"
"s_waitcnt vmcnt(0)\n"
"s_cbranch_scc1 LGPK_%=\n"
"v_subrev_u32 v244, s21, v244\n"
"v_cmp_lt_u32 vcc, s69, v244\n"
"s_nop 4\n"
"s_cbranch_vccnz LGPa_%=\n"
"s_branch LGPK_%=\n"
"LGQ_%=:\n"
"s_mov_b32 s80, 0x40000\n"
"LGQa_%=:\n"
"s_sleep 1\n"
"global_load_dword v244, v245, s[6:7] sc1\n"
"s_sub_u32 s80, s80, 1\n"
"s_cmp_eq_u32 s80, 0\n"
"s_waitcnt vmcnt(0)\n"
"s_cbranch_scc1 LGQK_%=\n"
"v_subrev_u32 v244, s21, v244\n"
"v_cmp_lt_u32 vcc, s69, v244\n"
"s_nop 4\n"
"s_cbranch_vccnz LGQa_%=\n"
"s_branch LGQK_%=\n"
"LGR_%=:\n"
"s_mov_b32 s80, 0x40000\n"
"LGRa_%=:\n"
"s_sleep 1\n"
"global_load_dword v244, v245, s[6:7] sc1\n"
"s_sub_u32 s80, s80, 1\n"
"s_cmp_eq_u32 s80, 0\n"
"s_waitcnt vmcnt(0)\n"
"s_cbranch_scc1 LGRK_%=\n"
"v_subrev_u32 v244, s21, v244\n"
"v_cmp_lt_u32 vcc, s69, v244\n"
"s_nop 4\n"
"s_cbranch_vccnz LGRa_%=\n"
"s_branch LGRK_%=\n"
"LGS_%=:\n"
"v_mov_b32 v255, s93\n"
"s_mov_b64 exec, 1\n"
"global_store_dword v255, v241, s[6:7] sc1\n"
"s_mov_b64 exec, -1\n"
"s_branch LGSK_%=\n"
"LGT_%=:\n"
"v_mov_b32 v255, s93\n"
"s_mov_b64 exec, 1\n"
"global_store_dword v255, v241, s[6:7] sc1\n"
"s_mov_b64 exec, -1\n"
"s_branch LGTK_%=\n"
"LEP_%=:\n"
"s_add_u32 s92, s92, 1\n"
"v_mov_b32 v244, s92\n"
"v_mov_b32 v255, 0\n"
"s_mov_b64 exec, 1\n"
"global_store_dword v255, v244, s[6:7] offset:128 sc1\n"
"s_mov_b64 exec, -1\n"
"s_branch LEPK_%=\n"
"LPCE0_%=:\n"
"s_memrealtime s[80:81]\n"
"s_sub_u32 s82, s67, s66\n"
"s_cmp_gt_i32 s82, 0\n"
"s_cselect_b32 s83, s67, s66\n"
"s_waitcnt lgkmcnt(0)\n"
"s_sub_u32 s82, s83, s80\n"
"s_cmp_gt_i32 s82, 3\n"
"s_cbranch_scc0 LPEE0_%=\n"
"LPDE0_%=:\n"
"s_sleep 1\n"
"s_memrealtime s[80:81]\n"
"s_waitcnt lgkmcnt(0)\n"
"s_sub_u32 s82, s83, s80\n"
"s_cmp_gt_i32 s82, 3\n"
"s_cbranch_scc1 LPDE0_%=\n"
"LPEE0_%=:\n"
"s_add_u32 s66, s66, 126\n"
"s_add_u32 s67, s80, s72\n"
"s_sub_u32 s72, s72, 0\n"
"s_max_u32 s72, s72, 122\n"
"s_branch LPKE0_%=\n"
"LPCE1_%=:\n"
"s_memrealtime s[80:81]\n"
"s_sub_u32 s82, s67, s66\n"
"s_cmp_gt_i32 s82, 0\n"
"s_cselect_b32 s83, s67, s66\n"
"s_waitcnt lgkmcnt(0)\n"
"s_sub_u32 s82, s83, s80\n"
"s_cmp_gt_i32 s82, 3\n"
"s_cbranch_scc0 LPEE1_%=\n"
"LPDE1_%=:\n"
"s_sleep 1\n"
"s_memrealtime s[80:81]\n"
"s_waitcnt lgkmcnt(0)\n"
"s_sub_u32 s82, s83, s80\n"
"s_cmp_gt_i32 s82, 3\n"
"s_cbranch_scc1 LPDE1_%=\n"
"LPEE1_%=:\n"
"s_add_u32 s66, s66, 126\n"
"s_add_u32 s67, s80, s72\n"
"s_sub_u32 s72, s72, 0\n"
"s_max_u32 s72, s72, 122\n"
"s_branch LPKE1_%=\n"
"LPCT0_%=:\n"
"s_memrealtime s[80:81]\n"
"s_sub_u32 s82, s67, s66\n"
"s_cmp_gt_i32 s82, 0\n"
"s_cselect_b32 s83, s67, s66\n"
"s_waitcnt lgkmcnt(0)\n"
"s_sub_u32 s82, s83, s80\n"
"s_cmp_gt_i32 s82, 3\n"
"s_cbranch_scc0 LPET0_%=\n"
"LPDT0_%=:\n"
"s_sleep 1\n"
"s_memrealtime s[80:81]\n"
"s_waitcnt lgkmcnt(0)\n"
"s_sub_u32 s82, s83, s80\n"
"s_cmp_gt_i32 s82, 3\n"
"s_cbranch_scc1 LPDT0_%=\n"
"LPET0_%=:\n"
"s_add_u32 s66, s66, 126\n"
"s_add_u32 s67, s80, s72\n"
"s_sub_u32 s72, s72, 0\n"
"s_max_u32 s72, s72, 122\n"
"s_branch LPKT0_%=\n"
"LPCT1_%=:\n"
"s_memrealtime s[80:81]\n"
"s_sub_u32 s82, s67, s66\n"
"s_cmp_gt_i32 s82, 0\n"
"s_cselect_b32 s83, s67, s66\n"
"s_waitcnt lgkmcnt(0)\n"
"s_sub_u32 s82, s83, s80\n"
"s_cmp_gt_i32 s82, 3\n"
"s_cbranch_scc0 LPET1_%=\n"
"LPDT1_%=:\n"
"s_sleep 1\n"
"s_memrealtime s[80:81]\n"
"s_waitcnt lgkmcnt(0)\n"
"s_sub_u32 s82, s83, s80\n"
"s_cmp_gt_i32 s82, 3\n"
"s_cbranch_scc1 LPDT1_%=\n"
"LPET1_%=:\n"
"s_add_u32 s66, s66, 126\n"
"s_add_u32 s67, s80, s72\n"
"s_sub_u32 s72, s72, 0\n"
"s_max_u32 s72, s72, 122\n"
"s_branch LPKT1_%=\n"
"LPCL0_%=:\n"
"s_memrealtime s[80:81]\n"
"s_sub_u32 s82, s67, s66\n"
"s_cmp_gt_i32 s82, 0\n"
"s_cselect_b32 s83, s67, s66\n"
"s_waitcnt lgkmcnt(0)\n"
"s_sub_u32 s82, s83, s80\n"
"s_cmp_gt_i32 s82, 3\n"
"s_cbranch_scc0 LPEL0_%=\n"
"LPDL0_%=:\n"
"s_sleep 1\n"
"s_memrealtime s[80:81]\n"
"s_waitcnt lgkmcnt(0)\n"
"s_sub_u32 s82, s83, s80\n"
"s_cmp_gt_i32 s82, 3\n"
"s_cbranch_scc1 LPDL0_%=\n"
"LPEL0_%=:\n"
"s_add_u32 s66, s66, 126\n"
"s_add_u32 s67, s80, s72\n"
"s_sub_u32 s72, s72, 0\n"
"s_max_u32 s72, s72, 122\n"
"s_branch LPKL0_%=\n"
"LPCL1_%=:\n"
"s_memrealtime s[80:81]\n"
"s_sub_u32 s82, s67, s66\n"
"s_cmp_gt_i32 s82, 0\n"
"s_cselect_b32 s83, s67, s66\n"
"s_waitcnt lgkmcnt(0)\n"
"s_sub_u32 s82, s83, s80\n"
"s_cmp_gt_i32 s82, 3\n"
"s_cbranch_scc0 LPEL1_%=\n"
"LPDL1_%=:\n"
"s_sleep 1\n"
"s_memrealtime s[80:81]\n"
"s_waitcnt lgkmcnt(0)\n"
"s_sub_u32 s82, s83, s80\n"
"s_cmp_gt_i32 s82, 3\n"
"s_cbranch_scc1 LPDL1_%=\n"
"LPEL1_%=:\n"
"s_add_u32 s66, s66, 126\n"
"s_add_u32 s67, s80, s72\n"
"s_sub_u32 s72, s72, 0\n"
"s_max_u32 s72, s72, 122\n"
"s_branch LPKL1_%=\n"
"LPCL2_%=:\n"
"s_memrealtime s[80:81]\n"
"s_sub_u32 s82, s67, s66\n"
"s_cmp_gt_i32 s82, 0\n"
"s_cselect_b32 s83, s67, s66\n"
"s_waitcnt lgkmcnt(0)\n"
"s_sub_u32 s82, s83, s80\n"
"s_cmp_gt_i32 s82, 3\n"
"s_cbranch_scc0 LPEL2_%=\n"
"LPDL2_%=:\n"
"s_sleep 1\n"
"s_memrealtime s[80:81]\n"
"s_waitcnt lgkmcnt(0)\n"
"s_sub_u32 s82, s83, s80\n"
"s_cmp_gt_i32 s82, 3\n"
"s_cbranch_scc1 LPDL2_%=\n"
"LPEL2_%=:\n"
"s_add_u32 s66, s66, 126\n"
"s_add_u32 s67, s80, s72\n"
"s_sub_u32 s72, s72, 0\n"
"s_max_u32 s72, s72, 122\n"
"s_branch LPKL2_%=\n"
"LPCL3_%=:\n"
"s_memrealtime s[80:81]\n"
"s_sub_u32 s82, s67, s66\n"
"s_cmp_gt_i32 s82, 0\n"
"s_cselect_b32 s83, s67, s66\n"
"s_waitcnt lgkmcnt(0)\n"
"s_sub_u32 s82, s83, s80\n"
"s_cmp_gt_i32 s82, 3\n"
"s_cbranch_scc0 LPEL3_%=\n"
"LPDL3_%=:\n"
"s_sleep 1\n"
"s_memrealtime s[80:81]\n"
"s_waitcnt lgkmcnt(0)\n"
"s_sub_u32 s82, s83, s80\n"
"s_cmp_gt_i32 s82, 3\n"
"s_cbranch_scc1 LPDL3_%=\n"
"LPEL3_%=:\n"
"s_add_u32 s66, s66, 126\n"
"s_add_u32 s67, s80, s72\n"
"s_sub_u32 s72, s72, 0\n"
"s_max_u32 s72, s72, 122\n"
"s_branch LPKL3_%=\n"
"LEND_%=:\n"
"LSLOW_%=:\n"
"s_waitcnt vmcnt(0) lgkmcnt(0)\n"
"s_nop 9\n"
"v_bfe_u32 v148, v242, 4, 4\n"
"v_bfe_u32 v149, v242, 8, 2\n"
"v_lshlrev_b32 v144, 8, v148\n"
"v_lshl_add_u32 v144, v149, 4, v144\n"
"v_add_u32 v144, s43, v144\n"
"v_xor_b32 v145, 64, v144\n"
"v_lshlrev_b32 v146, 1, v148\n"
"v_lshl_add_u32 v146, v149, 10, v146\n"
"v_add_u32 v146, s43, v146\n"
"v_xor_b32 v147, 64, v146\n"
"v_mov_b32 v152, 0xff7fffff\n"
"v_mov_b32 v155, 0\n"
"v_mov_b32 v0, 0\n"
"v_mov_b32 v1, 0\n"
"v_mov_b32 v2, 0\n"
"v_mov_b32 v3, 0\n"
"v_mov_b32 v4, 0\n"
"v_mov_b32 v5, 0\n"
"v_mov_b32 v6, 0\n"
"v_mov_b32 v7, 0\n"
"v_mov_b32 v8, 0\n"
"v_mov_b32 v9, 0\n"
"v_mov_b32 v10, 0\n"
"v_mov_b32 v11, 0\n"
"v_mov_b32 v12, 0\n"
"v_mov_b32 v13, 0\n"
"v_mov_b32 v14, 0\n"
"v_mov_b32 v15, 0\n"
"v_mov_b32 v16, 0\n"
"v_mov_b32 v17, 0\n"
"v_mov_b32 v18, 0\n"
"v_mov_b32 v19, 0\n"
"v_mov_b32 v20, 0\n"
"v_mov_b32 v21, 0\n"
"v_mov_b32 v22, 0\n"
"v_mov_b32 v23, 0\n"
"v_mov_b32 v24, 0\n"
"v_mov_b32 v25, 0\n"
"v_mov_b32 v26, 0\n"
"v_mov_b32 v27, 0\n"
"v_mov_b32 v28, 0\n"
"v_mov_b32 v29, 0\n"
"v_mov_b32 v30, 0\n"
"v_mov_b32 v31, 0\n"
"v_mov_b32 v156, 0xff7fffff\n"
"v_mov_b32 v159, 0\n"
"v_mov_b32 v32, 0\n"
"v_mov_b32 v33, 0\n"
"v_mov_b32 v34, 0\n"
"v_mov_b32 v35, 0\n"
"v_mov_b32 v36, 0\n"
"v_mov_b32 v37, 0\n"
"v_mov_b32 v38, 0\n"
"v_mov_b32 v39, 0\n"
"v_mov_b32 v40, 0\n"
"v_mov_b32 v41, 0\n"
"v_mov_b32 v42, 0\n"
"v_mov_b32 v43, 0\n"
"v_mov_b32 v44, 0\n"
"v_mov_b32 v45, 0\n"
"v_mov_b32 v46, 0\n"
"v_mov_b32 v47, 0\n"
"v_mov_b32 v48, 0\n"
"v_mov_b32 v49, 0\n"
"v_mov_b32 v50, 0\n"
"v_mov_b32 v51, 0\n"
"v_mov_b32 v52, 0\n"
"v_mov_b32 v53, 0\n"
"v_mov_b32 v54, 0\n"
"v_mov_b32 v55, 0\n"
"v_mov_b32 v56, 0\n"
"v_mov_b32 v57, 0\n"
"v_mov_b32 v58, 0\n"
"v_mov_b32 v59, 0\n"
"v_mov_b32 v60, 0\n"
"v_mov_b32 v61, 0\n"
"v_mov_b32 v62, 0\n"
"v_mov_b32 v63, 0\n"
"v_mov_b32 v160, 0xff7fffff\n"
"v_mov_b32 v163, 0\n"
"v_mov_b32 v64, 0\n"
"v_mov_b32 v65, 0\n"
"v_mov_b32 v66, 0\n"
"v_mov_b32 v67, 0\n"
"v_mov_b32 v68, 0\n"
"v_mov_b32 v69, 0\n"
"v_mov_b32 v70, 0\n"
"v_mov_b32 v71, 0\n"
"v_mov_b32 v72, 0\n"
"v_mov_b32 v73, 0\n"
"v_mov_b32 v74, 0\n"
"v_mov_b32 v75, 0\n"
"v_mov_b32 v76, 0\n"
"v_mov_b32 v77, 0\n"
"v_mov_b32 v78, 0\n"
"v_mov_b32 v79, 0\n"
"v_mov_b32 v80, 0\n"
"v_mov_b32 v81, 0\n"
"v_mov_b32 v82, 0\n"
"v_mov_b32 v83, 0\n"
"v_mov_b32 v84, 0\n"
"v_mov_b32 v85, 0\n"
"v_mov_b32 v86, 0\n"
"v_mov_b32 v87, 0\n"
"v_mov_b32 v88, 0\n"
"v_mov_b32 v89, 0\n"
"v_mov_b32 v90, 0\n"
"v_mov_b32 v91, 0\n"
"v_mov_b32 v92, 0\n"
"v_mov_b32 v93, 0\n"
"v_mov_b32 v94, 0\n"
"v_mov_b32 v95, 0\n"
"v_mov_b32 v164, 0xff7fffff\n"
"v_mov_b32 v167, 0\n"
"v_accvgpr_write_b32 a208, 0\n"
"v_accvgpr_write_b32 a209, 0\n"
"v_accvgpr_write_b32 a210, 0\n"
"v_accvgpr_write_b32 a211, 0\n"
"v_accvgpr_write_b32 a212, 0\n"
"v_accvgpr_write_b32 a213, 0\n"
"v_accvgpr_write_b32 a214, 0\n"
"v_accvgpr_write_b32 a215, 0\n"
"v_accvgpr_write_b32 a216, 0\n"
"v_accvgpr_write_b32 a217, 0\n"
"v_accvgpr_write_b32 a218, 0\n"
"v_accvgpr_write_b32 a219, 0\n"
"v_accvgpr_write_b32 a220, 0\n"
"v_accvgpr_write_b32 a221, 0\n"
"v_accvgpr_write_b32 a222, 0\n"
"v_accvgpr_write_b32 a223, 0\n"
"v_accvgpr_write_b32 a224, 0\n"
"v_accvgpr_write_b32 a225, 0\n"
"v_accvgpr_write_b32 a226, 0\n"
"v_accvgpr_write_b32 a227, 0\n"
"v_accvgpr_write_b32 a228, 0\n"
"v_accvgpr_write_b32 a229, 0\n"
"v_accvgpr_write_b32 a230, 0\n"
"v_accvgpr_write_b32 a231, 0\n"
"v_accvgpr_write_b32 a232, 0\n"
"v_accvgpr_write_b32 a233, 0\n"
"v_accvgpr_write_b32 a234, 0\n"
"v_accvgpr_write_b32 a235, 0\n"
"v_accvgpr_write_b32 a236, 0\n"
"v_accvgpr_write_b32 a237, 0\n"
"v_accvgpr_write_b32 a238, 0\n"
"v_accvgpr_write_b32 a239, 0\n"
"s_lshl_b32 s80, s47, 16\n"
"s_sub_u32 s80, s80, 261248\n"
"s_add_u32 s81, s80, 4096\n"
"s_mov_b32 s82, 0\n"
"LSLOOP_%=:\n"
"s_lshl_b32 s83, s82, 6\n"
"s_and_b32 s83, s83, 64\n"
"buffer_load_dwordx4 v[128:131], v144, s[84:87], s80 offen offset:0\n"
"buffer_load_dwordx4 v[132:135], v145, s[84:87], s80 offen offset:0\n"
"buffer_load_dwordx4 v[136:139], v144, s[84:87], s80 offen offset:128\n"
"buffer_load_dwordx4 v[140:143], v145, s[84:87], s80 offen offset:128\n"
"s_waitcnt vmcnt(0)\n"
"v_mfma_f32_16x16x32_bf16 v[96:99], v[128:131], a[0:3], 0\n"
"v_mfma_f32_16x16x32_bf16 v[96:99], v[132:135], a[4:7], v[96:99]\n"
"v_mfma_f32_16x16x32_bf16 v[96:99], v[136:139], a[8:11], v[96:99]\n"
"v_mfma_f32_16x16x32_bf16 v[96:99], v[140:143], a[12:15], v[96:99]\n"
"v_mfma_f32_16x16x32_bf16 v[104:107], v[128:131], a[16:19], 0\n"
"v_mfma_f32_16x16x32_bf16 v[104:107], v[132:135], a[20:23], v[104:107]\n"
"v_mfma_f32_16x16x32_bf16 v[104:107], v[136:139], a[24:27], v[104:107]\n"
"v_mfma_f32_16x16x32_bf16 v[104:107], v[140:143], a[28:31], v[104:107]\n"
"v_mfma_f32_16x16x32_bf16 v[112:115], v[128:131], a[32:35], 0\n"
"v_mfma_f32_16x16x32_bf16 v[112:115], v[132:135], a[36:39], v[112:115]\n"
"v_mfma_f32_16x16x32_bf16 v[112:115], v[136:139], a[40:43], v[112:115]\n"
"v_mfma_f32_16x16x32_bf16 v[112:115], v[140:143], a[44:47], v[112:115]\n"
"v_mfma_f32_16x16x32_bf16 v[120:123], v[128:131], a[192:195], 0\n"
"v_mfma_f32_16x16x32_bf16 v[120:123], v[132:135], a[196:199], v[120:123]\n"
"v_mfma_f32_16x16x32_bf16 v[120:123], v[136:139], a[200:203], v[120:123]\n"
"v_mfma_f32_16x16x32_bf16 v[120:123], v[140:143], a[204:207], v[120:123]\n"
"s_nop 7\n"
"buffer_load_dwordx4 v[128:131], v144, s[84:87], s81 offen offset:0\n"
"buffer_load_dwordx4 v[132:135], v145, s[84:87], s81 offen offset:0\n"
"buffer_load_dwordx4 v[136:139], v144, s[84:87], s81 offen offset:128\n"
"buffer_load_dwordx4 v[140:143], v145, s[84:87], s81 offen offset:128\n"
"s_waitcnt vmcnt(0)\n"
"v_mfma_f32_16x16x32_bf16 v[100:103], v[128:131], a[0:3], 0\n"
"v_mfma_f32_16x16x32_bf16 v[100:103], v[132:135], a[4:7], v[100:103]\n"
"v_mfma_f32_16x16x32_bf16 v[100:103], v[136:139], a[8:11], v[100:103]\n"
"v_mfma_f32_16x16x32_bf16 v[100:103], v[140:143], a[12:15], v[100:103]\n"
"v_mfma_f32_16x16x32_bf16 v[108:111], v[128:131], a[16:19], 0\n"
"v_mfma_f32_16x16x32_bf16 v[108:111], v[132:135], a[20:23], v[108:111]\n"
"v_mfma_f32_16x16x32_bf16 v[108:111], v[136:139], a[24:27], v[108:111]\n"
"v_mfma_f32_16x16x32_bf16 v[108:111], v[140:143], a[28:31], v[108:111]\n"
"v_mfma_f32_16x16x32_bf16 v[116:119], v[128:131], a[32:35], 0\n"
"v_mfma_f32_16x16x32_bf16 v[116:119], v[132:135], a[36:39], v[116:119]\n"
"v_mfma_f32_16x16x32_bf16 v[116:119], v[136:139], a[40:43], v[116:119]\n"
"v_mfma_f32_16x16x32_bf16 v[116:119], v[140:143], a[44:47], v[116:119]\n"
"v_mfma_f32_16x16x32_bf16 v[124:127], v[128:131], a[192:195], 0\n"
"v_mfma_f32_16x16x32_bf16 v[124:127], v[132:135], a[196:199], v[124:127]\n"
"v_mfma_f32_16x16x32_bf16 v[124:127], v[136:139], a[200:203], v[124:127]\n"
"v_mfma_f32_16x16x32_bf16 v[124:127], v[140:143], a[204:207], v[124:127]\n"
"s_nop 7\n"
"v_max3_f32 v153, v96, v97, v98\n"
"v_max3_f32 v153, v153, v99, v100\n"
"v_max3_f32 v153, v153, v101, v102\n"
"v_max3_f32 v153, v153, v103, v152\n"
"v_mov_b32 v154, v153\n"
"s_nop 1\n"
"v_permlane32_swap_b32 v153, v154\n"
"v_max_f32 v153, v153, v154\n"
"v_mov_b32 v154, v153\n"
"s_nop 1\n"
"v_permlane16_swap_b32 v153, v154\n"
"v_max_f32 v153, v153, v154\n"
"v_sub_f32 v154, v152, v153\n"
"v_exp_f32 v154, v154\n"
"v_mov_b32 v152, v153\n"
"v_sub_f32 v96, v96, v152\n"
"v_sub_f32 v97, v97, v152\n"
"v_sub_f32 v98, v98, v152\n"
"v_sub_f32 v99, v99, v152\n"
"v_sub_f32 v100, v100, v152\n"
"v_sub_f32 v101, v101, v152\n"
"v_sub_f32 v102, v102, v152\n"
"v_sub_f32 v103, v103, v152\n"
"v_exp_f32 v96, v96\n"
"v_exp_f32 v97, v97\n"
"v_exp_f32 v98, v98\n"
"v_exp_f32 v99, v99\n"
"v_exp_f32 v100, v100\n"
"v_exp_f32 v101, v101\n"
"v_exp_f32 v102, v102\n"
"v_exp_f32 v103, v103\n"
"v_mul_f32 v155, v155, v154\n"
"v_add_f32 v155, v155, v96\n"
"v_add_f32 v155, v155, v97\n"
"v_add_f32 v155, v155, v98\n"
"v_add_f32 v155, v155, v99\n"
"v_add_f32 v155, v155, v100\n"
"v_add_f32 v155, v155, v101\n"
"v_add_f32 v155, v155, v102\n"
"v_add_f32 v155, v155, v103\n"
"v_cvt_pk_bf16_f32 v96, v96, v97\n"
"v_cvt_pk_bf16_f32 v97, v98, v99\n"
"v_cvt_pk_bf16_f32 v98, v100, v101\n"
"v_cvt_pk_bf16_f32 v99, v102, v103\n"
"v_mov_b32_dpp v154, v154 row_ror:12 row_mask:0x2 bank_mask:0xf\n"
"s_nop 1\n"
"v_mov_b32_dpp v154, v154 row_ror:8 row_mask:0x4 bank_mask:0xf\n"
"s_nop 1\n"
"v_mov_b32_dpp v154, v154 row_ror:4 row_mask:0x8 bank_mask:0xf\n"
"s_nop 1\n"
"v_mul_f32_dpp v0, v154, v0 row_newbcast:0 row_mask:0xf bank_mask:0xf\n"
"v_mul_f32_dpp v1, v154, v1 row_newbcast:1 row_mask:0xf bank_mask:0xf\n"
"v_mul_f32_dpp v2, v154, v2 row_newbcast:2 row_mask:0xf bank_mask:0xf\n"
"v_mul_f32_dpp v3, v154, v3 row_newbcast:3 row_mask:0xf bank_mask:0xf\n"
"v_mul_f32_dpp v4, v154, v4 row_newbcast:0 row_mask:0xf bank_mask:0xf\n"
"v_mul_f32_dpp v5, v154, v5 row_newbcast:1 row_mask:0xf bank_mask:0xf\n"
"v_mul_f32_dpp v6, v154, v6 row_newbcast:2 row_mask:0xf bank_mask:0xf\n"
"v_mul_f32_dpp v7, v154, v7 row_newbcast:3 row_mask:0xf bank_mask:0xf\n"
"v_mul_f32_dpp v8, v154, v8 row_newbcast:0 row_mask:0xf bank_mask:0xf\n"
"v_mul_f32_dpp v9, v154, v9 row_newbcast:1 row_mask:0xf bank_mask:0xf\n"
"v_mul_f32_dpp v10, v154, v10 row_newbcast:2 row_mask:0xf bank_mask:0xf\n"
"v_mul_f32_dpp v11, v154, v11 row_newbcast:3 row_mask:0xf bank_mask:0xf\n"
"v_mul_f32_dpp v12, v154, v12 row_newbcast:0 row_mask:0xf bank_mask:0xf\n"
"v_mul_f32_dpp v13, v154, v13 row_newbcast:1 row_mask:0xf bank_mask:0xf\n"
"v_mul_f32_dpp v14, v154, v14 row_newbcast:2 row_mask:0xf bank_mask:0xf\n"
"v_mul_f32_dpp v15, v154, v15 row_newbcast:3 row_mask:0xf bank_mask:0xf\n"
"v_mul_f32_dpp v16, v154, v16 row_newbcast:0 row_mask:0xf bank_mask:0xf\n"
"v_mul_f32_dpp v17, v154, v17 row_newbcast:1 row_mask:0xf bank_mask:0xf\n"
"v_mul_f32_dpp v18, v154, v18 row_newbcast:2 row_mask:0xf bank_mask:0xf\n"
"v_mul_f32_dpp v19, v154, v19 row_newbcast:3 row_mask:0xf bank_mask:0xf\n"
"v_mul_f32_dpp v20, v154, v20 row_newbcast:0 row_mask:0xf bank_mask:0xf\n"
"v_mul_f32_dpp v21, v154, v21 row_newbcast:1 row_mask:0xf bank_mask:0xf\n"
"v_mul_f32_dpp v22, v154, v22 row_newbcast:2 row_mask:0xf bank_mask:0xf\n"
"v_mul_f32_dpp v23, v154, v23 row_newbcast:3 row_mask:0xf bank_mask:0xf\n"
"v_mul_f32_dpp v24, v154, v24 row_newbcast:0 row_mask:0xf bank_mask:0xf\n"
"v_mul_f32_dpp v25, v154, v25 row_newbcast:1 row_mask:0xf bank_mask:0xf\n"
"v_mul_f32_dpp v26, v154, v26 row_newbcast:2 row_mask:0xf bank_mask:0xf\n"
"v_mul_f32_dpp v27, v154, v27 row_newbcast:3 row_mask:0xf bank_mask:0xf\n"
"v_mul_f32_dpp v28, v154, v28 row_newbcast:0 row_mask:0xf bank_mask:0xf\n"
"v_mul_f32_dpp v29, v154, v29 row_newbcast:1 row_mask:0xf bank_mask:0xf\n"
"v_mul_f32_dpp v30, v154, v30 row_newbcast:2 row_mask:0xf bank_mask:0xf\n"
"v_mul_f32_dpp v31, v154, v31 row_newbcast:3 row_mask:0xf bank_mask:0xf\n"
"v_max3_f32 v157, v104, v105, v106\n"
"v_max3_f32 v157, v157, v107, v108\n"
"v_max3_f32 v157, v157, v109, v110\n"
"v_max3_f32 v157, v157, v111, v156\n"
"v_mov_b32 v158, v157\n"
"s_nop 1\n"
"v_permlane32_swap_b32 v157, v158\n"
"v_max_f32 v157, v157, v158\n"
"v_mov_b32 v158, v157\n"
"s_nop 1\n"
"v_permlane16_swap_b32 v157, v158\n"
"v_max_f32 v157, v157, v158\n"
"v_sub_f32 v158, v156, v157\n"
"v_exp_f32 v158, v158\n"
"v_mov_b32 v156, v157\n"
"v_sub_f32 v104, v104, v156\n"
"v_sub_f32 v105, v105, v156\n"
"v_sub_f32 v106, v106, v156\n"
"v_sub_f32 v107, v107, v156\n"
"v_sub_f32 v108, v108, v156\n"
"v_sub_f32 v109, v109, v156\n"
"v_sub_f32 v110, v110, v156\n"
"v_sub_f32 v111, v111, v156\n"
"v_exp_f32 v104, v104\n"
"v_exp_f32 v105, v105\n"
"v_exp_f32 v106, v106\n"
"v_exp_f32 v107, v107\n"
"v_exp_f32 v108, v108\n"
"v_exp_f32 v109, v109\n"
"v_exp_f32 v110, v110\n"
"v_exp_f32 v111, v111\n"
"v_mul_f32 v159, v159, v158\n"
"v_add_f32 v159, v159, v104\n"
"v_add_f32 v159, v159, v105\n"
"v_add_f32 v159, v159, v106\n"
"v_add_f32 v159, v159, v107\n"
"v_add_f32 v159, v159, v108\n"
"v_add_f32 v159, v159, v109\n"
"v_add_f32 v159, v159, v110\n"
"v_add_f32 v159, v159, v111\n"
"v_cvt_pk_bf16_f32 v104, v104, v105\n"
"v_cvt_pk_bf16_f32 v105, v106, v107\n"
"v_cvt_pk_bf16_f32 v106, v108, v109\n"
"v_cvt_pk_bf16_f32 v107, v110, v111\n"
"v_mov_b32_dpp v158, v158 row_ror:12 row_mask:0x2 bank_mask:0xf\n"
"s_nop 1\n"
"v_mov_b32_dpp v158, v158 row_ror:8 row_mask:0x4 bank_mask:0xf\n"
"s_nop 1\n"
"v_mov_b32_dpp v158, v158 row_ror:4 row_mask:0x8 bank_mask:0xf\n"
"s_nop 1\n"
"v_mul_f32_dpp v32, v158, v32 row_newbcast:0 row_mask:0xf bank_mask:0xf\n"
"v_mul_f32_dpp v33, v158, v33 row_newbcast:1 row_mask:0xf bank_mask:0xf\n"
"v_mul_f32_dpp v34, v158, v34 row_newbcast:2 row_mask:0xf bank_mask:0xf\n"
"v_mul_f32_dpp v35, v158, v35 row_newbcast:3 row_mask:0xf bank_mask:0xf\n"
"v_mul_f32_dpp v36, v158, v36 row_newbcast:0 row_mask:0xf bank_mask:0xf\n"
"v_mul_f32_dpp v37, v158, v37 row_newbcast:1 row_mask:0xf bank_mask:0xf\n"
"v_mul_f32_dpp v38, v158, v38 row_newbcast:2 row_mask:0xf bank_mask:0xf\n"
"v_mul_f32_dpp v39, v158, v39 row_newbcast:3 row_mask:0xf bank_mask:0xf\n"
"v_mul_f32_dpp v40, v158, v40 row_newbcast:0 row_mask:0xf bank_mask:0xf\n"
"v_mul_f32_dpp v41, v158, v41 row_newbcast:1 row_mask:0xf bank_mask:0xf\n"
"v_mul_f32_dpp v42, v158, v42 row_newbcast:2 row_mask:0xf bank_mask:0xf\n"
"v_mul_f32_dpp v43, v158, v43 row_newbcast:3 row_mask:0xf bank_mask:0xf\n"
"v_mul_f32_dpp v44, v158, v44 row_newbcast:0 row_mask:0xf bank_mask:0xf\n"
"v_mul_f32_dpp v45, v158, v45 row_newbcast:1 row_mask:0xf bank_mask:0xf\n"
"v_mul_f32_dpp v46, v158, v46 row_newbcast:2 row_mask:0xf bank_mask:0xf\n"
"v_mul_f32_dpp v47, v158, v47 row_newbcast:3 row_mask:0xf bank_mask:0xf\n"
"v_mul_f32_dpp v48, v158, v48 row_newbcast:0 row_mask:0xf bank_mask:0xf\n"
"v_mul_f32_dpp v49, v158, v49 row_newbcast:1 row_mask:0xf bank_mask:0xf\n"
"v_mul_f32_dpp v50, v158, v50 row_newbcast:2 row_mask:0xf bank_mask:0xf\n"
"v_mul_f32_dpp v51, v158, v51 row_newbcast:3 row_mask:0xf bank_mask:0xf\n"
"v_mul_f32_dpp v52, v158, v52 row_newbcast:0 row_mask:0xf bank_mask:0xf\n"
"v_mul_f32_dpp v53, v158, v53 row_newbcast:1 row_mask:0xf bank_mask:0xf\n"
"v_mul_f32_dpp v54, v158, v54 row_newbcast:2 row_mask:0xf bank_mask:0xf\n"
"v_mul_f32_dpp v55, v158, v55 row_newbcast:3 row_mask:0xf bank_mask:0xf\n"
"v_mul_f32_dpp v56, v158, v56 row_newbcast:0 row_mask:0xf bank_mask:0xf\n"
"v_mul_f32_dpp v57, v158, v57 row_newbcast:1 row_mask:0xf bank_mask:0xf\n"
"v_mul_f32_dpp v58, v158, v58 row_newbcast:2 row_mask:0xf bank_mask:0xf\n"
"v_mul_f32_dpp v59, v158, v59 row_newbcast:3 row_mask:0xf bank_mask:0xf\n"
"v_mul_f32_dpp v60, v158, v60 row_newbcast:0 row_mask:0xf bank_mask:0xf\n"
"v_mul_f32_dpp v61, v158, v61 row_newbcast:1 row_mask:0xf bank_mask:0xf\n"
"v_mul_f32_dpp v62, v158, v62 row_newbcast:2 row_mask:0xf bank_mask:0xf\n"
"v_mul_f32_dpp v63, v158, v63 row_newbcast:3 row_mask:0xf bank_mask:0xf\n"
"v_max3_f32 v161, v112, v113, v114\n"
"v_max3_f32 v161, v161, v115, v116\n"
"v_max3_f32 v161, v161, v117, v118\n"
"v_max3_f32 v161, v161, v119, v160\n"
"v_mov_b32 v162, v161\n"
"s_nop 1\n"
"v_permlane32_swap_b32 v161, v162\n"
"v_max_f32 v161, v161, v162\n"
"v_mov_b32 v162, v161\n"
"s_nop 1\n"
"v_permlane16_swap_b32 v161, v162\n"
"v_max_f32 v161, v161, v162\n"
"v_sub_f32 v162, v160, v161\n"
"v_exp_f32 v162, v162\n"
"v_mov_b32 v160, v161\n"
"v_sub_f32 v112, v112, v160\n"
"v_sub_f32 v113, v113, v160\n"
"v_sub_f32 v114, v114, v160\n"
"v_sub_f32 v115, v115, v160\n"
"v_sub_f32 v116, v116, v160\n"
"v_sub_f32 v117, v117, v160\n"
"v_sub_f32 v118, v118, v160\n"
"v_sub_f32 v119, v119, v160\n"
"v_exp_f32 v112, v112\n"
"v_exp_f32 v113, v113\n"
"v_exp_f32 v114, v114\n"
"v_exp_f32 v115, v115\n"
"v_exp_f32 v116, v116\n"
"v_exp_f32 v117, v117\n"
"v_exp_f32 v118, v118\n"
"v_exp_f32 v119, v119\n"
"v_mul_f32 v163, v163, v162\n"
"v_add_f32 v163, v163, v112\n"
"v_add_f32 v163, v163, v113\n"
"v_add_f32 v163, v163, v114\n"
"v_add_f32 v163, v163, v115\n"
"v_add_f32 v163, v163, v116\n"
"v_add_f32 v163, v163, v117\n"
"v_add_f32 v163, v163, v118\n"
"v_add_f32 v163, v163, v119\n"
"v_cvt_pk_bf16_f32 v112, v112, v113\n"
"v_cvt_pk_bf16_f32 v113, v114, v115\n"
"v_cvt_pk_bf16_f32 v114, v116, v117\n"
"v_cvt_pk_bf16_f32 v115, v118, v119\n"
"v_mov_b32_dpp v162, v162 row_ror:12 row_mask:0x2 bank_mask:0xf\n"
"s_nop 1\n"
"v_mov_b32_dpp v162, v162 row_ror:8 row_mask:0x4 bank_mask:0xf\n"
"s_nop 1\n"
"v_mov_b32_dpp v162, v162 row_ror:4 row_mask:0x8 bank_mask:0xf\n"
"s_nop 1\n"
"v_mul_f32_dpp v64, v162, v64 row_newbcast:0 row_mask:0xf bank_mask:0xf\n"
"v_mul_f32_dpp v65, v162, v65 row_newbcast:1 row_mask:0xf bank_mask:0xf\n"
"v_mul_f32_dpp v66, v162, v66 row_newbcast:2 row_mask:0xf bank_mask:0xf\n"
"v_mul_f32_dpp v67, v162, v67 row_newbcast:3 row_mask:0xf bank_mask:0xf\n"
"v_mul_f32_dpp v68, v162, v68 row_newbcast:0 row_mask:0xf bank_mask:0xf\n"
"v_mul_f32_dpp v69, v162, v69 row_newbcast:1 row_mask:0xf bank_mask:0xf\n"
"v_mul_f32_dpp v70, v162, v70 row_newbcast:2 row_mask:0xf bank_mask:0xf\n"
"v_mul_f32_dpp v71, v162, v71 row_newbcast:3 row_mask:0xf bank_mask:0xf\n"
"v_mul_f32_dpp v72, v162, v72 row_newbcast:0 row_mask:0xf bank_mask:0xf\n"
"v_mul_f32_dpp v73, v162, v73 row_newbcast:1 row_mask:0xf bank_mask:0xf\n"
"v_mul_f32_dpp v74, v162, v74 row_newbcast:2 row_mask:0xf bank_mask:0xf\n"
"v_mul_f32_dpp v75, v162, v75 row_newbcast:3 row_mask:0xf bank_mask:0xf\n"
"v_mul_f32_dpp v76, v162, v76 row_newbcast:0 row_mask:0xf bank_mask:0xf\n"
"v_mul_f32_dpp v77, v162, v77 row_newbcast:1 row_mask:0xf bank_mask:0xf\n"
"v_mul_f32_dpp v78, v162, v78 row_newbcast:2 row_mask:0xf bank_mask:0xf\n"
"v_mul_f32_dpp v79, v162, v79 row_newbcast:3 row_mask:0xf bank_mask:0xf\n"
"v_mul_f32_dpp v80, v162, v80 row_newbcast:0 row_mask:0xf bank_mask:0xf\n"
"v_mul_f32_dpp v81, v162, v81 row_newbcast:1 row_mask:0xf bank_mask:0xf\n"
"v_mul_f32_dpp v82, v162, v82 row_newbcast:2 row_mask:0xf bank_mask:0xf\n"
"v_mul_f32_dpp v83, v162, v83 row_newbcast:3 row_mask:0xf bank_mask:0xf\n"
"v_mul_f32_dpp v84, v162, v84 row_newbcast:0 row_mask:0xf bank_mask:0xf\n"
"v_mul_f32_dpp v85, v162, v85 row_newbcast:1 row_mask:0xf bank_mask:0xf\n"
"v_mul_f32_dpp v86, v162, v86 row_newbcast:2 row_mask:0xf bank_mask:0xf\n"
"v_mul_f32_dpp v87, v162, v87 row_newbcast:3 row_mask:0xf bank_mask:0xf\n"
"v_mul_f32_dpp v88, v162, v88 row_newbcast:0 row_mask:0xf bank_mask:0xf\n"
"v_mul_f32_dpp v89, v162, v89 row_newbcast:1 row_mask:0xf bank_mask:0xf\n"
"v_mul_f32_dpp v90, v162, v90 row_newbcast:2 row_mask:0xf bank_mask:0xf\n"
"v_mul_f32_dpp v91, v162, v91 row_newbcast:3 row_mask:0xf bank_mask:0xf\n"
"v_mul_f32_dpp v92, v162, v92 row_newbcast:0 row_mask:0xf bank_mask:0xf\n"
"v_mul_f32_dpp v93, v162, v93 row_newbcast:1 row_mask:0xf bank_mask:0xf\n"
"v_mul_f32_dpp v94, v162, v94 row_newbcast:2 row_mask:0xf bank_mask:0xf\n"
"v_mul_f32_dpp v95, v162, v95 row_newbcast:3 row_mask:0xf bank_mask:0xf\n"
"s_cmp_lg_u32 s83, s43\n"
"s_cbranch_scc1 LSK3A_%=\n"
"v_max3_f32 v165, v120, v121, v122\n"
"v_max3_f32 v165, v165, v123, v124\n"
"v_max3_f32 v165, v165, v125, v126\n"
"v_max3_f32 v165, v165, v127, v164\n"
"v_mov_b32 v166, v165\n"
"s_nop 1\n"
"v_permlane32_swap_b32 v165, v166\n"
"v_max_f32 v165, v165, v166\n"
"v_mov_b32 v166, v165\n"
"s_nop 1\n"
"v_permlane16_swap_b32 v165, v166\n"
"v_max_f32 v165, v165, v166\n"
"v_sub_f32 v166, v164, v165\n"
"v_exp_f32 v166, v166\n"
"v_mov_b32 v164, v165\n"
"v_sub_f32 v120, v120, v164\n"
"v_sub_f32 v121, v121, v164\n"
"v_sub_f32 v122, v122, v164\n"
"v_sub_f32 v123, v123, v164\n"
"v_sub_f32 v124, v124, v164\n"
"v_sub_f32 v125, v125, v164\n"
"v_sub_f32 v126, v126, v164\n"
"v_sub_f32 v127, v127, v164\n"
"v_exp_f32 v120, v120\n"
"v_exp_f32 v121, v121\n"
"v_exp_f32 v122, v122\n"
"v_exp_f32 v123, v123\n"
"v_exp_f32 v124, v124\n"
"v_exp_f32 v125, v125\n"
"v_exp_f32 v126, v126\n"
"v_exp_f32 v127, v127\n"
"v_mul_f32 v167, v167, v166\n"
"v_add_f32 v167, v167, v120\n"
"v_add_f32 v167, v167, v121\n"
"v_add_f32 v167, v167, v122\n"
"v_add_f32 v167, v167, v123\n"
"v_add_f32 v167, v167, v124\n"
"v_add_f32 v167, v167, v125\n"
"v_add_f32 v167, v167, v126\n"
"v_add_f32 v167, v167, v127\n"
"v_cvt_pk_bf16_f32 v120, v120, v121\n"
"v_cvt_pk_bf16_f32 v121, v122, v123\n"
"v_cvt_pk_bf16_f32 v122, v124, v125\n"
"v_cvt_pk_bf16_f32 v123, v126, v127\n"
"v_mov_b32_dpp v166, v166 row_ror:12 row_mask:0x2 bank_mask:0xf\n"
"s_nop 1\n"
"v_mov_b32_dpp v166, v166 row_ror:8 row_mask:0x4 bank_mask:0xf\n"
"s_nop 1\n"
"v_mov_b32_dpp v166, v166 row_ror:4 row_mask:0x8 bank_mask:0xf\n"
"v_accvgpr_read_b32 v124, a208\n"
"s_nop 1\n"
"v_mul_f32_dpp v124, v166, v124 row_newbcast:0 row_mask:0xf bank_mask:0xf\n"
"v_accvgpr_write_b32 a208, v124\n"
"v_accvgpr_read_b32 v125, a209\n"
"s_nop 1\n"
"v_mul_f32_dpp v125, v166, v125 row_newbcast:1 row_mask:0xf bank_mask:0xf\n"
"v_accvgpr_write_b32 a209, v125\n"
"v_accvgpr_read_b32 v126, a210\n"
"s_nop 1\n"
"v_mul_f32_dpp v126, v166, v126 row_newbcast:2 row_mask:0xf bank_mask:0xf\n"
"v_accvgpr_write_b32 a210, v126\n"
"v_accvgpr_read_b32 v127, a211\n"
"s_nop 1\n"
"v_mul_f32_dpp v127, v166, v127 row_newbcast:3 row_mask:0xf bank_mask:0xf\n"
"v_accvgpr_write_b32 a211, v127\n"
"v_accvgpr_read_b32 v124, a212\n"
"s_nop 1\n"
"v_mul_f32_dpp v124, v166, v124 row_newbcast:0 row_mask:0xf bank_mask:0xf\n"
"v_accvgpr_write_b32 a212, v124\n"
"v_accvgpr_read_b32 v125, a213\n"
"s_nop 1\n"
"v_mul_f32_dpp v125, v166, v125 row_newbcast:1 row_mask:0xf bank_mask:0xf\n"
"v_accvgpr_write_b32 a213, v125\n"
"v_accvgpr_read_b32 v126, a214\n"
"s_nop 1\n"
"v_mul_f32_dpp v126, v166, v126 row_newbcast:2 row_mask:0xf bank_mask:0xf\n"
"v_accvgpr_write_b32 a214, v126\n"
"v_accvgpr_read_b32 v127, a215\n"
"s_nop 1\n"
"v_mul_f32_dpp v127, v166, v127 row_newbcast:3 row_mask:0xf bank_mask:0xf\n"
"v_accvgpr_write_b32 a215, v127\n"
"v_accvgpr_read_b32 v124, a216\n"
"s_nop 1\n"
"v_mul_f32_dpp v124, v166, v124 row_newbcast:0 row_mask:0xf bank_mask:0xf\n"
"v_accvgpr_write_b32 a216, v124\n"
"v_accvgpr_read_b32 v125, a217\n"
"s_nop 1\n"
"v_mul_f32_dpp v125, v166, v125 row_newbcast:1 row_mask:0xf bank_mask:0xf\n"
"v_accvgpr_write_b32 a217, v125\n"
"v_accvgpr_read_b32 v126, a218\n"
"s_nop 1\n"
"v_mul_f32_dpp v126, v166, v126 row_newbcast:2 row_mask:0xf bank_mask:0xf\n"
"v_accvgpr_write_b32 a218, v126\n"
"v_accvgpr_read_b32 v127, a219\n"
"s_nop 1\n"
"v_mul_f32_dpp v127, v166, v127 row_newbcast:3 row_mask:0xf bank_mask:0xf\n"
"v_accvgpr_write_b32 a219, v127\n"
"v_accvgpr_read_b32 v124, a220\n"
"s_nop 1\n"
"v_mul_f32_dpp v124, v166, v124 row_newbcast:0 row_mask:0xf bank_mask:0xf\n"
"v_accvgpr_write_b32 a220, v124\n"
"v_accvgpr_read_b32 v125, a221\n"
"s_nop 1\n"
"v_mul_f32_dpp v125, v166, v125 row_newbcast:1 row_mask:0xf bank_mask:0xf\n"
"v_accvgpr_write_b32 a221, v125\n"
"v_accvgpr_read_b32 v126, a222\n"
"s_nop 1\n"
"v_mul_f32_dpp v126, v166, v126 row_newbcast:2 row_mask:0xf bank_mask:0xf\n"
"v_accvgpr_write_b32 a222, v126\n"
"v_accvgpr_read_b32 v127, a223\n"
"s_nop 1\n"
"v_mul_f32_dpp v127, v166, v127 row_newbcast:3 row_mask:0xf bank_mask:0xf\n"
"v_accvgpr_write_b32 a223, v127\n"
"v_accvgpr_read_b32 v124, a224\n"
"s_nop 1\n"
"v_mul_f32_dpp v124, v166, v124 row_newbcast:0 row_mask:0xf bank_mask:0xf\n"
"v_accvgpr_write_b32 a224, v124\n"
"v_accvgpr_read_b32 v125, a225\n"
"s_nop 1\n"
"v_mul_f32_dpp v125, v166, v125 row_newbcast:1 row_mask:0xf bank_mask:0xf\n"
"v_accvgpr_write_b32 a225, v125\n"
"v_accvgpr_read_b32 v126, a226\n"
"s_nop 1\n"
"v_mul_f32_dpp v126, v166, v126 row_newbcast:2 row_mask:0xf bank_mask:0xf\n"
"v_accvgpr_write_b32 a226, v126\n"
"v_accvgpr_read_b32 v127, a227\n"
"s_nop 1\n"
"v_mul_f32_dpp v127, v166, v127 row_newbcast:3 row_mask:0xf bank_mask:0xf\n"
"v_accvgpr_write_b32 a227, v127\n"
"v_accvgpr_read_b32 v124, a228\n"
"s_nop 1\n"
"v_mul_f32_dpp v124, v166, v124 row_newbcast:0 row_mask:0xf bank_mask:0xf\n"
"v_accvgpr_write_b32 a228, v124\n"
"v_accvgpr_read_b32 v125, a229\n"
"s_nop 1\n"
"v_mul_f32_dpp v125, v166, v125 row_newbcast:1 row_mask:0xf bank_mask:0xf\n"
"v_accvgpr_write_b32 a229, v125\n"
"v_accvgpr_read_b32 v126, a230\n"
"s_nop 1\n"
"v_mul_f32_dpp v126, v166, v126 row_newbcast:2 row_mask:0xf bank_mask:0xf\n"
"v_accvgpr_write_b32 a230, v126\n"
"v_accvgpr_read_b32 v127, a231\n"
"s_nop 1\n"
"v_mul_f32_dpp v127, v166, v127 row_newbcast:3 row_mask:0xf bank_mask:0xf\n"
"v_accvgpr_write_b32 a231, v127\n"
"v_accvgpr_read_b32 v124, a232\n"
"s_nop 1\n"
"v_mul_f32_dpp v124, v166, v124 row_newbcast:0 row_mask:0xf bank_mask:0xf\n"
"v_accvgpr_write_b32 a232, v124\n"
"v_accvgpr_read_b32 v125, a233\n"
"s_nop 1\n"
"v_mul_f32_dpp v125, v166, v125 row_newbcast:1 row_mask:0xf bank_mask:0xf\n"
"v_accvgpr_write_b32 a233, v125\n"
"v_accvgpr_read_b32 v126, a234\n"
"s_nop 1\n"
"v_mul_f32_dpp v126, v166, v126 row_newbcast:2 row_mask:0xf bank_mask:0xf\n"
"v_accvgpr_write_b32 a234, v126\n"
"v_accvgpr_read_b32 v127, a235\n"
"s_nop 1\n"
"v_mul_f32_dpp v127, v166, v127 row_newbcast:3 row_mask:0xf bank_mask:0xf\n"
"v_accvgpr_write_b32 a235, v127\n"
"v_accvgpr_read_b32 v124, a236\n"
"s_nop 1\n"
"v_mul_f32_dpp v124, v166, v124 row_newbcast:0 row_mask:0xf bank_mask:0xf\n"
"v_accvgpr_write_b32 a236, v124\n"
"v_accvgpr_read_b32 v125, a237\n"
"s_nop 1\n"
"v_mul_f32_dpp v125, v166, v125 row_newbcast:1 row_mask:0xf bank_mask:0xf\n"
"v_accvgpr_write_b32 a237, v125\n"
"v_accvgpr_read_b32 v126, a238\n"
"s_nop 1\n"
"v_mul_f32_dpp v126, v166, v126 row_newbcast:2 row_mask:0xf bank_mask:0xf\n"
"v_accvgpr_write_b32 a238, v126\n"
"v_accvgpr_read_b32 v127, a239\n"
"s_nop 1\n"
"v_mul_f32_dpp v127, v166, v127 row_newbcast:3 row_mask:0xf bank_mask:0xf\n"
"v_accvgpr_write_b32 a239, v127\n"
"LSK3A_%=:\n"
"s_nop 3\n"
"buffer_load_ushort v128, v146, s[88:91], s80 offen offset:0\n"
"buffer_load_ushort v129, v146, s[88:91], s80 offen offset:256\n"
"buffer_load_ushort v130, v146, s[88:91], s80 offen offset:512\n"
"buffer_load_ushort v131, v146, s[88:91], s80 offen offset:768\n"
"buffer_load_ushort v132, v146, s[88:91], s81 offen offset:0\n"
"buffer_load_ushort v133, v146, s[88:91], s81 offen offset:256\n"
"buffer_load_ushort v134, v146, s[88:91], s81 offen offset:512\n"
"buffer_load_ushort v135, v146, s[88:91], s81 offen offset:768\n"
"buffer_load_ushort v136, v146, s[88:91], s80 offen offset:32\n"
"buffer_load_ushort v137, v146, s[88:91], s80 offen offset:288\n"
"buffer_load_ushort v138, v146, s[88:91], s80 offen offset:544\n"
"buffer_load_ushort v139, v146, s[88:91], s80 offen offset:800\n"
"buffer_load_ushort v140, v146, s[88:91], s81 offen offset:32\n"
"buffer_load_ushort v141, v146, s[88:91], s81 offen offset:288\n"
"buffer_load_ushort v142, v146, s[88:91], s81 offen offset:544\n"
"buffer_load_ushort v143, v146, s[88:91], s81 offen offset:800\n"
"s_waitcnt vmcnt(0)\n"
"v_lshl_or_b32 v128, v129, 16, v128\n"
"v_lshl_or_b32 v129, v131, 16, v130\n"
"v_lshl_or_b32 v130, v133, 16, v132\n"
"v_lshl_or_b32 v131, v135, 16, v134\n"
"v_lshl_or_b32 v136, v137, 16, v136\n"
"v_lshl_or_b32 v137, v139, 16, v138\n"
"v_lshl_or_b32 v138, v141, 16, v140\n"
"v_lshl_or_b32 v139, v143, 16, v142\n"
"v_mfma_f32_16x16x32_bf16 v[0:3], v[96:99], v[128:131], v[0:3]\n"
"s_nop 0\n"
"v_mfma_f32_16x16x32_bf16 v[4:7], v[96:99], v[136:139], v[4:7]\n"
"v_mfma_f32_16x16x32_bf16 v[32:35], v[104:107], v[128:131], v[32:35]\n"
"v_mfma_f32_16x16x32_bf16 v[36:39], v[104:107], v[136:139], v[36:39]\n"
"v_mfma_f32_16x16x32_bf16 v[64:67], v[112:115], v[128:131], v[64:67]\n"
"v_mfma_f32_16x16x32_bf16 v[68:71], v[112:115], v[136:139], v[68:71]\n"
"s_cmp_lg_u32 s83, s43\n"
"s_cbranch_scc1 LSK3B0_%=\n"
"v_mfma_f32_16x16x32_bf16 a[208:211], v[120:123], v[128:131], a[208:211]\n"
"v_mfma_f32_16x16x32_bf16 a[212:215], v[120:123], v[136:139], a[212:215]\n"
"LSK3B0_%=:\n"
"s_nop 3\n"
"buffer_load_ushort v128, v147, s[88:91], s80 offen offset:0\n"
"buffer_load_ushort v129, v147, s[88:91], s80 offen offset:256\n"
"buffer_load_ushort v130, v147, s[88:91], s80 offen offset:512\n"
"buffer_load_ushort v131, v147, s[88:91], s80 offen offset:768\n"
"buffer_load_ushort v132, v147, s[88:91], s81 offen offset:0\n"
"buffer_load_ushort v133, v147, s[88:91], s81 offen offset:256\n"
"buffer_load_ushort v134, v147, s[88:91], s81 offen offset:512\n"
"buffer_load_ushort v135, v147, s[88:91], s81 offen offset:768\n"
"buffer_load_ushort v136, v147, s[88:91], s80 offen offset:32\n"
"buffer_load_ushort v137, v147, s[88:91], s80 offen offset:288\n"
"buffer_load_ushort v138, v147, s[88:91], s80 offen offset:544\n"
"buffer_load_ushort v139, v147, s[88:91], s80 offen offset:800\n"
"buffer_load_ushort v140, v147, s[88:91], s81 offen offset:32\n"
"buffer_load_ushort v141, v147, s[88:91], s81 offen offset:288\n"
"buffer_load_ushort v142, v147, s[88:91], s81 offen offset:544\n"
"buffer_load_ushort v143, v147, s[88:91], s81 offen offset:800\n"
"s_waitcnt vmcnt(0)\n"
"v_lshl_or_b32 v128, v129, 16, v128\n"
"v_lshl_or_b32 v129, v131, 16, v130\n"
"v_lshl_or_b32 v130, v133, 16, v132\n"
"v_lshl_or_b32 v131, v135, 16, v134\n"
"v_lshl_or_b32 v136, v137, 16, v136\n"
"v_lshl_or_b32 v137, v139, 16, v138\n"
"v_lshl_or_b32 v138, v141, 16, v140\n"
"v_lshl_or_b32 v139, v143, 16, v142\n"
"v_mfma_f32_16x16x32_bf16 v[8:11], v[96:99], v[128:131], v[8:11]\n"
"s_nop 0\n"
"v_mfma_f32_16x16x32_bf16 v[12:15], v[96:99], v[136:139], v[12:15]\n"
"v_mfma_f32_16x16x32_bf16 v[40:43], v[104:107], v[128:131], v[40:43]\n"
"v_mfma_f32_16x16x32_bf16 v[44:47], v[104:107], v[136:139], v[44:47]\n"
"v_mfma_f32_16x16x32_bf16 v[72:75], v[112:115], v[128:131], v[72:75]\n"
"v_mfma_f32_16x16x32_bf16 v[76:79], v[112:115], v[136:139], v[76:79]\n"
"s_cmp_lg_u32 s83, s43\n"
"s_cbranch_scc1 LSK3B1_%=\n"
"v_mfma_f32_16x16x32_bf16 a[216:219], v[120:123], v[128:131], a[216:219]\n"
"v_mfma_f32_16x16x32_bf16 a[220:223], v[120:123], v[136:139], a[220:223]\n"
"LSK3B1_%=:\n"
"s_nop 3\n"
"buffer_load_ushort v128, v146, s[88:91], s80 offen offset:128\n"
"buffer_load_ushort v129, v146, s[88:91], s80 offen offset:384\n"
"buffer_load_ushort v130, v146, s[88:91], s80 offen offset:640\n"
"buffer_load_ushort v131, v146, s[88:91], s80 offen offset:896\n"
"buffer_load_ushort v132, v146, s[88:91], s81 offen offset:128\n"
"buffer_load_ushort v133, v146, s[88:91], s81 offen offset:384\n"
"buffer_load_ushort v134, v146, s[88:91], s81 offen offset:640\n"
"buffer_load_ushort v135, v146, s[88:91], s81 offen offset:896\n"
"buffer_load_ushort v136, v146, s[88:91], s80 offen offset:160\n"
"buffer_load_ushort v137, v146, s[88:91], s80 offen offset:416\n"
"buffer_load_ushort v138, v146, s[88:91], s80 offen offset:672\n"
"buffer_load_ushort v139, v146, s[88:91], s80 offen offset:928\n"
"buffer_load_ushort v140, v146, s[88:91], s81 offen offset:160\n"
"buffer_load_ushort v141, v146, s[88:91], s81 offen offset:416\n"
"buffer_load_ushort v142, v146, s[88:91], s81 offen offset:672\n"
"buffer_load_ushort v143, v146, s[88:91], s81 offen offset:928\n"
"s_waitcnt vmcnt(0)\n"
"v_lshl_or_b32 v128, v129, 16, v128\n"
"v_lshl_or_b32 v129, v131, 16, v130\n"
"v_lshl_or_b32 v130, v133, 16, v132\n"
"v_lshl_or_b32 v131, v135, 16, v134\n"
"v_lshl_or_b32 v136, v137, 16, v136\n"
"v_lshl_or_b32 v137, v139, 16, v138\n"
"v_lshl_or_b32 v138, v141, 16, v140\n"
"v_lshl_or_b32 v139, v143, 16, v142\n"
"v_mfma_f32_16x16x32_bf16 v[16:19], v[96:99], v[128:131], v[16:19]\n"
"s_nop 0\n"
"v_mfma_f32_16x16x32_bf16 v[20:23], v[96:99], v[136:139], v[20:23]\n"
"v_mfma_f32_16x16x32_bf16 v[48:51], v[104:107], v[128:131], v[48:51]\n"
"v_mfma_f32_16x16x32_bf16 v[52:55], v[104:107], v[136:139], v[52:55]\n"
"v_mfma_f32_16x16x32_bf16 v[80:83], v[112:115], v[128:131], v[80:83]\n"
"v_mfma_f32_16x16x32_bf16 v[84:87], v[112:115], v[136:139], v[84:87]\n"
"s_cmp_lg_u32 s83, s43\n"
"s_cbranch_scc1 LSK3B2_%=\n"
"v_mfma_f32_16x16x32_bf16 a[224:227], v[120:123], v[128:131], a[224:227]\n"
"v_mfma_f32_16x16x32_bf16 a[228:231], v[120:123], v[136:139], a[228:231]\n"
"LSK3B2_%=:\n"
"s_nop 3\n"
"buffer_load_ushort v128, v147, s[88:91], s80 offen offset:128\n"
"buffer_load_ushort v129, v147, s[88:91], s80 offen offset:384\n"
"buffer_load_ushort v130, v147, s[88:91], s80 offen offset:640\n"
"buffer_load_ushort v131, v147, s[88:91], s80 offen offset:896\n"
"buffer_load_ushort v132, v147, s[88:91], s81 offen offset:128\n"
"buffer_load_ushort v133, v147, s[88:91], s81 offen offset:384\n"
"buffer_load_ushort v134, v147, s[88:91], s81 offen offset:640\n"
"buffer_load_ushort v135, v147, s[88:91], s81 offen offset:896\n"
"buffer_load_ushort v136, v147, s[88:91], s80 offen offset:160\n"
"buffer_load_ushort v137, v147, s[88:91], s80 offen offset:416\n"
"buffer_load_ushort v138, v147, s[88:91], s80 offen offset:672\n"
"buffer_load_ushort v139, v147, s[88:91], s80 offen offset:928\n"
"buffer_load_ushort v140, v147, s[88:91], s81 offen offset:160\n"
"buffer_load_ushort v141, v147, s[88:91], s81 offen offset:416\n"
"buffer_load_ushort v142, v147, s[88:91], s81 offen offset:672\n"
"buffer_load_ushort v143, v147, s[88:91], s81 offen offset:928\n"
"s_waitcnt vmcnt(0)\n"
"v_lshl_or_b32 v128, v129, 16, v128\n"
"v_lshl_or_b32 v129, v131, 16, v130\n"
"v_lshl_or_b32 v130, v133, 16, v132\n"
"v_lshl_or_b32 v131, v135, 16, v134\n"
"v_lshl_or_b32 v136, v137, 16, v136\n"
"v_lshl_or_b32 v137, v139, 16, v138\n"
"v_lshl_or_b32 v138, v141, 16, v140\n"
"v_lshl_or_b32 v139, v143, 16, v142\n"
"v_mfma_f32_16x16x32_bf16 v[24:27], v[96:99], v[128:131], v[24:27]\n"
"s_nop 0\n"
"v_mfma_f32_16x16x32_bf16 v[28:31], v[96:99], v[136:139], v[28:31]\n"
"v_mfma_f32_16x16x32_bf16 v[56:59], v[104:107], v[128:131], v[56:59]\n"
"v_mfma_f32_16x16x32_bf16 v[60:63], v[104:107], v[136:139], v[60:63]\n"
"v_mfma_f32_16x16x32_bf16 v[88:91], v[112:115], v[128:131], v[88:91]\n"
"v_mfma_f32_16x16x32_bf16 v[92:95], v[112:115], v[136:139], v[92:95]\n"
"s_cmp_lg_u32 s83, s43\n"
"s_cbranch_scc1 LSK3B3_%=\n"
"v_mfma_f32_16x16x32_bf16 a[232:235], v[120:123], v[128:131], a[232:235]\n"
"v_mfma_f32_16x16x32_bf16 a[236:239], v[120:123], v[136:139], a[236:239]\n"
"LSK3B3_%=:\n"
"s_nop 3\n"
"s_nop 9\n"
"s_add_u32 s80, s80, 8192\n"
"s_add_u32 s81, s81, 8192\n"
"s_add_u32 s82, s82, 1\n"
"s_cmp_lt_u32 s82, 32\n"
"s_cbranch_scc1 LSLOOP_%=\n"
"v_mov_b32 v154, v155\n"
"s_nop 1\n"
"v_permlane32_swap_b32 v155, v154\n"
"v_add_f32 v155, v155, v154\n"
"v_mov_b32 v154, v155\n"
"s_nop 1\n"
"v_permlane16_swap_b32 v155, v154\n"
"v_add_f32 v155, v155, v154\n"
"v_mov_b32 v196, v155\n"
"v_mov_b32 v158, v159\n"
"s_nop 1\n"
"v_permlane32_swap_b32 v159, v158\n"
"v_add_f32 v159, v159, v158\n"
"v_mov_b32 v158, v159\n"
"s_nop 1\n"
"v_permlane16_swap_b32 v159, v158\n"
"v_add_f32 v159, v159, v158\n"
"v_mov_b32 v198, v159\n"
"v_mov_b32 v162, v163\n"
"s_nop 1\n"
"v_permlane32_swap_b32 v163, v162\n"
"v_add_f32 v163, v163, v162\n"
"v_mov_b32 v162, v163\n"
"s_nop 1\n"
"v_permlane16_swap_b32 v163, v162\n"
"v_add_f32 v163, v163, v162\n"
"v_mov_b32 v200, v163\n"
"v_mov_b32 v166, v167\n"
"s_nop 1\n"
"v_permlane32_swap_b32 v167, v166\n"
"v_add_f32 v167, v167, v166\n"
"v_mov_b32 v166, v167\n"
"s_nop 1\n"
"v_permlane16_swap_b32 v167, v166\n"
"v_add_f32 v167, v167, v166\n"
"v_mov_b32 v202, v167\n"
"v_sub_f32 v192, 0, v164\n"
"v_mov_b32 v96, 0\n"
"v_mov_b32 v97, 0\n"
"v_mov_b32 v98, 0\n"
"v_mov_b32 v99, 0\n"
"v_mov_b32 v100, 0\n"
"v_mov_b32 v101, 0\n"
"v_mov_b32 v102, 0\n"
"v_mov_b32 v103, 0\n"
"v_mov_b32 v104, 0\n"
"v_mov_b32 v105, 0\n"
"v_mov_b32 v106, 0\n"
"v_mov_b32 v107, 0\n"
"v_mov_b32 v108, 0\n"
"v_mov_b32 v109, 0\n"
"v_mov_b32 v110, 0\n"
"v_mov_b32 v111, 0\n"
"v_mov_b32 v112, 0\n"
"v_mov_b32 v113, 0\n"
"v_mov_b32 v114, 0\n"
"v_mov_b32 v115, 0\n"
"v_mov_b32 v116, 0\n"
"v_mov_b32 v117, 0\n"
"v_mov_b32 v118, 0\n"
"v_mov_b32 v119, 0\n"
"v_mov_b32 v120, 0\n"
"v_mov_b32 v121, 0\n"
"v_mov_b32 v122, 0\n"
"v_mov_b32 v123, 0\n"
"v_mov_b32 v204, 0\n"
"v_mov_b32 v205, 0\n"
"s_nop 3\n"
"s_cmp_eq_u32 s47, 4\n"
"s_cbranch_scc1 LBACK0_%=\n"
"s_cmp_eq_u32 s47, 8\n"
"s_cbranch_scc1 LBACK1_%=\n"
"s_cmp_eq_u32 s47, 12\n"
"s_cbranch_scc1 LBACK2_%=\n"
"s_branch LBACK3_%=\n"
"LFIN_%=:\n"
    :: [kalo]"s"((unsigned)ka), [kahi]"s"((unsigned)(ka >> 32)), [wg]"s"(blockIdx.x), [wid]"s"(wid)
    : "v0","v1","v2","v3","v4","v5","v6","v7","v8","v9","v10","v11","v12","v13","v14","v15","v16","v17","v18","v19","v20","v21","v22","v23","v24","v25","v26","v27","v28","v29","v30","v31","v32","v33","v34","v35","v36","v37","v38","v39","v40","v41","v42","v43","v44","v45","v46","v47","v48","v49","v50","v51","v52","v53","v54","v55","v56","v57","v58","v59","v60","v61","v62","v63","v64","v65","v66","v67","v68","v69","v70","v71","v72","v73","v74","v75","v76","v77","v78","v79","v80","v81","v82","v83","v84","v85","v86","v87","v88","v89","v90","v91","v92","v93","v94","v95","v96","v97","v98","v99","v100","v101","v102","v103","v104","v105","v106","v107","v108","v109","v110","v111","v112","v113","v114","v115","v116","v117","v118","v119","v120","v121","v122","v123","v124","v125","v126","v127","v128","v129","v130","v131","v132","v133","v134","v135","v136","v137","v138","v139","v140","v141","v142","v143","v144","v145","v146","v147","v148","v149","v150","v151","v152","v153","v154","v155","v156","v157","v158","v159","v160","v161","v162","v163","v164","v165","v166","v167","v168","v169","v170","v171","v172","v173","v174","v175","v176","v177","v178","v179","v180","v181","v182","v183","v184","v185","v186","v187","v188","v189","v190","v191","v192","v193","v194","v195","v196","v197","v198","v199","v200","v201","v202","v203","v204","v205","v206","v207","v208","v209","v210","v211","v212","v213","v214","v215","v216","v217","v218","v219","v220","v221","v222","v223","v224","v225","v226","v227","v228","v229","v230","v231","v232","v233","v234","v235","v236","v237","v238","v239","v240","v241","v242","v243","v244","v245","v246","v247","v248","v249","v250","v251","v252","v253","v254","v255","a0","a1","a2","a3","a4","a5","a6","a7","a8","a9","a10","a11","a12","a13","a14","a15","a16","a17","a18","a19","a20","a21","a22","a23","a24","a25","a26","a27","a28","a29","a30","a31","a32","a33","a34","a35","a36","a37","a38","a39","a40","a41","a42","a43","a44","a45","a46","a47","a48","a49","a50","a51","a52","a53","a54","a55","a56","a57","a58","a59","a60","a61","a62","a63","a64","a65","a66","a67","a68","a69","a70","a71","a72","a73","a74","a75","a76","a77","a78","a79","a80","a81","a82","a83","a84","a85","a86","a87","a88","a89","a90","a91","a92","a93","a94","a95","a96","a97","a98","a99","a100","a101","a102","a103","a104","a105","a106","a107","a108","a109","a110","a111","a112","a113","a114","a115","a116","a117","a118","a119","a120","a121","a122","a123","a124","a125","a126","a127","a128","a129","a130","a131","a132","a133","a134","a135","a136","a137","a138","a139","a140","a141","a142","a143","a144","a145","a146","a147","a148","a149","a150","a151","a152","a153","a154","a155","a156","a157","a158","a159","a160","a161","a162","a163","a164","a165","a166","a167","a168","a169","a170","a171","a172","a173","a174","a175","a176","a177","a178","a179","a180","a181","a182","a183","a184","a185","a186","a187","a188","a189","a190","a191","a192","a193","a194","a195","a196","a197","a198","a199","a200","a201","a202","a203","a204","a205","a206","a207","a208","a209","a210","a211","a212","a213","a214","a215","a216","a217","a218","a219","a220","a221","a222","a223","a224","a225","a226","a227","a228","a229","a230","a231","a232","a233","a234","a235","a236","a237","a238","a239","a240","a241","a242","a243","a244","a245","a246","a247","a248","a249","a250","a251","a252","a253","a254","a255","s6","s7","s8","s9","s10","s11","s12","s13","s14","s15","s16","s17","s18","s19","s20","s21","s22","s23","s24","s25","s26","s27","s28","s29","s30","s31","s32","s33","s34","s35","s36","s37","s38","s39","s40","s41","s42","s43","s44","s45","s46","s47","s48","s49","s50","s51","s52","s53","s54","s55","s56","s57","s58","s59","s60","s61","s62","s63","s64","s65","s66","s67","s68","s69","s70","s71","s72","s73","s74","s75","s76","s77","s78","s79","s80","s81","s82","s83","s84","s85","s86","s87","s88","s89","s90","s91","s92","s93","s94","s95","s96","s97","s98","s99","s100","vcc","scc","m0","memory");
}
extern "C" void kernel_launch(void* const* d_in, const int* in_sizes, int n_in, void* d_out, int out_size, void* d_ws, size_t ws_size, hipStream_t stream) {
  char* xch = (char*)d_ws + ((ws_size - (size_t)75497472) & ~(size_t)0xfffff);
  (void)hipFuncSetAttribute((const void*)fused_attn, hipFuncAttributeMaxDynamicSharedMemorySize, 163840);
  hipLaunchKernelGGL(fused_attn, dim3(256), dim3(256), 163840, stream,
                     (const char*)d_in[0], (const char*)d_in[1], (const char*)d_in[2], (const char*)d_in[3], (const char*)d_in[4],
                     (const char*)d_in[5], (const char*)d_in[6], (const char*)d_in[7], (const char*)d_in[8], (const char*)d_in[9], xch, (char*)d_out, (char*)d_ws);
}
```

```cpp
#include <hip/hip_runtime.h>
#include <hip/hip_bf16.h>
#include <stdint.h>

__global__ __launch_bounds__(256, 1)
void fused_attn(const char* xA, const char* kA, const char* vA, const char* wA, const char* uA,
                const char* xB, const char* kB, const char* vB, const char* wB, const char* uB, char* xch, char* out, char* flags) {
  extern __shared__ __attribute__((aligned(16))) char lds[];
  const unsigned wid = __builtin_amdgcn_readfirstlane(threadIdx.x >> 6);
  const unsigned long long ka = (unsigned long long)__builtin_amdgcn_kernarg_segment_ptr();
  asm volatile(
"s_memtime s[64:65]\n"
"s_memrealtime s[66:67]\n"
"s_bfe_u32 s10, %[wg], 0x10002\n"
"s_mul_i32 s40, s10, 40\n"
"s_add_u32 s40, s40, %[kalo]\n"
"s_addc_u32 s41, %[kahi], 0\n"
"s_mov_b32 s78, %[kalo]\n"
"s_mov_b32 s79, %[kahi]\n"
"s_load_dwordx2 s[24:25], s[40:41], 0\n"
"s_load_dwordx2 s[28:29], s[40:41], 24\n"
"s_load_dwordx2 s[6:7], s[78:79], 0x60\n"
"s_load_dwordx2 s[16:17], s[40:41], 8\n"
"s_load_dwordx2 s[20:21], s[40:41], 16\n"
"s_load_dwordx2 s[8:9], s[40:41], 32\n"
"s_load_dwordx4 s[12:15], s[78:79], 0x50\n"
"s_mov_b32 s42, %[wid]\n"
"s_and_b32 s11, %[wg], 1\n"
"s_lshl_b32 s11, s11, 3\n"
"s_lshr_b32 s60, %[wg], 5\n"
"s_add_u32 s11, s11, s60\n"
"s_bfe_u32 s61, %[wg], 0x10001\n"
"s_bfe_u32 s60, %[wg], 0x20003\n"
"s_lshl2_add_u32 s61, s61, s60\n"
"s_getpc_b64 s[80:81]\n"
"LCP_%=:\n"
"s_mov_b32 s82, LEND_%=-LCP_%=\n"
"s_mov_b32 s83, 0x20000\n"
"v_mbcnt_lo_u32_b32 v2, -1, 0\n"
"v_lshlrev_b32 v2, 7, v2\n"
"s_lshr_b32 s60, %[wg], 3\n"
"s_lshl2_add_u32 s60, s60, s42\n"
"s_mul_i32 s60, s60, 640\n"
"s_mov_b64 exec, 31\n"
"buffer_load_dword v3, v2, s[80:83], s60 offen\n"
"s_mov_b64 exec, -1\n"
"s_mov_b32 s19, 0x20000\n"
"s_mov_b32 s23, 0x20000\n"
"s_mov_b32 s27, 0x20000\n"
"s_mov_b32 s31, 0x20000\n"
"s_mov_b32 s35, 0x20000\n"
"s_mov_b32 s39, 0x20000\n"
"s_mov_b32 s26, 286720\n"
"s_lshl_b32 s44, s42, 12\n"
"s_mov_b32 s50, s44\n"
"s_mul_i32 s96, s42, 7168\n"
"s_add_u32 s51, s96, 0x10000\n"
"s_mov_b32 s74, 0x8000\n"
"s_mov_b32 s58, 524288\n"
"s_mov_b32 s47, 0\n"
"s_add_u32 s60, s11, s61\n"
"s_and_b32 s60, s60, 0\n"
"s_mul_i32 s60, s60, 3\n"
"s_lshl_b32 s49, s60, 7\n"
"s_lshl_b32 s48, s60, 19\n"
"v_mbcnt_lo_u32_b32 v4, -1, 0\n"
"v_mbcnt_hi_u32_b32 v4, -1, v4\n"
"v_and_b32 v5, 15, v4\n"
"v_lshrrev_b32 v6, 4, v4\n"
"v_lshrrev_b32 v7, 3, v4\n"
"v_and_b32 v8, 7, v4\n"
"v_lshlrev_b32 v9, 1, v6\n"
"v_xor_b32 v9, v9, v8\n"
"v_lshlrev_b32 v9, 4, v9\n"
"s_mul_i32 s60, s42, 71680\n"
"v_mul_u32_u24 v10, 1280, v7\n"
"v_add3_u32 v248, v10, v9, s60\n"
"v_add_u32 v249, 9216, v248\n"
"v_add_u32 v254, 18432, v248\n"
"v_add_u32 v255, 27648, v248\n"
"v_lshl_add_u32 v240, v7, 8, v9\n"
"v_add_u32 v240, s44, v240\n"
"v_and_b32 v10, 1, v6\n"
"v_xor_b32 v10, v10, v8\n"
"v_lshlrev_b32 v10, 4, v10\n"
"v_lshl_add_u32 v10, v7, 13, v10\n"
"s_lshl_b32 s60, s42, 17\n"
"v_add_u32 v10, s60, v10\n"
"v_add_u32 v224, 1024, v10\n"
"v_add_u32 v225, 128, v10\n"
"v_xor_b32 v10, 64, v10\n"
"v_add_u32 v226, 64512, v10\n"
"v_add_u32 v227, 63616, v10\n"
"s_mul_i32 s40, s11, 286720\n"
"s_lshl_b32 s41, s61, 10\n"
"s_sub_u32 s30, 5243904, s41\n"
"s_sub_u32 s41, s41, 1024\n"
"s_ashr_i32 s60, s41, 31\n"
"s_lshl4_add_u32 s92, s10, s11\n"
"s_lshl_b32 s93, s92, 8\n"
"v_mov_b32 v0, 0\n"
"s_waitcnt lgkmcnt(0)\n"
"s_add_u32 s24, s24, s40\n"
"s_addc_u32 s25, s25, 0\n"
"s_and_b32 s25, s25, 0xffff\n"
"s_add_u32 s28, s28, s41\n"
"s_addc_u32 s29, s29, s60\n"
"s_and_b32 s29, s29, 0xffff\n"
"s_add_u32 s6, s6, s93\n"
"s_addc_u32 s7, s7, 0\n"
"buffer_load_dword v12, v248, s[24:27], s49 offen\n"
"buffer_load_dword v13, v224, s[28:31], s48 offen\n"
"global_load_dword v1, v0, s[6:7] offset:128 sc1\n"
"s_mov_b32 m0, s51\n"
"s_nop 0\n"
"buffer_load_dwordx4 v248, s[24:27], s49 offen lds\n"
"buffer_load_dwordx4 v249, s[24:27], s49 offen offset:1024 lds\n"
"buffer_load_dwordx4 v254, s[24:27], s49 offen offset:2048 lds\n"
"buffer_load_dwordx4 v255, s[24:27], s49 offen offset:3072 lds\n"
"s_add_u32 m0, s51, 4096\n"
"s_add_u32 s40, s49, 40960\n"
"buffer_load_dwordx4 v248, s[24:27], s40 offen lds\n"
"buffer_load_dwordx4 v249, s[24:27], s40 offen offset:1024 lds\n"
"buffer_load_dwordx4 v254, s[24:27], s40 offen offset:2048 lds\n"
"s_add_u32 s49, s49, 128\n"
"s_sub_u32 s41, s49, 1280\n"
"s_min_u32 s49, s49, s41\n"
"s_xor_b32 s51, s51, s74\n"
"s_mov_b32 m0, s50\n"
"s_nop 0\n"
"buffer_load_dwordx4 v224, s[28:31], s48 offen lds\n"
"buffer_load_dwordx4 v225, s[28:31], s48 offen offset:1024 lds\n"
"buffer_load_dwordx4 v226, s[28:31], s48 offen offset:2048 lds\n"
"buffer_load_dwordx4 v227, s[28:31], s48 offen offset:3072 lds\n"
"s_xor_b32 s50, s50, s74\n"
"s_lshl2_add_u32 s63, s11, s42\n"
"s_mul_i32 s63, s63, 10240\n"
"s_and_b32 s78, s42, 1\n"
"s_lshl_b32 s43, s78, 6\n"
"s_mov_b32 s77, 0x80000000\n"
"s_mov_b32 s75, 0x10000\n"
"s_mov_b32 s73, 0xffff0000\n"
"s_mov_b32 s46, 0x3e0293ee\n"
"s_mov_b32 s45, 0x63800000\n"
"s_mov_b32 s62, 9\n"
"v_lshl_add_u32 v242, v4, 4, s44\n"
"s_lshr_b32 s79, s42, 1\n"
"s_lshl4_add_u32 s76, s79, s78\n"
"s_lshl_b32 s76, s76, 10\n"
"s_add_u32 s76, s76, 0x20000\n"
"s_sub_u32 s76, s76, s44\n"
"v_lshlrev_b32 v9, 6, v6\n"
"v_xor_b32 v9, v9, v242\n"
"v_add_u32 v243, s76, v9\n"
"v_and_b32 v9, 6, v5\n"
"s_xor_b32 m0, s50, 0xc000\n"
"s_add_u32 s40, s48, 256\n"
"buffer_load_dwordx4 v224, s[28:31], s40 offen lds\n"
"v_xor_b32 v9, v9, v6\n"
"v_lshlrev_b32 v9, 4, v9\n"
"v_lshl_add_u32 v10, v5, 7, v9\n"
"s_mul_i32 s79, s42, 6144\n"
"s_add_u32 s79, s79, 0x10000\n"
"v_add_u32 v250, s79, v10\n"
"v_xor_b32 v251, 64, v250\n"
"s_lshr_b32 s79, s42, 1\n"
"s_lshl_b32 s79, s79, 11\n"
"s_add_u32 s79, s79, 90112\n"
"v_add_u32 v252, s79, v10\n"
"v_xor_b32 v253, 64, v252\n"
"s_mul_i32 s79, s78, 8256\n"
"v_lshrrev_b32 v9, 5, v4\n"
"buffer_load_dwordx4 v225, s[28:31], s40 offen offset:1024 lds\n"
"v_lshlrev_b32 v9, 11, v9\n"
"v_bfe_u32 v10, v4, 2, 3\n"
"v_lshl_add_u32 v9, v10, 7, v9\n"
"v_and_b32 v10, 3, v4\n"
"v_lshl_add_u32 v9, v10, 3, v9\n"
"v_bfe_u32 v10, v4, 3, 2\n"
"v_lshl_add_u32 v9, v10, 5, v9\n"
"s_add_u32 s79, s79, 0x10000\n"
"v_xor_b32 v232, s79, v9\n"
"v_xor_b32 v233, 32, v232\n"
"v_xor_b32 v234, 64, v232\n"
"v_xor_b32 v235, 96, v232\n"
"v_xor_b32 v236, 0x2000, v232\n"
"v_xor_b32 v237, 0x2000, v233\n"
"buffer_load_dwordx4 v226, s[28:31], s40 offen offset:2048 lds\n"
"v_xor_b32 v238, 0x2000, v234\n"
"v_xor_b32 v239, 0x2000, v235\n"
"v_lshlrev_b32 v9, 11, v6\n"
"v_bfe_u32 v10, v4, 2, 2\n"
"v_lshl_add_u32 v9, v10, 7, v9\n"
"v_and_b32 v10, 1, v4\n"
"v_lshl_add_u32 v9, v10, 3, v9\n"
"v_bfe_u32 v10, v4, 1, 1\n"
"v_lshl_add_u32 v9, v10, 5, v9\n"
"v_bfe_u32 v10, v4, 3, 1\n"
"v_lshl_add_u32 v9, v10, 4, v9\n"
"v_bfe_u32 v10, v4, 4, 1\n"
"v_lshl_add_u32 v9, v10, 6, v9\n"
"v_xor_b32 v244, s43, v9\n"
"buffer_load_dwordx4 v227, s[28:31], s40 offen offset:3072 lds\n"
"s_cmp_eq_u32 s47, 8\ns_cselect_b32 s41, 524800, s58\n"
"s_add_u32 s48, s48, s41\n"
"s_sub_u32 s41, s48, 5242880\n"
"s_min_u32 s48, s48, s41\n"
"v_xor_b32 v245, 16, v244\n"
"v_xor_b32 v246, 64, v244\n"
"v_xor_b32 v247, 0x50, v244\n"
"s_lshl3_add_u32 s79, s11, s61\n"
"s_lshl_b32 s79, s79, 20\n"
"s_sub_u32 s79, s79, 896\n"
"s_ashr_i32 s60, s79, 31\n"
"s_add_u32 s16, s16, s79\n"
"s_addc_u32 s17, s17, s60\n"
"s_and_b32 s17, s17, 0xffff\n"
"s_mov_b32 s18, 1049472\n"
"s_mov_b64 s[84:85], s[16:17]\n"
"s_mov_b64 s[86:87], s[18:19]\n"
"s_add_u32 s20, s20, s79\n"
"s_addc_u32 s21, s21, s60\n"
"s_mov_b32 m0, s50\n"
"s_nop 0\n"
"buffer_load_dwordx4 v224, s[28:31], s48 offen lds\n"
"s_and_b32 s21, s21, 0xffff\n"
"s_mov_b32 s22, 1049472\n"
"s_mov_b64 s[88:89], s[20:21]\n"
"s_mov_b64 s[90:91], s[22:23]\n"
"s_lshr_b32 s57, %[wg], 3\n"
"s_mul_i32 s57, s57, 5\n"
"s_and_b32 s57, s57, 15\n"
"s_lshl_b32 s57, s57, 14\n"
"s_mov_b32 s55, 896\n"
"s_add_u32 s56, s57, 896\n"
"s_mov_b32 s59, 16384\n"
"s_mov_b32 s52, -896\n"
"s_mov_b32 s53, 0\n"
"buffer_load_dwordx4 v225, s[28:31], s48 offen offset:1024 lds\n"
"s_mov_b32 s54, -896\n"
"s_mul_i32 s79, s61, 655360\n"
"s_add_u32 s8, s8, s79\n"
"s_addc_u32 s9, s9, 0\n"
"s_and_b32 s9, s9, 0xffff\n"
"s_mul_i32 s79, s92, 2293760\n"
"s_add_u32 s32, s12, s79\n"
"s_addc_u32 s33, s13, 0\n"
"s_and_b32 s33, s33, 0xffff\n"
"s_mov_b32 s34, 2293760\n"
"s_mul_i32 s79, s11, 573440\n"
"s_mul_i32 s60, s10, 1280\n"
"s_sub_u32 s38, 573440, s60\n"
"s_add_u32 s79, s79, s60\n"
"buffer_load_dwordx4 v226, s[28:31], s48 offen offset:2048 lds\n"
"s_add_u32 s36, s14, s79\n"
"s_addc_u32 s37, s15, 0\n"
"s_and_b32 s37, s37, 0xffff\n"
"s_lshl2_add_u32 s93, s61, s42\n"
"s_lshl_b32 s94, s93, 10\n"
"s_add_u32 s94, s94, 229376\n"
"s_add_u32 s95, s61, 1\n"
"s_and_b32 s95, s95, 7\n"
"s_lshl_b32 s95, s95, 7\n"
"s_add_u32 s100, s61, 2\n"
"s_and_b32 s100, s100, 7\n"
"s_lshl_b32 s100, s100, 7\n"
"s_lshl_b32 s60, s61, 7\n"
"s_add_u32 s60, s60, s43\n"
"buffer_load_dwordx4 v227, s[28:31], s48 offen offset:3072 lds\n"
"s_xor_b32 s50, s50, s74\n"
"s_mul_i32 s97, s42, 122880\n"
"s_add_u32 s97, s97, s60\n"
"s_lshr_b32 s98, s42, 1\n"
"s_add_u32 s98, s98, 12\n"
"s_mul_i32 s98, s98, 40960\n"
"s_add_u32 s98, s98, s60\n"
"s_mul_i32 s60, s93, 37\n"
"s_lshr_b32 s60, s60, 8\n"
"s_mul_i32 s79, s60, 7\n"
"s_sub_u32 s79, s93, s79\n"
"s_mul_i32 s99, s60, 3\n"
"s_lshr_b32 s78, s79, 1\n"
"s_add_u32 s99, s99, s78\n"
"s_lshr_b32 s78, s60, 1\n"
"s_mov_b32 m0, s51\n"
"s_nop 0\n"
"buffer_load_dwordx4 v248, s[24:27], s49 offen lds\n"
"s_add_u32 s78, s78, 12\n"
"s_cmp_eq_u32 s79, 6\n"
"s_cselect_b32 s99, s78, s99\n"
"s_mul_i32 s99, s99, 40960\n"
"s_xor_b32 s79, s79, s60\n"
"s_and_b32 s79, s79, 1\n"
"s_lshl_b32 s79, s79, 6\n"
"s_add_u32 s99, s99, s79\n"
"s_add_u32 s99, s99, 1024\n"
"s_cmp_eq_u32 s61, 7\n"
"s_cselect_b32 s99, 0x40000000, s99\n"
"s_cselect_b32 s94, 0x40000000, s94\n"
"s_waitcnt vmcnt(21)\n"
"v_and_b32 v12, 0x7fff0000, v12\n"
"buffer_load_dwordx4 v249, s[24:27], s49 offen offset:1024 lds\n"
"v_and_b32 v13, 0x7fff0000, v13\n"
"s_nop 0\n"
"v_max_u32_dpp v12, v12, v12 quad_perm:[1,0,3,2] row_mask:0xf bank_mask:0xf\n"
"v_max_u32_dpp v13, v13, v13 quad_perm:[1,0,3,2] row_mask:0xf bank_mask:0xf\n"
"s_nop 0\n"
"v_max_u32_dpp v12, v12, v12 quad_perm:[2,3,0,1] row_mask:0xf bank_mask:0xf\n"
"v_max_u32_dpp v13, v13, v13 quad_perm:[2,3,0,1] row_mask:0xf bank_mask:0xf\n"
"s_nop 0\n"
"v_max_u32_dpp v12, v12, v12 row_half_mirror row_mask:0xf bank_mask:0xf\n"
"v_max_u32_dpp v13, v13, v13 row_half_mirror row_mask:0xf bank_mask:0xf\n"
"s_nop 0\n"
"v_min_u32_dpp v12, v12, v12 row_mirror row_mask:0xf bank_mask:0xf\n"
"v_min_u32_dpp v13, v13, v13 row_mirror row_mask:0xf bank_mask:0xf\n"
"s_nop 0\n"
"buffer_load_dwordx4 v254, s[24:27], s49 offen offset:2048 lds\n"
"v_min_u32_dpp v12, v12, v12 row_bcast:15 row_mask:0xa bank_mask:0xf\n"
"v_min_u32_dpp v13, v13, v13 row_bcast:15 row_mask:0xa bank_mask:0xf\n"
"s_nop 0\n"
"v_min_u32_dpp v12, v12, v12 row_bcast:31 row_mask:0xc bank_mask:0xf\n"
"v_min_u32_dpp v13, v13, v13 row_bcast:31 row_mask:0xc bank_mask:0xf\n"
"s_nop 0\n"
"v_mul_f32 v12, v12, v13\n"
"v_mul_f32 v12, 0x43c80000, v12\n"
"v_min_f32 v12, 0x71800000, v12\n"
"v_mul_f32 v13, s46, v12\n"
"s_nop 0\n"
"v_readlane_b32 s70, v12, 63\n"
"v_readlane_b32 s71, v12, 63\n"
"v_readlane_b32 s69, v13, 63\n"
"buffer_load_dwordx4 v255, s[24:27], s49 offen offset:3072 lds\n"
"s_nop 1\n"
"v_mov_b64 v[228:229], s[70:71]\n"
"v_mov_b64 v[230:231], s[70:71]\n"
"v_mov_b64 v[4:5], s[70:71]\n"
"v_mov_b64 v[6:7], s[70:71]\n"
"v_mov_b64 v[8:9], s[70:71]\n"
"v_mov_b64 v[10:11], s[70:71]\n"
"v_mov_b64 v[12:13], s[70:71]\n"
"v_mov_b64 v[14:15], s[70:71]\n"
"v_mov_b64 v[16:17], s[70:71]\n"
"v_mov_b64 v[18:19], s[70:71]\n"
"v_mov_b64 v[20:21], s[70:71]\n"
"v_mov_b64 v[22:23], s[70:71]\n"
"v_mov_b64 v[24:25], s[70:71]\n"
"s_add_u32 m0, s51, 4096\n"
"s_add_u32 s40, s49, 40960\n"
"buffer_load_dwordx4 v248, s[24:27], s40 offen lds\n"
"v_mov_b64 v[26:27], s[70:71]\n"
"v_mov_b64 v[28:29], s[70:71]\n"
"v_mov_b64 v[30:31], s[70:71]\n"
"v_mov_b64 v[32:33], s[70:71]\n"
"v_mov_b64 v[34:35], s[70:71]\n"
"v_mov_b64 v[36:37], s[70:71]\n"
"v_mov_b64 v[38:39], s[70:71]\n"
"v_mov_b64 v[40:41], s[70:71]\n"
"v_mov_b64 v[42:43], s[70:71]\n"
"v_mov_b64 v[44:45], s[70:71]\n"
"v_mov_b64 v[46:47], s[70:71]\n"
"v_mov_b64 v[48:49], s[70:71]\n"
"v_mov_b64 v[50:51], s[70:71]\n"
"v_mov_b64 v[52:53], s[70:71]\n"
"buffer_load_dwordx4 v249, s[24:27], s40 offen offset:1024 lds\n"
"v_mov_b64 v[54:55], s[70:71]\n"
"v_mov_b64 v[56:57], s[70:71]\n"
"v_mov_b64 v[58:59], s[70:71]\n"
"v_mov_b64 v[60:61], s[70:71]\n"
"v_mov_b64 v[62:63], s[70:71]\n"
"v_mov_b64 v[64:65], s[70:71]\n"
"v_mov_b64 v[66:67], s[70:71]\n"
"v_mov_b64 v[68:69], s[70:71]\n"
"v_mov_b64 v[70:71], s[70:71]\n"
"v_mov_b64 v[72:73], s[70:71]\n"
"v_mov_b64 v[74:75], s[70:71]\n"
"v_mov_b64 v[76:77], s[70:71]\n"
"v_mov_b64 v[78:79], s[70:71]\n"
"v_mov_b64 v[80:81], s[70:71]\n"
"buffer_load_dwordx4 v254, s[24:27], s40 offen offset:2048 lds\n"
"s_add_u32 s49, s49, 128\n"
"s_sub_u32 s41, s49, 1280\n"
"s_min_u32 s49, s49, s41\n"
"s_xor_b32 s51, s51, s74\n"
"v_mov_b64 v[82:83], s[70:71]\n"
"v_mov_b64 v[84:85], s[70:71]\n"
"v_mov_b64 v[86:87], s[70:71]\n"
"v_mov_b64 v[88:89], s[70:71]\n"
"v_mov_b64 v[90:91], s[70:71]\n"
"v_mov_b64 v[92:93], s[70:71]\n"
"v_mov_b64 v[94:95], s[70:71]\n"
"v_mov_b64 v[96:97], s[70:71]\n"
"v_mov_b64 v[98:99], s[70:71]\n"
"v_mov_b64 v[100:101], s[70:71]\n"
"v_mov_b64 v[102:103], s[70:71]\n"
"v_mov_b64 v[104:105], s[70:71]\n"
"v_mov_b64 v[106:107], s[70:71]\n"
"v_mov_b64 v[108:109], s[70:71]\n"
"v_mov_b64 v[110:111], s[70:71]\n"
"v_mov_b64 v[112:113], s[70:71]\n"
"v_mov_b64 v[114:115], s[70:71]\n"
"v_mov_b64 v[116:117], s[70:71]\n"
"v_mov_b64 v[118:119], s[70:71]\n"
"v_mov_b64 v[120:121], s[70:71]\n"
"v_mov_b64 v[122:123], s[70:71]\n"
"v_mov_b64 v[124:125], s[70:71]\n"
"v_mov_b64 v[126:127], s[70:71]\n"
"v_mov_b64 v[128:129], s[70:71]\n"
"v_mov_b64 v[130:131], s[70:71]\n"
"v_mov_b64 v[132:133], s[70:71]\n"
"v_mov_b64 v[134:135], s[70:71]\n"
"v_mov_b64 v[136:137], s[70:71]\n"
"v_mov_b64 v[138:139], s[70:71]\n"
"v_mov_b64 v[140:141], s[70:71]\n"
"v_mov_b64 v[142:143], s[70:71]\n"
"v_mov_b64 v[144:145], s[70:71]\n"
"v_mov_b64 v[146:147], s[70:71]\n"
"v_mov_b64 v[148:149], s[70:71]\n"
"v_mov_b64 v[150:151], s[70:71]\n"
"v_mov_b64 v[152:153], s[70:71]\n"
"v_mov_b64 v[154:155], s[70:71]\n"
"v_mov_b64 v[156:157], s[70:71]\n"
"v_mov_b64 v[158:159], s[70:71]\n"
"v_mov_b64 v[160:161], s[70:71]\n"
"v_mov_b64 v[162:163], s[70:71]\n"
"v_mov_b64 v[164:165], s[70:71]\n"
"v_mov_b64 v[166:167], s[70:71]\n"
"v_mov_b64 v[168:169], s[70:71]\n"
"v_mov_b64 v[170:171], s[70:71]\n"
"v_mov_b64 v[172:173], s[70:71]\n"
"v_mov_b64 v[174:175], s[70:71]\n"
"s_memtime s[80:81]\n"
"s_memrealtime s[82:83]\n"
"v_mov_b64 v[176:177], s[70:71]\n"
"v_mov_b64 v[178:179], s[70:71]\n"
"v_mov_b64 v[180:181], s[70:71]\n"
"v_mov_b64 v[182:183], s[70:71]\n"
"v_mov_b64 v[184:185], s[70:71]\n"
"v_mov_b64 v[186:187], s[70:71]\n"
"v_mov_b64 v[188:189], s[70:71]\n"
"v_mov_b64 v[190:191], s[70:71]\n"
"v_mov_b64 v[192:193], s[70:71]\n"
"v_mov_b64 v[194:195], s[70:71]\n"
"v_mov_b64 v[196:197], s[70:71]\n"
"v_mov_b64 v[198:199], s[70:71]\n"
"v_mov_b64 v[200:201], s[70:71]\n"
"v_mov_b64 v[202:203], s[70:71]\n"
"v_mov_b64 v[204:205], s[70:71]\n"
"v_mov_b64 v[206:207], s[70:71]\n"
"v_mov_b64 v[208:209], s[70:71]\n"
"v_mov_b64 v[210:211], s[70:71]\n"
"v_mov_b64 v[212:213], s[70:71]\n"
"v_mov_b64 v[214:215], s[70:71]\n"
"v_mov_b64 v[216:217], s[70:71]\n"
"v_mov_b64 v[218:219], s[70:71]\n"
"v_mov_b64 v[220:221], s[70:71]\n"
"v_mov_b64 v[222:223], s[70:71]\n"
"s_waitcnt vmcnt(26)\n"
"v_readfirstlane_b32 s92, v1\n"
"v_mov_b64 v[0:1], s[70:71]\n"
"v_mov_b64 v[2:3], s[70:71]\n"
"s_mov_b32 s70, s46\n"
"s_xor_b32 s71, s69, s77\n"
"s_waitcnt lgkmcnt(0)\n"
"s_sub_u32 s80, s80, s64\n"
"s_sub_u32 s82, s82, s66\n"
"s_mul_i32 s80, s80, 10\n"
"s_mul_i32 s82, s82, 211\n"
"s_cmp_gt_u32 s80, s82\n"
"s_cselect_b32 s64, 1, 0\n"
"s_cselect_b32 s68, 655360, 0\n"
"s_cmp_eq_u32 s42, 0\n"
"s_cselect_b32 s64, s64, 0\n"
"s_mov_b32 s65, 0\n"
"s_add_u32 s66, s66, 2800\n"
"s_mov_b32 s67, s66\n"
"s_mov_b32 s72, 122\n"
"s_mov_b64 vcc, s[64:65]\n"
"s_nop 2\n"
"s_mul_i32 s60, s92, 0x9e3779b1\n"
"s_add_u32 s60, s60, 0x7f4a7c15\n"
"s_waitcnt vmcnt(15)\n"
"s_barrier\n"
"ds_read_b128 a[160:163], v250 offset:0\n"
"ds_read_b128 a[164:167], v251 offset:0\n"
"ds_read_b64_tr_b16 a[96:97], v244 offset:0\n"
"ds_read_b64_tr_b16 a[98:99], v244 offset:512\n"
"ds_read_b64_tr_b16 a[100:101], v244 offset:8192\n"
"ds_read_b64_tr_b16 a[102:103], v244 offset:8704\n"
"ds_read_b64_tr_b16 a[104:105], v245 offset:0\n"
"ds_read_b64_tr_b16 a[106:107], v245 offset:512\n"
"ds_read_b64_tr_b16 a[108:109], v245 offset:8192\n"
"ds_read_b64_tr_b16 a[110:111], v245 offset:8704\n"
"ds_read_b64_tr_b16 a[112:113], v246 offset:0\n"
"ds_read_b64_tr_b16 a[114:115], v246 offset:512\n"
"ds_read_b64_tr_b16 a[116:117], v246 offset:8192\n"
"ds_read_b64_tr_b16 a[118:119], v246 offset:8704\n"
"ds_read_b64_tr_b16 a[120:121], v247 offset:0\n"
"ds_read_b64_tr_b16 a[122:123], v247 offset:512\n"
"ds_read_b64_tr_b16 a[124:125], v247 offset:8192\n"
"ds_read_b64_tr_b16 a[126:127], v247 offset:8704\n"
"ds_read_b64_tr_b16 a[128:129], v244 offset:1024\n"
"ds_read_b64_tr_b16 a[130:131], v244 offset:1536\n"
"ds_read_b64_tr_b16 a[132:133], v244 offset:9216\n"
"ds_read_b64_tr_b16 a[134:135], v244 offset:9728\n"
"ds_read_b64_tr_b16 a[136:137], v245 offset:1024\n"
"ds_read_b64_tr_b16 a[138:139], v245 offset:1536\n"
"ds_read_b64_tr_b16 a[140:141], v245 offset:9216\n"
"ds_read_b64_tr_b16 a[142:143], v245 offset:9728\n"
"ds_read_b64_tr_b16 a[144:145], v246 offset:1024\n"
"ds_read_b64_tr_b16 a[146:147], v246 offset:1536\n"
"ds_read_b64_tr_b16 a[148:149], v246 offset:9216\n"
"ds_read_b64_tr_b16 a[150:151], v246 offset:9728\n"
"ds_read_b64_tr_b16 a[152:153], v247 offset:1024\n"
"ds_read_b64_tr_b16 a[154:155], v247 offset:1536\n"
"ds_read_b64_tr_b16 a[156:157], v247 offset:9216\n"
"ds_read_b64_tr_b16 a[158:159], v247 offset:9728\n"
"ds_read_b128 a[168:171], v250 offset:2048\n"
"ds_read_b128 a[172:175], v251 offset:2048\n"
"ds_read_b128 a[176:179], v250 offset:4096\n"
"ds_read_b128 a[180:183], v251 offset:4096\n"
"ds_read_b128 a[184:187], v252 offset:0\n"
"ds_read_b128 a[188:191], v253 offset:0\n"
"LPL_%=:\n"
"s_waitcnt lgkmcnt(15)\n"
"v_mfma_f32_16x16x32_bf16 v[112:115], a[96:99], a[160:163], v[112:115]\n"
"v_mfma_f32_16x16x32_bf16 v[112:115], a[100:103], a[164:167], v[112:115]\n"
"v_mfma_f32_16x16x32_bf16 v[116:119], a[104:107], a[160:163], v[116:119]\n"
"s_waitcnt vmcnt(11) lgkmcnt(5)\n"
"v_mfma_f32_16x16x32_bf16 v[116:119], a[108:111], a[164:167], v[116:119]\n"
"s_barrier\n"
"v_mfma_f32_16x16x32_bf16 v[120:123], a[112:115], a[160:163], v[120:123]\n"
"s_add_u32 s40, s48, 256\n"
"s_xor_b32 m0, s50, 0xc000\n"
"ds_read_b64_tr_b16 a[192:193], v244 offset:16384\n"
"v_mfma_f32_16x16x32_bf16 v[120:123], a[116:119], a[164:167], v[120:123]\n"
"buffer_load_dwordx4 v224, s[28:31], s40 offen lds\n"
"v_mfma_f32_16x16x32_bf16 v[124:127], a[120:123], a[160:163], v[124:127]\n"
"ds_read_b64_tr_b16 a[194:195], v244 offset:16896\n"
"v_mfma_f32_16x16x32_bf16 v[124:127], a[124:127], a[164:167], v[124:127]\n"
"ds_read_b64_tr_b16 a[196:197], v244 offset:24576\n"
"v_mfma_f32_16x16x32_bf16 v[128:131], a[128:131], a[160:163], v[128:131]\n"
"ds_read_b64_tr_b16 a[198:199], v244 offset:25088\n"
"v_mfma_f32_16x16x32_bf16 v[128:131], a[132:135], a[164:167], v[128:131]\n"
"ds_read_b64_tr_b16 a[200:201], v245 offset:16384\n"
"v_mfma_f32_16x16x32_bf16 v[132:135], a[136:139], a[160:163], v[132:135]\n"
"ds_read_b64_tr_b16 a[202:203], v245 offset:16896\n"
"v_mfma_f32_16x16x32_bf16 v[132:135], a[140:143], a[164:167], v[132:135]\n"
"buffer_load_dwordx4 v225, s[28:31], s40 offen offset:1024 lds\n"
"v_mfma_f32_16x16x32_bf16 v[136:139], a[144:147], a[160:163], v[136:139]\n"
"ds_read_b64_tr_b16 a[204:205], v245 offset:24576\n"
"v_mfma_f32_16x16x32_bf16 v[136:139], a[148:151], a[164:167], v[136:139]\n"
"ds_read_b64_tr_b16 a[206:207], v245 offset:25088\n"
"v_mfma_f32_16x16x32_bf16 v[140:143], a[152:155], a[160:163], v[140:143]\n"
"ds_read_b64_tr_b16 a[208:209], v246 offset:16384\n"
"v_mfma_f32_16x16x32_bf16 v[140:143], a[156:159], a[164:167], v[140:143]\n"
"s_waitcnt lgkmcnt(6)\n"
"ds_read_b64_tr_b16 a[210:211], v246 offset:16896\n"
"v_mfma_f32_16x16x32_bf16 v[172:175], a[152:155], a[168:171], v[172:175]\n"
"s_waitcnt lgkmcnt(6)\n"
"ds_read_b64_tr_b16 a[212:213], v246 offset:24576\n"
"v_mfma_f32_16x16x32_bf16 v[172:175], a[156:159], a[172:175], v[172:175]\n"
"buffer_load_dwordx4 v226, s[28:31], s40 offen offset:2048 lds\n"
"v_mfma_f32_16x16x32_bf16 v[168:171], a[144:147], a[168:171], v[168:171]\n"
"ds_read_b64_tr_b16 a[214:215], v246 offset:25088\n"
"v_mfma_f32_16x16x32_bf16 v[168:171], a[148:151], a[172:175], v[168:171]\n"
"ds_read_b64_tr_b16 a[216:217], v247 offset:16384\n"
"v_mfma_f32_16x16x32_bf16 v[164:167], a[136:139], a[168:171], v[164:167]\n"
"ds_read_b64_tr_b16 a[218:219], v247 offset:16896\n"
"v_mfma_f32_16x16x32_bf16 v[164:167], a[140:143], a[172:175], v[164:167]\n"
"ds_read_b64_tr_b16 a[220:221], v247 offset:24576\n"
"v_mfma_f32_16x16x32_bf16 v[160:163], a[128:131], a[168:171], v[160:163]\n"
"ds_read_b64_tr_b16 a[222:223], v247 offset:25088\n"
"v_mfma_f32_16x16x32_bf16 v[160:163], a[132:135], a[172:175], v[160:163]\n"
"buffer_load_dwordx4 v227, s[28:31], s40 offen offset:3072 lds\n"
"v_mfma_f32_16x16x32_bf16 v[156:159], a[120:123], a[168:171], v[156:159]\n"
"s_cmp_eq_u32 s47, 8\ns_cselect_b32 s41, 524800, s58\n"
"ds_read_b64_tr_b16 a[224:225], v244 offset:17408\n"
"v_mfma_f32_16x16x32_bf16 v[156:159], a[124:127], a[172:175], v[156:159]\n"
"s_add_u32 s48, s48, s41\n"
"ds_read_b64_tr_b16 a[226:227], v244 offset:17920\n"
"v_mfma_f32_16x16x32_bf16 v[152:155], a[112:115], a[168:171], v[152:155]\n"
"s_sub_u32 s41, s48, 5242880\n"
"ds_read_b64_tr_b16 a[228:229], v244 offset:25600\n"
"v_mfma_f32_16x16x32_bf16 v[152:155], a[116:119], a[172:175], v[152:155]\n"
"s_min_u32 s48, s48, s41\n"
"ds_read_b64_tr_b16 a[230:231], v244 offset:26112\n"
"v_mfma_f32_16x16x32_bf16 v[148:151], a[104:107], a[168:171], v[148:151]\n"
"s_mov_b32 m0, s50\n"
"ds_read_b64_tr_b16 a[232:233], v245 offset:17408\n"
"v_mfma_f32_16x16x32_bf16 v[148:151], a[108:111], a[172:175], v[148:151]\n"
"buffer_load_dwordx4 v224, s[28:31], s48 offen lds\n"
"v_mfma_f32_16x16x32_bf16 v[144:147], a[96:99], a[168:171], v[144:147]\n"
"ds_read_b64_tr_b16 a[234:235], v245 offset:17920\n"
"v_xor_b32 v244, s74, v244\n"
"v_mfma_f32_16x16x32_bf16 v[144:147], a[100:103], a[172:175], v[144:147]\n"
"ds_read_b64_tr_b16 a[236:237], v245 offset:25600\n"
"v_xor_b32 v250, s74, v250\n"
"v_mfma_f32_16x16x32_bf16 v[176:179], a[96:99], a[176:179], v[176:179]\n"
"s_waitcnt lgkmcnt(6)\n"
"ds_read_b64_tr_b16 a[238:239], v245 offset:26112\n"
"v_xor_b32 v251, s74, v251\n"
"v_mfma_f32_16x16x32_bf16 v[176:179], a[100:103], a[180:183], v[176:179]\n"
"ds_read_b64_tr_b16 a[240:241], v246 offset:17408\n"
"v_xor_b32 v245, s74, v245\n"
"v_mfma_f32_16x16x32_bf16 v[180:183], a[104:107], a[176:179], v[180:183]\n"
"ds_read_b64_tr_b16 a[242:243], v246 offset:17920\n"
"v_xor_b32 v252, s74, v252\n"
"v_mfma_f32_16x16x32_bf16 v[180:183], a[108:111], a[180:183], v[180:183]\n"
"buffer_load_dwordx4 v225, s[28:31], s48 offen offset:1024 lds\n"
"v_mfma_f32_16x16x32_bf16 v[184:187], a[112:115], a[176:179], v[184:187]\n"
"ds_read_b64_tr_b16 a[244:245], v246 offset:25600\n"
"v_xor_b32 v253, s74, v253\n"
"v_mfma_f32_16x16x32_bf16 v[184:187], a[116:119], a[180:183], v[184:187]\n"
"ds_read_b64_tr_b16 a[246:247], v246 offset:26112\n"
"v_xor_b32 v246, s74, v246\n"
"v_mfma_f32_16x16x32_bf16 v[188:191], a[120:123], a[176:179], v[188:191]\n"
"ds_read_b64_tr_b16 a[248:249], v247 offset:17408\n"
"v_mfma_f32_16x16x32_bf16 v[188:191], a[124:127], a[180:183], v[188:191]\n"
"ds_read_b64_tr_b16 a[250:251], v247 offset:17920\n"
"v_mfma_f32_16x16x32_bf16 v[200:203], a[144:147], a[176:179], v[200:203]\n"
"ds_read_b64_tr_b16 a[252:253], v247 offset:25600\n"
"v_mfma_f32_16x16x32_bf16 v[200:203], a[148:151], a[180:183], v[200:203]\n"
"buffer_load_dwordx4 v226, s[28:31], s48 offen offset:2048 lds\n"
"v_mfma_f32_16x16x32_bf16 v[204:207], a[152:155], a[176:179], v[204:207]\n"
"ds_read_b64_tr_b16 a[254:255], v247 offset:26112\n"
"v_xor_b32 v247, s74, v247\n"
"v_mfma_f32_16x16x32_bf16 v[204:207], a[156:159], a[180:183], v[204:207]\n"
"v_mfma_f32_16x16x32_bf16 v[192:195], a[128:131], a[176:179], v[192:195]\n"
"v_mfma_f32_16x16x32_bf16 v[192:195], a[132:135], a[180:183], v[192:195]\n"
"v_mfma_f32_16x16x32_bf16 v[196:199], a[136:139], a[176:179], v[196:199]\n"
"v_mfma_f32_16x16x32_bf16 v[196:199], a[140:143], a[180:183], v[196:199]\n"
"buffer_load_dwordx4 v227, s[28:31], s48 offen offset:3072 lds\n"
"v_mfma_f32_16x16x32_bf16 v[220:223], a[136:139], a[184:187], v[220:223]\n"
"s_xor_b32 s50, s50, s74\n"
"v_mfma_f32_16x16x32_bf16 v[220:223], a[140:143], a[188:191], v[220:223]\n"
"v_mfma_f32_16x16x32_bf16 v[216:219], a[128:131], a[184:187], v[216:219]\n"
"v_mfma_f32_16x16x32_bf16 v[216:219], a[132:135], a[188:191], v[216:219]\n"
"v_mfma_f32_16x16x32_bf16 v[212:215], a[104:107], a[184:187], v[212:215]\n"
"v_mfma_f32_16x16x32_bf16 v[212:215], a[108:111], a[188:191], v[212:215]\n"
"v_mfma_f32_16x16x32_bf16 v[208:211], a[96:99], a[184:187], v[208:211]\n"
"v_mfma_f32_16x16x32_bf16 v[208:211], a[100:103], a[188:191], v[208:211]\n"
"v_mfma_f32_16x16x32_bf16 v[0:3], a[192:195], a[160:163], v[0:3]\n"
"s_waitcnt lgkmcnt(0)\n"
"v_mfma_f32_16x16x32_bf16 v[0:3], a[196:199], a[164:167], v[0:3]\n"
"s_waitcnt lgkmcnt(0)\n"
"v_mfma_f32_16x16x32_bf16 v[4:7], a[200:203], a[160:163], v[4:7]\n"
"s_waitcnt vmcnt(8) lgkmcnt(0)\n"
"v_mfma_f32_16x16x32_bf16 v[4:7], a[204:207], a[164:167], v[4:7]\n"
"s_barrier\n"
"v_mfma_f32_16x16x32_bf16 v[8:11], a[208:211], a[160:163], v[8:11]\n"
"s_mov_b32 m0, s51\n"
"ds_read_b64_tr_b16 a[96:97], v244 offset:0\n"
"v_mfma_f32_16x16x32_bf16 v[8:11], a[212:215], a[164:167], v[8:11]\n"
"buffer_load_dwordx4 v248, s[24:27], s49 offen lds\n"
"v_mfma_f32_16x16x32_bf16 v[12:15], a[216:219], a[160:163], v[12:15]\n"
"ds_read_b64_tr_b16 a[98:99], v244 offset:512\n"
"v_mfma_f32_16x16x32_bf16 v[12:15], a[220:223], a[164:167], v[12:15]\n"
"ds_read_b64_tr_b16 a[100:101], v244 offset:8192\n"
"v_mfma_f32_16x16x32_bf16 v[16:19], a[224:227], a[160:163], v[16:19]\n"
"ds_read_b64_tr_b16 a[102:103], v244 offset:8704\n"
"v_mfma_f32_16x16x32_bf16 v[16:19], a[228:231], a[164:167], v[16:19]\n"
"ds_read_b64_tr_b16 a[104:105], v245 offset:0\n"
"v_mfma_f32_16x16x32_bf16 v[20:23], a[232:235], a[160:163], v[20:23]\n"
"ds_read_b64_tr_b16 a[106:107], v245 offset:512\n"
"v_mfma_f32_16x16x32_bf16 v[20:23], a[236:239], a[164:167], v[20:23]\n"
"buffer_load_dwordx4 v249, s[24:27], s49 offen offset:1024 lds\n"
"v_mfma_f32_16x16x32_bf16 v[24:27], a[240:243], a[160:163], v[24:27]\n"
"ds_read_b64_tr_b16 a[108:109], v245 offset:8192\n"
"v_mfma_f32_16x16x32_bf16 v[24:27], a[244:247], a[164:167], v[24:27]\n"
"ds_read_b64_tr_b16 a[110:111], v245 offset:8704\n"
"v_mfma_f32_16x16x32_bf16 v[28:31], a[248:251], a[160:163], v[28:31]\n"
"ds_read_b128 a[160:163], v250 offset:0\n"
"v_mfma_f32_16x16x32_bf16 v[28:31], a[252:255], a[164:167], v[28:31]\n"
"ds_read_b128 a[164:167], v251 offset:0\n"
"v_mfma_f32_16x16x32_bf16 v[60:63], a[248:251], a[168:171], v[60:63]\n"
"ds_read_b64_tr_b16 a[112:113], v246 offset:0\n"
"v_mfma_f32_16x16x32_bf16 v[60:63], a[252:255], a[172:175], v[60:63]\n"
"buffer_load_dwordx4 v254, s[24:27], s49 offen offset:2048 lds\n"
"v_mfma_f32_16x16x32_bf16 v[56:59], a[240:243], a[168:171], v[56:59]\n"
"ds_read_b64_tr_b16 a[114:115], v246 offset:512\n"
"v_mfma_f32_16x16x32_bf16 v[56:59], a[244:247], a[172:175], v[56:59]\n"
"ds_read_b64_tr_b16 a[116:117], v246 offset:8192\n"
"v_mfma_f32_16x16x32_bf16 v[52:55], a[232:235], a[168:171], v[52:55]\n"
"ds_read_b64_tr_b16 a[118:119], v246 offset:8704\n"
"v_mfma_f32_16x16x32_bf16 v[52:55], a[236:239], a[172:175], v[52:55]\n"
"ds_read_b64_tr_b16 a[120:121], v247 offset:0\n"
"v_mfma_f32_16x16x32_bf16 v[48:51], a[224:227], a[168:171], v[48:51]\n"
"ds_read_b64_tr_b16 a[122:123], v247 offset:512\n"
"v_mfma_f32_16x16x32_bf16 v[48:51], a[228:231], a[172:175], v[48:51]\n"
"buffer_load_dwordx4 v255, s[24:27], s49 offen offset:3072 lds\n"
"v_mfma_f32_16x16x32_bf16 v[44:47], a[216:219], a[168:171], v[44:47]\n"
"ds_read_b64_tr_b16 a[124:125], v247 offset:8192\n"
"v_mfma_f32_16x16x32_bf16 v[44:47], a[220:223], a[172:175], v[44:47]\n"
"ds_read_b64_tr_b16 a[126:127], v247 offset:8704\n"
"v_mfma_f32_16x16x32_bf16 v[40:43], a[208:211], a[168:171], v[40:43]\n"
"ds_read_b64_tr_b16 a[128:129], v244 offset:1024\n"
"v_mfma_f32_16x16x32_bf16 v[40:43], a[212:215], a[172:175], v[40:43]\n"
"s_add_u32 s40, s49, 40960\n"
"ds_read_b64_tr_b16 a[130:131], v244 offset:1536\n"
"v_mfma_f32_16x16x32_bf16 v[36:39], a[200:203], a[168:171], v[36:39]\n"
"s_add_u32 m0, s51, 4096\n"
"ds_read_b64_tr_b16 a[132:133], v244 offset:9216\n"
"v_mfma_f32_16x16x32_bf16 v[36:39], a[204:207], a[172:175], v[36:39]\n"
"buffer_load_dwordx4 v248, s[24:27], s40 offen lds\n"
"v_mfma_f32_16x16x32_bf16 v[32:35], a[192:195], a[168:171], v[32:35]\n"
"ds_read_b64_tr_b16 a[134:135], v244 offset:9728\n"
"v_mfma_f32_16x16x32_bf16 v[32:35], a[196:199], a[172:175], v[32:35]\n"
"ds_read_b64_tr_b16 a[136:137], v245 offset:1024\n"
"v_mfma_f32_16x16x32_bf16 v[64:67], a[192:195], a[176:179], v[64:67]\n"
"ds_read_b64_tr_b16 a[138:139], v245 offset:1536\n"
"v_mfma_f32_16x16x32_bf16 v[64:67], a[196:199], a[180:183], v[64:67]\n"
"ds_read_b64_tr_b16 a[140:141], v245 offset:9216\n"
"v_mfma_f32_16x16x32_bf16 v[68:71], a[200:203], a[176:179], v[68:71]\n"
"ds_read_b64_tr_b16 a[142:143], v245 offset:9728\n"
"v_mfma_f32_16x16x32_bf16 v[68:71], a[204:207], a[180:183], v[68:71]\n"
"buffer_load_dwordx4 v249, s[24:27], s40 offen offset:1024 lds\n"
"v_mfma_f32_16x16x32_bf16 v[72:75], a[208:211], a[176:179], v[72:75]\n"
"ds_read_b64_tr_b16 a[144:145], v246 offset:1024\n"
"v_mfma_f32_16x16x32_bf16 v[72:75], a[212:215], a[180:183], v[72:75]\n"
"ds_read_b64_tr_b16 a[146:147], v246 offset:1536\n"
"v_mfma_f32_16x16x32_bf16 v[76:79], a[216:219], a[176:179], v[76:79]\n"
"ds_read_b64_tr_b16 a[148:149], v246 offset:9216\n"
"v_mfma_f32_16x16x32_bf16 v[76:79], a[220:223], a[180:183], v[76:79]\n"
"ds_read_b64_tr_b16 a[150:151], v246 offset:9728\n"
"v_mfma_f32_16x16x32_bf16 v[88:91], a[240:243], a[176:179], v[88:91]\n"
"ds_read_b64_tr_b16 a[152:153], v247 offset:1024\n"
"v_mfma_f32_16x16x32_bf16 v[88:91], a[244:247], a[180:183], v[88:91]\n"
"buffer_load_dwordx4 v254, s[24:27], s40 offen offset:2048 lds\n"
"v_mfma_f32_16x16x32_bf16 v[92:95], a[248:251], a[176:179], v[92:95]\n"
"s_add_u32 s49, s49, 128\n"
"ds_read_b64_tr_b16 a[154:155], v247 offset:1536\n"
"v_mfma_f32_16x16x32_bf16 v[92:95], a[252:255], a[180:183], v[92:95]\n"
"s_sub_u32 s41, s49, 1280\n"
"ds_read_b64_tr_b16 a[156:157], v247 offset:9216\n"
"v_mfma_f32_16x16x32_bf16 v[80:83], a[224:227], a[176:179], v[80:83]\n"
"s_min_u32 s49, s49, s41\n"
"ds_read_b64_tr_b16 a[158:159], v247 offset:9728\n"
"v_mfma_f32_16x16x32_bf16 v[80:83], a[228:231], a[180:183], v[80:83]\n"
"s_xor_b32 s51, s51, s74\n"
"ds_read_b128 a[168:171], v250 offset:2048\n"
"v_mfma_f32_16x16x32_bf16 v[84:87], a[232:235], a[176:179], v[84:87]\n"
"ds_read_b128 a[172:175], v251 offset:2048\n"
"v_mfma_f32_16x16x32_bf16 v[84:87], a[236:239], a[180:183], v[84:87]\n"
"ds_read_b128 a[176:179], v250 offset:4096\n"
"v_mfma_f32_16x16x32_bf16 v[108:111], a[232:235], a[184:187], v[108:111]\n"
"ds_read_b128 a[180:183], v251 offset:4096\n"
"v_mfma_f32_16x16x32_bf16 v[108:111], a[236:239], a[188:191], v[108:111]\n"
"v_mfma_f32_16x16x32_bf16 v[104:107], a[224:227], a[184:187], v[104:107]\n"
"v_mfma_f32_16x16x32_bf16 v[104:107], a[228:231], a[188:191], v[104:107]\n"
"v_mfma_f32_16x16x32_bf16 v[100:103], a[200:203], a[184:187], v[100:103]\n"
"v_mfma_f32_16x16x32_bf16 v[100:103], a[204:207], a[188:191], v[100:103]\n"
"v_mfma_f32_16x16x32_bf16 v[96:99], a[192:195], a[184:187], v[96:99]\n"
"s_add_u32 s47, s47, 1\n"
"ds_read_b128 a[184:187], v252 offset:0\n"
"v_mfma_f32_16x16x32_bf16 v[96:99], a[196:199], a[188:191], v[96:99]\n"
"s_waitcnt lgkmcnt(2)\n"
"s_cmp_lg_u32 s47, s62\n"
"ds_read_b128 a[188:191], v253 offset:0\n"
"s_cbranch_scc1 LPL_%=\n"
"s_cmp_eq_u32 s47, 18\n"
"s_cbranch_scc1 LPT2_%=\n"
"v_mfma_f32_16x16x32_bf16 v[112:115], a[96:99], a[160:163], v[112:115]\n"
"v_mfma_f32_16x16x32_bf16 v[112:115], a[100:103], a[164:167], v[112:115]\n"
"v_mfma_f32_16x16x32_bf16 v[116:119], a[104:107], a[160:163], v[116:119]\n"
"s_waitcnt vmcnt(11)\n"
"v_mfma_f32_16x16x32_bf16 v[116:119], a[108:111], a[164:167], v[116:119]\n"
"s_barrier\n"
"v_mfma_f32_16x16x32_bf16 v[120:123], a[112:115], a[160:163], v[120:123]\n"
"s_add_u32 s40, s48, 256\n"
"s_xor_b32 m0, s50, 0xc000\n"
"ds_read_b64_tr_b16 a[232:233], v245 offset:17408\n"
"v_mfma_f32_16x16x32_bf16 v[120:123], a[116:119], a[164:167], v[120:123]\n"
"buffer_load_dwordx4 v224, s[28:31], s40 offen lds\n"
"v_mfma_f32_16x16x32_bf16 v[124:127], a[120:123], a[160:163], v[124:127]\n"
"ds_read_b64_tr_b16 a[234:235], v245 offset:17920\n"
"v_mfma_f32_16x16x32_bf16 v[124:127], a[124:127], a[164:167], v[124:127]\n"
"ds_read_b64_tr_b16 a[236:237], v245 offset:25600\n"
"v_mfma_f32_16x16x32_bf16 v[128:131], a[128:131], a[160:163], v[128:131]\n"
"ds_read_b64_tr_b16 a[238:239], v245 offset:26112\n"
"v_mfma_f32_16x16x32_bf16 v[128:131], a[132:135], a[164:167], v[128:131]\n"
"ds_read_b64_tr_b16 a[224:225], v244 offset:17408\n"
"v_mfma_f32_16x16x32_bf16 v[132:135], a[136:139], a[160:163], v[132:135]\n"
"ds_read_b64_tr_b16 a[226:227], v244 offset:17920\n"
"v_mfma_f32_16x16x32_bf16 v[132:135], a[140:143], a[164:167], v[132:135]\n"
"buffer_load_dwordx4 v225, s[28:31], s40 offen offset:1024 lds\n"
"v_mfma_f32_16x16x32_bf16 v[136:139], a[144:147], a[160:163], v[136:139]\n"
"ds_read_b64_tr_b16 a[192:193], v244 offset:16384\n"
"v_mfma_f32_16x16x32_bf16 v[136:139], a[148:151], a[164:167], v[136:139]\n"
"ds_read_b64_tr_b16 a[194:195], v244 offset:16896\n"
"v_mfma_f32_16x16x32_bf16 v[140:143], a[152:155], a[160:163], v[140:143]\n"
"ds_read_b64_tr_b16 a[196:197], v244 offset:24576\n"
"v_mfma_f32_16x16x32_bf16 v[140:143], a[156:159], a[164:167], v[140:143]\n"
"s_waitcnt lgkmcnt(6)\n"
"ds_read_b64_tr_b16 a[198:199], v244 offset:25088\n"
"v_mfma_f32_16x16x32_bf16 v[220:223], a[136:139], a[184:187], v[220:223]\n"
"ds_read_b64_tr_b16 a[200:201], v245 offset:16384\n"
"v_mfma_f32_16x16x32_bf16 v[220:223], a[140:143], a[188:191], v[220:223]\n"
"buffer_load_dwordx4 v226, s[28:31], s40 offen offset:2048 lds\n"
"v_mfma_f32_16x16x32_bf16 v[216:219], a[128:131], a[184:187], v[216:219]\n"
"ds_read_b64_tr_b16 a[202:203], v245 offset:16896\n"
"v_mfma_f32_16x16x32_bf16 v[216:219], a[132:135], a[188:191], v[216:219]\n"
"ds_read_b64_tr_b16 a[204:205], v245 offset:24576\n"
"v_mfma_f32_16x16x32_bf16 v[212:215], a[104:107], a[184:187], v[212:215]\n"
"ds_read_b64_tr_b16 a[206:207], v245 offset:25088\n"
"v_mfma_f32_16x16x32_bf16 v[212:215], a[108:111], a[188:191], v[212:215]\n"
"v_pk_fma_f32 v[112:113], v[112:113], s[70:71], s[70:71] op_sel:[0,0,1] op_sel_hi:[1,0,1]\nv_pk_fma_f32 v[114:115], v[114:115], s[70:71], s[70:71] op_sel:[0,0,1] op_sel_hi:[1,0,1]\nv_pk_fma_f32 v[116:117], v[116:117], s[70:71], s[70:71] op_sel:[0,0,1] op_sel_hi:[1,0,1]\nv_pk_fma_f32 v[118:119], v[118:119], s[70:71], s[70:71] op_sel:[0,0,1] op_sel_hi:[1,0,1]\nv_pk_fma_f32 v[120:121], v[120:121], s[70:71], s[70:71] op_sel:[0,0,1] op_sel_hi:[1,0,1]\nv_pk_fma_f32 v[122:123], v[122:123], s[70:71], s[70:71] op_sel:[0,0,1] op_sel_hi:[1,0,1]\nv_pk_fma_f32 v[124:125], v[124:125], s[70:71], s[70:71] op_sel:[0,0,1] op_sel_hi:[1,0,1]\nv_pk_fma_f32 v[126:127], v[126:127], s[70:71], s[70:71] op_sel:[0,0,1] op_sel_hi:[1,0,1]\nv_pk_fma_f32 v[128:129], v[128:129], s[70:71], s[70:71] op_sel:[0,0,1] op_sel_hi:[1,0,1]\nv_pk_fma_f32 v[130:131], v[130:131], s[70:71], s[70:71] op_sel:[0,0,1] op_sel_hi:[1,0,1]\nv_pk_fma_f32 v[132:133], v[132:133], s[70:71], s[70:71] op_sel:[0,0,1] op_sel_hi:[1,0,1]\nv_pk_fma_f32 v[134:135], v[134:135], s[70:71], s[70:71] op_sel:[0,0,1] op_sel_hi:[1,0,1]\nv_pk_fma_f32 v[136:137], v[136:137], s[70:71], s[70:71] op_sel:[0,0,1] op_sel_hi:[1,0,1]\nv_pk_fma_f32 v[138:139], v[138:139], s[70:71], s[70:71] op_sel:[0,0,1] op_sel_hi:[1,0,1]\nv_pk_fma_f32 v[140:141], v[140:141], s[70:71], s[70:71] op_sel:[0,0,1] op_sel_hi:[1,0,1]\nv_pk_fma_f32 v[142:143], v[142:143], s[70:71], s[70:71] op_sel:[0,0,1] op_sel_hi:[1,0,1]\n"
"v_mfma_f32_16x16x32_bf16 v[208:211], a[96:99], a[184:187], v[208:211]\n"
"ds_read_b64_tr_b16 a[208:209], v246 offset:16384\n"
"v_cvt_pk_bf16_f32 v112, v112, v113\n"
"v_mfma_f32_16x16x32_bf16 v[208:211], a[100:103], a[188:191], v[208:211]\n"
"buffer_load_dwordx4 v227, s[28:31], s40 offen offset:3072 lds\n"
"v_mfma_f32_16x16x32_bf16 v[172:175], a[152:155], a[168:171], v[172:175]\n"
"s_cmp_eq_u32 s47, 8\ns_cselect_b32 s41, 524800, s58\n"
"ds_read_b64_tr_b16 a[210:211], v246 offset:16896\n"
"v_cvt_pk_bf16_f32 v113, v114, v115\n"
"v_mfma_f32_16x16x32_bf16 v[172:175], a[156:159], a[172:175], v[172:175]\n"
"s_add_u32 s48, s48, s41\n"
"ds_read_b64_tr_b16 a[212:213], v246 offset:24576\n"
"v_cvt_pk_bf16_f32 v114, v116, v117\n"
"v_mfma_f32_16x16x32_bf16 v[168:171], a[144:147], a[168:171], v[168:171]\n"
"s_sub_u32 s41, s48, 5242880\n"
"ds_read_b64_tr_b16 a[214:215], v246 offset:25088\n"
"v_cvt_pk_bf16_f32 v115, v118, v119\n"
"v_mfma_f32_16x16x32_bf16 v[168:171], a[148:151], a[172:175], v[168:171]\n"
"s_min_u32 s48, s48, s41\n"
"ds_read_b64_tr_b16 a[216:217], v247 offset:16384\n"
"v_cvt_pk_bf16_f32 v120, v120, v121\n"
"v_mfma_f32_16x16x32_bf16 v[164:167], a[136:139], a[168:171], v[164:167]\n"
"s_mov_b32 m0, s50\n"
"ds_read_b64_tr_b16 a[218:219], v247 offset:16896\n"
"v_cvt_pk_bf16_f32 v121, v122, v123\n"
"v_mfma_f32_16x16x32_bf16 v[164:167], a[140:143], a[172:175], v[164:167]\n"
"buffer_load_dwordx4 v224, s[28:31], s48 offen lds\n"
"v_mfma_f32_16x16x32_bf16 v[160:163], a[128:131], a[168:171], v[160:163]\n"
"ds_read_b64_tr_b16 a[220:221], v247 offset:24576\n"
"v_xor_b32 v250, s74, v250\n"
"v_mfma_f32_16x16x32_bf16 v[160:163], a[132:135], a[172:175], v[160:163]\n"
"ds_read_b64_tr_b16 a[222:223], v247 offset:25088\n"
"v_xor_b32 v251, s74, v251\n"
"v_mfma_f32_16x16x32_bf16 v[156:159], a[120:123], a[168:171], v[156:159]\n"
"ds_read_b64_tr_b16 a[228:229], v244 offset:25600\n"
"v_xor_b32 v245, s74, v245\n"
"v_mfma_f32_16x16x32_bf16 v[156:159], a[124:127], a[172:175], v[156:159]\n"
"s_mov_b64 s[80:81], s[8:9]\n"
"ds_read_b64_tr_b16 a[230:231], v244 offset:26112\n"
"v_xor_b32 v244, s74, v244\n"
"v_mfma_f32_16x16x32_bf16 v[152:155], a[112:115], a[168:171], v[152:155]\n"
"s_mov_b32 s82, s68\n"
"ds_read_b64_tr_b16 a[240:241], v246 offset:17408\n"
"v_cvt_pk_bf16_f32 v122, v124, v125\n"
"v_mfma_f32_16x16x32_bf16 v[152:155], a[116:119], a[172:175], v[152:155]\n"
"s_mov_b32 s83, 131072\n"
"buffer_load_dwordx4 v225, s[28:31], s48 offen offset:1024 lds\n"
"v_mfma_f32_16x16x32_bf16 v[148:151], a[104:107], a[168:171], v[148:151]\n"
"v_and_b32 v241, 0x3f0, v242\n"
"ds_read_b64_tr_b16 a[242:243], v246 offset:17920\n"
"v_cvt_pk_bf16_f32 v123, v126, v127\n"
"v_mfma_f32_16x16x32_bf16 v[148:151], a[108:111], a[172:175], v[148:151]\n"
"v_mul_u32_u24 v241, 10, v241\n"
"ds_read_b64_tr_b16 a[244:245], v246 offset:25600\n"
"v_cvt_pk_bf16_f32 v128, v128, v129\n"
"v_mfma_f32_16x16x32_bf16 v[144:147], a[96:99], a[168:171], v[144:147]\n"
"ds_read_b64_tr_b16 a[246:247], v246 offset:26112\n"
"v_xor_b32 v246, s74, v246\n"
"v_mfma_f32_16x16x32_bf16 v[144:147], a[100:103], a[172:175], v[144:147]\n"
"ds_read_b64_tr_b16 a[248:249], v247 offset:17408\n"
"v_cvt_pk_bf16_f32 v129, v130, v131\n"
"v_mfma_f32_16x16x32_bf16 v[176:179], a[96:99], a[176:179], v[176:179]\n"
"ds_read_b64_tr_b16 a[250:251], v247 offset:17920\n"
"v_cvt_pk_bf16_f32 v130, v132, v133\n"
"v_mfma_f32_16x16x32_bf16 v[176:179], a[100:103], a[180:183], v[176:179]\n"
"buffer_load_dwordx4 v226, s[28:31], s48 offen offset:2048 lds\n"
"v_mfma_f32_16x16x32_bf16 v[180:183], a[104:107], a[176:179], v[180:183]\n"
"ds_read_b64_tr_b16 a[252:253], v247 offset:25600\n"
"v_cvt_pk_bf16_f32 v131, v134, v135\n"
"v_mfma_f32_16x16x32_bf16 v[180:183], a[108:111], a[180:183], v[180:183]\n"
"ds_read_b64_tr_b16 a[254:255], v247 offset:26112\n"
"v_xor_b32 v247, s74, v247\n"
"v_mfma_f32_16x16x32_bf16 v[184:187], a[112:115], a[176:179], v[184:187]\n"
"v_permlane16_swap_b32 v112, v114\n"
"v_mfma_f32_16x16x32_bf16 v[184:187], a[116:119], a[180:183], v[184:187]\n"
"v_permlane16_swap_b32 v113, v115\n"
"v_mfma_f32_16x16x32_bf16 v[188:191], a[120:123], a[176:179], v[188:191]\n"
"v_permlane16_swap_b32 v120, v122\n"
"v_mfma_f32_16x16x32_bf16 v[188:191], a[124:127], a[180:183], v[188:191]\n"
"buffer_load_dwordx4 v227, s[28:31], s48 offen offset:3072 lds\n"
"v_mfma_f32_16x16x32_bf16 v[200:203], a[144:147], a[176:179], v[200:203]\n"
"s_xor_b32 s50, s50, s74\n"
"v_permlane16_swap_b32 v121, v123\n"
"v_mfma_f32_16x16x32_bf16 v[200:203], a[148:151], a[180:183], v[200:203]\n"
"v_permlane16_swap_b32 v128, v130\n"
"v_mfma_f32_16x16x32_bf16 v[204:207], a[152:155], a[176:179], v[204:207]\n"
"v_permlane16_swap_b32 v129, v131\n"
"v_mfma_f32_16x16x32_bf16 v[204:207], a[156:159], a[180:183], v[204:207]\n"
"v_pk_fma_f32 v[144:145], v[144:145], s[70:71], s[70:71] op_sel:[0,0,1] op_sel_hi:[1,0,1]\nv_pk_fma_f32 v[146:147], v[146:147], s[70:71], s[70:71] op_sel:[0,0,1] op_sel_hi:[1,0,1]\nv_pk_fma_f32 v[148:149], v[148:149], s[70:71], s[70:71] op_sel:[0,0,1] op_sel_hi:[1,0,1]\nv_pk_fma_f32 v[150:151], v[150:151], s[70:71], s[70:71] op_sel:[0,0,1] op_sel_hi:[1,0,1]\nv_pk_fma_f32 v[152:153], v[152:153], s[70:71], s[70:71] op_sel:[0,0,1] op_sel_hi:[1,0,1]\nv_pk_fma_f32 v[154:155], v[154:155], s[70:71], s[70:71] op_sel:[0,0,1] op_sel_hi:[1,0,1]\nv_pk_fma_f32 v[156:157], v[156:157], s[70:71], s[70:71] op_sel:[0,0,1] op_sel_hi:[1,0,1]\nv_pk_fma_f32 v[158:159], v[158:159], s[70:71], s[70:71] op_sel:[0,0,1] op_sel_hi:[1,0,1]\nv_pk_fma_f32 v[160:161], v[160:161], s[70:71], s[70:71] op_sel:[0,0,1] op_sel_hi:[1,0,1]\nv_pk_fma_f32 v[162:163], v[162:163], s[70:71], s[70:71] op_sel:[0,0,1] op_sel_hi:[1,0,1]\nv_pk_fma_f32 v[164:165], v[164:165], s[70:71], s[70:71] op_sel:[0,0,1] op_sel_hi:[1,0,1]\nv_pk_fma_f32 v[166:167], v[166:167], s[70:71], s[70:71] op_sel:[0,0,1] op_sel_hi:[1,0,1]\nv_pk_fma_f32 v[168:169], v[168:169], s[70:71], s[70:71] op_sel:[0,0,1] op_sel_hi:[1,0,1]\nv_pk_fma_f32 v[170:171], v[170:171], s[70:71], s[70:71] op_sel:[0,0,1] op_sel_hi:[1,0,1]\nv_pk_fma_f32 v[172:173], v[172:173], s[70:71], s[70:71] op_sel:[0,0,1] op_sel_hi:[1,0,1]\nv_pk_fma_f32 v[174:175], v[174:175], s[70:71], s[70:71] op_sel:[0,0,1] op_sel_hi:[1,0,1]\n"
"v_mfma_f32_16x16x32_bf16 v[192:195], a[128:131], a[176:179], v[192:195]\n"
"v_pk_fma_f32 v[208:209], v[208:209], s[70:71], s[70:71] op_sel:[0,0,1] op_sel_hi:[1,0,1]\nv_pk_fma_f32 v[210:211], v[210:211], s[70:71], s[70:71] op_sel:[0,0,1] op_sel_hi:[1,0,1]\nv_pk_fma_f32 v[212:213], v[212:213], s[70:71], s[70:71] op_sel:[0,0,1] op_sel_hi:[1,0,1]\nv_pk_fma_f32 v[214:215], v[214:215], s[70:71], s[70:71] op_sel:[0,0,1] op_sel_hi:[1,0,1]\nv_pk_fma_f32 v[216:217], v[216:217], s[70:71], s[70:71] op_sel:[0,0,1] op_sel_hi:[1,0,1]\nv_pk_fma_f32 v[218:219], v[218:219], s[70:71], s[70:71] op_sel:[0,0,1] op_sel_hi:[1,0,1]\nv_pk_fma_f32 v[220:221], v[220:221], s[70:71], s[70:71] op_sel:[0,0,1] op_sel_hi:[1,0,1]\nv_pk_fma_f32 v[222:223], v[222:223], s[70:71], s[70:71] op_sel:[0,0,1] op_sel_hi:[1,0,1]\n"
"v_mfma_f32_16x16x32_bf16 v[192:195], a[132:135], a[180:183], v[192:195]\n"
"v_accvgpr_write_b32 a0, v112\n"
"v_accvgpr_write_b32 a1, v113\n"
"v_mfma_f32_16x16x32_bf16 v[196:199], a[136:139], a[176:179], v[196:199]\n"
"v_accvgpr_write_b32 a2, v114\n"
"v_accvgpr_write_b32 a3, v115\n"
"v_mfma_f32_16x16x32_bf16 v[196:199], a[140:143], a[180:183], v[196:199]\n"
"v_accvgpr_write_b32 a4, v120\n"
"v_accvgpr_write_b32 a5, v121\n"
"v_mfma_f32_16x16x32_bf16 v[108:111], a[232:235], a[184:187], v[108:111]\n"
"s_waitcnt lgkmcnt(0)\n"
"v_accvgpr_write_b32 a6, v122\n"
"v_accvgpr_write_b32 a7, v123\n"
"v_mfma_f32_16x16x32_bf16 v[108:111], a[236:239], a[188:191], v[108:111]\n"
"v_accvgpr_write_b32 a8, v128\n"
"v_accvgpr_write_b32 a9, v129\n"
"v_mfma_f32_16x16x32_bf16 v[104:107], a[224:227], a[184:187], v[104:107]\n"
"s_waitcnt vmcnt(8)\n"
"v_accvgpr_write_b32 a10, v130\n"
"v_accvgpr_write_b32 a11, v131\n"
"v_mfma_f32_16x16x32_bf16 v[104:107], a[228:231], a[188:191], v[104:107]\n"
"s_barrier\n"
"v_mfma_f32_16x16x32_bf16 v[100:103], a[200:203], a[184:187], v[100:103]\n"
"s_mov_b32 m0, s51\n"
"ds_read_b64_tr_b16 a[96:97], v244 offset:0\n"
"v_cvt_pk_bf16_f32 v136, v136, v137\n"
"v_mfma_f32_16x16x32_bf16 v[100:103], a[204:207], a[188:191], v[100:103]\n"
"buffer_load_dwordx4 v248, s[24:27], s49 offen lds\n"
"v_mfma_f32_16x16x32_bf16 v[96:99], a[192:195], a[184:187], v[96:99]\n"
"ds_read_b64_tr_b16 a[98:99], v244 offset:512\n"
"v_cvt_pk_bf16_f32 v137, v138, v139\n"
"v_mfma_f32_16x16x32_bf16 v[96:99], a[196:199], a[188:191], v[96:99]\n"
"ds_read_b64_tr_b16 a[100:101], v244 offset:8192\n"
"v_cvt_pk_bf16_f32 v138, v140, v141\n"
"v_mfma_f32_16x16x32_bf16 v[0:3], a[192:195], a[160:163], v[0:3]\n"
"ds_read_b64_tr_b16 a[102:103], v244 offset:8704\n"
"v_cvt_pk_bf16_f32 v139, v142, v143\n"
"v_mfma_f32_16x16x32_bf16 v[0:3], a[196:199], a[164:167], v[0:3]\n"
"ds_read_b64_tr_b16 a[104:105], v245 offset:0\n"
"v_cvt_pk_bf16_f32 v168, v168, v169\n"
"v_mfma_f32_16x16x32_bf16 v[4:7], a[200:203], a[160:163], v[4:7]\n"
"ds_read_b64_tr_b16 a[106:107], v245 offset:512\n"
"v_cvt_pk_bf16_f32 v169, v170, v171\n"
"v_mfma_f32_16x16x32_bf16 v[4:7], a[204:207], a[164:167], v[4:7]\n"
"buffer_load_dwordx4 v249, s[24:27], s49 offen offset:1024 lds\n"
"v_mfma_f32_16x16x32_bf16 v[8:11], a[208:211], a[160:163], v[8:11]\n"
"ds_read_b64_tr_b16 a[108:109], v245 offset:8192\n"
"v_cvt_pk_bf16_f32 v170, v172, v173\n"
"v_mfma_f32_16x16x32_bf16 v[8:11], a[212:215], a[164:167], v[8:11]\n"
"ds_read_b64_tr_b16 a[110:111], v245 offset:8704\n"
"v_cvt_pk_bf16_f32 v171, v174, v175\n"
"v_mfma_f32_16x16x32_bf16 v[12:15], a[216:219], a[160:163], v[12:15]\n"
"ds_read_b64_tr_b16 a[112:113], v246 offset:0\n"
"v_cvt_pk_bf16_f32 v160, v160, v161\n"
"v_mfma_f32_16x16x32_bf16 v[12:15], a[220:223], a[164:167], v[12:15]\n"
"buffer_load_dwordx4 v254, s[24:27], s49 offen offset:2048 lds\n"
"v_mfma_f32_16x16x32_bf16 v[16:19], a[224:227], a[160:163], v[16:19]\n"
"ds_read_b64_tr_b16 a[114:115], v246 offset:512\n"
"v_cvt_pk_bf16_f32 v161, v162, v163\n"
"v_mfma_f32_16x16x32_bf16 v[16:19], a[228:231], a[164:167], v[16:19]\n"
"ds_read_b64_tr_b16 a[116:117], v246 offset:8192\n"
"v_cvt_pk_bf16_f32 v162, v164, v165\n"
"v_mfma_f32_16x16x32_bf16 v[20:23], a[232:235], a[160:163], v[20:23]\n"
"ds_read_b64_tr_b16 a[118:119], v246 offset:8704\n"
"v_cvt_pk_bf16_f32 v163, v166, v167\n"
"v_mfma_f32_16x16x32_bf16 v[20:23], a[236:239], a[164:167], v[20:23]\n"
"ds_read_b64_tr_b16 a[120:121], v247 offset:0\n"
"v_cvt_pk_bf16_f32 v152, v152, v153\n"
"v_mfma_f32_16x16x32_bf16 v[24:27], a[240:243], a[160:163], v[24:27]\n"
"ds_read_b64_tr_b16 a[122:123], v247 offset:512\n"
"v_cvt_pk_bf16_f32 v153, v154, v155\n"
"v_mfma_f32_16x16x32_bf16 v[24:27], a[244:247], a[164:167], v[24:27]\n"
"buffer_load_dwordx4 v255, s[24:27], s49 offen offset:3072 lds\n"
"v_mfma_f32_16x16x32_bf16 v[28:31], a[248:251], a[160:163], v[28:31]\n"
"ds_read_b128 a[160:163], v250 offset:0\n"
"v_cvt_pk_bf16_f32 v154, v156, v157\n"
"v_mfma_f32_16x16x32_bf16 v[28:31], a[252:255], a[164:167], v[28:31]\n"
"ds_read_b128 a[164:167], v251 offset:0\n"
"v_cvt_pk_bf16_f32 v155, v158, v159\n"
"v_mfma_f32_16x16x32_bf16 v[60:63], a[248:251], a[168:171], v[60:63]\n"
"ds_read_b64_tr_b16 a[124:125], v247 offset:8192\n"
"v_cvt_pk_bf16_f32 v144, v144, v145\n"
"v_mfma_f32_16x16x32_bf16 v[60:63], a[252:255], a[172:175], v[60:63]\n"
"s_add_u32 s40, s49, 40960\n"
"ds_read_b64_tr_b16 a[126:127], v247 offset:8704\n"
"v_cvt_pk_bf16_f32 v145, v146, v147\n"
"v_mfma_f32_16x16x32_bf16 v[56:59], a[240:243], a[168:171], v[56:59]\n"
"s_add_u32 m0, s51, 4096\n"
"ds_read_b64_tr_b16 a[128:129], v244 offset:1024\n"
"v_cvt_pk_bf16_f32 v146, v148, v149\n"
"v_mfma_f32_16x16x32_bf16 v[56:59], a[244:247], a[172:175], v[56:59]\n"
"buffer_load_dwordx4 v248, s[24:27], s40 offen lds\n"
"v_mfma_f32_16x16x32_bf16 v[52:55], a[232:235], a[168:171], v[52:55]\n"
"ds_read_b64_tr_b16 a[130:131], v244 offset:1536\n"
"v_cvt_pk_bf16_f32 v147, v150, v151\n"
"v_mfma_f32_16x16x32_bf16 v[52:55], a[236:239], a[172:175], v[52:55]\n"
"ds_read_b64_tr_b16 a[132:133], v244 offset:9216\n"
"v_xor_b32 v252, s74, v252\n"
"v_mfma_f32_16x16x32_bf16 v[48:51], a[224:227], a[168:171], v[48:51]\n"
"ds_read_b64_tr_b16 a[134:135], v244 offset:9728\n"
"v_cvt_pk_bf16_f32 v216, v216, v217\n"
"v_mfma_f32_16x16x32_bf16 v[48:51], a[228:231], a[172:175], v[48:51]\n"
"ds_read_b64_tr_b16 a[136:137], v245 offset:1024\n"
"v_cvt_pk_bf16_f32 v217, v218, v219\n"
"v_mfma_f32_16x16x32_bf16 v[44:47], a[216:219], a[168:171], v[44:47]\n"
"ds_read_b64_tr_b16 a[138:139], v245 offset:1536\n"
"v_cvt_pk_bf16_f32 v218, v220, v221\n"
"v_mfma_f32_16x16x32_bf16 v[44:47], a[220:223], a[172:175], v[44:47]\n"
"buffer_load_dwordx4 v249, s[24:27], s40 offen offset:1024 lds\n"
"v_mfma_f32_16x16x32_bf16 v[40:43], a[208:211], a[168:171], v[40:43]\n"
"ds_read_b64_tr_b16 a[140:141], v245 offset:9216\n"
"v_cvt_pk_bf16_f32 v219, v222, v223\n"
"v_mfma_f32_16x16x32_bf16 v[40:43], a[212:215], a[172:175], v[40:43]\n"
"ds_read_b64_tr_b16 a[142:143], v245 offset:9728\n"
"v_xor_b32 v253, s74, v253\n"
"v_mfma_f32_16x16x32_bf16 v[36:39], a[200:203], a[168:171], v[36:39]\n"
"ds_read_b64_tr_b16 a[144:145], v246 offset:1024\n"
"v_cvt_pk_bf16_f32 v208, v208, v209\n"
"v_mfma_f32_16x16x32_bf16 v[36:39], a[204:207], a[172:175], v[36:39]\n"
"buffer_load_dwordx4 v254, s[24:27], s40 offen offset:2048 lds\n"
"v_mfma_f32_16x16x32_bf16 v[32:35], a[192:195], a[168:171], v[32:35]\n"
"s_add_u32 s49, s49, 128\n"
"ds_read_b64_tr_b16 a[146:147], v246 offset:1536\n"
"v_cvt_pk_bf16_f32 v209, v210, v211\n"
"v_mfma_f32_16x16x32_bf16 v[32:35], a[196:199], a[172:175], v[32:35]\n"
"s_sub_u32 s41, s49, 1280\n"
"ds_read_b64_tr_b16 a[148:149], v246 offset:9216\n"
"v_cvt_pk_bf16_f32 v210, v212, v213\n"
"v_mfma_f32_16x16x32_bf16 v[64:67], a[192:195], a[176:179], v[64:67]\n"
"s_min_u32 s49, s49, s41\n"
"ds_read_b64_tr_b16 a[150:151], v246 offset:9728\n"
"v_cvt_pk_bf16_f32 v211, v214, v215\n"
"v_mfma_f32_16x16x32_bf16 v[64:67], a[196:199], a[180:183], v[64:67]\n"
"s_xor_b32 s51, s51, s74\n"
"v_permlane16_swap_b32 v136, v138\n"
"v_mfma_f32_16x16x32_bf16 v[68:71], a[200:203], a[176:179], v[68:71]\n"
"s_mov_b32 m0, 139264\n"
"ds_read_b64_tr_b16 a[152:153], v247 offset:1024\n"
"v_accvgpr_write_b32 a12, v136\n"
"v_mfma_f32_16x16x32_bf16 v[68:71], a[204:207], a[180:183], v[68:71]\n"
"buffer_load_dword v241, s[80:83], s63 offen lds\n"
"v_mfma_f32_16x16x32_bf16 v[72:75], a[208:211], a[176:179], v[72:75]\n"
"ds_read_b64_tr_b16 a[154:155], v247 offset:1536\n"
"v_accvgpr_write_b32 a14, v138\n"
"v_mfma_f32_16x16x32_bf16 v[72:75], a[212:215], a[180:183], v[72:75]\n"
"v_permlane16_swap_b32 v137, v139\n"
"v_mfma_f32_16x16x32_bf16 v[76:79], a[216:219], a[176:179], v[76:79]\n"
"ds_read_b64_tr_b16 a[156:157], v247 offset:9216\n"
"v_accvgpr_write_b32 a13, v137\n"
"v_mfma_f32_16x16x32_bf16 v[76:79], a[220:223], a[180:183], v[76:79]\n"
"ds_read_b64_tr_b16 a[158:159], v247 offset:9728\n"
"v_accvgpr_write_b32 a15, v139\n"
"v_mfma_f32_16x16x32_bf16 v[88:91], a[240:243], a[176:179], v[88:91]\n"
"v_permlane16_swap_b32 v168, v170\n"
"v_mfma_f32_16x16x32_bf16 v[88:91], a[244:247], a[180:183], v[88:91]\n"
"buffer_load_dword v241, s[80:83], s63 offen offset:80 lds\n"
"v_mfma_f32_16x16x32_bf16 v[92:95], a[248:251], a[176:179], v[92:95]\n"
"ds_read_b128 a[168:171], v250 offset:2048\n"
"v_accvgpr_write_b32 a28, v168\n"
"v_mfma_f32_16x16x32_bf16 v[92:95], a[252:255], a[180:183], v[92:95]\n"
"ds_read_b128 a[172:175], v251 offset:2048\n"
"v_accvgpr_write_b32 a30, v170\n"
"v_mfma_f32_16x16x32_bf16 v[80:83], a[224:227], a[176:179], v[80:83]\n"
"v_permlane16_swap_b32 v169, v171\n"
"v_mfma_f32_16x16x32_bf16 v[80:83], a[228:231], a[180:183], v[80:83]\n"
"ds_read_b128 a[184:187], v252 offset:0\n"
"v_accvgpr_write_b32 a29, v169\n"
"v_mfma_f32_16x16x32_bf16 v[84:87], a[232:235], a[176:179], v[84:87]\n"
"s_add_u32 s47, s47, 1\n"
"ds_read_b128 a[176:179], v250 offset:4096\n"
"v_accvgpr_write_b32 a31, v171\n"
"v_mfma_f32_16x16x32_bf16 v[84:87], a[236:239], a[180:183], v[84:87]\n"
"s_waitcnt lgkmcnt(4)\n"
"v_permlane16_swap_b32 v160, v162\n"
"v_mfma_f32_16x16x32_bf16 v[112:115], a[96:99], a[160:163], v[228:231]\n"
"ds_read_b128 a[180:183], v251 offset:4096\n"
"v_accvgpr_write_b32 a24, v160\n"
"v_mfma_f32_16x16x32_bf16 v[112:115], a[100:103], a[164:167], v[112:115]\n"
"ds_read_b128 a[188:191], v253 offset:0\n"
"v_accvgpr_write_b32 a26, v162\n"
"v_mfma_f32_16x16x32_bf16 v[116:119], a[104:107], a[160:163], v[228:231]\n"
"s_waitcnt vmcnt(13)\n"
"v_permlane16_swap_b32 v161, v163\n"
"v_mfma_f32_16x16x32_bf16 v[116:119], a[108:111], a[164:167], v[116:119]\n"
"s_barrier\n"
"v_mfma_f32_16x16x32_bf16 v[120:123], a[112:115], a[160:163], v[228:231]\n"
"s_add_u32 s40, s48, 256\n"
"s_xor_b32 m0, s50, 0xc000\n"
"ds_read_b64_tr_b16 a[192:193], v244 offset:16384\n"
"v_mfma_f32_16x16x32_bf16 v[120:123], a[116:119], a[164:167], v[120:123]\n"
"buffer_load_dwordx4 v224, s[28:31], s40 offen lds\n"
"v_mfma_f32_16x16x32_bf16 v[124:127], a[120:123], a[160:163], v[228:231]\n"
"ds_read_b64_tr_b16 a[194:195], v244 offset:16896\n"
"v_accvgpr_write_b32 a25, v161\n"
"v_mfma_f32_16x16x32_bf16 v[124:127], a[124:127], a[164:167], v[124:127]\n"
"ds_read_b64_tr_b16 a[196:197], v244 offset:24576\n"
"v_accvgpr_write_b32 a27, v163\n"
"v_mfma_f32_16x16x32_bf16 v[128:131], a[128:131], a[160:163], v[228:231]\n"
"v_permlane16_swap_b32 v152, v154\n"
"v_mfma_f32_16x16x32_bf16 v[128:131], a[132:135], a[164:167], v[128:131]\n"
"ds_read_b64_tr_b16 a[198:199], v244 offset:25088\n"
"v_accvgpr_write_b32 a20, v152\n"
"v_mfma_f32_16x16x32_bf16 v[132:135], a[136:139], a[160:163], v[228:231]\n"
"ds_read_b64_tr_b16 a[200:201], v245 offset:16384\n"
"v_accvgpr_write_b32 a22, v154\n"
"v_mfma_f32_16x16x32_bf16 v[132:135], a[140:143], a[164:167], v[132:135]\n"
"buffer_load_dwordx4 v225, s[28:31], s40 offen offset:1024 lds\n"
"v_mfma_f32_16x16x32_bf16 v[136:139], a[144:147], a[160:163], v[228:231]\n"
"v_permlane16_swap_b32 v153, v155\n"
"v_mfma_f32_16x16x32_bf16 v[136:139], a[148:151], a[164:167], v[136:139]\n"
"ds_read_b64_tr_b16 a[202:203], v245 offset:16896\n"
"v_accvgpr_write_b32 a21, v153\n"
"v_mfma_f32_16x16x32_bf16 v[140:143], a[152:155], a[160:163], v[228:231]\n"
"ds_read_b64_tr_b16 a[204:205], v245 offset:24576\n"
"v_accvgpr_write_b32 a23, v155\n"
"v_mfma_f32_16x16x32_bf16 v[140:143], a[156:159], a[164:167], v[140:143]\n"
"s_waitcnt lgkmcnt(4)\n"
"v_permlane16_swap_b32 v144, v146\n"
"v_mfma_f32_16x16x32_bf16 v[172:175], a[152:155], a[168:171], v[228:231]\n"
"ds_read_b64_tr_b16 a[206:207], v245 offset:25088\n"
"v_accvgpr_write_b32 a16, v144\n"
"v_mfma_f32_16x16x32_bf16 v[172:175], a[156:159], a[172:175], v[172:175]\n"
"buffer_load_dwordx4 v226, s[28:31], s40 offen offset:2048 lds\n"
"v_mfma_f32_16x16x32_bf16 v[168:171], a[144:147], a[168:171], v[228:231]\n"
"ds_read_b64_tr_b16 a[208:209], v246 offset:16384\n"
"v_accvgpr_write_b32 a18, v146\n"
"v_mfma_f32_16x16x32_bf16 v[168:171], a[148:151], a[172:175], v[168:171]\n"
"v_permlane16_swap_b32 v145, v147\n"
"v_mfma_f32_16x16x32_bf16 v[164:167], a[136:139], a[168:171], v[228:231]\n"
"ds_read_b64_tr_b16 a[210:211], v246 offset:16896\n"
"v_accvgpr_write_b32 a17, v145\n"
"v_mfma_f32_16x16x32_bf16 v[164:167], a[140:143], a[172:175], v[164:167]\n"
"ds_read_b64_tr_b16 a[212:213], v246 offset:24576\n"
"v_accvgpr_write_b32 a19, v147\n"
"v_mfma_f32_16x16x32_bf16 v[160:163], a[128:131], a[168:171], v[228:231]\n"
"v_pk_fma_f32 v[176:177], v[176:177], s[70:71], s[70:71] op_sel:[0,0,1] op_sel_hi:[1,0,1]\nv_pk_fma_f32 v[178:179], v[178:179], s[70:71], s[70:71] op_sel:[0,0,1] op_sel_hi:[1,0,1]\nv_pk_fma_f32 v[180:181], v[180:181], s[70:71], s[70:71] op_sel:[0,0,1] op_sel_hi:[1,0,1]\nv_pk_fma_f32 v[182:183], v[182:183], s[70:71], s[70:71] op_sel:[0,0,1] op_sel_hi:[1,0,1]\nv_pk_fma_f32 v[184:185], v[184:185], s[70:71], s[70:71] op_sel:[0,0,1] op_sel_hi:[1,0,1]\nv_pk_fma_f32 v[186:187], v[186:187], s[70:71], s[70:71] op_sel:[0,0,1] op_sel_hi:[1,0,1]\nv_pk_fma_f32 v[188:189], v[188:189], s[70:71], s[70:71] op_sel:[0,0,1] op_sel_hi:[1,0,1]\nv_pk_fma_f32 v[190:191], v[190:191], s[70:71], s[70:71] op_sel:[0,0,1] op_sel_hi:[1,0,1]\nv_pk_fma_f32 v[192:193], v[192:193], s[70:71], s[70:71] op_sel:[0,0,1] op_sel_hi:[1,0,1]\nv_pk_fma_f32 v[194:195], v[194:195], s[70:71], s[70:71] op_sel:[0,0,1] op_sel_hi:[1,0,1]\nv_pk_fma_f32 v[196:197], v[196:197], s[70:71], s[70:71] op_sel:[0,0,1] op_sel_hi:[1,0,1]\nv_pk_fma_f32 v[198:199], v[198:199], s[70:71], s[70:71] op_sel:[0,0,1] op_sel_hi:[1,0,1]\nv_pk_fma_f32 v[200:201], v[200:201], s[70:71], s[70:71] op_sel:[0,0,1] op_sel_hi:[1,0,1]\nv_pk_fma_f32 v[202:203], v[202:203], s[70:71], s[70:71] op_sel:[0,0,1] op_sel_hi:[1,0,1]\nv_pk_fma_f32 v[204:205], v[204:205], s[70:71], s[70:71] op_sel:[0,0,1] op_sel_hi:[1,0,1]\nv_pk_fma_f32 v[206:207], v[206:207], s[70:71], s[70:71] op_sel:[0,0,1] op_sel_hi:[1,0,1]\n"
"v_mfma_f32_16x16x32_bf16 v[160:163], a[132:135], a[172:175], v[160:163]\n"
"buffer_load_dwordx4 v227, s[28:31], s40 offen offset:3072 lds\n"
"v_mfma_f32_16x16x32_bf16 v[156:159], a[120:123], a[168:171], v[228:231]\n"
"s_cmp_eq_u32 s47, 8\ns_cselect_b32 s41, 524800, s58\n"
"ds_read_b64_tr_b16 a[214:215], v246 offset:25088\n"
"v_cvt_pk_bf16_f32 v176, v176, v177\n"
"v_mfma_f32_16x16x32_bf16 v[156:159], a[124:127], a[172:175], v[156:159]\n"
"s_add_u32 s48, s48, s41\n"
"ds_read_b64_tr_b16 a[216:217], v247 offset:16384\n"
"v_cvt_pk_bf16_f32 v177, v178, v179\n"
"v_mfma_f32_16x16x32_bf16 v[152:155], a[112:115], a[168:171], v[228:231]\n"
"s_sub_u32 s41, s48, 5242880\n"
"ds_read_b64_tr_b16 a[218:219], v247 offset:16896\n"
"v_cvt_pk_bf16_f32 v178, v180, v181\n"
"v_mfma_f32_16x16x32_bf16 v[152:155], a[116:119], a[172:175], v[152:155]\n"
"s_min_u32 s48, s48, s41\n"
"ds_read_b64_tr_b16 a[220:221], v247 offset:24576\n"
"v_cvt_pk_bf16_f32 v179, v182, v183\n"
"v_mfma_f32_16x16x32_bf16 v[148:151], a[104:107], a[168:171], v[228:231]\n"
"s_mov_b32 m0, s50\n"
"v_permlane16_swap_b32 v176, v178\n"
"v_mfma_f32_16x16x32_bf16 v[148:151], a[108:111], a[172:175], v[148:151]\n"
"buffer_load_dwordx4 v224, s[28:31], s48 offen lds\n"
"v_mfma_f32_16x16x32_bf16 v[144:147], a[96:99], a[168:171], v[228:231]\n"
"v_permlane16_swap_b32 v177, v179\n"
"v_mfma_f32_16x16x32_bf16 v[144:147], a[100:103], a[172:175], v[144:147]\n"
"v_accvgpr_write_b32 a32, v176\n"
"v_accvgpr_write_b32 a33, v177\n"
"v_accvgpr_write_b32 a34, v178\n"
"v_accvgpr_write_b32 a35, v179\n"
"v_mfma_f32_16x16x32_bf16 v[176:179], a[96:99], a[176:179], v[228:231]\n"
"v_permlane16_swap_b32 v216, v218\n"
"v_permlane16_swap_b32 v217, v219\n"
"v_mfma_f32_16x16x32_bf16 v[176:179], a[100:103], a[180:183], v[176:179]\n"
"ds_write_b128 v243, v[216:219] offset:2048\n"
"v_mfma_f32_16x16x32_bf16 v[180:183], a[104:107], a[176:179], v[228:231]\n"
"v_cvt_pk_bf16_f32 v184, v184, v185\n"
"v_cvt_pk_bf16_f32 v185, v186, v187\n"
"v_cvt_pk_bf16_f32 v186, v188, v189\n"
"v_cvt_pk_bf16_f32 v187, v190, v191\n"
"v_mfma_f32_16x16x32_bf16 v[180:183], a[108:111], a[180:183], v[180:183]\n"
"buffer_load_dwordx4 v225, s[28:31], s48 offen offset:1024 lds\n"
"v_permlane16_swap_b32 v184, v186\n"
"v_permlane16_swap_b32 v185, v187\n"
"v_accvgpr_write_b32 a36, v184\n"
"v_accvgpr_write_b32 a37, v185\n"
"v_accvgpr_write_b32 a38, v186\n"
"v_accvgpr_write_b32 a39, v187\n"
"v_mfma_f32_16x16x32_bf16 v[184:187], a[112:115], a[176:179], v[228:231]\n"
"v_permlane16_swap_b32 v208, v210\n"
"v_mfma_f32_16x16x32_bf16 v[184:187], a[116:119], a[180:183], v[184:187]\n"
"v_permlane16_swap_b32 v209, v211\n"
"v_mfma_f32_16x16x32_bf16 v[188:191], a[120:123], a[176:179], v[228:231]\n"
"ds_write_b128 v243, v[208:211] offset:0\n"
"v_cvt_pk_bf16_f32 v200, v200, v201\n"
"v_cvt_pk_bf16_f32 v201, v202, v203\n"
"v_cvt_pk_bf16_f32 v202, v204, v205\n"
"v_cvt_pk_bf16_f32 v203, v206, v207\n"
"v_mfma_f32_16x16x32_bf16 v[188:191], a[124:127], a[180:183], v[188:191]\n"
"v_permlane16_swap_b32 v200, v202\n"
"v_permlane16_swap_b32 v201, v203\n"
"v_accvgpr_write_b32 a44, v200\n"
"v_accvgpr_write_b32 a45, v201\n"
"v_accvgpr_write_b32 a46, v202\n"
"v_accvgpr_write_b32 a47, v203\n"
"v_mfma_f32_16x16x32_bf16 v[200:203], a[144:147], a[176:179], v[228:231]\n"
"ds_read_b64_tr_b16 a[222:223], v247 offset:25088\n"
"v_cvt_pk_bf16_f32 v192, v192, v193\n"
"v_mfma_f32_16x16x32_bf16 v[200:203], a[148:151], a[180:183], v[200:203]\n"
"buffer_load_dwordx4 v226, s[28:31], s48 offen offset:2048 lds\n"
"v_mfma_f32_16x16x32_bf16 v[204:207], a[152:155], a[176:179], v[228:231]\n"
"ds_read_b64_tr_b16 a[224:225], v244 offset:17408\n"
"v_cvt_pk_bf16_f32 v193, v194, v195\n"
"v_cvt_pk_bf16_f32 v194, v196, v197\n"
"v_cvt_pk_bf16_f32 v195, v198, v199\n"
"v_mfma_f32_16x16x32_bf16 v[204:207], a[156:159], a[180:183], v[204:207]\n"
"v_permlane16_swap_b32 v192, v194\n"
"v_permlane16_swap_b32 v193, v195\n"
"v_accvgpr_write_b32 a40, v192\n"
"v_accvgpr_write_b32 a41, v193\n"
"v_accvgpr_write_b32 a42, v194\n"
"v_accvgpr_write_b32 a43, v195\n"
"v_mfma_f32_16x16x32_bf16 v[192:195], a[128:131], a[176:179], v[228:231]\n"
"ds_read_b64_tr_b16 a[226:227], v244 offset:17920\n"
"v_xor_b32 v250, s74, v250\n"
"v_mfma_f32_16x16x32_bf16 v[192:195], a[132:135], a[180:183], v[192:195]\n"
"ds_read_b64_tr_b16 a[228:229], v244 offset:25600\n"
"v_xor_b32 v251, s74, v251\n"
"v_mfma_f32_16x16x32_bf16 v[196:199], a[136:139], a[176:179], v[228:231]\n"
"ds_read_b64_tr_b16 a[230:231], v244 offset:26112\n"
"v_xor_b32 v244, s74, v244\n"
"v_mfma_f32_16x16x32_bf16 v[196:199], a[140:143], a[180:183], v[196:199]\n"
"buffer_load_dwordx4 v227, s[28:31], s48 offen offset:3072 lds\n"
"v_mfma_f32_16x16x32_bf16 v[220:223], a[136:139], a[184:187], v[228:231]\n"
"s_xor_b32 s50, s50, s74\n"
"ds_read_b64_tr_b16 a[232:233], v245 offset:17408\n"
"ds_read_b64_tr_b16 a[234:235], v245 offset:17920\n"
"v_mfma_f32_16x16x32_bf16 v[220:223], a[140:143], a[188:191], v[220:223]\n"
"ds_read_b64_tr_b16 a[236:237], v245 offset:25600\n"
"ds_read_b64_tr_b16 a[238:239], v245 offset:26112\n"
"v_mfma_f32_16x16x32_bf16 v[216:219], a[128:131], a[184:187], v[228:231]\n"
"ds_read_b64_tr_b16 a[240:241], v246 offset:17408\n"
"ds_read_b64_tr_b16 a[242:243], v246 offset:17920\n"
"v_mfma_f32_16x16x32_bf16 v[216:219], a[132:135], a[188:191], v[216:219]\n"
"ds_read_b64_tr_b16 a[244:245], v246 offset:25600\n"
"ds_read_b64_tr_b16 a[246:247], v246 offset:26112\n"
"v_mfma_f32_16x16x32_bf16 v[212:215], a[104:107], a[184:187], v[228:231]\n"
"ds_read_b64_tr_b16 a[248:249], v247 offset:17408\n"
"ds_read_b64_tr_b16 a[250:251], v247 offset:17920\n"
"v_mfma_f32_16x16x32_bf16 v[212:215], a[108:111], a[188:191], v[212:215]\n"
"ds_read_b64_tr_b16 a[252:253], v247 offset:25600\n"
"ds_read_b64_tr_b16 a[254:255], v247 offset:26112\n"
"v_mfma_f32_16x16x32_bf16 v[208:211], a[96:99], a[184:187], v[228:231]\n"
"v_pk_fma_f32 v[0:1], v[0:1], s[70:71], s[70:71] op_sel:[0,0,1] op_sel_hi:[1,0,1]\nv_pk_fma_f32 v[2:3], v[2:3], s[70:71], s[70:71] op_sel:[0,0,1] op_sel_hi:[1,0,1]\nv_pk_fma_f32 v[4:5], v[4:5], s[70:71], s[70:71] op_sel:[0,0,1] op_sel_hi:[1,0,1]\nv_pk_fma_f32 v[6:7], v[6:7], s[70:71], s[70:71] op_sel:[0,0,1] op_sel_hi:[1,0,1]\nv_pk_fma_f32 v[8:9], v[8:9], s[70:71], s[70:71] op_sel:[0,0,1] op_sel_hi:[1,0,1]\nv_pk_fma_f32 v[10:11], v[10:11], s[70:71], s[70:71] op_sel:[0,0,1] op_sel_hi:[1,0,1]\nv_pk_fma_f32 v[12:13], v[12:13], s[70:71], s[70:71] op_sel:[0,0,1] op_sel_hi:[1,0,1]\nv_pk_fma_f32 v[14:15], v[14:15], s[70:71], s[70:71] op_sel:[0,0,1] op_sel_hi:[1,0,1]\nv_pk_fma_f32 v[16:17], v[16:17], s[70:71], s[70:71] op_sel:[0,0,1] op_sel_hi:[1,0,1]\nv_pk_fma_f32 v[18:19], v[18:19], s[70:71], s[70:71] op_sel:[0,0,1] op_sel_hi:[1,0,1]\nv_pk_fma_f32 v[20:21], v[20:21], s[70:71], s[70:71] op_sel:[0,0,1] op_sel_hi:[1,0,1]\nv_pk_fma_f32 v[22:23], v[22:23], s[70:71], s[70:71] op_sel:[0,0,1] op_sel_hi:[1,0,1]\nv_pk_fma_f32 v[24:25], v[24:25], s[70:71], s[70:71] op_sel:[0,0,1] op_sel_hi:[1,0,1]\nv_pk_fma_f32 v[26:27], v[26:27], s[70:71], s[70:71] op_sel:[0,0,1] op_sel_hi:[1,0,1]\nv_pk_fma_f32 v[28:29], v[28:29], s[70:71], s[70:71] op_sel:[0,0,1] op_sel_hi:[1,0,1]\nv_pk_fma_f32 v[30:31], v[30:31], s[70:71], s[70:71] op_sel:[0,0,1] op_sel_hi:[1,0,1]\n"
"v_cvt_pk_bf16_f32 v0, v0, v1\n"
"v_cvt_pk_bf16_f32 v1, v2, v3\n"
"v_cvt_pk_bf16_f32 v2, v4, v5\n"
"v_cvt_pk_bf16_f32 v3, v6, v7\n"
"v_mfma_f32_16x16x32_bf16 v[208:211], a[100:103], a[188:191], v[208:211]\n"
"v_permlane16_swap_b32 v0, v2\n"
"v_permlane16_swap_b32 v1, v3\n"
"v_accvgpr_write_b32 a48, v0\n"
"v_accvgpr_write_b32 a49, v1\n"
"v_accvgpr_write_b32 a50, v2\n"
"v_accvgpr_write_b32 a51, v3\n"
"v_mfma_f32_16x16x32_bf16 v[0:3], a[192:195], a[160:163], v[228:231]\n"
"s_waitcnt lgkmcnt(10)\n"
"v_cvt_pk_bf16_f32 v8, v8, v9\n"
"v_cvt_pk_bf16_f32 v9, v10, v11\n"
"v_mfma_f32_16x16x32_bf16 v[0:3], a[196:199], a[164:167], v[0:3]\n"
"v_cvt_pk_bf16_f32 v10, v12, v13\n"
"v_cvt_pk_bf16_f32 v11, v14, v15\n"
"v_mfma_f32_16x16x32_bf16 v[4:7], a[200:203], a[160:163], v[228:231]\n"
"s_waitcnt vmcnt(10) lgkmcnt(0)\n"
"v_permlane16_swap_b32 v8, v10\n"
"v_mfma_f32_16x16x32_bf16 v[4:7], a[204:207], a[164:167], v[4:7]\n"
"s_barrier\n"
"v_permlane16_swap_b32 v9, v11\n"
"v_accvgpr_write_b32 a52, v8\n"
"v_accvgpr_write_b32 a53, v9\n"
"v_accvgpr_write_b32 a54, v10\n"
"v_accvgpr_write_b32 a55, v11\n"
"v_mfma_f32_16x16x32_bf16 v[8:11], a[208:211], a[160:163], v[228:231]\n"
"s_mov_b32 m0, s51\n"
"ds_read_b64_tr_b16 a[96:97], v244 offset:0\n"
"v_cvt_pk_bf16_f32 v16, v16, v17\n"
"v_mfma_f32_16x16x32_bf16 v[8:11], a[212:215], a[164:167], v[8:11]\n"
"buffer_load_dwordx4 v248, s[24:27], s49 offen lds\n"
"v_mfma_f32_16x16x32_bf16 v[12:15], a[216:219], a[160:163], v[228:231]\n"
"ds_read_b64_tr_b16 a[98:99], v244 offset:512\n"
"v_cvt_pk_bf16_f32 v17, v18, v19\n"
"v_cvt_pk_bf16_f32 v18, v20, v21\n"
"v_cvt_pk_bf16_f32 v19, v22, v23\n"
"v_mfma_f32_16x16x32_bf16 v[12:15], a[220:223], a[164:167], v[12:15]\n"
"v_permlane16_swap_b32 v16, v18\n"
"v_permlane16_swap_b32 v17, v19\n"
"v_accvgpr_write_b32 a56, v16\n"
"v_accvgpr_write_b32 a57, v17\n"
"v_accvgpr_write_b32 a58, v18\n"
"v_accvgpr_write_b32 a59, v19\n"
"v_mfma_f32_16x16x32_bf16 v[16:19], a[224:227], a[160:163], v[228:231]\n"
"ds_read_b64_tr_b16 a[100:101], v244 offset:8192\n"
"v_cvt_pk_bf16_f32 v24, v24, v25\n"
"v_mfma_f32_16x16x32_bf16 v[16:19], a[228:231], a[164:167], v[16:19]\n"
"ds_read_b64_tr_b16 a[102:103], v244 offset:8704\n"
"v_cvt_pk_bf16_f32 v25, v26, v27\n"
"v_mfma_f32_16x16x32_bf16 v[20:23], a[232:235], a[160:163], v[228:231]\n"
"ds_read_b64_tr_b16 a[128:129], v244 offset:1024\n"
"v_cvt_pk_bf16_f32 v26, v28, v29\n"
"v_cvt_pk_bf16_f32 v27, v30, v31\n"
"v_mfma_f32_16x16x32_bf16 v[20:23], a[236:239], a[164:167], v[20:23]\n"
"buffer_load_dwordx4 v249, s[24:27], s49 offen offset:1024 lds\n"
"v_permlane16_swap_b32 v24, v26\n"
"v_permlane16_swap_b32 v25, v27\n"
"v_accvgpr_write_b32 a60, v24\n"
"v_accvgpr_write_b32 a61, v25\n"
"v_accvgpr_write_b32 a62, v26\n"
"v_accvgpr_write_b32 a63, v27\n"
"v_mfma_f32_16x16x32_bf16 v[24:27], a[240:243], a[160:163], v[228:231]\n"
"ds_read_b64_tr_b16 a[130:131], v244 offset:1536\n"
"v_xor_b32 v245, s74, v245\n"
"v_mfma_f32_16x16x32_bf16 v[24:27], a[244:247], a[164:167], v[24:27]\n"
"ds_read_b64_tr_b16 a[104:105], v245 offset:0\n"
"v_xor_b32 v246, s74, v246\n"
"v_mfma_f32_16x16x32_bf16 v[28:31], a[248:251], a[160:163], v[228:231]\n"
"v_pk_fma_f32 v[32:33], v[32:33], s[70:71], s[70:71] op_sel:[0,0,1] op_sel_hi:[1,0,1]\nv_pk_fma_f32 v[34:35], v[34:35], s[70:71], s[70:71] op_sel:[0,0,1] op_sel_hi:[1,0,1]\nv_pk_fma_f32 v[36:37], v[36:37], s[70:71], s[70:71] op_sel:[0,0,1] op_sel_hi:[1,0,1]\nv_pk_fma_f32 v[38:39], v[38:39], s[70:71], s[70:71] op_sel:[0,0,1] op_sel_hi:[1,0,1]\nv_pk_fma_f32 v[40:41], v[40:41], s[70:71], s[70:71] op_sel:[0,0,1] op_sel_hi:[1,0,1]\nv_pk_fma_f32 v[42:43], v[42:43], s[70:71], s[70:71] op_sel:[0,0,1] op_sel_hi:[1,0,1]\nv_pk_fma_f32 v[44:45], v[44:45], s[70:71], s[70:71] op_sel:[0,0,1] op_sel_hi:[1,0,1]\nv_pk_fma_f32 v[46:47], v[46:47], s[70:71], s[70:71] op_sel:[0,0,1] op_sel_hi:[1,0,1]\nv_pk_fma_f32 v[48:49], v[48:49], s[70:71], s[70:71] op_sel:[0,0,1] op_sel_hi:[1,0,1]\nv_pk_fma_f32 v[50:51], v[50:51], s[70:71], s[70:71] op_sel:[0,0,1] op_sel_hi:[1,0,1]\nv_pk_fma_f32 v[52:53], v[52:53], s[70:71], s[70:71] op_sel:[0,0,1] op_sel_hi:[1,0,1]\nv_pk_fma_f32 v[54:55], v[54:55], s[70:71], s[70:71] op_sel:[0,0,1] op_sel_hi:[1,0,1]\nv_pk_fma_f32 v[56:57], v[56:57], s[70:71], s[70:71] op_sel:[0,0,1] op_sel_hi:[1,0,1]\nv_pk_fma_f32 v[58:59], v[58:59], s[70:71], s[70:71] op_sel:[0,0,1] op_sel_hi:[1,0,1]\nv_pk_fma_f32 v[60:61], v[60:61], s[70:71], s[70:71] op_sel:[0,0,1] op_sel_hi:[1,0,1]\nv_pk_fma_f32 v[62:63], v[62:63], s[70:71], s[70:71] op_sel:[0,0,1] op_sel_hi:[1,0,1]\n"
"v_mfma_f32_16x16x32_bf16 v[28:31], a[252:255], a[164:167], v[28:31]\n"
"ds_read_b128 a[160:163], v250 offset:0\n"
"v_cvt_pk_bf16_f32 v56, v56, v57\n"
"v_cvt_pk_bf16_f32 v57, v58, v59\n"
"v_cvt_pk_bf16_f32 v58, v60, v61\n"
"v_cvt_pk_bf16_f32 v59, v62, v63\n"
"v_mfma_f32_16x16x32_bf16 v[60:63], a[248:251], a[168:171], v[228:231]\n"
"v_permlane16_swap_b32 v56, v58\n"
"v_mfma_f32_16x16x32_bf16 v[60:63], a[252:255], a[172:175], v[60:63]\n"
"buffer_load_dwordx4 v254, s[24:27], s49 offen offset:2048 lds\n"
"v_permlane16_swap_b32 v57, v59\n"
"v_accvgpr_write_b32 a76, v56\n"
"v_accvgpr_write_b32 a77, v57\n"
"v_accvgpr_write_b32 a78, v58\n"
"v_accvgpr_write_b32 a79, v59\n"
"v_mfma_f32_16x16x32_bf16 v[56:59], a[240:243], a[168:171], v[228:231]\n"
"ds_read_b128 a[164:167], v251 offset:0\n"
"v_cvt_pk_bf16_f32 v48, v48, v49\n"
"v_mfma_f32_16x16x32_bf16 v[56:59], a[244:247], a[172:175], v[56:59]\n"
"ds_read_b64_tr_b16 a[106:107], v245 offset:512\n"
"v_cvt_pk_bf16_f32 v49, v50, v51\n"
"v_cvt_pk_bf16_f32 v50, v52, v53\n"
"v_cvt_pk_bf16_f32 v51, v54, v55\n"
"v_mfma_f32_16x16x32_bf16 v[52:55], a[232:235], a[168:171], v[228:231]\n"
"v_permlane16_swap_b32 v48, v50\n"
"v_mfma_f32_16x16x32_bf16 v[52:55], a[236:239], a[172:175], v[52:55]\n"
"v_permlane16_swap_b32 v49, v51\n"
"v_accvgpr_write_b32 a72, v48\n"
"v_accvgpr_write_b32 a73, v49\n"
"v_accvgpr_write_b32 a74, v50\n"
"v_accvgpr_write_b32 a75, v51\n"
"v_mfma_f32_16x16x32_bf16 v[48:51], a[224:227], a[168:171], v[228:231]\n"
"ds_read_b64_tr_b16 a[108:109], v245 offset:8192\n"
"v_cvt_pk_bf16_f32 v40, v40, v41\n"
"v_mfma_f32_16x16x32_bf16 v[48:51], a[228:231], a[172:175], v[48:51]\n"
"buffer_load_dwordx4 v255, s[24:27], s49 offen offset:3072 lds\n"
"v_cvt_pk_bf16_f32 v41, v42, v43\n"
"v_cvt_pk_bf16_f32 v42, v44, v45\n"
"v_cvt_pk_bf16_f32 v43, v46, v47\n"
"v_mfma_f32_16x16x32_bf16 v[44:47], a[216:219], a[168:171], v[228:231]\n"
"v_pk_fma_f32 v[96:97], v[96:97], s[70:71], s[70:71] op_sel:[0,0,1] op_sel_hi:[1,0,1]\nv_pk_fma_f32 v[98:99], v[98:99], s[70:71], s[70:71] op_sel:[0,0,1] op_sel_hi:[1,0,1]\nv_pk_fma_f32 v[100:101], v[100:101], s[70:71], s[70:71] op_sel:[0,0,1] op_sel_hi:[1,0,1]\nv_pk_fma_f32 v[102:103], v[102:103], s[70:71], s[70:71] op_sel:[0,0,1] op_sel_hi:[1,0,1]\nv_pk_fma_f32 v[104:105], v[104:105], s[70:71], s[70:71] op_sel:[0,0,1] op_sel_hi:[1,0,1]\nv_pk_fma_f32 v[106:107], v[106:107], s[70:71], s[70:71] op_sel:[0,0,1] op_sel_hi:[1,0,1]\nv_pk_fma_f32 v[108:109], v[108:109], s[70:71], s[70:71] op_sel:[0,0,1] op_sel_hi:[1,0,1]\nv_pk_fma_f32 v[110:111], v[110:111], s[70:71], s[70:71] op_sel:[0,0,1] op_sel_hi:[1,0,1]\n"
"v_mfma_f32_16x16x32_bf16 v[44:47], a[220:223], a[172:175], v[44:47]\n"
"v_cvt_pk_bf16_f32 v104, v104, v105\n"
"v_cvt_pk_bf16_f32 v105, v106, v107\n"
"v_cvt_pk_bf16_f32 v106, v108, v109\n"
"v_cvt_pk_bf16_f32 v107, v110, v111\n"
"v_permlane16_swap_b32 v40, v42\n"
"v_permlane16_swap_b32 v41, v43\n"
"v_accvgpr_write_b32 a68, v40\n"
"v_accvgpr_write_b32 a69, v41\n"
"v_accvgpr_write_b32 a70, v42\n"
"v_accvgpr_write_b32 a71, v43\n"
"v_mfma_f32_16x16x32_bf16 v[40:43], a[208:211], a[168:171], v[228:231]\n"
"v_permlane16_swap_b32 v104, v106\n"
"v_permlane16_swap_b32 v105, v107\n"
"v_mfma_f32_16x16x32_bf16 v[40:43], a[212:215], a[172:175], v[40:43]\n"
"s_add_u32 s40, s49, 40960\n"
"ds_write_b128 v243, v[104:107] offset:6144\n"
"v_cvt_pk_bf16_f32 v32, v32, v33\n"
"v_cvt_pk_bf16_f32 v33, v34, v35\n"
"v_cvt_pk_bf16_f32 v34, v36, v37\n"
"v_cvt_pk_bf16_f32 v35, v38, v39\n"
"v_mfma_f32_16x16x32_bf16 v[36:39], a[200:203], a[168:171], v[228:231]\n"
"s_add_u32 m0, s51, 4096\n"
"v_permlane16_swap_b32 v32, v34\n"
"v_mfma_f32_16x16x32_bf16 v[36:39], a[204:207], a[172:175], v[36:39]\n"
"buffer_load_dwordx4 v248, s[24:27], s40 offen lds\n"
"v_permlane16_swap_b32 v33, v35\n"
"v_accvgpr_write_b32 a64, v32\n"
"v_accvgpr_write_b32 a65, v33\n"
"v_accvgpr_write_b32 a66, v34\n"
"v_accvgpr_write_b32 a67, v35\n"
"v_mfma_f32_16x16x32_bf16 v[32:35], a[192:195], a[168:171], v[228:231]\n"
"v_pk_fma_f32 v[64:65], v[64:65], s[70:71], s[70:71] op_sel:[0,0,1] op_sel_hi:[1,0,1]\nv_pk_fma_f32 v[66:67], v[66:67], s[70:71], s[70:71] op_sel:[0,0,1] op_sel_hi:[1,0,1]\nv_pk_fma_f32 v[68:69], v[68:69], s[70:71], s[70:71] op_sel:[0,0,1] op_sel_hi:[1,0,1]\nv_pk_fma_f32 v[70:71], v[70:71], s[70:71], s[70:71] op_sel:[0,0,1] op_sel_hi:[1,0,1]\nv_pk_fma_f32 v[72:73], v[72:73], s[70:71], s[70:71] op_sel:[0,0,1] op_sel_hi:[1,0,1]\nv_pk_fma_f32 v[74:75], v[74:75], s[70:71], s[70:71] op_sel:[0,0,1] op_sel_hi:[1,0,1]\nv_pk_fma_f32 v[76:77], v[76:77], s[70:71], s[70:71] op_sel:[0,0,1] op_sel_hi:[1,0,1]\nv_pk_fma_f32 v[78:79], v[78:79], s[70:71], s[70:71] op_sel:[0,0,1] op_sel_hi:[1,0,1]\nv_pk_fma_f32 v[80:81], v[80:81], s[70:71], s[70:71] op_sel:[0,0,1] op_sel_hi:[1,0,1]\nv_pk_fma_f32 v[82:83], v[82:83], s[70:71], s[70:71] op_sel:[0,0,1] op_sel_hi:[1,0,1]\nv_pk_fma_f32 v[84:85], v[84:85], s[70:71], s[70:71] op_sel:[0,0,1] op_sel_hi:[1,0,1]\nv_pk_fma_f32 v[86:87], v[86:87], s[70:71], s[70:71] op_sel:[0,0,1] op_sel_hi:[1,0,1]\nv_pk_fma_f32 v[88:89], v[88:89], s[70:71], s[70:71] op_sel:[0,0,1] op_sel_hi:[1,0,1]\nv_pk_fma_f32 v[90:91], v[90:91], s[70:71], s[70:71] op_sel:[0,0,1] op_sel_hi:[1,0,1]\nv_pk_fma_f32 v[92:93], v[92:93], s[70:71], s[70:71] op_sel:[0,0,1] op_sel_hi:[1,0,1]\nv_pk_fma_f32 v[94:95], v[94:95], s[70:71], s[70:71] op_sel:[0,0,1] op_sel_hi:[1,0,1]\n"
"v_cvt_pk_bf16_f32 v64, v64, v65\n"
"v_cvt_pk_bf16_f32 v65, v66, v67\n"
"v_cvt_pk_bf16_f32 v66, v68, v69\n"
"v_cvt_pk_bf16_f32 v67, v70, v71\n"
"v_mfma_f32_16x16x32_bf16 v[32:35], a[196:199], a[172:175], v[32:35]\n"
"v_permlane16_swap_b32 v64, v66\n"
"v_permlane16_swap_b32 v65, v67\n"
"v_accvgpr_write_b32 a80, v64\n"
"v_accvgpr_write_b32 a81, v65\n"
"v_accvgpr_write_b32 a82, v66\n"
"v_accvgpr_write_b32 a83, v67\n"
"v_mfma_f32_16x16x32_bf16 v[64:67], a[192:195], a[176:179], v[228:231]\n"
"ds_read_b64_tr_b16 a[110:111], v245 offset:8704\n"
"v_cvt_pk_bf16_f32 v96, v96, v97\n"
"v_cvt_pk_bf16_f32 v97, v98, v99\n"
"v_cvt_pk_bf16_f32 v98, v100, v101\n"
"v_cvt_pk_bf16_f32 v99, v102, v103\n"
"v_mfma_f32_16x16x32_bf16 v[64:67], a[196:199], a[180:183], v[64:67]\n"
"v_permlane16_swap_b32 v96, v98\n"
"v_permlane16_swap_b32 v97, v99\n"
"v_mfma_f32_16x16x32_bf16 v[68:71], a[200:203], a[176:179], v[228:231]\n"
"ds_write_b128 v243, v[96:99] offset:4096\n"
"v_cvt_pk_bf16_f32 v72, v72, v73\n"
"v_cvt_pk_bf16_f32 v73, v74, v75\n"
"v_cvt_pk_bf16_f32 v74, v76, v77\n"
"v_cvt_pk_bf16_f32 v75, v78, v79\n"
"v_mfma_f32_16x16x32_bf16 v[68:71], a[204:207], a[180:183], v[68:71]\n"
"buffer_load_dwordx4 v249, s[24:27], s40 offen offset:1024 lds\n"
"v_permlane16_swap_b32 v72, v74\n"
"v_permlane16_swap_b32 v73, v75\n"
"v_accvgpr_write_b32 a84, v72\n"
"v_accvgpr_write_b32 a85, v73\n"
"v_accvgpr_write_b32 a86, v74\n"
"v_accvgpr_write_b32 a87, v75\n"
"v_mfma_f32_16x16x32_bf16 v[72:75], a[208:211], a[176:179], v[228:231]\n"
"ds_read_b64_tr_b16 a[112:113], v246 offset:0\n"
"v_cvt_pk_bf16_f32 v88, v88, v89\n"
"v_mfma_f32_16x16x32_bf16 v[72:75], a[212:215], a[180:183], v[72:75]\n"
"ds_read_b64_tr_b16 a[114:115], v246 offset:512\n"
"v_cvt_pk_bf16_f32 v89, v90, v91\n"
"v_mfma_f32_16x16x32_bf16 v[76:79], a[216:219], a[176:179], v[228:231]\n"
"ds_read_b64_tr_b16 a[116:117], v246 offset:8192\n"
"ds_read_b64_tr_b16 a[118:119], v246 offset:8704\n"
"v_cvt_pk_bf16_f32 v90, v92, v93\n"
"v_cvt_pk_bf16_f32 v91, v94, v95\n"
"v_xor_b32 v247, s74, v247\n"
"v_mfma_f32_16x16x32_bf16 v[76:79], a[220:223], a[180:183], v[76:79]\n"
"ds_read_b64_tr_b16 a[120:121], v247 offset:0\n"
"ds_read_b64_tr_b16 a[122:123], v247 offset:512\n"
"v_permlane16_swap_b32 v88, v90\n"
"v_permlane16_swap_b32 v89, v91\n"
"v_accvgpr_write_b32 a92, v88\n"
"v_accvgpr_write_b32 a93, v89\n"
"v_accvgpr_write_b32 a94, v90\n"
"v_accvgpr_write_b32 a95, v91\n"
"v_mfma_f32_16x16x32_bf16 v[88:91], a[240:243], a[176:179], v[228:231]\n"
"ds_read_b64_tr_b16 a[124:125], v247 offset:8192\n"
"ds_read_b64_tr_b16 a[126:127], v247 offset:8704\n"
"v_mfma_f32_16x16x32_bf16 v[88:91], a[244:247], a[180:183], v[88:91]\n"
"buffer_load_dwordx4 v254, s[24:27], s40 offen offset:2048 lds\n"
"v_mfma_f32_16x16x32_bf16 v[92:95], a[248:251], a[176:179], v[228:231]\n"
"s_add_u32 s49, s49, 128\n"
"ds_read_b64_tr_b16 a[132:133], v244 offset:9216\n"
"v_cvt_pk_bf16_f32 v80, v80, v81\n"
"v_cvt_pk_bf16_f32 v81, v82, v83\n"
"v_cvt_pk_bf16_f32 v82, v84, v85\n"
"v_cvt_pk_bf16_f32 v83, v86, v87\n"
"v_mfma_f32_16x16x32_bf16 v[92:95], a[252:255], a[180:183], v[92:95]\n"
"s_sub_u32 s41, s49, 1280\n"
"ds_read_b64_tr_b16 a[134:135], v244 offset:9728\n"
"v_permlane16_swap_b32 v80, v82\n"
"v_permlane16_swap_b32 v81, v83\n"
"v_accvgpr_write_b32 a88, v80\n"
"v_accvgpr_write_b32 a89, v81\n"
"v_accvgpr_write_b32 a90, v82\n"
"v_accvgpr_write_b32 a91, v83\n"
"v_mfma_f32_16x16x32_bf16 v[80:83], a[224:227], a[176:179], v[228:231]\n"
"s_min_u32 s49, s49, s41\n"
"ds_read_b64_tr_b16 a[136:137], v245 offset:1024\n"
"ds_read_b64_tr_b16 a[138:139], v245 offset:1536\n"
"v_mfma_f32_16x16x32_bf16 v[80:83], a[228:231], a[180:183], v[80:83]\n"
"s_xor_b32 s51, s51, s74\n"
"ds_read_b64_tr_b16 a[140:141], v245 offset:9216\n"
"ds_read_b64_tr_b16 a[142:143], v245 offset:9728\n"
"v_mfma_f32_16x16x32_bf16 v[84:87], a[232:235], a[176:179], v[228:231]\n"
"ds_read_b64_tr_b16 a[144:145], v246 offset:1024\n"
"ds_read_b64_tr_b16 a[146:147], v246 offset:1536\n"
"v_mfma_f32_16x16x32_bf16 v[84:87], a[236:239], a[180:183], v[84:87]\n"
"ds_read_b64_tr_b16 a[148:149], v246 offset:9216\n"
"ds_read_b64_tr_b16 a[150:151], v246 offset:9728\n"
"v_mfma_f32_16x16x32_bf16 v[108:111], a[232:235], a[184:187], v[228:231]\n"
"ds_read_b64_tr_b16 a[152:153], v247 offset:1024\n"
"ds_read_b64_tr_b16 a[154:155], v247 offset:1536\n"
"v_mfma_f32_16x16x32_bf16 v[108:111], a[236:239], a[188:191], v[108:111]\n"
"ds_read_b64_tr_b16 a[156:157], v247 offset:9216\n"
"ds_read_b64_tr_b16 a[158:159], v247 offset:9728\n"
"v_mfma_f32_16x16x32_bf16 v[104:107], a[224:227], a[184:187], v[228:231]\n"
"ds_read_b128 a[168:171], v250 offset:2048\n"
"v_xor_b32 v252, s74, v252\n"
"v_mfma_f32_16x16x32_bf16 v[104:107], a[228:231], a[188:191], v[104:107]\n"
"ds_read_b128 a[172:175], v251 offset:2048\n"
"v_xor_b32 v253, s74, v253\n"
"v_mfma_f32_16x16x32_bf16 v[100:103], a[200:203], a[184:187], v[228:231]\n"
"ds_read_b128 a[176:179], v250 offset:4096\n"
"v_mfma_f32_16x16x32_bf16 v[100:103], a[204:207], a[188:191], v[100:103]\n"
"ds_read_b128 a[180:183], v251 offset:4096\n"
"v_mfma_f32_16x16x32_bf16 v[96:99], a[192:195], a[184:187], v[228:231]\n"
"s_add_u32 s47, s47, 1\n"
"ds_read_b128 a[184:187], v252 offset:0\n"
"v_mfma_f32_16x16x32_bf16 v[96:99], a[196:199], a[188:191], v[96:99]\n"
"s_waitcnt lgkmcnt(9)\n"
"ds_read_b128 a[188:191], v253 offset:0\n"
"s_mov_b32 s62, 18\n"
"s_branch LPL_%=\n"
"LPT2_%=:\n"
"v_mfma_f32_16x16x32_bf16 v[112:115], a[96:99], a[160:163], v[112:115]\n"
"v_bfe_u32 v228, v242, 8, 2\n"
"v_and_b32 v229, 0x60, v242\n"
"v_mfma_f32_16x16x32_bf16 v[112:115], a[100:103], a[164:167], v[112:115]\n"
"v_lshlrev_b32 v228, 4, v228\n"
"v_xor_b32 v228, v228, v229\n"
"v_mfma_f32_16x16x32_bf16 v[116:119], a[104:107], a[160:163], v[116:119]\n"
"s_waitcnt vmcnt(11)\n"
"v_and_b32 v229, 0x70, v242\n"
"v_lshl_add_u32 v228, v229, 3, v228\n"
"v_mfma_f32_16x16x32_bf16 v[116:119], a[108:111], a[164:167], v[116:119]\n"
"s_barrier\n"
"v_mfma_f32_16x16x32_bf16 v[120:123], a[112:115], a[160:163], v[120:123]\n"
"s_add_u32 s40, s48, 256\n"
"s_xor_b32 m0, s50, 0xc000\n"
"ds_read_b64_tr_b16 a[192:193], v244 offset:16384\n"
"v_mfma_f32_16x16x32_bf16 v[120:123], a[116:119], a[164:167], v[120:123]\n"
"buffer_load_dwordx4 v224, s[28:31], s40 offen lds\n"
"v_mfma_f32_16x16x32_bf16 v[124:127], a[120:123], a[160:163], v[124:127]\n"
"ds_read_b64_tr_b16 a[194:195], v244 offset:16896\n"
"v_and_b32 v229, 0x80, v242\n"
"v_mfma_f32_16x16x32_bf16 v[124:127], a[124:127], a[164:167], v[124:127]\n"
"ds_read_b64_tr_b16 a[196:197], v244 offset:24576\n"
"v_lshl_add_u32 v228, v229, 4, v228\n"
"v_mfma_f32_16x16x32_bf16 v[128:131], a[128:131], a[160:163], v[128:131]\n"
"ds_read_b64_tr_b16 a[198:199], v244 offset:25088\n"
"v_mov_b32 v229, s43\n"
"v_mfma_f32_16x16x32_bf16 v[128:131], a[132:135], a[164:167], v[128:131]\n"
"ds_read_b64_tr_b16 a[200:201], v245 offset:16384\n"
"v_mul_u32_u24 v229, 0x81, v229\n"
"v_mfma_f32_16x16x32_bf16 v[132:135], a[136:139], a[160:163], v[132:135]\n"
"ds_read_b64_tr_b16 a[202:203], v245 offset:16896\n"
"v_xor_b32 v228, v228, v229\n"
"v_mfma_f32_16x16x32_bf16 v[132:135], a[140:143], a[164:167], v[132:135]\n"
"buffer_load_dwordx4 v225, s[28:31], s40 offen offset:1024 lds\n"
"v_mfma_f32_16x16x32_bf16 v[136:139], a[144:147], a[160:163], v[136:139]\n"
"ds_read_b64_tr_b16 a[204:205], v245 offset:24576\n"
"v_xor_b32 v229, 64, v228\n"
"v_mfma_f32_16x16x32_bf16 v[136:139], a[148:151], a[164:167], v[136:139]\n"
"ds_read_b64_tr_b16 a[206:207], v245 offset:25088\n"
"v_xor_b32 v230, 0x2000, v228\n"
"v_mfma_f32_16x16x32_bf16 v[140:143], a[152:155], a[160:163], v[140:143]\n"
"ds_read_b64_tr_b16 a[208:209], v246 offset:16384\n"
"v_xor_b32 v231, 0x2000, v229\n"
"v_mfma_f32_16x16x32_bf16 v[140:143], a[156:159], a[164:167], v[140:143]\n"
"ds_read_b64_tr_b16 a[210:211], v246 offset:16896\n"
"v_and_b32 v254, 0x20, v242\n"
"v_mfma_f32_16x16x32_bf16 v[172:175], a[152:155], a[168:171], v[172:175]\n"
"ds_read_b64_tr_b16 a[212:213], v246 offset:24576\n"
"v_add_u32 v254, -1, v254\n"
"v_mfma_f32_16x16x32_bf16 v[172:175], a[156:159], a[172:175], v[172:175]\n"
"ds_read_b64_tr_b16 a[214:215], v246 offset:25088\n"
"v_ashrrev_i32 v254, 31, v254\n"
"v_mfma_f32_16x16x32_bf16 v[168:171], a[144:147], a[168:171], v[168:171]\n"
"ds_read_b64_tr_b16 a[216:217], v247 offset:16384\n"
"v_and_b32 v254, 0x3f803f80, v254\n"
"v_mfma_f32_16x16x32_bf16 v[168:171], a[148:151], a[172:175], v[168:171]\n"
"buffer_load_dwordx4 v226, s[28:31], s40 offen offset:2048 lds\n"
"v_mfma_f32_16x16x32_bf16 v[164:167], a[136:139], a[168:171], v[164:167]\n"
"ds_read_b64_tr_b16 a[218:219], v247 offset:16896\n"
"v_bfe_i32 v241, v242, 4, 1\n"
"v_mfma_f32_16x16x32_bf16 v[164:167], a[140:143], a[172:175], v[164:167]\n"
"ds_read_b64_tr_b16 a[220:221], v247 offset:24576\n"
"v_and_b32 v241, 0xaaaaaaaa, v241\n"
"v_mfma_f32_16x16x32_bf16 v[160:163], a[128:131], a[168:171], v[160:163]\n"
"ds_read_b64_tr_b16 a[222:223], v247 offset:25088\n"
"v_or_b32 v241, 0x44444444, v241\n"
"v_mfma_f32_16x16x32_bf16 v[160:163], a[132:135], a[172:175], v[160:163]\n"
"ds_read_b64_tr_b16 a[224:225], v244 offset:17408\n"
"v_bfe_u32 v248, v242, 8, 2\n"
"v_mfma_f32_16x16x32_bf16 v[156:159], a[120:123], a[168:171], v[156:159]\n"
"ds_read_b64_tr_b16 a[226:227], v244 offset:17920\n"
"v_lshlrev_b32 v248, 6, v248\n"
"v_mfma_f32_16x16x32_bf16 v[156:159], a[124:127], a[172:175], v[156:159]\n"
"buffer_load_dwordx4 v227, s[28:31], s40 offen offset:3072 lds\n"
"v_mfma_f32_16x16x32_bf16 v[152:155], a[112:115], a[168:171], v[152:155]\n"
"s_mov_b32 s58, 245760\n"
"ds_read_b64_tr_b16 a[228:229], v244 offset:25600\n"
"v_xor_b32 v248, v248, v242\n"
"v_mfma_f32_16x16x32_bf16 v[152:155], a[116:119], a[172:175], v[152:155]\n"
"s_mov_b32 s47, 0\n"
"ds_read_b64_tr_b16 a[230:231], v244 offset:26112\n"
"v_bfe_u32 v249, v242, 4, 2\n"
"v_mfma_f32_16x16x32_bf16 v[148:151], a[104:107], a[168:171], v[148:151]\n"
"ds_read_b64_tr_b16 a[232:233], v245 offset:17408\n"
"v_mul_u32_u24 v249, 20, v249\n"
"v_mfma_f32_16x16x32_bf16 v[148:151], a[108:111], a[172:175], v[148:151]\n"
"ds_read_b64_tr_b16 a[234:235], v245 offset:17920\n"
"v_xor_b32 v244, s74, v244\n"
"v_mfma_f32_16x16x32_bf16 v[144:147], a[96:99], a[168:171], v[144:147]\n"
"s_mov_b32 m0, s44\n"
"ds_read_b64_tr_b16 a[236:237], v245 offset:25600\n"
"v_xor_b32 v250, s74, v250\n"
"v_mfma_f32_16x16x32_bf16 v[144:147], a[100:103], a[172:175], v[144:147]\n"
"buffer_load_dwordx4 v240, s[16:19], s56 offen nt lds\n"
"v_mfma_f32_16x16x32_bf16 v[176:179], a[96:99], a[176:179], v[176:179]\n"
"s_waitcnt lgkmcnt(5)\n"
"ds_read_b64_tr_b16 a[238:239], v245 offset:26112\n"
"v_xor_b32 v251, s74, v251\n"
"v_mfma_f32_16x16x32_bf16 v[176:179], a[100:103], a[180:183], v[176:179]\n"
"ds_read_b64_tr_b16 a[240:241], v246 offset:17408\n"
"v_xor_b32 v245, s74, v245\n"
"v_mfma_f32_16x16x32_bf16 v[180:183], a[104:107], a[176:179], v[180:183]\n"
"ds_read_b64_tr_b16 a[242:243], v246 offset:17920\n"
"v_xor_b32 v252, s74, v252\n"
"v_mfma_f32_16x16x32_bf16 v[180:183], a[108:111], a[180:183], v[180:183]\n"
"ds_read_b64_tr_b16 a[244:245], v246 offset:25600\n"
"v_xor_b32 v253, s74, v253\n"
"v_mfma_f32_16x16x32_bf16 v[184:187], a[112:115], a[176:179], v[184:187]\n"
"ds_read_b64_tr_b16 a[246:247], v246 offset:26112\n"
"v_xor_b32 v246, s74, v246\n"
"v_mfma_f32_16x16x32_bf16 v[184:187], a[116:119], a[180:183], v[184:187]\n"
"ds_read_b64_tr_b16 a[248:249], v247 offset:17408\n"
"v_mfma_f32_16x16x32_bf16 v[188:191], a[120:123], a[176:179], v[188:191]\n"
"s_add_u32 s40, s56, s52\n"
"ds_read_b64_tr_b16 a[250:251], v247 offset:17920\n"
"v_mfma_f32_16x16x32_bf16 v[188:191], a[124:127], a[180:183], v[188:191]\n"
"buffer_load_dwordx4 v240, s[16:19], s40 offen offset:1024 nt lds\n"
"v_mfma_f32_16x16x32_bf16 v[200:203], a[144:147], a[176:179], v[200:203]\n"
"ds_read_b64_tr_b16 a[252:253], v247 offset:25600\n"
"v_mfma_f32_16x16x32_bf16 v[200:203], a[148:151], a[180:183], v[200:203]\n"
"ds_read_b64_tr_b16 a[254:255], v247 offset:26112\n"
"v_xor_b32 v247, s74, v247\n"
"v_mfma_f32_16x16x32_bf16 v[204:207], a[152:155], a[176:179], v[204:207]\n"
"v_mfma_f32_16x16x32_bf16 v[204:207], a[156:159], a[180:183], v[204:207]\n"
"v_mfma_f32_16x16x32_bf16 v[192:195], a[128:131], a[176:179], v[192:195]\n"
"s_add_u32 s40, s56, s53\n"
"v_mfma_f32_16x16x32_bf16 v[192:195], a[132:135], a[180:183], v[192:195]\n"
"buffer_load_dwordx4 v240, s[16:19], s40 offen offset:2048 nt lds\n"
"v_mfma_f32_16x16x32_bf16 v[196:199], a[136:139], a[176:179], v[196:199]\n"
"v_mfma_f32_16x16x32_bf16 v[196:199], a[140:143], a[180:183], v[196:199]\n"
"v_mfma_f32_16x16x32_bf16 v[220:223], a[136:139], a[184:187], v[220:223]\n"
"v_mfma_f32_16x16x32_bf16 v[220:223], a[140:143], a[188:191], v[220:223]\n"
"v_mfma_f32_16x16x32_bf16 v[216:219], a[128:131], a[184:187], v[216:219]\n"
"s_add_u32 s40, s56, s54\n"
"v_mfma_f32_16x16x32_bf16 v[216:219], a[132:135], a[188:191], v[216:219]\n"
"buffer_load_dwordx4 v240, s[16:19], s40 offen offset:3072 nt lds\n"
"v_mfma_f32_16x16x32_bf16 v[212:215], a[104:107], a[184:187], v[212:215]\n"
"v_mfma_f32_16x16x32_bf16 v[212:215], a[108:111], a[188:191], v[212:215]\n"
"v_mfma_f32_16x16x32_bf16 v[208:211], a[96:99], a[184:187], v[208:211]\n"
"v_mfma_f32_16x16x32_bf16 v[208:211], a[100:103], a[188:191], v[208:211]\n"
"v_mfma_f32_16x16x32_bf16 v[0:3], a[192:195], a[160:163], v[0:3]\n"
"v_mfma_f32_16x16x32_bf16 v[0:3], a[196:199], a[164:167], v[0:3]\n"
"v_mfma_f32_16x16x32_bf16 v[4:7], a[200:203], a[160:163], v[4:7]\n"
"s_waitcnt vmcnt(8) lgkmcnt(0)\n"
"v_mfma_f32_16x16x32_bf16 v[4:7], a[204:207], a[164:167], v[4:7]\n"
"s_barrier\n"
"v_mfma_f32_16x16x32_bf16 v[8:11], a[208:211], a[160:163], v[8:11]\n"
"s_add_u32 m0, s44, 65536\n"
"ds_read_b64_tr_b16 a[96:97], v244 offset:0\n"
"v_mfma_f32_16x16x32_bf16 v[8:11], a[212:215], a[164:167], v[8:11]\n"
"buffer_load_dwordx4 v240, s[20:23], s56 offen nt lds\n"
"v_mfma_f32_16x16x32_bf16 v[12:15], a[216:219], a[160:163], v[12:15]\n"
"ds_read_b64_tr_b16 a[98:99], v244 offset:512\n"
"v_mfma_f32_16x16x32_bf16 v[12:15], a[220:223], a[164:167], v[12:15]\n"
"ds_read_b64_tr_b16 a[100:101], v244 offset:8192\n"
"v_mfma_f32_16x16x32_bf16 v[16:19], a[224:227], a[160:163], v[16:19]\n"
"ds_read_b64_tr_b16 a[102:103], v244 offset:8704\n"
"v_mfma_f32_16x16x32_bf16 v[16:19], a[228:231], a[164:167], v[16:19]\n"
"ds_read_b64_tr_b16 a[104:105], v245 offset:0\n"
"v_mfma_f32_16x16x32_bf16 v[20:23], a[232:235], a[160:163], v[20:23]\n"
"s_add_u32 s40, s56, s52\n"
"ds_read_b64_tr_b16 a[106:107], v245 offset:512\n"
"v_mfma_f32_16x16x32_bf16 v[20:23], a[236:239], a[164:167], v[20:23]\n"
"buffer_load_dwordx4 v240, s[20:23], s40 offen offset:1024 nt lds\n"
"v_mfma_f32_16x16x32_bf16 v[24:27], a[240:243], a[160:163], v[24:27]\n"
"ds_read_b64_tr_b16 a[108:109], v245 offset:8192\n"
"v_mfma_f32_16x16x32_bf16 v[24:27], a[244:247], a[164:167], v[24:27]\n"
"ds_read_b64_tr_b16 a[110:111], v245 offset:8704\n"
"v_mfma_f32_16x16x32_bf16 v[28:31], a[248:251], a[160:163], v[28:31]\n"
"ds_read_b128 a[160:163], v250 offset:0\n"
"v_mfma_f32_16x16x32_bf16 v[28:31], a[252:255], a[164:167], v[28:31]\n"
"ds_read_b128 a[164:167], v251 offset:0\n"
"v_mfma_f32_16x16x32_bf16 v[60:63], a[248:251], a[168:171], v[60:63]\n"
"ds_read_b64_tr_b16 a[112:113], v246 offset:0\n"
"v_mfma_f32_16x16x32_bf16 v[60:63], a[252:255], a[172:175], v[60:63]\n"
"ds_read_b64_tr_b16 a[114:115], v246 offset:512\n"
"v_mfma_f32_16x16x32_bf16 v[56:59], a[240:243], a[168:171], v[56:59]\n"
"s_add_u32 s40, s56, s53\n"
"ds_read_b64_tr_b16 a[116:117], v246 offset:8192\n"
"v_mfma_f32_16x16x32_bf16 v[56:59], a[244:247], a[172:175], v[56:59]\n"
"buffer_load_dwordx4 v240, s[20:23], s40 offen offset:2048 nt lds\n"
"v_mfma_f32_16x16x32_bf16 v[52:55], a[232:235], a[168:171], v[52:55]\n"
"ds_read_b64_tr_b16 a[118:119], v246 offset:8704\n"
"v_mfma_f32_16x16x32_bf16 v[52:55], a[236:239], a[172:175], v[52:55]\n"
"ds_read_b64_tr_b16 a[120:121], v247 offset:0\n"
"v_mfma_f32_16x16x32_bf16 v[48:51], a[224:227], a[168:171], v[48:51]\n"
"ds_read_b64_tr_b16 a[122:123], v247 offset:512\n"
"v_mfma_f32_16x16x32_bf16 v[48:51], a[228:231], a[172:175], v[48:51]\n"
"ds_read_b64_tr_b16 a[124:125], v247 offset:8192\n"
"v_mfma_f32_16x16x32_bf16 v[44:47], a[216:219], a[168:171], v[44:47]\n"
"s_add_u32 s40, s56, s54\n"
"ds_read_b64_tr_b16 a[126:127], v247 offset:8704\n"
"v_mfma_f32_16x16x32_bf16 v[44:47], a[220:223], a[172:175], v[44:47]\n"
"buffer_load_dwordx4 v240, s[20:23], s40 offen offset:3072 nt lds\n"
"v_mfma_f32_16x16x32_bf16 v[40:43], a[208:211], a[168:171], v[40:43]\n"
"s_add_u32 s55, s55, s59\n"
"ds_read_b64_tr_b16 a[128:129], v244 offset:1024\n"
"v_mfma_f32_16x16x32_bf16 v[40:43], a[212:215], a[172:175], v[40:43]\n"
"s_add_u32 s41, s55, s57\n"
"ds_read_b64_tr_b16 a[130:131], v244 offset:1536\n"
"v_mfma_f32_16x16x32_bf16 v[36:39], a[200:203], a[168:171], v[36:39]\n"
"s_and_b32 s41, s41, s58\n"
"ds_read_b64_tr_b16 a[132:133], v244 offset:9216\n"
"v_mfma_f32_16x16x32_bf16 v[36:39], a[204:207], a[172:175], v[36:39]\n"
"s_andn2_b32 s56, s55, s58\n"
"ds_read_b64_tr_b16 a[134:135], v244 offset:9728\n"
"v_mfma_f32_16x16x32_bf16 v[32:35], a[192:195], a[168:171], v[32:35]\n"
"s_or_b32 s56, s56, s41\n"
"ds_read_b64_tr_b16 a[136:137], v245 offset:1024\n"
"v_mfma_f32_16x16x32_bf16 v[32:35], a[196:199], a[172:175], v[32:35]\n"
"ds_read_b64_tr_b16 a[138:139], v245 offset:1536\n"
"v_mfma_f32_16x16x32_bf16 v[64:67], a[192:195], a[176:179], v[64:67]\n"
"s_add_u32 m0, s44, 16384\n"
"ds_read_b64_tr_b16 a[140:141], v245 offset:9216\n"
"v_mfma_f32_16x16x32_bf16 v[64:67], a[196:199], a[180:183], v[64:67]\n"
"buffer_load_dwordx4 v240, s[16:19], s56 offen nt lds\n"
"v_mfma_f32_16x16x32_bf16 v[68:71], a[200:203], a[176:179], v[68:71]\n"
"ds_read_b64_tr_b16 a[142:143], v245 offset:9728\n"
"v_mfma_f32_16x16x32_bf16 v[68:71], a[204:207], a[180:183], v[68:71]\n"
"ds_read_b64_tr_b16 a[144:145], v246 offset:1024\n"
"v_mfma_f32_16x16x32_bf16 v[72:75], a[208:211], a[176:179], v[72:75]\n"
"ds_read_b64_tr_b16 a[146:147], v246 offset:1536\n"
"v_mfma_f32_16x16x32_bf16 v[72:75], a[212:215], a[180:183], v[72:75]\n"
"ds_read_b64_tr_b16 a[148:149], v246 offset:9216\n"
"v_mfma_f32_16x16x32_bf16 v[76:79], a[216:219], a[176:179], v[76:79]\n"
"s_add_u32 s40, s56, s52\n"
"ds_read_b64_tr_b16 a[150:151], v246 offset:9728\n"
"v_mfma_f32_16x16x32_bf16 v[76:79], a[220:223], a[180:183], v[76:79]\n"
"buffer_load_dwordx4 v240, s[16:19], s40 offen offset:1024 nt lds\n"
"v_mfma_f32_16x16x32_bf16 v[88:91], a[240:243], a[176:179], v[88:91]\n"
"ds_read_b64_tr_b16 a[152:153], v247 offset:1024\n"
"v_mfma_f32_16x16x32_bf16 v[88:91], a[244:247], a[180:183], v[88:91]\n"
"ds_read_b64_tr_b16 a[154:155], v247 offset:1536\n"
"v_mfma_f32_16x16x32_bf16 v[92:95], a[248:251], a[176:179], v[92:95]\n"
"ds_read_b64_tr_b16 a[156:157], v247 offset:9216\n"
"v_mfma_f32_16x16x32_bf16 v[92:95], a[252:255], a[180:183], v[92:95]\n"
"ds_read_b64_tr_b16 a[158:159], v247 offset:9728\n"
"v_mfma_f32_16x16x32_bf16 v[80:83], a[224:227], a[176:179], v[80:83]\n"
"s_add_u32 s40, s56, s53\n"
"ds_read_b128 a[168:171], v250 offset:2048\n"
"v_mfma_f32_16x16x32_bf16 v[80:83], a[228:231], a[180:183], v[80:83]\n"
"buffer_load_dwordx4 v240, s[16:19], s40 offen offset:2048 nt lds\n"
"v_mfma_f32_16x16x32_bf16 v[84:87], a[232:235], a[176:179], v[84:87]\n"
"ds_read_b128 a[172:175], v251 offset:2048\n"
"v_mfma_f32_16x16x32_bf16 v[84:87], a[236:239], a[180:183], v[84:87]\n"
"ds_read_b128 a[176:179], v250 offset:4096\n"
"v_mfma_f32_16x16x32_bf16 v[108:111], a[232:235], a[184:187], v[108:111]\n"
"ds_read_b128 a[180:183], v251 offset:4096\n"
"v_mfma_f32_16x16x32_bf16 v[108:111], a[236:239], a[188:191], v[108:111]\n"
"v_mfma_f32_16x16x32_bf16 v[104:107], a[224:227], a[184:187], v[104:107]\n"
"s_add_u32 s40, s56, s54\n"
"v_mfma_f32_16x16x32_bf16 v[104:107], a[228:231], a[188:191], v[104:107]\n"
"buffer_load_dwordx4 v240, s[16:19], s40 offen offset:3072 nt lds\n"
"v_mfma_f32_16x16x32_bf16 v[100:103], a[200:203], a[184:187], v[100:103]\n"
"v_mfma_f32_16x16x32_bf16 v[100:103], a[204:207], a[188:191], v[100:103]\n"
"v_mfma_f32_16x16x32_bf16 v[96:99], a[192:195], a[184:187], v[96:99]\n"
"ds_read_b128 a[184:187], v252 offset:0\n"
"v_mfma_f32_16x16x32_bf16 v[96:99], a[196:199], a[188:191], v[96:99]\n"
"s_waitcnt lgkmcnt(2)\n"
"ds_read_b128 a[188:191], v253 offset:0\n"
"v_mfma_f32_16x16x32_bf16 v[112:115], a[96:99], a[160:163], v[112:115]\n"
"v_mfma_f32_16x16x32_bf16 v[112:115], a[100:103], a[164:167], v[112:115]\n"
"v_mfma_f32_16x16x32_bf16 v[116:119], a[104:107], a[160:163], v[116:119]\n"
"s_waitcnt vmcnt(12)\n"
"v_mfma_f32_16x16x32_bf16 v[116:119], a[108:111], a[164:167], v[116:119]\n"
"s_barrier\n"
"v_mfma_f32_16x16x32_bf16 v[120:123], a[112:115], a[160:163], v[120:123]\n"
"s_add_u32 m0, s44, 81920\n"
"ds_read_b64_tr_b16 a[232:233], v245 offset:17408\n"
"v_mfma_f32_16x16x32_bf16 v[120:123], a[116:119], a[164:167], v[120:123]\n"
"buffer_load_dwordx4 v240, s[20:23], s56 offen nt lds\n"
"v_mfma_f32_16x16x32_bf16 v[124:127], a[120:123], a[160:163], v[124:127]\n"
"ds_read_b64_tr_b16 a[234:235], v245 offset:17920\n"
"v_mfma_f32_16x16x32_bf16 v[124:127], a[124:127], a[164:167], v[124:127]\n"
"ds_read_b64_tr_b16 a[236:237], v245 offset:25600\n"
"v_mfma_f32_16x16x32_bf16 v[128:131], a[128:131], a[160:163], v[128:131]\n"
"ds_read_b64_tr_b16 a[238:239], v245 offset:26112\n"
"v_mfma_f32_16x16x32_bf16 v[128:131], a[132:135], a[164:167], v[128:131]\n"
"ds_read_b64_tr_b16 a[224:225], v244 offset:17408\n"
"v_mfma_f32_16x16x32_bf16 v[132:135], a[136:139], a[160:163], v[132:135]\n"
"ds_read_b64_tr_b16 a[226:227], v244 offset:17920\n"
"v_mfma_f32_16x16x32_bf16 v[132:135], a[140:143], a[164:167], v[132:135]\n"
"ds_read_b64_tr_b16 a[192:193], v244 offset:16384\n"
"v_mfma_f32_16x16x32_bf16 v[136:139], a[144:147], a[160:163], v[136:139]\n"
"ds_read_b64_tr_b16 a[194:195], v244 offset:16896\n"
"v_mfma_f32_16x16x32_bf16 v[136:139], a[148:151], a[164:167], v[136:139]\n"
"ds_read_b64_tr_b16 a[196:197], v244 offset:24576\n"
"v_mfma_f32_16x16x32_bf16 v[140:143], a[152:155], a[160:163], v[140:143]\n"
"ds_read_b64_tr_b16 a[198:199], v244 offset:25088\n"
"v_mfma_f32_16x16x32_bf16 v[140:143], a[156:159], a[164:167], v[140:143]\n"
"s_waitcnt lgkmcnt(7)\n"
"ds_read_b64_tr_b16 a[200:201], v245 offset:16384\n"
"v_mfma_f32_16x16x32_bf16 v[220:223], a[136:139], a[184:187], v[220:223]\n"
"ds_read_b64_tr_b16 a[202:203], v245 offset:16896\n"
"v_mfma_f32_16x16x32_bf16 v[220:223], a[140:143], a[188:191], v[220:223]\n"
"ds_read_b64_tr_b16 a[204:205], v245 offset:24576\n"
"v_mfma_f32_16x16x32_bf16 v[216:219], a[128:131], a[184:187], v[216:219]\n"
"s_add_u32 s40, s56, s52\n"
"ds_read_b64_tr_b16 a[206:207], v245 offset:25088\n"
"v_mfma_f32_16x16x32_bf16 v[216:219], a[132:135], a[188:191], v[216:219]\n"
"buffer_load_dwordx4 v240, s[20:23], s40 offen offset:1024 nt lds\n"
"v_mfma_f32_16x16x32_bf16 v[212:215], a[104:107], a[184:187], v[212:215]\n"
"ds_read_b64_tr_b16 a[208:209], v246 offset:16384\n"
"v_mfma_f32_16x16x32_bf16 v[212:215], a[108:111], a[188:191], v[212:215]\n"
"v_pk_fma_f32 v[112:113], v[112:113], s[70:71], s[70:71] op_sel:[0,0,1] op_sel_hi:[1,0,1]\nv_pk_fma_f32 v[114:115], v[114:115], s[70:71], s[70:71] op_sel:[0,0,1] op_sel_hi:[1,0,1]\nv_pk_fma_f32 v[116:117], v[116:117], s[70:71], s[70:71] op_sel:[0,0,1] op_sel_hi:[1,0,1]\nv_pk_fma_f32 v[118:119], v[118:119], s[70:71], s[70:71] op_sel:[0,0,1] op_sel_hi:[1,0,1]\nv_pk_fma_f32 v[120:121], v[120:121], s[70:71], s[70:71] op_sel:[0,0,1] op_sel_hi:[1,0,1]\nv_pk_fma_f32 v[122:123], v[122:123], s[70:71], s[70:71] op_sel:[0,0,1] op_sel_hi:[1,0,1]\nv_pk_fma_f32 v[124:125], v[124:125], s[70:71], s[70:71] op_sel:[0,0,1] op_sel_hi:[1,0,1]\nv_pk_fma_f32 v[126:127], v[126:127], s[70:71], s[70:71] op_sel:[0,0,1] op_sel_hi:[1,0,1]\nv_pk_fma_f32 v[128:129], v[128:129], s[70:71], s[70:71] op_sel:[0,0,1] op_sel_hi:[1,0,1]\nv_pk_fma_f32 v[130:131], v[130:131], s[70:71], s[70:71] op_sel:[0,0,1] op_sel_hi:[1,0,1]\nv_pk_fma_f32 v[132:133], v[132:133], s[70:71], s[70:71] op_sel:[0,0,1] op_sel_hi:[1,0,1]\nv_pk_fma_f32 v[134:135], v[134:135], s[70:71], s[70:71] op_sel:[0,0,1] op_sel_hi:[1,0,1]\nv_pk_fma_f32 v[136:137], v[136:137], s[70:71], s[70:71] op_sel:[0,0,1] op_sel_hi:[1,0,1]\nv_pk_fma_f32 v[138:139], v[138:139], s[70:71], s[70:71] op_sel:[0,0,1] op_sel_hi:[1,0,1]\nv_pk_fma_f32 v[140:141], v[140:141], s[70:71], s[70:71] op_sel:[0,0,1] op_sel_hi:[1,0,1]\nv_pk_fma_f32 v[142:143], v[142:143], s[70:71], s[70:71] op_sel:[0,0,1] op_sel_hi:[1,0,1]\n"
"v_mfma_f32_16x16x32_bf16 v[208:211], a[96:99], a[184:187], v[208:211]\n"
"ds_read_b64_tr_b16 a[210:211], v246 offset:16896\n"
"v_cvt_pk_bf16_f32 v128, v128, v129\n"
"v_mfma_f32_16x16x32_bf16 v[208:211], a[100:103], a[188:191], v[208:211]\n"
"ds_read_b64_tr_b16 a[212:213], v246 offset:24576\n"
"v_cvt_pk_bf16_f32 v129, v130, v131\n"
"v_mfma_f32_16x16x32_bf16 v[172:175], a[152:155], a[168:171], v[172:175]\n"
"ds_read_b64_tr_b16 a[214:215], v246 offset:25088\n"
"v_cvt_pk_bf16_f32 v130, v132, v133\n"
"v_mfma_f32_16x16x32_bf16 v[172:175], a[156:159], a[172:175], v[172:175]\n"
"ds_read_b64_tr_b16 a[216:217], v247 offset:16384\n"
"v_cvt_pk_bf16_f32 v131, v134, v135\n"
"v_mfma_f32_16x16x32_bf16 v[168:171], a[144:147], a[168:171], v[168:171]\n"
"ds_read_b64_tr_b16 a[218:219], v247 offset:16896\n"
"v_cvt_pk_bf16_f32 v136, v136, v137\n"
"v_mfma_f32_16x16x32_bf16 v[168:171], a[148:151], a[172:175], v[168:171]\n"
"ds_read_b64_tr_b16 a[220:221], v247 offset:24576\n"
"v_cvt_pk_bf16_f32 v137, v138, v139\n"
"v_mfma_f32_16x16x32_bf16 v[164:167], a[136:139], a[168:171], v[164:167]\n"
"ds_read_b64_tr_b16 a[222:223], v247 offset:25088\n"
"v_cvt_pk_bf16_f32 v138, v140, v141\n"
"v_mfma_f32_16x16x32_bf16 v[164:167], a[140:143], a[172:175], v[164:167]\n"
"ds_read_b64_tr_b16 a[228:229], v244 offset:25600\n"
"v_cvt_pk_bf16_f32 v139, v142, v143\n"
"v_mfma_f32_16x16x32_bf16 v[160:163], a[128:131], a[168:171], v[160:163]\n"
"s_add_u32 s40, s56, s53\n"
"ds_read_b64_tr_b16 a[230:231], v244 offset:26112\n"
"v_cvt_pk_bf16_f32 v120, v120, v121\n"
"v_mfma_f32_16x16x32_bf16 v[160:163], a[132:135], a[172:175], v[160:163]\n"
"buffer_load_dwordx4 v240, s[20:23], s40 offen offset:2048 nt lds\n"
"v_mfma_f32_16x16x32_bf16 v[156:159], a[120:123], a[168:171], v[156:159]\n"
"ds_read_b64_tr_b16 a[240:241], v246 offset:17408\n"
"v_cvt_pk_bf16_f32 v121, v122, v123\n"
"v_mfma_f32_16x16x32_bf16 v[156:159], a[124:127], a[172:175], v[156:159]\n"
"ds_read_b64_tr_b16 a[242:243], v246 offset:17920\n"
"v_cvt_pk_bf16_f32 v122, v124, v125\n"
"v_mfma_f32_16x16x32_bf16 v[152:155], a[112:115], a[168:171], v[152:155]\n"
"ds_read_b64_tr_b16 a[244:245], v246 offset:25600\n"
"v_cvt_pk_bf16_f32 v123, v126, v127\n"
"v_mfma_f32_16x16x32_bf16 v[152:155], a[116:119], a[172:175], v[152:155]\n"
"ds_read_b64_tr_b16 a[246:247], v246 offset:26112\n"
"v_cvt_pk_bf16_f32 v112, v112, v113\n"
"v_mfma_f32_16x16x32_bf16 v[148:151], a[104:107], a[168:171], v[148:151]\n"
"ds_read_b64_tr_b16 a[248:249], v247 offset:17408\n"
"v_cvt_pk_bf16_f32 v113, v114, v115\n"
"v_mfma_f32_16x16x32_bf16 v[148:151], a[108:111], a[172:175], v[148:151]\n"
"ds_read_b64_tr_b16 a[250:251], v247 offset:17920\n"
"v_cvt_pk_bf16_f32 v114, v116, v117\n"
"v_mfma_f32_16x16x32_bf16 v[144:147], a[96:99], a[168:171], v[144:147]\n"
"ds_read_b64_tr_b16 a[252:253], v247 offset:25600\n"
"v_cvt_pk_bf16_f32 v115, v118, v119\n"
"v_mfma_f32_16x16x32_bf16 v[144:147], a[100:103], a[172:175], v[144:147]\n"
"v_pk_fma_f32 v[208:209], v[208:209], s[70:71], s[70:71] op_sel:[0,0,1] op_sel_hi:[1,0,1]\nv_pk_fma_f32 v[210:211], v[210:211], s[70:71], s[70:71] op_sel:[0,0,1] op_sel_hi:[1,0,1]\nv_pk_fma_f32 v[212:213], v[212:213], s[70:71], s[70:71] op_sel:[0,0,1] op_sel_hi:[1,0,1]\nv_pk_fma_f32 v[214:215], v[214:215], s[70:71], s[70:71] op_sel:[0,0,1] op_sel_hi:[1,0,1]\nv_pk_fma_f32 v[216:217], v[216:217], s[70:71], s[70:71] op_sel:[0,0,1] op_sel_hi:[1,0,1]\nv_pk_fma_f32 v[218:219], v[218:219], s[70:71], s[70:71] op_sel:[0,0,1] op_sel_hi:[1,0,1]\nv_pk_fma_f32 v[220:221], v[220:221], s[70:71], s[70:71] op_sel:[0,0,1] op_sel_hi:[1,0,1]\nv_pk_fma_f32 v[222:223], v[222:223], s[70:71], s[70:71] op_sel:[0,0,1] op_sel_hi:[1,0,1]\n"
"v_mfma_f32_16x16x32_bf16 v[176:179], a[96:99], a[176:179], v[176:179]\n"
"ds_read_b64_tr_b16 a[254:255], v247 offset:26112\n"
"v_cvt_pk_bf16_f32 v208, v208, v209\n"
"v_mfma_f32_16x16x32_bf16 v[176:179], a[100:103], a[180:183], v[176:179]\n"
"v_permlane16_swap_b32 v128, v130\n"
"v_mfma_f32_16x16x32_bf16 v[180:183], a[104:107], a[176:179], v[180:183]\n"
"v_permlane16_swap_b32 v129, v131\n"
"v_mfma_f32_16x16x32_bf16 v[180:183], a[108:111], a[180:183], v[180:183]\n"
"v_permlane16_swap_b32 v136, v138\n"
"v_mfma_f32_16x16x32_bf16 v[184:187], a[112:115], a[176:179], v[184:187]\n"
"s_add_u32 s40, s56, s54\n"
"v_permlane16_swap_b32 v137, v139\n"
"v_mfma_f32_16x16x32_bf16 v[184:187], a[116:119], a[180:183], v[184:187]\n"
"buffer_load_dwordx4 v240, s[20:23], s40 offen offset:3072 nt lds\n"
"v_mfma_f32_16x16x32_bf16 v[188:191], a[120:123], a[176:179], v[188:191]\n"
"s_add_u32 s55, s55, s59\n"
"v_pk_fma_f32 v[144:145], v[144:145], s[70:71], s[70:71] op_sel:[0,0,1] op_sel_hi:[1,0,1]\nv_pk_fma_f32 v[146:147], v[146:147], s[70:71], s[70:71] op_sel:[0,0,1] op_sel_hi:[1,0,1]\nv_pk_fma_f32 v[148:149], v[148:149], s[70:71], s[70:71] op_sel:[0,0,1] op_sel_hi:[1,0,1]\nv_pk_fma_f32 v[150:151], v[150:151], s[70:71], s[70:71] op_sel:[0,0,1] op_sel_hi:[1,0,1]\nv_pk_fma_f32 v[152:153], v[152:153], s[70:71], s[70:71] op_sel:[0,0,1] op_sel_hi:[1,0,1]\nv_pk_fma_f32 v[154:155], v[154:155], s[70:71], s[70:71] op_sel:[0,0,1] op_sel_hi:[1,0,1]\nv_pk_fma_f32 v[156:157], v[156:157], s[70:71], s[70:71] op_sel:[0,0,1] op_sel_hi:[1,0,1]\nv_pk_fma_f32 v[158:159], v[158:159], s[70:71], s[70:71] op_sel:[0,0,1] op_sel_hi:[1,0,1]\nv_pk_fma_f32 v[160:161], v[160:161], s[70:71], s[70:71] op_sel:[0,0,1] op_sel_hi:[1,0,1]\nv_pk_fma_f32 v[162:163], v[162:163], s[70:71], s[70:71] op_sel:[0,0,1] op_sel_hi:[1,0,1]\nv_pk_fma_f32 v[164:165], v[164:165], s[70:71], s[70:71] op_sel:[0,0,1] op_sel_hi:[1,0,1]\nv_pk_fma_f32 v[166:167], v[166:167], s[70:71], s[70:71] op_sel:[0,0,1] op_sel_hi:[1,0,1]\nv_pk_fma_f32 v[168:169], v[168:169], s[70:71], s[70:71] op_sel:[0,0,1] op_sel_hi:[1,0,1]\nv_pk_fma_f32 v[170:171], v[170:171], s[70:71], s[70:71] op_sel:[0,0,1] op_sel_hi:[1,0,1]\nv_pk_fma_f32 v[172:173], v[172:173], s[70:71], s[70:71] op_sel:[0,0,1] op_sel_hi:[1,0,1]\nv_pk_fma_f32 v[174:175], v[174:175], s[70:71], s[70:71] op_sel:[0,0,1] op_sel_hi:[1,0,1]\n"
"v_mfma_f32_16x16x32_bf16 v[188:191], a[124:127], a[180:183], v[188:191]\n"
"s_add_u32 s41, s55, s57\n"
"v_permlane16_swap_b32 v112, v114\n"
"v_mfma_f32_16x16x32_bf16 v[200:203], a[144:147], a[176:179], v[200:203]\n"
"s_and_b32 s41, s41, s58\n"
"v_permlane16_swap_b32 v113, v115\n"
"v_mfma_f32_16x16x32_bf16 v[200:203], a[148:151], a[180:183], v[200:203]\n"
"s_andn2_b32 s56, s55, s58\n"
"v_permlane16_swap_b32 v120, v122\n"
"v_mfma_f32_16x16x32_bf16 v[204:207], a[152:155], a[176:179], v[204:207]\n"
"s_or_b32 s56, s56, s41\n"
"v_permlane16_swap_b32 v121, v123\n"
"v_mfma_f32_16x16x32_bf16 v[204:207], a[156:159], a[180:183], v[204:207]\n"
"v_cvt_pk_bf16_f32 v209, v210, v211\n"
"v_cvt_pk_bf16_f32 v210, v212, v213\n"
"v_mfma_f32_16x16x32_bf16 v[192:195], a[128:131], a[176:179], v[192:195]\n"
"s_nop 0\n"
"v_permlane16_swap_b32 v208, v210\n"
"v_mfma_f32_16x16x32_bf16 v[192:195], a[132:135], a[180:183], v[192:195]\n"
"v_cvt_pk_bf16_f32 v211, v214, v215\n"
"v_cvt_pk_bf16_f32 v216, v216, v217\n"
"v_mfma_f32_16x16x32_bf16 v[196:199], a[136:139], a[176:179], v[196:199]\n"
"v_permlane16_swap_b32 v209, v211\n"
"v_mfma_f32_16x16x32_bf16 v[196:199], a[140:143], a[180:183], v[196:199]\n"
"ds_write_b128 v243, v[208:211] offset:8192\n"
"v_mfma_f32_16x16x32_bf16 v[108:111], a[232:235], a[184:187], v[108:111]\n"
"s_waitcnt lgkmcnt(1)\n"
"s_add_u32 m0, s44, 32768\n"
"v_cvt_pk_bf16_f32 v217, v218, v219\n"
"v_cvt_pk_bf16_f32 v218, v220, v221\n"
"v_mfma_f32_16x16x32_bf16 v[108:111], a[236:239], a[188:191], v[108:111]\n"
"buffer_load_dwordx4 v240, s[16:19], s56 offen nt lds\n"
"v_mfma_f32_16x16x32_bf16 v[104:107], a[224:227], a[184:187], v[104:107]\n"
"s_waitcnt vmcnt(13)\n"
"v_permlane16_swap_b32 v216, v218\n"
"v_mfma_f32_16x16x32_bf16 v[104:107], a[228:231], a[188:191], v[104:107]\n"
"s_barrier\n"
"v_mfma_f32_16x16x32_bf16 v[100:103], a[200:203], a[184:187], v[100:103]\n"
"v_cvt_pk_bf16_f32 v219, v222, v223\n"
"v_cvt_pk_bf16_f32 v144, v144, v145\n"
"v_mfma_f32_16x16x32_bf16 v[100:103], a[204:207], a[188:191], v[100:103]\n"
"s_mov_b32 s74, 0x41400000\n"
"v_permlane16_swap_b32 v217, v219\n"
"v_mfma_f32_16x16x32_bf16 v[96:99], a[192:195], a[184:187], v[96:99]\n"
"ds_write_b128 v243, v[216:219] offset:10240\n"
"v_mfma_f32_16x16x32_bf16 v[96:99], a[196:199], a[188:191], v[96:99]\n"
"v_pk_fma_f32 v[176:177], v[176:177], s[70:71], s[70:71] op_sel:[0,0,1] op_sel_hi:[1,0,1]\nv_pk_fma_f32 v[178:179], v[178:179], s[70:71], s[70:71] op_sel:[0,0,1] op_sel_hi:[1,0,1]\nv_pk_fma_f32 v[180:181], v[180:181], s[70:71], s[70:71] op_sel:[0,0,1] op_sel_hi:[1,0,1]\nv_pk_fma_f32 v[182:183], v[182:183], s[70:71], s[70:71] op_sel:[0,0,1] op_sel_hi:[1,0,1]\nv_pk_fma_f32 v[184:185], v[184:185], s[70:71], s[70:71] op_sel:[0,0,1] op_sel_hi:[1,0,1]\nv_pk_fma_f32 v[186:187], v[186:187], s[70:71], s[70:71] op_sel:[0,0,1] op_sel_hi:[1,0,1]\nv_pk_fma_f32 v[188:189], v[188:189], s[70:71], s[70:71] op_sel:[0,0,1] op_sel_hi:[1,0,1]\nv_pk_fma_f32 v[190:191], v[190:191], s[70:71], s[70:71] op_sel:[0,0,1] op_sel_hi:[1,0,1]\nv_pk_fma_f32 v[192:193], v[192:193], s[70:71], s[70:71] op_sel:[0,0,1] op_sel_hi:[1,0,1]\nv_pk_fma_f32 v[194:195], v[194:195], s[70:71], s[70:71] op_sel:[0,0,1] op_sel_hi:[1,0,1]\nv_pk_fma_f32 v[196:197], v[196:197], s[70:71], s[70:71] op_sel:[0,0,1] op_sel_hi:[1,0,1]\nv_pk_fma_f32 v[198:199], v[198:199], s[70:71], s[70:71] op_sel:[0,0,1] op_sel_hi:[1,0,1]\nv_pk_fma_f32 v[200:201], v[200:201], s[70:71], s[70:71] op_sel:[0,0,1] op_sel_hi:[1,0,1]\nv_pk_fma_f32 v[202:203], v[202:203], s[70:71], s[70:71] op_sel:[0,0,1] op_sel_hi:[1,0,1]\nv_pk_fma_f32 v[204:205], v[204:205], s[70:71], s[70:71] op_sel:[0,0,1] op_sel_hi:[1,0,1]\nv_pk_fma_f32 v[206:207], v[206:207], s[70:71], s[70:71] op_sel:[0,0,1] op_sel_hi:[1,0,1]\n"
"v_mfma_f32_16x16x32_bf16 v[0:3], a[192:195], a[160:163], v[0:3]\n"
"v_cvt_pk_bf16_f32 v145, v146, v147\n"
"v_cvt_pk_bf16_f32 v146, v148, v149\n"
"v_mfma_f32_16x16x32_bf16 v[0:3], a[196:199], a[164:167], v[0:3]\n"
"s_nop 0\n"
"v_permlane16_swap_b32 v144, v146\n"
"v_mfma_f32_16x16x32_bf16 v[4:7], a[200:203], a[160:163], v[4:7]\n"
"v_bfe_u32 v149, v242, 8, 2\n"
"v_lshl_add_u32 v149, v149, 4, s43\n"
"v_mfma_f32_16x16x32_bf16 v[4:7], a[204:207], a[164:167], v[4:7]\n"
"ds_read_b128 v[208:211], v149 offset:0\n"
"v_cvt_pk_bf16_f32 v147, v150, v151\n"
"v_mfma_f32_16x16x32_bf16 v[8:11], a[208:211], a[160:163], v[8:11]\n"
"ds_read_b128 v[216:219], v149 offset:1024\n"
"v_xor_b32 v150, 64, v149\n"
"v_mfma_f32_16x16x32_bf16 v[8:11], a[212:215], a[164:167], v[8:11]\n"
"ds_read_b128 v[212:215], v150 offset:0\n"
"v_cvt_pk_bf16_f32 v176, v176, v177\n"
"v_mfma_f32_16x16x32_bf16 v[12:15], a[216:219], a[160:163], v[12:15]\n"
"s_add_u32 s40, s56, s52\n"
"ds_read_b128 v[220:223], v150 offset:1024\n"
"v_cvt_pk_bf16_f32 v177, v178, v179\n"
"v_mfma_f32_16x16x32_bf16 v[12:15], a[220:223], a[164:167], v[12:15]\n"
"buffer_load_dwordx4 v240, s[16:19], s40 offen offset:1024 nt lds\n"
"v_mfma_f32_16x16x32_bf16 v[16:19], a[224:227], a[160:163], v[16:19]\n"
"v_permlane16_swap_b32 v145, v147\n"
"v_mfma_f32_16x16x32_bf16 v[16:19], a[228:231], a[164:167], v[16:19]\n"
"v_pk_fma_f32 v[96:97], v[96:97], s[70:71], s[70:71] op_sel:[0,0,1] op_sel_hi:[1,0,1]\nv_pk_fma_f32 v[98:99], v[98:99], s[70:71], s[70:71] op_sel:[0,0,1] op_sel_hi:[1,0,1]\nv_pk_fma_f32 v[100:101], v[100:101], s[70:71], s[70:71] op_sel:[0,0,1] op_sel_hi:[1,0,1]\nv_pk_fma_f32 v[102:103], v[102:103], s[70:71], s[70:71] op_sel:[0,0,1] op_sel_hi:[1,0,1]\nv_pk_fma_f32 v[104:105], v[104:105], s[70:71], s[70:71] op_sel:[0,0,1] op_sel_hi:[1,0,1]\nv_pk_fma_f32 v[106:107], v[106:107], s[70:71], s[70:71] op_sel:[0,0,1] op_sel_hi:[1,0,1]\nv_pk_fma_f32 v[108:109], v[108:109], s[70:71], s[70:71] op_sel:[0,0,1] op_sel_hi:[1,0,1]\nv_pk_fma_f32 v[110:111], v[110:111], s[70:71], s[70:71] op_sel:[0,0,1] op_sel_hi:[1,0,1]\n"
"v_mfma_f32_16x16x32_bf16 v[20:23], a[232:235], a[160:163], v[20:23]\n"
"v_cvt_pk_bf16_f32 v178, v180, v181\n"
"v_cvt_pk_bf16_f32 v179, v182, v183\n"
"v_mfma_f32_16x16x32_bf16 v[20:23], a[236:239], a[164:167], v[20:23]\n"
"v_permlane16_swap_b32 v176, v178\n"
"v_mfma_f32_16x16x32_bf16 v[24:27], a[240:243], a[160:163], v[24:27]\n"
"v_permlane16_swap_b32 v177, v179\n"
"v_mfma_f32_16x16x32_bf16 v[24:27], a[244:247], a[164:167], v[24:27]\n"
"v_cvt_pk_bf16_f32 v184, v184, v185\n"
"v_cvt_pk_bf16_f32 v185, v186, v187\n"
"v_mfma_f32_16x16x32_bf16 v[28:31], a[248:251], a[160:163], v[28:31]\n"
"v_cvt_pk_bf16_f32 v186, v188, v189\n"
"v_cvt_pk_bf16_f32 v187, v190, v191\n"
"v_mfma_f32_16x16x32_bf16 v[28:31], a[252:255], a[164:167], v[28:31]\n"
"v_permlane16_swap_b32 v184, v186\n"
"v_mfma_f32_16x16x32_bf16 v[60:63], a[248:251], a[168:171], v[60:63]\n"
"v_permlane16_swap_b32 v185, v187\n"
"v_mfma_f32_16x16x32_bf16 v[60:63], a[252:255], a[172:175], v[60:63]\n"
"v_accvgpr_write_b32 a132, v184\n"
"v_accvgpr_write_b32 a133, v185\n"
"v_mfma_f32_16x16x32_bf16 v[56:59], a[240:243], a[168:171], v[56:59]\n"
"s_add_u32 s40, s56, s53\n"
"v_accvgpr_write_b32 a134, v186\n"
"v_accvgpr_write_b32 a135, v187\n"
"v_mfma_f32_16x16x32_bf16 v[56:59], a[244:247], a[172:175], v[56:59]\n"
"buffer_load_dwordx4 v240, s[16:19], s40 offen offset:2048 nt lds\n"
"v_mfma_f32_16x16x32_bf16 v[52:55], a[232:235], a[168:171], v[52:55]\n"
"v_xor_b32 v148, 0x400, v243\n"
"v_cvt_pk_bf16_f32 v192, v192, v193\n"
"v_mfma_f32_16x16x32_bf16 v[52:55], a[236:239], a[172:175], v[52:55]\n"
"v_pk_fma_f32 v[0:1], v[0:1], s[70:71], s[70:71] op_sel:[0,0,1] op_sel_hi:[1,0,1]\nv_pk_fma_f32 v[2:3], v[2:3], s[70:71], s[70:71] op_sel:[0,0,1] op_sel_hi:[1,0,1]\nv_pk_fma_f32 v[4:5], v[4:5], s[70:71], s[70:71] op_sel:[0,0,1] op_sel_hi:[1,0,1]\nv_pk_fma_f32 v[6:7], v[6:7], s[70:71], s[70:71] op_sel:[0,0,1] op_sel_hi:[1,0,1]\nv_pk_fma_f32 v[8:9], v[8:9], s[70:71], s[70:71] op_sel:[0,0,1] op_sel_hi:[1,0,1]\nv_pk_fma_f32 v[10:11], v[10:11], s[70:71], s[70:71] op_sel:[0,0,1] op_sel_hi:[1,0,1]\nv_pk_fma_f32 v[12:13], v[12:13], s[70:71], s[70:71] op_sel:[0,0,1] op_sel_hi:[1,0,1]\nv_pk_fma_f32 v[14:15], v[14:15], s[70:71], s[70:71] op_sel:[0,0,1] op_sel_hi:[1,0,1]\nv_pk_fma_f32 v[16:17], v[16:17], s[70:71], s[70:71] op_sel:[0,0,1] op_sel_hi:[1,0,1]\nv_pk_fma_f32 v[18:19], v[18:19], s[70:71], s[70:71] op_sel:[0,0,1] op_sel_hi:[1,0,1]\nv_pk_fma_f32 v[20:21], v[20:21], s[70:71], s[70:71] op_sel:[0,0,1] op_sel_hi:[1,0,1]\nv_pk_fma_f32 v[22:23], v[22:23], s[70:71], s[70:71] op_sel:[0,0,1] op_sel_hi:[1,0,1]\nv_pk_fma_f32 v[24:25], v[24:25], s[70:71], s[70:71] op_sel:[0,0,1] op_sel_hi:[1,0,1]\nv_pk_fma_f32 v[26:27], v[26:27], s[70:71], s[70:71] op_sel:[0,0,1] op_sel_hi:[1,0,1]\nv_pk_fma_f32 v[28:29], v[28:29], s[70:71], s[70:71] op_sel:[0,0,1] op_sel_hi:[1,0,1]\nv_pk_fma_f32 v[30:31], v[30:31], s[70:71], s[70:71] op_sel:[0,0,1] op_sel_hi:[1,0,1]\n"
"v_mfma_f32_16x16x32_bf16 v[48:51], a[224:227], a[168:171], v[48:51]\n"
"v_cvt_pk_bf16_f32 v193, v194, v195\n"
"v_cvt_pk_bf16_f32 v194, v196, v197\n"
"v_mfma_f32_16x16x32_bf16 v[48:51], a[228:231], a[172:175], v[48:51]\n"
"s_nop 0\n"
"v_permlane16_swap_b32 v192, v194\n"
"v_mfma_f32_16x16x32_bf16 v[44:47], a[216:219], a[168:171], v[44:47]\n"
"v_cvt_pk_bf16_f32 v195, v198, v199\n"
"v_accvgpr_write_b32 a136, v192\n"
"v_mfma_f32_16x16x32_bf16 v[44:47], a[220:223], a[172:175], v[44:47]\n"
"v_permlane16_swap_b32 v193, v195\n"
"v_mfma_f32_16x16x32_bf16 v[40:43], a[208:211], a[168:171], v[40:43]\n"
"v_accvgpr_write_b32 a137, v193\n"
"v_accvgpr_write_b32 a138, v194\n"
"v_mfma_f32_16x16x32_bf16 v[40:43], a[212:215], a[172:175], v[40:43]\n"
"v_accvgpr_write_b32 a139, v195\n"
"v_cvt_pk_bf16_f32 v152, v152, v153\n"
"v_mfma_f32_16x16x32_bf16 v[36:39], a[200:203], a[168:171], v[36:39]\n"
"v_cvt_pk_bf16_f32 v153, v154, v155\n"
"v_cvt_pk_bf16_f32 v154, v156, v157\n"
"v_mfma_f32_16x16x32_bf16 v[36:39], a[204:207], a[172:175], v[36:39]\n"
"s_nop 0\n"
"v_permlane16_swap_b32 v152, v154\n"
"v_mfma_f32_16x16x32_bf16 v[32:35], a[192:195], a[168:171], v[32:35]\n"
"v_cvt_pk_bf16_f32 v155, v158, v159\n"
"v_accvgpr_write_b32 a116, v152\n"
"v_mfma_f32_16x16x32_bf16 v[32:35], a[196:199], a[172:175], v[32:35]\n"
"v_permlane16_swap_b32 v153, v155\n"
"v_mfma_f32_16x16x32_bf16 v[64:67], a[192:195], a[176:179], v[64:67]\n"
"s_add_u32 s40, s56, s54\n"
"ds_read_b128 a[192:195], v243 offset:0\n"
"v_accvgpr_write_b32 a117, v153\n"
"v_mfma_f32_16x16x32_bf16 v[64:67], a[196:199], a[180:183], v[64:67]\n"
"buffer_load_dwordx4 v240, s[16:19], s40 offen offset:3072 nt lds\n"
"v_mfma_f32_16x16x32_bf16 v[68:71], a[200:203], a[176:179], v[68:71]\n"
"ds_read_b128 a[196:199], v148 offset:0\n"
"v_accvgpr_write_b32 a118, v154\n"
"v_mfma_f32_16x16x32_bf16 v[68:71], a[204:207], a[180:183], v[68:71]\n"
"ds_read_b128 a[200:203], v243 offset:2048\n"
"v_accvgpr_write_b32 a119, v155\n"
"v_mfma_f32_16x16x32_bf16 v[72:75], a[208:211], a[176:179], v[72:75]\n"
"ds_read_b128 a[204:207], v148 offset:2048\n"
"v_cvt_pk_bf16_f32 v160, v160, v161\n"
"v_mfma_f32_16x16x32_bf16 v[72:75], a[212:215], a[180:183], v[72:75]\n"
"v_pk_fma_f32 v[32:33], v[32:33], s[70:71], s[70:71] op_sel:[0,0,1] op_sel_hi:[1,0,1]\nv_pk_fma_f32 v[34:35], v[34:35], s[70:71], s[70:71] op_sel:[0,0,1] op_sel_hi:[1,0,1]\nv_pk_fma_f32 v[36:37], v[36:37], s[70:71], s[70:71] op_sel:[0,0,1] op_sel_hi:[1,0,1]\nv_pk_fma_f32 v[38:39], v[38:39], s[70:71], s[70:71] op_sel:[0,0,1] op_sel_hi:[1,0,1]\nv_pk_fma_f32 v[40:41], v[40:41], s[70:71], s[70:71] op_sel:[0,0,1] op_sel_hi:[1,0,1]\nv_pk_fma_f32 v[42:43], v[42:43], s[70:71], s[70:71] op_sel:[0,0,1] op_sel_hi:[1,0,1]\nv_pk_fma_f32 v[44:45], v[44:45], s[70:71], s[70:71] op_sel:[0,0,1] op_sel_hi:[1,0,1]\nv_pk_fma_f32 v[46:47], v[46:47], s[70:71], s[70:71] op_sel:[0,0,1] op_sel_hi:[1,0,1]\nv_pk_fma_f32 v[48:49], v[48:49], s[70:71], s[70:71] op_sel:[0,0,1] op_sel_hi:[1,0,1]\nv_pk_fma_f32 v[50:51], v[50:51], s[70:71], s[70:71] op_sel:[0,0,1] op_sel_hi:[1,0,1]\nv_pk_fma_f32 v[52:53], v[52:53], s[70:71], s[70:71] op_sel:[0,0,1] op_sel_hi:[1,0,1]\nv_pk_fma_f32 v[54:55], v[54:55], s[70:71], s[70:71] op_sel:[0,0,1] op_sel_hi:[1,0,1]\nv_pk_fma_f32 v[56:57], v[56:57], s[70:71], s[70:71] op_sel:[0,0,1] op_sel_hi:[1,0,1]\nv_pk_fma_f32 v[58:59], v[58:59], s[70:71], s[70:71] op_sel:[0,0,1] op_sel_hi:[1,0,1]\nv_pk_fma_f32 v[60:61], v[60:61], s[70:71], s[70:71] op_sel:[0,0,1] op_sel_hi:[1,0,1]\nv_pk_fma_f32 v[62:63], v[62:63], s[70:71], s[70:71] op_sel:[0,0,1] op_sel_hi:[1,0,1]\n"
"v_mfma_f32_16x16x32_bf16 v[76:79], a[216:219], a[176:179], v[76:79]\n"
"v_cvt_pk_bf16_f32 v161, v162, v163\n"
"v_cvt_pk_bf16_f32 v162, v164, v165\n"
"v_mfma_f32_16x16x32_bf16 v[76:79], a[220:223], a[180:183], v[76:79]\n"
"s_nop 0\n"
"v_permlane16_swap_b32 v160, v162\n"
"v_mfma_f32_16x16x32_bf16 v[88:91], a[240:243], a[176:179], v[88:91]\n"
"v_cvt_pk_bf16_f32 v163, v166, v167\n"
"v_accvgpr_write_b32 a120, v160\n"
"v_mfma_f32_16x16x32_bf16 v[88:91], a[244:247], a[180:183], v[88:91]\n"
"v_permlane16_swap_b32 v161, v163\n"
"v_mfma_f32_16x16x32_bf16 v[92:95], a[248:251], a[176:179], v[92:95]\n"
"v_accvgpr_write_b32 a121, v161\n"
"v_accvgpr_write_b32 a122, v162\n"
"v_mfma_f32_16x16x32_bf16 v[92:95], a[252:255], a[180:183], v[92:95]\n"
"v_accvgpr_write_b32 a123, v163\n"
"v_cvt_pk_bf16_f32 v168, v168, v169\n"
"v_mfma_f32_16x16x32_bf16 v[80:83], a[224:227], a[176:179], v[80:83]\n"
"v_cvt_pk_bf16_f32 v169, v170, v171\n"
"v_cvt_pk_bf16_f32 v170, v172, v173\n"
"v_mfma_f32_16x16x32_bf16 v[80:83], a[228:231], a[180:183], v[80:83]\n"
"s_nop 0\n"
"v_permlane16_swap_b32 v168, v170\n"
"v_mfma_f32_16x16x32_bf16 v[84:87], a[232:235], a[176:179], v[84:87]\n"
"v_cvt_pk_bf16_f32 v171, v174, v175\n"
"v_accvgpr_write_b32 a124, v168\n"
"v_mfma_f32_16x16x32_bf16 v[84:87], a[236:239], a[180:183], v[84:87]\n"
"s_waitcnt lgkmcnt(0)\n"
"v_permlane16_swap_b32 v169, v171\n"
"v_mfma_f32_16x16x32_bf16 v[180:183], v[208:211], a[0:3], 0\n"
"v_accvgpr_write_b32 a125, v169\n"
"v_accvgpr_write_b32 a126, v170\n"
"v_mfma_f32_16x16x32_bf16 v[180:183], v[212:215], a[4:7], v[180:183]\n"
"v_accvgpr_write_b32 a127, v171\n"
"v_accvgpr_write_b32 a128, v176\n"
"v_mfma_f32_16x16x32_bf16 v[180:183], v[216:219], a[8:11], v[180:183]\n"
"v_accvgpr_write_b32 a129, v177\n"
"v_accvgpr_write_b32 a130, v178\n"
"v_mfma_f32_16x16x32_bf16 v[180:183], v[220:223], a[12:15], v[180:183]\n"
"v_accvgpr_write_b32 a131, v179\n"
"v_accvgpr_write_b32 a104, v128\n"
"v_mfma_f32_16x16x32_bf16 v[184:187], v[208:211], a[16:19], 0\n"
"v_accvgpr_write_b32 a105, v129\n"
"v_accvgpr_write_b32 a108, v136\n"
"v_mfma_f32_16x16x32_bf16 v[184:187], v[212:215], a[20:23], v[184:187]\n"
"v_pk_fma_f32 v[64:65], v[64:65], s[70:71], s[70:71] op_sel:[0,0,1] op_sel_hi:[1,0,1]\nv_pk_fma_f32 v[66:67], v[66:67], s[70:71], s[70:71] op_sel:[0,0,1] op_sel_hi:[1,0,1]\nv_pk_fma_f32 v[68:69], v[68:69], s[70:71], s[70:71] op_sel:[0,0,1] op_sel_hi:[1,0,1]\nv_pk_fma_f32 v[70:71], v[70:71], s[70:71], s[70:71] op_sel:[0,0,1] op_sel_hi:[1,0,1]\nv_pk_fma_f32 v[72:73], v[72:73], s[70:71], s[70:71] op_sel:[0,0,1] op_sel_hi:[1,0,1]\nv_pk_fma_f32 v[74:75], v[74:75], s[70:71], s[70:71] op_sel:[0,0,1] op_sel_hi:[1,0,1]\nv_pk_fma_f32 v[76:77], v[76:77], s[70:71], s[70:71] op_sel:[0,0,1] op_sel_hi:[1,0,1]\nv_pk_fma_f32 v[78:79], v[78:79], s[70:71], s[70:71] op_sel:[0,0,1] op_sel_hi:[1,0,1]\nv_pk_fma_f32 v[80:81], v[80:81], s[70:71], s[70:71] op_sel:[0,0,1] op_sel_hi:[1,0,1]\nv_pk_fma_f32 v[82:83], v[82:83], s[70:71], s[70:71] op_sel:[0,0,1] op_sel_hi:[1,0,1]\nv_pk_fma_f32 v[84:85], v[84:85], s[70:71], s[70:71] op_sel:[0,0,1] op_sel_hi:[1,0,1]\nv_pk_fma_f32 v[86:87], v[86:87], s[70:71], s[70:71] op_sel:[0,0,1] op_sel_hi:[1,0,1]\nv_pk_fma_f32 v[88:89], v[88:89], s[70:71], s[70:71] op_sel:[0,0,1] op_sel_hi:[1,0,1]\nv_pk_fma_f32 v[90:91], v[90:91], s[70:71], s[70:71] op_sel:[0,0,1] op_sel_hi:[1,0,1]\nv_pk_fma_f32 v[92:93], v[92:93], s[70:71], s[70:71] op_sel:[0,0,1] op_sel_hi:[1,0,1]\nv_pk_fma_f32 v[94:95], v[94:95], s[70:71], s[70:71] op_sel:[0,0,1] op_sel_hi:[1,0,1]\n"
"v_mfma_f32_16x16x32_bf16 v[184:187], v[216:219], a[24:27], v[184:187]\n"
"v_accvgpr_write_b32 a109, v137\n"
"v_accvgpr_write_b32 a112, v144\n"
"v_mfma_f32_16x16x32_bf16 v[184:187], v[220:223], a[28:31], v[184:187]\n"
"v_accvgpr_write_b32 a113, v145\n"
"v_accvgpr_write_b32 a106, v130\n"
"v_mfma_f32_16x16x32_bf16 v[188:191], v[208:211], a[32:35], 0\n"
"v_accvgpr_write_b32 a107, v131\n"
"v_accvgpr_write_b32 a110, v138\n"
"v_mfma_f32_16x16x32_bf16 v[188:191], v[212:215], a[36:39], v[188:191]\n"
"v_sub_f32 v180, s74, v180\n"
"v_sub_f32 v181, s74, v181\n"
"v_mfma_f32_16x16x32_bf16 v[188:191], v[216:219], a[40:43], v[188:191]\n"
"v_sub_f32 v182, s74, v182\n"
"v_sub_f32 v183, s74, v183\n"
"v_mfma_f32_16x16x32_bf16 v[188:191], v[220:223], a[44:47], v[188:191]\n"
"v_accvgpr_write_b32 a111, v139\n"
"v_cvt_pk_bf16_f32 v96, v96, v97\n"
"v_mfma_f32_16x16x32_bf16 v[192:195], v[208:211], a[192:195], 0\n"
"ds_read_b128 v[208:211], v228 offset:0\n"
"v_cvt_pk_bf16_f32 v97, v98, v99\n"
"v_mfma_f32_16x16x32_bf16 v[192:195], v[212:215], a[196:199], v[192:195]\n"
"ds_read_b128 v[212:215], v229 offset:0\n"
"v_sub_f32 v184, s74, v184\n"
"v_mfma_f32_16x16x32_bf16 v[192:195], v[216:219], a[200:203], v[192:195]\n"
"ds_read_b128 v[216:219], v228 offset:1024\n"
"v_sub_f32 v185, s74, v185\n"
"v_mfma_f32_16x16x32_bf16 v[192:195], v[220:223], a[204:207], v[192:195]\n"
"s_waitcnt lgkmcnt(2)\n"
"ds_read_b128 v[220:223], v229 offset:1024\n"
"v_sub_f32 v186, s74, v186\n"
"v_mfma_f32_16x16x32_bf16 v[152:155], v[208:211], a[0:3], v[180:183]\n"
"s_waitcnt lgkmcnt(2)\n"
"v_sub_f32 v187, s74, v187\n"
"v_cvt_pk_bf16_f32 v98, v100, v101\n"
"v_mfma_f32_16x16x32_bf16 v[152:155], v[212:215], a[4:7], v[152:155]\n"
"s_waitcnt lgkmcnt(1)\n"
"v_sub_f32 v188, s74, v188\n"
"v_sub_f32 v189, s74, v189\n"
"v_mfma_f32_16x16x32_bf16 v[152:155], v[216:219], a[8:11], v[152:155]\n"
"s_waitcnt lgkmcnt(0)\n"
"v_sub_f32 v190, s74, v190\n"
"v_sub_f32 v191, s74, v191\n"
"v_mfma_f32_16x16x32_bf16 v[152:155], v[220:223], a[12:15], v[152:155]\n"
"v_permlane16_swap_b32 v96, v98\n"
"v_mfma_f32_16x16x32_bf16 v[156:159], v[216:219], a[24:27], v[184:187]\n"
"v_cvt_pk_bf16_f32 v99, v102, v103\n"
"v_cvt_pk_bf16_f32 v104, v104, v105\n"
"v_mfma_f32_16x16x32_bf16 v[156:159], v[220:223], a[28:31], v[156:159]\n"
"v_sub_f32 v192, s74, v192\n"
"v_sub_f32 v193, s74, v193\n"
"v_mfma_f32_16x16x32_bf16 v[156:159], v[208:211], a[16:19], v[156:159]\n"
"v_sub_f32 v194, s74, v194\n"
"v_sub_f32 v195, s74, v195\n"
"v_mfma_f32_16x16x32_bf16 v[156:159], v[212:215], a[20:23], v[156:159]\n"
"v_permlane16_swap_b32 v97, v99\n"
"v_mfma_f32_16x16x32_bf16 v[160:163], v[208:211], a[32:35], v[188:191]\n"
"ds_write_b128 v243, v[96:99] offset:12288\n"
"v_mfma_f32_16x16x32_bf16 v[160:163], v[212:215], a[36:39], v[160:163]\n"
"v_exp_f32 v152, v152\n"
"v_mfma_f32_16x16x32_bf16 v[160:163], v[216:219], a[40:43], v[160:163]\n"
"v_exp_f32 v153, v153\n"
"v_mfma_f32_16x16x32_bf16 v[160:163], v[220:223], a[44:47], v[160:163]\n"
"v_exp_f32 v154, v154\n"
"v_mfma_f32_16x16x32_bf16 v[164:167], v[216:219], a[200:203], v[192:195]\n"
"ds_read_b128 v[216:219], v228 offset:5120\n"
"v_cvt_pk_bf16_f32 v128, v152, v153\n"
"v_mfma_f32_16x16x32_bf16 v[164:167], v[220:223], a[204:207], v[164:167]\n"
"ds_read_b128 v[220:223], v229 offset:5120\n"
"v_cvt_pk_bf16_f32 v105, v106, v107\n"
"v_mfma_f32_16x16x32_bf16 v[164:167], v[208:211], a[192:195], v[164:167]\n"
"ds_read_b128 v[208:211], v228 offset:4096\n"
"v_cvt_pk_bf16_f32 v106, v108, v109\n"
"v_mfma_f32_16x16x32_bf16 v[164:167], v[212:215], a[196:199], v[164:167]\n"
"s_waitcnt lgkmcnt(2)\n"
"ds_read_b128 v[212:215], v229 offset:4096\n"
"v_cvt_pk_bf16_f32 v107, v110, v111\n"
"v_mfma_f32_16x16x32_bf16 v[168:171], v[216:219], a[8:11], v[180:183]\n"
"s_waitcnt lgkmcnt(2)\n"
"v_exp_f32 v155, v155\n"
"v_mfma_f32_16x16x32_bf16 v[168:171], v[220:223], a[12:15], v[168:171]\n"
"s_waitcnt lgkmcnt(1)\n"
"v_exp_f32 v156, v156\n"
"v_mfma_f32_16x16x32_bf16 v[168:171], v[208:211], a[0:3], v[168:171]\n"
"s_waitcnt lgkmcnt(0)\n"
"v_exp_f32 v157, v157\n"
"v_mfma_f32_16x16x32_bf16 v[168:171], v[212:215], a[4:7], v[168:171]\n"
"v_exp_f32 v158, v158\n"
"v_mfma_f32_16x16x32_bf16 v[172:175], v[208:211], a[16:19], v[184:187]\n"
"v_exp_f32 v159, v159\n"
"v_mfma_f32_16x16x32_bf16 v[172:175], v[212:215], a[20:23], v[172:175]\n"
"v_exp_f32 v160, v160\n"
"v_mfma_f32_16x16x32_bf16 v[172:175], v[216:219], a[24:27], v[172:175]\n"
"v_exp_f32 v161, v161\n"
"v_mfma_f32_16x16x32_bf16 v[172:175], v[220:223], a[28:31], v[172:175]\n"
"v_exp_f32 v162, v162\n"
"v_mfma_f32_16x16x32_bf16 v[176:179], v[216:219], a[40:43], v[188:191]\n"
"v_exp_f32 v163, v163\n"
"v_mfma_f32_16x16x32_bf16 v[176:179], v[220:223], a[44:47], v[176:179]\n"
"v_cvt_pk_bf16_f32 v129, v154, v155\n"
"v_cvt_pk_bf16_f32 v136, v156, v157\n"
"v_mfma_f32_16x16x32_bf16 v[176:179], v[208:211], a[32:35], v[176:179]\n"
"v_exp_f32 v164, v164\n"
"v_mfma_f32_16x16x32_bf16 v[176:179], v[212:215], a[36:39], v[176:179]\n"
"v_exp_f32 v165, v165\n"
"v_mfma_f32_16x16x32_bf16 v[152:155], v[208:211], a[192:195], v[192:195]\n"
"ds_read_b128 v[208:211], v230 offset:0\n"
"v_cvt_pk_bf16_f32 v137, v158, v159\n"
"v_mfma_f32_16x16x32_bf16 v[152:155], v[212:215], a[196:199], v[152:155]\n"
"ds_read_b128 v[212:215], v231 offset:0\n"
"v_cvt_pk_bf16_f32 v144, v160, v161\n"
"v_mfma_f32_16x16x32_bf16 v[152:155], v[216:219], a[200:203], v[152:155]\n"
"ds_read_b128 v[216:219], v230 offset:1024\n"
"v_cvt_pk_bf16_f32 v145, v162, v163\n"
"v_mfma_f32_16x16x32_bf16 v[152:155], v[220:223], a[204:207], v[152:155]\n"
"s_waitcnt lgkmcnt(2)\n"
"ds_read_b128 v[220:223], v231 offset:1024\n"
"v_cvt_pk_bf16_f32 v124, v164, v165\n"
"v_mfma_f32_16x16x32_bf16 v[156:159], v[208:211], a[0:3], v[180:183]\n"
"s_waitcnt lgkmcnt(2)\n"
"v_exp_f32 v166, v166\n"
"v_mfma_f32_16x16x32_bf16 v[156:159], v[212:215], a[4:7], v[156:159]\n"
"s_waitcnt lgkmcnt(1)\n"
"v_exp_f32 v167, v167\n"
"v_mfma_f32_16x16x32_bf16 v[156:159], v[216:219], a[8:11], v[156:159]\n"
"s_waitcnt lgkmcnt(0)\n"
"v_exp_f32 v168, v168\n"
"v_mfma_f32_16x16x32_bf16 v[156:159], v[220:223], a[12:15], v[156:159]\n"
"v_exp_f32 v169, v169\n"
"v_mfma_f32_16x16x32_bf16 v[160:163], v[216:219], a[24:27], v[184:187]\n"
"v_exp_f32 v170, v170\n"
"v_mfma_f32_16x16x32_bf16 v[160:163], v[220:223], a[28:31], v[160:163]\n"
"v_exp_f32 v171, v171\n"
"v_mfma_f32_16x16x32_bf16 v[160:163], v[208:211], a[16:19], v[160:163]\n"
"v_cvt_pk_bf16_f32 v125, v166, v167\n"
"v_cvt_pk_bf16_f32 v130, v168, v169\n"
"v_mfma_f32_16x16x32_bf16 v[160:163], v[212:215], a[20:23], v[160:163]\n"
"v_exp_f32 v172, v172\n"
"v_mfma_f32_16x16x32_bf16 v[164:167], v[208:211], a[32:35], v[188:191]\n"
"ds_read_b128 v[208:211], v230 offset:4096\n"
"v_cvt_pk_bf16_f32 v131, v170, v171\n"
"v_mfma_f32_16x16x32_bf16 v[164:167], v[212:215], a[36:39], v[164:167]\n"
"ds_read_b128 v[212:215], v231 offset:4096\n"
"v_accvgpr_write_b32 a114, v146\n"
"v_mfma_f32_16x16x32_bf16 v[164:167], v[216:219], a[40:43], v[164:167]\n"
"ds_read_b128 v[216:219], v230 offset:5120\n"
"v_accvgpr_write_b32 a115, v147\n"
"v_mfma_f32_16x16x32_bf16 v[164:167], v[220:223], a[44:47], v[164:167]\n"
"s_waitcnt lgkmcnt(2)\n"
"ds_read_b128 v[220:223], v231 offset:5120\n"
"v_cvt_pk_bf16_f32 v8, v8, v9\n"
"v_mfma_f32_16x16x32_bf16 v[168:171], v[208:211], a[0:3], v[180:183]\n"
"s_waitcnt lgkmcnt(2)\n"
"v_exp_f32 v173, v173\n"
"v_mfma_f32_16x16x32_bf16 v[168:171], v[212:215], a[4:7], v[168:171]\n"
"s_waitcnt lgkmcnt(1)\n"
"v_exp_f32 v174, v174\n"
"v_mfma_f32_16x16x32_bf16 v[168:171], v[216:219], a[8:11], v[168:171]\n"
"s_waitcnt lgkmcnt(0)\n"
"v_exp_f32 v175, v175\n"
"v_mfma_f32_16x16x32_bf16 v[168:171], v[220:223], a[12:15], v[168:171]\n"
"v_cvt_pk_bf16_f32 v138, v172, v173\n"
"v_cvt_pk_bf16_f32 v139, v174, v175\n"
"v_mfma_f32_16x16x32_bf16 v[172:175], v[216:219], a[24:27], v[184:187]\n"
"v_permlane16_swap_b32 v104, v106\n"
"v_mfma_f32_16x16x32_bf16 v[172:175], v[220:223], a[28:31], v[172:175]\n"
"v_permlane16_swap_b32 v105, v107\n"
"v_mfma_f32_16x16x32_bf16 v[172:175], v[208:211], a[16:19], v[172:175]\n"
"ds_write_b128 v243, v[104:107] offset:14336\n"
"v_exp_f32 v176, v176\n"
"v_exp_f32 v177, v177\n"
"v_exp_f32 v178, v178\n"
"v_exp_f32 v179, v179\n"
"v_mfma_f32_16x16x32_bf16 v[172:175], v[212:215], a[20:23], v[172:175]\n"
"s_cbranch_vccnz LPCE0_%=\nLPKE0_%=:\n"
"v_cvt_pk_bf16_f32 v146, v176, v177\n"
"v_cvt_pk_bf16_f32 v147, v178, v179\n"
"v_mfma_f32_16x16x32_bf16 v[176:179], v[208:211], a[32:35], v[188:191]\n"
"v_exp_f32 v152, v152\n"
"v_mfma_f32_16x16x32_bf16 v[176:179], v[212:215], a[36:39], v[176:179]\n"
"v_exp_f32 v153, v153\n"
"v_mfma_f32_16x16x32_bf16 v[176:179], v[216:219], a[40:43], v[176:179]\n"
"v_exp_f32 v154, v154\n"
"v_mfma_f32_16x16x32_bf16 v[176:179], v[220:223], a[44:47], v[176:179]\n"
"v_exp_f32 v155, v155\n"
"s_waitcnt vmcnt(8) lgkmcnt(0)\n"
"s_barrier\n"
"ds_read_b128 v[208:211], v228 offset:16384\n"
"ds_read_b128 v[212:215], v229 offset:16384\n"
"ds_read_b128 v[216:219], v228 offset:17408\n"
"ds_read_b128 v[220:223], v229 offset:17408\n"
"v_cvt_pk_bf16_f32 v126, v152, v153\n"
"v_cvt_pk_bf16_f32 v127, v154, v155\n"
"s_waitcnt lgkmcnt(3)\n"
"v_mfma_f32_16x16x32_bf16 v[152:155], v[208:211], a[0:3], v[180:183]\n"
"v_exp_f32 v156, v156\n"
"s_waitcnt lgkmcnt(2)\n"
"v_mfma_f32_16x16x32_bf16 v[152:155], v[212:215], a[4:7], v[152:155]\n"
"v_exp_f32 v157, v157\n"
"s_waitcnt lgkmcnt(1)\n"
"v_mfma_f32_16x16x32_bf16 v[152:155], v[216:219], a[8:11], v[152:155]\n"
"s_waitcnt lgkmcnt(0)\n"
"s_add_u32 m0, s44, 98304\n"
"v_exp_f32 v158, v158\n"
"v_exp_f32 v159, v159\n"
"v_mfma_f32_16x16x32_bf16 v[152:155], v[220:223], a[12:15], v[152:155]\n"
"buffer_load_dwordx4 v240, s[20:23], s56 offen nt lds\n"
"v_cvt_pk_bf16_f32 v132, v156, v157\n"
"v_cvt_pk_bf16_f32 v133, v158, v159\n"
"v_mfma_f32_16x16x32_bf16 v[156:159], v[216:219], a[24:27], v[184:187]\n"
"v_exp_f32 v160, v160\n"
"v_mfma_f32_16x16x32_bf16 v[156:159], v[220:223], a[28:31], v[156:159]\n"
"v_exp_f32 v161, v161\n"
"v_mfma_f32_16x16x32_bf16 v[156:159], v[208:211], a[16:19], v[156:159]\n"
"v_exp_f32 v162, v162\n"
"v_exp_f32 v163, v163\n"
"v_mfma_f32_16x16x32_bf16 v[156:159], v[212:215], a[20:23], v[156:159]\n"
"v_cvt_pk_bf16_f32 v140, v160, v161\n"
"v_cvt_pk_bf16_f32 v141, v162, v163\n"
"v_mfma_f32_16x16x32_bf16 v[160:163], v[208:211], a[32:35], v[188:191]\n"
"v_exp_f32 v164, v164\n"
"v_mfma_f32_16x16x32_bf16 v[160:163], v[212:215], a[36:39], v[160:163]\n"
"v_exp_f32 v165, v165\n"
"v_mfma_f32_16x16x32_bf16 v[160:163], v[216:219], a[40:43], v[160:163]\n"
"ds_read_b64_tr_b16 a[244:245], v234 offset:0\n"
"ds_read_b64_tr_b16 a[246:247], v234 offset:4096\n"
"v_exp_f32 v166, v166\n"
"v_exp_f32 v167, v167\n"
"v_mfma_f32_16x16x32_bf16 v[160:163], v[220:223], a[44:47], v[160:163]\n"
"v_cvt_pk_bf16_f32 v148, v164, v165\n"
"v_cvt_pk_bf16_f32 v149, v166, v167\n"
"v_mfma_f32_16x16x32_bf16 v[164:167], v[216:219], a[200:203], v[192:195]\n"
"ds_read_b64_tr_b16 a[248:249], v238 offset:0\n"
"ds_read_b64_tr_b16 a[250:251], v238 offset:4096\n"
"v_mfma_f32_16x16x32_bf16 v[164:167], v[220:223], a[204:207], v[164:167]\n"
"v_exp_f32 v168, v168\n"
"v_mfma_f32_16x16x32_bf16 v[164:167], v[208:211], a[192:195], v[164:167]\n"
"s_add_u32 s40, s56, s52\n"
"v_exp_f32 v169, v169\n"
"v_mfma_f32_16x16x32_bf16 v[164:167], v[212:215], a[196:199], v[164:167]\n"
"s_waitcnt lgkmcnt(2)\n"
"buffer_load_dwordx4 v240, s[20:23], s40 offen offset:1024 nt lds\n"
"v_cvt_pk_bf16_f32 v9, v10, v11\n"
"v_cvt_pk_bf16_f32 v10, v12, v13\n"
"v_cvt_pk_bf16_f32 v11, v14, v15\n"
"v_exp_f32 v170, v170\n"
"v_exp_f32 v171, v171\n"
"v_mfma_f32_16x16x32_bf16 a[216:219], v[124:127], a[244:247], 0\n"
"v_permlane16_swap_b32 v8, v10\n"
"v_permlane16_swap_b32 v9, v11\n"
"v_accvgpr_write_b32 a148, v8\n"
"v_accvgpr_write_b32 a149, v9\n"
"v_accvgpr_write_b32 a150, v10\n"
"v_accvgpr_write_b32 a151, v11\n"
"v_cvt_pk_bf16_f32 v134, v168, v169\n"
"v_cvt_pk_bf16_f32 v135, v170, v171\n"
"v_mfma_f32_16x16x32_bf16 v[8:11], v[128:131], a[244:247], 0\n"
"s_waitcnt lgkmcnt(0)\n"
"ds_read_b64_tr_b16 v[224:225], v239 offset:0\n"
"ds_read_b64_tr_b16 v[226:227], v239 offset:4096\n"
"v_cvt_pk_bf16_f32 v40, v40, v41\n"
"v_cvt_pk_bf16_f32 v41, v42, v43\n"
"v_cvt_pk_bf16_f32 v42, v44, v45\n"
"v_cvt_pk_bf16_f32 v43, v46, v47\n"
"v_exp_f32 v172, v172\n"
"v_exp_f32 v173, v173\n"
"v_exp_f32 v174, v174\n"
"v_exp_f32 v175, v175\n"
"v_mfma_f32_16x16x32_bf16 v[8:11], v[132:135], a[248:251], v[8:11]\n"
"ds_read_b64_tr_b16 a[252:253], v235 offset:0\n"
"ds_read_b64_tr_b16 a[254:255], v235 offset:4096\n"
"v_permlane16_swap_b32 v40, v42\n"
"v_permlane16_swap_b32 v41, v43\n"
"v_accvgpr_write_b32 a164, v40\n"
"v_accvgpr_write_b32 a165, v41\n"
"v_accvgpr_write_b32 a166, v42\n"
"v_accvgpr_write_b32 a167, v43\n"
"v_cvt_pk_bf16_f32 v142, v172, v173\n"
"v_cvt_pk_bf16_f32 v143, v174, v175\n"
"v_mfma_f32_16x16x32_bf16 v[40:43], v[136:139], a[244:247], 0\n"
"v_cvt_pk_bf16_f32 v72, v72, v73\n"
"v_cvt_pk_bf16_f32 v73, v74, v75\n"
"v_cvt_pk_bf16_f32 v74, v76, v77\n"
"v_cvt_pk_bf16_f32 v75, v78, v79\n"
"v_exp_f32 v176, v176\n"
"v_exp_f32 v177, v177\n"
"v_exp_f32 v178, v178\n"
"v_exp_f32 v179, v179\n"
"v_mfma_f32_16x16x32_bf16 v[40:43], v[140:143], a[248:251], v[40:43]\n"
"v_permlane16_swap_b32 v72, v74\n"
"v_permlane16_swap_b32 v73, v75\n"
"v_accvgpr_write_b32 a180, v72\n"
"v_accvgpr_write_b32 a181, v73\n"
"v_accvgpr_write_b32 a182, v74\n"
"v_accvgpr_write_b32 a183, v75\n"
"v_cvt_pk_bf16_f32 v150, v176, v177\n"
"v_cvt_pk_bf16_f32 v151, v178, v179\n"
"v_mfma_f32_16x16x32_bf16 v[72:75], v[144:147], a[244:247], 0\n"
"ds_read_b128 v[216:219], v228 offset:21504\n"
"v_cvt_pk_bf16_f32 v200, v200, v201\n"
"v_mfma_f32_16x16x32_bf16 v[72:75], v[148:151], a[248:251], v[72:75]\n"
"s_waitcnt lgkmcnt(3)\n"
"ds_read_b128 v[220:223], v229 offset:21504\n"
"v_cvt_pk_bf16_f32 v201, v202, v203\n"
"v_mfma_f32_16x16x32_bf16 v[76:79], v[148:151], v[224:227], 0\n"
"s_waitcnt lgkmcnt(2)\n"
"ds_read_b128 v[208:211], v228 offset:20480\n"
"v_cvt_pk_bf16_f32 v202, v204, v205\n"
"v_mfma_f32_16x16x32_bf16 v[76:79], v[144:147], a[252:255], v[76:79]\n"
"ds_read_b128 v[212:215], v229 offset:20480\n"
"v_cvt_pk_bf16_f32 v203, v206, v207\n"
"v_mfma_f32_16x16x32_bf16 v[44:47], v[140:143], v[224:227], 0\n"
"s_add_u32 s40, s56, s53\n"
"ds_read_b64_tr_b16 a[244:245], v234 offset:1024\n"
"v_accvgpr_write_b32 a240, v254\n"
"v_mfma_f32_16x16x32_bf16 v[44:47], v[136:139], a[252:255], v[44:47]\n"
"buffer_load_dwordx4 v240, s[20:23], s40 offen offset:2048 nt lds\n"
"v_mfma_f32_16x16x32_bf16 v[12:15], v[132:135], v[224:227], 0\n"
"v_accvgpr_write_b32 a241, v254\n"
"v_accvgpr_write_b32 a242, v254\n"
"v_mfma_f32_16x16x32_bf16 v[12:15], v[128:131], a[252:255], v[12:15]\n"
"v_accvgpr_write_b32 a243, v254\n"
"v_mov_b32 v196, 0\n"
"v_mov_b32 v197, 0\n"
"v_mov_b32 v198, 0\n"
"v_mov_b32 v199, 0\n"
"v_mfma_f32_16x16x32_bf16 a[220:223], v[124:127], a[252:255], 0\n"
"v_permlane16_swap_b32 v200, v202\n"
"v_permlane16_swap_b32 v201, v203\n"
"v_accvgpr_write_b32 a142, v202\n"
"v_accvgpr_write_b32 a143, v203\n"
"v_mov_b32 v202, 0\n"
"v_mov_b32 v203, 0\n"
"v_mov_b32 v204, 0\n"
"v_mov_b32 v205, 0\n"
"v_smfmac_f32_16x16x64_bf16 v[196:199], a[240:243], v[128:135], v241\n"
"ds_read_b64_tr_b16 a[246:247], v234 offset:5120\n"
"v_cvt_pk_bf16_f32 v24, v24, v25\n"
"v_mfma_f32_16x16x32_bf16 v[202:205], a[240:243], v[124:127], v[202:205]\n"
"s_waitcnt lgkmcnt(3)\n"
"ds_read_b64_tr_b16 a[248:249], v238 offset:1024\n"
"v_cvt_pk_bf16_f32 v25, v26, v27\n"
"v_mfma_f32_16x16x32_bf16 v[168:171], v[216:219], a[8:11], v[180:183]\n"
"ds_read_b64_tr_b16 a[250:251], v238 offset:5120\n"
"v_cvt_pk_bf16_f32 v26, v28, v29\n"
"v_mfma_f32_16x16x32_bf16 v[168:171], v[220:223], a[12:15], v[168:171]\n"
"ds_read_b64_tr_b16 v[224:225], v239 offset:1024\n"
"v_cvt_pk_bf16_f32 v27, v30, v31\n"
"v_mfma_f32_16x16x32_bf16 v[168:171], v[208:211], a[0:3], v[168:171]\n"
"s_waitcnt lgkmcnt(4)\n"
"ds_read_b64_tr_b16 v[226:227], v239 offset:5120\n"
"v_cvt_pk_bf16_f32 v56, v56, v57\n"
"v_mfma_f32_16x16x32_bf16 v[168:171], v[212:215], a[4:7], v[168:171]\n"
"ds_read_b64_tr_b16 a[252:253], v235 offset:1024\n"
"v_cvt_pk_bf16_f32 v57, v58, v59\n"
"v_mfma_f32_16x16x32_bf16 v[172:175], v[208:211], a[16:19], v[184:187]\n"
"v_exp_f32 v152, v152\n"
"v_mfma_f32_16x16x32_bf16 v[172:175], v[212:215], a[20:23], v[172:175]\n"
"ds_read_b64_tr_b16 a[254:255], v235 offset:5120\n"
"v_cvt_pk_bf16_f32 v58, v60, v61\n"
"v_mfma_f32_16x16x32_bf16 v[172:175], v[216:219], a[24:27], v[172:175]\n"
"s_add_u32 s40, s56, s54\n"
"v_exp_f32 v153, v153\n"
"v_mfma_f32_16x16x32_bf16 v[172:175], v[220:223], a[28:31], v[172:175]\n"
"buffer_load_dwordx4 v240, s[20:23], s40 offen offset:3072 nt lds\n"
"v_mfma_f32_16x16x32_bf16 v[176:179], v[216:219], a[40:43], v[188:191]\n"
"s_add_u32 s55, s55, s59\n"
"v_exp_f32 v154, v154\n"
"v_mfma_f32_16x16x32_bf16 v[176:179], v[220:223], a[44:47], v[176:179]\n"
"s_add_u32 s41, s55, s57\n"
"v_exp_f32 v155, v155\n"
"v_mfma_f32_16x16x32_bf16 v[176:179], v[208:211], a[32:35], v[176:179]\n"
"s_and_b32 s41, s41, s58\n"
"v_cvt_pk_bf16_f32 v96, v152, v153\n"
"v_cvt_pk_bf16_f32 v97, v154, v155\n"
"v_mfma_f32_16x16x32_bf16 v[176:179], v[212:215], a[36:39], v[176:179]\n"
"s_andn2_b32 s56, s55, s58\n"
"v_permlane16_swap_b32 v24, v26\n"
"v_mfma_f32_16x16x32_bf16 v[152:155], v[208:211], a[192:195], v[192:195]\n"
"s_or_b32 s56, s56, s41\n"
"ds_read_b128 v[208:211], v230 offset:16384\n"
"v_accvgpr_write_b32 a156, v24\n"
"v_mfma_f32_16x16x32_bf16 v[152:155], v[212:215], a[196:199], v[152:155]\n"
"ds_read_b128 v[212:215], v231 offset:16384\n"
"v_accvgpr_write_b32 a158, v26\n"
"v_mfma_f32_16x16x32_bf16 v[152:155], v[216:219], a[200:203], v[152:155]\n"
"ds_read_b128 v[216:219], v230 offset:17408\n"
"v_cvt_pk_bf16_f32 v59, v62, v63\n"
"v_mfma_f32_16x16x32_bf16 v[152:155], v[220:223], a[204:207], v[152:155]\n"
"s_waitcnt lgkmcnt(3)\n"
"v_permlane16_swap_b32 v25, v27\n"
"v_mfma_f32_16x16x32_bf16 a[232:235], v[124:127], a[244:247], 0\n"
"v_accvgpr_write_b32 a157, v25\n"
"v_accvgpr_write_b32 a159, v27\n"
"v_mfma_f32_16x16x32_bf16 v[24:27], v[128:131], a[244:247], 0\n"
"s_add_u32 m0, s44, 49152\n"
"v_permlane16_swap_b32 v56, v58\n"
"v_mfma_f32_16x16x32_bf16 v[24:27], v[132:135], a[248:251], v[24:27]\n"
"buffer_load_dwordx4 v240, s[16:19], s56 offen nt lds\n"
"v_permlane16_swap_b32 v57, v59\n"
"v_accvgpr_write_b32 a172, v56\n"
"v_accvgpr_write_b32 a173, v57\n"
"v_accvgpr_write_b32 a174, v58\n"
"v_accvgpr_write_b32 a175, v59\n"
"v_mfma_f32_16x16x32_bf16 v[56:59], v[136:139], a[244:247], 0\n"
"v_cvt_pk_bf16_f32 v88, v88, v89\n"
"v_cvt_pk_bf16_f32 v89, v90, v91\n"
"v_cvt_pk_bf16_f32 v90, v92, v93\n"
"v_cvt_pk_bf16_f32 v91, v94, v95\n"
"v_mfma_f32_16x16x32_bf16 v[56:59], v[140:143], a[248:251], v[56:59]\n"
"v_permlane16_swap_b32 v88, v90\n"
"v_permlane16_swap_b32 v89, v91\n"
"v_accvgpr_write_b32 a188, v88\n"
"v_accvgpr_write_b32 a189, v89\n"
"v_accvgpr_write_b32 a190, v90\n"
"v_accvgpr_write_b32 a191, v91\n"
"v_mfma_f32_16x16x32_bf16 v[88:91], v[144:147], a[244:247], 0\n"
"v_exp_f32 v156, v156\n"
"v_mfma_f32_16x16x32_bf16 v[88:91], v[148:151], a[248:251], v[88:91]\n"
"v_accvgpr_write_b32 a140, v200\n"
"v_accvgpr_write_b32 a141, v201\n"
"v_mfma_f32_16x16x32_bf16 v[92:95], v[148:151], v[224:227], 0\n"
"v_mov_b32 v200, 0\n"
"v_mov_b32 v201, 0\n"
"v_mfma_f32_16x16x32_bf16 v[92:95], v[144:147], a[252:255], v[92:95]\n"
"v_exp_f32 v157, v157\n"
"v_mfma_f32_16x16x32_bf16 v[60:63], v[140:143], v[224:227], 0\n"
"v_exp_f32 v158, v158\n"
"v_mfma_f32_16x16x32_bf16 v[60:63], v[136:139], a[252:255], v[60:63]\n"
"v_exp_f32 v159, v159\n"
"v_mfma_f32_16x16x32_bf16 v[28:31], v[132:135], v[224:227], 0\n"
"v_cvt_pk_bf16_f32 v104, v156, v157\n"
"v_cvt_pk_bf16_f32 v105, v158, v159\n"
"v_mfma_f32_16x16x32_bf16 v[28:31], v[128:131], a[252:255], v[28:31]\n"
"ds_read_b128 v[220:223], v231 offset:17408\n"
"v_accvgpr_write_b32 a96, v112\n"
"v_mfma_f32_16x16x32_bf16 a[236:239], v[124:127], a[252:255], 0\n"
"s_add_u32 s40, s56, s52\n"
"v_exp_f32 v160, v160\n"
"v_smfmac_f32_16x16x64_bf16 v[198:201], a[240:243], v[136:143], v241\n"
"s_waitcnt lgkmcnt(1)\n"
"buffer_load_dwordx4 v240, s[16:19], s40 offen offset:1024 nt lds\n"
"v_mfma_f32_16x16x32_bf16 v[156:159], v[208:211], a[0:3], v[180:183]\n"
"v_exp_f32 v161, v161\n"
"v_mfma_f32_16x16x32_bf16 v[156:159], v[212:215], a[4:7], v[156:159]\n"
"v_exp_f32 v162, v162\n"
"v_mfma_f32_16x16x32_bf16 v[156:159], v[216:219], a[8:11], v[156:159]\n"
"s_waitcnt lgkmcnt(0)\n"
"v_exp_f32 v163, v163\n"
"v_mfma_f32_16x16x32_bf16 v[156:159], v[220:223], a[12:15], v[156:159]\n"
"v_accvgpr_write_b32 a97, v113\n"
"v_cvt_pk_bf16_f32 v112, v160, v161\n"
"v_cvt_pk_bf16_f32 v113, v162, v163\n"
"v_mfma_f32_16x16x32_bf16 v[160:163], v[216:219], a[24:27], v[184:187]\n"
"v_exp_f32 v164, v164\n"
"v_mfma_f32_16x16x32_bf16 v[160:163], v[220:223], a[28:31], v[160:163]\n"
"v_exp_f32 v165, v165\n"
"v_mfma_f32_16x16x32_bf16 v[160:163], v[208:211], a[16:19], v[160:163]\n"
"ds_read_b64_tr_b16 a[244:245], v232 offset:0\n"
"ds_read_b64_tr_b16 a[246:247], v232 offset:4096\n"
"v_exp_f32 v166, v166\n"
"v_exp_f32 v167, v167\n"
"v_mfma_f32_16x16x32_bf16 v[160:163], v[212:215], a[20:23], v[160:163]\n"
"ds_read_b64_tr_b16 a[248:249], v236 offset:0\n"
"v_accvgpr_write_b32 a100, v120\n"
"v_accvgpr_write_b32 a101, v121\n"
"v_cvt_pk_bf16_f32 v120, v164, v165\n"
"v_cvt_pk_bf16_f32 v121, v166, v167\n"
"v_mfma_f32_16x16x32_bf16 v[164:167], v[208:211], a[32:35], v[188:191]\n"
"ds_read_b64_tr_b16 a[250:251], v236 offset:4096\n"
"v_cvt_pk_bf16_f32 v0, v0, v1\n"
"v_mfma_f32_16x16x32_bf16 v[164:167], v[212:215], a[36:39], v[164:167]\n"
"ds_read_b64_tr_b16 v[224:225], v237 offset:0\n"
"v_cvt_pk_bf16_f32 v1, v2, v3\n"
"v_mfma_f32_16x16x32_bf16 v[164:167], v[216:219], a[40:43], v[164:167]\n"
"ds_read_b64_tr_b16 v[226:227], v237 offset:4096\n"
"v_cvt_pk_bf16_f32 v2, v4, v5\n"
"v_mfma_f32_16x16x32_bf16 v[164:167], v[220:223], a[44:47], v[164:167]\n"
"s_waitcnt lgkmcnt(4)\n"
"v_cvt_pk_bf16_f32 v3, v6, v7\n"
"v_cvt_pk_bf16_f32 v32, v32, v33\n"
"v_mfma_f32_16x16x32_bf16 a[208:211], v[124:127], a[244:247], 0\n"
"v_permlane16_swap_b32 v0, v2\n"
"v_permlane16_swap_b32 v1, v3\n"
"v_accvgpr_write_b32 a144, v0\n"
"v_accvgpr_write_b32 a145, v1\n"
"v_accvgpr_write_b32 a146, v2\n"
"v_accvgpr_write_b32 a147, v3\n"
"v_mfma_f32_16x16x32_bf16 v[0:3], v[128:131], a[244:247], 0\n"
"s_waitcnt lgkmcnt(2)\n"
"s_add_u32 s40, s56, s53\n"
"ds_read_b64_tr_b16 a[252:253], v233 offset:0\n"
"ds_read_b64_tr_b16 a[254:255], v233 offset:4096\n"
"v_cvt_pk_bf16_f32 v33, v34, v35\n"
"v_cvt_pk_bf16_f32 v34, v36, v37\n"
"v_cvt_pk_bf16_f32 v35, v38, v39\n"
"v_mfma_f32_16x16x32_bf16 v[0:3], v[132:135], a[248:251], v[0:3]\n"
"buffer_load_dwordx4 v240, s[16:19], s40 offen offset:2048 nt lds\n"
"v_permlane16_swap_b32 v32, v34\n"
"v_permlane16_swap_b32 v33, v35\n"
"v_accvgpr_write_b32 a160, v32\n"
"v_accvgpr_write_b32 a161, v33\n"
"v_accvgpr_write_b32 a162, v34\n"
"v_accvgpr_write_b32 a163, v35\n"
"v_mfma_f32_16x16x32_bf16 v[32:35], v[136:139], a[244:247], 0\n"
"v_cvt_pk_bf16_f32 v64, v64, v65\n"
"v_cvt_pk_bf16_f32 v65, v66, v67\n"
"v_cvt_pk_bf16_f32 v66, v68, v69\n"
"v_cvt_pk_bf16_f32 v67, v70, v71\n"
"v_mfma_f32_16x16x32_bf16 v[32:35], v[140:143], a[248:251], v[32:35]\n"
"v_permlane16_swap_b32 v64, v66\n"
"v_permlane16_swap_b32 v65, v67\n"
"v_accvgpr_write_b32 a176, v64\n"
"v_accvgpr_write_b32 a177, v65\n"
"v_accvgpr_write_b32 a178, v66\n"
"v_accvgpr_write_b32 a179, v67\n"
"v_mfma_f32_16x16x32_bf16 v[64:67], v[144:147], a[244:247], 0\n"
"v_exp_f32 v168, v168\n"
"v_mfma_f32_16x16x32_bf16 v[64:67], v[148:151], a[248:251], v[64:67]\n"
"s_waitcnt lgkmcnt(2)\n"
"v_exp_f32 v169, v169\n"
"v_mfma_f32_16x16x32_bf16 v[68:71], v[148:151], v[224:227], 0\n"
"s_waitcnt lgkmcnt(0)\n"
"v_exp_f32 v170, v170\n"
"v_mfma_f32_16x16x32_bf16 v[68:71], v[144:147], a[252:255], v[68:71]\n"
"ds_read_b128 v[208:211], v230 offset:20480\n"
"v_cvt_pk_bf16_f32 v98, v168, v169\n"
"v_mfma_f32_16x16x32_bf16 v[36:39], v[140:143], v[224:227], 0\n"
"ds_read_b128 v[212:215], v231 offset:20480\n"
"v_accvgpr_write_b32 a98, v114\n"
"v_mfma_f32_16x16x32_bf16 v[36:39], v[136:139], a[252:255], v[36:39]\n"
"ds_read_b128 v[216:219], v230 offset:21504\n"
"v_accvgpr_write_b32 a99, v115\n"
"v_mfma_f32_16x16x32_bf16 v[4:7], v[132:135], v[224:227], 0\n"
"s_add_u32 s40, s56, s54\n"
"ds_read_b128 v[220:223], v231 offset:21504\n"
"v_cvt_pk_bf16_f32 v16, v16, v17\n"
"v_mfma_f32_16x16x32_bf16 v[4:7], v[128:131], a[252:255], v[4:7]\n"
"buffer_load_dwordx4 v240, s[16:19], s40 offen offset:3072 nt lds\n"
"v_mfma_f32_16x16x32_bf16 a[212:215], v[124:127], a[252:255], 0\n"
"v_exp_f32 v171, v171\n"
"v_smfmac_f32_16x16x64_bf16 v[200:203], a[240:243], v[144:151], v241\n"
"s_waitcnt lgkmcnt(3)\n"
"v_cvt_pk_bf16_f32 v99, v170, v171\n"
"v_cvt_pk_bf16_f32 v17, v18, v19\n"
"v_mfma_f32_16x16x32_bf16 v[168:171], v[208:211], a[0:3], v[180:183]\n"
"s_waitcnt lgkmcnt(2)\n"
"v_exp_f32 v172, v172\n"
"v_mfma_f32_16x16x32_bf16 v[168:171], v[212:215], a[4:7], v[168:171]\n"
"s_waitcnt lgkmcnt(1)\n"
"v_exp_f32 v173, v173\n"
"v_mfma_f32_16x16x32_bf16 v[168:171], v[216:219], a[8:11], v[168:171]\n"
"s_waitcnt lgkmcnt(0)\n"
"v_exp_f32 v174, v174\n"
"v_exp_f32 v175, v175\n"
"v_mfma_f32_16x16x32_bf16 v[168:171], v[220:223], a[12:15], v[168:171]\n"
"v_cvt_pk_bf16_f32 v106, v172, v173\n"
"v_cvt_pk_bf16_f32 v107, v174, v175\n"
"v_mfma_f32_16x16x32_bf16 v[172:175], v[216:219], a[24:27], v[184:187]\n"
"v_exp_f32 v176, v176\n"
"v_mfma_f32_16x16x32_bf16 v[172:175], v[220:223], a[28:31], v[172:175]\n"
"v_exp_f32 v177, v177\n"
"v_mfma_f32_16x16x32_bf16 v[172:175], v[208:211], a[16:19], v[172:175]\n"
"ds_read_b64_tr_b16 a[244:245], v232 offset:1024\n"
"ds_read_b64_tr_b16 a[246:247], v232 offset:5120\n"
"v_exp_f32 v178, v178\n"
"v_exp_f32 v179, v179\n"
"v_mfma_f32_16x16x32_bf16 v[172:175], v[212:215], a[20:23], v[172:175]\n"
"s_cbranch_vccnz LPCE1_%=\nLPKE1_%=:\n"
"ds_read_b64_tr_b16 a[248:249], v236 offset:1024\n"
"v_cvt_pk_bf16_f32 v114, v176, v177\n"
"v_cvt_pk_bf16_f32 v115, v178, v179\n"
"v_mfma_f32_16x16x32_bf16 v[176:179], v[208:211], a[32:35], v[188:191]\n"
"ds_read_b64_tr_b16 a[250:251], v236 offset:5120\n"
"v_cvt_pk_bf16_f32 v18, v20, v21\n"
"v_mfma_f32_16x16x32_bf16 v[176:179], v[212:215], a[36:39], v[176:179]\n"
"s_nop 0\n"
"v_permlane16_swap_b32 v16, v18\n"
"v_mfma_f32_16x16x32_bf16 v[176:179], v[216:219], a[40:43], v[176:179]\n"
"v_cvt_pk_bf16_f32 v19, v22, v23\n"
"v_accvgpr_write_b32 a152, v16\n"
"v_mfma_f32_16x16x32_bf16 v[176:179], v[220:223], a[44:47], v[176:179]\n"
"s_waitcnt lgkmcnt(2)\n"
"v_permlane16_swap_b32 v17, v19\n"
"v_mfma_f32_16x16x32_bf16 a[224:227], v[124:127], a[244:247], 0\n"
"ds_read_b64_tr_b16 v[224:225], v237 offset:1024\n"
"ds_read_b64_tr_b16 v[226:227], v237 offset:5120\n"
"v_accvgpr_write_b32 a153, v17\n"
"v_accvgpr_write_b32 a154, v18\n"
"v_accvgpr_write_b32 a155, v19\n"
"v_mfma_f32_16x16x32_bf16 v[16:19], v[128:131], a[244:247], 0\n"
"s_waitcnt lgkmcnt(2)\n"
"s_waitcnt vmcnt(8)\n"
"ds_read_b64_tr_b16 a[252:253], v233 offset:1024\n"
"ds_read_b64_tr_b16 a[254:255], v233 offset:5120\n"
"v_cvt_pk_bf16_f32 v48, v48, v49\n"
"v_cvt_pk_bf16_f32 v49, v50, v51\n"
"v_cvt_pk_bf16_f32 v50, v52, v53\n"
"v_cvt_pk_bf16_f32 v51, v54, v55\n"
"v_mfma_f32_16x16x32_bf16 v[16:19], v[132:135], a[248:251], v[16:19]\n"
"s_barrier\n"
"v_permlane16_swap_b32 v48, v50\n"
"v_permlane16_swap_b32 v49, v51\n"
"v_accvgpr_write_b32 a168, v48\n"
"v_accvgpr_write_b32 a169, v49\n"
"v_accvgpr_write_b32 a170, v50\n"
"v_accvgpr_write_b32 a171, v51\n"
"v_mfma_f32_16x16x32_bf16 v[48:51], v[136:139], a[244:247], 0\n"
"v_cvt_pk_bf16_f32 v80, v80, v81\n"
"v_cvt_pk_bf16_f32 v81, v82, v83\n"
"v_cvt_pk_bf16_f32 v82, v84, v85\n"
"v_cvt_pk_bf16_f32 v83, v86, v87\n"
"v_mfma_f32_16x16x32_bf16 v[48:51], v[140:143], a[248:251], v[48:51]\n"
"v_permlane16_swap_b32 v80, v82\n"
"v_permlane16_swap_b32 v81, v83\n"
"v_accvgpr_write_b32 a184, v80\n"
"v_accvgpr_write_b32 a185, v81\n"
"v_accvgpr_write_b32 a186, v82\n"
"v_accvgpr_write_b32 a187, v83\n"
"v_mfma_f32_16x16x32_bf16 v[80:83], v[144:147], a[244:247], 0\n"
"v_exp_f32 v152, v152\n"
"v_mfma_f32_16x16x32_bf16 v[80:83], v[148:151], a[248:251], v[80:83]\n"
"s_waitcnt lgkmcnt(2)\n"
"v_exp_f32 v153, v153\n"
"v_mfma_f32_16x16x32_bf16 v[84:87], v[148:151], v[224:227], 0\n"
"s_waitcnt lgkmcnt(0)\n"
"ds_read_b128 v[208:211], v228 offset:32768\n"
"v_accvgpr_write_b32 a102, v122\n"
"v_mfma_f32_16x16x32_bf16 v[84:87], v[144:147], a[252:255], v[84:87]\n"
"ds_read_b128 v[212:215], v229 offset:32768\n"
"v_accvgpr_write_b32 a103, v123\n"
"v_mfma_f32_16x16x32_bf16 v[52:55], v[140:143], v[224:227], 0\n"
"ds_read_b128 v[216:219], v228 offset:33792\n"
"v_bfe_u32 v254, v242, 6, 4\n"
"v_mfma_f32_16x16x32_bf16 v[52:55], v[136:139], a[252:255], v[52:55]\n"
"ds_read_b128 v[220:223], v229 offset:33792\n"
"v_xor_b32 v249, v249, v254\n"
"v_mfma_f32_16x16x32_bf16 v[20:23], v[132:135], v[224:227], 0\n"
"ds_read_b64_tr_b16 a[244:245], v234 offset:16384\n"
"ds_read_b64_tr_b16 a[246:247], v234 offset:20480\n"
"v_mfma_f32_16x16x32_bf16 v[20:23], v[128:131], a[252:255], v[20:23]\n"
"ds_read_b64_tr_b16 a[248:249], v238 offset:16384\n"
"ds_read_b64_tr_b16 a[250:251], v238 offset:20480\n"
"v_exp_f32 v154, v154\n"
"v_exp_f32 v155, v155\n"
"v_exp_f32 v156, v156\n"
"v_exp_f32 v157, v157\n"
"v_exp_f32 v158, v158\n"
"v_exp_f32 v159, v159\n"
"v_exp_f32 v160, v160\n"
"v_exp_f32 v161, v161\n"
"v_exp_f32 v162, v162\n"
"v_exp_f32 v163, v163\n"
"v_exp_f32 v164, v164\n"
"v_exp_f32 v165, v165\n"
"v_exp_f32 v166, v166\n"
"v_exp_f32 v167, v167\n"
"v_mfma_f32_16x16x32_bf16 a[228:231], v[124:127], a[252:255], 0\n"
"s_waitcnt lgkmcnt(7)\n"
"ds_read_b64_tr_b16 a[254:255], v235 offset:20480\n"
"ds_read_b64_tr_b16 v[224:225], v239 offset:16384\n"
"v_lshl_add_u32 v249, v249, 4, s44\n"
"v_cvt_pk_bf16_f32 v122, v152, v153\n"
"v_cvt_pk_bf16_f32 v123, v154, v155\n"
"v_cvt_pk_bf16_f32 v100, v156, v157\n"
"v_cvt_pk_bf16_f32 v101, v158, v159\n"
"v_cvt_pk_bf16_f32 v108, v160, v161\n"
"v_cvt_pk_bf16_f32 v109, v162, v163\n"
"v_cvt_pk_bf16_f32 v116, v164, v165\n"
"v_cvt_pk_bf16_f32 v117, v166, v167\n"
"v_exp_f32 v169, v169\n"
"v_exp_f32 v172, v172\n"
"v_exp_f32 v173, v173\n"
"v_exp_f32 v175, v175\n"
"s_branch LAY2_%=\n"
"LAL_%=:\n"
"v_mfma_f32_16x16x32_bf16 a[224:227], v[124:127], a[244:247], a[224:227]\n"
"v_exp_f32 v169, v169\n"
"v_mfma_f32_16x16x32_bf16 v[16:19], v[128:131], a[244:247], v[16:19]\n"
"s_waitcnt vmcnt(8)\n"
"v_exp_f32 v172, v172\n"
"v_mfma_f32_16x16x32_bf16 v[16:19], v[132:135], a[248:251], v[16:19]\n"
"s_barrier\n"
"ds_read_b128 v[216:219], v228 offset:1024\n"
"v_mfma_f32_16x16x32_bf16 v[48:51], v[136:139], a[244:247], v[48:51]\n"
"ds_read_b128 v[212:215], v229 offset:0\n"
"v_cvt_pk_bf16_f32 v108, v160, v161\n"
"v_mfma_f32_16x16x32_bf16 v[48:51], v[140:143], a[248:251], v[48:51]\n"
"ds_read_b128 v[208:211], v228 offset:0\n"
"v_cvt_pk_bf16_f32 v100, v156, v157\n"
"v_mfma_f32_16x16x32_bf16 v[80:83], v[144:147], a[244:247], v[80:83]\n"
"ds_read_b128 v[220:223], v229 offset:1024\n"
"v_cvt_pk_bf16_f32 v117, v166, v167\n"
"v_mfma_f32_16x16x32_bf16 v[80:83], v[148:151], a[248:251], v[80:83]\n"
"ds_read_b64_tr_b16 a[244:245], v234 offset:49152\n"
"v_cvt_pk_bf16_f32 v109, v162, v163\n"
"v_mfma_f32_16x16x32_bf16 v[84:87], v[148:151], v[224:227], v[84:87]\n"
"ds_read_b64_tr_b16 a[248:249], v238 offset:49152\n"
"v_mfma_f32_16x16x32_bf16 v[84:87], v[144:147], a[252:255], v[84:87]\n"
"ds_read_b64_tr_b16 a[246:247], v234 offset:53248\n"
"v_mfma_f32_16x16x32_bf16 v[52:55], v[140:143], v[224:227], v[52:55]\n"
"ds_read_b64_tr_b16 a[250:251], v238 offset:53248\n"
"v_mfma_f32_16x16x32_bf16 v[52:55], v[136:139], a[252:255], v[52:55]\n"
"v_exp_f32 v175, v175\n"
"v_mfma_f32_16x16x32_bf16 v[20:23], v[132:135], v[224:227], v[20:23]\n"
"ds_read_b64_tr_b16 v[224:225], v239 offset:49152\n"
"v_mfma_f32_16x16x32_bf16 v[20:23], v[128:131], a[252:255], v[20:23]\n"
"v_exp_f32 v173, v173\n"
"v_mfma_f32_16x16x32_bf16 a[228:231], v[124:127], a[252:255], a[228:231]\n"
"s_waitcnt lgkmcnt(5)\n"
"ds_read_b64_tr_b16 a[254:255], v235 offset:53248\n"
"v_cvt_pk_bf16_f32 v116, v164, v165\n"
"v_mfma_f32_16x16x32_bf16 v[152:155], v[208:211], a[0:3], v[180:183]\n"
"v_exp_f32 v174, v174\n"
"v_mfma_f32_16x16x32_bf16 v[152:155], v[212:215], a[4:7], v[152:155]\n"
"ds_read_b64_tr_b16 a[252:253], v235 offset:49152\n"
"v_cvt_pk_bf16_f32 v111, v174, v175\n"
"v_mfma_f32_16x16x32_bf16 v[152:155], v[216:219], a[8:11], v[152:155]\n"
"s_add_u32 m0, s44, 81920\n"
"ds_read_b64_tr_b16 v[226:227], v239 offset:53248\n"
"v_mfma_f32_16x16x32_bf16 v[152:155], v[220:223], a[12:15], v[152:155]\n"
"buffer_load_dwordx4 v240, s[20:23], s56 offen nt lds\n"
"v_mfma_f32_16x16x32_bf16 v[156:159], v[216:219], a[24:27], v[184:187]\n"
"v_exp_f32 v171, v171\n"
"v_mfma_f32_16x16x32_bf16 v[156:159], v[220:223], a[28:31], v[156:159]\n"
"v_exp_f32 v168, v168\n"
"v_mfma_f32_16x16x32_bf16 v[156:159], v[208:211], a[16:19], v[156:159]\n"
"v_exp_f32 v176, v176\n"
"v_mfma_f32_16x16x32_bf16 v[156:159], v[212:215], a[20:23], v[156:159]\n"
"v_exp_f32 v177, v177\n"
"v_mfma_f32_16x16x32_bf16 v[160:163], v[208:211], a[32:35], v[188:191]\n"
"v_exp_f32 v179, v179\n"
"v_mfma_f32_16x16x32_bf16 v[160:163], v[212:215], a[36:39], v[160:163]\n"
"v_exp_f32 v170, v170\n"
"v_mfma_f32_16x16x32_bf16 v[160:163], v[216:219], a[40:43], v[160:163]\n"
"v_exp_f32 v153, v153\n"
"v_mfma_f32_16x16x32_bf16 v[160:163], v[220:223], a[44:47], v[160:163]\n"
"v_cvt_pk_bf16_f32 v102, v168, v169\n"
"v_mfma_f32_16x16x32_bf16 v[164:167], v[216:219], a[200:203], v[192:195]\n"
"ds_read_b128 v[216:219], v228 offset:5120\n"
"v_cvt_pk_bf16_f32 v103, v170, v171\n"
"v_mfma_f32_16x16x32_bf16 v[164:167], v[220:223], a[204:207], v[164:167]\n"
"v_cvt_pk_bf16_f32 v110, v172, v173\n"
"v_cvt_pk_bf16_f32 v118, v176, v177\n"
"v_mfma_f32_16x16x32_bf16 v[164:167], v[208:211], a[192:195], v[164:167]\n"
"s_add_u32 s40, s56, s52\n"
"ds_read_b128 v[208:211], v228 offset:4096\n"
"v_mfma_f32_16x16x32_bf16 v[164:167], v[212:215], a[196:199], v[164:167]\n"
"s_waitcnt lgkmcnt(2)\n"
"buffer_load_dwordx4 v240, s[20:23], s40 offen offset:1024 nt lds\n"
"v_mfma_f32_16x16x32_bf16 a[216:219], v[120:123], a[244:247], a[216:219]\n"
"v_exp_f32 v155, v155\n"
"v_mfma_f32_16x16x32_bf16 v[8:11], v[96:99], a[244:247], v[8:11]\n"
"v_exp_f32 v161, v161\n"
"v_mfma_f32_16x16x32_bf16 v[8:11], v[100:103], a[248:251], v[8:11]\n"
"v_exp_f32 v163, v163\n"
"v_mfma_f32_16x16x32_bf16 v[40:43], v[104:107], a[244:247], v[40:43]\n"
"v_exp_f32 v178, v178\n"
"v_mfma_f32_16x16x32_bf16 v[40:43], v[108:111], a[248:251], v[40:43]\n"
"ds_read_b128 v[220:223], v229 offset:5120\n"
"v_cvt_pk_bf16_f32 v119, v178, v179\n"
"v_mfma_f32_16x16x32_bf16 v[72:75], v[112:115], a[244:247], v[72:75]\n"
"ds_read_b64_tr_b16 a[244:245], v234 offset:50176\n"
"v_mfma_f32_16x16x32_bf16 v[72:75], v[116:119], a[248:251], v[72:75]\n"
"v_exp_f32 v158, v158\n"
"v_mfma_f32_16x16x32_bf16 v[76:79], v[116:119], v[224:227], v[76:79]\n"
"ds_read_b128 v[212:215], v229 offset:4096\n"
"v_mfma_f32_16x16x32_bf16 v[76:79], v[112:115], a[252:255], v[76:79]\n"
"v_exp_f32 v160, v160\n"
"v_mfma_f32_16x16x32_bf16 v[44:47], v[108:111], v[224:227], v[44:47]\n"
"s_add_u32 s40, s56, s53\n"
"ds_read_b64_tr_b16 a[248:249], v238 offset:50176\n"
"v_mfma_f32_16x16x32_bf16 v[44:47], v[104:107], a[252:255], v[44:47]\n"
"buffer_load_dwordx4 v240, s[20:23], s40 offen offset:2048 nt lds\n"
"v_mfma_f32_16x16x32_bf16 v[12:15], v[100:103], v[224:227], v[12:15]\n"
"v_exp_f32 v165, v165\n"
"v_mfma_f32_16x16x32_bf16 v[12:15], v[96:99], a[252:255], v[12:15]\n"
"v_exp_f32 v159, v159\n"
"v_mfma_f32_16x16x32_bf16 a[220:223], v[120:123], a[252:255], a[220:223]\n"
"v_exp_f32 v152, v152\n"
"v_smfmac_f32_16x16x64_bf16 v[196:199], a[240:243], v[96:103], v241\n"
"v_mfma_f32_16x16x32_bf16 v[202:205], a[240:243], v[120:123], v[202:205]\n"
"s_waitcnt lgkmcnt(1)\n"
"ds_read_b64_tr_b16 v[224:225], v239 offset:50176\n"
"v_cvt_pk_bf16_f32 v128, v152, v153\n"
"v_mfma_f32_16x16x32_bf16 v[168:171], v[216:219], a[8:11], v[180:183]\n"
"v_exp_f32 v162, v162\n"
"v_mfma_f32_16x16x32_bf16 v[168:171], v[220:223], a[12:15], v[168:171]\n"
"ds_read_b64_tr_b16 a[250:251], v238 offset:54272\n"
"v_mfma_f32_16x16x32_bf16 v[168:171], v[208:211], a[0:3], v[168:171]\n"
"ds_read_b64_tr_b16 a[252:253], v235 offset:50176\n"
"v_mfma_f32_16x16x32_bf16 v[168:171], v[212:215], a[4:7], v[168:171]\n"
"v_exp_f32 v154, v154\n"
"v_mfma_f32_16x16x32_bf16 v[172:175], v[208:211], a[16:19], v[184:187]\n"
"ds_read_b64_tr_b16 v[226:227], v239 offset:54272\n"
"v_mfma_f32_16x16x32_bf16 v[172:175], v[212:215], a[20:23], v[172:175]\n"
"ds_read_b64_tr_b16 a[246:247], v234 offset:54272\n"
"v_cvt_pk_bf16_f32 v129, v154, v155\n"
"v_mfma_f32_16x16x32_bf16 v[172:175], v[216:219], a[24:27], v[172:175]\n"
"s_add_u32 s40, s56, s54\n"
"ds_read_b64_tr_b16 a[254:255], v235 offset:54272\n"
"v_mfma_f32_16x16x32_bf16 v[172:175], v[220:223], a[28:31], v[172:175]\n"
"buffer_load_dwordx4 v240, s[20:23], s40 offen offset:3072 nt lds\n"
"v_mfma_f32_16x16x32_bf16 v[176:179], v[216:219], a[40:43], v[188:191]\n"
"s_add_u32 s55, s55, s59\n"
"v_exp_f32 v157, v157\n"
"v_mfma_f32_16x16x32_bf16 v[176:179], v[220:223], a[44:47], v[176:179]\n"
"s_add_u32 s41, s55, s57\n"
"v_exp_f32 v170, v170\n"
"v_mfma_f32_16x16x32_bf16 v[176:179], v[208:211], a[32:35], v[176:179]\n"
"s_and_b32 s41, s41, s58\n"
"v_exp_f32 v156, v156\n"
"v_mfma_f32_16x16x32_bf16 v[176:179], v[212:215], a[36:39], v[176:179]\n"
"s_andn2_b32 s56, s55, s58\n"
"v_exp_f32 v166, v166\n"
"v_mfma_f32_16x16x32_bf16 v[152:155], v[208:211], a[192:195], v[192:195]\n"
"s_or_b32 s56, s56, s41\n"
"ds_read_b128 v[208:211], v230 offset:0\n"
"v_mfma_f32_16x16x32_bf16 v[152:155], v[212:215], a[196:199], v[152:155]\n"
"v_exp_f32 v174, v174\n"
"v_mfma_f32_16x16x32_bf16 v[152:155], v[216:219], a[200:203], v[152:155]\n"
"ds_read_b128 v[212:215], v231 offset:0\n"
"v_mfma_f32_16x16x32_bf16 v[152:155], v[220:223], a[204:207], v[152:155]\n"
"s_waitcnt lgkmcnt(2)\n"
"ds_read_b128 v[216:219], v230 offset:1024\n"
"v_cvt_pk_bf16_f32 v137, v158, v159\n"
"v_mfma_f32_16x16x32_bf16 a[232:235], v[120:123], a[244:247], a[232:235]\n"
"v_exp_f32 v164, v164\n"
"v_mfma_f32_16x16x32_bf16 v[24:27], v[96:99], a[244:247], v[24:27]\n"
"s_add_u32 m0, s44, 32768\n"
"ds_read_b128 v[220:223], v231 offset:1024\n"
"v_mfma_f32_16x16x32_bf16 v[24:27], v[100:103], a[248:251], v[24:27]\n"
"buffer_load_dwordx4 v240, s[16:19], s56 offen nt lds\n"
"v_mfma_f32_16x16x32_bf16 v[56:59], v[104:107], a[244:247], v[56:59]\n"
"v_exp_f32 v167, v167\n"
"v_mfma_f32_16x16x32_bf16 v[56:59], v[108:111], a[248:251], v[56:59]\n"
"v_exp_f32 v176, v176\n"
"v_mfma_f32_16x16x32_bf16 v[88:91], v[112:115], a[244:247], v[88:91]\n"
"v_exp_f32 v153, v153\n"
"v_mfma_f32_16x16x32_bf16 v[88:91], v[116:119], a[248:251], v[88:91]\n"
"v_exp_f32 v173, v173\n"
"v_mfma_f32_16x16x32_bf16 v[92:95], v[116:119], v[224:227], v[92:95]\n"
"ds_read_b64_tr_b16 a[250:251], v236 offset:53248\n"
"v_cvt_pk_bf16_f32 v125, v166, v167\n"
"v_mfma_f32_16x16x32_bf16 v[92:95], v[112:115], a[252:255], v[92:95]\n"
"ds_read_b64_tr_b16 a[244:245], v232 offset:49152\n"
"v_cvt_pk_bf16_f32 v144, v160, v161\n"
"v_mfma_f32_16x16x32_bf16 v[60:63], v[108:111], v[224:227], v[60:63]\n"
"ds_read_b64_tr_b16 a[248:249], v236 offset:49152\n"
"v_cvt_pk_bf16_f32 v124, v164, v165\n"
"v_mfma_f32_16x16x32_bf16 v[60:63], v[104:107], a[252:255], v[60:63]\n"
"ds_read_b64_tr_b16 a[246:247], v232 offset:53248\n"
"v_cvt_pk_bf16_f32 v136, v156, v157\n"
"v_mfma_f32_16x16x32_bf16 v[28:31], v[100:103], v[224:227], v[28:31]\n"
"ds_read_b64_tr_b16 v[226:227], v237 offset:53248\n"
"v_cvt_pk_bf16_f32 v145, v162, v163\n"
"v_mfma_f32_16x16x32_bf16 v[28:31], v[96:99], a[252:255], v[28:31]\n"
"ds_read_b64_tr_b16 v[224:225], v237 offset:49152\n"
"v_mfma_f32_16x16x32_bf16 a[236:239], v[120:123], a[252:255], a[236:239]\n"
"s_add_u32 s40, s56, s52\n"
"ds_read_b64_tr_b16 a[252:253], v233 offset:49152\n"
"v_smfmac_f32_16x16x64_bf16 v[198:201], a[240:243], v[104:111], v241\n"
"s_waitcnt lgkmcnt(7)\n"
"buffer_load_dwordx4 v240, s[16:19], s40 offen offset:1024 nt lds\n"
"v_mfma_f32_16x16x32_bf16 v[156:159], v[208:211], a[0:3], v[180:183]\n"
"v_exp_f32 v169, v169\n"
"v_mfma_f32_16x16x32_bf16 v[156:159], v[212:215], a[4:7], v[156:159]\n"
"v_exp_f32 v168, v168\n"
"v_mfma_f32_16x16x32_bf16 v[156:159], v[216:219], a[8:11], v[156:159]\n"
"v_exp_f32 v175, v175\n"
"v_mfma_f32_16x16x32_bf16 v[156:159], v[220:223], a[12:15], v[156:159]\n"
"v_exp_f32 v172, v172\n"
"v_mfma_f32_16x16x32_bf16 v[160:163], v[216:219], a[24:27], v[184:187]\n"
"ds_read_b64_tr_b16 a[254:255], v233 offset:53248\n"
"v_mfma_f32_16x16x32_bf16 v[160:163], v[220:223], a[28:31], v[160:163]\n"
"v_exp_f32 v155, v155\n"
"v_mfma_f32_16x16x32_bf16 v[160:163], v[208:211], a[16:19], v[160:163]\n"
"v_exp_f32 v177, v177\n"
"v_mfma_f32_16x16x32_bf16 v[160:163], v[212:215], a[20:23], v[160:163]\n"
"v_exp_f32 v179, v179\n"
"v_mfma_f32_16x16x32_bf16 v[164:167], v[208:211], a[32:35], v[188:191]\n"
"ds_read_b128 v[208:211], v230 offset:4096\n"
"v_cvt_pk_bf16_f32 v139, v174, v175\n"
"v_mfma_f32_16x16x32_bf16 v[164:167], v[212:215], a[36:39], v[164:167]\n"
"v_exp_f32 v171, v171\n"
"v_mfma_f32_16x16x32_bf16 v[164:167], v[216:219], a[40:43], v[164:167]\n"
"ds_read_b128 v[212:215], v231 offset:4096\n"
"v_cvt_pk_bf16_f32 v138, v172, v173\n"
"v_mfma_f32_16x16x32_bf16 v[164:167], v[220:223], a[44:47], v[164:167]\n"
"s_waitcnt lgkmcnt(3)\n"
"ds_read_b128 v[220:223], v231 offset:5120\n"
"v_cvt_pk_bf16_f32 v130, v168, v169\n"
"v_mfma_f32_16x16x32_bf16 a[208:211], v[120:123], a[244:247], a[208:211]\n"
"v_exp_f32 v154, v154\n"
"v_mfma_f32_16x16x32_bf16 v[0:3], v[96:99], a[244:247], v[0:3]\n"
"s_add_u32 s40, s56, s53\n"
"ds_read_b128 v[216:219], v230 offset:5120\n"
"v_mfma_f32_16x16x32_bf16 v[0:3], v[100:103], a[248:251], v[0:3]\n"
"buffer_load_dwordx4 v240, s[16:19], s40 offen offset:2048 nt lds\n"
"v_mfma_f32_16x16x32_bf16 v[32:35], v[104:107], a[244:247], v[32:35]\n"
"v_exp_f32 v160, v160\n"
"v_mfma_f32_16x16x32_bf16 v[32:35], v[108:111], a[248:251], v[32:35]\n"
"v_exp_f32 v178, v178\n"
"v_mfma_f32_16x16x32_bf16 v[64:67], v[112:115], a[244:247], v[64:67]\n"
"v_exp_f32 v152, v152\n"
"v_mfma_f32_16x16x32_bf16 v[64:67], v[116:119], a[248:251], v[64:67]\n"
"v_exp_f32 v161, v161\n"
"v_mfma_f32_16x16x32_bf16 v[68:71], v[116:119], v[224:227], v[68:71]\n"
"s_waitcnt lgkmcnt(1)\n"
"ds_read_b64_tr_b16 a[244:245], v232 offset:50176\n"
"v_cvt_pk_bf16_f32 v147, v178, v179\n"
"v_mfma_f32_16x16x32_bf16 v[68:71], v[112:115], a[252:255], v[68:71]\n"
"ds_read_b64_tr_b16 a[248:249], v236 offset:50176\n"
"v_cvt_pk_bf16_f32 v146, v176, v177\n"
"v_mfma_f32_16x16x32_bf16 v[36:39], v[108:111], v[224:227], v[36:39]\n"
"ds_read_b64_tr_b16 a[250:251], v236 offset:54272\n"
"v_cvt_pk_bf16_f32 v131, v170, v171\n"
"v_mfma_f32_16x16x32_bf16 v[36:39], v[104:107], a[252:255], v[36:39]\n"
"v_exp_f32 v163, v163\n"
"v_mfma_f32_16x16x32_bf16 v[4:7], v[100:103], v[224:227], v[4:7]\n"
"s_add_u32 s40, s56, s54\n"
"ds_read_b64_tr_b16 a[246:247], v232 offset:54272\n"
"v_mfma_f32_16x16x32_bf16 v[4:7], v[96:99], a[252:255], v[4:7]\n"
"buffer_load_dwordx4 v240, s[16:19], s40 offen offset:3072 nt lds\n"
"v_mfma_f32_16x16x32_bf16 a[212:215], v[120:123], a[252:255], a[212:215]\n"
"v_exp_f32 v167, v167\n"
"v_smfmac_f32_16x16x64_bf16 v[200:203], a[240:243], v[112:119], v241\n"
"v_cvt_pk_bf16_f32 v126, v152, v153\n"
"v_mfma_f32_16x16x32_bf16 v[168:171], v[208:211], a[0:3], v[180:183]\n"
"v_exp_f32 v159, v159\n"
"v_mfma_f32_16x16x32_bf16 v[168:171], v[212:215], a[4:7], v[168:171]\n"
"s_waitcnt lgkmcnt(2)\n"
"v_exp_f32 v165, v165\n"
"v_mfma_f32_16x16x32_bf16 v[168:171], v[216:219], a[8:11], v[168:171]\n"
"ds_read_b64_tr_b16 a[252:253], v233 offset:50176\n"
"ds_read_b64_tr_b16 v[224:225], v237 offset:50176\n"
"v_mfma_f32_16x16x32_bf16 v[168:171], v[220:223], a[12:15], v[168:171]\n"
"ds_read_b64_tr_b16 a[254:255], v233 offset:54272\n"
"ds_read_b64_tr_b16 v[226:227], v237 offset:54272\n"
"v_mfma_f32_16x16x32_bf16 v[172:175], v[216:219], a[24:27], v[184:187]\n"
"v_exp_f32 v166, v166\n"
"v_mfma_f32_16x16x32_bf16 v[172:175], v[220:223], a[28:31], v[172:175]\n"
"v_exp_f32 v164, v164\n"
"v_mfma_f32_16x16x32_bf16 v[172:175], v[208:211], a[16:19], v[172:175]\n"
"v_exp_f32 v157, v157\n"
"v_mfma_f32_16x16x32_bf16 v[172:175], v[212:215], a[20:23], v[172:175]\n"
"s_cbranch_vccnz LPCL0_%=\nLPKL0_%=:\n"
"v_cvt_pk_bf16_f32 v127, v154, v155\n"
"v_mfma_f32_16x16x32_bf16 v[176:179], v[208:211], a[32:35], v[188:191]\n"
"v_exp_f32 v158, v158\n"
"v_mfma_f32_16x16x32_bf16 v[176:179], v[212:215], a[36:39], v[176:179]\n"
"v_exp_f32 v162, v162\n"
"v_mfma_f32_16x16x32_bf16 v[176:179], v[216:219], a[40:43], v[176:179]\n"
"v_exp_f32 v156, v156\n"
"v_mfma_f32_16x16x32_bf16 v[176:179], v[220:223], a[44:47], v[176:179]\n"
"s_waitcnt lgkmcnt(0)\n"
"v_cvt_pk_bf16_f32 v133, v158, v159\n"
"v_mfma_f32_16x16x32_bf16 a[224:227], v[120:123], a[244:247], a[224:227]\n"
"v_exp_f32 v169, v169\n"
"v_mfma_f32_16x16x32_bf16 v[16:19], v[96:99], a[244:247], v[16:19]\n"
"s_waitcnt vmcnt(8)\n"
"v_exp_f32 v172, v172\n"
"v_mfma_f32_16x16x32_bf16 v[16:19], v[100:103], a[248:251], v[16:19]\n"
"s_barrier\n"
"ds_read_b128 v[216:219], v228 offset:17408\n"
"v_mfma_f32_16x16x32_bf16 v[48:51], v[104:107], a[244:247], v[48:51]\n"
"ds_read_b128 v[212:215], v229 offset:16384\n"
"v_cvt_pk_bf16_f32 v140, v160, v161\n"
"v_mfma_f32_16x16x32_bf16 v[48:51], v[108:111], a[248:251], v[48:51]\n"
"ds_read_b128 v[208:211], v228 offset:16384\n"
"v_cvt_pk_bf16_f32 v132, v156, v157\n"
"v_mfma_f32_16x16x32_bf16 v[80:83], v[112:115], a[244:247], v[80:83]\n"
"ds_read_b128 v[220:223], v229 offset:17408\n"
"v_cvt_pk_bf16_f32 v149, v166, v167\n"
"v_mfma_f32_16x16x32_bf16 v[80:83], v[116:119], a[248:251], v[80:83]\n"
"ds_read_b64_tr_b16 a[244:245], v234 offset:0\n"
"v_cvt_pk_bf16_f32 v141, v162, v163\n"
"v_mfma_f32_16x16x32_bf16 v[84:87], v[116:119], v[224:227], v[84:87]\n"
"ds_read_b64_tr_b16 a[248:249], v238 offset:0\n"
"v_mfma_f32_16x16x32_bf16 v[84:87], v[112:115], a[252:255], v[84:87]\n"
"ds_read_b64_tr_b16 a[246:247], v234 offset:4096\n"
"v_mfma_f32_16x16x32_bf16 v[52:55], v[108:111], v[224:227], v[52:55]\n"
"ds_read_b64_tr_b16 a[250:251], v238 offset:4096\n"
"v_mfma_f32_16x16x32_bf16 v[52:55], v[104:107], a[252:255], v[52:55]\n"
"v_exp_f32 v175, v175\n"
"v_mfma_f32_16x16x32_bf16 v[20:23], v[100:103], v[224:227], v[20:23]\n"
"ds_read_b64_tr_b16 v[224:225], v239 offset:0\n"
"v_mfma_f32_16x16x32_bf16 v[20:23], v[96:99], a[252:255], v[20:23]\n"
"v_exp_f32 v173, v173\n"
"v_mfma_f32_16x16x32_bf16 a[228:231], v[120:123], a[252:255], a[228:231]\n"
"s_waitcnt lgkmcnt(5)\n"
"ds_read_b64_tr_b16 a[254:255], v235 offset:4096\n"
"v_cvt_pk_bf16_f32 v148, v164, v165\n"
"v_mfma_f32_16x16x32_bf16 v[152:155], v[208:211], a[0:3], v[180:183]\n"
"v_exp_f32 v174, v174\n"
"v_mfma_f32_16x16x32_bf16 v[152:155], v[212:215], a[4:7], v[152:155]\n"
"ds_read_b64_tr_b16 a[252:253], v235 offset:0\n"
"v_cvt_pk_bf16_f32 v143, v174, v175\n"
"v_mfma_f32_16x16x32_bf16 v[152:155], v[216:219], a[8:11], v[152:155]\n"
"s_add_u32 m0, s44, 98304\n"
"ds_read_b64_tr_b16 v[226:227], v239 offset:4096\n"
"v_mfma_f32_16x16x32_bf16 v[152:155], v[220:223], a[12:15], v[152:155]\n"
"buffer_load_dwordx4 v240, s[20:23], s56 offen nt lds\n"
"v_mfma_f32_16x16x32_bf16 v[156:159], v[216:219], a[24:27], v[184:187]\n"
"v_exp_f32 v171, v171\n"
"v_mfma_f32_16x16x32_bf16 v[156:159], v[220:223], a[28:31], v[156:159]\n"
"v_exp_f32 v168, v168\n"
"v_mfma_f32_16x16x32_bf16 v[156:159], v[208:211], a[16:19], v[156:159]\n"
"v_exp_f32 v176, v176\n"
"v_mfma_f32_16x16x32_bf16 v[156:159], v[212:215], a[20:23], v[156:159]\n"
"v_exp_f32 v177, v177\n"
"v_mfma_f32_16x16x32_bf16 v[160:163], v[208:211], a[32:35], v[188:191]\n"
"v_exp_f32 v179, v179\n"
"v_mfma_f32_16x16x32_bf16 v[160:163], v[212:215], a[36:39], v[160:163]\n"
"v_exp_f32 v170, v170\n"
"v_mfma_f32_16x16x32_bf16 v[160:163], v[216:219], a[40:43], v[160:163]\n"
"v_exp_f32 v153, v153\n"
"v_mfma_f32_16x16x32_bf16 v[160:163], v[220:223], a[44:47], v[160:163]\n"
"v_cvt_pk_bf16_f32 v134, v168, v169\n"
"v_mfma_f32_16x16x32_bf16 v[164:167], v[216:219], a[200:203], v[192:195]\n"
"ds_read_b128 v[216:219], v228 offset:21504\n"
"v_cvt_pk_bf16_f32 v135, v170, v171\n"
"v_mfma_f32_16x16x32_bf16 v[164:167], v[220:223], a[204:207], v[164:167]\n"
"v_cvt_pk_bf16_f32 v142, v172, v173\n"
"v_cvt_pk_bf16_f32 v150, v176, v177\n"
"v_mfma_f32_16x16x32_bf16 v[164:167], v[208:211], a[192:195], v[164:167]\n"
"s_add_u32 s40, s56, s52\n"
"ds_read_b128 v[208:211], v228 offset:20480\n"
"v_mfma_f32_16x16x32_bf16 v[164:167], v[212:215], a[196:199], v[164:167]\n"
"s_waitcnt lgkmcnt(2)\n"
"buffer_load_dwordx4 v240, s[20:23], s40 offen offset:1024 nt lds\n"
"v_mfma_f32_16x16x32_bf16 a[216:219], v[124:127], a[244:247], a[216:219]\n"
"v_exp_f32 v155, v155\n"
"v_mfma_f32_16x16x32_bf16 v[8:11], v[128:131], a[244:247], v[8:11]\n"
"v_exp_f32 v161, v161\n"
"v_mfma_f32_16x16x32_bf16 v[8:11], v[132:135], a[248:251], v[8:11]\n"
"v_exp_f32 v163, v163\n"
"v_mfma_f32_16x16x32_bf16 v[40:43], v[136:139], a[244:247], v[40:43]\n"
"v_exp_f32 v178, v178\n"
"v_mfma_f32_16x16x32_bf16 v[40:43], v[140:143], a[248:251], v[40:43]\n"
"ds_read_b128 v[220:223], v229 offset:21504\n"
"v_cvt_pk_bf16_f32 v151, v178, v179\n"
"v_mfma_f32_16x16x32_bf16 v[72:75], v[144:147], a[244:247], v[72:75]\n"
"ds_read_b64_tr_b16 a[244:245], v234 offset:1024\n"
"v_mfma_f32_16x16x32_bf16 v[72:75], v[148:151], a[248:251], v[72:75]\n"
"v_exp_f32 v158, v158\n"
"v_mfma_f32_16x16x32_bf16 v[76:79], v[148:151], v[224:227], v[76:79]\n"
"ds_read_b128 v[212:215], v229 offset:20480\n"
"v_mfma_f32_16x16x32_bf16 v[76:79], v[144:147], a[252:255], v[76:79]\n"
"v_exp_f32 v160, v160\n"
"v_mfma_f32_16x16x32_bf16 v[44:47], v[140:143], v[224:227], v[44:47]\n"
"s_add_u32 s40, s56, s53\n"
"ds_read_b64_tr_b16 a[248:249], v238 offset:1024\n"
"v_mfma_f32_16x16x32_bf16 v[44:47], v[136:139], a[252:255], v[44:47]\n"
"buffer_load_dwordx4 v240, s[20:23], s40 offen offset:2048 nt lds\n"
"v_mfma_f32_16x16x32_bf16 v[12:15], v[132:135], v[224:227], v[12:15]\n"
"v_exp_f32 v165, v165\n"
"v_mfma_f32_16x16x32_bf16 v[12:15], v[128:131], a[252:255], v[12:15]\n"
"v_exp_f32 v159, v159\n"
"v_mfma_f32_16x16x32_bf16 a[220:223], v[124:127], a[252:255], a[220:223]\n"
"v_exp_f32 v152, v152\n"
"v_smfmac_f32_16x16x64_bf16 v[196:199], a[240:243], v[128:135], v241\n"
"v_mfma_f32_16x16x32_bf16 v[202:205], a[240:243], v[124:127], v[202:205]\n"
"s_waitcnt lgkmcnt(1)\n"
"ds_read_b64_tr_b16 v[224:225], v239 offset:1024\n"
"v_cvt_pk_bf16_f32 v96, v152, v153\n"
"v_mfma_f32_16x16x32_bf16 v[168:171], v[216:219], a[8:11], v[180:183]\n"
"v_exp_f32 v162, v162\n"
"v_mfma_f32_16x16x32_bf16 v[168:171], v[220:223], a[12:15], v[168:171]\n"
"ds_read_b64_tr_b16 a[250:251], v238 offset:5120\n"
"v_mfma_f32_16x16x32_bf16 v[168:171], v[208:211], a[0:3], v[168:171]\n"
"ds_read_b64_tr_b16 a[252:253], v235 offset:1024\n"
"v_mfma_f32_16x16x32_bf16 v[168:171], v[212:215], a[4:7], v[168:171]\n"
"v_exp_f32 v154, v154\n"
"v_mfma_f32_16x16x32_bf16 v[172:175], v[208:211], a[16:19], v[184:187]\n"
"ds_read_b64_tr_b16 v[226:227], v239 offset:5120\n"
"v_mfma_f32_16x16x32_bf16 v[172:175], v[212:215], a[20:23], v[172:175]\n"
"ds_read_b64_tr_b16 a[246:247], v234 offset:5120\n"
"v_cvt_pk_bf16_f32 v97, v154, v155\n"
"v_mfma_f32_16x16x32_bf16 v[172:175], v[216:219], a[24:27], v[172:175]\n"
"s_add_u32 s40, s56, s54\n"
"ds_read_b64_tr_b16 a[254:255], v235 offset:5120\n"
"v_mfma_f32_16x16x32_bf16 v[172:175], v[220:223], a[28:31], v[172:175]\n"
"buffer_load_dwordx4 v240, s[20:23], s40 offen offset:3072 nt lds\n"
"v_mfma_f32_16x16x32_bf16 v[176:179], v[216:219], a[40:43], v[188:191]\n"
"s_add_u32 s55, s55, s59\n"
"v_exp_f32 v157, v157\n"
"v_mfma_f32_16x16x32_bf16 v[176:179], v[220:223], a[44:47], v[176:179]\n"
"s_add_u32 s41, s55, s57\n"
"v_exp_f32 v170, v170\n"
"v_mfma_f32_16x16x32_bf16 v[176:179], v[208:211], a[32:35], v[176:179]\n"
"s_and_b32 s41, s41, s58\n"
"v_exp_f32 v156, v156\n"
"v_mfma_f32_16x16x32_bf16 v[176:179], v[212:215], a[36:39], v[176:179]\n"
"s_andn2_b32 s56, s55, s58\n"
"v_exp_f32 v166, v166\n"
"v_mfma_f32_16x16x32_bf16 v[152:155], v[208:211], a[192:195], v[192:195]\n"
"s_or_b32 s56, s56, s41\n"
"ds_read_b128 v[208:211], v230 offset:16384\n"
"v_mfma_f32_16x16x32_bf16 v[152:155], v[212:215], a[196:199], v[152:155]\n"
"v_exp_f32 v174, v174\n"
"v_mfma_f32_16x16x32_bf16 v[152:155], v[216:219], a[200:203], v[152:155]\n"
"ds_read_b128 v[212:215], v231 offset:16384\n"
"v_mfma_f32_16x16x32_bf16 v[152:155], v[220:223], a[204:207], v[152:155]\n"
"s_waitcnt lgkmcnt(2)\n"
"ds_read_b128 v[216:219], v230 offset:17408\n"
"v_cvt_pk_bf16_f32 v105, v158, v159\n"
"v_mfma_f32_16x16x32_bf16 a[232:235], v[124:127], a[244:247], a[232:235]\n"
"v_exp_f32 v164, v164\n"
"v_mfma_f32_16x16x32_bf16 v[24:27], v[128:131], a[244:247], v[24:27]\n"
"s_add_u32 m0, s44, 49152\n"
"ds_read_b128 v[220:223], v231 offset:17408\n"
"v_mfma_f32_16x16x32_bf16 v[24:27], v[132:135], a[248:251], v[24:27]\n"
"buffer_load_dwordx4 v240, s[16:19], s56 offen nt lds\n"
"v_mfma_f32_16x16x32_bf16 v[56:59], v[136:139], a[244:247], v[56:59]\n"
"v_exp_f32 v167, v167\n"
"v_mfma_f32_16x16x32_bf16 v[56:59], v[140:143], a[248:251], v[56:59]\n"
"v_exp_f32 v176, v176\n"
"v_mfma_f32_16x16x32_bf16 v[88:91], v[144:147], a[244:247], v[88:91]\n"
"v_exp_f32 v153, v153\n"
"v_mfma_f32_16x16x32_bf16 v[88:91], v[148:151], a[248:251], v[88:91]\n"
"v_exp_f32 v173, v173\n"
"v_mfma_f32_16x16x32_bf16 v[92:95], v[148:151], v[224:227], v[92:95]\n"
"ds_read_b64_tr_b16 a[250:251], v236 offset:4096\n"
"v_cvt_pk_bf16_f32 v121, v166, v167\n"
"v_mfma_f32_16x16x32_bf16 v[92:95], v[144:147], a[252:255], v[92:95]\n"
"ds_read_b64_tr_b16 a[244:245], v232 offset:0\n"
"v_cvt_pk_bf16_f32 v112, v160, v161\n"
"v_mfma_f32_16x16x32_bf16 v[60:63], v[140:143], v[224:227], v[60:63]\n"
"ds_read_b64_tr_b16 a[248:249], v236 offset:0\n"
"v_cvt_pk_bf16_f32 v120, v164, v165\n"
"v_mfma_f32_16x16x32_bf16 v[60:63], v[136:139], a[252:255], v[60:63]\n"
"ds_read_b64_tr_b16 a[246:247], v232 offset:4096\n"
"v_cvt_pk_bf16_f32 v104, v156, v157\n"
"v_mfma_f32_16x16x32_bf16 v[28:31], v[132:135], v[224:227], v[28:31]\n"
"ds_read_b64_tr_b16 v[226:227], v237 offset:4096\n"
"v_cvt_pk_bf16_f32 v113, v162, v163\n"
"v_mfma_f32_16x16x32_bf16 v[28:31], v[128:131], a[252:255], v[28:31]\n"
"ds_read_b64_tr_b16 v[224:225], v237 offset:0\n"
"v_mfma_f32_16x16x32_bf16 a[236:239], v[124:127], a[252:255], a[236:239]\n"
"s_add_u32 s40, s56, s52\n"
"ds_read_b64_tr_b16 a[252:253], v233 offset:0\n"
"v_smfmac_f32_16x16x64_bf16 v[198:201], a[240:243], v[136:143], v241\n"
"s_waitcnt lgkmcnt(7)\n"
"buffer_load_dwordx4 v240, s[16:19], s40 offen offset:1024 nt lds\n"
"v_mfma_f32_16x16x32_bf16 v[156:159], v[208:211], a[0:3], v[180:183]\n"
"v_exp_f32 v169, v169\n"
"v_mfma_f32_16x16x32_bf16 v[156:159], v[212:215], a[4:7], v[156:159]\n"
"v_exp_f32 v168, v168\n"
"v_mfma_f32_16x16x32_bf16 v[156:159], v[216:219], a[8:11], v[156:159]\n"
"v_exp_f32 v175, v175\n"
"v_mfma_f32_16x16x32_bf16 v[156:159], v[220:223], a[12:15], v[156:159]\n"
"v_exp_f32 v172, v172\n"
"v_mfma_f32_16x16x32_bf16 v[160:163], v[216:219], a[24:27], v[184:187]\n"
"ds_read_b64_tr_b16 a[254:255], v233 offset:4096\n"
"v_mfma_f32_16x16x32_bf16 v[160:163], v[220:223], a[28:31], v[160:163]\n"
"v_exp_f32 v155, v155\n"
"v_mfma_f32_16x16x32_bf16 v[160:163], v[208:211], a[16:19], v[160:163]\n"
"v_exp_f32 v177, v177\n"
"v_mfma_f32_16x16x32_bf16 v[160:163], v[212:215], a[20:23], v[160:163]\n"
"v_exp_f32 v179, v179\n"
"v_mfma_f32_16x16x32_bf16 v[164:167], v[208:211], a[32:35], v[188:191]\n"
"ds_read_b128 v[208:211], v230 offset:20480\n"
"v_cvt_pk_bf16_f32 v107, v174, v175\n"
"v_mfma_f32_16x16x32_bf16 v[164:167], v[212:215], a[36:39], v[164:167]\n"
"v_exp_f32 v171, v171\n"
"v_mfma_f32_16x16x32_bf16 v[164:167], v[216:219], a[40:43], v[164:167]\n"
"ds_read_b128 v[212:215], v231 offset:20480\n"
"v_cvt_pk_bf16_f32 v106, v172, v173\n"
"v_mfma_f32_16x16x32_bf16 v[164:167], v[220:223], a[44:47], v[164:167]\n"
"s_waitcnt lgkmcnt(3)\n"
"ds_read_b128 v[220:223], v231 offset:21504\n"
"v_cvt_pk_bf16_f32 v98, v168, v169\n"
"v_mfma_f32_16x16x32_bf16 a[208:211], v[124:127], a[244:247], a[208:211]\n"
"v_exp_f32 v154, v154\n"
"v_mfma_f32_16x16x32_bf16 v[0:3], v[128:131], a[244:247], v[0:3]\n"
"s_add_u32 s40, s56, s53\n"
"ds_read_b128 v[216:219], v230 offset:21504\n"
"v_mfma_f32_16x16x32_bf16 v[0:3], v[132:135], a[248:251], v[0:3]\n"
"buffer_load_dwordx4 v240, s[16:19], s40 offen offset:2048 nt lds\n"
"v_mfma_f32_16x16x32_bf16 v[32:35], v[136:139], a[244:247], v[32:35]\n"
"v_exp_f32 v160, v160\n"
"v_mfma_f32_16x16x32_bf16 v[32:35], v[140:143], a[248:251], v[32:35]\n"
"v_exp_f32 v178, v178\n"
"v_mfma_f32_16x16x32_bf16 v[64:67], v[144:147], a[244:247], v[64:67]\n"
"v_exp_f32 v152, v152\n"
"v_mfma_f32_16x16x32_bf16 v[64:67], v[148:151], a[248:251], v[64:67]\n"
"v_exp_f32 v161, v161\n"
"v_mfma_f32_16x16x32_bf16 v[68:71], v[148:151], v[224:227], v[68:71]\n"
"s_waitcnt lgkmcnt(1)\n"
"ds_read_b64_tr_b16 a[244:245], v232 offset:1024\n"
"v_cvt_pk_bf16_f32 v115, v178, v179\n"
"v_mfma_f32_16x16x32_bf16 v[68:71], v[144:147], a[252:255], v[68:71]\n"
"ds_read_b64_tr_b16 a[248:249], v236 offset:1024\n"
"v_cvt_pk_bf16_f32 v114, v176, v177\n"
"v_mfma_f32_16x16x32_bf16 v[36:39], v[140:143], v[224:227], v[36:39]\n"
"ds_read_b64_tr_b16 a[250:251], v236 offset:5120\n"
"v_cvt_pk_bf16_f32 v99, v170, v171\n"
"v_mfma_f32_16x16x32_bf16 v[36:39], v[136:139], a[252:255], v[36:39]\n"
"v_exp_f32 v163, v163\n"
"v_mfma_f32_16x16x32_bf16 v[4:7], v[132:135], v[224:227], v[4:7]\n"
"s_add_u32 s40, s56, s54\n"
"ds_read_b64_tr_b16 a[246:247], v232 offset:5120\n"
"v_mfma_f32_16x16x32_bf16 v[4:7], v[128:131], a[252:255], v[4:7]\n"
"buffer_load_dwordx4 v240, s[16:19], s40 offen offset:3072 nt lds\n"
"v_mfma_f32_16x16x32_bf16 a[212:215], v[124:127], a[252:255], a[212:215]\n"
"v_exp_f32 v167, v167\n"
"v_smfmac_f32_16x16x64_bf16 v[200:203], a[240:243], v[144:151], v241\n"
"v_cvt_pk_bf16_f32 v122, v152, v153\n"
"v_mfma_f32_16x16x32_bf16 v[168:171], v[208:211], a[0:3], v[180:183]\n"
"v_exp_f32 v159, v159\n"
"v_mfma_f32_16x16x32_bf16 v[168:171], v[212:215], a[4:7], v[168:171]\n"
"s_waitcnt lgkmcnt(2)\n"
"v_exp_f32 v165, v165\n"
"v_mfma_f32_16x16x32_bf16 v[168:171], v[216:219], a[8:11], v[168:171]\n"
"ds_read_b64_tr_b16 a[252:253], v233 offset:1024\n"
"ds_read_b64_tr_b16 v[224:225], v237 offset:1024\n"
"v_mfma_f32_16x16x32_bf16 v[168:171], v[220:223], a[12:15], v[168:171]\n"
"ds_read_b64_tr_b16 a[254:255], v233 offset:5120\n"
"ds_read_b64_tr_b16 v[226:227], v237 offset:5120\n"
"v_mfma_f32_16x16x32_bf16 v[172:175], v[216:219], a[24:27], v[184:187]\n"
"v_exp_f32 v166, v166\n"
"v_mfma_f32_16x16x32_bf16 v[172:175], v[220:223], a[28:31], v[172:175]\n"
"v_exp_f32 v164, v164\n"
"v_mfma_f32_16x16x32_bf16 v[172:175], v[208:211], a[16:19], v[172:175]\n"
"v_exp_f32 v157, v157\n"
"v_mfma_f32_16x16x32_bf16 v[172:175], v[212:215], a[20:23], v[172:175]\n"
"s_cbranch_vccnz LPCL1_%=\nLPKL1_%=:\n"
"v_cvt_pk_bf16_f32 v123, v154, v155\n"
"v_mfma_f32_16x16x32_bf16 v[176:179], v[208:211], a[32:35], v[188:191]\n"
"v_exp_f32 v158, v158\n"
"v_mfma_f32_16x16x32_bf16 v[176:179], v[212:215], a[36:39], v[176:179]\n"
"v_exp_f32 v162, v162\n"
"v_mfma_f32_16x16x32_bf16 v[176:179], v[216:219], a[40:43], v[176:179]\n"
"v_exp_f32 v156, v156\n"
"v_mfma_f32_16x16x32_bf16 v[176:179], v[220:223], a[44:47], v[176:179]\n"
"s_waitcnt lgkmcnt(0)\n"
"v_cvt_pk_bf16_f32 v101, v158, v159\n"
"v_mfma_f32_16x16x32_bf16 a[224:227], v[124:127], a[244:247], a[224:227]\n"
"v_exp_f32 v169, v169\n"
"v_mfma_f32_16x16x32_bf16 v[16:19], v[128:131], a[244:247], v[16:19]\n"
"s_waitcnt vmcnt(8)\n"
"v_exp_f32 v172, v172\n"
"v_mfma_f32_16x16x32_bf16 v[16:19], v[132:135], a[248:251], v[16:19]\n"
"s_barrier\n"
"ds_read_b128 v[216:219], v228 offset:33792\n"
"v_mfma_f32_16x16x32_bf16 v[48:51], v[136:139], a[244:247], v[48:51]\n"
"ds_read_b128 v[212:215], v229 offset:32768\n"
"v_cvt_pk_bf16_f32 v108, v160, v161\n"
"v_mfma_f32_16x16x32_bf16 v[48:51], v[140:143], a[248:251], v[48:51]\n"
"ds_read_b128 v[208:211], v228 offset:32768\n"
"v_cvt_pk_bf16_f32 v100, v156, v157\n"
"v_mfma_f32_16x16x32_bf16 v[80:83], v[144:147], a[244:247], v[80:83]\n"
"ds_read_b128 v[220:223], v229 offset:33792\n"
"v_cvt_pk_bf16_f32 v117, v166, v167\n"
"v_mfma_f32_16x16x32_bf16 v[80:83], v[148:151], a[248:251], v[80:83]\n"
"ds_read_b64_tr_b16 a[244:245], v234 offset:16384\n"
"v_cvt_pk_bf16_f32 v109, v162, v163\n"
"v_mfma_f32_16x16x32_bf16 v[84:87], v[148:151], v[224:227], v[84:87]\n"
"ds_read_b64_tr_b16 a[248:249], v238 offset:16384\n"
"v_mfma_f32_16x16x32_bf16 v[84:87], v[144:147], a[252:255], v[84:87]\n"
"ds_read_b64_tr_b16 a[246:247], v234 offset:20480\n"
"v_mfma_f32_16x16x32_bf16 v[52:55], v[140:143], v[224:227], v[52:55]\n"
"ds_read_b64_tr_b16 a[250:251], v238 offset:20480\n"
"v_mfma_f32_16x16x32_bf16 v[52:55], v[136:139], a[252:255], v[52:55]\n"
"v_exp_f32 v175, v175\n"
"v_mfma_f32_16x16x32_bf16 v[20:23], v[132:135], v[224:227], v[20:23]\n"
"ds_read_b64_tr_b16 v[224:225], v239 offset:16384\n"
"v_mfma_f32_16x16x32_bf16 v[20:23], v[128:131], a[252:255], v[20:23]\n"
"v_exp_f32 v173, v173\n"
"v_mfma_f32_16x16x32_bf16 a[228:231], v[124:127], a[252:255], a[228:231]\n"
"s_waitcnt lgkmcnt(5)\n"
"ds_read_b64_tr_b16 a[254:255], v235 offset:20480\n"
"v_cvt_pk_bf16_f32 v116, v164, v165\n"
"LAY2_%=:\n"
"v_mfma_f32_16x16x32_bf16 v[152:155], v[208:211], a[0:3], v[180:183]\n"
"s_waitcnt lgkmcnt(8)\n"
"v_exp_f32 v174, v174\n"
"v_mfma_f32_16x16x32_bf16 v[152:155], v[212:215], a[4:7], v[152:155]\n"
"s_waitcnt lgkmcnt(7)\n"
"ds_read_b64_tr_b16 a[252:253], v235 offset:16384\n"
"v_cvt_pk_bf16_f32 v111, v174, v175\n"
"v_mfma_f32_16x16x32_bf16 v[152:155], v[216:219], a[8:11], v[152:155]\n"
"s_waitcnt lgkmcnt(7)\n"
"s_add_u32 m0, s44, 114688\n"
"ds_read_b64_tr_b16 v[226:227], v239 offset:20480\n"
"v_mfma_f32_16x16x32_bf16 v[152:155], v[220:223], a[12:15], v[152:155]\n"
"buffer_load_dwordx4 v240, s[20:23], s56 offen nt lds\n"
"v_mfma_f32_16x16x32_bf16 v[156:159], v[216:219], a[24:27], v[184:187]\n"
"v_exp_f32 v171, v171\n"
"v_mfma_f32_16x16x32_bf16 v[156:159], v[220:223], a[28:31], v[156:159]\n"
"v_exp_f32 v168, v168\n"
"v_mfma_f32_16x16x32_bf16 v[156:159], v[208:211], a[16:19], v[156:159]\n"
"v_exp_f32 v176, v176\n"
"v_mfma_f32_16x16x32_bf16 v[156:159], v[212:215], a[20:23], v[156:159]\n"
"v_exp_f32 v177, v177\n"
"v_mfma_f32_16x16x32_bf16 v[160:163], v[208:211], a[32:35], v[188:191]\n"
"v_exp_f32 v179, v179\n"
"v_mfma_f32_16x16x32_bf16 v[160:163], v[212:215], a[36:39], v[160:163]\n"
"v_exp_f32 v170, v170\n"
"v_mfma_f32_16x16x32_bf16 v[160:163], v[216:219], a[40:43], v[160:163]\n"
"v_exp_f32 v153, v153\n"
"v_mfma_f32_16x16x32_bf16 v[160:163], v[220:223], a[44:47], v[160:163]\n"
"v_cvt_pk_bf16_f32 v102, v168, v169\n"
"v_mfma_f32_16x16x32_bf16 v[164:167], v[216:219], a[200:203], v[192:195]\n"
"ds_read_b128 v[216:219], v228 offset:37888\n"
"v_cvt_pk_bf16_f32 v103, v170, v171\n"
"v_mfma_f32_16x16x32_bf16 v[164:167], v[220:223], a[204:207], v[164:167]\n"
"v_cvt_pk_bf16_f32 v110, v172, v173\n"
"v_cvt_pk_bf16_f32 v118, v176, v177\n"
"v_mfma_f32_16x16x32_bf16 v[164:167], v[208:211], a[192:195], v[164:167]\n"
"s_add_u32 s40, s56, s52\n"
"ds_read_b128 v[208:211], v228 offset:36864\n"
"v_mfma_f32_16x16x32_bf16 v[164:167], v[212:215], a[196:199], v[164:167]\n"
"s_waitcnt lgkmcnt(2)\n"
"buffer_load_dwordx4 v240, s[20:23], s40 offen offset:1024 nt lds\n"
"v_mfma_f32_16x16x32_bf16 a[216:219], v[120:123], a[244:247], a[216:219]\n"
"v_exp_f32 v155, v155\n"
"v_mfma_f32_16x16x32_bf16 v[8:11], v[96:99], a[244:247], v[8:11]\n"
"s_waitcnt lgkmcnt(2)\n"
"v_exp_f32 v161, v161\n"
"v_mfma_f32_16x16x32_bf16 v[8:11], v[100:103], a[248:251], v[8:11]\n"
"v_exp_f32 v163, v163\n"
"v_mfma_f32_16x16x32_bf16 v[40:43], v[104:107], a[244:247], v[40:43]\n"
"v_exp_f32 v178, v178\n"
"v_mfma_f32_16x16x32_bf16 v[40:43], v[108:111], a[248:251], v[40:43]\n"
"ds_read_b128 v[220:223], v229 offset:37888\n"
"v_cvt_pk_bf16_f32 v119, v178, v179\n"
"v_mfma_f32_16x16x32_bf16 v[72:75], v[112:115], a[244:247], v[72:75]\n"
"ds_read_b64_tr_b16 a[244:245], v234 offset:17408\n"
"v_mfma_f32_16x16x32_bf16 v[72:75], v[116:119], a[248:251], v[72:75]\n"
"v_exp_f32 v158, v158\n"
"v_mfma_f32_16x16x32_bf16 v[76:79], v[116:119], v[224:227], v[76:79]\n"
"ds_read_b128 v[212:215], v229 offset:36864\n"
"v_mfma_f32_16x16x32_bf16 v[76:79], v[112:115], a[252:255], v[76:79]\n"
"v_exp_f32 v160, v160\n"
"v_mfma_f32_16x16x32_bf16 v[44:47], v[108:111], v[224:227], v[44:47]\n"
"s_add_u32 s40, s56, s53\n"
"ds_read_b64_tr_b16 a[248:249], v238 offset:17408\n"
"v_mfma_f32_16x16x32_bf16 v[44:47], v[104:107], a[252:255], v[44:47]\n"
"buffer_load_dwordx4 v240, s[20:23], s40 offen offset:2048 nt lds\n"
"v_mfma_f32_16x16x32_bf16 v[12:15], v[100:103], v[224:227], v[12:15]\n"
"v_exp_f32 v165, v165\n"
"v_mfma_f32_16x16x32_bf16 v[12:15], v[96:99], a[252:255], v[12:15]\n"
"v_exp_f32 v159, v159\n"
"v_mfma_f32_16x16x32_bf16 a[220:223], v[120:123], a[252:255], a[220:223]\n"
"v_exp_f32 v152, v152\n"
"v_smfmac_f32_16x16x64_bf16 v[196:199], a[240:243], v[96:103], v241\n"
"v_mfma_f32_16x16x32_bf16 v[202:205], a[240:243], v[120:123], v[202:205]\n"
"s_waitcnt lgkmcnt(1)\n"
"ds_read_b64_tr_b16 v[224:225], v239 offset:17408\n"
"v_cvt_pk_bf16_f32 v128, v152, v153\n"
"v_mfma_f32_16x16x32_bf16 v[168:171], v[216:219], a[8:11], v[180:183]\n"
"v_exp_f32 v162, v162\n"
"v_mfma_f32_16x16x32_bf16 v[168:171], v[220:223], a[12:15], v[168:171]\n"
"ds_read_b64_tr_b16 a[250:251], v238 offset:21504\n"
"v_mfma_f32_16x16x32_bf16 v[168:171], v[208:211], a[0:3], v[168:171]\n"
"ds_read_b64_tr_b16 a[252:253], v235 offset:17408\n"
"v_mfma_f32_16x16x32_bf16 v[168:171], v[212:215], a[4:7], v[168:171]\n"
"v_exp_f32 v154, v154\n"
"v_mfma_f32_16x16x32_bf16 v[172:175], v[208:211], a[16:19], v[184:187]\n"
"ds_read_b64_tr_b16 v[226:227], v239 offset:21504\n"
"v_mfma_f32_16x16x32_bf16 v[172:175], v[212:215], a[20:23], v[172:175]\n"
"ds_read_b64_tr_b16 a[246:247], v234 offset:21504\n"
"v_cvt_pk_bf16_f32 v129, v154, v155\n"
"v_mfma_f32_16x16x32_bf16 v[172:175], v[216:219], a[24:27], v[172:175]\n"
"s_add_u32 s40, s56, s54\n"
"ds_read_b64_tr_b16 a[254:255], v235 offset:21504\n"
"v_mfma_f32_16x16x32_bf16 v[172:175], v[220:223], a[28:31], v[172:175]\n"
"buffer_load_dwordx4 v240, s[20:23], s40 offen offset:3072 nt lds\n"
"v_mfma_f32_16x16x32_bf16 v[176:179], v[216:219], a[40:43], v[188:191]\n"
"s_add_u32 s55, s55, s59\n"
"v_exp_f32 v157, v157\n"
"v_mfma_f32_16x16x32_bf16 v[176:179], v[220:223], a[44:47], v[176:179]\n"
"s_add_u32 s41, s55, s57\n"
"v_exp_f32 v170, v170\n"
"v_mfma_f32_16x16x32_bf16 v[176:179], v[208:211], a[32:35], v[176:179]\n"
"s_and_b32 s41, s41, s58\n"
"v_exp_f32 v156, v156\n"
"v_mfma_f32_16x16x32_bf16 v[176:179], v[212:215], a[36:39], v[176:179]\n"
"s_andn2_b32 s56, s55, s58\n"
"v_exp_f32 v166, v166\n"
"v_mfma_f32_16x16x32_bf16 v[152:155], v[208:211], a[192:195], v[192:195]\n"
"s_or_b32 s56, s56, s41\n"
"ds_read_b128 v[208:211], v230 offset:32768\n"
"v_mfma_f32_16x16x32_bf16 v[152:155], v[212:215], a[196:199], v[152:155]\n"
"s_cmp_ge_u32 s47, 15\n"
"v_exp_f32 v174, v174\n"
"v_mfma_f32_16x16x32_bf16 v[152:155], v[216:219], a[200:203], v[152:155]\n"
"s_cbranch_scc1 LSW_%=\nLSWK_%=:\n"
"ds_read_b128 v[212:215], v231 offset:32768\n"
"v_mfma_f32_16x16x32_bf16 v[152:155], v[220:223], a[204:207], v[152:155]\n"
"s_waitcnt lgkmcnt(2)\n"
"ds_read_b128 v[216:219], v230 offset:33792\n"
"v_cvt_pk_bf16_f32 v137, v158, v159\n"
"v_mfma_f32_16x16x32_bf16 a[232:235], v[120:123], a[244:247], a[232:235]\n"
"v_exp_f32 v164, v164\n"
"v_mfma_f32_16x16x32_bf16 v[24:27], v[96:99], a[244:247], v[24:27]\n"
"s_mov_b32 m0, s44\n"
"ds_read_b128 v[220:223], v231 offset:33792\n"
"v_mfma_f32_16x16x32_bf16 v[24:27], v[100:103], a[248:251], v[24:27]\n"
"buffer_load_dwordx4 v240, s[16:19], s56 offen nt lds\n"
"v_mfma_f32_16x16x32_bf16 v[56:59], v[104:107], a[244:247], v[56:59]\n"
"v_exp_f32 v167, v167\n"
"v_mfma_f32_16x16x32_bf16 v[56:59], v[108:111], a[248:251], v[56:59]\n"
"v_exp_f32 v176, v176\n"
"v_mfma_f32_16x16x32_bf16 v[88:91], v[112:115], a[244:247], v[88:91]\n"
"v_exp_f32 v153, v153\n"
"v_mfma_f32_16x16x32_bf16 v[88:91], v[116:119], a[248:251], v[88:91]\n"
"v_exp_f32 v173, v173\n"
"v_mfma_f32_16x16x32_bf16 v[92:95], v[116:119], v[224:227], v[92:95]\n"
"ds_read_b64_tr_b16 a[250:251], v236 offset:20480\n"
"v_cvt_pk_bf16_f32 v125, v166, v167\n"
"v_mfma_f32_16x16x32_bf16 v[92:95], v[112:115], a[252:255], v[92:95]\n"
"ds_read_b64_tr_b16 a[244:245], v232 offset:16384\n"
"v_cvt_pk_bf16_f32 v144, v160, v161\n"
"v_mfma_f32_16x16x32_bf16 v[60:63], v[108:111], v[224:227], v[60:63]\n"
"ds_read_b64_tr_b16 a[248:249], v236 offset:16384\n"
"v_cvt_pk_bf16_f32 v124, v164, v165\n"
"v_mfma_f32_16x16x32_bf16 v[60:63], v[104:107], a[252:255], v[60:63]\n"
"ds_read_b64_tr_b16 a[246:247], v232 offset:20480\n"
"v_cvt_pk_bf16_f32 v136, v156, v157\n"
"v_mfma_f32_16x16x32_bf16 v[28:31], v[100:103], v[224:227], v[28:31]\n"
"ds_read_b64_tr_b16 v[226:227], v237 offset:20480\n"
"v_cvt_pk_bf16_f32 v145, v162, v163\n"
"v_mfma_f32_16x16x32_bf16 v[28:31], v[96:99], a[252:255], v[28:31]\n"
"ds_read_b64_tr_b16 v[224:225], v237 offset:16384\n"
"v_mfma_f32_16x16x32_bf16 a[236:239], v[120:123], a[252:255], a[236:239]\n"
"s_add_u32 s40, s56, s52\n"
"ds_read_b64_tr_b16 a[252:253], v233 offset:16384\n"
"v_smfmac_f32_16x16x64_bf16 v[198:201], a[240:243], v[104:111], v241\n"
"s_waitcnt lgkmcnt(7)\n"
"buffer_load_dwordx4 v240, s[16:19], s40 offen offset:1024 nt lds\n"
"v_mfma_f32_16x16x32_bf16 v[156:159], v[208:211], a[0:3], v[180:183]\n"
"v_exp_f32 v169, v169\n"
"v_mfma_f32_16x16x32_bf16 v[156:159], v[212:215], a[4:7], v[156:159]\n"
"v_exp_f32 v168, v168\n"
"v_mfma_f32_16x16x32_bf16 v[156:159], v[216:219], a[8:11], v[156:159]\n"
"v_exp_f32 v175, v175\n"
"v_mfma_f32_16x16x32_bf16 v[156:159], v[220:223], a[12:15], v[156:159]\n"
"v_exp_f32 v172, v172\n"
"v_mfma_f32_16x16x32_bf16 v[160:163], v[216:219], a[24:27], v[184:187]\n"
"ds_read_b64_tr_b16 a[254:255], v233 offset:20480\n"
"v_mfma_f32_16x16x32_bf16 v[160:163], v[220:223], a[28:31], v[160:163]\n"
"v_exp_f32 v155, v155\n"
"v_mfma_f32_16x16x32_bf16 v[160:163], v[208:211], a[16:19], v[160:163]\n"
"v_exp_f32 v177, v177\n"
"v_mfma_f32_16x16x32_bf16 v[160:163], v[212:215], a[20:23], v[160:163]\n"
"v_exp_f32 v179, v179\n"
"v_mfma_f32_16x16x32_bf16 v[164:167], v[208:211], a[32:35], v[188:191]\n"
"ds_read_b128 v[208:211], v230 offset:36864\n"
"v_cvt_pk_bf16_f32 v139, v174, v175\n"
"v_mfma_f32_16x16x32_bf16 v[164:167], v[212:215], a[36:39], v[164:167]\n"
"v_exp_f32 v171, v171\n"
"v_mfma_f32_16x16x32_bf16 v[164:167], v[216:219], a[40:43], v[164:167]\n"
"ds_read_b128 v[212:215], v231 offset:36864\n"
"v_cvt_pk_bf16_f32 v138, v172, v173\n"
"v_mfma_f32_16x16x32_bf16 v[164:167], v[220:223], a[44:47], v[164:167]\n"
"s_waitcnt lgkmcnt(3)\n"
"ds_read_b128 v[220:223], v231 offset:37888\n"
"v_cvt_pk_bf16_f32 v130, v168, v169\n"
"v_mfma_f32_16x16x32_bf16 a[208:211], v[120:123], a[244:247], a[208:211]\n"
"v_exp_f32 v154, v154\n"
"v_mfma_f32_16x16x32_bf16 v[0:3], v[96:99], a[244:247], v[0:3]\n"
"s_add_u32 s40, s56, s53\n"
"ds_read_b128 v[216:219], v230 offset:37888\n"
"v_mfma_f32_16x16x32_bf16 v[0:3], v[100:103], a[248:251], v[0:3]\n"
"buffer_load_dwordx4 v240, s[16:19], s40 offen offset:2048 nt lds\n"
"v_mfma_f32_16x16x32_bf16 v[32:35], v[104:107], a[244:247], v[32:35]\n"
"v_exp_f32 v160, v160\n"
"v_mfma_f32_16x16x32_bf16 v[32:35], v[108:111], a[248:251], v[32:35]\n"
"v_exp_f32 v178, v178\n"
"v_mfma_f32_16x16x32_bf16 v[64:67], v[112:115], a[244:247], v[64:67]\n"
"v_exp_f32 v152, v152\n"
"v_mfma_f32_16x16x32_bf16 v[64:67], v[116:119], a[248:251], v[64:67]\n"
"v_exp_f32 v161, v161\n"
"v_mfma_f32_16x16x32_bf16 v[68:71], v[116:119], v[224:227], v[68:71]\n"
"s_waitcnt lgkmcnt(1)\n"
"ds_read_b64_tr_b16 a[244:245], v232 offset:17408\n"
"v_cvt_pk_bf16_f32 v147, v178, v179\n"
"v_mfma_f32_16x16x32_bf16 v[68:71], v[112:115], a[252:255], v[68:71]\n"
"ds_read_b64_tr_b16 a[248:249], v236 offset:17408\n"
"v_cvt_pk_bf16_f32 v146, v176, v177\n"
"v_mfma_f32_16x16x32_bf16 v[36:39], v[108:111], v[224:227], v[36:39]\n"
"ds_read_b64_tr_b16 a[250:251], v236 offset:21504\n"
"v_cvt_pk_bf16_f32 v131, v170, v171\n"
"v_mfma_f32_16x16x32_bf16 v[36:39], v[104:107], a[252:255], v[36:39]\n"
"v_exp_f32 v163, v163\n"
"v_mfma_f32_16x16x32_bf16 v[4:7], v[100:103], v[224:227], v[4:7]\n"
"s_add_u32 s40, s56, s54\n"
"ds_read_b64_tr_b16 a[246:247], v232 offset:21504\n"
"v_mfma_f32_16x16x32_bf16 v[4:7], v[96:99], a[252:255], v[4:7]\n"
"buffer_load_dwordx4 v240, s[16:19], s40 offen offset:3072 nt lds\n"
"v_mfma_f32_16x16x32_bf16 a[212:215], v[120:123], a[252:255], a[212:215]\n"
"v_exp_f32 v167, v167\n"
"v_smfmac_f32_16x16x64_bf16 v[200:203], a[240:243], v[112:119], v241\n"
"v_cvt_pk_bf16_f32 v126, v152, v153\n"
"v_mfma_f32_16x16x32_bf16 v[168:171], v[208:211], a[0:3], v[180:183]\n"
"v_exp_f32 v159, v159\n"
"v_mfma_f32_16x16x32_bf16 v[168:171], v[212:215], a[4:7], v[168:171]\n"
"s_waitcnt lgkmcnt(2)\n"
"v_exp_f32 v165, v165\n"
"v_mfma_f32_16x16x32_bf16 v[168:171], v[216:219], a[8:11], v[168:171]\n"
"ds_read_b64_tr_b16 a[252:253], v233 offset:17408\n"
"ds_read_b64_tr_b16 v[224:225], v237 offset:17408\n"
"v_mfma_f32_16x16x32_bf16 v[168:171], v[220:223], a[12:15], v[168:171]\n"
"ds_read_b64_tr_b16 a[254:255], v233 offset:21504\n"
"ds_read_b64_tr_b16 v[226:227], v237 offset:21504\n"
"v_mfma_f32_16x16x32_bf16 v[172:175], v[216:219], a[24:27], v[184:187]\n"
"v_exp_f32 v166, v166\n"
"v_mfma_f32_16x16x32_bf16 v[172:175], v[220:223], a[28:31], v[172:175]\n"
"v_exp_f32 v164, v164\n"
"v_mfma_f32_16x16x32_bf16 v[172:175], v[208:211], a[16:19], v[172:175]\n"
"v_exp_f32 v157, v157\n"
"v_mfma_f32_16x16x32_bf16 v[172:175], v[212:215], a[20:23], v[172:175]\n"
"s_cbranch_vccnz LPCL2_%=\nLPKL2_%=:\n"
"v_cvt_pk_bf16_f32 v127, v154, v155\n"
"v_mfma_f32_16x16x32_bf16 v[176:179], v[208:211], a[32:35], v[188:191]\n"
"v_exp_f32 v158, v158\n"
"v_mfma_f32_16x16x32_bf16 v[176:179], v[212:215], a[36:39], v[176:179]\n"
"v_exp_f32 v162, v162\n"
"v_mfma_f32_16x16x32_bf16 v[176:179], v[216:219], a[40:43], v[176:179]\n"
"v_exp_f32 v156, v156\n"
"v_mfma_f32_16x16x32_bf16 v[176:179], v[220:223], a[44:47], v[176:179]\n"
"s_waitcnt lgkmcnt(0)\n"
"v_cvt_pk_bf16_f32 v133, v158, v159\n"
"v_mfma_f32_16x16x32_bf16 a[224:227], v[120:123], a[244:247], a[224:227]\n"
"v_exp_f32 v169, v169\n"
"v_mfma_f32_16x16x32_bf16 v[16:19], v[96:99], a[244:247], v[16:19]\n"
"s_waitcnt vmcnt(8)\n"
"v_exp_f32 v172, v172\n"
"v_mfma_f32_16x16x32_bf16 v[16:19], v[100:103], a[248:251], v[16:19]\n"
"s_barrier\n"
"ds_read_b128 v[216:219], v228 offset:50176\n"
"v_mfma_f32_16x16x32_bf16 v[48:51], v[104:107], a[244:247], v[48:51]\n"
"ds_read_b128 v[212:215], v229 offset:49152\n"
"v_cvt_pk_bf16_f32 v140, v160, v161\n"
"v_mfma_f32_16x16x32_bf16 v[48:51], v[108:111], a[248:251], v[48:51]\n"
"ds_read_b128 v[208:211], v228 offset:49152\n"
"v_cvt_pk_bf16_f32 v132, v156, v157\n"
"v_mfma_f32_16x16x32_bf16 v[80:83], v[112:115], a[244:247], v[80:83]\n"
"ds_read_b128 v[220:223], v229 offset:50176\n"
"v_cvt_pk_bf16_f32 v149, v166, v167\n"
"v_mfma_f32_16x16x32_bf16 v[80:83], v[116:119], a[248:251], v[80:83]\n"
"ds_read_b64_tr_b16 a[244:245], v234 offset:32768\n"
"v_cvt_pk_bf16_f32 v141, v162, v163\n"
"v_mfma_f32_16x16x32_bf16 v[84:87], v[116:119], v[224:227], v[84:87]\n"
"ds_read_b64_tr_b16 a[248:249], v238 offset:32768\n"
"v_mfma_f32_16x16x32_bf16 v[84:87], v[112:115], a[252:255], v[84:87]\n"
"ds_read_b64_tr_b16 a[246:247], v234 offset:36864\n"
"v_mfma_f32_16x16x32_bf16 v[52:55], v[108:111], v[224:227], v[52:55]\n"
"ds_read_b64_tr_b16 a[250:251], v238 offset:36864\n"
"v_mfma_f32_16x16x32_bf16 v[52:55], v[104:107], a[252:255], v[52:55]\n"
"v_exp_f32 v175, v175\n"
"v_mfma_f32_16x16x32_bf16 v[20:23], v[100:103], v[224:227], v[20:23]\n"
"ds_read_b64_tr_b16 v[224:225], v239 offset:32768\n"
"v_mfma_f32_16x16x32_bf16 v[20:23], v[96:99], a[252:255], v[20:23]\n"
"v_exp_f32 v173, v173\n"
"v_mfma_f32_16x16x32_bf16 a[228:231], v[120:123], a[252:255], a[228:231]\n"
"s_waitcnt lgkmcnt(5)\n"
"ds_read_b64_tr_b16 a[254:255], v235 offset:36864\n"
"v_cvt_pk_bf16_f32 v148, v164, v165\n"
"v_mfma_f32_16x16x32_bf16 v[152:155], v[208:211], a[0:3], v[180:183]\n"
"v_exp_f32 v174, v174\n"
"v_mfma_f32_16x16x32_bf16 v[152:155], v[212:215], a[4:7], v[152:155]\n"
"ds_read_b64_tr_b16 a[252:253], v235 offset:32768\n"
"v_cvt_pk_bf16_f32 v143, v174, v175\n"
"v_mfma_f32_16x16x32_bf16 v[152:155], v[216:219], a[8:11], v[152:155]\n"
"s_add_u32 m0, s44, 65536\n"
"ds_read_b64_tr_b16 v[226:227], v239 offset:36864\n"
"v_mfma_f32_16x16x32_bf16 v[152:155], v[220:223], a[12:15], v[152:155]\n"
"buffer_load_dwordx4 v240, s[20:23], s56 offen nt lds\n"
"v_mfma_f32_16x16x32_bf16 v[156:159], v[216:219], a[24:27], v[184:187]\n"
"v_exp_f32 v171, v171\n"
"v_mfma_f32_16x16x32_bf16 v[156:159], v[220:223], a[28:31], v[156:159]\n"
"v_exp_f32 v168, v168\n"
"v_mfma_f32_16x16x32_bf16 v[156:159], v[208:211], a[16:19], v[156:159]\n"
"v_exp_f32 v176, v176\n"
"v_mfma_f32_16x16x32_bf16 v[156:159], v[212:215], a[20:23], v[156:159]\n"
"v_exp_f32 v177, v177\n"
"v_mfma_f32_16x16x32_bf16 v[160:163], v[208:211], a[32:35], v[188:191]\n"
"v_exp_f32 v179, v179\n"
"v_mfma_f32_16x16x32_bf16 v[160:163], v[212:215], a[36:39], v[160:163]\n"
"v_exp_f32 v170, v170\n"
"v_mfma_f32_16x16x32_bf16 v[160:163], v[216:219], a[40:43], v[160:163]\n"
"v_exp_f32 v153, v153\n"
"v_mfma_f32_16x16x32_bf16 v[160:163], v[220:223], a[44:47], v[160:163]\n"
"v_cvt_pk_bf16_f32 v134, v168, v169\n"
"v_mfma_f32_16x16x32_bf16 v[164:167], v[216:219], a[200:203], v[192:195]\n"
"ds_read_b128 v[216:219], v228 offset:54272\n"
"v_cvt_pk_bf16_f32 v135, v170, v171\n"
"v_mfma_f32_16x16x32_bf16 v[164:167], v[220:223], a[204:207], v[164:167]\n"
"v_cvt_pk_bf16_f32 v142, v172, v173\n"
"v_cvt_pk_bf16_f32 v150, v176, v177\n"
"v_mfma_f32_16x16x32_bf16 v[164:167], v[208:211], a[192:195], v[164:167]\n"
"s_add_u32 s40, s56, s52\n"
"ds_read_b128 v[208:211], v228 offset:53248\n"
"v_mfma_f32_16x16x32_bf16 v[164:167], v[212:215], a[196:199], v[164:167]\n"
"s_waitcnt lgkmcnt(2)\n"
"buffer_load_dwordx4 v240, s[20:23], s40 offen offset:1024 nt lds\n"
"v_mfma_f32_16x16x32_bf16 a[216:219], v[124:127], a[244:247], a[216:219]\n"
"v_exp_f32 v155, v155\n"
"v_mfma_f32_16x16x32_bf16 v[8:11], v[128:131], a[244:247], v[8:11]\n"
"v_exp_f32 v161, v161\n"
"v_mfma_f32_16x16x32_bf16 v[8:11], v[132:135], a[248:251], v[8:11]\n"
"v_exp_f32 v163, v163\n"
"v_mfma_f32_16x16x32_bf16 v[40:43], v[136:139], a[244:247], v[40:43]\n"
"v_exp_f32 v178, v178\n"
"v_mfma_f32_16x16x32_bf16 v[40:43], v[140:143], a[248:251], v[40:43]\n"
"ds_read_b128 v[220:223], v229 offset:54272\n"
"v_cvt_pk_bf16_f32 v151, v178, v179\n"
"v_mfma_f32_16x16x32_bf16 v[72:75], v[144:147], a[244:247], v[72:75]\n"
"ds_read_b64_tr_b16 a[244:245], v234 offset:33792\n"
"v_mfma_f32_16x16x32_bf16 v[72:75], v[148:151], a[248:251], v[72:75]\n"
"v_exp_f32 v158, v158\n"
"v_mfma_f32_16x16x32_bf16 v[76:79], v[148:151], v[224:227], v[76:79]\n"
"ds_read_b128 v[212:215], v229 offset:53248\n"
"v_mfma_f32_16x16x32_bf16 v[76:79], v[144:147], a[252:255], v[76:79]\n"
"v_exp_f32 v160, v160\n"
"v_mfma_f32_16x16x32_bf16 v[44:47], v[140:143], v[224:227], v[44:47]\n"
"s_add_u32 s40, s56, s53\n"
"ds_read_b64_tr_b16 a[248:249], v238 offset:33792\n"
"v_mfma_f32_16x16x32_bf16 v[44:47], v[136:139], a[252:255], v[44:47]\n"
"buffer_load_dwordx4 v240, s[20:23], s40 offen offset:2048 nt lds\n"
"v_mfma_f32_16x16x32_bf16 v[12:15], v[132:135], v[224:227], v[12:15]\n"
"v_exp_f32 v165, v165\n"
"v_mfma_f32_16x16x32_bf16 v[12:15], v[128:131], a[252:255], v[12:15]\n"
"v_exp_f32 v159, v159\n"
"v_mfma_f32_16x16x32_bf16 a[220:223], v[124:127], a[252:255], a[220:223]\n"
"v_exp_f32 v152, v152\n"
"v_smfmac_f32_16x16x64_bf16 v[196:199], a[240:243], v[128:135], v241\n"
"v_mfma_f32_16x16x32_bf16 v[202:205], a[240:243], v[124:127], v[202:205]\n"
"s_waitcnt lgkmcnt(1)\n"
"ds_read_b64_tr_b16 v[224:225], v239 offset:33792\n"
"v_cvt_pk_bf16_f32 v96, v152, v153\n"
"v_mfma_f32_16x16x32_bf16 v[168:171], v[216:219], a[8:11], v[180:183]\n"
"v_exp_f32 v162, v162\n"
"v_mfma_f32_16x16x32_bf16 v[168:171], v[220:223], a[12:15], v[168:171]\n"
"ds_read_b64_tr_b16 a[250:251], v238 offset:37888\n"
"v_mfma_f32_16x16x32_bf16 v[168:171], v[208:211], a[0:3], v[168:171]\n"
"ds_read_b64_tr_b16 a[252:253], v235 offset:33792\n"
"v_mfma_f32_16x16x32_bf16 v[168:171], v[212:215], a[4:7], v[168:171]\n"
"v_exp_f32 v154, v154\n"
"v_mfma_f32_16x16x32_bf16 v[172:175], v[208:211], a[16:19], v[184:187]\n"
"ds_read_b64_tr_b16 v[226:227], v239 offset:37888\n"
"v_mfma_f32_16x16x32_bf16 v[172:175], v[212:215], a[20:23], v[172:175]\n"
"ds_read_b64_tr_b16 a[246:247], v234 offset:37888\n"
"v_cvt_pk_bf16_f32 v97, v154, v155\n"
"v_mfma_f32_16x16x32_bf16 v[172:175], v[216:219], a[24:27], v[172:175]\n"
"s_add_u32 s40, s56, s54\n"
"ds_read_b64_tr_b16 a[254:255], v235 offset:37888\n"
"v_mfma_f32_16x16x32_bf16 v[172:175], v[220:223], a[28:31], v[172:175]\n"
"buffer_load_dwordx4 v240, s[20:23], s40 offen offset:3072 nt lds\n"
"v_mfma_f32_16x16x32_bf16 v[176:179], v[216:219], a[40:43], v[188:191]\n"
"s_add_u32 s55, s55, s59\n"
"v_exp_f32 v157, v157\n"
"v_mfma_f32_16x16x32_bf16 v[176:179], v[220:223], a[44:47], v[176:179]\n"
"s_add_u32 s41, s55, s57\n"
"v_exp_f32 v170, v170\n"
"v_mfma_f32_16x16x32_bf16 v[176:179], v[208:211], a[32:35], v[176:179]\n"
"s_and_b32 s41, s41, s58\n"
"v_exp_f32 v156, v156\n"
"v_mfma_f32_16x16x32_bf16 v[176:179], v[212:215], a[36:39], v[176:179]\n"
"s_andn2_b32 s56, s55, s58\n"
"v_exp_f32 v166, v166\n"
"v_mfma_f32_16x16x32_bf16 v[152:155], v[208:211], a[192:195], v[192:195]\n"
"s_or_b32 s56, s56, s41\n"
"ds_read_b128 v[208:211], v230 offset:49152\n"
"v_mfma_f32_16x16x32_bf16 v[152:155], v[212:215], a[196:199], v[152:155]\n"
"v_exp_f32 v174, v174\n"
"v_mfma_f32_16x16x32_bf16 v[152:155], v[216:219], a[200:203], v[152:155]\n"
"ds_read_b128 v[212:215], v231 offset:49152\n"
"v_mfma_f32_16x16x32_bf16 v[152:155], v[220:223], a[204:207], v[152:155]\n"
"s_waitcnt lgkmcnt(2)\n"
"ds_read_b128 v[216:219], v230 offset:50176\n"
"v_cvt_pk_bf16_f32 v105, v158, v159\n"
"v_mfma_f32_16x16x32_bf16 a[232:235], v[124:127], a[244:247], a[232:235]\n"
"v_exp_f32 v164, v164\n"
"v_mfma_f32_16x16x32_bf16 v[24:27], v[128:131], a[244:247], v[24:27]\n"
"s_add_u32 m0, s44, 16384\n"
"ds_read_b128 v[220:223], v231 offset:50176\n"
"v_mfma_f32_16x16x32_bf16 v[24:27], v[132:135], a[248:251], v[24:27]\n"
"buffer_load_dwordx4 v240, s[16:19], s56 offen nt lds\n"
"v_mfma_f32_16x16x32_bf16 v[56:59], v[136:139], a[244:247], v[56:59]\n"
"v_exp_f32 v167, v167\n"
"v_mfma_f32_16x16x32_bf16 v[56:59], v[140:143], a[248:251], v[56:59]\n"
"v_exp_f32 v176, v176\n"
"v_mfma_f32_16x16x32_bf16 v[88:91], v[144:147], a[244:247], v[88:91]\n"
"v_exp_f32 v153, v153\n"
"v_mfma_f32_16x16x32_bf16 v[88:91], v[148:151], a[248:251], v[88:91]\n"
"v_exp_f32 v173, v173\n"
"v_mfma_f32_16x16x32_bf16 v[92:95], v[148:151], v[224:227], v[92:95]\n"
"ds_read_b64_tr_b16 a[250:251], v236 offset:36864\n"
"v_cvt_pk_bf16_f32 v121, v166, v167\n"
"v_mfma_f32_16x16x32_bf16 v[92:95], v[144:147], a[252:255], v[92:95]\n"
"ds_read_b64_tr_b16 a[244:245], v232 offset:32768\n"
"v_cvt_pk_bf16_f32 v112, v160, v161\n"
"v_mfma_f32_16x16x32_bf16 v[60:63], v[140:143], v[224:227], v[60:63]\n"
"ds_read_b64_tr_b16 a[248:249], v236 offset:32768\n"
"v_cvt_pk_bf16_f32 v120, v164, v165\n"
"v_mfma_f32_16x16x32_bf16 v[60:63], v[136:139], a[252:255], v[60:63]\n"
"ds_read_b64_tr_b16 a[246:247], v232 offset:36864\n"
"v_cvt_pk_bf16_f32 v104, v156, v157\n"
"v_mfma_f32_16x16x32_bf16 v[28:31], v[132:135], v[224:227], v[28:31]\n"
"ds_read_b64_tr_b16 v[226:227], v237 offset:36864\n"
"v_cvt_pk_bf16_f32 v113, v162, v163\n"
"v_mfma_f32_16x16x32_bf16 v[28:31], v[128:131], a[252:255], v[28:31]\n"
"ds_read_b64_tr_b16 v[224:225], v237 offset:32768\n"
"v_mfma_f32_16x16x32_bf16 a[236:239], v[124:127], a[252:255], a[236:239]\n"
"s_add_u32 s40, s56, s52\n"
"ds_read_b64_tr_b16 a[252:253], v233 offset:32768\n"
"v_smfmac_f32_16x16x64_bf16 v[198:201], a[240:243], v[136:143], v241\n"
"s_waitcnt lgkmcnt(7)\n"
"buffer_load_dwordx4 v240, s[16:19], s40 offen offset:1024 nt lds\n"
"v_mfma_f32_16x16x32_bf16 v[156:159], v[208:211], a[0:3], v[180:183]\n"
"v_exp_f32 v169, v169\n"
"v_mfma_f32_16x16x32_bf16 v[156:159], v[212:215], a[4:7], v[156:159]\n"
"v_exp_f32 v168, v168\n"
"v_mfma_f32_16x16x32_bf16 v[156:159], v[216:219], a[8:11], v[156:159]\n"
"v_exp_f32 v175, v175\n"
"v_mfma_f32_16x16x32_bf16 v[156:159], v[220:223], a[12:15], v[156:159]\n"
"v_exp_f32 v172, v172\n"
"v_mfma_f32_16x16x32_bf16 v[160:163], v[216:219], a[24:27], v[184:187]\n"
"ds_read_b64_tr_b16 a[254:255], v233 offset:36864\n"
"v_mfma_f32_16x16x32_bf16 v[160:163], v[220:223], a[28:31], v[160:163]\n"
"v_exp_f32 v155, v155\n"
"v_mfma_f32_16x16x32_bf16 v[160:163], v[208:211], a[16:19], v[160:163]\n"
"v_exp_f32 v177, v177\n"
"v_mfma_f32_16x16x32_bf16 v[160:163], v[212:215], a[20:23], v[160:163]\n"
"v_exp_f32 v179, v179\n"
"v_mfma_f32_16x16x32_bf16 v[164:167], v[208:211], a[32:35], v[188:191]\n"
"ds_read_b128 v[208:211], v230 offset:53248\n"
"v_cvt_pk_bf16_f32 v107, v174, v175\n"
"v_mfma_f32_16x16x32_bf16 v[164:167], v[212:215], a[36:39], v[164:167]\n"
"v_exp_f32 v171, v171\n"
"v_mfma_f32_16x16x32_bf16 v[164:167], v[216:219], a[40:43], v[164:167]\n"
"ds_read_b128 v[212:215], v231 offset:53248\n"
"v_cvt_pk_bf16_f32 v106, v172, v173\n"
"v_mfma_f32_16x16x32_bf16 v[164:167], v[220:223], a[44:47], v[164:167]\n"
"s_waitcnt lgkmcnt(3)\n"
"ds_read_b128 v[220:223], v231 offset:54272\n"
"v_cvt_pk_bf16_f32 v98, v168, v169\n"
"v_mfma_f32_16x16x32_bf16 a[208:211], v[124:127], a[244:247], a[208:211]\n"
"v_exp_f32 v154, v154\n"
"v_mfma_f32_16x16x32_bf16 v[0:3], v[128:131], a[244:247], v[0:3]\n"
"s_add_u32 s40, s56, s53\n"
"ds_read_b128 v[216:219], v230 offset:54272\n"
"v_mfma_f32_16x16x32_bf16 v[0:3], v[132:135], a[248:251], v[0:3]\n"
"buffer_load_dwordx4 v240, s[16:19], s40 offen offset:2048 nt lds\n"
"v_mfma_f32_16x16x32_bf16 v[32:35], v[136:139], a[244:247], v[32:35]\n"
"v_exp_f32 v160, v160\n"
"v_mfma_f32_16x16x32_bf16 v[32:35], v[140:143], a[248:251], v[32:35]\n"
"v_exp_f32 v178, v178\n"
"v_mfma_f32_16x16x32_bf16 v[64:67], v[144:147], a[244:247], v[64:67]\n"
"v_exp_f32 v152, v152\n"
"v_mfma_f32_16x16x32_bf16 v[64:67], v[148:151], a[248:251], v[64:67]\n"
"v_exp_f32 v161, v161\n"
"v_mfma_f32_16x16x32_bf16 v[68:71], v[148:151], v[224:227], v[68:71]\n"
"s_waitcnt lgkmcnt(1)\n"
"ds_read_b64_tr_b16 a[244:245], v232 offset:33792\n"
"v_cvt_pk_bf16_f32 v115, v178, v179\n"
"v_mfma_f32_16x16x32_bf16 v[68:71], v[144:147], a[252:255], v[68:71]\n"
"ds_read_b64_tr_b16 a[248:249], v236 offset:33792\n"
"v_cvt_pk_bf16_f32 v114, v176, v177\n"
"v_mfma_f32_16x16x32_bf16 v[36:39], v[140:143], v[224:227], v[36:39]\n"
"ds_read_b64_tr_b16 a[250:251], v236 offset:37888\n"
"v_cvt_pk_bf16_f32 v99, v170, v171\n"
"v_mfma_f32_16x16x32_bf16 v[36:39], v[136:139], a[252:255], v[36:39]\n"
"v_exp_f32 v163, v163\n"
"v_mfma_f32_16x16x32_bf16 v[4:7], v[132:135], v[224:227], v[4:7]\n"
"s_add_u32 s40, s56, s54\n"
"ds_read_b64_tr_b16 a[246:247], v232 offset:37888\n"
"v_mfma_f32_16x16x32_bf16 v[4:7], v[128:131], a[252:255], v[4:7]\n"
"buffer_load_dwordx4 v240, s[16:19], s40 offen offset:3072 nt lds\n"
"v_mfma_f32_16x16x32_bf16 a[212:215], v[124:127], a[252:255], a[212:215]\n"
"v_exp_f32 v167, v167\n"
"v_smfmac_f32_16x16x64_bf16 v[200:203], a[240:243], v[144:151], v241\n"
"v_cvt_pk_bf16_f32 v122, v152, v153\n"
"v_mfma_f32_16x16x32_bf16 v[168:171], v[208:211], a[0:3], v[180:183]\n"
"v_exp_f32 v159, v159\n"
"v_mfma_f32_16x16x32_bf16 v[168:171], v[212:215], a[4:7], v[168:171]\n"
"s_waitcnt lgkmcnt(2)\n"
"v_exp_f32 v165, v165\n"
"v_mfma_f32_16x16x32_bf16 v[168:171], v[216:219], a[8:11], v[168:171]\n"
"ds_read_b64_tr_b16 a[252:253], v233 offset:33792\n"
"ds_read_b64_tr_b16 v[224:225], v237 offset:33792\n"
"v_mfma_f32_16x16x32_bf16 v[168:171], v[220:223], a[12:15], v[168:171]\n"
"ds_read_b64_tr_b16 a[254:255], v233 offset:37888\n"
"ds_read_b64_tr_b16 v[226:227], v237 offset:37888\n"
"v_mfma_f32_16x16x32_bf16 v[172:175], v[216:219], a[24:27], v[184:187]\n"
"v_exp_f32 v166, v166\n"
"v_mfma_f32_16x16x32_bf16 v[172:175], v[220:223], a[28:31], v[172:175]\n"
"v_exp_f32 v164, v164\n"
"v_mfma_f32_16x16x32_bf16 v[172:175], v[208:211], a[16:19], v[172:175]\n"
"v_exp_f32 v157, v157\n"
"v_mfma_f32_16x16x32_bf16 v[172:175], v[212:215], a[20:23], v[172:175]\n"
"s_cbranch_vccnz LPCL3_%=\nLPKL3_%=:\n"
"v_cvt_pk_bf16_f32 v123, v154, v155\n"
"v_mfma_f32_16x16x32_bf16 v[176:179], v[208:211], a[32:35], v[188:191]\n"
"s_add_u32 s47, s47, 1\n"
"v_exp_f32 v158, v158\n"
"v_mfma_f32_16x16x32_bf16 v[176:179], v[212:215], a[36:39], v[176:179]\n"
"v_exp_f32 v162, v162\n"
"v_mfma_f32_16x16x32_bf16 v[176:179], v[216:219], a[40:43], v[176:179]\n"
"s_and_b32 s40, s47, 3\n"
"v_exp_f32 v156, v156\n"
"v_mfma_f32_16x16x32_bf16 v[176:179], v[220:223], a[44:47], v[176:179]\n"
"s_waitcnt lgkmcnt(0)\n"
"v_cvt_pk_bf16_f32 v101, v158, v159\n"
"s_cbranch_scc1 LAL_%=\n"
"s_cmp_eq_u32 s47, 8\n"
"s_cbranch_scc1 LTH1_%=\n"
"s_cmp_eq_u32 s47, 12\n"
"s_cbranch_scc1 LTH2_%=\n"
"s_cmp_eq_u32 s47, 16\n"
"s_cbranch_scc1 LAX_%=\n"
"LTH0_%=:\n"
"v_mfma_f32_16x16x32_bf16 a[224:227], v[124:127], a[244:247], a[224:227]\n"
"v_exp_f32 v170, v170\n"
"v_mfma_f32_16x16x32_bf16 v[16:19], v[128:131], a[244:247], v[16:19]\n"
"s_waitcnt vmcnt(8)\n"
"v_exp_f32 v168, v168\n"
"v_mfma_f32_16x16x32_bf16 v[16:19], v[132:135], a[248:251], v[16:19]\n"
"s_barrier\n"
"v_mfma_f32_16x16x32_bf16 v[48:51], v[136:139], a[244:247], v[48:51]\n"
"v_exp_f32 v173, v173\n"
"v_mfma_f32_16x16x32_bf16 v[48:51], v[140:143], a[248:251], v[48:51]\n"
"v_exp_f32 v171, v171\n"
"v_mfma_f32_16x16x32_bf16 v[80:83], v[144:147], a[244:247], v[80:83]\n"
"ds_read_b64_tr_b16 a[244:245], v234 offset:49152\n"
"ds_read_b64_tr_b16 a[246:247], v234 offset:53248\n"
"v_mfma_f32_16x16x32_bf16 v[80:83], v[148:151], a[248:251], v[80:83]\n"
"v_exp_f32 v179, v179\n"
"v_mfma_f32_16x16x32_bf16 v[84:87], v[148:151], v[224:227], v[84:87]\n"
"ds_read_b64_tr_b16 a[250:251], v238 offset:53248\n"
"v_cvt_pk_bf16_f32 v100, v156, v157\n"
"v_mfma_f32_16x16x32_bf16 v[84:87], v[144:147], a[252:255], v[84:87]\n"
"v_exp_f32 v169, v169\n"
"v_exp_f32 v172, v172\n"
"v_exp_f32 v175, v175\n"
"v_exp_f32 v176, v176\n"
"v_exp_f32 v178, v178\n"
"v_mfma_f32_16x16x32_bf16 v[52:55], v[140:143], v[224:227], v[52:55]\n"
"ds_read_b64_tr_b16 a[248:249], v238 offset:49152\n"
"v_cvt_pk_bf16_f32 v117, v166, v167\n"
"v_mfma_f32_16x16x32_bf16 v[52:55], v[136:139], a[252:255], v[52:55]\n"
"v_exp_f32 v174, v174\n"
"v_mfma_f32_16x16x32_bf16 v[20:23], v[132:135], v[224:227], v[20:23]\n"
"ds_read_b64_tr_b16 v[224:225], v239 offset:49152\n"
"ds_read_b64_tr_b16 v[226:227], v239 offset:53248\n"
"v_mfma_f32_16x16x32_bf16 v[20:23], v[128:131], a[252:255], v[20:23]\n"
"v_cvt_pk_bf16_f32 v102, v168, v169\n"
"v_cvt_pk_bf16_f32 v103, v170, v171\n"
"v_cvt_pk_bf16_f32 v111, v174, v175\n"
"v_exp_f32 v177, v177\n"
"v_mfma_f32_16x16x32_bf16 a[228:231], v[124:127], a[252:255], a[228:231]\n"
"s_waitcnt lgkmcnt(4)\n"
"ds_read_b64_tr_b16 a[254:255], v235 offset:53248\n"
"v_cvt_pk_bf16_f32 v110, v172, v173\n"
"v_mfma_f32_16x16x32_bf16 a[216:219], v[120:123], a[244:247], a[216:219]\n"
"s_add_u32 m0, s44, 81920\n"
"ds_read_b64_tr_b16 a[252:253], v235 offset:49152\n"
"v_cvt_pk_bf16_f32 v116, v164, v165\n"
"v_smfmac_f32_16x16x64_bf16 v[196:199], a[240:243], v[96:103], v241\n"
"buffer_load_dwordx4 v240, s[20:23], s56 offen nt lds\n"
"v_mfma_f32_16x16x32_bf16 v[8:11], v[96:99], a[244:247], v[8:11]\n"
"s_waitcnt lgkmcnt(4)\n"
"v_cvt_pk_bf16_f32 v118, v176, v177\n"
"v_cvt_pk_bf16_f32 v119, v178, v179\n"
"v_mfma_f32_16x16x32_bf16 v[8:11], v[100:103], a[248:251], v[8:11]\n"
"v_cvt_pk_bf16_f32 v108, v160, v161\n"
"v_and_b32 v245, 0x3f0, v242\n"
"v_smfmac_f32_16x16x64_bf16 v[200:203], a[240:243], v[112:119], v241\n"
"ds_read_b128 v[212:215], v229 offset:0\n"
"v_cvt_pk_bf16_f32 v109, v162, v163\n"
"v_mfma_f32_16x16x32_bf16 v[40:43], v[104:107], a[244:247], v[40:43]\n"
"v_lshlrev_b32_e64 v244, 24, s43\n"
"v_lshrrev_b32 v245, 1, v245\n"
"v_mfma_f32_16x16x32_bf16 v[40:43], v[108:111], a[248:251], v[40:43]\n"
"ds_read_b128 v[208:211], v228 offset:0\n"
"v_sub_u32 v245, v242, v245\n"
"v_smfmac_f32_16x16x64_bf16 v[198:201], a[240:243], v[104:111], v241\n"
"ds_read_b128 v[220:223], v229 offset:1024\n"
"v_add_f32 v196, v196, v197\n"
"v_mfma_f32_16x16x32_bf16 v[72:75], v[112:115], a[244:247], v[72:75]\n"
"ds_read_b64_tr_b16 a[244:245], v234 offset:50176\n"
"v_mul_f32 v244, 0x45800000, v244\n"
"v_mfma_f32_16x16x32_bf16 v[72:75], v[116:119], a[248:251], v[72:75]\n"
"ds_read_b64_tr_b16 a[246:247], v234 offset:54272\n"
"ds_read_b64_tr_b16 a[250:251], v238 offset:54272\n"
"v_mfma_f32_16x16x32_bf16 v[202:205], a[240:243], v[120:123], v[202:205]\n"
"s_waitcnt lgkmcnt(6)\n"
"ds_read_b128 v[216:219], v228 offset:1024\n"
"v_add_f32 v198, v198, v199\n"
"v_mfma_f32_16x16x32_bf16 v[76:79], v[116:119], v[224:227], v[76:79]\n"
"ds_read_b64_tr_b16 a[248:249], v238 offset:50176\n"
"v_add_f32 v254, v196, v198\n"
"v_mfma_f32_16x16x32_bf16 v[76:79], v[112:115], a[252:255], v[76:79]\n"
"v_add_f32 v200, v200, v201\n"
"v_add_f32 v254, v254, v200\n"
"v_mfma_f32_16x16x32_bf16 v[44:47], v[108:111], v[224:227], v[44:47]\n"
"v_add_f32 v244, v244, v202\n"
"v_add_f32 v254, v254, v202\n"
"v_mfma_f32_16x16x32_bf16 v[44:47], v[104:107], a[252:255], v[44:47]\n"
"v_min3_f32 v244, v244, v196, v198\n"
"v_min_f32 v244, v244, v200\n"
"v_mfma_f32_16x16x32_bf16 v[12:15], v[100:103], v[224:227], v[12:15]\n"
"s_add_u32 s40, s56, s52\n"
"v_rcp_f32 v244, v244\n"
"v_mfma_f32_16x16x32_bf16 v[12:15], v[96:99], a[252:255], v[12:15]\n"
"buffer_load_dwordx4 v240, s[20:23], s40 offen offset:1024 nt lds\n"
"v_mfma_f32_16x16x32_bf16 a[220:223], v[120:123], a[252:255], a[220:223]\n"
"s_waitcnt lgkmcnt(2)\n"
"v_fmac_f32 v254, 0x68800000, v244\n"
"v_cmp_nge_f32 vcc, s45, v254\n"
"v_xor_b32 v244, 0x11000, v242\n"
"s_cbranch_vccnz LSLOW_%=\nLBACK0_%=:\ns_mov_b64 vcc, s[64:65]\n"
"v_mfma_f32_16x16x32_bf16 a[232:235], v[120:123], a[244:247], a[232:235]\n"
"v_accvgpr_mov_b32 a6, a54\n"
"v_accvgpr_mov_b32 a8, a56\n"
"v_accvgpr_mov_b32 a9, a57\n"
"v_accvgpr_mov_b32 a14, a62\n"
"v_accvgpr_mov_b32 a20, a68\n"
"v_accvgpr_mov_b32 a42, a90\n"
"v_accvgpr_mov_b32 a44, a92\n"
"v_xor_b32 v148, 0x400, v243\n"
"v_bfe_u32 v149, v242, 8, 2\n"
"v_lshl_add_u32 v149, v149, 4, s43\n"
"v_mfma_f32_16x16x32_bf16 v[24:27], v[96:99], a[244:247], v[24:27]\n"
"s_waitcnt lgkmcnt(0)\n"
"ds_read_b128 v[124:127], v149 offset:0\n"
"v_rcp_f32 v206, v200\n"
"v_accvgpr_mov_b32 a4, a52\n"
"v_accvgpr_mov_b32 a12, a60\n"
"v_accvgpr_mov_b32 a13, a61\n"
"v_accvgpr_mov_b32 a16, a64\n"
"v_accvgpr_mov_b32 a39, a87\n"
"v_accvgpr_mov_b32 a47, a95\n"
"v_xor_b32 v150, 64, v149\n"
"v_mfma_f32_16x16x32_bf16 v[24:27], v[100:103], a[248:251], v[24:27]\n"
"ds_read_b128 v[128:131], v150 offset:0\n"
"v_accvgpr_mov_b32 a0, a48\n"
"v_accvgpr_mov_b32 a1, a49\n"
"v_accvgpr_mov_b32 a5, a53\n"
"v_accvgpr_mov_b32 a10, a58\n"
"v_accvgpr_mov_b32 a17, a65\n"
"v_accvgpr_mov_b32 a40, a88\n"
"v_mfma_f32_16x16x32_bf16 v[56:59], v[104:107], a[244:247], v[56:59]\n"
"ds_read_b128 v[132:135], v149 offset:1024\n"
"v_rcp_f32 v246, v196\n"
"v_mov_b32_dpp v206, v206 row_ror:12 row_mask:0x2 bank_mask:0xf\n"
"v_accvgpr_mov_b32 a2, a50\n"
"v_accvgpr_mov_b32 a22, a70\n"
"v_accvgpr_mov_b32 a23, a71\n"
"v_accvgpr_mov_b32 a36, a84\n"
"v_accvgpr_mov_b32 a38, a86\n"
"v_accvgpr_mov_b32 a45, a93\n"
"v_mfma_f32_16x16x32_bf16 v[56:59], v[108:111], a[248:251], v[56:59]\n"
"ds_read_b128 v[136:139], v150 offset:1024\n"
"v_accvgpr_mov_b32 a33, a81\n"
"v_mfma_f32_16x16x32_bf16 v[88:91], v[112:115], a[244:247], v[88:91]\n"
"ds_read_b64_tr_b16 v[224:225], v239 offset:50176\n"
"v_accvgpr_mov_b32 a3, a51\n"
"v_accvgpr_mov_b32 a7, a55\n"
"v_accvgpr_mov_b32 a11, a59\n"
"v_accvgpr_mov_b32 a19, a67\n"
"v_accvgpr_mov_b32 a21, a69\n"
"v_accvgpr_mov_b32 a24, a72\n"
"v_accvgpr_mov_b32 a27, a75\n"
"v_accvgpr_mov_b32 a30, a78\n"
"v_accvgpr_mov_b32 a46, a94\n"
"v_mfma_f32_16x16x32_bf16 v[88:91], v[116:119], a[248:251], v[88:91]\n"
"ds_read_b128 a[204:207], v148 offset:6144\n"
"v_mov_b32_dpp v206, v206 row_ror:8 row_mask:0x4 bank_mask:0xf\n"
"v_accvgpr_mov_b32 a15, a63\n"
"v_accvgpr_mov_b32 a25, a73\n"
"v_accvgpr_mov_b32 a26, a74\n"
"v_accvgpr_mov_b32 a29, a77\n"
"v_accvgpr_mov_b32 a31, a79\n"
"v_accvgpr_mov_b32 a35, a83\n"
"v_accvgpr_mov_b32 a43, a91\n"
"s_waitcnt lgkmcnt(5)\n"
"v_mfma_f32_16x16x32_bf16 v[180:183], v[124:127], a[0:3], 0\n"
"v_mov_b32 v252, v192\n"
"s_waitcnt lgkmcnt(4)\n"
"v_mfma_f32_16x16x32_bf16 v[180:183], v[128:131], a[4:7], v[180:183]\n"
"s_waitcnt lgkmcnt(3)\n"
"ds_read_b128 a[192:195], v243 offset:4096\n"
"v_accvgpr_mov_b32 a18, a66\n"
"v_mfma_f32_16x16x32_bf16 v[180:183], v[132:135], a[8:11], v[180:183]\n"
"ds_read_b128 a[200:203], v243 offset:6144\n"
"s_waitcnt lgkmcnt(4)\n"
"v_mfma_f32_16x16x32_bf16 v[180:183], v[136:139], a[12:15], v[180:183]\n"
"v_rcp_f32 v247, v198\n"
"v_accvgpr_mov_b32 a28, a76\n"
"v_accvgpr_mov_b32 a34, a82\n"
"v_accvgpr_mov_b32 a41, a89\n"
"v_mfma_f32_16x16x32_bf16 v[184:187], v[124:127], a[16:19], 0\n"
"ds_read_b128 a[196:199], v148 offset:4096\n"
"v_mfma_f32_16x16x32_bf16 v[184:187], v[128:131], a[20:23], v[184:187]\n"
"v_mov_b32_dpp v247, v247 row_ror:12 row_mask:0x2 bank_mask:0xf\n"
"v_mfma_f32_16x16x32_bf16 v[184:187], v[132:135], a[24:27], v[184:187]\n"
"s_add_u32 s40, s56, s53\n"
"v_accvgpr_mov_b32 a32, a80\n"
"v_mfma_f32_16x16x32_bf16 v[184:187], v[136:139], a[28:31], v[184:187]\n"
"buffer_load_dwordx4 v240, s[20:23], s40 offen offset:2048 nt lds\n"
"v_mov_b32_dpp v206, v206 row_ror:4 row_mask:0x8 bank_mask:0xf\n"
"v_accvgpr_mov_b32 a37, a85\n"
"v_sub_f32 v180, s74, v180\n"
"v_sub_f32 v183, s74, v183\n"
"v_mfma_f32_16x16x32_bf16 v[188:191], v[124:127], a[32:35], 0\n"
"v_mov_b32_dpp v246, v246 row_ror:12 row_mask:0x2 bank_mask:0xf\n"
"v_mfma_f32_16x16x32_bf16 v[188:191], v[128:131], a[36:39], v[188:191]\n"
"v_mfma_f32_16x16x32_bf16 v[188:191], v[132:135], a[40:43], v[188:191]\n"
"v_mov_b32_dpp v246, v246 row_ror:8 row_mask:0x4 bank_mask:0xf\n"
"v_sub_f32 v182, s74, v182\n"
"v_mfma_f32_16x16x32_bf16 v[188:191], v[136:139], a[44:47], v[188:191]\n"
"ds_write_b128 v244, a[220:223] offset:33792\n"
"v_mov_b32_dpp v247, v247 row_ror:8 row_mask:0x4 bank_mask:0xf\n"
"s_waitcnt lgkmcnt(2)\n"
"v_mfma_f32_16x16x32_bf16 v[192:195], v[124:127], a[192:195], 0\n"
"v_sub_f32 v181, s74, v181\n"
"s_waitcnt lgkmcnt(1)\n"
"v_mfma_f32_16x16x32_bf16 v[192:195], v[128:131], a[196:199], v[192:195]\n"
"v_mfma_f32_16x16x32_bf16 v[192:195], v[132:135], a[200:203], v[192:195]\n"
"ds_read_b64_tr_b16 v[226:227], v239 offset:54272\n"
"ds_read_b64_tr_b16 a[246:247], v232 offset:53248\n"
"v_mfma_f32_16x16x32_bf16 v[192:195], v[136:139], a[204:207], v[192:195]\n"
"v_mov_b32_dpp v246, v246 row_ror:4 row_mask:0x8 bank_mask:0xf\n"
"v_mov_b32_dpp v247, v247 row_ror:4 row_mask:0x8 bank_mask:0xf\n"
"v_sub_f32 v184, s74, v184\n"
"v_sub_f32 v187, s74, v187\n"
"v_sub_f32 v190, s74, v190\n"
"v_mfma_f32_16x16x32_bf16 v[152:155], v[208:211], a[0:3], v[180:183]\n"
"v_mov_b32 v253, v202\n"
"v_sub_f32 v185, s74, v185\n"
"v_mfma_f32_16x16x32_bf16 v[152:155], v[212:215], a[4:7], v[152:155]\n"
"v_sub_f32 v186, s74, v186\n"
"v_mfma_f32_16x16x32_bf16 v[152:155], v[216:219], a[8:11], v[152:155]\n"
"s_add_u32 s40, s56, s54\n"
"ds_read_b64_tr_b16 a[254:255], v235 offset:54272\n"
"v_sub_f32 v189, s74, v189\n"
"v_mfma_f32_16x16x32_bf16 v[152:155], v[220:223], a[12:15], v[152:155]\n"
"buffer_load_dwordx4 v240, s[20:23], s40 offen offset:3072 nt lds\n"
"v_mul_f32_dpp v9, v246, v9 row_newbcast:1 row_mask:0xf bank_mask:0xf\n"
"v_mul_f32_dpp v12, v246, v12 row_newbcast:0 row_mask:0xf bank_mask:0xf\n"
"v_sub_f32 v188, s74, v188\n"
"v_sub_f32 v191, s74, v191\n"
"v_mfma_f32_16x16x32_bf16 v[156:159], v[216:219], a[24:27], v[184:187]\n"
"s_add_u32 s55, s55, s59\n"
"v_mfma_f32_16x16x32_bf16 v[156:159], v[220:223], a[28:31], v[156:159]\n"
"s_add_u32 s41, s55, s57\n"
"v_mfma_f32_16x16x32_bf16 v[156:159], v[208:211], a[16:19], v[156:159]\n"
"s_and_b32 s41, s41, s58\n"
"v_sub_f32 v192, s74, v192\n"
"v_mfma_f32_16x16x32_bf16 v[156:159], v[212:215], a[20:23], v[156:159]\n"
"s_andn2_b32 s56, s55, s58\n"
"ds_read_b64_tr_b16 a[250:251], v236 offset:53248\n"
"v_xor_b32 v250, 0x1000, v245\n"
"v_mul_f32_dpp v41, v247, v41 row_newbcast:1 row_mask:0xf bank_mask:0xf\n"
"v_mul_f32_dpp v46, v247, v46 row_newbcast:2 row_mask:0xf bank_mask:0xf\n"
"v_sub_f32 v195, s74, v195\n"
"v_exp_f32 v153, v153\n"
"v_mfma_f32_16x16x32_bf16 v[160:163], v[208:211], a[32:35], v[188:191]\n"
"s_or_b32 s56, s56, s41\n"
"v_mul_f32_dpp v43, v247, v43 row_newbcast:3 row_mask:0xf bank_mask:0xf\n"
"v_sub_f32 v193, s74, v193\n"
"v_mfma_f32_16x16x32_bf16 v[160:163], v[212:215], a[36:39], v[160:163]\n"
"v_mfma_f32_16x16x32_bf16 v[160:163], v[216:219], a[40:43], v[160:163]\n"
"v_sub_f32 v194, s74, v194\n"
"v_mfma_f32_16x16x32_bf16 v[160:163], v[220:223], a[44:47], v[160:163]\n"
"ds_read_b64_tr_b16 a[252:253], v235 offset:50176\n"
"v_exp_f32 v152, v152\n"
"v_exp_f32 v157, v157\n"
"v_exp_f32 v158, v158\n"
"v_mfma_f32_16x16x32_bf16 v[164:167], v[216:219], a[200:203], v[192:195]\n"
"ds_read_b128 v[216:219], v228 offset:5120\n"
"v_mfma_f32_16x16x32_bf16 v[164:167], v[220:223], a[204:207], v[164:167]\n"
"v_mul_f32_dpp v57, v247, v57 row_newbcast:1 row_mask:0xf bank_mask:0xf\n"
"v_mfma_f32_16x16x32_bf16 v[164:167], v[208:211], a[192:195], v[164:167]\n"
"ds_read_b128 v[208:211], v228 offset:4096\n"
"v_mfma_f32_16x16x32_bf16 v[164:167], v[212:215], a[196:199], v[164:167]\n"
"ds_read_b128 v[220:223], v229 offset:5120\n"
"v_mul_f32_dpp v10, v246, v10 row_newbcast:2 row_mask:0xf bank_mask:0xf\n"
"v_exp_f32 v154, v154\n"
"v_exp_f32 v155, v155\n"
"v_mul_f32_dpp v27, v246, v27 row_newbcast:3 row_mask:0xf bank_mask:0xf\n"
"v_mul_f32_dpp v58, v247, v58 row_newbcast:2 row_mask:0xf bank_mask:0xf\n"
"s_branch LTC_%=\n"
"LTH1_%=:\n"
"v_mfma_f32_16x16x32_bf16 a[224:227], v[124:127], a[244:247], a[224:227]\n"
"v_exp_f32 v170, v170\n"
"v_mfma_f32_16x16x32_bf16 v[16:19], v[128:131], a[244:247], v[16:19]\n"
"s_waitcnt vmcnt(8)\n"
"v_exp_f32 v168, v168\n"
"v_mfma_f32_16x16x32_bf16 v[16:19], v[132:135], a[248:251], v[16:19]\n"
"s_barrier\n"
"v_mfma_f32_16x16x32_bf16 v[48:51], v[136:139], a[244:247], v[48:51]\n"
"v_exp_f32 v173, v173\n"
"v_mfma_f32_16x16x32_bf16 v[48:51], v[140:143], a[248:251], v[48:51]\n"
"v_exp_f32 v171, v171\n"
"v_mfma_f32_16x16x32_bf16 v[80:83], v[144:147], a[244:247], v[80:83]\n"
"ds_read_b64_tr_b16 a[244:245], v234 offset:49152\n"
"ds_read_b64_tr_b16 a[246:247], v234 offset:53248\n"
"v_mfma_f32_16x16x32_bf16 v[80:83], v[148:151], a[248:251], v[80:83]\n"
"v_exp_f32 v179, v179\n"
"v_mfma_f32_16x16x32_bf16 v[84:87], v[148:151], v[224:227], v[84:87]\n"
"ds_read_b64_tr_b16 a[250:251], v238 offset:53248\n"
"v_cvt_pk_bf16_f32 v100, v156, v157\n"
"v_mfma_f32_16x16x32_bf16 v[84:87], v[144:147], a[252:255], v[84:87]\n"
"v_exp_f32 v169, v169\n"
"v_exp_f32 v172, v172\n"
"v_exp_f32 v175, v175\n"
"v_exp_f32 v176, v176\n"
"v_exp_f32 v178, v178\n"
"v_mfma_f32_16x16x32_bf16 v[52:55], v[140:143], v[224:227], v[52:55]\n"
"ds_read_b64_tr_b16 a[248:249], v238 offset:49152\n"
"v_cvt_pk_bf16_f32 v117, v166, v167\n"
"v_mfma_f32_16x16x32_bf16 v[52:55], v[136:139], a[252:255], v[52:55]\n"
"v_exp_f32 v174, v174\n"
"v_mfma_f32_16x16x32_bf16 v[20:23], v[132:135], v[224:227], v[20:23]\n"
"ds_read_b64_tr_b16 v[224:225], v239 offset:49152\n"
"ds_read_b64_tr_b16 v[226:227], v239 offset:53248\n"
"v_mfma_f32_16x16x32_bf16 v[20:23], v[128:131], a[252:255], v[20:23]\n"
"v_cvt_pk_bf16_f32 v102, v168, v169\n"
"v_cvt_pk_bf16_f32 v103, v170, v171\n"
"v_cvt_pk_bf16_f32 v111, v174, v175\n"
"v_exp_f32 v177, v177\n"
"v_mfma_f32_16x16x32_bf16 a[228:231], v[124:127], a[252:255], a[228:231]\n"
"s_waitcnt lgkmcnt(4)\n"
"ds_read_b64_tr_b16 a[254:255], v235 offset:53248\n"
"v_cvt_pk_bf16_f32 v110, v172, v173\n"
"v_mfma_f32_16x16x32_bf16 a[216:219], v[120:123], a[244:247], a[216:219]\n"
"s_add_u32 m0, s44, 81920\n"
"ds_read_b64_tr_b16 a[252:253], v235 offset:49152\n"
"v_cvt_pk_bf16_f32 v116, v164, v165\n"
"v_smfmac_f32_16x16x64_bf16 v[196:199], a[240:243], v[96:103], v241\n"
"buffer_load_dwordx4 v240, s[20:23], s56 offen nt lds\n"
"v_mfma_f32_16x16x32_bf16 v[8:11], v[96:99], a[244:247], v[8:11]\n"
"s_waitcnt lgkmcnt(4)\n"
"v_cvt_pk_bf16_f32 v118, v176, v177\n"
"v_cvt_pk_bf16_f32 v119, v178, v179\n"
"v_mfma_f32_16x16x32_bf16 v[8:11], v[100:103], a[248:251], v[8:11]\n"
"v_cvt_pk_bf16_f32 v108, v160, v161\n"
"v_and_b32 v245, 0x3f0, v242\n"
"v_smfmac_f32_16x16x64_bf16 v[200:203], a[240:243], v[112:119], v241\n"
"ds_read_b128 v[212:215], v229 offset:0\n"
"v_cvt_pk_bf16_f32 v109, v162, v163\n"
"v_mfma_f32_16x16x32_bf16 v[40:43], v[104:107], a[244:247], v[40:43]\n"
"v_lshlrev_b32_e64 v244, 24, s43\n"
"v_lshrrev_b32 v245, 1, v245\n"
"v_mfma_f32_16x16x32_bf16 v[40:43], v[108:111], a[248:251], v[40:43]\n"
"ds_read_b128 v[208:211], v228 offset:0\n"
"v_sub_u32 v245, v242, v245\n"
"v_smfmac_f32_16x16x64_bf16 v[198:201], a[240:243], v[104:111], v241\n"
"ds_read_b128 v[220:223], v229 offset:1024\n"
"v_add_f32 v196, v196, v197\n"
"v_mfma_f32_16x16x32_bf16 v[72:75], v[112:115], a[244:247], v[72:75]\n"
"ds_read_b64_tr_b16 a[244:245], v234 offset:50176\n"
"v_mul_f32 v244, 0x45800000, v244\n"
"v_mfma_f32_16x16x32_bf16 v[72:75], v[116:119], a[248:251], v[72:75]\n"
"ds_read_b64_tr_b16 a[246:247], v234 offset:54272\n"
"ds_read_b64_tr_b16 a[250:251], v238 offset:54272\n"
"v_mfma_f32_16x16x32_bf16 v[202:205], a[240:243], v[120:123], v[202:205]\n"
"s_waitcnt lgkmcnt(6)\n"
"ds_read_b128 v[216:219], v228 offset:1024\n"
"v_add_f32 v198, v198, v199\n"
"v_mfma_f32_16x16x32_bf16 v[76:79], v[116:119], v[224:227], v[76:79]\n"
"ds_read_b64_tr_b16 a[248:249], v238 offset:50176\n"
"v_add_f32 v254, v196, v198\n"
"v_mfma_f32_16x16x32_bf16 v[76:79], v[112:115], a[252:255], v[76:79]\n"
"v_add_f32 v200, v200, v201\n"
"v_add_f32 v254, v254, v200\n"
"v_mfma_f32_16x16x32_bf16 v[44:47], v[108:111], v[224:227], v[44:47]\n"
"v_add_f32 v244, v244, v202\n"
"v_add_f32 v254, v254, v202\n"
"v_mfma_f32_16x16x32_bf16 v[44:47], v[104:107], a[252:255], v[44:47]\n"
"v_min3_f32 v244, v244, v196, v198\n"
"v_min_f32 v244, v244, v200\n"
"v_mfma_f32_16x16x32_bf16 v[12:15], v[100:103], v[224:227], v[12:15]\n"
"s_add_u32 s40, s56, s52\n"
"v_rcp_f32 v244, v244\n"
"v_mfma_f32_16x16x32_bf16 v[12:15], v[96:99], a[252:255], v[12:15]\n"
"buffer_load_dwordx4 v240, s[20:23], s40 offen offset:1024 nt lds\n"
"v_mfma_f32_16x16x32_bf16 a[220:223], v[120:123], a[252:255], a[220:223]\n"
"s_waitcnt lgkmcnt(2)\n"
"v_fmac_f32 v254, 0x68800000, v244\n"
"v_cmp_nge_f32 vcc, s45, v254\n"
"v_xor_b32 v244, 0x11000, v242\n"
"s_cbranch_vccnz LSLOW_%=\nLBACK1_%=:\ns_mov_b64 vcc, s[64:65]\n"
"v_mfma_f32_16x16x32_bf16 a[232:235], v[120:123], a[244:247], a[232:235]\n"
"v_accvgpr_mov_b32 a6, a102\n"
"v_accvgpr_mov_b32 a8, a104\n"
"v_accvgpr_mov_b32 a9, a105\n"
"v_accvgpr_mov_b32 a14, a110\n"
"v_accvgpr_mov_b32 a20, a116\n"
"v_accvgpr_mov_b32 a42, a138\n"
"v_accvgpr_mov_b32 a44, a140\n"
"v_xor_b32 v148, 0x400, v243\n"
"v_bfe_u32 v149, v242, 8, 2\n"
"v_lshl_add_u32 v149, v149, 4, s43\n"
"v_mfma_f32_16x16x32_bf16 v[24:27], v[96:99], a[244:247], v[24:27]\n"
"s_waitcnt lgkmcnt(0)\n"
"ds_read_b128 v[124:127], v149 offset:0\n"
"v_rcp_f32 v206, v200\n"
"v_accvgpr_mov_b32 a4, a100\n"
"v_accvgpr_mov_b32 a12, a108\n"
"v_accvgpr_mov_b32 a13, a109\n"
"v_accvgpr_mov_b32 a16, a112\n"
"v_accvgpr_mov_b32 a39, a135\n"
"v_accvgpr_mov_b32 a47, a143\n"
"v_xor_b32 v150, 64, v149\n"
"v_mfma_f32_16x16x32_bf16 v[24:27], v[100:103], a[248:251], v[24:27]\n"
"ds_read_b128 v[128:131], v150 offset:0\n"
"v_accvgpr_mov_b32 a0, a96\n"
"v_accvgpr_mov_b32 a1, a97\n"
"v_accvgpr_mov_b32 a5, a101\n"
"v_accvgpr_mov_b32 a10, a106\n"
"v_accvgpr_mov_b32 a17, a113\n"
"v_accvgpr_mov_b32 a40, a136\n"
"v_mfma_f32_16x16x32_bf16 v[56:59], v[104:107], a[244:247], v[56:59]\n"
"ds_read_b128 v[132:135], v149 offset:1024\n"
"v_rcp_f32 v246, v196\n"
"v_mov_b32_dpp v206, v206 row_ror:12 row_mask:0x2 bank_mask:0xf\n"
"v_accvgpr_mov_b32 a2, a98\n"
"v_accvgpr_mov_b32 a22, a118\n"
"v_accvgpr_mov_b32 a23, a119\n"
"v_accvgpr_mov_b32 a36, a132\n"
"v_accvgpr_mov_b32 a38, a134\n"
"v_accvgpr_mov_b32 a45, a141\n"
"v_mfma_f32_16x16x32_bf16 v[56:59], v[108:111], a[248:251], v[56:59]\n"
"ds_read_b128 v[136:139], v150 offset:1024\n"
"v_accvgpr_mov_b32 a33, a129\n"
"v_mfma_f32_16x16x32_bf16 v[88:91], v[112:115], a[244:247], v[88:91]\n"
"ds_read_b64_tr_b16 v[224:225], v239 offset:50176\n"
"v_accvgpr_mov_b32 a3, a99\n"
"v_accvgpr_mov_b32 a7, a103\n"
"v_accvgpr_mov_b32 a11, a107\n"
"v_accvgpr_mov_b32 a19, a115\n"
"v_accvgpr_mov_b32 a21, a117\n"
"v_accvgpr_mov_b32 a24, a120\n"
"v_accvgpr_mov_b32 a27, a123\n"
"v_accvgpr_mov_b32 a30, a126\n"
"v_accvgpr_mov_b32 a46, a142\n"
"v_mfma_f32_16x16x32_bf16 v[88:91], v[116:119], a[248:251], v[88:91]\n"
"s_waitcnt lgkmcnt(4)\n"
"ds_read_b128 a[204:207], v148 offset:6144\n"
"v_mov_b32_dpp v206, v206 row_ror:8 row_mask:0x4 bank_mask:0xf\n"
"v_accvgpr_mov_b32 a15, a111\n"
"v_accvgpr_mov_b32 a25, a121\n"
"v_accvgpr_mov_b32 a26, a122\n"
"v_accvgpr_mov_b32 a29, a125\n"
"v_accvgpr_mov_b32 a31, a127\n"
"v_accvgpr_mov_b32 a35, a131\n"
"v_accvgpr_mov_b32 a43, a139\n"
"v_accvgpr_mov_b32 a112, a64\n"
"v_mfma_f32_16x16x32_bf16 v[180:183], v[124:127], a[0:3], 0\n"
"s_waitcnt lgkmcnt(4)\n"
"v_mov_b32 v252, v192\n"
"v_accvgpr_mov_b32 a126, a78\n"
"v_mfma_f32_16x16x32_bf16 v[180:183], v[128:131], a[4:7], v[180:183]\n"
"s_waitcnt lgkmcnt(3)\n"
"ds_read_b128 a[192:195], v243 offset:4096\n"
"v_accvgpr_mov_b32 a18, a114\n"
"v_mfma_f32_16x16x32_bf16 v[180:183], v[132:135], a[8:11], v[180:183]\n"
"s_waitcnt lgkmcnt(3)\n"
"ds_read_b128 a[200:203], v243 offset:6144\n"
"v_accvgpr_mov_b32 a135, a87\n"
"v_mfma_f32_16x16x32_bf16 v[180:183], v[136:139], a[12:15], v[180:183]\n"
"v_rcp_f32 v247, v198\n"
"v_accvgpr_mov_b32 a28, a124\n"
"v_accvgpr_mov_b32 a34, a130\n"
"v_accvgpr_mov_b32 a41, a137\n"
"v_accvgpr_mov_b32 a103, a55\n"
"v_accvgpr_mov_b32 a119, a71\n"
"v_accvgpr_mov_b32 a108, a60\n"
"v_accvgpr_mov_b32 a130, a82\n"
"v_mfma_f32_16x16x32_bf16 v[184:187], v[124:127], a[16:19], 0\n"
"ds_read_b128 a[196:199], v148 offset:4096\n"
"v_accvgpr_mov_b32 a110, a62\n"
"v_mfma_f32_16x16x32_bf16 v[184:187], v[128:131], a[20:23], v[184:187]\n"
"v_mov_b32_dpp v247, v247 row_ror:12 row_mask:0x2 bank_mask:0xf\n"
"v_accvgpr_mov_b32 a104, a56\n"
"v_mfma_f32_16x16x32_bf16 v[184:187], v[132:135], a[24:27], v[184:187]\n"
"s_add_u32 s40, s56, s53\n"
"v_accvgpr_mov_b32 a32, a128\n"
"v_accvgpr_mov_b32 a109, a61\n"
"v_mfma_f32_16x16x32_bf16 v[184:187], v[136:139], a[28:31], v[184:187]\n"
"buffer_load_dwordx4 v240, s[20:23], s40 offen offset:2048 nt lds\n"
"v_mov_b32_dpp v206, v206 row_ror:4 row_mask:0x8 bank_mask:0xf\n"
"v_accvgpr_mov_b32 a37, a133\n"
"v_accvgpr_mov_b32 a101, a53\n"
"v_accvgpr_mov_b32 a102, a54\n"
"v_accvgpr_mov_b32 a133, a85\n"
"v_accvgpr_mov_b32 a105, a57\n"
"v_sub_f32 v180, s74, v180\n"
"v_sub_f32 v183, s74, v183\n"
"v_mfma_f32_16x16x32_bf16 v[188:191], v[124:127], a[32:35], 0\n"
"v_mov_b32_dpp v246, v246 row_ror:12 row_mask:0x2 bank_mask:0xf\n"
"v_accvgpr_mov_b32 a132, a84\n"
"v_mfma_f32_16x16x32_bf16 v[188:191], v[128:131], a[36:39], v[188:191]\n"
"v_accvgpr_mov_b32 a114, a66\n"
"v_accvgpr_mov_b32 a131, a83\n"
"v_mfma_f32_16x16x32_bf16 v[188:191], v[132:135], a[40:43], v[188:191]\n"
"v_mov_b32_dpp v246, v246 row_ror:8 row_mask:0x4 bank_mask:0xf\n"
"v_sub_f32 v182, s74, v182\n"
"v_mfma_f32_16x16x32_bf16 v[188:191], v[136:139], a[44:47], v[188:191]\n"
"s_waitcnt lgkmcnt(1)\n"
"ds_write_b128 v244, a[220:223] offset:33792\n"
"v_mov_b32_dpp v247, v247 row_ror:8 row_mask:0x4 bank_mask:0xf\n"
"v_accvgpr_mov_b32 a115, a67\n"
"v_accvgpr_mov_b32 a129, a81\n"
"v_accvgpr_mov_b32 a106, a58\n"
"v_mfma_f32_16x16x32_bf16 v[192:195], v[124:127], a[192:195], 0\n"
"s_waitcnt lgkmcnt(1)\n"
"v_accvgpr_mov_b32 a116, a68\n"
"v_sub_f32 v181, s74, v181\n"
"v_mfma_f32_16x16x32_bf16 v[192:195], v[128:131], a[196:199], v[192:195]\n"
"v_accvgpr_mov_b32 a125, a77\n"
"v_accvgpr_mov_b32 a96, a48\n"
"v_mfma_f32_16x16x32_bf16 v[192:195], v[132:135], a[200:203], v[192:195]\n"
"ds_read_b64_tr_b16 v[226:227], v239 offset:54272\n"
"ds_read_b64_tr_b16 a[246:247], v232 offset:53248\n"
"v_mfma_f32_16x16x32_bf16 v[192:195], v[136:139], a[204:207], v[192:195]\n"
"v_mov_b32_dpp v246, v246 row_ror:4 row_mask:0x8 bank_mask:0xf\n"
"v_mov_b32_dpp v247, v247 row_ror:4 row_mask:0x8 bank_mask:0xf\n"
"v_accvgpr_mov_b32 a100, a52\n"
"v_accvgpr_mov_b32 a117, a69\n"
"v_accvgpr_mov_b32 a127, a79\n"
"v_accvgpr_mov_b32 a128, a80\n"
"v_sub_f32 v184, s74, v184\n"
"v_sub_f32 v187, s74, v187\n"
"v_sub_f32 v190, s74, v190\n"
"v_mfma_f32_16x16x32_bf16 v[152:155], v[208:211], a[0:3], v[180:183]\n"
"v_mov_b32 v253, v202\n"
"v_sub_f32 v185, s74, v185\n"
"v_mfma_f32_16x16x32_bf16 v[152:155], v[212:215], a[4:7], v[152:155]\n"
"v_accvgpr_mov_b32 a138, a90\n"
"v_sub_f32 v186, s74, v186\n"
"v_mfma_f32_16x16x32_bf16 v[152:155], v[216:219], a[8:11], v[152:155]\n"
"s_add_u32 s40, s56, s54\n"
"ds_read_b64_tr_b16 a[254:255], v235 offset:54272\n"
"v_sub_f32 v189, s74, v189\n"
"v_mfma_f32_16x16x32_bf16 v[152:155], v[220:223], a[12:15], v[152:155]\n"
"buffer_load_dwordx4 v240, s[20:23], s40 offen offset:3072 nt lds\n"
"v_accvgpr_mov_b32 a142, a94\n"
"v_accvgpr_mov_b32 a143, a95\n"
"v_accvgpr_mov_b32 a120, a72\n"
"v_mul_f32_dpp v9, v246, v9 row_newbcast:1 row_mask:0xf bank_mask:0xf\n"
"v_mul_f32_dpp v12, v246, v12 row_newbcast:0 row_mask:0xf bank_mask:0xf\n"
"v_sub_f32 v188, s74, v188\n"
"v_sub_f32 v191, s74, v191\n"
"v_mfma_f32_16x16x32_bf16 v[156:159], v[216:219], a[24:27], v[184:187]\n"
"s_add_u32 s55, s55, s59\n"
"v_accvgpr_mov_b32 a111, a63\n"
"v_accvgpr_mov_b32 a107, a59\n"
"v_mfma_f32_16x16x32_bf16 v[156:159], v[220:223], a[28:31], v[156:159]\n"
"s_add_u32 s41, s55, s57\n"
"v_accvgpr_mov_b32 a97, a49\n"
"v_accvgpr_mov_b32 a122, a74\n"
"v_mfma_f32_16x16x32_bf16 v[156:159], v[208:211], a[16:19], v[156:159]\n"
"s_and_b32 s41, s41, s58\n"
"v_accvgpr_mov_b32 a99, a51\n"
"v_sub_f32 v192, s74, v192\n"
"v_mfma_f32_16x16x32_bf16 v[156:159], v[212:215], a[20:23], v[156:159]\n"
"s_andn2_b32 s56, s55, s58\n"
"ds_read_b64_tr_b16 a[250:251], v236 offset:53248\n"
"v_xor_b32 v250, 0x1000, v245\n"
"v_accvgpr_mov_b32 a98, a50\n"
"v_accvgpr_mov_b32 a113, a65\n"
"v_accvgpr_mov_b32 a139, a91\n"
"v_mul_f32_dpp v41, v247, v41 row_newbcast:1 row_mask:0xf bank_mask:0xf\n"
"v_mul_f32_dpp v46, v247, v46 row_newbcast:2 row_mask:0xf bank_mask:0xf\n"
"v_sub_f32 v195, s74, v195\n"
"v_exp_f32 v153, v153\n"
"v_mfma_f32_16x16x32_bf16 v[160:163], v[208:211], a[32:35], v[188:191]\n"
"s_or_b32 s56, s56, s41\n"
"v_mul_f32_dpp v43, v247, v43 row_newbcast:3 row_mask:0xf bank_mask:0xf\n"
"v_sub_f32 v193, s74, v193\n"
"v_mfma_f32_16x16x32_bf16 v[160:163], v[212:215], a[36:39], v[160:163]\n"
"v_accvgpr_mov_b32 a123, a75\n"
"v_accvgpr_mov_b32 a137, a89\n"
"v_mfma_f32_16x16x32_bf16 v[160:163], v[216:219], a[40:43], v[160:163]\n"
"v_accvgpr_mov_b32 a134, a86\n"
"v_sub_f32 v194, s74, v194\n"
"v_mfma_f32_16x16x32_bf16 v[160:163], v[220:223], a[44:47], v[160:163]\n"
"ds_read_b64_tr_b16 a[252:253], v235 offset:50176\n"
"v_accvgpr_mov_b32 a124, a76\n"
"v_accvgpr_mov_b32 a136, a88\n"
"v_exp_f32 v152, v152\n"
"v_exp_f32 v157, v157\n"
"v_exp_f32 v158, v158\n"
"v_mfma_f32_16x16x32_bf16 v[164:167], v[216:219], a[200:203], v[192:195]\n"
"ds_read_b128 v[216:219], v228 offset:5120\n"
"v_accvgpr_mov_b32 a118, a70\n"
"v_mfma_f32_16x16x32_bf16 v[164:167], v[220:223], a[204:207], v[164:167]\n"
"v_accvgpr_mov_b32 a140, a92\n"
"v_mul_f32_dpp v57, v247, v57 row_newbcast:1 row_mask:0xf bank_mask:0xf\n"
"v_mfma_f32_16x16x32_bf16 v[164:167], v[208:211], a[192:195], v[164:167]\n"
"ds_read_b128 v[208:211], v228 offset:4096\n"
"v_accvgpr_mov_b32 a141, a93\n"
"v_mfma_f32_16x16x32_bf16 v[164:167], v[212:215], a[196:199], v[164:167]\n"
"s_waitcnt lgkmcnt(3)\n"
"ds_read_b128 v[220:223], v229 offset:5120\n"
"v_accvgpr_mov_b32 a121, a73\n"
"v_mul_f32_dpp v10, v246, v10 row_newbcast:2 row_mask:0xf bank_mask:0xf\n"
"v_exp_f32 v154, v154\n"
"v_exp_f32 v155, v155\n"
"v_mul_f32_dpp v27, v246, v27 row_newbcast:3 row_mask:0xf bank_mask:0xf\n"
"v_mul_f32_dpp v58, v247, v58 row_newbcast:2 row_mask:0xf bank_mask:0xf\n"
"s_branch LTC_%=\n"
"LTH2_%=:\n"
"v_mfma_f32_16x16x32_bf16 a[224:227], v[124:127], a[244:247], a[224:227]\n"
"v_exp_f32 v170, v170\n"
"v_mfma_f32_16x16x32_bf16 v[16:19], v[128:131], a[244:247], v[16:19]\n"
"s_waitcnt vmcnt(8)\n"
"v_exp_f32 v168, v168\n"
"v_mfma_f32_16x16x32_bf16 v[16:19], v[132:135], a[248:251], v[16:19]\n"
"s_barrier\n"
"v_mfma_f32_16x16x32_bf16 v[48:51], v[136:139], a[244:247], v[48:51]\n"
"v_exp_f32 v173, v173\n"
"v_mfma_f32_16x16x32_bf16 v[48:51], v[140:143], a[248:251], v[48:51]\n"
"v_exp_f32 v171, v171\n"
"v_mfma_f32_16x16x32_bf16 v[80:83], v[144:147], a[244:247], v[80:83]\n"
"ds_read_b64_tr_b16 a[244:245], v234 offset:49152\n"
"ds_read_b64_tr_b16 a[246:247], v234 offset:53248\n"
"v_mfma_f32_16x16x32_bf16 v[80:83], v[148:151], a[248:251], v[80:83]\n"
"v_exp_f32 v179, v179\n"
"v_mfma_f32_16x16x32_bf16 v[84:87], v[148:151], v[224:227], v[84:87]\n"
"ds_read_b64_tr_b16 a[250:251], v238 offset:53248\n"
"v_cvt_pk_bf16_f32 v100, v156, v157\n"
"v_mfma_f32_16x16x32_bf16 v[84:87], v[144:147], a[252:255], v[84:87]\n"
"v_exp_f32 v169, v169\n"
"v_exp_f32 v172, v172\n"
"v_exp_f32 v175, v175\n"
"v_exp_f32 v176, v176\n"
"v_exp_f32 v178, v178\n"
"v_mfma_f32_16x16x32_bf16 v[52:55], v[140:143], v[224:227], v[52:55]\n"
"ds_read_b64_tr_b16 a[248:249], v238 offset:49152\n"
"v_cvt_pk_bf16_f32 v117, v166, v167\n"
"v_mfma_f32_16x16x32_bf16 v[52:55], v[136:139], a[252:255], v[52:55]\n"
"v_exp_f32 v174, v174\n"
"v_mfma_f32_16x16x32_bf16 v[20:23], v[132:135], v[224:227], v[20:23]\n"
"ds_read_b64_tr_b16 v[224:225], v239 offset:49152\n"
"ds_read_b64_tr_b16 v[226:227], v239 offset:53248\n"
"v_mfma_f32_16x16x32_bf16 v[20:23], v[128:131], a[252:255], v[20:23]\n"
"v_cvt_pk_bf16_f32 v102, v168, v169\n"
"v_cvt_pk_bf16_f32 v103, v170, v171\n"
"v_cvt_pk_bf16_f32 v111, v174, v175\n"
"v_exp_f32 v177, v177\n"
"v_mfma_f32_16x16x32_bf16 a[228:231], v[124:127], a[252:255], a[228:231]\n"
"s_waitcnt lgkmcnt(4)\n"
"ds_read_b64_tr_b16 a[254:255], v235 offset:53248\n"
"v_cvt_pk_bf16_f32 v110, v172, v173\n"
"v_mfma_f32_16x16x32_bf16 a[216:219], v[120:123], a[244:247], a[216:219]\n"
"s_add_u32 m0, s44, 81920\n"
"ds_read_b64_tr_b16 a[252:253], v235 offset:49152\n"
"v_cvt_pk_bf16_f32 v116, v164, v165\n"
"v_smfmac_f32_16x16x64_bf16 v[196:199], a[240:243], v[96:103], v241\n"
"buffer_load_dwordx4 v240, s[20:23], s56 offen nt lds\n"
"v_mfma_f32_16x16x32_bf16 v[8:11], v[96:99], a[244:247], v[8:11]\n"
"s_waitcnt lgkmcnt(4)\n"
"v_cvt_pk_bf16_f32 v118, v176, v177\n"
"v_cvt_pk_bf16_f32 v119, v178, v179\n"
"v_mfma_f32_16x16x32_bf16 v[8:11], v[100:103], a[248:251], v[8:11]\n"
"v_cvt_pk_bf16_f32 v108, v160, v161\n"
"v_and_b32 v245, 0x3f0, v242\n"
"v_smfmac_f32_16x16x64_bf16 v[200:203], a[240:243], v[112:119], v241\n"
"ds_read_b128 v[212:215], v229 offset:0\n"
"v_cvt_pk_bf16_f32 v109, v162, v163\n"
"v_mfma_f32_16x16x32_bf16 v[40:43], v[104:107], a[244:247], v[40:43]\n"
"v_lshlrev_b32_e64 v244, 24, s43\n"
"v_lshrrev_b32 v245, 1, v245\n"
"v_mfma_f32_16x16x32_bf16 v[40:43], v[108:111], a[248:251], v[40:43]\n"
"ds_read_b128 v[208:211], v228 offset:0\n"
"v_sub_u32 v245, v242, v245\n"
"v_smfmac_f32_16x16x64_bf16 v[198:201], a[240:243], v[104:111], v241\n"
"ds_read_b128 v[220:223], v229 offset:1024\n"
"v_add_f32 v196, v196, v197\n"
"v_mfma_f32_16x16x32_bf16 v[72:75], v[112:115], a[244:247], v[72:75]\n"
"ds_read_b64_tr_b16 a[244:245], v234 offset:50176\n"
"v_mul_f32 v244, 0x45800000, v244\n"
"v_mfma_f32_16x16x32_bf16 v[72:75], v[116:119], a[248:251], v[72:75]\n"
"ds_read_b64_tr_b16 a[246:247], v234 offset:54272\n"
"ds_read_b64_tr_b16 a[250:251], v238 offset:54272\n"
"v_mfma_f32_16x16x32_bf16 v[202:205], a[240:243], v[120:123], v[202:205]\n"
"s_waitcnt lgkmcnt(6)\n"
"ds_read_b128 v[216:219], v228 offset:1024\n"
"v_add_f32 v198, v198, v199\n"
"v_mfma_f32_16x16x32_bf16 v[76:79], v[116:119], v[224:227], v[76:79]\n"
"ds_read_b64_tr_b16 a[248:249], v238 offset:50176\n"
"v_add_f32 v254, v196, v198\n"
"v_mfma_f32_16x16x32_bf16 v[76:79], v[112:115], a[252:255], v[76:79]\n"
"v_add_f32 v200, v200, v201\n"
"v_add_f32 v254, v254, v200\n"
"v_mfma_f32_16x16x32_bf16 v[44:47], v[108:111], v[224:227], v[44:47]\n"
"v_add_f32 v244, v244, v202\n"
"v_add_f32 v254, v254, v202\n"
"v_mfma_f32_16x16x32_bf16 v[44:47], v[104:107], a[252:255], v[44:47]\n"
"v_min3_f32 v244, v244, v196, v198\n"
"v_min_f32 v244, v244, v200\n"
"v_mfma_f32_16x16x32_bf16 v[12:15], v[100:103], v[224:227], v[12:15]\n"
"s_add_u32 s40, s56, s52\n"
"v_rcp_f32 v244, v244\n"
"v_mfma_f32_16x16x32_bf16 v[12:15], v[96:99], a[252:255], v[12:15]\n"
"buffer_load_dwordx4 v240, s[20:23], s40 offen offset:1024 nt lds\n"
"v_mfma_f32_16x16x32_bf16 a[220:223], v[120:123], a[252:255], a[220:223]\n"
"s_waitcnt lgkmcnt(2)\n"
"v_fmac_f32 v254, 0x68800000, v244\n"
"v_cmp_nge_f32 vcc, s45, v254\n"
"v_xor_b32 v244, 0x11000, v242\n"
"s_cbranch_vccnz LSLOW_%=\nLBACK2_%=:\ns_mov_b64 vcc, s[64:65]\n"
"v_mfma_f32_16x16x32_bf16 a[232:235], v[120:123], a[244:247], a[232:235]\n"
"v_accvgpr_mov_b32 a6, a150\n"
"v_accvgpr_mov_b32 a8, a152\n"
"v_accvgpr_mov_b32 a9, a153\n"
"v_accvgpr_mov_b32 a14, a158\n"
"v_accvgpr_mov_b32 a20, a164\n"
"v_accvgpr_mov_b32 a42, a186\n"
"v_accvgpr_mov_b32 a44, a188\n"
"v_xor_b32 v148, 0x400, v243\n"
"v_bfe_u32 v149, v242, 8, 2\n"
"v_lshl_add_u32 v149, v149, 4, s43\n"
"v_mfma_f32_16x16x32_bf16 v[24:27], v[96:99], a[244:247], v[24:27]\n"
"s_waitcnt lgkmcnt(0)\n"
"ds_read_b128 v[124:127], v149 offset:0\n"
"v_rcp_f32 v206, v200\n"
"v_accvgpr_mov_b32 a4, a148\n"
"v_accvgpr_mov_b32 a12, a156\n"
"v_accvgpr_mov_b32 a13, a157\n"
"v_accvgpr_mov_b32 a16, a160\n"
"v_accvgpr_mov_b32 a39, a183\n"
"v_accvgpr_mov_b32 a47, a191\n"
"v_xor_b32 v150, 64, v149\n"
"v_mfma_f32_16x16x32_bf16 v[24:27], v[100:103], a[248:251], v[24:27]\n"
"ds_read_b128 v[128:131], v150 offset:0\n"
"v_accvgpr_mov_b32 a0, a144\n"
"v_accvgpr_mov_b32 a1, a145\n"
"v_accvgpr_mov_b32 a5, a149\n"
"v_accvgpr_mov_b32 a10, a154\n"
"v_accvgpr_mov_b32 a17, a161\n"
"v_accvgpr_mov_b32 a40, a184\n"
"v_mfma_f32_16x16x32_bf16 v[56:59], v[104:107], a[244:247], v[56:59]\n"
"ds_read_b128 v[132:135], v149 offset:1024\n"
"v_rcp_f32 v246, v196\n"
"v_mov_b32_dpp v206, v206 row_ror:12 row_mask:0x2 bank_mask:0xf\n"
"v_accvgpr_mov_b32 a2, a146\n"
"v_accvgpr_mov_b32 a22, a166\n"
"v_accvgpr_mov_b32 a23, a167\n"
"v_accvgpr_mov_b32 a36, a180\n"
"v_accvgpr_mov_b32 a38, a182\n"
"v_accvgpr_mov_b32 a45, a189\n"
"v_mfma_f32_16x16x32_bf16 v[56:59], v[108:111], a[248:251], v[56:59]\n"
"ds_read_b128 v[136:139], v150 offset:1024\n"
"v_accvgpr_mov_b32 a33, a177\n"
"v_mfma_f32_16x16x32_bf16 v[88:91], v[112:115], a[244:247], v[88:91]\n"
"ds_read_b64_tr_b16 v[224:225], v239 offset:50176\n"
"v_accvgpr_mov_b32 a3, a147\n"
"v_accvgpr_mov_b32 a7, a151\n"
"v_accvgpr_mov_b32 a11, a155\n"
"v_accvgpr_mov_b32 a19, a163\n"
"v_accvgpr_mov_b32 a21, a165\n"
"v_accvgpr_mov_b32 a24, a168\n"
"v_accvgpr_mov_b32 a27, a171\n"
"v_accvgpr_mov_b32 a30, a174\n"
"v_accvgpr_mov_b32 a46, a190\n"
"v_mfma_f32_16x16x32_bf16 v[88:91], v[116:119], a[248:251], v[88:91]\n"
"s_waitcnt lgkmcnt(4)\n"
"ds_read_b128 a[204:207], v148 offset:6144\n"
"v_mov_b32_dpp v206, v206 row_ror:8 row_mask:0x4 bank_mask:0xf\n"
"v_accvgpr_mov_b32 a15, a159\n"
"v_accvgpr_mov_b32 a25, a169\n"
"v_accvgpr_mov_b32 a26, a170\n"
"v_accvgpr_mov_b32 a29, a173\n"
"v_accvgpr_mov_b32 a31, a175\n"
"v_accvgpr_mov_b32 a35, a179\n"
"v_accvgpr_mov_b32 a43, a187\n"
"v_accvgpr_mov_b32 a160, a64\n"
"v_mfma_f32_16x16x32_bf16 v[180:183], v[124:127], a[0:3], 0\n"
"s_waitcnt lgkmcnt(4)\n"
"v_mov_b32 v252, v192\n"
"v_accvgpr_mov_b32 a174, a78\n"
"v_mfma_f32_16x16x32_bf16 v[180:183], v[128:131], a[4:7], v[180:183]\n"
"s_waitcnt lgkmcnt(3)\n"
"ds_read_b128 a[192:195], v243 offset:4096\n"
"v_accvgpr_mov_b32 a18, a162\n"
"v_mfma_f32_16x16x32_bf16 v[180:183], v[132:135], a[8:11], v[180:183]\n"
"s_waitcnt lgkmcnt(3)\n"
"ds_read_b128 a[200:203], v243 offset:6144\n"
"v_accvgpr_mov_b32 a183, a87\n"
"v_mfma_f32_16x16x32_bf16 v[180:183], v[136:139], a[12:15], v[180:183]\n"
"v_rcp_f32 v247, v198\n"
"v_accvgpr_mov_b32 a28, a172\n"
"v_accvgpr_mov_b32 a34, a178\n"
"v_accvgpr_mov_b32 a41, a185\n"
"v_accvgpr_mov_b32 a151, a55\n"
"v_accvgpr_mov_b32 a167, a71\n"
"v_accvgpr_mov_b32 a156, a60\n"
"v_accvgpr_mov_b32 a178, a82\n"
"v_mfma_f32_16x16x32_bf16 v[184:187], v[124:127], a[16:19], 0\n"
"ds_read_b128 a[196:199], v148 offset:4096\n"
"v_accvgpr_mov_b32 a158, a62\n"
"v_mfma_f32_16x16x32_bf16 v[184:187], v[128:131], a[20:23], v[184:187]\n"
"v_mov_b32_dpp v247, v247 row_ror:12 row_mask:0x2 bank_mask:0xf\n"
"v_accvgpr_mov_b32 a152, a56\n"
"v_mfma_f32_16x16x32_bf16 v[184:187], v[132:135], a[24:27], v[184:187]\n"
"s_add_u32 s40, s56, s53\n"
"v_accvgpr_mov_b32 a32, a176\n"
"v_accvgpr_mov_b32 a157, a61\n"
"v_mfma_f32_16x16x32_bf16 v[184:187], v[136:139], a[28:31], v[184:187]\n"
"buffer_load_dwordx4 v240, s[20:23], s40 offen offset:2048 nt lds\n"
"v_mov_b32_dpp v206, v206 row_ror:4 row_mask:0x8 bank_mask:0xf\n"
"v_accvgpr_mov_b32 a37, a181\n"
"v_accvgpr_mov_b32 a149, a53\n"
"v_accvgpr_mov_b32 a150, a54\n"
"v_accvgpr_mov_b32 a181, a85\n"
"v_accvgpr_mov_b32 a153, a57\n"
"v_sub_f32 v180, s74, v180\n"
"v_sub_f32 v183, s74, v183\n"
"v_mfma_f32_16x16x32_bf16 v[188:191], v[124:127], a[32:35], 0\n"
"v_mov_b32_dpp v246, v246 row_ror:12 row_mask:0x2 bank_mask:0xf\n"
"v_accvgpr_mov_b32 a180, a84\n"
"v_mfma_f32_16x16x32_bf16 v[188:191], v[128:131], a[36:39], v[188:191]\n"
"v_accvgpr_mov_b32 a162, a66\n"
"v_accvgpr_mov_b32 a179, a83\n"
"v_mfma_f32_16x16x32_bf16 v[188:191], v[132:135], a[40:43], v[188:191]\n"
"v_mov_b32_dpp v246, v246 row_ror:8 row_mask:0x4 bank_mask:0xf\n"
"v_sub_f32 v182, s74, v182\n"
"v_mfma_f32_16x16x32_bf16 v[188:191], v[136:139], a[44:47], v[188:191]\n"
"s_waitcnt lgkmcnt(1)\n"
"ds_write_b128 v244, a[220:223] offset:33792\n"
"v_mov_b32_dpp v247, v247 row_ror:8 row_mask:0x4 bank_mask:0xf\n"
"v_accvgpr_mov_b32 a163, a67\n"
"v_accvgpr_mov_b32 a177, a81\n"
"v_accvgpr_mov_b32 a154, a58\n"
"v_mfma_f32_16x16x32_bf16 v[192:195], v[124:127], a[192:195], 0\n"
"s_waitcnt lgkmcnt(1)\n"
"v_accvgpr_mov_b32 a164, a68\n"
"v_sub_f32 v181, s74, v181\n"
"v_mfma_f32_16x16x32_bf16 v[192:195], v[128:131], a[196:199], v[192:195]\n"
"v_accvgpr_mov_b32 a173, a77\n"
"v_accvgpr_mov_b32 a144, a48\n"
"v_mfma_f32_16x16x32_bf16 v[192:195], v[132:135], a[200:203], v[192:195]\n"
"ds_read_b64_tr_b16 v[226:227], v239 offset:54272\n"
"ds_read_b64_tr_b16 a[246:247], v232 offset:53248\n"
"v_mfma_f32_16x16x32_bf16 v[192:195], v[136:139], a[204:207], v[192:195]\n"
"v_mov_b32_dpp v246, v246 row_ror:4 row_mask:0x8 bank_mask:0xf\n"
"v_mov_b32_dpp v247, v247 row_ror:4 row_mask:0x8 bank_mask:0xf\n"
"v_accvgpr_mov_b32 a148, a52\n"
"v_accvgpr_mov_b32 a165, a69\n"
"v_accvgpr_mov_b32 a175, a79\n"
"v_accvgpr_mov_b32 a176, a80\n"
"v_sub_f32 v184, s74, v184\n"
"v_sub_f32 v187, s74, v187\n"
"v_sub_f32 v190, s74, v190\n"
"v_mfma_f32_16x16x32_bf16 v[152:155], v[208:211], a[0:3], v[180:183]\n"
"v_mov_b32 v253, v202\n"
"v_sub_f32 v185, s74, v185\n"
"v_mfma_f32_16x16x32_bf16 v[152:155], v[212:215], a[4:7], v[152:155]\n"
"v_accvgpr_mov_b32 a186, a90\n"
"v_sub_f32 v186, s74, v186\n"
"v_mfma_f32_16x16x32_bf16 v[152:155], v[216:219], a[8:11], v[152:155]\n"
"s_add_u32 s40, s56, s54\n"
"ds_read_b64_tr_b16 a[254:255], v235 offset:54272\n"
"v_sub_f32 v189, s74, v189\n"
"v_mfma_f32_16x16x32_bf16 v[152:155], v[220:223], a[12:15], v[152:155]\n"
"buffer_load_dwordx4 v240, s[20:23], s40 offen offset:3072 nt lds\n"
"v_accvgpr_mov_b32 a190, a94\n"
"v_accvgpr_mov_b32 a191, a95\n"
"v_accvgpr_mov_b32 a168, a72\n"
"v_mul_f32_dpp v9, v246, v9 row_newbcast:1 row_mask:0xf bank_mask:0xf\n"
"v_mul_f32_dpp v12, v246, v12 row_newbcast:0 row_mask:0xf bank_mask:0xf\n"
"v_sub_f32 v188, s74, v188\n"
"v_sub_f32 v191, s74, v191\n"
"v_mfma_f32_16x16x32_bf16 v[156:159], v[216:219], a[24:27], v[184:187]\n"
"s_add_u32 s55, s55, s59\n"
"v_accvgpr_mov_b32 a159, a63\n"
"v_accvgpr_mov_b32 a155, a59\n"
"v_mfma_f32_16x16x32_bf16 v[156:159], v[220:223], a[28:31], v[156:159]\n"
"s_add_u32 s41, s55, s57\n"
"v_accvgpr_mov_b32 a145, a49\n"
"v_accvgpr_mov_b32 a170, a74\n"
"v_mfma_f32_16x16x32_bf16 v[156:159], v[208:211], a[16:19], v[156:159]\n"
"s_and_b32 s41, s41, s58\n"
"v_accvgpr_mov_b32 a147, a51\n"
"v_sub_f32 v192, s74, v192\n"
"v_mfma_f32_16x16x32_bf16 v[156:159], v[212:215], a[20:23], v[156:159]\n"
"s_andn2_b32 s56, s55, s58\n"
"ds_read_b64_tr_b16 a[250:251], v236 offset:53248\n"
"v_xor_b32 v250, 0x1000, v245\n"
"v_accvgpr_mov_b32 a146, a50\n"
"v_accvgpr_mov_b32 a161, a65\n"
"v_accvgpr_mov_b32 a187, a91\n"
"v_mul_f32_dpp v41, v247, v41 row_newbcast:1 row_mask:0xf bank_mask:0xf\n"
"v_mul_f32_dpp v46, v247, v46 row_newbcast:2 row_mask:0xf bank_mask:0xf\n"
"v_sub_f32 v195, s74, v195\n"
"v_exp_f32 v153, v153\n"
"v_mfma_f32_16x16x32_bf16 v[160:163], v[208:211], a[32:35], v[188:191]\n"
"s_or_b32 s56, s56, s41\n"
"v_mul_f32_dpp v43, v247, v43 row_newbcast:3 row_mask:0xf bank_mask:0xf\n"
"v_sub_f32 v193, s74, v193\n"
"v_mfma_f32_16x16x32_bf16 v[160:163], v[212:215], a[36:39], v[160:163]\n"
"v_accvgpr_mov_b32 a171, a75\n"
"v_accvgpr_mov_b32 a185, a89\n"
"v_mfma_f32_16x16x32_bf16 v[160:163], v[216:219], a[40:43], v[160:163]\n"
"v_accvgpr_mov_b32 a182, a86\n"
"v_sub_f32 v194, s74, v194\n"
"v_mfma_f32_16x16x32_bf16 v[160:163], v[220:223], a[44:47], v[160:163]\n"
"ds_read_b64_tr_b16 a[252:253], v235 offset:50176\n"
"v_accvgpr_mov_b32 a172, a76\n"
"v_accvgpr_mov_b32 a184, a88\n"
"v_exp_f32 v152, v152\n"
"v_exp_f32 v157, v157\n"
"v_exp_f32 v158, v158\n"
"v_mfma_f32_16x16x32_bf16 v[164:167], v[216:219], a[200:203], v[192:195]\n"
"ds_read_b128 v[216:219], v228 offset:5120\n"
"v_accvgpr_mov_b32 a166, a70\n"
"v_mfma_f32_16x16x32_bf16 v[164:167], v[220:223], a[204:207], v[164:167]\n"
"v_accvgpr_mov_b32 a188, a92\n"
"v_mul_f32_dpp v57, v247, v57 row_newbcast:1 row_mask:0xf bank_mask:0xf\n"
"v_mfma_f32_16x16x32_bf16 v[164:167], v[208:211], a[192:195], v[164:167]\n"
"ds_read_b128 v[208:211], v228 offset:4096\n"
"v_accvgpr_mov_b32 a189, a93\n"
"v_mfma_f32_16x16x32_bf16 v[164:167], v[212:215], a[196:199], v[164:167]\n"
"s_waitcnt lgkmcnt(3)\n"
"ds_read_b128 v[220:223], v229 offset:5120\n"
"v_accvgpr_mov_b32 a169, a73\n"
"v_mul_f32_dpp v10, v246, v10 row_newbcast:2 row_mask:0xf bank_mask:0xf\n"
"v_exp_f32 v154, v154\n"
"v_exp_f32 v155, v155\n"
"v_mul_f32_dpp v27, v246, v27 row_newbcast:3 row_mask:0xf bank_mask:0xf\n"
"v_mul_f32_dpp v58, v247, v58 row_newbcast:2 row_mask:0xf bank_mask:0xf\n"
"LTC_%=:\n"
"s_waitcnt lgkmcnt(4)\n"
"v_mfma_f32_16x16x32_bf16 v[92:95], v[116:119], v[224:227], v[92:95]\n"
"s_waitcnt lgkmcnt(3)\n"
"v_mul_f32_dpp v56, v247, v56 row_newbcast:0 row_mask:0xf bank_mask:0xf\n"
"v_cvt_pk_bf16_f32 v56, v56, v57\n"
"v_mfma_f32_16x16x32_bf16 v[92:95], v[112:115], a[252:255], v[92:95]\n"
"v_mul_f32_dpp v13, v246, v13 row_newbcast:1 row_mask:0xf bank_mask:0xf\n"
"v_mul_f32_dpp v44, v247, v44 row_newbcast:0 row_mask:0xf bank_mask:0xf\n"
"v_mfma_f32_16x16x32_bf16 v[60:63], v[108:111], v[224:227], v[60:63]\n"
"s_add_u32 m0, s44, 32768\n"
"ds_read_b128 v[212:215], v229 offset:4096\n"
"v_mul_f32_dpp v8, v246, v8 row_newbcast:0 row_mask:0xf bank_mask:0xf\n"
"v_mfma_f32_16x16x32_bf16 v[60:63], v[104:107], a[252:255], v[60:63]\n"
"buffer_load_dwordx4 v240, s[16:19], s56 offen nt lds\n"
"v_mfma_f32_16x16x32_bf16 v[28:31], v[100:103], v[224:227], v[28:31]\n"
"v_exp_f32 v165, v165\n"
"v_mfma_f32_16x16x32_bf16 v[28:31], v[96:99], a[252:255], v[28:31]\n"
"v_exp_f32 v166, v166\n"
"v_mfma_f32_16x16x32_bf16 a[236:239], v[120:123], a[252:255], a[236:239]\n"
"s_waitcnt lgkmcnt(2)\n"
"ds_read_b64_tr_b16 a[254:255], v233 offset:53248\n"
"v_mul_f32_dpp v42, v247, v42 row_newbcast:2 row_mask:0xf bank_mask:0xf\n"
"v_mfma_f32_16x16x32_bf16 v[168:171], v[216:219], a[8:11], v[180:183]\n"
"s_waitcnt lgkmcnt(2)\n"
"v_exp_f32 v156, v156\n"
"v_mfma_f32_16x16x32_bf16 v[168:171], v[220:223], a[12:15], v[168:171]\n"
"v_cvt_pk_bf16_f32 v8, v8, v9\n"
"v_mul_f32_dpp v76, v206, v76 row_newbcast:0 row_mask:0xf bank_mask:0xf\n"
"v_mfma_f32_16x16x32_bf16 v[168:171], v[208:211], a[0:3], v[168:171]\n"
"s_waitcnt lgkmcnt(1)\n"
"ds_read_b64_tr_b16 v[224:225], v237 offset:49152\n"
"v_mov_b64 v[202:203], 0\n"
"v_mfma_f32_16x16x32_bf16 v[168:171], v[212:215], a[4:7], v[168:171]\n"
"ds_write_b128 v244, a[236:239] offset:35840\n"
"v_mul_f32_dpp v77, v206, v77 row_newbcast:1 row_mask:0xf bank_mask:0xf\n"
"v_mul_f32_dpp v78, v206, v78 row_newbcast:2 row_mask:0xf bank_mask:0xf\n"
"v_cvt_pk_bf16_f32 v136, v156, v157\n"
"v_mul_f32_dpp v59, v247, v59 row_newbcast:3 row_mask:0xf bank_mask:0xf\n"
"v_mul_f32_dpp v88, v206, v88 row_newbcast:0 row_mask:0xf bank_mask:0xf\n"
"v_mfma_f32_16x16x32_bf16 v[172:175], v[208:211], a[16:19], v[184:187]\n"
"v_mul_f32_dpp v15, v246, v15 row_newbcast:3 row_mask:0xf bank_mask:0xf\n"
"v_mul_f32_dpp v25, v246, v25 row_newbcast:1 row_mask:0xf bank_mask:0xf\n"
"v_mfma_f32_16x16x32_bf16 v[172:175], v[212:215], a[20:23], v[172:175]\n"
"ds_read_b64_tr_b16 a[244:245], v232 offset:49152\n"
"v_cvt_pk_bf16_f32 v129, v154, v155\n"
"v_mfma_f32_16x16x32_bf16 v[172:175], v[216:219], a[24:27], v[172:175]\n"
"ds_read_b64_tr_b16 a[248:249], v236 offset:49152\n"
"v_mul_f32_dpp v11, v246, v11 row_newbcast:3 row_mask:0xf bank_mask:0xf\n"
"v_mfma_f32_16x16x32_bf16 v[172:175], v[220:223], a[28:31], v[172:175]\n"
"v_mul_f32_dpp v14, v246, v14 row_newbcast:2 row_mask:0xf bank_mask:0xf\n"
"v_mul_f32_dpp v45, v247, v45 row_newbcast:1 row_mask:0xf bank_mask:0xf\n"
"v_mfma_f32_16x16x32_bf16 v[176:179], v[216:219], a[40:43], v[188:191]\n"
"ds_read_b64_tr_b16 a[252:253], v233 offset:49152\n"
"v_mul_f32_dpp v73, v206, v73 row_newbcast:1 row_mask:0xf bank_mask:0xf\n"
"v_mfma_f32_16x16x32_bf16 v[176:179], v[220:223], a[44:47], v[176:179]\n"
"v_mul_f32_dpp v72, v206, v72 row_newbcast:0 row_mask:0xf bank_mask:0xf\n"
"v_mul_f32_dpp v93, v206, v93 row_newbcast:1 row_mask:0xf bank_mask:0xf\n"
"v_mfma_f32_16x16x32_bf16 v[176:179], v[208:211], a[32:35], v[176:179]\n"
"s_add_u32 s40, s56, s52\n"
"ds_read_b64_tr_b16 v[226:227], v237 offset:53248\n"
"v_cvt_pk_bf16_f32 v128, v152, v153\n"
"v_mfma_f32_16x16x32_bf16 v[176:179], v[212:215], a[36:39], v[176:179]\n"
"buffer_load_dwordx4 v240, s[16:19], s40 offen offset:1024 nt lds\n"
"v_mul_f32_dpp v47, v247, v47 row_newbcast:3 row_mask:0xf bank_mask:0xf\n"
"v_exp_f32 v161, v161\n"
"v_mul_f32_dpp v90, v206, v90 row_newbcast:2 row_mask:0xf bank_mask:0xf\n"
"v_exp_f32 v169, v169\n"
"v_exp_f32 v170, v170\n"
"v_mfma_f32_16x16x32_bf16 v[152:155], v[208:211], a[192:195], v[192:195]\n"
"v_exp_f32 v162, v162\n"
"v_mfma_f32_16x16x32_bf16 v[152:155], v[212:215], a[196:199], v[152:155]\n"
"v_exp_f32 v159, v159\n"
"v_mfma_f32_16x16x32_bf16 v[152:155], v[216:219], a[200:203], v[152:155]\n"
"ds_read_b128 v[212:215], v231 offset:0\n"
"v_cvt_pk_bf16_f32 v137, v158, v159\n"
"v_mfma_f32_16x16x32_bf16 v[152:155], v[220:223], a[204:207], v[152:155]\n"
"s_waitcnt lgkmcnt(3)\n"
"ds_read_b128 v[220:223], v231 offset:1024\n"
"v_mul_f32_dpp v30, v246, v30 row_newbcast:2 row_mask:0xf bank_mask:0xf\n"
"v_mfma_f32_16x16x32_bf16 a[208:211], v[120:123], a[244:247], a[208:211]\n"
"ds_read_b128 v[208:211], v230 offset:0\n"
"v_exp_f32 v163, v163\n"
"v_exp_f32 v167, v167\n"
"v_mul_f32_dpp v62, v247, v62 row_newbcast:2 row_mask:0xf bank_mask:0xf\n"
"v_exp_f32 v168, v168\n"
"v_exp_f32 v178, v178\n"
"v_mfma_f32_16x16x32_bf16 v[0:3], v[96:99], a[244:247], v[0:3]\n"
"v_cvt_pk_bf16_f32 v125, v166, v167\n"
"v_mul_f32_dpp v61, v247, v61 row_newbcast:1 row_mask:0xf bank_mask:0xf\n"
"v_mfma_f32_16x16x32_bf16 v[0:3], v[100:103], a[248:251], v[0:3]\n"
"ds_read_b128 v[216:219], v230 offset:1024\n"
"v_mul_f32_dpp v40, v247, v40 row_newbcast:0 row_mask:0xf bank_mask:0xf\n"
"v_cvt_pk_bf16_f32 v40, v40, v41\n"
"v_exp_f32 v160, v160\n"
"v_cvt_pk_bf16_f32 v145, v162, v163\n"
"v_exp_f32 v164, v164\n"
"v_cvt_pk_bf16_f32 v57, v58, v59\n"
"v_mfma_f32_16x16x32_bf16 v[32:35], v[104:107], a[244:247], v[32:35]\n"
"v_exp_f32 v174, v174\n"
"v_mfma_f32_16x16x32_bf16 v[32:35], v[108:111], a[248:251], v[32:35]\n"
"ds_write_b128 v244, a[232:235] offset:34816\n"
"v_mul_f32_dpp v74, v206, v74 row_newbcast:2 row_mask:0xf bank_mask:0xf\n"
"v_mul_f32_dpp v26, v246, v26 row_newbcast:2 row_mask:0xf bank_mask:0xf\n"
"v_exp_f32 v176, v176\n"
"v_mfma_f32_16x16x32_bf16 v[64:67], v[112:115], a[244:247], v[64:67]\n"
"v_mul_f32_dpp v94, v206, v94 row_newbcast:2 row_mask:0xf bank_mask:0xf\n"
"v_mul_f32_dpp v95, v206, v95 row_newbcast:3 row_mask:0xf bank_mask:0xf\n"
"v_mfma_f32_16x16x32_bf16 v[64:67], v[116:119], a[248:251], v[64:67]\n"
"s_waitcnt lgkmcnt(2)\n"
"ds_write_b128 v244, a[216:219] offset:32768\n"
"v_cvt_pk_bf16_f32 v41, v42, v43\n"
"v_mul_f32_dpp v79, v206, v79 row_newbcast:3 row_mask:0xf bank_mask:0xf\n"
"v_mul_f32_dpp v28, v246, v28 row_newbcast:0 row_mask:0xf bank_mask:0xf\n"
"v_mul_f32_dpp v29, v246, v29 row_newbcast:1 row_mask:0xf bank_mask:0xf\n"
"v_mul_f32_dpp v89, v206, v89 row_newbcast:1 row_mask:0xf bank_mask:0xf\n"
"v_mfma_f32_16x16x32_bf16 v[156:159], v[208:211], a[0:3], v[180:183]\n"
"v_exp_f32 v172, v172\n"
"v_mfma_f32_16x16x32_bf16 v[156:159], v[212:215], a[4:7], v[156:159]\n"
"s_waitcnt lgkmcnt(2)\n"
"v_exp_f32 v175, v175\n"
"v_mfma_f32_16x16x32_bf16 v[156:159], v[216:219], a[8:11], v[156:159]\n"
"s_add_u32 s40, s56, s53\n"
"ds_read_b64_tr_b16 a[250:251], v236 offset:54272\n"
"v_cvt_pk_bf16_f32 v144, v160, v161\n"
"v_mfma_f32_16x16x32_bf16 v[156:159], v[220:223], a[12:15], v[156:159]\n"
"buffer_load_dwordx4 v240, s[16:19], s40 offen offset:2048 nt lds\n"
"v_cvt_pk_bf16_f32 v72, v72, v73\n"
"v_mul_f32_dpp v24, v246, v24 row_newbcast:0 row_mask:0xf bank_mask:0xf\n"
"v_mul_f32_dpp v92, v206, v92 row_newbcast:0 row_mask:0xf bank_mask:0xf\n"
"v_cvt_pk_bf16_f32 v88, v88, v89\n"
"v_exp_f32 v177, v177\n"
"v_exp_f32 v155, v155\n"
"v_mfma_f32_16x16x32_bf16 v[160:163], v[216:219], a[24:27], v[184:187]\n"
"v_exp_f32 v171, v171\n"
"v_mfma_f32_16x16x32_bf16 v[160:163], v[220:223], a[28:31], v[160:163]\n"
"v_mul_f32_dpp v60, v247, v60 row_newbcast:0 row_mask:0xf bank_mask:0xf\n"
"v_mul_f32_dpp v32, v247, v32 row_newbcast:0 row_mask:0xf bank_mask:0xf\n"
"v_mfma_f32_16x16x32_bf16 v[160:163], v[208:211], a[16:19], v[160:163]\n"
"v_cvt_pk_bf16_f32 v130, v168, v169\n"
"v_mul_f32_dpp v0, v246, v0 row_newbcast:0 row_mask:0xf bank_mask:0xf\n"
"v_mfma_f32_16x16x32_bf16 v[160:163], v[212:215], a[20:23], v[160:163]\n"
"ds_read_b64_tr_b16 a[244:245], v232 offset:50176\n"
"v_cvt_pk_bf16_f32 v124, v164, v165\n"
"v_mul_f32_dpp v63, v247, v63 row_newbcast:3 row_mask:0xf bank_mask:0xf\n"
"v_mul_f32_dpp v91, v206, v91 row_newbcast:3 row_mask:0xf bank_mask:0xf\n"
"v_exp_f32 v179, v179\n"
"v_exp_f32 v152, v152\n"
"v_mul_f32_dpp v64, v206, v64 row_newbcast:0 row_mask:0xf bank_mask:0xf\n"
"v_mfma_f32_16x16x32_bf16 v[164:167], v[208:211], a[32:35], v[188:191]\n"
"ds_read_b64_tr_b16 a[248:249], v236 offset:50176\n"
"v_cvt_pk_bf16_f32 v24, v24, v25\n"
"v_mfma_f32_16x16x32_bf16 v[164:167], v[212:215], a[36:39], v[164:167]\n"
"v_cvt_pk_bf16_f32 v9, v10, v11\n"
"v_cvt_pk_bf16_f32 v11, v14, v15\n"
"v_mfma_f32_16x16x32_bf16 v[164:167], v[216:219], a[40:43], v[164:167]\n"
"ds_read_b128 v[216:219], v230 offset:5120\n"
"v_cvt_pk_bf16_f32 v25, v26, v27\n"
"v_mfma_f32_16x16x32_bf16 v[164:167], v[220:223], a[44:47], v[164:167]\n"
"ds_read_b128 v[212:215], v231 offset:4096\n"
"v_cvt_pk_bf16_f32 v43, v46, v47\n"
"v_cvt_pk_bf16_f32 v26, v28, v29\n"
"v_exp_f32 v173, v173\n"
"v_exp_f32 v154, v154\n"
"v_exp_f32 v157, v157\n"
"v_mul_f32_dpp v65, v206, v65 row_newbcast:1 row_mask:0xf bank_mask:0xf\n"
"v_mfma_f32_16x16x32_bf16 v[68:71], v[116:119], v[224:227], v[68:71]\n"
"ds_read_b128 v[208:211], v230 offset:4096\n"
"v_mul_f32_dpp v75, v206, v75 row_newbcast:3 row_mask:0xf bank_mask:0xf\n"
"v_mfma_f32_16x16x32_bf16 v[68:71], v[112:115], a[252:255], v[68:71]\n"
"ds_read_b64_tr_b16 a[246:247], v232 offset:54272\n"
"v_cvt_pk_bf16_f32 v58, v60, v61\n"
"v_cvt_pk_bf16_f32 v139, v174, v175\n"
"v_cvt_pk_bf16_f32 v146, v176, v177\n"
"v_exp_f32 v161, v161\n"
"v_exp_f32 v163, v163\n"
"v_mul_f32_dpp v2, v246, v2 row_newbcast:2 row_mask:0xf bank_mask:0xf\n"
"v_mfma_f32_16x16x32_bf16 v[36:39], v[108:111], v[224:227], v[36:39]\n"
"s_add_u32 s40, s56, s54\n"
"ds_read_b128 v[220:223], v231 offset:5120\n"
"v_cvt_pk_bf16_f32 v138, v172, v173\n"
"v_mfma_f32_16x16x32_bf16 v[36:39], v[104:107], a[252:255], v[36:39]\n"
"buffer_load_dwordx4 v240, s[16:19], s40 offen offset:3072 nt lds\n"
"v_cvt_pk_bf16_f32 v10, v12, v13\n"
"v_cvt_pk_bf16_f32 v73, v74, v75\n"
"v_cvt_pk_bf16_f32 v127, v154, v155\n"
"v_exp_f32 v156, v156\n"
"v_exp_f32 v162, v162\n"
"v_mfma_f32_16x16x32_bf16 v[4:7], v[100:103], v[224:227], v[4:7]\n"
"v_mul_f32_dpp v31, v246, v31 row_newbcast:3 row_mask:0xf bank_mask:0xf\n"
"v_mul_f32_dpp v34, v247, v34 row_newbcast:2 row_mask:0xf bank_mask:0xf\n"
"v_mfma_f32_16x16x32_bf16 v[4:7], v[96:99], a[252:255], v[4:7]\n"
"v_cvt_pk_bf16_f32 v59, v62, v63\n"
"v_cvt_pk_bf16_f32 v131, v170, v171\n"
"v_mfma_f32_16x16x32_bf16 a[212:215], v[120:123], a[252:255], a[212:215]\n"
"s_waitcnt lgkmcnt(2)\n"
"ds_write_b64 v250, v[252:253] offset:52224\n"
"v_cvt_pk_bf16_f32 v42, v44, v45\n"
"v_cvt_pk_bf16_f32 v89, v90, v91\n"
"v_exp_f32 v153, v153\n"
"v_mul_f32_dpp v3, v246, v3 row_newbcast:3 row_mask:0xf bank_mask:0xf\n"
"v_mul_f32_dpp v33, v247, v33 row_newbcast:1 row_mask:0xf bank_mask:0xf\n"
"v_mul_f32_dpp v69, v206, v69 row_newbcast:1 row_mask:0xf bank_mask:0xf\n"
"v_mfma_f32_16x16x32_bf16 v[168:171], v[208:211], a[0:3], v[180:183]\n"
"ds_read_b64_tr_b16 v[226:227], v237 offset:54272\n"
"v_mul_f32_dpp v39, v247, v39 row_newbcast:3 row_mask:0xf bank_mask:0xf\n"
"v_mfma_f32_16x16x32_bf16 v[168:171], v[212:215], a[4:7], v[168:171]\n"
"v_cvt_pk_bf16_f32 v74, v76, v77\n"
"v_mul_f32_dpp v1, v246, v1 row_newbcast:1 row_mask:0xf bank_mask:0xf\n"
"v_mfma_f32_16x16x32_bf16 v[168:171], v[216:219], a[8:11], v[168:171]\n"
"s_waitcnt lgkmcnt(2)\n"
"ds_read_b64_tr_b16 a[254:255], v233 offset:54272\n"
"v_mul_f32_dpp v68, v206, v68 row_newbcast:0 row_mask:0xf bank_mask:0xf\n"
"v_mfma_f32_16x16x32_bf16 v[168:171], v[220:223], a[12:15], v[168:171]\n"
"ds_write_b128 v248, v[40:43] offset:50176\n"
"v_cvt_pk_bf16_f32 v75, v78, v79\n"
"v_exp_f32 v158, v158\n"
"v_exp_f32 v160, v160\n"
"v_mfma_f32_16x16x32_bf16 v[172:175], v[216:219], a[24:27], v[184:187]\n"
"ds_read_b64_tr_b16 a[68:69], v249 offset:50176\n"
"v_cvt_pk_bf16_f32 v140, v160, v161\n"
"v_mfma_f32_16x16x32_bf16 v[172:175], v[220:223], a[28:31], v[172:175]\n"
"v_cvt_pk_bf16_f32 v147, v178, v179\n"
"v_mul_f32_dpp v70, v206, v70 row_newbcast:2 row_mask:0xf bank_mask:0xf\n"
"v_mfma_f32_16x16x32_bf16 v[172:175], v[208:211], a[16:19], v[172:175]\n"
"v_mul_f32_dpp v35, v247, v35 row_newbcast:3 row_mask:0xf bank_mask:0xf\n"
"v_mul_f32_dpp v36, v247, v36 row_newbcast:0 row_mask:0xf bank_mask:0xf\n"
"v_mfma_f32_16x16x32_bf16 v[172:175], v[212:215], a[20:23], v[172:175]\n"
"s_cbranch_vccnz LPCT0_%=\nLPKT0_%=:\n"
"ds_write_b128 v248, v[8:11] offset:49152\n"
"v_cvt_pk_bf16_f32 v91, v94, v95\n"
"v_mul_f32_dpp v37, v247, v37 row_newbcast:1 row_mask:0xf bank_mask:0xf\n"
"v_mul_f32_dpp v66, v206, v66 row_newbcast:2 row_mask:0xf bank_mask:0xf\n"
"v_mul_f32_dpp v67, v206, v67 row_newbcast:3 row_mask:0xf bank_mask:0xf\n"
"v_mfma_f32_16x16x32_bf16 v[176:179], v[208:211], a[32:35], v[188:191]\n"
"ds_read_b64_tr_b16 a[70:71], v249 offset:50184\n"
"ds_read_b64_tr_b16 v[224:225], v237 offset:50176\n"
"v_mfma_f32_16x16x32_bf16 v[176:179], v[212:215], a[36:39], v[176:179]\n"
"ds_read_b64_tr_b16 a[252:253], v233 offset:50176\n"
"v_mul_f32_dpp v7, v246, v7 row_newbcast:3 row_mask:0xf bank_mask:0xf\n"
"v_mfma_f32_16x16x32_bf16 v[176:179], v[216:219], a[40:43], v[176:179]\n"
"ds_read_b64_tr_b16 a[52:53], v249 offset:49152\n"
"ds_read_b64_tr_b16 a[54:55], v249 offset:49160\n"
"v_mfma_f32_16x16x32_bf16 v[176:179], v[220:223], a[44:47], v[176:179]\n"
"ds_write_b128 v248, v[56:59] offset:50176\n"
"v_cvt_pk_bf16_f32 v27, v30, v31\n"
"v_exp_f32 v171, v171\n"
"v_exp_f32 v172, v172\n"
"v_mfma_f32_16x16x32_bf16 a[224:227], v[120:123], a[244:247], a[224:227]\n"
"ds_write_b128 v248, v[24:27] offset:49152\n"
"v_exp_f32 v164, v164\n"
"v_mul_f32_dpp v6, v246, v6 row_newbcast:2 row_mask:0xf bank_mask:0xf\n"
"v_cvt_pk_bf16_f32 v0, v0, v1\n"
"v_xor_b32 v244, 0x1000, v244\n"
"v_mfma_f32_16x16x32_bf16 v[16:19], v[96:99], a[244:247], v[16:19]\n"
"s_waitcnt vmcnt(8) lgkmcnt(4)\n"
"ds_read_b64_tr_b16 a[78:79], v249 offset:50184\n"
"v_cvt_pk_bf16_f32 v1, v2, v3\n"
"v_mfma_f32_16x16x32_bf16 v[16:19], v[100:103], a[248:251], v[16:19]\n"
"s_barrier\n"
"ds_read_b64_tr_b16 a[60:61], v249 offset:49152\n"
"ds_read_b128 v[216:219], v228 offset:17408\n"
"v_cvt_pk_bf16_f32 v32, v32, v33\n"
"v_mul_f32_dpp v71, v206, v71 row_newbcast:3 row_mask:0xf bank_mask:0xf\n"
"v_cvt_pk_bf16_f32 v64, v64, v65\n"
"v_cvt_pk_bf16_f32 v65, v66, v67\n"
"v_exp_f32 v174, v174\n"
"v_mfma_f32_16x16x32_bf16 v[48:51], v[104:107], a[244:247], v[48:51]\n"
"ds_read_b128 v[28:31], v244 offset:33792\n"
"v_mul_f32_dpp v4, v246, v4 row_newbcast:0 row_mask:0xf bank_mask:0xf\n"
"v_mfma_f32_16x16x32_bf16 v[48:51], v[108:111], a[248:251], v[48:51]\n"
"ds_write_b128 v248, v[72:75] offset:51200\n"
"v_cvt_pk_bf16_f32 v126, v152, v153\n"
"v_cvt_pk_bf16_f32 v132, v156, v157\n"
"v_exp_f32 v165, v165\n"
"v_mfma_f32_16x16x32_bf16 v[80:83], v[112:115], a[244:247], v[80:83]\n"
"ds_read_b128 v[208:211], v228 offset:16384\n"
"v_mul_f32_dpp v38, v247, v38 row_newbcast:2 row_mask:0xf bank_mask:0xf\n"
"v_mfma_f32_16x16x32_bf16 v[80:83], v[116:119], a[248:251], v[80:83]\n"
"ds_read_b64_tr_b16 a[76:77], v249 offset:50176\n"
"ds_read_b128 v[212:215], v229 offset:16384\n"
"v_exp_f32 v159, v159\n"
"v_cvt_pk_bf16_f32 v33, v34, v35\n"
"v_exp_f32 v168, v168\n"
"v_exp_f32 v173, v173\n"
"v_mfma_f32_16x16x32_bf16 v[84:87], v[116:119], v[224:227], v[84:87]\n"
"ds_read_b64_tr_b16 a[84:85], v249 offset:51200\n"
"ds_read_b64 v[250:251], v245 offset:52224\n"
"v_mfma_f32_16x16x32_bf16 v[84:87], v[112:115], a[252:255], v[84:87]\n"
"ds_read_b64_tr_b16 a[86:87], v249 offset:51208\n"
"ds_read_b128 v[220:223], v229 offset:17408\n"
"v_cvt_pk_bf16_f32 v90, v92, v93\n"
"v_exp_f32 v166, v166\n"
"v_mul_f32_dpp v5, v246, v5 row_newbcast:1 row_mask:0xf bank_mask:0xf\n"
"v_cvt_pk_bf16_f32 v66, v68, v69\n"
"v_exp_f32 v175, v175\n"
"v_mfma_f32_16x16x32_bf16 v[52:55], v[108:111], v[224:227], v[52:55]\n"
"v_cvt_pk_bf16_f32 v34, v36, v37\n"
"v_mul_f32_dpp v81, v206, v81 row_newbcast:1 row_mask:0xf bank_mask:0xf\n"
"v_mfma_f32_16x16x32_bf16 v[52:55], v[104:107], a[252:255], v[52:55]\n"
"ds_write_b128 v248, v[88:91] offset:51200\n"
"v_cvt_pk_bf16_f32 v133, v158, v159\n"
"v_exp_f32 v177, v177\n"
"v_exp_f32 v179, v179\n"
"v_mfma_f32_16x16x32_bf16 v[20:23], v[100:103], v[224:227], v[20:23]\n"
"ds_read_b128 v[24:27], v244 offset:32768\n"
"v_cvt_pk_bf16_f32 v2, v4, v5\n"
"v_mfma_f32_16x16x32_bf16 v[20:23], v[96:99], a[252:255], v[20:23]\n"
"s_waitcnt lgkmcnt(4)\n"
"ds_read_b64_tr_b16 a[62:63], v249 offset:49160\n"
"ds_read_b64_tr_b16 a[244:245], v234 offset:0\n"
"ds_read_b64_tr_b16 a[248:249], v238 offset:0\n"
"v_exp_f32 v167, v167\n"
"v_cvt_pk_bf16_f32 v3, v6, v7\n"
"v_cvt_pk_bf16_f32 v35, v38, v39\n"
"v_exp_f32 v178, v178\n"
"v_min_f32 v254, v252, v250\n"
"v_mfma_f32_16x16x32_bf16 a[228:231], v[120:123], a[252:255], a[228:231]\n"
"ds_read_b64_tr_b16 a[92:93], v249 offset:51200\n"
"ds_read_b64_tr_b16 a[94:95], v249 offset:51208\n"
"v_mfma_f32_16x16x32_bf16 v[152:155], v[208:211], a[0:3], v[180:183]\n"
"ds_read_b128 v[60:63], v244 offset:35840\n"
"v_cvt_pk_bf16_f32 v67, v70, v71\n"
"v_mfma_f32_16x16x32_bf16 v[152:155], v[212:215], a[4:7], v[152:155]\n"
"ds_write_b128 v248, v[64:67] offset:51200\n"
"v_exp_f32 v169, v169\n"
"v_mul_f32_dpp v48, v247, v48 row_newbcast:0 row_mask:0xf bank_mask:0xf\n"
"v_mul_f32_dpp v84, v206, v84 row_newbcast:0 row_mask:0xf bank_mask:0xf\n"
"v_mov_b64 v[198:199], 0\n"
"v_mfma_f32_16x16x32_bf16 v[152:155], v[216:219], a[8:11], v[152:155]\n"
"s_waitcnt lgkmcnt(9)\n"
"ds_write_b128 v248, v[32:35] offset:50176\n"
"v_cvt_pk_bf16_f32 v141, v162, v163\n"
"v_cvt_pk_bf16_f32 v149, v166, v167\n"
"v_exp_f32 v170, v170\n"
"v_sub_f32 v252, v254, v252\n"
"v_mfma_f32_16x16x32_bf16 v[152:155], v[220:223], a[12:15], v[152:155]\n"
"ds_write_b128 v248, v[0:3] offset:49152\n"
"v_sub_f32 v250, v254, v250\n"
"v_exp_f32 v250, v250\n"
"v_mul_f32_dpp v50, v247, v50 row_newbcast:2 row_mask:0xf bank_mask:0xf\n"
"v_mfma_f32_16x16x32_bf16 v[156:159], v[216:219], a[24:27], v[184:187]\n"
"ds_read_b64_tr_b16 a[64:65], v249 offset:50176\n"
"ds_read_b128 v[56:59], v244 offset:34816\n"
"v_mfma_f32_16x16x32_bf16 v[156:159], v[220:223], a[28:31], v[156:159]\n"
"ds_read_b64_tr_b16 a[80:81], v249 offset:51200\n"
"v_mul_f32_dpp v17, v246, v17 row_newbcast:1 row_mask:0xf bank_mask:0xf\n"
"v_mfma_f32_16x16x32_bf16 v[156:159], v[208:211], a[16:19], v[156:159]\n"
"ds_read_b64_tr_b16 a[246:247], v234 offset:4096\n"
"v_cvt_pk_bf16_f32 v134, v168, v169\n"
"v_mfma_f32_16x16x32_bf16 v[156:159], v[212:215], a[20:23], v[156:159]\n"
"ds_read_b64_tr_b16 a[48:49], v249 offset:49152\n"
"ds_read_b64_tr_b16 a[50:51], v249 offset:49160\n"
"ds_read_b64_tr_b16 a[66:67], v249 offset:50184\n"
"ds_read_b64_tr_b16 a[82:83], v249 offset:51208\n"
"v_exp_f32 v176, v176\n"
"v_exp_f32 v252, v252\n"
"v_exp_f32 v153, v153\n"
"v_mfma_f32_16x16x32_bf16 v[160:163], v[208:211], a[32:35], v[188:191]\n"
"v_cvt_pk_bf16_f32 v142, v172, v173\n"
"v_accvgpr_read_b32 v254, a208\n"
"v_mfma_f32_16x16x32_bf16 v[160:163], v[212:215], a[36:39], v[160:163]\n"
"ds_read_b64_tr_b16 a[250:251], v238 offset:4096\n"
"v_cvt_pk_bf16_f32 v148, v164, v165\n"
"v_mfma_f32_16x16x32_bf16 v[160:163], v[216:219], a[40:43], v[160:163]\n"
"ds_read_b64_tr_b16 a[252:253], v235 offset:0\n"
"ds_read_b64_tr_b16 a[254:255], v235 offset:4096\n"
"v_mfma_f32_16x16x32_bf16 v[160:163], v[220:223], a[44:47], v[160:163]\n"
"v_exp_f32 v159, v159\n"
"v_mfma_f32_16x16x32_bf16 v[164:167], v[216:219], a[200:203], v[192:195]\n"
"ds_read_b64_tr_b16 v[226:227], v239 offset:4096\n"
"v_mul_f32 v253, v253, v252\n"
"v_mfma_f32_16x16x32_bf16 v[164:167], v[220:223], a[204:207], v[164:167]\n"
"v_cvt_pk_bf16_f32 v135, v170, v171\n"
"v_fmac_f32 v253, v251, v250\n"
"v_mfma_f32_16x16x32_bf16 v[164:167], v[208:211], a[192:195], v[164:167]\n"
"ds_read_b128 v[220:223], v229 offset:21504\n"
"v_cvt_pk_bf16_f32 v150, v176, v177\n"
"v_mfma_f32_16x16x32_bf16 v[164:167], v[212:215], a[196:199], v[164:167]\n"
"s_waitcnt lgkmcnt(5)\n"
"ds_read_b64_tr_b16 v[224:225], v239 offset:0\n"
"v_mfma_f32_16x16x32_bf16 a[216:219], v[124:127], a[244:247], 0\n"
"v_rcp_f32 v253, v253\n"
"v_mfma_f32_16x16x32_bf16 v[8:11], v[128:131], a[244:247], 0\n"
"s_waitcnt lgkmcnt(5)\n"
"s_add_u32 m0, s44, 98304\n"
"v_exp_f32 v154, v154\n"
"v_mfma_f32_16x16x32_bf16 v[8:11], v[132:135], a[248:251], v[8:11]\n"
"buffer_load_dwordx4 v240, s[20:23], s56 offen nt lds\n"
"v_cvt_pk_bf16_f32 v143, v174, v175\n"
"v_mul_f32 v250, v250, v253\n"
"v_mul_f32_dpp v49, v247, v49 row_newbcast:1 row_mask:0xf bank_mask:0xf\n"
"v_exp_f32 v152, v152\n"
"v_exp_f32 v164, v164\n"
"v_mfma_f32_16x16x32_bf16 v[40:43], v[136:139], a[244:247], 0\n"
"v_mul_f32 v252, v252, v253\n"
"v_mul_f32_dpp v16, v246, v16 row_newbcast:0 row_mask:0xf bank_mask:0xf\n"
"v_mfma_f32_16x16x32_bf16 v[40:43], v[140:143], a[248:251], v[40:43]\n"
"v_cvt_pk_bf16_f32 v151, v178, v179\n"
"v_mov_b32_dpp v250, v250 row_ror:12 row_mask:0x2 bank_mask:0xf\n"
"v_mov_b32_dpp v252, v252 row_ror:12 row_mask:0x2 bank_mask:0xf\n"
"v_accvgpr_read_b32 v253, a210\n"
"v_mul_f32_dpp v22, v246, v22 row_newbcast:2 row_mask:0xf bank_mask:0xf\n"
"v_exp_f32 v155, v155\n"
"v_exp_f32 v162, v162\n"
"v_mfma_f32_16x16x32_bf16 v[72:75], v[144:147], a[244:247], 0\n"
"ds_read_b128 v[212:215], v229 offset:20480\n"
"v_cvt_pk_bf16_f32 v97, v154, v155\n"
"v_mfma_f32_16x16x32_bf16 v[72:75], v[148:151], a[248:251], v[72:75]\n"
"s_waitcnt lgkmcnt(1)\n"
"ds_read_b64_tr_b16 a[246:247], v234 offset:5120\n"
"ds_read_b64_tr_b16 a[248:249], v238 offset:1024\n"
"v_mfma_f32_16x16x32_bf16 v[76:79], v[148:151], v[224:227], 0\n"
"v_mov_b32_dpp v252, v252 row_ror:8 row_mask:0x4 bank_mask:0xf\n"
"v_mul_f32_dpp v20, v246, v20 row_newbcast:0 row_mask:0xf bank_mask:0xf\n"
"v_mfma_f32_16x16x32_bf16 v[76:79], v[144:147], a[252:255], v[76:79]\n"
"ds_read_b128 v[216:219], v228 offset:21504\n"
"v_mov_b32_dpp v250, v250 row_ror:8 row_mask:0x4 bank_mask:0xf\n"
"v_mov_b32_dpp v252, v252 row_ror:4 row_mask:0x8 bank_mask:0xf\n"
"v_mov_b64 v[196:197], 0\n"
"v_exp_f32 v156, v156\n"
"v_exp_f32 v158, v158\n"
"v_exp_f32 v165, v165\n"
"v_mfma_f32_16x16x32_bf16 v[44:47], v[140:143], v[224:227], 0\n"
"v_mov_b32_dpp v250, v250 row_ror:4 row_mask:0x8 bank_mask:0xf\n"
"v_mul_f32_dpp v18, v246, v18 row_newbcast:2 row_mask:0xf bank_mask:0xf\n"
"v_mfma_f32_16x16x32_bf16 v[44:47], v[136:139], a[252:255], v[44:47]\n"
"v_mul_f32_dpp v24, v250, v24 row_newbcast:0 row_mask:0xf bank_mask:0xf\n"
"v_accvgpr_read_b32 v255, a209\n"
"v_mul_f32_dpp v25, v250, v25 row_newbcast:1 row_mask:0xf bank_mask:0xf\n"
"v_mul_f32_dpp v26, v250, v26 row_newbcast:2 row_mask:0xf bank_mask:0xf\n"
"v_mul_f32_dpp v19, v246, v19 row_newbcast:3 row_mask:0xf bank_mask:0xf\n"
"v_mul_f32_dpp v80, v206, v80 row_newbcast:0 row_mask:0xf bank_mask:0xf\n"
"v_mul_f32_dpp v85, v206, v85 row_newbcast:1 row_mask:0xf bank_mask:0xf\n"
"v_mul_f32_dpp v59, v250, v59 row_newbcast:3 row_mask:0xf bank_mask:0xf\n"
"v_mul_f32_dpp v61, v250, v61 row_newbcast:1 row_mask:0xf bank_mask:0xf\n"
"v_mov_b64 v[200:201], 0\n"
"v_mfma_f32_16x16x32_bf16 v[12:15], v[132:135], v[224:227], 0\n"
"s_add_u32 s40, s56, s52\n"
"ds_read_b128 v[208:211], v228 offset:20480\n"
"v_mul_f32_dpp v30, v250, v30 row_newbcast:2 row_mask:0xf bank_mask:0xf\n"
"v_mfma_f32_16x16x32_bf16 v[12:15], v[128:131], a[252:255], v[12:15]\n"
"buffer_load_dwordx4 v240, s[20:23], s40 offen offset:1024 nt lds\n"
"v_accvgpr_read_b32 v251, a211\n"
"v_mul_f32_dpp v27, v250, v27 row_newbcast:3 row_mask:0xf bank_mask:0xf\n"
"v_fmac_f32_dpp v24, v252, v254 row_newbcast:0 row_mask:0xf bank_mask:0xf\n"
"v_fmac_f32_dpp v25, v252, v255 row_newbcast:1 row_mask:0xf bank_mask:0xf\n"
"v_fmac_f32_dpp v26, v252, v253 row_newbcast:2 row_mask:0xf bank_mask:0xf\n"
"v_accvgpr_read_b32 v253, a214\n"
"v_cvt_pk_bf16_f32 v16, v16, v17\n"
"v_mfma_f32_16x16x32_bf16 a[220:223], v[124:127], a[252:255], 0\n"
"v_mul_f32_dpp v53, v247, v53 row_newbcast:1 row_mask:0xf bank_mask:0xf\n"
"v_mul_f32_dpp v57, v250, v57 row_newbcast:1 row_mask:0xf bank_mask:0xf\n"
"v_smfmac_f32_16x16x64_bf16 v[196:199], a[240:243], v[128:135], v241\n"
"v_fmac_f32_dpp v27, v252, v251 row_newbcast:3 row_mask:0xf bank_mask:0xf\n"
"v_accvgpr_read_b32 v254, a212\n"
"v_mul_f32_dpp v28, v250, v28 row_newbcast:0 row_mask:0xf bank_mask:0xf\n"
"v_accvgpr_read_b32 v255, a213\n"
"v_accvgpr_read_b32 v251, a215\n"
"v_mul_f32_dpp v31, v250, v31 row_newbcast:3 row_mask:0xf bank_mask:0xf\n"
"v_cvt_pk_bf16_f32 v24, v24, v25\n"
"v_mul_f32_dpp v54, v247, v54 row_newbcast:2 row_mask:0xf bank_mask:0xf\n"
"v_mul_f32_dpp v58, v250, v58 row_newbcast:2 row_mask:0xf bank_mask:0xf\n"
"v_mul_f32_dpp v60, v250, v60 row_newbcast:0 row_mask:0xf bank_mask:0xf\n"
"v_mfma_f32_16x16x32_bf16 v[202:205], a[240:243], v[124:127], v[202:205]\n"
"s_waitcnt lgkmcnt(1)\n"
"ds_read_b64_tr_b16 a[244:245], v234 offset:1024\n"
"v_mul_f32_dpp v29, v250, v29 row_newbcast:1 row_mask:0xf bank_mask:0xf\n"
"v_fmac_f32_dpp v28, v252, v254 row_newbcast:0 row_mask:0xf bank_mask:0xf\n"
"v_fmac_f32_dpp v30, v252, v253 row_newbcast:2 row_mask:0xf bank_mask:0xf\n"
"v_mul_f32_dpp v56, v250, v56 row_newbcast:0 row_mask:0xf bank_mask:0xf\n"
"v_mul_f32_dpp v63, v250, v63 row_newbcast:3 row_mask:0xf bank_mask:0xf\n"
"v_exp_f32 v157, v157\n"
"v_exp_f32 v160, v160\n"
"v_mfma_f32_16x16x32_bf16 v[168:171], v[216:219], a[8:11], v[180:183]\n"
"v_fmac_f32_dpp v31, v252, v251 row_newbcast:3 row_mask:0xf bank_mask:0xf\n"
"v_mul_f32_dpp v83, v206, v83 row_newbcast:3 row_mask:0xf bank_mask:0xf\n"
"v_mfma_f32_16x16x32_bf16 v[168:171], v[220:223], a[12:15], v[168:171]\n"
"s_waitcnt lgkmcnt(1)\n"
"ds_read_b64_tr_b16 a[250:251], v238 offset:5120\n"
"v_accvgpr_read_b32 v251, a227\n"
"v_mfma_f32_16x16x32_bf16 v[168:171], v[208:211], a[0:3], v[168:171]\n"
"v_fmac_f32_dpp v29, v252, v255 row_newbcast:1 row_mask:0xf bank_mask:0xf\n"
"v_accvgpr_read_b32 v255, a225\n"
"v_mfma_f32_16x16x32_bf16 v[168:171], v[212:215], a[4:7], v[168:171]\n"
"ds_read_b64_tr_b16 v[226:227], v239 offset:5120\n"
"v_accvgpr_read_b32 v254, a224\n"
"v_fmac_f32_dpp v57, v252, v255 row_newbcast:1 row_mask:0xf bank_mask:0xf\n"
"v_fmac_f32_dpp v59, v252, v251 row_newbcast:3 row_mask:0xf bank_mask:0xf\n"
"v_cvt_pk_bf16_f32 v96, v152, v153\n"
"v_exp_f32 v161, v161\n"
"v_exp_f32 v163, v163\n"
"v_mfma_f32_16x16x32_bf16 v[172:175], v[208:211], a[16:19], v[184:187]\n"
"v_accvgpr_read_b32 v253, a226\n"
"v_accvgpr_read_b32 v255, a229\n"
"v_mfma_f32_16x16x32_bf16 v[172:175], v[212:215], a[20:23], v[172:175]\n"
"ds_read_b64_tr_b16 v[224:225], v239 offset:1024\n"
"v_fmac_f32_dpp v58, v252, v253 row_newbcast:2 row_mask:0xf bank_mask:0xf\n"
"v_mfma_f32_16x16x32_bf16 v[172:175], v[216:219], a[24:27], v[172:175]\n"
"s_add_u32 s40, s56, s53\n"
"v_fmac_f32_dpp v56, v252, v254 row_newbcast:0 row_mask:0xf bank_mask:0xf\n"
"v_accvgpr_read_b32 v251, a231\n"
"v_mfma_f32_16x16x32_bf16 v[172:175], v[220:223], a[28:31], v[172:175]\n"
"buffer_load_dwordx4 v240, s[20:23], s40 offen offset:2048 nt lds\n"
"v_cvt_pk_bf16_f32 v25, v26, v27\n"
"v_cvt_pk_bf16_f32 v26, v28, v29\n"
"v_cvt_pk_bf16_f32 v27, v30, v31\n"
"v_mul_f32_dpp v82, v206, v82 row_newbcast:2 row_mask:0xf bank_mask:0xf\n"
"v_mul_f32_dpp v62, v250, v62 row_newbcast:2 row_mask:0xf bank_mask:0xf\n"
"v_cvt_pk_bf16_f32 v56, v56, v57\n"
"v_exp_f32 v170, v170\n"
"v_mfma_f32_16x16x32_bf16 v[176:179], v[216:219], a[40:43], v[188:191]\n"
"v_accvgpr_read_b32 v253, a230\n"
"v_cvt_pk_bf16_f32 v113, v162, v163\n"
"v_mfma_f32_16x16x32_bf16 v[176:179], v[220:223], a[44:47], v[176:179]\n"
"v_accvgpr_read_b32 v254, a228\n"
"v_cvt_pk_bf16_f32 v57, v58, v59\n"
"v_mfma_f32_16x16x32_bf16 v[176:179], v[208:211], a[32:35], v[176:179]\n"
"ds_read_b64_tr_b16 a[254:255], v235 offset:5120\n"
"v_fmac_f32_dpp v63, v252, v251 row_newbcast:3 row_mask:0xf bank_mask:0xf\n"
"v_mfma_f32_16x16x32_bf16 v[176:179], v[212:215], a[36:39], v[176:179]\n"
"ds_write_b128 v243, v[24:27] offset:0\n"
"v_cvt_pk_bf16_f32 v80, v80, v81\n"
"v_fmac_f32_dpp v60, v252, v254 row_newbcast:0 row_mask:0xf bank_mask:0xf\n"
"v_fmac_f32_dpp v61, v252, v255 row_newbcast:1 row_mask:0xf bank_mask:0xf\n"
"v_fmac_f32_dpp v62, v252, v253 row_newbcast:2 row_mask:0xf bank_mask:0xf\n"
"v_cvt_pk_bf16_f32 v59, v62, v63\n"
"v_mfma_f32_16x16x32_bf16 v[152:155], v[208:211], a[192:195], v[192:195]\n"
"v_cvt_pk_bf16_f32 v48, v48, v49\n"
"v_cvt_pk_bf16_f32 v58, v60, v61\n"
"v_mfma_f32_16x16x32_bf16 v[152:155], v[212:215], a[196:199], v[152:155]\n"
"ds_write_b128 v243, v[56:59] offset:2048\n"
"v_exp_f32 v167, v167\n"
"v_exp_f32 v171, v171\n"
"v_mfma_f32_16x16x32_bf16 v[152:155], v[216:219], a[200:203], v[152:155]\n"
"v_mul_f32_dpp v86, v206, v86 row_newbcast:2 row_mask:0xf bank_mask:0xf\n"
"v_cvt_pk_bf16_f32 v112, v160, v161\n"
"v_mfma_f32_16x16x32_bf16 v[152:155], v[220:223], a[204:207], v[152:155]\n"
"s_waitcnt lgkmcnt(3)\n"
"ds_read_b64_tr_b16 a[252:253], v235 offset:1024\n"
"v_mul_f32_dpp v55, v247, v55 row_newbcast:3 row_mask:0xf bank_mask:0xf\n"
"v_mfma_f32_16x16x32_bf16 a[232:235], v[124:127], a[244:247], 0\n"
"ds_read_b128 v[208:211], v230 offset:16384\n"
"v_cvt_pk_bf16_f32 v81, v82, v83\n"
"v_mfma_f32_16x16x32_bf16 v[24:27], v[128:131], a[244:247], 0\n"
"s_add_u32 s40, s56, s54\n"
"v_exp_f32 v166, v166\n"
"v_mfma_f32_16x16x32_bf16 v[24:27], v[132:135], a[248:251], v[24:27]\n"
"buffer_load_dwordx4 v240, s[20:23], s40 offen offset:3072 nt lds\n"
"v_mfma_f32_16x16x32_bf16 v[56:59], v[136:139], a[244:247], 0\n"
"s_add_u32 s55, s55, s59\n"
"v_mul_f32_dpp v51, v247, v51 row_newbcast:3 row_mask:0xf bank_mask:0xf\n"
"v_cvt_pk_bf16_f32 v105, v158, v159\n"
"v_mfma_f32_16x16x32_bf16 v[56:59], v[140:143], a[248:251], v[56:59]\n"
"s_add_u32 s41, s55, s57\n"
"v_exp_f32 v168, v168\n"
"v_mfma_f32_16x16x32_bf16 v[88:91], v[144:147], a[244:247], 0\n"
"s_and_b32 s41, s41, s58\n"
"v_exp_f32 v172, v172\n"
"v_mfma_f32_16x16x32_bf16 v[88:91], v[148:151], a[248:251], v[88:91]\n"
"s_andn2_b32 s56, s55, s58\n"
"ds_read_b128 v[212:215], v231 offset:16384\n"
"v_cvt_pk_bf16_f32 v104, v156, v157\n"
"v_mfma_f32_16x16x32_bf16 v[92:95], v[148:151], v[224:227], 0\n"
"s_waitcnt lgkmcnt(2)\n"
"s_or_b32 s56, s56, s41\n"
"v_exp_f32 v169, v169\n"
"v_mfma_f32_16x16x32_bf16 v[92:95], v[144:147], a[252:255], v[92:95]\n"
"ds_read_b128 v[216:219], v230 offset:17408\n"
"v_cvt_pk_bf16_f32 v49, v50, v51\n"
"v_mfma_f32_16x16x32_bf16 v[60:63], v[140:143], v[224:227], 0\n"
"s_add_u32 m0, s44, 49152\n"
"ds_read_b128 v[220:223], v231 offset:17408\n"
"v_mul_f32_dpp v21, v246, v21 row_newbcast:1 row_mask:0xf bank_mask:0xf\n"
"v_mfma_f32_16x16x32_bf16 v[60:63], v[136:139], a[252:255], v[60:63]\n"
"buffer_load_dwordx4 v240, s[16:19], s56 offen nt lds\n"
"v_mfma_f32_16x16x32_bf16 v[28:31], v[132:135], v[224:227], 0\n"
"v_xor_b32 v248, s75, v248\n"
"v_cvt_pk_bf16_f32 v121, v166, v167\n"
"v_mfma_f32_16x16x32_bf16 v[28:31], v[128:131], a[252:255], v[28:31]\n"
"v_cvt_pk_bf16_f32 v17, v18, v19\n"
"v_cvt_pk_bf16_f32 v120, v164, v165\n"
"v_mfma_f32_16x16x32_bf16 a[236:239], v[124:127], a[252:255], 0\n"
"ds_read_b64_tr_b16 a[248:249], v236 offset:0\n"
"v_exp_f32 v173, v173\n"
"v_exp_f32 v174, v174\n"
"v_exp_f32 v178, v178\n"
"v_smfmac_f32_16x16x64_bf16 v[198:201], a[240:243], v[136:143], v241\n"
"s_waitcnt lgkmcnt(3)\n"
"ds_read_b64_tr_b16 v[224:225], v237 offset:0\n"
"v_mul_f32_dpp v23, v246, v23 row_newbcast:3 row_mask:0xf bank_mask:0xf\n"
"v_mfma_f32_16x16x32_bf16 v[156:159], v[208:211], a[0:3], v[180:183]\n"
"ds_read_b64_tr_b16 a[244:245], v232 offset:0\n"
"ds_read_b64_tr_b16 v[226:227], v237 offset:4096\n"
"v_mfma_f32_16x16x32_bf16 v[156:159], v[212:215], a[4:7], v[156:159]\n"
"s_waitcnt lgkmcnt(5)\n"
"v_cvt_pk_bf16_f32 v18, v20, v21\n"
"v_cvt_pk_bf16_f32 v106, v172, v173\n"
"v_mfma_f32_16x16x32_bf16 v[156:159], v[216:219], a[8:11], v[156:159]\n"
"s_waitcnt lgkmcnt(4)\n"
"ds_read_b64_tr_b16 a[254:255], v233 offset:4096\n"
"v_cvt_pk_bf16_f32 v98, v168, v169\n"
"v_mfma_f32_16x16x32_bf16 v[156:159], v[220:223], a[12:15], v[156:159]\n"
"ds_read_b64_tr_b16 a[246:247], v232 offset:4096\n"
"v_mul_f32_dpp v52, v247, v52 row_newbcast:0 row_mask:0xf bank_mask:0xf\n"
"v_mfma_f32_16x16x32_bf16 v[160:163], v[216:219], a[24:27], v[184:187]\n"
"v_exp_f32 v155, v155\n"
"v_mfma_f32_16x16x32_bf16 v[160:163], v[220:223], a[28:31], v[160:163]\n"
"ds_read_b64_tr_b16 a[250:251], v236 offset:4096\n"
"v_cvt_pk_bf16_f32 v19, v22, v23\n"
"v_mfma_f32_16x16x32_bf16 v[160:163], v[208:211], a[16:19], v[160:163]\n"
"s_add_u32 s40, s56, s52\n"
"v_xor_b32 v249, s75, v249\n"
"v_mul_f32_dpp v87, v206, v87 row_newbcast:3 row_mask:0xf bank_mask:0xf\n"
"v_mfma_f32_16x16x32_bf16 v[160:163], v[212:215], a[20:23], v[160:163]\n"
"buffer_load_dwordx4 v240, s[16:19], s40 offen offset:1024 nt lds\n"
"v_mfma_f32_16x16x32_bf16 v[164:167], v[208:211], a[32:35], v[188:191]\n"
"v_exp_f32 v158, v158\n"
"v_mfma_f32_16x16x32_bf16 v[164:167], v[212:215], a[36:39], v[164:167]\n"
"v_cvt_pk_bf16_f32 v51, v54, v55\n"
"v_cvt_pk_bf16_f32 v99, v170, v171\n"
"v_mfma_f32_16x16x32_bf16 v[164:167], v[216:219], a[40:43], v[164:167]\n"
"ds_read_b64_tr_b16 a[252:253], v233 offset:0\n"
"v_cvt_pk_bf16_f32 v50, v52, v53\n"
"v_mfma_f32_16x16x32_bf16 v[164:167], v[220:223], a[44:47], v[164:167]\n"
"s_waitcnt lgkmcnt(2)\n"
"ds_write_b128 v248, v[16:19] offset:49152\n"
"v_exp_f32 v177, v177\n"
"v_exp_f32 v153, v153\n"
"v_mfma_f32_16x16x32_bf16 a[208:211], v[124:127], a[244:247], 0\n"
"ds_write_b128 v248, v[48:51] offset:50176\n"
"v_exp_f32 v154, v154\n"
"v_mfma_f32_16x16x32_bf16 v[0:3], v[128:131], a[244:247], 0\n"
"s_waitcnt lgkmcnt(3)\n"
"v_exp_f32 v179, v179\n"
"v_mfma_f32_16x16x32_bf16 v[0:3], v[132:135], a[248:251], v[0:3]\n"
"ds_read_b128 v[208:211], v230 offset:20480\n"
"v_cvt_pk_bf16_f32 v83, v86, v87\n"
"v_mfma_f32_16x16x32_bf16 v[32:35], v[136:139], a[244:247], 0\n"
"s_add_u32 s40, s56, s53\n"
"ds_read_b128 v[216:219], v230 offset:21504\n"
"v_cvt_pk_bf16_f32 v82, v84, v85\n"
"v_mfma_f32_16x16x32_bf16 v[32:35], v[140:143], a[248:251], v[32:35]\n"
"buffer_load_dwordx4 v240, s[16:19], s40 offen offset:2048 nt lds\n"
"v_mfma_f32_16x16x32_bf16 v[64:67], v[144:147], a[244:247], 0\n"
"v_exp_f32 v166, v166\n"
"v_mfma_f32_16x16x32_bf16 v[64:67], v[148:151], a[248:251], v[64:67]\n"
"v_exp_f32 v163, v163\n"
"v_mfma_f32_16x16x32_bf16 v[68:71], v[148:151], v[224:227], 0\n"
"s_waitcnt lgkmcnt(3)\n"
"ds_read_b64_tr_b16 a[58:59], v249 offset:49160\n"
"ds_read_b128 v[212:215], v231 offset:20480\n"
"v_mfma_f32_16x16x32_bf16 v[68:71], v[144:147], a[252:255], v[68:71]\n"
"v_exp_f32 v152, v152\n"
"v_mfma_f32_16x16x32_bf16 v[36:39], v[140:143], v[224:227], 0\n"
"v_exp_f32 v162, v162\n"
"v_mfma_f32_16x16x32_bf16 v[36:39], v[136:139], a[252:255], v[36:39]\n"
"ds_read_b128 v[220:223], v231 offset:21504\n"
"v_exp_f32 v175, v175\n"
"v_exp_f32 v176, v176\n"
"v_exp_f32 v164, v164\n"
"v_exp_f32 v165, v165\n"
"v_mfma_f32_16x16x32_bf16 v[4:7], v[132:135], v[224:227], 0\n"
"ds_read_b64_tr_b16 a[56:57], v249 offset:49152\n"
"ds_read_b64_tr_b16 a[248:249], v236 offset:1024\n"
"v_mfma_f32_16x16x32_bf16 v[4:7], v[128:131], a[252:255], v[4:7]\n"
"ds_read_b64_tr_b16 a[74:75], v249 offset:50184\n"
"v_cvt_pk_bf16_f32 v107, v174, v175\n"
"v_mfma_f32_16x16x32_bf16 a[212:215], v[124:127], a[252:255], 0\n"
"s_add_u32 s40, s56, s54\n"
"ds_write_b128 v248, v[80:83] offset:51200\n"
"v_exp_f32 v161, v161\n"
"v_smfmac_f32_16x16x64_bf16 v[200:203], a[240:243], v[144:151], v241\n"
"s_waitcnt lgkmcnt(7)\n"
"buffer_load_dwordx4 v240, s[16:19], s40 offen offset:3072 nt lds\n"
"v_mfma_f32_16x16x32_bf16 v[168:171], v[208:211], a[0:3], v[180:183]\n"
"s_waitcnt lgkmcnt(5)\n"
"v_cvt_pk_bf16_f32 v114, v176, v177\n"
"v_add_u32 v243, 0x1000, v243\n"
"v_mfma_f32_16x16x32_bf16 v[168:171], v[212:215], a[4:7], v[168:171]\n"
"ds_read_b64_tr_b16 a[88:89], v249 offset:51200\n"
"ds_read_b64_tr_b16 a[244:245], v232 offset:1024\n"
"v_mfma_f32_16x16x32_bf16 v[168:171], v[216:219], a[8:11], v[168:171]\n"
"s_waitcnt lgkmcnt(6)\n"
"ds_read_b64_tr_b16 a[72:73], v249 offset:50176\n"
"ds_read_b64_tr_b16 a[90:91], v249 offset:51208\n"
"v_mfma_f32_16x16x32_bf16 v[168:171], v[220:223], a[12:15], v[168:171]\n"
"v_exp_f32 v167, v167\n"
"v_mfma_f32_16x16x32_bf16 v[172:175], v[216:219], a[24:27], v[184:187]\n"
"ds_read_b64_tr_b16 a[246:247], v232 offset:5120\n"
"v_cvt_pk_bf16_f32 v117, v166, v167\n"
"v_mfma_f32_16x16x32_bf16 v[172:175], v[220:223], a[28:31], v[172:175]\n"
"ds_read_b64_tr_b16 a[250:251], v236 offset:5120\n"
"v_cvt_pk_bf16_f32 v115, v178, v179\n"
"v_mfma_f32_16x16x32_bf16 v[172:175], v[208:211], a[16:19], v[172:175]\n"
"ds_read_b64_tr_b16 a[254:255], v233 offset:5120\n"
"ds_read_b64_tr_b16 v[224:225], v237 offset:1024\n"
"v_mfma_f32_16x16x32_bf16 v[172:175], v[212:215], a[20:23], v[172:175]\n"
"s_cbranch_vccnz LPCT1_%=\nLPKT1_%=:\n"
"v_exp_f32 v160, v160\n"
"v_mfma_f32_16x16x32_bf16 v[176:179], v[208:211], a[32:35], v[188:191]\n"
"ds_read_b64_tr_b16 a[252:253], v233 offset:1024\n"
"v_cvt_pk_bf16_f32 v116, v164, v165\n"
"v_mfma_f32_16x16x32_bf16 v[176:179], v[212:215], a[36:39], v[176:179]\n"
"v_exp_f32 v159, v159\n"
"v_mfma_f32_16x16x32_bf16 v[176:179], v[216:219], a[40:43], v[176:179]\n"
"v_exp_f32 v157, v157\n"
"v_mfma_f32_16x16x32_bf16 v[176:179], v[220:223], a[44:47], v[176:179]\n"
"s_waitcnt lgkmcnt(4)\n"
"v_cvt_pk_bf16_f32 v101, v158, v159\n"
"v_mfma_f32_16x16x32_bf16 a[224:227], v[124:127], a[244:247], 0\n"
"ds_read_b64_tr_b16 v[226:227], v237 offset:5120\n"
"v_cvt_pk_bf16_f32 v122, v152, v153\n"
"v_mfma_f32_16x16x32_bf16 v[16:19], v[128:131], a[244:247], 0\n"
"s_waitcnt vmcnt(8) lgkmcnt(4)\n"
"v_exp_f32 v156, v156\n"
"v_mfma_f32_16x16x32_bf16 v[16:19], v[132:135], a[248:251], v[16:19]\n"
"s_barrier\n"
"ds_read_b128 v[208:211], v228 offset:32768\n"
"v_exp_f32 v175, v175\n"
"v_mfma_f32_16x16x32_bf16 v[48:51], v[136:139], a[244:247], 0\n"
"ds_read_b128 v[220:223], v229 offset:33792\n"
"v_cvt_pk_bf16_f32 v100, v156, v157\n"
"v_mfma_f32_16x16x32_bf16 v[48:51], v[140:143], a[248:251], v[48:51]\n"
"ds_read_b128 v[212:215], v229 offset:32768\n"
"ds_read_b128 v[216:219], v228 offset:33792\n"
"v_mfma_f32_16x16x32_bf16 v[80:83], v[144:147], a[244:247], 0\n"
"ds_read_b64_tr_b16 a[244:245], v234 offset:16384\n"
"ds_read_b64_tr_b16 a[246:247], v234 offset:20480\n"
"v_mfma_f32_16x16x32_bf16 v[80:83], v[148:151], a[248:251], v[80:83]\n"
"s_waitcnt lgkmcnt(6)\n"
"ds_read_b64_tr_b16 a[248:249], v238 offset:16384\n"
"ds_read_b64_tr_b16 a[250:251], v238 offset:20480\n"
"v_mfma_f32_16x16x32_bf16 v[84:87], v[148:151], v[224:227], 0\n"
"v_xor_b32 v248, s75, v248\n"
"v_cvt_pk_bf16_f32 v108, v160, v161\n"
"v_mfma_f32_16x16x32_bf16 v[84:87], v[144:147], a[252:255], v[84:87]\n"
"v_exp_f32 v173, v173\n"
"v_mfma_f32_16x16x32_bf16 v[52:55], v[140:143], v[224:227], 0\n"
"v_exp_f32 v172, v172\n"
"v_mfma_f32_16x16x32_bf16 v[52:55], v[136:139], a[252:255], v[52:55]\n"
"v_cvt_pk_bf16_f32 v123, v154, v155\n"
"v_cvt_pk_bf16_f32 v109, v162, v163\n"
"v_mfma_f32_16x16x32_bf16 v[20:23], v[132:135], v[224:227], 0\n"
"ds_read_b64_tr_b16 v[224:225], v239 offset:16384\n"
"v_xor_b32 v249, s75, v249\n"
"v_mfma_f32_16x16x32_bf16 v[20:23], v[128:131], a[252:255], v[20:23]\n"
"v_exp_f32 v169, v169\n"
"v_mfma_f32_16x16x32_bf16 a[228:231], v[124:127], a[252:255], 0\n"
"s_waitcnt lgkmcnt(3)\n"
"ds_read_b64_tr_b16 a[254:255], v235 offset:20480\n"
"s_branch LAY2_%=\n"
"LAX_%=:\n"
"v_mfma_f32_16x16x32_bf16 a[224:227], v[124:127], a[244:247], a[224:227]\n"
"v_exp_f32 v171, v171\n"
"v_mfma_f32_16x16x32_bf16 v[16:19], v[128:131], a[244:247], v[16:19]\n"
"s_waitcnt vmcnt(8)\n"
"v_exp_f32 v170, v170\n"
"v_mfma_f32_16x16x32_bf16 v[16:19], v[132:135], a[248:251], v[16:19]\n"
"s_barrier\n"
"v_mfma_f32_16x16x32_bf16 v[48:51], v[136:139], a[244:247], v[48:51]\n"
"v_cvt_pk_bf16_f32 v117, v166, v167\n"
"v_cvt_pk_bf16_f32 v103, v170, v171\n"
"v_mfma_f32_16x16x32_bf16 v[48:51], v[140:143], a[248:251], v[48:51]\n"
"v_cvt_pk_bf16_f32 v116, v164, v165\n"
"v_accvgpr_read_b32 v253, a96\n"
"v_mfma_f32_16x16x32_bf16 v[80:83], v[144:147], a[244:247], v[80:83]\n"
"ds_read_b64_tr_b16 a[244:245], v234 offset:49152\n"
"v_and_b32 v253, 0x7fff0000, v253\n"
"v_mfma_f32_16x16x32_bf16 v[80:83], v[148:151], a[248:251], v[80:83]\n"
"ds_read_b64_tr_b16 a[246:247], v234 offset:53248\n"
"v_exp_f32 v168, v168\n"
"v_exp_f32 v169, v169\n"
"v_exp_f32 v174, v174\n"
"v_exp_f32 v175, v175\n"
"v_mfma_f32_16x16x32_bf16 v[84:87], v[148:151], v[224:227], v[84:87]\n"
"ds_read_b64_tr_b16 a[248:249], v238 offset:49152\n"
"ds_read_b64_tr_b16 a[250:251], v238 offset:53248\n"
"v_mfma_f32_16x16x32_bf16 v[84:87], v[144:147], a[252:255], v[84:87]\n"
"v_exp_f32 v177, v177\n"
"v_mfma_f32_16x16x32_bf16 v[52:55], v[140:143], v[224:227], v[52:55]\n"
"v_cvt_pk_bf16_f32 v100, v156, v157\n"
"v_max_u32_dpp v253, v253, v253 quad_perm:[1,0,3,2] row_mask:0xf bank_mask:0xf\n"
"v_mfma_f32_16x16x32_bf16 v[52:55], v[136:139], a[252:255], v[52:55]\n"
"v_cvt_pk_bf16_f32 v108, v160, v161\n"
"v_cvt_pk_bf16_f32 v102, v168, v169\n"
"v_exp_f32 v172, v172\n"
"v_exp_f32 v176, v176\n"
"v_exp_f32 v178, v178\n"
"v_mfma_f32_16x16x32_bf16 v[20:23], v[132:135], v[224:227], v[20:23]\n"
"v_exp_f32 v173, v173\n"
"v_mfma_f32_16x16x32_bf16 v[20:23], v[128:131], a[252:255], v[20:23]\n"
"v_exp_f32 v179, v179\n"
"v_mfma_f32_16x16x32_bf16 a[228:231], v[124:127], a[252:255], a[228:231]\n"
"s_waitcnt lgkmcnt(2)\n"
"ds_read_b64_tr_b16 v[226:227], v239 offset:53248\n"
"v_cvt_pk_bf16_f32 v110, v172, v173\n"
"v_mfma_f32_16x16x32_bf16 a[216:219], v[120:123], a[244:247], a[216:219]\n"
"s_mov_b64 s[28:29], s[8:9]\n"
"v_cvt_pk_bf16_f32 v109, v162, v163\n"
"v_cvt_pk_bf16_f32 v111, v174, v175\n"
"v_smfmac_f32_16x16x64_bf16 v[196:199], a[240:243], v[96:103], v241\n"
"s_mov_b32 s30, 655360\n"
"ds_read_b64_tr_b16 a[254:255], v235 offset:53248\n"
"ds_read_b64_tr_b16 v[224:225], v239 offset:49152\n"
"v_mfma_f32_16x16x32_bf16 v[8:11], v[96:99], a[244:247], v[8:11]\n"
"s_waitcnt lgkmcnt(3)\n"
"s_mov_b32 s71, s44\n"
"v_cvt_pk_bf16_f32 v118, v176, v177\n"
"v_cvt_pk_bf16_f32 v119, v178, v179\n"
"v_mfma_f32_16x16x32_bf16 v[8:11], v[100:103], a[248:251], v[8:11]\n"
"s_mov_b32 s70, s95\n"
"v_max_u32_dpp v253, v253, v253 quad_perm:[2,3,0,1] row_mask:0xf bank_mask:0xf\n"
"v_and_b32 v245, 0x3f0, v242\n"
"v_smfmac_f32_16x16x64_bf16 v[200:203], a[240:243], v[112:119], v241\n"
"ds_read_b64_tr_b16 a[252:253], v235 offset:49152\n"
"v_and_b32 v252, 0x7fff0000, v252\n"
"v_mfma_f32_16x16x32_bf16 v[40:43], v[104:107], a[244:247], v[40:43]\n"
"s_nop 0\n"
"v_max_u32_dpp v252, v252, v252 quad_perm:[1,0,3,2] row_mask:0xf bank_mask:0xf\n"
"v_lshrrev_b32 v245, 1, v245\n"
"v_mfma_f32_16x16x32_bf16 v[40:43], v[108:111], a[248:251], v[40:43]\n"
"s_add_u32 s41, s70, 327680\n"
"s_add_u32 m0, s71, 32768\n"
"v_lshlrev_b32_e64 v244, 24, s43\n"
"v_smfmac_f32_16x16x64_bf16 v[198:201], a[240:243], v[104:111], v241\n"
"buffer_load_dwordx4 v240, s[28:31], s41 offen lds\n"
"v_mfma_f32_16x16x32_bf16 v[72:75], v[112:115], a[244:247], v[72:75]\n"
"v_max_u32_dpp v253, v253, v253 row_half_mirror row_mask:0xf bank_mask:0xf\n"
"v_add_f32 v196, v196, v197\n"
"v_mfma_f32_16x16x32_bf16 v[72:75], v[116:119], a[248:251], v[72:75]\n"
"v_max_u32_dpp v252, v252, v252 quad_perm:[2,3,0,1] row_mask:0xf bank_mask:0xf\n"
"v_sub_u32 v245, v242, v245\n"
"v_mfma_f32_16x16x32_bf16 v[202:205], a[240:243], v[120:123], v[202:205]\n"
"s_waitcnt lgkmcnt(1)\n"
"ds_read_b64_tr_b16 a[246:247], v234 offset:54272\n"
"ds_read_b64_tr_b16 a[248:249], v238 offset:50176\n"
"ds_read_b64_tr_b16 a[250:251], v238 offset:54272\n"
"v_add_f32 v198, v198, v199\n"
"v_add_f32 v200, v200, v201\n"
"v_mul_f32 v244, 0x45800000, v244\n"
"v_mfma_f32_16x16x32_bf16 v[76:79], v[116:119], v[224:227], v[76:79]\n"
"s_waitcnt lgkmcnt(3)\n"
"s_add_u32 s40, s41, s52\n"
"ds_read_b64_tr_b16 a[244:245], v234 offset:50176\n"
"v_xor_b32 v250, 0x1000, v245\n"
"v_mfma_f32_16x16x32_bf16 v[76:79], v[112:115], a[252:255], v[76:79]\n"
"buffer_load_dwordx4 v240, s[28:31], s40 offen offset:1024 lds\n"
"v_mfma_f32_16x16x32_bf16 v[44:47], v[108:111], v[224:227], v[44:47]\n"
"v_max_u32_dpp v252, v252, v252 row_half_mirror row_mask:0xf bank_mask:0xf\n"
"v_add_f32 v244, v244, v202\n"
"v_mfma_f32_16x16x32_bf16 v[44:47], v[104:107], a[252:255], v[44:47]\n"
"v_min3_f32 v244, v244, v196, v198\n"
"v_min_f32 v244, v244, v200\n"
"v_mfma_f32_16x16x32_bf16 v[12:15], v[100:103], v[224:227], v[12:15]\n"
"s_add_u32 s40, s41, s53\n"
"ds_read_b64_tr_b16 v[224:225], v239 offset:50176\n"
"v_add_f32 v254, v196, v198\n"
"v_mfma_f32_16x16x32_bf16 v[12:15], v[96:99], a[252:255], v[12:15]\n"
"buffer_load_dwordx4 v240, s[28:31], s40 offen offset:2048 lds\n"
"v_add_f32 v254, v254, v200\n"
"v_rcp_f32 v244, v244\n"
"v_mfma_f32_16x16x32_bf16 a[220:223], v[120:123], a[252:255], a[220:223]\n"
"s_waitcnt lgkmcnt(1)\n"
"v_add_f32 v254, v254, v202\n"
"v_fmac_f32 v254, 0x68800000, v244\n"
"v_cmp_nge_f32 vcc, s45, v254\n"
"s_cbranch_vccnz LSLOW_%=\nLBACK3_%=:\ns_mov_b64 vcc, s[64:65]\n"
"v_mfma_f32_16x16x32_bf16 a[232:235], v[120:123], a[244:247], a[232:235]\n"
"ds_read_b64_tr_b16 a[252:253], v235 offset:50176\n"
"ds_read_b64_tr_b16 v[226:227], v239 offset:54272\n"
"v_rcp_f32 v247, v198\n"
"v_rcp_f32 v206, v200\n"
"v_xor_b32 v244, 0x11000, v242\n"
"v_mfma_f32_16x16x32_bf16 v[24:27], v[96:99], a[244:247], v[24:27]\n"
"ds_read_b64_tr_b16 a[254:255], v235 offset:54272\n"
"v_mov_b32_dpp v247, v247 row_ror:12 row_mask:0x2 bank_mask:0xf\n"
"v_mfma_f32_16x16x32_bf16 v[24:27], v[100:103], a[248:251], v[24:27]\n"
"ds_write_b128 v244, a[216:219] offset:32768\n"
"v_min_u32_dpp v253, v253, v253 row_mirror row_mask:0xf bank_mask:0xf\n"
"v_rcp_f32 v246, v196\n"
"v_mov_b32_dpp v206, v206 row_ror:12 row_mask:0x2 bank_mask:0xf\n"
"v_mov_b32_dpp v247, v247 row_ror:8 row_mask:0x4 bank_mask:0xf\n"
"v_mfma_f32_16x16x32_bf16 v[56:59], v[104:107], a[244:247], v[56:59]\n"
"s_add_u32 s40, s41, s54\n"
"v_mov_b32_dpp v246, v246 row_ror:12 row_mask:0x2 bank_mask:0xf\n"
"v_mov_b32_dpp v206, v206 row_ror:8 row_mask:0x4 bank_mask:0xf\n"
"v_mfma_f32_16x16x32_bf16 v[56:59], v[108:111], a[248:251], v[56:59]\n"
"buffer_load_dwordx4 v240, s[28:31], s40 offen offset:3072 lds\n"
"v_min_u32_dpp v253, v253, v253 row_bcast:15 row_mask:0xa bank_mask:0xf\n"
"v_mov_b32_dpp v247, v247 row_ror:4 row_mask:0x8 bank_mask:0xf\n"
"v_mov_b32_dpp v206, v206 row_ror:4 row_mask:0x8 bank_mask:0xf\n"
"v_bfe_u32 v254, v242, 9, 1\n"
"v_mfma_f32_16x16x32_bf16 v[88:91], v[112:115], a[244:247], v[88:91]\n"
"s_add_u32 s71, s44, s75\n"
"v_mov_b32_dpp v246, v246 row_ror:8 row_mask:0x4 bank_mask:0xf\n"
"v_mul_f32_dpp v79, v206, v79 row_newbcast:3 row_mask:0xf bank_mask:0xf\n"
"v_mfma_f32_16x16x32_bf16 v[88:91], v[116:119], a[248:251], v[88:91]\n"
"s_waitcnt lgkmcnt(2)\n"
"s_mov_b32 s70, s100\n"
"ds_read_b64_tr_b16 a[246:247], v232 offset:53248\n"
"ds_read_b64_tr_b16 a[248:249], v236 offset:49152\n"
"ds_read_b64_tr_b16 a[250:251], v236 offset:53248\n"
"v_min_u32_dpp v253, v253, v253 row_bcast:31 row_mask:0xc bank_mask:0xf\n"
"v_lshl_add_u32 v254, v254, 4, s43\n"
"v_mul_f32_dpp v47, v247, v47 row_newbcast:3 row_mask:0xf bank_mask:0xf\n"
"v_mfma_f32_16x16x32_bf16 v[92:95], v[116:119], v[224:227], v[92:95]\n"
"s_waitcnt lgkmcnt(4)\n"
"s_add_u32 s41, s70, 163840\n"
"s_add_u32 m0, s71, 16384\n"
"ds_read_b64_tr_b16 a[244:245], v232 offset:49152\n"
"v_mfma_f32_16x16x32_bf16 v[92:95], v[112:115], a[252:255], v[92:95]\n"
"buffer_load_dwordx4 v240, s[28:31], s41 offen lds\n"
"v_mfma_f32_16x16x32_bf16 v[60:63], v[108:111], v[224:227], v[60:63]\n"
"v_mov_b32_dpp v246, v246 row_ror:4 row_mask:0x8 bank_mask:0xf\n"
"v_mul_f32_dpp v40, v247, v40 row_newbcast:0 row_mask:0xf bank_mask:0xf\n"
"v_mfma_f32_16x16x32_bf16 v[60:63], v[104:107], a[252:255], v[60:63]\n"
"ds_write_b128 v244, a[220:223] offset:33792\n"
"v_min_u32_dpp v252, v252, v252 row_mirror row_mask:0xf bank_mask:0xf\n"
"v_mul_f32_dpp v43, v247, v43 row_newbcast:3 row_mask:0xf bank_mask:0xf\n"
"v_mul_f32_dpp v73, v206, v73 row_newbcast:1 row_mask:0xf bank_mask:0xf\n"
"v_mul_f32_dpp v27, v246, v27 row_newbcast:3 row_mask:0xf bank_mask:0xf\n"
"v_mul_f32_dpp v56, v247, v56 row_newbcast:0 row_mask:0xf bank_mask:0xf\n"
"v_mfma_f32_16x16x32_bf16 v[28:31], v[100:103], v[224:227], v[28:31]\n"
"s_add_u32 s40, s41, s52\n"
"v_min_u32_dpp v252, v252, v252 row_bcast:15 row_mask:0xa bank_mask:0xf\n"
"v_mul_f32_dpp v24, v246, v24 row_newbcast:0 row_mask:0xf bank_mask:0xf\n"
"v_mfma_f32_16x16x32_bf16 v[28:31], v[96:99], a[252:255], v[28:31]\n"
"buffer_load_dwordx4 v240, s[28:31], s40 offen offset:1024 lds\n"
"v_min_u32_dpp v252, v252, v252 row_bcast:31 row_mask:0xc bank_mask:0xf\n"
"v_mul_f32 v252, 0x43340000, v252\n"
"v_mul_f32_dpp v8, v246, v8 row_newbcast:0 row_mask:0xf bank_mask:0xf\n"
"v_mul_f32_dpp v9, v246, v9 row_newbcast:1 row_mask:0xf bank_mask:0xf\n"
"v_mul_f32_dpp v74, v206, v74 row_newbcast:2 row_mask:0xf bank_mask:0xf\n"
"v_mul_f32_dpp v57, v247, v57 row_newbcast:1 row_mask:0xf bank_mask:0xf\n"
"v_cvt_pk_bf16_f32 v56, v56, v57\n"
"v_mul_f32_dpp v88, v206, v88 row_newbcast:0 row_mask:0xf bank_mask:0xf\n"
"v_mfma_f32_16x16x32_bf16 a[236:239], v[120:123], a[252:255], a[236:239]\n"
"s_waitcnt lgkmcnt(1)\n"
"v_mul_f32_dpp v14, v246, v14 row_newbcast:2 row_mask:0xf bank_mask:0xf\n"
"v_mul_f32_dpp v25, v246, v25 row_newbcast:1 row_mask:0xf bank_mask:0xf\n"
"v_mfma_f32_16x16x32_bf16 a[208:211], v[120:123], a[244:247], a[208:211]\n"
"v_lshl_add_u32 v254, v254, 6, s75\n"
"v_mul_f32_dpp v44, v247, v44 row_newbcast:0 row_mask:0xf bank_mask:0xf\n"
"v_mfma_f32_16x16x32_bf16 v[0:3], v[96:99], a[244:247], v[0:3]\n"
"ds_read_b64_tr_b16 a[254:255], v233 offset:53248\n"
"v_mul_f32_dpp v31, v246, v31 row_newbcast:3 row_mask:0xf bank_mask:0xf\n"
"v_mfma_f32_16x16x32_bf16 v[0:3], v[100:103], a[248:251], v[0:3]\n"
"ds_write_b128 v244, a[236:239] offset:35840\n"
"v_mul_f32 v252, v252, v253\n"
"v_sub_u32 v229, v233, v254\n"
"v_mul_f32_dpp v12, v246, v12 row_newbcast:0 row_mask:0xf bank_mask:0xf\n"
"v_mul_f32_dpp v42, v247, v42 row_newbcast:2 row_mask:0xf bank_mask:0xf\n"
"v_mul_f32_dpp v75, v206, v75 row_newbcast:3 row_mask:0xf bank_mask:0xf\n"
"v_mfma_f32_16x16x32_bf16 v[32:35], v[104:107], a[244:247], v[32:35]\n"
"s_add_u32 s40, s41, s53\n"
"ds_read_b64_tr_b16 a[252:253], v233 offset:49152\n"
"ds_read_b64_tr_b16 v[224:225], v237 offset:49152\n"
"ds_read_b64_tr_b16 v[226:227], v237 offset:53248\n"
"v_cvt_pk_bf16_f32 v8, v8, v9\n"
"v_mul_f32_dpp v41, v247, v41 row_newbcast:1 row_mask:0xf bank_mask:0xf\n"
"v_mul_f32_dpp v45, v247, v45 row_newbcast:1 row_mask:0xf bank_mask:0xf\n"
"v_cvt_pk_bf16_f32 v40, v40, v41\n"
"v_cvt_pk_bf16_f32 v41, v42, v43\n"
"v_mul_f32_dpp v95, v206, v95 row_newbcast:3 row_mask:0xf bank_mask:0xf\n"
"v_mfma_f32_16x16x32_bf16 v[32:35], v[108:111], a[248:251], v[32:35]\n"
"buffer_load_dwordx4 v240, s[28:31], s40 offen offset:2048 lds\n"
"v_min_f32 v252, 0x71800000, v252\n"
"s_nop 0\n"
"v_readlane_b32 s22, v252, 63\n"
"v_readlane_b32 s23, v252, 63\n"
"v_mul_f32_dpp v15, v246, v15 row_newbcast:3 row_mask:0xf bank_mask:0xf\n"
"v_mul_f32_dpp v46, v247, v46 row_newbcast:2 row_mask:0xf bank_mask:0xf\n"
"v_mul_f32_dpp v26, v246, v26 row_newbcast:2 row_mask:0xf bank_mask:0xf\n"
"v_mfma_f32_16x16x32_bf16 v[64:67], v[112:115], a[244:247], v[64:67]\n"
"v_mul_f32_dpp v77, v206, v77 row_newbcast:1 row_mask:0xf bank_mask:0xf\n"
"v_mul_f32_dpp v58, v247, v58 row_newbcast:2 row_mask:0xf bank_mask:0xf\n"
"v_mfma_f32_16x16x32_bf16 v[64:67], v[116:119], a[248:251], v[64:67]\n"
"s_waitcnt lgkmcnt(0)\n"
"ds_read_b64_tr_b16 a[246:247], v232 offset:54272\n"
"ds_read_b64_tr_b16 a[248:249], v236 offset:50176\n"
"ds_read_b64_tr_b16 a[250:251], v236 offset:54272\n"
"v_mul_f32_dpp v11, v246, v11 row_newbcast:3 row_mask:0xf bank_mask:0xf\n"
"v_mul_f32_dpp v13, v246, v13 row_newbcast:1 row_mask:0xf bank_mask:0xf\n"
"v_cvt_pk_bf16_f32 v43, v46, v47\n"
"v_mul_f32_dpp v72, v206, v72 row_newbcast:0 row_mask:0xf bank_mask:0xf\n"
"v_mul_f32_dpp v78, v206, v78 row_newbcast:2 row_mask:0xf bank_mask:0xf\n"
"v_cvt_pk_bf16_f32 v72, v72, v73\n"
"v_cvt_pk_bf16_f32 v73, v74, v75\n"
"v_mfma_f32_16x16x32_bf16 v[68:71], v[116:119], v[224:227], v[68:71]\n"
"s_add_u32 s40, s41, s54\n"
"ds_read_b64_tr_b16 a[244:245], v232 offset:50176\n"
"v_mul_f32_dpp v63, v247, v63 row_newbcast:3 row_mask:0xf bank_mask:0xf\n"
"v_mfma_f32_16x16x32_bf16 v[68:71], v[112:115], a[252:255], v[68:71]\n"
"buffer_load_dwordx4 v240, s[28:31], s40 offen offset:3072 lds\n"
"v_mov_b32 v252, v192\n"
"v_mov_b32 v253, v202\n"
"v_sub_u32 v228, v232, v254\n"
"v_mul_f32_dpp v76, v206, v76 row_newbcast:0 row_mask:0xf bank_mask:0xf\n"
"v_mul_f32_dpp v30, v246, v30 row_newbcast:2 row_mask:0xf bank_mask:0xf\n"
"v_cvt_pk_bf16_f32 v24, v24, v25\n"
"v_mul_f32_dpp v62, v247, v62 row_newbcast:2 row_mask:0xf bank_mask:0xf\n"
"v_mul_f32_dpp v32, v247, v32 row_newbcast:0 row_mask:0xf bank_mask:0xf\n"
"v_mfma_f32_16x16x32_bf16 v[36:39], v[108:111], v[224:227], v[36:39]\n"
"v_mul_f32_dpp v92, v206, v92 row_newbcast:0 row_mask:0xf bank_mask:0xf\n"
"v_mul_f32_dpp v2, v246, v2 row_newbcast:2 row_mask:0xf bank_mask:0xf\n"
"v_mfma_f32_16x16x32_bf16 v[36:39], v[104:107], a[252:255], v[36:39]\n"
"ds_write_b128 v244, a[232:235] offset:34816\n"
"v_cvt_pk_bf16_f32 v42, v44, v45\n"
"v_cvt_pk_bf16_f32 v74, v76, v77\n"
"v_mul_f32_dpp v59, v247, v59 row_newbcast:3 row_mask:0xf bank_mask:0xf\n"
"v_mul_f32_dpp v61, v247, v61 row_newbcast:1 row_mask:0xf bank_mask:0xf\n"
"v_mov_b64 v[196:197], s[22:23]\n"
"v_mfma_f32_16x16x32_bf16 v[4:7], v[100:103], v[224:227], v[4:7]\n"
"ds_read_b64_tr_b16 v[224:225], v237 offset:50176\n"
"ds_read_b64_tr_b16 v[226:227], v237 offset:54272\n"
"v_mfma_f32_16x16x32_bf16 v[4:7], v[96:99], a[252:255], v[4:7]\n"
"ds_write_b64 v250, v[252:253] offset:52224\n"
"v_mul_f32_dpp v10, v246, v10 row_newbcast:2 row_mask:0xf bank_mask:0xf\n"
"v_mul_f32_dpp v28, v246, v28 row_newbcast:0 row_mask:0xf bank_mask:0xf\n"
"v_mul_f32_dpp v29, v246, v29 row_newbcast:1 row_mask:0xf bank_mask:0xf\n"
"v_cvt_pk_bf16_f32 v25, v26, v27\n"
"v_mul_f32_dpp v60, v247, v60 row_newbcast:0 row_mask:0xf bank_mask:0xf\n"
"v_mul_f32_dpp v34, v247, v34 row_newbcast:2 row_mask:0xf bank_mask:0xf\n"
"v_mfma_f32_16x16x32_bf16 a[212:215], v[120:123], a[252:255], a[212:215]\n"
"s_waitcnt lgkmcnt(4)\n"
"ds_read_b64_tr_b16 a[252:253], v233 offset:50176\n"
"ds_read_b64_tr_b16 a[254:255], v233 offset:54272\n"
"v_cvt_pk_bf16_f32 v9, v10, v11\n"
"v_cvt_pk_bf16_f32 v10, v12, v13\n"
"v_cvt_pk_bf16_f32 v11, v14, v15\n"
"v_cvt_pk_bf16_f32 v57, v58, v59\n"
"v_mul_f32_dpp v93, v206, v93 row_newbcast:1 row_mask:0xf bank_mask:0xf\n"
"v_mul_f32_dpp v3, v246, v3 row_newbcast:3 row_mask:0xf bank_mask:0xf\n"
"v_mul_f32_dpp v65, v206, v65 row_newbcast:1 row_mask:0xf bank_mask:0xf\n"
"v_xor_b32 v232, 0x1000, v228\n"
"v_mfma_f32_16x16x32_bf16 a[224:227], v[120:123], a[244:247], a[224:227]\n"
"ds_write_b128 v248, v[40:43] offset:50176\n"
"v_cvt_pk_bf16_f32 v75, v78, v79\n"
"v_mul_f32_dpp v90, v206, v90 row_newbcast:2 row_mask:0xf bank_mask:0xf\n"
"v_mul_f32_dpp v36, v247, v36 row_newbcast:0 row_mask:0xf bank_mask:0xf\n"
"v_add_u32 v236, s76, v249\n"
"v_mfma_f32_16x16x32_bf16 v[16:19], v[96:99], a[244:247], v[16:19]\n"
"s_waitcnt vmcnt(8) lgkmcnt(1)\n"
"ds_write_b128 v248, v[8:11] offset:49152\n"
"v_cvt_pk_bf16_f32 v27, v30, v31\n"
"v_mul_f32_dpp v89, v206, v89 row_newbcast:1 row_mask:0xf bank_mask:0xf\n"
"v_mul_f32_dpp v4, v246, v4 row_newbcast:0 row_mask:0xf bank_mask:0xf\n"
"v_mul_f32_dpp v33, v247, v33 row_newbcast:1 row_mask:0xf bank_mask:0xf\n"
"v_mul_f32_dpp v37, v247, v37 row_newbcast:1 row_mask:0xf bank_mask:0xf\n"
"v_mfma_f32_16x16x32_bf16 v[16:19], v[100:103], a[248:251], v[16:19]\n"
"s_barrier\n"
"ds_write_b128 v248, v[72:75] offset:51200\n"
"v_mul_f32_dpp v91, v206, v91 row_newbcast:3 row_mask:0xf bank_mask:0xf\n"
"v_mul_f32_dpp v70, v206, v70 row_newbcast:2 row_mask:0xf bank_mask:0xf\n"
"v_mov_b64 v[198:199], s[22:23]\n"
"v_mfma_f32_16x16x32_bf16 v[48:51], v[104:107], a[244:247], v[48:51]\n"
"ds_read_b64_tr_b16 a[20:21], v249 offset:50176\n"
"ds_read_b64_tr_b16 v[188:189], v228 offset:8192\n"
"ds_read_b64_tr_b16 v[192:193], v232 offset:8192\n"
"ds_read_b64_tr_b16 v[210:211], v228 offset:18432\n"
"v_cvt_pk_bf16_f32 v88, v88, v89\n"
"v_mul_f32_dpp v5, v246, v5 row_newbcast:1 row_mask:0xf bank_mask:0xf\n"
"v_mul_f32_dpp v35, v247, v35 row_newbcast:3 row_mask:0xf bank_mask:0xf\n"
"v_cvt_pk_bf16_f32 v32, v32, v33\n"
"v_mul_f32_dpp v66, v206, v66 row_newbcast:2 row_mask:0xf bank_mask:0xf\n"
"v_mul_f32_dpp v67, v206, v67 row_newbcast:3 row_mask:0xf bank_mask:0xf\n"
"v_mfma_f32_16x16x32_bf16 v[48:51], v[108:111], a[248:251], v[48:51]\n"
"ds_read_b64 v[250:251], v245 offset:52224\n"
"ds_read_b64_tr_b16 v[182:183], v228 offset:2048\n"
"ds_read_b64_tr_b16 v[186:187], v232 offset:2048\n"
"ds_read_b64_tr_b16 v[214:215], v232 offset:18432\n"
"v_cvt_pk_bf16_f32 v89, v90, v91\n"
"v_mul_f32_dpp v38, v247, v38 row_newbcast:2 row_mask:0xf bank_mask:0xf\n"
"v_cvt_pk_bf16_f32 v33, v34, v35\n"
"v_mul_f32_dpp v68, v206, v68 row_newbcast:0 row_mask:0xf bank_mask:0xf\n"
"v_mul_f32_dpp v71, v206, v71 row_newbcast:3 row_mask:0xf bank_mask:0xf\n"
"v_xor_b32 v237, 0x400, v236\n"
"v_mfma_f32_16x16x32_bf16 v[80:83], v[112:115], a[244:247], v[80:83]\n"
"ds_read_b64_tr_b16 a[22:23], v249 offset:50184\n"
"ds_read_b64_tr_b16 v[216:217], v228 offset:24576\n"
"v_mfma_f32_16x16x32_bf16 v[80:83], v[116:119], a[248:251], v[80:83]\n"
"ds_read_b64_tr_b16 a[6:7], v249 offset:49160\n"
"ds_read_b64_tr_b16 v[180:181], v228 offset:0\n"
"ds_read_b64_tr_b16 v[190:191], v228 offset:10240\n"
"ds_read_b64_tr_b16 a[194:195], v236 offset:8\n"
"ds_read_b64_tr_b16 a[240:241], v236 offset:4096\n"
"v_sub_u32 v230, v234, v254\n"
"v_cvt_pk_bf16_f32 v58, v60, v61\n"
"v_cvt_pk_bf16_f32 v59, v62, v63\n"
"v_mul_f32_dpp v94, v206, v94 row_newbcast:2 row_mask:0xf bank_mask:0xf\n"
"v_mul_f32_dpp v69, v206, v69 row_newbcast:1 row_mask:0xf bank_mask:0xf\n"
"v_mfma_f32_16x16x32_bf16 v[84:87], v[116:119], v[224:227], v[84:87]\n"
"ds_write_b128 v248, v[56:59] offset:50176\n"
"v_sub_u32 v231, v235, v254\n"
"v_cvt_pk_bf16_f32 v26, v28, v29\n"
"v_mul_f32_dpp v1, v246, v1 row_newbcast:1 row_mask:0xf bank_mask:0xf\n"
"v_mul_f32_dpp v64, v206, v64 row_newbcast:0 row_mask:0xf bank_mask:0xf\n"
"v_xor_b32 v244, 0x1000, v244\n"
"v_mfma_f32_16x16x32_bf16 v[84:87], v[112:115], a[252:255], v[84:87]\n"
"ds_read_b64_tr_b16 a[38:39], v249 offset:51208\n"
"ds_read_b64_tr_b16 v[208:209], v228 offset:16384\n"
"ds_read_b64_tr_b16 v[212:213], v232 offset:16384\n"
"ds_read_b64_tr_b16 a[196:197], v237 offset:0\n"
"v_mul_f32_dpp v6, v246, v6 row_newbcast:2 row_mask:0xf bank_mask:0xf\n"
"v_mul_f32_dpp v7, v246, v7 row_newbcast:3 row_mask:0xf bank_mask:0xf\n"
"v_mfma_f32_16x16x32_bf16 v[52:55], v[108:111], v[224:227], v[52:55]\n"
"ds_read_b64_tr_b16 a[36:37], v249 offset:51200\n"
"ds_read_b64_tr_b16 v[184:185], v232 offset:0\n"
"v_mfma_f32_16x16x32_bf16 v[52:55], v[104:107], a[252:255], v[52:55]\n"
"ds_read_b64_tr_b16 a[4:5], v249 offset:49152\n"
"ds_read_b64_tr_b16 v[220:221], v232 offset:24576\n"
"ds_read_b64_tr_b16 a[192:193], v236 offset:0\n"
"ds_read_b64_tr_b16 v[114:115], v229 offset:18432\n"
"ds_read_b64_tr_b16 a[248:249], v236 offset:6144\n"
"v_cvt_pk_bf16_f32 v91, v94, v95\n"
"v_mul_f32_dpp v39, v247, v39 row_newbcast:3 row_mask:0xf bank_mask:0xf\n"
"v_cvt_pk_bf16_f32 v64, v64, v65\n"
"v_xor_b32 v233, 0x1000, v229\n"
"v_mfma_f32_16x16x32_bf16 v[20:23], v[100:103], v[224:227], v[20:23]\n"
"ds_read_b64_tr_b16 v[194:195], v232 offset:10240\n"
"v_cvt_pk_bf16_f32 v65, v66, v67\n"
"v_mfma_f32_16x16x32_bf16 v[20:23], v[96:99], a[252:255], v[20:23]\n"
"s_waitcnt lgkmcnt(15)\n"
"ds_read_b64_tr_b16 a[30:31], v249 offset:50184\n"
"ds_read_b128 v[56:59], v244 offset:34816\n"
"ds_read_b64_tr_b16 v[218:219], v228 offset:26624\n"
"ds_read_b64_tr_b16 v[222:223], v232 offset:26624\n"
"v_mul_f32_dpp v0, v246, v0 row_newbcast:0 row_mask:0xf bank_mask:0xf\n"
"v_cvt_pk_bf16_f32 v0, v0, v1\n"
"v_cvt_pk_bf16_f32 v1, v2, v3\n"
"v_cvt_pk_bf16_f32 v66, v68, v69\n"
"v_min_f32 v254, v252, v250\n"
"v_sub_f32 v252, v254, v252\n"
"v_mfma_f32_16x16x32_bf16 a[228:231], v[120:123], a[252:255], a[228:231]\n"
"ds_write_b128 v248, v[24:27] offset:49152\n"
"v_cvt_pk_bf16_f32 v90, v92, v93\n"
"v_cvt_pk_bf16_f32 v2, v4, v5\n"
"v_cvt_pk_bf16_f32 v3, v6, v7\n"
"v_mul_f32_dpp v52, v247, v52 row_newbcast:0 row_mask:0xf bank_mask:0xf\n"
"v_mfma_f32_16x16x32_bf16 v[124:127], v[180:183], a[96:99], v[196:199]\n"
"s_waitcnt lgkmcnt(11)\n"
"ds_write_b128 v248, v[88:91] offset:51200\n"
"v_cvt_pk_bf16_f32 v34, v36, v37\n"
"v_cvt_pk_bf16_f32 v35, v38, v39\n"
"v_mul_f32_dpp v18, v246, v18 row_newbcast:2 row_mask:0xf bank_mask:0xf\n"
"v_mul_f32_dpp v48, v247, v48 row_newbcast:0 row_mask:0xf bank_mask:0xf\n"
"v_mul_f32_dpp v50, v247, v50 row_newbcast:2 row_mask:0xf bank_mask:0xf\n"
"v_mfma_f32_16x16x32_bf16 v[124:127], v[184:187], a[100:103], v[124:127]\n"
"ds_read_b64_tr_b16 a[28:29], v249 offset:50176\n"
"ds_read_b128 v[24:27], v244 offset:32768\n"
"v_mfma_f32_16x16x32_bf16 v[124:127], v[188:191], a[104:107], v[124:127]\n"
"s_waitcnt lgkmcnt(8)\n"
"ds_read_b64_tr_b16 a[12:13], v249 offset:49152\n"
"ds_read_b64_tr_b16 a[46:47], v249 offset:51208\n"
"v_mfma_f32_16x16x32_bf16 v[124:127], v[192:195], a[108:111], v[124:127]\n"
"ds_read_b64_tr_b16 a[14:15], v249 offset:49160\n"
"ds_read_b64_tr_b16 a[44:45], v249 offset:51200\n"
"v_mfma_f32_16x16x32_bf16 v[124:127], v[208:211], a[144:147], v[124:127]\n"
"ds_write_b128 v248, v[0:3] offset:49152\n"
"v_cvt_pk_bf16_f32 v67, v70, v71\n"
"v_exp_f32 v252, v252\n"
"v_mul_f32_dpp v20, v246, v20 row_newbcast:0 row_mask:0xf bank_mask:0xf\n"
"v_mfma_f32_16x16x32_bf16 v[124:127], v[212:215], a[148:151], v[124:127]\n"
"s_waitcnt lgkmcnt(10)\n"
"ds_write_b128 v248, v[32:35] offset:50176\n"
"ds_write_b128 v248, v[64:67] offset:51200\n"
"v_mfma_f32_16x16x32_bf16 v[124:127], v[216:219], a[152:155], v[124:127]\n"
"s_waitcnt lgkmcnt(11)\n"
"ds_read_b128 v[60:63], v244 offset:35840\n"
"v_sub_f32 v250, v254, v250\n"
"v_mfma_f32_16x16x32_bf16 v[124:127], v[220:223], a[156:159], v[124:127]\n"
"ds_read_b128 v[28:31], v244 offset:33792\n"
"ds_read_b64_tr_b16 v[110:111], v233 offset:10240\n"
"v_mfma_f32_16x16x32_bf16 v[140:143], v[220:223], a[172:175], v[196:199]\n"
"ds_read_b64_tr_b16 a[0:1], v249 offset:49152\n"
"ds_read_b64_tr_b16 v[100:101], v233 offset:0\n"
"v_mfma_f32_16x16x32_bf16 v[140:143], v[216:219], a[168:171], v[140:143]\n"
"ds_read_b64_tr_b16 a[16:17], v249 offset:50176\n"
"ds_read_b64_tr_b16 a[18:19], v249 offset:50184\n"
"v_mfma_f32_16x16x32_bf16 v[140:143], v[212:215], a[164:167], v[140:143]\n"
"ds_read_b64_tr_b16 a[2:3], v249 offset:49160\n"
"ds_read_b64_tr_b16 a[32:33], v249 offset:51200\n"
"v_mfma_f32_16x16x32_bf16 v[140:143], v[208:211], a[160:163], v[140:143]\n"
"ds_read_b64_tr_b16 a[34:35], v249 offset:51208\n"
"ds_read_b64_tr_b16 a[254:255], v237 offset:6152\n"
"v_mfma_f32_16x16x32_bf16 v[140:143], v[192:195], a[124:127], v[140:143]\n"
"ds_read_b64_tr_b16 a[200:201], v236 offset:2048\n"
"ds_read_b64_tr_b16 v[122:123], v229 offset:26624\n"
"v_mfma_f32_16x16x32_bf16 v[140:143], v[188:191], a[120:123], v[140:143]\n"
"ds_read_b64_tr_b16 a[202:203], v236 offset:2056\n"
"ds_read_b64_tr_b16 a[246:247], v237 offset:4104\n"
"v_mfma_f32_16x16x32_bf16 v[140:143], v[184:187], a[116:119], v[140:143]\n"
"ds_read_b64_tr_b16 v[116:117], v233 offset:16384\n"
"ds_read_b64_tr_b16 v[224:225], v233 offset:24576\n"
"v_mfma_f32_16x16x32_bf16 v[140:143], v[180:183], a[112:115], v[140:143]\n"
"ds_read_b64_tr_b16 a[242:243], v236 offset:4104\n"
"ds_read_b64_tr_b16 a[252:253], v237 offset:6144\n"
"v_mfma_f32_16x16x32_bf16 v[156:159], v[180:183], a[128:131], v[196:199]\n"
"ds_read_b64_tr_b16 a[204:205], v237 offset:2048\n"
"ds_read_b64_tr_b16 a[250:251], v236 offset:6152\n"
"v_mfma_f32_16x16x32_bf16 v[156:159], v[184:187], a[132:135], v[156:159]\n"
"ds_read_b64_tr_b16 a[206:207], v237 offset:2056\n"
"ds_read_b64_tr_b16 a[244:245], v237 offset:4096\n"
"v_mfma_f32_16x16x32_bf16 v[156:159], v[188:191], a[136:139], v[156:159]\n"
"ds_read_b64_tr_b16 a[198:199], v237 offset:8\n"
"ds_read_b64_tr_b16 a[236:237], v237 offset:10240\n"
"v_mfma_f32_16x16x32_bf16 v[156:159], v[192:195], a[140:143], v[156:159]\n"
"s_waitcnt lgkmcnt(14)\n"
"ds_read_b64_tr_b16 v[118:119], v233 offset:18432\n"
"ds_read_b64_tr_b16 v[120:121], v229 offset:24576\n"
"v_mfma_f32_16x16x32_bf16 v[156:159], v[208:211], a[176:179], v[156:159]\n"
"ds_read_b64_tr_b16 v[98:99], v229 offset:2048\n"
"ds_read_b64_tr_b16 v[112:113], v229 offset:16384\n"
"v_mfma_f32_16x16x32_bf16 v[156:159], v[212:215], a[180:183], v[156:159]\n"
"s_add_u32 s41, s95, 491520\n"
"ds_read_b64_tr_b16 v[96:97], v229 offset:0\n"
"ds_read_b64_tr_b16 v[102:103], v233 offset:2048\n"
"v_mfma_f32_16x16x32_bf16 v[156:159], v[216:219], a[184:187], v[156:159]\n"
"s_add_u32 m0, s44, 49152\n"
"ds_read_b64_tr_b16 v[104:105], v229 offset:8192\n"
"ds_read_b64_tr_b16 a[238:239], v237 offset:10248\n"
"v_mfma_f32_16x16x32_bf16 v[156:159], v[220:223], a[188:191], v[156:159]\n"
"s_waitcnt lgkmcnt(14)\n"
"buffer_load_dwordx4 v240, s[28:31], s41 offen lds\n"
"v_mfma_f32_16x16x32_bf16 v[172:175], v[220:223], a[252:255], v[196:199]\n"
"s_waitcnt lgkmcnt(12)\n"
"s_add_u32 s40, s41, s52\n"
"ds_read_b64_tr_b16 v[106:107], v229 offset:10240\n"
"ds_read_b64_tr_b16 v[108:109], v233 offset:8192\n"
"v_mfma_f32_16x16x32_bf16 v[172:175], v[216:219], a[248:251], v[172:175]\n"
"s_waitcnt lgkmcnt(12)\n"
"buffer_load_dwordx4 v240, s[28:31], s40 offen offset:1024 lds\n"
"v_mfma_f32_16x16x32_bf16 v[172:175], v[212:215], a[244:247], v[172:175]\n"
"s_add_u32 s40, s41, s53\n"
"v_exp_f32 v250, v250\n"
"v_mfma_f32_16x16x32_bf16 v[172:175], v[208:211], a[240:243], v[172:175]\n"
"buffer_load_dwordx4 v240, s[28:31], s40 offen offset:2048 lds\n"
"v_mfma_f32_16x16x32_bf16 v[172:175], v[192:195], a[204:207], v[172:175]\n"
"v_mul_f32 v253, v253, v252\n"
"v_mul_f32_dpp v55, v247, v55 row_newbcast:3 row_mask:0xf bank_mask:0xf\n"
"v_mfma_f32_16x16x32_bf16 v[172:175], v[188:191], a[200:203], v[172:175]\n"
"s_waitcnt lgkmcnt(4)\n"
"ds_read_b64_tr_b16 v[226:227], v233 offset:26624\n"
"v_fmac_f32 v253, v251, v250\n"
"v_mfma_f32_16x16x32_bf16 v[172:175], v[184:187], a[196:199], v[172:175]\n"
"s_add_u32 s40, s41, s54\n"
"v_xor_b32 v234, 0x1000, v230\n"
"v_mul_f32_dpp v54, v247, v54 row_newbcast:2 row_mask:0xf bank_mask:0xf\n"
"v_mfma_f32_16x16x32_bf16 v[172:175], v[180:183], a[192:195], v[172:175]\n"
"buffer_load_dwordx4 v240, s[28:31], s40 offen offset:3072 lds\n"
"v_mfma_f32_16x16x32_bf16 v[128:131], v[96:99], a[96:99], v[196:199]\n"
"v_accvgpr_read_b32 v255, a209\n"
"v_mul_f32_dpp v82, v206, v82 row_newbcast:2 row_mask:0xf bank_mask:0xf\n"
"v_mfma_f32_16x16x32_bf16 v[128:131], v[100:103], a[100:103], v[128:131]\n"
"s_waitcnt lgkmcnt(1)\n"
"ds_read_b64_tr_b16 v[188:189], v230 offset:8192\n"
"ds_read_b64_tr_b16 v[190:191], v230 offset:10240\n"
"v_mfma_f32_16x16x32_bf16 v[128:131], v[104:107], a[104:107], v[128:131]\n"
"ds_read_b64_tr_b16 v[220:221], v234 offset:24576\n"
"v_mul_f32_dpp v21, v246, v21 row_newbcast:1 row_mask:0xf bank_mask:0xf\n"
"v_mfma_f32_16x16x32_bf16 v[128:131], v[108:111], a[108:111], v[128:131]\n"
"ds_read_b64_tr_b16 a[218:219], v236 offset:8200\n"
"v_accvgpr_read_b32 v254, a208\n"
"v_mfma_f32_16x16x32_bf16 v[128:131], v[112:115], a[144:147], v[128:131]\n"
"v_rcp_f32 v253, v253\n"
"v_mfma_f32_16x16x32_bf16 v[128:131], v[116:119], a[148:151], v[128:131]\n"
"ds_read_b64_tr_b16 v[186:187], v234 offset:2048\n"
"v_mul_f32 v250, v250, v253\n"
"v_mfma_f32_16x16x32_bf16 v[128:131], v[120:123], a[152:155], v[128:131]\n"
"s_waitcnt lgkmcnt(5)\n"
"v_mul_f32_dpp v51, v247, v51 row_newbcast:3 row_mask:0xf bank_mask:0xf\n"
"v_mul_f32_dpp v80, v206, v80 row_newbcast:0 row_mask:0xf bank_mask:0xf\n"
"v_mfma_f32_16x16x32_bf16 v[128:131], v[224:227], a[156:159], v[128:131]\n"
"ds_read_b64_tr_b16 v[208:209], v230 offset:16384\n"
"v_mov_b32_dpp v250, v250 row_ror:12 row_mask:0x2 bank_mask:0xf\n"
"v_mfma_f32_16x16x32_bf16 v[144:147], v[224:227], a[172:175], v[196:199]\n"
"s_nop 0\n"
"v_mov_b32_dpp v250, v250 row_ror:8 row_mask:0x4 bank_mask:0xf\n"
"v_accvgpr_read_b32 v251, a211\n"
"v_mfma_f32_16x16x32_bf16 v[144:147], v[120:123], a[168:171], v[144:147]\n"
"v_mul_f32 v252, v252, v253\n"
"v_mov_b32_dpp v250, v250 row_ror:4 row_mask:0x8 bank_mask:0xf\n"
"v_mfma_f32_16x16x32_bf16 v[144:147], v[116:119], a[164:167], v[144:147]\n"
"v_xor_b32 v235, 0x1000, v231\n"
"v_mul_f32_dpp v81, v206, v81 row_newbcast:1 row_mask:0xf bank_mask:0xf\n"
"v_mfma_f32_16x16x32_bf16 v[144:147], v[112:115], a[160:163], v[144:147]\n"
"v_mov_b32_dpp v252, v252 row_ror:12 row_mask:0x2 bank_mask:0xf\n"
"v_mul_f32_dpp v25, v250, v25 row_newbcast:1 row_mask:0xf bank_mask:0xf\n"
"v_mfma_f32_16x16x32_bf16 v[144:147], v[108:111], a[124:127], v[144:147]\n"
"ds_read_b64_tr_b16 v[210:211], v230 offset:18432\n"
"v_mul_f32_dpp v24, v250, v24 row_newbcast:0 row_mask:0xf bank_mask:0xf\n"
"v_mfma_f32_16x16x32_bf16 v[144:147], v[104:107], a[120:123], v[144:147]\n"
"v_mov_b32_dpp v252, v252 row_ror:8 row_mask:0x4 bank_mask:0xf\n"
"v_mul_f32_dpp v62, v250, v62 row_newbcast:2 row_mask:0xf bank_mask:0xf\n"
"v_mfma_f32_16x16x32_bf16 v[144:147], v[100:103], a[116:119], v[144:147]\n"
"v_accvgpr_read_b32 v253, a210\n"
"v_mul_f32_dpp v49, v247, v49 row_newbcast:1 row_mask:0xf bank_mask:0xf\n"
"v_mfma_f32_16x16x32_bf16 v[144:147], v[96:99], a[112:115], v[144:147]\n"
"v_mov_b32_dpp v252, v252 row_ror:4 row_mask:0x8 bank_mask:0xf\n"
"v_mul_f32_dpp v26, v250, v26 row_newbcast:2 row_mask:0xf bank_mask:0xf\n"
"v_mfma_f32_16x16x32_bf16 v[160:163], v[96:99], a[128:131], v[196:199]\n"
"ds_read_b64_tr_b16 v[194:195], v234 offset:10240\n"
"v_fmac_f32_dpp v26, v252, v253 row_newbcast:2 row_mask:0xf bank_mask:0xf\n"
"v_mfma_f32_16x16x32_bf16 v[160:163], v[100:103], a[132:135], v[160:163]\n"
"ds_read_b64_tr_b16 v[192:193], v234 offset:8192\n"
"v_mul_f32_dpp v27, v250, v27 row_newbcast:3 row_mask:0xf bank_mask:0xf\n"
"v_mfma_f32_16x16x32_bf16 v[160:163], v[104:107], a[136:139], v[160:163]\n"
"ds_read_b64_tr_b16 v[212:213], v234 offset:16384\n"
"v_cvt_pk_bf16_f32 v48, v48, v49\n"
"v_mfma_f32_16x16x32_bf16 v[160:163], v[108:111], a[140:143], v[160:163]\n"
"ds_read_b64_tr_b16 v[182:183], v230 offset:2048\n"
"v_accvgpr_read_b32 v253, a214\n"
"v_mfma_f32_16x16x32_bf16 v[160:163], v[112:115], a[176:179], v[160:163]\n"
"ds_read_b64_tr_b16 v[216:217], v230 offset:24576\n"
"v_fmac_f32_dpp v24, v252, v254 row_newbcast:0 row_mask:0xf bank_mask:0xf\n"
"v_mfma_f32_16x16x32_bf16 v[160:163], v[116:119], a[180:183], v[160:163]\n"
"ds_read_b64_tr_b16 v[184:185], v234 offset:0\n"
"v_mul_f32_dpp v30, v250, v30 row_newbcast:2 row_mask:0xf bank_mask:0xf\n"
"v_mfma_f32_16x16x32_bf16 v[160:163], v[120:123], a[184:187], v[160:163]\n"
"ds_read_b64_tr_b16 a[234:235], v236 offset:10248\n"
"v_fmac_f32_dpp v25, v252, v255 row_newbcast:1 row_mask:0xf bank_mask:0xf\n"
"v_mfma_f32_16x16x32_bf16 v[160:163], v[224:227], a[188:191], v[160:163]\n"
"ds_read_b64_tr_b16 v[180:181], v230 offset:0\n"
"ds_read_b64_tr_b16 v[214:215], v234 offset:18432\n"
"v_fmac_f32_dpp v27, v252, v251 row_newbcast:3 row_mask:0xf bank_mask:0xf\n"
"v_accvgpr_read_b32 v254, a212\n"
"v_mul_f32_dpp v28, v250, v28 row_newbcast:0 row_mask:0xf bank_mask:0xf\n"
"v_mul_f32_dpp v31, v250, v31 row_newbcast:3 row_mask:0xf bank_mask:0xf\n"
"v_cvt_pk_bf16_f32 v24, v24, v25\n"
"v_mfma_f32_16x16x32_bf16 v[176:179], v[224:227], a[252:255], v[196:199]\n"
"v_fmac_f32_dpp v28, v252, v254 row_newbcast:0 row_mask:0xf bank_mask:0xf\n"
"v_accvgpr_read_b32 v254, a224\n"
"v_mfma_f32_16x16x32_bf16 v[176:179], v[120:123], a[248:251], v[176:179]\n"
"ds_read_b64_tr_b16 v[222:223], v234 offset:26624\n"
"v_mul_f32_dpp v57, v250, v57 row_newbcast:1 row_mask:0xf bank_mask:0xf\n"
"v_mfma_f32_16x16x32_bf16 v[176:179], v[116:119], a[244:247], v[176:179]\n"
"ds_read_b64_tr_b16 v[218:219], v230 offset:26624\n"
"v_mul_f32_dpp v56, v250, v56 row_newbcast:0 row_mask:0xf bank_mask:0xf\n"
"v_mfma_f32_16x16x32_bf16 v[176:179], v[112:115], a[240:243], v[176:179]\n"
"v_accvgpr_read_b32 v251, a215\n"
"v_mul_f32_dpp v17, v246, v17 row_newbcast:1 row_mask:0xf bank_mask:0xf\n"
"v_mfma_f32_16x16x32_bf16 v[176:179], v[108:111], a[204:207], v[176:179]\n"
"v_accvgpr_read_b32 v255, a213\n"
"v_cvt_pk_bf16_f32 v49, v50, v51\n"
"v_mfma_f32_16x16x32_bf16 v[176:179], v[104:107], a[200:203], v[176:179]\n"
"ds_read_b64_tr_b16 v[120:121], v231 offset:24576\n"
"v_mul_f32_dpp v29, v250, v29 row_newbcast:1 row_mask:0xf bank_mask:0xf\n"
"v_mfma_f32_16x16x32_bf16 v[176:179], v[100:103], a[196:199], v[176:179]\n"
"v_mul_f32_dpp v61, v250, v61 row_newbcast:1 row_mask:0xf bank_mask:0xf\n"
"v_mov_b64 v[2:3], s[22:23]\n"
"v_mfma_f32_16x16x32_bf16 v[176:179], v[96:99], a[192:195], v[176:179]\n"
"s_waitcnt lgkmcnt(3)\n"
"ds_read_b64_tr_b16 v[96:97], v231 offset:0\n"
"ds_read_b64_tr_b16 v[122:123], v231 offset:26624\n"
"v_fmac_f32_dpp v29, v252, v255 row_newbcast:1 row_mask:0xf bank_mask:0xf\n"
"v_fmac_f32_dpp v31, v252, v251 row_newbcast:3 row_mask:0xf bank_mask:0xf\n"
"v_mul_f32_dpp v16, v246, v16 row_newbcast:0 row_mask:0xf bank_mask:0xf\n"
"v_mul_f32_dpp v19, v246, v19 row_newbcast:3 row_mask:0xf bank_mask:0xf\n"
"v_accvgpr_read_b32 v255, a225\n"
"v_fmac_f32_dpp v56, v252, v254 row_newbcast:0 row_mask:0xf bank_mask:0xf\n"
"v_mfma_f32_16x16x32_bf16 v[132:135], v[180:183], a[96:99], v[196:199]\n"
"s_mov_b32 s47, 0\n"
"v_fmac_f32_dpp v30, v252, v253 row_newbcast:2 row_mask:0xf bank_mask:0xf\n"
"v_accvgpr_read_b32 v253, a226\n"
"v_mfma_f32_16x16x32_bf16 v[132:135], v[184:187], a[100:103], v[132:135]\n"
"s_mul_i32 s16, s61, 1280\n"
"ds_read_b64_tr_b16 v[108:109], v235 offset:8192\n"
"v_mul_f32_dpp v59, v250, v59 row_newbcast:3 row_mask:0xf bank_mask:0xf\n"
"v_mfma_f32_16x16x32_bf16 v[132:135], v[188:191], a[104:107], v[132:135]\n"
"s_add_u32 s16, s16, s95\n"
"ds_read_b64_tr_b16 v[110:111], v235 offset:10240\n"
"v_mul_f32_dpp v83, v206, v83 row_newbcast:3 row_mask:0xf bank_mask:0xf\n"
"v_mfma_f32_16x16x32_bf16 v[132:135], v[192:195], a[108:111], v[132:135]\n"
"s_mul_i32 s16, s16, 224\n"
"v_mul_f32_dpp v58, v250, v58 row_newbcast:2 row_mask:0xf bank_mask:0xf\n"
"v_mul_f32_dpp v63, v250, v63 row_newbcast:3 row_mask:0xf bank_mask:0xf\n"
"v_mfma_f32_16x16x32_bf16 v[132:135], v[208:211], a[144:147], v[132:135]\n"
"s_add_u32 s17, s16, 4096\n"
"ds_read_b64_tr_b16 v[226:227], v235 offset:26624\n"
"v_accvgpr_read_b32 v254, a228\n"
"v_mfma_f32_16x16x32_bf16 v[132:135], v[212:215], a[148:151], v[132:135]\n"
"s_waitcnt lgkmcnt(5)\n"
"s_mov_b32 s72, s61\n"
"ds_read_b64_tr_b16 v[106:107], v231 offset:10240\n"
"v_fmac_f32_dpp v58, v252, v253 row_newbcast:2 row_mask:0xf bank_mask:0xf\n"
"v_mfma_f32_16x16x32_bf16 v[132:135], v[216:219], a[152:155], v[132:135]\n"
"s_mov_b32 s21, s60\n"
"v_fmac_f32_dpp v57, v252, v255 row_newbcast:1 row_mask:0xf bank_mask:0xf\n"
"v_mul_f32_dpp v60, v250, v60 row_newbcast:0 row_mask:0xf bank_mask:0xf\n"
"v_mfma_f32_16x16x32_bf16 v[132:135], v[220:223], a[156:159], v[132:135]\n"
"s_mov_b32 s18, s77\n"
"ds_read_b64_tr_b16 v[104:105], v231 offset:8192\n"
"v_cvt_pk_bf16_f32 v25, v26, v27\n"
"v_cvt_pk_bf16_f32 v27, v30, v31\n"
"v_mul_f32_dpp v87, v206, v87 row_newbcast:3 row_mask:0xf bank_mask:0xf\n"
"v_accvgpr_read_b32 v251, a227\n"
"v_accvgpr_read_b32 v255, a229\n"
"v_accvgpr_read_b32 v253, a230\n"
"v_fmac_f32_dpp v60, v252, v254 row_newbcast:0 row_mask:0xf bank_mask:0xf\n"
"v_mfma_f32_16x16x32_bf16 v[148:151], v[220:223], a[172:175], v[196:199]\n"
"s_mov_b32 s19, s77\n"
"ds_read_b64_tr_b16 v[98:99], v231 offset:2048\n"
"v_xor_b32 v248, s75, v248\n"
"v_mfma_f32_16x16x32_bf16 v[148:151], v[216:219], a[168:171], v[148:151]\n"
"s_mov_b32 s69, -1\n"
"ds_read_b64_tr_b16 a[216:217], v236 offset:8192\n"
"ds_read_b64_tr_b16 v[116:117], v235 offset:16384\n"
"v_mfma_f32_16x16x32_bf16 v[148:151], v[212:215], a[164:167], v[148:151]\n"
"v_fmac_f32_dpp v59, v252, v251 row_newbcast:3 row_mask:0xf bank_mask:0xf\n"
"v_accvgpr_read_b32 v251, a231\n"
"v_mfma_f32_16x16x32_bf16 v[148:151], v[208:211], a[160:163], v[148:151]\n"
"ds_read_b64_tr_b16 v[100:101], v235 offset:0\n"
"ds_read_b64_tr_b16 v[224:225], v235 offset:24576\n"
"v_mfma_f32_16x16x32_bf16 v[148:151], v[192:195], a[124:127], v[148:151]\n"
"v_cvt_pk_bf16_f32 v56, v56, v57\n"
"v_cvt_pk_bf16_f32 v57, v58, v59\n"
"v_mfma_f32_16x16x32_bf16 v[148:151], v[188:191], a[120:123], v[148:151]\n"
"v_cvt_pk_bf16_f32 v26, v28, v29\n"
"v_cvt_pk_bf16_f32 v16, v16, v17\n"
"v_mfma_f32_16x16x32_bf16 v[148:151], v[184:187], a[116:119], v[148:151]\n"
"ds_read_b64_tr_b16 v[112:113], v231 offset:16384\n"
"v_fmac_f32_dpp v61, v252, v255 row_newbcast:1 row_mask:0xf bank_mask:0xf\n"
"v_mfma_f32_16x16x32_bf16 v[148:151], v[180:183], a[112:115], v[148:151]\n"
"ds_write_b128 v243, v[24:27] offset:0\n"
"v_fmac_f32_dpp v62, v252, v253 row_newbcast:2 row_mask:0xf bank_mask:0xf\n"
"v_fmac_f32_dpp v63, v252, v251 row_newbcast:3 row_mask:0xf bank_mask:0xf\n"
"v_cvt_pk_bf16_f32 v58, v60, v61\n"
"v_cvt_pk_bf16_f32 v59, v62, v63\n"
"v_mfma_f32_16x16x32_bf16 v[164:167], v[180:183], a[128:131], v[196:199]\n"
"ds_write_b128 v243, v[56:59] offset:2048\n"
"v_mul_f32_dpp v23, v246, v23 row_newbcast:3 row_mask:0xf bank_mask:0xf\n"
"v_mfma_f32_16x16x32_bf16 v[164:167], v[184:187], a[132:135], v[164:167]\n"
"v_cvt_pk_bf16_f32 v17, v18, v19\n"
"v_cvt_pk_bf16_f32 v51, v54, v55\n"
"v_mfma_f32_16x16x32_bf16 v[164:167], v[188:191], a[136:139], v[164:167]\n"
"ds_read_b64_tr_b16 v[102:103], v235 offset:2048\n"
"v_cvt_pk_bf16_f32 v18, v20, v21\n"
"v_mfma_f32_16x16x32_bf16 v[164:167], v[192:195], a[140:143], v[164:167]\n"
"ds_read_b64_tr_b16 v[114:115], v231 offset:18432\n"
"ds_read_b64_tr_b16 v[118:119], v235 offset:18432\n"
"v_mfma_f32_16x16x32_bf16 v[164:167], v[208:211], a[176:179], v[164:167]\n"
"v_mul_f32_dpp v53, v247, v53 row_newbcast:1 row_mask:0xf bank_mask:0xf\n"
"v_cvt_pk_bf16_f32 v50, v52, v53\n"
"v_mfma_f32_16x16x32_bf16 v[164:167], v[212:215], a[180:183], v[164:167]\n"
"ds_write_b128 v248, v[48:51] offset:50176\n"
"v_mul_f32_dpp v86, v206, v86 row_newbcast:2 row_mask:0xf bank_mask:0xf\n"
"v_cvt_pk_bf16_f32 v80, v80, v81\n"
"v_mfma_f32_16x16x32_bf16 v[164:167], v[216:219], a[184:187], v[164:167]\n"
"s_waitcnt vmcnt(0) lgkmcnt(4)\n"
"v_mul_f32_dpp v22, v246, v22 row_newbcast:2 row_mask:0xf bank_mask:0xf\n"
"v_cvt_pk_bf16_f32 v19, v22, v23\n"
"v_mfma_f32_16x16x32_bf16 v[164:167], v[220:223], a[188:191], v[164:167]\n"
"s_barrier\n"
"ds_write_b128 v248, v[16:19] offset:49152\n"
"v_cvt_pk_bf16_f32 v81, v82, v83\n"
"v_mfma_f32_16x16x32_bf16 v[136:139], v[96:99], a[96:99], v[196:199]\n"
"s_waitcnt lgkmcnt(4)\n"
"v_mul_f32_dpp v84, v206, v84 row_newbcast:0 row_mask:0xf bank_mask:0xf\n"
"v_cvt_pk_bf16_f32 v83, v86, v87\n"
"v_mfma_f32_16x16x32_bf16 v[136:139], v[100:103], a[100:103], v[136:139]\n"
"ds_read_b64_tr_b16 v[190:191], v228 offset:43008\n"
"v_mul_f32_dpp v85, v206, v85 row_newbcast:1 row_mask:0xf bank_mask:0xf\n"
"v_mfma_f32_16x16x32_bf16 v[136:139], v[104:107], a[104:107], v[136:139]\n"
"ds_read_b64_tr_b16 v[218:219], v228 offset:59392\n"
"v_cvt_pk_bf16_f32 v82, v84, v85\n"
"v_mfma_f32_16x16x32_bf16 v[136:139], v[108:111], a[108:111], v[136:139]\n"
"s_waitcnt lgkmcnt(4)\n"
"ds_write_b128 v248, v[80:83] offset:51200\n"
"v_mfma_f32_16x16x32_bf16 v[136:139], v[112:115], a[144:147], v[136:139]\n"
"v_xor_b32 v249, s75, v249\n"
"v_bfe_u32 v254, v242, 4, 4\n"
"v_mfma_f32_16x16x32_bf16 v[136:139], v[116:119], a[148:151], v[136:139]\n"
"v_mov_b64 v[4:5], s[22:23]\n"
"v_mov_b64 v[32:33], s[22:23]\n"
"v_mfma_f32_16x16x32_bf16 v[136:139], v[120:123], a[152:155], v[136:139]\n"
"ds_read_b64_tr_b16 a[42:43], v249 offset:51208\n"
"ds_read_b64_tr_b16 v[180:181], v228 offset:32768\n"
"v_mfma_f32_16x16x32_bf16 v[136:139], v[224:227], a[156:159], v[136:139]\n"
"ds_read_b64_tr_b16 a[40:41], v249 offset:51200\n"
"ds_read_b64_tr_b16 v[212:213], v232 offset:49152\n"
"v_mfma_f32_16x16x32_bf16 v[152:155], v[224:227], a[172:175], v[196:199]\n"
"ds_read_b64_tr_b16 a[10:11], v249 offset:49160\n"
"ds_read_b64_tr_b16 v[216:217], v228 offset:57344\n"
"v_mfma_f32_16x16x32_bf16 v[152:155], v[120:123], a[168:171], v[152:155]\n"
"ds_read_b64_tr_b16 a[8:9], v249 offset:49152\n"
"v_mfma_f32_16x16x32_bf16 v[152:155], v[116:119], a[164:167], v[152:155]\n"
"ds_read_b64_tr_b16 a[24:25], v249 offset:50176\n"
"ds_read_b64_tr_b16 a[26:27], v249 offset:50184\n"
"v_mfma_f32_16x16x32_bf16 v[152:155], v[112:115], a[160:163], v[152:155]\n"
"ds_read_b64_tr_b16 a[228:229], v237 offset:14336\n"
"ds_read_b64_tr_b16 v[186:187], v232 offset:34816\n"
"v_mfma_f32_16x16x32_bf16 v[152:155], v[108:111], a[124:127], v[152:155]\n"
"ds_read_b64_tr_b16 a[226:227], v236 offset:14344\n"
"v_mov_b64 v[6:7], s[22:23]\n"
"v_mfma_f32_16x16x32_bf16 v[152:155], v[104:107], a[120:123], v[152:155]\n"
"ds_read_b64_tr_b16 a[220:221], v237 offset:8192\n"
"ds_read_b64_tr_b16 v[182:183], v228 offset:34816\n"
"v_mfma_f32_16x16x32_bf16 v[152:155], v[100:103], a[116:119], v[152:155]\n"
"ds_read_b64_tr_b16 a[210:211], v236 offset:12296\n"
"v_mov_b64 v[42:43], s[22:23]\n"
"v_mfma_f32_16x16x32_bf16 v[152:155], v[96:99], a[112:115], v[152:155]\n"
"ds_read_b64_tr_b16 v[208:209], v228 offset:49152\n"
"ds_read_b64_tr_b16 v[210:211], v228 offset:51200\n"
"v_mfma_f32_16x16x32_bf16 v[168:171], v[96:99], a[128:131], v[196:199]\n"
"ds_read_b64_tr_b16 v[184:185], v232 offset:32768\n"
"ds_read_b64_tr_b16 v[194:195], v232 offset:43008\n"
"v_mfma_f32_16x16x32_bf16 v[168:171], v[100:103], a[132:135], v[168:171]\n"
"ds_read_b64_tr_b16 v[188:189], v228 offset:40960\n"
"ds_read_b64_tr_b16 v[192:193], v232 offset:40960\n"
"v_mfma_f32_16x16x32_bf16 v[168:171], v[104:107], a[136:139], v[168:171]\n"
"ds_read_b64_tr_b16 v[220:221], v232 offset:57344\n"
"v_mov_b64 v[40:41], s[22:23]\n"
"v_mfma_f32_16x16x32_bf16 v[168:171], v[108:111], a[140:143], v[168:171]\n"
"s_waitcnt lgkmcnt(11)\n"
"ds_read_b64_tr_b16 a[224:225], v236 offset:14336\n"
"ds_read_b64_tr_b16 v[222:223], v232 offset:59392\n"
"v_mfma_f32_16x16x32_bf16 v[168:171], v[112:115], a[176:179], v[168:171]\n"
"ds_read_b64_tr_b16 v[214:215], v232 offset:51200\n"
"ds_read_b64_tr_b16 v[112:113], v229 offset:49152\n"
"v_mfma_f32_16x16x32_bf16 v[168:171], v[116:119], a[180:183], v[168:171]\n"
"ds_read_b64_tr_b16 a[212:213], v237 offset:12288\n"
"ds_read_b64_tr_b16 a[214:215], v237 offset:12296\n"
"v_mfma_f32_16x16x32_bf16 v[168:171], v[120:123], a[184:187], v[168:171]\n"
"s_add_u32 s41, s70, 327680\n"
"s_add_u32 m0, s71, 32768\n"
"ds_read_b64_tr_b16 v[98:99], v229 offset:34816\n"
"v_mfma_f32_16x16x32_bf16 v[168:171], v[224:227], a[188:191], v[168:171]\n"
"s_waitcnt lgkmcnt(12)\n"
"buffer_load_dwordx4 v240, s[28:31], s41 offen lds\n"
"v_mfma_f32_16x16x32_bf16 v[124:127], v[180:183], a[48:51], v[124:127]\n"
"s_waitcnt lgkmcnt(10)\n"
"v_mov_b64 v[38:39], s[22:23]\n"
"v_mfma_f32_16x16x32_bf16 v[124:127], v[184:187], a[52:55], v[124:127]\n"
"s_waitcnt lgkmcnt(8)\n"
"v_mul_u32_u24 v254, 2560, v254\n"
"v_mov_b64 v[50:51], s[22:23]\n"
"v_mfma_f32_16x16x32_bf16 v[124:127], v[188:191], a[56:59], v[124:127]\n"
"s_add_u32 s40, s41, s52\n"
"ds_read_b64_tr_b16 v[102:103], v233 offset:34816\n"
"ds_read_b64_tr_b16 v[110:111], v233 offset:43008\n"
"v_mfma_f32_16x16x32_bf16 v[124:127], v[192:195], a[60:63], v[124:127]\n"
"buffer_load_dwordx4 v240, s[28:31], s40 offen offset:1024 lds\n"
"v_mfma_f32_16x16x32_bf16 v[124:127], v[208:211], a[0:3], v[124:127]\n"
"s_waitcnt lgkmcnt(5)\n"
"v_mov_b64 v[20:21], s[22:23]\n"
"v_mfma_f32_16x16x32_bf16 v[124:127], v[212:215], a[4:7], v[124:127]\n"
"v_and_b32 v243, 0x3f0, v242\n"
"v_mov_b64 v[16:17], s[22:23]\n"
"v_mfma_f32_16x16x32_bf16 v[124:127], v[216:219], a[8:11], v[124:127]\n"
"s_add_u32 s40, s41, s53\n"
"ds_read_b64_tr_b16 a[232:233], v236 offset:10240\n"
"ds_read_b64_tr_b16 v[106:107], v229 offset:43008\n"
"v_mfma_f32_16x16x32_bf16 v[124:127], v[220:223], a[12:15], v[124:127]\n"
"buffer_load_dwordx4 v240, s[28:31], s40 offen offset:2048 lds\n"
"v_mfma_f32_16x16x32_bf16 v[140:143], v[220:223], a[28:31], v[140:143]\n"
"v_mov_b64 v[8:9], s[22:23]\n"
"v_mov_b64 v[28:29], s[22:23]\n"
"v_mfma_f32_16x16x32_bf16 v[140:143], v[216:219], a[24:27], v[140:143]\n"
"ds_read_b64_tr_b16 a[222:223], v237 offset:8200\n"
"ds_read_b64_tr_b16 v[122:123], v229 offset:59392\n"
"v_mfma_f32_16x16x32_bf16 v[140:143], v[212:215], a[20:23], v[140:143]\n"
"ds_read_b64_tr_b16 v[118:119], v233 offset:51200\n"
"v_subrev_f32 v124, s22, v124\n"
"v_mfma_f32_16x16x32_bf16 v[140:143], v[208:211], a[16:19], v[140:143]\n"
"ds_read_b64_tr_b16 v[114:115], v229 offset:51200\n"
"v_bfe_u32 v255, v242, 8, 1\n"
"v_mfma_f32_16x16x32_bf16 v[140:143], v[192:195], a[76:79], v[140:143]\n"
"ds_read_b64_tr_b16 a[208:209], v236 offset:12288\n"
"v_mov_b64 v[10:11], s[22:23]\n"
"v_mfma_f32_16x16x32_bf16 v[140:143], v[188:191], a[72:75], v[140:143]\n"
"ds_read_b64_tr_b16 v[120:121], v229 offset:57344\n"
"v_mfma_f32_16x16x32_bf16 v[140:143], v[184:187], a[68:71], v[140:143]\n"
"s_add_u32 s40, s41, s54\n"
"ds_read_b64_tr_b16 a[230:231], v237 offset:14344\n"
"ds_read_b64_tr_b16 v[96:97], v229 offset:32768\n"
"v_mfma_f32_16x16x32_bf16 v[140:143], v[180:183], a[64:67], v[140:143]\n"
"buffer_load_dwordx4 v240, s[28:31], s40 offen offset:3072 lds\n"
"v_mfma_f32_16x16x32_bf16 v[156:159], v[180:183], a[80:83], v[156:159]\n"
"s_add_u32 s41, s70, 491520\n"
"s_add_u32 m0, s71, 49152\n"
"v_mfma_f32_16x16x32_bf16 v[156:159], v[184:187], a[84:87], v[156:159]\n"
"buffer_load_dwordx4 v240, s[28:31], s41 offen lds\n"
"v_mfma_f32_16x16x32_bf16 v[156:159], v[188:191], a[88:91], v[156:159]\n"
"v_mfma_f32_16x16x32_bf16 v[156:159], v[192:195], a[92:95], v[156:159]\n"
"v_subrev_f32 v143, s22, v143\n"
"v_xor_b32 v228, s75, v228\n"
"v_mfma_f32_16x16x32_bf16 v[156:159], v[208:211], a[32:35], v[156:159]\n"
"ds_read_b64_tr_b16 v[104:105], v229 offset:40960\n"
"ds_read_b64_tr_b16 v[108:109], v233 offset:40960\n"
"v_mfma_f32_16x16x32_bf16 v[156:159], v[212:215], a[36:39], v[156:159]\n"
"ds_read_b64_tr_b16 v[100:101], v233 offset:32768\n"
"v_subrev_f32 v127, s22, v127\n"
"v_mfma_f32_16x16x32_bf16 v[156:159], v[216:219], a[40:43], v[156:159]\n"
"s_add_u32 s40, s41, s52\n"
"ds_read_b64_tr_b16 v[116:117], v233 offset:49152\n"
"v_add_u32 v236, s94, v243\n"
"v_mfma_f32_16x16x32_bf16 v[156:159], v[220:223], a[44:47], v[156:159]\n"
"s_waitcnt lgkmcnt(4)\n"
"buffer_load_dwordx4 v240, s[28:31], s40 offen offset:1024 lds\n"
"v_mfma_f32_16x16x32_bf16 v[172:175], v[220:223], a[228:231], v[172:175]\n"
"s_add_u32 s40, s41, s53\n"
"v_mov_b32 v241, s60\n"
"v_bfe_u32 v237, v242, 4, 3\n"
"v_mfma_f32_16x16x32_bf16 v[172:175], v[216:219], a[224:227], v[172:175]\n"
"buffer_load_dwordx4 v240, s[28:31], s40 offen offset:2048 lds\n"
"v_mfma_f32_16x16x32_bf16 v[172:175], v[212:215], a[212:215], v[172:175]\n"
"s_add_u32 s40, s41, s54\n"
"v_mov_b64 v[86:87], 0\n"
"v_mfma_f32_16x16x32_bf16 v[172:175], v[208:211], a[208:211], v[172:175]\n"
"buffer_load_dwordx4 v240, s[28:31], s40 offen offset:3072 lds\n"
"v_mfma_f32_16x16x32_bf16 v[172:175], v[192:195], a[236:239], v[172:175]\n"
"s_mul_i32 s62, s70, 224\n"
"v_subrev_f32 v142, s22, v142\n"
"v_subrev_f32 v159, s22, v159\n"
"v_mfma_f32_16x16x32_bf16 v[172:175], v[188:191], a[232:235], v[172:175]\n"
"s_add_u32 s20, s47, 3\n"
"v_lshl_add_u32 v254, v255, 5, v254\n"
"v_mov_b64 v[0:1], s[22:23]\n"
"v_mfma_f32_16x16x32_bf16 v[172:175], v[184:187], a[220:223], v[172:175]\n"
"s_add_u32 s70, s61, s20\n"
"ds_read_b64_tr_b16 v[224:225], v233 offset:57344\n"
"ds_read_b64_tr_b16 v[226:227], v233 offset:59392\n"
"v_mfma_f32_16x16x32_bf16 v[172:175], v[180:183], a[216:219], v[172:175]\n"
"s_and_b32 s70, s70, 7\n"
"v_subrev_f32 v126, s22, v126\n"
"v_xor_b32 v229, s75, v229\n"
"v_mfma_f32_16x16x32_bf16 v[128:131], v[96:99], a[48:51], v[128:131]\n"
"s_waitcnt lgkmcnt(2)\n"
"s_cmp_ge_u32 s47, 5\ns_cselect_b32 s70, s20, s70\n"
"ds_read_b64_tr_b16 v[184:185], v234 offset:32768\n"
"v_mov_b64 v[90:91], 0\n"
"v_mfma_f32_16x16x32_bf16 v[128:131], v[100:103], a[52:55], v[128:131]\n"
"s_cmp_eq_u32 s47, 7\ns_cselect_b32 s70, s61, s70\n"
"ds_read_b64_tr_b16 v[208:209], v230 offset:49152\n"
"v_subrev_f32 v141, s22, v141\n"
"v_mfma_f32_16x16x32_bf16 v[128:131], v[104:107], a[56:59], v[128:131]\n"
"s_lshl_b32 s70, s70, 7\n"
"ds_read_b64_tr_b16 v[216:217], v230 offset:57344\n"
"v_mov_b64 v[244:245], 0\n"
"v_mfma_f32_16x16x32_bf16 v[128:131], v[108:111], a[60:63], v[128:131]\n"
"s_xor_b32 s71, s71, s75\n"
"ds_read_b64_tr_b16 v[222:223], v234 offset:59392\n"
"v_mov_b64 v[22:23], s[22:23]\n"
"v_mfma_f32_16x16x32_bf16 v[128:131], v[112:115], a[0:3], v[128:131]\n"
"v_lshlrev_b32_e64 v201, 2, s72\n"
"ds_read_b64_tr_b16 v[182:183], v230 offset:34816\n"
"v_xor_b32 v232, s75, v232\n"
"v_mfma_f32_16x16x32_bf16 v[128:131], v[116:119], a[4:7], v[128:131]\n"
"global_load_dword v200, v201, s[6:7] sc1\n"
"v_mfma_f32_16x16x32_bf16 v[128:131], v[120:123], a[8:11], v[128:131]\n"
"s_waitcnt lgkmcnt(5)\n"
"v_bfe_u32 v255, v242, 9, 1\n"
"v_subrev_f32 v125, s22, v125\n"
"v_mfma_f32_16x16x32_bf16 v[128:131], v[224:227], a[12:15], v[128:131]\n"
"v_lshl_add_u32 v254, v255, 4, v254\n"
"v_subrev_f32 v157, s22, v157\n"
"v_mfma_f32_16x16x32_bf16 v[144:147], v[224:227], a[28:31], v[144:147]\n"
"ds_read_b64_tr_b16 v[210:211], v230 offset:51200\n"
"v_mfma_f32_16x16x32_bf16 v[144:147], v[120:123], a[24:27], v[144:147]\n"
"v_subrev_f32 v140, s22, v140\n"
"v_mfma_f32_16x16x32_bf16 v[144:147], v[116:119], a[20:23], v[144:147]\n"
"ds_read_b64_tr_b16 v[188:189], v230 offset:40960\n"
"v_add_u32 v243, s96, v243\n"
"v_mfma_f32_16x16x32_bf16 v[144:147], v[112:115], a[16:19], v[144:147]\n"
"ds_read_b64_tr_b16 v[214:215], v234 offset:51200\n"
"v_subrev_f32 v129, s22, v129\n"
"v_mfma_f32_16x16x32_bf16 v[144:147], v[108:111], a[76:79], v[144:147]\n"
"v_subrev_f32 v156, s22, v156\n"
"v_subrev_f32 v130, s22, v130\n"
"v_mfma_f32_16x16x32_bf16 v[144:147], v[104:107], a[72:75], v[144:147]\n"
"ds_read_b64_tr_b16 v[186:187], v234 offset:34816\n"
"v_subrev_f32 v131, s22, v131\n"
"v_mfma_f32_16x16x32_bf16 v[144:147], v[100:103], a[68:71], v[144:147]\n"
"ds_read_b64_tr_b16 v[190:191], v230 offset:43008\n"
"v_cvt_pk_bf16_f32 v124, v124, v125\n"
"v_mfma_f32_16x16x32_bf16 v[144:147], v[96:99], a[64:67], v[144:147]\n"
"v_mov_b64 v[12:13], s[22:23]\n"
"v_cvt_pk_bf16_f32 v125, v126, v127\n"
"v_mfma_f32_16x16x32_bf16 v[160:163], v[96:99], a[80:83], v[160:163]\n"
"ds_read_b64_tr_b16 v[220:221], v234 offset:57344\n"
"v_subrev_f32 v128, s22, v128\n"
"v_mfma_f32_16x16x32_bf16 v[160:163], v[100:103], a[84:87], v[160:163]\n"
"v_subrev_f32 v172, s22, v172\n"
"v_mfma_f32_16x16x32_bf16 v[160:163], v[104:107], a[88:91], v[160:163]\n"
"ds_read_b64_tr_b16 v[192:193], v234 offset:40960\n"
"v_cvt_pk_bf16_f32 v127, v130, v131\n"
"v_mfma_f32_16x16x32_bf16 v[160:163], v[108:111], a[92:95], v[160:163]\n"
"v_cvt_pk_bf16_f32 v140, v140, v141\n"
"v_cvt_pk_bf16_f32 v126, v128, v129\n"
"v_mfma_f32_16x16x32_bf16 v[160:163], v[112:115], a[32:35], v[160:163]\n"
"ds_read_b64_tr_b16 v[194:195], v234 offset:43008\n"
"v_mfma_f32_16x16x32_bf16 v[160:163], v[116:119], a[36:39], v[160:163]\n"
"v_mov_b64 v[54:55], s[22:23]\n"
"v_subrev_f32 v147, s22, v147\n"
"v_mfma_f32_16x16x32_bf16 v[160:163], v[120:123], a[40:43], v[160:163]\n"
"ds_read_b64_tr_b16 v[180:181], v230 offset:32768\n"
"v_cvt_pk_bf16_f32 v141, v142, v143\n"
"v_mfma_f32_16x16x32_bf16 v[160:163], v[224:227], a[44:47], v[160:163]\n"
"buffer_store_dwordx4 v[124:127], v243, s[32:35], s16 offen sc0 sc1\n"
"v_subrev_f32 v146, s22, v146\n"
"v_mfma_f32_16x16x32_bf16 v[176:179], v[224:227], a[228:231], v[176:179]\n"
"ds_read_b64_tr_b16 v[218:219], v230 offset:59392\n"
"v_max_u32 v237, 1, v237\n"
"v_mfma_f32_16x16x32_bf16 v[176:179], v[120:123], a[224:227], v[176:179]\n"
"v_mov_b64 v[52:53], s[22:23]\n"
"v_mov_b64 v[94:95], 0\n"
"v_mfma_f32_16x16x32_bf16 v[176:179], v[116:119], a[212:215], v[176:179]\n"
"ds_read_b64_tr_b16 v[122:123], v231 offset:59392\n"
"v_subrev_f32 v158, s22, v158\n"
"v_mfma_f32_16x16x32_bf16 v[176:179], v[112:115], a[208:211], v[176:179]\n"
"v_mov_b64 v[92:93], 0\n"
"v_mfma_f32_16x16x32_bf16 v[176:179], v[108:111], a[236:239], v[176:179]\n"
"ds_read_b64_tr_b16 v[212:213], v234 offset:49152\n"
"v_subrev_f32 v162, s22, v162\n"
"v_mfma_f32_16x16x32_bf16 v[176:179], v[104:107], a[232:235], v[176:179]\n"
"v_cvt_pk_bf16_f32 v143, v146, v147\n"
"v_subrev_f32 v163, s22, v163\n"
"v_mfma_f32_16x16x32_bf16 v[176:179], v[100:103], a[220:223], v[176:179]\n"
"ds_read_b64_tr_b16 v[100:101], v235 offset:32768\n"
"v_mfma_f32_16x16x32_bf16 v[176:179], v[96:99], a[216:219], v[176:179]\n"
"s_waitcnt lgkmcnt(4)\n"
"v_subrev_f32 v145, s22, v145\n"
"v_mfma_f32_16x16x32_bf16 v[132:135], v[180:183], a[48:51], v[132:135]\n"
"ds_read_b64_tr_b16 v[106:107], v231 offset:43008\n"
"v_add_u32 v252, s98, v254\n"
"v_mfma_f32_16x16x32_bf16 v[132:135], v[184:187], a[52:55], v[132:135]\n"
"ds_read_b64_tr_b16 v[112:113], v231 offset:49152\n"
"v_subrev_f32 v144, s22, v144\n"
"v_mfma_f32_16x16x32_bf16 v[132:135], v[188:191], a[56:59], v[132:135]\n"
"ds_read_b64_tr_b16 v[96:97], v231 offset:32768\n"
"v_cvt_pk_bf16_f32 v142, v144, v145\n"
"v_mfma_f32_16x16x32_bf16 v[132:135], v[192:195], a[60:63], v[132:135]\n"
"ds_read_b64_tr_b16 v[98:99], v231 offset:34816\n"
"v_subrev_f32 v173, s22, v173\n"
"v_mfma_f32_16x16x32_bf16 v[132:135], v[208:211], a[0:3], v[132:135]\n"
"s_waitcnt lgkmcnt(5)\n"
"v_subrev_f32 v160, s22, v160\n"
"v_mfma_f32_16x16x32_bf16 v[132:135], v[212:215], a[4:7], v[132:135]\n"
"ds_read_b64_tr_b16 v[110:111], v235 offset:43008\n"
"v_xor_b32 v248, s75, v248\n"
"v_mfma_f32_16x16x32_bf16 v[132:135], v[216:219], a[8:11], v[132:135]\n"
"v_subrev_f32 v175, s22, v175\n"
"v_subrev_f32 v161, s22, v161\n"
"v_mfma_f32_16x16x32_bf16 v[132:135], v[220:223], a[12:15], v[132:135]\n"
"buffer_store_dwordx4 v[140:143], v243, s[32:35], s16 offen offset:2048 sc0 sc1\n"
"ds_read_b64_tr_b16 v[116:117], v235 offset:49152\n"
"v_mfma_f32_16x16x32_bf16 v[148:151], v[220:223], a[28:31], v[148:151]\n"
"v_cvt_pk_bf16_f32 v156, v156, v157\n"
"v_subrev_f32 v179, s22, v179\n"
"v_mfma_f32_16x16x32_bf16 v[148:151], v[216:219], a[24:27], v[148:151]\n"
"ds_read_b64_tr_b16 v[104:105], v231 offset:40960\n"
"v_mfma_f32_16x16x32_bf16 v[148:151], v[212:215], a[20:23], v[148:151]\n"
"ds_read_b64_tr_b16 v[108:109], v235 offset:40960\n"
"v_mov_b64 v[26:27], s[22:23]\n"
"v_mfma_f32_16x16x32_bf16 v[148:151], v[208:211], a[16:19], v[148:151]\n"
"v_subrev_f32 v174, s22, v174\n"
"v_subrev_f32 v177, s22, v177\n"
"v_mfma_f32_16x16x32_bf16 v[148:151], v[192:195], a[76:79], v[148:151]\n"
"ds_read_b64_tr_b16 v[102:103], v235 offset:34816\n"
"v_mov_b64 v[18:19], s[22:23]\n"
"v_mfma_f32_16x16x32_bf16 v[148:151], v[188:191], a[72:75], v[148:151]\n"
"v_mov_b64 v[46:47], s[22:23]\n"
"v_cvt_pk_bf16_f32 v172, v172, v173\n"
"v_mfma_f32_16x16x32_bf16 v[148:151], v[184:187], a[68:71], v[148:151]\n"
"ds_read_b64_tr_b16 v[118:119], v235 offset:51200\n"
"v_mfma_f32_16x16x32_bf16 v[148:151], v[180:183], a[64:67], v[148:151]\n"
"v_xor_b32 v237, s61, v237\n"
"v_mov_b64 v[44:45], s[22:23]\n"
"v_mfma_f32_16x16x32_bf16 v[164:167], v[180:183], a[80:83], v[164:167]\n"
"ds_read_b64_tr_b16 v[114:115], v231 offset:51200\n"
"v_cvt_pk_bf16_f32 v173, v174, v175\n"
"v_mfma_f32_16x16x32_bf16 v[164:167], v[184:187], a[84:87], v[164:167]\n"
"v_add_u32 v253, s99, v254\n"
"v_mfma_f32_16x16x32_bf16 v[164:167], v[188:191], a[88:91], v[164:167]\n"
"ds_read_b64_tr_b16 v[120:121], v231 offset:57344\n"
"v_mov_b64 v[24:25], s[22:23]\n"
"v_mfma_f32_16x16x32_bf16 v[164:167], v[192:195], a[92:95], v[164:167]\n"
"v_mov_b64 v[34:35], s[22:23]\n"
"v_cvt_pk_bf16_f32 v157, v158, v159\n"
"v_mfma_f32_16x16x32_bf16 v[164:167], v[208:211], a[32:35], v[164:167]\n"
"ds_read_b64_tr_b16 v[226:227], v235 offset:59392\n"
"v_cvt_pk_bf16_f32 v158, v160, v161\n"
"v_mfma_f32_16x16x32_bf16 v[164:167], v[212:215], a[36:39], v[164:167]\n"
"v_xor_b32 v233, s75, v233\n"
"v_mfma_f32_16x16x32_bf16 v[164:167], v[216:219], a[40:43], v[164:167]\n"
"ds_read_b64_tr_b16 v[224:225], v235 offset:57344\n"
"v_mfma_f32_16x16x32_bf16 v[164:167], v[220:223], a[44:47], v[164:167]\n"
"s_barrier\n"
"s_waitcnt lgkmcnt(4)\n"
"v_mfma_f32_16x16x32_bf16 v[136:139], v[96:99], a[48:51], v[136:139]\n"
"ds_read_b64_tr_b16 v[190:191], v228 offset:10240\n"
"v_mov_b64 v[246:247], 0\n"
"v_mfma_f32_16x16x32_bf16 v[136:139], v[100:103], a[52:55], v[136:139]\n"
"ds_read_b64_tr_b16 v[192:193], v232 offset:8192\n"
"v_subrev_f32 v135, s22, v135\n"
"v_mfma_f32_16x16x32_bf16 v[136:139], v[104:107], a[56:59], v[136:139]\n"
"ds_read_b64_tr_b16 v[182:183], v228 offset:2048\n"
"v_subrev_f32 v133, s22, v133\n"
"v_mfma_f32_16x16x32_bf16 v[136:139], v[108:111], a[60:63], v[136:139]\n"
"s_waitcnt lgkmcnt(5)\n"
"ds_read_b64_tr_b16 v[214:215], v232 offset:18432\n"
"v_mfma_f32_16x16x32_bf16 v[136:139], v[112:115], a[0:3], v[136:139]\n"
"ds_read_b64_tr_b16 v[184:185], v232 offset:0\n"
"v_cvt_pk_bf16_f32 v159, v162, v163\n"
"v_mfma_f32_16x16x32_bf16 v[136:139], v[116:119], a[4:7], v[136:139]\n"
"ds_read_b64_tr_b16 v[180:181], v228 offset:0\n"
"v_subrev_f32 v178, s22, v178\n"
"v_mfma_f32_16x16x32_bf16 v[136:139], v[120:123], a[8:11], v[136:139]\n"
"s_waitcnt lgkmcnt(6)\n"
"ds_read_b64_tr_b16 v[188:189], v228 offset:8192\n"
"v_subrev_f32 v176, s22, v176\n"
"v_mfma_f32_16x16x32_bf16 v[136:139], v[224:227], a[12:15], v[136:139]\n"
"buffer_store_dwordx4 v[156:159], v243, s[32:35], s17 offen sc0 sc1\n"
"ds_read_b64_tr_b16 v[210:211], v228 offset:18432\n"
"v_mfma_f32_16x16x32_bf16 v[152:155], v[224:227], a[28:31], v[152:155]\n"
"ds_read_b64_tr_b16 v[218:219], v228 offset:26624\n"
"v_xor_b32 v249, s75, v249\n"
"v_mfma_f32_16x16x32_bf16 v[152:155], v[120:123], a[24:27], v[152:155]\n"
"ds_read_b64_tr_b16 v[222:223], v232 offset:26624\n"
"v_subrev_f32 v132, s22, v132\n"
"v_mfma_f32_16x16x32_bf16 v[152:155], v[116:119], a[20:23], v[152:155]\n"
"ds_read_b64_tr_b16 v[208:209], v228 offset:16384\n"
"v_cvt_pk_bf16_f32 v174, v176, v177\n"
"v_mfma_f32_16x16x32_bf16 v[152:155], v[112:115], a[16:19], v[152:155]\n"
"ds_read_b64_tr_b16 v[216:217], v228 offset:24576\n"
"v_mfma_f32_16x16x32_bf16 v[152:155], v[108:111], a[76:79], v[152:155]\n"
"ds_read_b64_tr_b16 v[194:195], v232 offset:10240\n"
"v_cvt_pk_bf16_f32 v132, v132, v133\n"
"v_mfma_f32_16x16x32_bf16 v[152:155], v[104:107], a[72:75], v[152:155]\n"
"ds_read_b64_tr_b16 v[186:187], v232 offset:2048\n"
"v_mov_b64 v[14:15], s[22:23]\n"
"v_mfma_f32_16x16x32_bf16 v[152:155], v[100:103], a[68:71], v[152:155]\n"
"ds_read_b64_tr_b16 v[220:221], v232 offset:24576\n"
"v_cvt_pk_bf16_f32 v175, v178, v179\n"
"v_mfma_f32_16x16x32_bf16 v[152:155], v[96:99], a[64:67], v[152:155]\n"
"ds_read_b64_tr_b16 v[212:213], v232 offset:16384\n"
"v_mov_b64 v[30:31], s[22:23]\n"
"v_mfma_f32_16x16x32_bf16 v[168:171], v[96:99], a[80:83], v[168:171]\n"
"ds_read_b64_tr_b16 v[96:97], v229 offset:0\n"
"ds_read_b64_tr_b16 v[98:99], v229 offset:2048\n"
"v_mfma_f32_16x16x32_bf16 v[168:171], v[100:103], a[84:87], v[168:171]\n"
"ds_read_b64_tr_b16 v[100:101], v233 offset:0\n"
"ds_read_b64_tr_b16 v[102:103], v233 offset:2048\n"
"v_mfma_f32_16x16x32_bf16 v[168:171], v[104:107], a[88:91], v[168:171]\n"
"ds_read_b64_tr_b16 v[104:105], v229 offset:8192\n"
"v_mov_b64 v[48:49], s[22:23]\n"
"v_mfma_f32_16x16x32_bf16 v[168:171], v[108:111], a[92:95], v[168:171]\n"
"v_mov_b64 v[84:85], 0\n"
"v_mov_b64 v[88:89], 0\n"
"v_mfma_f32_16x16x32_bf16 v[168:171], v[112:115], a[32:35], v[168:171]\n"
"v_add_u32 v238, s97, v254\n"
"v_subrev_f32 v134, s22, v134\n"
"v_mfma_f32_16x16x32_bf16 v[168:171], v[116:119], a[36:39], v[168:171]\n"
"v_xor_b32 v239, 64, v238\n"
"v_mfma_f32_16x16x32_bf16 v[168:171], v[120:123], a[40:43], v[168:171]\n"
"s_add_u32 s47, s47, 1\n"
"v_lshlrev_b32 v237, 2, v237\n"
"v_mov_b64 v[36:37], s[22:23]\n"
"v_mfma_f32_16x16x32_bf16 v[168:171], v[224:227], a[44:47], v[168:171]\n"
"s_waitcnt lgkmcnt(5)\n"
"LGL_%=:\n"
"v_mfma_f32_16x16x32_bf16 v[124:127], v[180:183], a[96:99], v[196:199]\n"
"ds_read_b64_tr_b16 v[106:107], v229 offset:10240\n"
"v_cvt_pk_bf16_f32 v133, v134, v135\n"
"v_mfma_f32_16x16x32_bf16 v[124:127], v[184:187], a[100:103], v[124:127]\n"
"ds_read_b64_tr_b16 v[108:109], v233 offset:8192\n"
"v_subrev_f32 v136, s22, v136\n"
"v_mfma_f32_16x16x32_bf16 v[124:127], v[188:191], a[104:107], v[124:127]\n"
"ds_read_b64_tr_b16 v[110:111], v233 offset:10240\n"
"v_subrev_f32 v137, s22, v137\n"
"v_mfma_f32_16x16x32_bf16 v[124:127], v[192:195], a[108:111], v[124:127]\n"
"ds_read_b64_tr_b16 v[112:113], v229 offset:16384\n"
"v_subrev_f32 v138, s22, v138\n"
"v_mfma_f32_16x16x32_bf16 v[124:127], v[208:211], a[144:147], v[124:127]\n"
"ds_read_b64_tr_b16 v[114:115], v229 offset:18432\n"
"v_subrev_f32 v139, s22, v139\n"
"v_mfma_f32_16x16x32_bf16 v[124:127], v[212:215], a[148:151], v[124:127]\n"
"s_waitcnt lgkmcnt(7)\n"
"ds_read_b64_tr_b16 v[116:117], v233 offset:16384\n"
"v_cvt_pk_bf16_f32 v134, v136, v137\n"
"v_mfma_f32_16x16x32_bf16 v[124:127], v[216:219], a[152:155], v[124:127]\n"
"ds_read_b64_tr_b16 v[118:119], v233 offset:18432\n"
"v_cvt_pk_bf16_f32 v135, v138, v139\n"
"v_mfma_f32_16x16x32_bf16 v[124:127], v[220:223], a[156:159], v[124:127]\n"
"buffer_store_dwordx4 v[172:175], v243, s[32:35], s17 offen offset:2048 sc0 sc1\n"
"v_subrev_f32 v148, s22, v148\n"
"v_mfma_f32_16x16x32_bf16 v[140:143], v[220:223], a[172:175], v[196:199]\n"
"ds_read_b64_tr_b16 v[120:121], v229 offset:24576\n"
"v_subrev_f32 v149, s22, v149\n"
"v_mfma_f32_16x16x32_bf16 v[140:143], v[216:219], a[168:171], v[140:143]\n"
"ds_read_b64_tr_b16 v[122:123], v229 offset:26624\n"
"v_subrev_f32 v150, s22, v150\n"
"v_mfma_f32_16x16x32_bf16 v[140:143], v[212:215], a[164:167], v[140:143]\n"
"ds_read_b64_tr_b16 v[224:225], v233 offset:24576\n"
"v_subrev_f32 v151, s22, v151\n"
"v_mfma_f32_16x16x32_bf16 v[140:143], v[208:211], a[160:163], v[140:143]\n"
"ds_read_b64_tr_b16 v[226:227], v233 offset:26624\n"
"v_cvt_pk_bf16_f32 v148, v148, v149\n"
"v_mfma_f32_16x16x32_bf16 v[140:143], v[192:195], a[124:127], v[140:143]\n"
"v_cvt_pk_bf16_f32 v149, v150, v151\n"
"v_subrev_f32 v152, s22, v152\n"
"v_mfma_f32_16x16x32_bf16 v[140:143], v[188:191], a[120:123], v[140:143]\n"
"s_add_u32 s41, s70, 0\n"
"v_subrev_f32 v153, s22, v153\n"
"v_subrev_f32 v154, s22, v154\n"
"v_mfma_f32_16x16x32_bf16 v[140:143], v[184:187], a[116:119], v[140:143]\n"
"s_mov_b32 m0, s71\n"
"v_subrev_f32 v155, s22, v155\n"
"v_cvt_pk_bf16_f32 v150, v152, v153\n"
"v_mfma_f32_16x16x32_bf16 v[140:143], v[180:183], a[112:115], v[140:143]\n"
"s_waitcnt vmcnt(14)\n"
"buffer_load_dwordx4 v240, s[28:31], s41 offen lds\n"
"v_mfma_f32_16x16x32_bf16 v[156:159], v[180:183], a[128:131], v[196:199]\n"
"v_xor_b32 v230, s75, v230\n"
"v_cvt_pk_bf16_f32 v151, v154, v155\n"
"v_mfma_f32_16x16x32_bf16 v[156:159], v[184:187], a[132:135], v[156:159]\n"
"v_xor_b32 v234, s75, v234\n"
"v_subrev_f32 v164, s22, v164\n"
"v_mfma_f32_16x16x32_bf16 v[156:159], v[188:191], a[136:139], v[156:159]\n"
"v_subrev_f32 v165, s22, v165\n"
"v_subrev_f32 v166, s22, v166\n"
"v_mfma_f32_16x16x32_bf16 v[156:159], v[192:195], a[140:143], v[156:159]\n"
"v_subrev_f32 v167, s22, v167\n"
"v_cvt_pk_bf16_f32 v164, v164, v165\n"
"v_mfma_f32_16x16x32_bf16 v[156:159], v[208:211], a[176:179], v[156:159]\n"
"v_cvt_pk_bf16_f32 v165, v166, v167\n"
"v_subrev_f32 v168, s22, v168\n"
"v_mfma_f32_16x16x32_bf16 v[156:159], v[212:215], a[180:183], v[156:159]\n"
"v_subrev_f32 v169, s22, v169\n"
"v_subrev_f32 v170, s22, v170\n"
"v_mfma_f32_16x16x32_bf16 v[156:159], v[216:219], a[184:187], v[156:159]\n"
"v_subrev_f32 v171, s22, v171\n"
"v_cvt_pk_bf16_f32 v166, v168, v169\n"
"v_mfma_f32_16x16x32_bf16 v[156:159], v[220:223], a[188:191], v[156:159]\n"
"buffer_store_dwordx4 v[132:135], v243, s[32:35], s16 offen offset:1024 sc0 sc1\n"
"v_cvt_pk_bf16_f32 v167, v170, v171\n"
"v_mfma_f32_16x16x32_bf16 v[172:175], v[220:223], a[252:255], v[196:199]\n"
"ds_read_b64_tr_b16 v[220:221], v234 offset:24576\n"
"v_xor_b32 v231, s75, v231\n"
"v_mfma_f32_16x16x32_bf16 v[172:175], v[216:219], a[248:251], v[172:175]\n"
"ds_read_b64_tr_b16 v[216:217], v230 offset:24576\n"
"v_xor_b32 v235, s75, v235\n"
"v_mfma_f32_16x16x32_bf16 v[172:175], v[212:215], a[244:247], v[172:175]\n"
"ds_read_b64_tr_b16 v[212:213], v234 offset:16384\n"
"v_add_u32 v241, 1, v241\n"
"v_mfma_f32_16x16x32_bf16 v[172:175], v[208:211], a[240:243], v[172:175]\n"
"ds_read_b64_tr_b16 v[208:209], v230 offset:16384\n"
"v_lshlrev_b32 v202, 16, v84\n"
"v_mfma_f32_16x16x32_bf16 v[172:175], v[192:195], a[204:207], v[172:175]\n"
"ds_read_b64_tr_b16 v[192:193], v234 offset:8192\n"
"v_and_b32 v84, s73, v84\n"
"v_mfma_f32_16x16x32_bf16 v[172:175], v[188:191], a[200:203], v[172:175]\n"
"ds_read_b64_tr_b16 v[188:189], v230 offset:8192\n"
"v_add_f32 v25, v25, v84\n"
"v_mfma_f32_16x16x32_bf16 v[172:175], v[184:187], a[196:199], v[172:175]\n"
"s_add_u32 s40, s41, s52\n"
"ds_read_b64_tr_b16 v[184:185], v234 offset:0\n"
"v_lshlrev_b32 v203, 16, v85\n"
"v_mfma_f32_16x16x32_bf16 v[172:175], v[180:183], a[192:195], v[172:175]\n"
"s_waitcnt vmcnt(13) lgkmcnt(7)\n"
"buffer_load_dwordx4 v240, s[28:31], s40 offen offset:1024 lds\n"
"v_mfma_f32_16x16x32_bf16 v[128:131], v[96:99], a[96:99], v[196:199]\n"
"s_waitcnt lgkmcnt(6)\n"
"ds_read_b64_tr_b16 v[180:181], v230 offset:0\n"
"v_and_b32 v85, s73, v85\n"
"v_mfma_f32_16x16x32_bf16 v[128:131], v[100:103], a[100:103], v[128:131]\n"
"ds_read_b64_tr_b16 v[182:183], v230 offset:2048\n"
"v_add_f32 v27, v27, v85\n"
"v_mfma_f32_16x16x32_bf16 v[128:131], v[104:107], a[104:107], v[128:131]\n"
"ds_read_b64_tr_b16 v[186:187], v234 offset:2048\n"
"v_lshlrev_b32 v204, 16, v86\n"
"v_mfma_f32_16x16x32_bf16 v[128:131], v[108:111], a[108:111], v[128:131]\n"
"ds_read_b64_tr_b16 v[190:191], v230 offset:10240\n"
"v_and_b32 v86, s73, v86\n"
"v_mfma_f32_16x16x32_bf16 v[128:131], v[112:115], a[144:147], v[128:131]\n"
"s_waitcnt vmcnt(6)\n"
"ds_read_b64_tr_b16 v[194:195], v234 offset:10240\n"
"v_add_f32 v29, v29, v86\n"
"v_mfma_f32_16x16x32_bf16 v[128:131], v[116:119], a[148:151], v[128:131]\n"
"v_subrev_u32 v200, s21, v200\n"
"ds_read_b64_tr_b16 v[210:211], v230 offset:18432\n"
"v_mfma_f32_16x16x32_bf16 v[128:131], v[120:123], a[152:155], v[128:131]\n"
"v_cmp_lt_u32 vcc, s69, v200\n"
"ds_read_b64_tr_b16 v[214:215], v234 offset:18432\n"
"v_mfma_f32_16x16x32_bf16 v[128:131], v[224:227], a[156:159], v[128:131]\n"
"ds_read_b64_tr_b16 v[218:219], v230 offset:26624\n"
"s_cbranch_vccnz LGP_%=\nLGPK_%=:\n"
"v_mfma_f32_16x16x32_bf16 v[144:147], v[224:227], a[172:175], v[196:199]\n"
"s_sub_u32 s72, s72, 1\n"
"ds_read_b64_tr_b16 v[222:223], v234 offset:26624\n"
"v_lshlrev_b32 v205, 16, v87\n"
"v_mfma_f32_16x16x32_bf16 v[144:147], v[120:123], a[168:171], v[144:147]\n"
"s_and_b32 s72, s72, 7\n"
"v_and_b32 v87, s73, v87\n"
"v_add_f32 v31, v31, v87\n"
"v_mfma_f32_16x16x32_bf16 v[144:147], v[116:119], a[164:167], v[144:147]\n"
"v_add_f32 v24, v24, v202\n"
"v_add_f32 v26, v26, v203\n"
"v_mfma_f32_16x16x32_bf16 v[144:147], v[112:115], a[160:163], v[144:147]\n"
"buffer_load_dwordx4 v[72:75], v243, s[32:35], s18 offen sc1\n"
"v_mfma_f32_16x16x32_bf16 v[144:147], v[108:111], a[124:127], v[144:147]\n"
"v_lshlrev_b32 v206, 16, v88\n"
"v_and_b32 v88, s73, v88\n"
"v_mfma_f32_16x16x32_bf16 v[144:147], v[104:107], a[120:123], v[144:147]\n"
"v_add_f32 v33, v33, v88\n"
"v_lshlrev_b32 v207, 16, v89\n"
"v_mfma_f32_16x16x32_bf16 v[144:147], v[100:103], a[116:119], v[144:147]\n"
"v_and_b32 v89, s73, v89\n"
"v_add_f32 v35, v35, v89\n"
"v_mfma_f32_16x16x32_bf16 v[144:147], v[96:99], a[112:115], v[144:147]\n"
"buffer_store_dwordx4 v[148:151], v243, s[32:35], s16 offen offset:3072 sc0 sc1\n"
"v_lshlrev_b32 v202, 16, v90\n"
"v_mfma_f32_16x16x32_bf16 v[160:163], v[96:99], a[128:131], v[196:199]\n"
"v_and_b32 v90, s73, v90\n"
"v_add_f32 v37, v37, v90\n"
"v_mfma_f32_16x16x32_bf16 v[160:163], v[100:103], a[132:135], v[160:163]\n"
"v_lshlrev_b32 v203, 16, v91\n"
"v_and_b32 v91, s73, v91\n"
"v_mfma_f32_16x16x32_bf16 v[160:163], v[104:107], a[136:139], v[160:163]\n"
"v_add_f32 v28, v28, v204\n"
"v_add_f32 v39, v39, v91\n"
"v_mfma_f32_16x16x32_bf16 v[160:163], v[108:111], a[140:143], v[160:163]\n"
"v_add_f32 v30, v30, v205\n"
"v_add_f32 v32, v32, v206\n"
"v_mfma_f32_16x16x32_bf16 v[160:163], v[112:115], a[176:179], v[160:163]\n"
"v_add_f32 v34, v34, v207\n"
"v_lshlrev_b32 v204, 16, v92\n"
"v_mfma_f32_16x16x32_bf16 v[160:163], v[116:119], a[180:183], v[160:163]\n"
"v_and_b32 v92, s73, v92\n"
"v_add_f32 v41, v41, v92\n"
"v_mfma_f32_16x16x32_bf16 v[160:163], v[120:123], a[184:187], v[160:163]\n"
"s_add_u32 s40, s41, s53\n"
"v_lshlrev_b32 v205, 16, v93\n"
"v_and_b32 v93, s73, v93\n"
"v_mfma_f32_16x16x32_bf16 v[160:163], v[224:227], a[188:191], v[160:163]\n"
"buffer_load_dwordx4 v240, s[28:31], s40 offen offset:2048 lds\n"
"v_mfma_f32_16x16x32_bf16 v[176:179], v[224:227], a[252:255], v[196:199]\n"
"ds_read_b64_tr_b16 v[224:225], v235 offset:24576\n"
"v_add_f32 v43, v43, v93\n"
"v_mfma_f32_16x16x32_bf16 v[176:179], v[120:123], a[248:251], v[176:179]\n"
"ds_read_b64_tr_b16 v[120:121], v231 offset:24576\n"
"v_lshlrev_b32 v206, 16, v94\n"
"v_mfma_f32_16x16x32_bf16 v[176:179], v[116:119], a[244:247], v[176:179]\n"
"ds_read_b64_tr_b16 v[116:117], v235 offset:16384\n"
"v_and_b32 v94, s73, v94\n"
"v_mfma_f32_16x16x32_bf16 v[176:179], v[112:115], a[240:243], v[176:179]\n"
"buffer_load_dwordx4 v[76:79], v243, s[32:35], s18 offen offset:1024 sc1\n"
"v_mfma_f32_16x16x32_bf16 v[176:179], v[108:111], a[204:207], v[176:179]\n"
"ds_read_b64_tr_b16 v[108:109], v235 offset:8192\n"
"v_add_f32 v45, v45, v94\n"
"v_mfma_f32_16x16x32_bf16 v[176:179], v[104:107], a[200:203], v[176:179]\n"
"ds_read_b64_tr_b16 v[104:105], v231 offset:8192\n"
"v_lshlrev_b32 v207, 16, v95\n"
"v_mfma_f32_16x16x32_bf16 v[176:179], v[100:103], a[196:199], v[176:179]\n"
"s_add_u32 s40, s41, s54\n"
"ds_read_b64_tr_b16 v[100:101], v235 offset:0\n"
"v_and_b32 v95, s73, v95\n"
"v_mfma_f32_16x16x32_bf16 v[176:179], v[96:99], a[192:195], v[176:179]\n"
"s_waitcnt lgkmcnt(6)\n"
"buffer_load_dwordx4 v240, s[28:31], s40 offen offset:3072 lds\n"
"v_mfma_f32_16x16x32_bf16 v[132:135], v[180:183], a[96:99], v[196:199]\n"
"ds_read_b64_tr_b16 v[96:97], v231 offset:0\n"
"v_add_f32 v47, v47, v95\n"
"v_mfma_f32_16x16x32_bf16 v[132:135], v[184:187], a[100:103], v[132:135]\n"
"ds_read_b64_tr_b16 v[98:99], v231 offset:2048\n"
"v_add_f32 v36, v36, v202\n"
"v_mfma_f32_16x16x32_bf16 v[132:135], v[188:191], a[104:107], v[132:135]\n"
"ds_read_b64_tr_b16 v[102:103], v235 offset:2048\n"
"v_add_f32 v38, v38, v203\n"
"v_mfma_f32_16x16x32_bf16 v[132:135], v[192:195], a[108:111], v[132:135]\n"
"ds_read_b64_tr_b16 v[106:107], v231 offset:10240\n"
"v_add_f32 v44, v44, v206\n"
"v_mfma_f32_16x16x32_bf16 v[132:135], v[208:211], a[144:147], v[132:135]\n"
"ds_read_b64_tr_b16 v[110:111], v235 offset:10240\n"
"v_add_f32 v46, v46, v207\n"
"v_mfma_f32_16x16x32_bf16 v[132:135], v[212:215], a[148:151], v[132:135]\n"
"ds_read_b64_tr_b16 v[112:113], v231 offset:16384\n"
"v_add_f32 v40, v40, v204\n"
"v_mfma_f32_16x16x32_bf16 v[132:135], v[216:219], a[152:155], v[132:135]\n"
"ds_read_b64_tr_b16 v[114:115], v231 offset:18432\n"
"v_add_f32 v42, v42, v205\n"
"v_mfma_f32_16x16x32_bf16 v[132:135], v[220:223], a[156:159], v[132:135]\n"
"buffer_store_dwordx4 v[164:167], v243, s[32:35], s17 offen offset:1024 sc0 sc1\n"
"v_lshlrev_b32 v206, 16, v244\n"
"v_mfma_f32_16x16x32_bf16 v[148:151], v[220:223], a[172:175], v[196:199]\n"
"ds_read_b64_tr_b16 v[118:119], v235 offset:18432\n"
"v_and_b32 v244, s73, v244\n"
"v_mfma_f32_16x16x32_bf16 v[148:151], v[216:219], a[168:171], v[148:151]\n"
"ds_read_b64_tr_b16 v[122:123], v231 offset:26624\n"
"v_add_f32 v49, v49, v244\n"
"v_mfma_f32_16x16x32_bf16 v[148:151], v[212:215], a[164:167], v[148:151]\n"
"ds_read_b64_tr_b16 v[226:227], v235 offset:26624\n"
"v_lshlrev_b32 v207, 16, v245\n"
"v_mfma_f32_16x16x32_bf16 v[148:151], v[208:211], a[160:163], v[148:151]\n"
"buffer_load_dwordx4 v[80:83], v243, s[32:35], s18 offen offset:2048 sc1\n"
"v_mfma_f32_16x16x32_bf16 v[148:151], v[192:195], a[124:127], v[148:151]\n"
"v_and_b32 v245, s73, v245\n"
"v_add_f32 v51, v51, v245\n"
"v_mfma_f32_16x16x32_bf16 v[148:151], v[188:191], a[120:123], v[148:151]\n"
"s_add_u32 s41, s70, 163840\n"
"v_lshlrev_b32 v202, 16, v246\n"
"v_and_b32 v246, s73, v246\n"
"v_mfma_f32_16x16x32_bf16 v[148:151], v[184:187], a[116:119], v[148:151]\n"
"s_add_u32 m0, s71, 16384\n"
"v_add_f32 v53, v53, v246\n"
"v_lshlrev_b32 v203, 16, v247\n"
"v_mfma_f32_16x16x32_bf16 v[148:151], v[180:183], a[112:115], v[148:151]\n"
"buffer_load_dwordx4 v240, s[28:31], s41 offen lds\n"
"v_mfma_f32_16x16x32_bf16 v[164:167], v[180:183], a[128:131], v[196:199]\n"
"v_and_b32 v247, s73, v247\n"
"v_add_f32 v55, v55, v247\n"
"v_mfma_f32_16x16x32_bf16 v[164:167], v[184:187], a[132:135], v[164:167]\n"
"v_add_f32 v48, v48, v206\n"
"v_add_f32 v50, v50, v207\n"
"v_mfma_f32_16x16x32_bf16 v[164:167], v[188:191], a[136:139], v[164:167]\n"
"v_add_f32 v52, v52, v202\n"
"v_add_f32 v54, v54, v203\n"
"v_mfma_f32_16x16x32_bf16 v[164:167], v[192:195], a[140:143], v[164:167]\n"
"v_mfma_f32_16x16x32_bf16 v[164:167], v[208:211], a[176:179], v[164:167]\n"
"v_mfma_f32_16x16x32_bf16 v[164:167], v[212:215], a[180:183], v[164:167]\n"
"v_mfma_f32_16x16x32_bf16 v[164:167], v[216:219], a[184:187], v[164:167]\n"
"s_waitcnt vmcnt(12)\n"
"v_mfma_f32_16x16x32_bf16 v[164:167], v[220:223], a[188:191], v[164:167]\n"
"s_barrier\n"
"s_waitcnt lgkmcnt(0)\n"
"v_mfma_f32_16x16x32_bf16 v[136:139], v[96:99], a[96:99], v[196:199]\n"
"ds_read_b64_tr_b16 v[180:181], v228 offset:32768\n"
"v_mfma_f32_16x16x32_bf16 v[136:139], v[100:103], a[100:103], v[136:139]\n"
"ds_read_b64_tr_b16 v[182:183], v228 offset:34816\n"
"v_mfma_f32_16x16x32_bf16 v[136:139], v[104:107], a[104:107], v[136:139]\n"
"ds_read_b64_tr_b16 v[184:185], v232 offset:32768\n"
"v_mfma_f32_16x16x32_bf16 v[136:139], v[108:111], a[108:111], v[136:139]\n"
"ds_read_b64_tr_b16 v[186:187], v232 offset:34816\n"
"v_mfma_f32_16x16x32_bf16 v[136:139], v[112:115], a[144:147], v[136:139]\n"
"ds_read_b64_tr_b16 v[188:189], v228 offset:40960\n"
"v_mfma_f32_16x16x32_bf16 v[136:139], v[116:119], a[148:151], v[136:139]\n"
"ds_read_b64_tr_b16 v[190:191], v228 offset:43008\n"
"v_mfma_f32_16x16x32_bf16 v[136:139], v[120:123], a[152:155], v[136:139]\n"
"s_add_u32 s40, s41, s52\n"
"ds_read_b64_tr_b16 v[192:193], v232 offset:40960\n"
"v_mfma_f32_16x16x32_bf16 v[136:139], v[224:227], a[156:159], v[136:139]\n"
"buffer_load_dwordx4 v240, s[28:31], s40 offen offset:1024 lds\n"
"v_mfma_f32_16x16x32_bf16 v[152:155], v[224:227], a[172:175], v[196:199]\n"
"ds_read_b64_tr_b16 v[194:195], v232 offset:43008\n"
"v_mfma_f32_16x16x32_bf16 v[152:155], v[120:123], a[168:171], v[152:155]\n"
"ds_read_b64_tr_b16 v[208:209], v228 offset:49152\n"
"v_mfma_f32_16x16x32_bf16 v[152:155], v[116:119], a[164:167], v[152:155]\n"
"ds_read_b64_tr_b16 v[210:211], v228 offset:51200\n"
"v_mfma_f32_16x16x32_bf16 v[152:155], v[112:115], a[160:163], v[152:155]\n"
"ds_read_b64_tr_b16 v[212:213], v232 offset:49152\n"
"v_mfma_f32_16x16x32_bf16 v[152:155], v[108:111], a[124:127], v[152:155]\n"
"ds_read_b64_tr_b16 v[214:215], v232 offset:51200\n"
"v_mfma_f32_16x16x32_bf16 v[152:155], v[104:107], a[120:123], v[152:155]\n"
"ds_read_b64_tr_b16 v[216:217], v228 offset:57344\n"
"v_mfma_f32_16x16x32_bf16 v[152:155], v[100:103], a[116:119], v[152:155]\n"
"s_add_u32 s40, s41, s53\n"
"ds_read_b64_tr_b16 v[218:219], v228 offset:59392\n"
"v_xor_b32 v228, s75, v228\n"
"v_mfma_f32_16x16x32_bf16 v[152:155], v[96:99], a[112:115], v[152:155]\n"
"buffer_load_dwordx4 v240, s[28:31], s40 offen offset:2048 lds\n"
"v_mfma_f32_16x16x32_bf16 v[168:171], v[96:99], a[128:131], v[196:199]\n"
"ds_read_b64_tr_b16 v[220:221], v232 offset:57344\n"
"v_mfma_f32_16x16x32_bf16 v[168:171], v[100:103], a[132:135], v[168:171]\n"
"ds_read_b64_tr_b16 v[222:223], v232 offset:59392\n"
"v_xor_b32 v232, s75, v232\n"
"v_mfma_f32_16x16x32_bf16 v[168:171], v[104:107], a[136:139], v[168:171]\n"
"ds_read_b64_tr_b16 v[96:97], v229 offset:32768\n"
"v_mfma_f32_16x16x32_bf16 v[168:171], v[108:111], a[140:143], v[168:171]\n"
"ds_read_b64_tr_b16 v[98:99], v229 offset:34816\n"
"v_mfma_f32_16x16x32_bf16 v[168:171], v[112:115], a[176:179], v[168:171]\n"
"ds_read_b64_tr_b16 v[100:101], v233 offset:32768\n"
"v_mfma_f32_16x16x32_bf16 v[168:171], v[116:119], a[180:183], v[168:171]\n"
"ds_read_b64_tr_b16 v[102:103], v233 offset:34816\n"
"v_mfma_f32_16x16x32_bf16 v[168:171], v[120:123], a[184:187], v[168:171]\n"
"s_add_u32 s40, s41, s54\n"
"ds_read_b64_tr_b16 v[104:105], v229 offset:40960\n"
"v_mfma_f32_16x16x32_bf16 v[168:171], v[224:227], a[188:191], v[168:171]\n"
"s_waitcnt lgkmcnt(7)\n"
"buffer_load_dwordx4 v240, s[28:31], s40 offen offset:3072 lds\n"
"v_mfma_f32_16x16x32_bf16 v[124:127], v[180:183], a[48:51], v[124:127]\n"
"ds_read_b64_tr_b16 v[106:107], v229 offset:43008\n"
"v_mfma_f32_16x16x32_bf16 v[124:127], v[184:187], a[52:55], v[124:127]\n"
"ds_read_b64_tr_b16 v[108:109], v233 offset:40960\n"
"v_mfma_f32_16x16x32_bf16 v[124:127], v[188:191], a[56:59], v[124:127]\n"
"ds_read_b64_tr_b16 v[110:111], v233 offset:43008\n"
"v_mfma_f32_16x16x32_bf16 v[124:127], v[192:195], a[60:63], v[124:127]\n"
"ds_read_b64_tr_b16 v[112:113], v229 offset:49152\n"
"v_mfma_f32_16x16x32_bf16 v[124:127], v[208:211], a[0:3], v[124:127]\n"
"ds_read_b64_tr_b16 v[114:115], v229 offset:51200\n"
"v_mfma_f32_16x16x32_bf16 v[124:127], v[212:215], a[4:7], v[124:127]\n"
"s_add_u32 s41, s70, 327680\n"
"ds_read_b64_tr_b16 v[116:117], v233 offset:49152\n"
"v_mfma_f32_16x16x32_bf16 v[124:127], v[216:219], a[8:11], v[124:127]\n"
"s_waitcnt lgkmcnt(6)\n"
"s_add_u32 m0, s71, 32768\n"
"ds_read_b64_tr_b16 v[118:119], v233 offset:51200\n"
"v_mfma_f32_16x16x32_bf16 v[124:127], v[220:223], a[12:15], v[124:127]\n"
"buffer_load_dwordx4 v240, s[28:31], s41 offen lds\n"
"v_mfma_f32_16x16x32_bf16 v[140:143], v[220:223], a[28:31], v[140:143]\n"
"ds_read_b64_tr_b16 v[120:121], v229 offset:57344\n"
"v_mfma_f32_16x16x32_bf16 v[140:143], v[216:219], a[24:27], v[140:143]\n"
"ds_read_b64_tr_b16 v[122:123], v229 offset:59392\n"
"v_xor_b32 v229, s75, v229\n"
"v_mfma_f32_16x16x32_bf16 v[140:143], v[212:215], a[20:23], v[140:143]\n"
"ds_read_b64_tr_b16 v[224:225], v233 offset:57344\n"
"v_mfma_f32_16x16x32_bf16 v[140:143], v[208:211], a[16:19], v[140:143]\n"
"ds_read_b64_tr_b16 v[226:227], v233 offset:59392\n"
"v_xor_b32 v233, s75, v233\n"
"v_mfma_f32_16x16x32_bf16 v[140:143], v[192:195], a[76:79], v[140:143]\n"
"v_mfma_f32_16x16x32_bf16 v[140:143], v[188:191], a[72:75], v[140:143]\n"
"v_subrev_f32 v124, s22, v124\n"
"v_subrev_f32 v125, s22, v125\n"
"v_mfma_f32_16x16x32_bf16 v[140:143], v[184:187], a[68:71], v[140:143]\n"
"s_add_u32 s40, s41, s52\n"
"v_subrev_f32 v126, s22, v126\n"
"v_subrev_f32 v127, s22, v127\n"
"v_mfma_f32_16x16x32_bf16 v[140:143], v[180:183], a[64:67], v[140:143]\n"
"buffer_load_dwordx4 v240, s[28:31], s40 offen offset:1024 lds\n"
"v_mfma_f32_16x16x32_bf16 v[156:159], v[180:183], a[80:83], v[156:159]\n"
"v_cvt_pk_bf16_f32 v124, v124, v125\n"
"v_cvt_pk_bf16_f32 v125, v126, v127\n"
"v_mfma_f32_16x16x32_bf16 v[156:159], v[184:187], a[84:87], v[156:159]\n"
"v_mfma_f32_16x16x32_bf16 v[156:159], v[188:191], a[88:91], v[156:159]\n"
"v_mfma_f32_16x16x32_bf16 v[156:159], v[192:195], a[92:95], v[156:159]\n"
"v_mfma_f32_16x16x32_bf16 v[156:159], v[208:211], a[32:35], v[156:159]\n"
"v_mfma_f32_16x16x32_bf16 v[156:159], v[212:215], a[36:39], v[156:159]\n"
"v_subrev_f32 v140, s22, v140\n"
"v_subrev_f32 v141, s22, v141\n"
"v_mfma_f32_16x16x32_bf16 v[156:159], v[216:219], a[40:43], v[156:159]\n"
"s_waitcnt vmcnt(7)\n"
"v_subrev_f32 v142, s22, v142\n"
"v_subrev_f32 v143, s22, v143\n"
"v_mfma_f32_16x16x32_bf16 v[156:159], v[220:223], a[44:47], v[156:159]\n"
"s_barrier\n"
"v_mfma_f32_16x16x32_bf16 v[172:175], v[220:223], a[228:231], v[172:175]\n"
"ds_read_b64_tr_b16 v[220:221], v234 offset:57344\n"
"s_cmp_eq_u32 s42, 0\n"
"v_mfma_f32_16x16x32_bf16 v[172:175], v[216:219], a[224:227], v[172:175]\n"
"s_cbranch_scc1 LGS_%=\nLGSK_%=:\n"
"ds_read_b64_tr_b16 v[216:217], v230 offset:57344\n"
"v_mfma_f32_16x16x32_bf16 v[172:175], v[212:215], a[212:215], v[172:175]\n"
"ds_read_b64_tr_b16 v[212:213], v234 offset:49152\n"
"v_cvt_pk_bf16_f32 v140, v140, v141\n"
"v_mfma_f32_16x16x32_bf16 v[172:175], v[208:211], a[208:211], v[172:175]\n"
"s_mul_i32 s16, s61, 286720\n"
"buffer_load_dwordx4 v[84:87], v243, s[32:35], s18 offen offset:3072 sc1\n"
"v_mfma_f32_16x16x32_bf16 v[172:175], v[192:195], a[236:239], v[172:175]\n"
"s_add_u32 s16, s16, s62\n"
"ds_read_b64_tr_b16 v[192:193], v234 offset:40960\n"
"v_cvt_pk_bf16_f32 v141, v142, v143\n"
"v_mfma_f32_16x16x32_bf16 v[172:175], v[188:191], a[232:235], v[172:175]\n"
"s_add_u32 s17, s16, 4096\n"
"ds_read_b64_tr_b16 v[188:189], v230 offset:40960\n"
"v_subrev_f32 v156, s22, v156\n"
"v_mfma_f32_16x16x32_bf16 v[172:175], v[184:187], a[220:223], v[172:175]\n"
"s_add_u32 s40, s41, s53\n"
"s_mul_i32 s62, s70, 224\n"
"ds_read_b64_tr_b16 v[184:185], v234 offset:32768\n"
"v_mfma_f32_16x16x32_bf16 v[172:175], v[180:183], a[216:219], v[172:175]\n"
"buffer_load_dwordx4 v240, s[28:31], s40 offen offset:2048 lds\n"
"v_mfma_f32_16x16x32_bf16 v[128:131], v[96:99], a[48:51], v[128:131]\n"
"ds_read_b64_tr_b16 v[180:181], v230 offset:32768\n"
"v_subrev_f32 v157, s22, v157\n"
"v_mfma_f32_16x16x32_bf16 v[128:131], v[100:103], a[52:55], v[128:131]\n"
"s_waitcnt lgkmcnt(5)\n"
"ds_read_b64_tr_b16 v[182:183], v230 offset:34816\n"
"v_subrev_f32 v158, s22, v158\n"
"v_mfma_f32_16x16x32_bf16 v[128:131], v[104:107], a[56:59], v[128:131]\n"
"ds_read_b64_tr_b16 v[186:187], v234 offset:34816\n"
"v_subrev_f32 v159, s22, v159\n"
"v_mfma_f32_16x16x32_bf16 v[128:131], v[108:111], a[60:63], v[128:131]\n"
"ds_read_b64_tr_b16 v[190:191], v230 offset:43008\n"
"v_cvt_pk_bf16_f32 v156, v156, v157\n"
"v_mfma_f32_16x16x32_bf16 v[128:131], v[112:115], a[0:3], v[128:131]\n"
"ds_read_b64_tr_b16 v[194:195], v234 offset:43008\n"
"v_cvt_pk_bf16_f32 v157, v158, v159\n"
"v_mfma_f32_16x16x32_bf16 v[128:131], v[116:119], a[4:7], v[128:131]\n"
"ds_read_b64_tr_b16 v[208:209], v230 offset:49152\n"
"v_lshlrev_b32 v202, 16, v72\n"
"v_mfma_f32_16x16x32_bf16 v[128:131], v[120:123], a[8:11], v[128:131]\n"
"s_add_u32 s40, s41, s54\n"
"ds_read_b64_tr_b16 v[210:211], v230 offset:51200\n"
"v_and_b32 v72, s73, v72\n"
"v_mfma_f32_16x16x32_bf16 v[128:131], v[224:227], a[12:15], v[128:131]\n"
"buffer_load_dwordx4 v240, s[28:31], s40 offen offset:3072 lds\n"
"v_mfma_f32_16x16x32_bf16 v[144:147], v[224:227], a[28:31], v[144:147]\n"
"ds_read_b64_tr_b16 v[214:215], v234 offset:51200\n"
"v_add_f32 v0, v0, v202\n"
"v_mfma_f32_16x16x32_bf16 v[144:147], v[120:123], a[24:27], v[144:147]\n"
"ds_read_b64_tr_b16 v[218:219], v230 offset:59392\n"
"v_add_f32 v1, v1, v72\n"
"v_mfma_f32_16x16x32_bf16 v[144:147], v[116:119], a[20:23], v[144:147]\n"
"ds_read_b64_tr_b16 v[222:223], v234 offset:59392\n"
"v_lshlrev_b32 v203, 16, v73\n"
"v_mfma_f32_16x16x32_bf16 v[144:147], v[112:115], a[16:19], v[144:147]\n"
"buffer_load_dwordx4 v[88:91], v243, s[32:35], s19 offen sc1\n"
"v_mfma_f32_16x16x32_bf16 v[144:147], v[108:111], a[76:79], v[144:147]\n"
"v_and_b32 v73, s73, v73\n"
"v_add_f32 v2, v2, v203\n"
"v_mfma_f32_16x16x32_bf16 v[144:147], v[104:107], a[72:75], v[144:147]\n"
"s_add_u32 s41, s70, 491520\n"
"v_subrev_f32 v128, s22, v128\n"
"v_subrev_f32 v129, s22, v129\n"
"v_mfma_f32_16x16x32_bf16 v[144:147], v[100:103], a[68:71], v[144:147]\n"
"s_add_u32 m0, s71, 49152\n"
"v_subrev_f32 v130, s22, v130\n"
"v_subrev_f32 v131, s22, v131\n"
"v_mfma_f32_16x16x32_bf16 v[144:147], v[96:99], a[64:67], v[144:147]\n"
"buffer_load_dwordx4 v240, s[28:31], s41 offen lds\n"
"v_mfma_f32_16x16x32_bf16 v[160:163], v[96:99], a[80:83], v[160:163]\n"
"s_add_u32 s20, s47, 3\n"
"v_cvt_pk_bf16_f32 v126, v128, v129\n"
"v_cvt_pk_bf16_f32 v127, v130, v131\n"
"v_mfma_f32_16x16x32_bf16 v[160:163], v[100:103], a[84:87], v[160:163]\n"
"s_add_u32 s70, s61, s20\n"
"v_add_f32 v3, v3, v73\n"
"v_lshlrev_b32 v204, 16, v74\n"
"v_mfma_f32_16x16x32_bf16 v[160:163], v[104:107], a[88:91], v[160:163]\n"
"s_and_b32 s70, s70, 7\n"
"v_and_b32 v74, s73, v74\n"
"v_add_f32 v4, v4, v204\n"
"v_mfma_f32_16x16x32_bf16 v[160:163], v[108:111], a[92:95], v[160:163]\n"
"s_cmp_ge_u32 s47, 5\ns_cselect_b32 s70, s20, s70\n"
"v_add_f32 v5, v5, v74\n"
"v_lshlrev_b32 v205, 16, v75\n"
"v_mfma_f32_16x16x32_bf16 v[160:163], v[112:115], a[32:35], v[160:163]\n"
"s_cmp_eq_u32 s47, 7\ns_cselect_b32 s70, s61, s70\n"
"v_and_b32 v75, s73, v75\n"
"v_add_f32 v6, v6, v205\n"
"v_mfma_f32_16x16x32_bf16 v[160:163], v[116:119], a[36:39], v[160:163]\n"
"s_lshl_b32 s70, s70, 7\n"
"v_subrev_f32 v144, s22, v144\n"
"v_subrev_f32 v145, s22, v145\n"
"v_mfma_f32_16x16x32_bf16 v[160:163], v[120:123], a[40:43], v[160:163]\n"
"s_xor_b32 s71, s71, s75\n"
"v_subrev_f32 v146, s22, v146\n"
"v_subrev_f32 v147, s22, v147\n"
"v_mfma_f32_16x16x32_bf16 v[160:163], v[224:227], a[44:47], v[160:163]\n"
"buffer_store_dwordx4 v[124:127], v243, s[32:35], s16 offen sc0 sc1\n"
"v_cvt_pk_bf16_f32 v142, v144, v145\n"
"v_mfma_f32_16x16x32_bf16 v[176:179], v[224:227], a[228:231], v[176:179]\n"
"ds_read_b64_tr_b16 v[224:225], v235 offset:57344\n"
"v_cvt_pk_bf16_f32 v143, v146, v147\n"
"v_mfma_f32_16x16x32_bf16 v[176:179], v[120:123], a[224:227], v[176:179]\n"
"ds_read_b64_tr_b16 v[120:121], v231 offset:57344\n"
"v_add_f32 v7, v7, v75\n"
"v_mfma_f32_16x16x32_bf16 v[176:179], v[116:119], a[212:215], v[176:179]\n"
"ds_read_b64_tr_b16 v[116:117], v235 offset:49152\n"
"v_lshlrev_b32 v206, 16, v76\n"
"v_mfma_f32_16x16x32_bf16 v[176:179], v[112:115], a[208:211], v[176:179]\n"
"buffer_load_dwordx4 v[92:95], v243, s[32:35], s19 offen offset:1024 sc1\n"
"v_mfma_f32_16x16x32_bf16 v[176:179], v[108:111], a[236:239], v[176:179]\n"
"ds_read_b64_tr_b16 v[108:109], v235 offset:40960\n"
"v_and_b32 v76, s73, v76\n"
"v_mfma_f32_16x16x32_bf16 v[176:179], v[104:107], a[232:235], v[176:179]\n"
"ds_read_b64_tr_b16 v[104:105], v231 offset:40960\n"
"v_subrev_f32 v160, s22, v160\n"
"v_mfma_f32_16x16x32_bf16 v[176:179], v[100:103], a[220:223], v[176:179]\n"
"s_add_u32 s40, s41, s52\n"
"ds_read_b64_tr_b16 v[100:101], v235 offset:32768\n"
"v_subrev_f32 v161, s22, v161\n"
"v_mfma_f32_16x16x32_bf16 v[176:179], v[96:99], a[216:219], v[176:179]\n"
"s_waitcnt vmcnt(13) lgkmcnt(6)\n"
"buffer_load_dwordx4 v240, s[28:31], s40 offen offset:1024 lds\n"
"v_mfma_f32_16x16x32_bf16 v[132:135], v[180:183], a[48:51], v[132:135]\n"
"ds_read_b64_tr_b16 v[96:97], v231 offset:32768\n"
"v_subrev_f32 v162, s22, v162\n"
"v_mfma_f32_16x16x32_bf16 v[132:135], v[184:187], a[52:55], v[132:135]\n"
"ds_read_b64_tr_b16 v[98:99], v231 offset:34816\n"
"v_subrev_f32 v163, s22, v163\n"
"v_mfma_f32_16x16x32_bf16 v[132:135], v[188:191], a[56:59], v[132:135]\n"
"ds_read_b64_tr_b16 v[102:103], v235 offset:34816\n"
"v_cvt_pk_bf16_f32 v158, v160, v161\n"
"v_mfma_f32_16x16x32_bf16 v[132:135], v[192:195], a[60:63], v[132:135]\n"
"ds_read_b64_tr_b16 v[106:107], v231 offset:43008\n"
"v_cvt_pk_bf16_f32 v159, v162, v163\n"
"v_mfma_f32_16x16x32_bf16 v[132:135], v[208:211], a[0:3], v[132:135]\n"
"ds_read_b64_tr_b16 v[110:111], v235 offset:43008\n"
"v_add_f32 v8, v8, v206\n"
"v_mfma_f32_16x16x32_bf16 v[132:135], v[212:215], a[4:7], v[132:135]\n"
"ds_read_b64_tr_b16 v[112:113], v231 offset:49152\n"
"v_add_f32 v9, v9, v76\n"
"v_mfma_f32_16x16x32_bf16 v[132:135], v[216:219], a[8:11], v[132:135]\n"
"ds_read_b64_tr_b16 v[114:115], v231 offset:51200\n"
"v_lshlrev_b32 v207, 16, v77\n"
"v_mfma_f32_16x16x32_bf16 v[132:135], v[220:223], a[12:15], v[132:135]\n"
"buffer_store_dwordx4 v[140:143], v243, s[32:35], s16 offen offset:2048 sc0 sc1\n"
"v_and_b32 v77, s73, v77\n"
"v_mfma_f32_16x16x32_bf16 v[148:151], v[220:223], a[28:31], v[148:151]\n"
"ds_read_b64_tr_b16 v[118:119], v235 offset:51200\n"
"v_add_f32 v10, v10, v207\n"
"v_mfma_f32_16x16x32_bf16 v[148:151], v[216:219], a[24:27], v[148:151]\n"
"ds_read_b64_tr_b16 v[122:123], v231 offset:59392\n"
"v_add_f32 v11, v11, v77\n"
"v_mfma_f32_16x16x32_bf16 v[148:151], v[212:215], a[20:23], v[148:151]\n"
"ds_read_b64_tr_b16 v[226:227], v235 offset:59392\n"
"v_lshlrev_b32 v202, 16, v78\n"
"v_mfma_f32_16x16x32_bf16 v[148:151], v[208:211], a[16:19], v[148:151]\n"
"v_and_b32 v78, s73, v78\n"
"v_add_f32 v12, v12, v202\n"
"v_mfma_f32_16x16x32_bf16 v[148:151], v[192:195], a[76:79], v[148:151]\n"
"v_add_f32 v13, v13, v78\n"
"v_lshlrev_b32 v203, 16, v79\n"
"v_mfma_f32_16x16x32_bf16 v[148:151], v[188:191], a[72:75], v[148:151]\n"
"v_and_b32 v79, s73, v79\n"
"v_add_f32 v14, v14, v203\n"
"v_mfma_f32_16x16x32_bf16 v[148:151], v[184:187], a[68:71], v[148:151]\n"
"s_add_u32 s40, s41, s53\n"
"v_add_f32 v15, v15, v79\n"
"v_lshlrev_b32 v204, 16, v80\n"
"v_mfma_f32_16x16x32_bf16 v[148:151], v[180:183], a[64:67], v[148:151]\n"
"buffer_load_dwordx4 v240, s[28:31], s40 offen offset:2048 lds\n"
"v_mfma_f32_16x16x32_bf16 v[164:167], v[180:183], a[80:83], v[164:167]\n"
"v_and_b32 v80, s73, v80\n"
"v_add_f32 v16, v16, v204\n"
"v_mfma_f32_16x16x32_bf16 v[164:167], v[184:187], a[84:87], v[164:167]\n"
"v_add_f32 v17, v17, v80\n"
"v_lshlrev_b32 v205, 16, v81\n"
"v_mfma_f32_16x16x32_bf16 v[164:167], v[188:191], a[88:91], v[164:167]\n"
"v_and_b32 v81, s73, v81\n"
"v_add_f32 v18, v18, v205\n"
"v_mfma_f32_16x16x32_bf16 v[164:167], v[192:195], a[92:95], v[164:167]\n"
"buffer_load_dwordx4 v[244:247], v243, s[32:35], s19 offen offset:2048 sc1\n"
"v_mfma_f32_16x16x32_bf16 v[164:167], v[208:211], a[32:35], v[164:167]\n"
"s_mul_i32 s18, s72, 10\n"
"v_add_f32 v19, v19, v81\n"
"v_lshlrev_b32 v206, 16, v82\n"
"v_mfma_f32_16x16x32_bf16 v[164:167], v[212:215], a[36:39], v[164:167]\n"
"s_add_u32 s18, s18, s61\n"
"v_and_b32 v82, s73, v82\n"
"v_add_f32 v20, v20, v206\n"
"v_mfma_f32_16x16x32_bf16 v[164:167], v[216:219], a[40:43], v[164:167]\n"
"s_waitcnt vmcnt(13)\n"
"s_mul_i32 s18, s18, 28672\n"
"v_add_f32 v21, v21, v82\n"
"v_mfma_f32_16x16x32_bf16 v[164:167], v[220:223], a[44:47], v[164:167]\n"
"s_barrier\n"
"s_add_u32 s19, s18, 4096\n"
"s_waitcnt lgkmcnt(0)\n"
"v_mfma_f32_16x16x32_bf16 v[136:139], v[96:99], a[48:51], v[136:139]\n"
"s_mov_b32 s69, 15\n"
"ds_read_b64_tr_b16 v[180:181], v228 offset:0\n"
"v_lshlrev_b32 v207, 16, v83\n"
"v_mfma_f32_16x16x32_bf16 v[136:139], v[100:103], a[52:55], v[136:139]\n"
"s_add_u32 s21, s21, 1\n"
"ds_read_b64_tr_b16 v[182:183], v228 offset:2048\n"
"v_and_b32 v83, s73, v83\n"
"v_mfma_f32_16x16x32_bf16 v[136:139], v[104:107], a[56:59], v[136:139]\n"
"v_lshlrev_b32_e64 v201, 2, s72\n"
"ds_read_b64_tr_b16 v[184:185], v232 offset:0\n"
"v_add_f32 v22, v22, v207\n"
"v_mfma_f32_16x16x32_bf16 v[136:139], v[108:111], a[60:63], v[136:139]\n"
"global_load_dword v200, v201, s[6:7] sc1\n"
"v_mfma_f32_16x16x32_bf16 v[136:139], v[112:115], a[0:3], v[136:139]\n"
"ds_read_b64_tr_b16 v[186:187], v232 offset:2048\n"
"v_add_f32 v23, v23, v83\n"
"v_mfma_f32_16x16x32_bf16 v[136:139], v[116:119], a[4:7], v[136:139]\n"
"ds_read_b64_tr_b16 v[188:189], v228 offset:8192\n"
"v_subrev_f32 v172, s22, v172\n"
"v_mfma_f32_16x16x32_bf16 v[136:139], v[120:123], a[8:11], v[136:139]\n"
"ds_read_b64_tr_b16 v[190:191], v228 offset:10240\n"
"v_subrev_f32 v173, s22, v173\n"
"v_mfma_f32_16x16x32_bf16 v[136:139], v[224:227], a[12:15], v[136:139]\n"
"buffer_store_dwordx4 v[156:159], v243, s[32:35], s17 offen sc0 sc1\n"
"v_subrev_f32 v174, s22, v174\n"
"v_mfma_f32_16x16x32_bf16 v[152:155], v[224:227], a[28:31], v[152:155]\n"
"ds_read_b64_tr_b16 v[192:193], v232 offset:8192\n"
"v_subrev_f32 v175, s22, v175\n"
"v_mfma_f32_16x16x32_bf16 v[152:155], v[120:123], a[24:27], v[152:155]\n"
"ds_read_b64_tr_b16 v[194:195], v232 offset:10240\n"
"v_cvt_pk_bf16_f32 v172, v172, v173\n"
"v_mfma_f32_16x16x32_bf16 v[152:155], v[116:119], a[20:23], v[152:155]\n"
"ds_read_b64_tr_b16 v[208:209], v228 offset:16384\n"
"v_cvt_pk_bf16_f32 v173, v174, v175\n"
"v_mfma_f32_16x16x32_bf16 v[152:155], v[112:115], a[16:19], v[152:155]\n"
"ds_read_b64_tr_b16 v[210:211], v228 offset:18432\n"
"v_subrev_f32 v176, s22, v176\n"
"v_mfma_f32_16x16x32_bf16 v[152:155], v[108:111], a[76:79], v[152:155]\n"
"ds_read_b64_tr_b16 v[212:213], v232 offset:16384\n"
"v_subrev_f32 v177, s22, v177\n"
"v_mfma_f32_16x16x32_bf16 v[152:155], v[104:107], a[72:75], v[152:155]\n"
"ds_read_b64_tr_b16 v[214:215], v232 offset:18432\n"
"v_subrev_f32 v178, s22, v178\n"
"v_mfma_f32_16x16x32_bf16 v[152:155], v[100:103], a[68:71], v[152:155]\n"
"s_add_u32 s40, s41, s54\n"
"ds_read_b64_tr_b16 v[216:217], v228 offset:24576\n"
"v_subrev_f32 v179, s22, v179\n"
"v_mfma_f32_16x16x32_bf16 v[152:155], v[96:99], a[64:67], v[152:155]\n"
"buffer_load_dwordx4 v240, s[28:31], s40 offen offset:3072 lds\n"
"v_mfma_f32_16x16x32_bf16 v[168:171], v[96:99], a[80:83], v[168:171]\n"
"ds_read_b64_tr_b16 v[218:219], v228 offset:26624\n"
"v_cvt_pk_bf16_f32 v174, v176, v177\n"
"v_mfma_f32_16x16x32_bf16 v[168:171], v[100:103], a[84:87], v[168:171]\n"
"ds_read_b64_tr_b16 v[220:221], v232 offset:24576\n"
"v_cvt_pk_bf16_f32 v175, v178, v179\n"
"v_mfma_f32_16x16x32_bf16 v[168:171], v[104:107], a[88:91], v[168:171]\n"
"ds_read_b64_tr_b16 v[222:223], v232 offset:26624\n"
"v_subrev_f32 v132, s22, v132\n"
"v_mfma_f32_16x16x32_bf16 v[168:171], v[108:111], a[92:95], v[168:171]\n"
"ds_read_b64_tr_b16 v[96:97], v229 offset:0\n"
"v_subrev_f32 v133, s22, v133\n"
"v_mfma_f32_16x16x32_bf16 v[168:171], v[112:115], a[32:35], v[168:171]\n"
"ds_read_b64_tr_b16 v[98:99], v229 offset:2048\n"
"v_subrev_f32 v134, s22, v134\n"
"v_mfma_f32_16x16x32_bf16 v[168:171], v[116:119], a[36:39], v[168:171]\n"
"ds_read_b64_tr_b16 v[100:101], v233 offset:0\n"
"v_subrev_f32 v135, s22, v135\n"
"v_mfma_f32_16x16x32_bf16 v[168:171], v[120:123], a[40:43], v[168:171]\n"
"s_add_u32 s47, s47, 1\n"
"ds_read_b64_tr_b16 v[102:103], v233 offset:2048\n"
"v_cvt_pk_bf16_f32 v132, v132, v133\n"
"v_mfma_f32_16x16x32_bf16 v[168:171], v[224:227], a[44:47], v[168:171]\n"
"s_waitcnt lgkmcnt(7)\n"
"s_cmp_lg_u32 s47, 9\n"
"ds_read_b64_tr_b16 v[104:105], v229 offset:8192\n"
"s_cbranch_scc1 LGL_%=\n"
"v_mfma_f32_16x16x32_bf16 v[0:3], v[180:183], a[96:99], v[0:3]\n"
"v_subrev_f32 v136, s22, v136\n"
"v_subrev_f32 v139, s22, v139\n"
"v_mfma_f32_16x16x32_bf16 v[0:3], v[184:187], a[100:103], v[0:3]\n"
"v_cvt_pk_bf16_f32 v133, v134, v135\n"
"v_subrev_f32 v137, s22, v137\n"
"v_mfma_f32_16x16x32_bf16 v[0:3], v[188:191], a[104:107], v[0:3]\n"
"v_mov_b32 v201, v237\n"
"v_subrev_f32 v148, s22, v148\n"
"v_subrev_f32 v165, s22, v165\n"
"v_mfma_f32_16x16x32_bf16 v[0:3], v[192:195], a[108:111], v[0:3]\n"
"global_load_dword v200, v201, s[6:7] sc1\n"
"v_mfma_f32_16x16x32_bf16 v[0:3], v[208:211], a[144:147], v[0:3]\n"
"v_subrev_f32 v138, s22, v138\n"
"v_subrev_f32 v171, s22, v171\n"
"v_mfma_f32_16x16x32_bf16 v[0:3], v[212:215], a[148:151], v[0:3]\n"
"s_waitcnt lgkmcnt(2)\n"
"v_subrev_f32 v164, s22, v164\n"
"v_cvt_pk_bf16_f32 v134, v136, v137\n"
"v_mfma_f32_16x16x32_bf16 v[0:3], v[216:219], a[152:155], v[0:3]\n"
"v_subrev_f32 v150, s22, v150\n"
"v_cvt_pk_bf16_f32 v135, v138, v139\n"
"v_mfma_f32_16x16x32_bf16 v[0:3], v[220:223], a[156:159], v[0:3]\n"
"buffer_store_dwordx4 v[172:175], v243, s[32:35], s17 offen offset:2048 sc0 sc1\n"
"v_subrev_f32 v151, s22, v151\n"
"v_subrev_f32 v152, s22, v152\n"
"v_subrev_f32 v153, s22, v153\n"
"v_mfma_f32_16x16x32_bf16 v[16:19], v[220:223], a[172:175], v[16:19]\n"
"buffer_store_dwordx4 v[132:135], v243, s[32:35], s16 offen offset:1024 sc0 sc1\n"
"v_subrev_f32 v166, s22, v166\n"
"v_subrev_f32 v167, s22, v167\n"
"v_subrev_f32 v155, s22, v155\n"
"v_mfma_f32_16x16x32_bf16 v[16:19], v[216:219], a[168:171], v[16:19]\n"
"v_subrev_f32 v168, s22, v168\n"
"v_subrev_f32 v170, s22, v170\n"
"v_mfma_f32_16x16x32_bf16 v[16:19], v[212:215], a[164:167], v[16:19]\n"
"v_cvt_pk_bf16_f32 v164, v164, v165\n"
"v_cvt_pk_bf16_f32 v165, v166, v167\n"
"v_mfma_f32_16x16x32_bf16 v[16:19], v[208:211], a[160:163], v[16:19]\n"
"v_subrev_f32 v149, s22, v149\n"
"v_cvt_pk_bf16_f32 v148, v148, v149\n"
"v_mfma_f32_16x16x32_bf16 v[16:19], v[192:195], a[124:127], v[16:19]\n"
"v_subrev_f32 v169, s22, v169\n"
"v_cvt_pk_bf16_f32 v167, v170, v171\n"
"v_mfma_f32_16x16x32_bf16 v[16:19], v[188:191], a[120:123], v[16:19]\n"
"v_cvt_pk_bf16_f32 v149, v150, v151\n"
"v_cvt_pk_bf16_f32 v150, v152, v153\n"
"v_mfma_f32_16x16x32_bf16 v[16:19], v[184:187], a[116:119], v[16:19]\n"
"v_subrev_f32 v154, s22, v154\n"
"v_cvt_pk_bf16_f32 v151, v154, v155\n"
"v_mfma_f32_16x16x32_bf16 v[16:19], v[180:183], a[112:115], v[16:19]\n"
"s_waitcnt vmcnt(16)\n"
"buffer_store_dwordx4 v[148:151], v243, s[32:35], s16 offen offset:3072 sc0 sc1\n"
"v_cvt_pk_bf16_f32 v166, v168, v169\n"
"v_mfma_f32_16x16x32_bf16 v[32:35], v[180:183], a[128:131], v[32:35]\n"
"buffer_store_dwordx4 v[164:167], v243, s[32:35], s17 offen offset:1024 sc0 sc1\n"
"ds_read_b64_tr_b16 v[122:123], v229 offset:26624\n"
"v_mfma_f32_16x16x32_bf16 v[32:35], v[184:187], a[132:135], v[32:35]\n"
"ds_read_b64_tr_b16 v[226:227], v233 offset:26624\n"
"v_lshlrev_b32 v203, 16, v85\n"
"v_mfma_f32_16x16x32_bf16 v[32:35], v[188:191], a[136:139], v[32:35]\n"
"ds_read_b64_tr_b16 v[106:107], v229 offset:10240\n"
"v_xor_b32 v230, s75, v230\n"
"v_mfma_f32_16x16x32_bf16 v[32:35], v[192:195], a[140:143], v[32:35]\n"
"ds_read_b64_tr_b16 v[120:121], v229 offset:24576\n"
"v_lshlrev_b32 v205, 16, v87\n"
"v_mfma_f32_16x16x32_bf16 v[32:35], v[208:211], a[176:179], v[32:35]\n"
"ds_read_b64_tr_b16 v[108:109], v233 offset:8192\n"
"v_lshlrev_b32 v202, 16, v84\n"
"v_mfma_f32_16x16x32_bf16 v[32:35], v[212:215], a[180:183], v[32:35]\n"
"ds_read_b64_tr_b16 v[110:111], v233 offset:10240\n"
"v_xor_b32 v231, s75, v231\n"
"v_mfma_f32_16x16x32_bf16 v[32:35], v[216:219], a[184:187], v[32:35]\n"
"v_lshlrev_b32 v204, 16, v86\n"
"v_bfe_u32 v160, v242, 6, 2\n"
"v_mfma_f32_16x16x32_bf16 v[32:35], v[220:223], a[188:191], v[32:35]\n"
"ds_read_b64_tr_b16 v[118:119], v233 offset:18432\n"
"v_xor_b32 v234, s75, v234\n"
"v_mfma_f32_16x16x32_bf16 v[48:51], v[220:223], a[252:255], v[48:51]\n"
"ds_read_b64_tr_b16 v[112:113], v229 offset:16384\n"
"v_add_f32 v30, v30, v205\n"
"v_mfma_f32_16x16x32_bf16 v[48:51], v[216:219], a[248:251], v[48:51]\n"
"ds_read_b64_tr_b16 v[116:117], v233 offset:16384\n"
"v_bfe_u32 v161, v242, 8, 2\n"
"v_mfma_f32_16x16x32_bf16 v[48:51], v[212:215], a[244:247], v[48:51]\n"
"ds_read_b64_tr_b16 v[114:115], v229 offset:18432\n"
"v_and_b32 v86, s73, v86\n"
"v_mfma_f32_16x16x32_bf16 v[48:51], v[208:211], a[240:243], v[48:51]\n"
"ds_read_b64_tr_b16 v[218:219], v230 offset:26624\n"
"v_add_f32 v26, v26, v203\n"
"v_mfma_f32_16x16x32_bf16 v[48:51], v[192:195], a[204:207], v[48:51]\n"
"ds_read_b64_tr_b16 v[212:213], v234 offset:16384\n"
"v_xor_b32 v235, s75, v235\n"
"v_mfma_f32_16x16x32_bf16 v[48:51], v[188:191], a[200:203], v[48:51]\n"
"ds_read_b64_tr_b16 v[190:191], v230 offset:10240\n"
"v_add_f32 v28, v28, v204\n"
"v_mfma_f32_16x16x32_bf16 v[48:51], v[184:187], a[196:199], v[48:51]\n"
"ds_read_b64_tr_b16 v[224:225], v233 offset:24576\n"
"v_add_f32 v29, v29, v86\n"
"v_mfma_f32_16x16x32_bf16 v[48:51], v[180:183], a[192:195], v[48:51]\n"
"s_waitcnt vmcnt(15)\n"
"ds_read_b64_tr_b16 v[180:181], v230 offset:0\n"
"v_add_f32 v24, v24, v202\n"
"v_mfma_f32_16x16x32_bf16 v[4:7], v[96:99], a[96:99], v[4:7]\n"
"s_waitcnt lgkmcnt(7)\n"
"ds_read_b64_tr_b16 v[208:209], v230 offset:16384\n"
"v_lshlrev_b32 v206, 16, v88\n"
"v_mfma_f32_16x16x32_bf16 v[4:7], v[100:103], a[100:103], v[4:7]\n"
"ds_read_b64_tr_b16 v[182:183], v230 offset:2048\n"
"v_lshlrev_b32 v202, 16, v90\n"
"v_mfma_f32_16x16x32_bf16 v[4:7], v[104:107], a[104:107], v[4:7]\n"
"ds_read_b64_tr_b16 v[184:185], v234 offset:0\n"
"v_and_b32 v90, s73, v90\n"
"v_mfma_f32_16x16x32_bf16 v[4:7], v[108:111], a[108:111], v[4:7]\n"
"s_waitcnt lgkmcnt(7)\n"
"ds_read_b64_tr_b16 v[194:195], v234 offset:10240\n"
"v_xor_b32 v160, v160, v161\n"
"v_mfma_f32_16x16x32_bf16 v[4:7], v[112:115], a[144:147], v[4:7]\n"
"s_waitcnt vmcnt(4)\n"
"ds_read_b64_tr_b16 v[186:187], v234 offset:2048\n"
"v_lshlrev_b32 v203, 16, v91\n"
"v_mfma_f32_16x16x32_bf16 v[4:7], v[116:119], a[148:151], v[4:7]\n"
"v_subrev_u32 v200, s21, v200\n"
"ds_read_b64_tr_b16 v[216:217], v230 offset:24576\n"
"v_mfma_f32_16x16x32_bf16 v[4:7], v[120:123], a[152:155], v[4:7]\n"
"s_waitcnt lgkmcnt(7)\n"
"v_cmp_lt_u32 vcc, s69, v200\n"
"v_and_b32 v91, s73, v91\n"
"v_mfma_f32_16x16x32_bf16 v[4:7], v[224:227], a[156:159], v[4:7]\n"
"ds_read_b64_tr_b16 v[188:189], v230 offset:8192\n"
"s_cbranch_vccnz LGQ_%=\nLGQK_%=:\n"
"v_mfma_f32_16x16x32_bf16 v[20:23], v[224:227], a[172:175], v[20:23]\n"
"s_mov_b32 s20, 0\n"
"ds_read_b64_tr_b16 v[214:215], v234 offset:18432\n"
"v_and_b32 v88, s73, v88\n"
"v_mfma_f32_16x16x32_bf16 v[20:23], v[120:123], a[168:171], v[20:23]\n"
"buffer_load_dwordx4 v[124:127], v236, s[32:35], s20 offen sc1\n"
"v_mfma_f32_16x16x32_bf16 v[20:23], v[116:119], a[164:167], v[20:23]\n"
"v_add_u32 v160, -1, v160\n"
"v_add_f32 v37, v37, v90\n"
"v_mfma_f32_16x16x32_bf16 v[20:23], v[112:115], a[160:163], v[20:23]\n"
"s_mov_b32 s20, 286720\n"
"v_add_f32 v32, v32, v206\n"
"v_add_f32 v36, v36, v202\n"
"v_mfma_f32_16x16x32_bf16 v[20:23], v[108:111], a[124:127], v[20:23]\n"
"buffer_load_dwordx4 v[128:131], v236, s[32:35], s20 offen sc1\n"
"v_mfma_f32_16x16x32_bf16 v[20:23], v[104:107], a[120:123], v[20:23]\n"
"v_add_f32 v38, v38, v203\n"
"v_add_f32 v39, v39, v91\n"
"v_mfma_f32_16x16x32_bf16 v[20:23], v[100:103], a[116:119], v[20:23]\n"
"s_mov_b32 s20, 573440\n"
"v_lshlrev_b32 v207, 16, v89\n"
"v_add_f32 v34, v34, v207\n"
"v_mfma_f32_16x16x32_bf16 v[20:23], v[96:99], a[112:115], v[20:23]\n"
"buffer_load_dwordx4 v[132:135], v236, s[32:35], s20 offen sc1\n"
"v_mfma_f32_16x16x32_bf16 v[36:39], v[96:99], a[128:131], v[36:39]\n"
"v_and_b32 v87, s73, v87\n"
"v_lshlrev_b32 v207, 16, v95\n"
"v_mfma_f32_16x16x32_bf16 v[36:39], v[100:103], a[132:135], v[36:39]\n"
"s_mov_b32 s20, 860160\n"
"ds_read_b64_tr_b16 v[220:221], v234 offset:24576\n"
"v_lshlrev_b32 v206, 16, v94\n"
"v_mfma_f32_16x16x32_bf16 v[36:39], v[104:107], a[136:139], v[36:39]\n"
"buffer_load_dwordx4 v[136:139], v236, s[32:35], s20 offen sc1\n"
"v_mfma_f32_16x16x32_bf16 v[36:39], v[108:111], a[140:143], v[36:39]\n"
"v_add_f32 v33, v33, v88\n"
"v_add_u32 v241, 1, v241\n"
"v_mfma_f32_16x16x32_bf16 v[36:39], v[112:115], a[176:179], v[36:39]\n"
"s_mov_b32 s20, 1146880\n"
"v_lshlrev_b32 v205, 16, v93\n"
"v_add_f32 v46, v46, v207\n"
"v_mfma_f32_16x16x32_bf16 v[36:39], v[116:119], a[180:183], v[36:39]\n"
"buffer_load_dwordx4 v[140:143], v236, s[32:35], s20 offen sc1\n"
"v_mfma_f32_16x16x32_bf16 v[36:39], v[120:123], a[184:187], v[36:39]\n"
"v_and_b32 v84, s73, v84\n"
"v_and_b32 v85, s73, v85\n"
"v_mfma_f32_16x16x32_bf16 v[36:39], v[224:227], a[188:191], v[36:39]\n"
"s_mov_b32 s20, 1433600\n"
"ds_read_b64_tr_b16 v[192:193], v234 offset:8192\n"
"ds_read_b64_tr_b16 v[222:223], v234 offset:26624\n"
"v_mfma_f32_16x16x32_bf16 v[52:55], v[224:227], a[252:255], v[52:55]\n"
"buffer_load_dwordx4 v[144:147], v236, s[32:35], s20 offen sc1\n"
"v_mfma_f32_16x16x32_bf16 v[52:55], v[120:123], a[248:251], v[52:55]\n"
"v_add_f32 v25, v25, v84\n"
"v_mov_b32 v165, 0\n"
"v_mfma_f32_16x16x32_bf16 v[52:55], v[116:119], a[244:247], v[52:55]\n"
"s_mov_b32 s20, 1720320\n"
"ds_read_b64_tr_b16 v[210:211], v230 offset:18432\n"
"v_and_b32 v161, 16, v242\n"
"v_mfma_f32_16x16x32_bf16 v[52:55], v[112:115], a[240:243], v[52:55]\n"
"buffer_load_dwordx4 v[148:151], v236, s[32:35], s20 offen sc1\n"
"v_mfma_f32_16x16x32_bf16 v[52:55], v[108:111], a[204:207], v[52:55]\n"
"s_mov_b32 s20, 2007040\n"
"v_ashrrev_i32 v160, 31, v160\n"
"v_and_b32 v160, 0x3f80, v160\n"
"v_mfma_f32_16x16x32_bf16 v[52:55], v[104:107], a[200:203], v[52:55]\n"
"buffer_load_dwordx4 v[152:155], v236, s[32:35], s20 offen sc1\n"
"v_mfma_f32_16x16x32_bf16 v[52:55], v[100:103], a[196:199], v[52:55]\n"
"s_waitcnt vmcnt(8)\n"
"s_add_u32 s21, s21, 1\n"
"v_and_b32 v94, s73, v94\n"
"v_mfma_f32_16x16x32_bf16 v[52:55], v[96:99], a[192:195], v[52:55]\n"
"s_barrier\n"
"s_mov_b32 s52, 40960\n"
"s_waitcnt lgkmcnt(1)\n"
"v_mfma_f32_16x16x32_bf16 v[8:11], v[180:183], a[96:99], v[8:11]\n"
"s_mov_b32 s53, 81920\n"
"ds_read_b64_tr_b16 v[106:107], v231 offset:10240\n"
"s_cmp_eq_u32 s42, 0\n"
"v_mfma_f32_16x16x32_bf16 v[8:11], v[184:187], a[100:103], v[8:11]\n"
"s_cbranch_scc1 LGT_%=\nLGTK_%=:\n"
"v_add_f32 v44, v44, v206\n"
"v_mfma_f32_16x16x32_bf16 v[8:11], v[188:191], a[104:107], v[8:11]\n"
"ds_read_b64_tr_b16 v[104:105], v231 offset:8192\n"
"v_lshlrev_b32 v160, v161, v160\n"
"v_mfma_f32_16x16x32_bf16 v[8:11], v[192:195], a[108:111], v[8:11]\n"
"s_waitcnt lgkmcnt(2)\n"
"ds_read_b64_tr_b16 v[110:111], v235 offset:10240\n"
"v_add_f32 v45, v45, v94\n"
"v_mfma_f32_16x16x32_bf16 v[8:11], v[208:211], a[144:147], v[8:11]\n"
"v_mov_b32 v164, 0\n"
"v_mfma_f32_16x16x32_bf16 v[8:11], v[212:215], a[148:151], v[8:11]\n"
"ds_read_b64_tr_b16 v[96:97], v231 offset:0\n"
"v_add_f32 v27, v27, v85\n"
"v_mfma_f32_16x16x32_bf16 v[8:11], v[216:219], a[152:155], v[8:11]\n"
"ds_read_b64_tr_b16 v[100:101], v235 offset:0\n"
"v_lshlrev_b32 v204, 16, v92\n"
"v_mfma_f32_16x16x32_bf16 v[8:11], v[220:223], a[156:159], v[8:11]\n"
"v_add_f32 v40, v40, v204\n"
"v_add_f32 v42, v42, v205\n"
"v_mfma_f32_16x16x32_bf16 v[24:27], v[220:223], a[172:175], v[24:27]\n"
"ds_read_b64_tr_b16 v[120:121], v231 offset:24576\n"
"v_lshlrev_b32 v206, 16, v244\n"
"v_mfma_f32_16x16x32_bf16 v[24:27], v[216:219], a[168:171], v[24:27]\n"
"ds_read_b64_tr_b16 v[98:99], v231 offset:2048\n"
"v_lshlrev_b32 v202, 16, v246\n"
"v_mfma_f32_16x16x32_bf16 v[24:27], v[212:215], a[164:167], v[24:27]\n"
"v_and_b32 v92, s73, v92\n"
"v_and_b32 v93, s73, v93\n"
"v_mfma_f32_16x16x32_bf16 v[24:27], v[208:211], a[160:163], v[24:27]\n"
"ds_read_b64_tr_b16 v[112:113], v231 offset:16384\n"
"v_lshlrev_b32 v207, 16, v245\n"
"v_mfma_f32_16x16x32_bf16 v[24:27], v[192:195], a[124:127], v[24:27]\n"
"v_add_f32 v43, v43, v93\n"
"v_lshlrev_b32 v203, 16, v247\n"
"v_mfma_f32_16x16x32_bf16 v[24:27], v[188:191], a[120:123], v[24:27]\n"
"ds_read_b64_tr_b16 v[116:117], v235 offset:16384\n"
"v_and_b32 v95, s73, v95\n"
"v_mfma_f32_16x16x32_bf16 v[24:27], v[184:187], a[116:119], v[24:27]\n"
"ds_read_b64_tr_b16 v[122:123], v231 offset:26624\n"
"v_add_f32 v41, v41, v92\n"
"v_mfma_f32_16x16x32_bf16 v[24:27], v[180:183], a[112:115], v[24:27]\n"
"ds_read_b64_tr_b16 v[102:103], v235 offset:2048\n"
"v_bfe_i32 v161, v242, 5, 1\n"
"v_mfma_f32_16x16x32_bf16 v[40:43], v[180:183], a[128:131], v[40:43]\n"
"v_and_b32 v163, v160, v161\n"
"v_mfma_f32_16x16x32_bf16 v[40:43], v[184:187], a[132:135], v[40:43]\n"
"ds_read_b64_tr_b16 v[108:109], v235 offset:8192\n"
"v_add_f32 v48, v48, v206\n"
"v_mfma_f32_16x16x32_bf16 v[40:43], v[188:191], a[136:139], v[40:43]\n"
"ds_read_b64_tr_b16 v[114:115], v231 offset:18432\n"
"v_and_b32 v247, s73, v247\n"
"v_mfma_f32_16x16x32_bf16 v[40:43], v[192:195], a[140:143], v[40:43]\n"
"v_add_f32 v50, v50, v207\n"
"v_and_b32 v246, s73, v246\n"
"v_mfma_f32_16x16x32_bf16 v[40:43], v[208:211], a[176:179], v[40:43]\n"
"ds_read_b64_tr_b16 v[118:119], v235 offset:18432\n"
"v_add_f32 v52, v52, v202\n"
"v_mfma_f32_16x16x32_bf16 v[40:43], v[212:215], a[180:183], v[40:43]\n"
"ds_read_b64_tr_b16 v[226:227], v235 offset:26624\n"
"v_mov_b32 v161, 0\n"
"v_mfma_f32_16x16x32_bf16 v[40:43], v[216:219], a[184:187], v[40:43]\n"
"ds_read_b64_tr_b16 v[224:225], v235 offset:24576\n"
"v_and_b32 v245, s73, v245\n"
"v_mfma_f32_16x16x32_bf16 v[40:43], v[220:223], a[188:191], v[40:43]\n"
"s_barrier\n"
"s_waitcnt lgkmcnt(5)\n"
"v_mfma_f32_16x16x32_bf16 v[12:15], v[96:99], a[96:99], v[12:15]\n"
"ds_read_b64_tr_b16 v[192:193], v232 offset:40960\n"
"ds_read_b64_tr_b16 v[222:223], v232 offset:59392\n"
"v_mfma_f32_16x16x32_bf16 v[12:15], v[100:103], a[100:103], v[12:15]\n"
"ds_read_b64_tr_b16 v[190:191], v228 offset:43008\n"
"v_add_f32 v53, v53, v246\n"
"v_mfma_f32_16x16x32_bf16 v[12:15], v[104:107], a[104:107], v[12:15]\n"
"s_waitcnt lgkmcnt(6)\n"
"ds_read_b64_tr_b16 v[216:217], v228 offset:57344\n"
"v_add_f32 v54, v54, v203\n"
"v_mfma_f32_16x16x32_bf16 v[12:15], v[108:111], a[108:111], v[12:15]\n"
"ds_read_b64_tr_b16 v[210:211], v228 offset:51200\n"
"ds_read_b64_tr_b16 v[214:215], v232 offset:51200\n"
"v_mfma_f32_16x16x32_bf16 v[12:15], v[112:115], a[144:147], v[12:15]\n"
"s_waitcnt lgkmcnt(8)\n"
"ds_read_b64_tr_b16 v[194:195], v232 offset:43008\n"
"v_add_f32 v47, v47, v95\n"
"v_mfma_f32_16x16x32_bf16 v[12:15], v[116:119], a[148:151], v[12:15]\n"
"ds_read_b64_tr_b16 v[208:209], v228 offset:49152\n"
"v_add_f32 v31, v31, v87\n"
"v_mfma_f32_16x16x32_bf16 v[12:15], v[120:123], a[152:155], v[12:15]\n"
"s_waitcnt lgkmcnt(8)\n"
"ds_read_b64_tr_b16 v[184:185], v232 offset:32768\n"
"ds_read_b64_tr_b16 v[186:187], v232 offset:34816\n"
"v_mfma_f32_16x16x32_bf16 v[12:15], v[224:227], a[156:159], v[12:15]\n"
"global_load_dword v200, v201, s[6:7] sc1\n"
"v_mfma_f32_16x16x32_bf16 v[28:31], v[224:227], a[172:175], v[28:31]\n"
"v_and_b32 v89, s73, v89\n"
"v_and_b32 v244, s73, v244\n"
"v_mfma_f32_16x16x32_bf16 v[28:31], v[120:123], a[168:171], v[28:31]\n"
"s_waitcnt vmcnt(8)\n"
"v_lshlrev_b32 v56, 16, v124\n"
"v_and_b32 v63, s73, v127\n"
"v_mfma_f32_16x16x32_bf16 v[28:31], v[116:119], a[164:167], v[28:31]\n"
"ds_read_b64_tr_b16 v[188:189], v228 offset:40960\n"
"ds_read_b64_tr_b16 v[212:213], v232 offset:49152\n"
"v_mfma_f32_16x16x32_bf16 v[28:31], v[112:115], a[160:163], v[28:31]\n"
"ds_read_b64_tr_b16 v[220:221], v232 offset:57344\n"
"v_lshlrev_b32 v58, 16, v125\n"
"v_mfma_f32_16x16x32_bf16 v[28:31], v[108:111], a[124:127], v[28:31]\n"
"s_waitcnt vmcnt(7)\n"
"ds_read_b64_tr_b16 v[182:183], v228 offset:34816\n"
"v_lshlrev_b32 v206, 16, v130\n"
"v_mfma_f32_16x16x32_bf16 v[28:31], v[104:107], a[120:123], v[28:31]\n"
"ds_read_b64_tr_b16 v[180:181], v228 offset:32768\n"
"v_and_b32 v61, s73, v126\n"
"v_mfma_f32_16x16x32_bf16 v[28:31], v[100:103], a[116:119], v[28:31]\n"
"v_lshlrev_b32 v204, 16, v128\n"
"v_lshlrev_b32 v205, 16, v129\n"
"v_mfma_f32_16x16x32_bf16 v[28:31], v[96:99], a[112:115], v[28:31]\n"
"s_waitcnt vmcnt(6)\n"
"v_and_b32 v57, s73, v124\n"
"v_lshlrev_b32 v60, 16, v126\n"
"v_lshlrev_b32 v62, 16, v127\n"
"v_add_f32 v56, v56, v204\n"
"v_and_b32 v129, s73, v129\n"
"v_add_f32 v58, v58, v205\n"
"v_add_f32 v60, v60, v206\n"
"v_lshlrev_b32 v203, 16, v133\n"
"v_lshlrev_b32 v204, 16, v134\n"
"v_mfma_f32_16x16x32_bf16 v[44:47], v[96:99], a[128:131], v[44:47]\n"
"ds_read_b64_tr_b16 v[218:219], v228 offset:59392\n"
"v_and_b32 v130, s73, v130\n"
"v_mfma_f32_16x16x32_bf16 v[44:47], v[100:103], a[132:135], v[44:47]\n"
"v_lshlrev_b32 v207, 16, v131\n"
"v_add_f32 v60, v60, v204\n"
"v_mfma_f32_16x16x32_bf16 v[44:47], v[104:107], a[136:139], v[44:47]\n"
"s_waitcnt vmcnt(5)\n"
"v_and_b32 v131, s73, v131\n"
"v_lshlrev_b32 v202, 16, v132\n"
"v_mfma_f32_16x16x32_bf16 v[44:47], v[108:111], a[140:143], v[44:47]\n"
"ds_read_b64_tr_b16 v[96:97], v229 offset:32768\n"
"v_add_f32 v58, v58, v203\n"
"v_mfma_f32_16x16x32_bf16 v[44:47], v[112:115], a[176:179], v[44:47]\n"
"v_and_b32 v134, s73, v134\n"
"v_lshlrev_b32 v205, 16, v135\n"
"v_mfma_f32_16x16x32_bf16 v[44:47], v[116:119], a[180:183], v[44:47]\n"
"s_waitcnt vmcnt(4)\n"
"ds_read_b64_tr_b16 v[102:103], v233 offset:34816\n"
"ds_read_b64_tr_b16 v[110:111], v233 offset:43008\n"
"v_mfma_f32_16x16x32_bf16 v[44:47], v[120:123], a[184:187], v[44:47]\n"
"v_and_b32 v59, s73, v125\n"
"v_and_b32 v128, s73, v128\n"
"v_mfma_f32_16x16x32_bf16 v[44:47], v[224:227], a[188:191], v[44:47]\n"
"s_waitcnt lgkmcnt(4)\n"
"ds_read_b64_tr_b16 v[226:227], v233 offset:59392\n"
"v_add_f32 v62, v62, v207\n"
"v_mfma_f32_16x16x32_bf16 v[0:3], v[180:183], a[48:51], v[0:3]\n"
"s_waitcnt vmcnt(3)\n"
"v_add_f32 v51, v51, v245\n"
"v_and_b32 v133, s73, v133\n"
"v_mfma_f32_16x16x32_bf16 v[0:3], v[184:187], a[52:55], v[0:3]\n"
"ds_read_b64_tr_b16 v[104:105], v229 offset:40960\n"
"v_lshlrev_b32 v203, 16, v139\n"
"v_mfma_f32_16x16x32_bf16 v[0:3], v[188:191], a[56:59], v[0:3]\n"
"ds_read_b64_tr_b16 v[116:117], v233 offset:49152\n"
"v_add_f32 v55, v55, v247\n"
"v_mfma_f32_16x16x32_bf16 v[0:3], v[192:195], a[60:63], v[0:3]\n"
"s_waitcnt vmcnt(2)\n"
"v_add_f32 v56, v56, v202\n"
"v_lshlrev_b32 v202, 16, v138\n"
"v_mfma_f32_16x16x32_bf16 v[0:3], v[208:211], a[0:3], v[0:3]\n"
"ds_read_b64_tr_b16 v[120:121], v229 offset:57344\n"
"v_and_b32 v135, s73, v135\n"
"v_mfma_f32_16x16x32_bf16 v[0:3], v[212:215], a[4:7], v[0:3]\n"
"s_waitcnt lgkmcnt(4)\n"
"s_waitcnt vmcnt(1)\n"
"v_lshlrev_b32 v206, 16, v136\n"
"v_lshlrev_b32 v207, 16, v137\n"
"v_mfma_f32_16x16x32_bf16 v[0:3], v[216:219], a[8:11], v[0:3]\n"
"ds_read_b64_tr_b16 v[100:101], v233 offset:32768\n"
"ds_read_b64_tr_b16 v[112:113], v229 offset:49152\n"
"v_mfma_f32_16x16x32_bf16 v[0:3], v[220:223], a[12:15], v[0:3]\n"
"ds_read_b64_tr_b16 v[98:99], v229 offset:34816\n"
"v_add_f32 v59, v59, v129\n"
"v_add_f32 v62, v62, v205\n"
"v_add_f32 v56, v56, v206\n"
"v_lshlrev_b32 v204, 16, v140\n"
"v_lshlrev_b32 v206, 16, v142\n"
"v_mfma_f32_16x16x32_bf16 v[16:19], v[220:223], a[28:31], v[16:19]\n"
"v_add_f32 v61, v61, v130\n"
"v_lshlrev_b32 v205, 16, v141\n"
"v_mfma_f32_16x16x32_bf16 v[16:19], v[216:219], a[24:27], v[16:19]\n"
"v_add_f32 v35, v35, v89\n"
"v_and_b32 v138, s73, v138\n"
"v_mfma_f32_16x16x32_bf16 v[16:19], v[212:215], a[20:23], v[16:19]\n"
"ds_read_b64_tr_b16 v[118:119], v233 offset:51200\n"
"v_add_f32 v58, v58, v207\n"
"v_mfma_f32_16x16x32_bf16 v[16:19], v[208:211], a[16:19], v[16:19]\n"
"ds_read_b64_tr_b16 v[224:225], v233 offset:57344\n"
"v_and_b32 v136, s73, v136\n"
"v_mfma_f32_16x16x32_bf16 v[16:19], v[192:195], a[76:79], v[16:19]\n"
"s_waitcnt vmcnt(0)\n"
"v_and_b32 v139, s73, v139\n"
"v_and_b32 v140, s73, v140\n"
"v_mfma_f32_16x16x32_bf16 v[16:19], v[188:191], a[72:75], v[16:19]\n"
"v_subrev_u32 v200, s21, v200\n"
"v_add_f32 v49, v49, v244\n"
"v_mfma_f32_16x16x32_bf16 v[16:19], v[184:187], a[68:71], v[16:19]\n"
"v_cmp_lt_u32 vcc, s69, v200\n"
"ds_read_b64_tr_b16 v[108:109], v233 offset:40960\n"
"v_mfma_f32_16x16x32_bf16 v[16:19], v[180:183], a[64:67], v[16:19]\n"
"ds_read_b64_tr_b16 v[114:115], v229 offset:51200\n"
"ds_read_b64_tr_b16 v[122:123], v229 offset:59392\n"
"v_add_f32 v60, v60, v202\n"
"v_add_f32 v56, v56, v204\n"
"v_lshlrev_b32 v202, 16, v144\n"
"v_lshlrev_b32 v204, 16, v146\n"
"v_subrev_f32 v0, s22, v0\n"
"s_cbranch_vccnz LGR_%=\nLGRK_%=:\n"
"v_mfma_f32_16x16x32_bf16 v[32:35], v[180:183], a[80:83], v[32:35]\n"
"ds_read_b64_tr_b16 v[106:107], v229 offset:43008\n"
"s_cmp_eq_u32 s93, 0\ns_cbranch_scc1 LEP_%=\nLEPK_%=:\n"
"v_mfma_f32_16x16x32_bf16 v[32:35], v[184:187], a[84:87], v[32:35]\n"
"s_mov_b32 s20, 28672\n"
"v_add_f32 v57, v57, v128\n"
"v_lshlrev_b32 v207, 16, v143\n"
"v_mfma_f32_16x16x32_bf16 v[32:35], v[188:191], a[88:91], v[32:35]\n"
"buffer_load_dwordx4 v[72:75], v236, s[32:35], s20 offen sc1\n"
"v_mfma_f32_16x16x32_bf16 v[32:35], v[192:195], a[92:95], v[32:35]\n"
"v_add_f32 v61, v61, v134\n"
"v_add_f32 v60, v60, v206\n"
"v_mfma_f32_16x16x32_bf16 v[32:35], v[208:211], a[32:35], v[32:35]\n"
"s_mov_b32 s20, 315392\n"
"v_and_b32 v137, s73, v137\n"
"v_subrev_f32 v1, s22, v1\n"
"v_mfma_f32_16x16x32_bf16 v[32:35], v[212:215], a[36:39], v[32:35]\n"
"buffer_load_dwordx4 v[76:79], v236, s[32:35], s20 offen sc1\n"
"v_mfma_f32_16x16x32_bf16 v[32:35], v[216:219], a[40:43], v[32:35]\n"
"v_add_f32 v63, v63, v131\n"
"v_and_b32 v144, s73, v144\n"
"v_mfma_f32_16x16x32_bf16 v[32:35], v[220:223], a[44:47], v[32:35]\n"
"s_mov_b32 s20, 602112\n"
"v_add_f32 v58, v58, v205\n"
"v_subrev_f32 v2, s22, v2\n"
"v_mfma_f32_16x16x32_bf16 v[48:51], v[220:223], a[228:231], v[48:51]\n"
"buffer_load_dwordx4 v[80:83], v236, s[32:35], s20 offen sc1\n"
"v_mfma_f32_16x16x32_bf16 v[48:51], v[216:219], a[224:227], v[48:51]\n"
"v_and_b32 v143, s73, v143\n"
"v_lshlrev_b32 v205, 16, v147\n"
"v_mfma_f32_16x16x32_bf16 v[48:51], v[212:215], a[212:215], v[48:51]\n"
"s_mov_b32 s20, 888832\n"
"v_add_f32 v62, v62, v203\n"
"v_cvt_pk_bf16_f32 v0, v0, v1\n"
"v_mfma_f32_16x16x32_bf16 v[48:51], v[208:211], a[208:211], v[48:51]\n"
"buffer_load_dwordx4 v[84:87], v236, s[32:35], s20 offen sc1\n"
"v_mfma_f32_16x16x32_bf16 v[48:51], v[192:195], a[236:239], v[48:51]\n"
"v_lshlrev_b32 v206, 16, v148\n"
"v_and_b32 v148, s73, v148\n"
"v_mfma_f32_16x16x32_bf16 v[48:51], v[188:191], a[232:235], v[48:51]\n"
"s_mov_b32 s20, 1175552\n"
"v_add_f32 v56, v56, v202\n"
"v_subrev_f32 v33, s22, v33\n"
"v_mfma_f32_16x16x32_bf16 v[48:51], v[184:187], a[220:223], v[48:51]\n"
"buffer_load_dwordx4 v[88:91], v236, s[32:35], s20 offen sc1\n"
"v_mfma_f32_16x16x32_bf16 v[48:51], v[180:183], a[216:219], v[48:51]\n"
"s_waitcnt lgkmcnt(0)\n"
"v_add_f32 v61, v61, v138\n"
"v_lshlrev_b32 v203, 16, v145\n"
"v_mfma_f32_16x16x32_bf16 v[4:7], v[96:99], a[48:51], v[4:7]\n"
"s_mov_b32 s20, 1462272\n"
"v_add_f32 v59, v59, v133\n"
"v_subrev_f32 v17, s22, v17\n"
"v_mfma_f32_16x16x32_bf16 v[4:7], v[100:103], a[52:55], v[4:7]\n"
"buffer_load_dwordx4 v[92:95], v236, s[32:35], s20 offen sc1\n"
"v_and_b32 v132, s73, v132\n"
"v_add_f32 v60, v60, v204\n"
"v_subrev_f32 v3, s22, v3\n"
"v_subrev_f32 v16, s22, v16\n"
"v_subrev_f32 v34, s22, v34\n"
"v_mfma_f32_16x16x32_bf16 v[4:7], v[104:107], a[56:59], v[4:7]\n"
"v_and_b32 v146, s73, v146\n"
"v_subrev_f32 v19, s22, v19\n"
"v_mfma_f32_16x16x32_bf16 v[4:7], v[108:111], a[60:63], v[4:7]\n"
"s_mov_b32 s20, 1748992\n"
"v_add_f32 v63, v63, v135\n"
"v_add_f32 v62, v62, v207\n"
"v_mfma_f32_16x16x32_bf16 v[4:7], v[112:115], a[0:3], v[4:7]\n"
"buffer_load_dwordx4 v[244:247], v236, s[32:35], s20 offen sc1\n"
"v_mfma_f32_16x16x32_bf16 v[4:7], v[116:119], a[4:7], v[4:7]\n"
"v_and_b32 v141, s73, v141\n"
"v_lshlrev_b32 v202, 16, v150\n"
"v_mfma_f32_16x16x32_bf16 v[4:7], v[120:123], a[8:11], v[4:7]\n"
"s_mov_b32 s20, 2035712\n"
"v_add_f32 v57, v57, v132\n"
"v_subrev_f32 v32, s22, v32\n"
"v_mfma_f32_16x16x32_bf16 v[4:7], v[224:227], a[12:15], v[4:7]\n"
"buffer_load_dwordx4 v[156:159], v236, s[32:35], s20 offen sc1\n"
"v_add_f32 v57, v57, v136\n"
"v_add_f32 v63, v63, v139\n"
"v_add_f32 v63, v63, v143\n"
"v_add_f32 v62, v62, v205\n"
"v_lshlrev_b32 v207, 16, v149\n"
"v_lshlrev_b32 v204, 16, v152\n"
"v_subrev_f32 v18, s22, v18\n"
"v_cvt_pk_bf16_f32 v16, v16, v17\n"
"v_mfma_f32_16x16x32_bf16 v[20:23], v[224:227], a[28:31], v[20:23]\n"
"v_add_f32 v57, v57, v140\n"
"v_cvt_pk_bf16_f32 v1, v2, v3\n"
"v_mfma_f32_16x16x32_bf16 v[20:23], v[120:123], a[24:27], v[20:23]\n"
"v_add_f32 v56, v56, v206\n"
"v_subrev_f32 v50, s22, v50\n"
"v_mfma_f32_16x16x32_bf16 v[20:23], v[116:119], a[20:23], v[20:23]\n"
"ds_read_b64_tr_b16 v[186:187], v234 offset:34816\n"
"v_add_f32 v60, v60, v202\n"
"v_mfma_f32_16x16x32_bf16 v[20:23], v[112:115], a[16:19], v[20:23]\n"
"v_add_f32 v59, v59, v137\n"
"v_and_b32 v142, s73, v142\n"
"v_mfma_f32_16x16x32_bf16 v[20:23], v[108:111], a[76:79], v[20:23]\n"
"v_subrev_f32 v6, s22, v6\n"
"v_subrev_f32 v35, s22, v35\n"
"v_mfma_f32_16x16x32_bf16 v[20:23], v[104:107], a[72:75], v[20:23]\n"
"ds_read_b64_tr_b16 v[220:221], v234 offset:57344\n"
"v_cvt_pk_bf16_f32 v17, v18, v19\n"
"v_mfma_f32_16x16x32_bf16 v[20:23], v[100:103], a[68:71], v[20:23]\n"
"ds_read_b64_tr_b16 v[210:211], v230 offset:51200\n"
"v_and_b32 v152, s73, v152\n"
"v_mfma_f32_16x16x32_bf16 v[20:23], v[96:99], a[64:67], v[20:23]\n"
"ds_read_b64_tr_b16 v[188:189], v230 offset:40960\n"
"v_and_b32 v145, s73, v145\n"
"v_add_f32 v58, v58, v203\n"
"v_and_b32 v147, s73, v147\n"
"v_add_f32 v63, v63, v147\n"
"v_add_f32 v58, v58, v207\n"
"v_lshlrev_b32 v203, 16, v151\n"
"v_lshlrev_b32 v205, 16, v153\n"
"v_subrev_f32 v7, s22, v7\n"
"v_cvt_pk_bf16_f32 v3, v6, v7\n"
"v_mfma_f32_16x16x32_bf16 v[36:39], v[96:99], a[80:83], v[36:39]\n"
"ds_read_b64_tr_b16 v[184:185], v234 offset:32768\n"
"v_subrev_f32 v5, s22, v5\n"
"v_mfma_f32_16x16x32_bf16 v[36:39], v[100:103], a[84:87], v[36:39]\n"
"ds_read_b64_tr_b16 v[218:219], v230 offset:59392\n"
"v_subrev_f32 v4, s22, v4\n"
"v_mfma_f32_16x16x32_bf16 v[36:39], v[104:107], a[88:91], v[36:39]\n"
"ds_read_b64_tr_b16 v[194:195], v234 offset:43008\n"
"v_add_f32 v59, v59, v141\n"
"v_lshlrev_b32 v207, 16, v155\n"
"v_cvt_pk_bf16_f32 v2, v4, v5\n"
"v_subrev_f32 v20, s22, v20\n"
"v_subrev_f32 v21, s22, v21\n"
"v_subrev_f32 v22, s22, v22\n"
"v_subrev_f32 v23, s22, v23\n"
"v_mfma_f32_16x16x32_bf16 v[36:39], v[108:111], a[92:95], v[36:39]\n"
"ds_read_b64_tr_b16 v[216:217], v230 offset:57344\n"
"v_add_f32 v59, v59, v145\n"
"v_mfma_f32_16x16x32_bf16 v[36:39], v[112:115], a[32:35], v[36:39]\n"
"ds_read_b64_tr_b16 v[214:215], v234 offset:51200\n"
"v_add_f32 v61, v61, v142\n"
"v_add_f32 v62, v62, v203\n"
"v_and_b32 v153, s73, v153\n"
"v_permlane16_swap_b32 v0, v2\n"
"v_permlane16_swap_b32 v1, v3\n"
"v_cvt_pk_bf16_f32 v19, v22, v23\n"
"v_cvt_pk_bf16_f32 v32, v32, v33\n"
"v_mfma_f32_16x16x32_bf16 v[36:39], v[116:119], a[36:39], v[36:39]\n"
"ds_read_b64_tr_b16 v[180:181], v230 offset:32768\n"
"buffer_store_dwordx4 v[0:3], v238, s[36:39], 0 offen sc0 sc1\n"
"v_and_b32 v149, s73, v149\n"
"v_and_b32 v151, s73, v151\n"
"v_add_f32 v56, v56, v204\n"
"v_permlane16_swap_b32 v17, v19\n"
"v_mfma_f32_16x16x32_bf16 v[36:39], v[120:123], a[40:43], v[36:39]\n"
"ds_read_b64_tr_b16 v[192:193], v234 offset:40960\n"
"v_cvt_pk_bf16_f32 v18, v20, v21\n"
"v_mfma_f32_16x16x32_bf16 v[36:39], v[224:227], a[44:47], v[36:39]\n"
"s_nop 0\n"
"v_permlane16_swap_b32 v16, v18\n"
"v_mfma_f32_16x16x32_bf16 v[52:55], v[224:227], a[228:231], v[52:55]\n"
"ds_read_b64_tr_b16 v[182:183], v230 offset:34816\n"
"ds_read_b64_tr_b16 v[208:209], v230 offset:49152\n"
"v_mfma_f32_16x16x32_bf16 v[52:55], v[120:123], a[224:227], v[52:55]\n"
"v_add_f32 v63, v63, v151\n"
"v_subrev_f32 v51, s22, v51\n"
"v_mfma_f32_16x16x32_bf16 v[52:55], v[116:119], a[212:215], v[52:55]\n"
"v_lshlrev_b32 v206, 16, v154\n"
"v_subrev_f32 v37, s22, v37\n"
"v_mfma_f32_16x16x32_bf16 v[52:55], v[112:115], a[208:211], v[52:55]\n"
"ds_read_b64_tr_b16 v[190:191], v230 offset:43008\n"
"ds_read_b64_tr_b16 v[212:213], v234 offset:49152\n"
"buffer_store_dwordx4 v[16:19], v238, s[36:39], s52 offen sc0 sc1\n"
"v_add_f32 v57, v57, v144\n"
"v_and_b32 v150, s73, v150\n"
"v_subrev_f32 v36, s22, v36\n"
"v_cvt_pk_bf16_f32 v33, v34, v35\n"
"v_mfma_f32_16x16x32_bf16 v[52:55], v[108:111], a[236:239], v[52:55]\n"
"v_and_b32 v155, s73, v155\n"
"v_add_f32 v63, v63, v155\n"
"v_mfma_f32_16x16x32_bf16 v[52:55], v[104:107], a[232:235], v[52:55]\n"
"v_add_f32 v58, v58, v205\n"
"v_subrev_f32 v48, s22, v48\n"
"v_mfma_f32_16x16x32_bf16 v[52:55], v[100:103], a[220:223], v[52:55]\n"
"ds_read_b64_tr_b16 v[222:223], v234 offset:59392\n"
"v_subrev_f32 v38, s22, v38\n"
"v_mfma_f32_16x16x32_bf16 v[52:55], v[96:99], a[216:219], v[52:55]\n"
"s_waitcnt lgkmcnt(4)\n"
"v_add_f32 v57, v57, v148\n"
"v_subrev_f32 v39, s22, v39\n"
"v_mfma_f32_16x16x32_bf16 v[8:11], v[180:183], a[48:51], v[8:11]\n"
"ds_read_b64_tr_b16 v[98:99], v231 offset:34816\n"
"v_add_f32 v57, v57, v152\n"
"v_mfma_f32_16x16x32_bf16 v[8:11], v[184:187], a[52:55], v[8:11]\n"
"s_waitcnt lgkmcnt(3)\n"
"v_add_f32 v59, v59, v149\n"
"v_cvt_pk_bf16_f32 v56, v56, v57\n"
"v_mfma_f32_16x16x32_bf16 v[8:11], v[188:191], a[56:59], v[8:11]\n"
"v_cvt_pk_bf16_f32 v34, v36, v37\n"
"v_cvt_pk_bf16_f32 v35, v38, v39\n"
"v_mfma_f32_16x16x32_bf16 v[8:11], v[192:195], a[60:63], v[8:11]\n"
"v_add_f32 v61, v61, v146\n"
"v_add_f32 v59, v59, v153\n"
"v_add_f32 v60, v60, v206\n"
"v_add_f32 v62, v62, v207\n"
"v_cvt_pk_bf16_f32 v57, v58, v59\n"
"v_cvt_pk_bf16_f32 v59, v62, v63\n"
"v_permlane16_swap_b32 v32, v34\n"
"v_permlane16_swap_b32 v33, v35\n"
"v_mfma_f32_16x16x32_bf16 v[8:11], v[208:211], a[0:3], v[8:11]\n"
"s_waitcnt lgkmcnt(2)\n"
"buffer_store_dwordx4 v[32:35], v238, s[36:39], s53 offen sc0 sc1\n"
"v_add_f32 v61, v61, v150\n"
"v_and_b32 v154, s73, v154\n"
"v_add_f32 v61, v61, v154\n"
"v_permlane16_swap_b32 v57, v59\n"
"v_mfma_f32_16x16x32_bf16 v[8:11], v[212:215], a[4:7], v[8:11]\n"
"v_cvt_pk_bf16_f32 v58, v60, v61\n"
"v_subrev_f32 v49, s22, v49\n"
"v_mfma_f32_16x16x32_bf16 v[8:11], v[216:219], a[8:11], v[8:11]\n"
"s_waitcnt lgkmcnt(1)\n"
"v_permlane16_swap_b32 v56, v58\n"
"v_mfma_f32_16x16x32_bf16 v[8:11], v[220:223], a[12:15], v[8:11]\n"
"buffer_store_dwordx4 v[56:59], v253, s[36:39], 0 offen sc0 sc1\n"
"v_subrev_f32 v53, s22, v53\n"
"v_subrev_f32 v55, s22, v55\n"
"v_mfma_f32_16x16x32_bf16 v[24:27], v[220:223], a[28:31], v[24:27]\n"
"v_subrev_f32 v52, s22, v52\n"
"v_cvt_pk_bf16_f32 v48, v48, v49\n"
"v_mfma_f32_16x16x32_bf16 v[24:27], v[216:219], a[24:27], v[24:27]\n"
"ds_read_b64_tr_b16 v[112:113], v231 offset:49152\n"
"v_cvt_pk_bf16_f32 v49, v50, v51\n"
"v_mfma_f32_16x16x32_bf16 v[24:27], v[212:215], a[20:23], v[24:27]\n"
"ds_read_b64_tr_b16 v[100:101], v235 offset:32768\n"
"v_cvt_pk_bf16_f32 v50, v52, v53\n"
"v_mfma_f32_16x16x32_bf16 v[24:27], v[208:211], a[16:19], v[24:27]\n"
"ds_read_b64_tr_b16 v[104:105], v231 offset:40960\n"
"v_subrev_f32 v54, s22, v54\n"
"v_mfma_f32_16x16x32_bf16 v[24:27], v[192:195], a[76:79], v[24:27]\n"
"ds_read_b64_tr_b16 v[96:97], v231 offset:32768\n"
"v_cvt_pk_bf16_f32 v51, v54, v55\n"
"v_mfma_f32_16x16x32_bf16 v[24:27], v[188:191], a[72:75], v[24:27]\n"
"s_nop 0\n"
"v_permlane16_swap_b32 v49, v51\n"
"v_mfma_f32_16x16x32_bf16 v[24:27], v[184:187], a[68:71], v[24:27]\n"
"v_permlane16_swap_b32 v48, v50\n"
"v_mfma_f32_16x16x32_bf16 v[24:27], v[180:183], a[64:67], v[24:27]\n"
"buffer_store_dwordx4 v[48:51], v252, s[36:39], 0 offen sc0 sc1\n"
"ds_read_b64_tr_b16 v[106:107], v231 offset:43008\n"
"v_mfma_f32_16x16x32_bf16 v[40:43], v[180:183], a[80:83], v[40:43]\n"
"ds_read_b64_tr_b16 v[102:103], v235 offset:34816\n"
"ds_read_b64_tr_b16 v[114:115], v231 offset:51200\n"
"v_mfma_f32_16x16x32_bf16 v[40:43], v[184:187], a[84:87], v[40:43]\n"
"ds_read_b64_tr_b16 v[110:111], v235 offset:43008\n"
"v_subrev_f32 v11, s22, v11\n"
"v_mfma_f32_16x16x32_bf16 v[40:43], v[188:191], a[88:91], v[40:43]\n"
"ds_read_b64_tr_b16 v[108:109], v235 offset:40960\n"
"ds_read_b64_tr_b16 v[116:117], v235 offset:49152\n"
"v_mfma_f32_16x16x32_bf16 v[40:43], v[192:195], a[92:95], v[40:43]\n"
"ds_read_b64_tr_b16 v[122:123], v231 offset:59392\n"
"v_subrev_f32 v9, s22, v9\n"
"v_mfma_f32_16x16x32_bf16 v[40:43], v[208:211], a[32:35], v[40:43]\n"
"ds_read_b64_tr_b16 v[118:119], v235 offset:51200\n"
"v_mfma_f32_16x16x32_bf16 v[40:43], v[212:215], a[36:39], v[40:43]\n"
"ds_read_b64_tr_b16 v[120:121], v231 offset:57344\n"
"v_subrev_f32 v8, s22, v8\n"
"v_mfma_f32_16x16x32_bf16 v[40:43], v[216:219], a[40:43], v[40:43]\n"
"ds_read_b64_tr_b16 v[224:225], v235 offset:57344\n"
"ds_read_b64_tr_b16 v[226:227], v235 offset:59392\n"
"v_mfma_f32_16x16x32_bf16 v[40:43], v[220:223], a[44:47], v[40:43]\n"
"s_waitcnt lgkmcnt(10)\n"
"v_subrev_f32 v25, s22, v25\n"
"v_mfma_f32_16x16x32_bf16 v[12:15], v[96:99], a[48:51], v[12:15]\n"
"s_waitcnt lgkmcnt(8)\n"
"v_mfma_f32_16x16x32_bf16 v[12:15], v[100:103], a[52:55], v[12:15]\n"
"v_mfma_f32_16x16x32_bf16 v[12:15], v[104:107], a[56:59], v[12:15]\n"
"s_waitcnt lgkmcnt(5)\n"
"v_xor_b32 v162, v160, v163\n"
"v_cvt_pk_bf16_f32 v8, v8, v9\n"
"v_mfma_f32_16x16x32_bf16 v[12:15], v[108:111], a[60:63], v[12:15]\n"
"v_mfma_f32_16x16x32_bf16 v[12:15], v[112:115], a[0:3], v[12:15]\n"
"s_waitcnt lgkmcnt(3)\n"
"v_subrev_f32 v40, s22, v40\n"
"v_mfma_f32_16x16x32_bf16 v[12:15], v[116:119], a[4:7], v[12:15]\n"
"s_waitcnt lgkmcnt(2)\n"
"v_subrev_f32 v10, s22, v10\n"
"v_mfma_f32_16x16x32_bf16 v[12:15], v[120:123], a[8:11], v[12:15]\n"
"s_waitcnt lgkmcnt(0)\n"
"v_subrev_f32 v41, s22, v41\n"
"v_mfma_f32_16x16x32_bf16 v[12:15], v[224:227], a[12:15], v[12:15]\n"
"v_subrev_f32 v27, s22, v27\n"
"s_waitcnt vmcnt(5)\n"
"v_mfma_f32_16x16x32_bf16 v[64:67], v[162:165], v[72:75], 0\n"
"v_mov_b32 v160, 0\n"
"v_subrev_f32 v24, s22, v24\n"
"v_mfma_f32_16x16x32_bf16 v[64:67], v[162:165], v[76:79], v[64:67]\n"
"v_mfma_f32_16x16x32_bf16 v[64:67], v[162:165], v[80:83], v[64:67]\n"
"s_nop 0\n"
"v_subrev_f32 v12, s22, v12\n"
"v_cvt_pk_bf16_f32 v9, v10, v11\n"
"v_mfma_f32_16x16x32_bf16 v[64:67], v[162:165], v[84:87], v[64:67]\n"
"v_subrev_f32 v14, s22, v14\n"
"v_subrev_f32 v26, s22, v26\n"
"v_mfma_f32_16x16x32_bf16 v[64:67], v[162:165], v[88:91], v[64:67]\n"
"v_subrev_f32 v15, s22, v15\n"
"v_cvt_pk_bf16_f32 v11, v14, v15\n"
"v_mfma_f32_16x16x32_bf16 v[64:67], v[162:165], v[92:95], v[64:67]\n"
"v_mfma_f32_16x16x32_bf16 v[64:67], v[162:165], v[244:247], v[64:67]\n"
"v_mfma_f32_16x16x32_bf16 v[64:67], v[162:165], v[156:159], v[64:67]\n"
"v_subrev_f32 v13, s22, v13\n"
"v_cvt_pk_bf16_f32 v10, v12, v13\n"
"v_mfma_f32_16x16x32_bf16 v[68:71], v[160:163], v[72:75], 0\n"
"v_mfma_f32_16x16x32_bf16 v[68:71], v[160:163], v[76:79], v[68:71]\n"
"v_permlane16_swap_b32 v9, v11\n"
"v_mfma_f32_16x16x32_bf16 v[68:71], v[160:163], v[80:83], v[68:71]\n"
"v_permlane16_swap_b32 v8, v10\n"
"v_mfma_f32_16x16x32_bf16 v[68:71], v[160:163], v[84:87], v[68:71]\n"
"buffer_store_dwordx4 v[8:11], v239, s[36:39], 0 offen sc0 sc1\n"
"v_cvt_pk_bf16_f32 v24, v24, v25\n"
"v_mfma_f32_16x16x32_bf16 v[68:71], v[160:163], v[88:91], v[68:71]\n"
"v_cvt_pk_bf16_f32 v64, v64, v65\n"
"v_subrev_f32 v42, s22, v42\n"
"v_mfma_f32_16x16x32_bf16 v[68:71], v[160:163], v[92:95], v[68:71]\n"
"v_cvt_pk_bf16_f32 v65, v66, v67\n"
"v_mfma_f32_16x16x32_bf16 v[68:71], v[160:163], v[244:247], v[68:71]\n"
"v_mfma_f32_16x16x32_bf16 v[68:71], v[160:163], v[156:159], v[68:71]\n"
"v_cvt_pk_bf16_f32 v25, v26, v27\n"
"v_mfma_f32_16x16x32_bf16 v[28:31], v[224:227], a[28:31], v[28:31]\n"
"v_mfma_f32_16x16x32_bf16 v[28:31], v[120:123], a[24:27], v[28:31]\n"
"v_mfma_f32_16x16x32_bf16 v[28:31], v[116:119], a[20:23], v[28:31]\n"
"v_mfma_f32_16x16x32_bf16 v[28:31], v[112:115], a[16:19], v[28:31]\n"
"s_nop 2\n"
"v_cvt_pk_bf16_f32 v66, v68, v69\n"
"v_mfma_f32_16x16x32_bf16 v[28:31], v[108:111], a[76:79], v[28:31]\n"
"s_nop 0\n"
"v_permlane16_swap_b32 v64, v66\n"
"v_mfma_f32_16x16x32_bf16 v[28:31], v[104:107], a[72:75], v[28:31]\n"
"v_cvt_pk_bf16_f32 v67, v70, v71\n"
"v_mfma_f32_16x16x32_bf16 v[28:31], v[100:103], a[68:71], v[28:31]\n"
"s_nop 0\n"
"v_permlane16_swap_b32 v65, v67\n"
"v_mfma_f32_16x16x32_bf16 v[28:31], v[96:99], a[64:67], v[28:31]\n"
"buffer_store_dwordx4 v[64:67], v253, s[36:39], 0 offen offset:128 sc0 sc1\n"
"v_mfma_f32_16x16x32_bf16 v[44:47], v[96:99], a[80:83], v[44:47]\n"
"v_mfma_f32_16x16x32_bf16 v[44:47], v[100:103], a[84:87], v[44:47]\n"
"v_subrev_f32 v43, s22, v43\n"
"v_cvt_pk_bf16_f32 v40, v40, v41\n"
"v_mfma_f32_16x16x32_bf16 v[44:47], v[104:107], a[88:91], v[44:47]\n"
"s_nop 1\n"
"v_subrev_f32 v28, s22, v28\n"
"v_subrev_f32 v30, s22, v30\n"
"v_mfma_f32_16x16x32_bf16 v[44:47], v[108:111], a[92:95], v[44:47]\n"
"v_subrev_f32 v31, s22, v31\n"
"v_cvt_pk_bf16_f32 v27, v30, v31\n"
"v_mfma_f32_16x16x32_bf16 v[44:47], v[112:115], a[32:35], v[44:47]\n"
"v_subrev_f32 v29, s22, v29\n"
"v_cvt_pk_bf16_f32 v26, v28, v29\n"
"v_mfma_f32_16x16x32_bf16 v[44:47], v[116:119], a[36:39], v[44:47]\n"
"s_nop 0\n"
"v_permlane16_swap_b32 v24, v26\n"
"v_mfma_f32_16x16x32_bf16 v[44:47], v[120:123], a[40:43], v[44:47]\n"
"v_permlane16_swap_b32 v25, v27\n"
"v_mfma_f32_16x16x32_bf16 v[44:47], v[224:227], a[44:47], v[44:47]\n"
"buffer_store_dwordx4 v[24:27], v239, s[36:39], s52 offen sc0 sc1\n"
"v_cvt_pk_bf16_f32 v41, v42, v43\n"
"s_nop 5\n"
"v_subrev_f32 v44, s22, v44\n"
"v_subrev_f32 v45, s22, v45\n"
"v_subrev_f32 v46, s22, v46\n"
"v_subrev_f32 v47, s22, v47\n"
"v_cvt_pk_bf16_f32 v42, v44, v45\n"
"v_cvt_pk_bf16_f32 v43, v46, v47\n"
"s_nop 0\n"
"v_permlane16_swap_b32 v40, v42\n"
"v_permlane16_swap_b32 v41, v43\n"
"buffer_store_dwordx4 v[40:43], v239, s[36:39], s53 offen sc0 sc1\n"
"s_branch LFIN_%=\n"
"LSW_%=:\n"
"s_add_u32 s16, s8, s95\n"
"s_addc_u32 s17, s9, 0\n"
"s_sub_u32 s18, 655360, s95\n"
"s_add_u32 s20, s8, s100\n"
"s_addc_u32 s21, s9, 0\n"
"s_sub_u32 s22, 655360, s100\n"
"s_mov_b32 s56, 0\n"
"s_mov_b32 s55, 0\n"
"s_mov_b32 s58, 0\n"
"s_mov_b32 s59, 163840\n"
"s_mov_b32 s52, 9216\n"
"s_mov_b32 s53, 18432\n"
"s_mov_b32 s54, 27648\n"
"v_lshrrev_b32 v206, 7, v242\n"
"v_mul_u32_u24 v206, 1280, v206\n"
"v_and_b32 v240, 0x7f, v240\n"
"v_add_u32 v240, v206, v240\n"
"buffer_load_dword v252, v240, s[16:19], 0 offen\n"
"s_branch LSWK_%=\n"
"LGP_%=:\n"
"s_mov_b32 s80, 0x40000\n"
"LGPa_%=:\n"
"s_sleep 1\n"
"global_load_dword v200, v201, s[6:7] sc1\n"
"s_sub_u32 s80, s80, 1\n"
"s_cmp_eq_u32 s80, 0\n"
"s_waitcnt vmcnt(0)\n"
"s_cbranch_scc1 LGPK_%=\n"
"v_subrev_u32 v200, s21, v200\n"
"v_cmp_lt_u32 vcc, s69, v200\n"
"s_nop 4\n"
"s_cbranch_vccnz LGPa_%=\n"
"s_branch LGPK_%=\n"
"LGQ_%=:\n"
"s_mov_b32 s80, 0x40000\n"
"LGQa_%=:\n"
"s_sleep 1\n"
"global_load_dword v200, v201, s[6:7] sc1\n"
"s_sub_u32 s80, s80, 1\n"
"s_cmp_eq_u32 s80, 0\n"
"s_waitcnt vmcnt(0)\n"
"s_cbranch_scc1 LGQK_%=\n"
"v_subrev_u32 v200, s21, v200\n"
"v_cmp_lt_u32 vcc, s69, v200\n"
"s_nop 4\n"
"s_cbranch_vccnz LGQa_%=\n"
"s_branch LGQK_%=\n"
"LGR_%=:\n"
"s_mov_b32 s80, 0x40000\n"
"LGRa_%=:\n"
"s_sleep 1\n"
"global_load_dword v200, v201, s[6:7] sc1\n"
"s_sub_u32 s80, s80, 1\n"
"s_cmp_eq_u32 s80, 0\n"
"s_waitcnt vmcnt(0)\n"
"s_cbranch_scc1 LGRK_%=\n"
"v_subrev_u32 v200, s21, v200\n"
"v_cmp_lt_u32 vcc, s69, v200\n"
"s_nop 4\n"
"s_cbranch_vccnz LGRa_%=\n"
"s_branch LGRK_%=\n"
"LGS_%=:\n"
"v_mov_b32 v255, s93\n"
"s_mov_b64 exec, 1\n"
"global_store_dword v255, v241, s[6:7] sc1\n"
"s_mov_b64 exec, -1\n"
"s_branch LGSK_%=\n"
"LGT_%=:\n"
"v_mov_b32 v255, s93\n"
"s_mov_b64 exec, 1\n"
"global_store_dword v255, v241, s[6:7] sc1\n"
"s_mov_b64 exec, -1\n"
"s_branch LGTK_%=\n"
"LEP_%=:\n"
"s_add_u32 s92, s92, 1\n"
"v_mov_b32 v200, s92\n"
"v_mov_b32 v255, 0\n"
"s_mov_b64 exec, 1\n"
"global_store_dword v255, v200, s[6:7] offset:128 sc1\n"
"s_mov_b64 exec, -1\n"
"s_branch LEPK_%=\n"
"LPCE0_%=:\n"
"s_memrealtime s[80:81]\n"
"s_sub_u32 s82, s67, s66\n"
"s_cmp_gt_i32 s82, 0\n"
"s_cselect_b32 s83, s67, s66\n"
"s_waitcnt lgkmcnt(0)\n"
"s_sub_u32 s82, s83, s80\n"
"s_cmp_gt_i32 s82, 3\n"
"s_cbranch_scc0 LPEE0_%=\n"
"LPDE0_%=:\n"
"s_sleep 1\n"
"s_memrealtime s[80:81]\n"
"s_waitcnt lgkmcnt(0)\n"
"s_sub_u32 s82, s83, s80\n"
"s_cmp_gt_i32 s82, 3\n"
"s_cbranch_scc1 LPDE0_%=\n"
"LPEE0_%=:\n"
"s_add_u32 s66, s66, 126\n"
"s_add_u32 s67, s80, s72\n"
"s_sub_u32 s72, s72, 0\n"
"s_max_u32 s72, s72, 122\n"
"s_branch LPKE0_%=\n"
"LPCE1_%=:\n"
"s_memrealtime s[80:81]\n"
"s_sub_u32 s82, s67, s66\n"
"s_cmp_gt_i32 s82, 0\n"
"s_cselect_b32 s83, s67, s66\n"
"s_waitcnt lgkmcnt(0)\n"
"s_sub_u32 s82, s83, s80\n"
"s_cmp_gt_i32 s82, 3\n"
"s_cbranch_scc0 LPEE1_%=\n"
"LPDE1_%=:\n"
"s_sleep 1\n"
"s_memrealtime s[80:81]\n"
"s_waitcnt lgkmcnt(0)\n"
"s_sub_u32 s82, s83, s80\n"
"s_cmp_gt_i32 s82, 3\n"
"s_cbranch_scc1 LPDE1_%=\n"
"LPEE1_%=:\n"
"s_add_u32 s66, s66, 126\n"
"s_add_u32 s67, s80, s72\n"
"s_sub_u32 s72, s72, 0\n"
"s_max_u32 s72, s72, 122\n"
"s_branch LPKE1_%=\n"
"LPCT0_%=:\n"
"s_memrealtime s[80:81]\n"
"s_sub_u32 s82, s67, s66\n"
"s_cmp_gt_i32 s82, 0\n"
"s_cselect_b32 s83, s67, s66\n"
"s_waitcnt lgkmcnt(0)\n"
"s_sub_u32 s82, s83, s80\n"
"s_cmp_gt_i32 s82, 3\n"
"s_cbranch_scc0 LPET0_%=\n"
"LPDT0_%=:\n"
"s_sleep 1\n"
"s_memrealtime s[80:81]\n"
"s_waitcnt lgkmcnt(0)\n"
"s_sub_u32 s82, s83, s80\n"
"s_cmp_gt_i32 s82, 3\n"
"s_cbranch_scc1 LPDT0_%=\n"
"LPET0_%=:\n"
"s_add_u32 s66, s66, 126\n"
"s_add_u32 s67, s80, s72\n"
"s_sub_u32 s72, s72, 0\n"
"s_max_u32 s72, s72, 122\n"
"s_branch LPKT0_%=\n"
"LPCT1_%=:\n"
"s_memrealtime s[80:81]\n"
"s_sub_u32 s82, s67, s66\n"
"s_cmp_gt_i32 s82, 0\n"
"s_cselect_b32 s83, s67, s66\n"
"s_waitcnt lgkmcnt(0)\n"
"s_sub_u32 s82, s83, s80\n"
"s_cmp_gt_i32 s82, 3\n"
"s_cbranch_scc0 LPET1_%=\n"
"LPDT1_%=:\n"
"s_sleep 1\n"
"s_memrealtime s[80:81]\n"
"s_waitcnt lgkmcnt(0)\n"
"s_sub_u32 s82, s83, s80\n"
"s_cmp_gt_i32 s82, 3\n"
"s_cbranch_scc1 LPDT1_%=\n"
"LPET1_%=:\n"
"s_add_u32 s66, s66, 126\n"
"s_add_u32 s67, s80, s72\n"
"s_sub_u32 s72, s72, 0\n"
"s_max_u32 s72, s72, 122\n"
"s_branch LPKT1_%=\n"
"LPCL0_%=:\n"
"s_memrealtime s[80:81]\n"
"s_sub_u32 s82, s67, s66\n"
"s_cmp_gt_i32 s82, 0\n"
"s_cselect_b32 s83, s67, s66\n"
"s_waitcnt lgkmcnt(0)\n"
"s_sub_u32 s82, s83, s80\n"
"s_cmp_gt_i32 s82, 3\n"
"s_cbranch_scc0 LPEL0_%=\n"
"LPDL0_%=:\n"
"s_sleep 1\n"
"s_memrealtime s[80:81]\n"
"s_waitcnt lgkmcnt(0)\n"
"s_sub_u32 s82, s83, s80\n"
"s_cmp_gt_i32 s82, 3\n"
"s_cbranch_scc1 LPDL0_%=\n"
"LPEL0_%=:\n"
"s_add_u32 s66, s66, 126\n"
"s_add_u32 s67, s80, s72\n"
"s_sub_u32 s72, s72, 0\n"
"s_max_u32 s72, s72, 122\n"
"s_branch LPKL0_%=\n"
"LPCL1_%=:\n"
"s_memrealtime s[80:81]\n"
"s_sub_u32 s82, s67, s66\n"
"s_cmp_gt_i32 s82, 0\n"
"s_cselect_b32 s83, s67, s66\n"
"s_waitcnt lgkmcnt(0)\n"
"s_sub_u32 s82, s83, s80\n"
"s_cmp_gt_i32 s82, 3\n"
"s_cbranch_scc0 LPEL1_%=\n"
"LPDL1_%=:\n"
"s_sleep 1\n"
"s_memrealtime s[80:81]\n"
"s_waitcnt lgkmcnt(0)\n"
"s_sub_u32 s82, s83, s80\n"
"s_cmp_gt_i32 s82, 3\n"
"s_cbranch_scc1 LPDL1_%=\n"
"LPEL1_%=:\n"
"s_add_u32 s66, s66, 126\n"
"s_add_u32 s67, s80, s72\n"
"s_sub_u32 s72, s72, 0\n"
"s_max_u32 s72, s72, 122\n"
"s_branch LPKL1_%=\n"
"LPCL2_%=:\n"
"s_memrealtime s[80:81]\n"
"s_sub_u32 s82, s67, s66\n"
"s_cmp_gt_i32 s82, 0\n"
"s_cselect_b32 s83, s67, s66\n"
"s_waitcnt lgkmcnt(0)\n"
"s_sub_u32 s82, s83, s80\n"
"s_cmp_gt_i32 s82, 3\n"
"s_cbranch_scc0 LPEL2_%=\n"
"LPDL2_%=:\n"
"s_sleep 1\n"
"s_memrealtime s[80:81]\n"
"s_waitcnt lgkmcnt(0)\n"
"s_sub_u32 s82, s83, s80\n"
"s_cmp_gt_i32 s82, 3\n"
"s_cbranch_scc1 LPDL2_%=\n"
"LPEL2_%=:\n"
"s_add_u32 s66, s66, 126\n"
"s_add_u32 s67, s80, s72\n"
"s_sub_u32 s72, s72, 0\n"
"s_max_u32 s72, s72, 122\n"
"s_branch LPKL2_%=\n"
"LPCL3_%=:\n"
"s_memrealtime s[80:81]\n"
"s_sub_u32 s82, s67, s66\n"
"s_cmp_gt_i32 s82, 0\n"
"s_cselect_b32 s83, s67, s66\n"
"s_waitcnt lgkmcnt(0)\n"
"s_sub_u32 s82, s83, s80\n"
"s_cmp_gt_i32 s82, 3\n"
"s_cbranch_scc0 LPEL3_%=\n"
"LPDL3_%=:\n"
"s_sleep 1\n"
"s_memrealtime s[80:81]\n"
"s_waitcnt lgkmcnt(0)\n"
"s_sub_u32 s82, s83, s80\n"
"s_cmp_gt_i32 s82, 3\n"
"s_cbranch_scc1 LPDL3_%=\n"
"LPEL3_%=:\n"
"s_add_u32 s66, s66, 126\n"
"s_add_u32 s67, s80, s72\n"
"s_sub_u32 s72, s72, 0\n"
"s_max_u32 s72, s72, 122\n"
"s_branch LPKL3_%=\n"
"LEND_%=:\n"
"LSLOW_%=:\n"
"s_waitcnt vmcnt(0) lgkmcnt(0)\n"
"s_nop 9\n"
"v_bfe_u32 v148, v242, 4, 4\n"
"v_bfe_u32 v149, v242, 8, 2\n"
"v_lshlrev_b32 v144, 8, v148\n"
"v_lshl_add_u32 v144, v149, 4, v144\n"
"v_add_u32 v144, s43, v144\n"
"v_xor_b32 v145, 64, v144\n"
"v_lshlrev_b32 v146, 1, v148\n"
"v_lshl_add_u32 v146, v149, 10, v146\n"
"v_add_u32 v146, s43, v146\n"
"v_xor_b32 v147, 64, v146\n"
"v_mov_b32 v152, 0xff7fffff\n"
"v_mov_b32 v155, 0\n"
"v_mov_b32 v0, 0\n"
"v_mov_b32 v1, 0\n"
"v_mov_b32 v2, 0\n"
"v_mov_b32 v3, 0\n"
"v_mov_b32 v4, 0\n"
"v_mov_b32 v5, 0\n"
"v_mov_b32 v6, 0\n"
"v_mov_b32 v7, 0\n"
"v_mov_b32 v8, 0\n"
"v_mov_b32 v9, 0\n"
"v_mov_b32 v10, 0\n"
"v_mov_b32 v11, 0\n"
"v_mov_b32 v12, 0\n"
"v_mov_b32 v13, 0\n"
"v_mov_b32 v14, 0\n"
"v_mov_b32 v15, 0\n"
"v_mov_b32 v16, 0\n"
"v_mov_b32 v17, 0\n"
"v_mov_b32 v18, 0\n"
"v_mov_b32 v19, 0\n"
"v_mov_b32 v20, 0\n"
"v_mov_b32 v21, 0\n"
"v_mov_b32 v22, 0\n"
"v_mov_b32 v23, 0\n"
"v_mov_b32 v24, 0\n"
"v_mov_b32 v25, 0\n"
"v_mov_b32 v26, 0\n"
"v_mov_b32 v27, 0\n"
"v_mov_b32 v28, 0\n"
"v_mov_b32 v29, 0\n"
"v_mov_b32 v30, 0\n"
"v_mov_b32 v31, 0\n"
"v_mov_b32 v156, 0xff7fffff\n"
"v_mov_b32 v159, 0\n"
"v_mov_b32 v32, 0\n"
"v_mov_b32 v33, 0\n"
"v_mov_b32 v34, 0\n"
"v_mov_b32 v35, 0\n"
"v_mov_b32 v36, 0\n"
"v_mov_b32 v37, 0\n"
"v_mov_b32 v38, 0\n"
"v_mov_b32 v39, 0\n"
"v_mov_b32 v40, 0\n"
"v_mov_b32 v41, 0\n"
"v_mov_b32 v42, 0\n"
"v_mov_b32 v43, 0\n"
"v_mov_b32 v44, 0\n"
"v_mov_b32 v45, 0\n"
"v_mov_b32 v46, 0\n"
"v_mov_b32 v47, 0\n"
"v_mov_b32 v48, 0\n"
"v_mov_b32 v49, 0\n"
"v_mov_b32 v50, 0\n"
"v_mov_b32 v51, 0\n"
"v_mov_b32 v52, 0\n"
"v_mov_b32 v53, 0\n"
"v_mov_b32 v54, 0\n"
"v_mov_b32 v55, 0\n"
"v_mov_b32 v56, 0\n"
"v_mov_b32 v57, 0\n"
"v_mov_b32 v58, 0\n"
"v_mov_b32 v59, 0\n"
"v_mov_b32 v60, 0\n"
"v_mov_b32 v61, 0\n"
"v_mov_b32 v62, 0\n"
"v_mov_b32 v63, 0\n"
"v_mov_b32 v160, 0xff7fffff\n"
"v_mov_b32 v163, 0\n"
"v_mov_b32 v64, 0\n"
"v_mov_b32 v65, 0\n"
"v_mov_b32 v66, 0\n"
"v_mov_b32 v67, 0\n"
"v_mov_b32 v68, 0\n"
"v_mov_b32 v69, 0\n"
"v_mov_b32 v70, 0\n"
"v_mov_b32 v71, 0\n"
"v_mov_b32 v72, 0\n"
"v_mov_b32 v73, 0\n"
"v_mov_b32 v74, 0\n"
"v_mov_b32 v75, 0\n"
"v_mov_b32 v76, 0\n"
"v_mov_b32 v77, 0\n"
"v_mov_b32 v78, 0\n"
"v_mov_b32 v79, 0\n"
"v_mov_b32 v80, 0\n"
"v_mov_b32 v81, 0\n"
"v_mov_b32 v82, 0\n"
"v_mov_b32 v83, 0\n"
"v_mov_b32 v84, 0\n"
"v_mov_b32 v85, 0\n"
"v_mov_b32 v86, 0\n"
"v_mov_b32 v87, 0\n"
"v_mov_b32 v88, 0\n"
"v_mov_b32 v89, 0\n"
"v_mov_b32 v90, 0\n"
"v_mov_b32 v91, 0\n"
"v_mov_b32 v92, 0\n"
"v_mov_b32 v93, 0\n"
"v_mov_b32 v94, 0\n"
"v_mov_b32 v95, 0\n"
"v_mov_b32 v164, 0xff7fffff\n"
"v_mov_b32 v167, 0\n"
"v_accvgpr_write_b32 a208, 0\n"
"v_accvgpr_write_b32 a209, 0\n"
"v_accvgpr_write_b32 a210, 0\n"
"v_accvgpr_write_b32 a211, 0\n"
"v_accvgpr_write_b32 a212, 0\n"
"v_accvgpr_write_b32 a213, 0\n"
"v_accvgpr_write_b32 a214, 0\n"
"v_accvgpr_write_b32 a215, 0\n"
"v_accvgpr_write_b32 a216, 0\n"
"v_accvgpr_write_b32 a217, 0\n"
"v_accvgpr_write_b32 a218, 0\n"
"v_accvgpr_write_b32 a219, 0\n"
"v_accvgpr_write_b32 a220, 0\n"
"v_accvgpr_write_b32 a221, 0\n"
"v_accvgpr_write_b32 a222, 0\n"
"v_accvgpr_write_b32 a223, 0\n"
"v_accvgpr_write_b32 a224, 0\n"
"v_accvgpr_write_b32 a225, 0\n"
"v_accvgpr_write_b32 a226, 0\n"
"v_accvgpr_write_b32 a227, 0\n"
"v_accvgpr_write_b32 a228, 0\n"
"v_accvgpr_write_b32 a229, 0\n"
"v_accvgpr_write_b32 a230, 0\n"
"v_accvgpr_write_b32 a231, 0\n"
"v_accvgpr_write_b32 a232, 0\n"
"v_accvgpr_write_b32 a233, 0\n"
"v_accvgpr_write_b32 a234, 0\n"
"v_accvgpr_write_b32 a235, 0\n"
"v_accvgpr_write_b32 a236, 0\n"
"v_accvgpr_write_b32 a237, 0\n"
"v_accvgpr_write_b32 a238, 0\n"
"v_accvgpr_write_b32 a239, 0\n"
"s_lshl_b32 s80, s47, 16\n"
"s_sub_u32 s80, s80, 261248\n"
"s_add_u32 s81, s80, 4096\n"
"s_mov_b32 s82, 0\n"
"LSLOOP_%=:\n"
"s_lshl_b32 s83, s82, 6\n"
"s_and_b32 s83, s83, 64\n"
"buffer_load_dwordx4 v[128:131], v144, s[84:87], s80 offen offset:0\n"
"buffer_load_dwordx4 v[132:135], v145, s[84:87], s80 offen offset:0\n"
"buffer_load_dwordx4 v[136:139], v144, s[84:87], s80 offen offset:128\n"
"buffer_load_dwordx4 v[140:143], v145, s[84:87], s80 offen offset:128\n"
"s_waitcnt vmcnt(0)\n"
"v_mfma_f32_16x16x32_bf16 v[96:99], v[128:131], a[0:3], 0\n"
"v_mfma_f32_16x16x32_bf16 v[96:99], v[132:135], a[4:7], v[96:99]\n"
"v_mfma_f32_16x16x32_bf16 v[96:99], v[136:139], a[8:11], v[96:99]\n"
"v_mfma_f32_16x16x32_bf16 v[96:99], v[140:143], a[12:15], v[96:99]\n"
"v_mfma_f32_16x16x32_bf16 v[104:107], v[128:131], a[16:19], 0\n"
"v_mfma_f32_16x16x32_bf16 v[104:107], v[132:135], a[20:23], v[104:107]\n"
"v_mfma_f32_16x16x32_bf16 v[104:107], v[136:139], a[24:27], v[104:107]\n"
"v_mfma_f32_16x16x32_bf16 v[104:107], v[140:143], a[28:31], v[104:107]\n"
"v_mfma_f32_16x16x32_bf16 v[112:115], v[128:131], a[32:35], 0\n"
"v_mfma_f32_16x16x32_bf16 v[112:115], v[132:135], a[36:39], v[112:115]\n"
"v_mfma_f32_16x16x32_bf16 v[112:115], v[136:139], a[40:43], v[112:115]\n"
"v_mfma_f32_16x16x32_bf16 v[112:115], v[140:143], a[44:47], v[112:115]\n"
"v_mfma_f32_16x16x32_bf16 v[120:123], v[128:131], a[192:195], 0\n"
"v_mfma_f32_16x16x32_bf16 v[120:123], v[132:135], a[196:199], v[120:123]\n"
"v_mfma_f32_16x16x32_bf16 v[120:123], v[136:139], a[200:203], v[120:123]\n"
"v_mfma_f32_16x16x32_bf16 v[120:123], v[140:143], a[204:207], v[120:123]\n"
"s_nop 7\n"
"buffer_load_dwordx4 v[128:131], v144, s[84:87], s81 offen offset:0\n"
"buffer_load_dwordx4 v[132:135], v145, s[84:87], s81 offen offset:0\n"
"buffer_load_dwordx4 v[136:139], v144, s[84:87], s81 offen offset:128\n"
"buffer_load_dwordx4 v[140:143], v145, s[84:87], s81 offen offset:128\n"
"s_waitcnt vmcnt(0)\n"
"v_mfma_f32_16x16x32_bf16 v[100:103], v[128:131], a[0:3], 0\n"
"v_mfma_f32_16x16x32_bf16 v[100:103], v[132:135], a[4:7], v[100:103]\n"
"v_mfma_f32_16x16x32_bf16 v[100:103], v[136:139], a[8:11], v[100:103]\n"
"v_mfma_f32_16x16x32_bf16 v[100:103], v[140:143], a[12:15], v[100:103]\n"
"v_mfma_f32_16x16x32_bf16 v[108:111], v[128:131], a[16:19], 0\n"
"v_mfma_f32_16x16x32_bf16 v[108:111], v[132:135], a[20:23], v[108:111]\n"
"v_mfma_f32_16x16x32_bf16 v[108:111], v[136:139], a[24:27], v[108:111]\n"
"v_mfma_f32_16x16x32_bf16 v[108:111], v[140:143], a[28:31], v[108:111]\n"
"v_mfma_f32_16x16x32_bf16 v[116:119], v[128:131], a[32:35], 0\n"
"v_mfma_f32_16x16x32_bf16 v[116:119], v[132:135], a[36:39], v[116:119]\n"
"v_mfma_f32_16x16x32_bf16 v[116:119], v[136:139], a[40:43], v[116:119]\n"
"v_mfma_f32_16x16x32_bf16 v[116:119], v[140:143], a[44:47], v[116:119]\n"
"v_mfma_f32_16x16x32_bf16 v[124:127], v[128:131], a[192:195], 0\n"
"v_mfma_f32_16x16x32_bf16 v[124:127], v[132:135], a[196:199], v[124:127]\n"
"v_mfma_f32_16x16x32_bf16 v[124:127], v[136:139], a[200:203], v[124:127]\n"
"v_mfma_f32_16x16x32_bf16 v[124:127], v[140:143], a[204:207], v[124:127]\n"
"s_nop 7\n"
"v_max3_f32 v153, v96, v97, v98\n"
"v_max3_f32 v153, v153, v99, v100\n"
"v_max3_f32 v153, v153, v101, v102\n"
"v_max3_f32 v153, v153, v103, v152\n"
"v_mov_b32 v154, v153\n"
"s_nop 1\n"
"v_permlane32_swap_b32 v153, v154\n"
"v_max_f32 v153, v153, v154\n"
"v_mov_b32 v154, v153\n"
"s_nop 1\n"
"v_permlane16_swap_b32 v153, v154\n"
"v_max_f32 v153, v153, v154\n"
"v_sub_f32 v154, v152, v153\n"
"v_exp_f32 v154, v154\n"
"v_mov_b32 v152, v153\n"
"v_sub_f32 v96, v96, v152\n"
"v_sub_f32 v97, v97, v152\n"
"v_sub_f32 v98, v98, v152\n"
"v_sub_f32 v99, v99, v152\n"
"v_sub_f32 v100, v100, v152\n"
"v_sub_f32 v101, v101, v152\n"
"v_sub_f32 v102, v102, v152\n"
"v_sub_f32 v103, v103, v152\n"
"v_exp_f32 v96, v96\n"
"v_exp_f32 v97, v97\n"
"v_exp_f32 v98, v98\n"
"v_exp_f32 v99, v99\n"
"v_exp_f32 v100, v100\n"
"v_exp_f32 v101, v101\n"
"v_exp_f32 v102, v102\n"
"v_exp_f32 v103, v103\n"
"v_mul_f32 v155, v155, v154\n"
"v_add_f32 v155, v155, v96\n"
"v_add_f32 v155, v155, v97\n"
"v_add_f32 v155, v155, v98\n"
"v_add_f32 v155, v155, v99\n"
"v_add_f32 v155, v155, v100\n"
"v_add_f32 v155, v155, v101\n"
"v_add_f32 v155, v155, v102\n"
"v_add_f32 v155, v155, v103\n"
"v_cvt_pk_bf16_f32 v96, v96, v97\n"
"v_cvt_pk_bf16_f32 v97, v98, v99\n"
"v_cvt_pk_bf16_f32 v98, v100, v101\n"
"v_cvt_pk_bf16_f32 v99, v102, v103\n"
"v_mov_b32_dpp v154, v154 row_ror:12 row_mask:0x2 bank_mask:0xf\n"
"s_nop 1\n"
"v_mov_b32_dpp v154, v154 row_ror:8 row_mask:0x4 bank_mask:0xf\n"
"s_nop 1\n"
"v_mov_b32_dpp v154, v154 row_ror:4 row_mask:0x8 bank_mask:0xf\n"
"s_nop 1\n"
"v_mul_f32_dpp v0, v154, v0 row_newbcast:0 row_mask:0xf bank_mask:0xf\n"
"v_mul_f32_dpp v1, v154, v1 row_newbcast:1 row_mask:0xf bank_mask:0xf\n"
"v_mul_f32_dpp v2, v154, v2 row_newbcast:2 row_mask:0xf bank_mask:0xf\n"
"v_mul_f32_dpp v3, v154, v3 row_newbcast:3 row_mask:0xf bank_mask:0xf\n"
"v_mul_f32_dpp v4, v154, v4 row_newbcast:0 row_mask:0xf bank_mask:0xf\n"
"v_mul_f32_dpp v5, v154, v5 row_newbcast:1 row_mask:0xf bank_mask:0xf\n"
"v_mul_f32_dpp v6, v154, v6 row_newbcast:2 row_mask:0xf bank_mask:0xf\n"
"v_mul_f32_dpp v7, v154, v7 row_newbcast:3 row_mask:0xf bank_mask:0xf\n"
"v_mul_f32_dpp v8, v154, v8 row_newbcast:0 row_mask:0xf bank_mask:0xf\n"
"v_mul_f32_dpp v9, v154, v9 row_newbcast:1 row_mask:0xf bank_mask:0xf\n"
"v_mul_f32_dpp v10, v154, v10 row_newbcast:2 row_mask:0xf bank_mask:0xf\n"
"v_mul_f32_dpp v11, v154, v11 row_newbcast:3 row_mask:0xf bank_mask:0xf\n"
"v_mul_f32_dpp v12, v154, v12 row_newbcast:0 row_mask:0xf bank_mask:0xf\n"
"v_mul_f32_dpp v13, v154, v13 row_newbcast:1 row_mask:0xf bank_mask:0xf\n"
"v_mul_f32_dpp v14, v154, v14 row_newbcast:2 row_mask:0xf bank_mask:0xf\n"
"v_mul_f32_dpp v15, v154, v15 row_newbcast:3 row_mask:0xf bank_mask:0xf\n"
"v_mul_f32_dpp v16, v154, v16 row_newbcast:0 row_mask:0xf bank_mask:0xf\n"
"v_mul_f32_dpp v17, v154, v17 row_newbcast:1 row_mask:0xf bank_mask:0xf\n"
"v_mul_f32_dpp v18, v154, v18 row_newbcast:2 row_mask:0xf bank_mask:0xf\n"
"v_mul_f32_dpp v19, v154, v19 row_newbcast:3 row_mask:0xf bank_mask:0xf\n"
"v_mul_f32_dpp v20, v154, v20 row_newbcast:0 row_mask:0xf bank_mask:0xf\n"
"v_mul_f32_dpp v21, v154, v21 row_newbcast:1 row_mask:0xf bank_mask:0xf\n"
"v_mul_f32_dpp v22, v154, v22 row_newbcast:2 row_mask:0xf bank_mask:0xf\n"
"v_mul_f32_dpp v23, v154, v23 row_newbcast:3 row_mask:0xf bank_mask:0xf\n"
"v_mul_f32_dpp v24, v154, v24 row_newbcast:0 row_mask:0xf bank_mask:0xf\n"
"v_mul_f32_dpp v25, v154, v25 row_newbcast:1 row_mask:0xf bank_mask:0xf\n"
"v_mul_f32_dpp v26, v154, v26 row_newbcast:2 row_mask:0xf bank_mask:0xf\n"
"v_mul_f32_dpp v27, v154, v27 row_newbcast:3 row_mask:0xf bank_mask:0xf\n"
"v_mul_f32_dpp v28, v154, v28 row_newbcast:0 row_mask:0xf bank_mask:0xf\n"
"v_mul_f32_dpp v29, v154, v29 row_newbcast:1 row_mask:0xf bank_mask:0xf\n"
"v_mul_f32_dpp v30, v154, v30 row_newbcast:2 row_mask:0xf bank_mask:0xf\n"
"v_mul_f32_dpp v31, v154, v31 row_newbcast:3 row_mask:0xf bank_mask:0xf\n"
"v_max3_f32 v157, v104, v105, v106\n"
"v_max3_f32 v157, v157, v107, v108\n"
"v_max3_f32 v157, v157, v109, v110\n"
"v_max3_f32 v157, v157, v111, v156\n"
"v_mov_b32 v158, v157\n"
"s_nop 1\n"
"v_permlane32_swap_b32 v157, v158\n"
"v_max_f32 v157, v157, v158\n"
"v_mov_b32 v158, v157\n"
"s_nop 1\n"
"v_permlane16_swap_b32 v157, v158\n"
"v_max_f32 v157, v157, v158\n"
"v_sub_f32 v158, v156, v157\n"
"v_exp_f32 v158, v158\n"
"v_mov_b32 v156, v157\n"
"v_sub_f32 v104, v104, v156\n"
"v_sub_f32 v105, v105, v156\n"
"v_sub_f32 v106, v106, v156\n"
"v_sub_f32 v107, v107, v156\n"
"v_sub_f32 v108, v108, v156\n"
"v_sub_f32 v109, v109, v156\n"
"v_sub_f32 v110, v110, v156\n"
"v_sub_f32 v111, v111, v156\n"
"v_exp_f32 v104, v104\n"
"v_exp_f32 v105, v105\n"
"v_exp_f32 v106, v106\n"
"v_exp_f32 v107, v107\n"
"v_exp_f32 v108, v108\n"
"v_exp_f32 v109, v109\n"
"v_exp_f32 v110, v110\n"
"v_exp_f32 v111, v111\n"
"v_mul_f32 v159, v159, v158\n"
"v_add_f32 v159, v159, v104\n"
"v_add_f32 v159, v159, v105\n"
"v_add_f32 v159, v159, v106\n"
"v_add_f32 v159, v159, v107\n"
"v_add_f32 v159, v159, v108\n"
"v_add_f32 v159, v159, v109\n"
"v_add_f32 v159, v159, v110\n"
"v_add_f32 v159, v159, v111\n"
"v_cvt_pk_bf16_f32 v104, v104, v105\n"
"v_cvt_pk_bf16_f32 v105, v106, v107\n"
"v_cvt_pk_bf16_f32 v106, v108, v109\n"
"v_cvt_pk_bf16_f32 v107, v110, v111\n"
"v_mov_b32_dpp v158, v158 row_ror:12 row_mask:0x2 bank_mask:0xf\n"
"s_nop 1\n"
"v_mov_b32_dpp v158, v158 row_ror:8 row_mask:0x4 bank_mask:0xf\n"
"s_nop 1\n"
"v_mov_b32_dpp v158, v158 row_ror:4 row_mask:0x8 bank_mask:0xf\n"
"s_nop 1\n"
"v_mul_f32_dpp v32, v158, v32 row_newbcast:0 row_mask:0xf bank_mask:0xf\n"
"v_mul_f32_dpp v33, v158, v33 row_newbcast:1 row_mask:0xf bank_mask:0xf\n"
"v_mul_f32_dpp v34, v158, v34 row_newbcast:2 row_mask:0xf bank_mask:0xf\n"
"v_mul_f32_dpp v35, v158, v35 row_newbcast:3 row_mask:0xf bank_mask:0xf\n"
"v_mul_f32_dpp v36, v158, v36 row_newbcast:0 row_mask:0xf bank_mask:0xf\n"
"v_mul_f32_dpp v37, v158, v37 row_newbcast:1 row_mask:0xf bank_mask:0xf\n"
"v_mul_f32_dpp v38, v158, v38 row_newbcast:2 row_mask:0xf bank_mask:0xf\n"
"v_mul_f32_dpp v39, v158, v39 row_newbcast:3 row_mask:0xf bank_mask:0xf\n"
"v_mul_f32_dpp v40, v158, v40 row_newbcast:0 row_mask:0xf bank_mask:0xf\n"
"v_mul_f32_dpp v41, v158, v41 row_newbcast:1 row_mask:0xf bank_mask:0xf\n"
"v_mul_f32_dpp v42, v158, v42 row_newbcast:2 row_mask:0xf bank_mask:0xf\n"
"v_mul_f32_dpp v43, v158, v43 row_newbcast:3 row_mask:0xf bank_mask:0xf\n"
"v_mul_f32_dpp v44, v158, v44 row_newbcast:0 row_mask:0xf bank_mask:0xf\n"
"v_mul_f32_dpp v45, v158, v45 row_newbcast:1 row_mask:0xf bank_mask:0xf\n"
"v_mul_f32_dpp v46, v158, v46 row_newbcast:2 row_mask:0xf bank_mask:0xf\n"
"v_mul_f32_dpp v47, v158, v47 row_newbcast:3 row_mask:0xf bank_mask:0xf\n"
"v_mul_f32_dpp v48, v158, v48 row_newbcast:0 row_mask:0xf bank_mask:0xf\n"
"v_mul_f32_dpp v49, v158, v49 row_newbcast:1 row_mask:0xf bank_mask:0xf\n"
"v_mul_f32_dpp v50, v158, v50 row_newbcast:2 row_mask:0xf bank_mask:0xf\n"
"v_mul_f32_dpp v51, v158, v51 row_newbcast:3 row_mask:0xf bank_mask:0xf\n"
"v_mul_f32_dpp v52, v158, v52 row_newbcast:0 row_mask:0xf bank_mask:0xf\n"
"v_mul_f32_dpp v53, v158, v53 row_newbcast:1 row_mask:0xf bank_mask:0xf\n"
"v_mul_f32_dpp v54, v158, v54 row_newbcast:2 row_mask:0xf bank_mask:0xf\n"
"v_mul_f32_dpp v55, v158, v55 row_newbcast:3 row_mask:0xf bank_mask:0xf\n"
"v_mul_f32_dpp v56, v158, v56 row_newbcast:0 row_mask:0xf bank_mask:0xf\n"
"v_mul_f32_dpp v57, v158, v57 row_newbcast:1 row_mask:0xf bank_mask:0xf\n"
"v_mul_f32_dpp v58, v158, v58 row_newbcast:2 row_mask:0xf bank_mask:0xf\n"
"v_mul_f32_dpp v59, v158, v59 row_newbcast:3 row_mask:0xf bank_mask:0xf\n"
"v_mul_f32_dpp v60, v158, v60 row_newbcast:0 row_mask:0xf bank_mask:0xf\n"
"v_mul_f32_dpp v61, v158, v61 row_newbcast:1 row_mask:0xf bank_mask:0xf\n"
"v_mul_f32_dpp v62, v158, v62 row_newbcast:2 row_mask:0xf bank_mask:0xf\n"
"v_mul_f32_dpp v63, v158, v63 row_newbcast:3 row_mask:0xf bank_mask:0xf\n"
"v_max3_f32 v161, v112, v113, v114\n"
"v_max3_f32 v161, v161, v115, v116\n"
"v_max3_f32 v161, v161, v117, v118\n"
"v_max3_f32 v161, v161, v119, v160\n"
"v_mov_b32 v162, v161\n"
"s_nop 1\n"
"v_permlane32_swap_b32 v161, v162\n"
"v_max_f32 v161, v161, v162\n"
"v_mov_b32 v162, v161\n"
"s_nop 1\n"
"v_permlane16_swap_b32 v161, v162\n"
"v_max_f32 v161, v161, v162\n"
"v_sub_f32 v162, v160, v161\n"
"v_exp_f32 v162, v162\n"
"v_mov_b32 v160, v161\n"
"v_sub_f32 v112, v112, v160\n"
"v_sub_f32 v113, v113, v160\n"
"v_sub_f32 v114, v114, v160\n"
"v_sub_f32 v115, v115, v160\n"
"v_sub_f32 v116, v116, v160\n"
"v_sub_f32 v117, v117, v160\n"
"v_sub_f32 v118, v118, v160\n"
"v_sub_f32 v119, v119, v160\n"
"v_exp_f32 v112, v112\n"
"v_exp_f32 v113, v113\n"
"v_exp_f32 v114, v114\n"
"v_exp_f32 v115, v115\n"
"v_exp_f32 v116, v116\n"
"v_exp_f32 v117, v117\n"
"v_exp_f32 v118, v118\n"
"v_exp_f32 v119, v119\n"
"v_mul_f32 v163, v163, v162\n"
"v_add_f32 v163, v163, v112\n"
"v_add_f32 v163, v163, v113\n"
"v_add_f32 v163, v163, v114\n"
"v_add_f32 v163, v163, v115\n"
"v_add_f32 v163, v163, v116\n"
"v_add_f32 v163, v163, v117\n"
"v_add_f32 v163, v163, v118\n"
"v_add_f32 v163, v163, v119\n"
"v_cvt_pk_bf16_f32 v112, v112, v113\n"
"v_cvt_pk_bf16_f32 v113, v114, v115\n"
"v_cvt_pk_bf16_f32 v114, v116, v117\n"
"v_cvt_pk_bf16_f32 v115, v118, v119\n"
"v_mov_b32_dpp v162, v162 row_ror:12 row_mask:0x2 bank_mask:0xf\n"
"s_nop 1\n"
"v_mov_b32_dpp v162, v162 row_ror:8 row_mask:0x4 bank_mask:0xf\n"
"s_nop 1\n"
"v_mov_b32_dpp v162, v162 row_ror:4 row_mask:0x8 bank_mask:0xf\n"
"s_nop 1\n"
"v_mul_f32_dpp v64, v162, v64 row_newbcast:0 row_mask:0xf bank_mask:0xf\n"
"v_mul_f32_dpp v65, v162, v65 row_newbcast:1 row_mask:0xf bank_mask:0xf\n"
"v_mul_f32_dpp v66, v162, v66 row_newbcast:2 row_mask:0xf bank_mask:0xf\n"
"v_mul_f32_dpp v67, v162, v67 row_newbcast:3 row_mask:0xf bank_mask:0xf\n"
"v_mul_f32_dpp v68, v162, v68 row_newbcast:0 row_mask:0xf bank_mask:0xf\n"
"v_mul_f32_dpp v69, v162, v69 row_newbcast:1 row_mask:0xf bank_mask:0xf\n"
"v_mul_f32_dpp v70, v162, v70 row_newbcast:2 row_mask:0xf bank_mask:0xf\n"
"v_mul_f32_dpp v71, v162, v71 row_newbcast:3 row_mask:0xf bank_mask:0xf\n"
"v_mul_f32_dpp v72, v162, v72 row_newbcast:0 row_mask:0xf bank_mask:0xf\n"
"v_mul_f32_dpp v73, v162, v73 row_newbcast:1 row_mask:0xf bank_mask:0xf\n"
"v_mul_f32_dpp v74, v162, v74 row_newbcast:2 row_mask:0xf bank_mask:0xf\n"
"v_mul_f32_dpp v75, v162, v75 row_newbcast:3 row_mask:0xf bank_mask:0xf\n"
"v_mul_f32_dpp v76, v162, v76 row_newbcast:0 row_mask:0xf bank_mask:0xf\n"
"v_mul_f32_dpp v77, v162, v77 row_newbcast:1 row_mask:0xf bank_mask:0xf\n"
"v_mul_f32_dpp v78, v162, v78 row_newbcast:2 row_mask:0xf bank_mask:0xf\n"
"v_mul_f32_dpp v79, v162, v79 row_newbcast:3 row_mask:0xf bank_mask:0xf\n"
"v_mul_f32_dpp v80, v162, v80 row_newbcast:0 row_mask:0xf bank_mask:0xf\n"
"v_mul_f32_dpp v81, v162, v81 row_newbcast:1 row_mask:0xf bank_mask:0xf\n"
"v_mul_f32_dpp v82, v162, v82 row_newbcast:2 row_mask:0xf bank_mask:0xf\n"
"v_mul_f32_dpp v83, v162, v83 row_newbcast:3 row_mask:0xf bank_mask:0xf\n"
"v_mul_f32_dpp v84, v162, v84 row_newbcast:0 row_mask:0xf bank_mask:0xf\n"
"v_mul_f32_dpp v85, v162, v85 row_newbcast:1 row_mask:0xf bank_mask:0xf\n"
"v_mul_f32_dpp v86, v162, v86 row_newbcast:2 row_mask:0xf bank_mask:0xf\n"
"v_mul_f32_dpp v87, v162, v87 row_newbcast:3 row_mask:0xf bank_mask:0xf\n"
"v_mul_f32_dpp v88, v162, v88 row_newbcast:0 row_mask:0xf bank_mask:0xf\n"
"v_mul_f32_dpp v89, v162, v89 row_newbcast:1 row_mask:0xf bank_mask:0xf\n"
"v_mul_f32_dpp v90, v162, v90 row_newbcast:2 row_mask:0xf bank_mask:0xf\n"
"v_mul_f32_dpp v91, v162, v91 row_newbcast:3 row_mask:0xf bank_mask:0xf\n"
"v_mul_f32_dpp v92, v162, v92 row_newbcast:0 row_mask:0xf bank_mask:0xf\n"
"v_mul_f32_dpp v93, v162, v93 row_newbcast:1 row_mask:0xf bank_mask:0xf\n"
"v_mul_f32_dpp v94, v162, v94 row_newbcast:2 row_mask:0xf bank_mask:0xf\n"
"v_mul_f32_dpp v95, v162, v95 row_newbcast:3 row_mask:0xf bank_mask:0xf\n"
"s_cmp_lg_u32 s83, s43\n"
"s_cbranch_scc1 LSK3A_%=\n"
"v_max3_f32 v165, v120, v121, v122\n"
"v_max3_f32 v165, v165, v123, v124\n"
"v_max3_f32 v165, v165, v125, v126\n"
"v_max3_f32 v165, v165, v127, v164\n"
"v_mov_b32 v166, v165\n"
"s_nop 1\n"
"v_permlane32_swap_b32 v165, v166\n"
"v_max_f32 v165, v165, v166\n"
"v_mov_b32 v166, v165\n"
"s_nop 1\n"
"v_permlane16_swap_b32 v165, v166\n"
"v_max_f32 v165, v165, v166\n"
"v_sub_f32 v166, v164, v165\n"
"v_exp_f32 v166, v166\n"
"v_mov_b32 v164, v165\n"
"v_sub_f32 v120, v120, v164\n"
"v_sub_f32 v121, v121, v164\n"
"v_sub_f32 v122, v122, v164\n"
"v_sub_f32 v123, v123, v164\n"
"v_sub_f32 v124, v124, v164\n"
"v_sub_f32 v125, v125, v164\n"
"v_sub_f32 v126, v126, v164\n"
"v_sub_f32 v127, v127, v164\n"
"v_exp_f32 v120, v120\n"
"v_exp_f32 v121, v121\n"
"v_exp_f32 v122, v122\n"
"v_exp_f32 v123, v123\n"
"v_exp_f32 v124, v124\n"
"v_exp_f32 v125, v125\n"
"v_exp_f32 v126, v126\n"
"v_exp_f32 v127, v127\n"
"v_mul_f32 v167, v167, v166\n"
"v_add_f32 v167, v167, v120\n"
"v_add_f32 v167, v167, v121\n"
"v_add_f32 v167, v167, v122\n"
"v_add_f32 v167, v167, v123\n"
"v_add_f32 v167, v167, v124\n"
"v_add_f32 v167, v167, v125\n"
"v_add_f32 v167, v167, v126\n"
"v_add_f32 v167, v167, v127\n"
"v_cvt_pk_bf16_f32 v120, v120, v121\n"
"v_cvt_pk_bf16_f32 v121, v122, v123\n"
"v_cvt_pk_bf16_f32 v122, v124, v125\n"
"v_cvt_pk_bf16_f32 v123, v126, v127\n"
"v_mov_b32_dpp v166, v166 row_ror:12 row_mask:0x2 bank_mask:0xf\n"
"s_nop 1\n"
"v_mov_b32_dpp v166, v166 row_ror:8 row_mask:0x4 bank_mask:0xf\n"
"s_nop 1\n"
"v_mov_b32_dpp v166, v166 row_ror:4 row_mask:0x8 bank_mask:0xf\n"
"v_accvgpr_read_b32 v124, a208\n"
"s_nop 1\n"
"v_mul_f32_dpp v124, v166, v124 row_newbcast:0 row_mask:0xf bank_mask:0xf\n"
"v_accvgpr_write_b32 a208, v124\n"
"v_accvgpr_read_b32 v125, a209\n"
"s_nop 1\n"
"v_mul_f32_dpp v125, v166, v125 row_newbcast:1 row_mask:0xf bank_mask:0xf\n"
"v_accvgpr_write_b32 a209, v125\n"
"v_accvgpr_read_b32 v126, a210\n"
"s_nop 1\n"
"v_mul_f32_dpp v126, v166, v126 row_newbcast:2 row_mask:0xf bank_mask:0xf\n"
"v_accvgpr_write_b32 a210, v126\n"
"v_accvgpr_read_b32 v127, a211\n"
"s_nop 1\n"
"v_mul_f32_dpp v127, v166, v127 row_newbcast:3 row_mask:0xf bank_mask:0xf\n"
"v_accvgpr_write_b32 a211, v127\n"
"v_accvgpr_read_b32 v124, a212\n"
"s_nop 1\n"
"v_mul_f32_dpp v124, v166, v124 row_newbcast:0 row_mask:0xf bank_mask:0xf\n"
"v_accvgpr_write_b32 a212, v124\n"
"v_accvgpr_read_b32 v125, a213\n"
"s_nop 1\n"
"v_mul_f32_dpp v125, v166, v125 row_newbcast:1 row_mask:0xf bank_mask:0xf\n"
"v_accvgpr_write_b32 a213, v125\n"
"v_accvgpr_read_b32 v126, a214\n"
"s_nop 1\n"
"v_mul_f32_dpp v126, v166, v126 row_newbcast:2 row_mask:0xf bank_mask:0xf\n"
"v_accvgpr_write_b32 a214, v126\n"
"v_accvgpr_read_b32 v127, a215\n"
"s_nop 1\n"
"v_mul_f32_dpp v127, v166, v127 row_newbcast:3 row_mask:0xf bank_mask:0xf\n"
"v_accvgpr_write_b32 a215, v127\n"
"v_accvgpr_read_b32 v124, a216\n"
"s_nop 1\n"
"v_mul_f32_dpp v124, v166, v124 row_newbcast:0 row_mask:0xf bank_mask:0xf\n"
"v_accvgpr_write_b32 a216, v124\n"
"v_accvgpr_read_b32 v125, a217\n"
"s_nop 1\n"
"v_mul_f32_dpp v125, v166, v125 row_newbcast:1 row_mask:0xf bank_mask:0xf\n"
"v_accvgpr_write_b32 a217, v125\n"
"v_accvgpr_read_b32 v126, a218\n"
"s_nop 1\n"
"v_mul_f32_dpp v126, v166, v126 row_newbcast:2 row_mask:0xf bank_mask:0xf\n"
"v_accvgpr_write_b32 a218, v126\n"
"v_accvgpr_read_b32 v127, a219\n"
"s_nop 1\n"
"v_mul_f32_dpp v127, v166, v127 row_newbcast:3 row_mask:0xf bank_mask:0xf\n"
"v_accvgpr_write_b32 a219, v127\n"
"v_accvgpr_read_b32 v124, a220\n"
"s_nop 1\n"
"v_mul_f32_dpp v124, v166, v124 row_newbcast:0 row_mask:0xf bank_mask:0xf\n"
"v_accvgpr_write_b32 a220, v124\n"
"v_accvgpr_read_b32 v125, a221\n"
"s_nop 1\n"
"v_mul_f32_dpp v125, v166, v125 row_newbcast:1 row_mask:0xf bank_mask:0xf\n"
"v_accvgpr_write_b32 a221, v125\n"
"v_accvgpr_read_b32 v126, a222\n"
"s_nop 1\n"
"v_mul_f32_dpp v126, v166, v126 row_newbcast:2 row_mask:0xf bank_mask:0xf\n"
"v_accvgpr_write_b32 a222, v126\n"
"v_accvgpr_read_b32 v127, a223\n"
"s_nop 1\n"
"v_mul_f32_dpp v127, v166, v127 row_newbcast:3 row_mask:0xf bank_mask:0xf\n"
"v_accvgpr_write_b32 a223, v127\n"
"v_accvgpr_read_b32 v124, a224\n"
"s_nop 1\n"
"v_mul_f32_dpp v124, v166, v124 row_newbcast:0 row_mask:0xf bank_mask:0xf\n"
"v_accvgpr_write_b32 a224, v124\n"
"v_accvgpr_read_b32 v125, a225\n"
"s_nop 1\n"
"v_mul_f32_dpp v125, v166, v125 row_newbcast:1 row_mask:0xf bank_mask:0xf\n"
"v_accvgpr_write_b32 a225, v125\n"
"v_accvgpr_read_b32 v126, a226\n"
"s_nop 1\n"
"v_mul_f32_dpp v126, v166, v126 row_newbcast:2 row_mask:0xf bank_mask:0xf\n"
"v_accvgpr_write_b32 a226, v126\n"
"v_accvgpr_read_b32 v127, a227\n"
"s_nop 1\n"
"v_mul_f32_dpp v127, v166, v127 row_newbcast:3 row_mask:0xf bank_mask:0xf\n"
"v_accvgpr_write_b32 a227, v127\n"
"v_accvgpr_read_b32 v124, a228\n"
"s_nop 1\n"
"v_mul_f32_dpp v124, v166, v124 row_newbcast:0 row_mask:0xf bank_mask:0xf\n"
"v_accvgpr_write_b32 a228, v124\n"
"v_accvgpr_read_b32 v125, a229\n"
"s_nop 1\n"
"v_mul_f32_dpp v125, v166, v125 row_newbcast:1 row_mask:0xf bank_mask:0xf\n"
"v_accvgpr_write_b32 a229, v125\n"
"v_accvgpr_read_b32 v126, a230\n"
"s_nop 1\n"
"v_mul_f32_dpp v126, v166, v126 row_newbcast:2 row_mask:0xf bank_mask:0xf\n"
"v_accvgpr_write_b32 a230, v126\n"
"v_accvgpr_read_b32 v127, a231\n"
"s_nop 1\n"
"v_mul_f32_dpp v127, v166, v127 row_newbcast:3 row_mask:0xf bank_mask:0xf\n"
"v_accvgpr_write_b32 a231, v127\n"
"v_accvgpr_read_b32 v124, a232\n"
"s_nop 1\n"
"v_mul_f32_dpp v124, v166, v124 row_newbcast:0 row_mask:0xf bank_mask:0xf\n"
"v_accvgpr_write_b32 a232, v124\n"
"v_accvgpr_read_b32 v125, a233\n"
"s_nop 1\n"
"v_mul_f32_dpp v125, v166, v125 row_newbcast:1 row_mask:0xf bank_mask:0xf\n"
"v_accvgpr_write_b32 a233, v125\n"
"v_accvgpr_read_b32 v126, a234\n"
"s_nop 1\n"
"v_mul_f32_dpp v126, v166, v126 row_newbcast:2 row_mask:0xf bank_mask:0xf\n"
"v_accvgpr_write_b32 a234, v126\n"
"v_accvgpr_read_b32 v127, a235\n"
"s_nop 1\n"
"v_mul_f32_dpp v127, v166, v127 row_newbcast:3 row_mask:0xf bank_mask:0xf\n"
"v_accvgpr_write_b32 a235, v127\n"
"v_accvgpr_read_b32 v124, a236\n"
"s_nop 1\n"
"v_mul_f32_dpp v124, v166, v124 row_newbcast:0 row_mask:0xf bank_mask:0xf\n"
"v_accvgpr_write_b32 a236, v124\n"
"v_accvgpr_read_b32 v125, a237\n"
"s_nop 1\n"
"v_mul_f32_dpp v125, v166, v125 row_newbcast:1 row_mask:0xf bank_mask:0xf\n"
"v_accvgpr_write_b32 a237, v125\n"
"v_accvgpr_read_b32 v126, a238\n"
"s_nop 1\n"
"v_mul_f32_dpp v126, v166, v126 row_newbcast:2 row_mask:0xf bank_mask:0xf\n"
"v_accvgpr_write_b32 a238, v126\n"
"v_accvgpr_read_b32 v127, a239\n"
"s_nop 1\n"
"v_mul_f32_dpp v127, v166, v127 row_newbcast:3 row_mask:0xf bank_mask:0xf\n"
"v_accvgpr_write_b32 a239, v127\n"
"LSK3A_%=:\n"
"s_nop 3\n"
"buffer_load_ushort v128, v146, s[88:91], s80 offen offset:0\n"
"buffer_load_ushort v129, v146, s[88:91], s80 offen offset:256\n"
"buffer_load_ushort v130, v146, s[88:91], s80 offen offset:512\n"
"buffer_load_ushort v131, v146, s[88:91], s80 offen offset:768\n"
"buffer_load_ushort v132, v146, s[88:91], s81 offen offset:0\n"
"buffer_load_ushort v133, v146, s[88:91], s81 offen offset:256\n"
"buffer_load_ushort v134, v146, s[88:91], s81 offen offset:512\n"
"buffer_load_ushort v135, v146, s[88:91], s81 offen offset:768\n"
"buffer_load_ushort v136, v146, s[88:91], s80 offen offset:32\n"
"buffer_load_ushort v137, v146, s[88:91], s80 offen offset:288\n"
"buffer_load_ushort v138, v146, s[88:91], s80 offen offset:544\n"
"buffer_load_ushort v139, v146, s[88:91], s80 offen offset:800\n"
"buffer_load_ushort v140, v146, s[88:91], s81 offen offset:32\n"
"buffer_load_ushort v141, v146, s[88:91], s81 offen offset:288\n"
"buffer_load_ushort v142, v146, s[88:91], s81 offen offset:544\n"
"buffer_load_ushort v143, v146, s[88:91], s81 offen offset:800\n"
"s_waitcnt vmcnt(0)\n"
"v_lshl_or_b32 v128, v129, 16, v128\n"
"v_lshl_or_b32 v129, v131, 16, v130\n"
"v_lshl_or_b32 v130, v133, 16, v132\n"
"v_lshl_or_b32 v131, v135, 16, v134\n"
"v_lshl_or_b32 v136, v137, 16, v136\n"
"v_lshl_or_b32 v137, v139, 16, v138\n"
"v_lshl_or_b32 v138, v141, 16, v140\n"
"v_lshl_or_b32 v139, v143, 16, v142\n"
"v_mfma_f32_16x16x32_bf16 v[0:3], v[96:99], v[128:131], v[0:3]\n"
"s_nop 0\n"
"v_mfma_f32_16x16x32_bf16 v[4:7], v[96:99], v[136:139], v[4:7]\n"
"v_mfma_f32_16x16x32_bf16 v[32:35], v[104:107], v[128:131], v[32:35]\n"
"v_mfma_f32_16x16x32_bf16 v[36:39], v[104:107], v[136:139], v[36:39]\n"
"v_mfma_f32_16x16x32_bf16 v[64:67], v[112:115], v[128:131], v[64:67]\n"
"v_mfma_f32_16x16x32_bf16 v[68:71], v[112:115], v[136:139], v[68:71]\n"
"s_cmp_lg_u32 s83, s43\n"
"s_cbranch_scc1 LSK3B0_%=\n"
"v_mfma_f32_16x16x32_bf16 a[208:211], v[120:123], v[128:131], a[208:211]\n"
"v_mfma_f32_16x16x32_bf16 a[212:215], v[120:123], v[136:139], a[212:215]\n"
"LSK3B0_%=:\n"
"s_nop 3\n"
"buffer_load_ushort v128, v147, s[88:91], s80 offen offset:0\n"
"buffer_load_ushort v129, v147, s[88:91], s80 offen offset:256\n"
"buffer_load_ushort v130, v147, s[88:91], s80 offen offset:512\n"
"buffer_load_ushort v131, v147, s[88:91], s80 offen offset:768\n"
"buffer_load_ushort v132, v147, s[88:91], s81 offen offset:0\n"
"buffer_load_ushort v133, v147, s[88:91], s81 offen offset:256\n"
"buffer_load_ushort v134, v147, s[88:91], s81 offen offset:512\n"
"buffer_load_ushort v135, v147, s[88:91], s81 offen offset:768\n"
"buffer_load_ushort v136, v147, s[88:91], s80 offen offset:32\n"
"buffer_load_ushort v137, v147, s[88:91], s80 offen offset:288\n"
"buffer_load_ushort v138, v147, s[88:91], s80 offen offset:544\n"
"buffer_load_ushort v139, v147, s[88:91], s80 offen offset:800\n"
"buffer_load_ushort v140, v147, s[88:91], s81 offen offset:32\n"
"buffer_load_ushort v141, v147, s[88:91], s81 offen offset:288\n"
"buffer_load_ushort v142, v147, s[88:91], s81 offen offset:544\n"
"buffer_load_ushort v143, v147, s[88:91], s81 offen offset:800\n"
"s_waitcnt vmcnt(0)\n"
"v_lshl_or_b32 v128, v129, 16, v128\n"
"v_lshl_or_b32 v129, v131, 16, v130\n"
"v_lshl_or_b32 v130, v133, 16, v132\n"
"v_lshl_or_b32 v131, v135, 16, v134\n"
"v_lshl_or_b32 v136, v137, 16, v136\n"
"v_lshl_or_b32 v137, v139, 16, v138\n"
"v_lshl_or_b32 v138, v141, 16, v140\n"
"v_lshl_or_b32 v139, v143, 16, v142\n"
"v_mfma_f32_16x16x32_bf16 v[8:11], v[96:99], v[128:131], v[8:11]\n"
"s_nop 0\n"
"v_mfma_f32_16x16x32_bf16 v[12:15], v[96:99], v[136:139], v[12:15]\n"
"v_mfma_f32_16x16x32_bf16 v[40:43], v[104:107], v[128:131], v[40:43]\n"
"v_mfma_f32_16x16x32_bf16 v[44:47], v[104:107], v[136:139], v[44:47]\n"
"v_mfma_f32_16x16x32_bf16 v[72:75], v[112:115], v[128:131], v[72:75]\n"
"v_mfma_f32_16x16x32_bf16 v[76:79], v[112:115], v[136:139], v[76:79]\n"
"s_cmp_lg_u32 s83, s43\n"
"s_cbranch_scc1 LSK3B1_%=\n"
"v_mfma_f32_16x16x32_bf16 a[216:219], v[120:123], v[128:131], a[216:219]\n"
"v_mfma_f32_16x16x32_bf16 a[220:223], v[120:123], v[136:139], a[220:223]\n"
"LSK3B1_%=:\n"
"s_nop 3\n"
"buffer_load_ushort v128, v146, s[88:91], s80 offen offset:128\n"
"buffer_load_ushort v129, v146, s[88:91], s80 offen offset:384\n"
"buffer_load_ushort v130, v146, s[88:91], s80 offen offset:640\n"
"buffer_load_ushort v131, v146, s[88:91], s80 offen offset:896\n"
"buffer_load_ushort v132, v146, s[88:91], s81 offen offset:128\n"
"buffer_load_ushort v133, v146, s[88:91], s81 offen offset:384\n"
"buffer_load_ushort v134, v146, s[88:91], s81 offen offset:640\n"
"buffer_load_ushort v135, v146, s[88:91], s81 offen offset:896\n"
"buffer_load_ushort v136, v146, s[88:91], s80 offen offset:160\n"
"buffer_load_ushort v137, v146, s[88:91], s80 offen offset:416\n"
"buffer_load_ushort v138, v146, s[88:91], s80 offen offset:672\n"
"buffer_load_ushort v139, v146, s[88:91], s80 offen offset:928\n"
"buffer_load_ushort v140, v146, s[88:91], s81 offen offset:160\n"
"buffer_load_ushort v141, v146, s[88:91], s81 offen offset:416\n"
"buffer_load_ushort v142, v146, s[88:91], s81 offen offset:672\n"
"buffer_load_ushort v143, v146, s[88:91], s81 offen offset:928\n"
"s_waitcnt vmcnt(0)\n"
"v_lshl_or_b32 v128, v129, 16, v128\n"
"v_lshl_or_b32 v129, v131, 16, v130\n"
"v_lshl_or_b32 v130, v133, 16, v132\n"
"v_lshl_or_b32 v131, v135, 16, v134\n"
"v_lshl_or_b32 v136, v137, 16, v136\n"
"v_lshl_or_b32 v137, v139, 16, v138\n"
"v_lshl_or_b32 v138, v141, 16, v140\n"
"v_lshl_or_b32 v139, v143, 16, v142\n"
"v_mfma_f32_16x16x32_bf16 v[16:19], v[96:99], v[128:131], v[16:19]\n"
"s_nop 0\n"
"v_mfma_f32_16x16x32_bf16 v[20:23], v[96:99], v[136:139], v[20:23]\n"
"v_mfma_f32_16x16x32_bf16 v[48:51], v[104:107], v[128:131], v[48:51]\n"
"v_mfma_f32_16x16x32_bf16 v[52:55], v[104:107], v[136:139], v[52:55]\n"
"v_mfma_f32_16x16x32_bf16 v[80:83], v[112:115], v[128:131], v[80:83]\n"
"v_mfma_f32_16x16x32_bf16 v[84:87], v[112:115], v[136:139], v[84:87]\n"
"s_cmp_lg_u32 s83, s43\n"
"s_cbranch_scc1 LSK3B2_%=\n"
"v_mfma_f32_16x16x32_bf16 a[224:227], v[120:123], v[128:131], a[224:227]\n"
"v_mfma_f32_16x16x32_bf16 a[228:231], v[120:123], v[136:139], a[228:231]\n"
"LSK3B2_%=:\n"
"s_nop 3\n"
"buffer_load_ushort v128, v147, s[88:91], s80 offen offset:128\n"
"buffer_load_ushort v129, v147, s[88:91], s80 offen offset:384\n"
"buffer_load_ushort v130, v147, s[88:91], s80 offen offset:640\n"
"buffer_load_ushort v131, v147, s[88:91], s80 offen offset:896\n"
"buffer_load_ushort v132, v147, s[88:91], s81 offen offset:128\n"
"buffer_load_ushort v133, v147, s[88:91], s81 offen offset:384\n"
"buffer_load_ushort v134, v147, s[88:91], s81 offen offset:640\n"
"buffer_load_ushort v135, v147, s[88:91], s81 offen offset:896\n"
"buffer_load_ushort v136, v147, s[88:91], s80 offen offset:160\n"
"buffer_load_ushort v137, v147, s[88:91], s80 offen offset:416\n"
"buffer_load_ushort v138, v147, s[88:91], s80 offen offset:672\n"
"buffer_load_ushort v139, v147, s[88:91], s80 offen offset:928\n"
"buffer_load_ushort v140, v147, s[88:91], s81 offen offset:160\n"
"buffer_load_ushort v141, v147, s[88:91], s81 offen offset:416\n"
"buffer_load_ushort v142, v147, s[88:91], s81 offen offset:672\n"
"buffer_load_ushort v143, v147, s[88:91], s81 offen offset:928\n"
"s_waitcnt vmcnt(0)\n"
"v_lshl_or_b32 v128, v129, 16, v128\n"
"v_lshl_or_b32 v129, v131, 16, v130\n"
"v_lshl_or_b32 v130, v133, 16, v132\n"
"v_lshl_or_b32 v131, v135, 16, v134\n"
"v_lshl_or_b32 v136, v137, 16, v136\n"
"v_lshl_or_b32 v137, v139, 16, v138\n"
"v_lshl_or_b32 v138, v141, 16, v140\n"
"v_lshl_or_b32 v139, v143, 16, v142\n"
"v_mfma_f32_16x16x32_bf16 v[24:27], v[96:99], v[128:131], v[24:27]\n"
"s_nop 0\n"
"v_mfma_f32_16x16x32_bf16 v[28:31], v[96:99], v[136:139], v[28:31]\n"
"v_mfma_f32_16x16x32_bf16 v[56:59], v[104:107], v[128:131], v[56:59]\n"
"v_mfma_f32_16x16x32_bf16 v[60:63], v[104:107], v[136:139], v[60:63]\n"
"v_mfma_f32_16x16x32_bf16 v[88:91], v[112:115], v[128:131], v[88:91]\n"
"v_mfma_f32_16x16x32_bf16 v[92:95], v[112:115], v[136:139], v[92:95]\n"
"s_cmp_lg_u32 s83, s43\n"
"s_cbranch_scc1 LSK3B3_%=\n"
"v_mfma_f32_16x16x32_bf16 a[232:235], v[120:123], v[128:131], a[232:235]\n"
"v_mfma_f32_16x16x32_bf16 a[236:239], v[120:123], v[136:139], a[236:239]\n"
"LSK3B3_%=:\n"
"s_nop 3\n"
"s_nop 9\n"
"s_add_u32 s80, s80, 8192\n"
"s_add_u32 s81, s81, 8192\n"
"s_add_u32 s82, s82, 1\n"
"s_cmp_lt_u32 s82, 32\n"
"s_cbranch_scc1 LSLOOP_%=\n"
"v_mov_b32 v154, v155\n"
"s_nop 1\n"
"v_permlane32_swap_b32 v155, v154\n"
"v_add_f32 v155, v155, v154\n"
"v_mov_b32 v154, v155\n"
"s_nop 1\n"
"v_permlane16_swap_b32 v155, v154\n"
"v_add_f32 v155, v155, v154\n"
"v_mov_b32 v196, v155\n"
"v_mov_b32 v158, v159\n"
"s_nop 1\n"
"v_permlane32_swap_b32 v159, v158\n"
"v_add_f32 v159, v159, v158\n"
"v_mov_b32 v158, v159\n"
"s_nop 1\n"
"v_permlane16_swap_b32 v159, v158\n"
"v_add_f32 v159, v159, v158\n"
"v_mov_b32 v198, v159\n"
"v_mov_b32 v162, v163\n"
"s_nop 1\n"
"v_permlane32_swap_b32 v163, v162\n"
"v_add_f32 v163, v163, v162\n"
"v_mov_b32 v162, v163\n"
"s_nop 1\n"
"v_permlane16_swap_b32 v163, v162\n"
"v_add_f32 v163, v163, v162\n"
"v_mov_b32 v200, v163\n"
"v_mov_b32 v166, v167\n"
"s_nop 1\n"
"v_permlane32_swap_b32 v167, v166\n"
"v_add_f32 v167, v167, v166\n"
"v_mov_b32 v166, v167\n"
"s_nop 1\n"
"v_permlane16_swap_b32 v167, v166\n"
"v_add_f32 v167, v167, v166\n"
"v_mov_b32 v202, v167\n"
"v_sub_f32 v192, 0, v164\n"
"v_mov_b32 v96, 0\n"
"v_mov_b32 v97, 0\n"
"v_mov_b32 v98, 0\n"
"v_mov_b32 v99, 0\n"
"v_mov_b32 v100, 0\n"
"v_mov_b32 v101, 0\n"
"v_mov_b32 v102, 0\n"
"v_mov_b32 v103, 0\n"
"v_mov_b32 v104, 0\n"
"v_mov_b32 v105, 0\n"
"v_mov_b32 v106, 0\n"
"v_mov_b32 v107, 0\n"
"v_mov_b32 v108, 0\n"
"v_mov_b32 v109, 0\n"
"v_mov_b32 v110, 0\n"
"v_mov_b32 v111, 0\n"
"v_mov_b32 v112, 0\n"
"v_mov_b32 v113, 0\n"
"v_mov_b32 v114, 0\n"
"v_mov_b32 v115, 0\n"
"v_mov_b32 v116, 0\n"
"v_mov_b32 v117, 0\n"
"v_mov_b32 v118, 0\n"
"v_mov_b32 v119, 0\n"
"v_mov_b32 v120, 0\n"
"v_mov_b32 v121, 0\n"
"v_mov_b32 v122, 0\n"
"v_mov_b32 v123, 0\n"
"v_mov_b32 v204, 0\n"
"v_mov_b32 v205, 0\n"
"s_nop 3\n"
"s_mov_b32 s68, 0\n"
"s_cmp_eq_u32 s47, 4\n"
"s_cbranch_scc1 LBACK0_%=\n"
"s_cmp_eq_u32 s47, 8\n"
"s_cbranch_scc1 LBACK1_%=\n"
"s_cmp_eq_u32 s47, 12\n"
"s_cbranch_scc1 LBACK2_%=\n"
"s_branch LBACK3_%=\n"
"LFIN_%=:\n"
    :: [kalo]"s"((unsigned)ka), [kahi]"s"((unsigned)(ka >> 32)), [wg]"s"(blockIdx.x), [wid]"s"(wid)
    : "v0","v1","v2","v3","v4","v5","v6","v7","v8","v9","v10","v11","v12","v13","v14","v15","v16","v17","v18","v19","v20","v21","v22","v23","v24","v25","v26","v27","v28","v29","v30","v31","v32","v33","v34","v35","v36","v37","v38","v39","v40","v41","v42","v43","v44","v45","v46","v47","v48","v49","v50","v51","v52","v53","v54","v55","v56","v57","v58","v59","v60","v61","v62","v63","v64","v65","v66","v67","v68","v69","v70","v71","v72","v73","v74","v75","v76","v77","v78","v79","v80","v81","v82","v83","v84","v85","v86","v87","v88","v89","v90","v91","v92","v93","v94","v95","v96","v97","v98","v99","v100","v101","v102","v103","v104","v105","v106","v107","v108","v109","v110","v111","v112","v113","v114","v115","v116","v117","v118","v119","v120","v121","v122","v123","v124","v125","v126","v127","v128","v129","v130","v131","v132","v133","v134","v135","v136","v137","v138","v139","v140","v141","v142","v143","v144","v145","v146","v147","v148","v149","v150","v151","v152","v153","v154","v155","v156","v157","v158","v159","v160","v161","v162","v163","v164","v165","v166","v167","v168","v169","v170","v171","v172","v173","v174","v175","v176","v177","v178","v179","v180","v181","v182","v183","v184","v185","v186","v187","v188","v189","v190","v191","v192","v193","v194","v195","v196","v197","v198","v199","v200","v201","v202","v203","v204","v205","v206","v207","v208","v209","v210","v211","v212","v213","v214","v215","v216","v217","v218","v219","v220","v221","v222","v223","v224","v225","v226","v227","v228","v229","v230","v231","v232","v233","v234","v235","v236","v237","v238","v239","v240","v241","v242","v243","v244","v245","v246","v247","v248","v249","v250","v251","v252","v253","v254","v255","a0","a1","a2","a3","a4","a5","a6","a7","a8","a9","a10","a11","a12","a13","a14","a15","a16","a17","a18","a19","a20","a21","a22","a23","a24","a25","a26","a27","a28","a29","a30","a31","a32","a33","a34","a35","a36","a37","a38","a39","a40","a41","a42","a43","a44","a45","a46","a47","a48","a49","a50","a51","a52","a53","a54","a55","a56","a57","a58","a59","a60","a61","a62","a63","a64","a65","a66","a67","a68","a69","a70","a71","a72","a73","a74","a75","a76","a77","a78","a79","a80","a81","a82","a83","a84","a85","a86","a87","a88","a89","a90","a91","a92","a93","a94","a95","a96","a97","a98","a99","a100","a101","a102","a103","a104","a105","a106","a107","a108","a109","a110","a111","a112","a113","a114","a115","a116","a117","a118","a119","a120","a121","a122","a123","a124","a125","a126","a127","a128","a129","a130","a131","a132","a133","a134","a135","a136","a137","a138","a139","a140","a141","a142","a143","a144","a145","a146","a147","a148","a149","a150","a151","a152","a153","a154","a155","a156","a157","a158","a159","a160","a161","a162","a163","a164","a165","a166","a167","a168","a169","a170","a171","a172","a173","a174","a175","a176","a177","a178","a179","a180","a181","a182","a183","a184","a185","a186","a187","a188","a189","a190","a191","a192","a193","a194","a195","a196","a197","a198","a199","a200","a201","a202","a203","a204","a205","a206","a207","a208","a209","a210","a211","a212","a213","a214","a215","a216","a217","a218","a219","a220","a221","a222","a223","a224","a225","a226","a227","a228","a229","a230","a231","a232","a233","a234","a235","a236","a237","a238","a239","a240","a241","a242","a243","a244","a245","a246","a247","a248","a249","a250","a251","a252","a253","a254","a255","s6","s7","s8","s9","s10","s11","s12","s13","s14","s15","s16","s17","s18","s19","s20","s21","s22","s23","s24","s25","s26","s27","s28","s29","s30","s31","s32","s33","s34","s35","s36","s37","s38","s39","s40","s41","s42","s43","s44","s45","s46","s47","s48","s49","s50","s51","s52","s53","s54","s55","s56","s57","s58","s59","s60","s61","s62","s63","s64","s65","s66","s67","s68","s69","s70","s71","s72","s73","s74","s75","s76","s77","s78","s79","s80","s81","s82","s83","s84","s85","s86","s87","s88","s89","s90","s91","s92","s93","s94","s95","s96","s97","s98","s99","s100","vcc","scc","m0","memory");
}
extern "C" void kernel_launch(void* const* d_in, const int* in_sizes, int n_in, void* d_out, int out_size, void* d_ws, size_t ws_size, hipStream_t stream) {
  char* xch = (char*)d_ws + ((ws_size - (size_t)75497472) & ~(size_t)0xfffff);
  (void)hipFuncSetAttribute((const void*)fused_attn, hipFuncAttributeMaxDynamicSharedMemorySize, 163840);
  hipLaunchKernelGGL(fused_attn, dim3(256), dim3(256), 163840, stream,
                     (const char*)d_in[0], (const char*)d_in[1], (const char*)d_in[2], (const char*)d_in[3], (const char*)d_in[4],
                     (const char*)d_in[5], (const char*)d_in[6], (const char*)d_in[7], (const char*)d_in[8], (const char*)d_in[9], xch, (char*)d_out, (char*)d_ws);
}
```

```cpp
#include <hip/hip_runtime.h>
#include <hip/hip_bf16.h>
#include <stdint.h>

__global__ __launch_bounds__(256, 1)
void fused_attn(const char* xA, const char* kA, const char* vA, const char* wA, const char* uA,
                const char* xB, const char* kB, const char* vB, const char* wB, const char* uB, char* xch, char* out, char* flags) {
  extern __shared__ __attribute__((aligned(16))) char lds[];
  const unsigned wid = __builtin_amdgcn_readfirstlane(threadIdx.x >> 6);
  const unsigned long long ka = (unsigned long long)__builtin_amdgcn_kernarg_segment_ptr();
  asm volatile(
"s_memtime s[64:65]\n"
"s_memrealtime s[66:67]\n"
"s_bfe_u32 s10, %[wg], 0x10002\n"
"s_mul_i32 s40, s10, 40\n"
"s_add_u32 s40, s40, %[kalo]\n"
"s_addc_u32 s41, %[kahi], 0\n"
"s_mov_b32 s78, %[kalo]\n"
"s_mov_b32 s79, %[kahi]\n"
"s_load_dwordx2 s[24:25], s[40:41], 0\n"
"s_load_dwordx2 s[28:29], s[40:41], 24\n"
"s_load_dwordx2 s[6:7], s[78:79], 0x60\n"
"s_load_dwordx2 s[16:17], s[40:41], 8\n"
"s_load_dwordx2 s[20:21], s[40:41], 16\n"
"s_load_dwordx2 s[8:9], s[40:41], 32\n"
"s_load_dwordx4 s[12:15], s[78:79], 0x50\n"
"s_mov_b32 s42, %[wid]\n"
"s_and_b32 s11, %[wg], 1\n"
"s_lshl_b32 s11, s11, 3\n"
"s_lshr_b32 s60, %[wg], 5\n"
"s_add_u32 s11, s11, s60\n"
"s_bfe_u32 s61, %[wg], 0x10001\n"
"s_bfe_u32 s60, %[wg], 0x20003\n"
"s_lshl2_add_u32 s61, s61, s60\n"
"s_getpc_b64 s[80:81]\n"
"LCP_%=:\n"
"s_mov_b32 s82, LEND_%=-LCP_%=\n"
"s_mov_b32 s83, 0x20000\n"
"v_mbcnt_lo_u32_b32 v2, -1, 0\n"
"v_lshlrev_b32 v2, 7, v2\n"
"s_lshr_b32 s60, %[wg], 3\n"
"s_lshl2_add_u32 s60, s60, s42\n"
"s_mul_i32 s60, s60, 640\n"
"s_mov_b64 exec, 31\n"
"buffer_load_dword v3, v2, s[80:83], s60 offen\n"
"s_mov_b64 exec, -1\n"
"s_mov_b32 s19, 0x20000\n"
"s_mov_b32 s23, 0x20000\n"
"s_mov_b32 s27, 0x20000\n"
"s_mov_b32 s31, 0x20000\n"
"s_mov_b32 s35, 0x20000\n"
"s_mov_b32 s39, 0x20000\n"
"s_mov_b32 s26, 286720\n"
"s_lshl_b32 s44, s42, 12\n"
"s_mov_b32 s50, s44\n"
"s_mul_i32 s51, s42, 7168\n"
"s_add_u32 s51, s51, 0x10000\n"
"s_mov_b32 s74, 0x8000\n"
"s_mov_b32 s58, 524288\n"
"s_mov_b32 s47, 0\n"
"s_add_u32 s60, s11, s61\n"
"s_and_b32 s60, s60, 0\n"
"s_mul_i32 s60, s60, 3\n"
"s_lshl_b32 s49, s60, 7\n"
"s_lshl_b32 s48, s60, 19\n"
"v_mbcnt_lo_u32_b32 v4, -1, 0\n"
"v_mbcnt_hi_u32_b32 v4, -1, v4\n"
"v_and_b32 v5, 15, v4\n"
"v_lshrrev_b32 v6, 4, v4\n"
"v_lshrrev_b32 v7, 3, v4\n"
"v_and_b32 v8, 7, v4\n"
"v_lshlrev_b32 v9, 1, v6\n"
"v_xor_b32 v9, v9, v8\n"
"v_lshlrev_b32 v9, 4, v9\n"
"s_mul_i32 s60, s42, 71680\n"
"v_mul_u32_u24 v10, 1280, v7\n"
"v_add3_u32 v248, v10, v9, s60\n"
"v_add_u32 v249, 9216, v248\n"
"v_add_u32 v254, 18432, v248\n"
"v_add_u32 v255, 27648, v248\n"
"v_lshl_add_u32 v240, v7, 8, v9\n"
"v_add_u32 v240, s44, v240\n"
"v_and_b32 v10, 1, v6\n"
"v_xor_b32 v10, v10, v8\n"
"v_lshlrev_b32 v10, 4, v10\n"
"v_lshl_add_u32 v10, v7, 13, v10\n"
"s_lshl_b32 s60, s42, 17\n"
"v_add_u32 v10, s60, v10\n"
"v_add_u32 v224, 1024, v10\n"
"v_add_u32 v225, 128, v10\n"
"v_xor_b32 v10, 64, v10\n"
"v_add_u32 v226, 64512, v10\n"
"v_add_u32 v227, 63616, v10\n"
"s_mul_i32 s40, s11, 286720\n"
"s_lshl_b32 s41, s61, 10\n"
"s_sub_u32 s30, 5243904, s41\n"
"s_sub_u32 s41, s41, 1024\n"
"s_ashr_i32 s60, s41, 31\n"
"s_lshl4_add_u32 s92, s10, s11\n"
"s_lshl_b32 s93, s92, 9\n"
"v_mov_b32 v0, 0\n"
"s_waitcnt lgkmcnt(0)\n"
"s_add_u32 s24, s24, s40\n"
"s_addc_u32 s25, s25, 0\n"
"s_and_b32 s25, s25, 0xffff\n"
"s_add_u32 s28, s28, s41\n"
"s_addc_u32 s29, s29, s60\n"
"s_and_b32 s29, s29, 0xffff\n"
"s_add_u32 s6, s6, s93\n"
"s_addc_u32 s7, s7, 0\n"
"buffer_load_dword v12, v248, s[24:27], s49 offen\n"
"buffer_load_dword v13, v224, s[28:31], s48 offen\n"
"global_load_dword v1, v0, s[6:7] offset:128 sc1\n"
"s_mov_b32 m0, s51\n"
"s_nop 0\n"
"buffer_load_dwordx4 v248, s[24:27], s49 offen lds\n"
"buffer_load_dwordx4 v249, s[24:27], s49 offen offset:1024 lds\n"
"buffer_load_dwordx4 v254, s[24:27], s49 offen offset:2048 lds\n"
"buffer_load_dwordx4 v255, s[24:27], s49 offen offset:3072 lds\n"
"s_add_u32 m0, s51, 4096\n"
"s_add_u32 s40, s49, 40960\n"
"buffer_load_dwordx4 v248, s[24:27], s40 offen lds\n"
"buffer_load_dwordx4 v249, s[24:27], s40 offen offset:1024 lds\n"
"buffer_load_dwordx4 v254, s[24:27], s40 offen offset:2048 lds\n"
"s_add_u32 s49, s49, 128\n"
"s_sub_u32 s41, s49, 1280\n"
"s_min_u32 s49, s49, s41\n"
"s_xor_b32 s51, s51, s74\n"
"s_mov_b32 m0, s50\n"
"s_nop 0\n"
"buffer_load_dwordx4 v224, s[28:31], s48 offen lds\n"
"buffer_load_dwordx4 v225, s[28:31], s48 offen offset:1024 lds\n"
"buffer_load_dwordx4 v226, s[28:31], s48 offen offset:2048 lds\n"
"buffer_load_dwordx4 v227, s[28:31], s48 offen offset:3072 lds\n"
"s_xor_b32 s50, s50, s74\n"
"s_lshl2_add_u32 s63, s11, s42\n"
"s_mul_i32 s63, s63, 10240\n"
"s_and_b32 s78, s42, 1\n"
"s_lshl_b32 s43, s78, 6\n"
"s_mov_b32 s77, 0x80000000\n"
"s_mov_b32 s75, 0x10000\n"
"s_mov_b32 s73, 0xffff0000\n"
"s_mov_b32 s46, 0x3e0293ee\n"
"s_mov_b32 s45, 0x63800000\n"
"s_mov_b32 s62, 9\n"
"v_lshl_add_u32 v242, v4, 4, s44\n"
"s_lshr_b32 s79, s42, 1\n"
"s_lshl4_add_u32 s76, s79, s78\n"
"s_lshl_b32 s76, s76, 10\n"
"s_add_u32 s76, s76, 0x20000\n"
"s_sub_u32 s76, s76, s44\n"
"v_lshlrev_b32 v9, 6, v6\n"
"v_xor_b32 v9, v9, v242\n"
"v_add_u32 v243, s76, v9\n"
"v_and_b32 v9, 6, v5\n"
"s_xor_b32 m0, s50, 0xc000\n"
"s_add_u32 s40, s48, 256\n"
"buffer_load_dwordx4 v224, s[28:31], s40 offen lds\n"
"v_xor_b32 v9, v9, v6\n"
"v_lshlrev_b32 v9, 4, v9\n"
"v_lshl_add_u32 v10, v5, 7, v9\n"
"s_mul_i32 s79, s42, 6144\n"
"s_add_u32 s79, s79, 0x10000\n"
"v_add_u32 v250, s79, v10\n"
"v_xor_b32 v251, 64, v250\n"
"s_lshr_b32 s79, s42, 1\n"
"s_lshl_b32 s79, s79, 11\n"
"s_add_u32 s79, s79, 90112\n"
"v_add_u32 v252, s79, v10\n"
"v_xor_b32 v253, 64, v252\n"
"s_mul_i32 s79, s78, 8256\n"
"v_lshrrev_b32 v9, 5, v4\n"
"buffer_load_dwordx4 v225, s[28:31], s40 offen offset:1024 lds\n"
"v_lshlrev_b32 v9, 11, v9\n"
"v_bfe_u32 v10, v4, 2, 3\n"
"v_lshl_add_u32 v9, v10, 7, v9\n"
"v_and_b32 v10, 3, v4\n"
"v_lshl_add_u32 v9, v10, 3, v9\n"
"v_bfe_u32 v10, v4, 3, 2\n"
"v_lshl_add_u32 v9, v10, 5, v9\n"
"s_add_u32 s79, s79, 0x10000\n"
"v_xor_b32 v232, s79, v9\n"
"v_xor_b32 v233, 32, v232\n"
"v_xor_b32 v234, 64, v232\n"
"v_xor_b32 v235, 96, v232\n"
"v_xor_b32 v236, 0x2000, v232\n"
"v_xor_b32 v237, 0x2000, v233\n"
"buffer_load_dwordx4 v226, s[28:31], s40 offen offset:2048 lds\n"
"v_xor_b32 v238, 0x2000, v234\n"
"v_xor_b32 v239, 0x2000, v235\n"
"v_lshlrev_b32 v9, 11, v6\n"
"v_bfe_u32 v10, v4, 2, 2\n"
"v_lshl_add_u32 v9, v10, 7, v9\n"
"v_and_b32 v10, 1, v4\n"
"v_lshl_add_u32 v9, v10, 3, v9\n"
"v_bfe_u32 v10, v4, 1, 1\n"
"v_lshl_add_u32 v9, v10, 5, v9\n"
"v_bfe_u32 v10, v4, 3, 1\n"
"v_lshl_add_u32 v9, v10, 4, v9\n"
"v_bfe_u32 v10, v4, 4, 1\n"
"v_lshl_add_u32 v9, v10, 6, v9\n"
"v_xor_b32 v244, s43, v9\n"
"buffer_load_dwordx4 v227, s[28:31], s40 offen offset:3072 lds\n"
"s_cmp_eq_u32 s47, 8\n"
"s_cselect_b32 s41, 524800, s58\n"
"s_add_u32 s48, s48, s41\n"
"s_sub_u32 s41, s48, 5242880\n"
"s_min_u32 s48, s48, s41\n"
"v_xor_b32 v245, 16, v244\n"
"v_xor_b32 v246, 64, v244\n"
"v_xor_b32 v247, 0x50, v244\n"
"s_lshl3_add_u32 s79, s11, s61\n"
"s_lshl_b32 s79, s79, 20\n"
"s_sub_u32 s79, s79, 896\n"
"s_ashr_i32 s60, s79, 31\n"
"s_add_u32 s16, s16, s79\n"
"s_addc_u32 s17, s17, s60\n"
"s_and_b32 s17, s17, 0xffff\n"
"s_mov_b32 s18, 1049472\n"
"s_mov_b64 s[84:85], s[16:17]\n"
"s_mov_b64 s[86:87], s[18:19]\n"
"s_add_u32 s20, s20, s79\n"
"s_addc_u32 s21, s21, s60\n"
"s_mov_b32 m0, s50\n"
"s_nop 0\n"
"buffer_load_dwordx4 v224, s[28:31], s48 offen lds\n"
"s_and_b32 s21, s21, 0xffff\n"
"s_mov_b32 s22, 1049472\n"
"s_mov_b64 s[88:89], s[20:21]\n"
"s_mov_b64 s[90:91], s[22:23]\n"
"s_lshr_b32 s57, %[wg], 3\n"
"s_mul_i32 s57, s57, 5\n"
"s_and_b32 s57, s57, 15\n"
"s_lshl_b32 s57, s57, 14\n"
"s_mov_b32 s55, 896\n"
"s_add_u32 s56, s57, 896\n"
"s_mov_b32 s59, 16384\n"
"s_mov_b32 s52, -896\n"
"s_mov_b32 s53, 0\n"
"buffer_load_dwordx4 v225, s[28:31], s48 offen offset:1024 lds\n"
"s_mov_b32 s54, -896\n"
"s_mul_i32 s79, s61, 655360\n"
"s_add_u32 s8, s8, s79\n"
"s_addc_u32 s9, s9, 0\n"
"s_and_b32 s9, s9, 0xffff\n"
"s_add_u32 s12, s12, 0x1fffff\n"
"s_addc_u32 s13, s13, 0\n"
"s_and_b32 s12, s12, 0xffe00000\n"
"s_mul_i32 s96, s42, 3\n"
"s_and_b32 s96, s96, 5\n"
"s_cmp_eq_u32 s42, 3\n"
"s_cselect_b32 s96, 5, s96\n"
"s_lshl_b32 s79, s61, 8\n"
"s_add_u32 s96, s96, s79\n"
"buffer_load_dwordx4 v226, s[28:31], s48 offen offset:2048 lds\n"
"s_add_u32 s96, s96, 160\n"
"s_lshl_b32 s96, s96, 12\n"
"s_mul_i32 s79, s92, 8388608\n"
"s_add_u32 s32, s12, s79\n"
"s_addc_u32 s33, s13, 0\n"
"s_and_b32 s33, s33, 0xffff\n"
"s_mov_b32 s34, 8388608\n"
"s_mul_i32 s79, s11, 573440\n"
"s_mul_i32 s60, s10, 1280\n"
"s_sub_u32 s38, 573440, s60\n"
"s_add_u32 s79, s79, s60\n"
"s_add_u32 s36, s14, s79\n"
"s_addc_u32 s37, s15, 0\n"
"s_and_b32 s37, s37, 0xffff\n"
"buffer_load_dwordx4 v227, s[28:31], s48 offen offset:3072 lds\n"
"s_xor_b32 s50, s50, s74\n"
"s_lshl2_add_u32 s93, s61, s42\n"
"s_add_u32 s95, s61, 1\n"
"s_and_b32 s95, s95, 7\n"
"s_lshl_b32 s95, s95, 7\n"
"s_add_u32 s100, s61, 2\n"
"s_and_b32 s100, s100, 7\n"
"s_lshl_b32 s100, s100, 7\n"
"s_lshl_b32 s60, s61, 7\n"
"s_add_u32 s60, s60, s43\n"
"s_mul_i32 s97, s42, 122880\n"
"s_add_u32 s97, s97, s60\n"
"s_lshr_b32 s98, s42, 1\n"
"s_add_u32 s98, s98, 12\n"
"s_mul_i32 s98, s98, 40960\n"
"s_mov_b32 m0, s51\n"
"s_nop 0\n"
"buffer_load_dwordx4 v248, s[24:27], s49 offen lds\n"
"s_add_u32 s98, s98, s60\n"
"s_mul_i32 s60, s93, 37\n"
"s_lshr_b32 s60, s60, 8\n"
"s_mul_i32 s79, s60, 7\n"
"s_sub_u32 s79, s93, s79\n"
"s_lshl4_add_u32 s94, s60, s79\n"
"s_lshl_b32 s94, s94, 10\n"
"s_add_u32 s94, s94, 524288\n"
"s_mul_i32 s99, s60, 3\n"
"s_lshr_b32 s78, s79, 1\n"
"s_add_u32 s99, s99, s78\n"
"s_lshr_b32 s78, s60, 1\n"
"s_add_u32 s78, s78, 12\n"
"s_cmp_eq_u32 s79, 6\n"
"s_cselect_b32 s99, s78, s99\n"
"buffer_load_dwordx4 v249, s[24:27], s49 offen offset:1024 lds\n"
"s_mul_i32 s99, s99, 40960\n"
"s_xor_b32 s79, s79, s60\n"
"s_and_b32 s79, s79, 1\n"
"s_lshl_b32 s79, s79, 6\n"
"s_add_u32 s99, s99, s79\n"
"s_add_u32 s99, s99, 1024\n"
"s_cmp_eq_u32 s61, 7\n"
"s_cselect_b32 s99, 0x40000000, s99\n"
"s_cselect_b32 s94, 0x40000000, s94\n"
"s_waitcnt vmcnt(22)\n"
"v_and_b32 v12, 0x7fff0000, v12\n"
"v_and_b32 v13, 0x7fff0000, v13\n"
"s_nop 0\n"
"buffer_load_dwordx4 v254, s[24:27], s49 offen offset:2048 lds\n"
"v_max_u32_dpp v12, v12, v12 quad_perm:[1,0,3,2] row_mask:0xf bank_mask:0xf\n"
"v_max_u32_dpp v13, v13, v13 quad_perm:[1,0,3,2] row_mask:0xf bank_mask:0xf\n"
"s_nop 0\n"
"v_max_u32_dpp v12, v12, v12 quad_perm:[2,3,0,1] row_mask:0xf bank_mask:0xf\n"
"v_max_u32_dpp v13, v13, v13 quad_perm:[2,3,0,1] row_mask:0xf bank_mask:0xf\n"
"s_nop 0\n"
"v_max_u32_dpp v12, v12, v12 row_half_mirror row_mask:0xf bank_mask:0xf\n"
"v_max_u32_dpp v13, v13, v13 row_half_mirror row_mask:0xf bank_mask:0xf\n"
"s_nop 0\n"
"v_min_u32_dpp v12, v12, v12 row_mirror row_mask:0xf bank_mask:0xf\n"
"v_min_u32_dpp v13, v13, v13 row_mirror row_mask:0xf bank_mask:0xf\n"
"s_nop 0\n"
"v_min_u32_dpp v12, v12, v12 row_bcast:15 row_mask:0xa bank_mask:0xf\n"
"v_min_u32_dpp v13, v13, v13 row_bcast:15 row_mask:0xa bank_mask:0xf\n"
"buffer_load_dwordx4 v255, s[24:27], s49 offen offset:3072 lds\n"
"s_nop 0\n"
"v_min_u32_dpp v12, v12, v12 row_bcast:31 row_mask:0xc bank_mask:0xf\n"
"v_min_u32_dpp v13, v13, v13 row_bcast:31 row_mask:0xc bank_mask:0xf\n"
"s_nop 0\n"
"v_mul_f32 v12, v12, v13\n"
"v_mul_f32 v12, 0x43c80000, v12\n"
"v_min_f32 v12, 0x71800000, v12\n"
"v_mul_f32 v13, s46, v12\n"
"s_nop 0\n"
"v_readlane_b32 s70, v12, 63\n"
"v_readlane_b32 s71, v12, 63\n"
"v_readlane_b32 s69, v13, 63\n"
"s_nop 1\n"
"v_mov_b64 v[228:229], s[70:71]\n"
"s_add_u32 m0, s51, 4096\n"
"s_add_u32 s40, s49, 40960\n"
"buffer_load_dwordx4 v248, s[24:27], s40 offen lds\n"
"v_mov_b64 v[230:231], s[70:71]\n"
"v_mov_b64 v[4:5], s[70:71]\n"
"v_mov_b64 v[6:7], s[70:71]\n"
"v_mov_b64 v[8:9], s[70:71]\n"
"v_mov_b64 v[10:11], s[70:71]\n"
"v_mov_b64 v[12:13], s[70:71]\n"
"v_mov_b64 v[14:15], s[70:71]\n"
"v_mov_b64 v[16:17], s[70:71]\n"
"v_mov_b64 v[18:19], s[70:71]\n"
"v_mov_b64 v[20:21], s[70:71]\n"
"v_mov_b64 v[22:23], s[70:71]\n"
"v_mov_b64 v[24:25], s[70:71]\n"
"v_mov_b64 v[26:27], s[70:71]\n"
"v_mov_b64 v[28:29], s[70:71]\n"
"buffer_load_dwordx4 v249, s[24:27], s40 offen offset:1024 lds\n"
"v_mov_b64 v[30:31], s[70:71]\n"
"v_mov_b64 v[32:33], s[70:71]\n"
"v_mov_b64 v[34:35], s[70:71]\n"
"v_mov_b64 v[36:37], s[70:71]\n"
"v_mov_b64 v[38:39], s[70:71]\n"
"v_mov_b64 v[40:41], s[70:71]\n"
"v_mov_b64 v[42:43], s[70:71]\n"
"v_mov_b64 v[44:45], s[70:71]\n"
"v_mov_b64 v[46:47], s[70:71]\n"
"v_mov_b64 v[48:49], s[70:71]\n"
"v_mov_b64 v[50:51], s[70:71]\n"
"v_mov_b64 v[52:53], s[70:71]\n"
"v_mov_b64 v[54:55], s[70:71]\n"
"v_mov_b64 v[56:57], s[70:71]\n"
"buffer_load_dwordx4 v254, s[24:27], s40 offen offset:2048 lds\n"
"s_add_u32 s49, s49, 128\n"
"s_sub_u32 s41, s49, 1280\n"
"s_min_u32 s49, s49, s41\n"
"s_xor_b32 s51, s51, s74\n"
"v_mov_b64 v[58:59], s[70:71]\n"
"v_mov_b64 v[60:61], s[70:71]\n"
"v_mov_b64 v[62:63], s[70:71]\n"
"v_mov_b64 v[64:65], s[70:71]\n"
"v_mov_b64 v[66:67], s[70:71]\n"
"v_mov_b64 v[68:69], s[70:71]\n"
"v_mov_b64 v[70:71], s[70:71]\n"
"v_mov_b64 v[72:73], s[70:71]\n"
"v_mov_b64 v[74:75], s[70:71]\n"
"v_mov_b64 v[76:77], s[70:71]\n"
"v_mov_b64 v[78:79], s[70:71]\n"
"v_mov_b64 v[80:81], s[70:71]\n"
"v_mov_b64 v[82:83], s[70:71]\n"
"v_mov_b64 v[84:85], s[70:71]\n"
"v_mov_b64 v[86:87], s[70:71]\n"
"v_mov_b64 v[88:89], s[70:71]\n"
"v_mov_b64 v[90:91], s[70:71]\n"
"v_mov_b64 v[92:93], s[70:71]\n"
"v_mov_b64 v[94:95], s[70:71]\n"
"v_mov_b64 v[96:97], s[70:71]\n"
"v_mov_b64 v[98:99], s[70:71]\n"
"v_mov_b64 v[100:101], s[70:71]\n"
"v_mov_b64 v[102:103], s[70:71]\n"
"v_mov_b64 v[104:105], s[70:71]\n"
"v_mov_b64 v[106:107], s[70:71]\n"
"v_mov_b64 v[108:109], s[70:71]\n"
"v_mov_b64 v[110:111], s[70:71]\n"
"v_mov_b64 v[112:113], s[70:71]\n"
"v_mov_b64 v[114:115], s[70:71]\n"
"v_mov_b64 v[116:117], s[70:71]\n"
"v_mov_b64 v[118:119], s[70:71]\n"
"v_mov_b64 v[120:121], s[70:71]\n"
"v_mov_b64 v[122:123], s[70:71]\n"
"v_mov_b64 v[124:125], s[70:71]\n"
"v_mov_b64 v[126:127], s[70:71]\n"
"v_mov_b64 v[128:129], s[70:71]\n"
"v_mov_b64 v[130:131], s[70:71]\n"
"v_mov_b64 v[132:133], s[70:71]\n"
"v_mov_b64 v[134:135], s[70:71]\n"
"v_mov_b64 v[136:137], s[70:71]\n"
"v_mov_b64 v[138:139], s[70:71]\n"
"v_mov_b64 v[140:141], s[70:71]\n"
"v_mov_b64 v[142:143], s[70:71]\n"
"v_mov_b64 v[144:145], s[70:71]\n"
"v_mov_b64 v[146:147], s[70:71]\n"
"v_mov_b64 v[148:149], s[70:71]\n"
"v_mov_b64 v[150:151], s[70:71]\n"
"v_mov_b64 v[152:153], s[70:71]\n"
"v_mov_b64 v[154:155], s[70:71]\n"
"v_mov_b64 v[156:157], s[70:71]\n"
"v_mov_b64 v[158:159], s[70:71]\n"
"v_mov_b64 v[160:161], s[70:71]\n"
"v_mov_b64 v[162:163], s[70:71]\n"
"v_mov_b64 v[164:165], s[70:71]\n"
"v_mov_b64 v[166:167], s[70:71]\n"
"v_mov_b64 v[168:169], s[70:71]\n"
"v_mov_b64 v[170:171], s[70:71]\n"
"v_mov_b64 v[172:173], s[70:71]\n"
"v_mov_b64 v[174:175], s[70:71]\n"
"s_memtime s[80:81]\n"
"s_memrealtime s[82:83]\n"
"v_mov_b64 v[176:177], s[70:71]\n"
"v_mov_b64 v[178:179], s[70:71]\n"
"v_mov_b64 v[180:181], s[70:71]\n"
"v_mov_b64 v[182:183], s[70:71]\n"
"v_mov_b64 v[184:185], s[70:71]\n"
"v_mov_b64 v[186:187], s[70:71]\n"
"v_mov_b64 v[188:189], s[70:71]\n"
"v_mov_b64 v[190:191], s[70:71]\n"
"v_mov_b64 v[192:193], s[70:71]\n"
"v_mov_b64 v[194:195], s[70:71]\n"
"v_mov_b64 v[196:197], s[70:71]\n"
"v_mov_b64 v[198:199], s[70:71]\n"
"v_mov_b64 v[200:201], s[70:71]\n"
"v_mov_b64 v[202:203], s[70:71]\n"
"v_mov_b64 v[204:205], s[70:71]\n"
"v_mov_b64 v[206:207], s[70:71]\n"
"v_mov_b64 v[208:209], s[70:71]\n"
"v_mov_b64 v[210:211], s[70:71]\n"
"v_mov_b64 v[212:213], s[70:71]\n"
"v_mov_b64 v[214:215], s[70:71]\n"
"v_mov_b64 v[216:217], s[70:71]\n"
"v_mov_b64 v[218:219], s[70:71]\n"
"v_mov_b64 v[220:221], s[70:71]\n"
"v_mov_b64 v[222:223], s[70:71]\n"
"s_waitcnt vmcnt(26)\n"
"v_readfirstlane_b32 s92, v1\n"
"v_mov_b64 v[0:1], s[70:71]\n"
"v_mov_b64 v[2:3], s[70:71]\n"
"s_mov_b32 s70, s46\n"
"s_xor_b32 s71, s69, s77\n"
"s_waitcnt lgkmcnt(0)\n"
"s_sub_u32 s80, s80, s64\n"
"s_sub_u32 s82, s82, s66\n"
"s_mul_i32 s80, s80, 10\n"
"s_mul_i32 s82, s82, 211\n"
"s_cmp_gt_u32 s80, s82\n"
"s_cselect_b32 s64, 1, 0\n"
"s_cselect_b32 s68, 655360, 0\n"
"s_cmp_eq_u32 s42, 0\n"
"s_cselect_b32 s64, s64, 0\n"
"s_mov_b32 s65, 0\n"
"s_add_u32 s66, s66, 2800\n"
"s_mov_b32 s67, s66\n"
"s_mov_b32 s72, 122\n"
"s_mov_b64 vcc, s[64:65]\n"
"s_nop 2\n"
"s_mul_i32 s60, s92, 0x9e3779b1\n"
"s_add_u32 s60, s60, 0x7f4a7c15\n"
"s_waitcnt vmcnt(15)\n"
"s_barrier\n"
"ds_read_b128 a[160:163], v250 offset:0\n"
"ds_read_b128 a[164:167], v251 offset:0\n"
"ds_read_b64_tr_b16 a[96:97], v244 offset:0\n"
"ds_read_b64_tr_b16 a[98:99], v244 offset:512\n"
"ds_read_b64_tr_b16 a[100:101], v244 offset:8192\n"
"ds_read_b64_tr_b16 a[102:103], v244 offset:8704\n"
"ds_read_b64_tr_b16 a[104:105], v245 offset:0\n"
"ds_read_b64_tr_b16 a[106:107], v245 offset:512\n"
"ds_read_b64_tr_b16 a[108:109], v245 offset:8192\n"
"ds_read_b64_tr_b16 a[110:111], v245 offset:8704\n"
"ds_read_b64_tr_b16 a[112:113], v246 offset:0\n"
"ds_read_b64_tr_b16 a[114:115], v246 offset:512\n"
"ds_read_b64_tr_b16 a[116:117], v246 offset:8192\n"
"ds_read_b64_tr_b16 a[118:119], v246 offset:8704\n"
"ds_read_b64_tr_b16 a[120:121], v247 offset:0\n"
"ds_read_b64_tr_b16 a[122:123], v247 offset:512\n"
"ds_read_b64_tr_b16 a[124:125], v247 offset:8192\n"
"ds_read_b64_tr_b16 a[126:127], v247 offset:8704\n"
"ds_read_b64_tr_b16 a[128:129], v244 offset:1024\n"
"ds_read_b64_tr_b16 a[130:131], v244 offset:1536\n"
"ds_read_b64_tr_b16 a[132:133], v244 offset:9216\n"
"ds_read_b64_tr_b16 a[134:135], v244 offset:9728\n"
"ds_read_b64_tr_b16 a[136:137], v245 offset:1024\n"
"ds_read_b64_tr_b16 a[138:139], v245 offset:1536\n"
"ds_read_b64_tr_b16 a[140:141], v245 offset:9216\n"
"ds_read_b64_tr_b16 a[142:143], v245 offset:9728\n"
"ds_read_b64_tr_b16 a[144:145], v246 offset:1024\n"
"ds_read_b64_tr_b16 a[146:147], v246 offset:1536\n"
"ds_read_b64_tr_b16 a[148:149], v246 offset:9216\n"
"ds_read_b64_tr_b16 a[150:151], v246 offset:9728\n"
"ds_read_b64_tr_b16 a[152:153], v247 offset:1024\n"
"ds_read_b64_tr_b16 a[154:155], v247 offset:1536\n"
"ds_read_b64_tr_b16 a[156:157], v247 offset:9216\n"
"ds_read_b64_tr_b16 a[158:159], v247 offset:9728\n"
"ds_read_b128 a[168:171], v250 offset:2048\n"
"ds_read_b128 a[172:175], v251 offset:2048\n"
"ds_read_b128 a[176:179], v250 offset:4096\n"
"ds_read_b128 a[180:183], v251 offset:4096\n"
"ds_read_b128 a[184:187], v252 offset:0\n"
"ds_read_b128 a[188:191], v253 offset:0\n"
"LPL_%=:\n"
"s_waitcnt lgkmcnt(15)\n"
"v_mfma_f32_16x16x32_bf16 v[112:115], a[96:99], a[160:163], v[112:115]\n"
"v_mfma_f32_16x16x32_bf16 v[112:115], a[100:103], a[164:167], v[112:115]\n"
"v_mfma_f32_16x16x32_bf16 v[116:119], a[104:107], a[160:163], v[116:119]\n"
"s_waitcnt vmcnt(11) lgkmcnt(5)\n"
"v_mfma_f32_16x16x32_bf16 v[116:119], a[108:111], a[164:167], v[116:119]\n"
"s_barrier\n"
"v_mfma_f32_16x16x32_bf16 v[120:123], a[112:115], a[160:163], v[120:123]\n"
"s_add_u32 s40, s48, 256\n"
"s_xor_b32 m0, s50, 0xc000\n"
"ds_read_b64_tr_b16 a[192:193], v244 offset:16384\n"
"v_mfma_f32_16x16x32_bf16 v[120:123], a[116:119], a[164:167], v[120:123]\n"
"buffer_load_dwordx4 v224, s[28:31], s40 offen lds\n"
"v_mfma_f32_16x16x32_bf16 v[124:127], a[120:123], a[160:163], v[124:127]\n"
"ds_read_b64_tr_b16 a[194:195], v244 offset:16896\n"
"v_mfma_f32_16x16x32_bf16 v[124:127], a[124:127], a[164:167], v[124:127]\n"
"ds_read_b64_tr_b16 a[196:197], v244 offset:24576\n"
"v_mfma_f32_16x16x32_bf16 v[128:131], a[128:131], a[160:163], v[128:131]\n"
"ds_read_b64_tr_b16 a[198:199], v244 offset:25088\n"
"v_mfma_f32_16x16x32_bf16 v[128:131], a[132:135], a[164:167], v[128:131]\n"
"ds_read_b64_tr_b16 a[200:201], v245 offset:16384\n"
"v_mfma_f32_16x16x32_bf16 v[132:135], a[136:139], a[160:163], v[132:135]\n"
"ds_read_b64_tr_b16 a[202:203], v245 offset:16896\n"
"v_mfma_f32_16x16x32_bf16 v[132:135], a[140:143], a[164:167], v[132:135]\n"
"buffer_load_dwordx4 v225, s[28:31], s40 offen offset:1024 lds\n"
"v_mfma_f32_16x16x32_bf16 v[136:139], a[144:147], a[160:163], v[136:139]\n"
"ds_read_b64_tr_b16 a[204:205], v245 offset:24576\n"
"v_mfma_f32_16x16x32_bf16 v[136:139], a[148:151], a[164:167], v[136:139]\n"
"ds_read_b64_tr_b16 a[206:207], v245 offset:25088\n"
"v_mfma_f32_16x16x32_bf16 v[140:143], a[152:155], a[160:163], v[140:143]\n"
"ds_read_b64_tr_b16 a[208:209], v246 offset:16384\n"
"v_mfma_f32_16x16x32_bf16 v[140:143], a[156:159], a[164:167], v[140:143]\n"
"s_waitcnt lgkmcnt(6)\n"
"ds_read_b64_tr_b16 a[210:211], v246 offset:16896\n"
"v_mfma_f32_16x16x32_bf16 v[172:175], a[152:155], a[168:171], v[172:175]\n"
"s_waitcnt lgkmcnt(6)\n"
"ds_read_b64_tr_b16 a[212:213], v246 offset:24576\n"
"v_mfma_f32_16x16x32_bf16 v[172:175], a[156:159], a[172:175], v[172:175]\n"
"buffer_load_dwordx4 v226, s[28:31], s40 offen offset:2048 lds\n"
"v_mfma_f32_16x16x32_bf16 v[168:171], a[144:147], a[168:171], v[168:171]\n"
"ds_read_b64_tr_b16 a[214:215], v246 offset:25088\n"
"v_mfma_f32_16x16x32_bf16 v[168:171], a[148:151], a[172:175], v[168:171]\n"
"ds_read_b64_tr_b16 a[216:217], v247 offset:16384\n"
"v_mfma_f32_16x16x32_bf16 v[164:167], a[136:139], a[168:171], v[164:167]\n"
"ds_read_b64_tr_b16 a[218:219], v247 offset:16896\n"
"v_mfma_f32_16x16x32_bf16 v[164:167], a[140:143], a[172:175], v[164:167]\n"
"ds_read_b64_tr_b16 a[220:221], v247 offset:24576\n"
"v_mfma_f32_16x16x32_bf16 v[160:163], a[128:131], a[168:171], v[160:163]\n"
"ds_read_b64_tr_b16 a[222:223], v247 offset:25088\n"
"v_mfma_f32_16x16x32_bf16 v[160:163], a[132:135], a[172:175], v[160:163]\n"
"buffer_load_dwordx4 v227, s[28:31], s40 offen offset:3072 lds\n"
"v_mfma_f32_16x16x32_bf16 v[156:159], a[120:123], a[168:171], v[156:159]\n"
"s_cmp_eq_u32 s47, 8\n"
"ds_read_b64_tr_b16 a[224:225], v244 offset:17408\n"
"v_mfma_f32_16x16x32_bf16 v[156:159], a[124:127], a[172:175], v[156:159]\n"
"s_cselect_b32 s41, 524800, s58\n"
"ds_read_b64_tr_b16 a[226:227], v244 offset:17920\n"
"v_mfma_f32_16x16x32_bf16 v[152:155], a[112:115], a[168:171], v[152:155]\n"
"s_add_u32 s48, s48, s41\n"
"ds_read_b64_tr_b16 a[228:229], v244 offset:25600\n"
"v_mfma_f32_16x16x32_bf16 v[152:155], a[116:119], a[172:175], v[152:155]\n"
"s_sub_u32 s41, s48, 5242880\n"
"ds_read_b64_tr_b16 a[230:231], v244 offset:26112\n"
"v_mfma_f32_16x16x32_bf16 v[148:151], a[104:107], a[168:171], v[148:151]\n"
"s_min_u32 s48, s48, s41\n"
"ds_read_b64_tr_b16 a[232:233], v245 offset:17408\n"
"v_mfma_f32_16x16x32_bf16 v[148:151], a[108:111], a[172:175], v[148:151]\n"
"ds_read_b64_tr_b16 a[234:235], v245 offset:17920\n"
"v_xor_b32 v244, s74, v244\n"
"v_mfma_f32_16x16x32_bf16 v[144:147], a[96:99], a[168:171], v[144:147]\n"
"s_mov_b32 m0, s50\n"
"ds_read_b64_tr_b16 a[236:237], v245 offset:25600\n"
"v_xor_b32 v250, s74, v250\n"
"v_mfma_f32_16x16x32_bf16 v[144:147], a[100:103], a[172:175], v[144:147]\n"
"buffer_load_dwordx4 v224, s[28:31], s48 offen lds\n"
"v_mfma_f32_16x16x32_bf16 v[176:179], a[96:99], a[176:179], v[176:179]\n"
"s_waitcnt lgkmcnt(6)\n"
"ds_read_b64_tr_b16 a[238:239], v245 offset:26112\n"
"v_xor_b32 v251, s74, v251\n"
"v_mfma_f32_16x16x32_bf16 v[176:179], a[100:103], a[180:183], v[176:179]\n"
"ds_read_b64_tr_b16 a[240:241], v246 offset:17408\n"
"v_xor_b32 v245, s74, v245\n"
"v_mfma_f32_16x16x32_bf16 v[180:183], a[104:107], a[176:179], v[180:183]\n"
"ds_read_b64_tr_b16 a[242:243], v246 offset:17920\n"
"v_xor_b32 v252, s74, v252\n"
"v_mfma_f32_16x16x32_bf16 v[180:183], a[108:111], a[180:183], v[180:183]\n"
"buffer_load_dwordx4 v225, s[28:31], s48 offen offset:1024 lds\n"
"v_mfma_f32_16x16x32_bf16 v[184:187], a[112:115], a[176:179], v[184:187]\n"
"ds_read_b64_tr_b16 a[244:245], v246 offset:25600\n"
"v_xor_b32 v253, s74, v253\n"
"v_mfma_f32_16x16x32_bf16 v[184:187], a[116:119], a[180:183], v[184:187]\n"
"ds_read_b64_tr_b16 a[246:247], v246 offset:26112\n"
"v_xor_b32 v246, s74, v246\n"
"v_mfma_f32_16x16x32_bf16 v[188:191], a[120:123], a[176:179], v[188:191]\n"
"ds_read_b64_tr_b16 a[248:249], v247 offset:17408\n"
"v_mfma_f32_16x16x32_bf16 v[188:191], a[124:127], a[180:183], v[188:191]\n"
"ds_read_b64_tr_b16 a[250:251], v247 offset:17920\n"
"v_mfma_f32_16x16x32_bf16 v[200:203], a[144:147], a[176:179], v[200:203]\n"
"ds_read_b64_tr_b16 a[252:253], v247 offset:25600\n"
"v_mfma_f32_16x16x32_bf16 v[200:203], a[148:151], a[180:183], v[200:203]\n"
"buffer_load_dwordx4 v226, s[28:31], s48 offen offset:2048 lds\n"
"v_mfma_f32_16x16x32_bf16 v[204:207], a[152:155], a[176:179], v[204:207]\n"
"ds_read_b64_tr_b16 a[254:255], v247 offset:26112\n"
"v_xor_b32 v247, s74, v247\n"
"v_mfma_f32_16x16x32_bf16 v[204:207], a[156:159], a[180:183], v[204:207]\n"
"v_mfma_f32_16x16x32_bf16 v[192:195], a[128:131], a[176:179], v[192:195]\n"
"v_mfma_f32_16x16x32_bf16 v[192:195], a[132:135], a[180:183], v[192:195]\n"
"v_mfma_f32_16x16x32_bf16 v[196:199], a[136:139], a[176:179], v[196:199]\n"
"v_mfma_f32_16x16x32_bf16 v[196:199], a[140:143], a[180:183], v[196:199]\n"
"buffer_load_dwordx4 v227, s[28:31], s48 offen offset:3072 lds\n"
"v_mfma_f32_16x16x32_bf16 v[220:223], a[136:139], a[184:187], v[220:223]\n"
"s_xor_b32 s50, s50, s74\n"
"v_mfma_f32_16x16x32_bf16 v[220:223], a[140:143], a[188:191], v[220:223]\n"
"v_mfma_f32_16x16x32_bf16 v[216:219], a[128:131], a[184:187], v[216:219]\n"
"v_mfma_f32_16x16x32_bf16 v[216:219], a[132:135], a[188:191], v[216:219]\n"
"v_mfma_f32_16x16x32_bf16 v[212:215], a[104:107], a[184:187], v[212:215]\n"
"v_mfma_f32_16x16x32_bf16 v[212:215], a[108:111], a[188:191], v[212:215]\n"
"v_mfma_f32_16x16x32_bf16 v[208:211], a[96:99], a[184:187], v[208:211]\n"
"v_mfma_f32_16x16x32_bf16 v[208:211], a[100:103], a[188:191], v[208:211]\n"
"v_mfma_f32_16x16x32_bf16 v[0:3], a[192:195], a[160:163], v[0:3]\n"
"s_waitcnt lgkmcnt(0)\n"
"v_mfma_f32_16x16x32_bf16 v[0:3], a[196:199], a[164:167], v[0:3]\n"
"s_waitcnt lgkmcnt(0)\n"
"v_mfma_f32_16x16x32_bf16 v[4:7], a[200:203], a[160:163], v[4:7]\n"
"s_waitcnt vmcnt(8) lgkmcnt(0)\n"
"v_mfma_f32_16x16x32_bf16 v[4:7], a[204:207], a[164:167], v[4:7]\n"
"s_barrier\n"
"v_mfma_f32_16x16x32_bf16 v[8:11], a[208:211], a[160:163], v[8:11]\n"
"s_mov_b32 m0, s51\n"
"ds_read_b64_tr_b16 a[96:97], v244 offset:0\n"
"v_mfma_f32_16x16x32_bf16 v[8:11], a[212:215], a[164:167], v[8:11]\n"
"buffer_load_dwordx4 v248, s[24:27], s49 offen lds\n"
"v_mfma_f32_16x16x32_bf16 v[12:15], a[216:219], a[160:163], v[12:15]\n"
"ds_read_b64_tr_b16 a[98:99], v244 offset:512\n"
"v_mfma_f32_16x16x32_bf16 v[12:15], a[220:223], a[164:167], v[12:15]\n"
"ds_read_b64_tr_b16 a[100:101], v244 offset:8192\n"
"v_mfma_f32_16x16x32_bf16 v[16:19], a[224:227], a[160:163], v[16:19]\n"
"ds_read_b64_tr_b16 a[102:103], v244 offset:8704\n"
"v_mfma_f32_16x16x32_bf16 v[16:19], a[228:231], a[164:167], v[16:19]\n"
"ds_read_b64_tr_b16 a[104:105], v245 offset:0\n"
"v_mfma_f32_16x16x32_bf16 v[20:23], a[232:235], a[160:163], v[20:23]\n"
"ds_read_b64_tr_b16 a[106:107], v245 offset:512\n"
"v_mfma_f32_16x16x32_bf16 v[20:23], a[236:239], a[164:167], v[20:23]\n"
"buffer_load_dwordx4 v249, s[24:27], s49 offen offset:1024 lds\n"
"v_mfma_f32_16x16x32_bf16 v[24:27], a[240:243], a[160:163], v[24:27]\n"
"ds_read_b64_tr_b16 a[108:109], v245 offset:8192\n"
"v_mfma_f32_16x16x32_bf16 v[24:27], a[244:247], a[164:167], v[24:27]\n"
"ds_read_b64_tr_b16 a[110:111], v245 offset:8704\n"
"v_mfma_f32_16x16x32_bf16 v[28:31], a[248:251], a[160:163], v[28:31]\n"
"ds_read_b128 a[160:163], v250 offset:0\n"
"v_mfma_f32_16x16x32_bf16 v[28:31], a[252:255], a[164:167], v[28:31]\n"
"ds_read_b128 a[164:167], v251 offset:0\n"
"v_mfma_f32_16x16x32_bf16 v[60:63], a[248:251], a[168:171], v[60:63]\n"
"ds_read_b64_tr_b16 a[112:113], v246 offset:0\n"
"v_mfma_f32_16x16x32_bf16 v[60:63], a[252:255], a[172:175], v[60:63]\n"
"buffer_load_dwordx4 v254, s[24:27], s49 offen offset:2048 lds\n"
"v_mfma_f32_16x16x32_bf16 v[56:59], a[240:243], a[168:171], v[56:59]\n"
"ds_read_b64_tr_b16 a[114:115], v246 offset:512\n"
"v_mfma_f32_16x16x32_bf16 v[56:59], a[244:247], a[172:175], v[56:59]\n"
"ds_read_b64_tr_b16 a[116:117], v246 offset:8192\n"
"v_mfma_f32_16x16x32_bf16 v[52:55], a[232:235], a[168:171], v[52:55]\n"
"ds_read_b64_tr_b16 a[118:119], v246 offset:8704\n"
"v_mfma_f32_16x16x32_bf16 v[52:55], a[236:239], a[172:175], v[52:55]\n"
"ds_read_b64_tr_b16 a[120:121], v247 offset:0\n"
"v_mfma_f32_16x16x32_bf16 v[48:51], a[224:227], a[168:171], v[48:51]\n"
"ds_read_b64_tr_b16 a[122:123], v247 offset:512\n"
"v_mfma_f32_16x16x32_bf16 v[48:51], a[228:231], a[172:175], v[48:51]\n"
"buffer_load_dwordx4 v255, s[24:27], s49 offen offset:3072 lds\n"
"v_mfma_f32_16x16x32_bf16 v[44:47], a[216:219], a[168:171], v[44:47]\n"
"ds_read_b64_tr_b16 a[124:125], v247 offset:8192\n"
"v_mfma_f32_16x16x32_bf16 v[44:47], a[220:223], a[172:175], v[44:47]\n"
"ds_read_b64_tr_b16 a[126:127], v247 offset:8704\n"
"v_mfma_f32_16x16x32_bf16 v[40:43], a[208:211], a[168:171], v[40:43]\n"
"ds_read_b64_tr_b16 a[128:129], v244 offset:1024\n"
"v_mfma_f32_16x16x32_bf16 v[40:43], a[212:215], a[172:175], v[40:43]\n"
"s_add_u32 s40, s49, 40960\n"
"ds_read_b64_tr_b16 a[130:131], v244 offset:1536\n"
"v_mfma_f32_16x16x32_bf16 v[36:39], a[200:203], a[168:171], v[36:39]\n"
"s_add_u32 m0, s51, 4096\n"
"ds_read_b64_tr_b16 a[132:133], v244 offset:9216\n"
"v_mfma_f32_16x16x32_bf16 v[36:39], a[204:207], a[172:175], v[36:39]\n"
"buffer_load_dwordx4 v248, s[24:27], s40 offen lds\n"
"v_mfma_f32_16x16x32_bf16 v[32:35], a[192:195], a[168:171], v[32:35]\n"
"ds_read_b64_tr_b16 a[134:135], v244 offset:9728\n"
"v_mfma_f32_16x16x32_bf16 v[32:35], a[196:199], a[172:175], v[32:35]\n"
"ds_read_b64_tr_b16 a[136:137], v245 offset:1024\n"
"v_mfma_f32_16x16x32_bf16 v[64:67], a[192:195], a[176:179], v[64:67]\n"
"ds_read_b64_tr_b16 a[138:139], v245 offset:1536\n"
"v_mfma_f32_16x16x32_bf16 v[64:67], a[196:199], a[180:183], v[64:67]\n"
"ds_read_b64_tr_b16 a[140:141], v245 offset:9216\n"
"v_mfma_f32_16x16x32_bf16 v[68:71], a[200:203], a[176:179], v[68:71]\n"
"ds_read_b64_tr_b16 a[142:143], v245 offset:9728\n"
"v_mfma_f32_16x16x32_bf16 v[68:71], a[204:207], a[180:183], v[68:71]\n"
"buffer_load_dwordx4 v249, s[24:27], s40 offen offset:1024 lds\n"
"v_mfma_f32_16x16x32_bf16 v[72:75], a[208:211], a[176:179], v[72:75]\n"
"ds_read_b64_tr_b16 a[144:145], v246 offset:1024\n"
"v_mfma_f32_16x16x32_bf16 v[72:75], a[212:215], a[180:183], v[72:75]\n"
"ds_read_b64_tr_b16 a[146:147], v246 offset:1536\n"
"v_mfma_f32_16x16x32_bf16 v[76:79], a[216:219], a[176:179], v[76:79]\n"
"ds_read_b64_tr_b16 a[148:149], v246 offset:9216\n"
"v_mfma_f32_16x16x32_bf16 v[76:79], a[220:223], a[180:183], v[76:79]\n"
"ds_read_b64_tr_b16 a[150:151], v246 offset:9728\n"
"v_mfma_f32_16x16x32_bf16 v[88:91], a[240:243], a[176:179], v[88:91]\n"
"ds_read_b64_tr_b16 a[152:153], v247 offset:1024\n"
"v_mfma_f32_16x16x32_bf16 v[88:91], a[244:247], a[180:183], v[88:91]\n"
"buffer_load_dwordx4 v254, s[24:27], s40 offen offset:2048 lds\n"
"v_mfma_f32_16x16x32_bf16 v[92:95], a[248:251], a[176:179], v[92:95]\n"
"s_add_u32 s49, s49, 128\n"
"ds_read_b64_tr_b16 a[154:155], v247 offset:1536\n"
"v_mfma_f32_16x16x32_bf16 v[92:95], a[252:255], a[180:183], v[92:95]\n"
"s_sub_u32 s41, s49, 1280\n"
"ds_read_b64_tr_b16 a[156:157], v247 offset:9216\n"
"v_mfma_f32_16x16x32_bf16 v[80:83], a[224:227], a[176:179], v[80:83]\n"
"s_min_u32 s49, s49, s41\n"
"ds_read_b64_tr_b16 a[158:159], v247 offset:9728\n"
"v_mfma_f32_16x16x32_bf16 v[80:83], a[228:231], a[180:183], v[80:83]\n"
"s_xor_b32 s51, s51, s74\n"
"ds_read_b128 a[168:171], v250 offset:2048\n"
"v_mfma_f32_16x16x32_bf16 v[84:87], a[232:235], a[176:179], v[84:87]\n"
"ds_read_b128 a[172:175], v251 offset:2048\n"
"v_mfma_f32_16x16x32_bf16 v[84:87], a[236:239], a[180:183], v[84:87]\n"
"ds_read_b128 a[176:179], v250 offset:4096\n"
"v_mfma_f32_16x16x32_bf16 v[108:111], a[232:235], a[184:187], v[108:111]\n"
"ds_read_b128 a[180:183], v251 offset:4096\n"
"v_mfma_f32_16x16x32_bf16 v[108:111], a[236:239], a[188:191], v[108:111]\n"
"v_mfma_f32_16x16x32_bf16 v[104:107], a[224:227], a[184:187], v[104:107]\n"
"v_mfma_f32_16x16x32_bf16 v[104:107], a[228:231], a[188:191], v[104:107]\n"
"v_mfma_f32_16x16x32_bf16 v[100:103], a[200:203], a[184:187], v[100:103]\n"
"v_mfma_f32_16x16x32_bf16 v[100:103], a[204:207], a[188:191], v[100:103]\n"
"v_mfma_f32_16x16x32_bf16 v[96:99], a[192:195], a[184:187], v[96:99]\n"
"s_add_u32 s47, s47, 1\n"
"ds_read_b128 a[184:187], v252 offset:0\n"
"v_mfma_f32_16x16x32_bf16 v[96:99], a[196:199], a[188:191], v[96:99]\n"
"s_waitcnt lgkmcnt(2)\n"
"s_cmp_lg_u32 s47, s62\n"
"ds_read_b128 a[188:191], v253 offset:0\n"
"s_cbranch_scc1 LPL_%=\n"
"s_cmp_eq_u32 s47, 18\n"
"s_cbranch_scc1 LPT2_%=\n"
"v_mfma_f32_16x16x32_bf16 v[112:115], a[96:99], a[160:163], v[112:115]\n"
"s_add_u32 s80, s96, 8192\n"
"v_mov_b32 v241, 1\n"
"v_mfma_f32_16x16x32_bf16 v[112:115], a[100:103], a[164:167], v[112:115]\n"
"s_mov_b64 exec, 1\n"
"buffer_store_dword v241, off, s[32:35], s96 sc0 sc1\n"
"buffer_store_dword v241, off, s[32:35], s80 sc0 sc1\n"
"s_mov_b64 exec, -1\n"
"v_mfma_f32_16x16x32_bf16 v[116:119], a[104:107], a[160:163], v[116:119]\n"
"s_waitcnt vmcnt(13)\n"
"v_mfma_f32_16x16x32_bf16 v[116:119], a[108:111], a[164:167], v[116:119]\n"
"s_barrier\n"
"v_mfma_f32_16x16x32_bf16 v[120:123], a[112:115], a[160:163], v[120:123]\n"
"s_add_u32 s40, s48, 256\n"
"s_xor_b32 m0, s50, 0xc000\n"
"ds_read_b64_tr_b16 a[232:233], v245 offset:17408\n"
"v_mfma_f32_16x16x32_bf16 v[120:123], a[116:119], a[164:167], v[120:123]\n"
"buffer_load_dwordx4 v224, s[28:31], s40 offen lds\n"
"v_mfma_f32_16x16x32_bf16 v[124:127], a[120:123], a[160:163], v[124:127]\n"
"ds_read_b64_tr_b16 a[234:235], v245 offset:17920\n"
"v_mfma_f32_16x16x32_bf16 v[124:127], a[124:127], a[164:167], v[124:127]\n"
"ds_read_b64_tr_b16 a[236:237], v245 offset:25600\n"
"v_mfma_f32_16x16x32_bf16 v[128:131], a[128:131], a[160:163], v[128:131]\n"
"ds_read_b64_tr_b16 a[238:239], v245 offset:26112\n"
"v_mfma_f32_16x16x32_bf16 v[128:131], a[132:135], a[164:167], v[128:131]\n"
"ds_read_b64_tr_b16 a[224:225], v244 offset:17408\n"
"v_mfma_f32_16x16x32_bf16 v[132:135], a[136:139], a[160:163], v[132:135]\n"
"ds_read_b64_tr_b16 a[226:227], v244 offset:17920\n"
"v_mfma_f32_16x16x32_bf16 v[132:135], a[140:143], a[164:167], v[132:135]\n"
"buffer_load_dwordx4 v225, s[28:31], s40 offen offset:1024 lds\n"
"v_mfma_f32_16x16x32_bf16 v[136:139], a[144:147], a[160:163], v[136:139]\n"
"ds_read_b64_tr_b16 a[192:193], v244 offset:16384\n"
"v_mfma_f32_16x16x32_bf16 v[136:139], a[148:151], a[164:167], v[136:139]\n"
"ds_read_b64_tr_b16 a[194:195], v244 offset:16896\n"
"v_mfma_f32_16x16x32_bf16 v[140:143], a[152:155], a[160:163], v[140:143]\n"
"ds_read_b64_tr_b16 a[196:197], v244 offset:24576\n"
"v_mfma_f32_16x16x32_bf16 v[140:143], a[156:159], a[164:167], v[140:143]\n"
"s_waitcnt lgkmcnt(6)\n"
"ds_read_b64_tr_b16 a[198:199], v244 offset:25088\n"
"v_mfma_f32_16x16x32_bf16 v[220:223], a[136:139], a[184:187], v[220:223]\n"
"ds_read_b64_tr_b16 a[200:201], v245 offset:16384\n"
"v_mfma_f32_16x16x32_bf16 v[220:223], a[140:143], a[188:191], v[220:223]\n"
"buffer_load_dwordx4 v226, s[28:31], s40 offen offset:2048 lds\n"
"v_mfma_f32_16x16x32_bf16 v[216:219], a[128:131], a[184:187], v[216:219]\n"
"ds_read_b64_tr_b16 a[202:203], v245 offset:16896\n"
"v_mfma_f32_16x16x32_bf16 v[216:219], a[132:135], a[188:191], v[216:219]\n"
"ds_read_b64_tr_b16 a[204:205], v245 offset:24576\n"
"v_mfma_f32_16x16x32_bf16 v[212:215], a[104:107], a[184:187], v[212:215]\n"
"ds_read_b64_tr_b16 a[206:207], v245 offset:25088\n"
"v_mfma_f32_16x16x32_bf16 v[212:215], a[108:111], a[188:191], v[212:215]\n"
"v_pk_fma_f32 v[112:113], v[112:113], s[70:71], s[70:71] op_sel:[0,0,1] op_sel_hi:[1,0,1]\nv_pk_fma_f32 v[114:115], v[114:115], s[70:71], s[70:71] op_sel:[0,0,1] op_sel_hi:[1,0,1]\nv_pk_fma_f32 v[116:117], v[116:117], s[70:71], s[70:71] op_sel:[0,0,1] op_sel_hi:[1,0,1]\nv_pk_fma_f32 v[118:119], v[118:119], s[70:71], s[70:71] op_sel:[0,0,1] op_sel_hi:[1,0,1]\nv_pk_fma_f32 v[120:121], v[120:121], s[70:71], s[70:71] op_sel:[0,0,1] op_sel_hi:[1,0,1]\nv_pk_fma_f32 v[122:123], v[122:123], s[70:71], s[70:71] op_sel:[0,0,1] op_sel_hi:[1,0,1]\nv_pk_fma_f32 v[124:125], v[124:125], s[70:71], s[70:71] op_sel:[0,0,1] op_sel_hi:[1,0,1]\nv_pk_fma_f32 v[126:127], v[126:127], s[70:71], s[70:71] op_sel:[0,0,1] op_sel_hi:[1,0,1]\nv_pk_fma_f32 v[128:129], v[128:129], s[70:71], s[70:71] op_sel:[0,0,1] op_sel_hi:[1,0,1]\nv_pk_fma_f32 v[130:131], v[130:131], s[70:71], s[70:71] op_sel:[0,0,1] op_sel_hi:[1,0,1]\nv_pk_fma_f32 v[132:133], v[132:133], s[70:71], s[70:71] op_sel:[0,0,1] op_sel_hi:[1,0,1]\nv_pk_fma_f32 v[134:135], v[134:135], s[70:71], s[70:71] op_sel:[0,0,1] op_sel_hi:[1,0,1]\nv_pk_fma_f32 v[136:137], v[136:137], s[70:71], s[70:71] op_sel:[0,0,1] op_sel_hi:[1,0,1]\nv_pk_fma_f32 v[138:139], v[138:139], s[70:71], s[70:71] op_sel:[0,0,1] op_sel_hi:[1,0,1]\nv_pk_fma_f32 v[140:141], v[140:141], s[70:71], s[70:71] op_sel:[0,0,1] op_sel_hi:[1,0,1]\nv_pk_fma_f32 v[142:143], v[142:143], s[70:71], s[70:71] op_sel:[0,0,1] op_sel_hi:[1,0,1]\n"
"v_mfma_f32_16x16x32_bf16 v[208:211], a[96:99], a[184:187], v[208:211]\n"
"ds_read_b64_tr_b16 a[208:209], v246 offset:16384\n"
"v_cvt_pk_bf16_f32 v112, v112, v113\n"
"v_mfma_f32_16x16x32_bf16 v[208:211], a[100:103], a[188:191], v[208:211]\n"
"buffer_load_dwordx4 v227, s[28:31], s40 offen offset:3072 lds\n"
"v_mfma_f32_16x16x32_bf16 v[172:175], a[152:155], a[168:171], v[172:175]\n"
"s_cmp_eq_u32 s47, 8\n"
"ds_read_b64_tr_b16 a[210:211], v246 offset:16896\n"
"v_cvt_pk_bf16_f32 v113, v114, v115\n"
"v_mfma_f32_16x16x32_bf16 v[172:175], a[156:159], a[172:175], v[172:175]\n"
"s_cselect_b32 s41, 524800, s58\n"
"ds_read_b64_tr_b16 a[212:213], v246 offset:24576\n"
"v_cvt_pk_bf16_f32 v114, v116, v117\n"
"v_mfma_f32_16x16x32_bf16 v[168:171], a[144:147], a[168:171], v[168:171]\n"
"s_add_u32 s48, s48, s41\n"
"ds_read_b64_tr_b16 a[214:215], v246 offset:25088\n"
"v_cvt_pk_bf16_f32 v115, v118, v119\n"
"v_mfma_f32_16x16x32_bf16 v[168:171], a[148:151], a[172:175], v[168:171]\n"
"s_sub_u32 s41, s48, 5242880\n"
"ds_read_b64_tr_b16 a[216:217], v247 offset:16384\n"
"v_cvt_pk_bf16_f32 v120, v120, v121\n"
"v_mfma_f32_16x16x32_bf16 v[164:167], a[136:139], a[168:171], v[164:167]\n"
"s_min_u32 s48, s48, s41\n"
"ds_read_b64_tr_b16 a[218:219], v247 offset:16896\n"
"v_cvt_pk_bf16_f32 v121, v122, v123\n"
"v_mfma_f32_16x16x32_bf16 v[164:167], a[140:143], a[172:175], v[164:167]\n"
"ds_read_b64_tr_b16 a[220:221], v247 offset:24576\n"
"v_xor_b32 v250, s74, v250\n"
"v_mfma_f32_16x16x32_bf16 v[160:163], a[128:131], a[168:171], v[160:163]\n"
"s_mov_b32 m0, s50\n"
"ds_read_b64_tr_b16 a[222:223], v247 offset:25088\n"
"v_xor_b32 v251, s74, v251\n"
"v_mfma_f32_16x16x32_bf16 v[160:163], a[132:135], a[172:175], v[160:163]\n"
"buffer_load_dwordx4 v224, s[28:31], s48 offen lds\n"
"v_mfma_f32_16x16x32_bf16 v[156:159], a[120:123], a[168:171], v[156:159]\n"
"ds_read_b64_tr_b16 a[228:229], v244 offset:25600\n"
"v_xor_b32 v245, s74, v245\n"
"v_mfma_f32_16x16x32_bf16 v[156:159], a[124:127], a[172:175], v[156:159]\n"
"s_mov_b64 s[80:81], s[8:9]\n"
"ds_read_b64_tr_b16 a[230:231], v244 offset:26112\n"
"v_xor_b32 v244, s74, v244\n"
"v_mfma_f32_16x16x32_bf16 v[152:155], a[112:115], a[168:171], v[152:155]\n"
"s_mov_b32 s82, s68\n"
"ds_read_b64_tr_b16 a[240:241], v246 offset:17408\n"
"v_cvt_pk_bf16_f32 v122, v124, v125\n"
"v_mfma_f32_16x16x32_bf16 v[152:155], a[116:119], a[172:175], v[152:155]\n"
"s_mov_b32 s83, 131072\n"
"buffer_load_dwordx4 v225, s[28:31], s48 offen offset:1024 lds\n"
"v_mfma_f32_16x16x32_bf16 v[148:151], a[104:107], a[168:171], v[148:151]\n"
"v_and_b32 v241, 0x3f0, v242\n"
"ds_read_b64_tr_b16 a[242:243], v246 offset:17920\n"
"v_cvt_pk_bf16_f32 v123, v126, v127\n"
"v_mfma_f32_16x16x32_bf16 v[148:151], a[108:111], a[172:175], v[148:151]\n"
"v_mul_u32_u24 v241, 10, v241\n"
"ds_read_b64_tr_b16 a[244:245], v246 offset:25600\n"
"v_cvt_pk_bf16_f32 v128, v128, v129\n"
"v_mfma_f32_16x16x32_bf16 v[144:147], a[96:99], a[168:171], v[144:147]\n"
"ds_read_b64_tr_b16 a[246:247], v246 offset:26112\n"
"v_xor_b32 v246, s74, v246\n"
"v_mfma_f32_16x16x32_bf16 v[144:147], a[100:103], a[172:175], v[144:147]\n"
"ds_read_b64_tr_b16 a[248:249], v247 offset:17408\n"
"v_cvt_pk_bf16_f32 v129, v130, v131\n"
"v_mfma_f32_16x16x32_bf16 v[176:179], a[96:99], a[176:179], v[176:179]\n"
"ds_read_b64_tr_b16 a[250:251], v247 offset:17920\n"
"v_cvt_pk_bf16_f32 v130, v132, v133\n"
"v_mfma_f32_16x16x32_bf16 v[176:179], a[100:103], a[180:183], v[176:179]\n"
"buffer_load_dwordx4 v226, s[28:31], s48 offen offset:2048 lds\n"
"v_mfma_f32_16x16x32_bf16 v[180:183], a[104:107], a[176:179], v[180:183]\n"
"ds_read_b64_tr_b16 a[252:253], v247 offset:25600\n"
"v_cvt_pk_bf16_f32 v131, v134, v135\n"
"v_mfma_f32_16x16x32_bf16 v[180:183], a[108:111], a[180:183], v[180:183]\n"
"ds_read_b64_tr_b16 a[254:255], v247 offset:26112\n"
"v_xor_b32 v247, s74, v247\n"
"v_mfma_f32_16x16x32_bf16 v[184:187], a[112:115], a[176:179], v[184:187]\n"
"v_permlane16_swap_b32 v112, v114\n"
"v_mfma_f32_16x16x32_bf16 v[184:187], a[116:119], a[180:183], v[184:187]\n"
"v_permlane16_swap_b32 v113, v115\n"
"v_mfma_f32_16x16x32_bf16 v[188:191], a[120:123], a[176:179], v[188:191]\n"
"v_permlane16_swap_b32 v120, v122\n"
"v_mfma_f32_16x16x32_bf16 v[188:191], a[124:127], a[180:183], v[188:191]\n"
"buffer_load_dwordx4 v227, s[28:31], s48 offen offset:3072 lds\n"
"v_mfma_f32_16x16x32_bf16 v[200:203], a[144:147], a[176:179], v[200:203]\n"
"s_xor_b32 s50, s50, s74\n"
"v_permlane16_swap_b32 v121, v123\n"
"v_mfma_f32_16x16x32_bf16 v[200:203], a[148:151], a[180:183], v[200:203]\n"
"v_permlane16_swap_b32 v128, v130\n"
"v_mfma_f32_16x16x32_bf16 v[204:207], a[152:155], a[176:179], v[204:207]\n"
"v_permlane16_swap_b32 v129, v131\n"
"v_mfma_f32_16x16x32_bf16 v[204:207], a[156:159], a[180:183], v[204:207]\n"
"v_pk_fma_f32 v[144:145], v[144:145], s[70:71], s[70:71] op_sel:[0,0,1] op_sel_hi:[1,0,1]\nv_pk_fma_f32 v[146:147], v[146:147], s[70:71], s[70:71] op_sel:[0,0,1] op_sel_hi:[1,0,1]\nv_pk_fma_f32 v[148:149], v[148:149], s[70:71], s[70:71] op_sel:[0,0,1] op_sel_hi:[1,0,1]\nv_pk_fma_f32 v[150:151], v[150:151], s[70:71], s[70:71] op_sel:[0,0,1] op_sel_hi:[1,0,1]\nv_pk_fma_f32 v[152:153], v[152:153], s[70:71], s[70:71] op_sel:[0,0,1] op_sel_hi:[1,0,1]\nv_pk_fma_f32 v[154:155], v[154:155], s[70:71], s[70:71] op_sel:[0,0,1] op_sel_hi:[1,0,1]\nv_pk_fma_f32 v[156:157], v[156:157], s[70:71], s[70:71] op_sel:[0,0,1] op_sel_hi:[1,0,1]\nv_pk_fma_f32 v[158:159], v[158:159], s[70:71], s[70:71] op_sel:[0,0,1] op_sel_hi:[1,0,1]\nv_pk_fma_f32 v[160:161], v[160:161], s[70:71], s[70:71] op_sel:[0,0,1] op_sel_hi:[1,0,1]\nv_pk_fma_f32 v[162:163], v[162:163], s[70:71], s[70:71] op_sel:[0,0,1] op_sel_hi:[1,0,1]\nv_pk_fma_f32 v[164:165], v[164:165], s[70:71], s[70:71] op_sel:[0,0,1] op_sel_hi:[1,0,1]\nv_pk_fma_f32 v[166:167], v[166:167], s[70:71], s[70:71] op_sel:[0,0,1] op_sel_hi:[1,0,1]\nv_pk_fma_f32 v[168:169], v[168:169], s[70:71], s[70:71] op_sel:[0,0,1] op_sel_hi:[1,0,1]\nv_pk_fma_f32 v[170:171], v[170:171], s[70:71], s[70:71] op_sel:[0,0,1] op_sel_hi:[1,0,1]\nv_pk_fma_f32 v[172:173], v[172:173], s[70:71], s[70:71] op_sel:[0,0,1] op_sel_hi:[1,0,1]\nv_pk_fma_f32 v[174:175], v[174:175], s[70:71], s[70:71] op_sel:[0,0,1] op_sel_hi:[1,0,1]\n"
"v_mfma_f32_16x16x32_bf16 v[192:195], a[128:131], a[176:179], v[192:195]\n"
"v_pk_fma_f32 v[208:209], v[208:209], s[70:71], s[70:71] op_sel:[0,0,1] op_sel_hi:[1,0,1]\nv_pk_fma_f32 v[210:211], v[210:211], s[70:71], s[70:71] op_sel:[0,0,1] op_sel_hi:[1,0,1]\nv_pk_fma_f32 v[212:213], v[212:213], s[70:71], s[70:71] op_sel:[0,0,1] op_sel_hi:[1,0,1]\nv_pk_fma_f32 v[214:215], v[214:215], s[70:71], s[70:71] op_sel:[0,0,1] op_sel_hi:[1,0,1]\nv_pk_fma_f32 v[216:217], v[216:217], s[70:71], s[70:71] op_sel:[0,0,1] op_sel_hi:[1,0,1]\nv_pk_fma_f32 v[218:219], v[218:219], s[70:71], s[70:71] op_sel:[0,0,1] op_sel_hi:[1,0,1]\nv_pk_fma_f32 v[220:221], v[220:221], s[70:71], s[70:71] op_sel:[0,0,1] op_sel_hi:[1,0,1]\nv_pk_fma_f32 v[222:223], v[222:223], s[70:71], s[70:71] op_sel:[0,0,1] op_sel_hi:[1,0,1]\n"
"v_mfma_f32_16x16x32_bf16 v[192:195], a[132:135], a[180:183], v[192:195]\n"
"v_accvgpr_write_b32 a0, v112\n"
"v_accvgpr_write_b32 a1, v113\n"
"v_mfma_f32_16x16x32_bf16 v[196:199], a[136:139], a[176:179], v[196:199]\n"
"v_accvgpr_write_b32 a2, v114\n"
"v_accvgpr_write_b32 a3, v115\n"
"v_mfma_f32_16x16x32_bf16 v[196:199], a[140:143], a[180:183], v[196:199]\n"
"v_accvgpr_write_b32 a4, v120\n"
"v_accvgpr_write_b32 a5, v121\n"
"v_mfma_f32_16x16x32_bf16 v[108:111], a[232:235], a[184:187], v[108:111]\n"
"s_waitcnt lgkmcnt(0)\n"
"v_accvgpr_write_b32 a6, v122\n"
"v_accvgpr_write_b32 a7, v123\n"
"v_mfma_f32_16x16x32_bf16 v[108:111], a[236:239], a[188:191], v[108:111]\n"
"v_accvgpr_write_b32 a8, v128\n"
"v_accvgpr_write_b32 a9, v129\n"
"v_mfma_f32_16x16x32_bf16 v[104:107], a[224:227], a[184:187], v[104:107]\n"
"s_waitcnt vmcnt(10)\n"
"v_accvgpr_write_b32 a10, v130\n"
"v_accvgpr_write_b32 a11, v131\n"
"v_mfma_f32_16x16x32_bf16 v[104:107], a[228:231], a[188:191], v[104:107]\n"
"s_barrier\n"
"v_mfma_f32_16x16x32_bf16 v[100:103], a[200:203], a[184:187], v[100:103]\n"
"s_mov_b32 m0, s51\n"
"ds_read_b64_tr_b16 a[96:97], v244 offset:0\n"
"v_cvt_pk_bf16_f32 v136, v136, v137\n"
"v_mfma_f32_16x16x32_bf16 v[100:103], a[204:207], a[188:191], v[100:103]\n"
"buffer_load_dwordx4 v248, s[24:27], s49 offen lds\n"
"v_mfma_f32_16x16x32_bf16 v[96:99], a[192:195], a[184:187], v[96:99]\n"
"ds_read_b64_tr_b16 a[98:99], v244 offset:512\n"
"v_cvt_pk_bf16_f32 v137, v138, v139\n"
"v_mfma_f32_16x16x32_bf16 v[96:99], a[196:199], a[188:191], v[96:99]\n"
"ds_read_b64_tr_b16 a[100:101], v244 offset:8192\n"
"v_cvt_pk_bf16_f32 v138, v140, v141\n"
"v_mfma_f32_16x16x32_bf16 v[0:3], a[192:195], a[160:163], v[0:3]\n"
"ds_read_b64_tr_b16 a[102:103], v244 offset:8704\n"
"v_cvt_pk_bf16_f32 v139, v142, v143\n"
"v_mfma_f32_16x16x32_bf16 v[0:3], a[196:199], a[164:167], v[0:3]\n"
"ds_read_b64_tr_b16 a[104:105], v245 offset:0\n"
"v_cvt_pk_bf16_f32 v168, v168, v169\n"
"v_mfma_f32_16x16x32_bf16 v[4:7], a[200:203], a[160:163], v[4:7]\n"
"ds_read_b64_tr_b16 a[106:107], v245 offset:512\n"
"v_cvt_pk_bf16_f32 v169, v170, v171\n"
"v_mfma_f32_16x16x32_bf16 v[4:7], a[204:207], a[164:167], v[4:7]\n"
"buffer_load_dwordx4 v249, s[24:27], s49 offen offset:1024 lds\n"
"v_mfma_f32_16x16x32_bf16 v[8:11], a[208:211], a[160:163], v[8:11]\n"
"ds_read_b64_tr_b16 a[108:109], v245 offset:8192\n"
"v_cvt_pk_bf16_f32 v170, v172, v173\n"
"v_mfma_f32_16x16x32_bf16 v[8:11], a[212:215], a[164:167], v[8:11]\n"
"ds_read_b64_tr_b16 a[110:111], v245 offset:8704\n"
"v_cvt_pk_bf16_f32 v171, v174, v175\n"
"v_mfma_f32_16x16x32_bf16 v[12:15], a[216:219], a[160:163], v[12:15]\n"
"ds_read_b64_tr_b16 a[112:113], v246 offset:0\n"
"v_cvt_pk_bf16_f32 v160, v160, v161\n"
"v_mfma_f32_16x16x32_bf16 v[12:15], a[220:223], a[164:167], v[12:15]\n"
"buffer_load_dwordx4 v254, s[24:27], s49 offen offset:2048 lds\n"
"v_mfma_f32_16x16x32_bf16 v[16:19], a[224:227], a[160:163], v[16:19]\n"
"ds_read_b64_tr_b16 a[114:115], v246 offset:512\n"
"v_cvt_pk_bf16_f32 v161, v162, v163\n"
"v_mfma_f32_16x16x32_bf16 v[16:19], a[228:231], a[164:167], v[16:19]\n"
"ds_read_b64_tr_b16 a[116:117], v246 offset:8192\n"
"v_cvt_pk_bf16_f32 v162, v164, v165\n"
"v_mfma_f32_16x16x32_bf16 v[20:23], a[232:235], a[160:163], v[20:23]\n"
"ds_read_b64_tr_b16 a[118:119], v246 offset:8704\n"
"v_cvt_pk_bf16_f32 v163, v166, v167\n"
"v_mfma_f32_16x16x32_bf16 v[20:23], a[236:239], a[164:167], v[20:23]\n"
"ds_read_b64_tr_b16 a[120:121], v247 offset:0\n"
"v_cvt_pk_bf16_f32 v152, v152, v153\n"
"v_mfma_f32_16x16x32_bf16 v[24:27], a[240:243], a[160:163], v[24:27]\n"
"ds_read_b64_tr_b16 a[122:123], v247 offset:512\n"
"v_cvt_pk_bf16_f32 v153, v154, v155\n"
"v_mfma_f32_16x16x32_bf16 v[24:27], a[244:247], a[164:167], v[24:27]\n"
"buffer_load_dwordx4 v255, s[24:27], s49 offen offset:3072 lds\n"
"v_mfma_f32_16x16x32_bf16 v[28:31], a[248:251], a[160:163], v[28:31]\n"
"ds_read_b128 a[160:163], v250 offset:0\n"
"v_cvt_pk_bf16_f32 v154, v156, v157\n"
"v_mfma_f32_16x16x32_bf16 v[28:31], a[252:255], a[164:167], v[28:31]\n"
"ds_read_b128 a[164:167], v251 offset:0\n"
"v_cvt_pk_bf16_f32 v155, v158, v159\n"
"v_mfma_f32_16x16x32_bf16 v[60:63], a[248:251], a[168:171], v[60:63]\n"
"ds_read_b64_tr_b16 a[124:125], v247 offset:8192\n"
"v_cvt_pk_bf16_f32 v144, v144, v145\n"
"v_mfma_f32_16x16x32_bf16 v[60:63], a[252:255], a[172:175], v[60:63]\n"
"s_add_u32 s40, s49, 40960\n"
"ds_read_b64_tr_b16 a[126:127], v247 offset:8704\n"
"v_cvt_pk_bf16_f32 v145, v146, v147\n"
"v_mfma_f32_16x16x32_bf16 v[56:59], a[240:243], a[168:171], v[56:59]\n"
"s_add_u32 m0, s51, 4096\n"
"ds_read_b64_tr_b16 a[128:129], v244 offset:1024\n"
"v_cvt_pk_bf16_f32 v146, v148, v149\n"
"v_mfma_f32_16x16x32_bf16 v[56:59], a[244:247], a[172:175], v[56:59]\n"
"buffer_load_dwordx4 v248, s[24:27], s40 offen lds\n"
"v_mfma_f32_16x16x32_bf16 v[52:55], a[232:235], a[168:171], v[52:55]\n"
"ds_read_b64_tr_b16 a[130:131], v244 offset:1536\n"
"v_cvt_pk_bf16_f32 v147, v150, v151\n"
"v_mfma_f32_16x16x32_bf16 v[52:55], a[236:239], a[172:175], v[52:55]\n"
"ds_read_b64_tr_b16 a[132:133], v244 offset:9216\n"
"v_xor_b32 v252, s74, v252\n"
"v_mfma_f32_16x16x32_bf16 v[48:51], a[224:227], a[168:171], v[48:51]\n"
"ds_read_b64_tr_b16 a[134:135], v244 offset:9728\n"
"v_cvt_pk_bf16_f32 v216, v216, v217\n"
"v_mfma_f32_16x16x32_bf16 v[48:51], a[228:231], a[172:175], v[48:51]\n"
"ds_read_b64_tr_b16 a[136:137], v245 offset:1024\n"
"v_cvt_pk_bf16_f32 v217, v218, v219\n"
"v_mfma_f32_16x16x32_bf16 v[44:47], a[216:219], a[168:171], v[44:47]\n"
"ds_read_b64_tr_b16 a[138:139], v245 offset:1536\n"
"v_cvt_pk_bf16_f32 v218, v220, v221\n"
"v_mfma_f32_16x16x32_bf16 v[44:47], a[220:223], a[172:175], v[44:47]\n"
"buffer_load_dwordx4 v249, s[24:27], s40 offen offset:1024 lds\n"
"v_mfma_f32_16x16x32_bf16 v[40:43], a[208:211], a[168:171], v[40:43]\n"
"ds_read_b64_tr_b16 a[140:141], v245 offset:9216\n"
"v_cvt_pk_bf16_f32 v219, v222, v223\n"
"v_mfma_f32_16x16x32_bf16 v[40:43], a[212:215], a[172:175], v[40:43]\n"
"ds_read_b64_tr_b16 a[142:143], v245 offset:9728\n"
"v_xor_b32 v253, s74, v253\n"
"v_mfma_f32_16x16x32_bf16 v[36:39], a[200:203], a[168:171], v[36:39]\n"
"ds_read_b64_tr_b16 a[144:145], v246 offset:1024\n"
"v_cvt_pk_bf16_f32 v208, v208, v209\n"
"v_mfma_f32_16x16x32_bf16 v[36:39], a[204:207], a[172:175], v[36:39]\n"
"buffer_load_dwordx4 v254, s[24:27], s40 offen offset:2048 lds\n"
"v_mfma_f32_16x16x32_bf16 v[32:35], a[192:195], a[168:171], v[32:35]\n"
"s_add_u32 s49, s49, 128\n"
"ds_read_b64_tr_b16 a[146:147], v246 offset:1536\n"
"v_cvt_pk_bf16_f32 v209, v210, v211\n"
"v_mfma_f32_16x16x32_bf16 v[32:35], a[196:199], a[172:175], v[32:35]\n"
"s_sub_u32 s41, s49, 1280\n"
"ds_read_b64_tr_b16 a[148:149], v246 offset:9216\n"
"v_cvt_pk_bf16_f32 v210, v212, v213\n"
"v_mfma_f32_16x16x32_bf16 v[64:67], a[192:195], a[176:179], v[64:67]\n"
"s_min_u32 s49, s49, s41\n"
"ds_read_b64_tr_b16 a[150:151], v246 offset:9728\n"
"v_cvt_pk_bf16_f32 v211, v214, v215\n"
"v_mfma_f32_16x16x32_bf16 v[64:67], a[196:199], a[180:183], v[64:67]\n"
"s_xor_b32 s51, s51, s74\n"
"v_permlane16_swap_b32 v136, v138\n"
"v_mfma_f32_16x16x32_bf16 v[68:71], a[200:203], a[176:179], v[68:71]\n"
"s_mov_b32 m0, 139264\n"
"ds_read_b64_tr_b16 a[152:153], v247 offset:1024\n"
"v_accvgpr_write_b32 a12, v136\n"
"v_mfma_f32_16x16x32_bf16 v[68:71], a[204:207], a[180:183], v[68:71]\n"
"buffer_load_dword v241, s[80:83], s63 offen lds\n"
"v_mfma_f32_16x16x32_bf16 v[72:75], a[208:211], a[176:179], v[72:75]\n"
"ds_read_b64_tr_b16 a[154:155], v247 offset:1536\n"
"v_accvgpr_write_b32 a14, v138\n"
"v_mfma_f32_16x16x32_bf16 v[72:75], a[212:215], a[180:183], v[72:75]\n"
"v_permlane16_swap_b32 v137, v139\n"
"v_mfma_f32_16x16x32_bf16 v[76:79], a[216:219], a[176:179], v[76:79]\n"
"ds_read_b64_tr_b16 a[156:157], v247 offset:9216\n"
"v_accvgpr_write_b32 a13, v137\n"
"v_mfma_f32_16x16x32_bf16 v[76:79], a[220:223], a[180:183], v[76:79]\n"
"ds_read_b64_tr_b16 a[158:159], v247 offset:9728\n"
"v_accvgpr_write_b32 a15, v139\n"
"v_mfma_f32_16x16x32_bf16 v[88:91], a[240:243], a[176:179], v[88:91]\n"
"v_permlane16_swap_b32 v168, v170\n"
"v_mfma_f32_16x16x32_bf16 v[88:91], a[244:247], a[180:183], v[88:91]\n"
"buffer_load_dword v241, s[80:83], s63 offen offset:80 lds\n"
"v_mfma_f32_16x16x32_bf16 v[92:95], a[248:251], a[176:179], v[92:95]\n"
"ds_read_b128 a[168:171], v250 offset:2048\n"
"v_accvgpr_write_b32 a28, v168\n"
"v_mfma_f32_16x16x32_bf16 v[92:95], a[252:255], a[180:183], v[92:95]\n"
"ds_read_b128 a[172:175], v251 offset:2048\n"
"v_accvgpr_write_b32 a30, v170\n"
"v_mfma_f32_16x16x32_bf16 v[80:83], a[224:227], a[176:179], v[80:83]\n"
"v_permlane16_swap_b32 v169, v171\n"
"v_mfma_f32_16x16x32_bf16 v[80:83], a[228:231], a[180:183], v[80:83]\n"
"ds_read_b128 a[184:187], v252 offset:0\n"
"v_accvgpr_write_b32 a29, v169\n"
"v_mfma_f32_16x16x32_bf16 v[84:87], a[232:235], a[176:179], v[84:87]\n"
"s_add_u32 s47, s47, 1\n"
"ds_read_b128 a[176:179], v250 offset:4096\n"
"v_accvgpr_write_b32 a31, v171\n"
"v_mfma_f32_16x16x32_bf16 v[84:87], a[236:239], a[180:183], v[84:87]\n"
"s_waitcnt lgkmcnt(4)\n"
"v_permlane16_swap_b32 v160, v162\n"
"v_mfma_f32_16x16x32_bf16 v[112:115], a[96:99], a[160:163], v[228:231]\n"
"ds_read_b128 a[180:183], v251 offset:4096\n"
"v_accvgpr_write_b32 a24, v160\n"
"v_mfma_f32_16x16x32_bf16 v[112:115], a[100:103], a[164:167], v[112:115]\n"
"ds_read_b128 a[188:191], v253 offset:0\n"
"v_accvgpr_write_b32 a26, v162\n"
"v_mfma_f32_16x16x32_bf16 v[116:119], a[104:107], a[160:163], v[228:231]\n"
"s_waitcnt vmcnt(13)\n"
"v_permlane16_swap_b32 v161, v163\n"
"v_mfma_f32_16x16x32_bf16 v[116:119], a[108:111], a[164:167], v[116:119]\n"
"s_barrier\n"
"v_mfma_f32_16x16x32_bf16 v[120:123], a[112:115], a[160:163], v[228:231]\n"
"s_add_u32 s40, s48, 256\n"
"s_xor_b32 m0, s50, 0xc000\n"
"ds_read_b64_tr_b16 a[192:193], v244 offset:16384\n"
"v_mfma_f32_16x16x32_bf16 v[120:123], a[116:119], a[164:167], v[120:123]\n"
"buffer_load_dwordx4 v224, s[28:31], s40 offen lds\n"
"v_mfma_f32_16x16x32_bf16 v[124:127], a[120:123], a[160:163], v[228:231]\n"
"ds_read_b64_tr_b16 a[194:195], v244 offset:16896\n"
"v_accvgpr_write_b32 a25, v161\n"
"v_mfma_f32_16x16x32_bf16 v[124:127], a[124:127], a[164:167], v[124:127]\n"
"ds_read_b64_tr_b16 a[196:197], v244 offset:24576\n"
"v_accvgpr_write_b32 a27, v163\n"
"v_mfma_f32_16x16x32_bf16 v[128:131], a[128:131], a[160:163], v[228:231]\n"
"v_permlane16_swap_b32 v152, v154\n"
"v_mfma_f32_16x16x32_bf16 v[128:131], a[132:135], a[164:167], v[128:131]\n"
"ds_read_b64_tr_b16 a[198:199], v244 offset:25088\n"
"v_accvgpr_write_b32 a20, v152\n"
"v_mfma_f32_16x16x32_bf16 v[132:135], a[136:139], a[160:163], v[228:231]\n"
"ds_read_b64_tr_b16 a[200:201], v245 offset:16384\n"
"v_accvgpr_write_b32 a22, v154\n"
"v_mfma_f32_16x16x32_bf16 v[132:135], a[140:143], a[164:167], v[132:135]\n"
"buffer_load_dwordx4 v225, s[28:31], s40 offen offset:1024 lds\n"
"v_mfma_f32_16x16x32_bf16 v[136:139], a[144:147], a[160:163], v[228:231]\n"
"v_permlane16_swap_b32 v153, v155\n"
"v_mfma_f32_16x16x32_bf16 v[136:139], a[148:151], a[164:167], v[136:139]\n"
"ds_read_b64_tr_b16 a[202:203], v245 offset:16896\n"
"v_accvgpr_write_b32 a21, v153\n"
"v_mfma_f32_16x16x32_bf16 v[140:143], a[152:155], a[160:163], v[228:231]\n"
"ds_read_b64_tr_b16 a[204:205], v245 offset:24576\n"
"v_accvgpr_write_b32 a23, v155\n"
"v_mfma_f32_16x16x32_bf16 v[140:143], a[156:159], a[164:167], v[140:143]\n"
"s_waitcnt lgkmcnt(4)\n"
"v_permlane16_swap_b32 v144, v146\n"
"v_mfma_f32_16x16x32_bf16 v[172:175], a[152:155], a[168:171], v[228:231]\n"
"ds_read_b64_tr_b16 a[206:207], v245 offset:25088\n"
"v_accvgpr_write_b32 a16, v144\n"
"v_mfma_f32_16x16x32_bf16 v[172:175], a[156:159], a[172:175], v[172:175]\n"
"buffer_load_dwordx4 v226, s[28:31], s40 offen offset:2048 lds\n"
"v_mfma_f32_16x16x32_bf16 v[168:171], a[144:147], a[168:171], v[228:231]\n"
"ds_read_b64_tr_b16 a[208:209], v246 offset:16384\n"
"v_accvgpr_write_b32 a18, v146\n"
"v_mfma_f32_16x16x32_bf16 v[168:171], a[148:151], a[172:175], v[168:171]\n"
"v_permlane16_swap_b32 v145, v147\n"
"v_mfma_f32_16x16x32_bf16 v[164:167], a[136:139], a[168:171], v[228:231]\n"
"ds_read_b64_tr_b16 a[210:211], v246 offset:16896\n"
"v_accvgpr_write_b32 a17, v145\n"
"v_mfma_f32_16x16x32_bf16 v[164:167], a[140:143], a[172:175], v[164:167]\n"
"ds_read_b64_tr_b16 a[212:213], v246 offset:24576\n"
"v_accvgpr_write_b32 a19, v147\n"
"v_mfma_f32_16x16x32_bf16 v[160:163], a[128:131], a[168:171], v[228:231]\n"
"v_pk_fma_f32 v[176:177], v[176:177], s[70:71], s[70:71] op_sel:[0,0,1] op_sel_hi:[1,0,1]\nv_pk_fma_f32 v[178:179], v[178:179], s[70:71], s[70:71] op_sel:[0,0,1] op_sel_hi:[1,0,1]\nv_pk_fma_f32 v[180:181], v[180:181], s[70:71], s[70:71] op_sel:[0,0,1] op_sel_hi:[1,0,1]\nv_pk_fma_f32 v[182:183], v[182:183], s[70:71], s[70:71] op_sel:[0,0,1] op_sel_hi:[1,0,1]\nv_pk_fma_f32 v[184:185], v[184:185], s[70:71], s[70:71] op_sel:[0,0,1] op_sel_hi:[1,0,1]\nv_pk_fma_f32 v[186:187], v[186:187], s[70:71], s[70:71] op_sel:[0,0,1] op_sel_hi:[1,0,1]\nv_pk_fma_f32 v[188:189], v[188:189], s[70:71], s[70:71] op_sel:[0,0,1] op_sel_hi:[1,0,1]\nv_pk_fma_f32 v[190:191], v[190:191], s[70:71], s[70:71] op_sel:[0,0,1] op_sel_hi:[1,0,1]\nv_pk_fma_f32 v[192:193], v[192:193], s[70:71], s[70:71] op_sel:[0,0,1] op_sel_hi:[1,0,1]\nv_pk_fma_f32 v[194:195], v[194:195], s[70:71], s[70:71] op_sel:[0,0,1] op_sel_hi:[1,0,1]\nv_pk_fma_f32 v[196:197], v[196:197], s[70:71], s[70:71] op_sel:[0,0,1] op_sel_hi:[1,0,1]\nv_pk_fma_f32 v[198:199], v[198:199], s[70:71], s[70:71] op_sel:[0,0,1] op_sel_hi:[1,0,1]\nv_pk_fma_f32 v[200:201], v[200:201], s[70:71], s[70:71] op_sel:[0,0,1] op_sel_hi:[1,0,1]\nv_pk_fma_f32 v[202:203], v[202:203], s[70:71], s[70:71] op_sel:[0,0,1] op_sel_hi:[1,0,1]\nv_pk_fma_f32 v[204:205], v[204:205], s[70:71], s[70:71] op_sel:[0,0,1] op_sel_hi:[1,0,1]\nv_pk_fma_f32 v[206:207], v[206:207], s[70:71], s[70:71] op_sel:[0,0,1] op_sel_hi:[1,0,1]\n"
"v_mfma_f32_16x16x32_bf16 v[160:163], a[132:135], a[172:175], v[160:163]\n"
"buffer_load_dwordx4 v227, s[28:31], s40 offen offset:3072 lds\n"
"v_mfma_f32_16x16x32_bf16 v[156:159], a[120:123], a[168:171], v[228:231]\n"
"s_cmp_eq_u32 s47, 8\n"
"ds_read_b64_tr_b16 a[214:215], v246 offset:25088\n"
"v_cvt_pk_bf16_f32 v176, v176, v177\n"
"v_mfma_f32_16x16x32_bf16 v[156:159], a[124:127], a[172:175], v[156:159]\n"
"s_cselect_b32 s41, 524800, s58\n"
"ds_read_b64_tr_b16 a[216:217], v247 offset:16384\n"
"v_cvt_pk_bf16_f32 v177, v178, v179\n"
"v_mfma_f32_16x16x32_bf16 v[152:155], a[112:115], a[168:171], v[228:231]\n"
"s_add_u32 s48, s48, s41\n"
"ds_read_b64_tr_b16 a[218:219], v247 offset:16896\n"
"v_cvt_pk_bf16_f32 v178, v180, v181\n"
"v_mfma_f32_16x16x32_bf16 v[152:155], a[116:119], a[172:175], v[152:155]\n"
"s_sub_u32 s41, s48, 5242880\n"
"ds_read_b64_tr_b16 a[220:221], v247 offset:24576\n"
"v_cvt_pk_bf16_f32 v179, v182, v183\n"
"v_mfma_f32_16x16x32_bf16 v[148:151], a[104:107], a[168:171], v[228:231]\n"
"s_min_u32 s48, s48, s41\n"
"v_permlane16_swap_b32 v176, v178\n"
"v_mfma_f32_16x16x32_bf16 v[148:151], a[108:111], a[172:175], v[148:151]\n"
"v_permlane16_swap_b32 v177, v179\n"
"v_mfma_f32_16x16x32_bf16 v[144:147], a[96:99], a[168:171], v[228:231]\n"
"s_mov_b32 m0, s50\n"
"v_accvgpr_write_b32 a32, v176\n"
"v_accvgpr_write_b32 a33, v177\n"
"v_mfma_f32_16x16x32_bf16 v[144:147], a[100:103], a[172:175], v[144:147]\n"
"buffer_load_dwordx4 v224, s[28:31], s48 offen lds\n"
"v_accvgpr_write_b32 a34, v178\n"
"v_accvgpr_write_b32 a35, v179\n"
"v_mfma_f32_16x16x32_bf16 v[176:179], a[96:99], a[176:179], v[228:231]\n"
"v_permlane16_swap_b32 v216, v218\n"
"v_permlane16_swap_b32 v217, v219\n"
"v_mfma_f32_16x16x32_bf16 v[176:179], a[100:103], a[180:183], v[176:179]\n"
"ds_write_b128 v243, v[216:219] offset:2048\n"
"v_mfma_f32_16x16x32_bf16 v[180:183], a[104:107], a[176:179], v[228:231]\n"
"v_cvt_pk_bf16_f32 v184, v184, v185\n"
"v_cvt_pk_bf16_f32 v185, v186, v187\n"
"v_cvt_pk_bf16_f32 v186, v188, v189\n"
"v_cvt_pk_bf16_f32 v187, v190, v191\n"
"v_mfma_f32_16x16x32_bf16 v[180:183], a[108:111], a[180:183], v[180:183]\n"
"buffer_load_dwordx4 v225, s[28:31], s48 offen offset:1024 lds\n"
"v_permlane16_swap_b32 v184, v186\n"
"v_permlane16_swap_b32 v185, v187\n"
"v_accvgpr_write_b32 a36, v184\n"
"v_accvgpr_write_b32 a37, v185\n"
"v_accvgpr_write_b32 a38, v186\n"
"v_accvgpr_write_b32 a39, v187\n"
"v_mfma_f32_16x16x32_bf16 v[184:187], a[112:115], a[176:179], v[228:231]\n"
"v_permlane16_swap_b32 v208, v210\n"
"v_mfma_f32_16x16x32_bf16 v[184:187], a[116:119], a[180:183], v[184:187]\n"
"v_permlane16_swap_b32 v209, v211\n"
"v_mfma_f32_16x16x32_bf16 v[188:191], a[120:123], a[176:179], v[228:231]\n"
"ds_write_b128 v243, v[208:211] offset:0\n"
"v_cvt_pk_bf16_f32 v200, v200, v201\n"
"v_cvt_pk_bf16_f32 v201, v202, v203\n"
"v_cvt_pk_bf16_f32 v202, v204, v205\n"
"v_cvt_pk_bf16_f32 v203, v206, v207\n"
"v_mfma_f32_16x16x32_bf16 v[188:191], a[124:127], a[180:183], v[188:191]\n"
"v_permlane16_swap_b32 v200, v202\n"
"v_permlane16_swap_b32 v201, v203\n"
"v_accvgpr_write_b32 a44, v200\n"
"v_accvgpr_write_b32 a45, v201\n"
"v_accvgpr_write_b32 a46, v202\n"
"v_accvgpr_write_b32 a47, v203\n"
"v_mfma_f32_16x16x32_bf16 v[200:203], a[144:147], a[176:179], v[228:231]\n"
"ds_read_b64_tr_b16 a[222:223], v247 offset:25088\n"
"v_cvt_pk_bf16_f32 v192, v192, v193\n"
"v_mfma_f32_16x16x32_bf16 v[200:203], a[148:151], a[180:183], v[200:203]\n"
"buffer_load_dwordx4 v226, s[28:31], s48 offen offset:2048 lds\n"
"v_mfma_f32_16x16x32_bf16 v[204:207], a[152:155], a[176:179], v[228:231]\n"
"ds_read_b64_tr_b16 a[224:225], v244 offset:17408\n"
"v_cvt_pk_bf16_f32 v193, v194, v195\n"
"v_cvt_pk_bf16_f32 v194, v196, v197\n"
"v_cvt_pk_bf16_f32 v195, v198, v199\n"
"v_mfma_f32_16x16x32_bf16 v[204:207], a[156:159], a[180:183], v[204:207]\n"
"v_permlane16_swap_b32 v192, v194\n"
"v_permlane16_swap_b32 v193, v195\n"
"v_accvgpr_write_b32 a40, v192\n"
"v_accvgpr_write_b32 a41, v193\n"
"v_accvgpr_write_b32 a42, v194\n"
"v_accvgpr_write_b32 a43, v195\n"
"v_mfma_f32_16x16x32_bf16 v[192:195], a[128:131], a[176:179], v[228:231]\n"
"ds_read_b64_tr_b16 a[226:227], v244 offset:17920\n"
"v_xor_b32 v250, s74, v250\n"
"v_mfma_f32_16x16x32_bf16 v[192:195], a[132:135], a[180:183], v[192:195]\n"
"ds_read_b64_tr_b16 a[228:229], v244 offset:25600\n"
"v_xor_b32 v251, s74, v251\n"
"v_mfma_f32_16x16x32_bf16 v[196:199], a[136:139], a[176:179], v[228:231]\n"
"ds_read_b64_tr_b16 a[230:231], v244 offset:26112\n"
"v_xor_b32 v244, s74, v244\n"
"v_mfma_f32_16x16x32_bf16 v[196:199], a[140:143], a[180:183], v[196:199]\n"
"buffer_load_dwordx4 v227, s[28:31], s48 offen offset:3072 lds\n"
"v_mfma_f32_16x16x32_bf16 v[220:223], a[136:139], a[184:187], v[228:231]\n"
"s_xor_b32 s50, s50, s74\n"
"ds_read_b64_tr_b16 a[232:233], v245 offset:17408\n"
"ds_read_b64_tr_b16 a[234:235], v245 offset:17920\n"
"v_mfma_f32_16x16x32_bf16 v[220:223], a[140:143], a[188:191], v[220:223]\n"
"ds_read_b64_tr_b16 a[236:237], v245 offset:25600\n"
"ds_read_b64_tr_b16 a[238:239], v245 offset:26112\n"
"v_mfma_f32_16x16x32_bf16 v[216:219], a[128:131], a[184:187], v[228:231]\n"
"ds_read_b64_tr_b16 a[240:241], v246 offset:17408\n"
"ds_read_b64_tr_b16 a[242:243], v246 offset:17920\n"
"v_mfma_f32_16x16x32_bf16 v[216:219], a[132:135], a[188:191], v[216:219]\n"
"ds_read_b64_tr_b16 a[244:245], v246 offset:25600\n"
"ds_read_b64_tr_b16 a[246:247], v246 offset:26112\n"
"v_mfma_f32_16x16x32_bf16 v[212:215], a[104:107], a[184:187], v[228:231]\n"
"ds_read_b64_tr_b16 a[248:249], v247 offset:17408\n"
"ds_read_b64_tr_b16 a[250:251], v247 offset:17920\n"
"v_mfma_f32_16x16x32_bf16 v[212:215], a[108:111], a[188:191], v[212:215]\n"
"ds_read_b64_tr_b16 a[252:253], v247 offset:25600\n"
"ds_read_b64_tr_b16 a[254:255], v247 offset:26112\n"
"v_mfma_f32_16x16x32_bf16 v[208:211], a[96:99], a[184:187], v[228:231]\n"
"v_pk_fma_f32 v[0:1], v[0:1], s[70:71], s[70:71] op_sel:[0,0,1] op_sel_hi:[1,0,1]\nv_pk_fma_f32 v[2:3], v[2:3], s[70:71], s[70:71] op_sel:[0,0,1] op_sel_hi:[1,0,1]\nv_pk_fma_f32 v[4:5], v[4:5], s[70:71], s[70:71] op_sel:[0,0,1] op_sel_hi:[1,0,1]\nv_pk_fma_f32 v[6:7], v[6:7], s[70:71], s[70:71] op_sel:[0,0,1] op_sel_hi:[1,0,1]\nv_pk_fma_f32 v[8:9], v[8:9], s[70:71], s[70:71] op_sel:[0,0,1] op_sel_hi:[1,0,1]\nv_pk_fma_f32 v[10:11], v[10:11], s[70:71], s[70:71] op_sel:[0,0,1] op_sel_hi:[1,0,1]\nv_pk_fma_f32 v[12:13], v[12:13], s[70:71], s[70:71] op_sel:[0,0,1] op_sel_hi:[1,0,1]\nv_pk_fma_f32 v[14:15], v[14:15], s[70:71], s[70:71] op_sel:[0,0,1] op_sel_hi:[1,0,1]\nv_pk_fma_f32 v[16:17], v[16:17], s[70:71], s[70:71] op_sel:[0,0,1] op_sel_hi:[1,0,1]\nv_pk_fma_f32 v[18:19], v[18:19], s[70:71], s[70:71] op_sel:[0,0,1] op_sel_hi:[1,0,1]\nv_pk_fma_f32 v[20:21], v[20:21], s[70:71], s[70:71] op_sel:[0,0,1] op_sel_hi:[1,0,1]\nv_pk_fma_f32 v[22:23], v[22:23], s[70:71], s[70:71] op_sel:[0,0,1] op_sel_hi:[1,0,1]\nv_pk_fma_f32 v[24:25], v[24:25], s[70:71], s[70:71] op_sel:[0,0,1] op_sel_hi:[1,0,1]\nv_pk_fma_f32 v[26:27], v[26:27], s[70:71], s[70:71] op_sel:[0,0,1] op_sel_hi:[1,0,1]\nv_pk_fma_f32 v[28:29], v[28:29], s[70:71], s[70:71] op_sel:[0,0,1] op_sel_hi:[1,0,1]\nv_pk_fma_f32 v[30:31], v[30:31], s[70:71], s[70:71] op_sel:[0,0,1] op_sel_hi:[1,0,1]\n"
"v_cvt_pk_bf16_f32 v0, v0, v1\n"
"v_cvt_pk_bf16_f32 v1, v2, v3\n"
"v_cvt_pk_bf16_f32 v2, v4, v5\n"
"v_cvt_pk_bf16_f32 v3, v6, v7\n"
"v_mfma_f32_16x16x32_bf16 v[208:211], a[100:103], a[188:191], v[208:211]\n"
"v_permlane16_swap_b32 v0, v2\n"
"v_permlane16_swap_b32 v1, v3\n"
"v_accvgpr_write_b32 a48, v0\n"
"v_accvgpr_write_b32 a49, v1\n"
"v_accvgpr_write_b32 a50, v2\n"
"v_accvgpr_write_b32 a51, v3\n"
"v_mfma_f32_16x16x32_bf16 v[0:3], a[192:195], a[160:163], v[228:231]\n"
"s_waitcnt lgkmcnt(10)\n"
"v_cvt_pk_bf16_f32 v8, v8, v9\n"
"v_cvt_pk_bf16_f32 v9, v10, v11\n"
"v_mfma_f32_16x16x32_bf16 v[0:3], a[196:199], a[164:167], v[0:3]\n"
"v_cvt_pk_bf16_f32 v10, v12, v13\n"
"v_cvt_pk_bf16_f32 v11, v14, v15\n"
"v_mfma_f32_16x16x32_bf16 v[4:7], a[200:203], a[160:163], v[228:231]\n"
"s_waitcnt vmcnt(10) lgkmcnt(0)\n"
"v_permlane16_swap_b32 v8, v10\n"
"v_mfma_f32_16x16x32_bf16 v[4:7], a[204:207], a[164:167], v[4:7]\n"
"s_barrier\n"
"v_permlane16_swap_b32 v9, v11\n"
"v_accvgpr_write_b32 a52, v8\n"
"v_accvgpr_write_b32 a53, v9\n"
"v_accvgpr_write_b32 a54, v10\n"
"v_accvgpr_write_b32 a55, v11\n"
"v_mfma_f32_16x16x32_bf16 v[8:11], a[208:211], a[160:163], v[228:231]\n"
"s_mov_b32 m0, s51\n"
"ds_read_b64_tr_b16 a[96:97], v244 offset:0\n"
"v_cvt_pk_bf16_f32 v16, v16, v17\n"
"v_mfma_f32_16x16x32_bf16 v[8:11], a[212:215], a[164:167], v[8:11]\n"
"buffer_load_dwordx4 v248, s[24:27], s49 offen lds\n"
"v_mfma_f32_16x16x32_bf16 v[12:15], a[216:219], a[160:163], v[228:231]\n"
"ds_read_b64_tr_b16 a[98:99], v244 offset:512\n"
"v_cvt_pk_bf16_f32 v17, v18, v19\n"
"v_cvt_pk_bf16_f32 v18, v20, v21\n"
"v_cvt_pk_bf16_f32 v19, v22, v23\n"
"v_mfma_f32_16x16x32_bf16 v[12:15], a[220:223], a[164:167], v[12:15]\n"
"v_permlane16_swap_b32 v16, v18\n"
"v_permlane16_swap_b32 v17, v19\n"
"v_accvgpr_write_b32 a56, v16\n"
"v_accvgpr_write_b32 a57, v17\n"
"v_accvgpr_write_b32 a58, v18\n"
"v_accvgpr_write_b32 a59, v19\n"
"v_mfma_f32_16x16x32_bf16 v[16:19], a[224:227], a[160:163], v[228:231]\n"
"ds_read_b64_tr_b16 a[100:101], v244 offset:8192\n"
"v_cvt_pk_bf16_f32 v24, v24, v25\n"
"v_mfma_f32_16x16x32_bf16 v[16:19], a[228:231], a[164:167], v[16:19]\n"
"ds_read_b64_tr_b16 a[102:103], v244 offset:8704\n"
"v_cvt_pk_bf16_f32 v25, v26, v27\n"
"v_mfma_f32_16x16x32_bf16 v[20:23], a[232:235], a[160:163], v[228:231]\n"
"ds_read_b64_tr_b16 a[128:129], v244 offset:1024\n"
"v_cvt_pk_bf16_f32 v26, v28, v29\n"
"v_cvt_pk_bf16_f32 v27, v30, v31\n"
"v_mfma_f32_16x16x32_bf16 v[20:23], a[236:239], a[164:167], v[20:23]\n"
"buffer_load_dwordx4 v249, s[24:27], s49 offen offset:1024 lds\n"
"v_permlane16_swap_b32 v24, v26\n"
"v_permlane16_swap_b32 v25, v27\n"
"v_accvgpr_write_b32 a60, v24\n"
"v_accvgpr_write_b32 a61, v25\n"
"v_accvgpr_write_b32 a62, v26\n"
"v_accvgpr_write_b32 a63, v27\n"
"v_mfma_f32_16x16x32_bf16 v[24:27], a[240:243], a[160:163], v[228:231]\n"
"ds_read_b64_tr_b16 a[130:131], v244 offset:1536\n"
"v_xor_b32 v245, s74, v245\n"
"v_mfma_f32_16x16x32_bf16 v[24:27], a[244:247], a[164:167], v[24:27]\n"
"ds_read_b64_tr_b16 a[104:105], v245 offset:0\n"
"v_xor_b32 v246, s74, v246\n"
"v_mfma_f32_16x16x32_bf16 v[28:31], a[248:251], a[160:163], v[228:231]\n"
"v_pk_fma_f32 v[32:33], v[32:33], s[70:71], s[70:71] op_sel:[0,0,1] op_sel_hi:[1,0,1]\nv_pk_fma_f32 v[34:35], v[34:35], s[70:71], s[70:71] op_sel:[0,0,1] op_sel_hi:[1,0,1]\nv_pk_fma_f32 v[36:37], v[36:37], s[70:71], s[70:71] op_sel:[0,0,1] op_sel_hi:[1,0,1]\nv_pk_fma_f32 v[38:39], v[38:39], s[70:71], s[70:71] op_sel:[0,0,1] op_sel_hi:[1,0,1]\nv_pk_fma_f32 v[40:41], v[40:41], s[70:71], s[70:71] op_sel:[0,0,1] op_sel_hi:[1,0,1]\nv_pk_fma_f32 v[42:43], v[42:43], s[70:71], s[70:71] op_sel:[0,0,1] op_sel_hi:[1,0,1]\nv_pk_fma_f32 v[44:45], v[44:45], s[70:71], s[70:71] op_sel:[0,0,1] op_sel_hi:[1,0,1]\nv_pk_fma_f32 v[46:47], v[46:47], s[70:71], s[70:71] op_sel:[0,0,1] op_sel_hi:[1,0,1]\nv_pk_fma_f32 v[48:49], v[48:49], s[70:71], s[70:71] op_sel:[0,0,1] op_sel_hi:[1,0,1]\nv_pk_fma_f32 v[50:51], v[50:51], s[70:71], s[70:71] op_sel:[0,0,1] op_sel_hi:[1,0,1]\nv_pk_fma_f32 v[52:53], v[52:53], s[70:71], s[70:71] op_sel:[0,0,1] op_sel_hi:[1,0,1]\nv_pk_fma_f32 v[54:55], v[54:55], s[70:71], s[70:71] op_sel:[0,0,1] op_sel_hi:[1,0,1]\nv_pk_fma_f32 v[56:57], v[56:57], s[70:71], s[70:71] op_sel:[0,0,1] op_sel_hi:[1,0,1]\nv_pk_fma_f32 v[58:59], v[58:59], s[70:71], s[70:71] op_sel:[0,0,1] op_sel_hi:[1,0,1]\nv_pk_fma_f32 v[60:61], v[60:61], s[70:71], s[70:71] op_sel:[0,0,1] op_sel_hi:[1,0,1]\nv_pk_fma_f32 v[62:63], v[62:63], s[70:71], s[70:71] op_sel:[0,0,1] op_sel_hi:[1,0,1]\n"
"v_mfma_f32_16x16x32_bf16 v[28:31], a[252:255], a[164:167], v[28:31]\n"
"ds_read_b128 a[160:163], v250 offset:0\n"
"v_cvt_pk_bf16_f32 v56, v56, v57\n"
"v_cvt_pk_bf16_f32 v57, v58, v59\n"
"v_cvt_pk_bf16_f32 v58, v60, v61\n"
"v_cvt_pk_bf16_f32 v59, v62, v63\n"
"v_mfma_f32_16x16x32_bf16 v[60:63], a[248:251], a[168:171], v[228:231]\n"
"v_permlane16_swap_b32 v56, v58\n"
"v_mfma_f32_16x16x32_bf16 v[60:63], a[252:255], a[172:175], v[60:63]\n"
"buffer_load_dwordx4 v254, s[24:27], s49 offen offset:2048 lds\n"
"v_permlane16_swap_b32 v57, v59\n"
"v_accvgpr_write_b32 a76, v56\n"
"v_accvgpr_write_b32 a77, v57\n"
"v_accvgpr_write_b32 a78, v58\n"
"v_accvgpr_write_b32 a79, v59\n"
"v_mfma_f32_16x16x32_bf16 v[56:59], a[240:243], a[168:171], v[228:231]\n"
"ds_read_b128 a[164:167], v251 offset:0\n"
"v_cvt_pk_bf16_f32 v48, v48, v49\n"
"v_mfma_f32_16x16x32_bf16 v[56:59], a[244:247], a[172:175], v[56:59]\n"
"ds_read_b64_tr_b16 a[106:107], v245 offset:512\n"
"v_cvt_pk_bf16_f32 v49, v50, v51\n"
"v_cvt_pk_bf16_f32 v50, v52, v53\n"
"v_cvt_pk_bf16_f32 v51, v54, v55\n"
"v_mfma_f32_16x16x32_bf16 v[52:55], a[232:235], a[168:171], v[228:231]\n"
"v_permlane16_swap_b32 v48, v50\n"
"v_mfma_f32_16x16x32_bf16 v[52:55], a[236:239], a[172:175], v[52:55]\n"
"v_permlane16_swap_b32 v49, v51\n"
"v_accvgpr_write_b32 a72, v48\n"
"v_accvgpr_write_b32 a73, v49\n"
"v_accvgpr_write_b32 a74, v50\n"
"v_accvgpr_write_b32 a75, v51\n"
"v_mfma_f32_16x16x32_bf16 v[48:51], a[224:227], a[168:171], v[228:231]\n"
"ds_read_b64_tr_b16 a[108:109], v245 offset:8192\n"
"v_cvt_pk_bf16_f32 v40, v40, v41\n"
"v_mfma_f32_16x16x32_bf16 v[48:51], a[228:231], a[172:175], v[48:51]\n"
"buffer_load_dwordx4 v255, s[24:27], s49 offen offset:3072 lds\n"
"v_cvt_pk_bf16_f32 v41, v42, v43\n"
"v_cvt_pk_bf16_f32 v42, v44, v45\n"
"v_cvt_pk_bf16_f32 v43, v46, v47\n"
"v_mfma_f32_16x16x32_bf16 v[44:47], a[216:219], a[168:171], v[228:231]\n"
"v_pk_fma_f32 v[96:97], v[96:97], s[70:71], s[70:71] op_sel:[0,0,1] op_sel_hi:[1,0,1]\nv_pk_fma_f32 v[98:99], v[98:99], s[70:71], s[70:71] op_sel:[0,0,1] op_sel_hi:[1,0,1]\nv_pk_fma_f32 v[100:101], v[100:101], s[70:71], s[70:71] op_sel:[0,0,1] op_sel_hi:[1,0,1]\nv_pk_fma_f32 v[102:103], v[102:103], s[70:71], s[70:71] op_sel:[0,0,1] op_sel_hi:[1,0,1]\nv_pk_fma_f32 v[104:105], v[104:105], s[70:71], s[70:71] op_sel:[0,0,1] op_sel_hi:[1,0,1]\nv_pk_fma_f32 v[106:107], v[106:107], s[70:71], s[70:71] op_sel:[0,0,1] op_sel_hi:[1,0,1]\nv_pk_fma_f32 v[108:109], v[108:109], s[70:71], s[70:71] op_sel:[0,0,1] op_sel_hi:[1,0,1]\nv_pk_fma_f32 v[110:111], v[110:111], s[70:71], s[70:71] op_sel:[0,0,1] op_sel_hi:[1,0,1]\n"
"v_mfma_f32_16x16x32_bf16 v[44:47], a[220:223], a[172:175], v[44:47]\n"
"v_cvt_pk_bf16_f32 v104, v104, v105\n"
"v_cvt_pk_bf16_f32 v105, v106, v107\n"
"v_cvt_pk_bf16_f32 v106, v108, v109\n"
"v_cvt_pk_bf16_f32 v107, v110, v111\n"
"v_permlane16_swap_b32 v40, v42\n"
"v_permlane16_swap_b32 v41, v43\n"
"v_accvgpr_write_b32 a68, v40\n"
"v_accvgpr_write_b32 a69, v41\n"
"v_accvgpr_write_b32 a70, v42\n"
"v_accvgpr_write_b32 a71, v43\n"
"v_mfma_f32_16x16x32_bf16 v[40:43], a[208:211], a[168:171], v[228:231]\n"
"v_permlane16_swap_b32 v104, v106\n"
"v_permlane16_swap_b32 v105, v107\n"
"v_mfma_f32_16x16x32_bf16 v[40:43], a[212:215], a[172:175], v[40:43]\n"
"s_add_u32 s40, s49, 40960\n"
"ds_write_b128 v243, v[104:107] offset:6144\n"
"v_cvt_pk_bf16_f32 v32, v32, v33\n"
"v_cvt_pk_bf16_f32 v33, v34, v35\n"
"v_cvt_pk_bf16_f32 v34, v36, v37\n"
"v_cvt_pk_bf16_f32 v35, v38, v39\n"
"v_mfma_f32_16x16x32_bf16 v[36:39], a[200:203], a[168:171], v[228:231]\n"
"s_add_u32 m0, s51, 4096\n"
"v_permlane16_swap_b32 v32, v34\n"
"v_mfma_f32_16x16x32_bf16 v[36:39], a[204:207], a[172:175], v[36:39]\n"
"buffer_load_dwordx4 v248, s[24:27], s40 offen lds\n"
"v_permlane16_swap_b32 v33, v35\n"
"v_accvgpr_write_b32 a64, v32\n"
"v_accvgpr_write_b32 a65, v33\n"
"v_accvgpr_write_b32 a66, v34\n"
"v_accvgpr_write_b32 a67, v35\n"
"v_mfma_f32_16x16x32_bf16 v[32:35], a[192:195], a[168:171], v[228:231]\n"
"v_pk_fma_f32 v[64:65], v[64:65], s[70:71], s[70:71] op_sel:[0,0,1] op_sel_hi:[1,0,1]\nv_pk_fma_f32 v[66:67], v[66:67], s[70:71], s[70:71] op_sel:[0,0,1] op_sel_hi:[1,0,1]\nv_pk_fma_f32 v[68:69], v[68:69], s[70:71], s[70:71] op_sel:[0,0,1] op_sel_hi:[1,0,1]\nv_pk_fma_f32 v[70:71], v[70:71], s[70:71], s[70:71] op_sel:[0,0,1] op_sel_hi:[1,0,1]\nv_pk_fma_f32 v[72:73], v[72:73], s[70:71], s[70:71] op_sel:[0,0,1] op_sel_hi:[1,0,1]\nv_pk_fma_f32 v[74:75], v[74:75], s[70:71], s[70:71] op_sel:[0,0,1] op_sel_hi:[1,0,1]\nv_pk_fma_f32 v[76:77], v[76:77], s[70:71], s[70:71] op_sel:[0,0,1] op_sel_hi:[1,0,1]\nv_pk_fma_f32 v[78:79], v[78:79], s[70:71], s[70:71] op_sel:[0,0,1] op_sel_hi:[1,0,1]\nv_pk_fma_f32 v[80:81], v[80:81], s[70:71], s[70:71] op_sel:[0,0,1] op_sel_hi:[1,0,1]\nv_pk_fma_f32 v[82:83], v[82:83], s[70:71], s[70:71] op_sel:[0,0,1] op_sel_hi:[1,0,1]\nv_pk_fma_f32 v[84:85], v[84:85], s[70:71], s[70:71] op_sel:[0,0,1] op_sel_hi:[1,0,1]\nv_pk_fma_f32 v[86:87], v[86:87], s[70:71], s[70:71] op_sel:[0,0,1] op_sel_hi:[1,0,1]\nv_pk_fma_f32 v[88:89], v[88:89], s[70:71], s[70:71] op_sel:[0,0,1] op_sel_hi:[1,0,1]\nv_pk_fma_f32 v[90:91], v[90:91], s[70:71], s[70:71] op_sel:[0,0,1] op_sel_hi:[1,0,1]\nv_pk_fma_f32 v[92:93], v[92:93], s[70:71], s[70:71] op_sel:[0,0,1] op_sel_hi:[1,0,1]\nv_pk_fma_f32 v[94:95], v[94:95], s[70:71], s[70:71] op_sel:[0,0,1] op_sel_hi:[1,0,1]\n"
"v_cvt_pk_bf16_f32 v64, v64, v65\n"
"v_cvt_pk_bf16_f32 v65, v66, v67\n"
"v_cvt_pk_bf16_f32 v66, v68, v69\n"
"v_cvt_pk_bf16_f32 v67, v70, v71\n"
"v_mfma_f32_16x16x32_bf16 v[32:35], a[196:199], a[172:175], v[32:35]\n"
"v_permlane16_swap_b32 v64, v66\n"
"v_permlane16_swap_b32 v65, v67\n"
"v_accvgpr_write_b32 a80, v64\n"
"v_accvgpr_write_b32 a81, v65\n"
"v_accvgpr_write_b32 a82, v66\n"
"v_accvgpr_write_b32 a83, v67\n"
"v_mfma_f32_16x16x32_bf16 v[64:67], a[192:195], a[176:179], v[228:231]\n"
"ds_read_b64_tr_b16 a[110:111], v245 offset:8704\n"
"v_cvt_pk_bf16_f32 v96, v96, v97\n"
"v_cvt_pk_bf16_f32 v97, v98, v99\n"
"v_cvt_pk_bf16_f32 v98, v100, v101\n"
"v_cvt_pk_bf16_f32 v99, v102, v103\n"
"v_mfma_f32_16x16x32_bf16 v[64:67], a[196:199], a[180:183], v[64:67]\n"
"v_permlane16_swap_b32 v96, v98\n"
"v_permlane16_swap_b32 v97, v99\n"
"v_mfma_f32_16x16x32_bf16 v[68:71], a[200:203], a[176:179], v[228:231]\n"
"ds_write_b128 v243, v[96:99] offset:4096\n"
"v_cvt_pk_bf16_f32 v72, v72, v73\n"
"v_cvt_pk_bf16_f32 v73, v74, v75\n"
"v_cvt_pk_bf16_f32 v74, v76, v77\n"
"v_cvt_pk_bf16_f32 v75, v78, v79\n"
"v_mfma_f32_16x16x32_bf16 v[68:71], a[204:207], a[180:183], v[68:71]\n"
"buffer_load_dwordx4 v249, s[24:27], s40 offen offset:1024 lds\n"
"v_permlane16_swap_b32 v72, v74\n"
"v_permlane16_swap_b32 v73, v75\n"
"v_accvgpr_write_b32 a84, v72\n"
"v_accvgpr_write_b32 a85, v73\n"
"v_accvgpr_write_b32 a86, v74\n"
"v_accvgpr_write_b32 a87, v75\n"
"v_mfma_f32_16x16x32_bf16 v[72:75], a[208:211], a[176:179], v[228:231]\n"
"ds_read_b64_tr_b16 a[112:113], v246 offset:0\n"
"v_cvt_pk_bf16_f32 v88, v88, v89\n"
"v_mfma_f32_16x16x32_bf16 v[72:75], a[212:215], a[180:183], v[72:75]\n"
"ds_read_b64_tr_b16 a[114:115], v246 offset:512\n"
"v_cvt_pk_bf16_f32 v89, v90, v91\n"
"v_mfma_f32_16x16x32_bf16 v[76:79], a[216:219], a[176:179], v[228:231]\n"
"ds_read_b64_tr_b16 a[116:117], v246 offset:8192\n"
"ds_read_b64_tr_b16 a[118:119], v246 offset:8704\n"
"v_cvt_pk_bf16_f32 v90, v92, v93\n"
"v_cvt_pk_bf16_f32 v91, v94, v95\n"
"v_xor_b32 v247, s74, v247\n"
"v_mfma_f32_16x16x32_bf16 v[76:79], a[220:223], a[180:183], v[76:79]\n"
"ds_read_b64_tr_b16 a[120:121], v247 offset:0\n"
"ds_read_b64_tr_b16 a[122:123], v247 offset:512\n"
"v_permlane16_swap_b32 v88, v90\n"
"v_permlane16_swap_b32 v89, v91\n"
"v_accvgpr_write_b32 a92, v88\n"
"v_accvgpr_write_b32 a93, v89\n"
"v_accvgpr_write_b32 a94, v90\n"
"v_accvgpr_write_b32 a95, v91\n"
"v_mfma_f32_16x16x32_bf16 v[88:91], a[240:243], a[176:179], v[228:231]\n"
"ds_read_b64_tr_b16 a[124:125], v247 offset:8192\n"
"ds_read_b64_tr_b16 a[126:127], v247 offset:8704\n"
"v_mfma_f32_16x16x32_bf16 v[88:91], a[244:247], a[180:183], v[88:91]\n"
"buffer_load_dwordx4 v254, s[24:27], s40 offen offset:2048 lds\n"
"v_mfma_f32_16x16x32_bf16 v[92:95], a[248:251], a[176:179], v[228:231]\n"
"s_add_u32 s49, s49, 128\n"
"ds_read_b64_tr_b16 a[132:133], v244 offset:9216\n"
"v_cvt_pk_bf16_f32 v80, v80, v81\n"
"v_cvt_pk_bf16_f32 v81, v82, v83\n"
"v_cvt_pk_bf16_f32 v82, v84, v85\n"
"v_cvt_pk_bf16_f32 v83, v86, v87\n"
"v_mfma_f32_16x16x32_bf16 v[92:95], a[252:255], a[180:183], v[92:95]\n"
"s_sub_u32 s41, s49, 1280\n"
"ds_read_b64_tr_b16 a[134:135], v244 offset:9728\n"
"v_permlane16_swap_b32 v80, v82\n"
"v_permlane16_swap_b32 v81, v83\n"
"v_accvgpr_write_b32 a88, v80\n"
"v_accvgpr_write_b32 a89, v81\n"
"v_accvgpr_write_b32 a90, v82\n"
"v_accvgpr_write_b32 a91, v83\n"
"v_mfma_f32_16x16x32_bf16 v[80:83], a[224:227], a[176:179], v[228:231]\n"
"s_min_u32 s49, s49, s41\n"
"ds_read_b64_tr_b16 a[136:137], v245 offset:1024\n"
"ds_read_b64_tr_b16 a[138:139], v245 offset:1536\n"
"v_mfma_f32_16x16x32_bf16 v[80:83], a[228:231], a[180:183], v[80:83]\n"
"s_xor_b32 s51, s51, s74\n"
"ds_read_b64_tr_b16 a[140:141], v245 offset:9216\n"
"ds_read_b64_tr_b16 a[142:143], v245 offset:9728\n"
"v_mfma_f32_16x16x32_bf16 v[84:87], a[232:235], a[176:179], v[228:231]\n"
"ds_read_b64_tr_b16 a[144:145], v246 offset:1024\n"
"ds_read_b64_tr_b16 a[146:147], v246 offset:1536\n"
"v_mfma_f32_16x16x32_bf16 v[84:87], a[236:239], a[180:183], v[84:87]\n"
"ds_read_b64_tr_b16 a[148:149], v246 offset:9216\n"
"ds_read_b64_tr_b16 a[150:151], v246 offset:9728\n"
"v_mfma_f32_16x16x32_bf16 v[108:111], a[232:235], a[184:187], v[228:231]\n"
"ds_read_b64_tr_b16 a[152:153], v247 offset:1024\n"
"ds_read_b64_tr_b16 a[154:155], v247 offset:1536\n"
"v_mfma_f32_16x16x32_bf16 v[108:111], a[236:239], a[188:191], v[108:111]\n"
"ds_read_b64_tr_b16 a[156:157], v247 offset:9216\n"
"ds_read_b64_tr_b16 a[158:159], v247 offset:9728\n"
"v_mfma_f32_16x16x32_bf16 v[104:107], a[224:227], a[184:187], v[228:231]\n"
"ds_read_b128 a[168:171], v250 offset:2048\n"
"v_xor_b32 v252, s74, v252\n"
"v_mfma_f32_16x16x32_bf16 v[104:107], a[228:231], a[188:191], v[104:107]\n"
"ds_read_b128 a[172:175], v251 offset:2048\n"
"v_xor_b32 v253, s74, v253\n"
"v_mfma_f32_16x16x32_bf16 v[100:103], a[200:203], a[184:187], v[228:231]\n"
"ds_read_b128 a[176:179], v250 offset:4096\n"
"v_mfma_f32_16x16x32_bf16 v[100:103], a[204:207], a[188:191], v[100:103]\n"
"ds_read_b128 a[180:183], v251 offset:4096\n"
"v_mfma_f32_16x16x32_bf16 v[96:99], a[192:195], a[184:187], v[228:231]\n"
"s_add_u32 s47, s47, 1\n"
"ds_read_b128 a[184:187], v252 offset:0\n"
"v_mfma_f32_16x16x32_bf16 v[96:99], a[196:199], a[188:191], v[96:99]\n"
"s_waitcnt lgkmcnt(9)\n"
"ds_read_b128 a[188:191], v253 offset:0\n"
"s_mov_b32 s62, 18\n"
"s_branch LPL_%=\n"
"LPT2_%=:\n"
"v_mfma_f32_16x16x32_bf16 v[112:115], a[96:99], a[160:163], v[112:115]\n"
"s_add_u32 s78, s32, s96\ns_addc_u32 s79, s33, 0\n"
"s_mov_b64 s[80:81], 0\n"
"s_mov_b32 s82, 0\n"
"v_mfma_f32_16x16x32_bf16 v[112:115], a[100:103], a[164:167], v[112:115]\n"
"v_bfe_u32 v228, v242, 8, 2\n"
"v_and_b32 v229, 0x60, v242\n"
"v_mfma_f32_16x16x32_bf16 v[116:119], a[104:107], a[160:163], v[116:119]\n"
"s_waitcnt vmcnt(11)\n"
"s_load_dword s80, s[78:79], 0\n"
"v_lshlrev_b32 v228, 4, v228\n"
"v_mfma_f32_16x16x32_bf16 v[116:119], a[108:111], a[164:167], v[116:119]\n"
"s_barrier\n"
"v_mfma_f32_16x16x32_bf16 v[120:123], a[112:115], a[160:163], v[120:123]\n"
"s_add_u32 s40, s48, 256\n"
"s_xor_b32 m0, s50, 0xc000\n"
"ds_read_b64_tr_b16 a[192:193], v244 offset:16384\n"
"v_mfma_f32_16x16x32_bf16 v[120:123], a[116:119], a[164:167], v[120:123]\n"
"buffer_load_dwordx4 v224, s[28:31], s40 offen lds\n"
"v_mfma_f32_16x16x32_bf16 v[124:127], a[120:123], a[160:163], v[124:127]\n"
"ds_read_b64_tr_b16 a[194:195], v244 offset:16896\n"
"v_xor_b32 v228, v228, v229\n"
"v_mfma_f32_16x16x32_bf16 v[124:127], a[124:127], a[164:167], v[124:127]\n"
"ds_read_b64_tr_b16 a[196:197], v244 offset:24576\n"
"v_and_b32 v229, 0x70, v242\n"
"v_mfma_f32_16x16x32_bf16 v[128:131], a[128:131], a[160:163], v[128:131]\n"
"ds_read_b64_tr_b16 a[198:199], v244 offset:25088\n"
"v_lshl_add_u32 v228, v229, 3, v228\n"
"v_mfma_f32_16x16x32_bf16 v[128:131], a[132:135], a[164:167], v[128:131]\n"
"ds_read_b64_tr_b16 a[200:201], v245 offset:16384\n"
"v_and_b32 v229, 0x80, v242\n"
"v_mfma_f32_16x16x32_bf16 v[132:135], a[136:139], a[160:163], v[132:135]\n"
"ds_read_b64_tr_b16 a[202:203], v245 offset:16896\n"
"v_lshl_add_u32 v228, v229, 4, v228\n"
"v_mfma_f32_16x16x32_bf16 v[132:135], a[140:143], a[164:167], v[132:135]\n"
"buffer_load_dwordx4 v225, s[28:31], s40 offen offset:1024 lds\n"
"v_mfma_f32_16x16x32_bf16 v[136:139], a[144:147], a[160:163], v[136:139]\n"
"s_add_u32 s82, s82, s80\n"
"ds_read_b64_tr_b16 a[204:205], v245 offset:24576\n"
"v_mov_b32 v229, s43\n"
"v_mfma_f32_16x16x32_bf16 v[136:139], a[148:151], a[164:167], v[136:139]\n"
"s_add_u32 s82, s82, s80\n"
"ds_read_b64_tr_b16 a[206:207], v245 offset:25088\n"
"v_mul_u32_u24 v229, 0x81, v229\n"
"v_mfma_f32_16x16x32_bf16 v[140:143], a[152:155], a[160:163], v[140:143]\n"
"s_add_u32 s82, s82, s80\n"
"ds_read_b64_tr_b16 a[208:209], v246 offset:16384\n"
"v_xor_b32 v228, v228, v229\n"
"v_mfma_f32_16x16x32_bf16 v[140:143], a[156:159], a[164:167], v[140:143]\n"
"s_add_u32 s82, s82, s80\n"
"ds_read_b64_tr_b16 a[210:211], v246 offset:16896\n"
"v_xor_b32 v229, 64, v228\n"
"v_mfma_f32_16x16x32_bf16 v[172:175], a[152:155], a[168:171], v[172:175]\n"
"s_add_u32 s82, s82, s80\n"
"ds_read_b64_tr_b16 a[212:213], v246 offset:24576\n"
"v_xor_b32 v230, 0x2000, v228\n"
"v_mfma_f32_16x16x32_bf16 v[172:175], a[156:159], a[172:175], v[172:175]\n"
"s_add_u32 s82, s82, s80\n"
"ds_read_b64_tr_b16 a[214:215], v246 offset:25088\n"
"v_xor_b32 v231, 0x2000, v229\n"
"v_mfma_f32_16x16x32_bf16 v[168:171], a[144:147], a[168:171], v[168:171]\n"
"s_add_u32 s82, s82, s80\n"
"ds_read_b64_tr_b16 a[216:217], v247 offset:16384\n"
"v_bfe_i32 v241, v242, 4, 1\n"
"v_mfma_f32_16x16x32_bf16 v[168:171], a[148:151], a[172:175], v[168:171]\n"
"s_add_u32 s82, s82, s80\n"
"buffer_load_dwordx4 v226, s[28:31], s40 offen offset:2048 lds\n"
"v_mfma_f32_16x16x32_bf16 v[164:167], a[136:139], a[168:171], v[164:167]\n"
"s_add_u32 s82, s82, s80\n"
"ds_read_b64_tr_b16 a[218:219], v247 offset:16896\n"
"v_and_b32 v241, 0xaaaaaaaa, v241\n"
"v_mfma_f32_16x16x32_bf16 v[164:167], a[140:143], a[172:175], v[164:167]\n"
"s_add_u32 s82, s82, s80\n"
"ds_read_b64_tr_b16 a[220:221], v247 offset:24576\n"
"v_or_b32 v241, 0x44444444, v241\n"
"v_mfma_f32_16x16x32_bf16 v[160:163], a[128:131], a[168:171], v[160:163]\n"
"s_add_u32 s82, s82, s80\n"
"ds_read_b64_tr_b16 a[222:223], v247 offset:25088\n"
"v_bfe_u32 v248, v242, 8, 2\n"
"v_mfma_f32_16x16x32_bf16 v[160:163], a[132:135], a[172:175], v[160:163]\n"
"s_add_u32 s82, s82, s80\n"
"ds_read_b64_tr_b16 a[224:225], v244 offset:17408\n"
"v_lshlrev_b32 v248, 6, v248\n"
"v_mfma_f32_16x16x32_bf16 v[156:159], a[120:123], a[168:171], v[156:159]\n"
"s_add_u32 s82, s82, s80\n"
"ds_read_b64_tr_b16 a[226:227], v244 offset:17920\n"
"v_xor_b32 v248, v248, v242\n"
"v_mfma_f32_16x16x32_bf16 v[156:159], a[124:127], a[172:175], v[156:159]\n"
"s_add_u32 s82, s82, s80\n"
"buffer_load_dwordx4 v227, s[28:31], s40 offen offset:3072 lds\n"
"v_mfma_f32_16x16x32_bf16 v[152:155], a[112:115], a[168:171], v[152:155]\n"
"s_add_u32 s82, s82, s80\n"
"s_mov_b32 s58, 245760\n"
"ds_read_b64_tr_b16 a[228:229], v244 offset:25600\n"
"v_mfma_f32_16x16x32_bf16 v[152:155], a[116:119], a[172:175], v[152:155]\n"
"s_add_u32 s82, s82, s80\n"
"s_mov_b32 s47, 0\n"
"ds_read_b64_tr_b16 a[230:231], v244 offset:26112\n"
"v_mfma_f32_16x16x32_bf16 v[148:151], a[104:107], a[168:171], v[148:151]\n"
"s_add_u32 s82, s82, s80\n"
"ds_read_b64_tr_b16 a[232:233], v245 offset:17408\n"
"v_bfe_u32 v249, v242, 4, 2\n"
"v_mfma_f32_16x16x32_bf16 v[148:151], a[108:111], a[172:175], v[148:151]\n"
"s_load_dword s81, s[78:79], 0x2000\n"
"ds_read_b64_tr_b16 a[234:235], v245 offset:17920\n"
"v_xor_b32 v244, s74, v244\n"
"v_mfma_f32_16x16x32_bf16 v[144:147], a[96:99], a[168:171], v[144:147]\n"
"s_mov_b32 m0, s44\n"
"ds_read_b64_tr_b16 a[236:237], v245 offset:25600\n"
"v_xor_b32 v250, s74, v250\n"
"v_mfma_f32_16x16x32_bf16 v[144:147], a[100:103], a[172:175], v[144:147]\n"
"buffer_load_dwordx4 v240, s[16:19], s56 offen nt lds\n"
"v_mfma_f32_16x16x32_bf16 v[176:179], a[96:99], a[176:179], v[176:179]\n"
"s_waitcnt lgkmcnt(5)\n"
"ds_read_b64_tr_b16 a[238:239], v245 offset:26112\n"
"v_xor_b32 v251, s74, v251\n"
"v_mfma_f32_16x16x32_bf16 v[176:179], a[100:103], a[180:183], v[176:179]\n"
"ds_read_b64_tr_b16 a[240:241], v246 offset:17408\n"
"v_xor_b32 v245, s74, v245\n"
"v_mfma_f32_16x16x32_bf16 v[180:183], a[104:107], a[176:179], v[180:183]\n"
"ds_read_b64_tr_b16 a[242:243], v246 offset:17920\n"
"v_xor_b32 v252, s74, v252\n"
"v_mfma_f32_16x16x32_bf16 v[180:183], a[108:111], a[180:183], v[180:183]\n"
"ds_read_b64_tr_b16 a[244:245], v246 offset:25600\n"
"v_xor_b32 v253, s74, v253\n"
"v_mfma_f32_16x16x32_bf16 v[184:187], a[112:115], a[176:179], v[184:187]\n"
"ds_read_b64_tr_b16 a[246:247], v246 offset:26112\n"
"v_xor_b32 v246, s74, v246\n"
"v_mfma_f32_16x16x32_bf16 v[184:187], a[116:119], a[180:183], v[184:187]\n"
"ds_read_b64_tr_b16 a[248:249], v247 offset:17408\n"
"v_mul_u32_u24 v249, 20, v249\n"
"v_mfma_f32_16x16x32_bf16 v[188:191], a[120:123], a[176:179], v[188:191]\n"
"s_add_u32 s40, s56, s52\n"
"ds_read_b64_tr_b16 a[250:251], v247 offset:17920\n"
"v_mfma_f32_16x16x32_bf16 v[188:191], a[124:127], a[180:183], v[188:191]\n"
"s_sub_u32 s82, s82, s81\n"
"buffer_load_dwordx4 v240, s[16:19], s40 offen offset:1024 nt lds\n"
"v_mfma_f32_16x16x32_bf16 v[200:203], a[144:147], a[176:179], v[200:203]\n"
"s_sub_u32 s82, s82, s81\n"
"ds_read_b64_tr_b16 a[252:253], v247 offset:25600\n"
"v_mfma_f32_16x16x32_bf16 v[200:203], a[148:151], a[180:183], v[200:203]\n"
"s_sub_u32 s82, s82, s81\n"
"ds_read_b64_tr_b16 a[254:255], v247 offset:26112\n"
"v_xor_b32 v247, s74, v247\n"
"v_mfma_f32_16x16x32_bf16 v[204:207], a[152:155], a[176:179], v[204:207]\n"
"s_sub_u32 s82, s82, s81\n"
"v_mfma_f32_16x16x32_bf16 v[204:207], a[156:159], a[180:183], v[204:207]\n"
"s_sub_u32 s82, s82, s81\n"
"v_mfma_f32_16x16x32_bf16 v[192:195], a[128:131], a[176:179], v[192:195]\n"
"s_sub_u32 s82, s82, s81\n"
"s_add_u32 s40, s56, s53\n"
"v_mfma_f32_16x16x32_bf16 v[192:195], a[132:135], a[180:183], v[192:195]\n"
"s_sub_u32 s82, s82, s81\n"
"buffer_load_dwordx4 v240, s[16:19], s40 offen offset:2048 nt lds\n"
"v_mfma_f32_16x16x32_bf16 v[196:199], a[136:139], a[176:179], v[196:199]\n"
"s_sub_u32 s82, s82, s81\n"
"v_mfma_f32_16x16x32_bf16 v[196:199], a[140:143], a[180:183], v[196:199]\n"
"s_sub_u32 s82, s82, s81\n"
"v_mfma_f32_16x16x32_bf16 v[220:223], a[136:139], a[184:187], v[220:223]\n"
"s_sub_u32 s82, s82, s81\n"
"v_mfma_f32_16x16x32_bf16 v[220:223], a[140:143], a[188:191], v[220:223]\n"
"s_sub_u32 s82, s82, s81\n"
"v_mfma_f32_16x16x32_bf16 v[216:219], a[128:131], a[184:187], v[216:219]\n"
"s_sub_u32 s82, s82, s81\n"
"s_add_u32 s40, s56, s54\n"
"v_mfma_f32_16x16x32_bf16 v[216:219], a[132:135], a[188:191], v[216:219]\n"
"s_sub_u32 s82, s82, s81\n"
"buffer_load_dwordx4 v240, s[16:19], s40 offen offset:3072 nt lds\n"
"v_mfma_f32_16x16x32_bf16 v[212:215], a[104:107], a[184:187], v[212:215]\n"
"s_sub_u32 s82, s82, s81\n"
"v_mfma_f32_16x16x32_bf16 v[212:215], a[108:111], a[188:191], v[212:215]\n"
"s_sub_u32 s82, s82, s81\n"
"v_mfma_f32_16x16x32_bf16 v[208:211], a[96:99], a[184:187], v[208:211]\n"
"s_sub_u32 s82, s82, s81\n"
"v_mfma_f32_16x16x32_bf16 v[208:211], a[100:103], a[188:191], v[208:211]\n"
"s_sub_u32 s82, s82, s81\n"
"v_mfma_f32_16x16x32_bf16 v[0:3], a[192:195], a[160:163], v[0:3]\n"
"v_mfma_f32_16x16x32_bf16 v[0:3], a[196:199], a[164:167], v[0:3]\n"
"v_mfma_f32_16x16x32_bf16 v[4:7], a[200:203], a[160:163], v[4:7]\n"
"s_waitcnt vmcnt(8) lgkmcnt(0)\n"
"v_mfma_f32_16x16x32_bf16 v[4:7], a[204:207], a[164:167], v[4:7]\n"
"s_barrier\n"
"v_mfma_f32_16x16x32_bf16 v[8:11], a[208:211], a[160:163], v[8:11]\n"
"s_add_u32 m0, s44, 65536\n"
"ds_read_b64_tr_b16 a[96:97], v244 offset:0\n"
"v_mfma_f32_16x16x32_bf16 v[8:11], a[212:215], a[164:167], v[8:11]\n"
"buffer_load_dwordx4 v240, s[20:23], s56 offen nt lds\n"
"v_mfma_f32_16x16x32_bf16 v[12:15], a[216:219], a[160:163], v[12:15]\n"
"s_lshr_b32 s82, s82, 31\n"
"ds_read_b64_tr_b16 a[98:99], v244 offset:512\n"
"v_mfma_f32_16x16x32_bf16 v[12:15], a[220:223], a[164:167], v[12:15]\n"
"s_add_u32 s82, s82, s60\n"
"ds_read_b64_tr_b16 a[100:101], v244 offset:8192\n"
"v_mfma_f32_16x16x32_bf16 v[16:19], a[224:227], a[160:163], v[16:19]\n"
"s_add_u32 s82, s82, 32\n"
"ds_read_b64_tr_b16 a[102:103], v244 offset:8704\n"
"v_mfma_f32_16x16x32_bf16 v[16:19], a[228:231], a[164:167], v[16:19]\n"
"v_mov_b32 v254, s82\n"
"ds_read_b64_tr_b16 a[104:105], v245 offset:0\n"
"v_mfma_f32_16x16x32_bf16 v[20:23], a[232:235], a[160:163], v[20:23]\n"
"v_lshlrev_b32_e64 v255, 2, s93\n"
"ds_read_b64_tr_b16 a[106:107], v245 offset:512\n"
"v_mfma_f32_16x16x32_bf16 v[20:23], a[236:239], a[164:167], v[20:23]\n"
"ds_read_b64_tr_b16 a[108:109], v245 offset:8192\n"
"v_mfma_f32_16x16x32_bf16 v[24:27], a[240:243], a[160:163], v[24:27]\n"
"s_add_u32 s40, s56, s52\n"
"ds_read_b64_tr_b16 a[110:111], v245 offset:8704\n"
"v_mfma_f32_16x16x32_bf16 v[24:27], a[244:247], a[164:167], v[24:27]\n"
"buffer_load_dwordx4 v240, s[20:23], s40 offen offset:1024 nt lds\n"
"v_mfma_f32_16x16x32_bf16 v[28:31], a[248:251], a[160:163], v[28:31]\n"
"ds_read_b128 a[160:163], v250 offset:0\n"
"v_mfma_f32_16x16x32_bf16 v[28:31], a[252:255], a[164:167], v[28:31]\n"
"s_mov_b64 exec, 1\n"
"global_store_dword v255, v254, s[6:7] offset:256 sc1\n"
"s_mov_b64 exec, -1\n"
"v_mfma_f32_16x16x32_bf16 v[60:63], a[248:251], a[168:171], v[60:63]\n"
"ds_read_b128 a[164:167], v251 offset:0\n"
"v_mfma_f32_16x16x32_bf16 v[60:63], a[252:255], a[172:175], v[60:63]\n"
"ds_read_b64_tr_b16 a[112:113], v246 offset:0\n"
"v_and_b32 v254, 0x20, v242\n"
"v_mfma_f32_16x16x32_bf16 v[56:59], a[240:243], a[168:171], v[56:59]\n"
"s_add_u32 s40, s56, s53\n"
"ds_read_b64_tr_b16 a[114:115], v246 offset:512\n"
"v_add_u32 v254, -1, v254\n"
"v_mfma_f32_16x16x32_bf16 v[56:59], a[244:247], a[172:175], v[56:59]\n"
"buffer_load_dwordx4 v240, s[20:23], s40 offen offset:2048 nt lds\n"
"v_mfma_f32_16x16x32_bf16 v[52:55], a[232:235], a[168:171], v[52:55]\n"
"ds_read_b64_tr_b16 a[116:117], v246 offset:8192\n"
"v_ashrrev_i32 v254, 31, v254\n"
"v_mfma_f32_16x16x32_bf16 v[52:55], a[236:239], a[172:175], v[52:55]\n"
"ds_read_b64_tr_b16 a[118:119], v246 offset:8704\n"
"v_and_b32 v254, 0x3f803f80, v254\n"
"v_mfma_f32_16x16x32_bf16 v[48:51], a[224:227], a[168:171], v[48:51]\n"
"ds_read_b64_tr_b16 a[120:121], v247 offset:0\n"
"v_mfma_f32_16x16x32_bf16 v[48:51], a[228:231], a[172:175], v[48:51]\n"
"ds_read_b64_tr_b16 a[122:123], v247 offset:512\n"
"v_mfma_f32_16x16x32_bf16 v[44:47], a[216:219], a[168:171], v[44:47]\n"
"s_add_u32 s40, s56, s54\n"
"ds_read_b64_tr_b16 a[124:125], v247 offset:8192\n"
"v_mfma_f32_16x16x32_bf16 v[44:47], a[220:223], a[172:175], v[44:47]\n"
"buffer_load_dwordx4 v240, s[20:23], s40 offen offset:3072 nt lds\n"
"v_mfma_f32_16x16x32_bf16 v[40:43], a[208:211], a[168:171], v[40:43]\n"
"s_add_u32 s55, s55, s59\n"
"ds_read_b64_tr_b16 a[126:127], v247 offset:8704\n"
"v_mfma_f32_16x16x32_bf16 v[40:43], a[212:215], a[172:175], v[40:43]\n"
"s_add_u32 s41, s55, s57\n"
"ds_read_b64_tr_b16 a[128:129], v244 offset:1024\n"
"v_mfma_f32_16x16x32_bf16 v[36:39], a[200:203], a[168:171], v[36:39]\n"
"s_and_b32 s41, s41, s58\n"
"ds_read_b64_tr_b16 a[130:131], v244 offset:1536\n"
"v_mfma_f32_16x16x32_bf16 v[36:39], a[204:207], a[172:175], v[36:39]\n"
"s_andn2_b32 s56, s55, s58\n"
"ds_read_b64_tr_b16 a[132:133], v244 offset:9216\n"
"v_mfma_f32_16x16x32_bf16 v[32:35], a[192:195], a[168:171], v[32:35]\n"
"s_or_b32 s56, s56, s41\n"
"ds_read_b64_tr_b16 a[134:135], v244 offset:9728\n"
"v_mfma_f32_16x16x32_bf16 v[32:35], a[196:199], a[172:175], v[32:35]\n"
"ds_read_b64_tr_b16 a[136:137], v245 offset:1024\n"
"v_mfma_f32_16x16x32_bf16 v[64:67], a[192:195], a[176:179], v[64:67]\n"
"s_add_u32 m0, s44, 16384\n"
"ds_read_b64_tr_b16 a[138:139], v245 offset:1536\n"
"v_mfma_f32_16x16x32_bf16 v[64:67], a[196:199], a[180:183], v[64:67]\n"
"buffer_load_dwordx4 v240, s[16:19], s56 offen nt lds\n"
"v_mfma_f32_16x16x32_bf16 v[68:71], a[200:203], a[176:179], v[68:71]\n"
"ds_read_b64_tr_b16 a[140:141], v245 offset:9216\n"
"v_mfma_f32_16x16x32_bf16 v[68:71], a[204:207], a[180:183], v[68:71]\n"
"ds_read_b64_tr_b16 a[142:143], v245 offset:9728\n"
"v_mfma_f32_16x16x32_bf16 v[72:75], a[208:211], a[176:179], v[72:75]\n"
"ds_read_b64_tr_b16 a[144:145], v246 offset:1024\n"
"v_mfma_f32_16x16x32_bf16 v[72:75], a[212:215], a[180:183], v[72:75]\n"
"ds_read_b64_tr_b16 a[146:147], v246 offset:1536\n"
"v_mfma_f32_16x16x32_bf16 v[76:79], a[216:219], a[176:179], v[76:79]\n"
"s_add_u32 s40, s56, s52\n"
"ds_read_b64_tr_b16 a[148:149], v246 offset:9216\n"
"v_mfma_f32_16x16x32_bf16 v[76:79], a[220:223], a[180:183], v[76:79]\n"
"buffer_load_dwordx4 v240, s[16:19], s40 offen offset:1024 nt lds\n"
"v_mfma_f32_16x16x32_bf16 v[88:91], a[240:243], a[176:179], v[88:91]\n"
"ds_read_b64_tr_b16 a[150:151], v246 offset:9728\n"
"v_mfma_f32_16x16x32_bf16 v[88:91], a[244:247], a[180:183], v[88:91]\n"
"ds_read_b64_tr_b16 a[152:153], v247 offset:1024\n"
"v_mfma_f32_16x16x32_bf16 v[92:95], a[248:251], a[176:179], v[92:95]\n"
"ds_read_b64_tr_b16 a[154:155], v247 offset:1536\n"
"v_mfma_f32_16x16x32_bf16 v[92:95], a[252:255], a[180:183], v[92:95]\n"
"ds_read_b64_tr_b16 a[156:157], v247 offset:9216\n"
"v_mfma_f32_16x16x32_bf16 v[80:83], a[224:227], a[176:179], v[80:83]\n"
"s_add_u32 s40, s56, s53\n"
"ds_read_b64_tr_b16 a[158:159], v247 offset:9728\n"
"v_mfma_f32_16x16x32_bf16 v[80:83], a[228:231], a[180:183], v[80:83]\n"
"buffer_load_dwordx4 v240, s[16:19], s40 offen offset:2048 nt lds\n"
"v_mfma_f32_16x16x32_bf16 v[84:87], a[232:235], a[176:179], v[84:87]\n"
"ds_read_b128 a[168:171], v250 offset:2048\n"
"v_mfma_f32_16x16x32_bf16 v[84:87], a[236:239], a[180:183], v[84:87]\n"
"ds_read_b128 a[172:175], v251 offset:2048\n"
"v_mfma_f32_16x16x32_bf16 v[108:111], a[232:235], a[184:187], v[108:111]\n"
"ds_read_b128 a[176:179], v250 offset:4096\n"
"v_mfma_f32_16x16x32_bf16 v[108:111], a[236:239], a[188:191], v[108:111]\n"
"ds_read_b128 a[180:183], v251 offset:4096\n"
"v_mfma_f32_16x16x32_bf16 v[104:107], a[224:227], a[184:187], v[104:107]\n"
"s_add_u32 s40, s56, s54\n"
"v_mfma_f32_16x16x32_bf16 v[104:107], a[228:231], a[188:191], v[104:107]\n"
"buffer_load_dwordx4 v240, s[16:19], s40 offen offset:3072 nt lds\n"
"v_mfma_f32_16x16x32_bf16 v[100:103], a[200:203], a[184:187], v[100:103]\n"
"v_mfma_f32_16x16x32_bf16 v[100:103], a[204:207], a[188:191], v[100:103]\n"
"v_mfma_f32_16x16x32_bf16 v[96:99], a[192:195], a[184:187], v[96:99]\n"
"ds_read_b128 a[184:187], v252 offset:0\n"
"v_mfma_f32_16x16x32_bf16 v[96:99], a[196:199], a[188:191], v[96:99]\n"
"s_waitcnt lgkmcnt(3)\n"
"ds_read_b128 a[188:191], v253 offset:0\n"
"v_mfma_f32_16x16x32_bf16 v[112:115], a[96:99], a[160:163], v[112:115]\n"
"v_mfma_f32_16x16x32_bf16 v[112:115], a[100:103], a[164:167], v[112:115]\n"
"v_mfma_f32_16x16x32_bf16 v[116:119], a[104:107], a[160:163], v[116:119]\n"
"s_waitcnt vmcnt(13)\n"
"v_mfma_f32_16x16x32_bf16 v[116:119], a[108:111], a[164:167], v[116:119]\n"
"s_barrier\n"
"v_mfma_f32_16x16x32_bf16 v[120:123], a[112:115], a[160:163], v[120:123]\n"
"s_add_u32 m0, s44, 81920\n"
"ds_read_b64_tr_b16 a[232:233], v245 offset:17408\n"
"v_mfma_f32_16x16x32_bf16 v[120:123], a[116:119], a[164:167], v[120:123]\n"
"buffer_load_dwordx4 v240, s[20:23], s56 offen nt lds\n"
"v_mfma_f32_16x16x32_bf16 v[124:127], a[120:123], a[160:163], v[124:127]\n"
"ds_read_b64_tr_b16 a[234:235], v245 offset:17920\n"
"v_mfma_f32_16x16x32_bf16 v[124:127], a[124:127], a[164:167], v[124:127]\n"
"ds_read_b64_tr_b16 a[236:237], v245 offset:25600\n"
"v_mfma_f32_16x16x32_bf16 v[128:131], a[128:131], a[160:163], v[128:131]\n"
"ds_read_b64_tr_b16 a[238:239], v245 offset:26112\n"
"v_mfma_f32_16x16x32_bf16 v[128:131], a[132:135], a[164:167], v[128:131]\n"
"ds_read_b64_tr_b16 a[224:225], v244 offset:17408\n"
"v_mfma_f32_16x16x32_bf16 v[132:135], a[136:139], a[160:163], v[132:135]\n"
"ds_read_b64_tr_b16 a[226:227], v244 offset:17920\n"
"v_mfma_f32_16x16x32_bf16 v[132:135], a[140:143], a[164:167], v[132:135]\n"
"ds_read_b64_tr_b16 a[192:193], v244 offset:16384\n"
"v_mfma_f32_16x16x32_bf16 v[136:139], a[144:147], a[160:163], v[136:139]\n"
"ds_read_b64_tr_b16 a[194:195], v244 offset:16896\n"
"v_mfma_f32_16x16x32_bf16 v[136:139], a[148:151], a[164:167], v[136:139]\n"
"ds_read_b64_tr_b16 a[196:197], v244 offset:24576\n"
"v_mfma_f32_16x16x32_bf16 v[140:143], a[152:155], a[160:163], v[140:143]\n"
"ds_read_b64_tr_b16 a[198:199], v244 offset:25088\n"
"v_mfma_f32_16x16x32_bf16 v[140:143], a[156:159], a[164:167], v[140:143]\n"
"s_waitcnt lgkmcnt(7)\n"
"ds_read_b64_tr_b16 a[200:201], v245 offset:16384\n"
"v_mfma_f32_16x16x32_bf16 v[220:223], a[136:139], a[184:187], v[220:223]\n"
"ds_read_b64_tr_b16 a[202:203], v245 offset:16896\n"
"v_mfma_f32_16x16x32_bf16 v[220:223], a[140:143], a[188:191], v[220:223]\n"
"ds_read_b64_tr_b16 a[204:205], v245 offset:24576\n"
"v_mfma_f32_16x16x32_bf16 v[216:219], a[128:131], a[184:187], v[216:219]\n"
"s_add_u32 s40, s56, s52\n"
"ds_read_b64_tr_b16 a[206:207], v245 offset:25088\n"
"v_mfma_f32_16x16x32_bf16 v[216:219], a[132:135], a[188:191], v[216:219]\n"
"buffer_load_dwordx4 v240, s[20:23], s40 offen offset:1024 nt lds\n"
"v_mfma_f32_16x16x32_bf16 v[212:215], a[104:107], a[184:187], v[212:215]\n"
"ds_read_b64_tr_b16 a[208:209], v246 offset:16384\n"
"v_mfma_f32_16x16x32_bf16 v[212:215], a[108:111], a[188:191], v[212:215]\n"
"v_pk_fma_f32 v[112:113], v[112:113], s[70:71], s[70:71] op_sel:[0,0,1] op_sel_hi:[1,0,1]\nv_pk_fma_f32 v[114:115], v[114:115], s[70:71], s[70:71] op_sel:[0,0,1] op_sel_hi:[1,0,1]\nv_pk_fma_f32 v[116:117], v[116:117], s[70:71], s[70:71] op_sel:[0,0,1] op_sel_hi:[1,0,1]\nv_pk_fma_f32 v[118:119], v[118:119], s[70:71], s[70:71] op_sel:[0,0,1] op_sel_hi:[1,0,1]\nv_pk_fma_f32 v[120:121], v[120:121], s[70:71], s[70:71] op_sel:[0,0,1] op_sel_hi:[1,0,1]\nv_pk_fma_f32 v[122:123], v[122:123], s[70:71], s[70:71] op_sel:[0,0,1] op_sel_hi:[1,0,1]\nv_pk_fma_f32 v[124:125], v[124:125], s[70:71], s[70:71] op_sel:[0,0,1] op_sel_hi:[1,0,1]\nv_pk_fma_f32 v[126:127], v[126:127], s[70:71], s[70:71] op_sel:[0,0,1] op_sel_hi:[1,0,1]\nv_pk_fma_f32 v[128:129], v[128:129], s[70:71], s[70:71] op_sel:[0,0,1] op_sel_hi:[1,0,1]\nv_pk_fma_f32 v[130:131], v[130:131], s[70:71], s[70:71] op_sel:[0,0,1] op_sel_hi:[1,0,1]\nv_pk_fma_f32 v[132:133], v[132:133], s[70:71], s[70:71] op_sel:[0,0,1] op_sel_hi:[1,0,1]\nv_pk_fma_f32 v[134:135], v[134:135], s[70:71], s[70:71] op_sel:[0,0,1] op_sel_hi:[1,0,1]\nv_pk_fma_f32 v[136:137], v[136:137], s[70:71], s[70:71] op_sel:[0,0,1] op_sel_hi:[1,0,1]\nv_pk_fma_f32 v[138:139], v[138:139], s[70:71], s[70:71] op_sel:[0,0,1] op_sel_hi:[1,0,1]\nv_pk_fma_f32 v[140:141], v[140:141], s[70:71], s[70:71] op_sel:[0,0,1] op_sel_hi:[1,0,1]\nv_pk_fma_f32 v[142:143], v[142:143], s[70:71], s[70:71] op_sel:[0,0,1] op_sel_hi:[1,0,1]\n"
"v_mfma_f32_16x16x32_bf16 v[208:211], a[96:99], a[184:187], v[208:211]\n"
"ds_read_b64_tr_b16 a[210:211], v246 offset:16896\n"
"v_cvt_pk_bf16_f32 v128, v128, v129\n"
"v_mfma_f32_16x16x32_bf16 v[208:211], a[100:103], a[188:191], v[208:211]\n"
"ds_read_b64_tr_b16 a[212:213], v246 offset:24576\n"
"v_cvt_pk_bf16_f32 v129, v130, v131\n"
"v_mfma_f32_16x16x32_bf16 v[172:175], a[152:155], a[168:171], v[172:175]\n"
"ds_read_b64_tr_b16 a[214:215], v246 offset:25088\n"
"v_cvt_pk_bf16_f32 v130, v132, v133\n"
"v_mfma_f32_16x16x32_bf16 v[172:175], a[156:159], a[172:175], v[172:175]\n"
"ds_read_b64_tr_b16 a[216:217], v247 offset:16384\n"
"v_cvt_pk_bf16_f32 v131, v134, v135\n"
"v_mfma_f32_16x16x32_bf16 v[168:171], a[144:147], a[168:171], v[168:171]\n"
"ds_read_b64_tr_b16 a[218:219], v247 offset:16896\n"
"v_cvt_pk_bf16_f32 v136, v136, v137\n"
"v_mfma_f32_16x16x32_bf16 v[168:171], a[148:151], a[172:175], v[168:171]\n"
"ds_read_b64_tr_b16 a[220:221], v247 offset:24576\n"
"v_cvt_pk_bf16_f32 v137, v138, v139\n"
"v_mfma_f32_16x16x32_bf16 v[164:167], a[136:139], a[168:171], v[164:167]\n"
"ds_read_b64_tr_b16 a[222:223], v247 offset:25088\n"
"v_cvt_pk_bf16_f32 v138, v140, v141\n"
"v_mfma_f32_16x16x32_bf16 v[164:167], a[140:143], a[172:175], v[164:167]\n"
"ds_read_b64_tr_b16 a[228:229], v244 offset:25600\n"
"v_cvt_pk_bf16_f32 v139, v142, v143\n"
"v_mfma_f32_16x16x32_bf16 v[160:163], a[128:131], a[168:171], v[160:163]\n"
"s_add_u32 s40, s56, s53\n"
"ds_read_b64_tr_b16 a[230:231], v244 offset:26112\n"
"v_cvt_pk_bf16_f32 v120, v120, v121\n"
"v_mfma_f32_16x16x32_bf16 v[160:163], a[132:135], a[172:175], v[160:163]\n"
"buffer_load_dwordx4 v240, s[20:23], s40 offen offset:2048 nt lds\n"
"v_mfma_f32_16x16x32_bf16 v[156:159], a[120:123], a[168:171], v[156:159]\n"
"ds_read_b64_tr_b16 a[240:241], v246 offset:17408\n"
"v_cvt_pk_bf16_f32 v121, v122, v123\n"
"v_mfma_f32_16x16x32_bf16 v[156:159], a[124:127], a[172:175], v[156:159]\n"
"ds_read_b64_tr_b16 a[242:243], v246 offset:17920\n"
"v_cvt_pk_bf16_f32 v122, v124, v125\n"
"v_mfma_f32_16x16x32_bf16 v[152:155], a[112:115], a[168:171], v[152:155]\n"
"ds_read_b64_tr_b16 a[244:245], v246 offset:25600\n"
"v_cvt_pk_bf16_f32 v123, v126, v127\n"
"v_mfma_f32_16x16x32_bf16 v[152:155], a[116:119], a[172:175], v[152:155]\n"
"ds_read_b64_tr_b16 a[246:247], v246 offset:26112\n"
"v_cvt_pk_bf16_f32 v112, v112, v113\n"
"v_mfma_f32_16x16x32_bf16 v[148:151], a[104:107], a[168:171], v[148:151]\n"
"ds_read_b64_tr_b16 a[248:249], v247 offset:17408\n"
"v_cvt_pk_bf16_f32 v113, v114, v115\n"
"v_mfma_f32_16x16x32_bf16 v[148:151], a[108:111], a[172:175], v[148:151]\n"
"ds_read_b64_tr_b16 a[250:251], v247 offset:17920\n"
"v_cvt_pk_bf16_f32 v114, v116, v117\n"
"v_mfma_f32_16x16x32_bf16 v[144:147], a[96:99], a[168:171], v[144:147]\n"
"ds_read_b64_tr_b16 a[252:253], v247 offset:25600\n"
"v_cvt_pk_bf16_f32 v115, v118, v119\n"
"v_mfma_f32_16x16x32_bf16 v[144:147], a[100:103], a[172:175], v[144:147]\n"
"v_pk_fma_f32 v[208:209], v[208:209], s[70:71], s[70:71] op_sel:[0,0,1] op_sel_hi:[1,0,1]\nv_pk_fma_f32 v[210:211], v[210:211], s[70:71], s[70:71] op_sel:[0,0,1] op_sel_hi:[1,0,1]\nv_pk_fma_f32 v[212:213], v[212:213], s[70:71], s[70:71] op_sel:[0,0,1] op_sel_hi:[1,0,1]\nv_pk_fma_f32 v[214:215], v[214:215], s[70:71], s[70:71] op_sel:[0,0,1] op_sel_hi:[1,0,1]\nv_pk_fma_f32 v[216:217], v[216:217], s[70:71], s[70:71] op_sel:[0,0,1] op_sel_hi:[1,0,1]\nv_pk_fma_f32 v[218:219], v[218:219], s[70:71], s[70:71] op_sel:[0,0,1] op_sel_hi:[1,0,1]\nv_pk_fma_f32 v[220:221], v[220:221], s[70:71], s[70:71] op_sel:[0,0,1] op_sel_hi:[1,0,1]\nv_pk_fma_f32 v[222:223], v[222:223], s[70:71], s[70:71] op_sel:[0,0,1] op_sel_hi:[1,0,1]\n"
"v_mfma_f32_16x16x32_bf16 v[176:179], a[96:99], a[176:179], v[176:179]\n"
"ds_read_b64_tr_b16 a[254:255], v247 offset:26112\n"
"v_cvt_pk_bf16_f32 v208, v208, v209\n"
"v_mfma_f32_16x16x32_bf16 v[176:179], a[100:103], a[180:183], v[176:179]\n"
"v_permlane16_swap_b32 v128, v130\n"
"v_mfma_f32_16x16x32_bf16 v[180:183], a[104:107], a[176:179], v[180:183]\n"
"v_permlane16_swap_b32 v129, v131\n"
"v_mfma_f32_16x16x32_bf16 v[180:183], a[108:111], a[180:183], v[180:183]\n"
"v_permlane16_swap_b32 v136, v138\n"
"v_mfma_f32_16x16x32_bf16 v[184:187], a[112:115], a[176:179], v[184:187]\n"
"s_add_u32 s40, s56, s54\n"
"v_permlane16_swap_b32 v137, v139\n"
"v_mfma_f32_16x16x32_bf16 v[184:187], a[116:119], a[180:183], v[184:187]\n"
"buffer_load_dwordx4 v240, s[20:23], s40 offen offset:3072 nt lds\n"
"v_mfma_f32_16x16x32_bf16 v[188:191], a[120:123], a[176:179], v[188:191]\n"
"s_add_u32 s55, s55, s59\n"
"v_pk_fma_f32 v[144:145], v[144:145], s[70:71], s[70:71] op_sel:[0,0,1] op_sel_hi:[1,0,1]\nv_pk_fma_f32 v[146:147], v[146:147], s[70:71], s[70:71] op_sel:[0,0,1] op_sel_hi:[1,0,1]\nv_pk_fma_f32 v[148:149], v[148:149], s[70:71], s[70:71] op_sel:[0,0,1] op_sel_hi:[1,0,1]\nv_pk_fma_f32 v[150:151], v[150:151], s[70:71], s[70:71] op_sel:[0,0,1] op_sel_hi:[1,0,1]\nv_pk_fma_f32 v[152:153], v[152:153], s[70:71], s[70:71] op_sel:[0,0,1] op_sel_hi:[1,0,1]\nv_pk_fma_f32 v[154:155], v[154:155], s[70:71], s[70:71] op_sel:[0,0,1] op_sel_hi:[1,0,1]\nv_pk_fma_f32 v[156:157], v[156:157], s[70:71], s[70:71] op_sel:[0,0,1] op_sel_hi:[1,0,1]\nv_pk_fma_f32 v[158:159], v[158:159], s[70:71], s[70:71] op_sel:[0,0,1] op_sel_hi:[1,0,1]\nv_pk_fma_f32 v[160:161], v[160:161], s[70:71], s[70:71] op_sel:[0,0,1] op_sel_hi:[1,0,1]\nv_pk_fma_f32 v[162:163], v[162:163], s[70:71], s[70:71] op_sel:[0,0,1] op_sel_hi:[1,0,1]\nv_pk_fma_f32 v[164:165], v[164:165], s[70:71], s[70:71] op_sel:[0,0,1] op_sel_hi:[1,0,1]\nv_pk_fma_f32 v[166:167], v[166:167], s[70:71], s[70:71] op_sel:[0,0,1] op_sel_hi:[1,0,1]\nv_pk_fma_f32 v[168:169], v[168:169], s[70:71], s[70:71] op_sel:[0,0,1] op_sel_hi:[1,0,1]\nv_pk_fma_f32 v[170:171], v[170:171], s[70:71], s[70:71] op_sel:[0,0,1] op_sel_hi:[1,0,1]\nv_pk_fma_f32 v[172:173], v[172:173], s[70:71], s[70:71] op_sel:[0,0,1] op_sel_hi:[1,0,1]\nv_pk_fma_f32 v[174:175], v[174:175], s[70:71], s[70:71] op_sel:[0,0,1] op_sel_hi:[1,0,1]\n"
"v_mfma_f32_16x16x32_bf16 v[188:191], a[124:127], a[180:183], v[188:191]\n"
"s_add_u32 s41, s55, s57\n"
"v_permlane16_swap_b32 v112, v114\n"
"v_mfma_f32_16x16x32_bf16 v[200:203], a[144:147], a[176:179], v[200:203]\n"
"s_and_b32 s41, s41, s58\n"
"v_permlane16_swap_b32 v113, v115\n"
"v_mfma_f32_16x16x32_bf16 v[200:203], a[148:151], a[180:183], v[200:203]\n"
"s_andn2_b32 s56, s55, s58\n"
"v_permlane16_swap_b32 v120, v122\n"
"v_mfma_f32_16x16x32_bf16 v[204:207], a[152:155], a[176:179], v[204:207]\n"
"s_or_b32 s56, s56, s41\n"
"v_permlane16_swap_b32 v121, v123\n"
"v_mfma_f32_16x16x32_bf16 v[204:207], a[156:159], a[180:183], v[204:207]\n"
"v_cvt_pk_bf16_f32 v209, v210, v211\n"
"v_cvt_pk_bf16_f32 v210, v212, v213\n"
"v_mfma_f32_16x16x32_bf16 v[192:195], a[128:131], a[176:179], v[192:195]\n"
"s_nop 0\n"
"v_permlane16_swap_b32 v208, v210\n"
"v_mfma_f32_16x16x32_bf16 v[192:195], a[132:135], a[180:183], v[192:195]\n"
"v_cvt_pk_bf16_f32 v211, v214, v215\n"
"v_cvt_pk_bf16_f32 v216, v216, v217\n"
"v_mfma_f32_16x16x32_bf16 v[196:199], a[136:139], a[176:179], v[196:199]\n"
"v_permlane16_swap_b32 v209, v211\n"
"v_mfma_f32_16x16x32_bf16 v[196:199], a[140:143], a[180:183], v[196:199]\n"
"ds_write_b128 v243, v[208:211] offset:8192\n"
"v_mfma_f32_16x16x32_bf16 v[108:111], a[232:235], a[184:187], v[108:111]\n"
"s_waitcnt lgkmcnt(1)\n"
"s_add_u32 m0, s44, 32768\n"
"v_cvt_pk_bf16_f32 v217, v218, v219\n"
"v_cvt_pk_bf16_f32 v218, v220, v221\n"
"v_mfma_f32_16x16x32_bf16 v[108:111], a[236:239], a[188:191], v[108:111]\n"
"buffer_load_dwordx4 v240, s[16:19], s56 offen nt lds\n"
"v_mfma_f32_16x16x32_bf16 v[104:107], a[224:227], a[184:187], v[104:107]\n"
"s_waitcnt vmcnt(14)\n"
"v_permlane16_swap_b32 v216, v218\n"
"v_mfma_f32_16x16x32_bf16 v[104:107], a[228:231], a[188:191], v[104:107]\n"
"s_barrier\n"
"v_mfma_f32_16x16x32_bf16 v[100:103], a[200:203], a[184:187], v[100:103]\n"
"v_cvt_pk_bf16_f32 v219, v222, v223\n"
"v_cvt_pk_bf16_f32 v144, v144, v145\n"
"v_mfma_f32_16x16x32_bf16 v[100:103], a[204:207], a[188:191], v[100:103]\n"
"s_mov_b32 s74, 0x41400000\n"
"v_permlane16_swap_b32 v217, v219\n"
"v_mfma_f32_16x16x32_bf16 v[96:99], a[192:195], a[184:187], v[96:99]\n"
"ds_write_b128 v243, v[216:219] offset:10240\n"
"v_mfma_f32_16x16x32_bf16 v[96:99], a[196:199], a[188:191], v[96:99]\n"
"v_pk_fma_f32 v[176:177], v[176:177], s[70:71], s[70:71] op_sel:[0,0,1] op_sel_hi:[1,0,1]\nv_pk_fma_f32 v[178:179], v[178:179], s[70:71], s[70:71] op_sel:[0,0,1] op_sel_hi:[1,0,1]\nv_pk_fma_f32 v[180:181], v[180:181], s[70:71], s[70:71] op_sel:[0,0,1] op_sel_hi:[1,0,1]\nv_pk_fma_f32 v[182:183], v[182:183], s[70:71], s[70:71] op_sel:[0,0,1] op_sel_hi:[1,0,1]\nv_pk_fma_f32 v[184:185], v[184:185], s[70:71], s[70:71] op_sel:[0,0,1] op_sel_hi:[1,0,1]\nv_pk_fma_f32 v[186:187], v[186:187], s[70:71], s[70:71] op_sel:[0,0,1] op_sel_hi:[1,0,1]\nv_pk_fma_f32 v[188:189], v[188:189], s[70:71], s[70:71] op_sel:[0,0,1] op_sel_hi:[1,0,1]\nv_pk_fma_f32 v[190:191], v[190:191], s[70:71], s[70:71] op_sel:[0,0,1] op_sel_hi:[1,0,1]\nv_pk_fma_f32 v[192:193], v[192:193], s[70:71], s[70:71] op_sel:[0,0,1] op_sel_hi:[1,0,1]\nv_pk_fma_f32 v[194:195], v[194:195], s[70:71], s[70:71] op_sel:[0,0,1] op_sel_hi:[1,0,1]\nv_pk_fma_f32 v[196:197], v[196:197], s[70:71], s[70:71] op_sel:[0,0,1] op_sel_hi:[1,0,1]\nv_pk_fma_f32 v[198:199], v[198:199], s[70:71], s[70:71] op_sel:[0,0,1] op_sel_hi:[1,0,1]\nv_pk_fma_f32 v[200:201], v[200:201], s[70:71], s[70:71] op_sel:[0,0,1] op_sel_hi:[1,0,1]\nv_pk_fma_f32 v[202:203], v[202:203], s[70:71], s[70:71] op_sel:[0,0,1] op_sel_hi:[1,0,1]\nv_pk_fma_f32 v[204:205], v[204:205], s[70:71], s[70:71] op_sel:[0,0,1] op_sel_hi:[1,0,1]\nv_pk_fma_f32 v[206:207], v[206:207], s[70:71], s[70:71] op_sel:[0,0,1] op_sel_hi:[1,0,1]\n"
"v_mfma_f32_16x16x32_bf16 v[0:3], a[192:195], a[160:163], v[0:3]\n"
"v_cvt_pk_bf16_f32 v145, v146, v147\n"
"v_cvt_pk_bf16_f32 v146, v148, v149\n"
"v_mfma_f32_16x16x32_bf16 v[0:3], a[196:199], a[164:167], v[0:3]\n"
"s_nop 0\n"
"v_permlane16_swap_b32 v144, v146\n"
"v_mfma_f32_16x16x32_bf16 v[4:7], a[200:203], a[160:163], v[4:7]\n"
"v_bfe_u32 v149, v242, 8, 2\n"
"v_lshl_add_u32 v149, v149, 4, s43\n"
"v_mfma_f32_16x16x32_bf16 v[4:7], a[204:207], a[164:167], v[4:7]\n"
"ds_read_b128 v[208:211], v149 offset:0\n"
"v_cvt_pk_bf16_f32 v147, v150, v151\n"
"v_mfma_f32_16x16x32_bf16 v[8:11], a[208:211], a[160:163], v[8:11]\n"
"ds_read_b128 v[216:219], v149 offset:1024\n"
"v_xor_b32 v150, 64, v149\n"
"v_mfma_f32_16x16x32_bf16 v[8:11], a[212:215], a[164:167], v[8:11]\n"
"ds_read_b128 v[212:215], v150 offset:0\n"
"v_cvt_pk_bf16_f32 v176, v176, v177\n"
"v_mfma_f32_16x16x32_bf16 v[12:15], a[216:219], a[160:163], v[12:15]\n"
"s_add_u32 s40, s56, s52\n"
"ds_read_b128 v[220:223], v150 offset:1024\n"
"v_cvt_pk_bf16_f32 v177, v178, v179\n"
"v_mfma_f32_16x16x32_bf16 v[12:15], a[220:223], a[164:167], v[12:15]\n"
"buffer_load_dwordx4 v240, s[16:19], s40 offen offset:1024 nt lds\n"
"v_mfma_f32_16x16x32_bf16 v[16:19], a[224:227], a[160:163], v[16:19]\n"
"v_permlane16_swap_b32 v145, v147\n"
"v_mfma_f32_16x16x32_bf16 v[16:19], a[228:231], a[164:167], v[16:19]\n"
"v_pk_fma_f32 v[96:97], v[96:97], s[70:71], s[70:71] op_sel:[0,0,1] op_sel_hi:[1,0,1]\nv_pk_fma_f32 v[98:99], v[98:99], s[70:71], s[70:71] op_sel:[0,0,1] op_sel_hi:[1,0,1]\nv_pk_fma_f32 v[100:101], v[100:101], s[70:71], s[70:71] op_sel:[0,0,1] op_sel_hi:[1,0,1]\nv_pk_fma_f32 v[102:103], v[102:103], s[70:71], s[70:71] op_sel:[0,0,1] op_sel_hi:[1,0,1]\nv_pk_fma_f32 v[104:105], v[104:105], s[70:71], s[70:71] op_sel:[0,0,1] op_sel_hi:[1,0,1]\nv_pk_fma_f32 v[106:107], v[106:107], s[70:71], s[70:71] op_sel:[0,0,1] op_sel_hi:[1,0,1]\nv_pk_fma_f32 v[108:109], v[108:109], s[70:71], s[70:71] op_sel:[0,0,1] op_sel_hi:[1,0,1]\nv_pk_fma_f32 v[110:111], v[110:111], s[70:71], s[70:71] op_sel:[0,0,1] op_sel_hi:[1,0,1]\n"
"v_mfma_f32_16x16x32_bf16 v[20:23], a[232:235], a[160:163], v[20:23]\n"
"v_cvt_pk_bf16_f32 v178, v180, v181\n"
"v_cvt_pk_bf16_f32 v179, v182, v183\n"
"v_mfma_f32_16x16x32_bf16 v[20:23], a[236:239], a[164:167], v[20:23]\n"
"v_permlane16_swap_b32 v176, v178\n"
"v_mfma_f32_16x16x32_bf16 v[24:27], a[240:243], a[160:163], v[24:27]\n"
"v_permlane16_swap_b32 v177, v179\n"
"v_mfma_f32_16x16x32_bf16 v[24:27], a[244:247], a[164:167], v[24:27]\n"
"v_cvt_pk_bf16_f32 v184, v184, v185\n"
"v_cvt_pk_bf16_f32 v185, v186, v187\n"
"v_mfma_f32_16x16x32_bf16 v[28:31], a[248:251], a[160:163], v[28:31]\n"
"v_cvt_pk_bf16_f32 v186, v188, v189\n"
"v_cvt_pk_bf16_f32 v187, v190, v191\n"
"v_mfma_f32_16x16x32_bf16 v[28:31], a[252:255], a[164:167], v[28:31]\n"
"v_permlane16_swap_b32 v184, v186\n"
"v_mfma_f32_16x16x32_bf16 v[60:63], a[248:251], a[168:171], v[60:63]\n"
"v_permlane16_swap_b32 v185, v187\n"
"v_mfma_f32_16x16x32_bf16 v[60:63], a[252:255], a[172:175], v[60:63]\n"
"v_accvgpr_write_b32 a132, v184\n"
"v_accvgpr_write_b32 a133, v185\n"
"v_mfma_f32_16x16x32_bf16 v[56:59], a[240:243], a[168:171], v[56:59]\n"
"s_add_u32 s40, s56, s53\n"
"v_accvgpr_write_b32 a134, v186\n"
"v_accvgpr_write_b32 a135, v187\n"
"v_mfma_f32_16x16x32_bf16 v[56:59], a[244:247], a[172:175], v[56:59]\n"
"buffer_load_dwordx4 v240, s[16:19], s40 offen offset:2048 nt lds\n"
"v_mfma_f32_16x16x32_bf16 v[52:55], a[232:235], a[168:171], v[52:55]\n"
"v_xor_b32 v148, 0x400, v243\n"
"v_cvt_pk_bf16_f32 v192, v192, v193\n"
"v_mfma_f32_16x16x32_bf16 v[52:55], a[236:239], a[172:175], v[52:55]\n"
"v_pk_fma_f32 v[0:1], v[0:1], s[70:71], s[70:71] op_sel:[0,0,1] op_sel_hi:[1,0,1]\nv_pk_fma_f32 v[2:3], v[2:3], s[70:71], s[70:71] op_sel:[0,0,1] op_sel_hi:[1,0,1]\nv_pk_fma_f32 v[4:5], v[4:5], s[70:71], s[70:71] op_sel:[0,0,1] op_sel_hi:[1,0,1]\nv_pk_fma_f32 v[6:7], v[6:7], s[70:71], s[70:71] op_sel:[0,0,1] op_sel_hi:[1,0,1]\nv_pk_fma_f32 v[8:9], v[8:9], s[70:71], s[70:71] op_sel:[0,0,1] op_sel_hi:[1,0,1]\nv_pk_fma_f32 v[10:11], v[10:11], s[70:71], s[70:71] op_sel:[0,0,1] op_sel_hi:[1,0,1]\nv_pk_fma_f32 v[12:13], v[12:13], s[70:71], s[70:71] op_sel:[0,0,1] op_sel_hi:[1,0,1]\nv_pk_fma_f32 v[14:15], v[14:15], s[70:71], s[70:71] op_sel:[0,0,1] op_sel_hi:[1,0,1]\nv_pk_fma_f32 v[16:17], v[16:17], s[70:71], s[70:71] op_sel:[0,0,1] op_sel_hi:[1,0,1]\nv_pk_fma_f32 v[18:19], v[18:19], s[70:71], s[70:71] op_sel:[0,0,1] op_sel_hi:[1,0,1]\nv_pk_fma_f32 v[20:21], v[20:21], s[70:71], s[70:71] op_sel:[0,0,1] op_sel_hi:[1,0,1]\nv_pk_fma_f32 v[22:23], v[22:23], s[70:71], s[70:71] op_sel:[0,0,1] op_sel_hi:[1,0,1]\nv_pk_fma_f32 v[24:25], v[24:25], s[70:71], s[70:71] op_sel:[0,0,1] op_sel_hi:[1,0,1]\nv_pk_fma_f32 v[26:27], v[26:27], s[70:71], s[70:71] op_sel:[0,0,1] op_sel_hi:[1,0,1]\nv_pk_fma_f32 v[28:29], v[28:29], s[70:71], s[70:71] op_sel:[0,0,1] op_sel_hi:[1,0,1]\nv_pk_fma_f32 v[30:31], v[30:31], s[70:71], s[70:71] op_sel:[0,0,1] op_sel_hi:[1,0,1]\n"
"v_mfma_f32_16x16x32_bf16 v[48:51], a[224:227], a[168:171], v[48:51]\n"
"v_cvt_pk_bf16_f32 v193, v194, v195\n"
"v_cvt_pk_bf16_f32 v194, v196, v197\n"
"v_mfma_f32_16x16x32_bf16 v[48:51], a[228:231], a[172:175], v[48:51]\n"
"s_nop 0\n"
"v_permlane16_swap_b32 v192, v194\n"
"v_mfma_f32_16x16x32_bf16 v[44:47], a[216:219], a[168:171], v[44:47]\n"
"v_cvt_pk_bf16_f32 v195, v198, v199\n"
"v_accvgpr_write_b32 a136, v192\n"
"v_mfma_f32_16x16x32_bf16 v[44:47], a[220:223], a[172:175], v[44:47]\n"
"v_permlane16_swap_b32 v193, v195\n"
"v_mfma_f32_16x16x32_bf16 v[40:43], a[208:211], a[168:171], v[40:43]\n"
"v_accvgpr_write_b32 a137, v193\n"
"v_accvgpr_write_b32 a138, v194\n"
"v_mfma_f32_16x16x32_bf16 v[40:43], a[212:215], a[172:175], v[40:43]\n"
"v_accvgpr_write_b32 a139, v195\n"
"v_cvt_pk_bf16_f32 v152, v152, v153\n"
"v_mfma_f32_16x16x32_bf16 v[36:39], a[200:203], a[168:171], v[36:39]\n"
"v_cvt_pk_bf16_f32 v153, v154, v155\n"
"v_cvt_pk_bf16_f32 v154, v156, v157\n"
"v_mfma_f32_16x16x32_bf16 v[36:39], a[204:207], a[172:175], v[36:39]\n"
"s_nop 0\n"
"v_permlane16_swap_b32 v152, v154\n"
"v_mfma_f32_16x16x32_bf16 v[32:35], a[192:195], a[168:171], v[32:35]\n"
"v_cvt_pk_bf16_f32 v155, v158, v159\n"
"v_accvgpr_write_b32 a116, v152\n"
"v_mfma_f32_16x16x32_bf16 v[32:35], a[196:199], a[172:175], v[32:35]\n"
"v_permlane16_swap_b32 v153, v155\n"
"v_mfma_f32_16x16x32_bf16 v[64:67], a[192:195], a[176:179], v[64:67]\n"
"s_add_u32 s40, s56, s54\n"
"ds_read_b128 a[192:195], v243 offset:0\n"
"v_accvgpr_write_b32 a117, v153\n"
"v_mfma_f32_16x16x32_bf16 v[64:67], a[196:199], a[180:183], v[64:67]\n"
"buffer_load_dwordx4 v240, s[16:19], s40 offen offset:3072 nt lds\n"
"v_mfma_f32_16x16x32_bf16 v[68:71], a[200:203], a[176:179], v[68:71]\n"
"ds_read_b128 a[196:199], v148 offset:0\n"
"v_accvgpr_write_b32 a118, v154\n"
"v_mfma_f32_16x16x32_bf16 v[68:71], a[204:207], a[180:183], v[68:71]\n"
"ds_read_b128 a[200:203], v243 offset:2048\n"
"v_accvgpr_write_b32 a119, v155\n"
"v_mfma_f32_16x16x32_bf16 v[72:75], a[208:211], a[176:179], v[72:75]\n"
"ds_read_b128 a[204:207], v148 offset:2048\n"
"v_cvt_pk_bf16_f32 v160, v160, v161\n"
"v_mfma_f32_16x16x32_bf16 v[72:75], a[212:215], a[180:183], v[72:75]\n"
"v_pk_fma_f32 v[32:33], v[32:33], s[70:71], s[70:71] op_sel:[0,0,1] op_sel_hi:[1,0,1]\nv_pk_fma_f32 v[34:35], v[34:35], s[70:71], s[70:71] op_sel:[0,0,1] op_sel_hi:[1,0,1]\nv_pk_fma_f32 v[36:37], v[36:37], s[70:71], s[70:71] op_sel:[0,0,1] op_sel_hi:[1,0,1]\nv_pk_fma_f32 v[38:39], v[38:39], s[70:71], s[70:71] op_sel:[0,0,1] op_sel_hi:[1,0,1]\nv_pk_fma_f32 v[40:41], v[40:41], s[70:71], s[70:71] op_sel:[0,0,1] op_sel_hi:[1,0,1]\nv_pk_fma_f32 v[42:43], v[42:43], s[70:71], s[70:71] op_sel:[0,0,1] op_sel_hi:[1,0,1]\nv_pk_fma_f32 v[44:45], v[44:45], s[70:71], s[70:71] op_sel:[0,0,1] op_sel_hi:[1,0,1]\nv_pk_fma_f32 v[46:47], v[46:47], s[70:71], s[70:71] op_sel:[0,0,1] op_sel_hi:[1,0,1]\nv_pk_fma_f32 v[48:49], v[48:49], s[70:71], s[70:71] op_sel:[0,0,1] op_sel_hi:[1,0,1]\nv_pk_fma_f32 v[50:51], v[50:51], s[70:71], s[70:71] op_sel:[0,0,1] op_sel_hi:[1,0,1]\nv_pk_fma_f32 v[52:53], v[52:53], s[70:71], s[70:71] op_sel:[0,0,1] op_sel_hi:[1,0,1]\nv_pk_fma_f32 v[54:55], v[54:55], s[70:71], s[70:71] op_sel:[0,0,1] op_sel_hi:[1,0,1]\nv_pk_fma_f32 v[56:57], v[56:57], s[70:71], s[70:71] op_sel:[0,0,1] op_sel_hi:[1,0,1]\nv_pk_fma_f32 v[58:59], v[58:59], s[70:71], s[70:71] op_sel:[0,0,1] op_sel_hi:[1,0,1]\nv_pk_fma_f32 v[60:61], v[60:61], s[70:71], s[70:71] op_sel:[0,0,1] op_sel_hi:[1,0,1]\nv_pk_fma_f32 v[62:63], v[62:63], s[70:71], s[70:71] op_sel:[0,0,1] op_sel_hi:[1,0,1]\n"
"v_mfma_f32_16x16x32_bf16 v[76:79], a[216:219], a[176:179], v[76:79]\n"
"v_cvt_pk_bf16_f32 v161, v162, v163\n"
"v_cvt_pk_bf16_f32 v162, v164, v165\n"
"v_mfma_f32_16x16x32_bf16 v[76:79], a[220:223], a[180:183], v[76:79]\n"
"s_nop 0\n"
"v_permlane16_swap_b32 v160, v162\n"
"v_mfma_f32_16x16x32_bf16 v[88:91], a[240:243], a[176:179], v[88:91]\n"
"v_cvt_pk_bf16_f32 v163, v166, v167\n"
"v_accvgpr_write_b32 a120, v160\n"
"v_mfma_f32_16x16x32_bf16 v[88:91], a[244:247], a[180:183], v[88:91]\n"
"v_permlane16_swap_b32 v161, v163\n"
"v_mfma_f32_16x16x32_bf16 v[92:95], a[248:251], a[176:179], v[92:95]\n"
"v_accvgpr_write_b32 a121, v161\n"
"v_accvgpr_write_b32 a122, v162\n"
"v_mfma_f32_16x16x32_bf16 v[92:95], a[252:255], a[180:183], v[92:95]\n"
"v_accvgpr_write_b32 a123, v163\n"
"v_cvt_pk_bf16_f32 v168, v168, v169\n"
"v_mfma_f32_16x16x32_bf16 v[80:83], a[224:227], a[176:179], v[80:83]\n"
"v_cvt_pk_bf16_f32 v169, v170, v171\n"
"v_cvt_pk_bf16_f32 v170, v172, v173\n"
"v_mfma_f32_16x16x32_bf16 v[80:83], a[228:231], a[180:183], v[80:83]\n"
"s_nop 0\n"
"v_permlane16_swap_b32 v168, v170\n"
"v_mfma_f32_16x16x32_bf16 v[84:87], a[232:235], a[176:179], v[84:87]\n"
"v_cvt_pk_bf16_f32 v171, v174, v175\n"
"v_accvgpr_write_b32 a124, v168\n"
"v_mfma_f32_16x16x32_bf16 v[84:87], a[236:239], a[180:183], v[84:87]\n"
"s_waitcnt lgkmcnt(0)\n"
"v_permlane16_swap_b32 v169, v171\n"
"v_mfma_f32_16x16x32_bf16 v[180:183], v[208:211], a[0:3], 0\n"
"v_accvgpr_write_b32 a125, v169\n"
"v_accvgpr_write_b32 a126, v170\n"
"v_mfma_f32_16x16x32_bf16 v[180:183], v[212:215], a[4:7], v[180:183]\n"
"v_accvgpr_write_b32 a127, v171\n"
"v_accvgpr_write_b32 a128, v176\n"
"v_mfma_f32_16x16x32_bf16 v[180:183], v[216:219], a[8:11], v[180:183]\n"
"v_accvgpr_write_b32 a129, v177\n"
"v_accvgpr_write_b32 a130, v178\n"
"v_mfma_f32_16x16x32_bf16 v[180:183], v[220:223], a[12:15], v[180:183]\n"
"v_accvgpr_write_b32 a131, v179\n"
"v_accvgpr_write_b32 a104, v128\n"
"v_mfma_f32_16x16x32_bf16 v[184:187], v[208:211], a[16:19], 0\n"
"v_accvgpr_write_b32 a105, v129\n"
"v_accvgpr_write_b32 a108, v136\n"
"v_mfma_f32_16x16x32_bf16 v[184:187], v[212:215], a[20:23], v[184:187]\n"
"v_pk_fma_f32 v[64:65], v[64:65], s[70:71], s[70:71] op_sel:[0,0,1] op_sel_hi:[1,0,1]\nv_pk_fma_f32 v[66:67], v[66:67], s[70:71], s[70:71] op_sel:[0,0,1] op_sel_hi:[1,0,1]\nv_pk_fma_f32 v[68:69], v[68:69], s[70:71], s[70:71] op_sel:[0,0,1] op_sel_hi:[1,0,1]\nv_pk_fma_f32 v[70:71], v[70:71], s[70:71], s[70:71] op_sel:[0,0,1] op_sel_hi:[1,0,1]\nv_pk_fma_f32 v[72:73], v[72:73], s[70:71], s[70:71] op_sel:[0,0,1] op_sel_hi:[1,0,1]\nv_pk_fma_f32 v[74:75], v[74:75], s[70:71], s[70:71] op_sel:[0,0,1] op_sel_hi:[1,0,1]\nv_pk_fma_f32 v[76:77], v[76:77], s[70:71], s[70:71] op_sel:[0,0,1] op_sel_hi:[1,0,1]\nv_pk_fma_f32 v[78:79], v[78:79], s[70:71], s[70:71] op_sel:[0,0,1] op_sel_hi:[1,0,1]\nv_pk_fma_f32 v[80:81], v[80:81], s[70:71], s[70:71] op_sel:[0,0,1] op_sel_hi:[1,0,1]\nv_pk_fma_f32 v[82:83], v[82:83], s[70:71], s[70:71] op_sel:[0,0,1] op_sel_hi:[1,0,1]\nv_pk_fma_f32 v[84:85], v[84:85], s[70:71], s[70:71] op_sel:[0,0,1] op_sel_hi:[1,0,1]\nv_pk_fma_f32 v[86:87], v[86:87], s[70:71], s[70:71] op_sel:[0,0,1] op_sel_hi:[1,0,1]\nv_pk_fma_f32 v[88:89], v[88:89], s[70:71], s[70:71] op_sel:[0,0,1] op_sel_hi:[1,0,1]\nv_pk_fma_f32 v[90:91], v[90:91], s[70:71], s[70:71] op_sel:[0,0,1] op_sel_hi:[1,0,1]\nv_pk_fma_f32 v[92:93], v[92:93], s[70:71], s[70:71] op_sel:[0,0,1] op_sel_hi:[1,0,1]\nv_pk_fma_f32 v[94:95], v[94:95], s[70:71], s[70:71] op_sel:[0,0,1] op_sel_hi:[1,0,1]\n"
"v_mfma_f32_16x16x32_bf16 v[184:187], v[216:219], a[24:27], v[184:187]\n"
"v_accvgpr_write_b32 a109, v137\n"
"v_accvgpr_write_b32 a112, v144\n"
"v_mfma_f32_16x16x32_bf16 v[184:187], v[220:223], a[28:31], v[184:187]\n"
"v_accvgpr_write_b32 a113, v145\n"
"v_accvgpr_write_b32 a106, v130\n"
"v_mfma_f32_16x16x32_bf16 v[188:191], v[208:211], a[32:35], 0\n"
"v_accvgpr_write_b32 a107, v131\n"
"v_accvgpr_write_b32 a110, v138\n"
"v_mfma_f32_16x16x32_bf16 v[188:191], v[212:215], a[36:39], v[188:191]\n"
"v_sub_f32 v180, s74, v180\n"
"v_sub_f32 v181, s74, v181\n"
"v_mfma_f32_16x16x32_bf16 v[188:191], v[216:219], a[40:43], v[188:191]\n"
"v_sub_f32 v182, s74, v182\n"
"v_sub_f32 v183, s74, v183\n"
"v_mfma_f32_16x16x32_bf16 v[188:191], v[220:223], a[44:47], v[188:191]\n"
"v_accvgpr_write_b32 a111, v139\n"
"v_cvt_pk_bf16_f32 v96, v96, v97\n"
"v_mfma_f32_16x16x32_bf16 v[192:195], v[208:211], a[192:195], 0\n"
"ds_read_b128 v[208:211], v228 offset:0\n"
"v_cvt_pk_bf16_f32 v97, v98, v99\n"
"v_mfma_f32_16x16x32_bf16 v[192:195], v[212:215], a[196:199], v[192:195]\n"
"ds_read_b128 v[212:215], v229 offset:0\n"
"v_sub_f32 v184, s74, v184\n"
"v_mfma_f32_16x16x32_bf16 v[192:195], v[216:219], a[200:203], v[192:195]\n"
"ds_read_b128 v[216:219], v228 offset:1024\n"
"v_sub_f32 v185, s74, v185\n"
"v_mfma_f32_16x16x32_bf16 v[192:195], v[220:223], a[204:207], v[192:195]\n"
"s_waitcnt lgkmcnt(2)\n"
"ds_read_b128 v[220:223], v229 offset:1024\n"
"v_sub_f32 v186, s74, v186\n"
"v_mfma_f32_16x16x32_bf16 v[152:155], v[208:211], a[0:3], v[180:183]\n"
"s_waitcnt lgkmcnt(2)\n"
"v_sub_f32 v187, s74, v187\n"
"v_cvt_pk_bf16_f32 v98, v100, v101\n"
"v_mfma_f32_16x16x32_bf16 v[152:155], v[212:215], a[4:7], v[152:155]\n"
"s_waitcnt lgkmcnt(1)\n"
"v_sub_f32 v188, s74, v188\n"
"v_sub_f32 v189, s74, v189\n"
"v_mfma_f32_16x16x32_bf16 v[152:155], v[216:219], a[8:11], v[152:155]\n"
"s_waitcnt lgkmcnt(0)\n"
"v_sub_f32 v190, s74, v190\n"
"v_sub_f32 v191, s74, v191\n"
"v_mfma_f32_16x16x32_bf16 v[152:155], v[220:223], a[12:15], v[152:155]\n"
"v_permlane16_swap_b32 v96, v98\n"
"v_mfma_f32_16x16x32_bf16 v[156:159], v[216:219], a[24:27], v[184:187]\n"
"v_cvt_pk_bf16_f32 v99, v102, v103\n"
"v_cvt_pk_bf16_f32 v104, v104, v105\n"
"v_mfma_f32_16x16x32_bf16 v[156:159], v[220:223], a[28:31], v[156:159]\n"
"v_sub_f32 v192, s74, v192\n"
"v_sub_f32 v193, s74, v193\n"
"v_mfma_f32_16x16x32_bf16 v[156:159], v[208:211], a[16:19], v[156:159]\n"
"v_sub_f32 v194, s74, v194\n"
"v_sub_f32 v195, s74, v195\n"
"v_mfma_f32_16x16x32_bf16 v[156:159], v[212:215], a[20:23], v[156:159]\n"
"v_permlane16_swap_b32 v97, v99\n"
"v_mfma_f32_16x16x32_bf16 v[160:163], v[208:211], a[32:35], v[188:191]\n"
"ds_write_b128 v243, v[96:99] offset:12288\n"
"v_mfma_f32_16x16x32_bf16 v[160:163], v[212:215], a[36:39], v[160:163]\n"
"v_exp_f32 v152, v152\n"
"v_mfma_f32_16x16x32_bf16 v[160:163], v[216:219], a[40:43], v[160:163]\n"
"v_exp_f32 v153, v153\n"
"v_mfma_f32_16x16x32_bf16 v[160:163], v[220:223], a[44:47], v[160:163]\n"
"v_exp_f32 v154, v154\n"
"v_mfma_f32_16x16x32_bf16 v[164:167], v[216:219], a[200:203], v[192:195]\n"
"ds_read_b128 v[216:219], v228 offset:5120\n"
"v_cvt_pk_bf16_f32 v128, v152, v153\n"
"v_mfma_f32_16x16x32_bf16 v[164:167], v[220:223], a[204:207], v[164:167]\n"
"ds_read_b128 v[220:223], v229 offset:5120\n"
"v_cvt_pk_bf16_f32 v105, v106, v107\n"
"v_mfma_f32_16x16x32_bf16 v[164:167], v[208:211], a[192:195], v[164:167]\n"
"ds_read_b128 v[208:211], v228 offset:4096\n"
"v_cvt_pk_bf16_f32 v106, v108, v109\n"
"v_mfma_f32_16x16x32_bf16 v[164:167], v[212:215], a[196:199], v[164:167]\n"
"s_waitcnt lgkmcnt(2)\n"
"ds_read_b128 v[212:215], v229 offset:4096\n"
"v_cvt_pk_bf16_f32 v107, v110, v111\n"
"v_mfma_f32_16x16x32_bf16 v[168:171], v[216:219], a[8:11], v[180:183]\n"
"s_waitcnt lgkmcnt(2)\n"
"v_exp_f32 v155, v155\n"
"v_mfma_f32_16x16x32_bf16 v[168:171], v[220:223], a[12:15], v[168:171]\n"
"s_waitcnt lgkmcnt(1)\n"
"v_exp_f32 v156, v156\n"
"v_mfma_f32_16x16x32_bf16 v[168:171], v[208:211], a[0:3], v[168:171]\n"
"s_waitcnt lgkmcnt(0)\n"
"v_exp_f32 v157, v157\n"
"v_mfma_f32_16x16x32_bf16 v[168:171], v[212:215], a[4:7], v[168:171]\n"
"v_exp_f32 v158, v158\n"
"v_mfma_f32_16x16x32_bf16 v[172:175], v[208:211], a[16:19], v[184:187]\n"
"v_exp_f32 v159, v159\n"
"v_mfma_f32_16x16x32_bf16 v[172:175], v[212:215], a[20:23], v[172:175]\n"
"v_exp_f32 v160, v160\n"
"v_mfma_f32_16x16x32_bf16 v[172:175], v[216:219], a[24:27], v[172:175]\n"
"v_exp_f32 v161, v161\n"
"v_mfma_f32_16x16x32_bf16 v[172:175], v[220:223], a[28:31], v[172:175]\n"
"v_exp_f32 v162, v162\n"
"v_mfma_f32_16x16x32_bf16 v[176:179], v[216:219], a[40:43], v[188:191]\n"
"v_exp_f32 v163, v163\n"
"v_mfma_f32_16x16x32_bf16 v[176:179], v[220:223], a[44:47], v[176:179]\n"
"v_cvt_pk_bf16_f32 v129, v154, v155\n"
"v_cvt_pk_bf16_f32 v136, v156, v157\n"
"v_mfma_f32_16x16x32_bf16 v[176:179], v[208:211], a[32:35], v[176:179]\n"
"v_exp_f32 v164, v164\n"
"v_mfma_f32_16x16x32_bf16 v[176:179], v[212:215], a[36:39], v[176:179]\n"
"v_exp_f32 v165, v165\n"
"v_mfma_f32_16x16x32_bf16 v[152:155], v[208:211], a[192:195], v[192:195]\n"
"ds_read_b128 v[208:211], v230 offset:0\n"
"v_cvt_pk_bf16_f32 v137, v158, v159\n"
"v_mfma_f32_16x16x32_bf16 v[152:155], v[212:215], a[196:199], v[152:155]\n"
"ds_read_b128 v[212:215], v231 offset:0\n"
"v_cvt_pk_bf16_f32 v144, v160, v161\n"
"v_mfma_f32_16x16x32_bf16 v[152:155], v[216:219], a[200:203], v[152:155]\n"
"ds_read_b128 v[216:219], v230 offset:1024\n"
"v_cvt_pk_bf16_f32 v145, v162, v163\n"
"v_mfma_f32_16x16x32_bf16 v[152:155], v[220:223], a[204:207], v[152:155]\n"
"s_waitcnt lgkmcnt(2)\n"
"ds_read_b128 v[220:223], v231 offset:1024\n"
"v_cvt_pk_bf16_f32 v124, v164, v165\n"
"v_mfma_f32_16x16x32_bf16 v[156:159], v[208:211], a[0:3], v[180:183]\n"
"s_waitcnt lgkmcnt(2)\n"
"v_exp_f32 v166, v166\n"
"v_mfma_f32_16x16x32_bf16 v[156:159], v[212:215], a[4:7], v[156:159]\n"
"s_waitcnt lgkmcnt(1)\n"
"v_exp_f32 v167, v167\n"
"v_mfma_f32_16x16x32_bf16 v[156:159], v[216:219], a[8:11], v[156:159]\n"
"s_waitcnt lgkmcnt(0)\n"
"v_exp_f32 v168, v168\n"
"v_mfma_f32_16x16x32_bf16 v[156:159], v[220:223], a[12:15], v[156:159]\n"
"v_exp_f32 v169, v169\n"
"v_mfma_f32_16x16x32_bf16 v[160:163], v[216:219], a[24:27], v[184:187]\n"
"v_exp_f32 v170, v170\n"
"v_mfma_f32_16x16x32_bf16 v[160:163], v[220:223], a[28:31], v[160:163]\n"
"v_exp_f32 v171, v171\n"
"v_mfma_f32_16x16x32_bf16 v[160:163], v[208:211], a[16:19], v[160:163]\n"
"v_cvt_pk_bf16_f32 v125, v166, v167\n"
"v_cvt_pk_bf16_f32 v130, v168, v169\n"
"v_mfma_f32_16x16x32_bf16 v[160:163], v[212:215], a[20:23], v[160:163]\n"
"v_exp_f32 v172, v172\n"
"v_mfma_f32_16x16x32_bf16 v[164:167], v[208:211], a[32:35], v[188:191]\n"
"ds_read_b128 v[208:211], v230 offset:4096\n"
"v_cvt_pk_bf16_f32 v131, v170, v171\n"
"v_mfma_f32_16x16x32_bf16 v[164:167], v[212:215], a[36:39], v[164:167]\n"
"ds_read_b128 v[212:215], v231 offset:4096\n"
"v_accvgpr_write_b32 a114, v146\n"
"v_mfma_f32_16x16x32_bf16 v[164:167], v[216:219], a[40:43], v[164:167]\n"
"ds_read_b128 v[216:219], v230 offset:5120\n"
"v_accvgpr_write_b32 a115, v147\n"
"v_mfma_f32_16x16x32_bf16 v[164:167], v[220:223], a[44:47], v[164:167]\n"
"s_waitcnt lgkmcnt(2)\n"
"ds_read_b128 v[220:223], v231 offset:5120\n"
"v_cvt_pk_bf16_f32 v8, v8, v9\n"
"v_mfma_f32_16x16x32_bf16 v[168:171], v[208:211], a[0:3], v[180:183]\n"
"s_waitcnt lgkmcnt(2)\n"
"v_exp_f32 v173, v173\n"
"v_mfma_f32_16x16x32_bf16 v[168:171], v[212:215], a[4:7], v[168:171]\n"
"s_waitcnt lgkmcnt(1)\n"
"v_exp_f32 v174, v174\n"
"v_mfma_f32_16x16x32_bf16 v[168:171], v[216:219], a[8:11], v[168:171]\n"
"s_waitcnt lgkmcnt(0)\n"
"v_exp_f32 v175, v175\n"
"v_mfma_f32_16x16x32_bf16 v[168:171], v[220:223], a[12:15], v[168:171]\n"
"v_cvt_pk_bf16_f32 v138, v172, v173\n"
"v_cvt_pk_bf16_f32 v139, v174, v175\n"
"v_mfma_f32_16x16x32_bf16 v[172:175], v[216:219], a[24:27], v[184:187]\n"
"v_permlane16_swap_b32 v104, v106\n"
"v_mfma_f32_16x16x32_bf16 v[172:175], v[220:223], a[28:31], v[172:175]\n"
"v_permlane16_swap_b32 v105, v107\n"
"v_mfma_f32_16x16x32_bf16 v[172:175], v[208:211], a[16:19], v[172:175]\n"
"ds_write_b128 v243, v[104:107] offset:14336\n"
"v_exp_f32 v176, v176\n"
"v_exp_f32 v177, v177\n"
"v_exp_f32 v178, v178\n"
"v_exp_f32 v179, v179\n"
"v_mfma_f32_16x16x32_bf16 v[172:175], v[212:215], a[20:23], v[172:175]\n"
"s_cbranch_vccnz LPCE0_%=\nLPKE0_%=:\n"
"v_cvt_pk_bf16_f32 v146, v176, v177\n"
"v_cvt_pk_bf16_f32 v147, v178, v179\n"
"v_mfma_f32_16x16x32_bf16 v[176:179], v[208:211], a[32:35], v[188:191]\n"
"v_exp_f32 v152, v152\n"
"v_mfma_f32_16x16x32_bf16 v[176:179], v[212:215], a[36:39], v[176:179]\n"
"v_exp_f32 v153, v153\n"
"v_mfma_f32_16x16x32_bf16 v[176:179], v[216:219], a[40:43], v[176:179]\n"
"v_exp_f32 v154, v154\n"
"v_mfma_f32_16x16x32_bf16 v[176:179], v[220:223], a[44:47], v[176:179]\n"
"v_exp_f32 v155, v155\n"
"s_waitcnt vmcnt(8) lgkmcnt(0)\n"
"s_barrier\n"
"ds_read_b128 v[208:211], v228 offset:16384\n"
"ds_read_b128 v[212:215], v229 offset:16384\n"
"ds_read_b128 v[216:219], v228 offset:17408\n"
"ds_read_b128 v[220:223], v229 offset:17408\n"
"v_cvt_pk_bf16_f32 v126, v152, v153\n"
"v_cvt_pk_bf16_f32 v127, v154, v155\n"
"s_waitcnt lgkmcnt(3)\n"
"v_mfma_f32_16x16x32_bf16 v[152:155], v[208:211], a[0:3], v[180:183]\n"
"v_exp_f32 v156, v156\n"
"s_waitcnt lgkmcnt(2)\n"
"v_mfma_f32_16x16x32_bf16 v[152:155], v[212:215], a[4:7], v[152:155]\n"
"v_exp_f32 v157, v157\n"
"s_waitcnt lgkmcnt(1)\n"
"v_mfma_f32_16x16x32_bf16 v[152:155], v[216:219], a[8:11], v[152:155]\n"
"s_waitcnt lgkmcnt(0)\n"
"s_add_u32 m0, s44, 98304\n"
"v_exp_f32 v158, v158\n"
"v_exp_f32 v159, v159\n"
"v_mfma_f32_16x16x32_bf16 v[152:155], v[220:223], a[12:15], v[152:155]\n"
"buffer_load_dwordx4 v240, s[20:23], s56 offen nt lds\n"
"v_cvt_pk_bf16_f32 v132, v156, v157\n"
"v_cvt_pk_bf16_f32 v133, v158, v159\n"
"v_mfma_f32_16x16x32_bf16 v[156:159], v[216:219], a[24:27], v[184:187]\n"
"v_exp_f32 v160, v160\n"
"v_mfma_f32_16x16x32_bf16 v[156:159], v[220:223], a[28:31], v[156:159]\n"
"v_exp_f32 v161, v161\n"
"v_mfma_f32_16x16x32_bf16 v[156:159], v[208:211], a[16:19], v[156:159]\n"
"v_exp_f32 v162, v162\n"
"v_exp_f32 v163, v163\n"
"v_mfma_f32_16x16x32_bf16 v[156:159], v[212:215], a[20:23], v[156:159]\n"
"v_cvt_pk_bf16_f32 v140, v160, v161\n"
"v_cvt_pk_bf16_f32 v141, v162, v163\n"
"v_mfma_f32_16x16x32_bf16 v[160:163], v[208:211], a[32:35], v[188:191]\n"
"v_exp_f32 v164, v164\n"
"v_mfma_f32_16x16x32_bf16 v[160:163], v[212:215], a[36:39], v[160:163]\n"
"v_exp_f32 v165, v165\n"
"v_mfma_f32_16x16x32_bf16 v[160:163], v[216:219], a[40:43], v[160:163]\n"
"ds_read_b64_tr_b16 a[244:245], v234 offset:0\n"
"ds_read_b64_tr_b16 a[246:247], v234 offset:4096\n"
"v_exp_f32 v166, v166\n"
"v_exp_f32 v167, v167\n"
"v_mfma_f32_16x16x32_bf16 v[160:163], v[220:223], a[44:47], v[160:163]\n"
"v_cvt_pk_bf16_f32 v148, v164, v165\n"
"v_cvt_pk_bf16_f32 v149, v166, v167\n"
"v_mfma_f32_16x16x32_bf16 v[164:167], v[216:219], a[200:203], v[192:195]\n"
"ds_read_b64_tr_b16 a[248:249], v238 offset:0\n"
"ds_read_b64_tr_b16 a[250:251], v238 offset:4096\n"
"v_mfma_f32_16x16x32_bf16 v[164:167], v[220:223], a[204:207], v[164:167]\n"
"v_exp_f32 v168, v168\n"
"v_mfma_f32_16x16x32_bf16 v[164:167], v[208:211], a[192:195], v[164:167]\n"
"s_add_u32 s40, s56, s52\n"
"v_exp_f32 v169, v169\n"
"v_mfma_f32_16x16x32_bf16 v[164:167], v[212:215], a[196:199], v[164:167]\n"
"s_waitcnt lgkmcnt(2)\n"
"buffer_load_dwordx4 v240, s[20:23], s40 offen offset:1024 nt lds\n"
"v_cvt_pk_bf16_f32 v9, v10, v11\n"
"v_cvt_pk_bf16_f32 v10, v12, v13\n"
"v_cvt_pk_bf16_f32 v11, v14, v15\n"
"v_exp_f32 v170, v170\n"
"v_exp_f32 v171, v171\n"
"v_mfma_f32_16x16x32_bf16 a[216:219], v[124:127], a[244:247], 0\n"
"v_permlane16_swap_b32 v8, v10\n"
"v_permlane16_swap_b32 v9, v11\n"
"v_accvgpr_write_b32 a148, v8\n"
"v_accvgpr_write_b32 a149, v9\n"
"v_accvgpr_write_b32 a150, v10\n"
"v_accvgpr_write_b32 a151, v11\n"
"v_cvt_pk_bf16_f32 v134, v168, v169\n"
"v_cvt_pk_bf16_f32 v135, v170, v171\n"
"v_mfma_f32_16x16x32_bf16 v[8:11], v[128:131], a[244:247], 0\n"
"s_waitcnt lgkmcnt(0)\n"
"ds_read_b64_tr_b16 v[224:225], v239 offset:0\n"
"ds_read_b64_tr_b16 v[226:227], v239 offset:4096\n"
"v_cvt_pk_bf16_f32 v40, v40, v41\n"
"v_cvt_pk_bf16_f32 v41, v42, v43\n"
"v_cvt_pk_bf16_f32 v42, v44, v45\n"
"v_cvt_pk_bf16_f32 v43, v46, v47\n"
"v_exp_f32 v172, v172\n"
"v_exp_f32 v173, v173\n"
"v_exp_f32 v174, v174\n"
"v_exp_f32 v175, v175\n"
"v_mfma_f32_16x16x32_bf16 v[8:11], v[132:135], a[248:251], v[8:11]\n"
"ds_read_b64_tr_b16 a[252:253], v235 offset:0\n"
"ds_read_b64_tr_b16 a[254:255], v235 offset:4096\n"
"v_permlane16_swap_b32 v40, v42\n"
"v_permlane16_swap_b32 v41, v43\n"
"v_accvgpr_write_b32 a164, v40\n"
"v_accvgpr_write_b32 a165, v41\n"
"v_accvgpr_write_b32 a166, v42\n"
"v_accvgpr_write_b32 a167, v43\n"
"v_cvt_pk_bf16_f32 v142, v172, v173\n"
"v_cvt_pk_bf16_f32 v143, v174, v175\n"
"v_mfma_f32_16x16x32_bf16 v[40:43], v[136:139], a[244:247], 0\n"
"v_cvt_pk_bf16_f32 v72, v72, v73\n"
"v_cvt_pk_bf16_f32 v73, v74, v75\n"
"v_cvt_pk_bf16_f32 v74, v76, v77\n"
"v_cvt_pk_bf16_f32 v75, v78, v79\n"
"v_exp_f32 v176, v176\n"
"v_exp_f32 v177, v177\n"
"v_exp_f32 v178, v178\n"
"v_exp_f32 v179, v179\n"
"v_mfma_f32_16x16x32_bf16 v[40:43], v[140:143], a[248:251], v[40:43]\n"
"v_permlane16_swap_b32 v72, v74\n"
"v_permlane16_swap_b32 v73, v75\n"
"v_accvgpr_write_b32 a180, v72\n"
"v_accvgpr_write_b32 a181, v73\n"
"v_accvgpr_write_b32 a182, v74\n"
"v_accvgpr_write_b32 a183, v75\n"
"v_cvt_pk_bf16_f32 v150, v176, v177\n"
"v_cvt_pk_bf16_f32 v151, v178, v179\n"
"v_mfma_f32_16x16x32_bf16 v[72:75], v[144:147], a[244:247], 0\n"
"ds_read_b128 v[216:219], v228 offset:21504\n"
"v_cvt_pk_bf16_f32 v200, v200, v201\n"
"v_mfma_f32_16x16x32_bf16 v[72:75], v[148:151], a[248:251], v[72:75]\n"
"s_waitcnt lgkmcnt(3)\n"
"ds_read_b128 v[220:223], v229 offset:21504\n"
"v_cvt_pk_bf16_f32 v201, v202, v203\n"
"v_mfma_f32_16x16x32_bf16 v[76:79], v[148:151], v[224:227], 0\n"
"s_waitcnt lgkmcnt(2)\n"
"ds_read_b128 v[208:211], v228 offset:20480\n"
"v_cvt_pk_bf16_f32 v202, v204, v205\n"
"v_mfma_f32_16x16x32_bf16 v[76:79], v[144:147], a[252:255], v[76:79]\n"
"ds_read_b128 v[212:215], v229 offset:20480\n"
"v_cvt_pk_bf16_f32 v203, v206, v207\n"
"v_mfma_f32_16x16x32_bf16 v[44:47], v[140:143], v[224:227], 0\n"
"s_add_u32 s40, s56, s53\n"
"ds_read_b64_tr_b16 a[244:245], v234 offset:1024\n"
"v_accvgpr_write_b32 a240, v254\n"
"v_mfma_f32_16x16x32_bf16 v[44:47], v[136:139], a[252:255], v[44:47]\n"
"buffer_load_dwordx4 v240, s[20:23], s40 offen offset:2048 nt lds\n"
"v_mfma_f32_16x16x32_bf16 v[12:15], v[132:135], v[224:227], 0\n"
"v_accvgpr_write_b32 a241, v254\n"
"v_accvgpr_write_b32 a242, v254\n"
"v_mfma_f32_16x16x32_bf16 v[12:15], v[128:131], a[252:255], v[12:15]\n"
"v_accvgpr_write_b32 a243, v254\n"
"v_mov_b32 v196, 0\n"
"v_mov_b32 v197, 0\n"
"v_mov_b32 v198, 0\n"
"v_mov_b32 v199, 0\n"
"v_mfma_f32_16x16x32_bf16 a[220:223], v[124:127], a[252:255], 0\n"
"v_permlane16_swap_b32 v200, v202\n"
"v_permlane16_swap_b32 v201, v203\n"
"v_accvgpr_write_b32 a142, v202\n"
"v_accvgpr_write_b32 a143, v203\n"
"v_mov_b32 v202, 0\n"
"v_mov_b32 v203, 0\n"
"v_mov_b32 v204, 0\n"
"v_mov_b32 v205, 0\n"
"v_smfmac_f32_16x16x64_bf16 v[196:199], a[240:243], v[128:135], v241\n"
"ds_read_b64_tr_b16 a[246:247], v234 offset:5120\n"
"v_cvt_pk_bf16_f32 v24, v24, v25\n"
"v_mfma_f32_16x16x32_bf16 v[202:205], a[240:243], v[124:127], v[202:205]\n"
"s_waitcnt lgkmcnt(3)\n"
"ds_read_b64_tr_b16 a[248:249], v238 offset:1024\n"
"v_cvt_pk_bf16_f32 v25, v26, v27\n"
"v_mfma_f32_16x16x32_bf16 v[168:171], v[216:219], a[8:11], v[180:183]\n"
"ds_read_b64_tr_b16 a[250:251], v238 offset:5120\n"
"v_cvt_pk_bf16_f32 v26, v28, v29\n"
"v_mfma_f32_16x16x32_bf16 v[168:171], v[220:223], a[12:15], v[168:171]\n"
"ds_read_b64_tr_b16 v[224:225], v239 offset:1024\n"
"v_cvt_pk_bf16_f32 v27, v30, v31\n"
"v_mfma_f32_16x16x32_bf16 v[168:171], v[208:211], a[0:3], v[168:171]\n"
"s_waitcnt lgkmcnt(4)\n"
"ds_read_b64_tr_b16 v[226:227], v239 offset:5120\n"
"v_cvt_pk_bf16_f32 v56, v56, v57\n"
"v_mfma_f32_16x16x32_bf16 v[168:171], v[212:215], a[4:7], v[168:171]\n"
"ds_read_b64_tr_b16 a[252:253], v235 offset:1024\n"
"v_cvt_pk_bf16_f32 v57, v58, v59\n"
"v_mfma_f32_16x16x32_bf16 v[172:175], v[208:211], a[16:19], v[184:187]\n"
"v_exp_f32 v152, v152\n"
"v_mfma_f32_16x16x32_bf16 v[172:175], v[212:215], a[20:23], v[172:175]\n"
"ds_read_b64_tr_b16 a[254:255], v235 offset:5120\n"
"v_cvt_pk_bf16_f32 v58, v60, v61\n"
"v_mfma_f32_16x16x32_bf16 v[172:175], v[216:219], a[24:27], v[172:175]\n"
"s_add_u32 s40, s56, s54\n"
"v_exp_f32 v153, v153\n"
"v_mfma_f32_16x16x32_bf16 v[172:175], v[220:223], a[28:31], v[172:175]\n"
"buffer_load_dwordx4 v240, s[20:23], s40 offen offset:3072 nt lds\n"
"v_mfma_f32_16x16x32_bf16 v[176:179], v[216:219], a[40:43], v[188:191]\n"
"s_add_u32 s55, s55, s59\n"
"v_exp_f32 v154, v154\n"
"v_mfma_f32_16x16x32_bf16 v[176:179], v[220:223], a[44:47], v[176:179]\n"
"s_add_u32 s41, s55, s57\n"
"v_exp_f32 v155, v155\n"
"v_mfma_f32_16x16x32_bf16 v[176:179], v[208:211], a[32:35], v[176:179]\n"
"s_and_b32 s41, s41, s58\n"
"v_cvt_pk_bf16_f32 v96, v152, v153\n"
"v_cvt_pk_bf16_f32 v97, v154, v155\n"
"v_mfma_f32_16x16x32_bf16 v[176:179], v[212:215], a[36:39], v[176:179]\n"
"s_andn2_b32 s56, s55, s58\n"
"v_permlane16_swap_b32 v24, v26\n"
"v_mfma_f32_16x16x32_bf16 v[152:155], v[208:211], a[192:195], v[192:195]\n"
"s_or_b32 s56, s56, s41\n"
"ds_read_b128 v[208:211], v230 offset:16384\n"
"v_accvgpr_write_b32 a156, v24\n"
"v_mfma_f32_16x16x32_bf16 v[152:155], v[212:215], a[196:199], v[152:155]\n"
"ds_read_b128 v[212:215], v231 offset:16384\n"
"v_accvgpr_write_b32 a158, v26\n"
"v_mfma_f32_16x16x32_bf16 v[152:155], v[216:219], a[200:203], v[152:155]\n"
"ds_read_b128 v[216:219], v230 offset:17408\n"
"v_cvt_pk_bf16_f32 v59, v62, v63\n"
"v_mfma_f32_16x16x32_bf16 v[152:155], v[220:223], a[204:207], v[152:155]\n"
"s_waitcnt lgkmcnt(3)\n"
"v_permlane16_swap_b32 v25, v27\n"
"v_mfma_f32_16x16x32_bf16 a[232:235], v[124:127], a[244:247], 0\n"
"v_accvgpr_write_b32 a157, v25\n"
"v_accvgpr_write_b32 a159, v27\n"
"v_mfma_f32_16x16x32_bf16 v[24:27], v[128:131], a[244:247], 0\n"
"s_add_u32 m0, s44, 49152\n"
"v_permlane16_swap_b32 v56, v58\n"
"v_mfma_f32_16x16x32_bf16 v[24:27], v[132:135], a[248:251], v[24:27]\n"
"buffer_load_dwordx4 v240, s[16:19], s56 offen nt lds\n"
"v_permlane16_swap_b32 v57, v59\n"
"v_accvgpr_write_b32 a172, v56\n"
"v_accvgpr_write_b32 a173, v57\n"
"v_accvgpr_write_b32 a174, v58\n"
"v_accvgpr_write_b32 a175, v59\n"
"v_mfma_f32_16x16x32_bf16 v[56:59], v[136:139], a[244:247], 0\n"
"v_cvt_pk_bf16_f32 v88, v88, v89\n"
"v_cvt_pk_bf16_f32 v89, v90, v91\n"
"v_cvt_pk_bf16_f32 v90, v92, v93\n"
"v_cvt_pk_bf16_f32 v91, v94, v95\n"
"v_mfma_f32_16x16x32_bf16 v[56:59], v[140:143], a[248:251], v[56:59]\n"
"v_permlane16_swap_b32 v88, v90\n"
"v_permlane16_swap_b32 v89, v91\n"
"v_accvgpr_write_b32 a188, v88\n"
"v_accvgpr_write_b32 a189, v89\n"
"v_accvgpr_write_b32 a190, v90\n"
"v_accvgpr_write_b32 a191, v91\n"
"v_mfma_f32_16x16x32_bf16 v[88:91], v[144:147], a[244:247], 0\n"
"v_exp_f32 v156, v156\n"
"v_mfma_f32_16x16x32_bf16 v[88:91], v[148:151], a[248:251], v[88:91]\n"
"v_accvgpr_write_b32 a140, v200\n"
"v_accvgpr_write_b32 a141, v201\n"
"v_mfma_f32_16x16x32_bf16 v[92:95], v[148:151], v[224:227], 0\n"
"v_mov_b32 v200, 0\n"
"v_mov_b32 v201, 0\n"
"v_mfma_f32_16x16x32_bf16 v[92:95], v[144:147], a[252:255], v[92:95]\n"
"v_exp_f32 v157, v157\n"
"v_mfma_f32_16x16x32_bf16 v[60:63], v[140:143], v[224:227], 0\n"
"v_exp_f32 v158, v158\n"
"v_mfma_f32_16x16x32_bf16 v[60:63], v[136:139], a[252:255], v[60:63]\n"
"v_exp_f32 v159, v159\n"
"v_mfma_f32_16x16x32_bf16 v[28:31], v[132:135], v[224:227], 0\n"
"v_cvt_pk_bf16_f32 v104, v156, v157\n"
"v_cvt_pk_bf16_f32 v105, v158, v159\n"
"v_mfma_f32_16x16x32_bf16 v[28:31], v[128:131], a[252:255], v[28:31]\n"
"ds_read_b128 v[220:223], v231 offset:17408\n"
"v_accvgpr_write_b32 a96, v112\n"
"v_mfma_f32_16x16x32_bf16 a[236:239], v[124:127], a[252:255], 0\n"
"s_add_u32 s40, s56, s52\n"
"v_exp_f32 v160, v160\n"
"v_smfmac_f32_16x16x64_bf16 v[198:201], a[240:243], v[136:143], v241\n"
"s_waitcnt lgkmcnt(1)\n"
"buffer_load_dwordx4 v240, s[16:19], s40 offen offset:1024 nt lds\n"
"v_mfma_f32_16x16x32_bf16 v[156:159], v[208:211], a[0:3], v[180:183]\n"
"v_exp_f32 v161, v161\n"
"v_mfma_f32_16x16x32_bf16 v[156:159], v[212:215], a[4:7], v[156:159]\n"
"v_exp_f32 v162, v162\n"
"v_mfma_f32_16x16x32_bf16 v[156:159], v[216:219], a[8:11], v[156:159]\n"
"s_waitcnt lgkmcnt(0)\n"
"v_exp_f32 v163, v163\n"
"v_mfma_f32_16x16x32_bf16 v[156:159], v[220:223], a[12:15], v[156:159]\n"
"v_accvgpr_write_b32 a97, v113\n"
"v_cvt_pk_bf16_f32 v112, v160, v161\n"
"v_cvt_pk_bf16_f32 v113, v162, v163\n"
"v_mfma_f32_16x16x32_bf16 v[160:163], v[216:219], a[24:27], v[184:187]\n"
"v_exp_f32 v164, v164\n"
"v_mfma_f32_16x16x32_bf16 v[160:163], v[220:223], a[28:31], v[160:163]\n"
"v_exp_f32 v165, v165\n"
"v_mfma_f32_16x16x32_bf16 v[160:163], v[208:211], a[16:19], v[160:163]\n"
"ds_read_b64_tr_b16 a[244:245], v232 offset:0\n"
"ds_read_b64_tr_b16 a[246:247], v232 offset:4096\n"
"v_exp_f32 v166, v166\n"
"v_exp_f32 v167, v167\n"
"v_mfma_f32_16x16x32_bf16 v[160:163], v[212:215], a[20:23], v[160:163]\n"
"ds_read_b64_tr_b16 a[248:249], v236 offset:0\n"
"v_accvgpr_write_b32 a100, v120\n"
"v_accvgpr_write_b32 a101, v121\n"
"v_cvt_pk_bf16_f32 v120, v164, v165\n"
"v_cvt_pk_bf16_f32 v121, v166, v167\n"
"v_mfma_f32_16x16x32_bf16 v[164:167], v[208:211], a[32:35], v[188:191]\n"
"ds_read_b64_tr_b16 a[250:251], v236 offset:4096\n"
"v_cvt_pk_bf16_f32 v0, v0, v1\n"
"v_mfma_f32_16x16x32_bf16 v[164:167], v[212:215], a[36:39], v[164:167]\n"
"ds_read_b64_tr_b16 v[224:225], v237 offset:0\n"
"v_cvt_pk_bf16_f32 v1, v2, v3\n"
"v_mfma_f32_16x16x32_bf16 v[164:167], v[216:219], a[40:43], v[164:167]\n"
"ds_read_b64_tr_b16 v[226:227], v237 offset:4096\n"
"v_cvt_pk_bf16_f32 v2, v4, v5\n"
"v_mfma_f32_16x16x32_bf16 v[164:167], v[220:223], a[44:47], v[164:167]\n"
"s_waitcnt lgkmcnt(4)\n"
"v_cvt_pk_bf16_f32 v3, v6, v7\n"
"v_cvt_pk_bf16_f32 v32, v32, v33\n"
"v_mfma_f32_16x16x32_bf16 a[208:211], v[124:127], a[244:247], 0\n"
"v_permlane16_swap_b32 v0, v2\n"
"v_permlane16_swap_b32 v1, v3\n"
"v_accvgpr_write_b32 a144, v0\n"
"v_accvgpr_write_b32 a145, v1\n"
"v_accvgpr_write_b32 a146, v2\n"
"v_accvgpr_write_b32 a147, v3\n"
"v_mfma_f32_16x16x32_bf16 v[0:3], v[128:131], a[244:247], 0\n"
"s_waitcnt lgkmcnt(2)\n"
"s_add_u32 s40, s56, s53\n"
"ds_read_b64_tr_b16 a[252:253], v233 offset:0\n"
"ds_read_b64_tr_b16 a[254:255], v233 offset:4096\n"
"v_cvt_pk_bf16_f32 v33, v34, v35\n"
"v_cvt_pk_bf16_f32 v34, v36, v37\n"
"v_cvt_pk_bf16_f32 v35, v38, v39\n"
"v_mfma_f32_16x16x32_bf16 v[0:3], v[132:135], a[248:251], v[0:3]\n"
"buffer_load_dwordx4 v240, s[16:19], s40 offen offset:2048 nt lds\n"
"v_permlane16_swap_b32 v32, v34\n"
"v_permlane16_swap_b32 v33, v35\n"
"v_accvgpr_write_b32 a160, v32\n"
"v_accvgpr_write_b32 a161, v33\n"
"v_accvgpr_write_b32 a162, v34\n"
"v_accvgpr_write_b32 a163, v35\n"
"v_mfma_f32_16x16x32_bf16 v[32:35], v[136:139], a[244:247], 0\n"
"v_cvt_pk_bf16_f32 v64, v64, v65\n"
"v_cvt_pk_bf16_f32 v65, v66, v67\n"
"v_cvt_pk_bf16_f32 v66, v68, v69\n"
"v_cvt_pk_bf16_f32 v67, v70, v71\n"
"v_mfma_f32_16x16x32_bf16 v[32:35], v[140:143], a[248:251], v[32:35]\n"
"v_permlane16_swap_b32 v64, v66\n"
"v_permlane16_swap_b32 v65, v67\n"
"v_accvgpr_write_b32 a176, v64\n"
"v_accvgpr_write_b32 a177, v65\n"
"v_accvgpr_write_b32 a178, v66\n"
"v_accvgpr_write_b32 a179, v67\n"
"v_mfma_f32_16x16x32_bf16 v[64:67], v[144:147], a[244:247], 0\n"
"v_exp_f32 v168, v168\n"
"v_mfma_f32_16x16x32_bf16 v[64:67], v[148:151], a[248:251], v[64:67]\n"
"s_waitcnt lgkmcnt(2)\n"
"v_exp_f32 v169, v169\n"
"v_mfma_f32_16x16x32_bf16 v[68:71], v[148:151], v[224:227], 0\n"
"s_waitcnt lgkmcnt(0)\n"
"v_exp_f32 v170, v170\n"
"v_mfma_f32_16x16x32_bf16 v[68:71], v[144:147], a[252:255], v[68:71]\n"
"ds_read_b128 v[208:211], v230 offset:20480\n"
"v_cvt_pk_bf16_f32 v98, v168, v169\n"
"v_mfma_f32_16x16x32_bf16 v[36:39], v[140:143], v[224:227], 0\n"
"ds_read_b128 v[212:215], v231 offset:20480\n"
"v_accvgpr_write_b32 a98, v114\n"
"v_mfma_f32_16x16x32_bf16 v[36:39], v[136:139], a[252:255], v[36:39]\n"
"ds_read_b128 v[216:219], v230 offset:21504\n"
"v_accvgpr_write_b32 a99, v115\n"
"v_mfma_f32_16x16x32_bf16 v[4:7], v[132:135], v[224:227], 0\n"
"s_add_u32 s40, s56, s54\n"
"ds_read_b128 v[220:223], v231 offset:21504\n"
"v_cvt_pk_bf16_f32 v16, v16, v17\n"
"v_mfma_f32_16x16x32_bf16 v[4:7], v[128:131], a[252:255], v[4:7]\n"
"buffer_load_dwordx4 v240, s[16:19], s40 offen offset:3072 nt lds\n"
"v_mfma_f32_16x16x32_bf16 a[212:215], v[124:127], a[252:255], 0\n"
"v_exp_f32 v171, v171\n"
"v_smfmac_f32_16x16x64_bf16 v[200:203], a[240:243], v[144:151], v241\n"
"s_waitcnt lgkmcnt(3)\n"
"v_cvt_pk_bf16_f32 v99, v170, v171\n"
"v_cvt_pk_bf16_f32 v17, v18, v19\n"
"v_mfma_f32_16x16x32_bf16 v[168:171], v[208:211], a[0:3], v[180:183]\n"
"s_waitcnt lgkmcnt(2)\n"
"v_exp_f32 v172, v172\n"
"v_mfma_f32_16x16x32_bf16 v[168:171], v[212:215], a[4:7], v[168:171]\n"
"s_waitcnt lgkmcnt(1)\n"
"v_exp_f32 v173, v173\n"
"v_mfma_f32_16x16x32_bf16 v[168:171], v[216:219], a[8:11], v[168:171]\n"
"s_waitcnt lgkmcnt(0)\n"
"v_exp_f32 v174, v174\n"
"v_exp_f32 v175, v175\n"
"v_mfma_f32_16x16x32_bf16 v[168:171], v[220:223], a[12:15], v[168:171]\n"
"v_cvt_pk_bf16_f32 v106, v172, v173\n"
"v_cvt_pk_bf16_f32 v107, v174, v175\n"
"v_mfma_f32_16x16x32_bf16 v[172:175], v[216:219], a[24:27], v[184:187]\n"
"v_exp_f32 v176, v176\n"
"v_mfma_f32_16x16x32_bf16 v[172:175], v[220:223], a[28:31], v[172:175]\n"
"v_exp_f32 v177, v177\n"
"v_mfma_f32_16x16x32_bf16 v[172:175], v[208:211], a[16:19], v[172:175]\n"
"ds_read_b64_tr_b16 a[244:245], v232 offset:1024\n"
"ds_read_b64_tr_b16 a[246:247], v232 offset:5120\n"
"v_exp_f32 v178, v178\n"
"v_exp_f32 v179, v179\n"
"v_mfma_f32_16x16x32_bf16 v[172:175], v[212:215], a[20:23], v[172:175]\n"
"s_cbranch_vccnz LPCE1_%=\nLPKE1_%=:\n"
"ds_read_b64_tr_b16 a[248:249], v236 offset:1024\n"
"v_cvt_pk_bf16_f32 v114, v176, v177\n"
"v_cvt_pk_bf16_f32 v115, v178, v179\n"
"v_mfma_f32_16x16x32_bf16 v[176:179], v[208:211], a[32:35], v[188:191]\n"
"ds_read_b64_tr_b16 a[250:251], v236 offset:5120\n"
"v_cvt_pk_bf16_f32 v18, v20, v21\n"
"v_mfma_f32_16x16x32_bf16 v[176:179], v[212:215], a[36:39], v[176:179]\n"
"s_nop 0\n"
"v_permlane16_swap_b32 v16, v18\n"
"v_mfma_f32_16x16x32_bf16 v[176:179], v[216:219], a[40:43], v[176:179]\n"
"v_cvt_pk_bf16_f32 v19, v22, v23\n"
"v_accvgpr_write_b32 a152, v16\n"
"v_mfma_f32_16x16x32_bf16 v[176:179], v[220:223], a[44:47], v[176:179]\n"
"s_waitcnt lgkmcnt(2)\n"
"v_permlane16_swap_b32 v17, v19\n"
"v_mfma_f32_16x16x32_bf16 a[224:227], v[124:127], a[244:247], 0\n"
"ds_read_b64_tr_b16 v[224:225], v237 offset:1024\n"
"ds_read_b64_tr_b16 v[226:227], v237 offset:5120\n"
"v_accvgpr_write_b32 a153, v17\n"
"v_accvgpr_write_b32 a154, v18\n"
"v_accvgpr_write_b32 a155, v19\n"
"v_mfma_f32_16x16x32_bf16 v[16:19], v[128:131], a[244:247], 0\n"
"s_waitcnt lgkmcnt(2)\n"
"s_waitcnt vmcnt(8)\n"
"ds_read_b64_tr_b16 a[252:253], v233 offset:1024\n"
"ds_read_b64_tr_b16 a[254:255], v233 offset:5120\n"
"v_cvt_pk_bf16_f32 v48, v48, v49\n"
"v_cvt_pk_bf16_f32 v49, v50, v51\n"
"v_cvt_pk_bf16_f32 v50, v52, v53\n"
"v_cvt_pk_bf16_f32 v51, v54, v55\n"
"v_mfma_f32_16x16x32_bf16 v[16:19], v[132:135], a[248:251], v[16:19]\n"
"s_barrier\n"
"v_permlane16_swap_b32 v48, v50\n"
"v_permlane16_swap_b32 v49, v51\n"
"v_accvgpr_write_b32 a168, v48\n"
"v_accvgpr_write_b32 a169, v49\n"
"v_accvgpr_write_b32 a170, v50\n"
"v_accvgpr_write_b32 a171, v51\n"
"v_mfma_f32_16x16x32_bf16 v[48:51], v[136:139], a[244:247], 0\n"
"v_cvt_pk_bf16_f32 v80, v80, v81\n"
"v_cvt_pk_bf16_f32 v81, v82, v83\n"
"v_cvt_pk_bf16_f32 v82, v84, v85\n"
"v_cvt_pk_bf16_f32 v83, v86, v87\n"
"v_mfma_f32_16x16x32_bf16 v[48:51], v[140:143], a[248:251], v[48:51]\n"
"v_permlane16_swap_b32 v80, v82\n"
"v_permlane16_swap_b32 v81, v83\n"
"v_accvgpr_write_b32 a184, v80\n"
"v_accvgpr_write_b32 a185, v81\n"
"v_accvgpr_write_b32 a186, v82\n"
"v_accvgpr_write_b32 a187, v83\n"
"v_mfma_f32_16x16x32_bf16 v[80:83], v[144:147], a[244:247], 0\n"
"v_exp_f32 v152, v152\n"
"v_mfma_f32_16x16x32_bf16 v[80:83], v[148:151], a[248:251], v[80:83]\n"
"s_waitcnt lgkmcnt(2)\n"
"v_exp_f32 v153, v153\n"
"v_mfma_f32_16x16x32_bf16 v[84:87], v[148:151], v[224:227], 0\n"
"s_waitcnt lgkmcnt(0)\n"
"ds_read_b128 v[208:211], v228 offset:32768\n"
"v_accvgpr_write_b32 a102, v122\n"
"v_mfma_f32_16x16x32_bf16 v[84:87], v[144:147], a[252:255], v[84:87]\n"
"ds_read_b128 v[212:215], v229 offset:32768\n"
"v_accvgpr_write_b32 a103, v123\n"
"v_mfma_f32_16x16x32_bf16 v[52:55], v[140:143], v[224:227], 0\n"
"ds_read_b128 v[216:219], v228 offset:33792\n"
"v_bfe_u32 v254, v242, 6, 4\n"
"v_mfma_f32_16x16x32_bf16 v[52:55], v[136:139], a[252:255], v[52:55]\n"
"ds_read_b128 v[220:223], v229 offset:33792\n"
"v_xor_b32 v249, v249, v254\n"
"v_mfma_f32_16x16x32_bf16 v[20:23], v[132:135], v[224:227], 0\n"
"ds_read_b64_tr_b16 a[244:245], v234 offset:16384\n"
"ds_read_b64_tr_b16 a[246:247], v234 offset:20480\n"
"v_mfma_f32_16x16x32_bf16 v[20:23], v[128:131], a[252:255], v[20:23]\n"
"ds_read_b64_tr_b16 a[248:249], v238 offset:16384\n"
"ds_read_b64_tr_b16 a[250:251], v238 offset:20480\n"
"v_exp_f32 v154, v154\n"
"v_exp_f32 v155, v155\n"
"v_exp_f32 v156, v156\n"
"v_exp_f32 v157, v157\n"
"v_exp_f32 v158, v158\n"
"v_exp_f32 v159, v159\n"
"v_exp_f32 v160, v160\n"
"v_exp_f32 v161, v161\n"
"v_exp_f32 v162, v162\n"
"v_exp_f32 v163, v163\n"
"v_exp_f32 v164, v164\n"
"v_exp_f32 v165, v165\n"
"v_exp_f32 v166, v166\n"
"v_exp_f32 v167, v167\n"
"v_mfma_f32_16x16x32_bf16 a[228:231], v[124:127], a[252:255], 0\n"
"s_waitcnt lgkmcnt(7)\n"
"ds_read_b64_tr_b16 a[254:255], v235 offset:20480\n"
"ds_read_b64_tr_b16 v[224:225], v239 offset:16384\n"
"v_lshl_add_u32 v249, v249, 4, s44\n"
"v_cvt_pk_bf16_f32 v122, v152, v153\n"
"v_cvt_pk_bf16_f32 v123, v154, v155\n"
"v_cvt_pk_bf16_f32 v100, v156, v157\n"
"v_cvt_pk_bf16_f32 v101, v158, v159\n"
"v_cvt_pk_bf16_f32 v108, v160, v161\n"
"v_cvt_pk_bf16_f32 v109, v162, v163\n"
"v_cvt_pk_bf16_f32 v116, v164, v165\n"
"v_cvt_pk_bf16_f32 v117, v166, v167\n"
"v_exp_f32 v169, v169\n"
"v_exp_f32 v172, v172\n"
"v_exp_f32 v173, v173\n"
"v_exp_f32 v175, v175\n"
"s_branch LAY2_%=\n"
"LAL_%=:\n"
"v_mfma_f32_16x16x32_bf16 a[224:227], v[124:127], a[244:247], a[224:227]\n"
"v_exp_f32 v169, v169\n"
"v_mfma_f32_16x16x32_bf16 v[16:19], v[128:131], a[244:247], v[16:19]\n"
"s_waitcnt vmcnt(8)\n"
"v_exp_f32 v172, v172\n"
"v_mfma_f32_16x16x32_bf16 v[16:19], v[132:135], a[248:251], v[16:19]\n"
"s_barrier\n"
"ds_read_b128 v[216:219], v228 offset:1024\n"
"v_mfma_f32_16x16x32_bf16 v[48:51], v[136:139], a[244:247], v[48:51]\n"
"ds_read_b128 v[212:215], v229 offset:0\n"
"v_cvt_pk_bf16_f32 v108, v160, v161\n"
"v_mfma_f32_16x16x32_bf16 v[48:51], v[140:143], a[248:251], v[48:51]\n"
"ds_read_b128 v[208:211], v228 offset:0\n"
"v_cvt_pk_bf16_f32 v100, v156, v157\n"
"v_mfma_f32_16x16x32_bf16 v[80:83], v[144:147], a[244:247], v[80:83]\n"
"ds_read_b128 v[220:223], v229 offset:1024\n"
"v_cvt_pk_bf16_f32 v117, v166, v167\n"
"v_mfma_f32_16x16x32_bf16 v[80:83], v[148:151], a[248:251], v[80:83]\n"
"ds_read_b64_tr_b16 a[244:245], v234 offset:49152\n"
"v_cvt_pk_bf16_f32 v109, v162, v163\n"
"v_mfma_f32_16x16x32_bf16 v[84:87], v[148:151], v[224:227], v[84:87]\n"
"ds_read_b64_tr_b16 a[248:249], v238 offset:49152\n"
"v_mfma_f32_16x16x32_bf16 v[84:87], v[144:147], a[252:255], v[84:87]\n"
"ds_read_b64_tr_b16 a[246:247], v234 offset:53248\n"
"v_mfma_f32_16x16x32_bf16 v[52:55], v[140:143], v[224:227], v[52:55]\n"
"ds_read_b64_tr_b16 a[250:251], v238 offset:53248\n"
"v_mfma_f32_16x16x32_bf16 v[52:55], v[136:139], a[252:255], v[52:55]\n"
"v_exp_f32 v175, v175\n"
"v_mfma_f32_16x16x32_bf16 v[20:23], v[132:135], v[224:227], v[20:23]\n"
"ds_read_b64_tr_b16 v[224:225], v239 offset:49152\n"
"v_mfma_f32_16x16x32_bf16 v[20:23], v[128:131], a[252:255], v[20:23]\n"
"v_exp_f32 v173, v173\n"
"v_mfma_f32_16x16x32_bf16 a[228:231], v[124:127], a[252:255], a[228:231]\n"
"s_waitcnt lgkmcnt(5)\n"
"ds_read_b64_tr_b16 a[254:255], v235 offset:53248\n"
"v_cvt_pk_bf16_f32 v116, v164, v165\n"
"v_mfma_f32_16x16x32_bf16 v[152:155], v[208:211], a[0:3], v[180:183]\n"
"v_exp_f32 v174, v174\n"
"v_mfma_f32_16x16x32_bf16 v[152:155], v[212:215], a[4:7], v[152:155]\n"
"ds_read_b64_tr_b16 a[252:253], v235 offset:49152\n"
"v_cvt_pk_bf16_f32 v111, v174, v175\n"
"v_mfma_f32_16x16x32_bf16 v[152:155], v[216:219], a[8:11], v[152:155]\n"
"s_add_u32 m0, s44, 81920\n"
"ds_read_b64_tr_b16 v[226:227], v239 offset:53248\n"
"v_mfma_f32_16x16x32_bf16 v[152:155], v[220:223], a[12:15], v[152:155]\n"
"buffer_load_dwordx4 v240, s[20:23], s56 offen nt lds\n"
"v_mfma_f32_16x16x32_bf16 v[156:159], v[216:219], a[24:27], v[184:187]\n"
"v_exp_f32 v171, v171\n"
"v_mfma_f32_16x16x32_bf16 v[156:159], v[220:223], a[28:31], v[156:159]\n"
"v_exp_f32 v168, v168\n"
"v_mfma_f32_16x16x32_bf16 v[156:159], v[208:211], a[16:19], v[156:159]\n"
"v_exp_f32 v176, v176\n"
"v_mfma_f32_16x16x32_bf16 v[156:159], v[212:215], a[20:23], v[156:159]\n"
"v_exp_f32 v177, v177\n"
"v_mfma_f32_16x16x32_bf16 v[160:163], v[208:211], a[32:35], v[188:191]\n"
"v_exp_f32 v179, v179\n"
"v_mfma_f32_16x16x32_bf16 v[160:163], v[212:215], a[36:39], v[160:163]\n"
"v_exp_f32 v170, v170\n"
"v_mfma_f32_16x16x32_bf16 v[160:163], v[216:219], a[40:43], v[160:163]\n"
"v_exp_f32 v153, v153\n"
"v_mfma_f32_16x16x32_bf16 v[160:163], v[220:223], a[44:47], v[160:163]\n"
"v_cvt_pk_bf16_f32 v102, v168, v169\n"
"v_mfma_f32_16x16x32_bf16 v[164:167], v[216:219], a[200:203], v[192:195]\n"
"ds_read_b128 v[216:219], v228 offset:5120\n"
"v_cvt_pk_bf16_f32 v103, v170, v171\n"
"v_mfma_f32_16x16x32_bf16 v[164:167], v[220:223], a[204:207], v[164:167]\n"
"v_cvt_pk_bf16_f32 v110, v172, v173\n"
"v_cvt_pk_bf16_f32 v118, v176, v177\n"
"v_mfma_f32_16x16x32_bf16 v[164:167], v[208:211], a[192:195], v[164:167]\n"
"s_add_u32 s40, s56, s52\n"
"ds_read_b128 v[208:211], v228 offset:4096\n"
"v_mfma_f32_16x16x32_bf16 v[164:167], v[212:215], a[196:199], v[164:167]\n"
"s_waitcnt lgkmcnt(2)\n"
"buffer_load_dwordx4 v240, s[20:23], s40 offen offset:1024 nt lds\n"
"v_mfma_f32_16x16x32_bf16 a[216:219], v[120:123], a[244:247], a[216:219]\n"
"v_exp_f32 v155, v155\n"
"v_mfma_f32_16x16x32_bf16 v[8:11], v[96:99], a[244:247], v[8:11]\n"
"v_exp_f32 v161, v161\n"
"v_mfma_f32_16x16x32_bf16 v[8:11], v[100:103], a[248:251], v[8:11]\n"
"v_exp_f32 v163, v163\n"
"v_mfma_f32_16x16x32_bf16 v[40:43], v[104:107], a[244:247], v[40:43]\n"
"v_exp_f32 v178, v178\n"
"v_mfma_f32_16x16x32_bf16 v[40:43], v[108:111], a[248:251], v[40:43]\n"
"ds_read_b128 v[220:223], v229 offset:5120\n"
"v_cvt_pk_bf16_f32 v119, v178, v179\n"
"v_mfma_f32_16x16x32_bf16 v[72:75], v[112:115], a[244:247], v[72:75]\n"
"ds_read_b64_tr_b16 a[244:245], v234 offset:50176\n"
"v_mfma_f32_16x16x32_bf16 v[72:75], v[116:119], a[248:251], v[72:75]\n"
"v_exp_f32 v158, v158\n"
"v_mfma_f32_16x16x32_bf16 v[76:79], v[116:119], v[224:227], v[76:79]\n"
"ds_read_b128 v[212:215], v229 offset:4096\n"
"v_mfma_f32_16x16x32_bf16 v[76:79], v[112:115], a[252:255], v[76:79]\n"
"v_exp_f32 v160, v160\n"
"v_mfma_f32_16x16x32_bf16 v[44:47], v[108:111], v[224:227], v[44:47]\n"
"s_add_u32 s40, s56, s53\n"
"ds_read_b64_tr_b16 a[248:249], v238 offset:50176\n"
"v_mfma_f32_16x16x32_bf16 v[44:47], v[104:107], a[252:255], v[44:47]\n"
"buffer_load_dwordx4 v240, s[20:23], s40 offen offset:2048 nt lds\n"
"v_mfma_f32_16x16x32_bf16 v[12:15], v[100:103], v[224:227], v[12:15]\n"
"v_exp_f32 v165, v165\n"
"v_mfma_f32_16x16x32_bf16 v[12:15], v[96:99], a[252:255], v[12:15]\n"
"v_exp_f32 v159, v159\n"
"v_mfma_f32_16x16x32_bf16 a[220:223], v[120:123], a[252:255], a[220:223]\n"
"v_exp_f32 v152, v152\n"
"v_smfmac_f32_16x16x64_bf16 v[196:199], a[240:243], v[96:103], v241\n"
"v_mfma_f32_16x16x32_bf16 v[202:205], a[240:243], v[120:123], v[202:205]\n"
"s_waitcnt lgkmcnt(1)\n"
"ds_read_b64_tr_b16 v[224:225], v239 offset:50176\n"
"v_cvt_pk_bf16_f32 v128, v152, v153\n"
"v_mfma_f32_16x16x32_bf16 v[168:171], v[216:219], a[8:11], v[180:183]\n"
"v_exp_f32 v162, v162\n"
"v_mfma_f32_16x16x32_bf16 v[168:171], v[220:223], a[12:15], v[168:171]\n"
"ds_read_b64_tr_b16 a[250:251], v238 offset:54272\n"
"v_mfma_f32_16x16x32_bf16 v[168:171], v[208:211], a[0:3], v[168:171]\n"
"ds_read_b64_tr_b16 a[252:253], v235 offset:50176\n"
"v_mfma_f32_16x16x32_bf16 v[168:171], v[212:215], a[4:7], v[168:171]\n"
"v_exp_f32 v154, v154\n"
"v_mfma_f32_16x16x32_bf16 v[172:175], v[208:211], a[16:19], v[184:187]\n"
"ds_read_b64_tr_b16 v[226:227], v239 offset:54272\n"
"v_mfma_f32_16x16x32_bf16 v[172:175], v[212:215], a[20:23], v[172:175]\n"
"ds_read_b64_tr_b16 a[246:247], v234 offset:54272\n"
"v_cvt_pk_bf16_f32 v129, v154, v155\n"
"v_mfma_f32_16x16x32_bf16 v[172:175], v[216:219], a[24:27], v[172:175]\n"
"s_add_u32 s40, s56, s54\n"
"ds_read_b64_tr_b16 a[254:255], v235 offset:54272\n"
"v_mfma_f32_16x16x32_bf16 v[172:175], v[220:223], a[28:31], v[172:175]\n"
"buffer_load_dwordx4 v240, s[20:23], s40 offen offset:3072 nt lds\n"
"v_mfma_f32_16x16x32_bf16 v[176:179], v[216:219], a[40:43], v[188:191]\n"
"s_add_u32 s55, s55, s59\n"
"v_exp_f32 v157, v157\n"
"v_mfma_f32_16x16x32_bf16 v[176:179], v[220:223], a[44:47], v[176:179]\n"
"s_add_u32 s41, s55, s57\n"
"v_exp_f32 v170, v170\n"
"v_mfma_f32_16x16x32_bf16 v[176:179], v[208:211], a[32:35], v[176:179]\n"
"s_and_b32 s41, s41, s58\n"
"v_exp_f32 v156, v156\n"
"v_mfma_f32_16x16x32_bf16 v[176:179], v[212:215], a[36:39], v[176:179]\n"
"s_andn2_b32 s56, s55, s58\n"
"v_exp_f32 v166, v166\n"
"v_mfma_f32_16x16x32_bf16 v[152:155], v[208:211], a[192:195], v[192:195]\n"
"s_or_b32 s56, s56, s41\n"
"ds_read_b128 v[208:211], v230 offset:0\n"
"v_mfma_f32_16x16x32_bf16 v[152:155], v[212:215], a[196:199], v[152:155]\n"
"v_exp_f32 v174, v174\n"
"v_mfma_f32_16x16x32_bf16 v[152:155], v[216:219], a[200:203], v[152:155]\n"
"ds_read_b128 v[212:215], v231 offset:0\n"
"v_mfma_f32_16x16x32_bf16 v[152:155], v[220:223], a[204:207], v[152:155]\n"
"s_waitcnt lgkmcnt(2)\n"
"ds_read_b128 v[216:219], v230 offset:1024\n"
"v_cvt_pk_bf16_f32 v137, v158, v159\n"
"v_mfma_f32_16x16x32_bf16 a[232:235], v[120:123], a[244:247], a[232:235]\n"
"v_exp_f32 v164, v164\n"
"v_mfma_f32_16x16x32_bf16 v[24:27], v[96:99], a[244:247], v[24:27]\n"
"s_add_u32 m0, s44, 32768\n"
"ds_read_b128 v[220:223], v231 offset:1024\n"
"v_mfma_f32_16x16x32_bf16 v[24:27], v[100:103], a[248:251], v[24:27]\n"
"buffer_load_dwordx4 v240, s[16:19], s56 offen nt lds\n"
"v_mfma_f32_16x16x32_bf16 v[56:59], v[104:107], a[244:247], v[56:59]\n"
"v_exp_f32 v167, v167\n"
"v_mfma_f32_16x16x32_bf16 v[56:59], v[108:111], a[248:251], v[56:59]\n"
"v_exp_f32 v176, v176\n"
"v_mfma_f32_16x16x32_bf16 v[88:91], v[112:115], a[244:247], v[88:91]\n"
"v_exp_f32 v153, v153\n"
"v_mfma_f32_16x16x32_bf16 v[88:91], v[116:119], a[248:251], v[88:91]\n"
"v_exp_f32 v173, v173\n"
"v_mfma_f32_16x16x32_bf16 v[92:95], v[116:119], v[224:227], v[92:95]\n"
"ds_read_b64_tr_b16 a[250:251], v236 offset:53248\n"
"v_cvt_pk_bf16_f32 v125, v166, v167\n"
"v_mfma_f32_16x16x32_bf16 v[92:95], v[112:115], a[252:255], v[92:95]\n"
"ds_read_b64_tr_b16 a[244:245], v232 offset:49152\n"
"v_cvt_pk_bf16_f32 v144, v160, v161\n"
"v_mfma_f32_16x16x32_bf16 v[60:63], v[108:111], v[224:227], v[60:63]\n"
"ds_read_b64_tr_b16 a[248:249], v236 offset:49152\n"
"v_cvt_pk_bf16_f32 v124, v164, v165\n"
"v_mfma_f32_16x16x32_bf16 v[60:63], v[104:107], a[252:255], v[60:63]\n"
"ds_read_b64_tr_b16 a[246:247], v232 offset:53248\n"
"v_cvt_pk_bf16_f32 v136, v156, v157\n"
"v_mfma_f32_16x16x32_bf16 v[28:31], v[100:103], v[224:227], v[28:31]\n"
"ds_read_b64_tr_b16 v[226:227], v237 offset:53248\n"
"v_cvt_pk_bf16_f32 v145, v162, v163\n"
"v_mfma_f32_16x16x32_bf16 v[28:31], v[96:99], a[252:255], v[28:31]\n"
"ds_read_b64_tr_b16 v[224:225], v237 offset:49152\n"
"v_mfma_f32_16x16x32_bf16 a[236:239], v[120:123], a[252:255], a[236:239]\n"
"s_add_u32 s40, s56, s52\n"
"ds_read_b64_tr_b16 a[252:253], v233 offset:49152\n"
"v_smfmac_f32_16x16x64_bf16 v[198:201], a[240:243], v[104:111], v241\n"
"s_waitcnt lgkmcnt(7)\n"
"buffer_load_dwordx4 v240, s[16:19], s40 offen offset:1024 nt lds\n"
"v_mfma_f32_16x16x32_bf16 v[156:159], v[208:211], a[0:3], v[180:183]\n"
"v_exp_f32 v169, v169\n"
"v_mfma_f32_16x16x32_bf16 v[156:159], v[212:215], a[4:7], v[156:159]\n"
"v_exp_f32 v168, v168\n"
"v_mfma_f32_16x16x32_bf16 v[156:159], v[216:219], a[8:11], v[156:159]\n"
"v_exp_f32 v175, v175\n"
"v_mfma_f32_16x16x32_bf16 v[156:159], v[220:223], a[12:15], v[156:159]\n"
"v_exp_f32 v172, v172\n"
"v_mfma_f32_16x16x32_bf16 v[160:163], v[216:219], a[24:27], v[184:187]\n"
"ds_read_b64_tr_b16 a[254:255], v233 offset:53248\n"
"v_mfma_f32_16x16x32_bf16 v[160:163], v[220:223], a[28:31], v[160:163]\n"
"v_exp_f32 v155, v155\n"
"v_mfma_f32_16x16x32_bf16 v[160:163], v[208:211], a[16:19], v[160:163]\n"
"v_exp_f32 v177, v177\n"
"v_mfma_f32_16x16x32_bf16 v[160:163], v[212:215], a[20:23], v[160:163]\n"
"v_exp_f32 v179, v179\n"
"v_mfma_f32_16x16x32_bf16 v[164:167], v[208:211], a[32:35], v[188:191]\n"
"ds_read_b128 v[208:211], v230 offset:4096\n"
"v_cvt_pk_bf16_f32 v139, v174, v175\n"
"v_mfma_f32_16x16x32_bf16 v[164:167], v[212:215], a[36:39], v[164:167]\n"
"v_exp_f32 v171, v171\n"
"v_mfma_f32_16x16x32_bf16 v[164:167], v[216:219], a[40:43], v[164:167]\n"
"ds_read_b128 v[212:215], v231 offset:4096\n"
"v_cvt_pk_bf16_f32 v138, v172, v173\n"
"v_mfma_f32_16x16x32_bf16 v[164:167], v[220:223], a[44:47], v[164:167]\n"
"s_waitcnt lgkmcnt(3)\n"
"ds_read_b128 v[220:223], v231 offset:5120\n"
"v_cvt_pk_bf16_f32 v130, v168, v169\n"
"v_mfma_f32_16x16x32_bf16 a[208:211], v[120:123], a[244:247], a[208:211]\n"
"v_exp_f32 v154, v154\n"
"v_mfma_f32_16x16x32_bf16 v[0:3], v[96:99], a[244:247], v[0:3]\n"
"s_add_u32 s40, s56, s53\n"
"ds_read_b128 v[216:219], v230 offset:5120\n"
"v_mfma_f32_16x16x32_bf16 v[0:3], v[100:103], a[248:251], v[0:3]\n"
"buffer_load_dwordx4 v240, s[16:19], s40 offen offset:2048 nt lds\n"
"v_mfma_f32_16x16x32_bf16 v[32:35], v[104:107], a[244:247], v[32:35]\n"
"v_exp_f32 v160, v160\n"
"v_mfma_f32_16x16x32_bf16 v[32:35], v[108:111], a[248:251], v[32:35]\n"
"v_exp_f32 v178, v178\n"
"v_mfma_f32_16x16x32_bf16 v[64:67], v[112:115], a[244:247], v[64:67]\n"
"v_exp_f32 v152, v152\n"
"v_mfma_f32_16x16x32_bf16 v[64:67], v[116:119], a[248:251], v[64:67]\n"
"v_exp_f32 v161, v161\n"
"v_mfma_f32_16x16x32_bf16 v[68:71], v[116:119], v[224:227], v[68:71]\n"
"s_waitcnt lgkmcnt(1)\n"
"ds_read_b64_tr_b16 a[244:245], v232 offset:50176\n"
"v_cvt_pk_bf16_f32 v147, v178, v179\n"
"v_mfma_f32_16x16x32_bf16 v[68:71], v[112:115], a[252:255], v[68:71]\n"
"ds_read_b64_tr_b16 a[248:249], v236 offset:50176\n"
"v_cvt_pk_bf16_f32 v146, v176, v177\n"
"v_mfma_f32_16x16x32_bf16 v[36:39], v[108:111], v[224:227], v[36:39]\n"
"ds_read_b64_tr_b16 a[250:251], v236 offset:54272\n"
"v_cvt_pk_bf16_f32 v131, v170, v171\n"
"v_mfma_f32_16x16x32_bf16 v[36:39], v[104:107], a[252:255], v[36:39]\n"
"v_exp_f32 v163, v163\n"
"v_mfma_f32_16x16x32_bf16 v[4:7], v[100:103], v[224:227], v[4:7]\n"
"s_add_u32 s40, s56, s54\n"
"ds_read_b64_tr_b16 a[246:247], v232 offset:54272\n"
"v_mfma_f32_16x16x32_bf16 v[4:7], v[96:99], a[252:255], v[4:7]\n"
"buffer_load_dwordx4 v240, s[16:19], s40 offen offset:3072 nt lds\n"
"v_mfma_f32_16x16x32_bf16 a[212:215], v[120:123], a[252:255], a[212:215]\n"
"v_exp_f32 v167, v167\n"
"v_smfmac_f32_16x16x64_bf16 v[200:203], a[240:243], v[112:119], v241\n"
"v_cvt_pk_bf16_f32 v126, v152, v153\n"
"v_mfma_f32_16x16x32_bf16 v[168:171], v[208:211], a[0:3], v[180:183]\n"
"v_exp_f32 v159, v159\n"
"v_mfma_f32_16x16x32_bf16 v[168:171], v[212:215], a[4:7], v[168:171]\n"
"s_waitcnt lgkmcnt(2)\n"
"v_exp_f32 v165, v165\n"
"v_mfma_f32_16x16x32_bf16 v[168:171], v[216:219], a[8:11], v[168:171]\n"
"ds_read_b64_tr_b16 a[252:253], v233 offset:50176\n"
"ds_read_b64_tr_b16 v[224:225], v237 offset:50176\n"
"v_mfma_f32_16x16x32_bf16 v[168:171], v[220:223], a[12:15], v[168:171]\n"
"ds_read_b64_tr_b16 a[254:255], v233 offset:54272\n"
"ds_read_b64_tr_b16 v[226:227], v237 offset:54272\n"
"v_mfma_f32_16x16x32_bf16 v[172:175], v[216:219], a[24:27], v[184:187]\n"
"v_exp_f32 v166, v166\n"
"v_mfma_f32_16x16x32_bf16 v[172:175], v[220:223], a[28:31], v[172:175]\n"
"v_exp_f32 v164, v164\n"
"v_mfma_f32_16x16x32_bf16 v[172:175], v[208:211], a[16:19], v[172:175]\n"
"v_exp_f32 v157, v157\n"
"v_mfma_f32_16x16x32_bf16 v[172:175], v[212:215], a[20:23], v[172:175]\n"
"s_cbranch_vccnz LPCL0_%=\nLPKL0_%=:\n"
"v_cvt_pk_bf16_f32 v127, v154, v155\n"
"v_mfma_f32_16x16x32_bf16 v[176:179], v[208:211], a[32:35], v[188:191]\n"
"v_exp_f32 v158, v158\n"
"v_mfma_f32_16x16x32_bf16 v[176:179], v[212:215], a[36:39], v[176:179]\n"
"v_exp_f32 v162, v162\n"
"v_mfma_f32_16x16x32_bf16 v[176:179], v[216:219], a[40:43], v[176:179]\n"
"v_exp_f32 v156, v156\n"
"v_mfma_f32_16x16x32_bf16 v[176:179], v[220:223], a[44:47], v[176:179]\n"
"s_waitcnt lgkmcnt(0)\n"
"v_cvt_pk_bf16_f32 v133, v158, v159\n"
"v_mfma_f32_16x16x32_bf16 a[224:227], v[120:123], a[244:247], a[224:227]\n"
"v_exp_f32 v169, v169\n"
"v_mfma_f32_16x16x32_bf16 v[16:19], v[96:99], a[244:247], v[16:19]\n"
"s_waitcnt vmcnt(8)\n"
"v_exp_f32 v172, v172\n"
"v_mfma_f32_16x16x32_bf16 v[16:19], v[100:103], a[248:251], v[16:19]\n"
"s_barrier\n"
"ds_read_b128 v[216:219], v228 offset:17408\n"
"v_mfma_f32_16x16x32_bf16 v[48:51], v[104:107], a[244:247], v[48:51]\n"
"ds_read_b128 v[212:215], v229 offset:16384\n"
"v_cvt_pk_bf16_f32 v140, v160, v161\n"
"v_mfma_f32_16x16x32_bf16 v[48:51], v[108:111], a[248:251], v[48:51]\n"
"ds_read_b128 v[208:211], v228 offset:16384\n"
"v_cvt_pk_bf16_f32 v132, v156, v157\n"
"v_mfma_f32_16x16x32_bf16 v[80:83], v[112:115], a[244:247], v[80:83]\n"
"ds_read_b128 v[220:223], v229 offset:17408\n"
"v_cvt_pk_bf16_f32 v149, v166, v167\n"
"v_mfma_f32_16x16x32_bf16 v[80:83], v[116:119], a[248:251], v[80:83]\n"
"ds_read_b64_tr_b16 a[244:245], v234 offset:0\n"
"v_cvt_pk_bf16_f32 v141, v162, v163\n"
"v_mfma_f32_16x16x32_bf16 v[84:87], v[116:119], v[224:227], v[84:87]\n"
"ds_read_b64_tr_b16 a[248:249], v238 offset:0\n"
"v_mfma_f32_16x16x32_bf16 v[84:87], v[112:115], a[252:255], v[84:87]\n"
"ds_read_b64_tr_b16 a[246:247], v234 offset:4096\n"
"v_mfma_f32_16x16x32_bf16 v[52:55], v[108:111], v[224:227], v[52:55]\n"
"ds_read_b64_tr_b16 a[250:251], v238 offset:4096\n"
"v_mfma_f32_16x16x32_bf16 v[52:55], v[104:107], a[252:255], v[52:55]\n"
"v_exp_f32 v175, v175\n"
"v_mfma_f32_16x16x32_bf16 v[20:23], v[100:103], v[224:227], v[20:23]\n"
"ds_read_b64_tr_b16 v[224:225], v239 offset:0\n"
"v_mfma_f32_16x16x32_bf16 v[20:23], v[96:99], a[252:255], v[20:23]\n"
"v_exp_f32 v173, v173\n"
"v_mfma_f32_16x16x32_bf16 a[228:231], v[120:123], a[252:255], a[228:231]\n"
"s_waitcnt lgkmcnt(5)\n"
"ds_read_b64_tr_b16 a[254:255], v235 offset:4096\n"
"v_cvt_pk_bf16_f32 v148, v164, v165\n"
"v_mfma_f32_16x16x32_bf16 v[152:155], v[208:211], a[0:3], v[180:183]\n"
"v_exp_f32 v174, v174\n"
"v_mfma_f32_16x16x32_bf16 v[152:155], v[212:215], a[4:7], v[152:155]\n"
"ds_read_b64_tr_b16 a[252:253], v235 offset:0\n"
"v_cvt_pk_bf16_f32 v143, v174, v175\n"
"v_mfma_f32_16x16x32_bf16 v[152:155], v[216:219], a[8:11], v[152:155]\n"
"s_add_u32 m0, s44, 98304\n"
"ds_read_b64_tr_b16 v[226:227], v239 offset:4096\n"
"v_mfma_f32_16x16x32_bf16 v[152:155], v[220:223], a[12:15], v[152:155]\n"
"buffer_load_dwordx4 v240, s[20:23], s56 offen nt lds\n"
"v_mfma_f32_16x16x32_bf16 v[156:159], v[216:219], a[24:27], v[184:187]\n"
"v_exp_f32 v171, v171\n"
"v_mfma_f32_16x16x32_bf16 v[156:159], v[220:223], a[28:31], v[156:159]\n"
"v_exp_f32 v168, v168\n"
"v_mfma_f32_16x16x32_bf16 v[156:159], v[208:211], a[16:19], v[156:159]\n"
"v_exp_f32 v176, v176\n"
"v_mfma_f32_16x16x32_bf16 v[156:159], v[212:215], a[20:23], v[156:159]\n"
"v_exp_f32 v177, v177\n"
"v_mfma_f32_16x16x32_bf16 v[160:163], v[208:211], a[32:35], v[188:191]\n"
"v_exp_f32 v179, v179\n"
"v_mfma_f32_16x16x32_bf16 v[160:163], v[212:215], a[36:39], v[160:163]\n"
"v_exp_f32 v170, v170\n"
"v_mfma_f32_16x16x32_bf16 v[160:163], v[216:219], a[40:43], v[160:163]\n"
"v_exp_f32 v153, v153\n"
"v_mfma_f32_16x16x32_bf16 v[160:163], v[220:223], a[44:47], v[160:163]\n"
"v_cvt_pk_bf16_f32 v134, v168, v169\n"
"v_mfma_f32_16x16x32_bf16 v[164:167], v[216:219], a[200:203], v[192:195]\n"
"ds_read_b128 v[216:219], v228 offset:21504\n"
"v_cvt_pk_bf16_f32 v135, v170, v171\n"
"v_mfma_f32_16x16x32_bf16 v[164:167], v[220:223], a[204:207], v[164:167]\n"
"v_cvt_pk_bf16_f32 v142, v172, v173\n"
"v_cvt_pk_bf16_f32 v150, v176, v177\n"
"v_mfma_f32_16x16x32_bf16 v[164:167], v[208:211], a[192:195], v[164:167]\n"
"s_add_u32 s40, s56, s52\n"
"ds_read_b128 v[208:211], v228 offset:20480\n"
"v_mfma_f32_16x16x32_bf16 v[164:167], v[212:215], a[196:199], v[164:167]\n"
"s_waitcnt lgkmcnt(2)\n"
"buffer_load_dwordx4 v240, s[20:23], s40 offen offset:1024 nt lds\n"
"v_mfma_f32_16x16x32_bf16 a[216:219], v[124:127], a[244:247], a[216:219]\n"
"v_exp_f32 v155, v155\n"
"v_mfma_f32_16x16x32_bf16 v[8:11], v[128:131], a[244:247], v[8:11]\n"
"v_exp_f32 v161, v161\n"
"v_mfma_f32_16x16x32_bf16 v[8:11], v[132:135], a[248:251], v[8:11]\n"
"v_exp_f32 v163, v163\n"
"v_mfma_f32_16x16x32_bf16 v[40:43], v[136:139], a[244:247], v[40:43]\n"
"v_exp_f32 v178, v178\n"
"v_mfma_f32_16x16x32_bf16 v[40:43], v[140:143], a[248:251], v[40:43]\n"
"ds_read_b128 v[220:223], v229 offset:21504\n"
"v_cvt_pk_bf16_f32 v151, v178, v179\n"
"v_mfma_f32_16x16x32_bf16 v[72:75], v[144:147], a[244:247], v[72:75]\n"
"ds_read_b64_tr_b16 a[244:245], v234 offset:1024\n"
"v_mfma_f32_16x16x32_bf16 v[72:75], v[148:151], a[248:251], v[72:75]\n"
"v_exp_f32 v158, v158\n"
"v_mfma_f32_16x16x32_bf16 v[76:79], v[148:151], v[224:227], v[76:79]\n"
"ds_read_b128 v[212:215], v229 offset:20480\n"
"v_mfma_f32_16x16x32_bf16 v[76:79], v[144:147], a[252:255], v[76:79]\n"
"v_exp_f32 v160, v160\n"
"v_mfma_f32_16x16x32_bf16 v[44:47], v[140:143], v[224:227], v[44:47]\n"
"s_add_u32 s40, s56, s53\n"
"ds_read_b64_tr_b16 a[248:249], v238 offset:1024\n"
"v_mfma_f32_16x16x32_bf16 v[44:47], v[136:139], a[252:255], v[44:47]\n"
"buffer_load_dwordx4 v240, s[20:23], s40 offen offset:2048 nt lds\n"
"v_mfma_f32_16x16x32_bf16 v[12:15], v[132:135], v[224:227], v[12:15]\n"
"v_exp_f32 v165, v165\n"
"v_mfma_f32_16x16x32_bf16 v[12:15], v[128:131], a[252:255], v[12:15]\n"
"v_exp_f32 v159, v159\n"
"v_mfma_f32_16x16x32_bf16 a[220:223], v[124:127], a[252:255], a[220:223]\n"
"v_exp_f32 v152, v152\n"
"v_smfmac_f32_16x16x64_bf16 v[196:199], a[240:243], v[128:135], v241\n"
"v_mfma_f32_16x16x32_bf16 v[202:205], a[240:243], v[124:127], v[202:205]\n"
"s_waitcnt lgkmcnt(1)\n"
"ds_read_b64_tr_b16 v[224:225], v239 offset:1024\n"
"v_cvt_pk_bf16_f32 v96, v152, v153\n"
"v_mfma_f32_16x16x32_bf16 v[168:171], v[216:219], a[8:11], v[180:183]\n"
"v_exp_f32 v162, v162\n"
"v_mfma_f32_16x16x32_bf16 v[168:171], v[220:223], a[12:15], v[168:171]\n"
"ds_read_b64_tr_b16 a[250:251], v238 offset:5120\n"
"v_mfma_f32_16x16x32_bf16 v[168:171], v[208:211], a[0:3], v[168:171]\n"
"ds_read_b64_tr_b16 a[252:253], v235 offset:1024\n"
"v_mfma_f32_16x16x32_bf16 v[168:171], v[212:215], a[4:7], v[168:171]\n"
"v_exp_f32 v154, v154\n"
"v_mfma_f32_16x16x32_bf16 v[172:175], v[208:211], a[16:19], v[184:187]\n"
"ds_read_b64_tr_b16 v[226:227], v239 offset:5120\n"
"v_mfma_f32_16x16x32_bf16 v[172:175], v[212:215], a[20:23], v[172:175]\n"
"ds_read_b64_tr_b16 a[246:247], v234 offset:5120\n"
"v_cvt_pk_bf16_f32 v97, v154, v155\n"
"v_mfma_f32_16x16x32_bf16 v[172:175], v[216:219], a[24:27], v[172:175]\n"
"s_add_u32 s40, s56, s54\n"
"ds_read_b64_tr_b16 a[254:255], v235 offset:5120\n"
"v_mfma_f32_16x16x32_bf16 v[172:175], v[220:223], a[28:31], v[172:175]\n"
"buffer_load_dwordx4 v240, s[20:23], s40 offen offset:3072 nt lds\n"
"v_mfma_f32_16x16x32_bf16 v[176:179], v[216:219], a[40:43], v[188:191]\n"
"s_add_u32 s55, s55, s59\n"
"v_exp_f32 v157, v157\n"
"v_mfma_f32_16x16x32_bf16 v[176:179], v[220:223], a[44:47], v[176:179]\n"
"s_add_u32 s41, s55, s57\n"
"v_exp_f32 v170, v170\n"
"v_mfma_f32_16x16x32_bf16 v[176:179], v[208:211], a[32:35], v[176:179]\n"
"s_and_b32 s41, s41, s58\n"
"v_exp_f32 v156, v156\n"
"v_mfma_f32_16x16x32_bf16 v[176:179], v[212:215], a[36:39], v[176:179]\n"
"s_andn2_b32 s56, s55, s58\n"
"v_exp_f32 v166, v166\n"
"v_mfma_f32_16x16x32_bf16 v[152:155], v[208:211], a[192:195], v[192:195]\n"
"s_or_b32 s56, s56, s41\n"
"ds_read_b128 v[208:211], v230 offset:16384\n"
"v_mfma_f32_16x16x32_bf16 v[152:155], v[212:215], a[196:199], v[152:155]\n"
"v_exp_f32 v174, v174\n"
"v_mfma_f32_16x16x32_bf16 v[152:155], v[216:219], a[200:203], v[152:155]\n"
"ds_read_b128 v[212:215], v231 offset:16384\n"
"v_mfma_f32_16x16x32_bf16 v[152:155], v[220:223], a[204:207], v[152:155]\n"
"s_waitcnt lgkmcnt(2)\n"
"ds_read_b128 v[216:219], v230 offset:17408\n"
"v_cvt_pk_bf16_f32 v105, v158, v159\n"
"v_mfma_f32_16x16x32_bf16 a[232:235], v[124:127], a[244:247], a[232:235]\n"
"v_exp_f32 v164, v164\n"
"v_mfma_f32_16x16x32_bf16 v[24:27], v[128:131], a[244:247], v[24:27]\n"
"s_add_u32 m0, s44, 49152\n"
"ds_read_b128 v[220:223], v231 offset:17408\n"
"v_mfma_f32_16x16x32_bf16 v[24:27], v[132:135], a[248:251], v[24:27]\n"
"buffer_load_dwordx4 v240, s[16:19], s56 offen nt lds\n"
"v_mfma_f32_16x16x32_bf16 v[56:59], v[136:139], a[244:247], v[56:59]\n"
"v_exp_f32 v167, v167\n"
"v_mfma_f32_16x16x32_bf16 v[56:59], v[140:143], a[248:251], v[56:59]\n"
"v_exp_f32 v176, v176\n"
"v_mfma_f32_16x16x32_bf16 v[88:91], v[144:147], a[244:247], v[88:91]\n"
"v_exp_f32 v153, v153\n"
"v_mfma_f32_16x16x32_bf16 v[88:91], v[148:151], a[248:251], v[88:91]\n"
"v_exp_f32 v173, v173\n"
"v_mfma_f32_16x16x32_bf16 v[92:95], v[148:151], v[224:227], v[92:95]\n"
"ds_read_b64_tr_b16 a[250:251], v236 offset:4096\n"
"v_cvt_pk_bf16_f32 v121, v166, v167\n"
"v_mfma_f32_16x16x32_bf16 v[92:95], v[144:147], a[252:255], v[92:95]\n"
"ds_read_b64_tr_b16 a[244:245], v232 offset:0\n"
"v_cvt_pk_bf16_f32 v112, v160, v161\n"
"v_mfma_f32_16x16x32_bf16 v[60:63], v[140:143], v[224:227], v[60:63]\n"
"ds_read_b64_tr_b16 a[248:249], v236 offset:0\n"
"v_cvt_pk_bf16_f32 v120, v164, v165\n"
"v_mfma_f32_16x16x32_bf16 v[60:63], v[136:139], a[252:255], v[60:63]\n"
"ds_read_b64_tr_b16 a[246:247], v232 offset:4096\n"
"v_cvt_pk_bf16_f32 v104, v156, v157\n"
"v_mfma_f32_16x16x32_bf16 v[28:31], v[132:135], v[224:227], v[28:31]\n"
"ds_read_b64_tr_b16 v[226:227], v237 offset:4096\n"
"v_cvt_pk_bf16_f32 v113, v162, v163\n"
"v_mfma_f32_16x16x32_bf16 v[28:31], v[128:131], a[252:255], v[28:31]\n"
"ds_read_b64_tr_b16 v[224:225], v237 offset:0\n"
"v_mfma_f32_16x16x32_bf16 a[236:239], v[124:127], a[252:255], a[236:239]\n"
"s_add_u32 s40, s56, s52\n"
"ds_read_b64_tr_b16 a[252:253], v233 offset:0\n"
"v_smfmac_f32_16x16x64_bf16 v[198:201], a[240:243], v[136:143], v241\n"
"s_waitcnt lgkmcnt(7)\n"
"buffer_load_dwordx4 v240, s[16:19], s40 offen offset:1024 nt lds\n"
"v_mfma_f32_16x16x32_bf16 v[156:159], v[208:211], a[0:3], v[180:183]\n"
"v_exp_f32 v169, v169\n"
"v_mfma_f32_16x16x32_bf16 v[156:159], v[212:215], a[4:7], v[156:159]\n"
"v_exp_f32 v168, v168\n"
"v_mfma_f32_16x16x32_bf16 v[156:159], v[216:219], a[8:11], v[156:159]\n"
"v_exp_f32 v175, v175\n"
"v_mfma_f32_16x16x32_bf16 v[156:159], v[220:223], a[12:15], v[156:159]\n"
"v_exp_f32 v172, v172\n"
"v_mfma_f32_16x16x32_bf16 v[160:163], v[216:219], a[24:27], v[184:187]\n"
"ds_read_b64_tr_b16 a[254:255], v233 offset:4096\n"
"v_mfma_f32_16x16x32_bf16 v[160:163], v[220:223], a[28:31], v[160:163]\n"
"v_exp_f32 v155, v155\n"
"v_mfma_f32_16x16x32_bf16 v[160:163], v[208:211], a[16:19], v[160:163]\n"
"v_exp_f32 v177, v177\n"
"v_mfma_f32_16x16x32_bf16 v[160:163], v[212:215], a[20:23], v[160:163]\n"
"v_exp_f32 v179, v179\n"
"v_mfma_f32_16x16x32_bf16 v[164:167], v[208:211], a[32:35], v[188:191]\n"
"ds_read_b128 v[208:211], v230 offset:20480\n"
"v_cvt_pk_bf16_f32 v107, v174, v175\n"
"v_mfma_f32_16x16x32_bf16 v[164:167], v[212:215], a[36:39], v[164:167]\n"
"v_exp_f32 v171, v171\n"
"v_mfma_f32_16x16x32_bf16 v[164:167], v[216:219], a[40:43], v[164:167]\n"
"ds_read_b128 v[212:215], v231 offset:20480\n"
"v_cvt_pk_bf16_f32 v106, v172, v173\n"
"v_mfma_f32_16x16x32_bf16 v[164:167], v[220:223], a[44:47], v[164:167]\n"
"s_waitcnt lgkmcnt(3)\n"
"ds_read_b128 v[220:223], v231 offset:21504\n"
"v_cvt_pk_bf16_f32 v98, v168, v169\n"
"v_mfma_f32_16x16x32_bf16 a[208:211], v[124:127], a[244:247], a[208:211]\n"
"v_exp_f32 v154, v154\n"
"v_mfma_f32_16x16x32_bf16 v[0:3], v[128:131], a[244:247], v[0:3]\n"
"s_add_u32 s40, s56, s53\n"
"ds_read_b128 v[216:219], v230 offset:21504\n"
"v_mfma_f32_16x16x32_bf16 v[0:3], v[132:135], a[248:251], v[0:3]\n"
"buffer_load_dwordx4 v240, s[16:19], s40 offen offset:2048 nt lds\n"
"v_mfma_f32_16x16x32_bf16 v[32:35], v[136:139], a[244:247], v[32:35]\n"
"v_exp_f32 v160, v160\n"
"v_mfma_f32_16x16x32_bf16 v[32:35], v[140:143], a[248:251], v[32:35]\n"
"v_exp_f32 v178, v178\n"
"v_mfma_f32_16x16x32_bf16 v[64:67], v[144:147], a[244:247], v[64:67]\n"
"v_exp_f32 v152, v152\n"
"v_mfma_f32_16x16x32_bf16 v[64:67], v[148:151], a[248:251], v[64:67]\n"
"v_exp_f32 v161, v161\n"
"v_mfma_f32_16x16x32_bf16 v[68:71], v[148:151], v[224:227], v[68:71]\n"
"s_waitcnt lgkmcnt(1)\n"
"ds_read_b64_tr_b16 a[244:245], v232 offset:1024\n"
"v_cvt_pk_bf16_f32 v115, v178, v179\n"
"v_mfma_f32_16x16x32_bf16 v[68:71], v[144:147], a[252:255], v[68:71]\n"
"ds_read_b64_tr_b16 a[248:249], v236 offset:1024\n"
"v_cvt_pk_bf16_f32 v114, v176, v177\n"
"v_mfma_f32_16x16x32_bf16 v[36:39], v[140:143], v[224:227], v[36:39]\n"
"ds_read_b64_tr_b16 a[250:251], v236 offset:5120\n"
"v_cvt_pk_bf16_f32 v99, v170, v171\n"
"v_mfma_f32_16x16x32_bf16 v[36:39], v[136:139], a[252:255], v[36:39]\n"
"v_exp_f32 v163, v163\n"
"v_mfma_f32_16x16x32_bf16 v[4:7], v[132:135], v[224:227], v[4:7]\n"
"s_add_u32 s40, s56, s54\n"
"ds_read_b64_tr_b16 a[246:247], v232 offset:5120\n"
"v_mfma_f32_16x16x32_bf16 v[4:7], v[128:131], a[252:255], v[4:7]\n"
"buffer_load_dwordx4 v240, s[16:19], s40 offen offset:3072 nt lds\n"
"v_mfma_f32_16x16x32_bf16 a[212:215], v[124:127], a[252:255], a[212:215]\n"
"v_exp_f32 v167, v167\n"
"v_smfmac_f32_16x16x64_bf16 v[200:203], a[240:243], v[144:151], v241\n"
"v_cvt_pk_bf16_f32 v122, v152, v153\n"
"v_mfma_f32_16x16x32_bf16 v[168:171], v[208:211], a[0:3], v[180:183]\n"
"v_exp_f32 v159, v159\n"
"v_mfma_f32_16x16x32_bf16 v[168:171], v[212:215], a[4:7], v[168:171]\n"
"s_waitcnt lgkmcnt(2)\n"
"v_exp_f32 v165, v165\n"
"v_mfma_f32_16x16x32_bf16 v[168:171], v[216:219], a[8:11], v[168:171]\n"
"ds_read_b64_tr_b16 a[252:253], v233 offset:1024\n"
"ds_read_b64_tr_b16 v[224:225], v237 offset:1024\n"
"v_mfma_f32_16x16x32_bf16 v[168:171], v[220:223], a[12:15], v[168:171]\n"
"ds_read_b64_tr_b16 a[254:255], v233 offset:5120\n"
"ds_read_b64_tr_b16 v[226:227], v237 offset:5120\n"
"v_mfma_f32_16x16x32_bf16 v[172:175], v[216:219], a[24:27], v[184:187]\n"
"v_exp_f32 v166, v166\n"
"v_mfma_f32_16x16x32_bf16 v[172:175], v[220:223], a[28:31], v[172:175]\n"
"v_exp_f32 v164, v164\n"
"v_mfma_f32_16x16x32_bf16 v[172:175], v[208:211], a[16:19], v[172:175]\n"
"v_exp_f32 v157, v157\n"
"v_mfma_f32_16x16x32_bf16 v[172:175], v[212:215], a[20:23], v[172:175]\n"
"s_cbranch_vccnz LPCL1_%=\nLPKL1_%=:\n"
"v_cvt_pk_bf16_f32 v123, v154, v155\n"
"v_mfma_f32_16x16x32_bf16 v[176:179], v[208:211], a[32:35], v[188:191]\n"
"v_exp_f32 v158, v158\n"
"v_mfma_f32_16x16x32_bf16 v[176:179], v[212:215], a[36:39], v[176:179]\n"
"v_exp_f32 v162, v162\n"
"v_mfma_f32_16x16x32_bf16 v[176:179], v[216:219], a[40:43], v[176:179]\n"
"v_exp_f32 v156, v156\n"
"v_mfma_f32_16x16x32_bf16 v[176:179], v[220:223], a[44:47], v[176:179]\n"
"s_waitcnt lgkmcnt(0)\n"
"v_cvt_pk_bf16_f32 v101, v158, v159\n"
"v_mfma_f32_16x16x32_bf16 a[224:227], v[124:127], a[244:247], a[224:227]\n"
"v_exp_f32 v169, v169\n"
"v_mfma_f32_16x16x32_bf16 v[16:19], v[128:131], a[244:247], v[16:19]\n"
"s_waitcnt vmcnt(8)\n"
"v_exp_f32 v172, v172\n"
"v_mfma_f32_16x16x32_bf16 v[16:19], v[132:135], a[248:251], v[16:19]\n"
"s_barrier\n"
"ds_read_b128 v[216:219], v228 offset:33792\n"
"v_mfma_f32_16x16x32_bf16 v[48:51], v[136:139], a[244:247], v[48:51]\n"
"ds_read_b128 v[212:215], v229 offset:32768\n"
"v_cvt_pk_bf16_f32 v108, v160, v161\n"
"v_mfma_f32_16x16x32_bf16 v[48:51], v[140:143], a[248:251], v[48:51]\n"
"ds_read_b128 v[208:211], v228 offset:32768\n"
"v_cvt_pk_bf16_f32 v100, v156, v157\n"
"v_mfma_f32_16x16x32_bf16 v[80:83], v[144:147], a[244:247], v[80:83]\n"
"ds_read_b128 v[220:223], v229 offset:33792\n"
"v_cvt_pk_bf16_f32 v117, v166, v167\n"
"v_mfma_f32_16x16x32_bf16 v[80:83], v[148:151], a[248:251], v[80:83]\n"
"ds_read_b64_tr_b16 a[244:245], v234 offset:16384\n"
"v_cvt_pk_bf16_f32 v109, v162, v163\n"
"v_mfma_f32_16x16x32_bf16 v[84:87], v[148:151], v[224:227], v[84:87]\n"
"ds_read_b64_tr_b16 a[248:249], v238 offset:16384\n"
"v_mfma_f32_16x16x32_bf16 v[84:87], v[144:147], a[252:255], v[84:87]\n"
"ds_read_b64_tr_b16 a[246:247], v234 offset:20480\n"
"v_mfma_f32_16x16x32_bf16 v[52:55], v[140:143], v[224:227], v[52:55]\n"
"ds_read_b64_tr_b16 a[250:251], v238 offset:20480\n"
"v_mfma_f32_16x16x32_bf16 v[52:55], v[136:139], a[252:255], v[52:55]\n"
"v_exp_f32 v175, v175\n"
"v_mfma_f32_16x16x32_bf16 v[20:23], v[132:135], v[224:227], v[20:23]\n"
"ds_read_b64_tr_b16 v[224:225], v239 offset:16384\n"
"v_mfma_f32_16x16x32_bf16 v[20:23], v[128:131], a[252:255], v[20:23]\n"
"v_exp_f32 v173, v173\n"
"v_mfma_f32_16x16x32_bf16 a[228:231], v[124:127], a[252:255], a[228:231]\n"
"s_waitcnt lgkmcnt(5)\n"
"ds_read_b64_tr_b16 a[254:255], v235 offset:20480\n"
"v_cvt_pk_bf16_f32 v116, v164, v165\n"
"LAY2_%=:\n"
"v_mfma_f32_16x16x32_bf16 v[152:155], v[208:211], a[0:3], v[180:183]\n"
"s_waitcnt lgkmcnt(8)\n"
"v_exp_f32 v174, v174\n"
"v_mfma_f32_16x16x32_bf16 v[152:155], v[212:215], a[4:7], v[152:155]\n"
"s_waitcnt lgkmcnt(7)\n"
"ds_read_b64_tr_b16 a[252:253], v235 offset:16384\n"
"v_cvt_pk_bf16_f32 v111, v174, v175\n"
"v_mfma_f32_16x16x32_bf16 v[152:155], v[216:219], a[8:11], v[152:155]\n"
"s_waitcnt lgkmcnt(7)\n"
"s_add_u32 m0, s44, 114688\n"
"ds_read_b64_tr_b16 v[226:227], v239 offset:20480\n"
"v_mfma_f32_16x16x32_bf16 v[152:155], v[220:223], a[12:15], v[152:155]\n"
"buffer_load_dwordx4 v240, s[20:23], s56 offen nt lds\n"
"v_mfma_f32_16x16x32_bf16 v[156:159], v[216:219], a[24:27], v[184:187]\n"
"v_exp_f32 v171, v171\n"
"v_mfma_f32_16x16x32_bf16 v[156:159], v[220:223], a[28:31], v[156:159]\n"
"v_exp_f32 v168, v168\n"
"v_mfma_f32_16x16x32_bf16 v[156:159], v[208:211], a[16:19], v[156:159]\n"
"v_exp_f32 v176, v176\n"
"v_mfma_f32_16x16x32_bf16 v[156:159], v[212:215], a[20:23], v[156:159]\n"
"v_exp_f32 v177, v177\n"
"v_mfma_f32_16x16x32_bf16 v[160:163], v[208:211], a[32:35], v[188:191]\n"
"v_exp_f32 v179, v179\n"
"v_mfma_f32_16x16x32_bf16 v[160:163], v[212:215], a[36:39], v[160:163]\n"
"v_exp_f32 v170, v170\n"
"v_mfma_f32_16x16x32_bf16 v[160:163], v[216:219], a[40:43], v[160:163]\n"
"v_exp_f32 v153, v153\n"
"v_mfma_f32_16x16x32_bf16 v[160:163], v[220:223], a[44:47], v[160:163]\n"
"v_cvt_pk_bf16_f32 v102, v168, v169\n"
"v_mfma_f32_16x16x32_bf16 v[164:167], v[216:219], a[200:203], v[192:195]\n"
"ds_read_b128 v[216:219], v228 offset:37888\n"
"v_cvt_pk_bf16_f32 v103, v170, v171\n"
"v_mfma_f32_16x16x32_bf16 v[164:167], v[220:223], a[204:207], v[164:167]\n"
"v_cvt_pk_bf16_f32 v110, v172, v173\n"
"v_cvt_pk_bf16_f32 v118, v176, v177\n"
"v_mfma_f32_16x16x32_bf16 v[164:167], v[208:211], a[192:195], v[164:167]\n"
"s_add_u32 s40, s56, s52\n"
"ds_read_b128 v[208:211], v228 offset:36864\n"
"v_mfma_f32_16x16x32_bf16 v[164:167], v[212:215], a[196:199], v[164:167]\n"
"s_waitcnt lgkmcnt(2)\n"
"buffer_load_dwordx4 v240, s[20:23], s40 offen offset:1024 nt lds\n"
"v_mfma_f32_16x16x32_bf16 a[216:219], v[120:123], a[244:247], a[216:219]\n"
"v_exp_f32 v155, v155\n"
"v_mfma_f32_16x16x32_bf16 v[8:11], v[96:99], a[244:247], v[8:11]\n"
"s_waitcnt lgkmcnt(2)\n"
"v_exp_f32 v161, v161\n"
"v_mfma_f32_16x16x32_bf16 v[8:11], v[100:103], a[248:251], v[8:11]\n"
"v_exp_f32 v163, v163\n"
"v_mfma_f32_16x16x32_bf16 v[40:43], v[104:107], a[244:247], v[40:43]\n"
"v_exp_f32 v178, v178\n"
"v_mfma_f32_16x16x32_bf16 v[40:43], v[108:111], a[248:251], v[40:43]\n"
"ds_read_b128 v[220:223], v229 offset:37888\n"
"v_cvt_pk_bf16_f32 v119, v178, v179\n"
"v_mfma_f32_16x16x32_bf16 v[72:75], v[112:115], a[244:247], v[72:75]\n"
"ds_read_b64_tr_b16 a[244:245], v234 offset:17408\n"
"v_mfma_f32_16x16x32_bf16 v[72:75], v[116:119], a[248:251], v[72:75]\n"
"v_exp_f32 v158, v158\n"
"v_mfma_f32_16x16x32_bf16 v[76:79], v[116:119], v[224:227], v[76:79]\n"
"ds_read_b128 v[212:215], v229 offset:36864\n"
"v_mfma_f32_16x16x32_bf16 v[76:79], v[112:115], a[252:255], v[76:79]\n"
"v_exp_f32 v160, v160\n"
"v_mfma_f32_16x16x32_bf16 v[44:47], v[108:111], v[224:227], v[44:47]\n"
"s_add_u32 s40, s56, s53\n"
"ds_read_b64_tr_b16 a[248:249], v238 offset:17408\n"
"v_mfma_f32_16x16x32_bf16 v[44:47], v[104:107], a[252:255], v[44:47]\n"
"buffer_load_dwordx4 v240, s[20:23], s40 offen offset:2048 nt lds\n"
"v_mfma_f32_16x16x32_bf16 v[12:15], v[100:103], v[224:227], v[12:15]\n"
"v_exp_f32 v165, v165\n"
"v_mfma_f32_16x16x32_bf16 v[12:15], v[96:99], a[252:255], v[12:15]\n"
"v_exp_f32 v159, v159\n"
"v_mfma_f32_16x16x32_bf16 a[220:223], v[120:123], a[252:255], a[220:223]\n"
"v_exp_f32 v152, v152\n"
"v_smfmac_f32_16x16x64_bf16 v[196:199], a[240:243], v[96:103], v241\n"
"v_mfma_f32_16x16x32_bf16 v[202:205], a[240:243], v[120:123], v[202:205]\n"
"s_waitcnt lgkmcnt(1)\n"
"ds_read_b64_tr_b16 v[224:225], v239 offset:17408\n"
"v_cvt_pk_bf16_f32 v128, v152, v153\n"
"v_mfma_f32_16x16x32_bf16 v[168:171], v[216:219], a[8:11], v[180:183]\n"
"v_exp_f32 v162, v162\n"
"v_mfma_f32_16x16x32_bf16 v[168:171], v[220:223], a[12:15], v[168:171]\n"
"ds_read_b64_tr_b16 a[250:251], v238 offset:21504\n"
"v_mfma_f32_16x16x32_bf16 v[168:171], v[208:211], a[0:3], v[168:171]\n"
"ds_read_b64_tr_b16 a[252:253], v235 offset:17408\n"
"v_mfma_f32_16x16x32_bf16 v[168:171], v[212:215], a[4:7], v[168:171]\n"
"v_exp_f32 v154, v154\n"
"v_mfma_f32_16x16x32_bf16 v[172:175], v[208:211], a[16:19], v[184:187]\n"
"ds_read_b64_tr_b16 v[226:227], v239 offset:21504\n"
"v_mfma_f32_16x16x32_bf16 v[172:175], v[212:215], a[20:23], v[172:175]\n"
"ds_read_b64_tr_b16 a[246:247], v234 offset:21504\n"
"v_cvt_pk_bf16_f32 v129, v154, v155\n"
"v_mfma_f32_16x16x32_bf16 v[172:175], v[216:219], a[24:27], v[172:175]\n"
"s_add_u32 s40, s56, s54\n"
"ds_read_b64_tr_b16 a[254:255], v235 offset:21504\n"
"v_mfma_f32_16x16x32_bf16 v[172:175], v[220:223], a[28:31], v[172:175]\n"
"buffer_load_dwordx4 v240, s[20:23], s40 offen offset:3072 nt lds\n"
"v_mfma_f32_16x16x32_bf16 v[176:179], v[216:219], a[40:43], v[188:191]\n"
"s_add_u32 s55, s55, s59\n"
"v_exp_f32 v157, v157\n"
"v_mfma_f32_16x16x32_bf16 v[176:179], v[220:223], a[44:47], v[176:179]\n"
"s_add_u32 s41, s55, s57\n"
"v_exp_f32 v170, v170\n"
"v_mfma_f32_16x16x32_bf16 v[176:179], v[208:211], a[32:35], v[176:179]\n"
"s_and_b32 s41, s41, s58\n"
"v_exp_f32 v156, v156\n"
"v_mfma_f32_16x16x32_bf16 v[176:179], v[212:215], a[36:39], v[176:179]\n"
"s_andn2_b32 s56, s55, s58\n"
"v_exp_f32 v166, v166\n"
"v_mfma_f32_16x16x32_bf16 v[152:155], v[208:211], a[192:195], v[192:195]\n"
"s_or_b32 s56, s56, s41\n"
"ds_read_b128 v[208:211], v230 offset:32768\n"
"v_mfma_f32_16x16x32_bf16 v[152:155], v[212:215], a[196:199], v[152:155]\n"
"s_cmp_ge_u32 s47, 15\n"
"v_exp_f32 v174, v174\n"
"v_mfma_f32_16x16x32_bf16 v[152:155], v[216:219], a[200:203], v[152:155]\n"
"s_cbranch_scc1 LSW_%=\nLSWK_%=:\n"
"ds_read_b128 v[212:215], v231 offset:32768\n"
"v_mfma_f32_16x16x32_bf16 v[152:155], v[220:223], a[204:207], v[152:155]\n"
"s_waitcnt lgkmcnt(2)\n"
"ds_read_b128 v[216:219], v230 offset:33792\n"
"v_cvt_pk_bf16_f32 v137, v158, v159\n"
"v_mfma_f32_16x16x32_bf16 a[232:235], v[120:123], a[244:247], a[232:235]\n"
"v_exp_f32 v164, v164\n"
"v_mfma_f32_16x16x32_bf16 v[24:27], v[96:99], a[244:247], v[24:27]\n"
"s_mov_b32 m0, s44\n"
"ds_read_b128 v[220:223], v231 offset:33792\n"
"v_mfma_f32_16x16x32_bf16 v[24:27], v[100:103], a[248:251], v[24:27]\n"
"buffer_load_dwordx4 v240, s[16:19], s56 offen nt lds\n"
"v_mfma_f32_16x16x32_bf16 v[56:59], v[104:107], a[244:247], v[56:59]\n"
"v_exp_f32 v167, v167\n"
"v_mfma_f32_16x16x32_bf16 v[56:59], v[108:111], a[248:251], v[56:59]\n"
"v_exp_f32 v176, v176\n"
"v_mfma_f32_16x16x32_bf16 v[88:91], v[112:115], a[244:247], v[88:91]\n"
"v_exp_f32 v153, v153\n"
"v_mfma_f32_16x16x32_bf16 v[88:91], v[116:119], a[248:251], v[88:91]\n"
"v_exp_f32 v173, v173\n"
"v_mfma_f32_16x16x32_bf16 v[92:95], v[116:119], v[224:227], v[92:95]\n"
"ds_read_b64_tr_b16 a[250:251], v236 offset:20480\n"
"v_cvt_pk_bf16_f32 v125, v166, v167\n"
"v_mfma_f32_16x16x32_bf16 v[92:95], v[112:115], a[252:255], v[92:95]\n"
"ds_read_b64_tr_b16 a[244:245], v232 offset:16384\n"
"v_cvt_pk_bf16_f32 v144, v160, v161\n"
"v_mfma_f32_16x16x32_bf16 v[60:63], v[108:111], v[224:227], v[60:63]\n"
"ds_read_b64_tr_b16 a[248:249], v236 offset:16384\n"
"v_cvt_pk_bf16_f32 v124, v164, v165\n"
"v_mfma_f32_16x16x32_bf16 v[60:63], v[104:107], a[252:255], v[60:63]\n"
"ds_read_b64_tr_b16 a[246:247], v232 offset:20480\n"
"v_cvt_pk_bf16_f32 v136, v156, v157\n"
"v_mfma_f32_16x16x32_bf16 v[28:31], v[100:103], v[224:227], v[28:31]\n"
"ds_read_b64_tr_b16 v[226:227], v237 offset:20480\n"
"v_cvt_pk_bf16_f32 v145, v162, v163\n"
"v_mfma_f32_16x16x32_bf16 v[28:31], v[96:99], a[252:255], v[28:31]\n"
"ds_read_b64_tr_b16 v[224:225], v237 offset:16384\n"
"v_mfma_f32_16x16x32_bf16 a[236:239], v[120:123], a[252:255], a[236:239]\n"
"s_add_u32 s40, s56, s52\n"
"ds_read_b64_tr_b16 a[252:253], v233 offset:16384\n"
"v_smfmac_f32_16x16x64_bf16 v[198:201], a[240:243], v[104:111], v241\n"
"s_waitcnt lgkmcnt(7)\n"
"buffer_load_dwordx4 v240, s[16:19], s40 offen offset:1024 nt lds\n"
"v_mfma_f32_16x16x32_bf16 v[156:159], v[208:211], a[0:3], v[180:183]\n"
"v_exp_f32 v169, v169\n"
"v_mfma_f32_16x16x32_bf16 v[156:159], v[212:215], a[4:7], v[156:159]\n"
"v_exp_f32 v168, v168\n"
"v_mfma_f32_16x16x32_bf16 v[156:159], v[216:219], a[8:11], v[156:159]\n"
"v_exp_f32 v175, v175\n"
"v_mfma_f32_16x16x32_bf16 v[156:159], v[220:223], a[12:15], v[156:159]\n"
"v_exp_f32 v172, v172\n"
"v_mfma_f32_16x16x32_bf16 v[160:163], v[216:219], a[24:27], v[184:187]\n"
"ds_read_b64_tr_b16 a[254:255], v233 offset:20480\n"
"v_mfma_f32_16x16x32_bf16 v[160:163], v[220:223], a[28:31], v[160:163]\n"
"v_exp_f32 v155, v155\n"
"v_mfma_f32_16x16x32_bf16 v[160:163], v[208:211], a[16:19], v[160:163]\n"
"v_exp_f32 v177, v177\n"
"v_mfma_f32_16x16x32_bf16 v[160:163], v[212:215], a[20:23], v[160:163]\n"
"v_exp_f32 v179, v179\n"
"v_mfma_f32_16x16x32_bf16 v[164:167], v[208:211], a[32:35], v[188:191]\n"
"ds_read_b128 v[208:211], v230 offset:36864\n"
"v_cvt_pk_bf16_f32 v139, v174, v175\n"
"v_mfma_f32_16x16x32_bf16 v[164:167], v[212:215], a[36:39], v[164:167]\n"
"v_exp_f32 v171, v171\n"
"v_mfma_f32_16x16x32_bf16 v[164:167], v[216:219], a[40:43], v[164:167]\n"
"ds_read_b128 v[212:215], v231 offset:36864\n"
"v_cvt_pk_bf16_f32 v138, v172, v173\n"
"v_mfma_f32_16x16x32_bf16 v[164:167], v[220:223], a[44:47], v[164:167]\n"
"s_waitcnt lgkmcnt(3)\n"
"ds_read_b128 v[220:223], v231 offset:37888\n"
"v_cvt_pk_bf16_f32 v130, v168, v169\n"
"v_mfma_f32_16x16x32_bf16 a[208:211], v[120:123], a[244:247], a[208:211]\n"
"v_exp_f32 v154, v154\n"
"v_mfma_f32_16x16x32_bf16 v[0:3], v[96:99], a[244:247], v[0:3]\n"
"s_add_u32 s40, s56, s53\n"
"ds_read_b128 v[216:219], v230 offset:37888\n"
"v_mfma_f32_16x16x32_bf16 v[0:3], v[100:103], a[248:251], v[0:3]\n"
"buffer_load_dwordx4 v240, s[16:19], s40 offen offset:2048 nt lds\n"
"v_mfma_f32_16x16x32_bf16 v[32:35], v[104:107], a[244:247], v[32:35]\n"
"v_exp_f32 v160, v160\n"
"v_mfma_f32_16x16x32_bf16 v[32:35], v[108:111], a[248:251], v[32:35]\n"
"v_exp_f32 v178, v178\n"
"v_mfma_f32_16x16x32_bf16 v[64:67], v[112:115], a[244:247], v[64:67]\n"
"v_exp_f32 v152, v152\n"
"v_mfma_f32_16x16x32_bf16 v[64:67], v[116:119], a[248:251], v[64:67]\n"
"v_exp_f32 v161, v161\n"
"v_mfma_f32_16x16x32_bf16 v[68:71], v[116:119], v[224:227], v[68:71]\n"
"s_waitcnt lgkmcnt(1)\n"
"ds_read_b64_tr_b16 a[244:245], v232 offset:17408\n"
"v_cvt_pk_bf16_f32 v147, v178, v179\n"
"v_mfma_f32_16x16x32_bf16 v[68:71], v[112:115], a[252:255], v[68:71]\n"
"ds_read_b64_tr_b16 a[248:249], v236 offset:17408\n"
"v_cvt_pk_bf16_f32 v146, v176, v177\n"
"v_mfma_f32_16x16x32_bf16 v[36:39], v[108:111], v[224:227], v[36:39]\n"
"ds_read_b64_tr_b16 a[250:251], v236 offset:21504\n"
"v_cvt_pk_bf16_f32 v131, v170, v171\n"
"v_mfma_f32_16x16x32_bf16 v[36:39], v[104:107], a[252:255], v[36:39]\n"
"v_exp_f32 v163, v163\n"
"v_mfma_f32_16x16x32_bf16 v[4:7], v[100:103], v[224:227], v[4:7]\n"
"s_add_u32 s40, s56, s54\n"
"ds_read_b64_tr_b16 a[246:247], v232 offset:21504\n"
"v_mfma_f32_16x16x32_bf16 v[4:7], v[96:99], a[252:255], v[4:7]\n"
"buffer_load_dwordx4 v240, s[16:19], s40 offen offset:3072 nt lds\n"
"v_mfma_f32_16x16x32_bf16 a[212:215], v[120:123], a[252:255], a[212:215]\n"
"v_exp_f32 v167, v167\n"
"v_smfmac_f32_16x16x64_bf16 v[200:203], a[240:243], v[112:119], v241\n"
"v_cvt_pk_bf16_f32 v126, v152, v153\n"
"v_mfma_f32_16x16x32_bf16 v[168:171], v[208:211], a[0:3], v[180:183]\n"
"v_exp_f32 v159, v159\n"
"v_mfma_f32_16x16x32_bf16 v[168:171], v[212:215], a[4:7], v[168:171]\n"
"s_waitcnt lgkmcnt(2)\n"
"v_exp_f32 v165, v165\n"
"v_mfma_f32_16x16x32_bf16 v[168:171], v[216:219], a[8:11], v[168:171]\n"
"ds_read_b64_tr_b16 a[252:253], v233 offset:17408\n"
"ds_read_b64_tr_b16 v[224:225], v237 offset:17408\n"
"v_mfma_f32_16x16x32_bf16 v[168:171], v[220:223], a[12:15], v[168:171]\n"
"ds_read_b64_tr_b16 a[254:255], v233 offset:21504\n"
"ds_read_b64_tr_b16 v[226:227], v237 offset:21504\n"
"v_mfma_f32_16x16x32_bf16 v[172:175], v[216:219], a[24:27], v[184:187]\n"
"v_exp_f32 v166, v166\n"
"v_mfma_f32_16x16x32_bf16 v[172:175], v[220:223], a[28:31], v[172:175]\n"
"v_exp_f32 v164, v164\n"
"v_mfma_f32_16x16x32_bf16 v[172:175], v[208:211], a[16:19], v[172:175]\n"
"v_exp_f32 v157, v157\n"
"v_mfma_f32_16x16x32_bf16 v[172:175], v[212:215], a[20:23], v[172:175]\n"
"s_cbranch_vccnz LPCL2_%=\nLPKL2_%=:\n"
"v_cvt_pk_bf16_f32 v127, v154, v155\n"
"v_mfma_f32_16x16x32_bf16 v[176:179], v[208:211], a[32:35], v[188:191]\n"
"v_exp_f32 v158, v158\n"
"v_mfma_f32_16x16x32_bf16 v[176:179], v[212:215], a[36:39], v[176:179]\n"
"v_exp_f32 v162, v162\n"
"v_mfma_f32_16x16x32_bf16 v[176:179], v[216:219], a[40:43], v[176:179]\n"
"v_exp_f32 v156, v156\n"
"v_mfma_f32_16x16x32_bf16 v[176:179], v[220:223], a[44:47], v[176:179]\n"
"s_waitcnt lgkmcnt(0)\n"
"v_cvt_pk_bf16_f32 v133, v158, v159\n"
"v_mfma_f32_16x16x32_bf16 a[224:227], v[120:123], a[244:247], a[224:227]\n"
"v_exp_f32 v169, v169\n"
"v_mfma_f32_16x16x32_bf16 v[16:19], v[96:99], a[244:247], v[16:19]\n"
"s_waitcnt vmcnt(8)\n"
"v_exp_f32 v172, v172\n"
"v_mfma_f32_16x16x32_bf16 v[16:19], v[100:103], a[248:251], v[16:19]\n"
"s_barrier\n"
"ds_read_b128 v[216:219], v228 offset:50176\n"
"v_mfma_f32_16x16x32_bf16 v[48:51], v[104:107], a[244:247], v[48:51]\n"
"ds_read_b128 v[212:215], v229 offset:49152\n"
"v_cvt_pk_bf16_f32 v140, v160, v161\n"
"v_mfma_f32_16x16x32_bf16 v[48:51], v[108:111], a[248:251], v[48:51]\n"
"ds_read_b128 v[208:211], v228 offset:49152\n"
"v_cvt_pk_bf16_f32 v132, v156, v157\n"
"v_mfma_f32_16x16x32_bf16 v[80:83], v[112:115], a[244:247], v[80:83]\n"
"ds_read_b128 v[220:223], v229 offset:50176\n"
"v_cvt_pk_bf16_f32 v149, v166, v167\n"
"v_mfma_f32_16x16x32_bf16 v[80:83], v[116:119], a[248:251], v[80:83]\n"
"ds_read_b64_tr_b16 a[244:245], v234 offset:32768\n"
"v_cvt_pk_bf16_f32 v141, v162, v163\n"
"v_mfma_f32_16x16x32_bf16 v[84:87], v[116:119], v[224:227], v[84:87]\n"
"ds_read_b64_tr_b16 a[248:249], v238 offset:32768\n"
"v_mfma_f32_16x16x32_bf16 v[84:87], v[112:115], a[252:255], v[84:87]\n"
"ds_read_b64_tr_b16 a[246:247], v234 offset:36864\n"
"v_mfma_f32_16x16x32_bf16 v[52:55], v[108:111], v[224:227], v[52:55]\n"
"ds_read_b64_tr_b16 a[250:251], v238 offset:36864\n"
"v_mfma_f32_16x16x32_bf16 v[52:55], v[104:107], a[252:255], v[52:55]\n"
"v_exp_f32 v175, v175\n"
"v_mfma_f32_16x16x32_bf16 v[20:23], v[100:103], v[224:227], v[20:23]\n"
"ds_read_b64_tr_b16 v[224:225], v239 offset:32768\n"
"v_mfma_f32_16x16x32_bf16 v[20:23], v[96:99], a[252:255], v[20:23]\n"
"v_exp_f32 v173, v173\n"
"v_mfma_f32_16x16x32_bf16 a[228:231], v[120:123], a[252:255], a[228:231]\n"
"s_waitcnt lgkmcnt(5)\n"
"ds_read_b64_tr_b16 a[254:255], v235 offset:36864\n"
"v_cvt_pk_bf16_f32 v148, v164, v165\n"
"v_mfma_f32_16x16x32_bf16 v[152:155], v[208:211], a[0:3], v[180:183]\n"
"v_exp_f32 v174, v174\n"
"v_mfma_f32_16x16x32_bf16 v[152:155], v[212:215], a[4:7], v[152:155]\n"
"ds_read_b64_tr_b16 a[252:253], v235 offset:32768\n"
"v_cvt_pk_bf16_f32 v143, v174, v175\n"
"v_mfma_f32_16x16x32_bf16 v[152:155], v[216:219], a[8:11], v[152:155]\n"
"s_add_u32 m0, s44, 65536\n"
"ds_read_b64_tr_b16 v[226:227], v239 offset:36864\n"
"v_mfma_f32_16x16x32_bf16 v[152:155], v[220:223], a[12:15], v[152:155]\n"
"buffer_load_dwordx4 v240, s[20:23], s56 offen nt lds\n"
"v_mfma_f32_16x16x32_bf16 v[156:159], v[216:219], a[24:27], v[184:187]\n"
"v_exp_f32 v171, v171\n"
"v_mfma_f32_16x16x32_bf16 v[156:159], v[220:223], a[28:31], v[156:159]\n"
"v_exp_f32 v168, v168\n"
"v_mfma_f32_16x16x32_bf16 v[156:159], v[208:211], a[16:19], v[156:159]\n"
"v_exp_f32 v176, v176\n"
"v_mfma_f32_16x16x32_bf16 v[156:159], v[212:215], a[20:23], v[156:159]\n"
"v_exp_f32 v177, v177\n"
"v_mfma_f32_16x16x32_bf16 v[160:163], v[208:211], a[32:35], v[188:191]\n"
"v_exp_f32 v179, v179\n"
"v_mfma_f32_16x16x32_bf16 v[160:163], v[212:215], a[36:39], v[160:163]\n"
"v_exp_f32 v170, v170\n"
"v_mfma_f32_16x16x32_bf16 v[160:163], v[216:219], a[40:43], v[160:163]\n"
"v_exp_f32 v153, v153\n"
"v_mfma_f32_16x16x32_bf16 v[160:163], v[220:223], a[44:47], v[160:163]\n"
"v_cvt_pk_bf16_f32 v134, v168, v169\n"
"v_mfma_f32_16x16x32_bf16 v[164:167], v[216:219], a[200:203], v[192:195]\n"
"ds_read_b128 v[216:219], v228 offset:54272\n"
"v_cvt_pk_bf16_f32 v135, v170, v171\n"
"v_mfma_f32_16x16x32_bf16 v[164:167], v[220:223], a[204:207], v[164:167]\n"
"v_cvt_pk_bf16_f32 v142, v172, v173\n"
"v_cvt_pk_bf16_f32 v150, v176, v177\n"
"v_mfma_f32_16x16x32_bf16 v[164:167], v[208:211], a[192:195], v[164:167]\n"
"s_add_u32 s40, s56, s52\n"
"ds_read_b128 v[208:211], v228 offset:53248\n"
"v_mfma_f32_16x16x32_bf16 v[164:167], v[212:215], a[196:199], v[164:167]\n"
"s_waitcnt lgkmcnt(2)\n"
"buffer_load_dwordx4 v240, s[20:23], s40 offen offset:1024 nt lds\n"
"v_mfma_f32_16x16x32_bf16 a[216:219], v[124:127], a[244:247], a[216:219]\n"
"v_exp_f32 v155, v155\n"
"v_mfma_f32_16x16x32_bf16 v[8:11], v[128:131], a[244:247], v[8:11]\n"
"v_exp_f32 v161, v161\n"
"v_mfma_f32_16x16x32_bf16 v[8:11], v[132:135], a[248:251], v[8:11]\n"
"v_exp_f32 v163, v163\n"
"v_mfma_f32_16x16x32_bf16 v[40:43], v[136:139], a[244:247], v[40:43]\n"
"v_exp_f32 v178, v178\n"
"v_mfma_f32_16x16x32_bf16 v[40:43], v[140:143], a[248:251], v[40:43]\n"
"ds_read_b128 v[220:223], v229 offset:54272\n"
"v_cvt_pk_bf16_f32 v151, v178, v179\n"
"v_mfma_f32_16x16x32_bf16 v[72:75], v[144:147], a[244:247], v[72:75]\n"
"ds_read_b64_tr_b16 a[244:245], v234 offset:33792\n"
"v_mfma_f32_16x16x32_bf16 v[72:75], v[148:151], a[248:251], v[72:75]\n"
"v_exp_f32 v158, v158\n"
"v_mfma_f32_16x16x32_bf16 v[76:79], v[148:151], v[224:227], v[76:79]\n"
"ds_read_b128 v[212:215], v229 offset:53248\n"
"v_mfma_f32_16x16x32_bf16 v[76:79], v[144:147], a[252:255], v[76:79]\n"
"v_exp_f32 v160, v160\n"
"v_mfma_f32_16x16x32_bf16 v[44:47], v[140:143], v[224:227], v[44:47]\n"
"s_add_u32 s40, s56, s53\n"
"ds_read_b64_tr_b16 a[248:249], v238 offset:33792\n"
"v_mfma_f32_16x16x32_bf16 v[44:47], v[136:139], a[252:255], v[44:47]\n"
"buffer_load_dwordx4 v240, s[20:23], s40 offen offset:2048 nt lds\n"
"v_mfma_f32_16x16x32_bf16 v[12:15], v[132:135], v[224:227], v[12:15]\n"
"v_exp_f32 v165, v165\n"
"v_mfma_f32_16x16x32_bf16 v[12:15], v[128:131], a[252:255], v[12:15]\n"
"v_exp_f32 v159, v159\n"
"v_mfma_f32_16x16x32_bf16 a[220:223], v[124:127], a[252:255], a[220:223]\n"
"v_exp_f32 v152, v152\n"
"v_smfmac_f32_16x16x64_bf16 v[196:199], a[240:243], v[128:135], v241\n"
"v_mfma_f32_16x16x32_bf16 v[202:205], a[240:243], v[124:127], v[202:205]\n"
"s_waitcnt lgkmcnt(1)\n"
"ds_read_b64_tr_b16 v[224:225], v239 offset:33792\n"
"v_cvt_pk_bf16_f32 v96, v152, v153\n"
"v_mfma_f32_16x16x32_bf16 v[168:171], v[216:219], a[8:11], v[180:183]\n"
"v_exp_f32 v162, v162\n"
"v_mfma_f32_16x16x32_bf16 v[168:171], v[220:223], a[12:15], v[168:171]\n"
"ds_read_b64_tr_b16 a[250:251], v238 offset:37888\n"
"v_mfma_f32_16x16x32_bf16 v[168:171], v[208:211], a[0:3], v[168:171]\n"
"ds_read_b64_tr_b16 a[252:253], v235 offset:33792\n"
"v_mfma_f32_16x16x32_bf16 v[168:171], v[212:215], a[4:7], v[168:171]\n"
"v_exp_f32 v154, v154\n"
"v_mfma_f32_16x16x32_bf16 v[172:175], v[208:211], a[16:19], v[184:187]\n"
"ds_read_b64_tr_b16 v[226:227], v239 offset:37888\n"
"v_mfma_f32_16x16x32_bf16 v[172:175], v[212:215], a[20:23], v[172:175]\n"
"ds_read_b64_tr_b16 a[246:247], v234 offset:37888\n"
"v_cvt_pk_bf16_f32 v97, v154, v155\n"
"v_mfma_f32_16x16x32_bf16 v[172:175], v[216:219], a[24:27], v[172:175]\n"
"s_add_u32 s40, s56, s54\n"
"ds_read_b64_tr_b16 a[254:255], v235 offset:37888\n"
"v_mfma_f32_16x16x32_bf16 v[172:175], v[220:223], a[28:31], v[172:175]\n"
"buffer_load_dwordx4 v240, s[20:23], s40 offen offset:3072 nt lds\n"
"v_mfma_f32_16x16x32_bf16 v[176:179], v[216:219], a[40:43], v[188:191]\n"
"s_add_u32 s55, s55, s59\n"
"v_exp_f32 v157, v157\n"
"v_mfma_f32_16x16x32_bf16 v[176:179], v[220:223], a[44:47], v[176:179]\n"
"s_add_u32 s41, s55, s57\n"
"v_exp_f32 v170, v170\n"
"v_mfma_f32_16x16x32_bf16 v[176:179], v[208:211], a[32:35], v[176:179]\n"
"s_and_b32 s41, s41, s58\n"
"v_exp_f32 v156, v156\n"
"v_mfma_f32_16x16x32_bf16 v[176:179], v[212:215], a[36:39], v[176:179]\n"
"s_andn2_b32 s56, s55, s58\n"
"v_exp_f32 v166, v166\n"
"v_mfma_f32_16x16x32_bf16 v[152:155], v[208:211], a[192:195], v[192:195]\n"
"s_or_b32 s56, s56, s41\n"
"ds_read_b128 v[208:211], v230 offset:49152\n"
"v_mfma_f32_16x16x32_bf16 v[152:155], v[212:215], a[196:199], v[152:155]\n"
"v_exp_f32 v174, v174\n"
"v_mfma_f32_16x16x32_bf16 v[152:155], v[216:219], a[200:203], v[152:155]\n"
"ds_read_b128 v[212:215], v231 offset:49152\n"
"v_mfma_f32_16x16x32_bf16 v[152:155], v[220:223], a[204:207], v[152:155]\n"
"s_waitcnt lgkmcnt(2)\n"
"ds_read_b128 v[216:219], v230 offset:50176\n"
"v_cvt_pk_bf16_f32 v105, v158, v159\n"
"v_mfma_f32_16x16x32_bf16 a[232:235], v[124:127], a[244:247], a[232:235]\n"
"v_exp_f32 v164, v164\n"
"v_mfma_f32_16x16x32_bf16 v[24:27], v[128:131], a[244:247], v[24:27]\n"
"s_add_u32 m0, s44, 16384\n"
"ds_read_b128 v[220:223], v231 offset:50176\n"
"v_mfma_f32_16x16x32_bf16 v[24:27], v[132:135], a[248:251], v[24:27]\n"
"buffer_load_dwordx4 v240, s[16:19], s56 offen nt lds\n"
"v_mfma_f32_16x16x32_bf16 v[56:59], v[136:139], a[244:247], v[56:59]\n"
"v_exp_f32 v167, v167\n"
"v_mfma_f32_16x16x32_bf16 v[56:59], v[140:143], a[248:251], v[56:59]\n"
"v_exp_f32 v176, v176\n"
"v_mfma_f32_16x16x32_bf16 v[88:91], v[144:147], a[244:247], v[88:91]\n"
"v_exp_f32 v153, v153\n"
"v_mfma_f32_16x16x32_bf16 v[88:91], v[148:151], a[248:251], v[88:91]\n"
"v_exp_f32 v173, v173\n"
"v_mfma_f32_16x16x32_bf16 v[92:95], v[148:151], v[224:227], v[92:95]\n"
"ds_read_b64_tr_b16 a[250:251], v236 offset:36864\n"
"v_cvt_pk_bf16_f32 v121, v166, v167\n"
"v_mfma_f32_16x16x32_bf16 v[92:95], v[144:147], a[252:255], v[92:95]\n"
"ds_read_b64_tr_b16 a[244:245], v232 offset:32768\n"
"v_cvt_pk_bf16_f32 v112, v160, v161\n"
"v_mfma_f32_16x16x32_bf16 v[60:63], v[140:143], v[224:227], v[60:63]\n"
"ds_read_b64_tr_b16 a[248:249], v236 offset:32768\n"
"v_cvt_pk_bf16_f32 v120, v164, v165\n"
"v_mfma_f32_16x16x32_bf16 v[60:63], v[136:139], a[252:255], v[60:63]\n"
"ds_read_b64_tr_b16 a[246:247], v232 offset:36864\n"
"v_cvt_pk_bf16_f32 v104, v156, v157\n"
"v_mfma_f32_16x16x32_bf16 v[28:31], v[132:135], v[224:227], v[28:31]\n"
"ds_read_b64_tr_b16 v[226:227], v237 offset:36864\n"
"v_cvt_pk_bf16_f32 v113, v162, v163\n"
"v_mfma_f32_16x16x32_bf16 v[28:31], v[128:131], a[252:255], v[28:31]\n"
"ds_read_b64_tr_b16 v[224:225], v237 offset:32768\n"
"v_mfma_f32_16x16x32_bf16 a[236:239], v[124:127], a[252:255], a[236:239]\n"
"s_add_u32 s40, s56, s52\n"
"ds_read_b64_tr_b16 a[252:253], v233 offset:32768\n"
"v_smfmac_f32_16x16x64_bf16 v[198:201], a[240:243], v[136:143], v241\n"
"s_waitcnt lgkmcnt(7)\n"
"buffer_load_dwordx4 v240, s[16:19], s40 offen offset:1024 nt lds\n"
"v_mfma_f32_16x16x32_bf16 v[156:159], v[208:211], a[0:3], v[180:183]\n"
"v_exp_f32 v169, v169\n"
"v_mfma_f32_16x16x32_bf16 v[156:159], v[212:215], a[4:7], v[156:159]\n"
"v_exp_f32 v168, v168\n"
"v_mfma_f32_16x16x32_bf16 v[156:159], v[216:219], a[8:11], v[156:159]\n"
"v_exp_f32 v175, v175\n"
"v_mfma_f32_16x16x32_bf16 v[156:159], v[220:223], a[12:15], v[156:159]\n"
"v_exp_f32 v172, v172\n"
"v_mfma_f32_16x16x32_bf16 v[160:163], v[216:219], a[24:27], v[184:187]\n"
"ds_read_b64_tr_b16 a[254:255], v233 offset:36864\n"
"v_mfma_f32_16x16x32_bf16 v[160:163], v[220:223], a[28:31], v[160:163]\n"
"v_exp_f32 v155, v155\n"
"v_mfma_f32_16x16x32_bf16 v[160:163], v[208:211], a[16:19], v[160:163]\n"
"v_exp_f32 v177, v177\n"
"v_mfma_f32_16x16x32_bf16 v[160:163], v[212:215], a[20:23], v[160:163]\n"
"v_exp_f32 v179, v179\n"
"v_mfma_f32_16x16x32_bf16 v[164:167], v[208:211], a[32:35], v[188:191]\n"
"ds_read_b128 v[208:211], v230 offset:53248\n"
"v_cvt_pk_bf16_f32 v107, v174, v175\n"
"v_mfma_f32_16x16x32_bf16 v[164:167], v[212:215], a[36:39], v[164:167]\n"
"v_exp_f32 v171, v171\n"
"v_mfma_f32_16x16x32_bf16 v[164:167], v[216:219], a[40:43], v[164:167]\n"
"ds_read_b128 v[212:215], v231 offset:53248\n"
"v_cvt_pk_bf16_f32 v106, v172, v173\n"
"v_mfma_f32_16x16x32_bf16 v[164:167], v[220:223], a[44:47], v[164:167]\n"
"s_waitcnt lgkmcnt(3)\n"
"ds_read_b128 v[220:223], v231 offset:54272\n"
"v_cvt_pk_bf16_f32 v98, v168, v169\n"
"v_mfma_f32_16x16x32_bf16 a[208:211], v[124:127], a[244:247], a[208:211]\n"
"v_exp_f32 v154, v154\n"
"v_mfma_f32_16x16x32_bf16 v[0:3], v[128:131], a[244:247], v[0:3]\n"
"s_add_u32 s40, s56, s53\n"
"ds_read_b128 v[216:219], v230 offset:54272\n"
"v_mfma_f32_16x16x32_bf16 v[0:3], v[132:135], a[248:251], v[0:3]\n"
"buffer_load_dwordx4 v240, s[16:19], s40 offen offset:2048 nt lds\n"
"v_mfma_f32_16x16x32_bf16 v[32:35], v[136:139], a[244:247], v[32:35]\n"
"v_exp_f32 v160, v160\n"
"v_mfma_f32_16x16x32_bf16 v[32:35], v[140:143], a[248:251], v[32:35]\n"
"v_exp_f32 v178, v178\n"
"v_mfma_f32_16x16x32_bf16 v[64:67], v[144:147], a[244:247], v[64:67]\n"
"v_exp_f32 v152, v152\n"
"v_mfma_f32_16x16x32_bf16 v[64:67], v[148:151], a[248:251], v[64:67]\n"
"v_exp_f32 v161, v161\n"
"v_mfma_f32_16x16x32_bf16 v[68:71], v[148:151], v[224:227], v[68:71]\n"
"s_waitcnt lgkmcnt(1)\n"
"ds_read_b64_tr_b16 a[244:245], v232 offset:33792\n"
"v_cvt_pk_bf16_f32 v115, v178, v179\n"
"v_mfma_f32_16x16x32_bf16 v[68:71], v[144:147], a[252:255], v[68:71]\n"
"ds_read_b64_tr_b16 a[248:249], v236 offset:33792\n"
"v_cvt_pk_bf16_f32 v114, v176, v177\n"
"v_mfma_f32_16x16x32_bf16 v[36:39], v[140:143], v[224:227], v[36:39]\n"
"ds_read_b64_tr_b16 a[250:251], v236 offset:37888\n"
"v_cvt_pk_bf16_f32 v99, v170, v171\n"
"v_mfma_f32_16x16x32_bf16 v[36:39], v[136:139], a[252:255], v[36:39]\n"
"v_exp_f32 v163, v163\n"
"v_mfma_f32_16x16x32_bf16 v[4:7], v[132:135], v[224:227], v[4:7]\n"
"s_add_u32 s40, s56, s54\n"
"ds_read_b64_tr_b16 a[246:247], v232 offset:37888\n"
"v_mfma_f32_16x16x32_bf16 v[4:7], v[128:131], a[252:255], v[4:7]\n"
"buffer_load_dwordx4 v240, s[16:19], s40 offen offset:3072 nt lds\n"
"v_mfma_f32_16x16x32_bf16 a[212:215], v[124:127], a[252:255], a[212:215]\n"
"v_exp_f32 v167, v167\n"
"v_smfmac_f32_16x16x64_bf16 v[200:203], a[240:243], v[144:151], v241\n"
"v_cvt_pk_bf16_f32 v122, v152, v153\n"
"v_mfma_f32_16x16x32_bf16 v[168:171], v[208:211], a[0:3], v[180:183]\n"
"v_exp_f32 v159, v159\n"
"v_mfma_f32_16x16x32_bf16 v[168:171], v[212:215], a[4:7], v[168:171]\n"
"s_waitcnt lgkmcnt(2)\n"
"v_exp_f32 v165, v165\n"
"v_mfma_f32_16x16x32_bf16 v[168:171], v[216:219], a[8:11], v[168:171]\n"
"ds_read_b64_tr_b16 a[252:253], v233 offset:33792\n"
"ds_read_b64_tr_b16 v[224:225], v237 offset:33792\n"
"v_mfma_f32_16x16x32_bf16 v[168:171], v[220:223], a[12:15], v[168:171]\n"
"ds_read_b64_tr_b16 a[254:255], v233 offset:37888\n"
"ds_read_b64_tr_b16 v[226:227], v237 offset:37888\n"
"v_mfma_f32_16x16x32_bf16 v[172:175], v[216:219], a[24:27], v[184:187]\n"
"v_exp_f32 v166, v166\n"
"v_mfma_f32_16x16x32_bf16 v[172:175], v[220:223], a[28:31], v[172:175]\n"
"v_exp_f32 v164, v164\n"
"v_mfma_f32_16x16x32_bf16 v[172:175], v[208:211], a[16:19], v[172:175]\n"
"v_exp_f32 v157, v157\n"
"v_mfma_f32_16x16x32_bf16 v[172:175], v[212:215], a[20:23], v[172:175]\n"
"s_cbranch_vccnz LPCL3_%=\nLPKL3_%=:\n"
"v_cvt_pk_bf16_f32 v123, v154, v155\n"
"v_mfma_f32_16x16x32_bf16 v[176:179], v[208:211], a[32:35], v[188:191]\n"
"s_add_u32 s47, s47, 1\n"
"v_exp_f32 v158, v158\n"
"v_mfma_f32_16x16x32_bf16 v[176:179], v[212:215], a[36:39], v[176:179]\n"
"v_exp_f32 v162, v162\n"
"v_mfma_f32_16x16x32_bf16 v[176:179], v[216:219], a[40:43], v[176:179]\n"
"s_and_b32 s40, s47, 3\n"
"v_exp_f32 v156, v156\n"
"v_mfma_f32_16x16x32_bf16 v[176:179], v[220:223], a[44:47], v[176:179]\n"
"s_waitcnt lgkmcnt(0)\n"
"v_cvt_pk_bf16_f32 v101, v158, v159\n"
"s_cbranch_scc1 LAL_%=\n"
"s_cmp_eq_u32 s47, 8\n"
"s_cbranch_scc1 LTH1_%=\n"
"s_cmp_eq_u32 s47, 12\n"
"s_cbranch_scc1 LTH2_%=\n"
"s_cmp_eq_u32 s47, 16\n"
"s_cbranch_scc1 LAX_%=\n"
"LTH0_%=:\n"
"v_mfma_f32_16x16x32_bf16 a[224:227], v[124:127], a[244:247], a[224:227]\n"
"v_exp_f32 v170, v170\n"
"v_mfma_f32_16x16x32_bf16 v[16:19], v[128:131], a[244:247], v[16:19]\n"
"s_waitcnt vmcnt(8)\n"
"v_exp_f32 v168, v168\n"
"v_mfma_f32_16x16x32_bf16 v[16:19], v[132:135], a[248:251], v[16:19]\n"
"s_barrier\n"
"v_mfma_f32_16x16x32_bf16 v[48:51], v[136:139], a[244:247], v[48:51]\n"
"v_exp_f32 v173, v173\n"
"v_mfma_f32_16x16x32_bf16 v[48:51], v[140:143], a[248:251], v[48:51]\n"
"v_exp_f32 v171, v171\n"
"v_mfma_f32_16x16x32_bf16 v[80:83], v[144:147], a[244:247], v[80:83]\n"
"ds_read_b64_tr_b16 a[244:245], v234 offset:49152\n"
"ds_read_b64_tr_b16 a[246:247], v234 offset:53248\n"
"v_mfma_f32_16x16x32_bf16 v[80:83], v[148:151], a[248:251], v[80:83]\n"
"v_exp_f32 v179, v179\n"
"v_mfma_f32_16x16x32_bf16 v[84:87], v[148:151], v[224:227], v[84:87]\n"
"ds_read_b64_tr_b16 a[250:251], v238 offset:53248\n"
"v_cvt_pk_bf16_f32 v100, v156, v157\n"
"v_mfma_f32_16x16x32_bf16 v[84:87], v[144:147], a[252:255], v[84:87]\n"
"v_exp_f32 v169, v169\n"
"v_exp_f32 v172, v172\n"
"v_exp_f32 v175, v175\n"
"v_exp_f32 v176, v176\n"
"v_exp_f32 v178, v178\n"
"v_mfma_f32_16x16x32_bf16 v[52:55], v[140:143], v[224:227], v[52:55]\n"
"ds_read_b64_tr_b16 a[248:249], v238 offset:49152\n"
"v_cvt_pk_bf16_f32 v117, v166, v167\n"
"v_mfma_f32_16x16x32_bf16 v[52:55], v[136:139], a[252:255], v[52:55]\n"
"v_exp_f32 v174, v174\n"
"v_mfma_f32_16x16x32_bf16 v[20:23], v[132:135], v[224:227], v[20:23]\n"
"ds_read_b64_tr_b16 v[224:225], v239 offset:49152\n"
"ds_read_b64_tr_b16 v[226:227], v239 offset:53248\n"
"v_mfma_f32_16x16x32_bf16 v[20:23], v[128:131], a[252:255], v[20:23]\n"
"v_cvt_pk_bf16_f32 v102, v168, v169\n"
"v_cvt_pk_bf16_f32 v103, v170, v171\n"
"v_cvt_pk_bf16_f32 v111, v174, v175\n"
"v_exp_f32 v177, v177\n"
"v_mfma_f32_16x16x32_bf16 a[228:231], v[124:127], a[252:255], a[228:231]\n"
"s_waitcnt lgkmcnt(4)\n"
"ds_read_b64_tr_b16 a[254:255], v235 offset:53248\n"
"v_cvt_pk_bf16_f32 v110, v172, v173\n"
"v_mfma_f32_16x16x32_bf16 a[216:219], v[120:123], a[244:247], a[216:219]\n"
"s_add_u32 m0, s44, 81920\n"
"ds_read_b64_tr_b16 a[252:253], v235 offset:49152\n"
"v_cvt_pk_bf16_f32 v116, v164, v165\n"
"v_smfmac_f32_16x16x64_bf16 v[196:199], a[240:243], v[96:103], v241\n"
"buffer_load_dwordx4 v240, s[20:23], s56 offen nt lds\n"
"v_mfma_f32_16x16x32_bf16 v[8:11], v[96:99], a[244:247], v[8:11]\n"
"s_waitcnt lgkmcnt(4)\n"
"v_cvt_pk_bf16_f32 v118, v176, v177\n"
"v_cvt_pk_bf16_f32 v119, v178, v179\n"
"v_mfma_f32_16x16x32_bf16 v[8:11], v[100:103], a[248:251], v[8:11]\n"
"v_cvt_pk_bf16_f32 v108, v160, v161\n"
"v_and_b32 v245, 0x3f0, v242\n"
"v_smfmac_f32_16x16x64_bf16 v[200:203], a[240:243], v[112:119], v241\n"
"ds_read_b128 v[212:215], v229 offset:0\n"
"v_cvt_pk_bf16_f32 v109, v162, v163\n"
"v_mfma_f32_16x16x32_bf16 v[40:43], v[104:107], a[244:247], v[40:43]\n"
"v_lshlrev_b32_e64 v244, 24, s43\n"
"v_lshrrev_b32 v245, 1, v245\n"
"v_mfma_f32_16x16x32_bf16 v[40:43], v[108:111], a[248:251], v[40:43]\n"
"ds_read_b128 v[208:211], v228 offset:0\n"
"v_sub_u32 v245, v242, v245\n"
"v_smfmac_f32_16x16x64_bf16 v[198:201], a[240:243], v[104:111], v241\n"
"ds_read_b128 v[220:223], v229 offset:1024\n"
"v_add_f32 v196, v196, v197\n"
"v_mfma_f32_16x16x32_bf16 v[72:75], v[112:115], a[244:247], v[72:75]\n"
"ds_read_b64_tr_b16 a[244:245], v234 offset:50176\n"
"v_mul_f32 v244, 0x45800000, v244\n"
"v_mfma_f32_16x16x32_bf16 v[72:75], v[116:119], a[248:251], v[72:75]\n"
"ds_read_b64_tr_b16 a[246:247], v234 offset:54272\n"
"ds_read_b64_tr_b16 a[250:251], v238 offset:54272\n"
"v_mfma_f32_16x16x32_bf16 v[202:205], a[240:243], v[120:123], v[202:205]\n"
"s_waitcnt lgkmcnt(6)\n"
"ds_read_b128 v[216:219], v228 offset:1024\n"
"v_add_f32 v198, v198, v199\n"
"v_mfma_f32_16x16x32_bf16 v[76:79], v[116:119], v[224:227], v[76:79]\n"
"ds_read_b64_tr_b16 a[248:249], v238 offset:50176\n"
"v_add_f32 v254, v196, v198\n"
"v_mfma_f32_16x16x32_bf16 v[76:79], v[112:115], a[252:255], v[76:79]\n"
"v_add_f32 v200, v200, v201\n"
"v_add_f32 v254, v254, v200\n"
"v_mfma_f32_16x16x32_bf16 v[44:47], v[108:111], v[224:227], v[44:47]\n"
"v_add_f32 v244, v244, v202\n"
"v_add_f32 v254, v254, v202\n"
"v_mfma_f32_16x16x32_bf16 v[44:47], v[104:107], a[252:255], v[44:47]\n"
"v_min3_f32 v244, v244, v196, v198\n"
"v_min_f32 v244, v244, v200\n"
"v_mfma_f32_16x16x32_bf16 v[12:15], v[100:103], v[224:227], v[12:15]\n"
"s_add_u32 s40, s56, s52\n"
"v_rcp_f32 v244, v244\n"
"v_mfma_f32_16x16x32_bf16 v[12:15], v[96:99], a[252:255], v[12:15]\n"
"buffer_load_dwordx4 v240, s[20:23], s40 offen offset:1024 nt lds\n"
"v_mfma_f32_16x16x32_bf16 a[220:223], v[120:123], a[252:255], a[220:223]\n"
"s_waitcnt lgkmcnt(2)\n"
"v_fmac_f32 v254, 0x68800000, v244\n"
"v_cmp_nge_f32 vcc, s45, v254\n"
"v_xor_b32 v244, 0x11000, v242\n"
"s_cbranch_vccnz LSLOW_%=\nLBACK0_%=:\ns_mov_b64 vcc, s[64:65]\n"
"v_mfma_f32_16x16x32_bf16 a[232:235], v[120:123], a[244:247], a[232:235]\n"
"v_accvgpr_mov_b32 a6, a54\n"
"v_accvgpr_mov_b32 a8, a56\n"
"v_accvgpr_mov_b32 a9, a57\n"
"v_accvgpr_mov_b32 a14, a62\n"
"v_accvgpr_mov_b32 a20, a68\n"
"v_accvgpr_mov_b32 a42, a90\n"
"v_accvgpr_mov_b32 a44, a92\n"
"v_xor_b32 v148, 0x400, v243\n"
"v_bfe_u32 v149, v242, 8, 2\n"
"v_lshl_add_u32 v149, v149, 4, s43\n"
"v_mfma_f32_16x16x32_bf16 v[24:27], v[96:99], a[244:247], v[24:27]\n"
"s_waitcnt lgkmcnt(0)\n"
"ds_read_b128 v[124:127], v149 offset:0\n"
"v_rcp_f32 v206, v200\n"
"v_accvgpr_mov_b32 a4, a52\n"
"v_accvgpr_mov_b32 a12, a60\n"
"v_accvgpr_mov_b32 a13, a61\n"
"v_accvgpr_mov_b32 a16, a64\n"
"v_accvgpr_mov_b32 a39, a87\n"
"v_accvgpr_mov_b32 a47, a95\n"
"v_xor_b32 v150, 64, v149\n"
"v_mfma_f32_16x16x32_bf16 v[24:27], v[100:103], a[248:251], v[24:27]\n"
"ds_read_b128 v[128:131], v150 offset:0\n"
"v_accvgpr_mov_b32 a0, a48\n"
"v_accvgpr_mov_b32 a1, a49\n"
"v_accvgpr_mov_b32 a5, a53\n"
"v_accvgpr_mov_b32 a10, a58\n"
"v_accvgpr_mov_b32 a17, a65\n"
"v_accvgpr_mov_b32 a40, a88\n"
"v_mfma_f32_16x16x32_bf16 v[56:59], v[104:107], a[244:247], v[56:59]\n"
"ds_read_b128 v[132:135], v149 offset:1024\n"
"v_rcp_f32 v246, v196\n"
"v_mov_b32_dpp v206, v206 row_ror:12 row_mask:0x2 bank_mask:0xf\n"
"v_accvgpr_mov_b32 a2, a50\n"
"v_accvgpr_mov_b32 a22, a70\n"
"v_accvgpr_mov_b32 a23, a71\n"
"v_accvgpr_mov_b32 a36, a84\n"
"v_accvgpr_mov_b32 a38, a86\n"
"v_accvgpr_mov_b32 a45, a93\n"
"v_mfma_f32_16x16x32_bf16 v[56:59], v[108:111], a[248:251], v[56:59]\n"
"ds_read_b128 v[136:139], v150 offset:1024\n"
"v_accvgpr_mov_b32 a33, a81\n"
"v_mfma_f32_16x16x32_bf16 v[88:91], v[112:115], a[244:247], v[88:91]\n"
"ds_read_b64_tr_b16 v[224:225], v239 offset:50176\n"
"v_accvgpr_mov_b32 a3, a51\n"
"v_accvgpr_mov_b32 a7, a55\n"
"v_accvgpr_mov_b32 a11, a59\n"
"v_accvgpr_mov_b32 a19, a67\n"
"v_accvgpr_mov_b32 a21, a69\n"
"v_accvgpr_mov_b32 a24, a72\n"
"v_accvgpr_mov_b32 a27, a75\n"
"v_accvgpr_mov_b32 a30, a78\n"
"v_accvgpr_mov_b32 a46, a94\n"
"v_mfma_f32_16x16x32_bf16 v[88:91], v[116:119], a[248:251], v[88:91]\n"
"ds_read_b128 a[204:207], v148 offset:6144\n"
"v_mov_b32_dpp v206, v206 row_ror:8 row_mask:0x4 bank_mask:0xf\n"
"v_accvgpr_mov_b32 a15, a63\n"
"v_accvgpr_mov_b32 a25, a73\n"
"v_accvgpr_mov_b32 a26, a74\n"
"v_accvgpr_mov_b32 a29, a77\n"
"v_accvgpr_mov_b32 a31, a79\n"
"v_accvgpr_mov_b32 a35, a83\n"
"v_accvgpr_mov_b32 a43, a91\n"
"s_waitcnt lgkmcnt(5)\n"
"v_mfma_f32_16x16x32_bf16 v[180:183], v[124:127], a[0:3], 0\n"
"v_mov_b32 v252, v192\n"
"s_waitcnt lgkmcnt(4)\n"
"v_mfma_f32_16x16x32_bf16 v[180:183], v[128:131], a[4:7], v[180:183]\n"
"s_waitcnt lgkmcnt(3)\n"
"ds_read_b128 a[192:195], v243 offset:4096\n"
"v_accvgpr_mov_b32 a18, a66\n"
"v_mfma_f32_16x16x32_bf16 v[180:183], v[132:135], a[8:11], v[180:183]\n"
"ds_read_b128 a[200:203], v243 offset:6144\n"
"s_waitcnt lgkmcnt(4)\n"
"v_mfma_f32_16x16x32_bf16 v[180:183], v[136:139], a[12:15], v[180:183]\n"
"v_rcp_f32 v247, v198\n"
"v_accvgpr_mov_b32 a28, a76\n"
"v_accvgpr_mov_b32 a34, a82\n"
"v_accvgpr_mov_b32 a41, a89\n"
"v_mfma_f32_16x16x32_bf16 v[184:187], v[124:127], a[16:19], 0\n"
"ds_read_b128 a[196:199], v148 offset:4096\n"
"v_mfma_f32_16x16x32_bf16 v[184:187], v[128:131], a[20:23], v[184:187]\n"
"v_mov_b32_dpp v247, v247 row_ror:12 row_mask:0x2 bank_mask:0xf\n"
"v_mfma_f32_16x16x32_bf16 v[184:187], v[132:135], a[24:27], v[184:187]\n"
"s_add_u32 s40, s56, s53\n"
"v_accvgpr_mov_b32 a32, a80\n"
"v_mfma_f32_16x16x32_bf16 v[184:187], v[136:139], a[28:31], v[184:187]\n"
"buffer_load_dwordx4 v240, s[20:23], s40 offen offset:2048 nt lds\n"
"v_mov_b32_dpp v206, v206 row_ror:4 row_mask:0x8 bank_mask:0xf\n"
"v_accvgpr_mov_b32 a37, a85\n"
"v_sub_f32 v180, s74, v180\n"
"v_sub_f32 v183, s74, v183\n"
"v_mfma_f32_16x16x32_bf16 v[188:191], v[124:127], a[32:35], 0\n"
"v_mov_b32_dpp v246, v246 row_ror:12 row_mask:0x2 bank_mask:0xf\n"
"v_mfma_f32_16x16x32_bf16 v[188:191], v[128:131], a[36:39], v[188:191]\n"
"v_mfma_f32_16x16x32_bf16 v[188:191], v[132:135], a[40:43], v[188:191]\n"
"v_mov_b32_dpp v246, v246 row_ror:8 row_mask:0x4 bank_mask:0xf\n"
"v_sub_f32 v182, s74, v182\n"
"v_mfma_f32_16x16x32_bf16 v[188:191], v[136:139], a[44:47], v[188:191]\n"
"ds_write_b128 v244, a[220:223] offset:33792\n"
"v_mov_b32_dpp v247, v247 row_ror:8 row_mask:0x4 bank_mask:0xf\n"
"s_waitcnt lgkmcnt(2)\n"
"v_mfma_f32_16x16x32_bf16 v[192:195], v[124:127], a[192:195], 0\n"
"v_sub_f32 v181, s74, v181\n"
"s_waitcnt lgkmcnt(1)\n"
"v_mfma_f32_16x16x32_bf16 v[192:195], v[128:131], a[196:199], v[192:195]\n"
"v_mfma_f32_16x16x32_bf16 v[192:195], v[132:135], a[200:203], v[192:195]\n"
"ds_read_b64_tr_b16 v[226:227], v239 offset:54272\n"
"ds_read_b64_tr_b16 a[246:247], v232 offset:53248\n"
"v_mfma_f32_16x16x32_bf16 v[192:195], v[136:139], a[204:207], v[192:195]\n"
"v_mov_b32_dpp v246, v246 row_ror:4 row_mask:0x8 bank_mask:0xf\n"
"v_mov_b32_dpp v247, v247 row_ror:4 row_mask:0x8 bank_mask:0xf\n"
"v_sub_f32 v184, s74, v184\n"
"v_sub_f32 v187, s74, v187\n"
"v_sub_f32 v190, s74, v190\n"
"v_mfma_f32_16x16x32_bf16 v[152:155], v[208:211], a[0:3], v[180:183]\n"
"v_mov_b32 v253, v202\n"
"v_sub_f32 v185, s74, v185\n"
"v_mfma_f32_16x16x32_bf16 v[152:155], v[212:215], a[4:7], v[152:155]\n"
"v_sub_f32 v186, s74, v186\n"
"v_mfma_f32_16x16x32_bf16 v[152:155], v[216:219], a[8:11], v[152:155]\n"
"s_add_u32 s40, s56, s54\n"
"ds_read_b64_tr_b16 a[254:255], v235 offset:54272\n"
"v_sub_f32 v189, s74, v189\n"
"v_mfma_f32_16x16x32_bf16 v[152:155], v[220:223], a[12:15], v[152:155]\n"
"buffer_load_dwordx4 v240, s[20:23], s40 offen offset:3072 nt lds\n"
"v_mul_f32_dpp v9, v246, v9 row_newbcast:1 row_mask:0xf bank_mask:0xf\n"
"v_mul_f32_dpp v12, v246, v12 row_newbcast:0 row_mask:0xf bank_mask:0xf\n"
"v_sub_f32 v188, s74, v188\n"
"v_sub_f32 v191, s74, v191\n"
"v_mfma_f32_16x16x32_bf16 v[156:159], v[216:219], a[24:27], v[184:187]\n"
"s_add_u32 s55, s55, s59\n"
"v_mfma_f32_16x16x32_bf16 v[156:159], v[220:223], a[28:31], v[156:159]\n"
"s_add_u32 s41, s55, s57\n"
"v_mfma_f32_16x16x32_bf16 v[156:159], v[208:211], a[16:19], v[156:159]\n"
"s_and_b32 s41, s41, s58\n"
"v_sub_f32 v192, s74, v192\n"
"v_mfma_f32_16x16x32_bf16 v[156:159], v[212:215], a[20:23], v[156:159]\n"
"s_andn2_b32 s56, s55, s58\n"
"ds_read_b64_tr_b16 a[250:251], v236 offset:53248\n"
"v_xor_b32 v250, 0x1000, v245\n"
"v_mul_f32_dpp v41, v247, v41 row_newbcast:1 row_mask:0xf bank_mask:0xf\n"
"v_mul_f32_dpp v46, v247, v46 row_newbcast:2 row_mask:0xf bank_mask:0xf\n"
"v_sub_f32 v195, s74, v195\n"
"v_exp_f32 v153, v153\n"
"v_mfma_f32_16x16x32_bf16 v[160:163], v[208:211], a[32:35], v[188:191]\n"
"s_or_b32 s56, s56, s41\n"
"v_mul_f32_dpp v43, v247, v43 row_newbcast:3 row_mask:0xf bank_mask:0xf\n"
"v_sub_f32 v193, s74, v193\n"
"v_mfma_f32_16x16x32_bf16 v[160:163], v[212:215], a[36:39], v[160:163]\n"
"v_mfma_f32_16x16x32_bf16 v[160:163], v[216:219], a[40:43], v[160:163]\n"
"v_sub_f32 v194, s74, v194\n"
"v_mfma_f32_16x16x32_bf16 v[160:163], v[220:223], a[44:47], v[160:163]\n"
"ds_read_b64_tr_b16 a[252:253], v235 offset:50176\n"
"v_exp_f32 v152, v152\n"
"v_exp_f32 v157, v157\n"
"v_exp_f32 v158, v158\n"
"v_mfma_f32_16x16x32_bf16 v[164:167], v[216:219], a[200:203], v[192:195]\n"
"ds_read_b128 v[216:219], v228 offset:5120\n"
"v_mfma_f32_16x16x32_bf16 v[164:167], v[220:223], a[204:207], v[164:167]\n"
"v_mul_f32_dpp v57, v247, v57 row_newbcast:1 row_mask:0xf bank_mask:0xf\n"
"v_mfma_f32_16x16x32_bf16 v[164:167], v[208:211], a[192:195], v[164:167]\n"
"ds_read_b128 v[208:211], v228 offset:4096\n"
"v_mfma_f32_16x16x32_bf16 v[164:167], v[212:215], a[196:199], v[164:167]\n"
"ds_read_b128 v[220:223], v229 offset:5120\n"
"v_mul_f32_dpp v10, v246, v10 row_newbcast:2 row_mask:0xf bank_mask:0xf\n"
"v_exp_f32 v154, v154\n"
"v_exp_f32 v155, v155\n"
"v_mul_f32_dpp v27, v246, v27 row_newbcast:3 row_mask:0xf bank_mask:0xf\n"
"v_mul_f32_dpp v58, v247, v58 row_newbcast:2 row_mask:0xf bank_mask:0xf\n"
"s_branch LTC_%=\n"
"LTH1_%=:\n"
"v_mfma_f32_16x16x32_bf16 a[224:227], v[124:127], a[244:247], a[224:227]\n"
"v_exp_f32 v170, v170\n"
"v_mfma_f32_16x16x32_bf16 v[16:19], v[128:131], a[244:247], v[16:19]\n"
"s_waitcnt vmcnt(8)\n"
"v_exp_f32 v168, v168\n"
"v_mfma_f32_16x16x32_bf16 v[16:19], v[132:135], a[248:251], v[16:19]\n"
"s_barrier\n"
"v_mfma_f32_16x16x32_bf16 v[48:51], v[136:139], a[244:247], v[48:51]\n"
"v_exp_f32 v173, v173\n"
"v_mfma_f32_16x16x32_bf16 v[48:51], v[140:143], a[248:251], v[48:51]\n"
"v_exp_f32 v171, v171\n"
"v_mfma_f32_16x16x32_bf16 v[80:83], v[144:147], a[244:247], v[80:83]\n"
"ds_read_b64_tr_b16 a[244:245], v234 offset:49152\n"
"ds_read_b64_tr_b16 a[246:247], v234 offset:53248\n"
"v_mfma_f32_16x16x32_bf16 v[80:83], v[148:151], a[248:251], v[80:83]\n"
"v_exp_f32 v179, v179\n"
"v_mfma_f32_16x16x32_bf16 v[84:87], v[148:151], v[224:227], v[84:87]\n"
"ds_read_b64_tr_b16 a[250:251], v238 offset:53248\n"
"v_cvt_pk_bf16_f32 v100, v156, v157\n"
"v_mfma_f32_16x16x32_bf16 v[84:87], v[144:147], a[252:255], v[84:87]\n"
"v_exp_f32 v169, v169\n"
"v_exp_f32 v172, v172\n"
"v_exp_f32 v175, v175\n"
"v_exp_f32 v176, v176\n"
"v_exp_f32 v178, v178\n"
"v_mfma_f32_16x16x32_bf16 v[52:55], v[140:143], v[224:227], v[52:55]\n"
"ds_read_b64_tr_b16 a[248:249], v238 offset:49152\n"
"v_cvt_pk_bf16_f32 v117, v166, v167\n"
"v_mfma_f32_16x16x32_bf16 v[52:55], v[136:139], a[252:255], v[52:55]\n"
"v_exp_f32 v174, v174\n"
"v_mfma_f32_16x16x32_bf16 v[20:23], v[132:135], v[224:227], v[20:23]\n"
"ds_read_b64_tr_b16 v[224:225], v239 offset:49152\n"
"ds_read_b64_tr_b16 v[226:227], v239 offset:53248\n"
"v_mfma_f32_16x16x32_bf16 v[20:23], v[128:131], a[252:255], v[20:23]\n"
"v_cvt_pk_bf16_f32 v102, v168, v169\n"
"v_cvt_pk_bf16_f32 v103, v170, v171\n"
"v_cvt_pk_bf16_f32 v111, v174, v175\n"
"v_exp_f32 v177, v177\n"
"v_mfma_f32_16x16x32_bf16 a[228:231], v[124:127], a[252:255], a[228:231]\n"
"s_waitcnt lgkmcnt(4)\n"
"ds_read_b64_tr_b16 a[254:255], v235 offset:53248\n"
"v_cvt_pk_bf16_f32 v110, v172, v173\n"
"v_mfma_f32_16x16x32_bf16 a[216:219], v[120:123], a[244:247], a[216:219]\n"
"s_add_u32 m0, s44, 81920\n"
"ds_read_b64_tr_b16 a[252:253], v235 offset:49152\n"
"v_cvt_pk_bf16_f32 v116, v164, v165\n"
"v_smfmac_f32_16x16x64_bf16 v[196:199], a[240:243], v[96:103], v241\n"
"buffer_load_dwordx4 v240, s[20:23], s56 offen nt lds\n"
"v_mfma_f32_16x16x32_bf16 v[8:11], v[96:99], a[244:247], v[8:11]\n"
"s_waitcnt lgkmcnt(4)\n"
"v_cvt_pk_bf16_f32 v118, v176, v177\n"
"v_cvt_pk_bf16_f32 v119, v178, v179\n"
"v_mfma_f32_16x16x32_bf16 v[8:11], v[100:103], a[248:251], v[8:11]\n"
"v_cvt_pk_bf16_f32 v108, v160, v161\n"
"v_and_b32 v245, 0x3f0, v242\n"
"v_smfmac_f32_16x16x64_bf16 v[200:203], a[240:243], v[112:119], v241\n"
"ds_read_b128 v[212:215], v229 offset:0\n"
"v_cvt_pk_bf16_f32 v109, v162, v163\n"
"v_mfma_f32_16x16x32_bf16 v[40:43], v[104:107], a[244:247], v[40:43]\n"
"v_lshlrev_b32_e64 v244, 24, s43\n"
"v_lshrrev_b32 v245, 1, v245\n"
"v_mfma_f32_16x16x32_bf16 v[40:43], v[108:111], a[248:251], v[40:43]\n"
"ds_read_b128 v[208:211], v228 offset:0\n"
"v_sub_u32 v245, v242, v245\n"
"v_smfmac_f32_16x16x64_bf16 v[198:201], a[240:243], v[104:111], v241\n"
"ds_read_b128 v[220:223], v229 offset:1024\n"
"v_add_f32 v196, v196, v197\n"
"v_mfma_f32_16x16x32_bf16 v[72:75], v[112:115], a[244:247], v[72:75]\n"
"ds_read_b64_tr_b16 a[244:245], v234 offset:50176\n"
"v_mul_f32 v244, 0x45800000, v244\n"
"v_mfma_f32_16x16x32_bf16 v[72:75], v[116:119], a[248:251], v[72:75]\n"
"ds_read_b64_tr_b16 a[246:247], v234 offset:54272\n"
"ds_read_b64_tr_b16 a[250:251], v238 offset:54272\n"
"v_mfma_f32_16x16x32_bf16 v[202:205], a[240:243], v[120:123], v[202:205]\n"
"s_waitcnt lgkmcnt(6)\n"
"ds_read_b128 v[216:219], v228 offset:1024\n"
"v_add_f32 v198, v198, v199\n"
"v_mfma_f32_16x16x32_bf16 v[76:79], v[116:119], v[224:227], v[76:79]\n"
"ds_read_b64_tr_b16 a[248:249], v238 offset:50176\n"
"v_add_f32 v254, v196, v198\n"
"v_mfma_f32_16x16x32_bf16 v[76:79], v[112:115], a[252:255], v[76:79]\n"
"v_add_f32 v200, v200, v201\n"
"v_add_f32 v254, v254, v200\n"
"v_mfma_f32_16x16x32_bf16 v[44:47], v[108:111], v[224:227], v[44:47]\n"
"v_add_f32 v244, v244, v202\n"
"v_add_f32 v254, v254, v202\n"
"v_mfma_f32_16x16x32_bf16 v[44:47], v[104:107], a[252:255], v[44:47]\n"
"v_min3_f32 v244, v244, v196, v198\n"
"v_min_f32 v244, v244, v200\n"
"v_mfma_f32_16x16x32_bf16 v[12:15], v[100:103], v[224:227], v[12:15]\n"
"s_add_u32 s40, s56, s52\n"
"v_rcp_f32 v244, v244\n"
"v_mfma_f32_16x16x32_bf16 v[12:15], v[96:99], a[252:255], v[12:15]\n"
"buffer_load_dwordx4 v240, s[20:23], s40 offen offset:1024 nt lds\n"
"v_mfma_f32_16x16x32_bf16 a[220:223], v[120:123], a[252:255], a[220:223]\n"
"s_waitcnt lgkmcnt(2)\n"
"v_fmac_f32 v254, 0x68800000, v244\n"
"v_cmp_nge_f32 vcc, s45, v254\n"
"v_xor_b32 v244, 0x11000, v242\n"
"s_cbranch_vccnz LSLOW_%=\nLBACK1_%=:\ns_mov_b64 vcc, s[64:65]\n"
"v_mfma_f32_16x16x32_bf16 a[232:235], v[120:123], a[244:247], a[232:235]\n"
"v_accvgpr_mov_b32 a6, a102\n"
"v_accvgpr_mov_b32 a8, a104\n"
"v_accvgpr_mov_b32 a9, a105\n"
"v_accvgpr_mov_b32 a14, a110\n"
"v_accvgpr_mov_b32 a20, a116\n"
"v_accvgpr_mov_b32 a42, a138\n"
"v_accvgpr_mov_b32 a44, a140\n"
"v_xor_b32 v148, 0x400, v243\n"
"v_bfe_u32 v149, v242, 8, 2\n"
"v_lshl_add_u32 v149, v149, 4, s43\n"
"v_mfma_f32_16x16x32_bf16 v[24:27], v[96:99], a[244:247], v[24:27]\n"
"s_waitcnt lgkmcnt(0)\n"
"ds_read_b128 v[124:127], v149 offset:0\n"
"v_rcp_f32 v206, v200\n"
"v_accvgpr_mov_b32 a4, a100\n"
"v_accvgpr_mov_b32 a12, a108\n"
"v_accvgpr_mov_b32 a13, a109\n"
"v_accvgpr_mov_b32 a16, a112\n"
"v_accvgpr_mov_b32 a39, a135\n"
"v_accvgpr_mov_b32 a47, a143\n"
"v_xor_b32 v150, 64, v149\n"
"v_mfma_f32_16x16x32_bf16 v[24:27], v[100:103], a[248:251], v[24:27]\n"
"ds_read_b128 v[128:131], v150 offset:0\n"
"v_accvgpr_mov_b32 a0, a96\n"
"v_accvgpr_mov_b32 a1, a97\n"
"v_accvgpr_mov_b32 a5, a101\n"
"v_accvgpr_mov_b32 a10, a106\n"
"v_accvgpr_mov_b32 a17, a113\n"
"v_accvgpr_mov_b32 a40, a136\n"
"v_mfma_f32_16x16x32_bf16 v[56:59], v[104:107], a[244:247], v[56:59]\n"
"ds_read_b128 v[132:135], v149 offset:1024\n"
"v_rcp_f32 v246, v196\n"
"v_mov_b32_dpp v206, v206 row_ror:12 row_mask:0x2 bank_mask:0xf\n"
"v_accvgpr_mov_b32 a2, a98\n"
"v_accvgpr_mov_b32 a22, a118\n"
"v_accvgpr_mov_b32 a23, a119\n"
"v_accvgpr_mov_b32 a36, a132\n"
"v_accvgpr_mov_b32 a38, a134\n"
"v_accvgpr_mov_b32 a45, a141\n"
"v_mfma_f32_16x16x32_bf16 v[56:59], v[108:111], a[248:251], v[56:59]\n"
"ds_read_b128 v[136:139], v150 offset:1024\n"
"v_accvgpr_mov_b32 a33, a129\n"
"v_mfma_f32_16x16x32_bf16 v[88:91], v[112:115], a[244:247], v[88:91]\n"
"ds_read_b64_tr_b16 v[224:225], v239 offset:50176\n"
"v_accvgpr_mov_b32 a3, a99\n"
"v_accvgpr_mov_b32 a7, a103\n"
"v_accvgpr_mov_b32 a11, a107\n"
"v_accvgpr_mov_b32 a19, a115\n"
"v_accvgpr_mov_b32 a21, a117\n"
"v_accvgpr_mov_b32 a24, a120\n"
"v_accvgpr_mov_b32 a27, a123\n"
"v_accvgpr_mov_b32 a30, a126\n"
"v_accvgpr_mov_b32 a46, a142\n"
"v_mfma_f32_16x16x32_bf16 v[88:91], v[116:119], a[248:251], v[88:91]\n"
"s_waitcnt lgkmcnt(4)\n"
"ds_read_b128 a[204:207], v148 offset:6144\n"
"v_mov_b32_dpp v206, v206 row_ror:8 row_mask:0x4 bank_mask:0xf\n"
"v_accvgpr_mov_b32 a15, a111\n"
"v_accvgpr_mov_b32 a25, a121\n"
"v_accvgpr_mov_b32 a26, a122\n"
"v_accvgpr_mov_b32 a29, a125\n"
"v_accvgpr_mov_b32 a31, a127\n"
"v_accvgpr_mov_b32 a35, a131\n"
"v_accvgpr_mov_b32 a43, a139\n"
"v_accvgpr_mov_b32 a112, a64\n"
"v_mfma_f32_16x16x32_bf16 v[180:183], v[124:127], a[0:3], 0\n"
"s_waitcnt lgkmcnt(4)\n"
"v_mov_b32 v252, v192\n"
"v_accvgpr_mov_b32 a126, a78\n"
"v_mfma_f32_16x16x32_bf16 v[180:183], v[128:131], a[4:7], v[180:183]\n"
"s_waitcnt lgkmcnt(3)\n"
"ds_read_b128 a[192:195], v243 offset:4096\n"
"v_accvgpr_mov_b32 a18, a114\n"
"v_mfma_f32_16x16x32_bf16 v[180:183], v[132:135], a[8:11], v[180:183]\n"
"s_waitcnt lgkmcnt(3)\n"
"ds_read_b128 a[200:203], v243 offset:6144\n"
"v_accvgpr_mov_b32 a135, a87\n"
"v_mfma_f32_16x16x32_bf16 v[180:183], v[136:139], a[12:15], v[180:183]\n"
"v_rcp_f32 v247, v198\n"
"v_accvgpr_mov_b32 a28, a124\n"
"v_accvgpr_mov_b32 a34, a130\n"
"v_accvgpr_mov_b32 a41, a137\n"
"v_accvgpr_mov_b32 a103, a55\n"
"v_accvgpr_mov_b32 a119, a71\n"
"v_accvgpr_mov_b32 a108, a60\n"
"v_accvgpr_mov_b32 a130, a82\n"
"v_mfma_f32_16x16x32_bf16 v[184:187], v[124:127], a[16:19], 0\n"
"ds_read_b128 a[196:199], v148 offset:4096\n"
"v_accvgpr_mov_b32 a110, a62\n"
"v_mfma_f32_16x16x32_bf16 v[184:187], v[128:131], a[20:23], v[184:187]\n"
"v_mov_b32_dpp v247, v247 row_ror:12 row_mask:0x2 bank_mask:0xf\n"
"v_accvgpr_mov_b32 a104, a56\n"
"v_mfma_f32_16x16x32_bf16 v[184:187], v[132:135], a[24:27], v[184:187]\n"
"s_add_u32 s40, s56, s53\n"
"v_accvgpr_mov_b32 a32, a128\n"
"v_accvgpr_mov_b32 a109, a61\n"
"v_mfma_f32_16x16x32_bf16 v[184:187], v[136:139], a[28:31], v[184:187]\n"
"buffer_load_dwordx4 v240, s[20:23], s40 offen offset:2048 nt lds\n"
"v_mov_b32_dpp v206, v206 row_ror:4 row_mask:0x8 bank_mask:0xf\n"
"v_accvgpr_mov_b32 a37, a133\n"
"v_accvgpr_mov_b32 a101, a53\n"
"v_accvgpr_mov_b32 a102, a54\n"
"v_accvgpr_mov_b32 a133, a85\n"
"v_accvgpr_mov_b32 a105, a57\n"
"v_sub_f32 v180, s74, v180\n"
"v_sub_f32 v183, s74, v183\n"
"v_mfma_f32_16x16x32_bf16 v[188:191], v[124:127], a[32:35], 0\n"
"v_mov_b32_dpp v246, v246 row_ror:12 row_mask:0x2 bank_mask:0xf\n"
"v_accvgpr_mov_b32 a132, a84\n"
"v_mfma_f32_16x16x32_bf16 v[188:191], v[128:131], a[36:39], v[188:191]\n"
"v_accvgpr_mov_b32 a114, a66\n"
"v_accvgpr_mov_b32 a131, a83\n"
"v_mfma_f32_16x16x32_bf16 v[188:191], v[132:135], a[40:43], v[188:191]\n"
"v_mov_b32_dpp v246, v246 row_ror:8 row_mask:0x4 bank_mask:0xf\n"
"v_sub_f32 v182, s74, v182\n"
"v_mfma_f32_16x16x32_bf16 v[188:191], v[136:139], a[44:47], v[188:191]\n"
"s_waitcnt lgkmcnt(1)\n"
"ds_write_b128 v244, a[220:223] offset:33792\n"
"v_mov_b32_dpp v247, v247 row_ror:8 row_mask:0x4 bank_mask:0xf\n"
"v_accvgpr_mov_b32 a115, a67\n"
"v_accvgpr_mov_b32 a129, a81\n"
"v_accvgpr_mov_b32 a106, a58\n"
"v_mfma_f32_16x16x32_bf16 v[192:195], v[124:127], a[192:195], 0\n"
"s_waitcnt lgkmcnt(1)\n"
"v_accvgpr_mov_b32 a116, a68\n"
"v_sub_f32 v181, s74, v181\n"
"v_mfma_f32_16x16x32_bf16 v[192:195], v[128:131], a[196:199], v[192:195]\n"
"v_accvgpr_mov_b32 a125, a77\n"
"v_accvgpr_mov_b32 a96, a48\n"
"v_mfma_f32_16x16x32_bf16 v[192:195], v[132:135], a[200:203], v[192:195]\n"
"ds_read_b64_tr_b16 v[226:227], v239 offset:54272\n"
"ds_read_b64_tr_b16 a[246:247], v232 offset:53248\n"
"v_mfma_f32_16x16x32_bf16 v[192:195], v[136:139], a[204:207], v[192:195]\n"
"v_mov_b32_dpp v246, v246 row_ror:4 row_mask:0x8 bank_mask:0xf\n"
"v_mov_b32_dpp v247, v247 row_ror:4 row_mask:0x8 bank_mask:0xf\n"
"v_accvgpr_mov_b32 a100, a52\n"
"v_accvgpr_mov_b32 a117, a69\n"
"v_accvgpr_mov_b32 a127, a79\n"
"v_accvgpr_mov_b32 a128, a80\n"
"v_sub_f32 v184, s74, v184\n"
"v_sub_f32 v187, s74, v187\n"
"v_sub_f32 v190, s74, v190\n"
"v_mfma_f32_16x16x32_bf16 v[152:155], v[208:211], a[0:3], v[180:183]\n"
"v_mov_b32 v253, v202\n"
"v_sub_f32 v185, s74, v185\n"
"v_mfma_f32_16x16x32_bf16 v[152:155], v[212:215], a[4:7], v[152:155]\n"
"v_accvgpr_mov_b32 a138, a90\n"
"v_sub_f32 v186, s74, v186\n"
"v_mfma_f32_16x16x32_bf16 v[152:155], v[216:219], a[8:11], v[152:155]\n"
"s_add_u32 s40, s56, s54\n"
"ds_read_b64_tr_b16 a[254:255], v235 offset:54272\n"
"v_sub_f32 v189, s74, v189\n"
"v_mfma_f32_16x16x32_bf16 v[152:155], v[220:223], a[12:15], v[152:155]\n"
"buffer_load_dwordx4 v240, s[20:23], s40 offen offset:3072 nt lds\n"
"v_accvgpr_mov_b32 a142, a94\n"
"v_accvgpr_mov_b32 a143, a95\n"
"v_accvgpr_mov_b32 a120, a72\n"
"v_mul_f32_dpp v9, v246, v9 row_newbcast:1 row_mask:0xf bank_mask:0xf\n"
"v_mul_f32_dpp v12, v246, v12 row_newbcast:0 row_mask:0xf bank_mask:0xf\n"
"v_sub_f32 v188, s74, v188\n"
"v_sub_f32 v191, s74, v191\n"
"v_mfma_f32_16x16x32_bf16 v[156:159], v[216:219], a[24:27], v[184:187]\n"
"s_add_u32 s55, s55, s59\n"
"v_accvgpr_mov_b32 a111, a63\n"
"v_accvgpr_mov_b32 a107, a59\n"
"v_mfma_f32_16x16x32_bf16 v[156:159], v[220:223], a[28:31], v[156:159]\n"
"s_add_u32 s41, s55, s57\n"
"v_accvgpr_mov_b32 a97, a49\n"
"v_accvgpr_mov_b32 a122, a74\n"
"v_mfma_f32_16x16x32_bf16 v[156:159], v[208:211], a[16:19], v[156:159]\n"
"s_and_b32 s41, s41, s58\n"
"v_accvgpr_mov_b32 a99, a51\n"
"v_sub_f32 v192, s74, v192\n"
"v_mfma_f32_16x16x32_bf16 v[156:159], v[212:215], a[20:23], v[156:159]\n"
"s_andn2_b32 s56, s55, s58\n"
"ds_read_b64_tr_b16 a[250:251], v236 offset:53248\n"
"v_xor_b32 v250, 0x1000, v245\n"
"v_accvgpr_mov_b32 a98, a50\n"
"v_accvgpr_mov_b32 a113, a65\n"
"v_accvgpr_mov_b32 a139, a91\n"
"v_mul_f32_dpp v41, v247, v41 row_newbcast:1 row_mask:0xf bank_mask:0xf\n"
"v_mul_f32_dpp v46, v247, v46 row_newbcast:2 row_mask:0xf bank_mask:0xf\n"
"v_sub_f32 v195, s74, v195\n"
"v_exp_f32 v153, v153\n"
"v_mfma_f32_16x16x32_bf16 v[160:163], v[208:211], a[32:35], v[188:191]\n"
"s_or_b32 s56, s56, s41\n"
"v_mul_f32_dpp v43, v247, v43 row_newbcast:3 row_mask:0xf bank_mask:0xf\n"
"v_sub_f32 v193, s74, v193\n"
"v_mfma_f32_16x16x32_bf16 v[160:163], v[212:215], a[36:39], v[160:163]\n"
"v_accvgpr_mov_b32 a123, a75\n"
"v_accvgpr_mov_b32 a137, a89\n"
"v_mfma_f32_16x16x32_bf16 v[160:163], v[216:219], a[40:43], v[160:163]\n"
"v_accvgpr_mov_b32 a134, a86\n"
"v_sub_f32 v194, s74, v194\n"
"v_mfma_f32_16x16x32_bf16 v[160:163], v[220:223], a[44:47], v[160:163]\n"
"ds_read_b64_tr_b16 a[252:253], v235 offset:50176\n"
"v_accvgpr_mov_b32 a124, a76\n"
"v_accvgpr_mov_b32 a136, a88\n"
"v_exp_f32 v152, v152\n"
"v_exp_f32 v157, v157\n"
"v_exp_f32 v158, v158\n"
"v_mfma_f32_16x16x32_bf16 v[164:167], v[216:219], a[200:203], v[192:195]\n"
"ds_read_b128 v[216:219], v228 offset:5120\n"
"v_accvgpr_mov_b32 a118, a70\n"
"v_mfma_f32_16x16x32_bf16 v[164:167], v[220:223], a[204:207], v[164:167]\n"
"v_accvgpr_mov_b32 a140, a92\n"
"v_mul_f32_dpp v57, v247, v57 row_newbcast:1 row_mask:0xf bank_mask:0xf\n"
"v_mfma_f32_16x16x32_bf16 v[164:167], v[208:211], a[192:195], v[164:167]\n"
"ds_read_b128 v[208:211], v228 offset:4096\n"
"v_accvgpr_mov_b32 a141, a93\n"
"v_mfma_f32_16x16x32_bf16 v[164:167], v[212:215], a[196:199], v[164:167]\n"
"s_waitcnt lgkmcnt(3)\n"
"ds_read_b128 v[220:223], v229 offset:5120\n"
"v_accvgpr_mov_b32 a121, a73\n"
"v_mul_f32_dpp v10, v246, v10 row_newbcast:2 row_mask:0xf bank_mask:0xf\n"
"v_exp_f32 v154, v154\n"
"v_exp_f32 v155, v155\n"
"v_mul_f32_dpp v27, v246, v27 row_newbcast:3 row_mask:0xf bank_mask:0xf\n"
"v_mul_f32_dpp v58, v247, v58 row_newbcast:2 row_mask:0xf bank_mask:0xf\n"
"s_branch LTC_%=\n"
"LTH2_%=:\n"
"v_mfma_f32_16x16x32_bf16 a[224:227], v[124:127], a[244:247], a[224:227]\n"
"v_exp_f32 v170, v170\n"
"v_mfma_f32_16x16x32_bf16 v[16:19], v[128:131], a[244:247], v[16:19]\n"
"s_waitcnt vmcnt(8)\n"
"v_exp_f32 v168, v168\n"
"v_mfma_f32_16x16x32_bf16 v[16:19], v[132:135], a[248:251], v[16:19]\n"
"s_barrier\n"
"v_mfma_f32_16x16x32_bf16 v[48:51], v[136:139], a[244:247], v[48:51]\n"
"v_exp_f32 v173, v173\n"
"v_mfma_f32_16x16x32_bf16 v[48:51], v[140:143], a[248:251], v[48:51]\n"
"v_exp_f32 v171, v171\n"
"v_mfma_f32_16x16x32_bf16 v[80:83], v[144:147], a[244:247], v[80:83]\n"
"ds_read_b64_tr_b16 a[244:245], v234 offset:49152\n"
"ds_read_b64_tr_b16 a[246:247], v234 offset:53248\n"
"v_mfma_f32_16x16x32_bf16 v[80:83], v[148:151], a[248:251], v[80:83]\n"
"v_exp_f32 v179, v179\n"
"v_mfma_f32_16x16x32_bf16 v[84:87], v[148:151], v[224:227], v[84:87]\n"
"ds_read_b64_tr_b16 a[250:251], v238 offset:53248\n"
"v_cvt_pk_bf16_f32 v100, v156, v157\n"
"v_mfma_f32_16x16x32_bf16 v[84:87], v[144:147], a[252:255], v[84:87]\n"
"v_exp_f32 v169, v169\n"
"v_exp_f32 v172, v172\n"
"v_exp_f32 v175, v175\n"
"v_exp_f32 v176, v176\n"
"v_exp_f32 v178, v178\n"
"v_mfma_f32_16x16x32_bf16 v[52:55], v[140:143], v[224:227], v[52:55]\n"
"ds_read_b64_tr_b16 a[248:249], v238 offset:49152\n"
"v_cvt_pk_bf16_f32 v117, v166, v167\n"
"v_mfma_f32_16x16x32_bf16 v[52:55], v[136:139], a[252:255], v[52:55]\n"
"v_exp_f32 v174, v174\n"
"v_mfma_f32_16x16x32_bf16 v[20:23], v[132:135], v[224:227], v[20:23]\n"
"ds_read_b64_tr_b16 v[224:225], v239 offset:49152\n"
"ds_read_b64_tr_b16 v[226:227], v239 offset:53248\n"
"v_mfma_f32_16x16x32_bf16 v[20:23], v[128:131], a[252:255], v[20:23]\n"
"v_cvt_pk_bf16_f32 v102, v168, v169\n"
"v_cvt_pk_bf16_f32 v103, v170, v171\n"
"v_cvt_pk_bf16_f32 v111, v174, v175\n"
"v_exp_f32 v177, v177\n"
"v_mfma_f32_16x16x32_bf16 a[228:231], v[124:127], a[252:255], a[228:231]\n"
"s_waitcnt lgkmcnt(4)\n"
"ds_read_b64_tr_b16 a[254:255], v235 offset:53248\n"
"v_cvt_pk_bf16_f32 v110, v172, v173\n"
"v_mfma_f32_16x16x32_bf16 a[216:219], v[120:123], a[244:247], a[216:219]\n"
"s_add_u32 m0, s44, 81920\n"
"ds_read_b64_tr_b16 a[252:253], v235 offset:49152\n"
"v_cvt_pk_bf16_f32 v116, v164, v165\n"
"v_smfmac_f32_16x16x64_bf16 v[196:199], a[240:243], v[96:103], v241\n"
"buffer_load_dwordx4 v240, s[20:23], s56 offen nt lds\n"
"v_mfma_f32_16x16x32_bf16 v[8:11], v[96:99], a[244:247], v[8:11]\n"
"s_waitcnt lgkmcnt(4)\n"
"v_cvt_pk_bf16_f32 v118, v176, v177\n"
"v_cvt_pk_bf16_f32 v119, v178, v179\n"
"v_mfma_f32_16x16x32_bf16 v[8:11], v[100:103], a[248:251], v[8:11]\n"
"v_cvt_pk_bf16_f32 v108, v160, v161\n"
"v_and_b32 v245, 0x3f0, v242\n"
"v_smfmac_f32_16x16x64_bf16 v[200:203], a[240:243], v[112:119], v241\n"
"ds_read_b128 v[212:215], v229 offset:0\n"
"v_cvt_pk_bf16_f32 v109, v162, v163\n"
"v_mfma_f32_16x16x32_bf16 v[40:43], v[104:107], a[244:247], v[40:43]\n"
"v_lshlrev_b32_e64 v244, 24, s43\n"
"v_lshrrev_b32 v245, 1, v245\n"
"v_mfma_f32_16x16x32_bf16 v[40:43], v[108:111], a[248:251], v[40:43]\n"
"ds_read_b128 v[208:211], v228 offset:0\n"
"v_sub_u32 v245, v242, v245\n"
"v_smfmac_f32_16x16x64_bf16 v[198:201], a[240:243], v[104:111], v241\n"
"ds_read_b128 v[220:223], v229 offset:1024\n"
"v_add_f32 v196, v196, v197\n"
"v_mfma_f32_16x16x32_bf16 v[72:75], v[112:115], a[244:247], v[72:75]\n"
"ds_read_b64_tr_b16 a[244:245], v234 offset:50176\n"
"v_mul_f32 v244, 0x45800000, v244\n"
"v_mfma_f32_16x16x32_bf16 v[72:75], v[116:119], a[248:251], v[72:75]\n"
"ds_read_b64_tr_b16 a[246:247], v234 offset:54272\n"
"ds_read_b64_tr_b16 a[250:251], v238 offset:54272\n"
"v_mfma_f32_16x16x32_bf16 v[202:205], a[240:243], v[120:123], v[202:205]\n"
"s_waitcnt lgkmcnt(6)\n"
"ds_read_b128 v[216:219], v228 offset:1024\n"
"v_add_f32 v198, v198, v199\n"
"v_mfma_f32_16x16x32_bf16 v[76:79], v[116:119], v[224:227], v[76:79]\n"
"ds_read_b64_tr_b16 a[248:249], v238 offset:50176\n"
"v_add_f32 v254, v196, v198\n"
"v_mfma_f32_16x16x32_bf16 v[76:79], v[112:115], a[252:255], v[76:79]\n"
"v_add_f32 v200, v200, v201\n"
"v_add_f32 v254, v254, v200\n"
"v_mfma_f32_16x16x32_bf16 v[44:47], v[108:111], v[224:227], v[44:47]\n"
"v_add_f32 v244, v244, v202\n"
"v_add_f32 v254, v254, v202\n"
"v_mfma_f32_16x16x32_bf16 v[44:47], v[104:107], a[252:255], v[44:47]\n"
"v_min3_f32 v244, v244, v196, v198\n"
"v_min_f32 v244, v244, v200\n"
"v_mfma_f32_16x16x32_bf16 v[12:15], v[100:103], v[224:227], v[12:15]\n"
"s_add_u32 s40, s56, s52\n"
"v_rcp_f32 v244, v244\n"
"v_mfma_f32_16x16x32_bf16 v[12:15], v[96:99], a[252:255], v[12:15]\n"
"buffer_load_dwordx4 v240, s[20:23], s40 offen offset:1024 nt lds\n"
"v_mfma_f32_16x16x32_bf16 a[220:223], v[120:123], a[252:255], a[220:223]\n"
"s_waitcnt lgkmcnt(2)\n"
"v_fmac_f32 v254, 0x68800000, v244\n"
"v_cmp_nge_f32 vcc, s45, v254\n"
"v_xor_b32 v244, 0x11000, v242\n"
"s_cbranch_vccnz LSLOW_%=\nLBACK2_%=:\ns_mov_b64 vcc, s[64:65]\n"
"v_mfma_f32_16x16x32_bf16 a[232:235], v[120:123], a[244:247], a[232:235]\n"
"v_accvgpr_mov_b32 a6, a150\n"
"v_accvgpr_mov_b32 a8, a152\n"
"v_accvgpr_mov_b32 a9, a153\n"
"v_accvgpr_mov_b32 a14, a158\n"
"v_accvgpr_mov_b32 a20, a164\n"
"v_accvgpr_mov_b32 a42, a186\n"
"v_accvgpr_mov_b32 a44, a188\n"
"v_xor_b32 v148, 0x400, v243\n"
"v_bfe_u32 v149, v242, 8, 2\n"
"v_lshl_add_u32 v149, v149, 4, s43\n"
"v_mfma_f32_16x16x32_bf16 v[24:27], v[96:99], a[244:247], v[24:27]\n"
"s_waitcnt lgkmcnt(0)\n"
"ds_read_b128 v[124:127], v149 offset:0\n"
"v_rcp_f32 v206, v200\n"
"v_accvgpr_mov_b32 a4, a148\n"
"v_accvgpr_mov_b32 a12, a156\n"
"v_accvgpr_mov_b32 a13, a157\n"
"v_accvgpr_mov_b32 a16, a160\n"
"v_accvgpr_mov_b32 a39, a183\n"
"v_accvgpr_mov_b32 a47, a191\n"
"v_xor_b32 v150, 64, v149\n"
"v_mfma_f32_16x16x32_bf16 v[24:27], v[100:103], a[248:251], v[24:27]\n"
"ds_read_b128 v[128:131], v150 offset:0\n"
"v_accvgpr_mov_b32 a0, a144\n"
"v_accvgpr_mov_b32 a1, a145\n"
"v_accvgpr_mov_b32 a5, a149\n"
"v_accvgpr_mov_b32 a10, a154\n"
"v_accvgpr_mov_b32 a17, a161\n"
"v_accvgpr_mov_b32 a40, a184\n"
"v_mfma_f32_16x16x32_bf16 v[56:59], v[104:107], a[244:247], v[56:59]\n"
"ds_read_b128 v[132:135], v149 offset:1024\n"
"v_rcp_f32 v246, v196\n"
"v_mov_b32_dpp v206, v206 row_ror:12 row_mask:0x2 bank_mask:0xf\n"
"v_accvgpr_mov_b32 a2, a146\n"
"v_accvgpr_mov_b32 a22, a166\n"
"v_accvgpr_mov_b32 a23, a167\n"
"v_accvgpr_mov_b32 a36, a180\n"
"v_accvgpr_mov_b32 a38, a182\n"
"v_accvgpr_mov_b32 a45, a189\n"
"v_mfma_f32_16x16x32_bf16 v[56:59], v[108:111], a[248:251], v[56:59]\n"
"ds_read_b128 v[136:139], v150 offset:1024\n"
"v_accvgpr_mov_b32 a33, a177\n"
"v_mfma_f32_16x16x32_bf16 v[88:91], v[112:115], a[244:247], v[88:91]\n"
"ds_read_b64_tr_b16 v[224:225], v239 offset:50176\n"
"v_accvgpr_mov_b32 a3, a147\n"
"v_accvgpr_mov_b32 a7, a151\n"
"v_accvgpr_mov_b32 a11, a155\n"
"v_accvgpr_mov_b32 a19, a163\n"
"v_accvgpr_mov_b32 a21, a165\n"
"v_accvgpr_mov_b32 a24, a168\n"
"v_accvgpr_mov_b32 a27, a171\n"
"v_accvgpr_mov_b32 a30, a174\n"
"v_accvgpr_mov_b32 a46, a190\n"
"v_mfma_f32_16x16x32_bf16 v[88:91], v[116:119], a[248:251], v[88:91]\n"
"s_waitcnt lgkmcnt(4)\n"
"ds_read_b128 a[204:207], v148 offset:6144\n"
"v_mov_b32_dpp v206, v206 row_ror:8 row_mask:0x4 bank_mask:0xf\n"
"v_accvgpr_mov_b32 a15, a159\n"
"v_accvgpr_mov_b32 a25, a169\n"
"v_accvgpr_mov_b32 a26, a170\n"
"v_accvgpr_mov_b32 a29, a173\n"
"v_accvgpr_mov_b32 a31, a175\n"
"v_accvgpr_mov_b32 a35, a179\n"
"v_accvgpr_mov_b32 a43, a187\n"
"v_accvgpr_mov_b32 a160, a64\n"
"v_mfma_f32_16x16x32_bf16 v[180:183], v[124:127], a[0:3], 0\n"
"s_waitcnt lgkmcnt(4)\n"
"v_mov_b32 v252, v192\n"
"v_accvgpr_mov_b32 a174, a78\n"
"v_mfma_f32_16x16x32_bf16 v[180:183], v[128:131], a[4:7], v[180:183]\n"
"s_waitcnt lgkmcnt(3)\n"
"ds_read_b128 a[192:195], v243 offset:4096\n"
"v_accvgpr_mov_b32 a18, a162\n"
"v_mfma_f32_16x16x32_bf16 v[180:183], v[132:135], a[8:11], v[180:183]\n"
"s_waitcnt lgkmcnt(3)\n"
"ds_read_b128 a[200:203], v243 offset:6144\n"
"v_accvgpr_mov_b32 a183, a87\n"
"v_mfma_f32_16x16x32_bf16 v[180:183], v[136:139], a[12:15], v[180:183]\n"
"v_rcp_f32 v247, v198\n"
"v_accvgpr_mov_b32 a28, a172\n"
"v_accvgpr_mov_b32 a34, a178\n"
"v_accvgpr_mov_b32 a41, a185\n"
"v_accvgpr_mov_b32 a151, a55\n"
"v_accvgpr_mov_b32 a167, a71\n"
"v_accvgpr_mov_b32 a156, a60\n"
"v_accvgpr_mov_b32 a178, a82\n"
"v_mfma_f32_16x16x32_bf16 v[184:187], v[124:127], a[16:19], 0\n"
"ds_read_b128 a[196:199], v148 offset:4096\n"
"v_accvgpr_mov_b32 a158, a62\n"
"v_mfma_f32_16x16x32_bf16 v[184:187], v[128:131], a[20:23], v[184:187]\n"
"v_mov_b32_dpp v247, v247 row_ror:12 row_mask:0x2 bank_mask:0xf\n"
"v_accvgpr_mov_b32 a152, a56\n"
"v_mfma_f32_16x16x32_bf16 v[184:187], v[132:135], a[24:27], v[184:187]\n"
"s_add_u32 s40, s56, s53\n"
"v_accvgpr_mov_b32 a32, a176\n"
"v_accvgpr_mov_b32 a157, a61\n"
"v_mfma_f32_16x16x32_bf16 v[184:187], v[136:139], a[28:31], v[184:187]\n"
"buffer_load_dwordx4 v240, s[20:23], s40 offen offset:2048 nt lds\n"
"v_mov_b32_dpp v206, v206 row_ror:4 row_mask:0x8 bank_mask:0xf\n"
"v_accvgpr_mov_b32 a37, a181\n"
"v_accvgpr_mov_b32 a149, a53\n"
"v_accvgpr_mov_b32 a150, a54\n"
"v_accvgpr_mov_b32 a181, a85\n"
"v_accvgpr_mov_b32 a153, a57\n"
"v_sub_f32 v180, s74, v180\n"
"v_sub_f32 v183, s74, v183\n"
"v_mfma_f32_16x16x32_bf16 v[188:191], v[124:127], a[32:35], 0\n"
"v_mov_b32_dpp v246, v246 row_ror:12 row_mask:0x2 bank_mask:0xf\n"
"v_accvgpr_mov_b32 a180, a84\n"
"v_mfma_f32_16x16x32_bf16 v[188:191], v[128:131], a[36:39], v[188:191]\n"
"v_accvgpr_mov_b32 a162, a66\n"
"v_accvgpr_mov_b32 a179, a83\n"
"v_mfma_f32_16x16x32_bf16 v[188:191], v[132:135], a[40:43], v[188:191]\n"
"v_mov_b32_dpp v246, v246 row_ror:8 row_mask:0x4 bank_mask:0xf\n"
"v_sub_f32 v182, s74, v182\n"
"v_mfma_f32_16x16x32_bf16 v[188:191], v[136:139], a[44:47], v[188:191]\n"
"s_waitcnt lgkmcnt(1)\n"
"ds_write_b128 v244, a[220:223] offset:33792\n"
"v_mov_b32_dpp v247, v247 row_ror:8 row_mask:0x4 bank_mask:0xf\n"
"v_accvgpr_mov_b32 a163, a67\n"
"v_accvgpr_mov_b32 a177, a81\n"
"v_accvgpr_mov_b32 a154, a58\n"
"v_mfma_f32_16x16x32_bf16 v[192:195], v[124:127], a[192:195], 0\n"
"s_waitcnt lgkmcnt(1)\n"
"v_accvgpr_mov_b32 a164, a68\n"
"v_sub_f32 v181, s74, v181\n"
"v_mfma_f32_16x16x32_bf16 v[192:195], v[128:131], a[196:199], v[192:195]\n"
"v_accvgpr_mov_b32 a173, a77\n"
"v_accvgpr_mov_b32 a144, a48\n"
"v_mfma_f32_16x16x32_bf16 v[192:195], v[132:135], a[200:203], v[192:195]\n"
"ds_read_b64_tr_b16 v[226:227], v239 offset:54272\n"
"ds_read_b64_tr_b16 a[246:247], v232 offset:53248\n"
"v_mfma_f32_16x16x32_bf16 v[192:195], v[136:139], a[204:207], v[192:195]\n"
"v_mov_b32_dpp v246, v246 row_ror:4 row_mask:0x8 bank_mask:0xf\n"
"v_mov_b32_dpp v247, v247 row_ror:4 row_mask:0x8 bank_mask:0xf\n"
"v_accvgpr_mov_b32 a148, a52\n"
"v_accvgpr_mov_b32 a165, a69\n"
"v_accvgpr_mov_b32 a175, a79\n"
"v_accvgpr_mov_b32 a176, a80\n"
"v_sub_f32 v184, s74, v184\n"
"v_sub_f32 v187, s74, v187\n"
"v_sub_f32 v190, s74, v190\n"
"v_mfma_f32_16x16x32_bf16 v[152:155], v[208:211], a[0:3], v[180:183]\n"
"v_mov_b32 v253, v202\n"
"v_sub_f32 v185, s74, v185\n"
"v_mfma_f32_16x16x32_bf16 v[152:155], v[212:215], a[4:7], v[152:155]\n"
"v_accvgpr_mov_b32 a186, a90\n"
"v_sub_f32 v186, s74, v186\n"
"v_mfma_f32_16x16x32_bf16 v[152:155], v[216:219], a[8:11], v[152:155]\n"
"s_add_u32 s40, s56, s54\n"
"ds_read_b64_tr_b16 a[254:255], v235 offset:54272\n"
"v_sub_f32 v189, s74, v189\n"
"v_mfma_f32_16x16x32_bf16 v[152:155], v[220:223], a[12:15], v[152:155]\n"
"buffer_load_dwordx4 v240, s[20:23], s40 offen offset:3072 nt lds\n"
"v_accvgpr_mov_b32 a190, a94\n"
"v_accvgpr_mov_b32 a191, a95\n"
"v_accvgpr_mov_b32 a168, a72\n"
"v_mul_f32_dpp v9, v246, v9 row_newbcast:1 row_mask:0xf bank_mask:0xf\n"
"v_mul_f32_dpp v12, v246, v12 row_newbcast:0 row_mask:0xf bank_mask:0xf\n"
"v_sub_f32 v188, s74, v188\n"
"v_sub_f32 v191, s74, v191\n"
"v_mfma_f32_16x16x32_bf16 v[156:159], v[216:219], a[24:27], v[184:187]\n"
"s_add_u32 s55, s55, s59\n"
"v_accvgpr_mov_b32 a159, a63\n"
"v_accvgpr_mov_b32 a155, a59\n"
"v_mfma_f32_16x16x32_bf16 v[156:159], v[220:223], a[28:31], v[156:159]\n"
"s_add_u32 s41, s55, s57\n"
"v_accvgpr_mov_b32 a145, a49\n"
"v_accvgpr_mov_b32 a170, a74\n"
"v_mfma_f32_16x16x32_bf16 v[156:159], v[208:211], a[16:19], v[156:159]\n"
"s_and_b32 s41, s41, s58\n"
"v_accvgpr_mov_b32 a147, a51\n"
"v_sub_f32 v192, s74, v192\n"
"v_mfma_f32_16x16x32_bf16 v[156:159], v[212:215], a[20:23], v[156:159]\n"
"s_andn2_b32 s56, s55, s58\n"
"ds_read_b64_tr_b16 a[250:251], v236 offset:53248\n"
"v_xor_b32 v250, 0x1000, v245\n"
"v_accvgpr_mov_b32 a146, a50\n"
"v_accvgpr_mov_b32 a161, a65\n"
"v_accvgpr_mov_b32 a187, a91\n"
"v_mul_f32_dpp v41, v247, v41 row_newbcast:1 row_mask:0xf bank_mask:0xf\n"
"v_mul_f32_dpp v46, v247, v46 row_newbcast:2 row_mask:0xf bank_mask:0xf\n"
"v_sub_f32 v195, s74, v195\n"
"v_exp_f32 v153, v153\n"
"v_mfma_f32_16x16x32_bf16 v[160:163], v[208:211], a[32:35], v[188:191]\n"
"s_or_b32 s56, s56, s41\n"
"v_mul_f32_dpp v43, v247, v43 row_newbcast:3 row_mask:0xf bank_mask:0xf\n"
"v_sub_f32 v193, s74, v193\n"
"v_mfma_f32_16x16x32_bf16 v[160:163], v[212:215], a[36:39], v[160:163]\n"
"v_accvgpr_mov_b32 a171, a75\n"
"v_accvgpr_mov_b32 a185, a89\n"
"v_mfma_f32_16x16x32_bf16 v[160:163], v[216:219], a[40:43], v[160:163]\n"
"v_accvgpr_mov_b32 a182, a86\n"
"v_sub_f32 v194, s74, v194\n"
"v_mfma_f32_16x16x32_bf16 v[160:163], v[220:223], a[44:47], v[160:163]\n"
"ds_read_b64_tr_b16 a[252:253], v235 offset:50176\n"
"v_accvgpr_mov_b32 a172, a76\n"
"v_accvgpr_mov_b32 a184, a88\n"
"v_exp_f32 v152, v152\n"
"v_exp_f32 v157, v157\n"
"v_exp_f32 v158, v158\n"
"v_mfma_f32_16x16x32_bf16 v[164:167], v[216:219], a[200:203], v[192:195]\n"
"ds_read_b128 v[216:219], v228 offset:5120\n"
"v_accvgpr_mov_b32 a166, a70\n"
"v_mfma_f32_16x16x32_bf16 v[164:167], v[220:223], a[204:207], v[164:167]\n"
"v_accvgpr_mov_b32 a188, a92\n"
"v_mul_f32_dpp v57, v247, v57 row_newbcast:1 row_mask:0xf bank_mask:0xf\n"
"v_mfma_f32_16x16x32_bf16 v[164:167], v[208:211], a[192:195], v[164:167]\n"
"ds_read_b128 v[208:211], v228 offset:4096\n"
"v_accvgpr_mov_b32 a189, a93\n"
"v_mfma_f32_16x16x32_bf16 v[164:167], v[212:215], a[196:199], v[164:167]\n"
"s_waitcnt lgkmcnt(3)\n"
"ds_read_b128 v[220:223], v229 offset:5120\n"
"v_accvgpr_mov_b32 a169, a73\n"
"v_mul_f32_dpp v10, v246, v10 row_newbcast:2 row_mask:0xf bank_mask:0xf\n"
"v_exp_f32 v154, v154\n"
"v_exp_f32 v155, v155\n"
"v_mul_f32_dpp v27, v246, v27 row_newbcast:3 row_mask:0xf bank_mask:0xf\n"
"v_mul_f32_dpp v58, v247, v58 row_newbcast:2 row_mask:0xf bank_mask:0xf\n"
"LTC_%=:\n"
"s_waitcnt lgkmcnt(4)\n"
"v_mfma_f32_16x16x32_bf16 v[92:95], v[116:119], v[224:227], v[92:95]\n"
"s_waitcnt lgkmcnt(3)\n"
"v_mul_f32_dpp v56, v247, v56 row_newbcast:0 row_mask:0xf bank_mask:0xf\n"
"v_cvt_pk_bf16_f32 v56, v56, v57\n"
"v_mfma_f32_16x16x32_bf16 v[92:95], v[112:115], a[252:255], v[92:95]\n"
"v_mul_f32_dpp v13, v246, v13 row_newbcast:1 row_mask:0xf bank_mask:0xf\n"
"v_mul_f32_dpp v44, v247, v44 row_newbcast:0 row_mask:0xf bank_mask:0xf\n"
"v_mfma_f32_16x16x32_bf16 v[60:63], v[108:111], v[224:227], v[60:63]\n"
"s_add_u32 m0, s44, 32768\n"
"ds_read_b128 v[212:215], v229 offset:4096\n"
"v_mul_f32_dpp v8, v246, v8 row_newbcast:0 row_mask:0xf bank_mask:0xf\n"
"v_mfma_f32_16x16x32_bf16 v[60:63], v[104:107], a[252:255], v[60:63]\n"
"buffer_load_dwordx4 v240, s[16:19], s56 offen nt lds\n"
"v_mfma_f32_16x16x32_bf16 v[28:31], v[100:103], v[224:227], v[28:31]\n"
"v_exp_f32 v165, v165\n"
"v_mfma_f32_16x16x32_bf16 v[28:31], v[96:99], a[252:255], v[28:31]\n"
"v_exp_f32 v166, v166\n"
"v_mfma_f32_16x16x32_bf16 a[236:239], v[120:123], a[252:255], a[236:239]\n"
"s_waitcnt lgkmcnt(2)\n"
"ds_read_b64_tr_b16 a[254:255], v233 offset:53248\n"
"v_mul_f32_dpp v42, v247, v42 row_newbcast:2 row_mask:0xf bank_mask:0xf\n"
"v_mfma_f32_16x16x32_bf16 v[168:171], v[216:219], a[8:11], v[180:183]\n"
"s_waitcnt lgkmcnt(2)\n"
"v_exp_f32 v156, v156\n"
"v_mfma_f32_16x16x32_bf16 v[168:171], v[220:223], a[12:15], v[168:171]\n"
"v_cvt_pk_bf16_f32 v8, v8, v9\n"
"v_mul_f32_dpp v76, v206, v76 row_newbcast:0 row_mask:0xf bank_mask:0xf\n"
"v_mfma_f32_16x16x32_bf16 v[168:171], v[208:211], a[0:3], v[168:171]\n"
"s_waitcnt lgkmcnt(1)\n"
"ds_read_b64_tr_b16 v[224:225], v237 offset:49152\n"
"v_mov_b64 v[202:203], 0\n"
"v_mfma_f32_16x16x32_bf16 v[168:171], v[212:215], a[4:7], v[168:171]\n"
"ds_write_b128 v244, a[236:239] offset:35840\n"
"v_mul_f32_dpp v77, v206, v77 row_newbcast:1 row_mask:0xf bank_mask:0xf\n"
"v_mul_f32_dpp v78, v206, v78 row_newbcast:2 row_mask:0xf bank_mask:0xf\n"
"v_cvt_pk_bf16_f32 v136, v156, v157\n"
"v_mul_f32_dpp v59, v247, v59 row_newbcast:3 row_mask:0xf bank_mask:0xf\n"
"v_mul_f32_dpp v88, v206, v88 row_newbcast:0 row_mask:0xf bank_mask:0xf\n"
"v_mfma_f32_16x16x32_bf16 v[172:175], v[208:211], a[16:19], v[184:187]\n"
"v_mul_f32_dpp v15, v246, v15 row_newbcast:3 row_mask:0xf bank_mask:0xf\n"
"v_mul_f32_dpp v25, v246, v25 row_newbcast:1 row_mask:0xf bank_mask:0xf\n"
"v_mfma_f32_16x16x32_bf16 v[172:175], v[212:215], a[20:23], v[172:175]\n"
"ds_read_b64_tr_b16 a[244:245], v232 offset:49152\n"
"v_cvt_pk_bf16_f32 v129, v154, v155\n"
"v_mfma_f32_16x16x32_bf16 v[172:175], v[216:219], a[24:27], v[172:175]\n"
"ds_read_b64_tr_b16 a[248:249], v236 offset:49152\n"
"v_mul_f32_dpp v11, v246, v11 row_newbcast:3 row_mask:0xf bank_mask:0xf\n"
"v_mfma_f32_16x16x32_bf16 v[172:175], v[220:223], a[28:31], v[172:175]\n"
"v_mul_f32_dpp v14, v246, v14 row_newbcast:2 row_mask:0xf bank_mask:0xf\n"
"v_mul_f32_dpp v45, v247, v45 row_newbcast:1 row_mask:0xf bank_mask:0xf\n"
"v_mfma_f32_16x16x32_bf16 v[176:179], v[216:219], a[40:43], v[188:191]\n"
"ds_read_b64_tr_b16 a[252:253], v233 offset:49152\n"
"v_mul_f32_dpp v73, v206, v73 row_newbcast:1 row_mask:0xf bank_mask:0xf\n"
"v_mfma_f32_16x16x32_bf16 v[176:179], v[220:223], a[44:47], v[176:179]\n"
"v_mul_f32_dpp v72, v206, v72 row_newbcast:0 row_mask:0xf bank_mask:0xf\n"
"v_mul_f32_dpp v93, v206, v93 row_newbcast:1 row_mask:0xf bank_mask:0xf\n"
"v_mfma_f32_16x16x32_bf16 v[176:179], v[208:211], a[32:35], v[176:179]\n"
"s_add_u32 s40, s56, s52\n"
"ds_read_b64_tr_b16 v[226:227], v237 offset:53248\n"
"v_cvt_pk_bf16_f32 v128, v152, v153\n"
"v_mfma_f32_16x16x32_bf16 v[176:179], v[212:215], a[36:39], v[176:179]\n"
"buffer_load_dwordx4 v240, s[16:19], s40 offen offset:1024 nt lds\n"
"v_mul_f32_dpp v47, v247, v47 row_newbcast:3 row_mask:0xf bank_mask:0xf\n"
"v_exp_f32 v161, v161\n"
"v_mul_f32_dpp v90, v206, v90 row_newbcast:2 row_mask:0xf bank_mask:0xf\n"
"v_exp_f32 v169, v169\n"
"v_exp_f32 v170, v170\n"
"v_mfma_f32_16x16x32_bf16 v[152:155], v[208:211], a[192:195], v[192:195]\n"
"v_exp_f32 v162, v162\n"
"v_mfma_f32_16x16x32_bf16 v[152:155], v[212:215], a[196:199], v[152:155]\n"
"v_exp_f32 v159, v159\n"
"v_mfma_f32_16x16x32_bf16 v[152:155], v[216:219], a[200:203], v[152:155]\n"
"ds_read_b128 v[212:215], v231 offset:0\n"
"v_cvt_pk_bf16_f32 v137, v158, v159\n"
"v_mfma_f32_16x16x32_bf16 v[152:155], v[220:223], a[204:207], v[152:155]\n"
"s_waitcnt lgkmcnt(3)\n"
"ds_read_b128 v[220:223], v231 offset:1024\n"
"v_mul_f32_dpp v30, v246, v30 row_newbcast:2 row_mask:0xf bank_mask:0xf\n"
"v_mfma_f32_16x16x32_bf16 a[208:211], v[120:123], a[244:247], a[208:211]\n"
"ds_read_b128 v[208:211], v230 offset:0\n"
"v_exp_f32 v163, v163\n"
"v_exp_f32 v167, v167\n"
"v_mul_f32_dpp v62, v247, v62 row_newbcast:2 row_mask:0xf bank_mask:0xf\n"
"v_exp_f32 v168, v168\n"
"v_exp_f32 v178, v178\n"
"v_mfma_f32_16x16x32_bf16 v[0:3], v[96:99], a[244:247], v[0:3]\n"
"v_cvt_pk_bf16_f32 v125, v166, v167\n"
"v_mul_f32_dpp v61, v247, v61 row_newbcast:1 row_mask:0xf bank_mask:0xf\n"
"v_mfma_f32_16x16x32_bf16 v[0:3], v[100:103], a[248:251], v[0:3]\n"
"ds_read_b128 v[216:219], v230 offset:1024\n"
"v_mul_f32_dpp v40, v247, v40 row_newbcast:0 row_mask:0xf bank_mask:0xf\n"
"v_cvt_pk_bf16_f32 v40, v40, v41\n"
"v_exp_f32 v160, v160\n"
"v_cvt_pk_bf16_f32 v145, v162, v163\n"
"v_exp_f32 v164, v164\n"
"v_cvt_pk_bf16_f32 v57, v58, v59\n"
"v_mfma_f32_16x16x32_bf16 v[32:35], v[104:107], a[244:247], v[32:35]\n"
"v_exp_f32 v174, v174\n"
"v_mfma_f32_16x16x32_bf16 v[32:35], v[108:111], a[248:251], v[32:35]\n"
"ds_write_b128 v244, a[232:235] offset:34816\n"
"v_mul_f32_dpp v74, v206, v74 row_newbcast:2 row_mask:0xf bank_mask:0xf\n"
"v_mul_f32_dpp v26, v246, v26 row_newbcast:2 row_mask:0xf bank_mask:0xf\n"
"v_exp_f32 v176, v176\n"
"v_mfma_f32_16x16x32_bf16 v[64:67], v[112:115], a[244:247], v[64:67]\n"
"v_mul_f32_dpp v94, v206, v94 row_newbcast:2 row_mask:0xf bank_mask:0xf\n"
"v_mul_f32_dpp v95, v206, v95 row_newbcast:3 row_mask:0xf bank_mask:0xf\n"
"v_mfma_f32_16x16x32_bf16 v[64:67], v[116:119], a[248:251], v[64:67]\n"
"s_waitcnt lgkmcnt(2)\n"
"ds_write_b128 v244, a[216:219] offset:32768\n"
"v_cvt_pk_bf16_f32 v41, v42, v43\n"
"v_mul_f32_dpp v79, v206, v79 row_newbcast:3 row_mask:0xf bank_mask:0xf\n"
"v_mul_f32_dpp v28, v246, v28 row_newbcast:0 row_mask:0xf bank_mask:0xf\n"
"v_mul_f32_dpp v29, v246, v29 row_newbcast:1 row_mask:0xf bank_mask:0xf\n"
"v_mul_f32_dpp v89, v206, v89 row_newbcast:1 row_mask:0xf bank_mask:0xf\n"
"v_mfma_f32_16x16x32_bf16 v[156:159], v[208:211], a[0:3], v[180:183]\n"
"v_exp_f32 v172, v172\n"
"v_mfma_f32_16x16x32_bf16 v[156:159], v[212:215], a[4:7], v[156:159]\n"
"s_waitcnt lgkmcnt(2)\n"
"v_exp_f32 v175, v175\n"
"v_mfma_f32_16x16x32_bf16 v[156:159], v[216:219], a[8:11], v[156:159]\n"
"s_add_u32 s40, s56, s53\n"
"ds_read_b64_tr_b16 a[250:251], v236 offset:54272\n"
"v_cvt_pk_bf16_f32 v144, v160, v161\n"
"v_mfma_f32_16x16x32_bf16 v[156:159], v[220:223], a[12:15], v[156:159]\n"
"buffer_load_dwordx4 v240, s[16:19], s40 offen offset:2048 nt lds\n"
"v_cvt_pk_bf16_f32 v72, v72, v73\n"
"v_mul_f32_dpp v24, v246, v24 row_newbcast:0 row_mask:0xf bank_mask:0xf\n"
"v_mul_f32_dpp v92, v206, v92 row_newbcast:0 row_mask:0xf bank_mask:0xf\n"
"v_cvt_pk_bf16_f32 v88, v88, v89\n"
"v_exp_f32 v177, v177\n"
"v_exp_f32 v155, v155\n"
"v_mfma_f32_16x16x32_bf16 v[160:163], v[216:219], a[24:27], v[184:187]\n"
"v_exp_f32 v171, v171\n"
"v_mfma_f32_16x16x32_bf16 v[160:163], v[220:223], a[28:31], v[160:163]\n"
"v_mul_f32_dpp v60, v247, v60 row_newbcast:0 row_mask:0xf bank_mask:0xf\n"
"v_mul_f32_dpp v32, v247, v32 row_newbcast:0 row_mask:0xf bank_mask:0xf\n"
"v_mfma_f32_16x16x32_bf16 v[160:163], v[208:211], a[16:19], v[160:163]\n"
"v_cvt_pk_bf16_f32 v130, v168, v169\n"
"v_mul_f32_dpp v0, v246, v0 row_newbcast:0 row_mask:0xf bank_mask:0xf\n"
"v_mfma_f32_16x16x32_bf16 v[160:163], v[212:215], a[20:23], v[160:163]\n"
"ds_read_b64_tr_b16 a[244:245], v232 offset:50176\n"
"v_cvt_pk_bf16_f32 v124, v164, v165\n"
"v_mul_f32_dpp v63, v247, v63 row_newbcast:3 row_mask:0xf bank_mask:0xf\n"
"v_mul_f32_dpp v91, v206, v91 row_newbcast:3 row_mask:0xf bank_mask:0xf\n"
"v_exp_f32 v179, v179\n"
"v_exp_f32 v152, v152\n"
"v_mul_f32_dpp v64, v206, v64 row_newbcast:0 row_mask:0xf bank_mask:0xf\n"
"v_mfma_f32_16x16x32_bf16 v[164:167], v[208:211], a[32:35], v[188:191]\n"
"ds_read_b64_tr_b16 a[248:249], v236 offset:50176\n"
"v_cvt_pk_bf16_f32 v24, v24, v25\n"
"v_mfma_f32_16x16x32_bf16 v[164:167], v[212:215], a[36:39], v[164:167]\n"
"v_cvt_pk_bf16_f32 v9, v10, v11\n"
"v_cvt_pk_bf16_f32 v11, v14, v15\n"
"v_mfma_f32_16x16x32_bf16 v[164:167], v[216:219], a[40:43], v[164:167]\n"
"ds_read_b128 v[216:219], v230 offset:5120\n"
"v_cvt_pk_bf16_f32 v25, v26, v27\n"
"v_mfma_f32_16x16x32_bf16 v[164:167], v[220:223], a[44:47], v[164:167]\n"
"ds_read_b128 v[212:215], v231 offset:4096\n"
"v_cvt_pk_bf16_f32 v43, v46, v47\n"
"v_cvt_pk_bf16_f32 v26, v28, v29\n"
"v_exp_f32 v173, v173\n"
"v_exp_f32 v154, v154\n"
"v_exp_f32 v157, v157\n"
"v_mul_f32_dpp v65, v206, v65 row_newbcast:1 row_mask:0xf bank_mask:0xf\n"
"v_mfma_f32_16x16x32_bf16 v[68:71], v[116:119], v[224:227], v[68:71]\n"
"ds_read_b128 v[208:211], v230 offset:4096\n"
"v_mul_f32_dpp v75, v206, v75 row_newbcast:3 row_mask:0xf bank_mask:0xf\n"
"v_mfma_f32_16x16x32_bf16 v[68:71], v[112:115], a[252:255], v[68:71]\n"
"ds_read_b64_tr_b16 a[246:247], v232 offset:54272\n"
"v_cvt_pk_bf16_f32 v58, v60, v61\n"
"v_cvt_pk_bf16_f32 v139, v174, v175\n"
"v_cvt_pk_bf16_f32 v146, v176, v177\n"
"v_exp_f32 v161, v161\n"
"v_exp_f32 v163, v163\n"
"v_mul_f32_dpp v2, v246, v2 row_newbcast:2 row_mask:0xf bank_mask:0xf\n"
"v_mfma_f32_16x16x32_bf16 v[36:39], v[108:111], v[224:227], v[36:39]\n"
"s_add_u32 s40, s56, s54\n"
"ds_read_b128 v[220:223], v231 offset:5120\n"
"v_cvt_pk_bf16_f32 v138, v172, v173\n"
"v_mfma_f32_16x16x32_bf16 v[36:39], v[104:107], a[252:255], v[36:39]\n"
"buffer_load_dwordx4 v240, s[16:19], s40 offen offset:3072 nt lds\n"
"v_cvt_pk_bf16_f32 v10, v12, v13\n"
"v_cvt_pk_bf16_f32 v73, v74, v75\n"
"v_cvt_pk_bf16_f32 v127, v154, v155\n"
"v_exp_f32 v156, v156\n"
"v_exp_f32 v162, v162\n"
"v_mfma_f32_16x16x32_bf16 v[4:7], v[100:103], v[224:227], v[4:7]\n"
"v_mul_f32_dpp v31, v246, v31 row_newbcast:3 row_mask:0xf bank_mask:0xf\n"
"v_mul_f32_dpp v34, v247, v34 row_newbcast:2 row_mask:0xf bank_mask:0xf\n"
"v_mfma_f32_16x16x32_bf16 v[4:7], v[96:99], a[252:255], v[4:7]\n"
"v_cvt_pk_bf16_f32 v59, v62, v63\n"
"v_cvt_pk_bf16_f32 v131, v170, v171\n"
"v_mfma_f32_16x16x32_bf16 a[212:215], v[120:123], a[252:255], a[212:215]\n"
"s_waitcnt lgkmcnt(2)\n"
"ds_write_b64 v250, v[252:253] offset:52224\n"
"v_cvt_pk_bf16_f32 v42, v44, v45\n"
"v_cvt_pk_bf16_f32 v89, v90, v91\n"
"v_exp_f32 v153, v153\n"
"v_mul_f32_dpp v3, v246, v3 row_newbcast:3 row_mask:0xf bank_mask:0xf\n"
"v_mul_f32_dpp v33, v247, v33 row_newbcast:1 row_mask:0xf bank_mask:0xf\n"
"v_mul_f32_dpp v69, v206, v69 row_newbcast:1 row_mask:0xf bank_mask:0xf\n"
"v_mfma_f32_16x16x32_bf16 v[168:171], v[208:211], a[0:3], v[180:183]\n"
"ds_read_b64_tr_b16 v[226:227], v237 offset:54272\n"
"v_mul_f32_dpp v39, v247, v39 row_newbcast:3 row_mask:0xf bank_mask:0xf\n"
"v_mfma_f32_16x16x32_bf16 v[168:171], v[212:215], a[4:7], v[168:171]\n"
"v_cvt_pk_bf16_f32 v74, v76, v77\n"
"v_mul_f32_dpp v1, v246, v1 row_newbcast:1 row_mask:0xf bank_mask:0xf\n"
"v_mfma_f32_16x16x32_bf16 v[168:171], v[216:219], a[8:11], v[168:171]\n"
"s_waitcnt lgkmcnt(2)\n"
"ds_read_b64_tr_b16 a[254:255], v233 offset:54272\n"
"v_mul_f32_dpp v68, v206, v68 row_newbcast:0 row_mask:0xf bank_mask:0xf\n"
"v_mfma_f32_16x16x32_bf16 v[168:171], v[220:223], a[12:15], v[168:171]\n"
"ds_write_b128 v248, v[40:43] offset:50176\n"
"v_cvt_pk_bf16_f32 v75, v78, v79\n"
"v_exp_f32 v158, v158\n"
"v_exp_f32 v160, v160\n"
"v_mfma_f32_16x16x32_bf16 v[172:175], v[216:219], a[24:27], v[184:187]\n"
"ds_read_b64_tr_b16 a[68:69], v249 offset:50176\n"
"v_cvt_pk_bf16_f32 v140, v160, v161\n"
"v_mfma_f32_16x16x32_bf16 v[172:175], v[220:223], a[28:31], v[172:175]\n"
"v_cvt_pk_bf16_f32 v147, v178, v179\n"
"v_mul_f32_dpp v70, v206, v70 row_newbcast:2 row_mask:0xf bank_mask:0xf\n"
"v_mfma_f32_16x16x32_bf16 v[172:175], v[208:211], a[16:19], v[172:175]\n"
"v_mul_f32_dpp v35, v247, v35 row_newbcast:3 row_mask:0xf bank_mask:0xf\n"
"v_mul_f32_dpp v36, v247, v36 row_newbcast:0 row_mask:0xf bank_mask:0xf\n"
"v_mfma_f32_16x16x32_bf16 v[172:175], v[212:215], a[20:23], v[172:175]\n"
"s_cbranch_vccnz LPCT0_%=\nLPKT0_%=:\n"
"ds_write_b128 v248, v[8:11] offset:49152\n"
"v_cvt_pk_bf16_f32 v91, v94, v95\n"
"v_mul_f32_dpp v37, v247, v37 row_newbcast:1 row_mask:0xf bank_mask:0xf\n"
"v_mul_f32_dpp v66, v206, v66 row_newbcast:2 row_mask:0xf bank_mask:0xf\n"
"v_mul_f32_dpp v67, v206, v67 row_newbcast:3 row_mask:0xf bank_mask:0xf\n"
"v_mfma_f32_16x16x32_bf16 v[176:179], v[208:211], a[32:35], v[188:191]\n"
"ds_read_b64_tr_b16 a[70:71], v249 offset:50184\n"
"ds_read_b64_tr_b16 v[224:225], v237 offset:50176\n"
"v_mfma_f32_16x16x32_bf16 v[176:179], v[212:215], a[36:39], v[176:179]\n"
"ds_read_b64_tr_b16 a[252:253], v233 offset:50176\n"
"v_mul_f32_dpp v7, v246, v7 row_newbcast:3 row_mask:0xf bank_mask:0xf\n"
"v_mfma_f32_16x16x32_bf16 v[176:179], v[216:219], a[40:43], v[176:179]\n"
"ds_read_b64_tr_b16 a[52:53], v249 offset:49152\n"
"ds_read_b64_tr_b16 a[54:55], v249 offset:49160\n"
"v_mfma_f32_16x16x32_bf16 v[176:179], v[220:223], a[44:47], v[176:179]\n"
"ds_write_b128 v248, v[56:59] offset:50176\n"
"v_cvt_pk_bf16_f32 v27, v30, v31\n"
"v_exp_f32 v171, v171\n"
"v_exp_f32 v172, v172\n"
"v_mfma_f32_16x16x32_bf16 a[224:227], v[120:123], a[244:247], a[224:227]\n"
"ds_write_b128 v248, v[24:27] offset:49152\n"
"v_exp_f32 v164, v164\n"
"v_mul_f32_dpp v6, v246, v6 row_newbcast:2 row_mask:0xf bank_mask:0xf\n"
"v_cvt_pk_bf16_f32 v0, v0, v1\n"
"v_xor_b32 v244, 0x1000, v244\n"
"v_mfma_f32_16x16x32_bf16 v[16:19], v[96:99], a[244:247], v[16:19]\n"
"s_waitcnt vmcnt(8) lgkmcnt(4)\n"
"ds_read_b64_tr_b16 a[78:79], v249 offset:50184\n"
"v_cvt_pk_bf16_f32 v1, v2, v3\n"
"v_mfma_f32_16x16x32_bf16 v[16:19], v[100:103], a[248:251], v[16:19]\n"
"s_barrier\n"
"ds_read_b64_tr_b16 a[60:61], v249 offset:49152\n"
"ds_read_b128 v[216:219], v228 offset:17408\n"
"v_cvt_pk_bf16_f32 v32, v32, v33\n"
"v_mul_f32_dpp v71, v206, v71 row_newbcast:3 row_mask:0xf bank_mask:0xf\n"
"v_cvt_pk_bf16_f32 v64, v64, v65\n"
"v_cvt_pk_bf16_f32 v65, v66, v67\n"
"v_exp_f32 v174, v174\n"
"v_mfma_f32_16x16x32_bf16 v[48:51], v[104:107], a[244:247], v[48:51]\n"
"ds_read_b128 v[28:31], v244 offset:33792\n"
"v_mul_f32_dpp v4, v246, v4 row_newbcast:0 row_mask:0xf bank_mask:0xf\n"
"v_mfma_f32_16x16x32_bf16 v[48:51], v[108:111], a[248:251], v[48:51]\n"
"ds_write_b128 v248, v[72:75] offset:51200\n"
"v_cvt_pk_bf16_f32 v126, v152, v153\n"
"v_cvt_pk_bf16_f32 v132, v156, v157\n"
"v_exp_f32 v165, v165\n"
"v_mfma_f32_16x16x32_bf16 v[80:83], v[112:115], a[244:247], v[80:83]\n"
"ds_read_b128 v[208:211], v228 offset:16384\n"
"v_mul_f32_dpp v38, v247, v38 row_newbcast:2 row_mask:0xf bank_mask:0xf\n"
"v_mfma_f32_16x16x32_bf16 v[80:83], v[116:119], a[248:251], v[80:83]\n"
"ds_read_b64_tr_b16 a[76:77], v249 offset:50176\n"
"ds_read_b128 v[212:215], v229 offset:16384\n"
"v_exp_f32 v159, v159\n"
"v_cvt_pk_bf16_f32 v33, v34, v35\n"
"v_exp_f32 v168, v168\n"
"v_exp_f32 v173, v173\n"
"v_mfma_f32_16x16x32_bf16 v[84:87], v[116:119], v[224:227], v[84:87]\n"
"ds_read_b64_tr_b16 a[84:85], v249 offset:51200\n"
"ds_read_b64 v[250:251], v245 offset:52224\n"
"v_mfma_f32_16x16x32_bf16 v[84:87], v[112:115], a[252:255], v[84:87]\n"
"ds_read_b64_tr_b16 a[86:87], v249 offset:51208\n"
"ds_read_b128 v[220:223], v229 offset:17408\n"
"v_cvt_pk_bf16_f32 v90, v92, v93\n"
"v_exp_f32 v166, v166\n"
"v_mul_f32_dpp v5, v246, v5 row_newbcast:1 row_mask:0xf bank_mask:0xf\n"
"v_cvt_pk_bf16_f32 v66, v68, v69\n"
"v_exp_f32 v175, v175\n"
"v_mfma_f32_16x16x32_bf16 v[52:55], v[108:111], v[224:227], v[52:55]\n"
"v_cvt_pk_bf16_f32 v34, v36, v37\n"
"v_mul_f32_dpp v81, v206, v81 row_newbcast:1 row_mask:0xf bank_mask:0xf\n"
"v_mfma_f32_16x16x32_bf16 v[52:55], v[104:107], a[252:255], v[52:55]\n"
"ds_write_b128 v248, v[88:91] offset:51200\n"
"v_cvt_pk_bf16_f32 v133, v158, v159\n"
"v_exp_f32 v177, v177\n"
"v_exp_f32 v179, v179\n"
"v_mfma_f32_16x16x32_bf16 v[20:23], v[100:103], v[224:227], v[20:23]\n"
"ds_read_b128 v[24:27], v244 offset:32768\n"
"v_cvt_pk_bf16_f32 v2, v4, v5\n"
"v_mfma_f32_16x16x32_bf16 v[20:23], v[96:99], a[252:255], v[20:23]\n"
"s_waitcnt lgkmcnt(4)\n"
"ds_read_b64_tr_b16 a[62:63], v249 offset:49160\n"
"ds_read_b64_tr_b16 a[244:245], v234 offset:0\n"
"ds_read_b64_tr_b16 a[248:249], v238 offset:0\n"
"v_exp_f32 v167, v167\n"
"v_cvt_pk_bf16_f32 v3, v6, v7\n"
"v_cvt_pk_bf16_f32 v35, v38, v39\n"
"v_exp_f32 v178, v178\n"
"v_min_f32 v254, v252, v250\n"
"v_mfma_f32_16x16x32_bf16 a[228:231], v[120:123], a[252:255], a[228:231]\n"
"ds_read_b64_tr_b16 a[92:93], v249 offset:51200\n"
"ds_read_b64_tr_b16 a[94:95], v249 offset:51208\n"
"v_mfma_f32_16x16x32_bf16 v[152:155], v[208:211], a[0:3], v[180:183]\n"
"ds_read_b128 v[60:63], v244 offset:35840\n"
"v_cvt_pk_bf16_f32 v67, v70, v71\n"
"v_mfma_f32_16x16x32_bf16 v[152:155], v[212:215], a[4:7], v[152:155]\n"
"ds_write_b128 v248, v[64:67] offset:51200\n"
"v_exp_f32 v169, v169\n"
"v_mul_f32_dpp v48, v247, v48 row_newbcast:0 row_mask:0xf bank_mask:0xf\n"
"v_mul_f32_dpp v84, v206, v84 row_newbcast:0 row_mask:0xf bank_mask:0xf\n"
"v_mov_b64 v[198:199], 0\n"
"v_mfma_f32_16x16x32_bf16 v[152:155], v[216:219], a[8:11], v[152:155]\n"
"s_waitcnt lgkmcnt(9)\n"
"ds_write_b128 v248, v[32:35] offset:50176\n"
"v_cvt_pk_bf16_f32 v141, v162, v163\n"
"v_cvt_pk_bf16_f32 v149, v166, v167\n"
"v_exp_f32 v170, v170\n"
"v_sub_f32 v252, v254, v252\n"
"v_mfma_f32_16x16x32_bf16 v[152:155], v[220:223], a[12:15], v[152:155]\n"
"ds_write_b128 v248, v[0:3] offset:49152\n"
"v_sub_f32 v250, v254, v250\n"
"v_exp_f32 v250, v250\n"
"v_mul_f32_dpp v50, v247, v50 row_newbcast:2 row_mask:0xf bank_mask:0xf\n"
"v_mfma_f32_16x16x32_bf16 v[156:159], v[216:219], a[24:27], v[184:187]\n"
"ds_read_b64_tr_b16 a[64:65], v249 offset:50176\n"
"ds_read_b128 v[56:59], v244 offset:34816\n"
"v_mfma_f32_16x16x32_bf16 v[156:159], v[220:223], a[28:31], v[156:159]\n"
"ds_read_b64_tr_b16 a[80:81], v249 offset:51200\n"
"v_mul_f32_dpp v17, v246, v17 row_newbcast:1 row_mask:0xf bank_mask:0xf\n"
"v_mfma_f32_16x16x32_bf16 v[156:159], v[208:211], a[16:19], v[156:159]\n"
"ds_read_b64_tr_b16 a[246:247], v234 offset:4096\n"
"v_cvt_pk_bf16_f32 v134, v168, v169\n"
"v_mfma_f32_16x16x32_bf16 v[156:159], v[212:215], a[20:23], v[156:159]\n"
"ds_read_b64_tr_b16 a[48:49], v249 offset:49152\n"
"ds_read_b64_tr_b16 a[50:51], v249 offset:49160\n"
"ds_read_b64_tr_b16 a[66:67], v249 offset:50184\n"
"ds_read_b64_tr_b16 a[82:83], v249 offset:51208\n"
"v_exp_f32 v176, v176\n"
"v_exp_f32 v252, v252\n"
"v_exp_f32 v153, v153\n"
"v_mfma_f32_16x16x32_bf16 v[160:163], v[208:211], a[32:35], v[188:191]\n"
"v_cvt_pk_bf16_f32 v142, v172, v173\n"
"v_accvgpr_read_b32 v254, a208\n"
"v_mfma_f32_16x16x32_bf16 v[160:163], v[212:215], a[36:39], v[160:163]\n"
"ds_read_b64_tr_b16 a[250:251], v238 offset:4096\n"
"v_cvt_pk_bf16_f32 v148, v164, v165\n"
"v_mfma_f32_16x16x32_bf16 v[160:163], v[216:219], a[40:43], v[160:163]\n"
"ds_read_b64_tr_b16 a[252:253], v235 offset:0\n"
"ds_read_b64_tr_b16 a[254:255], v235 offset:4096\n"
"v_mfma_f32_16x16x32_bf16 v[160:163], v[220:223], a[44:47], v[160:163]\n"
"v_exp_f32 v159, v159\n"
"v_mfma_f32_16x16x32_bf16 v[164:167], v[216:219], a[200:203], v[192:195]\n"
"ds_read_b64_tr_b16 v[226:227], v239 offset:4096\n"
"v_mul_f32 v253, v253, v252\n"
"v_mfma_f32_16x16x32_bf16 v[164:167], v[220:223], a[204:207], v[164:167]\n"
"v_cvt_pk_bf16_f32 v135, v170, v171\n"
"v_fmac_f32 v253, v251, v250\n"
"v_mfma_f32_16x16x32_bf16 v[164:167], v[208:211], a[192:195], v[164:167]\n"
"ds_read_b128 v[220:223], v229 offset:21504\n"
"v_cvt_pk_bf16_f32 v150, v176, v177\n"
"v_mfma_f32_16x16x32_bf16 v[164:167], v[212:215], a[196:199], v[164:167]\n"
"s_waitcnt lgkmcnt(5)\n"
"ds_read_b64_tr_b16 v[224:225], v239 offset:0\n"
"v_mfma_f32_16x16x32_bf16 a[216:219], v[124:127], a[244:247], 0\n"
"v_rcp_f32 v253, v253\n"
"v_mfma_f32_16x16x32_bf16 v[8:11], v[128:131], a[244:247], 0\n"
"s_waitcnt lgkmcnt(5)\n"
"s_add_u32 m0, s44, 98304\n"
"v_exp_f32 v154, v154\n"
"v_mfma_f32_16x16x32_bf16 v[8:11], v[132:135], a[248:251], v[8:11]\n"
"buffer_load_dwordx4 v240, s[20:23], s56 offen nt lds\n"
"v_cvt_pk_bf16_f32 v143, v174, v175\n"
"v_mul_f32 v250, v250, v253\n"
"v_mul_f32_dpp v49, v247, v49 row_newbcast:1 row_mask:0xf bank_mask:0xf\n"
"v_exp_f32 v152, v152\n"
"v_exp_f32 v164, v164\n"
"v_mfma_f32_16x16x32_bf16 v[40:43], v[136:139], a[244:247], 0\n"
"v_mul_f32 v252, v252, v253\n"
"v_mul_f32_dpp v16, v246, v16 row_newbcast:0 row_mask:0xf bank_mask:0xf\n"
"v_mfma_f32_16x16x32_bf16 v[40:43], v[140:143], a[248:251], v[40:43]\n"
"v_cvt_pk_bf16_f32 v151, v178, v179\n"
"v_mov_b32_dpp v250, v250 row_ror:12 row_mask:0x2 bank_mask:0xf\n"
"v_mov_b32_dpp v252, v252 row_ror:12 row_mask:0x2 bank_mask:0xf\n"
"v_accvgpr_read_b32 v253, a210\n"
"v_mul_f32_dpp v22, v246, v22 row_newbcast:2 row_mask:0xf bank_mask:0xf\n"
"v_exp_f32 v155, v155\n"
"v_exp_f32 v162, v162\n"
"v_mfma_f32_16x16x32_bf16 v[72:75], v[144:147], a[244:247], 0\n"
"ds_read_b128 v[212:215], v229 offset:20480\n"
"v_cvt_pk_bf16_f32 v97, v154, v155\n"
"v_mfma_f32_16x16x32_bf16 v[72:75], v[148:151], a[248:251], v[72:75]\n"
"s_waitcnt lgkmcnt(1)\n"
"ds_read_b64_tr_b16 a[246:247], v234 offset:5120\n"
"ds_read_b64_tr_b16 a[248:249], v238 offset:1024\n"
"v_mfma_f32_16x16x32_bf16 v[76:79], v[148:151], v[224:227], 0\n"
"v_mov_b32_dpp v252, v252 row_ror:8 row_mask:0x4 bank_mask:0xf\n"
"v_mul_f32_dpp v20, v246, v20 row_newbcast:0 row_mask:0xf bank_mask:0xf\n"
"v_mfma_f32_16x16x32_bf16 v[76:79], v[144:147], a[252:255], v[76:79]\n"
"ds_read_b128 v[216:219], v228 offset:21504\n"
"v_mov_b32_dpp v250, v250 row_ror:8 row_mask:0x4 bank_mask:0xf\n"
"v_mov_b32_dpp v252, v252 row_ror:4 row_mask:0x8 bank_mask:0xf\n"
"v_mov_b64 v[196:197], 0\n"
"v_exp_f32 v156, v156\n"
"v_exp_f32 v158, v158\n"
"v_exp_f32 v165, v165\n"
"v_mfma_f32_16x16x32_bf16 v[44:47], v[140:143], v[224:227], 0\n"
"v_mov_b32_dpp v250, v250 row_ror:4 row_mask:0x8 bank_mask:0xf\n"
"v_mul_f32_dpp v18, v246, v18 row_newbcast:2 row_mask:0xf bank_mask:0xf\n"
"v_mfma_f32_16x16x32_bf16 v[44:47], v[136:139], a[252:255], v[44:47]\n"
"v_mul_f32_dpp v24, v250, v24 row_newbcast:0 row_mask:0xf bank_mask:0xf\n"
"v_accvgpr_read_b32 v255, a209\n"
"v_mul_f32_dpp v25, v250, v25 row_newbcast:1 row_mask:0xf bank_mask:0xf\n"
"v_mul_f32_dpp v26, v250, v26 row_newbcast:2 row_mask:0xf bank_mask:0xf\n"
"v_mul_f32_dpp v19, v246, v19 row_newbcast:3 row_mask:0xf bank_mask:0xf\n"
"v_mul_f32_dpp v80, v206, v80 row_newbcast:0 row_mask:0xf bank_mask:0xf\n"
"v_mul_f32_dpp v85, v206, v85 row_newbcast:1 row_mask:0xf bank_mask:0xf\n"
"v_mul_f32_dpp v59, v250, v59 row_newbcast:3 row_mask:0xf bank_mask:0xf\n"
"v_mul_f32_dpp v61, v250, v61 row_newbcast:1 row_mask:0xf bank_mask:0xf\n"
"v_mov_b64 v[200:201], 0\n"
"v_mfma_f32_16x16x32_bf16 v[12:15], v[132:135], v[224:227], 0\n"
"s_add_u32 s40, s56, s52\n"
"ds_read_b128 v[208:211], v228 offset:20480\n"
"v_mul_f32_dpp v30, v250, v30 row_newbcast:2 row_mask:0xf bank_mask:0xf\n"
"v_mfma_f32_16x16x32_bf16 v[12:15], v[128:131], a[252:255], v[12:15]\n"
"buffer_load_dwordx4 v240, s[20:23], s40 offen offset:1024 nt lds\n"
"v_accvgpr_read_b32 v251, a211\n"
"v_mul_f32_dpp v27, v250, v27 row_newbcast:3 row_mask:0xf bank_mask:0xf\n"
"v_fmac_f32_dpp v24, v252, v254 row_newbcast:0 row_mask:0xf bank_mask:0xf\n"
"v_fmac_f32_dpp v25, v252, v255 row_newbcast:1 row_mask:0xf bank_mask:0xf\n"
"v_fmac_f32_dpp v26, v252, v253 row_newbcast:2 row_mask:0xf bank_mask:0xf\n"
"v_accvgpr_read_b32 v253, a214\n"
"v_cvt_pk_bf16_f32 v16, v16, v17\n"
"v_mfma_f32_16x16x32_bf16 a[220:223], v[124:127], a[252:255], 0\n"
"v_mul_f32_dpp v53, v247, v53 row_newbcast:1 row_mask:0xf bank_mask:0xf\n"
"v_mul_f32_dpp v57, v250, v57 row_newbcast:1 row_mask:0xf bank_mask:0xf\n"
"v_smfmac_f32_16x16x64_bf16 v[196:199], a[240:243], v[128:135], v241\n"
"v_fmac_f32_dpp v27, v252, v251 row_newbcast:3 row_mask:0xf bank_mask:0xf\n"
"v_accvgpr_read_b32 v254, a212\n"
"v_mul_f32_dpp v28, v250, v28 row_newbcast:0 row_mask:0xf bank_mask:0xf\n"
"v_accvgpr_read_b32 v255, a213\n"
"v_accvgpr_read_b32 v251, a215\n"
"v_mul_f32_dpp v31, v250, v31 row_newbcast:3 row_mask:0xf bank_mask:0xf\n"
"v_cvt_pk_bf16_f32 v24, v24, v25\n"
"v_mul_f32_dpp v54, v247, v54 row_newbcast:2 row_mask:0xf bank_mask:0xf\n"
"v_mul_f32_dpp v58, v250, v58 row_newbcast:2 row_mask:0xf bank_mask:0xf\n"
"v_mul_f32_dpp v60, v250, v60 row_newbcast:0 row_mask:0xf bank_mask:0xf\n"
"v_mfma_f32_16x16x32_bf16 v[202:205], a[240:243], v[124:127], v[202:205]\n"
"s_waitcnt lgkmcnt(1)\n"
"ds_read_b64_tr_b16 a[244:245], v234 offset:1024\n"
"v_mul_f32_dpp v29, v250, v29 row_newbcast:1 row_mask:0xf bank_mask:0xf\n"
"v_fmac_f32_dpp v28, v252, v254 row_newbcast:0 row_mask:0xf bank_mask:0xf\n"
"v_fmac_f32_dpp v30, v252, v253 row_newbcast:2 row_mask:0xf bank_mask:0xf\n"
"v_mul_f32_dpp v56, v250, v56 row_newbcast:0 row_mask:0xf bank_mask:0xf\n"
"v_mul_f32_dpp v63, v250, v63 row_newbcast:3 row_mask:0xf bank_mask:0xf\n"
"v_exp_f32 v157, v157\n"
"v_exp_f32 v160, v160\n"
"v_mfma_f32_16x16x32_bf16 v[168:171], v[216:219], a[8:11], v[180:183]\n"
"v_fmac_f32_dpp v31, v252, v251 row_newbcast:3 row_mask:0xf bank_mask:0xf\n"
"v_mul_f32_dpp v83, v206, v83 row_newbcast:3 row_mask:0xf bank_mask:0xf\n"
"v_mfma_f32_16x16x32_bf16 v[168:171], v[220:223], a[12:15], v[168:171]\n"
"s_waitcnt lgkmcnt(1)\n"
"ds_read_b64_tr_b16 a[250:251], v238 offset:5120\n"
"v_accvgpr_read_b32 v251, a227\n"
"v_mfma_f32_16x16x32_bf16 v[168:171], v[208:211], a[0:3], v[168:171]\n"
"v_fmac_f32_dpp v29, v252, v255 row_newbcast:1 row_mask:0xf bank_mask:0xf\n"
"v_accvgpr_read_b32 v255, a225\n"
"v_mfma_f32_16x16x32_bf16 v[168:171], v[212:215], a[4:7], v[168:171]\n"
"ds_read_b64_tr_b16 v[226:227], v239 offset:5120\n"
"v_accvgpr_read_b32 v254, a224\n"
"v_fmac_f32_dpp v57, v252, v255 row_newbcast:1 row_mask:0xf bank_mask:0xf\n"
"v_fmac_f32_dpp v59, v252, v251 row_newbcast:3 row_mask:0xf bank_mask:0xf\n"
"v_cvt_pk_bf16_f32 v96, v152, v153\n"
"v_exp_f32 v161, v161\n"
"v_exp_f32 v163, v163\n"
"v_mfma_f32_16x16x32_bf16 v[172:175], v[208:211], a[16:19], v[184:187]\n"
"v_accvgpr_read_b32 v253, a226\n"
"v_accvgpr_read_b32 v255, a229\n"
"v_mfma_f32_16x16x32_bf16 v[172:175], v[212:215], a[20:23], v[172:175]\n"
"ds_read_b64_tr_b16 v[224:225], v239 offset:1024\n"
"v_fmac_f32_dpp v58, v252, v253 row_newbcast:2 row_mask:0xf bank_mask:0xf\n"
"v_mfma_f32_16x16x32_bf16 v[172:175], v[216:219], a[24:27], v[172:175]\n"
"s_add_u32 s40, s56, s53\n"
"v_fmac_f32_dpp v56, v252, v254 row_newbcast:0 row_mask:0xf bank_mask:0xf\n"
"v_accvgpr_read_b32 v251, a231\n"
"v_mfma_f32_16x16x32_bf16 v[172:175], v[220:223], a[28:31], v[172:175]\n"
"buffer_load_dwordx4 v240, s[20:23], s40 offen offset:2048 nt lds\n"
"v_cvt_pk_bf16_f32 v25, v26, v27\n"
"v_cvt_pk_bf16_f32 v26, v28, v29\n"
"v_cvt_pk_bf16_f32 v27, v30, v31\n"
"v_mul_f32_dpp v82, v206, v82 row_newbcast:2 row_mask:0xf bank_mask:0xf\n"
"v_mul_f32_dpp v62, v250, v62 row_newbcast:2 row_mask:0xf bank_mask:0xf\n"
"v_cvt_pk_bf16_f32 v56, v56, v57\n"
"v_exp_f32 v170, v170\n"
"v_mfma_f32_16x16x32_bf16 v[176:179], v[216:219], a[40:43], v[188:191]\n"
"v_accvgpr_read_b32 v253, a230\n"
"v_cvt_pk_bf16_f32 v113, v162, v163\n"
"v_mfma_f32_16x16x32_bf16 v[176:179], v[220:223], a[44:47], v[176:179]\n"
"v_accvgpr_read_b32 v254, a228\n"
"v_cvt_pk_bf16_f32 v57, v58, v59\n"
"v_mfma_f32_16x16x32_bf16 v[176:179], v[208:211], a[32:35], v[176:179]\n"
"ds_read_b64_tr_b16 a[254:255], v235 offset:5120\n"
"v_fmac_f32_dpp v63, v252, v251 row_newbcast:3 row_mask:0xf bank_mask:0xf\n"
"v_mfma_f32_16x16x32_bf16 v[176:179], v[212:215], a[36:39], v[176:179]\n"
"ds_write_b128 v243, v[24:27] offset:0\n"
"v_cvt_pk_bf16_f32 v80, v80, v81\n"
"v_fmac_f32_dpp v60, v252, v254 row_newbcast:0 row_mask:0xf bank_mask:0xf\n"
"v_fmac_f32_dpp v61, v252, v255 row_newbcast:1 row_mask:0xf bank_mask:0xf\n"
"v_fmac_f32_dpp v62, v252, v253 row_newbcast:2 row_mask:0xf bank_mask:0xf\n"
"v_cvt_pk_bf16_f32 v59, v62, v63\n"
"v_mfma_f32_16x16x32_bf16 v[152:155], v[208:211], a[192:195], v[192:195]\n"
"v_cvt_pk_bf16_f32 v48, v48, v49\n"
"v_cvt_pk_bf16_f32 v58, v60, v61\n"
"v_mfma_f32_16x16x32_bf16 v[152:155], v[212:215], a[196:199], v[152:155]\n"
"ds_write_b128 v243, v[56:59] offset:2048\n"
"v_exp_f32 v167, v167\n"
"v_exp_f32 v171, v171\n"
"v_mfma_f32_16x16x32_bf16 v[152:155], v[216:219], a[200:203], v[152:155]\n"
"v_mul_f32_dpp v86, v206, v86 row_newbcast:2 row_mask:0xf bank_mask:0xf\n"
"v_cvt_pk_bf16_f32 v112, v160, v161\n"
"v_mfma_f32_16x16x32_bf16 v[152:155], v[220:223], a[204:207], v[152:155]\n"
"s_waitcnt lgkmcnt(3)\n"
"ds_read_b64_tr_b16 a[252:253], v235 offset:1024\n"
"v_mul_f32_dpp v55, v247, v55 row_newbcast:3 row_mask:0xf bank_mask:0xf\n"
"v_mfma_f32_16x16x32_bf16 a[232:235], v[124:127], a[244:247], 0\n"
"ds_read_b128 v[208:211], v230 offset:16384\n"
"v_cvt_pk_bf16_f32 v81, v82, v83\n"
"v_mfma_f32_16x16x32_bf16 v[24:27], v[128:131], a[244:247], 0\n"
"s_add_u32 s40, s56, s54\n"
"v_exp_f32 v166, v166\n"
"v_mfma_f32_16x16x32_bf16 v[24:27], v[132:135], a[248:251], v[24:27]\n"
"buffer_load_dwordx4 v240, s[20:23], s40 offen offset:3072 nt lds\n"
"v_mfma_f32_16x16x32_bf16 v[56:59], v[136:139], a[244:247], 0\n"
"s_add_u32 s55, s55, s59\n"
"v_mul_f32_dpp v51, v247, v51 row_newbcast:3 row_mask:0xf bank_mask:0xf\n"
"v_cvt_pk_bf16_f32 v105, v158, v159\n"
"v_mfma_f32_16x16x32_bf16 v[56:59], v[140:143], a[248:251], v[56:59]\n"
"s_add_u32 s41, s55, s57\n"
"v_exp_f32 v168, v168\n"
"v_mfma_f32_16x16x32_bf16 v[88:91], v[144:147], a[244:247], 0\n"
"s_and_b32 s41, s41, s58\n"
"v_exp_f32 v172, v172\n"
"v_mfma_f32_16x16x32_bf16 v[88:91], v[148:151], a[248:251], v[88:91]\n"
"s_andn2_b32 s56, s55, s58\n"
"ds_read_b128 v[212:215], v231 offset:16384\n"
"v_cvt_pk_bf16_f32 v104, v156, v157\n"
"v_mfma_f32_16x16x32_bf16 v[92:95], v[148:151], v[224:227], 0\n"
"s_waitcnt lgkmcnt(2)\n"
"s_or_b32 s56, s56, s41\n"
"v_exp_f32 v169, v169\n"
"v_mfma_f32_16x16x32_bf16 v[92:95], v[144:147], a[252:255], v[92:95]\n"
"ds_read_b128 v[216:219], v230 offset:17408\n"
"v_cvt_pk_bf16_f32 v49, v50, v51\n"
"v_mfma_f32_16x16x32_bf16 v[60:63], v[140:143], v[224:227], 0\n"
"s_add_u32 m0, s44, 49152\n"
"ds_read_b128 v[220:223], v231 offset:17408\n"
"v_mul_f32_dpp v21, v246, v21 row_newbcast:1 row_mask:0xf bank_mask:0xf\n"
"v_mfma_f32_16x16x32_bf16 v[60:63], v[136:139], a[252:255], v[60:63]\n"
"buffer_load_dwordx4 v240, s[16:19], s56 offen nt lds\n"
"v_mfma_f32_16x16x32_bf16 v[28:31], v[132:135], v[224:227], 0\n"
"v_xor_b32 v248, s75, v248\n"
"v_cvt_pk_bf16_f32 v121, v166, v167\n"
"v_mfma_f32_16x16x32_bf16 v[28:31], v[128:131], a[252:255], v[28:31]\n"
"v_cvt_pk_bf16_f32 v17, v18, v19\n"
"v_cvt_pk_bf16_f32 v120, v164, v165\n"
"v_mfma_f32_16x16x32_bf16 a[236:239], v[124:127], a[252:255], 0\n"
"ds_read_b64_tr_b16 a[248:249], v236 offset:0\n"
"v_exp_f32 v173, v173\n"
"v_exp_f32 v174, v174\n"
"v_exp_f32 v178, v178\n"
"v_smfmac_f32_16x16x64_bf16 v[198:201], a[240:243], v[136:143], v241\n"
"s_waitcnt lgkmcnt(3)\n"
"ds_read_b64_tr_b16 v[224:225], v237 offset:0\n"
"v_mul_f32_dpp v23, v246, v23 row_newbcast:3 row_mask:0xf bank_mask:0xf\n"
"v_mfma_f32_16x16x32_bf16 v[156:159], v[208:211], a[0:3], v[180:183]\n"
"ds_read_b64_tr_b16 a[244:245], v232 offset:0\n"
"ds_read_b64_tr_b16 v[226:227], v237 offset:4096\n"
"v_mfma_f32_16x16x32_bf16 v[156:159], v[212:215], a[4:7], v[156:159]\n"
"s_waitcnt lgkmcnt(5)\n"
"v_cvt_pk_bf16_f32 v18, v20, v21\n"
"v_cvt_pk_bf16_f32 v106, v172, v173\n"
"v_mfma_f32_16x16x32_bf16 v[156:159], v[216:219], a[8:11], v[156:159]\n"
"s_waitcnt lgkmcnt(4)\n"
"ds_read_b64_tr_b16 a[254:255], v233 offset:4096\n"
"v_cvt_pk_bf16_f32 v98, v168, v169\n"
"v_mfma_f32_16x16x32_bf16 v[156:159], v[220:223], a[12:15], v[156:159]\n"
"ds_read_b64_tr_b16 a[246:247], v232 offset:4096\n"
"v_mul_f32_dpp v52, v247, v52 row_newbcast:0 row_mask:0xf bank_mask:0xf\n"
"v_mfma_f32_16x16x32_bf16 v[160:163], v[216:219], a[24:27], v[184:187]\n"
"v_exp_f32 v155, v155\n"
"v_mfma_f32_16x16x32_bf16 v[160:163], v[220:223], a[28:31], v[160:163]\n"
"ds_read_b64_tr_b16 a[250:251], v236 offset:4096\n"
"v_cvt_pk_bf16_f32 v19, v22, v23\n"
"v_mfma_f32_16x16x32_bf16 v[160:163], v[208:211], a[16:19], v[160:163]\n"
"s_add_u32 s40, s56, s52\n"
"v_xor_b32 v249, s75, v249\n"
"v_mul_f32_dpp v87, v206, v87 row_newbcast:3 row_mask:0xf bank_mask:0xf\n"
"v_mfma_f32_16x16x32_bf16 v[160:163], v[212:215], a[20:23], v[160:163]\n"
"buffer_load_dwordx4 v240, s[16:19], s40 offen offset:1024 nt lds\n"
"v_mfma_f32_16x16x32_bf16 v[164:167], v[208:211], a[32:35], v[188:191]\n"
"v_exp_f32 v158, v158\n"
"v_mfma_f32_16x16x32_bf16 v[164:167], v[212:215], a[36:39], v[164:167]\n"
"v_cvt_pk_bf16_f32 v51, v54, v55\n"
"v_cvt_pk_bf16_f32 v99, v170, v171\n"
"v_mfma_f32_16x16x32_bf16 v[164:167], v[216:219], a[40:43], v[164:167]\n"
"ds_read_b64_tr_b16 a[252:253], v233 offset:0\n"
"v_cvt_pk_bf16_f32 v50, v52, v53\n"
"v_mfma_f32_16x16x32_bf16 v[164:167], v[220:223], a[44:47], v[164:167]\n"
"s_waitcnt lgkmcnt(2)\n"
"ds_write_b128 v248, v[16:19] offset:49152\n"
"v_exp_f32 v177, v177\n"
"v_exp_f32 v153, v153\n"
"v_mfma_f32_16x16x32_bf16 a[208:211], v[124:127], a[244:247], 0\n"
"ds_write_b128 v248, v[48:51] offset:50176\n"
"v_exp_f32 v154, v154\n"
"v_mfma_f32_16x16x32_bf16 v[0:3], v[128:131], a[244:247], 0\n"
"s_waitcnt lgkmcnt(3)\n"
"v_exp_f32 v179, v179\n"
"v_mfma_f32_16x16x32_bf16 v[0:3], v[132:135], a[248:251], v[0:3]\n"
"ds_read_b128 v[208:211], v230 offset:20480\n"
"v_cvt_pk_bf16_f32 v83, v86, v87\n"
"v_mfma_f32_16x16x32_bf16 v[32:35], v[136:139], a[244:247], 0\n"
"s_add_u32 s40, s56, s53\n"
"ds_read_b128 v[216:219], v230 offset:21504\n"
"v_cvt_pk_bf16_f32 v82, v84, v85\n"
"v_mfma_f32_16x16x32_bf16 v[32:35], v[140:143], a[248:251], v[32:35]\n"
"buffer_load_dwordx4 v240, s[16:19], s40 offen offset:2048 nt lds\n"
"v_mfma_f32_16x16x32_bf16 v[64:67], v[144:147], a[244:247], 0\n"
"v_exp_f32 v166, v166\n"
"v_mfma_f32_16x16x32_bf16 v[64:67], v[148:151], a[248:251], v[64:67]\n"
"v_exp_f32 v163, v163\n"
"v_mfma_f32_16x16x32_bf16 v[68:71], v[148:151], v[224:227], 0\n"
"s_waitcnt lgkmcnt(3)\n"
"ds_read_b64_tr_b16 a[58:59], v249 offset:49160\n"
"ds_read_b128 v[212:215], v231 offset:20480\n"
"v_mfma_f32_16x16x32_bf16 v[68:71], v[144:147], a[252:255], v[68:71]\n"
"v_exp_f32 v152, v152\n"
"v_mfma_f32_16x16x32_bf16 v[36:39], v[140:143], v[224:227], 0\n"
"v_exp_f32 v162, v162\n"
"v_mfma_f32_16x16x32_bf16 v[36:39], v[136:139], a[252:255], v[36:39]\n"
"ds_read_b128 v[220:223], v231 offset:21504\n"
"v_exp_f32 v175, v175\n"
"v_exp_f32 v176, v176\n"
"v_exp_f32 v164, v164\n"
"v_exp_f32 v165, v165\n"
"v_mfma_f32_16x16x32_bf16 v[4:7], v[132:135], v[224:227], 0\n"
"ds_read_b64_tr_b16 a[56:57], v249 offset:49152\n"
"ds_read_b64_tr_b16 a[248:249], v236 offset:1024\n"
"v_mfma_f32_16x16x32_bf16 v[4:7], v[128:131], a[252:255], v[4:7]\n"
"ds_read_b64_tr_b16 a[74:75], v249 offset:50184\n"
"v_cvt_pk_bf16_f32 v107, v174, v175\n"
"v_mfma_f32_16x16x32_bf16 a[212:215], v[124:127], a[252:255], 0\n"
"s_add_u32 s40, s56, s54\n"
"ds_write_b128 v248, v[80:83] offset:51200\n"
"v_exp_f32 v161, v161\n"
"v_smfmac_f32_16x16x64_bf16 v[200:203], a[240:243], v[144:151], v241\n"
"s_waitcnt lgkmcnt(7)\n"
"buffer_load_dwordx4 v240, s[16:19], s40 offen offset:3072 nt lds\n"
"v_mfma_f32_16x16x32_bf16 v[168:171], v[208:211], a[0:3], v[180:183]\n"
"s_waitcnt lgkmcnt(5)\n"
"v_cvt_pk_bf16_f32 v114, v176, v177\n"
"v_add_u32 v243, 0x1000, v243\n"
"v_mfma_f32_16x16x32_bf16 v[168:171], v[212:215], a[4:7], v[168:171]\n"
"ds_read_b64_tr_b16 a[88:89], v249 offset:51200\n"
"ds_read_b64_tr_b16 a[244:245], v232 offset:1024\n"
"v_mfma_f32_16x16x32_bf16 v[168:171], v[216:219], a[8:11], v[168:171]\n"
"s_waitcnt lgkmcnt(6)\n"
"ds_read_b64_tr_b16 a[72:73], v249 offset:50176\n"
"ds_read_b64_tr_b16 a[90:91], v249 offset:51208\n"
"v_mfma_f32_16x16x32_bf16 v[168:171], v[220:223], a[12:15], v[168:171]\n"
"v_exp_f32 v167, v167\n"
"v_mfma_f32_16x16x32_bf16 v[172:175], v[216:219], a[24:27], v[184:187]\n"
"ds_read_b64_tr_b16 a[246:247], v232 offset:5120\n"
"v_cvt_pk_bf16_f32 v117, v166, v167\n"
"v_mfma_f32_16x16x32_bf16 v[172:175], v[220:223], a[28:31], v[172:175]\n"
"ds_read_b64_tr_b16 a[250:251], v236 offset:5120\n"
"v_cvt_pk_bf16_f32 v115, v178, v179\n"
"v_mfma_f32_16x16x32_bf16 v[172:175], v[208:211], a[16:19], v[172:175]\n"
"ds_read_b64_tr_b16 a[254:255], v233 offset:5120\n"
"ds_read_b64_tr_b16 v[224:225], v237 offset:1024\n"
"v_mfma_f32_16x16x32_bf16 v[172:175], v[212:215], a[20:23], v[172:175]\n"
"s_cbranch_vccnz LPCT1_%=\nLPKT1_%=:\n"
"v_exp_f32 v160, v160\n"
"v_mfma_f32_16x16x32_bf16 v[176:179], v[208:211], a[32:35], v[188:191]\n"
"ds_read_b64_tr_b16 a[252:253], v233 offset:1024\n"
"v_cvt_pk_bf16_f32 v116, v164, v165\n"
"v_mfma_f32_16x16x32_bf16 v[176:179], v[212:215], a[36:39], v[176:179]\n"
"v_exp_f32 v159, v159\n"
"v_mfma_f32_16x16x32_bf16 v[176:179], v[216:219], a[40:43], v[176:179]\n"
"v_exp_f32 v157, v157\n"
"v_mfma_f32_16x16x32_bf16 v[176:179], v[220:223], a[44:47], v[176:179]\n"
"s_waitcnt lgkmcnt(4)\n"
"v_cvt_pk_bf16_f32 v101, v158, v159\n"
"v_mfma_f32_16x16x32_bf16 a[224:227], v[124:127], a[244:247], 0\n"
"ds_read_b64_tr_b16 v[226:227], v237 offset:5120\n"
"v_cvt_pk_bf16_f32 v122, v152, v153\n"
"v_mfma_f32_16x16x32_bf16 v[16:19], v[128:131], a[244:247], 0\n"
"s_waitcnt vmcnt(8) lgkmcnt(4)\n"
"v_exp_f32 v156, v156\n"
"v_mfma_f32_16x16x32_bf16 v[16:19], v[132:135], a[248:251], v[16:19]\n"
"s_barrier\n"
"ds_read_b128 v[208:211], v228 offset:32768\n"
"v_exp_f32 v175, v175\n"
"v_mfma_f32_16x16x32_bf16 v[48:51], v[136:139], a[244:247], 0\n"
"ds_read_b128 v[220:223], v229 offset:33792\n"
"v_cvt_pk_bf16_f32 v100, v156, v157\n"
"v_mfma_f32_16x16x32_bf16 v[48:51], v[140:143], a[248:251], v[48:51]\n"
"ds_read_b128 v[212:215], v229 offset:32768\n"
"ds_read_b128 v[216:219], v228 offset:33792\n"
"v_mfma_f32_16x16x32_bf16 v[80:83], v[144:147], a[244:247], 0\n"
"ds_read_b64_tr_b16 a[244:245], v234 offset:16384\n"
"ds_read_b64_tr_b16 a[246:247], v234 offset:20480\n"
"v_mfma_f32_16x16x32_bf16 v[80:83], v[148:151], a[248:251], v[80:83]\n"
"s_waitcnt lgkmcnt(6)\n"
"ds_read_b64_tr_b16 a[248:249], v238 offset:16384\n"
"ds_read_b64_tr_b16 a[250:251], v238 offset:20480\n"
"v_mfma_f32_16x16x32_bf16 v[84:87], v[148:151], v[224:227], 0\n"
"v_xor_b32 v248, s75, v248\n"
"v_cvt_pk_bf16_f32 v108, v160, v161\n"
"v_mfma_f32_16x16x32_bf16 v[84:87], v[144:147], a[252:255], v[84:87]\n"
"v_exp_f32 v173, v173\n"
"v_mfma_f32_16x16x32_bf16 v[52:55], v[140:143], v[224:227], 0\n"
"v_exp_f32 v172, v172\n"
"v_mfma_f32_16x16x32_bf16 v[52:55], v[136:139], a[252:255], v[52:55]\n"
"v_cvt_pk_bf16_f32 v123, v154, v155\n"
"v_cvt_pk_bf16_f32 v109, v162, v163\n"
"v_mfma_f32_16x16x32_bf16 v[20:23], v[132:135], v[224:227], 0\n"
"ds_read_b64_tr_b16 v[224:225], v239 offset:16384\n"
"v_xor_b32 v249, s75, v249\n"
"v_mfma_f32_16x16x32_bf16 v[20:23], v[128:131], a[252:255], v[20:23]\n"
"v_exp_f32 v169, v169\n"
"v_mfma_f32_16x16x32_bf16 a[228:231], v[124:127], a[252:255], 0\n"
"s_waitcnt lgkmcnt(3)\n"
"ds_read_b64_tr_b16 a[254:255], v235 offset:20480\n"
"s_branch LAY2_%=\n"
"LAX_%=:\n"
"v_mfma_f32_16x16x32_bf16 a[224:227], v[124:127], a[244:247], a[224:227]\n"
"v_exp_f32 v169, v169\n"
"v_mfma_f32_16x16x32_bf16 v[16:19], v[128:131], a[244:247], v[16:19]\n"
"s_waitcnt vmcnt(8)\n"
"v_exp_f32 v171, v171\n"
"v_mfma_f32_16x16x32_bf16 v[16:19], v[132:135], a[248:251], v[16:19]\n"
"s_barrier\n"
"v_mfma_f32_16x16x32_bf16 v[48:51], v[136:139], a[244:247], v[48:51]\n"
"v_cvt_pk_bf16_f32 v108, v160, v161\n"
"v_cvt_pk_bf16_f32 v109, v162, v163\n"
"v_mfma_f32_16x16x32_bf16 v[48:51], v[140:143], a[248:251], v[48:51]\n"
"v_exp_f32 v168, v168\n"
"v_exp_f32 v170, v170\n"
"v_exp_f32 v177, v177\n"
"v_exp_f32 v179, v179\n"
"v_and_b32 v252, 0x7fff0000, v252\n"
"v_mfma_f32_16x16x32_bf16 v[80:83], v[144:147], a[244:247], v[80:83]\n"
"ds_read_b64_tr_b16 a[246:247], v234 offset:53248\n"
"v_cvt_pk_bf16_f32 v100, v156, v157\n"
"v_mfma_f32_16x16x32_bf16 v[80:83], v[148:151], a[248:251], v[80:83]\n"
"ds_read_b64_tr_b16 a[244:245], v234 offset:49152\n"
"v_accvgpr_read_b32 v253, a96\n"
"v_mfma_f32_16x16x32_bf16 v[84:87], v[148:151], v[224:227], v[84:87]\n"
"ds_read_b64_tr_b16 a[248:249], v238 offset:49152\n"
"v_cvt_pk_bf16_f32 v102, v168, v169\n"
"v_mfma_f32_16x16x32_bf16 v[84:87], v[144:147], a[252:255], v[84:87]\n"
"v_cvt_pk_bf16_f32 v116, v164, v165\n"
"v_cvt_pk_bf16_f32 v103, v170, v171\n"
"v_mfma_f32_16x16x32_bf16 v[52:55], v[140:143], v[224:227], v[52:55]\n"
"ds_read_b64_tr_b16 a[250:251], v238 offset:53248\n"
"v_lshlrev_b32_e64 v244, 24, s43\n"
"v_mfma_f32_16x16x32_bf16 v[52:55], v[136:139], a[252:255], v[52:55]\n"
"v_exp_f32 v172, v172\n"
"v_mfma_f32_16x16x32_bf16 v[20:23], v[132:135], v[224:227], v[20:23]\n"
"v_exp_f32 v176, v176\n"
"v_mfma_f32_16x16x32_bf16 v[20:23], v[128:131], a[252:255], v[20:23]\n"
"v_exp_f32 v174, v174\n"
"v_mfma_f32_16x16x32_bf16 a[228:231], v[124:127], a[252:255], a[228:231]\n"
"s_waitcnt lgkmcnt(2)\n"
"ds_read_b64_tr_b16 a[254:255], v235 offset:53248\n"
"v_cvt_pk_bf16_f32 v117, v166, v167\n"
"v_exp_f32 v173, v173\n"
"v_exp_f32 v175, v175\n"
"v_exp_f32 v178, v178\n"
"v_and_b32 v245, 0x3f0, v242\n"
"v_mfma_f32_16x16x32_bf16 a[216:219], v[120:123], a[244:247], a[216:219]\n"
"s_mov_b64 s[28:29], s[8:9]\n"
"v_cvt_pk_bf16_f32 v118, v176, v177\n"
"v_max_u32_dpp v252, v252, v252 quad_perm:[1,0,3,2] row_mask:0xf bank_mask:0xf\n"
"v_smfmac_f32_16x16x64_bf16 v[196:199], a[240:243], v[96:103], v241\n"
"s_mov_b32 s30, 655360\n"
"ds_read_b64_tr_b16 v[224:225], v239 offset:49152\n"
"v_cvt_pk_bf16_f32 v119, v178, v179\n"
"v_mfma_f32_16x16x32_bf16 v[8:11], v[96:99], a[244:247], v[8:11]\n"
"s_waitcnt lgkmcnt(2)\n"
"s_mov_b32 s71, s44\n"
"ds_read_b64_tr_b16 v[226:227], v239 offset:53248\n"
"v_cvt_pk_bf16_f32 v111, v174, v175\n"
"v_mfma_f32_16x16x32_bf16 v[8:11], v[100:103], a[248:251], v[8:11]\n"
"s_mov_b32 s70, s95\n"
"v_cvt_pk_bf16_f32 v110, v172, v173\n"
"v_max_u32_dpp v252, v252, v252 quad_perm:[2,3,0,1] row_mask:0xf bank_mask:0xf\n"
"v_smfmac_f32_16x16x64_bf16 v[200:203], a[240:243], v[112:119], v241\n"
"ds_read_b64_tr_b16 a[252:253], v235 offset:49152\n"
"v_lshrrev_b32 v245, 1, v245\n"
"v_mfma_f32_16x16x32_bf16 v[40:43], v[104:107], a[244:247], v[40:43]\n"
"v_and_b32 v253, 0x7fff0000, v253\n"
"v_mul_f32 v244, 0x45800000, v244\n"
"v_mfma_f32_16x16x32_bf16 v[40:43], v[108:111], a[248:251], v[40:43]\n"
"s_add_u32 s41, s70, 327680\n"
"s_add_u32 m0, s71, 32768\n"
"v_max_u32_dpp v253, v253, v253 quad_perm:[1,0,3,2] row_mask:0xf bank_mask:0xf\n"
"v_smfmac_f32_16x16x64_bf16 v[198:201], a[240:243], v[104:111], v241\n"
"buffer_load_dwordx4 v240, s[28:31], s41 offen lds\n"
"v_mfma_f32_16x16x32_bf16 v[72:75], v[112:115], a[244:247], v[72:75]\n"
"v_max_u32_dpp v253, v253, v253 quad_perm:[2,3,0,1] row_mask:0xf bank_mask:0xf\n"
"v_max_u32_dpp v252, v252, v252 row_half_mirror row_mask:0xf bank_mask:0xf\n"
"v_mfma_f32_16x16x32_bf16 v[72:75], v[116:119], a[248:251], v[72:75]\n"
"ds_read_b64_tr_b16 a[246:247], v234 offset:54272\n"
"ds_read_b64_tr_b16 a[248:249], v238 offset:50176\n"
"v_mfma_f32_16x16x32_bf16 v[202:205], a[240:243], v[120:123], v[202:205]\n"
"s_waitcnt lgkmcnt(3)\n"
"ds_read_b64_tr_b16 a[250:251], v238 offset:54272\n"
"v_add_f32 v196, v196, v197\n"
"v_mfma_f32_16x16x32_bf16 v[76:79], v[116:119], v[224:227], v[76:79]\n"
"s_waitcnt lgkmcnt(3)\n"
"s_add_u32 s40, s41, s52\n"
"ds_read_b64_tr_b16 a[244:245], v234 offset:50176\n"
"v_add_f32 v200, v200, v201\n"
"v_mfma_f32_16x16x32_bf16 v[76:79], v[112:115], a[252:255], v[76:79]\n"
"buffer_load_dwordx4 v240, s[28:31], s40 offen offset:1024 lds\n"
"v_mfma_f32_16x16x32_bf16 v[44:47], v[108:111], v[224:227], v[44:47]\n"
"v_min_u32_dpp v252, v252, v252 row_mirror row_mask:0xf bank_mask:0xf\n"
"v_add_f32 v244, v244, v202\n"
"v_mfma_f32_16x16x32_bf16 v[44:47], v[104:107], a[252:255], v[44:47]\n"
"v_add_f32 v198, v198, v199\n"
"v_min3_f32 v244, v244, v196, v198\n"
"v_mfma_f32_16x16x32_bf16 v[12:15], v[100:103], v[224:227], v[12:15]\n"
"s_add_u32 s40, s41, s53\n"
"ds_read_b64_tr_b16 v[226:227], v239 offset:54272\n"
"v_min_f32 v244, v244, v200\n"
"v_mfma_f32_16x16x32_bf16 v[12:15], v[96:99], a[252:255], v[12:15]\n"
"buffer_load_dwordx4 v240, s[28:31], s40 offen offset:2048 lds\n"
"v_max_u32_dpp v253, v253, v253 row_half_mirror row_mask:0xf bank_mask:0xf\n"
"v_add_f32 v254, v196, v198\n"
"v_add_f32 v254, v254, v200\n"
"v_add_f32 v254, v254, v202\n"
"v_rcp_f32 v244, v244\n"
"v_mfma_f32_16x16x32_bf16 a[220:223], v[120:123], a[252:255], a[220:223]\n"
"s_waitcnt lgkmcnt(1)\n"
"v_min_u32_dpp v253, v253, v253 row_mirror row_mask:0xf bank_mask:0xf\n"
"v_fmac_f32 v254, 0x68800000, v244\n"
"v_cmp_nge_f32 vcc, s45, v254\n"
"v_sub_u32 v245, v242, v245\n"
"v_bfe_u32 v254, v242, 9, 1\n"
"s_cbranch_vccnz LSLOW_%=\nLBACK3_%=:\ns_mov_b64 vcc, s[64:65]\n"
"v_mfma_f32_16x16x32_bf16 a[232:235], v[120:123], a[244:247], a[232:235]\n"
"ds_read_b64_tr_b16 a[252:253], v235 offset:50176\n"
"ds_read_b64_tr_b16 a[254:255], v235 offset:54272\n"
"ds_read_b64_tr_b16 v[224:225], v239 offset:50176\n"
"v_rcp_f32 v246, v196\n"
"v_rcp_f32 v247, v198\n"
"v_xor_b32 v244, 0x11000, v242\n"
"v_lshl_add_u32 v254, v254, 4, s43\n"
"v_mfma_f32_16x16x32_bf16 v[24:27], v[96:99], a[244:247], v[24:27]\n"
"v_rcp_f32 v206, v200\n"
"v_mfma_f32_16x16x32_bf16 v[24:27], v[100:103], a[248:251], v[24:27]\n"
"ds_write_b128 v244, a[220:223] offset:33792\n"
"v_min_u32_dpp v253, v253, v253 row_bcast:15 row_mask:0xa bank_mask:0xf\n"
"v_mov_b32_dpp v246, v246 row_ror:12 row_mask:0x2 bank_mask:0xf\n"
"v_mfma_f32_16x16x32_bf16 v[56:59], v[104:107], a[244:247], v[56:59]\n"
"s_add_u32 s40, s41, s54\n"
"v_mov_b32_dpp v247, v247 row_ror:12 row_mask:0x2 bank_mask:0xf\n"
"v_mov_b32_dpp v206, v206 row_ror:12 row_mask:0x2 bank_mask:0xf\n"
"v_mfma_f32_16x16x32_bf16 v[56:59], v[108:111], a[248:251], v[56:59]\n"
"buffer_load_dwordx4 v240, s[28:31], s40 offen offset:3072 lds\n"
"v_mfma_f32_16x16x32_bf16 v[88:91], v[112:115], a[244:247], v[88:91]\n"
"s_add_u32 s71, s44, s75\n"
"v_mov_b32_dpp v247, v247 row_ror:8 row_mask:0x4 bank_mask:0xf\n"
"v_mov_b32_dpp v206, v206 row_ror:8 row_mask:0x4 bank_mask:0xf\n"
"v_mfma_f32_16x16x32_bf16 v[88:91], v[116:119], a[248:251], v[88:91]\n"
"s_waitcnt lgkmcnt(1)\n"
"s_mov_b32 s70, s100\n"
"ds_read_b64_tr_b16 a[246:247], v232 offset:53248\n"
"ds_read_b64_tr_b16 a[248:249], v236 offset:49152\n"
"ds_read_b64_tr_b16 a[250:251], v236 offset:53248\n"
"v_mov_b32_dpp v246, v246 row_ror:8 row_mask:0x4 bank_mask:0xf\n"
"v_mov_b32_dpp v247, v247 row_ror:4 row_mask:0x8 bank_mask:0xf\n"
"v_mov_b32_dpp v206, v206 row_ror:4 row_mask:0x8 bank_mask:0xf\n"
"v_mfma_f32_16x16x32_bf16 v[92:95], v[116:119], v[224:227], v[92:95]\n"
"s_add_u32 s41, s70, 163840\n"
"s_add_u32 m0, s71, 16384\n"
"ds_read_b64_tr_b16 a[244:245], v232 offset:49152\n"
"v_mfma_f32_16x16x32_bf16 v[92:95], v[112:115], a[252:255], v[92:95]\n"
"buffer_load_dwordx4 v240, s[28:31], s41 offen lds\n"
"v_mov_b32_dpp v246, v246 row_ror:4 row_mask:0x8 bank_mask:0xf\n"
"v_mul_f32_dpp v41, v247, v41 row_newbcast:1 row_mask:0xf bank_mask:0xf\n"
"v_mul_f32_dpp v72, v206, v72 row_newbcast:0 row_mask:0xf bank_mask:0xf\n"
"v_mul_f32_dpp v77, v206, v77 row_newbcast:1 row_mask:0xf bank_mask:0xf\n"
"v_mfma_f32_16x16x32_bf16 v[60:63], v[108:111], v[224:227], v[60:63]\n"
"v_min_u32_dpp v252, v252, v252 row_bcast:15 row_mask:0xa bank_mask:0xf\n"
"v_mul_f32_dpp v76, v206, v76 row_newbcast:0 row_mask:0xf bank_mask:0xf\n"
"v_mfma_f32_16x16x32_bf16 v[60:63], v[104:107], a[252:255], v[60:63]\n"
"ds_write_b128 v244, a[232:235] offset:34816\n"
"v_min_u32_dpp v252, v252, v252 row_bcast:31 row_mask:0xc bank_mask:0xf\n"
"v_mul_f32 v252, 0x43340000, v252\n"
"v_lshl_add_u32 v254, v254, 6, s75\n"
"v_mul_f32_dpp v40, v247, v40 row_newbcast:0 row_mask:0xf bank_mask:0xf\n"
"v_mul_f32_dpp v24, v246, v24 row_newbcast:0 row_mask:0xf bank_mask:0xf\n"
"v_mfma_f32_16x16x32_bf16 v[28:31], v[100:103], v[224:227], v[28:31]\n"
"s_add_u32 s40, s41, s52\n"
"v_mul_f32_dpp v15, v246, v15 row_newbcast:3 row_mask:0xf bank_mask:0xf\n"
"v_mul_f32_dpp v73, v206, v73 row_newbcast:1 row_mask:0xf bank_mask:0xf\n"
"v_mfma_f32_16x16x32_bf16 v[28:31], v[96:99], a[252:255], v[28:31]\n"
"buffer_load_dwordx4 v240, s[28:31], s40 offen offset:1024 lds\n"
"v_min_u32_dpp v253, v253, v253 row_bcast:31 row_mask:0xc bank_mask:0xf\n"
"v_sub_u32 v229, v233, v254\n"
"v_mul_f32_dpp v9, v246, v9 row_newbcast:1 row_mask:0xf bank_mask:0xf\n"
"v_mul_f32_dpp v10, v246, v10 row_newbcast:2 row_mask:0xf bank_mask:0xf\n"
"v_mul_f32_dpp v12, v246, v12 row_newbcast:0 row_mask:0xf bank_mask:0xf\n"
"v_mfma_f32_16x16x32_bf16 a[236:239], v[120:123], a[252:255], a[236:239]\n"
"s_waitcnt lgkmcnt(1)\n"
"v_mul_f32_dpp v47, v247, v47 row_newbcast:3 row_mask:0xf bank_mask:0xf\n"
"v_mul_f32_dpp v90, v206, v90 row_newbcast:2 row_mask:0xf bank_mask:0xf\n"
"v_mfma_f32_16x16x32_bf16 a[208:211], v[120:123], a[244:247], a[208:211]\n"
"ds_write_b128 v244, a[216:219] offset:32768\n"
"v_sub_u32 v230, v234, v254\n"
"v_mul_f32_dpp v43, v247, v43 row_newbcast:3 row_mask:0xf bank_mask:0xf\n"
"v_mul_f32_dpp v45, v247, v45 row_newbcast:1 row_mask:0xf bank_mask:0xf\n"
"v_mul_f32_dpp v79, v206, v79 row_newbcast:3 row_mask:0xf bank_mask:0xf\n"
"v_cvt_pk_bf16_f32 v72, v72, v73\n"
"v_mfma_f32_16x16x32_bf16 v[0:3], v[96:99], a[244:247], v[0:3]\n"
"ds_read_b64_tr_b16 a[252:253], v233 offset:49152\n"
"v_mul_f32 v252, v252, v253\n"
"v_mfma_f32_16x16x32_bf16 v[0:3], v[100:103], a[248:251], v[0:3]\n"
"ds_read_b64_tr_b16 a[254:255], v233 offset:53248\n"
"ds_read_b64_tr_b16 v[224:225], v237 offset:49152\n"
"ds_read_b64_tr_b16 v[226:227], v237 offset:53248\n"
"v_sub_u32 v228, v232, v254\n"
"v_mul_f32_dpp v8, v246, v8 row_newbcast:0 row_mask:0xf bank_mask:0xf\n"
"v_mul_f32_dpp v44, v247, v44 row_newbcast:0 row_mask:0xf bank_mask:0xf\n"
"v_mul_f32_dpp v46, v247, v46 row_newbcast:2 row_mask:0xf bank_mask:0xf\n"
"v_mul_f32_dpp v27, v246, v27 row_newbcast:3 row_mask:0xf bank_mask:0xf\n"
"v_mul_f32_dpp v61, v247, v61 row_newbcast:1 row_mask:0xf bank_mask:0xf\n"
"v_mfma_f32_16x16x32_bf16 v[32:35], v[104:107], a[244:247], v[32:35]\n"
"s_add_u32 s40, s41, s53\n"
"v_mul_f32_dpp v14, v246, v14 row_newbcast:2 row_mask:0xf bank_mask:0xf\n"
"v_mul_f32_dpp v56, v247, v56 row_newbcast:0 row_mask:0xf bank_mask:0xf\n"
"v_mfma_f32_16x16x32_bf16 v[32:35], v[108:111], a[248:251], v[32:35]\n"
"buffer_load_dwordx4 v240, s[28:31], s40 offen offset:2048 lds\n"
"v_mul_f32_dpp v42, v247, v42 row_newbcast:2 row_mask:0xf bank_mask:0xf\n"
"v_mul_f32_dpp v75, v206, v75 row_newbcast:3 row_mask:0xf bank_mask:0xf\n"
"v_mul_f32_dpp v78, v206, v78 row_newbcast:2 row_mask:0xf bank_mask:0xf\n"
"v_mul_f32_dpp v57, v247, v57 row_newbcast:1 row_mask:0xf bank_mask:0xf\n"
"v_mul_f32_dpp v58, v247, v58 row_newbcast:2 row_mask:0xf bank_mask:0xf\n"
"v_cvt_pk_bf16_f32 v56, v56, v57\n"
"v_mfma_f32_16x16x32_bf16 v[64:67], v[112:115], a[244:247], v[64:67]\n"
"v_mul_f32_dpp v74, v206, v74 row_newbcast:2 row_mask:0xf bank_mask:0xf\n"
"v_mul_f32_dpp v28, v246, v28 row_newbcast:0 row_mask:0xf bank_mask:0xf\n"
"v_mfma_f32_16x16x32_bf16 v[64:67], v[116:119], a[248:251], v[64:67]\n"
"s_waitcnt lgkmcnt(0)\n"
"ds_write_b128 v244, a[236:239] offset:35840\n"
"v_min_f32 v252, 0x71800000, v252\n"
"v_cvt_pk_bf16_f32 v73, v74, v75\n"
"v_mul_f32_dpp v25, v246, v25 row_newbcast:1 row_mask:0xf bank_mask:0xf\n"
"v_mul_f32_dpp v63, v247, v63 row_newbcast:3 row_mask:0xf bank_mask:0xf\n"
"v_mfma_f32_16x16x32_bf16 v[68:71], v[116:119], v[224:227], v[68:71]\n"
"s_add_u32 s40, s41, s54\n"
"ds_read_b64_tr_b16 a[246:247], v232 offset:54272\n"
"v_mul_f32_dpp v60, v247, v60 row_newbcast:0 row_mask:0xf bank_mask:0xf\n"
"v_mfma_f32_16x16x32_bf16 v[68:71], v[112:115], a[252:255], v[68:71]\n"
"buffer_load_dwordx4 v240, s[28:31], s40 offen offset:3072 lds\n"
"v_readlane_b32 s22, v252, 63\n"
"v_xor_b32 v250, 0x1000, v245\n"
"v_mul_f32_dpp v11, v246, v11 row_newbcast:3 row_mask:0xf bank_mask:0xf\n"
"v_cvt_pk_bf16_f32 v8, v8, v9\n"
"v_cvt_pk_bf16_f32 v40, v40, v41\n"
"v_mul_f32_dpp v29, v246, v29 row_newbcast:1 row_mask:0xf bank_mask:0xf\n"
"v_mul_f32_dpp v62, v247, v62 row_newbcast:2 row_mask:0xf bank_mask:0xf\n"
"v_mul_f32_dpp v33, v247, v33 row_newbcast:1 row_mask:0xf bank_mask:0xf\n"
"v_mfma_f32_16x16x32_bf16 v[36:39], v[108:111], v[224:227], v[36:39]\n"
"v_readlane_b32 s23, v252, 63\n"
"v_mul_f32_dpp v67, v206, v67 row_newbcast:3 row_mask:0xf bank_mask:0xf\n"
"v_mfma_f32_16x16x32_bf16 v[36:39], v[104:107], a[252:255], v[36:39]\n"
"ds_read_b64_tr_b16 a[244:245], v232 offset:50176\n"
"ds_read_b64_tr_b16 a[248:249], v236 offset:50176\n"
"ds_read_b64_tr_b16 a[250:251], v236 offset:54272\n"
"v_mov_b32 v252, v192\n"
"v_mov_b32 v253, v202\n"
"v_sub_u32 v231, v235, v254\n"
"v_mul_f32_dpp v13, v246, v13 row_newbcast:1 row_mask:0xf bank_mask:0xf\n"
"v_mul_f32_dpp v59, v247, v59 row_newbcast:3 row_mask:0xf bank_mask:0xf\n"
"v_mul_f32_dpp v64, v206, v64 row_newbcast:0 row_mask:0xf bank_mask:0xf\n"
"v_xor_b32 v244, 0x1000, v244\n"
"v_mfma_f32_16x16x32_bf16 v[4:7], v[100:103], v[224:227], v[4:7]\n"
"ds_read_b64_tr_b16 v[224:225], v237 offset:50176\n"
"v_cvt_pk_bf16_f32 v9, v10, v11\n"
"v_mfma_f32_16x16x32_bf16 v[4:7], v[96:99], a[252:255], v[4:7]\n"
"ds_write_b64 v250, v[252:253] offset:52224\n"
"v_cvt_pk_bf16_f32 v10, v12, v13\n"
"v_cvt_pk_bf16_f32 v41, v42, v43\n"
"v_cvt_pk_bf16_f32 v43, v46, v47\n"
"v_cvt_pk_bf16_f32 v74, v76, v77\n"
"v_cvt_pk_bf16_f32 v57, v58, v59\n"
"v_mul_f32_dpp v92, v206, v92 row_newbcast:0 row_mask:0xf bank_mask:0xf\n"
"v_mul_f32_dpp v65, v206, v65 row_newbcast:1 row_mask:0xf bank_mask:0xf\n"
"v_mfma_f32_16x16x32_bf16 a[212:215], v[120:123], a[252:255], a[212:215]\n"
"s_waitcnt lgkmcnt(4)\n"
"ds_read_b64_tr_b16 a[252:253], v233 offset:50176\n"
"ds_read_b64_tr_b16 a[254:255], v233 offset:54272\n"
"ds_read_b64_tr_b16 v[226:227], v237 offset:54272\n"
"v_cvt_pk_bf16_f32 v42, v44, v45\n"
"v_cvt_pk_bf16_f32 v59, v62, v63\n"
"v_mul_f32_dpp v88, v206, v88 row_newbcast:0 row_mask:0xf bank_mask:0xf\n"
"v_mul_f32_dpp v94, v206, v94 row_newbcast:2 row_mask:0xf bank_mask:0xf\n"
"v_mul_f32_dpp v69, v206, v69 row_newbcast:1 row_mask:0xf bank_mask:0xf\n"
"v_mul_f32_dpp v71, v206, v71 row_newbcast:3 row_mask:0xf bank_mask:0xf\n"
"v_add_u32 v236, s76, v249\n"
"v_mfma_f32_16x16x32_bf16 a[224:227], v[120:123], a[244:247], a[224:227]\n"
"ds_write_b128 v248, v[40:43] offset:50176\n"
"v_cvt_pk_bf16_f32 v11, v14, v15\n"
"v_mul_f32_dpp v91, v206, v91 row_newbcast:3 row_mask:0xf bank_mask:0xf\n"
"v_mul_f32_dpp v32, v247, v32 row_newbcast:0 row_mask:0xf bank_mask:0xf\n"
"v_mul_f32_dpp v70, v206, v70 row_newbcast:2 row_mask:0xf bank_mask:0xf\n"
"v_mfma_f32_16x16x32_bf16 v[16:19], v[96:99], a[244:247], v[16:19]\n"
"s_waitcnt vmcnt(8) lgkmcnt(1)\n"
"ds_write_b128 v248, v[8:11] offset:49152\n"
"v_mul_f32_dpp v31, v246, v31 row_newbcast:3 row_mask:0xf bank_mask:0xf\n"
"v_mul_f32_dpp v35, v247, v35 row_newbcast:3 row_mask:0xf bank_mask:0xf\n"
"v_cvt_pk_bf16_f32 v64, v64, v65\n"
"v_xor_b32 v232, 0x1000, v228\n"
"v_xor_b32 v237, 0x400, v236\n"
"v_mfma_f32_16x16x32_bf16 v[16:19], v[100:103], a[248:251], v[16:19]\n"
"s_barrier\n"
"ds_read_b64 v[250:251], v245 offset:52224\n"
"ds_read_b64_tr_b16 v[184:185], v232 offset:0\n"
"ds_read_b64_tr_b16 v[192:193], v232 offset:8192\n"
"ds_read_b64_tr_b16 a[200:201], v236 offset:2048\n"
"v_cvt_pk_bf16_f32 v24, v24, v25\n"
"v_mul_f32_dpp v93, v206, v93 row_newbcast:1 row_mask:0xf bank_mask:0xf\n"
"v_mul_f32_dpp v0, v246, v0 row_newbcast:0 row_mask:0xf bank_mask:0xf\n"
"v_mul_f32_dpp v6, v246, v6 row_newbcast:2 row_mask:0xf bank_mask:0xf\n"
"v_mfma_f32_16x16x32_bf16 v[48:51], v[104:107], a[244:247], v[48:51]\n"
"ds_read_b64_tr_b16 a[4:5], v249 offset:49152\n"
"ds_read_b64_tr_b16 v[188:189], v228 offset:8192\n"
"v_mfma_f32_16x16x32_bf16 v[48:51], v[108:111], a[248:251], v[48:51]\n"
"ds_read_b64_tr_b16 a[6:7], v249 offset:49160\n"
"ds_read_b64_tr_b16 a[20:21], v249 offset:50176\n"
"ds_read_b64_tr_b16 a[22:23], v249 offset:50184\n"
"ds_read_b64_tr_b16 v[210:211], v228 offset:18432\n"
"v_cvt_pk_bf16_f32 v75, v78, v79\n"
"v_mul_f32_dpp v30, v246, v30 row_newbcast:2 row_mask:0xf bank_mask:0xf\n"
"v_mul_f32_dpp v89, v206, v89 row_newbcast:1 row_mask:0xf bank_mask:0xf\n"
"v_mul_f32_dpp v2, v246, v2 row_newbcast:2 row_mask:0xf bank_mask:0xf\n"
"v_mul_f32_dpp v38, v247, v38 row_newbcast:2 row_mask:0xf bank_mask:0xf\n"
"v_mfma_f32_16x16x32_bf16 v[80:83], v[112:115], a[244:247], v[80:83]\n"
"ds_read_b64_tr_b16 v[182:183], v228 offset:2048\n"
"ds_read_b64_tr_b16 a[242:243], v236 offset:4104\n"
"v_mfma_f32_16x16x32_bf16 v[80:83], v[116:119], a[248:251], v[80:83]\n"
"ds_write_b128 v248, v[72:75] offset:51200\n"
"v_mul_f32_dpp v26, v246, v26 row_newbcast:2 row_mask:0xf bank_mask:0xf\n"
"v_cvt_pk_bf16_f32 v88, v88, v89\n"
"v_mul_f32_dpp v39, v247, v39 row_newbcast:3 row_mask:0xf bank_mask:0xf\n"
"v_mov_b64 v[198:199], s[22:23]\n"
"v_mfma_f32_16x16x32_bf16 v[84:87], v[116:119], v[224:227], v[84:87]\n"
"ds_read_b64_tr_b16 v[180:181], v228 offset:0\n"
"ds_read_b64_tr_b16 v[190:191], v228 offset:10240\n"
"ds_read_b64_tr_b16 v[212:213], v232 offset:16384\n"
"ds_read_b64_tr_b16 v[220:221], v232 offset:24576\n"
"v_cvt_pk_bf16_f32 v25, v26, v27\n"
"v_cvt_pk_bf16_f32 v89, v90, v91\n"
"v_mul_f32_dpp v1, v246, v1 row_newbcast:1 row_mask:0xf bank_mask:0xf\n"
"v_mul_f32_dpp v4, v246, v4 row_newbcast:0 row_mask:0xf bank_mask:0xf\n"
"v_xor_b32 v234, 0x1000, v230\n"
"v_mul_f32_dpp v19, v246, v19 row_newbcast:3 row_mask:0xf bank_mask:0xf\n"
"v_mfma_f32_16x16x32_bf16 v[84:87], v[112:115], a[252:255], v[84:87]\n"
"s_waitcnt lgkmcnt(15)\n"
"ds_read_b64_tr_b16 a[38:39], v249 offset:51208\n"
"ds_read_b64_tr_b16 v[194:195], v232 offset:10240\n"
"ds_read_b64_tr_b16 v[208:209], v228 offset:16384\n"
"v_cvt_pk_bf16_f32 v26, v28, v29\n"
"v_cvt_pk_bf16_f32 v27, v30, v31\n"
"v_cvt_pk_bf16_f32 v58, v60, v61\n"
"v_cvt_pk_bf16_f32 v90, v92, v93\n"
"v_min_f32 v254, v252, v250\n"
"v_mov_b64 v[196:197], s[22:23]\n"
"v_mfma_f32_16x16x32_bf16 v[52:55], v[108:111], v[224:227], v[52:55]\n"
"ds_read_b64_tr_b16 v[186:187], v232 offset:2048\n"
"ds_read_b64_tr_b16 v[216:217], v228 offset:24576\n"
"ds_read_b64_tr_b16 v[222:223], v232 offset:26624\n"
"ds_read_b64_tr_b16 a[248:249], v236 offset:6144\n"
"v_mul_f32_dpp v95, v206, v95 row_newbcast:3 row_mask:0xf bank_mask:0xf\n"
"v_mul_f32_dpp v3, v246, v3 row_newbcast:3 row_mask:0xf bank_mask:0xf\n"
"v_mul_f32_dpp v37, v247, v37 row_newbcast:1 row_mask:0xf bank_mask:0xf\n"
"v_mul_f32_dpp v66, v206, v66 row_newbcast:2 row_mask:0xf bank_mask:0xf\n"
"v_xor_b32 v233, 0x1000, v229\n"
"v_mul_f32_dpp v48, v247, v48 row_newbcast:0 row_mask:0xf bank_mask:0xf\n"
"v_mfma_f32_16x16x32_bf16 v[52:55], v[104:107], a[252:255], v[52:55]\n"
"ds_read_b128 v[60:63], v244 offset:35840\n"
"ds_read_b64_tr_b16 v[214:215], v232 offset:18432\n"
"v_mfma_f32_16x16x32_bf16 v[20:23], v[100:103], v[224:227], v[20:23]\n"
"ds_write_b128 v248, v[24:27] offset:49152\n"
"v_mul_f32_dpp v7, v246, v7 row_newbcast:3 row_mask:0xf bank_mask:0xf\n"
"v_mul_f32_dpp v36, v247, v36 row_newbcast:0 row_mask:0xf bank_mask:0xf\n"
"v_cvt_pk_bf16_f32 v32, v32, v33\n"
"v_mul_f32_dpp v49, v247, v49 row_newbcast:1 row_mask:0xf bank_mask:0xf\n"
"v_mfma_f32_16x16x32_bf16 v[20:23], v[96:99], a[252:255], v[20:23]\n"
"ds_read_b64_tr_b16 a[36:37], v249 offset:51200\n"
"ds_read_b64_tr_b16 a[12:13], v249 offset:49152\n"
"ds_read_b64_tr_b16 a[206:207], v237 offset:2056\n"
"ds_read_b64_tr_b16 v[110:111], v233 offset:10240\n"
"v_cvt_pk_bf16_f32 v91, v94, v95\n"
"v_mul_f32_dpp v5, v246, v5 row_newbcast:1 row_mask:0xf bank_mask:0xf\n"
"v_mul_f32_dpp v34, v247, v34 row_newbcast:2 row_mask:0xf bank_mask:0xf\n"
"v_cvt_pk_bf16_f32 v65, v66, v67\n"
"v_cvt_pk_bf16_f32 v67, v70, v71\n"
"v_sub_f32 v250, v254, v250\n"
"v_mfma_f32_16x16x32_bf16 a[228:231], v[120:123], a[252:255], a[228:231]\n"
"s_waitcnt lgkmcnt(15)\n"
"ds_write_b128 v248, v[88:91] offset:51200\n"
"v_cvt_pk_bf16_f32 v0, v0, v1\n"
"v_cvt_pk_bf16_f32 v33, v34, v35\n"
"v_cvt_pk_bf16_f32 v35, v38, v39\n"
"v_mul_f32_dpp v68, v206, v68 row_newbcast:0 row_mask:0xf bank_mask:0xf\n"
"v_mul_f32_dpp v51, v247, v51 row_newbcast:3 row_mask:0xf bank_mask:0xf\n"
"v_mfma_f32_16x16x32_bf16 v[124:127], v[180:183], a[96:99], v[196:199]\n"
"s_waitcnt lgkmcnt(11)\n"
"ds_read_b64_tr_b16 a[44:45], v249 offset:51200\n"
"ds_read_b64_tr_b16 a[46:47], v249 offset:51208\n"
"v_mfma_f32_16x16x32_bf16 v[124:127], v[184:187], a[100:103], v[124:127]\n"
"ds_read_b64_tr_b16 v[218:219], v228 offset:26624\n"
"ds_read_b64_tr_b16 v[224:225], v233 offset:24576\n"
"v_mfma_f32_16x16x32_bf16 v[124:127], v[188:191], a[104:107], v[124:127]\n"
"ds_read_b64_tr_b16 a[14:15], v249 offset:49160\n"
"ds_read_b64_tr_b16 a[244:245], v237 offset:4096\n"
"v_mfma_f32_16x16x32_bf16 v[124:127], v[192:195], a[108:111], v[124:127]\n"
"ds_write_b128 v248, v[56:59] offset:50176\n"
"v_cvt_pk_bf16_f32 v1, v2, v3\n"
"v_cvt_pk_bf16_f32 v2, v4, v5\n"
"v_cvt_pk_bf16_f32 v34, v36, v37\n"
"v_sub_f32 v252, v254, v252\n"
"v_accvgpr_read_b32 v255, a209\n"
"v_mfma_f32_16x16x32_bf16 v[124:127], v[208:211], a[144:147], v[124:127]\n"
"s_waitcnt lgkmcnt(13)\n"
"ds_read_b64_tr_b16 a[28:29], v249 offset:50176\n"
"ds_read_b64_tr_b16 a[30:31], v249 offset:50184\n"
"v_mfma_f32_16x16x32_bf16 v[124:127], v[212:215], a[148:151], v[124:127]\n"
"s_waitcnt lgkmcnt(6)\n"
"ds_write_b128 v248, v[32:35] offset:50176\n"
"v_cvt_pk_bf16_f32 v3, v6, v7\n"
"v_cvt_pk_bf16_f32 v66, v68, v69\n"
"v_mul_f32_dpp v21, v246, v21 row_newbcast:1 row_mask:0xf bank_mask:0xf\n"
"v_mul_f32_dpp v84, v206, v84 row_newbcast:0 row_mask:0xf bank_mask:0xf\n"
"v_mfma_f32_16x16x32_bf16 v[124:127], v[216:219], a[152:155], v[124:127]\n"
"ds_read_b64_tr_b16 a[18:19], v249 offset:50184\n"
"ds_read_b128 v[56:59], v244 offset:34816\n"
"v_mfma_f32_16x16x32_bf16 v[124:127], v[220:223], a[156:159], v[124:127]\n"
"ds_write_b128 v248, v[0:3] offset:49152\n"
"ds_write_b128 v248, v[64:67] offset:51200\n"
"v_mfma_f32_16x16x32_bf16 v[140:143], v[220:223], a[172:175], v[196:199]\n"
"ds_read_b64_tr_b16 a[16:17], v249 offset:50176\n"
"ds_read_b64_tr_b16 a[32:33], v249 offset:51200\n"
"v_mfma_f32_16x16x32_bf16 v[140:143], v[216:219], a[168:171], v[140:143]\n"
"ds_read_b64_tr_b16 a[0:1], v249 offset:49152\n"
"ds_read_b64_tr_b16 v[112:113], v229 offset:16384\n"
"v_mfma_f32_16x16x32_bf16 v[140:143], v[212:215], a[164:167], v[140:143]\n"
"ds_read_b128 v[24:27], v244 offset:32768\n"
"ds_read_b128 v[28:31], v244 offset:33792\n"
"v_mfma_f32_16x16x32_bf16 v[140:143], v[208:211], a[160:163], v[140:143]\n"
"ds_read_b64_tr_b16 a[2:3], v249 offset:49160\n"
"ds_read_b64_tr_b16 a[34:35], v249 offset:51208\n"
"v_mfma_f32_16x16x32_bf16 v[140:143], v[192:195], a[124:127], v[140:143]\n"
"ds_read_b64_tr_b16 a[240:241], v236 offset:4096\n"
"v_mul_f32_dpp v52, v247, v52 row_newbcast:0 row_mask:0xf bank_mask:0xf\n"
"v_mfma_f32_16x16x32_bf16 v[140:143], v[188:191], a[120:123], v[140:143]\n"
"ds_read_b64_tr_b16 v[116:117], v233 offset:16384\n"
"ds_read_b64_tr_b16 a[250:251], v236 offset:6152\n"
"v_mfma_f32_16x16x32_bf16 v[140:143], v[184:187], a[116:119], v[140:143]\n"
"ds_read_b64_tr_b16 a[202:203], v236 offset:2056\n"
"ds_read_b64_tr_b16 a[254:255], v237 offset:6152\n"
"v_mfma_f32_16x16x32_bf16 v[140:143], v[180:183], a[112:115], v[140:143]\n"
"ds_read_b64_tr_b16 a[194:195], v236 offset:8\n"
"ds_read_b64_tr_b16 a[204:205], v237 offset:2048\n"
"ds_read_b64_tr_b16 a[252:253], v237 offset:6144\n"
"v_mfma_f32_16x16x32_bf16 v[156:159], v[180:183], a[128:131], v[196:199]\n"
"ds_read_b64_tr_b16 v[104:105], v229 offset:8192\n"
"ds_read_b64_tr_b16 a[246:247], v237 offset:4104\n"
"v_mfma_f32_16x16x32_bf16 v[156:159], v[184:187], a[132:135], v[156:159]\n"
"ds_read_b64_tr_b16 v[100:101], v233 offset:0\n"
"v_accvgpr_read_b32 v254, a208\n"
"v_mfma_f32_16x16x32_bf16 v[156:159], v[188:191], a[136:139], v[156:159]\n"
"ds_read_b64_tr_b16 a[198:199], v237 offset:8\n"
"ds_read_b64_tr_b16 v[98:99], v229 offset:2048\n"
"v_mfma_f32_16x16x32_bf16 v[156:159], v[192:195], a[140:143], v[156:159]\n"
"s_waitcnt lgkmcnt(13)\n"
"ds_read_b64_tr_b16 a[196:197], v237 offset:0\n"
"ds_read_b64_tr_b16 v[102:103], v233 offset:2048\n"
"v_mfma_f32_16x16x32_bf16 v[156:159], v[208:211], a[176:179], v[156:159]\n"
"ds_read_b64_tr_b16 v[120:121], v229 offset:24576\n"
"v_mul_f32_dpp v80, v206, v80 row_newbcast:0 row_mask:0xf bank_mask:0xf\n"
"v_mfma_f32_16x16x32_bf16 v[156:159], v[212:215], a[180:183], v[156:159]\n"
"s_add_u32 s41, s95, 491520\n"
"ds_read_b64_tr_b16 a[192:193], v236 offset:0\n"
"ds_read_b64_tr_b16 v[106:107], v229 offset:10240\n"
"v_mfma_f32_16x16x32_bf16 v[156:159], v[216:219], a[184:187], v[156:159]\n"
"s_add_u32 m0, s44, 49152\n"
"ds_read_b64_tr_b16 v[96:97], v229 offset:0\n"
"ds_read_b64_tr_b16 v[118:119], v233 offset:18432\n"
"v_mfma_f32_16x16x32_bf16 v[156:159], v[220:223], a[188:191], v[156:159]\n"
"s_waitcnt lgkmcnt(12)\n"
"buffer_load_dwordx4 v240, s[28:31], s41 offen lds\n"
"v_mfma_f32_16x16x32_bf16 v[172:175], v[220:223], a[252:255], v[196:199]\n"
"s_add_u32 s40, s41, s52\n"
"v_exp_f32 v252, v252\n"
"v_mfma_f32_16x16x32_bf16 v[172:175], v[216:219], a[248:251], v[172:175]\n"
"s_waitcnt lgkmcnt(9)\n"
"buffer_load_dwordx4 v240, s[28:31], s40 offen offset:1024 lds\n"
"v_mfma_f32_16x16x32_bf16 v[172:175], v[212:215], a[244:247], v[172:175]\n"
"s_add_u32 s40, s41, s53\n"
"ds_read_b64_tr_b16 v[108:109], v233 offset:8192\n"
"ds_read_b64_tr_b16 v[226:227], v233 offset:26624\n"
"v_mfma_f32_16x16x32_bf16 v[172:175], v[208:211], a[240:243], v[172:175]\n"
"buffer_load_dwordx4 v240, s[28:31], s40 offen offset:2048 lds\n"
"v_mfma_f32_16x16x32_bf16 v[172:175], v[192:195], a[204:207], v[172:175]\n"
"ds_read_b64_tr_b16 v[114:115], v229 offset:18432\n"
"v_mul_f32_dpp v55, v247, v55 row_newbcast:3 row_mask:0xf bank_mask:0xf\n"
"v_mfma_f32_16x16x32_bf16 v[172:175], v[188:191], a[200:203], v[172:175]\n"
"s_waitcnt lgkmcnt(5)\n"
"v_exp_f32 v250, v250\n"
"v_mfma_f32_16x16x32_bf16 v[172:175], v[184:187], a[196:199], v[172:175]\n"
"s_add_u32 s40, s41, s54\n"
"ds_read_b64_tr_b16 v[122:123], v229 offset:26624\n"
"ds_read_b64_tr_b16 v[184:185], v234 offset:0\n"
"v_mfma_f32_16x16x32_bf16 v[172:175], v[180:183], a[192:195], v[172:175]\n"
"s_waitcnt lgkmcnt(5)\n"
"buffer_load_dwordx4 v240, s[28:31], s40 offen offset:3072 lds\n"
"v_mfma_f32_16x16x32_bf16 v[128:131], v[96:99], a[96:99], v[196:199]\n"
"v_mul_f32 v253, v253, v252\n"
"v_fmac_f32 v253, v251, v250\n"
"v_mfma_f32_16x16x32_bf16 v[128:131], v[100:103], a[100:103], v[128:131]\n"
"v_mul_f32_dpp v17, v246, v17 row_newbcast:1 row_mask:0xf bank_mask:0xf\n"
"v_mul_f32_dpp v83, v206, v83 row_newbcast:3 row_mask:0xf bank_mask:0xf\n"
"v_mfma_f32_16x16x32_bf16 v[128:131], v[104:107], a[104:107], v[128:131]\n"
"s_waitcnt lgkmcnt(3)\n"
"ds_read_b64_tr_b16 a[234:235], v236 offset:10248\n"
"ds_read_b64_tr_b16 v[216:217], v230 offset:24576\n"
"v_mfma_f32_16x16x32_bf16 v[128:131], v[108:111], a[108:111], v[128:131]\n"
"s_waitcnt lgkmcnt(4)\n"
"ds_read_b64_tr_b16 v[208:209], v230 offset:16384\n"
"v_mul_f32_dpp v81, v206, v81 row_newbcast:1 row_mask:0xf bank_mask:0xf\n"
"v_mfma_f32_16x16x32_bf16 v[128:131], v[112:115], a[144:147], v[128:131]\n"
"s_add_u32 s21, s60, 32\n"
"ds_read_b64_tr_b16 v[188:189], v230 offset:8192\n"
"v_cvt_pk_bf16_f32 v80, v80, v81\n"
"v_mfma_f32_16x16x32_bf16 v[128:131], v[116:119], a[148:151], v[128:131]\n"
"s_waitcnt lgkmcnt(5)\n"
"s_mov_b32 s69, 1\n"
"ds_read_b64_tr_b16 a[238:239], v237 offset:10248\n"
"v_mul_f32_dpp v20, v246, v20 row_newbcast:0 row_mask:0xf bank_mask:0xf\n"
"v_mfma_f32_16x16x32_bf16 v[128:131], v[120:123], a[152:155], v[128:131]\n"
"v_bfe_u32 v201, v242, 2, 7\n"
"v_add_u32 v201, 0x100, v201\n"
"ds_read_b64_tr_b16 v[214:215], v234 offset:18432\n"
"v_mfma_f32_16x16x32_bf16 v[128:131], v[224:227], a[156:159], v[128:131]\n"
"global_load_dword v200, v201, s[6:7] sc1\n"
"v_mfma_f32_16x16x32_bf16 v[144:147], v[224:227], a[172:175], v[196:199]\n"
"v_rcp_f32 v253, v253\n"
"v_mfma_f32_16x16x32_bf16 v[144:147], v[120:123], a[168:171], v[144:147]\n"
"v_mul_f32 v252, v252, v253\n"
"v_mul_f32 v250, v250, v253\n"
"v_mfma_f32_16x16x32_bf16 v[144:147], v[116:119], a[164:167], v[144:147]\n"
"s_nop 0\n"
"v_mov_b32_dpp v250, v250 row_ror:12 row_mask:0x2 bank_mask:0xf\n"
"v_mov_b32_dpp v252, v252 row_ror:12 row_mask:0x2 bank_mask:0xf\n"
"v_mfma_f32_16x16x32_bf16 v[144:147], v[112:115], a[160:163], v[144:147]\n"
"v_mov_b32_dpp v250, v250 row_ror:8 row_mask:0x4 bank_mask:0xf\n"
"v_mov_b32_dpp v252, v252 row_ror:8 row_mask:0x4 bank_mask:0xf\n"
"v_mfma_f32_16x16x32_bf16 v[144:147], v[108:111], a[124:127], v[144:147]\n"
"ds_read_b64_tr_b16 v[222:223], v234 offset:26624\n"
"v_mov_b32_dpp v250, v250 row_ror:4 row_mask:0x8 bank_mask:0xf\n"
"v_mfma_f32_16x16x32_bf16 v[144:147], v[104:107], a[120:123], v[144:147]\n"
"ds_read_b64_tr_b16 a[232:233], v236 offset:10240\n"
"v_accvgpr_read_b32 v253, a210\n"
"v_mfma_f32_16x16x32_bf16 v[144:147], v[100:103], a[116:119], v[144:147]\n"
"ds_read_b64_tr_b16 v[182:183], v230 offset:2048\n"
"v_mul_f32_dpp v27, v250, v27 row_newbcast:3 row_mask:0xf bank_mask:0xf\n"
"v_mfma_f32_16x16x32_bf16 v[144:147], v[96:99], a[112:115], v[144:147]\n"
"ds_read_b64_tr_b16 v[194:195], v234 offset:10240\n"
"v_mov_b32_dpp v252, v252 row_ror:4 row_mask:0x8 bank_mask:0xf\n"
"v_mul_f32_dpp v24, v250, v24 row_newbcast:0 row_mask:0xf bank_mask:0xf\n"
"v_mul_f32_dpp v25, v250, v25 row_newbcast:1 row_mask:0xf bank_mask:0xf\n"
"v_mul_f32_dpp v26, v250, v26 row_newbcast:2 row_mask:0xf bank_mask:0xf\n"
"v_accvgpr_read_b32 v251, a211\n"
"v_mul_f32_dpp v28, v250, v28 row_newbcast:0 row_mask:0xf bank_mask:0xf\n"
"v_mul_f32_dpp v16, v246, v16 row_newbcast:0 row_mask:0xf bank_mask:0xf\n"
"v_mfma_f32_16x16x32_bf16 v[160:163], v[96:99], a[128:131], v[196:199]\n"
"ds_read_b64_tr_b16 v[210:211], v230 offset:18432\n"
"v_fmac_f32_dpp v24, v252, v254 row_newbcast:0 row_mask:0xf bank_mask:0xf\n"
"v_mfma_f32_16x16x32_bf16 v[160:163], v[100:103], a[132:135], v[160:163]\n"
"ds_read_b64_tr_b16 v[220:221], v234 offset:24576\n"
"v_fmac_f32_dpp v25, v252, v255 row_newbcast:1 row_mask:0xf bank_mask:0xf\n"
"v_mfma_f32_16x16x32_bf16 v[160:163], v[104:107], a[136:139], v[160:163]\n"
"ds_read_b64_tr_b16 v[186:187], v234 offset:2048\n"
"v_accvgpr_read_b32 v255, a213\n"
"v_mfma_f32_16x16x32_bf16 v[160:163], v[108:111], a[140:143], v[160:163]\n"
"v_xor_b32 v235, 0x1000, v231\n"
"v_mul_f32_dpp v57, v250, v57 row_newbcast:1 row_mask:0xf bank_mask:0xf\n"
"v_mfma_f32_16x16x32_bf16 v[160:163], v[112:115], a[176:179], v[160:163]\n"
"v_accvgpr_read_b32 v254, a212\n"
"v_mul_f32_dpp v56, v250, v56 row_newbcast:0 row_mask:0xf bank_mask:0xf\n"
"v_mfma_f32_16x16x32_bf16 v[160:163], v[116:119], a[180:183], v[160:163]\n"
"ds_read_b64_tr_b16 v[190:191], v230 offset:10240\n"
"ds_read_b64_tr_b16 v[192:193], v234 offset:8192\n"
"v_mfma_f32_16x16x32_bf16 v[160:163], v[120:123], a[184:187], v[160:163]\n"
"ds_read_b64_tr_b16 v[212:213], v234 offset:16384\n"
"v_fmac_f32_dpp v26, v252, v253 row_newbcast:2 row_mask:0xf bank_mask:0xf\n"
"v_mfma_f32_16x16x32_bf16 v[160:163], v[224:227], a[188:191], v[160:163]\n"
"ds_read_b64_tr_b16 a[236:237], v237 offset:10240\n"
"v_fmac_f32_dpp v27, v252, v251 row_newbcast:3 row_mask:0xf bank_mask:0xf\n"
"v_mul_f32_dpp v29, v250, v29 row_newbcast:1 row_mask:0xf bank_mask:0xf\n"
"v_accvgpr_read_b32 v253, a214\n"
"v_mul_f32_dpp v30, v250, v30 row_newbcast:2 row_mask:0xf bank_mask:0xf\n"
"v_accvgpr_read_b32 v251, a215\n"
"v_mul_f32_dpp v31, v250, v31 row_newbcast:3 row_mask:0xf bank_mask:0xf\n"
"v_fmac_f32_dpp v28, v252, v254 row_newbcast:0 row_mask:0xf bank_mask:0xf\n"
"v_mul_f32_dpp v59, v250, v59 row_newbcast:3 row_mask:0xf bank_mask:0xf\n"
"v_mfma_f32_16x16x32_bf16 v[176:179], v[224:227], a[252:255], v[196:199]\n"
"ds_read_b64_tr_b16 v[180:181], v230 offset:0\n"
"v_mul_f32_dpp v18, v246, v18 row_newbcast:2 row_mask:0xf bank_mask:0xf\n"
"v_mfma_f32_16x16x32_bf16 v[176:179], v[120:123], a[248:251], v[176:179]\n"
"ds_read_b64_tr_b16 v[224:225], v235 offset:24576\n"
"v_mov_b64 v[44:45], s[22:23]\n"
"v_mfma_f32_16x16x32_bf16 v[176:179], v[116:119], a[244:247], v[176:179]\n"
"v_cvt_pk_bf16_f32 v48, v48, v49\n"
"v_mul_f32_dpp v61, v250, v61 row_newbcast:1 row_mask:0xf bank_mask:0xf\n"
"v_mfma_f32_16x16x32_bf16 v[176:179], v[112:115], a[240:243], v[176:179]\n"
"ds_read_b64_tr_b16 a[220:221], v237 offset:8192\n"
"v_mul_f32_dpp v86, v206, v86 row_newbcast:2 row_mask:0xf bank_mask:0xf\n"
"v_mfma_f32_16x16x32_bf16 v[176:179], v[108:111], a[204:207], v[176:179]\n"
"v_fmac_f32_dpp v29, v252, v255 row_newbcast:1 row_mask:0xf bank_mask:0xf\n"
"v_accvgpr_read_b32 v254, a224\n"
"v_mfma_f32_16x16x32_bf16 v[176:179], v[104:107], a[200:203], v[176:179]\n"
"v_fmac_f32_dpp v31, v252, v251 row_newbcast:3 row_mask:0xf bank_mask:0xf\n"
"v_fmac_f32_dpp v56, v252, v254 row_newbcast:0 row_mask:0xf bank_mask:0xf\n"
"v_mfma_f32_16x16x32_bf16 v[176:179], v[100:103], a[196:199], v[176:179]\n"
"ds_read_b64_tr_b16 v[218:219], v230 offset:26624\n"
"v_mul_f32_dpp v58, v250, v58 row_newbcast:2 row_mask:0xf bank_mask:0xf\n"
"v_mfma_f32_16x16x32_bf16 v[176:179], v[96:99], a[192:195], v[176:179]\n"
"s_waitcnt lgkmcnt(3)\n"
"ds_read_b64_tr_b16 v[108:109], v235 offset:8192\n"
"v_fmac_f32_dpp v30, v252, v253 row_newbcast:2 row_mask:0xf bank_mask:0xf\n"
"v_cvt_pk_bf16_f32 v24, v24, v25\n"
"v_cvt_pk_bf16_f32 v25, v26, v27\n"
"v_mul_f32_dpp v50, v247, v50 row_newbcast:2 row_mask:0xf bank_mask:0xf\n"
"v_mul_f32_dpp v54, v247, v54 row_newbcast:2 row_mask:0xf bank_mask:0xf\n"
"v_accvgpr_read_b32 v255, a225\n"
"v_accvgpr_read_b32 v253, a226\n"
"v_mul_f32_dpp v60, v250, v60 row_newbcast:0 row_mask:0xf bank_mask:0xf\n"
"v_mfma_f32_16x16x32_bf16 v[132:135], v[180:183], a[96:99], v[196:199]\n"
"v_cvt_pk_bf16_f32 v27, v30, v31\n"
"v_accvgpr_read_b32 v251, a227\n"
"v_mfma_f32_16x16x32_bf16 v[132:135], v[184:187], a[100:103], v[132:135]\n"
"v_cvt_pk_bf16_f32 v26, v28, v29\n"
"v_fmac_f32_dpp v58, v252, v253 row_newbcast:2 row_mask:0xf bank_mask:0xf\n"
"v_mfma_f32_16x16x32_bf16 v[132:135], v[188:191], a[104:107], v[132:135]\n"
"ds_read_b64_tr_b16 v[100:101], v235 offset:0\n"
"v_accvgpr_read_b32 v254, a228\n"
"v_mfma_f32_16x16x32_bf16 v[132:135], v[192:195], a[108:111], v[132:135]\n"
"ds_read_b64_tr_b16 v[226:227], v235 offset:26624\n"
"v_mul_f32_dpp v63, v250, v63 row_newbcast:3 row_mask:0xf bank_mask:0xf\n"
"v_mfma_f32_16x16x32_bf16 v[132:135], v[208:211], a[144:147], v[132:135]\n"
"s_waitcnt vmcnt(0)\n"
"ds_read_b64_tr_b16 v[104:105], v231 offset:8192\n"
"v_cvt_pk_bf16_f32 v16, v16, v17\n"
"v_mfma_f32_16x16x32_bf16 v[132:135], v[212:215], a[148:151], v[132:135]\n"
"s_waitcnt lgkmcnt(4)\n"
"v_subrev_u32 v200, s21, v200\n"
"ds_read_b64_tr_b16 v[102:103], v235 offset:2048\n"
"v_mfma_f32_16x16x32_bf16 v[132:135], v[216:219], a[152:155], v[132:135]\n"
"v_cmp_lt_u32 vcc, s69, v200\n"
"ds_read_b64_tr_b16 v[110:111], v235 offset:10240\n"
"v_mfma_f32_16x16x32_bf16 v[132:135], v[220:223], a[156:159], v[132:135]\n"
"ds_write_b128 v243, v[24:27] offset:0\n"
"v_fmac_f32_dpp v57, v252, v255 row_newbcast:1 row_mask:0xf bank_mask:0xf\n"
"v_fmac_f32_dpp v59, v252, v251 row_newbcast:3 row_mask:0xf bank_mask:0xf\n"
"v_accvgpr_read_b32 v251, a231\n"
"v_fmac_f32_dpp v60, v252, v254 row_newbcast:0 row_mask:0xf bank_mask:0xf\n"
"s_cbranch_vccnz LGV_%=\nLGVK_%=:\n"
"v_mfma_f32_16x16x32_bf16 v[148:151], v[220:223], a[172:175], v[196:199]\n"
"v_cvt_pk_bf16_f32 v17, v18, v19\n"
"v_accvgpr_read_b32 v253, a230\n"
"v_mfma_f32_16x16x32_bf16 v[148:151], v[216:219], a[168:171], v[148:151]\n"
"v_add_u32_dpp v200, v200, v200 quad_perm:[1,0,3,2] row_mask:0xf bank_mask:0xf\n"
"ds_read_b64_tr_b16 v[98:99], v231 offset:2048\n"
"v_mfma_f32_16x16x32_bf16 v[148:151], v[212:215], a[164:167], v[148:151]\n"
"v_accvgpr_read_b32 v255, a229\n"
"v_mul_f32_dpp v62, v250, v62 row_newbcast:2 row_mask:0xf bank_mask:0xf\n"
"v_fmac_f32_dpp v63, v252, v251 row_newbcast:3 row_mask:0xf bank_mask:0xf\n"
"v_cvt_pk_bf16_f32 v56, v56, v57\n"
"v_cvt_pk_bf16_f32 v57, v58, v59\n"
"v_mfma_f32_16x16x32_bf16 v[148:151], v[208:211], a[160:163], v[148:151]\n"
"v_add_u32_dpp v200, v200, v200 quad_perm:[2,3,0,1] row_mask:0xf bank_mask:0xf\n"
"ds_read_b64_tr_b16 v[96:97], v231 offset:0\n"
"v_mfma_f32_16x16x32_bf16 v[148:151], v[192:195], a[124:127], v[148:151]\n"
"ds_read_b64_tr_b16 a[216:217], v236 offset:8192\n"
"v_fmac_f32_dpp v62, v252, v253 row_newbcast:2 row_mask:0xf bank_mask:0xf\n"
"v_mfma_f32_16x16x32_bf16 v[148:151], v[188:191], a[120:123], v[148:151]\n"
"v_cmp_lt_u32 vcc, 1, v200\n"
"v_fmac_f32_dpp v61, v252, v255 row_newbcast:1 row_mask:0xf bank_mask:0xf\n"
"v_mfma_f32_16x16x32_bf16 v[148:151], v[184:187], a[116:119], v[148:151]\n"
"v_cvt_pk_bf16_f32 v58, v60, v61\n"
"v_cvt_pk_bf16_f32 v59, v62, v63\n"
"v_mfma_f32_16x16x32_bf16 v[148:151], v[180:183], a[112:115], v[148:151]\n"
"ds_write_b128 v243, v[56:59] offset:2048\n"
"v_mul_f32_dpp v53, v247, v53 row_newbcast:1 row_mask:0xf bank_mask:0xf\n"
"v_cvt_pk_bf16_f32 v49, v50, v51\n"
"v_mfma_f32_16x16x32_bf16 v[164:167], v[180:183], a[128:131], v[196:199]\n"
"s_mov_b32 s47, 0\n"
"v_mul_f32_dpp v22, v246, v22 row_newbcast:2 row_mask:0xf bank_mask:0xf\n"
"v_cvt_pk_bf16_f32 v51, v54, v55\n"
"v_mfma_f32_16x16x32_bf16 v[164:167], v[184:187], a[132:135], v[164:167]\n"
"s_mov_b32 s89, vcc_lo\n"
"ds_read_b64_tr_b16 v[106:107], v231 offset:10240\n"
"ds_read_b64_tr_b16 v[112:113], v231 offset:16384\n"
"v_mfma_f32_16x16x32_bf16 v[164:167], v[188:191], a[136:139], v[164:167]\n"
"s_lshr_b32 s90, s89, s93\n"
"ds_read_b64_tr_b16 v[118:119], v235 offset:18432\n"
"ds_read_b64_tr_b16 v[122:123], v231 offset:26624\n"
"v_mfma_f32_16x16x32_bf16 v[164:167], v[192:195], a[140:143], v[164:167]\n"
"s_and_b32 s90, s90, 1\n"
"ds_read_b64_tr_b16 v[116:117], v235 offset:16384\n"
"v_mul_f32_dpp v82, v206, v82 row_newbcast:2 row_mask:0xf bank_mask:0xf\n"
"v_mfma_f32_16x16x32_bf16 v[164:167], v[208:211], a[176:179], v[164:167]\n"
"s_lshl_b32 s16, s61, 7\n"
"ds_read_b64_tr_b16 v[114:115], v231 offset:18432\n"
"v_cvt_pk_bf16_f32 v50, v52, v53\n"
"v_mfma_f32_16x16x32_bf16 v[164:167], v[212:215], a[180:183], v[164:167]\n"
"s_add_u32 s90, s90, s16\n"
"ds_read_b64_tr_b16 v[120:121], v231 offset:24576\n"
"v_xor_b32 v248, s75, v248\n"
"v_mfma_f32_16x16x32_bf16 v[164:167], v[216:219], a[184:187], v[164:167]\n"
"s_waitcnt vmcnt(63) lgkmcnt(7)\n"
"s_lshl_b32 s90, s90, 13\n"
"v_mul_f32_dpp v87, v206, v87 row_newbcast:3 row_mask:0xf bank_mask:0xf\n"
"v_mfma_f32_16x16x32_bf16 v[164:167], v[220:223], a[188:191], v[164:167]\n"
"s_barrier\n"
"s_lshl_b32 s16, s95, 9\n"
"ds_write_b128 v248, v[48:51] offset:50176\n"
"v_xor_b32 v249, s75, v249\n"
"v_mfma_f32_16x16x32_bf16 v[136:139], v[96:99], a[96:99], v[196:199]\n"
"s_add_u32 s16, s16, s90\n"
"ds_read_b64_tr_b16 a[24:25], v249 offset:50176\n"
"v_mul_f32_dpp v85, v206, v85 row_newbcast:1 row_mask:0xf bank_mask:0xf\n"
"v_mfma_f32_16x16x32_bf16 v[136:139], v[100:103], a[100:103], v[136:139]\n"
"s_waitcnt lgkmcnt(7)\n"
"s_add_u32 s17, s16, 4096\n"
"ds_read_b64_tr_b16 a[26:27], v249 offset:50184\n"
"ds_read_b64_tr_b16 v[188:189], v228 offset:40960\n"
"v_mfma_f32_16x16x32_bf16 v[136:139], v[104:107], a[104:107], v[136:139]\n"
"s_mov_b32 s72, s61\n"
"ds_read_b64_tr_b16 v[218:219], v228 offset:59392\n"
"v_cvt_pk_bf16_f32 v81, v82, v83\n"
"v_mfma_f32_16x16x32_bf16 v[136:139], v[108:111], a[108:111], v[136:139]\n"
"s_waitcnt lgkmcnt(6)\n"
"s_mov_b32 s21, s60\n"
"ds_read_b64_tr_b16 v[184:185], v232 offset:32768\n"
"v_cvt_pk_bf16_f32 v83, v86, v87\n"
"v_mfma_f32_16x16x32_bf16 v[136:139], v[112:115], a[144:147], v[136:139]\n"
"s_mov_b32 s18, s77\n"
"ds_read_b64_tr_b16 v[180:181], v228 offset:32768\n"
"v_cvt_pk_bf16_f32 v82, v84, v85\n"
"v_mfma_f32_16x16x32_bf16 v[136:139], v[116:119], a[148:151], v[136:139]\n"
"s_waitcnt lgkmcnt(7)\n"
"s_mov_b32 s19, s77\n"
"ds_write_b128 v248, v[80:83] offset:51200\n"
"v_cvt_pk_bf16_f32 v18, v20, v21\n"
"v_mfma_f32_16x16x32_bf16 v[136:139], v[120:123], a[152:155], v[136:139]\n"
"s_mov_b32 s69, -1\n"
"v_mul_f32_dpp v23, v246, v23 row_newbcast:3 row_mask:0xf bank_mask:0xf\n"
"v_cvt_pk_bf16_f32 v19, v22, v23\n"
"v_mfma_f32_16x16x32_bf16 v[136:139], v[224:227], a[156:159], v[136:139]\n"
"s_mul_i32 s88, s61, 0x10011111\n"
"ds_write_b128 v248, v[16:19] offset:49152\n"
"v_mfma_f32_16x16x32_bf16 v[152:155], v[224:227], a[172:175], v[196:199]\n"
"s_add_u32 s88, s88, 484675\n"
"v_mov_b64 v[20:21], s[22:23]\n"
"v_mov_b64 v[26:27], s[22:23]\n"
"v_mfma_f32_16x16x32_bf16 v[152:155], v[120:123], a[168:171], v[152:155]\n"
"s_and_b32 s88, s88, 0x70077777\n"
"ds_read_b64_tr_b16 a[10:11], v249 offset:49160\n"
"ds_read_b64_tr_b16 a[40:41], v249 offset:51200\n"
"v_mfma_f32_16x16x32_bf16 v[152:155], v[116:119], a[164:167], v[152:155]\n"
"s_or_b32 s88, s88, 0x9800000\n"
"ds_read_b64_tr_b16 a[8:9], v249 offset:49152\n"
"ds_read_b64_tr_b16 a[42:43], v249 offset:51208\n"
"v_mfma_f32_16x16x32_bf16 v[152:155], v[112:115], a[160:163], v[152:155]\n"
"s_mov_b32 s84, s6\n"
"ds_read_b64_tr_b16 v[186:187], v232 offset:34816\n"
"ds_read_b64_tr_b16 v[212:213], v232 offset:49152\n"
"v_mfma_f32_16x16x32_bf16 v[152:155], v[108:111], a[124:127], v[152:155]\n"
"s_and_b32 s85, s7, 0xffff\n"
"ds_read_b64_tr_b16 a[228:229], v237 offset:14336\n"
"v_mov_b64 v[10:11], s[22:23]\n"
"v_mfma_f32_16x16x32_bf16 v[152:155], v[104:107], a[120:123], v[152:155]\n"
"s_mov_b32 s86, 128\n"
"ds_read_b64_tr_b16 v[182:183], v228 offset:34816\n"
"ds_read_b64_tr_b16 v[194:195], v232 offset:43008\n"
"v_mfma_f32_16x16x32_bf16 v[152:155], v[100:103], a[116:119], v[152:155]\n"
"s_mov_b32 s87, 131072\n"
"ds_read_b64_tr_b16 v[216:217], v228 offset:57344\n"
"v_mfma_f32_16x16x32_bf16 v[152:155], v[96:99], a[112:115], v[152:155]\n"
"ds_read_b64_tr_b16 v[192:193], v232 offset:40960\n"
"ds_read_b64_tr_b16 v[210:211], v228 offset:51200\n"
"v_mfma_f32_16x16x32_bf16 v[168:171], v[96:99], a[128:131], v[196:199]\n"
"ds_read_b64_tr_b16 a[214:215], v237 offset:12296\n"
"ds_read_b64_tr_b16 v[214:215], v232 offset:51200\n"
"v_mfma_f32_16x16x32_bf16 v[168:171], v[100:103], a[132:135], v[168:171]\n"
"ds_read_b64_tr_b16 v[190:191], v228 offset:43008\n"
"v_mfma_f32_16x16x32_bf16 v[168:171], v[104:107], a[136:139], v[168:171]\n"
"ds_read_b64_tr_b16 v[208:209], v228 offset:49152\n"
"ds_read_b64_tr_b16 v[220:221], v232 offset:57344\n"
"v_mfma_f32_16x16x32_bf16 v[168:171], v[108:111], a[140:143], v[168:171]\n"
"s_waitcnt lgkmcnt(11)\n"
"ds_read_b64_tr_b16 v[98:99], v229 offset:34816\n"
"v_mfma_f32_16x16x32_bf16 v[168:171], v[112:115], a[176:179], v[168:171]\n"
"ds_read_b64_tr_b16 v[222:223], v232 offset:59392\n"
"ds_read_b64_tr_b16 v[104:105], v229 offset:40960\n"
"v_mfma_f32_16x16x32_bf16 v[168:171], v[116:119], a[180:183], v[168:171]\n"
"ds_read_b64_tr_b16 a[226:227], v236 offset:14344\n"
"ds_read_b64_tr_b16 v[96:97], v229 offset:32768\n"
"v_mfma_f32_16x16x32_bf16 v[168:171], v[120:123], a[184:187], v[168:171]\n"
"s_add_u32 s41, s70, 327680\n"
"s_add_u32 m0, s71, 32768\n"
"ds_read_b64_tr_b16 v[108:109], v233 offset:40960\n"
"v_mfma_f32_16x16x32_bf16 v[168:171], v[224:227], a[188:191], v[168:171]\n"
"s_waitcnt lgkmcnt(11)\n"
"buffer_load_dwordx4 v240, s[28:31], s41 offen lds\n"
"v_mfma_f32_16x16x32_bf16 v[124:127], v[180:183], a[48:51], v[124:127]\n"
"v_xor_b32 v228, s75, v228\n"
"v_mfma_f32_16x16x32_bf16 v[124:127], v[184:187], a[52:55], v[124:127]\n"
"s_waitcnt lgkmcnt(8)\n"
"v_bfe_u32 v254, v242, 4, 4\n"
"v_xor_b32 v232, s75, v232\n"
"v_mfma_f32_16x16x32_bf16 v[124:127], v[188:191], a[56:59], v[124:127]\n"
"s_add_u32 s40, s41, s52\n"
"ds_read_b64_tr_b16 a[230:231], v237 offset:14344\n"
"ds_read_b64_tr_b16 v[102:103], v233 offset:34816\n"
"v_mfma_f32_16x16x32_bf16 v[124:127], v[192:195], a[60:63], v[124:127]\n"
"s_waitcnt lgkmcnt(7)\n"
"buffer_load_dwordx4 v240, s[28:31], s40 offen offset:1024 lds\n"
"v_mfma_f32_16x16x32_bf16 v[124:127], v[208:211], a[0:3], v[124:127]\n"
"v_xor_b32 v248, s75, v248\n"
"v_mov_b64 v[24:25], s[22:23]\n"
"v_mfma_f32_16x16x32_bf16 v[124:127], v[212:215], a[4:7], v[124:127]\n"
"v_and_b32 v243, 0x3f0, v242\n"
"v_mov_b64 v[6:7], s[22:23]\n"
"v_mfma_f32_16x16x32_bf16 v[124:127], v[216:219], a[8:11], v[124:127]\n"
"s_waitcnt lgkmcnt(2)\n"
"s_add_u32 s40, s41, s53\n"
"ds_read_b64_tr_b16 a[212:213], v237 offset:12288\n"
"ds_read_b64_tr_b16 v[116:117], v233 offset:49152\n"
"v_mfma_f32_16x16x32_bf16 v[124:127], v[220:223], a[12:15], v[124:127]\n"
"buffer_load_dwordx4 v240, s[28:31], s40 offen offset:2048 lds\n"
"v_mfma_f32_16x16x32_bf16 v[140:143], v[220:223], a[28:31], v[140:143]\n"
"v_mov_b64 v[40:41], s[22:23]\n"
"v_mfma_f32_16x16x32_bf16 v[140:143], v[216:219], a[24:27], v[140:143]\n"
"v_xor_b32 v249, s75, v249\n"
"v_bfe_u32 v255, v242, 8, 1\n"
"v_mfma_f32_16x16x32_bf16 v[140:143], v[212:215], a[20:23], v[140:143]\n"
"ds_read_b64_tr_b16 a[210:211], v236 offset:12296\n"
"ds_read_b64_tr_b16 a[224:225], v236 offset:14336\n"
"v_mfma_f32_16x16x32_bf16 v[140:143], v[208:211], a[16:19], v[140:143]\n"
"ds_read_b64_tr_b16 v[110:111], v233 offset:43008\n"
"v_mov_b32 v241, s60\n"
"v_mfma_f32_16x16x32_bf16 v[140:143], v[192:195], a[76:79], v[140:143]\n"
"ds_read_b64_tr_b16 v[106:107], v229 offset:43008\n"
"ds_read_b64_tr_b16 v[224:225], v233 offset:57344\n"
"v_mfma_f32_16x16x32_bf16 v[140:143], v[188:191], a[72:75], v[140:143]\n"
"ds_read_b64_tr_b16 v[118:119], v233 offset:51200\n"
"v_mfma_f32_16x16x32_bf16 v[140:143], v[184:187], a[68:71], v[140:143]\n"
"s_add_u32 s40, s41, s54\n"
"ds_read_b64_tr_b16 a[208:209], v236 offset:12288\n"
"ds_read_b64_tr_b16 v[114:115], v229 offset:51200\n"
"v_mfma_f32_16x16x32_bf16 v[140:143], v[180:183], a[64:67], v[140:143]\n"
"buffer_load_dwordx4 v240, s[28:31], s40 offen offset:3072 lds\n"
"v_mfma_f32_16x16x32_bf16 v[156:159], v[180:183], a[80:83], v[156:159]\n"
"s_add_u32 s41, s70, 491520\n"
"s_add_u32 m0, s71, 49152\n"
"v_mov_b64 v[90:91], 0\n"
"v_mfma_f32_16x16x32_bf16 v[156:159], v[184:187], a[84:87], v[156:159]\n"
"buffer_load_dwordx4 v240, s[28:31], s41 offen lds\n"
"v_mfma_f32_16x16x32_bf16 v[156:159], v[188:191], a[88:91], v[156:159]\n"
"v_mul_u32_u24 v254, 2560, v254\n"
"v_mfma_f32_16x16x32_bf16 v[156:159], v[192:195], a[92:95], v[156:159]\n"
"v_mov_b64 v[246:247], 0\n"
"v_subrev_f32 v141, s22, v141\n"
"v_mfma_f32_16x16x32_bf16 v[156:159], v[208:211], a[32:35], v[156:159]\n"
"ds_read_b64_tr_b16 a[222:223], v237 offset:8200\n"
"ds_read_b64_tr_b16 v[100:101], v233 offset:32768\n"
"v_mfma_f32_16x16x32_bf16 v[156:159], v[212:215], a[36:39], v[156:159]\n"
"ds_read_b64_tr_b16 a[218:219], v236 offset:8200\n"
"v_subrev_f32 v140, s22, v140\n"
"v_mfma_f32_16x16x32_bf16 v[156:159], v[216:219], a[40:43], v[156:159]\n"
"s_add_u32 s40, s41, s52\n"
"ds_read_b64_tr_b16 v[112:113], v229 offset:49152\n"
"ds_read_b64_tr_b16 v[120:121], v229 offset:57344\n"
"v_mfma_f32_16x16x32_bf16 v[156:159], v[220:223], a[44:47], v[156:159]\n"
"s_waitcnt lgkmcnt(5)\n"
"buffer_load_dwordx4 v240, s[28:31], s40 offen offset:1024 lds\n"
"v_mfma_f32_16x16x32_bf16 v[172:175], v[220:223], a[228:231], v[172:175]\n"
"s_add_u32 s40, s41, s53\n"
"v_add_u32 v236, s94, v243\n"
"v_mov_b64 v[12:13], s[22:23]\n"
"v_mfma_f32_16x16x32_bf16 v[172:175], v[216:219], a[224:227], v[172:175]\n"
"buffer_load_dwordx4 v240, s[28:31], s40 offen offset:2048 lds\n"
"v_mfma_f32_16x16x32_bf16 v[172:175], v[212:215], a[212:215], v[172:175]\n"
"v_lshl_add_u32 v254, v255, 5, v254\n"
"v_subrev_f32 v124, s22, v124\n"
"v_mfma_f32_16x16x32_bf16 v[172:175], v[208:211], a[208:211], v[172:175]\n"
"v_mov_b64 v[2:3], s[22:23]\n"
"v_subrev_f32 v159, s22, v159\n"
"v_mfma_f32_16x16x32_bf16 v[172:175], v[192:195], a[236:239], v[172:175]\n"
"s_add_u32 s40, s41, s54\n"
"ds_read_b64_tr_b16 v[122:123], v229 offset:59392\n"
"ds_read_b64_tr_b16 v[226:227], v233 offset:59392\n"
"v_mfma_f32_16x16x32_bf16 v[172:175], v[188:191], a[232:235], v[172:175]\n"
"s_waitcnt lgkmcnt(4)\n"
"buffer_load_dwordx4 v240, s[28:31], s40 offen offset:3072 lds\n"
"v_mfma_f32_16x16x32_bf16 v[172:175], v[184:187], a[220:223], v[172:175]\n"
"s_lshl_b32 s62, s70, 9\n"
"v_mov_b64 v[92:93], 0\n"
"v_mfma_f32_16x16x32_bf16 v[172:175], v[180:183], a[216:219], v[172:175]\n"
"s_and_b32 s70, s88, 15\n"
"v_mov_b64 v[32:33], s[22:23]\n"
"v_subrev_f32 v125, s22, v125\n"
"v_mfma_f32_16x16x32_bf16 v[128:131], v[96:99], a[48:51], v[128:131]\n"
"s_lshr_b32 s88, s88, 4\n"
"ds_read_b64_tr_b16 v[180:181], v230 offset:32768\n"
"v_subrev_f32 v156, s22, v156\n"
"v_mfma_f32_16x16x32_bf16 v[128:131], v[100:103], a[52:55], v[128:131]\n"
"s_lshl_b32 s70, s70, 7\n"
"ds_read_b64_tr_b16 v[188:189], v230 offset:40960\n"
"v_bfe_u32 v255, v242, 9, 1\n"
"v_mfma_f32_16x16x32_bf16 v[128:131], v[104:107], a[56:59], v[128:131]\n"
"s_xor_b32 s71, s71, s75\n"
"v_cvt_pk_bf16_f32 v124, v124, v125\n"
"v_mfma_f32_16x16x32_bf16 v[128:131], v[108:111], a[60:63], v[128:131]\n"
"s_waitcnt lgkmcnt(4)\n"
"v_lshlrev_b32_e64 v201, 2, s72\n"
"ds_read_b64_tr_b16 v[216:217], v230 offset:57344\n"
"v_subrev_f32 v142, s22, v142\n"
"v_mfma_f32_16x16x32_bf16 v[128:131], v[112:115], a[0:3], v[128:131]\n"
"global_load_dword v200, v201, s[6:7] sc1\n"
"v_mfma_f32_16x16x32_bf16 v[128:131], v[116:119], a[4:7], v[128:131]\n"
"s_waitcnt lgkmcnt(3)\n"
"v_mov_b64 v[0:1], s[22:23]\n"
"v_subrev_f32 v127, s22, v127\n"
"v_mfma_f32_16x16x32_bf16 v[128:131], v[120:123], a[8:11], v[128:131]\n"
"v_mov_b64 v[86:87], 0\n"
"v_mfma_f32_16x16x32_bf16 v[128:131], v[224:227], a[12:15], v[128:131]\n"
"ds_read_b64_tr_b16 v[220:221], v234 offset:57344\n"
"v_mov_b64 v[16:17], s[22:23]\n"
"v_mfma_f32_16x16x32_bf16 v[144:147], v[224:227], a[28:31], v[144:147]\n"
"v_lshl_add_u32 v243, s44, 2, v243\n"
"v_mfma_f32_16x16x32_bf16 v[144:147], v[120:123], a[24:27], v[144:147]\n"
"ds_read_b64_tr_b16 v[186:187], v234 offset:34816\n"
"v_mov_b64 v[36:37], s[22:23]\n"
"v_mfma_f32_16x16x32_bf16 v[144:147], v[116:119], a[20:23], v[144:147]\n"
"ds_read_b64_tr_b16 v[222:223], v234 offset:59392\n"
"v_subrev_f32 v126, s22, v126\n"
"v_mfma_f32_16x16x32_bf16 v[144:147], v[112:115], a[16:19], v[144:147]\n"
"v_subrev_f32 v128, s22, v128\n"
"v_mfma_f32_16x16x32_bf16 v[144:147], v[108:111], a[76:79], v[144:147]\n"
"ds_read_b64_tr_b16 v[210:211], v230 offset:51200\n"
"v_subrev_f32 v129, s22, v129\n"
"v_mfma_f32_16x16x32_bf16 v[144:147], v[104:107], a[72:75], v[144:147]\n"
"v_subrev_f32 v130, s22, v130\n"
"v_subrev_f32 v131, s22, v131\n"
"v_mfma_f32_16x16x32_bf16 v[144:147], v[100:103], a[68:71], v[144:147]\n"
"ds_read_b64_tr_b16 v[214:215], v234 offset:51200\n"
"v_cvt_pk_bf16_f32 v125, v126, v127\n"
"v_mfma_f32_16x16x32_bf16 v[144:147], v[96:99], a[64:67], v[144:147]\n"
"ds_read_b64_tr_b16 v[190:191], v230 offset:43008\n"
"v_mov_b64 v[14:15], s[22:23]\n"
"v_mfma_f32_16x16x32_bf16 v[160:163], v[96:99], a[80:83], v[160:163]\n"
"v_lshl_add_u32 v254, v255, 4, v254\n"
"v_subrev_f32 v158, s22, v158\n"
"v_mfma_f32_16x16x32_bf16 v[160:163], v[100:103], a[84:87], v[160:163]\n"
"ds_read_b64_tr_b16 v[184:185], v234 offset:32768\n"
"v_subrev_f32 v143, s22, v143\n"
"v_mfma_f32_16x16x32_bf16 v[160:163], v[104:107], a[88:91], v[160:163]\n"
"v_cvt_pk_bf16_f32 v126, v128, v129\n"
"v_mfma_f32_16x16x32_bf16 v[160:163], v[108:111], a[92:95], v[160:163]\n"
"ds_read_b64_tr_b16 v[182:183], v230 offset:34816\n"
"v_cvt_pk_bf16_f32 v140, v140, v141\n"
"v_mfma_f32_16x16x32_bf16 v[160:163], v[112:115], a[32:35], v[160:163]\n"
"ds_read_b64_tr_b16 v[208:209], v230 offset:49152\n"
"v_cvt_pk_bf16_f32 v141, v142, v143\n"
"v_mfma_f32_16x16x32_bf16 v[160:163], v[116:119], a[36:39], v[160:163]\n"
"v_subrev_f32 v172, s22, v172\n"
"v_xor_b32 v233, s75, v233\n"
"v_mfma_f32_16x16x32_bf16 v[160:163], v[120:123], a[40:43], v[160:163]\n"
"ds_read_b64_tr_b16 v[194:195], v234 offset:43008\n"
"v_cvt_pk_bf16_f32 v127, v130, v131\n"
"v_mfma_f32_16x16x32_bf16 v[160:163], v[224:227], a[44:47], v[160:163]\n"
"buffer_store_dwordx4 v[124:127], v243, s[32:35], s16 offen sc0 sc1\n"
"v_subrev_f32 v157, s22, v157\n"
"v_mfma_f32_16x16x32_bf16 v[176:179], v[224:227], a[228:231], v[176:179]\n"
"ds_read_b64_tr_b16 v[218:219], v230 offset:59392\n"
"v_subrev_f32 v145, s22, v145\n"
"v_mfma_f32_16x16x32_bf16 v[176:179], v[120:123], a[224:227], v[176:179]\n"
"ds_read_b64_tr_b16 v[192:193], v234 offset:40960\n"
"v_mov_b64 v[244:245], 0\n"
"v_mfma_f32_16x16x32_bf16 v[176:179], v[116:119], a[212:215], v[176:179]\n"
"ds_read_b64_tr_b16 v[212:213], v234 offset:49152\n"
"v_subrev_f32 v144, s22, v144\n"
"v_mfma_f32_16x16x32_bf16 v[176:179], v[112:115], a[208:211], v[176:179]\n"
"v_subrev_f32 v163, s22, v163\n"
"v_mfma_f32_16x16x32_bf16 v[176:179], v[108:111], a[236:239], v[176:179]\n"
"ds_read_b64_tr_b16 v[224:225], v235 offset:57344\n"
"v_lshl_add_u32 v248, v242, 8, s93\n"
"v_mfma_f32_16x16x32_bf16 v[176:179], v[104:107], a[232:235], v[176:179]\n"
"v_subrev_f32 v146, s22, v146\n"
"v_subrev_f32 v147, s22, v147\n"
"v_mfma_f32_16x16x32_bf16 v[176:179], v[100:103], a[220:223], v[176:179]\n"
"ds_read_b64_tr_b16 v[226:227], v235 offset:59392\n"
"v_mfma_f32_16x16x32_bf16 v[176:179], v[96:99], a[216:219], v[176:179]\n"
"s_waitcnt lgkmcnt(5)\n"
"v_cvt_pk_bf16_f32 v156, v156, v157\n"
"v_mfma_f32_16x16x32_bf16 v[132:135], v[180:183], a[48:51], v[132:135]\n"
"ds_read_b64_tr_b16 v[102:103], v235 offset:34816\n"
"v_subrev_f32 v162, s22, v162\n"
"v_mfma_f32_16x16x32_bf16 v[132:135], v[184:187], a[52:55], v[132:135]\n"
"v_cvt_pk_bf16_f32 v157, v158, v159\n"
"v_xor_b32 v229, s75, v229\n"
"v_mfma_f32_16x16x32_bf16 v[132:135], v[188:191], a[56:59], v[132:135]\n"
"s_waitcnt lgkmcnt(3)\n"
"ds_read_b64_tr_b16 v[114:115], v231 offset:51200\n"
"v_mov_b64 v[88:89], 0\n"
"v_mfma_f32_16x16x32_bf16 v[132:135], v[192:195], a[60:63], v[132:135]\n"
"v_mov_b64 v[22:23], s[22:23]\n"
"v_mov_b64 v[94:95], 0\n"
"v_mfma_f32_16x16x32_bf16 v[132:135], v[208:211], a[0:3], v[132:135]\n"
"ds_read_b64_tr_b16 v[104:105], v231 offset:40960\n"
"v_subrev_f32 v174, s22, v174\n"
"v_mfma_f32_16x16x32_bf16 v[132:135], v[212:215], a[4:7], v[132:135]\n"
"v_subrev_f32 v173, s22, v173\n"
"v_cvt_pk_bf16_f32 v142, v144, v145\n"
"v_mfma_f32_16x16x32_bf16 v[132:135], v[216:219], a[8:11], v[132:135]\n"
"ds_read_b64_tr_b16 v[108:109], v235 offset:40960\n"
"v_cvt_pk_bf16_f32 v143, v146, v147\n"
"v_mfma_f32_16x16x32_bf16 v[132:135], v[220:223], a[12:15], v[132:135]\n"
"buffer_store_dwordx4 v[140:143], v243, s[32:35], s16 offen offset:2048 sc0 sc1\n"
"ds_read_b64_tr_b16 v[96:97], v231 offset:32768\n"
"v_mfma_f32_16x16x32_bf16 v[148:151], v[220:223], a[28:31], v[148:151]\n"
"ds_read_b64_tr_b16 v[110:111], v235 offset:43008\n"
"v_mov_b64 v[18:19], s[22:23]\n"
"v_mfma_f32_16x16x32_bf16 v[148:151], v[216:219], a[24:27], v[148:151]\n"
"v_bfe_u32 v237, v242, 4, 3\n"
"v_cvt_pk_bf16_f32 v159, v162, v163\n"
"v_mfma_f32_16x16x32_bf16 v[148:151], v[212:215], a[20:23], v[148:151]\n"
"ds_read_b64_tr_b16 v[120:121], v231 offset:57344\n"
"v_add_u32 v241, 1, v241\n"
"v_mfma_f32_16x16x32_bf16 v[148:151], v[208:211], a[16:19], v[148:151]\n"
"ds_read_b64_tr_b16 v[100:101], v235 offset:32768\n"
"v_mfma_f32_16x16x32_bf16 v[148:151], v[192:195], a[76:79], v[148:151]\n"
"ds_read_b64_tr_b16 v[98:99], v231 offset:34816\n"
"v_max_u32 v237, 1, v237\n"
"v_mfma_f32_16x16x32_bf16 v[148:151], v[188:191], a[72:75], v[148:151]\n"
"v_subrev_f32 v160, s22, v160\n"
"v_subrev_f32 v161, s22, v161\n"
"v_mfma_f32_16x16x32_bf16 v[148:151], v[184:187], a[68:71], v[148:151]\n"
"ds_read_b64_tr_b16 v[106:107], v231 offset:43008\n"
"v_subrev_f32 v179, s22, v179\n"
"v_mfma_f32_16x16x32_bf16 v[148:151], v[180:183], a[64:67], v[148:151]\n"
"v_mov_b64 v[30:31], s[22:23]\n"
"v_subrev_f32 v176, s22, v176\n"
"v_mfma_f32_16x16x32_bf16 v[164:167], v[180:183], a[80:83], v[164:167]\n"
"ds_read_b64_tr_b16 v[112:113], v231 offset:49152\n"
"v_mov_b64 v[38:39], s[22:23]\n"
"v_mfma_f32_16x16x32_bf16 v[164:167], v[184:187], a[84:87], v[164:167]\n"
"ds_read_b64_tr_b16 v[118:119], v235 offset:51200\n"
"v_subrev_f32 v132, s22, v132\n"
"v_mfma_f32_16x16x32_bf16 v[164:167], v[188:191], a[88:91], v[164:167]\n"
"v_xor_b32 v237, s61, v237\n"
"v_subrev_f32 v133, s22, v133\n"
"v_mfma_f32_16x16x32_bf16 v[164:167], v[192:195], a[92:95], v[164:167]\n"
"ds_read_b64_tr_b16 v[116:117], v235 offset:49152\n"
"v_cvt_pk_bf16_f32 v158, v160, v161\n"
"v_mfma_f32_16x16x32_bf16 v[164:167], v[208:211], a[32:35], v[164:167]\n"
"v_mov_b64 v[34:35], s[22:23]\n"
"v_subrev_f32 v175, s22, v175\n"
"v_mfma_f32_16x16x32_bf16 v[164:167], v[212:215], a[36:39], v[164:167]\n"
"ds_read_b64_tr_b16 v[122:123], v231 offset:59392\n"
"v_mfma_f32_16x16x32_bf16 v[164:167], v[216:219], a[40:43], v[164:167]\n"
"v_add_u32 v238, s97, v254\n"
"v_mov_b64 v[54:55], s[22:23]\n"
"v_mfma_f32_16x16x32_bf16 v[164:167], v[220:223], a[44:47], v[164:167]\n"
"s_barrier\n"
"s_waitcnt lgkmcnt(4)\n"
"v_mfma_f32_16x16x32_bf16 v[136:139], v[96:99], a[48:51], v[136:139]\n"
"ds_read_b64_tr_b16 v[182:183], v228 offset:2048\n"
"v_subrev_f32 v178, s22, v178\n"
"v_mfma_f32_16x16x32_bf16 v[136:139], v[100:103], a[52:55], v[136:139]\n"
"ds_read_b64_tr_b16 v[222:223], v232 offset:26624\n"
"v_add_u32 v253, s99, v254\n"
"v_mfma_f32_16x16x32_bf16 v[136:139], v[104:107], a[56:59], v[136:139]\n"
"ds_read_b64_tr_b16 v[186:187], v232 offset:2048\n"
"v_mov_b64 v[42:43], s[22:23]\n"
"v_mfma_f32_16x16x32_bf16 v[136:139], v[108:111], a[60:63], v[136:139]\n"
"s_waitcnt lgkmcnt(4)\n"
"ds_read_b64_tr_b16 v[194:195], v232 offset:10240\n"
"v_mov_b64 v[50:51], s[22:23]\n"
"v_mfma_f32_16x16x32_bf16 v[136:139], v[112:115], a[0:3], v[136:139]\n"
"ds_read_b64_tr_b16 v[210:211], v228 offset:18432\n"
"v_subrev_f32 v135, s22, v135\n"
"v_mfma_f32_16x16x32_bf16 v[136:139], v[116:119], a[4:7], v[136:139]\n"
"s_waitcnt lgkmcnt(5)\n"
"ds_read_b64_tr_b16 v[192:193], v232 offset:8192\n"
"v_mfma_f32_16x16x32_bf16 v[136:139], v[120:123], a[8:11], v[136:139]\n"
"ds_read_b64_tr_b16 v[188:189], v228 offset:8192\n"
"v_xor_b32 v239, 64, v238\n"
"v_mfma_f32_16x16x32_bf16 v[136:139], v[224:227], a[12:15], v[136:139]\n"
"buffer_store_dwordx4 v[156:159], v243, s[32:35], s17 offen sc0 sc1\n"
"ds_read_b64_tr_b16 v[214:215], v232 offset:18432\n"
"v_mfma_f32_16x16x32_bf16 v[152:155], v[224:227], a[28:31], v[152:155]\n"
"ds_read_b64_tr_b16 v[190:191], v228 offset:10240\n"
"v_subrev_f32 v134, s22, v134\n"
"v_mfma_f32_16x16x32_bf16 v[152:155], v[120:123], a[24:27], v[152:155]\n"
"ds_read_b64_tr_b16 v[208:209], v228 offset:16384\n"
"v_add_u32 v252, s98, v254\n"
"v_mfma_f32_16x16x32_bf16 v[152:155], v[116:119], a[20:23], v[152:155]\n"
"ds_read_b64_tr_b16 v[184:185], v232 offset:0\n"
"v_cvt_pk_bf16_f32 v172, v172, v173\n"
"v_mfma_f32_16x16x32_bf16 v[152:155], v[112:115], a[16:19], v[152:155]\n"
"ds_read_b64_tr_b16 v[216:217], v228 offset:24576\n"
"v_mov_b64 v[8:9], s[22:23]\n"
"v_mfma_f32_16x16x32_bf16 v[152:155], v[108:111], a[76:79], v[152:155]\n"
"ds_read_b64_tr_b16 v[218:219], v228 offset:26624\n"
"v_mov_b64 v[52:53], s[22:23]\n"
"v_mfma_f32_16x16x32_bf16 v[152:155], v[104:107], a[72:75], v[152:155]\n"
"ds_read_b64_tr_b16 v[212:213], v232 offset:16384\n"
"v_mov_b64 v[84:85], 0\n"
"v_mfma_f32_16x16x32_bf16 v[152:155], v[100:103], a[68:71], v[152:155]\n"
"ds_read_b64_tr_b16 v[220:221], v232 offset:24576\n"
"v_cvt_pk_bf16_f32 v173, v174, v175\n"
"v_mfma_f32_16x16x32_bf16 v[152:155], v[96:99], a[64:67], v[152:155]\n"
"ds_read_b64_tr_b16 v[180:181], v228 offset:0\n"
"v_mov_b64 v[28:29], s[22:23]\n"
"v_mfma_f32_16x16x32_bf16 v[168:171], v[96:99], a[80:83], v[168:171]\n"
"ds_read_b64_tr_b16 v[96:97], v229 offset:0\n"
"ds_read_b64_tr_b16 v[98:99], v229 offset:2048\n"
"v_mfma_f32_16x16x32_bf16 v[168:171], v[100:103], a[84:87], v[168:171]\n"
"ds_read_b64_tr_b16 v[100:101], v233 offset:0\n"
"ds_read_b64_tr_b16 v[102:103], v233 offset:2048\n"
"v_mfma_f32_16x16x32_bf16 v[168:171], v[104:107], a[88:91], v[168:171]\n"
"ds_read_b64_tr_b16 v[104:105], v229 offset:8192\n"
"v_mfma_f32_16x16x32_bf16 v[168:171], v[108:111], a[92:95], v[168:171]\n"
"v_cvt_pk_bf16_f32 v175, v178, v179\n"
"v_mfma_f32_16x16x32_bf16 v[168:171], v[112:115], a[32:35], v[168:171]\n"
"v_mov_b64 v[48:49], s[22:23]\n"
"v_subrev_f32 v177, s22, v177\n"
"v_mfma_f32_16x16x32_bf16 v[168:171], v[116:119], a[36:39], v[168:171]\n"
"v_lshlrev_b32 v237, 2, v237\n"
"v_mov_b64 v[46:47], s[22:23]\n"
"v_mfma_f32_16x16x32_bf16 v[168:171], v[120:123], a[40:43], v[168:171]\n"
"s_add_u32 s47, s47, 1\n"
"v_cvt_pk_bf16_f32 v132, v132, v133\n"
"v_mfma_f32_16x16x32_bf16 v[168:171], v[224:227], a[44:47], v[168:171]\n"
"s_waitcnt lgkmcnt(5)\n"
"v_mov_b64 v[4:5], s[22:23]\n"
"v_cvt_pk_bf16_f32 v174, v176, v177\n"
"LGL_%=:\n"
"v_mfma_f32_16x16x32_bf16 v[124:127], v[180:183], a[96:99], v[196:199]\n"
"ds_read_b64_tr_b16 v[106:107], v229 offset:10240\n"
"v_cvt_pk_bf16_f32 v133, v134, v135\n"
"v_mfma_f32_16x16x32_bf16 v[124:127], v[184:187], a[100:103], v[124:127]\n"
"ds_read_b64_tr_b16 v[108:109], v233 offset:8192\n"
"v_subrev_f32 v136, s22, v136\n"
"v_mfma_f32_16x16x32_bf16 v[124:127], v[188:191], a[104:107], v[124:127]\n"
"ds_read_b64_tr_b16 v[110:111], v233 offset:10240\n"
"v_subrev_f32 v137, s22, v137\n"
"v_mfma_f32_16x16x32_bf16 v[124:127], v[192:195], a[108:111], v[124:127]\n"
"ds_read_b64_tr_b16 v[112:113], v229 offset:16384\n"
"v_subrev_f32 v138, s22, v138\n"
"v_mfma_f32_16x16x32_bf16 v[124:127], v[208:211], a[144:147], v[124:127]\n"
"ds_read_b64_tr_b16 v[114:115], v229 offset:18432\n"
"v_subrev_f32 v139, s22, v139\n"
"v_mfma_f32_16x16x32_bf16 v[124:127], v[212:215], a[148:151], v[124:127]\n"
"s_waitcnt lgkmcnt(7)\n"
"ds_read_b64_tr_b16 v[116:117], v233 offset:16384\n"
"v_cvt_pk_bf16_f32 v134, v136, v137\n"
"v_mfma_f32_16x16x32_bf16 v[124:127], v[216:219], a[152:155], v[124:127]\n"
"ds_read_b64_tr_b16 v[118:119], v233 offset:18432\n"
"v_cvt_pk_bf16_f32 v135, v138, v139\n"
"v_mfma_f32_16x16x32_bf16 v[124:127], v[220:223], a[156:159], v[124:127]\n"
"buffer_store_dwordx4 v[172:175], v243, s[32:35], s17 offen offset:2048 sc0 sc1\n"
"v_subrev_f32 v148, s22, v148\n"
"v_mfma_f32_16x16x32_bf16 v[140:143], v[220:223], a[172:175], v[196:199]\n"
"ds_read_b64_tr_b16 v[120:121], v229 offset:24576\n"
"v_subrev_f32 v149, s22, v149\n"
"v_mfma_f32_16x16x32_bf16 v[140:143], v[216:219], a[168:171], v[140:143]\n"
"ds_read_b64_tr_b16 v[122:123], v229 offset:26624\n"
"v_subrev_f32 v150, s22, v150\n"
"v_mfma_f32_16x16x32_bf16 v[140:143], v[212:215], a[164:167], v[140:143]\n"
"ds_read_b64_tr_b16 v[224:225], v233 offset:24576\n"
"v_subrev_f32 v151, s22, v151\n"
"v_mfma_f32_16x16x32_bf16 v[140:143], v[208:211], a[160:163], v[140:143]\n"
"ds_read_b64_tr_b16 v[226:227], v233 offset:26624\n"
"v_cvt_pk_bf16_f32 v148, v148, v149\n"
"v_mfma_f32_16x16x32_bf16 v[140:143], v[192:195], a[124:127], v[140:143]\n"
"v_cvt_pk_bf16_f32 v149, v150, v151\n"
"v_subrev_f32 v152, s22, v152\n"
"v_mfma_f32_16x16x32_bf16 v[140:143], v[188:191], a[120:123], v[140:143]\n"
"s_add_u32 s41, s70, 0\n"
"v_subrev_f32 v153, s22, v153\n"
"v_subrev_f32 v154, s22, v154\n"
"v_mfma_f32_16x16x32_bf16 v[140:143], v[184:187], a[116:119], v[140:143]\n"
"s_mov_b32 m0, s71\n"
"v_subrev_f32 v155, s22, v155\n"
"v_cvt_pk_bf16_f32 v150, v152, v153\n"
"v_mfma_f32_16x16x32_bf16 v[140:143], v[180:183], a[112:115], v[140:143]\n"
"s_waitcnt vmcnt(14)\n"
"buffer_load_dwordx4 v240, s[28:31], s41 offen lds\n"
"v_mfma_f32_16x16x32_bf16 v[156:159], v[180:183], a[128:131], v[196:199]\n"
"v_xor_b32 v230, s75, v230\n"
"v_cvt_pk_bf16_f32 v151, v154, v155\n"
"v_mfma_f32_16x16x32_bf16 v[156:159], v[184:187], a[132:135], v[156:159]\n"
"v_xor_b32 v234, s75, v234\n"
"v_subrev_f32 v164, s22, v164\n"
"v_mfma_f32_16x16x32_bf16 v[156:159], v[188:191], a[136:139], v[156:159]\n"
"v_subrev_f32 v165, s22, v165\n"
"v_subrev_f32 v166, s22, v166\n"
"v_mfma_f32_16x16x32_bf16 v[156:159], v[192:195], a[140:143], v[156:159]\n"
"v_subrev_f32 v167, s22, v167\n"
"v_cvt_pk_bf16_f32 v164, v164, v165\n"
"v_mfma_f32_16x16x32_bf16 v[156:159], v[208:211], a[176:179], v[156:159]\n"
"v_cvt_pk_bf16_f32 v165, v166, v167\n"
"v_subrev_f32 v168, s22, v168\n"
"v_mfma_f32_16x16x32_bf16 v[156:159], v[212:215], a[180:183], v[156:159]\n"
"v_subrev_f32 v169, s22, v169\n"
"v_subrev_f32 v170, s22, v170\n"
"v_mfma_f32_16x16x32_bf16 v[156:159], v[216:219], a[184:187], v[156:159]\n"
"v_subrev_f32 v171, s22, v171\n"
"v_cvt_pk_bf16_f32 v166, v168, v169\n"
"v_mfma_f32_16x16x32_bf16 v[156:159], v[220:223], a[188:191], v[156:159]\n"
"buffer_store_dwordx4 v[132:135], v243, s[32:35], s16 offen offset:1024 sc0 sc1\n"
"v_cvt_pk_bf16_f32 v167, v170, v171\n"
"v_mfma_f32_16x16x32_bf16 v[172:175], v[220:223], a[252:255], v[196:199]\n"
"ds_read_b64_tr_b16 v[220:221], v234 offset:24576\n"
"v_xor_b32 v231, s75, v231\n"
"v_mfma_f32_16x16x32_bf16 v[172:175], v[216:219], a[248:251], v[172:175]\n"
"ds_read_b64_tr_b16 v[216:217], v230 offset:24576\n"
"v_xor_b32 v235, s75, v235\n"
"v_mfma_f32_16x16x32_bf16 v[172:175], v[212:215], a[244:247], v[172:175]\n"
"ds_read_b64_tr_b16 v[212:213], v234 offset:16384\n"
"v_lshlrev_b32 v202, 16, v84\n"
"v_mfma_f32_16x16x32_bf16 v[172:175], v[208:211], a[240:243], v[172:175]\n"
"ds_read_b64_tr_b16 v[208:209], v230 offset:16384\n"
"v_and_b32 v84, s73, v84\n"
"v_mfma_f32_16x16x32_bf16 v[172:175], v[192:195], a[204:207], v[172:175]\n"
"ds_read_b64_tr_b16 v[192:193], v234 offset:8192\n"
"v_add_f32 v25, v25, v84\n"
"v_mfma_f32_16x16x32_bf16 v[172:175], v[188:191], a[200:203], v[172:175]\n"
"ds_read_b64_tr_b16 v[188:189], v230 offset:8192\n"
"v_lshlrev_b32 v203, 16, v85\n"
"v_mfma_f32_16x16x32_bf16 v[172:175], v[184:187], a[196:199], v[172:175]\n"
"s_add_u32 s40, s41, s52\n"
"ds_read_b64_tr_b16 v[184:185], v234 offset:0\n"
"v_and_b32 v85, s73, v85\n"
"v_mfma_f32_16x16x32_bf16 v[172:175], v[180:183], a[192:195], v[172:175]\n"
"s_waitcnt vmcnt(13) lgkmcnt(7)\n"
"buffer_load_dwordx4 v240, s[28:31], s40 offen offset:1024 lds\n"
"v_mfma_f32_16x16x32_bf16 v[128:131], v[96:99], a[96:99], v[196:199]\n"
"s_waitcnt lgkmcnt(6)\n"
"ds_read_b64_tr_b16 v[180:181], v230 offset:0\n"
"v_add_f32 v27, v27, v85\n"
"v_mfma_f32_16x16x32_bf16 v[128:131], v[100:103], a[100:103], v[128:131]\n"
"ds_read_b64_tr_b16 v[182:183], v230 offset:2048\n"
"v_lshlrev_b32 v204, 16, v86\n"
"v_mfma_f32_16x16x32_bf16 v[128:131], v[104:107], a[104:107], v[128:131]\n"
"ds_read_b64_tr_b16 v[186:187], v234 offset:2048\n"
"v_and_b32 v86, s73, v86\n"
"v_mfma_f32_16x16x32_bf16 v[128:131], v[108:111], a[108:111], v[128:131]\n"
"ds_read_b64_tr_b16 v[190:191], v230 offset:10240\n"
"v_add_f32 v29, v29, v86\n"
"v_mfma_f32_16x16x32_bf16 v[128:131], v[112:115], a[144:147], v[128:131]\n"
"s_waitcnt vmcnt(6)\n"
"ds_read_b64_tr_b16 v[194:195], v234 offset:10240\n"
"v_lshlrev_b32 v205, 16, v87\n"
"v_mfma_f32_16x16x32_bf16 v[128:131], v[116:119], a[148:151], v[128:131]\n"
"v_subrev_u32 v200, s21, v200\n"
"ds_read_b64_tr_b16 v[210:211], v230 offset:18432\n"
"v_mfma_f32_16x16x32_bf16 v[128:131], v[120:123], a[152:155], v[128:131]\n"
"v_cmp_lt_u32 vcc, s69, v200\n"
"ds_read_b64_tr_b16 v[214:215], v234 offset:18432\n"
"v_mfma_f32_16x16x32_bf16 v[128:131], v[224:227], a[156:159], v[128:131]\n"
"ds_read_b64_tr_b16 v[218:219], v230 offset:26624\n"
"s_cbranch_vccnz LGP_%=\nLGPK_%=:\n"
"v_mfma_f32_16x16x32_bf16 v[144:147], v[224:227], a[172:175], v[196:199]\n"
"s_sub_u32 s72, s72, 1\n"
"ds_read_b64_tr_b16 v[222:223], v234 offset:26624\n"
"v_and_b32 v87, s73, v87\n"
"v_mfma_f32_16x16x32_bf16 v[144:147], v[120:123], a[168:171], v[144:147]\n"
"s_and_b32 s72, s72, 7\n"
"v_add_f32 v24, v24, v202\n"
"v_add_f32 v31, v31, v87\n"
"v_mfma_f32_16x16x32_bf16 v[144:147], v[116:119], a[164:167], v[144:147]\n"
"v_add_f32 v26, v26, v203\n"
"v_lshlrev_b32 v206, 16, v88\n"
"v_mfma_f32_16x16x32_bf16 v[144:147], v[112:115], a[160:163], v[144:147]\n"
"buffer_load_dwordx4 v[72:75], v243, s[32:35], s18 offen sc1\n"
"v_mfma_f32_16x16x32_bf16 v[144:147], v[108:111], a[124:127], v[144:147]\n"
"v_and_b32 v88, s73, v88\n"
"v_add_f32 v33, v33, v88\n"
"v_mfma_f32_16x16x32_bf16 v[144:147], v[104:107], a[120:123], v[144:147]\n"
"v_lshlrev_b32 v207, 16, v89\n"
"v_and_b32 v89, s73, v89\n"
"v_mfma_f32_16x16x32_bf16 v[144:147], v[100:103], a[116:119], v[144:147]\n"
"v_add_f32 v35, v35, v89\n"
"v_lshlrev_b32 v202, 16, v90\n"
"v_mfma_f32_16x16x32_bf16 v[144:147], v[96:99], a[112:115], v[144:147]\n"
"buffer_store_dwordx4 v[148:151], v243, s[32:35], s16 offen offset:3072 sc0 sc1\n"
"v_and_b32 v90, s73, v90\n"
"v_mfma_f32_16x16x32_bf16 v[160:163], v[96:99], a[128:131], v[196:199]\n"
"v_add_f32 v37, v37, v90\n"
"v_lshlrev_b32 v203, 16, v91\n"
"v_mfma_f32_16x16x32_bf16 v[160:163], v[100:103], a[132:135], v[160:163]\n"
"v_and_b32 v91, s73, v91\n"
"v_add_f32 v39, v39, v91\n"
"v_mfma_f32_16x16x32_bf16 v[160:163], v[104:107], a[136:139], v[160:163]\n"
"v_add_f32 v28, v28, v204\n"
"v_add_f32 v30, v30, v205\n"
"v_mfma_f32_16x16x32_bf16 v[160:163], v[108:111], a[140:143], v[160:163]\n"
"v_add_f32 v32, v32, v206\n"
"v_add_f32 v34, v34, v207\n"
"v_mfma_f32_16x16x32_bf16 v[160:163], v[112:115], a[176:179], v[160:163]\n"
"v_lshlrev_b32 v204, 16, v92\n"
"v_and_b32 v92, s73, v92\n"
"v_mfma_f32_16x16x32_bf16 v[160:163], v[116:119], a[180:183], v[160:163]\n"
"v_add_f32 v41, v41, v92\n"
"v_lshlrev_b32 v205, 16, v93\n"
"v_mfma_f32_16x16x32_bf16 v[160:163], v[120:123], a[184:187], v[160:163]\n"
"s_add_u32 s40, s41, s53\n"
"v_and_b32 v93, s73, v93\n"
"v_add_f32 v43, v43, v93\n"
"v_mfma_f32_16x16x32_bf16 v[160:163], v[224:227], a[188:191], v[160:163]\n"
"buffer_load_dwordx4 v240, s[28:31], s40 offen offset:2048 lds\n"
"v_mfma_f32_16x16x32_bf16 v[176:179], v[224:227], a[252:255], v[196:199]\n"
"ds_read_b64_tr_b16 v[224:225], v235 offset:24576\n"
"v_lshlrev_b32 v206, 16, v94\n"
"v_mfma_f32_16x16x32_bf16 v[176:179], v[120:123], a[248:251], v[176:179]\n"
"ds_read_b64_tr_b16 v[120:121], v231 offset:24576\n"
"v_and_b32 v94, s73, v94\n"
"v_mfma_f32_16x16x32_bf16 v[176:179], v[116:119], a[244:247], v[176:179]\n"
"ds_read_b64_tr_b16 v[116:117], v235 offset:16384\n"
"v_add_f32 v45, v45, v94\n"
"v_mfma_f32_16x16x32_bf16 v[176:179], v[112:115], a[240:243], v[176:179]\n"
"buffer_load_dwordx4 v[76:79], v243, s[32:35], s18 offen offset:1024 sc1\n"
"v_mfma_f32_16x16x32_bf16 v[176:179], v[108:111], a[204:207], v[176:179]\n"
"ds_read_b64_tr_b16 v[108:109], v235 offset:8192\n"
"v_lshlrev_b32 v207, 16, v95\n"
"v_mfma_f32_16x16x32_bf16 v[176:179], v[104:107], a[200:203], v[176:179]\n"
"ds_read_b64_tr_b16 v[104:105], v231 offset:8192\n"
"v_and_b32 v95, s73, v95\n"
"v_mfma_f32_16x16x32_bf16 v[176:179], v[100:103], a[196:199], v[176:179]\n"
"s_add_u32 s40, s41, s54\n"
"ds_read_b64_tr_b16 v[100:101], v235 offset:0\n"
"v_add_f32 v47, v47, v95\n"
"v_mfma_f32_16x16x32_bf16 v[176:179], v[96:99], a[192:195], v[176:179]\n"
"s_waitcnt lgkmcnt(6)\n"
"buffer_load_dwordx4 v240, s[28:31], s40 offen offset:3072 lds\n"
"v_mfma_f32_16x16x32_bf16 v[132:135], v[180:183], a[96:99], v[196:199]\n"
"ds_read_b64_tr_b16 v[96:97], v231 offset:0\n"
"v_add_f32 v36, v36, v202\n"
"v_mfma_f32_16x16x32_bf16 v[132:135], v[184:187], a[100:103], v[132:135]\n"
"ds_read_b64_tr_b16 v[98:99], v231 offset:2048\n"
"v_add_f32 v38, v38, v203\n"
"v_mfma_f32_16x16x32_bf16 v[132:135], v[188:191], a[104:107], v[132:135]\n"
"ds_read_b64_tr_b16 v[102:103], v235 offset:2048\n"
"v_add_f32 v44, v44, v206\n"
"v_mfma_f32_16x16x32_bf16 v[132:135], v[192:195], a[108:111], v[132:135]\n"
"ds_read_b64_tr_b16 v[106:107], v231 offset:10240\n"
"v_add_f32 v46, v46, v207\n"
"v_mfma_f32_16x16x32_bf16 v[132:135], v[208:211], a[144:147], v[132:135]\n"
"ds_read_b64_tr_b16 v[110:111], v235 offset:10240\n"
"v_add_f32 v40, v40, v204\n"
"v_mfma_f32_16x16x32_bf16 v[132:135], v[212:215], a[148:151], v[132:135]\n"
"ds_read_b64_tr_b16 v[112:113], v231 offset:16384\n"
"v_add_f32 v42, v42, v205\n"
"v_mfma_f32_16x16x32_bf16 v[132:135], v[216:219], a[152:155], v[132:135]\n"
"ds_read_b64_tr_b16 v[114:115], v231 offset:18432\n"
"v_mfma_f32_16x16x32_bf16 v[132:135], v[220:223], a[156:159], v[132:135]\n"
"buffer_store_dwordx4 v[164:167], v243, s[32:35], s17 offen offset:1024 sc0 sc1\n"
"v_lshlrev_b32 v206, 16, v244\n"
"v_mfma_f32_16x16x32_bf16 v[148:151], v[220:223], a[172:175], v[196:199]\n"
"ds_read_b64_tr_b16 v[118:119], v235 offset:18432\n"
"v_and_b32 v244, s73, v244\n"
"v_mfma_f32_16x16x32_bf16 v[148:151], v[216:219], a[168:171], v[148:151]\n"
"ds_read_b64_tr_b16 v[122:123], v231 offset:26624\n"
"v_add_f32 v49, v49, v244\n"
"v_mfma_f32_16x16x32_bf16 v[148:151], v[212:215], a[164:167], v[148:151]\n"
"ds_read_b64_tr_b16 v[226:227], v235 offset:26624\n"
"v_lshlrev_b32 v207, 16, v245\n"
"v_mfma_f32_16x16x32_bf16 v[148:151], v[208:211], a[160:163], v[148:151]\n"
"buffer_load_dwordx4 v[80:83], v243, s[32:35], s18 offen offset:2048 sc1\n"
"v_mfma_f32_16x16x32_bf16 v[148:151], v[192:195], a[124:127], v[148:151]\n"
"v_and_b32 v245, s73, v245\n"
"v_add_f32 v51, v51, v245\n"
"v_mfma_f32_16x16x32_bf16 v[148:151], v[188:191], a[120:123], v[148:151]\n"
"s_add_u32 s41, s70, 163840\n"
"v_lshlrev_b32 v202, 16, v246\n"
"v_and_b32 v246, s73, v246\n"
"v_mfma_f32_16x16x32_bf16 v[148:151], v[184:187], a[116:119], v[148:151]\n"
"s_add_u32 m0, s71, 16384\n"
"v_add_f32 v53, v53, v246\n"
"v_lshlrev_b32 v203, 16, v247\n"
"v_mfma_f32_16x16x32_bf16 v[148:151], v[180:183], a[112:115], v[148:151]\n"
"buffer_load_dwordx4 v240, s[28:31], s41 offen lds\n"
"v_mfma_f32_16x16x32_bf16 v[164:167], v[180:183], a[128:131], v[196:199]\n"
"v_and_b32 v247, s73, v247\n"
"v_add_f32 v55, v55, v247\n"
"v_mfma_f32_16x16x32_bf16 v[164:167], v[184:187], a[132:135], v[164:167]\n"
"v_add_f32 v48, v48, v206\n"
"v_add_f32 v50, v50, v207\n"
"v_mfma_f32_16x16x32_bf16 v[164:167], v[188:191], a[136:139], v[164:167]\n"
"v_add_f32 v52, v52, v202\n"
"v_add_f32 v54, v54, v203\n"
"v_mfma_f32_16x16x32_bf16 v[164:167], v[192:195], a[140:143], v[164:167]\n"
"v_mfma_f32_16x16x32_bf16 v[164:167], v[208:211], a[176:179], v[164:167]\n"
"v_mfma_f32_16x16x32_bf16 v[164:167], v[212:215], a[180:183], v[164:167]\n"
"v_mfma_f32_16x16x32_bf16 v[164:167], v[216:219], a[184:187], v[164:167]\n"
"s_waitcnt vmcnt(12)\n"
"v_mfma_f32_16x16x32_bf16 v[164:167], v[220:223], a[188:191], v[164:167]\n"
"s_barrier\n"
"s_waitcnt lgkmcnt(0)\n"
"v_mfma_f32_16x16x32_bf16 v[136:139], v[96:99], a[96:99], v[196:199]\n"
"ds_read_b64_tr_b16 v[180:181], v228 offset:32768\n"
"v_mfma_f32_16x16x32_bf16 v[136:139], v[100:103], a[100:103], v[136:139]\n"
"ds_read_b64_tr_b16 v[182:183], v228 offset:34816\n"
"v_mfma_f32_16x16x32_bf16 v[136:139], v[104:107], a[104:107], v[136:139]\n"
"ds_read_b64_tr_b16 v[184:185], v232 offset:32768\n"
"v_mfma_f32_16x16x32_bf16 v[136:139], v[108:111], a[108:111], v[136:139]\n"
"ds_read_b64_tr_b16 v[186:187], v232 offset:34816\n"
"v_mfma_f32_16x16x32_bf16 v[136:139], v[112:115], a[144:147], v[136:139]\n"
"ds_read_b64_tr_b16 v[188:189], v228 offset:40960\n"
"v_mfma_f32_16x16x32_bf16 v[136:139], v[116:119], a[148:151], v[136:139]\n"
"ds_read_b64_tr_b16 v[190:191], v228 offset:43008\n"
"v_mfma_f32_16x16x32_bf16 v[136:139], v[120:123], a[152:155], v[136:139]\n"
"s_add_u32 s40, s41, s52\n"
"ds_read_b64_tr_b16 v[192:193], v232 offset:40960\n"
"v_mfma_f32_16x16x32_bf16 v[136:139], v[224:227], a[156:159], v[136:139]\n"
"buffer_load_dwordx4 v240, s[28:31], s40 offen offset:1024 lds\n"
"v_mfma_f32_16x16x32_bf16 v[152:155], v[224:227], a[172:175], v[196:199]\n"
"ds_read_b64_tr_b16 v[194:195], v232 offset:43008\n"
"v_mfma_f32_16x16x32_bf16 v[152:155], v[120:123], a[168:171], v[152:155]\n"
"ds_read_b64_tr_b16 v[208:209], v228 offset:49152\n"
"v_mfma_f32_16x16x32_bf16 v[152:155], v[116:119], a[164:167], v[152:155]\n"
"ds_read_b64_tr_b16 v[210:211], v228 offset:51200\n"
"v_mfma_f32_16x16x32_bf16 v[152:155], v[112:115], a[160:163], v[152:155]\n"
"ds_read_b64_tr_b16 v[212:213], v232 offset:49152\n"
"v_mfma_f32_16x16x32_bf16 v[152:155], v[108:111], a[124:127], v[152:155]\n"
"ds_read_b64_tr_b16 v[214:215], v232 offset:51200\n"
"v_mfma_f32_16x16x32_bf16 v[152:155], v[104:107], a[120:123], v[152:155]\n"
"ds_read_b64_tr_b16 v[216:217], v228 offset:57344\n"
"v_mfma_f32_16x16x32_bf16 v[152:155], v[100:103], a[116:119], v[152:155]\n"
"s_add_u32 s40, s41, s53\n"
"ds_read_b64_tr_b16 v[218:219], v228 offset:59392\n"
"v_xor_b32 v228, s75, v228\n"
"v_mfma_f32_16x16x32_bf16 v[152:155], v[96:99], a[112:115], v[152:155]\n"
"buffer_load_dwordx4 v240, s[28:31], s40 offen offset:2048 lds\n"
"v_mfma_f32_16x16x32_bf16 v[168:171], v[96:99], a[128:131], v[196:199]\n"
"ds_read_b64_tr_b16 v[220:221], v232 offset:57344\n"
"v_mfma_f32_16x16x32_bf16 v[168:171], v[100:103], a[132:135], v[168:171]\n"
"ds_read_b64_tr_b16 v[222:223], v232 offset:59392\n"
"v_xor_b32 v232, s75, v232\n"
"v_mfma_f32_16x16x32_bf16 v[168:171], v[104:107], a[136:139], v[168:171]\n"
"ds_read_b64_tr_b16 v[96:97], v229 offset:32768\n"
"v_mfma_f32_16x16x32_bf16 v[168:171], v[108:111], a[140:143], v[168:171]\n"
"ds_read_b64_tr_b16 v[98:99], v229 offset:34816\n"
"v_mfma_f32_16x16x32_bf16 v[168:171], v[112:115], a[176:179], v[168:171]\n"
"ds_read_b64_tr_b16 v[100:101], v233 offset:32768\n"
"v_mfma_f32_16x16x32_bf16 v[168:171], v[116:119], a[180:183], v[168:171]\n"
"ds_read_b64_tr_b16 v[102:103], v233 offset:34816\n"
"v_mfma_f32_16x16x32_bf16 v[168:171], v[120:123], a[184:187], v[168:171]\n"
"s_add_u32 s40, s41, s54\n"
"ds_read_b64_tr_b16 v[104:105], v229 offset:40960\n"
"v_mfma_f32_16x16x32_bf16 v[168:171], v[224:227], a[188:191], v[168:171]\n"
"s_waitcnt lgkmcnt(7)\n"
"buffer_load_dwordx4 v240, s[28:31], s40 offen offset:3072 lds\n"
"v_mfma_f32_16x16x32_bf16 v[124:127], v[180:183], a[48:51], v[124:127]\n"
"ds_read_b64_tr_b16 v[106:107], v229 offset:43008\n"
"v_mfma_f32_16x16x32_bf16 v[124:127], v[184:187], a[52:55], v[124:127]\n"
"ds_read_b64_tr_b16 v[108:109], v233 offset:40960\n"
"v_mfma_f32_16x16x32_bf16 v[124:127], v[188:191], a[56:59], v[124:127]\n"
"ds_read_b64_tr_b16 v[110:111], v233 offset:43008\n"
"v_mfma_f32_16x16x32_bf16 v[124:127], v[192:195], a[60:63], v[124:127]\n"
"ds_read_b64_tr_b16 v[112:113], v229 offset:49152\n"
"v_mfma_f32_16x16x32_bf16 v[124:127], v[208:211], a[0:3], v[124:127]\n"
"ds_read_b64_tr_b16 v[114:115], v229 offset:51200\n"
"v_mfma_f32_16x16x32_bf16 v[124:127], v[212:215], a[4:7], v[124:127]\n"
"s_add_u32 s41, s70, 327680\n"
"ds_read_b64_tr_b16 v[116:117], v233 offset:49152\n"
"v_mfma_f32_16x16x32_bf16 v[124:127], v[216:219], a[8:11], v[124:127]\n"
"s_waitcnt lgkmcnt(6)\n"
"s_add_u32 m0, s71, 32768\n"
"ds_read_b64_tr_b16 v[118:119], v233 offset:51200\n"
"v_mfma_f32_16x16x32_bf16 v[124:127], v[220:223], a[12:15], v[124:127]\n"
"buffer_load_dwordx4 v240, s[28:31], s41 offen lds\n"
"v_mfma_f32_16x16x32_bf16 v[140:143], v[220:223], a[28:31], v[140:143]\n"
"ds_read_b64_tr_b16 v[120:121], v229 offset:57344\n"
"v_mfma_f32_16x16x32_bf16 v[140:143], v[216:219], a[24:27], v[140:143]\n"
"ds_read_b64_tr_b16 v[122:123], v229 offset:59392\n"
"v_xor_b32 v229, s75, v229\n"
"v_mfma_f32_16x16x32_bf16 v[140:143], v[212:215], a[20:23], v[140:143]\n"
"ds_read_b64_tr_b16 v[224:225], v233 offset:57344\n"
"v_mfma_f32_16x16x32_bf16 v[140:143], v[208:211], a[16:19], v[140:143]\n"
"ds_read_b64_tr_b16 v[226:227], v233 offset:59392\n"
"v_xor_b32 v233, s75, v233\n"
"v_mfma_f32_16x16x32_bf16 v[140:143], v[192:195], a[76:79], v[140:143]\n"
"v_mfma_f32_16x16x32_bf16 v[140:143], v[188:191], a[72:75], v[140:143]\n"
"v_subrev_f32 v124, s22, v124\n"
"v_subrev_f32 v125, s22, v125\n"
"v_mfma_f32_16x16x32_bf16 v[140:143], v[184:187], a[68:71], v[140:143]\n"
"s_add_u32 s40, s41, s52\n"
"v_subrev_f32 v126, s22, v126\n"
"v_subrev_f32 v127, s22, v127\n"
"v_mfma_f32_16x16x32_bf16 v[140:143], v[180:183], a[64:67], v[140:143]\n"
"buffer_load_dwordx4 v240, s[28:31], s40 offen offset:1024 lds\n"
"v_mfma_f32_16x16x32_bf16 v[156:159], v[180:183], a[80:83], v[156:159]\n"
"v_cvt_pk_bf16_f32 v124, v124, v125\n"
"v_cvt_pk_bf16_f32 v125, v126, v127\n"
"v_mfma_f32_16x16x32_bf16 v[156:159], v[184:187], a[84:87], v[156:159]\n"
"v_mfma_f32_16x16x32_bf16 v[156:159], v[188:191], a[88:91], v[156:159]\n"
"v_mfma_f32_16x16x32_bf16 v[156:159], v[192:195], a[92:95], v[156:159]\n"
"v_mfma_f32_16x16x32_bf16 v[156:159], v[208:211], a[32:35], v[156:159]\n"
"v_mfma_f32_16x16x32_bf16 v[156:159], v[212:215], a[36:39], v[156:159]\n"
"v_subrev_f32 v140, s22, v140\n"
"v_subrev_f32 v141, s22, v141\n"
"v_mfma_f32_16x16x32_bf16 v[156:159], v[216:219], a[40:43], v[156:159]\n"
"s_waitcnt vmcnt(7)\n"
"v_subrev_f32 v142, s22, v142\n"
"v_subrev_f32 v143, s22, v143\n"
"v_mfma_f32_16x16x32_bf16 v[156:159], v[220:223], a[44:47], v[156:159]\n"
"s_barrier\n"
"buffer_store_dword v241, v248, s[84:87], 0 offen sc1\n"
"v_mfma_f32_16x16x32_bf16 v[172:175], v[220:223], a[228:231], v[172:175]\n"
"ds_read_b64_tr_b16 v[220:221], v234 offset:57344\n"
"v_cvt_pk_bf16_f32 v140, v140, v141\n"
"v_mfma_f32_16x16x32_bf16 v[172:175], v[216:219], a[224:227], v[172:175]\n"
"ds_read_b64_tr_b16 v[216:217], v230 offset:57344\n"
"v_cvt_pk_bf16_f32 v141, v142, v143\n"
"v_mfma_f32_16x16x32_bf16 v[172:175], v[212:215], a[212:215], v[172:175]\n"
"ds_read_b64_tr_b16 v[212:213], v234 offset:49152\n"
"v_add_u32 v241, 1, v241\n"
"v_mfma_f32_16x16x32_bf16 v[172:175], v[208:211], a[208:211], v[172:175]\n"
"s_add_u32 s16, s90, s62\n"
"buffer_load_dwordx4 v[84:87], v243, s[32:35], s18 offen offset:3072 sc1\n"
"v_mfma_f32_16x16x32_bf16 v[172:175], v[192:195], a[236:239], v[172:175]\n"
"s_add_u32 s17, s16, 4096\n"
"ds_read_b64_tr_b16 v[192:193], v234 offset:40960\n"
"v_mfma_f32_16x16x32_bf16 v[172:175], v[188:191], a[232:235], v[172:175]\n"
"s_lshl_b32 s62, s70, 9\n"
"ds_read_b64_tr_b16 v[188:189], v230 offset:40960\n"
"v_subrev_f32 v156, s22, v156\n"
"v_mfma_f32_16x16x32_bf16 v[172:175], v[184:187], a[220:223], v[172:175]\n"
"s_add_u32 s40, s41, s53\n"
"ds_read_b64_tr_b16 v[184:185], v234 offset:32768\n"
"v_subrev_f32 v157, s22, v157\n"
"v_mfma_f32_16x16x32_bf16 v[172:175], v[180:183], a[216:219], v[172:175]\n"
"buffer_load_dwordx4 v240, s[28:31], s40 offen offset:2048 lds\n"
"v_mfma_f32_16x16x32_bf16 v[128:131], v[96:99], a[48:51], v[128:131]\n"
"ds_read_b64_tr_b16 v[180:181], v230 offset:32768\n"
"v_subrev_f32 v158, s22, v158\n"
"v_mfma_f32_16x16x32_bf16 v[128:131], v[100:103], a[52:55], v[128:131]\n"
"s_waitcnt lgkmcnt(5)\n"
"ds_read_b64_tr_b16 v[182:183], v230 offset:34816\n"
"v_subrev_f32 v159, s22, v159\n"
"v_mfma_f32_16x16x32_bf16 v[128:131], v[104:107], a[56:59], v[128:131]\n"
"ds_read_b64_tr_b16 v[186:187], v234 offset:34816\n"
"v_cvt_pk_bf16_f32 v156, v156, v157\n"
"v_mfma_f32_16x16x32_bf16 v[128:131], v[108:111], a[60:63], v[128:131]\n"
"ds_read_b64_tr_b16 v[190:191], v230 offset:43008\n"
"v_cvt_pk_bf16_f32 v157, v158, v159\n"
"v_mfma_f32_16x16x32_bf16 v[128:131], v[112:115], a[0:3], v[128:131]\n"
"ds_read_b64_tr_b16 v[194:195], v234 offset:43008\n"
"v_lshlrev_b32 v202, 16, v72\n"
"v_mfma_f32_16x16x32_bf16 v[128:131], v[116:119], a[4:7], v[128:131]\n"
"ds_read_b64_tr_b16 v[208:209], v230 offset:49152\n"
"v_and_b32 v72, s73, v72\n"
"v_mfma_f32_16x16x32_bf16 v[128:131], v[120:123], a[8:11], v[128:131]\n"
"s_add_u32 s40, s41, s54\n"
"ds_read_b64_tr_b16 v[210:211], v230 offset:51200\n"
"v_add_f32 v0, v0, v202\n"
"v_mfma_f32_16x16x32_bf16 v[128:131], v[224:227], a[12:15], v[128:131]\n"
"buffer_load_dwordx4 v240, s[28:31], s40 offen offset:3072 lds\n"
"v_mfma_f32_16x16x32_bf16 v[144:147], v[224:227], a[28:31], v[144:147]\n"
"ds_read_b64_tr_b16 v[214:215], v234 offset:51200\n"
"v_add_f32 v1, v1, v72\n"
"v_mfma_f32_16x16x32_bf16 v[144:147], v[120:123], a[24:27], v[144:147]\n"
"ds_read_b64_tr_b16 v[218:219], v230 offset:59392\n"
"v_lshlrev_b32 v203, 16, v73\n"
"v_mfma_f32_16x16x32_bf16 v[144:147], v[116:119], a[20:23], v[144:147]\n"
"ds_read_b64_tr_b16 v[222:223], v234 offset:59392\n"
"v_and_b32 v73, s73, v73\n"
"v_mfma_f32_16x16x32_bf16 v[144:147], v[112:115], a[16:19], v[144:147]\n"
"buffer_load_dwordx4 v[88:91], v243, s[32:35], s19 offen sc1\n"
"v_mfma_f32_16x16x32_bf16 v[144:147], v[108:111], a[76:79], v[144:147]\n"
"v_add_f32 v2, v2, v203\n"
"v_add_f32 v3, v3, v73\n"
"v_mfma_f32_16x16x32_bf16 v[144:147], v[104:107], a[72:75], v[144:147]\n"
"s_add_u32 s41, s70, 491520\n"
"v_subrev_f32 v128, s22, v128\n"
"v_subrev_f32 v129, s22, v129\n"
"v_mfma_f32_16x16x32_bf16 v[144:147], v[100:103], a[68:71], v[144:147]\n"
"s_add_u32 m0, s71, 49152\n"
"v_subrev_f32 v130, s22, v130\n"
"v_subrev_f32 v131, s22, v131\n"
"v_mfma_f32_16x16x32_bf16 v[144:147], v[96:99], a[64:67], v[144:147]\n"
"buffer_load_dwordx4 v240, s[28:31], s41 offen lds\n"
"v_mfma_f32_16x16x32_bf16 v[160:163], v[96:99], a[80:83], v[160:163]\n"
"s_and_b32 s70, s88, 15\n"
"v_cvt_pk_bf16_f32 v126, v128, v129\n"
"v_cvt_pk_bf16_f32 v127, v130, v131\n"
"v_mfma_f32_16x16x32_bf16 v[160:163], v[100:103], a[84:87], v[160:163]\n"
"s_lshr_b32 s88, s88, 4\n"
"v_lshlrev_b32 v204, 16, v74\n"
"v_and_b32 v74, s73, v74\n"
"v_mfma_f32_16x16x32_bf16 v[160:163], v[104:107], a[88:91], v[160:163]\n"
"s_lshl_b32 s70, s70, 7\n"
"v_add_f32 v4, v4, v204\n"
"v_add_f32 v5, v5, v74\n"
"v_mfma_f32_16x16x32_bf16 v[160:163], v[108:111], a[92:95], v[160:163]\n"
"s_xor_b32 s71, s71, s75\n"
"v_lshlrev_b32 v205, 16, v75\n"
"v_and_b32 v75, s73, v75\n"
"v_mfma_f32_16x16x32_bf16 v[160:163], v[112:115], a[32:35], v[160:163]\n"
"v_add_f32 v6, v6, v205\n"
"v_add_f32 v7, v7, v75\n"
"v_mfma_f32_16x16x32_bf16 v[160:163], v[116:119], a[36:39], v[160:163]\n"
"v_subrev_f32 v144, s22, v144\n"
"v_subrev_f32 v145, s22, v145\n"
"v_mfma_f32_16x16x32_bf16 v[160:163], v[120:123], a[40:43], v[160:163]\n"
"v_subrev_f32 v146, s22, v146\n"
"v_subrev_f32 v147, s22, v147\n"
"v_mfma_f32_16x16x32_bf16 v[160:163], v[224:227], a[44:47], v[160:163]\n"
"buffer_store_dwordx4 v[124:127], v243, s[32:35], s16 offen sc0 sc1\n"
"v_cvt_pk_bf16_f32 v142, v144, v145\n"
"v_mfma_f32_16x16x32_bf16 v[176:179], v[224:227], a[228:231], v[176:179]\n"
"ds_read_b64_tr_b16 v[224:225], v235 offset:57344\n"
"v_cvt_pk_bf16_f32 v143, v146, v147\n"
"v_mfma_f32_16x16x32_bf16 v[176:179], v[120:123], a[224:227], v[176:179]\n"
"ds_read_b64_tr_b16 v[120:121], v231 offset:57344\n"
"v_lshlrev_b32 v206, 16, v76\n"
"v_mfma_f32_16x16x32_bf16 v[176:179], v[116:119], a[212:215], v[176:179]\n"
"ds_read_b64_tr_b16 v[116:117], v235 offset:49152\n"
"v_and_b32 v76, s73, v76\n"
"v_mfma_f32_16x16x32_bf16 v[176:179], v[112:115], a[208:211], v[176:179]\n"
"buffer_load_dwordx4 v[92:95], v243, s[32:35], s19 offen offset:1024 sc1\n"
"v_mfma_f32_16x16x32_bf16 v[176:179], v[108:111], a[236:239], v[176:179]\n"
"ds_read_b64_tr_b16 v[108:109], v235 offset:40960\n"
"v_add_f32 v8, v8, v206\n"
"v_mfma_f32_16x16x32_bf16 v[176:179], v[104:107], a[232:235], v[176:179]\n"
"ds_read_b64_tr_b16 v[104:105], v231 offset:40960\n"
"v_subrev_f32 v160, s22, v160\n"
"v_mfma_f32_16x16x32_bf16 v[176:179], v[100:103], a[220:223], v[176:179]\n"
"s_add_u32 s40, s41, s52\n"
"ds_read_b64_tr_b16 v[100:101], v235 offset:32768\n"
"v_subrev_f32 v161, s22, v161\n"
"v_mfma_f32_16x16x32_bf16 v[176:179], v[96:99], a[216:219], v[176:179]\n"
"s_waitcnt vmcnt(14) lgkmcnt(6)\n"
"buffer_load_dwordx4 v240, s[28:31], s40 offen offset:1024 lds\n"
"v_mfma_f32_16x16x32_bf16 v[132:135], v[180:183], a[48:51], v[132:135]\n"
"ds_read_b64_tr_b16 v[96:97], v231 offset:32768\n"
"v_subrev_f32 v162, s22, v162\n"
"v_mfma_f32_16x16x32_bf16 v[132:135], v[184:187], a[52:55], v[132:135]\n"
"ds_read_b64_tr_b16 v[98:99], v231 offset:34816\n"
"v_subrev_f32 v163, s22, v163\n"
"v_mfma_f32_16x16x32_bf16 v[132:135], v[188:191], a[56:59], v[132:135]\n"
"ds_read_b64_tr_b16 v[102:103], v235 offset:34816\n"
"v_cvt_pk_bf16_f32 v158, v160, v161\n"
"v_mfma_f32_16x16x32_bf16 v[132:135], v[192:195], a[60:63], v[132:135]\n"
"ds_read_b64_tr_b16 v[106:107], v231 offset:43008\n"
"v_cvt_pk_bf16_f32 v159, v162, v163\n"
"v_mfma_f32_16x16x32_bf16 v[132:135], v[208:211], a[0:3], v[132:135]\n"
"ds_read_b64_tr_b16 v[110:111], v235 offset:43008\n"
"v_add_f32 v9, v9, v76\n"
"v_mfma_f32_16x16x32_bf16 v[132:135], v[212:215], a[4:7], v[132:135]\n"
"ds_read_b64_tr_b16 v[112:113], v231 offset:49152\n"
"v_lshlrev_b32 v207, 16, v77\n"
"v_mfma_f32_16x16x32_bf16 v[132:135], v[216:219], a[8:11], v[132:135]\n"
"ds_read_b64_tr_b16 v[114:115], v231 offset:51200\n"
"v_and_b32 v77, s73, v77\n"
"v_mfma_f32_16x16x32_bf16 v[132:135], v[220:223], a[12:15], v[132:135]\n"
"buffer_store_dwordx4 v[140:143], v243, s[32:35], s16 offen offset:2048 sc0 sc1\n"
"v_add_f32 v10, v10, v207\n"
"v_mfma_f32_16x16x32_bf16 v[148:151], v[220:223], a[28:31], v[148:151]\n"
"ds_read_b64_tr_b16 v[118:119], v235 offset:51200\n"
"v_add_f32 v11, v11, v77\n"
"v_mfma_f32_16x16x32_bf16 v[148:151], v[216:219], a[24:27], v[148:151]\n"
"ds_read_b64_tr_b16 v[122:123], v231 offset:59392\n"
"v_lshlrev_b32 v202, 16, v78\n"
"v_mfma_f32_16x16x32_bf16 v[148:151], v[212:215], a[20:23], v[148:151]\n"
"ds_read_b64_tr_b16 v[226:227], v235 offset:59392\n"
"v_and_b32 v78, s73, v78\n"
"v_mfma_f32_16x16x32_bf16 v[148:151], v[208:211], a[16:19], v[148:151]\n"
"v_add_f32 v12, v12, v202\n"
"v_add_f32 v13, v13, v78\n"
"v_mfma_f32_16x16x32_bf16 v[148:151], v[192:195], a[76:79], v[148:151]\n"
"v_lshlrev_b32 v203, 16, v79\n"
"v_and_b32 v79, s73, v79\n"
"v_mfma_f32_16x16x32_bf16 v[148:151], v[188:191], a[72:75], v[148:151]\n"
"v_add_f32 v14, v14, v203\n"
"v_add_f32 v15, v15, v79\n"
"v_mfma_f32_16x16x32_bf16 v[148:151], v[184:187], a[68:71], v[148:151]\n"
"s_add_u32 s40, s41, s53\n"
"v_lshlrev_b32 v204, 16, v80\n"
"v_and_b32 v80, s73, v80\n"
"v_mfma_f32_16x16x32_bf16 v[148:151], v[180:183], a[64:67], v[148:151]\n"
"buffer_load_dwordx4 v240, s[28:31], s40 offen offset:2048 lds\n"
"v_mfma_f32_16x16x32_bf16 v[164:167], v[180:183], a[80:83], v[164:167]\n"
"v_add_f32 v16, v16, v204\n"
"v_add_f32 v17, v17, v80\n"
"v_mfma_f32_16x16x32_bf16 v[164:167], v[184:187], a[84:87], v[164:167]\n"
"v_lshlrev_b32 v205, 16, v81\n"
"v_and_b32 v81, s73, v81\n"
"v_mfma_f32_16x16x32_bf16 v[164:167], v[188:191], a[88:91], v[164:167]\n"
"v_add_f32 v18, v18, v205\n"
"v_add_f32 v19, v19, v81\n"
"v_mfma_f32_16x16x32_bf16 v[164:167], v[192:195], a[92:95], v[164:167]\n"
"buffer_load_dwordx4 v[244:247], v243, s[32:35], s19 offen offset:2048 sc1\n"
"v_mfma_f32_16x16x32_bf16 v[164:167], v[208:211], a[32:35], v[164:167]\n"
"s_lshl4_add_u32 s18, s72, s61\n"
"v_lshlrev_b32 v206, 16, v82\n"
"v_and_b32 v82, s73, v82\n"
"v_mfma_f32_16x16x32_bf16 v[164:167], v[212:215], a[36:39], v[164:167]\n"
"s_lshl_b32 s19, s72, 2\n"
"v_add_f32 v20, v20, v206\n"
"v_add_f32 v21, v21, v82\n"
"v_mfma_f32_16x16x32_bf16 v[164:167], v[216:219], a[40:43], v[164:167]\n"
"s_waitcnt vmcnt(14)\n"
"s_lshr_b32 s19, s89, s19\n"
"v_lshlrev_b32 v207, 16, v83\n"
"v_mfma_f32_16x16x32_bf16 v[164:167], v[220:223], a[44:47], v[164:167]\n"
"s_barrier\n"
"s_and_b32 s19, s19, 1\n"
"s_waitcnt lgkmcnt(0)\n"
"v_mfma_f32_16x16x32_bf16 v[136:139], v[96:99], a[48:51], v[136:139]\n"
"s_lshl3_add_u32 s18, s18, s19\n"
"ds_read_b64_tr_b16 v[180:181], v228 offset:0\n"
"v_and_b32 v83, s73, v83\n"
"v_mfma_f32_16x16x32_bf16 v[136:139], v[100:103], a[52:55], v[136:139]\n"
"s_lshl_b32 s18, s18, 13\n"
"ds_read_b64_tr_b16 v[182:183], v228 offset:2048\n"
"v_add_f32 v22, v22, v207\n"
"v_mfma_f32_16x16x32_bf16 v[136:139], v[104:107], a[56:59], v[136:139]\n"
"s_add_u32 s19, s18, 4096\n"
"v_lshlrev_b32_e64 v201, 2, s72\n"
"ds_read_b64_tr_b16 v[184:185], v232 offset:0\n"
"v_mfma_f32_16x16x32_bf16 v[136:139], v[108:111], a[60:63], v[136:139]\n"
"s_mov_b32 s69, 15\n"
"global_load_dword v200, v201, s[6:7] sc1\n"
"v_mfma_f32_16x16x32_bf16 v[136:139], v[112:115], a[0:3], v[136:139]\n"
"s_add_u32 s21, s21, 1\n"
"ds_read_b64_tr_b16 v[186:187], v232 offset:2048\n"
"v_add_f32 v23, v23, v83\n"
"v_mfma_f32_16x16x32_bf16 v[136:139], v[116:119], a[4:7], v[136:139]\n"
"ds_read_b64_tr_b16 v[188:189], v228 offset:8192\n"
"v_subrev_f32 v172, s22, v172\n"
"v_mfma_f32_16x16x32_bf16 v[136:139], v[120:123], a[8:11], v[136:139]\n"
"ds_read_b64_tr_b16 v[190:191], v228 offset:10240\n"
"v_subrev_f32 v173, s22, v173\n"
"v_mfma_f32_16x16x32_bf16 v[136:139], v[224:227], a[12:15], v[136:139]\n"
"buffer_store_dwordx4 v[156:159], v243, s[32:35], s17 offen sc0 sc1\n"
"v_subrev_f32 v174, s22, v174\n"
"v_mfma_f32_16x16x32_bf16 v[152:155], v[224:227], a[28:31], v[152:155]\n"
"ds_read_b64_tr_b16 v[192:193], v232 offset:8192\n"
"v_subrev_f32 v175, s22, v175\n"
"v_mfma_f32_16x16x32_bf16 v[152:155], v[120:123], a[24:27], v[152:155]\n"
"ds_read_b64_tr_b16 v[194:195], v232 offset:10240\n"
"v_cvt_pk_bf16_f32 v172, v172, v173\n"
"v_mfma_f32_16x16x32_bf16 v[152:155], v[116:119], a[20:23], v[152:155]\n"
"ds_read_b64_tr_b16 v[208:209], v228 offset:16384\n"
"v_cvt_pk_bf16_f32 v173, v174, v175\n"
"v_mfma_f32_16x16x32_bf16 v[152:155], v[112:115], a[16:19], v[152:155]\n"
"ds_read_b64_tr_b16 v[210:211], v228 offset:18432\n"
"v_subrev_f32 v176, s22, v176\n"
"v_mfma_f32_16x16x32_bf16 v[152:155], v[108:111], a[76:79], v[152:155]\n"
"ds_read_b64_tr_b16 v[212:213], v232 offset:16384\n"
"v_subrev_f32 v177, s22, v177\n"
"v_mfma_f32_16x16x32_bf16 v[152:155], v[104:107], a[72:75], v[152:155]\n"
"ds_read_b64_tr_b16 v[214:215], v232 offset:18432\n"
"v_subrev_f32 v178, s22, v178\n"
"v_mfma_f32_16x16x32_bf16 v[152:155], v[100:103], a[68:71], v[152:155]\n"
"s_add_u32 s40, s41, s54\n"
"ds_read_b64_tr_b16 v[216:217], v228 offset:24576\n"
"v_subrev_f32 v179, s22, v179\n"
"v_mfma_f32_16x16x32_bf16 v[152:155], v[96:99], a[64:67], v[152:155]\n"
"buffer_load_dwordx4 v240, s[28:31], s40 offen offset:3072 lds\n"
"v_mfma_f32_16x16x32_bf16 v[168:171], v[96:99], a[80:83], v[168:171]\n"
"ds_read_b64_tr_b16 v[218:219], v228 offset:26624\n"
"v_cvt_pk_bf16_f32 v174, v176, v177\n"
"v_mfma_f32_16x16x32_bf16 v[168:171], v[100:103], a[84:87], v[168:171]\n"
"ds_read_b64_tr_b16 v[220:221], v232 offset:24576\n"
"v_cvt_pk_bf16_f32 v175, v178, v179\n"
"v_mfma_f32_16x16x32_bf16 v[168:171], v[104:107], a[88:91], v[168:171]\n"
"ds_read_b64_tr_b16 v[222:223], v232 offset:26624\n"
"v_subrev_f32 v132, s22, v132\n"
"v_mfma_f32_16x16x32_bf16 v[168:171], v[108:111], a[92:95], v[168:171]\n"
"ds_read_b64_tr_b16 v[96:97], v229 offset:0\n"
"v_subrev_f32 v133, s22, v133\n"
"v_mfma_f32_16x16x32_bf16 v[168:171], v[112:115], a[32:35], v[168:171]\n"
"ds_read_b64_tr_b16 v[98:99], v229 offset:2048\n"
"v_subrev_f32 v134, s22, v134\n"
"v_mfma_f32_16x16x32_bf16 v[168:171], v[116:119], a[36:39], v[168:171]\n"
"ds_read_b64_tr_b16 v[100:101], v233 offset:0\n"
"v_subrev_f32 v135, s22, v135\n"
"v_mfma_f32_16x16x32_bf16 v[168:171], v[120:123], a[40:43], v[168:171]\n"
"s_add_u32 s47, s47, 1\n"
"ds_read_b64_tr_b16 v[102:103], v233 offset:2048\n"
"v_cvt_pk_bf16_f32 v132, v132, v133\n"
"v_mfma_f32_16x16x32_bf16 v[168:171], v[224:227], a[44:47], v[168:171]\n"
"s_waitcnt lgkmcnt(7)\n"
"s_cmp_lg_u32 s47, 9\n"
"ds_read_b64_tr_b16 v[104:105], v229 offset:8192\n"
"s_cbranch_scc1 LGL_%=\n"
"v_mfma_f32_16x16x32_bf16 v[0:3], v[180:183], a[96:99], v[0:3]\n"
"v_subrev_f32 v136, s22, v136\n"
"v_subrev_f32 v138, s22, v138\n"
"v_mfma_f32_16x16x32_bf16 v[0:3], v[184:187], a[100:103], v[0:3]\n"
"v_subrev_f32 v151, s22, v151\n"
"v_subrev_f32 v139, s22, v139\n"
"v_mfma_f32_16x16x32_bf16 v[0:3], v[188:191], a[104:107], v[0:3]\n"
"v_mov_b32 v201, v237\n"
"v_cvt_pk_bf16_f32 v133, v134, v135\n"
"v_subrev_f32 v150, s22, v150\n"
"v_mfma_f32_16x16x32_bf16 v[0:3], v[192:195], a[108:111], v[0:3]\n"
"global_load_dword v200, v201, s[6:7] sc1\n"
"v_mfma_f32_16x16x32_bf16 v[0:3], v[208:211], a[144:147], v[0:3]\n"
"v_subrev_f32 v153, s22, v153\n"
"v_subrev_f32 v169, s22, v169\n"
"v_mfma_f32_16x16x32_bf16 v[0:3], v[212:215], a[148:151], v[0:3]\n"
"s_waitcnt lgkmcnt(2)\n"
"v_subrev_f32 v137, s22, v137\n"
"v_subrev_f32 v152, s22, v152\n"
"v_mfma_f32_16x16x32_bf16 v[0:3], v[216:219], a[152:155], v[0:3]\n"
"v_cvt_pk_bf16_f32 v134, v136, v137\n"
"v_subrev_f32 v168, s22, v168\n"
"v_mfma_f32_16x16x32_bf16 v[0:3], v[220:223], a[156:159], v[0:3]\n"
"buffer_store_dwordx4 v[172:175], v243, s[32:35], s17 offen offset:2048 sc0 sc1\n"
"v_cvt_pk_bf16_f32 v135, v138, v139\n"
"v_subrev_f32 v170, s22, v170\n"
"v_subrev_f32 v171, s22, v171\n"
"v_mfma_f32_16x16x32_bf16 v[16:19], v[220:223], a[172:175], v[16:19]\n"
"buffer_store_dwordx4 v[132:135], v243, s[32:35], s16 offen offset:1024 sc0 sc1\n"
"v_subrev_f32 v165, s22, v165\n"
"v_mfma_f32_16x16x32_bf16 v[16:19], v[216:219], a[168:171], v[16:19]\n"
"v_subrev_f32 v148, s22, v148\n"
"v_subrev_f32 v149, s22, v149\n"
"v_mfma_f32_16x16x32_bf16 v[16:19], v[212:215], a[164:167], v[16:19]\n"
"v_cvt_pk_bf16_f32 v148, v148, v149\n"
"v_subrev_f32 v167, s22, v167\n"
"v_mfma_f32_16x16x32_bf16 v[16:19], v[208:211], a[160:163], v[16:19]\n"
"v_cvt_pk_bf16_f32 v149, v150, v151\n"
"v_subrev_f32 v155, s22, v155\n"
"v_mfma_f32_16x16x32_bf16 v[16:19], v[192:195], a[124:127], v[16:19]\n"
"v_subrev_f32 v164, s22, v164\n"
"v_subrev_f32 v166, s22, v166\n"
"v_mfma_f32_16x16x32_bf16 v[16:19], v[188:191], a[120:123], v[16:19]\n"
"v_cvt_pk_bf16_f32 v164, v164, v165\n"
"v_subrev_f32 v154, s22, v154\n"
"v_mfma_f32_16x16x32_bf16 v[16:19], v[184:187], a[116:119], v[16:19]\n"
"v_cvt_pk_bf16_f32 v150, v152, v153\n"
"v_cvt_pk_bf16_f32 v151, v154, v155\n"
"v_mfma_f32_16x16x32_bf16 v[16:19], v[180:183], a[112:115], v[16:19]\n"
"s_waitcnt vmcnt(16)\n"
"buffer_store_dwordx4 v[148:151], v243, s[32:35], s16 offen offset:3072 sc0 sc1\n"
"v_cvt_pk_bf16_f32 v165, v166, v167\n"
"v_cvt_pk_bf16_f32 v166, v168, v169\n"
"v_cvt_pk_bf16_f32 v167, v170, v171\n"
"v_mfma_f32_16x16x32_bf16 v[32:35], v[180:183], a[128:131], v[32:35]\n"
"buffer_store_dwordx4 v[164:167], v243, s[32:35], s17 offen offset:1024 sc0 sc1\n"
"ds_read_b64_tr_b16 v[108:109], v233 offset:8192\n"
"v_mfma_f32_16x16x32_bf16 v[32:35], v[184:187], a[132:135], v[32:35]\n"
"ds_read_b64_tr_b16 v[122:123], v229 offset:26624\n"
"v_lshlrev_b32 v202, 16, v84\n"
"v_mfma_f32_16x16x32_bf16 v[32:35], v[188:191], a[136:139], v[32:35]\n"
"ds_read_b64_tr_b16 v[224:225], v233 offset:24576\n"
"v_xor_b32 v230, s75, v230\n"
"v_mfma_f32_16x16x32_bf16 v[32:35], v[192:195], a[140:143], v[32:35]\n"
"ds_read_b64_tr_b16 v[118:119], v233 offset:18432\n"
"v_bfe_u32 v161, v242, 8, 2\n"
"v_mfma_f32_16x16x32_bf16 v[32:35], v[208:211], a[176:179], v[32:35]\n"
"ds_read_b64_tr_b16 v[226:227], v233 offset:26624\n"
"v_bfe_u32 v160, v242, 6, 2\n"
"v_mfma_f32_16x16x32_bf16 v[32:35], v[212:215], a[180:183], v[32:35]\n"
"ds_read_b64_tr_b16 v[114:115], v229 offset:18432\n"
"v_mov_b32 v165, 0\n"
"v_mfma_f32_16x16x32_bf16 v[32:35], v[216:219], a[184:187], v[32:35]\n"
"ds_read_b64_tr_b16 v[120:121], v229 offset:24576\n"
"v_xor_b32 v160, v160, v161\n"
"v_mfma_f32_16x16x32_bf16 v[32:35], v[220:223], a[188:191], v[32:35]\n"
"ds_read_b64_tr_b16 v[116:117], v233 offset:16384\n"
"v_xor_b32 v234, s75, v234\n"
"v_mfma_f32_16x16x32_bf16 v[48:51], v[220:223], a[252:255], v[48:51]\n"
"ds_read_b64_tr_b16 v[110:111], v233 offset:10240\n"
"v_lshlrev_b32 v204, 16, v86\n"
"v_mfma_f32_16x16x32_bf16 v[48:51], v[216:219], a[248:251], v[48:51]\n"
"ds_read_b64_tr_b16 v[106:107], v229 offset:10240\n"
"v_lshlrev_b32 v205, 16, v87\n"
"v_mfma_f32_16x16x32_bf16 v[48:51], v[212:215], a[244:247], v[48:51]\n"
"ds_read_b64_tr_b16 v[112:113], v229 offset:16384\n"
"v_lshlrev_b32 v203, 16, v85\n"
"v_mfma_f32_16x16x32_bf16 v[48:51], v[208:211], a[240:243], v[48:51]\n"
"ds_read_b64_tr_b16 v[210:211], v230 offset:18432\n"
"v_add_f32 v24, v24, v202\n"
"v_mfma_f32_16x16x32_bf16 v[48:51], v[192:195], a[204:207], v[48:51]\n"
"ds_read_b64_tr_b16 v[220:221], v234 offset:24576\n"
"v_xor_b32 v231, s75, v231\n"
"v_mfma_f32_16x16x32_bf16 v[48:51], v[188:191], a[200:203], v[48:51]\n"
"ds_read_b64_tr_b16 v[194:195], v234 offset:10240\n"
"v_add_f32 v26, v26, v203\n"
"v_mfma_f32_16x16x32_bf16 v[48:51], v[184:187], a[196:199], v[48:51]\n"
"ds_read_b64_tr_b16 v[222:223], v234 offset:26624\n"
"v_add_f32 v28, v28, v204\n"
"v_mfma_f32_16x16x32_bf16 v[48:51], v[180:183], a[192:195], v[48:51]\n"
"s_waitcnt vmcnt(15)\n"
"ds_read_b64_tr_b16 v[180:181], v230 offset:0\n"
"v_lshlrev_b32 v202, 16, v90\n"
"v_mfma_f32_16x16x32_bf16 v[4:7], v[96:99], a[96:99], v[4:7]\n"
"s_waitcnt lgkmcnt(7)\n"
"ds_read_b64_tr_b16 v[218:219], v230 offset:26624\n"
"v_lshlrev_b32 v203, 16, v91\n"
"v_mfma_f32_16x16x32_bf16 v[4:7], v[100:103], a[100:103], v[4:7]\n"
"s_waitcnt lgkmcnt(7)\n"
"ds_read_b64_tr_b16 v[216:217], v230 offset:24576\n"
"v_and_b32 v91, s73, v91\n"
"v_mfma_f32_16x16x32_bf16 v[4:7], v[104:107], a[104:107], v[4:7]\n"
"ds_read_b64_tr_b16 v[190:191], v230 offset:10240\n"
"v_and_b32 v90, s73, v90\n"
"v_mfma_f32_16x16x32_bf16 v[4:7], v[108:111], a[108:111], v[4:7]\n"
"s_waitcnt lgkmcnt(7)\n"
"ds_read_b64_tr_b16 v[214:215], v234 offset:18432\n"
"v_add_u32 v160, -1, v160\n"
"v_mfma_f32_16x16x32_bf16 v[4:7], v[112:115], a[144:147], v[4:7]\n"
"s_waitcnt vmcnt(4)\n"
"ds_read_b64_tr_b16 v[208:209], v230 offset:16384\n"
"v_add_f32 v37, v37, v90\n"
"v_mfma_f32_16x16x32_bf16 v[4:7], v[116:119], a[148:151], v[4:7]\n"
"v_subrev_u32 v200, s21, v200\n"
"ds_read_b64_tr_b16 v[186:187], v234 offset:2048\n"
"v_mfma_f32_16x16x32_bf16 v[4:7], v[120:123], a[152:155], v[4:7]\n"
"v_cmp_lt_u32 vcc, s69, v200\n"
"ds_read_b64_tr_b16 v[192:193], v234 offset:8192\n"
"v_mfma_f32_16x16x32_bf16 v[4:7], v[224:227], a[156:159], v[4:7]\n"
"ds_read_b64_tr_b16 v[188:189], v230 offset:8192\n"
"s_cbranch_vccnz LGQ_%=\nLGQK_%=:\n"
"v_mfma_f32_16x16x32_bf16 v[20:23], v[224:227], a[172:175], v[20:23]\n"
"s_bfe_u32 s20, s89, 65536\ns_lshl_b32 s20, s20, 13\ns_add_u32 s20, s20, 0\n"
"ds_read_b64_tr_b16 v[184:185], v234 offset:0\n"
"v_add_f32 v39, v39, v91\n"
"v_mfma_f32_16x16x32_bf16 v[20:23], v[120:123], a[168:171], v[20:23]\n"
"buffer_load_dwordx4 v[124:127], v236, s[32:35], s20 offen sc1\n"
"v_mfma_f32_16x16x32_bf16 v[20:23], v[116:119], a[164:167], v[20:23]\n"
"v_add_f32 v30, v30, v205\n"
"v_ashrrev_i32 v160, 31, v160\n"
"v_mfma_f32_16x16x32_bf16 v[20:23], v[112:115], a[160:163], v[20:23]\n"
"s_bfe_u32 s20, s89, 65540\ns_lshl_b32 s20, s20, 13\ns_add_u32 s20, s20, 1048576\n"
"v_add_f32 v36, v36, v202\n"
"v_mfma_f32_16x16x32_bf16 v[20:23], v[108:111], a[124:127], v[20:23]\n"
"buffer_load_dwordx4 v[128:131], v236, s[32:35], s20 offen sc1\n"
"v_mfma_f32_16x16x32_bf16 v[20:23], v[104:107], a[120:123], v[20:23]\n"
"v_add_f32 v38, v38, v203\n"
"v_mfma_f32_16x16x32_bf16 v[20:23], v[100:103], a[116:119], v[20:23]\n"
"s_bfe_u32 s20, s89, 65544\ns_lshl_b32 s20, s20, 13\ns_add_u32 s20, s20, 2097152\n"
"v_and_b32 v84, s73, v84\n"
"v_lshlrev_b32 v207, 16, v89\n"
"v_mfma_f32_16x16x32_bf16 v[20:23], v[96:99], a[112:115], v[20:23]\n"
"buffer_load_dwordx4 v[132:135], v236, s[32:35], s20 offen sc1\n"
"v_mfma_f32_16x16x32_bf16 v[36:39], v[96:99], a[128:131], v[36:39]\n"
"v_and_b32 v85, s73, v85\n"
"v_lshlrev_b32 v204, 16, v92\n"
"v_mfma_f32_16x16x32_bf16 v[36:39], v[100:103], a[132:135], v[36:39]\n"
"s_bfe_u32 s20, s89, 65548\ns_lshl_b32 s20, s20, 13\ns_add_u32 s20, s20, 3145728\n"
"ds_read_b64_tr_b16 v[182:183], v230 offset:2048\n"
"ds_read_b64_tr_b16 v[212:213], v234 offset:16384\n"
"v_mfma_f32_16x16x32_bf16 v[36:39], v[104:107], a[136:139], v[36:39]\n"
"buffer_load_dwordx4 v[136:139], v236, s[32:35], s20 offen sc1\n"
"v_mfma_f32_16x16x32_bf16 v[36:39], v[108:111], a[140:143], v[36:39]\n"
"v_lshlrev_b32 v205, 16, v93\n"
"v_mfma_f32_16x16x32_bf16 v[36:39], v[112:115], a[176:179], v[36:39]\n"
"s_bfe_u32 s20, s89, 65552\ns_lshl_b32 s20, s20, 13\ns_add_u32 s20, s20, 4194304\n"
"v_and_b32 v160, 0x3f80, v160\n"
"v_add_f32 v40, v40, v204\n"
"v_mfma_f32_16x16x32_bf16 v[36:39], v[116:119], a[180:183], v[36:39]\n"
"buffer_load_dwordx4 v[140:143], v236, s[32:35], s20 offen sc1\n"
"v_mfma_f32_16x16x32_bf16 v[36:39], v[120:123], a[184:187], v[36:39]\n"
"v_and_b32 v161, 16, v242\n"
"v_lshlrev_b32 v206, 16, v88\n"
"v_mfma_f32_16x16x32_bf16 v[36:39], v[224:227], a[188:191], v[36:39]\n"
"s_bfe_u32 s20, s89, 65556\ns_lshl_b32 s20, s20, 13\ns_add_u32 s20, s20, 5242880\n"
"v_add_f32 v27, v27, v85\n"
"v_add_f32 v34, v34, v207\n"
"v_mfma_f32_16x16x32_bf16 v[52:55], v[224:227], a[252:255], v[52:55]\n"
"buffer_load_dwordx4 v[144:147], v236, s[32:35], s20 offen sc1\n"
"v_mfma_f32_16x16x32_bf16 v[52:55], v[120:123], a[248:251], v[52:55]\n"
"v_and_b32 v86, s73, v86\n"
"v_and_b32 v92, s73, v92\n"
"v_mfma_f32_16x16x32_bf16 v[52:55], v[116:119], a[244:247], v[52:55]\n"
"s_bfe_u32 s20, s89, 65560\ns_lshl_b32 s20, s20, 13\ns_add_u32 s20, s20, 6291456\n"
"v_xor_b32 v235, s75, v235\n"
"v_mov_b32 v164, 0\n"
"v_mfma_f32_16x16x32_bf16 v[52:55], v[112:115], a[240:243], v[52:55]\n"
"buffer_load_dwordx4 v[148:151], v236, s[32:35], s20 offen sc1\n"
"v_mfma_f32_16x16x32_bf16 v[52:55], v[108:111], a[204:207], v[52:55]\n"
"s_bfe_u32 s20, s89, 65564\ns_lshl_b32 s20, s20, 13\ns_add_u32 s20, s20, 7340032\n"
"v_and_b32 v88, s73, v88\n"
"v_lshlrev_b32 v207, 16, v95\n"
"v_mfma_f32_16x16x32_bf16 v[52:55], v[104:107], a[200:203], v[52:55]\n"
"buffer_load_dwordx4 v[152:155], v236, s[32:35], s20 offen sc1\n"
"v_mfma_f32_16x16x32_bf16 v[52:55], v[100:103], a[196:199], v[52:55]\n"
"s_waitcnt vmcnt(8)\n"
"s_add_u32 s21, s21, 1\n"
"v_add_f32 v32, v32, v206\n"
"v_mfma_f32_16x16x32_bf16 v[52:55], v[96:99], a[192:195], v[52:55]\n"
"s_barrier\n"
"s_mov_b32 s52, 40960\n"
"buffer_store_dword v241, v248, s[84:87], 0 offen sc1\n"
"s_waitcnt lgkmcnt(0)\n"
"v_mfma_f32_16x16x32_bf16 v[8:11], v[180:183], a[96:99], v[8:11]\n"
"s_mov_b32 s53, 81920\n"
"ds_read_b64_tr_b16 v[120:121], v231 offset:24576\n"
"v_lshlrev_b32 v206, 16, v94\n"
"v_mfma_f32_16x16x32_bf16 v[8:11], v[184:187], a[100:103], v[8:11]\n"
"ds_read_b64_tr_b16 v[96:97], v231 offset:0\n"
"v_and_b32 v89, s73, v89\n"
"v_mfma_f32_16x16x32_bf16 v[8:11], v[188:191], a[104:107], v[8:11]\n"
"ds_read_b64_tr_b16 v[102:103], v235 offset:2048\n"
"v_add_f32 v46, v46, v207\n"
"v_mfma_f32_16x16x32_bf16 v[8:11], v[192:195], a[108:111], v[8:11]\n"
"v_and_b32 v93, s73, v93\n"
"v_add_f32 v44, v44, v206\n"
"v_mfma_f32_16x16x32_bf16 v[8:11], v[208:211], a[144:147], v[8:11]\n"
"ds_read_b64_tr_b16 v[122:123], v231 offset:26624\n"
"v_add_f32 v43, v43, v93\n"
"v_mfma_f32_16x16x32_bf16 v[8:11], v[212:215], a[148:151], v[8:11]\n"
"ds_read_b64_tr_b16 v[116:117], v235 offset:16384\n"
"v_add_f32 v25, v25, v84\n"
"v_mfma_f32_16x16x32_bf16 v[8:11], v[216:219], a[152:155], v[8:11]\n"
"v_and_b32 v87, s73, v87\n"
"v_and_b32 v94, s73, v94\n"
"v_mfma_f32_16x16x32_bf16 v[8:11], v[220:223], a[156:159], v[8:11]\n"
"ds_read_b64_tr_b16 v[224:225], v235 offset:24576\n"
"v_lshlrev_b32 v202, 16, v246\n"
"v_mfma_f32_16x16x32_bf16 v[24:27], v[220:223], a[172:175], v[24:27]\n"
"v_add_f32 v31, v31, v87\n"
"v_add_f32 v42, v42, v205\n"
"v_mfma_f32_16x16x32_bf16 v[24:27], v[216:219], a[168:171], v[24:27]\n"
"ds_read_b64_tr_b16 v[100:101], v235 offset:0\n"
"v_lshlrev_b32 v207, 16, v245\n"
"v_mfma_f32_16x16x32_bf16 v[24:27], v[212:215], a[164:167], v[24:27]\n"
"v_add_f32 v29, v29, v86\n"
"v_add_f32 v41, v41, v92\n"
"v_mfma_f32_16x16x32_bf16 v[24:27], v[208:211], a[160:163], v[24:27]\n"
"ds_read_b64_tr_b16 v[98:99], v231 offset:2048\n"
"v_and_b32 v95, s73, v95\n"
"v_mfma_f32_16x16x32_bf16 v[24:27], v[192:195], a[124:127], v[24:27]\n"
"ds_read_b64_tr_b16 v[114:115], v231 offset:18432\n"
"v_add_f32 v33, v33, v88\n"
"v_mfma_f32_16x16x32_bf16 v[24:27], v[188:191], a[120:123], v[24:27]\n"
"ds_read_b64_tr_b16 v[110:111], v235 offset:10240\n"
"v_and_b32 v245, s73, v245\n"
"v_mfma_f32_16x16x32_bf16 v[24:27], v[184:187], a[116:119], v[24:27]\n"
"v_add_f32 v45, v45, v94\n"
"v_and_b32 v246, s73, v246\n"
"v_mfma_f32_16x16x32_bf16 v[24:27], v[180:183], a[112:115], v[24:27]\n"
"ds_read_b64_tr_b16 v[108:109], v235 offset:8192\n"
"v_lshlrev_b32 v206, 16, v244\n"
"v_mfma_f32_16x16x32_bf16 v[40:43], v[180:183], a[128:131], v[40:43]\n"
"ds_read_b64_tr_b16 v[106:107], v231 offset:10240\n"
"v_lshlrev_b32 v160, v161, v160\n"
"v_mfma_f32_16x16x32_bf16 v[40:43], v[184:187], a[132:135], v[40:43]\n"
"ds_read_b64_tr_b16 v[104:105], v231 offset:8192\n"
"v_mfma_f32_16x16x32_bf16 v[40:43], v[188:191], a[136:139], v[40:43]\n"
"v_bfe_i32 v161, v242, 5, 1\n"
"v_mfma_f32_16x16x32_bf16 v[40:43], v[192:195], a[140:143], v[40:43]\n"
"ds_read_b64_tr_b16 v[112:113], v231 offset:16384\n"
"v_and_b32 v244, s73, v244\n"
"v_mfma_f32_16x16x32_bf16 v[40:43], v[208:211], a[176:179], v[40:43]\n"
"ds_read_b64_tr_b16 v[118:119], v235 offset:18432\n"
"v_add_f32 v49, v49, v244\n"
"v_mfma_f32_16x16x32_bf16 v[40:43], v[212:215], a[180:183], v[40:43]\n"
"v_add_f32 v52, v52, v202\n"
"v_lshlrev_b32 v203, 16, v247\n"
"v_mfma_f32_16x16x32_bf16 v[40:43], v[216:219], a[184:187], v[40:43]\n"
"ds_read_b64_tr_b16 v[226:227], v235 offset:26624\n"
"v_add_f32 v48, v48, v206\n"
"v_mfma_f32_16x16x32_bf16 v[40:43], v[220:223], a[188:191], v[40:43]\n"
"s_barrier\n"
"s_waitcnt lgkmcnt(5)\n"
"v_mfma_f32_16x16x32_bf16 v[12:15], v[96:99], a[96:99], v[12:15]\n"
"ds_read_b64_tr_b16 v[194:195], v232 offset:43008\n"
"ds_read_b64_tr_b16 v[214:215], v232 offset:51200\n"
"v_mfma_f32_16x16x32_bf16 v[12:15], v[100:103], a[100:103], v[12:15]\n"
"s_waitcnt lgkmcnt(5)\n"
"ds_read_b64_tr_b16 v[186:187], v232 offset:34816\n"
"v_add_f32 v53, v53, v246\n"
"v_mfma_f32_16x16x32_bf16 v[12:15], v[104:107], a[104:107], v[12:15]\n"
"ds_read_b64_tr_b16 v[184:185], v232 offset:32768\n"
"ds_read_b64_tr_b16 v[208:209], v228 offset:49152\n"
"v_mfma_f32_16x16x32_bf16 v[12:15], v[108:111], a[108:111], v[12:15]\n"
"s_waitcnt lgkmcnt(7)\n"
"ds_read_b64_tr_b16 v[188:189], v228 offset:40960\n"
"v_add_f32 v47, v47, v95\n"
"v_mfma_f32_16x16x32_bf16 v[12:15], v[112:115], a[144:147], v[12:15]\n"
"s_waitcnt lgkmcnt(7)\n"
"ds_read_b64_tr_b16 v[180:181], v228 offset:32768\n"
"v_add_f32 v35, v35, v89\n"
"v_mfma_f32_16x16x32_bf16 v[12:15], v[116:119], a[148:151], v[12:15]\n"
"ds_read_b64_tr_b16 v[216:217], v228 offset:57344\n"
"v_add_f32 v51, v51, v245\n"
"v_mfma_f32_16x16x32_bf16 v[12:15], v[120:123], a[152:155], v[12:15]\n"
"s_waitcnt lgkmcnt(8)\n"
"ds_read_b64_tr_b16 v[182:183], v228 offset:34816\n"
"ds_read_b64_tr_b16 v[212:213], v232 offset:49152\n"
"v_mfma_f32_16x16x32_bf16 v[12:15], v[224:227], a[156:159], v[12:15]\n"
"global_load_dword v200, v201, s[6:7] sc1\n"
"v_mfma_f32_16x16x32_bf16 v[28:31], v[224:227], a[172:175], v[28:31]\n"
"v_add_f32 v50, v50, v207\n"
"v_add_f32 v54, v54, v203\n"
"v_mfma_f32_16x16x32_bf16 v[28:31], v[120:123], a[168:171], v[28:31]\n"
"s_waitcnt vmcnt(9)\n"
"v_lshlrev_b32 v58, 16, v125\n"
"v_lshlrev_b32 v62, 16, v127\n"
"v_mfma_f32_16x16x32_bf16 v[28:31], v[116:119], a[164:167], v[28:31]\n"
"ds_read_b64_tr_b16 v[210:211], v228 offset:51200\n"
"ds_read_b64_tr_b16 v[218:219], v228 offset:59392\n"
"v_mfma_f32_16x16x32_bf16 v[28:31], v[112:115], a[160:163], v[28:31]\n"
"ds_read_b64_tr_b16 v[190:191], v228 offset:43008\n"
"v_lshlrev_b32 v60, 16, v126\n"
"v_mfma_f32_16x16x32_bf16 v[28:31], v[108:111], a[124:127], v[28:31]\n"
"s_waitcnt vmcnt(8)\n"
"ds_read_b64_tr_b16 v[192:193], v232 offset:40960\n"
"v_lshlrev_b32 v205, 16, v129\n"
"v_mfma_f32_16x16x32_bf16 v[28:31], v[104:107], a[120:123], v[28:31]\n"
"v_lshlrev_b32 v56, 16, v124\n"
"v_lshlrev_b32 v206, 16, v130\n"
"v_mfma_f32_16x16x32_bf16 v[28:31], v[100:103], a[116:119], v[28:31]\n"
"ds_read_b64_tr_b16 v[222:223], v232 offset:59392\n"
"v_add_f32 v58, v58, v205\n"
"v_mfma_f32_16x16x32_bf16 v[28:31], v[96:99], a[112:115], v[28:31]\n"
"s_waitcnt vmcnt(7)\n"
"v_lshlrev_b32 v204, 16, v128\n"
"v_add_f32 v56, v56, v204\n"
"v_mfma_f32_16x16x32_bf16 v[44:47], v[96:99], a[128:131], v[44:47]\n"
"v_lshlrev_b32 v207, 16, v131\n"
"v_lshlrev_b32 v203, 16, v133\n"
"v_mfma_f32_16x16x32_bf16 v[44:47], v[100:103], a[132:135], v[44:47]\n"
"v_add_f32 v58, v58, v203\n"
"v_lshlrev_b32 v205, 16, v135\n"
"v_mfma_f32_16x16x32_bf16 v[44:47], v[104:107], a[136:139], v[44:47]\n"
"s_waitcnt vmcnt(6)\n"
"ds_read_b64_tr_b16 v[106:107], v229 offset:43008\n"
"v_and_b32 v59, s73, v125\n"
"v_mfma_f32_16x16x32_bf16 v[44:47], v[108:111], a[140:143], v[44:47]\n"
"v_add_f32 v60, v60, v206\n"
"v_lshlrev_b32 v206, 16, v136\n"
"v_mfma_f32_16x16x32_bf16 v[44:47], v[112:115], a[176:179], v[44:47]\n"
"v_lshlrev_b32 v202, 16, v132\n"
"v_and_b32 v132, s73, v132\n"
"v_mfma_f32_16x16x32_bf16 v[44:47], v[116:119], a[180:183], v[44:47]\n"
"s_waitcnt vmcnt(5)\n"
"ds_read_b64_tr_b16 v[100:101], v233 offset:32768\n"
"v_lshlrev_b32 v204, 16, v134\n"
"v_mfma_f32_16x16x32_bf16 v[44:47], v[120:123], a[184:187], v[44:47]\n"
"ds_read_b64_tr_b16 v[220:221], v232 offset:57344\n"
"v_add_f32 v62, v62, v207\n"
"v_mfma_f32_16x16x32_bf16 v[44:47], v[224:227], a[188:191], v[44:47]\n"
"s_waitcnt lgkmcnt(3)\n"
"ds_read_b64_tr_b16 v[224:225], v233 offset:57344\n"
"v_add_f32 v62, v62, v205\n"
"v_mfma_f32_16x16x32_bf16 v[0:3], v[180:183], a[48:51], v[0:3]\n"
"s_waitcnt vmcnt(4)\n"
"ds_read_b64_tr_b16 v[114:115], v229 offset:51200\n"
"v_lshlrev_b32 v207, 16, v137\n"
"v_mfma_f32_16x16x32_bf16 v[0:3], v[184:187], a[52:55], v[0:3]\n"
"v_and_b32 v128, s73, v128\n"
"v_lshlrev_b32 v203, 16, v139\n"
"v_mfma_f32_16x16x32_bf16 v[0:3], v[188:191], a[56:59], v[0:3]\n"
"v_and_b32 v57, s73, v124\n"
"v_add_f32 v60, v60, v204\n"
"v_mfma_f32_16x16x32_bf16 v[0:3], v[192:195], a[60:63], v[0:3]\n"
"s_waitcnt vmcnt(3)\n"
"ds_read_b64_tr_b16 v[96:97], v229 offset:32768\n"
"ds_read_b64_tr_b16 v[112:113], v229 offset:49152\n"
"v_mfma_f32_16x16x32_bf16 v[0:3], v[208:211], a[0:3], v[0:3]\n"
"v_and_b32 v247, s73, v247\n"
"v_add_f32 v58, v58, v207\n"
"v_mfma_f32_16x16x32_bf16 v[0:3], v[212:215], a[4:7], v[0:3]\n"
"s_waitcnt vmcnt(2)\n"
"ds_read_b64_tr_b16 v[102:103], v233 offset:34816\n"
"v_and_b32 v134, s73, v134\n"
"v_mfma_f32_16x16x32_bf16 v[0:3], v[216:219], a[8:11], v[0:3]\n"
"s_waitcnt lgkmcnt(5)\n"
"ds_read_b64_tr_b16 v[120:121], v229 offset:57344\n"
"v_lshlrev_b32 v204, 16, v140\n"
"v_mfma_f32_16x16x32_bf16 v[0:3], v[220:223], a[12:15], v[0:3]\n"
"ds_read_b64_tr_b16 v[98:99], v229 offset:34816\n"
"v_and_b32 v61, s73, v126\n"
"v_add_f32 v57, v57, v128\n"
"v_add_f32 v56, v56, v202\n"
"v_and_b32 v135, s73, v135\n"
"v_and_b32 v136, s73, v136\n"
"v_and_b32 v137, s73, v137\n"
"v_lshlrev_b32 v202, 16, v138\n"
"v_mfma_f32_16x16x32_bf16 v[16:19], v[220:223], a[28:31], v[16:19]\n"
"ds_read_b64_tr_b16 v[122:123], v229 offset:59392\n"
"v_and_b32 v129, s73, v129\n"
"v_mfma_f32_16x16x32_bf16 v[16:19], v[216:219], a[24:27], v[16:19]\n"
"ds_read_b64_tr_b16 v[108:109], v233 offset:40960\n"
"v_add_f32 v56, v56, v206\n"
"v_mfma_f32_16x16x32_bf16 v[16:19], v[212:215], a[20:23], v[16:19]\n"
"v_add_f32 v60, v60, v202\n"
"v_lshlrev_b32 v207, 16, v143\n"
"v_mfma_f32_16x16x32_bf16 v[16:19], v[208:211], a[16:19], v[16:19]\n"
"ds_read_b64_tr_b16 v[116:117], v233 offset:49152\n"
"v_lshlrev_b32 v205, 16, v141\n"
"v_mfma_f32_16x16x32_bf16 v[16:19], v[192:195], a[76:79], v[16:19]\n"
"s_waitcnt vmcnt(0)\n"
"v_add_f32 v55, v55, v247\n"
"v_lshlrev_b32 v202, 16, v144\n"
"v_mfma_f32_16x16x32_bf16 v[16:19], v[188:191], a[72:75], v[16:19]\n"
"v_subrev_u32 v200, s21, v200\n"
"ds_read_b64_tr_b16 v[104:105], v229 offset:40960\n"
"v_mfma_f32_16x16x32_bf16 v[16:19], v[184:187], a[68:71], v[16:19]\n"
"v_cmp_lt_u32 vcc, s69, v200\n"
"v_add_f32 v62, v62, v203\n"
"v_mfma_f32_16x16x32_bf16 v[16:19], v[180:183], a[64:67], v[16:19]\n"
"ds_read_b64_tr_b16 v[118:119], v233 offset:51200\n"
"v_and_b32 v63, s73, v127\n"
"v_add_f32 v59, v59, v129\n"
"v_add_f32 v56, v56, v204\n"
"v_add_f32 v58, v58, v205\n"
"v_lshlrev_b32 v206, 16, v142\n"
"v_lshlrev_b32 v203, 16, v145\n"
"v_lshlrev_b32 v205, 16, v147\n"
"v_subrev_f32 v1, s22, v1\n"
"s_cbranch_vccnz LGR_%=\nLGRK_%=:\n"
"v_mfma_f32_16x16x32_bf16 v[32:35], v[180:183], a[80:83], v[32:35]\n"
"ds_read_b64_tr_b16 v[110:111], v233 offset:43008\n"
"s_cmp_eq_u32 s93, 0\ns_cbranch_scc1 LEP_%=\nLEPK_%=:\n"
"v_mfma_f32_16x16x32_bf16 v[32:35], v[184:187], a[84:87], v[32:35]\n"
"s_bfe_u32 s20, s89, 65536\ns_lshl_b32 s20, s20, 13\ns_add_u32 s20, s20, 65536\n"
"ds_read_b64_tr_b16 v[226:227], v233 offset:59392\n"
"v_add_f32 v57, v57, v132\n"
"v_mfma_f32_16x16x32_bf16 v[32:35], v[188:191], a[88:91], v[32:35]\n"
"buffer_load_dwordx4 v[72:75], v236, s[32:35], s20 offen sc1\n"
"v_mfma_f32_16x16x32_bf16 v[32:35], v[192:195], a[92:95], v[32:35]\n"
"v_and_b32 v131, s73, v131\n"
"v_add_f32 v58, v58, v203\n"
"v_mfma_f32_16x16x32_bf16 v[32:35], v[208:211], a[32:35], v[32:35]\n"
"s_bfe_u32 s20, s89, 65540\ns_lshl_b32 s20, s20, 13\ns_add_u32 s20, s20, 1114112\n"
"v_subrev_f32 v2, s22, v2\n"
"v_subrev_f32 v16, s22, v16\n"
"v_mfma_f32_16x16x32_bf16 v[32:35], v[212:215], a[36:39], v[32:35]\n"
"buffer_load_dwordx4 v[76:79], v236, s[32:35], s20 offen sc1\n"
"v_mfma_f32_16x16x32_bf16 v[32:35], v[216:219], a[40:43], v[32:35]\n"
"v_and_b32 v139, s73, v139\n"
"v_add_f32 v62, v62, v207\n"
"v_mfma_f32_16x16x32_bf16 v[32:35], v[220:223], a[44:47], v[32:35]\n"
"s_bfe_u32 s20, s89, 65544\ns_lshl_b32 s20, s20, 13\ns_add_u32 s20, s20, 2162688\n"
"v_add_f32 v62, v62, v205\n"
"v_lshlrev_b32 v207, 16, v149\n"
"v_mfma_f32_16x16x32_bf16 v[48:51], v[220:223], a[228:231], v[48:51]\n"
"buffer_load_dwordx4 v[80:83], v236, s[32:35], s20 offen sc1\n"
"v_mfma_f32_16x16x32_bf16 v[48:51], v[216:219], a[224:227], v[48:51]\n"
"v_and_b32 v140, s73, v140\n"
"v_and_b32 v142, s73, v142\n"
"v_mfma_f32_16x16x32_bf16 v[48:51], v[212:215], a[212:215], v[48:51]\n"
"s_bfe_u32 s20, s89, 65548\ns_lshl_b32 s20, s20, 13\ns_add_u32 s20, s20, 3211264\n"
"v_add_f32 v57, v57, v136\n"
"v_subrev_f32 v3, s22, v3\n"
"v_mfma_f32_16x16x32_bf16 v[48:51], v[208:211], a[208:211], v[48:51]\n"
"buffer_load_dwordx4 v[84:87], v236, s[32:35], s20 offen sc1\n"
"v_mfma_f32_16x16x32_bf16 v[48:51], v[192:195], a[236:239], v[48:51]\n"
"v_lshlrev_b32 v204, 16, v146\n"
"v_and_b32 v147, s73, v147\n"
"v_mfma_f32_16x16x32_bf16 v[48:51], v[188:191], a[232:235], v[48:51]\n"
"s_bfe_u32 s20, s89, 65552\ns_lshl_b32 s20, s20, 13\ns_add_u32 s20, s20, 4259840\n"
"v_and_b32 v138, s73, v138\n"
"v_and_b32 v144, s73, v144\n"
"v_mfma_f32_16x16x32_bf16 v[48:51], v[184:187], a[220:223], v[48:51]\n"
"buffer_load_dwordx4 v[88:91], v236, s[32:35], s20 offen sc1\n"
"v_mfma_f32_16x16x32_bf16 v[48:51], v[180:183], a[216:219], v[48:51]\n"
"s_waitcnt lgkmcnt(0)\n"
"v_and_b32 v133, s73, v133\n"
"v_add_f32 v58, v58, v207\n"
"v_mfma_f32_16x16x32_bf16 v[4:7], v[96:99], a[48:51], v[4:7]\n"
"s_bfe_u32 s20, s89, 65556\ns_lshl_b32 s20, s20, 13\ns_add_u32 s20, s20, 5308416\n"
"v_add_f32 v59, v59, v133\n"
"v_add_f32 v60, v60, v206\n"
"v_mfma_f32_16x16x32_bf16 v[4:7], v[100:103], a[52:55], v[4:7]\n"
"buffer_load_dwordx4 v[92:95], v236, s[32:35], s20 offen sc1\n"
"v_mfma_f32_16x16x32_bf16 v[4:7], v[104:107], a[56:59], v[4:7]\n"
"v_subrev_f32 v0, s22, v0\n"
"v_subrev_f32 v17, s22, v17\n"
"v_mfma_f32_16x16x32_bf16 v[4:7], v[108:111], a[60:63], v[4:7]\n"
"s_bfe_u32 s20, s89, 65560\ns_lshl_b32 s20, s20, 13\ns_add_u32 s20, s20, 6356992\n"
"v_add_f32 v56, v56, v202\n"
"v_cvt_pk_bf16_f32 v16, v16, v17\n"
"v_mfma_f32_16x16x32_bf16 v[4:7], v[112:115], a[0:3], v[4:7]\n"
"buffer_load_dwordx4 v[244:247], v236, s[32:35], s20 offen sc1\n"
"v_and_b32 v130, s73, v130\n"
"v_add_f32 v59, v59, v137\n"
"v_add_f32 v57, v57, v140\n"
"v_add_f32 v60, v60, v204\n"
"v_lshlrev_b32 v202, 16, v150\n"
"v_lshlrev_b32 v203, 16, v151\n"
"v_cvt_pk_bf16_f32 v0, v0, v1\n"
"v_subrev_f32 v32, s22, v32\n"
"v_mfma_f32_16x16x32_bf16 v[4:7], v[116:119], a[4:7], v[4:7]\n"
"v_add_f32 v63, v63, v131\n"
"v_lshlrev_b32 v206, 16, v148\n"
"v_mfma_f32_16x16x32_bf16 v[4:7], v[120:123], a[8:11], v[4:7]\n"
"s_bfe_u32 s20, s89, 65564\ns_lshl_b32 s20, s20, 13\ns_add_u32 s20, s20, 7405568\n"
"v_and_b32 v151, s73, v151\n"
"v_lshlrev_b32 v204, 16, v152\n"
"v_mfma_f32_16x16x32_bf16 v[4:7], v[224:227], a[12:15], v[4:7]\n"
"buffer_load_dwordx4 v[156:159], v236, s[32:35], s20 offen sc1\n"
"v_mfma_f32_16x16x32_bf16 v[20:23], v[224:227], a[28:31], v[20:23]\n"
"v_add_f32 v57, v57, v144\n"
"v_and_b32 v146, s73, v146\n"
"v_mfma_f32_16x16x32_bf16 v[20:23], v[120:123], a[24:27], v[20:23]\n"
"v_and_b32 v141, s73, v141\n"
"v_cvt_pk_bf16_f32 v1, v2, v3\n"
"v_mfma_f32_16x16x32_bf16 v[20:23], v[116:119], a[20:23], v[20:23]\n"
"v_lshlrev_b32 v207, 16, v155\n"
"v_subrev_f32 v19, s22, v19\n"
"v_mfma_f32_16x16x32_bf16 v[20:23], v[112:115], a[16:19], v[20:23]\n"
"v_add_f32 v62, v62, v203\n"
"v_subrev_f32 v18, s22, v18\n"
"v_mfma_f32_16x16x32_bf16 v[20:23], v[108:111], a[76:79], v[20:23]\n"
"ds_read_b64_tr_b16 v[210:211], v230 offset:51200\n"
"v_add_f32 v61, v61, v130\n"
"v_add_f32 v63, v63, v135\n"
"v_and_b32 v143, s73, v143\n"
"v_and_b32 v148, s73, v148\n"
"v_add_f32 v56, v56, v206\n"
"v_and_b32 v149, s73, v149\n"
"v_add_f32 v56, v56, v204\n"
"v_subrev_f32 v6, s22, v6\n"
"v_subrev_f32 v7, s22, v7\n"
"v_mfma_f32_16x16x32_bf16 v[20:23], v[104:107], a[72:75], v[20:23]\n"
"ds_read_b64_tr_b16 v[182:183], v230 offset:34816\n"
"v_subrev_f32 v35, s22, v35\n"
"v_mfma_f32_16x16x32_bf16 v[20:23], v[100:103], a[68:71], v[20:23]\n"
"v_and_b32 v155, s73, v155\n"
"v_subrev_f32 v4, s22, v4\n"
"v_mfma_f32_16x16x32_bf16 v[20:23], v[96:99], a[64:67], v[20:23]\n"
"ds_read_b64_tr_b16 v[186:187], v234 offset:34816\n"
"v_add_f32 v61, v61, v134\n"
"v_add_f32 v57, v57, v148\n"
"v_and_b32 v152, s73, v152\n"
"v_add_f32 v57, v57, v152\n"
"v_lshlrev_b32 v205, 16, v153\n"
"v_subrev_f32 v5, s22, v5\n"
"v_cvt_pk_bf16_f32 v2, v4, v5\n"
"v_cvt_pk_bf16_f32 v3, v6, v7\n"
"v_subrev_f32 v34, s22, v34\n"
"v_mfma_f32_16x16x32_bf16 v[36:39], v[96:99], a[80:83], v[36:39]\n"
"ds_read_b64_tr_b16 v[208:209], v230 offset:49152\n"
"v_and_b32 v150, s73, v150\n"
"v_mfma_f32_16x16x32_bf16 v[36:39], v[100:103], a[84:87], v[36:39]\n"
"ds_read_b64_tr_b16 v[192:193], v234 offset:40960\n"
"v_add_f32 v61, v61, v138\n"
"v_add_f32 v63, v63, v139\n"
"v_and_b32 v145, s73, v145\n"
"v_permlane16_swap_b32 v0, v2\n"
"v_permlane16_swap_b32 v1, v3\n"
"v_cvt_pk_bf16_f32 v17, v18, v19\n"
"v_mfma_f32_16x16x32_bf16 v[36:39], v[104:107], a[88:91], v[36:39]\n"
"ds_read_b64_tr_b16 v[214:215], v234 offset:51200\n"
"v_add_f32 v59, v59, v141\n"
"v_mfma_f32_16x16x32_bf16 v[36:39], v[108:111], a[92:95], v[36:39]\n"
"ds_read_b64_tr_b16 v[190:191], v230 offset:43008\n"
"v_add_f32 v61, v61, v142\n"
"v_mfma_f32_16x16x32_bf16 v[36:39], v[112:115], a[32:35], v[36:39]\n"
"ds_read_b64_tr_b16 v[184:185], v234 offset:32768\n"
"buffer_store_dwordx4 v[0:3], v238, s[36:39], 0 offen sc0 sc1\n"
"v_lshlrev_b32 v206, 16, v154\n"
"v_subrev_f32 v20, s22, v20\n"
"v_subrev_f32 v21, s22, v21\n"
"v_subrev_f32 v23, s22, v23\n"
"v_cvt_pk_bf16_f32 v18, v20, v21\n"
"v_mfma_f32_16x16x32_bf16 v[36:39], v[116:119], a[36:39], v[36:39]\n"
"ds_read_b64_tr_b16 v[194:195], v234 offset:43008\n"
"v_and_b32 v154, s73, v154\n"
"v_mfma_f32_16x16x32_bf16 v[36:39], v[120:123], a[40:43], v[36:39]\n"
"ds_read_b64_tr_b16 v[188:189], v230 offset:40960\n"
"v_and_b32 v153, s73, v153\n"
"v_mfma_f32_16x16x32_bf16 v[36:39], v[224:227], a[44:47], v[36:39]\n"
"ds_read_b64_tr_b16 v[212:213], v234 offset:49152\n"
"v_add_f32 v59, v59, v145\n"
"v_add_f32 v59, v59, v149\n"
"v_add_f32 v62, v62, v207\n"
"v_subrev_f32 v22, s22, v22\n"
"v_permlane16_swap_b32 v16, v18\n"
"v_subrev_f32 v48, s22, v48\n"
"v_subrev_f32 v50, s22, v50\n"
"v_subrev_f32 v51, s22, v51\n"
"v_mfma_f32_16x16x32_bf16 v[52:55], v[224:227], a[228:231], v[52:55]\n"
"ds_read_b64_tr_b16 v[220:221], v234 offset:57344\n"
"v_cvt_pk_bf16_f32 v19, v22, v23\n"
"v_mfma_f32_16x16x32_bf16 v[52:55], v[120:123], a[224:227], v[52:55]\n"
"v_add_f32 v60, v60, v202\n"
"v_add_f32 v59, v59, v153\n"
"v_mfma_f32_16x16x32_bf16 v[52:55], v[116:119], a[212:215], v[52:55]\n"
"ds_read_b64_tr_b16 v[180:181], v230 offset:32768\n"
"v_add_f32 v63, v63, v143\n"
"v_add_f32 v61, v61, v146\n"
"v_add_f32 v63, v63, v147\n"
"v_add_f32 v61, v61, v150\n"
"v_add_f32 v63, v63, v151\n"
"v_add_f32 v60, v60, v206\n"
"v_cvt_pk_bf16_f32 v56, v56, v57\n"
"v_permlane16_swap_b32 v17, v19\n"
"v_mfma_f32_16x16x32_bf16 v[52:55], v[112:115], a[208:211], v[52:55]\n"
"v_add_f32 v58, v58, v205\n"
"v_subrev_f32 v37, s22, v37\n"
"v_mfma_f32_16x16x32_bf16 v[52:55], v[108:111], a[236:239], v[52:55]\n"
"ds_read_b64_tr_b16 v[218:219], v230 offset:59392\n"
"v_subrev_f32 v36, s22, v36\n"
"v_mfma_f32_16x16x32_bf16 v[52:55], v[104:107], a[232:235], v[52:55]\n"
"ds_read_b64_tr_b16 v[216:217], v230 offset:57344\n"
"buffer_store_dwordx4 v[16:19], v238, s[36:39], s52 offen sc0 sc1\n"
"v_add_f32 v61, v61, v154\n"
"v_add_f32 v63, v63, v155\n"
"v_cvt_pk_bf16_f32 v57, v58, v59\n"
"v_cvt_pk_bf16_f32 v58, v60, v61\n"
"v_subrev_f32 v38, s22, v38\n"
"v_mfma_f32_16x16x32_bf16 v[52:55], v[100:103], a[220:223], v[52:55]\n"
"ds_read_b64_tr_b16 v[222:223], v234 offset:59392\n"
"v_cvt_pk_bf16_f32 v59, v62, v63\n"
"v_mfma_f32_16x16x32_bf16 v[52:55], v[96:99], a[216:219], v[52:55]\n"
"s_waitcnt lgkmcnt(3)\n"
"v_permlane16_swap_b32 v56, v58\n"
"v_mfma_f32_16x16x32_bf16 v[8:11], v[180:183], a[48:51], v[8:11]\n"
"v_permlane16_swap_b32 v57, v59\n"
"v_mfma_f32_16x16x32_bf16 v[8:11], v[184:187], a[52:55], v[8:11]\n"
"buffer_store_dwordx4 v[56:59], v253, s[36:39], 0 offen sc0 sc1\n"
"v_subrev_f32 v33, s22, v33\n"
"v_subrev_f32 v39, s22, v39\n"
"v_cvt_pk_bf16_f32 v32, v32, v33\n"
"v_cvt_pk_bf16_f32 v33, v34, v35\n"
"v_cvt_pk_bf16_f32 v34, v36, v37\n"
"v_cvt_pk_bf16_f32 v35, v38, v39\n"
"v_mfma_f32_16x16x32_bf16 v[8:11], v[188:191], a[56:59], v[8:11]\n"
"v_subrev_f32 v49, s22, v49\n"
"v_subrev_f32 v52, s22, v52\n"
"v_mfma_f32_16x16x32_bf16 v[8:11], v[192:195], a[60:63], v[8:11]\n"
"v_permlane16_swap_b32 v32, v34\n"
"v_mfma_f32_16x16x32_bf16 v[8:11], v[208:211], a[0:3], v[8:11]\n"
"v_permlane16_swap_b32 v33, v35\n"
"v_mfma_f32_16x16x32_bf16 v[8:11], v[212:215], a[4:7], v[8:11]\n"
"s_waitcnt lgkmcnt(1)\n"
"buffer_store_dwordx4 v[32:35], v238, s[36:39], s53 offen sc0 sc1\n"
"v_cvt_pk_bf16_f32 v48, v48, v49\n"
"v_mfma_f32_16x16x32_bf16 v[8:11], v[216:219], a[8:11], v[8:11]\n"
"s_waitcnt lgkmcnt(0)\n"
"ds_read_b64_tr_b16 v[110:111], v235 offset:43008\n"
"v_subrev_f32 v53, s22, v53\n"
"v_mfma_f32_16x16x32_bf16 v[8:11], v[220:223], a[12:15], v[8:11]\n"
"v_cvt_pk_bf16_f32 v49, v50, v51\n"
"v_cvt_pk_bf16_f32 v50, v52, v53\n"
"v_mfma_f32_16x16x32_bf16 v[24:27], v[220:223], a[28:31], v[24:27]\n"
"s_nop 0\n"
"v_permlane16_swap_b32 v48, v50\n"
"v_mfma_f32_16x16x32_bf16 v[24:27], v[216:219], a[24:27], v[24:27]\n"
"ds_read_b64_tr_b16 v[114:115], v231 offset:51200\n"
"v_subrev_f32 v55, s22, v55\n"
"v_mfma_f32_16x16x32_bf16 v[24:27], v[212:215], a[20:23], v[24:27]\n"
"ds_read_b64_tr_b16 v[102:103], v235 offset:34816\n"
"v_subrev_f32 v54, s22, v54\n"
"v_mfma_f32_16x16x32_bf16 v[24:27], v[208:211], a[16:19], v[24:27]\n"
"ds_read_b64_tr_b16 v[112:113], v231 offset:49152\n"
"v_cvt_pk_bf16_f32 v51, v54, v55\n"
"v_mfma_f32_16x16x32_bf16 v[24:27], v[192:195], a[76:79], v[24:27]\n"
"s_nop 0\n"
"v_permlane16_swap_b32 v49, v51\n"
"v_mfma_f32_16x16x32_bf16 v[24:27], v[188:191], a[72:75], v[24:27]\n"
"buffer_store_dwordx4 v[48:51], v252, s[36:39], 0 offen sc0 sc1\n"
"ds_read_b64_tr_b16 v[106:107], v231 offset:43008\n"
"v_mfma_f32_16x16x32_bf16 v[24:27], v[184:187], a[68:71], v[24:27]\n"
"ds_read_b64_tr_b16 v[96:97], v231 offset:32768\n"
"ds_read_b64_tr_b16 v[98:99], v231 offset:34816\n"
"v_mfma_f32_16x16x32_bf16 v[24:27], v[180:183], a[64:67], v[24:27]\n"
"ds_read_b64_tr_b16 v[100:101], v235 offset:32768\n"
"v_and_b32 v163, v160, v161\n"
"v_mfma_f32_16x16x32_bf16 v[40:43], v[180:183], a[80:83], v[40:43]\n"
"ds_read_b64_tr_b16 v[118:119], v235 offset:51200\n"
"v_xor_b32 v162, v160, v163\n"
"v_mfma_f32_16x16x32_bf16 v[40:43], v[184:187], a[84:87], v[40:43]\n"
"ds_read_b64_tr_b16 v[104:105], v231 offset:40960\n"
"v_mfma_f32_16x16x32_bf16 v[40:43], v[188:191], a[88:91], v[40:43]\n"
"ds_read_b64_tr_b16 v[108:109], v235 offset:40960\n"
"v_subrev_f32 v8, s22, v8\n"
"v_mfma_f32_16x16x32_bf16 v[40:43], v[192:195], a[92:95], v[40:43]\n"
"ds_read_b64_tr_b16 v[116:117], v235 offset:49152\n"
"v_mfma_f32_16x16x32_bf16 v[40:43], v[208:211], a[32:35], v[40:43]\n"
"ds_read_b64_tr_b16 v[122:123], v231 offset:59392\n"
"v_subrev_f32 v9, s22, v9\n"
"v_mfma_f32_16x16x32_bf16 v[40:43], v[212:215], a[36:39], v[40:43]\n"
"ds_read_b64_tr_b16 v[120:121], v231 offset:57344\n"
"v_subrev_f32 v10, s22, v10\n"
"v_mfma_f32_16x16x32_bf16 v[40:43], v[216:219], a[40:43], v[40:43]\n"
"ds_read_b64_tr_b16 v[224:225], v235 offset:57344\n"
"ds_read_b64_tr_b16 v[226:227], v235 offset:59392\n"
"v_mfma_f32_16x16x32_bf16 v[40:43], v[220:223], a[44:47], v[40:43]\n"
"s_waitcnt lgkmcnt(8)\n"
"v_subrev_f32 v11, s22, v11\n"
"v_mfma_f32_16x16x32_bf16 v[12:15], v[96:99], a[48:51], v[12:15]\n"
"v_mfma_f32_16x16x32_bf16 v[12:15], v[100:103], a[52:55], v[12:15]\n"
"s_waitcnt lgkmcnt(6)\n"
"v_mov_b32 v160, 0\n"
"v_subrev_f32 v24, s22, v24\n"
"v_mfma_f32_16x16x32_bf16 v[12:15], v[104:107], a[56:59], v[12:15]\n"
"s_waitcnt lgkmcnt(5)\n"
"v_mfma_f32_16x16x32_bf16 v[12:15], v[108:111], a[60:63], v[12:15]\n"
"v_mfma_f32_16x16x32_bf16 v[12:15], v[112:115], a[0:3], v[12:15]\n"
"s_waitcnt lgkmcnt(3)\n"
"v_cvt_pk_bf16_f32 v8, v8, v9\n"
"v_mfma_f32_16x16x32_bf16 v[12:15], v[116:119], a[4:7], v[12:15]\n"
"s_waitcnt lgkmcnt(2)\n"
"v_mfma_f32_16x16x32_bf16 v[12:15], v[120:123], a[8:11], v[12:15]\n"
"s_waitcnt lgkmcnt(0)\n"
"v_mov_b32 v161, 0\n"
"v_subrev_f32 v25, s22, v25\n"
"v_mfma_f32_16x16x32_bf16 v[12:15], v[224:227], a[12:15], v[12:15]\n"
"v_subrev_f32 v26, s22, v26\n"
"s_waitcnt vmcnt(5)\n"
"v_mfma_f32_16x16x32_bf16 v[64:67], v[162:165], v[72:75], 0\n"
"v_subrev_f32 v40, s22, v40\n"
"v_subrev_f32 v43, s22, v43\n"
"v_mfma_f32_16x16x32_bf16 v[64:67], v[162:165], v[76:79], v[64:67]\n"
"v_subrev_f32 v41, s22, v41\n"
"v_mfma_f32_16x16x32_bf16 v[64:67], v[162:165], v[80:83], v[64:67]\n"
"v_mfma_f32_16x16x32_bf16 v[64:67], v[162:165], v[84:87], v[64:67]\n"
"v_subrev_f32 v13, s22, v13\n"
"v_mfma_f32_16x16x32_bf16 v[64:67], v[162:165], v[88:91], v[64:67]\n"
"v_mfma_f32_16x16x32_bf16 v[64:67], v[162:165], v[92:95], v[64:67]\n"
"v_subrev_f32 v15, s22, v15\n"
"v_cvt_pk_bf16_f32 v40, v40, v41\n"
"v_mfma_f32_16x16x32_bf16 v[64:67], v[162:165], v[244:247], v[64:67]\n"
"v_subrev_f32 v14, s22, v14\n"
"v_cvt_pk_bf16_f32 v9, v10, v11\n"
"v_mfma_f32_16x16x32_bf16 v[64:67], v[162:165], v[156:159], v[64:67]\n"
"v_subrev_f32 v12, s22, v12\n"
"v_cvt_pk_bf16_f32 v24, v24, v25\n"
"v_mfma_f32_16x16x32_bf16 v[68:71], v[160:163], v[72:75], 0\n"
"v_cvt_pk_bf16_f32 v10, v12, v13\n"
"v_cvt_pk_bf16_f32 v11, v14, v15\n"
"v_mfma_f32_16x16x32_bf16 v[68:71], v[160:163], v[76:79], v[68:71]\n"
"s_nop 0\n"
"v_permlane16_swap_b32 v9, v11\n"
"v_mfma_f32_16x16x32_bf16 v[68:71], v[160:163], v[80:83], v[68:71]\n"
"v_cvt_pk_bf16_f32 v64, v64, v65\n"
"v_mfma_f32_16x16x32_bf16 v[68:71], v[160:163], v[84:87], v[68:71]\n"
"v_permlane16_swap_b32 v8, v10\n"
"v_mfma_f32_16x16x32_bf16 v[68:71], v[160:163], v[88:91], v[68:71]\n"
"v_subrev_f32 v42, s22, v42\n"
"v_mfma_f32_16x16x32_bf16 v[68:71], v[160:163], v[92:95], v[68:71]\n"
"buffer_store_dwordx4 v[8:11], v239, s[36:39], 0 offen sc0 sc1\n"
"v_mfma_f32_16x16x32_bf16 v[68:71], v[160:163], v[244:247], v[68:71]\n"
"v_mfma_f32_16x16x32_bf16 v[68:71], v[160:163], v[156:159], v[68:71]\n"
"v_cvt_pk_bf16_f32 v65, v66, v67\n"
"v_mfma_f32_16x16x32_bf16 v[28:31], v[224:227], a[28:31], v[28:31]\n"
"v_subrev_f32 v27, s22, v27\n"
"v_mfma_f32_16x16x32_bf16 v[28:31], v[120:123], a[24:27], v[28:31]\n"
"v_mfma_f32_16x16x32_bf16 v[28:31], v[116:119], a[20:23], v[28:31]\n"
"v_mfma_f32_16x16x32_bf16 v[28:31], v[112:115], a[16:19], v[28:31]\n"
"s_nop 1\n"
"v_cvt_pk_bf16_f32 v66, v68, v69\n"
"v_cvt_pk_bf16_f32 v25, v26, v27\n"
"v_mfma_f32_16x16x32_bf16 v[28:31], v[108:111], a[76:79], v[28:31]\n"
"v_permlane16_swap_b32 v64, v66\n"
"v_mfma_f32_16x16x32_bf16 v[28:31], v[104:107], a[72:75], v[28:31]\n"
"v_cvt_pk_bf16_f32 v67, v70, v71\n"
"v_cvt_pk_bf16_f32 v41, v42, v43\n"
"v_mfma_f32_16x16x32_bf16 v[28:31], v[100:103], a[68:71], v[28:31]\n"
"v_permlane16_swap_b32 v65, v67\n"
"v_mfma_f32_16x16x32_bf16 v[28:31], v[96:99], a[64:67], v[28:31]\n"
"buffer_store_dwordx4 v[64:67], v253, s[36:39], 0 offen offset:128 sc0 sc1\n"
"v_mfma_f32_16x16x32_bf16 v[44:47], v[96:99], a[80:83], v[44:47]\n"
"v_mfma_f32_16x16x32_bf16 v[44:47], v[100:103], a[84:87], v[44:47]\n"
"v_mfma_f32_16x16x32_bf16 v[44:47], v[104:107], a[88:91], v[44:47]\n"
"s_nop 3\n"
"v_subrev_f32 v29, s22, v29\n"
"v_subrev_f32 v30, s22, v30\n"
"v_mfma_f32_16x16x32_bf16 v[44:47], v[108:111], a[92:95], v[44:47]\n"
"v_subrev_f32 v28, s22, v28\n"
"v_subrev_f32 v31, s22, v31\n"
"v_mfma_f32_16x16x32_bf16 v[44:47], v[112:115], a[32:35], v[44:47]\n"
"v_cvt_pk_bf16_f32 v26, v28, v29\n"
"v_cvt_pk_bf16_f32 v27, v30, v31\n"
"v_mfma_f32_16x16x32_bf16 v[44:47], v[116:119], a[36:39], v[44:47]\n"
"v_permlane16_swap_b32 v24, v26\n"
"v_mfma_f32_16x16x32_bf16 v[44:47], v[120:123], a[40:43], v[44:47]\n"
"v_permlane16_swap_b32 v25, v27\n"
"v_mfma_f32_16x16x32_bf16 v[44:47], v[224:227], a[44:47], v[44:47]\n"
"buffer_store_dwordx4 v[24:27], v239, s[36:39], s52 offen sc0 sc1\n"
"s_nop 6\n"
"v_subrev_f32 v44, s22, v44\n"
"v_subrev_f32 v45, s22, v45\n"
"v_subrev_f32 v46, s22, v46\n"
"v_subrev_f32 v47, s22, v47\n"
"v_cvt_pk_bf16_f32 v42, v44, v45\n"
"v_cvt_pk_bf16_f32 v43, v46, v47\n"
"s_nop 0\n"
"v_permlane16_swap_b32 v40, v42\n"
"v_permlane16_swap_b32 v41, v43\n"
"buffer_store_dwordx4 v[40:43], v239, s[36:39], s53 offen sc0 sc1\n"
"s_branch LFIN_%=\n"
"LSW_%=:\n"
"s_add_u32 s16, s8, s95\n"
"s_addc_u32 s17, s9, 0\n"
"s_sub_u32 s18, 655360, s95\n"
"s_add_u32 s20, s8, s100\n"
"s_addc_u32 s21, s9, 0\n"
"s_sub_u32 s22, 655360, s100\n"
"s_mov_b32 s56, 0\n"
"s_mov_b32 s55, 0\n"
"s_mov_b32 s58, 0\n"
"s_mov_b32 s59, 163840\n"
"s_mov_b32 s52, 9216\n"
"s_mov_b32 s53, 18432\n"
"s_mov_b32 s54, 27648\n"
"v_lshrrev_b32 v206, 7, v242\n"
"v_mul_u32_u24 v206, 1280, v206\n"
"v_and_b32 v240, 0x7f, v240\n"
"v_add_u32 v240, v206, v240\n"
"buffer_load_dword v252, v240, s[16:19], 0 offen\n"
"s_branch LSWK_%=\n"
"LGP_%=:\n"
"s_mov_b32 s80, 0x40000\n"
"LGPa_%=:\n"
"s_sleep 1\n"
"global_load_dword v200, v201, s[6:7] sc1\n"
"s_sub_u32 s80, s80, 1\n"
"s_cmp_eq_u32 s80, 0\n"
"s_waitcnt vmcnt(0)\n"
"s_cbranch_scc1 LGPK_%=\n"
"v_subrev_u32 v200, s21, v200\n"
"v_cmp_lt_u32 vcc, s69, v200\n"
"s_nop 4\n"
"s_cbranch_vccnz LGPa_%=\n"
"s_branch LGPK_%=\n"
"LGQ_%=:\n"
"s_mov_b32 s80, 0x40000\n"
"LGQa_%=:\n"
"s_sleep 1\n"
"global_load_dword v200, v201, s[6:7] sc1\n"
"s_sub_u32 s80, s80, 1\n"
"s_cmp_eq_u32 s80, 0\n"
"s_waitcnt vmcnt(0)\n"
"s_cbranch_scc1 LGQK_%=\n"
"v_subrev_u32 v200, s21, v200\n"
"v_cmp_lt_u32 vcc, s69, v200\n"
"s_nop 4\n"
"s_cbranch_vccnz LGQa_%=\n"
"s_branch LGQK_%=\n"
"LGR_%=:\n"
"s_mov_b32 s80, 0x40000\n"
"LGRa_%=:\n"
"s_sleep 1\n"
"global_load_dword v200, v201, s[6:7] sc1\n"
"s_sub_u32 s80, s80, 1\n"
"s_cmp_eq_u32 s80, 0\n"
"s_waitcnt vmcnt(0)\n"
"s_cbranch_scc1 LGRK_%=\n"
"v_subrev_u32 v200, s21, v200\n"
"v_cmp_lt_u32 vcc, s69, v200\n"
"s_nop 4\n"
"s_cbranch_vccnz LGRa_%=\n"
"s_branch LGRK_%=\n"
"LGV_%=:\n"
"s_mov_b32 s80, 0x40000\n"
"LGVa_%=:\n"
"s_sleep 1\n"
"global_load_dword v200, v201, s[6:7] sc1\n"
"s_sub_u32 s80, s80, 1\n"
"s_cmp_eq_u32 s80, 0\n"
"s_waitcnt vmcnt(0)\n"
"s_cbranch_scc1 LGVK_%=\n"
"v_subrev_u32 v200, s21, v200\n"
"v_cmp_lt_u32 vcc, s69, v200\n"
"s_nop 4\n"
"s_cbranch_vccnz LGVa_%=\n"
"s_branch LGVK_%=\n"
"LEP_%=:\n"
"s_add_u32 s92, s92, 1\n"
"v_mov_b32 v200, s92\n"
"v_mov_b32 v255, 0\n"
"s_mov_b64 exec, 1\n"
"global_store_dword v255, v200, s[6:7] offset:128 sc1\n"
"s_mov_b64 exec, -1\n"
"s_branch LEPK_%=\n"
"LPCE0_%=:\n"
"s_memrealtime s[80:81]\n"
"s_sub_u32 s82, s67, s66\n"
"s_cmp_gt_i32 s82, 0\n"
"s_cselect_b32 s83, s67, s66\n"
"s_waitcnt lgkmcnt(0)\n"
"s_sub_u32 s82, s83, s80\n"
"s_cmp_gt_i32 s82, 3\n"
"s_cbranch_scc0 LPEE0_%=\n"
"LPDE0_%=:\n"
"s_sleep 1\n"
"s_memrealtime s[80:81]\n"
"s_waitcnt lgkmcnt(0)\n"
"s_sub_u32 s82, s83, s80\n"
"s_cmp_gt_i32 s82, 3\n"
"s_cbranch_scc1 LPDE0_%=\n"
"LPEE0_%=:\n"
"s_add_u32 s66, s66, 126\n"
"s_add_u32 s67, s80, s72\n"
"s_sub_u32 s72, s72, 0\n"
"s_max_u32 s72, s72, 122\n"
"s_branch LPKE0_%=\n"
"LPCE1_%=:\n"
"s_memrealtime s[80:81]\n"
"s_sub_u32 s82, s67, s66\n"
"s_cmp_gt_i32 s82, 0\n"
"s_cselect_b32 s83, s67, s66\n"
"s_waitcnt lgkmcnt(0)\n"
"s_sub_u32 s82, s83, s80\n"
"s_cmp_gt_i32 s82, 3\n"
"s_cbranch_scc0 LPEE1_%=\n"
"LPDE1_%=:\n"
"s_sleep 1\n"
"s_memrealtime s[80:81]\n"
"s_waitcnt lgkmcnt(0)\n"
"s_sub_u32 s82, s83, s80\n"
"s_cmp_gt_i32 s82, 3\n"
"s_cbranch_scc1 LPDE1_%=\n"
"LPEE1_%=:\n"
"s_add_u32 s66, s66, 126\n"
"s_add_u32 s67, s80, s72\n"
"s_sub_u32 s72, s72, 0\n"
"s_max_u32 s72, s72, 122\n"
"s_branch LPKE1_%=\n"
"LPCT0_%=:\n"
"s_memrealtime s[80:81]\n"
"s_sub_u32 s82, s67, s66\n"
"s_cmp_gt_i32 s82, 0\n"
"s_cselect_b32 s83, s67, s66\n"
"s_waitcnt lgkmcnt(0)\n"
"s_sub_u32 s82, s83, s80\n"
"s_cmp_gt_i32 s82, 3\n"
"s_cbranch_scc0 LPET0_%=\n"
"LPDT0_%=:\n"
"s_sleep 1\n"
"s_memrealtime s[80:81]\n"
"s_waitcnt lgkmcnt(0)\n"
"s_sub_u32 s82, s83, s80\n"
"s_cmp_gt_i32 s82, 3\n"
"s_cbranch_scc1 LPDT0_%=\n"
"LPET0_%=:\n"
"s_add_u32 s66, s66, 126\n"
"s_add_u32 s67, s80, s72\n"
"s_sub_u32 s72, s72, 0\n"
"s_max_u32 s72, s72, 122\n"
"s_branch LPKT0_%=\n"
"LPCT1_%=:\n"
"s_memrealtime s[80:81]\n"
"s_sub_u32 s82, s67, s66\n"
"s_cmp_gt_i32 s82, 0\n"
"s_cselect_b32 s83, s67, s66\n"
"s_waitcnt lgkmcnt(0)\n"
"s_sub_u32 s82, s83, s80\n"
"s_cmp_gt_i32 s82, 3\n"
"s_cbranch_scc0 LPET1_%=\n"
"LPDT1_%=:\n"
"s_sleep 1\n"
"s_memrealtime s[80:81]\n"
"s_waitcnt lgkmcnt(0)\n"
"s_sub_u32 s82, s83, s80\n"
"s_cmp_gt_i32 s82, 3\n"
"s_cbranch_scc1 LPDT1_%=\n"
"LPET1_%=:\n"
"s_add_u32 s66, s66, 126\n"
"s_add_u32 s67, s80, s72\n"
"s_sub_u32 s72, s72, 0\n"
"s_max_u32 s72, s72, 122\n"
"s_branch LPKT1_%=\n"
"LPCL0_%=:\n"
"s_memrealtime s[80:81]\n"
"s_sub_u32 s82, s67, s66\n"
"s_cmp_gt_i32 s82, 0\n"
"s_cselect_b32 s83, s67, s66\n"
"s_waitcnt lgkmcnt(0)\n"
"s_sub_u32 s82, s83, s80\n"
"s_cmp_gt_i32 s82, 3\n"
"s_cbranch_scc0 LPEL0_%=\n"
"LPDL0_%=:\n"
"s_sleep 1\n"
"s_memrealtime s[80:81]\n"
"s_waitcnt lgkmcnt(0)\n"
"s_sub_u32 s82, s83, s80\n"
"s_cmp_gt_i32 s82, 3\n"
"s_cbranch_scc1 LPDL0_%=\n"
"LPEL0_%=:\n"
"s_add_u32 s66, s66, 126\n"
"s_add_u32 s67, s80, s72\n"
"s_sub_u32 s72, s72, 0\n"
"s_max_u32 s72, s72, 122\n"
"s_branch LPKL0_%=\n"
"LPCL1_%=:\n"
"s_memrealtime s[80:81]\n"
"s_sub_u32 s82, s67, s66\n"
"s_cmp_gt_i32 s82, 0\n"
"s_cselect_b32 s83, s67, s66\n"
"s_waitcnt lgkmcnt(0)\n"
"s_sub_u32 s82, s83, s80\n"
"s_cmp_gt_i32 s82, 3\n"
"s_cbranch_scc0 LPEL1_%=\n"
"LPDL1_%=:\n"
"s_sleep 1\n"
"s_memrealtime s[80:81]\n"
"s_waitcnt lgkmcnt(0)\n"
"s_sub_u32 s82, s83, s80\n"
"s_cmp_gt_i32 s82, 3\n"
"s_cbranch_scc1 LPDL1_%=\n"
"LPEL1_%=:\n"
"s_add_u32 s66, s66, 126\n"
"s_add_u32 s67, s80, s72\n"
"s_sub_u32 s72, s72, 0\n"
"s_max_u32 s72, s72, 122\n"
"s_branch LPKL1_%=\n"
"LPCL2_%=:\n"
"s_memrealtime s[80:81]\n"
"s_sub_u32 s82, s67, s66\n"
"s_cmp_gt_i32 s82, 0\n"
"s_cselect_b32 s83, s67, s66\n"
"s_waitcnt lgkmcnt(0)\n"
"s_sub_u32 s82, s83, s80\n"
"s_cmp_gt_i32 s82, 3\n"
"s_cbranch_scc0 LPEL2_%=\n"
"LPDL2_%=:\n"
"s_sleep 1\n"
"s_memrealtime s[80:81]\n"
"s_waitcnt lgkmcnt(0)\n"
"s_sub_u32 s82, s83, s80\n"
"s_cmp_gt_i32 s82, 3\n"
"s_cbranch_scc1 LPDL2_%=\n"
"LPEL2_%=:\n"
"s_add_u32 s66, s66, 126\n"
"s_add_u32 s67, s80, s72\n"
"s_sub_u32 s72, s72, 0\n"
"s_max_u32 s72, s72, 122\n"
"s_branch LPKL2_%=\n"
"LPCL3_%=:\n"
"s_memrealtime s[80:81]\n"
"s_sub_u32 s82, s67, s66\n"
"s_cmp_gt_i32 s82, 0\n"
"s_cselect_b32 s83, s67, s66\n"
"s_waitcnt lgkmcnt(0)\n"
"s_sub_u32 s82, s83, s80\n"
"s_cmp_gt_i32 s82, 3\n"
"s_cbranch_scc0 LPEL3_%=\n"
"LPDL3_%=:\n"
"s_sleep 1\n"
"s_memrealtime s[80:81]\n"
"s_waitcnt lgkmcnt(0)\n"
"s_sub_u32 s82, s83, s80\n"
"s_cmp_gt_i32 s82, 3\n"
"s_cbranch_scc1 LPDL3_%=\n"
"LPEL3_%=:\n"
"s_add_u32 s66, s66, 126\n"
"s_add_u32 s67, s80, s72\n"
"s_sub_u32 s72, s72, 0\n"
"s_max_u32 s72, s72, 122\n"
"s_branch LPKL3_%=\n"
"LEND_%=:\n"
"LSLOW_%=:\n"
"s_waitcnt vmcnt(0) lgkmcnt(0)\n"
"s_nop 9\n"
"v_bfe_u32 v148, v242, 4, 4\n"
"v_bfe_u32 v149, v242, 8, 2\n"
"v_lshlrev_b32 v144, 8, v148\n"
"v_lshl_add_u32 v144, v149, 4, v144\n"
"v_add_u32 v144, s43, v144\n"
"v_xor_b32 v145, 64, v144\n"
"v_lshlrev_b32 v146, 1, v148\n"
"v_lshl_add_u32 v146, v149, 10, v146\n"
"v_add_u32 v146, s43, v146\n"
"v_xor_b32 v147, 64, v146\n"
"v_mov_b32 v152, 0xff7fffff\n"
"v_mov_b32 v155, 0\n"
"v_mov_b32 v0, 0\n"
"v_mov_b32 v1, 0\n"
"v_mov_b32 v2, 0\n"
"v_mov_b32 v3, 0\n"
"v_mov_b32 v4, 0\n"
"v_mov_b32 v5, 0\n"
"v_mov_b32 v6, 0\n"
"v_mov_b32 v7, 0\n"
"v_mov_b32 v8, 0\n"
"v_mov_b32 v9, 0\n"
"v_mov_b32 v10, 0\n"
"v_mov_b32 v11, 0\n"
"v_mov_b32 v12, 0\n"
"v_mov_b32 v13, 0\n"
"v_mov_b32 v14, 0\n"
"v_mov_b32 v15, 0\n"
"v_mov_b32 v16, 0\n"
"v_mov_b32 v17, 0\n"
"v_mov_b32 v18, 0\n"
"v_mov_b32 v19, 0\n"
"v_mov_b32 v20, 0\n"
"v_mov_b32 v21, 0\n"
"v_mov_b32 v22, 0\n"
"v_mov_b32 v23, 0\n"
"v_mov_b32 v24, 0\n"
"v_mov_b32 v25, 0\n"
"v_mov_b32 v26, 0\n"
"v_mov_b32 v27, 0\n"
"v_mov_b32 v28, 0\n"
"v_mov_b32 v29, 0\n"
"v_mov_b32 v30, 0\n"
"v_mov_b32 v31, 0\n"
"v_mov_b32 v156, 0xff7fffff\n"
"v_mov_b32 v159, 0\n"
"v_mov_b32 v32, 0\n"
"v_mov_b32 v33, 0\n"
"v_mov_b32 v34, 0\n"
"v_mov_b32 v35, 0\n"
"v_mov_b32 v36, 0\n"
"v_mov_b32 v37, 0\n"
"v_mov_b32 v38, 0\n"
"v_mov_b32 v39, 0\n"
"v_mov_b32 v40, 0\n"
"v_mov_b32 v41, 0\n"
"v_mov_b32 v42, 0\n"
"v_mov_b32 v43, 0\n"
"v_mov_b32 v44, 0\n"
"v_mov_b32 v45, 0\n"
"v_mov_b32 v46, 0\n"
"v_mov_b32 v47, 0\n"
"v_mov_b32 v48, 0\n"
"v_mov_b32 v49, 0\n"
"v_mov_b32 v50, 0\n"
"v_mov_b32 v51, 0\n"
"v_mov_b32 v52, 0\n"
"v_mov_b32 v53, 0\n"
"v_mov_b32 v54, 0\n"
"v_mov_b32 v55, 0\n"
"v_mov_b32 v56, 0\n"
"v_mov_b32 v57, 0\n"
"v_mov_b32 v58, 0\n"
"v_mov_b32 v59, 0\n"
"v_mov_b32 v60, 0\n"
"v_mov_b32 v61, 0\n"
"v_mov_b32 v62, 0\n"
"v_mov_b32 v63, 0\n"
"v_mov_b32 v160, 0xff7fffff\n"
"v_mov_b32 v163, 0\n"
"v_mov_b32 v64, 0\n"
"v_mov_b32 v65, 0\n"
"v_mov_b32 v66, 0\n"
"v_mov_b32 v67, 0\n"
"v_mov_b32 v68, 0\n"
"v_mov_b32 v69, 0\n"
"v_mov_b32 v70, 0\n"
"v_mov_b32 v71, 0\n"
"v_mov_b32 v72, 0\n"
"v_mov_b32 v73, 0\n"
"v_mov_b32 v74, 0\n"
"v_mov_b32 v75, 0\n"
"v_mov_b32 v76, 0\n"
"v_mov_b32 v77, 0\n"
"v_mov_b32 v78, 0\n"
"v_mov_b32 v79, 0\n"
"v_mov_b32 v80, 0\n"
"v_mov_b32 v81, 0\n"
"v_mov_b32 v82, 0\n"
"v_mov_b32 v83, 0\n"
"v_mov_b32 v84, 0\n"
"v_mov_b32 v85, 0\n"
"v_mov_b32 v86, 0\n"
"v_mov_b32 v87, 0\n"
"v_mov_b32 v88, 0\n"
"v_mov_b32 v89, 0\n"
"v_mov_b32 v90, 0\n"
"v_mov_b32 v91, 0\n"
"v_mov_b32 v92, 0\n"
"v_mov_b32 v93, 0\n"
"v_mov_b32 v94, 0\n"
"v_mov_b32 v95, 0\n"
"v_mov_b32 v164, 0xff7fffff\n"
"v_mov_b32 v167, 0\n"
"v_accvgpr_write_b32 a208, 0\n"
"v_accvgpr_write_b32 a209, 0\n"
"v_accvgpr_write_b32 a210, 0\n"
"v_accvgpr_write_b32 a211, 0\n"
"v_accvgpr_write_b32 a212, 0\n"
"v_accvgpr_write_b32 a213, 0\n"
"v_accvgpr_write_b32 a214, 0\n"
"v_accvgpr_write_b32 a215, 0\n"
"v_accvgpr_write_b32 a216, 0\n"
"v_accvgpr_write_b32 a217, 0\n"
"v_accvgpr_write_b32 a218, 0\n"
"v_accvgpr_write_b32 a219, 0\n"
"v_accvgpr_write_b32 a220, 0\n"
"v_accvgpr_write_b32 a221, 0\n"
"v_accvgpr_write_b32 a222, 0\n"
"v_accvgpr_write_b32 a223, 0\n"
"v_accvgpr_write_b32 a224, 0\n"
"v_accvgpr_write_b32 a225, 0\n"
"v_accvgpr_write_b32 a226, 0\n"
"v_accvgpr_write_b32 a227, 0\n"
"v_accvgpr_write_b32 a228, 0\n"
"v_accvgpr_write_b32 a229, 0\n"
"v_accvgpr_write_b32 a230, 0\n"
"v_accvgpr_write_b32 a231, 0\n"
"v_accvgpr_write_b32 a232, 0\n"
"v_accvgpr_write_b32 a233, 0\n"
"v_accvgpr_write_b32 a234, 0\n"
"v_accvgpr_write_b32 a235, 0\n"
"v_accvgpr_write_b32 a236, 0\n"
"v_accvgpr_write_b32 a237, 0\n"
"v_accvgpr_write_b32 a238, 0\n"
"v_accvgpr_write_b32 a239, 0\n"
"s_lshl_b32 s80, s47, 16\n"
"s_sub_u32 s80, s80, 261248\n"
"s_add_u32 s81, s80, 4096\n"
"s_mov_b32 s82, 0\n"
"LSLOOP_%=:\n"
"s_lshl_b32 s83, s82, 6\n"
"s_and_b32 s83, s83, 64\n"
"buffer_load_dwordx4 v[128:131], v144, s[84:87], s80 offen offset:0\n"
"buffer_load_dwordx4 v[132:135], v145, s[84:87], s80 offen offset:0\n"
"buffer_load_dwordx4 v[136:139], v144, s[84:87], s80 offen offset:128\n"
"buffer_load_dwordx4 v[140:143], v145, s[84:87], s80 offen offset:128\n"
"s_waitcnt vmcnt(0)\n"
"v_mfma_f32_16x16x32_bf16 v[96:99], v[128:131], a[0:3], 0\n"
"v_mfma_f32_16x16x32_bf16 v[96:99], v[132:135], a[4:7], v[96:99]\n"
"v_mfma_f32_16x16x32_bf16 v[96:99], v[136:139], a[8:11], v[96:99]\n"
"v_mfma_f32_16x16x32_bf16 v[96:99], v[140:143], a[12:15], v[96:99]\n"
"v_mfma_f32_16x16x32_bf16 v[104:107], v[128:131], a[16:19], 0\n"
"v_mfma_f32_16x16x32_bf16 v[104:107], v[132:135], a[20:23], v[104:107]\n"
"v_mfma_f32_16x16x32_bf16 v[104:107], v[136:139], a[24:27], v[104:107]\n"
"v_mfma_f32_16x16x32_bf16 v[104:107], v[140:143], a[28:31], v[104:107]\n"
"v_mfma_f32_16x16x32_bf16 v[112:115], v[128:131], a[32:35], 0\n"
"v_mfma_f32_16x16x32_bf16 v[112:115], v[132:135], a[36:39], v[112:115]\n"
"v_mfma_f32_16x16x32_bf16 v[112:115], v[136:139], a[40:43], v[112:115]\n"
"v_mfma_f32_16x16x32_bf16 v[112:115], v[140:143], a[44:47], v[112:115]\n"
"v_mfma_f32_16x16x32_bf16 v[120:123], v[128:131], a[192:195], 0\n"
"v_mfma_f32_16x16x32_bf16 v[120:123], v[132:135], a[196:199], v[120:123]\n"
"v_mfma_f32_16x16x32_bf16 v[120:123], v[136:139], a[200:203], v[120:123]\n"
"v_mfma_f32_16x16x32_bf16 v[120:123], v[140:143], a[204:207], v[120:123]\n"
"s_nop 7\n"
"buffer_load_dwordx4 v[128:131], v144, s[84:87], s81 offen offset:0\n"
"buffer_load_dwordx4 v[132:135], v145, s[84:87], s81 offen offset:0\n"
"buffer_load_dwordx4 v[136:139], v144, s[84:87], s81 offen offset:128\n"
"buffer_load_dwordx4 v[140:143], v145, s[84:87], s81 offen offset:128\n"
"s_waitcnt vmcnt(0)\n"
"v_mfma_f32_16x16x32_bf16 v[100:103], v[128:131], a[0:3], 0\n"
"v_mfma_f32_16x16x32_bf16 v[100:103], v[132:135], a[4:7], v[100:103]\n"
"v_mfma_f32_16x16x32_bf16 v[100:103], v[136:139], a[8:11], v[100:103]\n"
"v_mfma_f32_16x16x32_bf16 v[100:103], v[140:143], a[12:15], v[100:103]\n"
"v_mfma_f32_16x16x32_bf16 v[108:111], v[128:131], a[16:19], 0\n"
"v_mfma_f32_16x16x32_bf16 v[108:111], v[132:135], a[20:23], v[108:111]\n"
"v_mfma_f32_16x16x32_bf16 v[108:111], v[136:139], a[24:27], v[108:111]\n"
"v_mfma_f32_16x16x32_bf16 v[108:111], v[140:143], a[28:31], v[108:111]\n"
"v_mfma_f32_16x16x32_bf16 v[116:119], v[128:131], a[32:35], 0\n"
"v_mfma_f32_16x16x32_bf16 v[116:119], v[132:135], a[36:39], v[116:119]\n"
"v_mfma_f32_16x16x32_bf16 v[116:119], v[136:139], a[40:43], v[116:119]\n"
"v_mfma_f32_16x16x32_bf16 v[116:119], v[140:143], a[44:47], v[116:119]\n"
"v_mfma_f32_16x16x32_bf16 v[124:127], v[128:131], a[192:195], 0\n"
"v_mfma_f32_16x16x32_bf16 v[124:127], v[132:135], a[196:199], v[124:127]\n"
"v_mfma_f32_16x16x32_bf16 v[124:127], v[136:139], a[200:203], v[124:127]\n"
"v_mfma_f32_16x16x32_bf16 v[124:127], v[140:143], a[204:207], v[124:127]\n"
"s_nop 7\n"
"v_max3_f32 v153, v96, v97, v98\n"
"v_max3_f32 v153, v153, v99, v100\n"
"v_max3_f32 v153, v153, v101, v102\n"
"v_max3_f32 v153, v153, v103, v152\n"
"v_mov_b32 v154, v153\n"
"s_nop 1\n"
"v_permlane32_swap_b32 v153, v154\n"
"v_max_f32 v153, v153, v154\n"
"v_mov_b32 v154, v153\n"
"s_nop 1\n"
"v_permlane16_swap_b32 v153, v154\n"
"v_max_f32 v153, v153, v154\n"
"v_sub_f32 v154, v152, v153\n"
"v_exp_f32 v154, v154\n"
"v_mov_b32 v152, v153\n"
"v_sub_f32 v96, v96, v152\n"
"v_sub_f32 v97, v97, v152\n"
"v_sub_f32 v98, v98, v152\n"
"v_sub_f32 v99, v99, v152\n"
"v_sub_f32 v100, v100, v152\n"
"v_sub_f32 v101, v101, v152\n"
"v_sub_f32 v102, v102, v152\n"
"v_sub_f32 v103, v103, v152\n"
"v_exp_f32 v96, v96\n"
"v_exp_f32 v97, v97\n"
"v_exp_f32 v98, v98\n"
"v_exp_f32 v99, v99\n"
"v_exp_f32 v100, v100\n"
"v_exp_f32 v101, v101\n"
"v_exp_f32 v102, v102\n"
"v_exp_f32 v103, v103\n"
"v_mul_f32 v155, v155, v154\n"
"v_add_f32 v155, v155, v96\n"
"v_add_f32 v155, v155, v97\n"
"v_add_f32 v155, v155, v98\n"
"v_add_f32 v155, v155, v99\n"
"v_add_f32 v155, v155, v100\n"
"v_add_f32 v155, v155, v101\n"
"v_add_f32 v155, v155, v102\n"
"v_add_f32 v155, v155, v103\n"
"v_cvt_pk_bf16_f32 v96, v96, v97\n"
"v_cvt_pk_bf16_f32 v97, v98, v99\n"
"v_cvt_pk_bf16_f32 v98, v100, v101\n"
"v_cvt_pk_bf16_f32 v99, v102, v103\n"
"v_mov_b32_dpp v154, v154 row_ror:12 row_mask:0x2 bank_mask:0xf\n"
"s_nop 1\n"
"v_mov_b32_dpp v154, v154 row_ror:8 row_mask:0x4 bank_mask:0xf\n"
"s_nop 1\n"
"v_mov_b32_dpp v154, v154 row_ror:4 row_mask:0x8 bank_mask:0xf\n"
"s_nop 1\n"
"v_mul_f32_dpp v0, v154, v0 row_newbcast:0 row_mask:0xf bank_mask:0xf\n"
"v_mul_f32_dpp v1, v154, v1 row_newbcast:1 row_mask:0xf bank_mask:0xf\n"
"v_mul_f32_dpp v2, v154, v2 row_newbcast:2 row_mask:0xf bank_mask:0xf\n"
"v_mul_f32_dpp v3, v154, v3 row_newbcast:3 row_mask:0xf bank_mask:0xf\n"
"v_mul_f32_dpp v4, v154, v4 row_newbcast:0 row_mask:0xf bank_mask:0xf\n"
"v_mul_f32_dpp v5, v154, v5 row_newbcast:1 row_mask:0xf bank_mask:0xf\n"
"v_mul_f32_dpp v6, v154, v6 row_newbcast:2 row_mask:0xf bank_mask:0xf\n"
"v_mul_f32_dpp v7, v154, v7 row_newbcast:3 row_mask:0xf bank_mask:0xf\n"
"v_mul_f32_dpp v8, v154, v8 row_newbcast:0 row_mask:0xf bank_mask:0xf\n"
"v_mul_f32_dpp v9, v154, v9 row_newbcast:1 row_mask:0xf bank_mask:0xf\n"
"v_mul_f32_dpp v10, v154, v10 row_newbcast:2 row_mask:0xf bank_mask:0xf\n"
"v_mul_f32_dpp v11, v154, v11 row_newbcast:3 row_mask:0xf bank_mask:0xf\n"
"v_mul_f32_dpp v12, v154, v12 row_newbcast:0 row_mask:0xf bank_mask:0xf\n"
"v_mul_f32_dpp v13, v154, v13 row_newbcast:1 row_mask:0xf bank_mask:0xf\n"
"v_mul_f32_dpp v14, v154, v14 row_newbcast:2 row_mask:0xf bank_mask:0xf\n"
"v_mul_f32_dpp v15, v154, v15 row_newbcast:3 row_mask:0xf bank_mask:0xf\n"
"v_mul_f32_dpp v16, v154, v16 row_newbcast:0 row_mask:0xf bank_mask:0xf\n"
"v_mul_f32_dpp v17, v154, v17 row_newbcast:1 row_mask:0xf bank_mask:0xf\n"
"v_mul_f32_dpp v18, v154, v18 row_newbcast:2 row_mask:0xf bank_mask:0xf\n"
"v_mul_f32_dpp v19, v154, v19 row_newbcast:3 row_mask:0xf bank_mask:0xf\n"
"v_mul_f32_dpp v20, v154, v20 row_newbcast:0 row_mask:0xf bank_mask:0xf\n"
"v_mul_f32_dpp v21, v154, v21 row_newbcast:1 row_mask:0xf bank_mask:0xf\n"
"v_mul_f32_dpp v22, v154, v22 row_newbcast:2 row_mask:0xf bank_mask:0xf\n"
"v_mul_f32_dpp v23, v154, v23 row_newbcast:3 row_mask:0xf bank_mask:0xf\n"
"v_mul_f32_dpp v24, v154, v24 row_newbcast:0 row_mask:0xf bank_mask:0xf\n"
"v_mul_f32_dpp v25, v154, v25 row_newbcast:1 row_mask:0xf bank_mask:0xf\n"
"v_mul_f32_dpp v26, v154, v26 row_newbcast:2 row_mask:0xf bank_mask:0xf\n"
"v_mul_f32_dpp v27, v154, v27 row_newbcast:3 row_mask:0xf bank_mask:0xf\n"
"v_mul_f32_dpp v28, v154, v28 row_newbcast:0 row_mask:0xf bank_mask:0xf\n"
"v_mul_f32_dpp v29, v154, v29 row_newbcast:1 row_mask:0xf bank_mask:0xf\n"
"v_mul_f32_dpp v30, v154, v30 row_newbcast:2 row_mask:0xf bank_mask:0xf\n"
"v_mul_f32_dpp v31, v154, v31 row_newbcast:3 row_mask:0xf bank_mask:0xf\n"
"v_max3_f32 v157, v104, v105, v106\n"
"v_max3_f32 v157, v157, v107, v108\n"
"v_max3_f32 v157, v157, v109, v110\n"
"v_max3_f32 v157, v157, v111, v156\n"
"v_mov_b32 v158, v157\n"
"s_nop 1\n"
"v_permlane32_swap_b32 v157, v158\n"
"v_max_f32 v157, v157, v158\n"
"v_mov_b32 v158, v157\n"
"s_nop 1\n"
"v_permlane16_swap_b32 v157, v158\n"
"v_max_f32 v157, v157, v158\n"
"v_sub_f32 v158, v156, v157\n"
"v_exp_f32 v158, v158\n"
"v_mov_b32 v156, v157\n"
"v_sub_f32 v104, v104, v156\n"
"v_sub_f32 v105, v105, v156\n"
"v_sub_f32 v106, v106, v156\n"
"v_sub_f32 v107, v107, v156\n"
"v_sub_f32 v108, v108, v156\n"
"v_sub_f32 v109, v109, v156\n"
"v_sub_f32 v110, v110, v156\n"
"v_sub_f32 v111, v111, v156\n"
"v_exp_f32 v104, v104\n"
"v_exp_f32 v105, v105\n"
"v_exp_f32 v106, v106\n"
"v_exp_f32 v107, v107\n"
"v_exp_f32 v108, v108\n"
"v_exp_f32 v109, v109\n"
"v_exp_f32 v110, v110\n"
"v_exp_f32 v111, v111\n"
"v_mul_f32 v159, v159, v158\n"
"v_add_f32 v159, v159, v104\n"
"v_add_f32 v159, v159, v105\n"
"v_add_f32 v159, v159, v106\n"
"v_add_f32 v159, v159, v107\n"
"v_add_f32 v159, v159, v108\n"
"v_add_f32 v159, v159, v109\n"
"v_add_f32 v159, v159, v110\n"
"v_add_f32 v159, v159, v111\n"
"v_cvt_pk_bf16_f32 v104, v104, v105\n"
"v_cvt_pk_bf16_f32 v105, v106, v107\n"
"v_cvt_pk_bf16_f32 v106, v108, v109\n"
"v_cvt_pk_bf16_f32 v107, v110, v111\n"
"v_mov_b32_dpp v158, v158 row_ror:12 row_mask:0x2 bank_mask:0xf\n"
"s_nop 1\n"
"v_mov_b32_dpp v158, v158 row_ror:8 row_mask:0x4 bank_mask:0xf\n"
"s_nop 1\n"
"v_mov_b32_dpp v158, v158 row_ror:4 row_mask:0x8 bank_mask:0xf\n"
"s_nop 1\n"
"v_mul_f32_dpp v32, v158, v32 row_newbcast:0 row_mask:0xf bank_mask:0xf\n"
"v_mul_f32_dpp v33, v158, v33 row_newbcast:1 row_mask:0xf bank_mask:0xf\n"
"v_mul_f32_dpp v34, v158, v34 row_newbcast:2 row_mask:0xf bank_mask:0xf\n"
"v_mul_f32_dpp v35, v158, v35 row_newbcast:3 row_mask:0xf bank_mask:0xf\n"
"v_mul_f32_dpp v36, v158, v36 row_newbcast:0 row_mask:0xf bank_mask:0xf\n"
"v_mul_f32_dpp v37, v158, v37 row_newbcast:1 row_mask:0xf bank_mask:0xf\n"
"v_mul_f32_dpp v38, v158, v38 row_newbcast:2 row_mask:0xf bank_mask:0xf\n"
"v_mul_f32_dpp v39, v158, v39 row_newbcast:3 row_mask:0xf bank_mask:0xf\n"
"v_mul_f32_dpp v40, v158, v40 row_newbcast:0 row_mask:0xf bank_mask:0xf\n"
"v_mul_f32_dpp v41, v158, v41 row_newbcast:1 row_mask:0xf bank_mask:0xf\n"
"v_mul_f32_dpp v42, v158, v42 row_newbcast:2 row_mask:0xf bank_mask:0xf\n"
"v_mul_f32_dpp v43, v158, v43 row_newbcast:3 row_mask:0xf bank_mask:0xf\n"
"v_mul_f32_dpp v44, v158, v44 row_newbcast:0 row_mask:0xf bank_mask:0xf\n"
"v_mul_f32_dpp v45, v158, v45 row_newbcast:1 row_mask:0xf bank_mask:0xf\n"
"v_mul_f32_dpp v46, v158, v46 row_newbcast:2 row_mask:0xf bank_mask:0xf\n"
"v_mul_f32_dpp v47, v158, v47 row_newbcast:3 row_mask:0xf bank_mask:0xf\n"
"v_mul_f32_dpp v48, v158, v48 row_newbcast:0 row_mask:0xf bank_mask:0xf\n"
"v_mul_f32_dpp v49, v158, v49 row_newbcast:1 row_mask:0xf bank_mask:0xf\n"
"v_mul_f32_dpp v50, v158, v50 row_newbcast:2 row_mask:0xf bank_mask:0xf\n"
"v_mul_f32_dpp v51, v158, v51 row_newbcast:3 row_mask:0xf bank_mask:0xf\n"
"v_mul_f32_dpp v52, v158, v52 row_newbcast:0 row_mask:0xf bank_mask:0xf\n"
"v_mul_f32_dpp v53, v158, v53 row_newbcast:1 row_mask:0xf bank_mask:0xf\n"
"v_mul_f32_dpp v54, v158, v54 row_newbcast:2 row_mask:0xf bank_mask:0xf\n"
"v_mul_f32_dpp v55, v158, v55 row_newbcast:3 row_mask:0xf bank_mask:0xf\n"
"v_mul_f32_dpp v56, v158, v56 row_newbcast:0 row_mask:0xf bank_mask:0xf\n"
"v_mul_f32_dpp v57, v158, v57 row_newbcast:1 row_mask:0xf bank_mask:0xf\n"
"v_mul_f32_dpp v58, v158, v58 row_newbcast:2 row_mask:0xf bank_mask:0xf\n"
"v_mul_f32_dpp v59, v158, v59 row_newbcast:3 row_mask:0xf bank_mask:0xf\n"
"v_mul_f32_dpp v60, v158, v60 row_newbcast:0 row_mask:0xf bank_mask:0xf\n"
"v_mul_f32_dpp v61, v158, v61 row_newbcast:1 row_mask:0xf bank_mask:0xf\n"
"v_mul_f32_dpp v62, v158, v62 row_newbcast:2 row_mask:0xf bank_mask:0xf\n"
"v_mul_f32_dpp v63, v158, v63 row_newbcast:3 row_mask:0xf bank_mask:0xf\n"
"v_max3_f32 v161, v112, v113, v114\n"
"v_max3_f32 v161, v161, v115, v116\n"
"v_max3_f32 v161, v161, v117, v118\n"
"v_max3_f32 v161, v161, v119, v160\n"
"v_mov_b32 v162, v161\n"
"s_nop 1\n"
"v_permlane32_swap_b32 v161, v162\n"
"v_max_f32 v161, v161, v162\n"
"v_mov_b32 v162, v161\n"
"s_nop 1\n"
"v_permlane16_swap_b32 v161, v162\n"
"v_max_f32 v161, v161, v162\n"
"v_sub_f32 v162, v160, v161\n"
"v_exp_f32 v162, v162\n"
"v_mov_b32 v160, v161\n"
"v_sub_f32 v112, v112, v160\n"
"v_sub_f32 v113, v113, v160\n"
"v_sub_f32 v114, v114, v160\n"
"v_sub_f32 v115, v115, v160\n"
"v_sub_f32 v116, v116, v160\n"
"v_sub_f32 v117, v117, v160\n"
"v_sub_f32 v118, v118, v160\n"
"v_sub_f32 v119, v119, v160\n"
"v_exp_f32 v112, v112\n"
"v_exp_f32 v113, v113\n"
"v_exp_f32 v114, v114\n"
"v_exp_f32 v115, v115\n"
"v_exp_f32 v116, v116\n"
"v_exp_f32 v117, v117\n"
"v_exp_f32 v118, v118\n"
"v_exp_f32 v119, v119\n"
"v_mul_f32 v163, v163, v162\n"
"v_add_f32 v163, v163, v112\n"
"v_add_f32 v163, v163, v113\n"
"v_add_f32 v163, v163, v114\n"
"v_add_f32 v163, v163, v115\n"
"v_add_f32 v163, v163, v116\n"
"v_add_f32 v163, v163, v117\n"
"v_add_f32 v163, v163, v118\n"
"v_add_f32 v163, v163, v119\n"
"v_cvt_pk_bf16_f32 v112, v112, v113\n"
"v_cvt_pk_bf16_f32 v113, v114, v115\n"
"v_cvt_pk_bf16_f32 v114, v116, v117\n"
"v_cvt_pk_bf16_f32 v115, v118, v119\n"
"v_mov_b32_dpp v162, v162 row_ror:12 row_mask:0x2 bank_mask:0xf\n"
"s_nop 1\n"
"v_mov_b32_dpp v162, v162 row_ror:8 row_mask:0x4 bank_mask:0xf\n"
"s_nop 1\n"
"v_mov_b32_dpp v162, v162 row_ror:4 row_mask:0x8 bank_mask:0xf\n"
"s_nop 1\n"
"v_mul_f32_dpp v64, v162, v64 row_newbcast:0 row_mask:0xf bank_mask:0xf\n"
"v_mul_f32_dpp v65, v162, v65 row_newbcast:1 row_mask:0xf bank_mask:0xf\n"
"v_mul_f32_dpp v66, v162, v66 row_newbcast:2 row_mask:0xf bank_mask:0xf\n"
"v_mul_f32_dpp v67, v162, v67 row_newbcast:3 row_mask:0xf bank_mask:0xf\n"
"v_mul_f32_dpp v68, v162, v68 row_newbcast:0 row_mask:0xf bank_mask:0xf\n"
"v_mul_f32_dpp v69, v162, v69 row_newbcast:1 row_mask:0xf bank_mask:0xf\n"
"v_mul_f32_dpp v70, v162, v70 row_newbcast:2 row_mask:0xf bank_mask:0xf\n"
"v_mul_f32_dpp v71, v162, v71 row_newbcast:3 row_mask:0xf bank_mask:0xf\n"
"v_mul_f32_dpp v72, v162, v72 row_newbcast:0 row_mask:0xf bank_mask:0xf\n"
"v_mul_f32_dpp v73, v162, v73 row_newbcast:1 row_mask:0xf bank_mask:0xf\n"
"v_mul_f32_dpp v74, v162, v74 row_newbcast:2 row_mask:0xf bank_mask:0xf\n"
"v_mul_f32_dpp v75, v162, v75 row_newbcast:3 row_mask:0xf bank_mask:0xf\n"
"v_mul_f32_dpp v76, v162, v76 row_newbcast:0 row_mask:0xf bank_mask:0xf\n"
"v_mul_f32_dpp v77, v162, v77 row_newbcast:1 row_mask:0xf bank_mask:0xf\n"
"v_mul_f32_dpp v78, v162, v78 row_newbcast:2 row_mask:0xf bank_mask:0xf\n"
"v_mul_f32_dpp v79, v162, v79 row_newbcast:3 row_mask:0xf bank_mask:0xf\n"
"v_mul_f32_dpp v80, v162, v80 row_newbcast:0 row_mask:0xf bank_mask:0xf\n"
"v_mul_f32_dpp v81, v162, v81 row_newbcast:1 row_mask:0xf bank_mask:0xf\n"
"v_mul_f32_dpp v82, v162, v82 row_newbcast:2 row_mask:0xf bank_mask:0xf\n"
"v_mul_f32_dpp v83, v162, v83 row_newbcast:3 row_mask:0xf bank_mask:0xf\n"
"v_mul_f32_dpp v84, v162, v84 row_newbcast:0 row_mask:0xf bank_mask:0xf\n"
"v_mul_f32_dpp v85, v162, v85 row_newbcast:1 row_mask:0xf bank_mask:0xf\n"
"v_mul_f32_dpp v86, v162, v86 row_newbcast:2 row_mask:0xf bank_mask:0xf\n"
"v_mul_f32_dpp v87, v162, v87 row_newbcast:3 row_mask:0xf bank_mask:0xf\n"
"v_mul_f32_dpp v88, v162, v88 row_newbcast:0 row_mask:0xf bank_mask:0xf\n"
"v_mul_f32_dpp v89, v162, v89 row_newbcast:1 row_mask:0xf bank_mask:0xf\n"
"v_mul_f32_dpp v90, v162, v90 row_newbcast:2 row_mask:0xf bank_mask:0xf\n"
"v_mul_f32_dpp v91, v162, v91 row_newbcast:3 row_mask:0xf bank_mask:0xf\n"
"v_mul_f32_dpp v92, v162, v92 row_newbcast:0 row_mask:0xf bank_mask:0xf\n"
"v_mul_f32_dpp v93, v162, v93 row_newbcast:1 row_mask:0xf bank_mask:0xf\n"
"v_mul_f32_dpp v94, v162, v94 row_newbcast:2 row_mask:0xf bank_mask:0xf\n"
"v_mul_f32_dpp v95, v162, v95 row_newbcast:3 row_mask:0xf bank_mask:0xf\n"
"s_cmp_lg_u32 s83, s43\n"
"s_cbranch_scc1 LSK3A_%=\n"
"v_max3_f32 v165, v120, v121, v122\n"
"v_max3_f32 v165, v165, v123, v124\n"
"v_max3_f32 v165, v165, v125, v126\n"
"v_max3_f32 v165, v165, v127, v164\n"
"v_mov_b32 v166, v165\n"
"s_nop 1\n"
"v_permlane32_swap_b32 v165, v166\n"
"v_max_f32 v165, v165, v166\n"
"v_mov_b32 v166, v165\n"
"s_nop 1\n"
"v_permlane16_swap_b32 v165, v166\n"
"v_max_f32 v165, v165, v166\n"
"v_sub_f32 v166, v164, v165\n"
"v_exp_f32 v166, v166\n"
"v_mov_b32 v164, v165\n"
"v_sub_f32 v120, v120, v164\n"
"v_sub_f32 v121, v121, v164\n"
"v_sub_f32 v122, v122, v164\n"
"v_sub_f32 v123, v123, v164\n"
"v_sub_f32 v124, v124, v164\n"
"v_sub_f32 v125, v125, v164\n"
"v_sub_f32 v126, v126, v164\n"
"v_sub_f32 v127, v127, v164\n"
"v_exp_f32 v120, v120\n"
"v_exp_f32 v121, v121\n"
"v_exp_f32 v122, v122\n"
"v_exp_f32 v123, v123\n"
"v_exp_f32 v124, v124\n"
"v_exp_f32 v125, v125\n"
"v_exp_f32 v126, v126\n"
"v_exp_f32 v127, v127\n"
"v_mul_f32 v167, v167, v166\n"
"v_add_f32 v167, v167, v120\n"
"v_add_f32 v167, v167, v121\n"
"v_add_f32 v167, v167, v122\n"
"v_add_f32 v167, v167, v123\n"
"v_add_f32 v167, v167, v124\n"
"v_add_f32 v167, v167, v125\n"
"v_add_f32 v167, v167, v126\n"
"v_add_f32 v167, v167, v127\n"
"v_cvt_pk_bf16_f32 v120, v120, v121\n"
"v_cvt_pk_bf16_f32 v121, v122, v123\n"
"v_cvt_pk_bf16_f32 v122, v124, v125\n"
"v_cvt_pk_bf16_f32 v123, v126, v127\n"
"v_mov_b32_dpp v166, v166 row_ror:12 row_mask:0x2 bank_mask:0xf\n"
"s_nop 1\n"
"v_mov_b32_dpp v166, v166 row_ror:8 row_mask:0x4 bank_mask:0xf\n"
"s_nop 1\n"
"v_mov_b32_dpp v166, v166 row_ror:4 row_mask:0x8 bank_mask:0xf\n"
"v_accvgpr_read_b32 v124, a208\n"
"s_nop 1\n"
"v_mul_f32_dpp v124, v166, v124 row_newbcast:0 row_mask:0xf bank_mask:0xf\n"
"v_accvgpr_write_b32 a208, v124\n"
"v_accvgpr_read_b32 v125, a209\n"
"s_nop 1\n"
"v_mul_f32_dpp v125, v166, v125 row_newbcast:1 row_mask:0xf bank_mask:0xf\n"
"v_accvgpr_write_b32 a209, v125\n"
"v_accvgpr_read_b32 v126, a210\n"
"s_nop 1\n"
"v_mul_f32_dpp v126, v166, v126 row_newbcast:2 row_mask:0xf bank_mask:0xf\n"
"v_accvgpr_write_b32 a210, v126\n"
"v_accvgpr_read_b32 v127, a211\n"
"s_nop 1\n"
"v_mul_f32_dpp v127, v166, v127 row_newbcast:3 row_mask:0xf bank_mask:0xf\n"
"v_accvgpr_write_b32 a211, v127\n"
"v_accvgpr_read_b32 v124, a212\n"
"s_nop 1\n"
"v_mul_f32_dpp v124, v166, v124 row_newbcast:0 row_mask:0xf bank_mask:0xf\n"
"v_accvgpr_write_b32 a212, v124\n"
"v_accvgpr_read_b32 v125, a213\n"
"s_nop 1\n"
"v_mul_f32_dpp v125, v166, v125 row_newbcast:1 row_mask:0xf bank_mask:0xf\n"
"v_accvgpr_write_b32 a213, v125\n"
"v_accvgpr_read_b32 v126, a214\n"
"s_nop 1\n"
"v_mul_f32_dpp v126, v166, v126 row_newbcast:2 row_mask:0xf bank_mask:0xf\n"
"v_accvgpr_write_b32 a214, v126\n"
"v_accvgpr_read_b32 v127, a215\n"
"s_nop 1\n"
"v_mul_f32_dpp v127, v166, v127 row_newbcast:3 row_mask:0xf bank_mask:0xf\n"
"v_accvgpr_write_b32 a215, v127\n"
"v_accvgpr_read_b32 v124, a216\n"
"s_nop 1\n"
"v_mul_f32_dpp v124, v166, v124 row_newbcast:0 row_mask:0xf bank_mask:0xf\n"
"v_accvgpr_write_b32 a216, v124\n"
"v_accvgpr_read_b32 v125, a217\n"
"s_nop 1\n"
"v_mul_f32_dpp v125, v166, v125 row_newbcast:1 row_mask:0xf bank_mask:0xf\n"
"v_accvgpr_write_b32 a217, v125\n"
"v_accvgpr_read_b32 v126, a218\n"
"s_nop 1\n"
"v_mul_f32_dpp v126, v166, v126 row_newbcast:2 row_mask:0xf bank_mask:0xf\n"
"v_accvgpr_write_b32 a218, v126\n"
"v_accvgpr_read_b32 v127, a219\n"
"s_nop 1\n"
"v_mul_f32_dpp v127, v166, v127 row_newbcast:3 row_mask:0xf bank_mask:0xf\n"
"v_accvgpr_write_b32 a219, v127\n"
"v_accvgpr_read_b32 v124, a220\n"
"s_nop 1\n"
"v_mul_f32_dpp v124, v166, v124 row_newbcast:0 row_mask:0xf bank_mask:0xf\n"
"v_accvgpr_write_b32 a220, v124\n"
"v_accvgpr_read_b32 v125, a221\n"
"s_nop 1\n"
"v_mul_f32_dpp v125, v166, v125 row_newbcast:1 row_mask:0xf bank_mask:0xf\n"
"v_accvgpr_write_b32 a221, v125\n"
"v_accvgpr_read_b32 v126, a222\n"
"s_nop 1\n"
"v_mul_f32_dpp v126, v166, v126 row_newbcast:2 row_mask:0xf bank_mask:0xf\n"
"v_accvgpr_write_b32 a222, v126\n"
"v_accvgpr_read_b32 v127, a223\n"
"s_nop 1\n"
"v_mul_f32_dpp v127, v166, v127 row_newbcast:3 row_mask:0xf bank_mask:0xf\n"
"v_accvgpr_write_b32 a223, v127\n"
"v_accvgpr_read_b32 v124, a224\n"
"s_nop 1\n"
"v_mul_f32_dpp v124, v166, v124 row_newbcast:0 row_mask:0xf bank_mask:0xf\n"
"v_accvgpr_write_b32 a224, v124\n"
"v_accvgpr_read_b32 v125, a225\n"
"s_nop 1\n"
"v_mul_f32_dpp v125, v166, v125 row_newbcast:1 row_mask:0xf bank_mask:0xf\n"
"v_accvgpr_write_b32 a225, v125\n"
"v_accvgpr_read_b32 v126, a226\n"
"s_nop 1\n"
"v_mul_f32_dpp v126, v166, v126 row_newbcast:2 row_mask:0xf bank_mask:0xf\n"
"v_accvgpr_write_b32 a226, v126\n"
"v_accvgpr_read_b32 v127, a227\n"
"s_nop 1\n"
"v_mul_f32_dpp v127, v166, v127 row_newbcast:3 row_mask:0xf bank_mask:0xf\n"
"v_accvgpr_write_b32 a227, v127\n"
"v_accvgpr_read_b32 v124, a228\n"
"s_nop 1\n"
"v_mul_f32_dpp v124, v166, v124 row_newbcast:0 row_mask:0xf bank_mask:0xf\n"
"v_accvgpr_write_b32 a228, v124\n"
"v_accvgpr_read_b32 v125, a229\n"
"s_nop 1\n"
"v_mul_f32_dpp v125, v166, v125 row_newbcast:1 row_mask:0xf bank_mask:0xf\n"
"v_accvgpr_write_b32 a229, v125\n"
"v_accvgpr_read_b32 v126, a230\n"
"s_nop 1\n"
"v_mul_f32_dpp v126, v166, v126 row_newbcast:2 row_mask:0xf bank_mask:0xf\n"
"v_accvgpr_write_b32 a230, v126\n"
"v_accvgpr_read_b32 v127, a231\n"
"s_nop 1\n"
"v_mul_f32_dpp v127, v166, v127 row_newbcast:3 row_mask:0xf bank_mask:0xf\n"
"v_accvgpr_write_b32 a231, v127\n"
"v_accvgpr_read_b32 v124, a232\n"
"s_nop 1\n"
"v_mul_f32_dpp v124, v166, v124 row_newbcast:0 row_mask:0xf bank_mask:0xf\n"
"v_accvgpr_write_b32 a232, v124\n"
"v_accvgpr_read_b32 v125, a233\n"
"s_nop 1\n"
"v_mul_f32_dpp v125, v166, v125 row_newbcast:1 row_mask:0xf bank_mask:0xf\n"
"v_accvgpr_write_b32 a233, v125\n"
"v_accvgpr_read_b32 v126, a234\n"
"s_nop 1\n"
"v_mul_f32_dpp v126, v166, v126 row_newbcast:2 row_mask:0xf bank_mask:0xf\n"
"v_accvgpr_write_b32 a234, v126\n"
"v_accvgpr_read_b32 v127, a235\n"
"s_nop 1\n"
"v_mul_f32_dpp v127, v166, v127 row_newbcast:3 row_mask:0xf bank_mask:0xf\n"
"v_accvgpr_write_b32 a235, v127\n"
"v_accvgpr_read_b32 v124, a236\n"
"s_nop 1\n"
"v_mul_f32_dpp v124, v166, v124 row_newbcast:0 row_mask:0xf bank_mask:0xf\n"
"v_accvgpr_write_b32 a236, v124\n"
"v_accvgpr_read_b32 v125, a237\n"
"s_nop 1\n"
"v_mul_f32_dpp v125, v166, v125 row_newbcast:1 row_mask:0xf bank_mask:0xf\n"
"v_accvgpr_write_b32 a237, v125\n"
"v_accvgpr_read_b32 v126, a238\n"
"s_nop 1\n"
"v_mul_f32_dpp v126, v166, v126 row_newbcast:2 row_mask:0xf bank_mask:0xf\n"
"v_accvgpr_write_b32 a238, v126\n"
"v_accvgpr_read_b32 v127, a239\n"
"s_nop 1\n"
"v_mul_f32_dpp v127, v166, v127 row_newbcast:3 row_mask:0xf bank_mask:0xf\n"
"v_accvgpr_write_b32 a239, v127\n"
"LSK3A_%=:\n"
"s_nop 3\n"
"buffer_load_ushort v128, v146, s[88:91], s80 offen offset:0\n"
"buffer_load_ushort v129, v146, s[88:91], s80 offen offset:256\n"
"buffer_load_ushort v130, v146, s[88:91], s80 offen offset:512\n"
"buffer_load_ushort v131, v146, s[88:91], s80 offen offset:768\n"
"buffer_load_ushort v132, v146, s[88:91], s81 offen offset:0\n"
"buffer_load_ushort v133, v146, s[88:91], s81 offen offset:256\n"
"buffer_load_ushort v134, v146, s[88:91], s81 offen offset:512\n"
"buffer_load_ushort v135, v146, s[88:91], s81 offen offset:768\n"
"buffer_load_ushort v136, v146, s[88:91], s80 offen offset:32\n"
"buffer_load_ushort v137, v146, s[88:91], s80 offen offset:288\n"
"buffer_load_ushort v138, v146, s[88:91], s80 offen offset:544\n"
"buffer_load_ushort v139, v146, s[88:91], s80 offen offset:800\n"
"buffer_load_ushort v140, v146, s[88:91], s81 offen offset:32\n"
"buffer_load_ushort v141, v146, s[88:91], s81 offen offset:288\n"
"buffer_load_ushort v142, v146, s[88:91], s81 offen offset:544\n"
"buffer_load_ushort v143, v146, s[88:91], s81 offen offset:800\n"
"s_waitcnt vmcnt(0)\n"
"v_lshl_or_b32 v128, v129, 16, v128\n"
"v_lshl_or_b32 v129, v131, 16, v130\n"
"v_lshl_or_b32 v130, v133, 16, v132\n"
"v_lshl_or_b32 v131, v135, 16, v134\n"
"v_lshl_or_b32 v136, v137, 16, v136\n"
"v_lshl_or_b32 v137, v139, 16, v138\n"
"v_lshl_or_b32 v138, v141, 16, v140\n"
"v_lshl_or_b32 v139, v143, 16, v142\n"
"v_mfma_f32_16x16x32_bf16 v[0:3], v[96:99], v[128:131], v[0:3]\n"
"s_nop 0\n"
"v_mfma_f32_16x16x32_bf16 v[4:7], v[96:99], v[136:139], v[4:7]\n"
"v_mfma_f32_16x16x32_bf16 v[32:35], v[104:107], v[128:131], v[32:35]\n"
"v_mfma_f32_16x16x32_bf16 v[36:39], v[104:107], v[136:139], v[36:39]\n"
"v_mfma_f32_16x16x32_bf16 v[64:67], v[112:115], v[128:131], v[64:67]\n"
"v_mfma_f32_16x16x32_bf16 v[68:71], v[112:115], v[136:139], v[68:71]\n"
"s_cmp_lg_u32 s83, s43\n"
"s_cbranch_scc1 LSK3B0_%=\n"
"v_mfma_f32_16x16x32_bf16 a[208:211], v[120:123], v[128:131], a[208:211]\n"
"v_mfma_f32_16x16x32_bf16 a[212:215], v[120:123], v[136:139], a[212:215]\n"
"LSK3B0_%=:\n"
"s_nop 3\n"
"buffer_load_ushort v128, v147, s[88:91], s80 offen offset:0\n"
"buffer_load_ushort v129, v147, s[88:91], s80 offen offset:256\n"
"buffer_load_ushort v130, v147, s[88:91], s80 offen offset:512\n"
"buffer_load_ushort v131, v147, s[88:91], s80 offen offset:768\n"
"buffer_load_ushort v132, v147, s[88:91], s81 offen offset:0\n"
"buffer_load_ushort v133, v147, s[88:91], s81 offen offset:256\n"
"buffer_load_ushort v134, v147, s[88:91], s81 offen offset:512\n"
"buffer_load_ushort v135, v147, s[88:91], s81 offen offset:768\n"
"buffer_load_ushort v136, v147, s[88:91], s80 offen offset:32\n"
"buffer_load_ushort v137, v147, s[88:91], s80 offen offset:288\n"
"buffer_load_ushort v138, v147, s[88:91], s80 offen offset:544\n"
"buffer_load_ushort v139, v147, s[88:91], s80 offen offset:800\n"
"buffer_load_ushort v140, v147, s[88:91], s81 offen offset:32\n"
"buffer_load_ushort v141, v147, s[88:91], s81 offen offset:288\n"
"buffer_load_ushort v142, v147, s[88:91], s81 offen offset:544\n"
"buffer_load_ushort v143, v147, s[88:91], s81 offen offset:800\n"
"s_waitcnt vmcnt(0)\n"
"v_lshl_or_b32 v128, v129, 16, v128\n"
"v_lshl_or_b32 v129, v131, 16, v130\n"
"v_lshl_or_b32 v130, v133, 16, v132\n"
"v_lshl_or_b32 v131, v135, 16, v134\n"
"v_lshl_or_b32 v136, v137, 16, v136\n"
"v_lshl_or_b32 v137, v139, 16, v138\n"
"v_lshl_or_b32 v138, v141, 16, v140\n"
"v_lshl_or_b32 v139, v143, 16, v142\n"
"v_mfma_f32_16x16x32_bf16 v[8:11], v[96:99], v[128:131], v[8:11]\n"
"s_nop 0\n"
"v_mfma_f32_16x16x32_bf16 v[12:15], v[96:99], v[136:139], v[12:15]\n"
"v_mfma_f32_16x16x32_bf16 v[40:43], v[104:107], v[128:131], v[40:43]\n"
"v_mfma_f32_16x16x32_bf16 v[44:47], v[104:107], v[136:139], v[44:47]\n"
"v_mfma_f32_16x16x32_bf16 v[72:75], v[112:115], v[128:131], v[72:75]\n"
"v_mfma_f32_16x16x32_bf16 v[76:79], v[112:115], v[136:139], v[76:79]\n"
"s_cmp_lg_u32 s83, s43\n"
"s_cbranch_scc1 LSK3B1_%=\n"
"v_mfma_f32_16x16x32_bf16 a[216:219], v[120:123], v[128:131], a[216:219]\n"
"v_mfma_f32_16x16x32_bf16 a[220:223], v[120:123], v[136:139], a[220:223]\n"
"LSK3B1_%=:\n"
"s_nop 3\n"
"buffer_load_ushort v128, v146, s[88:91], s80 offen offset:128\n"
"buffer_load_ushort v129, v146, s[88:91], s80 offen offset:384\n"
"buffer_load_ushort v130, v146, s[88:91], s80 offen offset:640\n"
"buffer_load_ushort v131, v146, s[88:91], s80 offen offset:896\n"
"buffer_load_ushort v132, v146, s[88:91], s81 offen offset:128\n"
"buffer_load_ushort v133, v146, s[88:91], s81 offen offset:384\n"
"buffer_load_ushort v134, v146, s[88:91], s81 offen offset:640\n"
"buffer_load_ushort v135, v146, s[88:91], s81 offen offset:896\n"
"buffer_load_ushort v136, v146, s[88:91], s80 offen offset:160\n"
"buffer_load_ushort v137, v146, s[88:91], s80 offen offset:416\n"
"buffer_load_ushort v138, v146, s[88:91], s80 offen offset:672\n"
"buffer_load_ushort v139, v146, s[88:91], s80 offen offset:928\n"
"buffer_load_ushort v140, v146, s[88:91], s81 offen offset:160\n"
"buffer_load_ushort v141, v146, s[88:91], s81 offen offset:416\n"
"buffer_load_ushort v142, v146, s[88:91], s81 offen offset:672\n"
"buffer_load_ushort v143, v146, s[88:91], s81 offen offset:928\n"
"s_waitcnt vmcnt(0)\n"
"v_lshl_or_b32 v128, v129, 16, v128\n"
"v_lshl_or_b32 v129, v131, 16, v130\n"
"v_lshl_or_b32 v130, v133, 16, v132\n"
"v_lshl_or_b32 v131, v135, 16, v134\n"
"v_lshl_or_b32 v136, v137, 16, v136\n"
"v_lshl_or_b32 v137, v139, 16, v138\n"
"v_lshl_or_b32 v138, v141, 16, v140\n"
"v_lshl_or_b32 v139, v143, 16, v142\n"
"v_mfma_f32_16x16x32_bf16 v[16:19], v[96:99], v[128:131], v[16:19]\n"
"s_nop 0\n"
"v_mfma_f32_16x16x32_bf16 v[20:23], v[96:99], v[136:139], v[20:23]\n"
"v_mfma_f32_16x16x32_bf16 v[48:51], v[104:107], v[128:131], v[48:51]\n"
"v_mfma_f32_16x16x32_bf16 v[52:55], v[104:107], v[136:139], v[52:55]\n"
"v_mfma_f32_16x16x32_bf16 v[80:83], v[112:115], v[128:131], v[80:83]\n"
"v_mfma_f32_16x16x32_bf16 v[84:87], v[112:115], v[136:139], v[84:87]\n"
"s_cmp_lg_u32 s83, s43\n"
"s_cbranch_scc1 LSK3B2_%=\n"
"v_mfma_f32_16x16x32_bf16 a[224:227], v[120:123], v[128:131], a[224:227]\n"
"v_mfma_f32_16x16x32_bf16 a[228:231], v[120:123], v[136:139], a[228:231]\n"
"LSK3B2_%=:\n"
"s_nop 3\n"
"buffer_load_ushort v128, v147, s[88:91], s80 offen offset:128\n"
"buffer_load_ushort v129, v147, s[88:91], s80 offen offset:384\n"
"buffer_load_ushort v130, v147, s[88:91], s80 offen offset:640\n"
"buffer_load_ushort v131, v147, s[88:91], s80 offen offset:896\n"
"buffer_load_ushort v132, v147, s[88:91], s81 offen offset:128\n"
"buffer_load_ushort v133, v147, s[88:91], s81 offen offset:384\n"
"buffer_load_ushort v134, v147, s[88:91], s81 offen offset:640\n"
"buffer_load_ushort v135, v147, s[88:91], s81 offen offset:896\n"
"buffer_load_ushort v136, v147, s[88:91], s80 offen offset:160\n"
"buffer_load_ushort v137, v147, s[88:91], s80 offen offset:416\n"
"buffer_load_ushort v138, v147, s[88:91], s80 offen offset:672\n"
"buffer_load_ushort v139, v147, s[88:91], s80 offen offset:928\n"
"buffer_load_ushort v140, v147, s[88:91], s81 offen offset:160\n"
"buffer_load_ushort v141, v147, s[88:91], s81 offen offset:416\n"
"buffer_load_ushort v142, v147, s[88:91], s81 offen offset:672\n"
"buffer_load_ushort v143, v147, s[88:91], s81 offen offset:928\n"
"s_waitcnt vmcnt(0)\n"
"v_lshl_or_b32 v128, v129, 16, v128\n"
"v_lshl_or_b32 v129, v131, 16, v130\n"
"v_lshl_or_b32 v130, v133, 16, v132\n"
"v_lshl_or_b32 v131, v135, 16, v134\n"
"v_lshl_or_b32 v136, v137, 16, v136\n"
"v_lshl_or_b32 v137, v139, 16, v138\n"
"v_lshl_or_b32 v138, v141, 16, v140\n"
"v_lshl_or_b32 v139, v143, 16, v142\n"
"v_mfma_f32_16x16x32_bf16 v[24:27], v[96:99], v[128:131], v[24:27]\n"
"s_nop 0\n"
"v_mfma_f32_16x16x32_bf16 v[28:31], v[96:99], v[136:139], v[28:31]\n"
"v_mfma_f32_16x16x32_bf16 v[56:59], v[104:107], v[128:131], v[56:59]\n"
"v_mfma_f32_16x16x32_bf16 v[60:63], v[104:107], v[136:139], v[60:63]\n"
"v_mfma_f32_16x16x32_bf16 v[88:91], v[112:115], v[128:131], v[88:91]\n"
"v_mfma_f32_16x16x32_bf16 v[92:95], v[112:115], v[136:139], v[92:95]\n"
"s_cmp_lg_u32 s83, s43\n"
"s_cbranch_scc1 LSK3B3_%=\n"
"v_mfma_f32_16x16x32_bf16 a[232:235], v[120:123], v[128:131], a[232:235]\n"
"v_mfma_f32_16x16x32_bf16 a[236:239], v[120:123], v[136:139], a[236:239]\n"
"LSK3B3_%=:\n"
"s_nop 3\n"
"s_nop 9\n"
"s_add_u32 s80, s80, 8192\n"
"s_add_u32 s81, s81, 8192\n"
"s_add_u32 s82, s82, 1\n"
"s_cmp_lt_u32 s82, 32\n"
"s_cbranch_scc1 LSLOOP_%=\n"
"v_mov_b32 v154, v155\n"
"s_nop 1\n"
"v_permlane32_swap_b32 v155, v154\n"
"v_add_f32 v155, v155, v154\n"
"v_mov_b32 v154, v155\n"
"s_nop 1\n"
"v_permlane16_swap_b32 v155, v154\n"
"v_add_f32 v155, v155, v154\n"
"v_mov_b32 v196, v155\n"
"v_mov_b32 v158, v159\n"
"s_nop 1\n"
"v_permlane32_swap_b32 v159, v158\n"
"v_add_f32 v159, v159, v158\n"
"v_mov_b32 v158, v159\n"
"s_nop 1\n"
"v_permlane16_swap_b32 v159, v158\n"
"v_add_f32 v159, v159, v158\n"
"v_mov_b32 v198, v159\n"
"v_mov_b32 v162, v163\n"
"s_nop 1\n"
"v_permlane32_swap_b32 v163, v162\n"
"v_add_f32 v163, v163, v162\n"
"v_mov_b32 v162, v163\n"
"s_nop 1\n"
"v_permlane16_swap_b32 v163, v162\n"
"v_add_f32 v163, v163, v162\n"
"v_mov_b32 v200, v163\n"
"v_mov_b32 v166, v167\n"
"s_nop 1\n"
"v_permlane32_swap_b32 v167, v166\n"
"v_add_f32 v167, v167, v166\n"
"v_mov_b32 v166, v167\n"
"s_nop 1\n"
"v_permlane16_swap_b32 v167, v166\n"
"v_add_f32 v167, v167, v166\n"
"v_mov_b32 v202, v167\n"
"v_sub_f32 v192, 0, v164\n"
"v_mov_b32 v96, 0\n"
"v_mov_b32 v97, 0\n"
"v_mov_b32 v98, 0\n"
"v_mov_b32 v99, 0\n"
"v_mov_b32 v100, 0\n"
"v_mov_b32 v101, 0\n"
"v_mov_b32 v102, 0\n"
"v_mov_b32 v103, 0\n"
"v_mov_b32 v104, 0\n"
"v_mov_b32 v105, 0\n"
"v_mov_b32 v106, 0\n"
"v_mov_b32 v107, 0\n"
"v_mov_b32 v108, 0\n"
"v_mov_b32 v109, 0\n"
"v_mov_b32 v110, 0\n"
"v_mov_b32 v111, 0\n"
"v_mov_b32 v112, 0\n"
"v_mov_b32 v113, 0\n"
"v_mov_b32 v114, 0\n"
"v_mov_b32 v115, 0\n"
"v_mov_b32 v116, 0\n"
"v_mov_b32 v117, 0\n"
"v_mov_b32 v118, 0\n"
"v_mov_b32 v119, 0\n"
"v_mov_b32 v120, 0\n"
"v_mov_b32 v121, 0\n"
"v_mov_b32 v122, 0\n"
"v_mov_b32 v123, 0\n"
"v_mov_b32 v204, 0\n"
"v_mov_b32 v205, 0\n"
"s_nop 3\n"
"s_mov_b32 s68, 0\n"
"s_cmp_eq_u32 s47, 4\n"
"s_cbranch_scc1 LBACK0_%=\n"
"s_cmp_eq_u32 s47, 8\n"
"s_cbranch_scc1 LBACK1_%=\n"
"s_cmp_eq_u32 s47, 12\n"
"s_cbranch_scc1 LBACK2_%=\n"
"s_branch LBACK3_%=\n"
"LFIN_%=:\n"
    :: [kalo]"s"((unsigned)ka), [kahi]"s"((unsigned)(ka >> 32)), [wg]"s"(blockIdx.x), [wid]"s"(wid)
    : "v0","v1","v2","v3","v4","v5","v6","v7","v8","v9","v10","v11","v12","v13","v14","v15","v16","v17","v18","v19","v20","v21","v22","v23","v24","v25","v26","v27","v28","v29","v30","v31","v32","v33","v34","v35","v36","v37","v38","v39","v40","v41","v42","v43","v44","v45","v46","v47","v48","v49","v50","v51","v52","v53","v54","v55","v56","v57","v58","v59","v60","v61","v62","v63","v64","v65","v66","v67","v68","v69","v70","v71","v72","v73","v74","v75","v76","v77","v78","v79","v80","v81","v82","v83","v84","v85","v86","v87","v88","v89","v90","v91","v92","v93","v94","v95","v96","v97","v98","v99","v100","v101","v102","v103","v104","v105","v106","v107","v108","v109","v110","v111","v112","v113","v114","v115","v116","v117","v118","v119","v120","v121","v122","v123","v124","v125","v126","v127","v128","v129","v130","v131","v132","v133","v134","v135","v136","v137","v138","v139","v140","v141","v142","v143","v144","v145","v146","v147","v148","v149","v150","v151","v152","v153","v154","v155","v156","v157","v158","v159","v160","v161","v162","v163","v164","v165","v166","v167","v168","v169","v170","v171","v172","v173","v174","v175","v176","v177","v178","v179","v180","v181","v182","v183","v184","v185","v186","v187","v188","v189","v190","v191","v192","v193","v194","v195","v196","v197","v198","v199","v200","v201","v202","v203","v204","v205","v206","v207","v208","v209","v210","v211","v212","v213","v214","v215","v216","v217","v218","v219","v220","v221","v222","v223","v224","v225","v226","v227","v228","v229","v230","v231","v232","v233","v234","v235","v236","v237","v238","v239","v240","v241","v242","v243","v244","v245","v246","v247","v248","v249","v250","v251","v252","v253","v254","v255","a0","a1","a2","a3","a4","a5","a6","a7","a8","a9","a10","a11","a12","a13","a14","a15","a16","a17","a18","a19","a20","a21","a22","a23","a24","a25","a26","a27","a28","a29","a30","a31","a32","a33","a34","a35","a36","a37","a38","a39","a40","a41","a42","a43","a44","a45","a46","a47","a48","a49","a50","a51","a52","a53","a54","a55","a56","a57","a58","a59","a60","a61","a62","a63","a64","a65","a66","a67","a68","a69","a70","a71","a72","a73","a74","a75","a76","a77","a78","a79","a80","a81","a82","a83","a84","a85","a86","a87","a88","a89","a90","a91","a92","a93","a94","a95","a96","a97","a98","a99","a100","a101","a102","a103","a104","a105","a106","a107","a108","a109","a110","a111","a112","a113","a114","a115","a116","a117","a118","a119","a120","a121","a122","a123","a124","a125","a126","a127","a128","a129","a130","a131","a132","a133","a134","a135","a136","a137","a138","a139","a140","a141","a142","a143","a144","a145","a146","a147","a148","a149","a150","a151","a152","a153","a154","a155","a156","a157","a158","a159","a160","a161","a162","a163","a164","a165","a166","a167","a168","a169","a170","a171","a172","a173","a174","a175","a176","a177","a178","a179","a180","a181","a182","a183","a184","a185","a186","a187","a188","a189","a190","a191","a192","a193","a194","a195","a196","a197","a198","a199","a200","a201","a202","a203","a204","a205","a206","a207","a208","a209","a210","a211","a212","a213","a214","a215","a216","a217","a218","a219","a220","a221","a222","a223","a224","a225","a226","a227","a228","a229","a230","a231","a232","a233","a234","a235","a236","a237","a238","a239","a240","a241","a242","a243","a244","a245","a246","a247","a248","a249","a250","a251","a252","a253","a254","a255","s6","s7","s8","s9","s10","s11","s12","s13","s14","s15","s16","s17","s18","s19","s20","s21","s22","s23","s24","s25","s26","s27","s28","s29","s30","s31","s32","s33","s34","s35","s36","s37","s38","s39","s40","s41","s42","s43","s44","s45","s46","s47","s48","s49","s50","s51","s52","s53","s54","s55","s56","s57","s58","s59","s60","s61","s62","s63","s64","s65","s66","s67","s68","s69","s70","s71","s72","s73","s74","s75","s76","s77","s78","s79","s80","s81","s82","s83","s84","s85","s86","s87","s88","s89","s90","s91","s92","s93","s94","s95","s96","s97","s98","s99","s100","vcc","scc","m0","memory");
}
extern "C" void kernel_launch(void* const* d_in, const int* in_sizes, int n_in, void* d_out, int out_size, void* d_ws, size_t ws_size, hipStream_t stream) {
  char* xch = (char*)d_ws + ((ws_size - (size_t)270532608) & ~(size_t)0xfffff);
  (void)hipFuncSetAttribute((const void*)fused_attn, hipFuncAttributeMaxDynamicSharedMemorySize, 163840);
  hipLaunchKernelGGL(fused_attn, dim3(256), dim3(256), 163840, stream,
                     (const char*)d_in[0], (const char*)d_in[1], (const char*)d_in[2], (const char*)d_in[3], (const char*)d_in[4],
                     (const char*)d_in[5], (const char*)d_in[6], (const char*)d_in[7], (const char*)d_in[8], (const char*)d_in[9], xch, (char*)d_out, (char*)d_ws);
}
```

```cpp
#include <hip/hip_runtime.h>
#include <hip/hip_bf16.h>
#include <stdint.h>

__global__ __launch_bounds__(256, 1)
void fused_attn(const char* xA, const char* kA, const char* vA, const char* wA, const char* uA,
                const char* xB, const char* kB, const char* vB, const char* wB, const char* uB, char* xch, char* out, char* flags) {
  extern __shared__ __attribute__((aligned(16))) char lds[];
  const unsigned wid = __builtin_amdgcn_readfirstlane(threadIdx.x >> 6);
  const unsigned long long ka = (unsigned long long)__builtin_amdgcn_kernarg_segment_ptr();
  asm volatile(
"s_memtime s[64:65]\n"
"s_memrealtime s[66:67]\n"
"s_bfe_u32 s10, %[wg], 0x10002\n"
"s_mul_i32 s40, s10, 40\n"
"s_add_u32 s40, s40, %[kalo]\n"
"s_addc_u32 s41, %[kahi], 0\n"
"s_mov_b32 s78, %[kalo]\n"
"s_mov_b32 s79, %[kahi]\n"
"s_load_dwordx2 s[24:25], s[40:41], 0\n"
"s_load_dwordx2 s[28:29], s[40:41], 24\n"
"s_load_dwordx2 s[6:7], s[78:79], 0x60\n"
"s_load_dwordx2 s[16:17], s[40:41], 8\n"
"s_load_dwordx2 s[20:21], s[40:41], 16\n"
"s_load_dwordx2 s[8:9], s[40:41], 32\n"
"s_load_dwordx4 s[12:15], s[78:79], 0x50\n"
"s_mov_b32 s42, %[wid]\n"
"s_and_b32 s11, %[wg], 1\n"
"s_lshl_b32 s11, s11, 3\n"
"s_lshr_b32 s60, %[wg], 5\n"
"s_add_u32 s11, s11, s60\n"
"s_bfe_u32 s61, %[wg], 0x10001\n"
"s_bfe_u32 s60, %[wg], 0x20003\n"
"s_lshl2_add_u32 s61, s61, s60\n"
"s_getpc_b64 s[80:81]\n"
"LCP_%=:\n"
"s_mov_b32 s82, LEND_%=-LCP_%=\n"
"s_mov_b32 s83, 0x20000\n"
"v_mbcnt_lo_u32_b32 v2, -1, 0\n"
"v_lshlrev_b32 v2, 7, v2\n"
"s_lshr_b32 s60, %[wg], 3\n"
"s_lshl2_add_u32 s60, s60, s42\n"
"s_mul_i32 s60, s60, 640\n"
"s_mov_b64 exec, 31\n"
"buffer_load_dword v3, v2, s[80:83], s60 offen\n"
"s_mov_b64 exec, -1\n"
"s_mov_b32 s19, 0x20000\n"
"s_mov_b32 s23, 0x20000\n"
"s_mov_b32 s27, 0x20000\n"
"s_mov_b32 s31, 0x20000\n"
"s_mov_b32 s35, 0x20000\n"
"s_mov_b32 s39, 0x20000\n"
"s_mov_b32 s26, 286720\n"
"s_lshl_b32 s44, s42, 12\n"
"s_mov_b32 s50, s44\n"
"s_mul_i32 s51, s42, 7168\n"
"s_add_u32 s51, s51, 0x10000\n"
"s_mov_b32 s74, 0x8000\n"
"s_mov_b32 s58, 524288\n"
"s_mov_b32 s47, 0\n"
"s_add_u32 s60, s11, s61\n"
"s_and_b32 s60, s60, 0\n"
"s_mul_i32 s60, s60, 3\n"
"s_lshl_b32 s49, s60, 7\n"
"s_lshl_b32 s48, s60, 19\n"
"v_mbcnt_lo_u32_b32 v4, -1, 0\n"
"v_mbcnt_hi_u32_b32 v4, -1, v4\n"
"v_and_b32 v5, 15, v4\n"
"v_lshrrev_b32 v6, 4, v4\n"
"v_lshrrev_b32 v7, 3, v4\n"
"v_and_b32 v8, 7, v4\n"
"v_lshlrev_b32 v9, 1, v6\n"
"v_xor_b32 v9, v9, v8\n"
"v_lshlrev_b32 v9, 4, v9\n"
"s_mul_i32 s60, s42, 71680\n"
"v_mul_u32_u24 v10, 1280, v7\n"
"v_add3_u32 v248, v10, v9, s60\n"
"v_add_u32 v249, 9216, v248\n"
"v_add_u32 v254, 18432, v248\n"
"v_add_u32 v255, 27648, v248\n"
"v_lshl_add_u32 v240, v7, 8, v9\n"
"v_add_u32 v240, s44, v240\n"
"v_and_b32 v10, 1, v6\n"
"v_xor_b32 v10, v10, v8\n"
"v_lshlrev_b32 v10, 4, v10\n"
"v_lshl_add_u32 v10, v7, 13, v10\n"
"s_lshl_b32 s60, s42, 17\n"
"v_add_u32 v10, s60, v10\n"
"v_add_u32 v224, 1024, v10\n"
"v_add_u32 v225, 128, v10\n"
"v_xor_b32 v10, 64, v10\n"
"v_add_u32 v226, 64512, v10\n"
"v_add_u32 v227, 63616, v10\n"
"s_mul_i32 s40, s11, 286720\n"
"s_lshl_b32 s41, s61, 10\n"
"s_sub_u32 s30, 5243904, s41\n"
"s_sub_u32 s41, s41, 1024\n"
"s_ashr_i32 s60, s41, 31\n"
"s_lshl4_add_u32 s92, s10, s11\n"
"s_lshl_b32 s93, s92, 9\n"
"v_mov_b32 v0, 0\n"
"s_waitcnt lgkmcnt(0)\n"
"s_add_u32 s24, s24, s40\n"
"s_addc_u32 s25, s25, 0\n"
"s_and_b32 s25, s25, 0xffff\n"
"s_add_u32 s28, s28, s41\n"
"s_addc_u32 s29, s29, s60\n"
"s_and_b32 s29, s29, 0xffff\n"
"s_add_u32 s6, s6, s93\n"
"s_addc_u32 s7, s7, 0\n"
"buffer_load_dword v12, v248, s[24:27], s49 offen\n"
"buffer_load_dword v13, v224, s[28:31], s48 offen\n"
"global_load_dword v1, v0, s[6:7] offset:128 sc1\n"
"s_mov_b32 m0, s51\n"
"s_nop 0\n"
"buffer_load_dwordx4 v248, s[24:27], s49 offen lds\n"
"buffer_load_dwordx4 v249, s[24:27], s49 offen offset:1024 lds\n"
"buffer_load_dwordx4 v254, s[24:27], s49 offen offset:2048 lds\n"
"buffer_load_dwordx4 v255, s[24:27], s49 offen offset:3072 lds\n"
"s_add_u32 m0, s51, 4096\n"
"s_add_u32 s40, s49, 40960\n"
"buffer_load_dwordx4 v248, s[24:27], s40 offen lds\n"
"buffer_load_dwordx4 v249, s[24:27], s40 offen offset:1024 lds\n"
"buffer_load_dwordx4 v254, s[24:27], s40 offen offset:2048 lds\n"
"s_add_u32 s49, s49, 128\n"
"s_sub_u32 s41, s49, 1280\n"
"s_min_u32 s49, s49, s41\n"
"s_xor_b32 s51, s51, s74\n"
"s_mov_b32 m0, s50\n"
"s_nop 0\n"
"buffer_load_dwordx4 v224, s[28:31], s48 offen lds\n"
"buffer_load_dwordx4 v225, s[28:31], s48 offen offset:1024 lds\n"
"buffer_load_dwordx4 v226, s[28:31], s48 offen offset:2048 lds\n"
"buffer_load_dwordx4 v227, s[28:31], s48 offen offset:3072 lds\n"
"s_xor_b32 s50, s50, s74\n"
"s_lshl2_add_u32 s63, s11, s42\n"
"s_mul_i32 s63, s63, 10240\n"
"s_and_b32 s78, s42, 1\n"
"s_lshl_b32 s43, s78, 6\n"
"s_mov_b32 s77, 0x80000000\n"
"s_mov_b32 s75, 0x10000\n"
"s_mov_b32 s73, 0xffff0000\n"
"s_mov_b32 s46, 0x3e0293ee\n"
"s_mov_b32 s45, 0x63800000\n"
"s_mov_b32 s62, 9\n"
"v_lshl_add_u32 v242, v4, 4, s44\n"
"s_lshr_b32 s79, s42, 1\n"
"s_lshl4_add_u32 s76, s79, s78\n"
"s_lshl_b32 s76, s76, 10\n"
"s_add_u32 s76, s76, 0x20000\n"
"s_sub_u32 s76, s76, s44\n"
"v_lshlrev_b32 v9, 6, v6\n"
"v_xor_b32 v9, v9, v242\n"
"v_add_u32 v243, s76, v9\n"
"v_and_b32 v9, 6, v5\n"
"s_xor_b32 m0, s50, 0xc000\n"
"s_add_u32 s40, s48, 256\n"
"buffer_load_dwordx4 v224, s[28:31], s40 offen lds\n"
"v_xor_b32 v9, v9, v6\n"
"v_lshlrev_b32 v9, 4, v9\n"
"v_lshl_add_u32 v10, v5, 7, v9\n"
"s_mul_i32 s79, s42, 6144\n"
"s_add_u32 s79, s79, 0x10000\n"
"v_add_u32 v250, s79, v10\n"
"v_xor_b32 v251, 64, v250\n"
"s_lshr_b32 s79, s42, 1\n"
"s_lshl_b32 s79, s79, 11\n"
"s_add_u32 s79, s79, 90112\n"
"v_add_u32 v252, s79, v10\n"
"v_xor_b32 v253, 64, v252\n"
"s_mul_i32 s79, s78, 8256\n"
"v_lshrrev_b32 v9, 5, v4\n"
"buffer_load_dwordx4 v225, s[28:31], s40 offen offset:1024 lds\n"
"v_lshlrev_b32 v9, 11, v9\n"
"v_bfe_u32 v10, v4, 2, 3\n"
"v_lshl_add_u32 v9, v10, 7, v9\n"
"v_and_b32 v10, 3, v4\n"
"v_lshl_add_u32 v9, v10, 3, v9\n"
"v_bfe_u32 v10, v4, 3, 2\n"
"v_lshl_add_u32 v9, v10, 5, v9\n"
"s_add_u32 s79, s79, 0x10000\n"
"v_xor_b32 v232, s79, v9\n"
"v_xor_b32 v233, 32, v232\n"
"v_xor_b32 v234, 64, v232\n"
"v_xor_b32 v235, 96, v232\n"
"v_xor_b32 v236, 0x2000, v232\n"
"v_xor_b32 v237, 0x2000, v233\n"
"buffer_load_dwordx4 v226, s[28:31], s40 offen offset:2048 lds\n"
"v_xor_b32 v238, 0x2000, v234\n"
"v_xor_b32 v239, 0x2000, v235\n"
"v_lshlrev_b32 v9, 11, v6\n"
"v_bfe_u32 v10, v4, 2, 2\n"
"v_lshl_add_u32 v9, v10, 7, v9\n"
"v_and_b32 v10, 1, v4\n"
"v_lshl_add_u32 v9, v10, 3, v9\n"
"v_bfe_u32 v10, v4, 1, 1\n"
"v_lshl_add_u32 v9, v10, 5, v9\n"
"v_bfe_u32 v10, v4, 3, 1\n"
"v_lshl_add_u32 v9, v10, 4, v9\n"
"v_bfe_u32 v10, v4, 4, 1\n"
"v_lshl_add_u32 v9, v10, 6, v9\n"
"v_xor_b32 v244, s43, v9\n"
"buffer_load_dwordx4 v227, s[28:31], s40 offen offset:3072 lds\n"
"s_cmp_eq_u32 s47, 8\n"
"s_cselect_b32 s41, 524800, s58\n"
"s_add_u32 s48, s48, s41\n"
"s_sub_u32 s41, s48, 5242880\n"
"s_min_u32 s48, s48, s41\n"
"v_xor_b32 v245, 16, v244\n"
"v_xor_b32 v246, 64, v244\n"
"v_xor_b32 v247, 0x50, v244\n"
"s_lshl3_add_u32 s79, s11, s61\n"
"s_lshl_b32 s79, s79, 20\n"
"s_sub_u32 s79, s79, 896\n"
"s_ashr_i32 s60, s79, 31\n"
"s_add_u32 s16, s16, s79\n"
"s_addc_u32 s17, s17, s60\n"
"s_and_b32 s17, s17, 0xffff\n"
"s_mov_b32 s18, 1049472\n"
"s_mov_b64 s[84:85], s[16:17]\n"
"s_mov_b64 s[86:87], s[18:19]\n"
"s_add_u32 s20, s20, s79\n"
"s_addc_u32 s21, s21, s60\n"
"s_mov_b32 m0, s50\n"
"s_nop 0\n"
"buffer_load_dwordx4 v224, s[28:31], s48 offen lds\n"
"s_and_b32 s21, s21, 0xffff\n"
"s_mov_b32 s22, 1049472\n"
"s_mov_b64 s[88:89], s[20:21]\n"
"s_mov_b64 s[90:91], s[22:23]\n"
"s_lshr_b32 s57, %[wg], 3\n"
"s_mul_i32 s57, s57, 5\n"
"s_and_b32 s57, s57, 15\n"
"s_lshl_b32 s57, s57, 14\n"
"s_mov_b32 s55, 896\n"
"s_add_u32 s56, s57, 896\n"
"s_mov_b32 s59, 16384\n"
"s_mov_b32 s52, -896\n"
"s_mov_b32 s53, 0\n"
"buffer_load_dwordx4 v225, s[28:31], s48 offen offset:1024 lds\n"
"s_mov_b32 s54, -896\n"
"s_mul_i32 s79, s61, 655360\n"
"s_add_u32 s8, s8, s79\n"
"s_addc_u32 s9, s9, 0\n"
"s_and_b32 s9, s9, 0xffff\n"
"s_add_u32 s12, s12, 0x1fffff\n"
"s_addc_u32 s13, s13, 0\n"
"s_and_b32 s12, s12, 0xffe00000\n"
"s_mul_i32 s96, s42, 3\n"
"s_and_b32 s96, s96, 5\n"
"s_cmp_eq_u32 s42, 3\n"
"s_cselect_b32 s96, 5, s96\n"
"s_lshl_b32 s79, s61, 8\n"
"s_add_u32 s96, s96, s79\n"
"buffer_load_dwordx4 v226, s[28:31], s48 offen offset:2048 lds\n"
"s_add_u32 s96, s96, 160\n"
"s_lshl_b32 s96, s96, 12\n"
"s_mul_i32 s79, s92, 8388608\n"
"s_add_u32 s32, s12, s79\n"
"s_addc_u32 s33, s13, 0\n"
"s_and_b32 s33, s33, 0xffff\n"
"s_mov_b32 s34, 8388608\n"
"s_mul_i32 s79, s11, 573440\n"
"s_mul_i32 s60, s10, 1280\n"
"s_sub_u32 s38, 573440, s60\n"
"s_add_u32 s79, s79, s60\n"
"s_add_u32 s36, s14, s79\n"
"s_addc_u32 s37, s15, 0\n"
"s_and_b32 s37, s37, 0xffff\n"
"buffer_load_dwordx4 v227, s[28:31], s48 offen offset:3072 lds\n"
"s_xor_b32 s50, s50, s74\n"
"s_lshl2_add_u32 s93, s61, s42\n"
"s_add_u32 s95, s61, 1\n"
"s_and_b32 s95, s95, 7\n"
"s_lshl_b32 s95, s95, 7\n"
"s_add_u32 s100, s61, 2\n"
"s_and_b32 s100, s100, 7\n"
"s_lshl_b32 s100, s100, 7\n"
"s_lshl_b32 s60, s61, 7\n"
"s_add_u32 s60, s60, s43\n"
"s_mul_i32 s97, s42, 122880\n"
"s_add_u32 s97, s97, s60\n"
"s_lshr_b32 s98, s42, 1\n"
"s_add_u32 s98, s98, 12\n"
"s_mul_i32 s98, s98, 40960\n"
"s_mov_b32 m0, s51\n"
"s_nop 0\n"
"buffer_load_dwordx4 v248, s[24:27], s49 offen lds\n"
"s_add_u32 s98, s98, s60\n"
"s_mul_i32 s60, s93, 37\n"
"s_lshr_b32 s60, s60, 8\n"
"s_mul_i32 s79, s60, 7\n"
"s_sub_u32 s79, s93, s79\n"
"s_lshl4_add_u32 s94, s60, s79\n"
"s_lshl_b32 s94, s94, 10\n"
"s_add_u32 s94, s94, 524288\n"
"s_mul_i32 s99, s60, 3\n"
"s_lshr_b32 s78, s79, 1\n"
"s_add_u32 s99, s99, s78\n"
"s_lshr_b32 s78, s60, 1\n"
"s_add_u32 s78, s78, 12\n"
"s_cmp_eq_u32 s79, 6\n"
"s_cselect_b32 s99, s78, s99\n"
"buffer_load_dwordx4 v249, s[24:27], s49 offen offset:1024 lds\n"
"s_mul_i32 s99, s99, 40960\n"
"s_xor_b32 s79, s79, s60\n"
"s_and_b32 s79, s79, 1\n"
"s_lshl_b32 s79, s79, 6\n"
"s_add_u32 s99, s99, s79\n"
"s_add_u32 s99, s99, 1024\n"
"s_cmp_eq_u32 s61, 7\n"
"s_cselect_b32 s99, 0x40000000, s99\n"
"s_cselect_b32 s94, 0x40000000, s94\n"
"s_waitcnt vmcnt(22)\n"
"v_and_b32 v12, 0x7fff0000, v12\n"
"v_and_b32 v13, 0x7fff0000, v13\n"
"s_nop 0\n"
"buffer_load_dwordx4 v254, s[24:27], s49 offen offset:2048 lds\n"
"v_max_u32_dpp v12, v12, v12 quad_perm:[1,0,3,2] row_mask:0xf bank_mask:0xf\n"
"v_max_u32_dpp v13, v13, v13 quad_perm:[1,0,3,2] row_mask:0xf bank_mask:0xf\n"
"s_nop 0\n"
"v_max_u32_dpp v12, v12, v12 quad_perm:[2,3,0,1] row_mask:0xf bank_mask:0xf\n"
"v_max_u32_dpp v13, v13, v13 quad_perm:[2,3,0,1] row_mask:0xf bank_mask:0xf\n"
"s_nop 0\n"
"v_max_u32_dpp v12, v12, v12 row_half_mirror row_mask:0xf bank_mask:0xf\n"
"v_max_u32_dpp v13, v13, v13 row_half_mirror row_mask:0xf bank_mask:0xf\n"
"s_nop 0\n"
"v_min_u32_dpp v12, v12, v12 row_mirror row_mask:0xf bank_mask:0xf\n"
"v_min_u32_dpp v13, v13, v13 row_mirror row_mask:0xf bank_mask:0xf\n"
"s_nop 0\n"
"v_min_u32_dpp v12, v12, v12 row_bcast:15 row_mask:0xa bank_mask:0xf\n"
"v_min_u32_dpp v13, v13, v13 row_bcast:15 row_mask:0xa bank_mask:0xf\n"
"buffer_load_dwordx4 v255, s[24:27], s49 offen offset:3072 lds\n"
"s_nop 0\n"
"v_min_u32_dpp v12, v12, v12 row_bcast:31 row_mask:0xc bank_mask:0xf\n"
"v_min_u32_dpp v13, v13, v13 row_bcast:31 row_mask:0xc bank_mask:0xf\n"
"s_nop 0\n"
"v_mul_f32 v12, v12, v13\n"
"v_mul_f32 v12, 0x43c80000, v12\n"
"v_min_f32 v12, 0x71800000, v12\n"
"v_mul_f32 v13, s46, v12\n"
"s_nop 0\n"
"v_readlane_b32 s70, v12, 63\n"
"v_readlane_b32 s71, v12, 63\n"
"v_readlane_b32 s69, v13, 63\n"
"s_nop 1\n"
"v_mov_b64 v[228:229], s[70:71]\n"
"s_add_u32 m0, s51, 4096\n"
"s_add_u32 s40, s49, 40960\n"
"buffer_load_dwordx4 v248, s[24:27], s40 offen lds\n"
"v_mov_b64 v[230:231], s[70:71]\n"
"v_mov_b64 v[4:5], s[70:71]\n"
"v_mov_b64 v[6:7], s[70:71]\n"
"v_mov_b64 v[8:9], s[70:71]\n"
"v_mov_b64 v[10:11], s[70:71]\n"
"v_mov_b64 v[12:13], s[70:71]\n"
"v_mov_b64 v[14:15], s[70:71]\n"
"v_mov_b64 v[16:17], s[70:71]\n"
"v_mov_b64 v[18:19], s[70:71]\n"
"v_mov_b64 v[20:21], s[70:71]\n"
"v_mov_b64 v[22:23], s[70:71]\n"
"v_mov_b64 v[24:25], s[70:71]\n"
"v_mov_b64 v[26:27], s[70:71]\n"
"v_mov_b64 v[28:29], s[70:71]\n"
"buffer_load_dwordx4 v249, s[24:27], s40 offen offset:1024 lds\n"
"v_mov_b64 v[30:31], s[70:71]\n"
"v_mov_b64 v[32:33], s[70:71]\n"
"v_mov_b64 v[34:35], s[70:71]\n"
"v_mov_b64 v[36:37], s[70:71]\n"
"v_mov_b64 v[38:39], s[70:71]\n"
"v_mov_b64 v[40:41], s[70:71]\n"
"v_mov_b64 v[42:43], s[70:71]\n"
"v_mov_b64 v[44:45], s[70:71]\n"
"v_mov_b64 v[46:47], s[70:71]\n"
"v_mov_b64 v[48:49], s[70:71]\n"
"v_mov_b64 v[50:51], s[70:71]\n"
"v_mov_b64 v[52:53], s[70:71]\n"
"v_mov_b64 v[54:55], s[70:71]\n"
"v_mov_b64 v[56:57], s[70:71]\n"
"buffer_load_dwordx4 v254, s[24:27], s40 offen offset:2048 lds\n"
"s_add_u32 s49, s49, 128\n"
"s_sub_u32 s41, s49, 1280\n"
"s_min_u32 s49, s49, s41\n"
"s_xor_b32 s51, s51, s74\n"
"v_mov_b64 v[58:59], s[70:71]\n"
"v_mov_b64 v[60:61], s[70:71]\n"
"v_mov_b64 v[62:63], s[70:71]\n"
"v_mov_b64 v[64:65], s[70:71]\n"
"v_mov_b64 v[66:67], s[70:71]\n"
"v_mov_b64 v[68:69], s[70:71]\n"
"v_mov_b64 v[70:71], s[70:71]\n"
"v_mov_b64 v[72:73], s[70:71]\n"
"v_mov_b64 v[74:75], s[70:71]\n"
"v_mov_b64 v[76:77], s[70:71]\n"
"v_mov_b64 v[78:79], s[70:71]\n"
"v_mov_b64 v[80:81], s[70:71]\n"
"v_mov_b64 v[82:83], s[70:71]\n"
"v_mov_b64 v[84:85], s[70:71]\n"
"v_mov_b64 v[86:87], s[70:71]\n"
"v_mov_b64 v[88:89], s[70:71]\n"
"v_mov_b64 v[90:91], s[70:71]\n"
"v_mov_b64 v[92:93], s[70:71]\n"
"v_mov_b64 v[94:95], s[70:71]\n"
"v_mov_b64 v[96:97], s[70:71]\n"
"v_mov_b64 v[98:99], s[70:71]\n"
"v_mov_b64 v[100:101], s[70:71]\n"
"v_mov_b64 v[102:103], s[70:71]\n"
"v_mov_b64 v[104:105], s[70:71]\n"
"v_mov_b64 v[106:107], s[70:71]\n"
"v_mov_b64 v[108:109], s[70:71]\n"
"v_mov_b64 v[110:111], s[70:71]\n"
"v_mov_b64 v[112:113], s[70:71]\n"
"v_mov_b64 v[114:115], s[70:71]\n"
"v_mov_b64 v[116:117], s[70:71]\n"
"v_mov_b64 v[118:119], s[70:71]\n"
"v_mov_b64 v[120:121], s[70:71]\n"
"v_mov_b64 v[122:123], s[70:71]\n"
"v_mov_b64 v[124:125], s[70:71]\n"
"v_mov_b64 v[126:127], s[70:71]\n"
"v_mov_b64 v[128:129], s[70:71]\n"
"v_mov_b64 v[130:131], s[70:71]\n"
"v_mov_b64 v[132:133], s[70:71]\n"
"v_mov_b64 v[134:135], s[70:71]\n"
"v_mov_b64 v[136:137], s[70:71]\n"
"v_mov_b64 v[138:139], s[70:71]\n"
"v_mov_b64 v[140:141], s[70:71]\n"
"v_mov_b64 v[142:143], s[70:71]\n"
"v_mov_b64 v[144:145], s[70:71]\n"
"v_mov_b64 v[146:147], s[70:71]\n"
"v_mov_b64 v[148:149], s[70:71]\n"
"v_mov_b64 v[150:151], s[70:71]\n"
"v_mov_b64 v[152:153], s[70:71]\n"
"v_mov_b64 v[154:155], s[70:71]\n"
"v_mov_b64 v[156:157], s[70:71]\n"
"v_mov_b64 v[158:159], s[70:71]\n"
"v_mov_b64 v[160:161], s[70:71]\n"
"v_mov_b64 v[162:163], s[70:71]\n"
"v_mov_b64 v[164:165], s[70:71]\n"
"v_mov_b64 v[166:167], s[70:71]\n"
"v_mov_b64 v[168:169], s[70:71]\n"
"v_mov_b64 v[170:171], s[70:71]\n"
"v_mov_b64 v[172:173], s[70:71]\n"
"v_mov_b64 v[174:175], s[70:71]\n"
"s_memtime s[80:81]\n"
"s_memrealtime s[82:83]\n"
"v_mov_b64 v[176:177], s[70:71]\n"
"v_mov_b64 v[178:179], s[70:71]\n"
"v_mov_b64 v[180:181], s[70:71]\n"
"v_mov_b64 v[182:183], s[70:71]\n"
"v_mov_b64 v[184:185], s[70:71]\n"
"v_mov_b64 v[186:187], s[70:71]\n"
"v_mov_b64 v[188:189], s[70:71]\n"
"v_mov_b64 v[190:191], s[70:71]\n"
"v_mov_b64 v[192:193], s[70:71]\n"
"v_mov_b64 v[194:195], s[70:71]\n"
"v_mov_b64 v[196:197], s[70:71]\n"
"v_mov_b64 v[198:199], s[70:71]\n"
"v_mov_b64 v[200:201], s[70:71]\n"
"v_mov_b64 v[202:203], s[70:71]\n"
"v_mov_b64 v[204:205], s[70:71]\n"
"v_mov_b64 v[206:207], s[70:71]\n"
"v_mov_b64 v[208:209], s[70:71]\n"
"v_mov_b64 v[210:211], s[70:71]\n"
"v_mov_b64 v[212:213], s[70:71]\n"
"v_mov_b64 v[214:215], s[70:71]\n"
"v_mov_b64 v[216:217], s[70:71]\n"
"v_mov_b64 v[218:219], s[70:71]\n"
"v_mov_b64 v[220:221], s[70:71]\n"
"v_mov_b64 v[222:223], s[70:71]\n"
"s_waitcnt vmcnt(26)\n"
"v_readfirstlane_b32 s92, v1\n"
"v_mov_b64 v[0:1], s[70:71]\n"
"v_mov_b64 v[2:3], s[70:71]\n"
"s_mov_b32 s70, s46\n"
"s_xor_b32 s71, s69, s77\n"
"s_waitcnt lgkmcnt(0)\n"
"s_sub_u32 s80, s80, s64\n"
"s_sub_u32 s82, s82, s66\n"
"s_mul_i32 s80, s80, 10\n"
"s_mul_i32 s82, s82, 211\n"
"s_cmp_gt_u32 s80, s82\n"
"s_cselect_b32 s64, 1, 0\n"
"s_cselect_b32 s68, 655360, 0\n"
"s_cmp_eq_u32 s42, 0\n"
"s_cselect_b32 s64, s64, 0\n"
"s_mov_b32 s65, 0\n"
"s_add_u32 s66, s66, 2800\n"
"s_mov_b32 s67, s66\n"
"s_mov_b32 s72, 122\n"
"s_mov_b64 vcc, s[64:65]\n"
"s_nop 2\n"
"s_mul_i32 s60, s92, 0x9e3779b1\n"
"s_add_u32 s60, s60, 0x7f4a7c15\n"
"s_waitcnt vmcnt(15)\n"
"s_barrier\n"
"ds_read_b128 a[160:163], v250 offset:0\n"
"ds_read_b128 a[164:167], v251 offset:0\n"
"ds_read_b64_tr_b16 a[96:97], v244 offset:0\n"
"ds_read_b64_tr_b16 a[98:99], v244 offset:512\n"
"ds_read_b64_tr_b16 a[100:101], v244 offset:8192\n"
"ds_read_b64_tr_b16 a[102:103], v244 offset:8704\n"
"ds_read_b64_tr_b16 a[104:105], v245 offset:0\n"
"ds_read_b64_tr_b16 a[106:107], v245 offset:512\n"
"ds_read_b64_tr_b16 a[108:109], v245 offset:8192\n"
"ds_read_b64_tr_b16 a[110:111], v245 offset:8704\n"
"ds_read_b64_tr_b16 a[112:113], v246 offset:0\n"
"ds_read_b64_tr_b16 a[114:115], v246 offset:512\n"
"ds_read_b64_tr_b16 a[116:117], v246 offset:8192\n"
"ds_read_b64_tr_b16 a[118:119], v246 offset:8704\n"
"ds_read_b64_tr_b16 a[120:121], v247 offset:0\n"
"ds_read_b64_tr_b16 a[122:123], v247 offset:512\n"
"ds_read_b64_tr_b16 a[124:125], v247 offset:8192\n"
"ds_read_b64_tr_b16 a[126:127], v247 offset:8704\n"
"ds_read_b64_tr_b16 a[128:129], v244 offset:1024\n"
"ds_read_b64_tr_b16 a[130:131], v244 offset:1536\n"
"ds_read_b64_tr_b16 a[132:133], v244 offset:9216\n"
"ds_read_b64_tr_b16 a[134:135], v244 offset:9728\n"
"ds_read_b64_tr_b16 a[136:137], v245 offset:1024\n"
"ds_read_b64_tr_b16 a[138:139], v245 offset:1536\n"
"ds_read_b64_tr_b16 a[140:141], v245 offset:9216\n"
"ds_read_b64_tr_b16 a[142:143], v245 offset:9728\n"
"ds_read_b64_tr_b16 a[144:145], v246 offset:1024\n"
"ds_read_b64_tr_b16 a[146:147], v246 offset:1536\n"
"ds_read_b64_tr_b16 a[148:149], v246 offset:9216\n"
"ds_read_b64_tr_b16 a[150:151], v246 offset:9728\n"
"ds_read_b64_tr_b16 a[152:153], v247 offset:1024\n"
"ds_read_b64_tr_b16 a[154:155], v247 offset:1536\n"
"ds_read_b64_tr_b16 a[156:157], v247 offset:9216\n"
"ds_read_b64_tr_b16 a[158:159], v247 offset:9728\n"
"ds_read_b128 a[168:171], v250 offset:2048\n"
"ds_read_b128 a[172:175], v251 offset:2048\n"
"ds_read_b128 a[176:179], v250 offset:4096\n"
"ds_read_b128 a[180:183], v251 offset:4096\n"
"ds_read_b128 a[184:187], v252 offset:0\n"
"ds_read_b128 a[188:191], v253 offset:0\n"
"LPL_%=:\n"
"s_waitcnt lgkmcnt(15)\n"
"v_mfma_f32_16x16x32_bf16 v[112:115], a[96:99], a[160:163], v[112:115]\n"
"v_mfma_f32_16x16x32_bf16 v[112:115], a[100:103], a[164:167], v[112:115]\n"
"v_mfma_f32_16x16x32_bf16 v[116:119], a[104:107], a[160:163], v[116:119]\n"
"s_waitcnt vmcnt(11) lgkmcnt(5)\n"
"v_mfma_f32_16x16x32_bf16 v[116:119], a[108:111], a[164:167], v[116:119]\n"
"s_barrier\n"
"v_mfma_f32_16x16x32_bf16 v[120:123], a[112:115], a[160:163], v[120:123]\n"
"s_add_u32 s40, s48, 256\n"
"s_xor_b32 m0, s50, 0xc000\n"
"ds_read_b64_tr_b16 a[192:193], v244 offset:16384\n"
"v_mfma_f32_16x16x32_bf16 v[120:123], a[116:119], a[164:167], v[120:123]\n"
"buffer_load_dwordx4 v224, s[28:31], s40 offen lds\n"
"v_mfma_f32_16x16x32_bf16 v[124:127], a[120:123], a[160:163], v[124:127]\n"
"ds_read_b64_tr_b16 a[194:195], v244 offset:16896\n"
"v_mfma_f32_16x16x32_bf16 v[124:127], a[124:127], a[164:167], v[124:127]\n"
"ds_read_b64_tr_b16 a[196:197], v244 offset:24576\n"
"v_mfma_f32_16x16x32_bf16 v[128:131], a[128:131], a[160:163], v[128:131]\n"
"ds_read_b64_tr_b16 a[198:199], v244 offset:25088\n"
"v_mfma_f32_16x16x32_bf16 v[128:131], a[132:135], a[164:167], v[128:131]\n"
"ds_read_b64_tr_b16 a[200:201], v245 offset:16384\n"
"v_mfma_f32_16x16x32_bf16 v[132:135], a[136:139], a[160:163], v[132:135]\n"
"ds_read_b64_tr_b16 a[202:203], v245 offset:16896\n"
"v_mfma_f32_16x16x32_bf16 v[132:135], a[140:143], a[164:167], v[132:135]\n"
"buffer_load_dwordx4 v225, s[28:31], s40 offen offset:1024 lds\n"
"v_mfma_f32_16x16x32_bf16 v[136:139], a[144:147], a[160:163], v[136:139]\n"
"ds_read_b64_tr_b16 a[204:205], v245 offset:24576\n"
"v_mfma_f32_16x16x32_bf16 v[136:139], a[148:151], a[164:167], v[136:139]\n"
"ds_read_b64_tr_b16 a[206:207], v245 offset:25088\n"
"v_mfma_f32_16x16x32_bf16 v[140:143], a[152:155], a[160:163], v[140:143]\n"
"ds_read_b64_tr_b16 a[208:209], v246 offset:16384\n"
"v_mfma_f32_16x16x32_bf16 v[140:143], a[156:159], a[164:167], v[140:143]\n"
"s_waitcnt lgkmcnt(6)\n"
"ds_read_b64_tr_b16 a[210:211], v246 offset:16896\n"
"v_mfma_f32_16x16x32_bf16 v[172:175], a[152:155], a[168:171], v[172:175]\n"
"s_waitcnt lgkmcnt(6)\n"
"ds_read_b64_tr_b16 a[212:213], v246 offset:24576\n"
"v_mfma_f32_16x16x32_bf16 v[172:175], a[156:159], a[172:175], v[172:175]\n"
"buffer_load_dwordx4 v226, s[28:31], s40 offen offset:2048 lds\n"
"v_mfma_f32_16x16x32_bf16 v[168:171], a[144:147], a[168:171], v[168:171]\n"
"ds_read_b64_tr_b16 a[214:215], v246 offset:25088\n"
"v_mfma_f32_16x16x32_bf16 v[168:171], a[148:151], a[172:175], v[168:171]\n"
"ds_read_b64_tr_b16 a[216:217], v247 offset:16384\n"
"v_mfma_f32_16x16x32_bf16 v[164:167], a[136:139], a[168:171], v[164:167]\n"
"ds_read_b64_tr_b16 a[218:219], v247 offset:16896\n"
"v_mfma_f32_16x16x32_bf16 v[164:167], a[140:143], a[172:175], v[164:167]\n"
"ds_read_b64_tr_b16 a[220:221], v247 offset:24576\n"
"v_mfma_f32_16x16x32_bf16 v[160:163], a[128:131], a[168:171], v[160:163]\n"
"ds_read_b64_tr_b16 a[222:223], v247 offset:25088\n"
"v_mfma_f32_16x16x32_bf16 v[160:163], a[132:135], a[172:175], v[160:163]\n"
"buffer_load_dwordx4 v227, s[28:31], s40 offen offset:3072 lds\n"
"v_mfma_f32_16x16x32_bf16 v[156:159], a[120:123], a[168:171], v[156:159]\n"
"s_cmp_eq_u32 s47, 8\n"
"ds_read_b64_tr_b16 a[224:225], v244 offset:17408\n"
"v_mfma_f32_16x16x32_bf16 v[156:159], a[124:127], a[172:175], v[156:159]\n"
"s_cselect_b32 s41, 524800, s58\n"
"ds_read_b64_tr_b16 a[226:227], v244 offset:17920\n"
"v_mfma_f32_16x16x32_bf16 v[152:155], a[112:115], a[168:171], v[152:155]\n"
"s_add_u32 s48, s48, s41\n"
"ds_read_b64_tr_b16 a[228:229], v244 offset:25600\n"
"v_mfma_f32_16x16x32_bf16 v[152:155], a[116:119], a[172:175], v[152:155]\n"
"s_sub_u32 s41, s48, 5242880\n"
"ds_read_b64_tr_b16 a[230:231], v244 offset:26112\n"
"v_mfma_f32_16x16x32_bf16 v[148:151], a[104:107], a[168:171], v[148:151]\n"
"s_min_u32 s48, s48, s41\n"
"ds_read_b64_tr_b16 a[232:233], v245 offset:17408\n"
"v_mfma_f32_16x16x32_bf16 v[148:151], a[108:111], a[172:175], v[148:151]\n"
"ds_read_b64_tr_b16 a[234:235], v245 offset:17920\n"
"v_xor_b32 v244, s74, v244\n"
"v_mfma_f32_16x16x32_bf16 v[144:147], a[96:99], a[168:171], v[144:147]\n"
"s_mov_b32 m0, s50\n"
"ds_read_b64_tr_b16 a[236:237], v245 offset:25600\n"
"v_xor_b32 v250, s74, v250\n"
"v_mfma_f32_16x16x32_bf16 v[144:147], a[100:103], a[172:175], v[144:147]\n"
"buffer_load_dwordx4 v224, s[28:31], s48 offen lds\n"
"v_mfma_f32_16x16x32_bf16 v[176:179], a[96:99], a[176:179], v[176:179]\n"
"s_waitcnt lgkmcnt(6)\n"
"ds_read_b64_tr_b16 a[238:239], v245 offset:26112\n"
"v_xor_b32 v251, s74, v251\n"
"v_mfma_f32_16x16x32_bf16 v[176:179], a[100:103], a[180:183], v[176:179]\n"
"ds_read_b64_tr_b16 a[240:241], v246 offset:17408\n"
"v_xor_b32 v245, s74, v245\n"
"v_mfma_f32_16x16x32_bf16 v[180:183], a[104:107], a[176:179], v[180:183]\n"
"ds_read_b64_tr_b16 a[242:243], v246 offset:17920\n"
"v_xor_b32 v252, s74, v252\n"
"v_mfma_f32_16x16x32_bf16 v[180:183], a[108:111], a[180:183], v[180:183]\n"
"buffer_load_dwordx4 v225, s[28:31], s48 offen offset:1024 lds\n"
"v_mfma_f32_16x16x32_bf16 v[184:187], a[112:115], a[176:179], v[184:187]\n"
"ds_read_b64_tr_b16 a[244:245], v246 offset:25600\n"
"v_xor_b32 v253, s74, v253\n"
"v_mfma_f32_16x16x32_bf16 v[184:187], a[116:119], a[180:183], v[184:187]\n"
"ds_read_b64_tr_b16 a[246:247], v246 offset:26112\n"
"v_xor_b32 v246, s74, v246\n"
"v_mfma_f32_16x16x32_bf16 v[188:191], a[120:123], a[176:179], v[188:191]\n"
"ds_read_b64_tr_b16 a[248:249], v247 offset:17408\n"
"v_mfma_f32_16x16x32_bf16 v[188:191], a[124:127], a[180:183], v[188:191]\n"
"ds_read_b64_tr_b16 a[250:251], v247 offset:17920\n"
"v_mfma_f32_16x16x32_bf16 v[200:203], a[144:147], a[176:179], v[200:203]\n"
"ds_read_b64_tr_b16 a[252:253], v247 offset:25600\n"
"v_mfma_f32_16x16x32_bf16 v[200:203], a[148:151], a[180:183], v[200:203]\n"
"buffer_load_dwordx4 v226, s[28:31], s48 offen offset:2048 lds\n"
"v_mfma_f32_16x16x32_bf16 v[204:207], a[152:155], a[176:179], v[204:207]\n"
"ds_read_b64_tr_b16 a[254:255], v247 offset:26112\n"
"v_xor_b32 v247, s74, v247\n"
"v_mfma_f32_16x16x32_bf16 v[204:207], a[156:159], a[180:183], v[204:207]\n"
"v_mfma_f32_16x16x32_bf16 v[192:195], a[128:131], a[176:179], v[192:195]\n"
"v_mfma_f32_16x16x32_bf16 v[192:195], a[132:135], a[180:183], v[192:195]\n"
"v_mfma_f32_16x16x32_bf16 v[196:199], a[136:139], a[176:179], v[196:199]\n"
"v_mfma_f32_16x16x32_bf16 v[196:199], a[140:143], a[180:183], v[196:199]\n"
"buffer_load_dwordx4 v227, s[28:31], s48 offen offset:3072 lds\n"
"v_mfma_f32_16x16x32_bf16 v[220:223], a[136:139], a[184:187], v[220:223]\n"
"s_xor_b32 s50, s50, s74\n"
"v_mfma_f32_16x16x32_bf16 v[220:223], a[140:143], a[188:191], v[220:223]\n"
"v_mfma_f32_16x16x32_bf16 v[216:219], a[128:131], a[184:187], v[216:219]\n"
"v_mfma_f32_16x16x32_bf16 v[216:219], a[132:135], a[188:191], v[216:219]\n"
"v_mfma_f32_16x16x32_bf16 v[212:215], a[104:107], a[184:187], v[212:215]\n"
"v_mfma_f32_16x16x32_bf16 v[212:215], a[108:111], a[188:191], v[212:215]\n"
"v_mfma_f32_16x16x32_bf16 v[208:211], a[96:99], a[184:187], v[208:211]\n"
"v_mfma_f32_16x16x32_bf16 v[208:211], a[100:103], a[188:191], v[208:211]\n"
"v_mfma_f32_16x16x32_bf16 v[0:3], a[192:195], a[160:163], v[0:3]\n"
"s_waitcnt lgkmcnt(0)\n"
"v_mfma_f32_16x16x32_bf16 v[0:3], a[196:199], a[164:167], v[0:3]\n"
"s_waitcnt lgkmcnt(0)\n"
"v_mfma_f32_16x16x32_bf16 v[4:7], a[200:203], a[160:163], v[4:7]\n"
"s_waitcnt vmcnt(8) lgkmcnt(0)\n"
"v_mfma_f32_16x16x32_bf16 v[4:7], a[204:207], a[164:167], v[4:7]\n"
"s_barrier\n"
"v_mfma_f32_16x16x32_bf16 v[8:11], a[208:211], a[160:163], v[8:11]\n"
"s_mov_b32 m0, s51\n"
"ds_read_b64_tr_b16 a[96:97], v244 offset:0\n"
"v_mfma_f32_16x16x32_bf16 v[8:11], a[212:215], a[164:167], v[8:11]\n"
"buffer_load_dwordx4 v248, s[24:27], s49 offen lds\n"
"v_mfma_f32_16x16x32_bf16 v[12:15], a[216:219], a[160:163], v[12:15]\n"
"ds_read_b64_tr_b16 a[98:99], v244 offset:512\n"
"v_mfma_f32_16x16x32_bf16 v[12:15], a[220:223], a[164:167], v[12:15]\n"
"ds_read_b64_tr_b16 a[100:101], v244 offset:8192\n"
"v_mfma_f32_16x16x32_bf16 v[16:19], a[224:227], a[160:163], v[16:19]\n"
"ds_read_b64_tr_b16 a[102:103], v244 offset:8704\n"
"v_mfma_f32_16x16x32_bf16 v[16:19], a[228:231], a[164:167], v[16:19]\n"
"ds_read_b64_tr_b16 a[104:105], v245 offset:0\n"
"v_mfma_f32_16x16x32_bf16 v[20:23], a[232:235], a[160:163], v[20:23]\n"
"ds_read_b64_tr_b16 a[106:107], v245 offset:512\n"
"v_mfma_f32_16x16x32_bf16 v[20:23], a[236:239], a[164:167], v[20:23]\n"
"buffer_load_dwordx4 v249, s[24:27], s49 offen offset:1024 lds\n"
"v_mfma_f32_16x16x32_bf16 v[24:27], a[240:243], a[160:163], v[24:27]\n"
"ds_read_b64_tr_b16 a[108:109], v245 offset:8192\n"
"v_mfma_f32_16x16x32_bf16 v[24:27], a[244:247], a[164:167], v[24:27]\n"
"ds_read_b64_tr_b16 a[110:111], v245 offset:8704\n"
"v_mfma_f32_16x16x32_bf16 v[28:31], a[248:251], a[160:163], v[28:31]\n"
"ds_read_b128 a[160:163], v250 offset:0\n"
"v_mfma_f32_16x16x32_bf16 v[28:31], a[252:255], a[164:167], v[28:31]\n"
"ds_read_b128 a[164:167], v251 offset:0\n"
"v_mfma_f32_16x16x32_bf16 v[60:63], a[248:251], a[168:171], v[60:63]\n"
"ds_read_b64_tr_b16 a[112:113], v246 offset:0\n"
"v_mfma_f32_16x16x32_bf16 v[60:63], a[252:255], a[172:175], v[60:63]\n"
"buffer_load_dwordx4 v254, s[24:27], s49 offen offset:2048 lds\n"
"v_mfma_f32_16x16x32_bf16 v[56:59], a[240:243], a[168:171], v[56:59]\n"
"ds_read_b64_tr_b16 a[114:115], v246 offset:512\n"
"v_mfma_f32_16x16x32_bf16 v[56:59], a[244:247], a[172:175], v[56:59]\n"
"ds_read_b64_tr_b16 a[116:117], v246 offset:8192\n"
"v_mfma_f32_16x16x32_bf16 v[52:55], a[232:235], a[168:171], v[52:55]\n"
"ds_read_b64_tr_b16 a[118:119], v246 offset:8704\n"
"v_mfma_f32_16x16x32_bf16 v[52:55], a[236:239], a[172:175], v[52:55]\n"
"ds_read_b64_tr_b16 a[120:121], v247 offset:0\n"
"v_mfma_f32_16x16x32_bf16 v[48:51], a[224:227], a[168:171], v[48:51]\n"
"ds_read_b64_tr_b16 a[122:123], v247 offset:512\n"
"v_mfma_f32_16x16x32_bf16 v[48:51], a[228:231], a[172:175], v[48:51]\n"
"buffer_load_dwordx4 v255, s[24:27], s49 offen offset:3072 lds\n"
"v_mfma_f32_16x16x32_bf16 v[44:47], a[216:219], a[168:171], v[44:47]\n"
"ds_read_b64_tr_b16 a[124:125], v247 offset:8192\n"
"v_mfma_f32_16x16x32_bf16 v[44:47], a[220:223], a[172:175], v[44:47]\n"
"ds_read_b64_tr_b16 a[126:127], v247 offset:8704\n"
"v_mfma_f32_16x16x32_bf16 v[40:43], a[208:211], a[168:171], v[40:43]\n"
"ds_read_b64_tr_b16 a[128:129], v244 offset:1024\n"
"v_mfma_f32_16x16x32_bf16 v[40:43], a[212:215], a[172:175], v[40:43]\n"
"s_add_u32 s40, s49, 40960\n"
"ds_read_b64_tr_b16 a[130:131], v244 offset:1536\n"
"v_mfma_f32_16x16x32_bf16 v[36:39], a[200:203], a[168:171], v[36:39]\n"
"s_add_u32 m0, s51, 4096\n"
"ds_read_b64_tr_b16 a[132:133], v244 offset:9216\n"
"v_mfma_f32_16x16x32_bf16 v[36:39], a[204:207], a[172:175], v[36:39]\n"
"buffer_load_dwordx4 v248, s[24:27], s40 offen lds\n"
"v_mfma_f32_16x16x32_bf16 v[32:35], a[192:195], a[168:171], v[32:35]\n"
"ds_read_b64_tr_b16 a[134:135], v244 offset:9728\n"
"v_mfma_f32_16x16x32_bf16 v[32:35], a[196:199], a[172:175], v[32:35]\n"
"ds_read_b64_tr_b16 a[136:137], v245 offset:1024\n"
"v_mfma_f32_16x16x32_bf16 v[64:67], a[192:195], a[176:179], v[64:67]\n"
"ds_read_b64_tr_b16 a[138:139], v245 offset:1536\n"
"v_mfma_f32_16x16x32_bf16 v[64:67], a[196:199], a[180:183], v[64:67]\n"
"ds_read_b64_tr_b16 a[140:141], v245 offset:9216\n"
"v_mfma_f32_16x16x32_bf16 v[68:71], a[200:203], a[176:179], v[68:71]\n"
"ds_read_b64_tr_b16 a[142:143], v245 offset:9728\n"
"v_mfma_f32_16x16x32_bf16 v[68:71], a[204:207], a[180:183], v[68:71]\n"
"buffer_load_dwordx4 v249, s[24:27], s40 offen offset:1024 lds\n"
"v_mfma_f32_16x16x32_bf16 v[72:75], a[208:211], a[176:179], v[72:75]\n"
"ds_read_b64_tr_b16 a[144:145], v246 offset:1024\n"
"v_mfma_f32_16x16x32_bf16 v[72:75], a[212:215], a[180:183], v[72:75]\n"
"ds_read_b64_tr_b16 a[146:147], v246 offset:1536\n"
"v_mfma_f32_16x16x32_bf16 v[76:79], a[216:219], a[176:179], v[76:79]\n"
"ds_read_b64_tr_b16 a[148:149], v246 offset:9216\n"
"v_mfma_f32_16x16x32_bf16 v[76:79], a[220:223], a[180:183], v[76:79]\n"
"ds_read_b64_tr_b16 a[150:151], v246 offset:9728\n"
"v_mfma_f32_16x16x32_bf16 v[88:91], a[240:243], a[176:179], v[88:91]\n"
"ds_read_b64_tr_b16 a[152:153], v247 offset:1024\n"
"v_mfma_f32_16x16x32_bf16 v[88:91], a[244:247], a[180:183], v[88:91]\n"
"buffer_load_dwordx4 v254, s[24:27], s40 offen offset:2048 lds\n"
"v_mfma_f32_16x16x32_bf16 v[92:95], a[248:251], a[176:179], v[92:95]\n"
"s_add_u32 s49, s49, 128\n"
"ds_read_b64_tr_b16 a[154:155], v247 offset:1536\n"
"v_mfma_f32_16x16x32_bf16 v[92:95], a[252:255], a[180:183], v[92:95]\n"
"s_sub_u32 s41, s49, 1280\n"
"ds_read_b64_tr_b16 a[156:157], v247 offset:9216\n"
"v_mfma_f32_16x16x32_bf16 v[80:83], a[224:227], a[176:179], v[80:83]\n"
"s_min_u32 s49, s49, s41\n"
"ds_read_b64_tr_b16 a[158:159], v247 offset:9728\n"
"v_mfma_f32_16x16x32_bf16 v[80:83], a[228:231], a[180:183], v[80:83]\n"
"s_xor_b32 s51, s51, s74\n"
"ds_read_b128 a[168:171], v250 offset:2048\n"
"v_mfma_f32_16x16x32_bf16 v[84:87], a[232:235], a[176:179], v[84:87]\n"
"ds_read_b128 a[172:175], v251 offset:2048\n"
"v_mfma_f32_16x16x32_bf16 v[84:87], a[236:239], a[180:183], v[84:87]\n"
"ds_read_b128 a[176:179], v250 offset:4096\n"
"v_mfma_f32_16x16x32_bf16 v[108:111], a[232:235], a[184:187], v[108:111]\n"
"ds_read_b128 a[180:183], v251 offset:4096\n"
"v_mfma_f32_16x16x32_bf16 v[108:111], a[236:239], a[188:191], v[108:111]\n"
"v_mfma_f32_16x16x32_bf16 v[104:107], a[224:227], a[184:187], v[104:107]\n"
"v_mfma_f32_16x16x32_bf16 v[104:107], a[228:231], a[188:191], v[104:107]\n"
"v_mfma_f32_16x16x32_bf16 v[100:103], a[200:203], a[184:187], v[100:103]\n"
"v_mfma_f32_16x16x32_bf16 v[100:103], a[204:207], a[188:191], v[100:103]\n"
"v_mfma_f32_16x16x32_bf16 v[96:99], a[192:195], a[184:187], v[96:99]\n"
"s_add_u32 s47, s47, 1\n"
"ds_read_b128 a[184:187], v252 offset:0\n"
"v_mfma_f32_16x16x32_bf16 v[96:99], a[196:199], a[188:191], v[96:99]\n"
"s_waitcnt lgkmcnt(2)\n"
"s_cmp_lg_u32 s47, s62\n"
"ds_read_b128 a[188:191], v253 offset:0\n"
"s_cbranch_scc1 LPL_%=\n"
"s_cmp_eq_u32 s47, 18\n"
"s_cbranch_scc1 LPT2_%=\n"
"v_mfma_f32_16x16x32_bf16 v[112:115], a[96:99], a[160:163], v[112:115]\n"
"s_add_u32 s80, s96, 8192\n"
"v_mov_b32 v241, 1\n"
"v_mfma_f32_16x16x32_bf16 v[112:115], a[100:103], a[164:167], v[112:115]\n"
"s_mov_b64 exec, 1\n"
"buffer_store_dword v241, off, s[32:35], s96 sc0 sc1\n"
"buffer_store_dword v241, off, s[32:35], s80 sc0 sc1\n"
"s_mov_b64 exec, -1\n"
"v_mfma_f32_16x16x32_bf16 v[116:119], a[104:107], a[160:163], v[116:119]\n"
"s_waitcnt vmcnt(13)\n"
"v_mfma_f32_16x16x32_bf16 v[116:119], a[108:111], a[164:167], v[116:119]\n"
"s_barrier\n"
"v_mfma_f32_16x16x32_bf16 v[120:123], a[112:115], a[160:163], v[120:123]\n"
"s_add_u32 s40, s48, 256\n"
"s_xor_b32 m0, s50, 0xc000\n"
"ds_read_b64_tr_b16 a[232:233], v245 offset:17408\n"
"v_mfma_f32_16x16x32_bf16 v[120:123], a[116:119], a[164:167], v[120:123]\n"
"buffer_load_dwordx4 v224, s[28:31], s40 offen lds\n"
"v_mfma_f32_16x16x32_bf16 v[124:127], a[120:123], a[160:163], v[124:127]\n"
"ds_read_b64_tr_b16 a[234:235], v245 offset:17920\n"
"v_mfma_f32_16x16x32_bf16 v[124:127], a[124:127], a[164:167], v[124:127]\n"
"ds_read_b64_tr_b16 a[236:237], v245 offset:25600\n"
"v_mfma_f32_16x16x32_bf16 v[128:131], a[128:131], a[160:163], v[128:131]\n"
"ds_read_b64_tr_b16 a[238:239], v245 offset:26112\n"
"v_mfma_f32_16x16x32_bf16 v[128:131], a[132:135], a[164:167], v[128:131]\n"
"ds_read_b64_tr_b16 a[224:225], v244 offset:17408\n"
"v_mfma_f32_16x16x32_bf16 v[132:135], a[136:139], a[160:163], v[132:135]\n"
"ds_read_b64_tr_b16 a[226:227], v244 offset:17920\n"
"v_mfma_f32_16x16x32_bf16 v[132:135], a[140:143], a[164:167], v[132:135]\n"
"buffer_load_dwordx4 v225, s[28:31], s40 offen offset:1024 lds\n"
"v_mfma_f32_16x16x32_bf16 v[136:139], a[144:147], a[160:163], v[136:139]\n"
"ds_read_b64_tr_b16 a[192:193], v244 offset:16384\n"
"v_mfma_f32_16x16x32_bf16 v[136:139], a[148:151], a[164:167], v[136:139]\n"
"ds_read_b64_tr_b16 a[194:195], v244 offset:16896\n"
"v_mfma_f32_16x16x32_bf16 v[140:143], a[152:155], a[160:163], v[140:143]\n"
"ds_read_b64_tr_b16 a[196:197], v244 offset:24576\n"
"v_mfma_f32_16x16x32_bf16 v[140:143], a[156:159], a[164:167], v[140:143]\n"
"s_waitcnt lgkmcnt(6)\n"
"ds_read_b64_tr_b16 a[198:199], v244 offset:25088\n"
"v_mfma_f32_16x16x32_bf16 v[220:223], a[136:139], a[184:187], v[220:223]\n"
"ds_read_b64_tr_b16 a[200:201], v245 offset:16384\n"
"v_mfma_f32_16x16x32_bf16 v[220:223], a[140:143], a[188:191], v[220:223]\n"
"buffer_load_dwordx4 v226, s[28:31], s40 offen offset:2048 lds\n"
"v_mfma_f32_16x16x32_bf16 v[216:219], a[128:131], a[184:187], v[216:219]\n"
"ds_read_b64_tr_b16 a[202:203], v245 offset:16896\n"
"v_mfma_f32_16x16x32_bf16 v[216:219], a[132:135], a[188:191], v[216:219]\n"
"ds_read_b64_tr_b16 a[204:205], v245 offset:24576\n"
"v_mfma_f32_16x16x32_bf16 v[212:215], a[104:107], a[184:187], v[212:215]\n"
"ds_read_b64_tr_b16 a[206:207], v245 offset:25088\n"
"v_mfma_f32_16x16x32_bf16 v[212:215], a[108:111], a[188:191], v[212:215]\n"
"v_pk_fma_f32 v[112:113], v[112:113], s[70:71], s[70:71] op_sel:[0,0,1] op_sel_hi:[1,0,1]\nv_pk_fma_f32 v[114:115], v[114:115], s[70:71], s[70:71] op_sel:[0,0,1] op_sel_hi:[1,0,1]\nv_pk_fma_f32 v[116:117], v[116:117], s[70:71], s[70:71] op_sel:[0,0,1] op_sel_hi:[1,0,1]\nv_pk_fma_f32 v[118:119], v[118:119], s[70:71], s[70:71] op_sel:[0,0,1] op_sel_hi:[1,0,1]\nv_pk_fma_f32 v[120:121], v[120:121], s[70:71], s[70:71] op_sel:[0,0,1] op_sel_hi:[1,0,1]\nv_pk_fma_f32 v[122:123], v[122:123], s[70:71], s[70:71] op_sel:[0,0,1] op_sel_hi:[1,0,1]\nv_pk_fma_f32 v[124:125], v[124:125], s[70:71], s[70:71] op_sel:[0,0,1] op_sel_hi:[1,0,1]\nv_pk_fma_f32 v[126:127], v[126:127], s[70:71], s[70:71] op_sel:[0,0,1] op_sel_hi:[1,0,1]\nv_pk_fma_f32 v[128:129], v[128:129], s[70:71], s[70:71] op_sel:[0,0,1] op_sel_hi:[1,0,1]\nv_pk_fma_f32 v[130:131], v[130:131], s[70:71], s[70:71] op_sel:[0,0,1] op_sel_hi:[1,0,1]\nv_pk_fma_f32 v[132:133], v[132:133], s[70:71], s[70:71] op_sel:[0,0,1] op_sel_hi:[1,0,1]\nv_pk_fma_f32 v[134:135], v[134:135], s[70:71], s[70:71] op_sel:[0,0,1] op_sel_hi:[1,0,1]\nv_pk_fma_f32 v[136:137], v[136:137], s[70:71], s[70:71] op_sel:[0,0,1] op_sel_hi:[1,0,1]\nv_pk_fma_f32 v[138:139], v[138:139], s[70:71], s[70:71] op_sel:[0,0,1] op_sel_hi:[1,0,1]\nv_pk_fma_f32 v[140:141], v[140:141], s[70:71], s[70:71] op_sel:[0,0,1] op_sel_hi:[1,0,1]\nv_pk_fma_f32 v[142:143], v[142:143], s[70:71], s[70:71] op_sel:[0,0,1] op_sel_hi:[1,0,1]\n"
"v_mfma_f32_16x16x32_bf16 v[208:211], a[96:99], a[184:187], v[208:211]\n"
"ds_read_b64_tr_b16 a[208:209], v246 offset:16384\n"
"v_cvt_pk_bf16_f32 v112, v112, v113\n"
"v_mfma_f32_16x16x32_bf16 v[208:211], a[100:103], a[188:191], v[208:211]\n"
"buffer_load_dwordx4 v227, s[28:31], s40 offen offset:3072 lds\n"
"v_mfma_f32_16x16x32_bf16 v[172:175], a[152:155], a[168:171], v[172:175]\n"
"s_cmp_eq_u32 s47, 8\n"
"ds_read_b64_tr_b16 a[210:211], v246 offset:16896\n"
"v_cvt_pk_bf16_f32 v113, v114, v115\n"
"v_mfma_f32_16x16x32_bf16 v[172:175], a[156:159], a[172:175], v[172:175]\n"
"s_cselect_b32 s41, 524800, s58\n"
"ds_read_b64_tr_b16 a[212:213], v246 offset:24576\n"
"v_cvt_pk_bf16_f32 v114, v116, v117\n"
"v_mfma_f32_16x16x32_bf16 v[168:171], a[144:147], a[168:171], v[168:171]\n"
"s_add_u32 s48, s48, s41\n"
"ds_read_b64_tr_b16 a[214:215], v246 offset:25088\n"
"v_cvt_pk_bf16_f32 v115, v118, v119\n"
"v_mfma_f32_16x16x32_bf16 v[168:171], a[148:151], a[172:175], v[168:171]\n"
"s_sub_u32 s41, s48, 5242880\n"
"ds_read_b64_tr_b16 a[216:217], v247 offset:16384\n"
"v_cvt_pk_bf16_f32 v120, v120, v121\n"
"v_mfma_f32_16x16x32_bf16 v[164:167], a[136:139], a[168:171], v[164:167]\n"
"s_min_u32 s48, s48, s41\n"
"ds_read_b64_tr_b16 a[218:219], v247 offset:16896\n"
"v_cvt_pk_bf16_f32 v121, v122, v123\n"
"v_mfma_f32_16x16x32_bf16 v[164:167], a[140:143], a[172:175], v[164:167]\n"
"ds_read_b64_tr_b16 a[220:221], v247 offset:24576\n"
"v_xor_b32 v250, s74, v250\n"
"v_mfma_f32_16x16x32_bf16 v[160:163], a[128:131], a[168:171], v[160:163]\n"
"s_mov_b32 m0, s50\n"
"ds_read_b64_tr_b16 a[222:223], v247 offset:25088\n"
"v_xor_b32 v251, s74, v251\n"
"v_mfma_f32_16x16x32_bf16 v[160:163], a[132:135], a[172:175], v[160:163]\n"
"buffer_load_dwordx4 v224, s[28:31], s48 offen lds\n"
"v_mfma_f32_16x16x32_bf16 v[156:159], a[120:123], a[168:171], v[156:159]\n"
"ds_read_b64_tr_b16 a[228:229], v244 offset:25600\n"
"v_xor_b32 v245, s74, v245\n"
"v_mfma_f32_16x16x32_bf16 v[156:159], a[124:127], a[172:175], v[156:159]\n"
"s_mov_b64 s[80:81], s[8:9]\n"
"ds_read_b64_tr_b16 a[230:231], v244 offset:26112\n"
"v_xor_b32 v244, s74, v244\n"
"v_mfma_f32_16x16x32_bf16 v[152:155], a[112:115], a[168:171], v[152:155]\n"
"s_mov_b32 s82, s68\n"
"ds_read_b64_tr_b16 a[240:241], v246 offset:17408\n"
"v_cvt_pk_bf16_f32 v122, v124, v125\n"
"v_mfma_f32_16x16x32_bf16 v[152:155], a[116:119], a[172:175], v[152:155]\n"
"s_mov_b32 s83, 131072\n"
"buffer_load_dwordx4 v225, s[28:31], s48 offen offset:1024 lds\n"
"v_mfma_f32_16x16x32_bf16 v[148:151], a[104:107], a[168:171], v[148:151]\n"
"v_and_b32 v241, 0x3f0, v242\n"
"ds_read_b64_tr_b16 a[242:243], v246 offset:17920\n"
"v_cvt_pk_bf16_f32 v123, v126, v127\n"
"v_mfma_f32_16x16x32_bf16 v[148:151], a[108:111], a[172:175], v[148:151]\n"
"v_mul_u32_u24 v241, 10, v241\n"
"ds_read_b64_tr_b16 a[244:245], v246 offset:25600\n"
"v_cvt_pk_bf16_f32 v128, v128, v129\n"
"v_mfma_f32_16x16x32_bf16 v[144:147], a[96:99], a[168:171], v[144:147]\n"
"ds_read_b64_tr_b16 a[246:247], v246 offset:26112\n"
"v_xor_b32 v246, s74, v246\n"
"v_mfma_f32_16x16x32_bf16 v[144:147], a[100:103], a[172:175], v[144:147]\n"
"ds_read_b64_tr_b16 a[248:249], v247 offset:17408\n"
"v_cvt_pk_bf16_f32 v129, v130, v131\n"
"v_mfma_f32_16x16x32_bf16 v[176:179], a[96:99], a[176:179], v[176:179]\n"
"ds_read_b64_tr_b16 a[250:251], v247 offset:17920\n"
"v_cvt_pk_bf16_f32 v130, v132, v133\n"
"v_mfma_f32_16x16x32_bf16 v[176:179], a[100:103], a[180:183], v[176:179]\n"
"buffer_load_dwordx4 v226, s[28:31], s48 offen offset:2048 lds\n"
"v_mfma_f32_16x16x32_bf16 v[180:183], a[104:107], a[176:179], v[180:183]\n"
"ds_read_b64_tr_b16 a[252:253], v247 offset:25600\n"
"v_cvt_pk_bf16_f32 v131, v134, v135\n"
"v_mfma_f32_16x16x32_bf16 v[180:183], a[108:111], a[180:183], v[180:183]\n"
"ds_read_b64_tr_b16 a[254:255], v247 offset:26112\n"
"v_xor_b32 v247, s74, v247\n"
"v_mfma_f32_16x16x32_bf16 v[184:187], a[112:115], a[176:179], v[184:187]\n"
"v_permlane16_swap_b32 v112, v114\n"
"v_mfma_f32_16x16x32_bf16 v[184:187], a[116:119], a[180:183], v[184:187]\n"
"v_permlane16_swap_b32 v113, v115\n"
"v_mfma_f32_16x16x32_bf16 v[188:191], a[120:123], a[176:179], v[188:191]\n"
"v_permlane16_swap_b32 v120, v122\n"
"v_mfma_f32_16x16x32_bf16 v[188:191], a[124:127], a[180:183], v[188:191]\n"
"buffer_load_dwordx4 v227, s[28:31], s48 offen offset:3072 lds\n"
"v_mfma_f32_16x16x32_bf16 v[200:203], a[144:147], a[176:179], v[200:203]\n"
"s_xor_b32 s50, s50, s74\n"
"v_permlane16_swap_b32 v121, v123\n"
"v_mfma_f32_16x16x32_bf16 v[200:203], a[148:151], a[180:183], v[200:203]\n"
"v_permlane16_swap_b32 v128, v130\n"
"v_mfma_f32_16x16x32_bf16 v[204:207], a[152:155], a[176:179], v[204:207]\n"
"v_permlane16_swap_b32 v129, v131\n"
"v_mfma_f32_16x16x32_bf16 v[204:207], a[156:159], a[180:183], v[204:207]\n"
"v_pk_fma_f32 v[144:145], v[144:145], s[70:71], s[70:71] op_sel:[0,0,1] op_sel_hi:[1,0,1]\nv_pk_fma_f32 v[146:147], v[146:147], s[70:71], s[70:71] op_sel:[0,0,1] op_sel_hi:[1,0,1]\nv_pk_fma_f32 v[148:149], v[148:149], s[70:71], s[70:71] op_sel:[0,0,1] op_sel_hi:[1,0,1]\nv_pk_fma_f32 v[150:151], v[150:151], s[70:71], s[70:71] op_sel:[0,0,1] op_sel_hi:[1,0,1]\nv_pk_fma_f32 v[152:153], v[152:153], s[70:71], s[70:71] op_sel:[0,0,1] op_sel_hi:[1,0,1]\nv_pk_fma_f32 v[154:155], v[154:155], s[70:71], s[70:71] op_sel:[0,0,1] op_sel_hi:[1,0,1]\nv_pk_fma_f32 v[156:157], v[156:157], s[70:71], s[70:71] op_sel:[0,0,1] op_sel_hi:[1,0,1]\nv_pk_fma_f32 v[158:159], v[158:159], s[70:71], s[70:71] op_sel:[0,0,1] op_sel_hi:[1,0,1]\nv_pk_fma_f32 v[160:161], v[160:161], s[70:71], s[70:71] op_sel:[0,0,1] op_sel_hi:[1,0,1]\nv_pk_fma_f32 v[162:163], v[162:163], s[70:71], s[70:71] op_sel:[0,0,1] op_sel_hi:[1,0,1]\nv_pk_fma_f32 v[164:165], v[164:165], s[70:71], s[70:71] op_sel:[0,0,1] op_sel_hi:[1,0,1]\nv_pk_fma_f32 v[166:167], v[166:167], s[70:71], s[70:71] op_sel:[0,0,1] op_sel_hi:[1,0,1]\nv_pk_fma_f32 v[168:169], v[168:169], s[70:71], s[70:71] op_sel:[0,0,1] op_sel_hi:[1,0,1]\nv_pk_fma_f32 v[170:171], v[170:171], s[70:71], s[70:71] op_sel:[0,0,1] op_sel_hi:[1,0,1]\nv_pk_fma_f32 v[172:173], v[172:173], s[70:71], s[70:71] op_sel:[0,0,1] op_sel_hi:[1,0,1]\nv_pk_fma_f32 v[174:175], v[174:175], s[70:71], s[70:71] op_sel:[0,0,1] op_sel_hi:[1,0,1]\n"
"v_mfma_f32_16x16x32_bf16 v[192:195], a[128:131], a[176:179], v[192:195]\n"
"v_pk_fma_f32 v[208:209], v[208:209], s[70:71], s[70:71] op_sel:[0,0,1] op_sel_hi:[1,0,1]\nv_pk_fma_f32 v[210:211], v[210:211], s[70:71], s[70:71] op_sel:[0,0,1] op_sel_hi:[1,0,1]\nv_pk_fma_f32 v[212:213], v[212:213], s[70:71], s[70:71] op_sel:[0,0,1] op_sel_hi:[1,0,1]\nv_pk_fma_f32 v[214:215], v[214:215], s[70:71], s[70:71] op_sel:[0,0,1] op_sel_hi:[1,0,1]\nv_pk_fma_f32 v[216:217], v[216:217], s[70:71], s[70:71] op_sel:[0,0,1] op_sel_hi:[1,0,1]\nv_pk_fma_f32 v[218:219], v[218:219], s[70:71], s[70:71] op_sel:[0,0,1] op_sel_hi:[1,0,1]\nv_pk_fma_f32 v[220:221], v[220:221], s[70:71], s[70:71] op_sel:[0,0,1] op_sel_hi:[1,0,1]\nv_pk_fma_f32 v[222:223], v[222:223], s[70:71], s[70:71] op_sel:[0,0,1] op_sel_hi:[1,0,1]\n"
"v_mfma_f32_16x16x32_bf16 v[192:195], a[132:135], a[180:183], v[192:195]\n"
"v_accvgpr_write_b32 a0, v112\n"
"v_accvgpr_write_b32 a1, v113\n"
"v_mfma_f32_16x16x32_bf16 v[196:199], a[136:139], a[176:179], v[196:199]\n"
"v_accvgpr_write_b32 a2, v114\n"
"v_accvgpr_write_b32 a3, v115\n"
"v_mfma_f32_16x16x32_bf16 v[196:199], a[140:143], a[180:183], v[196:199]\n"
"v_accvgpr_write_b32 a4, v120\n"
"v_accvgpr_write_b32 a5, v121\n"
"v_mfma_f32_16x16x32_bf16 v[108:111], a[232:235], a[184:187], v[108:111]\n"
"s_waitcnt lgkmcnt(0)\n"
"v_accvgpr_write_b32 a6, v122\n"
"v_accvgpr_write_b32 a7, v123\n"
"v_mfma_f32_16x16x32_bf16 v[108:111], a[236:239], a[188:191], v[108:111]\n"
"v_accvgpr_write_b32 a8, v128\n"
"v_accvgpr_write_b32 a9, v129\n"
"v_mfma_f32_16x16x32_bf16 v[104:107], a[224:227], a[184:187], v[104:107]\n"
"s_waitcnt vmcnt(10)\n"
"v_accvgpr_write_b32 a10, v130\n"
"v_accvgpr_write_b32 a11, v131\n"
"v_mfma_f32_16x16x32_bf16 v[104:107], a[228:231], a[188:191], v[104:107]\n"
"s_barrier\n"
"v_mfma_f32_16x16x32_bf16 v[100:103], a[200:203], a[184:187], v[100:103]\n"
"s_mov_b32 m0, s51\n"
"ds_read_b64_tr_b16 a[96:97], v244 offset:0\n"
"v_cvt_pk_bf16_f32 v136, v136, v137\n"
"v_mfma_f32_16x16x32_bf16 v[100:103], a[204:207], a[188:191], v[100:103]\n"
"buffer_load_dwordx4 v248, s[24:27], s49 offen lds\n"
"v_mfma_f32_16x16x32_bf16 v[96:99], a[192:195], a[184:187], v[96:99]\n"
"ds_read_b64_tr_b16 a[98:99], v244 offset:512\n"
"v_cvt_pk_bf16_f32 v137, v138, v139\n"
"v_mfma_f32_16x16x32_bf16 v[96:99], a[196:199], a[188:191], v[96:99]\n"
"ds_read_b64_tr_b16 a[100:101], v244 offset:8192\n"
"v_cvt_pk_bf16_f32 v138, v140, v141\n"
"v_mfma_f32_16x16x32_bf16 v[0:3], a[192:195], a[160:163], v[0:3]\n"
"ds_read_b64_tr_b16 a[102:103], v244 offset:8704\n"
"v_cvt_pk_bf16_f32 v139, v142, v143\n"
"v_mfma_f32_16x16x32_bf16 v[0:3], a[196:199], a[164:167], v[0:3]\n"
"ds_read_b64_tr_b16 a[104:105], v245 offset:0\n"
"v_cvt_pk_bf16_f32 v168, v168, v169\n"
"v_mfma_f32_16x16x32_bf16 v[4:7], a[200:203], a[160:163], v[4:7]\n"
"ds_read_b64_tr_b16 a[106:107], v245 offset:512\n"
"v_cvt_pk_bf16_f32 v169, v170, v171\n"
"v_mfma_f32_16x16x32_bf16 v[4:7], a[204:207], a[164:167], v[4:7]\n"
"buffer_load_dwordx4 v249, s[24:27], s49 offen offset:1024 lds\n"
"v_mfma_f32_16x16x32_bf16 v[8:11], a[208:211], a[160:163], v[8:11]\n"
"ds_read_b64_tr_b16 a[108:109], v245 offset:8192\n"
"v_cvt_pk_bf16_f32 v170, v172, v173\n"
"v_mfma_f32_16x16x32_bf16 v[8:11], a[212:215], a[164:167], v[8:11]\n"
"ds_read_b64_tr_b16 a[110:111], v245 offset:8704\n"
"v_cvt_pk_bf16_f32 v171, v174, v175\n"
"v_mfma_f32_16x16x32_bf16 v[12:15], a[216:219], a[160:163], v[12:15]\n"
"ds_read_b64_tr_b16 a[112:113], v246 offset:0\n"
"v_cvt_pk_bf16_f32 v160, v160, v161\n"
"v_mfma_f32_16x16x32_bf16 v[12:15], a[220:223], a[164:167], v[12:15]\n"
"buffer_load_dwordx4 v254, s[24:27], s49 offen offset:2048 lds\n"
"v_mfma_f32_16x16x32_bf16 v[16:19], a[224:227], a[160:163], v[16:19]\n"
"ds_read_b64_tr_b16 a[114:115], v246 offset:512\n"
"v_cvt_pk_bf16_f32 v161, v162, v163\n"
"v_mfma_f32_16x16x32_bf16 v[16:19], a[228:231], a[164:167], v[16:19]\n"
"ds_read_b64_tr_b16 a[116:117], v246 offset:8192\n"
"v_cvt_pk_bf16_f32 v162, v164, v165\n"
"v_mfma_f32_16x16x32_bf16 v[20:23], a[232:235], a[160:163], v[20:23]\n"
"ds_read_b64_tr_b16 a[118:119], v246 offset:8704\n"
"v_cvt_pk_bf16_f32 v163, v166, v167\n"
"v_mfma_f32_16x16x32_bf16 v[20:23], a[236:239], a[164:167], v[20:23]\n"
"ds_read_b64_tr_b16 a[120:121], v247 offset:0\n"
"v_cvt_pk_bf16_f32 v152, v152, v153\n"
"v_mfma_f32_16x16x32_bf16 v[24:27], a[240:243], a[160:163], v[24:27]\n"
"ds_read_b64_tr_b16 a[122:123], v247 offset:512\n"
"v_cvt_pk_bf16_f32 v153, v154, v155\n"
"v_mfma_f32_16x16x32_bf16 v[24:27], a[244:247], a[164:167], v[24:27]\n"
"buffer_load_dwordx4 v255, s[24:27], s49 offen offset:3072 lds\n"
"v_mfma_f32_16x16x32_bf16 v[28:31], a[248:251], a[160:163], v[28:31]\n"
"ds_read_b128 a[160:163], v250 offset:0\n"
"v_cvt_pk_bf16_f32 v154, v156, v157\n"
"v_mfma_f32_16x16x32_bf16 v[28:31], a[252:255], a[164:167], v[28:31]\n"
"ds_read_b128 a[164:167], v251 offset:0\n"
"v_cvt_pk_bf16_f32 v155, v158, v159\n"
"v_mfma_f32_16x16x32_bf16 v[60:63], a[248:251], a[168:171], v[60:63]\n"
"ds_read_b64_tr_b16 a[124:125], v247 offset:8192\n"
"v_cvt_pk_bf16_f32 v144, v144, v145\n"
"v_mfma_f32_16x16x32_bf16 v[60:63], a[252:255], a[172:175], v[60:63]\n"
"s_add_u32 s40, s49, 40960\n"
"ds_read_b64_tr_b16 a[126:127], v247 offset:8704\n"
"v_cvt_pk_bf16_f32 v145, v146, v147\n"
"v_mfma_f32_16x16x32_bf16 v[56:59], a[240:243], a[168:171], v[56:59]\n"
"s_add_u32 m0, s51, 4096\n"
"ds_read_b64_tr_b16 a[128:129], v244 offset:1024\n"
"v_cvt_pk_bf16_f32 v146, v148, v149\n"
"v_mfma_f32_16x16x32_bf16 v[56:59], a[244:247], a[172:175], v[56:59]\n"
"buffer_load_dwordx4 v248, s[24:27], s40 offen lds\n"
"v_mfma_f32_16x16x32_bf16 v[52:55], a[232:235], a[168:171], v[52:55]\n"
"ds_read_b64_tr_b16 a[130:131], v244 offset:1536\n"
"v_cvt_pk_bf16_f32 v147, v150, v151\n"
"v_mfma_f32_16x16x32_bf16 v[52:55], a[236:239], a[172:175], v[52:55]\n"
"ds_read_b64_tr_b16 a[132:133], v244 offset:9216\n"
"v_xor_b32 v252, s74, v252\n"
"v_mfma_f32_16x16x32_bf16 v[48:51], a[224:227], a[168:171], v[48:51]\n"
"ds_read_b64_tr_b16 a[134:135], v244 offset:9728\n"
"v_cvt_pk_bf16_f32 v216, v216, v217\n"
"v_mfma_f32_16x16x32_bf16 v[48:51], a[228:231], a[172:175], v[48:51]\n"
"ds_read_b64_tr_b16 a[136:137], v245 offset:1024\n"
"v_cvt_pk_bf16_f32 v217, v218, v219\n"
"v_mfma_f32_16x16x32_bf16 v[44:47], a[216:219], a[168:171], v[44:47]\n"
"ds_read_b64_tr_b16 a[138:139], v245 offset:1536\n"
"v_cvt_pk_bf16_f32 v218, v220, v221\n"
"v_mfma_f32_16x16x32_bf16 v[44:47], a[220:223], a[172:175], v[44:47]\n"
"buffer_load_dwordx4 v249, s[24:27], s40 offen offset:1024 lds\n"
"v_mfma_f32_16x16x32_bf16 v[40:43], a[208:211], a[168:171], v[40:43]\n"
"ds_read_b64_tr_b16 a[140:141], v245 offset:9216\n"
"v_cvt_pk_bf16_f32 v219, v222, v223\n"
"v_mfma_f32_16x16x32_bf16 v[40:43], a[212:215], a[172:175], v[40:43]\n"
"ds_read_b64_tr_b16 a[142:143], v245 offset:9728\n"
"v_xor_b32 v253, s74, v253\n"
"v_mfma_f32_16x16x32_bf16 v[36:39], a[200:203], a[168:171], v[36:39]\n"
"ds_read_b64_tr_b16 a[144:145], v246 offset:1024\n"
"v_cvt_pk_bf16_f32 v208, v208, v209\n"
"v_mfma_f32_16x16x32_bf16 v[36:39], a[204:207], a[172:175], v[36:39]\n"
"buffer_load_dwordx4 v254, s[24:27], s40 offen offset:2048 lds\n"
"v_mfma_f32_16x16x32_bf16 v[32:35], a[192:195], a[168:171], v[32:35]\n"
"s_add_u32 s49, s49, 128\n"
"ds_read_b64_tr_b16 a[146:147], v246 offset:1536\n"
"v_cvt_pk_bf16_f32 v209, v210, v211\n"
"v_mfma_f32_16x16x32_bf16 v[32:35], a[196:199], a[172:175], v[32:35]\n"
"s_sub_u32 s41, s49, 1280\n"
"ds_read_b64_tr_b16 a[148:149], v246 offset:9216\n"
"v_cvt_pk_bf16_f32 v210, v212, v213\n"
"v_mfma_f32_16x16x32_bf16 v[64:67], a[192:195], a[176:179], v[64:67]\n"
"s_min_u32 s49, s49, s41\n"
"ds_read_b64_tr_b16 a[150:151], v246 offset:9728\n"
"v_cvt_pk_bf16_f32 v211, v214, v215\n"
"v_mfma_f32_16x16x32_bf16 v[64:67], a[196:199], a[180:183], v[64:67]\n"
"s_xor_b32 s51, s51, s74\n"
"v_permlane16_swap_b32 v136, v138\n"
"v_mfma_f32_16x16x32_bf16 v[68:71], a[200:203], a[176:179], v[68:71]\n"
"s_mov_b32 m0, 139264\n"
"ds_read_b64_tr_b16 a[152:153], v247 offset:1024\n"
"v_accvgpr_write_b32 a12, v136\n"
"v_mfma_f32_16x16x32_bf16 v[68:71], a[204:207], a[180:183], v[68:71]\n"
"buffer_load_dword v241, s[80:83], s63 offen lds\n"
"v_mfma_f32_16x16x32_bf16 v[72:75], a[208:211], a[176:179], v[72:75]\n"
"ds_read_b64_tr_b16 a[154:155], v247 offset:1536\n"
"v_accvgpr_write_b32 a14, v138\n"
"v_mfma_f32_16x16x32_bf16 v[72:75], a[212:215], a[180:183], v[72:75]\n"
"v_permlane16_swap_b32 v137, v139\n"
"v_mfma_f32_16x16x32_bf16 v[76:79], a[216:219], a[176:179], v[76:79]\n"
"ds_read_b64_tr_b16 a[156:157], v247 offset:9216\n"
"v_accvgpr_write_b32 a13, v137\n"
"v_mfma_f32_16x16x32_bf16 v[76:79], a[220:223], a[180:183], v[76:79]\n"
"ds_read_b64_tr_b16 a[158:159], v247 offset:9728\n"
"v_accvgpr_write_b32 a15, v139\n"
"v_mfma_f32_16x16x32_bf16 v[88:91], a[240:243], a[176:179], v[88:91]\n"
"v_permlane16_swap_b32 v168, v170\n"
"v_mfma_f32_16x16x32_bf16 v[88:91], a[244:247], a[180:183], v[88:91]\n"
"buffer_load_dword v241, s[80:83], s63 offen offset:80 lds\n"
"v_mfma_f32_16x16x32_bf16 v[92:95], a[248:251], a[176:179], v[92:95]\n"
"ds_read_b128 a[168:171], v250 offset:2048\n"
"v_accvgpr_write_b32 a28, v168\n"
"v_mfma_f32_16x16x32_bf16 v[92:95], a[252:255], a[180:183], v[92:95]\n"
"ds_read_b128 a[172:175], v251 offset:2048\n"
"v_accvgpr_write_b32 a30, v170\n"
"v_mfma_f32_16x16x32_bf16 v[80:83], a[224:227], a[176:179], v[80:83]\n"
"v_permlane16_swap_b32 v169, v171\n"
"v_mfma_f32_16x16x32_bf16 v[80:83], a[228:231], a[180:183], v[80:83]\n"
"ds_read_b128 a[184:187], v252 offset:0\n"
"v_accvgpr_write_b32 a29, v169\n"
"v_mfma_f32_16x16x32_bf16 v[84:87], a[232:235], a[176:179], v[84:87]\n"
"s_add_u32 s47, s47, 1\n"
"ds_read_b128 a[176:179], v250 offset:4096\n"
"v_accvgpr_write_b32 a31, v171\n"
"v_mfma_f32_16x16x32_bf16 v[84:87], a[236:239], a[180:183], v[84:87]\n"
"s_waitcnt lgkmcnt(4)\n"
"v_permlane16_swap_b32 v160, v162\n"
"v_mfma_f32_16x16x32_bf16 v[112:115], a[96:99], a[160:163], v[228:231]\n"
"ds_read_b128 a[180:183], v251 offset:4096\n"
"v_accvgpr_write_b32 a24, v160\n"
"v_mfma_f32_16x16x32_bf16 v[112:115], a[100:103], a[164:167], v[112:115]\n"
"ds_read_b128 a[188:191], v253 offset:0\n"
"v_accvgpr_write_b32 a26, v162\n"
"v_mfma_f32_16x16x32_bf16 v[116:119], a[104:107], a[160:163], v[228:231]\n"
"s_waitcnt vmcnt(13)\n"
"v_permlane16_swap_b32 v161, v163\n"
"v_mfma_f32_16x16x32_bf16 v[116:119], a[108:111], a[164:167], v[116:119]\n"
"s_barrier\n"
"v_mfma_f32_16x16x32_bf16 v[120:123], a[112:115], a[160:163], v[228:231]\n"
"s_add_u32 s40, s48, 256\n"
"s_xor_b32 m0, s50, 0xc000\n"
"ds_read_b64_tr_b16 a[192:193], v244 offset:16384\n"
"v_mfma_f32_16x16x32_bf16 v[120:123], a[116:119], a[164:167], v[120:123]\n"
"buffer_load_dwordx4 v224, s[28:31], s40 offen lds\n"
"v_mfma_f32_16x16x32_bf16 v[124:127], a[120:123], a[160:163], v[228:231]\n"
"ds_read_b64_tr_b16 a[194:195], v244 offset:16896\n"
"v_accvgpr_write_b32 a25, v161\n"
"v_mfma_f32_16x16x32_bf16 v[124:127], a[124:127], a[164:167], v[124:127]\n"
"ds_read_b64_tr_b16 a[196:197], v244 offset:24576\n"
"v_accvgpr_write_b32 a27, v163\n"
"v_mfma_f32_16x16x32_bf16 v[128:131], a[128:131], a[160:163], v[228:231]\n"
"v_permlane16_swap_b32 v152, v154\n"
"v_mfma_f32_16x16x32_bf16 v[128:131], a[132:135], a[164:167], v[128:131]\n"
"ds_read_b64_tr_b16 a[198:199], v244 offset:25088\n"
"v_accvgpr_write_b32 a20, v152\n"
"v_mfma_f32_16x16x32_bf16 v[132:135], a[136:139], a[160:163], v[228:231]\n"
"ds_read_b64_tr_b16 a[200:201], v245 offset:16384\n"
"v_accvgpr_write_b32 a22, v154\n"
"v_mfma_f32_16x16x32_bf16 v[132:135], a[140:143], a[164:167], v[132:135]\n"
"buffer_load_dwordx4 v225, s[28:31], s40 offen offset:1024 lds\n"
"v_mfma_f32_16x16x32_bf16 v[136:139], a[144:147], a[160:163], v[228:231]\n"
"v_permlane16_swap_b32 v153, v155\n"
"v_mfma_f32_16x16x32_bf16 v[136:139], a[148:151], a[164:167], v[136:139]\n"
"ds_read_b64_tr_b16 a[202:203], v245 offset:16896\n"
"v_accvgpr_write_b32 a21, v153\n"
"v_mfma_f32_16x16x32_bf16 v[140:143], a[152:155], a[160:163], v[228:231]\n"
"ds_read_b64_tr_b16 a[204:205], v245 offset:24576\n"
"v_accvgpr_write_b32 a23, v155\n"
"v_mfma_f32_16x16x32_bf16 v[140:143], a[156:159], a[164:167], v[140:143]\n"
"s_waitcnt lgkmcnt(4)\n"
"v_permlane16_swap_b32 v144, v146\n"
"v_mfma_f32_16x16x32_bf16 v[172:175], a[152:155], a[168:171], v[228:231]\n"
"ds_read_b64_tr_b16 a[206:207], v245 offset:25088\n"
"v_accvgpr_write_b32 a16, v144\n"
"v_mfma_f32_16x16x32_bf16 v[172:175], a[156:159], a[172:175], v[172:175]\n"
"buffer_load_dwordx4 v226, s[28:31], s40 offen offset:2048 lds\n"
"v_mfma_f32_16x16x32_bf16 v[168:171], a[144:147], a[168:171], v[228:231]\n"
"ds_read_b64_tr_b16 a[208:209], v246 offset:16384\n"
"v_accvgpr_write_b32 a18, v146\n"
"v_mfma_f32_16x16x32_bf16 v[168:171], a[148:151], a[172:175], v[168:171]\n"
"v_permlane16_swap_b32 v145, v147\n"
"v_mfma_f32_16x16x32_bf16 v[164:167], a[136:139], a[168:171], v[228:231]\n"
"ds_read_b64_tr_b16 a[210:211], v246 offset:16896\n"
"v_accvgpr_write_b32 a17, v145\n"
"v_mfma_f32_16x16x32_bf16 v[164:167], a[140:143], a[172:175], v[164:167]\n"
"ds_read_b64_tr_b16 a[212:213], v246 offset:24576\n"
"v_accvgpr_write_b32 a19, v147\n"
"v_mfma_f32_16x16x32_bf16 v[160:163], a[128:131], a[168:171], v[228:231]\n"
"v_pk_fma_f32 v[176:177], v[176:177], s[70:71], s[70:71] op_sel:[0,0,1] op_sel_hi:[1,0,1]\nv_pk_fma_f32 v[178:179], v[178:179], s[70:71], s[70:71] op_sel:[0,0,1] op_sel_hi:[1,0,1]\nv_pk_fma_f32 v[180:181], v[180:181], s[70:71], s[70:71] op_sel:[0,0,1] op_sel_hi:[1,0,1]\nv_pk_fma_f32 v[182:183], v[182:183], s[70:71], s[70:71] op_sel:[0,0,1] op_sel_hi:[1,0,1]\nv_pk_fma_f32 v[184:185], v[184:185], s[70:71], s[70:71] op_sel:[0,0,1] op_sel_hi:[1,0,1]\nv_pk_fma_f32 v[186:187], v[186:187], s[70:71], s[70:71] op_sel:[0,0,1] op_sel_hi:[1,0,1]\nv_pk_fma_f32 v[188:189], v[188:189], s[70:71], s[70:71] op_sel:[0,0,1] op_sel_hi:[1,0,1]\nv_pk_fma_f32 v[190:191], v[190:191], s[70:71], s[70:71] op_sel:[0,0,1] op_sel_hi:[1,0,1]\nv_pk_fma_f32 v[192:193], v[192:193], s[70:71], s[70:71] op_sel:[0,0,1] op_sel_hi:[1,0,1]\nv_pk_fma_f32 v[194:195], v[194:195], s[70:71], s[70:71] op_sel:[0,0,1] op_sel_hi:[1,0,1]\nv_pk_fma_f32 v[196:197], v[196:197], s[70:71], s[70:71] op_sel:[0,0,1] op_sel_hi:[1,0,1]\nv_pk_fma_f32 v[198:199], v[198:199], s[70:71], s[70:71] op_sel:[0,0,1] op_sel_hi:[1,0,1]\nv_pk_fma_f32 v[200:201], v[200:201], s[70:71], s[70:71] op_sel:[0,0,1] op_sel_hi:[1,0,1]\nv_pk_fma_f32 v[202:203], v[202:203], s[70:71], s[70:71] op_sel:[0,0,1] op_sel_hi:[1,0,1]\nv_pk_fma_f32 v[204:205], v[204:205], s[70:71], s[70:71] op_sel:[0,0,1] op_sel_hi:[1,0,1]\nv_pk_fma_f32 v[206:207], v[206:207], s[70:71], s[70:71] op_sel:[0,0,1] op_sel_hi:[1,0,1]\n"
"v_mfma_f32_16x16x32_bf16 v[160:163], a[132:135], a[172:175], v[160:163]\n"
"buffer_load_dwordx4 v227, s[28:31], s40 offen offset:3072 lds\n"
"v_mfma_f32_16x16x32_bf16 v[156:159], a[120:123], a[168:171], v[228:231]\n"
"s_cmp_eq_u32 s47, 8\n"
"ds_read_b64_tr_b16 a[214:215], v246 offset:25088\n"
"v_cvt_pk_bf16_f32 v176, v176, v177\n"
"v_mfma_f32_16x16x32_bf16 v[156:159], a[124:127], a[172:175], v[156:159]\n"
"s_cselect_b32 s41, 524800, s58\n"
"ds_read_b64_tr_b16 a[216:217], v247 offset:16384\n"
"v_cvt_pk_bf16_f32 v177, v178, v179\n"
"v_mfma_f32_16x16x32_bf16 v[152:155], a[112:115], a[168:171], v[228:231]\n"
"s_add_u32 s48, s48, s41\n"
"ds_read_b64_tr_b16 a[218:219], v247 offset:16896\n"
"v_cvt_pk_bf16_f32 v178, v180, v181\n"
"v_mfma_f32_16x16x32_bf16 v[152:155], a[116:119], a[172:175], v[152:155]\n"
"s_sub_u32 s41, s48, 5242880\n"
"ds_read_b64_tr_b16 a[220:221], v247 offset:24576\n"
"v_cvt_pk_bf16_f32 v179, v182, v183\n"
"v_mfma_f32_16x16x32_bf16 v[148:151], a[104:107], a[168:171], v[228:231]\n"
"s_min_u32 s48, s48, s41\n"
"v_permlane16_swap_b32 v176, v178\n"
"v_mfma_f32_16x16x32_bf16 v[148:151], a[108:111], a[172:175], v[148:151]\n"
"v_permlane16_swap_b32 v177, v179\n"
"v_mfma_f32_16x16x32_bf16 v[144:147], a[96:99], a[168:171], v[228:231]\n"
"s_mov_b32 m0, s50\n"
"v_accvgpr_write_b32 a32, v176\n"
"v_accvgpr_write_b32 a33, v177\n"
"v_mfma_f32_16x16x32_bf16 v[144:147], a[100:103], a[172:175], v[144:147]\n"
"buffer_load_dwordx4 v224, s[28:31], s48 offen lds\n"
"v_accvgpr_write_b32 a34, v178\n"
"v_accvgpr_write_b32 a35, v179\n"
"v_mfma_f32_16x16x32_bf16 v[176:179], a[96:99], a[176:179], v[228:231]\n"
"v_permlane16_swap_b32 v216, v218\n"
"v_permlane16_swap_b32 v217, v219\n"
"v_mfma_f32_16x16x32_bf16 v[176:179], a[100:103], a[180:183], v[176:179]\n"
"ds_write_b128 v243, v[216:219] offset:2048\n"
"v_mfma_f32_16x16x32_bf16 v[180:183], a[104:107], a[176:179], v[228:231]\n"
"v_cvt_pk_bf16_f32 v184, v184, v185\n"
"v_cvt_pk_bf16_f32 v185, v186, v187\n"
"v_cvt_pk_bf16_f32 v186, v188, v189\n"
"v_cvt_pk_bf16_f32 v187, v190, v191\n"
"v_mfma_f32_16x16x32_bf16 v[180:183], a[108:111], a[180:183], v[180:183]\n"
"buffer_load_dwordx4 v225, s[28:31], s48 offen offset:1024 lds\n"
"v_permlane16_swap_b32 v184, v186\n"
"v_permlane16_swap_b32 v185, v187\n"
"v_accvgpr_write_b32 a36, v184\n"
"v_accvgpr_write_b32 a37, v185\n"
"v_accvgpr_write_b32 a38, v186\n"
"v_accvgpr_write_b32 a39, v187\n"
"v_mfma_f32_16x16x32_bf16 v[184:187], a[112:115], a[176:179], v[228:231]\n"
"v_permlane16_swap_b32 v208, v210\n"
"v_mfma_f32_16x16x32_bf16 v[184:187], a[116:119], a[180:183], v[184:187]\n"
"v_permlane16_swap_b32 v209, v211\n"
"v_mfma_f32_16x16x32_bf16 v[188:191], a[120:123], a[176:179], v[228:231]\n"
"ds_write_b128 v243, v[208:211] offset:0\n"
"v_cvt_pk_bf16_f32 v200, v200, v201\n"
"v_cvt_pk_bf16_f32 v201, v202, v203\n"
"v_cvt_pk_bf16_f32 v202, v204, v205\n"
"v_cvt_pk_bf16_f32 v203, v206, v207\n"
"v_mfma_f32_16x16x32_bf16 v[188:191], a[124:127], a[180:183], v[188:191]\n"
"v_permlane16_swap_b32 v200, v202\n"
"v_permlane16_swap_b32 v201, v203\n"
"v_accvgpr_write_b32 a44, v200\n"
"v_accvgpr_write_b32 a45, v201\n"
"v_accvgpr_write_b32 a46, v202\n"
"v_accvgpr_write_b32 a47, v203\n"
"v_mfma_f32_16x16x32_bf16 v[200:203], a[144:147], a[176:179], v[228:231]\n"
"ds_read_b64_tr_b16 a[222:223], v247 offset:25088\n"
"v_cvt_pk_bf16_f32 v192, v192, v193\n"
"v_mfma_f32_16x16x32_bf16 v[200:203], a[148:151], a[180:183], v[200:203]\n"
"buffer_load_dwordx4 v226, s[28:31], s48 offen offset:2048 lds\n"
"v_mfma_f32_16x16x32_bf16 v[204:207], a[152:155], a[176:179], v[228:231]\n"
"ds_read_b64_tr_b16 a[224:225], v244 offset:17408\n"
"v_cvt_pk_bf16_f32 v193, v194, v195\n"
"v_cvt_pk_bf16_f32 v194, v196, v197\n"
"v_cvt_pk_bf16_f32 v195, v198, v199\n"
"v_mfma_f32_16x16x32_bf16 v[204:207], a[156:159], a[180:183], v[204:207]\n"
"v_permlane16_swap_b32 v192, v194\n"
"v_permlane16_swap_b32 v193, v195\n"
"v_accvgpr_write_b32 a40, v192\n"
"v_accvgpr_write_b32 a41, v193\n"
"v_accvgpr_write_b32 a42, v194\n"
"v_accvgpr_write_b32 a43, v195\n"
"v_mfma_f32_16x16x32_bf16 v[192:195], a[128:131], a[176:179], v[228:231]\n"
"ds_read_b64_tr_b16 a[226:227], v244 offset:17920\n"
"v_xor_b32 v250, s74, v250\n"
"v_mfma_f32_16x16x32_bf16 v[192:195], a[132:135], a[180:183], v[192:195]\n"
"ds_read_b64_tr_b16 a[228:229], v244 offset:25600\n"
"v_xor_b32 v251, s74, v251\n"
"v_mfma_f32_16x16x32_bf16 v[196:199], a[136:139], a[176:179], v[228:231]\n"
"ds_read_b64_tr_b16 a[230:231], v244 offset:26112\n"
"v_xor_b32 v244, s74, v244\n"
"v_mfma_f32_16x16x32_bf16 v[196:199], a[140:143], a[180:183], v[196:199]\n"
"buffer_load_dwordx4 v227, s[28:31], s48 offen offset:3072 lds\n"
"v_mfma_f32_16x16x32_bf16 v[220:223], a[136:139], a[184:187], v[228:231]\n"
"s_xor_b32 s50, s50, s74\n"
"ds_read_b64_tr_b16 a[232:233], v245 offset:17408\n"
"ds_read_b64_tr_b16 a[234:235], v245 offset:17920\n"
"v_mfma_f32_16x16x32_bf16 v[220:223], a[140:143], a[188:191], v[220:223]\n"
"ds_read_b64_tr_b16 a[236:237], v245 offset:25600\n"
"ds_read_b64_tr_b16 a[238:239], v245 offset:26112\n"
"v_mfma_f32_16x16x32_bf16 v[216:219], a[128:131], a[184:187], v[228:231]\n"
"ds_read_b64_tr_b16 a[240:241], v246 offset:17408\n"
"ds_read_b64_tr_b16 a[242:243], v246 offset:17920\n"
"v_mfma_f32_16x16x32_bf16 v[216:219], a[132:135], a[188:191], v[216:219]\n"
"ds_read_b64_tr_b16 a[244:245], v246 offset:25600\n"
"ds_read_b64_tr_b16 a[246:247], v246 offset:26112\n"
"v_mfma_f32_16x16x32_bf16 v[212:215], a[104:107], a[184:187], v[228:231]\n"
"ds_read_b64_tr_b16 a[248:249], v247 offset:17408\n"
"ds_read_b64_tr_b16 a[250:251], v247 offset:17920\n"
"v_mfma_f32_16x16x32_bf16 v[212:215], a[108:111], a[188:191], v[212:215]\n"
"ds_read_b64_tr_b16 a[252:253], v247 offset:25600\n"
"ds_read_b64_tr_b16 a[254:255], v247 offset:26112\n"
"v_mfma_f32_16x16x32_bf16 v[208:211], a[96:99], a[184:187], v[228:231]\n"
"v_pk_fma_f32 v[0:1], v[0:1], s[70:71], s[70:71] op_sel:[0,0,1] op_sel_hi:[1,0,1]\nv_pk_fma_f32 v[2:3], v[2:3], s[70:71], s[70:71] op_sel:[0,0,1] op_sel_hi:[1,0,1]\nv_pk_fma_f32 v[4:5], v[4:5], s[70:71], s[70:71] op_sel:[0,0,1] op_sel_hi:[1,0,1]\nv_pk_fma_f32 v[6:7], v[6:7], s[70:71], s[70:71] op_sel:[0,0,1] op_sel_hi:[1,0,1]\nv_pk_fma_f32 v[8:9], v[8:9], s[70:71], s[70:71] op_sel:[0,0,1] op_sel_hi:[1,0,1]\nv_pk_fma_f32 v[10:11], v[10:11], s[70:71], s[70:71] op_sel:[0,0,1] op_sel_hi:[1,0,1]\nv_pk_fma_f32 v[12:13], v[12:13], s[70:71], s[70:71] op_sel:[0,0,1] op_sel_hi:[1,0,1]\nv_pk_fma_f32 v[14:15], v[14:15], s[70:71], s[70:71] op_sel:[0,0,1] op_sel_hi:[1,0,1]\nv_pk_fma_f32 v[16:17], v[16:17], s[70:71], s[70:71] op_sel:[0,0,1] op_sel_hi:[1,0,1]\nv_pk_fma_f32 v[18:19], v[18:19], s[70:71], s[70:71] op_sel:[0,0,1] op_sel_hi:[1,0,1]\nv_pk_fma_f32 v[20:21], v[20:21], s[70:71], s[70:71] op_sel:[0,0,1] op_sel_hi:[1,0,1]\nv_pk_fma_f32 v[22:23], v[22:23], s[70:71], s[70:71] op_sel:[0,0,1] op_sel_hi:[1,0,1]\nv_pk_fma_f32 v[24:25], v[24:25], s[70:71], s[70:71] op_sel:[0,0,1] op_sel_hi:[1,0,1]\nv_pk_fma_f32 v[26:27], v[26:27], s[70:71], s[70:71] op_sel:[0,0,1] op_sel_hi:[1,0,1]\nv_pk_fma_f32 v[28:29], v[28:29], s[70:71], s[70:71] op_sel:[0,0,1] op_sel_hi:[1,0,1]\nv_pk_fma_f32 v[30:31], v[30:31], s[70:71], s[70:71] op_sel:[0,0,1] op_sel_hi:[1,0,1]\n"
"v_cvt_pk_bf16_f32 v0, v0, v1\n"
"v_cvt_pk_bf16_f32 v1, v2, v3\n"
"v_cvt_pk_bf16_f32 v2, v4, v5\n"
"v_cvt_pk_bf16_f32 v3, v6, v7\n"
"v_mfma_f32_16x16x32_bf16 v[208:211], a[100:103], a[188:191], v[208:211]\n"
"v_permlane16_swap_b32 v0, v2\n"
"v_permlane16_swap_b32 v1, v3\n"
"v_accvgpr_write_b32 a48, v0\n"
"v_accvgpr_write_b32 a49, v1\n"
"v_accvgpr_write_b32 a50, v2\n"
"v_accvgpr_write_b32 a51, v3\n"
"v_mfma_f32_16x16x32_bf16 v[0:3], a[192:195], a[160:163], v[228:231]\n"
"s_waitcnt lgkmcnt(10)\n"
"v_cvt_pk_bf16_f32 v8, v8, v9\n"
"v_cvt_pk_bf16_f32 v9, v10, v11\n"
"v_mfma_f32_16x16x32_bf16 v[0:3], a[196:199], a[164:167], v[0:3]\n"
"v_cvt_pk_bf16_f32 v10, v12, v13\n"
"v_cvt_pk_bf16_f32 v11, v14, v15\n"
"v_mfma_f32_16x16x32_bf16 v[4:7], a[200:203], a[160:163], v[228:231]\n"
"s_waitcnt vmcnt(10) lgkmcnt(0)\n"
"v_permlane16_swap_b32 v8, v10\n"
"v_mfma_f32_16x16x32_bf16 v[4:7], a[204:207], a[164:167], v[4:7]\n"
"s_barrier\n"
"v_permlane16_swap_b32 v9, v11\n"
"v_accvgpr_write_b32 a52, v8\n"
"v_accvgpr_write_b32 a53, v9\n"
"v_accvgpr_write_b32 a54, v10\n"
"v_accvgpr_write_b32 a55, v11\n"
"v_mfma_f32_16x16x32_bf16 v[8:11], a[208:211], a[160:163], v[228:231]\n"
"s_mov_b32 m0, s51\n"
"ds_read_b64_tr_b16 a[96:97], v244 offset:0\n"
"v_cvt_pk_bf16_f32 v16, v16, v17\n"
"v_mfma_f32_16x16x32_bf16 v[8:11], a[212:215], a[164:167], v[8:11]\n"
"buffer_load_dwordx4 v248, s[24:27], s49 offen lds\n"
"v_mfma_f32_16x16x32_bf16 v[12:15], a[216:219], a[160:163], v[228:231]\n"
"ds_read_b64_tr_b16 a[98:99], v244 offset:512\n"
"v_cvt_pk_bf16_f32 v17, v18, v19\n"
"v_cvt_pk_bf16_f32 v18, v20, v21\n"
"v_cvt_pk_bf16_f32 v19, v22, v23\n"
"v_mfma_f32_16x16x32_bf16 v[12:15], a[220:223], a[164:167], v[12:15]\n"
"v_permlane16_swap_b32 v16, v18\n"
"v_permlane16_swap_b32 v17, v19\n"
"v_accvgpr_write_b32 a56, v16\n"
"v_accvgpr_write_b32 a57, v17\n"
"v_accvgpr_write_b32 a58, v18\n"
"v_accvgpr_write_b32 a59, v19\n"
"v_mfma_f32_16x16x32_bf16 v[16:19], a[224:227], a[160:163], v[228:231]\n"
"ds_read_b64_tr_b16 a[100:101], v244 offset:8192\n"
"v_cvt_pk_bf16_f32 v24, v24, v25\n"
"v_mfma_f32_16x16x32_bf16 v[16:19], a[228:231], a[164:167], v[16:19]\n"
"ds_read_b64_tr_b16 a[102:103], v244 offset:8704\n"
"v_cvt_pk_bf16_f32 v25, v26, v27\n"
"v_mfma_f32_16x16x32_bf16 v[20:23], a[232:235], a[160:163], v[228:231]\n"
"ds_read_b64_tr_b16 a[128:129], v244 offset:1024\n"
"v_cvt_pk_bf16_f32 v26, v28, v29\n"
"v_cvt_pk_bf16_f32 v27, v30, v31\n"
"v_mfma_f32_16x16x32_bf16 v[20:23], a[236:239], a[164:167], v[20:23]\n"
"buffer_load_dwordx4 v249, s[24:27], s49 offen offset:1024 lds\n"
"v_permlane16_swap_b32 v24, v26\n"
"v_permlane16_swap_b32 v25, v27\n"
"v_accvgpr_write_b32 a60, v24\n"
"v_accvgpr_write_b32 a61, v25\n"
"v_accvgpr_write_b32 a62, v26\n"
"v_accvgpr_write_b32 a63, v27\n"
"v_mfma_f32_16x16x32_bf16 v[24:27], a[240:243], a[160:163], v[228:231]\n"
"ds_read_b64_tr_b16 a[130:131], v244 offset:1536\n"
"v_xor_b32 v245, s74, v245\n"
"v_mfma_f32_16x16x32_bf16 v[24:27], a[244:247], a[164:167], v[24:27]\n"
"ds_read_b64_tr_b16 a[104:105], v245 offset:0\n"
"v_xor_b32 v246, s74, v246\n"
"v_mfma_f32_16x16x32_bf16 v[28:31], a[248:251], a[160:163], v[228:231]\n"
"v_pk_fma_f32 v[32:33], v[32:33], s[70:71], s[70:71] op_sel:[0,0,1] op_sel_hi:[1,0,1]\nv_pk_fma_f32 v[34:35], v[34:35], s[70:71], s[70:71] op_sel:[0,0,1] op_sel_hi:[1,0,1]\nv_pk_fma_f32 v[36:37], v[36:37], s[70:71], s[70:71] op_sel:[0,0,1] op_sel_hi:[1,0,1]\nv_pk_fma_f32 v[38:39], v[38:39], s[70:71], s[70:71] op_sel:[0,0,1] op_sel_hi:[1,0,1]\nv_pk_fma_f32 v[40:41], v[40:41], s[70:71], s[70:71] op_sel:[0,0,1] op_sel_hi:[1,0,1]\nv_pk_fma_f32 v[42:43], v[42:43], s[70:71], s[70:71] op_sel:[0,0,1] op_sel_hi:[1,0,1]\nv_pk_fma_f32 v[44:45], v[44:45], s[70:71], s[70:71] op_sel:[0,0,1] op_sel_hi:[1,0,1]\nv_pk_fma_f32 v[46:47], v[46:47], s[70:71], s[70:71] op_sel:[0,0,1] op_sel_hi:[1,0,1]\nv_pk_fma_f32 v[48:49], v[48:49], s[70:71], s[70:71] op_sel:[0,0,1] op_sel_hi:[1,0,1]\nv_pk_fma_f32 v[50:51], v[50:51], s[70:71], s[70:71] op_sel:[0,0,1] op_sel_hi:[1,0,1]\nv_pk_fma_f32 v[52:53], v[52:53], s[70:71], s[70:71] op_sel:[0,0,1] op_sel_hi:[1,0,1]\nv_pk_fma_f32 v[54:55], v[54:55], s[70:71], s[70:71] op_sel:[0,0,1] op_sel_hi:[1,0,1]\nv_pk_fma_f32 v[56:57], v[56:57], s[70:71], s[70:71] op_sel:[0,0,1] op_sel_hi:[1,0,1]\nv_pk_fma_f32 v[58:59], v[58:59], s[70:71], s[70:71] op_sel:[0,0,1] op_sel_hi:[1,0,1]\nv_pk_fma_f32 v[60:61], v[60:61], s[70:71], s[70:71] op_sel:[0,0,1] op_sel_hi:[1,0,1]\nv_pk_fma_f32 v[62:63], v[62:63], s[70:71], s[70:71] op_sel:[0,0,1] op_sel_hi:[1,0,1]\n"
"v_mfma_f32_16x16x32_bf16 v[28:31], a[252:255], a[164:167], v[28:31]\n"
"ds_read_b128 a[160:163], v250 offset:0\n"
"v_cvt_pk_bf16_f32 v56, v56, v57\n"
"v_cvt_pk_bf16_f32 v57, v58, v59\n"
"v_cvt_pk_bf16_f32 v58, v60, v61\n"
"v_cvt_pk_bf16_f32 v59, v62, v63\n"
"v_mfma_f32_16x16x32_bf16 v[60:63], a[248:251], a[168:171], v[228:231]\n"
"v_permlane16_swap_b32 v56, v58\n"
"v_mfma_f32_16x16x32_bf16 v[60:63], a[252:255], a[172:175], v[60:63]\n"
"buffer_load_dwordx4 v254, s[24:27], s49 offen offset:2048 lds\n"
"v_permlane16_swap_b32 v57, v59\n"
"v_accvgpr_write_b32 a76, v56\n"
"v_accvgpr_write_b32 a77, v57\n"
"v_accvgpr_write_b32 a78, v58\n"
"v_accvgpr_write_b32 a79, v59\n"
"v_mfma_f32_16x16x32_bf16 v[56:59], a[240:243], a[168:171], v[228:231]\n"
"ds_read_b128 a[164:167], v251 offset:0\n"
"v_cvt_pk_bf16_f32 v48, v48, v49\n"
"v_mfma_f32_16x16x32_bf16 v[56:59], a[244:247], a[172:175], v[56:59]\n"
"ds_read_b64_tr_b16 a[106:107], v245 offset:512\n"
"v_cvt_pk_bf16_f32 v49, v50, v51\n"
"v_cvt_pk_bf16_f32 v50, v52, v53\n"
"v_cvt_pk_bf16_f32 v51, v54, v55\n"
"v_mfma_f32_16x16x32_bf16 v[52:55], a[232:235], a[168:171], v[228:231]\n"
"v_permlane16_swap_b32 v48, v50\n"
"v_mfma_f32_16x16x32_bf16 v[52:55], a[236:239], a[172:175], v[52:55]\n"
"v_permlane16_swap_b32 v49, v51\n"
"v_accvgpr_write_b32 a72, v48\n"
"v_accvgpr_write_b32 a73, v49\n"
"v_accvgpr_write_b32 a74, v50\n"
"v_accvgpr_write_b32 a75, v51\n"
"v_mfma_f32_16x16x32_bf16 v[48:51], a[224:227], a[168:171], v[228:231]\n"
"ds_read_b64_tr_b16 a[108:109], v245 offset:8192\n"
"v_cvt_pk_bf16_f32 v40, v40, v41\n"
"v_mfma_f32_16x16x32_bf16 v[48:51], a[228:231], a[172:175], v[48:51]\n"
"buffer_load_dwordx4 v255, s[24:27], s49 offen offset:3072 lds\n"
"v_cvt_pk_bf16_f32 v41, v42, v43\n"
"v_cvt_pk_bf16_f32 v42, v44, v45\n"
"v_cvt_pk_bf16_f32 v43, v46, v47\n"
"v_mfma_f32_16x16x32_bf16 v[44:47], a[216:219], a[168:171], v[228:231]\n"
"v_pk_fma_f32 v[96:97], v[96:97], s[70:71], s[70:71] op_sel:[0,0,1] op_sel_hi:[1,0,1]\nv_pk_fma_f32 v[98:99], v[98:99], s[70:71], s[70:71] op_sel:[0,0,1] op_sel_hi:[1,0,1]\nv_pk_fma_f32 v[100:101], v[100:101], s[70:71], s[70:71] op_sel:[0,0,1] op_sel_hi:[1,0,1]\nv_pk_fma_f32 v[102:103], v[102:103], s[70:71], s[70:71] op_sel:[0,0,1] op_sel_hi:[1,0,1]\nv_pk_fma_f32 v[104:105], v[104:105], s[70:71], s[70:71] op_sel:[0,0,1] op_sel_hi:[1,0,1]\nv_pk_fma_f32 v[106:107], v[106:107], s[70:71], s[70:71] op_sel:[0,0,1] op_sel_hi:[1,0,1]\nv_pk_fma_f32 v[108:109], v[108:109], s[70:71], s[70:71] op_sel:[0,0,1] op_sel_hi:[1,0,1]\nv_pk_fma_f32 v[110:111], v[110:111], s[70:71], s[70:71] op_sel:[0,0,1] op_sel_hi:[1,0,1]\n"
"v_mfma_f32_16x16x32_bf16 v[44:47], a[220:223], a[172:175], v[44:47]\n"
"v_cvt_pk_bf16_f32 v104, v104, v105\n"
"v_cvt_pk_bf16_f32 v105, v106, v107\n"
"v_cvt_pk_bf16_f32 v106, v108, v109\n"
"v_cvt_pk_bf16_f32 v107, v110, v111\n"
"v_permlane16_swap_b32 v40, v42\n"
"v_permlane16_swap_b32 v41, v43\n"
"v_accvgpr_write_b32 a68, v40\n"
"v_accvgpr_write_b32 a69, v41\n"
"v_accvgpr_write_b32 a70, v42\n"
"v_accvgpr_write_b32 a71, v43\n"
"v_mfma_f32_16x16x32_bf16 v[40:43], a[208:211], a[168:171], v[228:231]\n"
"v_permlane16_swap_b32 v104, v106\n"
"v_permlane16_swap_b32 v105, v107\n"
"v_mfma_f32_16x16x32_bf16 v[40:43], a[212:215], a[172:175], v[40:43]\n"
"s_add_u32 s40, s49, 40960\n"
"ds_write_b128 v243, v[104:107] offset:6144\n"
"v_cvt_pk_bf16_f32 v32, v32, v33\n"
"v_cvt_pk_bf16_f32 v33, v34, v35\n"
"v_cvt_pk_bf16_f32 v34, v36, v37\n"
"v_cvt_pk_bf16_f32 v35, v38, v39\n"
"v_mfma_f32_16x16x32_bf16 v[36:39], a[200:203], a[168:171], v[228:231]\n"
"s_add_u32 m0, s51, 4096\n"
"v_permlane16_swap_b32 v32, v34\n"
"v_mfma_f32_16x16x32_bf16 v[36:39], a[204:207], a[172:175], v[36:39]\n"
"buffer_load_dwordx4 v248, s[24:27], s40 offen lds\n"
"v_permlane16_swap_b32 v33, v35\n"
"v_accvgpr_write_b32 a64, v32\n"
"v_accvgpr_write_b32 a65, v33\n"
"v_accvgpr_write_b32 a66, v34\n"
"v_accvgpr_write_b32 a67, v35\n"
"v_mfma_f32_16x16x32_bf16 v[32:35], a[192:195], a[168:171], v[228:231]\n"
"v_pk_fma_f32 v[64:65], v[64:65], s[70:71], s[70:71] op_sel:[0,0,1] op_sel_hi:[1,0,1]\nv_pk_fma_f32 v[66:67], v[66:67], s[70:71], s[70:71] op_sel:[0,0,1] op_sel_hi:[1,0,1]\nv_pk_fma_f32 v[68:69], v[68:69], s[70:71], s[70:71] op_sel:[0,0,1] op_sel_hi:[1,0,1]\nv_pk_fma_f32 v[70:71], v[70:71], s[70:71], s[70:71] op_sel:[0,0,1] op_sel_hi:[1,0,1]\nv_pk_fma_f32 v[72:73], v[72:73], s[70:71], s[70:71] op_sel:[0,0,1] op_sel_hi:[1,0,1]\nv_pk_fma_f32 v[74:75], v[74:75], s[70:71], s[70:71] op_sel:[0,0,1] op_sel_hi:[1,0,1]\nv_pk_fma_f32 v[76:77], v[76:77], s[70:71], s[70:71] op_sel:[0,0,1] op_sel_hi:[1,0,1]\nv_pk_fma_f32 v[78:79], v[78:79], s[70:71], s[70:71] op_sel:[0,0,1] op_sel_hi:[1,0,1]\nv_pk_fma_f32 v[80:81], v[80:81], s[70:71], s[70:71] op_sel:[0,0,1] op_sel_hi:[1,0,1]\nv_pk_fma_f32 v[82:83], v[82:83], s[70:71], s[70:71] op_sel:[0,0,1] op_sel_hi:[1,0,1]\nv_pk_fma_f32 v[84:85], v[84:85], s[70:71], s[70:71] op_sel:[0,0,1] op_sel_hi:[1,0,1]\nv_pk_fma_f32 v[86:87], v[86:87], s[70:71], s[70:71] op_sel:[0,0,1] op_sel_hi:[1,0,1]\nv_pk_fma_f32 v[88:89], v[88:89], s[70:71], s[70:71] op_sel:[0,0,1] op_sel_hi:[1,0,1]\nv_pk_fma_f32 v[90:91], v[90:91], s[70:71], s[70:71] op_sel:[0,0,1] op_sel_hi:[1,0,1]\nv_pk_fma_f32 v[92:93], v[92:93], s[70:71], s[70:71] op_sel:[0,0,1] op_sel_hi:[1,0,1]\nv_pk_fma_f32 v[94:95], v[94:95], s[70:71], s[70:71] op_sel:[0,0,1] op_sel_hi:[1,0,1]\n"
"v_cvt_pk_bf16_f32 v64, v64, v65\n"
"v_cvt_pk_bf16_f32 v65, v66, v67\n"
"v_cvt_pk_bf16_f32 v66, v68, v69\n"
"v_cvt_pk_bf16_f32 v67, v70, v71\n"
"v_mfma_f32_16x16x32_bf16 v[32:35], a[196:199], a[172:175], v[32:35]\n"
"v_permlane16_swap_b32 v64, v66\n"
"v_permlane16_swap_b32 v65, v67\n"
"v_accvgpr_write_b32 a80, v64\n"
"v_accvgpr_write_b32 a81, v65\n"
"v_accvgpr_write_b32 a82, v66\n"
"v_accvgpr_write_b32 a83, v67\n"
"v_mfma_f32_16x16x32_bf16 v[64:67], a[192:195], a[176:179], v[228:231]\n"
"ds_read_b64_tr_b16 a[110:111], v245 offset:8704\n"
"v_cvt_pk_bf16_f32 v96, v96, v97\n"
"v_cvt_pk_bf16_f32 v97, v98, v99\n"
"v_cvt_pk_bf16_f32 v98, v100, v101\n"
"v_cvt_pk_bf16_f32 v99, v102, v103\n"
"v_mfma_f32_16x16x32_bf16 v[64:67], a[196:199], a[180:183], v[64:67]\n"
"v_permlane16_swap_b32 v96, v98\n"
"v_permlane16_swap_b32 v97, v99\n"
"v_mfma_f32_16x16x32_bf16 v[68:71], a[200:203], a[176:179], v[228:231]\n"
"ds_write_b128 v243, v[96:99] offset:4096\n"
"v_cvt_pk_bf16_f32 v72, v72, v73\n"
"v_cvt_pk_bf16_f32 v73, v74, v75\n"
"v_cvt_pk_bf16_f32 v74, v76, v77\n"
"v_cvt_pk_bf16_f32 v75, v78, v79\n"
"v_mfma_f32_16x16x32_bf16 v[68:71], a[204:207], a[180:183], v[68:71]\n"
"buffer_load_dwordx4 v249, s[24:27], s40 offen offset:1024 lds\n"
"v_permlane16_swap_b32 v72, v74\n"
"v_permlane16_swap_b32 v73, v75\n"
"v_accvgpr_write_b32 a84, v72\n"
"v_accvgpr_write_b32 a85, v73\n"
"v_accvgpr_write_b32 a86, v74\n"
"v_accvgpr_write_b32 a87, v75\n"
"v_mfma_f32_16x16x32_bf16 v[72:75], a[208:211], a[176:179], v[228:231]\n"
"ds_read_b64_tr_b16 a[112:113], v246 offset:0\n"
"v_cvt_pk_bf16_f32 v88, v88, v89\n"
"v_mfma_f32_16x16x32_bf16 v[72:75], a[212:215], a[180:183], v[72:75]\n"
"ds_read_b64_tr_b16 a[114:115], v246 offset:512\n"
"v_cvt_pk_bf16_f32 v89, v90, v91\n"
"v_mfma_f32_16x16x32_bf16 v[76:79], a[216:219], a[176:179], v[228:231]\n"
"ds_read_b64_tr_b16 a[116:117], v246 offset:8192\n"
"ds_read_b64_tr_b16 a[118:119], v246 offset:8704\n"
"v_cvt_pk_bf16_f32 v90, v92, v93\n"
"v_cvt_pk_bf16_f32 v91, v94, v95\n"
"v_xor_b32 v247, s74, v247\n"
"v_mfma_f32_16x16x32_bf16 v[76:79], a[220:223], a[180:183], v[76:79]\n"
"ds_read_b64_tr_b16 a[120:121], v247 offset:0\n"
"ds_read_b64_tr_b16 a[122:123], v247 offset:512\n"
"v_permlane16_swap_b32 v88, v90\n"
"v_permlane16_swap_b32 v89, v91\n"
"v_accvgpr_write_b32 a92, v88\n"
"v_accvgpr_write_b32 a93, v89\n"
"v_accvgpr_write_b32 a94, v90\n"
"v_accvgpr_write_b32 a95, v91\n"
"v_mfma_f32_16x16x32_bf16 v[88:91], a[240:243], a[176:179], v[228:231]\n"
"ds_read_b64_tr_b16 a[124:125], v247 offset:8192\n"
"ds_read_b64_tr_b16 a[126:127], v247 offset:8704\n"
"v_mfma_f32_16x16x32_bf16 v[88:91], a[244:247], a[180:183], v[88:91]\n"
"buffer_load_dwordx4 v254, s[24:27], s40 offen offset:2048 lds\n"
"v_mfma_f32_16x16x32_bf16 v[92:95], a[248:251], a[176:179], v[228:231]\n"
"s_add_u32 s49, s49, 128\n"
"ds_read_b64_tr_b16 a[132:133], v244 offset:9216\n"
"v_cvt_pk_bf16_f32 v80, v80, v81\n"
"v_cvt_pk_bf16_f32 v81, v82, v83\n"
"v_cvt_pk_bf16_f32 v82, v84, v85\n"
"v_cvt_pk_bf16_f32 v83, v86, v87\n"
"v_mfma_f32_16x16x32_bf16 v[92:95], a[252:255], a[180:183], v[92:95]\n"
"s_sub_u32 s41, s49, 1280\n"
"ds_read_b64_tr_b16 a[134:135], v244 offset:9728\n"
"v_permlane16_swap_b32 v80, v82\n"
"v_permlane16_swap_b32 v81, v83\n"
"v_accvgpr_write_b32 a88, v80\n"
"v_accvgpr_write_b32 a89, v81\n"
"v_accvgpr_write_b32 a90, v82\n"
"v_accvgpr_write_b32 a91, v83\n"
"v_mfma_f32_16x16x32_bf16 v[80:83], a[224:227], a[176:179], v[228:231]\n"
"s_min_u32 s49, s49, s41\n"
"ds_read_b64_tr_b16 a[136:137], v245 offset:1024\n"
"ds_read_b64_tr_b16 a[138:139], v245 offset:1536\n"
"v_mfma_f32_16x16x32_bf16 v[80:83], a[228:231], a[180:183], v[80:83]\n"
"s_xor_b32 s51, s51, s74\n"
"ds_read_b64_tr_b16 a[140:141], v245 offset:9216\n"
"ds_read_b64_tr_b16 a[142:143], v245 offset:9728\n"
"v_mfma_f32_16x16x32_bf16 v[84:87], a[232:235], a[176:179], v[228:231]\n"
"ds_read_b64_tr_b16 a[144:145], v246 offset:1024\n"
"ds_read_b64_tr_b16 a[146:147], v246 offset:1536\n"
"v_mfma_f32_16x16x32_bf16 v[84:87], a[236:239], a[180:183], v[84:87]\n"
"ds_read_b64_tr_b16 a[148:149], v246 offset:9216\n"
"ds_read_b64_tr_b16 a[150:151], v246 offset:9728\n"
"v_mfma_f32_16x16x32_bf16 v[108:111], a[232:235], a[184:187], v[228:231]\n"
"ds_read_b64_tr_b16 a[152:153], v247 offset:1024\n"
"ds_read_b64_tr_b16 a[154:155], v247 offset:1536\n"
"v_mfma_f32_16x16x32_bf16 v[108:111], a[236:239], a[188:191], v[108:111]\n"
"ds_read_b64_tr_b16 a[156:157], v247 offset:9216\n"
"ds_read_b64_tr_b16 a[158:159], v247 offset:9728\n"
"v_mfma_f32_16x16x32_bf16 v[104:107], a[224:227], a[184:187], v[228:231]\n"
"ds_read_b128 a[168:171], v250 offset:2048\n"
"v_xor_b32 v252, s74, v252\n"
"v_mfma_f32_16x16x32_bf16 v[104:107], a[228:231], a[188:191], v[104:107]\n"
"ds_read_b128 a[172:175], v251 offset:2048\n"
"v_xor_b32 v253, s74, v253\n"
"v_mfma_f32_16x16x32_bf16 v[100:103], a[200:203], a[184:187], v[228:231]\n"
"ds_read_b128 a[176:179], v250 offset:4096\n"
"v_mfma_f32_16x16x32_bf16 v[100:103], a[204:207], a[188:191], v[100:103]\n"
"ds_read_b128 a[180:183], v251 offset:4096\n"
"v_mfma_f32_16x16x32_bf16 v[96:99], a[192:195], a[184:187], v[228:231]\n"
"s_add_u32 s47, s47, 1\n"
"ds_read_b128 a[184:187], v252 offset:0\n"
"v_mfma_f32_16x16x32_bf16 v[96:99], a[196:199], a[188:191], v[96:99]\n"
"s_waitcnt lgkmcnt(9)\n"
"ds_read_b128 a[188:191], v253 offset:0\n"
"s_mov_b32 s62, 18\n"
"s_branch LPL_%=\n"
"LPT2_%=:\n"
"v_mfma_f32_16x16x32_bf16 v[112:115], a[96:99], a[160:163], v[112:115]\n"
"s_add_u32 s78, s32, s96\ns_addc_u32 s79, s33, 0\n"
"s_mov_b64 s[80:81], 0\n"
"s_mov_b32 s82, 0\n"
"v_mfma_f32_16x16x32_bf16 v[112:115], a[100:103], a[164:167], v[112:115]\n"
"v_bfe_u32 v228, v242, 8, 2\n"
"v_and_b32 v229, 0x60, v242\n"
"v_mfma_f32_16x16x32_bf16 v[116:119], a[104:107], a[160:163], v[116:119]\n"
"s_waitcnt vmcnt(11)\n"
"s_load_dword s80, s[78:79], 0\n"
"v_lshlrev_b32 v228, 4, v228\n"
"v_mfma_f32_16x16x32_bf16 v[116:119], a[108:111], a[164:167], v[116:119]\n"
"s_barrier\n"
"v_mfma_f32_16x16x32_bf16 v[120:123], a[112:115], a[160:163], v[120:123]\n"
"s_add_u32 s40, s48, 256\n"
"s_xor_b32 m0, s50, 0xc000\n"
"ds_read_b64_tr_b16 a[192:193], v244 offset:16384\n"
"v_mfma_f32_16x16x32_bf16 v[120:123], a[116:119], a[164:167], v[120:123]\n"
"buffer_load_dwordx4 v224, s[28:31], s40 offen lds\n"
"v_mfma_f32_16x16x32_bf16 v[124:127], a[120:123], a[160:163], v[124:127]\n"
"ds_read_b64_tr_b16 a[194:195], v244 offset:16896\n"
"v_xor_b32 v228, v228, v229\n"
"v_mfma_f32_16x16x32_bf16 v[124:127], a[124:127], a[164:167], v[124:127]\n"
"ds_read_b64_tr_b16 a[196:197], v244 offset:24576\n"
"v_and_b32 v229, 0x70, v242\n"
"v_mfma_f32_16x16x32_bf16 v[128:131], a[128:131], a[160:163], v[128:131]\n"
"ds_read_b64_tr_b16 a[198:199], v244 offset:25088\n"
"v_lshl_add_u32 v228, v229, 3, v228\n"
"v_mfma_f32_16x16x32_bf16 v[128:131], a[132:135], a[164:167], v[128:131]\n"
"ds_read_b64_tr_b16 a[200:201], v245 offset:16384\n"
"v_and_b32 v229, 0x80, v242\n"
"v_mfma_f32_16x16x32_bf16 v[132:135], a[136:139], a[160:163], v[132:135]\n"
"ds_read_b64_tr_b16 a[202:203], v245 offset:16896\n"
"v_lshl_add_u32 v228, v229, 4, v228\n"
"v_mfma_f32_16x16x32_bf16 v[132:135], a[140:143], a[164:167], v[132:135]\n"
"buffer_load_dwordx4 v225, s[28:31], s40 offen offset:1024 lds\n"
"v_mfma_f32_16x16x32_bf16 v[136:139], a[144:147], a[160:163], v[136:139]\n"
"s_add_u32 s82, s82, s80\n"
"ds_read_b64_tr_b16 a[204:205], v245 offset:24576\n"
"v_mov_b32 v229, s43\n"
"v_mfma_f32_16x16x32_bf16 v[136:139], a[148:151], a[164:167], v[136:139]\n"
"s_add_u32 s82, s82, s80\n"
"ds_read_b64_tr_b16 a[206:207], v245 offset:25088\n"
"v_mul_u32_u24 v229, 0x81, v229\n"
"v_mfma_f32_16x16x32_bf16 v[140:143], a[152:155], a[160:163], v[140:143]\n"
"s_add_u32 s82, s82, s80\n"
"ds_read_b64_tr_b16 a[208:209], v246 offset:16384\n"
"v_xor_b32 v228, v228, v229\n"
"v_mfma_f32_16x16x32_bf16 v[140:143], a[156:159], a[164:167], v[140:143]\n"
"s_add_u32 s82, s82, s80\n"
"ds_read_b64_tr_b16 a[210:211], v246 offset:16896\n"
"v_xor_b32 v229, 64, v228\n"
"v_mfma_f32_16x16x32_bf16 v[172:175], a[152:155], a[168:171], v[172:175]\n"
"s_add_u32 s82, s82, s80\n"
"ds_read_b64_tr_b16 a[212:213], v246 offset:24576\n"
"v_xor_b32 v230, 0x2000, v228\n"
"v_mfma_f32_16x16x32_bf16 v[172:175], a[156:159], a[172:175], v[172:175]\n"
"s_add_u32 s82, s82, s80\n"
"ds_read_b64_tr_b16 a[214:215], v246 offset:25088\n"
"v_xor_b32 v231, 0x2000, v229\n"
"v_mfma_f32_16x16x32_bf16 v[168:171], a[144:147], a[168:171], v[168:171]\n"
"s_add_u32 s82, s82, s80\n"
"ds_read_b64_tr_b16 a[216:217], v247 offset:16384\n"
"v_bfe_i32 v241, v242, 4, 1\n"
"v_mfma_f32_16x16x32_bf16 v[168:171], a[148:151], a[172:175], v[168:171]\n"
"s_add_u32 s82, s82, s80\n"
"buffer_load_dwordx4 v226, s[28:31], s40 offen offset:2048 lds\n"
"v_mfma_f32_16x16x32_bf16 v[164:167], a[136:139], a[168:171], v[164:167]\n"
"s_add_u32 s82, s82, s80\n"
"ds_read_b64_tr_b16 a[218:219], v247 offset:16896\n"
"v_and_b32 v241, 0xaaaaaaaa, v241\n"
"v_mfma_f32_16x16x32_bf16 v[164:167], a[140:143], a[172:175], v[164:167]\n"
"s_add_u32 s82, s82, s80\n"
"ds_read_b64_tr_b16 a[220:221], v247 offset:24576\n"
"v_or_b32 v241, 0x44444444, v241\n"
"v_mfma_f32_16x16x32_bf16 v[160:163], a[128:131], a[168:171], v[160:163]\n"
"s_add_u32 s82, s82, s80\n"
"ds_read_b64_tr_b16 a[222:223], v247 offset:25088\n"
"v_bfe_u32 v248, v242, 8, 2\n"
"v_mfma_f32_16x16x32_bf16 v[160:163], a[132:135], a[172:175], v[160:163]\n"
"s_add_u32 s82, s82, s80\n"
"ds_read_b64_tr_b16 a[224:225], v244 offset:17408\n"
"v_lshlrev_b32 v248, 6, v248\n"
"v_mfma_f32_16x16x32_bf16 v[156:159], a[120:123], a[168:171], v[156:159]\n"
"s_add_u32 s82, s82, s80\n"
"ds_read_b64_tr_b16 a[226:227], v244 offset:17920\n"
"v_xor_b32 v248, v248, v242\n"
"v_mfma_f32_16x16x32_bf16 v[156:159], a[124:127], a[172:175], v[156:159]\n"
"s_add_u32 s82, s82, s80\n"
"buffer_load_dwordx4 v227, s[28:31], s40 offen offset:3072 lds\n"
"v_mfma_f32_16x16x32_bf16 v[152:155], a[112:115], a[168:171], v[152:155]\n"
"s_add_u32 s82, s82, s80\n"
"s_mov_b32 s58, 245760\n"
"ds_read_b64_tr_b16 a[228:229], v244 offset:25600\n"
"v_mfma_f32_16x16x32_bf16 v[152:155], a[116:119], a[172:175], v[152:155]\n"
"s_add_u32 s82, s82, s80\n"
"s_mov_b32 s47, 0\n"
"ds_read_b64_tr_b16 a[230:231], v244 offset:26112\n"
"v_mfma_f32_16x16x32_bf16 v[148:151], a[104:107], a[168:171], v[148:151]\n"
"s_add_u32 s82, s82, s80\n"
"ds_read_b64_tr_b16 a[232:233], v245 offset:17408\n"
"v_bfe_u32 v249, v242, 4, 2\n"
"v_mfma_f32_16x16x32_bf16 v[148:151], a[108:111], a[172:175], v[148:151]\n"
"s_load_dword s81, s[78:79], 0x2000\n"
"ds_read_b64_tr_b16 a[234:235], v245 offset:17920\n"
"v_xor_b32 v244, s74, v244\n"
"v_mfma_f32_16x16x32_bf16 v[144:147], a[96:99], a[168:171], v[144:147]\n"
"s_mov_b32 m0, s44\n"
"ds_read_b64_tr_b16 a[236:237], v245 offset:25600\n"
"v_xor_b32 v250, s74, v250\n"
"v_mfma_f32_16x16x32_bf16 v[144:147], a[100:103], a[172:175], v[144:147]\n"
"buffer_load_dwordx4 v240, s[16:19], s56 offen nt lds\n"
"v_mfma_f32_16x16x32_bf16 v[176:179], a[96:99], a[176:179], v[176:179]\n"
"s_waitcnt lgkmcnt(5)\n"
"ds_read_b64_tr_b16 a[238:239], v245 offset:26112\n"
"v_xor_b32 v251, s74, v251\n"
"v_mfma_f32_16x16x32_bf16 v[176:179], a[100:103], a[180:183], v[176:179]\n"
"ds_read_b64_tr_b16 a[240:241], v246 offset:17408\n"
"v_xor_b32 v245, s74, v245\n"
"v_mfma_f32_16x16x32_bf16 v[180:183], a[104:107], a[176:179], v[180:183]\n"
"ds_read_b64_tr_b16 a[242:243], v246 offset:17920\n"
"v_xor_b32 v252, s74, v252\n"
"v_mfma_f32_16x16x32_bf16 v[180:183], a[108:111], a[180:183], v[180:183]\n"
"ds_read_b64_tr_b16 a[244:245], v246 offset:25600\n"
"v_xor_b32 v253, s74, v253\n"
"v_mfma_f32_16x16x32_bf16 v[184:187], a[112:115], a[176:179], v[184:187]\n"
"ds_read_b64_tr_b16 a[246:247], v246 offset:26112\n"
"v_xor_b32 v246, s74, v246\n"
"v_mfma_f32_16x16x32_bf16 v[184:187], a[116:119], a[180:183], v[184:187]\n"
"ds_read_b64_tr_b16 a[248:249], v247 offset:17408\n"
"v_mul_u32_u24 v249, 20, v249\n"
"v_mfma_f32_16x16x32_bf16 v[188:191], a[120:123], a[176:179], v[188:191]\n"
"s_add_u32 s40, s56, s52\n"
"ds_read_b64_tr_b16 a[250:251], v247 offset:17920\n"
"v_mfma_f32_16x16x32_bf16 v[188:191], a[124:127], a[180:183], v[188:191]\n"
"s_sub_u32 s82, s82, s81\n"
"buffer_load_dwordx4 v240, s[16:19], s40 offen offset:1024 nt lds\n"
"v_mfma_f32_16x16x32_bf16 v[200:203], a[144:147], a[176:179], v[200:203]\n"
"s_sub_u32 s82, s82, s81\n"
"ds_read_b64_tr_b16 a[252:253], v247 offset:25600\n"
"v_mfma_f32_16x16x32_bf16 v[200:203], a[148:151], a[180:183], v[200:203]\n"
"s_sub_u32 s82, s82, s81\n"
"ds_read_b64_tr_b16 a[254:255], v247 offset:26112\n"
"v_xor_b32 v247, s74, v247\n"
"v_mfma_f32_16x16x32_bf16 v[204:207], a[152:155], a[176:179], v[204:207]\n"
"s_sub_u32 s82, s82, s81\n"
"v_mfma_f32_16x16x32_bf16 v[204:207], a[156:159], a[180:183], v[204:207]\n"
"s_sub_u32 s82, s82, s81\n"
"v_mfma_f32_16x16x32_bf16 v[192:195], a[128:131], a[176:179], v[192:195]\n"
"s_sub_u32 s82, s82, s81\n"
"s_add_u32 s40, s56, s53\n"
"v_mfma_f32_16x16x32_bf16 v[192:195], a[132:135], a[180:183], v[192:195]\n"
"s_sub_u32 s82, s82, s81\n"
"buffer_load_dwordx4 v240, s[16:19], s40 offen offset:2048 nt lds\n"
"v_mfma_f32_16x16x32_bf16 v[196:199], a[136:139], a[176:179], v[196:199]\n"
"s_sub_u32 s82, s82, s81\n"
"v_mfma_f32_16x16x32_bf16 v[196:199], a[140:143], a[180:183], v[196:199]\n"
"s_sub_u32 s82, s82, s81\n"
"v_mfma_f32_16x16x32_bf16 v[220:223], a[136:139], a[184:187], v[220:223]\n"
"s_sub_u32 s82, s82, s81\n"
"v_mfma_f32_16x16x32_bf16 v[220:223], a[140:143], a[188:191], v[220:223]\n"
"s_sub_u32 s82, s82, s81\n"
"v_mfma_f32_16x16x32_bf16 v[216:219], a[128:131], a[184:187], v[216:219]\n"
"s_sub_u32 s82, s82, s81\n"
"s_add_u32 s40, s56, s54\n"
"v_mfma_f32_16x16x32_bf16 v[216:219], a[132:135], a[188:191], v[216:219]\n"
"s_sub_u32 s82, s82, s81\n"
"buffer_load_dwordx4 v240, s[16:19], s40 offen offset:3072 nt lds\n"
"v_mfma_f32_16x16x32_bf16 v[212:215], a[104:107], a[184:187], v[212:215]\n"
"s_sub_u32 s82, s82, s81\n"
"v_mfma_f32_16x16x32_bf16 v[212:215], a[108:111], a[188:191], v[212:215]\n"
"s_sub_u32 s82, s82, s81\n"
"v_mfma_f32_16x16x32_bf16 v[208:211], a[96:99], a[184:187], v[208:211]\n"
"s_sub_u32 s82, s82, s81\n"
"v_mfma_f32_16x16x32_bf16 v[208:211], a[100:103], a[188:191], v[208:211]\n"
"s_sub_u32 s82, s82, s81\n"
"v_mfma_f32_16x16x32_bf16 v[0:3], a[192:195], a[160:163], v[0:3]\n"
"v_mfma_f32_16x16x32_bf16 v[0:3], a[196:199], a[164:167], v[0:3]\n"
"v_mfma_f32_16x16x32_bf16 v[4:7], a[200:203], a[160:163], v[4:7]\n"
"s_waitcnt vmcnt(8) lgkmcnt(0)\n"
"v_mfma_f32_16x16x32_bf16 v[4:7], a[204:207], a[164:167], v[4:7]\n"
"s_barrier\n"
"v_mfma_f32_16x16x32_bf16 v[8:11], a[208:211], a[160:163], v[8:11]\n"
"s_add_u32 m0, s44, 65536\n"
"ds_read_b64_tr_b16 a[96:97], v244 offset:0\n"
"v_mfma_f32_16x16x32_bf16 v[8:11], a[212:215], a[164:167], v[8:11]\n"
"buffer_load_dwordx4 v240, s[20:23], s56 offen nt lds\n"
"v_mfma_f32_16x16x32_bf16 v[12:15], a[216:219], a[160:163], v[12:15]\n"
"s_lshr_b32 s82, s82, 31\n"
"ds_read_b64_tr_b16 a[98:99], v244 offset:512\n"
"v_mfma_f32_16x16x32_bf16 v[12:15], a[220:223], a[164:167], v[12:15]\n"
"s_add_u32 s82, s82, s60\n"
"ds_read_b64_tr_b16 a[100:101], v244 offset:8192\n"
"v_mfma_f32_16x16x32_bf16 v[16:19], a[224:227], a[160:163], v[16:19]\n"
"s_add_u32 s82, s82, 32\n"
"ds_read_b64_tr_b16 a[102:103], v244 offset:8704\n"
"v_mfma_f32_16x16x32_bf16 v[16:19], a[228:231], a[164:167], v[16:19]\n"
"v_mov_b32 v254, s82\n"
"ds_read_b64_tr_b16 a[104:105], v245 offset:0\n"
"v_mfma_f32_16x16x32_bf16 v[20:23], a[232:235], a[160:163], v[20:23]\n"
"v_lshlrev_b32_e64 v255, 2, s93\n"
"ds_read_b64_tr_b16 a[106:107], v245 offset:512\n"
"v_mfma_f32_16x16x32_bf16 v[20:23], a[236:239], a[164:167], v[20:23]\n"
"ds_read_b64_tr_b16 a[108:109], v245 offset:8192\n"
"v_mfma_f32_16x16x32_bf16 v[24:27], a[240:243], a[160:163], v[24:27]\n"
"s_add_u32 s40, s56, s52\n"
"ds_read_b64_tr_b16 a[110:111], v245 offset:8704\n"
"v_mfma_f32_16x16x32_bf16 v[24:27], a[244:247], a[164:167], v[24:27]\n"
"buffer_load_dwordx4 v240, s[20:23], s40 offen offset:1024 nt lds\n"
"v_mfma_f32_16x16x32_bf16 v[28:31], a[248:251], a[160:163], v[28:31]\n"
"ds_read_b128 a[160:163], v250 offset:0\n"
"v_mfma_f32_16x16x32_bf16 v[28:31], a[252:255], a[164:167], v[28:31]\n"
"s_mov_b64 exec, 1\n"
"global_store_dword v255, v254, s[6:7] offset:256 sc1\n"
"s_mov_b64 exec, -1\n"
"v_mfma_f32_16x16x32_bf16 v[60:63], a[248:251], a[168:171], v[60:63]\n"
"ds_read_b128 a[164:167], v251 offset:0\n"
"v_mfma_f32_16x16x32_bf16 v[60:63], a[252:255], a[172:175], v[60:63]\n"
"ds_read_b64_tr_b16 a[112:113], v246 offset:0\n"
"v_and_b32 v254, 0x20, v242\n"
"v_mfma_f32_16x16x32_bf16 v[56:59], a[240:243], a[168:171], v[56:59]\n"
"s_add_u32 s40, s56, s53\n"
"ds_read_b64_tr_b16 a[114:115], v246 offset:512\n"
"v_add_u32 v254, -1, v254\n"
"v_mfma_f32_16x16x32_bf16 v[56:59], a[244:247], a[172:175], v[56:59]\n"
"buffer_load_dwordx4 v240, s[20:23], s40 offen offset:2048 nt lds\n"
"v_mfma_f32_16x16x32_bf16 v[52:55], a[232:235], a[168:171], v[52:55]\n"
"ds_read_b64_tr_b16 a[116:117], v246 offset:8192\n"
"v_ashrrev_i32 v254, 31, v254\n"
"v_mfma_f32_16x16x32_bf16 v[52:55], a[236:239], a[172:175], v[52:55]\n"
"ds_read_b64_tr_b16 a[118:119], v246 offset:8704\n"
"v_and_b32 v254, 0x3f803f80, v254\n"
"v_mfma_f32_16x16x32_bf16 v[48:51], a[224:227], a[168:171], v[48:51]\n"
"ds_read_b64_tr_b16 a[120:121], v247 offset:0\n"
"v_mfma_f32_16x16x32_bf16 v[48:51], a[228:231], a[172:175], v[48:51]\n"
"ds_read_b64_tr_b16 a[122:123], v247 offset:512\n"
"v_mfma_f32_16x16x32_bf16 v[44:47], a[216:219], a[168:171], v[44:47]\n"
"s_add_u32 s40, s56, s54\n"
"ds_read_b64_tr_b16 a[124:125], v247 offset:8192\n"
"v_mfma_f32_16x16x32_bf16 v[44:47], a[220:223], a[172:175], v[44:47]\n"
"buffer_load_dwordx4 v240, s[20:23], s40 offen offset:3072 nt lds\n"
"v_mfma_f32_16x16x32_bf16 v[40:43], a[208:211], a[168:171], v[40:43]\n"
"s_add_u32 s55, s55, s59\n"
"ds_read_b64_tr_b16 a[126:127], v247 offset:8704\n"
"v_mfma_f32_16x16x32_bf16 v[40:43], a[212:215], a[172:175], v[40:43]\n"
"s_add_u32 s41, s55, s57\n"
"ds_read_b64_tr_b16 a[128:129], v244 offset:1024\n"
"v_mfma_f32_16x16x32_bf16 v[36:39], a[200:203], a[168:171], v[36:39]\n"
"s_and_b32 s41, s41, s58\n"
"ds_read_b64_tr_b16 a[130:131], v244 offset:1536\n"
"v_mfma_f32_16x16x32_bf16 v[36:39], a[204:207], a[172:175], v[36:39]\n"
"s_andn2_b32 s56, s55, s58\n"
"ds_read_b64_tr_b16 a[132:133], v244 offset:9216\n"
"v_mfma_f32_16x16x32_bf16 v[32:35], a[192:195], a[168:171], v[32:35]\n"
"s_or_b32 s56, s56, s41\n"
"ds_read_b64_tr_b16 a[134:135], v244 offset:9728\n"
"v_mfma_f32_16x16x32_bf16 v[32:35], a[196:199], a[172:175], v[32:35]\n"
"ds_read_b64_tr_b16 a[136:137], v245 offset:1024\n"
"v_mfma_f32_16x16x32_bf16 v[64:67], a[192:195], a[176:179], v[64:67]\n"
"s_add_u32 m0, s44, 16384\n"
"ds_read_b64_tr_b16 a[138:139], v245 offset:1536\n"
"v_mfma_f32_16x16x32_bf16 v[64:67], a[196:199], a[180:183], v[64:67]\n"
"buffer_load_dwordx4 v240, s[16:19], s56 offen nt lds\n"
"v_mfma_f32_16x16x32_bf16 v[68:71], a[200:203], a[176:179], v[68:71]\n"
"ds_read_b64_tr_b16 a[140:141], v245 offset:9216\n"
"v_mfma_f32_16x16x32_bf16 v[68:71], a[204:207], a[180:183], v[68:71]\n"
"ds_read_b64_tr_b16 a[142:143], v245 offset:9728\n"
"v_mfma_f32_16x16x32_bf16 v[72:75], a[208:211], a[176:179], v[72:75]\n"
"ds_read_b64_tr_b16 a[144:145], v246 offset:1024\n"
"v_mfma_f32_16x16x32_bf16 v[72:75], a[212:215], a[180:183], v[72:75]\n"
"ds_read_b64_tr_b16 a[146:147], v246 offset:1536\n"
"v_mfma_f32_16x16x32_bf16 v[76:79], a[216:219], a[176:179], v[76:79]\n"
"s_add_u32 s40, s56, s52\n"
"ds_read_b64_tr_b16 a[148:149], v246 offset:9216\n"
"v_mfma_f32_16x16x32_bf16 v[76:79], a[220:223], a[180:183], v[76:79]\n"
"buffer_load_dwordx4 v240, s[16:19], s40 offen offset:1024 nt lds\n"
"v_mfma_f32_16x16x32_bf16 v[88:91], a[240:243], a[176:179], v[88:91]\n"
"ds_read_b64_tr_b16 a[150:151], v246 offset:9728\n"
"v_mfma_f32_16x16x32_bf16 v[88:91], a[244:247], a[180:183], v[88:91]\n"
"ds_read_b64_tr_b16 a[152:153], v247 offset:1024\n"
"v_mfma_f32_16x16x32_bf16 v[92:95], a[248:251], a[176:179], v[92:95]\n"
"ds_read_b64_tr_b16 a[154:155], v247 offset:1536\n"
"v_mfma_f32_16x16x32_bf16 v[92:95], a[252:255], a[180:183], v[92:95]\n"
"ds_read_b64_tr_b16 a[156:157], v247 offset:9216\n"
"v_mfma_f32_16x16x32_bf16 v[80:83], a[224:227], a[176:179], v[80:83]\n"
"s_add_u32 s40, s56, s53\n"
"ds_read_b64_tr_b16 a[158:159], v247 offset:9728\n"
"v_mfma_f32_16x16x32_bf16 v[80:83], a[228:231], a[180:183], v[80:83]\n"
"buffer_load_dwordx4 v240, s[16:19], s40 offen offset:2048 nt lds\n"
"v_mfma_f32_16x16x32_bf16 v[84:87], a[232:235], a[176:179], v[84:87]\n"
"ds_read_b128 a[168:171], v250 offset:2048\n"
"v_mfma_f32_16x16x32_bf16 v[84:87], a[236:239], a[180:183], v[84:87]\n"
"ds_read_b128 a[172:175], v251 offset:2048\n"
"v_mfma_f32_16x16x32_bf16 v[108:111], a[232:235], a[184:187], v[108:111]\n"
"ds_read_b128 a[176:179], v250 offset:4096\n"
"v_mfma_f32_16x16x32_bf16 v[108:111], a[236:239], a[188:191], v[108:111]\n"
"ds_read_b128 a[180:183], v251 offset:4096\n"
"v_mfma_f32_16x16x32_bf16 v[104:107], a[224:227], a[184:187], v[104:107]\n"
"s_add_u32 s40, s56, s54\n"
"v_mfma_f32_16x16x32_bf16 v[104:107], a[228:231], a[188:191], v[104:107]\n"
"buffer_load_dwordx4 v240, s[16:19], s40 offen offset:3072 nt lds\n"
"v_mfma_f32_16x16x32_bf16 v[100:103], a[200:203], a[184:187], v[100:103]\n"
"v_mfma_f32_16x16x32_bf16 v[100:103], a[204:207], a[188:191], v[100:103]\n"
"v_mfma_f32_16x16x32_bf16 v[96:99], a[192:195], a[184:187], v[96:99]\n"
"ds_read_b128 a[184:187], v252 offset:0\n"
"v_mfma_f32_16x16x32_bf16 v[96:99], a[196:199], a[188:191], v[96:99]\n"
"s_waitcnt lgkmcnt(3)\n"
"ds_read_b128 a[188:191], v253 offset:0\n"
"v_mfma_f32_16x16x32_bf16 v[112:115], a[96:99], a[160:163], v[112:115]\n"
"v_mfma_f32_16x16x32_bf16 v[112:115], a[100:103], a[164:167], v[112:115]\n"
"v_mfma_f32_16x16x32_bf16 v[116:119], a[104:107], a[160:163], v[116:119]\n"
"s_waitcnt vmcnt(13)\n"
"v_mfma_f32_16x16x32_bf16 v[116:119], a[108:111], a[164:167], v[116:119]\n"
"s_barrier\n"
"v_mfma_f32_16x16x32_bf16 v[120:123], a[112:115], a[160:163], v[120:123]\n"
"s_add_u32 m0, s44, 81920\n"
"ds_read_b64_tr_b16 a[232:233], v245 offset:17408\n"
"v_mfma_f32_16x16x32_bf16 v[120:123], a[116:119], a[164:167], v[120:123]\n"
"buffer_load_dwordx4 v240, s[20:23], s56 offen nt lds\n"
"v_mfma_f32_16x16x32_bf16 v[124:127], a[120:123], a[160:163], v[124:127]\n"
"ds_read_b64_tr_b16 a[234:235], v245 offset:17920\n"
"v_mfma_f32_16x16x32_bf16 v[124:127], a[124:127], a[164:167], v[124:127]\n"
"ds_read_b64_tr_b16 a[236:237], v245 offset:25600\n"
"v_mfma_f32_16x16x32_bf16 v[128:131], a[128:131], a[160:163], v[128:131]\n"
"ds_read_b64_tr_b16 a[238:239], v245 offset:26112\n"
"v_mfma_f32_16x16x32_bf16 v[128:131], a[132:135], a[164:167], v[128:131]\n"
"ds_read_b64_tr_b16 a[224:225], v244 offset:17408\n"
"v_mfma_f32_16x16x32_bf16 v[132:135], a[136:139], a[160:163], v[132:135]\n"
"ds_read_b64_tr_b16 a[226:227], v244 offset:17920\n"
"v_mfma_f32_16x16x32_bf16 v[132:135], a[140:143], a[164:167], v[132:135]\n"
"ds_read_b64_tr_b16 a[192:193], v244 offset:16384\n"
"v_mfma_f32_16x16x32_bf16 v[136:139], a[144:147], a[160:163], v[136:139]\n"
"ds_read_b64_tr_b16 a[194:195], v244 offset:16896\n"
"v_mfma_f32_16x16x32_bf16 v[136:139], a[148:151], a[164:167], v[136:139]\n"
"ds_read_b64_tr_b16 a[196:197], v244 offset:24576\n"
"v_mfma_f32_16x16x32_bf16 v[140:143], a[152:155], a[160:163], v[140:143]\n"
"ds_read_b64_tr_b16 a[198:199], v244 offset:25088\n"
"v_mfma_f32_16x16x32_bf16 v[140:143], a[156:159], a[164:167], v[140:143]\n"
"s_waitcnt lgkmcnt(7)\n"
"ds_read_b64_tr_b16 a[200:201], v245 offset:16384\n"
"v_mfma_f32_16x16x32_bf16 v[220:223], a[136:139], a[184:187], v[220:223]\n"
"ds_read_b64_tr_b16 a[202:203], v245 offset:16896\n"
"v_mfma_f32_16x16x32_bf16 v[220:223], a[140:143], a[188:191], v[220:223]\n"
"ds_read_b64_tr_b16 a[204:205], v245 offset:24576\n"
"v_mfma_f32_16x16x32_bf16 v[216:219], a[128:131], a[184:187], v[216:219]\n"
"s_add_u32 s40, s56, s52\n"
"ds_read_b64_tr_b16 a[206:207], v245 offset:25088\n"
"v_mfma_f32_16x16x32_bf16 v[216:219], a[132:135], a[188:191], v[216:219]\n"
"buffer_load_dwordx4 v240, s[20:23], s40 offen offset:1024 nt lds\n"
"v_mfma_f32_16x16x32_bf16 v[212:215], a[104:107], a[184:187], v[212:215]\n"
"ds_read_b64_tr_b16 a[208:209], v246 offset:16384\n"
"v_mfma_f32_16x16x32_bf16 v[212:215], a[108:111], a[188:191], v[212:215]\n"
"v_pk_fma_f32 v[112:113], v[112:113], s[70:71], s[70:71] op_sel:[0,0,1] op_sel_hi:[1,0,1]\nv_pk_fma_f32 v[114:115], v[114:115], s[70:71], s[70:71] op_sel:[0,0,1] op_sel_hi:[1,0,1]\nv_pk_fma_f32 v[116:117], v[116:117], s[70:71], s[70:71] op_sel:[0,0,1] op_sel_hi:[1,0,1]\nv_pk_fma_f32 v[118:119], v[118:119], s[70:71], s[70:71] op_sel:[0,0,1] op_sel_hi:[1,0,1]\nv_pk_fma_f32 v[120:121], v[120:121], s[70:71], s[70:71] op_sel:[0,0,1] op_sel_hi:[1,0,1]\nv_pk_fma_f32 v[122:123], v[122:123], s[70:71], s[70:71] op_sel:[0,0,1] op_sel_hi:[1,0,1]\nv_pk_fma_f32 v[124:125], v[124:125], s[70:71], s[70:71] op_sel:[0,0,1] op_sel_hi:[1,0,1]\nv_pk_fma_f32 v[126:127], v[126:127], s[70:71], s[70:71] op_sel:[0,0,1] op_sel_hi:[1,0,1]\nv_pk_fma_f32 v[128:129], v[128:129], s[70:71], s[70:71] op_sel:[0,0,1] op_sel_hi:[1,0,1]\nv_pk_fma_f32 v[130:131], v[130:131], s[70:71], s[70:71] op_sel:[0,0,1] op_sel_hi:[1,0,1]\nv_pk_fma_f32 v[132:133], v[132:133], s[70:71], s[70:71] op_sel:[0,0,1] op_sel_hi:[1,0,1]\nv_pk_fma_f32 v[134:135], v[134:135], s[70:71], s[70:71] op_sel:[0,0,1] op_sel_hi:[1,0,1]\nv_pk_fma_f32 v[136:137], v[136:137], s[70:71], s[70:71] op_sel:[0,0,1] op_sel_hi:[1,0,1]\nv_pk_fma_f32 v[138:139], v[138:139], s[70:71], s[70:71] op_sel:[0,0,1] op_sel_hi:[1,0,1]\nv_pk_fma_f32 v[140:141], v[140:141], s[70:71], s[70:71] op_sel:[0,0,1] op_sel_hi:[1,0,1]\nv_pk_fma_f32 v[142:143], v[142:143], s[70:71], s[70:71] op_sel:[0,0,1] op_sel_hi:[1,0,1]\n"
"v_mfma_f32_16x16x32_bf16 v[208:211], a[96:99], a[184:187], v[208:211]\n"
"ds_read_b64_tr_b16 a[210:211], v246 offset:16896\n"
"v_cvt_pk_bf16_f32 v128, v128, v129\n"
"v_mfma_f32_16x16x32_bf16 v[208:211], a[100:103], a[188:191], v[208:211]\n"
"ds_read_b64_tr_b16 a[212:213], v246 offset:24576\n"
"v_cvt_pk_bf16_f32 v129, v130, v131\n"
"v_mfma_f32_16x16x32_bf16 v[172:175], a[152:155], a[168:171], v[172:175]\n"
"ds_read_b64_tr_b16 a[214:215], v246 offset:25088\n"
"v_cvt_pk_bf16_f32 v130, v132, v133\n"
"v_mfma_f32_16x16x32_bf16 v[172:175], a[156:159], a[172:175], v[172:175]\n"
"ds_read_b64_tr_b16 a[216:217], v247 offset:16384\n"
"v_cvt_pk_bf16_f32 v131, v134, v135\n"
"v_mfma_f32_16x16x32_bf16 v[168:171], a[144:147], a[168:171], v[168:171]\n"
"ds_read_b64_tr_b16 a[218:219], v247 offset:16896\n"
"v_cvt_pk_bf16_f32 v136, v136, v137\n"
"v_mfma_f32_16x16x32_bf16 v[168:171], a[148:151], a[172:175], v[168:171]\n"
"ds_read_b64_tr_b16 a[220:221], v247 offset:24576\n"
"v_cvt_pk_bf16_f32 v137, v138, v139\n"
"v_mfma_f32_16x16x32_bf16 v[164:167], a[136:139], a[168:171], v[164:167]\n"
"ds_read_b64_tr_b16 a[222:223], v247 offset:25088\n"
"v_cvt_pk_bf16_f32 v138, v140, v141\n"
"v_mfma_f32_16x16x32_bf16 v[164:167], a[140:143], a[172:175], v[164:167]\n"
"ds_read_b64_tr_b16 a[228:229], v244 offset:25600\n"
"v_cvt_pk_bf16_f32 v139, v142, v143\n"
"v_mfma_f32_16x16x32_bf16 v[160:163], a[128:131], a[168:171], v[160:163]\n"
"s_add_u32 s40, s56, s53\n"
"ds_read_b64_tr_b16 a[230:231], v244 offset:26112\n"
"v_cvt_pk_bf16_f32 v120, v120, v121\n"
"v_mfma_f32_16x16x32_bf16 v[160:163], a[132:135], a[172:175], v[160:163]\n"
"buffer_load_dwordx4 v240, s[20:23], s40 offen offset:2048 nt lds\n"
"v_mfma_f32_16x16x32_bf16 v[156:159], a[120:123], a[168:171], v[156:159]\n"
"ds_read_b64_tr_b16 a[240:241], v246 offset:17408\n"
"v_cvt_pk_bf16_f32 v121, v122, v123\n"
"v_mfma_f32_16x16x32_bf16 v[156:159], a[124:127], a[172:175], v[156:159]\n"
"ds_read_b64_tr_b16 a[242:243], v246 offset:17920\n"
"v_cvt_pk_bf16_f32 v122, v124, v125\n"
"v_mfma_f32_16x16x32_bf16 v[152:155], a[112:115], a[168:171], v[152:155]\n"
"ds_read_b64_tr_b16 a[244:245], v246 offset:25600\n"
"v_cvt_pk_bf16_f32 v123, v126, v127\n"
"v_mfma_f32_16x16x32_bf16 v[152:155], a[116:119], a[172:175], v[152:155]\n"
"ds_read_b64_tr_b16 a[246:247], v246 offset:26112\n"
"v_cvt_pk_bf16_f32 v112, v112, v113\n"
"v_mfma_f32_16x16x32_bf16 v[148:151], a[104:107], a[168:171], v[148:151]\n"
"ds_read_b64_tr_b16 a[248:249], v247 offset:17408\n"
"v_cvt_pk_bf16_f32 v113, v114, v115\n"
"v_mfma_f32_16x16x32_bf16 v[148:151], a[108:111], a[172:175], v[148:151]\n"
"ds_read_b64_tr_b16 a[250:251], v247 offset:17920\n"
"v_cvt_pk_bf16_f32 v114, v116, v117\n"
"v_mfma_f32_16x16x32_bf16 v[144:147], a[96:99], a[168:171], v[144:147]\n"
"ds_read_b64_tr_b16 a[252:253], v247 offset:25600\n"
"v_cvt_pk_bf16_f32 v115, v118, v119\n"
"v_mfma_f32_16x16x32_bf16 v[144:147], a[100:103], a[172:175], v[144:147]\n"
"v_pk_fma_f32 v[208:209], v[208:209], s[70:71], s[70:71] op_sel:[0,0,1] op_sel_hi:[1,0,1]\nv_pk_fma_f32 v[210:211], v[210:211], s[70:71], s[70:71] op_sel:[0,0,1] op_sel_hi:[1,0,1]\nv_pk_fma_f32 v[212:213], v[212:213], s[70:71], s[70:71] op_sel:[0,0,1] op_sel_hi:[1,0,1]\nv_pk_fma_f32 v[214:215], v[214:215], s[70:71], s[70:71] op_sel:[0,0,1] op_sel_hi:[1,0,1]\nv_pk_fma_f32 v[216:217], v[216:217], s[70:71], s[70:71] op_sel:[0,0,1] op_sel_hi:[1,0,1]\nv_pk_fma_f32 v[218:219], v[218:219], s[70:71], s[70:71] op_sel:[0,0,1] op_sel_hi:[1,0,1]\nv_pk_fma_f32 v[220:221], v[220:221], s[70:71], s[70:71] op_sel:[0,0,1] op_sel_hi:[1,0,1]\nv_pk_fma_f32 v[222:223], v[222:223], s[70:71], s[70:71] op_sel:[0,0,1] op_sel_hi:[1,0,1]\n"
"v_mfma_f32_16x16x32_bf16 v[176:179], a[96:99], a[176:179], v[176:179]\n"
"ds_read_b64_tr_b16 a[254:255], v247 offset:26112\n"
"v_cvt_pk_bf16_f32 v208, v208, v209\n"
"v_mfma_f32_16x16x32_bf16 v[176:179], a[100:103], a[180:183], v[176:179]\n"
"v_permlane16_swap_b32 v128, v130\n"
"v_mfma_f32_16x16x32_bf16 v[180:183], a[104:107], a[176:179], v[180:183]\n"
"v_permlane16_swap_b32 v129, v131\n"
"v_mfma_f32_16x16x32_bf16 v[180:183], a[108:111], a[180:183], v[180:183]\n"
"v_permlane16_swap_b32 v136, v138\n"
"v_mfma_f32_16x16x32_bf16 v[184:187], a[112:115], a[176:179], v[184:187]\n"
"s_add_u32 s40, s56, s54\n"
"v_permlane16_swap_b32 v137, v139\n"
"v_mfma_f32_16x16x32_bf16 v[184:187], a[116:119], a[180:183], v[184:187]\n"
"buffer_load_dwordx4 v240, s[20:23], s40 offen offset:3072 nt lds\n"
"v_mfma_f32_16x16x32_bf16 v[188:191], a[120:123], a[176:179], v[188:191]\n"
"s_add_u32 s55, s55, s59\n"
"v_pk_fma_f32 v[144:145], v[144:145], s[70:71], s[70:71] op_sel:[0,0,1] op_sel_hi:[1,0,1]\nv_pk_fma_f32 v[146:147], v[146:147], s[70:71], s[70:71] op_sel:[0,0,1] op_sel_hi:[1,0,1]\nv_pk_fma_f32 v[148:149], v[148:149], s[70:71], s[70:71] op_sel:[0,0,1] op_sel_hi:[1,0,1]\nv_pk_fma_f32 v[150:151], v[150:151], s[70:71], s[70:71] op_sel:[0,0,1] op_sel_hi:[1,0,1]\nv_pk_fma_f32 v[152:153], v[152:153], s[70:71], s[70:71] op_sel:[0,0,1] op_sel_hi:[1,0,1]\nv_pk_fma_f32 v[154:155], v[154:155], s[70:71], s[70:71] op_sel:[0,0,1] op_sel_hi:[1,0,1]\nv_pk_fma_f32 v[156:157], v[156:157], s[70:71], s[70:71] op_sel:[0,0,1] op_sel_hi:[1,0,1]\nv_pk_fma_f32 v[158:159], v[158:159], s[70:71], s[70:71] op_sel:[0,0,1] op_sel_hi:[1,0,1]\nv_pk_fma_f32 v[160:161], v[160:161], s[70:71], s[70:71] op_sel:[0,0,1] op_sel_hi:[1,0,1]\nv_pk_fma_f32 v[162:163], v[162:163], s[70:71], s[70:71] op_sel:[0,0,1] op_sel_hi:[1,0,1]\nv_pk_fma_f32 v[164:165], v[164:165], s[70:71], s[70:71] op_sel:[0,0,1] op_sel_hi:[1,0,1]\nv_pk_fma_f32 v[166:167], v[166:167], s[70:71], s[70:71] op_sel:[0,0,1] op_sel_hi:[1,0,1]\nv_pk_fma_f32 v[168:169], v[168:169], s[70:71], s[70:71] op_sel:[0,0,1] op_sel_hi:[1,0,1]\nv_pk_fma_f32 v[170:171], v[170:171], s[70:71], s[70:71] op_sel:[0,0,1] op_sel_hi:[1,0,1]\nv_pk_fma_f32 v[172:173], v[172:173], s[70:71], s[70:71] op_sel:[0,0,1] op_sel_hi:[1,0,1]\nv_pk_fma_f32 v[174:175], v[174:175], s[70:71], s[70:71] op_sel:[0,0,1] op_sel_hi:[1,0,1]\n"
"v_mfma_f32_16x16x32_bf16 v[188:191], a[124:127], a[180:183], v[188:191]\n"
"s_add_u32 s41, s55, s57\n"
"v_permlane16_swap_b32 v112, v114\n"
"v_mfma_f32_16x16x32_bf16 v[200:203], a[144:147], a[176:179], v[200:203]\n"
"s_and_b32 s41, s41, s58\n"
"v_permlane16_swap_b32 v113, v115\n"
"v_mfma_f32_16x16x32_bf16 v[200:203], a[148:151], a[180:183], v[200:203]\n"
"s_andn2_b32 s56, s55, s58\n"
"v_permlane16_swap_b32 v120, v122\n"
"v_mfma_f32_16x16x32_bf16 v[204:207], a[152:155], a[176:179], v[204:207]\n"
"s_or_b32 s56, s56, s41\n"
"v_permlane16_swap_b32 v121, v123\n"
"v_mfma_f32_16x16x32_bf16 v[204:207], a[156:159], a[180:183], v[204:207]\n"
"v_cvt_pk_bf16_f32 v209, v210, v211\n"
"v_cvt_pk_bf16_f32 v210, v212, v213\n"
"v_mfma_f32_16x16x32_bf16 v[192:195], a[128:131], a[176:179], v[192:195]\n"
"s_nop 0\n"
"v_permlane16_swap_b32 v208, v210\n"
"v_mfma_f32_16x16x32_bf16 v[192:195], a[132:135], a[180:183], v[192:195]\n"
"v_cvt_pk_bf16_f32 v211, v214, v215\n"
"v_cvt_pk_bf16_f32 v216, v216, v217\n"
"v_mfma_f32_16x16x32_bf16 v[196:199], a[136:139], a[176:179], v[196:199]\n"
"v_permlane16_swap_b32 v209, v211\n"
"v_mfma_f32_16x16x32_bf16 v[196:199], a[140:143], a[180:183], v[196:199]\n"
"ds_write_b128 v243, v[208:211] offset:8192\n"
"v_mfma_f32_16x16x32_bf16 v[108:111], a[232:235], a[184:187], v[108:111]\n"
"s_waitcnt lgkmcnt(1)\n"
"s_add_u32 m0, s44, 32768\n"
"v_cvt_pk_bf16_f32 v217, v218, v219\n"
"v_cvt_pk_bf16_f32 v218, v220, v221\n"
"v_mfma_f32_16x16x32_bf16 v[108:111], a[236:239], a[188:191], v[108:111]\n"
"buffer_load_dwordx4 v240, s[16:19], s56 offen nt lds\n"
"v_mfma_f32_16x16x32_bf16 v[104:107], a[224:227], a[184:187], v[104:107]\n"
"s_waitcnt vmcnt(14)\n"
"v_permlane16_swap_b32 v216, v218\n"
"v_mfma_f32_16x16x32_bf16 v[104:107], a[228:231], a[188:191], v[104:107]\n"
"s_barrier\n"
"v_mfma_f32_16x16x32_bf16 v[100:103], a[200:203], a[184:187], v[100:103]\n"
"v_cvt_pk_bf16_f32 v219, v222, v223\n"
"v_cvt_pk_bf16_f32 v144, v144, v145\n"
"v_mfma_f32_16x16x32_bf16 v[100:103], a[204:207], a[188:191], v[100:103]\n"
"s_mov_b32 s74, 0x41400000\n"
"v_permlane16_swap_b32 v217, v219\n"
"v_mfma_f32_16x16x32_bf16 v[96:99], a[192:195], a[184:187], v[96:99]\n"
"ds_write_b128 v243, v[216:219] offset:10240\n"
"v_mfma_f32_16x16x32_bf16 v[96:99], a[196:199], a[188:191], v[96:99]\n"
"v_pk_fma_f32 v[176:177], v[176:177], s[70:71], s[70:71] op_sel:[0,0,1] op_sel_hi:[1,0,1]\nv_pk_fma_f32 v[178:179], v[178:179], s[70:71], s[70:71] op_sel:[0,0,1] op_sel_hi:[1,0,1]\nv_pk_fma_f32 v[180:181], v[180:181], s[70:71], s[70:71] op_sel:[0,0,1] op_sel_hi:[1,0,1]\nv_pk_fma_f32 v[182:183], v[182:183], s[70:71], s[70:71] op_sel:[0,0,1] op_sel_hi:[1,0,1]\nv_pk_fma_f32 v[184:185], v[184:185], s[70:71], s[70:71] op_sel:[0,0,1] op_sel_hi:[1,0,1]\nv_pk_fma_f32 v[186:187], v[186:187], s[70:71], s[70:71] op_sel:[0,0,1] op_sel_hi:[1,0,1]\nv_pk_fma_f32 v[188:189], v[188:189], s[70:71], s[70:71] op_sel:[0,0,1] op_sel_hi:[1,0,1]\nv_pk_fma_f32 v[190:191], v[190:191], s[70:71], s[70:71] op_sel:[0,0,1] op_sel_hi:[1,0,1]\nv_pk_fma_f32 v[192:193], v[192:193], s[70:71], s[70:71] op_sel:[0,0,1] op_sel_hi:[1,0,1]\nv_pk_fma_f32 v[194:195], v[194:195], s[70:71], s[70:71] op_sel:[0,0,1] op_sel_hi:[1,0,1]\nv_pk_fma_f32 v[196:197], v[196:197], s[70:71], s[70:71] op_sel:[0,0,1] op_sel_hi:[1,0,1]\nv_pk_fma_f32 v[198:199], v[198:199], s[70:71], s[70:71] op_sel:[0,0,1] op_sel_hi:[1,0,1]\nv_pk_fma_f32 v[200:201], v[200:201], s[70:71], s[70:71] op_sel:[0,0,1] op_sel_hi:[1,0,1]\nv_pk_fma_f32 v[202:203], v[202:203], s[70:71], s[70:71] op_sel:[0,0,1] op_sel_hi:[1,0,1]\nv_pk_fma_f32 v[204:205], v[204:205], s[70:71], s[70:71] op_sel:[0,0,1] op_sel_hi:[1,0,1]\nv_pk_fma_f32 v[206:207], v[206:207], s[70:71], s[70:71] op_sel:[0,0,1] op_sel_hi:[1,0,1]\n"
"v_mfma_f32_16x16x32_bf16 v[0:3], a[192:195], a[160:163], v[0:3]\n"
"v_cvt_pk_bf16_f32 v145, v146, v147\n"
"v_cvt_pk_bf16_f32 v146, v148, v149\n"
"v_mfma_f32_16x16x32_bf16 v[0:3], a[196:199], a[164:167], v[0:3]\n"
"s_nop 0\n"
"v_permlane16_swap_b32 v144, v146\n"
"v_mfma_f32_16x16x32_bf16 v[4:7], a[200:203], a[160:163], v[4:7]\n"
"v_bfe_u32 v149, v242, 8, 2\n"
"v_lshl_add_u32 v149, v149, 4, s43\n"
"v_mfma_f32_16x16x32_bf16 v[4:7], a[204:207], a[164:167], v[4:7]\n"
"ds_read_b128 v[208:211], v149 offset:0\n"
"v_cvt_pk_bf16_f32 v147, v150, v151\n"
"v_mfma_f32_16x16x32_bf16 v[8:11], a[208:211], a[160:163], v[8:11]\n"
"ds_read_b128 v[216:219], v149 offset:1024\n"
"v_xor_b32 v150, 64, v149\n"
"v_mfma_f32_16x16x32_bf16 v[8:11], a[212:215], a[164:167], v[8:11]\n"
"ds_read_b128 v[212:215], v150 offset:0\n"
"v_cvt_pk_bf16_f32 v176, v176, v177\n"
"v_mfma_f32_16x16x32_bf16 v[12:15], a[216:219], a[160:163], v[12:15]\n"
"s_add_u32 s40, s56, s52\n"
"ds_read_b128 v[220:223], v150 offset:1024\n"
"v_cvt_pk_bf16_f32 v177, v178, v179\n"
"v_mfma_f32_16x16x32_bf16 v[12:15], a[220:223], a[164:167], v[12:15]\n"
"buffer_load_dwordx4 v240, s[16:19], s40 offen offset:1024 nt lds\n"
"v_mfma_f32_16x16x32_bf16 v[16:19], a[224:227], a[160:163], v[16:19]\n"
"v_permlane16_swap_b32 v145, v147\n"
"v_mfma_f32_16x16x32_bf16 v[16:19], a[228:231], a[164:167], v[16:19]\n"
"v_pk_fma_f32 v[96:97], v[96:97], s[70:71], s[70:71] op_sel:[0,0,1] op_sel_hi:[1,0,1]\nv_pk_fma_f32 v[98:99], v[98:99], s[70:71], s[70:71] op_sel:[0,0,1] op_sel_hi:[1,0,1]\nv_pk_fma_f32 v[100:101], v[100:101], s[70:71], s[70:71] op_sel:[0,0,1] op_sel_hi:[1,0,1]\nv_pk_fma_f32 v[102:103], v[102:103], s[70:71], s[70:71] op_sel:[0,0,1] op_sel_hi:[1,0,1]\nv_pk_fma_f32 v[104:105], v[104:105], s[70:71], s[70:71] op_sel:[0,0,1] op_sel_hi:[1,0,1]\nv_pk_fma_f32 v[106:107], v[106:107], s[70:71], s[70:71] op_sel:[0,0,1] op_sel_hi:[1,0,1]\nv_pk_fma_f32 v[108:109], v[108:109], s[70:71], s[70:71] op_sel:[0,0,1] op_sel_hi:[1,0,1]\nv_pk_fma_f32 v[110:111], v[110:111], s[70:71], s[70:71] op_sel:[0,0,1] op_sel_hi:[1,0,1]\n"
"v_mfma_f32_16x16x32_bf16 v[20:23], a[232:235], a[160:163], v[20:23]\n"
"v_cvt_pk_bf16_f32 v178, v180, v181\n"
"v_cvt_pk_bf16_f32 v179, v182, v183\n"
"v_mfma_f32_16x16x32_bf16 v[20:23], a[236:239], a[164:167], v[20:23]\n"
"v_permlane16_swap_b32 v176, v178\n"
"v_mfma_f32_16x16x32_bf16 v[24:27], a[240:243], a[160:163], v[24:27]\n"
"v_permlane16_swap_b32 v177, v179\n"
"v_mfma_f32_16x16x32_bf16 v[24:27], a[244:247], a[164:167], v[24:27]\n"
"v_cvt_pk_bf16_f32 v184, v184, v185\n"
"v_cvt_pk_bf16_f32 v185, v186, v187\n"
"v_mfma_f32_16x16x32_bf16 v[28:31], a[248:251], a[160:163], v[28:31]\n"
"v_cvt_pk_bf16_f32 v186, v188, v189\n"
"v_cvt_pk_bf16_f32 v187, v190, v191\n"
"v_mfma_f32_16x16x32_bf16 v[28:31], a[252:255], a[164:167], v[28:31]\n"
"v_permlane16_swap_b32 v184, v186\n"
"v_mfma_f32_16x16x32_bf16 v[60:63], a[248:251], a[168:171], v[60:63]\n"
"v_permlane16_swap_b32 v185, v187\n"
"v_mfma_f32_16x16x32_bf16 v[60:63], a[252:255], a[172:175], v[60:63]\n"
"v_accvgpr_write_b32 a132, v184\n"
"v_accvgpr_write_b32 a133, v185\n"
"v_mfma_f32_16x16x32_bf16 v[56:59], a[240:243], a[168:171], v[56:59]\n"
"s_add_u32 s40, s56, s53\n"
"v_accvgpr_write_b32 a134, v186\n"
"v_accvgpr_write_b32 a135, v187\n"
"v_mfma_f32_16x16x32_bf16 v[56:59], a[244:247], a[172:175], v[56:59]\n"
"buffer_load_dwordx4 v240, s[16:19], s40 offen offset:2048 nt lds\n"
"v_mfma_f32_16x16x32_bf16 v[52:55], a[232:235], a[168:171], v[52:55]\n"
"v_xor_b32 v148, 0x400, v243\n"
"v_cvt_pk_bf16_f32 v192, v192, v193\n"
"v_mfma_f32_16x16x32_bf16 v[52:55], a[236:239], a[172:175], v[52:55]\n"
"v_pk_fma_f32 v[0:1], v[0:1], s[70:71], s[70:71] op_sel:[0,0,1] op_sel_hi:[1,0,1]\nv_pk_fma_f32 v[2:3], v[2:3], s[70:71], s[70:71] op_sel:[0,0,1] op_sel_hi:[1,0,1]\nv_pk_fma_f32 v[4:5], v[4:5], s[70:71], s[70:71] op_sel:[0,0,1] op_sel_hi:[1,0,1]\nv_pk_fma_f32 v[6:7], v[6:7], s[70:71], s[70:71] op_sel:[0,0,1] op_sel_hi:[1,0,1]\nv_pk_fma_f32 v[8:9], v[8:9], s[70:71], s[70:71] op_sel:[0,0,1] op_sel_hi:[1,0,1]\nv_pk_fma_f32 v[10:11], v[10:11], s[70:71], s[70:71] op_sel:[0,0,1] op_sel_hi:[1,0,1]\nv_pk_fma_f32 v[12:13], v[12:13], s[70:71], s[70:71] op_sel:[0,0,1] op_sel_hi:[1,0,1]\nv_pk_fma_f32 v[14:15], v[14:15], s[70:71], s[70:71] op_sel:[0,0,1] op_sel_hi:[1,0,1]\nv_pk_fma_f32 v[16:17], v[16:17], s[70:71], s[70:71] op_sel:[0,0,1] op_sel_hi:[1,0,1]\nv_pk_fma_f32 v[18:19], v[18:19], s[70:71], s[70:71] op_sel:[0,0,1] op_sel_hi:[1,0,1]\nv_pk_fma_f32 v[20:21], v[20:21], s[70:71], s[70:71] op_sel:[0,0,1] op_sel_hi:[1,0,1]\nv_pk_fma_f32 v[22:23], v[22:23], s[70:71], s[70:71] op_sel:[0,0,1] op_sel_hi:[1,0,1]\nv_pk_fma_f32 v[24:25], v[24:25], s[70:71], s[70:71] op_sel:[0,0,1] op_sel_hi:[1,0,1]\nv_pk_fma_f32 v[26:27], v[26:27], s[70:71], s[70:71] op_sel:[0,0,1] op_sel_hi:[1,0,1]\nv_pk_fma_f32 v[28:29], v[28:29], s[70:71], s[70:71] op_sel:[0,0,1] op_sel_hi:[1,0,1]\nv_pk_fma_f32 v[30:31], v[30:31], s[70:71], s[70:71] op_sel:[0,0,1] op_sel_hi:[1,0,1]\n"
"v_mfma_f32_16x16x32_bf16 v[48:51], a[224:227], a[168:171], v[48:51]\n"
"v_cvt_pk_bf16_f32 v193, v194, v195\n"
"v_cvt_pk_bf16_f32 v194, v196, v197\n"
"v_mfma_f32_16x16x32_bf16 v[48:51], a[228:231], a[172:175], v[48:51]\n"
"s_nop 0\n"
"v_permlane16_swap_b32 v192, v194\n"
"v_mfma_f32_16x16x32_bf16 v[44:47], a[216:219], a[168:171], v[44:47]\n"
"v_cvt_pk_bf16_f32 v195, v198, v199\n"
"v_accvgpr_write_b32 a136, v192\n"
"v_mfma_f32_16x16x32_bf16 v[44:47], a[220:223], a[172:175], v[44:47]\n"
"v_permlane16_swap_b32 v193, v195\n"
"v_mfma_f32_16x16x32_bf16 v[40:43], a[208:211], a[168:171], v[40:43]\n"
"v_accvgpr_write_b32 a137, v193\n"
"v_accvgpr_write_b32 a138, v194\n"
"v_mfma_f32_16x16x32_bf16 v[40:43], a[212:215], a[172:175], v[40:43]\n"
"v_accvgpr_write_b32 a139, v195\n"
"v_cvt_pk_bf16_f32 v152, v152, v153\n"
"v_mfma_f32_16x16x32_bf16 v[36:39], a[200:203], a[168:171], v[36:39]\n"
"v_cvt_pk_bf16_f32 v153, v154, v155\n"
"v_cvt_pk_bf16_f32 v154, v156, v157\n"
"v_mfma_f32_16x16x32_bf16 v[36:39], a[204:207], a[172:175], v[36:39]\n"
"s_nop 0\n"
"v_permlane16_swap_b32 v152, v154\n"
"v_mfma_f32_16x16x32_bf16 v[32:35], a[192:195], a[168:171], v[32:35]\n"
"v_cvt_pk_bf16_f32 v155, v158, v159\n"
"v_accvgpr_write_b32 a116, v152\n"
"v_mfma_f32_16x16x32_bf16 v[32:35], a[196:199], a[172:175], v[32:35]\n"
"v_permlane16_swap_b32 v153, v155\n"
"v_mfma_f32_16x16x32_bf16 v[64:67], a[192:195], a[176:179], v[64:67]\n"
"s_add_u32 s40, s56, s54\n"
"ds_read_b128 a[192:195], v243 offset:0\n"
"v_accvgpr_write_b32 a117, v153\n"
"v_mfma_f32_16x16x32_bf16 v[64:67], a[196:199], a[180:183], v[64:67]\n"
"buffer_load_dwordx4 v240, s[16:19], s40 offen offset:3072 nt lds\n"
"v_mfma_f32_16x16x32_bf16 v[68:71], a[200:203], a[176:179], v[68:71]\n"
"ds_read_b128 a[196:199], v148 offset:0\n"
"v_accvgpr_write_b32 a118, v154\n"
"v_mfma_f32_16x16x32_bf16 v[68:71], a[204:207], a[180:183], v[68:71]\n"
"ds_read_b128 a[200:203], v243 offset:2048\n"
"v_accvgpr_write_b32 a119, v155\n"
"v_mfma_f32_16x16x32_bf16 v[72:75], a[208:211], a[176:179], v[72:75]\n"
"ds_read_b128 a[204:207], v148 offset:2048\n"
"v_cvt_pk_bf16_f32 v160, v160, v161\n"
"v_mfma_f32_16x16x32_bf16 v[72:75], a[212:215], a[180:183], v[72:75]\n"
"v_pk_fma_f32 v[32:33], v[32:33], s[70:71], s[70:71] op_sel:[0,0,1] op_sel_hi:[1,0,1]\nv_pk_fma_f32 v[34:35], v[34:35], s[70:71], s[70:71] op_sel:[0,0,1] op_sel_hi:[1,0,1]\nv_pk_fma_f32 v[36:37], v[36:37], s[70:71], s[70:71] op_sel:[0,0,1] op_sel_hi:[1,0,1]\nv_pk_fma_f32 v[38:39], v[38:39], s[70:71], s[70:71] op_sel:[0,0,1] op_sel_hi:[1,0,1]\nv_pk_fma_f32 v[40:41], v[40:41], s[70:71], s[70:71] op_sel:[0,0,1] op_sel_hi:[1,0,1]\nv_pk_fma_f32 v[42:43], v[42:43], s[70:71], s[70:71] op_sel:[0,0,1] op_sel_hi:[1,0,1]\nv_pk_fma_f32 v[44:45], v[44:45], s[70:71], s[70:71] op_sel:[0,0,1] op_sel_hi:[1,0,1]\nv_pk_fma_f32 v[46:47], v[46:47], s[70:71], s[70:71] op_sel:[0,0,1] op_sel_hi:[1,0,1]\nv_pk_fma_f32 v[48:49], v[48:49], s[70:71], s[70:71] op_sel:[0,0,1] op_sel_hi:[1,0,1]\nv_pk_fma_f32 v[50:51], v[50:51], s[70:71], s[70:71] op_sel:[0,0,1] op_sel_hi:[1,0,1]\nv_pk_fma_f32 v[52:53], v[52:53], s[70:71], s[70:71] op_sel:[0,0,1] op_sel_hi:[1,0,1]\nv_pk_fma_f32 v[54:55], v[54:55], s[70:71], s[70:71] op_sel:[0,0,1] op_sel_hi:[1,0,1]\nv_pk_fma_f32 v[56:57], v[56:57], s[70:71], s[70:71] op_sel:[0,0,1] op_sel_hi:[1,0,1]\nv_pk_fma_f32 v[58:59], v[58:59], s[70:71], s[70:71] op_sel:[0,0,1] op_sel_hi:[1,0,1]\nv_pk_fma_f32 v[60:61], v[60:61], s[70:71], s[70:71] op_sel:[0,0,1] op_sel_hi:[1,0,1]\nv_pk_fma_f32 v[62:63], v[62:63], s[70:71], s[70:71] op_sel:[0,0,1] op_sel_hi:[1,0,1]\n"
"v_mfma_f32_16x16x32_bf16 v[76:79], a[216:219], a[176:179], v[76:79]\n"
"v_cvt_pk_bf16_f32 v161, v162, v163\n"
"v_cvt_pk_bf16_f32 v162, v164, v165\n"
"v_mfma_f32_16x16x32_bf16 v[76:79], a[220:223], a[180:183], v[76:79]\n"
"s_nop 0\n"
"v_permlane16_swap_b32 v160, v162\n"
"v_mfma_f32_16x16x32_bf16 v[88:91], a[240:243], a[176:179], v[88:91]\n"
"v_cvt_pk_bf16_f32 v163, v166, v167\n"
"v_accvgpr_write_b32 a120, v160\n"
"v_mfma_f32_16x16x32_bf16 v[88:91], a[244:247], a[180:183], v[88:91]\n"
"v_permlane16_swap_b32 v161, v163\n"
"v_mfma_f32_16x16x32_bf16 v[92:95], a[248:251], a[176:179], v[92:95]\n"
"v_accvgpr_write_b32 a121, v161\n"
"v_accvgpr_write_b32 a122, v162\n"
"v_mfma_f32_16x16x32_bf16 v[92:95], a[252:255], a[180:183], v[92:95]\n"
"v_accvgpr_write_b32 a123, v163\n"
"v_cvt_pk_bf16_f32 v168, v168, v169\n"
"v_mfma_f32_16x16x32_bf16 v[80:83], a[224:227], a[176:179], v[80:83]\n"
"v_cvt_pk_bf16_f32 v169, v170, v171\n"
"v_cvt_pk_bf16_f32 v170, v172, v173\n"
"v_mfma_f32_16x16x32_bf16 v[80:83], a[228:231], a[180:183], v[80:83]\n"
"s_nop 0\n"
"v_permlane16_swap_b32 v168, v170\n"
"v_mfma_f32_16x16x32_bf16 v[84:87], a[232:235], a[176:179], v[84:87]\n"
"v_cvt_pk_bf16_f32 v171, v174, v175\n"
"v_accvgpr_write_b32 a124, v168\n"
"v_mfma_f32_16x16x32_bf16 v[84:87], a[236:239], a[180:183], v[84:87]\n"
"s_waitcnt lgkmcnt(0)\n"
"v_permlane16_swap_b32 v169, v171\n"
"v_mfma_f32_16x16x32_bf16 v[180:183], v[208:211], a[0:3], 0\n"
"v_accvgpr_write_b32 a125, v169\n"
"v_accvgpr_write_b32 a126, v170\n"
"v_mfma_f32_16x16x32_bf16 v[180:183], v[212:215], a[4:7], v[180:183]\n"
"v_accvgpr_write_b32 a127, v171\n"
"v_accvgpr_write_b32 a128, v176\n"
"v_mfma_f32_16x16x32_bf16 v[180:183], v[216:219], a[8:11], v[180:183]\n"
"v_accvgpr_write_b32 a129, v177\n"
"v_accvgpr_write_b32 a130, v178\n"
"v_mfma_f32_16x16x32_bf16 v[180:183], v[220:223], a[12:15], v[180:183]\n"
"v_accvgpr_write_b32 a131, v179\n"
"v_accvgpr_write_b32 a104, v128\n"
"v_mfma_f32_16x16x32_bf16 v[184:187], v[208:211], a[16:19], 0\n"
"v_accvgpr_write_b32 a105, v129\n"
"v_accvgpr_write_b32 a108, v136\n"
"v_mfma_f32_16x16x32_bf16 v[184:187], v[212:215], a[20:23], v[184:187]\n"
"v_pk_fma_f32 v[64:65], v[64:65], s[70:71], s[70:71] op_sel:[0,0,1] op_sel_hi:[1,0,1]\nv_pk_fma_f32 v[66:67], v[66:67], s[70:71], s[70:71] op_sel:[0,0,1] op_sel_hi:[1,0,1]\nv_pk_fma_f32 v[68:69], v[68:69], s[70:71], s[70:71] op_sel:[0,0,1] op_sel_hi:[1,0,1]\nv_pk_fma_f32 v[70:71], v[70:71], s[70:71], s[70:71] op_sel:[0,0,1] op_sel_hi:[1,0,1]\nv_pk_fma_f32 v[72:73], v[72:73], s[70:71], s[70:71] op_sel:[0,0,1] op_sel_hi:[1,0,1]\nv_pk_fma_f32 v[74:75], v[74:75], s[70:71], s[70:71] op_sel:[0,0,1] op_sel_hi:[1,0,1]\nv_pk_fma_f32 v[76:77], v[76:77], s[70:71], s[70:71] op_sel:[0,0,1] op_sel_hi:[1,0,1]\nv_pk_fma_f32 v[78:79], v[78:79], s[70:71], s[70:71] op_sel:[0,0,1] op_sel_hi:[1,0,1]\nv_pk_fma_f32 v[80:81], v[80:81], s[70:71], s[70:71] op_sel:[0,0,1] op_sel_hi:[1,0,1]\nv_pk_fma_f32 v[82:83], v[82:83], s[70:71], s[70:71] op_sel:[0,0,1] op_sel_hi:[1,0,1]\nv_pk_fma_f32 v[84:85], v[84:85], s[70:71], s[70:71] op_sel:[0,0,1] op_sel_hi:[1,0,1]\nv_pk_fma_f32 v[86:87], v[86:87], s[70:71], s[70:71] op_sel:[0,0,1] op_sel_hi:[1,0,1]\nv_pk_fma_f32 v[88:89], v[88:89], s[70:71], s[70:71] op_sel:[0,0,1] op_sel_hi:[1,0,1]\nv_pk_fma_f32 v[90:91], v[90:91], s[70:71], s[70:71] op_sel:[0,0,1] op_sel_hi:[1,0,1]\nv_pk_fma_f32 v[92:93], v[92:93], s[70:71], s[70:71] op_sel:[0,0,1] op_sel_hi:[1,0,1]\nv_pk_fma_f32 v[94:95], v[94:95], s[70:71], s[70:71] op_sel:[0,0,1] op_sel_hi:[1,0,1]\n"
"v_mfma_f32_16x16x32_bf16 v[184:187], v[216:219], a[24:27], v[184:187]\n"
"v_accvgpr_write_b32 a109, v137\n"
"v_accvgpr_write_b32 a112, v144\n"
"v_mfma_f32_16x16x32_bf16 v[184:187], v[220:223], a[28:31], v[184:187]\n"
"v_accvgpr_write_b32 a113, v145\n"
"v_accvgpr_write_b32 a106, v130\n"
"v_mfma_f32_16x16x32_bf16 v[188:191], v[208:211], a[32:35], 0\n"
"v_accvgpr_write_b32 a107, v131\n"
"v_accvgpr_write_b32 a110, v138\n"
"v_mfma_f32_16x16x32_bf16 v[188:191], v[212:215], a[36:39], v[188:191]\n"
"v_sub_f32 v180, s74, v180\n"
"v_sub_f32 v181, s74, v181\n"
"v_mfma_f32_16x16x32_bf16 v[188:191], v[216:219], a[40:43], v[188:191]\n"
"v_sub_f32 v182, s74, v182\n"
"v_sub_f32 v183, s74, v183\n"
"v_mfma_f32_16x16x32_bf16 v[188:191], v[220:223], a[44:47], v[188:191]\n"
"v_accvgpr_write_b32 a111, v139\n"
"v_cvt_pk_bf16_f32 v96, v96, v97\n"
"v_mfma_f32_16x16x32_bf16 v[192:195], v[208:211], a[192:195], 0\n"
"ds_read_b128 v[208:211], v228 offset:0\n"
"v_cvt_pk_bf16_f32 v97, v98, v99\n"
"v_mfma_f32_16x16x32_bf16 v[192:195], v[212:215], a[196:199], v[192:195]\n"
"ds_read_b128 v[212:215], v229 offset:0\n"
"v_sub_f32 v184, s74, v184\n"
"v_mfma_f32_16x16x32_bf16 v[192:195], v[216:219], a[200:203], v[192:195]\n"
"ds_read_b128 v[216:219], v228 offset:1024\n"
"v_sub_f32 v185, s74, v185\n"
"v_mfma_f32_16x16x32_bf16 v[192:195], v[220:223], a[204:207], v[192:195]\n"
"s_waitcnt lgkmcnt(2)\n"
"ds_read_b128 v[220:223], v229 offset:1024\n"
"v_sub_f32 v186, s74, v186\n"
"v_mfma_f32_16x16x32_bf16 v[152:155], v[208:211], a[0:3], v[180:183]\n"
"s_waitcnt lgkmcnt(2)\n"
"v_sub_f32 v187, s74, v187\n"
"v_cvt_pk_bf16_f32 v98, v100, v101\n"
"v_mfma_f32_16x16x32_bf16 v[152:155], v[212:215], a[4:7], v[152:155]\n"
"s_waitcnt lgkmcnt(1)\n"
"v_sub_f32 v188, s74, v188\n"
"v_sub_f32 v189, s74, v189\n"
"v_mfma_f32_16x16x32_bf16 v[152:155], v[216:219], a[8:11], v[152:155]\n"
"s_waitcnt lgkmcnt(0)\n"
"v_sub_f32 v190, s74, v190\n"
"v_sub_f32 v191, s74, v191\n"
"v_mfma_f32_16x16x32_bf16 v[152:155], v[220:223], a[12:15], v[152:155]\n"
"v_permlane16_swap_b32 v96, v98\n"
"v_mfma_f32_16x16x32_bf16 v[156:159], v[216:219], a[24:27], v[184:187]\n"
"v_cvt_pk_bf16_f32 v99, v102, v103\n"
"v_cvt_pk_bf16_f32 v104, v104, v105\n"
"v_mfma_f32_16x16x32_bf16 v[156:159], v[220:223], a[28:31], v[156:159]\n"
"v_sub_f32 v192, s74, v192\n"
"v_sub_f32 v193, s74, v193\n"
"v_mfma_f32_16x16x32_bf16 v[156:159], v[208:211], a[16:19], v[156:159]\n"
"v_sub_f32 v194, s74, v194\n"
"v_sub_f32 v195, s74, v195\n"
"v_mfma_f32_16x16x32_bf16 v[156:159], v[212:215], a[20:23], v[156:159]\n"
"v_permlane16_swap_b32 v97, v99\n"
"v_mfma_f32_16x16x32_bf16 v[160:163], v[208:211], a[32:35], v[188:191]\n"
"ds_write_b128 v243, v[96:99] offset:12288\n"
"v_mfma_f32_16x16x32_bf16 v[160:163], v[212:215], a[36:39], v[160:163]\n"
"v_exp_f32 v152, v152\n"
"v_mfma_f32_16x16x32_bf16 v[160:163], v[216:219], a[40:43], v[160:163]\n"
"v_exp_f32 v153, v153\n"
"v_mfma_f32_16x16x32_bf16 v[160:163], v[220:223], a[44:47], v[160:163]\n"
"v_exp_f32 v154, v154\n"
"v_mfma_f32_16x16x32_bf16 v[164:167], v[216:219], a[200:203], v[192:195]\n"
"ds_read_b128 v[216:219], v228 offset:5120\n"
"v_cvt_pk_bf16_f32 v128, v152, v153\n"
"v_mfma_f32_16x16x32_bf16 v[164:167], v[220:223], a[204:207], v[164:167]\n"
"ds_read_b128 v[220:223], v229 offset:5120\n"
"v_cvt_pk_bf16_f32 v105, v106, v107\n"
"v_mfma_f32_16x16x32_bf16 v[164:167], v[208:211], a[192:195], v[164:167]\n"
"ds_read_b128 v[208:211], v228 offset:4096\n"
"v_cvt_pk_bf16_f32 v106, v108, v109\n"
"v_mfma_f32_16x16x32_bf16 v[164:167], v[212:215], a[196:199], v[164:167]\n"
"s_waitcnt lgkmcnt(2)\n"
"ds_read_b128 v[212:215], v229 offset:4096\n"
"v_cvt_pk_bf16_f32 v107, v110, v111\n"
"v_mfma_f32_16x16x32_bf16 v[168:171], v[216:219], a[8:11], v[180:183]\n"
"s_waitcnt lgkmcnt(2)\n"
"v_exp_f32 v155, v155\n"
"v_mfma_f32_16x16x32_bf16 v[168:171], v[220:223], a[12:15], v[168:171]\n"
"s_waitcnt lgkmcnt(1)\n"
"v_exp_f32 v156, v156\n"
"v_mfma_f32_16x16x32_bf16 v[168:171], v[208:211], a[0:3], v[168:171]\n"
"s_waitcnt lgkmcnt(0)\n"
"v_exp_f32 v157, v157\n"
"v_mfma_f32_16x16x32_bf16 v[168:171], v[212:215], a[4:7], v[168:171]\n"
"v_exp_f32 v158, v158\n"
"v_mfma_f32_16x16x32_bf16 v[172:175], v[208:211], a[16:19], v[184:187]\n"
"v_exp_f32 v159, v159\n"
"v_mfma_f32_16x16x32_bf16 v[172:175], v[212:215], a[20:23], v[172:175]\n"
"v_exp_f32 v160, v160\n"
"v_mfma_f32_16x16x32_bf16 v[172:175], v[216:219], a[24:27], v[172:175]\n"
"v_exp_f32 v161, v161\n"
"v_mfma_f32_16x16x32_bf16 v[172:175], v[220:223], a[28:31], v[172:175]\n"
"v_exp_f32 v162, v162\n"
"v_mfma_f32_16x16x32_bf16 v[176:179], v[216:219], a[40:43], v[188:191]\n"
"v_exp_f32 v163, v163\n"
"v_mfma_f32_16x16x32_bf16 v[176:179], v[220:223], a[44:47], v[176:179]\n"
"v_cvt_pk_bf16_f32 v129, v154, v155\n"
"v_cvt_pk_bf16_f32 v136, v156, v157\n"
"v_mfma_f32_16x16x32_bf16 v[176:179], v[208:211], a[32:35], v[176:179]\n"
"v_exp_f32 v164, v164\n"
"v_mfma_f32_16x16x32_bf16 v[176:179], v[212:215], a[36:39], v[176:179]\n"
"v_exp_f32 v165, v165\n"
"v_mfma_f32_16x16x32_bf16 v[152:155], v[208:211], a[192:195], v[192:195]\n"
"ds_read_b128 v[208:211], v230 offset:0\n"
"v_cvt_pk_bf16_f32 v137, v158, v159\n"
"v_mfma_f32_16x16x32_bf16 v[152:155], v[212:215], a[196:199], v[152:155]\n"
"ds_read_b128 v[212:215], v231 offset:0\n"
"v_cvt_pk_bf16_f32 v144, v160, v161\n"
"v_mfma_f32_16x16x32_bf16 v[152:155], v[216:219], a[200:203], v[152:155]\n"
"ds_read_b128 v[216:219], v230 offset:1024\n"
"v_cvt_pk_bf16_f32 v145, v162, v163\n"
"v_mfma_f32_16x16x32_bf16 v[152:155], v[220:223], a[204:207], v[152:155]\n"
"s_waitcnt lgkmcnt(2)\n"
"ds_read_b128 v[220:223], v231 offset:1024\n"
"v_cvt_pk_bf16_f32 v124, v164, v165\n"
"v_mfma_f32_16x16x32_bf16 v[156:159], v[208:211], a[0:3], v[180:183]\n"
"s_waitcnt lgkmcnt(2)\n"
"v_exp_f32 v166, v166\n"
"v_mfma_f32_16x16x32_bf16 v[156:159], v[212:215], a[4:7], v[156:159]\n"
"s_waitcnt lgkmcnt(1)\n"
"v_exp_f32 v167, v167\n"
"v_mfma_f32_16x16x32_bf16 v[156:159], v[216:219], a[8:11], v[156:159]\n"
"s_waitcnt lgkmcnt(0)\n"
"v_exp_f32 v168, v168\n"
"v_mfma_f32_16x16x32_bf16 v[156:159], v[220:223], a[12:15], v[156:159]\n"
"v_exp_f32 v169, v169\n"
"v_mfma_f32_16x16x32_bf16 v[160:163], v[216:219], a[24:27], v[184:187]\n"
"v_exp_f32 v170, v170\n"
"v_mfma_f32_16x16x32_bf16 v[160:163], v[220:223], a[28:31], v[160:163]\n"
"v_exp_f32 v171, v171\n"
"v_mfma_f32_16x16x32_bf16 v[160:163], v[208:211], a[16:19], v[160:163]\n"
"v_cvt_pk_bf16_f32 v125, v166, v167\n"
"v_cvt_pk_bf16_f32 v130, v168, v169\n"
"v_mfma_f32_16x16x32_bf16 v[160:163], v[212:215], a[20:23], v[160:163]\n"
"v_exp_f32 v172, v172\n"
"v_mfma_f32_16x16x32_bf16 v[164:167], v[208:211], a[32:35], v[188:191]\n"
"ds_read_b128 v[208:211], v230 offset:4096\n"
"v_cvt_pk_bf16_f32 v131, v170, v171\n"
"v_mfma_f32_16x16x32_bf16 v[164:167], v[212:215], a[36:39], v[164:167]\n"
"ds_read_b128 v[212:215], v231 offset:4096\n"
"v_accvgpr_write_b32 a114, v146\n"
"v_mfma_f32_16x16x32_bf16 v[164:167], v[216:219], a[40:43], v[164:167]\n"
"ds_read_b128 v[216:219], v230 offset:5120\n"
"v_accvgpr_write_b32 a115, v147\n"
"v_mfma_f32_16x16x32_bf16 v[164:167], v[220:223], a[44:47], v[164:167]\n"
"s_waitcnt lgkmcnt(2)\n"
"ds_read_b128 v[220:223], v231 offset:5120\n"
"v_cvt_pk_bf16_f32 v8, v8, v9\n"
"v_mfma_f32_16x16x32_bf16 v[168:171], v[208:211], a[0:3], v[180:183]\n"
"s_waitcnt lgkmcnt(2)\n"
"v_exp_f32 v173, v173\n"
"v_mfma_f32_16x16x32_bf16 v[168:171], v[212:215], a[4:7], v[168:171]\n"
"s_waitcnt lgkmcnt(1)\n"
"v_exp_f32 v174, v174\n"
"v_mfma_f32_16x16x32_bf16 v[168:171], v[216:219], a[8:11], v[168:171]\n"
"s_waitcnt lgkmcnt(0)\n"
"v_exp_f32 v175, v175\n"
"v_mfma_f32_16x16x32_bf16 v[168:171], v[220:223], a[12:15], v[168:171]\n"
"v_cvt_pk_bf16_f32 v138, v172, v173\n"
"v_cvt_pk_bf16_f32 v139, v174, v175\n"
"v_mfma_f32_16x16x32_bf16 v[172:175], v[216:219], a[24:27], v[184:187]\n"
"v_permlane16_swap_b32 v104, v106\n"
"v_mfma_f32_16x16x32_bf16 v[172:175], v[220:223], a[28:31], v[172:175]\n"
"v_permlane16_swap_b32 v105, v107\n"
"v_mfma_f32_16x16x32_bf16 v[172:175], v[208:211], a[16:19], v[172:175]\n"
"ds_write_b128 v243, v[104:107] offset:14336\n"
"v_exp_f32 v176, v176\n"
"v_exp_f32 v177, v177\n"
"v_exp_f32 v178, v178\n"
"v_exp_f32 v179, v179\n"
"v_mfma_f32_16x16x32_bf16 v[172:175], v[212:215], a[20:23], v[172:175]\n"
"s_cbranch_vccnz LPCE0_%=\nLPKE0_%=:\n"
"v_cvt_pk_bf16_f32 v146, v176, v177\n"
"v_cvt_pk_bf16_f32 v147, v178, v179\n"
"v_mfma_f32_16x16x32_bf16 v[176:179], v[208:211], a[32:35], v[188:191]\n"
"v_exp_f32 v152, v152\n"
"v_mfma_f32_16x16x32_bf16 v[176:179], v[212:215], a[36:39], v[176:179]\n"
"v_exp_f32 v153, v153\n"
"v_mfma_f32_16x16x32_bf16 v[176:179], v[216:219], a[40:43], v[176:179]\n"
"v_exp_f32 v154, v154\n"
"v_mfma_f32_16x16x32_bf16 v[176:179], v[220:223], a[44:47], v[176:179]\n"
"v_exp_f32 v155, v155\n"
"s_waitcnt vmcnt(8) lgkmcnt(0)\n"
"s_barrier\n"
"ds_read_b128 v[208:211], v228 offset:16384\n"
"ds_read_b128 v[212:215], v229 offset:16384\n"
"ds_read_b128 v[216:219], v228 offset:17408\n"
"ds_read_b128 v[220:223], v229 offset:17408\n"
"v_cvt_pk_bf16_f32 v126, v152, v153\n"
"v_cvt_pk_bf16_f32 v127, v154, v155\n"
"s_waitcnt lgkmcnt(3)\n"
"v_mfma_f32_16x16x32_bf16 v[152:155], v[208:211], a[0:3], v[180:183]\n"
"v_exp_f32 v156, v156\n"
"s_waitcnt lgkmcnt(2)\n"
"v_mfma_f32_16x16x32_bf16 v[152:155], v[212:215], a[4:7], v[152:155]\n"
"v_exp_f32 v157, v157\n"
"s_waitcnt lgkmcnt(1)\n"
"v_mfma_f32_16x16x32_bf16 v[152:155], v[216:219], a[8:11], v[152:155]\n"
"s_waitcnt lgkmcnt(0)\n"
"s_add_u32 m0, s44, 98304\n"
"v_exp_f32 v158, v158\n"
"v_exp_f32 v159, v159\n"
"v_mfma_f32_16x16x32_bf16 v[152:155], v[220:223], a[12:15], v[152:155]\n"
"buffer_load_dwordx4 v240, s[20:23], s56 offen nt lds\n"
"v_cvt_pk_bf16_f32 v132, v156, v157\n"
"v_cvt_pk_bf16_f32 v133, v158, v159\n"
"v_mfma_f32_16x16x32_bf16 v[156:159], v[216:219], a[24:27], v[184:187]\n"
"v_exp_f32 v160, v160\n"
"v_mfma_f32_16x16x32_bf16 v[156:159], v[220:223], a[28:31], v[156:159]\n"
"v_exp_f32 v161, v161\n"
"v_mfma_f32_16x16x32_bf16 v[156:159], v[208:211], a[16:19], v[156:159]\n"
"v_exp_f32 v162, v162\n"
"v_exp_f32 v163, v163\n"
"v_mfma_f32_16x16x32_bf16 v[156:159], v[212:215], a[20:23], v[156:159]\n"
"v_cvt_pk_bf16_f32 v140, v160, v161\n"
"v_cvt_pk_bf16_f32 v141, v162, v163\n"
"v_mfma_f32_16x16x32_bf16 v[160:163], v[208:211], a[32:35], v[188:191]\n"
"v_exp_f32 v164, v164\n"
"v_mfma_f32_16x16x32_bf16 v[160:163], v[212:215], a[36:39], v[160:163]\n"
"v_exp_f32 v165, v165\n"
"v_mfma_f32_16x16x32_bf16 v[160:163], v[216:219], a[40:43], v[160:163]\n"
"ds_read_b64_tr_b16 a[244:245], v234 offset:0\n"
"ds_read_b64_tr_b16 a[246:247], v234 offset:4096\n"
"v_exp_f32 v166, v166\n"
"v_exp_f32 v167, v167\n"
"v_mfma_f32_16x16x32_bf16 v[160:163], v[220:223], a[44:47], v[160:163]\n"
"v_cvt_pk_bf16_f32 v148, v164, v165\n"
"v_cvt_pk_bf16_f32 v149, v166, v167\n"
"v_mfma_f32_16x16x32_bf16 v[164:167], v[216:219], a[200:203], v[192:195]\n"
"ds_read_b64_tr_b16 a[248:249], v238 offset:0\n"
"ds_read_b64_tr_b16 a[250:251], v238 offset:4096\n"
"v_mfma_f32_16x16x32_bf16 v[164:167], v[220:223], a[204:207], v[164:167]\n"
"v_exp_f32 v168, v168\n"
"v_mfma_f32_16x16x32_bf16 v[164:167], v[208:211], a[192:195], v[164:167]\n"
"s_add_u32 s40, s56, s52\n"
"v_exp_f32 v169, v169\n"
"v_mfma_f32_16x16x32_bf16 v[164:167], v[212:215], a[196:199], v[164:167]\n"
"s_waitcnt lgkmcnt(2)\n"
"buffer_load_dwordx4 v240, s[20:23], s40 offen offset:1024 nt lds\n"
"v_cvt_pk_bf16_f32 v9, v10, v11\n"
"v_cvt_pk_bf16_f32 v10, v12, v13\n"
"v_cvt_pk_bf16_f32 v11, v14, v15\n"
"v_exp_f32 v170, v170\n"
"v_exp_f32 v171, v171\n"
"v_mfma_f32_16x16x32_bf16 a[216:219], v[124:127], a[244:247], 0\n"
"v_permlane16_swap_b32 v8, v10\n"
"v_permlane16_swap_b32 v9, v11\n"
"v_accvgpr_write_b32 a148, v8\n"
"v_accvgpr_write_b32 a149, v9\n"
"v_accvgpr_write_b32 a150, v10\n"
"v_accvgpr_write_b32 a151, v11\n"
"v_cvt_pk_bf16_f32 v134, v168, v169\n"
"v_cvt_pk_bf16_f32 v135, v170, v171\n"
"v_mfma_f32_16x16x32_bf16 v[8:11], v[128:131], a[244:247], 0\n"
"s_waitcnt lgkmcnt(0)\n"
"ds_read_b64_tr_b16 v[224:225], v239 offset:0\n"
"ds_read_b64_tr_b16 v[226:227], v239 offset:4096\n"
"v_cvt_pk_bf16_f32 v40, v40, v41\n"
"v_cvt_pk_bf16_f32 v41, v42, v43\n"
"v_cvt_pk_bf16_f32 v42, v44, v45\n"
"v_cvt_pk_bf16_f32 v43, v46, v47\n"
"v_exp_f32 v172, v172\n"
"v_exp_f32 v173, v173\n"
"v_exp_f32 v174, v174\n"
"v_exp_f32 v175, v175\n"
"v_mfma_f32_16x16x32_bf16 v[8:11], v[132:135], a[248:251], v[8:11]\n"
"ds_read_b64_tr_b16 a[252:253], v235 offset:0\n"
"ds_read_b64_tr_b16 a[254:255], v235 offset:4096\n"
"v_permlane16_swap_b32 v40, v42\n"
"v_permlane16_swap_b32 v41, v43\n"
"v_accvgpr_write_b32 a164, v40\n"
"v_accvgpr_write_b32 a165, v41\n"
"v_accvgpr_write_b32 a166, v42\n"
"v_accvgpr_write_b32 a167, v43\n"
"v_cvt_pk_bf16_f32 v142, v172, v173\n"
"v_cvt_pk_bf16_f32 v143, v174, v175\n"
"v_mfma_f32_16x16x32_bf16 v[40:43], v[136:139], a[244:247], 0\n"
"v_cvt_pk_bf16_f32 v72, v72, v73\n"
"v_cvt_pk_bf16_f32 v73, v74, v75\n"
"v_cvt_pk_bf16_f32 v74, v76, v77\n"
"v_cvt_pk_bf16_f32 v75, v78, v79\n"
"v_exp_f32 v176, v176\n"
"v_exp_f32 v177, v177\n"
"v_exp_f32 v178, v178\n"
"v_exp_f32 v179, v179\n"
"v_mfma_f32_16x16x32_bf16 v[40:43], v[140:143], a[248:251], v[40:43]\n"
"v_permlane16_swap_b32 v72, v74\n"
"v_permlane16_swap_b32 v73, v75\n"
"v_accvgpr_write_b32 a180, v72\n"
"v_accvgpr_write_b32 a181, v73\n"
"v_accvgpr_write_b32 a182, v74\n"
"v_accvgpr_write_b32 a183, v75\n"
"v_cvt_pk_bf16_f32 v150, v176, v177\n"
"v_cvt_pk_bf16_f32 v151, v178, v179\n"
"v_mfma_f32_16x16x32_bf16 v[72:75], v[144:147], a[244:247], 0\n"
"ds_read_b128 v[216:219], v228 offset:21504\n"
"v_cvt_pk_bf16_f32 v200, v200, v201\n"
"v_mfma_f32_16x16x32_bf16 v[72:75], v[148:151], a[248:251], v[72:75]\n"
"s_waitcnt lgkmcnt(3)\n"
"ds_read_b128 v[220:223], v229 offset:21504\n"
"v_cvt_pk_bf16_f32 v201, v202, v203\n"
"v_mfma_f32_16x16x32_bf16 v[76:79], v[148:151], v[224:227], 0\n"
"s_waitcnt lgkmcnt(2)\n"
"ds_read_b128 v[208:211], v228 offset:20480\n"
"v_cvt_pk_bf16_f32 v202, v204, v205\n"
"v_mfma_f32_16x16x32_bf16 v[76:79], v[144:147], a[252:255], v[76:79]\n"
"ds_read_b128 v[212:215], v229 offset:20480\n"
"v_cvt_pk_bf16_f32 v203, v206, v207\n"
"v_mfma_f32_16x16x32_bf16 v[44:47], v[140:143], v[224:227], 0\n"
"s_add_u32 s40, s56, s53\n"
"ds_read_b64_tr_b16 a[244:245], v234 offset:1024\n"
"v_accvgpr_write_b32 a240, v254\n"
"v_mfma_f32_16x16x32_bf16 v[44:47], v[136:139], a[252:255], v[44:47]\n"
"buffer_load_dwordx4 v240, s[20:23], s40 offen offset:2048 nt lds\n"
"v_mfma_f32_16x16x32_bf16 v[12:15], v[132:135], v[224:227], 0\n"
"v_accvgpr_write_b32 a241, v254\n"
"v_accvgpr_write_b32 a242, v254\n"
"v_mfma_f32_16x16x32_bf16 v[12:15], v[128:131], a[252:255], v[12:15]\n"
"v_accvgpr_write_b32 a243, v254\n"
"v_mov_b32 v196, 0\n"
"v_mov_b32 v197, 0\n"
"v_mov_b32 v198, 0\n"
"v_mov_b32 v199, 0\n"
"v_mfma_f32_16x16x32_bf16 a[220:223], v[124:127], a[252:255], 0\n"
"v_permlane16_swap_b32 v200, v202\n"
"v_permlane16_swap_b32 v201, v203\n"
"v_accvgpr_write_b32 a142, v202\n"
"v_accvgpr_write_b32 a143, v203\n"
"v_mov_b32 v202, 0\n"
"v_mov_b32 v203, 0\n"
"v_mov_b32 v204, 0\n"
"v_mov_b32 v205, 0\n"
"v_smfmac_f32_16x16x64_bf16 v[196:199], a[240:243], v[128:135], v241\n"
"ds_read_b64_tr_b16 a[246:247], v234 offset:5120\n"
"v_cvt_pk_bf16_f32 v24, v24, v25\n"
"v_mfma_f32_16x16x32_bf16 v[202:205], a[240:243], v[124:127], v[202:205]\n"
"s_waitcnt lgkmcnt(3)\n"
"ds_read_b64_tr_b16 a[248:249], v238 offset:1024\n"
"v_cvt_pk_bf16_f32 v25, v26, v27\n"
"v_mfma_f32_16x16x32_bf16 v[168:171], v[216:219], a[8:11], v[180:183]\n"
"ds_read_b64_tr_b16 a[250:251], v238 offset:5120\n"
"v_cvt_pk_bf16_f32 v26, v28, v29\n"
"v_mfma_f32_16x16x32_bf16 v[168:171], v[220:223], a[12:15], v[168:171]\n"
"ds_read_b64_tr_b16 v[224:225], v239 offset:1024\n"
"v_cvt_pk_bf16_f32 v27, v30, v31\n"
"v_mfma_f32_16x16x32_bf16 v[168:171], v[208:211], a[0:3], v[168:171]\n"
"s_waitcnt lgkmcnt(4)\n"
"ds_read_b64_tr_b16 v[226:227], v239 offset:5120\n"
"v_cvt_pk_bf16_f32 v56, v56, v57\n"
"v_mfma_f32_16x16x32_bf16 v[168:171], v[212:215], a[4:7], v[168:171]\n"
"ds_read_b64_tr_b16 a[252:253], v235 offset:1024\n"
"v_cvt_pk_bf16_f32 v57, v58, v59\n"
"v_mfma_f32_16x16x32_bf16 v[172:175], v[208:211], a[16:19], v[184:187]\n"
"v_exp_f32 v152, v152\n"
"v_mfma_f32_16x16x32_bf16 v[172:175], v[212:215], a[20:23], v[172:175]\n"
"ds_read_b64_tr_b16 a[254:255], v235 offset:5120\n"
"v_cvt_pk_bf16_f32 v58, v60, v61\n"
"v_mfma_f32_16x16x32_bf16 v[172:175], v[216:219], a[24:27], v[172:175]\n"
"s_add_u32 s40, s56, s54\n"
"v_exp_f32 v153, v153\n"
"v_mfma_f32_16x16x32_bf16 v[172:175], v[220:223], a[28:31], v[172:175]\n"
"buffer_load_dwordx4 v240, s[20:23], s40 offen offset:3072 nt lds\n"
"v_mfma_f32_16x16x32_bf16 v[176:179], v[216:219], a[40:43], v[188:191]\n"
"s_add_u32 s55, s55, s59\n"
"v_exp_f32 v154, v154\n"
"v_mfma_f32_16x16x32_bf16 v[176:179], v[220:223], a[44:47], v[176:179]\n"
"s_add_u32 s41, s55, s57\n"
"v_exp_f32 v155, v155\n"
"v_mfma_f32_16x16x32_bf16 v[176:179], v[208:211], a[32:35], v[176:179]\n"
"s_and_b32 s41, s41, s58\n"
"v_cvt_pk_bf16_f32 v96, v152, v153\n"
"v_cvt_pk_bf16_f32 v97, v154, v155\n"
"v_mfma_f32_16x16x32_bf16 v[176:179], v[212:215], a[36:39], v[176:179]\n"
"s_andn2_b32 s56, s55, s58\n"
"v_permlane16_swap_b32 v24, v26\n"
"v_mfma_f32_16x16x32_bf16 v[152:155], v[208:211], a[192:195], v[192:195]\n"
"s_or_b32 s56, s56, s41\n"
"ds_read_b128 v[208:211], v230 offset:16384\n"
"v_accvgpr_write_b32 a156, v24\n"
"v_mfma_f32_16x16x32_bf16 v[152:155], v[212:215], a[196:199], v[152:155]\n"
"ds_read_b128 v[212:215], v231 offset:16384\n"
"v_accvgpr_write_b32 a158, v26\n"
"v_mfma_f32_16x16x32_bf16 v[152:155], v[216:219], a[200:203], v[152:155]\n"
"ds_read_b128 v[216:219], v230 offset:17408\n"
"v_cvt_pk_bf16_f32 v59, v62, v63\n"
"v_mfma_f32_16x16x32_bf16 v[152:155], v[220:223], a[204:207], v[152:155]\n"
"s_waitcnt lgkmcnt(3)\n"
"v_permlane16_swap_b32 v25, v27\n"
"v_mfma_f32_16x16x32_bf16 a[232:235], v[124:127], a[244:247], 0\n"
"v_accvgpr_write_b32 a157, v25\n"
"v_accvgpr_write_b32 a159, v27\n"
"v_mfma_f32_16x16x32_bf16 v[24:27], v[128:131], a[244:247], 0\n"
"s_add_u32 m0, s44, 49152\n"
"v_permlane16_swap_b32 v56, v58\n"
"v_mfma_f32_16x16x32_bf16 v[24:27], v[132:135], a[248:251], v[24:27]\n"
"buffer_load_dwordx4 v240, s[16:19], s56 offen nt lds\n"
"v_permlane16_swap_b32 v57, v59\n"
"v_accvgpr_write_b32 a172, v56\n"
"v_accvgpr_write_b32 a173, v57\n"
"v_accvgpr_write_b32 a174, v58\n"
"v_accvgpr_write_b32 a175, v59\n"
"v_mfma_f32_16x16x32_bf16 v[56:59], v[136:139], a[244:247], 0\n"
"v_cvt_pk_bf16_f32 v88, v88, v89\n"
"v_cvt_pk_bf16_f32 v89, v90, v91\n"
"v_cvt_pk_bf16_f32 v90, v92, v93\n"
"v_cvt_pk_bf16_f32 v91, v94, v95\n"
"v_mfma_f32_16x16x32_bf16 v[56:59], v[140:143], a[248:251], v[56:59]\n"
"v_permlane16_swap_b32 v88, v90\n"
"v_permlane16_swap_b32 v89, v91\n"
"v_accvgpr_write_b32 a188, v88\n"
"v_accvgpr_write_b32 a189, v89\n"
"v_accvgpr_write_b32 a190, v90\n"
"v_accvgpr_write_b32 a191, v91\n"
"v_mfma_f32_16x16x32_bf16 v[88:91], v[144:147], a[244:247], 0\n"
"v_exp_f32 v156, v156\n"
"v_mfma_f32_16x16x32_bf16 v[88:91], v[148:151], a[248:251], v[88:91]\n"
"v_accvgpr_write_b32 a140, v200\n"
"v_accvgpr_write_b32 a141, v201\n"
"v_mfma_f32_16x16x32_bf16 v[92:95], v[148:151], v[224:227], 0\n"
"v_mov_b32 v200, 0\n"
"v_mov_b32 v201, 0\n"
"v_mfma_f32_16x16x32_bf16 v[92:95], v[144:147], a[252:255], v[92:95]\n"
"v_exp_f32 v157, v157\n"
"v_mfma_f32_16x16x32_bf16 v[60:63], v[140:143], v[224:227], 0\n"
"v_exp_f32 v158, v158\n"
"v_mfma_f32_16x16x32_bf16 v[60:63], v[136:139], a[252:255], v[60:63]\n"
"v_exp_f32 v159, v159\n"
"v_mfma_f32_16x16x32_bf16 v[28:31], v[132:135], v[224:227], 0\n"
"v_cvt_pk_bf16_f32 v104, v156, v157\n"
"v_cvt_pk_bf16_f32 v105, v158, v159\n"
"v_mfma_f32_16x16x32_bf16 v[28:31], v[128:131], a[252:255], v[28:31]\n"
"ds_read_b128 v[220:223], v231 offset:17408\n"
"v_accvgpr_write_b32 a96, v112\n"
"v_mfma_f32_16x16x32_bf16 a[236:239], v[124:127], a[252:255], 0\n"
"s_add_u32 s40, s56, s52\n"
"v_exp_f32 v160, v160\n"
"v_smfmac_f32_16x16x64_bf16 v[198:201], a[240:243], v[136:143], v241\n"
"s_waitcnt lgkmcnt(1)\n"
"buffer_load_dwordx4 v240, s[16:19], s40 offen offset:1024 nt lds\n"
"v_mfma_f32_16x16x32_bf16 v[156:159], v[208:211], a[0:3], v[180:183]\n"
"v_exp_f32 v161, v161\n"
"v_mfma_f32_16x16x32_bf16 v[156:159], v[212:215], a[4:7], v[156:159]\n"
"v_exp_f32 v162, v162\n"
"v_mfma_f32_16x16x32_bf16 v[156:159], v[216:219], a[8:11], v[156:159]\n"
"s_waitcnt lgkmcnt(0)\n"
"v_exp_f32 v163, v163\n"
"v_mfma_f32_16x16x32_bf16 v[156:159], v[220:223], a[12:15], v[156:159]\n"
"v_accvgpr_write_b32 a97, v113\n"
"v_cvt_pk_bf16_f32 v112, v160, v161\n"
"v_cvt_pk_bf16_f32 v113, v162, v163\n"
"v_mfma_f32_16x16x32_bf16 v[160:163], v[216:219], a[24:27], v[184:187]\n"
"v_exp_f32 v164, v164\n"
"v_mfma_f32_16x16x32_bf16 v[160:163], v[220:223], a[28:31], v[160:163]\n"
"v_exp_f32 v165, v165\n"
"v_mfma_f32_16x16x32_bf16 v[160:163], v[208:211], a[16:19], v[160:163]\n"
"ds_read_b64_tr_b16 a[244:245], v232 offset:0\n"
"ds_read_b64_tr_b16 a[246:247], v232 offset:4096\n"
"v_exp_f32 v166, v166\n"
"v_exp_f32 v167, v167\n"
"v_mfma_f32_16x16x32_bf16 v[160:163], v[212:215], a[20:23], v[160:163]\n"
"ds_read_b64_tr_b16 a[248:249], v236 offset:0\n"
"v_accvgpr_write_b32 a100, v120\n"
"v_accvgpr_write_b32 a101, v121\n"
"v_cvt_pk_bf16_f32 v120, v164, v165\n"
"v_cvt_pk_bf16_f32 v121, v166, v167\n"
"v_mfma_f32_16x16x32_bf16 v[164:167], v[208:211], a[32:35], v[188:191]\n"
"ds_read_b64_tr_b16 a[250:251], v236 offset:4096\n"
"v_cvt_pk_bf16_f32 v0, v0, v1\n"
"v_mfma_f32_16x16x32_bf16 v[164:167], v[212:215], a[36:39], v[164:167]\n"
"ds_read_b64_tr_b16 v[224:225], v237 offset:0\n"
"v_cvt_pk_bf16_f32 v1, v2, v3\n"
"v_mfma_f32_16x16x32_bf16 v[164:167], v[216:219], a[40:43], v[164:167]\n"
"ds_read_b64_tr_b16 v[226:227], v237 offset:4096\n"
"v_cvt_pk_bf16_f32 v2, v4, v5\n"
"v_mfma_f32_16x16x32_bf16 v[164:167], v[220:223], a[44:47], v[164:167]\n"
"s_waitcnt lgkmcnt(4)\n"
"v_cvt_pk_bf16_f32 v3, v6, v7\n"
"v_cvt_pk_bf16_f32 v32, v32, v33\n"
"v_mfma_f32_16x16x32_bf16 a[208:211], v[124:127], a[244:247], 0\n"
"v_permlane16_swap_b32 v0, v2\n"
"v_permlane16_swap_b32 v1, v3\n"
"v_accvgpr_write_b32 a144, v0\n"
"v_accvgpr_write_b32 a145, v1\n"
"v_accvgpr_write_b32 a146, v2\n"
"v_accvgpr_write_b32 a147, v3\n"
"v_mfma_f32_16x16x32_bf16 v[0:3], v[128:131], a[244:247], 0\n"
"s_waitcnt lgkmcnt(2)\n"
"s_add_u32 s40, s56, s53\n"
"ds_read_b64_tr_b16 a[252:253], v233 offset:0\n"
"ds_read_b64_tr_b16 a[254:255], v233 offset:4096\n"
"v_cvt_pk_bf16_f32 v33, v34, v35\n"
"v_cvt_pk_bf16_f32 v34, v36, v37\n"
"v_cvt_pk_bf16_f32 v35, v38, v39\n"
"v_mfma_f32_16x16x32_bf16 v[0:3], v[132:135], a[248:251], v[0:3]\n"
"buffer_load_dwordx4 v240, s[16:19], s40 offen offset:2048 nt lds\n"
"v_permlane16_swap_b32 v32, v34\n"
"v_permlane16_swap_b32 v33, v35\n"
"v_accvgpr_write_b32 a160, v32\n"
"v_accvgpr_write_b32 a161, v33\n"
"v_accvgpr_write_b32 a162, v34\n"
"v_accvgpr_write_b32 a163, v35\n"
"v_mfma_f32_16x16x32_bf16 v[32:35], v[136:139], a[244:247], 0\n"
"v_cvt_pk_bf16_f32 v64, v64, v65\n"
"v_cvt_pk_bf16_f32 v65, v66, v67\n"
"v_cvt_pk_bf16_f32 v66, v68, v69\n"
"v_cvt_pk_bf16_f32 v67, v70, v71\n"
"v_mfma_f32_16x16x32_bf16 v[32:35], v[140:143], a[248:251], v[32:35]\n"
"v_permlane16_swap_b32 v64, v66\n"
"v_permlane16_swap_b32 v65, v67\n"
"v_accvgpr_write_b32 a176, v64\n"
"v_accvgpr_write_b32 a177, v65\n"
"v_accvgpr_write_b32 a178, v66\n"
"v_accvgpr_write_b32 a179, v67\n"
"v_mfma_f32_16x16x32_bf16 v[64:67], v[144:147], a[244:247], 0\n"
"v_exp_f32 v168, v168\n"
"v_mfma_f32_16x16x32_bf16 v[64:67], v[148:151], a[248:251], v[64:67]\n"
"s_waitcnt lgkmcnt(2)\n"
"v_exp_f32 v169, v169\n"
"v_mfma_f32_16x16x32_bf16 v[68:71], v[148:151], v[224:227], 0\n"
"s_waitcnt lgkmcnt(0)\n"
"v_exp_f32 v170, v170\n"
"v_mfma_f32_16x16x32_bf16 v[68:71], v[144:147], a[252:255], v[68:71]\n"
"ds_read_b128 v[208:211], v230 offset:20480\n"
"v_cvt_pk_bf16_f32 v98, v168, v169\n"
"v_mfma_f32_16x16x32_bf16 v[36:39], v[140:143], v[224:227], 0\n"
"ds_read_b128 v[212:215], v231 offset:20480\n"
"v_accvgpr_write_b32 a98, v114\n"
"v_mfma_f32_16x16x32_bf16 v[36:39], v[136:139], a[252:255], v[36:39]\n"
"ds_read_b128 v[216:219], v230 offset:21504\n"
"v_accvgpr_write_b32 a99, v115\n"
"v_mfma_f32_16x16x32_bf16 v[4:7], v[132:135], v[224:227], 0\n"
"s_add_u32 s40, s56, s54\n"
"ds_read_b128 v[220:223], v231 offset:21504\n"
"v_cvt_pk_bf16_f32 v16, v16, v17\n"
"v_mfma_f32_16x16x32_bf16 v[4:7], v[128:131], a[252:255], v[4:7]\n"
"buffer_load_dwordx4 v240, s[16:19], s40 offen offset:3072 nt lds\n"
"v_mfma_f32_16x16x32_bf16 a[212:215], v[124:127], a[252:255], 0\n"
"v_exp_f32 v171, v171\n"
"v_smfmac_f32_16x16x64_bf16 v[200:203], a[240:243], v[144:151], v241\n"
"s_waitcnt lgkmcnt(3)\n"
"v_cvt_pk_bf16_f32 v99, v170, v171\n"
"v_cvt_pk_bf16_f32 v17, v18, v19\n"
"v_mfma_f32_16x16x32_bf16 v[168:171], v[208:211], a[0:3], v[180:183]\n"
"s_waitcnt lgkmcnt(2)\n"
"v_exp_f32 v172, v172\n"
"v_mfma_f32_16x16x32_bf16 v[168:171], v[212:215], a[4:7], v[168:171]\n"
"s_waitcnt lgkmcnt(1)\n"
"v_exp_f32 v173, v173\n"
"v_mfma_f32_16x16x32_bf16 v[168:171], v[216:219], a[8:11], v[168:171]\n"
"s_waitcnt lgkmcnt(0)\n"
"v_exp_f32 v174, v174\n"
"v_exp_f32 v175, v175\n"
"v_mfma_f32_16x16x32_bf16 v[168:171], v[220:223], a[12:15], v[168:171]\n"
"v_cvt_pk_bf16_f32 v106, v172, v173\n"
"v_cvt_pk_bf16_f32 v107, v174, v175\n"
"v_mfma_f32_16x16x32_bf16 v[172:175], v[216:219], a[24:27], v[184:187]\n"
"v_exp_f32 v176, v176\n"
"v_mfma_f32_16x16x32_bf16 v[172:175], v[220:223], a[28:31], v[172:175]\n"
"v_exp_f32 v177, v177\n"
"v_mfma_f32_16x16x32_bf16 v[172:175], v[208:211], a[16:19], v[172:175]\n"
"ds_read_b64_tr_b16 a[244:245], v232 offset:1024\n"
"ds_read_b64_tr_b16 a[246:247], v232 offset:5120\n"
"v_exp_f32 v178, v178\n"
"v_exp_f32 v179, v179\n"
"v_mfma_f32_16x16x32_bf16 v[172:175], v[212:215], a[20:23], v[172:175]\n"
"s_cbranch_vccnz LPCE1_%=\nLPKE1_%=:\n"
"ds_read_b64_tr_b16 a[248:249], v236 offset:1024\n"
"v_cvt_pk_bf16_f32 v114, v176, v177\n"
"v_cvt_pk_bf16_f32 v115, v178, v179\n"
"v_mfma_f32_16x16x32_bf16 v[176:179], v[208:211], a[32:35], v[188:191]\n"
"ds_read_b64_tr_b16 a[250:251], v236 offset:5120\n"
"v_cvt_pk_bf16_f32 v18, v20, v21\n"
"v_mfma_f32_16x16x32_bf16 v[176:179], v[212:215], a[36:39], v[176:179]\n"
"s_nop 0\n"
"v_permlane16_swap_b32 v16, v18\n"
"v_mfma_f32_16x16x32_bf16 v[176:179], v[216:219], a[40:43], v[176:179]\n"
"v_cvt_pk_bf16_f32 v19, v22, v23\n"
"v_accvgpr_write_b32 a152, v16\n"
"v_mfma_f32_16x16x32_bf16 v[176:179], v[220:223], a[44:47], v[176:179]\n"
"s_waitcnt lgkmcnt(2)\n"
"v_permlane16_swap_b32 v17, v19\n"
"v_mfma_f32_16x16x32_bf16 a[224:227], v[124:127], a[244:247], 0\n"
"ds_read_b64_tr_b16 v[224:225], v237 offset:1024\n"
"ds_read_b64_tr_b16 v[226:227], v237 offset:5120\n"
"v_accvgpr_write_b32 a153, v17\n"
"v_accvgpr_write_b32 a154, v18\n"
"v_accvgpr_write_b32 a155, v19\n"
"v_mfma_f32_16x16x32_bf16 v[16:19], v[128:131], a[244:247], 0\n"
"s_waitcnt lgkmcnt(2)\n"
"s_waitcnt vmcnt(8)\n"
"ds_read_b64_tr_b16 a[252:253], v233 offset:1024\n"
"ds_read_b64_tr_b16 a[254:255], v233 offset:5120\n"
"v_cvt_pk_bf16_f32 v48, v48, v49\n"
"v_cvt_pk_bf16_f32 v49, v50, v51\n"
"v_cvt_pk_bf16_f32 v50, v52, v53\n"
"v_cvt_pk_bf16_f32 v51, v54, v55\n"
"v_mfma_f32_16x16x32_bf16 v[16:19], v[132:135], a[248:251], v[16:19]\n"
"s_barrier\n"
"v_permlane16_swap_b32 v48, v50\n"
"v_permlane16_swap_b32 v49, v51\n"
"v_accvgpr_write_b32 a168, v48\n"
"v_accvgpr_write_b32 a169, v49\n"
"v_accvgpr_write_b32 a170, v50\n"
"v_accvgpr_write_b32 a171, v51\n"
"v_mfma_f32_16x16x32_bf16 v[48:51], v[136:139], a[244:247], 0\n"
"v_cvt_pk_bf16_f32 v80, v80, v81\n"
"v_cvt_pk_bf16_f32 v81, v82, v83\n"
"v_cvt_pk_bf16_f32 v82, v84, v85\n"
"v_cvt_pk_bf16_f32 v83, v86, v87\n"
"v_mfma_f32_16x16x32_bf16 v[48:51], v[140:143], a[248:251], v[48:51]\n"
"v_permlane16_swap_b32 v80, v82\n"
"v_permlane16_swap_b32 v81, v83\n"
"v_accvgpr_write_b32 a184, v80\n"
"v_accvgpr_write_b32 a185, v81\n"
"v_accvgpr_write_b32 a186, v82\n"
"v_accvgpr_write_b32 a187, v83\n"
"v_mfma_f32_16x16x32_bf16 v[80:83], v[144:147], a[244:247], 0\n"
"v_exp_f32 v152, v152\n"
"v_mfma_f32_16x16x32_bf16 v[80:83], v[148:151], a[248:251], v[80:83]\n"
"s_waitcnt lgkmcnt(2)\n"
"v_exp_f32 v153, v153\n"
"v_mfma_f32_16x16x32_bf16 v[84:87], v[148:151], v[224:227], 0\n"
"s_waitcnt lgkmcnt(0)\n"
"ds_read_b128 v[208:211], v228 offset:32768\n"
"v_accvgpr_write_b32 a102, v122\n"
"v_mfma_f32_16x16x32_bf16 v[84:87], v[144:147], a[252:255], v[84:87]\n"
"ds_read_b128 v[212:215], v229 offset:32768\n"
"v_accvgpr_write_b32 a103, v123\n"
"v_mfma_f32_16x16x32_bf16 v[52:55], v[140:143], v[224:227], 0\n"
"ds_read_b128 v[216:219], v228 offset:33792\n"
"v_bfe_u32 v254, v242, 6, 4\n"
"v_mfma_f32_16x16x32_bf16 v[52:55], v[136:139], a[252:255], v[52:55]\n"
"ds_read_b128 v[220:223], v229 offset:33792\n"
"v_xor_b32 v249, v249, v254\n"
"v_mfma_f32_16x16x32_bf16 v[20:23], v[132:135], v[224:227], 0\n"
"ds_read_b64_tr_b16 a[244:245], v234 offset:16384\n"
"ds_read_b64_tr_b16 a[246:247], v234 offset:20480\n"
"v_mfma_f32_16x16x32_bf16 v[20:23], v[128:131], a[252:255], v[20:23]\n"
"ds_read_b64_tr_b16 a[248:249], v238 offset:16384\n"
"ds_read_b64_tr_b16 a[250:251], v238 offset:20480\n"
"v_exp_f32 v154, v154\n"
"v_exp_f32 v155, v155\n"
"v_exp_f32 v156, v156\n"
"v_exp_f32 v157, v157\n"
"v_exp_f32 v158, v158\n"
"v_exp_f32 v159, v159\n"
"v_exp_f32 v160, v160\n"
"v_exp_f32 v161, v161\n"
"v_exp_f32 v162, v162\n"
"v_exp_f32 v163, v163\n"
"v_exp_f32 v164, v164\n"
"v_exp_f32 v165, v165\n"
"v_exp_f32 v166, v166\n"
"v_exp_f32 v167, v167\n"
"v_mfma_f32_16x16x32_bf16 a[228:231], v[124:127], a[252:255], 0\n"
"s_waitcnt lgkmcnt(7)\n"
"ds_read_b64_tr_b16 a[254:255], v235 offset:20480\n"
"ds_read_b64_tr_b16 v[224:225], v239 offset:16384\n"
"v_lshl_add_u32 v249, v249, 4, s44\n"
"v_cvt_pk_bf16_f32 v122, v152, v153\n"
"v_cvt_pk_bf16_f32 v123, v154, v155\n"
"v_cvt_pk_bf16_f32 v100, v156, v157\n"
"v_cvt_pk_bf16_f32 v101, v158, v159\n"
"v_cvt_pk_bf16_f32 v108, v160, v161\n"
"v_cvt_pk_bf16_f32 v109, v162, v163\n"
"v_cvt_pk_bf16_f32 v116, v164, v165\n"
"v_cvt_pk_bf16_f32 v117, v166, v167\n"
"v_exp_f32 v169, v169\n"
"v_exp_f32 v172, v172\n"
"v_exp_f32 v173, v173\n"
"v_exp_f32 v175, v175\n"
"s_branch LAY2_%=\n"
"LAL_%=:\n"
"v_mfma_f32_16x16x32_bf16 a[224:227], v[124:127], a[244:247], a[224:227]\n"
"v_exp_f32 v169, v169\n"
"v_mfma_f32_16x16x32_bf16 v[16:19], v[128:131], a[244:247], v[16:19]\n"
"s_waitcnt vmcnt(8)\n"
"v_exp_f32 v172, v172\n"
"v_mfma_f32_16x16x32_bf16 v[16:19], v[132:135], a[248:251], v[16:19]\n"
"s_barrier\n"
"ds_read_b128 v[216:219], v228 offset:1024\n"
"v_mfma_f32_16x16x32_bf16 v[48:51], v[136:139], a[244:247], v[48:51]\n"
"ds_read_b128 v[212:215], v229 offset:0\n"
"v_cvt_pk_bf16_f32 v108, v160, v161\n"
"v_mfma_f32_16x16x32_bf16 v[48:51], v[140:143], a[248:251], v[48:51]\n"
"ds_read_b128 v[208:211], v228 offset:0\n"
"v_cvt_pk_bf16_f32 v100, v156, v157\n"
"v_mfma_f32_16x16x32_bf16 v[80:83], v[144:147], a[244:247], v[80:83]\n"
"ds_read_b128 v[220:223], v229 offset:1024\n"
"v_cvt_pk_bf16_f32 v117, v166, v167\n"
"v_mfma_f32_16x16x32_bf16 v[80:83], v[148:151], a[248:251], v[80:83]\n"
"ds_read_b64_tr_b16 a[244:245], v234 offset:49152\n"
"v_cvt_pk_bf16_f32 v109, v162, v163\n"
"v_mfma_f32_16x16x32_bf16 v[84:87], v[148:151], v[224:227], v[84:87]\n"
"ds_read_b64_tr_b16 a[248:249], v238 offset:49152\n"
"v_mfma_f32_16x16x32_bf16 v[84:87], v[144:147], a[252:255], v[84:87]\n"
"ds_read_b64_tr_b16 a[246:247], v234 offset:53248\n"
"v_mfma_f32_16x16x32_bf16 v[52:55], v[140:143], v[224:227], v[52:55]\n"
"ds_read_b64_tr_b16 a[250:251], v238 offset:53248\n"
"v_mfma_f32_16x16x32_bf16 v[52:55], v[136:139], a[252:255], v[52:55]\n"
"v_exp_f32 v175, v175\n"
"v_mfma_f32_16x16x32_bf16 v[20:23], v[132:135], v[224:227], v[20:23]\n"
"ds_read_b64_tr_b16 v[224:225], v239 offset:49152\n"
"v_mfma_f32_16x16x32_bf16 v[20:23], v[128:131], a[252:255], v[20:23]\n"
"v_exp_f32 v173, v173\n"
"v_mfma_f32_16x16x32_bf16 a[228:231], v[124:127], a[252:255], a[228:231]\n"
"s_waitcnt lgkmcnt(5)\n"
"ds_read_b64_tr_b16 a[254:255], v235 offset:53248\n"
"v_cvt_pk_bf16_f32 v116, v164, v165\n"
"v_mfma_f32_16x16x32_bf16 v[152:155], v[208:211], a[0:3], v[180:183]\n"
"v_exp_f32 v174, v174\n"
"v_mfma_f32_16x16x32_bf16 v[152:155], v[212:215], a[4:7], v[152:155]\n"
"ds_read_b64_tr_b16 a[252:253], v235 offset:49152\n"
"v_cvt_pk_bf16_f32 v111, v174, v175\n"
"v_mfma_f32_16x16x32_bf16 v[152:155], v[216:219], a[8:11], v[152:155]\n"
"s_add_u32 m0, s44, 81920\n"
"ds_read_b64_tr_b16 v[226:227], v239 offset:53248\n"
"v_mfma_f32_16x16x32_bf16 v[152:155], v[220:223], a[12:15], v[152:155]\n"
"buffer_load_dwordx4 v240, s[20:23], s56 offen nt lds\n"
"v_mfma_f32_16x16x32_bf16 v[156:159], v[216:219], a[24:27], v[184:187]\n"
"v_exp_f32 v171, v171\n"
"v_mfma_f32_16x16x32_bf16 v[156:159], v[220:223], a[28:31], v[156:159]\n"
"v_exp_f32 v168, v168\n"
"v_mfma_f32_16x16x32_bf16 v[156:159], v[208:211], a[16:19], v[156:159]\n"
"v_exp_f32 v176, v176\n"
"v_mfma_f32_16x16x32_bf16 v[156:159], v[212:215], a[20:23], v[156:159]\n"
"v_exp_f32 v177, v177\n"
"v_mfma_f32_16x16x32_bf16 v[160:163], v[208:211], a[32:35], v[188:191]\n"
"v_exp_f32 v179, v179\n"
"v_mfma_f32_16x16x32_bf16 v[160:163], v[212:215], a[36:39], v[160:163]\n"
"v_exp_f32 v170, v170\n"
"v_mfma_f32_16x16x32_bf16 v[160:163], v[216:219], a[40:43], v[160:163]\n"
"v_exp_f32 v153, v153\n"
"v_mfma_f32_16x16x32_bf16 v[160:163], v[220:223], a[44:47], v[160:163]\n"
"v_cvt_pk_bf16_f32 v102, v168, v169\n"
"v_mfma_f32_16x16x32_bf16 v[164:167], v[216:219], a[200:203], v[192:195]\n"
"ds_read_b128 v[216:219], v228 offset:5120\n"
"v_cvt_pk_bf16_f32 v103, v170, v171\n"
"v_mfma_f32_16x16x32_bf16 v[164:167], v[220:223], a[204:207], v[164:167]\n"
"v_cvt_pk_bf16_f32 v110, v172, v173\n"
"v_cvt_pk_bf16_f32 v118, v176, v177\n"
"v_mfma_f32_16x16x32_bf16 v[164:167], v[208:211], a[192:195], v[164:167]\n"
"s_add_u32 s40, s56, s52\n"
"ds_read_b128 v[208:211], v228 offset:4096\n"
"v_mfma_f32_16x16x32_bf16 v[164:167], v[212:215], a[196:199], v[164:167]\n"
"s_waitcnt lgkmcnt(2)\n"
"buffer_load_dwordx4 v240, s[20:23], s40 offen offset:1024 nt lds\n"
"v_mfma_f32_16x16x32_bf16 a[216:219], v[120:123], a[244:247], a[216:219]\n"
"v_exp_f32 v155, v155\n"
"v_mfma_f32_16x16x32_bf16 v[8:11], v[96:99], a[244:247], v[8:11]\n"
"v_exp_f32 v161, v161\n"
"v_mfma_f32_16x16x32_bf16 v[8:11], v[100:103], a[248:251], v[8:11]\n"
"v_exp_f32 v163, v163\n"
"v_mfma_f32_16x16x32_bf16 v[40:43], v[104:107], a[244:247], v[40:43]\n"
"v_exp_f32 v178, v178\n"
"v_mfma_f32_16x16x32_bf16 v[40:43], v[108:111], a[248:251], v[40:43]\n"
"ds_read_b128 v[220:223], v229 offset:5120\n"
"v_cvt_pk_bf16_f32 v119, v178, v179\n"
"v_mfma_f32_16x16x32_bf16 v[72:75], v[112:115], a[244:247], v[72:75]\n"
"ds_read_b64_tr_b16 a[244:245], v234 offset:50176\n"
"v_mfma_f32_16x16x32_bf16 v[72:75], v[116:119], a[248:251], v[72:75]\n"
"v_exp_f32 v158, v158\n"
"v_mfma_f32_16x16x32_bf16 v[76:79], v[116:119], v[224:227], v[76:79]\n"
"ds_read_b128 v[212:215], v229 offset:4096\n"
"v_mfma_f32_16x16x32_bf16 v[76:79], v[112:115], a[252:255], v[76:79]\n"
"v_exp_f32 v160, v160\n"
"v_mfma_f32_16x16x32_bf16 v[44:47], v[108:111], v[224:227], v[44:47]\n"
"s_add_u32 s40, s56, s53\n"
"ds_read_b64_tr_b16 a[248:249], v238 offset:50176\n"
"v_mfma_f32_16x16x32_bf16 v[44:47], v[104:107], a[252:255], v[44:47]\n"
"buffer_load_dwordx4 v240, s[20:23], s40 offen offset:2048 nt lds\n"
"v_mfma_f32_16x16x32_bf16 v[12:15], v[100:103], v[224:227], v[12:15]\n"
"v_exp_f32 v165, v165\n"
"v_mfma_f32_16x16x32_bf16 v[12:15], v[96:99], a[252:255], v[12:15]\n"
"v_exp_f32 v159, v159\n"
"v_mfma_f32_16x16x32_bf16 a[220:223], v[120:123], a[252:255], a[220:223]\n"
"v_exp_f32 v152, v152\n"
"v_smfmac_f32_16x16x64_bf16 v[196:199], a[240:243], v[96:103], v241\n"
"v_mfma_f32_16x16x32_bf16 v[202:205], a[240:243], v[120:123], v[202:205]\n"
"s_waitcnt lgkmcnt(1)\n"
"ds_read_b64_tr_b16 v[224:225], v239 offset:50176\n"
"v_cvt_pk_bf16_f32 v128, v152, v153\n"
"v_mfma_f32_16x16x32_bf16 v[168:171], v[216:219], a[8:11], v[180:183]\n"
"v_exp_f32 v162, v162\n"
"v_mfma_f32_16x16x32_bf16 v[168:171], v[220:223], a[12:15], v[168:171]\n"
"ds_read_b64_tr_b16 a[250:251], v238 offset:54272\n"
"v_mfma_f32_16x16x32_bf16 v[168:171], v[208:211], a[0:3], v[168:171]\n"
"ds_read_b64_tr_b16 a[252:253], v235 offset:50176\n"
"v_mfma_f32_16x16x32_bf16 v[168:171], v[212:215], a[4:7], v[168:171]\n"
"v_exp_f32 v154, v154\n"
"v_mfma_f32_16x16x32_bf16 v[172:175], v[208:211], a[16:19], v[184:187]\n"
"ds_read_b64_tr_b16 v[226:227], v239 offset:54272\n"
"v_mfma_f32_16x16x32_bf16 v[172:175], v[212:215], a[20:23], v[172:175]\n"
"ds_read_b64_tr_b16 a[246:247], v234 offset:54272\n"
"v_cvt_pk_bf16_f32 v129, v154, v155\n"
"v_mfma_f32_16x16x32_bf16 v[172:175], v[216:219], a[24:27], v[172:175]\n"
"s_add_u32 s40, s56, s54\n"
"ds_read_b64_tr_b16 a[254:255], v235 offset:54272\n"
"v_mfma_f32_16x16x32_bf16 v[172:175], v[220:223], a[28:31], v[172:175]\n"
"buffer_load_dwordx4 v240, s[20:23], s40 offen offset:3072 nt lds\n"
"v_mfma_f32_16x16x32_bf16 v[176:179], v[216:219], a[40:43], v[188:191]\n"
"s_add_u32 s55, s55, s59\n"
"v_exp_f32 v157, v157\n"
"v_mfma_f32_16x16x32_bf16 v[176:179], v[220:223], a[44:47], v[176:179]\n"
"s_add_u32 s41, s55, s57\n"
"v_exp_f32 v170, v170\n"
"v_mfma_f32_16x16x32_bf16 v[176:179], v[208:211], a[32:35], v[176:179]\n"
"s_and_b32 s41, s41, s58\n"
"v_exp_f32 v156, v156\n"
"v_mfma_f32_16x16x32_bf16 v[176:179], v[212:215], a[36:39], v[176:179]\n"
"s_andn2_b32 s56, s55, s58\n"
"v_exp_f32 v166, v166\n"
"v_mfma_f32_16x16x32_bf16 v[152:155], v[208:211], a[192:195], v[192:195]\n"
"s_or_b32 s56, s56, s41\n"
"ds_read_b128 v[208:211], v230 offset:0\n"
"v_mfma_f32_16x16x32_bf16 v[152:155], v[212:215], a[196:199], v[152:155]\n"
"v_exp_f32 v174, v174\n"
"v_mfma_f32_16x16x32_bf16 v[152:155], v[216:219], a[200:203], v[152:155]\n"
"ds_read_b128 v[212:215], v231 offset:0\n"
"v_mfma_f32_16x16x32_bf16 v[152:155], v[220:223], a[204:207], v[152:155]\n"
"s_waitcnt lgkmcnt(2)\n"
"ds_read_b128 v[216:219], v230 offset:1024\n"
"v_cvt_pk_bf16_f32 v137, v158, v159\n"
"v_mfma_f32_16x16x32_bf16 a[232:235], v[120:123], a[244:247], a[232:235]\n"
"v_exp_f32 v164, v164\n"
"v_mfma_f32_16x16x32_bf16 v[24:27], v[96:99], a[244:247], v[24:27]\n"
"s_add_u32 m0, s44, 32768\n"
"ds_read_b128 v[220:223], v231 offset:1024\n"
"v_mfma_f32_16x16x32_bf16 v[24:27], v[100:103], a[248:251], v[24:27]\n"
"buffer_load_dwordx4 v240, s[16:19], s56 offen nt lds\n"
"v_mfma_f32_16x16x32_bf16 v[56:59], v[104:107], a[244:247], v[56:59]\n"
"v_exp_f32 v167, v167\n"
"v_mfma_f32_16x16x32_bf16 v[56:59], v[108:111], a[248:251], v[56:59]\n"
"v_exp_f32 v176, v176\n"
"v_mfma_f32_16x16x32_bf16 v[88:91], v[112:115], a[244:247], v[88:91]\n"
"v_exp_f32 v153, v153\n"
"v_mfma_f32_16x16x32_bf16 v[88:91], v[116:119], a[248:251], v[88:91]\n"
"v_exp_f32 v173, v173\n"
"v_mfma_f32_16x16x32_bf16 v[92:95], v[116:119], v[224:227], v[92:95]\n"
"ds_read_b64_tr_b16 a[250:251], v236 offset:53248\n"
"v_cvt_pk_bf16_f32 v125, v166, v167\n"
"v_mfma_f32_16x16x32_bf16 v[92:95], v[112:115], a[252:255], v[92:95]\n"
"ds_read_b64_tr_b16 a[244:245], v232 offset:49152\n"
"v_cvt_pk_bf16_f32 v144, v160, v161\n"
"v_mfma_f32_16x16x32_bf16 v[60:63], v[108:111], v[224:227], v[60:63]\n"
"ds_read_b64_tr_b16 a[248:249], v236 offset:49152\n"
"v_cvt_pk_bf16_f32 v124, v164, v165\n"
"v_mfma_f32_16x16x32_bf16 v[60:63], v[104:107], a[252:255], v[60:63]\n"
"ds_read_b64_tr_b16 a[246:247], v232 offset:53248\n"
"v_cvt_pk_bf16_f32 v136, v156, v157\n"
"v_mfma_f32_16x16x32_bf16 v[28:31], v[100:103], v[224:227], v[28:31]\n"
"ds_read_b64_tr_b16 v[226:227], v237 offset:53248\n"
"v_cvt_pk_bf16_f32 v145, v162, v163\n"
"v_mfma_f32_16x16x32_bf16 v[28:31], v[96:99], a[252:255], v[28:31]\n"
"ds_read_b64_tr_b16 v[224:225], v237 offset:49152\n"
"v_mfma_f32_16x16x32_bf16 a[236:239], v[120:123], a[252:255], a[236:239]\n"
"s_add_u32 s40, s56, s52\n"
"ds_read_b64_tr_b16 a[252:253], v233 offset:49152\n"
"v_smfmac_f32_16x16x64_bf16 v[198:201], a[240:243], v[104:111], v241\n"
"s_waitcnt lgkmcnt(7)\n"
"buffer_load_dwordx4 v240, s[16:19], s40 offen offset:1024 nt lds\n"
"v_mfma_f32_16x16x32_bf16 v[156:159], v[208:211], a[0:3], v[180:183]\n"
"v_exp_f32 v169, v169\n"
"v_mfma_f32_16x16x32_bf16 v[156:159], v[212:215], a[4:7], v[156:159]\n"
"v_exp_f32 v168, v168\n"
"v_mfma_f32_16x16x32_bf16 v[156:159], v[216:219], a[8:11], v[156:159]\n"
"v_exp_f32 v175, v175\n"
"v_mfma_f32_16x16x32_bf16 v[156:159], v[220:223], a[12:15], v[156:159]\n"
"v_exp_f32 v172, v172\n"
"v_mfma_f32_16x16x32_bf16 v[160:163], v[216:219], a[24:27], v[184:187]\n"
"ds_read_b64_tr_b16 a[254:255], v233 offset:53248\n"
"v_mfma_f32_16x16x32_bf16 v[160:163], v[220:223], a[28:31], v[160:163]\n"
"v_exp_f32 v155, v155\n"
"v_mfma_f32_16x16x32_bf16 v[160:163], v[208:211], a[16:19], v[160:163]\n"
"v_exp_f32 v177, v177\n"
"v_mfma_f32_16x16x32_bf16 v[160:163], v[212:215], a[20:23], v[160:163]\n"
"v_exp_f32 v179, v179\n"
"v_mfma_f32_16x16x32_bf16 v[164:167], v[208:211], a[32:35], v[188:191]\n"
"ds_read_b128 v[208:211], v230 offset:4096\n"
"v_cvt_pk_bf16_f32 v139, v174, v175\n"
"v_mfma_f32_16x16x32_bf16 v[164:167], v[212:215], a[36:39], v[164:167]\n"
"v_exp_f32 v171, v171\n"
"v_mfma_f32_16x16x32_bf16 v[164:167], v[216:219], a[40:43], v[164:167]\n"
"ds_read_b128 v[212:215], v231 offset:4096\n"
"v_cvt_pk_bf16_f32 v138, v172, v173\n"
"v_mfma_f32_16x16x32_bf16 v[164:167], v[220:223], a[44:47], v[164:167]\n"
"s_waitcnt lgkmcnt(3)\n"
"ds_read_b128 v[220:223], v231 offset:5120\n"
"v_cvt_pk_bf16_f32 v130, v168, v169\n"
"v_mfma_f32_16x16x32_bf16 a[208:211], v[120:123], a[244:247], a[208:211]\n"
"v_exp_f32 v154, v154\n"
"v_mfma_f32_16x16x32_bf16 v[0:3], v[96:99], a[244:247], v[0:3]\n"
"s_add_u32 s40, s56, s53\n"
"ds_read_b128 v[216:219], v230 offset:5120\n"
"v_mfma_f32_16x16x32_bf16 v[0:3], v[100:103], a[248:251], v[0:3]\n"
"buffer_load_dwordx4 v240, s[16:19], s40 offen offset:2048 nt lds\n"
"v_mfma_f32_16x16x32_bf16 v[32:35], v[104:107], a[244:247], v[32:35]\n"
"v_exp_f32 v160, v160\n"
"v_mfma_f32_16x16x32_bf16 v[32:35], v[108:111], a[248:251], v[32:35]\n"
"v_exp_f32 v178, v178\n"
"v_mfma_f32_16x16x32_bf16 v[64:67], v[112:115], a[244:247], v[64:67]\n"
"v_exp_f32 v152, v152\n"
"v_mfma_f32_16x16x32_bf16 v[64:67], v[116:119], a[248:251], v[64:67]\n"
"v_exp_f32 v161, v161\n"
"v_mfma_f32_16x16x32_bf16 v[68:71], v[116:119], v[224:227], v[68:71]\n"
"s_waitcnt lgkmcnt(1)\n"
"ds_read_b64_tr_b16 a[244:245], v232 offset:50176\n"
"v_cvt_pk_bf16_f32 v147, v178, v179\n"
"v_mfma_f32_16x16x32_bf16 v[68:71], v[112:115], a[252:255], v[68:71]\n"
"ds_read_b64_tr_b16 a[248:249], v236 offset:50176\n"
"v_cvt_pk_bf16_f32 v146, v176, v177\n"
"v_mfma_f32_16x16x32_bf16 v[36:39], v[108:111], v[224:227], v[36:39]\n"
"ds_read_b64_tr_b16 a[250:251], v236 offset:54272\n"
"v_cvt_pk_bf16_f32 v131, v170, v171\n"
"v_mfma_f32_16x16x32_bf16 v[36:39], v[104:107], a[252:255], v[36:39]\n"
"v_exp_f32 v163, v163\n"
"v_mfma_f32_16x16x32_bf16 v[4:7], v[100:103], v[224:227], v[4:7]\n"
"s_add_u32 s40, s56, s54\n"
"ds_read_b64_tr_b16 a[246:247], v232 offset:54272\n"
"v_mfma_f32_16x16x32_bf16 v[4:7], v[96:99], a[252:255], v[4:7]\n"
"buffer_load_dwordx4 v240, s[16:19], s40 offen offset:3072 nt lds\n"
"v_mfma_f32_16x16x32_bf16 a[212:215], v[120:123], a[252:255], a[212:215]\n"
"v_exp_f32 v167, v167\n"
"v_smfmac_f32_16x16x64_bf16 v[200:203], a[240:243], v[112:119], v241\n"
"v_cvt_pk_bf16_f32 v126, v152, v153\n"
"v_mfma_f32_16x16x32_bf16 v[168:171], v[208:211], a[0:3], v[180:183]\n"
"v_exp_f32 v159, v159\n"
"v_mfma_f32_16x16x32_bf16 v[168:171], v[212:215], a[4:7], v[168:171]\n"
"s_waitcnt lgkmcnt(2)\n"
"v_exp_f32 v165, v165\n"
"v_mfma_f32_16x16x32_bf16 v[168:171], v[216:219], a[8:11], v[168:171]\n"
"ds_read_b64_tr_b16 a[252:253], v233 offset:50176\n"
"ds_read_b64_tr_b16 v[224:225], v237 offset:50176\n"
"v_mfma_f32_16x16x32_bf16 v[168:171], v[220:223], a[12:15], v[168:171]\n"
"ds_read_b64_tr_b16 a[254:255], v233 offset:54272\n"
"ds_read_b64_tr_b16 v[226:227], v237 offset:54272\n"
"v_mfma_f32_16x16x32_bf16 v[172:175], v[216:219], a[24:27], v[184:187]\n"
"v_exp_f32 v166, v166\n"
"v_mfma_f32_16x16x32_bf16 v[172:175], v[220:223], a[28:31], v[172:175]\n"
"v_exp_f32 v164, v164\n"
"v_mfma_f32_16x16x32_bf16 v[172:175], v[208:211], a[16:19], v[172:175]\n"
"v_exp_f32 v157, v157\n"
"v_mfma_f32_16x16x32_bf16 v[172:175], v[212:215], a[20:23], v[172:175]\n"
"s_cbranch_vccnz LPCL0_%=\nLPKL0_%=:\n"
"v_cvt_pk_bf16_f32 v127, v154, v155\n"
"v_mfma_f32_16x16x32_bf16 v[176:179], v[208:211], a[32:35], v[188:191]\n"
"v_exp_f32 v158, v158\n"
"v_mfma_f32_16x16x32_bf16 v[176:179], v[212:215], a[36:39], v[176:179]\n"
"v_exp_f32 v162, v162\n"
"v_mfma_f32_16x16x32_bf16 v[176:179], v[216:219], a[40:43], v[176:179]\n"
"v_exp_f32 v156, v156\n"
"v_mfma_f32_16x16x32_bf16 v[176:179], v[220:223], a[44:47], v[176:179]\n"
"s_waitcnt lgkmcnt(0)\n"
"v_cvt_pk_bf16_f32 v133, v158, v159\n"
"v_mfma_f32_16x16x32_bf16 a[224:227], v[120:123], a[244:247], a[224:227]\n"
"v_exp_f32 v169, v169\n"
"v_mfma_f32_16x16x32_bf16 v[16:19], v[96:99], a[244:247], v[16:19]\n"
"s_waitcnt vmcnt(8)\n"
"v_exp_f32 v172, v172\n"
"v_mfma_f32_16x16x32_bf16 v[16:19], v[100:103], a[248:251], v[16:19]\n"
"s_barrier\n"
"ds_read_b128 v[216:219], v228 offset:17408\n"
"v_mfma_f32_16x16x32_bf16 v[48:51], v[104:107], a[244:247], v[48:51]\n"
"ds_read_b128 v[212:215], v229 offset:16384\n"
"v_cvt_pk_bf16_f32 v140, v160, v161\n"
"v_mfma_f32_16x16x32_bf16 v[48:51], v[108:111], a[248:251], v[48:51]\n"
"ds_read_b128 v[208:211], v228 offset:16384\n"
"v_cvt_pk_bf16_f32 v132, v156, v157\n"
"v_mfma_f32_16x16x32_bf16 v[80:83], v[112:115], a[244:247], v[80:83]\n"
"ds_read_b128 v[220:223], v229 offset:17408\n"
"v_cvt_pk_bf16_f32 v149, v166, v167\n"
"v_mfma_f32_16x16x32_bf16 v[80:83], v[116:119], a[248:251], v[80:83]\n"
"ds_read_b64_tr_b16 a[244:245], v234 offset:0\n"
"v_cvt_pk_bf16_f32 v141, v162, v163\n"
"v_mfma_f32_16x16x32_bf16 v[84:87], v[116:119], v[224:227], v[84:87]\n"
"ds_read_b64_tr_b16 a[248:249], v238 offset:0\n"
"v_mfma_f32_16x16x32_bf16 v[84:87], v[112:115], a[252:255], v[84:87]\n"
"ds_read_b64_tr_b16 a[246:247], v234 offset:4096\n"
"v_mfma_f32_16x16x32_bf16 v[52:55], v[108:111], v[224:227], v[52:55]\n"
"ds_read_b64_tr_b16 a[250:251], v238 offset:4096\n"
"v_mfma_f32_16x16x32_bf16 v[52:55], v[104:107], a[252:255], v[52:55]\n"
"v_exp_f32 v175, v175\n"
"v_mfma_f32_16x16x32_bf16 v[20:23], v[100:103], v[224:227], v[20:23]\n"
"ds_read_b64_tr_b16 v[224:225], v239 offset:0\n"
"v_mfma_f32_16x16x32_bf16 v[20:23], v[96:99], a[252:255], v[20:23]\n"
"v_exp_f32 v173, v173\n"
"v_mfma_f32_16x16x32_bf16 a[228:231], v[120:123], a[252:255], a[228:231]\n"
"s_waitcnt lgkmcnt(5)\n"
"ds_read_b64_tr_b16 a[254:255], v235 offset:4096\n"
"v_cvt_pk_bf16_f32 v148, v164, v165\n"
"v_mfma_f32_16x16x32_bf16 v[152:155], v[208:211], a[0:3], v[180:183]\n"
"v_exp_f32 v174, v174\n"
"v_mfma_f32_16x16x32_bf16 v[152:155], v[212:215], a[4:7], v[152:155]\n"
"ds_read_b64_tr_b16 a[252:253], v235 offset:0\n"
"v_cvt_pk_bf16_f32 v143, v174, v175\n"
"v_mfma_f32_16x16x32_bf16 v[152:155], v[216:219], a[8:11], v[152:155]\n"
"s_add_u32 m0, s44, 98304\n"
"ds_read_b64_tr_b16 v[226:227], v239 offset:4096\n"
"v_mfma_f32_16x16x32_bf16 v[152:155], v[220:223], a[12:15], v[152:155]\n"
"buffer_load_dwordx4 v240, s[20:23], s56 offen nt lds\n"
"v_mfma_f32_16x16x32_bf16 v[156:159], v[216:219], a[24:27], v[184:187]\n"
"v_exp_f32 v171, v171\n"
"v_mfma_f32_16x16x32_bf16 v[156:159], v[220:223], a[28:31], v[156:159]\n"
"v_exp_f32 v168, v168\n"
"v_mfma_f32_16x16x32_bf16 v[156:159], v[208:211], a[16:19], v[156:159]\n"
"v_exp_f32 v176, v176\n"
"v_mfma_f32_16x16x32_bf16 v[156:159], v[212:215], a[20:23], v[156:159]\n"
"v_exp_f32 v177, v177\n"
"v_mfma_f32_16x16x32_bf16 v[160:163], v[208:211], a[32:35], v[188:191]\n"
"v_exp_f32 v179, v179\n"
"v_mfma_f32_16x16x32_bf16 v[160:163], v[212:215], a[36:39], v[160:163]\n"
"v_exp_f32 v170, v170\n"
"v_mfma_f32_16x16x32_bf16 v[160:163], v[216:219], a[40:43], v[160:163]\n"
"v_exp_f32 v153, v153\n"
"v_mfma_f32_16x16x32_bf16 v[160:163], v[220:223], a[44:47], v[160:163]\n"
"v_cvt_pk_bf16_f32 v134, v168, v169\n"
"v_mfma_f32_16x16x32_bf16 v[164:167], v[216:219], a[200:203], v[192:195]\n"
"ds_read_b128 v[216:219], v228 offset:21504\n"
"v_cvt_pk_bf16_f32 v135, v170, v171\n"
"v_mfma_f32_16x16x32_bf16 v[164:167], v[220:223], a[204:207], v[164:167]\n"
"v_cvt_pk_bf16_f32 v142, v172, v173\n"
"v_cvt_pk_bf16_f32 v150, v176, v177\n"
"v_mfma_f32_16x16x32_bf16 v[164:167], v[208:211], a[192:195], v[164:167]\n"
"s_add_u32 s40, s56, s52\n"
"ds_read_b128 v[208:211], v228 offset:20480\n"
"v_mfma_f32_16x16x32_bf16 v[164:167], v[212:215], a[196:199], v[164:167]\n"
"s_waitcnt lgkmcnt(2)\n"
"buffer_load_dwordx4 v240, s[20:23], s40 offen offset:1024 nt lds\n"
"v_mfma_f32_16x16x32_bf16 a[216:219], v[124:127], a[244:247], a[216:219]\n"
"v_exp_f32 v155, v155\n"
"v_mfma_f32_16x16x32_bf16 v[8:11], v[128:131], a[244:247], v[8:11]\n"
"v_exp_f32 v161, v161\n"
"v_mfma_f32_16x16x32_bf16 v[8:11], v[132:135], a[248:251], v[8:11]\n"
"v_exp_f32 v163, v163\n"
"v_mfma_f32_16x16x32_bf16 v[40:43], v[136:139], a[244:247], v[40:43]\n"
"v_exp_f32 v178, v178\n"
"v_mfma_f32_16x16x32_bf16 v[40:43], v[140:143], a[248:251], v[40:43]\n"
"ds_read_b128 v[220:223], v229 offset:21504\n"
"v_cvt_pk_bf16_f32 v151, v178, v179\n"
"v_mfma_f32_16x16x32_bf16 v[72:75], v[144:147], a[244:247], v[72:75]\n"
"ds_read_b64_tr_b16 a[244:245], v234 offset:1024\n"
"v_mfma_f32_16x16x32_bf16 v[72:75], v[148:151], a[248:251], v[72:75]\n"
"v_exp_f32 v158, v158\n"
"v_mfma_f32_16x16x32_bf16 v[76:79], v[148:151], v[224:227], v[76:79]\n"
"ds_read_b128 v[212:215], v229 offset:20480\n"
"v_mfma_f32_16x16x32_bf16 v[76:79], v[144:147], a[252:255], v[76:79]\n"
"v_exp_f32 v160, v160\n"
"v_mfma_f32_16x16x32_bf16 v[44:47], v[140:143], v[224:227], v[44:47]\n"
"s_add_u32 s40, s56, s53\n"
"ds_read_b64_tr_b16 a[248:249], v238 offset:1024\n"
"v_mfma_f32_16x16x32_bf16 v[44:47], v[136:139], a[252:255], v[44:47]\n"
"buffer_load_dwordx4 v240, s[20:23], s40 offen offset:2048 nt lds\n"
"v_mfma_f32_16x16x32_bf16 v[12:15], v[132:135], v[224:227], v[12:15]\n"
"v_exp_f32 v165, v165\n"
"v_mfma_f32_16x16x32_bf16 v[12:15], v[128:131], a[252:255], v[12:15]\n"
"v_exp_f32 v159, v159\n"
"v_mfma_f32_16x16x32_bf16 a[220:223], v[124:127], a[252:255], a[220:223]\n"
"v_exp_f32 v152, v152\n"
"v_smfmac_f32_16x16x64_bf16 v[196:199], a[240:243], v[128:135], v241\n"
"v_mfma_f32_16x16x32_bf16 v[202:205], a[240:243], v[124:127], v[202:205]\n"
"s_waitcnt lgkmcnt(1)\n"
"ds_read_b64_tr_b16 v[224:225], v239 offset:1024\n"
"v_cvt_pk_bf16_f32 v96, v152, v153\n"
"v_mfma_f32_16x16x32_bf16 v[168:171], v[216:219], a[8:11], v[180:183]\n"
"v_exp_f32 v162, v162\n"
"v_mfma_f32_16x16x32_bf16 v[168:171], v[220:223], a[12:15], v[168:171]\n"
"ds_read_b64_tr_b16 a[250:251], v238 offset:5120\n"
"v_mfma_f32_16x16x32_bf16 v[168:171], v[208:211], a[0:3], v[168:171]\n"
"ds_read_b64_tr_b16 a[252:253], v235 offset:1024\n"
"v_mfma_f32_16x16x32_bf16 v[168:171], v[212:215], a[4:7], v[168:171]\n"
"v_exp_f32 v154, v154\n"
"v_mfma_f32_16x16x32_bf16 v[172:175], v[208:211], a[16:19], v[184:187]\n"
"ds_read_b64_tr_b16 v[226:227], v239 offset:5120\n"
"v_mfma_f32_16x16x32_bf16 v[172:175], v[212:215], a[20:23], v[172:175]\n"
"ds_read_b64_tr_b16 a[246:247], v234 offset:5120\n"
"v_cvt_pk_bf16_f32 v97, v154, v155\n"
"v_mfma_f32_16x16x32_bf16 v[172:175], v[216:219], a[24:27], v[172:175]\n"
"s_add_u32 s40, s56, s54\n"
"ds_read_b64_tr_b16 a[254:255], v235 offset:5120\n"
"v_mfma_f32_16x16x32_bf16 v[172:175], v[220:223], a[28:31], v[172:175]\n"
"buffer_load_dwordx4 v240, s[20:23], s40 offen offset:3072 nt lds\n"
"v_mfma_f32_16x16x32_bf16 v[176:179], v[216:219], a[40:43], v[188:191]\n"
"s_add_u32 s55, s55, s59\n"
"v_exp_f32 v157, v157\n"
"v_mfma_f32_16x16x32_bf16 v[176:179], v[220:223], a[44:47], v[176:179]\n"
"s_add_u32 s41, s55, s57\n"
"v_exp_f32 v170, v170\n"
"v_mfma_f32_16x16x32_bf16 v[176:179], v[208:211], a[32:35], v[176:179]\n"
"s_and_b32 s41, s41, s58\n"
"v_exp_f32 v156, v156\n"
"v_mfma_f32_16x16x32_bf16 v[176:179], v[212:215], a[36:39], v[176:179]\n"
"s_andn2_b32 s56, s55, s58\n"
"v_exp_f32 v166, v166\n"
"v_mfma_f32_16x16x32_bf16 v[152:155], v[208:211], a[192:195], v[192:195]\n"
"s_or_b32 s56, s56, s41\n"
"ds_read_b128 v[208:211], v230 offset:16384\n"
"v_mfma_f32_16x16x32_bf16 v[152:155], v[212:215], a[196:199], v[152:155]\n"
"v_exp_f32 v174, v174\n"
"v_mfma_f32_16x16x32_bf16 v[152:155], v[216:219], a[200:203], v[152:155]\n"
"ds_read_b128 v[212:215], v231 offset:16384\n"
"v_mfma_f32_16x16x32_bf16 v[152:155], v[220:223], a[204:207], v[152:155]\n"
"s_waitcnt lgkmcnt(2)\n"
"ds_read_b128 v[216:219], v230 offset:17408\n"
"v_cvt_pk_bf16_f32 v105, v158, v159\n"
"v_mfma_f32_16x16x32_bf16 a[232:235], v[124:127], a[244:247], a[232:235]\n"
"v_exp_f32 v164, v164\n"
"v_mfma_f32_16x16x32_bf16 v[24:27], v[128:131], a[244:247], v[24:27]\n"
"s_add_u32 m0, s44, 49152\n"
"ds_read_b128 v[220:223], v231 offset:17408\n"
"v_mfma_f32_16x16x32_bf16 v[24:27], v[132:135], a[248:251], v[24:27]\n"
"buffer_load_dwordx4 v240, s[16:19], s56 offen nt lds\n"
"v_mfma_f32_16x16x32_bf16 v[56:59], v[136:139], a[244:247], v[56:59]\n"
"v_exp_f32 v167, v167\n"
"v_mfma_f32_16x16x32_bf16 v[56:59], v[140:143], a[248:251], v[56:59]\n"
"v_exp_f32 v176, v176\n"
"v_mfma_f32_16x16x32_bf16 v[88:91], v[144:147], a[244:247], v[88:91]\n"
"v_exp_f32 v153, v153\n"
"v_mfma_f32_16x16x32_bf16 v[88:91], v[148:151], a[248:251], v[88:91]\n"
"v_exp_f32 v173, v173\n"
"v_mfma_f32_16x16x32_bf16 v[92:95], v[148:151], v[224:227], v[92:95]\n"
"ds_read_b64_tr_b16 a[250:251], v236 offset:4096\n"
"v_cvt_pk_bf16_f32 v121, v166, v167\n"
"v_mfma_f32_16x16x32_bf16 v[92:95], v[144:147], a[252:255], v[92:95]\n"
"ds_read_b64_tr_b16 a[244:245], v232 offset:0\n"
"v_cvt_pk_bf16_f32 v112, v160, v161\n"
"v_mfma_f32_16x16x32_bf16 v[60:63], v[140:143], v[224:227], v[60:63]\n"
"ds_read_b64_tr_b16 a[248:249], v236 offset:0\n"
"v_cvt_pk_bf16_f32 v120, v164, v165\n"
"v_mfma_f32_16x16x32_bf16 v[60:63], v[136:139], a[252:255], v[60:63]\n"
"ds_read_b64_tr_b16 a[246:247], v232 offset:4096\n"
"v_cvt_pk_bf16_f32 v104, v156, v157\n"
"v_mfma_f32_16x16x32_bf16 v[28:31], v[132:135], v[224:227], v[28:31]\n"
"ds_read_b64_tr_b16 v[226:227], v237 offset:4096\n"
"v_cvt_pk_bf16_f32 v113, v162, v163\n"
"v_mfma_f32_16x16x32_bf16 v[28:31], v[128:131], a[252:255], v[28:31]\n"
"ds_read_b64_tr_b16 v[224:225], v237 offset:0\n"
"v_mfma_f32_16x16x32_bf16 a[236:239], v[124:127], a[252:255], a[236:239]\n"
"s_add_u32 s40, s56, s52\n"
"ds_read_b64_tr_b16 a[252:253], v233 offset:0\n"
"v_smfmac_f32_16x16x64_bf16 v[198:201], a[240:243], v[136:143], v241\n"
"s_waitcnt lgkmcnt(7)\n"
"buffer_load_dwordx4 v240, s[16:19], s40 offen offset:1024 nt lds\n"
"v_mfma_f32_16x16x32_bf16 v[156:159], v[208:211], a[0:3], v[180:183]\n"
"v_exp_f32 v169, v169\n"
"v_mfma_f32_16x16x32_bf16 v[156:159], v[212:215], a[4:7], v[156:159]\n"
"v_exp_f32 v168, v168\n"
"v_mfma_f32_16x16x32_bf16 v[156:159], v[216:219], a[8:11], v[156:159]\n"
"v_exp_f32 v175, v175\n"
"v_mfma_f32_16x16x32_bf16 v[156:159], v[220:223], a[12:15], v[156:159]\n"
"v_exp_f32 v172, v172\n"
"v_mfma_f32_16x16x32_bf16 v[160:163], v[216:219], a[24:27], v[184:187]\n"
"ds_read_b64_tr_b16 a[254:255], v233 offset:4096\n"
"v_mfma_f32_16x16x32_bf16 v[160:163], v[220:223], a[28:31], v[160:163]\n"
"v_exp_f32 v155, v155\n"
"v_mfma_f32_16x16x32_bf16 v[160:163], v[208:211], a[16:19], v[160:163]\n"
"v_exp_f32 v177, v177\n"
"v_mfma_f32_16x16x32_bf16 v[160:163], v[212:215], a[20:23], v[160:163]\n"
"v_exp_f32 v179, v179\n"
"v_mfma_f32_16x16x32_bf16 v[164:167], v[208:211], a[32:35], v[188:191]\n"
"ds_read_b128 v[208:211], v230 offset:20480\n"
"v_cvt_pk_bf16_f32 v107, v174, v175\n"
"v_mfma_f32_16x16x32_bf16 v[164:167], v[212:215], a[36:39], v[164:167]\n"
"v_exp_f32 v171, v171\n"
"v_mfma_f32_16x16x32_bf16 v[164:167], v[216:219], a[40:43], v[164:167]\n"
"ds_read_b128 v[212:215], v231 offset:20480\n"
"v_cvt_pk_bf16_f32 v106, v172, v173\n"
"v_mfma_f32_16x16x32_bf16 v[164:167], v[220:223], a[44:47], v[164:167]\n"
"s_waitcnt lgkmcnt(3)\n"
"ds_read_b128 v[220:223], v231 offset:21504\n"
"v_cvt_pk_bf16_f32 v98, v168, v169\n"
"v_mfma_f32_16x16x32_bf16 a[208:211], v[124:127], a[244:247], a[208:211]\n"
"v_exp_f32 v154, v154\n"
"v_mfma_f32_16x16x32_bf16 v[0:3], v[128:131], a[244:247], v[0:3]\n"
"s_add_u32 s40, s56, s53\n"
"ds_read_b128 v[216:219], v230 offset:21504\n"
"v_mfma_f32_16x16x32_bf16 v[0:3], v[132:135], a[248:251], v[0:3]\n"
"buffer_load_dwordx4 v240, s[16:19], s40 offen offset:2048 nt lds\n"
"v_mfma_f32_16x16x32_bf16 v[32:35], v[136:139], a[244:247], v[32:35]\n"
"v_exp_f32 v160, v160\n"
"v_mfma_f32_16x16x32_bf16 v[32:35], v[140:143], a[248:251], v[32:35]\n"
"v_exp_f32 v178, v178\n"
"v_mfma_f32_16x16x32_bf16 v[64:67], v[144:147], a[244:247], v[64:67]\n"
"v_exp_f32 v152, v152\n"
"v_mfma_f32_16x16x32_bf16 v[64:67], v[148:151], a[248:251], v[64:67]\n"
"v_exp_f32 v161, v161\n"
"v_mfma_f32_16x16x32_bf16 v[68:71], v[148:151], v[224:227], v[68:71]\n"
"s_waitcnt lgkmcnt(1)\n"
"ds_read_b64_tr_b16 a[244:245], v232 offset:1024\n"
"v_cvt_pk_bf16_f32 v115, v178, v179\n"
"v_mfma_f32_16x16x32_bf16 v[68:71], v[144:147], a[252:255], v[68:71]\n"
"ds_read_b64_tr_b16 a[248:249], v236 offset:1024\n"
"v_cvt_pk_bf16_f32 v114, v176, v177\n"
"v_mfma_f32_16x16x32_bf16 v[36:39], v[140:143], v[224:227], v[36:39]\n"
"ds_read_b64_tr_b16 a[250:251], v236 offset:5120\n"
"v_cvt_pk_bf16_f32 v99, v170, v171\n"
"v_mfma_f32_16x16x32_bf16 v[36:39], v[136:139], a[252:255], v[36:39]\n"
"v_exp_f32 v163, v163\n"
"v_mfma_f32_16x16x32_bf16 v[4:7], v[132:135], v[224:227], v[4:7]\n"
"s_add_u32 s40, s56, s54\n"
"ds_read_b64_tr_b16 a[246:247], v232 offset:5120\n"
"v_mfma_f32_16x16x32_bf16 v[4:7], v[128:131], a[252:255], v[4:7]\n"
"buffer_load_dwordx4 v240, s[16:19], s40 offen offset:3072 nt lds\n"
"v_mfma_f32_16x16x32_bf16 a[212:215], v[124:127], a[252:255], a[212:215]\n"
"v_exp_f32 v167, v167\n"
"v_smfmac_f32_16x16x64_bf16 v[200:203], a[240:243], v[144:151], v241\n"
"v_cvt_pk_bf16_f32 v122, v152, v153\n"
"v_mfma_f32_16x16x32_bf16 v[168:171], v[208:211], a[0:3], v[180:183]\n"
"v_exp_f32 v159, v159\n"
"v_mfma_f32_16x16x32_bf16 v[168:171], v[212:215], a[4:7], v[168:171]\n"
"s_waitcnt lgkmcnt(2)\n"
"v_exp_f32 v165, v165\n"
"v_mfma_f32_16x16x32_bf16 v[168:171], v[216:219], a[8:11], v[168:171]\n"
"ds_read_b64_tr_b16 a[252:253], v233 offset:1024\n"
"ds_read_b64_tr_b16 v[224:225], v237 offset:1024\n"
"v_mfma_f32_16x16x32_bf16 v[168:171], v[220:223], a[12:15], v[168:171]\n"
"ds_read_b64_tr_b16 a[254:255], v233 offset:5120\n"
"ds_read_b64_tr_b16 v[226:227], v237 offset:5120\n"
"v_mfma_f32_16x16x32_bf16 v[172:175], v[216:219], a[24:27], v[184:187]\n"
"v_exp_f32 v166, v166\n"
"v_mfma_f32_16x16x32_bf16 v[172:175], v[220:223], a[28:31], v[172:175]\n"
"v_exp_f32 v164, v164\n"
"v_mfma_f32_16x16x32_bf16 v[172:175], v[208:211], a[16:19], v[172:175]\n"
"v_exp_f32 v157, v157\n"
"v_mfma_f32_16x16x32_bf16 v[172:175], v[212:215], a[20:23], v[172:175]\n"
"s_cbranch_vccnz LPCL1_%=\nLPKL1_%=:\n"
"v_cvt_pk_bf16_f32 v123, v154, v155\n"
"v_mfma_f32_16x16x32_bf16 v[176:179], v[208:211], a[32:35], v[188:191]\n"
"v_exp_f32 v158, v158\n"
"v_mfma_f32_16x16x32_bf16 v[176:179], v[212:215], a[36:39], v[176:179]\n"
"v_exp_f32 v162, v162\n"
"v_mfma_f32_16x16x32_bf16 v[176:179], v[216:219], a[40:43], v[176:179]\n"
"v_exp_f32 v156, v156\n"
"v_mfma_f32_16x16x32_bf16 v[176:179], v[220:223], a[44:47], v[176:179]\n"
"s_waitcnt lgkmcnt(0)\n"
"v_cvt_pk_bf16_f32 v101, v158, v159\n"
"v_mfma_f32_16x16x32_bf16 a[224:227], v[124:127], a[244:247], a[224:227]\n"
"v_exp_f32 v169, v169\n"
"v_mfma_f32_16x16x32_bf16 v[16:19], v[128:131], a[244:247], v[16:19]\n"
"s_waitcnt vmcnt(8)\n"
"v_exp_f32 v172, v172\n"
"v_mfma_f32_16x16x32_bf16 v[16:19], v[132:135], a[248:251], v[16:19]\n"
"s_barrier\n"
"ds_read_b128 v[216:219], v228 offset:33792\n"
"v_mfma_f32_16x16x32_bf16 v[48:51], v[136:139], a[244:247], v[48:51]\n"
"ds_read_b128 v[212:215], v229 offset:32768\n"
"v_cvt_pk_bf16_f32 v108, v160, v161\n"
"v_mfma_f32_16x16x32_bf16 v[48:51], v[140:143], a[248:251], v[48:51]\n"
"ds_read_b128 v[208:211], v228 offset:32768\n"
"v_cvt_pk_bf16_f32 v100, v156, v157\n"
"v_mfma_f32_16x16x32_bf16 v[80:83], v[144:147], a[244:247], v[80:83]\n"
"ds_read_b128 v[220:223], v229 offset:33792\n"
"v_cvt_pk_bf16_f32 v117, v166, v167\n"
"v_mfma_f32_16x16x32_bf16 v[80:83], v[148:151], a[248:251], v[80:83]\n"
"ds_read_b64_tr_b16 a[244:245], v234 offset:16384\n"
"v_cvt_pk_bf16_f32 v109, v162, v163\n"
"v_mfma_f32_16x16x32_bf16 v[84:87], v[148:151], v[224:227], v[84:87]\n"
"ds_read_b64_tr_b16 a[248:249], v238 offset:16384\n"
"v_mfma_f32_16x16x32_bf16 v[84:87], v[144:147], a[252:255], v[84:87]\n"
"ds_read_b64_tr_b16 a[246:247], v234 offset:20480\n"
"v_mfma_f32_16x16x32_bf16 v[52:55], v[140:143], v[224:227], v[52:55]\n"
"ds_read_b64_tr_b16 a[250:251], v238 offset:20480\n"
"v_mfma_f32_16x16x32_bf16 v[52:55], v[136:139], a[252:255], v[52:55]\n"
"v_exp_f32 v175, v175\n"
"v_mfma_f32_16x16x32_bf16 v[20:23], v[132:135], v[224:227], v[20:23]\n"
"ds_read_b64_tr_b16 v[224:225], v239 offset:16384\n"
"v_mfma_f32_16x16x32_bf16 v[20:23], v[128:131], a[252:255], v[20:23]\n"
"v_exp_f32 v173, v173\n"
"v_mfma_f32_16x16x32_bf16 a[228:231], v[124:127], a[252:255], a[228:231]\n"
"s_waitcnt lgkmcnt(5)\n"
"ds_read_b64_tr_b16 a[254:255], v235 offset:20480\n"
"v_cvt_pk_bf16_f32 v116, v164, v165\n"
"LAY2_%=:\n"
"v_mfma_f32_16x16x32_bf16 v[152:155], v[208:211], a[0:3], v[180:183]\n"
"s_waitcnt lgkmcnt(8)\n"
"v_exp_f32 v174, v174\n"
"v_mfma_f32_16x16x32_bf16 v[152:155], v[212:215], a[4:7], v[152:155]\n"
"s_waitcnt lgkmcnt(7)\n"
"ds_read_b64_tr_b16 a[252:253], v235 offset:16384\n"
"v_cvt_pk_bf16_f32 v111, v174, v175\n"
"v_mfma_f32_16x16x32_bf16 v[152:155], v[216:219], a[8:11], v[152:155]\n"
"s_waitcnt lgkmcnt(7)\n"
"s_add_u32 m0, s44, 114688\n"
"ds_read_b64_tr_b16 v[226:227], v239 offset:20480\n"
"v_mfma_f32_16x16x32_bf16 v[152:155], v[220:223], a[12:15], v[152:155]\n"
"buffer_load_dwordx4 v240, s[20:23], s56 offen nt lds\n"
"v_mfma_f32_16x16x32_bf16 v[156:159], v[216:219], a[24:27], v[184:187]\n"
"v_exp_f32 v171, v171\n"
"v_mfma_f32_16x16x32_bf16 v[156:159], v[220:223], a[28:31], v[156:159]\n"
"v_exp_f32 v168, v168\n"
"v_mfma_f32_16x16x32_bf16 v[156:159], v[208:211], a[16:19], v[156:159]\n"
"v_exp_f32 v176, v176\n"
"v_mfma_f32_16x16x32_bf16 v[156:159], v[212:215], a[20:23], v[156:159]\n"
"v_exp_f32 v177, v177\n"
"v_mfma_f32_16x16x32_bf16 v[160:163], v[208:211], a[32:35], v[188:191]\n"
"v_exp_f32 v179, v179\n"
"v_mfma_f32_16x16x32_bf16 v[160:163], v[212:215], a[36:39], v[160:163]\n"
"v_exp_f32 v170, v170\n"
"v_mfma_f32_16x16x32_bf16 v[160:163], v[216:219], a[40:43], v[160:163]\n"
"v_exp_f32 v153, v153\n"
"v_mfma_f32_16x16x32_bf16 v[160:163], v[220:223], a[44:47], v[160:163]\n"
"v_cvt_pk_bf16_f32 v102, v168, v169\n"
"v_mfma_f32_16x16x32_bf16 v[164:167], v[216:219], a[200:203], v[192:195]\n"
"ds_read_b128 v[216:219], v228 offset:37888\n"
"v_cvt_pk_bf16_f32 v103, v170, v171\n"
"v_mfma_f32_16x16x32_bf16 v[164:167], v[220:223], a[204:207], v[164:167]\n"
"v_cvt_pk_bf16_f32 v110, v172, v173\n"
"v_cvt_pk_bf16_f32 v118, v176, v177\n"
"v_mfma_f32_16x16x32_bf16 v[164:167], v[208:211], a[192:195], v[164:167]\n"
"s_add_u32 s40, s56, s52\n"
"ds_read_b128 v[208:211], v228 offset:36864\n"
"v_mfma_f32_16x16x32_bf16 v[164:167], v[212:215], a[196:199], v[164:167]\n"
"s_waitcnt lgkmcnt(2)\n"
"buffer_load_dwordx4 v240, s[20:23], s40 offen offset:1024 nt lds\n"
"v_mfma_f32_16x16x32_bf16 a[216:219], v[120:123], a[244:247], a[216:219]\n"
"v_exp_f32 v155, v155\n"
"v_mfma_f32_16x16x32_bf16 v[8:11], v[96:99], a[244:247], v[8:11]\n"
"s_waitcnt lgkmcnt(2)\n"
"v_exp_f32 v161, v161\n"
"v_mfma_f32_16x16x32_bf16 v[8:11], v[100:103], a[248:251], v[8:11]\n"
"v_exp_f32 v163, v163\n"
"v_mfma_f32_16x16x32_bf16 v[40:43], v[104:107], a[244:247], v[40:43]\n"
"v_exp_f32 v178, v178\n"
"v_mfma_f32_16x16x32_bf16 v[40:43], v[108:111], a[248:251], v[40:43]\n"
"ds_read_b128 v[220:223], v229 offset:37888\n"
"v_cvt_pk_bf16_f32 v119, v178, v179\n"
"v_mfma_f32_16x16x32_bf16 v[72:75], v[112:115], a[244:247], v[72:75]\n"
"ds_read_b64_tr_b16 a[244:245], v234 offset:17408\n"
"v_mfma_f32_16x16x32_bf16 v[72:75], v[116:119], a[248:251], v[72:75]\n"
"v_exp_f32 v158, v158\n"
"v_mfma_f32_16x16x32_bf16 v[76:79], v[116:119], v[224:227], v[76:79]\n"
"ds_read_b128 v[212:215], v229 offset:36864\n"
"v_mfma_f32_16x16x32_bf16 v[76:79], v[112:115], a[252:255], v[76:79]\n"
"v_exp_f32 v160, v160\n"
"v_mfma_f32_16x16x32_bf16 v[44:47], v[108:111], v[224:227], v[44:47]\n"
"s_add_u32 s40, s56, s53\n"
"ds_read_b64_tr_b16 a[248:249], v238 offset:17408\n"
"v_mfma_f32_16x16x32_bf16 v[44:47], v[104:107], a[252:255], v[44:47]\n"
"buffer_load_dwordx4 v240, s[20:23], s40 offen offset:2048 nt lds\n"
"v_mfma_f32_16x16x32_bf16 v[12:15], v[100:103], v[224:227], v[12:15]\n"
"v_exp_f32 v165, v165\n"
"v_mfma_f32_16x16x32_bf16 v[12:15], v[96:99], a[252:255], v[12:15]\n"
"v_exp_f32 v159, v159\n"
"v_mfma_f32_16x16x32_bf16 a[220:223], v[120:123], a[252:255], a[220:223]\n"
"v_exp_f32 v152, v152\n"
"v_smfmac_f32_16x16x64_bf16 v[196:199], a[240:243], v[96:103], v241\n"
"v_mfma_f32_16x16x32_bf16 v[202:205], a[240:243], v[120:123], v[202:205]\n"
"s_waitcnt lgkmcnt(1)\n"
"ds_read_b64_tr_b16 v[224:225], v239 offset:17408\n"
"v_cvt_pk_bf16_f32 v128, v152, v153\n"
"v_mfma_f32_16x16x32_bf16 v[168:171], v[216:219], a[8:11], v[180:183]\n"
"v_exp_f32 v162, v162\n"
"v_mfma_f32_16x16x32_bf16 v[168:171], v[220:223], a[12:15], v[168:171]\n"
"ds_read_b64_tr_b16 a[250:251], v238 offset:21504\n"
"v_mfma_f32_16x16x32_bf16 v[168:171], v[208:211], a[0:3], v[168:171]\n"
"ds_read_b64_tr_b16 a[252:253], v235 offset:17408\n"
"v_mfma_f32_16x16x32_bf16 v[168:171], v[212:215], a[4:7], v[168:171]\n"
"v_exp_f32 v154, v154\n"
"v_mfma_f32_16x16x32_bf16 v[172:175], v[208:211], a[16:19], v[184:187]\n"
"ds_read_b64_tr_b16 v[226:227], v239 offset:21504\n"
"v_mfma_f32_16x16x32_bf16 v[172:175], v[212:215], a[20:23], v[172:175]\n"
"ds_read_b64_tr_b16 a[246:247], v234 offset:21504\n"
"v_cvt_pk_bf16_f32 v129, v154, v155\n"
"v_mfma_f32_16x16x32_bf16 v[172:175], v[216:219], a[24:27], v[172:175]\n"
"s_add_u32 s40, s56, s54\n"
"ds_read_b64_tr_b16 a[254:255], v235 offset:21504\n"
"v_mfma_f32_16x16x32_bf16 v[172:175], v[220:223], a[28:31], v[172:175]\n"
"buffer_load_dwordx4 v240, s[20:23], s40 offen offset:3072 nt lds\n"
"v_mfma_f32_16x16x32_bf16 v[176:179], v[216:219], a[40:43], v[188:191]\n"
"s_add_u32 s55, s55, s59\n"
"v_exp_f32 v157, v157\n"
"v_mfma_f32_16x16x32_bf16 v[176:179], v[220:223], a[44:47], v[176:179]\n"
"s_add_u32 s41, s55, s57\n"
"v_exp_f32 v170, v170\n"
"v_mfma_f32_16x16x32_bf16 v[176:179], v[208:211], a[32:35], v[176:179]\n"
"s_and_b32 s41, s41, s58\n"
"v_exp_f32 v156, v156\n"
"v_mfma_f32_16x16x32_bf16 v[176:179], v[212:215], a[36:39], v[176:179]\n"
"s_andn2_b32 s56, s55, s58\n"
"v_exp_f32 v166, v166\n"
"v_mfma_f32_16x16x32_bf16 v[152:155], v[208:211], a[192:195], v[192:195]\n"
"s_or_b32 s56, s56, s41\n"
"ds_read_b128 v[208:211], v230 offset:32768\n"
"v_mfma_f32_16x16x32_bf16 v[152:155], v[212:215], a[196:199], v[152:155]\n"
"s_cmp_ge_u32 s47, 15\n"
"v_exp_f32 v174, v174\n"
"v_mfma_f32_16x16x32_bf16 v[152:155], v[216:219], a[200:203], v[152:155]\n"
"s_cbranch_scc1 LSW_%=\nLSWK_%=:\n"
"ds_read_b128 v[212:215], v231 offset:32768\n"
"v_mfma_f32_16x16x32_bf16 v[152:155], v[220:223], a[204:207], v[152:155]\n"
"s_waitcnt lgkmcnt(2)\n"
"ds_read_b128 v[216:219], v230 offset:33792\n"
"v_cvt_pk_bf16_f32 v137, v158, v159\n"
"v_mfma_f32_16x16x32_bf16 a[232:235], v[120:123], a[244:247], a[232:235]\n"
"v_exp_f32 v164, v164\n"
"v_mfma_f32_16x16x32_bf16 v[24:27], v[96:99], a[244:247], v[24:27]\n"
"s_mov_b32 m0, s44\n"
"ds_read_b128 v[220:223], v231 offset:33792\n"
"v_mfma_f32_16x16x32_bf16 v[24:27], v[100:103], a[248:251], v[24:27]\n"
"buffer_load_dwordx4 v240, s[16:19], s56 offen nt lds\n"
"v_mfma_f32_16x16x32_bf16 v[56:59], v[104:107], a[244:247], v[56:59]\n"
"v_exp_f32 v167, v167\n"
"v_mfma_f32_16x16x32_bf16 v[56:59], v[108:111], a[248:251], v[56:59]\n"
"v_exp_f32 v176, v176\n"
"v_mfma_f32_16x16x32_bf16 v[88:91], v[112:115], a[244:247], v[88:91]\n"
"v_exp_f32 v153, v153\n"
"v_mfma_f32_16x16x32_bf16 v[88:91], v[116:119], a[248:251], v[88:91]\n"
"v_exp_f32 v173, v173\n"
"v_mfma_f32_16x16x32_bf16 v[92:95], v[116:119], v[224:227], v[92:95]\n"
"ds_read_b64_tr_b16 a[250:251], v236 offset:20480\n"
"v_cvt_pk_bf16_f32 v125, v166, v167\n"
"v_mfma_f32_16x16x32_bf16 v[92:95], v[112:115], a[252:255], v[92:95]\n"
"ds_read_b64_tr_b16 a[244:245], v232 offset:16384\n"
"v_cvt_pk_bf16_f32 v144, v160, v161\n"
"v_mfma_f32_16x16x32_bf16 v[60:63], v[108:111], v[224:227], v[60:63]\n"
"ds_read_b64_tr_b16 a[248:249], v236 offset:16384\n"
"v_cvt_pk_bf16_f32 v124, v164, v165\n"
"v_mfma_f32_16x16x32_bf16 v[60:63], v[104:107], a[252:255], v[60:63]\n"
"ds_read_b64_tr_b16 a[246:247], v232 offset:20480\n"
"v_cvt_pk_bf16_f32 v136, v156, v157\n"
"v_mfma_f32_16x16x32_bf16 v[28:31], v[100:103], v[224:227], v[28:31]\n"
"ds_read_b64_tr_b16 v[226:227], v237 offset:20480\n"
"v_cvt_pk_bf16_f32 v145, v162, v163\n"
"v_mfma_f32_16x16x32_bf16 v[28:31], v[96:99], a[252:255], v[28:31]\n"
"ds_read_b64_tr_b16 v[224:225], v237 offset:16384\n"
"v_mfma_f32_16x16x32_bf16 a[236:239], v[120:123], a[252:255], a[236:239]\n"
"s_add_u32 s40, s56, s52\n"
"ds_read_b64_tr_b16 a[252:253], v233 offset:16384\n"
"v_smfmac_f32_16x16x64_bf16 v[198:201], a[240:243], v[104:111], v241\n"
"s_waitcnt lgkmcnt(7)\n"
"buffer_load_dwordx4 v240, s[16:19], s40 offen offset:1024 nt lds\n"
"v_mfma_f32_16x16x32_bf16 v[156:159], v[208:211], a[0:3], v[180:183]\n"
"v_exp_f32 v169, v169\n"
"v_mfma_f32_16x16x32_bf16 v[156:159], v[212:215], a[4:7], v[156:159]\n"
"v_exp_f32 v168, v168\n"
"v_mfma_f32_16x16x32_bf16 v[156:159], v[216:219], a[8:11], v[156:159]\n"
"v_exp_f32 v175, v175\n"
"v_mfma_f32_16x16x32_bf16 v[156:159], v[220:223], a[12:15], v[156:159]\n"
"v_exp_f32 v172, v172\n"
"v_mfma_f32_16x16x32_bf16 v[160:163], v[216:219], a[24:27], v[184:187]\n"
"ds_read_b64_tr_b16 a[254:255], v233 offset:20480\n"
"v_mfma_f32_16x16x32_bf16 v[160:163], v[220:223], a[28:31], v[160:163]\n"
"v_exp_f32 v155, v155\n"
"v_mfma_f32_16x16x32_bf16 v[160:163], v[208:211], a[16:19], v[160:163]\n"
"v_exp_f32 v177, v177\n"
"v_mfma_f32_16x16x32_bf16 v[160:163], v[212:215], a[20:23], v[160:163]\n"
"v_exp_f32 v179, v179\n"
"v_mfma_f32_16x16x32_bf16 v[164:167], v[208:211], a[32:35], v[188:191]\n"
"ds_read_b128 v[208:211], v230 offset:36864\n"
"v_cvt_pk_bf16_f32 v139, v174, v175\n"
"v_mfma_f32_16x16x32_bf16 v[164:167], v[212:215], a[36:39], v[164:167]\n"
"v_exp_f32 v171, v171\n"
"v_mfma_f32_16x16x32_bf16 v[164:167], v[216:219], a[40:43], v[164:167]\n"
"ds_read_b128 v[212:215], v231 offset:36864\n"
"v_cvt_pk_bf16_f32 v138, v172, v173\n"
"v_mfma_f32_16x16x32_bf16 v[164:167], v[220:223], a[44:47], v[164:167]\n"
"s_waitcnt lgkmcnt(3)\n"
"ds_read_b128 v[220:223], v231 offset:37888\n"
"v_cvt_pk_bf16_f32 v130, v168, v169\n"
"v_mfma_f32_16x16x32_bf16 a[208:211], v[120:123], a[244:247], a[208:211]\n"
"v_exp_f32 v154, v154\n"
"v_mfma_f32_16x16x32_bf16 v[0:3], v[96:99], a[244:247], v[0:3]\n"
"s_add_u32 s40, s56, s53\n"
"ds_read_b128 v[216:219], v230 offset:37888\n"
"v_mfma_f32_16x16x32_bf16 v[0:3], v[100:103], a[248:251], v[0:3]\n"
"buffer_load_dwordx4 v240, s[16:19], s40 offen offset:2048 nt lds\n"
"v_mfma_f32_16x16x32_bf16 v[32:35], v[104:107], a[244:247], v[32:35]\n"
"v_exp_f32 v160, v160\n"
"v_mfma_f32_16x16x32_bf16 v[32:35], v[108:111], a[248:251], v[32:35]\n"
"v_exp_f32 v178, v178\n"
"v_mfma_f32_16x16x32_bf16 v[64:67], v[112:115], a[244:247], v[64:67]\n"
"v_exp_f32 v152, v152\n"
"v_mfma_f32_16x16x32_bf16 v[64:67], v[116:119], a[248:251], v[64:67]\n"
"v_exp_f32 v161, v161\n"
"v_mfma_f32_16x16x32_bf16 v[68:71], v[116:119], v[224:227], v[68:71]\n"
"s_waitcnt lgkmcnt(1)\n"
"ds_read_b64_tr_b16 a[244:245], v232 offset:17408\n"
"v_cvt_pk_bf16_f32 v147, v178, v179\n"
"v_mfma_f32_16x16x32_bf16 v[68:71], v[112:115], a[252:255], v[68:71]\n"
"ds_read_b64_tr_b16 a[248:249], v236 offset:17408\n"
"v_cvt_pk_bf16_f32 v146, v176, v177\n"
"v_mfma_f32_16x16x32_bf16 v[36:39], v[108:111], v[224:227], v[36:39]\n"
"ds_read_b64_tr_b16 a[250:251], v236 offset:21504\n"
"v_cvt_pk_bf16_f32 v131, v170, v171\n"
"v_mfma_f32_16x16x32_bf16 v[36:39], v[104:107], a[252:255], v[36:39]\n"
"v_exp_f32 v163, v163\n"
"v_mfma_f32_16x16x32_bf16 v[4:7], v[100:103], v[224:227], v[4:7]\n"
"s_add_u32 s40, s56, s54\n"
"ds_read_b64_tr_b16 a[246:247], v232 offset:21504\n"
"v_mfma_f32_16x16x32_bf16 v[4:7], v[96:99], a[252:255], v[4:7]\n"
"buffer_load_dwordx4 v240, s[16:19], s40 offen offset:3072 nt lds\n"
"v_mfma_f32_16x16x32_bf16 a[212:215], v[120:123], a[252:255], a[212:215]\n"
"v_exp_f32 v167, v167\n"
"v_smfmac_f32_16x16x64_bf16 v[200:203], a[240:243], v[112:119], v241\n"
"v_cvt_pk_bf16_f32 v126, v152, v153\n"
"v_mfma_f32_16x16x32_bf16 v[168:171], v[208:211], a[0:3], v[180:183]\n"
"v_exp_f32 v159, v159\n"
"v_mfma_f32_16x16x32_bf16 v[168:171], v[212:215], a[4:7], v[168:171]\n"
"s_waitcnt lgkmcnt(2)\n"
"v_exp_f32 v165, v165\n"
"v_mfma_f32_16x16x32_bf16 v[168:171], v[216:219], a[8:11], v[168:171]\n"
"ds_read_b64_tr_b16 a[252:253], v233 offset:17408\n"
"ds_read_b64_tr_b16 v[224:225], v237 offset:17408\n"
"v_mfma_f32_16x16x32_bf16 v[168:171], v[220:223], a[12:15], v[168:171]\n"
"ds_read_b64_tr_b16 a[254:255], v233 offset:21504\n"
"ds_read_b64_tr_b16 v[226:227], v237 offset:21504\n"
"v_mfma_f32_16x16x32_bf16 v[172:175], v[216:219], a[24:27], v[184:187]\n"
"v_exp_f32 v166, v166\n"
"v_mfma_f32_16x16x32_bf16 v[172:175], v[220:223], a[28:31], v[172:175]\n"
"v_exp_f32 v164, v164\n"
"v_mfma_f32_16x16x32_bf16 v[172:175], v[208:211], a[16:19], v[172:175]\n"
"v_exp_f32 v157, v157\n"
"v_mfma_f32_16x16x32_bf16 v[172:175], v[212:215], a[20:23], v[172:175]\n"
"s_cbranch_vccnz LPCL2_%=\nLPKL2_%=:\n"
"v_cvt_pk_bf16_f32 v127, v154, v155\n"
"v_mfma_f32_16x16x32_bf16 v[176:179], v[208:211], a[32:35], v[188:191]\n"
"v_exp_f32 v158, v158\n"
"v_mfma_f32_16x16x32_bf16 v[176:179], v[212:215], a[36:39], v[176:179]\n"
"v_exp_f32 v162, v162\n"
"v_mfma_f32_16x16x32_bf16 v[176:179], v[216:219], a[40:43], v[176:179]\n"
"v_exp_f32 v156, v156\n"
"v_mfma_f32_16x16x32_bf16 v[176:179], v[220:223], a[44:47], v[176:179]\n"
"s_waitcnt lgkmcnt(0)\n"
"v_cvt_pk_bf16_f32 v133, v158, v159\n"
"v_mfma_f32_16x16x32_bf16 a[224:227], v[120:123], a[244:247], a[224:227]\n"
"v_exp_f32 v169, v169\n"
"v_mfma_f32_16x16x32_bf16 v[16:19], v[96:99], a[244:247], v[16:19]\n"
"s_waitcnt vmcnt(8)\n"
"v_exp_f32 v172, v172\n"
"v_mfma_f32_16x16x32_bf16 v[16:19], v[100:103], a[248:251], v[16:19]\n"
"s_barrier\n"
"ds_read_b128 v[216:219], v228 offset:50176\n"
"v_mfma_f32_16x16x32_bf16 v[48:51], v[104:107], a[244:247], v[48:51]\n"
"ds_read_b128 v[212:215], v229 offset:49152\n"
"v_cvt_pk_bf16_f32 v140, v160, v161\n"
"v_mfma_f32_16x16x32_bf16 v[48:51], v[108:111], a[248:251], v[48:51]\n"
"ds_read_b128 v[208:211], v228 offset:49152\n"
"v_cvt_pk_bf16_f32 v132, v156, v157\n"
"v_mfma_f32_16x16x32_bf16 v[80:83], v[112:115], a[244:247], v[80:83]\n"
"ds_read_b128 v[220:223], v229 offset:50176\n"
"v_cvt_pk_bf16_f32 v149, v166, v167\n"
"v_mfma_f32_16x16x32_bf16 v[80:83], v[116:119], a[248:251], v[80:83]\n"
"ds_read_b64_tr_b16 a[244:245], v234 offset:32768\n"
"v_cvt_pk_bf16_f32 v141, v162, v163\n"
"v_mfma_f32_16x16x32_bf16 v[84:87], v[116:119], v[224:227], v[84:87]\n"
"ds_read_b64_tr_b16 a[248:249], v238 offset:32768\n"
"v_mfma_f32_16x16x32_bf16 v[84:87], v[112:115], a[252:255], v[84:87]\n"
"ds_read_b64_tr_b16 a[246:247], v234 offset:36864\n"
"v_mfma_f32_16x16x32_bf16 v[52:55], v[108:111], v[224:227], v[52:55]\n"
"ds_read_b64_tr_b16 a[250:251], v238 offset:36864\n"
"v_mfma_f32_16x16x32_bf16 v[52:55], v[104:107], a[252:255], v[52:55]\n"
"v_exp_f32 v175, v175\n"
"v_mfma_f32_16x16x32_bf16 v[20:23], v[100:103], v[224:227], v[20:23]\n"
"ds_read_b64_tr_b16 v[224:225], v239 offset:32768\n"
"v_mfma_f32_16x16x32_bf16 v[20:23], v[96:99], a[252:255], v[20:23]\n"
"v_exp_f32 v173, v173\n"
"v_mfma_f32_16x16x32_bf16 a[228:231], v[120:123], a[252:255], a[228:231]\n"
"s_waitcnt lgkmcnt(5)\n"
"ds_read_b64_tr_b16 a[254:255], v235 offset:36864\n"
"v_cvt_pk_bf16_f32 v148, v164, v165\n"
"v_mfma_f32_16x16x32_bf16 v[152:155], v[208:211], a[0:3], v[180:183]\n"
"v_exp_f32 v174, v174\n"
"v_mfma_f32_16x16x32_bf16 v[152:155], v[212:215], a[4:7], v[152:155]\n"
"ds_read_b64_tr_b16 a[252:253], v235 offset:32768\n"
"v_cvt_pk_bf16_f32 v143, v174, v175\n"
"v_mfma_f32_16x16x32_bf16 v[152:155], v[216:219], a[8:11], v[152:155]\n"
"s_add_u32 m0, s44, 65536\n"
"ds_read_b64_tr_b16 v[226:227], v239 offset:36864\n"
"v_mfma_f32_16x16x32_bf16 v[152:155], v[220:223], a[12:15], v[152:155]\n"
"buffer_load_dwordx4 v240, s[20:23], s56 offen nt lds\n"
"v_mfma_f32_16x16x32_bf16 v[156:159], v[216:219], a[24:27], v[184:187]\n"
"v_exp_f32 v171, v171\n"
"v_mfma_f32_16x16x32_bf16 v[156:159], v[220:223], a[28:31], v[156:159]\n"
"v_exp_f32 v168, v168\n"
"v_mfma_f32_16x16x32_bf16 v[156:159], v[208:211], a[16:19], v[156:159]\n"
"v_exp_f32 v176, v176\n"
"v_mfma_f32_16x16x32_bf16 v[156:159], v[212:215], a[20:23], v[156:159]\n"
"v_exp_f32 v177, v177\n"
"v_mfma_f32_16x16x32_bf16 v[160:163], v[208:211], a[32:35], v[188:191]\n"
"v_exp_f32 v179, v179\n"
"v_mfma_f32_16x16x32_bf16 v[160:163], v[212:215], a[36:39], v[160:163]\n"
"v_exp_f32 v170, v170\n"
"v_mfma_f32_16x16x32_bf16 v[160:163], v[216:219], a[40:43], v[160:163]\n"
"v_exp_f32 v153, v153\n"
"v_mfma_f32_16x16x32_bf16 v[160:163], v[220:223], a[44:47], v[160:163]\n"
"v_cvt_pk_bf16_f32 v134, v168, v169\n"
"v_mfma_f32_16x16x32_bf16 v[164:167], v[216:219], a[200:203], v[192:195]\n"
"ds_read_b128 v[216:219], v228 offset:54272\n"
"v_cvt_pk_bf16_f32 v135, v170, v171\n"
"v_mfma_f32_16x16x32_bf16 v[164:167], v[220:223], a[204:207], v[164:167]\n"
"v_cvt_pk_bf16_f32 v142, v172, v173\n"
"v_cvt_pk_bf16_f32 v150, v176, v177\n"
"v_mfma_f32_16x16x32_bf16 v[164:167], v[208:211], a[192:195], v[164:167]\n"
"s_add_u32 s40, s56, s52\n"
"ds_read_b128 v[208:211], v228 offset:53248\n"
"v_mfma_f32_16x16x32_bf16 v[164:167], v[212:215], a[196:199], v[164:167]\n"
"s_waitcnt lgkmcnt(2)\n"
"buffer_load_dwordx4 v240, s[20:23], s40 offen offset:1024 nt lds\n"
"v_mfma_f32_16x16x32_bf16 a[216:219], v[124:127], a[244:247], a[216:219]\n"
"v_exp_f32 v155, v155\n"
"v_mfma_f32_16x16x32_bf16 v[8:11], v[128:131], a[244:247], v[8:11]\n"
"v_exp_f32 v161, v161\n"
"v_mfma_f32_16x16x32_bf16 v[8:11], v[132:135], a[248:251], v[8:11]\n"
"v_exp_f32 v163, v163\n"
"v_mfma_f32_16x16x32_bf16 v[40:43], v[136:139], a[244:247], v[40:43]\n"
"v_exp_f32 v178, v178\n"
"v_mfma_f32_16x16x32_bf16 v[40:43], v[140:143], a[248:251], v[40:43]\n"
"ds_read_b128 v[220:223], v229 offset:54272\n"
"v_cvt_pk_bf16_f32 v151, v178, v179\n"
"v_mfma_f32_16x16x32_bf16 v[72:75], v[144:147], a[244:247], v[72:75]\n"
"ds_read_b64_tr_b16 a[244:245], v234 offset:33792\n"
"v_mfma_f32_16x16x32_bf16 v[72:75], v[148:151], a[248:251], v[72:75]\n"
"v_exp_f32 v158, v158\n"
"v_mfma_f32_16x16x32_bf16 v[76:79], v[148:151], v[224:227], v[76:79]\n"
"ds_read_b128 v[212:215], v229 offset:53248\n"
"v_mfma_f32_16x16x32_bf16 v[76:79], v[144:147], a[252:255], v[76:79]\n"
"v_exp_f32 v160, v160\n"
"v_mfma_f32_16x16x32_bf16 v[44:47], v[140:143], v[224:227], v[44:47]\n"
"s_add_u32 s40, s56, s53\n"
"ds_read_b64_tr_b16 a[248:249], v238 offset:33792\n"
"v_mfma_f32_16x16x32_bf16 v[44:47], v[136:139], a[252:255], v[44:47]\n"
"buffer_load_dwordx4 v240, s[20:23], s40 offen offset:2048 nt lds\n"
"v_mfma_f32_16x16x32_bf16 v[12:15], v[132:135], v[224:227], v[12:15]\n"
"v_exp_f32 v165, v165\n"
"v_mfma_f32_16x16x32_bf16 v[12:15], v[128:131], a[252:255], v[12:15]\n"
"v_exp_f32 v159, v159\n"
"v_mfma_f32_16x16x32_bf16 a[220:223], v[124:127], a[252:255], a[220:223]\n"
"v_exp_f32 v152, v152\n"
"v_smfmac_f32_16x16x64_bf16 v[196:199], a[240:243], v[128:135], v241\n"
"v_mfma_f32_16x16x32_bf16 v[202:205], a[240:243], v[124:127], v[202:205]\n"
"s_waitcnt lgkmcnt(1)\n"
"ds_read_b64_tr_b16 v[224:225], v239 offset:33792\n"
"v_cvt_pk_bf16_f32 v96, v152, v153\n"
"v_mfma_f32_16x16x32_bf16 v[168:171], v[216:219], a[8:11], v[180:183]\n"
"v_exp_f32 v162, v162\n"
"v_mfma_f32_16x16x32_bf16 v[168:171], v[220:223], a[12:15], v[168:171]\n"
"ds_read_b64_tr_b16 a[250:251], v238 offset:37888\n"
"v_mfma_f32_16x16x32_bf16 v[168:171], v[208:211], a[0:3], v[168:171]\n"
"ds_read_b64_tr_b16 a[252:253], v235 offset:33792\n"
"v_mfma_f32_16x16x32_bf16 v[168:171], v[212:215], a[4:7], v[168:171]\n"
"v_exp_f32 v154, v154\n"
"v_mfma_f32_16x16x32_bf16 v[172:175], v[208:211], a[16:19], v[184:187]\n"
"ds_read_b64_tr_b16 v[226:227], v239 offset:37888\n"
"v_mfma_f32_16x16x32_bf16 v[172:175], v[212:215], a[20:23], v[172:175]\n"
"ds_read_b64_tr_b16 a[246:247], v234 offset:37888\n"
"v_cvt_pk_bf16_f32 v97, v154, v155\n"
"v_mfma_f32_16x16x32_bf16 v[172:175], v[216:219], a[24:27], v[172:175]\n"
"s_add_u32 s40, s56, s54\n"
"ds_read_b64_tr_b16 a[254:255], v235 offset:37888\n"
"v_mfma_f32_16x16x32_bf16 v[172:175], v[220:223], a[28:31], v[172:175]\n"
"buffer_load_dwordx4 v240, s[20:23], s40 offen offset:3072 nt lds\n"
"v_mfma_f32_16x16x32_bf16 v[176:179], v[216:219], a[40:43], v[188:191]\n"
"s_add_u32 s55, s55, s59\n"
"v_exp_f32 v157, v157\n"
"v_mfma_f32_16x16x32_bf16 v[176:179], v[220:223], a[44:47], v[176:179]\n"
"s_add_u32 s41, s55, s57\n"
"v_exp_f32 v170, v170\n"
"v_mfma_f32_16x16x32_bf16 v[176:179], v[208:211], a[32:35], v[176:179]\n"
"s_and_b32 s41, s41, s58\n"
"v_exp_f32 v156, v156\n"
"v_mfma_f32_16x16x32_bf16 v[176:179], v[212:215], a[36:39], v[176:179]\n"
"s_andn2_b32 s56, s55, s58\n"
"v_exp_f32 v166, v166\n"
"v_mfma_f32_16x16x32_bf16 v[152:155], v[208:211], a[192:195], v[192:195]\n"
"s_or_b32 s56, s56, s41\n"
"ds_read_b128 v[208:211], v230 offset:49152\n"
"v_mfma_f32_16x16x32_bf16 v[152:155], v[212:215], a[196:199], v[152:155]\n"
"v_exp_f32 v174, v174\n"
"v_mfma_f32_16x16x32_bf16 v[152:155], v[216:219], a[200:203], v[152:155]\n"
"ds_read_b128 v[212:215], v231 offset:49152\n"
"v_mfma_f32_16x16x32_bf16 v[152:155], v[220:223], a[204:207], v[152:155]\n"
"s_waitcnt lgkmcnt(2)\n"
"ds_read_b128 v[216:219], v230 offset:50176\n"
"v_cvt_pk_bf16_f32 v105, v158, v159\n"
"v_mfma_f32_16x16x32_bf16 a[232:235], v[124:127], a[244:247], a[232:235]\n"
"v_exp_f32 v164, v164\n"
"v_mfma_f32_16x16x32_bf16 v[24:27], v[128:131], a[244:247], v[24:27]\n"
"s_add_u32 m0, s44, 16384\n"
"ds_read_b128 v[220:223], v231 offset:50176\n"
"v_mfma_f32_16x16x32_bf16 v[24:27], v[132:135], a[248:251], v[24:27]\n"
"buffer_load_dwordx4 v240, s[16:19], s56 offen nt lds\n"
"v_mfma_f32_16x16x32_bf16 v[56:59], v[136:139], a[244:247], v[56:59]\n"
"v_exp_f32 v167, v167\n"
"v_mfma_f32_16x16x32_bf16 v[56:59], v[140:143], a[248:251], v[56:59]\n"
"v_exp_f32 v176, v176\n"
"v_mfma_f32_16x16x32_bf16 v[88:91], v[144:147], a[244:247], v[88:91]\n"
"v_exp_f32 v153, v153\n"
"v_mfma_f32_16x16x32_bf16 v[88:91], v[148:151], a[248:251], v[88:91]\n"
"v_exp_f32 v173, v173\n"
"v_mfma_f32_16x16x32_bf16 v[92:95], v[148:151], v[224:227], v[92:95]\n"
"ds_read_b64_tr_b16 a[250:251], v236 offset:36864\n"
"v_cvt_pk_bf16_f32 v121, v166, v167\n"
"v_mfma_f32_16x16x32_bf16 v[92:95], v[144:147], a[252:255], v[92:95]\n"
"ds_read_b64_tr_b16 a[244:245], v232 offset:32768\n"
"v_cvt_pk_bf16_f32 v112, v160, v161\n"
"v_mfma_f32_16x16x32_bf16 v[60:63], v[140:143], v[224:227], v[60:63]\n"
"ds_read_b64_tr_b16 a[248:249], v236 offset:32768\n"
"v_cvt_pk_bf16_f32 v120, v164, v165\n"
"v_mfma_f32_16x16x32_bf16 v[60:63], v[136:139], a[252:255], v[60:63]\n"
"ds_read_b64_tr_b16 a[246:247], v232 offset:36864\n"
"v_cvt_pk_bf16_f32 v104, v156, v157\n"
"v_mfma_f32_16x16x32_bf16 v[28:31], v[132:135], v[224:227], v[28:31]\n"
"ds_read_b64_tr_b16 v[226:227], v237 offset:36864\n"
"v_cvt_pk_bf16_f32 v113, v162, v163\n"
"v_mfma_f32_16x16x32_bf16 v[28:31], v[128:131], a[252:255], v[28:31]\n"
"ds_read_b64_tr_b16 v[224:225], v237 offset:32768\n"
"v_mfma_f32_16x16x32_bf16 a[236:239], v[124:127], a[252:255], a[236:239]\n"
"s_add_u32 s40, s56, s52\n"
"ds_read_b64_tr_b16 a[252:253], v233 offset:32768\n"
"v_smfmac_f32_16x16x64_bf16 v[198:201], a[240:243], v[136:143], v241\n"
"s_waitcnt lgkmcnt(7)\n"
"buffer_load_dwordx4 v240, s[16:19], s40 offen offset:1024 nt lds\n"
"v_mfma_f32_16x16x32_bf16 v[156:159], v[208:211], a[0:3], v[180:183]\n"
"v_exp_f32 v169, v169\n"
"v_mfma_f32_16x16x32_bf16 v[156:159], v[212:215], a[4:7], v[156:159]\n"
"v_exp_f32 v168, v168\n"
"v_mfma_f32_16x16x32_bf16 v[156:159], v[216:219], a[8:11], v[156:159]\n"
"v_exp_f32 v175, v175\n"
"v_mfma_f32_16x16x32_bf16 v[156:159], v[220:223], a[12:15], v[156:159]\n"
"v_exp_f32 v172, v172\n"
"v_mfma_f32_16x16x32_bf16 v[160:163], v[216:219], a[24:27], v[184:187]\n"
"ds_read_b64_tr_b16 a[254:255], v233 offset:36864\n"
"v_mfma_f32_16x16x32_bf16 v[160:163], v[220:223], a[28:31], v[160:163]\n"
"v_exp_f32 v155, v155\n"
"v_mfma_f32_16x16x32_bf16 v[160:163], v[208:211], a[16:19], v[160:163]\n"
"v_exp_f32 v177, v177\n"
"v_mfma_f32_16x16x32_bf16 v[160:163], v[212:215], a[20:23], v[160:163]\n"
"v_exp_f32 v179, v179\n"
"v_mfma_f32_16x16x32_bf16 v[164:167], v[208:211], a[32:35], v[188:191]\n"
"ds_read_b128 v[208:211], v230 offset:53248\n"
"v_cvt_pk_bf16_f32 v107, v174, v175\n"
"v_mfma_f32_16x16x32_bf16 v[164:167], v[212:215], a[36:39], v[164:167]\n"
"v_exp_f32 v171, v171\n"
"v_mfma_f32_16x16x32_bf16 v[164:167], v[216:219], a[40:43], v[164:167]\n"
"ds_read_b128 v[212:215], v231 offset:53248\n"
"v_cvt_pk_bf16_f32 v106, v172, v173\n"
"v_mfma_f32_16x16x32_bf16 v[164:167], v[220:223], a[44:47], v[164:167]\n"
"s_waitcnt lgkmcnt(3)\n"
"ds_read_b128 v[220:223], v231 offset:54272\n"
"v_cvt_pk_bf16_f32 v98, v168, v169\n"
"v_mfma_f32_16x16x32_bf16 a[208:211], v[124:127], a[244:247], a[208:211]\n"
"v_exp_f32 v154, v154\n"
"v_mfma_f32_16x16x32_bf16 v[0:3], v[128:131], a[244:247], v[0:3]\n"
"s_add_u32 s40, s56, s53\n"
"ds_read_b128 v[216:219], v230 offset:54272\n"
"v_mfma_f32_16x16x32_bf16 v[0:3], v[132:135], a[248:251], v[0:3]\n"
"buffer_load_dwordx4 v240, s[16:19], s40 offen offset:2048 nt lds\n"
"v_mfma_f32_16x16x32_bf16 v[32:35], v[136:139], a[244:247], v[32:35]\n"
"v_exp_f32 v160, v160\n"
"v_mfma_f32_16x16x32_bf16 v[32:35], v[140:143], a[248:251], v[32:35]\n"
"v_exp_f32 v178, v178\n"
"v_mfma_f32_16x16x32_bf16 v[64:67], v[144:147], a[244:247], v[64:67]\n"
"v_exp_f32 v152, v152\n"
"v_mfma_f32_16x16x32_bf16 v[64:67], v[148:151], a[248:251], v[64:67]\n"
"v_exp_f32 v161, v161\n"
"v_mfma_f32_16x16x32_bf16 v[68:71], v[148:151], v[224:227], v[68:71]\n"
"s_waitcnt lgkmcnt(1)\n"
"ds_read_b64_tr_b16 a[244:245], v232 offset:33792\n"
"v_cvt_pk_bf16_f32 v115, v178, v179\n"
"v_mfma_f32_16x16x32_bf16 v[68:71], v[144:147], a[252:255], v[68:71]\n"
"ds_read_b64_tr_b16 a[248:249], v236 offset:33792\n"
"v_cvt_pk_bf16_f32 v114, v176, v177\n"
"v_mfma_f32_16x16x32_bf16 v[36:39], v[140:143], v[224:227], v[36:39]\n"
"ds_read_b64_tr_b16 a[250:251], v236 offset:37888\n"
"v_cvt_pk_bf16_f32 v99, v170, v171\n"
"v_mfma_f32_16x16x32_bf16 v[36:39], v[136:139], a[252:255], v[36:39]\n"
"v_exp_f32 v163, v163\n"
"v_mfma_f32_16x16x32_bf16 v[4:7], v[132:135], v[224:227], v[4:7]\n"
"s_add_u32 s40, s56, s54\n"
"ds_read_b64_tr_b16 a[246:247], v232 offset:37888\n"
"v_mfma_f32_16x16x32_bf16 v[4:7], v[128:131], a[252:255], v[4:7]\n"
"buffer_load_dwordx4 v240, s[16:19], s40 offen offset:3072 nt lds\n"
"v_mfma_f32_16x16x32_bf16 a[212:215], v[124:127], a[252:255], a[212:215]\n"
"v_exp_f32 v167, v167\n"
"v_smfmac_f32_16x16x64_bf16 v[200:203], a[240:243], v[144:151], v241\n"
"v_cvt_pk_bf16_f32 v122, v152, v153\n"
"v_mfma_f32_16x16x32_bf16 v[168:171], v[208:211], a[0:3], v[180:183]\n"
"v_exp_f32 v159, v159\n"
"v_mfma_f32_16x16x32_bf16 v[168:171], v[212:215], a[4:7], v[168:171]\n"
"s_waitcnt lgkmcnt(2)\n"
"v_exp_f32 v165, v165\n"
"v_mfma_f32_16x16x32_bf16 v[168:171], v[216:219], a[8:11], v[168:171]\n"
"ds_read_b64_tr_b16 a[252:253], v233 offset:33792\n"
"ds_read_b64_tr_b16 v[224:225], v237 offset:33792\n"
"v_mfma_f32_16x16x32_bf16 v[168:171], v[220:223], a[12:15], v[168:171]\n"
"ds_read_b64_tr_b16 a[254:255], v233 offset:37888\n"
"ds_read_b64_tr_b16 v[226:227], v237 offset:37888\n"
"v_mfma_f32_16x16x32_bf16 v[172:175], v[216:219], a[24:27], v[184:187]\n"
"v_exp_f32 v166, v166\n"
"v_mfma_f32_16x16x32_bf16 v[172:175], v[220:223], a[28:31], v[172:175]\n"
"v_exp_f32 v164, v164\n"
"v_mfma_f32_16x16x32_bf16 v[172:175], v[208:211], a[16:19], v[172:175]\n"
"v_exp_f32 v157, v157\n"
"v_mfma_f32_16x16x32_bf16 v[172:175], v[212:215], a[20:23], v[172:175]\n"
"s_cbranch_vccnz LPCL3_%=\nLPKL3_%=:\n"
"v_cvt_pk_bf16_f32 v123, v154, v155\n"
"v_mfma_f32_16x16x32_bf16 v[176:179], v[208:211], a[32:35], v[188:191]\n"
"s_add_u32 s47, s47, 1\n"
"v_exp_f32 v158, v158\n"
"v_mfma_f32_16x16x32_bf16 v[176:179], v[212:215], a[36:39], v[176:179]\n"
"v_exp_f32 v162, v162\n"
"v_mfma_f32_16x16x32_bf16 v[176:179], v[216:219], a[40:43], v[176:179]\n"
"s_and_b32 s40, s47, 3\n"
"v_exp_f32 v156, v156\n"
"v_mfma_f32_16x16x32_bf16 v[176:179], v[220:223], a[44:47], v[176:179]\n"
"s_waitcnt lgkmcnt(0)\n"
"v_cvt_pk_bf16_f32 v101, v158, v159\n"
"s_cbranch_scc1 LAL_%=\n"
"s_cmp_eq_u32 s47, 8\n"
"s_cbranch_scc1 LTH1_%=\n"
"s_cmp_eq_u32 s47, 12\n"
"s_cbranch_scc1 LTH2_%=\n"
"s_cmp_eq_u32 s47, 16\n"
"s_cbranch_scc1 LAX_%=\n"
"LTH0_%=:\n"
"v_mfma_f32_16x16x32_bf16 a[224:227], v[124:127], a[244:247], a[224:227]\n"
"v_exp_f32 v170, v170\n"
"v_mfma_f32_16x16x32_bf16 v[16:19], v[128:131], a[244:247], v[16:19]\n"
"s_waitcnt vmcnt(8)\n"
"v_exp_f32 v168, v168\n"
"v_mfma_f32_16x16x32_bf16 v[16:19], v[132:135], a[248:251], v[16:19]\n"
"s_barrier\n"
"v_mfma_f32_16x16x32_bf16 v[48:51], v[136:139], a[244:247], v[48:51]\n"
"v_exp_f32 v173, v173\n"
"v_mfma_f32_16x16x32_bf16 v[48:51], v[140:143], a[248:251], v[48:51]\n"
"v_exp_f32 v171, v171\n"
"v_mfma_f32_16x16x32_bf16 v[80:83], v[144:147], a[244:247], v[80:83]\n"
"ds_read_b64_tr_b16 a[244:245], v234 offset:49152\n"
"ds_read_b64_tr_b16 a[246:247], v234 offset:53248\n"
"v_mfma_f32_16x16x32_bf16 v[80:83], v[148:151], a[248:251], v[80:83]\n"
"v_exp_f32 v179, v179\n"
"v_mfma_f32_16x16x32_bf16 v[84:87], v[148:151], v[224:227], v[84:87]\n"
"ds_read_b64_tr_b16 a[250:251], v238 offset:53248\n"
"v_cvt_pk_bf16_f32 v100, v156, v157\n"
"v_mfma_f32_16x16x32_bf16 v[84:87], v[144:147], a[252:255], v[84:87]\n"
"v_exp_f32 v169, v169\n"
"v_exp_f32 v172, v172\n"
"v_exp_f32 v175, v175\n"
"v_exp_f32 v176, v176\n"
"v_exp_f32 v178, v178\n"
"v_mfma_f32_16x16x32_bf16 v[52:55], v[140:143], v[224:227], v[52:55]\n"
"ds_read_b64_tr_b16 a[248:249], v238 offset:49152\n"
"v_cvt_pk_bf16_f32 v117, v166, v167\n"
"v_mfma_f32_16x16x32_bf16 v[52:55], v[136:139], a[252:255], v[52:55]\n"
"v_exp_f32 v174, v174\n"
"v_mfma_f32_16x16x32_bf16 v[20:23], v[132:135], v[224:227], v[20:23]\n"
"ds_read_b64_tr_b16 v[224:225], v239 offset:49152\n"
"ds_read_b64_tr_b16 v[226:227], v239 offset:53248\n"
"v_mfma_f32_16x16x32_bf16 v[20:23], v[128:131], a[252:255], v[20:23]\n"
"v_cvt_pk_bf16_f32 v102, v168, v169\n"
"v_cvt_pk_bf16_f32 v103, v170, v171\n"
"v_cvt_pk_bf16_f32 v111, v174, v175\n"
"v_exp_f32 v177, v177\n"
"v_mfma_f32_16x16x32_bf16 a[228:231], v[124:127], a[252:255], a[228:231]\n"
"s_waitcnt lgkmcnt(4)\n"
"ds_read_b64_tr_b16 a[254:255], v235 offset:53248\n"
"v_cvt_pk_bf16_f32 v110, v172, v173\n"
"v_mfma_f32_16x16x32_bf16 a[216:219], v[120:123], a[244:247], a[216:219]\n"
"s_add_u32 m0, s44, 81920\n"
"ds_read_b64_tr_b16 a[252:253], v235 offset:49152\n"
"v_cvt_pk_bf16_f32 v116, v164, v165\n"
"v_smfmac_f32_16x16x64_bf16 v[196:199], a[240:243], v[96:103], v241\n"
"buffer_load_dwordx4 v240, s[20:23], s56 offen nt lds\n"
"v_mfma_f32_16x16x32_bf16 v[8:11], v[96:99], a[244:247], v[8:11]\n"
"s_waitcnt lgkmcnt(4)\n"
"v_cvt_pk_bf16_f32 v118, v176, v177\n"
"v_cvt_pk_bf16_f32 v119, v178, v179\n"
"v_mfma_f32_16x16x32_bf16 v[8:11], v[100:103], a[248:251], v[8:11]\n"
"v_cvt_pk_bf16_f32 v108, v160, v161\n"
"v_and_b32 v245, 0x3f0, v242\n"
"v_smfmac_f32_16x16x64_bf16 v[200:203], a[240:243], v[112:119], v241\n"
"ds_read_b128 v[212:215], v229 offset:0\n"
"v_cvt_pk_bf16_f32 v109, v162, v163\n"
"v_mfma_f32_16x16x32_bf16 v[40:43], v[104:107], a[244:247], v[40:43]\n"
"v_lshlrev_b32_e64 v244, 24, s43\n"
"v_lshrrev_b32 v245, 1, v245\n"
"v_mfma_f32_16x16x32_bf16 v[40:43], v[108:111], a[248:251], v[40:43]\n"
"ds_read_b128 v[208:211], v228 offset:0\n"
"v_sub_u32 v245, v242, v245\n"
"v_smfmac_f32_16x16x64_bf16 v[198:201], a[240:243], v[104:111], v241\n"
"ds_read_b128 v[220:223], v229 offset:1024\n"
"v_add_f32 v196, v196, v197\n"
"v_mfma_f32_16x16x32_bf16 v[72:75], v[112:115], a[244:247], v[72:75]\n"
"ds_read_b64_tr_b16 a[244:245], v234 offset:50176\n"
"v_mul_f32 v244, 0x45800000, v244\n"
"v_mfma_f32_16x16x32_bf16 v[72:75], v[116:119], a[248:251], v[72:75]\n"
"ds_read_b64_tr_b16 a[246:247], v234 offset:54272\n"
"ds_read_b64_tr_b16 a[250:251], v238 offset:54272\n"
"v_mfma_f32_16x16x32_bf16 v[202:205], a[240:243], v[120:123], v[202:205]\n"
"s_waitcnt lgkmcnt(6)\n"
"ds_read_b128 v[216:219], v228 offset:1024\n"
"v_add_f32 v198, v198, v199\n"
"v_mfma_f32_16x16x32_bf16 v[76:79], v[116:119], v[224:227], v[76:79]\n"
"ds_read_b64_tr_b16 a[248:249], v238 offset:50176\n"
"v_add_f32 v254, v196, v198\n"
"v_mfma_f32_16x16x32_bf16 v[76:79], v[112:115], a[252:255], v[76:79]\n"
"v_add_f32 v200, v200, v201\n"
"v_add_f32 v254, v254, v200\n"
"v_mfma_f32_16x16x32_bf16 v[44:47], v[108:111], v[224:227], v[44:47]\n"
"v_add_f32 v244, v244, v202\n"
"v_add_f32 v254, v254, v202\n"
"v_mfma_f32_16x16x32_bf16 v[44:47], v[104:107], a[252:255], v[44:47]\n"
"v_min3_f32 v244, v244, v196, v198\n"
"v_min_f32 v244, v244, v200\n"
"v_mfma_f32_16x16x32_bf16 v[12:15], v[100:103], v[224:227], v[12:15]\n"
"s_add_u32 s40, s56, s52\n"
"v_rcp_f32 v244, v244\n"
"v_mfma_f32_16x16x32_bf16 v[12:15], v[96:99], a[252:255], v[12:15]\n"
"buffer_load_dwordx4 v240, s[20:23], s40 offen offset:1024 nt lds\n"
"v_mfma_f32_16x16x32_bf16 a[220:223], v[120:123], a[252:255], a[220:223]\n"
"s_waitcnt lgkmcnt(2)\n"
"v_fmac_f32 v254, 0x68800000, v244\n"
"v_cmp_nge_f32 vcc, s45, v254\n"
"v_xor_b32 v244, 0x11000, v242\n"
"s_cbranch_vccnz LSLOW_%=\nLBACK0_%=:\ns_mov_b64 vcc, s[64:65]\n"
"v_mfma_f32_16x16x32_bf16 a[232:235], v[120:123], a[244:247], a[232:235]\n"
"v_accvgpr_mov_b32 a6, a54\n"
"v_accvgpr_mov_b32 a8, a56\n"
"v_accvgpr_mov_b32 a9, a57\n"
"v_accvgpr_mov_b32 a14, a62\n"
"v_accvgpr_mov_b32 a20, a68\n"
"v_accvgpr_mov_b32 a42, a90\n"
"v_accvgpr_mov_b32 a44, a92\n"
"v_xor_b32 v148, 0x400, v243\n"
"v_bfe_u32 v149, v242, 8, 2\n"
"v_lshl_add_u32 v149, v149, 4, s43\n"
"v_mfma_f32_16x16x32_bf16 v[24:27], v[96:99], a[244:247], v[24:27]\n"
"s_waitcnt lgkmcnt(0)\n"
"ds_read_b128 v[124:127], v149 offset:0\n"
"v_rcp_f32 v206, v200\n"
"v_accvgpr_mov_b32 a4, a52\n"
"v_accvgpr_mov_b32 a12, a60\n"
"v_accvgpr_mov_b32 a13, a61\n"
"v_accvgpr_mov_b32 a16, a64\n"
"v_accvgpr_mov_b32 a39, a87\n"
"v_accvgpr_mov_b32 a47, a95\n"
"v_xor_b32 v150, 64, v149\n"
"v_mfma_f32_16x16x32_bf16 v[24:27], v[100:103], a[248:251], v[24:27]\n"
"ds_read_b128 v[128:131], v150 offset:0\n"
"v_accvgpr_mov_b32 a0, a48\n"
"v_accvgpr_mov_b32 a1, a49\n"
"v_accvgpr_mov_b32 a5, a53\n"
"v_accvgpr_mov_b32 a10, a58\n"
"v_accvgpr_mov_b32 a17, a65\n"
"v_accvgpr_mov_b32 a40, a88\n"
"v_mfma_f32_16x16x32_bf16 v[56:59], v[104:107], a[244:247], v[56:59]\n"
"ds_read_b128 v[132:135], v149 offset:1024\n"
"v_rcp_f32 v246, v196\n"
"v_mov_b32_dpp v206, v206 row_ror:12 row_mask:0x2 bank_mask:0xf\n"
"v_accvgpr_mov_b32 a2, a50\n"
"v_accvgpr_mov_b32 a22, a70\n"
"v_accvgpr_mov_b32 a23, a71\n"
"v_accvgpr_mov_b32 a36, a84\n"
"v_accvgpr_mov_b32 a38, a86\n"
"v_accvgpr_mov_b32 a45, a93\n"
"v_mfma_f32_16x16x32_bf16 v[56:59], v[108:111], a[248:251], v[56:59]\n"
"ds_read_b128 v[136:139], v150 offset:1024\n"
"v_accvgpr_mov_b32 a33, a81\n"
"v_mfma_f32_16x16x32_bf16 v[88:91], v[112:115], a[244:247], v[88:91]\n"
"ds_read_b64_tr_b16 v[224:225], v239 offset:50176\n"
"v_accvgpr_mov_b32 a3, a51\n"
"v_accvgpr_mov_b32 a7, a55\n"
"v_accvgpr_mov_b32 a11, a59\n"
"v_accvgpr_mov_b32 a19, a67\n"
"v_accvgpr_mov_b32 a21, a69\n"
"v_accvgpr_mov_b32 a24, a72\n"
"v_accvgpr_mov_b32 a27, a75\n"
"v_accvgpr_mov_b32 a30, a78\n"
"v_accvgpr_mov_b32 a46, a94\n"
"v_mfma_f32_16x16x32_bf16 v[88:91], v[116:119], a[248:251], v[88:91]\n"
"ds_read_b128 a[204:207], v148 offset:6144\n"
"v_mov_b32_dpp v206, v206 row_ror:8 row_mask:0x4 bank_mask:0xf\n"
"v_accvgpr_mov_b32 a15, a63\n"
"v_accvgpr_mov_b32 a25, a73\n"
"v_accvgpr_mov_b32 a26, a74\n"
"v_accvgpr_mov_b32 a29, a77\n"
"v_accvgpr_mov_b32 a31, a79\n"
"v_accvgpr_mov_b32 a35, a83\n"
"v_accvgpr_mov_b32 a43, a91\n"
"s_waitcnt lgkmcnt(5)\n"
"v_mfma_f32_16x16x32_bf16 v[180:183], v[124:127], a[0:3], 0\n"
"v_mov_b32 v252, v192\n"
"s_waitcnt lgkmcnt(4)\n"
"v_mfma_f32_16x16x32_bf16 v[180:183], v[128:131], a[4:7], v[180:183]\n"
"s_waitcnt lgkmcnt(3)\n"
"ds_read_b128 a[192:195], v243 offset:4096\n"
"v_accvgpr_mov_b32 a18, a66\n"
"v_mfma_f32_16x16x32_bf16 v[180:183], v[132:135], a[8:11], v[180:183]\n"
"ds_read_b128 a[200:203], v243 offset:6144\n"
"s_waitcnt lgkmcnt(4)\n"
"v_mfma_f32_16x16x32_bf16 v[180:183], v[136:139], a[12:15], v[180:183]\n"
"v_rcp_f32 v247, v198\n"
"v_accvgpr_mov_b32 a28, a76\n"
"v_accvgpr_mov_b32 a34, a82\n"
"v_accvgpr_mov_b32 a41, a89\n"
"v_mfma_f32_16x16x32_bf16 v[184:187], v[124:127], a[16:19], 0\n"
"ds_read_b128 a[196:199], v148 offset:4096\n"
"v_mfma_f32_16x16x32_bf16 v[184:187], v[128:131], a[20:23], v[184:187]\n"
"v_mov_b32_dpp v247, v247 row_ror:12 row_mask:0x2 bank_mask:0xf\n"
"v_mfma_f32_16x16x32_bf16 v[184:187], v[132:135], a[24:27], v[184:187]\n"
"s_add_u32 s40, s56, s53\n"
"v_accvgpr_mov_b32 a32, a80\n"
"v_mfma_f32_16x16x32_bf16 v[184:187], v[136:139], a[28:31], v[184:187]\n"
"buffer_load_dwordx4 v240, s[20:23], s40 offen offset:2048 nt lds\n"
"v_mov_b32_dpp v206, v206 row_ror:4 row_mask:0x8 bank_mask:0xf\n"
"v_accvgpr_mov_b32 a37, a85\n"
"v_sub_f32 v180, s74, v180\n"
"v_sub_f32 v183, s74, v183\n"
"v_mfma_f32_16x16x32_bf16 v[188:191], v[124:127], a[32:35], 0\n"
"v_mov_b32_dpp v246, v246 row_ror:12 row_mask:0x2 bank_mask:0xf\n"
"v_mfma_f32_16x16x32_bf16 v[188:191], v[128:131], a[36:39], v[188:191]\n"
"v_mfma_f32_16x16x32_bf16 v[188:191], v[132:135], a[40:43], v[188:191]\n"
"v_mov_b32_dpp v246, v246 row_ror:8 row_mask:0x4 bank_mask:0xf\n"
"v_sub_f32 v182, s74, v182\n"
"v_mfma_f32_16x16x32_bf16 v[188:191], v[136:139], a[44:47], v[188:191]\n"
"ds_write_b128 v244, a[220:223] offset:33792\n"
"v_mov_b32_dpp v247, v247 row_ror:8 row_mask:0x4 bank_mask:0xf\n"
"s_waitcnt lgkmcnt(2)\n"
"v_mfma_f32_16x16x32_bf16 v[192:195], v[124:127], a[192:195], 0\n"
"v_sub_f32 v181, s74, v181\n"
"s_waitcnt lgkmcnt(1)\n"
"v_mfma_f32_16x16x32_bf16 v[192:195], v[128:131], a[196:199], v[192:195]\n"
"v_mfma_f32_16x16x32_bf16 v[192:195], v[132:135], a[200:203], v[192:195]\n"
"ds_read_b64_tr_b16 v[226:227], v239 offset:54272\n"
"ds_read_b64_tr_b16 a[246:247], v232 offset:53248\n"
"v_mfma_f32_16x16x32_bf16 v[192:195], v[136:139], a[204:207], v[192:195]\n"
"v_mov_b32_dpp v246, v246 row_ror:4 row_mask:0x8 bank_mask:0xf\n"
"v_mov_b32_dpp v247, v247 row_ror:4 row_mask:0x8 bank_mask:0xf\n"
"v_sub_f32 v184, s74, v184\n"
"v_sub_f32 v187, s74, v187\n"
"v_sub_f32 v190, s74, v190\n"
"v_mfma_f32_16x16x32_bf16 v[152:155], v[208:211], a[0:3], v[180:183]\n"
"v_mov_b32 v253, v202\n"
"v_sub_f32 v185, s74, v185\n"
"v_mfma_f32_16x16x32_bf16 v[152:155], v[212:215], a[4:7], v[152:155]\n"
"v_sub_f32 v186, s74, v186\n"
"v_mfma_f32_16x16x32_bf16 v[152:155], v[216:219], a[8:11], v[152:155]\n"
"s_add_u32 s40, s56, s54\n"
"ds_read_b64_tr_b16 a[254:255], v235 offset:54272\n"
"v_sub_f32 v189, s74, v189\n"
"v_mfma_f32_16x16x32_bf16 v[152:155], v[220:223], a[12:15], v[152:155]\n"
"buffer_load_dwordx4 v240, s[20:23], s40 offen offset:3072 nt lds\n"
"v_mul_f32_dpp v9, v246, v9 row_newbcast:1 row_mask:0xf bank_mask:0xf\n"
"v_mul_f32_dpp v12, v246, v12 row_newbcast:0 row_mask:0xf bank_mask:0xf\n"
"v_sub_f32 v188, s74, v188\n"
"v_sub_f32 v191, s74, v191\n"
"v_mfma_f32_16x16x32_bf16 v[156:159], v[216:219], a[24:27], v[184:187]\n"
"s_add_u32 s55, s55, s59\n"
"v_mfma_f32_16x16x32_bf16 v[156:159], v[220:223], a[28:31], v[156:159]\n"
"s_add_u32 s41, s55, s57\n"
"v_mfma_f32_16x16x32_bf16 v[156:159], v[208:211], a[16:19], v[156:159]\n"
"s_and_b32 s41, s41, s58\n"
"v_sub_f32 v192, s74, v192\n"
"v_mfma_f32_16x16x32_bf16 v[156:159], v[212:215], a[20:23], v[156:159]\n"
"s_andn2_b32 s56, s55, s58\n"
"ds_read_b64_tr_b16 a[250:251], v236 offset:53248\n"
"v_xor_b32 v250, 0x1000, v245\n"
"v_mul_f32_dpp v41, v247, v41 row_newbcast:1 row_mask:0xf bank_mask:0xf\n"
"v_mul_f32_dpp v46, v247, v46 row_newbcast:2 row_mask:0xf bank_mask:0xf\n"
"v_sub_f32 v195, s74, v195\n"
"v_exp_f32 v153, v153\n"
"v_mfma_f32_16x16x32_bf16 v[160:163], v[208:211], a[32:35], v[188:191]\n"
"s_or_b32 s56, s56, s41\n"
"v_mul_f32_dpp v43, v247, v43 row_newbcast:3 row_mask:0xf bank_mask:0xf\n"
"v_sub_f32 v193, s74, v193\n"
"v_mfma_f32_16x16x32_bf16 v[160:163], v[212:215], a[36:39], v[160:163]\n"
"v_mfma_f32_16x16x32_bf16 v[160:163], v[216:219], a[40:43], v[160:163]\n"
"v_sub_f32 v194, s74, v194\n"
"v_mfma_f32_16x16x32_bf16 v[160:163], v[220:223], a[44:47], v[160:163]\n"
"ds_read_b64_tr_b16 a[252:253], v235 offset:50176\n"
"v_exp_f32 v152, v152\n"
"v_exp_f32 v157, v157\n"
"v_exp_f32 v158, v158\n"
"v_mfma_f32_16x16x32_bf16 v[164:167], v[216:219], a[200:203], v[192:195]\n"
"ds_read_b128 v[216:219], v228 offset:5120\n"
"v_mfma_f32_16x16x32_bf16 v[164:167], v[220:223], a[204:207], v[164:167]\n"
"v_mul_f32_dpp v57, v247, v57 row_newbcast:1 row_mask:0xf bank_mask:0xf\n"
"v_mfma_f32_16x16x32_bf16 v[164:167], v[208:211], a[192:195], v[164:167]\n"
"ds_read_b128 v[208:211], v228 offset:4096\n"
"v_mfma_f32_16x16x32_bf16 v[164:167], v[212:215], a[196:199], v[164:167]\n"
"ds_read_b128 v[220:223], v229 offset:5120\n"
"v_mul_f32_dpp v10, v246, v10 row_newbcast:2 row_mask:0xf bank_mask:0xf\n"
"v_exp_f32 v154, v154\n"
"v_exp_f32 v155, v155\n"
"v_mul_f32_dpp v27, v246, v27 row_newbcast:3 row_mask:0xf bank_mask:0xf\n"
"v_mul_f32_dpp v58, v247, v58 row_newbcast:2 row_mask:0xf bank_mask:0xf\n"
"s_branch LTC_%=\n"
"LTH1_%=:\n"
"v_mfma_f32_16x16x32_bf16 a[224:227], v[124:127], a[244:247], a[224:227]\n"
"v_exp_f32 v170, v170\n"
"v_mfma_f32_16x16x32_bf16 v[16:19], v[128:131], a[244:247], v[16:19]\n"
"s_waitcnt vmcnt(8)\n"
"v_exp_f32 v168, v168\n"
"v_mfma_f32_16x16x32_bf16 v[16:19], v[132:135], a[248:251], v[16:19]\n"
"s_barrier\n"
"v_mfma_f32_16x16x32_bf16 v[48:51], v[136:139], a[244:247], v[48:51]\n"
"v_exp_f32 v173, v173\n"
"v_mfma_f32_16x16x32_bf16 v[48:51], v[140:143], a[248:251], v[48:51]\n"
"v_exp_f32 v171, v171\n"
"v_mfma_f32_16x16x32_bf16 v[80:83], v[144:147], a[244:247], v[80:83]\n"
"ds_read_b64_tr_b16 a[244:245], v234 offset:49152\n"
"ds_read_b64_tr_b16 a[246:247], v234 offset:53248\n"
"v_mfma_f32_16x16x32_bf16 v[80:83], v[148:151], a[248:251], v[80:83]\n"
"v_exp_f32 v179, v179\n"
"v_mfma_f32_16x16x32_bf16 v[84:87], v[148:151], v[224:227], v[84:87]\n"
"ds_read_b64_tr_b16 a[250:251], v238 offset:53248\n"
"v_cvt_pk_bf16_f32 v100, v156, v157\n"
"v_mfma_f32_16x16x32_bf16 v[84:87], v[144:147], a[252:255], v[84:87]\n"
"v_exp_f32 v169, v169\n"
"v_exp_f32 v172, v172\n"
"v_exp_f32 v175, v175\n"
"v_exp_f32 v176, v176\n"
"v_exp_f32 v178, v178\n"
"v_mfma_f32_16x16x32_bf16 v[52:55], v[140:143], v[224:227], v[52:55]\n"
"ds_read_b64_tr_b16 a[248:249], v238 offset:49152\n"
"v_cvt_pk_bf16_f32 v117, v166, v167\n"
"v_mfma_f32_16x16x32_bf16 v[52:55], v[136:139], a[252:255], v[52:55]\n"
"v_exp_f32 v174, v174\n"
"v_mfma_f32_16x16x32_bf16 v[20:23], v[132:135], v[224:227], v[20:23]\n"
"ds_read_b64_tr_b16 v[224:225], v239 offset:49152\n"
"ds_read_b64_tr_b16 v[226:227], v239 offset:53248\n"
"v_mfma_f32_16x16x32_bf16 v[20:23], v[128:131], a[252:255], v[20:23]\n"
"v_cvt_pk_bf16_f32 v102, v168, v169\n"
"v_cvt_pk_bf16_f32 v103, v170, v171\n"
"v_cvt_pk_bf16_f32 v111, v174, v175\n"
"v_exp_f32 v177, v177\n"
"v_mfma_f32_16x16x32_bf16 a[228:231], v[124:127], a[252:255], a[228:231]\n"
"s_waitcnt lgkmcnt(4)\n"
"ds_read_b64_tr_b16 a[254:255], v235 offset:53248\n"
"v_cvt_pk_bf16_f32 v110, v172, v173\n"
"v_mfma_f32_16x16x32_bf16 a[216:219], v[120:123], a[244:247], a[216:219]\n"
"s_add_u32 m0, s44, 81920\n"
"ds_read_b64_tr_b16 a[252:253], v235 offset:49152\n"
"v_cvt_pk_bf16_f32 v116, v164, v165\n"
"v_smfmac_f32_16x16x64_bf16 v[196:199], a[240:243], v[96:103], v241\n"
"buffer_load_dwordx4 v240, s[20:23], s56 offen nt lds\n"
"v_mfma_f32_16x16x32_bf16 v[8:11], v[96:99], a[244:247], v[8:11]\n"
"s_waitcnt lgkmcnt(4)\n"
"v_cvt_pk_bf16_f32 v118, v176, v177\n"
"v_cvt_pk_bf16_f32 v119, v178, v179\n"
"v_mfma_f32_16x16x32_bf16 v[8:11], v[100:103], a[248:251], v[8:11]\n"
"v_cvt_pk_bf16_f32 v108, v160, v161\n"
"v_and_b32 v245, 0x3f0, v242\n"
"v_smfmac_f32_16x16x64_bf16 v[200:203], a[240:243], v[112:119], v241\n"
"ds_read_b128 v[212:215], v229 offset:0\n"
"v_cvt_pk_bf16_f32 v109, v162, v163\n"
"v_mfma_f32_16x16x32_bf16 v[40:43], v[104:107], a[244:247], v[40:43]\n"
"v_lshlrev_b32_e64 v244, 24, s43\n"
"v_lshrrev_b32 v245, 1, v245\n"
"v_mfma_f32_16x16x32_bf16 v[40:43], v[108:111], a[248:251], v[40:43]\n"
"ds_read_b128 v[208:211], v228 offset:0\n"
"v_sub_u32 v245, v242, v245\n"
"v_smfmac_f32_16x16x64_bf16 v[198:201], a[240:243], v[104:111], v241\n"
"ds_read_b128 v[220:223], v229 offset:1024\n"
"v_add_f32 v196, v196, v197\n"
"v_mfma_f32_16x16x32_bf16 v[72:75], v[112:115], a[244:247], v[72:75]\n"
"ds_read_b64_tr_b16 a[244:245], v234 offset:50176\n"
"v_mul_f32 v244, 0x45800000, v244\n"
"v_mfma_f32_16x16x32_bf16 v[72:75], v[116:119], a[248:251], v[72:75]\n"
"ds_read_b64_tr_b16 a[246:247], v234 offset:54272\n"
"ds_read_b64_tr_b16 a[250:251], v238 offset:54272\n"
"v_mfma_f32_16x16x32_bf16 v[202:205], a[240:243], v[120:123], v[202:205]\n"
"s_waitcnt lgkmcnt(6)\n"
"ds_read_b128 v[216:219], v228 offset:1024\n"
"v_add_f32 v198, v198, v199\n"
"v_mfma_f32_16x16x32_bf16 v[76:79], v[116:119], v[224:227], v[76:79]\n"
"ds_read_b64_tr_b16 a[248:249], v238 offset:50176\n"
"v_add_f32 v254, v196, v198\n"
"v_mfma_f32_16x16x32_bf16 v[76:79], v[112:115], a[252:255], v[76:79]\n"
"v_add_f32 v200, v200, v201\n"
"v_add_f32 v254, v254, v200\n"
"v_mfma_f32_16x16x32_bf16 v[44:47], v[108:111], v[224:227], v[44:47]\n"
"v_add_f32 v244, v244, v202\n"
"v_add_f32 v254, v254, v202\n"
"v_mfma_f32_16x16x32_bf16 v[44:47], v[104:107], a[252:255], v[44:47]\n"
"v_min3_f32 v244, v244, v196, v198\n"
"v_min_f32 v244, v244, v200\n"
"v_mfma_f32_16x16x32_bf16 v[12:15], v[100:103], v[224:227], v[12:15]\n"
"s_add_u32 s40, s56, s52\n"
"v_rcp_f32 v244, v244\n"
"v_mfma_f32_16x16x32_bf16 v[12:15], v[96:99], a[252:255], v[12:15]\n"
"buffer_load_dwordx4 v240, s[20:23], s40 offen offset:1024 nt lds\n"
"v_mfma_f32_16x16x32_bf16 a[220:223], v[120:123], a[252:255], a[220:223]\n"
"s_waitcnt lgkmcnt(2)\n"
"v_fmac_f32 v254, 0x68800000, v244\n"
"v_cmp_nge_f32 vcc, s45, v254\n"
"v_xor_b32 v244, 0x11000, v242\n"
"s_cbranch_vccnz LSLOW_%=\nLBACK1_%=:\ns_mov_b64 vcc, s[64:65]\n"
"v_mfma_f32_16x16x32_bf16 a[232:235], v[120:123], a[244:247], a[232:235]\n"
"v_accvgpr_mov_b32 a6, a102\n"
"v_accvgpr_mov_b32 a8, a104\n"
"v_accvgpr_mov_b32 a9, a105\n"
"v_accvgpr_mov_b32 a14, a110\n"
"v_accvgpr_mov_b32 a20, a116\n"
"v_accvgpr_mov_b32 a42, a138\n"
"v_accvgpr_mov_b32 a44, a140\n"
"v_xor_b32 v148, 0x400, v243\n"
"v_bfe_u32 v149, v242, 8, 2\n"
"v_lshl_add_u32 v149, v149, 4, s43\n"
"v_mfma_f32_16x16x32_bf16 v[24:27], v[96:99], a[244:247], v[24:27]\n"
"s_waitcnt lgkmcnt(0)\n"
"ds_read_b128 v[124:127], v149 offset:0\n"
"v_rcp_f32 v206, v200\n"
"v_accvgpr_mov_b32 a4, a100\n"
"v_accvgpr_mov_b32 a12, a108\n"
"v_accvgpr_mov_b32 a13, a109\n"
"v_accvgpr_mov_b32 a16, a112\n"
"v_accvgpr_mov_b32 a39, a135\n"
"v_accvgpr_mov_b32 a47, a143\n"
"v_xor_b32 v150, 64, v149\n"
"v_mfma_f32_16x16x32_bf16 v[24:27], v[100:103], a[248:251], v[24:27]\n"
"ds_read_b128 v[128:131], v150 offset:0\n"
"v_accvgpr_mov_b32 a0, a96\n"
"v_accvgpr_mov_b32 a1, a97\n"
"v_accvgpr_mov_b32 a5, a101\n"
"v_accvgpr_mov_b32 a10, a106\n"
"v_accvgpr_mov_b32 a17, a113\n"
"v_accvgpr_mov_b32 a40, a136\n"
"v_mfma_f32_16x16x32_bf16 v[56:59], v[104:107], a[244:247], v[56:59]\n"
"ds_read_b128 v[132:135], v149 offset:1024\n"
"v_rcp_f32 v246, v196\n"
"v_mov_b32_dpp v206, v206 row_ror:12 row_mask:0x2 bank_mask:0xf\n"
"v_accvgpr_mov_b32 a2, a98\n"
"v_accvgpr_mov_b32 a22, a118\n"
"v_accvgpr_mov_b32 a23, a119\n"
"v_accvgpr_mov_b32 a36, a132\n"
"v_accvgpr_mov_b32 a38, a134\n"
"v_accvgpr_mov_b32 a45, a141\n"
"v_mfma_f32_16x16x32_bf16 v[56:59], v[108:111], a[248:251], v[56:59]\n"
"ds_read_b128 v[136:139], v150 offset:1024\n"
"v_accvgpr_mov_b32 a33, a129\n"
"v_mfma_f32_16x16x32_bf16 v[88:91], v[112:115], a[244:247], v[88:91]\n"
"ds_read_b64_tr_b16 v[224:225], v239 offset:50176\n"
"v_accvgpr_mov_b32 a3, a99\n"
"v_accvgpr_mov_b32 a7, a103\n"
"v_accvgpr_mov_b32 a11, a107\n"
"v_accvgpr_mov_b32 a19, a115\n"
"v_accvgpr_mov_b32 a21, a117\n"
"v_accvgpr_mov_b32 a24, a120\n"
"v_accvgpr_mov_b32 a27, a123\n"
"v_accvgpr_mov_b32 a30, a126\n"
"v_accvgpr_mov_b32 a46, a142\n"
"v_mfma_f32_16x16x32_bf16 v[88:91], v[116:119], a[248:251], v[88:91]\n"
"s_waitcnt lgkmcnt(4)\n"
"ds_read_b128 a[204:207], v148 offset:6144\n"
"v_mov_b32_dpp v206, v206 row_ror:8 row_mask:0x4 bank_mask:0xf\n"
"v_accvgpr_mov_b32 a15, a111\n"
"v_accvgpr_mov_b32 a25, a121\n"
"v_accvgpr_mov_b32 a26, a122\n"
"v_accvgpr_mov_b32 a29, a125\n"
"v_accvgpr_mov_b32 a31, a127\n"
"v_accvgpr_mov_b32 a35, a131\n"
"v_accvgpr_mov_b32 a43, a139\n"
"v_accvgpr_mov_b32 a112, a64\n"
"v_mfma_f32_16x16x32_bf16 v[180:183], v[124:127], a[0:3], 0\n"
"s_waitcnt lgkmcnt(4)\n"
"v_mov_b32 v252, v192\n"
"v_accvgpr_mov_b32 a126, a78\n"
"v_mfma_f32_16x16x32_bf16 v[180:183], v[128:131], a[4:7], v[180:183]\n"
"s_waitcnt lgkmcnt(3)\n"
"ds_read_b128 a[192:195], v243 offset:4096\n"
"v_accvgpr_mov_b32 a18, a114\n"
"v_mfma_f32_16x16x32_bf16 v[180:183], v[132:135], a[8:11], v[180:183]\n"
"s_waitcnt lgkmcnt(3)\n"
"ds_read_b128 a[200:203], v243 offset:6144\n"
"v_accvgpr_mov_b32 a135, a87\n"
"v_mfma_f32_16x16x32_bf16 v[180:183], v[136:139], a[12:15], v[180:183]\n"
"v_rcp_f32 v247, v198\n"
"v_accvgpr_mov_b32 a28, a124\n"
"v_accvgpr_mov_b32 a34, a130\n"
"v_accvgpr_mov_b32 a41, a137\n"
"v_accvgpr_mov_b32 a103, a55\n"
"v_accvgpr_mov_b32 a119, a71\n"
"v_accvgpr_mov_b32 a108, a60\n"
"v_accvgpr_mov_b32 a130, a82\n"
"v_mfma_f32_16x16x32_bf16 v[184:187], v[124:127], a[16:19], 0\n"
"ds_read_b128 a[196:199], v148 offset:4096\n"
"v_accvgpr_mov_b32 a110, a62\n"
"v_mfma_f32_16x16x32_bf16 v[184:187], v[128:131], a[20:23], v[184:187]\n"
"v_mov_b32_dpp v247, v247 row_ror:12 row_mask:0x2 bank_mask:0xf\n"
"v_accvgpr_mov_b32 a104, a56\n"
"v_mfma_f32_16x16x32_bf16 v[184:187], v[132:135], a[24:27], v[184:187]\n"
"s_add_u32 s40, s56, s53\n"
"v_accvgpr_mov_b32 a32, a128\n"
"v_accvgpr_mov_b32 a109, a61\n"
"v_mfma_f32_16x16x32_bf16 v[184:187], v[136:139], a[28:31], v[184:187]\n"
"buffer_load_dwordx4 v240, s[20:23], s40 offen offset:2048 nt lds\n"
"v_mov_b32_dpp v206, v206 row_ror:4 row_mask:0x8 bank_mask:0xf\n"
"v_accvgpr_mov_b32 a37, a133\n"
"v_accvgpr_mov_b32 a101, a53\n"
"v_accvgpr_mov_b32 a102, a54\n"
"v_accvgpr_mov_b32 a133, a85\n"
"v_accvgpr_mov_b32 a105, a57\n"
"v_sub_f32 v180, s74, v180\n"
"v_sub_f32 v183, s74, v183\n"
"v_mfma_f32_16x16x32_bf16 v[188:191], v[124:127], a[32:35], 0\n"
"v_mov_b32_dpp v246, v246 row_ror:12 row_mask:0x2 bank_mask:0xf\n"
"v_accvgpr_mov_b32 a132, a84\n"
"v_mfma_f32_16x16x32_bf16 v[188:191], v[128:131], a[36:39], v[188:191]\n"
"v_accvgpr_mov_b32 a114, a66\n"
"v_accvgpr_mov_b32 a131, a83\n"
"v_mfma_f32_16x16x32_bf16 v[188:191], v[132:135], a[40:43], v[188:191]\n"
"v_mov_b32_dpp v246, v246 row_ror:8 row_mask:0x4 bank_mask:0xf\n"
"v_sub_f32 v182, s74, v182\n"
"v_mfma_f32_16x16x32_bf16 v[188:191], v[136:139], a[44:47], v[188:191]\n"
"s_waitcnt lgkmcnt(1)\n"
"ds_write_b128 v244, a[220:223] offset:33792\n"
"v_mov_b32_dpp v247, v247 row_ror:8 row_mask:0x4 bank_mask:0xf\n"
"v_accvgpr_mov_b32 a115, a67\n"
"v_accvgpr_mov_b32 a129, a81\n"
"v_accvgpr_mov_b32 a106, a58\n"
"v_mfma_f32_16x16x32_bf16 v[192:195], v[124:127], a[192:195], 0\n"
"s_waitcnt lgkmcnt(1)\n"
"v_accvgpr_mov_b32 a116, a68\n"
"v_sub_f32 v181, s74, v181\n"
"v_mfma_f32_16x16x32_bf16 v[192:195], v[128:131], a[196:199], v[192:195]\n"
"v_accvgpr_mov_b32 a125, a77\n"
"v_accvgpr_mov_b32 a96, a48\n"
"v_mfma_f32_16x16x32_bf16 v[192:195], v[132:135], a[200:203], v[192:195]\n"
"ds_read_b64_tr_b16 v[226:227], v239 offset:54272\n"
"ds_read_b64_tr_b16 a[246:247], v232 offset:53248\n"
"v_mfma_f32_16x16x32_bf16 v[192:195], v[136:139], a[204:207], v[192:195]\n"
"v_mov_b32_dpp v246, v246 row_ror:4 row_mask:0x8 bank_mask:0xf\n"
"v_mov_b32_dpp v247, v247 row_ror:4 row_mask:0x8 bank_mask:0xf\n"
"v_accvgpr_mov_b32 a100, a52\n"
"v_accvgpr_mov_b32 a117, a69\n"
"v_accvgpr_mov_b32 a127, a79\n"
"v_accvgpr_mov_b32 a128, a80\n"
"v_sub_f32 v184, s74, v184\n"
"v_sub_f32 v187, s74, v187\n"
"v_sub_f32 v190, s74, v190\n"
"v_mfma_f32_16x16x32_bf16 v[152:155], v[208:211], a[0:3], v[180:183]\n"
"v_mov_b32 v253, v202\n"
"v_sub_f32 v185, s74, v185\n"
"v_mfma_f32_16x16x32_bf16 v[152:155], v[212:215], a[4:7], v[152:155]\n"
"v_accvgpr_mov_b32 a138, a90\n"
"v_sub_f32 v186, s74, v186\n"
"v_mfma_f32_16x16x32_bf16 v[152:155], v[216:219], a[8:11], v[152:155]\n"
"s_add_u32 s40, s56, s54\n"
"ds_read_b64_tr_b16 a[254:255], v235 offset:54272\n"
"v_sub_f32 v189, s74, v189\n"
"v_mfma_f32_16x16x32_bf16 v[152:155], v[220:223], a[12:15], v[152:155]\n"
"buffer_load_dwordx4 v240, s[20:23], s40 offen offset:3072 nt lds\n"
"v_accvgpr_mov_b32 a142, a94\n"
"v_accvgpr_mov_b32 a143, a95\n"
"v_accvgpr_mov_b32 a120, a72\n"
"v_mul_f32_dpp v9, v246, v9 row_newbcast:1 row_mask:0xf bank_mask:0xf\n"
"v_mul_f32_dpp v12, v246, v12 row_newbcast:0 row_mask:0xf bank_mask:0xf\n"
"v_sub_f32 v188, s74, v188\n"
"v_sub_f32 v191, s74, v191\n"
"v_mfma_f32_16x16x32_bf16 v[156:159], v[216:219], a[24:27], v[184:187]\n"
"s_add_u32 s55, s55, s59\n"
"v_accvgpr_mov_b32 a111, a63\n"
"v_accvgpr_mov_b32 a107, a59\n"
"v_mfma_f32_16x16x32_bf16 v[156:159], v[220:223], a[28:31], v[156:159]\n"
"s_add_u32 s41, s55, s57\n"
"v_accvgpr_mov_b32 a97, a49\n"
"v_accvgpr_mov_b32 a122, a74\n"
"v_mfma_f32_16x16x32_bf16 v[156:159], v[208:211], a[16:19], v[156:159]\n"
"s_and_b32 s41, s41, s58\n"
"v_accvgpr_mov_b32 a99, a51\n"
"v_sub_f32 v192, s74, v192\n"
"v_mfma_f32_16x16x32_bf16 v[156:159], v[212:215], a[20:23], v[156:159]\n"
"s_andn2_b32 s56, s55, s58\n"
"ds_read_b64_tr_b16 a[250:251], v236 offset:53248\n"
"v_xor_b32 v250, 0x1000, v245\n"
"v_accvgpr_mov_b32 a98, a50\n"
"v_accvgpr_mov_b32 a113, a65\n"
"v_accvgpr_mov_b32 a139, a91\n"
"v_mul_f32_dpp v41, v247, v41 row_newbcast:1 row_mask:0xf bank_mask:0xf\n"
"v_mul_f32_dpp v46, v247, v46 row_newbcast:2 row_mask:0xf bank_mask:0xf\n"
"v_sub_f32 v195, s74, v195\n"
"v_exp_f32 v153, v153\n"
"v_mfma_f32_16x16x32_bf16 v[160:163], v[208:211], a[32:35], v[188:191]\n"
"s_or_b32 s56, s56, s41\n"
"v_mul_f32_dpp v43, v247, v43 row_newbcast:3 row_mask:0xf bank_mask:0xf\n"
"v_sub_f32 v193, s74, v193\n"
"v_mfma_f32_16x16x32_bf16 v[160:163], v[212:215], a[36:39], v[160:163]\n"
"v_accvgpr_mov_b32 a123, a75\n"
"v_accvgpr_mov_b32 a137, a89\n"
"v_mfma_f32_16x16x32_bf16 v[160:163], v[216:219], a[40:43], v[160:163]\n"
"v_accvgpr_mov_b32 a134, a86\n"
"v_sub_f32 v194, s74, v194\n"
"v_mfma_f32_16x16x32_bf16 v[160:163], v[220:223], a[44:47], v[160:163]\n"
"ds_read_b64_tr_b16 a[252:253], v235 offset:50176\n"
"v_accvgpr_mov_b32 a124, a76\n"
"v_accvgpr_mov_b32 a136, a88\n"
"v_exp_f32 v152, v152\n"
"v_exp_f32 v157, v157\n"
"v_exp_f32 v158, v158\n"
"v_mfma_f32_16x16x32_bf16 v[164:167], v[216:219], a[200:203], v[192:195]\n"
"ds_read_b128 v[216:219], v228 offset:5120\n"
"v_accvgpr_mov_b32 a118, a70\n"
"v_mfma_f32_16x16x32_bf16 v[164:167], v[220:223], a[204:207], v[164:167]\n"
"v_accvgpr_mov_b32 a140, a92\n"
"v_mul_f32_dpp v57, v247, v57 row_newbcast:1 row_mask:0xf bank_mask:0xf\n"
"v_mfma_f32_16x16x32_bf16 v[164:167], v[208:211], a[192:195], v[164:167]\n"
"ds_read_b128 v[208:211], v228 offset:4096\n"
"v_accvgpr_mov_b32 a141, a93\n"
"v_mfma_f32_16x16x32_bf16 v[164:167], v[212:215], a[196:199], v[164:167]\n"
"s_waitcnt lgkmcnt(3)\n"
"ds_read_b128 v[220:223], v229 offset:5120\n"
"v_accvgpr_mov_b32 a121, a73\n"
"v_mul_f32_dpp v10, v246, v10 row_newbcast:2 row_mask:0xf bank_mask:0xf\n"
"v_exp_f32 v154, v154\n"
"v_exp_f32 v155, v155\n"
"v_mul_f32_dpp v27, v246, v27 row_newbcast:3 row_mask:0xf bank_mask:0xf\n"
"v_mul_f32_dpp v58, v247, v58 row_newbcast:2 row_mask:0xf bank_mask:0xf\n"
"s_branch LTC_%=\n"
"LTH2_%=:\n"
"v_mfma_f32_16x16x32_bf16 a[224:227], v[124:127], a[244:247], a[224:227]\n"
"v_exp_f32 v170, v170\n"
"v_mfma_f32_16x16x32_bf16 v[16:19], v[128:131], a[244:247], v[16:19]\n"
"s_waitcnt vmcnt(8)\n"
"v_exp_f32 v168, v168\n"
"v_mfma_f32_16x16x32_bf16 v[16:19], v[132:135], a[248:251], v[16:19]\n"
"s_barrier\n"
"v_mfma_f32_16x16x32_bf16 v[48:51], v[136:139], a[244:247], v[48:51]\n"
"v_exp_f32 v173, v173\n"
"v_mfma_f32_16x16x32_bf16 v[48:51], v[140:143], a[248:251], v[48:51]\n"
"v_exp_f32 v171, v171\n"
"v_mfma_f32_16x16x32_bf16 v[80:83], v[144:147], a[244:247], v[80:83]\n"
"ds_read_b64_tr_b16 a[244:245], v234 offset:49152\n"
"ds_read_b64_tr_b16 a[246:247], v234 offset:53248\n"
"v_mfma_f32_16x16x32_bf16 v[80:83], v[148:151], a[248:251], v[80:83]\n"
"v_exp_f32 v179, v179\n"
"v_mfma_f32_16x16x32_bf16 v[84:87], v[148:151], v[224:227], v[84:87]\n"
"ds_read_b64_tr_b16 a[250:251], v238 offset:53248\n"
"v_cvt_pk_bf16_f32 v100, v156, v157\n"
"v_mfma_f32_16x16x32_bf16 v[84:87], v[144:147], a[252:255], v[84:87]\n"
"v_exp_f32 v169, v169\n"
"v_exp_f32 v172, v172\n"
"v_exp_f32 v175, v175\n"
"v_exp_f32 v176, v176\n"
"v_exp_f32 v178, v178\n"
"v_mfma_f32_16x16x32_bf16 v[52:55], v[140:143], v[224:227], v[52:55]\n"
"ds_read_b64_tr_b16 a[248:249], v238 offset:49152\n"
"v_cvt_pk_bf16_f32 v117, v166, v167\n"
"v_mfma_f32_16x16x32_bf16 v[52:55], v[136:139], a[252:255], v[52:55]\n"
"v_exp_f32 v174, v174\n"
"v_mfma_f32_16x16x32_bf16 v[20:23], v[132:135], v[224:227], v[20:23]\n"
"ds_read_b64_tr_b16 v[224:225], v239 offset:49152\n"
"ds_read_b64_tr_b16 v[226:227], v239 offset:53248\n"
"v_mfma_f32_16x16x32_bf16 v[20:23], v[128:131], a[252:255], v[20:23]\n"
"v_cvt_pk_bf16_f32 v102, v168, v169\n"
"v_cvt_pk_bf16_f32 v103, v170, v171\n"
"v_cvt_pk_bf16_f32 v111, v174, v175\n"
"v_exp_f32 v177, v177\n"
"v_mfma_f32_16x16x32_bf16 a[228:231], v[124:127], a[252:255], a[228:231]\n"
"s_waitcnt lgkmcnt(4)\n"
"ds_read_b64_tr_b16 a[254:255], v235 offset:53248\n"
"v_cvt_pk_bf16_f32 v110, v172, v173\n"
"v_mfma_f32_16x16x32_bf16 a[216:219], v[120:123], a[244:247], a[216:219]\n"
"s_add_u32 m0, s44, 81920\n"
"ds_read_b64_tr_b16 a[252:253], v235 offset:49152\n"
"v_cvt_pk_bf16_f32 v116, v164, v165\n"
"v_smfmac_f32_16x16x64_bf16 v[196:199], a[240:243], v[96:103], v241\n"
"buffer_load_dwordx4 v240, s[20:23], s56 offen nt lds\n"
"v_mfma_f32_16x16x32_bf16 v[8:11], v[96:99], a[244:247], v[8:11]\n"
"s_waitcnt lgkmcnt(4)\n"
"v_cvt_pk_bf16_f32 v118, v176, v177\n"
"v_cvt_pk_bf16_f32 v119, v178, v179\n"
"v_mfma_f32_16x16x32_bf16 v[8:11], v[100:103], a[248:251], v[8:11]\n"
"v_cvt_pk_bf16_f32 v108, v160, v161\n"
"v_and_b32 v245, 0x3f0, v242\n"
"v_smfmac_f32_16x16x64_bf16 v[200:203], a[240:243], v[112:119], v241\n"
"ds_read_b128 v[212:215], v229 offset:0\n"
"v_cvt_pk_bf16_f32 v109, v162, v163\n"
"v_mfma_f32_16x16x32_bf16 v[40:43], v[104:107], a[244:247], v[40:43]\n"
"v_lshlrev_b32_e64 v244, 24, s43\n"
"v_lshrrev_b32 v245, 1, v245\n"
"v_mfma_f32_16x16x32_bf16 v[40:43], v[108:111], a[248:251], v[40:43]\n"
"ds_read_b128 v[208:211], v228 offset:0\n"
"v_sub_u32 v245, v242, v245\n"
"v_smfmac_f32_16x16x64_bf16 v[198:201], a[240:243], v[104:111], v241\n"
"ds_read_b128 v[220:223], v229 offset:1024\n"
"v_add_f32 v196, v196, v197\n"
"v_mfma_f32_16x16x32_bf16 v[72:75], v[112:115], a[244:247], v[72:75]\n"
"ds_read_b64_tr_b16 a[244:245], v234 offset:50176\n"
"v_mul_f32 v244, 0x45800000, v244\n"
"v_mfma_f32_16x16x32_bf16 v[72:75], v[116:119], a[248:251], v[72:75]\n"
"ds_read_b64_tr_b16 a[246:247], v234 offset:54272\n"
"ds_read_b64_tr_b16 a[250:251], v238 offset:54272\n"
"v_mfma_f32_16x16x32_bf16 v[202:205], a[240:243], v[120:123], v[202:205]\n"
"s_waitcnt lgkmcnt(6)\n"
"ds_read_b128 v[216:219], v228 offset:1024\n"
"v_add_f32 v198, v198, v199\n"
"v_mfma_f32_16x16x32_bf16 v[76:79], v[116:119], v[224:227], v[76:79]\n"
"ds_read_b64_tr_b16 a[248:249], v238 offset:50176\n"
"v_add_f32 v254, v196, v198\n"
"v_mfma_f32_16x16x32_bf16 v[76:79], v[112:115], a[252:255], v[76:79]\n"
"v_add_f32 v200, v200, v201\n"
"v_add_f32 v254, v254, v200\n"
"v_mfma_f32_16x16x32_bf16 v[44:47], v[108:111], v[224:227], v[44:47]\n"
"v_add_f32 v244, v244, v202\n"
"v_add_f32 v254, v254, v202\n"
"v_mfma_f32_16x16x32_bf16 v[44:47], v[104:107], a[252:255], v[44:47]\n"
"v_min3_f32 v244, v244, v196, v198\n"
"v_min_f32 v244, v244, v200\n"
"v_mfma_f32_16x16x32_bf16 v[12:15], v[100:103], v[224:227], v[12:15]\n"
"s_add_u32 s40, s56, s52\n"
"v_rcp_f32 v244, v244\n"
"v_mfma_f32_16x16x32_bf16 v[12:15], v[96:99], a[252:255], v[12:15]\n"
"buffer_load_dwordx4 v240, s[20:23], s40 offen offset:1024 nt lds\n"
"v_mfma_f32_16x16x32_bf16 a[220:223], v[120:123], a[252:255], a[220:223]\n"
"s_waitcnt lgkmcnt(2)\n"
"v_fmac_f32 v254, 0x68800000, v244\n"
"v_cmp_nge_f32 vcc, s45, v254\n"
"v_xor_b32 v244, 0x11000, v242\n"
"s_cbranch_vccnz LSLOW_%=\nLBACK2_%=:\ns_mov_b64 vcc, s[64:65]\n"
"v_mfma_f32_16x16x32_bf16 a[232:235], v[120:123], a[244:247], a[232:235]\n"
"v_accvgpr_mov_b32 a6, a150\n"
"v_accvgpr_mov_b32 a8, a152\n"
"v_accvgpr_mov_b32 a9, a153\n"
"v_accvgpr_mov_b32 a14, a158\n"
"v_accvgpr_mov_b32 a20, a164\n"
"v_accvgpr_mov_b32 a42, a186\n"
"v_accvgpr_mov_b32 a44, a188\n"
"v_xor_b32 v148, 0x400, v243\n"
"v_bfe_u32 v149, v242, 8, 2\n"
"v_lshl_add_u32 v149, v149, 4, s43\n"
"v_mfma_f32_16x16x32_bf16 v[24:27], v[96:99], a[244:247], v[24:27]\n"
"s_waitcnt lgkmcnt(0)\n"
"ds_read_b128 v[124:127], v149 offset:0\n"
"v_rcp_f32 v206, v200\n"
"v_accvgpr_mov_b32 a4, a148\n"
"v_accvgpr_mov_b32 a12, a156\n"
"v_accvgpr_mov_b32 a13, a157\n"
"v_accvgpr_mov_b32 a16, a160\n"
"v_accvgpr_mov_b32 a39, a183\n"
"v_accvgpr_mov_b32 a47, a191\n"
"v_xor_b32 v150, 64, v149\n"
"v_mfma_f32_16x16x32_bf16 v[24:27], v[100:103], a[248:251], v[24:27]\n"
"ds_read_b128 v[128:131], v150 offset:0\n"
"v_accvgpr_mov_b32 a0, a144\n"
"v_accvgpr_mov_b32 a1, a145\n"
"v_accvgpr_mov_b32 a5, a149\n"
"v_accvgpr_mov_b32 a10, a154\n"
"v_accvgpr_mov_b32 a17, a161\n"
"v_accvgpr_mov_b32 a40, a184\n"
"v_mfma_f32_16x16x32_bf16 v[56:59], v[104:107], a[244:247], v[56:59]\n"
"ds_read_b128 v[132:135], v149 offset:1024\n"
"v_rcp_f32 v246, v196\n"
"v_mov_b32_dpp v206, v206 row_ror:12 row_mask:0x2 bank_mask:0xf\n"
"v_accvgpr_mov_b32 a2, a146\n"
"v_accvgpr_mov_b32 a22, a166\n"
"v_accvgpr_mov_b32 a23, a167\n"
"v_accvgpr_mov_b32 a36, a180\n"
"v_accvgpr_mov_b32 a38, a182\n"
"v_accvgpr_mov_b32 a45, a189\n"
"v_mfma_f32_16x16x32_bf16 v[56:59], v[108:111], a[248:251], v[56:59]\n"
"ds_read_b128 v[136:139], v150 offset:1024\n"
"v_accvgpr_mov_b32 a33, a177\n"
"v_mfma_f32_16x16x32_bf16 v[88:91], v[112:115], a[244:247], v[88:91]\n"
"ds_read_b64_tr_b16 v[224:225], v239 offset:50176\n"
"v_accvgpr_mov_b32 a3, a147\n"
"v_accvgpr_mov_b32 a7, a151\n"
"v_accvgpr_mov_b32 a11, a155\n"
"v_accvgpr_mov_b32 a19, a163\n"
"v_accvgpr_mov_b32 a21, a165\n"
"v_accvgpr_mov_b32 a24, a168\n"
"v_accvgpr_mov_b32 a27, a171\n"
"v_accvgpr_mov_b32 a30, a174\n"
"v_accvgpr_mov_b32 a46, a190\n"
"v_mfma_f32_16x16x32_bf16 v[88:91], v[116:119], a[248:251], v[88:91]\n"
"s_waitcnt lgkmcnt(4)\n"
"ds_read_b128 a[204:207], v148 offset:6144\n"
"v_mov_b32_dpp v206, v206 row_ror:8 row_mask:0x4 bank_mask:0xf\n"
"v_accvgpr_mov_b32 a15, a159\n"
"v_accvgpr_mov_b32 a25, a169\n"
"v_accvgpr_mov_b32 a26, a170\n"
"v_accvgpr_mov_b32 a29, a173\n"
"v_accvgpr_mov_b32 a31, a175\n"
"v_accvgpr_mov_b32 a35, a179\n"
"v_accvgpr_mov_b32 a43, a187\n"
"v_accvgpr_mov_b32 a160, a64\n"
"v_mfma_f32_16x16x32_bf16 v[180:183], v[124:127], a[0:3], 0\n"
"s_waitcnt lgkmcnt(4)\n"
"v_mov_b32 v252, v192\n"
"v_accvgpr_mov_b32 a174, a78\n"
"v_mfma_f32_16x16x32_bf16 v[180:183], v[128:131], a[4:7], v[180:183]\n"
"s_waitcnt lgkmcnt(3)\n"
"ds_read_b128 a[192:195], v243 offset:4096\n"
"v_accvgpr_mov_b32 a18, a162\n"
"v_mfma_f32_16x16x32_bf16 v[180:183], v[132:135], a[8:11], v[180:183]\n"
"s_waitcnt lgkmcnt(3)\n"
"ds_read_b128 a[200:203], v243 offset:6144\n"
"v_accvgpr_mov_b32 a183, a87\n"
"v_mfma_f32_16x16x32_bf16 v[180:183], v[136:139], a[12:15], v[180:183]\n"
"v_rcp_f32 v247, v198\n"
"v_accvgpr_mov_b32 a28, a172\n"
"v_accvgpr_mov_b32 a34, a178\n"
"v_accvgpr_mov_b32 a41, a185\n"
"v_accvgpr_mov_b32 a151, a55\n"
"v_accvgpr_mov_b32 a167, a71\n"
"v_accvgpr_mov_b32 a156, a60\n"
"v_accvgpr_mov_b32 a178, a82\n"
"v_mfma_f32_16x16x32_bf16 v[184:187], v[124:127], a[16:19], 0\n"
"ds_read_b128 a[196:199], v148 offset:4096\n"
"v_accvgpr_mov_b32 a158, a62\n"
"v_mfma_f32_16x16x32_bf16 v[184:187], v[128:131], a[20:23], v[184:187]\n"
"v_mov_b32_dpp v247, v247 row_ror:12 row_mask:0x2 bank_mask:0xf\n"
"v_accvgpr_mov_b32 a152, a56\n"
"v_mfma_f32_16x16x32_bf16 v[184:187], v[132:135], a[24:27], v[184:187]\n"
"s_add_u32 s40, s56, s53\n"
"v_accvgpr_mov_b32 a32, a176\n"
"v_accvgpr_mov_b32 a157, a61\n"
"v_mfma_f32_16x16x32_bf16 v[184:187], v[136:139], a[28:31], v[184:187]\n"
"buffer_load_dwordx4 v240, s[20:23], s40 offen offset:2048 nt lds\n"
"v_mov_b32_dpp v206, v206 row_ror:4 row_mask:0x8 bank_mask:0xf\n"
"v_accvgpr_mov_b32 a37, a181\n"
"v_accvgpr_mov_b32 a149, a53\n"
"v_accvgpr_mov_b32 a150, a54\n"
"v_accvgpr_mov_b32 a181, a85\n"
"v_accvgpr_mov_b32 a153, a57\n"
"v_sub_f32 v180, s74, v180\n"
"v_sub_f32 v183, s74, v183\n"
"v_mfma_f32_16x16x32_bf16 v[188:191], v[124:127], a[32:35], 0\n"
"v_mov_b32_dpp v246, v246 row_ror:12 row_mask:0x2 bank_mask:0xf\n"
"v_accvgpr_mov_b32 a180, a84\n"
"v_mfma_f32_16x16x32_bf16 v[188:191], v[128:131], a[36:39], v[188:191]\n"
"v_accvgpr_mov_b32 a162, a66\n"
"v_accvgpr_mov_b32 a179, a83\n"
"v_mfma_f32_16x16x32_bf16 v[188:191], v[132:135], a[40:43], v[188:191]\n"
"v_mov_b32_dpp v246, v246 row_ror:8 row_mask:0x4 bank_mask:0xf\n"
"v_sub_f32 v182, s74, v182\n"
"v_mfma_f32_16x16x32_bf16 v[188:191], v[136:139], a[44:47], v[188:191]\n"
"s_waitcnt lgkmcnt(1)\n"
"ds_write_b128 v244, a[220:223] offset:33792\n"
"v_mov_b32_dpp v247, v247 row_ror:8 row_mask:0x4 bank_mask:0xf\n"
"v_accvgpr_mov_b32 a163, a67\n"
"v_accvgpr_mov_b32 a177, a81\n"
"v_accvgpr_mov_b32 a154, a58\n"
"v_mfma_f32_16x16x32_bf16 v[192:195], v[124:127], a[192:195], 0\n"
"s_waitcnt lgkmcnt(1)\n"
"v_accvgpr_mov_b32 a164, a68\n"
"v_sub_f32 v181, s74, v181\n"
"v_mfma_f32_16x16x32_bf16 v[192:195], v[128:131], a[196:199], v[192:195]\n"
"v_accvgpr_mov_b32 a173, a77\n"
"v_accvgpr_mov_b32 a144, a48\n"
"v_mfma_f32_16x16x32_bf16 v[192:195], v[132:135], a[200:203], v[192:195]\n"
"ds_read_b64_tr_b16 v[226:227], v239 offset:54272\n"
"ds_read_b64_tr_b16 a[246:247], v232 offset:53248\n"
"v_mfma_f32_16x16x32_bf16 v[192:195], v[136:139], a[204:207], v[192:195]\n"
"v_mov_b32_dpp v246, v246 row_ror:4 row_mask:0x8 bank_mask:0xf\n"
"v_mov_b32_dpp v247, v247 row_ror:4 row_mask:0x8 bank_mask:0xf\n"
"v_accvgpr_mov_b32 a148, a52\n"
"v_accvgpr_mov_b32 a165, a69\n"
"v_accvgpr_mov_b32 a175, a79\n"
"v_accvgpr_mov_b32 a176, a80\n"
"v_sub_f32 v184, s74, v184\n"
"v_sub_f32 v187, s74, v187\n"
"v_sub_f32 v190, s74, v190\n"
"v_mfma_f32_16x16x32_bf16 v[152:155], v[208:211], a[0:3], v[180:183]\n"
"v_mov_b32 v253, v202\n"
"v_sub_f32 v185, s74, v185\n"
"v_mfma_f32_16x16x32_bf16 v[152:155], v[212:215], a[4:7], v[152:155]\n"
"v_accvgpr_mov_b32 a186, a90\n"
"v_sub_f32 v186, s74, v186\n"
"v_mfma_f32_16x16x32_bf16 v[152:155], v[216:219], a[8:11], v[152:155]\n"
"s_add_u32 s40, s56, s54\n"
"ds_read_b64_tr_b16 a[254:255], v235 offset:54272\n"
"v_sub_f32 v189, s74, v189\n"
"v_mfma_f32_16x16x32_bf16 v[152:155], v[220:223], a[12:15], v[152:155]\n"
"buffer_load_dwordx4 v240, s[20:23], s40 offen offset:3072 nt lds\n"
"v_accvgpr_mov_b32 a190, a94\n"
"v_accvgpr_mov_b32 a191, a95\n"
"v_accvgpr_mov_b32 a168, a72\n"
"v_mul_f32_dpp v9, v246, v9 row_newbcast:1 row_mask:0xf bank_mask:0xf\n"
"v_mul_f32_dpp v12, v246, v12 row_newbcast:0 row_mask:0xf bank_mask:0xf\n"
"v_sub_f32 v188, s74, v188\n"
"v_sub_f32 v191, s74, v191\n"
"v_mfma_f32_16x16x32_bf16 v[156:159], v[216:219], a[24:27], v[184:187]\n"
"s_add_u32 s55, s55, s59\n"
"v_accvgpr_mov_b32 a159, a63\n"
"v_accvgpr_mov_b32 a155, a59\n"
"v_mfma_f32_16x16x32_bf16 v[156:159], v[220:223], a[28:31], v[156:159]\n"
"s_add_u32 s41, s55, s57\n"
"v_accvgpr_mov_b32 a145, a49\n"
"v_accvgpr_mov_b32 a170, a74\n"
"v_mfma_f32_16x16x32_bf16 v[156:159], v[208:211], a[16:19], v[156:159]\n"
"s_and_b32 s41, s41, s58\n"
"v_accvgpr_mov_b32 a147, a51\n"
"v_sub_f32 v192, s74, v192\n"
"v_mfma_f32_16x16x32_bf16 v[156:159], v[212:215], a[20:23], v[156:159]\n"
"s_andn2_b32 s56, s55, s58\n"
"ds_read_b64_tr_b16 a[250:251], v236 offset:53248\n"
"v_xor_b32 v250, 0x1000, v245\n"
"v_accvgpr_mov_b32 a146, a50\n"
"v_accvgpr_mov_b32 a161, a65\n"
"v_accvgpr_mov_b32 a187, a91\n"
"v_mul_f32_dpp v41, v247, v41 row_newbcast:1 row_mask:0xf bank_mask:0xf\n"
"v_mul_f32_dpp v46, v247, v46 row_newbcast:2 row_mask:0xf bank_mask:0xf\n"
"v_sub_f32 v195, s74, v195\n"
"v_exp_f32 v153, v153\n"
"v_mfma_f32_16x16x32_bf16 v[160:163], v[208:211], a[32:35], v[188:191]\n"
"s_or_b32 s56, s56, s41\n"
"v_mul_f32_dpp v43, v247, v43 row_newbcast:3 row_mask:0xf bank_mask:0xf\n"
"v_sub_f32 v193, s74, v193\n"
"v_mfma_f32_16x16x32_bf16 v[160:163], v[212:215], a[36:39], v[160:163]\n"
"v_accvgpr_mov_b32 a171, a75\n"
"v_accvgpr_mov_b32 a185, a89\n"
"v_mfma_f32_16x16x32_bf16 v[160:163], v[216:219], a[40:43], v[160:163]\n"
"v_accvgpr_mov_b32 a182, a86\n"
"v_sub_f32 v194, s74, v194\n"
"v_mfma_f32_16x16x32_bf16 v[160:163], v[220:223], a[44:47], v[160:163]\n"
"ds_read_b64_tr_b16 a[252:253], v235 offset:50176\n"
"v_accvgpr_mov_b32 a172, a76\n"
"v_accvgpr_mov_b32 a184, a88\n"
"v_exp_f32 v152, v152\n"
"v_exp_f32 v157, v157\n"
"v_exp_f32 v158, v158\n"
"v_mfma_f32_16x16x32_bf16 v[164:167], v[216:219], a[200:203], v[192:195]\n"
"ds_read_b128 v[216:219], v228 offset:5120\n"
"v_accvgpr_mov_b32 a166, a70\n"
"v_mfma_f32_16x16x32_bf16 v[164:167], v[220:223], a[204:207], v[164:167]\n"
"v_accvgpr_mov_b32 a188, a92\n"
"v_mul_f32_dpp v57, v247, v57 row_newbcast:1 row_mask:0xf bank_mask:0xf\n"
"v_mfma_f32_16x16x32_bf16 v[164:167], v[208:211], a[192:195], v[164:167]\n"
"ds_read_b128 v[208:211], v228 offset:4096\n"
"v_accvgpr_mov_b32 a189, a93\n"
"v_mfma_f32_16x16x32_bf16 v[164:167], v[212:215], a[196:199], v[164:167]\n"
"s_waitcnt lgkmcnt(3)\n"
"ds_read_b128 v[220:223], v229 offset:5120\n"
"v_accvgpr_mov_b32 a169, a73\n"
"v_mul_f32_dpp v10, v246, v10 row_newbcast:2 row_mask:0xf bank_mask:0xf\n"
"v_exp_f32 v154, v154\n"
"v_exp_f32 v155, v155\n"
"v_mul_f32_dpp v27, v246, v27 row_newbcast:3 row_mask:0xf bank_mask:0xf\n"
"v_mul_f32_dpp v58, v247, v58 row_newbcast:2 row_mask:0xf bank_mask:0xf\n"
"LTC_%=:\n"
"s_waitcnt lgkmcnt(4)\n"
"v_mfma_f32_16x16x32_bf16 v[92:95], v[116:119], v[224:227], v[92:95]\n"
"s_waitcnt lgkmcnt(3)\n"
"v_mul_f32_dpp v56, v247, v56 row_newbcast:0 row_mask:0xf bank_mask:0xf\n"
"v_cvt_pk_bf16_f32 v56, v56, v57\n"
"v_mfma_f32_16x16x32_bf16 v[92:95], v[112:115], a[252:255], v[92:95]\n"
"v_mul_f32_dpp v13, v246, v13 row_newbcast:1 row_mask:0xf bank_mask:0xf\n"
"v_mul_f32_dpp v44, v247, v44 row_newbcast:0 row_mask:0xf bank_mask:0xf\n"
"v_mfma_f32_16x16x32_bf16 v[60:63], v[108:111], v[224:227], v[60:63]\n"
"s_add_u32 m0, s44, 32768\n"
"ds_read_b128 v[212:215], v229 offset:4096\n"
"v_mul_f32_dpp v8, v246, v8 row_newbcast:0 row_mask:0xf bank_mask:0xf\n"
"v_mfma_f32_16x16x32_bf16 v[60:63], v[104:107], a[252:255], v[60:63]\n"
"buffer_load_dwordx4 v240, s[16:19], s56 offen nt lds\n"
"v_mfma_f32_16x16x32_bf16 v[28:31], v[100:103], v[224:227], v[28:31]\n"
"v_exp_f32 v165, v165\n"
"v_mfma_f32_16x16x32_bf16 v[28:31], v[96:99], a[252:255], v[28:31]\n"
"v_exp_f32 v166, v166\n"
"v_mfma_f32_16x16x32_bf16 a[236:239], v[120:123], a[252:255], a[236:239]\n"
"s_waitcnt lgkmcnt(2)\n"
"ds_read_b64_tr_b16 a[254:255], v233 offset:53248\n"
"v_mul_f32_dpp v42, v247, v42 row_newbcast:2 row_mask:0xf bank_mask:0xf\n"
"v_mfma_f32_16x16x32_bf16 v[168:171], v[216:219], a[8:11], v[180:183]\n"
"s_waitcnt lgkmcnt(2)\n"
"v_exp_f32 v156, v156\n"
"v_mfma_f32_16x16x32_bf16 v[168:171], v[220:223], a[12:15], v[168:171]\n"
"v_cvt_pk_bf16_f32 v8, v8, v9\n"
"v_mul_f32_dpp v76, v206, v76 row_newbcast:0 row_mask:0xf bank_mask:0xf\n"
"v_mfma_f32_16x16x32_bf16 v[168:171], v[208:211], a[0:3], v[168:171]\n"
"s_waitcnt lgkmcnt(1)\n"
"ds_read_b64_tr_b16 v[224:225], v237 offset:49152\n"
"v_mov_b64 v[202:203], 0\n"
"v_mfma_f32_16x16x32_bf16 v[168:171], v[212:215], a[4:7], v[168:171]\n"
"ds_write_b128 v244, a[236:239] offset:35840\n"
"v_mul_f32_dpp v77, v206, v77 row_newbcast:1 row_mask:0xf bank_mask:0xf\n"
"v_mul_f32_dpp v78, v206, v78 row_newbcast:2 row_mask:0xf bank_mask:0xf\n"
"v_cvt_pk_bf16_f32 v136, v156, v157\n"
"v_mul_f32_dpp v59, v247, v59 row_newbcast:3 row_mask:0xf bank_mask:0xf\n"
"v_mul_f32_dpp v88, v206, v88 row_newbcast:0 row_mask:0xf bank_mask:0xf\n"
"v_mfma_f32_16x16x32_bf16 v[172:175], v[208:211], a[16:19], v[184:187]\n"
"v_mul_f32_dpp v15, v246, v15 row_newbcast:3 row_mask:0xf bank_mask:0xf\n"
"v_mul_f32_dpp v25, v246, v25 row_newbcast:1 row_mask:0xf bank_mask:0xf\n"
"v_mfma_f32_16x16x32_bf16 v[172:175], v[212:215], a[20:23], v[172:175]\n"
"ds_read_b64_tr_b16 a[244:245], v232 offset:49152\n"
"v_cvt_pk_bf16_f32 v129, v154, v155\n"
"v_mfma_f32_16x16x32_bf16 v[172:175], v[216:219], a[24:27], v[172:175]\n"
"ds_read_b64_tr_b16 a[248:249], v236 offset:49152\n"
"v_mul_f32_dpp v11, v246, v11 row_newbcast:3 row_mask:0xf bank_mask:0xf\n"
"v_mfma_f32_16x16x32_bf16 v[172:175], v[220:223], a[28:31], v[172:175]\n"
"v_mul_f32_dpp v14, v246, v14 row_newbcast:2 row_mask:0xf bank_mask:0xf\n"
"v_mul_f32_dpp v45, v247, v45 row_newbcast:1 row_mask:0xf bank_mask:0xf\n"
"v_mfma_f32_16x16x32_bf16 v[176:179], v[216:219], a[40:43], v[188:191]\n"
"ds_read_b64_tr_b16 a[252:253], v233 offset:49152\n"
"v_mul_f32_dpp v73, v206, v73 row_newbcast:1 row_mask:0xf bank_mask:0xf\n"
"v_mfma_f32_16x16x32_bf16 v[176:179], v[220:223], a[44:47], v[176:179]\n"
"v_mul_f32_dpp v72, v206, v72 row_newbcast:0 row_mask:0xf bank_mask:0xf\n"
"v_mul_f32_dpp v93, v206, v93 row_newbcast:1 row_mask:0xf bank_mask:0xf\n"
"v_mfma_f32_16x16x32_bf16 v[176:179], v[208:211], a[32:35], v[176:179]\n"
"s_add_u32 s40, s56, s52\n"
"ds_read_b64_tr_b16 v[226:227], v237 offset:53248\n"
"v_cvt_pk_bf16_f32 v128, v152, v153\n"
"v_mfma_f32_16x16x32_bf16 v[176:179], v[212:215], a[36:39], v[176:179]\n"
"buffer_load_dwordx4 v240, s[16:19], s40 offen offset:1024 nt lds\n"
"v_mul_f32_dpp v47, v247, v47 row_newbcast:3 row_mask:0xf bank_mask:0xf\n"
"v_exp_f32 v161, v161\n"
"v_mul_f32_dpp v90, v206, v90 row_newbcast:2 row_mask:0xf bank_mask:0xf\n"
"v_exp_f32 v169, v169\n"
"v_exp_f32 v170, v170\n"
"v_mfma_f32_16x16x32_bf16 v[152:155], v[208:211], a[192:195], v[192:195]\n"
"v_exp_f32 v162, v162\n"
"v_mfma_f32_16x16x32_bf16 v[152:155], v[212:215], a[196:199], v[152:155]\n"
"v_exp_f32 v159, v159\n"
"v_mfma_f32_16x16x32_bf16 v[152:155], v[216:219], a[200:203], v[152:155]\n"
"ds_read_b128 v[212:215], v231 offset:0\n"
"v_cvt_pk_bf16_f32 v137, v158, v159\n"
"v_mfma_f32_16x16x32_bf16 v[152:155], v[220:223], a[204:207], v[152:155]\n"
"s_waitcnt lgkmcnt(3)\n"
"ds_read_b128 v[220:223], v231 offset:1024\n"
"v_mul_f32_dpp v30, v246, v30 row_newbcast:2 row_mask:0xf bank_mask:0xf\n"
"v_mfma_f32_16x16x32_bf16 a[208:211], v[120:123], a[244:247], a[208:211]\n"
"ds_read_b128 v[208:211], v230 offset:0\n"
"v_exp_f32 v163, v163\n"
"v_exp_f32 v167, v167\n"
"v_mul_f32_dpp v62, v247, v62 row_newbcast:2 row_mask:0xf bank_mask:0xf\n"
"v_exp_f32 v168, v168\n"
"v_exp_f32 v178, v178\n"
"v_mfma_f32_16x16x32_bf16 v[0:3], v[96:99], a[244:247], v[0:3]\n"
"v_cvt_pk_bf16_f32 v125, v166, v167\n"
"v_mul_f32_dpp v61, v247, v61 row_newbcast:1 row_mask:0xf bank_mask:0xf\n"
"v_mfma_f32_16x16x32_bf16 v[0:3], v[100:103], a[248:251], v[0:3]\n"
"ds_read_b128 v[216:219], v230 offset:1024\n"
"v_mul_f32_dpp v40, v247, v40 row_newbcast:0 row_mask:0xf bank_mask:0xf\n"
"v_cvt_pk_bf16_f32 v40, v40, v41\n"
"v_exp_f32 v160, v160\n"
"v_cvt_pk_bf16_f32 v145, v162, v163\n"
"v_exp_f32 v164, v164\n"
"v_cvt_pk_bf16_f32 v57, v58, v59\n"
"v_mfma_f32_16x16x32_bf16 v[32:35], v[104:107], a[244:247], v[32:35]\n"
"v_exp_f32 v174, v174\n"
"v_mfma_f32_16x16x32_bf16 v[32:35], v[108:111], a[248:251], v[32:35]\n"
"ds_write_b128 v244, a[232:235] offset:34816\n"
"v_mul_f32_dpp v74, v206, v74 row_newbcast:2 row_mask:0xf bank_mask:0xf\n"
"v_mul_f32_dpp v26, v246, v26 row_newbcast:2 row_mask:0xf bank_mask:0xf\n"
"v_exp_f32 v176, v176\n"
"v_mfma_f32_16x16x32_bf16 v[64:67], v[112:115], a[244:247], v[64:67]\n"
"v_mul_f32_dpp v94, v206, v94 row_newbcast:2 row_mask:0xf bank_mask:0xf\n"
"v_mul_f32_dpp v95, v206, v95 row_newbcast:3 row_mask:0xf bank_mask:0xf\n"
"v_mfma_f32_16x16x32_bf16 v[64:67], v[116:119], a[248:251], v[64:67]\n"
"s_waitcnt lgkmcnt(2)\n"
"ds_write_b128 v244, a[216:219] offset:32768\n"
"v_cvt_pk_bf16_f32 v41, v42, v43\n"
"v_mul_f32_dpp v79, v206, v79 row_newbcast:3 row_mask:0xf bank_mask:0xf\n"
"v_mul_f32_dpp v28, v246, v28 row_newbcast:0 row_mask:0xf bank_mask:0xf\n"
"v_mul_f32_dpp v29, v246, v29 row_newbcast:1 row_mask:0xf bank_mask:0xf\n"
"v_mul_f32_dpp v89, v206, v89 row_newbcast:1 row_mask:0xf bank_mask:0xf\n"
"v_mfma_f32_16x16x32_bf16 v[156:159], v[208:211], a[0:3], v[180:183]\n"
"v_exp_f32 v172, v172\n"
"v_mfma_f32_16x16x32_bf16 v[156:159], v[212:215], a[4:7], v[156:159]\n"
"s_waitcnt lgkmcnt(2)\n"
"v_exp_f32 v175, v175\n"
"v_mfma_f32_16x16x32_bf16 v[156:159], v[216:219], a[8:11], v[156:159]\n"
"s_add_u32 s40, s56, s53\n"
"ds_read_b64_tr_b16 a[250:251], v236 offset:54272\n"
"v_cvt_pk_bf16_f32 v144, v160, v161\n"
"v_mfma_f32_16x16x32_bf16 v[156:159], v[220:223], a[12:15], v[156:159]\n"
"buffer_load_dwordx4 v240, s[16:19], s40 offen offset:2048 nt lds\n"
"v_cvt_pk_bf16_f32 v72, v72, v73\n"
"v_mul_f32_dpp v24, v246, v24 row_newbcast:0 row_mask:0xf bank_mask:0xf\n"
"v_mul_f32_dpp v92, v206, v92 row_newbcast:0 row_mask:0xf bank_mask:0xf\n"
"v_cvt_pk_bf16_f32 v88, v88, v89\n"
"v_exp_f32 v177, v177\n"
"v_exp_f32 v155, v155\n"
"v_mfma_f32_16x16x32_bf16 v[160:163], v[216:219], a[24:27], v[184:187]\n"
"v_exp_f32 v171, v171\n"
"v_mfma_f32_16x16x32_bf16 v[160:163], v[220:223], a[28:31], v[160:163]\n"
"v_mul_f32_dpp v60, v247, v60 row_newbcast:0 row_mask:0xf bank_mask:0xf\n"
"v_mul_f32_dpp v32, v247, v32 row_newbcast:0 row_mask:0xf bank_mask:0xf\n"
"v_mfma_f32_16x16x32_bf16 v[160:163], v[208:211], a[16:19], v[160:163]\n"
"v_cvt_pk_bf16_f32 v130, v168, v169\n"
"v_mul_f32_dpp v0, v246, v0 row_newbcast:0 row_mask:0xf bank_mask:0xf\n"
"v_mfma_f32_16x16x32_bf16 v[160:163], v[212:215], a[20:23], v[160:163]\n"
"ds_read_b64_tr_b16 a[244:245], v232 offset:50176\n"
"v_cvt_pk_bf16_f32 v124, v164, v165\n"
"v_mul_f32_dpp v63, v247, v63 row_newbcast:3 row_mask:0xf bank_mask:0xf\n"
"v_mul_f32_dpp v91, v206, v91 row_newbcast:3 row_mask:0xf bank_mask:0xf\n"
"v_exp_f32 v179, v179\n"
"v_exp_f32 v152, v152\n"
"v_mul_f32_dpp v64, v206, v64 row_newbcast:0 row_mask:0xf bank_mask:0xf\n"
"v_mfma_f32_16x16x32_bf16 v[164:167], v[208:211], a[32:35], v[188:191]\n"
"ds_read_b64_tr_b16 a[248:249], v236 offset:50176\n"
"v_cvt_pk_bf16_f32 v24, v24, v25\n"
"v_mfma_f32_16x16x32_bf16 v[164:167], v[212:215], a[36:39], v[164:167]\n"
"v_cvt_pk_bf16_f32 v9, v10, v11\n"
"v_cvt_pk_bf16_f32 v11, v14, v15\n"
"v_mfma_f32_16x16x32_bf16 v[164:167], v[216:219], a[40:43], v[164:167]\n"
"ds_read_b128 v[216:219], v230 offset:5120\n"
"v_cvt_pk_bf16_f32 v25, v26, v27\n"
"v_mfma_f32_16x16x32_bf16 v[164:167], v[220:223], a[44:47], v[164:167]\n"
"ds_read_b128 v[212:215], v231 offset:4096\n"
"v_cvt_pk_bf16_f32 v43, v46, v47\n"
"v_cvt_pk_bf16_f32 v26, v28, v29\n"
"v_exp_f32 v173, v173\n"
"v_exp_f32 v154, v154\n"
"v_exp_f32 v157, v157\n"
"v_mul_f32_dpp v65, v206, v65 row_newbcast:1 row_mask:0xf bank_mask:0xf\n"
"v_mfma_f32_16x16x32_bf16 v[68:71], v[116:119], v[224:227], v[68:71]\n"
"ds_read_b128 v[208:211], v230 offset:4096\n"
"v_mul_f32_dpp v75, v206, v75 row_newbcast:3 row_mask:0xf bank_mask:0xf\n"
"v_mfma_f32_16x16x32_bf16 v[68:71], v[112:115], a[252:255], v[68:71]\n"
"ds_read_b64_tr_b16 a[246:247], v232 offset:54272\n"
"v_cvt_pk_bf16_f32 v58, v60, v61\n"
"v_cvt_pk_bf16_f32 v139, v174, v175\n"
"v_cvt_pk_bf16_f32 v146, v176, v177\n"
"v_exp_f32 v161, v161\n"
"v_exp_f32 v163, v163\n"
"v_mul_f32_dpp v2, v246, v2 row_newbcast:2 row_mask:0xf bank_mask:0xf\n"
"v_mfma_f32_16x16x32_bf16 v[36:39], v[108:111], v[224:227], v[36:39]\n"
"s_add_u32 s40, s56, s54\n"
"ds_read_b128 v[220:223], v231 offset:5120\n"
"v_cvt_pk_bf16_f32 v138, v172, v173\n"
"v_mfma_f32_16x16x32_bf16 v[36:39], v[104:107], a[252:255], v[36:39]\n"
"buffer_load_dwordx4 v240, s[16:19], s40 offen offset:3072 nt lds\n"
"v_cvt_pk_bf16_f32 v10, v12, v13\n"
"v_cvt_pk_bf16_f32 v73, v74, v75\n"
"v_cvt_pk_bf16_f32 v127, v154, v155\n"
"v_exp_f32 v156, v156\n"
"v_exp_f32 v162, v162\n"
"v_mfma_f32_16x16x32_bf16 v[4:7], v[100:103], v[224:227], v[4:7]\n"
"v_mul_f32_dpp v31, v246, v31 row_newbcast:3 row_mask:0xf bank_mask:0xf\n"
"v_mul_f32_dpp v34, v247, v34 row_newbcast:2 row_mask:0xf bank_mask:0xf\n"
"v_mfma_f32_16x16x32_bf16 v[4:7], v[96:99], a[252:255], v[4:7]\n"
"v_cvt_pk_bf16_f32 v59, v62, v63\n"
"v_cvt_pk_bf16_f32 v131, v170, v171\n"
"v_mfma_f32_16x16x32_bf16 a[212:215], v[120:123], a[252:255], a[212:215]\n"
"s_waitcnt lgkmcnt(2)\n"
"ds_write_b64 v250, v[252:253] offset:52224\n"
"v_cvt_pk_bf16_f32 v42, v44, v45\n"
"v_cvt_pk_bf16_f32 v89, v90, v91\n"
"v_exp_f32 v153, v153\n"
"v_mul_f32_dpp v3, v246, v3 row_newbcast:3 row_mask:0xf bank_mask:0xf\n"
"v_mul_f32_dpp v33, v247, v33 row_newbcast:1 row_mask:0xf bank_mask:0xf\n"
"v_mul_f32_dpp v69, v206, v69 row_newbcast:1 row_mask:0xf bank_mask:0xf\n"
"v_mfma_f32_16x16x32_bf16 v[168:171], v[208:211], a[0:3], v[180:183]\n"
"ds_read_b64_tr_b16 v[226:227], v237 offset:54272\n"
"v_mul_f32_dpp v39, v247, v39 row_newbcast:3 row_mask:0xf bank_mask:0xf\n"
"v_mfma_f32_16x16x32_bf16 v[168:171], v[212:215], a[4:7], v[168:171]\n"
"v_cvt_pk_bf16_f32 v74, v76, v77\n"
"v_mul_f32_dpp v1, v246, v1 row_newbcast:1 row_mask:0xf bank_mask:0xf\n"
"v_mfma_f32_16x16x32_bf16 v[168:171], v[216:219], a[8:11], v[168:171]\n"
"s_waitcnt lgkmcnt(2)\n"
"ds_read_b64_tr_b16 a[254:255], v233 offset:54272\n"
"v_mul_f32_dpp v68, v206, v68 row_newbcast:0 row_mask:0xf bank_mask:0xf\n"
"v_mfma_f32_16x16x32_bf16 v[168:171], v[220:223], a[12:15], v[168:171]\n"
"ds_write_b128 v248, v[40:43] offset:50176\n"
"v_cvt_pk_bf16_f32 v75, v78, v79\n"
"v_exp_f32 v158, v158\n"
"v_exp_f32 v160, v160\n"
"v_mfma_f32_16x16x32_bf16 v[172:175], v[216:219], a[24:27], v[184:187]\n"
"ds_read_b64_tr_b16 a[68:69], v249 offset:50176\n"
"v_cvt_pk_bf16_f32 v140, v160, v161\n"
"v_mfma_f32_16x16x32_bf16 v[172:175], v[220:223], a[28:31], v[172:175]\n"
"v_cvt_pk_bf16_f32 v147, v178, v179\n"
"v_mul_f32_dpp v70, v206, v70 row_newbcast:2 row_mask:0xf bank_mask:0xf\n"
"v_mfma_f32_16x16x32_bf16 v[172:175], v[208:211], a[16:19], v[172:175]\n"
"v_mul_f32_dpp v35, v247, v35 row_newbcast:3 row_mask:0xf bank_mask:0xf\n"
"v_mul_f32_dpp v36, v247, v36 row_newbcast:0 row_mask:0xf bank_mask:0xf\n"
"v_mfma_f32_16x16x32_bf16 v[172:175], v[212:215], a[20:23], v[172:175]\n"
"s_cbranch_vccnz LPCT0_%=\nLPKT0_%=:\n"
"ds_write_b128 v248, v[8:11] offset:49152\n"
"v_cvt_pk_bf16_f32 v91, v94, v95\n"
"v_mul_f32_dpp v37, v247, v37 row_newbcast:1 row_mask:0xf bank_mask:0xf\n"
"v_mul_f32_dpp v66, v206, v66 row_newbcast:2 row_mask:0xf bank_mask:0xf\n"
"v_mul_f32_dpp v67, v206, v67 row_newbcast:3 row_mask:0xf bank_mask:0xf\n"
"v_mfma_f32_16x16x32_bf16 v[176:179], v[208:211], a[32:35], v[188:191]\n"
"ds_read_b64_tr_b16 a[70:71], v249 offset:50184\n"
"ds_read_b64_tr_b16 v[224:225], v237 offset:50176\n"
"v_mfma_f32_16x16x32_bf16 v[176:179], v[212:215], a[36:39], v[176:179]\n"
"ds_read_b64_tr_b16 a[252:253], v233 offset:50176\n"
"v_mul_f32_dpp v7, v246, v7 row_newbcast:3 row_mask:0xf bank_mask:0xf\n"
"v_mfma_f32_16x16x32_bf16 v[176:179], v[216:219], a[40:43], v[176:179]\n"
"ds_read_b64_tr_b16 a[52:53], v249 offset:49152\n"
"ds_read_b64_tr_b16 a[54:55], v249 offset:49160\n"
"v_mfma_f32_16x16x32_bf16 v[176:179], v[220:223], a[44:47], v[176:179]\n"
"ds_write_b128 v248, v[56:59] offset:50176\n"
"v_cvt_pk_bf16_f32 v27, v30, v31\n"
"v_exp_f32 v171, v171\n"
"v_exp_f32 v172, v172\n"
"v_mfma_f32_16x16x32_bf16 a[224:227], v[120:123], a[244:247], a[224:227]\n"
"ds_write_b128 v248, v[24:27] offset:49152\n"
"v_exp_f32 v164, v164\n"
"v_mul_f32_dpp v6, v246, v6 row_newbcast:2 row_mask:0xf bank_mask:0xf\n"
"v_cvt_pk_bf16_f32 v0, v0, v1\n"
"v_xor_b32 v244, 0x1000, v244\n"
"v_mfma_f32_16x16x32_bf16 v[16:19], v[96:99], a[244:247], v[16:19]\n"
"s_waitcnt vmcnt(8) lgkmcnt(4)\n"
"ds_read_b64_tr_b16 a[78:79], v249 offset:50184\n"
"v_cvt_pk_bf16_f32 v1, v2, v3\n"
"v_mfma_f32_16x16x32_bf16 v[16:19], v[100:103], a[248:251], v[16:19]\n"
"s_barrier\n"
"ds_read_b64_tr_b16 a[60:61], v249 offset:49152\n"
"ds_read_b128 v[216:219], v228 offset:17408\n"
"v_cvt_pk_bf16_f32 v32, v32, v33\n"
"v_mul_f32_dpp v71, v206, v71 row_newbcast:3 row_mask:0xf bank_mask:0xf\n"
"v_cvt_pk_bf16_f32 v64, v64, v65\n"
"v_cvt_pk_bf16_f32 v65, v66, v67\n"
"v_exp_f32 v174, v174\n"
"v_mfma_f32_16x16x32_bf16 v[48:51], v[104:107], a[244:247], v[48:51]\n"
"ds_read_b128 v[28:31], v244 offset:33792\n"
"v_mul_f32_dpp v4, v246, v4 row_newbcast:0 row_mask:0xf bank_mask:0xf\n"
"v_mfma_f32_16x16x32_bf16 v[48:51], v[108:111], a[248:251], v[48:51]\n"
"ds_write_b128 v248, v[72:75] offset:51200\n"
"v_cvt_pk_bf16_f32 v126, v152, v153\n"
"v_cvt_pk_bf16_f32 v132, v156, v157\n"
"v_exp_f32 v165, v165\n"
"v_mfma_f32_16x16x32_bf16 v[80:83], v[112:115], a[244:247], v[80:83]\n"
"ds_read_b128 v[208:211], v228 offset:16384\n"
"v_mul_f32_dpp v38, v247, v38 row_newbcast:2 row_mask:0xf bank_mask:0xf\n"
"v_mfma_f32_16x16x32_bf16 v[80:83], v[116:119], a[248:251], v[80:83]\n"
"ds_read_b64_tr_b16 a[76:77], v249 offset:50176\n"
"ds_read_b128 v[212:215], v229 offset:16384\n"
"v_exp_f32 v159, v159\n"
"v_cvt_pk_bf16_f32 v33, v34, v35\n"
"v_exp_f32 v168, v168\n"
"v_exp_f32 v173, v173\n"
"v_mfma_f32_16x16x32_bf16 v[84:87], v[116:119], v[224:227], v[84:87]\n"
"ds_read_b64_tr_b16 a[84:85], v249 offset:51200\n"
"ds_read_b64 v[250:251], v245 offset:52224\n"
"v_mfma_f32_16x16x32_bf16 v[84:87], v[112:115], a[252:255], v[84:87]\n"
"ds_read_b64_tr_b16 a[86:87], v249 offset:51208\n"
"ds_read_b128 v[220:223], v229 offset:17408\n"
"v_cvt_pk_bf16_f32 v90, v92, v93\n"
"v_exp_f32 v166, v166\n"
"v_mul_f32_dpp v5, v246, v5 row_newbcast:1 row_mask:0xf bank_mask:0xf\n"
"v_cvt_pk_bf16_f32 v66, v68, v69\n"
"v_exp_f32 v175, v175\n"
"v_mfma_f32_16x16x32_bf16 v[52:55], v[108:111], v[224:227], v[52:55]\n"
"v_cvt_pk_bf16_f32 v34, v36, v37\n"
"v_mul_f32_dpp v81, v206, v81 row_newbcast:1 row_mask:0xf bank_mask:0xf\n"
"v_mfma_f32_16x16x32_bf16 v[52:55], v[104:107], a[252:255], v[52:55]\n"
"ds_write_b128 v248, v[88:91] offset:51200\n"
"v_cvt_pk_bf16_f32 v133, v158, v159\n"
"v_exp_f32 v177, v177\n"
"v_exp_f32 v179, v179\n"
"v_mfma_f32_16x16x32_bf16 v[20:23], v[100:103], v[224:227], v[20:23]\n"
"ds_read_b128 v[24:27], v244 offset:32768\n"
"v_cvt_pk_bf16_f32 v2, v4, v5\n"
"v_mfma_f32_16x16x32_bf16 v[20:23], v[96:99], a[252:255], v[20:23]\n"
"s_waitcnt lgkmcnt(4)\n"
"ds_read_b64_tr_b16 a[62:63], v249 offset:49160\n"
"ds_read_b64_tr_b16 a[244:245], v234 offset:0\n"
"ds_read_b64_tr_b16 a[248:249], v238 offset:0\n"
"v_exp_f32 v167, v167\n"
"v_cvt_pk_bf16_f32 v3, v6, v7\n"
"v_cvt_pk_bf16_f32 v35, v38, v39\n"
"v_exp_f32 v178, v178\n"
"v_min_f32 v254, v252, v250\n"
"v_mfma_f32_16x16x32_bf16 a[228:231], v[120:123], a[252:255], a[228:231]\n"
"ds_read_b64_tr_b16 a[92:93], v249 offset:51200\n"
"ds_read_b64_tr_b16 a[94:95], v249 offset:51208\n"
"v_mfma_f32_16x16x32_bf16 v[152:155], v[208:211], a[0:3], v[180:183]\n"
"ds_read_b128 v[60:63], v244 offset:35840\n"
"v_cvt_pk_bf16_f32 v67, v70, v71\n"
"v_mfma_f32_16x16x32_bf16 v[152:155], v[212:215], a[4:7], v[152:155]\n"
"ds_write_b128 v248, v[64:67] offset:51200\n"
"v_exp_f32 v169, v169\n"
"v_mul_f32_dpp v48, v247, v48 row_newbcast:0 row_mask:0xf bank_mask:0xf\n"
"v_mul_f32_dpp v84, v206, v84 row_newbcast:0 row_mask:0xf bank_mask:0xf\n"
"v_mov_b64 v[198:199], 0\n"
"v_mfma_f32_16x16x32_bf16 v[152:155], v[216:219], a[8:11], v[152:155]\n"
"s_waitcnt lgkmcnt(9)\n"
"ds_write_b128 v248, v[32:35] offset:50176\n"
"v_cvt_pk_bf16_f32 v141, v162, v163\n"
"v_cvt_pk_bf16_f32 v149, v166, v167\n"
"v_exp_f32 v170, v170\n"
"v_sub_f32 v252, v254, v252\n"
"v_mfma_f32_16x16x32_bf16 v[152:155], v[220:223], a[12:15], v[152:155]\n"
"ds_write_b128 v248, v[0:3] offset:49152\n"
"v_sub_f32 v250, v254, v250\n"
"v_exp_f32 v250, v250\n"
"v_mul_f32_dpp v50, v247, v50 row_newbcast:2 row_mask:0xf bank_mask:0xf\n"
"v_mfma_f32_16x16x32_bf16 v[156:159], v[216:219], a[24:27], v[184:187]\n"
"ds_read_b64_tr_b16 a[64:65], v249 offset:50176\n"
"ds_read_b128 v[56:59], v244 offset:34816\n"
"v_mfma_f32_16x16x32_bf16 v[156:159], v[220:223], a[28:31], v[156:159]\n"
"ds_read_b64_tr_b16 a[80:81], v249 offset:51200\n"
"v_mul_f32_dpp v17, v246, v17 row_newbcast:1 row_mask:0xf bank_mask:0xf\n"
"v_mfma_f32_16x16x32_bf16 v[156:159], v[208:211], a[16:19], v[156:159]\n"
"ds_read_b64_tr_b16 a[246:247], v234 offset:4096\n"
"v_cvt_pk_bf16_f32 v134, v168, v169\n"
"v_mfma_f32_16x16x32_bf16 v[156:159], v[212:215], a[20:23], v[156:159]\n"
"ds_read_b64_tr_b16 a[48:49], v249 offset:49152\n"
"ds_read_b64_tr_b16 a[50:51], v249 offset:49160\n"
"ds_read_b64_tr_b16 a[66:67], v249 offset:50184\n"
"ds_read_b64_tr_b16 a[82:83], v249 offset:51208\n"
"v_exp_f32 v176, v176\n"
"v_exp_f32 v252, v252\n"
"v_exp_f32 v153, v153\n"
"v_mfma_f32_16x16x32_bf16 v[160:163], v[208:211], a[32:35], v[188:191]\n"
"v_cvt_pk_bf16_f32 v142, v172, v173\n"
"v_accvgpr_read_b32 v254, a208\n"
"v_mfma_f32_16x16x32_bf16 v[160:163], v[212:215], a[36:39], v[160:163]\n"
"ds_read_b64_tr_b16 a[250:251], v238 offset:4096\n"
"v_cvt_pk_bf16_f32 v148, v164, v165\n"
"v_mfma_f32_16x16x32_bf16 v[160:163], v[216:219], a[40:43], v[160:163]\n"
"ds_read_b64_tr_b16 a[252:253], v235 offset:0\n"
"ds_read_b64_tr_b16 a[254:255], v235 offset:4096\n"
"v_mfma_f32_16x16x32_bf16 v[160:163], v[220:223], a[44:47], v[160:163]\n"
"v_exp_f32 v159, v159\n"
"v_mfma_f32_16x16x32_bf16 v[164:167], v[216:219], a[200:203], v[192:195]\n"
"ds_read_b64_tr_b16 v[226:227], v239 offset:4096\n"
"v_mul_f32 v253, v253, v252\n"
"v_mfma_f32_16x16x32_bf16 v[164:167], v[220:223], a[204:207], v[164:167]\n"
"v_cvt_pk_bf16_f32 v135, v170, v171\n"
"v_fmac_f32 v253, v251, v250\n"
"v_mfma_f32_16x16x32_bf16 v[164:167], v[208:211], a[192:195], v[164:167]\n"
"ds_read_b128 v[220:223], v229 offset:21504\n"
"v_cvt_pk_bf16_f32 v150, v176, v177\n"
"v_mfma_f32_16x16x32_bf16 v[164:167], v[212:215], a[196:199], v[164:167]\n"
"s_waitcnt lgkmcnt(5)\n"
"ds_read_b64_tr_b16 v[224:225], v239 offset:0\n"
"v_mfma_f32_16x16x32_bf16 a[216:219], v[124:127], a[244:247], 0\n"
"v_rcp_f32 v253, v253\n"
"v_mfma_f32_16x16x32_bf16 v[8:11], v[128:131], a[244:247], 0\n"
"s_waitcnt lgkmcnt(5)\n"
"s_add_u32 m0, s44, 98304\n"
"v_exp_f32 v154, v154\n"
"v_mfma_f32_16x16x32_bf16 v[8:11], v[132:135], a[248:251], v[8:11]\n"
"buffer_load_dwordx4 v240, s[20:23], s56 offen nt lds\n"
"v_cvt_pk_bf16_f32 v143, v174, v175\n"
"v_mul_f32 v250, v250, v253\n"
"v_mul_f32_dpp v49, v247, v49 row_newbcast:1 row_mask:0xf bank_mask:0xf\n"
"v_exp_f32 v152, v152\n"
"v_exp_f32 v164, v164\n"
"v_mfma_f32_16x16x32_bf16 v[40:43], v[136:139], a[244:247], 0\n"
"v_mul_f32 v252, v252, v253\n"
"v_mul_f32_dpp v16, v246, v16 row_newbcast:0 row_mask:0xf bank_mask:0xf\n"
"v_mfma_f32_16x16x32_bf16 v[40:43], v[140:143], a[248:251], v[40:43]\n"
"v_cvt_pk_bf16_f32 v151, v178, v179\n"
"v_mov_b32_dpp v250, v250 row_ror:12 row_mask:0x2 bank_mask:0xf\n"
"v_mov_b32_dpp v252, v252 row_ror:12 row_mask:0x2 bank_mask:0xf\n"
"v_accvgpr_read_b32 v253, a210\n"
"v_mul_f32_dpp v22, v246, v22 row_newbcast:2 row_mask:0xf bank_mask:0xf\n"
"v_exp_f32 v155, v155\n"
"v_exp_f32 v162, v162\n"
"v_mfma_f32_16x16x32_bf16 v[72:75], v[144:147], a[244:247], 0\n"
"ds_read_b128 v[212:215], v229 offset:20480\n"
"v_cvt_pk_bf16_f32 v97, v154, v155\n"
"v_mfma_f32_16x16x32_bf16 v[72:75], v[148:151], a[248:251], v[72:75]\n"
"s_waitcnt lgkmcnt(1)\n"
"ds_read_b64_tr_b16 a[246:247], v234 offset:5120\n"
"ds_read_b64_tr_b16 a[248:249], v238 offset:1024\n"
"v_mfma_f32_16x16x32_bf16 v[76:79], v[148:151], v[224:227], 0\n"
"v_mov_b32_dpp v252, v252 row_ror:8 row_mask:0x4 bank_mask:0xf\n"
"v_mul_f32_dpp v20, v246, v20 row_newbcast:0 row_mask:0xf bank_mask:0xf\n"
"v_mfma_f32_16x16x32_bf16 v[76:79], v[144:147], a[252:255], v[76:79]\n"
"ds_read_b128 v[216:219], v228 offset:21504\n"
"v_mov_b32_dpp v250, v250 row_ror:8 row_mask:0x4 bank_mask:0xf\n"
"v_mov_b32_dpp v252, v252 row_ror:4 row_mask:0x8 bank_mask:0xf\n"
"v_mov_b64 v[196:197], 0\n"
"v_exp_f32 v156, v156\n"
"v_exp_f32 v158, v158\n"
"v_exp_f32 v165, v165\n"
"v_mfma_f32_16x16x32_bf16 v[44:47], v[140:143], v[224:227], 0\n"
"v_mov_b32_dpp v250, v250 row_ror:4 row_mask:0x8 bank_mask:0xf\n"
"v_mul_f32_dpp v18, v246, v18 row_newbcast:2 row_mask:0xf bank_mask:0xf\n"
"v_mfma_f32_16x16x32_bf16 v[44:47], v[136:139], a[252:255], v[44:47]\n"
"v_mul_f32_dpp v24, v250, v24 row_newbcast:0 row_mask:0xf bank_mask:0xf\n"
"v_accvgpr_read_b32 v255, a209\n"
"v_mul_f32_dpp v25, v250, v25 row_newbcast:1 row_mask:0xf bank_mask:0xf\n"
"v_mul_f32_dpp v26, v250, v26 row_newbcast:2 row_mask:0xf bank_mask:0xf\n"
"v_mul_f32_dpp v19, v246, v19 row_newbcast:3 row_mask:0xf bank_mask:0xf\n"
"v_mul_f32_dpp v80, v206, v80 row_newbcast:0 row_mask:0xf bank_mask:0xf\n"
"v_mul_f32_dpp v85, v206, v85 row_newbcast:1 row_mask:0xf bank_mask:0xf\n"
"v_mul_f32_dpp v59, v250, v59 row_newbcast:3 row_mask:0xf bank_mask:0xf\n"
"v_mul_f32_dpp v61, v250, v61 row_newbcast:1 row_mask:0xf bank_mask:0xf\n"
"v_mov_b64 v[200:201], 0\n"
"v_mfma_f32_16x16x32_bf16 v[12:15], v[132:135], v[224:227], 0\n"
"s_add_u32 s40, s56, s52\n"
"ds_read_b128 v[208:211], v228 offset:20480\n"
"v_mul_f32_dpp v30, v250, v30 row_newbcast:2 row_mask:0xf bank_mask:0xf\n"
"v_mfma_f32_16x16x32_bf16 v[12:15], v[128:131], a[252:255], v[12:15]\n"
"buffer_load_dwordx4 v240, s[20:23], s40 offen offset:1024 nt lds\n"
"v_accvgpr_read_b32 v251, a211\n"
"v_mul_f32_dpp v27, v250, v27 row_newbcast:3 row_mask:0xf bank_mask:0xf\n"
"v_fmac_f32_dpp v24, v252, v254 row_newbcast:0 row_mask:0xf bank_mask:0xf\n"
"v_fmac_f32_dpp v25, v252, v255 row_newbcast:1 row_mask:0xf bank_mask:0xf\n"
"v_fmac_f32_dpp v26, v252, v253 row_newbcast:2 row_mask:0xf bank_mask:0xf\n"
"v_accvgpr_read_b32 v253, a214\n"
"v_cvt_pk_bf16_f32 v16, v16, v17\n"
"v_mfma_f32_16x16x32_bf16 a[220:223], v[124:127], a[252:255], 0\n"
"v_mul_f32_dpp v53, v247, v53 row_newbcast:1 row_mask:0xf bank_mask:0xf\n"
"v_mul_f32_dpp v57, v250, v57 row_newbcast:1 row_mask:0xf bank_mask:0xf\n"
"v_smfmac_f32_16x16x64_bf16 v[196:199], a[240:243], v[128:135], v241\n"
"v_fmac_f32_dpp v27, v252, v251 row_newbcast:3 row_mask:0xf bank_mask:0xf\n"
"v_accvgpr_read_b32 v254, a212\n"
"v_mul_f32_dpp v28, v250, v28 row_newbcast:0 row_mask:0xf bank_mask:0xf\n"
"v_accvgpr_read_b32 v255, a213\n"
"v_accvgpr_read_b32 v251, a215\n"
"v_mul_f32_dpp v31, v250, v31 row_newbcast:3 row_mask:0xf bank_mask:0xf\n"
"v_cvt_pk_bf16_f32 v24, v24, v25\n"
"v_mul_f32_dpp v54, v247, v54 row_newbcast:2 row_mask:0xf bank_mask:0xf\n"
"v_mul_f32_dpp v58, v250, v58 row_newbcast:2 row_mask:0xf bank_mask:0xf\n"
"v_mul_f32_dpp v60, v250, v60 row_newbcast:0 row_mask:0xf bank_mask:0xf\n"
"v_mfma_f32_16x16x32_bf16 v[202:205], a[240:243], v[124:127], v[202:205]\n"
"s_waitcnt lgkmcnt(1)\n"
"ds_read_b64_tr_b16 a[244:245], v234 offset:1024\n"
"v_mul_f32_dpp v29, v250, v29 row_newbcast:1 row_mask:0xf bank_mask:0xf\n"
"v_fmac_f32_dpp v28, v252, v254 row_newbcast:0 row_mask:0xf bank_mask:0xf\n"
"v_fmac_f32_dpp v30, v252, v253 row_newbcast:2 row_mask:0xf bank_mask:0xf\n"
"v_mul_f32_dpp v56, v250, v56 row_newbcast:0 row_mask:0xf bank_mask:0xf\n"
"v_mul_f32_dpp v63, v250, v63 row_newbcast:3 row_mask:0xf bank_mask:0xf\n"
"v_exp_f32 v157, v157\n"
"v_exp_f32 v160, v160\n"
"v_mfma_f32_16x16x32_bf16 v[168:171], v[216:219], a[8:11], v[180:183]\n"
"v_fmac_f32_dpp v31, v252, v251 row_newbcast:3 row_mask:0xf bank_mask:0xf\n"
"v_mul_f32_dpp v83, v206, v83 row_newbcast:3 row_mask:0xf bank_mask:0xf\n"
"v_mfma_f32_16x16x32_bf16 v[168:171], v[220:223], a[12:15], v[168:171]\n"
"s_waitcnt lgkmcnt(1)\n"
"ds_read_b64_tr_b16 a[250:251], v238 offset:5120\n"
"v_accvgpr_read_b32 v251, a227\n"
"v_mfma_f32_16x16x32_bf16 v[168:171], v[208:211], a[0:3], v[168:171]\n"
"v_fmac_f32_dpp v29, v252, v255 row_newbcast:1 row_mask:0xf bank_mask:0xf\n"
"v_accvgpr_read_b32 v255, a225\n"
"v_mfma_f32_16x16x32_bf16 v[168:171], v[212:215], a[4:7], v[168:171]\n"
"ds_read_b64_tr_b16 v[226:227], v239 offset:5120\n"
"v_accvgpr_read_b32 v254, a224\n"
"v_fmac_f32_dpp v57, v252, v255 row_newbcast:1 row_mask:0xf bank_mask:0xf\n"
"v_fmac_f32_dpp v59, v252, v251 row_newbcast:3 row_mask:0xf bank_mask:0xf\n"
"v_cvt_pk_bf16_f32 v96, v152, v153\n"
"v_exp_f32 v161, v161\n"
"v_exp_f32 v163, v163\n"
"v_mfma_f32_16x16x32_bf16 v[172:175], v[208:211], a[16:19], v[184:187]\n"
"v_accvgpr_read_b32 v253, a226\n"
"v_accvgpr_read_b32 v255, a229\n"
"v_mfma_f32_16x16x32_bf16 v[172:175], v[212:215], a[20:23], v[172:175]\n"
"ds_read_b64_tr_b16 v[224:225], v239 offset:1024\n"
"v_fmac_f32_dpp v58, v252, v253 row_newbcast:2 row_mask:0xf bank_mask:0xf\n"
"v_mfma_f32_16x16x32_bf16 v[172:175], v[216:219], a[24:27], v[172:175]\n"
"s_add_u32 s40, s56, s53\n"
"v_fmac_f32_dpp v56, v252, v254 row_newbcast:0 row_mask:0xf bank_mask:0xf\n"
"v_accvgpr_read_b32 v251, a231\n"
"v_mfma_f32_16x16x32_bf16 v[172:175], v[220:223], a[28:31], v[172:175]\n"
"buffer_load_dwordx4 v240, s[20:23], s40 offen offset:2048 nt lds\n"
"v_cvt_pk_bf16_f32 v25, v26, v27\n"
"v_cvt_pk_bf16_f32 v26, v28, v29\n"
"v_cvt_pk_bf16_f32 v27, v30, v31\n"
"v_mul_f32_dpp v82, v206, v82 row_newbcast:2 row_mask:0xf bank_mask:0xf\n"
"v_mul_f32_dpp v62, v250, v62 row_newbcast:2 row_mask:0xf bank_mask:0xf\n"
"v_cvt_pk_bf16_f32 v56, v56, v57\n"
"v_exp_f32 v170, v170\n"
"v_mfma_f32_16x16x32_bf16 v[176:179], v[216:219], a[40:43], v[188:191]\n"
"v_accvgpr_read_b32 v253, a230\n"
"v_cvt_pk_bf16_f32 v113, v162, v163\n"
"v_mfma_f32_16x16x32_bf16 v[176:179], v[220:223], a[44:47], v[176:179]\n"
"v_accvgpr_read_b32 v254, a228\n"
"v_cvt_pk_bf16_f32 v57, v58, v59\n"
"v_mfma_f32_16x16x32_bf16 v[176:179], v[208:211], a[32:35], v[176:179]\n"
"ds_read_b64_tr_b16 a[254:255], v235 offset:5120\n"
"v_fmac_f32_dpp v63, v252, v251 row_newbcast:3 row_mask:0xf bank_mask:0xf\n"
"v_mfma_f32_16x16x32_bf16 v[176:179], v[212:215], a[36:39], v[176:179]\n"
"ds_write_b128 v243, v[24:27] offset:0\n"
"v_cvt_pk_bf16_f32 v80, v80, v81\n"
"v_fmac_f32_dpp v60, v252, v254 row_newbcast:0 row_mask:0xf bank_mask:0xf\n"
"v_fmac_f32_dpp v61, v252, v255 row_newbcast:1 row_mask:0xf bank_mask:0xf\n"
"v_fmac_f32_dpp v62, v252, v253 row_newbcast:2 row_mask:0xf bank_mask:0xf\n"
"v_cvt_pk_bf16_f32 v59, v62, v63\n"
"v_mfma_f32_16x16x32_bf16 v[152:155], v[208:211], a[192:195], v[192:195]\n"
"v_cvt_pk_bf16_f32 v48, v48, v49\n"
"v_cvt_pk_bf16_f32 v58, v60, v61\n"
"v_mfma_f32_16x16x32_bf16 v[152:155], v[212:215], a[196:199], v[152:155]\n"
"ds_write_b128 v243, v[56:59] offset:2048\n"
"v_exp_f32 v167, v167\n"
"v_exp_f32 v171, v171\n"
"v_mfma_f32_16x16x32_bf16 v[152:155], v[216:219], a[200:203], v[152:155]\n"
"v_mul_f32_dpp v86, v206, v86 row_newbcast:2 row_mask:0xf bank_mask:0xf\n"
"v_cvt_pk_bf16_f32 v112, v160, v161\n"
"v_mfma_f32_16x16x32_bf16 v[152:155], v[220:223], a[204:207], v[152:155]\n"
"s_waitcnt lgkmcnt(3)\n"
"ds_read_b64_tr_b16 a[252:253], v235 offset:1024\n"
"v_mul_f32_dpp v55, v247, v55 row_newbcast:3 row_mask:0xf bank_mask:0xf\n"
"v_mfma_f32_16x16x32_bf16 a[232:235], v[124:127], a[244:247], 0\n"
"ds_read_b128 v[208:211], v230 offset:16384\n"
"v_cvt_pk_bf16_f32 v81, v82, v83\n"
"v_mfma_f32_16x16x32_bf16 v[24:27], v[128:131], a[244:247], 0\n"
"s_add_u32 s40, s56, s54\n"
"v_exp_f32 v166, v166\n"
"v_mfma_f32_16x16x32_bf16 v[24:27], v[132:135], a[248:251], v[24:27]\n"
"buffer_load_dwordx4 v240, s[20:23], s40 offen offset:3072 nt lds\n"
"v_mfma_f32_16x16x32_bf16 v[56:59], v[136:139], a[244:247], 0\n"
"s_add_u32 s55, s55, s59\n"
"v_mul_f32_dpp v51, v247, v51 row_newbcast:3 row_mask:0xf bank_mask:0xf\n"
"v_cvt_pk_bf16_f32 v105, v158, v159\n"
"v_mfma_f32_16x16x32_bf16 v[56:59], v[140:143], a[248:251], v[56:59]\n"
"s_add_u32 s41, s55, s57\n"
"v_exp_f32 v168, v168\n"
"v_mfma_f32_16x16x32_bf16 v[88:91], v[144:147], a[244:247], 0\n"
"s_and_b32 s41, s41, s58\n"
"v_exp_f32 v172, v172\n"
"v_mfma_f32_16x16x32_bf16 v[88:91], v[148:151], a[248:251], v[88:91]\n"
"s_andn2_b32 s56, s55, s58\n"
"ds_read_b128 v[212:215], v231 offset:16384\n"
"v_cvt_pk_bf16_f32 v104, v156, v157\n"
"v_mfma_f32_16x16x32_bf16 v[92:95], v[148:151], v[224:227], 0\n"
"s_waitcnt lgkmcnt(2)\n"
"s_or_b32 s56, s56, s41\n"
"v_exp_f32 v169, v169\n"
"v_mfma_f32_16x16x32_bf16 v[92:95], v[144:147], a[252:255], v[92:95]\n"
"ds_read_b128 v[216:219], v230 offset:17408\n"
"v_cvt_pk_bf16_f32 v49, v50, v51\n"
"v_mfma_f32_16x16x32_bf16 v[60:63], v[140:143], v[224:227], 0\n"
"s_add_u32 m0, s44, 49152\n"
"ds_read_b128 v[220:223], v231 offset:17408\n"
"v_mul_f32_dpp v21, v246, v21 row_newbcast:1 row_mask:0xf bank_mask:0xf\n"
"v_mfma_f32_16x16x32_bf16 v[60:63], v[136:139], a[252:255], v[60:63]\n"
"buffer_load_dwordx4 v240, s[16:19], s56 offen nt lds\n"
"v_mfma_f32_16x16x32_bf16 v[28:31], v[132:135], v[224:227], 0\n"
"v_xor_b32 v248, s75, v248\n"
"v_cvt_pk_bf16_f32 v121, v166, v167\n"
"v_mfma_f32_16x16x32_bf16 v[28:31], v[128:131], a[252:255], v[28:31]\n"
"v_cvt_pk_bf16_f32 v17, v18, v19\n"
"v_cvt_pk_bf16_f32 v120, v164, v165\n"
"v_mfma_f32_16x16x32_bf16 a[236:239], v[124:127], a[252:255], 0\n"
"ds_read_b64_tr_b16 a[248:249], v236 offset:0\n"
"v_exp_f32 v173, v173\n"
"v_exp_f32 v174, v174\n"
"v_exp_f32 v178, v178\n"
"v_smfmac_f32_16x16x64_bf16 v[198:201], a[240:243], v[136:143], v241\n"
"s_waitcnt lgkmcnt(3)\n"
"ds_read_b64_tr_b16 v[224:225], v237 offset:0\n"
"v_mul_f32_dpp v23, v246, v23 row_newbcast:3 row_mask:0xf bank_mask:0xf\n"
"v_mfma_f32_16x16x32_bf16 v[156:159], v[208:211], a[0:3], v[180:183]\n"
"ds_read_b64_tr_b16 a[244:245], v232 offset:0\n"
"ds_read_b64_tr_b16 v[226:227], v237 offset:4096\n"
"v_mfma_f32_16x16x32_bf16 v[156:159], v[212:215], a[4:7], v[156:159]\n"
"s_waitcnt lgkmcnt(5)\n"
"v_cvt_pk_bf16_f32 v18, v20, v21\n"
"v_cvt_pk_bf16_f32 v106, v172, v173\n"
"v_mfma_f32_16x16x32_bf16 v[156:159], v[216:219], a[8:11], v[156:159]\n"
"s_waitcnt lgkmcnt(4)\n"
"ds_read_b64_tr_b16 a[254:255], v233 offset:4096\n"
"v_cvt_pk_bf16_f32 v98, v168, v169\n"
"v_mfma_f32_16x16x32_bf16 v[156:159], v[220:223], a[12:15], v[156:159]\n"
"ds_read_b64_tr_b16 a[246:247], v232 offset:4096\n"
"v_mul_f32_dpp v52, v247, v52 row_newbcast:0 row_mask:0xf bank_mask:0xf\n"
"v_mfma_f32_16x16x32_bf16 v[160:163], v[216:219], a[24:27], v[184:187]\n"
"v_exp_f32 v155, v155\n"
"v_mfma_f32_16x16x32_bf16 v[160:163], v[220:223], a[28:31], v[160:163]\n"
"ds_read_b64_tr_b16 a[250:251], v236 offset:4096\n"
"v_cvt_pk_bf16_f32 v19, v22, v23\n"
"v_mfma_f32_16x16x32_bf16 v[160:163], v[208:211], a[16:19], v[160:163]\n"
"s_add_u32 s40, s56, s52\n"
"v_xor_b32 v249, s75, v249\n"
"v_mul_f32_dpp v87, v206, v87 row_newbcast:3 row_mask:0xf bank_mask:0xf\n"
"v_mfma_f32_16x16x32_bf16 v[160:163], v[212:215], a[20:23], v[160:163]\n"
"buffer_load_dwordx4 v240, s[16:19], s40 offen offset:1024 nt lds\n"
"v_mfma_f32_16x16x32_bf16 v[164:167], v[208:211], a[32:35], v[188:191]\n"
"v_exp_f32 v158, v158\n"
"v_mfma_f32_16x16x32_bf16 v[164:167], v[212:215], a[36:39], v[164:167]\n"
"v_cvt_pk_bf16_f32 v51, v54, v55\n"
"v_cvt_pk_bf16_f32 v99, v170, v171\n"
"v_mfma_f32_16x16x32_bf16 v[164:167], v[216:219], a[40:43], v[164:167]\n"
"ds_read_b64_tr_b16 a[252:253], v233 offset:0\n"
"v_cvt_pk_bf16_f32 v50, v52, v53\n"
"v_mfma_f32_16x16x32_bf16 v[164:167], v[220:223], a[44:47], v[164:167]\n"
"s_waitcnt lgkmcnt(2)\n"
"ds_write_b128 v248, v[16:19] offset:49152\n"
"v_exp_f32 v177, v177\n"
"v_exp_f32 v153, v153\n"
"v_mfma_f32_16x16x32_bf16 a[208:211], v[124:127], a[244:247], 0\n"
"ds_write_b128 v248, v[48:51] offset:50176\n"
"v_exp_f32 v154, v154\n"
"v_mfma_f32_16x16x32_bf16 v[0:3], v[128:131], a[244:247], 0\n"
"s_waitcnt lgkmcnt(3)\n"
"v_exp_f32 v179, v179\n"
"v_mfma_f32_16x16x32_bf16 v[0:3], v[132:135], a[248:251], v[0:3]\n"
"ds_read_b128 v[208:211], v230 offset:20480\n"
"v_cvt_pk_bf16_f32 v83, v86, v87\n"
"v_mfma_f32_16x16x32_bf16 v[32:35], v[136:139], a[244:247], 0\n"
"s_add_u32 s40, s56, s53\n"
"ds_read_b128 v[216:219], v230 offset:21504\n"
"v_cvt_pk_bf16_f32 v82, v84, v85\n"
"v_mfma_f32_16x16x32_bf16 v[32:35], v[140:143], a[248:251], v[32:35]\n"
"buffer_load_dwordx4 v240, s[16:19], s40 offen offset:2048 nt lds\n"
"v_mfma_f32_16x16x32_bf16 v[64:67], v[144:147], a[244:247], 0\n"
"v_exp_f32 v166, v166\n"
"v_mfma_f32_16x16x32_bf16 v[64:67], v[148:151], a[248:251], v[64:67]\n"
"v_exp_f32 v163, v163\n"
"v_mfma_f32_16x16x32_bf16 v[68:71], v[148:151], v[224:227], 0\n"
"s_waitcnt lgkmcnt(3)\n"
"ds_read_b64_tr_b16 a[58:59], v249 offset:49160\n"
"ds_read_b128 v[212:215], v231 offset:20480\n"
"v_mfma_f32_16x16x32_bf16 v[68:71], v[144:147], a[252:255], v[68:71]\n"
"v_exp_f32 v152, v152\n"
"v_mfma_f32_16x16x32_bf16 v[36:39], v[140:143], v[224:227], 0\n"
"v_exp_f32 v162, v162\n"
"v_mfma_f32_16x16x32_bf16 v[36:39], v[136:139], a[252:255], v[36:39]\n"
"ds_read_b128 v[220:223], v231 offset:21504\n"
"v_exp_f32 v175, v175\n"
"v_exp_f32 v176, v176\n"
"v_exp_f32 v164, v164\n"
"v_exp_f32 v165, v165\n"
"v_mfma_f32_16x16x32_bf16 v[4:7], v[132:135], v[224:227], 0\n"
"ds_read_b64_tr_b16 a[56:57], v249 offset:49152\n"
"ds_read_b64_tr_b16 a[248:249], v236 offset:1024\n"
"v_mfma_f32_16x16x32_bf16 v[4:7], v[128:131], a[252:255], v[4:7]\n"
"ds_read_b64_tr_b16 a[74:75], v249 offset:50184\n"
"v_cvt_pk_bf16_f32 v107, v174, v175\n"
"v_mfma_f32_16x16x32_bf16 a[212:215], v[124:127], a[252:255], 0\n"
"s_add_u32 s40, s56, s54\n"
"ds_write_b128 v248, v[80:83] offset:51200\n"
"v_exp_f32 v161, v161\n"
"v_smfmac_f32_16x16x64_bf16 v[200:203], a[240:243], v[144:151], v241\n"
"s_waitcnt lgkmcnt(7)\n"
"buffer_load_dwordx4 v240, s[16:19], s40 offen offset:3072 nt lds\n"
"v_mfma_f32_16x16x32_bf16 v[168:171], v[208:211], a[0:3], v[180:183]\n"
"s_waitcnt lgkmcnt(5)\n"
"v_cvt_pk_bf16_f32 v114, v176, v177\n"
"v_add_u32 v243, 0x1000, v243\n"
"v_mfma_f32_16x16x32_bf16 v[168:171], v[212:215], a[4:7], v[168:171]\n"
"ds_read_b64_tr_b16 a[88:89], v249 offset:51200\n"
"ds_read_b64_tr_b16 a[244:245], v232 offset:1024\n"
"v_mfma_f32_16x16x32_bf16 v[168:171], v[216:219], a[8:11], v[168:171]\n"
"s_waitcnt lgkmcnt(6)\n"
"ds_read_b64_tr_b16 a[72:73], v249 offset:50176\n"
"ds_read_b64_tr_b16 a[90:91], v249 offset:51208\n"
"v_mfma_f32_16x16x32_bf16 v[168:171], v[220:223], a[12:15], v[168:171]\n"
"v_exp_f32 v167, v167\n"
"v_mfma_f32_16x16x32_bf16 v[172:175], v[216:219], a[24:27], v[184:187]\n"
"ds_read_b64_tr_b16 a[246:247], v232 offset:5120\n"
"v_cvt_pk_bf16_f32 v117, v166, v167\n"
"v_mfma_f32_16x16x32_bf16 v[172:175], v[220:223], a[28:31], v[172:175]\n"
"ds_read_b64_tr_b16 a[250:251], v236 offset:5120\n"
"v_cvt_pk_bf16_f32 v115, v178, v179\n"
"v_mfma_f32_16x16x32_bf16 v[172:175], v[208:211], a[16:19], v[172:175]\n"
"ds_read_b64_tr_b16 a[254:255], v233 offset:5120\n"
"ds_read_b64_tr_b16 v[224:225], v237 offset:1024\n"
"v_mfma_f32_16x16x32_bf16 v[172:175], v[212:215], a[20:23], v[172:175]\n"
"s_cbranch_vccnz LPCT1_%=\nLPKT1_%=:\n"
"v_exp_f32 v160, v160\n"
"v_mfma_f32_16x16x32_bf16 v[176:179], v[208:211], a[32:35], v[188:191]\n"
"ds_read_b64_tr_b16 a[252:253], v233 offset:1024\n"
"v_cvt_pk_bf16_f32 v116, v164, v165\n"
"v_mfma_f32_16x16x32_bf16 v[176:179], v[212:215], a[36:39], v[176:179]\n"
"v_exp_f32 v159, v159\n"
"v_mfma_f32_16x16x32_bf16 v[176:179], v[216:219], a[40:43], v[176:179]\n"
"v_exp_f32 v157, v157\n"
"v_mfma_f32_16x16x32_bf16 v[176:179], v[220:223], a[44:47], v[176:179]\n"
"s_waitcnt lgkmcnt(4)\n"
"v_cvt_pk_bf16_f32 v101, v158, v159\n"
"v_mfma_f32_16x16x32_bf16 a[224:227], v[124:127], a[244:247], 0\n"
"ds_read_b64_tr_b16 v[226:227], v237 offset:5120\n"
"v_cvt_pk_bf16_f32 v122, v152, v153\n"
"v_mfma_f32_16x16x32_bf16 v[16:19], v[128:131], a[244:247], 0\n"
"s_waitcnt vmcnt(8) lgkmcnt(4)\n"
"v_exp_f32 v156, v156\n"
"v_mfma_f32_16x16x32_bf16 v[16:19], v[132:135], a[248:251], v[16:19]\n"
"s_barrier\n"
"ds_read_b128 v[208:211], v228 offset:32768\n"
"v_exp_f32 v175, v175\n"
"v_mfma_f32_16x16x32_bf16 v[48:51], v[136:139], a[244:247], 0\n"
"ds_read_b128 v[220:223], v229 offset:33792\n"
"v_cvt_pk_bf16_f32 v100, v156, v157\n"
"v_mfma_f32_16x16x32_bf16 v[48:51], v[140:143], a[248:251], v[48:51]\n"
"ds_read_b128 v[212:215], v229 offset:32768\n"
"ds_read_b128 v[216:219], v228 offset:33792\n"
"v_mfma_f32_16x16x32_bf16 v[80:83], v[144:147], a[244:247], 0\n"
"ds_read_b64_tr_b16 a[244:245], v234 offset:16384\n"
"ds_read_b64_tr_b16 a[246:247], v234 offset:20480\n"
"v_mfma_f32_16x16x32_bf16 v[80:83], v[148:151], a[248:251], v[80:83]\n"
"s_waitcnt lgkmcnt(6)\n"
"ds_read_b64_tr_b16 a[248:249], v238 offset:16384\n"
"ds_read_b64_tr_b16 a[250:251], v238 offset:20480\n"
"v_mfma_f32_16x16x32_bf16 v[84:87], v[148:151], v[224:227], 0\n"
"v_xor_b32 v248, s75, v248\n"
"v_cvt_pk_bf16_f32 v108, v160, v161\n"
"v_mfma_f32_16x16x32_bf16 v[84:87], v[144:147], a[252:255], v[84:87]\n"
"v_exp_f32 v173, v173\n"
"v_mfma_f32_16x16x32_bf16 v[52:55], v[140:143], v[224:227], 0\n"
"v_exp_f32 v172, v172\n"
"v_mfma_f32_16x16x32_bf16 v[52:55], v[136:139], a[252:255], v[52:55]\n"
"v_cvt_pk_bf16_f32 v123, v154, v155\n"
"v_cvt_pk_bf16_f32 v109, v162, v163\n"
"v_mfma_f32_16x16x32_bf16 v[20:23], v[132:135], v[224:227], 0\n"
"ds_read_b64_tr_b16 v[224:225], v239 offset:16384\n"
"v_xor_b32 v249, s75, v249\n"
"v_mfma_f32_16x16x32_bf16 v[20:23], v[128:131], a[252:255], v[20:23]\n"
"v_exp_f32 v169, v169\n"
"v_mfma_f32_16x16x32_bf16 a[228:231], v[124:127], a[252:255], 0\n"
"s_waitcnt lgkmcnt(3)\n"
"ds_read_b64_tr_b16 a[254:255], v235 offset:20480\n"
"s_branch LAY2_%=\n"
"LAX_%=:\n"
"v_mfma_f32_16x16x32_bf16 a[224:227], v[124:127], a[244:247], a[224:227]\n"
"v_exp_f32 v169, v169\n"
"v_mfma_f32_16x16x32_bf16 v[16:19], v[128:131], a[244:247], v[16:19]\n"
"s_waitcnt vmcnt(8)\n"
"v_exp_f32 v171, v171\n"
"v_mfma_f32_16x16x32_bf16 v[16:19], v[132:135], a[248:251], v[16:19]\n"
"s_barrier\n"
"v_mfma_f32_16x16x32_bf16 v[48:51], v[136:139], a[244:247], v[48:51]\n"
"v_cvt_pk_bf16_f32 v108, v160, v161\n"
"v_cvt_pk_bf16_f32 v109, v162, v163\n"
"v_mfma_f32_16x16x32_bf16 v[48:51], v[140:143], a[248:251], v[48:51]\n"
"v_exp_f32 v168, v168\n"
"v_exp_f32 v170, v170\n"
"v_exp_f32 v177, v177\n"
"v_exp_f32 v179, v179\n"
"v_and_b32 v252, 0x7fff0000, v252\n"
"v_mfma_f32_16x16x32_bf16 v[80:83], v[144:147], a[244:247], v[80:83]\n"
"ds_read_b64_tr_b16 a[246:247], v234 offset:53248\n"
"v_cvt_pk_bf16_f32 v100, v156, v157\n"
"v_mfma_f32_16x16x32_bf16 v[80:83], v[148:151], a[248:251], v[80:83]\n"
"ds_read_b64_tr_b16 a[244:245], v234 offset:49152\n"
"v_accvgpr_read_b32 v253, a96\n"
"v_mfma_f32_16x16x32_bf16 v[84:87], v[148:151], v[224:227], v[84:87]\n"
"ds_read_b64_tr_b16 a[248:249], v238 offset:49152\n"
"v_cvt_pk_bf16_f32 v102, v168, v169\n"
"v_mfma_f32_16x16x32_bf16 v[84:87], v[144:147], a[252:255], v[84:87]\n"
"v_cvt_pk_bf16_f32 v116, v164, v165\n"
"v_cvt_pk_bf16_f32 v103, v170, v171\n"
"v_mfma_f32_16x16x32_bf16 v[52:55], v[140:143], v[224:227], v[52:55]\n"
"ds_read_b64_tr_b16 a[250:251], v238 offset:53248\n"
"v_lshlrev_b32_e64 v244, 24, s43\n"
"v_mfma_f32_16x16x32_bf16 v[52:55], v[136:139], a[252:255], v[52:55]\n"
"v_exp_f32 v172, v172\n"
"v_mfma_f32_16x16x32_bf16 v[20:23], v[132:135], v[224:227], v[20:23]\n"
"v_exp_f32 v176, v176\n"
"v_mfma_f32_16x16x32_bf16 v[20:23], v[128:131], a[252:255], v[20:23]\n"
"v_exp_f32 v174, v174\n"
"v_mfma_f32_16x16x32_bf16 a[228:231], v[124:127], a[252:255], a[228:231]\n"
"s_waitcnt lgkmcnt(2)\n"
"ds_read_b64_tr_b16 a[254:255], v235 offset:53248\n"
"v_cvt_pk_bf16_f32 v117, v166, v167\n"
"v_exp_f32 v173, v173\n"
"v_exp_f32 v175, v175\n"
"v_exp_f32 v178, v178\n"
"v_and_b32 v245, 0x3f0, v242\n"
"v_mfma_f32_16x16x32_bf16 a[216:219], v[120:123], a[244:247], a[216:219]\n"
"s_mov_b64 s[28:29], s[8:9]\n"
"v_cvt_pk_bf16_f32 v118, v176, v177\n"
"v_max_u32_dpp v252, v252, v252 quad_perm:[1,0,3,2] row_mask:0xf bank_mask:0xf\n"
"v_smfmac_f32_16x16x64_bf16 v[196:199], a[240:243], v[96:103], v241\n"
"s_mov_b32 s30, 655360\n"
"ds_read_b64_tr_b16 v[224:225], v239 offset:49152\n"
"v_cvt_pk_bf16_f32 v119, v178, v179\n"
"v_mfma_f32_16x16x32_bf16 v[8:11], v[96:99], a[244:247], v[8:11]\n"
"s_waitcnt lgkmcnt(2)\n"
"s_mov_b32 s71, s44\n"
"ds_read_b64_tr_b16 v[226:227], v239 offset:53248\n"
"v_cvt_pk_bf16_f32 v111, v174, v175\n"
"v_mfma_f32_16x16x32_bf16 v[8:11], v[100:103], a[248:251], v[8:11]\n"
"s_mov_b32 s70, s95\n"
"v_cvt_pk_bf16_f32 v110, v172, v173\n"
"v_max_u32_dpp v252, v252, v252 quad_perm:[2,3,0,1] row_mask:0xf bank_mask:0xf\n"
"v_smfmac_f32_16x16x64_bf16 v[200:203], a[240:243], v[112:119], v241\n"
"ds_read_b64_tr_b16 a[252:253], v235 offset:49152\n"
"v_lshrrev_b32 v245, 1, v245\n"
"v_mfma_f32_16x16x32_bf16 v[40:43], v[104:107], a[244:247], v[40:43]\n"
"v_and_b32 v253, 0x7fff0000, v253\n"
"v_mul_f32 v244, 0x45800000, v244\n"
"v_mfma_f32_16x16x32_bf16 v[40:43], v[108:111], a[248:251], v[40:43]\n"
"s_add_u32 s41, s70, 327680\n"
"s_add_u32 m0, s71, 32768\n"
"v_max_u32_dpp v253, v253, v253 quad_perm:[1,0,3,2] row_mask:0xf bank_mask:0xf\n"
"v_smfmac_f32_16x16x64_bf16 v[198:201], a[240:243], v[104:111], v241\n"
"buffer_load_dwordx4 v240, s[28:31], s41 offen lds\n"
"v_mfma_f32_16x16x32_bf16 v[72:75], v[112:115], a[244:247], v[72:75]\n"
"v_max_u32_dpp v253, v253, v253 quad_perm:[2,3,0,1] row_mask:0xf bank_mask:0xf\n"
"v_max_u32_dpp v252, v252, v252 row_half_mirror row_mask:0xf bank_mask:0xf\n"
"v_mfma_f32_16x16x32_bf16 v[72:75], v[116:119], a[248:251], v[72:75]\n"
"ds_read_b64_tr_b16 a[246:247], v234 offset:54272\n"
"ds_read_b64_tr_b16 a[248:249], v238 offset:50176\n"
"v_mfma_f32_16x16x32_bf16 v[202:205], a[240:243], v[120:123], v[202:205]\n"
"s_waitcnt lgkmcnt(3)\n"
"ds_read_b64_tr_b16 a[250:251], v238 offset:54272\n"
"v_add_f32 v196, v196, v197\n"
"v_mfma_f32_16x16x32_bf16 v[76:79], v[116:119], v[224:227], v[76:79]\n"
"s_waitcnt lgkmcnt(3)\n"
"s_add_u32 s40, s41, s52\n"
"ds_read_b64_tr_b16 a[244:245], v234 offset:50176\n"
"v_add_f32 v200, v200, v201\n"
"v_mfma_f32_16x16x32_bf16 v[76:79], v[112:115], a[252:255], v[76:79]\n"
"buffer_load_dwordx4 v240, s[28:31], s40 offen offset:1024 lds\n"
"v_mfma_f32_16x16x32_bf16 v[44:47], v[108:111], v[224:227], v[44:47]\n"
"v_min_u32_dpp v252, v252, v252 row_mirror row_mask:0xf bank_mask:0xf\n"
"v_add_f32 v244, v244, v202\n"
"v_mfma_f32_16x16x32_bf16 v[44:47], v[104:107], a[252:255], v[44:47]\n"
"v_add_f32 v198, v198, v199\n"
"v_min3_f32 v244, v244, v196, v198\n"
"v_mfma_f32_16x16x32_bf16 v[12:15], v[100:103], v[224:227], v[12:15]\n"
"s_add_u32 s40, s41, s53\n"
"ds_read_b64_tr_b16 v[226:227], v239 offset:54272\n"
"v_min_f32 v244, v244, v200\n"
"v_mfma_f32_16x16x32_bf16 v[12:15], v[96:99], a[252:255], v[12:15]\n"
"buffer_load_dwordx4 v240, s[28:31], s40 offen offset:2048 lds\n"
"v_max_u32_dpp v253, v253, v253 row_half_mirror row_mask:0xf bank_mask:0xf\n"
"v_add_f32 v254, v196, v198\n"
"v_add_f32 v254, v254, v200\n"
"v_add_f32 v254, v254, v202\n"
"v_rcp_f32 v244, v244\n"
"v_mfma_f32_16x16x32_bf16 a[220:223], v[120:123], a[252:255], a[220:223]\n"
"s_waitcnt lgkmcnt(1)\n"
"v_min_u32_dpp v253, v253, v253 row_mirror row_mask:0xf bank_mask:0xf\n"
"v_fmac_f32 v254, 0x68800000, v244\n"
"v_cmp_nge_f32 vcc, s45, v254\n"
"v_sub_u32 v245, v242, v245\n"
"v_bfe_u32 v254, v242, 9, 1\n"
"s_cbranch_vccnz LSLOW_%=\nLBACK3_%=:\ns_mov_b64 vcc, s[64:65]\n"
"v_mfma_f32_16x16x32_bf16 a[232:235], v[120:123], a[244:247], a[232:235]\n"
"ds_read_b64_tr_b16 a[252:253], v235 offset:50176\n"
"ds_read_b64_tr_b16 a[254:255], v235 offset:54272\n"
"ds_read_b64_tr_b16 v[224:225], v239 offset:50176\n"
"v_rcp_f32 v246, v196\n"
"v_rcp_f32 v247, v198\n"
"v_xor_b32 v244, 0x11000, v242\n"
"v_lshl_add_u32 v254, v254, 4, s43\n"
"v_mfma_f32_16x16x32_bf16 v[24:27], v[96:99], a[244:247], v[24:27]\n"
"v_rcp_f32 v206, v200\n"
"v_mfma_f32_16x16x32_bf16 v[24:27], v[100:103], a[248:251], v[24:27]\n"
"ds_write_b128 v244, a[220:223] offset:33792\n"
"v_min_u32_dpp v253, v253, v253 row_bcast:15 row_mask:0xa bank_mask:0xf\n"
"v_mov_b32_dpp v246, v246 row_ror:12 row_mask:0x2 bank_mask:0xf\n"
"v_mfma_f32_16x16x32_bf16 v[56:59], v[104:107], a[244:247], v[56:59]\n"
"s_add_u32 s40, s41, s54\n"
"v_mov_b32_dpp v247, v247 row_ror:12 row_mask:0x2 bank_mask:0xf\n"
"v_mov_b32_dpp v206, v206 row_ror:12 row_mask:0x2 bank_mask:0xf\n"
"v_mfma_f32_16x16x32_bf16 v[56:59], v[108:111], a[248:251], v[56:59]\n"
"buffer_load_dwordx4 v240, s[28:31], s40 offen offset:3072 lds\n"
"v_mfma_f32_16x16x32_bf16 v[88:91], v[112:115], a[244:247], v[88:91]\n"
"s_add_u32 s71, s44, s75\n"
"v_mov_b32_dpp v247, v247 row_ror:8 row_mask:0x4 bank_mask:0xf\n"
"v_mov_b32_dpp v206, v206 row_ror:8 row_mask:0x4 bank_mask:0xf\n"
"v_mfma_f32_16x16x32_bf16 v[88:91], v[116:119], a[248:251], v[88:91]\n"
"s_waitcnt lgkmcnt(1)\n"
"s_mov_b32 s70, s100\n"
"ds_read_b64_tr_b16 a[246:247], v232 offset:53248\n"
"ds_read_b64_tr_b16 a[248:249], v236 offset:49152\n"
"ds_read_b64_tr_b16 a[250:251], v236 offset:53248\n"
"v_mov_b32_dpp v246, v246 row_ror:8 row_mask:0x4 bank_mask:0xf\n"
"v_mov_b32_dpp v247, v247 row_ror:4 row_mask:0x8 bank_mask:0xf\n"
"v_mov_b32_dpp v206, v206 row_ror:4 row_mask:0x8 bank_mask:0xf\n"
"v_mfma_f32_16x16x32_bf16 v[92:95], v[116:119], v[224:227], v[92:95]\n"
"s_add_u32 s41, s70, 163840\n"
"s_add_u32 m0, s71, 16384\n"
"ds_read_b64_tr_b16 a[244:245], v232 offset:49152\n"
"v_mfma_f32_16x16x32_bf16 v[92:95], v[112:115], a[252:255], v[92:95]\n"
"buffer_load_dwordx4 v240, s[28:31], s41 offen lds\n"
"v_mov_b32_dpp v246, v246 row_ror:4 row_mask:0x8 bank_mask:0xf\n"
"v_mul_f32_dpp v41, v247, v41 row_newbcast:1 row_mask:0xf bank_mask:0xf\n"
"v_mul_f32_dpp v72, v206, v72 row_newbcast:0 row_mask:0xf bank_mask:0xf\n"
"v_mul_f32_dpp v77, v206, v77 row_newbcast:1 row_mask:0xf bank_mask:0xf\n"
"v_mfma_f32_16x16x32_bf16 v[60:63], v[108:111], v[224:227], v[60:63]\n"
"v_min_u32_dpp v252, v252, v252 row_bcast:15 row_mask:0xa bank_mask:0xf\n"
"v_mul_f32_dpp v76, v206, v76 row_newbcast:0 row_mask:0xf bank_mask:0xf\n"
"v_mfma_f32_16x16x32_bf16 v[60:63], v[104:107], a[252:255], v[60:63]\n"
"ds_write_b128 v244, a[232:235] offset:34816\n"
"v_min_u32_dpp v252, v252, v252 row_bcast:31 row_mask:0xc bank_mask:0xf\n"
"v_mul_f32 v252, 0x43340000, v252\n"
"v_lshl_add_u32 v254, v254, 6, s75\n"
"v_mul_f32_dpp v40, v247, v40 row_newbcast:0 row_mask:0xf bank_mask:0xf\n"
"v_mul_f32_dpp v24, v246, v24 row_newbcast:0 row_mask:0xf bank_mask:0xf\n"
"v_mfma_f32_16x16x32_bf16 v[28:31], v[100:103], v[224:227], v[28:31]\n"
"s_add_u32 s40, s41, s52\n"
"v_mul_f32_dpp v15, v246, v15 row_newbcast:3 row_mask:0xf bank_mask:0xf\n"
"v_mul_f32_dpp v73, v206, v73 row_newbcast:1 row_mask:0xf bank_mask:0xf\n"
"v_mfma_f32_16x16x32_bf16 v[28:31], v[96:99], a[252:255], v[28:31]\n"
"buffer_load_dwordx4 v240, s[28:31], s40 offen offset:1024 lds\n"
"v_min_u32_dpp v253, v253, v253 row_bcast:31 row_mask:0xc bank_mask:0xf\n"
"v_sub_u32 v229, v233, v254\n"
"v_mul_f32_dpp v9, v246, v9 row_newbcast:1 row_mask:0xf bank_mask:0xf\n"
"v_mul_f32_dpp v10, v246, v10 row_newbcast:2 row_mask:0xf bank_mask:0xf\n"
"v_mul_f32_dpp v12, v246, v12 row_newbcast:0 row_mask:0xf bank_mask:0xf\n"
"v_mfma_f32_16x16x32_bf16 a[236:239], v[120:123], a[252:255], a[236:239]\n"
"s_waitcnt lgkmcnt(1)\n"
"v_mul_f32_dpp v47, v247, v47 row_newbcast:3 row_mask:0xf bank_mask:0xf\n"
"v_mul_f32_dpp v90, v206, v90 row_newbcast:2 row_mask:0xf bank_mask:0xf\n"
"v_mfma_f32_16x16x32_bf16 a[208:211], v[120:123], a[244:247], a[208:211]\n"
"ds_write_b128 v244, a[216:219] offset:32768\n"
"v_sub_u32 v230, v234, v254\n"
"v_mul_f32_dpp v43, v247, v43 row_newbcast:3 row_mask:0xf bank_mask:0xf\n"
"v_mul_f32_dpp v45, v247, v45 row_newbcast:1 row_mask:0xf bank_mask:0xf\n"
"v_mul_f32_dpp v79, v206, v79 row_newbcast:3 row_mask:0xf bank_mask:0xf\n"
"v_cvt_pk_bf16_f32 v72, v72, v73\n"
"v_mfma_f32_16x16x32_bf16 v[0:3], v[96:99], a[244:247], v[0:3]\n"
"ds_read_b64_tr_b16 a[252:253], v233 offset:49152\n"
"v_mul_f32 v252, v252, v253\n"
"v_mfma_f32_16x16x32_bf16 v[0:3], v[100:103], a[248:251], v[0:3]\n"
"ds_read_b64_tr_b16 a[254:255], v233 offset:53248\n"
"ds_read_b64_tr_b16 v[224:225], v237 offset:49152\n"
"ds_read_b64_tr_b16 v[226:227], v237 offset:53248\n"
"v_sub_u32 v228, v232, v254\n"
"v_mul_f32_dpp v8, v246, v8 row_newbcast:0 row_mask:0xf bank_mask:0xf\n"
"v_mul_f32_dpp v44, v247, v44 row_newbcast:0 row_mask:0xf bank_mask:0xf\n"
"v_mul_f32_dpp v46, v247, v46 row_newbcast:2 row_mask:0xf bank_mask:0xf\n"
"v_mul_f32_dpp v27, v246, v27 row_newbcast:3 row_mask:0xf bank_mask:0xf\n"
"v_mul_f32_dpp v61, v247, v61 row_newbcast:1 row_mask:0xf bank_mask:0xf\n"
"v_mfma_f32_16x16x32_bf16 v[32:35], v[104:107], a[244:247], v[32:35]\n"
"s_add_u32 s40, s41, s53\n"
"v_mul_f32_dpp v14, v246, v14 row_newbcast:2 row_mask:0xf bank_mask:0xf\n"
"v_mul_f32_dpp v56, v247, v56 row_newbcast:0 row_mask:0xf bank_mask:0xf\n"
"v_mfma_f32_16x16x32_bf16 v[32:35], v[108:111], a[248:251], v[32:35]\n"
"buffer_load_dwordx4 v240, s[28:31], s40 offen offset:2048 lds\n"
"v_mul_f32_dpp v42, v247, v42 row_newbcast:2 row_mask:0xf bank_mask:0xf\n"
"v_mul_f32_dpp v75, v206, v75 row_newbcast:3 row_mask:0xf bank_mask:0xf\n"
"v_mul_f32_dpp v78, v206, v78 row_newbcast:2 row_mask:0xf bank_mask:0xf\n"
"v_mul_f32_dpp v57, v247, v57 row_newbcast:1 row_mask:0xf bank_mask:0xf\n"
"v_mul_f32_dpp v58, v247, v58 row_newbcast:2 row_mask:0xf bank_mask:0xf\n"
"v_cvt_pk_bf16_f32 v56, v56, v57\n"
"v_mfma_f32_16x16x32_bf16 v[64:67], v[112:115], a[244:247], v[64:67]\n"
"v_mul_f32_dpp v74, v206, v74 row_newbcast:2 row_mask:0xf bank_mask:0xf\n"
"v_mul_f32_dpp v28, v246, v28 row_newbcast:0 row_mask:0xf bank_mask:0xf\n"
"v_mfma_f32_16x16x32_bf16 v[64:67], v[116:119], a[248:251], v[64:67]\n"
"s_waitcnt lgkmcnt(0)\n"
"ds_write_b128 v244, a[236:239] offset:35840\n"
"v_min_f32 v252, 0x71800000, v252\n"
"v_cvt_pk_bf16_f32 v73, v74, v75\n"
"v_mul_f32_dpp v25, v246, v25 row_newbcast:1 row_mask:0xf bank_mask:0xf\n"
"v_mul_f32_dpp v63, v247, v63 row_newbcast:3 row_mask:0xf bank_mask:0xf\n"
"v_mfma_f32_16x16x32_bf16 v[68:71], v[116:119], v[224:227], v[68:71]\n"
"s_add_u32 s40, s41, s54\n"
"ds_read_b64_tr_b16 a[246:247], v232 offset:54272\n"
"v_mul_f32_dpp v60, v247, v60 row_newbcast:0 row_mask:0xf bank_mask:0xf\n"
"v_mfma_f32_16x16x32_bf16 v[68:71], v[112:115], a[252:255], v[68:71]\n"
"buffer_load_dwordx4 v240, s[28:31], s40 offen offset:3072 lds\n"
"v_readlane_b32 s22, v252, 63\n"
"v_xor_b32 v250, 0x1000, v245\n"
"v_mul_f32_dpp v11, v246, v11 row_newbcast:3 row_mask:0xf bank_mask:0xf\n"
"v_cvt_pk_bf16_f32 v8, v8, v9\n"
"v_cvt_pk_bf16_f32 v40, v40, v41\n"
"v_mul_f32_dpp v29, v246, v29 row_newbcast:1 row_mask:0xf bank_mask:0xf\n"
"v_mul_f32_dpp v62, v247, v62 row_newbcast:2 row_mask:0xf bank_mask:0xf\n"
"v_mul_f32_dpp v33, v247, v33 row_newbcast:1 row_mask:0xf bank_mask:0xf\n"
"v_mfma_f32_16x16x32_bf16 v[36:39], v[108:111], v[224:227], v[36:39]\n"
"v_readlane_b32 s23, v252, 63\n"
"v_mul_f32_dpp v67, v206, v67 row_newbcast:3 row_mask:0xf bank_mask:0xf\n"
"v_mfma_f32_16x16x32_bf16 v[36:39], v[104:107], a[252:255], v[36:39]\n"
"ds_read_b64_tr_b16 a[244:245], v232 offset:50176\n"
"ds_read_b64_tr_b16 a[248:249], v236 offset:50176\n"
"ds_read_b64_tr_b16 a[250:251], v236 offset:54272\n"
"v_mov_b32 v252, v192\n"
"v_mov_b32 v253, v202\n"
"v_sub_u32 v231, v235, v254\n"
"v_mul_f32_dpp v13, v246, v13 row_newbcast:1 row_mask:0xf bank_mask:0xf\n"
"v_mul_f32_dpp v59, v247, v59 row_newbcast:3 row_mask:0xf bank_mask:0xf\n"
"v_mul_f32_dpp v64, v206, v64 row_newbcast:0 row_mask:0xf bank_mask:0xf\n"
"v_xor_b32 v244, 0x1000, v244\n"
"v_mfma_f32_16x16x32_bf16 v[4:7], v[100:103], v[224:227], v[4:7]\n"
"ds_read_b64_tr_b16 v[224:225], v237 offset:50176\n"
"v_cvt_pk_bf16_f32 v9, v10, v11\n"
"v_mfma_f32_16x16x32_bf16 v[4:7], v[96:99], a[252:255], v[4:7]\n"
"ds_write_b64 v250, v[252:253] offset:52224\n"
"v_cvt_pk_bf16_f32 v10, v12, v13\n"
"v_cvt_pk_bf16_f32 v41, v42, v43\n"
"v_cvt_pk_bf16_f32 v43, v46, v47\n"
"v_cvt_pk_bf16_f32 v74, v76, v77\n"
"v_cvt_pk_bf16_f32 v57, v58, v59\n"
"v_mul_f32_dpp v92, v206, v92 row_newbcast:0 row_mask:0xf bank_mask:0xf\n"
"v_mul_f32_dpp v65, v206, v65 row_newbcast:1 row_mask:0xf bank_mask:0xf\n"
"v_mfma_f32_16x16x32_bf16 a[212:215], v[120:123], a[252:255], a[212:215]\n"
"s_waitcnt lgkmcnt(4)\n"
"ds_read_b64_tr_b16 a[252:253], v233 offset:50176\n"
"ds_read_b64_tr_b16 a[254:255], v233 offset:54272\n"
"ds_read_b64_tr_b16 v[226:227], v237 offset:54272\n"
"v_cvt_pk_bf16_f32 v42, v44, v45\n"
"v_cvt_pk_bf16_f32 v59, v62, v63\n"
"v_mul_f32_dpp v88, v206, v88 row_newbcast:0 row_mask:0xf bank_mask:0xf\n"
"v_mul_f32_dpp v94, v206, v94 row_newbcast:2 row_mask:0xf bank_mask:0xf\n"
"v_mul_f32_dpp v69, v206, v69 row_newbcast:1 row_mask:0xf bank_mask:0xf\n"
"v_mul_f32_dpp v71, v206, v71 row_newbcast:3 row_mask:0xf bank_mask:0xf\n"
"v_add_u32 v236, s76, v249\n"
"v_mfma_f32_16x16x32_bf16 a[224:227], v[120:123], a[244:247], a[224:227]\n"
"ds_write_b128 v248, v[40:43] offset:50176\n"
"v_cvt_pk_bf16_f32 v11, v14, v15\n"
"v_mul_f32_dpp v91, v206, v91 row_newbcast:3 row_mask:0xf bank_mask:0xf\n"
"v_mul_f32_dpp v32, v247, v32 row_newbcast:0 row_mask:0xf bank_mask:0xf\n"
"v_mul_f32_dpp v70, v206, v70 row_newbcast:2 row_mask:0xf bank_mask:0xf\n"
"v_mfma_f32_16x16x32_bf16 v[16:19], v[96:99], a[244:247], v[16:19]\n"
"s_waitcnt vmcnt(8) lgkmcnt(1)\n"
"ds_write_b128 v248, v[8:11] offset:49152\n"
"v_mul_f32_dpp v31, v246, v31 row_newbcast:3 row_mask:0xf bank_mask:0xf\n"
"v_mul_f32_dpp v35, v247, v35 row_newbcast:3 row_mask:0xf bank_mask:0xf\n"
"v_cvt_pk_bf16_f32 v64, v64, v65\n"
"v_xor_b32 v232, 0x1000, v228\n"
"v_xor_b32 v237, 0x400, v236\n"
"v_mfma_f32_16x16x32_bf16 v[16:19], v[100:103], a[248:251], v[16:19]\n"
"s_barrier\n"
"ds_read_b64 v[250:251], v245 offset:52224\n"
"ds_read_b64_tr_b16 v[184:185], v232 offset:0\n"
"ds_read_b64_tr_b16 v[192:193], v232 offset:8192\n"
"ds_read_b64_tr_b16 a[200:201], v236 offset:2048\n"
"v_cvt_pk_bf16_f32 v24, v24, v25\n"
"v_mul_f32_dpp v93, v206, v93 row_newbcast:1 row_mask:0xf bank_mask:0xf\n"
"v_mul_f32_dpp v0, v246, v0 row_newbcast:0 row_mask:0xf bank_mask:0xf\n"
"v_mul_f32_dpp v6, v246, v6 row_newbcast:2 row_mask:0xf bank_mask:0xf\n"
"v_mfma_f32_16x16x32_bf16 v[48:51], v[104:107], a[244:247], v[48:51]\n"
"ds_read_b64_tr_b16 a[4:5], v249 offset:49152\n"
"ds_read_b64_tr_b16 v[188:189], v228 offset:8192\n"
"v_mfma_f32_16x16x32_bf16 v[48:51], v[108:111], a[248:251], v[48:51]\n"
"ds_read_b64_tr_b16 a[6:7], v249 offset:49160\n"
"ds_read_b64_tr_b16 a[20:21], v249 offset:50176\n"
"ds_read_b64_tr_b16 a[22:23], v249 offset:50184\n"
"ds_read_b64_tr_b16 v[210:211], v228 offset:18432\n"
"v_cvt_pk_bf16_f32 v75, v78, v79\n"
"v_mul_f32_dpp v30, v246, v30 row_newbcast:2 row_mask:0xf bank_mask:0xf\n"
"v_mul_f32_dpp v89, v206, v89 row_newbcast:1 row_mask:0xf bank_mask:0xf\n"
"v_mul_f32_dpp v2, v246, v2 row_newbcast:2 row_mask:0xf bank_mask:0xf\n"
"v_mul_f32_dpp v38, v247, v38 row_newbcast:2 row_mask:0xf bank_mask:0xf\n"
"v_mfma_f32_16x16x32_bf16 v[80:83], v[112:115], a[244:247], v[80:83]\n"
"ds_read_b64_tr_b16 v[182:183], v228 offset:2048\n"
"ds_read_b64_tr_b16 a[242:243], v236 offset:4104\n"
"v_mfma_f32_16x16x32_bf16 v[80:83], v[116:119], a[248:251], v[80:83]\n"
"ds_write_b128 v248, v[72:75] offset:51200\n"
"v_mul_f32_dpp v26, v246, v26 row_newbcast:2 row_mask:0xf bank_mask:0xf\n"
"v_cvt_pk_bf16_f32 v88, v88, v89\n"
"v_mul_f32_dpp v39, v247, v39 row_newbcast:3 row_mask:0xf bank_mask:0xf\n"
"v_mov_b64 v[198:199], s[22:23]\n"
"v_mfma_f32_16x16x32_bf16 v[84:87], v[116:119], v[224:227], v[84:87]\n"
"ds_read_b64_tr_b16 v[180:181], v228 offset:0\n"
"ds_read_b64_tr_b16 v[190:191], v228 offset:10240\n"
"ds_read_b64_tr_b16 v[212:213], v232 offset:16384\n"
"ds_read_b64_tr_b16 v[220:221], v232 offset:24576\n"
"v_cvt_pk_bf16_f32 v25, v26, v27\n"
"v_cvt_pk_bf16_f32 v89, v90, v91\n"
"v_mul_f32_dpp v1, v246, v1 row_newbcast:1 row_mask:0xf bank_mask:0xf\n"
"v_mul_f32_dpp v4, v246, v4 row_newbcast:0 row_mask:0xf bank_mask:0xf\n"
"v_xor_b32 v234, 0x1000, v230\n"
"v_mul_f32_dpp v19, v246, v19 row_newbcast:3 row_mask:0xf bank_mask:0xf\n"
"v_mfma_f32_16x16x32_bf16 v[84:87], v[112:115], a[252:255], v[84:87]\n"
"s_waitcnt lgkmcnt(15)\n"
"ds_read_b64_tr_b16 a[38:39], v249 offset:51208\n"
"ds_read_b64_tr_b16 v[194:195], v232 offset:10240\n"
"ds_read_b64_tr_b16 v[208:209], v228 offset:16384\n"
"v_cvt_pk_bf16_f32 v26, v28, v29\n"
"v_cvt_pk_bf16_f32 v27, v30, v31\n"
"v_cvt_pk_bf16_f32 v58, v60, v61\n"
"v_cvt_pk_bf16_f32 v90, v92, v93\n"
"v_min_f32 v254, v252, v250\n"
"v_mov_b64 v[196:197], s[22:23]\n"
"v_mfma_f32_16x16x32_bf16 v[52:55], v[108:111], v[224:227], v[52:55]\n"
"ds_read_b64_tr_b16 v[186:187], v232 offset:2048\n"
"ds_read_b64_tr_b16 v[216:217], v228 offset:24576\n"
"ds_read_b64_tr_b16 v[222:223], v232 offset:26624\n"
"ds_read_b64_tr_b16 a[248:249], v236 offset:6144\n"
"v_mul_f32_dpp v95, v206, v95 row_newbcast:3 row_mask:0xf bank_mask:0xf\n"
"v_mul_f32_dpp v3, v246, v3 row_newbcast:3 row_mask:0xf bank_mask:0xf\n"
"v_mul_f32_dpp v37, v247, v37 row_newbcast:1 row_mask:0xf bank_mask:0xf\n"
"v_mul_f32_dpp v66, v206, v66 row_newbcast:2 row_mask:0xf bank_mask:0xf\n"
"v_xor_b32 v233, 0x1000, v229\n"
"v_mul_f32_dpp v48, v247, v48 row_newbcast:0 row_mask:0xf bank_mask:0xf\n"
"v_mfma_f32_16x16x32_bf16 v[52:55], v[104:107], a[252:255], v[52:55]\n"
"ds_read_b128 v[60:63], v244 offset:35840\n"
"ds_read_b64_tr_b16 v[214:215], v232 offset:18432\n"
"v_mfma_f32_16x16x32_bf16 v[20:23], v[100:103], v[224:227], v[20:23]\n"
"ds_write_b128 v248, v[24:27] offset:49152\n"
"v_mul_f32_dpp v7, v246, v7 row_newbcast:3 row_mask:0xf bank_mask:0xf\n"
"v_mul_f32_dpp v36, v247, v36 row_newbcast:0 row_mask:0xf bank_mask:0xf\n"
"v_cvt_pk_bf16_f32 v32, v32, v33\n"
"v_mul_f32_dpp v49, v247, v49 row_newbcast:1 row_mask:0xf bank_mask:0xf\n"
"v_mfma_f32_16x16x32_bf16 v[20:23], v[96:99], a[252:255], v[20:23]\n"
"ds_read_b64_tr_b16 a[36:37], v249 offset:51200\n"
"ds_read_b64_tr_b16 a[12:13], v249 offset:49152\n"
"ds_read_b64_tr_b16 a[206:207], v237 offset:2056\n"
"ds_read_b64_tr_b16 v[110:111], v233 offset:10240\n"
"v_cvt_pk_bf16_f32 v91, v94, v95\n"
"v_mul_f32_dpp v5, v246, v5 row_newbcast:1 row_mask:0xf bank_mask:0xf\n"
"v_mul_f32_dpp v34, v247, v34 row_newbcast:2 row_mask:0xf bank_mask:0xf\n"
"v_cvt_pk_bf16_f32 v65, v66, v67\n"
"v_cvt_pk_bf16_f32 v67, v70, v71\n"
"v_sub_f32 v250, v254, v250\n"
"v_mfma_f32_16x16x32_bf16 a[228:231], v[120:123], a[252:255], a[228:231]\n"
"s_waitcnt lgkmcnt(15)\n"
"ds_write_b128 v248, v[88:91] offset:51200\n"
"v_cvt_pk_bf16_f32 v0, v0, v1\n"
"v_cvt_pk_bf16_f32 v33, v34, v35\n"
"v_cvt_pk_bf16_f32 v35, v38, v39\n"
"v_mul_f32_dpp v68, v206, v68 row_newbcast:0 row_mask:0xf bank_mask:0xf\n"
"v_mul_f32_dpp v51, v247, v51 row_newbcast:3 row_mask:0xf bank_mask:0xf\n"
"v_mfma_f32_16x16x32_bf16 v[124:127], v[180:183], a[96:99], v[196:199]\n"
"s_waitcnt lgkmcnt(11)\n"
"ds_read_b64_tr_b16 a[44:45], v249 offset:51200\n"
"ds_read_b64_tr_b16 a[46:47], v249 offset:51208\n"
"v_mfma_f32_16x16x32_bf16 v[124:127], v[184:187], a[100:103], v[124:127]\n"
"ds_read_b64_tr_b16 v[218:219], v228 offset:26624\n"
"ds_read_b64_tr_b16 v[224:225], v233 offset:24576\n"
"v_mfma_f32_16x16x32_bf16 v[124:127], v[188:191], a[104:107], v[124:127]\n"
"ds_read_b64_tr_b16 a[14:15], v249 offset:49160\n"
"ds_read_b64_tr_b16 a[244:245], v237 offset:4096\n"
"v_mfma_f32_16x16x32_bf16 v[124:127], v[192:195], a[108:111], v[124:127]\n"
"ds_write_b128 v248, v[56:59] offset:50176\n"
"v_cvt_pk_bf16_f32 v1, v2, v3\n"
"v_cvt_pk_bf16_f32 v2, v4, v5\n"
"v_cvt_pk_bf16_f32 v34, v36, v37\n"
"v_sub_f32 v252, v254, v252\n"
"v_accvgpr_read_b32 v255, a209\n"
"v_mfma_f32_16x16x32_bf16 v[124:127], v[208:211], a[144:147], v[124:127]\n"
"s_waitcnt lgkmcnt(13)\n"
"ds_read_b64_tr_b16 a[28:29], v249 offset:50176\n"
"ds_read_b64_tr_b16 a[30:31], v249 offset:50184\n"
"v_mfma_f32_16x16x32_bf16 v[124:127], v[212:215], a[148:151], v[124:127]\n"
"s_waitcnt lgkmcnt(6)\n"
"ds_write_b128 v248, v[32:35] offset:50176\n"
"v_cvt_pk_bf16_f32 v3, v6, v7\n"
"v_cvt_pk_bf16_f32 v66, v68, v69\n"
"v_mul_f32_dpp v21, v246, v21 row_newbcast:1 row_mask:0xf bank_mask:0xf\n"
"v_mul_f32_dpp v84, v206, v84 row_newbcast:0 row_mask:0xf bank_mask:0xf\n"
"v_mfma_f32_16x16x32_bf16 v[124:127], v[216:219], a[152:155], v[124:127]\n"
"ds_read_b64_tr_b16 a[18:19], v249 offset:50184\n"
"ds_read_b128 v[56:59], v244 offset:34816\n"
"v_mfma_f32_16x16x32_bf16 v[124:127], v[220:223], a[156:159], v[124:127]\n"
"ds_write_b128 v248, v[0:3] offset:49152\n"
"ds_write_b128 v248, v[64:67] offset:51200\n"
"v_mfma_f32_16x16x32_bf16 v[140:143], v[220:223], a[172:175], v[196:199]\n"
"ds_read_b64_tr_b16 a[16:17], v249 offset:50176\n"
"ds_read_b64_tr_b16 a[32:33], v249 offset:51200\n"
"v_mfma_f32_16x16x32_bf16 v[140:143], v[216:219], a[168:171], v[140:143]\n"
"ds_read_b64_tr_b16 a[0:1], v249 offset:49152\n"
"ds_read_b64_tr_b16 v[112:113], v229 offset:16384\n"
"v_mfma_f32_16x16x32_bf16 v[140:143], v[212:215], a[164:167], v[140:143]\n"
"ds_read_b128 v[24:27], v244 offset:32768\n"
"ds_read_b128 v[28:31], v244 offset:33792\n"
"v_mfma_f32_16x16x32_bf16 v[140:143], v[208:211], a[160:163], v[140:143]\n"
"ds_read_b64_tr_b16 a[2:3], v249 offset:49160\n"
"ds_read_b64_tr_b16 a[34:35], v249 offset:51208\n"
"v_mfma_f32_16x16x32_bf16 v[140:143], v[192:195], a[124:127], v[140:143]\n"
"ds_read_b64_tr_b16 a[240:241], v236 offset:4096\n"
"v_mul_f32_dpp v52, v247, v52 row_newbcast:0 row_mask:0xf bank_mask:0xf\n"
"v_mfma_f32_16x16x32_bf16 v[140:143], v[188:191], a[120:123], v[140:143]\n"
"ds_read_b64_tr_b16 v[116:117], v233 offset:16384\n"
"ds_read_b64_tr_b16 a[250:251], v236 offset:6152\n"
"v_mfma_f32_16x16x32_bf16 v[140:143], v[184:187], a[116:119], v[140:143]\n"
"ds_read_b64_tr_b16 a[202:203], v236 offset:2056\n"
"ds_read_b64_tr_b16 a[254:255], v237 offset:6152\n"
"v_mfma_f32_16x16x32_bf16 v[140:143], v[180:183], a[112:115], v[140:143]\n"
"ds_read_b64_tr_b16 a[194:195], v236 offset:8\n"
"ds_read_b64_tr_b16 a[204:205], v237 offset:2048\n"
"ds_read_b64_tr_b16 a[252:253], v237 offset:6144\n"
"v_mfma_f32_16x16x32_bf16 v[156:159], v[180:183], a[128:131], v[196:199]\n"
"ds_read_b64_tr_b16 v[104:105], v229 offset:8192\n"
"ds_read_b64_tr_b16 a[246:247], v237 offset:4104\n"
"v_mfma_f32_16x16x32_bf16 v[156:159], v[184:187], a[132:135], v[156:159]\n"
"ds_read_b64_tr_b16 v[100:101], v233 offset:0\n"
"v_accvgpr_read_b32 v254, a208\n"
"v_mfma_f32_16x16x32_bf16 v[156:159], v[188:191], a[136:139], v[156:159]\n"
"ds_read_b64_tr_b16 a[198:199], v237 offset:8\n"
"ds_read_b64_tr_b16 v[98:99], v229 offset:2048\n"
"v_mfma_f32_16x16x32_bf16 v[156:159], v[192:195], a[140:143], v[156:159]\n"
"s_waitcnt lgkmcnt(13)\n"
"ds_read_b64_tr_b16 a[196:197], v237 offset:0\n"
"ds_read_b64_tr_b16 v[102:103], v233 offset:2048\n"
"v_mfma_f32_16x16x32_bf16 v[156:159], v[208:211], a[176:179], v[156:159]\n"
"ds_read_b64_tr_b16 v[120:121], v229 offset:24576\n"
"v_mul_f32_dpp v80, v206, v80 row_newbcast:0 row_mask:0xf bank_mask:0xf\n"
"v_mfma_f32_16x16x32_bf16 v[156:159], v[212:215], a[180:183], v[156:159]\n"
"s_add_u32 s41, s95, 491520\n"
"ds_read_b64_tr_b16 a[192:193], v236 offset:0\n"
"ds_read_b64_tr_b16 v[106:107], v229 offset:10240\n"
"v_mfma_f32_16x16x32_bf16 v[156:159], v[216:219], a[184:187], v[156:159]\n"
"s_add_u32 m0, s44, 49152\n"
"ds_read_b64_tr_b16 v[96:97], v229 offset:0\n"
"ds_read_b64_tr_b16 v[118:119], v233 offset:18432\n"
"v_mfma_f32_16x16x32_bf16 v[156:159], v[220:223], a[188:191], v[156:159]\n"
"s_waitcnt lgkmcnt(12)\n"
"buffer_load_dwordx4 v240, s[28:31], s41 offen lds\n"
"v_mfma_f32_16x16x32_bf16 v[172:175], v[220:223], a[252:255], v[196:199]\n"
"s_add_u32 s40, s41, s52\n"
"v_exp_f32 v252, v252\n"
"v_mfma_f32_16x16x32_bf16 v[172:175], v[216:219], a[248:251], v[172:175]\n"
"s_waitcnt lgkmcnt(9)\n"
"buffer_load_dwordx4 v240, s[28:31], s40 offen offset:1024 lds\n"
"v_mfma_f32_16x16x32_bf16 v[172:175], v[212:215], a[244:247], v[172:175]\n"
"s_add_u32 s40, s41, s53\n"
"ds_read_b64_tr_b16 v[108:109], v233 offset:8192\n"
"ds_read_b64_tr_b16 v[226:227], v233 offset:26624\n"
"v_mfma_f32_16x16x32_bf16 v[172:175], v[208:211], a[240:243], v[172:175]\n"
"buffer_load_dwordx4 v240, s[28:31], s40 offen offset:2048 lds\n"
"v_mfma_f32_16x16x32_bf16 v[172:175], v[192:195], a[204:207], v[172:175]\n"
"ds_read_b64_tr_b16 v[114:115], v229 offset:18432\n"
"v_mul_f32_dpp v55, v247, v55 row_newbcast:3 row_mask:0xf bank_mask:0xf\n"
"v_mfma_f32_16x16x32_bf16 v[172:175], v[188:191], a[200:203], v[172:175]\n"
"s_waitcnt lgkmcnt(5)\n"
"v_exp_f32 v250, v250\n"
"v_mfma_f32_16x16x32_bf16 v[172:175], v[184:187], a[196:199], v[172:175]\n"
"s_add_u32 s40, s41, s54\n"
"ds_read_b64_tr_b16 v[122:123], v229 offset:26624\n"
"ds_read_b64_tr_b16 v[184:185], v234 offset:0\n"
"v_mfma_f32_16x16x32_bf16 v[172:175], v[180:183], a[192:195], v[172:175]\n"
"s_waitcnt lgkmcnt(5)\n"
"buffer_load_dwordx4 v240, s[28:31], s40 offen offset:3072 lds\n"
"v_mfma_f32_16x16x32_bf16 v[128:131], v[96:99], a[96:99], v[196:199]\n"
"v_mul_f32 v253, v253, v252\n"
"v_fmac_f32 v253, v251, v250\n"
"v_mfma_f32_16x16x32_bf16 v[128:131], v[100:103], a[100:103], v[128:131]\n"
"v_mul_f32_dpp v17, v246, v17 row_newbcast:1 row_mask:0xf bank_mask:0xf\n"
"v_mul_f32_dpp v83, v206, v83 row_newbcast:3 row_mask:0xf bank_mask:0xf\n"
"v_mfma_f32_16x16x32_bf16 v[128:131], v[104:107], a[104:107], v[128:131]\n"
"s_waitcnt lgkmcnt(3)\n"
"ds_read_b64_tr_b16 a[234:235], v236 offset:10248\n"
"ds_read_b64_tr_b16 v[216:217], v230 offset:24576\n"
"v_mfma_f32_16x16x32_bf16 v[128:131], v[108:111], a[108:111], v[128:131]\n"
"s_waitcnt lgkmcnt(4)\n"
"ds_read_b64_tr_b16 v[208:209], v230 offset:16384\n"
"v_mul_f32_dpp v81, v206, v81 row_newbcast:1 row_mask:0xf bank_mask:0xf\n"
"v_mfma_f32_16x16x32_bf16 v[128:131], v[112:115], a[144:147], v[128:131]\n"
"s_add_u32 s21, s60, 32\n"
"ds_read_b64_tr_b16 v[188:189], v230 offset:8192\n"
"v_cvt_pk_bf16_f32 v80, v80, v81\n"
"v_mfma_f32_16x16x32_bf16 v[128:131], v[116:119], a[148:151], v[128:131]\n"
"s_waitcnt lgkmcnt(5)\n"
"s_mov_b32 s69, 1\n"
"ds_read_b64_tr_b16 a[238:239], v237 offset:10248\n"
"v_mul_f32_dpp v20, v246, v20 row_newbcast:0 row_mask:0xf bank_mask:0xf\n"
"v_mfma_f32_16x16x32_bf16 v[128:131], v[120:123], a[152:155], v[128:131]\n"
"v_bfe_u32 v201, v242, 2, 7\n"
"v_add_u32 v201, 0x100, v201\n"
"ds_read_b64_tr_b16 v[214:215], v234 offset:18432\n"
"v_mfma_f32_16x16x32_bf16 v[128:131], v[224:227], a[156:159], v[128:131]\n"
"global_load_dword v200, v201, s[6:7] sc1\n"
"v_mfma_f32_16x16x32_bf16 v[144:147], v[224:227], a[172:175], v[196:199]\n"
"v_rcp_f32 v253, v253\n"
"v_mfma_f32_16x16x32_bf16 v[144:147], v[120:123], a[168:171], v[144:147]\n"
"v_mul_f32 v252, v252, v253\n"
"v_mul_f32 v250, v250, v253\n"
"v_mfma_f32_16x16x32_bf16 v[144:147], v[116:119], a[164:167], v[144:147]\n"
"s_nop 0\n"
"v_mov_b32_dpp v250, v250 row_ror:12 row_mask:0x2 bank_mask:0xf\n"
"v_mov_b32_dpp v252, v252 row_ror:12 row_mask:0x2 bank_mask:0xf\n"
"v_mfma_f32_16x16x32_bf16 v[144:147], v[112:115], a[160:163], v[144:147]\n"
"v_mov_b32_dpp v250, v250 row_ror:8 row_mask:0x4 bank_mask:0xf\n"
"v_mov_b32_dpp v252, v252 row_ror:8 row_mask:0x4 bank_mask:0xf\n"
"v_mfma_f32_16x16x32_bf16 v[144:147], v[108:111], a[124:127], v[144:147]\n"
"ds_read_b64_tr_b16 v[222:223], v234 offset:26624\n"
"v_mov_b32_dpp v250, v250 row_ror:4 row_mask:0x8 bank_mask:0xf\n"
"v_mfma_f32_16x16x32_bf16 v[144:147], v[104:107], a[120:123], v[144:147]\n"
"ds_read_b64_tr_b16 a[232:233], v236 offset:10240\n"
"v_accvgpr_read_b32 v253, a210\n"
"v_mfma_f32_16x16x32_bf16 v[144:147], v[100:103], a[116:119], v[144:147]\n"
"ds_read_b64_tr_b16 v[182:183], v230 offset:2048\n"
"v_mul_f32_dpp v27, v250, v27 row_newbcast:3 row_mask:0xf bank_mask:0xf\n"
"v_mfma_f32_16x16x32_bf16 v[144:147], v[96:99], a[112:115], v[144:147]\n"
"ds_read_b64_tr_b16 v[194:195], v234 offset:10240\n"
"v_mov_b32_dpp v252, v252 row_ror:4 row_mask:0x8 bank_mask:0xf\n"
"v_mul_f32_dpp v24, v250, v24 row_newbcast:0 row_mask:0xf bank_mask:0xf\n"
"v_mul_f32_dpp v25, v250, v25 row_newbcast:1 row_mask:0xf bank_mask:0xf\n"
"v_mul_f32_dpp v26, v250, v26 row_newbcast:2 row_mask:0xf bank_mask:0xf\n"
"v_accvgpr_read_b32 v251, a211\n"
"v_mul_f32_dpp v28, v250, v28 row_newbcast:0 row_mask:0xf bank_mask:0xf\n"
"v_mul_f32_dpp v16, v246, v16 row_newbcast:0 row_mask:0xf bank_mask:0xf\n"
"v_mfma_f32_16x16x32_bf16 v[160:163], v[96:99], a[128:131], v[196:199]\n"
"ds_read_b64_tr_b16 v[210:211], v230 offset:18432\n"
"v_fmac_f32_dpp v24, v252, v254 row_newbcast:0 row_mask:0xf bank_mask:0xf\n"
"v_mfma_f32_16x16x32_bf16 v[160:163], v[100:103], a[132:135], v[160:163]\n"
"ds_read_b64_tr_b16 v[220:221], v234 offset:24576\n"
"v_fmac_f32_dpp v25, v252, v255 row_newbcast:1 row_mask:0xf bank_mask:0xf\n"
"v_mfma_f32_16x16x32_bf16 v[160:163], v[104:107], a[136:139], v[160:163]\n"
"ds_read_b64_tr_b16 v[186:187], v234 offset:2048\n"
"v_accvgpr_read_b32 v255, a213\n"
"v_mfma_f32_16x16x32_bf16 v[160:163], v[108:111], a[140:143], v[160:163]\n"
"v_xor_b32 v235, 0x1000, v231\n"
"v_mul_f32_dpp v57, v250, v57 row_newbcast:1 row_mask:0xf bank_mask:0xf\n"
"v_mfma_f32_16x16x32_bf16 v[160:163], v[112:115], a[176:179], v[160:163]\n"
"v_accvgpr_read_b32 v254, a212\n"
"v_mul_f32_dpp v56, v250, v56 row_newbcast:0 row_mask:0xf bank_mask:0xf\n"
"v_mfma_f32_16x16x32_bf16 v[160:163], v[116:119], a[180:183], v[160:163]\n"
"ds_read_b64_tr_b16 v[190:191], v230 offset:10240\n"
"ds_read_b64_tr_b16 v[192:193], v234 offset:8192\n"
"v_mfma_f32_16x16x32_bf16 v[160:163], v[120:123], a[184:187], v[160:163]\n"
"ds_read_b64_tr_b16 v[212:213], v234 offset:16384\n"
"v_fmac_f32_dpp v26, v252, v253 row_newbcast:2 row_mask:0xf bank_mask:0xf\n"
"v_mfma_f32_16x16x32_bf16 v[160:163], v[224:227], a[188:191], v[160:163]\n"
"ds_read_b64_tr_b16 a[236:237], v237 offset:10240\n"
"v_fmac_f32_dpp v27, v252, v251 row_newbcast:3 row_mask:0xf bank_mask:0xf\n"
"v_mul_f32_dpp v29, v250, v29 row_newbcast:1 row_mask:0xf bank_mask:0xf\n"
"v_accvgpr_read_b32 v253, a214\n"
"v_mul_f32_dpp v30, v250, v30 row_newbcast:2 row_mask:0xf bank_mask:0xf\n"
"v_accvgpr_read_b32 v251, a215\n"
"v_mul_f32_dpp v31, v250, v31 row_newbcast:3 row_mask:0xf bank_mask:0xf\n"
"v_fmac_f32_dpp v28, v252, v254 row_newbcast:0 row_mask:0xf bank_mask:0xf\n"
"v_mul_f32_dpp v59, v250, v59 row_newbcast:3 row_mask:0xf bank_mask:0xf\n"
"v_mfma_f32_16x16x32_bf16 v[176:179], v[224:227], a[252:255], v[196:199]\n"
"ds_read_b64_tr_b16 v[180:181], v230 offset:0\n"
"v_mul_f32_dpp v18, v246, v18 row_newbcast:2 row_mask:0xf bank_mask:0xf\n"
"v_mfma_f32_16x16x32_bf16 v[176:179], v[120:123], a[248:251], v[176:179]\n"
"ds_read_b64_tr_b16 v[224:225], v235 offset:24576\n"
"v_mov_b64 v[44:45], s[22:23]\n"
"v_mfma_f32_16x16x32_bf16 v[176:179], v[116:119], a[244:247], v[176:179]\n"
"v_cvt_pk_bf16_f32 v48, v48, v49\n"
"v_mul_f32_dpp v61, v250, v61 row_newbcast:1 row_mask:0xf bank_mask:0xf\n"
"v_mfma_f32_16x16x32_bf16 v[176:179], v[112:115], a[240:243], v[176:179]\n"
"ds_read_b64_tr_b16 a[220:221], v237 offset:8192\n"
"v_mul_f32_dpp v86, v206, v86 row_newbcast:2 row_mask:0xf bank_mask:0xf\n"
"v_mfma_f32_16x16x32_bf16 v[176:179], v[108:111], a[204:207], v[176:179]\n"
"v_fmac_f32_dpp v29, v252, v255 row_newbcast:1 row_mask:0xf bank_mask:0xf\n"
"v_accvgpr_read_b32 v254, a224\n"
"v_mfma_f32_16x16x32_bf16 v[176:179], v[104:107], a[200:203], v[176:179]\n"
"v_fmac_f32_dpp v31, v252, v251 row_newbcast:3 row_mask:0xf bank_mask:0xf\n"
"v_fmac_f32_dpp v56, v252, v254 row_newbcast:0 row_mask:0xf bank_mask:0xf\n"
"v_mfma_f32_16x16x32_bf16 v[176:179], v[100:103], a[196:199], v[176:179]\n"
"ds_read_b64_tr_b16 v[218:219], v230 offset:26624\n"
"v_mul_f32_dpp v58, v250, v58 row_newbcast:2 row_mask:0xf bank_mask:0xf\n"
"v_mfma_f32_16x16x32_bf16 v[176:179], v[96:99], a[192:195], v[176:179]\n"
"s_waitcnt lgkmcnt(3)\n"
"ds_read_b64_tr_b16 v[108:109], v235 offset:8192\n"
"v_fmac_f32_dpp v30, v252, v253 row_newbcast:2 row_mask:0xf bank_mask:0xf\n"
"v_cvt_pk_bf16_f32 v24, v24, v25\n"
"v_cvt_pk_bf16_f32 v25, v26, v27\n"
"v_mul_f32_dpp v50, v247, v50 row_newbcast:2 row_mask:0xf bank_mask:0xf\n"
"v_mul_f32_dpp v54, v247, v54 row_newbcast:2 row_mask:0xf bank_mask:0xf\n"
"v_accvgpr_read_b32 v255, a225\n"
"v_accvgpr_read_b32 v253, a226\n"
"v_mul_f32_dpp v60, v250, v60 row_newbcast:0 row_mask:0xf bank_mask:0xf\n"
"v_mfma_f32_16x16x32_bf16 v[132:135], v[180:183], a[96:99], v[196:199]\n"
"v_cvt_pk_bf16_f32 v27, v30, v31\n"
"v_accvgpr_read_b32 v251, a227\n"
"v_mfma_f32_16x16x32_bf16 v[132:135], v[184:187], a[100:103], v[132:135]\n"
"v_cvt_pk_bf16_f32 v26, v28, v29\n"
"v_fmac_f32_dpp v58, v252, v253 row_newbcast:2 row_mask:0xf bank_mask:0xf\n"
"v_mfma_f32_16x16x32_bf16 v[132:135], v[188:191], a[104:107], v[132:135]\n"
"ds_read_b64_tr_b16 v[100:101], v235 offset:0\n"
"v_accvgpr_read_b32 v254, a228\n"
"v_mfma_f32_16x16x32_bf16 v[132:135], v[192:195], a[108:111], v[132:135]\n"
"ds_read_b64_tr_b16 v[226:227], v235 offset:26624\n"
"v_mul_f32_dpp v63, v250, v63 row_newbcast:3 row_mask:0xf bank_mask:0xf\n"
"v_mfma_f32_16x16x32_bf16 v[132:135], v[208:211], a[144:147], v[132:135]\n"
"s_waitcnt vmcnt(0)\n"
"ds_read_b64_tr_b16 v[104:105], v231 offset:8192\n"
"v_cvt_pk_bf16_f32 v16, v16, v17\n"
"v_mfma_f32_16x16x32_bf16 v[132:135], v[212:215], a[148:151], v[132:135]\n"
"s_waitcnt lgkmcnt(4)\n"
"v_subrev_u32 v200, s21, v200\n"
"ds_read_b64_tr_b16 v[102:103], v235 offset:2048\n"
"v_mfma_f32_16x16x32_bf16 v[132:135], v[216:219], a[152:155], v[132:135]\n"
"v_cmp_lt_u32 vcc, s69, v200\n"
"ds_read_b64_tr_b16 v[110:111], v235 offset:10240\n"
"v_mfma_f32_16x16x32_bf16 v[132:135], v[220:223], a[156:159], v[132:135]\n"
"ds_write_b128 v243, v[24:27] offset:0\n"
"v_fmac_f32_dpp v57, v252, v255 row_newbcast:1 row_mask:0xf bank_mask:0xf\n"
"v_fmac_f32_dpp v59, v252, v251 row_newbcast:3 row_mask:0xf bank_mask:0xf\n"
"v_accvgpr_read_b32 v251, a231\n"
"v_fmac_f32_dpp v60, v252, v254 row_newbcast:0 row_mask:0xf bank_mask:0xf\n"
"s_cbranch_vccnz LGV_%=\nLGVK_%=:\n"
"v_mfma_f32_16x16x32_bf16 v[148:151], v[220:223], a[172:175], v[196:199]\n"
"v_cvt_pk_bf16_f32 v17, v18, v19\n"
"v_accvgpr_read_b32 v253, a230\n"
"v_mfma_f32_16x16x32_bf16 v[148:151], v[216:219], a[168:171], v[148:151]\n"
"v_add_u32_dpp v200, v200, v200 quad_perm:[1,0,3,2] row_mask:0xf bank_mask:0xf\n"
"ds_read_b64_tr_b16 v[98:99], v231 offset:2048\n"
"v_mfma_f32_16x16x32_bf16 v[148:151], v[212:215], a[164:167], v[148:151]\n"
"v_accvgpr_read_b32 v255, a229\n"
"v_mul_f32_dpp v62, v250, v62 row_newbcast:2 row_mask:0xf bank_mask:0xf\n"
"v_fmac_f32_dpp v63, v252, v251 row_newbcast:3 row_mask:0xf bank_mask:0xf\n"
"v_cvt_pk_bf16_f32 v56, v56, v57\n"
"v_cvt_pk_bf16_f32 v57, v58, v59\n"
"v_mfma_f32_16x16x32_bf16 v[148:151], v[208:211], a[160:163], v[148:151]\n"
"v_add_u32_dpp v200, v200, v200 quad_perm:[2,3,0,1] row_mask:0xf bank_mask:0xf\n"
"ds_read_b64_tr_b16 v[96:97], v231 offset:0\n"
"v_mfma_f32_16x16x32_bf16 v[148:151], v[192:195], a[124:127], v[148:151]\n"
"ds_read_b64_tr_b16 a[216:217], v236 offset:8192\n"
"v_fmac_f32_dpp v62, v252, v253 row_newbcast:2 row_mask:0xf bank_mask:0xf\n"
"v_mfma_f32_16x16x32_bf16 v[148:151], v[188:191], a[120:123], v[148:151]\n"
"v_cmp_lt_u32 vcc, 1, v200\n"
"v_fmac_f32_dpp v61, v252, v255 row_newbcast:1 row_mask:0xf bank_mask:0xf\n"
"v_mfma_f32_16x16x32_bf16 v[148:151], v[184:187], a[116:119], v[148:151]\n"
"v_cvt_pk_bf16_f32 v58, v60, v61\n"
"v_cvt_pk_bf16_f32 v59, v62, v63\n"
"v_mfma_f32_16x16x32_bf16 v[148:151], v[180:183], a[112:115], v[148:151]\n"
"ds_write_b128 v243, v[56:59] offset:2048\n"
"v_mul_f32_dpp v53, v247, v53 row_newbcast:1 row_mask:0xf bank_mask:0xf\n"
"v_cvt_pk_bf16_f32 v49, v50, v51\n"
"v_mfma_f32_16x16x32_bf16 v[164:167], v[180:183], a[128:131], v[196:199]\n"
"s_mov_b32 s47, 0\n"
"v_mul_f32_dpp v22, v246, v22 row_newbcast:2 row_mask:0xf bank_mask:0xf\n"
"v_cvt_pk_bf16_f32 v51, v54, v55\n"
"v_mfma_f32_16x16x32_bf16 v[164:167], v[184:187], a[132:135], v[164:167]\n"
"s_mov_b32 s89, vcc_lo\n"
"ds_read_b64_tr_b16 v[106:107], v231 offset:10240\n"
"ds_read_b64_tr_b16 v[112:113], v231 offset:16384\n"
"v_mfma_f32_16x16x32_bf16 v[164:167], v[188:191], a[136:139], v[164:167]\n"
"s_lshr_b32 s90, s89, s93\n"
"ds_read_b64_tr_b16 v[118:119], v235 offset:18432\n"
"ds_read_b64_tr_b16 v[122:123], v231 offset:26624\n"
"v_mfma_f32_16x16x32_bf16 v[164:167], v[192:195], a[140:143], v[164:167]\n"
"s_and_b32 s90, s90, 1\n"
"ds_read_b64_tr_b16 v[116:117], v235 offset:16384\n"
"v_mul_f32_dpp v82, v206, v82 row_newbcast:2 row_mask:0xf bank_mask:0xf\n"
"v_mfma_f32_16x16x32_bf16 v[164:167], v[208:211], a[176:179], v[164:167]\n"
"s_lshl_b32 s16, s61, 7\n"
"ds_read_b64_tr_b16 v[114:115], v231 offset:18432\n"
"v_cvt_pk_bf16_f32 v50, v52, v53\n"
"v_mfma_f32_16x16x32_bf16 v[164:167], v[212:215], a[180:183], v[164:167]\n"
"s_add_u32 s90, s90, s16\n"
"ds_read_b64_tr_b16 v[120:121], v231 offset:24576\n"
"v_xor_b32 v248, s75, v248\n"
"v_mfma_f32_16x16x32_bf16 v[164:167], v[216:219], a[184:187], v[164:167]\n"
"s_waitcnt vmcnt(63) lgkmcnt(7)\n"
"s_lshl_b32 s90, s90, 13\n"
"v_mul_f32_dpp v87, v206, v87 row_newbcast:3 row_mask:0xf bank_mask:0xf\n"
"v_mfma_f32_16x16x32_bf16 v[164:167], v[220:223], a[188:191], v[164:167]\n"
"s_barrier\n"
"s_lshl_b32 s16, s95, 9\n"
"ds_write_b128 v248, v[48:51] offset:50176\n"
"v_xor_b32 v249, s75, v249\n"
"v_mfma_f32_16x16x32_bf16 v[136:139], v[96:99], a[96:99], v[196:199]\n"
"s_add_u32 s16, s16, s90\n"
"ds_read_b64_tr_b16 a[24:25], v249 offset:50176\n"
"v_mul_f32_dpp v85, v206, v85 row_newbcast:1 row_mask:0xf bank_mask:0xf\n"
"v_mfma_f32_16x16x32_bf16 v[136:139], v[100:103], a[100:103], v[136:139]\n"
"s_waitcnt lgkmcnt(7)\n"
"s_add_u32 s17, s16, 4096\n"
"ds_read_b64_tr_b16 a[26:27], v249 offset:50184\n"
"ds_read_b64_tr_b16 v[188:189], v228 offset:40960\n"
"v_mfma_f32_16x16x32_bf16 v[136:139], v[104:107], a[104:107], v[136:139]\n"
"s_mov_b32 s72, s61\n"
"ds_read_b64_tr_b16 v[218:219], v228 offset:59392\n"
"v_cvt_pk_bf16_f32 v81, v82, v83\n"
"v_mfma_f32_16x16x32_bf16 v[136:139], v[108:111], a[108:111], v[136:139]\n"
"s_waitcnt lgkmcnt(6)\n"
"s_mov_b32 s21, s60\n"
"ds_read_b64_tr_b16 v[184:185], v232 offset:32768\n"
"v_cvt_pk_bf16_f32 v83, v86, v87\n"
"v_mfma_f32_16x16x32_bf16 v[136:139], v[112:115], a[144:147], v[136:139]\n"
"s_mov_b32 s18, s77\n"
"ds_read_b64_tr_b16 v[180:181], v228 offset:32768\n"
"v_cvt_pk_bf16_f32 v82, v84, v85\n"
"v_mfma_f32_16x16x32_bf16 v[136:139], v[116:119], a[148:151], v[136:139]\n"
"s_waitcnt lgkmcnt(7)\n"
"s_mov_b32 s19, s77\n"
"ds_write_b128 v248, v[80:83] offset:51200\n"
"v_cvt_pk_bf16_f32 v18, v20, v21\n"
"v_mfma_f32_16x16x32_bf16 v[136:139], v[120:123], a[152:155], v[136:139]\n"
"s_mov_b32 s69, -1\n"
"v_mul_f32_dpp v23, v246, v23 row_newbcast:3 row_mask:0xf bank_mask:0xf\n"
"v_cvt_pk_bf16_f32 v19, v22, v23\n"
"v_mfma_f32_16x16x32_bf16 v[136:139], v[224:227], a[156:159], v[136:139]\n"
"s_mul_i32 s88, s61, 0x10011111\n"
"ds_write_b128 v248, v[16:19] offset:49152\n"
"v_mfma_f32_16x16x32_bf16 v[152:155], v[224:227], a[172:175], v[196:199]\n"
"s_add_u32 s88, s88, 484675\n"
"v_mov_b64 v[20:21], s[22:23]\n"
"v_mov_b64 v[26:27], s[22:23]\n"
"v_mfma_f32_16x16x32_bf16 v[152:155], v[120:123], a[168:171], v[152:155]\n"
"s_and_b32 s88, s88, 0x70077777\n"
"ds_read_b64_tr_b16 a[10:11], v249 offset:49160\n"
"ds_read_b64_tr_b16 a[40:41], v249 offset:51200\n"
"v_mfma_f32_16x16x32_bf16 v[152:155], v[116:119], a[164:167], v[152:155]\n"
"s_or_b32 s88, s88, 0x9800000\n"
"ds_read_b64_tr_b16 a[8:9], v249 offset:49152\n"
"ds_read_b64_tr_b16 a[42:43], v249 offset:51208\n"
"v_mfma_f32_16x16x32_bf16 v[152:155], v[112:115], a[160:163], v[152:155]\n"
"s_mov_b32 s84, s6\n"
"ds_read_b64_tr_b16 v[186:187], v232 offset:34816\n"
"ds_read_b64_tr_b16 v[212:213], v232 offset:49152\n"
"v_mfma_f32_16x16x32_bf16 v[152:155], v[108:111], a[124:127], v[152:155]\n"
"s_and_b32 s85, s7, 0xffff\n"
"ds_read_b64_tr_b16 a[228:229], v237 offset:14336\n"
"v_mov_b64 v[10:11], s[22:23]\n"
"v_mfma_f32_16x16x32_bf16 v[152:155], v[104:107], a[120:123], v[152:155]\n"
"s_mov_b32 s86, 128\n"
"ds_read_b64_tr_b16 v[182:183], v228 offset:34816\n"
"ds_read_b64_tr_b16 v[194:195], v232 offset:43008\n"
"v_mfma_f32_16x16x32_bf16 v[152:155], v[100:103], a[116:119], v[152:155]\n"
"s_mov_b32 s87, 131072\n"
"ds_read_b64_tr_b16 v[216:217], v228 offset:57344\n"
"v_mfma_f32_16x16x32_bf16 v[152:155], v[96:99], a[112:115], v[152:155]\n"
"ds_read_b64_tr_b16 v[192:193], v232 offset:40960\n"
"ds_read_b64_tr_b16 v[210:211], v228 offset:51200\n"
"v_mfma_f32_16x16x32_bf16 v[168:171], v[96:99], a[128:131], v[196:199]\n"
"ds_read_b64_tr_b16 a[214:215], v237 offset:12296\n"
"ds_read_b64_tr_b16 v[214:215], v232 offset:51200\n"
"v_mfma_f32_16x16x32_bf16 v[168:171], v[100:103], a[132:135], v[168:171]\n"
"ds_read_b64_tr_b16 v[190:191], v228 offset:43008\n"
"v_mfma_f32_16x16x32_bf16 v[168:171], v[104:107], a[136:139], v[168:171]\n"
"ds_read_b64_tr_b16 v[208:209], v228 offset:49152\n"
"ds_read_b64_tr_b16 v[220:221], v232 offset:57344\n"
"v_mfma_f32_16x16x32_bf16 v[168:171], v[108:111], a[140:143], v[168:171]\n"
"s_waitcnt lgkmcnt(11)\n"
"ds_read_b64_tr_b16 v[98:99], v229 offset:34816\n"
"v_mfma_f32_16x16x32_bf16 v[168:171], v[112:115], a[176:179], v[168:171]\n"
"ds_read_b64_tr_b16 v[222:223], v232 offset:59392\n"
"ds_read_b64_tr_b16 v[104:105], v229 offset:40960\n"
"v_mfma_f32_16x16x32_bf16 v[168:171], v[116:119], a[180:183], v[168:171]\n"
"ds_read_b64_tr_b16 a[226:227], v236 offset:14344\n"
"ds_read_b64_tr_b16 v[96:97], v229 offset:32768\n"
"v_mfma_f32_16x16x32_bf16 v[168:171], v[120:123], a[184:187], v[168:171]\n"
"s_add_u32 s41, s70, 327680\n"
"s_add_u32 m0, s71, 32768\n"
"ds_read_b64_tr_b16 v[108:109], v233 offset:40960\n"
"v_mfma_f32_16x16x32_bf16 v[168:171], v[224:227], a[188:191], v[168:171]\n"
"s_waitcnt lgkmcnt(11)\n"
"buffer_load_dwordx4 v240, s[28:31], s41 offen lds\n"
"v_mfma_f32_16x16x32_bf16 v[124:127], v[180:183], a[48:51], v[124:127]\n"
"v_xor_b32 v228, s75, v228\n"
"v_mfma_f32_16x16x32_bf16 v[124:127], v[184:187], a[52:55], v[124:127]\n"
"s_waitcnt lgkmcnt(8)\n"
"v_bfe_u32 v254, v242, 4, 4\n"
"v_xor_b32 v232, s75, v232\n"
"v_mfma_f32_16x16x32_bf16 v[124:127], v[188:191], a[56:59], v[124:127]\n"
"s_add_u32 s40, s41, s52\n"
"ds_read_b64_tr_b16 a[230:231], v237 offset:14344\n"
"ds_read_b64_tr_b16 v[102:103], v233 offset:34816\n"
"v_mfma_f32_16x16x32_bf16 v[124:127], v[192:195], a[60:63], v[124:127]\n"
"s_waitcnt lgkmcnt(7)\n"
"buffer_load_dwordx4 v240, s[28:31], s40 offen offset:1024 lds\n"
"v_mfma_f32_16x16x32_bf16 v[124:127], v[208:211], a[0:3], v[124:127]\n"
"v_xor_b32 v248, s75, v248\n"
"v_mov_b64 v[24:25], s[22:23]\n"
"v_mfma_f32_16x16x32_bf16 v[124:127], v[212:215], a[4:7], v[124:127]\n"
"v_and_b32 v243, 0x3f0, v242\n"
"v_mov_b64 v[6:7], s[22:23]\n"
"v_mfma_f32_16x16x32_bf16 v[124:127], v[216:219], a[8:11], v[124:127]\n"
"s_waitcnt lgkmcnt(2)\n"
"s_add_u32 s40, s41, s53\n"
"ds_read_b64_tr_b16 a[212:213], v237 offset:12288\n"
"ds_read_b64_tr_b16 v[116:117], v233 offset:49152\n"
"v_mfma_f32_16x16x32_bf16 v[124:127], v[220:223], a[12:15], v[124:127]\n"
"buffer_load_dwordx4 v240, s[28:31], s40 offen offset:2048 lds\n"
"v_mfma_f32_16x16x32_bf16 v[140:143], v[220:223], a[28:31], v[140:143]\n"
"v_mov_b64 v[40:41], s[22:23]\n"
"v_mfma_f32_16x16x32_bf16 v[140:143], v[216:219], a[24:27], v[140:143]\n"
"v_xor_b32 v249, s75, v249\n"
"v_bfe_u32 v255, v242, 8, 1\n"
"v_mfma_f32_16x16x32_bf16 v[140:143], v[212:215], a[20:23], v[140:143]\n"
"ds_read_b64_tr_b16 a[210:211], v236 offset:12296\n"
"ds_read_b64_tr_b16 a[224:225], v236 offset:14336\n"
"v_mfma_f32_16x16x32_bf16 v[140:143], v[208:211], a[16:19], v[140:143]\n"
"ds_read_b64_tr_b16 v[110:111], v233 offset:43008\n"
"v_mov_b32 v241, s60\n"
"v_mfma_f32_16x16x32_bf16 v[140:143], v[192:195], a[76:79], v[140:143]\n"
"ds_read_b64_tr_b16 v[106:107], v229 offset:43008\n"
"ds_read_b64_tr_b16 v[224:225], v233 offset:57344\n"
"v_mfma_f32_16x16x32_bf16 v[140:143], v[188:191], a[72:75], v[140:143]\n"
"ds_read_b64_tr_b16 v[118:119], v233 offset:51200\n"
"v_mfma_f32_16x16x32_bf16 v[140:143], v[184:187], a[68:71], v[140:143]\n"
"s_add_u32 s40, s41, s54\n"
"ds_read_b64_tr_b16 a[208:209], v236 offset:12288\n"
"ds_read_b64_tr_b16 v[114:115], v229 offset:51200\n"
"v_mfma_f32_16x16x32_bf16 v[140:143], v[180:183], a[64:67], v[140:143]\n"
"buffer_load_dwordx4 v240, s[28:31], s40 offen offset:3072 lds\n"
"v_mfma_f32_16x16x32_bf16 v[156:159], v[180:183], a[80:83], v[156:159]\n"
"s_add_u32 s41, s70, 491520\n"
"s_add_u32 m0, s71, 49152\n"
"v_mov_b64 v[90:91], 0\n"
"v_mfma_f32_16x16x32_bf16 v[156:159], v[184:187], a[84:87], v[156:159]\n"
"buffer_load_dwordx4 v240, s[28:31], s41 offen lds\n"
"v_mfma_f32_16x16x32_bf16 v[156:159], v[188:191], a[88:91], v[156:159]\n"
"v_mul_u32_u24 v254, 2560, v254\n"
"v_mfma_f32_16x16x32_bf16 v[156:159], v[192:195], a[92:95], v[156:159]\n"
"v_mov_b64 v[246:247], 0\n"
"v_subrev_f32 v141, s22, v141\n"
"v_mfma_f32_16x16x32_bf16 v[156:159], v[208:211], a[32:35], v[156:159]\n"
"ds_read_b64_tr_b16 a[222:223], v237 offset:8200\n"
"ds_read_b64_tr_b16 v[100:101], v233 offset:32768\n"
"v_mfma_f32_16x16x32_bf16 v[156:159], v[212:215], a[36:39], v[156:159]\n"
"ds_read_b64_tr_b16 a[218:219], v236 offset:8200\n"
"v_subrev_f32 v140, s22, v140\n"
"v_mfma_f32_16x16x32_bf16 v[156:159], v[216:219], a[40:43], v[156:159]\n"
"s_add_u32 s40, s41, s52\n"
"ds_read_b64_tr_b16 v[112:113], v229 offset:49152\n"
"ds_read_b64_tr_b16 v[120:121], v229 offset:57344\n"
"v_mfma_f32_16x16x32_bf16 v[156:159], v[220:223], a[44:47], v[156:159]\n"
"s_waitcnt lgkmcnt(5)\n"
"buffer_load_dwordx4 v240, s[28:31], s40 offen offset:1024 lds\n"
"v_mfma_f32_16x16x32_bf16 v[172:175], v[220:223], a[228:231], v[172:175]\n"
"s_add_u32 s40, s41, s53\n"
"v_add_u32 v236, s94, v243\n"
"v_mov_b64 v[12:13], s[22:23]\n"
"v_mfma_f32_16x16x32_bf16 v[172:175], v[216:219], a[224:227], v[172:175]\n"
"buffer_load_dwordx4 v240, s[28:31], s40 offen offset:2048 lds\n"
"v_mfma_f32_16x16x32_bf16 v[172:175], v[212:215], a[212:215], v[172:175]\n"
"v_lshl_add_u32 v254, v255, 5, v254\n"
"v_subrev_f32 v124, s22, v124\n"
"v_mfma_f32_16x16x32_bf16 v[172:175], v[208:211], a[208:211], v[172:175]\n"
"v_mov_b64 v[2:3], s[22:23]\n"
"v_subrev_f32 v159, s22, v159\n"
"v_mfma_f32_16x16x32_bf16 v[172:175], v[192:195], a[236:239], v[172:175]\n"
"s_add_u32 s40, s41, s54\n"
"ds_read_b64_tr_b16 v[122:123], v229 offset:59392\n"
"ds_read_b64_tr_b16 v[226:227], v233 offset:59392\n"
"v_mfma_f32_16x16x32_bf16 v[172:175], v[188:191], a[232:235], v[172:175]\n"
"s_waitcnt lgkmcnt(4)\n"
"buffer_load_dwordx4 v240, s[28:31], s40 offen offset:3072 lds\n"
"v_mfma_f32_16x16x32_bf16 v[172:175], v[184:187], a[220:223], v[172:175]\n"
"s_lshl_b32 s62, s70, 9\n"
"v_mov_b64 v[92:93], 0\n"
"v_mfma_f32_16x16x32_bf16 v[172:175], v[180:183], a[216:219], v[172:175]\n"
"s_and_b32 s70, s88, 15\n"
"v_mov_b64 v[32:33], s[22:23]\n"
"v_subrev_f32 v125, s22, v125\n"
"v_mfma_f32_16x16x32_bf16 v[128:131], v[96:99], a[48:51], v[128:131]\n"
"s_lshr_b32 s88, s88, 4\n"
"ds_read_b64_tr_b16 v[180:181], v230 offset:32768\n"
"v_subrev_f32 v156, s22, v156\n"
"v_mfma_f32_16x16x32_bf16 v[128:131], v[100:103], a[52:55], v[128:131]\n"
"s_lshl_b32 s70, s70, 7\n"
"ds_read_b64_tr_b16 v[188:189], v230 offset:40960\n"
"v_bfe_u32 v255, v242, 9, 1\n"
"v_mfma_f32_16x16x32_bf16 v[128:131], v[104:107], a[56:59], v[128:131]\n"
"s_xor_b32 s71, s71, s75\n"
"v_cvt_pk_bf16_f32 v124, v124, v125\n"
"v_mfma_f32_16x16x32_bf16 v[128:131], v[108:111], a[60:63], v[128:131]\n"
"s_waitcnt lgkmcnt(4)\n"
"v_lshlrev_b32_e64 v201, 2, s72\n"
"ds_read_b64_tr_b16 v[216:217], v230 offset:57344\n"
"v_subrev_f32 v142, s22, v142\n"
"v_mfma_f32_16x16x32_bf16 v[128:131], v[112:115], a[0:3], v[128:131]\n"
"global_load_dword v200, v201, s[6:7] sc1\n"
"v_mfma_f32_16x16x32_bf16 v[128:131], v[116:119], a[4:7], v[128:131]\n"
"s_waitcnt lgkmcnt(3)\n"
"v_mov_b64 v[0:1], s[22:23]\n"
"v_subrev_f32 v127, s22, v127\n"
"v_mfma_f32_16x16x32_bf16 v[128:131], v[120:123], a[8:11], v[128:131]\n"
"v_mov_b64 v[86:87], 0\n"
"v_mfma_f32_16x16x32_bf16 v[128:131], v[224:227], a[12:15], v[128:131]\n"
"ds_read_b64_tr_b16 v[220:221], v234 offset:57344\n"
"v_mov_b64 v[16:17], s[22:23]\n"
"v_mfma_f32_16x16x32_bf16 v[144:147], v[224:227], a[28:31], v[144:147]\n"
"v_lshl_add_u32 v243, s44, 2, v243\n"
"v_mfma_f32_16x16x32_bf16 v[144:147], v[120:123], a[24:27], v[144:147]\n"
"ds_read_b64_tr_b16 v[186:187], v234 offset:34816\n"
"v_mov_b64 v[36:37], s[22:23]\n"
"v_mfma_f32_16x16x32_bf16 v[144:147], v[116:119], a[20:23], v[144:147]\n"
"ds_read_b64_tr_b16 v[222:223], v234 offset:59392\n"
"v_subrev_f32 v126, s22, v126\n"
"v_mfma_f32_16x16x32_bf16 v[144:147], v[112:115], a[16:19], v[144:147]\n"
"v_subrev_f32 v128, s22, v128\n"
"v_mfma_f32_16x16x32_bf16 v[144:147], v[108:111], a[76:79], v[144:147]\n"
"ds_read_b64_tr_b16 v[210:211], v230 offset:51200\n"
"v_subrev_f32 v129, s22, v129\n"
"v_mfma_f32_16x16x32_bf16 v[144:147], v[104:107], a[72:75], v[144:147]\n"
"v_subrev_f32 v130, s22, v130\n"
"v_subrev_f32 v131, s22, v131\n"
"v_mfma_f32_16x16x32_bf16 v[144:147], v[100:103], a[68:71], v[144:147]\n"
"ds_read_b64_tr_b16 v[214:215], v234 offset:51200\n"
"v_cvt_pk_bf16_f32 v125, v126, v127\n"
"v_mfma_f32_16x16x32_bf16 v[144:147], v[96:99], a[64:67], v[144:147]\n"
"ds_read_b64_tr_b16 v[190:191], v230 offset:43008\n"
"v_mov_b64 v[14:15], s[22:23]\n"
"v_mfma_f32_16x16x32_bf16 v[160:163], v[96:99], a[80:83], v[160:163]\n"
"v_lshl_add_u32 v254, v255, 4, v254\n"
"v_subrev_f32 v158, s22, v158\n"
"v_mfma_f32_16x16x32_bf16 v[160:163], v[100:103], a[84:87], v[160:163]\n"
"ds_read_b64_tr_b16 v[184:185], v234 offset:32768\n"
"v_subrev_f32 v143, s22, v143\n"
"v_mfma_f32_16x16x32_bf16 v[160:163], v[104:107], a[88:91], v[160:163]\n"
"v_cvt_pk_bf16_f32 v126, v128, v129\n"
"v_mfma_f32_16x16x32_bf16 v[160:163], v[108:111], a[92:95], v[160:163]\n"
"ds_read_b64_tr_b16 v[182:183], v230 offset:34816\n"
"v_cvt_pk_bf16_f32 v140, v140, v141\n"
"v_mfma_f32_16x16x32_bf16 v[160:163], v[112:115], a[32:35], v[160:163]\n"
"ds_read_b64_tr_b16 v[208:209], v230 offset:49152\n"
"v_cvt_pk_bf16_f32 v141, v142, v143\n"
"v_mfma_f32_16x16x32_bf16 v[160:163], v[116:119], a[36:39], v[160:163]\n"
"v_subrev_f32 v172, s22, v172\n"
"v_xor_b32 v233, s75, v233\n"
"v_mfma_f32_16x16x32_bf16 v[160:163], v[120:123], a[40:43], v[160:163]\n"
"ds_read_b64_tr_b16 v[194:195], v234 offset:43008\n"
"v_cvt_pk_bf16_f32 v127, v130, v131\n"
"v_mfma_f32_16x16x32_bf16 v[160:163], v[224:227], a[44:47], v[160:163]\n"
"buffer_store_dwordx4 v[124:127], v243, s[32:35], s16 offen sc0 sc1\n"
"v_subrev_f32 v157, s22, v157\n"
"v_mfma_f32_16x16x32_bf16 v[176:179], v[224:227], a[228:231], v[176:179]\n"
"ds_read_b64_tr_b16 v[218:219], v230 offset:59392\n"
"v_subrev_f32 v145, s22, v145\n"
"v_mfma_f32_16x16x32_bf16 v[176:179], v[120:123], a[224:227], v[176:179]\n"
"ds_read_b64_tr_b16 v[192:193], v234 offset:40960\n"
"v_mov_b64 v[244:245], 0\n"
"v_mfma_f32_16x16x32_bf16 v[176:179], v[116:119], a[212:215], v[176:179]\n"
"ds_read_b64_tr_b16 v[212:213], v234 offset:49152\n"
"v_subrev_f32 v144, s22, v144\n"
"v_mfma_f32_16x16x32_bf16 v[176:179], v[112:115], a[208:211], v[176:179]\n"
"v_subrev_f32 v163, s22, v163\n"
"v_mfma_f32_16x16x32_bf16 v[176:179], v[108:111], a[236:239], v[176:179]\n"
"ds_read_b64_tr_b16 v[224:225], v235 offset:57344\n"
"v_lshl_add_u32 v248, v242, 8, s93\n"
"v_mfma_f32_16x16x32_bf16 v[176:179], v[104:107], a[232:235], v[176:179]\n"
"v_subrev_f32 v146, s22, v146\n"
"v_subrev_f32 v147, s22, v147\n"
"v_mfma_f32_16x16x32_bf16 v[176:179], v[100:103], a[220:223], v[176:179]\n"
"ds_read_b64_tr_b16 v[226:227], v235 offset:59392\n"
"v_mfma_f32_16x16x32_bf16 v[176:179], v[96:99], a[216:219], v[176:179]\n"
"s_waitcnt lgkmcnt(5)\n"
"v_cvt_pk_bf16_f32 v156, v156, v157\n"
"v_mfma_f32_16x16x32_bf16 v[132:135], v[180:183], a[48:51], v[132:135]\n"
"ds_read_b64_tr_b16 v[102:103], v235 offset:34816\n"
"v_subrev_f32 v162, s22, v162\n"
"v_mfma_f32_16x16x32_bf16 v[132:135], v[184:187], a[52:55], v[132:135]\n"
"v_cvt_pk_bf16_f32 v157, v158, v159\n"
"v_xor_b32 v229, s75, v229\n"
"v_mfma_f32_16x16x32_bf16 v[132:135], v[188:191], a[56:59], v[132:135]\n"
"s_waitcnt lgkmcnt(3)\n"
"ds_read_b64_tr_b16 v[114:115], v231 offset:51200\n"
"v_mov_b64 v[88:89], 0\n"
"v_mfma_f32_16x16x32_bf16 v[132:135], v[192:195], a[60:63], v[132:135]\n"
"v_mov_b64 v[22:23], s[22:23]\n"
"v_mov_b64 v[94:95], 0\n"
"v_mfma_f32_16x16x32_bf16 v[132:135], v[208:211], a[0:3], v[132:135]\n"
"ds_read_b64_tr_b16 v[104:105], v231 offset:40960\n"
"v_subrev_f32 v174, s22, v174\n"
"v_mfma_f32_16x16x32_bf16 v[132:135], v[212:215], a[4:7], v[132:135]\n"
"v_subrev_f32 v173, s22, v173\n"
"v_cvt_pk_bf16_f32 v142, v144, v145\n"
"v_mfma_f32_16x16x32_bf16 v[132:135], v[216:219], a[8:11], v[132:135]\n"
"ds_read_b64_tr_b16 v[108:109], v235 offset:40960\n"
"v_cvt_pk_bf16_f32 v143, v146, v147\n"
"v_mfma_f32_16x16x32_bf16 v[132:135], v[220:223], a[12:15], v[132:135]\n"
"buffer_store_dwordx4 v[140:143], v243, s[32:35], s16 offen offset:2048 sc0 sc1\n"
"ds_read_b64_tr_b16 v[96:97], v231 offset:32768\n"
"v_mfma_f32_16x16x32_bf16 v[148:151], v[220:223], a[28:31], v[148:151]\n"
"ds_read_b64_tr_b16 v[110:111], v235 offset:43008\n"
"v_mov_b64 v[18:19], s[22:23]\n"
"v_mfma_f32_16x16x32_bf16 v[148:151], v[216:219], a[24:27], v[148:151]\n"
"v_bfe_u32 v237, v242, 4, 3\n"
"v_cvt_pk_bf16_f32 v159, v162, v163\n"
"v_mfma_f32_16x16x32_bf16 v[148:151], v[212:215], a[20:23], v[148:151]\n"
"ds_read_b64_tr_b16 v[120:121], v231 offset:57344\n"
"v_add_u32 v241, 1, v241\n"
"v_mfma_f32_16x16x32_bf16 v[148:151], v[208:211], a[16:19], v[148:151]\n"
"ds_read_b64_tr_b16 v[100:101], v235 offset:32768\n"
"v_mfma_f32_16x16x32_bf16 v[148:151], v[192:195], a[76:79], v[148:151]\n"
"ds_read_b64_tr_b16 v[98:99], v231 offset:34816\n"
"v_max_u32 v237, 1, v237\n"
"v_mfma_f32_16x16x32_bf16 v[148:151], v[188:191], a[72:75], v[148:151]\n"
"v_subrev_f32 v160, s22, v160\n"
"v_subrev_f32 v161, s22, v161\n"
"v_mfma_f32_16x16x32_bf16 v[148:151], v[184:187], a[68:71], v[148:151]\n"
"ds_read_b64_tr_b16 v[106:107], v231 offset:43008\n"
"v_subrev_f32 v179, s22, v179\n"
"v_mfma_f32_16x16x32_bf16 v[148:151], v[180:183], a[64:67], v[148:151]\n"
"v_mov_b64 v[30:31], s[22:23]\n"
"v_subrev_f32 v176, s22, v176\n"
"v_mfma_f32_16x16x32_bf16 v[164:167], v[180:183], a[80:83], v[164:167]\n"
"ds_read_b64_tr_b16 v[112:113], v231 offset:49152\n"
"v_mov_b64 v[38:39], s[22:23]\n"
"v_mfma_f32_16x16x32_bf16 v[164:167], v[184:187], a[84:87], v[164:167]\n"
"ds_read_b64_tr_b16 v[118:119], v235 offset:51200\n"
"v_subrev_f32 v132, s22, v132\n"
"v_mfma_f32_16x16x32_bf16 v[164:167], v[188:191], a[88:91], v[164:167]\n"
"v_xor_b32 v237, s61, v237\n"
"v_subrev_f32 v133, s22, v133\n"
"v_mfma_f32_16x16x32_bf16 v[164:167], v[192:195], a[92:95], v[164:167]\n"
"ds_read_b64_tr_b16 v[116:117], v235 offset:49152\n"
"v_cvt_pk_bf16_f32 v158, v160, v161\n"
"v_mfma_f32_16x16x32_bf16 v[164:167], v[208:211], a[32:35], v[164:167]\n"
"v_mov_b64 v[34:35], s[22:23]\n"
"v_subrev_f32 v175, s22, v175\n"
"v_mfma_f32_16x16x32_bf16 v[164:167], v[212:215], a[36:39], v[164:167]\n"
"ds_read_b64_tr_b16 v[122:123], v231 offset:59392\n"
"v_mfma_f32_16x16x32_bf16 v[164:167], v[216:219], a[40:43], v[164:167]\n"
"v_add_u32 v238, s97, v254\n"
"v_mov_b64 v[54:55], s[22:23]\n"
"v_mfma_f32_16x16x32_bf16 v[164:167], v[220:223], a[44:47], v[164:167]\n"
"s_barrier\n"
"s_waitcnt lgkmcnt(4)\n"
"v_mfma_f32_16x16x32_bf16 v[136:139], v[96:99], a[48:51], v[136:139]\n"
"ds_read_b64_tr_b16 v[182:183], v228 offset:2048\n"
"v_subrev_f32 v178, s22, v178\n"
"v_mfma_f32_16x16x32_bf16 v[136:139], v[100:103], a[52:55], v[136:139]\n"
"ds_read_b64_tr_b16 v[222:223], v232 offset:26624\n"
"v_add_u32 v253, s99, v254\n"
"v_mfma_f32_16x16x32_bf16 v[136:139], v[104:107], a[56:59], v[136:139]\n"
"ds_read_b64_tr_b16 v[186:187], v232 offset:2048\n"
"v_mov_b64 v[42:43], s[22:23]\n"
"v_mfma_f32_16x16x32_bf16 v[136:139], v[108:111], a[60:63], v[136:139]\n"
"s_waitcnt lgkmcnt(4)\n"
"ds_read_b64_tr_b16 v[194:195], v232 offset:10240\n"
"v_mov_b64 v[50:51], s[22:23]\n"
"v_mfma_f32_16x16x32_bf16 v[136:139], v[112:115], a[0:3], v[136:139]\n"
"ds_read_b64_tr_b16 v[210:211], v228 offset:18432\n"
"v_subrev_f32 v135, s22, v135\n"
"v_mfma_f32_16x16x32_bf16 v[136:139], v[116:119], a[4:7], v[136:139]\n"
"s_waitcnt lgkmcnt(5)\n"
"ds_read_b64_tr_b16 v[192:193], v232 offset:8192\n"
"v_mfma_f32_16x16x32_bf16 v[136:139], v[120:123], a[8:11], v[136:139]\n"
"ds_read_b64_tr_b16 v[188:189], v228 offset:8192\n"
"v_xor_b32 v239, 64, v238\n"
"v_mfma_f32_16x16x32_bf16 v[136:139], v[224:227], a[12:15], v[136:139]\n"
"buffer_store_dwordx4 v[156:159], v243, s[32:35], s17 offen sc0 sc1\n"
"ds_read_b64_tr_b16 v[214:215], v232 offset:18432\n"
"v_mfma_f32_16x16x32_bf16 v[152:155], v[224:227], a[28:31], v[152:155]\n"
"ds_read_b64_tr_b16 v[190:191], v228 offset:10240\n"
"v_subrev_f32 v134, s22, v134\n"
"v_mfma_f32_16x16x32_bf16 v[152:155], v[120:123], a[24:27], v[152:155]\n"
"ds_read_b64_tr_b16 v[208:209], v228 offset:16384\n"
"v_add_u32 v252, s98, v254\n"
"v_mfma_f32_16x16x32_bf16 v[152:155], v[116:119], a[20:23], v[152:155]\n"
"ds_read_b64_tr_b16 v[184:185], v232 offset:0\n"
"v_cvt_pk_bf16_f32 v172, v172, v173\n"
"v_mfma_f32_16x16x32_bf16 v[152:155], v[112:115], a[16:19], v[152:155]\n"
"ds_read_b64_tr_b16 v[216:217], v228 offset:24576\n"
"v_mov_b64 v[8:9], s[22:23]\n"
"v_mfma_f32_16x16x32_bf16 v[152:155], v[108:111], a[76:79], v[152:155]\n"
"ds_read_b64_tr_b16 v[218:219], v228 offset:26624\n"
"v_mov_b64 v[52:53], s[22:23]\n"
"v_mfma_f32_16x16x32_bf16 v[152:155], v[104:107], a[72:75], v[152:155]\n"
"ds_read_b64_tr_b16 v[212:213], v232 offset:16384\n"
"v_mov_b64 v[84:85], 0\n"
"v_mfma_f32_16x16x32_bf16 v[152:155], v[100:103], a[68:71], v[152:155]\n"
"ds_read_b64_tr_b16 v[220:221], v232 offset:24576\n"
"v_cvt_pk_bf16_f32 v173, v174, v175\n"
"v_mfma_f32_16x16x32_bf16 v[152:155], v[96:99], a[64:67], v[152:155]\n"
"ds_read_b64_tr_b16 v[180:181], v228 offset:0\n"
"v_mov_b64 v[28:29], s[22:23]\n"
"v_mfma_f32_16x16x32_bf16 v[168:171], v[96:99], a[80:83], v[168:171]\n"
"ds_read_b64_tr_b16 v[96:97], v229 offset:0\n"
"ds_read_b64_tr_b16 v[98:99], v229 offset:2048\n"
"v_mfma_f32_16x16x32_bf16 v[168:171], v[100:103], a[84:87], v[168:171]\n"
"ds_read_b64_tr_b16 v[100:101], v233 offset:0\n"
"ds_read_b64_tr_b16 v[102:103], v233 offset:2048\n"
"v_mfma_f32_16x16x32_bf16 v[168:171], v[104:107], a[88:91], v[168:171]\n"
"ds_read_b64_tr_b16 v[104:105], v229 offset:8192\n"
"v_mfma_f32_16x16x32_bf16 v[168:171], v[108:111], a[92:95], v[168:171]\n"
"v_cvt_pk_bf16_f32 v175, v178, v179\n"
"v_mfma_f32_16x16x32_bf16 v[168:171], v[112:115], a[32:35], v[168:171]\n"
"v_mov_b64 v[48:49], s[22:23]\n"
"v_subrev_f32 v177, s22, v177\n"
"v_mfma_f32_16x16x32_bf16 v[168:171], v[116:119], a[36:39], v[168:171]\n"
"v_lshlrev_b32 v237, 2, v237\n"
"v_mov_b64 v[46:47], s[22:23]\n"
"v_mfma_f32_16x16x32_bf16 v[168:171], v[120:123], a[40:43], v[168:171]\n"
"s_add_u32 s47, s47, 1\n"
"v_cvt_pk_bf16_f32 v132, v132, v133\n"
"v_mfma_f32_16x16x32_bf16 v[168:171], v[224:227], a[44:47], v[168:171]\n"
"s_waitcnt lgkmcnt(5)\n"
"v_mov_b64 v[4:5], s[22:23]\n"
"v_cvt_pk_bf16_f32 v174, v176, v177\n"
"LGL_%=:\n"
"v_mfma_f32_16x16x32_bf16 v[124:127], v[180:183], a[96:99], v[196:199]\n"
"ds_read_b64_tr_b16 v[106:107], v229 offset:10240\n"
"v_cvt_pk_bf16_f32 v133, v134, v135\n"
"v_mfma_f32_16x16x32_bf16 v[124:127], v[184:187], a[100:103], v[124:127]\n"
"ds_read_b64_tr_b16 v[108:109], v233 offset:8192\n"
"v_subrev_f32 v136, s22, v136\n"
"v_mfma_f32_16x16x32_bf16 v[124:127], v[188:191], a[104:107], v[124:127]\n"
"ds_read_b64_tr_b16 v[110:111], v233 offset:10240\n"
"v_subrev_f32 v137, s22, v137\n"
"v_mfma_f32_16x16x32_bf16 v[124:127], v[192:195], a[108:111], v[124:127]\n"
"ds_read_b64_tr_b16 v[112:113], v229 offset:16384\n"
"v_subrev_f32 v138, s22, v138\n"
"v_mfma_f32_16x16x32_bf16 v[124:127], v[208:211], a[144:147], v[124:127]\n"
"ds_read_b64_tr_b16 v[114:115], v229 offset:18432\n"
"v_subrev_f32 v139, s22, v139\n"
"v_mfma_f32_16x16x32_bf16 v[124:127], v[212:215], a[148:151], v[124:127]\n"
"s_waitcnt lgkmcnt(7)\n"
"ds_read_b64_tr_b16 v[116:117], v233 offset:16384\n"
"v_cvt_pk_bf16_f32 v134, v136, v137\n"
"v_mfma_f32_16x16x32_bf16 v[124:127], v[216:219], a[152:155], v[124:127]\n"
"ds_read_b64_tr_b16 v[118:119], v233 offset:18432\n"
"v_cvt_pk_bf16_f32 v135, v138, v139\n"
"v_mfma_f32_16x16x32_bf16 v[124:127], v[220:223], a[156:159], v[124:127]\n"
"buffer_store_dwordx4 v[172:175], v243, s[32:35], s17 offen offset:2048 sc0 sc1\n"
"v_subrev_f32 v148, s22, v148\n"
"v_mfma_f32_16x16x32_bf16 v[140:143], v[220:223], a[172:175], v[196:199]\n"
"ds_read_b64_tr_b16 v[120:121], v229 offset:24576\n"
"v_subrev_f32 v149, s22, v149\n"
"v_mfma_f32_16x16x32_bf16 v[140:143], v[216:219], a[168:171], v[140:143]\n"
"ds_read_b64_tr_b16 v[122:123], v229 offset:26624\n"
"v_subrev_f32 v150, s22, v150\n"
"v_mfma_f32_16x16x32_bf16 v[140:143], v[212:215], a[164:167], v[140:143]\n"
"ds_read_b64_tr_b16 v[224:225], v233 offset:24576\n"
"v_subrev_f32 v151, s22, v151\n"
"v_mfma_f32_16x16x32_bf16 v[140:143], v[208:211], a[160:163], v[140:143]\n"
"ds_read_b64_tr_b16 v[226:227], v233 offset:26624\n"
"v_cvt_pk_bf16_f32 v148, v148, v149\n"
"v_mfma_f32_16x16x32_bf16 v[140:143], v[192:195], a[124:127], v[140:143]\n"
"v_cvt_pk_bf16_f32 v149, v150, v151\n"
"v_subrev_f32 v152, s22, v152\n"
"v_mfma_f32_16x16x32_bf16 v[140:143], v[188:191], a[120:123], v[140:143]\n"
"s_add_u32 s41, s70, 0\n"
"v_subrev_f32 v153, s22, v153\n"
"v_subrev_f32 v154, s22, v154\n"
"v_mfma_f32_16x16x32_bf16 v[140:143], v[184:187], a[116:119], v[140:143]\n"
"s_mov_b32 m0, s71\n"
"v_subrev_f32 v155, s22, v155\n"
"v_cvt_pk_bf16_f32 v150, v152, v153\n"
"v_mfma_f32_16x16x32_bf16 v[140:143], v[180:183], a[112:115], v[140:143]\n"
"s_waitcnt vmcnt(14)\n"
"buffer_load_dwordx4 v240, s[28:31], s41 offen lds\n"
"v_mfma_f32_16x16x32_bf16 v[156:159], v[180:183], a[128:131], v[196:199]\n"
"v_xor_b32 v230, s75, v230\n"
"v_cvt_pk_bf16_f32 v151, v154, v155\n"
"v_mfma_f32_16x16x32_bf16 v[156:159], v[184:187], a[132:135], v[156:159]\n"
"v_xor_b32 v234, s75, v234\n"
"v_subrev_f32 v164, s22, v164\n"
"v_mfma_f32_16x16x32_bf16 v[156:159], v[188:191], a[136:139], v[156:159]\n"
"v_subrev_f32 v165, s22, v165\n"
"v_subrev_f32 v166, s22, v166\n"
"v_mfma_f32_16x16x32_bf16 v[156:159], v[192:195], a[140:143], v[156:159]\n"
"v_subrev_f32 v167, s22, v167\n"
"v_cvt_pk_bf16_f32 v164, v164, v165\n"
"v_mfma_f32_16x16x32_bf16 v[156:159], v[208:211], a[176:179], v[156:159]\n"
"v_cvt_pk_bf16_f32 v165, v166, v167\n"
"v_subrev_f32 v168, s22, v168\n"
"v_mfma_f32_16x16x32_bf16 v[156:159], v[212:215], a[180:183], v[156:159]\n"
"v_subrev_f32 v169, s22, v169\n"
"v_subrev_f32 v170, s22, v170\n"
"v_mfma_f32_16x16x32_bf16 v[156:159], v[216:219], a[184:187], v[156:159]\n"
"v_subrev_f32 v171, s22, v171\n"
"v_cvt_pk_bf16_f32 v166, v168, v169\n"
"v_mfma_f32_16x16x32_bf16 v[156:159], v[220:223], a[188:191], v[156:159]\n"
"buffer_store_dwordx4 v[132:135], v243, s[32:35], s16 offen offset:1024 sc0 sc1\n"
"v_cvt_pk_bf16_f32 v167, v170, v171\n"
"v_mfma_f32_16x16x32_bf16 v[172:175], v[220:223], a[252:255], v[196:199]\n"
"ds_read_b64_tr_b16 v[220:221], v234 offset:24576\n"
"v_xor_b32 v231, s75, v231\n"
"v_mfma_f32_16x16x32_bf16 v[172:175], v[216:219], a[248:251], v[172:175]\n"
"ds_read_b64_tr_b16 v[216:217], v230 offset:24576\n"
"v_xor_b32 v235, s75, v235\n"
"v_mfma_f32_16x16x32_bf16 v[172:175], v[212:215], a[244:247], v[172:175]\n"
"ds_read_b64_tr_b16 v[212:213], v234 offset:16384\n"
"v_lshlrev_b32 v202, 16, v84\n"
"v_mfma_f32_16x16x32_bf16 v[172:175], v[208:211], a[240:243], v[172:175]\n"
"ds_read_b64_tr_b16 v[208:209], v230 offset:16384\n"
"v_and_b32 v84, s73, v84\n"
"v_mfma_f32_16x16x32_bf16 v[172:175], v[192:195], a[204:207], v[172:175]\n"
"ds_read_b64_tr_b16 v[192:193], v234 offset:8192\n"
"v_add_f32 v25, v25, v84\n"
"v_mfma_f32_16x16x32_bf16 v[172:175], v[188:191], a[200:203], v[172:175]\n"
"ds_read_b64_tr_b16 v[188:189], v230 offset:8192\n"
"v_lshlrev_b32 v203, 16, v85\n"
"v_mfma_f32_16x16x32_bf16 v[172:175], v[184:187], a[196:199], v[172:175]\n"
"s_add_u32 s40, s41, s52\n"
"ds_read_b64_tr_b16 v[184:185], v234 offset:0\n"
"v_and_b32 v85, s73, v85\n"
"v_mfma_f32_16x16x32_bf16 v[172:175], v[180:183], a[192:195], v[172:175]\n"
"s_waitcnt vmcnt(13) lgkmcnt(7)\n"
"buffer_load_dwordx4 v240, s[28:31], s40 offen offset:1024 lds\n"
"v_mfma_f32_16x16x32_bf16 v[128:131], v[96:99], a[96:99], v[196:199]\n"
"s_waitcnt lgkmcnt(6)\n"
"ds_read_b64_tr_b16 v[180:181], v230 offset:0\n"
"v_add_f32 v27, v27, v85\n"
"v_mfma_f32_16x16x32_bf16 v[128:131], v[100:103], a[100:103], v[128:131]\n"
"ds_read_b64_tr_b16 v[182:183], v230 offset:2048\n"
"v_lshlrev_b32 v204, 16, v86\n"
"v_mfma_f32_16x16x32_bf16 v[128:131], v[104:107], a[104:107], v[128:131]\n"
"ds_read_b64_tr_b16 v[186:187], v234 offset:2048\n"
"v_and_b32 v86, s73, v86\n"
"v_mfma_f32_16x16x32_bf16 v[128:131], v[108:111], a[108:111], v[128:131]\n"
"ds_read_b64_tr_b16 v[190:191], v230 offset:10240\n"
"v_add_f32 v29, v29, v86\n"
"v_mfma_f32_16x16x32_bf16 v[128:131], v[112:115], a[144:147], v[128:131]\n"
"s_waitcnt vmcnt(6)\n"
"ds_read_b64_tr_b16 v[194:195], v234 offset:10240\n"
"v_lshlrev_b32 v205, 16, v87\n"
"v_mfma_f32_16x16x32_bf16 v[128:131], v[116:119], a[148:151], v[128:131]\n"
"v_subrev_u32 v200, s21, v200\n"
"ds_read_b64_tr_b16 v[210:211], v230 offset:18432\n"
"v_mfma_f32_16x16x32_bf16 v[128:131], v[120:123], a[152:155], v[128:131]\n"
"v_cmp_lt_u32 vcc, s69, v200\n"
"ds_read_b64_tr_b16 v[214:215], v234 offset:18432\n"
"v_mfma_f32_16x16x32_bf16 v[128:131], v[224:227], a[156:159], v[128:131]\n"
"ds_read_b64_tr_b16 v[218:219], v230 offset:26624\n"
"s_cbranch_vccnz LGP_%=\nLGPK_%=:\n"
"v_mfma_f32_16x16x32_bf16 v[144:147], v[224:227], a[172:175], v[196:199]\n"
"s_sub_u32 s72, s72, 1\n"
"ds_read_b64_tr_b16 v[222:223], v234 offset:26624\n"
"v_and_b32 v87, s73, v87\n"
"v_mfma_f32_16x16x32_bf16 v[144:147], v[120:123], a[168:171], v[144:147]\n"
"s_and_b32 s72, s72, 7\n"
"v_add_f32 v24, v24, v202\n"
"v_add_f32 v31, v31, v87\n"
"v_mfma_f32_16x16x32_bf16 v[144:147], v[116:119], a[164:167], v[144:147]\n"
"v_add_f32 v26, v26, v203\n"
"v_lshlrev_b32 v206, 16, v88\n"
"v_mfma_f32_16x16x32_bf16 v[144:147], v[112:115], a[160:163], v[144:147]\n"
"buffer_load_dwordx4 v[72:75], v243, s[32:35], s18 offen sc1\n"
"v_mfma_f32_16x16x32_bf16 v[144:147], v[108:111], a[124:127], v[144:147]\n"
"v_and_b32 v88, s73, v88\n"
"v_add_f32 v33, v33, v88\n"
"v_mfma_f32_16x16x32_bf16 v[144:147], v[104:107], a[120:123], v[144:147]\n"
"v_lshlrev_b32 v207, 16, v89\n"
"v_and_b32 v89, s73, v89\n"
"v_mfma_f32_16x16x32_bf16 v[144:147], v[100:103], a[116:119], v[144:147]\n"
"v_add_f32 v35, v35, v89\n"
"v_lshlrev_b32 v202, 16, v90\n"
"v_mfma_f32_16x16x32_bf16 v[144:147], v[96:99], a[112:115], v[144:147]\n"
"buffer_store_dwordx4 v[148:151], v243, s[32:35], s16 offen offset:3072 sc0 sc1\n"
"v_and_b32 v90, s73, v90\n"
"v_mfma_f32_16x16x32_bf16 v[160:163], v[96:99], a[128:131], v[196:199]\n"
"v_add_f32 v37, v37, v90\n"
"v_lshlrev_b32 v203, 16, v91\n"
"v_mfma_f32_16x16x32_bf16 v[160:163], v[100:103], a[132:135], v[160:163]\n"
"v_and_b32 v91, s73, v91\n"
"v_add_f32 v39, v39, v91\n"
"v_mfma_f32_16x16x32_bf16 v[160:163], v[104:107], a[136:139], v[160:163]\n"
"v_add_f32 v28, v28, v204\n"
"v_add_f32 v30, v30, v205\n"
"v_mfma_f32_16x16x32_bf16 v[160:163], v[108:111], a[140:143], v[160:163]\n"
"v_add_f32 v32, v32, v206\n"
"v_add_f32 v34, v34, v207\n"
"v_mfma_f32_16x16x32_bf16 v[160:163], v[112:115], a[176:179], v[160:163]\n"
"v_lshlrev_b32 v204, 16, v92\n"
"v_and_b32 v92, s73, v92\n"
"v_mfma_f32_16x16x32_bf16 v[160:163], v[116:119], a[180:183], v[160:163]\n"
"v_add_f32 v41, v41, v92\n"
"v_lshlrev_b32 v205, 16, v93\n"
"v_mfma_f32_16x16x32_bf16 v[160:163], v[120:123], a[184:187], v[160:163]\n"
"s_add_u32 s40, s41, s53\n"
"v_and_b32 v93, s73, v93\n"
"v_add_f32 v43, v43, v93\n"
"v_mfma_f32_16x16x32_bf16 v[160:163], v[224:227], a[188:191], v[160:163]\n"
"buffer_load_dwordx4 v240, s[28:31], s40 offen offset:2048 lds\n"
"v_mfma_f32_16x16x32_bf16 v[176:179], v[224:227], a[252:255], v[196:199]\n"
"ds_read_b64_tr_b16 v[224:225], v235 offset:24576\n"
"v_lshlrev_b32 v206, 16, v94\n"
"v_mfma_f32_16x16x32_bf16 v[176:179], v[120:123], a[248:251], v[176:179]\n"
"ds_read_b64_tr_b16 v[120:121], v231 offset:24576\n"
"v_and_b32 v94, s73, v94\n"
"v_mfma_f32_16x16x32_bf16 v[176:179], v[116:119], a[244:247], v[176:179]\n"
"ds_read_b64_tr_b16 v[116:117], v235 offset:16384\n"
"v_add_f32 v45, v45, v94\n"
"v_mfma_f32_16x16x32_bf16 v[176:179], v[112:115], a[240:243], v[176:179]\n"
"buffer_load_dwordx4 v[76:79], v243, s[32:35], s18 offen offset:1024 sc1\n"
"v_mfma_f32_16x16x32_bf16 v[176:179], v[108:111], a[204:207], v[176:179]\n"
"ds_read_b64_tr_b16 v[108:109], v235 offset:8192\n"
"v_lshlrev_b32 v207, 16, v95\n"
"v_mfma_f32_16x16x32_bf16 v[176:179], v[104:107], a[200:203], v[176:179]\n"
"ds_read_b64_tr_b16 v[104:105], v231 offset:8192\n"
"v_and_b32 v95, s73, v95\n"
"v_mfma_f32_16x16x32_bf16 v[176:179], v[100:103], a[196:199], v[176:179]\n"
"s_add_u32 s40, s41, s54\n"
"ds_read_b64_tr_b16 v[100:101], v235 offset:0\n"
"v_add_f32 v47, v47, v95\n"
"v_mfma_f32_16x16x32_bf16 v[176:179], v[96:99], a[192:195], v[176:179]\n"
"s_waitcnt lgkmcnt(6)\n"
"buffer_load_dwordx4 v240, s[28:31], s40 offen offset:3072 lds\n"
"v_mfma_f32_16x16x32_bf16 v[132:135], v[180:183], a[96:99], v[196:199]\n"
"ds_read_b64_tr_b16 v[96:97], v231 offset:0\n"
"v_add_f32 v36, v36, v202\n"
"v_mfma_f32_16x16x32_bf16 v[132:135], v[184:187], a[100:103], v[132:135]\n"
"ds_read_b64_tr_b16 v[98:99], v231 offset:2048\n"
"v_add_f32 v38, v38, v203\n"
"v_mfma_f32_16x16x32_bf16 v[132:135], v[188:191], a[104:107], v[132:135]\n"
"ds_read_b64_tr_b16 v[102:103], v235 offset:2048\n"
"v_add_f32 v44, v44, v206\n"
"v_mfma_f32_16x16x32_bf16 v[132:135], v[192:195], a[108:111], v[132:135]\n"
"ds_read_b64_tr_b16 v[106:107], v231 offset:10240\n"
"v_add_f32 v46, v46, v207\n"
"v_mfma_f32_16x16x32_bf16 v[132:135], v[208:211], a[144:147], v[132:135]\n"
"ds_read_b64_tr_b16 v[110:111], v235 offset:10240\n"
"v_add_f32 v40, v40, v204\n"
"v_mfma_f32_16x16x32_bf16 v[132:135], v[212:215], a[148:151], v[132:135]\n"
"ds_read_b64_tr_b16 v[112:113], v231 offset:16384\n"
"v_add_f32 v42, v42, v205\n"
"v_mfma_f32_16x16x32_bf16 v[132:135], v[216:219], a[152:155], v[132:135]\n"
"ds_read_b64_tr_b16 v[114:115], v231 offset:18432\n"
"v_mfma_f32_16x16x32_bf16 v[132:135], v[220:223], a[156:159], v[132:135]\n"
"buffer_store_dwordx4 v[164:167], v243, s[32:35], s17 offen offset:1024 sc0 sc1\n"
"v_lshlrev_b32 v206, 16, v244\n"
"v_mfma_f32_16x16x32_bf16 v[148:151], v[220:223], a[172:175], v[196:199]\n"
"ds_read_b64_tr_b16 v[118:119], v235 offset:18432\n"
"v_and_b32 v244, s73, v244\n"
"v_mfma_f32_16x16x32_bf16 v[148:151], v[216:219], a[168:171], v[148:151]\n"
"ds_read_b64_tr_b16 v[122:123], v231 offset:26624\n"
"v_add_f32 v49, v49, v244\n"
"v_mfma_f32_16x16x32_bf16 v[148:151], v[212:215], a[164:167], v[148:151]\n"
"ds_read_b64_tr_b16 v[226:227], v235 offset:26624\n"
"v_lshlrev_b32 v207, 16, v245\n"
"v_mfma_f32_16x16x32_bf16 v[148:151], v[208:211], a[160:163], v[148:151]\n"
"buffer_load_dwordx4 v[80:83], v243, s[32:35], s18 offen offset:2048 sc1\n"
"v_mfma_f32_16x16x32_bf16 v[148:151], v[192:195], a[124:127], v[148:151]\n"
"v_and_b32 v245, s73, v245\n"
"v_add_f32 v51, v51, v245\n"
"v_mfma_f32_16x16x32_bf16 v[148:151], v[188:191], a[120:123], v[148:151]\n"
"s_add_u32 s41, s70, 163840\n"
"v_lshlrev_b32 v202, 16, v246\n"
"v_and_b32 v246, s73, v246\n"
"v_mfma_f32_16x16x32_bf16 v[148:151], v[184:187], a[116:119], v[148:151]\n"
"s_add_u32 m0, s71, 16384\n"
"v_add_f32 v53, v53, v246\n"
"v_lshlrev_b32 v203, 16, v247\n"
"v_mfma_f32_16x16x32_bf16 v[148:151], v[180:183], a[112:115], v[148:151]\n"
"buffer_load_dwordx4 v240, s[28:31], s41 offen lds\n"
"v_mfma_f32_16x16x32_bf16 v[164:167], v[180:183], a[128:131], v[196:199]\n"
"v_and_b32 v247, s73, v247\n"
"v_add_f32 v55, v55, v247\n"
"v_mfma_f32_16x16x32_bf16 v[164:167], v[184:187], a[132:135], v[164:167]\n"
"v_add_f32 v48, v48, v206\n"
"v_add_f32 v50, v50, v207\n"
"v_mfma_f32_16x16x32_bf16 v[164:167], v[188:191], a[136:139], v[164:167]\n"
"v_add_f32 v52, v52, v202\n"
"v_add_f32 v54, v54, v203\n"
"v_mfma_f32_16x16x32_bf16 v[164:167], v[192:195], a[140:143], v[164:167]\n"
"v_mfma_f32_16x16x32_bf16 v[164:167], v[208:211], a[176:179], v[164:167]\n"
"v_mfma_f32_16x16x32_bf16 v[164:167], v[212:215], a[180:183], v[164:167]\n"
"v_mfma_f32_16x16x32_bf16 v[164:167], v[216:219], a[184:187], v[164:167]\n"
"s_waitcnt vmcnt(12)\n"
"v_mfma_f32_16x16x32_bf16 v[164:167], v[220:223], a[188:191], v[164:167]\n"
"s_barrier\n"
"s_waitcnt lgkmcnt(0)\n"
"v_mfma_f32_16x16x32_bf16 v[136:139], v[96:99], a[96:99], v[196:199]\n"
"ds_read_b64_tr_b16 v[180:181], v228 offset:32768\n"
"v_mfma_f32_16x16x32_bf16 v[136:139], v[100:103], a[100:103], v[136:139]\n"
"ds_read_b64_tr_b16 v[182:183], v228 offset:34816\n"
"v_mfma_f32_16x16x32_bf16 v[136:139], v[104:107], a[104:107], v[136:139]\n"
"ds_read_b64_tr_b16 v[184:185], v232 offset:32768\n"
"v_mfma_f32_16x16x32_bf16 v[136:139], v[108:111], a[108:111], v[136:139]\n"
"ds_read_b64_tr_b16 v[186:187], v232 offset:34816\n"
"v_mfma_f32_16x16x32_bf16 v[136:139], v[112:115], a[144:147], v[136:139]\n"
"ds_read_b64_tr_b16 v[188:189], v228 offset:40960\n"
"v_mfma_f32_16x16x32_bf16 v[136:139], v[116:119], a[148:151], v[136:139]\n"
"ds_read_b64_tr_b16 v[190:191], v228 offset:43008\n"
"v_mfma_f32_16x16x32_bf16 v[136:139], v[120:123], a[152:155], v[136:139]\n"
"s_add_u32 s40, s41, s52\n"
"ds_read_b64_tr_b16 v[192:193], v232 offset:40960\n"
"v_mfma_f32_16x16x32_bf16 v[136:139], v[224:227], a[156:159], v[136:139]\n"
"buffer_load_dwordx4 v240, s[28:31], s40 offen offset:1024 lds\n"
"v_mfma_f32_16x16x32_bf16 v[152:155], v[224:227], a[172:175], v[196:199]\n"
"ds_read_b64_tr_b16 v[194:195], v232 offset:43008\n"
"v_mfma_f32_16x16x32_bf16 v[152:155], v[120:123], a[168:171], v[152:155]\n"
"ds_read_b64_tr_b16 v[208:209], v228 offset:49152\n"
"v_mfma_f32_16x16x32_bf16 v[152:155], v[116:119], a[164:167], v[152:155]\n"
"ds_read_b64_tr_b16 v[210:211], v228 offset:51200\n"
"v_mfma_f32_16x16x32_bf16 v[152:155], v[112:115], a[160:163], v[152:155]\n"
"ds_read_b64_tr_b16 v[212:213], v232 offset:49152\n"
"v_mfma_f32_16x16x32_bf16 v[152:155], v[108:111], a[124:127], v[152:155]\n"
"ds_read_b64_tr_b16 v[214:215], v232 offset:51200\n"
"v_mfma_f32_16x16x32_bf16 v[152:155], v[104:107], a[120:123], v[152:155]\n"
"ds_read_b64_tr_b16 v[216:217], v228 offset:57344\n"
"v_mfma_f32_16x16x32_bf16 v[152:155], v[100:103], a[116:119], v[152:155]\n"
"s_add_u32 s40, s41, s53\n"
"ds_read_b64_tr_b16 v[218:219], v228 offset:59392\n"
"v_xor_b32 v228, s75, v228\n"
"v_mfma_f32_16x16x32_bf16 v[152:155], v[96:99], a[112:115], v[152:155]\n"
"buffer_load_dwordx4 v240, s[28:31], s40 offen offset:2048 lds\n"
"v_mfma_f32_16x16x32_bf16 v[168:171], v[96:99], a[128:131], v[196:199]\n"
"ds_read_b64_tr_b16 v[220:221], v232 offset:57344\n"
"v_mfma_f32_16x16x32_bf16 v[168:171], v[100:103], a[132:135], v[168:171]\n"
"ds_read_b64_tr_b16 v[222:223], v232 offset:59392\n"
"v_xor_b32 v232, s75, v232\n"
"v_mfma_f32_16x16x32_bf16 v[168:171], v[104:107], a[136:139], v[168:171]\n"
"ds_read_b64_tr_b16 v[96:97], v229 offset:32768\n"
"v_mfma_f32_16x16x32_bf16 v[168:171], v[108:111], a[140:143], v[168:171]\n"
"ds_read_b64_tr_b16 v[98:99], v229 offset:34816\n"
"v_mfma_f32_16x16x32_bf16 v[168:171], v[112:115], a[176:179], v[168:171]\n"
"ds_read_b64_tr_b16 v[100:101], v233 offset:32768\n"
"v_mfma_f32_16x16x32_bf16 v[168:171], v[116:119], a[180:183], v[168:171]\n"
"ds_read_b64_tr_b16 v[102:103], v233 offset:34816\n"
"v_mfma_f32_16x16x32_bf16 v[168:171], v[120:123], a[184:187], v[168:171]\n"
"s_add_u32 s40, s41, s54\n"
"ds_read_b64_tr_b16 v[104:105], v229 offset:40960\n"
"v_mfma_f32_16x16x32_bf16 v[168:171], v[224:227], a[188:191], v[168:171]\n"
"s_waitcnt lgkmcnt(7)\n"
"buffer_load_dwordx4 v240, s[28:31], s40 offen offset:3072 lds\n"
"v_mfma_f32_16x16x32_bf16 v[124:127], v[180:183], a[48:51], v[124:127]\n"
"ds_read_b64_tr_b16 v[106:107], v229 offset:43008\n"
"v_mfma_f32_16x16x32_bf16 v[124:127], v[184:187], a[52:55], v[124:127]\n"
"ds_read_b64_tr_b16 v[108:109], v233 offset:40960\n"
"v_mfma_f32_16x16x32_bf16 v[124:127], v[188:191], a[56:59], v[124:127]\n"
"ds_read_b64_tr_b16 v[110:111], v233 offset:43008\n"
"v_mfma_f32_16x16x32_bf16 v[124:127], v[192:195], a[60:63], v[124:127]\n"
"ds_read_b64_tr_b16 v[112:113], v229 offset:49152\n"
"v_mfma_f32_16x16x32_bf16 v[124:127], v[208:211], a[0:3], v[124:127]\n"
"ds_read_b64_tr_b16 v[114:115], v229 offset:51200\n"
"v_mfma_f32_16x16x32_bf16 v[124:127], v[212:215], a[4:7], v[124:127]\n"
"s_add_u32 s41, s70, 327680\n"
"ds_read_b64_tr_b16 v[116:117], v233 offset:49152\n"
"v_mfma_f32_16x16x32_bf16 v[124:127], v[216:219], a[8:11], v[124:127]\n"
"s_waitcnt lgkmcnt(6)\n"
"s_add_u32 m0, s71, 32768\n"
"ds_read_b64_tr_b16 v[118:119], v233 offset:51200\n"
"v_mfma_f32_16x16x32_bf16 v[124:127], v[220:223], a[12:15], v[124:127]\n"
"buffer_load_dwordx4 v240, s[28:31], s41 offen lds\n"
"v_mfma_f32_16x16x32_bf16 v[140:143], v[220:223], a[28:31], v[140:143]\n"
"ds_read_b64_tr_b16 v[120:121], v229 offset:57344\n"
"v_mfma_f32_16x16x32_bf16 v[140:143], v[216:219], a[24:27], v[140:143]\n"
"ds_read_b64_tr_b16 v[122:123], v229 offset:59392\n"
"v_xor_b32 v229, s75, v229\n"
"v_mfma_f32_16x16x32_bf16 v[140:143], v[212:215], a[20:23], v[140:143]\n"
"ds_read_b64_tr_b16 v[224:225], v233 offset:57344\n"
"v_mfma_f32_16x16x32_bf16 v[140:143], v[208:211], a[16:19], v[140:143]\n"
"ds_read_b64_tr_b16 v[226:227], v233 offset:59392\n"
"v_xor_b32 v233, s75, v233\n"
"v_mfma_f32_16x16x32_bf16 v[140:143], v[192:195], a[76:79], v[140:143]\n"
"v_mfma_f32_16x16x32_bf16 v[140:143], v[188:191], a[72:75], v[140:143]\n"
"v_subrev_f32 v124, s22, v124\n"
"v_subrev_f32 v125, s22, v125\n"
"v_mfma_f32_16x16x32_bf16 v[140:143], v[184:187], a[68:71], v[140:143]\n"
"s_add_u32 s40, s41, s52\n"
"v_subrev_f32 v126, s22, v126\n"
"v_subrev_f32 v127, s22, v127\n"
"v_mfma_f32_16x16x32_bf16 v[140:143], v[180:183], a[64:67], v[140:143]\n"
"buffer_load_dwordx4 v240, s[28:31], s40 offen offset:1024 lds\n"
"v_mfma_f32_16x16x32_bf16 v[156:159], v[180:183], a[80:83], v[156:159]\n"
"v_cvt_pk_bf16_f32 v124, v124, v125\n"
"v_cvt_pk_bf16_f32 v125, v126, v127\n"
"v_mfma_f32_16x16x32_bf16 v[156:159], v[184:187], a[84:87], v[156:159]\n"
"v_mfma_f32_16x16x32_bf16 v[156:159], v[188:191], a[88:91], v[156:159]\n"
"v_mfma_f32_16x16x32_bf16 v[156:159], v[192:195], a[92:95], v[156:159]\n"
"v_mfma_f32_16x16x32_bf16 v[156:159], v[208:211], a[32:35], v[156:159]\n"
"v_mfma_f32_16x16x32_bf16 v[156:159], v[212:215], a[36:39], v[156:159]\n"
"v_subrev_f32 v140, s22, v140\n"
"v_subrev_f32 v141, s22, v141\n"
"v_mfma_f32_16x16x32_bf16 v[156:159], v[216:219], a[40:43], v[156:159]\n"
"s_waitcnt vmcnt(7)\n"
"v_subrev_f32 v142, s22, v142\n"
"v_subrev_f32 v143, s22, v143\n"
"v_mfma_f32_16x16x32_bf16 v[156:159], v[220:223], a[44:47], v[156:159]\n"
"s_barrier\n"
"buffer_store_dword v241, v248, s[84:87], 0 offen sc1\n"
"v_mfma_f32_16x16x32_bf16 v[172:175], v[220:223], a[228:231], v[172:175]\n"
"ds_read_b64_tr_b16 v[220:221], v234 offset:57344\n"
"v_cvt_pk_bf16_f32 v140, v140, v141\n"
"v_mfma_f32_16x16x32_bf16 v[172:175], v[216:219], a[224:227], v[172:175]\n"
"ds_read_b64_tr_b16 v[216:217], v230 offset:57344\n"
"v_cvt_pk_bf16_f32 v141, v142, v143\n"
"v_mfma_f32_16x16x32_bf16 v[172:175], v[212:215], a[212:215], v[172:175]\n"
"ds_read_b64_tr_b16 v[212:213], v234 offset:49152\n"
"v_add_u32 v241, 1, v241\n"
"v_mfma_f32_16x16x32_bf16 v[172:175], v[208:211], a[208:211], v[172:175]\n"
"s_add_u32 s16, s90, s62\n"
"buffer_load_dwordx4 v[84:87], v243, s[32:35], s18 offen offset:3072 sc1\n"
"v_mfma_f32_16x16x32_bf16 v[172:175], v[192:195], a[236:239], v[172:175]\n"
"s_add_u32 s17, s16, 4096\n"
"ds_read_b64_tr_b16 v[192:193], v234 offset:40960\n"
"v_mfma_f32_16x16x32_bf16 v[172:175], v[188:191], a[232:235], v[172:175]\n"
"s_lshl_b32 s62, s70, 9\n"
"ds_read_b64_tr_b16 v[188:189], v230 offset:40960\n"
"v_subrev_f32 v156, s22, v156\n"
"v_mfma_f32_16x16x32_bf16 v[172:175], v[184:187], a[220:223], v[172:175]\n"
"s_add_u32 s40, s41, s53\n"
"ds_read_b64_tr_b16 v[184:185], v234 offset:32768\n"
"v_subrev_f32 v157, s22, v157\n"
"v_mfma_f32_16x16x32_bf16 v[172:175], v[180:183], a[216:219], v[172:175]\n"
"buffer_load_dwordx4 v240, s[28:31], s40 offen offset:2048 lds\n"
"v_mfma_f32_16x16x32_bf16 v[128:131], v[96:99], a[48:51], v[128:131]\n"
"ds_read_b64_tr_b16 v[180:181], v230 offset:32768\n"
"v_subrev_f32 v158, s22, v158\n"
"v_mfma_f32_16x16x32_bf16 v[128:131], v[100:103], a[52:55], v[128:131]\n"
"s_waitcnt lgkmcnt(5)\n"
"ds_read_b64_tr_b16 v[182:183], v230 offset:34816\n"
"v_subrev_f32 v159, s22, v159\n"
"v_mfma_f32_16x16x32_bf16 v[128:131], v[104:107], a[56:59], v[128:131]\n"
"ds_read_b64_tr_b16 v[186:187], v234 offset:34816\n"
"v_cvt_pk_bf16_f32 v156, v156, v157\n"
"v_mfma_f32_16x16x32_bf16 v[128:131], v[108:111], a[60:63], v[128:131]\n"
"ds_read_b64_tr_b16 v[190:191], v230 offset:43008\n"
"v_cvt_pk_bf16_f32 v157, v158, v159\n"
"v_mfma_f32_16x16x32_bf16 v[128:131], v[112:115], a[0:3], v[128:131]\n"
"ds_read_b64_tr_b16 v[194:195], v234 offset:43008\n"
"v_lshlrev_b32 v202, 16, v72\n"
"v_mfma_f32_16x16x32_bf16 v[128:131], v[116:119], a[4:7], v[128:131]\n"
"ds_read_b64_tr_b16 v[208:209], v230 offset:49152\n"
"v_and_b32 v72, s73, v72\n"
"v_mfma_f32_16x16x32_bf16 v[128:131], v[120:123], a[8:11], v[128:131]\n"
"s_add_u32 s40, s41, s54\n"
"ds_read_b64_tr_b16 v[210:211], v230 offset:51200\n"
"v_add_f32 v0, v0, v202\n"
"v_mfma_f32_16x16x32_bf16 v[128:131], v[224:227], a[12:15], v[128:131]\n"
"buffer_load_dwordx4 v240, s[28:31], s40 offen offset:3072 lds\n"
"v_mfma_f32_16x16x32_bf16 v[144:147], v[224:227], a[28:31], v[144:147]\n"
"ds_read_b64_tr_b16 v[214:215], v234 offset:51200\n"
"v_add_f32 v1, v1, v72\n"
"v_mfma_f32_16x16x32_bf16 v[144:147], v[120:123], a[24:27], v[144:147]\n"
"ds_read_b64_tr_b16 v[218:219], v230 offset:59392\n"
"v_lshlrev_b32 v203, 16, v73\n"
"v_mfma_f32_16x16x32_bf16 v[144:147], v[116:119], a[20:23], v[144:147]\n"
"ds_read_b64_tr_b16 v[222:223], v234 offset:59392\n"
"v_and_b32 v73, s73, v73\n"
"v_mfma_f32_16x16x32_bf16 v[144:147], v[112:115], a[16:19], v[144:147]\n"
"buffer_load_dwordx4 v[88:91], v243, s[32:35], s19 offen sc1\n"
"v_mfma_f32_16x16x32_bf16 v[144:147], v[108:111], a[76:79], v[144:147]\n"
"v_add_f32 v2, v2, v203\n"
"v_add_f32 v3, v3, v73\n"
"v_mfma_f32_16x16x32_bf16 v[144:147], v[104:107], a[72:75], v[144:147]\n"
"s_add_u32 s41, s70, 491520\n"
"v_subrev_f32 v128, s22, v128\n"
"v_subrev_f32 v129, s22, v129\n"
"v_mfma_f32_16x16x32_bf16 v[144:147], v[100:103], a[68:71], v[144:147]\n"
"s_add_u32 m0, s71, 49152\n"
"v_subrev_f32 v130, s22, v130\n"
"v_subrev_f32 v131, s22, v131\n"
"v_mfma_f32_16x16x32_bf16 v[144:147], v[96:99], a[64:67], v[144:147]\n"
"buffer_load_dwordx4 v240, s[28:31], s41 offen lds\n"
"v_mfma_f32_16x16x32_bf16 v[160:163], v[96:99], a[80:83], v[160:163]\n"
"s_and_b32 s70, s88, 15\n"
"v_cvt_pk_bf16_f32 v126, v128, v129\n"
"v_cvt_pk_bf16_f32 v127, v130, v131\n"
"v_mfma_f32_16x16x32_bf16 v[160:163], v[100:103], a[84:87], v[160:163]\n"
"s_lshr_b32 s88, s88, 4\n"
"v_lshlrev_b32 v204, 16, v74\n"
"v_and_b32 v74, s73, v74\n"
"v_mfma_f32_16x16x32_bf16 v[160:163], v[104:107], a[88:91], v[160:163]\n"
"s_lshl_b32 s70, s70, 7\n"
"v_add_f32 v4, v4, v204\n"
"v_add_f32 v5, v5, v74\n"
"v_mfma_f32_16x16x32_bf16 v[160:163], v[108:111], a[92:95], v[160:163]\n"
"s_xor_b32 s71, s71, s75\n"
"v_lshlrev_b32 v205, 16, v75\n"
"v_and_b32 v75, s73, v75\n"
"v_mfma_f32_16x16x32_bf16 v[160:163], v[112:115], a[32:35], v[160:163]\n"
"v_add_f32 v6, v6, v205\n"
"v_add_f32 v7, v7, v75\n"
"v_mfma_f32_16x16x32_bf16 v[160:163], v[116:119], a[36:39], v[160:163]\n"
"v_subrev_f32 v144, s22, v144\n"
"v_subrev_f32 v145, s22, v145\n"
"v_mfma_f32_16x16x32_bf16 v[160:163], v[120:123], a[40:43], v[160:163]\n"
"v_subrev_f32 v146, s22, v146\n"
"v_subrev_f32 v147, s22, v147\n"
"v_mfma_f32_16x16x32_bf16 v[160:163], v[224:227], a[44:47], v[160:163]\n"
"buffer_store_dwordx4 v[124:127], v243, s[32:35], s16 offen sc0 sc1\n"
"v_cvt_pk_bf16_f32 v142, v144, v145\n"
"v_mfma_f32_16x16x32_bf16 v[176:179], v[224:227], a[228:231], v[176:179]\n"
"ds_read_b64_tr_b16 v[224:225], v235 offset:57344\n"
"v_cvt_pk_bf16_f32 v143, v146, v147\n"
"v_mfma_f32_16x16x32_bf16 v[176:179], v[120:123], a[224:227], v[176:179]\n"
"ds_read_b64_tr_b16 v[120:121], v231 offset:57344\n"
"v_lshlrev_b32 v206, 16, v76\n"
"v_mfma_f32_16x16x32_bf16 v[176:179], v[116:119], a[212:215], v[176:179]\n"
"ds_read_b64_tr_b16 v[116:117], v235 offset:49152\n"
"v_and_b32 v76, s73, v76\n"
"v_mfma_f32_16x16x32_bf16 v[176:179], v[112:115], a[208:211], v[176:179]\n"
"buffer_load_dwordx4 v[92:95], v243, s[32:35], s19 offen offset:1024 sc1\n"
"v_mfma_f32_16x16x32_bf16 v[176:179], v[108:111], a[236:239], v[176:179]\n"
"ds_read_b64_tr_b16 v[108:109], v235 offset:40960\n"
"v_add_f32 v8, v8, v206\n"
"v_mfma_f32_16x16x32_bf16 v[176:179], v[104:107], a[232:235], v[176:179]\n"
"ds_read_b64_tr_b16 v[104:105], v231 offset:40960\n"
"v_subrev_f32 v160, s22, v160\n"
"v_mfma_f32_16x16x32_bf16 v[176:179], v[100:103], a[220:223], v[176:179]\n"
"s_add_u32 s40, s41, s52\n"
"ds_read_b64_tr_b16 v[100:101], v235 offset:32768\n"
"v_subrev_f32 v161, s22, v161\n"
"v_mfma_f32_16x16x32_bf16 v[176:179], v[96:99], a[216:219], v[176:179]\n"
"s_waitcnt vmcnt(14) lgkmcnt(6)\n"
"buffer_load_dwordx4 v240, s[28:31], s40 offen offset:1024 lds\n"
"v_mfma_f32_16x16x32_bf16 v[132:135], v[180:183], a[48:51], v[132:135]\n"
"ds_read_b64_tr_b16 v[96:97], v231 offset:32768\n"
"v_subrev_f32 v162, s22, v162\n"
"v_mfma_f32_16x16x32_bf16 v[132:135], v[184:187], a[52:55], v[132:135]\n"
"ds_read_b64_tr_b16 v[98:99], v231 offset:34816\n"
"v_subrev_f32 v163, s22, v163\n"
"v_mfma_f32_16x16x32_bf16 v[132:135], v[188:191], a[56:59], v[132:135]\n"
"ds_read_b64_tr_b16 v[102:103], v235 offset:34816\n"
"v_cvt_pk_bf16_f32 v158, v160, v161\n"
"v_mfma_f32_16x16x32_bf16 v[132:135], v[192:195], a[60:63], v[132:135]\n"
"ds_read_b64_tr_b16 v[106:107], v231 offset:43008\n"
"v_cvt_pk_bf16_f32 v159, v162, v163\n"
"v_mfma_f32_16x16x32_bf16 v[132:135], v[208:211], a[0:3], v[132:135]\n"
"ds_read_b64_tr_b16 v[110:111], v235 offset:43008\n"
"v_add_f32 v9, v9, v76\n"
"v_mfma_f32_16x16x32_bf16 v[132:135], v[212:215], a[4:7], v[132:135]\n"
"ds_read_b64_tr_b16 v[112:113], v231 offset:49152\n"
"v_lshlrev_b32 v207, 16, v77\n"
"v_mfma_f32_16x16x32_bf16 v[132:135], v[216:219], a[8:11], v[132:135]\n"
"ds_read_b64_tr_b16 v[114:115], v231 offset:51200\n"
"v_and_b32 v77, s73, v77\n"
"v_mfma_f32_16x16x32_bf16 v[132:135], v[220:223], a[12:15], v[132:135]\n"
"buffer_store_dwordx4 v[140:143], v243, s[32:35], s16 offen offset:2048 sc0 sc1\n"
"v_add_f32 v10, v10, v207\n"
"v_mfma_f32_16x16x32_bf16 v[148:151], v[220:223], a[28:31], v[148:151]\n"
"ds_read_b64_tr_b16 v[118:119], v235 offset:51200\n"
"v_add_f32 v11, v11, v77\n"
"v_mfma_f32_16x16x32_bf16 v[148:151], v[216:219], a[24:27], v[148:151]\n"
"ds_read_b64_tr_b16 v[122:123], v231 offset:59392\n"
"v_lshlrev_b32 v202, 16, v78\n"
"v_mfma_f32_16x16x32_bf16 v[148:151], v[212:215], a[20:23], v[148:151]\n"
"ds_read_b64_tr_b16 v[226:227], v235 offset:59392\n"
"v_and_b32 v78, s73, v78\n"
"v_mfma_f32_16x16x32_bf16 v[148:151], v[208:211], a[16:19], v[148:151]\n"
"v_add_f32 v12, v12, v202\n"
"v_add_f32 v13, v13, v78\n"
"v_mfma_f32_16x16x32_bf16 v[148:151], v[192:195], a[76:79], v[148:151]\n"
"v_lshlrev_b32 v203, 16, v79\n"
"v_and_b32 v79, s73, v79\n"
"v_mfma_f32_16x16x32_bf16 v[148:151], v[188:191], a[72:75], v[148:151]\n"
"v_add_f32 v14, v14, v203\n"
"v_add_f32 v15, v15, v79\n"
"v_mfma_f32_16x16x32_bf16 v[148:151], v[184:187], a[68:71], v[148:151]\n"
"s_add_u32 s40, s41, s53\n"
"v_lshlrev_b32 v204, 16, v80\n"
"v_and_b32 v80, s73, v80\n"
"v_mfma_f32_16x16x32_bf16 v[148:151], v[180:183], a[64:67], v[148:151]\n"
"buffer_load_dwordx4 v240, s[28:31], s40 offen offset:2048 lds\n"
"v_mfma_f32_16x16x32_bf16 v[164:167], v[180:183], a[80:83], v[164:167]\n"
"v_add_f32 v16, v16, v204\n"
"v_add_f32 v17, v17, v80\n"
"v_mfma_f32_16x16x32_bf16 v[164:167], v[184:187], a[84:87], v[164:167]\n"
"v_lshlrev_b32 v205, 16, v81\n"
"v_and_b32 v81, s73, v81\n"
"v_mfma_f32_16x16x32_bf16 v[164:167], v[188:191], a[88:91], v[164:167]\n"
"v_add_f32 v18, v18, v205\n"
"v_add_f32 v19, v19, v81\n"
"v_mfma_f32_16x16x32_bf16 v[164:167], v[192:195], a[92:95], v[164:167]\n"
"buffer_load_dwordx4 v[244:247], v243, s[32:35], s19 offen offset:2048 sc1\n"
"v_mfma_f32_16x16x32_bf16 v[164:167], v[208:211], a[32:35], v[164:167]\n"
"s_lshl4_add_u32 s18, s72, s61\n"
"v_lshlrev_b32 v206, 16, v82\n"
"v_and_b32 v82, s73, v82\n"
"v_mfma_f32_16x16x32_bf16 v[164:167], v[212:215], a[36:39], v[164:167]\n"
"s_lshl_b32 s19, s72, 2\n"
"v_add_f32 v20, v20, v206\n"
"v_add_f32 v21, v21, v82\n"
"v_mfma_f32_16x16x32_bf16 v[164:167], v[216:219], a[40:43], v[164:167]\n"
"s_waitcnt vmcnt(14)\n"
"s_lshr_b32 s19, s89, s19\n"
"v_lshlrev_b32 v207, 16, v83\n"
"v_mfma_f32_16x16x32_bf16 v[164:167], v[220:223], a[44:47], v[164:167]\n"
"s_barrier\n"
"s_and_b32 s19, s19, 1\n"
"s_waitcnt lgkmcnt(0)\n"
"v_mfma_f32_16x16x32_bf16 v[136:139], v[96:99], a[48:51], v[136:139]\n"
"s_lshl3_add_u32 s18, s18, s19\n"
"ds_read_b64_tr_b16 v[180:181], v228 offset:0\n"
"v_and_b32 v83, s73, v83\n"
"v_mfma_f32_16x16x32_bf16 v[136:139], v[100:103], a[52:55], v[136:139]\n"
"s_lshl_b32 s18, s18, 13\n"
"ds_read_b64_tr_b16 v[182:183], v228 offset:2048\n"
"v_add_f32 v22, v22, v207\n"
"v_mfma_f32_16x16x32_bf16 v[136:139], v[104:107], a[56:59], v[136:139]\n"
"s_add_u32 s19, s18, 4096\n"
"v_lshlrev_b32_e64 v201, 2, s72\n"
"ds_read_b64_tr_b16 v[184:185], v232 offset:0\n"
"v_mfma_f32_16x16x32_bf16 v[136:139], v[108:111], a[60:63], v[136:139]\n"
"s_mov_b32 s69, 15\n"
"global_load_dword v200, v201, s[6:7] sc1\n"
"v_mfma_f32_16x16x32_bf16 v[136:139], v[112:115], a[0:3], v[136:139]\n"
"s_add_u32 s21, s21, 1\n"
"ds_read_b64_tr_b16 v[186:187], v232 offset:2048\n"
"v_add_f32 v23, v23, v83\n"
"v_mfma_f32_16x16x32_bf16 v[136:139], v[116:119], a[4:7], v[136:139]\n"
"ds_read_b64_tr_b16 v[188:189], v228 offset:8192\n"
"v_subrev_f32 v172, s22, v172\n"
"v_mfma_f32_16x16x32_bf16 v[136:139], v[120:123], a[8:11], v[136:139]\n"
"ds_read_b64_tr_b16 v[190:191], v228 offset:10240\n"
"v_subrev_f32 v173, s22, v173\n"
"v_mfma_f32_16x16x32_bf16 v[136:139], v[224:227], a[12:15], v[136:139]\n"
"buffer_store_dwordx4 v[156:159], v243, s[32:35], s17 offen sc0 sc1\n"
"v_subrev_f32 v174, s22, v174\n"
"v_mfma_f32_16x16x32_bf16 v[152:155], v[224:227], a[28:31], v[152:155]\n"
"ds_read_b64_tr_b16 v[192:193], v232 offset:8192\n"
"v_subrev_f32 v175, s22, v175\n"
"v_mfma_f32_16x16x32_bf16 v[152:155], v[120:123], a[24:27], v[152:155]\n"
"ds_read_b64_tr_b16 v[194:195], v232 offset:10240\n"
"v_cvt_pk_bf16_f32 v172, v172, v173\n"
"v_mfma_f32_16x16x32_bf16 v[152:155], v[116:119], a[20:23], v[152:155]\n"
"ds_read_b64_tr_b16 v[208:209], v228 offset:16384\n"
"v_cvt_pk_bf16_f32 v173, v174, v175\n"
"v_mfma_f32_16x16x32_bf16 v[152:155], v[112:115], a[16:19], v[152:155]\n"
"ds_read_b64_tr_b16 v[210:211], v228 offset:18432\n"
"v_subrev_f32 v176, s22, v176\n"
"v_mfma_f32_16x16x32_bf16 v[152:155], v[108:111], a[76:79], v[152:155]\n"
"ds_read_b64_tr_b16 v[212:213], v232 offset:16384\n"
"v_subrev_f32 v177, s22, v177\n"
"v_mfma_f32_16x16x32_bf16 v[152:155], v[104:107], a[72:75], v[152:155]\n"
"ds_read_b64_tr_b16 v[214:215], v232 offset:18432\n"
"v_subrev_f32 v178, s22, v178\n"
"v_mfma_f32_16x16x32_bf16 v[152:155], v[100:103], a[68:71], v[152:155]\n"
"s_add_u32 s40, s41, s54\n"
"ds_read_b64_tr_b16 v[216:217], v228 offset:24576\n"
"v_subrev_f32 v179, s22, v179\n"
"v_mfma_f32_16x16x32_bf16 v[152:155], v[96:99], a[64:67], v[152:155]\n"
"buffer_load_dwordx4 v240, s[28:31], s40 offen offset:3072 lds\n"
"v_mfma_f32_16x16x32_bf16 v[168:171], v[96:99], a[80:83], v[168:171]\n"
"ds_read_b64_tr_b16 v[218:219], v228 offset:26624\n"
"v_cvt_pk_bf16_f32 v174, v176, v177\n"
"v_mfma_f32_16x16x32_bf16 v[168:171], v[100:103], a[84:87], v[168:171]\n"
"ds_read_b64_tr_b16 v[220:221], v232 offset:24576\n"
"v_cvt_pk_bf16_f32 v175, v178, v179\n"
"v_mfma_f32_16x16x32_bf16 v[168:171], v[104:107], a[88:91], v[168:171]\n"
"ds_read_b64_tr_b16 v[222:223], v232 offset:26624\n"
"v_subrev_f32 v132, s22, v132\n"
"v_mfma_f32_16x16x32_bf16 v[168:171], v[108:111], a[92:95], v[168:171]\n"
"ds_read_b64_tr_b16 v[96:97], v229 offset:0\n"
"v_subrev_f32 v133, s22, v133\n"
"v_mfma_f32_16x16x32_bf16 v[168:171], v[112:115], a[32:35], v[168:171]\n"
"ds_read_b64_tr_b16 v[98:99], v229 offset:2048\n"
"v_subrev_f32 v134, s22, v134\n"
"v_mfma_f32_16x16x32_bf16 v[168:171], v[116:119], a[36:39], v[168:171]\n"
"ds_read_b64_tr_b16 v[100:101], v233 offset:0\n"
"v_subrev_f32 v135, s22, v135\n"
"v_mfma_f32_16x16x32_bf16 v[168:171], v[120:123], a[40:43], v[168:171]\n"
"s_add_u32 s47, s47, 1\n"
"ds_read_b64_tr_b16 v[102:103], v233 offset:2048\n"
"v_cvt_pk_bf16_f32 v132, v132, v133\n"
"v_mfma_f32_16x16x32_bf16 v[168:171], v[224:227], a[44:47], v[168:171]\n"
"s_waitcnt lgkmcnt(7)\n"
"s_cmp_lg_u32 s47, 9\n"
"ds_read_b64_tr_b16 v[104:105], v229 offset:8192\n"
"s_cbranch_scc1 LGL_%=\n"
"v_mfma_f32_16x16x32_bf16 v[0:3], v[180:183], a[96:99], v[0:3]\n"
"v_subrev_f32 v136, s22, v136\n"
"v_subrev_f32 v138, s22, v138\n"
"v_mfma_f32_16x16x32_bf16 v[0:3], v[184:187], a[100:103], v[0:3]\n"
"v_subrev_f32 v151, s22, v151\n"
"v_subrev_f32 v139, s22, v139\n"
"v_mfma_f32_16x16x32_bf16 v[0:3], v[188:191], a[104:107], v[0:3]\n"
"v_mov_b32 v201, v237\n"
"v_cvt_pk_bf16_f32 v133, v134, v135\n"
"v_subrev_f32 v150, s22, v150\n"
"v_mfma_f32_16x16x32_bf16 v[0:3], v[192:195], a[108:111], v[0:3]\n"
"global_load_dword v200, v201, s[6:7] sc1\n"
"v_mfma_f32_16x16x32_bf16 v[0:3], v[208:211], a[144:147], v[0:3]\n"
"v_subrev_f32 v153, s22, v153\n"
"v_subrev_f32 v169, s22, v169\n"
"v_mfma_f32_16x16x32_bf16 v[0:3], v[212:215], a[148:151], v[0:3]\n"
"s_waitcnt lgkmcnt(2)\n"
"v_subrev_f32 v137, s22, v137\n"
"v_subrev_f32 v152, s22, v152\n"
"v_mfma_f32_16x16x32_bf16 v[0:3], v[216:219], a[152:155], v[0:3]\n"
"v_cvt_pk_bf16_f32 v134, v136, v137\n"
"v_subrev_f32 v168, s22, v168\n"
"v_mfma_f32_16x16x32_bf16 v[0:3], v[220:223], a[156:159], v[0:3]\n"
"buffer_store_dwordx4 v[172:175], v243, s[32:35], s17 offen offset:2048 sc0 sc1\n"
"v_cvt_pk_bf16_f32 v135, v138, v139\n"
"v_subrev_f32 v170, s22, v170\n"
"v_subrev_f32 v171, s22, v171\n"
"v_mfma_f32_16x16x32_bf16 v[16:19], v[220:223], a[172:175], v[16:19]\n"
"buffer_store_dwordx4 v[132:135], v243, s[32:35], s16 offen offset:1024 sc0 sc1\n"
"v_subrev_f32 v165, s22, v165\n"
"v_mfma_f32_16x16x32_bf16 v[16:19], v[216:219], a[168:171], v[16:19]\n"
"v_subrev_f32 v148, s22, v148\n"
"v_subrev_f32 v149, s22, v149\n"
"v_mfma_f32_16x16x32_bf16 v[16:19], v[212:215], a[164:167], v[16:19]\n"
"v_cvt_pk_bf16_f32 v148, v148, v149\n"
"v_subrev_f32 v167, s22, v167\n"
"v_mfma_f32_16x16x32_bf16 v[16:19], v[208:211], a[160:163], v[16:19]\n"
"v_cvt_pk_bf16_f32 v149, v150, v151\n"
"v_subrev_f32 v155, s22, v155\n"
"v_mfma_f32_16x16x32_bf16 v[16:19], v[192:195], a[124:127], v[16:19]\n"
"v_subrev_f32 v164, s22, v164\n"
"v_subrev_f32 v166, s22, v166\n"
"v_mfma_f32_16x16x32_bf16 v[16:19], v[188:191], a[120:123], v[16:19]\n"
"v_cvt_pk_bf16_f32 v164, v164, v165\n"
"v_subrev_f32 v154, s22, v154\n"
"v_mfma_f32_16x16x32_bf16 v[16:19], v[184:187], a[116:119], v[16:19]\n"
"v_cvt_pk_bf16_f32 v150, v152, v153\n"
"v_cvt_pk_bf16_f32 v151, v154, v155\n"
"v_mfma_f32_16x16x32_bf16 v[16:19], v[180:183], a[112:115], v[16:19]\n"
"s_waitcnt vmcnt(16)\n"
"buffer_store_dwordx4 v[148:151], v243, s[32:35], s16 offen offset:3072 sc0 sc1\n"
"v_cvt_pk_bf16_f32 v165, v166, v167\n"
"v_cvt_pk_bf16_f32 v166, v168, v169\n"
"v_cvt_pk_bf16_f32 v167, v170, v171\n"
"v_mfma_f32_16x16x32_bf16 v[32:35], v[180:183], a[128:131], v[32:35]\n"
"buffer_store_dwordx4 v[164:167], v243, s[32:35], s17 offen offset:1024 sc0 sc1\n"
"ds_read_b64_tr_b16 v[108:109], v233 offset:8192\n"
"v_mfma_f32_16x16x32_bf16 v[32:35], v[184:187], a[132:135], v[32:35]\n"
"ds_read_b64_tr_b16 v[122:123], v229 offset:26624\n"
"v_lshlrev_b32 v202, 16, v84\n"
"v_mfma_f32_16x16x32_bf16 v[32:35], v[188:191], a[136:139], v[32:35]\n"
"ds_read_b64_tr_b16 v[224:225], v233 offset:24576\n"
"v_xor_b32 v230, s75, v230\n"
"v_mfma_f32_16x16x32_bf16 v[32:35], v[192:195], a[140:143], v[32:35]\n"
"ds_read_b64_tr_b16 v[118:119], v233 offset:18432\n"
"v_bfe_u32 v161, v242, 8, 2\n"
"v_mfma_f32_16x16x32_bf16 v[32:35], v[208:211], a[176:179], v[32:35]\n"
"ds_read_b64_tr_b16 v[226:227], v233 offset:26624\n"
"v_bfe_u32 v160, v242, 6, 2\n"
"v_mfma_f32_16x16x32_bf16 v[32:35], v[212:215], a[180:183], v[32:35]\n"
"ds_read_b64_tr_b16 v[114:115], v229 offset:18432\n"
"v_mov_b32 v165, 0\n"
"v_mfma_f32_16x16x32_bf16 v[32:35], v[216:219], a[184:187], v[32:35]\n"
"ds_read_b64_tr_b16 v[120:121], v229 offset:24576\n"
"v_xor_b32 v160, v160, v161\n"
"v_mfma_f32_16x16x32_bf16 v[32:35], v[220:223], a[188:191], v[32:35]\n"
"ds_read_b64_tr_b16 v[116:117], v233 offset:16384\n"
"v_xor_b32 v234, s75, v234\n"
"v_mfma_f32_16x16x32_bf16 v[48:51], v[220:223], a[252:255], v[48:51]\n"
"ds_read_b64_tr_b16 v[110:111], v233 offset:10240\n"
"v_lshlrev_b32 v204, 16, v86\n"
"v_mfma_f32_16x16x32_bf16 v[48:51], v[216:219], a[248:251], v[48:51]\n"
"ds_read_b64_tr_b16 v[106:107], v229 offset:10240\n"
"v_lshlrev_b32 v205, 16, v87\n"
"v_mfma_f32_16x16x32_bf16 v[48:51], v[212:215], a[244:247], v[48:51]\n"
"ds_read_b64_tr_b16 v[112:113], v229 offset:16384\n"
"v_lshlrev_b32 v203, 16, v85\n"
"v_mfma_f32_16x16x32_bf16 v[48:51], v[208:211], a[240:243], v[48:51]\n"
"ds_read_b64_tr_b16 v[210:211], v230 offset:18432\n"
"v_add_f32 v24, v24, v202\n"
"v_mfma_f32_16x16x32_bf16 v[48:51], v[192:195], a[204:207], v[48:51]\n"
"ds_read_b64_tr_b16 v[220:221], v234 offset:24576\n"
"v_xor_b32 v231, s75, v231\n"
"v_mfma_f32_16x16x32_bf16 v[48:51], v[188:191], a[200:203], v[48:51]\n"
"ds_read_b64_tr_b16 v[194:195], v234 offset:10240\n"
"v_add_f32 v26, v26, v203\n"
"v_mfma_f32_16x16x32_bf16 v[48:51], v[184:187], a[196:199], v[48:51]\n"
"ds_read_b64_tr_b16 v[222:223], v234 offset:26624\n"
"v_add_f32 v28, v28, v204\n"
"v_mfma_f32_16x16x32_bf16 v[48:51], v[180:183], a[192:195], v[48:51]\n"
"s_waitcnt vmcnt(15)\n"
"ds_read_b64_tr_b16 v[180:181], v230 offset:0\n"
"v_lshlrev_b32 v202, 16, v90\n"
"v_mfma_f32_16x16x32_bf16 v[4:7], v[96:99], a[96:99], v[4:7]\n"
"s_waitcnt lgkmcnt(7)\n"
"ds_read_b64_tr_b16 v[218:219], v230 offset:26624\n"
"v_lshlrev_b32 v203, 16, v91\n"
"v_mfma_f32_16x16x32_bf16 v[4:7], v[100:103], a[100:103], v[4:7]\n"
"s_waitcnt lgkmcnt(7)\n"
"ds_read_b64_tr_b16 v[216:217], v230 offset:24576\n"
"v_and_b32 v91, s73, v91\n"
"v_mfma_f32_16x16x32_bf16 v[4:7], v[104:107], a[104:107], v[4:7]\n"
"ds_read_b64_tr_b16 v[190:191], v230 offset:10240\n"
"v_and_b32 v90, s73, v90\n"
"v_mfma_f32_16x16x32_bf16 v[4:7], v[108:111], a[108:111], v[4:7]\n"
"s_waitcnt lgkmcnt(7)\n"
"ds_read_b64_tr_b16 v[214:215], v234 offset:18432\n"
"v_add_u32 v160, -1, v160\n"
"v_mfma_f32_16x16x32_bf16 v[4:7], v[112:115], a[144:147], v[4:7]\n"
"s_waitcnt vmcnt(4)\n"
"ds_read_b64_tr_b16 v[208:209], v230 offset:16384\n"
"v_add_f32 v37, v37, v90\n"
"v_mfma_f32_16x16x32_bf16 v[4:7], v[116:119], a[148:151], v[4:7]\n"
"v_subrev_u32 v200, s21, v200\n"
"ds_read_b64_tr_b16 v[186:187], v234 offset:2048\n"
"v_mfma_f32_16x16x32_bf16 v[4:7], v[120:123], a[152:155], v[4:7]\n"
"v_cmp_lt_u32 vcc, s69, v200\n"
"ds_read_b64_tr_b16 v[192:193], v234 offset:8192\n"
"v_mfma_f32_16x16x32_bf16 v[4:7], v[224:227], a[156:159], v[4:7]\n"
"ds_read_b64_tr_b16 v[188:189], v230 offset:8192\n"
"s_cbranch_vccnz LGQ_%=\nLGQK_%=:\n"
"v_mfma_f32_16x16x32_bf16 v[20:23], v[224:227], a[172:175], v[20:23]\n"
"s_bfe_u32 s20, s89, 65536\ns_lshl_b32 s20, s20, 13\ns_add_u32 s20, s20, 0\n"
"ds_read_b64_tr_b16 v[184:185], v234 offset:0\n"
"v_add_f32 v39, v39, v91\n"
"v_mfma_f32_16x16x32_bf16 v[20:23], v[120:123], a[168:171], v[20:23]\n"
"buffer_load_dwordx4 v[124:127], v236, s[32:35], s20 offen sc1\n"
"v_mfma_f32_16x16x32_bf16 v[20:23], v[116:119], a[164:167], v[20:23]\n"
"v_add_f32 v30, v30, v205\n"
"v_ashrrev_i32 v160, 31, v160\n"
"v_mfma_f32_16x16x32_bf16 v[20:23], v[112:115], a[160:163], v[20:23]\n"
"s_bfe_u32 s20, s89, 65540\ns_lshl_b32 s20, s20, 13\ns_add_u32 s20, s20, 1048576\n"
"v_add_f32 v36, v36, v202\n"
"v_mfma_f32_16x16x32_bf16 v[20:23], v[108:111], a[124:127], v[20:23]\n"
"buffer_load_dwordx4 v[128:131], v236, s[32:35], s20 offen sc1\n"
"v_mfma_f32_16x16x32_bf16 v[20:23], v[104:107], a[120:123], v[20:23]\n"
"v_add_f32 v38, v38, v203\n"
"v_mfma_f32_16x16x32_bf16 v[20:23], v[100:103], a[116:119], v[20:23]\n"
"s_bfe_u32 s20, s89, 65544\ns_lshl_b32 s20, s20, 13\ns_add_u32 s20, s20, 2097152\n"
"v_and_b32 v84, s73, v84\n"
"v_lshlrev_b32 v207, 16, v89\n"
"v_mfma_f32_16x16x32_bf16 v[20:23], v[96:99], a[112:115], v[20:23]\n"
"buffer_load_dwordx4 v[132:135], v236, s[32:35], s20 offen sc1\n"
"v_mfma_f32_16x16x32_bf16 v[36:39], v[96:99], a[128:131], v[36:39]\n"
"v_and_b32 v85, s73, v85\n"
"v_lshlrev_b32 v204, 16, v92\n"
"v_mfma_f32_16x16x32_bf16 v[36:39], v[100:103], a[132:135], v[36:39]\n"
"s_bfe_u32 s20, s89, 65548\ns_lshl_b32 s20, s20, 13\ns_add_u32 s20, s20, 3145728\n"
"ds_read_b64_tr_b16 v[182:183], v230 offset:2048\n"
"ds_read_b64_tr_b16 v[212:213], v234 offset:16384\n"
"v_mfma_f32_16x16x32_bf16 v[36:39], v[104:107], a[136:139], v[36:39]\n"
"buffer_load_dwordx4 v[136:139], v236, s[32:35], s20 offen sc1\n"
"v_mfma_f32_16x16x32_bf16 v[36:39], v[108:111], a[140:143], v[36:39]\n"
"v_lshlrev_b32 v205, 16, v93\n"
"v_mfma_f32_16x16x32_bf16 v[36:39], v[112:115], a[176:179], v[36:39]\n"
"s_bfe_u32 s20, s89, 65552\ns_lshl_b32 s20, s20, 13\ns_add_u32 s20, s20, 4194304\n"
"v_and_b32 v160, 0x3f80, v160\n"
"v_add_f32 v40, v40, v204\n"
"v_mfma_f32_16x16x32_bf16 v[36:39], v[116:119], a[180:183], v[36:39]\n"
"buffer_load_dwordx4 v[140:143], v236, s[32:35], s20 offen sc1\n"
"v_mfma_f32_16x16x32_bf16 v[36:39], v[120:123], a[184:187], v[36:39]\n"
"v_and_b32 v161, 16, v242\n"
"v_lshlrev_b32 v206, 16, v88\n"
"v_mfma_f32_16x16x32_bf16 v[36:39], v[224:227], a[188:191], v[36:39]\n"
"s_bfe_u32 s20, s89, 65556\ns_lshl_b32 s20, s20, 13\ns_add_u32 s20, s20, 5242880\n"
"v_add_f32 v27, v27, v85\n"
"v_add_f32 v34, v34, v207\n"
"v_mfma_f32_16x16x32_bf16 v[52:55], v[224:227], a[252:255], v[52:55]\n"
"buffer_load_dwordx4 v[144:147], v236, s[32:35], s20 offen sc1\n"
"v_mfma_f32_16x16x32_bf16 v[52:55], v[120:123], a[248:251], v[52:55]\n"
"v_and_b32 v86, s73, v86\n"
"v_and_b32 v92, s73, v92\n"
"v_mfma_f32_16x16x32_bf16 v[52:55], v[116:119], a[244:247], v[52:55]\n"
"s_bfe_u32 s20, s89, 65560\ns_lshl_b32 s20, s20, 13\ns_add_u32 s20, s20, 6291456\n"
"v_xor_b32 v235, s75, v235\n"
"v_mov_b32 v164, 0\n"
"v_mfma_f32_16x16x32_bf16 v[52:55], v[112:115], a[240:243], v[52:55]\n"
"buffer_load_dwordx4 v[148:151], v236, s[32:35], s20 offen sc1\n"
"v_mfma_f32_16x16x32_bf16 v[52:55], v[108:111], a[204:207], v[52:55]\n"
"s_bfe_u32 s20, s89, 65564\ns_lshl_b32 s20, s20, 13\ns_add_u32 s20, s20, 7340032\n"
"v_and_b32 v88, s73, v88\n"
"v_lshlrev_b32 v207, 16, v95\n"
"v_mfma_f32_16x16x32_bf16 v[52:55], v[104:107], a[200:203], v[52:55]\n"
"buffer_load_dwordx4 v[152:155], v236, s[32:35], s20 offen sc1\n"
"v_mfma_f32_16x16x32_bf16 v[52:55], v[100:103], a[196:199], v[52:55]\n"
"s_waitcnt vmcnt(8)\n"
"s_add_u32 s21, s21, 1\n"
"v_add_f32 v32, v32, v206\n"
"v_mfma_f32_16x16x32_bf16 v[52:55], v[96:99], a[192:195], v[52:55]\n"
"s_barrier\n"
"s_mov_b32 s52, 40960\n"
"buffer_store_dword v241, v248, s[84:87], 0 offen sc1\n"
"s_waitcnt lgkmcnt(0)\n"
"v_mfma_f32_16x16x32_bf16 v[8:11], v[180:183], a[96:99], v[8:11]\n"
"s_mov_b32 s53, 81920\n"
"ds_read_b64_tr_b16 v[120:121], v231 offset:24576\n"
"v_lshlrev_b32 v206, 16, v94\n"
"v_mfma_f32_16x16x32_bf16 v[8:11], v[184:187], a[100:103], v[8:11]\n"
"ds_read_b64_tr_b16 v[96:97], v231 offset:0\n"
"v_and_b32 v89, s73, v89\n"
"v_mfma_f32_16x16x32_bf16 v[8:11], v[188:191], a[104:107], v[8:11]\n"
"ds_read_b64_tr_b16 v[102:103], v235 offset:2048\n"
"v_add_f32 v46, v46, v207\n"
"v_mfma_f32_16x16x32_bf16 v[8:11], v[192:195], a[108:111], v[8:11]\n"
"v_and_b32 v93, s73, v93\n"
"v_add_f32 v44, v44, v206\n"
"v_mfma_f32_16x16x32_bf16 v[8:11], v[208:211], a[144:147], v[8:11]\n"
"ds_read_b64_tr_b16 v[122:123], v231 offset:26624\n"
"v_add_f32 v43, v43, v93\n"
"v_mfma_f32_16x16x32_bf16 v[8:11], v[212:215], a[148:151], v[8:11]\n"
"ds_read_b64_tr_b16 v[116:117], v235 offset:16384\n"
"v_add_f32 v25, v25, v84\n"
"v_mfma_f32_16x16x32_bf16 v[8:11], v[216:219], a[152:155], v[8:11]\n"
"v_and_b32 v87, s73, v87\n"
"v_and_b32 v94, s73, v94\n"
"v_mfma_f32_16x16x32_bf16 v[8:11], v[220:223], a[156:159], v[8:11]\n"
"ds_read_b64_tr_b16 v[224:225], v235 offset:24576\n"
"v_lshlrev_b32 v202, 16, v246\n"
"v_mfma_f32_16x16x32_bf16 v[24:27], v[220:223], a[172:175], v[24:27]\n"
"v_add_f32 v31, v31, v87\n"
"v_add_f32 v42, v42, v205\n"
"v_mfma_f32_16x16x32_bf16 v[24:27], v[216:219], a[168:171], v[24:27]\n"
"ds_read_b64_tr_b16 v[100:101], v235 offset:0\n"
"v_lshlrev_b32 v207, 16, v245\n"
"v_mfma_f32_16x16x32_bf16 v[24:27], v[212:215], a[164:167], v[24:27]\n"
"v_add_f32 v29, v29, v86\n"
"v_add_f32 v41, v41, v92\n"
"v_mfma_f32_16x16x32_bf16 v[24:27], v[208:211], a[160:163], v[24:27]\n"
"ds_read_b64_tr_b16 v[98:99], v231 offset:2048\n"
"v_and_b32 v95, s73, v95\n"
"v_mfma_f32_16x16x32_bf16 v[24:27], v[192:195], a[124:127], v[24:27]\n"
"ds_read_b64_tr_b16 v[114:115], v231 offset:18432\n"
"v_add_f32 v33, v33, v88\n"
"v_mfma_f32_16x16x32_bf16 v[24:27], v[188:191], a[120:123], v[24:27]\n"
"ds_read_b64_tr_b16 v[110:111], v235 offset:10240\n"
"v_and_b32 v245, s73, v245\n"
"v_mfma_f32_16x16x32_bf16 v[24:27], v[184:187], a[116:119], v[24:27]\n"
"v_add_f32 v45, v45, v94\n"
"v_and_b32 v246, s73, v246\n"
"v_mfma_f32_16x16x32_bf16 v[24:27], v[180:183], a[112:115], v[24:27]\n"
"ds_read_b64_tr_b16 v[108:109], v235 offset:8192\n"
"v_lshlrev_b32 v206, 16, v244\n"
"v_mfma_f32_16x16x32_bf16 v[40:43], v[180:183], a[128:131], v[40:43]\n"
"ds_read_b64_tr_b16 v[106:107], v231 offset:10240\n"
"v_lshlrev_b32 v160, v161, v160\n"
"v_mfma_f32_16x16x32_bf16 v[40:43], v[184:187], a[132:135], v[40:43]\n"
"ds_read_b64_tr_b16 v[104:105], v231 offset:8192\n"
"v_mfma_f32_16x16x32_bf16 v[40:43], v[188:191], a[136:139], v[40:43]\n"
"v_bfe_i32 v161, v242, 5, 1\n"
"v_mfma_f32_16x16x32_bf16 v[40:43], v[192:195], a[140:143], v[40:43]\n"
"ds_read_b64_tr_b16 v[112:113], v231 offset:16384\n"
"v_and_b32 v244, s73, v244\n"
"v_mfma_f32_16x16x32_bf16 v[40:43], v[208:211], a[176:179], v[40:43]\n"
"ds_read_b64_tr_b16 v[118:119], v235 offset:18432\n"
"v_add_f32 v49, v49, v244\n"
"v_mfma_f32_16x16x32_bf16 v[40:43], v[212:215], a[180:183], v[40:43]\n"
"v_add_f32 v52, v52, v202\n"
"v_lshlrev_b32 v203, 16, v247\n"
"v_mfma_f32_16x16x32_bf16 v[40:43], v[216:219], a[184:187], v[40:43]\n"
"ds_read_b64_tr_b16 v[226:227], v235 offset:26624\n"
"v_add_f32 v48, v48, v206\n"
"v_mfma_f32_16x16x32_bf16 v[40:43], v[220:223], a[188:191], v[40:43]\n"
"s_barrier\n"
"s_waitcnt lgkmcnt(5)\n"
"v_mfma_f32_16x16x32_bf16 v[12:15], v[96:99], a[96:99], v[12:15]\n"
"ds_read_b64_tr_b16 v[194:195], v232 offset:43008\n"
"ds_read_b64_tr_b16 v[214:215], v232 offset:51200\n"
"v_mfma_f32_16x16x32_bf16 v[12:15], v[100:103], a[100:103], v[12:15]\n"
"s_waitcnt lgkmcnt(5)\n"
"ds_read_b64_tr_b16 v[186:187], v232 offset:34816\n"
"v_add_f32 v53, v53, v246\n"
"v_mfma_f32_16x16x32_bf16 v[12:15], v[104:107], a[104:107], v[12:15]\n"
"ds_read_b64_tr_b16 v[184:185], v232 offset:32768\n"
"ds_read_b64_tr_b16 v[208:209], v228 offset:49152\n"
"v_mfma_f32_16x16x32_bf16 v[12:15], v[108:111], a[108:111], v[12:15]\n"
"s_waitcnt lgkmcnt(7)\n"
"ds_read_b64_tr_b16 v[188:189], v228 offset:40960\n"
"v_add_f32 v47, v47, v95\n"
"v_mfma_f32_16x16x32_bf16 v[12:15], v[112:115], a[144:147], v[12:15]\n"
"s_waitcnt lgkmcnt(7)\n"
"ds_read_b64_tr_b16 v[180:181], v228 offset:32768\n"
"v_add_f32 v35, v35, v89\n"
"v_mfma_f32_16x16x32_bf16 v[12:15], v[116:119], a[148:151], v[12:15]\n"
"ds_read_b64_tr_b16 v[216:217], v228 offset:57344\n"
"v_add_f32 v51, v51, v245\n"
"v_mfma_f32_16x16x32_bf16 v[12:15], v[120:123], a[152:155], v[12:15]\n"
"s_waitcnt lgkmcnt(8)\n"
"ds_read_b64_tr_b16 v[182:183], v228 offset:34816\n"
"ds_read_b64_tr_b16 v[212:213], v232 offset:49152\n"
"v_mfma_f32_16x16x32_bf16 v[12:15], v[224:227], a[156:159], v[12:15]\n"
"global_load_dword v200, v201, s[6:7] sc1\n"
"v_mfma_f32_16x16x32_bf16 v[28:31], v[224:227], a[172:175], v[28:31]\n"
"v_add_f32 v50, v50, v207\n"
"v_add_f32 v54, v54, v203\n"
"v_mfma_f32_16x16x32_bf16 v[28:31], v[120:123], a[168:171], v[28:31]\n"
"s_waitcnt vmcnt(9)\n"
"v_lshlrev_b32 v58, 16, v125\n"
"v_lshlrev_b32 v62, 16, v127\n"
"v_mfma_f32_16x16x32_bf16 v[28:31], v[116:119], a[164:167], v[28:31]\n"
"ds_read_b64_tr_b16 v[210:211], v228 offset:51200\n"
"ds_read_b64_tr_b16 v[218:219], v228 offset:59392\n"
"v_mfma_f32_16x16x32_bf16 v[28:31], v[112:115], a[160:163], v[28:31]\n"
"ds_read_b64_tr_b16 v[190:191], v228 offset:43008\n"
"v_lshlrev_b32 v60, 16, v126\n"
"v_mfma_f32_16x16x32_bf16 v[28:31], v[108:111], a[124:127], v[28:31]\n"
"s_waitcnt vmcnt(8)\n"
"ds_read_b64_tr_b16 v[192:193], v232 offset:40960\n"
"v_lshlrev_b32 v205, 16, v129\n"
"v_mfma_f32_16x16x32_bf16 v[28:31], v[104:107], a[120:123], v[28:31]\n"
"v_lshlrev_b32 v56, 16, v124\n"
"v_lshlrev_b32 v206, 16, v130\n"
"v_mfma_f32_16x16x32_bf16 v[28:31], v[100:103], a[116:119], v[28:31]\n"
"ds_read_b64_tr_b16 v[222:223], v232 offset:59392\n"
"v_add_f32 v58, v58, v205\n"
"v_mfma_f32_16x16x32_bf16 v[28:31], v[96:99], a[112:115], v[28:31]\n"
"s_waitcnt vmcnt(7)\n"
"v_lshlrev_b32 v204, 16, v128\n"
"v_add_f32 v56, v56, v204\n"
"v_mfma_f32_16x16x32_bf16 v[44:47], v[96:99], a[128:131], v[44:47]\n"
"v_lshlrev_b32 v207, 16, v131\n"
"v_lshlrev_b32 v203, 16, v133\n"
"v_mfma_f32_16x16x32_bf16 v[44:47], v[100:103], a[132:135], v[44:47]\n"
"v_add_f32 v58, v58, v203\n"
"v_lshlrev_b32 v205, 16, v135\n"
"v_mfma_f32_16x16x32_bf16 v[44:47], v[104:107], a[136:139], v[44:47]\n"
"s_waitcnt vmcnt(6)\n"
"ds_read_b64_tr_b16 v[106:107], v229 offset:43008\n"
"v_and_b32 v59, s73, v125\n"
"v_mfma_f32_16x16x32_bf16 v[44:47], v[108:111], a[140:143], v[44:47]\n"
"v_add_f32 v60, v60, v206\n"
"v_lshlrev_b32 v206, 16, v136\n"
"v_mfma_f32_16x16x32_bf16 v[44:47], v[112:115], a[176:179], v[44:47]\n"
"v_lshlrev_b32 v202, 16, v132\n"
"v_and_b32 v132, s73, v132\n"
"v_mfma_f32_16x16x32_bf16 v[44:47], v[116:119], a[180:183], v[44:47]\n"
"s_waitcnt vmcnt(5)\n"
"ds_read_b64_tr_b16 v[100:101], v233 offset:32768\n"
"v_lshlrev_b32 v204, 16, v134\n"
"v_mfma_f32_16x16x32_bf16 v[44:47], v[120:123], a[184:187], v[44:47]\n"
"ds_read_b64_tr_b16 v[220:221], v232 offset:57344\n"
"v_add_f32 v62, v62, v207\n"
"v_mfma_f32_16x16x32_bf16 v[44:47], v[224:227], a[188:191], v[44:47]\n"
"s_waitcnt lgkmcnt(3)\n"
"ds_read_b64_tr_b16 v[224:225], v233 offset:57344\n"
"v_add_f32 v62, v62, v205\n"
"v_mfma_f32_16x16x32_bf16 v[0:3], v[180:183], a[48:51], v[0:3]\n"
"s_waitcnt vmcnt(4)\n"
"ds_read_b64_tr_b16 v[114:115], v229 offset:51200\n"
"v_lshlrev_b32 v207, 16, v137\n"
"v_mfma_f32_16x16x32_bf16 v[0:3], v[184:187], a[52:55], v[0:3]\n"
"v_and_b32 v128, s73, v128\n"
"v_lshlrev_b32 v203, 16, v139\n"
"v_mfma_f32_16x16x32_bf16 v[0:3], v[188:191], a[56:59], v[0:3]\n"
"v_and_b32 v57, s73, v124\n"
"v_add_f32 v60, v60, v204\n"
"v_mfma_f32_16x16x32_bf16 v[0:3], v[192:195], a[60:63], v[0:3]\n"
"s_waitcnt vmcnt(3)\n"
"ds_read_b64_tr_b16 v[96:97], v229 offset:32768\n"
"ds_read_b64_tr_b16 v[112:113], v229 offset:49152\n"
"v_mfma_f32_16x16x32_bf16 v[0:3], v[208:211], a[0:3], v[0:3]\n"
"v_and_b32 v247, s73, v247\n"
"v_add_f32 v58, v58, v207\n"
"v_mfma_f32_16x16x32_bf16 v[0:3], v[212:215], a[4:7], v[0:3]\n"
"s_waitcnt vmcnt(2)\n"
"ds_read_b64_tr_b16 v[102:103], v233 offset:34816\n"
"v_and_b32 v134, s73, v134\n"
"v_mfma_f32_16x16x32_bf16 v[0:3], v[216:219], a[8:11], v[0:3]\n"
"s_waitcnt lgkmcnt(5)\n"
"ds_read_b64_tr_b16 v[120:121], v229 offset:57344\n"
"v_lshlrev_b32 v204, 16, v140\n"
"v_mfma_f32_16x16x32_bf16 v[0:3], v[220:223], a[12:15], v[0:3]\n"
"ds_read_b64_tr_b16 v[98:99], v229 offset:34816\n"
"v_and_b32 v61, s73, v126\n"
"v_add_f32 v57, v57, v128\n"
"v_add_f32 v56, v56, v202\n"
"v_and_b32 v135, s73, v135\n"
"v_and_b32 v136, s73, v136\n"
"v_and_b32 v137, s73, v137\n"
"v_lshlrev_b32 v202, 16, v138\n"
"v_mfma_f32_16x16x32_bf16 v[16:19], v[220:223], a[28:31], v[16:19]\n"
"ds_read_b64_tr_b16 v[122:123], v229 offset:59392\n"
"v_and_b32 v129, s73, v129\n"
"v_mfma_f32_16x16x32_bf16 v[16:19], v[216:219], a[24:27], v[16:19]\n"
"ds_read_b64_tr_b16 v[108:109], v233 offset:40960\n"
"v_add_f32 v56, v56, v206\n"
"v_mfma_f32_16x16x32_bf16 v[16:19], v[212:215], a[20:23], v[16:19]\n"
"v_add_f32 v60, v60, v202\n"
"v_lshlrev_b32 v207, 16, v143\n"
"v_mfma_f32_16x16x32_bf16 v[16:19], v[208:211], a[16:19], v[16:19]\n"
"ds_read_b64_tr_b16 v[116:117], v233 offset:49152\n"
"v_lshlrev_b32 v205, 16, v141\n"
"v_mfma_f32_16x16x32_bf16 v[16:19], v[192:195], a[76:79], v[16:19]\n"
"s_waitcnt vmcnt(0)\n"
"v_add_f32 v55, v55, v247\n"
"v_lshlrev_b32 v202, 16, v144\n"
"v_mfma_f32_16x16x32_bf16 v[16:19], v[188:191], a[72:75], v[16:19]\n"
"v_subrev_u32 v200, s21, v200\n"
"ds_read_b64_tr_b16 v[104:105], v229 offset:40960\n"
"v_mfma_f32_16x16x32_bf16 v[16:19], v[184:187], a[68:71], v[16:19]\n"
"v_cmp_lt_u32 vcc, s69, v200\n"
"v_add_f32 v62, v62, v203\n"
"v_mfma_f32_16x16x32_bf16 v[16:19], v[180:183], a[64:67], v[16:19]\n"
"ds_read_b64_tr_b16 v[118:119], v233 offset:51200\n"
"v_and_b32 v63, s73, v127\n"
"v_add_f32 v59, v59, v129\n"
"v_add_f32 v56, v56, v204\n"
"v_add_f32 v58, v58, v205\n"
"v_lshlrev_b32 v206, 16, v142\n"
"v_lshlrev_b32 v203, 16, v145\n"
"v_lshlrev_b32 v205, 16, v147\n"
"v_subrev_f32 v1, s22, v1\n"
"s_cbranch_vccnz LGR_%=\nLGRK_%=:\n"
"v_mfma_f32_16x16x32_bf16 v[32:35], v[180:183], a[80:83], v[32:35]\n"
"ds_read_b64_tr_b16 v[110:111], v233 offset:43008\n"
"s_cmp_eq_u32 s93, 0\ns_cbranch_scc1 LEP_%=\nLEPK_%=:\n"
"v_mfma_f32_16x16x32_bf16 v[32:35], v[184:187], a[84:87], v[32:35]\n"
"s_bfe_u32 s20, s89, 65536\ns_lshl_b32 s20, s20, 13\ns_add_u32 s20, s20, 65536\n"
"ds_read_b64_tr_b16 v[226:227], v233 offset:59392\n"
"v_add_f32 v57, v57, v132\n"
"v_mfma_f32_16x16x32_bf16 v[32:35], v[188:191], a[88:91], v[32:35]\n"
"buffer_load_dwordx4 v[72:75], v236, s[32:35], s20 offen sc1\n"
"v_mfma_f32_16x16x32_bf16 v[32:35], v[192:195], a[92:95], v[32:35]\n"
"v_and_b32 v131, s73, v131\n"
"v_add_f32 v58, v58, v203\n"
"v_mfma_f32_16x16x32_bf16 v[32:35], v[208:211], a[32:35], v[32:35]\n"
"s_bfe_u32 s20, s89, 65540\ns_lshl_b32 s20, s20, 13\ns_add_u32 s20, s20, 1114112\n"
"v_subrev_f32 v2, s22, v2\n"
"v_subrev_f32 v16, s22, v16\n"
"v_mfma_f32_16x16x32_bf16 v[32:35], v[212:215], a[36:39], v[32:35]\n"
"buffer_load_dwordx4 v[76:79], v236, s[32:35], s20 offen sc1\n"
"v_mfma_f32_16x16x32_bf16 v[32:35], v[216:219], a[40:43], v[32:35]\n"
"v_and_b32 v139, s73, v139\n"
"v_add_f32 v62, v62, v207\n"
"v_mfma_f32_16x16x32_bf16 v[32:35], v[220:223], a[44:47], v[32:35]\n"
"s_bfe_u32 s20, s89, 65544\ns_lshl_b32 s20, s20, 13\ns_add_u32 s20, s20, 2162688\n"
"v_add_f32 v62, v62, v205\n"
"v_lshlrev_b32 v207, 16, v149\n"
"v_mfma_f32_16x16x32_bf16 v[48:51], v[220:223], a[228:231], v[48:51]\n"
"buffer_load_dwordx4 v[80:83], v236, s[32:35], s20 offen sc1\n"
"v_mfma_f32_16x16x32_bf16 v[48:51], v[216:219], a[224:227], v[48:51]\n"
"v_and_b32 v140, s73, v140\n"
"v_and_b32 v142, s73, v142\n"
"v_mfma_f32_16x16x32_bf16 v[48:51], v[212:215], a[212:215], v[48:51]\n"
"s_bfe_u32 s20, s89, 65548\ns_lshl_b32 s20, s20, 13\ns_add_u32 s20, s20, 3211264\n"
"v_add_f32 v57, v57, v136\n"
"v_subrev_f32 v3, s22, v3\n"
"v_mfma_f32_16x16x32_bf16 v[48:51], v[208:211], a[208:211], v[48:51]\n"
"buffer_load_dwordx4 v[84:87], v236, s[32:35], s20 offen sc1\n"
"v_mfma_f32_16x16x32_bf16 v[48:51], v[192:195], a[236:239], v[48:51]\n"
"v_lshlrev_b32 v204, 16, v146\n"
"v_and_b32 v147, s73, v147\n"
"v_mfma_f32_16x16x32_bf16 v[48:51], v[188:191], a[232:235], v[48:51]\n"
"s_bfe_u32 s20, s89, 65552\ns_lshl_b32 s20, s20, 13\ns_add_u32 s20, s20, 4259840\n"
"v_and_b32 v138, s73, v138\n"
"v_and_b32 v144, s73, v144\n"
"v_mfma_f32_16x16x32_bf16 v[48:51], v[184:187], a[220:223], v[48:51]\n"
"buffer_load_dwordx4 v[88:91], v236, s[32:35], s20 offen sc1\n"
"v_mfma_f32_16x16x32_bf16 v[48:51], v[180:183], a[216:219], v[48:51]\n"
"s_waitcnt lgkmcnt(0)\n"
"v_and_b32 v133, s73, v133\n"
"v_add_f32 v58, v58, v207\n"
"v_mfma_f32_16x16x32_bf16 v[4:7], v[96:99], a[48:51], v[4:7]\n"
"s_bfe_u32 s20, s89, 65556\ns_lshl_b32 s20, s20, 13\ns_add_u32 s20, s20, 5308416\n"
"v_add_f32 v59, v59, v133\n"
"v_add_f32 v60, v60, v206\n"
"v_mfma_f32_16x16x32_bf16 v[4:7], v[100:103], a[52:55], v[4:7]\n"
"buffer_load_dwordx4 v[92:95], v236, s[32:35], s20 offen sc1\n"
"v_mfma_f32_16x16x32_bf16 v[4:7], v[104:107], a[56:59], v[4:7]\n"
"v_subrev_f32 v0, s22, v0\n"
"v_subrev_f32 v17, s22, v17\n"
"v_mfma_f32_16x16x32_bf16 v[4:7], v[108:111], a[60:63], v[4:7]\n"
"s_bfe_u32 s20, s89, 65560\ns_lshl_b32 s20, s20, 13\ns_add_u32 s20, s20, 6356992\n"
"v_add_f32 v56, v56, v202\n"
"v_cvt_pk_bf16_f32 v16, v16, v17\n"
"v_mfma_f32_16x16x32_bf16 v[4:7], v[112:115], a[0:3], v[4:7]\n"
"buffer_load_dwordx4 v[244:247], v236, s[32:35], s20 offen sc1\n"
"v_and_b32 v130, s73, v130\n"
"v_add_f32 v59, v59, v137\n"
"v_add_f32 v57, v57, v140\n"
"v_add_f32 v60, v60, v204\n"
"v_lshlrev_b32 v202, 16, v150\n"
"v_lshlrev_b32 v203, 16, v151\n"
"v_cvt_pk_bf16_f32 v0, v0, v1\n"
"v_subrev_f32 v32, s22, v32\n"
"v_mfma_f32_16x16x32_bf16 v[4:7], v[116:119], a[4:7], v[4:7]\n"
"v_add_f32 v63, v63, v131\n"
"v_lshlrev_b32 v206, 16, v148\n"
"v_mfma_f32_16x16x32_bf16 v[4:7], v[120:123], a[8:11], v[4:7]\n"
"s_bfe_u32 s20, s89, 65564\ns_lshl_b32 s20, s20, 13\ns_add_u32 s20, s20, 7405568\n"
"v_and_b32 v151, s73, v151\n"
"v_lshlrev_b32 v204, 16, v152\n"
"v_mfma_f32_16x16x32_bf16 v[4:7], v[224:227], a[12:15], v[4:7]\n"
"buffer_load_dwordx4 v[156:159], v236, s[32:35], s20 offen sc1\n"
"v_mfma_f32_16x16x32_bf16 v[20:23], v[224:227], a[28:31], v[20:23]\n"
"v_add_f32 v57, v57, v144\n"
"v_and_b32 v146, s73, v146\n"
"v_mfma_f32_16x16x32_bf16 v[20:23], v[120:123], a[24:27], v[20:23]\n"
"v_and_b32 v141, s73, v141\n"
"v_cvt_pk_bf16_f32 v1, v2, v3\n"
"v_mfma_f32_16x16x32_bf16 v[20:23], v[116:119], a[20:23], v[20:23]\n"
"v_lshlrev_b32 v207, 16, v155\n"
"v_subrev_f32 v19, s22, v19\n"
"v_mfma_f32_16x16x32_bf16 v[20:23], v[112:115], a[16:19], v[20:23]\n"
"v_add_f32 v62, v62, v203\n"
"v_subrev_f32 v18, s22, v18\n"
"v_mfma_f32_16x16x32_bf16 v[20:23], v[108:111], a[76:79], v[20:23]\n"
"ds_read_b64_tr_b16 v[210:211], v230 offset:51200\n"
"v_add_f32 v61, v61, v130\n"
"v_add_f32 v63, v63, v135\n"
"v_and_b32 v143, s73, v143\n"
"v_and_b32 v148, s73, v148\n"
"v_add_f32 v56, v56, v206\n"
"v_and_b32 v149, s73, v149\n"
"v_add_f32 v56, v56, v204\n"
"v_subrev_f32 v6, s22, v6\n"
"v_subrev_f32 v7, s22, v7\n"
"v_mfma_f32_16x16x32_bf16 v[20:23], v[104:107], a[72:75], v[20:23]\n"
"ds_read_b64_tr_b16 v[182:183], v230 offset:34816\n"
"v_subrev_f32 v35, s22, v35\n"
"v_mfma_f32_16x16x32_bf16 v[20:23], v[100:103], a[68:71], v[20:23]\n"
"v_and_b32 v155, s73, v155\n"
"v_subrev_f32 v4, s22, v4\n"
"v_mfma_f32_16x16x32_bf16 v[20:23], v[96:99], a[64:67], v[20:23]\n"
"ds_read_b64_tr_b16 v[186:187], v234 offset:34816\n"
"v_add_f32 v61, v61, v134\n"
"v_add_f32 v57, v57, v148\n"
"v_and_b32 v152, s73, v152\n"
"v_add_f32 v57, v57, v152\n"
"v_lshlrev_b32 v205, 16, v153\n"
"v_subrev_f32 v5, s22, v5\n"
"v_cvt_pk_bf16_f32 v2, v4, v5\n"
"v_cvt_pk_bf16_f32 v3, v6, v7\n"
"v_subrev_f32 v34, s22, v34\n"
"v_mfma_f32_16x16x32_bf16 v[36:39], v[96:99], a[80:83], v[36:39]\n"
"ds_read_b64_tr_b16 v[208:209], v230 offset:49152\n"
"v_and_b32 v150, s73, v150\n"
"v_mfma_f32_16x16x32_bf16 v[36:39], v[100:103], a[84:87], v[36:39]\n"
"ds_read_b64_tr_b16 v[192:193], v234 offset:40960\n"
"v_add_f32 v61, v61, v138\n"
"v_add_f32 v63, v63, v139\n"
"v_and_b32 v145, s73, v145\n"
"v_permlane16_swap_b32 v0, v2\n"
"v_permlane16_swap_b32 v1, v3\n"
"v_cvt_pk_bf16_f32 v17, v18, v19\n"
"v_mfma_f32_16x16x32_bf16 v[36:39], v[104:107], a[88:91], v[36:39]\n"
"ds_read_b64_tr_b16 v[214:215], v234 offset:51200\n"
"v_add_f32 v59, v59, v141\n"
"v_mfma_f32_16x16x32_bf16 v[36:39], v[108:111], a[92:95], v[36:39]\n"
"ds_read_b64_tr_b16 v[190:191], v230 offset:43008\n"
"v_add_f32 v61, v61, v142\n"
"v_mfma_f32_16x16x32_bf16 v[36:39], v[112:115], a[32:35], v[36:39]\n"
"ds_read_b64_tr_b16 v[184:185], v234 offset:32768\n"
"buffer_store_dwordx4 v[0:3], v238, s[36:39], 0 offen sc0 sc1\n"
"v_lshlrev_b32 v206, 16, v154\n"
"v_subrev_f32 v20, s22, v20\n"
"v_subrev_f32 v21, s22, v21\n"
"v_subrev_f32 v23, s22, v23\n"
"v_cvt_pk_bf16_f32 v18, v20, v21\n"
"v_mfma_f32_16x16x32_bf16 v[36:39], v[116:119], a[36:39], v[36:39]\n"
"ds_read_b64_tr_b16 v[194:195], v234 offset:43008\n"
"v_and_b32 v154, s73, v154\n"
"v_mfma_f32_16x16x32_bf16 v[36:39], v[120:123], a[40:43], v[36:39]\n"
"ds_read_b64_tr_b16 v[188:189], v230 offset:40960\n"
"v_and_b32 v153, s73, v153\n"
"v_mfma_f32_16x16x32_bf16 v[36:39], v[224:227], a[44:47], v[36:39]\n"
"ds_read_b64_tr_b16 v[212:213], v234 offset:49152\n"
"v_add_f32 v59, v59, v145\n"
"v_add_f32 v59, v59, v149\n"
"v_add_f32 v62, v62, v207\n"
"v_subrev_f32 v22, s22, v22\n"
"v_permlane16_swap_b32 v16, v18\n"
"v_subrev_f32 v48, s22, v48\n"
"v_subrev_f32 v50, s22, v50\n"
"v_subrev_f32 v51, s22, v51\n"
"v_mfma_f32_16x16x32_bf16 v[52:55], v[224:227], a[228:231], v[52:55]\n"
"ds_read_b64_tr_b16 v[220:221], v234 offset:57344\n"
"v_cvt_pk_bf16_f32 v19, v22, v23\n"
"v_mfma_f32_16x16x32_bf16 v[52:55], v[120:123], a[224:227], v[52:55]\n"
"v_add_f32 v60, v60, v202\n"
"v_add_f32 v59, v59, v153\n"
"v_mfma_f32_16x16x32_bf16 v[52:55], v[116:119], a[212:215], v[52:55]\n"
"ds_read_b64_tr_b16 v[180:181], v230 offset:32768\n"
"v_add_f32 v63, v63, v143\n"
"v_add_f32 v61, v61, v146\n"
"v_add_f32 v63, v63, v147\n"
"v_add_f32 v61, v61, v150\n"
"v_add_f32 v63, v63, v151\n"
"v_add_f32 v60, v60, v206\n"
"v_cvt_pk_bf16_f32 v56, v56, v57\n"
"v_permlane16_swap_b32 v17, v19\n"
"v_mfma_f32_16x16x32_bf16 v[52:55], v[112:115], a[208:211], v[52:55]\n"
"v_add_f32 v58, v58, v205\n"
"v_subrev_f32 v37, s22, v37\n"
"v_mfma_f32_16x16x32_bf16 v[52:55], v[108:111], a[236:239], v[52:55]\n"
"ds_read_b64_tr_b16 v[218:219], v230 offset:59392\n"
"v_subrev_f32 v36, s22, v36\n"
"v_mfma_f32_16x16x32_bf16 v[52:55], v[104:107], a[232:235], v[52:55]\n"
"ds_read_b64_tr_b16 v[216:217], v230 offset:57344\n"
"buffer_store_dwordx4 v[16:19], v238, s[36:39], s52 offen sc0 sc1\n"
"v_add_f32 v61, v61, v154\n"
"v_add_f32 v63, v63, v155\n"
"v_cvt_pk_bf16_f32 v57, v58, v59\n"
"v_cvt_pk_bf16_f32 v58, v60, v61\n"
"v_subrev_f32 v38, s22, v38\n"
"v_mfma_f32_16x16x32_bf16 v[52:55], v[100:103], a[220:223], v[52:55]\n"
"ds_read_b64_tr_b16 v[222:223], v234 offset:59392\n"
"v_cvt_pk_bf16_f32 v59, v62, v63\n"
"v_mfma_f32_16x16x32_bf16 v[52:55], v[96:99], a[216:219], v[52:55]\n"
"s_waitcnt lgkmcnt(3)\n"
"v_permlane16_swap_b32 v56, v58\n"
"v_mfma_f32_16x16x32_bf16 v[8:11], v[180:183], a[48:51], v[8:11]\n"
"v_permlane16_swap_b32 v57, v59\n"
"v_mfma_f32_16x16x32_bf16 v[8:11], v[184:187], a[52:55], v[8:11]\n"
"buffer_store_dwordx4 v[56:59], v253, s[36:39], 0 offen sc0 sc1\n"
"v_subrev_f32 v33, s22, v33\n"
"v_subrev_f32 v39, s22, v39\n"
"v_cvt_pk_bf16_f32 v32, v32, v33\n"
"v_cvt_pk_bf16_f32 v33, v34, v35\n"
"v_cvt_pk_bf16_f32 v34, v36, v37\n"
"v_cvt_pk_bf16_f32 v35, v38, v39\n"
"v_mfma_f32_16x16x32_bf16 v[8:11], v[188:191], a[56:59], v[8:11]\n"
"v_subrev_f32 v49, s22, v49\n"
"v_subrev_f32 v52, s22, v52\n"
"v_mfma_f32_16x16x32_bf16 v[8:11], v[192:195], a[60:63], v[8:11]\n"
"v_permlane16_swap_b32 v32, v34\n"
"v_mfma_f32_16x16x32_bf16 v[8:11], v[208:211], a[0:3], v[8:11]\n"
"v_permlane16_swap_b32 v33, v35\n"
"v_mfma_f32_16x16x32_bf16 v[8:11], v[212:215], a[4:7], v[8:11]\n"
"s_waitcnt lgkmcnt(1)\n"
"buffer_store_dwordx4 v[32:35], v238, s[36:39], s53 offen sc0 sc1\n"
"v_cvt_pk_bf16_f32 v48, v48, v49\n"
"v_mfma_f32_16x16x32_bf16 v[8:11], v[216:219], a[8:11], v[8:11]\n"
"s_waitcnt lgkmcnt(0)\n"
"ds_read_b64_tr_b16 v[110:111], v235 offset:43008\n"
"v_subrev_f32 v53, s22, v53\n"
"v_mfma_f32_16x16x32_bf16 v[8:11], v[220:223], a[12:15], v[8:11]\n"
"v_cvt_pk_bf16_f32 v49, v50, v51\n"
"v_cvt_pk_bf16_f32 v50, v52, v53\n"
"v_mfma_f32_16x16x32_bf16 v[24:27], v[220:223], a[28:31], v[24:27]\n"
"s_nop 0\n"
"v_permlane16_swap_b32 v48, v50\n"
"v_mfma_f32_16x16x32_bf16 v[24:27], v[216:219], a[24:27], v[24:27]\n"
"ds_read_b64_tr_b16 v[114:115], v231 offset:51200\n"
"v_subrev_f32 v55, s22, v55\n"
"v_mfma_f32_16x16x32_bf16 v[24:27], v[212:215], a[20:23], v[24:27]\n"
"ds_read_b64_tr_b16 v[102:103], v235 offset:34816\n"
"v_subrev_f32 v54, s22, v54\n"
"v_mfma_f32_16x16x32_bf16 v[24:27], v[208:211], a[16:19], v[24:27]\n"
"ds_read_b64_tr_b16 v[112:113], v231 offset:49152\n"
"v_cvt_pk_bf16_f32 v51, v54, v55\n"
"v_mfma_f32_16x16x32_bf16 v[24:27], v[192:195], a[76:79], v[24:27]\n"
"s_nop 0\n"
"v_permlane16_swap_b32 v49, v51\n"
"v_mfma_f32_16x16x32_bf16 v[24:27], v[188:191], a[72:75], v[24:27]\n"
"buffer_store_dwordx4 v[48:51], v252, s[36:39], 0 offen sc0 sc1\n"
"ds_read_b64_tr_b16 v[106:107], v231 offset:43008\n"
"v_mfma_f32_16x16x32_bf16 v[24:27], v[184:187], a[68:71], v[24:27]\n"
"ds_read_b64_tr_b16 v[96:97], v231 offset:32768\n"
"ds_read_b64_tr_b16 v[98:99], v231 offset:34816\n"
"v_mfma_f32_16x16x32_bf16 v[24:27], v[180:183], a[64:67], v[24:27]\n"
"ds_read_b64_tr_b16 v[100:101], v235 offset:32768\n"
"v_and_b32 v163, v160, v161\n"
"v_mfma_f32_16x16x32_bf16 v[40:43], v[180:183], a[80:83], v[40:43]\n"
"ds_read_b64_tr_b16 v[118:119], v235 offset:51200\n"
"v_xor_b32 v162, v160, v163\n"
"v_mfma_f32_16x16x32_bf16 v[40:43], v[184:187], a[84:87], v[40:43]\n"
"ds_read_b64_tr_b16 v[104:105], v231 offset:40960\n"
"v_mfma_f32_16x16x32_bf16 v[40:43], v[188:191], a[88:91], v[40:43]\n"
"ds_read_b64_tr_b16 v[108:109], v235 offset:40960\n"
"v_subrev_f32 v8, s22, v8\n"
"v_mfma_f32_16x16x32_bf16 v[40:43], v[192:195], a[92:95], v[40:43]\n"
"ds_read_b64_tr_b16 v[116:117], v235 offset:49152\n"
"v_mfma_f32_16x16x32_bf16 v[40:43], v[208:211], a[32:35], v[40:43]\n"
"ds_read_b64_tr_b16 v[122:123], v231 offset:59392\n"
"v_subrev_f32 v9, s22, v9\n"
"v_mfma_f32_16x16x32_bf16 v[40:43], v[212:215], a[36:39], v[40:43]\n"
"ds_read_b64_tr_b16 v[120:121], v231 offset:57344\n"
"v_subrev_f32 v10, s22, v10\n"
"v_mfma_f32_16x16x32_bf16 v[40:43], v[216:219], a[40:43], v[40:43]\n"
"ds_read_b64_tr_b16 v[224:225], v235 offset:57344\n"
"ds_read_b64_tr_b16 v[226:227], v235 offset:59392\n"
"v_mfma_f32_16x16x32_bf16 v[40:43], v[220:223], a[44:47], v[40:43]\n"
"s_waitcnt lgkmcnt(8)\n"
"v_subrev_f32 v11, s22, v11\n"
"v_mfma_f32_16x16x32_bf16 v[12:15], v[96:99], a[48:51], v[12:15]\n"
"v_mfma_f32_16x16x32_bf16 v[12:15], v[100:103], a[52:55], v[12:15]\n"
"s_waitcnt lgkmcnt(6)\n"
"v_mov_b32 v160, 0\n"
"v_subrev_f32 v24, s22, v24\n"
"v_mfma_f32_16x16x32_bf16 v[12:15], v[104:107], a[56:59], v[12:15]\n"
"s_waitcnt lgkmcnt(5)\n"
"v_mfma_f32_16x16x32_bf16 v[12:15], v[108:111], a[60:63], v[12:15]\n"
"v_mfma_f32_16x16x32_bf16 v[12:15], v[112:115], a[0:3], v[12:15]\n"
"s_waitcnt lgkmcnt(3)\n"
"v_cvt_pk_bf16_f32 v8, v8, v9\n"
"v_mfma_f32_16x16x32_bf16 v[12:15], v[116:119], a[4:7], v[12:15]\n"
"s_waitcnt lgkmcnt(2)\n"
"v_mfma_f32_16x16x32_bf16 v[12:15], v[120:123], a[8:11], v[12:15]\n"
"s_waitcnt lgkmcnt(0)\n"
"v_mov_b32 v161, 0\n"
"v_subrev_f32 v25, s22, v25\n"
"v_mfma_f32_16x16x32_bf16 v[12:15], v[224:227], a[12:15], v[12:15]\n"
"v_subrev_f32 v26, s22, v26\n"
"s_waitcnt vmcnt(5)\n"
"v_mfma_f32_16x16x32_bf16 v[64:67], v[162:165], v[72:75], 0\n"
"v_subrev_f32 v40, s22, v40\n"
"v_subrev_f32 v43, s22, v43\n"
"v_mfma_f32_16x16x32_bf16 v[64:67], v[162:165], v[76:79], v[64:67]\n"
"v_subrev_f32 v41, s22, v41\n"
"v_mfma_f32_16x16x32_bf16 v[64:67], v[162:165], v[80:83], v[64:67]\n"
"v_mfma_f32_16x16x32_bf16 v[64:67], v[162:165], v[84:87], v[64:67]\n"
"v_subrev_f32 v13, s22, v13\n"
"v_mfma_f32_16x16x32_bf16 v[64:67], v[162:165], v[88:91], v[64:67]\n"
"v_mfma_f32_16x16x32_bf16 v[64:67], v[162:165], v[92:95], v[64:67]\n"
"v_subrev_f32 v15, s22, v15\n"
"v_cvt_pk_bf16_f32 v40, v40, v41\n"
"v_mfma_f32_16x16x32_bf16 v[64:67], v[162:165], v[244:247], v[64:67]\n"
"v_subrev_f32 v14, s22, v14\n"
"v_cvt_pk_bf16_f32 v9, v10, v11\n"
"v_mfma_f32_16x16x32_bf16 v[64:67], v[162:165], v[156:159], v[64:67]\n"
"v_subrev_f32 v12, s22, v12\n"
"v_cvt_pk_bf16_f32 v24, v24, v25\n"
"v_mfma_f32_16x16x32_bf16 v[68:71], v[160:163], v[72:75], 0\n"
"v_cvt_pk_bf16_f32 v10, v12, v13\n"
"v_cvt_pk_bf16_f32 v11, v14, v15\n"
"v_mfma_f32_16x16x32_bf16 v[68:71], v[160:163], v[76:79], v[68:71]\n"
"s_nop 0\n"
"v_permlane16_swap_b32 v9, v11\n"
"v_mfma_f32_16x16x32_bf16 v[68:71], v[160:163], v[80:83], v[68:71]\n"
"v_cvt_pk_bf16_f32 v64, v64, v65\n"
"v_mfma_f32_16x16x32_bf16 v[68:71], v[160:163], v[84:87], v[68:71]\n"
"v_permlane16_swap_b32 v8, v10\n"
"v_mfma_f32_16x16x32_bf16 v[68:71], v[160:163], v[88:91], v[68:71]\n"
"v_subrev_f32 v42, s22, v42\n"
"v_mfma_f32_16x16x32_bf16 v[68:71], v[160:163], v[92:95], v[68:71]\n"
"buffer_store_dwordx4 v[8:11], v239, s[36:39], 0 offen sc0 sc1\n"
"v_mfma_f32_16x16x32_bf16 v[68:71], v[160:163], v[244:247], v[68:71]\n"
"v_mfma_f32_16x16x32_bf16 v[68:71], v[160:163], v[156:159], v[68:71]\n"
"v_cvt_pk_bf16_f32 v65, v66, v67\n"
"v_mfma_f32_16x16x32_bf16 v[28:31], v[224:227], a[28:31], v[28:31]\n"
"v_subrev_f32 v27, s22, v27\n"
"v_mfma_f32_16x16x32_bf16 v[28:31], v[120:123], a[24:27], v[28:31]\n"
"v_mfma_f32_16x16x32_bf16 v[28:31], v[116:119], a[20:23], v[28:31]\n"
"v_mfma_f32_16x16x32_bf16 v[28:31], v[112:115], a[16:19], v[28:31]\n"
"s_nop 1\n"
"v_cvt_pk_bf16_f32 v66, v68, v69\n"
"v_cvt_pk_bf16_f32 v25, v26, v27\n"
"v_mfma_f32_16x16x32_bf16 v[28:31], v[108:111], a[76:79], v[28:31]\n"
"v_permlane16_swap_b32 v64, v66\n"
"v_mfma_f32_16x16x32_bf16 v[28:31], v[104:107], a[72:75], v[28:31]\n"
"v_cvt_pk_bf16_f32 v67, v70, v71\n"
"v_cvt_pk_bf16_f32 v41, v42, v43\n"
"v_mfma_f32_16x16x32_bf16 v[28:31], v[100:103], a[68:71], v[28:31]\n"
"v_permlane16_swap_b32 v65, v67\n"
"v_mfma_f32_16x16x32_bf16 v[28:31], v[96:99], a[64:67], v[28:31]\n"
"buffer_store_dwordx4 v[64:67], v253, s[36:39], 0 offen offset:128 sc0 sc1\n"
"v_mfma_f32_16x16x32_bf16 v[44:47], v[96:99], a[80:83], v[44:47]\n"
"v_mfma_f32_16x16x32_bf16 v[44:47], v[100:103], a[84:87], v[44:47]\n"
"v_mfma_f32_16x16x32_bf16 v[44:47], v[104:107], a[88:91], v[44:47]\n"
"s_nop 3\n"
"v_subrev_f32 v29, s22, v29\n"
"v_subrev_f32 v30, s22, v30\n"
"v_mfma_f32_16x16x32_bf16 v[44:47], v[108:111], a[92:95], v[44:47]\n"
"v_subrev_f32 v28, s22, v28\n"
"v_subrev_f32 v31, s22, v31\n"
"v_mfma_f32_16x16x32_bf16 v[44:47], v[112:115], a[32:35], v[44:47]\n"
"v_cvt_pk_bf16_f32 v26, v28, v29\n"
"v_cvt_pk_bf16_f32 v27, v30, v31\n"
"v_mfma_f32_16x16x32_bf16 v[44:47], v[116:119], a[36:39], v[44:47]\n"
"v_permlane16_swap_b32 v24, v26\n"
"v_mfma_f32_16x16x32_bf16 v[44:47], v[120:123], a[40:43], v[44:47]\n"
"v_permlane16_swap_b32 v25, v27\n"
"v_mfma_f32_16x16x32_bf16 v[44:47], v[224:227], a[44:47], v[44:47]\n"
"buffer_store_dwordx4 v[24:27], v239, s[36:39], s52 offen sc0 sc1\n"
"s_nop 6\n"
"v_subrev_f32 v44, s22, v44\n"
"v_subrev_f32 v45, s22, v45\n"
"v_subrev_f32 v46, s22, v46\n"
"v_subrev_f32 v47, s22, v47\n"
"v_cvt_pk_bf16_f32 v42, v44, v45\n"
"v_cvt_pk_bf16_f32 v43, v46, v47\n"
"s_nop 0\n"
"v_permlane16_swap_b32 v40, v42\n"
"v_permlane16_swap_b32 v41, v43\n"
"buffer_store_dwordx4 v[40:43], v239, s[36:39], s53 offen sc0 sc1\n"
"s_branch LFIN_%=\n"
"LSW_%=:\n"
"s_add_u32 s16, s8, s95\n"
"s_addc_u32 s17, s9, 0\n"
"s_sub_u32 s18, 655360, s95\n"
"s_add_u32 s20, s8, s100\n"
"s_addc_u32 s21, s9, 0\n"
"s_sub_u32 s22, 655360, s100\n"
"s_mov_b32 s56, 0\n"
"s_mov_b32 s55, 0\n"
"s_mov_b32 s58, 0\n"
"s_mov_b32 s59, 163840\n"
"s_mov_b32 s52, 9216\n"
"s_mov_b32 s53, 18432\n"
"s_mov_b32 s54, 27648\n"
"v_lshrrev_b32 v206, 7, v242\n"
"v_mul_u32_u24 v206, 1280, v206\n"
"v_and_b32 v240, 0x7f, v240\n"
"v_add_u32 v240, v206, v240\n"
"buffer_load_dword v252, v240, s[16:19], 0 offen\n"
"s_branch LSWK_%=\n"
"LGP_%=:\n"
"s_mov_b32 s80, 0x40000\n"
"LGPa_%=:\n"
"s_sleep 1\n"
"global_load_dword v200, v201, s[6:7] sc1\n"
"s_sub_u32 s80, s80, 1\n"
"s_cmp_eq_u32 s80, 0\n"
"s_waitcnt vmcnt(0)\n"
"s_cbranch_scc1 LGPK_%=\n"
"v_subrev_u32 v200, s21, v200\n"
"v_cmp_lt_u32 vcc, s69, v200\n"
"s_nop 4\n"
"s_cbranch_vccnz LGPa_%=\n"
"s_branch LGPK_%=\n"
"LGQ_%=:\n"
"s_mov_b32 s80, 0x40000\n"
"LGQa_%=:\n"
"s_sleep 1\n"
"global_load_dword v200, v201, s[6:7] sc1\n"
"s_sub_u32 s80, s80, 1\n"
"s_cmp_eq_u32 s80, 0\n"
"s_waitcnt vmcnt(0)\n"
"s_cbranch_scc1 LGQK_%=\n"
"v_subrev_u32 v200, s21, v200\n"
"v_cmp_lt_u32 vcc, s69, v200\n"
"s_nop 4\n"
"s_cbranch_vccnz LGQa_%=\n"
"s_branch LGQK_%=\n"
"LGR_%=:\n"
"s_mov_b32 s80, 0x40000\n"
"LGRa_%=:\n"
"s_sleep 1\n"
"global_load_dword v200, v201, s[6:7] sc1\n"
"s_sub_u32 s80, s80, 1\n"
"s_cmp_eq_u32 s80, 0\n"
"s_waitcnt vmcnt(0)\n"
"s_cbranch_scc1 LGRK_%=\n"
"v_subrev_u32 v200, s21, v200\n"
"v_cmp_lt_u32 vcc, s69, v200\n"
"s_nop 4\n"
"s_cbranch_vccnz LGRa_%=\n"
"s_branch LGRK_%=\n"
"LGV_%=:\n"
"s_mov_b32 s80, 0x40000\n"
"LGVa_%=:\n"
"s_sleep 1\n"
"global_load_dword v200, v201, s[6:7] sc1\n"
"s_sub_u32 s80, s80, 1\n"
"s_cmp_eq_u32 s80, 0\n"
"s_waitcnt vmcnt(0)\n"
"s_cbranch_scc1 LGVK_%=\n"
"v_subrev_u32 v200, s21, v200\n"
"v_cmp_lt_u32 vcc, s69, v200\n"
"s_nop 4\n"
"s_cbranch_vccnz LGVa_%=\n"
"s_branch LGVK_%=\n"
"LEP_%=:\n"
"s_add_u32 s92, s92, 1\n"
"v_mov_b32 v200, s92\n"
"v_mov_b32 v255, 0\n"
"s_mov_b64 exec, 1\n"
"global_store_dword v255, v200, s[6:7] offset:128 sc1\n"
"s_mov_b64 exec, -1\n"
"s_branch LEPK_%=\n"
"LPCE0_%=:\n"
"s_memrealtime s[80:81]\n"
"s_sub_u32 s82, s67, s66\n"
"s_cmp_gt_i32 s82, 0\n"
"s_cselect_b32 s83, s67, s66\n"
"s_waitcnt lgkmcnt(0)\n"
"s_mov_b32 s63, 0\n"
"s_sub_u32 s82, s83, s80\n"
"s_cmp_gt_i32 s82, 3\n"
"s_cbranch_scc0 LPEE0_%=\n"
"LPDE0_%=:\n"
"s_sleep 1\n"
"s_memrealtime s[80:81]\n"
"s_waitcnt lgkmcnt(0)\n"
"s_sub_u32 s82, s83, s80\n"
"s_cmp_gt_i32 s82, 3\n"
"s_cbranch_scc1 LPDE0_%=\n"
"LPEE0_%=:\n"
"s_add_u32 s66, s66, s72\n"
"s_add_u32 s66, s66, 4\n"
"s_add_u32 s67, s80, s72\n"
"s_sub_u32 s72, s72, 0\n"
"s_max_u32 s72, s72, 122\n"
"s_branch LPKE0_%=\n"
"LPCE1_%=:\n"
"s_memrealtime s[80:81]\n"
"s_sub_u32 s82, s67, s66\n"
"s_cmp_gt_i32 s82, 0\n"
"s_cselect_b32 s83, s67, s66\n"
"s_waitcnt lgkmcnt(0)\n"
"s_sub_u32 s82, s83, s80\n"
"s_cmp_gt_i32 s82, 3\n"
"s_cbranch_scc0 LPEE1_%=\n"
"LPDE1_%=:\n"
"s_sleep 1\n"
"s_memrealtime s[80:81]\n"
"s_waitcnt lgkmcnt(0)\n"
"s_sub_u32 s82, s83, s80\n"
"s_cmp_gt_i32 s82, 3\n"
"s_cbranch_scc1 LPDE1_%=\n"
"LPEE1_%=:\n"
"s_add_u32 s66, s66, s72\n"
"s_add_u32 s66, s66, 4\n"
"s_add_u32 s67, s80, s72\n"
"s_sub_u32 s72, s72, 0\n"
"s_max_u32 s72, s72, 122\n"
"s_branch LPKE1_%=\n"
"LPCT0_%=:\n"
"s_memrealtime s[80:81]\n"
"s_sub_u32 s82, s67, s66\n"
"s_cmp_gt_i32 s82, 0\n"
"s_cselect_b32 s83, s67, s66\n"
"s_waitcnt lgkmcnt(0)\n"
"s_sub_u32 s82, s83, s80\n"
"s_cmp_gt_i32 s82, 3\n"
"s_cbranch_scc0 LPET0_%=\n"
"LPDT0_%=:\n"
"s_sleep 1\n"
"s_memrealtime s[80:81]\n"
"s_waitcnt lgkmcnt(0)\n"
"s_sub_u32 s82, s83, s80\n"
"s_cmp_gt_i32 s82, 3\n"
"s_cbranch_scc1 LPDT0_%=\n"
"LPET0_%=:\n"
"s_add_u32 s66, s66, s72\n"
"s_add_u32 s66, s66, 4\n"
"s_add_u32 s67, s80, s72\n"
"s_sub_u32 s72, s72, 0\n"
"s_max_u32 s72, s72, 122\n"
"s_branch LPKT0_%=\n"
"LPCT1_%=:\n"
"s_memrealtime s[80:81]\n"
"s_sub_u32 s82, s67, s66\n"
"s_cmp_gt_i32 s82, 0\n"
"s_cselect_b32 s83, s67, s66\n"
"s_waitcnt lgkmcnt(0)\n"
"s_sub_u32 s82, s83, s80\n"
"s_cmp_gt_i32 s82, 3\n"
"s_cbranch_scc0 LPET1_%=\n"
"LPDT1_%=:\n"
"s_sleep 1\n"
"s_memrealtime s[80:81]\n"
"s_waitcnt lgkmcnt(0)\n"
"s_sub_u32 s82, s83, s80\n"
"s_cmp_gt_i32 s82, 3\n"
"s_cbranch_scc1 LPDT1_%=\n"
"LPET1_%=:\n"
"s_add_u32 s66, s66, s72\n"
"s_add_u32 s66, s66, 4\n"
"s_add_u32 s67, s80, s72\n"
"s_sub_u32 s72, s72, 0\n"
"s_max_u32 s72, s72, 122\n"
"s_branch LPKT1_%=\n"
"LPCL0_%=:\n"
"s_memrealtime s[80:81]\n"
"s_sub_u32 s82, s67, s66\n"
"s_cmp_gt_i32 s82, 0\n"
"s_cselect_b32 s83, s67, s66\n"
"s_waitcnt lgkmcnt(0)\n"
"s_sub_u32 s82, s80, s67\n"
"s_add_u32 s63, s63, 1\n"
"s_min_i32 s81, s63, 0\n"
"s_cmp_gt_i32 s82, 20\n"
"s_cselect_b32 s63, s63, s81\n"
"s_cmp_ge_i32 s63, 2\n"
"s_cbranch_scc0 LPRL0_%=\n"
"s_mov_b32 s63, 0\n"
"s_add_u32 s72, s72, 20\n"
"s_min_u32 s72, s72, 142\n"
"s_add_u32 s83, s80, 200\n"
"s_and_b32 s83, s83, -1\n"
"LPRL0_%=:\n"
"s_sub_u32 s82, s83, s80\n"
"s_cmp_gt_i32 s82, 3\n"
"s_cbranch_scc0 LPEL0_%=\n"
"LPDL0_%=:\n"
"s_sleep 1\n"
"s_memrealtime s[80:81]\n"
"s_waitcnt lgkmcnt(0)\n"
"s_sub_u32 s82, s83, s80\n"
"s_cmp_gt_i32 s82, 3\n"
"s_cbranch_scc1 LPDL0_%=\n"
"LPEL0_%=:\n"
"s_add_u32 s66, s66, s72\n"
"s_add_u32 s66, s66, 4\n"
"s_add_u32 s67, s80, s72\n"
"s_sub_u32 s72, s72, 0\n"
"s_max_u32 s72, s72, 122\n"
"s_branch LPKL0_%=\n"
"LPCL1_%=:\n"
"s_memrealtime s[80:81]\n"
"s_sub_u32 s82, s67, s66\n"
"s_cmp_gt_i32 s82, 0\n"
"s_cselect_b32 s83, s67, s66\n"
"s_waitcnt lgkmcnt(0)\n"
"s_sub_u32 s82, s80, s67\n"
"s_add_u32 s63, s63, 1\n"
"s_min_i32 s81, s63, 0\n"
"s_cmp_gt_i32 s82, 20\n"
"s_cselect_b32 s63, s63, s81\n"
"s_cmp_ge_i32 s63, 2\n"
"s_cbranch_scc0 LPRL1_%=\n"
"s_mov_b32 s63, 0\n"
"s_add_u32 s72, s72, 20\n"
"s_min_u32 s72, s72, 142\n"
"s_add_u32 s83, s80, 200\n"
"s_and_b32 s83, s83, -1\n"
"LPRL1_%=:\n"
"s_sub_u32 s82, s83, s80\n"
"s_cmp_gt_i32 s82, 3\n"
"s_cbranch_scc0 LPEL1_%=\n"
"LPDL1_%=:\n"
"s_sleep 1\n"
"s_memrealtime s[80:81]\n"
"s_waitcnt lgkmcnt(0)\n"
"s_sub_u32 s82, s83, s80\n"
"s_cmp_gt_i32 s82, 3\n"
"s_cbranch_scc1 LPDL1_%=\n"
"LPEL1_%=:\n"
"s_add_u32 s66, s66, s72\n"
"s_add_u32 s66, s66, 4\n"
"s_add_u32 s67, s80, s72\n"
"s_sub_u32 s72, s72, 0\n"
"s_max_u32 s72, s72, 122\n"
"s_branch LPKL1_%=\n"
"LPCL2_%=:\n"
"s_memrealtime s[80:81]\n"
"s_sub_u32 s82, s67, s66\n"
"s_cmp_gt_i32 s82, 0\n"
"s_cselect_b32 s83, s67, s66\n"
"s_waitcnt lgkmcnt(0)\n"
"s_sub_u32 s82, s80, s67\n"
"s_add_u32 s63, s63, 1\n"
"s_min_i32 s81, s63, 0\n"
"s_cmp_gt_i32 s82, 20\n"
"s_cselect_b32 s63, s63, s81\n"
"s_cmp_ge_i32 s63, 2\n"
"s_cbranch_scc0 LPRL2_%=\n"
"s_mov_b32 s63, 0\n"
"s_add_u32 s72, s72, 20\n"
"s_min_u32 s72, s72, 142\n"
"s_add_u32 s83, s80, 200\n"
"s_and_b32 s83, s83, -1\n"
"LPRL2_%=:\n"
"s_sub_u32 s82, s83, s80\n"
"s_cmp_gt_i32 s82, 3\n"
"s_cbranch_scc0 LPEL2_%=\n"
"LPDL2_%=:\n"
"s_sleep 1\n"
"s_memrealtime s[80:81]\n"
"s_waitcnt lgkmcnt(0)\n"
"s_sub_u32 s82, s83, s80\n"
"s_cmp_gt_i32 s82, 3\n"
"s_cbranch_scc1 LPDL2_%=\n"
"LPEL2_%=:\n"
"s_add_u32 s66, s66, s72\n"
"s_add_u32 s66, s66, 4\n"
"s_add_u32 s67, s80, s72\n"
"s_sub_u32 s72, s72, 0\n"
"s_max_u32 s72, s72, 122\n"
"s_branch LPKL2_%=\n"
"LPCL3_%=:\n"
"s_memrealtime s[80:81]\n"
"s_sub_u32 s82, s67, s66\n"
"s_cmp_gt_i32 s82, 0\n"
"s_cselect_b32 s83, s67, s66\n"
"s_waitcnt lgkmcnt(0)\n"
"s_sub_u32 s82, s80, s67\n"
"s_add_u32 s63, s63, 1\n"
"s_min_i32 s81, s63, 0\n"
"s_cmp_gt_i32 s82, 20\n"
"s_cselect_b32 s63, s63, s81\n"
"s_cmp_ge_i32 s63, 2\n"
"s_cbranch_scc0 LPRL3_%=\n"
"s_mov_b32 s63, 0\n"
"s_add_u32 s72, s72, 20\n"
"s_min_u32 s72, s72, 142\n"
"s_add_u32 s83, s80, 200\n"
"s_and_b32 s83, s83, -1\n"
"LPRL3_%=:\n"
"s_sub_u32 s82, s83, s80\n"
"s_cmp_gt_i32 s82, 3\n"
"s_cbranch_scc0 LPEL3_%=\n"
"LPDL3_%=:\n"
"s_sleep 1\n"
"s_memrealtime s[80:81]\n"
"s_waitcnt lgkmcnt(0)\n"
"s_sub_u32 s82, s83, s80\n"
"s_cmp_gt_i32 s82, 3\n"
"s_cbranch_scc1 LPDL3_%=\n"
"LPEL3_%=:\n"
"s_add_u32 s66, s66, s72\n"
"s_add_u32 s66, s66, 4\n"
"s_add_u32 s67, s80, s72\n"
"s_sub_u32 s72, s72, 0\n"
"s_max_u32 s72, s72, 122\n"
"s_branch LPKL3_%=\n"
"LEND_%=:\n"
"LSLOW_%=:\n"
"s_waitcnt vmcnt(0) lgkmcnt(0)\n"
"s_nop 9\n"
"v_bfe_u32 v148, v242, 4, 4\n"
"v_bfe_u32 v149, v242, 8, 2\n"
"v_lshlrev_b32 v144, 8, v148\n"
"v_lshl_add_u32 v144, v149, 4, v144\n"
"v_add_u32 v144, s43, v144\n"
"v_xor_b32 v145, 64, v144\n"
"v_lshlrev_b32 v146, 1, v148\n"
"v_lshl_add_u32 v146, v149, 10, v146\n"
"v_add_u32 v146, s43, v146\n"
"v_xor_b32 v147, 64, v146\n"
"v_mov_b32 v152, 0xff7fffff\n"
"v_mov_b32 v155, 0\n"
"v_mov_b32 v0, 0\n"
"v_mov_b32 v1, 0\n"
"v_mov_b32 v2, 0\n"
"v_mov_b32 v3, 0\n"
"v_mov_b32 v4, 0\n"
"v_mov_b32 v5, 0\n"
"v_mov_b32 v6, 0\n"
"v_mov_b32 v7, 0\n"
"v_mov_b32 v8, 0\n"
"v_mov_b32 v9, 0\n"
"v_mov_b32 v10, 0\n"
"v_mov_b32 v11, 0\n"
"v_mov_b32 v12, 0\n"
"v_mov_b32 v13, 0\n"
"v_mov_b32 v14, 0\n"
"v_mov_b32 v15, 0\n"
"v_mov_b32 v16, 0\n"
"v_mov_b32 v17, 0\n"
"v_mov_b32 v18, 0\n"
"v_mov_b32 v19, 0\n"
"v_mov_b32 v20, 0\n"
"v_mov_b32 v21, 0\n"
"v_mov_b32 v22, 0\n"
"v_mov_b32 v23, 0\n"
"v_mov_b32 v24, 0\n"
"v_mov_b32 v25, 0\n"
"v_mov_b32 v26, 0\n"
"v_mov_b32 v27, 0\n"
"v_mov_b32 v28, 0\n"
"v_mov_b32 v29, 0\n"
"v_mov_b32 v30, 0\n"
"v_mov_b32 v31, 0\n"
"v_mov_b32 v156, 0xff7fffff\n"
"v_mov_b32 v159, 0\n"
"v_mov_b32 v32, 0\n"
"v_mov_b32 v33, 0\n"
"v_mov_b32 v34, 0\n"
"v_mov_b32 v35, 0\n"
"v_mov_b32 v36, 0\n"
"v_mov_b32 v37, 0\n"
"v_mov_b32 v38, 0\n"
"v_mov_b32 v39, 0\n"
"v_mov_b32 v40, 0\n"
"v_mov_b32 v41, 0\n"
"v_mov_b32 v42, 0\n"
"v_mov_b32 v43, 0\n"
"v_mov_b32 v44, 0\n"
"v_mov_b32 v45, 0\n"
"v_mov_b32 v46, 0\n"
"v_mov_b32 v47, 0\n"
"v_mov_b32 v48, 0\n"
"v_mov_b32 v49, 0\n"
"v_mov_b32 v50, 0\n"
"v_mov_b32 v51, 0\n"
"v_mov_b32 v52, 0\n"
"v_mov_b32 v53, 0\n"
"v_mov_b32 v54, 0\n"
"v_mov_b32 v55, 0\n"
"v_mov_b32 v56, 0\n"
"v_mov_b32 v57, 0\n"
"v_mov_b32 v58, 0\n"
"v_mov_b32 v59, 0\n"
"v_mov_b32 v60, 0\n"
"v_mov_b32 v61, 0\n"
"v_mov_b32 v62, 0\n"
"v_mov_b32 v63, 0\n"
"v_mov_b32 v160, 0xff7fffff\n"
"v_mov_b32 v163, 0\n"
"v_mov_b32 v64, 0\n"
"v_mov_b32 v65, 0\n"
"v_mov_b32 v66, 0\n"
"v_mov_b32 v67, 0\n"
"v_mov_b32 v68, 0\n"
"v_mov_b32 v69, 0\n"
"v_mov_b32 v70, 0\n"
"v_mov_b32 v71, 0\n"
"v_mov_b32 v72, 0\n"
"v_mov_b32 v73, 0\n"
"v_mov_b32 v74, 0\n"
"v_mov_b32 v75, 0\n"
"v_mov_b32 v76, 0\n"
"v_mov_b32 v77, 0\n"
"v_mov_b32 v78, 0\n"
"v_mov_b32 v79, 0\n"
"v_mov_b32 v80, 0\n"
"v_mov_b32 v81, 0\n"
"v_mov_b32 v82, 0\n"
"v_mov_b32 v83, 0\n"
"v_mov_b32 v84, 0\n"
"v_mov_b32 v85, 0\n"
"v_mov_b32 v86, 0\n"
"v_mov_b32 v87, 0\n"
"v_mov_b32 v88, 0\n"
"v_mov_b32 v89, 0\n"
"v_mov_b32 v90, 0\n"
"v_mov_b32 v91, 0\n"
"v_mov_b32 v92, 0\n"
"v_mov_b32 v93, 0\n"
"v_mov_b32 v94, 0\n"
"v_mov_b32 v95, 0\n"
"v_mov_b32 v164, 0xff7fffff\n"
"v_mov_b32 v167, 0\n"
"v_accvgpr_write_b32 a208, 0\n"
"v_accvgpr_write_b32 a209, 0\n"
"v_accvgpr_write_b32 a210, 0\n"
"v_accvgpr_write_b32 a211, 0\n"
"v_accvgpr_write_b32 a212, 0\n"
"v_accvgpr_write_b32 a213, 0\n"
"v_accvgpr_write_b32 a214, 0\n"
"v_accvgpr_write_b32 a215, 0\n"
"v_accvgpr_write_b32 a216, 0\n"
"v_accvgpr_write_b32 a217, 0\n"
"v_accvgpr_write_b32 a218, 0\n"
"v_accvgpr_write_b32 a219, 0\n"
"v_accvgpr_write_b32 a220, 0\n"
"v_accvgpr_write_b32 a221, 0\n"
"v_accvgpr_write_b32 a222, 0\n"
"v_accvgpr_write_b32 a223, 0\n"
"v_accvgpr_write_b32 a224, 0\n"
"v_accvgpr_write_b32 a225, 0\n"
"v_accvgpr_write_b32 a226, 0\n"
"v_accvgpr_write_b32 a227, 0\n"
"v_accvgpr_write_b32 a228, 0\n"
"v_accvgpr_write_b32 a229, 0\n"
"v_accvgpr_write_b32 a230, 0\n"
"v_accvgpr_write_b32 a231, 0\n"
"v_accvgpr_write_b32 a232, 0\n"
"v_accvgpr_write_b32 a233, 0\n"
"v_accvgpr_write_b32 a234, 0\n"
"v_accvgpr_write_b32 a235, 0\n"
"v_accvgpr_write_b32 a236, 0\n"
"v_accvgpr_write_b32 a237, 0\n"
"v_accvgpr_write_b32 a238, 0\n"
"v_accvgpr_write_b32 a239, 0\n"
"s_lshl_b32 s80, s47, 16\n"
"s_sub_u32 s80, s80, 261248\n"
"s_add_u32 s81, s80, 4096\n"
"s_mov_b32 s82, 0\n"
"LSLOOP_%=:\n"
"s_lshl_b32 s83, s82, 6\n"
"s_and_b32 s83, s83, 64\n"
"buffer_load_dwordx4 v[128:131], v144, s[84:87], s80 offen offset:0\n"
"buffer_load_dwordx4 v[132:135], v145, s[84:87], s80 offen offset:0\n"
"buffer_load_dwordx4 v[136:139], v144, s[84:87], s80 offen offset:128\n"
"buffer_load_dwordx4 v[140:143], v145, s[84:87], s80 offen offset:128\n"
"s_waitcnt vmcnt(0)\n"
"v_mfma_f32_16x16x32_bf16 v[96:99], v[128:131], a[0:3], 0\n"
"v_mfma_f32_16x16x32_bf16 v[96:99], v[132:135], a[4:7], v[96:99]\n"
"v_mfma_f32_16x16x32_bf16 v[96:99], v[136:139], a[8:11], v[96:99]\n"
"v_mfma_f32_16x16x32_bf16 v[96:99], v[140:143], a[12:15], v[96:99]\n"
"v_mfma_f32_16x16x32_bf16 v[104:107], v[128:131], a[16:19], 0\n"
"v_mfma_f32_16x16x32_bf16 v[104:107], v[132:135], a[20:23], v[104:107]\n"
"v_mfma_f32_16x16x32_bf16 v[104:107], v[136:139], a[24:27], v[104:107]\n"
"v_mfma_f32_16x16x32_bf16 v[104:107], v[140:143], a[28:31], v[104:107]\n"
"v_mfma_f32_16x16x32_bf16 v[112:115], v[128:131], a[32:35], 0\n"
"v_mfma_f32_16x16x32_bf16 v[112:115], v[132:135], a[36:39], v[112:115]\n"
"v_mfma_f32_16x16x32_bf16 v[112:115], v[136:139], a[40:43], v[112:115]\n"
"v_mfma_f32_16x16x32_bf16 v[112:115], v[140:143], a[44:47], v[112:115]\n"
"v_mfma_f32_16x16x32_bf16 v[120:123], v[128:131], a[192:195], 0\n"
"v_mfma_f32_16x16x32_bf16 v[120:123], v[132:135], a[196:199], v[120:123]\n"
"v_mfma_f32_16x16x32_bf16 v[120:123], v[136:139], a[200:203], v[120:123]\n"
"v_mfma_f32_16x16x32_bf16 v[120:123], v[140:143], a[204:207], v[120:123]\n"
"s_nop 7\n"
"buffer_load_dwordx4 v[128:131], v144, s[84:87], s81 offen offset:0\n"
"buffer_load_dwordx4 v[132:135], v145, s[84:87], s81 offen offset:0\n"
"buffer_load_dwordx4 v[136:139], v144, s[84:87], s81 offen offset:128\n"
"buffer_load_dwordx4 v[140:143], v145, s[84:87], s81 offen offset:128\n"
"s_waitcnt vmcnt(0)\n"
"v_mfma_f32_16x16x32_bf16 v[100:103], v[128:131], a[0:3], 0\n"
"v_mfma_f32_16x16x32_bf16 v[100:103], v[132:135], a[4:7], v[100:103]\n"
"v_mfma_f32_16x16x32_bf16 v[100:103], v[136:139], a[8:11], v[100:103]\n"
"v_mfma_f32_16x16x32_bf16 v[100:103], v[140:143], a[12:15], v[100:103]\n"
"v_mfma_f32_16x16x32_bf16 v[108:111], v[128:131], a[16:19], 0\n"
"v_mfma_f32_16x16x32_bf16 v[108:111], v[132:135], a[20:23], v[108:111]\n"
"v_mfma_f32_16x16x32_bf16 v[108:111], v[136:139], a[24:27], v[108:111]\n"
"v_mfma_f32_16x16x32_bf16 v[108:111], v[140:143], a[28:31], v[108:111]\n"
"v_mfma_f32_16x16x32_bf16 v[116:119], v[128:131], a[32:35], 0\n"
"v_mfma_f32_16x16x32_bf16 v[116:119], v[132:135], a[36:39], v[116:119]\n"
"v_mfma_f32_16x16x32_bf16 v[116:119], v[136:139], a[40:43], v[116:119]\n"
"v_mfma_f32_16x16x32_bf16 v[116:119], v[140:143], a[44:47], v[116:119]\n"
"v_mfma_f32_16x16x32_bf16 v[124:127], v[128:131], a[192:195], 0\n"
"v_mfma_f32_16x16x32_bf16 v[124:127], v[132:135], a[196:199], v[124:127]\n"
"v_mfma_f32_16x16x32_bf16 v[124:127], v[136:139], a[200:203], v[124:127]\n"
"v_mfma_f32_16x16x32_bf16 v[124:127], v[140:143], a[204:207], v[124:127]\n"
"s_nop 7\n"
"v_max3_f32 v153, v96, v97, v98\n"
"v_max3_f32 v153, v153, v99, v100\n"
"v_max3_f32 v153, v153, v101, v102\n"
"v_max3_f32 v153, v153, v103, v152\n"
"v_mov_b32 v154, v153\n"
"s_nop 1\n"
"v_permlane32_swap_b32 v153, v154\n"
"v_max_f32 v153, v153, v154\n"
"v_mov_b32 v154, v153\n"
"s_nop 1\n"
"v_permlane16_swap_b32 v153, v154\n"
"v_max_f32 v153, v153, v154\n"
"v_sub_f32 v154, v152, v153\n"
"v_exp_f32 v154, v154\n"
"v_mov_b32 v152, v153\n"
"v_sub_f32 v96, v96, v152\n"
"v_sub_f32 v97, v97, v152\n"
"v_sub_f32 v98, v98, v152\n"
"v_sub_f32 v99, v99, v152\n"
"v_sub_f32 v100, v100, v152\n"
"v_sub_f32 v101, v101, v152\n"
"v_sub_f32 v102, v102, v152\n"
"v_sub_f32 v103, v103, v152\n"
"v_exp_f32 v96, v96\n"
"v_exp_f32 v97, v97\n"
"v_exp_f32 v98, v98\n"
"v_exp_f32 v99, v99\n"
"v_exp_f32 v100, v100\n"
"v_exp_f32 v101, v101\n"
"v_exp_f32 v102, v102\n"
"v_exp_f32 v103, v103\n"
"v_mul_f32 v155, v155, v154\n"
"v_add_f32 v155, v155, v96\n"
"v_add_f32 v155, v155, v97\n"
"v_add_f32 v155, v155, v98\n"
"v_add_f32 v155, v155, v99\n"
"v_add_f32 v155, v155, v100\n"
"v_add_f32 v155, v155, v101\n"
"v_add_f32 v155, v155, v102\n"
"v_add_f32 v155, v155, v103\n"
"v_cvt_pk_bf16_f32 v96, v96, v97\n"
"v_cvt_pk_bf16_f32 v97, v98, v99\n"
"v_cvt_pk_bf16_f32 v98, v100, v101\n"
"v_cvt_pk_bf16_f32 v99, v102, v103\n"
"v_mov_b32_dpp v154, v154 row_ror:12 row_mask:0x2 bank_mask:0xf\n"
"s_nop 1\n"
"v_mov_b32_dpp v154, v154 row_ror:8 row_mask:0x4 bank_mask:0xf\n"
"s_nop 1\n"
"v_mov_b32_dpp v154, v154 row_ror:4 row_mask:0x8 bank_mask:0xf\n"
"s_nop 1\n"
"v_mul_f32_dpp v0, v154, v0 row_newbcast:0 row_mask:0xf bank_mask:0xf\n"
"v_mul_f32_dpp v1, v154, v1 row_newbcast:1 row_mask:0xf bank_mask:0xf\n"
"v_mul_f32_dpp v2, v154, v2 row_newbcast:2 row_mask:0xf bank_mask:0xf\n"
"v_mul_f32_dpp v3, v154, v3 row_newbcast:3 row_mask:0xf bank_mask:0xf\n"
"v_mul_f32_dpp v4, v154, v4 row_newbcast:0 row_mask:0xf bank_mask:0xf\n"
"v_mul_f32_dpp v5, v154, v5 row_newbcast:1 row_mask:0xf bank_mask:0xf\n"
"v_mul_f32_dpp v6, v154, v6 row_newbcast:2 row_mask:0xf bank_mask:0xf\n"
"v_mul_f32_dpp v7, v154, v7 row_newbcast:3 row_mask:0xf bank_mask:0xf\n"
"v_mul_f32_dpp v8, v154, v8 row_newbcast:0 row_mask:0xf bank_mask:0xf\n"
"v_mul_f32_dpp v9, v154, v9 row_newbcast:1 row_mask:0xf bank_mask:0xf\n"
"v_mul_f32_dpp v10, v154, v10 row_newbcast:2 row_mask:0xf bank_mask:0xf\n"
"v_mul_f32_dpp v11, v154, v11 row_newbcast:3 row_mask:0xf bank_mask:0xf\n"
"v_mul_f32_dpp v12, v154, v12 row_newbcast:0 row_mask:0xf bank_mask:0xf\n"
"v_mul_f32_dpp v13, v154, v13 row_newbcast:1 row_mask:0xf bank_mask:0xf\n"
"v_mul_f32_dpp v14, v154, v14 row_newbcast:2 row_mask:0xf bank_mask:0xf\n"
"v_mul_f32_dpp v15, v154, v15 row_newbcast:3 row_mask:0xf bank_mask:0xf\n"
"v_mul_f32_dpp v16, v154, v16 row_newbcast:0 row_mask:0xf bank_mask:0xf\n"
"v_mul_f32_dpp v17, v154, v17 row_newbcast:1 row_mask:0xf bank_mask:0xf\n"
"v_mul_f32_dpp v18, v154, v18 row_newbcast:2 row_mask:0xf bank_mask:0xf\n"
"v_mul_f32_dpp v19, v154, v19 row_newbcast:3 row_mask:0xf bank_mask:0xf\n"
"v_mul_f32_dpp v20, v154, v20 row_newbcast:0 row_mask:0xf bank_mask:0xf\n"
"v_mul_f32_dpp v21, v154, v21 row_newbcast:1 row_mask:0xf bank_mask:0xf\n"
"v_mul_f32_dpp v22, v154, v22 row_newbcast:2 row_mask:0xf bank_mask:0xf\n"
"v_mul_f32_dpp v23, v154, v23 row_newbcast:3 row_mask:0xf bank_mask:0xf\n"
"v_mul_f32_dpp v24, v154, v24 row_newbcast:0 row_mask:0xf bank_mask:0xf\n"
"v_mul_f32_dpp v25, v154, v25 row_newbcast:1 row_mask:0xf bank_mask:0xf\n"
"v_mul_f32_dpp v26, v154, v26 row_newbcast:2 row_mask:0xf bank_mask:0xf\n"
"v_mul_f32_dpp v27, v154, v27 row_newbcast:3 row_mask:0xf bank_mask:0xf\n"
"v_mul_f32_dpp v28, v154, v28 row_newbcast:0 row_mask:0xf bank_mask:0xf\n"
"v_mul_f32_dpp v29, v154, v29 row_newbcast:1 row_mask:0xf bank_mask:0xf\n"
"v_mul_f32_dpp v30, v154, v30 row_newbcast:2 row_mask:0xf bank_mask:0xf\n"
"v_mul_f32_dpp v31, v154, v31 row_newbcast:3 row_mask:0xf bank_mask:0xf\n"
"v_max3_f32 v157, v104, v105, v106\n"
"v_max3_f32 v157, v157, v107, v108\n"
"v_max3_f32 v157, v157, v109, v110\n"
"v_max3_f32 v157, v157, v111, v156\n"
"v_mov_b32 v158, v157\n"
"s_nop 1\n"
"v_permlane32_swap_b32 v157, v158\n"
"v_max_f32 v157, v157, v158\n"
"v_mov_b32 v158, v157\n"
"s_nop 1\n"
"v_permlane16_swap_b32 v157, v158\n"
"v_max_f32 v157, v157, v158\n"
"v_sub_f32 v158, v156, v157\n"
"v_exp_f32 v158, v158\n"
"v_mov_b32 v156, v157\n"
"v_sub_f32 v104, v104, v156\n"
"v_sub_f32 v105, v105, v156\n"
"v_sub_f32 v106, v106, v156\n"
"v_sub_f32 v107, v107, v156\n"
"v_sub_f32 v108, v108, v156\n"
"v_sub_f32 v109, v109, v156\n"
"v_sub_f32 v110, v110, v156\n"
"v_sub_f32 v111, v111, v156\n"
"v_exp_f32 v104, v104\n"
"v_exp_f32 v105, v105\n"
"v_exp_f32 v106, v106\n"
"v_exp_f32 v107, v107\n"
"v_exp_f32 v108, v108\n"
"v_exp_f32 v109, v109\n"
"v_exp_f32 v110, v110\n"
"v_exp_f32 v111, v111\n"
"v_mul_f32 v159, v159, v158\n"
"v_add_f32 v159, v159, v104\n"
"v_add_f32 v159, v159, v105\n"
"v_add_f32 v159, v159, v106\n"
"v_add_f32 v159, v159, v107\n"
"v_add_f32 v159, v159, v108\n"
"v_add_f32 v159, v159, v109\n"
"v_add_f32 v159, v159, v110\n"
"v_add_f32 v159, v159, v111\n"
"v_cvt_pk_bf16_f32 v104, v104, v105\n"
"v_cvt_pk_bf16_f32 v105, v106, v107\n"
"v_cvt_pk_bf16_f32 v106, v108, v109\n"
"v_cvt_pk_bf16_f32 v107, v110, v111\n"
"v_mov_b32_dpp v158, v158 row_ror:12 row_mask:0x2 bank_mask:0xf\n"
"s_nop 1\n"
"v_mov_b32_dpp v158, v158 row_ror:8 row_mask:0x4 bank_mask:0xf\n"
"s_nop 1\n"
"v_mov_b32_dpp v158, v158 row_ror:4 row_mask:0x8 bank_mask:0xf\n"
"s_nop 1\n"
"v_mul_f32_dpp v32, v158, v32 row_newbcast:0 row_mask:0xf bank_mask:0xf\n"
"v_mul_f32_dpp v33, v158, v33 row_newbcast:1 row_mask:0xf bank_mask:0xf\n"
"v_mul_f32_dpp v34, v158, v34 row_newbcast:2 row_mask:0xf bank_mask:0xf\n"
"v_mul_f32_dpp v35, v158, v35 row_newbcast:3 row_mask:0xf bank_mask:0xf\n"
"v_mul_f32_dpp v36, v158, v36 row_newbcast:0 row_mask:0xf bank_mask:0xf\n"
"v_mul_f32_dpp v37, v158, v37 row_newbcast:1 row_mask:0xf bank_mask:0xf\n"
"v_mul_f32_dpp v38, v158, v38 row_newbcast:2 row_mask:0xf bank_mask:0xf\n"
"v_mul_f32_dpp v39, v158, v39 row_newbcast:3 row_mask:0xf bank_mask:0xf\n"
"v_mul_f32_dpp v40, v158, v40 row_newbcast:0 row_mask:0xf bank_mask:0xf\n"
"v_mul_f32_dpp v41, v158, v41 row_newbcast:1 row_mask:0xf bank_mask:0xf\n"
"v_mul_f32_dpp v42, v158, v42 row_newbcast:2 row_mask:0xf bank_mask:0xf\n"
"v_mul_f32_dpp v43, v158, v43 row_newbcast:3 row_mask:0xf bank_mask:0xf\n"
"v_mul_f32_dpp v44, v158, v44 row_newbcast:0 row_mask:0xf bank_mask:0xf\n"
"v_mul_f32_dpp v45, v158, v45 row_newbcast:1 row_mask:0xf bank_mask:0xf\n"
"v_mul_f32_dpp v46, v158, v46 row_newbcast:2 row_mask:0xf bank_mask:0xf\n"
"v_mul_f32_dpp v47, v158, v47 row_newbcast:3 row_mask:0xf bank_mask:0xf\n"
"v_mul_f32_dpp v48, v158, v48 row_newbcast:0 row_mask:0xf bank_mask:0xf\n"
"v_mul_f32_dpp v49, v158, v49 row_newbcast:1 row_mask:0xf bank_mask:0xf\n"
"v_mul_f32_dpp v50, v158, v50 row_newbcast:2 row_mask:0xf bank_mask:0xf\n"
"v_mul_f32_dpp v51, v158, v51 row_newbcast:3 row_mask:0xf bank_mask:0xf\n"
"v_mul_f32_dpp v52, v158, v52 row_newbcast:0 row_mask:0xf bank_mask:0xf\n"
"v_mul_f32_dpp v53, v158, v53 row_newbcast:1 row_mask:0xf bank_mask:0xf\n"
"v_mul_f32_dpp v54, v158, v54 row_newbcast:2 row_mask:0xf bank_mask:0xf\n"
"v_mul_f32_dpp v55, v158, v55 row_newbcast:3 row_mask:0xf bank_mask:0xf\n"
"v_mul_f32_dpp v56, v158, v56 row_newbcast:0 row_mask:0xf bank_mask:0xf\n"
"v_mul_f32_dpp v57, v158, v57 row_newbcast:1 row_mask:0xf bank_mask:0xf\n"
"v_mul_f32_dpp v58, v158, v58 row_newbcast:2 row_mask:0xf bank_mask:0xf\n"
"v_mul_f32_dpp v59, v158, v59 row_newbcast:3 row_mask:0xf bank_mask:0xf\n"
"v_mul_f32_dpp v60, v158, v60 row_newbcast:0 row_mask:0xf bank_mask:0xf\n"
"v_mul_f32_dpp v61, v158, v61 row_newbcast:1 row_mask:0xf bank_mask:0xf\n"
"v_mul_f32_dpp v62, v158, v62 row_newbcast:2 row_mask:0xf bank_mask:0xf\n"
"v_mul_f32_dpp v63, v158, v63 row_newbcast:3 row_mask:0xf bank_mask:0xf\n"
"v_max3_f32 v161, v112, v113, v114\n"
"v_max3_f32 v161, v161, v115, v116\n"
"v_max3_f32 v161, v161, v117, v118\n"
"v_max3_f32 v161, v161, v119, v160\n"
"v_mov_b32 v162, v161\n"
"s_nop 1\n"
"v_permlane32_swap_b32 v161, v162\n"
"v_max_f32 v161, v161, v162\n"
"v_mov_b32 v162, v161\n"
"s_nop 1\n"
"v_permlane16_swap_b32 v161, v162\n"
"v_max_f32 v161, v161, v162\n"
"v_sub_f32 v162, v160, v161\n"
"v_exp_f32 v162, v162\n"
"v_mov_b32 v160, v161\n"
"v_sub_f32 v112, v112, v160\n"
"v_sub_f32 v113, v113, v160\n"
"v_sub_f32 v114, v114, v160\n"
"v_sub_f32 v115, v115, v160\n"
"v_sub_f32 v116, v116, v160\n"
"v_sub_f32 v117, v117, v160\n"
"v_sub_f32 v118, v118, v160\n"
"v_sub_f32 v119, v119, v160\n"
"v_exp_f32 v112, v112\n"
"v_exp_f32 v113, v113\n"
"v_exp_f32 v114, v114\n"
"v_exp_f32 v115, v115\n"
"v_exp_f32 v116, v116\n"
"v_exp_f32 v117, v117\n"
"v_exp_f32 v118, v118\n"
"v_exp_f32 v119, v119\n"
"v_mul_f32 v163, v163, v162\n"
"v_add_f32 v163, v163, v112\n"
"v_add_f32 v163, v163, v113\n"
"v_add_f32 v163, v163, v114\n"
"v_add_f32 v163, v163, v115\n"
"v_add_f32 v163, v163, v116\n"
"v_add_f32 v163, v163, v117\n"
"v_add_f32 v163, v163, v118\n"
"v_add_f32 v163, v163, v119\n"
"v_cvt_pk_bf16_f32 v112, v112, v113\n"
"v_cvt_pk_bf16_f32 v113, v114, v115\n"
"v_cvt_pk_bf16_f32 v114, v116, v117\n"
"v_cvt_pk_bf16_f32 v115, v118, v119\n"
"v_mov_b32_dpp v162, v162 row_ror:12 row_mask:0x2 bank_mask:0xf\n"
"s_nop 1\n"
"v_mov_b32_dpp v162, v162 row_ror:8 row_mask:0x4 bank_mask:0xf\n"
"s_nop 1\n"
"v_mov_b32_dpp v162, v162 row_ror:4 row_mask:0x8 bank_mask:0xf\n"
"s_nop 1\n"
"v_mul_f32_dpp v64, v162, v64 row_newbcast:0 row_mask:0xf bank_mask:0xf\n"
"v_mul_f32_dpp v65, v162, v65 row_newbcast:1 row_mask:0xf bank_mask:0xf\n"
"v_mul_f32_dpp v66, v162, v66 row_newbcast:2 row_mask:0xf bank_mask:0xf\n"
"v_mul_f32_dpp v67, v162, v67 row_newbcast:3 row_mask:0xf bank_mask:0xf\n"
"v_mul_f32_dpp v68, v162, v68 row_newbcast:0 row_mask:0xf bank_mask:0xf\n"
"v_mul_f32_dpp v69, v162, v69 row_newbcast:1 row_mask:0xf bank_mask:0xf\n"
"v_mul_f32_dpp v70, v162, v70 row_newbcast:2 row_mask:0xf bank_mask:0xf\n"
"v_mul_f32_dpp v71, v162, v71 row_newbcast:3 row_mask:0xf bank_mask:0xf\n"
"v_mul_f32_dpp v72, v162, v72 row_newbcast:0 row_mask:0xf bank_mask:0xf\n"
"v_mul_f32_dpp v73, v162, v73 row_newbcast:1 row_mask:0xf bank_mask:0xf\n"
"v_mul_f32_dpp v74, v162, v74 row_newbcast:2 row_mask:0xf bank_mask:0xf\n"
"v_mul_f32_dpp v75, v162, v75 row_newbcast:3 row_mask:0xf bank_mask:0xf\n"
"v_mul_f32_dpp v76, v162, v76 row_newbcast:0 row_mask:0xf bank_mask:0xf\n"
"v_mul_f32_dpp v77, v162, v77 row_newbcast:1 row_mask:0xf bank_mask:0xf\n"
"v_mul_f32_dpp v78, v162, v78 row_newbcast:2 row_mask:0xf bank_mask:0xf\n"
"v_mul_f32_dpp v79, v162, v79 row_newbcast:3 row_mask:0xf bank_mask:0xf\n"
"v_mul_f32_dpp v80, v162, v80 row_newbcast:0 row_mask:0xf bank_mask:0xf\n"
"v_mul_f32_dpp v81, v162, v81 row_newbcast:1 row_mask:0xf bank_mask:0xf\n"
"v_mul_f32_dpp v82, v162, v82 row_newbcast:2 row_mask:0xf bank_mask:0xf\n"
"v_mul_f32_dpp v83, v162, v83 row_newbcast:3 row_mask:0xf bank_mask:0xf\n"
"v_mul_f32_dpp v84, v162, v84 row_newbcast:0 row_mask:0xf bank_mask:0xf\n"
"v_mul_f32_dpp v85, v162, v85 row_newbcast:1 row_mask:0xf bank_mask:0xf\n"
"v_mul_f32_dpp v86, v162, v86 row_newbcast:2 row_mask:0xf bank_mask:0xf\n"
"v_mul_f32_dpp v87, v162, v87 row_newbcast:3 row_mask:0xf bank_mask:0xf\n"
"v_mul_f32_dpp v88, v162, v88 row_newbcast:0 row_mask:0xf bank_mask:0xf\n"
"v_mul_f32_dpp v89, v162, v89 row_newbcast:1 row_mask:0xf bank_mask:0xf\n"
"v_mul_f32_dpp v90, v162, v90 row_newbcast:2 row_mask:0xf bank_mask:0xf\n"
"v_mul_f32_dpp v91, v162, v91 row_newbcast:3 row_mask:0xf bank_mask:0xf\n"
"v_mul_f32_dpp v92, v162, v92 row_newbcast:0 row_mask:0xf bank_mask:0xf\n"
"v_mul_f32_dpp v93, v162, v93 row_newbcast:1 row_mask:0xf bank_mask:0xf\n"
"v_mul_f32_dpp v94, v162, v94 row_newbcast:2 row_mask:0xf bank_mask:0xf\n"
"v_mul_f32_dpp v95, v162, v95 row_newbcast:3 row_mask:0xf bank_mask:0xf\n"
"s_cmp_lg_u32 s83, s43\n"
"s_cbranch_scc1 LSK3A_%=\n"
"v_max3_f32 v165, v120, v121, v122\n"
"v_max3_f32 v165, v165, v123, v124\n"
"v_max3_f32 v165, v165, v125, v126\n"
"v_max3_f32 v165, v165, v127, v164\n"
"v_mov_b32 v166, v165\n"
"s_nop 1\n"
"v_permlane32_swap_b32 v165, v166\n"
"v_max_f32 v165, v165, v166\n"
"v_mov_b32 v166, v165\n"
"s_nop 1\n"
"v_permlane16_swap_b32 v165, v166\n"
"v_max_f32 v165, v165, v166\n"
"v_sub_f32 v166, v164, v165\n"
"v_exp_f32 v166, v166\n"
"v_mov_b32 v164, v165\n"
"v_sub_f32 v120, v120, v164\n"
"v_sub_f32 v121, v121, v164\n"
"v_sub_f32 v122, v122, v164\n"
"v_sub_f32 v123, v123, v164\n"
"v_sub_f32 v124, v124, v164\n"
"v_sub_f32 v125, v125, v164\n"
"v_sub_f32 v126, v126, v164\n"
"v_sub_f32 v127, v127, v164\n"
"v_exp_f32 v120, v120\n"
"v_exp_f32 v121, v121\n"
"v_exp_f32 v122, v122\n"
"v_exp_f32 v123, v123\n"
"v_exp_f32 v124, v124\n"
"v_exp_f32 v125, v125\n"
"v_exp_f32 v126, v126\n"
"v_exp_f32 v127, v127\n"
"v_mul_f32 v167, v167, v166\n"
"v_add_f32 v167, v167, v120\n"
"v_add_f32 v167, v167, v121\n"
"v_add_f32 v167, v167, v122\n"
"v_add_f32 v167, v167, v123\n"
"v_add_f32 v167, v167, v124\n"
"v_add_f32 v167, v167, v125\n"
"v_add_f32 v167, v167, v126\n"
"v_add_f32 v167, v167, v127\n"
"v_cvt_pk_bf16_f32 v120, v120, v121\n"
"v_cvt_pk_bf16_f32 v121, v122, v123\n"
"v_cvt_pk_bf16_f32 v122, v124, v125\n"
"v_cvt_pk_bf16_f32 v123, v126, v127\n"
"v_mov_b32_dpp v166, v166 row_ror:12 row_mask:0x2 bank_mask:0xf\n"
"s_nop 1\n"
"v_mov_b32_dpp v166, v166 row_ror:8 row_mask:0x4 bank_mask:0xf\n"
"s_nop 1\n"
"v_mov_b32_dpp v166, v166 row_ror:4 row_mask:0x8 bank_mask:0xf\n"
"v_accvgpr_read_b32 v124, a208\n"
"s_nop 1\n"
"v_mul_f32_dpp v124, v166, v124 row_newbcast:0 row_mask:0xf bank_mask:0xf\n"
"v_accvgpr_write_b32 a208, v124\n"
"v_accvgpr_read_b32 v125, a209\n"
"s_nop 1\n"
"v_mul_f32_dpp v125, v166, v125 row_newbcast:1 row_mask:0xf bank_mask:0xf\n"
"v_accvgpr_write_b32 a209, v125\n"
"v_accvgpr_read_b32 v126, a210\n"
"s_nop 1\n"
"v_mul_f32_dpp v126, v166, v126 row_newbcast:2 row_mask:0xf bank_mask:0xf\n"
"v_accvgpr_write_b32 a210, v126\n"
"v_accvgpr_read_b32 v127, a211\n"
"s_nop 1\n"
"v_mul_f32_dpp v127, v166, v127 row_newbcast:3 row_mask:0xf bank_mask:0xf\n"
"v_accvgpr_write_b32 a211, v127\n"
"v_accvgpr_read_b32 v124, a212\n"
"s_nop 1\n"
"v_mul_f32_dpp v124, v166, v124 row_newbcast:0 row_mask:0xf bank_mask:0xf\n"
"v_accvgpr_write_b32 a212, v124\n"
"v_accvgpr_read_b32 v125, a213\n"
"s_nop 1\n"
"v_mul_f32_dpp v125, v166, v125 row_newbcast:1 row_mask:0xf bank_mask:0xf\n"
"v_accvgpr_write_b32 a213, v125\n"
"v_accvgpr_read_b32 v126, a214\n"
"s_nop 1\n"
"v_mul_f32_dpp v126, v166, v126 row_newbcast:2 row_mask:0xf bank_mask:0xf\n"
"v_accvgpr_write_b32 a214, v126\n"
"v_accvgpr_read_b32 v127, a215\n"
"s_nop 1\n"
"v_mul_f32_dpp v127, v166, v127 row_newbcast:3 row_mask:0xf bank_mask:0xf\n"
"v_accvgpr_write_b32 a215, v127\n"
"v_accvgpr_read_b32 v124, a216\n"
"s_nop 1\n"
"v_mul_f32_dpp v124, v166, v124 row_newbcast:0 row_mask:0xf bank_mask:0xf\n"
"v_accvgpr_write_b32 a216, v124\n"
"v_accvgpr_read_b32 v125, a217\n"
"s_nop 1\n"
"v_mul_f32_dpp v125, v166, v125 row_newbcast:1 row_mask:0xf bank_mask:0xf\n"
"v_accvgpr_write_b32 a217, v125\n"
"v_accvgpr_read_b32 v126, a218\n"
"s_nop 1\n"
"v_mul_f32_dpp v126, v166, v126 row_newbcast:2 row_mask:0xf bank_mask:0xf\n"
"v_accvgpr_write_b32 a218, v126\n"
"v_accvgpr_read_b32 v127, a219\n"
"s_nop 1\n"
"v_mul_f32_dpp v127, v166, v127 row_newbcast:3 row_mask:0xf bank_mask:0xf\n"
"v_accvgpr_write_b32 a219, v127\n"
"v_accvgpr_read_b32 v124, a220\n"
"s_nop 1\n"
"v_mul_f32_dpp v124, v166, v124 row_newbcast:0 row_mask:0xf bank_mask:0xf\n"
"v_accvgpr_write_b32 a220, v124\n"
"v_accvgpr_read_b32 v125, a221\n"
"s_nop 1\n"
"v_mul_f32_dpp v125, v166, v125 row_newbcast:1 row_mask:0xf bank_mask:0xf\n"
"v_accvgpr_write_b32 a221, v125\n"
"v_accvgpr_read_b32 v126, a222\n"
"s_nop 1\n"
"v_mul_f32_dpp v126, v166, v126 row_newbcast:2 row_mask:0xf bank_mask:0xf\n"
"v_accvgpr_write_b32 a222, v126\n"
"v_accvgpr_read_b32 v127, a223\n"
"s_nop 1\n"
"v_mul_f32_dpp v127, v166, v127 row_newbcast:3 row_mask:0xf bank_mask:0xf\n"
"v_accvgpr_write_b32 a223, v127\n"
"v_accvgpr_read_b32 v124, a224\n"
"s_nop 1\n"
"v_mul_f32_dpp v124, v166, v124 row_newbcast:0 row_mask:0xf bank_mask:0xf\n"
"v_accvgpr_write_b32 a224, v124\n"
"v_accvgpr_read_b32 v125, a225\n"
"s_nop 1\n"
"v_mul_f32_dpp v125, v166, v125 row_newbcast:1 row_mask:0xf bank_mask:0xf\n"
"v_accvgpr_write_b32 a225, v125\n"
"v_accvgpr_read_b32 v126, a226\n"
"s_nop 1\n"
"v_mul_f32_dpp v126, v166, v126 row_newbcast:2 row_mask:0xf bank_mask:0xf\n"
"v_accvgpr_write_b32 a226, v126\n"
"v_accvgpr_read_b32 v127, a227\n"
"s_nop 1\n"
"v_mul_f32_dpp v127, v166, v127 row_newbcast:3 row_mask:0xf bank_mask:0xf\n"
"v_accvgpr_write_b32 a227, v127\n"
"v_accvgpr_read_b32 v124, a228\n"
"s_nop 1\n"
"v_mul_f32_dpp v124, v166, v124 row_newbcast:0 row_mask:0xf bank_mask:0xf\n"
"v_accvgpr_write_b32 a228, v124\n"
"v_accvgpr_read_b32 v125, a229\n"
"s_nop 1\n"
"v_mul_f32_dpp v125, v166, v125 row_newbcast:1 row_mask:0xf bank_mask:0xf\n"
"v_accvgpr_write_b32 a229, v125\n"
"v_accvgpr_read_b32 v126, a230\n"
"s_nop 1\n"
"v_mul_f32_dpp v126, v166, v126 row_newbcast:2 row_mask:0xf bank_mask:0xf\n"
"v_accvgpr_write_b32 a230, v126\n"
"v_accvgpr_read_b32 v127, a231\n"
"s_nop 1\n"
"v_mul_f32_dpp v127, v166, v127 row_newbcast:3 row_mask:0xf bank_mask:0xf\n"
"v_accvgpr_write_b32 a231, v127\n"
"v_accvgpr_read_b32 v124, a232\n"
"s_nop 1\n"
"v_mul_f32_dpp v124, v166, v124 row_newbcast:0 row_mask:0xf bank_mask:0xf\n"
"v_accvgpr_write_b32 a232, v124\n"
"v_accvgpr_read_b32 v125, a233\n"
"s_nop 1\n"
"v_mul_f32_dpp v125, v166, v125 row_newbcast:1 row_mask:0xf bank_mask:0xf\n"
"v_accvgpr_write_b32 a233, v125\n"
"v_accvgpr_read_b32 v126, a234\n"
"s_nop 1\n"
"v_mul_f32_dpp v126, v166, v126 row_newbcast:2 row_mask:0xf bank_mask:0xf\n"
"v_accvgpr_write_b32 a234, v126\n"
"v_accvgpr_read_b32 v127, a235\n"
"s_nop 1\n"
"v_mul_f32_dpp v127, v166, v127 row_newbcast:3 row_mask:0xf bank_mask:0xf\n"
"v_accvgpr_write_b32 a235, v127\n"
"v_accvgpr_read_b32 v124, a236\n"
"s_nop 1\n"
"v_mul_f32_dpp v124, v166, v124 row_newbcast:0 row_mask:0xf bank_mask:0xf\n"
"v_accvgpr_write_b32 a236, v124\n"
"v_accvgpr_read_b32 v125, a237\n"
"s_nop 1\n"
"v_mul_f32_dpp v125, v166, v125 row_newbcast:1 row_mask:0xf bank_mask:0xf\n"
"v_accvgpr_write_b32 a237, v125\n"
"v_accvgpr_read_b32 v126, a238\n"
"s_nop 1\n"
"v_mul_f32_dpp v126, v166, v126 row_newbcast:2 row_mask:0xf bank_mask:0xf\n"
"v_accvgpr_write_b32 a238, v126\n"
"v_accvgpr_read_b32 v127, a239\n"
"s_nop 1\n"
"v_mul_f32_dpp v127, v166, v127 row_newbcast:3 row_mask:0xf bank_mask:0xf\n"
"v_accvgpr_write_b32 a239, v127\n"
"LSK3A_%=:\n"
"s_nop 3\n"
"buffer_load_ushort v128, v146, s[88:91], s80 offen offset:0\n"
"buffer_load_ushort v129, v146, s[88:91], s80 offen offset:256\n"
"buffer_load_ushort v130, v146, s[88:91], s80 offen offset:512\n"
"buffer_load_ushort v131, v146, s[88:91], s80 offen offset:768\n"
"buffer_load_ushort v132, v146, s[88:91], s81 offen offset:0\n"
"buffer_load_ushort v133, v146, s[88:91], s81 offen offset:256\n"
"buffer_load_ushort v134, v146, s[88:91], s81 offen offset:512\n"
"buffer_load_ushort v135, v146, s[88:91], s81 offen offset:768\n"
"buffer_load_ushort v136, v146, s[88:91], s80 offen offset:32\n"
"buffer_load_ushort v137, v146, s[88:91], s80 offen offset:288\n"
"buffer_load_ushort v138, v146, s[88:91], s80 offen offset:544\n"
"buffer_load_ushort v139, v146, s[88:91], s80 offen offset:800\n"
"buffer_load_ushort v140, v146, s[88:91], s81 offen offset:32\n"
"buffer_load_ushort v141, v146, s[88:91], s81 offen offset:288\n"
"buffer_load_ushort v142, v146, s[88:91], s81 offen offset:544\n"
"buffer_load_ushort v143, v146, s[88:91], s81 offen offset:800\n"
"s_waitcnt vmcnt(0)\n"
"v_lshl_or_b32 v128, v129, 16, v128\n"
"v_lshl_or_b32 v129, v131, 16, v130\n"
"v_lshl_or_b32 v130, v133, 16, v132\n"
"v_lshl_or_b32 v131, v135, 16, v134\n"
"v_lshl_or_b32 v136, v137, 16, v136\n"
"v_lshl_or_b32 v137, v139, 16, v138\n"
"v_lshl_or_b32 v138, v141, 16, v140\n"
"v_lshl_or_b32 v139, v143, 16, v142\n"
"v_mfma_f32_16x16x32_bf16 v[0:3], v[96:99], v[128:131], v[0:3]\n"
"s_nop 0\n"
"v_mfma_f32_16x16x32_bf16 v[4:7], v[96:99], v[136:139], v[4:7]\n"
"v_mfma_f32_16x16x32_bf16 v[32:35], v[104:107], v[128:131], v[32:35]\n"
"v_mfma_f32_16x16x32_bf16 v[36:39], v[104:107], v[136:139], v[36:39]\n"
"v_mfma_f32_16x16x32_bf16 v[64:67], v[112:115], v[128:131], v[64:67]\n"
"v_mfma_f32_16x16x32_bf16 v[68:71], v[112:115], v[136:139], v[68:71]\n"
"s_cmp_lg_u32 s83, s43\n"
"s_cbranch_scc1 LSK3B0_%=\n"
"v_mfma_f32_16x16x32_bf16 a[208:211], v[120:123], v[128:131], a[208:211]\n"
"v_mfma_f32_16x16x32_bf16 a[212:215], v[120:123], v[136:139], a[212:215]\n"
"LSK3B0_%=:\n"
"s_nop 3\n"
"buffer_load_ushort v128, v147, s[88:91], s80 offen offset:0\n"
"buffer_load_ushort v129, v147, s[88:91], s80 offen offset:256\n"
"buffer_load_ushort v130, v147, s[88:91], s80 offen offset:512\n"
"buffer_load_ushort v131, v147, s[88:91], s80 offen offset:768\n"
"buffer_load_ushort v132, v147, s[88:91], s81 offen offset:0\n"
"buffer_load_ushort v133, v147, s[88:91], s81 offen offset:256\n"
"buffer_load_ushort v134, v147, s[88:91], s81 offen offset:512\n"
"buffer_load_ushort v135, v147, s[88:91], s81 offen offset:768\n"
"buffer_load_ushort v136, v147, s[88:91], s80 offen offset:32\n"
"buffer_load_ushort v137, v147, s[88:91], s80 offen offset:288\n"
"buffer_load_ushort v138, v147, s[88:91], s80 offen offset:544\n"
"buffer_load_ushort v139, v147, s[88:91], s80 offen offset:800\n"
"buffer_load_ushort v140, v147, s[88:91], s81 offen offset:32\n"
"buffer_load_ushort v141, v147, s[88:91], s81 offen offset:288\n"
"buffer_load_ushort v142, v147, s[88:91], s81 offen offset:544\n"
"buffer_load_ushort v143, v147, s[88:91], s81 offen offset:800\n"
"s_waitcnt vmcnt(0)\n"
"v_lshl_or_b32 v128, v129, 16, v128\n"
"v_lshl_or_b32 v129, v131, 16, v130\n"
"v_lshl_or_b32 v130, v133, 16, v132\n"
"v_lshl_or_b32 v131, v135, 16, v134\n"
"v_lshl_or_b32 v136, v137, 16, v136\n"
"v_lshl_or_b32 v137, v139, 16, v138\n"
"v_lshl_or_b32 v138, v141, 16, v140\n"
"v_lshl_or_b32 v139, v143, 16, v142\n"
"v_mfma_f32_16x16x32_bf16 v[8:11], v[96:99], v[128:131], v[8:11]\n"
"s_nop 0\n"
"v_mfma_f32_16x16x32_bf16 v[12:15], v[96:99], v[136:139], v[12:15]\n"
"v_mfma_f32_16x16x32_bf16 v[40:43], v[104:107], v[128:131], v[40:43]\n"
"v_mfma_f32_16x16x32_bf16 v[44:47], v[104:107], v[136:139], v[44:47]\n"
"v_mfma_f32_16x16x32_bf16 v[72:75], v[112:115], v[128:131], v[72:75]\n"
"v_mfma_f32_16x16x32_bf16 v[76:79], v[112:115], v[136:139], v[76:79]\n"
"s_cmp_lg_u32 s83, s43\n"
"s_cbranch_scc1 LSK3B1_%=\n"
"v_mfma_f32_16x16x32_bf16 a[216:219], v[120:123], v[128:131], a[216:219]\n"
"v_mfma_f32_16x16x32_bf16 a[220:223], v[120:123], v[136:139], a[220:223]\n"
"LSK3B1_%=:\n"
"s_nop 3\n"
"buffer_load_ushort v128, v146, s[88:91], s80 offen offset:128\n"
"buffer_load_ushort v129, v146, s[88:91], s80 offen offset:384\n"
"buffer_load_ushort v130, v146, s[88:91], s80 offen offset:640\n"
"buffer_load_ushort v131, v146, s[88:91], s80 offen offset:896\n"
"buffer_load_ushort v132, v146, s[88:91], s81 offen offset:128\n"
"buffer_load_ushort v133, v146, s[88:91], s81 offen offset:384\n"
"buffer_load_ushort v134, v146, s[88:91], s81 offen offset:640\n"
"buffer_load_ushort v135, v146, s[88:91], s81 offen offset:896\n"
"buffer_load_ushort v136, v146, s[88:91], s80 offen offset:160\n"
"buffer_load_ushort v137, v146, s[88:91], s80 offen offset:416\n"
"buffer_load_ushort v138, v146, s[88:91], s80 offen offset:672\n"
"buffer_load_ushort v139, v146, s[88:91], s80 offen offset:928\n"
"buffer_load_ushort v140, v146, s[88:91], s81 offen offset:160\n"
"buffer_load_ushort v141, v146, s[88:91], s81 offen offset:416\n"
"buffer_load_ushort v142, v146, s[88:91], s81 offen offset:672\n"
"buffer_load_ushort v143, v146, s[88:91], s81 offen offset:928\n"
"s_waitcnt vmcnt(0)\n"
"v_lshl_or_b32 v128, v129, 16, v128\n"
"v_lshl_or_b32 v129, v131, 16, v130\n"
"v_lshl_or_b32 v130, v133, 16, v132\n"
"v_lshl_or_b32 v131, v135, 16, v134\n"
"v_lshl_or_b32 v136, v137, 16, v136\n"
"v_lshl_or_b32 v137, v139, 16, v138\n"
"v_lshl_or_b32 v138, v141, 16, v140\n"
"v_lshl_or_b32 v139, v143, 16, v142\n"
"v_mfma_f32_16x16x32_bf16 v[16:19], v[96:99], v[128:131], v[16:19]\n"
"s_nop 0\n"
"v_mfma_f32_16x16x32_bf16 v[20:23], v[96:99], v[136:139], v[20:23]\n"
"v_mfma_f32_16x16x32_bf16 v[48:51], v[104:107], v[128:131], v[48:51]\n"
"v_mfma_f32_16x16x32_bf16 v[52:55], v[104:107], v[136:139], v[52:55]\n"
"v_mfma_f32_16x16x32_bf16 v[80:83], v[112:115], v[128:131], v[80:83]\n"
"v_mfma_f32_16x16x32_bf16 v[84:87], v[112:115], v[136:139], v[84:87]\n"
"s_cmp_lg_u32 s83, s43\n"
"s_cbranch_scc1 LSK3B2_%=\n"
"v_mfma_f32_16x16x32_bf16 a[224:227], v[120:123], v[128:131], a[224:227]\n"
"v_mfma_f32_16x16x32_bf16 a[228:231], v[120:123], v[136:139], a[228:231]\n"
"LSK3B2_%=:\n"
"s_nop 3\n"
"buffer_load_ushort v128, v147, s[88:91], s80 offen offset:128\n"
"buffer_load_ushort v129, v147, s[88:91], s80 offen offset:384\n"
"buffer_load_ushort v130, v147, s[88:91], s80 offen offset:640\n"
"buffer_load_ushort v131, v147, s[88:91], s80 offen offset:896\n"
"buffer_load_ushort v132, v147, s[88:91], s81 offen offset:128\n"
"buffer_load_ushort v133, v147, s[88:91], s81 offen offset:384\n"
"buffer_load_ushort v134, v147, s[88:91], s81 offen offset:640\n"
"buffer_load_ushort v135, v147, s[88:91], s81 offen offset:896\n"
"buffer_load_ushort v136, v147, s[88:91], s80 offen offset:160\n"
"buffer_load_ushort v137, v147, s[88:91], s80 offen offset:416\n"
"buffer_load_ushort v138, v147, s[88:91], s80 offen offset:672\n"
"buffer_load_ushort v139, v147, s[88:91], s80 offen offset:928\n"
"buffer_load_ushort v140, v147, s[88:91], s81 offen offset:160\n"
"buffer_load_ushort v141, v147, s[88:91], s81 offen offset:416\n"
"buffer_load_ushort v142, v147, s[88:91], s81 offen offset:672\n"
"buffer_load_ushort v143, v147, s[88:91], s81 offen offset:928\n"
"s_waitcnt vmcnt(0)\n"
"v_lshl_or_b32 v128, v129, 16, v128\n"
"v_lshl_or_b32 v129, v131, 16, v130\n"
"v_lshl_or_b32 v130, v133, 16, v132\n"
"v_lshl_or_b32 v131, v135, 16, v134\n"
"v_lshl_or_b32 v136, v137, 16, v136\n"
"v_lshl_or_b32 v137, v139, 16, v138\n"
"v_lshl_or_b32 v138, v141, 16, v140\n"
"v_lshl_or_b32 v139, v143, 16, v142\n"
"v_mfma_f32_16x16x32_bf16 v[24:27], v[96:99], v[128:131], v[24:27]\n"
"s_nop 0\n"
"v_mfma_f32_16x16x32_bf16 v[28:31], v[96:99], v[136:139], v[28:31]\n"
"v_mfma_f32_16x16x32_bf16 v[56:59], v[104:107], v[128:131], v[56:59]\n"
"v_mfma_f32_16x16x32_bf16 v[60:63], v[104:107], v[136:139], v[60:63]\n"
"v_mfma_f32_16x16x32_bf16 v[88:91], v[112:115], v[128:131], v[88:91]\n"
"v_mfma_f32_16x16x32_bf16 v[92:95], v[112:115], v[136:139], v[92:95]\n"
"s_cmp_lg_u32 s83, s43\n"
"s_cbranch_scc1 LSK3B3_%=\n"
"v_mfma_f32_16x16x32_bf16 a[232:235], v[120:123], v[128:131], a[232:235]\n"
"v_mfma_f32_16x16x32_bf16 a[236:239], v[120:123], v[136:139], a[236:239]\n"
"LSK3B3_%=:\n"
"s_nop 3\n"
"s_nop 9\n"
"s_add_u32 s80, s80, 8192\n"
"s_add_u32 s81, s81, 8192\n"
"s_add_u32 s82, s82, 1\n"
"s_cmp_lt_u32 s82, 32\n"
"s_cbranch_scc1 LSLOOP_%=\n"
"v_mov_b32 v154, v155\n"
"s_nop 1\n"
"v_permlane32_swap_b32 v155, v154\n"
"v_add_f32 v155, v155, v154\n"
"v_mov_b32 v154, v155\n"
"s_nop 1\n"
"v_permlane16_swap_b32 v155, v154\n"
"v_add_f32 v155, v155, v154\n"
"v_mov_b32 v196, v155\n"
"v_mov_b32 v158, v159\n"
"s_nop 1\n"
"v_permlane32_swap_b32 v159, v158\n"
"v_add_f32 v159, v159, v158\n"
"v_mov_b32 v158, v159\n"
"s_nop 1\n"
"v_permlane16_swap_b32 v159, v158\n"
"v_add_f32 v159, v159, v158\n"
"v_mov_b32 v198, v159\n"
"v_mov_b32 v162, v163\n"
"s_nop 1\n"
"v_permlane32_swap_b32 v163, v162\n"
"v_add_f32 v163, v163, v162\n"
"v_mov_b32 v162, v163\n"
"s_nop 1\n"
"v_permlane16_swap_b32 v163, v162\n"
"v_add_f32 v163, v163, v162\n"
"v_mov_b32 v200, v163\n"
"v_mov_b32 v166, v167\n"
"s_nop 1\n"
"v_permlane32_swap_b32 v167, v166\n"
"v_add_f32 v167, v167, v166\n"
"v_mov_b32 v166, v167\n"
"s_nop 1\n"
"v_permlane16_swap_b32 v167, v166\n"
"v_add_f32 v167, v167, v166\n"
"v_mov_b32 v202, v167\n"
"v_sub_f32 v192, 0, v164\n"
"v_mov_b32 v96, 0\n"
"v_mov_b32 v97, 0\n"
"v_mov_b32 v98, 0\n"
"v_mov_b32 v99, 0\n"
"v_mov_b32 v100, 0\n"
"v_mov_b32 v101, 0\n"
"v_mov_b32 v102, 0\n"
"v_mov_b32 v103, 0\n"
"v_mov_b32 v104, 0\n"
"v_mov_b32 v105, 0\n"
"v_mov_b32 v106, 0\n"
"v_mov_b32 v107, 0\n"
"v_mov_b32 v108, 0\n"
"v_mov_b32 v109, 0\n"
"v_mov_b32 v110, 0\n"
"v_mov_b32 v111, 0\n"
"v_mov_b32 v112, 0\n"
"v_mov_b32 v113, 0\n"
"v_mov_b32 v114, 0\n"
"v_mov_b32 v115, 0\n"
"v_mov_b32 v116, 0\n"
"v_mov_b32 v117, 0\n"
"v_mov_b32 v118, 0\n"
"v_mov_b32 v119, 0\n"
"v_mov_b32 v120, 0\n"
"v_mov_b32 v121, 0\n"
"v_mov_b32 v122, 0\n"
"v_mov_b32 v123, 0\n"
"v_mov_b32 v204, 0\n"
"v_mov_b32 v205, 0\n"
"s_nop 3\n"
"s_mov_b32 s68, 0\n"
"s_cmp_eq_u32 s47, 4\n"
"s_cbranch_scc1 LBACK0_%=\n"
"s_cmp_eq_u32 s47, 8\n"
"s_cbranch_scc1 LBACK1_%=\n"
"s_cmp_eq_u32 s47, 12\n"
"s_cbranch_scc1 LBACK2_%=\n"
"s_branch LBACK3_%=\n"
"LFIN_%=:\n"
    :: [kalo]"s"((unsigned)ka), [kahi]"s"((unsigned)(ka >> 32)), [wg]"s"(blockIdx.x), [wid]"s"(wid)
    : "v0","v1","v2","v3","v4","v5","v6","v7","v8","v9","v10","v11","v12","v13","v14","v15","v16","v17","v18","v19","v20","v21","v22","v23","v24","v25","v26","v27","v28","v29","v30","v31","v32","v33","v34","v35","v36","v37","v38","v39","v40","v41","v42","v43","v44","v45","v46","v47","v48","v49","v50","v51","v52","v53","v54","v55","v56","v57","v58","v59","v60","v61","v62","v63","v64","v65","v66","v67","v68","v69","v70","v71","v72","v73","v74","v75","v76","v77","v78","v79","v80","v81","v82","v83","v84","v85","v86","v87","v88","v89","v90","v91","v92","v93","v94","v95","v96","v97","v98","v99","v100","v101","v102","v103","v104","v105","v106","v107","v108","v109","v110","v111","v112","v113","v114","v115","v116","v117","v118","v119","v120","v121","v122","v123","v124","v125","v126","v127","v128","v129","v130","v131","v132","v133","v134","v135","v136","v137","v138","v139","v140","v141","v142","v143","v144","v145","v146","v147","v148","v149","v150","v151","v152","v153","v154","v155","v156","v157","v158","v159","v160","v161","v162","v163","v164","v165","v166","v167","v168","v169","v170","v171","v172","v173","v174","v175","v176","v177","v178","v179","v180","v181","v182","v183","v184","v185","v186","v187","v188","v189","v190","v191","v192","v193","v194","v195","v196","v197","v198","v199","v200","v201","v202","v203","v204","v205","v206","v207","v208","v209","v210","v211","v212","v213","v214","v215","v216","v217","v218","v219","v220","v221","v222","v223","v224","v225","v226","v227","v228","v229","v230","v231","v232","v233","v234","v235","v236","v237","v238","v239","v240","v241","v242","v243","v244","v245","v246","v247","v248","v249","v250","v251","v252","v253","v254","v255","a0","a1","a2","a3","a4","a5","a6","a7","a8","a9","a10","a11","a12","a13","a14","a15","a16","a17","a18","a19","a20","a21","a22","a23","a24","a25","a26","a27","a28","a29","a30","a31","a32","a33","a34","a35","a36","a37","a38","a39","a40","a41","a42","a43","a44","a45","a46","a47","a48","a49","a50","a51","a52","a53","a54","a55","a56","a57","a58","a59","a60","a61","a62","a63","a64","a65","a66","a67","a68","a69","a70","a71","a72","a73","a74","a75","a76","a77","a78","a79","a80","a81","a82","a83","a84","a85","a86","a87","a88","a89","a90","a91","a92","a93","a94","a95","a96","a97","a98","a99","a100","a101","a102","a103","a104","a105","a106","a107","a108","a109","a110","a111","a112","a113","a114","a115","a116","a117","a118","a119","a120","a121","a122","a123","a124","a125","a126","a127","a128","a129","a130","a131","a132","a133","a134","a135","a136","a137","a138","a139","a140","a141","a142","a143","a144","a145","a146","a147","a148","a149","a150","a151","a152","a153","a154","a155","a156","a157","a158","a159","a160","a161","a162","a163","a164","a165","a166","a167","a168","a169","a170","a171","a172","a173","a174","a175","a176","a177","a178","a179","a180","a181","a182","a183","a184","a185","a186","a187","a188","a189","a190","a191","a192","a193","a194","a195","a196","a197","a198","a199","a200","a201","a202","a203","a204","a205","a206","a207","a208","a209","a210","a211","a212","a213","a214","a215","a216","a217","a218","a219","a220","a221","a222","a223","a224","a225","a226","a227","a228","a229","a230","a231","a232","a233","a234","a235","a236","a237","a238","a239","a240","a241","a242","a243","a244","a245","a246","a247","a248","a249","a250","a251","a252","a253","a254","a255","s6","s7","s8","s9","s10","s11","s12","s13","s14","s15","s16","s17","s18","s19","s20","s21","s22","s23","s24","s25","s26","s27","s28","s29","s30","s31","s32","s33","s34","s35","s36","s37","s38","s39","s40","s41","s42","s43","s44","s45","s46","s47","s48","s49","s50","s51","s52","s53","s54","s55","s56","s57","s58","s59","s60","s61","s62","s63","s64","s65","s66","s67","s68","s69","s70","s71","s72","s73","s74","s75","s76","s77","s78","s79","s80","s81","s82","s83","s84","s85","s86","s87","s88","s89","s90","s91","s92","s93","s94","s95","s96","s97","s98","s99","s100","vcc","scc","m0","memory");
}
extern "C" void kernel_launch(void* const* d_in, const int* in_sizes, int n_in, void* d_out, int out_size, void* d_ws, size_t ws_size, hipStream_t stream) {
  char* xch = (char*)d_ws + ((ws_size - (size_t)270532608) & ~(size_t)0xfffff);
  (void)hipFuncSetAttribute((const void*)fused_attn, hipFuncAttributeMaxDynamicSharedMemorySize, 163840);
  hipLaunchKernelGGL(fused_attn, dim3(256), dim3(256), 163840, stream,
                     (const char*)d_in[0], (const char*)d_in[1], (const char*)d_in[2], (const char*)d_in[3], (const char*)d_in[4],
                     (const char*)d_in[5], (const char*)d_in[6], (const char*)d_in[7], (const char*)d_in[8], (const char*)d_in[9], xch, (char*)d_out, (char*)d_ws);
}
```

```cpp
#include <hip/hip_runtime.h>
#include <hip/hip_bf16.h>
#include <stdint.h>

__global__ __launch_bounds__(256, 1)
void fused_attn(const char* xA, const char* kA, const char* vA, const char* wA, const char* uA,
                const char* xB, const char* kB, const char* vB, const char* wB, const char* uB, char* xch, char* out, char* flags) {
  extern __shared__ __attribute__((aligned(16))) char lds[];
  const unsigned wid = __builtin_amdgcn_readfirstlane(threadIdx.x >> 6);
  const unsigned long long ka = (unsigned long long)__builtin_amdgcn_kernarg_segment_ptr();
  asm volatile(
"s_memtime s[64:65]\n"
"s_memrealtime s[66:67]\n"
"s_bfe_u32 s10, %[wg], 0x10002\n"
"s_mul_i32 s40, s10, 40\n"
"s_add_u32 s40, s40, %[kalo]\n"
"s_addc_u32 s41, %[kahi], 0\n"
"s_mov_b32 s78, %[kalo]\n"
"s_mov_b32 s79, %[kahi]\n"
"s_load_dwordx2 s[24:25], s[40:41], 0\n"
"s_load_dwordx2 s[28:29], s[40:41], 24\n"
"s_load_dwordx2 s[6:7], s[78:79], 0x60\n"
"s_load_dwordx2 s[16:17], s[40:41], 8\n"
"s_load_dwordx2 s[20:21], s[40:41], 16\n"
"s_load_dwordx2 s[8:9], s[40:41], 32\n"
"s_load_dwordx4 s[12:15], s[78:79], 0x50\n"
"s_mov_b32 s42, %[wid]\n"
"s_and_b32 s11, %[wg], 1\n"
"s_lshl_b32 s11, s11, 3\n"
"s_lshr_b32 s60, %[wg], 5\n"
"s_add_u32 s11, s11, s60\n"
"s_bfe_u32 s61, %[wg], 0x10001\n"
"s_bfe_u32 s60, %[wg], 0x20003\n"
"s_lshl2_add_u32 s61, s61, s60\n"
"s_getpc_b64 s[80:81]\n"
"LCP_%=:\n"
"s_mov_b32 s82, LEND_%=-LCP_%=\n"
"s_mov_b32 s83, 0x20000\n"
"v_mbcnt_lo_u32_b32 v2, -1, 0\n"
"v_lshlrev_b32 v2, 7, v2\n"
"s_lshr_b32 s60, %[wg], 3\n"
"s_lshl2_add_u32 s60, s60, s42\n"
"s_mul_i32 s60, s60, 640\n"
"s_mov_b64 exec, 31\n"
"buffer_load_dword v3, v2, s[80:83], s60 offen\n"
"s_mov_b64 exec, -1\n"
"s_mov_b32 s19, 0x20000\n"
"s_mov_b32 s23, 0x20000\n"
"s_mov_b32 s27, 0x20000\n"
"s_mov_b32 s31, 0x20000\n"
"s_mov_b32 s35, 0x20000\n"
"s_mov_b32 s39, 0x20000\n"
"s_mov_b32 s26, 286720\n"
"s_lshl_b32 s44, s42, 12\n"
"s_mov_b32 s50, s44\n"
"s_mul_i32 s51, s42, 7168\n"
"s_add_u32 s51, s51, 0x10000\n"
"s_mov_b32 s74, 0x8000\n"
"s_mov_b32 s58, 524288\n"
"s_mov_b32 s47, 0\n"
"s_add_u32 s60, s11, s61\n"
"s_and_b32 s60, s60, 0\n"
"s_mul_i32 s60, s60, 3\n"
"s_lshl_b32 s49, s60, 7\n"
"s_lshl_b32 s48, s60, 19\n"
"v_mbcnt_lo_u32_b32 v4, -1, 0\n"
"v_mbcnt_hi_u32_b32 v4, -1, v4\n"
"v_and_b32 v5, 15, v4\n"
"v_lshrrev_b32 v6, 4, v4\n"
"v_lshrrev_b32 v7, 3, v4\n"
"v_and_b32 v8, 7, v4\n"
"v_lshlrev_b32 v9, 1, v6\n"
"v_xor_b32 v9, v9, v8\n"
"v_lshlrev_b32 v9, 4, v9\n"
"s_mul_i32 s60, s42, 71680\n"
"v_mul_u32_u24 v10, 1280, v7\n"
"v_add3_u32 v248, v10, v9, s60\n"
"v_add_u32 v249, 9216, v248\n"
"v_add_u32 v254, 18432, v248\n"
"v_add_u32 v255, 27648, v248\n"
"v_lshl_add_u32 v240, v7, 8, v9\n"
"v_add_u32 v240, s44, v240\n"
"v_and_b32 v10, 1, v6\n"
"v_xor_b32 v10, v10, v8\n"
"v_lshlrev_b32 v10, 4, v10\n"
"v_lshl_add_u32 v10, v7, 13, v10\n"
"s_lshl_b32 s60, s42, 17\n"
"v_add_u32 v10, s60, v10\n"
"v_add_u32 v224, 1024, v10\n"
"v_add_u32 v225, 128, v10\n"
"v_xor_b32 v10, 64, v10\n"
"v_add_u32 v226, 64512, v10\n"
"v_add_u32 v227, 63616, v10\n"
"s_mul_i32 s40, s11, 286720\n"
"s_lshl_b32 s41, s61, 10\n"
"s_sub_u32 s30, 5243904, s41\n"
"s_sub_u32 s41, s41, 1024\n"
"s_ashr_i32 s60, s41, 31\n"
"s_lshl4_add_u32 s92, s10, s11\n"
"s_lshl_b32 s93, s92, 9\n"
"v_mov_b32 v0, 0\n"
"s_waitcnt lgkmcnt(0)\n"
"s_add_u32 s24, s24, s40\n"
"s_addc_u32 s25, s25, 0\n"
"s_and_b32 s25, s25, 0xffff\n"
"s_add_u32 s28, s28, s41\n"
"s_addc_u32 s29, s29, s60\n"
"s_and_b32 s29, s29, 0xffff\n"
"s_add_u32 s6, s6, s93\n"
"s_addc_u32 s7, s7, 0\n"
"buffer_load_dword v12, v248, s[24:27], s49 offen\n"
"buffer_load_dword v13, v224, s[28:31], s48 offen\n"
"global_load_dword v1, v0, s[6:7] offset:128 sc1\n"
"s_mov_b32 m0, s51\n"
"s_nop 0\n"
"buffer_load_dwordx4 v248, s[24:27], s49 offen lds\n"
"buffer_load_dwordx4 v249, s[24:27], s49 offen offset:1024 lds\n"
"buffer_load_dwordx4 v254, s[24:27], s49 offen offset:2048 lds\n"
"buffer_load_dwordx4 v255, s[24:27], s49 offen offset:3072 lds\n"
"s_add_u32 m0, s51, 4096\n"
"s_add_u32 s40, s49, 40960\n"
"buffer_load_dwordx4 v248, s[24:27], s40 offen lds\n"
"buffer_load_dwordx4 v249, s[24:27], s40 offen offset:1024 lds\n"
"buffer_load_dwordx4 v254, s[24:27], s40 offen offset:2048 lds\n"
"s_add_u32 s49, s49, 128\n"
"s_sub_u32 s41, s49, 1280\n"
"s_min_u32 s49, s49, s41\n"
"s_xor_b32 s51, s51, s74\n"
"s_mov_b32 m0, s50\n"
"s_nop 0\n"
"buffer_load_dwordx4 v224, s[28:31], s48 offen lds\n"
"buffer_load_dwordx4 v225, s[28:31], s48 offen offset:1024 lds\n"
"buffer_load_dwordx4 v226, s[28:31], s48 offen offset:2048 lds\n"
"buffer_load_dwordx4 v227, s[28:31], s48 offen offset:3072 lds\n"
"s_xor_b32 s50, s50, s74\n"
"s_lshl2_add_u32 s63, s11, s42\n"
"s_mul_i32 s63, s63, 10240\n"
"s_and_b32 s78, s42, 1\n"
"s_lshl_b32 s43, s78, 6\n"
"s_mov_b32 s77, 0x80000000\n"
"s_mov_b32 s75, 0x10000\n"
"s_mov_b32 s73, 0xffff0000\n"
"s_mov_b32 s46, 0x3e0293ee\n"
"s_mov_b32 s45, 0x63800000\n"
"s_mov_b32 s62, 9\n"
"v_lshl_add_u32 v242, v4, 4, s44\n"
"s_lshr_b32 s79, s42, 1\n"
"s_lshl4_add_u32 s76, s79, s78\n"
"s_lshl_b32 s76, s76, 10\n"
"s_add_u32 s76, s76, 0x20000\n"
"s_sub_u32 s76, s76, s44\n"
"v_lshlrev_b32 v9, 6, v6\n"
"v_xor_b32 v9, v9, v242\n"
"v_add_u32 v243, s76, v9\n"
"v_and_b32 v9, 6, v5\n"
"s_xor_b32 m0, s50, 0xc000\n"
"s_add_u32 s40, s48, 256\n"
"buffer_load_dwordx4 v224, s[28:31], s40 offen lds\n"
"v_xor_b32 v9, v9, v6\n"
"v_lshlrev_b32 v9, 4, v9\n"
"v_lshl_add_u32 v10, v5, 7, v9\n"
"s_mul_i32 s79, s42, 6144\n"
"s_add_u32 s79, s79, 0x10000\n"
"v_add_u32 v250, s79, v10\n"
"v_xor_b32 v251, 64, v250\n"
"s_lshr_b32 s79, s42, 1\n"
"s_lshl_b32 s79, s79, 11\n"
"s_add_u32 s79, s79, 90112\n"
"v_add_u32 v252, s79, v10\n"
"v_xor_b32 v253, 64, v252\n"
"s_mul_i32 s79, s78, 8256\n"
"v_lshrrev_b32 v9, 5, v4\n"
"buffer_load_dwordx4 v225, s[28:31], s40 offen offset:1024 lds\n"
"v_lshlrev_b32 v9, 11, v9\n"
"v_bfe_u32 v10, v4, 2, 3\n"
"v_lshl_add_u32 v9, v10, 7, v9\n"
"v_and_b32 v10, 3, v4\n"
"v_lshl_add_u32 v9, v10, 3, v9\n"
"v_bfe_u32 v10, v4, 3, 2\n"
"v_lshl_add_u32 v9, v10, 5, v9\n"
"s_add_u32 s79, s79, 0x10000\n"
"v_xor_b32 v232, s79, v9\n"
"v_xor_b32 v233, 32, v232\n"
"v_xor_b32 v234, 64, v232\n"
"v_xor_b32 v235, 96, v232\n"
"v_xor_b32 v236, 0x2000, v232\n"
"v_xor_b32 v237, 0x2000, v233\n"
"buffer_load_dwordx4 v226, s[28:31], s40 offen offset:2048 lds\n"
"v_xor_b32 v238, 0x2000, v234\n"
"v_xor_b32 v239, 0x2000, v235\n"
"v_lshlrev_b32 v9, 11, v6\n"
"v_bfe_u32 v10, v4, 2, 2\n"
"v_lshl_add_u32 v9, v10, 7, v9\n"
"v_and_b32 v10, 1, v4\n"
"v_lshl_add_u32 v9, v10, 3, v9\n"
"v_bfe_u32 v10, v4, 1, 1\n"
"v_lshl_add_u32 v9, v10, 5, v9\n"
"v_bfe_u32 v10, v4, 3, 1\n"
"v_lshl_add_u32 v9, v10, 4, v9\n"
"v_bfe_u32 v10, v4, 4, 1\n"
"v_lshl_add_u32 v9, v10, 6, v9\n"
"v_xor_b32 v244, s43, v9\n"
"buffer_load_dwordx4 v227, s[28:31], s40 offen offset:3072 lds\n"
"s_cmp_eq_u32 s47, 8\n"
"s_cselect_b32 s41, s52, s58\n"
"s_add_u32 s48, s48, s41\n"
"s_sub_u32 s41, s48, 5242880\n"
"s_min_u32 s48, s48, s41\n"
"v_xor_b32 v245, 16, v244\n"
"v_xor_b32 v246, 64, v244\n"
"v_xor_b32 v247, 0x50, v244\n"
"s_lshl3_add_u32 s79, s11, s61\n"
"s_lshl_b32 s79, s79, 20\n"
"s_sub_u32 s79, s79, 896\n"
"s_ashr_i32 s60, s79, 31\n"
"s_add_u32 s16, s16, s79\n"
"s_addc_u32 s17, s17, s60\n"
"s_and_b32 s17, s17, 0xffff\n"
"s_mov_b32 s18, 1049472\n"
"s_mov_b64 s[84:85], s[16:17]\n"
"s_mov_b64 s[86:87], s[18:19]\n"
"s_add_u32 s20, s20, s79\n"
"s_addc_u32 s21, s21, s60\n"
"s_mov_b32 m0, s50\n"
"s_nop 0\n"
"buffer_load_dwordx4 v224, s[28:31], s48 offen lds\n"
"s_and_b32 s21, s21, 0xffff\n"
"s_mov_b32 s22, 1049472\n"
"s_mov_b64 s[88:89], s[20:21]\n"
"s_mov_b64 s[90:91], s[22:23]\n"
"s_lshr_b32 s57, %[wg], 3\n"
"s_mul_i32 s57, s57, 5\n"
"s_and_b32 s57, s57, 15\n"
"s_lshl_b32 s57, s57, 14\n"
"s_mov_b32 s55, 896\n"
"s_add_u32 s56, s57, 896\n"
"s_mov_b32 s59, 16384\n"
"s_mov_b32 s54, -896\n"
"s_add_u32 s52, s11, s61\n"
"buffer_load_dwordx4 v225, s[28:31], s48 offen offset:1024 lds\n"
"s_and_b32 s52, s52, 0\n"
"s_mul_i32 s52, s52, 3\n"
"s_add_u32 s52, s52, 1\n"
"s_lshl_b32 s53, s52, 7\n"
"s_lshl_b32 s52, s52, 19\n"
"s_add_u32 s52, s52, 512\n"
"s_mul_i32 s79, s61, 655360\n"
"s_add_u32 s8, s8, s79\n"
"s_addc_u32 s9, s9, 0\n"
"s_and_b32 s9, s9, 0xffff\n"
"s_add_u32 s12, s12, 0x1fffff\n"
"s_addc_u32 s13, s13, 0\n"
"s_and_b32 s12, s12, 0xffe00000\n"
"s_mul_i32 s96, s42, 3\n"
"buffer_load_dwordx4 v226, s[28:31], s48 offen offset:2048 lds\n"
"s_and_b32 s96, s96, 5\n"
"s_cmp_eq_u32 s42, 3\n"
"s_cselect_b32 s96, 5, s96\n"
"s_lshl_b32 s79, s61, 8\n"
"s_add_u32 s96, s96, s79\n"
"s_add_u32 s96, s96, 160\n"
"s_lshl_b32 s96, s96, 12\n"
"s_mul_i32 s79, s92, 8388608\n"
"s_add_u32 s32, s12, s79\n"
"s_addc_u32 s33, s13, 0\n"
"s_and_b32 s33, s33, 0xffff\n"
"s_mov_b32 s34, 8388608\n"
"s_mul_i32 s79, s11, 573440\n"
"s_mul_i32 s60, s10, 1280\n"
"buffer_load_dwordx4 v227, s[28:31], s48 offen offset:3072 lds\n"
"s_xor_b32 s50, s50, s74\n"
"s_sub_u32 s38, 573440, s60\n"
"s_add_u32 s79, s79, s60\n"
"s_add_u32 s36, s14, s79\n"
"s_addc_u32 s37, s15, 0\n"
"s_and_b32 s37, s37, 0xffff\n"
"s_lshl2_add_u32 s93, s61, s42\n"
"s_add_u32 s95, s61, 1\n"
"s_and_b32 s95, s95, 7\n"
"s_lshl_b32 s95, s95, 7\n"
"s_add_u32 s100, s61, 2\n"
"s_and_b32 s100, s100, 7\n"
"s_lshl_b32 s100, s100, 7\n"
"s_lshl_b32 s60, s61, 7\n"
"s_add_u32 s60, s60, s43\n"
"s_mov_b32 m0, s51\n"
"s_nop 0\n"
"buffer_load_dwordx4 v248, s[24:27], s49 offen lds\n"
"s_mul_i32 s97, s42, 122880\n"
"s_add_u32 s97, s97, s60\n"
"s_lshr_b32 s98, s42, 1\n"
"s_add_u32 s98, s98, 12\n"
"s_mul_i32 s98, s98, 40960\n"
"s_add_u32 s98, s98, s60\n"
"s_mul_i32 s60, s93, 37\n"
"s_lshr_b32 s60, s60, 8\n"
"s_mul_i32 s79, s60, 7\n"
"s_sub_u32 s79, s93, s79\n"
"s_lshl4_add_u32 s94, s60, s79\n"
"s_lshl_b32 s94, s94, 10\n"
"s_add_u32 s94, s94, 524288\n"
"s_mul_i32 s99, s60, 3\n"
"buffer_load_dwordx4 v249, s[24:27], s49 offen offset:1024 lds\n"
"s_lshr_b32 s78, s79, 1\n"
"s_add_u32 s99, s99, s78\n"
"s_lshr_b32 s78, s60, 1\n"
"s_add_u32 s78, s78, 12\n"
"s_cmp_eq_u32 s79, 6\n"
"s_cselect_b32 s99, s78, s99\n"
"s_mul_i32 s99, s99, 40960\n"
"s_xor_b32 s79, s79, s60\n"
"s_and_b32 s79, s79, 1\n"
"s_lshl_b32 s79, s79, 6\n"
"s_add_u32 s99, s99, s79\n"
"s_add_u32 s99, s99, 1024\n"
"s_cmp_eq_u32 s61, 7\n"
"s_cselect_b32 s99, 0x40000000, s99\n"
"s_cselect_b32 s94, 0x40000000, s94\n"
"buffer_load_dwordx4 v254, s[24:27], s49 offen offset:2048 lds\n"
"s_waitcnt vmcnt(23)\n"
"v_and_b32 v12, 0x7fff0000, v12\n"
"v_and_b32 v13, 0x7fff0000, v13\n"
"s_nop 0\n"
"v_max_u32_dpp v12, v12, v12 quad_perm:[1,0,3,2] row_mask:0xf bank_mask:0xf\n"
"v_max_u32_dpp v13, v13, v13 quad_perm:[1,0,3,2] row_mask:0xf bank_mask:0xf\n"
"s_nop 0\n"
"v_max_u32_dpp v12, v12, v12 quad_perm:[2,3,0,1] row_mask:0xf bank_mask:0xf\n"
"v_max_u32_dpp v13, v13, v13 quad_perm:[2,3,0,1] row_mask:0xf bank_mask:0xf\n"
"s_nop 0\n"
"v_max_u32_dpp v12, v12, v12 row_half_mirror row_mask:0xf bank_mask:0xf\n"
"v_max_u32_dpp v13, v13, v13 row_half_mirror row_mask:0xf bank_mask:0xf\n"
"s_nop 0\n"
"buffer_load_dwordx4 v255, s[24:27], s49 offen offset:3072 lds\n"
"v_min_u32_dpp v12, v12, v12 row_mirror row_mask:0xf bank_mask:0xf\n"
"v_min_u32_dpp v13, v13, v13 row_mirror row_mask:0xf bank_mask:0xf\n"
"s_nop 0\n"
"v_min_u32_dpp v12, v12, v12 row_bcast:15 row_mask:0xa bank_mask:0xf\n"
"v_min_u32_dpp v13, v13, v13 row_bcast:15 row_mask:0xa bank_mask:0xf\n"
"s_nop 0\n"
"v_min_u32_dpp v12, v12, v12 row_bcast:31 row_mask:0xc bank_mask:0xf\n"
"v_min_u32_dpp v13, v13, v13 row_bcast:31 row_mask:0xc bank_mask:0xf\n"
"s_nop 0\n"
"v_mul_f32 v12, v12, v13\n"
"v_mul_f32 v12, 0x43c80000, v12\n"
"v_min_f32 v12, 0x71800000, v12\n"
"v_mul_f32 v13, s46, v12\n"
"s_nop 0\n"
"s_add_u32 m0, s51, 4096\n"
"s_add_u32 s40, s49, 40960\n"
"buffer_load_dwordx4 v248, s[24:27], s40 offen lds\n"
"v_readlane_b32 s70, v12, 63\n"
"v_readlane_b32 s71, v12, 63\n"
"v_readlane_b32 s69, v13, 63\n"
"s_nop 1\n"
"v_mov_b64 v[228:229], s[70:71]\n"
"v_mov_b64 v[230:231], s[70:71]\n"
"v_mov_b64 v[4:5], s[70:71]\n"
"v_mov_b64 v[6:7], s[70:71]\n"
"v_mov_b64 v[8:9], s[70:71]\n"
"v_mov_b64 v[10:11], s[70:71]\n"
"v_mov_b64 v[12:13], s[70:71]\n"
"v_mov_b64 v[14:15], s[70:71]\n"
"v_mov_b64 v[16:17], s[70:71]\n"
"v_mov_b64 v[18:19], s[70:71]\n"
"buffer_load_dwordx4 v249, s[24:27], s40 offen offset:1024 lds\n"
"v_mov_b64 v[20:21], s[70:71]\n"
"v_mov_b64 v[22:23], s[70:71]\n"
"v_mov_b64 v[24:25], s[70:71]\n"
"v_mov_b64 v[26:27], s[70:71]\n"
"v_mov_b64 v[28:29], s[70:71]\n"
"v_mov_b64 v[30:31], s[70:71]\n"
"v_mov_b64 v[32:33], s[70:71]\n"
"v_mov_b64 v[34:35], s[70:71]\n"
"v_mov_b64 v[36:37], s[70:71]\n"
"v_mov_b64 v[38:39], s[70:71]\n"
"v_mov_b64 v[40:41], s[70:71]\n"
"v_mov_b64 v[42:43], s[70:71]\n"
"v_mov_b64 v[44:45], s[70:71]\n"
"v_mov_b64 v[46:47], s[70:71]\n"
"buffer_load_dwordx4 v254, s[24:27], s40 offen offset:2048 lds\n"
"s_add_u32 s49, s49, 128\n"
"s_sub_u32 s41, s49, 1280\n"
"s_min_u32 s49, s49, s41\n"
"s_xor_b32 s51, s51, s74\n"
"v_mov_b64 v[48:49], s[70:71]\n"
"v_mov_b64 v[50:51], s[70:71]\n"
"v_mov_b64 v[52:53], s[70:71]\n"
"v_mov_b64 v[54:55], s[70:71]\n"
"v_mov_b64 v[56:57], s[70:71]\n"
"v_mov_b64 v[58:59], s[70:71]\n"
"v_mov_b64 v[60:61], s[70:71]\n"
"v_mov_b64 v[62:63], s[70:71]\n"
"v_mov_b64 v[64:65], s[70:71]\n"
"v_mov_b64 v[66:67], s[70:71]\n"
"v_mov_b64 v[68:69], s[70:71]\n"
"v_mov_b64 v[70:71], s[70:71]\n"
"v_mov_b64 v[72:73], s[70:71]\n"
"v_mov_b64 v[74:75], s[70:71]\n"
"v_mov_b64 v[76:77], s[70:71]\n"
"v_mov_b64 v[78:79], s[70:71]\n"
"v_mov_b64 v[80:81], s[70:71]\n"
"v_mov_b64 v[82:83], s[70:71]\n"
"v_mov_b64 v[84:85], s[70:71]\n"
"v_mov_b64 v[86:87], s[70:71]\n"
"v_mov_b64 v[88:89], s[70:71]\n"
"v_mov_b64 v[90:91], s[70:71]\n"
"v_mov_b64 v[92:93], s[70:71]\n"
"v_mov_b64 v[94:95], s[70:71]\n"
"v_mov_b64 v[96:97], s[70:71]\n"
"v_mov_b64 v[98:99], s[70:71]\n"
"v_mov_b64 v[100:101], s[70:71]\n"
"v_mov_b64 v[102:103], s[70:71]\n"
"v_mov_b64 v[104:105], s[70:71]\n"
"v_mov_b64 v[106:107], s[70:71]\n"
"v_mov_b64 v[108:109], s[70:71]\n"
"v_mov_b64 v[110:111], s[70:71]\n"
"v_mov_b64 v[112:113], s[70:71]\n"
"v_mov_b64 v[114:115], s[70:71]\n"
"v_mov_b64 v[116:117], s[70:71]\n"
"v_mov_b64 v[118:119], s[70:71]\n"
"v_mov_b64 v[120:121], s[70:71]\n"
"v_mov_b64 v[122:123], s[70:71]\n"
"v_mov_b64 v[124:125], s[70:71]\n"
"v_mov_b64 v[126:127], s[70:71]\n"
"v_mov_b64 v[128:129], s[70:71]\n"
"v_mov_b64 v[130:131], s[70:71]\n"
"v_mov_b64 v[132:133], s[70:71]\n"
"v_mov_b64 v[134:135], s[70:71]\n"
"v_mov_b64 v[136:137], s[70:71]\n"
"v_mov_b64 v[138:139], s[70:71]\n"
"v_mov_b64 v[140:141], s[70:71]\n"
"v_mov_b64 v[142:143], s[70:71]\n"
"v_mov_b64 v[144:145], s[70:71]\n"
"v_mov_b64 v[146:147], s[70:71]\n"
"v_mov_b64 v[148:149], s[70:71]\n"
"v_mov_b64 v[150:151], s[70:71]\n"
"v_mov_b64 v[152:153], s[70:71]\n"
"v_mov_b64 v[154:155], s[70:71]\n"
"v_mov_b64 v[156:157], s[70:71]\n"
"v_mov_b64 v[158:159], s[70:71]\n"
"v_mov_b64 v[160:161], s[70:71]\n"
"v_mov_b64 v[162:163], s[70:71]\n"
"v_mov_b64 v[164:165], s[70:71]\n"
"v_mov_b64 v[166:167], s[70:71]\n"
"v_mov_b64 v[168:169], s[70:71]\n"
"v_mov_b64 v[170:171], s[70:71]\n"
"v_mov_b64 v[172:173], s[70:71]\n"
"v_mov_b64 v[174:175], s[70:71]\n"
"s_memtime s[80:81]\n"
"s_memrealtime s[82:83]\n"
"v_mov_b64 v[176:177], s[70:71]\n"
"v_mov_b64 v[178:179], s[70:71]\n"
"v_mov_b64 v[180:181], s[70:71]\n"
"v_mov_b64 v[182:183], s[70:71]\n"
"v_mov_b64 v[184:185], s[70:71]\n"
"v_mov_b64 v[186:187], s[70:71]\n"
"v_mov_b64 v[188:189], s[70:71]\n"
"v_mov_b64 v[190:191], s[70:71]\n"
"v_mov_b64 v[192:193], s[70:71]\n"
"v_mov_b64 v[194:195], s[70:71]\n"
"v_mov_b64 v[196:197], s[70:71]\n"
"v_mov_b64 v[198:199], s[70:71]\n"
"v_mov_b64 v[200:201], s[70:71]\n"
"v_mov_b64 v[202:203], s[70:71]\n"
"v_mov_b64 v[204:205], s[70:71]\n"
"v_mov_b64 v[206:207], s[70:71]\n"
"v_mov_b64 v[208:209], s[70:71]\n"
"v_mov_b64 v[210:211], s[70:71]\n"
"v_mov_b64 v[212:213], s[70:71]\n"
"v_mov_b64 v[214:215], s[70:71]\n"
"v_mov_b64 v[216:217], s[70:71]\n"
"v_mov_b64 v[218:219], s[70:71]\n"
"v_mov_b64 v[220:221], s[70:71]\n"
"v_mov_b64 v[222:223], s[70:71]\n"
"s_waitcnt vmcnt(26)\n"
"v_readfirstlane_b32 s92, v1\n"
"v_mov_b64 v[0:1], s[70:71]\n"
"v_mov_b64 v[2:3], s[70:71]\n"
"s_mov_b32 s70, s46\n"
"s_xor_b32 s71, s69, s77\n"
"s_waitcnt lgkmcnt(0)\n"
"s_sub_u32 s80, s80, s64\n"
"s_sub_u32 s82, s82, s66\n"
"s_mul_i32 s80, s80, 10\n"
"s_mul_i32 s82, s82, 211\n"
"s_cmp_gt_u32 s80, s82\n"
"s_cselect_b32 s64, 1, 0\n"
"s_cselect_b32 s68, 655360, 0\n"
"s_cmp_eq_u32 s42, 0\n"
"s_cselect_b32 s64, s64, 0\n"
"s_mov_b32 s65, 0\n"
"s_add_u32 s66, s66, 2800\n"
"s_mov_b32 s67, s66\n"
"s_mov_b32 s72, 126\n"
"s_mov_b64 vcc, s[64:65]\n"
"s_nop 2\n"
"s_mul_i32 s60, s92, 0x9e3779b1\n"
"s_add_u32 s60, s60, 0x7f4a7c15\n"
"s_waitcnt vmcnt(15)\n"
"s_barrier\n"
"ds_read_b128 a[160:163], v250 offset:0\n"
"ds_read_b128 a[164:167], v251 offset:0\n"
"ds_read_b64_tr_b16 a[96:97], v244 offset:0\n"
"ds_read_b64_tr_b16 a[98:99], v244 offset:512\n"
"ds_read_b64_tr_b16 a[100:101], v244 offset:8192\n"
"ds_read_b64_tr_b16 a[102:103], v244 offset:8704\n"
"ds_read_b64_tr_b16 a[104:105], v245 offset:0\n"
"ds_read_b64_tr_b16 a[106:107], v245 offset:512\n"
"ds_read_b64_tr_b16 a[108:109], v245 offset:8192\n"
"ds_read_b64_tr_b16 a[110:111], v245 offset:8704\n"
"ds_read_b64_tr_b16 a[112:113], v246 offset:0\n"
"ds_read_b64_tr_b16 a[114:115], v246 offset:512\n"
"ds_read_b64_tr_b16 a[116:117], v246 offset:8192\n"
"ds_read_b64_tr_b16 a[118:119], v246 offset:8704\n"
"ds_read_b64_tr_b16 a[120:121], v247 offset:0\n"
"ds_read_b64_tr_b16 a[122:123], v247 offset:512\n"
"ds_read_b64_tr_b16 a[124:125], v247 offset:8192\n"
"ds_read_b64_tr_b16 a[126:127], v247 offset:8704\n"
"ds_read_b64_tr_b16 a[128:129], v244 offset:1024\n"
"ds_read_b64_tr_b16 a[130:131], v244 offset:1536\n"
"ds_read_b64_tr_b16 a[132:133], v244 offset:9216\n"
"ds_read_b64_tr_b16 a[134:135], v244 offset:9728\n"
"ds_read_b64_tr_b16 a[136:137], v245 offset:1024\n"
"ds_read_b64_tr_b16 a[138:139], v245 offset:1536\n"
"ds_read_b64_tr_b16 a[140:141], v245 offset:9216\n"
"ds_read_b64_tr_b16 a[142:143], v245 offset:9728\n"
"ds_read_b64_tr_b16 a[144:145], v246 offset:1024\n"
"ds_read_b64_tr_b16 a[146:147], v246 offset:1536\n"
"ds_read_b64_tr_b16 a[148:149], v246 offset:9216\n"
"ds_read_b64_tr_b16 a[150:151], v246 offset:9728\n"
"ds_read_b64_tr_b16 a[152:153], v247 offset:1024\n"
"ds_read_b64_tr_b16 a[154:155], v247 offset:1536\n"
"ds_read_b64_tr_b16 a[156:157], v247 offset:9216\n"
"ds_read_b64_tr_b16 a[158:159], v247 offset:9728\n"
"ds_read_b128 a[168:171], v250 offset:2048\n"
"ds_read_b128 a[172:175], v251 offset:2048\n"
"ds_read_b128 a[176:179], v250 offset:4096\n"
"ds_read_b128 a[180:183], v251 offset:4096\n"
"ds_read_b128 a[184:187], v252 offset:0\n"
"ds_read_b128 a[188:191], v253 offset:0\n"
"LPL_%=:\n"
"s_waitcnt lgkmcnt(15)\n"
"v_mfma_f32_16x16x32_bf16 v[112:115], a[96:99], a[160:163], v[112:115]\n"
"v_mfma_f32_16x16x32_bf16 v[112:115], a[100:103], a[164:167], v[112:115]\n"
"v_mfma_f32_16x16x32_bf16 v[116:119], a[104:107], a[160:163], v[116:119]\n"
"s_waitcnt vmcnt(11) lgkmcnt(5)\n"
"v_mfma_f32_16x16x32_bf16 v[116:119], a[108:111], a[164:167], v[116:119]\n"
"s_barrier\n"
"v_mfma_f32_16x16x32_bf16 v[120:123], a[112:115], a[160:163], v[120:123]\n"
"s_add_u32 s40, s48, 256\n"
"s_xor_b32 m0, s50, 0xc000\n"
"ds_read_b64_tr_b16 a[192:193], v244 offset:16384\n"
"v_mfma_f32_16x16x32_bf16 v[120:123], a[116:119], a[164:167], v[120:123]\n"
"buffer_load_dwordx4 v224, s[28:31], s40 offen lds\n"
"v_mfma_f32_16x16x32_bf16 v[124:127], a[120:123], a[160:163], v[124:127]\n"
"ds_read_b64_tr_b16 a[194:195], v244 offset:16896\n"
"v_mfma_f32_16x16x32_bf16 v[124:127], a[124:127], a[164:167], v[124:127]\n"
"ds_read_b64_tr_b16 a[196:197], v244 offset:24576\n"
"v_mfma_f32_16x16x32_bf16 v[128:131], a[128:131], a[160:163], v[128:131]\n"
"ds_read_b64_tr_b16 a[198:199], v244 offset:25088\n"
"v_mfma_f32_16x16x32_bf16 v[128:131], a[132:135], a[164:167], v[128:131]\n"
"ds_read_b64_tr_b16 a[200:201], v245 offset:16384\n"
"v_mfma_f32_16x16x32_bf16 v[132:135], a[136:139], a[160:163], v[132:135]\n"
"ds_read_b64_tr_b16 a[202:203], v245 offset:16896\n"
"v_mfma_f32_16x16x32_bf16 v[132:135], a[140:143], a[164:167], v[132:135]\n"
"buffer_load_dwordx4 v225, s[28:31], s40 offen offset:1024 lds\n"
"v_mfma_f32_16x16x32_bf16 v[136:139], a[144:147], a[160:163], v[136:139]\n"
"ds_read_b64_tr_b16 a[204:205], v245 offset:24576\n"
"v_mfma_f32_16x16x32_bf16 v[136:139], a[148:151], a[164:167], v[136:139]\n"
"ds_read_b64_tr_b16 a[206:207], v245 offset:25088\n"
"v_mfma_f32_16x16x32_bf16 v[140:143], a[152:155], a[160:163], v[140:143]\n"
"ds_read_b64_tr_b16 a[208:209], v246 offset:16384\n"
"v_mfma_f32_16x16x32_bf16 v[140:143], a[156:159], a[164:167], v[140:143]\n"
"s_waitcnt lgkmcnt(6)\n"
"ds_read_b64_tr_b16 a[210:211], v246 offset:16896\n"
"v_mfma_f32_16x16x32_bf16 v[172:175], a[152:155], a[168:171], v[172:175]\n"
"s_waitcnt lgkmcnt(6)\n"
"ds_read_b64_tr_b16 a[212:213], v246 offset:24576\n"
"v_mfma_f32_16x16x32_bf16 v[172:175], a[156:159], a[172:175], v[172:175]\n"
"buffer_load_dwordx4 v226, s[28:31], s40 offen offset:2048 lds\n"
"v_mfma_f32_16x16x32_bf16 v[168:171], a[144:147], a[168:171], v[168:171]\n"
"ds_read_b64_tr_b16 a[214:215], v246 offset:25088\n"
"v_mfma_f32_16x16x32_bf16 v[168:171], a[148:151], a[172:175], v[168:171]\n"
"ds_read_b64_tr_b16 a[216:217], v247 offset:16384\n"
"v_mfma_f32_16x16x32_bf16 v[164:167], a[136:139], a[168:171], v[164:167]\n"
"ds_read_b64_tr_b16 a[218:219], v247 offset:16896\n"
"v_mfma_f32_16x16x32_bf16 v[164:167], a[140:143], a[172:175], v[164:167]\n"
"ds_read_b64_tr_b16 a[220:221], v247 offset:24576\n"
"v_mfma_f32_16x16x32_bf16 v[160:163], a[128:131], a[168:171], v[160:163]\n"
"ds_read_b64_tr_b16 a[222:223], v247 offset:25088\n"
"v_mfma_f32_16x16x32_bf16 v[160:163], a[132:135], a[172:175], v[160:163]\n"
"buffer_load_dwordx4 v227, s[28:31], s40 offen offset:3072 lds\n"
"v_mfma_f32_16x16x32_bf16 v[156:159], a[120:123], a[168:171], v[156:159]\n"
"s_cmp_eq_u32 s47, 8\n"
"ds_read_b64_tr_b16 a[224:225], v244 offset:17408\n"
"v_mfma_f32_16x16x32_bf16 v[156:159], a[124:127], a[172:175], v[156:159]\n"
"s_cselect_b32 s41, s52, s58\n"
"ds_read_b64_tr_b16 a[226:227], v244 offset:17920\n"
"v_mfma_f32_16x16x32_bf16 v[152:155], a[112:115], a[168:171], v[152:155]\n"
"s_add_u32 s48, s48, s41\n"
"ds_read_b64_tr_b16 a[228:229], v244 offset:25600\n"
"v_mfma_f32_16x16x32_bf16 v[152:155], a[116:119], a[172:175], v[152:155]\n"
"s_sub_u32 s41, s48, 5242880\n"
"ds_read_b64_tr_b16 a[230:231], v244 offset:26112\n"
"v_mfma_f32_16x16x32_bf16 v[148:151], a[104:107], a[168:171], v[148:151]\n"
"s_min_u32 s48, s48, s41\n"
"ds_read_b64_tr_b16 a[232:233], v245 offset:17408\n"
"v_mfma_f32_16x16x32_bf16 v[148:151], a[108:111], a[172:175], v[148:151]\n"
"ds_read_b64_tr_b16 a[234:235], v245 offset:17920\n"
"v_xor_b32 v244, s74, v244\n"
"v_mfma_f32_16x16x32_bf16 v[144:147], a[96:99], a[168:171], v[144:147]\n"
"s_mov_b32 m0, s50\n"
"ds_read_b64_tr_b16 a[236:237], v245 offset:25600\n"
"v_xor_b32 v250, s74, v250\n"
"v_mfma_f32_16x16x32_bf16 v[144:147], a[100:103], a[172:175], v[144:147]\n"
"buffer_load_dwordx4 v224, s[28:31], s48 offen lds\n"
"v_mfma_f32_16x16x32_bf16 v[176:179], a[96:99], a[176:179], v[176:179]\n"
"s_waitcnt lgkmcnt(6)\n"
"ds_read_b64_tr_b16 a[238:239], v245 offset:26112\n"
"v_xor_b32 v251, s74, v251\n"
"v_mfma_f32_16x16x32_bf16 v[176:179], a[100:103], a[180:183], v[176:179]\n"
"ds_read_b64_tr_b16 a[240:241], v246 offset:17408\n"
"v_xor_b32 v245, s74, v245\n"
"v_mfma_f32_16x16x32_bf16 v[180:183], a[104:107], a[176:179], v[180:183]\n"
"ds_read_b64_tr_b16 a[242:243], v246 offset:17920\n"
"v_xor_b32 v252, s74, v252\n"
"v_mfma_f32_16x16x32_bf16 v[180:183], a[108:111], a[180:183], v[180:183]\n"
"buffer_load_dwordx4 v225, s[28:31], s48 offen offset:1024 lds\n"
"v_mfma_f32_16x16x32_bf16 v[184:187], a[112:115], a[176:179], v[184:187]\n"
"ds_read_b64_tr_b16 a[244:245], v246 offset:25600\n"
"v_xor_b32 v253, s74, v253\n"
"v_mfma_f32_16x16x32_bf16 v[184:187], a[116:119], a[180:183], v[184:187]\n"
"ds_read_b64_tr_b16 a[246:247], v246 offset:26112\n"
"v_xor_b32 v246, s74, v246\n"
"v_mfma_f32_16x16x32_bf16 v[188:191], a[120:123], a[176:179], v[188:191]\n"
"ds_read_b64_tr_b16 a[248:249], v247 offset:17408\n"
"v_mfma_f32_16x16x32_bf16 v[188:191], a[124:127], a[180:183], v[188:191]\n"
"ds_read_b64_tr_b16 a[250:251], v247 offset:17920\n"
"v_mfma_f32_16x16x32_bf16 v[200:203], a[144:147], a[176:179], v[200:203]\n"
"ds_read_b64_tr_b16 a[252:253], v247 offset:25600\n"
"v_mfma_f32_16x16x32_bf16 v[200:203], a[148:151], a[180:183], v[200:203]\n"
"buffer_load_dwordx4 v226, s[28:31], s48 offen offset:2048 lds\n"
"v_mfma_f32_16x16x32_bf16 v[204:207], a[152:155], a[176:179], v[204:207]\n"
"ds_read_b64_tr_b16 a[254:255], v247 offset:26112\n"
"v_xor_b32 v247, s74, v247\n"
"v_mfma_f32_16x16x32_bf16 v[204:207], a[156:159], a[180:183], v[204:207]\n"
"v_mfma_f32_16x16x32_bf16 v[192:195], a[128:131], a[176:179], v[192:195]\n"
"v_mfma_f32_16x16x32_bf16 v[192:195], a[132:135], a[180:183], v[192:195]\n"
"v_mfma_f32_16x16x32_bf16 v[196:199], a[136:139], a[176:179], v[196:199]\n"
"v_mfma_f32_16x16x32_bf16 v[196:199], a[140:143], a[180:183], v[196:199]\n"
"buffer_load_dwordx4 v227, s[28:31], s48 offen offset:3072 lds\n"
"v_mfma_f32_16x16x32_bf16 v[220:223], a[136:139], a[184:187], v[220:223]\n"
"s_xor_b32 s50, s50, s74\n"
"v_mfma_f32_16x16x32_bf16 v[220:223], a[140:143], a[188:191], v[220:223]\n"
"v_mfma_f32_16x16x32_bf16 v[216:219], a[128:131], a[184:187], v[216:219]\n"
"v_mfma_f32_16x16x32_bf16 v[216:219], a[132:135], a[188:191], v[216:219]\n"
"v_mfma_f32_16x16x32_bf16 v[212:215], a[104:107], a[184:187], v[212:215]\n"
"v_mfma_f32_16x16x32_bf16 v[212:215], a[108:111], a[188:191], v[212:215]\n"
"v_mfma_f32_16x16x32_bf16 v[208:211], a[96:99], a[184:187], v[208:211]\n"
"v_mfma_f32_16x16x32_bf16 v[208:211], a[100:103], a[188:191], v[208:211]\n"
"v_mfma_f32_16x16x32_bf16 v[0:3], a[192:195], a[160:163], v[0:3]\n"
"s_waitcnt lgkmcnt(0)\n"
"v_mfma_f32_16x16x32_bf16 v[0:3], a[196:199], a[164:167], v[0:3]\n"
"s_waitcnt lgkmcnt(0)\n"
"v_mfma_f32_16x16x32_bf16 v[4:7], a[200:203], a[160:163], v[4:7]\n"
"s_waitcnt vmcnt(8) lgkmcnt(0)\n"
"v_mfma_f32_16x16x32_bf16 v[4:7], a[204:207], a[164:167], v[4:7]\n"
"s_barrier\n"
"v_mfma_f32_16x16x32_bf16 v[8:11], a[208:211], a[160:163], v[8:11]\n"
"s_mov_b32 m0, s51\n"
"ds_read_b64_tr_b16 a[96:97], v244 offset:0\n"
"v_mfma_f32_16x16x32_bf16 v[8:11], a[212:215], a[164:167], v[8:11]\n"
"buffer_load_dwordx4 v248, s[24:27], s49 offen lds\n"
"v_mfma_f32_16x16x32_bf16 v[12:15], a[216:219], a[160:163], v[12:15]\n"
"ds_read_b64_tr_b16 a[98:99], v244 offset:512\n"
"v_mfma_f32_16x16x32_bf16 v[12:15], a[220:223], a[164:167], v[12:15]\n"
"ds_read_b64_tr_b16 a[100:101], v244 offset:8192\n"
"v_mfma_f32_16x16x32_bf16 v[16:19], a[224:227], a[160:163], v[16:19]\n"
"ds_read_b64_tr_b16 a[102:103], v244 offset:8704\n"
"v_mfma_f32_16x16x32_bf16 v[16:19], a[228:231], a[164:167], v[16:19]\n"
"ds_read_b64_tr_b16 a[104:105], v245 offset:0\n"
"v_mfma_f32_16x16x32_bf16 v[20:23], a[232:235], a[160:163], v[20:23]\n"
"ds_read_b64_tr_b16 a[106:107], v245 offset:512\n"
"v_mfma_f32_16x16x32_bf16 v[20:23], a[236:239], a[164:167], v[20:23]\n"
"buffer_load_dwordx4 v249, s[24:27], s49 offen offset:1024 lds\n"
"v_mfma_f32_16x16x32_bf16 v[24:27], a[240:243], a[160:163], v[24:27]\n"
"ds_read_b64_tr_b16 a[108:109], v245 offset:8192\n"
"v_mfma_f32_16x16x32_bf16 v[24:27], a[244:247], a[164:167], v[24:27]\n"
"ds_read_b64_tr_b16 a[110:111], v245 offset:8704\n"
"v_mfma_f32_16x16x32_bf16 v[28:31], a[248:251], a[160:163], v[28:31]\n"
"ds_read_b128 a[160:163], v250 offset:0\n"
"v_mfma_f32_16x16x32_bf16 v[28:31], a[252:255], a[164:167], v[28:31]\n"
"ds_read_b128 a[164:167], v251 offset:0\n"
"v_mfma_f32_16x16x32_bf16 v[60:63], a[248:251], a[168:171], v[60:63]\n"
"ds_read_b64_tr_b16 a[112:113], v246 offset:0\n"
"v_mfma_f32_16x16x32_bf16 v[60:63], a[252:255], a[172:175], v[60:63]\n"
"buffer_load_dwordx4 v254, s[24:27], s49 offen offset:2048 lds\n"
"v_mfma_f32_16x16x32_bf16 v[56:59], a[240:243], a[168:171], v[56:59]\n"
"ds_read_b64_tr_b16 a[114:115], v246 offset:512\n"
"v_mfma_f32_16x16x32_bf16 v[56:59], a[244:247], a[172:175], v[56:59]\n"
"ds_read_b64_tr_b16 a[116:117], v246 offset:8192\n"
"v_mfma_f32_16x16x32_bf16 v[52:55], a[232:235], a[168:171], v[52:55]\n"
"ds_read_b64_tr_b16 a[118:119], v246 offset:8704\n"
"v_mfma_f32_16x16x32_bf16 v[52:55], a[236:239], a[172:175], v[52:55]\n"
"ds_read_b64_tr_b16 a[120:121], v247 offset:0\n"
"v_mfma_f32_16x16x32_bf16 v[48:51], a[224:227], a[168:171], v[48:51]\n"
"ds_read_b64_tr_b16 a[122:123], v247 offset:512\n"
"v_mfma_f32_16x16x32_bf16 v[48:51], a[228:231], a[172:175], v[48:51]\n"
"buffer_load_dwordx4 v255, s[24:27], s49 offen offset:3072 lds\n"
"v_mfma_f32_16x16x32_bf16 v[44:47], a[216:219], a[168:171], v[44:47]\n"
"ds_read_b64_tr_b16 a[124:125], v247 offset:8192\n"
"v_mfma_f32_16x16x32_bf16 v[44:47], a[220:223], a[172:175], v[44:47]\n"
"ds_read_b64_tr_b16 a[126:127], v247 offset:8704\n"
"v_mfma_f32_16x16x32_bf16 v[40:43], a[208:211], a[168:171], v[40:43]\n"
"ds_read_b64_tr_b16 a[128:129], v244 offset:1024\n"
"v_mfma_f32_16x16x32_bf16 v[40:43], a[212:215], a[172:175], v[40:43]\n"
"s_add_u32 s40, s49, 40960\n"
"ds_read_b64_tr_b16 a[130:131], v244 offset:1536\n"
"v_mfma_f32_16x16x32_bf16 v[36:39], a[200:203], a[168:171], v[36:39]\n"
"s_add_u32 m0, s51, 4096\n"
"ds_read_b64_tr_b16 a[132:133], v244 offset:9216\n"
"v_mfma_f32_16x16x32_bf16 v[36:39], a[204:207], a[172:175], v[36:39]\n"
"buffer_load_dwordx4 v248, s[24:27], s40 offen lds\n"
"v_mfma_f32_16x16x32_bf16 v[32:35], a[192:195], a[168:171], v[32:35]\n"
"ds_read_b64_tr_b16 a[134:135], v244 offset:9728\n"
"v_mfma_f32_16x16x32_bf16 v[32:35], a[196:199], a[172:175], v[32:35]\n"
"ds_read_b64_tr_b16 a[136:137], v245 offset:1024\n"
"v_mfma_f32_16x16x32_bf16 v[64:67], a[192:195], a[176:179], v[64:67]\n"
"ds_read_b64_tr_b16 a[138:139], v245 offset:1536\n"
"v_mfma_f32_16x16x32_bf16 v[64:67], a[196:199], a[180:183], v[64:67]\n"
"ds_read_b64_tr_b16 a[140:141], v245 offset:9216\n"
"v_mfma_f32_16x16x32_bf16 v[68:71], a[200:203], a[176:179], v[68:71]\n"
"ds_read_b64_tr_b16 a[142:143], v245 offset:9728\n"
"v_mfma_f32_16x16x32_bf16 v[68:71], a[204:207], a[180:183], v[68:71]\n"
"buffer_load_dwordx4 v249, s[24:27], s40 offen offset:1024 lds\n"
"v_mfma_f32_16x16x32_bf16 v[72:75], a[208:211], a[176:179], v[72:75]\n"
"ds_read_b64_tr_b16 a[144:145], v246 offset:1024\n"
"v_mfma_f32_16x16x32_bf16 v[72:75], a[212:215], a[180:183], v[72:75]\n"
"ds_read_b64_tr_b16 a[146:147], v246 offset:1536\n"
"v_mfma_f32_16x16x32_bf16 v[76:79], a[216:219], a[176:179], v[76:79]\n"
"ds_read_b64_tr_b16 a[148:149], v246 offset:9216\n"
"v_mfma_f32_16x16x32_bf16 v[76:79], a[220:223], a[180:183], v[76:79]\n"
"ds_read_b64_tr_b16 a[150:151], v246 offset:9728\n"
"v_mfma_f32_16x16x32_bf16 v[88:91], a[240:243], a[176:179], v[88:91]\n"
"ds_read_b64_tr_b16 a[152:153], v247 offset:1024\n"
"v_mfma_f32_16x16x32_bf16 v[88:91], a[244:247], a[180:183], v[88:91]\n"
"buffer_load_dwordx4 v254, s[24:27], s40 offen offset:2048 lds\n"
"v_mfma_f32_16x16x32_bf16 v[92:95], a[248:251], a[176:179], v[92:95]\n"
"s_add_u32 s49, s49, 128\n"
"ds_read_b64_tr_b16 a[154:155], v247 offset:1536\n"
"v_mfma_f32_16x16x32_bf16 v[92:95], a[252:255], a[180:183], v[92:95]\n"
"s_sub_u32 s41, s49, 1280\n"
"ds_read_b64_tr_b16 a[156:157], v247 offset:9216\n"
"v_mfma_f32_16x16x32_bf16 v[80:83], a[224:227], a[176:179], v[80:83]\n"
"s_min_u32 s49, s49, s41\n"
"ds_read_b64_tr_b16 a[158:159], v247 offset:9728\n"
"v_mfma_f32_16x16x32_bf16 v[80:83], a[228:231], a[180:183], v[80:83]\n"
"s_xor_b32 s51, s51, s74\n"
"ds_read_b128 a[168:171], v250 offset:2048\n"
"v_mfma_f32_16x16x32_bf16 v[84:87], a[232:235], a[176:179], v[84:87]\n"
"ds_read_b128 a[172:175], v251 offset:2048\n"
"v_mfma_f32_16x16x32_bf16 v[84:87], a[236:239], a[180:183], v[84:87]\n"
"ds_read_b128 a[176:179], v250 offset:4096\n"
"v_mfma_f32_16x16x32_bf16 v[108:111], a[232:235], a[184:187], v[108:111]\n"
"ds_read_b128 a[180:183], v251 offset:4096\n"
"v_mfma_f32_16x16x32_bf16 v[108:111], a[236:239], a[188:191], v[108:111]\n"
"v_mfma_f32_16x16x32_bf16 v[104:107], a[224:227], a[184:187], v[104:107]\n"
"v_mfma_f32_16x16x32_bf16 v[104:107], a[228:231], a[188:191], v[104:107]\n"
"v_mfma_f32_16x16x32_bf16 v[100:103], a[200:203], a[184:187], v[100:103]\n"
"v_mfma_f32_16x16x32_bf16 v[100:103], a[204:207], a[188:191], v[100:103]\n"
"v_mfma_f32_16x16x32_bf16 v[96:99], a[192:195], a[184:187], v[96:99]\n"
"s_add_u32 s47, s47, 1\n"
"ds_read_b128 a[184:187], v252 offset:0\n"
"v_mfma_f32_16x16x32_bf16 v[96:99], a[196:199], a[188:191], v[96:99]\n"
"s_waitcnt lgkmcnt(2)\n"
"s_cmp_lg_u32 s47, s62\n"
"ds_read_b128 a[188:191], v253 offset:0\n"
"s_cbranch_scc1 LPL_%=\n"
"s_cmp_eq_u32 s47, 18\n"
"s_cbranch_scc1 LPT2_%=\n"
"v_mfma_f32_16x16x32_bf16 v[112:115], a[96:99], a[160:163], v[112:115]\n"
"s_add_u32 s78, s96, 8192\n"
"v_mov_b32 v241, 1\n"
"v_mfma_f32_16x16x32_bf16 v[112:115], a[100:103], a[164:167], v[112:115]\n"
"s_mov_b64 exec, 1\n"
"buffer_store_dword v241, off, s[32:35], s96 sc0 sc1\n"
"buffer_store_dword v241, off, s[32:35], s78 sc0 sc1\n"
"s_mov_b64 exec, -1\n"
"v_mfma_f32_16x16x32_bf16 v[116:119], a[104:107], a[160:163], v[116:119]\n"
"s_waitcnt vmcnt(13)\n"
"v_mfma_f32_16x16x32_bf16 v[116:119], a[108:111], a[164:167], v[116:119]\n"
"s_barrier\n"
"v_mfma_f32_16x16x32_bf16 v[120:123], a[112:115], a[160:163], v[120:123]\n"
"s_add_u32 s40, s48, 256\n"
"s_xor_b32 m0, s50, 0xc000\n"
"ds_read_b64_tr_b16 a[232:233], v245 offset:17408\n"
"v_mfma_f32_16x16x32_bf16 v[120:123], a[116:119], a[164:167], v[120:123]\n"
"buffer_load_dwordx4 v224, s[28:31], s40 offen lds\n"
"v_mfma_f32_16x16x32_bf16 v[124:127], a[120:123], a[160:163], v[124:127]\n"
"ds_read_b64_tr_b16 a[234:235], v245 offset:17920\n"
"v_mfma_f32_16x16x32_bf16 v[124:127], a[124:127], a[164:167], v[124:127]\n"
"ds_read_b64_tr_b16 a[236:237], v245 offset:25600\n"
"v_mfma_f32_16x16x32_bf16 v[128:131], a[128:131], a[160:163], v[128:131]\n"
"ds_read_b64_tr_b16 a[238:239], v245 offset:26112\n"
"v_mfma_f32_16x16x32_bf16 v[128:131], a[132:135], a[164:167], v[128:131]\n"
"ds_read_b64_tr_b16 a[224:225], v244 offset:17408\n"
"v_mfma_f32_16x16x32_bf16 v[132:135], a[136:139], a[160:163], v[132:135]\n"
"ds_read_b64_tr_b16 a[226:227], v244 offset:17920\n"
"v_mfma_f32_16x16x32_bf16 v[132:135], a[140:143], a[164:167], v[132:135]\n"
"buffer_load_dwordx4 v225, s[28:31], s40 offen offset:1024 lds\n"
"v_mfma_f32_16x16x32_bf16 v[136:139], a[144:147], a[160:163], v[136:139]\n"
"ds_read_b64_tr_b16 a[192:193], v244 offset:16384\n"
"v_mfma_f32_16x16x32_bf16 v[136:139], a[148:151], a[164:167], v[136:139]\n"
"ds_read_b64_tr_b16 a[194:195], v244 offset:16896\n"
"v_mfma_f32_16x16x32_bf16 v[140:143], a[152:155], a[160:163], v[140:143]\n"
"ds_read_b64_tr_b16 a[196:197], v244 offset:24576\n"
"v_mfma_f32_16x16x32_bf16 v[140:143], a[156:159], a[164:167], v[140:143]\n"
"s_waitcnt lgkmcnt(6)\n"
"ds_read_b64_tr_b16 a[198:199], v244 offset:25088\n"
"v_mfma_f32_16x16x32_bf16 v[220:223], a[136:139], a[184:187], v[220:223]\n"
"ds_read_b64_tr_b16 a[200:201], v245 offset:16384\n"
"v_mfma_f32_16x16x32_bf16 v[220:223], a[140:143], a[188:191], v[220:223]\n"
"buffer_load_dwordx4 v226, s[28:31], s40 offen offset:2048 lds\n"
"v_mfma_f32_16x16x32_bf16 v[216:219], a[128:131], a[184:187], v[216:219]\n"
"ds_read_b64_tr_b16 a[202:203], v245 offset:16896\n"
"v_mfma_f32_16x16x32_bf16 v[216:219], a[132:135], a[188:191], v[216:219]\n"
"ds_read_b64_tr_b16 a[204:205], v245 offset:24576\n"
"v_mfma_f32_16x16x32_bf16 v[212:215], a[104:107], a[184:187], v[212:215]\n"
"ds_read_b64_tr_b16 a[206:207], v245 offset:25088\n"
"v_mfma_f32_16x16x32_bf16 v[212:215], a[108:111], a[188:191], v[212:215]\n"
"v_pk_fma_f32 v[112:113], v[112:113], s[70:71], s[70:71] op_sel:[0,0,1] op_sel_hi:[1,0,1]\nv_pk_fma_f32 v[114:115], v[114:115], s[70:71], s[70:71] op_sel:[0,0,1] op_sel_hi:[1,0,1]\nv_pk_fma_f32 v[116:117], v[116:117], s[70:71], s[70:71] op_sel:[0,0,1] op_sel_hi:[1,0,1]\nv_pk_fma_f32 v[118:119], v[118:119], s[70:71], s[70:71] op_sel:[0,0,1] op_sel_hi:[1,0,1]\nv_pk_fma_f32 v[120:121], v[120:121], s[70:71], s[70:71] op_sel:[0,0,1] op_sel_hi:[1,0,1]\nv_pk_fma_f32 v[122:123], v[122:123], s[70:71], s[70:71] op_sel:[0,0,1] op_sel_hi:[1,0,1]\nv_pk_fma_f32 v[124:125], v[124:125], s[70:71], s[70:71] op_sel:[0,0,1] op_sel_hi:[1,0,1]\nv_pk_fma_f32 v[126:127], v[126:127], s[70:71], s[70:71] op_sel:[0,0,1] op_sel_hi:[1,0,1]\nv_pk_fma_f32 v[128:129], v[128:129], s[70:71], s[70:71] op_sel:[0,0,1] op_sel_hi:[1,0,1]\nv_pk_fma_f32 v[130:131], v[130:131], s[70:71], s[70:71] op_sel:[0,0,1] op_sel_hi:[1,0,1]\nv_pk_fma_f32 v[132:133], v[132:133], s[70:71], s[70:71] op_sel:[0,0,1] op_sel_hi:[1,0,1]\nv_pk_fma_f32 v[134:135], v[134:135], s[70:71], s[70:71] op_sel:[0,0,1] op_sel_hi:[1,0,1]\nv_pk_fma_f32 v[136:137], v[136:137], s[70:71], s[70:71] op_sel:[0,0,1] op_sel_hi:[1,0,1]\nv_pk_fma_f32 v[138:139], v[138:139], s[70:71], s[70:71] op_sel:[0,0,1] op_sel_hi:[1,0,1]\nv_pk_fma_f32 v[140:141], v[140:141], s[70:71], s[70:71] op_sel:[0,0,1] op_sel_hi:[1,0,1]\nv_pk_fma_f32 v[142:143], v[142:143], s[70:71], s[70:71] op_sel:[0,0,1] op_sel_hi:[1,0,1]\n"
"v_mfma_f32_16x16x32_bf16 v[208:211], a[96:99], a[184:187], v[208:211]\n"
"ds_read_b64_tr_b16 a[208:209], v246 offset:16384\n"
"v_cvt_pk_bf16_f32 v112, v112, v113\n"
"v_mfma_f32_16x16x32_bf16 v[208:211], a[100:103], a[188:191], v[208:211]\n"
"buffer_load_dwordx4 v227, s[28:31], s40 offen offset:3072 lds\n"
"v_mfma_f32_16x16x32_bf16 v[172:175], a[152:155], a[168:171], v[172:175]\n"
"s_cmp_eq_u32 s47, 8\n"
"ds_read_b64_tr_b16 a[210:211], v246 offset:16896\n"
"v_cvt_pk_bf16_f32 v113, v114, v115\n"
"v_mfma_f32_16x16x32_bf16 v[172:175], a[156:159], a[172:175], v[172:175]\n"
"s_cselect_b32 s41, s52, s58\n"
"ds_read_b64_tr_b16 a[212:213], v246 offset:24576\n"
"v_cvt_pk_bf16_f32 v114, v116, v117\n"
"v_mfma_f32_16x16x32_bf16 v[168:171], a[144:147], a[168:171], v[168:171]\n"
"s_add_u32 s48, s48, s41\n"
"ds_read_b64_tr_b16 a[214:215], v246 offset:25088\n"
"v_cvt_pk_bf16_f32 v115, v118, v119\n"
"v_mfma_f32_16x16x32_bf16 v[168:171], a[148:151], a[172:175], v[168:171]\n"
"s_sub_u32 s41, s48, 5242880\n"
"ds_read_b64_tr_b16 a[216:217], v247 offset:16384\n"
"v_cvt_pk_bf16_f32 v120, v120, v121\n"
"v_mfma_f32_16x16x32_bf16 v[164:167], a[136:139], a[168:171], v[164:167]\n"
"s_min_u32 s48, s48, s41\n"
"ds_read_b64_tr_b16 a[218:219], v247 offset:16896\n"
"v_cvt_pk_bf16_f32 v121, v122, v123\n"
"v_mfma_f32_16x16x32_bf16 v[164:167], a[140:143], a[172:175], v[164:167]\n"
"ds_read_b64_tr_b16 a[220:221], v247 offset:24576\n"
"v_xor_b32 v250, s74, v250\n"
"v_mfma_f32_16x16x32_bf16 v[160:163], a[128:131], a[168:171], v[160:163]\n"
"s_mov_b32 m0, s50\n"
"ds_read_b64_tr_b16 a[222:223], v247 offset:25088\n"
"v_xor_b32 v251, s74, v251\n"
"v_mfma_f32_16x16x32_bf16 v[160:163], a[132:135], a[172:175], v[160:163]\n"
"buffer_load_dwordx4 v224, s[28:31], s48 offen lds\n"
"v_mfma_f32_16x16x32_bf16 v[156:159], a[120:123], a[168:171], v[156:159]\n"
"ds_read_b64_tr_b16 a[228:229], v244 offset:25600\n"
"v_xor_b32 v245, s74, v245\n"
"v_mfma_f32_16x16x32_bf16 v[156:159], a[124:127], a[172:175], v[156:159]\n"
"s_mov_b64 s[80:81], s[8:9]\n"
"ds_read_b64_tr_b16 a[230:231], v244 offset:26112\n"
"v_xor_b32 v244, s74, v244\n"
"v_mfma_f32_16x16x32_bf16 v[152:155], a[112:115], a[168:171], v[152:155]\n"
"s_mov_b32 s82, s68\n"
"ds_read_b64_tr_b16 a[240:241], v246 offset:17408\n"
"v_cvt_pk_bf16_f32 v122, v124, v125\n"
"v_mfma_f32_16x16x32_bf16 v[152:155], a[116:119], a[172:175], v[152:155]\n"
"s_mov_b32 s83, 131072\n"
"buffer_load_dwordx4 v225, s[28:31], s48 offen offset:1024 lds\n"
"v_mfma_f32_16x16x32_bf16 v[148:151], a[104:107], a[168:171], v[148:151]\n"
"v_and_b32 v241, 0x3f0, v242\n"
"ds_read_b64_tr_b16 a[242:243], v246 offset:17920\n"
"v_cvt_pk_bf16_f32 v123, v126, v127\n"
"v_mfma_f32_16x16x32_bf16 v[148:151], a[108:111], a[172:175], v[148:151]\n"
"v_mul_u32_u24 v241, 10, v241\n"
"ds_read_b64_tr_b16 a[244:245], v246 offset:25600\n"
"v_cvt_pk_bf16_f32 v128, v128, v129\n"
"v_mfma_f32_16x16x32_bf16 v[144:147], a[96:99], a[168:171], v[144:147]\n"
"ds_read_b64_tr_b16 a[246:247], v246 offset:26112\n"
"v_xor_b32 v246, s74, v246\n"
"v_mfma_f32_16x16x32_bf16 v[144:147], a[100:103], a[172:175], v[144:147]\n"
"ds_read_b64_tr_b16 a[248:249], v247 offset:17408\n"
"v_cvt_pk_bf16_f32 v129, v130, v131\n"
"v_mfma_f32_16x16x32_bf16 v[176:179], a[96:99], a[176:179], v[176:179]\n"
"ds_read_b64_tr_b16 a[250:251], v247 offset:17920\n"
"v_cvt_pk_bf16_f32 v130, v132, v133\n"
"v_mfma_f32_16x16x32_bf16 v[176:179], a[100:103], a[180:183], v[176:179]\n"
"buffer_load_dwordx4 v226, s[28:31], s48 offen offset:2048 lds\n"
"v_mfma_f32_16x16x32_bf16 v[180:183], a[104:107], a[176:179], v[180:183]\n"
"ds_read_b64_tr_b16 a[252:253], v247 offset:25600\n"
"v_cvt_pk_bf16_f32 v131, v134, v135\n"
"v_mfma_f32_16x16x32_bf16 v[180:183], a[108:111], a[180:183], v[180:183]\n"
"ds_read_b64_tr_b16 a[254:255], v247 offset:26112\n"
"v_xor_b32 v247, s74, v247\n"
"v_mfma_f32_16x16x32_bf16 v[184:187], a[112:115], a[176:179], v[184:187]\n"
"v_permlane16_swap_b32 v112, v114\n"
"v_mfma_f32_16x16x32_bf16 v[184:187], a[116:119], a[180:183], v[184:187]\n"
"v_permlane16_swap_b32 v113, v115\n"
"v_mfma_f32_16x16x32_bf16 v[188:191], a[120:123], a[176:179], v[188:191]\n"
"v_permlane16_swap_b32 v120, v122\n"
"v_mfma_f32_16x16x32_bf16 v[188:191], a[124:127], a[180:183], v[188:191]\n"
"buffer_load_dwordx4 v227, s[28:31], s48 offen offset:3072 lds\n"
"v_mfma_f32_16x16x32_bf16 v[200:203], a[144:147], a[176:179], v[200:203]\n"
"s_xor_b32 s50, s50, s74\n"
"v_permlane16_swap_b32 v121, v123\n"
"v_mfma_f32_16x16x32_bf16 v[200:203], a[148:151], a[180:183], v[200:203]\n"
"v_permlane16_swap_b32 v128, v130\n"
"v_mfma_f32_16x16x32_bf16 v[204:207], a[152:155], a[176:179], v[204:207]\n"
"v_permlane16_swap_b32 v129, v131\n"
"v_mfma_f32_16x16x32_bf16 v[204:207], a[156:159], a[180:183], v[204:207]\n"
"v_pk_fma_f32 v[144:145], v[144:145], s[70:71], s[70:71] op_sel:[0,0,1] op_sel_hi:[1,0,1]\nv_pk_fma_f32 v[146:147], v[146:147], s[70:71], s[70:71] op_sel:[0,0,1] op_sel_hi:[1,0,1]\nv_pk_fma_f32 v[148:149], v[148:149], s[70:71], s[70:71] op_sel:[0,0,1] op_sel_hi:[1,0,1]\nv_pk_fma_f32 v[150:151], v[150:151], s[70:71], s[70:71] op_sel:[0,0,1] op_sel_hi:[1,0,1]\nv_pk_fma_f32 v[152:153], v[152:153], s[70:71], s[70:71] op_sel:[0,0,1] op_sel_hi:[1,0,1]\nv_pk_fma_f32 v[154:155], v[154:155], s[70:71], s[70:71] op_sel:[0,0,1] op_sel_hi:[1,0,1]\nv_pk_fma_f32 v[156:157], v[156:157], s[70:71], s[70:71] op_sel:[0,0,1] op_sel_hi:[1,0,1]\nv_pk_fma_f32 v[158:159], v[158:159], s[70:71], s[70:71] op_sel:[0,0,1] op_sel_hi:[1,0,1]\nv_pk_fma_f32 v[160:161], v[160:161], s[70:71], s[70:71] op_sel:[0,0,1] op_sel_hi:[1,0,1]\nv_pk_fma_f32 v[162:163], v[162:163], s[70:71], s[70:71] op_sel:[0,0,1] op_sel_hi:[1,0,1]\nv_pk_fma_f32 v[164:165], v[164:165], s[70:71], s[70:71] op_sel:[0,0,1] op_sel_hi:[1,0,1]\nv_pk_fma_f32 v[166:167], v[166:167], s[70:71], s[70:71] op_sel:[0,0,1] op_sel_hi:[1,0,1]\nv_pk_fma_f32 v[168:169], v[168:169], s[70:71], s[70:71] op_sel:[0,0,1] op_sel_hi:[1,0,1]\nv_pk_fma_f32 v[170:171], v[170:171], s[70:71], s[70:71] op_sel:[0,0,1] op_sel_hi:[1,0,1]\nv_pk_fma_f32 v[172:173], v[172:173], s[70:71], s[70:71] op_sel:[0,0,1] op_sel_hi:[1,0,1]\nv_pk_fma_f32 v[174:175], v[174:175], s[70:71], s[70:71] op_sel:[0,0,1] op_sel_hi:[1,0,1]\n"
"v_mfma_f32_16x16x32_bf16 v[192:195], a[128:131], a[176:179], v[192:195]\n"
"v_pk_fma_f32 v[208:209], v[208:209], s[70:71], s[70:71] op_sel:[0,0,1] op_sel_hi:[1,0,1]\nv_pk_fma_f32 v[210:211], v[210:211], s[70:71], s[70:71] op_sel:[0,0,1] op_sel_hi:[1,0,1]\nv_pk_fma_f32 v[212:213], v[212:213], s[70:71], s[70:71] op_sel:[0,0,1] op_sel_hi:[1,0,1]\nv_pk_fma_f32 v[214:215], v[214:215], s[70:71], s[70:71] op_sel:[0,0,1] op_sel_hi:[1,0,1]\nv_pk_fma_f32 v[216:217], v[216:217], s[70:71], s[70:71] op_sel:[0,0,1] op_sel_hi:[1,0,1]\nv_pk_fma_f32 v[218:219], v[218:219], s[70:71], s[70:71] op_sel:[0,0,1] op_sel_hi:[1,0,1]\nv_pk_fma_f32 v[220:221], v[220:221], s[70:71], s[70:71] op_sel:[0,0,1] op_sel_hi:[1,0,1]\nv_pk_fma_f32 v[222:223], v[222:223], s[70:71], s[70:71] op_sel:[0,0,1] op_sel_hi:[1,0,1]\n"
"v_mfma_f32_16x16x32_bf16 v[192:195], a[132:135], a[180:183], v[192:195]\n"
"v_accvgpr_write_b32 a0, v112\n"
"v_accvgpr_write_b32 a1, v113\n"
"v_mfma_f32_16x16x32_bf16 v[196:199], a[136:139], a[176:179], v[196:199]\n"
"v_accvgpr_write_b32 a2, v114\n"
"v_accvgpr_write_b32 a3, v115\n"
"v_mfma_f32_16x16x32_bf16 v[196:199], a[140:143], a[180:183], v[196:199]\n"
"v_accvgpr_write_b32 a4, v120\n"
"v_accvgpr_write_b32 a5, v121\n"
"v_mfma_f32_16x16x32_bf16 v[108:111], a[232:235], a[184:187], v[108:111]\n"
"s_waitcnt lgkmcnt(0)\n"
"v_accvgpr_write_b32 a6, v122\n"
"v_accvgpr_write_b32 a7, v123\n"
"v_mfma_f32_16x16x32_bf16 v[108:111], a[236:239], a[188:191], v[108:111]\n"
"v_accvgpr_write_b32 a8, v128\n"
"v_accvgpr_write_b32 a9, v129\n"
"v_mfma_f32_16x16x32_bf16 v[104:107], a[224:227], a[184:187], v[104:107]\n"
"s_waitcnt vmcnt(10)\n"
"v_accvgpr_write_b32 a10, v130\n"
"v_accvgpr_write_b32 a11, v131\n"
"v_mfma_f32_16x16x32_bf16 v[104:107], a[228:231], a[188:191], v[104:107]\n"
"s_barrier\n"
"v_mfma_f32_16x16x32_bf16 v[100:103], a[200:203], a[184:187], v[100:103]\n"
"s_mov_b32 m0, s51\n"
"ds_read_b64_tr_b16 a[96:97], v244 offset:0\n"
"v_cvt_pk_bf16_f32 v136, v136, v137\n"
"v_mfma_f32_16x16x32_bf16 v[100:103], a[204:207], a[188:191], v[100:103]\n"
"buffer_load_dwordx4 v248, s[24:27], s49 offen lds\n"
"v_mfma_f32_16x16x32_bf16 v[96:99], a[192:195], a[184:187], v[96:99]\n"
"ds_read_b64_tr_b16 a[98:99], v244 offset:512\n"
"v_cvt_pk_bf16_f32 v137, v138, v139\n"
"v_mfma_f32_16x16x32_bf16 v[96:99], a[196:199], a[188:191], v[96:99]\n"
"ds_read_b64_tr_b16 a[100:101], v244 offset:8192\n"
"v_cvt_pk_bf16_f32 v138, v140, v141\n"
"v_mfma_f32_16x16x32_bf16 v[0:3], a[192:195], a[160:163], v[0:3]\n"
"ds_read_b64_tr_b16 a[102:103], v244 offset:8704\n"
"v_cvt_pk_bf16_f32 v139, v142, v143\n"
"v_mfma_f32_16x16x32_bf16 v[0:3], a[196:199], a[164:167], v[0:3]\n"
"ds_read_b64_tr_b16 a[104:105], v245 offset:0\n"
"v_cvt_pk_bf16_f32 v168, v168, v169\n"
"v_mfma_f32_16x16x32_bf16 v[4:7], a[200:203], a[160:163], v[4:7]\n"
"ds_read_b64_tr_b16 a[106:107], v245 offset:512\n"
"v_cvt_pk_bf16_f32 v169, v170, v171\n"
"v_mfma_f32_16x16x32_bf16 v[4:7], a[204:207], a[164:167], v[4:7]\n"
"buffer_load_dwordx4 v249, s[24:27], s49 offen offset:1024 lds\n"
"v_mfma_f32_16x16x32_bf16 v[8:11], a[208:211], a[160:163], v[8:11]\n"
"ds_read_b64_tr_b16 a[108:109], v245 offset:8192\n"
"v_cvt_pk_bf16_f32 v170, v172, v173\n"
"v_mfma_f32_16x16x32_bf16 v[8:11], a[212:215], a[164:167], v[8:11]\n"
"ds_read_b64_tr_b16 a[110:111], v245 offset:8704\n"
"v_cvt_pk_bf16_f32 v171, v174, v175\n"
"v_mfma_f32_16x16x32_bf16 v[12:15], a[216:219], a[160:163], v[12:15]\n"
"ds_read_b64_tr_b16 a[112:113], v246 offset:0\n"
"v_cvt_pk_bf16_f32 v160, v160, v161\n"
"v_mfma_f32_16x16x32_bf16 v[12:15], a[220:223], a[164:167], v[12:15]\n"
"buffer_load_dwordx4 v254, s[24:27], s49 offen offset:2048 lds\n"
"v_mfma_f32_16x16x32_bf16 v[16:19], a[224:227], a[160:163], v[16:19]\n"
"ds_read_b64_tr_b16 a[114:115], v246 offset:512\n"
"v_cvt_pk_bf16_f32 v161, v162, v163\n"
"v_mfma_f32_16x16x32_bf16 v[16:19], a[228:231], a[164:167], v[16:19]\n"
"ds_read_b64_tr_b16 a[116:117], v246 offset:8192\n"
"v_cvt_pk_bf16_f32 v162, v164, v165\n"
"v_mfma_f32_16x16x32_bf16 v[20:23], a[232:235], a[160:163], v[20:23]\n"
"ds_read_b64_tr_b16 a[118:119], v246 offset:8704\n"
"v_cvt_pk_bf16_f32 v163, v166, v167\n"
"v_mfma_f32_16x16x32_bf16 v[20:23], a[236:239], a[164:167], v[20:23]\n"
"ds_read_b64_tr_b16 a[120:121], v247 offset:0\n"
"v_cvt_pk_bf16_f32 v152, v152, v153\n"
"v_mfma_f32_16x16x32_bf16 v[24:27], a[240:243], a[160:163], v[24:27]\n"
"ds_read_b64_tr_b16 a[122:123], v247 offset:512\n"
"v_cvt_pk_bf16_f32 v153, v154, v155\n"
"v_mfma_f32_16x16x32_bf16 v[24:27], a[244:247], a[164:167], v[24:27]\n"
"buffer_load_dwordx4 v255, s[24:27], s49 offen offset:3072 lds\n"
"v_mfma_f32_16x16x32_bf16 v[28:31], a[248:251], a[160:163], v[28:31]\n"
"ds_read_b128 a[160:163], v250 offset:0\n"
"v_cvt_pk_bf16_f32 v154, v156, v157\n"
"v_mfma_f32_16x16x32_bf16 v[28:31], a[252:255], a[164:167], v[28:31]\n"
"ds_read_b128 a[164:167], v251 offset:0\n"
"v_cvt_pk_bf16_f32 v155, v158, v159\n"
"v_mfma_f32_16x16x32_bf16 v[60:63], a[248:251], a[168:171], v[60:63]\n"
"ds_read_b64_tr_b16 a[124:125], v247 offset:8192\n"
"v_cvt_pk_bf16_f32 v144, v144, v145\n"
"v_mfma_f32_16x16x32_bf16 v[60:63], a[252:255], a[172:175], v[60:63]\n"
"s_add_u32 s40, s49, 40960\n"
"ds_read_b64_tr_b16 a[126:127], v247 offset:8704\n"
"v_cvt_pk_bf16_f32 v145, v146, v147\n"
"v_mfma_f32_16x16x32_bf16 v[56:59], a[240:243], a[168:171], v[56:59]\n"
"s_add_u32 m0, s51, 4096\n"
"ds_read_b64_tr_b16 a[128:129], v244 offset:1024\n"
"v_cvt_pk_bf16_f32 v146, v148, v149\n"
"v_mfma_f32_16x16x32_bf16 v[56:59], a[244:247], a[172:175], v[56:59]\n"
"buffer_load_dwordx4 v248, s[24:27], s40 offen lds\n"
"v_mfma_f32_16x16x32_bf16 v[52:55], a[232:235], a[168:171], v[52:55]\n"
"ds_read_b64_tr_b16 a[130:131], v244 offset:1536\n"
"v_cvt_pk_bf16_f32 v147, v150, v151\n"
"v_mfma_f32_16x16x32_bf16 v[52:55], a[236:239], a[172:175], v[52:55]\n"
"ds_read_b64_tr_b16 a[132:133], v244 offset:9216\n"
"v_xor_b32 v252, s74, v252\n"
"v_mfma_f32_16x16x32_bf16 v[48:51], a[224:227], a[168:171], v[48:51]\n"
"ds_read_b64_tr_b16 a[134:135], v244 offset:9728\n"
"v_cvt_pk_bf16_f32 v216, v216, v217\n"
"v_mfma_f32_16x16x32_bf16 v[48:51], a[228:231], a[172:175], v[48:51]\n"
"ds_read_b64_tr_b16 a[136:137], v245 offset:1024\n"
"v_cvt_pk_bf16_f32 v217, v218, v219\n"
"v_mfma_f32_16x16x32_bf16 v[44:47], a[216:219], a[168:171], v[44:47]\n"
"ds_read_b64_tr_b16 a[138:139], v245 offset:1536\n"
"v_cvt_pk_bf16_f32 v218, v220, v221\n"
"v_mfma_f32_16x16x32_bf16 v[44:47], a[220:223], a[172:175], v[44:47]\n"
"buffer_load_dwordx4 v249, s[24:27], s40 offen offset:1024 lds\n"
"v_mfma_f32_16x16x32_bf16 v[40:43], a[208:211], a[168:171], v[40:43]\n"
"ds_read_b64_tr_b16 a[140:141], v245 offset:9216\n"
"v_cvt_pk_bf16_f32 v219, v222, v223\n"
"v_mfma_f32_16x16x32_bf16 v[40:43], a[212:215], a[172:175], v[40:43]\n"
"ds_read_b64_tr_b16 a[142:143], v245 offset:9728\n"
"v_xor_b32 v253, s74, v253\n"
"v_mfma_f32_16x16x32_bf16 v[36:39], a[200:203], a[168:171], v[36:39]\n"
"ds_read_b64_tr_b16 a[144:145], v246 offset:1024\n"
"v_cvt_pk_bf16_f32 v208, v208, v209\n"
"v_mfma_f32_16x16x32_bf16 v[36:39], a[204:207], a[172:175], v[36:39]\n"
"buffer_load_dwordx4 v254, s[24:27], s40 offen offset:2048 lds\n"
"v_mfma_f32_16x16x32_bf16 v[32:35], a[192:195], a[168:171], v[32:35]\n"
"s_add_u32 s49, s49, 128\n"
"ds_read_b64_tr_b16 a[146:147], v246 offset:1536\n"
"v_cvt_pk_bf16_f32 v209, v210, v211\n"
"v_mfma_f32_16x16x32_bf16 v[32:35], a[196:199], a[172:175], v[32:35]\n"
"s_sub_u32 s41, s49, 1280\n"
"ds_read_b64_tr_b16 a[148:149], v246 offset:9216\n"
"v_cvt_pk_bf16_f32 v210, v212, v213\n"
"v_mfma_f32_16x16x32_bf16 v[64:67], a[192:195], a[176:179], v[64:67]\n"
"s_min_u32 s49, s49, s41\n"
"ds_read_b64_tr_b16 a[150:151], v246 offset:9728\n"
"v_cvt_pk_bf16_f32 v211, v214, v215\n"
"v_mfma_f32_16x16x32_bf16 v[64:67], a[196:199], a[180:183], v[64:67]\n"
"s_xor_b32 s51, s51, s74\n"
"v_permlane16_swap_b32 v136, v138\n"
"v_mfma_f32_16x16x32_bf16 v[68:71], a[200:203], a[176:179], v[68:71]\n"
"s_mov_b32 m0, 139264\n"
"ds_read_b64_tr_b16 a[152:153], v247 offset:1024\n"
"v_accvgpr_write_b32 a12, v136\n"
"v_mfma_f32_16x16x32_bf16 v[68:71], a[204:207], a[180:183], v[68:71]\n"
"buffer_load_dword v241, s[80:83], s63 offen lds\n"
"v_mfma_f32_16x16x32_bf16 v[72:75], a[208:211], a[176:179], v[72:75]\n"
"ds_read_b64_tr_b16 a[154:155], v247 offset:1536\n"
"v_accvgpr_write_b32 a14, v138\n"
"v_mfma_f32_16x16x32_bf16 v[72:75], a[212:215], a[180:183], v[72:75]\n"
"v_permlane16_swap_b32 v137, v139\n"
"v_mfma_f32_16x16x32_bf16 v[76:79], a[216:219], a[176:179], v[76:79]\n"
"ds_read_b64_tr_b16 a[156:157], v247 offset:9216\n"
"v_accvgpr_write_b32 a13, v137\n"
"v_mfma_f32_16x16x32_bf16 v[76:79], a[220:223], a[180:183], v[76:79]\n"
"ds_read_b64_tr_b16 a[158:159], v247 offset:9728\n"
"v_accvgpr_write_b32 a15, v139\n"
"v_mfma_f32_16x16x32_bf16 v[88:91], a[240:243], a[176:179], v[88:91]\n"
"v_permlane16_swap_b32 v168, v170\n"
"v_mfma_f32_16x16x32_bf16 v[88:91], a[244:247], a[180:183], v[88:91]\n"
"buffer_load_dword v241, s[80:83], s63 offen offset:80 lds\n"
"v_mfma_f32_16x16x32_bf16 v[92:95], a[248:251], a[176:179], v[92:95]\n"
"ds_read_b128 a[168:171], v250 offset:2048\n"
"v_accvgpr_write_b32 a28, v168\n"
"v_mfma_f32_16x16x32_bf16 v[92:95], a[252:255], a[180:183], v[92:95]\n"
"ds_read_b128 a[172:175], v251 offset:2048\n"
"v_accvgpr_write_b32 a30, v170\n"
"v_mfma_f32_16x16x32_bf16 v[80:83], a[224:227], a[176:179], v[80:83]\n"
"v_permlane16_swap_b32 v169, v171\n"
"v_mfma_f32_16x16x32_bf16 v[80:83], a[228:231], a[180:183], v[80:83]\n"
"ds_read_b128 a[184:187], v252 offset:0\n"
"v_accvgpr_write_b32 a29, v169\n"
"v_mfma_f32_16x16x32_bf16 v[84:87], a[232:235], a[176:179], v[84:87]\n"
"s_add_u32 s47, s47, 1\n"
"ds_read_b128 a[176:179], v250 offset:4096\n"
"v_accvgpr_write_b32 a31, v171\n"
"v_mfma_f32_16x16x32_bf16 v[84:87], a[236:239], a[180:183], v[84:87]\n"
"s_waitcnt lgkmcnt(4)\n"
"v_permlane16_swap_b32 v160, v162\n"
"v_mfma_f32_16x16x32_bf16 v[112:115], a[96:99], a[160:163], v[228:231]\n"
"ds_read_b128 a[180:183], v251 offset:4096\n"
"v_accvgpr_write_b32 a24, v160\n"
"v_mfma_f32_16x16x32_bf16 v[112:115], a[100:103], a[164:167], v[112:115]\n"
"ds_read_b128 a[188:191], v253 offset:0\n"
"v_accvgpr_write_b32 a26, v162\n"
"v_mfma_f32_16x16x32_bf16 v[116:119], a[104:107], a[160:163], v[228:231]\n"
"s_waitcnt vmcnt(13)\n"
"v_permlane16_swap_b32 v161, v163\n"
"v_mfma_f32_16x16x32_bf16 v[116:119], a[108:111], a[164:167], v[116:119]\n"
"s_barrier\n"
"v_mfma_f32_16x16x32_bf16 v[120:123], a[112:115], a[160:163], v[228:231]\n"
"s_add_u32 s40, s48, 256\n"
"s_xor_b32 m0, s50, 0xc000\n"
"ds_read_b64_tr_b16 a[192:193], v244 offset:16384\n"
"v_mfma_f32_16x16x32_bf16 v[120:123], a[116:119], a[164:167], v[120:123]\n"
"buffer_load_dwordx4 v224, s[28:31], s40 offen lds\n"
"v_mfma_f32_16x16x32_bf16 v[124:127], a[120:123], a[160:163], v[228:231]\n"
"ds_read_b64_tr_b16 a[194:195], v244 offset:16896\n"
"v_accvgpr_write_b32 a25, v161\n"
"v_mfma_f32_16x16x32_bf16 v[124:127], a[124:127], a[164:167], v[124:127]\n"
"ds_read_b64_tr_b16 a[196:197], v244 offset:24576\n"
"v_accvgpr_write_b32 a27, v163\n"
"v_mfma_f32_16x16x32_bf16 v[128:131], a[128:131], a[160:163], v[228:231]\n"
"v_permlane16_swap_b32 v152, v154\n"
"v_mfma_f32_16x16x32_bf16 v[128:131], a[132:135], a[164:167], v[128:131]\n"
"ds_read_b64_tr_b16 a[198:199], v244 offset:25088\n"
"v_accvgpr_write_b32 a20, v152\n"
"v_mfma_f32_16x16x32_bf16 v[132:135], a[136:139], a[160:163], v[228:231]\n"
"ds_read_b64_tr_b16 a[200:201], v245 offset:16384\n"
"v_accvgpr_write_b32 a22, v154\n"
"v_mfma_f32_16x16x32_bf16 v[132:135], a[140:143], a[164:167], v[132:135]\n"
"buffer_load_dwordx4 v225, s[28:31], s40 offen offset:1024 lds\n"
"v_mfma_f32_16x16x32_bf16 v[136:139], a[144:147], a[160:163], v[228:231]\n"
"v_permlane16_swap_b32 v153, v155\n"
"v_mfma_f32_16x16x32_bf16 v[136:139], a[148:151], a[164:167], v[136:139]\n"
"ds_read_b64_tr_b16 a[202:203], v245 offset:16896\n"
"v_accvgpr_write_b32 a21, v153\n"
"v_mfma_f32_16x16x32_bf16 v[140:143], a[152:155], a[160:163], v[228:231]\n"
"ds_read_b64_tr_b16 a[204:205], v245 offset:24576\n"
"v_accvgpr_write_b32 a23, v155\n"
"v_mfma_f32_16x16x32_bf16 v[140:143], a[156:159], a[164:167], v[140:143]\n"
"s_waitcnt lgkmcnt(4)\n"
"v_permlane16_swap_b32 v144, v146\n"
"v_mfma_f32_16x16x32_bf16 v[172:175], a[152:155], a[168:171], v[228:231]\n"
"ds_read_b64_tr_b16 a[206:207], v245 offset:25088\n"
"v_accvgpr_write_b32 a16, v144\n"
"v_mfma_f32_16x16x32_bf16 v[172:175], a[156:159], a[172:175], v[172:175]\n"
"buffer_load_dwordx4 v226, s[28:31], s40 offen offset:2048 lds\n"
"v_mfma_f32_16x16x32_bf16 v[168:171], a[144:147], a[168:171], v[228:231]\n"
"ds_read_b64_tr_b16 a[208:209], v246 offset:16384\n"
"v_accvgpr_write_b32 a18, v146\n"
"v_mfma_f32_16x16x32_bf16 v[168:171], a[148:151], a[172:175], v[168:171]\n"
"v_permlane16_swap_b32 v145, v147\n"
"v_mfma_f32_16x16x32_bf16 v[164:167], a[136:139], a[168:171], v[228:231]\n"
"ds_read_b64_tr_b16 a[210:211], v246 offset:16896\n"
"v_accvgpr_write_b32 a17, v145\n"
"v_mfma_f32_16x16x32_bf16 v[164:167], a[140:143], a[172:175], v[164:167]\n"
"ds_read_b64_tr_b16 a[212:213], v246 offset:24576\n"
"v_accvgpr_write_b32 a19, v147\n"
"v_mfma_f32_16x16x32_bf16 v[160:163], a[128:131], a[168:171], v[228:231]\n"
"v_pk_fma_f32 v[176:177], v[176:177], s[70:71], s[70:71] op_sel:[0,0,1] op_sel_hi:[1,0,1]\nv_pk_fma_f32 v[178:179], v[178:179], s[70:71], s[70:71] op_sel:[0,0,1] op_sel_hi:[1,0,1]\nv_pk_fma_f32 v[180:181], v[180:181], s[70:71], s[70:71] op_sel:[0,0,1] op_sel_hi:[1,0,1]\nv_pk_fma_f32 v[182:183], v[182:183], s[70:71], s[70:71] op_sel:[0,0,1] op_sel_hi:[1,0,1]\nv_pk_fma_f32 v[184:185], v[184:185], s[70:71], s[70:71] op_sel:[0,0,1] op_sel_hi:[1,0,1]\nv_pk_fma_f32 v[186:187], v[186:187], s[70:71], s[70:71] op_sel:[0,0,1] op_sel_hi:[1,0,1]\nv_pk_fma_f32 v[188:189], v[188:189], s[70:71], s[70:71] op_sel:[0,0,1] op_sel_hi:[1,0,1]\nv_pk_fma_f32 v[190:191], v[190:191], s[70:71], s[70:71] op_sel:[0,0,1] op_sel_hi:[1,0,1]\nv_pk_fma_f32 v[192:193], v[192:193], s[70:71], s[70:71] op_sel:[0,0,1] op_sel_hi:[1,0,1]\nv_pk_fma_f32 v[194:195], v[194:195], s[70:71], s[70:71] op_sel:[0,0,1] op_sel_hi:[1,0,1]\nv_pk_fma_f32 v[196:197], v[196:197], s[70:71], s[70:71] op_sel:[0,0,1] op_sel_hi:[1,0,1]\nv_pk_fma_f32 v[198:199], v[198:199], s[70:71], s[70:71] op_sel:[0,0,1] op_sel_hi:[1,0,1]\nv_pk_fma_f32 v[200:201], v[200:201], s[70:71], s[70:71] op_sel:[0,0,1] op_sel_hi:[1,0,1]\nv_pk_fma_f32 v[202:203], v[202:203], s[70:71], s[70:71] op_sel:[0,0,1] op_sel_hi:[1,0,1]\nv_pk_fma_f32 v[204:205], v[204:205], s[70:71], s[70:71] op_sel:[0,0,1] op_sel_hi:[1,0,1]\nv_pk_fma_f32 v[206:207], v[206:207], s[70:71], s[70:71] op_sel:[0,0,1] op_sel_hi:[1,0,1]\n"
"v_mfma_f32_16x16x32_bf16 v[160:163], a[132:135], a[172:175], v[160:163]\n"
"buffer_load_dwordx4 v227, s[28:31], s40 offen offset:3072 lds\n"
"v_mfma_f32_16x16x32_bf16 v[156:159], a[120:123], a[168:171], v[228:231]\n"
"s_cmp_eq_u32 s47, 8\n"
"ds_read_b64_tr_b16 a[214:215], v246 offset:25088\n"
"v_cvt_pk_bf16_f32 v176, v176, v177\n"
"v_mfma_f32_16x16x32_bf16 v[156:159], a[124:127], a[172:175], v[156:159]\n"
"s_cselect_b32 s41, s52, s58\n"
"ds_read_b64_tr_b16 a[216:217], v247 offset:16384\n"
"v_cvt_pk_bf16_f32 v177, v178, v179\n"
"v_mfma_f32_16x16x32_bf16 v[152:155], a[112:115], a[168:171], v[228:231]\n"
"s_add_u32 s48, s48, s41\n"
"ds_read_b64_tr_b16 a[218:219], v247 offset:16896\n"
"v_cvt_pk_bf16_f32 v178, v180, v181\n"
"v_mfma_f32_16x16x32_bf16 v[152:155], a[116:119], a[172:175], v[152:155]\n"
"s_sub_u32 s41, s48, 5242880\n"
"ds_read_b64_tr_b16 a[220:221], v247 offset:24576\n"
"v_cvt_pk_bf16_f32 v179, v182, v183\n"
"v_mfma_f32_16x16x32_bf16 v[148:151], a[104:107], a[168:171], v[228:231]\n"
"s_min_u32 s48, s48, s41\n"
"v_permlane16_swap_b32 v176, v178\n"
"v_mfma_f32_16x16x32_bf16 v[148:151], a[108:111], a[172:175], v[148:151]\n"
"v_permlane16_swap_b32 v177, v179\n"
"v_mfma_f32_16x16x32_bf16 v[144:147], a[96:99], a[168:171], v[228:231]\n"
"s_mov_b32 m0, s50\n"
"v_accvgpr_write_b32 a32, v176\n"
"v_accvgpr_write_b32 a33, v177\n"
"v_mfma_f32_16x16x32_bf16 v[144:147], a[100:103], a[172:175], v[144:147]\n"
"buffer_load_dwordx4 v224, s[28:31], s48 offen lds\n"
"v_accvgpr_write_b32 a34, v178\n"
"v_accvgpr_write_b32 a35, v179\n"
"v_mfma_f32_16x16x32_bf16 v[176:179], a[96:99], a[176:179], v[228:231]\n"
"v_permlane16_swap_b32 v216, v218\n"
"v_permlane16_swap_b32 v217, v219\n"
"v_mfma_f32_16x16x32_bf16 v[176:179], a[100:103], a[180:183], v[176:179]\n"
"ds_write_b128 v243, v[216:219] offset:2048\n"
"v_mfma_f32_16x16x32_bf16 v[180:183], a[104:107], a[176:179], v[228:231]\n"
"v_cvt_pk_bf16_f32 v184, v184, v185\n"
"v_cvt_pk_bf16_f32 v185, v186, v187\n"
"v_cvt_pk_bf16_f32 v186, v188, v189\n"
"v_cvt_pk_bf16_f32 v187, v190, v191\n"
"v_mfma_f32_16x16x32_bf16 v[180:183], a[108:111], a[180:183], v[180:183]\n"
"buffer_load_dwordx4 v225, s[28:31], s48 offen offset:1024 lds\n"
"v_permlane16_swap_b32 v184, v186\n"
"v_permlane16_swap_b32 v185, v187\n"
"v_accvgpr_write_b32 a36, v184\n"
"v_accvgpr_write_b32 a37, v185\n"
"v_accvgpr_write_b32 a38, v186\n"
"v_accvgpr_write_b32 a39, v187\n"
"v_mfma_f32_16x16x32_bf16 v[184:187], a[112:115], a[176:179], v[228:231]\n"
"v_permlane16_swap_b32 v208, v210\n"
"v_mfma_f32_16x16x32_bf16 v[184:187], a[116:119], a[180:183], v[184:187]\n"
"v_permlane16_swap_b32 v209, v211\n"
"v_mfma_f32_16x16x32_bf16 v[188:191], a[120:123], a[176:179], v[228:231]\n"
"ds_write_b128 v243, v[208:211] offset:0\n"
"v_cvt_pk_bf16_f32 v200, v200, v201\n"
"v_cvt_pk_bf16_f32 v201, v202, v203\n"
"v_cvt_pk_bf16_f32 v202, v204, v205\n"
"v_cvt_pk_bf16_f32 v203, v206, v207\n"
"v_mfma_f32_16x16x32_bf16 v[188:191], a[124:127], a[180:183], v[188:191]\n"
"v_permlane16_swap_b32 v200, v202\n"
"v_permlane16_swap_b32 v201, v203\n"
"v_accvgpr_write_b32 a44, v200\n"
"v_accvgpr_write_b32 a45, v201\n"
"v_accvgpr_write_b32 a46, v202\n"
"v_accvgpr_write_b32 a47, v203\n"
"v_mfma_f32_16x16x32_bf16 v[200:203], a[144:147], a[176:179], v[228:231]\n"
"ds_read_b64_tr_b16 a[222:223], v247 offset:25088\n"
"v_cvt_pk_bf16_f32 v192, v192, v193\n"
"v_mfma_f32_16x16x32_bf16 v[200:203], a[148:151], a[180:183], v[200:203]\n"
"buffer_load_dwordx4 v226, s[28:31], s48 offen offset:2048 lds\n"
"v_mfma_f32_16x16x32_bf16 v[204:207], a[152:155], a[176:179], v[228:231]\n"
"ds_read_b64_tr_b16 a[224:225], v244 offset:17408\n"
"v_cvt_pk_bf16_f32 v193, v194, v195\n"
"v_cvt_pk_bf16_f32 v194, v196, v197\n"
"v_cvt_pk_bf16_f32 v195, v198, v199\n"
"v_mfma_f32_16x16x32_bf16 v[204:207], a[156:159], a[180:183], v[204:207]\n"
"v_permlane16_swap_b32 v192, v194\n"
"v_permlane16_swap_b32 v193, v195\n"
"v_accvgpr_write_b32 a40, v192\n"
"v_accvgpr_write_b32 a41, v193\n"
"v_accvgpr_write_b32 a42, v194\n"
"v_accvgpr_write_b32 a43, v195\n"
"v_mfma_f32_16x16x32_bf16 v[192:195], a[128:131], a[176:179], v[228:231]\n"
"ds_read_b64_tr_b16 a[226:227], v244 offset:17920\n"
"v_xor_b32 v250, s74, v250\n"
"v_mfma_f32_16x16x32_bf16 v[192:195], a[132:135], a[180:183], v[192:195]\n"
"ds_read_b64_tr_b16 a[228:229], v244 offset:25600\n"
"v_xor_b32 v251, s74, v251\n"
"v_mfma_f32_16x16x32_bf16 v[196:199], a[136:139], a[176:179], v[228:231]\n"
"ds_read_b64_tr_b16 a[230:231], v244 offset:26112\n"
"v_xor_b32 v244, s74, v244\n"
"v_mfma_f32_16x16x32_bf16 v[196:199], a[140:143], a[180:183], v[196:199]\n"
"buffer_load_dwordx4 v227, s[28:31], s48 offen offset:3072 lds\n"
"v_mfma_f32_16x16x32_bf16 v[220:223], a[136:139], a[184:187], v[228:231]\n"
"s_xor_b32 s50, s50, s74\n"
"ds_read_b64_tr_b16 a[232:233], v245 offset:17408\n"
"ds_read_b64_tr_b16 a[234:235], v245 offset:17920\n"
"v_mfma_f32_16x16x32_bf16 v[220:223], a[140:143], a[188:191], v[220:223]\n"
"ds_read_b64_tr_b16 a[236:237], v245 offset:25600\n"
"ds_read_b64_tr_b16 a[238:239], v245 offset:26112\n"
"v_mfma_f32_16x16x32_bf16 v[216:219], a[128:131], a[184:187], v[228:231]\n"
"ds_read_b64_tr_b16 a[240:241], v246 offset:17408\n"
"ds_read_b64_tr_b16 a[242:243], v246 offset:17920\n"
"v_mfma_f32_16x16x32_bf16 v[216:219], a[132:135], a[188:191], v[216:219]\n"
"ds_read_b64_tr_b16 a[244:245], v246 offset:25600\n"
"ds_read_b64_tr_b16 a[246:247], v246 offset:26112\n"
"v_mfma_f32_16x16x32_bf16 v[212:215], a[104:107], a[184:187], v[228:231]\n"
"ds_read_b64_tr_b16 a[248:249], v247 offset:17408\n"
"ds_read_b64_tr_b16 a[250:251], v247 offset:17920\n"
"v_mfma_f32_16x16x32_bf16 v[212:215], a[108:111], a[188:191], v[212:215]\n"
"ds_read_b64_tr_b16 a[252:253], v247 offset:25600\n"
"ds_read_b64_tr_b16 a[254:255], v247 offset:26112\n"
"v_mfma_f32_16x16x32_bf16 v[208:211], a[96:99], a[184:187], v[228:231]\n"
"v_pk_fma_f32 v[0:1], v[0:1], s[70:71], s[70:71] op_sel:[0,0,1] op_sel_hi:[1,0,1]\nv_pk_fma_f32 v[2:3], v[2:3], s[70:71], s[70:71] op_sel:[0,0,1] op_sel_hi:[1,0,1]\nv_pk_fma_f32 v[4:5], v[4:5], s[70:71], s[70:71] op_sel:[0,0,1] op_sel_hi:[1,0,1]\nv_pk_fma_f32 v[6:7], v[6:7], s[70:71], s[70:71] op_sel:[0,0,1] op_sel_hi:[1,0,1]\nv_pk_fma_f32 v[8:9], v[8:9], s[70:71], s[70:71] op_sel:[0,0,1] op_sel_hi:[1,0,1]\nv_pk_fma_f32 v[10:11], v[10:11], s[70:71], s[70:71] op_sel:[0,0,1] op_sel_hi:[1,0,1]\nv_pk_fma_f32 v[12:13], v[12:13], s[70:71], s[70:71] op_sel:[0,0,1] op_sel_hi:[1,0,1]\nv_pk_fma_f32 v[14:15], v[14:15], s[70:71], s[70:71] op_sel:[0,0,1] op_sel_hi:[1,0,1]\nv_pk_fma_f32 v[16:17], v[16:17], s[70:71], s[70:71] op_sel:[0,0,1] op_sel_hi:[1,0,1]\nv_pk_fma_f32 v[18:19], v[18:19], s[70:71], s[70:71] op_sel:[0,0,1] op_sel_hi:[1,0,1]\nv_pk_fma_f32 v[20:21], v[20:21], s[70:71], s[70:71] op_sel:[0,0,1] op_sel_hi:[1,0,1]\nv_pk_fma_f32 v[22:23], v[22:23], s[70:71], s[70:71] op_sel:[0,0,1] op_sel_hi:[1,0,1]\nv_pk_fma_f32 v[24:25], v[24:25], s[70:71], s[70:71] op_sel:[0,0,1] op_sel_hi:[1,0,1]\nv_pk_fma_f32 v[26:27], v[26:27], s[70:71], s[70:71] op_sel:[0,0,1] op_sel_hi:[1,0,1]\nv_pk_fma_f32 v[28:29], v[28:29], s[70:71], s[70:71] op_sel:[0,0,1] op_sel_hi:[1,0,1]\nv_pk_fma_f32 v[30:31], v[30:31], s[70:71], s[70:71] op_sel:[0,0,1] op_sel_hi:[1,0,1]\n"
"v_cvt_pk_bf16_f32 v0, v0, v1\n"
"v_cvt_pk_bf16_f32 v1, v2, v3\n"
"v_cvt_pk_bf16_f32 v2, v4, v5\n"
"v_cvt_pk_bf16_f32 v3, v6, v7\n"
"v_mfma_f32_16x16x32_bf16 v[208:211], a[100:103], a[188:191], v[208:211]\n"
"v_permlane16_swap_b32 v0, v2\n"
"v_permlane16_swap_b32 v1, v3\n"
"v_accvgpr_write_b32 a48, v0\n"
"v_accvgpr_write_b32 a49, v1\n"
"v_accvgpr_write_b32 a50, v2\n"
"v_accvgpr_write_b32 a51, v3\n"
"v_mfma_f32_16x16x32_bf16 v[0:3], a[192:195], a[160:163], v[228:231]\n"
"s_waitcnt lgkmcnt(10)\n"
"v_cvt_pk_bf16_f32 v8, v8, v9\n"
"v_cvt_pk_bf16_f32 v9, v10, v11\n"
"v_mfma_f32_16x16x32_bf16 v[0:3], a[196:199], a[164:167], v[0:3]\n"
"v_cvt_pk_bf16_f32 v10, v12, v13\n"
"v_cvt_pk_bf16_f32 v11, v14, v15\n"
"v_mfma_f32_16x16x32_bf16 v[4:7], a[200:203], a[160:163], v[228:231]\n"
"s_waitcnt vmcnt(10) lgkmcnt(0)\n"
"v_permlane16_swap_b32 v8, v10\n"
"v_mfma_f32_16x16x32_bf16 v[4:7], a[204:207], a[164:167], v[4:7]\n"
"s_barrier\n"
"v_permlane16_swap_b32 v9, v11\n"
"v_accvgpr_write_b32 a52, v8\n"
"v_accvgpr_write_b32 a53, v9\n"
"v_accvgpr_write_b32 a54, v10\n"
"v_accvgpr_write_b32 a55, v11\n"
"v_mfma_f32_16x16x32_bf16 v[8:11], a[208:211], a[160:163], v[228:231]\n"
"s_mov_b32 m0, s51\n"
"ds_read_b64_tr_b16 a[96:97], v244 offset:0\n"
"v_cvt_pk_bf16_f32 v16, v16, v17\n"
"v_mfma_f32_16x16x32_bf16 v[8:11], a[212:215], a[164:167], v[8:11]\n"
"buffer_load_dwordx4 v248, s[24:27], s49 offen lds\n"
"v_mfma_f32_16x16x32_bf16 v[12:15], a[216:219], a[160:163], v[228:231]\n"
"ds_read_b64_tr_b16 a[98:99], v244 offset:512\n"
"v_cvt_pk_bf16_f32 v17, v18, v19\n"
"v_cvt_pk_bf16_f32 v18, v20, v21\n"
"v_cvt_pk_bf16_f32 v19, v22, v23\n"
"v_mfma_f32_16x16x32_bf16 v[12:15], a[220:223], a[164:167], v[12:15]\n"
"v_permlane16_swap_b32 v16, v18\n"
"v_permlane16_swap_b32 v17, v19\n"
"v_accvgpr_write_b32 a56, v16\n"
"v_accvgpr_write_b32 a57, v17\n"
"v_accvgpr_write_b32 a58, v18\n"
"v_accvgpr_write_b32 a59, v19\n"
"v_mfma_f32_16x16x32_bf16 v[16:19], a[224:227], a[160:163], v[228:231]\n"
"ds_read_b64_tr_b16 a[100:101], v244 offset:8192\n"
"v_cvt_pk_bf16_f32 v24, v24, v25\n"
"v_mfma_f32_16x16x32_bf16 v[16:19], a[228:231], a[164:167], v[16:19]\n"
"ds_read_b64_tr_b16 a[102:103], v244 offset:8704\n"
"v_cvt_pk_bf16_f32 v25, v26, v27\n"
"v_mfma_f32_16x16x32_bf16 v[20:23], a[232:235], a[160:163], v[228:231]\n"
"ds_read_b64_tr_b16 a[128:129], v244 offset:1024\n"
"v_cvt_pk_bf16_f32 v26, v28, v29\n"
"v_cvt_pk_bf16_f32 v27, v30, v31\n"
"v_mfma_f32_16x16x32_bf16 v[20:23], a[236:239], a[164:167], v[20:23]\n"
"buffer_load_dwordx4 v249, s[24:27], s49 offen offset:1024 lds\n"
"v_permlane16_swap_b32 v24, v26\n"
"v_permlane16_swap_b32 v25, v27\n"
"v_accvgpr_write_b32 a60, v24\n"
"v_accvgpr_write_b32 a61, v25\n"
"v_accvgpr_write_b32 a62, v26\n"
"v_accvgpr_write_b32 a63, v27\n"
"v_mfma_f32_16x16x32_bf16 v[24:27], a[240:243], a[160:163], v[228:231]\n"
"ds_read_b64_tr_b16 a[130:131], v244 offset:1536\n"
"v_xor_b32 v245, s74, v245\n"
"v_mfma_f32_16x16x32_bf16 v[24:27], a[244:247], a[164:167], v[24:27]\n"
"ds_read_b64_tr_b16 a[104:105], v245 offset:0\n"
"v_xor_b32 v246, s74, v246\n"
"v_mfma_f32_16x16x32_bf16 v[28:31], a[248:251], a[160:163], v[228:231]\n"
"v_pk_fma_f32 v[32:33], v[32:33], s[70:71], s[70:71] op_sel:[0,0,1] op_sel_hi:[1,0,1]\nv_pk_fma_f32 v[34:35], v[34:35], s[70:71], s[70:71] op_sel:[0,0,1] op_sel_hi:[1,0,1]\nv_pk_fma_f32 v[36:37], v[36:37], s[70:71], s[70:71] op_sel:[0,0,1] op_sel_hi:[1,0,1]\nv_pk_fma_f32 v[38:39], v[38:39], s[70:71], s[70:71] op_sel:[0,0,1] op_sel_hi:[1,0,1]\nv_pk_fma_f32 v[40:41], v[40:41], s[70:71], s[70:71] op_sel:[0,0,1] op_sel_hi:[1,0,1]\nv_pk_fma_f32 v[42:43], v[42:43], s[70:71], s[70:71] op_sel:[0,0,1] op_sel_hi:[1,0,1]\nv_pk_fma_f32 v[44:45], v[44:45], s[70:71], s[70:71] op_sel:[0,0,1] op_sel_hi:[1,0,1]\nv_pk_fma_f32 v[46:47], v[46:47], s[70:71], s[70:71] op_sel:[0,0,1] op_sel_hi:[1,0,1]\nv_pk_fma_f32 v[48:49], v[48:49], s[70:71], s[70:71] op_sel:[0,0,1] op_sel_hi:[1,0,1]\nv_pk_fma_f32 v[50:51], v[50:51], s[70:71], s[70:71] op_sel:[0,0,1] op_sel_hi:[1,0,1]\nv_pk_fma_f32 v[52:53], v[52:53], s[70:71], s[70:71] op_sel:[0,0,1] op_sel_hi:[1,0,1]\nv_pk_fma_f32 v[54:55], v[54:55], s[70:71], s[70:71] op_sel:[0,0,1] op_sel_hi:[1,0,1]\nv_pk_fma_f32 v[56:57], v[56:57], s[70:71], s[70:71] op_sel:[0,0,1] op_sel_hi:[1,0,1]\nv_pk_fma_f32 v[58:59], v[58:59], s[70:71], s[70:71] op_sel:[0,0,1] op_sel_hi:[1,0,1]\nv_pk_fma_f32 v[60:61], v[60:61], s[70:71], s[70:71] op_sel:[0,0,1] op_sel_hi:[1,0,1]\nv_pk_fma_f32 v[62:63], v[62:63], s[70:71], s[70:71] op_sel:[0,0,1] op_sel_hi:[1,0,1]\n"
"v_mfma_f32_16x16x32_bf16 v[28:31], a[252:255], a[164:167], v[28:31]\n"
"ds_read_b128 a[160:163], v250 offset:0\n"
"v_cvt_pk_bf16_f32 v56, v56, v57\n"
"v_cvt_pk_bf16_f32 v57, v58, v59\n"
"v_cvt_pk_bf16_f32 v58, v60, v61\n"
"v_cvt_pk_bf16_f32 v59, v62, v63\n"
"v_mfma_f32_16x16x32_bf16 v[60:63], a[248:251], a[168:171], v[228:231]\n"
"v_permlane16_swap_b32 v56, v58\n"
"v_mfma_f32_16x16x32_bf16 v[60:63], a[252:255], a[172:175], v[60:63]\n"
"buffer_load_dwordx4 v254, s[24:27], s49 offen offset:2048 lds\n"
"v_permlane16_swap_b32 v57, v59\n"
"v_accvgpr_write_b32 a76, v56\n"
"v_accvgpr_write_b32 a77, v57\n"
"v_accvgpr_write_b32 a78, v58\n"
"v_accvgpr_write_b32 a79, v59\n"
"v_mfma_f32_16x16x32_bf16 v[56:59], a[240:243], a[168:171], v[228:231]\n"
"ds_read_b128 a[164:167], v251 offset:0\n"
"v_cvt_pk_bf16_f32 v48, v48, v49\n"
"v_mfma_f32_16x16x32_bf16 v[56:59], a[244:247], a[172:175], v[56:59]\n"
"ds_read_b64_tr_b16 a[106:107], v245 offset:512\n"
"v_cvt_pk_bf16_f32 v49, v50, v51\n"
"v_cvt_pk_bf16_f32 v50, v52, v53\n"
"v_cvt_pk_bf16_f32 v51, v54, v55\n"
"v_mfma_f32_16x16x32_bf16 v[52:55], a[232:235], a[168:171], v[228:231]\n"
"v_permlane16_swap_b32 v48, v50\n"
"v_mfma_f32_16x16x32_bf16 v[52:55], a[236:239], a[172:175], v[52:55]\n"
"v_permlane16_swap_b32 v49, v51\n"
"v_accvgpr_write_b32 a72, v48\n"
"v_accvgpr_write_b32 a73, v49\n"
"v_accvgpr_write_b32 a74, v50\n"
"v_accvgpr_write_b32 a75, v51\n"
"v_mfma_f32_16x16x32_bf16 v[48:51], a[224:227], a[168:171], v[228:231]\n"
"ds_read_b64_tr_b16 a[108:109], v245 offset:8192\n"
"v_cvt_pk_bf16_f32 v40, v40, v41\n"
"v_mfma_f32_16x16x32_bf16 v[48:51], a[228:231], a[172:175], v[48:51]\n"
"buffer_load_dwordx4 v255, s[24:27], s49 offen offset:3072 lds\n"
"v_cvt_pk_bf16_f32 v41, v42, v43\n"
"v_cvt_pk_bf16_f32 v42, v44, v45\n"
"v_cvt_pk_bf16_f32 v43, v46, v47\n"
"v_mfma_f32_16x16x32_bf16 v[44:47], a[216:219], a[168:171], v[228:231]\n"
"v_pk_fma_f32 v[96:97], v[96:97], s[70:71], s[70:71] op_sel:[0,0,1] op_sel_hi:[1,0,1]\nv_pk_fma_f32 v[98:99], v[98:99], s[70:71], s[70:71] op_sel:[0,0,1] op_sel_hi:[1,0,1]\nv_pk_fma_f32 v[100:101], v[100:101], s[70:71], s[70:71] op_sel:[0,0,1] op_sel_hi:[1,0,1]\nv_pk_fma_f32 v[102:103], v[102:103], s[70:71], s[70:71] op_sel:[0,0,1] op_sel_hi:[1,0,1]\nv_pk_fma_f32 v[104:105], v[104:105], s[70:71], s[70:71] op_sel:[0,0,1] op_sel_hi:[1,0,1]\nv_pk_fma_f32 v[106:107], v[106:107], s[70:71], s[70:71] op_sel:[0,0,1] op_sel_hi:[1,0,1]\nv_pk_fma_f32 v[108:109], v[108:109], s[70:71], s[70:71] op_sel:[0,0,1] op_sel_hi:[1,0,1]\nv_pk_fma_f32 v[110:111], v[110:111], s[70:71], s[70:71] op_sel:[0,0,1] op_sel_hi:[1,0,1]\n"
"v_mfma_f32_16x16x32_bf16 v[44:47], a[220:223], a[172:175], v[44:47]\n"
"v_cvt_pk_bf16_f32 v104, v104, v105\n"
"v_cvt_pk_bf16_f32 v105, v106, v107\n"
"v_cvt_pk_bf16_f32 v106, v108, v109\n"
"v_cvt_pk_bf16_f32 v107, v110, v111\n"
"v_permlane16_swap_b32 v40, v42\n"
"v_permlane16_swap_b32 v41, v43\n"
"v_accvgpr_write_b32 a68, v40\n"
"v_accvgpr_write_b32 a69, v41\n"
"v_accvgpr_write_b32 a70, v42\n"
"v_accvgpr_write_b32 a71, v43\n"
"v_mfma_f32_16x16x32_bf16 v[40:43], a[208:211], a[168:171], v[228:231]\n"
"v_permlane16_swap_b32 v104, v106\n"
"v_permlane16_swap_b32 v105, v107\n"
"v_mfma_f32_16x16x32_bf16 v[40:43], a[212:215], a[172:175], v[40:43]\n"
"s_add_u32 s40, s49, 40960\n"
"ds_write_b128 v243, v[104:107] offset:6144\n"
"v_cvt_pk_bf16_f32 v32, v32, v33\n"
"v_cvt_pk_bf16_f32 v33, v34, v35\n"
"v_cvt_pk_bf16_f32 v34, v36, v37\n"
"v_cvt_pk_bf16_f32 v35, v38, v39\n"
"v_mfma_f32_16x16x32_bf16 v[36:39], a[200:203], a[168:171], v[228:231]\n"
"s_add_u32 m0, s51, 4096\n"
"v_permlane16_swap_b32 v32, v34\n"
"v_mfma_f32_16x16x32_bf16 v[36:39], a[204:207], a[172:175], v[36:39]\n"
"buffer_load_dwordx4 v248, s[24:27], s40 offen lds\n"
"v_permlane16_swap_b32 v33, v35\n"
"v_accvgpr_write_b32 a64, v32\n"
"v_accvgpr_write_b32 a65, v33\n"
"v_accvgpr_write_b32 a66, v34\n"
"v_accvgpr_write_b32 a67, v35\n"
"v_mfma_f32_16x16x32_bf16 v[32:35], a[192:195], a[168:171], v[228:231]\n"
"v_pk_fma_f32 v[64:65], v[64:65], s[70:71], s[70:71] op_sel:[0,0,1] op_sel_hi:[1,0,1]\nv_pk_fma_f32 v[66:67], v[66:67], s[70:71], s[70:71] op_sel:[0,0,1] op_sel_hi:[1,0,1]\nv_pk_fma_f32 v[68:69], v[68:69], s[70:71], s[70:71] op_sel:[0,0,1] op_sel_hi:[1,0,1]\nv_pk_fma_f32 v[70:71], v[70:71], s[70:71], s[70:71] op_sel:[0,0,1] op_sel_hi:[1,0,1]\nv_pk_fma_f32 v[72:73], v[72:73], s[70:71], s[70:71] op_sel:[0,0,1] op_sel_hi:[1,0,1]\nv_pk_fma_f32 v[74:75], v[74:75], s[70:71], s[70:71] op_sel:[0,0,1] op_sel_hi:[1,0,1]\nv_pk_fma_f32 v[76:77], v[76:77], s[70:71], s[70:71] op_sel:[0,0,1] op_sel_hi:[1,0,1]\nv_pk_fma_f32 v[78:79], v[78:79], s[70:71], s[70:71] op_sel:[0,0,1] op_sel_hi:[1,0,1]\nv_pk_fma_f32 v[80:81], v[80:81], s[70:71], s[70:71] op_sel:[0,0,1] op_sel_hi:[1,0,1]\nv_pk_fma_f32 v[82:83], v[82:83], s[70:71], s[70:71] op_sel:[0,0,1] op_sel_hi:[1,0,1]\nv_pk_fma_f32 v[84:85], v[84:85], s[70:71], s[70:71] op_sel:[0,0,1] op_sel_hi:[1,0,1]\nv_pk_fma_f32 v[86:87], v[86:87], s[70:71], s[70:71] op_sel:[0,0,1] op_sel_hi:[1,0,1]\nv_pk_fma_f32 v[88:89], v[88:89], s[70:71], s[70:71] op_sel:[0,0,1] op_sel_hi:[1,0,1]\nv_pk_fma_f32 v[90:91], v[90:91], s[70:71], s[70:71] op_sel:[0,0,1] op_sel_hi:[1,0,1]\nv_pk_fma_f32 v[92:93], v[92:93], s[70:71], s[70:71] op_sel:[0,0,1] op_sel_hi:[1,0,1]\nv_pk_fma_f32 v[94:95], v[94:95], s[70:71], s[70:71] op_sel:[0,0,1] op_sel_hi:[1,0,1]\n"
"v_cvt_pk_bf16_f32 v64, v64, v65\n"
"v_cvt_pk_bf16_f32 v65, v66, v67\n"
"v_cvt_pk_bf16_f32 v66, v68, v69\n"
"v_cvt_pk_bf16_f32 v67, v70, v71\n"
"v_mfma_f32_16x16x32_bf16 v[32:35], a[196:199], a[172:175], v[32:35]\n"
"v_permlane16_swap_b32 v64, v66\n"
"v_permlane16_swap_b32 v65, v67\n"
"v_accvgpr_write_b32 a80, v64\n"
"v_accvgpr_write_b32 a81, v65\n"
"v_accvgpr_write_b32 a82, v66\n"
"v_accvgpr_write_b32 a83, v67\n"
"v_mfma_f32_16x16x32_bf16 v[64:67], a[192:195], a[176:179], v[228:231]\n"
"ds_read_b64_tr_b16 a[110:111], v245 offset:8704\n"
"v_cvt_pk_bf16_f32 v96, v96, v97\n"
"v_cvt_pk_bf16_f32 v97, v98, v99\n"
"v_cvt_pk_bf16_f32 v98, v100, v101\n"
"v_cvt_pk_bf16_f32 v99, v102, v103\n"
"v_mfma_f32_16x16x32_bf16 v[64:67], a[196:199], a[180:183], v[64:67]\n"
"v_permlane16_swap_b32 v96, v98\n"
"v_permlane16_swap_b32 v97, v99\n"
"v_mfma_f32_16x16x32_bf16 v[68:71], a[200:203], a[176:179], v[228:231]\n"
"ds_write_b128 v243, v[96:99] offset:4096\n"
"v_cvt_pk_bf16_f32 v72, v72, v73\n"
"v_cvt_pk_bf16_f32 v73, v74, v75\n"
"v_cvt_pk_bf16_f32 v74, v76, v77\n"
"v_cvt_pk_bf16_f32 v75, v78, v79\n"
"v_mfma_f32_16x16x32_bf16 v[68:71], a[204:207], a[180:183], v[68:71]\n"
"buffer_load_dwordx4 v249, s[24:27], s40 offen offset:1024 lds\n"
"v_permlane16_swap_b32 v72, v74\n"
"v_permlane16_swap_b32 v73, v75\n"
"v_accvgpr_write_b32 a84, v72\n"
"v_accvgpr_write_b32 a85, v73\n"
"v_accvgpr_write_b32 a86, v74\n"
"v_accvgpr_write_b32 a87, v75\n"
"v_mfma_f32_16x16x32_bf16 v[72:75], a[208:211], a[176:179], v[228:231]\n"
"ds_read_b64_tr_b16 a[112:113], v246 offset:0\n"
"v_cvt_pk_bf16_f32 v88, v88, v89\n"
"v_mfma_f32_16x16x32_bf16 v[72:75], a[212:215], a[180:183], v[72:75]\n"
"ds_read_b64_tr_b16 a[114:115], v246 offset:512\n"
"v_cvt_pk_bf16_f32 v89, v90, v91\n"
"v_mfma_f32_16x16x32_bf16 v[76:79], a[216:219], a[176:179], v[228:231]\n"
"ds_read_b64_tr_b16 a[116:117], v246 offset:8192\n"
"ds_read_b64_tr_b16 a[118:119], v246 offset:8704\n"
"v_cvt_pk_bf16_f32 v90, v92, v93\n"
"v_cvt_pk_bf16_f32 v91, v94, v95\n"
"v_xor_b32 v247, s74, v247\n"
"v_mfma_f32_16x16x32_bf16 v[76:79], a[220:223], a[180:183], v[76:79]\n"
"ds_read_b64_tr_b16 a[120:121], v247 offset:0\n"
"ds_read_b64_tr_b16 a[122:123], v247 offset:512\n"
"v_permlane16_swap_b32 v88, v90\n"
"v_permlane16_swap_b32 v89, v91\n"
"v_accvgpr_write_b32 a92, v88\n"
"v_accvgpr_write_b32 a93, v89\n"
"v_accvgpr_write_b32 a94, v90\n"
"v_accvgpr_write_b32 a95, v91\n"
"v_mfma_f32_16x16x32_bf16 v[88:91], a[240:243], a[176:179], v[228:231]\n"
"ds_read_b64_tr_b16 a[124:125], v247 offset:8192\n"
"ds_read_b64_tr_b16 a[126:127], v247 offset:8704\n"
"v_mfma_f32_16x16x32_bf16 v[88:91], a[244:247], a[180:183], v[88:91]\n"
"buffer_load_dwordx4 v254, s[24:27], s40 offen offset:2048 lds\n"
"v_mfma_f32_16x16x32_bf16 v[92:95], a[248:251], a[176:179], v[228:231]\n"
"s_add_u32 s49, s49, 128\n"
"ds_read_b64_tr_b16 a[132:133], v244 offset:9216\n"
"v_cvt_pk_bf16_f32 v80, v80, v81\n"
"v_cvt_pk_bf16_f32 v81, v82, v83\n"
"v_cvt_pk_bf16_f32 v82, v84, v85\n"
"v_cvt_pk_bf16_f32 v83, v86, v87\n"
"v_mfma_f32_16x16x32_bf16 v[92:95], a[252:255], a[180:183], v[92:95]\n"
"s_sub_u32 s41, s49, 1280\n"
"ds_read_b64_tr_b16 a[134:135], v244 offset:9728\n"
"v_permlane16_swap_b32 v80, v82\n"
"v_permlane16_swap_b32 v81, v83\n"
"v_accvgpr_write_b32 a88, v80\n"
"v_accvgpr_write_b32 a89, v81\n"
"v_accvgpr_write_b32 a90, v82\n"
"v_accvgpr_write_b32 a91, v83\n"
"v_mfma_f32_16x16x32_bf16 v[80:83], a[224:227], a[176:179], v[228:231]\n"
"s_min_u32 s49, s49, s41\n"
"ds_read_b64_tr_b16 a[136:137], v245 offset:1024\n"
"ds_read_b64_tr_b16 a[138:139], v245 offset:1536\n"
"v_mfma_f32_16x16x32_bf16 v[80:83], a[228:231], a[180:183], v[80:83]\n"
"s_xor_b32 s51, s51, s74\n"
"ds_read_b64_tr_b16 a[140:141], v245 offset:9216\n"
"ds_read_b64_tr_b16 a[142:143], v245 offset:9728\n"
"v_mfma_f32_16x16x32_bf16 v[84:87], a[232:235], a[176:179], v[228:231]\n"
"ds_read_b64_tr_b16 a[144:145], v246 offset:1024\n"
"ds_read_b64_tr_b16 a[146:147], v246 offset:1536\n"
"v_mfma_f32_16x16x32_bf16 v[84:87], a[236:239], a[180:183], v[84:87]\n"
"ds_read_b64_tr_b16 a[148:149], v246 offset:9216\n"
"ds_read_b64_tr_b16 a[150:151], v246 offset:9728\n"
"v_mfma_f32_16x16x32_bf16 v[108:111], a[232:235], a[184:187], v[228:231]\n"
"ds_read_b64_tr_b16 a[152:153], v247 offset:1024\n"
"ds_read_b64_tr_b16 a[154:155], v247 offset:1536\n"
"v_mfma_f32_16x16x32_bf16 v[108:111], a[236:239], a[188:191], v[108:111]\n"
"ds_read_b64_tr_b16 a[156:157], v247 offset:9216\n"
"ds_read_b64_tr_b16 a[158:159], v247 offset:9728\n"
"v_mfma_f32_16x16x32_bf16 v[104:107], a[224:227], a[184:187], v[228:231]\n"
"ds_read_b128 a[168:171], v250 offset:2048\n"
"v_xor_b32 v252, s74, v252\n"
"v_mfma_f32_16x16x32_bf16 v[104:107], a[228:231], a[188:191], v[104:107]\n"
"ds_read_b128 a[172:175], v251 offset:2048\n"
"v_xor_b32 v253, s74, v253\n"
"v_mfma_f32_16x16x32_bf16 v[100:103], a[200:203], a[184:187], v[228:231]\n"
"ds_read_b128 a[176:179], v250 offset:4096\n"
"v_mfma_f32_16x16x32_bf16 v[100:103], a[204:207], a[188:191], v[100:103]\n"
"ds_read_b128 a[180:183], v251 offset:4096\n"
"v_mfma_f32_16x16x32_bf16 v[96:99], a[192:195], a[184:187], v[228:231]\n"
"s_add_u32 s47, s47, 1\n"
"ds_read_b128 a[184:187], v252 offset:0\n"
"v_mfma_f32_16x16x32_bf16 v[96:99], a[196:199], a[188:191], v[96:99]\n"
"s_waitcnt lgkmcnt(9)\n"
"ds_read_b128 a[188:191], v253 offset:0\n"
"s_mov_b32 s62, 18\n"
"s_branch LPL_%=\n"
"LPT2_%=:\n"
"v_mfma_f32_16x16x32_bf16 v[112:115], a[96:99], a[160:163], v[112:115]\n"
"s_add_u32 s78, s32, s96\ns_addc_u32 s79, s33, 0\n"
"s_mov_b64 s[80:81], 0\n"
"s_mov_b32 s82, 0\n"
"v_mfma_f32_16x16x32_bf16 v[112:115], a[100:103], a[164:167], v[112:115]\n"
"v_bfe_u32 v228, v242, 8, 2\n"
"v_and_b32 v229, 0x60, v242\n"
"v_mfma_f32_16x16x32_bf16 v[116:119], a[104:107], a[160:163], v[116:119]\n"
"s_waitcnt vmcnt(11)\n"
"s_load_dword s80, s[78:79], 0\n"
"v_lshlrev_b32 v228, 4, v228\n"
"v_mfma_f32_16x16x32_bf16 v[116:119], a[108:111], a[164:167], v[116:119]\n"
"s_barrier\n"
"v_mfma_f32_16x16x32_bf16 v[120:123], a[112:115], a[160:163], v[120:123]\n"
"s_add_u32 s40, s48, 256\n"
"s_xor_b32 m0, s50, 0xc000\n"
"ds_read_b64_tr_b16 a[192:193], v244 offset:16384\n"
"v_mfma_f32_16x16x32_bf16 v[120:123], a[116:119], a[164:167], v[120:123]\n"
"buffer_load_dwordx4 v224, s[28:31], s40 offen lds\n"
"v_mfma_f32_16x16x32_bf16 v[124:127], a[120:123], a[160:163], v[124:127]\n"
"ds_read_b64_tr_b16 a[194:195], v244 offset:16896\n"
"v_xor_b32 v228, v228, v229\n"
"v_mfma_f32_16x16x32_bf16 v[124:127], a[124:127], a[164:167], v[124:127]\n"
"ds_read_b64_tr_b16 a[196:197], v244 offset:24576\n"
"v_and_b32 v229, 0x70, v242\n"
"v_mfma_f32_16x16x32_bf16 v[128:131], a[128:131], a[160:163], v[128:131]\n"
"ds_read_b64_tr_b16 a[198:199], v244 offset:25088\n"
"v_lshl_add_u32 v228, v229, 3, v228\n"
"v_mfma_f32_16x16x32_bf16 v[128:131], a[132:135], a[164:167], v[128:131]\n"
"ds_read_b64_tr_b16 a[200:201], v245 offset:16384\n"
"v_and_b32 v229, 0x80, v242\n"
"v_mfma_f32_16x16x32_bf16 v[132:135], a[136:139], a[160:163], v[132:135]\n"
"ds_read_b64_tr_b16 a[202:203], v245 offset:16896\n"
"v_lshl_add_u32 v228, v229, 4, v228\n"
"v_mfma_f32_16x16x32_bf16 v[132:135], a[140:143], a[164:167], v[132:135]\n"
"buffer_load_dwordx4 v225, s[28:31], s40 offen offset:1024 lds\n"
"v_mfma_f32_16x16x32_bf16 v[136:139], a[144:147], a[160:163], v[136:139]\n"
"s_add_u32 s82, s82, s80\n"
"ds_read_b64_tr_b16 a[204:205], v245 offset:24576\n"
"v_mov_b32 v229, s43\n"
"v_mfma_f32_16x16x32_bf16 v[136:139], a[148:151], a[164:167], v[136:139]\n"
"s_add_u32 s82, s82, s80\n"
"ds_read_b64_tr_b16 a[206:207], v245 offset:25088\n"
"v_mul_u32_u24 v229, 0x81, v229\n"
"v_mfma_f32_16x16x32_bf16 v[140:143], a[152:155], a[160:163], v[140:143]\n"
"s_add_u32 s82, s82, s80\n"
"ds_read_b64_tr_b16 a[208:209], v246 offset:16384\n"
"v_xor_b32 v228, v228, v229\n"
"v_mfma_f32_16x16x32_bf16 v[140:143], a[156:159], a[164:167], v[140:143]\n"
"s_add_u32 s82, s82, s80\n"
"ds_read_b64_tr_b16 a[210:211], v246 offset:16896\n"
"v_xor_b32 v229, 64, v228\n"
"v_mfma_f32_16x16x32_bf16 v[172:175], a[152:155], a[168:171], v[172:175]\n"
"s_add_u32 s82, s82, s80\n"
"ds_read_b64_tr_b16 a[212:213], v246 offset:24576\n"
"v_xor_b32 v230, 0x2000, v228\n"
"v_mfma_f32_16x16x32_bf16 v[172:175], a[156:159], a[172:175], v[172:175]\n"
"s_add_u32 s82, s82, s80\n"
"ds_read_b64_tr_b16 a[214:215], v246 offset:25088\n"
"v_xor_b32 v231, 0x2000, v229\n"
"v_mfma_f32_16x16x32_bf16 v[168:171], a[144:147], a[168:171], v[168:171]\n"
"s_add_u32 s82, s82, s80\n"
"ds_read_b64_tr_b16 a[216:217], v247 offset:16384\n"
"v_bfe_i32 v241, v242, 4, 1\n"
"v_mfma_f32_16x16x32_bf16 v[168:171], a[148:151], a[172:175], v[168:171]\n"
"s_add_u32 s82, s82, s80\n"
"buffer_load_dwordx4 v226, s[28:31], s40 offen offset:2048 lds\n"
"v_mfma_f32_16x16x32_bf16 v[164:167], a[136:139], a[168:171], v[164:167]\n"
"s_add_u32 s82, s82, s80\n"
"ds_read_b64_tr_b16 a[218:219], v247 offset:16896\n"
"v_and_b32 v241, 0xaaaaaaaa, v241\n"
"v_mfma_f32_16x16x32_bf16 v[164:167], a[140:143], a[172:175], v[164:167]\n"
"s_add_u32 s82, s82, s80\n"
"ds_read_b64_tr_b16 a[220:221], v247 offset:24576\n"
"v_or_b32 v241, 0x44444444, v241\n"
"v_mfma_f32_16x16x32_bf16 v[160:163], a[128:131], a[168:171], v[160:163]\n"
"s_add_u32 s82, s82, s80\n"
"ds_read_b64_tr_b16 a[222:223], v247 offset:25088\n"
"v_bfe_u32 v248, v242, 8, 2\n"
"v_mfma_f32_16x16x32_bf16 v[160:163], a[132:135], a[172:175], v[160:163]\n"
"s_add_u32 s82, s82, s80\n"
"ds_read_b64_tr_b16 a[224:225], v244 offset:17408\n"
"v_lshlrev_b32 v248, 6, v248\n"
"v_mfma_f32_16x16x32_bf16 v[156:159], a[120:123], a[168:171], v[156:159]\n"
"s_add_u32 s82, s82, s80\n"
"ds_read_b64_tr_b16 a[226:227], v244 offset:17920\n"
"v_xor_b32 v248, v248, v242\n"
"v_mfma_f32_16x16x32_bf16 v[156:159], a[124:127], a[172:175], v[156:159]\n"
"s_add_u32 s82, s82, s80\n"
"buffer_load_dwordx4 v227, s[28:31], s40 offen offset:3072 lds\n"
"v_mfma_f32_16x16x32_bf16 v[152:155], a[112:115], a[168:171], v[152:155]\n"
"s_add_u32 s82, s82, s80\n"
"s_mov_b32 s58, 245760\n"
"ds_read_b64_tr_b16 a[228:229], v244 offset:25600\n"
"v_mfma_f32_16x16x32_bf16 v[152:155], a[116:119], a[172:175], v[152:155]\n"
"s_add_u32 s82, s82, s80\n"
"s_mov_b32 s47, 0\n"
"ds_read_b64_tr_b16 a[230:231], v244 offset:26112\n"
"v_mfma_f32_16x16x32_bf16 v[148:151], a[104:107], a[168:171], v[148:151]\n"
"s_add_u32 s82, s82, s80\n"
"s_mov_b32 s52, -896\n"
"ds_read_b64_tr_b16 a[232:233], v245 offset:17408\n"
"v_mfma_f32_16x16x32_bf16 v[148:151], a[108:111], a[172:175], v[148:151]\n"
"s_load_dword s81, s[78:79], 0x2000\n"
"s_mov_b32 s53, 0\n"
"ds_read_b64_tr_b16 a[234:235], v245 offset:17920\n"
"v_mfma_f32_16x16x32_bf16 v[144:147], a[96:99], a[168:171], v[144:147]\n"
"s_mov_b32 m0, s44\n"
"ds_read_b64_tr_b16 a[236:237], v245 offset:25600\n"
"v_xor_b32 v244, s74, v244\n"
"v_mfma_f32_16x16x32_bf16 v[144:147], a[100:103], a[172:175], v[144:147]\n"
"buffer_load_dwordx4 v240, s[16:19], s56 offen nt lds\n"
"v_mfma_f32_16x16x32_bf16 v[176:179], a[96:99], a[176:179], v[176:179]\n"
"s_waitcnt lgkmcnt(5)\n"
"ds_read_b64_tr_b16 a[238:239], v245 offset:26112\n"
"v_xor_b32 v250, s74, v250\n"
"v_mfma_f32_16x16x32_bf16 v[176:179], a[100:103], a[180:183], v[176:179]\n"
"ds_read_b64_tr_b16 a[240:241], v246 offset:17408\n"
"v_xor_b32 v251, s74, v251\n"
"v_mfma_f32_16x16x32_bf16 v[180:183], a[104:107], a[176:179], v[180:183]\n"
"ds_read_b64_tr_b16 a[242:243], v246 offset:17920\n"
"v_xor_b32 v245, s74, v245\n"
"v_mfma_f32_16x16x32_bf16 v[180:183], a[108:111], a[180:183], v[180:183]\n"
"ds_read_b64_tr_b16 a[244:245], v246 offset:25600\n"
"v_xor_b32 v252, s74, v252\n"
"v_mfma_f32_16x16x32_bf16 v[184:187], a[112:115], a[176:179], v[184:187]\n"
"ds_read_b64_tr_b16 a[246:247], v246 offset:26112\n"
"v_xor_b32 v246, s74, v246\n"
"v_mfma_f32_16x16x32_bf16 v[184:187], a[116:119], a[180:183], v[184:187]\n"
"ds_read_b64_tr_b16 a[248:249], v247 offset:17408\n"
"v_xor_b32 v253, s74, v253\n"
"v_mfma_f32_16x16x32_bf16 v[188:191], a[120:123], a[176:179], v[188:191]\n"
"s_add_u32 s40, s56, s52\n"
"ds_read_b64_tr_b16 a[250:251], v247 offset:17920\n"
"v_bfe_u32 v249, v242, 4, 2\n"
"v_mfma_f32_16x16x32_bf16 v[188:191], a[124:127], a[180:183], v[188:191]\n"
"s_sub_u32 s82, s82, s81\n"
"buffer_load_dwordx4 v240, s[16:19], s40 offen offset:1024 nt lds\n"
"v_mfma_f32_16x16x32_bf16 v[200:203], a[144:147], a[176:179], v[200:203]\n"
"s_sub_u32 s82, s82, s81\n"
"ds_read_b64_tr_b16 a[252:253], v247 offset:25600\n"
"v_mul_u32_u24 v249, 20, v249\n"
"v_mfma_f32_16x16x32_bf16 v[200:203], a[148:151], a[180:183], v[200:203]\n"
"s_sub_u32 s82, s82, s81\n"
"ds_read_b64_tr_b16 a[254:255], v247 offset:26112\n"
"v_xor_b32 v247, s74, v247\n"
"v_mfma_f32_16x16x32_bf16 v[204:207], a[152:155], a[176:179], v[204:207]\n"
"s_sub_u32 s82, s82, s81\n"
"v_mfma_f32_16x16x32_bf16 v[204:207], a[156:159], a[180:183], v[204:207]\n"
"s_sub_u32 s82, s82, s81\n"
"v_mfma_f32_16x16x32_bf16 v[192:195], a[128:131], a[176:179], v[192:195]\n"
"s_sub_u32 s82, s82, s81\n"
"s_add_u32 s40, s56, s53\n"
"v_mfma_f32_16x16x32_bf16 v[192:195], a[132:135], a[180:183], v[192:195]\n"
"s_sub_u32 s82, s82, s81\n"
"buffer_load_dwordx4 v240, s[16:19], s40 offen offset:2048 nt lds\n"
"v_mfma_f32_16x16x32_bf16 v[196:199], a[136:139], a[176:179], v[196:199]\n"
"s_sub_u32 s82, s82, s81\n"
"v_mfma_f32_16x16x32_bf16 v[196:199], a[140:143], a[180:183], v[196:199]\n"
"s_sub_u32 s82, s82, s81\n"
"v_mfma_f32_16x16x32_bf16 v[220:223], a[136:139], a[184:187], v[220:223]\n"
"s_sub_u32 s82, s82, s81\n"
"v_mfma_f32_16x16x32_bf16 v[220:223], a[140:143], a[188:191], v[220:223]\n"
"s_sub_u32 s82, s82, s81\n"
"v_mfma_f32_16x16x32_bf16 v[216:219], a[128:131], a[184:187], v[216:219]\n"
"s_sub_u32 s82, s82, s81\n"
"s_add_u32 s40, s56, s54\n"
"v_mfma_f32_16x16x32_bf16 v[216:219], a[132:135], a[188:191], v[216:219]\n"
"s_sub_u32 s82, s82, s81\n"
"buffer_load_dwordx4 v240, s[16:19], s40 offen offset:3072 nt lds\n"
"v_mfma_f32_16x16x32_bf16 v[212:215], a[104:107], a[184:187], v[212:215]\n"
"s_sub_u32 s82, s82, s81\n"
"v_mfma_f32_16x16x32_bf16 v[212:215], a[108:111], a[188:191], v[212:215]\n"
"s_sub_u32 s82, s82, s81\n"
"v_mfma_f32_16x16x32_bf16 v[208:211], a[96:99], a[184:187], v[208:211]\n"
"s_sub_u32 s82, s82, s81\n"
"v_mfma_f32_16x16x32_bf16 v[208:211], a[100:103], a[188:191], v[208:211]\n"
"s_sub_u32 s82, s82, s81\n"
"v_mfma_f32_16x16x32_bf16 v[0:3], a[192:195], a[160:163], v[0:3]\n"
"v_mfma_f32_16x16x32_bf16 v[0:3], a[196:199], a[164:167], v[0:3]\n"
"v_mfma_f32_16x16x32_bf16 v[4:7], a[200:203], a[160:163], v[4:7]\n"
"s_waitcnt vmcnt(8) lgkmcnt(0)\n"
"v_mfma_f32_16x16x32_bf16 v[4:7], a[204:207], a[164:167], v[4:7]\n"
"s_barrier\n"
"v_mfma_f32_16x16x32_bf16 v[8:11], a[208:211], a[160:163], v[8:11]\n"
"s_add_u32 m0, s44, 65536\n"
"ds_read_b64_tr_b16 a[96:97], v244 offset:0\n"
"v_mfma_f32_16x16x32_bf16 v[8:11], a[212:215], a[164:167], v[8:11]\n"
"buffer_load_dwordx4 v240, s[20:23], s56 offen nt lds\n"
"v_mfma_f32_16x16x32_bf16 v[12:15], a[216:219], a[160:163], v[12:15]\n"
"s_lshr_b32 s82, s82, 31\n"
"ds_read_b64_tr_b16 a[98:99], v244 offset:512\n"
"v_mfma_f32_16x16x32_bf16 v[12:15], a[220:223], a[164:167], v[12:15]\n"
"s_add_u32 s82, s82, s60\n"
"ds_read_b64_tr_b16 a[100:101], v244 offset:8192\n"
"v_mfma_f32_16x16x32_bf16 v[16:19], a[224:227], a[160:163], v[16:19]\n"
"s_add_u32 s82, s82, 32\n"
"ds_read_b64_tr_b16 a[102:103], v244 offset:8704\n"
"v_mfma_f32_16x16x32_bf16 v[16:19], a[228:231], a[164:167], v[16:19]\n"
"v_mov_b32 v254, s82\n"
"ds_read_b64_tr_b16 a[104:105], v245 offset:0\n"
"v_mfma_f32_16x16x32_bf16 v[20:23], a[232:235], a[160:163], v[20:23]\n"
"v_lshlrev_b32_e64 v255, 2, s93\n"
"ds_read_b64_tr_b16 a[106:107], v245 offset:512\n"
"v_mfma_f32_16x16x32_bf16 v[20:23], a[236:239], a[164:167], v[20:23]\n"
"ds_read_b64_tr_b16 a[108:109], v245 offset:8192\n"
"v_mfma_f32_16x16x32_bf16 v[24:27], a[240:243], a[160:163], v[24:27]\n"
"s_add_u32 s40, s56, s52\n"
"ds_read_b64_tr_b16 a[110:111], v245 offset:8704\n"
"v_mfma_f32_16x16x32_bf16 v[24:27], a[244:247], a[164:167], v[24:27]\n"
"buffer_load_dwordx4 v240, s[20:23], s40 offen offset:1024 nt lds\n"
"v_mfma_f32_16x16x32_bf16 v[28:31], a[248:251], a[160:163], v[28:31]\n"
"ds_read_b128 a[160:163], v250 offset:0\n"
"v_mfma_f32_16x16x32_bf16 v[28:31], a[252:255], a[164:167], v[28:31]\n"
"s_mov_b64 exec, 1\n"
"global_store_dword v255, v254, s[6:7] offset:256 sc1\n"
"s_mov_b64 exec, -1\n"
"v_mfma_f32_16x16x32_bf16 v[60:63], a[248:251], a[168:171], v[60:63]\n"
"ds_read_b128 a[164:167], v251 offset:0\n"
"v_mfma_f32_16x16x32_bf16 v[60:63], a[252:255], a[172:175], v[60:63]\n"
"ds_read_b64_tr_b16 a[112:113], v246 offset:0\n"
"v_and_b32 v254, 0x20, v242\n"
"v_mfma_f32_16x16x32_bf16 v[56:59], a[240:243], a[168:171], v[56:59]\n"
"s_add_u32 s40, s56, s53\n"
"ds_read_b64_tr_b16 a[114:115], v246 offset:512\n"
"v_add_u32 v254, -1, v254\n"
"v_mfma_f32_16x16x32_bf16 v[56:59], a[244:247], a[172:175], v[56:59]\n"
"buffer_load_dwordx4 v240, s[20:23], s40 offen offset:2048 nt lds\n"
"v_mfma_f32_16x16x32_bf16 v[52:55], a[232:235], a[168:171], v[52:55]\n"
"ds_read_b64_tr_b16 a[116:117], v246 offset:8192\n"
"v_ashrrev_i32 v254, 31, v254\n"
"v_mfma_f32_16x16x32_bf16 v[52:55], a[236:239], a[172:175], v[52:55]\n"
"ds_read_b64_tr_b16 a[118:119], v246 offset:8704\n"
"v_and_b32 v254, 0x3f803f80, v254\n"
"v_mfma_f32_16x16x32_bf16 v[48:51], a[224:227], a[168:171], v[48:51]\n"
"ds_read_b64_tr_b16 a[120:121], v247 offset:0\n"
"v_mfma_f32_16x16x32_bf16 v[48:51], a[228:231], a[172:175], v[48:51]\n"
"ds_read_b64_tr_b16 a[122:123], v247 offset:512\n"
"v_mfma_f32_16x16x32_bf16 v[44:47], a[216:219], a[168:171], v[44:47]\n"
"s_add_u32 s40, s56, s54\n"
"ds_read_b64_tr_b16 a[124:125], v247 offset:8192\n"
"v_mfma_f32_16x16x32_bf16 v[44:47], a[220:223], a[172:175], v[44:47]\n"
"buffer_load_dwordx4 v240, s[20:23], s40 offen offset:3072 nt lds\n"
"v_mfma_f32_16x16x32_bf16 v[40:43], a[208:211], a[168:171], v[40:43]\n"
"s_add_u32 s55, s55, s59\n"
"ds_read_b64_tr_b16 a[126:127], v247 offset:8704\n"
"v_mfma_f32_16x16x32_bf16 v[40:43], a[212:215], a[172:175], v[40:43]\n"
"s_add_u32 s41, s55, s57\n"
"ds_read_b64_tr_b16 a[128:129], v244 offset:1024\n"
"v_mfma_f32_16x16x32_bf16 v[36:39], a[200:203], a[168:171], v[36:39]\n"
"s_and_b32 s41, s41, s58\n"
"ds_read_b64_tr_b16 a[130:131], v244 offset:1536\n"
"v_mfma_f32_16x16x32_bf16 v[36:39], a[204:207], a[172:175], v[36:39]\n"
"s_andn2_b32 s56, s55, s58\n"
"ds_read_b64_tr_b16 a[132:133], v244 offset:9216\n"
"v_mfma_f32_16x16x32_bf16 v[32:35], a[192:195], a[168:171], v[32:35]\n"
"s_or_b32 s56, s56, s41\n"
"ds_read_b64_tr_b16 a[134:135], v244 offset:9728\n"
"v_mfma_f32_16x16x32_bf16 v[32:35], a[196:199], a[172:175], v[32:35]\n"
"ds_read_b64_tr_b16 a[136:137], v245 offset:1024\n"
"v_mfma_f32_16x16x32_bf16 v[64:67], a[192:195], a[176:179], v[64:67]\n"
"s_add_u32 m0, s44, 16384\n"
"ds_read_b64_tr_b16 a[138:139], v245 offset:1536\n"
"v_mfma_f32_16x16x32_bf16 v[64:67], a[196:199], a[180:183], v[64:67]\n"
"buffer_load_dwordx4 v240, s[16:19], s56 offen nt lds\n"
"v_mfma_f32_16x16x32_bf16 v[68:71], a[200:203], a[176:179], v[68:71]\n"
"ds_read_b64_tr_b16 a[140:141], v245 offset:9216\n"
"v_mfma_f32_16x16x32_bf16 v[68:71], a[204:207], a[180:183], v[68:71]\n"
"ds_read_b64_tr_b16 a[142:143], v245 offset:9728\n"
"v_mfma_f32_16x16x32_bf16 v[72:75], a[208:211], a[176:179], v[72:75]\n"
"ds_read_b64_tr_b16 a[144:145], v246 offset:1024\n"
"v_mfma_f32_16x16x32_bf16 v[72:75], a[212:215], a[180:183], v[72:75]\n"
"ds_read_b64_tr_b16 a[146:147], v246 offset:1536\n"
"v_mfma_f32_16x16x32_bf16 v[76:79], a[216:219], a[176:179], v[76:79]\n"
"s_add_u32 s40, s56, s52\n"
"ds_read_b64_tr_b16 a[148:149], v246 offset:9216\n"
"v_mfma_f32_16x16x32_bf16 v[76:79], a[220:223], a[180:183], v[76:79]\n"
"buffer_load_dwordx4 v240, s[16:19], s40 offen offset:1024 nt lds\n"
"v_mfma_f32_16x16x32_bf16 v[88:91], a[240:243], a[176:179], v[88:91]\n"
"ds_read_b64_tr_b16 a[150:151], v246 offset:9728\n"
"v_mfma_f32_16x16x32_bf16 v[88:91], a[244:247], a[180:183], v[88:91]\n"
"ds_read_b64_tr_b16 a[152:153], v247 offset:1024\n"
"v_mfma_f32_16x16x32_bf16 v[92:95], a[248:251], a[176:179], v[92:95]\n"
"ds_read_b64_tr_b16 a[154:155], v247 offset:1536\n"
"v_mfma_f32_16x16x32_bf16 v[92:95], a[252:255], a[180:183], v[92:95]\n"
"ds_read_b64_tr_b16 a[156:157], v247 offset:9216\n"
"v_mfma_f32_16x16x32_bf16 v[80:83], a[224:227], a[176:179], v[80:83]\n"
"s_add_u32 s40, s56, s53\n"
"ds_read_b64_tr_b16 a[158:159], v247 offset:9728\n"
"v_mfma_f32_16x16x32_bf16 v[80:83], a[228:231], a[180:183], v[80:83]\n"
"buffer_load_dwordx4 v240, s[16:19], s40 offen offset:2048 nt lds\n"
"v_mfma_f32_16x16x32_bf16 v[84:87], a[232:235], a[176:179], v[84:87]\n"
"ds_read_b128 a[168:171], v250 offset:2048\n"
"v_mfma_f32_16x16x32_bf16 v[84:87], a[236:239], a[180:183], v[84:87]\n"
"ds_read_b128 a[172:175], v251 offset:2048\n"
"v_mfma_f32_16x16x32_bf16 v[108:111], a[232:235], a[184:187], v[108:111]\n"
"ds_read_b128 a[176:179], v250 offset:4096\n"
"v_mfma_f32_16x16x32_bf16 v[108:111], a[236:239], a[188:191], v[108:111]\n"
"ds_read_b128 a[180:183], v251 offset:4096\n"
"v_mfma_f32_16x16x32_bf16 v[104:107], a[224:227], a[184:187], v[104:107]\n"
"s_add_u32 s40, s56, s54\n"
"v_mfma_f32_16x16x32_bf16 v[104:107], a[228:231], a[188:191], v[104:107]\n"
"buffer_load_dwordx4 v240, s[16:19], s40 offen offset:3072 nt lds\n"
"v_mfma_f32_16x16x32_bf16 v[100:103], a[200:203], a[184:187], v[100:103]\n"
"v_mfma_f32_16x16x32_bf16 v[100:103], a[204:207], a[188:191], v[100:103]\n"
"v_mfma_f32_16x16x32_bf16 v[96:99], a[192:195], a[184:187], v[96:99]\n"
"ds_read_b128 a[184:187], v252 offset:0\n"
"v_mfma_f32_16x16x32_bf16 v[96:99], a[196:199], a[188:191], v[96:99]\n"
"s_waitcnt lgkmcnt(3)\n"
"ds_read_b128 a[188:191], v253 offset:0\n"
"v_mfma_f32_16x16x32_bf16 v[112:115], a[96:99], a[160:163], v[112:115]\n"
"v_mfma_f32_16x16x32_bf16 v[112:115], a[100:103], a[164:167], v[112:115]\n"
"v_mfma_f32_16x16x32_bf16 v[116:119], a[104:107], a[160:163], v[116:119]\n"
"s_waitcnt vmcnt(13)\n"
"v_mfma_f32_16x16x32_bf16 v[116:119], a[108:111], a[164:167], v[116:119]\n"
"s_barrier\n"
"v_mfma_f32_16x16x32_bf16 v[120:123], a[112:115], a[160:163], v[120:123]\n"
"s_add_u32 m0, s44, 81920\n"
"ds_read_b64_tr_b16 a[232:233], v245 offset:17408\n"
"v_mfma_f32_16x16x32_bf16 v[120:123], a[116:119], a[164:167], v[120:123]\n"
"buffer_load_dwordx4 v240, s[20:23], s56 offen nt lds\n"
"v_mfma_f32_16x16x32_bf16 v[124:127], a[120:123], a[160:163], v[124:127]\n"
"ds_read_b64_tr_b16 a[234:235], v245 offset:17920\n"
"v_mfma_f32_16x16x32_bf16 v[124:127], a[124:127], a[164:167], v[124:127]\n"
"ds_read_b64_tr_b16 a[236:237], v245 offset:25600\n"
"v_mfma_f32_16x16x32_bf16 v[128:131], a[128:131], a[160:163], v[128:131]\n"
"ds_read_b64_tr_b16 a[238:239], v245 offset:26112\n"
"v_mfma_f32_16x16x32_bf16 v[128:131], a[132:135], a[164:167], v[128:131]\n"
"ds_read_b64_tr_b16 a[224:225], v244 offset:17408\n"
"v_mfma_f32_16x16x32_bf16 v[132:135], a[136:139], a[160:163], v[132:135]\n"
"ds_read_b64_tr_b16 a[226:227], v244 offset:17920\n"
"v_mfma_f32_16x16x32_bf16 v[132:135], a[140:143], a[164:167], v[132:135]\n"
"ds_read_b64_tr_b16 a[192:193], v244 offset:16384\n"
"v_mfma_f32_16x16x32_bf16 v[136:139], a[144:147], a[160:163], v[136:139]\n"
"ds_read_b64_tr_b16 a[194:195], v244 offset:16896\n"
"v_mfma_f32_16x16x32_bf16 v[136:139], a[148:151], a[164:167], v[136:139]\n"
"ds_read_b64_tr_b16 a[196:197], v244 offset:24576\n"
"v_mfma_f32_16x16x32_bf16 v[140:143], a[152:155], a[160:163], v[140:143]\n"
"ds_read_b64_tr_b16 a[198:199], v244 offset:25088\n"
"v_mfma_f32_16x16x32_bf16 v[140:143], a[156:159], a[164:167], v[140:143]\n"
"s_waitcnt lgkmcnt(7)\n"
"ds_read_b64_tr_b16 a[200:201], v245 offset:16384\n"
"v_mfma_f32_16x16x32_bf16 v[220:223], a[136:139], a[184:187], v[220:223]\n"
"ds_read_b64_tr_b16 a[202:203], v245 offset:16896\n"
"v_mfma_f32_16x16x32_bf16 v[220:223], a[140:143], a[188:191], v[220:223]\n"
"ds_read_b64_tr_b16 a[204:205], v245 offset:24576\n"
"v_mfma_f32_16x16x32_bf16 v[216:219], a[128:131], a[184:187], v[216:219]\n"
"s_add_u32 s40, s56, s52\n"
"ds_read_b64_tr_b16 a[206:207], v245 offset:25088\n"
"v_mfma_f32_16x16x32_bf16 v[216:219], a[132:135], a[188:191], v[216:219]\n"
"buffer_load_dwordx4 v240, s[20:23], s40 offen offset:1024 nt lds\n"
"v_mfma_f32_16x16x32_bf16 v[212:215], a[104:107], a[184:187], v[212:215]\n"
"ds_read_b64_tr_b16 a[208:209], v246 offset:16384\n"
"v_mfma_f32_16x16x32_bf16 v[212:215], a[108:111], a[188:191], v[212:215]\n"
"v_pk_fma_f32 v[112:113], v[112:113], s[70:71], s[70:71] op_sel:[0,0,1] op_sel_hi:[1,0,1]\nv_pk_fma_f32 v[114:115], v[114:115], s[70:71], s[70:71] op_sel:[0,0,1] op_sel_hi:[1,0,1]\nv_pk_fma_f32 v[116:117], v[116:117], s[70:71], s[70:71] op_sel:[0,0,1] op_sel_hi:[1,0,1]\nv_pk_fma_f32 v[118:119], v[118:119], s[70:71], s[70:71] op_sel:[0,0,1] op_sel_hi:[1,0,1]\nv_pk_fma_f32 v[120:121], v[120:121], s[70:71], s[70:71] op_sel:[0,0,1] op_sel_hi:[1,0,1]\nv_pk_fma_f32 v[122:123], v[122:123], s[70:71], s[70:71] op_sel:[0,0,1] op_sel_hi:[1,0,1]\nv_pk_fma_f32 v[124:125], v[124:125], s[70:71], s[70:71] op_sel:[0,0,1] op_sel_hi:[1,0,1]\nv_pk_fma_f32 v[126:127], v[126:127], s[70:71], s[70:71] op_sel:[0,0,1] op_sel_hi:[1,0,1]\nv_pk_fma_f32 v[128:129], v[128:129], s[70:71], s[70:71] op_sel:[0,0,1] op_sel_hi:[1,0,1]\nv_pk_fma_f32 v[130:131], v[130:131], s[70:71], s[70:71] op_sel:[0,0,1] op_sel_hi:[1,0,1]\nv_pk_fma_f32 v[132:133], v[132:133], s[70:71], s[70:71] op_sel:[0,0,1] op_sel_hi:[1,0,1]\nv_pk_fma_f32 v[134:135], v[134:135], s[70:71], s[70:71] op_sel:[0,0,1] op_sel_hi:[1,0,1]\nv_pk_fma_f32 v[136:137], v[136:137], s[70:71], s[70:71] op_sel:[0,0,1] op_sel_hi:[1,0,1]\nv_pk_fma_f32 v[138:139], v[138:139], s[70:71], s[70:71] op_sel:[0,0,1] op_sel_hi:[1,0,1]\nv_pk_fma_f32 v[140:141], v[140:141], s[70:71], s[70:71] op_sel:[0,0,1] op_sel_hi:[1,0,1]\nv_pk_fma_f32 v[142:143], v[142:143], s[70:71], s[70:71] op_sel:[0,0,1] op_sel_hi:[1,0,1]\n"
"v_mfma_f32_16x16x32_bf16 v[208:211], a[96:99], a[184:187], v[208:211]\n"
"ds_read_b64_tr_b16 a[210:211], v246 offset:16896\n"
"v_cvt_pk_bf16_f32 v128, v128, v129\n"
"v_mfma_f32_16x16x32_bf16 v[208:211], a[100:103], a[188:191], v[208:211]\n"
"ds_read_b64_tr_b16 a[212:213], v246 offset:24576\n"
"v_cvt_pk_bf16_f32 v129, v130, v131\n"
"v_mfma_f32_16x16x32_bf16 v[172:175], a[152:155], a[168:171], v[172:175]\n"
"ds_read_b64_tr_b16 a[214:215], v246 offset:25088\n"
"v_cvt_pk_bf16_f32 v130, v132, v133\n"
"v_mfma_f32_16x16x32_bf16 v[172:175], a[156:159], a[172:175], v[172:175]\n"
"ds_read_b64_tr_b16 a[216:217], v247 offset:16384\n"
"v_cvt_pk_bf16_f32 v131, v134, v135\n"
"v_mfma_f32_16x16x32_bf16 v[168:171], a[144:147], a[168:171], v[168:171]\n"
"ds_read_b64_tr_b16 a[218:219], v247 offset:16896\n"
"v_cvt_pk_bf16_f32 v136, v136, v137\n"
"v_mfma_f32_16x16x32_bf16 v[168:171], a[148:151], a[172:175], v[168:171]\n"
"ds_read_b64_tr_b16 a[220:221], v247 offset:24576\n"
"v_cvt_pk_bf16_f32 v137, v138, v139\n"
"v_mfma_f32_16x16x32_bf16 v[164:167], a[136:139], a[168:171], v[164:167]\n"
"ds_read_b64_tr_b16 a[222:223], v247 offset:25088\n"
"v_cvt_pk_bf16_f32 v138, v140, v141\n"
"v_mfma_f32_16x16x32_bf16 v[164:167], a[140:143], a[172:175], v[164:167]\n"
"ds_read_b64_tr_b16 a[228:229], v244 offset:25600\n"
"v_cvt_pk_bf16_f32 v139, v142, v143\n"
"v_mfma_f32_16x16x32_bf16 v[160:163], a[128:131], a[168:171], v[160:163]\n"
"s_add_u32 s40, s56, s53\n"
"ds_read_b64_tr_b16 a[230:231], v244 offset:26112\n"
"v_cvt_pk_bf16_f32 v120, v120, v121\n"
"v_mfma_f32_16x16x32_bf16 v[160:163], a[132:135], a[172:175], v[160:163]\n"
"buffer_load_dwordx4 v240, s[20:23], s40 offen offset:2048 nt lds\n"
"v_mfma_f32_16x16x32_bf16 v[156:159], a[120:123], a[168:171], v[156:159]\n"
"ds_read_b64_tr_b16 a[240:241], v246 offset:17408\n"
"v_cvt_pk_bf16_f32 v121, v122, v123\n"
"v_mfma_f32_16x16x32_bf16 v[156:159], a[124:127], a[172:175], v[156:159]\n"
"ds_read_b64_tr_b16 a[242:243], v246 offset:17920\n"
"v_cvt_pk_bf16_f32 v122, v124, v125\n"
"v_mfma_f32_16x16x32_bf16 v[152:155], a[112:115], a[168:171], v[152:155]\n"
"ds_read_b64_tr_b16 a[244:245], v246 offset:25600\n"
"v_cvt_pk_bf16_f32 v123, v126, v127\n"
"v_mfma_f32_16x16x32_bf16 v[152:155], a[116:119], a[172:175], v[152:155]\n"
"ds_read_b64_tr_b16 a[246:247], v246 offset:26112\n"
"v_cvt_pk_bf16_f32 v112, v112, v113\n"
"v_mfma_f32_16x16x32_bf16 v[148:151], a[104:107], a[168:171], v[148:151]\n"
"ds_read_b64_tr_b16 a[248:249], v247 offset:17408\n"
"v_cvt_pk_bf16_f32 v113, v114, v115\n"
"v_mfma_f32_16x16x32_bf16 v[148:151], a[108:111], a[172:175], v[148:151]\n"
"ds_read_b64_tr_b16 a[250:251], v247 offset:17920\n"
"v_cvt_pk_bf16_f32 v114, v116, v117\n"
"v_mfma_f32_16x16x32_bf16 v[144:147], a[96:99], a[168:171], v[144:147]\n"
"ds_read_b64_tr_b16 a[252:253], v247 offset:25600\n"
"v_cvt_pk_bf16_f32 v115, v118, v119\n"
"v_mfma_f32_16x16x32_bf16 v[144:147], a[100:103], a[172:175], v[144:147]\n"
"v_pk_fma_f32 v[208:209], v[208:209], s[70:71], s[70:71] op_sel:[0,0,1] op_sel_hi:[1,0,1]\nv_pk_fma_f32 v[210:211], v[210:211], s[70:71], s[70:71] op_sel:[0,0,1] op_sel_hi:[1,0,1]\nv_pk_fma_f32 v[212:213], v[212:213], s[70:71], s[70:71] op_sel:[0,0,1] op_sel_hi:[1,0,1]\nv_pk_fma_f32 v[214:215], v[214:215], s[70:71], s[70:71] op_sel:[0,0,1] op_sel_hi:[1,0,1]\nv_pk_fma_f32 v[216:217], v[216:217], s[70:71], s[70:71] op_sel:[0,0,1] op_sel_hi:[1,0,1]\nv_pk_fma_f32 v[218:219], v[218:219], s[70:71], s[70:71] op_sel:[0,0,1] op_sel_hi:[1,0,1]\nv_pk_fma_f32 v[220:221], v[220:221], s[70:71], s[70:71] op_sel:[0,0,1] op_sel_hi:[1,0,1]\nv_pk_fma_f32 v[222:223], v[222:223], s[70:71], s[70:71] op_sel:[0,0,1] op_sel_hi:[1,0,1]\n"
"v_mfma_f32_16x16x32_bf16 v[176:179], a[96:99], a[176:179], v[176:179]\n"
"ds_read_b64_tr_b16 a[254:255], v247 offset:26112\n"
"v_cvt_pk_bf16_f32 v208, v208, v209\n"
"v_mfma_f32_16x16x32_bf16 v[176:179], a[100:103], a[180:183], v[176:179]\n"
"v_permlane16_swap_b32 v128, v130\n"
"v_mfma_f32_16x16x32_bf16 v[180:183], a[104:107], a[176:179], v[180:183]\n"
"v_permlane16_swap_b32 v129, v131\n"
"v_mfma_f32_16x16x32_bf16 v[180:183], a[108:111], a[180:183], v[180:183]\n"
"v_permlane16_swap_b32 v136, v138\n"
"v_mfma_f32_16x16x32_bf16 v[184:187], a[112:115], a[176:179], v[184:187]\n"
"s_add_u32 s40, s56, s54\n"
"v_permlane16_swap_b32 v137, v139\n"
"v_mfma_f32_16x16x32_bf16 v[184:187], a[116:119], a[180:183], v[184:187]\n"
"buffer_load_dwordx4 v240, s[20:23], s40 offen offset:3072 nt lds\n"
"v_mfma_f32_16x16x32_bf16 v[188:191], a[120:123], a[176:179], v[188:191]\n"
"s_add_u32 s55, s55, s59\n"
"v_pk_fma_f32 v[144:145], v[144:145], s[70:71], s[70:71] op_sel:[0,0,1] op_sel_hi:[1,0,1]\nv_pk_fma_f32 v[146:147], v[146:147], s[70:71], s[70:71] op_sel:[0,0,1] op_sel_hi:[1,0,1]\nv_pk_fma_f32 v[148:149], v[148:149], s[70:71], s[70:71] op_sel:[0,0,1] op_sel_hi:[1,0,1]\nv_pk_fma_f32 v[150:151], v[150:151], s[70:71], s[70:71] op_sel:[0,0,1] op_sel_hi:[1,0,1]\nv_pk_fma_f32 v[152:153], v[152:153], s[70:71], s[70:71] op_sel:[0,0,1] op_sel_hi:[1,0,1]\nv_pk_fma_f32 v[154:155], v[154:155], s[70:71], s[70:71] op_sel:[0,0,1] op_sel_hi:[1,0,1]\nv_pk_fma_f32 v[156:157], v[156:157], s[70:71], s[70:71] op_sel:[0,0,1] op_sel_hi:[1,0,1]\nv_pk_fma_f32 v[158:159], v[158:159], s[70:71], s[70:71] op_sel:[0,0,1] op_sel_hi:[1,0,1]\nv_pk_fma_f32 v[160:161], v[160:161], s[70:71], s[70:71] op_sel:[0,0,1] op_sel_hi:[1,0,1]\nv_pk_fma_f32 v[162:163], v[162:163], s[70:71], s[70:71] op_sel:[0,0,1] op_sel_hi:[1,0,1]\nv_pk_fma_f32 v[164:165], v[164:165], s[70:71], s[70:71] op_sel:[0,0,1] op_sel_hi:[1,0,1]\nv_pk_fma_f32 v[166:167], v[166:167], s[70:71], s[70:71] op_sel:[0,0,1] op_sel_hi:[1,0,1]\nv_pk_fma_f32 v[168:169], v[168:169], s[70:71], s[70:71] op_sel:[0,0,1] op_sel_hi:[1,0,1]\nv_pk_fma_f32 v[170:171], v[170:171], s[70:71], s[70:71] op_sel:[0,0,1] op_sel_hi:[1,0,1]\nv_pk_fma_f32 v[172:173], v[172:173], s[70:71], s[70:71] op_sel:[0,0,1] op_sel_hi:[1,0,1]\nv_pk_fma_f32 v[174:175], v[174:175], s[70:71], s[70:71] op_sel:[0,0,1] op_sel_hi:[1,0,1]\n"
"v_mfma_f32_16x16x32_bf16 v[188:191], a[124:127], a[180:183], v[188:191]\n"
"s_add_u32 s41, s55, s57\n"
"v_permlane16_swap_b32 v112, v114\n"
"v_mfma_f32_16x16x32_bf16 v[200:203], a[144:147], a[176:179], v[200:203]\n"
"s_and_b32 s41, s41, s58\n"
"v_permlane16_swap_b32 v113, v115\n"
"v_mfma_f32_16x16x32_bf16 v[200:203], a[148:151], a[180:183], v[200:203]\n"
"s_andn2_b32 s56, s55, s58\n"
"v_permlane16_swap_b32 v120, v122\n"
"v_mfma_f32_16x16x32_bf16 v[204:207], a[152:155], a[176:179], v[204:207]\n"
"s_or_b32 s56, s56, s41\n"
"v_permlane16_swap_b32 v121, v123\n"
"v_mfma_f32_16x16x32_bf16 v[204:207], a[156:159], a[180:183], v[204:207]\n"
"v_cvt_pk_bf16_f32 v209, v210, v211\n"
"v_cvt_pk_bf16_f32 v210, v212, v213\n"
"v_mfma_f32_16x16x32_bf16 v[192:195], a[128:131], a[176:179], v[192:195]\n"
"s_nop 0\n"
"v_permlane16_swap_b32 v208, v210\n"
"v_mfma_f32_16x16x32_bf16 v[192:195], a[132:135], a[180:183], v[192:195]\n"
"v_cvt_pk_bf16_f32 v211, v214, v215\n"
"v_cvt_pk_bf16_f32 v216, v216, v217\n"
"v_mfma_f32_16x16x32_bf16 v[196:199], a[136:139], a[176:179], v[196:199]\n"
"v_permlane16_swap_b32 v209, v211\n"
"v_mfma_f32_16x16x32_bf16 v[196:199], a[140:143], a[180:183], v[196:199]\n"
"ds_write_b128 v243, v[208:211] offset:8192\n"
"v_mfma_f32_16x16x32_bf16 v[108:111], a[232:235], a[184:187], v[108:111]\n"
"s_waitcnt lgkmcnt(1)\n"
"s_add_u32 m0, s44, 32768\n"
"v_cvt_pk_bf16_f32 v217, v218, v219\n"
"v_cvt_pk_bf16_f32 v218, v220, v221\n"
"v_mfma_f32_16x16x32_bf16 v[108:111], a[236:239], a[188:191], v[108:111]\n"
"buffer_load_dwordx4 v240, s[16:19], s56 offen nt lds\n"
"v_mfma_f32_16x16x32_bf16 v[104:107], a[224:227], a[184:187], v[104:107]\n"
"s_waitcnt vmcnt(14)\n"
"v_permlane16_swap_b32 v216, v218\n"
"v_mfma_f32_16x16x32_bf16 v[104:107], a[228:231], a[188:191], v[104:107]\n"
"s_barrier\n"
"v_mfma_f32_16x16x32_bf16 v[100:103], a[200:203], a[184:187], v[100:103]\n"
"v_cvt_pk_bf16_f32 v219, v222, v223\n"
"v_cvt_pk_bf16_f32 v144, v144, v145\n"
"v_mfma_f32_16x16x32_bf16 v[100:103], a[204:207], a[188:191], v[100:103]\n"
"s_mov_b32 s74, 0x41400000\n"
"v_permlane16_swap_b32 v217, v219\n"
"v_mfma_f32_16x16x32_bf16 v[96:99], a[192:195], a[184:187], v[96:99]\n"
"ds_write_b128 v243, v[216:219] offset:10240\n"
"v_mfma_f32_16x16x32_bf16 v[96:99], a[196:199], a[188:191], v[96:99]\n"
"v_pk_fma_f32 v[176:177], v[176:177], s[70:71], s[70:71] op_sel:[0,0,1] op_sel_hi:[1,0,1]\nv_pk_fma_f32 v[178:179], v[178:179], s[70:71], s[70:71] op_sel:[0,0,1] op_sel_hi:[1,0,1]\nv_pk_fma_f32 v[180:181], v[180:181], s[70:71], s[70:71] op_sel:[0,0,1] op_sel_hi:[1,0,1]\nv_pk_fma_f32 v[182:183], v[182:183], s[70:71], s[70:71] op_sel:[0,0,1] op_sel_hi:[1,0,1]\nv_pk_fma_f32 v[184:185], v[184:185], s[70:71], s[70:71] op_sel:[0,0,1] op_sel_hi:[1,0,1]\nv_pk_fma_f32 v[186:187], v[186:187], s[70:71], s[70:71] op_sel:[0,0,1] op_sel_hi:[1,0,1]\nv_pk_fma_f32 v[188:189], v[188:189], s[70:71], s[70:71] op_sel:[0,0,1] op_sel_hi:[1,0,1]\nv_pk_fma_f32 v[190:191], v[190:191], s[70:71], s[70:71] op_sel:[0,0,1] op_sel_hi:[1,0,1]\nv_pk_fma_f32 v[192:193], v[192:193], s[70:71], s[70:71] op_sel:[0,0,1] op_sel_hi:[1,0,1]\nv_pk_fma_f32 v[194:195], v[194:195], s[70:71], s[70:71] op_sel:[0,0,1] op_sel_hi:[1,0,1]\nv_pk_fma_f32 v[196:197], v[196:197], s[70:71], s[70:71] op_sel:[0,0,1] op_sel_hi:[1,0,1]\nv_pk_fma_f32 v[198:199], v[198:199], s[70:71], s[70:71] op_sel:[0,0,1] op_sel_hi:[1,0,1]\nv_pk_fma_f32 v[200:201], v[200:201], s[70:71], s[70:71] op_sel:[0,0,1] op_sel_hi:[1,0,1]\nv_pk_fma_f32 v[202:203], v[202:203], s[70:71], s[70:71] op_sel:[0,0,1] op_sel_hi:[1,0,1]\nv_pk_fma_f32 v[204:205], v[204:205], s[70:71], s[70:71] op_sel:[0,0,1] op_sel_hi:[1,0,1]\nv_pk_fma_f32 v[206:207], v[206:207], s[70:71], s[70:71] op_sel:[0,0,1] op_sel_hi:[1,0,1]\n"
"v_mfma_f32_16x16x32_bf16 v[0:3], a[192:195], a[160:163], v[0:3]\n"
"v_cvt_pk_bf16_f32 v145, v146, v147\n"
"v_cvt_pk_bf16_f32 v146, v148, v149\n"
"v_mfma_f32_16x16x32_bf16 v[0:3], a[196:199], a[164:167], v[0:3]\n"
"s_nop 0\n"
"v_permlane16_swap_b32 v144, v146\n"
"v_mfma_f32_16x16x32_bf16 v[4:7], a[200:203], a[160:163], v[4:7]\n"
"v_bfe_u32 v149, v242, 8, 2\n"
"v_lshl_add_u32 v149, v149, 4, s43\n"
"v_mfma_f32_16x16x32_bf16 v[4:7], a[204:207], a[164:167], v[4:7]\n"
"ds_read_b128 v[208:211], v149 offset:0\n"
"v_cvt_pk_bf16_f32 v147, v150, v151\n"
"v_mfma_f32_16x16x32_bf16 v[8:11], a[208:211], a[160:163], v[8:11]\n"
"ds_read_b128 v[216:219], v149 offset:1024\n"
"v_xor_b32 v150, 64, v149\n"
"v_mfma_f32_16x16x32_bf16 v[8:11], a[212:215], a[164:167], v[8:11]\n"
"ds_read_b128 v[212:215], v150 offset:0\n"
"v_cvt_pk_bf16_f32 v176, v176, v177\n"
"v_mfma_f32_16x16x32_bf16 v[12:15], a[216:219], a[160:163], v[12:15]\n"
"s_add_u32 s40, s56, s52\n"
"ds_read_b128 v[220:223], v150 offset:1024\n"
"v_cvt_pk_bf16_f32 v177, v178, v179\n"
"v_mfma_f32_16x16x32_bf16 v[12:15], a[220:223], a[164:167], v[12:15]\n"
"buffer_load_dwordx4 v240, s[16:19], s40 offen offset:1024 nt lds\n"
"v_mfma_f32_16x16x32_bf16 v[16:19], a[224:227], a[160:163], v[16:19]\n"
"v_permlane16_swap_b32 v145, v147\n"
"v_mfma_f32_16x16x32_bf16 v[16:19], a[228:231], a[164:167], v[16:19]\n"
"v_pk_fma_f32 v[96:97], v[96:97], s[70:71], s[70:71] op_sel:[0,0,1] op_sel_hi:[1,0,1]\nv_pk_fma_f32 v[98:99], v[98:99], s[70:71], s[70:71] op_sel:[0,0,1] op_sel_hi:[1,0,1]\nv_pk_fma_f32 v[100:101], v[100:101], s[70:71], s[70:71] op_sel:[0,0,1] op_sel_hi:[1,0,1]\nv_pk_fma_f32 v[102:103], v[102:103], s[70:71], s[70:71] op_sel:[0,0,1] op_sel_hi:[1,0,1]\nv_pk_fma_f32 v[104:105], v[104:105], s[70:71], s[70:71] op_sel:[0,0,1] op_sel_hi:[1,0,1]\nv_pk_fma_f32 v[106:107], v[106:107], s[70:71], s[70:71] op_sel:[0,0,1] op_sel_hi:[1,0,1]\nv_pk_fma_f32 v[108:109], v[108:109], s[70:71], s[70:71] op_sel:[0,0,1] op_sel_hi:[1,0,1]\nv_pk_fma_f32 v[110:111], v[110:111], s[70:71], s[70:71] op_sel:[0,0,1] op_sel_hi:[1,0,1]\n"
"v_mfma_f32_16x16x32_bf16 v[20:23], a[232:235], a[160:163], v[20:23]\n"
"v_cvt_pk_bf16_f32 v178, v180, v181\n"
"v_cvt_pk_bf16_f32 v179, v182, v183\n"
"v_mfma_f32_16x16x32_bf16 v[20:23], a[236:239], a[164:167], v[20:23]\n"
"v_permlane16_swap_b32 v176, v178\n"
"v_mfma_f32_16x16x32_bf16 v[24:27], a[240:243], a[160:163], v[24:27]\n"
"v_permlane16_swap_b32 v177, v179\n"
"v_mfma_f32_16x16x32_bf16 v[24:27], a[244:247], a[164:167], v[24:27]\n"
"v_cvt_pk_bf16_f32 v184, v184, v185\n"
"v_cvt_pk_bf16_f32 v185, v186, v187\n"
"v_mfma_f32_16x16x32_bf16 v[28:31], a[248:251], a[160:163], v[28:31]\n"
"v_cvt_pk_bf16_f32 v186, v188, v189\n"
"v_cvt_pk_bf16_f32 v187, v190, v191\n"
"v_mfma_f32_16x16x32_bf16 v[28:31], a[252:255], a[164:167], v[28:31]\n"
"v_permlane16_swap_b32 v184, v186\n"
"v_mfma_f32_16x16x32_bf16 v[60:63], a[248:251], a[168:171], v[60:63]\n"
"v_permlane16_swap_b32 v185, v187\n"
"v_mfma_f32_16x16x32_bf16 v[60:63], a[252:255], a[172:175], v[60:63]\n"
"v_accvgpr_write_b32 a132, v184\n"
"v_accvgpr_write_b32 a133, v185\n"
"v_mfma_f32_16x16x32_bf16 v[56:59], a[240:243], a[168:171], v[56:59]\n"
"s_add_u32 s40, s56, s53\n"
"v_accvgpr_write_b32 a134, v186\n"
"v_accvgpr_write_b32 a135, v187\n"
"v_mfma_f32_16x16x32_bf16 v[56:59], a[244:247], a[172:175], v[56:59]\n"
"buffer_load_dwordx4 v240, s[16:19], s40 offen offset:2048 nt lds\n"
"v_mfma_f32_16x16x32_bf16 v[52:55], a[232:235], a[168:171], v[52:55]\n"
"v_xor_b32 v148, 0x400, v243\n"
"v_cvt_pk_bf16_f32 v192, v192, v193\n"
"v_mfma_f32_16x16x32_bf16 v[52:55], a[236:239], a[172:175], v[52:55]\n"
"v_pk_fma_f32 v[0:1], v[0:1], s[70:71], s[70:71] op_sel:[0,0,1] op_sel_hi:[1,0,1]\nv_pk_fma_f32 v[2:3], v[2:3], s[70:71], s[70:71] op_sel:[0,0,1] op_sel_hi:[1,0,1]\nv_pk_fma_f32 v[4:5], v[4:5], s[70:71], s[70:71] op_sel:[0,0,1] op_sel_hi:[1,0,1]\nv_pk_fma_f32 v[6:7], v[6:7], s[70:71], s[70:71] op_sel:[0,0,1] op_sel_hi:[1,0,1]\nv_pk_fma_f32 v[8:9], v[8:9], s[70:71], s[70:71] op_sel:[0,0,1] op_sel_hi:[1,0,1]\nv_pk_fma_f32 v[10:11], v[10:11], s[70:71], s[70:71] op_sel:[0,0,1] op_sel_hi:[1,0,1]\nv_pk_fma_f32 v[12:13], v[12:13], s[70:71], s[70:71] op_sel:[0,0,1] op_sel_hi:[1,0,1]\nv_pk_fma_f32 v[14:15], v[14:15], s[70:71], s[70:71] op_sel:[0,0,1] op_sel_hi:[1,0,1]\nv_pk_fma_f32 v[16:17], v[16:17], s[70:71], s[70:71] op_sel:[0,0,1] op_sel_hi:[1,0,1]\nv_pk_fma_f32 v[18:19], v[18:19], s[70:71], s[70:71] op_sel:[0,0,1] op_sel_hi:[1,0,1]\nv_pk_fma_f32 v[20:21], v[20:21], s[70:71], s[70:71] op_sel:[0,0,1] op_sel_hi:[1,0,1]\nv_pk_fma_f32 v[22:23], v[22:23], s[70:71], s[70:71] op_sel:[0,0,1] op_sel_hi:[1,0,1]\nv_pk_fma_f32 v[24:25], v[24:25], s[70:71], s[70:71] op_sel:[0,0,1] op_sel_hi:[1,0,1]\nv_pk_fma_f32 v[26:27], v[26:27], s[70:71], s[70:71] op_sel:[0,0,1] op_sel_hi:[1,0,1]\nv_pk_fma_f32 v[28:29], v[28:29], s[70:71], s[70:71] op_sel:[0,0,1] op_sel_hi:[1,0,1]\nv_pk_fma_f32 v[30:31], v[30:31], s[70:71], s[70:71] op_sel:[0,0,1] op_sel_hi:[1,0,1]\n"
"v_mfma_f32_16x16x32_bf16 v[48:51], a[224:227], a[168:171], v[48:51]\n"
"v_cvt_pk_bf16_f32 v193, v194, v195\n"
"v_cvt_pk_bf16_f32 v194, v196, v197\n"
"v_mfma_f32_16x16x32_bf16 v[48:51], a[228:231], a[172:175], v[48:51]\n"
"s_nop 0\n"
"v_permlane16_swap_b32 v192, v194\n"
"v_mfma_f32_16x16x32_bf16 v[44:47], a[216:219], a[168:171], v[44:47]\n"
"v_cvt_pk_bf16_f32 v195, v198, v199\n"
"v_accvgpr_write_b32 a136, v192\n"
"v_mfma_f32_16x16x32_bf16 v[44:47], a[220:223], a[172:175], v[44:47]\n"
"v_permlane16_swap_b32 v193, v195\n"
"v_mfma_f32_16x16x32_bf16 v[40:43], a[208:211], a[168:171], v[40:43]\n"
"v_accvgpr_write_b32 a137, v193\n"
"v_accvgpr_write_b32 a138, v194\n"
"v_mfma_f32_16x16x32_bf16 v[40:43], a[212:215], a[172:175], v[40:43]\n"
"v_accvgpr_write_b32 a139, v195\n"
"v_cvt_pk_bf16_f32 v152, v152, v153\n"
"v_mfma_f32_16x16x32_bf16 v[36:39], a[200:203], a[168:171], v[36:39]\n"
"v_cvt_pk_bf16_f32 v153, v154, v155\n"
"v_cvt_pk_bf16_f32 v154, v156, v157\n"
"v_mfma_f32_16x16x32_bf16 v[36:39], a[204:207], a[172:175], v[36:39]\n"
"s_nop 0\n"
"v_permlane16_swap_b32 v152, v154\n"
"v_mfma_f32_16x16x32_bf16 v[32:35], a[192:195], a[168:171], v[32:35]\n"
"v_cvt_pk_bf16_f32 v155, v158, v159\n"
"v_accvgpr_write_b32 a116, v152\n"
"v_mfma_f32_16x16x32_bf16 v[32:35], a[196:199], a[172:175], v[32:35]\n"
"v_permlane16_swap_b32 v153, v155\n"
"v_mfma_f32_16x16x32_bf16 v[64:67], a[192:195], a[176:179], v[64:67]\n"
"s_add_u32 s40, s56, s54\n"
"ds_read_b128 a[192:195], v243 offset:0\n"
"v_accvgpr_write_b32 a117, v153\n"
"v_mfma_f32_16x16x32_bf16 v[64:67], a[196:199], a[180:183], v[64:67]\n"
"buffer_load_dwordx4 v240, s[16:19], s40 offen offset:3072 nt lds\n"
"v_mfma_f32_16x16x32_bf16 v[68:71], a[200:203], a[176:179], v[68:71]\n"
"ds_read_b128 a[196:199], v148 offset:0\n"
"v_accvgpr_write_b32 a118, v154\n"
"v_mfma_f32_16x16x32_bf16 v[68:71], a[204:207], a[180:183], v[68:71]\n"
"ds_read_b128 a[200:203], v243 offset:2048\n"
"v_accvgpr_write_b32 a119, v155\n"
"v_mfma_f32_16x16x32_bf16 v[72:75], a[208:211], a[176:179], v[72:75]\n"
"ds_read_b128 a[204:207], v148 offset:2048\n"
"v_cvt_pk_bf16_f32 v160, v160, v161\n"
"v_mfma_f32_16x16x32_bf16 v[72:75], a[212:215], a[180:183], v[72:75]\n"
"v_pk_fma_f32 v[32:33], v[32:33], s[70:71], s[70:71] op_sel:[0,0,1] op_sel_hi:[1,0,1]\nv_pk_fma_f32 v[34:35], v[34:35], s[70:71], s[70:71] op_sel:[0,0,1] op_sel_hi:[1,0,1]\nv_pk_fma_f32 v[36:37], v[36:37], s[70:71], s[70:71] op_sel:[0,0,1] op_sel_hi:[1,0,1]\nv_pk_fma_f32 v[38:39], v[38:39], s[70:71], s[70:71] op_sel:[0,0,1] op_sel_hi:[1,0,1]\nv_pk_fma_f32 v[40:41], v[40:41], s[70:71], s[70:71] op_sel:[0,0,1] op_sel_hi:[1,0,1]\nv_pk_fma_f32 v[42:43], v[42:43], s[70:71], s[70:71] op_sel:[0,0,1] op_sel_hi:[1,0,1]\nv_pk_fma_f32 v[44:45], v[44:45], s[70:71], s[70:71] op_sel:[0,0,1] op_sel_hi:[1,0,1]\nv_pk_fma_f32 v[46:47], v[46:47], s[70:71], s[70:71] op_sel:[0,0,1] op_sel_hi:[1,0,1]\nv_pk_fma_f32 v[48:49], v[48:49], s[70:71], s[70:71] op_sel:[0,0,1] op_sel_hi:[1,0,1]\nv_pk_fma_f32 v[50:51], v[50:51], s[70:71], s[70:71] op_sel:[0,0,1] op_sel_hi:[1,0,1]\nv_pk_fma_f32 v[52:53], v[52:53], s[70:71], s[70:71] op_sel:[0,0,1] op_sel_hi:[1,0,1]\nv_pk_fma_f32 v[54:55], v[54:55], s[70:71], s[70:71] op_sel:[0,0,1] op_sel_hi:[1,0,1]\nv_pk_fma_f32 v[56:57], v[56:57], s[70:71], s[70:71] op_sel:[0,0,1] op_sel_hi:[1,0,1]\nv_pk_fma_f32 v[58:59], v[58:59], s[70:71], s[70:71] op_sel:[0,0,1] op_sel_hi:[1,0,1]\nv_pk_fma_f32 v[60:61], v[60:61], s[70:71], s[70:71] op_sel:[0,0,1] op_sel_hi:[1,0,1]\nv_pk_fma_f32 v[62:63], v[62:63], s[70:71], s[70:71] op_sel:[0,0,1] op_sel_hi:[1,0,1]\n"
"v_mfma_f32_16x16x32_bf16 v[76:79], a[216:219], a[176:179], v[76:79]\n"
"v_cvt_pk_bf16_f32 v161, v162, v163\n"
"v_cvt_pk_bf16_f32 v162, v164, v165\n"
"v_mfma_f32_16x16x32_bf16 v[76:79], a[220:223], a[180:183], v[76:79]\n"
"s_nop 0\n"
"v_permlane16_swap_b32 v160, v162\n"
"v_mfma_f32_16x16x32_bf16 v[88:91], a[240:243], a[176:179], v[88:91]\n"
"v_cvt_pk_bf16_f32 v163, v166, v167\n"
"v_accvgpr_write_b32 a120, v160\n"
"v_mfma_f32_16x16x32_bf16 v[88:91], a[244:247], a[180:183], v[88:91]\n"
"v_permlane16_swap_b32 v161, v163\n"
"v_mfma_f32_16x16x32_bf16 v[92:95], a[248:251], a[176:179], v[92:95]\n"
"v_accvgpr_write_b32 a121, v161\n"
"v_accvgpr_write_b32 a122, v162\n"
"v_mfma_f32_16x16x32_bf16 v[92:95], a[252:255], a[180:183], v[92:95]\n"
"v_accvgpr_write_b32 a123, v163\n"
"v_cvt_pk_bf16_f32 v168, v168, v169\n"
"v_mfma_f32_16x16x32_bf16 v[80:83], a[224:227], a[176:179], v[80:83]\n"
"v_cvt_pk_bf16_f32 v169, v170, v171\n"
"v_cvt_pk_bf16_f32 v170, v172, v173\n"
"v_mfma_f32_16x16x32_bf16 v[80:83], a[228:231], a[180:183], v[80:83]\n"
"s_nop 0\n"
"v_permlane16_swap_b32 v168, v170\n"
"v_mfma_f32_16x16x32_bf16 v[84:87], a[232:235], a[176:179], v[84:87]\n"
"v_cvt_pk_bf16_f32 v171, v174, v175\n"
"v_accvgpr_write_b32 a124, v168\n"
"v_mfma_f32_16x16x32_bf16 v[84:87], a[236:239], a[180:183], v[84:87]\n"
"s_waitcnt lgkmcnt(0)\n"
"v_permlane16_swap_b32 v169, v171\n"
"v_mfma_f32_16x16x32_bf16 v[180:183], v[208:211], a[0:3], 0\n"
"v_accvgpr_write_b32 a125, v169\n"
"v_accvgpr_write_b32 a126, v170\n"
"v_mfma_f32_16x16x32_bf16 v[180:183], v[212:215], a[4:7], v[180:183]\n"
"v_accvgpr_write_b32 a127, v171\n"
"v_accvgpr_write_b32 a128, v176\n"
"v_mfma_f32_16x16x32_bf16 v[180:183], v[216:219], a[8:11], v[180:183]\n"
"v_accvgpr_write_b32 a129, v177\n"
"v_accvgpr_write_b32 a130, v178\n"
"v_mfma_f32_16x16x32_bf16 v[180:183], v[220:223], a[12:15], v[180:183]\n"
"v_accvgpr_write_b32 a131, v179\n"
"v_accvgpr_write_b32 a104, v128\n"
"v_mfma_f32_16x16x32_bf16 v[184:187], v[208:211], a[16:19], 0\n"
"v_accvgpr_write_b32 a105, v129\n"
"v_accvgpr_write_b32 a108, v136\n"
"v_mfma_f32_16x16x32_bf16 v[184:187], v[212:215], a[20:23], v[184:187]\n"
"v_pk_fma_f32 v[64:65], v[64:65], s[70:71], s[70:71] op_sel:[0,0,1] op_sel_hi:[1,0,1]\nv_pk_fma_f32 v[66:67], v[66:67], s[70:71], s[70:71] op_sel:[0,0,1] op_sel_hi:[1,0,1]\nv_pk_fma_f32 v[68:69], v[68:69], s[70:71], s[70:71] op_sel:[0,0,1] op_sel_hi:[1,0,1]\nv_pk_fma_f32 v[70:71], v[70:71], s[70:71], s[70:71] op_sel:[0,0,1] op_sel_hi:[1,0,1]\nv_pk_fma_f32 v[72:73], v[72:73], s[70:71], s[70:71] op_sel:[0,0,1] op_sel_hi:[1,0,1]\nv_pk_fma_f32 v[74:75], v[74:75], s[70:71], s[70:71] op_sel:[0,0,1] op_sel_hi:[1,0,1]\nv_pk_fma_f32 v[76:77], v[76:77], s[70:71], s[70:71] op_sel:[0,0,1] op_sel_hi:[1,0,1]\nv_pk_fma_f32 v[78:79], v[78:79], s[70:71], s[70:71] op_sel:[0,0,1] op_sel_hi:[1,0,1]\nv_pk_fma_f32 v[80:81], v[80:81], s[70:71], s[70:71] op_sel:[0,0,1] op_sel_hi:[1,0,1]\nv_pk_fma_f32 v[82:83], v[82:83], s[70:71], s[70:71] op_sel:[0,0,1] op_sel_hi:[1,0,1]\nv_pk_fma_f32 v[84:85], v[84:85], s[70:71], s[70:71] op_sel:[0,0,1] op_sel_hi:[1,0,1]\nv_pk_fma_f32 v[86:87], v[86:87], s[70:71], s[70:71] op_sel:[0,0,1] op_sel_hi:[1,0,1]\nv_pk_fma_f32 v[88:89], v[88:89], s[70:71], s[70:71] op_sel:[0,0,1] op_sel_hi:[1,0,1]\nv_pk_fma_f32 v[90:91], v[90:91], s[70:71], s[70:71] op_sel:[0,0,1] op_sel_hi:[1,0,1]\nv_pk_fma_f32 v[92:93], v[92:93], s[70:71], s[70:71] op_sel:[0,0,1] op_sel_hi:[1,0,1]\nv_pk_fma_f32 v[94:95], v[94:95], s[70:71], s[70:71] op_sel:[0,0,1] op_sel_hi:[1,0,1]\n"
"v_mfma_f32_16x16x32_bf16 v[184:187], v[216:219], a[24:27], v[184:187]\n"
"v_accvgpr_write_b32 a109, v137\n"
"v_accvgpr_write_b32 a112, v144\n"
"v_mfma_f32_16x16x32_bf16 v[184:187], v[220:223], a[28:31], v[184:187]\n"
"v_accvgpr_write_b32 a113, v145\n"
"v_accvgpr_write_b32 a106, v130\n"
"v_mfma_f32_16x16x32_bf16 v[188:191], v[208:211], a[32:35], 0\n"
"v_accvgpr_write_b32 a107, v131\n"
"v_accvgpr_write_b32 a110, v138\n"
"v_mfma_f32_16x16x32_bf16 v[188:191], v[212:215], a[36:39], v[188:191]\n"
"v_sub_f32 v180, s74, v180\n"
"v_sub_f32 v181, s74, v181\n"
"v_mfma_f32_16x16x32_bf16 v[188:191], v[216:219], a[40:43], v[188:191]\n"
"v_sub_f32 v182, s74, v182\n"
"v_sub_f32 v183, s74, v183\n"
"v_mfma_f32_16x16x32_bf16 v[188:191], v[220:223], a[44:47], v[188:191]\n"
"v_accvgpr_write_b32 a111, v139\n"
"v_cvt_pk_bf16_f32 v96, v96, v97\n"
"v_mfma_f32_16x16x32_bf16 v[192:195], v[208:211], a[192:195], 0\n"
"ds_read_b128 v[208:211], v228 offset:0\n"
"v_cvt_pk_bf16_f32 v97, v98, v99\n"
"v_mfma_f32_16x16x32_bf16 v[192:195], v[212:215], a[196:199], v[192:195]\n"
"ds_read_b128 v[212:215], v229 offset:0\n"
"v_sub_f32 v184, s74, v184\n"
"v_mfma_f32_16x16x32_bf16 v[192:195], v[216:219], a[200:203], v[192:195]\n"
"ds_read_b128 v[216:219], v228 offset:1024\n"
"v_sub_f32 v185, s74, v185\n"
"v_mfma_f32_16x16x32_bf16 v[192:195], v[220:223], a[204:207], v[192:195]\n"
"s_waitcnt lgkmcnt(2)\n"
"ds_read_b128 v[220:223], v229 offset:1024\n"
"v_sub_f32 v186, s74, v186\n"
"v_mfma_f32_16x16x32_bf16 v[152:155], v[208:211], a[0:3], v[180:183]\n"
"s_waitcnt lgkmcnt(2)\n"
"v_sub_f32 v187, s74, v187\n"
"v_cvt_pk_bf16_f32 v98, v100, v101\n"
"v_mfma_f32_16x16x32_bf16 v[152:155], v[212:215], a[4:7], v[152:155]\n"
"s_waitcnt lgkmcnt(1)\n"
"v_sub_f32 v188, s74, v188\n"
"v_sub_f32 v189, s74, v189\n"
"v_mfma_f32_16x16x32_bf16 v[152:155], v[216:219], a[8:11], v[152:155]\n"
"s_waitcnt lgkmcnt(0)\n"
"v_sub_f32 v190, s74, v190\n"
"v_sub_f32 v191, s74, v191\n"
"v_mfma_f32_16x16x32_bf16 v[152:155], v[220:223], a[12:15], v[152:155]\n"
"v_permlane16_swap_b32 v96, v98\n"
"v_mfma_f32_16x16x32_bf16 v[156:159], v[216:219], a[24:27], v[184:187]\n"
"v_cvt_pk_bf16_f32 v99, v102, v103\n"
"v_cvt_pk_bf16_f32 v104, v104, v105\n"
"v_mfma_f32_16x16x32_bf16 v[156:159], v[220:223], a[28:31], v[156:159]\n"
"v_sub_f32 v192, s74, v192\n"
"v_sub_f32 v193, s74, v193\n"
"v_mfma_f32_16x16x32_bf16 v[156:159], v[208:211], a[16:19], v[156:159]\n"
"v_sub_f32 v194, s74, v194\n"
"v_sub_f32 v195, s74, v195\n"
"v_mfma_f32_16x16x32_bf16 v[156:159], v[212:215], a[20:23], v[156:159]\n"
"v_permlane16_swap_b32 v97, v99\n"
"v_mfma_f32_16x16x32_bf16 v[160:163], v[208:211], a[32:35], v[188:191]\n"
"ds_write_b128 v243, v[96:99] offset:12288\n"
"v_mfma_f32_16x16x32_bf16 v[160:163], v[212:215], a[36:39], v[160:163]\n"
"v_exp_f32 v152, v152\n"
"v_mfma_f32_16x16x32_bf16 v[160:163], v[216:219], a[40:43], v[160:163]\n"
"v_exp_f32 v153, v153\n"
"v_mfma_f32_16x16x32_bf16 v[160:163], v[220:223], a[44:47], v[160:163]\n"
"v_exp_f32 v154, v154\n"
"v_mfma_f32_16x16x32_bf16 v[164:167], v[216:219], a[200:203], v[192:195]\n"
"ds_read_b128 v[216:219], v228 offset:5120\n"
"v_cvt_pk_bf16_f32 v128, v152, v153\n"
"v_mfma_f32_16x16x32_bf16 v[164:167], v[220:223], a[204:207], v[164:167]\n"
"ds_read_b128 v[220:223], v229 offset:5120\n"
"v_cvt_pk_bf16_f32 v105, v106, v107\n"
"v_mfma_f32_16x16x32_bf16 v[164:167], v[208:211], a[192:195], v[164:167]\n"
"ds_read_b128 v[208:211], v228 offset:4096\n"
"v_cvt_pk_bf16_f32 v106, v108, v109\n"
"v_mfma_f32_16x16x32_bf16 v[164:167], v[212:215], a[196:199], v[164:167]\n"
"s_waitcnt lgkmcnt(2)\n"
"ds_read_b128 v[212:215], v229 offset:4096\n"
"v_cvt_pk_bf16_f32 v107, v110, v111\n"
"v_mfma_f32_16x16x32_bf16 v[168:171], v[216:219], a[8:11], v[180:183]\n"
"s_waitcnt lgkmcnt(2)\n"
"v_exp_f32 v155, v155\n"
"v_mfma_f32_16x16x32_bf16 v[168:171], v[220:223], a[12:15], v[168:171]\n"
"s_waitcnt lgkmcnt(1)\n"
"v_exp_f32 v156, v156\n"
"v_mfma_f32_16x16x32_bf16 v[168:171], v[208:211], a[0:3], v[168:171]\n"
"s_waitcnt lgkmcnt(0)\n"
"v_exp_f32 v157, v157\n"
"v_mfma_f32_16x16x32_bf16 v[168:171], v[212:215], a[4:7], v[168:171]\n"
"v_exp_f32 v158, v158\n"
"v_mfma_f32_16x16x32_bf16 v[172:175], v[208:211], a[16:19], v[184:187]\n"
"v_exp_f32 v159, v159\n"
"v_mfma_f32_16x16x32_bf16 v[172:175], v[212:215], a[20:23], v[172:175]\n"
"v_exp_f32 v160, v160\n"
"v_mfma_f32_16x16x32_bf16 v[172:175], v[216:219], a[24:27], v[172:175]\n"
"v_exp_f32 v161, v161\n"
"v_mfma_f32_16x16x32_bf16 v[172:175], v[220:223], a[28:31], v[172:175]\n"
"v_exp_f32 v162, v162\n"
"v_mfma_f32_16x16x32_bf16 v[176:179], v[216:219], a[40:43], v[188:191]\n"
"v_exp_f32 v163, v163\n"
"v_mfma_f32_16x16x32_bf16 v[176:179], v[220:223], a[44:47], v[176:179]\n"
"v_cvt_pk_bf16_f32 v129, v154, v155\n"
"v_cvt_pk_bf16_f32 v136, v156, v157\n"
"v_mfma_f32_16x16x32_bf16 v[176:179], v[208:211], a[32:35], v[176:179]\n"
"v_exp_f32 v164, v164\n"
"v_mfma_f32_16x16x32_bf16 v[176:179], v[212:215], a[36:39], v[176:179]\n"
"v_exp_f32 v165, v165\n"
"v_mfma_f32_16x16x32_bf16 v[152:155], v[208:211], a[192:195], v[192:195]\n"
"ds_read_b128 v[208:211], v230 offset:0\n"
"v_cvt_pk_bf16_f32 v137, v158, v159\n"
"v_mfma_f32_16x16x32_bf16 v[152:155], v[212:215], a[196:199], v[152:155]\n"
"ds_read_b128 v[212:215], v231 offset:0\n"
"v_cvt_pk_bf16_f32 v144, v160, v161\n"
"v_mfma_f32_16x16x32_bf16 v[152:155], v[216:219], a[200:203], v[152:155]\n"
"ds_read_b128 v[216:219], v230 offset:1024\n"
"v_cvt_pk_bf16_f32 v145, v162, v163\n"
"v_mfma_f32_16x16x32_bf16 v[152:155], v[220:223], a[204:207], v[152:155]\n"
"s_waitcnt lgkmcnt(2)\n"
"ds_read_b128 v[220:223], v231 offset:1024\n"
"v_cvt_pk_bf16_f32 v124, v164, v165\n"
"v_mfma_f32_16x16x32_bf16 v[156:159], v[208:211], a[0:3], v[180:183]\n"
"s_waitcnt lgkmcnt(2)\n"
"v_exp_f32 v166, v166\n"
"v_mfma_f32_16x16x32_bf16 v[156:159], v[212:215], a[4:7], v[156:159]\n"
"s_waitcnt lgkmcnt(1)\n"
"v_exp_f32 v167, v167\n"
"v_mfma_f32_16x16x32_bf16 v[156:159], v[216:219], a[8:11], v[156:159]\n"
"s_waitcnt lgkmcnt(0)\n"
"v_exp_f32 v168, v168\n"
"v_mfma_f32_16x16x32_bf16 v[156:159], v[220:223], a[12:15], v[156:159]\n"
"v_exp_f32 v169, v169\n"
"v_mfma_f32_16x16x32_bf16 v[160:163], v[216:219], a[24:27], v[184:187]\n"
"v_exp_f32 v170, v170\n"
"v_mfma_f32_16x16x32_bf16 v[160:163], v[220:223], a[28:31], v[160:163]\n"
"v_exp_f32 v171, v171\n"
"v_mfma_f32_16x16x32_bf16 v[160:163], v[208:211], a[16:19], v[160:163]\n"
"v_cvt_pk_bf16_f32 v125, v166, v167\n"
"v_cvt_pk_bf16_f32 v130, v168, v169\n"
"v_mfma_f32_16x16x32_bf16 v[160:163], v[212:215], a[20:23], v[160:163]\n"
"v_exp_f32 v172, v172\n"
"v_mfma_f32_16x16x32_bf16 v[164:167], v[208:211], a[32:35], v[188:191]\n"
"ds_read_b128 v[208:211], v230 offset:4096\n"
"v_cvt_pk_bf16_f32 v131, v170, v171\n"
"v_mfma_f32_16x16x32_bf16 v[164:167], v[212:215], a[36:39], v[164:167]\n"
"ds_read_b128 v[212:215], v231 offset:4096\n"
"v_accvgpr_write_b32 a114, v146\n"
"v_mfma_f32_16x16x32_bf16 v[164:167], v[216:219], a[40:43], v[164:167]\n"
"ds_read_b128 v[216:219], v230 offset:5120\n"
"v_accvgpr_write_b32 a115, v147\n"
"v_mfma_f32_16x16x32_bf16 v[164:167], v[220:223], a[44:47], v[164:167]\n"
"s_waitcnt lgkmcnt(2)\n"
"ds_read_b128 v[220:223], v231 offset:5120\n"
"v_cvt_pk_bf16_f32 v8, v8, v9\n"
"v_mfma_f32_16x16x32_bf16 v[168:171], v[208:211], a[0:3], v[180:183]\n"
"s_waitcnt lgkmcnt(2)\n"
"v_exp_f32 v173, v173\n"
"v_mfma_f32_16x16x32_bf16 v[168:171], v[212:215], a[4:7], v[168:171]\n"
"s_waitcnt lgkmcnt(1)\n"
"v_exp_f32 v174, v174\n"
"v_mfma_f32_16x16x32_bf16 v[168:171], v[216:219], a[8:11], v[168:171]\n"
"s_waitcnt lgkmcnt(0)\n"
"v_exp_f32 v175, v175\n"
"v_mfma_f32_16x16x32_bf16 v[168:171], v[220:223], a[12:15], v[168:171]\n"
"v_cvt_pk_bf16_f32 v138, v172, v173\n"
"v_cvt_pk_bf16_f32 v139, v174, v175\n"
"v_mfma_f32_16x16x32_bf16 v[172:175], v[216:219], a[24:27], v[184:187]\n"
"v_permlane16_swap_b32 v104, v106\n"
"v_mfma_f32_16x16x32_bf16 v[172:175], v[220:223], a[28:31], v[172:175]\n"
"v_permlane16_swap_b32 v105, v107\n"
"v_mfma_f32_16x16x32_bf16 v[172:175], v[208:211], a[16:19], v[172:175]\n"
"ds_write_b128 v243, v[104:107] offset:14336\n"
"v_exp_f32 v176, v176\n"
"v_exp_f32 v177, v177\n"
"v_exp_f32 v178, v178\n"
"v_exp_f32 v179, v179\n"
"v_mfma_f32_16x16x32_bf16 v[172:175], v[212:215], a[20:23], v[172:175]\n"
"s_cbranch_vccnz LPCE0_%=\nLPKE0_%=:\n"
"v_cvt_pk_bf16_f32 v146, v176, v177\n"
"v_cvt_pk_bf16_f32 v147, v178, v179\n"
"v_mfma_f32_16x16x32_bf16 v[176:179], v[208:211], a[32:35], v[188:191]\n"
"v_exp_f32 v152, v152\n"
"v_mfma_f32_16x16x32_bf16 v[176:179], v[212:215], a[36:39], v[176:179]\n"
"v_exp_f32 v153, v153\n"
"v_mfma_f32_16x16x32_bf16 v[176:179], v[216:219], a[40:43], v[176:179]\n"
"v_exp_f32 v154, v154\n"
"v_mfma_f32_16x16x32_bf16 v[176:179], v[220:223], a[44:47], v[176:179]\n"
"v_exp_f32 v155, v155\n"
"s_waitcnt vmcnt(8) lgkmcnt(0)\n"
"s_barrier\n"
"ds_read_b128 v[208:211], v228 offset:16384\n"
"ds_read_b128 v[212:215], v229 offset:16384\n"
"ds_read_b128 v[216:219], v228 offset:17408\n"
"ds_read_b128 v[220:223], v229 offset:17408\n"
"v_cvt_pk_bf16_f32 v126, v152, v153\n"
"v_cvt_pk_bf16_f32 v127, v154, v155\n"
"s_waitcnt lgkmcnt(3)\n"
"v_mfma_f32_16x16x32_bf16 v[152:155], v[208:211], a[0:3], v[180:183]\n"
"v_exp_f32 v156, v156\n"
"s_waitcnt lgkmcnt(2)\n"
"v_mfma_f32_16x16x32_bf16 v[152:155], v[212:215], a[4:7], v[152:155]\n"
"v_exp_f32 v157, v157\n"
"s_waitcnt lgkmcnt(1)\n"
"v_mfma_f32_16x16x32_bf16 v[152:155], v[216:219], a[8:11], v[152:155]\n"
"s_waitcnt lgkmcnt(0)\n"
"s_add_u32 m0, s44, 98304\n"
"v_exp_f32 v158, v158\n"
"v_exp_f32 v159, v159\n"
"v_mfma_f32_16x16x32_bf16 v[152:155], v[220:223], a[12:15], v[152:155]\n"
"buffer_load_dwordx4 v240, s[20:23], s56 offen nt lds\n"
"v_cvt_pk_bf16_f32 v132, v156, v157\n"
"v_cvt_pk_bf16_f32 v133, v158, v159\n"
"v_mfma_f32_16x16x32_bf16 v[156:159], v[216:219], a[24:27], v[184:187]\n"
"v_exp_f32 v160, v160\n"
"v_mfma_f32_16x16x32_bf16 v[156:159], v[220:223], a[28:31], v[156:159]\n"
"v_exp_f32 v161, v161\n"
"v_mfma_f32_16x16x32_bf16 v[156:159], v[208:211], a[16:19], v[156:159]\n"
"v_exp_f32 v162, v162\n"
"v_exp_f32 v163, v163\n"
"v_mfma_f32_16x16x32_bf16 v[156:159], v[212:215], a[20:23], v[156:159]\n"
"v_cvt_pk_bf16_f32 v140, v160, v161\n"
"v_cvt_pk_bf16_f32 v141, v162, v163\n"
"v_mfma_f32_16x16x32_bf16 v[160:163], v[208:211], a[32:35], v[188:191]\n"
"v_exp_f32 v164, v164\n"
"v_mfma_f32_16x16x32_bf16 v[160:163], v[212:215], a[36:39], v[160:163]\n"
"v_exp_f32 v165, v165\n"
"v_mfma_f32_16x16x32_bf16 v[160:163], v[216:219], a[40:43], v[160:163]\n"
"ds_read_b64_tr_b16 a[244:245], v234 offset:0\n"
"ds_read_b64_tr_b16 a[246:247], v234 offset:4096\n"
"v_exp_f32 v166, v166\n"
"v_exp_f32 v167, v167\n"
"v_mfma_f32_16x16x32_bf16 v[160:163], v[220:223], a[44:47], v[160:163]\n"
"v_cvt_pk_bf16_f32 v148, v164, v165\n"
"v_cvt_pk_bf16_f32 v149, v166, v167\n"
"v_mfma_f32_16x16x32_bf16 v[164:167], v[216:219], a[200:203], v[192:195]\n"
"ds_read_b64_tr_b16 a[248:249], v238 offset:0\n"
"ds_read_b64_tr_b16 a[250:251], v238 offset:4096\n"
"v_mfma_f32_16x16x32_bf16 v[164:167], v[220:223], a[204:207], v[164:167]\n"
"v_exp_f32 v168, v168\n"
"v_mfma_f32_16x16x32_bf16 v[164:167], v[208:211], a[192:195], v[164:167]\n"
"s_add_u32 s40, s56, s52\n"
"v_exp_f32 v169, v169\n"
"v_mfma_f32_16x16x32_bf16 v[164:167], v[212:215], a[196:199], v[164:167]\n"
"s_waitcnt lgkmcnt(2)\n"
"buffer_load_dwordx4 v240, s[20:23], s40 offen offset:1024 nt lds\n"
"v_cvt_pk_bf16_f32 v9, v10, v11\n"
"v_cvt_pk_bf16_f32 v10, v12, v13\n"
"v_cvt_pk_bf16_f32 v11, v14, v15\n"
"v_exp_f32 v170, v170\n"
"v_exp_f32 v171, v171\n"
"v_mfma_f32_16x16x32_bf16 a[216:219], v[124:127], a[244:247], 0\n"
"v_permlane16_swap_b32 v8, v10\n"
"v_permlane16_swap_b32 v9, v11\n"
"v_accvgpr_write_b32 a148, v8\n"
"v_accvgpr_write_b32 a149, v9\n"
"v_accvgpr_write_b32 a150, v10\n"
"v_accvgpr_write_b32 a151, v11\n"
"v_cvt_pk_bf16_f32 v134, v168, v169\n"
"v_cvt_pk_bf16_f32 v135, v170, v171\n"
"v_mfma_f32_16x16x32_bf16 v[8:11], v[128:131], a[244:247], 0\n"
"s_waitcnt lgkmcnt(0)\n"
"ds_read_b64_tr_b16 v[224:225], v239 offset:0\n"
"ds_read_b64_tr_b16 v[226:227], v239 offset:4096\n"
"v_cvt_pk_bf16_f32 v40, v40, v41\n"
"v_cvt_pk_bf16_f32 v41, v42, v43\n"
"v_cvt_pk_bf16_f32 v42, v44, v45\n"
"v_cvt_pk_bf16_f32 v43, v46, v47\n"
"v_exp_f32 v172, v172\n"
"v_exp_f32 v173, v173\n"
"v_exp_f32 v174, v174\n"
"v_exp_f32 v175, v175\n"
"v_mfma_f32_16x16x32_bf16 v[8:11], v[132:135], a[248:251], v[8:11]\n"
"ds_read_b64_tr_b16 a[252:253], v235 offset:0\n"
"ds_read_b64_tr_b16 a[254:255], v235 offset:4096\n"
"v_permlane16_swap_b32 v40, v42\n"
"v_permlane16_swap_b32 v41, v43\n"
"v_accvgpr_write_b32 a164, v40\n"
"v_accvgpr_write_b32 a165, v41\n"
"v_accvgpr_write_b32 a166, v42\n"
"v_accvgpr_write_b32 a167, v43\n"
"v_cvt_pk_bf16_f32 v142, v172, v173\n"
"v_cvt_pk_bf16_f32 v143, v174, v175\n"
"v_mfma_f32_16x16x32_bf16 v[40:43], v[136:139], a[244:247], 0\n"
"v_cvt_pk_bf16_f32 v72, v72, v73\n"
"v_cvt_pk_bf16_f32 v73, v74, v75\n"
"v_cvt_pk_bf16_f32 v74, v76, v77\n"
"v_cvt_pk_bf16_f32 v75, v78, v79\n"
"v_exp_f32 v176, v176\n"
"v_exp_f32 v177, v177\n"
"v_exp_f32 v178, v178\n"
"v_exp_f32 v179, v179\n"
"v_mfma_f32_16x16x32_bf16 v[40:43], v[140:143], a[248:251], v[40:43]\n"
"v_permlane16_swap_b32 v72, v74\n"
"v_permlane16_swap_b32 v73, v75\n"
"v_accvgpr_write_b32 a180, v72\n"
"v_accvgpr_write_b32 a181, v73\n"
"v_accvgpr_write_b32 a182, v74\n"
"v_accvgpr_write_b32 a183, v75\n"
"v_cvt_pk_bf16_f32 v150, v176, v177\n"
"v_cvt_pk_bf16_f32 v151, v178, v179\n"
"v_mfma_f32_16x16x32_bf16 v[72:75], v[144:147], a[244:247], 0\n"
"ds_read_b128 v[216:219], v228 offset:21504\n"
"v_cvt_pk_bf16_f32 v200, v200, v201\n"
"v_mfma_f32_16x16x32_bf16 v[72:75], v[148:151], a[248:251], v[72:75]\n"
"s_waitcnt lgkmcnt(3)\n"
"ds_read_b128 v[220:223], v229 offset:21504\n"
"v_cvt_pk_bf16_f32 v201, v202, v203\n"
"v_mfma_f32_16x16x32_bf16 v[76:79], v[148:151], v[224:227], 0\n"
"s_waitcnt lgkmcnt(2)\n"
"ds_read_b128 v[208:211], v228 offset:20480\n"
"v_cvt_pk_bf16_f32 v202, v204, v205\n"
"v_mfma_f32_16x16x32_bf16 v[76:79], v[144:147], a[252:255], v[76:79]\n"
"ds_read_b128 v[212:215], v229 offset:20480\n"
"v_cvt_pk_bf16_f32 v203, v206, v207\n"
"v_mfma_f32_16x16x32_bf16 v[44:47], v[140:143], v[224:227], 0\n"
"s_add_u32 s40, s56, s53\n"
"ds_read_b64_tr_b16 a[244:245], v234 offset:1024\n"
"v_accvgpr_write_b32 a240, v254\n"
"v_mfma_f32_16x16x32_bf16 v[44:47], v[136:139], a[252:255], v[44:47]\n"
"buffer_load_dwordx4 v240, s[20:23], s40 offen offset:2048 nt lds\n"
"v_mfma_f32_16x16x32_bf16 v[12:15], v[132:135], v[224:227], 0\n"
"v_accvgpr_write_b32 a241, v254\n"
"v_accvgpr_write_b32 a242, v254\n"
"v_mfma_f32_16x16x32_bf16 v[12:15], v[128:131], a[252:255], v[12:15]\n"
"v_accvgpr_write_b32 a243, v254\n"
"v_mov_b32 v196, 0\n"
"v_mov_b32 v197, 0\n"
"v_mov_b32 v198, 0\n"
"v_mov_b32 v199, 0\n"
"v_mfma_f32_16x16x32_bf16 a[220:223], v[124:127], a[252:255], 0\n"
"v_permlane16_swap_b32 v200, v202\n"
"v_permlane16_swap_b32 v201, v203\n"
"v_accvgpr_write_b32 a142, v202\n"
"v_accvgpr_write_b32 a143, v203\n"
"v_mov_b32 v202, 0\n"
"v_mov_b32 v203, 0\n"
"v_mov_b32 v204, 0\n"
"v_mov_b32 v205, 0\n"
"v_smfmac_f32_16x16x64_bf16 v[196:199], a[240:243], v[128:135], v241\n"
"ds_read_b64_tr_b16 a[246:247], v234 offset:5120\n"
"v_cvt_pk_bf16_f32 v24, v24, v25\n"
"v_mfma_f32_16x16x32_bf16 v[202:205], a[240:243], v[124:127], v[202:205]\n"
"s_waitcnt lgkmcnt(3)\n"
"ds_read_b64_tr_b16 a[248:249], v238 offset:1024\n"
"v_cvt_pk_bf16_f32 v25, v26, v27\n"
"v_mfma_f32_16x16x32_bf16 v[168:171], v[216:219], a[8:11], v[180:183]\n"
"ds_read_b64_tr_b16 a[250:251], v238 offset:5120\n"
"v_cvt_pk_bf16_f32 v26, v28, v29\n"
"v_mfma_f32_16x16x32_bf16 v[168:171], v[220:223], a[12:15], v[168:171]\n"
"ds_read_b64_tr_b16 v[224:225], v239 offset:1024\n"
"v_cvt_pk_bf16_f32 v27, v30, v31\n"
"v_mfma_f32_16x16x32_bf16 v[168:171], v[208:211], a[0:3], v[168:171]\n"
"s_waitcnt lgkmcnt(4)\n"
"ds_read_b64_tr_b16 v[226:227], v239 offset:5120\n"
"v_cvt_pk_bf16_f32 v56, v56, v57\n"
"v_mfma_f32_16x16x32_bf16 v[168:171], v[212:215], a[4:7], v[168:171]\n"
"ds_read_b64_tr_b16 a[252:253], v235 offset:1024\n"
"v_cvt_pk_bf16_f32 v57, v58, v59\n"
"v_mfma_f32_16x16x32_bf16 v[172:175], v[208:211], a[16:19], v[184:187]\n"
"v_exp_f32 v152, v152\n"
"v_mfma_f32_16x16x32_bf16 v[172:175], v[212:215], a[20:23], v[172:175]\n"
"ds_read_b64_tr_b16 a[254:255], v235 offset:5120\n"
"v_cvt_pk_bf16_f32 v58, v60, v61\n"
"v_mfma_f32_16x16x32_bf16 v[172:175], v[216:219], a[24:27], v[172:175]\n"
"s_add_u32 s40, s56, s54\n"
"v_exp_f32 v153, v153\n"
"v_mfma_f32_16x16x32_bf16 v[172:175], v[220:223], a[28:31], v[172:175]\n"
"buffer_load_dwordx4 v240, s[20:23], s40 offen offset:3072 nt lds\n"
"v_mfma_f32_16x16x32_bf16 v[176:179], v[216:219], a[40:43], v[188:191]\n"
"s_add_u32 s55, s55, s59\n"
"v_exp_f32 v154, v154\n"
"v_mfma_f32_16x16x32_bf16 v[176:179], v[220:223], a[44:47], v[176:179]\n"
"s_add_u32 s41, s55, s57\n"
"v_exp_f32 v155, v155\n"
"v_mfma_f32_16x16x32_bf16 v[176:179], v[208:211], a[32:35], v[176:179]\n"
"s_and_b32 s41, s41, s58\n"
"v_cvt_pk_bf16_f32 v96, v152, v153\n"
"v_cvt_pk_bf16_f32 v97, v154, v155\n"
"v_mfma_f32_16x16x32_bf16 v[176:179], v[212:215], a[36:39], v[176:179]\n"
"s_andn2_b32 s56, s55, s58\n"
"v_permlane16_swap_b32 v24, v26\n"
"v_mfma_f32_16x16x32_bf16 v[152:155], v[208:211], a[192:195], v[192:195]\n"
"s_or_b32 s56, s56, s41\n"
"ds_read_b128 v[208:211], v230 offset:16384\n"
"v_accvgpr_write_b32 a156, v24\n"
"v_mfma_f32_16x16x32_bf16 v[152:155], v[212:215], a[196:199], v[152:155]\n"
"ds_read_b128 v[212:215], v231 offset:16384\n"
"v_accvgpr_write_b32 a158, v26\n"
"v_mfma_f32_16x16x32_bf16 v[152:155], v[216:219], a[200:203], v[152:155]\n"
"ds_read_b128 v[216:219], v230 offset:17408\n"
"v_cvt_pk_bf16_f32 v59, v62, v63\n"
"v_mfma_f32_16x16x32_bf16 v[152:155], v[220:223], a[204:207], v[152:155]\n"
"s_waitcnt lgkmcnt(3)\n"
"v_permlane16_swap_b32 v25, v27\n"
"v_mfma_f32_16x16x32_bf16 a[232:235], v[124:127], a[244:247], 0\n"
"v_accvgpr_write_b32 a157, v25\n"
"v_accvgpr_write_b32 a159, v27\n"
"v_mfma_f32_16x16x32_bf16 v[24:27], v[128:131], a[244:247], 0\n"
"s_add_u32 m0, s44, 49152\n"
"v_permlane16_swap_b32 v56, v58\n"
"v_mfma_f32_16x16x32_bf16 v[24:27], v[132:135], a[248:251], v[24:27]\n"
"buffer_load_dwordx4 v240, s[16:19], s56 offen nt lds\n"
"v_permlane16_swap_b32 v57, v59\n"
"v_accvgpr_write_b32 a172, v56\n"
"v_accvgpr_write_b32 a173, v57\n"
"v_accvgpr_write_b32 a174, v58\n"
"v_accvgpr_write_b32 a175, v59\n"
"v_mfma_f32_16x16x32_bf16 v[56:59], v[136:139], a[244:247], 0\n"
"v_cvt_pk_bf16_f32 v88, v88, v89\n"
"v_cvt_pk_bf16_f32 v89, v90, v91\n"
"v_cvt_pk_bf16_f32 v90, v92, v93\n"
"v_cvt_pk_bf16_f32 v91, v94, v95\n"
"v_mfma_f32_16x16x32_bf16 v[56:59], v[140:143], a[248:251], v[56:59]\n"
"v_permlane16_swap_b32 v88, v90\n"
"v_permlane16_swap_b32 v89, v91\n"
"v_accvgpr_write_b32 a188, v88\n"
"v_accvgpr_write_b32 a189, v89\n"
"v_accvgpr_write_b32 a190, v90\n"
"v_accvgpr_write_b32 a191, v91\n"
"v_mfma_f32_16x16x32_bf16 v[88:91], v[144:147], a[244:247], 0\n"
"v_exp_f32 v156, v156\n"
"v_mfma_f32_16x16x32_bf16 v[88:91], v[148:151], a[248:251], v[88:91]\n"
"v_accvgpr_write_b32 a140, v200\n"
"v_accvgpr_write_b32 a141, v201\n"
"v_mfma_f32_16x16x32_bf16 v[92:95], v[148:151], v[224:227], 0\n"
"v_mov_b32 v200, 0\n"
"v_mov_b32 v201, 0\n"
"v_mfma_f32_16x16x32_bf16 v[92:95], v[144:147], a[252:255], v[92:95]\n"
"v_exp_f32 v157, v157\n"
"v_mfma_f32_16x16x32_bf16 v[60:63], v[140:143], v[224:227], 0\n"
"v_exp_f32 v158, v158\n"
"v_mfma_f32_16x16x32_bf16 v[60:63], v[136:139], a[252:255], v[60:63]\n"
"v_exp_f32 v159, v159\n"
"v_mfma_f32_16x16x32_bf16 v[28:31], v[132:135], v[224:227], 0\n"
"v_cvt_pk_bf16_f32 v104, v156, v157\n"
"v_cvt_pk_bf16_f32 v105, v158, v159\n"
"v_mfma_f32_16x16x32_bf16 v[28:31], v[128:131], a[252:255], v[28:31]\n"
"ds_read_b128 v[220:223], v231 offset:17408\n"
"v_accvgpr_write_b32 a96, v112\n"
"v_mfma_f32_16x16x32_bf16 a[236:239], v[124:127], a[252:255], 0\n"
"s_add_u32 s40, s56, s52\n"
"v_exp_f32 v160, v160\n"
"v_smfmac_f32_16x16x64_bf16 v[198:201], a[240:243], v[136:143], v241\n"
"s_waitcnt lgkmcnt(1)\n"
"buffer_load_dwordx4 v240, s[16:19], s40 offen offset:1024 nt lds\n"
"v_mfma_f32_16x16x32_bf16 v[156:159], v[208:211], a[0:3], v[180:183]\n"
"v_exp_f32 v161, v161\n"
"v_mfma_f32_16x16x32_bf16 v[156:159], v[212:215], a[4:7], v[156:159]\n"
"v_exp_f32 v162, v162\n"
"v_mfma_f32_16x16x32_bf16 v[156:159], v[216:219], a[8:11], v[156:159]\n"
"s_waitcnt lgkmcnt(0)\n"
"v_exp_f32 v163, v163\n"
"v_mfma_f32_16x16x32_bf16 v[156:159], v[220:223], a[12:15], v[156:159]\n"
"v_accvgpr_write_b32 a97, v113\n"
"v_cvt_pk_bf16_f32 v112, v160, v161\n"
"v_cvt_pk_bf16_f32 v113, v162, v163\n"
"v_mfma_f32_16x16x32_bf16 v[160:163], v[216:219], a[24:27], v[184:187]\n"
"v_exp_f32 v164, v164\n"
"v_mfma_f32_16x16x32_bf16 v[160:163], v[220:223], a[28:31], v[160:163]\n"
"v_exp_f32 v165, v165\n"
"v_mfma_f32_16x16x32_bf16 v[160:163], v[208:211], a[16:19], v[160:163]\n"
"ds_read_b64_tr_b16 a[244:245], v232 offset:0\n"
"ds_read_b64_tr_b16 a[246:247], v232 offset:4096\n"
"v_exp_f32 v166, v166\n"
"v_exp_f32 v167, v167\n"
"v_mfma_f32_16x16x32_bf16 v[160:163], v[212:215], a[20:23], v[160:163]\n"
"ds_read_b64_tr_b16 a[248:249], v236 offset:0\n"
"v_accvgpr_write_b32 a100, v120\n"
"v_accvgpr_write_b32 a101, v121\n"
"v_cvt_pk_bf16_f32 v120, v164, v165\n"
"v_cvt_pk_bf16_f32 v121, v166, v167\n"
"v_mfma_f32_16x16x32_bf16 v[164:167], v[208:211], a[32:35], v[188:191]\n"
"ds_read_b64_tr_b16 a[250:251], v236 offset:4096\n"
"v_cvt_pk_bf16_f32 v0, v0, v1\n"
"v_mfma_f32_16x16x32_bf16 v[164:167], v[212:215], a[36:39], v[164:167]\n"
"ds_read_b64_tr_b16 v[224:225], v237 offset:0\n"
"v_cvt_pk_bf16_f32 v1, v2, v3\n"
"v_mfma_f32_16x16x32_bf16 v[164:167], v[216:219], a[40:43], v[164:167]\n"
"ds_read_b64_tr_b16 v[226:227], v237 offset:4096\n"
"v_cvt_pk_bf16_f32 v2, v4, v5\n"
"v_mfma_f32_16x16x32_bf16 v[164:167], v[220:223], a[44:47], v[164:167]\n"
"s_waitcnt lgkmcnt(4)\n"
"v_cvt_pk_bf16_f32 v3, v6, v7\n"
"v_cvt_pk_bf16_f32 v32, v32, v33\n"
"v_mfma_f32_16x16x32_bf16 a[208:211], v[124:127], a[244:247], 0\n"
"v_permlane16_swap_b32 v0, v2\n"
"v_permlane16_swap_b32 v1, v3\n"
"v_accvgpr_write_b32 a144, v0\n"
"v_accvgpr_write_b32 a145, v1\n"
"v_accvgpr_write_b32 a146, v2\n"
"v_accvgpr_write_b32 a147, v3\n"
"v_mfma_f32_16x16x32_bf16 v[0:3], v[128:131], a[244:247], 0\n"
"s_waitcnt lgkmcnt(2)\n"
"s_add_u32 s40, s56, s53\n"
"ds_read_b64_tr_b16 a[252:253], v233 offset:0\n"
"ds_read_b64_tr_b16 a[254:255], v233 offset:4096\n"
"v_cvt_pk_bf16_f32 v33, v34, v35\n"
"v_cvt_pk_bf16_f32 v34, v36, v37\n"
"v_cvt_pk_bf16_f32 v35, v38, v39\n"
"v_mfma_f32_16x16x32_bf16 v[0:3], v[132:135], a[248:251], v[0:3]\n"
"buffer_load_dwordx4 v240, s[16:19], s40 offen offset:2048 nt lds\n"
"v_permlane16_swap_b32 v32, v34\n"
"v_permlane16_swap_b32 v33, v35\n"
"v_accvgpr_write_b32 a160, v32\n"
"v_accvgpr_write_b32 a161, v33\n"
"v_accvgpr_write_b32 a162, v34\n"
"v_accvgpr_write_b32 a163, v35\n"
"v_mfma_f32_16x16x32_bf16 v[32:35], v[136:139], a[244:247], 0\n"
"v_cvt_pk_bf16_f32 v64, v64, v65\n"
"v_cvt_pk_bf16_f32 v65, v66, v67\n"
"v_cvt_pk_bf16_f32 v66, v68, v69\n"
"v_cvt_pk_bf16_f32 v67, v70, v71\n"
"v_mfma_f32_16x16x32_bf16 v[32:35], v[140:143], a[248:251], v[32:35]\n"
"v_permlane16_swap_b32 v64, v66\n"
"v_permlane16_swap_b32 v65, v67\n"
"v_accvgpr_write_b32 a176, v64\n"
"v_accvgpr_write_b32 a177, v65\n"
"v_accvgpr_write_b32 a178, v66\n"
"v_accvgpr_write_b32 a179, v67\n"
"v_mfma_f32_16x16x32_bf16 v[64:67], v[144:147], a[244:247], 0\n"
"v_exp_f32 v168, v168\n"
"v_mfma_f32_16x16x32_bf16 v[64:67], v[148:151], a[248:251], v[64:67]\n"
"s_waitcnt lgkmcnt(2)\n"
"v_exp_f32 v169, v169\n"
"v_mfma_f32_16x16x32_bf16 v[68:71], v[148:151], v[224:227], 0\n"
"s_waitcnt lgkmcnt(0)\n"
"v_exp_f32 v170, v170\n"
"v_mfma_f32_16x16x32_bf16 v[68:71], v[144:147], a[252:255], v[68:71]\n"
"ds_read_b128 v[208:211], v230 offset:20480\n"
"v_cvt_pk_bf16_f32 v98, v168, v169\n"
"v_mfma_f32_16x16x32_bf16 v[36:39], v[140:143], v[224:227], 0\n"
"ds_read_b128 v[212:215], v231 offset:20480\n"
"v_accvgpr_write_b32 a98, v114\n"
"v_mfma_f32_16x16x32_bf16 v[36:39], v[136:139], a[252:255], v[36:39]\n"
"ds_read_b128 v[216:219], v230 offset:21504\n"
"v_accvgpr_write_b32 a99, v115\n"
"v_mfma_f32_16x16x32_bf16 v[4:7], v[132:135], v[224:227], 0\n"
"s_add_u32 s40, s56, s54\n"
"ds_read_b128 v[220:223], v231 offset:21504\n"
"v_cvt_pk_bf16_f32 v16, v16, v17\n"
"v_mfma_f32_16x16x32_bf16 v[4:7], v[128:131], a[252:255], v[4:7]\n"
"buffer_load_dwordx4 v240, s[16:19], s40 offen offset:3072 nt lds\n"
"v_mfma_f32_16x16x32_bf16 a[212:215], v[124:127], a[252:255], 0\n"
"v_exp_f32 v171, v171\n"
"v_smfmac_f32_16x16x64_bf16 v[200:203], a[240:243], v[144:151], v241\n"
"s_waitcnt lgkmcnt(3)\n"
"v_cvt_pk_bf16_f32 v99, v170, v171\n"
"v_cvt_pk_bf16_f32 v17, v18, v19\n"
"v_mfma_f32_16x16x32_bf16 v[168:171], v[208:211], a[0:3], v[180:183]\n"
"s_waitcnt lgkmcnt(2)\n"
"v_exp_f32 v172, v172\n"
"v_mfma_f32_16x16x32_bf16 v[168:171], v[212:215], a[4:7], v[168:171]\n"
"s_waitcnt lgkmcnt(1)\n"
"v_exp_f32 v173, v173\n"
"v_mfma_f32_16x16x32_bf16 v[168:171], v[216:219], a[8:11], v[168:171]\n"
"s_waitcnt lgkmcnt(0)\n"
"v_exp_f32 v174, v174\n"
"v_exp_f32 v175, v175\n"
"v_mfma_f32_16x16x32_bf16 v[168:171], v[220:223], a[12:15], v[168:171]\n"
"v_cvt_pk_bf16_f32 v106, v172, v173\n"
"v_cvt_pk_bf16_f32 v107, v174, v175\n"
"v_mfma_f32_16x16x32_bf16 v[172:175], v[216:219], a[24:27], v[184:187]\n"
"v_exp_f32 v176, v176\n"
"v_mfma_f32_16x16x32_bf16 v[172:175], v[220:223], a[28:31], v[172:175]\n"
"v_exp_f32 v177, v177\n"
"v_mfma_f32_16x16x32_bf16 v[172:175], v[208:211], a[16:19], v[172:175]\n"
"ds_read_b64_tr_b16 a[244:245], v232 offset:1024\n"
"ds_read_b64_tr_b16 a[246:247], v232 offset:5120\n"
"v_exp_f32 v178, v178\n"
"v_exp_f32 v179, v179\n"
"v_mfma_f32_16x16x32_bf16 v[172:175], v[212:215], a[20:23], v[172:175]\n"
"s_cbranch_vccnz LPCE1_%=\nLPKE1_%=:\n"
"ds_read_b64_tr_b16 a[248:249], v236 offset:1024\n"
"v_cvt_pk_bf16_f32 v114, v176, v177\n"
"v_cvt_pk_bf16_f32 v115, v178, v179\n"
"v_mfma_f32_16x16x32_bf16 v[176:179], v[208:211], a[32:35], v[188:191]\n"
"ds_read_b64_tr_b16 a[250:251], v236 offset:5120\n"
"v_cvt_pk_bf16_f32 v18, v20, v21\n"
"v_mfma_f32_16x16x32_bf16 v[176:179], v[212:215], a[36:39], v[176:179]\n"
"s_nop 0\n"
"v_permlane16_swap_b32 v16, v18\n"
"v_mfma_f32_16x16x32_bf16 v[176:179], v[216:219], a[40:43], v[176:179]\n"
"v_cvt_pk_bf16_f32 v19, v22, v23\n"
"v_accvgpr_write_b32 a152, v16\n"
"v_mfma_f32_16x16x32_bf16 v[176:179], v[220:223], a[44:47], v[176:179]\n"
"s_waitcnt lgkmcnt(2)\n"
"v_permlane16_swap_b32 v17, v19\n"
"v_mfma_f32_16x16x32_bf16 a[224:227], v[124:127], a[244:247], 0\n"
"ds_read_b64_tr_b16 v[224:225], v237 offset:1024\n"
"ds_read_b64_tr_b16 v[226:227], v237 offset:5120\n"
"v_accvgpr_write_b32 a153, v17\n"
"v_accvgpr_write_b32 a154, v18\n"
"v_accvgpr_write_b32 a155, v19\n"
"v_mfma_f32_16x16x32_bf16 v[16:19], v[128:131], a[244:247], 0\n"
"s_waitcnt lgkmcnt(2)\n"
"s_waitcnt vmcnt(8)\n"
"ds_read_b64_tr_b16 a[252:253], v233 offset:1024\n"
"ds_read_b64_tr_b16 a[254:255], v233 offset:5120\n"
"v_cvt_pk_bf16_f32 v48, v48, v49\n"
"v_cvt_pk_bf16_f32 v49, v50, v51\n"
"v_cvt_pk_bf16_f32 v50, v52, v53\n"
"v_cvt_pk_bf16_f32 v51, v54, v55\n"
"v_mfma_f32_16x16x32_bf16 v[16:19], v[132:135], a[248:251], v[16:19]\n"
"s_barrier\n"
"v_permlane16_swap_b32 v48, v50\n"
"v_permlane16_swap_b32 v49, v51\n"
"v_accvgpr_write_b32 a168, v48\n"
"v_accvgpr_write_b32 a169, v49\n"
"v_accvgpr_write_b32 a170, v50\n"
"v_accvgpr_write_b32 a171, v51\n"
"v_mfma_f32_16x16x32_bf16 v[48:51], v[136:139], a[244:247], 0\n"
"v_cvt_pk_bf16_f32 v80, v80, v81\n"
"v_cvt_pk_bf16_f32 v81, v82, v83\n"
"v_cvt_pk_bf16_f32 v82, v84, v85\n"
"v_cvt_pk_bf16_f32 v83, v86, v87\n"
"v_mfma_f32_16x16x32_bf16 v[48:51], v[140:143], a[248:251], v[48:51]\n"
"v_permlane16_swap_b32 v80, v82\n"
"v_permlane16_swap_b32 v81, v83\n"
"v_accvgpr_write_b32 a184, v80\n"
"v_accvgpr_write_b32 a185, v81\n"
"v_accvgpr_write_b32 a186, v82\n"
"v_accvgpr_write_b32 a187, v83\n"
"v_mfma_f32_16x16x32_bf16 v[80:83], v[144:147], a[244:247], 0\n"
"v_exp_f32 v152, v152\n"
"v_mfma_f32_16x16x32_bf16 v[80:83], v[148:151], a[248:251], v[80:83]\n"
"s_waitcnt lgkmcnt(2)\n"
"v_exp_f32 v153, v153\n"
"v_mfma_f32_16x16x32_bf16 v[84:87], v[148:151], v[224:227], 0\n"
"s_waitcnt lgkmcnt(0)\n"
"ds_read_b128 v[208:211], v228 offset:32768\n"
"v_accvgpr_write_b32 a102, v122\n"
"v_mfma_f32_16x16x32_bf16 v[84:87], v[144:147], a[252:255], v[84:87]\n"
"ds_read_b128 v[212:215], v229 offset:32768\n"
"v_accvgpr_write_b32 a103, v123\n"
"v_mfma_f32_16x16x32_bf16 v[52:55], v[140:143], v[224:227], 0\n"
"ds_read_b128 v[216:219], v228 offset:33792\n"
"v_bfe_u32 v254, v242, 6, 4\n"
"v_mfma_f32_16x16x32_bf16 v[52:55], v[136:139], a[252:255], v[52:55]\n"
"ds_read_b128 v[220:223], v229 offset:33792\n"
"v_xor_b32 v249, v249, v254\n"
"v_mfma_f32_16x16x32_bf16 v[20:23], v[132:135], v[224:227], 0\n"
"ds_read_b64_tr_b16 a[244:245], v234 offset:16384\n"
"ds_read_b64_tr_b16 a[246:247], v234 offset:20480\n"
"v_mfma_f32_16x16x32_bf16 v[20:23], v[128:131], a[252:255], v[20:23]\n"
"ds_read_b64_tr_b16 a[248:249], v238 offset:16384\n"
"ds_read_b64_tr_b16 a[250:251], v238 offset:20480\n"
"v_exp_f32 v154, v154\n"
"v_exp_f32 v155, v155\n"
"v_exp_f32 v156, v156\n"
"v_exp_f32 v157, v157\n"
"v_exp_f32 v158, v158\n"
"v_exp_f32 v159, v159\n"
"v_exp_f32 v160, v160\n"
"v_exp_f32 v161, v161\n"
"v_exp_f32 v162, v162\n"
"v_exp_f32 v163, v163\n"
"v_exp_f32 v164, v164\n"
"v_exp_f32 v165, v165\n"
"v_exp_f32 v166, v166\n"
"v_exp_f32 v167, v167\n"
"v_mfma_f32_16x16x32_bf16 a[228:231], v[124:127], a[252:255], 0\n"
"s_waitcnt lgkmcnt(7)\n"
"ds_read_b64_tr_b16 a[254:255], v235 offset:20480\n"
"ds_read_b64_tr_b16 v[224:225], v239 offset:16384\n"
"v_lshl_add_u32 v249, v249, 4, s44\n"
"v_cvt_pk_bf16_f32 v122, v152, v153\n"
"v_cvt_pk_bf16_f32 v123, v154, v155\n"
"v_cvt_pk_bf16_f32 v100, v156, v157\n"
"v_cvt_pk_bf16_f32 v101, v158, v159\n"
"v_cvt_pk_bf16_f32 v108, v160, v161\n"
"v_cvt_pk_bf16_f32 v109, v162, v163\n"
"v_cvt_pk_bf16_f32 v116, v164, v165\n"
"v_cvt_pk_bf16_f32 v117, v166, v167\n"
"v_exp_f32 v169, v169\n"
"v_exp_f32 v172, v172\n"
"v_exp_f32 v173, v173\n"
"v_exp_f32 v175, v175\n"
"s_branch LAY2_%=\n"
"LAL_%=:\n"
"v_mfma_f32_16x16x32_bf16 a[224:227], v[124:127], a[244:247], a[224:227]\n"
"v_exp_f32 v169, v169\n"
"v_mfma_f32_16x16x32_bf16 v[16:19], v[128:131], a[244:247], v[16:19]\n"
"s_waitcnt vmcnt(8)\n"
"v_exp_f32 v172, v172\n"
"v_mfma_f32_16x16x32_bf16 v[16:19], v[132:135], a[248:251], v[16:19]\n"
"s_barrier\n"
"ds_read_b128 v[216:219], v228 offset:1024\n"
"v_mfma_f32_16x16x32_bf16 v[48:51], v[136:139], a[244:247], v[48:51]\n"
"ds_read_b128 v[212:215], v229 offset:0\n"
"v_cvt_pk_bf16_f32 v108, v160, v161\n"
"v_mfma_f32_16x16x32_bf16 v[48:51], v[140:143], a[248:251], v[48:51]\n"
"ds_read_b128 v[208:211], v228 offset:0\n"
"v_cvt_pk_bf16_f32 v100, v156, v157\n"
"v_mfma_f32_16x16x32_bf16 v[80:83], v[144:147], a[244:247], v[80:83]\n"
"ds_read_b128 v[220:223], v229 offset:1024\n"
"v_cvt_pk_bf16_f32 v117, v166, v167\n"
"v_mfma_f32_16x16x32_bf16 v[80:83], v[148:151], a[248:251], v[80:83]\n"
"ds_read_b64_tr_b16 a[244:245], v234 offset:49152\n"
"v_cvt_pk_bf16_f32 v109, v162, v163\n"
"v_mfma_f32_16x16x32_bf16 v[84:87], v[148:151], v[224:227], v[84:87]\n"
"ds_read_b64_tr_b16 a[248:249], v238 offset:49152\n"
"v_mfma_f32_16x16x32_bf16 v[84:87], v[144:147], a[252:255], v[84:87]\n"
"ds_read_b64_tr_b16 a[246:247], v234 offset:53248\n"
"v_mfma_f32_16x16x32_bf16 v[52:55], v[140:143], v[224:227], v[52:55]\n"
"ds_read_b64_tr_b16 a[250:251], v238 offset:53248\n"
"v_mfma_f32_16x16x32_bf16 v[52:55], v[136:139], a[252:255], v[52:55]\n"
"v_exp_f32 v175, v175\n"
"v_mfma_f32_16x16x32_bf16 v[20:23], v[132:135], v[224:227], v[20:23]\n"
"ds_read_b64_tr_b16 v[224:225], v239 offset:49152\n"
"v_mfma_f32_16x16x32_bf16 v[20:23], v[128:131], a[252:255], v[20:23]\n"
"v_exp_f32 v173, v173\n"
"v_mfma_f32_16x16x32_bf16 a[228:231], v[124:127], a[252:255], a[228:231]\n"
"s_waitcnt lgkmcnt(5)\n"
"ds_read_b64_tr_b16 a[254:255], v235 offset:53248\n"
"v_cvt_pk_bf16_f32 v116, v164, v165\n"
"v_mfma_f32_16x16x32_bf16 v[152:155], v[208:211], a[0:3], v[180:183]\n"
"v_exp_f32 v174, v174\n"
"v_mfma_f32_16x16x32_bf16 v[152:155], v[212:215], a[4:7], v[152:155]\n"
"ds_read_b64_tr_b16 a[252:253], v235 offset:49152\n"
"v_cvt_pk_bf16_f32 v111, v174, v175\n"
"v_mfma_f32_16x16x32_bf16 v[152:155], v[216:219], a[8:11], v[152:155]\n"
"s_add_u32 m0, s44, 81920\n"
"ds_read_b64_tr_b16 v[226:227], v239 offset:53248\n"
"v_mfma_f32_16x16x32_bf16 v[152:155], v[220:223], a[12:15], v[152:155]\n"
"buffer_load_dwordx4 v240, s[20:23], s56 offen nt lds\n"
"v_mfma_f32_16x16x32_bf16 v[156:159], v[216:219], a[24:27], v[184:187]\n"
"v_exp_f32 v171, v171\n"
"v_mfma_f32_16x16x32_bf16 v[156:159], v[220:223], a[28:31], v[156:159]\n"
"v_exp_f32 v168, v168\n"
"v_mfma_f32_16x16x32_bf16 v[156:159], v[208:211], a[16:19], v[156:159]\n"
"v_exp_f32 v176, v176\n"
"v_mfma_f32_16x16x32_bf16 v[156:159], v[212:215], a[20:23], v[156:159]\n"
"v_exp_f32 v177, v177\n"
"v_mfma_f32_16x16x32_bf16 v[160:163], v[208:211], a[32:35], v[188:191]\n"
"v_exp_f32 v179, v179\n"
"v_mfma_f32_16x16x32_bf16 v[160:163], v[212:215], a[36:39], v[160:163]\n"
"v_exp_f32 v170, v170\n"
"v_mfma_f32_16x16x32_bf16 v[160:163], v[216:219], a[40:43], v[160:163]\n"
"v_exp_f32 v153, v153\n"
"v_mfma_f32_16x16x32_bf16 v[160:163], v[220:223], a[44:47], v[160:163]\n"
"v_cvt_pk_bf16_f32 v102, v168, v169\n"
"v_mfma_f32_16x16x32_bf16 v[164:167], v[216:219], a[200:203], v[192:195]\n"
"ds_read_b128 v[216:219], v228 offset:5120\n"
"v_cvt_pk_bf16_f32 v103, v170, v171\n"
"v_mfma_f32_16x16x32_bf16 v[164:167], v[220:223], a[204:207], v[164:167]\n"
"v_cvt_pk_bf16_f32 v110, v172, v173\n"
"v_cvt_pk_bf16_f32 v118, v176, v177\n"
"v_mfma_f32_16x16x32_bf16 v[164:167], v[208:211], a[192:195], v[164:167]\n"
"s_add_u32 s40, s56, s52\n"
"ds_read_b128 v[208:211], v228 offset:4096\n"
"v_mfma_f32_16x16x32_bf16 v[164:167], v[212:215], a[196:199], v[164:167]\n"
"s_waitcnt lgkmcnt(2)\n"
"buffer_load_dwordx4 v240, s[20:23], s40 offen offset:1024 nt lds\n"
"v_mfma_f32_16x16x32_bf16 a[216:219], v[120:123], a[244:247], a[216:219]\n"
"v_exp_f32 v155, v155\n"
"v_mfma_f32_16x16x32_bf16 v[8:11], v[96:99], a[244:247], v[8:11]\n"
"v_exp_f32 v161, v161\n"
"v_mfma_f32_16x16x32_bf16 v[8:11], v[100:103], a[248:251], v[8:11]\n"
"v_exp_f32 v163, v163\n"
"v_mfma_f32_16x16x32_bf16 v[40:43], v[104:107], a[244:247], v[40:43]\n"
"v_exp_f32 v178, v178\n"
"v_mfma_f32_16x16x32_bf16 v[40:43], v[108:111], a[248:251], v[40:43]\n"
"ds_read_b128 v[220:223], v229 offset:5120\n"
"v_cvt_pk_bf16_f32 v119, v178, v179\n"
"v_mfma_f32_16x16x32_bf16 v[72:75], v[112:115], a[244:247], v[72:75]\n"
"ds_read_b64_tr_b16 a[244:245], v234 offset:50176\n"
"v_mfma_f32_16x16x32_bf16 v[72:75], v[116:119], a[248:251], v[72:75]\n"
"v_exp_f32 v158, v158\n"
"v_mfma_f32_16x16x32_bf16 v[76:79], v[116:119], v[224:227], v[76:79]\n"
"ds_read_b128 v[212:215], v229 offset:4096\n"
"v_mfma_f32_16x16x32_bf16 v[76:79], v[112:115], a[252:255], v[76:79]\n"
"v_exp_f32 v160, v160\n"
"v_mfma_f32_16x16x32_bf16 v[44:47], v[108:111], v[224:227], v[44:47]\n"
"s_add_u32 s40, s56, s53\n"
"ds_read_b64_tr_b16 a[248:249], v238 offset:50176\n"
"v_mfma_f32_16x16x32_bf16 v[44:47], v[104:107], a[252:255], v[44:47]\n"
"buffer_load_dwordx4 v240, s[20:23], s40 offen offset:2048 nt lds\n"
"v_mfma_f32_16x16x32_bf16 v[12:15], v[100:103], v[224:227], v[12:15]\n"
"v_exp_f32 v165, v165\n"
"v_mfma_f32_16x16x32_bf16 v[12:15], v[96:99], a[252:255], v[12:15]\n"
"v_exp_f32 v159, v159\n"
"v_mfma_f32_16x16x32_bf16 a[220:223], v[120:123], a[252:255], a[220:223]\n"
"v_exp_f32 v152, v152\n"
"v_smfmac_f32_16x16x64_bf16 v[196:199], a[240:243], v[96:103], v241\n"
"v_mfma_f32_16x16x32_bf16 v[202:205], a[240:243], v[120:123], v[202:205]\n"
"s_waitcnt lgkmcnt(1)\n"
"ds_read_b64_tr_b16 v[224:225], v239 offset:50176\n"
"v_cvt_pk_bf16_f32 v128, v152, v153\n"
"v_mfma_f32_16x16x32_bf16 v[168:171], v[216:219], a[8:11], v[180:183]\n"
"v_exp_f32 v162, v162\n"
"v_mfma_f32_16x16x32_bf16 v[168:171], v[220:223], a[12:15], v[168:171]\n"
"ds_read_b64_tr_b16 a[250:251], v238 offset:54272\n"
"v_mfma_f32_16x16x32_bf16 v[168:171], v[208:211], a[0:3], v[168:171]\n"
"ds_read_b64_tr_b16 a[252:253], v235 offset:50176\n"
"v_mfma_f32_16x16x32_bf16 v[168:171], v[212:215], a[4:7], v[168:171]\n"
"v_exp_f32 v154, v154\n"
"v_mfma_f32_16x16x32_bf16 v[172:175], v[208:211], a[16:19], v[184:187]\n"
"ds_read_b64_tr_b16 v[226:227], v239 offset:54272\n"
"v_mfma_f32_16x16x32_bf16 v[172:175], v[212:215], a[20:23], v[172:175]\n"
"ds_read_b64_tr_b16 a[246:247], v234 offset:54272\n"
"v_cvt_pk_bf16_f32 v129, v154, v155\n"
"v_mfma_f32_16x16x32_bf16 v[172:175], v[216:219], a[24:27], v[172:175]\n"
"s_add_u32 s40, s56, s54\n"
"ds_read_b64_tr_b16 a[254:255], v235 offset:54272\n"
"v_mfma_f32_16x16x32_bf16 v[172:175], v[220:223], a[28:31], v[172:175]\n"
"buffer_load_dwordx4 v240, s[20:23], s40 offen offset:3072 nt lds\n"
"v_mfma_f32_16x16x32_bf16 v[176:179], v[216:219], a[40:43], v[188:191]\n"
"s_add_u32 s55, s55, s59\n"
"v_exp_f32 v157, v157\n"
"v_mfma_f32_16x16x32_bf16 v[176:179], v[220:223], a[44:47], v[176:179]\n"
"s_add_u32 s41, s55, s57\n"
"v_exp_f32 v170, v170\n"
"v_mfma_f32_16x16x32_bf16 v[176:179], v[208:211], a[32:35], v[176:179]\n"
"s_and_b32 s41, s41, s58\n"
"v_exp_f32 v156, v156\n"
"v_mfma_f32_16x16x32_bf16 v[176:179], v[212:215], a[36:39], v[176:179]\n"
"s_andn2_b32 s56, s55, s58\n"
"v_exp_f32 v166, v166\n"
"v_mfma_f32_16x16x32_bf16 v[152:155], v[208:211], a[192:195], v[192:195]\n"
"s_or_b32 s56, s56, s41\n"
"ds_read_b128 v[208:211], v230 offset:0\n"
"v_mfma_f32_16x16x32_bf16 v[152:155], v[212:215], a[196:199], v[152:155]\n"
"v_exp_f32 v174, v174\n"
"v_mfma_f32_16x16x32_bf16 v[152:155], v[216:219], a[200:203], v[152:155]\n"
"ds_read_b128 v[212:215], v231 offset:0\n"
"v_mfma_f32_16x16x32_bf16 v[152:155], v[220:223], a[204:207], v[152:155]\n"
"s_waitcnt lgkmcnt(2)\n"
"ds_read_b128 v[216:219], v230 offset:1024\n"
"v_cvt_pk_bf16_f32 v137, v158, v159\n"
"v_mfma_f32_16x16x32_bf16 a[232:235], v[120:123], a[244:247], a[232:235]\n"
"v_exp_f32 v164, v164\n"
"v_mfma_f32_16x16x32_bf16 v[24:27], v[96:99], a[244:247], v[24:27]\n"
"s_add_u32 m0, s44, 32768\n"
"ds_read_b128 v[220:223], v231 offset:1024\n"
"v_mfma_f32_16x16x32_bf16 v[24:27], v[100:103], a[248:251], v[24:27]\n"
"buffer_load_dwordx4 v240, s[16:19], s56 offen nt lds\n"
"v_mfma_f32_16x16x32_bf16 v[56:59], v[104:107], a[244:247], v[56:59]\n"
"v_exp_f32 v167, v167\n"
"v_mfma_f32_16x16x32_bf16 v[56:59], v[108:111], a[248:251], v[56:59]\n"
"v_exp_f32 v176, v176\n"
"v_mfma_f32_16x16x32_bf16 v[88:91], v[112:115], a[244:247], v[88:91]\n"
"v_exp_f32 v153, v153\n"
"v_mfma_f32_16x16x32_bf16 v[88:91], v[116:119], a[248:251], v[88:91]\n"
"v_exp_f32 v173, v173\n"
"v_mfma_f32_16x16x32_bf16 v[92:95], v[116:119], v[224:227], v[92:95]\n"
"ds_read_b64_tr_b16 a[250:251], v236 offset:53248\n"
"v_cvt_pk_bf16_f32 v125, v166, v167\n"
"v_mfma_f32_16x16x32_bf16 v[92:95], v[112:115], a[252:255], v[92:95]\n"
"ds_read_b64_tr_b16 a[244:245], v232 offset:49152\n"
"v_cvt_pk_bf16_f32 v144, v160, v161\n"
"v_mfma_f32_16x16x32_bf16 v[60:63], v[108:111], v[224:227], v[60:63]\n"
"ds_read_b64_tr_b16 a[248:249], v236 offset:49152\n"
"v_cvt_pk_bf16_f32 v124, v164, v165\n"
"v_mfma_f32_16x16x32_bf16 v[60:63], v[104:107], a[252:255], v[60:63]\n"
"ds_read_b64_tr_b16 a[246:247], v232 offset:53248\n"
"v_cvt_pk_bf16_f32 v136, v156, v157\n"
"v_mfma_f32_16x16x32_bf16 v[28:31], v[100:103], v[224:227], v[28:31]\n"
"ds_read_b64_tr_b16 v[226:227], v237 offset:53248\n"
"v_cvt_pk_bf16_f32 v145, v162, v163\n"
"v_mfma_f32_16x16x32_bf16 v[28:31], v[96:99], a[252:255], v[28:31]\n"
"ds_read_b64_tr_b16 v[224:225], v237 offset:49152\n"
"v_mfma_f32_16x16x32_bf16 a[236:239], v[120:123], a[252:255], a[236:239]\n"
"s_add_u32 s40, s56, s52\n"
"ds_read_b64_tr_b16 a[252:253], v233 offset:49152\n"
"v_smfmac_f32_16x16x64_bf16 v[198:201], a[240:243], v[104:111], v241\n"
"s_waitcnt lgkmcnt(7)\n"
"buffer_load_dwordx4 v240, s[16:19], s40 offen offset:1024 nt lds\n"
"v_mfma_f32_16x16x32_bf16 v[156:159], v[208:211], a[0:3], v[180:183]\n"
"v_exp_f32 v169, v169\n"
"v_mfma_f32_16x16x32_bf16 v[156:159], v[212:215], a[4:7], v[156:159]\n"
"v_exp_f32 v168, v168\n"
"v_mfma_f32_16x16x32_bf16 v[156:159], v[216:219], a[8:11], v[156:159]\n"
"v_exp_f32 v175, v175\n"
"v_mfma_f32_16x16x32_bf16 v[156:159], v[220:223], a[12:15], v[156:159]\n"
"v_exp_f32 v172, v172\n"
"v_mfma_f32_16x16x32_bf16 v[160:163], v[216:219], a[24:27], v[184:187]\n"
"ds_read_b64_tr_b16 a[254:255], v233 offset:53248\n"
"v_mfma_f32_16x16x32_bf16 v[160:163], v[220:223], a[28:31], v[160:163]\n"
"v_exp_f32 v155, v155\n"
"v_mfma_f32_16x16x32_bf16 v[160:163], v[208:211], a[16:19], v[160:163]\n"
"v_exp_f32 v177, v177\n"
"v_mfma_f32_16x16x32_bf16 v[160:163], v[212:215], a[20:23], v[160:163]\n"
"v_exp_f32 v179, v179\n"
"v_mfma_f32_16x16x32_bf16 v[164:167], v[208:211], a[32:35], v[188:191]\n"
"ds_read_b128 v[208:211], v230 offset:4096\n"
"v_cvt_pk_bf16_f32 v139, v174, v175\n"
"v_mfma_f32_16x16x32_bf16 v[164:167], v[212:215], a[36:39], v[164:167]\n"
"v_exp_f32 v171, v171\n"
"v_mfma_f32_16x16x32_bf16 v[164:167], v[216:219], a[40:43], v[164:167]\n"
"ds_read_b128 v[212:215], v231 offset:4096\n"
"v_cvt_pk_bf16_f32 v138, v172, v173\n"
"v_mfma_f32_16x16x32_bf16 v[164:167], v[220:223], a[44:47], v[164:167]\n"
"s_waitcnt lgkmcnt(3)\n"
"ds_read_b128 v[220:223], v231 offset:5120\n"
"v_cvt_pk_bf16_f32 v130, v168, v169\n"
"v_mfma_f32_16x16x32_bf16 a[208:211], v[120:123], a[244:247], a[208:211]\n"
"v_exp_f32 v154, v154\n"
"v_mfma_f32_16x16x32_bf16 v[0:3], v[96:99], a[244:247], v[0:3]\n"
"s_add_u32 s40, s56, s53\n"
"ds_read_b128 v[216:219], v230 offset:5120\n"
"v_mfma_f32_16x16x32_bf16 v[0:3], v[100:103], a[248:251], v[0:3]\n"
"buffer_load_dwordx4 v240, s[16:19], s40 offen offset:2048 nt lds\n"
"v_mfma_f32_16x16x32_bf16 v[32:35], v[104:107], a[244:247], v[32:35]\n"
"v_exp_f32 v160, v160\n"
"v_mfma_f32_16x16x32_bf16 v[32:35], v[108:111], a[248:251], v[32:35]\n"
"v_exp_f32 v178, v178\n"
"v_mfma_f32_16x16x32_bf16 v[64:67], v[112:115], a[244:247], v[64:67]\n"
"v_exp_f32 v152, v152\n"
"v_mfma_f32_16x16x32_bf16 v[64:67], v[116:119], a[248:251], v[64:67]\n"
"v_exp_f32 v161, v161\n"
"v_mfma_f32_16x16x32_bf16 v[68:71], v[116:119], v[224:227], v[68:71]\n"
"s_waitcnt lgkmcnt(1)\n"
"ds_read_b64_tr_b16 a[244:245], v232 offset:50176\n"
"v_cvt_pk_bf16_f32 v147, v178, v179\n"
"v_mfma_f32_16x16x32_bf16 v[68:71], v[112:115], a[252:255], v[68:71]\n"
"ds_read_b64_tr_b16 a[248:249], v236 offset:50176\n"
"v_cvt_pk_bf16_f32 v146, v176, v177\n"
"v_mfma_f32_16x16x32_bf16 v[36:39], v[108:111], v[224:227], v[36:39]\n"
"ds_read_b64_tr_b16 a[250:251], v236 offset:54272\n"
"v_cvt_pk_bf16_f32 v131, v170, v171\n"
"v_mfma_f32_16x16x32_bf16 v[36:39], v[104:107], a[252:255], v[36:39]\n"
"v_exp_f32 v163, v163\n"
"v_mfma_f32_16x16x32_bf16 v[4:7], v[100:103], v[224:227], v[4:7]\n"
"s_add_u32 s40, s56, s54\n"
"ds_read_b64_tr_b16 a[246:247], v232 offset:54272\n"
"v_mfma_f32_16x16x32_bf16 v[4:7], v[96:99], a[252:255], v[4:7]\n"
"buffer_load_dwordx4 v240, s[16:19], s40 offen offset:3072 nt lds\n"
"v_mfma_f32_16x16x32_bf16 a[212:215], v[120:123], a[252:255], a[212:215]\n"
"v_exp_f32 v167, v167\n"
"v_smfmac_f32_16x16x64_bf16 v[200:203], a[240:243], v[112:119], v241\n"
"v_cvt_pk_bf16_f32 v126, v152, v153\n"
"v_mfma_f32_16x16x32_bf16 v[168:171], v[208:211], a[0:3], v[180:183]\n"
"v_exp_f32 v159, v159\n"
"v_mfma_f32_16x16x32_bf16 v[168:171], v[212:215], a[4:7], v[168:171]\n"
"s_waitcnt lgkmcnt(2)\n"
"v_exp_f32 v165, v165\n"
"v_mfma_f32_16x16x32_bf16 v[168:171], v[216:219], a[8:11], v[168:171]\n"
"ds_read_b64_tr_b16 a[252:253], v233 offset:50176\n"
"ds_read_b64_tr_b16 v[224:225], v237 offset:50176\n"
"v_mfma_f32_16x16x32_bf16 v[168:171], v[220:223], a[12:15], v[168:171]\n"
"ds_read_b64_tr_b16 a[254:255], v233 offset:54272\n"
"ds_read_b64_tr_b16 v[226:227], v237 offset:54272\n"
"v_mfma_f32_16x16x32_bf16 v[172:175], v[216:219], a[24:27], v[184:187]\n"
"v_exp_f32 v166, v166\n"
"v_mfma_f32_16x16x32_bf16 v[172:175], v[220:223], a[28:31], v[172:175]\n"
"v_exp_f32 v164, v164\n"
"v_mfma_f32_16x16x32_bf16 v[172:175], v[208:211], a[16:19], v[172:175]\n"
"v_exp_f32 v157, v157\n"
"v_mfma_f32_16x16x32_bf16 v[172:175], v[212:215], a[20:23], v[172:175]\n"
"s_cbranch_vccnz LPCL0_%=\nLPKL0_%=:\n"
"v_cvt_pk_bf16_f32 v127, v154, v155\n"
"v_mfma_f32_16x16x32_bf16 v[176:179], v[208:211], a[32:35], v[188:191]\n"
"v_exp_f32 v158, v158\n"
"v_mfma_f32_16x16x32_bf16 v[176:179], v[212:215], a[36:39], v[176:179]\n"
"v_exp_f32 v162, v162\n"
"v_mfma_f32_16x16x32_bf16 v[176:179], v[216:219], a[40:43], v[176:179]\n"
"v_exp_f32 v156, v156\n"
"v_mfma_f32_16x16x32_bf16 v[176:179], v[220:223], a[44:47], v[176:179]\n"
"s_waitcnt lgkmcnt(0)\n"
"v_cvt_pk_bf16_f32 v133, v158, v159\n"
"v_mfma_f32_16x16x32_bf16 a[224:227], v[120:123], a[244:247], a[224:227]\n"
"v_exp_f32 v169, v169\n"
"v_mfma_f32_16x16x32_bf16 v[16:19], v[96:99], a[244:247], v[16:19]\n"
"s_waitcnt vmcnt(8)\n"
"v_exp_f32 v172, v172\n"
"v_mfma_f32_16x16x32_bf16 v[16:19], v[100:103], a[248:251], v[16:19]\n"
"s_barrier\n"
"ds_read_b128 v[216:219], v228 offset:17408\n"
"v_mfma_f32_16x16x32_bf16 v[48:51], v[104:107], a[244:247], v[48:51]\n"
"ds_read_b128 v[212:215], v229 offset:16384\n"
"v_cvt_pk_bf16_f32 v140, v160, v161\n"
"v_mfma_f32_16x16x32_bf16 v[48:51], v[108:111], a[248:251], v[48:51]\n"
"ds_read_b128 v[208:211], v228 offset:16384\n"
"v_cvt_pk_bf16_f32 v132, v156, v157\n"
"v_mfma_f32_16x16x32_bf16 v[80:83], v[112:115], a[244:247], v[80:83]\n"
"ds_read_b128 v[220:223], v229 offset:17408\n"
"v_cvt_pk_bf16_f32 v149, v166, v167\n"
"v_mfma_f32_16x16x32_bf16 v[80:83], v[116:119], a[248:251], v[80:83]\n"
"ds_read_b64_tr_b16 a[244:245], v234 offset:0\n"
"v_cvt_pk_bf16_f32 v141, v162, v163\n"
"v_mfma_f32_16x16x32_bf16 v[84:87], v[116:119], v[224:227], v[84:87]\n"
"ds_read_b64_tr_b16 a[248:249], v238 offset:0\n"
"v_mfma_f32_16x16x32_bf16 v[84:87], v[112:115], a[252:255], v[84:87]\n"
"ds_read_b64_tr_b16 a[246:247], v234 offset:4096\n"
"v_mfma_f32_16x16x32_bf16 v[52:55], v[108:111], v[224:227], v[52:55]\n"
"ds_read_b64_tr_b16 a[250:251], v238 offset:4096\n"
"v_mfma_f32_16x16x32_bf16 v[52:55], v[104:107], a[252:255], v[52:55]\n"
"v_exp_f32 v175, v175\n"
"v_mfma_f32_16x16x32_bf16 v[20:23], v[100:103], v[224:227], v[20:23]\n"
"ds_read_b64_tr_b16 v[224:225], v239 offset:0\n"
"v_mfma_f32_16x16x32_bf16 v[20:23], v[96:99], a[252:255], v[20:23]\n"
"v_exp_f32 v173, v173\n"
"v_mfma_f32_16x16x32_bf16 a[228:231], v[120:123], a[252:255], a[228:231]\n"
"s_waitcnt lgkmcnt(5)\n"
"ds_read_b64_tr_b16 a[254:255], v235 offset:4096\n"
"v_cvt_pk_bf16_f32 v148, v164, v165\n"
"v_mfma_f32_16x16x32_bf16 v[152:155], v[208:211], a[0:3], v[180:183]\n"
"v_exp_f32 v174, v174\n"
"v_mfma_f32_16x16x32_bf16 v[152:155], v[212:215], a[4:7], v[152:155]\n"
"ds_read_b64_tr_b16 a[252:253], v235 offset:0\n"
"v_cvt_pk_bf16_f32 v143, v174, v175\n"
"v_mfma_f32_16x16x32_bf16 v[152:155], v[216:219], a[8:11], v[152:155]\n"
"s_add_u32 m0, s44, 98304\n"
"ds_read_b64_tr_b16 v[226:227], v239 offset:4096\n"
"v_mfma_f32_16x16x32_bf16 v[152:155], v[220:223], a[12:15], v[152:155]\n"
"buffer_load_dwordx4 v240, s[20:23], s56 offen nt lds\n"
"v_mfma_f32_16x16x32_bf16 v[156:159], v[216:219], a[24:27], v[184:187]\n"
"v_exp_f32 v171, v171\n"
"v_mfma_f32_16x16x32_bf16 v[156:159], v[220:223], a[28:31], v[156:159]\n"
"v_exp_f32 v168, v168\n"
"v_mfma_f32_16x16x32_bf16 v[156:159], v[208:211], a[16:19], v[156:159]\n"
"v_exp_f32 v176, v176\n"
"v_mfma_f32_16x16x32_bf16 v[156:159], v[212:215], a[20:23], v[156:159]\n"
"v_exp_f32 v177, v177\n"
"v_mfma_f32_16x16x32_bf16 v[160:163], v[208:211], a[32:35], v[188:191]\n"
"v_exp_f32 v179, v179\n"
"v_mfma_f32_16x16x32_bf16 v[160:163], v[212:215], a[36:39], v[160:163]\n"
"v_exp_f32 v170, v170\n"
"v_mfma_f32_16x16x32_bf16 v[160:163], v[216:219], a[40:43], v[160:163]\n"
"v_exp_f32 v153, v153\n"
"v_mfma_f32_16x16x32_bf16 v[160:163], v[220:223], a[44:47], v[160:163]\n"
"v_cvt_pk_bf16_f32 v134, v168, v169\n"
"v_mfma_f32_16x16x32_bf16 v[164:167], v[216:219], a[200:203], v[192:195]\n"
"ds_read_b128 v[216:219], v228 offset:21504\n"
"v_cvt_pk_bf16_f32 v135, v170, v171\n"
"v_mfma_f32_16x16x32_bf16 v[164:167], v[220:223], a[204:207], v[164:167]\n"
"v_cvt_pk_bf16_f32 v142, v172, v173\n"
"v_cvt_pk_bf16_f32 v150, v176, v177\n"
"v_mfma_f32_16x16x32_bf16 v[164:167], v[208:211], a[192:195], v[164:167]\n"
"s_add_u32 s40, s56, s52\n"
"ds_read_b128 v[208:211], v228 offset:20480\n"
"v_mfma_f32_16x16x32_bf16 v[164:167], v[212:215], a[196:199], v[164:167]\n"
"s_waitcnt lgkmcnt(2)\n"
"buffer_load_dwordx4 v240, s[20:23], s40 offen offset:1024 nt lds\n"
"v_mfma_f32_16x16x32_bf16 a[216:219], v[124:127], a[244:247], a[216:219]\n"
"v_exp_f32 v155, v155\n"
"v_mfma_f32_16x16x32_bf16 v[8:11], v[128:131], a[244:247], v[8:11]\n"
"v_exp_f32 v161, v161\n"
"v_mfma_f32_16x16x32_bf16 v[8:11], v[132:135], a[248:251], v[8:11]\n"
"v_exp_f32 v163, v163\n"
"v_mfma_f32_16x16x32_bf16 v[40:43], v[136:139], a[244:247], v[40:43]\n"
"v_exp_f32 v178, v178\n"
"v_mfma_f32_16x16x32_bf16 v[40:43], v[140:143], a[248:251], v[40:43]\n"
"ds_read_b128 v[220:223], v229 offset:21504\n"
"v_cvt_pk_bf16_f32 v151, v178, v179\n"
"v_mfma_f32_16x16x32_bf16 v[72:75], v[144:147], a[244:247], v[72:75]\n"
"ds_read_b64_tr_b16 a[244:245], v234 offset:1024\n"
"v_mfma_f32_16x16x32_bf16 v[72:75], v[148:151], a[248:251], v[72:75]\n"
"v_exp_f32 v158, v158\n"
"v_mfma_f32_16x16x32_bf16 v[76:79], v[148:151], v[224:227], v[76:79]\n"
"ds_read_b128 v[212:215], v229 offset:20480\n"
"v_mfma_f32_16x16x32_bf16 v[76:79], v[144:147], a[252:255], v[76:79]\n"
"v_exp_f32 v160, v160\n"
"v_mfma_f32_16x16x32_bf16 v[44:47], v[140:143], v[224:227], v[44:47]\n"
"s_add_u32 s40, s56, s53\n"
"ds_read_b64_tr_b16 a[248:249], v238 offset:1024\n"
"v_mfma_f32_16x16x32_bf16 v[44:47], v[136:139], a[252:255], v[44:47]\n"
"buffer_load_dwordx4 v240, s[20:23], s40 offen offset:2048 nt lds\n"
"v_mfma_f32_16x16x32_bf16 v[12:15], v[132:135], v[224:227], v[12:15]\n"
"v_exp_f32 v165, v165\n"
"v_mfma_f32_16x16x32_bf16 v[12:15], v[128:131], a[252:255], v[12:15]\n"
"v_exp_f32 v159, v159\n"
"v_mfma_f32_16x16x32_bf16 a[220:223], v[124:127], a[252:255], a[220:223]\n"
"v_exp_f32 v152, v152\n"
"v_smfmac_f32_16x16x64_bf16 v[196:199], a[240:243], v[128:135], v241\n"
"v_mfma_f32_16x16x32_bf16 v[202:205], a[240:243], v[124:127], v[202:205]\n"
"s_waitcnt lgkmcnt(1)\n"
"ds_read_b64_tr_b16 v[224:225], v239 offset:1024\n"
"v_cvt_pk_bf16_f32 v96, v152, v153\n"
"v_mfma_f32_16x16x32_bf16 v[168:171], v[216:219], a[8:11], v[180:183]\n"
"v_exp_f32 v162, v162\n"
"v_mfma_f32_16x16x32_bf16 v[168:171], v[220:223], a[12:15], v[168:171]\n"
"ds_read_b64_tr_b16 a[250:251], v238 offset:5120\n"
"v_mfma_f32_16x16x32_bf16 v[168:171], v[208:211], a[0:3], v[168:171]\n"
"ds_read_b64_tr_b16 a[252:253], v235 offset:1024\n"
"v_mfma_f32_16x16x32_bf16 v[168:171], v[212:215], a[4:7], v[168:171]\n"
"v_exp_f32 v154, v154\n"
"v_mfma_f32_16x16x32_bf16 v[172:175], v[208:211], a[16:19], v[184:187]\n"
"ds_read_b64_tr_b16 v[226:227], v239 offset:5120\n"
"v_mfma_f32_16x16x32_bf16 v[172:175], v[212:215], a[20:23], v[172:175]\n"
"ds_read_b64_tr_b16 a[246:247], v234 offset:5120\n"
"v_cvt_pk_bf16_f32 v97, v154, v155\n"
"v_mfma_f32_16x16x32_bf16 v[172:175], v[216:219], a[24:27], v[172:175]\n"
"s_add_u32 s40, s56, s54\n"
"ds_read_b64_tr_b16 a[254:255], v235 offset:5120\n"
"v_mfma_f32_16x16x32_bf16 v[172:175], v[220:223], a[28:31], v[172:175]\n"
"buffer_load_dwordx4 v240, s[20:23], s40 offen offset:3072 nt lds\n"
"v_mfma_f32_16x16x32_bf16 v[176:179], v[216:219], a[40:43], v[188:191]\n"
"s_add_u32 s55, s55, s59\n"
"v_exp_f32 v157, v157\n"
"v_mfma_f32_16x16x32_bf16 v[176:179], v[220:223], a[44:47], v[176:179]\n"
"s_add_u32 s41, s55, s57\n"
"v_exp_f32 v170, v170\n"
"v_mfma_f32_16x16x32_bf16 v[176:179], v[208:211], a[32:35], v[176:179]\n"
"s_and_b32 s41, s41, s58\n"
"v_exp_f32 v156, v156\n"
"v_mfma_f32_16x16x32_bf16 v[176:179], v[212:215], a[36:39], v[176:179]\n"
"s_andn2_b32 s56, s55, s58\n"
"v_exp_f32 v166, v166\n"
"v_mfma_f32_16x16x32_bf16 v[152:155], v[208:211], a[192:195], v[192:195]\n"
"s_or_b32 s56, s56, s41\n"
"ds_read_b128 v[208:211], v230 offset:16384\n"
"v_mfma_f32_16x16x32_bf16 v[152:155], v[212:215], a[196:199], v[152:155]\n"
"v_exp_f32 v174, v174\n"
"v_mfma_f32_16x16x32_bf16 v[152:155], v[216:219], a[200:203], v[152:155]\n"
"ds_read_b128 v[212:215], v231 offset:16384\n"
"v_mfma_f32_16x16x32_bf16 v[152:155], v[220:223], a[204:207], v[152:155]\n"
"s_waitcnt lgkmcnt(2)\n"
"ds_read_b128 v[216:219], v230 offset:17408\n"
"v_cvt_pk_bf16_f32 v105, v158, v159\n"
"v_mfma_f32_16x16x32_bf16 a[232:235], v[124:127], a[244:247], a[232:235]\n"
"v_exp_f32 v164, v164\n"
"v_mfma_f32_16x16x32_bf16 v[24:27], v[128:131], a[244:247], v[24:27]\n"
"s_add_u32 m0, s44, 49152\n"
"ds_read_b128 v[220:223], v231 offset:17408\n"
"v_mfma_f32_16x16x32_bf16 v[24:27], v[132:135], a[248:251], v[24:27]\n"
"buffer_load_dwordx4 v240, s[16:19], s56 offen nt lds\n"
"v_mfma_f32_16x16x32_bf16 v[56:59], v[136:139], a[244:247], v[56:59]\n"
"v_exp_f32 v167, v167\n"
"v_mfma_f32_16x16x32_bf16 v[56:59], v[140:143], a[248:251], v[56:59]\n"
"v_exp_f32 v176, v176\n"
"v_mfma_f32_16x16x32_bf16 v[88:91], v[144:147], a[244:247], v[88:91]\n"
"v_exp_f32 v153, v153\n"
"v_mfma_f32_16x16x32_bf16 v[88:91], v[148:151], a[248:251], v[88:91]\n"
"v_exp_f32 v173, v173\n"
"v_mfma_f32_16x16x32_bf16 v[92:95], v[148:151], v[224:227], v[92:95]\n"
"ds_read_b64_tr_b16 a[250:251], v236 offset:4096\n"
"v_cvt_pk_bf16_f32 v121, v166, v167\n"
"v_mfma_f32_16x16x32_bf16 v[92:95], v[144:147], a[252:255], v[92:95]\n"
"ds_read_b64_tr_b16 a[244:245], v232 offset:0\n"
"v_cvt_pk_bf16_f32 v112, v160, v161\n"
"v_mfma_f32_16x16x32_bf16 v[60:63], v[140:143], v[224:227], v[60:63]\n"
"ds_read_b64_tr_b16 a[248:249], v236 offset:0\n"
"v_cvt_pk_bf16_f32 v120, v164, v165\n"
"v_mfma_f32_16x16x32_bf16 v[60:63], v[136:139], a[252:255], v[60:63]\n"
"ds_read_b64_tr_b16 a[246:247], v232 offset:4096\n"
"v_cvt_pk_bf16_f32 v104, v156, v157\n"
"v_mfma_f32_16x16x32_bf16 v[28:31], v[132:135], v[224:227], v[28:31]\n"
"ds_read_b64_tr_b16 v[226:227], v237 offset:4096\n"
"v_cvt_pk_bf16_f32 v113, v162, v163\n"
"v_mfma_f32_16x16x32_bf16 v[28:31], v[128:131], a[252:255], v[28:31]\n"
"ds_read_b64_tr_b16 v[224:225], v237 offset:0\n"
"v_mfma_f32_16x16x32_bf16 a[236:239], v[124:127], a[252:255], a[236:239]\n"
"s_add_u32 s40, s56, s52\n"
"ds_read_b64_tr_b16 a[252:253], v233 offset:0\n"
"v_smfmac_f32_16x16x64_bf16 v[198:201], a[240:243], v[136:143], v241\n"
"s_waitcnt lgkmcnt(7)\n"
"buffer_load_dwordx4 v240, s[16:19], s40 offen offset:1024 nt lds\n"
"v_mfma_f32_16x16x32_bf16 v[156:159], v[208:211], a[0:3], v[180:183]\n"
"v_exp_f32 v169, v169\n"
"v_mfma_f32_16x16x32_bf16 v[156:159], v[212:215], a[4:7], v[156:159]\n"
"v_exp_f32 v168, v168\n"
"v_mfma_f32_16x16x32_bf16 v[156:159], v[216:219], a[8:11], v[156:159]\n"
"v_exp_f32 v175, v175\n"
"v_mfma_f32_16x16x32_bf16 v[156:159], v[220:223], a[12:15], v[156:159]\n"
"v_exp_f32 v172, v172\n"
"v_mfma_f32_16x16x32_bf16 v[160:163], v[216:219], a[24:27], v[184:187]\n"
"ds_read_b64_tr_b16 a[254:255], v233 offset:4096\n"
"v_mfma_f32_16x16x32_bf16 v[160:163], v[220:223], a[28:31], v[160:163]\n"
"v_exp_f32 v155, v155\n"
"v_mfma_f32_16x16x32_bf16 v[160:163], v[208:211], a[16:19], v[160:163]\n"
"v_exp_f32 v177, v177\n"
"v_mfma_f32_16x16x32_bf16 v[160:163], v[212:215], a[20:23], v[160:163]\n"
"v_exp_f32 v179, v179\n"
"v_mfma_f32_16x16x32_bf16 v[164:167], v[208:211], a[32:35], v[188:191]\n"
"ds_read_b128 v[208:211], v230 offset:20480\n"
"v_cvt_pk_bf16_f32 v107, v174, v175\n"
"v_mfma_f32_16x16x32_bf16 v[164:167], v[212:215], a[36:39], v[164:167]\n"
"v_exp_f32 v171, v171\n"
"v_mfma_f32_16x16x32_bf16 v[164:167], v[216:219], a[40:43], v[164:167]\n"
"ds_read_b128 v[212:215], v231 offset:20480\n"
"v_cvt_pk_bf16_f32 v106, v172, v173\n"
"v_mfma_f32_16x16x32_bf16 v[164:167], v[220:223], a[44:47], v[164:167]\n"
"s_waitcnt lgkmcnt(3)\n"
"ds_read_b128 v[220:223], v231 offset:21504\n"
"v_cvt_pk_bf16_f32 v98, v168, v169\n"
"v_mfma_f32_16x16x32_bf16 a[208:211], v[124:127], a[244:247], a[208:211]\n"
"v_exp_f32 v154, v154\n"
"v_mfma_f32_16x16x32_bf16 v[0:3], v[128:131], a[244:247], v[0:3]\n"
"s_add_u32 s40, s56, s53\n"
"ds_read_b128 v[216:219], v230 offset:21504\n"
"v_mfma_f32_16x16x32_bf16 v[0:3], v[132:135], a[248:251], v[0:3]\n"
"buffer_load_dwordx4 v240, s[16:19], s40 offen offset:2048 nt lds\n"
"v_mfma_f32_16x16x32_bf16 v[32:35], v[136:139], a[244:247], v[32:35]\n"
"v_exp_f32 v160, v160\n"
"v_mfma_f32_16x16x32_bf16 v[32:35], v[140:143], a[248:251], v[32:35]\n"
"v_exp_f32 v178, v178\n"
"v_mfma_f32_16x16x32_bf16 v[64:67], v[144:147], a[244:247], v[64:67]\n"
"v_exp_f32 v152, v152\n"
"v_mfma_f32_16x16x32_bf16 v[64:67], v[148:151], a[248:251], v[64:67]\n"
"v_exp_f32 v161, v161\n"
"v_mfma_f32_16x16x32_bf16 v[68:71], v[148:151], v[224:227], v[68:71]\n"
"s_waitcnt lgkmcnt(1)\n"
"ds_read_b64_tr_b16 a[244:245], v232 offset:1024\n"
"v_cvt_pk_bf16_f32 v115, v178, v179\n"
"v_mfma_f32_16x16x32_bf16 v[68:71], v[144:147], a[252:255], v[68:71]\n"
"ds_read_b64_tr_b16 a[248:249], v236 offset:1024\n"
"v_cvt_pk_bf16_f32 v114, v176, v177\n"
"v_mfma_f32_16x16x32_bf16 v[36:39], v[140:143], v[224:227], v[36:39]\n"
"ds_read_b64_tr_b16 a[250:251], v236 offset:5120\n"
"v_cvt_pk_bf16_f32 v99, v170, v171\n"
"v_mfma_f32_16x16x32_bf16 v[36:39], v[136:139], a[252:255], v[36:39]\n"
"v_exp_f32 v163, v163\n"
"v_mfma_f32_16x16x32_bf16 v[4:7], v[132:135], v[224:227], v[4:7]\n"
"s_add_u32 s40, s56, s54\n"
"ds_read_b64_tr_b16 a[246:247], v232 offset:5120\n"
"v_mfma_f32_16x16x32_bf16 v[4:7], v[128:131], a[252:255], v[4:7]\n"
"buffer_load_dwordx4 v240, s[16:19], s40 offen offset:3072 nt lds\n"
"v_mfma_f32_16x16x32_bf16 a[212:215], v[124:127], a[252:255], a[212:215]\n"
"v_exp_f32 v167, v167\n"
"v_smfmac_f32_16x16x64_bf16 v[200:203], a[240:243], v[144:151], v241\n"
"v_cvt_pk_bf16_f32 v122, v152, v153\n"
"v_mfma_f32_16x16x32_bf16 v[168:171], v[208:211], a[0:3], v[180:183]\n"
"v_exp_f32 v159, v159\n"
"v_mfma_f32_16x16x32_bf16 v[168:171], v[212:215], a[4:7], v[168:171]\n"
"s_waitcnt lgkmcnt(2)\n"
"v_exp_f32 v165, v165\n"
"v_mfma_f32_16x16x32_bf16 v[168:171], v[216:219], a[8:11], v[168:171]\n"
"ds_read_b64_tr_b16 a[252:253], v233 offset:1024\n"
"ds_read_b64_tr_b16 v[224:225], v237 offset:1024\n"
"v_mfma_f32_16x16x32_bf16 v[168:171], v[220:223], a[12:15], v[168:171]\n"
"ds_read_b64_tr_b16 a[254:255], v233 offset:5120\n"
"ds_read_b64_tr_b16 v[226:227], v237 offset:5120\n"
"v_mfma_f32_16x16x32_bf16 v[172:175], v[216:219], a[24:27], v[184:187]\n"
"v_exp_f32 v166, v166\n"
"v_mfma_f32_16x16x32_bf16 v[172:175], v[220:223], a[28:31], v[172:175]\n"
"v_exp_f32 v164, v164\n"
"v_mfma_f32_16x16x32_bf16 v[172:175], v[208:211], a[16:19], v[172:175]\n"
"v_exp_f32 v157, v157\n"
"v_mfma_f32_16x16x32_bf16 v[172:175], v[212:215], a[20:23], v[172:175]\n"
"s_cbranch_vccnz LPCL1_%=\nLPKL1_%=:\n"
"v_cvt_pk_bf16_f32 v123, v154, v155\n"
"v_mfma_f32_16x16x32_bf16 v[176:179], v[208:211], a[32:35], v[188:191]\n"
"v_exp_f32 v158, v158\n"
"v_mfma_f32_16x16x32_bf16 v[176:179], v[212:215], a[36:39], v[176:179]\n"
"v_exp_f32 v162, v162\n"
"v_mfma_f32_16x16x32_bf16 v[176:179], v[216:219], a[40:43], v[176:179]\n"
"v_exp_f32 v156, v156\n"
"v_mfma_f32_16x16x32_bf16 v[176:179], v[220:223], a[44:47], v[176:179]\n"
"s_waitcnt lgkmcnt(0)\n"
"v_cvt_pk_bf16_f32 v101, v158, v159\n"
"v_mfma_f32_16x16x32_bf16 a[224:227], v[124:127], a[244:247], a[224:227]\n"
"v_exp_f32 v169, v169\n"
"v_mfma_f32_16x16x32_bf16 v[16:19], v[128:131], a[244:247], v[16:19]\n"
"s_waitcnt vmcnt(8)\n"
"v_exp_f32 v172, v172\n"
"v_mfma_f32_16x16x32_bf16 v[16:19], v[132:135], a[248:251], v[16:19]\n"
"s_barrier\n"
"ds_read_b128 v[216:219], v228 offset:33792\n"
"v_mfma_f32_16x16x32_bf16 v[48:51], v[136:139], a[244:247], v[48:51]\n"
"ds_read_b128 v[212:215], v229 offset:32768\n"
"v_cvt_pk_bf16_f32 v108, v160, v161\n"
"v_mfma_f32_16x16x32_bf16 v[48:51], v[140:143], a[248:251], v[48:51]\n"
"ds_read_b128 v[208:211], v228 offset:32768\n"
"v_cvt_pk_bf16_f32 v100, v156, v157\n"
"v_mfma_f32_16x16x32_bf16 v[80:83], v[144:147], a[244:247], v[80:83]\n"
"ds_read_b128 v[220:223], v229 offset:33792\n"
"v_cvt_pk_bf16_f32 v117, v166, v167\n"
"v_mfma_f32_16x16x32_bf16 v[80:83], v[148:151], a[248:251], v[80:83]\n"
"ds_read_b64_tr_b16 a[244:245], v234 offset:16384\n"
"v_cvt_pk_bf16_f32 v109, v162, v163\n"
"v_mfma_f32_16x16x32_bf16 v[84:87], v[148:151], v[224:227], v[84:87]\n"
"ds_read_b64_tr_b16 a[248:249], v238 offset:16384\n"
"v_mfma_f32_16x16x32_bf16 v[84:87], v[144:147], a[252:255], v[84:87]\n"
"ds_read_b64_tr_b16 a[246:247], v234 offset:20480\n"
"v_mfma_f32_16x16x32_bf16 v[52:55], v[140:143], v[224:227], v[52:55]\n"
"ds_read_b64_tr_b16 a[250:251], v238 offset:20480\n"
"v_mfma_f32_16x16x32_bf16 v[52:55], v[136:139], a[252:255], v[52:55]\n"
"v_exp_f32 v175, v175\n"
"v_mfma_f32_16x16x32_bf16 v[20:23], v[132:135], v[224:227], v[20:23]\n"
"ds_read_b64_tr_b16 v[224:225], v239 offset:16384\n"
"v_mfma_f32_16x16x32_bf16 v[20:23], v[128:131], a[252:255], v[20:23]\n"
"v_exp_f32 v173, v173\n"
"v_mfma_f32_16x16x32_bf16 a[228:231], v[124:127], a[252:255], a[228:231]\n"
"s_waitcnt lgkmcnt(5)\n"
"ds_read_b64_tr_b16 a[254:255], v235 offset:20480\n"
"v_cvt_pk_bf16_f32 v116, v164, v165\n"
"LAY2_%=:\n"
"v_mfma_f32_16x16x32_bf16 v[152:155], v[208:211], a[0:3], v[180:183]\n"
"s_waitcnt lgkmcnt(8)\n"
"v_exp_f32 v174, v174\n"
"v_mfma_f32_16x16x32_bf16 v[152:155], v[212:215], a[4:7], v[152:155]\n"
"s_waitcnt lgkmcnt(7)\n"
"ds_read_b64_tr_b16 a[252:253], v235 offset:16384\n"
"v_cvt_pk_bf16_f32 v111, v174, v175\n"
"v_mfma_f32_16x16x32_bf16 v[152:155], v[216:219], a[8:11], v[152:155]\n"
"s_waitcnt lgkmcnt(7)\n"
"s_add_u32 m0, s44, 114688\n"
"ds_read_b64_tr_b16 v[226:227], v239 offset:20480\n"
"v_mfma_f32_16x16x32_bf16 v[152:155], v[220:223], a[12:15], v[152:155]\n"
"buffer_load_dwordx4 v240, s[20:23], s56 offen nt lds\n"
"v_mfma_f32_16x16x32_bf16 v[156:159], v[216:219], a[24:27], v[184:187]\n"
"v_exp_f32 v171, v171\n"
"v_mfma_f32_16x16x32_bf16 v[156:159], v[220:223], a[28:31], v[156:159]\n"
"v_exp_f32 v168, v168\n"
"v_mfma_f32_16x16x32_bf16 v[156:159], v[208:211], a[16:19], v[156:159]\n"
"v_exp_f32 v176, v176\n"
"v_mfma_f32_16x16x32_bf16 v[156:159], v[212:215], a[20:23], v[156:159]\n"
"v_exp_f32 v177, v177\n"
"v_mfma_f32_16x16x32_bf16 v[160:163], v[208:211], a[32:35], v[188:191]\n"
"v_exp_f32 v179, v179\n"
"v_mfma_f32_16x16x32_bf16 v[160:163], v[212:215], a[36:39], v[160:163]\n"
"v_exp_f32 v170, v170\n"
"v_mfma_f32_16x16x32_bf16 v[160:163], v[216:219], a[40:43], v[160:163]\n"
"v_exp_f32 v153, v153\n"
"v_mfma_f32_16x16x32_bf16 v[160:163], v[220:223], a[44:47], v[160:163]\n"
"v_cvt_pk_bf16_f32 v102, v168, v169\n"
"v_mfma_f32_16x16x32_bf16 v[164:167], v[216:219], a[200:203], v[192:195]\n"
"ds_read_b128 v[216:219], v228 offset:37888\n"
"v_cvt_pk_bf16_f32 v103, v170, v171\n"
"v_mfma_f32_16x16x32_bf16 v[164:167], v[220:223], a[204:207], v[164:167]\n"
"v_cvt_pk_bf16_f32 v110, v172, v173\n"
"v_cvt_pk_bf16_f32 v118, v176, v177\n"
"v_mfma_f32_16x16x32_bf16 v[164:167], v[208:211], a[192:195], v[164:167]\n"
"s_add_u32 s40, s56, s52\n"
"ds_read_b128 v[208:211], v228 offset:36864\n"
"v_mfma_f32_16x16x32_bf16 v[164:167], v[212:215], a[196:199], v[164:167]\n"
"s_waitcnt lgkmcnt(2)\n"
"buffer_load_dwordx4 v240, s[20:23], s40 offen offset:1024 nt lds\n"
"v_mfma_f32_16x16x32_bf16 a[216:219], v[120:123], a[244:247], a[216:219]\n"
"v_exp_f32 v155, v155\n"
"v_mfma_f32_16x16x32_bf16 v[8:11], v[96:99], a[244:247], v[8:11]\n"
"s_waitcnt lgkmcnt(2)\n"
"v_exp_f32 v161, v161\n"
"v_mfma_f32_16x16x32_bf16 v[8:11], v[100:103], a[248:251], v[8:11]\n"
"v_exp_f32 v163, v163\n"
"v_mfma_f32_16x16x32_bf16 v[40:43], v[104:107], a[244:247], v[40:43]\n"
"v_exp_f32 v178, v178\n"
"v_mfma_f32_16x16x32_bf16 v[40:43], v[108:111], a[248:251], v[40:43]\n"
"ds_read_b128 v[220:223], v229 offset:37888\n"
"v_cvt_pk_bf16_f32 v119, v178, v179\n"
"v_mfma_f32_16x16x32_bf16 v[72:75], v[112:115], a[244:247], v[72:75]\n"
"ds_read_b64_tr_b16 a[244:245], v234 offset:17408\n"
"v_mfma_f32_16x16x32_bf16 v[72:75], v[116:119], a[248:251], v[72:75]\n"
"v_exp_f32 v158, v158\n"
"v_mfma_f32_16x16x32_bf16 v[76:79], v[116:119], v[224:227], v[76:79]\n"
"ds_read_b128 v[212:215], v229 offset:36864\n"
"v_mfma_f32_16x16x32_bf16 v[76:79], v[112:115], a[252:255], v[76:79]\n"
"v_exp_f32 v160, v160\n"
"v_mfma_f32_16x16x32_bf16 v[44:47], v[108:111], v[224:227], v[44:47]\n"
"s_add_u32 s40, s56, s53\n"
"ds_read_b64_tr_b16 a[248:249], v238 offset:17408\n"
"v_mfma_f32_16x16x32_bf16 v[44:47], v[104:107], a[252:255], v[44:47]\n"
"buffer_load_dwordx4 v240, s[20:23], s40 offen offset:2048 nt lds\n"
"v_mfma_f32_16x16x32_bf16 v[12:15], v[100:103], v[224:227], v[12:15]\n"
"v_exp_f32 v165, v165\n"
"v_mfma_f32_16x16x32_bf16 v[12:15], v[96:99], a[252:255], v[12:15]\n"
"v_exp_f32 v159, v159\n"
"v_mfma_f32_16x16x32_bf16 a[220:223], v[120:123], a[252:255], a[220:223]\n"
"v_exp_f32 v152, v152\n"
"v_smfmac_f32_16x16x64_bf16 v[196:199], a[240:243], v[96:103], v241\n"
"v_mfma_f32_16x16x32_bf16 v[202:205], a[240:243], v[120:123], v[202:205]\n"
"s_waitcnt lgkmcnt(1)\n"
"ds_read_b64_tr_b16 v[224:225], v239 offset:17408\n"
"v_cvt_pk_bf16_f32 v128, v152, v153\n"
"v_mfma_f32_16x16x32_bf16 v[168:171], v[216:219], a[8:11], v[180:183]\n"
"v_exp_f32 v162, v162\n"
"v_mfma_f32_16x16x32_bf16 v[168:171], v[220:223], a[12:15], v[168:171]\n"
"ds_read_b64_tr_b16 a[250:251], v238 offset:21504\n"
"v_mfma_f32_16x16x32_bf16 v[168:171], v[208:211], a[0:3], v[168:171]\n"
"ds_read_b64_tr_b16 a[252:253], v235 offset:17408\n"
"v_mfma_f32_16x16x32_bf16 v[168:171], v[212:215], a[4:7], v[168:171]\n"
"v_exp_f32 v154, v154\n"
"v_mfma_f32_16x16x32_bf16 v[172:175], v[208:211], a[16:19], v[184:187]\n"
"ds_read_b64_tr_b16 v[226:227], v239 offset:21504\n"
"v_mfma_f32_16x16x32_bf16 v[172:175], v[212:215], a[20:23], v[172:175]\n"
"ds_read_b64_tr_b16 a[246:247], v234 offset:21504\n"
"v_cvt_pk_bf16_f32 v129, v154, v155\n"
"v_mfma_f32_16x16x32_bf16 v[172:175], v[216:219], a[24:27], v[172:175]\n"
"s_add_u32 s40, s56, s54\n"
"ds_read_b64_tr_b16 a[254:255], v235 offset:21504\n"
"v_mfma_f32_16x16x32_bf16 v[172:175], v[220:223], a[28:31], v[172:175]\n"
"buffer_load_dwordx4 v240, s[20:23], s40 offen offset:3072 nt lds\n"
"v_mfma_f32_16x16x32_bf16 v[176:179], v[216:219], a[40:43], v[188:191]\n"
"s_add_u32 s55, s55, s59\n"
"v_exp_f32 v157, v157\n"
"v_mfma_f32_16x16x32_bf16 v[176:179], v[220:223], a[44:47], v[176:179]\n"
"s_add_u32 s41, s55, s57\n"
"v_exp_f32 v170, v170\n"
"v_mfma_f32_16x16x32_bf16 v[176:179], v[208:211], a[32:35], v[176:179]\n"
"s_and_b32 s41, s41, s58\n"
"v_exp_f32 v156, v156\n"
"v_mfma_f32_16x16x32_bf16 v[176:179], v[212:215], a[36:39], v[176:179]\n"
"s_andn2_b32 s56, s55, s58\n"
"v_exp_f32 v166, v166\n"
"v_mfma_f32_16x16x32_bf16 v[152:155], v[208:211], a[192:195], v[192:195]\n"
"s_or_b32 s56, s56, s41\n"
"ds_read_b128 v[208:211], v230 offset:32768\n"
"v_mfma_f32_16x16x32_bf16 v[152:155], v[212:215], a[196:199], v[152:155]\n"
"s_cmp_ge_u32 s47, 15\n"
"v_exp_f32 v174, v174\n"
"v_mfma_f32_16x16x32_bf16 v[152:155], v[216:219], a[200:203], v[152:155]\n"
"s_cbranch_scc1 LSW_%=\nLSWK_%=:\n"
"ds_read_b128 v[212:215], v231 offset:32768\n"
"v_mfma_f32_16x16x32_bf16 v[152:155], v[220:223], a[204:207], v[152:155]\n"
"s_waitcnt lgkmcnt(2)\n"
"ds_read_b128 v[216:219], v230 offset:33792\n"
"v_cvt_pk_bf16_f32 v137, v158, v159\n"
"v_mfma_f32_16x16x32_bf16 a[232:235], v[120:123], a[244:247], a[232:235]\n"
"v_exp_f32 v164, v164\n"
"v_mfma_f32_16x16x32_bf16 v[24:27], v[96:99], a[244:247], v[24:27]\n"
"s_mov_b32 m0, s44\n"
"ds_read_b128 v[220:223], v231 offset:33792\n"
"v_mfma_f32_16x16x32_bf16 v[24:27], v[100:103], a[248:251], v[24:27]\n"
"buffer_load_dwordx4 v240, s[16:19], s56 offen nt lds\n"
"v_mfma_f32_16x16x32_bf16 v[56:59], v[104:107], a[244:247], v[56:59]\n"
"v_exp_f32 v167, v167\n"
"v_mfma_f32_16x16x32_bf16 v[56:59], v[108:111], a[248:251], v[56:59]\n"
"v_exp_f32 v176, v176\n"
"v_mfma_f32_16x16x32_bf16 v[88:91], v[112:115], a[244:247], v[88:91]\n"
"v_exp_f32 v153, v153\n"
"v_mfma_f32_16x16x32_bf16 v[88:91], v[116:119], a[248:251], v[88:91]\n"
"v_exp_f32 v173, v173\n"
"v_mfma_f32_16x16x32_bf16 v[92:95], v[116:119], v[224:227], v[92:95]\n"
"ds_read_b64_tr_b16 a[250:251], v236 offset:20480\n"
"v_cvt_pk_bf16_f32 v125, v166, v167\n"
"v_mfma_f32_16x16x32_bf16 v[92:95], v[112:115], a[252:255], v[92:95]\n"
"ds_read_b64_tr_b16 a[244:245], v232 offset:16384\n"
"v_cvt_pk_bf16_f32 v144, v160, v161\n"
"v_mfma_f32_16x16x32_bf16 v[60:63], v[108:111], v[224:227], v[60:63]\n"
"ds_read_b64_tr_b16 a[248:249], v236 offset:16384\n"
"v_cvt_pk_bf16_f32 v124, v164, v165\n"
"v_mfma_f32_16x16x32_bf16 v[60:63], v[104:107], a[252:255], v[60:63]\n"
"ds_read_b64_tr_b16 a[246:247], v232 offset:20480\n"
"v_cvt_pk_bf16_f32 v136, v156, v157\n"
"v_mfma_f32_16x16x32_bf16 v[28:31], v[100:103], v[224:227], v[28:31]\n"
"ds_read_b64_tr_b16 v[226:227], v237 offset:20480\n"
"v_cvt_pk_bf16_f32 v145, v162, v163\n"
"v_mfma_f32_16x16x32_bf16 v[28:31], v[96:99], a[252:255], v[28:31]\n"
"ds_read_b64_tr_b16 v[224:225], v237 offset:16384\n"
"v_mfma_f32_16x16x32_bf16 a[236:239], v[120:123], a[252:255], a[236:239]\n"
"s_add_u32 s40, s56, s52\n"
"ds_read_b64_tr_b16 a[252:253], v233 offset:16384\n"
"v_smfmac_f32_16x16x64_bf16 v[198:201], a[240:243], v[104:111], v241\n"
"s_waitcnt lgkmcnt(7)\n"
"buffer_load_dwordx4 v240, s[16:19], s40 offen offset:1024 nt lds\n"
"v_mfma_f32_16x16x32_bf16 v[156:159], v[208:211], a[0:3], v[180:183]\n"
"v_exp_f32 v169, v169\n"
"v_mfma_f32_16x16x32_bf16 v[156:159], v[212:215], a[4:7], v[156:159]\n"
"v_exp_f32 v168, v168\n"
"v_mfma_f32_16x16x32_bf16 v[156:159], v[216:219], a[8:11], v[156:159]\n"
"v_exp_f32 v175, v175\n"
"v_mfma_f32_16x16x32_bf16 v[156:159], v[220:223], a[12:15], v[156:159]\n"
"v_exp_f32 v172, v172\n"
"v_mfma_f32_16x16x32_bf16 v[160:163], v[216:219], a[24:27], v[184:187]\n"
"ds_read_b64_tr_b16 a[254:255], v233 offset:20480\n"
"v_mfma_f32_16x16x32_bf16 v[160:163], v[220:223], a[28:31], v[160:163]\n"
"v_exp_f32 v155, v155\n"
"v_mfma_f32_16x16x32_bf16 v[160:163], v[208:211], a[16:19], v[160:163]\n"
"v_exp_f32 v177, v177\n"
"v_mfma_f32_16x16x32_bf16 v[160:163], v[212:215], a[20:23], v[160:163]\n"
"v_exp_f32 v179, v179\n"
"v_mfma_f32_16x16x32_bf16 v[164:167], v[208:211], a[32:35], v[188:191]\n"
"ds_read_b128 v[208:211], v230 offset:36864\n"
"v_cvt_pk_bf16_f32 v139, v174, v175\n"
"v_mfma_f32_16x16x32_bf16 v[164:167], v[212:215], a[36:39], v[164:167]\n"
"v_exp_f32 v171, v171\n"
"v_mfma_f32_16x16x32_bf16 v[164:167], v[216:219], a[40:43], v[164:167]\n"
"ds_read_b128 v[212:215], v231 offset:36864\n"
"v_cvt_pk_bf16_f32 v138, v172, v173\n"
"v_mfma_f32_16x16x32_bf16 v[164:167], v[220:223], a[44:47], v[164:167]\n"
"s_waitcnt lgkmcnt(3)\n"
"ds_read_b128 v[220:223], v231 offset:37888\n"
"v_cvt_pk_bf16_f32 v130, v168, v169\n"
"v_mfma_f32_16x16x32_bf16 a[208:211], v[120:123], a[244:247], a[208:211]\n"
"v_exp_f32 v154, v154\n"
"v_mfma_f32_16x16x32_bf16 v[0:3], v[96:99], a[244:247], v[0:3]\n"
"s_add_u32 s40, s56, s53\n"
"ds_read_b128 v[216:219], v230 offset:37888\n"
"v_mfma_f32_16x16x32_bf16 v[0:3], v[100:103], a[248:251], v[0:3]\n"
"buffer_load_dwordx4 v240, s[16:19], s40 offen offset:2048 nt lds\n"
"v_mfma_f32_16x16x32_bf16 v[32:35], v[104:107], a[244:247], v[32:35]\n"
"v_exp_f32 v160, v160\n"
"v_mfma_f32_16x16x32_bf16 v[32:35], v[108:111], a[248:251], v[32:35]\n"
"v_exp_f32 v178, v178\n"
"v_mfma_f32_16x16x32_bf16 v[64:67], v[112:115], a[244:247], v[64:67]\n"
"v_exp_f32 v152, v152\n"
"v_mfma_f32_16x16x32_bf16 v[64:67], v[116:119], a[248:251], v[64:67]\n"
"v_exp_f32 v161, v161\n"
"v_mfma_f32_16x16x32_bf16 v[68:71], v[116:119], v[224:227], v[68:71]\n"
"s_waitcnt lgkmcnt(1)\n"
"ds_read_b64_tr_b16 a[244:245], v232 offset:17408\n"
"v_cvt_pk_bf16_f32 v147, v178, v179\n"
"v_mfma_f32_16x16x32_bf16 v[68:71], v[112:115], a[252:255], v[68:71]\n"
"ds_read_b64_tr_b16 a[248:249], v236 offset:17408\n"
"v_cvt_pk_bf16_f32 v146, v176, v177\n"
"v_mfma_f32_16x16x32_bf16 v[36:39], v[108:111], v[224:227], v[36:39]\n"
"ds_read_b64_tr_b16 a[250:251], v236 offset:21504\n"
"v_cvt_pk_bf16_f32 v131, v170, v171\n"
"v_mfma_f32_16x16x32_bf16 v[36:39], v[104:107], a[252:255], v[36:39]\n"
"v_exp_f32 v163, v163\n"
"v_mfma_f32_16x16x32_bf16 v[4:7], v[100:103], v[224:227], v[4:7]\n"
"s_add_u32 s40, s56, s54\n"
"ds_read_b64_tr_b16 a[246:247], v232 offset:21504\n"
"v_mfma_f32_16x16x32_bf16 v[4:7], v[96:99], a[252:255], v[4:7]\n"
"buffer_load_dwordx4 v240, s[16:19], s40 offen offset:3072 nt lds\n"
"v_mfma_f32_16x16x32_bf16 a[212:215], v[120:123], a[252:255], a[212:215]\n"
"v_exp_f32 v167, v167\n"
"v_smfmac_f32_16x16x64_bf16 v[200:203], a[240:243], v[112:119], v241\n"
"v_cvt_pk_bf16_f32 v126, v152, v153\n"
"v_mfma_f32_16x16x32_bf16 v[168:171], v[208:211], a[0:3], v[180:183]\n"
"v_exp_f32 v159, v159\n"
"v_mfma_f32_16x16x32_bf16 v[168:171], v[212:215], a[4:7], v[168:171]\n"
"s_waitcnt lgkmcnt(2)\n"
"v_exp_f32 v165, v165\n"
"v_mfma_f32_16x16x32_bf16 v[168:171], v[216:219], a[8:11], v[168:171]\n"
"ds_read_b64_tr_b16 a[252:253], v233 offset:17408\n"
"ds_read_b64_tr_b16 v[224:225], v237 offset:17408\n"
"v_mfma_f32_16x16x32_bf16 v[168:171], v[220:223], a[12:15], v[168:171]\n"
"ds_read_b64_tr_b16 a[254:255], v233 offset:21504\n"
"ds_read_b64_tr_b16 v[226:227], v237 offset:21504\n"
"v_mfma_f32_16x16x32_bf16 v[172:175], v[216:219], a[24:27], v[184:187]\n"
"v_exp_f32 v166, v166\n"
"v_mfma_f32_16x16x32_bf16 v[172:175], v[220:223], a[28:31], v[172:175]\n"
"v_exp_f32 v164, v164\n"
"v_mfma_f32_16x16x32_bf16 v[172:175], v[208:211], a[16:19], v[172:175]\n"
"v_exp_f32 v157, v157\n"
"v_mfma_f32_16x16x32_bf16 v[172:175], v[212:215], a[20:23], v[172:175]\n"
"s_cbranch_vccnz LPCL2_%=\nLPKL2_%=:\n"
"v_cvt_pk_bf16_f32 v127, v154, v155\n"
"v_mfma_f32_16x16x32_bf16 v[176:179], v[208:211], a[32:35], v[188:191]\n"
"v_exp_f32 v158, v158\n"
"v_mfma_f32_16x16x32_bf16 v[176:179], v[212:215], a[36:39], v[176:179]\n"
"v_exp_f32 v162, v162\n"
"v_mfma_f32_16x16x32_bf16 v[176:179], v[216:219], a[40:43], v[176:179]\n"
"v_exp_f32 v156, v156\n"
"v_mfma_f32_16x16x32_bf16 v[176:179], v[220:223], a[44:47], v[176:179]\n"
"s_waitcnt lgkmcnt(0)\n"
"v_cvt_pk_bf16_f32 v133, v158, v159\n"
"v_mfma_f32_16x16x32_bf16 a[224:227], v[120:123], a[244:247], a[224:227]\n"
"v_exp_f32 v169, v169\n"
"v_mfma_f32_16x16x32_bf16 v[16:19], v[96:99], a[244:247], v[16:19]\n"
"s_waitcnt vmcnt(8)\n"
"v_exp_f32 v172, v172\n"
"v_mfma_f32_16x16x32_bf16 v[16:19], v[100:103], a[248:251], v[16:19]\n"
"s_barrier\n"
"ds_read_b128 v[216:219], v228 offset:50176\n"
"v_mfma_f32_16x16x32_bf16 v[48:51], v[104:107], a[244:247], v[48:51]\n"
"ds_read_b128 v[212:215], v229 offset:49152\n"
"v_cvt_pk_bf16_f32 v140, v160, v161\n"
"v_mfma_f32_16x16x32_bf16 v[48:51], v[108:111], a[248:251], v[48:51]\n"
"ds_read_b128 v[208:211], v228 offset:49152\n"
"v_cvt_pk_bf16_f32 v132, v156, v157\n"
"v_mfma_f32_16x16x32_bf16 v[80:83], v[112:115], a[244:247], v[80:83]\n"
"ds_read_b128 v[220:223], v229 offset:50176\n"
"v_cvt_pk_bf16_f32 v149, v166, v167\n"
"v_mfma_f32_16x16x32_bf16 v[80:83], v[116:119], a[248:251], v[80:83]\n"
"ds_read_b64_tr_b16 a[244:245], v234 offset:32768\n"
"v_cvt_pk_bf16_f32 v141, v162, v163\n"
"v_mfma_f32_16x16x32_bf16 v[84:87], v[116:119], v[224:227], v[84:87]\n"
"ds_read_b64_tr_b16 a[248:249], v238 offset:32768\n"
"v_mfma_f32_16x16x32_bf16 v[84:87], v[112:115], a[252:255], v[84:87]\n"
"ds_read_b64_tr_b16 a[246:247], v234 offset:36864\n"
"v_mfma_f32_16x16x32_bf16 v[52:55], v[108:111], v[224:227], v[52:55]\n"
"ds_read_b64_tr_b16 a[250:251], v238 offset:36864\n"
"v_mfma_f32_16x16x32_bf16 v[52:55], v[104:107], a[252:255], v[52:55]\n"
"v_exp_f32 v175, v175\n"
"v_mfma_f32_16x16x32_bf16 v[20:23], v[100:103], v[224:227], v[20:23]\n"
"ds_read_b64_tr_b16 v[224:225], v239 offset:32768\n"
"v_mfma_f32_16x16x32_bf16 v[20:23], v[96:99], a[252:255], v[20:23]\n"
"v_exp_f32 v173, v173\n"
"v_mfma_f32_16x16x32_bf16 a[228:231], v[120:123], a[252:255], a[228:231]\n"
"s_waitcnt lgkmcnt(5)\n"
"ds_read_b64_tr_b16 a[254:255], v235 offset:36864\n"
"v_cvt_pk_bf16_f32 v148, v164, v165\n"
"v_mfma_f32_16x16x32_bf16 v[152:155], v[208:211], a[0:3], v[180:183]\n"
"v_exp_f32 v174, v174\n"
"v_mfma_f32_16x16x32_bf16 v[152:155], v[212:215], a[4:7], v[152:155]\n"
"ds_read_b64_tr_b16 a[252:253], v235 offset:32768\n"
"v_cvt_pk_bf16_f32 v143, v174, v175\n"
"v_mfma_f32_16x16x32_bf16 v[152:155], v[216:219], a[8:11], v[152:155]\n"
"s_add_u32 m0, s44, 65536\n"
"ds_read_b64_tr_b16 v[226:227], v239 offset:36864\n"
"v_mfma_f32_16x16x32_bf16 v[152:155], v[220:223], a[12:15], v[152:155]\n"
"buffer_load_dwordx4 v240, s[20:23], s56 offen nt lds\n"
"v_mfma_f32_16x16x32_bf16 v[156:159], v[216:219], a[24:27], v[184:187]\n"
"v_exp_f32 v171, v171\n"
"v_mfma_f32_16x16x32_bf16 v[156:159], v[220:223], a[28:31], v[156:159]\n"
"v_exp_f32 v168, v168\n"
"v_mfma_f32_16x16x32_bf16 v[156:159], v[208:211], a[16:19], v[156:159]\n"
"v_exp_f32 v176, v176\n"
"v_mfma_f32_16x16x32_bf16 v[156:159], v[212:215], a[20:23], v[156:159]\n"
"v_exp_f32 v177, v177\n"
"v_mfma_f32_16x16x32_bf16 v[160:163], v[208:211], a[32:35], v[188:191]\n"
"v_exp_f32 v179, v179\n"
"v_mfma_f32_16x16x32_bf16 v[160:163], v[212:215], a[36:39], v[160:163]\n"
"v_exp_f32 v170, v170\n"
"v_mfma_f32_16x16x32_bf16 v[160:163], v[216:219], a[40:43], v[160:163]\n"
"v_exp_f32 v153, v153\n"
"v_mfma_f32_16x16x32_bf16 v[160:163], v[220:223], a[44:47], v[160:163]\n"
"v_cvt_pk_bf16_f32 v134, v168, v169\n"
"v_mfma_f32_16x16x32_bf16 v[164:167], v[216:219], a[200:203], v[192:195]\n"
"ds_read_b128 v[216:219], v228 offset:54272\n"
"v_cvt_pk_bf16_f32 v135, v170, v171\n"
"v_mfma_f32_16x16x32_bf16 v[164:167], v[220:223], a[204:207], v[164:167]\n"
"v_cvt_pk_bf16_f32 v142, v172, v173\n"
"v_cvt_pk_bf16_f32 v150, v176, v177\n"
"v_mfma_f32_16x16x32_bf16 v[164:167], v[208:211], a[192:195], v[164:167]\n"
"s_add_u32 s40, s56, s52\n"
"ds_read_b128 v[208:211], v228 offset:53248\n"
"v_mfma_f32_16x16x32_bf16 v[164:167], v[212:215], a[196:199], v[164:167]\n"
"s_waitcnt lgkmcnt(2)\n"
"buffer_load_dwordx4 v240, s[20:23], s40 offen offset:1024 nt lds\n"
"v_mfma_f32_16x16x32_bf16 a[216:219], v[124:127], a[244:247], a[216:219]\n"
"v_exp_f32 v155, v155\n"
"v_mfma_f32_16x16x32_bf16 v[8:11], v[128:131], a[244:247], v[8:11]\n"
"v_exp_f32 v161, v161\n"
"v_mfma_f32_16x16x32_bf16 v[8:11], v[132:135], a[248:251], v[8:11]\n"
"v_exp_f32 v163, v163\n"
"v_mfma_f32_16x16x32_bf16 v[40:43], v[136:139], a[244:247], v[40:43]\n"
"v_exp_f32 v178, v178\n"
"v_mfma_f32_16x16x32_bf16 v[40:43], v[140:143], a[248:251], v[40:43]\n"
"ds_read_b128 v[220:223], v229 offset:54272\n"
"v_cvt_pk_bf16_f32 v151, v178, v179\n"
"v_mfma_f32_16x16x32_bf16 v[72:75], v[144:147], a[244:247], v[72:75]\n"
"ds_read_b64_tr_b16 a[244:245], v234 offset:33792\n"
"v_mfma_f32_16x16x32_bf16 v[72:75], v[148:151], a[248:251], v[72:75]\n"
"v_exp_f32 v158, v158\n"
"v_mfma_f32_16x16x32_bf16 v[76:79], v[148:151], v[224:227], v[76:79]\n"
"ds_read_b128 v[212:215], v229 offset:53248\n"
"v_mfma_f32_16x16x32_bf16 v[76:79], v[144:147], a[252:255], v[76:79]\n"
"v_exp_f32 v160, v160\n"
"v_mfma_f32_16x16x32_bf16 v[44:47], v[140:143], v[224:227], v[44:47]\n"
"s_add_u32 s40, s56, s53\n"
"ds_read_b64_tr_b16 a[248:249], v238 offset:33792\n"
"v_mfma_f32_16x16x32_bf16 v[44:47], v[136:139], a[252:255], v[44:47]\n"
"buffer_load_dwordx4 v240, s[20:23], s40 offen offset:2048 nt lds\n"
"v_mfma_f32_16x16x32_bf16 v[12:15], v[132:135], v[224:227], v[12:15]\n"
"v_exp_f32 v165, v165\n"
"v_mfma_f32_16x16x32_bf16 v[12:15], v[128:131], a[252:255], v[12:15]\n"
"v_exp_f32 v159, v159\n"
"v_mfma_f32_16x16x32_bf16 a[220:223], v[124:127], a[252:255], a[220:223]\n"
"v_exp_f32 v152, v152\n"
"v_smfmac_f32_16x16x64_bf16 v[196:199], a[240:243], v[128:135], v241\n"
"v_mfma_f32_16x16x32_bf16 v[202:205], a[240:243], v[124:127], v[202:205]\n"
"s_waitcnt lgkmcnt(1)\n"
"ds_read_b64_tr_b16 v[224:225], v239 offset:33792\n"
"v_cvt_pk_bf16_f32 v96, v152, v153\n"
"v_mfma_f32_16x16x32_bf16 v[168:171], v[216:219], a[8:11], v[180:183]\n"
"v_exp_f32 v162, v162\n"
"v_mfma_f32_16x16x32_bf16 v[168:171], v[220:223], a[12:15], v[168:171]\n"
"ds_read_b64_tr_b16 a[250:251], v238 offset:37888\n"
"v_mfma_f32_16x16x32_bf16 v[168:171], v[208:211], a[0:3], v[168:171]\n"
"ds_read_b64_tr_b16 a[252:253], v235 offset:33792\n"
"v_mfma_f32_16x16x32_bf16 v[168:171], v[212:215], a[4:7], v[168:171]\n"
"v_exp_f32 v154, v154\n"
"v_mfma_f32_16x16x32_bf16 v[172:175], v[208:211], a[16:19], v[184:187]\n"
"ds_read_b64_tr_b16 v[226:227], v239 offset:37888\n"
"v_mfma_f32_16x16x32_bf16 v[172:175], v[212:215], a[20:23], v[172:175]\n"
"ds_read_b64_tr_b16 a[246:247], v234 offset:37888\n"
"v_cvt_pk_bf16_f32 v97, v154, v155\n"
"v_mfma_f32_16x16x32_bf16 v[172:175], v[216:219], a[24:27], v[172:175]\n"
"s_add_u32 s40, s56, s54\n"
"ds_read_b64_tr_b16 a[254:255], v235 offset:37888\n"
"v_mfma_f32_16x16x32_bf16 v[172:175], v[220:223], a[28:31], v[172:175]\n"
"buffer_load_dwordx4 v240, s[20:23], s40 offen offset:3072 nt lds\n"
"v_mfma_f32_16x16x32_bf16 v[176:179], v[216:219], a[40:43], v[188:191]\n"
"s_add_u32 s55, s55, s59\n"
"v_exp_f32 v157, v157\n"
"v_mfma_f32_16x16x32_bf16 v[176:179], v[220:223], a[44:47], v[176:179]\n"
"s_add_u32 s41, s55, s57\n"
"v_exp_f32 v170, v170\n"
"v_mfma_f32_16x16x32_bf16 v[176:179], v[208:211], a[32:35], v[176:179]\n"
"s_and_b32 s41, s41, s58\n"
"v_exp_f32 v156, v156\n"
"v_mfma_f32_16x16x32_bf16 v[176:179], v[212:215], a[36:39], v[176:179]\n"
"s_andn2_b32 s56, s55, s58\n"
"v_exp_f32 v166, v166\n"
"v_mfma_f32_16x16x32_bf16 v[152:155], v[208:211], a[192:195], v[192:195]\n"
"s_or_b32 s56, s56, s41\n"
"ds_read_b128 v[208:211], v230 offset:49152\n"
"v_mfma_f32_16x16x32_bf16 v[152:155], v[212:215], a[196:199], v[152:155]\n"
"v_exp_f32 v174, v174\n"
"v_mfma_f32_16x16x32_bf16 v[152:155], v[216:219], a[200:203], v[152:155]\n"
"ds_read_b128 v[212:215], v231 offset:49152\n"
"v_mfma_f32_16x16x32_bf16 v[152:155], v[220:223], a[204:207], v[152:155]\n"
"s_waitcnt lgkmcnt(2)\n"
"ds_read_b128 v[216:219], v230 offset:50176\n"
"v_cvt_pk_bf16_f32 v105, v158, v159\n"
"v_mfma_f32_16x16x32_bf16 a[232:235], v[124:127], a[244:247], a[232:235]\n"
"v_exp_f32 v164, v164\n"
"v_mfma_f32_16x16x32_bf16 v[24:27], v[128:131], a[244:247], v[24:27]\n"
"s_add_u32 m0, s44, 16384\n"
"ds_read_b128 v[220:223], v231 offset:50176\n"
"v_mfma_f32_16x16x32_bf16 v[24:27], v[132:135], a[248:251], v[24:27]\n"
"buffer_load_dwordx4 v240, s[16:19], s56 offen nt lds\n"
"v_mfma_f32_16x16x32_bf16 v[56:59], v[136:139], a[244:247], v[56:59]\n"
"v_exp_f32 v167, v167\n"
"v_mfma_f32_16x16x32_bf16 v[56:59], v[140:143], a[248:251], v[56:59]\n"
"v_exp_f32 v176, v176\n"
"v_mfma_f32_16x16x32_bf16 v[88:91], v[144:147], a[244:247], v[88:91]\n"
"v_exp_f32 v153, v153\n"
"v_mfma_f32_16x16x32_bf16 v[88:91], v[148:151], a[248:251], v[88:91]\n"
"v_exp_f32 v173, v173\n"
"v_mfma_f32_16x16x32_bf16 v[92:95], v[148:151], v[224:227], v[92:95]\n"
"ds_read_b64_tr_b16 a[250:251], v236 offset:36864\n"
"v_cvt_pk_bf16_f32 v121, v166, v167\n"
"v_mfma_f32_16x16x32_bf16 v[92:95], v[144:147], a[252:255], v[92:95]\n"
"ds_read_b64_tr_b16 a[244:245], v232 offset:32768\n"
"v_cvt_pk_bf16_f32 v112, v160, v161\n"
"v_mfma_f32_16x16x32_bf16 v[60:63], v[140:143], v[224:227], v[60:63]\n"
"ds_read_b64_tr_b16 a[248:249], v236 offset:32768\n"
"v_cvt_pk_bf16_f32 v120, v164, v165\n"
"v_mfma_f32_16x16x32_bf16 v[60:63], v[136:139], a[252:255], v[60:63]\n"
"ds_read_b64_tr_b16 a[246:247], v232 offset:36864\n"
"v_cvt_pk_bf16_f32 v104, v156, v157\n"
"v_mfma_f32_16x16x32_bf16 v[28:31], v[132:135], v[224:227], v[28:31]\n"
"ds_read_b64_tr_b16 v[226:227], v237 offset:36864\n"
"v_cvt_pk_bf16_f32 v113, v162, v163\n"
"v_mfma_f32_16x16x32_bf16 v[28:31], v[128:131], a[252:255], v[28:31]\n"
"ds_read_b64_tr_b16 v[224:225], v237 offset:32768\n"
"v_mfma_f32_16x16x32_bf16 a[236:239], v[124:127], a[252:255], a[236:239]\n"
"s_add_u32 s40, s56, s52\n"
"ds_read_b64_tr_b16 a[252:253], v233 offset:32768\n"
"v_smfmac_f32_16x16x64_bf16 v[198:201], a[240:243], v[136:143], v241\n"
"s_waitcnt lgkmcnt(7)\n"
"buffer_load_dwordx4 v240, s[16:19], s40 offen offset:1024 nt lds\n"
"v_mfma_f32_16x16x32_bf16 v[156:159], v[208:211], a[0:3], v[180:183]\n"
"v_exp_f32 v169, v169\n"
"v_mfma_f32_16x16x32_bf16 v[156:159], v[212:215], a[4:7], v[156:159]\n"
"v_exp_f32 v168, v168\n"
"v_mfma_f32_16x16x32_bf16 v[156:159], v[216:219], a[8:11], v[156:159]\n"
"v_exp_f32 v175, v175\n"
"v_mfma_f32_16x16x32_bf16 v[156:159], v[220:223], a[12:15], v[156:159]\n"
"v_exp_f32 v172, v172\n"
"v_mfma_f32_16x16x32_bf16 v[160:163], v[216:219], a[24:27], v[184:187]\n"
"ds_read_b64_tr_b16 a[254:255], v233 offset:36864\n"
"v_mfma_f32_16x16x32_bf16 v[160:163], v[220:223], a[28:31], v[160:163]\n"
"v_exp_f32 v155, v155\n"
"v_mfma_f32_16x16x32_bf16 v[160:163], v[208:211], a[16:19], v[160:163]\n"
"v_exp_f32 v177, v177\n"
"v_mfma_f32_16x16x32_bf16 v[160:163], v[212:215], a[20:23], v[160:163]\n"
"v_exp_f32 v179, v179\n"
"v_mfma_f32_16x16x32_bf16 v[164:167], v[208:211], a[32:35], v[188:191]\n"
"ds_read_b128 v[208:211], v230 offset:53248\n"
"v_cvt_pk_bf16_f32 v107, v174, v175\n"
"v_mfma_f32_16x16x32_bf16 v[164:167], v[212:215], a[36:39], v[164:167]\n"
"v_exp_f32 v171, v171\n"
"v_mfma_f32_16x16x32_bf16 v[164:167], v[216:219], a[40:43], v[164:167]\n"
"ds_read_b128 v[212:215], v231 offset:53248\n"
"v_cvt_pk_bf16_f32 v106, v172, v173\n"
"v_mfma_f32_16x16x32_bf16 v[164:167], v[220:223], a[44:47], v[164:167]\n"
"s_waitcnt lgkmcnt(3)\n"
"ds_read_b128 v[220:223], v231 offset:54272\n"
"v_cvt_pk_bf16_f32 v98, v168, v169\n"
"v_mfma_f32_16x16x32_bf16 a[208:211], v[124:127], a[244:247], a[208:211]\n"
"v_exp_f32 v154, v154\n"
"v_mfma_f32_16x16x32_bf16 v[0:3], v[128:131], a[244:247], v[0:3]\n"
"s_add_u32 s40, s56, s53\n"
"ds_read_b128 v[216:219], v230 offset:54272\n"
"v_mfma_f32_16x16x32_bf16 v[0:3], v[132:135], a[248:251], v[0:3]\n"
"buffer_load_dwordx4 v240, s[16:19], s40 offen offset:2048 nt lds\n"
"v_mfma_f32_16x16x32_bf16 v[32:35], v[136:139], a[244:247], v[32:35]\n"
"v_exp_f32 v160, v160\n"
"v_mfma_f32_16x16x32_bf16 v[32:35], v[140:143], a[248:251], v[32:35]\n"
"v_exp_f32 v178, v178\n"
"v_mfma_f32_16x16x32_bf16 v[64:67], v[144:147], a[244:247], v[64:67]\n"
"v_exp_f32 v152, v152\n"
"v_mfma_f32_16x16x32_bf16 v[64:67], v[148:151], a[248:251], v[64:67]\n"
"v_exp_f32 v161, v161\n"
"v_mfma_f32_16x16x32_bf16 v[68:71], v[148:151], v[224:227], v[68:71]\n"
"s_waitcnt lgkmcnt(1)\n"
"ds_read_b64_tr_b16 a[244:245], v232 offset:33792\n"
"v_cvt_pk_bf16_f32 v115, v178, v179\n"
"v_mfma_f32_16x16x32_bf16 v[68:71], v[144:147], a[252:255], v[68:71]\n"
"ds_read_b64_tr_b16 a[248:249], v236 offset:33792\n"
"v_cvt_pk_bf16_f32 v114, v176, v177\n"
"v_mfma_f32_16x16x32_bf16 v[36:39], v[140:143], v[224:227], v[36:39]\n"
"ds_read_b64_tr_b16 a[250:251], v236 offset:37888\n"
"v_cvt_pk_bf16_f32 v99, v170, v171\n"
"v_mfma_f32_16x16x32_bf16 v[36:39], v[136:139], a[252:255], v[36:39]\n"
"v_exp_f32 v163, v163\n"
"v_mfma_f32_16x16x32_bf16 v[4:7], v[132:135], v[224:227], v[4:7]\n"
"s_add_u32 s40, s56, s54\n"
"ds_read_b64_tr_b16 a[246:247], v232 offset:37888\n"
"v_mfma_f32_16x16x32_bf16 v[4:7], v[128:131], a[252:255], v[4:7]\n"
"buffer_load_dwordx4 v240, s[16:19], s40 offen offset:3072 nt lds\n"
"v_mfma_f32_16x16x32_bf16 a[212:215], v[124:127], a[252:255], a[212:215]\n"
"v_exp_f32 v167, v167\n"
"v_smfmac_f32_16x16x64_bf16 v[200:203], a[240:243], v[144:151], v241\n"
"v_cvt_pk_bf16_f32 v122, v152, v153\n"
"v_mfma_f32_16x16x32_bf16 v[168:171], v[208:211], a[0:3], v[180:183]\n"
"v_exp_f32 v159, v159\n"
"v_mfma_f32_16x16x32_bf16 v[168:171], v[212:215], a[4:7], v[168:171]\n"
"s_waitcnt lgkmcnt(2)\n"
"v_exp_f32 v165, v165\n"
"v_mfma_f32_16x16x32_bf16 v[168:171], v[216:219], a[8:11], v[168:171]\n"
"ds_read_b64_tr_b16 a[252:253], v233 offset:33792\n"
"ds_read_b64_tr_b16 v[224:225], v237 offset:33792\n"
"v_mfma_f32_16x16x32_bf16 v[168:171], v[220:223], a[12:15], v[168:171]\n"
"ds_read_b64_tr_b16 a[254:255], v233 offset:37888\n"
"ds_read_b64_tr_b16 v[226:227], v237 offset:37888\n"
"v_mfma_f32_16x16x32_bf16 v[172:175], v[216:219], a[24:27], v[184:187]\n"
"v_exp_f32 v166, v166\n"
"v_mfma_f32_16x16x32_bf16 v[172:175], v[220:223], a[28:31], v[172:175]\n"
"v_exp_f32 v164, v164\n"
"v_mfma_f32_16x16x32_bf16 v[172:175], v[208:211], a[16:19], v[172:175]\n"
"v_exp_f32 v157, v157\n"
"v_mfma_f32_16x16x32_bf16 v[172:175], v[212:215], a[20:23], v[172:175]\n"
"s_cbranch_vccnz LPCL3_%=\nLPKL3_%=:\n"
"v_cvt_pk_bf16_f32 v123, v154, v155\n"
"v_mfma_f32_16x16x32_bf16 v[176:179], v[208:211], a[32:35], v[188:191]\n"
"s_add_u32 s47, s47, 1\n"
"v_exp_f32 v158, v158\n"
"v_mfma_f32_16x16x32_bf16 v[176:179], v[212:215], a[36:39], v[176:179]\n"
"v_exp_f32 v162, v162\n"
"v_mfma_f32_16x16x32_bf16 v[176:179], v[216:219], a[40:43], v[176:179]\n"
"s_and_b32 s40, s47, 3\n"
"v_exp_f32 v156, v156\n"
"v_mfma_f32_16x16x32_bf16 v[176:179], v[220:223], a[44:47], v[176:179]\n"
"s_waitcnt lgkmcnt(0)\n"
"v_cvt_pk_bf16_f32 v101, v158, v159\n"
"s_cbranch_scc1 LAL_%=\n"
"s_cmp_eq_u32 s47, 8\n"
"s_cbranch_scc1 LTH1_%=\n"
"s_cmp_eq_u32 s47, 12\n"
"s_cbranch_scc1 LTH2_%=\n"
"s_cmp_eq_u32 s47, 16\n"
"s_cbranch_scc1 LAX_%=\n"
"LTH0_%=:\n"
"v_mfma_f32_16x16x32_bf16 a[224:227], v[124:127], a[244:247], a[224:227]\n"
"v_exp_f32 v170, v170\n"
"v_mfma_f32_16x16x32_bf16 v[16:19], v[128:131], a[244:247], v[16:19]\n"
"s_waitcnt vmcnt(8)\n"
"v_exp_f32 v168, v168\n"
"v_mfma_f32_16x16x32_bf16 v[16:19], v[132:135], a[248:251], v[16:19]\n"
"s_barrier\n"
"v_mfma_f32_16x16x32_bf16 v[48:51], v[136:139], a[244:247], v[48:51]\n"
"v_exp_f32 v173, v173\n"
"v_mfma_f32_16x16x32_bf16 v[48:51], v[140:143], a[248:251], v[48:51]\n"
"v_exp_f32 v171, v171\n"
"v_mfma_f32_16x16x32_bf16 v[80:83], v[144:147], a[244:247], v[80:83]\n"
"ds_read_b64_tr_b16 a[244:245], v234 offset:49152\n"
"ds_read_b64_tr_b16 a[246:247], v234 offset:53248\n"
"v_mfma_f32_16x16x32_bf16 v[80:83], v[148:151], a[248:251], v[80:83]\n"
"v_exp_f32 v179, v179\n"
"v_mfma_f32_16x16x32_bf16 v[84:87], v[148:151], v[224:227], v[84:87]\n"
"ds_read_b64_tr_b16 a[250:251], v238 offset:53248\n"
"v_cvt_pk_bf16_f32 v100, v156, v157\n"
"v_mfma_f32_16x16x32_bf16 v[84:87], v[144:147], a[252:255], v[84:87]\n"
"v_exp_f32 v169, v169\n"
"v_exp_f32 v172, v172\n"
"v_exp_f32 v175, v175\n"
"v_exp_f32 v176, v176\n"
"v_exp_f32 v178, v178\n"
"v_mfma_f32_16x16x32_bf16 v[52:55], v[140:143], v[224:227], v[52:55]\n"
"ds_read_b64_tr_b16 a[248:249], v238 offset:49152\n"
"v_cvt_pk_bf16_f32 v117, v166, v167\n"
"v_mfma_f32_16x16x32_bf16 v[52:55], v[136:139], a[252:255], v[52:55]\n"
"v_exp_f32 v174, v174\n"
"v_mfma_f32_16x16x32_bf16 v[20:23], v[132:135], v[224:227], v[20:23]\n"
"ds_read_b64_tr_b16 v[224:225], v239 offset:49152\n"
"ds_read_b64_tr_b16 v[226:227], v239 offset:53248\n"
"v_mfma_f32_16x16x32_bf16 v[20:23], v[128:131], a[252:255], v[20:23]\n"
"v_cvt_pk_bf16_f32 v102, v168, v169\n"
"v_cvt_pk_bf16_f32 v103, v170, v171\n"
"v_cvt_pk_bf16_f32 v111, v174, v175\n"
"v_exp_f32 v177, v177\n"
"v_mfma_f32_16x16x32_bf16 a[228:231], v[124:127], a[252:255], a[228:231]\n"
"s_waitcnt lgkmcnt(4)\n"
"ds_read_b64_tr_b16 a[254:255], v235 offset:53248\n"
"v_cvt_pk_bf16_f32 v110, v172, v173\n"
"v_mfma_f32_16x16x32_bf16 a[216:219], v[120:123], a[244:247], a[216:219]\n"
"s_add_u32 m0, s44, 81920\n"
"ds_read_b64_tr_b16 a[252:253], v235 offset:49152\n"
"v_cvt_pk_bf16_f32 v116, v164, v165\n"
"v_smfmac_f32_16x16x64_bf16 v[196:199], a[240:243], v[96:103], v241\n"
"buffer_load_dwordx4 v240, s[20:23], s56 offen nt lds\n"
"v_mfma_f32_16x16x32_bf16 v[8:11], v[96:99], a[244:247], v[8:11]\n"
"s_waitcnt lgkmcnt(4)\n"
"v_cvt_pk_bf16_f32 v118, v176, v177\n"
"v_cvt_pk_bf16_f32 v119, v178, v179\n"
"v_mfma_f32_16x16x32_bf16 v[8:11], v[100:103], a[248:251], v[8:11]\n"
"v_cvt_pk_bf16_f32 v108, v160, v161\n"
"v_and_b32 v245, 0x3f0, v242\n"
"v_smfmac_f32_16x16x64_bf16 v[200:203], a[240:243], v[112:119], v241\n"
"ds_read_b128 v[212:215], v229 offset:0\n"
"v_cvt_pk_bf16_f32 v109, v162, v163\n"
"v_mfma_f32_16x16x32_bf16 v[40:43], v[104:107], a[244:247], v[40:43]\n"
"v_lshlrev_b32_e64 v244, 24, s43\n"
"v_lshrrev_b32 v245, 1, v245\n"
"v_mfma_f32_16x16x32_bf16 v[40:43], v[108:111], a[248:251], v[40:43]\n"
"ds_read_b128 v[208:211], v228 offset:0\n"
"v_sub_u32 v245, v242, v245\n"
"v_smfmac_f32_16x16x64_bf16 v[198:201], a[240:243], v[104:111], v241\n"
"ds_read_b128 v[220:223], v229 offset:1024\n"
"v_add_f32 v196, v196, v197\n"
"v_mfma_f32_16x16x32_bf16 v[72:75], v[112:115], a[244:247], v[72:75]\n"
"ds_read_b64_tr_b16 a[244:245], v234 offset:50176\n"
"v_mul_f32 v244, 0x45800000, v244\n"
"v_mfma_f32_16x16x32_bf16 v[72:75], v[116:119], a[248:251], v[72:75]\n"
"ds_read_b64_tr_b16 a[246:247], v234 offset:54272\n"
"ds_read_b64_tr_b16 a[250:251], v238 offset:54272\n"
"v_mfma_f32_16x16x32_bf16 v[202:205], a[240:243], v[120:123], v[202:205]\n"
"s_waitcnt lgkmcnt(6)\n"
"ds_read_b128 v[216:219], v228 offset:1024\n"
"v_add_f32 v198, v198, v199\n"
"v_mfma_f32_16x16x32_bf16 v[76:79], v[116:119], v[224:227], v[76:79]\n"
"ds_read_b64_tr_b16 a[248:249], v238 offset:50176\n"
"v_add_f32 v254, v196, v198\n"
"v_mfma_f32_16x16x32_bf16 v[76:79], v[112:115], a[252:255], v[76:79]\n"
"v_add_f32 v200, v200, v201\n"
"v_add_f32 v254, v254, v200\n"
"v_mfma_f32_16x16x32_bf16 v[44:47], v[108:111], v[224:227], v[44:47]\n"
"v_add_f32 v244, v244, v202\n"
"v_add_f32 v254, v254, v202\n"
"v_mfma_f32_16x16x32_bf16 v[44:47], v[104:107], a[252:255], v[44:47]\n"
"v_min3_f32 v244, v244, v196, v198\n"
"v_min_f32 v244, v244, v200\n"
"v_mfma_f32_16x16x32_bf16 v[12:15], v[100:103], v[224:227], v[12:15]\n"
"s_add_u32 s40, s56, s52\n"
"v_rcp_f32 v244, v244\n"
"v_mfma_f32_16x16x32_bf16 v[12:15], v[96:99], a[252:255], v[12:15]\n"
"buffer_load_dwordx4 v240, s[20:23], s40 offen offset:1024 nt lds\n"
"v_mfma_f32_16x16x32_bf16 a[220:223], v[120:123], a[252:255], a[220:223]\n"
"s_waitcnt lgkmcnt(2)\n"
"v_fmac_f32 v254, 0x68800000, v244\n"
"v_cmp_nge_f32 vcc, s45, v254\n"
"v_xor_b32 v244, 0x11000, v242\n"
"s_cbranch_vccnz LSLOW_%=\nLBACK0_%=:\ns_mov_b64 vcc, s[64:65]\n"
"v_mfma_f32_16x16x32_bf16 a[232:235], v[120:123], a[244:247], a[232:235]\n"
"v_accvgpr_mov_b32 a6, a54\n"
"v_accvgpr_mov_b32 a8, a56\n"
"v_accvgpr_mov_b32 a9, a57\n"
"v_accvgpr_mov_b32 a14, a62\n"
"v_accvgpr_mov_b32 a20, a68\n"
"v_accvgpr_mov_b32 a42, a90\n"
"v_accvgpr_mov_b32 a44, a92\n"
"v_xor_b32 v148, 0x400, v243\n"
"v_bfe_u32 v149, v242, 8, 2\n"
"v_lshl_add_u32 v149, v149, 4, s43\n"
"v_mfma_f32_16x16x32_bf16 v[24:27], v[96:99], a[244:247], v[24:27]\n"
"s_waitcnt lgkmcnt(0)\n"
"ds_read_b128 v[124:127], v149 offset:0\n"
"v_rcp_f32 v206, v200\n"
"v_accvgpr_mov_b32 a4, a52\n"
"v_accvgpr_mov_b32 a12, a60\n"
"v_accvgpr_mov_b32 a13, a61\n"
"v_accvgpr_mov_b32 a16, a64\n"
"v_accvgpr_mov_b32 a39, a87\n"
"v_accvgpr_mov_b32 a47, a95\n"
"v_xor_b32 v150, 64, v149\n"
"v_mfma_f32_16x16x32_bf16 v[24:27], v[100:103], a[248:251], v[24:27]\n"
"ds_read_b128 v[128:131], v150 offset:0\n"
"v_accvgpr_mov_b32 a0, a48\n"
"v_accvgpr_mov_b32 a1, a49\n"
"v_accvgpr_mov_b32 a5, a53\n"
"v_accvgpr_mov_b32 a10, a58\n"
"v_accvgpr_mov_b32 a17, a65\n"
"v_accvgpr_mov_b32 a40, a88\n"
"v_mfma_f32_16x16x32_bf16 v[56:59], v[104:107], a[244:247], v[56:59]\n"
"ds_read_b128 v[132:135], v149 offset:1024\n"
"v_rcp_f32 v246, v196\n"
"v_mov_b32_dpp v206, v206 row_ror:12 row_mask:0x2 bank_mask:0xf\n"
"v_accvgpr_mov_b32 a2, a50\n"
"v_accvgpr_mov_b32 a22, a70\n"
"v_accvgpr_mov_b32 a23, a71\n"
"v_accvgpr_mov_b32 a36, a84\n"
"v_accvgpr_mov_b32 a38, a86\n"
"v_accvgpr_mov_b32 a45, a93\n"
"v_mfma_f32_16x16x32_bf16 v[56:59], v[108:111], a[248:251], v[56:59]\n"
"ds_read_b128 v[136:139], v150 offset:1024\n"
"v_accvgpr_mov_b32 a33, a81\n"
"v_mfma_f32_16x16x32_bf16 v[88:91], v[112:115], a[244:247], v[88:91]\n"
"ds_read_b64_tr_b16 v[224:225], v239 offset:50176\n"
"v_accvgpr_mov_b32 a3, a51\n"
"v_accvgpr_mov_b32 a7, a55\n"
"v_accvgpr_mov_b32 a11, a59\n"
"v_accvgpr_mov_b32 a19, a67\n"
"v_accvgpr_mov_b32 a21, a69\n"
"v_accvgpr_mov_b32 a24, a72\n"
"v_accvgpr_mov_b32 a27, a75\n"
"v_accvgpr_mov_b32 a30, a78\n"
"v_accvgpr_mov_b32 a46, a94\n"
"v_mfma_f32_16x16x32_bf16 v[88:91], v[116:119], a[248:251], v[88:91]\n"
"ds_read_b128 a[204:207], v148 offset:6144\n"
"v_mov_b32_dpp v206, v206 row_ror:8 row_mask:0x4 bank_mask:0xf\n"
"v_accvgpr_mov_b32 a15, a63\n"
"v_accvgpr_mov_b32 a25, a73\n"
"v_accvgpr_mov_b32 a26, a74\n"
"v_accvgpr_mov_b32 a29, a77\n"
"v_accvgpr_mov_b32 a31, a79\n"
"v_accvgpr_mov_b32 a35, a83\n"
"v_accvgpr_mov_b32 a43, a91\n"
"s_waitcnt lgkmcnt(5)\n"
"v_mfma_f32_16x16x32_bf16 v[180:183], v[124:127], a[0:3], 0\n"
"v_mov_b32 v252, v192\n"
"s_waitcnt lgkmcnt(4)\n"
"v_mfma_f32_16x16x32_bf16 v[180:183], v[128:131], a[4:7], v[180:183]\n"
"s_waitcnt lgkmcnt(3)\n"
"ds_read_b128 a[192:195], v243 offset:4096\n"
"v_accvgpr_mov_b32 a18, a66\n"
"v_mfma_f32_16x16x32_bf16 v[180:183], v[132:135], a[8:11], v[180:183]\n"
"ds_read_b128 a[200:203], v243 offset:6144\n"
"s_waitcnt lgkmcnt(4)\n"
"v_mfma_f32_16x16x32_bf16 v[180:183], v[136:139], a[12:15], v[180:183]\n"
"v_rcp_f32 v247, v198\n"
"v_accvgpr_mov_b32 a28, a76\n"
"v_accvgpr_mov_b32 a34, a82\n"
"v_accvgpr_mov_b32 a41, a89\n"
"v_mfma_f32_16x16x32_bf16 v[184:187], v[124:127], a[16:19], 0\n"
"ds_read_b128 a[196:199], v148 offset:4096\n"
"v_mfma_f32_16x16x32_bf16 v[184:187], v[128:131], a[20:23], v[184:187]\n"
"v_mov_b32_dpp v247, v247 row_ror:12 row_mask:0x2 bank_mask:0xf\n"
"v_mfma_f32_16x16x32_bf16 v[184:187], v[132:135], a[24:27], v[184:187]\n"
"s_add_u32 s40, s56, s53\n"
"v_accvgpr_mov_b32 a32, a80\n"
"v_mfma_f32_16x16x32_bf16 v[184:187], v[136:139], a[28:31], v[184:187]\n"
"buffer_load_dwordx4 v240, s[20:23], s40 offen offset:2048 nt lds\n"
"v_mov_b32_dpp v206, v206 row_ror:4 row_mask:0x8 bank_mask:0xf\n"
"v_accvgpr_mov_b32 a37, a85\n"
"v_sub_f32 v180, s74, v180\n"
"v_sub_f32 v183, s74, v183\n"
"v_mfma_f32_16x16x32_bf16 v[188:191], v[124:127], a[32:35], 0\n"
"v_mov_b32_dpp v246, v246 row_ror:12 row_mask:0x2 bank_mask:0xf\n"
"v_mfma_f32_16x16x32_bf16 v[188:191], v[128:131], a[36:39], v[188:191]\n"
"v_mfma_f32_16x16x32_bf16 v[188:191], v[132:135], a[40:43], v[188:191]\n"
"v_mov_b32_dpp v246, v246 row_ror:8 row_mask:0x4 bank_mask:0xf\n"
"v_sub_f32 v182, s74, v182\n"
"v_mfma_f32_16x16x32_bf16 v[188:191], v[136:139], a[44:47], v[188:191]\n"
"ds_write_b128 v244, a[220:223] offset:33792\n"
"v_mov_b32_dpp v247, v247 row_ror:8 row_mask:0x4 bank_mask:0xf\n"
"s_waitcnt lgkmcnt(2)\n"
"v_mfma_f32_16x16x32_bf16 v[192:195], v[124:127], a[192:195], 0\n"
"v_sub_f32 v181, s74, v181\n"
"s_waitcnt lgkmcnt(1)\n"
"v_mfma_f32_16x16x32_bf16 v[192:195], v[128:131], a[196:199], v[192:195]\n"
"v_mfma_f32_16x16x32_bf16 v[192:195], v[132:135], a[200:203], v[192:195]\n"
"ds_read_b64_tr_b16 v[226:227], v239 offset:54272\n"
"ds_read_b64_tr_b16 a[246:247], v232 offset:53248\n"
"v_mfma_f32_16x16x32_bf16 v[192:195], v[136:139], a[204:207], v[192:195]\n"
"v_mov_b32_dpp v246, v246 row_ror:4 row_mask:0x8 bank_mask:0xf\n"
"v_mov_b32_dpp v247, v247 row_ror:4 row_mask:0x8 bank_mask:0xf\n"
"v_sub_f32 v184, s74, v184\n"
"v_sub_f32 v187, s74, v187\n"
"v_sub_f32 v190, s74, v190\n"
"v_mfma_f32_16x16x32_bf16 v[152:155], v[208:211], a[0:3], v[180:183]\n"
"v_mov_b32 v253, v202\n"
"v_sub_f32 v185, s74, v185\n"
"v_mfma_f32_16x16x32_bf16 v[152:155], v[212:215], a[4:7], v[152:155]\n"
"v_sub_f32 v186, s74, v186\n"
"v_mfma_f32_16x16x32_bf16 v[152:155], v[216:219], a[8:11], v[152:155]\n"
"s_add_u32 s40, s56, s54\n"
"ds_read_b64_tr_b16 a[254:255], v235 offset:54272\n"
"v_sub_f32 v189, s74, v189\n"
"v_mfma_f32_16x16x32_bf16 v[152:155], v[220:223], a[12:15], v[152:155]\n"
"buffer_load_dwordx4 v240, s[20:23], s40 offen offset:3072 nt lds\n"
"v_mul_f32_dpp v9, v246, v9 row_newbcast:1 row_mask:0xf bank_mask:0xf\n"
"v_mul_f32_dpp v12, v246, v12 row_newbcast:0 row_mask:0xf bank_mask:0xf\n"
"v_sub_f32 v188, s74, v188\n"
"v_sub_f32 v191, s74, v191\n"
"v_mfma_f32_16x16x32_bf16 v[156:159], v[216:219], a[24:27], v[184:187]\n"
"s_add_u32 s55, s55, s59\n"
"v_mfma_f32_16x16x32_bf16 v[156:159], v[220:223], a[28:31], v[156:159]\n"
"s_add_u32 s41, s55, s57\n"
"v_mfma_f32_16x16x32_bf16 v[156:159], v[208:211], a[16:19], v[156:159]\n"
"s_and_b32 s41, s41, s58\n"
"v_sub_f32 v192, s74, v192\n"
"v_mfma_f32_16x16x32_bf16 v[156:159], v[212:215], a[20:23], v[156:159]\n"
"s_andn2_b32 s56, s55, s58\n"
"ds_read_b64_tr_b16 a[250:251], v236 offset:53248\n"
"v_xor_b32 v250, 0x1000, v245\n"
"v_mul_f32_dpp v41, v247, v41 row_newbcast:1 row_mask:0xf bank_mask:0xf\n"
"v_mul_f32_dpp v46, v247, v46 row_newbcast:2 row_mask:0xf bank_mask:0xf\n"
"v_sub_f32 v195, s74, v195\n"
"v_exp_f32 v153, v153\n"
"v_mfma_f32_16x16x32_bf16 v[160:163], v[208:211], a[32:35], v[188:191]\n"
"s_or_b32 s56, s56, s41\n"
"v_mul_f32_dpp v43, v247, v43 row_newbcast:3 row_mask:0xf bank_mask:0xf\n"
"v_sub_f32 v193, s74, v193\n"
"v_mfma_f32_16x16x32_bf16 v[160:163], v[212:215], a[36:39], v[160:163]\n"
"v_mfma_f32_16x16x32_bf16 v[160:163], v[216:219], a[40:43], v[160:163]\n"
"v_sub_f32 v194, s74, v194\n"
"v_mfma_f32_16x16x32_bf16 v[160:163], v[220:223], a[44:47], v[160:163]\n"
"ds_read_b64_tr_b16 a[252:253], v235 offset:50176\n"
"v_exp_f32 v152, v152\n"
"v_exp_f32 v157, v157\n"
"v_exp_f32 v158, v158\n"
"v_mfma_f32_16x16x32_bf16 v[164:167], v[216:219], a[200:203], v[192:195]\n"
"ds_read_b128 v[216:219], v228 offset:5120\n"
"v_mfma_f32_16x16x32_bf16 v[164:167], v[220:223], a[204:207], v[164:167]\n"
"v_mul_f32_dpp v57, v247, v57 row_newbcast:1 row_mask:0xf bank_mask:0xf\n"
"v_mfma_f32_16x16x32_bf16 v[164:167], v[208:211], a[192:195], v[164:167]\n"
"ds_read_b128 v[208:211], v228 offset:4096\n"
"v_mfma_f32_16x16x32_bf16 v[164:167], v[212:215], a[196:199], v[164:167]\n"
"ds_read_b128 v[220:223], v229 offset:5120\n"
"v_mul_f32_dpp v10, v246, v10 row_newbcast:2 row_mask:0xf bank_mask:0xf\n"
"v_exp_f32 v154, v154\n"
"v_exp_f32 v155, v155\n"
"v_mul_f32_dpp v27, v246, v27 row_newbcast:3 row_mask:0xf bank_mask:0xf\n"
"v_mul_f32_dpp v58, v247, v58 row_newbcast:2 row_mask:0xf bank_mask:0xf\n"
"s_branch LTC_%=\n"
"LTH1_%=:\n"
"v_mfma_f32_16x16x32_bf16 a[224:227], v[124:127], a[244:247], a[224:227]\n"
"v_exp_f32 v170, v170\n"
"v_mfma_f32_16x16x32_bf16 v[16:19], v[128:131], a[244:247], v[16:19]\n"
"s_waitcnt vmcnt(8)\n"
"v_exp_f32 v168, v168\n"
"v_mfma_f32_16x16x32_bf16 v[16:19], v[132:135], a[248:251], v[16:19]\n"
"s_barrier\n"
"v_mfma_f32_16x16x32_bf16 v[48:51], v[136:139], a[244:247], v[48:51]\n"
"v_exp_f32 v173, v173\n"
"v_mfma_f32_16x16x32_bf16 v[48:51], v[140:143], a[248:251], v[48:51]\n"
"v_exp_f32 v171, v171\n"
"v_mfma_f32_16x16x32_bf16 v[80:83], v[144:147], a[244:247], v[80:83]\n"
"ds_read_b64_tr_b16 a[244:245], v234 offset:49152\n"
"ds_read_b64_tr_b16 a[246:247], v234 offset:53248\n"
"v_mfma_f32_16x16x32_bf16 v[80:83], v[148:151], a[248:251], v[80:83]\n"
"v_exp_f32 v179, v179\n"
"v_mfma_f32_16x16x32_bf16 v[84:87], v[148:151], v[224:227], v[84:87]\n"
"ds_read_b64_tr_b16 a[250:251], v238 offset:53248\n"
"v_cvt_pk_bf16_f32 v100, v156, v157\n"
"v_mfma_f32_16x16x32_bf16 v[84:87], v[144:147], a[252:255], v[84:87]\n"
"v_exp_f32 v169, v169\n"
"v_exp_f32 v172, v172\n"
"v_exp_f32 v175, v175\n"
"v_exp_f32 v176, v176\n"
"v_exp_f32 v178, v178\n"
"v_mfma_f32_16x16x32_bf16 v[52:55], v[140:143], v[224:227], v[52:55]\n"
"ds_read_b64_tr_b16 a[248:249], v238 offset:49152\n"
"v_cvt_pk_bf16_f32 v117, v166, v167\n"
"v_mfma_f32_16x16x32_bf16 v[52:55], v[136:139], a[252:255], v[52:55]\n"
"v_exp_f32 v174, v174\n"
"v_mfma_f32_16x16x32_bf16 v[20:23], v[132:135], v[224:227], v[20:23]\n"
"ds_read_b64_tr_b16 v[224:225], v239 offset:49152\n"
"ds_read_b64_tr_b16 v[226:227], v239 offset:53248\n"
"v_mfma_f32_16x16x32_bf16 v[20:23], v[128:131], a[252:255], v[20:23]\n"
"v_cvt_pk_bf16_f32 v102, v168, v169\n"
"v_cvt_pk_bf16_f32 v103, v170, v171\n"
"v_cvt_pk_bf16_f32 v111, v174, v175\n"
"v_exp_f32 v177, v177\n"
"v_mfma_f32_16x16x32_bf16 a[228:231], v[124:127], a[252:255], a[228:231]\n"
"s_waitcnt lgkmcnt(4)\n"
"ds_read_b64_tr_b16 a[254:255], v235 offset:53248\n"
"v_cvt_pk_bf16_f32 v110, v172, v173\n"
"v_mfma_f32_16x16x32_bf16 a[216:219], v[120:123], a[244:247], a[216:219]\n"
"s_add_u32 m0, s44, 81920\n"
"ds_read_b64_tr_b16 a[252:253], v235 offset:49152\n"
"v_cvt_pk_bf16_f32 v116, v164, v165\n"
"v_smfmac_f32_16x16x64_bf16 v[196:199], a[240:243], v[96:103], v241\n"
"buffer_load_dwordx4 v240, s[20:23], s56 offen nt lds\n"
"v_mfma_f32_16x16x32_bf16 v[8:11], v[96:99], a[244:247], v[8:11]\n"
"s_waitcnt lgkmcnt(4)\n"
"v_cvt_pk_bf16_f32 v118, v176, v177\n"
"v_cvt_pk_bf16_f32 v119, v178, v179\n"
"v_mfma_f32_16x16x32_bf16 v[8:11], v[100:103], a[248:251], v[8:11]\n"
"v_cvt_pk_bf16_f32 v108, v160, v161\n"
"v_and_b32 v245, 0x3f0, v242\n"
"v_smfmac_f32_16x16x64_bf16 v[200:203], a[240:243], v[112:119], v241\n"
"ds_read_b128 v[212:215], v229 offset:0\n"
"v_cvt_pk_bf16_f32 v109, v162, v163\n"
"v_mfma_f32_16x16x32_bf16 v[40:43], v[104:107], a[244:247], v[40:43]\n"
"v_lshlrev_b32_e64 v244, 24, s43\n"
"v_lshrrev_b32 v245, 1, v245\n"
"v_mfma_f32_16x16x32_bf16 v[40:43], v[108:111], a[248:251], v[40:43]\n"
"ds_read_b128 v[208:211], v228 offset:0\n"
"v_sub_u32 v245, v242, v245\n"
"v_smfmac_f32_16x16x64_bf16 v[198:201], a[240:243], v[104:111], v241\n"
"ds_read_b128 v[220:223], v229 offset:1024\n"
"v_add_f32 v196, v196, v197\n"
"v_mfma_f32_16x16x32_bf16 v[72:75], v[112:115], a[244:247], v[72:75]\n"
"ds_read_b64_tr_b16 a[244:245], v234 offset:50176\n"
"v_mul_f32 v244, 0x45800000, v244\n"
"v_mfma_f32_16x16x32_bf16 v[72:75], v[116:119], a[248:251], v[72:75]\n"
"ds_read_b64_tr_b16 a[246:247], v234 offset:54272\n"
"ds_read_b64_tr_b16 a[250:251], v238 offset:54272\n"
"v_mfma_f32_16x16x32_bf16 v[202:205], a[240:243], v[120:123], v[202:205]\n"
"s_waitcnt lgkmcnt(6)\n"
"ds_read_b128 v[216:219], v228 offset:1024\n"
"v_add_f32 v198, v198, v199\n"
"v_mfma_f32_16x16x32_bf16 v[76:79], v[116:119], v[224:227], v[76:79]\n"
"ds_read_b64_tr_b16 a[248:249], v238 offset:50176\n"
"v_add_f32 v254, v196, v198\n"
"v_mfma_f32_16x16x32_bf16 v[76:79], v[112:115], a[252:255], v[76:79]\n"
"v_add_f32 v200, v200, v201\n"
"v_add_f32 v254, v254, v200\n"
"v_mfma_f32_16x16x32_bf16 v[44:47], v[108:111], v[224:227], v[44:47]\n"
"v_add_f32 v244, v244, v202\n"
"v_add_f32 v254, v254, v202\n"
"v_mfma_f32_16x16x32_bf16 v[44:47], v[104:107], a[252:255], v[44:47]\n"
"v_min3_f32 v244, v244, v196, v198\n"
"v_min_f32 v244, v244, v200\n"
"v_mfma_f32_16x16x32_bf16 v[12:15], v[100:103], v[224:227], v[12:15]\n"
"s_add_u32 s40, s56, s52\n"
"v_rcp_f32 v244, v244\n"
"v_mfma_f32_16x16x32_bf16 v[12:15], v[96:99], a[252:255], v[12:15]\n"
"buffer_load_dwordx4 v240, s[20:23], s40 offen offset:1024 nt lds\n"
"v_mfma_f32_16x16x32_bf16 a[220:223], v[120:123], a[252:255], a[220:223]\n"
"s_waitcnt lgkmcnt(2)\n"
"v_fmac_f32 v254, 0x68800000, v244\n"
"v_cmp_nge_f32 vcc, s45, v254\n"
"v_xor_b32 v244, 0x11000, v242\n"
"s_cbranch_vccnz LSLOW_%=\nLBACK1_%=:\ns_mov_b64 vcc, s[64:65]\n"
"v_mfma_f32_16x16x32_bf16 a[232:235], v[120:123], a[244:247], a[232:235]\n"
"v_accvgpr_mov_b32 a6, a102\n"
"v_accvgpr_mov_b32 a8, a104\n"
"v_accvgpr_mov_b32 a9, a105\n"
"v_accvgpr_mov_b32 a14, a110\n"
"v_accvgpr_mov_b32 a20, a116\n"
"v_accvgpr_mov_b32 a42, a138\n"
"v_accvgpr_mov_b32 a44, a140\n"
"v_xor_b32 v148, 0x400, v243\n"
"v_bfe_u32 v149, v242, 8, 2\n"
"v_lshl_add_u32 v149, v149, 4, s43\n"
"v_mfma_f32_16x16x32_bf16 v[24:27], v[96:99], a[244:247], v[24:27]\n"
"s_waitcnt lgkmcnt(0)\n"
"ds_read_b128 v[124:127], v149 offset:0\n"
"v_rcp_f32 v206, v200\n"
"v_accvgpr_mov_b32 a4, a100\n"
"v_accvgpr_mov_b32 a12, a108\n"
"v_accvgpr_mov_b32 a13, a109\n"
"v_accvgpr_mov_b32 a16, a112\n"
"v_accvgpr_mov_b32 a39, a135\n"
"v_accvgpr_mov_b32 a47, a143\n"
"v_xor_b32 v150, 64, v149\n"
"v_mfma_f32_16x16x32_bf16 v[24:27], v[100:103], a[248:251], v[24:27]\n"
"ds_read_b128 v[128:131], v150 offset:0\n"
"v_accvgpr_mov_b32 a0, a96\n"
"v_accvgpr_mov_b32 a1, a97\n"
"v_accvgpr_mov_b32 a5, a101\n"
"v_accvgpr_mov_b32 a10, a106\n"
"v_accvgpr_mov_b32 a17, a113\n"
"v_accvgpr_mov_b32 a40, a136\n"
"v_mfma_f32_16x16x32_bf16 v[56:59], v[104:107], a[244:247], v[56:59]\n"
"ds_read_b128 v[132:135], v149 offset:1024\n"
"v_rcp_f32 v246, v196\n"
"v_mov_b32_dpp v206, v206 row_ror:12 row_mask:0x2 bank_mask:0xf\n"
"v_accvgpr_mov_b32 a2, a98\n"
"v_accvgpr_mov_b32 a22, a118\n"
"v_accvgpr_mov_b32 a23, a119\n"
"v_accvgpr_mov_b32 a36, a132\n"
"v_accvgpr_mov_b32 a38, a134\n"
"v_accvgpr_mov_b32 a45, a141\n"
"v_mfma_f32_16x16x32_bf16 v[56:59], v[108:111], a[248:251], v[56:59]\n"
"ds_read_b128 v[136:139], v150 offset:1024\n"
"v_accvgpr_mov_b32 a33, a129\n"
"v_mfma_f32_16x16x32_bf16 v[88:91], v[112:115], a[244:247], v[88:91]\n"
"ds_read_b64_tr_b16 v[224:225], v239 offset:50176\n"
"v_accvgpr_mov_b32 a3, a99\n"
"v_accvgpr_mov_b32 a7, a103\n"
"v_accvgpr_mov_b32 a11, a107\n"
"v_accvgpr_mov_b32 a19, a115\n"
"v_accvgpr_mov_b32 a21, a117\n"
"v_accvgpr_mov_b32 a24, a120\n"
"v_accvgpr_mov_b32 a27, a123\n"
"v_accvgpr_mov_b32 a30, a126\n"
"v_accvgpr_mov_b32 a46, a142\n"
"v_mfma_f32_16x16x32_bf16 v[88:91], v[116:119], a[248:251], v[88:91]\n"
"s_waitcnt lgkmcnt(4)\n"
"ds_read_b128 a[204:207], v148 offset:6144\n"
"v_mov_b32_dpp v206, v206 row_ror:8 row_mask:0x4 bank_mask:0xf\n"
"v_accvgpr_mov_b32 a15, a111\n"
"v_accvgpr_mov_b32 a25, a121\n"
"v_accvgpr_mov_b32 a26, a122\n"
"v_accvgpr_mov_b32 a29, a125\n"
"v_accvgpr_mov_b32 a31, a127\n"
"v_accvgpr_mov_b32 a35, a131\n"
"v_accvgpr_mov_b32 a43, a139\n"
"v_accvgpr_mov_b32 a112, a64\n"
"v_mfma_f32_16x16x32_bf16 v[180:183], v[124:127], a[0:3], 0\n"
"s_waitcnt lgkmcnt(4)\n"
"v_mov_b32 v252, v192\n"
"v_accvgpr_mov_b32 a126, a78\n"
"v_mfma_f32_16x16x32_bf16 v[180:183], v[128:131], a[4:7], v[180:183]\n"
"s_waitcnt lgkmcnt(3)\n"
"ds_read_b128 a[192:195], v243 offset:4096\n"
"v_accvgpr_mov_b32 a18, a114\n"
"v_mfma_f32_16x16x32_bf16 v[180:183], v[132:135], a[8:11], v[180:183]\n"
"s_waitcnt lgkmcnt(3)\n"
"ds_read_b128 a[200:203], v243 offset:6144\n"
"v_accvgpr_mov_b32 a135, a87\n"
"v_mfma_f32_16x16x32_bf16 v[180:183], v[136:139], a[12:15], v[180:183]\n"
"v_rcp_f32 v247, v198\n"
"v_accvgpr_mov_b32 a28, a124\n"
"v_accvgpr_mov_b32 a34, a130\n"
"v_accvgpr_mov_b32 a41, a137\n"
"v_accvgpr_mov_b32 a103, a55\n"
"v_accvgpr_mov_b32 a119, a71\n"
"v_accvgpr_mov_b32 a108, a60\n"
"v_accvgpr_mov_b32 a130, a82\n"
"v_mfma_f32_16x16x32_bf16 v[184:187], v[124:127], a[16:19], 0\n"
"ds_read_b128 a[196:199], v148 offset:4096\n"
"v_accvgpr_mov_b32 a110, a62\n"
"v_mfma_f32_16x16x32_bf16 v[184:187], v[128:131], a[20:23], v[184:187]\n"
"v_mov_b32_dpp v247, v247 row_ror:12 row_mask:0x2 bank_mask:0xf\n"
"v_accvgpr_mov_b32 a104, a56\n"
"v_mfma_f32_16x16x32_bf16 v[184:187], v[132:135], a[24:27], v[184:187]\n"
"s_add_u32 s40, s56, s53\n"
"v_accvgpr_mov_b32 a32, a128\n"
"v_accvgpr_mov_b32 a109, a61\n"
"v_mfma_f32_16x16x32_bf16 v[184:187], v[136:139], a[28:31], v[184:187]\n"
"buffer_load_dwordx4 v240, s[20:23], s40 offen offset:2048 nt lds\n"
"v_mov_b32_dpp v206, v206 row_ror:4 row_mask:0x8 bank_mask:0xf\n"
"v_accvgpr_mov_b32 a37, a133\n"
"v_accvgpr_mov_b32 a101, a53\n"
"v_accvgpr_mov_b32 a102, a54\n"
"v_accvgpr_mov_b32 a133, a85\n"
"v_accvgpr_mov_b32 a105, a57\n"
"v_sub_f32 v180, s74, v180\n"
"v_sub_f32 v183, s74, v183\n"
"v_mfma_f32_16x16x32_bf16 v[188:191], v[124:127], a[32:35], 0\n"
"v_mov_b32_dpp v246, v246 row_ror:12 row_mask:0x2 bank_mask:0xf\n"
"v_accvgpr_mov_b32 a132, a84\n"
"v_mfma_f32_16x16x32_bf16 v[188:191], v[128:131], a[36:39], v[188:191]\n"
"v_accvgpr_mov_b32 a114, a66\n"
"v_accvgpr_mov_b32 a131, a83\n"
"v_mfma_f32_16x16x32_bf16 v[188:191], v[132:135], a[40:43], v[188:191]\n"
"v_mov_b32_dpp v246, v246 row_ror:8 row_mask:0x4 bank_mask:0xf\n"
"v_sub_f32 v182, s74, v182\n"
"v_mfma_f32_16x16x32_bf16 v[188:191], v[136:139], a[44:47], v[188:191]\n"
"s_waitcnt lgkmcnt(1)\n"
"ds_write_b128 v244, a[220:223] offset:33792\n"
"v_mov_b32_dpp v247, v247 row_ror:8 row_mask:0x4 bank_mask:0xf\n"
"v_accvgpr_mov_b32 a115, a67\n"
"v_accvgpr_mov_b32 a129, a81\n"
"v_accvgpr_mov_b32 a106, a58\n"
"v_mfma_f32_16x16x32_bf16 v[192:195], v[124:127], a[192:195], 0\n"
"s_waitcnt lgkmcnt(1)\n"
"v_accvgpr_mov_b32 a116, a68\n"
"v_sub_f32 v181, s74, v181\n"
"v_mfma_f32_16x16x32_bf16 v[192:195], v[128:131], a[196:199], v[192:195]\n"
"v_accvgpr_mov_b32 a125, a77\n"
"v_accvgpr_mov_b32 a96, a48\n"
"v_mfma_f32_16x16x32_bf16 v[192:195], v[132:135], a[200:203], v[192:195]\n"
"ds_read_b64_tr_b16 v[226:227], v239 offset:54272\n"
"ds_read_b64_tr_b16 a[246:247], v232 offset:53248\n"
"v_mfma_f32_16x16x32_bf16 v[192:195], v[136:139], a[204:207], v[192:195]\n"
"v_mov_b32_dpp v246, v246 row_ror:4 row_mask:0x8 bank_mask:0xf\n"
"v_mov_b32_dpp v247, v247 row_ror:4 row_mask:0x8 bank_mask:0xf\n"
"v_accvgpr_mov_b32 a100, a52\n"
"v_accvgpr_mov_b32 a117, a69\n"
"v_accvgpr_mov_b32 a127, a79\n"
"v_accvgpr_mov_b32 a128, a80\n"
"v_sub_f32 v184, s74, v184\n"
"v_sub_f32 v187, s74, v187\n"
"v_sub_f32 v190, s74, v190\n"
"v_mfma_f32_16x16x32_bf16 v[152:155], v[208:211], a[0:3], v[180:183]\n"
"v_mov_b32 v253, v202\n"
"v_sub_f32 v185, s74, v185\n"
"v_mfma_f32_16x16x32_bf16 v[152:155], v[212:215], a[4:7], v[152:155]\n"
"v_accvgpr_mov_b32 a138, a90\n"
"v_sub_f32 v186, s74, v186\n"
"v_mfma_f32_16x16x32_bf16 v[152:155], v[216:219], a[8:11], v[152:155]\n"
"s_add_u32 s40, s56, s54\n"
"ds_read_b64_tr_b16 a[254:255], v235 offset:54272\n"
"v_sub_f32 v189, s74, v189\n"
"v_mfma_f32_16x16x32_bf16 v[152:155], v[220:223], a[12:15], v[152:155]\n"
"buffer_load_dwordx4 v240, s[20:23], s40 offen offset:3072 nt lds\n"
"v_accvgpr_mov_b32 a142, a94\n"
"v_accvgpr_mov_b32 a143, a95\n"
"v_accvgpr_mov_b32 a120, a72\n"
"v_mul_f32_dpp v9, v246, v9 row_newbcast:1 row_mask:0xf bank_mask:0xf\n"
"v_mul_f32_dpp v12, v246, v12 row_newbcast:0 row_mask:0xf bank_mask:0xf\n"
"v_sub_f32 v188, s74, v188\n"
"v_sub_f32 v191, s74, v191\n"
"v_mfma_f32_16x16x32_bf16 v[156:159], v[216:219], a[24:27], v[184:187]\n"
"s_add_u32 s55, s55, s59\n"
"v_accvgpr_mov_b32 a111, a63\n"
"v_accvgpr_mov_b32 a107, a59\n"
"v_mfma_f32_16x16x32_bf16 v[156:159], v[220:223], a[28:31], v[156:159]\n"
"s_add_u32 s41, s55, s57\n"
"v_accvgpr_mov_b32 a97, a49\n"
"v_accvgpr_mov_b32 a122, a74\n"
"v_mfma_f32_16x16x32_bf16 v[156:159], v[208:211], a[16:19], v[156:159]\n"
"s_and_b32 s41, s41, s58\n"
"v_accvgpr_mov_b32 a99, a51\n"
"v_sub_f32 v192, s74, v192\n"
"v_mfma_f32_16x16x32_bf16 v[156:159], v[212:215], a[20:23], v[156:159]\n"
"s_andn2_b32 s56, s55, s58\n"
"ds_read_b64_tr_b16 a[250:251], v236 offset:53248\n"
"v_xor_b32 v250, 0x1000, v245\n"
"v_accvgpr_mov_b32 a98, a50\n"
"v_accvgpr_mov_b32 a113, a65\n"
"v_accvgpr_mov_b32 a139, a91\n"
"v_mul_f32_dpp v41, v247, v41 row_newbcast:1 row_mask:0xf bank_mask:0xf\n"
"v_mul_f32_dpp v46, v247, v46 row_newbcast:2 row_mask:0xf bank_mask:0xf\n"
"v_sub_f32 v195, s74, v195\n"
"v_exp_f32 v153, v153\n"
"v_mfma_f32_16x16x32_bf16 v[160:163], v[208:211], a[32:35], v[188:191]\n"
"s_or_b32 s56, s56, s41\n"
"v_mul_f32_dpp v43, v247, v43 row_newbcast:3 row_mask:0xf bank_mask:0xf\n"
"v_sub_f32 v193, s74, v193\n"
"v_mfma_f32_16x16x32_bf16 v[160:163], v[212:215], a[36:39], v[160:163]\n"
"v_accvgpr_mov_b32 a123, a75\n"
"v_accvgpr_mov_b32 a137, a89\n"
"v_mfma_f32_16x16x32_bf16 v[160:163], v[216:219], a[40:43], v[160:163]\n"
"v_accvgpr_mov_b32 a134, a86\n"
"v_sub_f32 v194, s74, v194\n"
"v_mfma_f32_16x16x32_bf16 v[160:163], v[220:223], a[44:47], v[160:163]\n"
"ds_read_b64_tr_b16 a[252:253], v235 offset:50176\n"
"v_accvgpr_mov_b32 a124, a76\n"
"v_accvgpr_mov_b32 a136, a88\n"
"v_exp_f32 v152, v152\n"
"v_exp_f32 v157, v157\n"
"v_exp_f32 v158, v158\n"
"v_mfma_f32_16x16x32_bf16 v[164:167], v[216:219], a[200:203], v[192:195]\n"
"ds_read_b128 v[216:219], v228 offset:5120\n"
"v_accvgpr_mov_b32 a118, a70\n"
"v_mfma_f32_16x16x32_bf16 v[164:167], v[220:223], a[204:207], v[164:167]\n"
"v_accvgpr_mov_b32 a140, a92\n"
"v_mul_f32_dpp v57, v247, v57 row_newbcast:1 row_mask:0xf bank_mask:0xf\n"
"v_mfma_f32_16x16x32_bf16 v[164:167], v[208:211], a[192:195], v[164:167]\n"
"ds_read_b128 v[208:211], v228 offset:4096\n"
"v_accvgpr_mov_b32 a141, a93\n"
"v_mfma_f32_16x16x32_bf16 v[164:167], v[212:215], a[196:199], v[164:167]\n"
"s_waitcnt lgkmcnt(3)\n"
"ds_read_b128 v[220:223], v229 offset:5120\n"
"v_accvgpr_mov_b32 a121, a73\n"
"v_mul_f32_dpp v10, v246, v10 row_newbcast:2 row_mask:0xf bank_mask:0xf\n"
"v_exp_f32 v154, v154\n"
"v_exp_f32 v155, v155\n"
"v_mul_f32_dpp v27, v246, v27 row_newbcast:3 row_mask:0xf bank_mask:0xf\n"
"v_mul_f32_dpp v58, v247, v58 row_newbcast:2 row_mask:0xf bank_mask:0xf\n"
"s_branch LTC_%=\n"
"LTH2_%=:\n"
"v_mfma_f32_16x16x32_bf16 a[224:227], v[124:127], a[244:247], a[224:227]\n"
"v_exp_f32 v170, v170\n"
"v_mfma_f32_16x16x32_bf16 v[16:19], v[128:131], a[244:247], v[16:19]\n"
"s_waitcnt vmcnt(8)\n"
"v_exp_f32 v168, v168\n"
"v_mfma_f32_16x16x32_bf16 v[16:19], v[132:135], a[248:251], v[16:19]\n"
"s_barrier\n"
"v_mfma_f32_16x16x32_bf16 v[48:51], v[136:139], a[244:247], v[48:51]\n"
"v_exp_f32 v173, v173\n"
"v_mfma_f32_16x16x32_bf16 v[48:51], v[140:143], a[248:251], v[48:51]\n"
"v_exp_f32 v171, v171\n"
"v_mfma_f32_16x16x32_bf16 v[80:83], v[144:147], a[244:247], v[80:83]\n"
"ds_read_b64_tr_b16 a[244:245], v234 offset:49152\n"
"ds_read_b64_tr_b16 a[246:247], v234 offset:53248\n"
"v_mfma_f32_16x16x32_bf16 v[80:83], v[148:151], a[248:251], v[80:83]\n"
"v_exp_f32 v179, v179\n"
"v_mfma_f32_16x16x32_bf16 v[84:87], v[148:151], v[224:227], v[84:87]\n"
"ds_read_b64_tr_b16 a[250:251], v238 offset:53248\n"
"v_cvt_pk_bf16_f32 v100, v156, v157\n"
"v_mfma_f32_16x16x32_bf16 v[84:87], v[144:147], a[252:255], v[84:87]\n"
"v_exp_f32 v169, v169\n"
"v_exp_f32 v172, v172\n"
"v_exp_f32 v175, v175\n"
"v_exp_f32 v176, v176\n"
"v_exp_f32 v178, v178\n"
"v_mfma_f32_16x16x32_bf16 v[52:55], v[140:143], v[224:227], v[52:55]\n"
"ds_read_b64_tr_b16 a[248:249], v238 offset:49152\n"
"v_cvt_pk_bf16_f32 v117, v166, v167\n"
"v_mfma_f32_16x16x32_bf16 v[52:55], v[136:139], a[252:255], v[52:55]\n"
"v_exp_f32 v174, v174\n"
"v_mfma_f32_16x16x32_bf16 v[20:23], v[132:135], v[224:227], v[20:23]\n"
"ds_read_b64_tr_b16 v[224:225], v239 offset:49152\n"
"ds_read_b64_tr_b16 v[226:227], v239 offset:53248\n"
"v_mfma_f32_16x16x32_bf16 v[20:23], v[128:131], a[252:255], v[20:23]\n"
"v_cvt_pk_bf16_f32 v102, v168, v169\n"
"v_cvt_pk_bf16_f32 v103, v170, v171\n"
"v_cvt_pk_bf16_f32 v111, v174, v175\n"
"v_exp_f32 v177, v177\n"
"v_mfma_f32_16x16x32_bf16 a[228:231], v[124:127], a[252:255], a[228:231]\n"
"s_waitcnt lgkmcnt(4)\n"
"ds_read_b64_tr_b16 a[254:255], v235 offset:53248\n"
"v_cvt_pk_bf16_f32 v110, v172, v173\n"
"v_mfma_f32_16x16x32_bf16 a[216:219], v[120:123], a[244:247], a[216:219]\n"
"s_add_u32 m0, s44, 81920\n"
"ds_read_b64_tr_b16 a[252:253], v235 offset:49152\n"
"v_cvt_pk_bf16_f32 v116, v164, v165\n"
"v_smfmac_f32_16x16x64_bf16 v[196:199], a[240:243], v[96:103], v241\n"
"buffer_load_dwordx4 v240, s[20:23], s56 offen nt lds\n"
"v_mfma_f32_16x16x32_bf16 v[8:11], v[96:99], a[244:247], v[8:11]\n"
"s_waitcnt lgkmcnt(4)\n"
"v_cvt_pk_bf16_f32 v118, v176, v177\n"
"v_cvt_pk_bf16_f32 v119, v178, v179\n"
"v_mfma_f32_16x16x32_bf16 v[8:11], v[100:103], a[248:251], v[8:11]\n"
"v_cvt_pk_bf16_f32 v108, v160, v161\n"
"v_and_b32 v245, 0x3f0, v242\n"
"v_smfmac_f32_16x16x64_bf16 v[200:203], a[240:243], v[112:119], v241\n"
"ds_read_b128 v[212:215], v229 offset:0\n"
"v_cvt_pk_bf16_f32 v109, v162, v163\n"
"v_mfma_f32_16x16x32_bf16 v[40:43], v[104:107], a[244:247], v[40:43]\n"
"v_lshlrev_b32_e64 v244, 24, s43\n"
"v_lshrrev_b32 v245, 1, v245\n"
"v_mfma_f32_16x16x32_bf16 v[40:43], v[108:111], a[248:251], v[40:43]\n"
"ds_read_b128 v[208:211], v228 offset:0\n"
"v_sub_u32 v245, v242, v245\n"
"v_smfmac_f32_16x16x64_bf16 v[198:201], a[240:243], v[104:111], v241\n"
"ds_read_b128 v[220:223], v229 offset:1024\n"
"v_add_f32 v196, v196, v197\n"
"v_mfma_f32_16x16x32_bf16 v[72:75], v[112:115], a[244:247], v[72:75]\n"
"ds_read_b64_tr_b16 a[244:245], v234 offset:50176\n"
"v_mul_f32 v244, 0x45800000, v244\n"
"v_mfma_f32_16x16x32_bf16 v[72:75], v[116:119], a[248:251], v[72:75]\n"
"ds_read_b64_tr_b16 a[246:247], v234 offset:54272\n"
"ds_read_b64_tr_b16 a[250:251], v238 offset:54272\n"
"v_mfma_f32_16x16x32_bf16 v[202:205], a[240:243], v[120:123], v[202:205]\n"
"s_waitcnt lgkmcnt(6)\n"
"ds_read_b128 v[216:219], v228 offset:1024\n"
"v_add_f32 v198, v198, v199\n"
"v_mfma_f32_16x16x32_bf16 v[76:79], v[116:119], v[224:227], v[76:79]\n"
"ds_read_b64_tr_b16 a[248:249], v238 offset:50176\n"
"v_add_f32 v254, v196, v198\n"
"v_mfma_f32_16x16x32_bf16 v[76:79], v[112:115], a[252:255], v[76:79]\n"
"v_add_f32 v200, v200, v201\n"
"v_add_f32 v254, v254, v200\n"
"v_mfma_f32_16x16x32_bf16 v[44:47], v[108:111], v[224:227], v[44:47]\n"
"v_add_f32 v244, v244, v202\n"
"v_add_f32 v254, v254, v202\n"
"v_mfma_f32_16x16x32_bf16 v[44:47], v[104:107], a[252:255], v[44:47]\n"
"v_min3_f32 v244, v244, v196, v198\n"
"v_min_f32 v244, v244, v200\n"
"v_mfma_f32_16x16x32_bf16 v[12:15], v[100:103], v[224:227], v[12:15]\n"
"s_add_u32 s40, s56, s52\n"
"v_rcp_f32 v244, v244\n"
"v_mfma_f32_16x16x32_bf16 v[12:15], v[96:99], a[252:255], v[12:15]\n"
"buffer_load_dwordx4 v240, s[20:23], s40 offen offset:1024 nt lds\n"
"v_mfma_f32_16x16x32_bf16 a[220:223], v[120:123], a[252:255], a[220:223]\n"
"s_waitcnt lgkmcnt(2)\n"
"v_fmac_f32 v254, 0x68800000, v244\n"
"v_cmp_nge_f32 vcc, s45, v254\n"
"v_xor_b32 v244, 0x11000, v242\n"
"s_cbranch_vccnz LSLOW_%=\nLBACK2_%=:\ns_mov_b64 vcc, s[64:65]\n"
"v_mfma_f32_16x16x32_bf16 a[232:235], v[120:123], a[244:247], a[232:235]\n"
"v_accvgpr_mov_b32 a6, a150\n"
"v_accvgpr_mov_b32 a8, a152\n"
"v_accvgpr_mov_b32 a9, a153\n"
"v_accvgpr_mov_b32 a14, a158\n"
"v_accvgpr_mov_b32 a20, a164\n"
"v_accvgpr_mov_b32 a42, a186\n"
"v_accvgpr_mov_b32 a44, a188\n"
"v_xor_b32 v148, 0x400, v243\n"
"v_bfe_u32 v149, v242, 8, 2\n"
"v_lshl_add_u32 v149, v149, 4, s43\n"
"v_mfma_f32_16x16x32_bf16 v[24:27], v[96:99], a[244:247], v[24:27]\n"
"s_waitcnt lgkmcnt(0)\n"
"ds_read_b128 v[124:127], v149 offset:0\n"
"v_rcp_f32 v206, v200\n"
"v_accvgpr_mov_b32 a4, a148\n"
"v_accvgpr_mov_b32 a12, a156\n"
"v_accvgpr_mov_b32 a13, a157\n"
"v_accvgpr_mov_b32 a16, a160\n"
"v_accvgpr_mov_b32 a39, a183\n"
"v_accvgpr_mov_b32 a47, a191\n"
"v_xor_b32 v150, 64, v149\n"
"v_mfma_f32_16x16x32_bf16 v[24:27], v[100:103], a[248:251], v[24:27]\n"
"ds_read_b128 v[128:131], v150 offset:0\n"
"v_accvgpr_mov_b32 a0, a144\n"
"v_accvgpr_mov_b32 a1, a145\n"
"v_accvgpr_mov_b32 a5, a149\n"
"v_accvgpr_mov_b32 a10, a154\n"
"v_accvgpr_mov_b32 a17, a161\n"
"v_accvgpr_mov_b32 a40, a184\n"
"v_mfma_f32_16x16x32_bf16 v[56:59], v[104:107], a[244:247], v[56:59]\n"
"ds_read_b128 v[132:135], v149 offset:1024\n"
"v_rcp_f32 v246, v196\n"
"v_mov_b32_dpp v206, v206 row_ror:12 row_mask:0x2 bank_mask:0xf\n"
"v_accvgpr_mov_b32 a2, a146\n"
"v_accvgpr_mov_b32 a22, a166\n"
"v_accvgpr_mov_b32 a23, a167\n"
"v_accvgpr_mov_b32 a36, a180\n"
"v_accvgpr_mov_b32 a38, a182\n"
"v_accvgpr_mov_b32 a45, a189\n"
"v_mfma_f32_16x16x32_bf16 v[56:59], v[108:111], a[248:251], v[56:59]\n"
"ds_read_b128 v[136:139], v150 offset:1024\n"
"v_accvgpr_mov_b32 a33, a177\n"
"v_mfma_f32_16x16x32_bf16 v[88:91], v[112:115], a[244:247], v[88:91]\n"
"ds_read_b64_tr_b16 v[224:225], v239 offset:50176\n"
"v_accvgpr_mov_b32 a3, a147\n"
"v_accvgpr_mov_b32 a7, a151\n"
"v_accvgpr_mov_b32 a11, a155\n"
"v_accvgpr_mov_b32 a19, a163\n"
"v_accvgpr_mov_b32 a21, a165\n"
"v_accvgpr_mov_b32 a24, a168\n"
"v_accvgpr_mov_b32 a27, a171\n"
"v_accvgpr_mov_b32 a30, a174\n"
"v_accvgpr_mov_b32 a46, a190\n"
"v_mfma_f32_16x16x32_bf16 v[88:91], v[116:119], a[248:251], v[88:91]\n"
"s_waitcnt lgkmcnt(4)\n"
"ds_read_b128 a[204:207], v148 offset:6144\n"
"v_mov_b32_dpp v206, v206 row_ror:8 row_mask:0x4 bank_mask:0xf\n"
"v_accvgpr_mov_b32 a15, a159\n"
"v_accvgpr_mov_b32 a25, a169\n"
"v_accvgpr_mov_b32 a26, a170\n"
"v_accvgpr_mov_b32 a29, a173\n"
"v_accvgpr_mov_b32 a31, a175\n"
"v_accvgpr_mov_b32 a35, a179\n"
"v_accvgpr_mov_b32 a43, a187\n"
"v_accvgpr_mov_b32 a160, a64\n"
"v_mfma_f32_16x16x32_bf16 v[180:183], v[124:127], a[0:3], 0\n"
"s_waitcnt lgkmcnt(4)\n"
"v_mov_b32 v252, v192\n"
"v_accvgpr_mov_b32 a174, a78\n"
"v_mfma_f32_16x16x32_bf16 v[180:183], v[128:131], a[4:7], v[180:183]\n"
"s_waitcnt lgkmcnt(3)\n"
"ds_read_b128 a[192:195], v243 offset:4096\n"
"v_accvgpr_mov_b32 a18, a162\n"
"v_mfma_f32_16x16x32_bf16 v[180:183], v[132:135], a[8:11], v[180:183]\n"
"s_waitcnt lgkmcnt(3)\n"
"ds_read_b128 a[200:203], v243 offset:6144\n"
"v_accvgpr_mov_b32 a183, a87\n"
"v_mfma_f32_16x16x32_bf16 v[180:183], v[136:139], a[12:15], v[180:183]\n"
"v_rcp_f32 v247, v198\n"
"v_accvgpr_mov_b32 a28, a172\n"
"v_accvgpr_mov_b32 a34, a178\n"
"v_accvgpr_mov_b32 a41, a185\n"
"v_accvgpr_mov_b32 a151, a55\n"
"v_accvgpr_mov_b32 a167, a71\n"
"v_accvgpr_mov_b32 a156, a60\n"
"v_accvgpr_mov_b32 a178, a82\n"
"v_mfma_f32_16x16x32_bf16 v[184:187], v[124:127], a[16:19], 0\n"
"ds_read_b128 a[196:199], v148 offset:4096\n"
"v_accvgpr_mov_b32 a158, a62\n"
"v_mfma_f32_16x16x32_bf16 v[184:187], v[128:131], a[20:23], v[184:187]\n"
"v_mov_b32_dpp v247, v247 row_ror:12 row_mask:0x2 bank_mask:0xf\n"
"v_accvgpr_mov_b32 a152, a56\n"
"v_mfma_f32_16x16x32_bf16 v[184:187], v[132:135], a[24:27], v[184:187]\n"
"s_add_u32 s40, s56, s53\n"
"v_accvgpr_mov_b32 a32, a176\n"
"v_accvgpr_mov_b32 a157, a61\n"
"v_mfma_f32_16x16x32_bf16 v[184:187], v[136:139], a[28:31], v[184:187]\n"
"buffer_load_dwordx4 v240, s[20:23], s40 offen offset:2048 nt lds\n"
"v_mov_b32_dpp v206, v206 row_ror:4 row_mask:0x8 bank_mask:0xf\n"
"v_accvgpr_mov_b32 a37, a181\n"
"v_accvgpr_mov_b32 a149, a53\n"
"v_accvgpr_mov_b32 a150, a54\n"
"v_accvgpr_mov_b32 a181, a85\n"
"v_accvgpr_mov_b32 a153, a57\n"
"v_sub_f32 v180, s74, v180\n"
"v_sub_f32 v183, s74, v183\n"
"v_mfma_f32_16x16x32_bf16 v[188:191], v[124:127], a[32:35], 0\n"
"v_mov_b32_dpp v246, v246 row_ror:12 row_mask:0x2 bank_mask:0xf\n"
"v_accvgpr_mov_b32 a180, a84\n"
"v_mfma_f32_16x16x32_bf16 v[188:191], v[128:131], a[36:39], v[188:191]\n"
"v_accvgpr_mov_b32 a162, a66\n"
"v_accvgpr_mov_b32 a179, a83\n"
"v_mfma_f32_16x16x32_bf16 v[188:191], v[132:135], a[40:43], v[188:191]\n"
"v_mov_b32_dpp v246, v246 row_ror:8 row_mask:0x4 bank_mask:0xf\n"
"v_sub_f32 v182, s74, v182\n"
"v_mfma_f32_16x16x32_bf16 v[188:191], v[136:139], a[44:47], v[188:191]\n"
"s_waitcnt lgkmcnt(1)\n"
"ds_write_b128 v244, a[220:223] offset:33792\n"
"v_mov_b32_dpp v247, v247 row_ror:8 row_mask:0x4 bank_mask:0xf\n"
"v_accvgpr_mov_b32 a163, a67\n"
"v_accvgpr_mov_b32 a177, a81\n"
"v_accvgpr_mov_b32 a154, a58\n"
"v_mfma_f32_16x16x32_bf16 v[192:195], v[124:127], a[192:195], 0\n"
"s_waitcnt lgkmcnt(1)\n"
"v_accvgpr_mov_b32 a164, a68\n"
"v_sub_f32 v181, s74, v181\n"
"v_mfma_f32_16x16x32_bf16 v[192:195], v[128:131], a[196:199], v[192:195]\n"
"v_accvgpr_mov_b32 a173, a77\n"
"v_accvgpr_mov_b32 a144, a48\n"
"v_mfma_f32_16x16x32_bf16 v[192:195], v[132:135], a[200:203], v[192:195]\n"
"ds_read_b64_tr_b16 v[226:227], v239 offset:54272\n"
"ds_read_b64_tr_b16 a[246:247], v232 offset:53248\n"
"v_mfma_f32_16x16x32_bf16 v[192:195], v[136:139], a[204:207], v[192:195]\n"
"v_mov_b32_dpp v246, v246 row_ror:4 row_mask:0x8 bank_mask:0xf\n"
"v_mov_b32_dpp v247, v247 row_ror:4 row_mask:0x8 bank_mask:0xf\n"
"v_accvgpr_mov_b32 a148, a52\n"
"v_accvgpr_mov_b32 a165, a69\n"
"v_accvgpr_mov_b32 a175, a79\n"
"v_accvgpr_mov_b32 a176, a80\n"
"v_sub_f32 v184, s74, v184\n"
"v_sub_f32 v187, s74, v187\n"
"v_sub_f32 v190, s74, v190\n"
"v_mfma_f32_16x16x32_bf16 v[152:155], v[208:211], a[0:3], v[180:183]\n"
"v_mov_b32 v253, v202\n"
"v_sub_f32 v185, s74, v185\n"
"v_mfma_f32_16x16x32_bf16 v[152:155], v[212:215], a[4:7], v[152:155]\n"
"v_accvgpr_mov_b32 a186, a90\n"
"v_sub_f32 v186, s74, v186\n"
"v_mfma_f32_16x16x32_bf16 v[152:155], v[216:219], a[8:11], v[152:155]\n"
"s_add_u32 s40, s56, s54\n"
"ds_read_b64_tr_b16 a[254:255], v235 offset:54272\n"
"v_sub_f32 v189, s74, v189\n"
"v_mfma_f32_16x16x32_bf16 v[152:155], v[220:223], a[12:15], v[152:155]\n"
"buffer_load_dwordx4 v240, s[20:23], s40 offen offset:3072 nt lds\n"
"v_accvgpr_mov_b32 a190, a94\n"
"v_accvgpr_mov_b32 a191, a95\n"
"v_accvgpr_mov_b32 a168, a72\n"
"v_mul_f32_dpp v9, v246, v9 row_newbcast:1 row_mask:0xf bank_mask:0xf\n"
"v_mul_f32_dpp v12, v246, v12 row_newbcast:0 row_mask:0xf bank_mask:0xf\n"
"v_sub_f32 v188, s74, v188\n"
"v_sub_f32 v191, s74, v191\n"
"v_mfma_f32_16x16x32_bf16 v[156:159], v[216:219], a[24:27], v[184:187]\n"
"s_add_u32 s55, s55, s59\n"
"v_accvgpr_mov_b32 a159, a63\n"
"v_accvgpr_mov_b32 a155, a59\n"
"v_mfma_f32_16x16x32_bf16 v[156:159], v[220:223], a[28:31], v[156:159]\n"
"s_add_u32 s41, s55, s57\n"
"v_accvgpr_mov_b32 a145, a49\n"
"v_accvgpr_mov_b32 a170, a74\n"
"v_mfma_f32_16x16x32_bf16 v[156:159], v[208:211], a[16:19], v[156:159]\n"
"s_and_b32 s41, s41, s58\n"
"v_accvgpr_mov_b32 a147, a51\n"
"v_sub_f32 v192, s74, v192\n"
"v_mfma_f32_16x16x32_bf16 v[156:159], v[212:215], a[20:23], v[156:159]\n"
"s_andn2_b32 s56, s55, s58\n"
"ds_read_b64_tr_b16 a[250:251], v236 offset:53248\n"
"v_xor_b32 v250, 0x1000, v245\n"
"v_accvgpr_mov_b32 a146, a50\n"
"v_accvgpr_mov_b32 a161, a65\n"
"v_accvgpr_mov_b32 a187, a91\n"
"v_mul_f32_dpp v41, v247, v41 row_newbcast:1 row_mask:0xf bank_mask:0xf\n"
"v_mul_f32_dpp v46, v247, v46 row_newbcast:2 row_mask:0xf bank_mask:0xf\n"
"v_sub_f32 v195, s74, v195\n"
"v_exp_f32 v153, v153\n"
"v_mfma_f32_16x16x32_bf16 v[160:163], v[208:211], a[32:35], v[188:191]\n"
"s_or_b32 s56, s56, s41\n"
"v_mul_f32_dpp v43, v247, v43 row_newbcast:3 row_mask:0xf bank_mask:0xf\n"
"v_sub_f32 v193, s74, v193\n"
"v_mfma_f32_16x16x32_bf16 v[160:163], v[212:215], a[36:39], v[160:163]\n"
"v_accvgpr_mov_b32 a171, a75\n"
"v_accvgpr_mov_b32 a185, a89\n"
"v_mfma_f32_16x16x32_bf16 v[160:163], v[216:219], a[40:43], v[160:163]\n"
"v_accvgpr_mov_b32 a182, a86\n"
"v_sub_f32 v194, s74, v194\n"
"v_mfma_f32_16x16x32_bf16 v[160:163], v[220:223], a[44:47], v[160:163]\n"
"ds_read_b64_tr_b16 a[252:253], v235 offset:50176\n"
"v_accvgpr_mov_b32 a172, a76\n"
"v_accvgpr_mov_b32 a184, a88\n"
"v_exp_f32 v152, v152\n"
"v_exp_f32 v157, v157\n"
"v_exp_f32 v158, v158\n"
"v_mfma_f32_16x16x32_bf16 v[164:167], v[216:219], a[200:203], v[192:195]\n"
"ds_read_b128 v[216:219], v228 offset:5120\n"
"v_accvgpr_mov_b32 a166, a70\n"
"v_mfma_f32_16x16x32_bf16 v[164:167], v[220:223], a[204:207], v[164:167]\n"
"v_accvgpr_mov_b32 a188, a92\n"
"v_mul_f32_dpp v57, v247, v57 row_newbcast:1 row_mask:0xf bank_mask:0xf\n"
"v_mfma_f32_16x16x32_bf16 v[164:167], v[208:211], a[192:195], v[164:167]\n"
"ds_read_b128 v[208:211], v228 offset:4096\n"
"v_accvgpr_mov_b32 a189, a93\n"
"v_mfma_f32_16x16x32_bf16 v[164:167], v[212:215], a[196:199], v[164:167]\n"
"s_waitcnt lgkmcnt(3)\n"
"ds_read_b128 v[220:223], v229 offset:5120\n"
"v_accvgpr_mov_b32 a169, a73\n"
"v_mul_f32_dpp v10, v246, v10 row_newbcast:2 row_mask:0xf bank_mask:0xf\n"
"v_exp_f32 v154, v154\n"
"v_exp_f32 v155, v155\n"
"v_mul_f32_dpp v27, v246, v27 row_newbcast:3 row_mask:0xf bank_mask:0xf\n"
"v_mul_f32_dpp v58, v247, v58 row_newbcast:2 row_mask:0xf bank_mask:0xf\n"
"LTC_%=:\n"
"s_waitcnt lgkmcnt(4)\n"
"v_mfma_f32_16x16x32_bf16 v[92:95], v[116:119], v[224:227], v[92:95]\n"
"s_waitcnt lgkmcnt(3)\n"
"v_mul_f32_dpp v56, v247, v56 row_newbcast:0 row_mask:0xf bank_mask:0xf\n"
"v_cvt_pk_bf16_f32 v56, v56, v57\n"
"v_mfma_f32_16x16x32_bf16 v[92:95], v[112:115], a[252:255], v[92:95]\n"
"v_mul_f32_dpp v13, v246, v13 row_newbcast:1 row_mask:0xf bank_mask:0xf\n"
"v_mul_f32_dpp v44, v247, v44 row_newbcast:0 row_mask:0xf bank_mask:0xf\n"
"v_mfma_f32_16x16x32_bf16 v[60:63], v[108:111], v[224:227], v[60:63]\n"
"s_add_u32 m0, s44, 32768\n"
"ds_read_b128 v[212:215], v229 offset:4096\n"
"v_mul_f32_dpp v8, v246, v8 row_newbcast:0 row_mask:0xf bank_mask:0xf\n"
"v_mfma_f32_16x16x32_bf16 v[60:63], v[104:107], a[252:255], v[60:63]\n"
"buffer_load_dwordx4 v240, s[16:19], s56 offen nt lds\n"
"v_mfma_f32_16x16x32_bf16 v[28:31], v[100:103], v[224:227], v[28:31]\n"
"v_exp_f32 v165, v165\n"
"v_mfma_f32_16x16x32_bf16 v[28:31], v[96:99], a[252:255], v[28:31]\n"
"v_exp_f32 v166, v166\n"
"v_mfma_f32_16x16x32_bf16 a[236:239], v[120:123], a[252:255], a[236:239]\n"
"s_waitcnt lgkmcnt(2)\n"
"ds_read_b64_tr_b16 a[254:255], v233 offset:53248\n"
"v_mul_f32_dpp v42, v247, v42 row_newbcast:2 row_mask:0xf bank_mask:0xf\n"
"v_mfma_f32_16x16x32_bf16 v[168:171], v[216:219], a[8:11], v[180:183]\n"
"s_waitcnt lgkmcnt(2)\n"
"v_exp_f32 v156, v156\n"
"v_mfma_f32_16x16x32_bf16 v[168:171], v[220:223], a[12:15], v[168:171]\n"
"v_cvt_pk_bf16_f32 v8, v8, v9\n"
"v_mul_f32_dpp v76, v206, v76 row_newbcast:0 row_mask:0xf bank_mask:0xf\n"
"v_mfma_f32_16x16x32_bf16 v[168:171], v[208:211], a[0:3], v[168:171]\n"
"s_waitcnt lgkmcnt(1)\n"
"ds_read_b64_tr_b16 v[224:225], v237 offset:49152\n"
"v_mov_b64 v[202:203], 0\n"
"v_mfma_f32_16x16x32_bf16 v[168:171], v[212:215], a[4:7], v[168:171]\n"
"ds_write_b128 v244, a[236:239] offset:35840\n"
"v_mul_f32_dpp v77, v206, v77 row_newbcast:1 row_mask:0xf bank_mask:0xf\n"
"v_mul_f32_dpp v78, v206, v78 row_newbcast:2 row_mask:0xf bank_mask:0xf\n"
"v_cvt_pk_bf16_f32 v136, v156, v157\n"
"v_mul_f32_dpp v59, v247, v59 row_newbcast:3 row_mask:0xf bank_mask:0xf\n"
"v_mul_f32_dpp v88, v206, v88 row_newbcast:0 row_mask:0xf bank_mask:0xf\n"
"v_mfma_f32_16x16x32_bf16 v[172:175], v[208:211], a[16:19], v[184:187]\n"
"v_mul_f32_dpp v15, v246, v15 row_newbcast:3 row_mask:0xf bank_mask:0xf\n"
"v_mul_f32_dpp v25, v246, v25 row_newbcast:1 row_mask:0xf bank_mask:0xf\n"
"v_mfma_f32_16x16x32_bf16 v[172:175], v[212:215], a[20:23], v[172:175]\n"
"ds_read_b64_tr_b16 a[244:245], v232 offset:49152\n"
"v_cvt_pk_bf16_f32 v129, v154, v155\n"
"v_mfma_f32_16x16x32_bf16 v[172:175], v[216:219], a[24:27], v[172:175]\n"
"ds_read_b64_tr_b16 a[248:249], v236 offset:49152\n"
"v_mul_f32_dpp v11, v246, v11 row_newbcast:3 row_mask:0xf bank_mask:0xf\n"
"v_mfma_f32_16x16x32_bf16 v[172:175], v[220:223], a[28:31], v[172:175]\n"
"v_mul_f32_dpp v14, v246, v14 row_newbcast:2 row_mask:0xf bank_mask:0xf\n"
"v_mul_f32_dpp v45, v247, v45 row_newbcast:1 row_mask:0xf bank_mask:0xf\n"
"v_mfma_f32_16x16x32_bf16 v[176:179], v[216:219], a[40:43], v[188:191]\n"
"ds_read_b64_tr_b16 a[252:253], v233 offset:49152\n"
"v_mul_f32_dpp v73, v206, v73 row_newbcast:1 row_mask:0xf bank_mask:0xf\n"
"v_mfma_f32_16x16x32_bf16 v[176:179], v[220:223], a[44:47], v[176:179]\n"
"v_mul_f32_dpp v72, v206, v72 row_newbcast:0 row_mask:0xf bank_mask:0xf\n"
"v_mul_f32_dpp v93, v206, v93 row_newbcast:1 row_mask:0xf bank_mask:0xf\n"
"v_mfma_f32_16x16x32_bf16 v[176:179], v[208:211], a[32:35], v[176:179]\n"
"s_add_u32 s40, s56, s52\n"
"ds_read_b64_tr_b16 v[226:227], v237 offset:53248\n"
"v_cvt_pk_bf16_f32 v128, v152, v153\n"
"v_mfma_f32_16x16x32_bf16 v[176:179], v[212:215], a[36:39], v[176:179]\n"
"buffer_load_dwordx4 v240, s[16:19], s40 offen offset:1024 nt lds\n"
"v_mul_f32_dpp v47, v247, v47 row_newbcast:3 row_mask:0xf bank_mask:0xf\n"
"v_exp_f32 v161, v161\n"
"v_mul_f32_dpp v90, v206, v90 row_newbcast:2 row_mask:0xf bank_mask:0xf\n"
"v_exp_f32 v169, v169\n"
"v_exp_f32 v170, v170\n"
"v_mfma_f32_16x16x32_bf16 v[152:155], v[208:211], a[192:195], v[192:195]\n"
"v_exp_f32 v162, v162\n"
"v_mfma_f32_16x16x32_bf16 v[152:155], v[212:215], a[196:199], v[152:155]\n"
"v_exp_f32 v159, v159\n"
"v_mfma_f32_16x16x32_bf16 v[152:155], v[216:219], a[200:203], v[152:155]\n"
"ds_read_b128 v[212:215], v231 offset:0\n"
"v_cvt_pk_bf16_f32 v137, v158, v159\n"
"v_mfma_f32_16x16x32_bf16 v[152:155], v[220:223], a[204:207], v[152:155]\n"
"s_waitcnt lgkmcnt(3)\n"
"ds_read_b128 v[220:223], v231 offset:1024\n"
"v_mul_f32_dpp v30, v246, v30 row_newbcast:2 row_mask:0xf bank_mask:0xf\n"
"v_mfma_f32_16x16x32_bf16 a[208:211], v[120:123], a[244:247], a[208:211]\n"
"ds_read_b128 v[208:211], v230 offset:0\n"
"v_exp_f32 v163, v163\n"
"v_exp_f32 v167, v167\n"
"v_mul_f32_dpp v62, v247, v62 row_newbcast:2 row_mask:0xf bank_mask:0xf\n"
"v_exp_f32 v168, v168\n"
"v_exp_f32 v178, v178\n"
"v_mfma_f32_16x16x32_bf16 v[0:3], v[96:99], a[244:247], v[0:3]\n"
"v_cvt_pk_bf16_f32 v125, v166, v167\n"
"v_mul_f32_dpp v61, v247, v61 row_newbcast:1 row_mask:0xf bank_mask:0xf\n"
"v_mfma_f32_16x16x32_bf16 v[0:3], v[100:103], a[248:251], v[0:3]\n"
"ds_read_b128 v[216:219], v230 offset:1024\n"
"v_mul_f32_dpp v40, v247, v40 row_newbcast:0 row_mask:0xf bank_mask:0xf\n"
"v_cvt_pk_bf16_f32 v40, v40, v41\n"
"v_exp_f32 v160, v160\n"
"v_cvt_pk_bf16_f32 v145, v162, v163\n"
"v_exp_f32 v164, v164\n"
"v_cvt_pk_bf16_f32 v57, v58, v59\n"
"v_mfma_f32_16x16x32_bf16 v[32:35], v[104:107], a[244:247], v[32:35]\n"
"v_exp_f32 v174, v174\n"
"v_mfma_f32_16x16x32_bf16 v[32:35], v[108:111], a[248:251], v[32:35]\n"
"ds_write_b128 v244, a[232:235] offset:34816\n"
"v_mul_f32_dpp v74, v206, v74 row_newbcast:2 row_mask:0xf bank_mask:0xf\n"
"v_mul_f32_dpp v26, v246, v26 row_newbcast:2 row_mask:0xf bank_mask:0xf\n"
"v_exp_f32 v176, v176\n"
"v_mfma_f32_16x16x32_bf16 v[64:67], v[112:115], a[244:247], v[64:67]\n"
"v_mul_f32_dpp v94, v206, v94 row_newbcast:2 row_mask:0xf bank_mask:0xf\n"
"v_mul_f32_dpp v95, v206, v95 row_newbcast:3 row_mask:0xf bank_mask:0xf\n"
"v_mfma_f32_16x16x32_bf16 v[64:67], v[116:119], a[248:251], v[64:67]\n"
"s_waitcnt lgkmcnt(2)\n"
"ds_write_b128 v244, a[216:219] offset:32768\n"
"v_cvt_pk_bf16_f32 v41, v42, v43\n"
"v_mul_f32_dpp v79, v206, v79 row_newbcast:3 row_mask:0xf bank_mask:0xf\n"
"v_mul_f32_dpp v28, v246, v28 row_newbcast:0 row_mask:0xf bank_mask:0xf\n"
"v_mul_f32_dpp v29, v246, v29 row_newbcast:1 row_mask:0xf bank_mask:0xf\n"
"v_mul_f32_dpp v89, v206, v89 row_newbcast:1 row_mask:0xf bank_mask:0xf\n"
"v_mfma_f32_16x16x32_bf16 v[156:159], v[208:211], a[0:3], v[180:183]\n"
"v_exp_f32 v172, v172\n"
"v_mfma_f32_16x16x32_bf16 v[156:159], v[212:215], a[4:7], v[156:159]\n"
"s_waitcnt lgkmcnt(2)\n"
"v_exp_f32 v175, v175\n"
"v_mfma_f32_16x16x32_bf16 v[156:159], v[216:219], a[8:11], v[156:159]\n"
"s_add_u32 s40, s56, s53\n"
"ds_read_b64_tr_b16 a[250:251], v236 offset:54272\n"
"v_cvt_pk_bf16_f32 v144, v160, v161\n"
"v_mfma_f32_16x16x32_bf16 v[156:159], v[220:223], a[12:15], v[156:159]\n"
"buffer_load_dwordx4 v240, s[16:19], s40 offen offset:2048 nt lds\n"
"v_cvt_pk_bf16_f32 v72, v72, v73\n"
"v_mul_f32_dpp v24, v246, v24 row_newbcast:0 row_mask:0xf bank_mask:0xf\n"
"v_mul_f32_dpp v92, v206, v92 row_newbcast:0 row_mask:0xf bank_mask:0xf\n"
"v_cvt_pk_bf16_f32 v88, v88, v89\n"
"v_exp_f32 v177, v177\n"
"v_exp_f32 v155, v155\n"
"v_mfma_f32_16x16x32_bf16 v[160:163], v[216:219], a[24:27], v[184:187]\n"
"v_exp_f32 v171, v171\n"
"v_mfma_f32_16x16x32_bf16 v[160:163], v[220:223], a[28:31], v[160:163]\n"
"v_mul_f32_dpp v60, v247, v60 row_newbcast:0 row_mask:0xf bank_mask:0xf\n"
"v_mul_f32_dpp v32, v247, v32 row_newbcast:0 row_mask:0xf bank_mask:0xf\n"
"v_mfma_f32_16x16x32_bf16 v[160:163], v[208:211], a[16:19], v[160:163]\n"
"v_cvt_pk_bf16_f32 v130, v168, v169\n"
"v_mul_f32_dpp v0, v246, v0 row_newbcast:0 row_mask:0xf bank_mask:0xf\n"
"v_mfma_f32_16x16x32_bf16 v[160:163], v[212:215], a[20:23], v[160:163]\n"
"ds_read_b64_tr_b16 a[244:245], v232 offset:50176\n"
"v_cvt_pk_bf16_f32 v124, v164, v165\n"
"v_mul_f32_dpp v63, v247, v63 row_newbcast:3 row_mask:0xf bank_mask:0xf\n"
"v_mul_f32_dpp v91, v206, v91 row_newbcast:3 row_mask:0xf bank_mask:0xf\n"
"v_exp_f32 v179, v179\n"
"v_exp_f32 v152, v152\n"
"v_mul_f32_dpp v64, v206, v64 row_newbcast:0 row_mask:0xf bank_mask:0xf\n"
"v_mfma_f32_16x16x32_bf16 v[164:167], v[208:211], a[32:35], v[188:191]\n"
"ds_read_b64_tr_b16 a[248:249], v236 offset:50176\n"
"v_cvt_pk_bf16_f32 v24, v24, v25\n"
"v_mfma_f32_16x16x32_bf16 v[164:167], v[212:215], a[36:39], v[164:167]\n"
"v_cvt_pk_bf16_f32 v9, v10, v11\n"
"v_cvt_pk_bf16_f32 v11, v14, v15\n"
"v_mfma_f32_16x16x32_bf16 v[164:167], v[216:219], a[40:43], v[164:167]\n"
"ds_read_b128 v[216:219], v230 offset:5120\n"
"v_cvt_pk_bf16_f32 v25, v26, v27\n"
"v_mfma_f32_16x16x32_bf16 v[164:167], v[220:223], a[44:47], v[164:167]\n"
"ds_read_b128 v[212:215], v231 offset:4096\n"
"v_cvt_pk_bf16_f32 v43, v46, v47\n"
"v_cvt_pk_bf16_f32 v26, v28, v29\n"
"v_exp_f32 v173, v173\n"
"v_exp_f32 v154, v154\n"
"v_exp_f32 v157, v157\n"
"v_mul_f32_dpp v65, v206, v65 row_newbcast:1 row_mask:0xf bank_mask:0xf\n"
"v_mfma_f32_16x16x32_bf16 v[68:71], v[116:119], v[224:227], v[68:71]\n"
"ds_read_b128 v[208:211], v230 offset:4096\n"
"v_mul_f32_dpp v75, v206, v75 row_newbcast:3 row_mask:0xf bank_mask:0xf\n"
"v_mfma_f32_16x16x32_bf16 v[68:71], v[112:115], a[252:255], v[68:71]\n"
"ds_read_b64_tr_b16 a[246:247], v232 offset:54272\n"
"v_cvt_pk_bf16_f32 v58, v60, v61\n"
"v_cvt_pk_bf16_f32 v139, v174, v175\n"
"v_cvt_pk_bf16_f32 v146, v176, v177\n"
"v_exp_f32 v161, v161\n"
"v_exp_f32 v163, v163\n"
"v_mul_f32_dpp v2, v246, v2 row_newbcast:2 row_mask:0xf bank_mask:0xf\n"
"v_mfma_f32_16x16x32_bf16 v[36:39], v[108:111], v[224:227], v[36:39]\n"
"s_add_u32 s40, s56, s54\n"
"ds_read_b128 v[220:223], v231 offset:5120\n"
"v_cvt_pk_bf16_f32 v138, v172, v173\n"
"v_mfma_f32_16x16x32_bf16 v[36:39], v[104:107], a[252:255], v[36:39]\n"
"buffer_load_dwordx4 v240, s[16:19], s40 offen offset:3072 nt lds\n"
"v_cvt_pk_bf16_f32 v10, v12, v13\n"
"v_cvt_pk_bf16_f32 v73, v74, v75\n"
"v_cvt_pk_bf16_f32 v127, v154, v155\n"
"v_exp_f32 v156, v156\n"
"v_exp_f32 v162, v162\n"
"v_mfma_f32_16x16x32_bf16 v[4:7], v[100:103], v[224:227], v[4:7]\n"
"v_mul_f32_dpp v31, v246, v31 row_newbcast:3 row_mask:0xf bank_mask:0xf\n"
"v_mul_f32_dpp v34, v247, v34 row_newbcast:2 row_mask:0xf bank_mask:0xf\n"
"v_mfma_f32_16x16x32_bf16 v[4:7], v[96:99], a[252:255], v[4:7]\n"
"v_cvt_pk_bf16_f32 v59, v62, v63\n"
"v_cvt_pk_bf16_f32 v131, v170, v171\n"
"v_mfma_f32_16x16x32_bf16 a[212:215], v[120:123], a[252:255], a[212:215]\n"
"s_waitcnt lgkmcnt(2)\n"
"ds_write_b64 v250, v[252:253] offset:52224\n"
"v_cvt_pk_bf16_f32 v42, v44, v45\n"
"v_cvt_pk_bf16_f32 v89, v90, v91\n"
"v_exp_f32 v153, v153\n"
"v_mul_f32_dpp v3, v246, v3 row_newbcast:3 row_mask:0xf bank_mask:0xf\n"
"v_mul_f32_dpp v33, v247, v33 row_newbcast:1 row_mask:0xf bank_mask:0xf\n"
"v_mul_f32_dpp v69, v206, v69 row_newbcast:1 row_mask:0xf bank_mask:0xf\n"
"v_mfma_f32_16x16x32_bf16 v[168:171], v[208:211], a[0:3], v[180:183]\n"
"ds_read_b64_tr_b16 v[226:227], v237 offset:54272\n"
"v_mul_f32_dpp v39, v247, v39 row_newbcast:3 row_mask:0xf bank_mask:0xf\n"
"v_mfma_f32_16x16x32_bf16 v[168:171], v[212:215], a[4:7], v[168:171]\n"
"v_cvt_pk_bf16_f32 v74, v76, v77\n"
"v_mul_f32_dpp v1, v246, v1 row_newbcast:1 row_mask:0xf bank_mask:0xf\n"
"v_mfma_f32_16x16x32_bf16 v[168:171], v[216:219], a[8:11], v[168:171]\n"
"s_waitcnt lgkmcnt(2)\n"
"ds_read_b64_tr_b16 a[254:255], v233 offset:54272\n"
"v_mul_f32_dpp v68, v206, v68 row_newbcast:0 row_mask:0xf bank_mask:0xf\n"
"v_mfma_f32_16x16x32_bf16 v[168:171], v[220:223], a[12:15], v[168:171]\n"
"ds_write_b128 v248, v[40:43] offset:50176\n"
"v_cvt_pk_bf16_f32 v75, v78, v79\n"
"v_exp_f32 v158, v158\n"
"v_exp_f32 v160, v160\n"
"v_mfma_f32_16x16x32_bf16 v[172:175], v[216:219], a[24:27], v[184:187]\n"
"ds_read_b64_tr_b16 a[68:69], v249 offset:50176\n"
"v_cvt_pk_bf16_f32 v140, v160, v161\n"
"v_mfma_f32_16x16x32_bf16 v[172:175], v[220:223], a[28:31], v[172:175]\n"
"v_cvt_pk_bf16_f32 v147, v178, v179\n"
"v_mul_f32_dpp v70, v206, v70 row_newbcast:2 row_mask:0xf bank_mask:0xf\n"
"v_mfma_f32_16x16x32_bf16 v[172:175], v[208:211], a[16:19], v[172:175]\n"
"v_mul_f32_dpp v35, v247, v35 row_newbcast:3 row_mask:0xf bank_mask:0xf\n"
"v_mul_f32_dpp v36, v247, v36 row_newbcast:0 row_mask:0xf bank_mask:0xf\n"
"v_mfma_f32_16x16x32_bf16 v[172:175], v[212:215], a[20:23], v[172:175]\n"
"s_cbranch_vccnz LPCT0_%=\nLPKT0_%=:\n"
"ds_write_b128 v248, v[8:11] offset:49152\n"
"v_cvt_pk_bf16_f32 v91, v94, v95\n"
"v_mul_f32_dpp v37, v247, v37 row_newbcast:1 row_mask:0xf bank_mask:0xf\n"
"v_mul_f32_dpp v66, v206, v66 row_newbcast:2 row_mask:0xf bank_mask:0xf\n"
"v_mul_f32_dpp v67, v206, v67 row_newbcast:3 row_mask:0xf bank_mask:0xf\n"
"v_mfma_f32_16x16x32_bf16 v[176:179], v[208:211], a[32:35], v[188:191]\n"
"ds_read_b64_tr_b16 a[70:71], v249 offset:50184\n"
"ds_read_b64_tr_b16 v[224:225], v237 offset:50176\n"
"v_mfma_f32_16x16x32_bf16 v[176:179], v[212:215], a[36:39], v[176:179]\n"
"ds_read_b64_tr_b16 a[252:253], v233 offset:50176\n"
"v_mul_f32_dpp v7, v246, v7 row_newbcast:3 row_mask:0xf bank_mask:0xf\n"
"v_mfma_f32_16x16x32_bf16 v[176:179], v[216:219], a[40:43], v[176:179]\n"
"ds_read_b64_tr_b16 a[52:53], v249 offset:49152\n"
"ds_read_b64_tr_b16 a[54:55], v249 offset:49160\n"
"v_mfma_f32_16x16x32_bf16 v[176:179], v[220:223], a[44:47], v[176:179]\n"
"ds_write_b128 v248, v[56:59] offset:50176\n"
"v_cvt_pk_bf16_f32 v27, v30, v31\n"
"v_exp_f32 v171, v171\n"
"v_exp_f32 v172, v172\n"
"v_mfma_f32_16x16x32_bf16 a[224:227], v[120:123], a[244:247], a[224:227]\n"
"ds_write_b128 v248, v[24:27] offset:49152\n"
"v_exp_f32 v164, v164\n"
"v_mul_f32_dpp v6, v246, v6 row_newbcast:2 row_mask:0xf bank_mask:0xf\n"
"v_cvt_pk_bf16_f32 v0, v0, v1\n"
"v_xor_b32 v244, 0x1000, v244\n"
"v_mfma_f32_16x16x32_bf16 v[16:19], v[96:99], a[244:247], v[16:19]\n"
"s_waitcnt vmcnt(8) lgkmcnt(4)\n"
"ds_read_b64_tr_b16 a[78:79], v249 offset:50184\n"
"v_cvt_pk_bf16_f32 v1, v2, v3\n"
"v_mfma_f32_16x16x32_bf16 v[16:19], v[100:103], a[248:251], v[16:19]\n"
"s_barrier\n"
"ds_read_b64_tr_b16 a[60:61], v249 offset:49152\n"
"ds_read_b128 v[216:219], v228 offset:17408\n"
"v_cvt_pk_bf16_f32 v32, v32, v33\n"
"v_mul_f32_dpp v71, v206, v71 row_newbcast:3 row_mask:0xf bank_mask:0xf\n"
"v_cvt_pk_bf16_f32 v64, v64, v65\n"
"v_cvt_pk_bf16_f32 v65, v66, v67\n"
"v_exp_f32 v174, v174\n"
"v_mfma_f32_16x16x32_bf16 v[48:51], v[104:107], a[244:247], v[48:51]\n"
"ds_read_b128 v[28:31], v244 offset:33792\n"
"v_mul_f32_dpp v4, v246, v4 row_newbcast:0 row_mask:0xf bank_mask:0xf\n"
"v_mfma_f32_16x16x32_bf16 v[48:51], v[108:111], a[248:251], v[48:51]\n"
"ds_write_b128 v248, v[72:75] offset:51200\n"
"v_cvt_pk_bf16_f32 v126, v152, v153\n"
"v_cvt_pk_bf16_f32 v132, v156, v157\n"
"v_exp_f32 v165, v165\n"
"v_mfma_f32_16x16x32_bf16 v[80:83], v[112:115], a[244:247], v[80:83]\n"
"ds_read_b128 v[208:211], v228 offset:16384\n"
"v_mul_f32_dpp v38, v247, v38 row_newbcast:2 row_mask:0xf bank_mask:0xf\n"
"v_mfma_f32_16x16x32_bf16 v[80:83], v[116:119], a[248:251], v[80:83]\n"
"ds_read_b64_tr_b16 a[76:77], v249 offset:50176\n"
"ds_read_b128 v[212:215], v229 offset:16384\n"
"v_exp_f32 v159, v159\n"
"v_cvt_pk_bf16_f32 v33, v34, v35\n"
"v_exp_f32 v168, v168\n"
"v_exp_f32 v173, v173\n"
"v_mfma_f32_16x16x32_bf16 v[84:87], v[116:119], v[224:227], v[84:87]\n"
"ds_read_b64_tr_b16 a[84:85], v249 offset:51200\n"
"ds_read_b64 v[250:251], v245 offset:52224\n"
"v_mfma_f32_16x16x32_bf16 v[84:87], v[112:115], a[252:255], v[84:87]\n"
"ds_read_b64_tr_b16 a[86:87], v249 offset:51208\n"
"ds_read_b128 v[220:223], v229 offset:17408\n"
"v_cvt_pk_bf16_f32 v90, v92, v93\n"
"v_exp_f32 v166, v166\n"
"v_mul_f32_dpp v5, v246, v5 row_newbcast:1 row_mask:0xf bank_mask:0xf\n"
"v_cvt_pk_bf16_f32 v66, v68, v69\n"
"v_exp_f32 v175, v175\n"
"v_mfma_f32_16x16x32_bf16 v[52:55], v[108:111], v[224:227], v[52:55]\n"
"v_cvt_pk_bf16_f32 v34, v36, v37\n"
"v_mul_f32_dpp v81, v206, v81 row_newbcast:1 row_mask:0xf bank_mask:0xf\n"
"v_mfma_f32_16x16x32_bf16 v[52:55], v[104:107], a[252:255], v[52:55]\n"
"ds_write_b128 v248, v[88:91] offset:51200\n"
"v_cvt_pk_bf16_f32 v133, v158, v159\n"
"v_exp_f32 v177, v177\n"
"v_exp_f32 v179, v179\n"
"v_mfma_f32_16x16x32_bf16 v[20:23], v[100:103], v[224:227], v[20:23]\n"
"ds_read_b128 v[24:27], v244 offset:32768\n"
"v_cvt_pk_bf16_f32 v2, v4, v5\n"
"v_mfma_f32_16x16x32_bf16 v[20:23], v[96:99], a[252:255], v[20:23]\n"
"s_waitcnt lgkmcnt(4)\n"
"ds_read_b64_tr_b16 a[62:63], v249 offset:49160\n"
"ds_read_b64_tr_b16 a[244:245], v234 offset:0\n"
"ds_read_b64_tr_b16 a[248:249], v238 offset:0\n"
"v_exp_f32 v167, v167\n"
"v_cvt_pk_bf16_f32 v3, v6, v7\n"
"v_cvt_pk_bf16_f32 v35, v38, v39\n"
"v_exp_f32 v178, v178\n"
"v_min_f32 v254, v252, v250\n"
"v_mfma_f32_16x16x32_bf16 a[228:231], v[120:123], a[252:255], a[228:231]\n"
"ds_read_b64_tr_b16 a[92:93], v249 offset:51200\n"
"ds_read_b64_tr_b16 a[94:95], v249 offset:51208\n"
"v_mfma_f32_16x16x32_bf16 v[152:155], v[208:211], a[0:3], v[180:183]\n"
"ds_read_b128 v[60:63], v244 offset:35840\n"
"v_cvt_pk_bf16_f32 v67, v70, v71\n"
"v_mfma_f32_16x16x32_bf16 v[152:155], v[212:215], a[4:7], v[152:155]\n"
"ds_write_b128 v248, v[64:67] offset:51200\n"
"v_exp_f32 v169, v169\n"
"v_mul_f32_dpp v48, v247, v48 row_newbcast:0 row_mask:0xf bank_mask:0xf\n"
"v_mul_f32_dpp v84, v206, v84 row_newbcast:0 row_mask:0xf bank_mask:0xf\n"
"v_mov_b64 v[198:199], 0\n"
"v_mfma_f32_16x16x32_bf16 v[152:155], v[216:219], a[8:11], v[152:155]\n"
"s_waitcnt lgkmcnt(9)\n"
"ds_write_b128 v248, v[32:35] offset:50176\n"
"v_cvt_pk_bf16_f32 v141, v162, v163\n"
"v_cvt_pk_bf16_f32 v149, v166, v167\n"
"v_exp_f32 v170, v170\n"
"v_sub_f32 v252, v254, v252\n"
"v_mfma_f32_16x16x32_bf16 v[152:155], v[220:223], a[12:15], v[152:155]\n"
"ds_write_b128 v248, v[0:3] offset:49152\n"
"v_sub_f32 v250, v254, v250\n"
"v_exp_f32 v250, v250\n"
"v_mul_f32_dpp v50, v247, v50 row_newbcast:2 row_mask:0xf bank_mask:0xf\n"
"v_mfma_f32_16x16x32_bf16 v[156:159], v[216:219], a[24:27], v[184:187]\n"
"ds_read_b64_tr_b16 a[64:65], v249 offset:50176\n"
"ds_read_b128 v[56:59], v244 offset:34816\n"
"v_mfma_f32_16x16x32_bf16 v[156:159], v[220:223], a[28:31], v[156:159]\n"
"ds_read_b64_tr_b16 a[80:81], v249 offset:51200\n"
"v_mul_f32_dpp v17, v246, v17 row_newbcast:1 row_mask:0xf bank_mask:0xf\n"
"v_mfma_f32_16x16x32_bf16 v[156:159], v[208:211], a[16:19], v[156:159]\n"
"ds_read_b64_tr_b16 a[246:247], v234 offset:4096\n"
"v_cvt_pk_bf16_f32 v134, v168, v169\n"
"v_mfma_f32_16x16x32_bf16 v[156:159], v[212:215], a[20:23], v[156:159]\n"
"ds_read_b64_tr_b16 a[48:49], v249 offset:49152\n"
"ds_read_b64_tr_b16 a[50:51], v249 offset:49160\n"
"ds_read_b64_tr_b16 a[66:67], v249 offset:50184\n"
"ds_read_b64_tr_b16 a[82:83], v249 offset:51208\n"
"v_exp_f32 v176, v176\n"
"v_exp_f32 v252, v252\n"
"v_exp_f32 v153, v153\n"
"v_mfma_f32_16x16x32_bf16 v[160:163], v[208:211], a[32:35], v[188:191]\n"
"v_cvt_pk_bf16_f32 v142, v172, v173\n"
"v_accvgpr_read_b32 v254, a208\n"
"v_mfma_f32_16x16x32_bf16 v[160:163], v[212:215], a[36:39], v[160:163]\n"
"ds_read_b64_tr_b16 a[250:251], v238 offset:4096\n"
"v_cvt_pk_bf16_f32 v148, v164, v165\n"
"v_mfma_f32_16x16x32_bf16 v[160:163], v[216:219], a[40:43], v[160:163]\n"
"ds_read_b64_tr_b16 a[252:253], v235 offset:0\n"
"ds_read_b64_tr_b16 a[254:255], v235 offset:4096\n"
"v_mfma_f32_16x16x32_bf16 v[160:163], v[220:223], a[44:47], v[160:163]\n"
"v_exp_f32 v159, v159\n"
"v_mfma_f32_16x16x32_bf16 v[164:167], v[216:219], a[200:203], v[192:195]\n"
"ds_read_b64_tr_b16 v[226:227], v239 offset:4096\n"
"v_mul_f32 v253, v253, v252\n"
"v_mfma_f32_16x16x32_bf16 v[164:167], v[220:223], a[204:207], v[164:167]\n"
"v_cvt_pk_bf16_f32 v135, v170, v171\n"
"v_fmac_f32 v253, v251, v250\n"
"v_mfma_f32_16x16x32_bf16 v[164:167], v[208:211], a[192:195], v[164:167]\n"
"ds_read_b128 v[220:223], v229 offset:21504\n"
"v_cvt_pk_bf16_f32 v150, v176, v177\n"
"v_mfma_f32_16x16x32_bf16 v[164:167], v[212:215], a[196:199], v[164:167]\n"
"s_waitcnt lgkmcnt(5)\n"
"ds_read_b64_tr_b16 v[224:225], v239 offset:0\n"
"v_mfma_f32_16x16x32_bf16 a[216:219], v[124:127], a[244:247], 0\n"
"v_rcp_f32 v253, v253\n"
"v_mfma_f32_16x16x32_bf16 v[8:11], v[128:131], a[244:247], 0\n"
"s_waitcnt lgkmcnt(5)\n"
"s_add_u32 m0, s44, 98304\n"
"v_exp_f32 v154, v154\n"
"v_mfma_f32_16x16x32_bf16 v[8:11], v[132:135], a[248:251], v[8:11]\n"
"buffer_load_dwordx4 v240, s[20:23], s56 offen nt lds\n"
"v_cvt_pk_bf16_f32 v143, v174, v175\n"
"v_mul_f32 v250, v250, v253\n"
"v_mul_f32_dpp v49, v247, v49 row_newbcast:1 row_mask:0xf bank_mask:0xf\n"
"v_exp_f32 v152, v152\n"
"v_exp_f32 v164, v164\n"
"v_mfma_f32_16x16x32_bf16 v[40:43], v[136:139], a[244:247], 0\n"
"v_mul_f32 v252, v252, v253\n"
"v_mul_f32_dpp v16, v246, v16 row_newbcast:0 row_mask:0xf bank_mask:0xf\n"
"v_mfma_f32_16x16x32_bf16 v[40:43], v[140:143], a[248:251], v[40:43]\n"
"v_cvt_pk_bf16_f32 v151, v178, v179\n"
"v_mov_b32_dpp v250, v250 row_ror:12 row_mask:0x2 bank_mask:0xf\n"
"v_mov_b32_dpp v252, v252 row_ror:12 row_mask:0x2 bank_mask:0xf\n"
"v_accvgpr_read_b32 v253, a210\n"
"v_mul_f32_dpp v22, v246, v22 row_newbcast:2 row_mask:0xf bank_mask:0xf\n"
"v_exp_f32 v155, v155\n"
"v_exp_f32 v162, v162\n"
"v_mfma_f32_16x16x32_bf16 v[72:75], v[144:147], a[244:247], 0\n"
"ds_read_b128 v[212:215], v229 offset:20480\n"
"v_cvt_pk_bf16_f32 v97, v154, v155\n"
"v_mfma_f32_16x16x32_bf16 v[72:75], v[148:151], a[248:251], v[72:75]\n"
"s_waitcnt lgkmcnt(1)\n"
"ds_read_b64_tr_b16 a[246:247], v234 offset:5120\n"
"ds_read_b64_tr_b16 a[248:249], v238 offset:1024\n"
"v_mfma_f32_16x16x32_bf16 v[76:79], v[148:151], v[224:227], 0\n"
"v_mov_b32_dpp v252, v252 row_ror:8 row_mask:0x4 bank_mask:0xf\n"
"v_mul_f32_dpp v20, v246, v20 row_newbcast:0 row_mask:0xf bank_mask:0xf\n"
"v_mfma_f32_16x16x32_bf16 v[76:79], v[144:147], a[252:255], v[76:79]\n"
"ds_read_b128 v[216:219], v228 offset:21504\n"
"v_mov_b32_dpp v250, v250 row_ror:8 row_mask:0x4 bank_mask:0xf\n"
"v_mov_b32_dpp v252, v252 row_ror:4 row_mask:0x8 bank_mask:0xf\n"
"v_mov_b64 v[196:197], 0\n"
"v_exp_f32 v156, v156\n"
"v_exp_f32 v158, v158\n"
"v_exp_f32 v165, v165\n"
"v_mfma_f32_16x16x32_bf16 v[44:47], v[140:143], v[224:227], 0\n"
"v_mov_b32_dpp v250, v250 row_ror:4 row_mask:0x8 bank_mask:0xf\n"
"v_mul_f32_dpp v18, v246, v18 row_newbcast:2 row_mask:0xf bank_mask:0xf\n"
"v_mfma_f32_16x16x32_bf16 v[44:47], v[136:139], a[252:255], v[44:47]\n"
"v_mul_f32_dpp v24, v250, v24 row_newbcast:0 row_mask:0xf bank_mask:0xf\n"
"v_accvgpr_read_b32 v255, a209\n"
"v_mul_f32_dpp v25, v250, v25 row_newbcast:1 row_mask:0xf bank_mask:0xf\n"
"v_mul_f32_dpp v26, v250, v26 row_newbcast:2 row_mask:0xf bank_mask:0xf\n"
"v_mul_f32_dpp v19, v246, v19 row_newbcast:3 row_mask:0xf bank_mask:0xf\n"
"v_mul_f32_dpp v80, v206, v80 row_newbcast:0 row_mask:0xf bank_mask:0xf\n"
"v_mul_f32_dpp v85, v206, v85 row_newbcast:1 row_mask:0xf bank_mask:0xf\n"
"v_mul_f32_dpp v59, v250, v59 row_newbcast:3 row_mask:0xf bank_mask:0xf\n"
"v_mul_f32_dpp v61, v250, v61 row_newbcast:1 row_mask:0xf bank_mask:0xf\n"
"v_mov_b64 v[200:201], 0\n"
"v_mfma_f32_16x16x32_bf16 v[12:15], v[132:135], v[224:227], 0\n"
"s_add_u32 s40, s56, s52\n"
"ds_read_b128 v[208:211], v228 offset:20480\n"
"v_mul_f32_dpp v30, v250, v30 row_newbcast:2 row_mask:0xf bank_mask:0xf\n"
"v_mfma_f32_16x16x32_bf16 v[12:15], v[128:131], a[252:255], v[12:15]\n"
"buffer_load_dwordx4 v240, s[20:23], s40 offen offset:1024 nt lds\n"
"v_accvgpr_read_b32 v251, a211\n"
"v_mul_f32_dpp v27, v250, v27 row_newbcast:3 row_mask:0xf bank_mask:0xf\n"
"v_fmac_f32_dpp v24, v252, v254 row_newbcast:0 row_mask:0xf bank_mask:0xf\n"
"v_fmac_f32_dpp v25, v252, v255 row_newbcast:1 row_mask:0xf bank_mask:0xf\n"
"v_fmac_f32_dpp v26, v252, v253 row_newbcast:2 row_mask:0xf bank_mask:0xf\n"
"v_accvgpr_read_b32 v253, a214\n"
"v_cvt_pk_bf16_f32 v16, v16, v17\n"
"v_mfma_f32_16x16x32_bf16 a[220:223], v[124:127], a[252:255], 0\n"
"v_mul_f32_dpp v53, v247, v53 row_newbcast:1 row_mask:0xf bank_mask:0xf\n"
"v_mul_f32_dpp v57, v250, v57 row_newbcast:1 row_mask:0xf bank_mask:0xf\n"
"v_smfmac_f32_16x16x64_bf16 v[196:199], a[240:243], v[128:135], v241\n"
"v_fmac_f32_dpp v27, v252, v251 row_newbcast:3 row_mask:0xf bank_mask:0xf\n"
"v_accvgpr_read_b32 v254, a212\n"
"v_mul_f32_dpp v28, v250, v28 row_newbcast:0 row_mask:0xf bank_mask:0xf\n"
"v_accvgpr_read_b32 v255, a213\n"
"v_accvgpr_read_b32 v251, a215\n"
"v_mul_f32_dpp v31, v250, v31 row_newbcast:3 row_mask:0xf bank_mask:0xf\n"
"v_cvt_pk_bf16_f32 v24, v24, v25\n"
"v_mul_f32_dpp v54, v247, v54 row_newbcast:2 row_mask:0xf bank_mask:0xf\n"
"v_mul_f32_dpp v58, v250, v58 row_newbcast:2 row_mask:0xf bank_mask:0xf\n"
"v_mul_f32_dpp v60, v250, v60 row_newbcast:0 row_mask:0xf bank_mask:0xf\n"
"v_mfma_f32_16x16x32_bf16 v[202:205], a[240:243], v[124:127], v[202:205]\n"
"s_waitcnt lgkmcnt(1)\n"
"ds_read_b64_tr_b16 a[244:245], v234 offset:1024\n"
"v_mul_f32_dpp v29, v250, v29 row_newbcast:1 row_mask:0xf bank_mask:0xf\n"
"v_fmac_f32_dpp v28, v252, v254 row_newbcast:0 row_mask:0xf bank_mask:0xf\n"
"v_fmac_f32_dpp v30, v252, v253 row_newbcast:2 row_mask:0xf bank_mask:0xf\n"
"v_mul_f32_dpp v56, v250, v56 row_newbcast:0 row_mask:0xf bank_mask:0xf\n"
"v_mul_f32_dpp v63, v250, v63 row_newbcast:3 row_mask:0xf bank_mask:0xf\n"
"v_exp_f32 v157, v157\n"
"v_exp_f32 v160, v160\n"
"v_mfma_f32_16x16x32_bf16 v[168:171], v[216:219], a[8:11], v[180:183]\n"
"v_fmac_f32_dpp v31, v252, v251 row_newbcast:3 row_mask:0xf bank_mask:0xf\n"
"v_mul_f32_dpp v83, v206, v83 row_newbcast:3 row_mask:0xf bank_mask:0xf\n"
"v_mfma_f32_16x16x32_bf16 v[168:171], v[220:223], a[12:15], v[168:171]\n"
"s_waitcnt lgkmcnt(1)\n"
"ds_read_b64_tr_b16 a[250:251], v238 offset:5120\n"
"v_accvgpr_read_b32 v251, a227\n"
"v_mfma_f32_16x16x32_bf16 v[168:171], v[208:211], a[0:3], v[168:171]\n"
"v_fmac_f32_dpp v29, v252, v255 row_newbcast:1 row_mask:0xf bank_mask:0xf\n"
"v_accvgpr_read_b32 v255, a225\n"
"v_mfma_f32_16x16x32_bf16 v[168:171], v[212:215], a[4:7], v[168:171]\n"
"ds_read_b64_tr_b16 v[226:227], v239 offset:5120\n"
"v_accvgpr_read_b32 v254, a224\n"
"v_fmac_f32_dpp v57, v252, v255 row_newbcast:1 row_mask:0xf bank_mask:0xf\n"
"v_fmac_f32_dpp v59, v252, v251 row_newbcast:3 row_mask:0xf bank_mask:0xf\n"
"v_cvt_pk_bf16_f32 v96, v152, v153\n"
"v_exp_f32 v161, v161\n"
"v_exp_f32 v163, v163\n"
"v_mfma_f32_16x16x32_bf16 v[172:175], v[208:211], a[16:19], v[184:187]\n"
"v_accvgpr_read_b32 v253, a226\n"
"v_accvgpr_read_b32 v255, a229\n"
"v_mfma_f32_16x16x32_bf16 v[172:175], v[212:215], a[20:23], v[172:175]\n"
"ds_read_b64_tr_b16 v[224:225], v239 offset:1024\n"
"v_fmac_f32_dpp v58, v252, v253 row_newbcast:2 row_mask:0xf bank_mask:0xf\n"
"v_mfma_f32_16x16x32_bf16 v[172:175], v[216:219], a[24:27], v[172:175]\n"
"s_add_u32 s40, s56, s53\n"
"v_fmac_f32_dpp v56, v252, v254 row_newbcast:0 row_mask:0xf bank_mask:0xf\n"
"v_accvgpr_read_b32 v251, a231\n"
"v_mfma_f32_16x16x32_bf16 v[172:175], v[220:223], a[28:31], v[172:175]\n"
"buffer_load_dwordx4 v240, s[20:23], s40 offen offset:2048 nt lds\n"
"v_cvt_pk_bf16_f32 v25, v26, v27\n"
"v_cvt_pk_bf16_f32 v26, v28, v29\n"
"v_cvt_pk_bf16_f32 v27, v30, v31\n"
"v_mul_f32_dpp v82, v206, v82 row_newbcast:2 row_mask:0xf bank_mask:0xf\n"
"v_mul_f32_dpp v62, v250, v62 row_newbcast:2 row_mask:0xf bank_mask:0xf\n"
"v_cvt_pk_bf16_f32 v56, v56, v57\n"
"v_exp_f32 v170, v170\n"
"v_mfma_f32_16x16x32_bf16 v[176:179], v[216:219], a[40:43], v[188:191]\n"
"v_accvgpr_read_b32 v253, a230\n"
"v_cvt_pk_bf16_f32 v113, v162, v163\n"
"v_mfma_f32_16x16x32_bf16 v[176:179], v[220:223], a[44:47], v[176:179]\n"
"v_accvgpr_read_b32 v254, a228\n"
"v_cvt_pk_bf16_f32 v57, v58, v59\n"
"v_mfma_f32_16x16x32_bf16 v[176:179], v[208:211], a[32:35], v[176:179]\n"
"ds_read_b64_tr_b16 a[254:255], v235 offset:5120\n"
"v_fmac_f32_dpp v63, v252, v251 row_newbcast:3 row_mask:0xf bank_mask:0xf\n"
"v_mfma_f32_16x16x32_bf16 v[176:179], v[212:215], a[36:39], v[176:179]\n"
"ds_write_b128 v243, v[24:27] offset:0\n"
"v_cvt_pk_bf16_f32 v80, v80, v81\n"
"v_fmac_f32_dpp v60, v252, v254 row_newbcast:0 row_mask:0xf bank_mask:0xf\n"
"v_fmac_f32_dpp v61, v252, v255 row_newbcast:1 row_mask:0xf bank_mask:0xf\n"
"v_fmac_f32_dpp v62, v252, v253 row_newbcast:2 row_mask:0xf bank_mask:0xf\n"
"v_cvt_pk_bf16_f32 v59, v62, v63\n"
"v_mfma_f32_16x16x32_bf16 v[152:155], v[208:211], a[192:195], v[192:195]\n"
"v_cvt_pk_bf16_f32 v48, v48, v49\n"
"v_cvt_pk_bf16_f32 v58, v60, v61\n"
"v_mfma_f32_16x16x32_bf16 v[152:155], v[212:215], a[196:199], v[152:155]\n"
"ds_write_b128 v243, v[56:59] offset:2048\n"
"v_exp_f32 v167, v167\n"
"v_exp_f32 v171, v171\n"
"v_mfma_f32_16x16x32_bf16 v[152:155], v[216:219], a[200:203], v[152:155]\n"
"v_mul_f32_dpp v86, v206, v86 row_newbcast:2 row_mask:0xf bank_mask:0xf\n"
"v_cvt_pk_bf16_f32 v112, v160, v161\n"
"v_mfma_f32_16x16x32_bf16 v[152:155], v[220:223], a[204:207], v[152:155]\n"
"s_waitcnt lgkmcnt(3)\n"
"ds_read_b64_tr_b16 a[252:253], v235 offset:1024\n"
"v_mul_f32_dpp v55, v247, v55 row_newbcast:3 row_mask:0xf bank_mask:0xf\n"
"v_mfma_f32_16x16x32_bf16 a[232:235], v[124:127], a[244:247], 0\n"
"ds_read_b128 v[208:211], v230 offset:16384\n"
"v_cvt_pk_bf16_f32 v81, v82, v83\n"
"v_mfma_f32_16x16x32_bf16 v[24:27], v[128:131], a[244:247], 0\n"
"s_add_u32 s40, s56, s54\n"
"v_exp_f32 v166, v166\n"
"v_mfma_f32_16x16x32_bf16 v[24:27], v[132:135], a[248:251], v[24:27]\n"
"buffer_load_dwordx4 v240, s[20:23], s40 offen offset:3072 nt lds\n"
"v_mfma_f32_16x16x32_bf16 v[56:59], v[136:139], a[244:247], 0\n"
"s_add_u32 s55, s55, s59\n"
"v_mul_f32_dpp v51, v247, v51 row_newbcast:3 row_mask:0xf bank_mask:0xf\n"
"v_cvt_pk_bf16_f32 v105, v158, v159\n"
"v_mfma_f32_16x16x32_bf16 v[56:59], v[140:143], a[248:251], v[56:59]\n"
"s_add_u32 s41, s55, s57\n"
"v_exp_f32 v168, v168\n"
"v_mfma_f32_16x16x32_bf16 v[88:91], v[144:147], a[244:247], 0\n"
"s_and_b32 s41, s41, s58\n"
"v_exp_f32 v172, v172\n"
"v_mfma_f32_16x16x32_bf16 v[88:91], v[148:151], a[248:251], v[88:91]\n"
"s_andn2_b32 s56, s55, s58\n"
"ds_read_b128 v[212:215], v231 offset:16384\n"
"v_cvt_pk_bf16_f32 v104, v156, v157\n"
"v_mfma_f32_16x16x32_bf16 v[92:95], v[148:151], v[224:227], 0\n"
"s_waitcnt lgkmcnt(2)\n"
"s_or_b32 s56, s56, s41\n"
"v_exp_f32 v169, v169\n"
"v_mfma_f32_16x16x32_bf16 v[92:95], v[144:147], a[252:255], v[92:95]\n"
"ds_read_b128 v[216:219], v230 offset:17408\n"
"v_cvt_pk_bf16_f32 v49, v50, v51\n"
"v_mfma_f32_16x16x32_bf16 v[60:63], v[140:143], v[224:227], 0\n"
"s_add_u32 m0, s44, 49152\n"
"ds_read_b128 v[220:223], v231 offset:17408\n"
"v_mul_f32_dpp v21, v246, v21 row_newbcast:1 row_mask:0xf bank_mask:0xf\n"
"v_mfma_f32_16x16x32_bf16 v[60:63], v[136:139], a[252:255], v[60:63]\n"
"buffer_load_dwordx4 v240, s[16:19], s56 offen nt lds\n"
"v_mfma_f32_16x16x32_bf16 v[28:31], v[132:135], v[224:227], 0\n"
"v_xor_b32 v248, s75, v248\n"
"v_cvt_pk_bf16_f32 v121, v166, v167\n"
"v_mfma_f32_16x16x32_bf16 v[28:31], v[128:131], a[252:255], v[28:31]\n"
"v_cvt_pk_bf16_f32 v17, v18, v19\n"
"v_cvt_pk_bf16_f32 v120, v164, v165\n"
"v_mfma_f32_16x16x32_bf16 a[236:239], v[124:127], a[252:255], 0\n"
"ds_read_b64_tr_b16 a[248:249], v236 offset:0\n"
"v_exp_f32 v173, v173\n"
"v_exp_f32 v174, v174\n"
"v_exp_f32 v178, v178\n"
"v_smfmac_f32_16x16x64_bf16 v[198:201], a[240:243], v[136:143], v241\n"
"s_waitcnt lgkmcnt(3)\n"
"ds_read_b64_tr_b16 v[224:225], v237 offset:0\n"
"v_mul_f32_dpp v23, v246, v23 row_newbcast:3 row_mask:0xf bank_mask:0xf\n"
"v_mfma_f32_16x16x32_bf16 v[156:159], v[208:211], a[0:3], v[180:183]\n"
"ds_read_b64_tr_b16 a[244:245], v232 offset:0\n"
"ds_read_b64_tr_b16 v[226:227], v237 offset:4096\n"
"v_mfma_f32_16x16x32_bf16 v[156:159], v[212:215], a[4:7], v[156:159]\n"
"s_waitcnt lgkmcnt(5)\n"
"v_cvt_pk_bf16_f32 v18, v20, v21\n"
"v_cvt_pk_bf16_f32 v106, v172, v173\n"
"v_mfma_f32_16x16x32_bf16 v[156:159], v[216:219], a[8:11], v[156:159]\n"
"s_waitcnt lgkmcnt(4)\n"
"ds_read_b64_tr_b16 a[254:255], v233 offset:4096\n"
"v_cvt_pk_bf16_f32 v98, v168, v169\n"
"v_mfma_f32_16x16x32_bf16 v[156:159], v[220:223], a[12:15], v[156:159]\n"
"ds_read_b64_tr_b16 a[246:247], v232 offset:4096\n"
"v_mul_f32_dpp v52, v247, v52 row_newbcast:0 row_mask:0xf bank_mask:0xf\n"
"v_mfma_f32_16x16x32_bf16 v[160:163], v[216:219], a[24:27], v[184:187]\n"
"v_exp_f32 v155, v155\n"
"v_mfma_f32_16x16x32_bf16 v[160:163], v[220:223], a[28:31], v[160:163]\n"
"ds_read_b64_tr_b16 a[250:251], v236 offset:4096\n"
"v_cvt_pk_bf16_f32 v19, v22, v23\n"
"v_mfma_f32_16x16x32_bf16 v[160:163], v[208:211], a[16:19], v[160:163]\n"
"s_add_u32 s40, s56, s52\n"
"v_xor_b32 v249, s75, v249\n"
"v_mul_f32_dpp v87, v206, v87 row_newbcast:3 row_mask:0xf bank_mask:0xf\n"
"v_mfma_f32_16x16x32_bf16 v[160:163], v[212:215], a[20:23], v[160:163]\n"
"buffer_load_dwordx4 v240, s[16:19], s40 offen offset:1024 nt lds\n"
"v_mfma_f32_16x16x32_bf16 v[164:167], v[208:211], a[32:35], v[188:191]\n"
"v_exp_f32 v158, v158\n"
"v_mfma_f32_16x16x32_bf16 v[164:167], v[212:215], a[36:39], v[164:167]\n"
"v_cvt_pk_bf16_f32 v51, v54, v55\n"
"v_cvt_pk_bf16_f32 v99, v170, v171\n"
"v_mfma_f32_16x16x32_bf16 v[164:167], v[216:219], a[40:43], v[164:167]\n"
"ds_read_b64_tr_b16 a[252:253], v233 offset:0\n"
"v_cvt_pk_bf16_f32 v50, v52, v53\n"
"v_mfma_f32_16x16x32_bf16 v[164:167], v[220:223], a[44:47], v[164:167]\n"
"s_waitcnt lgkmcnt(2)\n"
"ds_write_b128 v248, v[16:19] offset:49152\n"
"v_exp_f32 v177, v177\n"
"v_exp_f32 v153, v153\n"
"v_mfma_f32_16x16x32_bf16 a[208:211], v[124:127], a[244:247], 0\n"
"ds_write_b128 v248, v[48:51] offset:50176\n"
"v_exp_f32 v154, v154\n"
"v_mfma_f32_16x16x32_bf16 v[0:3], v[128:131], a[244:247], 0\n"
"s_waitcnt lgkmcnt(3)\n"
"v_exp_f32 v179, v179\n"
"v_mfma_f32_16x16x32_bf16 v[0:3], v[132:135], a[248:251], v[0:3]\n"
"ds_read_b128 v[208:211], v230 offset:20480\n"
"v_cvt_pk_bf16_f32 v83, v86, v87\n"
"v_mfma_f32_16x16x32_bf16 v[32:35], v[136:139], a[244:247], 0\n"
"s_add_u32 s40, s56, s53\n"
"ds_read_b128 v[216:219], v230 offset:21504\n"
"v_cvt_pk_bf16_f32 v82, v84, v85\n"
"v_mfma_f32_16x16x32_bf16 v[32:35], v[140:143], a[248:251], v[32:35]\n"
"buffer_load_dwordx4 v240, s[16:19], s40 offen offset:2048 nt lds\n"
"v_mfma_f32_16x16x32_bf16 v[64:67], v[144:147], a[244:247], 0\n"
"v_exp_f32 v166, v166\n"
"v_mfma_f32_16x16x32_bf16 v[64:67], v[148:151], a[248:251], v[64:67]\n"
"v_exp_f32 v163, v163\n"
"v_mfma_f32_16x16x32_bf16 v[68:71], v[148:151], v[224:227], 0\n"
"s_waitcnt lgkmcnt(3)\n"
"ds_read_b64_tr_b16 a[58:59], v249 offset:49160\n"
"ds_read_b128 v[212:215], v231 offset:20480\n"
"v_mfma_f32_16x16x32_bf16 v[68:71], v[144:147], a[252:255], v[68:71]\n"
"v_exp_f32 v152, v152\n"
"v_mfma_f32_16x16x32_bf16 v[36:39], v[140:143], v[224:227], 0\n"
"v_exp_f32 v162, v162\n"
"v_mfma_f32_16x16x32_bf16 v[36:39], v[136:139], a[252:255], v[36:39]\n"
"ds_read_b128 v[220:223], v231 offset:21504\n"
"v_exp_f32 v175, v175\n"
"v_exp_f32 v176, v176\n"
"v_exp_f32 v164, v164\n"
"v_exp_f32 v165, v165\n"
"v_mfma_f32_16x16x32_bf16 v[4:7], v[132:135], v[224:227], 0\n"
"ds_read_b64_tr_b16 a[56:57], v249 offset:49152\n"
"ds_read_b64_tr_b16 a[248:249], v236 offset:1024\n"
"v_mfma_f32_16x16x32_bf16 v[4:7], v[128:131], a[252:255], v[4:7]\n"
"ds_read_b64_tr_b16 a[74:75], v249 offset:50184\n"
"v_cvt_pk_bf16_f32 v107, v174, v175\n"
"v_mfma_f32_16x16x32_bf16 a[212:215], v[124:127], a[252:255], 0\n"
"s_add_u32 s40, s56, s54\n"
"ds_write_b128 v248, v[80:83] offset:51200\n"
"v_exp_f32 v161, v161\n"
"v_smfmac_f32_16x16x64_bf16 v[200:203], a[240:243], v[144:151], v241\n"
"s_waitcnt lgkmcnt(7)\n"
"buffer_load_dwordx4 v240, s[16:19], s40 offen offset:3072 nt lds\n"
"v_mfma_f32_16x16x32_bf16 v[168:171], v[208:211], a[0:3], v[180:183]\n"
"s_waitcnt lgkmcnt(5)\n"
"v_cvt_pk_bf16_f32 v114, v176, v177\n"
"v_add_u32 v243, 0x1000, v243\n"
"v_mfma_f32_16x16x32_bf16 v[168:171], v[212:215], a[4:7], v[168:171]\n"
"ds_read_b64_tr_b16 a[88:89], v249 offset:51200\n"
"ds_read_b64_tr_b16 a[244:245], v232 offset:1024\n"
"v_mfma_f32_16x16x32_bf16 v[168:171], v[216:219], a[8:11], v[168:171]\n"
"s_waitcnt lgkmcnt(6)\n"
"ds_read_b64_tr_b16 a[72:73], v249 offset:50176\n"
"ds_read_b64_tr_b16 a[90:91], v249 offset:51208\n"
"v_mfma_f32_16x16x32_bf16 v[168:171], v[220:223], a[12:15], v[168:171]\n"
"v_exp_f32 v167, v167\n"
"v_mfma_f32_16x16x32_bf16 v[172:175], v[216:219], a[24:27], v[184:187]\n"
"ds_read_b64_tr_b16 a[246:247], v232 offset:5120\n"
"v_cvt_pk_bf16_f32 v117, v166, v167\n"
"v_mfma_f32_16x16x32_bf16 v[172:175], v[220:223], a[28:31], v[172:175]\n"
"ds_read_b64_tr_b16 a[250:251], v236 offset:5120\n"
"v_cvt_pk_bf16_f32 v115, v178, v179\n"
"v_mfma_f32_16x16x32_bf16 v[172:175], v[208:211], a[16:19], v[172:175]\n"
"ds_read_b64_tr_b16 a[254:255], v233 offset:5120\n"
"ds_read_b64_tr_b16 v[224:225], v237 offset:1024\n"
"v_mfma_f32_16x16x32_bf16 v[172:175], v[212:215], a[20:23], v[172:175]\n"
"s_cbranch_vccnz LPCT1_%=\nLPKT1_%=:\n"
"v_exp_f32 v160, v160\n"
"v_mfma_f32_16x16x32_bf16 v[176:179], v[208:211], a[32:35], v[188:191]\n"
"ds_read_b64_tr_b16 a[252:253], v233 offset:1024\n"
"v_cvt_pk_bf16_f32 v116, v164, v165\n"
"v_mfma_f32_16x16x32_bf16 v[176:179], v[212:215], a[36:39], v[176:179]\n"
"v_exp_f32 v159, v159\n"
"v_mfma_f32_16x16x32_bf16 v[176:179], v[216:219], a[40:43], v[176:179]\n"
"v_exp_f32 v157, v157\n"
"v_mfma_f32_16x16x32_bf16 v[176:179], v[220:223], a[44:47], v[176:179]\n"
"s_waitcnt lgkmcnt(4)\n"
"v_cvt_pk_bf16_f32 v101, v158, v159\n"
"v_mfma_f32_16x16x32_bf16 a[224:227], v[124:127], a[244:247], 0\n"
"ds_read_b64_tr_b16 v[226:227], v237 offset:5120\n"
"v_cvt_pk_bf16_f32 v122, v152, v153\n"
"v_mfma_f32_16x16x32_bf16 v[16:19], v[128:131], a[244:247], 0\n"
"s_waitcnt vmcnt(8) lgkmcnt(4)\n"
"v_exp_f32 v156, v156\n"
"v_mfma_f32_16x16x32_bf16 v[16:19], v[132:135], a[248:251], v[16:19]\n"
"s_barrier\n"
"ds_read_b128 v[208:211], v228 offset:32768\n"
"v_exp_f32 v175, v175\n"
"v_mfma_f32_16x16x32_bf16 v[48:51], v[136:139], a[244:247], 0\n"
"ds_read_b128 v[220:223], v229 offset:33792\n"
"v_cvt_pk_bf16_f32 v100, v156, v157\n"
"v_mfma_f32_16x16x32_bf16 v[48:51], v[140:143], a[248:251], v[48:51]\n"
"ds_read_b128 v[212:215], v229 offset:32768\n"
"ds_read_b128 v[216:219], v228 offset:33792\n"
"v_mfma_f32_16x16x32_bf16 v[80:83], v[144:147], a[244:247], 0\n"
"ds_read_b64_tr_b16 a[244:245], v234 offset:16384\n"
"ds_read_b64_tr_b16 a[246:247], v234 offset:20480\n"
"v_mfma_f32_16x16x32_bf16 v[80:83], v[148:151], a[248:251], v[80:83]\n"
"s_waitcnt lgkmcnt(6)\n"
"ds_read_b64_tr_b16 a[248:249], v238 offset:16384\n"
"ds_read_b64_tr_b16 a[250:251], v238 offset:20480\n"
"v_mfma_f32_16x16x32_bf16 v[84:87], v[148:151], v[224:227], 0\n"
"v_xor_b32 v248, s75, v248\n"
"v_cvt_pk_bf16_f32 v108, v160, v161\n"
"v_mfma_f32_16x16x32_bf16 v[84:87], v[144:147], a[252:255], v[84:87]\n"
"v_exp_f32 v173, v173\n"
"v_mfma_f32_16x16x32_bf16 v[52:55], v[140:143], v[224:227], 0\n"
"v_exp_f32 v172, v172\n"
"v_mfma_f32_16x16x32_bf16 v[52:55], v[136:139], a[252:255], v[52:55]\n"
"v_cvt_pk_bf16_f32 v123, v154, v155\n"
"v_cvt_pk_bf16_f32 v109, v162, v163\n"
"v_mfma_f32_16x16x32_bf16 v[20:23], v[132:135], v[224:227], 0\n"
"ds_read_b64_tr_b16 v[224:225], v239 offset:16384\n"
"v_xor_b32 v249, s75, v249\n"
"v_mfma_f32_16x16x32_bf16 v[20:23], v[128:131], a[252:255], v[20:23]\n"
"v_exp_f32 v169, v169\n"
"v_mfma_f32_16x16x32_bf16 a[228:231], v[124:127], a[252:255], 0\n"
"s_waitcnt lgkmcnt(3)\n"
"ds_read_b64_tr_b16 a[254:255], v235 offset:20480\n"
"s_branch LAY2_%=\n"
"LAX_%=:\n"
"v_mfma_f32_16x16x32_bf16 a[224:227], v[124:127], a[244:247], a[224:227]\n"
"v_exp_f32 v169, v169\n"
"v_mfma_f32_16x16x32_bf16 v[16:19], v[128:131], a[244:247], v[16:19]\n"
"s_waitcnt vmcnt(8)\n"
"v_exp_f32 v171, v171\n"
"v_mfma_f32_16x16x32_bf16 v[16:19], v[132:135], a[248:251], v[16:19]\n"
"s_barrier\n"
"v_mfma_f32_16x16x32_bf16 v[48:51], v[136:139], a[244:247], v[48:51]\n"
"v_cvt_pk_bf16_f32 v108, v160, v161\n"
"v_cvt_pk_bf16_f32 v109, v162, v163\n"
"v_mfma_f32_16x16x32_bf16 v[48:51], v[140:143], a[248:251], v[48:51]\n"
"v_exp_f32 v168, v168\n"
"v_exp_f32 v170, v170\n"
"v_exp_f32 v177, v177\n"
"v_exp_f32 v179, v179\n"
"v_and_b32 v252, 0x7fff0000, v252\n"
"v_mfma_f32_16x16x32_bf16 v[80:83], v[144:147], a[244:247], v[80:83]\n"
"ds_read_b64_tr_b16 a[246:247], v234 offset:53248\n"
"v_cvt_pk_bf16_f32 v100, v156, v157\n"
"v_mfma_f32_16x16x32_bf16 v[80:83], v[148:151], a[248:251], v[80:83]\n"
"ds_read_b64_tr_b16 a[244:245], v234 offset:49152\n"
"v_accvgpr_read_b32 v253, a96\n"
"v_mfma_f32_16x16x32_bf16 v[84:87], v[148:151], v[224:227], v[84:87]\n"
"ds_read_b64_tr_b16 a[248:249], v238 offset:49152\n"
"v_cvt_pk_bf16_f32 v102, v168, v169\n"
"v_mfma_f32_16x16x32_bf16 v[84:87], v[144:147], a[252:255], v[84:87]\n"
"v_cvt_pk_bf16_f32 v116, v164, v165\n"
"v_cvt_pk_bf16_f32 v103, v170, v171\n"
"v_mfma_f32_16x16x32_bf16 v[52:55], v[140:143], v[224:227], v[52:55]\n"
"ds_read_b64_tr_b16 a[250:251], v238 offset:53248\n"
"v_lshlrev_b32_e64 v244, 24, s43\n"
"v_mfma_f32_16x16x32_bf16 v[52:55], v[136:139], a[252:255], v[52:55]\n"
"v_exp_f32 v172, v172\n"
"v_mfma_f32_16x16x32_bf16 v[20:23], v[132:135], v[224:227], v[20:23]\n"
"v_exp_f32 v176, v176\n"
"v_mfma_f32_16x16x32_bf16 v[20:23], v[128:131], a[252:255], v[20:23]\n"
"v_exp_f32 v174, v174\n"
"v_mfma_f32_16x16x32_bf16 a[228:231], v[124:127], a[252:255], a[228:231]\n"
"s_waitcnt lgkmcnt(2)\n"
"ds_read_b64_tr_b16 a[254:255], v235 offset:53248\n"
"v_cvt_pk_bf16_f32 v117, v166, v167\n"
"v_exp_f32 v173, v173\n"
"v_exp_f32 v175, v175\n"
"v_exp_f32 v178, v178\n"
"v_and_b32 v245, 0x3f0, v242\n"
"v_mfma_f32_16x16x32_bf16 a[216:219], v[120:123], a[244:247], a[216:219]\n"
"s_mov_b64 s[28:29], s[8:9]\n"
"v_cvt_pk_bf16_f32 v118, v176, v177\n"
"v_max_u32_dpp v252, v252, v252 quad_perm:[1,0,3,2] row_mask:0xf bank_mask:0xf\n"
"v_smfmac_f32_16x16x64_bf16 v[196:199], a[240:243], v[96:103], v241\n"
"s_mov_b32 s30, 655360\n"
"ds_read_b64_tr_b16 v[224:225], v239 offset:49152\n"
"v_cvt_pk_bf16_f32 v119, v178, v179\n"
"v_mfma_f32_16x16x32_bf16 v[8:11], v[96:99], a[244:247], v[8:11]\n"
"s_waitcnt lgkmcnt(2)\n"
"s_mov_b32 s71, s44\n"
"ds_read_b64_tr_b16 v[226:227], v239 offset:53248\n"
"v_cvt_pk_bf16_f32 v111, v174, v175\n"
"v_mfma_f32_16x16x32_bf16 v[8:11], v[100:103], a[248:251], v[8:11]\n"
"s_mov_b32 s70, s95\n"
"v_cvt_pk_bf16_f32 v110, v172, v173\n"
"v_max_u32_dpp v252, v252, v252 quad_perm:[2,3,0,1] row_mask:0xf bank_mask:0xf\n"
"v_smfmac_f32_16x16x64_bf16 v[200:203], a[240:243], v[112:119], v241\n"
"ds_read_b64_tr_b16 a[252:253], v235 offset:49152\n"
"v_lshrrev_b32 v245, 1, v245\n"
"v_mfma_f32_16x16x32_bf16 v[40:43], v[104:107], a[244:247], v[40:43]\n"
"v_and_b32 v253, 0x7fff0000, v253\n"
"v_mul_f32 v244, 0x45800000, v244\n"
"v_mfma_f32_16x16x32_bf16 v[40:43], v[108:111], a[248:251], v[40:43]\n"
"s_add_u32 s41, s70, 327680\n"
"s_add_u32 m0, s71, 32768\n"
"v_max_u32_dpp v253, v253, v253 quad_perm:[1,0,3,2] row_mask:0xf bank_mask:0xf\n"
"v_smfmac_f32_16x16x64_bf16 v[198:201], a[240:243], v[104:111], v241\n"
"buffer_load_dwordx4 v240, s[28:31], s41 offen lds\n"
"v_mfma_f32_16x16x32_bf16 v[72:75], v[112:115], a[244:247], v[72:75]\n"
"v_max_u32_dpp v253, v253, v253 quad_perm:[2,3,0,1] row_mask:0xf bank_mask:0xf\n"
"v_max_u32_dpp v252, v252, v252 row_half_mirror row_mask:0xf bank_mask:0xf\n"
"v_mfma_f32_16x16x32_bf16 v[72:75], v[116:119], a[248:251], v[72:75]\n"
"ds_read_b64_tr_b16 a[246:247], v234 offset:54272\n"
"ds_read_b64_tr_b16 a[248:249], v238 offset:50176\n"
"v_mfma_f32_16x16x32_bf16 v[202:205], a[240:243], v[120:123], v[202:205]\n"
"s_waitcnt lgkmcnt(3)\n"
"ds_read_b64_tr_b16 a[250:251], v238 offset:54272\n"
"v_add_f32 v196, v196, v197\n"
"v_mfma_f32_16x16x32_bf16 v[76:79], v[116:119], v[224:227], v[76:79]\n"
"s_waitcnt lgkmcnt(3)\n"
"s_add_u32 s40, s41, s52\n"
"ds_read_b64_tr_b16 a[244:245], v234 offset:50176\n"
"v_add_f32 v200, v200, v201\n"
"v_mfma_f32_16x16x32_bf16 v[76:79], v[112:115], a[252:255], v[76:79]\n"
"buffer_load_dwordx4 v240, s[28:31], s40 offen offset:1024 lds\n"
"v_mfma_f32_16x16x32_bf16 v[44:47], v[108:111], v[224:227], v[44:47]\n"
"v_min_u32_dpp v252, v252, v252 row_mirror row_mask:0xf bank_mask:0xf\n"
"v_add_f32 v244, v244, v202\n"
"v_mfma_f32_16x16x32_bf16 v[44:47], v[104:107], a[252:255], v[44:47]\n"
"v_add_f32 v198, v198, v199\n"
"v_min3_f32 v244, v244, v196, v198\n"
"v_mfma_f32_16x16x32_bf16 v[12:15], v[100:103], v[224:227], v[12:15]\n"
"s_add_u32 s40, s41, s53\n"
"ds_read_b64_tr_b16 v[226:227], v239 offset:54272\n"
"v_min_f32 v244, v244, v200\n"
"v_mfma_f32_16x16x32_bf16 v[12:15], v[96:99], a[252:255], v[12:15]\n"
"buffer_load_dwordx4 v240, s[28:31], s40 offen offset:2048 lds\n"
"v_max_u32_dpp v253, v253, v253 row_half_mirror row_mask:0xf bank_mask:0xf\n"
"v_add_f32 v254, v196, v198\n"
"v_add_f32 v254, v254, v200\n"
"v_add_f32 v254, v254, v202\n"
"v_rcp_f32 v244, v244\n"
"v_mfma_f32_16x16x32_bf16 a[220:223], v[120:123], a[252:255], a[220:223]\n"
"s_waitcnt lgkmcnt(1)\n"
"v_min_u32_dpp v253, v253, v253 row_mirror row_mask:0xf bank_mask:0xf\n"
"v_fmac_f32 v254, 0x68800000, v244\n"
"v_cmp_nge_f32 vcc, s45, v254\n"
"v_sub_u32 v245, v242, v245\n"
"v_bfe_u32 v254, v242, 9, 1\n"
"s_cbranch_vccnz LSLOW_%=\nLBACK3_%=:\ns_mov_b64 vcc, s[64:65]\n"
"v_mfma_f32_16x16x32_bf16 a[232:235], v[120:123], a[244:247], a[232:235]\n"
"ds_read_b64_tr_b16 a[252:253], v235 offset:50176\n"
"ds_read_b64_tr_b16 a[254:255], v235 offset:54272\n"
"ds_read_b64_tr_b16 v[224:225], v239 offset:50176\n"
"v_rcp_f32 v246, v196\n"
"v_rcp_f32 v247, v198\n"
"v_xor_b32 v244, 0x11000, v242\n"
"v_lshl_add_u32 v254, v254, 4, s43\n"
"v_mfma_f32_16x16x32_bf16 v[24:27], v[96:99], a[244:247], v[24:27]\n"
"v_rcp_f32 v206, v200\n"
"v_mfma_f32_16x16x32_bf16 v[24:27], v[100:103], a[248:251], v[24:27]\n"
"ds_write_b128 v244, a[220:223] offset:33792\n"
"v_min_u32_dpp v253, v253, v253 row_bcast:15 row_mask:0xa bank_mask:0xf\n"
"v_mov_b32_dpp v246, v246 row_ror:12 row_mask:0x2 bank_mask:0xf\n"
"v_mfma_f32_16x16x32_bf16 v[56:59], v[104:107], a[244:247], v[56:59]\n"
"s_add_u32 s40, s41, s54\n"
"v_mov_b32_dpp v247, v247 row_ror:12 row_mask:0x2 bank_mask:0xf\n"
"v_mov_b32_dpp v206, v206 row_ror:12 row_mask:0x2 bank_mask:0xf\n"
"v_mfma_f32_16x16x32_bf16 v[56:59], v[108:111], a[248:251], v[56:59]\n"
"buffer_load_dwordx4 v240, s[28:31], s40 offen offset:3072 lds\n"
"v_mfma_f32_16x16x32_bf16 v[88:91], v[112:115], a[244:247], v[88:91]\n"
"s_add_u32 s71, s44, s75\n"
"v_mov_b32_dpp v247, v247 row_ror:8 row_mask:0x4 bank_mask:0xf\n"
"v_mov_b32_dpp v206, v206 row_ror:8 row_mask:0x4 bank_mask:0xf\n"
"v_mfma_f32_16x16x32_bf16 v[88:91], v[116:119], a[248:251], v[88:91]\n"
"s_waitcnt lgkmcnt(1)\n"
"s_mov_b32 s70, s100\n"
"ds_read_b64_tr_b16 a[246:247], v232 offset:53248\n"
"ds_read_b64_tr_b16 a[248:249], v236 offset:49152\n"
"ds_read_b64_tr_b16 a[250:251], v236 offset:53248\n"
"v_mov_b32_dpp v246, v246 row_ror:8 row_mask:0x4 bank_mask:0xf\n"
"v_mov_b32_dpp v247, v247 row_ror:4 row_mask:0x8 bank_mask:0xf\n"
"v_mov_b32_dpp v206, v206 row_ror:4 row_mask:0x8 bank_mask:0xf\n"
"v_mfma_f32_16x16x32_bf16 v[92:95], v[116:119], v[224:227], v[92:95]\n"
"s_add_u32 s41, s70, 163840\n"
"s_add_u32 m0, s71, 16384\n"
"ds_read_b64_tr_b16 a[244:245], v232 offset:49152\n"
"v_mfma_f32_16x16x32_bf16 v[92:95], v[112:115], a[252:255], v[92:95]\n"
"buffer_load_dwordx4 v240, s[28:31], s41 offen lds\n"
"v_mov_b32_dpp v246, v246 row_ror:4 row_mask:0x8 bank_mask:0xf\n"
"v_mul_f32_dpp v41, v247, v41 row_newbcast:1 row_mask:0xf bank_mask:0xf\n"
"v_mul_f32_dpp v72, v206, v72 row_newbcast:0 row_mask:0xf bank_mask:0xf\n"
"v_mul_f32_dpp v77, v206, v77 row_newbcast:1 row_mask:0xf bank_mask:0xf\n"
"v_mfma_f32_16x16x32_bf16 v[60:63], v[108:111], v[224:227], v[60:63]\n"
"v_min_u32_dpp v252, v252, v252 row_bcast:15 row_mask:0xa bank_mask:0xf\n"
"v_mul_f32_dpp v76, v206, v76 row_newbcast:0 row_mask:0xf bank_mask:0xf\n"
"v_mfma_f32_16x16x32_bf16 v[60:63], v[104:107], a[252:255], v[60:63]\n"
"ds_write_b128 v244, a[232:235] offset:34816\n"
"v_min_u32_dpp v252, v252, v252 row_bcast:31 row_mask:0xc bank_mask:0xf\n"
"v_mul_f32 v252, 0x43340000, v252\n"
"v_lshl_add_u32 v254, v254, 6, s75\n"
"v_mul_f32_dpp v40, v247, v40 row_newbcast:0 row_mask:0xf bank_mask:0xf\n"
"v_mul_f32_dpp v24, v246, v24 row_newbcast:0 row_mask:0xf bank_mask:0xf\n"
"v_mfma_f32_16x16x32_bf16 v[28:31], v[100:103], v[224:227], v[28:31]\n"
"s_add_u32 s40, s41, s52\n"
"v_mul_f32_dpp v15, v246, v15 row_newbcast:3 row_mask:0xf bank_mask:0xf\n"
"v_mul_f32_dpp v73, v206, v73 row_newbcast:1 row_mask:0xf bank_mask:0xf\n"
"v_mfma_f32_16x16x32_bf16 v[28:31], v[96:99], a[252:255], v[28:31]\n"
"buffer_load_dwordx4 v240, s[28:31], s40 offen offset:1024 lds\n"
"v_min_u32_dpp v253, v253, v253 row_bcast:31 row_mask:0xc bank_mask:0xf\n"
"v_sub_u32 v229, v233, v254\n"
"v_mul_f32_dpp v9, v246, v9 row_newbcast:1 row_mask:0xf bank_mask:0xf\n"
"v_mul_f32_dpp v10, v246, v10 row_newbcast:2 row_mask:0xf bank_mask:0xf\n"
"v_mul_f32_dpp v12, v246, v12 row_newbcast:0 row_mask:0xf bank_mask:0xf\n"
"v_mfma_f32_16x16x32_bf16 a[236:239], v[120:123], a[252:255], a[236:239]\n"
"s_waitcnt lgkmcnt(1)\n"
"v_mul_f32_dpp v47, v247, v47 row_newbcast:3 row_mask:0xf bank_mask:0xf\n"
"v_mul_f32_dpp v90, v206, v90 row_newbcast:2 row_mask:0xf bank_mask:0xf\n"
"v_mfma_f32_16x16x32_bf16 a[208:211], v[120:123], a[244:247], a[208:211]\n"
"ds_write_b128 v244, a[216:219] offset:32768\n"
"v_sub_u32 v230, v234, v254\n"
"v_mul_f32_dpp v43, v247, v43 row_newbcast:3 row_mask:0xf bank_mask:0xf\n"
"v_mul_f32_dpp v45, v247, v45 row_newbcast:1 row_mask:0xf bank_mask:0xf\n"
"v_mul_f32_dpp v79, v206, v79 row_newbcast:3 row_mask:0xf bank_mask:0xf\n"
"v_cvt_pk_bf16_f32 v72, v72, v73\n"
"v_mfma_f32_16x16x32_bf16 v[0:3], v[96:99], a[244:247], v[0:3]\n"
"ds_read_b64_tr_b16 a[252:253], v233 offset:49152\n"
"v_mul_f32 v252, v252, v253\n"
"v_mfma_f32_16x16x32_bf16 v[0:3], v[100:103], a[248:251], v[0:3]\n"
"ds_read_b64_tr_b16 a[254:255], v233 offset:53248\n"
"ds_read_b64_tr_b16 v[224:225], v237 offset:49152\n"
"ds_read_b64_tr_b16 v[226:227], v237 offset:53248\n"
"v_sub_u32 v228, v232, v254\n"
"v_mul_f32_dpp v8, v246, v8 row_newbcast:0 row_mask:0xf bank_mask:0xf\n"
"v_mul_f32_dpp v44, v247, v44 row_newbcast:0 row_mask:0xf bank_mask:0xf\n"
"v_mul_f32_dpp v46, v247, v46 row_newbcast:2 row_mask:0xf bank_mask:0xf\n"
"v_mul_f32_dpp v27, v246, v27 row_newbcast:3 row_mask:0xf bank_mask:0xf\n"
"v_mul_f32_dpp v61, v247, v61 row_newbcast:1 row_mask:0xf bank_mask:0xf\n"
"v_mfma_f32_16x16x32_bf16 v[32:35], v[104:107], a[244:247], v[32:35]\n"
"s_add_u32 s40, s41, s53\n"
"v_mul_f32_dpp v14, v246, v14 row_newbcast:2 row_mask:0xf bank_mask:0xf\n"
"v_mul_f32_dpp v56, v247, v56 row_newbcast:0 row_mask:0xf bank_mask:0xf\n"
"v_mfma_f32_16x16x32_bf16 v[32:35], v[108:111], a[248:251], v[32:35]\n"
"buffer_load_dwordx4 v240, s[28:31], s40 offen offset:2048 lds\n"
"v_mul_f32_dpp v42, v247, v42 row_newbcast:2 row_mask:0xf bank_mask:0xf\n"
"v_mul_f32_dpp v75, v206, v75 row_newbcast:3 row_mask:0xf bank_mask:0xf\n"
"v_mul_f32_dpp v78, v206, v78 row_newbcast:2 row_mask:0xf bank_mask:0xf\n"
"v_mul_f32_dpp v57, v247, v57 row_newbcast:1 row_mask:0xf bank_mask:0xf\n"
"v_mul_f32_dpp v58, v247, v58 row_newbcast:2 row_mask:0xf bank_mask:0xf\n"
"v_cvt_pk_bf16_f32 v56, v56, v57\n"
"v_mfma_f32_16x16x32_bf16 v[64:67], v[112:115], a[244:247], v[64:67]\n"
"v_mul_f32_dpp v74, v206, v74 row_newbcast:2 row_mask:0xf bank_mask:0xf\n"
"v_mul_f32_dpp v28, v246, v28 row_newbcast:0 row_mask:0xf bank_mask:0xf\n"
"v_mfma_f32_16x16x32_bf16 v[64:67], v[116:119], a[248:251], v[64:67]\n"
"s_waitcnt lgkmcnt(0)\n"
"ds_write_b128 v244, a[236:239] offset:35840\n"
"v_min_f32 v252, 0x71800000, v252\n"
"v_cvt_pk_bf16_f32 v73, v74, v75\n"
"v_mul_f32_dpp v25, v246, v25 row_newbcast:1 row_mask:0xf bank_mask:0xf\n"
"v_mul_f32_dpp v63, v247, v63 row_newbcast:3 row_mask:0xf bank_mask:0xf\n"
"v_mfma_f32_16x16x32_bf16 v[68:71], v[116:119], v[224:227], v[68:71]\n"
"s_add_u32 s40, s41, s54\n"
"ds_read_b64_tr_b16 a[246:247], v232 offset:54272\n"
"v_mul_f32_dpp v60, v247, v60 row_newbcast:0 row_mask:0xf bank_mask:0xf\n"
"v_mfma_f32_16x16x32_bf16 v[68:71], v[112:115], a[252:255], v[68:71]\n"
"buffer_load_dwordx4 v240, s[28:31], s40 offen offset:3072 lds\n"
"v_readlane_b32 s22, v252, 63\n"
"v_xor_b32 v250, 0x1000, v245\n"
"v_mul_f32_dpp v11, v246, v11 row_newbcast:3 row_mask:0xf bank_mask:0xf\n"
"v_cvt_pk_bf16_f32 v8, v8, v9\n"
"v_cvt_pk_bf16_f32 v40, v40, v41\n"
"v_mul_f32_dpp v29, v246, v29 row_newbcast:1 row_mask:0xf bank_mask:0xf\n"
"v_mul_f32_dpp v62, v247, v62 row_newbcast:2 row_mask:0xf bank_mask:0xf\n"
"v_mul_f32_dpp v33, v247, v33 row_newbcast:1 row_mask:0xf bank_mask:0xf\n"
"v_mfma_f32_16x16x32_bf16 v[36:39], v[108:111], v[224:227], v[36:39]\n"
"v_readlane_b32 s23, v252, 63\n"
"v_mul_f32_dpp v67, v206, v67 row_newbcast:3 row_mask:0xf bank_mask:0xf\n"
"v_mfma_f32_16x16x32_bf16 v[36:39], v[104:107], a[252:255], v[36:39]\n"
"ds_read_b64_tr_b16 a[244:245], v232 offset:50176\n"
"ds_read_b64_tr_b16 a[248:249], v236 offset:50176\n"
"ds_read_b64_tr_b16 a[250:251], v236 offset:54272\n"
"v_mov_b32 v252, v192\n"
"v_mov_b32 v253, v202\n"
"v_sub_u32 v231, v235, v254\n"
"v_mul_f32_dpp v13, v246, v13 row_newbcast:1 row_mask:0xf bank_mask:0xf\n"
"v_mul_f32_dpp v59, v247, v59 row_newbcast:3 row_mask:0xf bank_mask:0xf\n"
"v_mul_f32_dpp v64, v206, v64 row_newbcast:0 row_mask:0xf bank_mask:0xf\n"
"v_xor_b32 v244, 0x1000, v244\n"
"v_mfma_f32_16x16x32_bf16 v[4:7], v[100:103], v[224:227], v[4:7]\n"
"ds_read_b64_tr_b16 v[224:225], v237 offset:50176\n"
"v_cvt_pk_bf16_f32 v9, v10, v11\n"
"v_mfma_f32_16x16x32_bf16 v[4:7], v[96:99], a[252:255], v[4:7]\n"
"ds_write_b64 v250, v[252:253] offset:52224\n"
"v_cvt_pk_bf16_f32 v10, v12, v13\n"
"v_cvt_pk_bf16_f32 v41, v42, v43\n"
"v_cvt_pk_bf16_f32 v43, v46, v47\n"
"v_cvt_pk_bf16_f32 v74, v76, v77\n"
"v_cvt_pk_bf16_f32 v57, v58, v59\n"
"v_mul_f32_dpp v92, v206, v92 row_newbcast:0 row_mask:0xf bank_mask:0xf\n"
"v_mul_f32_dpp v65, v206, v65 row_newbcast:1 row_mask:0xf bank_mask:0xf\n"
"v_mfma_f32_16x16x32_bf16 a[212:215], v[120:123], a[252:255], a[212:215]\n"
"s_waitcnt lgkmcnt(4)\n"
"ds_read_b64_tr_b16 a[252:253], v233 offset:50176\n"
"ds_read_b64_tr_b16 a[254:255], v233 offset:54272\n"
"ds_read_b64_tr_b16 v[226:227], v237 offset:54272\n"
"v_cvt_pk_bf16_f32 v42, v44, v45\n"
"v_cvt_pk_bf16_f32 v59, v62, v63\n"
"v_mul_f32_dpp v88, v206, v88 row_newbcast:0 row_mask:0xf bank_mask:0xf\n"
"v_mul_f32_dpp v94, v206, v94 row_newbcast:2 row_mask:0xf bank_mask:0xf\n"
"v_mul_f32_dpp v69, v206, v69 row_newbcast:1 row_mask:0xf bank_mask:0xf\n"
"v_mul_f32_dpp v71, v206, v71 row_newbcast:3 row_mask:0xf bank_mask:0xf\n"
"v_add_u32 v236, s76, v249\n"
"v_mfma_f32_16x16x32_bf16 a[224:227], v[120:123], a[244:247], a[224:227]\n"
"ds_write_b128 v248, v[40:43] offset:50176\n"
"v_cvt_pk_bf16_f32 v11, v14, v15\n"
"v_mul_f32_dpp v91, v206, v91 row_newbcast:3 row_mask:0xf bank_mask:0xf\n"
"v_mul_f32_dpp v32, v247, v32 row_newbcast:0 row_mask:0xf bank_mask:0xf\n"
"v_mul_f32_dpp v70, v206, v70 row_newbcast:2 row_mask:0xf bank_mask:0xf\n"
"v_mfma_f32_16x16x32_bf16 v[16:19], v[96:99], a[244:247], v[16:19]\n"
"s_waitcnt vmcnt(8) lgkmcnt(1)\n"
"ds_write_b128 v248, v[8:11] offset:49152\n"
"v_mul_f32_dpp v31, v246, v31 row_newbcast:3 row_mask:0xf bank_mask:0xf\n"
"v_mul_f32_dpp v35, v247, v35 row_newbcast:3 row_mask:0xf bank_mask:0xf\n"
"v_cvt_pk_bf16_f32 v64, v64, v65\n"
"v_xor_b32 v232, 0x1000, v228\n"
"v_xor_b32 v237, 0x400, v236\n"
"v_mfma_f32_16x16x32_bf16 v[16:19], v[100:103], a[248:251], v[16:19]\n"
"s_barrier\n"
"ds_read_b64 v[250:251], v245 offset:52224\n"
"ds_read_b64_tr_b16 v[184:185], v232 offset:0\n"
"ds_read_b64_tr_b16 v[192:193], v232 offset:8192\n"
"ds_read_b64_tr_b16 a[200:201], v236 offset:2048\n"
"v_cvt_pk_bf16_f32 v24, v24, v25\n"
"v_mul_f32_dpp v93, v206, v93 row_newbcast:1 row_mask:0xf bank_mask:0xf\n"
"v_mul_f32_dpp v0, v246, v0 row_newbcast:0 row_mask:0xf bank_mask:0xf\n"
"v_mul_f32_dpp v6, v246, v6 row_newbcast:2 row_mask:0xf bank_mask:0xf\n"
"v_mfma_f32_16x16x32_bf16 v[48:51], v[104:107], a[244:247], v[48:51]\n"
"ds_read_b64_tr_b16 a[4:5], v249 offset:49152\n"
"ds_read_b64_tr_b16 v[188:189], v228 offset:8192\n"
"v_mfma_f32_16x16x32_bf16 v[48:51], v[108:111], a[248:251], v[48:51]\n"
"ds_read_b64_tr_b16 a[6:7], v249 offset:49160\n"
"ds_read_b64_tr_b16 a[20:21], v249 offset:50176\n"
"ds_read_b64_tr_b16 a[22:23], v249 offset:50184\n"
"ds_read_b64_tr_b16 v[210:211], v228 offset:18432\n"
"v_cvt_pk_bf16_f32 v75, v78, v79\n"
"v_mul_f32_dpp v30, v246, v30 row_newbcast:2 row_mask:0xf bank_mask:0xf\n"
"v_mul_f32_dpp v89, v206, v89 row_newbcast:1 row_mask:0xf bank_mask:0xf\n"
"v_mul_f32_dpp v2, v246, v2 row_newbcast:2 row_mask:0xf bank_mask:0xf\n"
"v_mul_f32_dpp v38, v247, v38 row_newbcast:2 row_mask:0xf bank_mask:0xf\n"
"v_mfma_f32_16x16x32_bf16 v[80:83], v[112:115], a[244:247], v[80:83]\n"
"ds_read_b64_tr_b16 v[182:183], v228 offset:2048\n"
"ds_read_b64_tr_b16 a[242:243], v236 offset:4104\n"
"v_mfma_f32_16x16x32_bf16 v[80:83], v[116:119], a[248:251], v[80:83]\n"
"ds_write_b128 v248, v[72:75] offset:51200\n"
"v_mul_f32_dpp v26, v246, v26 row_newbcast:2 row_mask:0xf bank_mask:0xf\n"
"v_cvt_pk_bf16_f32 v88, v88, v89\n"
"v_mul_f32_dpp v39, v247, v39 row_newbcast:3 row_mask:0xf bank_mask:0xf\n"
"v_mov_b64 v[198:199], s[22:23]\n"
"v_mfma_f32_16x16x32_bf16 v[84:87], v[116:119], v[224:227], v[84:87]\n"
"ds_read_b64_tr_b16 v[180:181], v228 offset:0\n"
"ds_read_b64_tr_b16 v[190:191], v228 offset:10240\n"
"ds_read_b64_tr_b16 v[212:213], v232 offset:16384\n"
"ds_read_b64_tr_b16 v[220:221], v232 offset:24576\n"
"v_cvt_pk_bf16_f32 v25, v26, v27\n"
"v_cvt_pk_bf16_f32 v89, v90, v91\n"
"v_mul_f32_dpp v1, v246, v1 row_newbcast:1 row_mask:0xf bank_mask:0xf\n"
"v_mul_f32_dpp v4, v246, v4 row_newbcast:0 row_mask:0xf bank_mask:0xf\n"
"v_xor_b32 v234, 0x1000, v230\n"
"v_mul_f32_dpp v19, v246, v19 row_newbcast:3 row_mask:0xf bank_mask:0xf\n"
"v_mfma_f32_16x16x32_bf16 v[84:87], v[112:115], a[252:255], v[84:87]\n"
"s_waitcnt lgkmcnt(15)\n"
"ds_read_b64_tr_b16 a[38:39], v249 offset:51208\n"
"ds_read_b64_tr_b16 v[194:195], v232 offset:10240\n"
"ds_read_b64_tr_b16 v[208:209], v228 offset:16384\n"
"v_cvt_pk_bf16_f32 v26, v28, v29\n"
"v_cvt_pk_bf16_f32 v27, v30, v31\n"
"v_cvt_pk_bf16_f32 v58, v60, v61\n"
"v_cvt_pk_bf16_f32 v90, v92, v93\n"
"v_min_f32 v254, v252, v250\n"
"v_mov_b64 v[196:197], s[22:23]\n"
"v_mfma_f32_16x16x32_bf16 v[52:55], v[108:111], v[224:227], v[52:55]\n"
"ds_read_b64_tr_b16 v[186:187], v232 offset:2048\n"
"ds_read_b64_tr_b16 v[216:217], v228 offset:24576\n"
"ds_read_b64_tr_b16 v[222:223], v232 offset:26624\n"
"ds_read_b64_tr_b16 a[248:249], v236 offset:6144\n"
"v_mul_f32_dpp v95, v206, v95 row_newbcast:3 row_mask:0xf bank_mask:0xf\n"
"v_mul_f32_dpp v3, v246, v3 row_newbcast:3 row_mask:0xf bank_mask:0xf\n"
"v_mul_f32_dpp v37, v247, v37 row_newbcast:1 row_mask:0xf bank_mask:0xf\n"
"v_mul_f32_dpp v66, v206, v66 row_newbcast:2 row_mask:0xf bank_mask:0xf\n"
"v_xor_b32 v233, 0x1000, v229\n"
"v_mul_f32_dpp v48, v247, v48 row_newbcast:0 row_mask:0xf bank_mask:0xf\n"
"v_mfma_f32_16x16x32_bf16 v[52:55], v[104:107], a[252:255], v[52:55]\n"
"ds_read_b128 v[60:63], v244 offset:35840\n"
"ds_read_b64_tr_b16 v[214:215], v232 offset:18432\n"
"v_mfma_f32_16x16x32_bf16 v[20:23], v[100:103], v[224:227], v[20:23]\n"
"ds_write_b128 v248, v[24:27] offset:49152\n"
"v_mul_f32_dpp v7, v246, v7 row_newbcast:3 row_mask:0xf bank_mask:0xf\n"
"v_mul_f32_dpp v36, v247, v36 row_newbcast:0 row_mask:0xf bank_mask:0xf\n"
"v_cvt_pk_bf16_f32 v32, v32, v33\n"
"v_mul_f32_dpp v49, v247, v49 row_newbcast:1 row_mask:0xf bank_mask:0xf\n"
"v_mfma_f32_16x16x32_bf16 v[20:23], v[96:99], a[252:255], v[20:23]\n"
"ds_read_b64_tr_b16 a[36:37], v249 offset:51200\n"
"ds_read_b64_tr_b16 a[12:13], v249 offset:49152\n"
"ds_read_b64_tr_b16 a[206:207], v237 offset:2056\n"
"ds_read_b64_tr_b16 v[110:111], v233 offset:10240\n"
"v_cvt_pk_bf16_f32 v91, v94, v95\n"
"v_mul_f32_dpp v5, v246, v5 row_newbcast:1 row_mask:0xf bank_mask:0xf\n"
"v_mul_f32_dpp v34, v247, v34 row_newbcast:2 row_mask:0xf bank_mask:0xf\n"
"v_cvt_pk_bf16_f32 v65, v66, v67\n"
"v_cvt_pk_bf16_f32 v67, v70, v71\n"
"v_sub_f32 v250, v254, v250\n"
"v_mfma_f32_16x16x32_bf16 a[228:231], v[120:123], a[252:255], a[228:231]\n"
"s_waitcnt lgkmcnt(15)\n"
"ds_write_b128 v248, v[88:91] offset:51200\n"
"v_cvt_pk_bf16_f32 v0, v0, v1\n"
"v_cvt_pk_bf16_f32 v33, v34, v35\n"
"v_cvt_pk_bf16_f32 v35, v38, v39\n"
"v_mul_f32_dpp v68, v206, v68 row_newbcast:0 row_mask:0xf bank_mask:0xf\n"
"v_mul_f32_dpp v51, v247, v51 row_newbcast:3 row_mask:0xf bank_mask:0xf\n"
"v_mfma_f32_16x16x32_bf16 v[124:127], v[180:183], a[96:99], v[196:199]\n"
"s_waitcnt lgkmcnt(11)\n"
"ds_read_b64_tr_b16 a[44:45], v249 offset:51200\n"
"ds_read_b64_tr_b16 a[46:47], v249 offset:51208\n"
"v_mfma_f32_16x16x32_bf16 v[124:127], v[184:187], a[100:103], v[124:127]\n"
"ds_read_b64_tr_b16 v[218:219], v228 offset:26624\n"
"ds_read_b64_tr_b16 v[224:225], v233 offset:24576\n"
"v_mfma_f32_16x16x32_bf16 v[124:127], v[188:191], a[104:107], v[124:127]\n"
"ds_read_b64_tr_b16 a[14:15], v249 offset:49160\n"
"ds_read_b64_tr_b16 a[244:245], v237 offset:4096\n"
"v_mfma_f32_16x16x32_bf16 v[124:127], v[192:195], a[108:111], v[124:127]\n"
"ds_write_b128 v248, v[56:59] offset:50176\n"
"v_cvt_pk_bf16_f32 v1, v2, v3\n"
"v_cvt_pk_bf16_f32 v2, v4, v5\n"
"v_cvt_pk_bf16_f32 v34, v36, v37\n"
"v_sub_f32 v252, v254, v252\n"
"v_accvgpr_read_b32 v255, a209\n"
"v_mfma_f32_16x16x32_bf16 v[124:127], v[208:211], a[144:147], v[124:127]\n"
"s_waitcnt lgkmcnt(13)\n"
"ds_read_b64_tr_b16 a[28:29], v249 offset:50176\n"
"ds_read_b64_tr_b16 a[30:31], v249 offset:50184\n"
"v_mfma_f32_16x16x32_bf16 v[124:127], v[212:215], a[148:151], v[124:127]\n"
"s_waitcnt lgkmcnt(6)\n"
"ds_write_b128 v248, v[32:35] offset:50176\n"
"v_cvt_pk_bf16_f32 v3, v6, v7\n"
"v_cvt_pk_bf16_f32 v66, v68, v69\n"
"v_mul_f32_dpp v21, v246, v21 row_newbcast:1 row_mask:0xf bank_mask:0xf\n"
"v_mul_f32_dpp v84, v206, v84 row_newbcast:0 row_mask:0xf bank_mask:0xf\n"
"v_mfma_f32_16x16x32_bf16 v[124:127], v[216:219], a[152:155], v[124:127]\n"
"ds_read_b64_tr_b16 a[18:19], v249 offset:50184\n"
"ds_read_b128 v[56:59], v244 offset:34816\n"
"v_mfma_f32_16x16x32_bf16 v[124:127], v[220:223], a[156:159], v[124:127]\n"
"ds_write_b128 v248, v[0:3] offset:49152\n"
"ds_write_b128 v248, v[64:67] offset:51200\n"
"v_mfma_f32_16x16x32_bf16 v[140:143], v[220:223], a[172:175], v[196:199]\n"
"ds_read_b64_tr_b16 a[16:17], v249 offset:50176\n"
"ds_read_b64_tr_b16 a[32:33], v249 offset:51200\n"
"v_mfma_f32_16x16x32_bf16 v[140:143], v[216:219], a[168:171], v[140:143]\n"
"ds_read_b64_tr_b16 a[0:1], v249 offset:49152\n"
"ds_read_b64_tr_b16 v[112:113], v229 offset:16384\n"
"v_mfma_f32_16x16x32_bf16 v[140:143], v[212:215], a[164:167], v[140:143]\n"
"ds_read_b128 v[24:27], v244 offset:32768\n"
"ds_read_b128 v[28:31], v244 offset:33792\n"
"v_mfma_f32_16x16x32_bf16 v[140:143], v[208:211], a[160:163], v[140:143]\n"
"ds_read_b64_tr_b16 a[2:3], v249 offset:49160\n"
"ds_read_b64_tr_b16 a[34:35], v249 offset:51208\n"
"v_mfma_f32_16x16x32_bf16 v[140:143], v[192:195], a[124:127], v[140:143]\n"
"ds_read_b64_tr_b16 a[240:241], v236 offset:4096\n"
"v_mul_f32_dpp v52, v247, v52 row_newbcast:0 row_mask:0xf bank_mask:0xf\n"
"v_mfma_f32_16x16x32_bf16 v[140:143], v[188:191], a[120:123], v[140:143]\n"
"ds_read_b64_tr_b16 v[116:117], v233 offset:16384\n"
"ds_read_b64_tr_b16 a[250:251], v236 offset:6152\n"
"v_mfma_f32_16x16x32_bf16 v[140:143], v[184:187], a[116:119], v[140:143]\n"
"ds_read_b64_tr_b16 a[202:203], v236 offset:2056\n"
"ds_read_b64_tr_b16 a[254:255], v237 offset:6152\n"
"v_mfma_f32_16x16x32_bf16 v[140:143], v[180:183], a[112:115], v[140:143]\n"
"ds_read_b64_tr_b16 a[194:195], v236 offset:8\n"
"ds_read_b64_tr_b16 a[204:205], v237 offset:2048\n"
"ds_read_b64_tr_b16 a[252:253], v237 offset:6144\n"
"v_mfma_f32_16x16x32_bf16 v[156:159], v[180:183], a[128:131], v[196:199]\n"
"ds_read_b64_tr_b16 v[104:105], v229 offset:8192\n"
"ds_read_b64_tr_b16 a[246:247], v237 offset:4104\n"
"v_mfma_f32_16x16x32_bf16 v[156:159], v[184:187], a[132:135], v[156:159]\n"
"ds_read_b64_tr_b16 v[100:101], v233 offset:0\n"
"v_accvgpr_read_b32 v254, a208\n"
"v_mfma_f32_16x16x32_bf16 v[156:159], v[188:191], a[136:139], v[156:159]\n"
"ds_read_b64_tr_b16 a[198:199], v237 offset:8\n"
"ds_read_b64_tr_b16 v[98:99], v229 offset:2048\n"
"v_mfma_f32_16x16x32_bf16 v[156:159], v[192:195], a[140:143], v[156:159]\n"
"s_waitcnt lgkmcnt(13)\n"
"ds_read_b64_tr_b16 a[196:197], v237 offset:0\n"
"ds_read_b64_tr_b16 v[102:103], v233 offset:2048\n"
"v_mfma_f32_16x16x32_bf16 v[156:159], v[208:211], a[176:179], v[156:159]\n"
"ds_read_b64_tr_b16 v[120:121], v229 offset:24576\n"
"v_mul_f32_dpp v80, v206, v80 row_newbcast:0 row_mask:0xf bank_mask:0xf\n"
"v_mfma_f32_16x16x32_bf16 v[156:159], v[212:215], a[180:183], v[156:159]\n"
"s_add_u32 s41, s95, 491520\n"
"ds_read_b64_tr_b16 a[192:193], v236 offset:0\n"
"ds_read_b64_tr_b16 v[106:107], v229 offset:10240\n"
"v_mfma_f32_16x16x32_bf16 v[156:159], v[216:219], a[184:187], v[156:159]\n"
"s_add_u32 m0, s44, 49152\n"
"ds_read_b64_tr_b16 v[96:97], v229 offset:0\n"
"ds_read_b64_tr_b16 v[118:119], v233 offset:18432\n"
"v_mfma_f32_16x16x32_bf16 v[156:159], v[220:223], a[188:191], v[156:159]\n"
"s_waitcnt lgkmcnt(12)\n"
"buffer_load_dwordx4 v240, s[28:31], s41 offen lds\n"
"v_mfma_f32_16x16x32_bf16 v[172:175], v[220:223], a[252:255], v[196:199]\n"
"s_add_u32 s40, s41, s52\n"
"v_exp_f32 v252, v252\n"
"v_mfma_f32_16x16x32_bf16 v[172:175], v[216:219], a[248:251], v[172:175]\n"
"s_waitcnt lgkmcnt(9)\n"
"buffer_load_dwordx4 v240, s[28:31], s40 offen offset:1024 lds\n"
"v_mfma_f32_16x16x32_bf16 v[172:175], v[212:215], a[244:247], v[172:175]\n"
"s_add_u32 s40, s41, s53\n"
"ds_read_b64_tr_b16 v[108:109], v233 offset:8192\n"
"ds_read_b64_tr_b16 v[226:227], v233 offset:26624\n"
"v_mfma_f32_16x16x32_bf16 v[172:175], v[208:211], a[240:243], v[172:175]\n"
"buffer_load_dwordx4 v240, s[28:31], s40 offen offset:2048 lds\n"
"v_mfma_f32_16x16x32_bf16 v[172:175], v[192:195], a[204:207], v[172:175]\n"
"ds_read_b64_tr_b16 v[114:115], v229 offset:18432\n"
"v_mul_f32_dpp v55, v247, v55 row_newbcast:3 row_mask:0xf bank_mask:0xf\n"
"v_mfma_f32_16x16x32_bf16 v[172:175], v[188:191], a[200:203], v[172:175]\n"
"s_waitcnt lgkmcnt(5)\n"
"v_exp_f32 v250, v250\n"
"v_mfma_f32_16x16x32_bf16 v[172:175], v[184:187], a[196:199], v[172:175]\n"
"s_add_u32 s40, s41, s54\n"
"ds_read_b64_tr_b16 v[122:123], v229 offset:26624\n"
"ds_read_b64_tr_b16 v[184:185], v234 offset:0\n"
"v_mfma_f32_16x16x32_bf16 v[172:175], v[180:183], a[192:195], v[172:175]\n"
"s_waitcnt lgkmcnt(5)\n"
"buffer_load_dwordx4 v240, s[28:31], s40 offen offset:3072 lds\n"
"v_mfma_f32_16x16x32_bf16 v[128:131], v[96:99], a[96:99], v[196:199]\n"
"v_mul_f32 v253, v253, v252\n"
"v_fmac_f32 v253, v251, v250\n"
"v_mfma_f32_16x16x32_bf16 v[128:131], v[100:103], a[100:103], v[128:131]\n"
"v_mul_f32_dpp v17, v246, v17 row_newbcast:1 row_mask:0xf bank_mask:0xf\n"
"v_mul_f32_dpp v83, v206, v83 row_newbcast:3 row_mask:0xf bank_mask:0xf\n"
"v_mfma_f32_16x16x32_bf16 v[128:131], v[104:107], a[104:107], v[128:131]\n"
"s_waitcnt lgkmcnt(3)\n"
"ds_read_b64_tr_b16 a[234:235], v236 offset:10248\n"
"ds_read_b64_tr_b16 v[216:217], v230 offset:24576\n"
"v_mfma_f32_16x16x32_bf16 v[128:131], v[108:111], a[108:111], v[128:131]\n"
"s_waitcnt lgkmcnt(4)\n"
"ds_read_b64_tr_b16 v[208:209], v230 offset:16384\n"
"v_mul_f32_dpp v81, v206, v81 row_newbcast:1 row_mask:0xf bank_mask:0xf\n"
"v_mfma_f32_16x16x32_bf16 v[128:131], v[112:115], a[144:147], v[128:131]\n"
"s_add_u32 s21, s60, 32\n"
"ds_read_b64_tr_b16 v[188:189], v230 offset:8192\n"
"v_cvt_pk_bf16_f32 v80, v80, v81\n"
"v_mfma_f32_16x16x32_bf16 v[128:131], v[116:119], a[148:151], v[128:131]\n"
"s_waitcnt lgkmcnt(5)\n"
"s_mov_b32 s69, 1\n"
"ds_read_b64_tr_b16 a[238:239], v237 offset:10248\n"
"v_mul_f32_dpp v20, v246, v20 row_newbcast:0 row_mask:0xf bank_mask:0xf\n"
"v_mfma_f32_16x16x32_bf16 v[128:131], v[120:123], a[152:155], v[128:131]\n"
"v_bfe_u32 v201, v242, 2, 7\n"
"v_add_u32 v201, 0x100, v201\n"
"ds_read_b64_tr_b16 v[214:215], v234 offset:18432\n"
"v_mfma_f32_16x16x32_bf16 v[128:131], v[224:227], a[156:159], v[128:131]\n"
"global_load_dword v200, v201, s[6:7] sc1\n"
"v_mfma_f32_16x16x32_bf16 v[144:147], v[224:227], a[172:175], v[196:199]\n"
"v_rcp_f32 v253, v253\n"
"v_mfma_f32_16x16x32_bf16 v[144:147], v[120:123], a[168:171], v[144:147]\n"
"v_mul_f32 v252, v252, v253\n"
"v_mul_f32 v250, v250, v253\n"
"v_mfma_f32_16x16x32_bf16 v[144:147], v[116:119], a[164:167], v[144:147]\n"
"s_nop 0\n"
"v_mov_b32_dpp v250, v250 row_ror:12 row_mask:0x2 bank_mask:0xf\n"
"v_mov_b32_dpp v252, v252 row_ror:12 row_mask:0x2 bank_mask:0xf\n"
"v_mfma_f32_16x16x32_bf16 v[144:147], v[112:115], a[160:163], v[144:147]\n"
"v_mov_b32_dpp v250, v250 row_ror:8 row_mask:0x4 bank_mask:0xf\n"
"v_mov_b32_dpp v252, v252 row_ror:8 row_mask:0x4 bank_mask:0xf\n"
"v_mfma_f32_16x16x32_bf16 v[144:147], v[108:111], a[124:127], v[144:147]\n"
"ds_read_b64_tr_b16 v[222:223], v234 offset:26624\n"
"v_mov_b32_dpp v250, v250 row_ror:4 row_mask:0x8 bank_mask:0xf\n"
"v_mfma_f32_16x16x32_bf16 v[144:147], v[104:107], a[120:123], v[144:147]\n"
"ds_read_b64_tr_b16 a[232:233], v236 offset:10240\n"
"v_accvgpr_read_b32 v253, a210\n"
"v_mfma_f32_16x16x32_bf16 v[144:147], v[100:103], a[116:119], v[144:147]\n"
"ds_read_b64_tr_b16 v[182:183], v230 offset:2048\n"
"v_mul_f32_dpp v27, v250, v27 row_newbcast:3 row_mask:0xf bank_mask:0xf\n"
"v_mfma_f32_16x16x32_bf16 v[144:147], v[96:99], a[112:115], v[144:147]\n"
"ds_read_b64_tr_b16 v[194:195], v234 offset:10240\n"
"v_mov_b32_dpp v252, v252 row_ror:4 row_mask:0x8 bank_mask:0xf\n"
"v_mul_f32_dpp v24, v250, v24 row_newbcast:0 row_mask:0xf bank_mask:0xf\n"
"v_mul_f32_dpp v25, v250, v25 row_newbcast:1 row_mask:0xf bank_mask:0xf\n"
"v_mul_f32_dpp v26, v250, v26 row_newbcast:2 row_mask:0xf bank_mask:0xf\n"
"v_accvgpr_read_b32 v251, a211\n"
"v_mul_f32_dpp v28, v250, v28 row_newbcast:0 row_mask:0xf bank_mask:0xf\n"
"v_mul_f32_dpp v16, v246, v16 row_newbcast:0 row_mask:0xf bank_mask:0xf\n"
"v_mfma_f32_16x16x32_bf16 v[160:163], v[96:99], a[128:131], v[196:199]\n"
"ds_read_b64_tr_b16 v[210:211], v230 offset:18432\n"
"v_fmac_f32_dpp v24, v252, v254 row_newbcast:0 row_mask:0xf bank_mask:0xf\n"
"v_mfma_f32_16x16x32_bf16 v[160:163], v[100:103], a[132:135], v[160:163]\n"
"ds_read_b64_tr_b16 v[220:221], v234 offset:24576\n"
"v_fmac_f32_dpp v25, v252, v255 row_newbcast:1 row_mask:0xf bank_mask:0xf\n"
"v_mfma_f32_16x16x32_bf16 v[160:163], v[104:107], a[136:139], v[160:163]\n"
"ds_read_b64_tr_b16 v[186:187], v234 offset:2048\n"
"v_accvgpr_read_b32 v255, a213\n"
"v_mfma_f32_16x16x32_bf16 v[160:163], v[108:111], a[140:143], v[160:163]\n"
"v_xor_b32 v235, 0x1000, v231\n"
"v_mul_f32_dpp v57, v250, v57 row_newbcast:1 row_mask:0xf bank_mask:0xf\n"
"v_mfma_f32_16x16x32_bf16 v[160:163], v[112:115], a[176:179], v[160:163]\n"
"v_accvgpr_read_b32 v254, a212\n"
"v_mul_f32_dpp v56, v250, v56 row_newbcast:0 row_mask:0xf bank_mask:0xf\n"
"v_mfma_f32_16x16x32_bf16 v[160:163], v[116:119], a[180:183], v[160:163]\n"
"ds_read_b64_tr_b16 v[190:191], v230 offset:10240\n"
"ds_read_b64_tr_b16 v[192:193], v234 offset:8192\n"
"v_mfma_f32_16x16x32_bf16 v[160:163], v[120:123], a[184:187], v[160:163]\n"
"ds_read_b64_tr_b16 v[212:213], v234 offset:16384\n"
"v_fmac_f32_dpp v26, v252, v253 row_newbcast:2 row_mask:0xf bank_mask:0xf\n"
"v_mfma_f32_16x16x32_bf16 v[160:163], v[224:227], a[188:191], v[160:163]\n"
"ds_read_b64_tr_b16 a[236:237], v237 offset:10240\n"
"v_fmac_f32_dpp v27, v252, v251 row_newbcast:3 row_mask:0xf bank_mask:0xf\n"
"v_mul_f32_dpp v29, v250, v29 row_newbcast:1 row_mask:0xf bank_mask:0xf\n"
"v_accvgpr_read_b32 v253, a214\n"
"v_mul_f32_dpp v30, v250, v30 row_newbcast:2 row_mask:0xf bank_mask:0xf\n"
"v_accvgpr_read_b32 v251, a215\n"
"v_mul_f32_dpp v31, v250, v31 row_newbcast:3 row_mask:0xf bank_mask:0xf\n"
"v_fmac_f32_dpp v28, v252, v254 row_newbcast:0 row_mask:0xf bank_mask:0xf\n"
"v_mul_f32_dpp v59, v250, v59 row_newbcast:3 row_mask:0xf bank_mask:0xf\n"
"v_mfma_f32_16x16x32_bf16 v[176:179], v[224:227], a[252:255], v[196:199]\n"
"ds_read_b64_tr_b16 v[180:181], v230 offset:0\n"
"v_mul_f32_dpp v18, v246, v18 row_newbcast:2 row_mask:0xf bank_mask:0xf\n"
"v_mfma_f32_16x16x32_bf16 v[176:179], v[120:123], a[248:251], v[176:179]\n"
"ds_read_b64_tr_b16 v[224:225], v235 offset:24576\n"
"v_mov_b64 v[44:45], s[22:23]\n"
"v_mfma_f32_16x16x32_bf16 v[176:179], v[116:119], a[244:247], v[176:179]\n"
"v_cvt_pk_bf16_f32 v48, v48, v49\n"
"v_mul_f32_dpp v61, v250, v61 row_newbcast:1 row_mask:0xf bank_mask:0xf\n"
"v_mfma_f32_16x16x32_bf16 v[176:179], v[112:115], a[240:243], v[176:179]\n"
"ds_read_b64_tr_b16 a[220:221], v237 offset:8192\n"
"v_mul_f32_dpp v86, v206, v86 row_newbcast:2 row_mask:0xf bank_mask:0xf\n"
"v_mfma_f32_16x16x32_bf16 v[176:179], v[108:111], a[204:207], v[176:179]\n"
"v_fmac_f32_dpp v29, v252, v255 row_newbcast:1 row_mask:0xf bank_mask:0xf\n"
"v_accvgpr_read_b32 v254, a224\n"
"v_mfma_f32_16x16x32_bf16 v[176:179], v[104:107], a[200:203], v[176:179]\n"
"v_fmac_f32_dpp v31, v252, v251 row_newbcast:3 row_mask:0xf bank_mask:0xf\n"
"v_fmac_f32_dpp v56, v252, v254 row_newbcast:0 row_mask:0xf bank_mask:0xf\n"
"v_mfma_f32_16x16x32_bf16 v[176:179], v[100:103], a[196:199], v[176:179]\n"
"ds_read_b64_tr_b16 v[218:219], v230 offset:26624\n"
"v_mul_f32_dpp v58, v250, v58 row_newbcast:2 row_mask:0xf bank_mask:0xf\n"
"v_mfma_f32_16x16x32_bf16 v[176:179], v[96:99], a[192:195], v[176:179]\n"
"s_waitcnt lgkmcnt(3)\n"
"ds_read_b64_tr_b16 v[108:109], v235 offset:8192\n"
"v_fmac_f32_dpp v30, v252, v253 row_newbcast:2 row_mask:0xf bank_mask:0xf\n"
"v_cvt_pk_bf16_f32 v24, v24, v25\n"
"v_cvt_pk_bf16_f32 v25, v26, v27\n"
"v_mul_f32_dpp v50, v247, v50 row_newbcast:2 row_mask:0xf bank_mask:0xf\n"
"v_mul_f32_dpp v54, v247, v54 row_newbcast:2 row_mask:0xf bank_mask:0xf\n"
"v_accvgpr_read_b32 v255, a225\n"
"v_accvgpr_read_b32 v253, a226\n"
"v_mul_f32_dpp v60, v250, v60 row_newbcast:0 row_mask:0xf bank_mask:0xf\n"
"v_mfma_f32_16x16x32_bf16 v[132:135], v[180:183], a[96:99], v[196:199]\n"
"v_cvt_pk_bf16_f32 v27, v30, v31\n"
"v_accvgpr_read_b32 v251, a227\n"
"v_mfma_f32_16x16x32_bf16 v[132:135], v[184:187], a[100:103], v[132:135]\n"
"v_cvt_pk_bf16_f32 v26, v28, v29\n"
"v_fmac_f32_dpp v58, v252, v253 row_newbcast:2 row_mask:0xf bank_mask:0xf\n"
"v_mfma_f32_16x16x32_bf16 v[132:135], v[188:191], a[104:107], v[132:135]\n"
"ds_read_b64_tr_b16 v[100:101], v235 offset:0\n"
"v_accvgpr_read_b32 v254, a228\n"
"v_mfma_f32_16x16x32_bf16 v[132:135], v[192:195], a[108:111], v[132:135]\n"
"ds_read_b64_tr_b16 v[226:227], v235 offset:26624\n"
"v_mul_f32_dpp v63, v250, v63 row_newbcast:3 row_mask:0xf bank_mask:0xf\n"
"v_mfma_f32_16x16x32_bf16 v[132:135], v[208:211], a[144:147], v[132:135]\n"
"s_waitcnt vmcnt(0)\n"
"ds_read_b64_tr_b16 v[104:105], v231 offset:8192\n"
"v_cvt_pk_bf16_f32 v16, v16, v17\n"
"v_mfma_f32_16x16x32_bf16 v[132:135], v[212:215], a[148:151], v[132:135]\n"
"s_waitcnt lgkmcnt(4)\n"
"v_subrev_u32 v200, s21, v200\n"
"ds_read_b64_tr_b16 v[102:103], v235 offset:2048\n"
"v_mfma_f32_16x16x32_bf16 v[132:135], v[216:219], a[152:155], v[132:135]\n"
"v_cmp_lt_u32 vcc, s69, v200\n"
"ds_read_b64_tr_b16 v[110:111], v235 offset:10240\n"
"v_mfma_f32_16x16x32_bf16 v[132:135], v[220:223], a[156:159], v[132:135]\n"
"ds_write_b128 v243, v[24:27] offset:0\n"
"v_fmac_f32_dpp v57, v252, v255 row_newbcast:1 row_mask:0xf bank_mask:0xf\n"
"v_fmac_f32_dpp v59, v252, v251 row_newbcast:3 row_mask:0xf bank_mask:0xf\n"
"v_accvgpr_read_b32 v251, a231\n"
"v_fmac_f32_dpp v60, v252, v254 row_newbcast:0 row_mask:0xf bank_mask:0xf\n"
"s_cbranch_vccnz LGV_%=\nLGVK_%=:\n"
"v_mfma_f32_16x16x32_bf16 v[148:151], v[220:223], a[172:175], v[196:199]\n"
"v_cvt_pk_bf16_f32 v17, v18, v19\n"
"v_accvgpr_read_b32 v253, a230\n"
"v_mfma_f32_16x16x32_bf16 v[148:151], v[216:219], a[168:171], v[148:151]\n"
"v_add_u32_dpp v200, v200, v200 quad_perm:[1,0,3,2] row_mask:0xf bank_mask:0xf\n"
"ds_read_b64_tr_b16 v[98:99], v231 offset:2048\n"
"v_mfma_f32_16x16x32_bf16 v[148:151], v[212:215], a[164:167], v[148:151]\n"
"v_accvgpr_read_b32 v255, a229\n"
"v_mul_f32_dpp v62, v250, v62 row_newbcast:2 row_mask:0xf bank_mask:0xf\n"
"v_fmac_f32_dpp v63, v252, v251 row_newbcast:3 row_mask:0xf bank_mask:0xf\n"
"v_cvt_pk_bf16_f32 v56, v56, v57\n"
"v_cvt_pk_bf16_f32 v57, v58, v59\n"
"v_mfma_f32_16x16x32_bf16 v[148:151], v[208:211], a[160:163], v[148:151]\n"
"v_add_u32_dpp v200, v200, v200 quad_perm:[2,3,0,1] row_mask:0xf bank_mask:0xf\n"
"ds_read_b64_tr_b16 v[96:97], v231 offset:0\n"
"v_mfma_f32_16x16x32_bf16 v[148:151], v[192:195], a[124:127], v[148:151]\n"
"ds_read_b64_tr_b16 a[216:217], v236 offset:8192\n"
"v_fmac_f32_dpp v62, v252, v253 row_newbcast:2 row_mask:0xf bank_mask:0xf\n"
"v_mfma_f32_16x16x32_bf16 v[148:151], v[188:191], a[120:123], v[148:151]\n"
"v_cmp_lt_u32 vcc, 1, v200\n"
"v_fmac_f32_dpp v61, v252, v255 row_newbcast:1 row_mask:0xf bank_mask:0xf\n"
"v_mfma_f32_16x16x32_bf16 v[148:151], v[184:187], a[116:119], v[148:151]\n"
"v_cvt_pk_bf16_f32 v58, v60, v61\n"
"v_cvt_pk_bf16_f32 v59, v62, v63\n"
"v_mfma_f32_16x16x32_bf16 v[148:151], v[180:183], a[112:115], v[148:151]\n"
"ds_write_b128 v243, v[56:59] offset:2048\n"
"v_mul_f32_dpp v53, v247, v53 row_newbcast:1 row_mask:0xf bank_mask:0xf\n"
"v_cvt_pk_bf16_f32 v49, v50, v51\n"
"v_mfma_f32_16x16x32_bf16 v[164:167], v[180:183], a[128:131], v[196:199]\n"
"s_mov_b32 s47, 0\n"
"v_mul_f32_dpp v22, v246, v22 row_newbcast:2 row_mask:0xf bank_mask:0xf\n"
"v_cvt_pk_bf16_f32 v51, v54, v55\n"
"v_mfma_f32_16x16x32_bf16 v[164:167], v[184:187], a[132:135], v[164:167]\n"
"s_mov_b32 s89, vcc_lo\n"
"ds_read_b64_tr_b16 v[106:107], v231 offset:10240\n"
"ds_read_b64_tr_b16 v[112:113], v231 offset:16384\n"
"v_mfma_f32_16x16x32_bf16 v[164:167], v[188:191], a[136:139], v[164:167]\n"
"s_lshr_b32 s90, s89, s93\n"
"ds_read_b64_tr_b16 v[118:119], v235 offset:18432\n"
"ds_read_b64_tr_b16 v[122:123], v231 offset:26624\n"
"v_mfma_f32_16x16x32_bf16 v[164:167], v[192:195], a[140:143], v[164:167]\n"
"s_and_b32 s90, s90, 1\n"
"ds_read_b64_tr_b16 v[116:117], v235 offset:16384\n"
"v_mul_f32_dpp v82, v206, v82 row_newbcast:2 row_mask:0xf bank_mask:0xf\n"
"v_mfma_f32_16x16x32_bf16 v[164:167], v[208:211], a[176:179], v[164:167]\n"
"s_lshl_b32 s16, s61, 7\n"
"ds_read_b64_tr_b16 v[114:115], v231 offset:18432\n"
"v_cvt_pk_bf16_f32 v50, v52, v53\n"
"v_mfma_f32_16x16x32_bf16 v[164:167], v[212:215], a[180:183], v[164:167]\n"
"s_add_u32 s90, s90, s16\n"
"ds_read_b64_tr_b16 v[120:121], v231 offset:24576\n"
"v_xor_b32 v248, s75, v248\n"
"v_mfma_f32_16x16x32_bf16 v[164:167], v[216:219], a[184:187], v[164:167]\n"
"s_waitcnt vmcnt(63) lgkmcnt(7)\n"
"s_lshl_b32 s90, s90, 13\n"
"v_mul_f32_dpp v87, v206, v87 row_newbcast:3 row_mask:0xf bank_mask:0xf\n"
"v_mfma_f32_16x16x32_bf16 v[164:167], v[220:223], a[188:191], v[164:167]\n"
"s_barrier\n"
"s_lshl_b32 s16, s95, 9\n"
"ds_write_b128 v248, v[48:51] offset:50176\n"
"v_xor_b32 v249, s75, v249\n"
"v_mfma_f32_16x16x32_bf16 v[136:139], v[96:99], a[96:99], v[196:199]\n"
"s_add_u32 s16, s16, s90\n"
"ds_read_b64_tr_b16 a[24:25], v249 offset:50176\n"
"v_mul_f32_dpp v85, v206, v85 row_newbcast:1 row_mask:0xf bank_mask:0xf\n"
"v_mfma_f32_16x16x32_bf16 v[136:139], v[100:103], a[100:103], v[136:139]\n"
"s_waitcnt lgkmcnt(7)\n"
"s_add_u32 s17, s16, 4096\n"
"ds_read_b64_tr_b16 a[26:27], v249 offset:50184\n"
"ds_read_b64_tr_b16 v[188:189], v228 offset:40960\n"
"v_mfma_f32_16x16x32_bf16 v[136:139], v[104:107], a[104:107], v[136:139]\n"
"s_mov_b32 s72, s61\n"
"ds_read_b64_tr_b16 v[218:219], v228 offset:59392\n"
"v_cvt_pk_bf16_f32 v81, v82, v83\n"
"v_mfma_f32_16x16x32_bf16 v[136:139], v[108:111], a[108:111], v[136:139]\n"
"s_waitcnt lgkmcnt(6)\n"
"s_mov_b32 s21, s60\n"
"ds_read_b64_tr_b16 v[184:185], v232 offset:32768\n"
"v_cvt_pk_bf16_f32 v83, v86, v87\n"
"v_mfma_f32_16x16x32_bf16 v[136:139], v[112:115], a[144:147], v[136:139]\n"
"s_mov_b32 s18, s77\n"
"ds_read_b64_tr_b16 v[180:181], v228 offset:32768\n"
"v_cvt_pk_bf16_f32 v82, v84, v85\n"
"v_mfma_f32_16x16x32_bf16 v[136:139], v[116:119], a[148:151], v[136:139]\n"
"s_waitcnt lgkmcnt(7)\n"
"s_mov_b32 s19, s77\n"
"ds_write_b128 v248, v[80:83] offset:51200\n"
"v_cvt_pk_bf16_f32 v18, v20, v21\n"
"v_mfma_f32_16x16x32_bf16 v[136:139], v[120:123], a[152:155], v[136:139]\n"
"s_mov_b32 s69, -1\n"
"v_mul_f32_dpp v23, v246, v23 row_newbcast:3 row_mask:0xf bank_mask:0xf\n"
"v_cvt_pk_bf16_f32 v19, v22, v23\n"
"v_mfma_f32_16x16x32_bf16 v[136:139], v[224:227], a[156:159], v[136:139]\n"
"s_mul_i32 s88, s61, 0x10011111\n"
"ds_write_b128 v248, v[16:19] offset:49152\n"
"v_mfma_f32_16x16x32_bf16 v[152:155], v[224:227], a[172:175], v[196:199]\n"
"s_add_u32 s88, s88, 484675\n"
"v_mov_b64 v[20:21], s[22:23]\n"
"v_mov_b64 v[26:27], s[22:23]\n"
"v_mfma_f32_16x16x32_bf16 v[152:155], v[120:123], a[168:171], v[152:155]\n"
"s_and_b32 s88, s88, 0x70077777\n"
"ds_read_b64_tr_b16 a[10:11], v249 offset:49160\n"
"ds_read_b64_tr_b16 a[40:41], v249 offset:51200\n"
"v_mfma_f32_16x16x32_bf16 v[152:155], v[116:119], a[164:167], v[152:155]\n"
"s_or_b32 s88, s88, 0x9800000\n"
"ds_read_b64_tr_b16 a[8:9], v249 offset:49152\n"
"ds_read_b64_tr_b16 a[42:43], v249 offset:51208\n"
"v_mfma_f32_16x16x32_bf16 v[152:155], v[112:115], a[160:163], v[152:155]\n"
"s_mov_b32 s84, s6\n"
"ds_read_b64_tr_b16 v[186:187], v232 offset:34816\n"
"ds_read_b64_tr_b16 v[212:213], v232 offset:49152\n"
"v_mfma_f32_16x16x32_bf16 v[152:155], v[108:111], a[124:127], v[152:155]\n"
"s_and_b32 s85, s7, 0xffff\n"
"ds_read_b64_tr_b16 a[228:229], v237 offset:14336\n"
"v_mov_b64 v[10:11], s[22:23]\n"
"v_mfma_f32_16x16x32_bf16 v[152:155], v[104:107], a[120:123], v[152:155]\n"
"s_mov_b32 s86, 128\n"
"ds_read_b64_tr_b16 v[182:183], v228 offset:34816\n"
"ds_read_b64_tr_b16 v[194:195], v232 offset:43008\n"
"v_mfma_f32_16x16x32_bf16 v[152:155], v[100:103], a[116:119], v[152:155]\n"
"s_mov_b32 s87, 131072\n"
"ds_read_b64_tr_b16 v[216:217], v228 offset:57344\n"
"v_mfma_f32_16x16x32_bf16 v[152:155], v[96:99], a[112:115], v[152:155]\n"
"ds_read_b64_tr_b16 v[192:193], v232 offset:40960\n"
"ds_read_b64_tr_b16 v[210:211], v228 offset:51200\n"
"v_mfma_f32_16x16x32_bf16 v[168:171], v[96:99], a[128:131], v[196:199]\n"
"ds_read_b64_tr_b16 a[214:215], v237 offset:12296\n"
"ds_read_b64_tr_b16 v[214:215], v232 offset:51200\n"
"v_mfma_f32_16x16x32_bf16 v[168:171], v[100:103], a[132:135], v[168:171]\n"
"ds_read_b64_tr_b16 v[190:191], v228 offset:43008\n"
"v_mfma_f32_16x16x32_bf16 v[168:171], v[104:107], a[136:139], v[168:171]\n"
"ds_read_b64_tr_b16 v[208:209], v228 offset:49152\n"
"ds_read_b64_tr_b16 v[220:221], v232 offset:57344\n"
"v_mfma_f32_16x16x32_bf16 v[168:171], v[108:111], a[140:143], v[168:171]\n"
"s_waitcnt lgkmcnt(11)\n"
"ds_read_b64_tr_b16 v[98:99], v229 offset:34816\n"
"v_mfma_f32_16x16x32_bf16 v[168:171], v[112:115], a[176:179], v[168:171]\n"
"ds_read_b64_tr_b16 v[222:223], v232 offset:59392\n"
"ds_read_b64_tr_b16 v[104:105], v229 offset:40960\n"
"v_mfma_f32_16x16x32_bf16 v[168:171], v[116:119], a[180:183], v[168:171]\n"
"ds_read_b64_tr_b16 a[226:227], v236 offset:14344\n"
"ds_read_b64_tr_b16 v[96:97], v229 offset:32768\n"
"v_mfma_f32_16x16x32_bf16 v[168:171], v[120:123], a[184:187], v[168:171]\n"
"s_add_u32 s41, s70, 327680\n"
"s_add_u32 m0, s71, 32768\n"
"ds_read_b64_tr_b16 v[108:109], v233 offset:40960\n"
"v_mfma_f32_16x16x32_bf16 v[168:171], v[224:227], a[188:191], v[168:171]\n"
"s_waitcnt lgkmcnt(11)\n"
"buffer_load_dwordx4 v240, s[28:31], s41 offen lds\n"
"v_mfma_f32_16x16x32_bf16 v[124:127], v[180:183], a[48:51], v[124:127]\n"
"v_xor_b32 v228, s75, v228\n"
"v_mfma_f32_16x16x32_bf16 v[124:127], v[184:187], a[52:55], v[124:127]\n"
"s_waitcnt lgkmcnt(8)\n"
"v_bfe_u32 v254, v242, 4, 4\n"
"v_xor_b32 v232, s75, v232\n"
"v_mfma_f32_16x16x32_bf16 v[124:127], v[188:191], a[56:59], v[124:127]\n"
"s_add_u32 s40, s41, s52\n"
"ds_read_b64_tr_b16 a[230:231], v237 offset:14344\n"
"ds_read_b64_tr_b16 v[102:103], v233 offset:34816\n"
"v_mfma_f32_16x16x32_bf16 v[124:127], v[192:195], a[60:63], v[124:127]\n"
"s_waitcnt lgkmcnt(7)\n"
"buffer_load_dwordx4 v240, s[28:31], s40 offen offset:1024 lds\n"
"v_mfma_f32_16x16x32_bf16 v[124:127], v[208:211], a[0:3], v[124:127]\n"
"v_xor_b32 v248, s75, v248\n"
"v_mov_b64 v[24:25], s[22:23]\n"
"v_mfma_f32_16x16x32_bf16 v[124:127], v[212:215], a[4:7], v[124:127]\n"
"v_and_b32 v243, 0x3f0, v242\n"
"v_mov_b64 v[6:7], s[22:23]\n"
"v_mfma_f32_16x16x32_bf16 v[124:127], v[216:219], a[8:11], v[124:127]\n"
"s_waitcnt lgkmcnt(2)\n"
"s_add_u32 s40, s41, s53\n"
"ds_read_b64_tr_b16 a[212:213], v237 offset:12288\n"
"ds_read_b64_tr_b16 v[116:117], v233 offset:49152\n"
"v_mfma_f32_16x16x32_bf16 v[124:127], v[220:223], a[12:15], v[124:127]\n"
"buffer_load_dwordx4 v240, s[28:31], s40 offen offset:2048 lds\n"
"v_mfma_f32_16x16x32_bf16 v[140:143], v[220:223], a[28:31], v[140:143]\n"
"v_mov_b64 v[40:41], s[22:23]\n"
"v_mfma_f32_16x16x32_bf16 v[140:143], v[216:219], a[24:27], v[140:143]\n"
"v_xor_b32 v249, s75, v249\n"
"v_bfe_u32 v255, v242, 8, 1\n"
"v_mfma_f32_16x16x32_bf16 v[140:143], v[212:215], a[20:23], v[140:143]\n"
"ds_read_b64_tr_b16 a[210:211], v236 offset:12296\n"
"ds_read_b64_tr_b16 a[224:225], v236 offset:14336\n"
"v_mfma_f32_16x16x32_bf16 v[140:143], v[208:211], a[16:19], v[140:143]\n"
"ds_read_b64_tr_b16 v[110:111], v233 offset:43008\n"
"v_mov_b32 v241, s60\n"
"v_mfma_f32_16x16x32_bf16 v[140:143], v[192:195], a[76:79], v[140:143]\n"
"ds_read_b64_tr_b16 v[106:107], v229 offset:43008\n"
"ds_read_b64_tr_b16 v[224:225], v233 offset:57344\n"
"v_mfma_f32_16x16x32_bf16 v[140:143], v[188:191], a[72:75], v[140:143]\n"
"ds_read_b64_tr_b16 v[118:119], v233 offset:51200\n"
"v_mfma_f32_16x16x32_bf16 v[140:143], v[184:187], a[68:71], v[140:143]\n"
"s_add_u32 s40, s41, s54\n"
"ds_read_b64_tr_b16 a[208:209], v236 offset:12288\n"
"ds_read_b64_tr_b16 v[114:115], v229 offset:51200\n"
"v_mfma_f32_16x16x32_bf16 v[140:143], v[180:183], a[64:67], v[140:143]\n"
"buffer_load_dwordx4 v240, s[28:31], s40 offen offset:3072 lds\n"
"v_mfma_f32_16x16x32_bf16 v[156:159], v[180:183], a[80:83], v[156:159]\n"
"s_add_u32 s41, s70, 491520\n"
"s_add_u32 m0, s71, 49152\n"
"v_mov_b64 v[90:91], 0\n"
"v_mfma_f32_16x16x32_bf16 v[156:159], v[184:187], a[84:87], v[156:159]\n"
"buffer_load_dwordx4 v240, s[28:31], s41 offen lds\n"
"v_mfma_f32_16x16x32_bf16 v[156:159], v[188:191], a[88:91], v[156:159]\n"
"v_mul_u32_u24 v254, 2560, v254\n"
"v_mfma_f32_16x16x32_bf16 v[156:159], v[192:195], a[92:95], v[156:159]\n"
"v_mov_b64 v[246:247], 0\n"
"v_subrev_f32 v141, s22, v141\n"
"v_mfma_f32_16x16x32_bf16 v[156:159], v[208:211], a[32:35], v[156:159]\n"
"ds_read_b64_tr_b16 a[222:223], v237 offset:8200\n"
"ds_read_b64_tr_b16 v[100:101], v233 offset:32768\n"
"v_mfma_f32_16x16x32_bf16 v[156:159], v[212:215], a[36:39], v[156:159]\n"
"ds_read_b64_tr_b16 a[218:219], v236 offset:8200\n"
"v_subrev_f32 v140, s22, v140\n"
"v_mfma_f32_16x16x32_bf16 v[156:159], v[216:219], a[40:43], v[156:159]\n"
"s_add_u32 s40, s41, s52\n"
"ds_read_b64_tr_b16 v[112:113], v229 offset:49152\n"
"ds_read_b64_tr_b16 v[120:121], v229 offset:57344\n"
"v_mfma_f32_16x16x32_bf16 v[156:159], v[220:223], a[44:47], v[156:159]\n"
"s_waitcnt lgkmcnt(5)\n"
"buffer_load_dwordx4 v240, s[28:31], s40 offen offset:1024 lds\n"
"v_mfma_f32_16x16x32_bf16 v[172:175], v[220:223], a[228:231], v[172:175]\n"
"s_add_u32 s40, s41, s53\n"
"v_add_u32 v236, s94, v243\n"
"v_mov_b64 v[12:13], s[22:23]\n"
"v_mfma_f32_16x16x32_bf16 v[172:175], v[216:219], a[224:227], v[172:175]\n"
"buffer_load_dwordx4 v240, s[28:31], s40 offen offset:2048 lds\n"
"v_mfma_f32_16x16x32_bf16 v[172:175], v[212:215], a[212:215], v[172:175]\n"
"v_lshl_add_u32 v254, v255, 5, v254\n"
"v_subrev_f32 v124, s22, v124\n"
"v_mfma_f32_16x16x32_bf16 v[172:175], v[208:211], a[208:211], v[172:175]\n"
"v_mov_b64 v[2:3], s[22:23]\n"
"v_subrev_f32 v159, s22, v159\n"
"v_mfma_f32_16x16x32_bf16 v[172:175], v[192:195], a[236:239], v[172:175]\n"
"s_add_u32 s40, s41, s54\n"
"ds_read_b64_tr_b16 v[122:123], v229 offset:59392\n"
"ds_read_b64_tr_b16 v[226:227], v233 offset:59392\n"
"v_mfma_f32_16x16x32_bf16 v[172:175], v[188:191], a[232:235], v[172:175]\n"
"s_waitcnt lgkmcnt(4)\n"
"buffer_load_dwordx4 v240, s[28:31], s40 offen offset:3072 lds\n"
"v_mfma_f32_16x16x32_bf16 v[172:175], v[184:187], a[220:223], v[172:175]\n"
"s_lshl_b32 s62, s70, 9\n"
"v_mov_b64 v[92:93], 0\n"
"v_mfma_f32_16x16x32_bf16 v[172:175], v[180:183], a[216:219], v[172:175]\n"
"s_and_b32 s70, s88, 15\n"
"v_mov_b64 v[32:33], s[22:23]\n"
"v_subrev_f32 v125, s22, v125\n"
"v_mfma_f32_16x16x32_bf16 v[128:131], v[96:99], a[48:51], v[128:131]\n"
"s_lshr_b32 s88, s88, 4\n"
"ds_read_b64_tr_b16 v[180:181], v230 offset:32768\n"
"v_subrev_f32 v156, s22, v156\n"
"v_mfma_f32_16x16x32_bf16 v[128:131], v[100:103], a[52:55], v[128:131]\n"
"s_lshl_b32 s70, s70, 7\n"
"ds_read_b64_tr_b16 v[188:189], v230 offset:40960\n"
"v_bfe_u32 v255, v242, 9, 1\n"
"v_mfma_f32_16x16x32_bf16 v[128:131], v[104:107], a[56:59], v[128:131]\n"
"s_xor_b32 s71, s71, s75\n"
"v_cvt_pk_bf16_f32 v124, v124, v125\n"
"v_mfma_f32_16x16x32_bf16 v[128:131], v[108:111], a[60:63], v[128:131]\n"
"s_waitcnt lgkmcnt(4)\n"
"v_lshlrev_b32_e64 v201, 2, s72\n"
"ds_read_b64_tr_b16 v[216:217], v230 offset:57344\n"
"v_subrev_f32 v142, s22, v142\n"
"v_mfma_f32_16x16x32_bf16 v[128:131], v[112:115], a[0:3], v[128:131]\n"
"global_load_dword v200, v201, s[6:7] sc1\n"
"v_mfma_f32_16x16x32_bf16 v[128:131], v[116:119], a[4:7], v[128:131]\n"
"s_waitcnt lgkmcnt(3)\n"
"v_mov_b64 v[0:1], s[22:23]\n"
"v_subrev_f32 v127, s22, v127\n"
"v_mfma_f32_16x16x32_bf16 v[128:131], v[120:123], a[8:11], v[128:131]\n"
"v_mov_b64 v[86:87], 0\n"
"v_mfma_f32_16x16x32_bf16 v[128:131], v[224:227], a[12:15], v[128:131]\n"
"ds_read_b64_tr_b16 v[220:221], v234 offset:57344\n"
"v_mov_b64 v[16:17], s[22:23]\n"
"v_mfma_f32_16x16x32_bf16 v[144:147], v[224:227], a[28:31], v[144:147]\n"
"v_lshl_add_u32 v243, s44, 2, v243\n"
"v_mfma_f32_16x16x32_bf16 v[144:147], v[120:123], a[24:27], v[144:147]\n"
"ds_read_b64_tr_b16 v[186:187], v234 offset:34816\n"
"v_mov_b64 v[36:37], s[22:23]\n"
"v_mfma_f32_16x16x32_bf16 v[144:147], v[116:119], a[20:23], v[144:147]\n"
"ds_read_b64_tr_b16 v[222:223], v234 offset:59392\n"
"v_subrev_f32 v126, s22, v126\n"
"v_mfma_f32_16x16x32_bf16 v[144:147], v[112:115], a[16:19], v[144:147]\n"
"v_subrev_f32 v128, s22, v128\n"
"v_mfma_f32_16x16x32_bf16 v[144:147], v[108:111], a[76:79], v[144:147]\n"
"ds_read_b64_tr_b16 v[210:211], v230 offset:51200\n"
"v_subrev_f32 v129, s22, v129\n"
"v_mfma_f32_16x16x32_bf16 v[144:147], v[104:107], a[72:75], v[144:147]\n"
"v_subrev_f32 v130, s22, v130\n"
"v_subrev_f32 v131, s22, v131\n"
"v_mfma_f32_16x16x32_bf16 v[144:147], v[100:103], a[68:71], v[144:147]\n"
"ds_read_b64_tr_b16 v[214:215], v234 offset:51200\n"
"v_cvt_pk_bf16_f32 v125, v126, v127\n"
"v_mfma_f32_16x16x32_bf16 v[144:147], v[96:99], a[64:67], v[144:147]\n"
"ds_read_b64_tr_b16 v[190:191], v230 offset:43008\n"
"v_mov_b64 v[14:15], s[22:23]\n"
"v_mfma_f32_16x16x32_bf16 v[160:163], v[96:99], a[80:83], v[160:163]\n"
"v_lshl_add_u32 v254, v255, 4, v254\n"
"v_subrev_f32 v158, s22, v158\n"
"v_mfma_f32_16x16x32_bf16 v[160:163], v[100:103], a[84:87], v[160:163]\n"
"ds_read_b64_tr_b16 v[184:185], v234 offset:32768\n"
"v_subrev_f32 v143, s22, v143\n"
"v_mfma_f32_16x16x32_bf16 v[160:163], v[104:107], a[88:91], v[160:163]\n"
"v_cvt_pk_bf16_f32 v126, v128, v129\n"
"v_mfma_f32_16x16x32_bf16 v[160:163], v[108:111], a[92:95], v[160:163]\n"
"ds_read_b64_tr_b16 v[182:183], v230 offset:34816\n"
"v_cvt_pk_bf16_f32 v140, v140, v141\n"
"v_mfma_f32_16x16x32_bf16 v[160:163], v[112:115], a[32:35], v[160:163]\n"
"ds_read_b64_tr_b16 v[208:209], v230 offset:49152\n"
"v_cvt_pk_bf16_f32 v141, v142, v143\n"
"v_mfma_f32_16x16x32_bf16 v[160:163], v[116:119], a[36:39], v[160:163]\n"
"v_subrev_f32 v172, s22, v172\n"
"v_xor_b32 v233, s75, v233\n"
"v_mfma_f32_16x16x32_bf16 v[160:163], v[120:123], a[40:43], v[160:163]\n"
"ds_read_b64_tr_b16 v[194:195], v234 offset:43008\n"
"v_cvt_pk_bf16_f32 v127, v130, v131\n"
"v_mfma_f32_16x16x32_bf16 v[160:163], v[224:227], a[44:47], v[160:163]\n"
"buffer_store_dwordx4 v[124:127], v243, s[32:35], s16 offen sc0 sc1\n"
"v_subrev_f32 v157, s22, v157\n"
"v_mfma_f32_16x16x32_bf16 v[176:179], v[224:227], a[228:231], v[176:179]\n"
"ds_read_b64_tr_b16 v[218:219], v230 offset:59392\n"
"v_subrev_f32 v145, s22, v145\n"
"v_mfma_f32_16x16x32_bf16 v[176:179], v[120:123], a[224:227], v[176:179]\n"
"ds_read_b64_tr_b16 v[192:193], v234 offset:40960\n"
"v_mov_b64 v[244:245], 0\n"
"v_mfma_f32_16x16x32_bf16 v[176:179], v[116:119], a[212:215], v[176:179]\n"
"ds_read_b64_tr_b16 v[212:213], v234 offset:49152\n"
"v_subrev_f32 v144, s22, v144\n"
"v_mfma_f32_16x16x32_bf16 v[176:179], v[112:115], a[208:211], v[176:179]\n"
"v_subrev_f32 v163, s22, v163\n"
"v_mfma_f32_16x16x32_bf16 v[176:179], v[108:111], a[236:239], v[176:179]\n"
"ds_read_b64_tr_b16 v[224:225], v235 offset:57344\n"
"v_lshl_add_u32 v248, v242, 8, s93\n"
"v_mfma_f32_16x16x32_bf16 v[176:179], v[104:107], a[232:235], v[176:179]\n"
"v_subrev_f32 v146, s22, v146\n"
"v_subrev_f32 v147, s22, v147\n"
"v_mfma_f32_16x16x32_bf16 v[176:179], v[100:103], a[220:223], v[176:179]\n"
"ds_read_b64_tr_b16 v[226:227], v235 offset:59392\n"
"v_mfma_f32_16x16x32_bf16 v[176:179], v[96:99], a[216:219], v[176:179]\n"
"s_waitcnt lgkmcnt(5)\n"
"v_cvt_pk_bf16_f32 v156, v156, v157\n"
"v_mfma_f32_16x16x32_bf16 v[132:135], v[180:183], a[48:51], v[132:135]\n"
"ds_read_b64_tr_b16 v[102:103], v235 offset:34816\n"
"v_subrev_f32 v162, s22, v162\n"
"v_mfma_f32_16x16x32_bf16 v[132:135], v[184:187], a[52:55], v[132:135]\n"
"v_cvt_pk_bf16_f32 v157, v158, v159\n"
"v_xor_b32 v229, s75, v229\n"
"v_mfma_f32_16x16x32_bf16 v[132:135], v[188:191], a[56:59], v[132:135]\n"
"s_waitcnt lgkmcnt(3)\n"
"ds_read_b64_tr_b16 v[114:115], v231 offset:51200\n"
"v_mov_b64 v[88:89], 0\n"
"v_mfma_f32_16x16x32_bf16 v[132:135], v[192:195], a[60:63], v[132:135]\n"
"v_mov_b64 v[22:23], s[22:23]\n"
"v_mov_b64 v[94:95], 0\n"
"v_mfma_f32_16x16x32_bf16 v[132:135], v[208:211], a[0:3], v[132:135]\n"
"ds_read_b64_tr_b16 v[104:105], v231 offset:40960\n"
"v_subrev_f32 v174, s22, v174\n"
"v_mfma_f32_16x16x32_bf16 v[132:135], v[212:215], a[4:7], v[132:135]\n"
"v_subrev_f32 v173, s22, v173\n"
"v_cvt_pk_bf16_f32 v142, v144, v145\n"
"v_mfma_f32_16x16x32_bf16 v[132:135], v[216:219], a[8:11], v[132:135]\n"
"ds_read_b64_tr_b16 v[108:109], v235 offset:40960\n"
"v_cvt_pk_bf16_f32 v143, v146, v147\n"
"v_mfma_f32_16x16x32_bf16 v[132:135], v[220:223], a[12:15], v[132:135]\n"
"buffer_store_dwordx4 v[140:143], v243, s[32:35], s16 offen offset:2048 sc0 sc1\n"
"ds_read_b64_tr_b16 v[96:97], v231 offset:32768\n"
"v_mfma_f32_16x16x32_bf16 v[148:151], v[220:223], a[28:31], v[148:151]\n"
"ds_read_b64_tr_b16 v[110:111], v235 offset:43008\n"
"v_mov_b64 v[18:19], s[22:23]\n"
"v_mfma_f32_16x16x32_bf16 v[148:151], v[216:219], a[24:27], v[148:151]\n"
"v_bfe_u32 v237, v242, 4, 3\n"
"v_cvt_pk_bf16_f32 v159, v162, v163\n"
"v_mfma_f32_16x16x32_bf16 v[148:151], v[212:215], a[20:23], v[148:151]\n"
"ds_read_b64_tr_b16 v[120:121], v231 offset:57344\n"
"v_add_u32 v241, 1, v241\n"
"v_mfma_f32_16x16x32_bf16 v[148:151], v[208:211], a[16:19], v[148:151]\n"
"ds_read_b64_tr_b16 v[100:101], v235 offset:32768\n"
"v_mfma_f32_16x16x32_bf16 v[148:151], v[192:195], a[76:79], v[148:151]\n"
"ds_read_b64_tr_b16 v[98:99], v231 offset:34816\n"
"v_max_u32 v237, 1, v237\n"
"v_mfma_f32_16x16x32_bf16 v[148:151], v[188:191], a[72:75], v[148:151]\n"
"v_subrev_f32 v160, s22, v160\n"
"v_subrev_f32 v161, s22, v161\n"
"v_mfma_f32_16x16x32_bf16 v[148:151], v[184:187], a[68:71], v[148:151]\n"
"ds_read_b64_tr_b16 v[106:107], v231 offset:43008\n"
"v_subrev_f32 v179, s22, v179\n"
"v_mfma_f32_16x16x32_bf16 v[148:151], v[180:183], a[64:67], v[148:151]\n"
"v_mov_b64 v[30:31], s[22:23]\n"
"v_subrev_f32 v176, s22, v176\n"
"v_mfma_f32_16x16x32_bf16 v[164:167], v[180:183], a[80:83], v[164:167]\n"
"ds_read_b64_tr_b16 v[112:113], v231 offset:49152\n"
"v_mov_b64 v[38:39], s[22:23]\n"
"v_mfma_f32_16x16x32_bf16 v[164:167], v[184:187], a[84:87], v[164:167]\n"
"ds_read_b64_tr_b16 v[118:119], v235 offset:51200\n"
"v_subrev_f32 v132, s22, v132\n"
"v_mfma_f32_16x16x32_bf16 v[164:167], v[188:191], a[88:91], v[164:167]\n"
"v_xor_b32 v237, s61, v237\n"
"v_subrev_f32 v133, s22, v133\n"
"v_mfma_f32_16x16x32_bf16 v[164:167], v[192:195], a[92:95], v[164:167]\n"
"ds_read_b64_tr_b16 v[116:117], v235 offset:49152\n"
"v_cvt_pk_bf16_f32 v158, v160, v161\n"
"v_mfma_f32_16x16x32_bf16 v[164:167], v[208:211], a[32:35], v[164:167]\n"
"v_mov_b64 v[34:35], s[22:23]\n"
"v_subrev_f32 v175, s22, v175\n"
"v_mfma_f32_16x16x32_bf16 v[164:167], v[212:215], a[36:39], v[164:167]\n"
"ds_read_b64_tr_b16 v[122:123], v231 offset:59392\n"
"v_mfma_f32_16x16x32_bf16 v[164:167], v[216:219], a[40:43], v[164:167]\n"
"v_add_u32 v238, s97, v254\n"
"v_mov_b64 v[54:55], s[22:23]\n"
"v_mfma_f32_16x16x32_bf16 v[164:167], v[220:223], a[44:47], v[164:167]\n"
"s_barrier\n"
"s_waitcnt lgkmcnt(4)\n"
"v_mfma_f32_16x16x32_bf16 v[136:139], v[96:99], a[48:51], v[136:139]\n"
"ds_read_b64_tr_b16 v[182:183], v228 offset:2048\n"
"v_subrev_f32 v178, s22, v178\n"
"v_mfma_f32_16x16x32_bf16 v[136:139], v[100:103], a[52:55], v[136:139]\n"
"ds_read_b64_tr_b16 v[222:223], v232 offset:26624\n"
"v_add_u32 v253, s99, v254\n"
"v_mfma_f32_16x16x32_bf16 v[136:139], v[104:107], a[56:59], v[136:139]\n"
"ds_read_b64_tr_b16 v[186:187], v232 offset:2048\n"
"v_mov_b64 v[42:43], s[22:23]\n"
"v_mfma_f32_16x16x32_bf16 v[136:139], v[108:111], a[60:63], v[136:139]\n"
"s_waitcnt lgkmcnt(4)\n"
"ds_read_b64_tr_b16 v[194:195], v232 offset:10240\n"
"v_mov_b64 v[50:51], s[22:23]\n"
"v_mfma_f32_16x16x32_bf16 v[136:139], v[112:115], a[0:3], v[136:139]\n"
"ds_read_b64_tr_b16 v[210:211], v228 offset:18432\n"
"v_subrev_f32 v135, s22, v135\n"
"v_mfma_f32_16x16x32_bf16 v[136:139], v[116:119], a[4:7], v[136:139]\n"
"s_waitcnt lgkmcnt(5)\n"
"ds_read_b64_tr_b16 v[192:193], v232 offset:8192\n"
"v_mfma_f32_16x16x32_bf16 v[136:139], v[120:123], a[8:11], v[136:139]\n"
"ds_read_b64_tr_b16 v[188:189], v228 offset:8192\n"
"v_xor_b32 v239, 64, v238\n"
"v_mfma_f32_16x16x32_bf16 v[136:139], v[224:227], a[12:15], v[136:139]\n"
"buffer_store_dwordx4 v[156:159], v243, s[32:35], s17 offen sc0 sc1\n"
"ds_read_b64_tr_b16 v[214:215], v232 offset:18432\n"
"v_mfma_f32_16x16x32_bf16 v[152:155], v[224:227], a[28:31], v[152:155]\n"
"ds_read_b64_tr_b16 v[190:191], v228 offset:10240\n"
"v_subrev_f32 v134, s22, v134\n"
"v_mfma_f32_16x16x32_bf16 v[152:155], v[120:123], a[24:27], v[152:155]\n"
"ds_read_b64_tr_b16 v[208:209], v228 offset:16384\n"
"v_add_u32 v252, s98, v254\n"
"v_mfma_f32_16x16x32_bf16 v[152:155], v[116:119], a[20:23], v[152:155]\n"
"ds_read_b64_tr_b16 v[184:185], v232 offset:0\n"
"v_cvt_pk_bf16_f32 v172, v172, v173\n"
"v_mfma_f32_16x16x32_bf16 v[152:155], v[112:115], a[16:19], v[152:155]\n"
"ds_read_b64_tr_b16 v[216:217], v228 offset:24576\n"
"v_mov_b64 v[8:9], s[22:23]\n"
"v_mfma_f32_16x16x32_bf16 v[152:155], v[108:111], a[76:79], v[152:155]\n"
"ds_read_b64_tr_b16 v[218:219], v228 offset:26624\n"
"v_mov_b64 v[52:53], s[22:23]\n"
"v_mfma_f32_16x16x32_bf16 v[152:155], v[104:107], a[72:75], v[152:155]\n"
"ds_read_b64_tr_b16 v[212:213], v232 offset:16384\n"
"v_mov_b64 v[84:85], 0\n"
"v_mfma_f32_16x16x32_bf16 v[152:155], v[100:103], a[68:71], v[152:155]\n"
"ds_read_b64_tr_b16 v[220:221], v232 offset:24576\n"
"v_cvt_pk_bf16_f32 v173, v174, v175\n"
"v_mfma_f32_16x16x32_bf16 v[152:155], v[96:99], a[64:67], v[152:155]\n"
"ds_read_b64_tr_b16 v[180:181], v228 offset:0\n"
"v_mov_b64 v[28:29], s[22:23]\n"
"v_mfma_f32_16x16x32_bf16 v[168:171], v[96:99], a[80:83], v[168:171]\n"
"ds_read_b64_tr_b16 v[96:97], v229 offset:0\n"
"ds_read_b64_tr_b16 v[98:99], v229 offset:2048\n"
"v_mfma_f32_16x16x32_bf16 v[168:171], v[100:103], a[84:87], v[168:171]\n"
"ds_read_b64_tr_b16 v[100:101], v233 offset:0\n"
"ds_read_b64_tr_b16 v[102:103], v233 offset:2048\n"
"v_mfma_f32_16x16x32_bf16 v[168:171], v[104:107], a[88:91], v[168:171]\n"
"ds_read_b64_tr_b16 v[104:105], v229 offset:8192\n"
"v_mfma_f32_16x16x32_bf16 v[168:171], v[108:111], a[92:95], v[168:171]\n"
"v_cvt_pk_bf16_f32 v175, v178, v179\n"
"v_mfma_f32_16x16x32_bf16 v[168:171], v[112:115], a[32:35], v[168:171]\n"
"v_mov_b64 v[48:49], s[22:23]\n"
"v_subrev_f32 v177, s22, v177\n"
"v_mfma_f32_16x16x32_bf16 v[168:171], v[116:119], a[36:39], v[168:171]\n"
"v_lshlrev_b32 v237, 2, v237\n"
"v_mov_b64 v[46:47], s[22:23]\n"
"v_mfma_f32_16x16x32_bf16 v[168:171], v[120:123], a[40:43], v[168:171]\n"
"s_add_u32 s47, s47, 1\n"
"v_cvt_pk_bf16_f32 v132, v132, v133\n"
"v_mfma_f32_16x16x32_bf16 v[168:171], v[224:227], a[44:47], v[168:171]\n"
"s_waitcnt lgkmcnt(5)\n"
"v_mov_b64 v[4:5], s[22:23]\n"
"v_cvt_pk_bf16_f32 v174, v176, v177\n"
"LGL_%=:\n"
"v_mfma_f32_16x16x32_bf16 v[124:127], v[180:183], a[96:99], v[196:199]\n"
"ds_read_b64_tr_b16 v[106:107], v229 offset:10240\n"
"v_cvt_pk_bf16_f32 v133, v134, v135\n"
"v_mfma_f32_16x16x32_bf16 v[124:127], v[184:187], a[100:103], v[124:127]\n"
"ds_read_b64_tr_b16 v[108:109], v233 offset:8192\n"
"v_subrev_f32 v136, s22, v136\n"
"v_mfma_f32_16x16x32_bf16 v[124:127], v[188:191], a[104:107], v[124:127]\n"
"ds_read_b64_tr_b16 v[110:111], v233 offset:10240\n"
"v_subrev_f32 v137, s22, v137\n"
"v_mfma_f32_16x16x32_bf16 v[124:127], v[192:195], a[108:111], v[124:127]\n"
"ds_read_b64_tr_b16 v[112:113], v229 offset:16384\n"
"v_subrev_f32 v138, s22, v138\n"
"v_mfma_f32_16x16x32_bf16 v[124:127], v[208:211], a[144:147], v[124:127]\n"
"ds_read_b64_tr_b16 v[114:115], v229 offset:18432\n"
"v_subrev_f32 v139, s22, v139\n"
"v_mfma_f32_16x16x32_bf16 v[124:127], v[212:215], a[148:151], v[124:127]\n"
"s_waitcnt lgkmcnt(7)\n"
"ds_read_b64_tr_b16 v[116:117], v233 offset:16384\n"
"v_cvt_pk_bf16_f32 v134, v136, v137\n"
"v_mfma_f32_16x16x32_bf16 v[124:127], v[216:219], a[152:155], v[124:127]\n"
"ds_read_b64_tr_b16 v[118:119], v233 offset:18432\n"
"v_cvt_pk_bf16_f32 v135, v138, v139\n"
"v_mfma_f32_16x16x32_bf16 v[124:127], v[220:223], a[156:159], v[124:127]\n"
"buffer_store_dwordx4 v[172:175], v243, s[32:35], s17 offen offset:2048 sc0 sc1\n"
"v_subrev_f32 v148, s22, v148\n"
"v_mfma_f32_16x16x32_bf16 v[140:143], v[220:223], a[172:175], v[196:199]\n"
"ds_read_b64_tr_b16 v[120:121], v229 offset:24576\n"
"v_subrev_f32 v149, s22, v149\n"
"v_mfma_f32_16x16x32_bf16 v[140:143], v[216:219], a[168:171], v[140:143]\n"
"ds_read_b64_tr_b16 v[122:123], v229 offset:26624\n"
"v_subrev_f32 v150, s22, v150\n"
"v_mfma_f32_16x16x32_bf16 v[140:143], v[212:215], a[164:167], v[140:143]\n"
"ds_read_b64_tr_b16 v[224:225], v233 offset:24576\n"
"v_subrev_f32 v151, s22, v151\n"
"v_mfma_f32_16x16x32_bf16 v[140:143], v[208:211], a[160:163], v[140:143]\n"
"ds_read_b64_tr_b16 v[226:227], v233 offset:26624\n"
"v_cvt_pk_bf16_f32 v148, v148, v149\n"
"v_mfma_f32_16x16x32_bf16 v[140:143], v[192:195], a[124:127], v[140:143]\n"
"v_cvt_pk_bf16_f32 v149, v150, v151\n"
"v_subrev_f32 v152, s22, v152\n"
"v_mfma_f32_16x16x32_bf16 v[140:143], v[188:191], a[120:123], v[140:143]\n"
"s_add_u32 s41, s70, 0\n"
"v_subrev_f32 v153, s22, v153\n"
"v_subrev_f32 v154, s22, v154\n"
"v_mfma_f32_16x16x32_bf16 v[140:143], v[184:187], a[116:119], v[140:143]\n"
"s_mov_b32 m0, s71\n"
"v_subrev_f32 v155, s22, v155\n"
"v_cvt_pk_bf16_f32 v150, v152, v153\n"
"v_mfma_f32_16x16x32_bf16 v[140:143], v[180:183], a[112:115], v[140:143]\n"
"s_waitcnt vmcnt(14)\n"
"buffer_load_dwordx4 v240, s[28:31], s41 offen lds\n"
"v_mfma_f32_16x16x32_bf16 v[156:159], v[180:183], a[128:131], v[196:199]\n"
"v_xor_b32 v230, s75, v230\n"
"v_cvt_pk_bf16_f32 v151, v154, v155\n"
"v_mfma_f32_16x16x32_bf16 v[156:159], v[184:187], a[132:135], v[156:159]\n"
"v_xor_b32 v234, s75, v234\n"
"v_subrev_f32 v164, s22, v164\n"
"v_mfma_f32_16x16x32_bf16 v[156:159], v[188:191], a[136:139], v[156:159]\n"
"v_subrev_f32 v165, s22, v165\n"
"v_subrev_f32 v166, s22, v166\n"
"v_mfma_f32_16x16x32_bf16 v[156:159], v[192:195], a[140:143], v[156:159]\n"
"v_subrev_f32 v167, s22, v167\n"
"v_cvt_pk_bf16_f32 v164, v164, v165\n"
"v_mfma_f32_16x16x32_bf16 v[156:159], v[208:211], a[176:179], v[156:159]\n"
"v_cvt_pk_bf16_f32 v165, v166, v167\n"
"v_subrev_f32 v168, s22, v168\n"
"v_mfma_f32_16x16x32_bf16 v[156:159], v[212:215], a[180:183], v[156:159]\n"
"v_subrev_f32 v169, s22, v169\n"
"v_subrev_f32 v170, s22, v170\n"
"v_mfma_f32_16x16x32_bf16 v[156:159], v[216:219], a[184:187], v[156:159]\n"
"v_subrev_f32 v171, s22, v171\n"
"v_cvt_pk_bf16_f32 v166, v168, v169\n"
"v_mfma_f32_16x16x32_bf16 v[156:159], v[220:223], a[188:191], v[156:159]\n"
"buffer_store_dwordx4 v[132:135], v243, s[32:35], s16 offen offset:1024 sc0 sc1\n"
"v_cvt_pk_bf16_f32 v167, v170, v171\n"
"v_mfma_f32_16x16x32_bf16 v[172:175], v[220:223], a[252:255], v[196:199]\n"
"ds_read_b64_tr_b16 v[220:221], v234 offset:24576\n"
"v_xor_b32 v231, s75, v231\n"
"v_mfma_f32_16x16x32_bf16 v[172:175], v[216:219], a[248:251], v[172:175]\n"
"ds_read_b64_tr_b16 v[216:217], v230 offset:24576\n"
"v_xor_b32 v235, s75, v235\n"
"v_mfma_f32_16x16x32_bf16 v[172:175], v[212:215], a[244:247], v[172:175]\n"
"ds_read_b64_tr_b16 v[212:213], v234 offset:16384\n"
"v_lshlrev_b32 v202, 16, v84\n"
"v_mfma_f32_16x16x32_bf16 v[172:175], v[208:211], a[240:243], v[172:175]\n"
"ds_read_b64_tr_b16 v[208:209], v230 offset:16384\n"
"v_and_b32 v84, s73, v84\n"
"v_mfma_f32_16x16x32_bf16 v[172:175], v[192:195], a[204:207], v[172:175]\n"
"ds_read_b64_tr_b16 v[192:193], v234 offset:8192\n"
"v_add_f32 v25, v25, v84\n"
"v_mfma_f32_16x16x32_bf16 v[172:175], v[188:191], a[200:203], v[172:175]\n"
"ds_read_b64_tr_b16 v[188:189], v230 offset:8192\n"
"v_lshlrev_b32 v203, 16, v85\n"
"v_mfma_f32_16x16x32_bf16 v[172:175], v[184:187], a[196:199], v[172:175]\n"
"s_add_u32 s40, s41, s52\n"
"ds_read_b64_tr_b16 v[184:185], v234 offset:0\n"
"v_and_b32 v85, s73, v85\n"
"v_mfma_f32_16x16x32_bf16 v[172:175], v[180:183], a[192:195], v[172:175]\n"
"s_waitcnt vmcnt(13) lgkmcnt(7)\n"
"buffer_load_dwordx4 v240, s[28:31], s40 offen offset:1024 lds\n"
"v_mfma_f32_16x16x32_bf16 v[128:131], v[96:99], a[96:99], v[196:199]\n"
"s_waitcnt lgkmcnt(6)\n"
"ds_read_b64_tr_b16 v[180:181], v230 offset:0\n"
"v_add_f32 v27, v27, v85\n"
"v_mfma_f32_16x16x32_bf16 v[128:131], v[100:103], a[100:103], v[128:131]\n"
"ds_read_b64_tr_b16 v[182:183], v230 offset:2048\n"
"v_lshlrev_b32 v204, 16, v86\n"
"v_mfma_f32_16x16x32_bf16 v[128:131], v[104:107], a[104:107], v[128:131]\n"
"ds_read_b64_tr_b16 v[186:187], v234 offset:2048\n"
"v_and_b32 v86, s73, v86\n"
"v_mfma_f32_16x16x32_bf16 v[128:131], v[108:111], a[108:111], v[128:131]\n"
"ds_read_b64_tr_b16 v[190:191], v230 offset:10240\n"
"v_add_f32 v29, v29, v86\n"
"v_mfma_f32_16x16x32_bf16 v[128:131], v[112:115], a[144:147], v[128:131]\n"
"s_waitcnt vmcnt(6)\n"
"ds_read_b64_tr_b16 v[194:195], v234 offset:10240\n"
"v_lshlrev_b32 v205, 16, v87\n"
"v_mfma_f32_16x16x32_bf16 v[128:131], v[116:119], a[148:151], v[128:131]\n"
"v_subrev_u32 v200, s21, v200\n"
"ds_read_b64_tr_b16 v[210:211], v230 offset:18432\n"
"v_mfma_f32_16x16x32_bf16 v[128:131], v[120:123], a[152:155], v[128:131]\n"
"v_cmp_lt_u32 vcc, s69, v200\n"
"ds_read_b64_tr_b16 v[214:215], v234 offset:18432\n"
"v_mfma_f32_16x16x32_bf16 v[128:131], v[224:227], a[156:159], v[128:131]\n"
"ds_read_b64_tr_b16 v[218:219], v230 offset:26624\n"
"s_cbranch_vccnz LGP_%=\nLGPK_%=:\n"
"v_mfma_f32_16x16x32_bf16 v[144:147], v[224:227], a[172:175], v[196:199]\n"
"s_sub_u32 s72, s72, 1\n"
"ds_read_b64_tr_b16 v[222:223], v234 offset:26624\n"
"v_and_b32 v87, s73, v87\n"
"v_mfma_f32_16x16x32_bf16 v[144:147], v[120:123], a[168:171], v[144:147]\n"
"s_and_b32 s72, s72, 7\n"
"v_add_f32 v24, v24, v202\n"
"v_add_f32 v31, v31, v87\n"
"v_mfma_f32_16x16x32_bf16 v[144:147], v[116:119], a[164:167], v[144:147]\n"
"v_add_f32 v26, v26, v203\n"
"v_lshlrev_b32 v206, 16, v88\n"
"v_mfma_f32_16x16x32_bf16 v[144:147], v[112:115], a[160:163], v[144:147]\n"
"buffer_load_dwordx4 v[72:75], v243, s[32:35], s18 offen sc1\n"
"v_mfma_f32_16x16x32_bf16 v[144:147], v[108:111], a[124:127], v[144:147]\n"
"v_and_b32 v88, s73, v88\n"
"v_add_f32 v33, v33, v88\n"
"v_mfma_f32_16x16x32_bf16 v[144:147], v[104:107], a[120:123], v[144:147]\n"
"v_lshlrev_b32 v207, 16, v89\n"
"v_and_b32 v89, s73, v89\n"
"v_mfma_f32_16x16x32_bf16 v[144:147], v[100:103], a[116:119], v[144:147]\n"
"v_add_f32 v35, v35, v89\n"
"v_lshlrev_b32 v202, 16, v90\n"
"v_mfma_f32_16x16x32_bf16 v[144:147], v[96:99], a[112:115], v[144:147]\n"
"buffer_store_dwordx4 v[148:151], v243, s[32:35], s16 offen offset:3072 sc0 sc1\n"
"v_and_b32 v90, s73, v90\n"
"v_mfma_f32_16x16x32_bf16 v[160:163], v[96:99], a[128:131], v[196:199]\n"
"v_add_f32 v37, v37, v90\n"
"v_lshlrev_b32 v203, 16, v91\n"
"v_mfma_f32_16x16x32_bf16 v[160:163], v[100:103], a[132:135], v[160:163]\n"
"v_and_b32 v91, s73, v91\n"
"v_add_f32 v39, v39, v91\n"
"v_mfma_f32_16x16x32_bf16 v[160:163], v[104:107], a[136:139], v[160:163]\n"
"v_add_f32 v28, v28, v204\n"
"v_add_f32 v30, v30, v205\n"
"v_mfma_f32_16x16x32_bf16 v[160:163], v[108:111], a[140:143], v[160:163]\n"
"v_add_f32 v32, v32, v206\n"
"v_add_f32 v34, v34, v207\n"
"v_mfma_f32_16x16x32_bf16 v[160:163], v[112:115], a[176:179], v[160:163]\n"
"v_lshlrev_b32 v204, 16, v92\n"
"v_and_b32 v92, s73, v92\n"
"v_mfma_f32_16x16x32_bf16 v[160:163], v[116:119], a[180:183], v[160:163]\n"
"v_add_f32 v41, v41, v92\n"
"v_lshlrev_b32 v205, 16, v93\n"
"v_mfma_f32_16x16x32_bf16 v[160:163], v[120:123], a[184:187], v[160:163]\n"
"s_add_u32 s40, s41, s53\n"
"v_and_b32 v93, s73, v93\n"
"v_add_f32 v43, v43, v93\n"
"v_mfma_f32_16x16x32_bf16 v[160:163], v[224:227], a[188:191], v[160:163]\n"
"buffer_load_dwordx4 v240, s[28:31], s40 offen offset:2048 lds\n"
"v_mfma_f32_16x16x32_bf16 v[176:179], v[224:227], a[252:255], v[196:199]\n"
"ds_read_b64_tr_b16 v[224:225], v235 offset:24576\n"
"v_lshlrev_b32 v206, 16, v94\n"
"v_mfma_f32_16x16x32_bf16 v[176:179], v[120:123], a[248:251], v[176:179]\n"
"ds_read_b64_tr_b16 v[120:121], v231 offset:24576\n"
"v_and_b32 v94, s73, v94\n"
"v_mfma_f32_16x16x32_bf16 v[176:179], v[116:119], a[244:247], v[176:179]\n"
"ds_read_b64_tr_b16 v[116:117], v235 offset:16384\n"
"v_add_f32 v45, v45, v94\n"
"v_mfma_f32_16x16x32_bf16 v[176:179], v[112:115], a[240:243], v[176:179]\n"
"buffer_load_dwordx4 v[76:79], v243, s[32:35], s18 offen offset:1024 sc1\n"
"v_mfma_f32_16x16x32_bf16 v[176:179], v[108:111], a[204:207], v[176:179]\n"
"ds_read_b64_tr_b16 v[108:109], v235 offset:8192\n"
"v_lshlrev_b32 v207, 16, v95\n"
"v_mfma_f32_16x16x32_bf16 v[176:179], v[104:107], a[200:203], v[176:179]\n"
"ds_read_b64_tr_b16 v[104:105], v231 offset:8192\n"
"v_and_b32 v95, s73, v95\n"
"v_mfma_f32_16x16x32_bf16 v[176:179], v[100:103], a[196:199], v[176:179]\n"
"s_add_u32 s40, s41, s54\n"
"ds_read_b64_tr_b16 v[100:101], v235 offset:0\n"
"v_add_f32 v47, v47, v95\n"
"v_mfma_f32_16x16x32_bf16 v[176:179], v[96:99], a[192:195], v[176:179]\n"
"s_waitcnt lgkmcnt(6)\n"
"buffer_load_dwordx4 v240, s[28:31], s40 offen offset:3072 lds\n"
"v_mfma_f32_16x16x32_bf16 v[132:135], v[180:183], a[96:99], v[196:199]\n"
"ds_read_b64_tr_b16 v[96:97], v231 offset:0\n"
"v_add_f32 v36, v36, v202\n"
"v_mfma_f32_16x16x32_bf16 v[132:135], v[184:187], a[100:103], v[132:135]\n"
"ds_read_b64_tr_b16 v[98:99], v231 offset:2048\n"
"v_add_f32 v38, v38, v203\n"
"v_mfma_f32_16x16x32_bf16 v[132:135], v[188:191], a[104:107], v[132:135]\n"
"ds_read_b64_tr_b16 v[102:103], v235 offset:2048\n"
"v_add_f32 v44, v44, v206\n"
"v_mfma_f32_16x16x32_bf16 v[132:135], v[192:195], a[108:111], v[132:135]\n"
"ds_read_b64_tr_b16 v[106:107], v231 offset:10240\n"
"v_add_f32 v46, v46, v207\n"
"v_mfma_f32_16x16x32_bf16 v[132:135], v[208:211], a[144:147], v[132:135]\n"
"ds_read_b64_tr_b16 v[110:111], v235 offset:10240\n"
"v_add_f32 v40, v40, v204\n"
"v_mfma_f32_16x16x32_bf16 v[132:135], v[212:215], a[148:151], v[132:135]\n"
"ds_read_b64_tr_b16 v[112:113], v231 offset:16384\n"
"v_add_f32 v42, v42, v205\n"
"v_mfma_f32_16x16x32_bf16 v[132:135], v[216:219], a[152:155], v[132:135]\n"
"ds_read_b64_tr_b16 v[114:115], v231 offset:18432\n"
"v_mfma_f32_16x16x32_bf16 v[132:135], v[220:223], a[156:159], v[132:135]\n"
"buffer_store_dwordx4 v[164:167], v243, s[32:35], s17 offen offset:1024 sc0 sc1\n"
"v_lshlrev_b32 v206, 16, v244\n"
"v_mfma_f32_16x16x32_bf16 v[148:151], v[220:223], a[172:175], v[196:199]\n"
"ds_read_b64_tr_b16 v[118:119], v235 offset:18432\n"
"v_and_b32 v244, s73, v244\n"
"v_mfma_f32_16x16x32_bf16 v[148:151], v[216:219], a[168:171], v[148:151]\n"
"ds_read_b64_tr_b16 v[122:123], v231 offset:26624\n"
"v_add_f32 v49, v49, v244\n"
"v_mfma_f32_16x16x32_bf16 v[148:151], v[212:215], a[164:167], v[148:151]\n"
"ds_read_b64_tr_b16 v[226:227], v235 offset:26624\n"
"v_lshlrev_b32 v207, 16, v245\n"
"v_mfma_f32_16x16x32_bf16 v[148:151], v[208:211], a[160:163], v[148:151]\n"
"buffer_load_dwordx4 v[80:83], v243, s[32:35], s18 offen offset:2048 sc1\n"
"v_mfma_f32_16x16x32_bf16 v[148:151], v[192:195], a[124:127], v[148:151]\n"
"v_and_b32 v245, s73, v245\n"
"v_add_f32 v51, v51, v245\n"
"v_mfma_f32_16x16x32_bf16 v[148:151], v[188:191], a[120:123], v[148:151]\n"
"s_add_u32 s41, s70, 163840\n"
"v_lshlrev_b32 v202, 16, v246\n"
"v_and_b32 v246, s73, v246\n"
"v_mfma_f32_16x16x32_bf16 v[148:151], v[184:187], a[116:119], v[148:151]\n"
"s_add_u32 m0, s71, 16384\n"
"v_add_f32 v53, v53, v246\n"
"v_lshlrev_b32 v203, 16, v247\n"
"v_mfma_f32_16x16x32_bf16 v[148:151], v[180:183], a[112:115], v[148:151]\n"
"buffer_load_dwordx4 v240, s[28:31], s41 offen lds\n"
"v_mfma_f32_16x16x32_bf16 v[164:167], v[180:183], a[128:131], v[196:199]\n"
"v_and_b32 v247, s73, v247\n"
"v_add_f32 v55, v55, v247\n"
"v_mfma_f32_16x16x32_bf16 v[164:167], v[184:187], a[132:135], v[164:167]\n"
"v_add_f32 v48, v48, v206\n"
"v_add_f32 v50, v50, v207\n"
"v_mfma_f32_16x16x32_bf16 v[164:167], v[188:191], a[136:139], v[164:167]\n"
"v_add_f32 v52, v52, v202\n"
"v_add_f32 v54, v54, v203\n"
"v_mfma_f32_16x16x32_bf16 v[164:167], v[192:195], a[140:143], v[164:167]\n"
"v_mfma_f32_16x16x32_bf16 v[164:167], v[208:211], a[176:179], v[164:167]\n"
"v_mfma_f32_16x16x32_bf16 v[164:167], v[212:215], a[180:183], v[164:167]\n"
"v_mfma_f32_16x16x32_bf16 v[164:167], v[216:219], a[184:187], v[164:167]\n"
"s_waitcnt vmcnt(12)\n"
"v_mfma_f32_16x16x32_bf16 v[164:167], v[220:223], a[188:191], v[164:167]\n"
"s_barrier\n"
"s_waitcnt lgkmcnt(0)\n"
"v_mfma_f32_16x16x32_bf16 v[136:139], v[96:99], a[96:99], v[196:199]\n"
"ds_read_b64_tr_b16 v[180:181], v228 offset:32768\n"
"v_mfma_f32_16x16x32_bf16 v[136:139], v[100:103], a[100:103], v[136:139]\n"
"ds_read_b64_tr_b16 v[182:183], v228 offset:34816\n"
"v_mfma_f32_16x16x32_bf16 v[136:139], v[104:107], a[104:107], v[136:139]\n"
"ds_read_b64_tr_b16 v[184:185], v232 offset:32768\n"
"v_mfma_f32_16x16x32_bf16 v[136:139], v[108:111], a[108:111], v[136:139]\n"
"ds_read_b64_tr_b16 v[186:187], v232 offset:34816\n"
"v_mfma_f32_16x16x32_bf16 v[136:139], v[112:115], a[144:147], v[136:139]\n"
"ds_read_b64_tr_b16 v[188:189], v228 offset:40960\n"
"v_mfma_f32_16x16x32_bf16 v[136:139], v[116:119], a[148:151], v[136:139]\n"
"ds_read_b64_tr_b16 v[190:191], v228 offset:43008\n"
"v_mfma_f32_16x16x32_bf16 v[136:139], v[120:123], a[152:155], v[136:139]\n"
"s_add_u32 s40, s41, s52\n"
"ds_read_b64_tr_b16 v[192:193], v232 offset:40960\n"
"v_mfma_f32_16x16x32_bf16 v[136:139], v[224:227], a[156:159], v[136:139]\n"
"buffer_load_dwordx4 v240, s[28:31], s40 offen offset:1024 lds\n"
"v_mfma_f32_16x16x32_bf16 v[152:155], v[224:227], a[172:175], v[196:199]\n"
"ds_read_b64_tr_b16 v[194:195], v232 offset:43008\n"
"v_mfma_f32_16x16x32_bf16 v[152:155], v[120:123], a[168:171], v[152:155]\n"
"ds_read_b64_tr_b16 v[208:209], v228 offset:49152\n"
"v_mfma_f32_16x16x32_bf16 v[152:155], v[116:119], a[164:167], v[152:155]\n"
"ds_read_b64_tr_b16 v[210:211], v228 offset:51200\n"
"v_mfma_f32_16x16x32_bf16 v[152:155], v[112:115], a[160:163], v[152:155]\n"
"ds_read_b64_tr_b16 v[212:213], v232 offset:49152\n"
"v_mfma_f32_16x16x32_bf16 v[152:155], v[108:111], a[124:127], v[152:155]\n"
"ds_read_b64_tr_b16 v[214:215], v232 offset:51200\n"
"v_mfma_f32_16x16x32_bf16 v[152:155], v[104:107], a[120:123], v[152:155]\n"
"ds_read_b64_tr_b16 v[216:217], v228 offset:57344\n"
"v_mfma_f32_16x16x32_bf16 v[152:155], v[100:103], a[116:119], v[152:155]\n"
"s_add_u32 s40, s41, s53\n"
"ds_read_b64_tr_b16 v[218:219], v228 offset:59392\n"
"v_xor_b32 v228, s75, v228\n"
"v_mfma_f32_16x16x32_bf16 v[152:155], v[96:99], a[112:115], v[152:155]\n"
"buffer_load_dwordx4 v240, s[28:31], s40 offen offset:2048 lds\n"
"v_mfma_f32_16x16x32_bf16 v[168:171], v[96:99], a[128:131], v[196:199]\n"
"ds_read_b64_tr_b16 v[220:221], v232 offset:57344\n"
"v_mfma_f32_16x16x32_bf16 v[168:171], v[100:103], a[132:135], v[168:171]\n"
"ds_read_b64_tr_b16 v[222:223], v232 offset:59392\n"
"v_xor_b32 v232, s75, v232\n"
"v_mfma_f32_16x16x32_bf16 v[168:171], v[104:107], a[136:139], v[168:171]\n"
"ds_read_b64_tr_b16 v[96:97], v229 offset:32768\n"
"v_mfma_f32_16x16x32_bf16 v[168:171], v[108:111], a[140:143], v[168:171]\n"
"ds_read_b64_tr_b16 v[98:99], v229 offset:34816\n"
"v_mfma_f32_16x16x32_bf16 v[168:171], v[112:115], a[176:179], v[168:171]\n"
"ds_read_b64_tr_b16 v[100:101], v233 offset:32768\n"
"v_mfma_f32_16x16x32_bf16 v[168:171], v[116:119], a[180:183], v[168:171]\n"
"ds_read_b64_tr_b16 v[102:103], v233 offset:34816\n"
"v_mfma_f32_16x16x32_bf16 v[168:171], v[120:123], a[184:187], v[168:171]\n"
"s_add_u32 s40, s41, s54\n"
"ds_read_b64_tr_b16 v[104:105], v229 offset:40960\n"
"v_mfma_f32_16x16x32_bf16 v[168:171], v[224:227], a[188:191], v[168:171]\n"
"s_waitcnt lgkmcnt(7)\n"
"buffer_load_dwordx4 v240, s[28:31], s40 offen offset:3072 lds\n"
"v_mfma_f32_16x16x32_bf16 v[124:127], v[180:183], a[48:51], v[124:127]\n"
"ds_read_b64_tr_b16 v[106:107], v229 offset:43008\n"
"v_mfma_f32_16x16x32_bf16 v[124:127], v[184:187], a[52:55], v[124:127]\n"
"ds_read_b64_tr_b16 v[108:109], v233 offset:40960\n"
"v_mfma_f32_16x16x32_bf16 v[124:127], v[188:191], a[56:59], v[124:127]\n"
"ds_read_b64_tr_b16 v[110:111], v233 offset:43008\n"
"v_mfma_f32_16x16x32_bf16 v[124:127], v[192:195], a[60:63], v[124:127]\n"
"ds_read_b64_tr_b16 v[112:113], v229 offset:49152\n"
"v_mfma_f32_16x16x32_bf16 v[124:127], v[208:211], a[0:3], v[124:127]\n"
"ds_read_b64_tr_b16 v[114:115], v229 offset:51200\n"
"v_mfma_f32_16x16x32_bf16 v[124:127], v[212:215], a[4:7], v[124:127]\n"
"s_add_u32 s41, s70, 327680\n"
"ds_read_b64_tr_b16 v[116:117], v233 offset:49152\n"
"v_mfma_f32_16x16x32_bf16 v[124:127], v[216:219], a[8:11], v[124:127]\n"
"s_waitcnt lgkmcnt(6)\n"
"s_add_u32 m0, s71, 32768\n"
"ds_read_b64_tr_b16 v[118:119], v233 offset:51200\n"
"v_mfma_f32_16x16x32_bf16 v[124:127], v[220:223], a[12:15], v[124:127]\n"
"buffer_load_dwordx4 v240, s[28:31], s41 offen lds\n"
"v_mfma_f32_16x16x32_bf16 v[140:143], v[220:223], a[28:31], v[140:143]\n"
"ds_read_b64_tr_b16 v[120:121], v229 offset:57344\n"
"v_mfma_f32_16x16x32_bf16 v[140:143], v[216:219], a[24:27], v[140:143]\n"
"ds_read_b64_tr_b16 v[122:123], v229 offset:59392\n"
"v_xor_b32 v229, s75, v229\n"
"v_mfma_f32_16x16x32_bf16 v[140:143], v[212:215], a[20:23], v[140:143]\n"
"ds_read_b64_tr_b16 v[224:225], v233 offset:57344\n"
"v_mfma_f32_16x16x32_bf16 v[140:143], v[208:211], a[16:19], v[140:143]\n"
"ds_read_b64_tr_b16 v[226:227], v233 offset:59392\n"
"v_xor_b32 v233, s75, v233\n"
"v_mfma_f32_16x16x32_bf16 v[140:143], v[192:195], a[76:79], v[140:143]\n"
"v_mfma_f32_16x16x32_bf16 v[140:143], v[188:191], a[72:75], v[140:143]\n"
"v_subrev_f32 v124, s22, v124\n"
"v_subrev_f32 v125, s22, v125\n"
"v_mfma_f32_16x16x32_bf16 v[140:143], v[184:187], a[68:71], v[140:143]\n"
"s_add_u32 s40, s41, s52\n"
"v_subrev_f32 v126, s22, v126\n"
"v_subrev_f32 v127, s22, v127\n"
"v_mfma_f32_16x16x32_bf16 v[140:143], v[180:183], a[64:67], v[140:143]\n"
"buffer_load_dwordx4 v240, s[28:31], s40 offen offset:1024 lds\n"
"v_mfma_f32_16x16x32_bf16 v[156:159], v[180:183], a[80:83], v[156:159]\n"
"v_cvt_pk_bf16_f32 v124, v124, v125\n"
"v_cvt_pk_bf16_f32 v125, v126, v127\n"
"v_mfma_f32_16x16x32_bf16 v[156:159], v[184:187], a[84:87], v[156:159]\n"
"v_mfma_f32_16x16x32_bf16 v[156:159], v[188:191], a[88:91], v[156:159]\n"
"v_mfma_f32_16x16x32_bf16 v[156:159], v[192:195], a[92:95], v[156:159]\n"
"v_mfma_f32_16x16x32_bf16 v[156:159], v[208:211], a[32:35], v[156:159]\n"
"v_mfma_f32_16x16x32_bf16 v[156:159], v[212:215], a[36:39], v[156:159]\n"
"v_subrev_f32 v140, s22, v140\n"
"v_subrev_f32 v141, s22, v141\n"
"v_mfma_f32_16x16x32_bf16 v[156:159], v[216:219], a[40:43], v[156:159]\n"
"s_waitcnt vmcnt(7)\n"
"v_subrev_f32 v142, s22, v142\n"
"v_subrev_f32 v143, s22, v143\n"
"v_mfma_f32_16x16x32_bf16 v[156:159], v[220:223], a[44:47], v[156:159]\n"
"s_barrier\n"
"buffer_store_dword v241, v248, s[84:87], 0 offen sc1\n"
"v_mfma_f32_16x16x32_bf16 v[172:175], v[220:223], a[228:231], v[172:175]\n"
"ds_read_b64_tr_b16 v[220:221], v234 offset:57344\n"
"v_cvt_pk_bf16_f32 v140, v140, v141\n"
"v_mfma_f32_16x16x32_bf16 v[172:175], v[216:219], a[224:227], v[172:175]\n"
"ds_read_b64_tr_b16 v[216:217], v230 offset:57344\n"
"v_cvt_pk_bf16_f32 v141, v142, v143\n"
"v_mfma_f32_16x16x32_bf16 v[172:175], v[212:215], a[212:215], v[172:175]\n"
"ds_read_b64_tr_b16 v[212:213], v234 offset:49152\n"
"v_add_u32 v241, 1, v241\n"
"v_mfma_f32_16x16x32_bf16 v[172:175], v[208:211], a[208:211], v[172:175]\n"
"s_add_u32 s16, s90, s62\n"
"buffer_load_dwordx4 v[84:87], v243, s[32:35], s18 offen offset:3072 sc1\n"
"v_mfma_f32_16x16x32_bf16 v[172:175], v[192:195], a[236:239], v[172:175]\n"
"s_add_u32 s17, s16, 4096\n"
"ds_read_b64_tr_b16 v[192:193], v234 offset:40960\n"
"v_mfma_f32_16x16x32_bf16 v[172:175], v[188:191], a[232:235], v[172:175]\n"
"s_lshl_b32 s62, s70, 9\n"
"ds_read_b64_tr_b16 v[188:189], v230 offset:40960\n"
"v_subrev_f32 v156, s22, v156\n"
"v_mfma_f32_16x16x32_bf16 v[172:175], v[184:187], a[220:223], v[172:175]\n"
"s_add_u32 s40, s41, s53\n"
"ds_read_b64_tr_b16 v[184:185], v234 offset:32768\n"
"v_subrev_f32 v157, s22, v157\n"
"v_mfma_f32_16x16x32_bf16 v[172:175], v[180:183], a[216:219], v[172:175]\n"
"buffer_load_dwordx4 v240, s[28:31], s40 offen offset:2048 lds\n"
"v_mfma_f32_16x16x32_bf16 v[128:131], v[96:99], a[48:51], v[128:131]\n"
"ds_read_b64_tr_b16 v[180:181], v230 offset:32768\n"
"v_subrev_f32 v158, s22, v158\n"
"v_mfma_f32_16x16x32_bf16 v[128:131], v[100:103], a[52:55], v[128:131]\n"
"s_waitcnt lgkmcnt(5)\n"
"ds_read_b64_tr_b16 v[182:183], v230 offset:34816\n"
"v_subrev_f32 v159, s22, v159\n"
"v_mfma_f32_16x16x32_bf16 v[128:131], v[104:107], a[56:59], v[128:131]\n"
"ds_read_b64_tr_b16 v[186:187], v234 offset:34816\n"
"v_cvt_pk_bf16_f32 v156, v156, v157\n"
"v_mfma_f32_16x16x32_bf16 v[128:131], v[108:111], a[60:63], v[128:131]\n"
"ds_read_b64_tr_b16 v[190:191], v230 offset:43008\n"
"v_cvt_pk_bf16_f32 v157, v158, v159\n"
"v_mfma_f32_16x16x32_bf16 v[128:131], v[112:115], a[0:3], v[128:131]\n"
"ds_read_b64_tr_b16 v[194:195], v234 offset:43008\n"
"v_lshlrev_b32 v202, 16, v72\n"
"v_mfma_f32_16x16x32_bf16 v[128:131], v[116:119], a[4:7], v[128:131]\n"
"ds_read_b64_tr_b16 v[208:209], v230 offset:49152\n"
"v_and_b32 v72, s73, v72\n"
"v_mfma_f32_16x16x32_bf16 v[128:131], v[120:123], a[8:11], v[128:131]\n"
"s_add_u32 s40, s41, s54\n"
"ds_read_b64_tr_b16 v[210:211], v230 offset:51200\n"
"v_add_f32 v0, v0, v202\n"
"v_mfma_f32_16x16x32_bf16 v[128:131], v[224:227], a[12:15], v[128:131]\n"
"buffer_load_dwordx4 v240, s[28:31], s40 offen offset:3072 lds\n"
"v_mfma_f32_16x16x32_bf16 v[144:147], v[224:227], a[28:31], v[144:147]\n"
"ds_read_b64_tr_b16 v[214:215], v234 offset:51200\n"
"v_add_f32 v1, v1, v72\n"
"v_mfma_f32_16x16x32_bf16 v[144:147], v[120:123], a[24:27], v[144:147]\n"
"ds_read_b64_tr_b16 v[218:219], v230 offset:59392\n"
"v_lshlrev_b32 v203, 16, v73\n"
"v_mfma_f32_16x16x32_bf16 v[144:147], v[116:119], a[20:23], v[144:147]\n"
"ds_read_b64_tr_b16 v[222:223], v234 offset:59392\n"
"v_and_b32 v73, s73, v73\n"
"v_mfma_f32_16x16x32_bf16 v[144:147], v[112:115], a[16:19], v[144:147]\n"
"buffer_load_dwordx4 v[88:91], v243, s[32:35], s19 offen sc1\n"
"v_mfma_f32_16x16x32_bf16 v[144:147], v[108:111], a[76:79], v[144:147]\n"
"v_add_f32 v2, v2, v203\n"
"v_add_f32 v3, v3, v73\n"
"v_mfma_f32_16x16x32_bf16 v[144:147], v[104:107], a[72:75], v[144:147]\n"
"s_add_u32 s41, s70, 491520\n"
"v_subrev_f32 v128, s22, v128\n"
"v_subrev_f32 v129, s22, v129\n"
"v_mfma_f32_16x16x32_bf16 v[144:147], v[100:103], a[68:71], v[144:147]\n"
"s_add_u32 m0, s71, 49152\n"
"v_subrev_f32 v130, s22, v130\n"
"v_subrev_f32 v131, s22, v131\n"
"v_mfma_f32_16x16x32_bf16 v[144:147], v[96:99], a[64:67], v[144:147]\n"
"buffer_load_dwordx4 v240, s[28:31], s41 offen lds\n"
"v_mfma_f32_16x16x32_bf16 v[160:163], v[96:99], a[80:83], v[160:163]\n"
"s_and_b32 s70, s88, 15\n"
"v_cvt_pk_bf16_f32 v126, v128, v129\n"
"v_cvt_pk_bf16_f32 v127, v130, v131\n"
"v_mfma_f32_16x16x32_bf16 v[160:163], v[100:103], a[84:87], v[160:163]\n"
"s_lshr_b32 s88, s88, 4\n"
"v_lshlrev_b32 v204, 16, v74\n"
"v_and_b32 v74, s73, v74\n"
"v_mfma_f32_16x16x32_bf16 v[160:163], v[104:107], a[88:91], v[160:163]\n"
"s_lshl_b32 s70, s70, 7\n"
"v_add_f32 v4, v4, v204\n"
"v_add_f32 v5, v5, v74\n"
"v_mfma_f32_16x16x32_bf16 v[160:163], v[108:111], a[92:95], v[160:163]\n"
"s_xor_b32 s71, s71, s75\n"
"v_lshlrev_b32 v205, 16, v75\n"
"v_and_b32 v75, s73, v75\n"
"v_mfma_f32_16x16x32_bf16 v[160:163], v[112:115], a[32:35], v[160:163]\n"
"v_add_f32 v6, v6, v205\n"
"v_add_f32 v7, v7, v75\n"
"v_mfma_f32_16x16x32_bf16 v[160:163], v[116:119], a[36:39], v[160:163]\n"
"v_subrev_f32 v144, s22, v144\n"
"v_subrev_f32 v145, s22, v145\n"
"v_mfma_f32_16x16x32_bf16 v[160:163], v[120:123], a[40:43], v[160:163]\n"
"v_subrev_f32 v146, s22, v146\n"
"v_subrev_f32 v147, s22, v147\n"
"v_mfma_f32_16x16x32_bf16 v[160:163], v[224:227], a[44:47], v[160:163]\n"
"buffer_store_dwordx4 v[124:127], v243, s[32:35], s16 offen sc0 sc1\n"
"v_cvt_pk_bf16_f32 v142, v144, v145\n"
"v_mfma_f32_16x16x32_bf16 v[176:179], v[224:227], a[228:231], v[176:179]\n"
"ds_read_b64_tr_b16 v[224:225], v235 offset:57344\n"
"v_cvt_pk_bf16_f32 v143, v146, v147\n"
"v_mfma_f32_16x16x32_bf16 v[176:179], v[120:123], a[224:227], v[176:179]\n"
"ds_read_b64_tr_b16 v[120:121], v231 offset:57344\n"
"v_lshlrev_b32 v206, 16, v76\n"
"v_mfma_f32_16x16x32_bf16 v[176:179], v[116:119], a[212:215], v[176:179]\n"
"ds_read_b64_tr_b16 v[116:117], v235 offset:49152\n"
"v_and_b32 v76, s73, v76\n"
"v_mfma_f32_16x16x32_bf16 v[176:179], v[112:115], a[208:211], v[176:179]\n"
"buffer_load_dwordx4 v[92:95], v243, s[32:35], s19 offen offset:1024 sc1\n"
"v_mfma_f32_16x16x32_bf16 v[176:179], v[108:111], a[236:239], v[176:179]\n"
"ds_read_b64_tr_b16 v[108:109], v235 offset:40960\n"
"v_add_f32 v8, v8, v206\n"
"v_mfma_f32_16x16x32_bf16 v[176:179], v[104:107], a[232:235], v[176:179]\n"
"ds_read_b64_tr_b16 v[104:105], v231 offset:40960\n"
"v_subrev_f32 v160, s22, v160\n"
"v_mfma_f32_16x16x32_bf16 v[176:179], v[100:103], a[220:223], v[176:179]\n"
"s_add_u32 s40, s41, s52\n"
"ds_read_b64_tr_b16 v[100:101], v235 offset:32768\n"
"v_subrev_f32 v161, s22, v161\n"
"v_mfma_f32_16x16x32_bf16 v[176:179], v[96:99], a[216:219], v[176:179]\n"
"s_waitcnt vmcnt(14) lgkmcnt(6)\n"
"buffer_load_dwordx4 v240, s[28:31], s40 offen offset:1024 lds\n"
"v_mfma_f32_16x16x32_bf16 v[132:135], v[180:183], a[48:51], v[132:135]\n"
"ds_read_b64_tr_b16 v[96:97], v231 offset:32768\n"
"v_subrev_f32 v162, s22, v162\n"
"v_mfma_f32_16x16x32_bf16 v[132:135], v[184:187], a[52:55], v[132:135]\n"
"ds_read_b64_tr_b16 v[98:99], v231 offset:34816\n"
"v_subrev_f32 v163, s22, v163\n"
"v_mfma_f32_16x16x32_bf16 v[132:135], v[188:191], a[56:59], v[132:135]\n"
"ds_read_b64_tr_b16 v[102:103], v235 offset:34816\n"
"v_cvt_pk_bf16_f32 v158, v160, v161\n"
"v_mfma_f32_16x16x32_bf16 v[132:135], v[192:195], a[60:63], v[132:135]\n"
"ds_read_b64_tr_b16 v[106:107], v231 offset:43008\n"
"v_cvt_pk_bf16_f32 v159, v162, v163\n"
"v_mfma_f32_16x16x32_bf16 v[132:135], v[208:211], a[0:3], v[132:135]\n"
"ds_read_b64_tr_b16 v[110:111], v235 offset:43008\n"
"v_add_f32 v9, v9, v76\n"
"v_mfma_f32_16x16x32_bf16 v[132:135], v[212:215], a[4:7], v[132:135]\n"
"ds_read_b64_tr_b16 v[112:113], v231 offset:49152\n"
"v_lshlrev_b32 v207, 16, v77\n"
"v_mfma_f32_16x16x32_bf16 v[132:135], v[216:219], a[8:11], v[132:135]\n"
"ds_read_b64_tr_b16 v[114:115], v231 offset:51200\n"
"v_and_b32 v77, s73, v77\n"
"v_mfma_f32_16x16x32_bf16 v[132:135], v[220:223], a[12:15], v[132:135]\n"
"buffer_store_dwordx4 v[140:143], v243, s[32:35], s16 offen offset:2048 sc0 sc1\n"
"v_add_f32 v10, v10, v207\n"
"v_mfma_f32_16x16x32_bf16 v[148:151], v[220:223], a[28:31], v[148:151]\n"
"ds_read_b64_tr_b16 v[118:119], v235 offset:51200\n"
"v_add_f32 v11, v11, v77\n"
"v_mfma_f32_16x16x32_bf16 v[148:151], v[216:219], a[24:27], v[148:151]\n"
"ds_read_b64_tr_b16 v[122:123], v231 offset:59392\n"
"v_lshlrev_b32 v202, 16, v78\n"
"v_mfma_f32_16x16x32_bf16 v[148:151], v[212:215], a[20:23], v[148:151]\n"
"ds_read_b64_tr_b16 v[226:227], v235 offset:59392\n"
"v_and_b32 v78, s73, v78\n"
"v_mfma_f32_16x16x32_bf16 v[148:151], v[208:211], a[16:19], v[148:151]\n"
"v_add_f32 v12, v12, v202\n"
"v_add_f32 v13, v13, v78\n"
"v_mfma_f32_16x16x32_bf16 v[148:151], v[192:195], a[76:79], v[148:151]\n"
"v_lshlrev_b32 v203, 16, v79\n"
"v_and_b32 v79, s73, v79\n"
"v_mfma_f32_16x16x32_bf16 v[148:151], v[188:191], a[72:75], v[148:151]\n"
"v_add_f32 v14, v14, v203\n"
"v_add_f32 v15, v15, v79\n"
"v_mfma_f32_16x16x32_bf16 v[148:151], v[184:187], a[68:71], v[148:151]\n"
"s_add_u32 s40, s41, s53\n"
"v_lshlrev_b32 v204, 16, v80\n"
"v_and_b32 v80, s73, v80\n"
"v_mfma_f32_16x16x32_bf16 v[148:151], v[180:183], a[64:67], v[148:151]\n"
"buffer_load_dwordx4 v240, s[28:31], s40 offen offset:2048 lds\n"
"v_mfma_f32_16x16x32_bf16 v[164:167], v[180:183], a[80:83], v[164:167]\n"
"v_add_f32 v16, v16, v204\n"
"v_add_f32 v17, v17, v80\n"
"v_mfma_f32_16x16x32_bf16 v[164:167], v[184:187], a[84:87], v[164:167]\n"
"v_lshlrev_b32 v205, 16, v81\n"
"v_and_b32 v81, s73, v81\n"
"v_mfma_f32_16x16x32_bf16 v[164:167], v[188:191], a[88:91], v[164:167]\n"
"v_add_f32 v18, v18, v205\n"
"v_add_f32 v19, v19, v81\n"
"v_mfma_f32_16x16x32_bf16 v[164:167], v[192:195], a[92:95], v[164:167]\n"
"buffer_load_dwordx4 v[244:247], v243, s[32:35], s19 offen offset:2048 sc1\n"
"v_mfma_f32_16x16x32_bf16 v[164:167], v[208:211], a[32:35], v[164:167]\n"
"s_lshl4_add_u32 s18, s72, s61\n"
"v_lshlrev_b32 v206, 16, v82\n"
"v_and_b32 v82, s73, v82\n"
"v_mfma_f32_16x16x32_bf16 v[164:167], v[212:215], a[36:39], v[164:167]\n"
"s_lshl_b32 s19, s72, 2\n"
"v_add_f32 v20, v20, v206\n"
"v_add_f32 v21, v21, v82\n"
"v_mfma_f32_16x16x32_bf16 v[164:167], v[216:219], a[40:43], v[164:167]\n"
"s_waitcnt vmcnt(14)\n"
"s_lshr_b32 s19, s89, s19\n"
"v_lshlrev_b32 v207, 16, v83\n"
"v_mfma_f32_16x16x32_bf16 v[164:167], v[220:223], a[44:47], v[164:167]\n"
"s_barrier\n"
"s_and_b32 s19, s19, 1\n"
"s_waitcnt lgkmcnt(0)\n"
"v_mfma_f32_16x16x32_bf16 v[136:139], v[96:99], a[48:51], v[136:139]\n"
"s_lshl3_add_u32 s18, s18, s19\n"
"ds_read_b64_tr_b16 v[180:181], v228 offset:0\n"
"v_and_b32 v83, s73, v83\n"
"v_mfma_f32_16x16x32_bf16 v[136:139], v[100:103], a[52:55], v[136:139]\n"
"s_lshl_b32 s18, s18, 13\n"
"ds_read_b64_tr_b16 v[182:183], v228 offset:2048\n"
"v_add_f32 v22, v22, v207\n"
"v_mfma_f32_16x16x32_bf16 v[136:139], v[104:107], a[56:59], v[136:139]\n"
"s_add_u32 s19, s18, 4096\n"
"v_lshlrev_b32_e64 v201, 2, s72\n"
"ds_read_b64_tr_b16 v[184:185], v232 offset:0\n"
"v_mfma_f32_16x16x32_bf16 v[136:139], v[108:111], a[60:63], v[136:139]\n"
"s_mov_b32 s69, 15\n"
"global_load_dword v200, v201, s[6:7] sc1\n"
"v_mfma_f32_16x16x32_bf16 v[136:139], v[112:115], a[0:3], v[136:139]\n"
"s_add_u32 s21, s21, 1\n"
"ds_read_b64_tr_b16 v[186:187], v232 offset:2048\n"
"v_add_f32 v23, v23, v83\n"
"v_mfma_f32_16x16x32_bf16 v[136:139], v[116:119], a[4:7], v[136:139]\n"
"ds_read_b64_tr_b16 v[188:189], v228 offset:8192\n"
"v_subrev_f32 v172, s22, v172\n"
"v_mfma_f32_16x16x32_bf16 v[136:139], v[120:123], a[8:11], v[136:139]\n"
"ds_read_b64_tr_b16 v[190:191], v228 offset:10240\n"
"v_subrev_f32 v173, s22, v173\n"
"v_mfma_f32_16x16x32_bf16 v[136:139], v[224:227], a[12:15], v[136:139]\n"
"buffer_store_dwordx4 v[156:159], v243, s[32:35], s17 offen sc0 sc1\n"
"v_subrev_f32 v174, s22, v174\n"
"v_mfma_f32_16x16x32_bf16 v[152:155], v[224:227], a[28:31], v[152:155]\n"
"ds_read_b64_tr_b16 v[192:193], v232 offset:8192\n"
"v_subrev_f32 v175, s22, v175\n"
"v_mfma_f32_16x16x32_bf16 v[152:155], v[120:123], a[24:27], v[152:155]\n"
"ds_read_b64_tr_b16 v[194:195], v232 offset:10240\n"
"v_cvt_pk_bf16_f32 v172, v172, v173\n"
"v_mfma_f32_16x16x32_bf16 v[152:155], v[116:119], a[20:23], v[152:155]\n"
"ds_read_b64_tr_b16 v[208:209], v228 offset:16384\n"
"v_cvt_pk_bf16_f32 v173, v174, v175\n"
"v_mfma_f32_16x16x32_bf16 v[152:155], v[112:115], a[16:19], v[152:155]\n"
"ds_read_b64_tr_b16 v[210:211], v228 offset:18432\n"
"v_subrev_f32 v176, s22, v176\n"
"v_mfma_f32_16x16x32_bf16 v[152:155], v[108:111], a[76:79], v[152:155]\n"
"ds_read_b64_tr_b16 v[212:213], v232 offset:16384\n"
"v_subrev_f32 v177, s22, v177\n"
"v_mfma_f32_16x16x32_bf16 v[152:155], v[104:107], a[72:75], v[152:155]\n"
"ds_read_b64_tr_b16 v[214:215], v232 offset:18432\n"
"v_subrev_f32 v178, s22, v178\n"
"v_mfma_f32_16x16x32_bf16 v[152:155], v[100:103], a[68:71], v[152:155]\n"
"s_add_u32 s40, s41, s54\n"
"ds_read_b64_tr_b16 v[216:217], v228 offset:24576\n"
"v_subrev_f32 v179, s22, v179\n"
"v_mfma_f32_16x16x32_bf16 v[152:155], v[96:99], a[64:67], v[152:155]\n"
"buffer_load_dwordx4 v240, s[28:31], s40 offen offset:3072 lds\n"
"v_mfma_f32_16x16x32_bf16 v[168:171], v[96:99], a[80:83], v[168:171]\n"
"ds_read_b64_tr_b16 v[218:219], v228 offset:26624\n"
"v_cvt_pk_bf16_f32 v174, v176, v177\n"
"v_mfma_f32_16x16x32_bf16 v[168:171], v[100:103], a[84:87], v[168:171]\n"
"ds_read_b64_tr_b16 v[220:221], v232 offset:24576\n"
"v_cvt_pk_bf16_f32 v175, v178, v179\n"
"v_mfma_f32_16x16x32_bf16 v[168:171], v[104:107], a[88:91], v[168:171]\n"
"ds_read_b64_tr_b16 v[222:223], v232 offset:26624\n"
"v_subrev_f32 v132, s22, v132\n"
"v_mfma_f32_16x16x32_bf16 v[168:171], v[108:111], a[92:95], v[168:171]\n"
"ds_read_b64_tr_b16 v[96:97], v229 offset:0\n"
"v_subrev_f32 v133, s22, v133\n"
"v_mfma_f32_16x16x32_bf16 v[168:171], v[112:115], a[32:35], v[168:171]\n"
"ds_read_b64_tr_b16 v[98:99], v229 offset:2048\n"
"v_subrev_f32 v134, s22, v134\n"
"v_mfma_f32_16x16x32_bf16 v[168:171], v[116:119], a[36:39], v[168:171]\n"
"ds_read_b64_tr_b16 v[100:101], v233 offset:0\n"
"v_subrev_f32 v135, s22, v135\n"
"v_mfma_f32_16x16x32_bf16 v[168:171], v[120:123], a[40:43], v[168:171]\n"
"s_add_u32 s47, s47, 1\n"
"ds_read_b64_tr_b16 v[102:103], v233 offset:2048\n"
"v_cvt_pk_bf16_f32 v132, v132, v133\n"
"v_mfma_f32_16x16x32_bf16 v[168:171], v[224:227], a[44:47], v[168:171]\n"
"s_waitcnt lgkmcnt(7)\n"
"s_cmp_lg_u32 s47, 9\n"
"ds_read_b64_tr_b16 v[104:105], v229 offset:8192\n"
"s_cbranch_scc1 LGL_%=\n"
"v_mfma_f32_16x16x32_bf16 v[0:3], v[180:183], a[96:99], v[0:3]\n"
"v_subrev_f32 v136, s22, v136\n"
"v_subrev_f32 v138, s22, v138\n"
"v_mfma_f32_16x16x32_bf16 v[0:3], v[184:187], a[100:103], v[0:3]\n"
"v_subrev_f32 v151, s22, v151\n"
"v_subrev_f32 v139, s22, v139\n"
"v_mfma_f32_16x16x32_bf16 v[0:3], v[188:191], a[104:107], v[0:3]\n"
"v_mov_b32 v201, v237\n"
"v_cvt_pk_bf16_f32 v133, v134, v135\n"
"v_subrev_f32 v150, s22, v150\n"
"v_mfma_f32_16x16x32_bf16 v[0:3], v[192:195], a[108:111], v[0:3]\n"
"global_load_dword v200, v201, s[6:7] sc1\n"
"v_mfma_f32_16x16x32_bf16 v[0:3], v[208:211], a[144:147], v[0:3]\n"
"v_subrev_f32 v153, s22, v153\n"
"v_subrev_f32 v169, s22, v169\n"
"v_mfma_f32_16x16x32_bf16 v[0:3], v[212:215], a[148:151], v[0:3]\n"
"s_waitcnt lgkmcnt(2)\n"
"v_subrev_f32 v137, s22, v137\n"
"v_subrev_f32 v152, s22, v152\n"
"v_mfma_f32_16x16x32_bf16 v[0:3], v[216:219], a[152:155], v[0:3]\n"
"v_cvt_pk_bf16_f32 v134, v136, v137\n"
"v_subrev_f32 v168, s22, v168\n"
"v_mfma_f32_16x16x32_bf16 v[0:3], v[220:223], a[156:159], v[0:3]\n"
"buffer_store_dwordx4 v[172:175], v243, s[32:35], s17 offen offset:2048 sc0 sc1\n"
"v_cvt_pk_bf16_f32 v135, v138, v139\n"
"v_subrev_f32 v170, s22, v170\n"
"v_subrev_f32 v171, s22, v171\n"
"v_mfma_f32_16x16x32_bf16 v[16:19], v[220:223], a[172:175], v[16:19]\n"
"buffer_store_dwordx4 v[132:135], v243, s[32:35], s16 offen offset:1024 sc0 sc1\n"
"v_subrev_f32 v165, s22, v165\n"
"v_mfma_f32_16x16x32_bf16 v[16:19], v[216:219], a[168:171], v[16:19]\n"
"v_subrev_f32 v148, s22, v148\n"
"v_subrev_f32 v149, s22, v149\n"
"v_mfma_f32_16x16x32_bf16 v[16:19], v[212:215], a[164:167], v[16:19]\n"
"v_cvt_pk_bf16_f32 v148, v148, v149\n"
"v_subrev_f32 v167, s22, v167\n"
"v_mfma_f32_16x16x32_bf16 v[16:19], v[208:211], a[160:163], v[16:19]\n"
"v_cvt_pk_bf16_f32 v149, v150, v151\n"
"v_subrev_f32 v155, s22, v155\n"
"v_mfma_f32_16x16x32_bf16 v[16:19], v[192:195], a[124:127], v[16:19]\n"
"v_subrev_f32 v164, s22, v164\n"
"v_subrev_f32 v166, s22, v166\n"
"v_mfma_f32_16x16x32_bf16 v[16:19], v[188:191], a[120:123], v[16:19]\n"
"v_cvt_pk_bf16_f32 v164, v164, v165\n"
"v_subrev_f32 v154, s22, v154\n"
"v_mfma_f32_16x16x32_bf16 v[16:19], v[184:187], a[116:119], v[16:19]\n"
"v_cvt_pk_bf16_f32 v150, v152, v153\n"
"v_cvt_pk_bf16_f32 v151, v154, v155\n"
"v_mfma_f32_16x16x32_bf16 v[16:19], v[180:183], a[112:115], v[16:19]\n"
"s_waitcnt vmcnt(16)\n"
"buffer_store_dwordx4 v[148:151], v243, s[32:35], s16 offen offset:3072 sc0 sc1\n"
"v_cvt_pk_bf16_f32 v165, v166, v167\n"
"v_cvt_pk_bf16_f32 v166, v168, v169\n"
"v_cvt_pk_bf16_f32 v167, v170, v171\n"
"v_mfma_f32_16x16x32_bf16 v[32:35], v[180:183], a[128:131], v[32:35]\n"
"buffer_store_dwordx4 v[164:167], v243, s[32:35], s17 offen offset:1024 sc0 sc1\n"
"ds_read_b64_tr_b16 v[108:109], v233 offset:8192\n"
"v_mfma_f32_16x16x32_bf16 v[32:35], v[184:187], a[132:135], v[32:35]\n"
"ds_read_b64_tr_b16 v[122:123], v229 offset:26624\n"
"v_lshlrev_b32 v202, 16, v84\n"
"v_mfma_f32_16x16x32_bf16 v[32:35], v[188:191], a[136:139], v[32:35]\n"
"ds_read_b64_tr_b16 v[224:225], v233 offset:24576\n"
"v_xor_b32 v230, s75, v230\n"
"v_mfma_f32_16x16x32_bf16 v[32:35], v[192:195], a[140:143], v[32:35]\n"
"ds_read_b64_tr_b16 v[118:119], v233 offset:18432\n"
"v_bfe_u32 v161, v242, 8, 2\n"
"v_mfma_f32_16x16x32_bf16 v[32:35], v[208:211], a[176:179], v[32:35]\n"
"ds_read_b64_tr_b16 v[226:227], v233 offset:26624\n"
"v_bfe_u32 v160, v242, 6, 2\n"
"v_mfma_f32_16x16x32_bf16 v[32:35], v[212:215], a[180:183], v[32:35]\n"
"ds_read_b64_tr_b16 v[114:115], v229 offset:18432\n"
"v_mov_b32 v165, 0\n"
"v_mfma_f32_16x16x32_bf16 v[32:35], v[216:219], a[184:187], v[32:35]\n"
"ds_read_b64_tr_b16 v[120:121], v229 offset:24576\n"
"v_xor_b32 v160, v160, v161\n"
"v_mfma_f32_16x16x32_bf16 v[32:35], v[220:223], a[188:191], v[32:35]\n"
"ds_read_b64_tr_b16 v[116:117], v233 offset:16384\n"
"v_xor_b32 v234, s75, v234\n"
"v_mfma_f32_16x16x32_bf16 v[48:51], v[220:223], a[252:255], v[48:51]\n"
"ds_read_b64_tr_b16 v[110:111], v233 offset:10240\n"
"v_lshlrev_b32 v204, 16, v86\n"
"v_mfma_f32_16x16x32_bf16 v[48:51], v[216:219], a[248:251], v[48:51]\n"
"ds_read_b64_tr_b16 v[106:107], v229 offset:10240\n"
"v_lshlrev_b32 v205, 16, v87\n"
"v_mfma_f32_16x16x32_bf16 v[48:51], v[212:215], a[244:247], v[48:51]\n"
"ds_read_b64_tr_b16 v[112:113], v229 offset:16384\n"
"v_lshlrev_b32 v203, 16, v85\n"
"v_mfma_f32_16x16x32_bf16 v[48:51], v[208:211], a[240:243], v[48:51]\n"
"ds_read_b64_tr_b16 v[210:211], v230 offset:18432\n"
"v_add_f32 v24, v24, v202\n"
"v_mfma_f32_16x16x32_bf16 v[48:51], v[192:195], a[204:207], v[48:51]\n"
"ds_read_b64_tr_b16 v[220:221], v234 offset:24576\n"
"v_xor_b32 v231, s75, v231\n"
"v_mfma_f32_16x16x32_bf16 v[48:51], v[188:191], a[200:203], v[48:51]\n"
"ds_read_b64_tr_b16 v[194:195], v234 offset:10240\n"
"v_add_f32 v26, v26, v203\n"
"v_mfma_f32_16x16x32_bf16 v[48:51], v[184:187], a[196:199], v[48:51]\n"
"ds_read_b64_tr_b16 v[222:223], v234 offset:26624\n"
"v_add_f32 v28, v28, v204\n"
"v_mfma_f32_16x16x32_bf16 v[48:51], v[180:183], a[192:195], v[48:51]\n"
"s_waitcnt vmcnt(15)\n"
"ds_read_b64_tr_b16 v[180:181], v230 offset:0\n"
"v_lshlrev_b32 v202, 16, v90\n"
"v_mfma_f32_16x16x32_bf16 v[4:7], v[96:99], a[96:99], v[4:7]\n"
"s_waitcnt lgkmcnt(7)\n"
"ds_read_b64_tr_b16 v[218:219], v230 offset:26624\n"
"v_lshlrev_b32 v203, 16, v91\n"
"v_mfma_f32_16x16x32_bf16 v[4:7], v[100:103], a[100:103], v[4:7]\n"
"s_waitcnt lgkmcnt(7)\n"
"ds_read_b64_tr_b16 v[216:217], v230 offset:24576\n"
"v_and_b32 v91, s73, v91\n"
"v_mfma_f32_16x16x32_bf16 v[4:7], v[104:107], a[104:107], v[4:7]\n"
"ds_read_b64_tr_b16 v[190:191], v230 offset:10240\n"
"v_and_b32 v90, s73, v90\n"
"v_mfma_f32_16x16x32_bf16 v[4:7], v[108:111], a[108:111], v[4:7]\n"
"s_waitcnt lgkmcnt(7)\n"
"ds_read_b64_tr_b16 v[214:215], v234 offset:18432\n"
"v_add_u32 v160, -1, v160\n"
"v_mfma_f32_16x16x32_bf16 v[4:7], v[112:115], a[144:147], v[4:7]\n"
"s_waitcnt vmcnt(4)\n"
"ds_read_b64_tr_b16 v[208:209], v230 offset:16384\n"
"v_add_f32 v37, v37, v90\n"
"v_mfma_f32_16x16x32_bf16 v[4:7], v[116:119], a[148:151], v[4:7]\n"
"v_subrev_u32 v200, s21, v200\n"
"ds_read_b64_tr_b16 v[186:187], v234 offset:2048\n"
"v_mfma_f32_16x16x32_bf16 v[4:7], v[120:123], a[152:155], v[4:7]\n"
"v_cmp_lt_u32 vcc, s69, v200\n"
"ds_read_b64_tr_b16 v[192:193], v234 offset:8192\n"
"v_mfma_f32_16x16x32_bf16 v[4:7], v[224:227], a[156:159], v[4:7]\n"
"ds_read_b64_tr_b16 v[188:189], v230 offset:8192\n"
"s_cbranch_vccnz LGQ_%=\nLGQK_%=:\n"
"v_mfma_f32_16x16x32_bf16 v[20:23], v[224:227], a[172:175], v[20:23]\n"
"s_bfe_u32 s20, s89, 65536\ns_lshl_b32 s20, s20, 13\ns_add_u32 s20, s20, 0\n"
"ds_read_b64_tr_b16 v[184:185], v234 offset:0\n"
"v_add_f32 v39, v39, v91\n"
"v_mfma_f32_16x16x32_bf16 v[20:23], v[120:123], a[168:171], v[20:23]\n"
"buffer_load_dwordx4 v[124:127], v236, s[32:35], s20 offen sc1\n"
"v_mfma_f32_16x16x32_bf16 v[20:23], v[116:119], a[164:167], v[20:23]\n"
"v_add_f32 v30, v30, v205\n"
"v_ashrrev_i32 v160, 31, v160\n"
"v_mfma_f32_16x16x32_bf16 v[20:23], v[112:115], a[160:163], v[20:23]\n"
"s_bfe_u32 s20, s89, 65540\ns_lshl_b32 s20, s20, 13\ns_add_u32 s20, s20, 1048576\n"
"v_add_f32 v36, v36, v202\n"
"v_mfma_f32_16x16x32_bf16 v[20:23], v[108:111], a[124:127], v[20:23]\n"
"buffer_load_dwordx4 v[128:131], v236, s[32:35], s20 offen sc1\n"
"v_mfma_f32_16x16x32_bf16 v[20:23], v[104:107], a[120:123], v[20:23]\n"
"v_add_f32 v38, v38, v203\n"
"v_mfma_f32_16x16x32_bf16 v[20:23], v[100:103], a[116:119], v[20:23]\n"
"s_bfe_u32 s20, s89, 65544\ns_lshl_b32 s20, s20, 13\ns_add_u32 s20, s20, 2097152\n"
"v_and_b32 v84, s73, v84\n"
"v_lshlrev_b32 v207, 16, v89\n"
"v_mfma_f32_16x16x32_bf16 v[20:23], v[96:99], a[112:115], v[20:23]\n"
"buffer_load_dwordx4 v[132:135], v236, s[32:35], s20 offen sc1\n"
"v_mfma_f32_16x16x32_bf16 v[36:39], v[96:99], a[128:131], v[36:39]\n"
"v_and_b32 v85, s73, v85\n"
"v_lshlrev_b32 v204, 16, v92\n"
"v_mfma_f32_16x16x32_bf16 v[36:39], v[100:103], a[132:135], v[36:39]\n"
"s_bfe_u32 s20, s89, 65548\ns_lshl_b32 s20, s20, 13\ns_add_u32 s20, s20, 3145728\n"
"ds_read_b64_tr_b16 v[182:183], v230 offset:2048\n"
"ds_read_b64_tr_b16 v[212:213], v234 offset:16384\n"
"v_mfma_f32_16x16x32_bf16 v[36:39], v[104:107], a[136:139], v[36:39]\n"
"buffer_load_dwordx4 v[136:139], v236, s[32:35], s20 offen sc1\n"
"v_mfma_f32_16x16x32_bf16 v[36:39], v[108:111], a[140:143], v[36:39]\n"
"v_lshlrev_b32 v205, 16, v93\n"
"v_mfma_f32_16x16x32_bf16 v[36:39], v[112:115], a[176:179], v[36:39]\n"
"s_bfe_u32 s20, s89, 65552\ns_lshl_b32 s20, s20, 13\ns_add_u32 s20, s20, 4194304\n"
"v_and_b32 v160, 0x3f80, v160\n"
"v_add_f32 v40, v40, v204\n"
"v_mfma_f32_16x16x32_bf16 v[36:39], v[116:119], a[180:183], v[36:39]\n"
"buffer_load_dwordx4 v[140:143], v236, s[32:35], s20 offen sc1\n"
"v_mfma_f32_16x16x32_bf16 v[36:39], v[120:123], a[184:187], v[36:39]\n"
"v_and_b32 v161, 16, v242\n"
"v_lshlrev_b32 v206, 16, v88\n"
"v_mfma_f32_16x16x32_bf16 v[36:39], v[224:227], a[188:191], v[36:39]\n"
"s_bfe_u32 s20, s89, 65556\ns_lshl_b32 s20, s20, 13\ns_add_u32 s20, s20, 5242880\n"
"v_add_f32 v27, v27, v85\n"
"v_add_f32 v34, v34, v207\n"
"v_mfma_f32_16x16x32_bf16 v[52:55], v[224:227], a[252:255], v[52:55]\n"
"buffer_load_dwordx4 v[144:147], v236, s[32:35], s20 offen sc1\n"
"v_mfma_f32_16x16x32_bf16 v[52:55], v[120:123], a[248:251], v[52:55]\n"
"v_and_b32 v86, s73, v86\n"
"v_and_b32 v92, s73, v92\n"
"v_mfma_f32_16x16x32_bf16 v[52:55], v[116:119], a[244:247], v[52:55]\n"
"s_bfe_u32 s20, s89, 65560\ns_lshl_b32 s20, s20, 13\ns_add_u32 s20, s20, 6291456\n"
"v_xor_b32 v235, s75, v235\n"
"v_mov_b32 v164, 0\n"
"v_mfma_f32_16x16x32_bf16 v[52:55], v[112:115], a[240:243], v[52:55]\n"
"buffer_load_dwordx4 v[148:151], v236, s[32:35], s20 offen sc1\n"
"v_mfma_f32_16x16x32_bf16 v[52:55], v[108:111], a[204:207], v[52:55]\n"
"s_bfe_u32 s20, s89, 65564\ns_lshl_b32 s20, s20, 13\ns_add_u32 s20, s20, 7340032\n"
"v_and_b32 v88, s73, v88\n"
"v_lshlrev_b32 v207, 16, v95\n"
"v_mfma_f32_16x16x32_bf16 v[52:55], v[104:107], a[200:203], v[52:55]\n"
"buffer_load_dwordx4 v[152:155], v236, s[32:35], s20 offen sc1\n"
"v_mfma_f32_16x16x32_bf16 v[52:55], v[100:103], a[196:199], v[52:55]\n"
"s_waitcnt vmcnt(8)\n"
"s_add_u32 s21, s21, 1\n"
"v_add_f32 v32, v32, v206\n"
"v_mfma_f32_16x16x32_bf16 v[52:55], v[96:99], a[192:195], v[52:55]\n"
"s_barrier\n"
"s_mov_b32 s52, 40960\n"
"buffer_store_dword v241, v248, s[84:87], 0 offen sc1\n"
"s_waitcnt lgkmcnt(0)\n"
"v_mfma_f32_16x16x32_bf16 v[8:11], v[180:183], a[96:99], v[8:11]\n"
"s_mov_b32 s53, 81920\n"
"ds_read_b64_tr_b16 v[120:121], v231 offset:24576\n"
"v_lshlrev_b32 v206, 16, v94\n"
"v_mfma_f32_16x16x32_bf16 v[8:11], v[184:187], a[100:103], v[8:11]\n"
"ds_read_b64_tr_b16 v[96:97], v231 offset:0\n"
"v_and_b32 v89, s73, v89\n"
"v_mfma_f32_16x16x32_bf16 v[8:11], v[188:191], a[104:107], v[8:11]\n"
"ds_read_b64_tr_b16 v[102:103], v235 offset:2048\n"
"v_add_f32 v46, v46, v207\n"
"v_mfma_f32_16x16x32_bf16 v[8:11], v[192:195], a[108:111], v[8:11]\n"
"v_and_b32 v93, s73, v93\n"
"v_add_f32 v44, v44, v206\n"
"v_mfma_f32_16x16x32_bf16 v[8:11], v[208:211], a[144:147], v[8:11]\n"
"ds_read_b64_tr_b16 v[122:123], v231 offset:26624\n"
"v_add_f32 v43, v43, v93\n"
"v_mfma_f32_16x16x32_bf16 v[8:11], v[212:215], a[148:151], v[8:11]\n"
"ds_read_b64_tr_b16 v[116:117], v235 offset:16384\n"
"v_add_f32 v25, v25, v84\n"
"v_mfma_f32_16x16x32_bf16 v[8:11], v[216:219], a[152:155], v[8:11]\n"
"v_and_b32 v87, s73, v87\n"
"v_and_b32 v94, s73, v94\n"
"v_mfma_f32_16x16x32_bf16 v[8:11], v[220:223], a[156:159], v[8:11]\n"
"ds_read_b64_tr_b16 v[224:225], v235 offset:24576\n"
"v_lshlrev_b32 v202, 16, v246\n"
"v_mfma_f32_16x16x32_bf16 v[24:27], v[220:223], a[172:175], v[24:27]\n"
"v_add_f32 v31, v31, v87\n"
"v_add_f32 v42, v42, v205\n"
"v_mfma_f32_16x16x32_bf16 v[24:27], v[216:219], a[168:171], v[24:27]\n"
"ds_read_b64_tr_b16 v[100:101], v235 offset:0\n"
"v_lshlrev_b32 v207, 16, v245\n"
"v_mfma_f32_16x16x32_bf16 v[24:27], v[212:215], a[164:167], v[24:27]\n"
"v_add_f32 v29, v29, v86\n"
"v_add_f32 v41, v41, v92\n"
"v_mfma_f32_16x16x32_bf16 v[24:27], v[208:211], a[160:163], v[24:27]\n"
"ds_read_b64_tr_b16 v[98:99], v231 offset:2048\n"
"v_and_b32 v95, s73, v95\n"
"v_mfma_f32_16x16x32_bf16 v[24:27], v[192:195], a[124:127], v[24:27]\n"
"ds_read_b64_tr_b16 v[114:115], v231 offset:18432\n"
"v_add_f32 v33, v33, v88\n"
"v_mfma_f32_16x16x32_bf16 v[24:27], v[188:191], a[120:123], v[24:27]\n"
"ds_read_b64_tr_b16 v[110:111], v235 offset:10240\n"
"v_and_b32 v245, s73, v245\n"
"v_mfma_f32_16x16x32_bf16 v[24:27], v[184:187], a[116:119], v[24:27]\n"
"v_add_f32 v45, v45, v94\n"
"v_and_b32 v246, s73, v246\n"
"v_mfma_f32_16x16x32_bf16 v[24:27], v[180:183], a[112:115], v[24:27]\n"
"ds_read_b64_tr_b16 v[108:109], v235 offset:8192\n"
"v_lshlrev_b32 v206, 16, v244\n"
"v_mfma_f32_16x16x32_bf16 v[40:43], v[180:183], a[128:131], v[40:43]\n"
"ds_read_b64_tr_b16 v[106:107], v231 offset:10240\n"
"v_lshlrev_b32 v160, v161, v160\n"
"v_mfma_f32_16x16x32_bf16 v[40:43], v[184:187], a[132:135], v[40:43]\n"
"ds_read_b64_tr_b16 v[104:105], v231 offset:8192\n"
"v_mfma_f32_16x16x32_bf16 v[40:43], v[188:191], a[136:139], v[40:43]\n"
"v_bfe_i32 v161, v242, 5, 1\n"
"v_mfma_f32_16x16x32_bf16 v[40:43], v[192:195], a[140:143], v[40:43]\n"
"ds_read_b64_tr_b16 v[112:113], v231 offset:16384\n"
"v_and_b32 v244, s73, v244\n"
"v_mfma_f32_16x16x32_bf16 v[40:43], v[208:211], a[176:179], v[40:43]\n"
"ds_read_b64_tr_b16 v[118:119], v235 offset:18432\n"
"v_add_f32 v49, v49, v244\n"
"v_mfma_f32_16x16x32_bf16 v[40:43], v[212:215], a[180:183], v[40:43]\n"
"v_add_f32 v52, v52, v202\n"
"v_lshlrev_b32 v203, 16, v247\n"
"v_mfma_f32_16x16x32_bf16 v[40:43], v[216:219], a[184:187], v[40:43]\n"
"ds_read_b64_tr_b16 v[226:227], v235 offset:26624\n"
"v_add_f32 v48, v48, v206\n"
"v_mfma_f32_16x16x32_bf16 v[40:43], v[220:223], a[188:191], v[40:43]\n"
"s_barrier\n"
"s_waitcnt lgkmcnt(5)\n"
"v_mfma_f32_16x16x32_bf16 v[12:15], v[96:99], a[96:99], v[12:15]\n"
"ds_read_b64_tr_b16 v[194:195], v232 offset:43008\n"
"ds_read_b64_tr_b16 v[214:215], v232 offset:51200\n"
"v_mfma_f32_16x16x32_bf16 v[12:15], v[100:103], a[100:103], v[12:15]\n"
"s_waitcnt lgkmcnt(5)\n"
"ds_read_b64_tr_b16 v[186:187], v232 offset:34816\n"
"v_add_f32 v53, v53, v246\n"
"v_mfma_f32_16x16x32_bf16 v[12:15], v[104:107], a[104:107], v[12:15]\n"
"ds_read_b64_tr_b16 v[184:185], v232 offset:32768\n"
"ds_read_b64_tr_b16 v[208:209], v228 offset:49152\n"
"v_mfma_f32_16x16x32_bf16 v[12:15], v[108:111], a[108:111], v[12:15]\n"
"s_waitcnt lgkmcnt(7)\n"
"ds_read_b64_tr_b16 v[188:189], v228 offset:40960\n"
"v_add_f32 v47, v47, v95\n"
"v_mfma_f32_16x16x32_bf16 v[12:15], v[112:115], a[144:147], v[12:15]\n"
"s_waitcnt lgkmcnt(7)\n"
"ds_read_b64_tr_b16 v[180:181], v228 offset:32768\n"
"v_add_f32 v35, v35, v89\n"
"v_mfma_f32_16x16x32_bf16 v[12:15], v[116:119], a[148:151], v[12:15]\n"
"ds_read_b64_tr_b16 v[216:217], v228 offset:57344\n"
"v_add_f32 v51, v51, v245\n"
"v_mfma_f32_16x16x32_bf16 v[12:15], v[120:123], a[152:155], v[12:15]\n"
"s_waitcnt lgkmcnt(8)\n"
"ds_read_b64_tr_b16 v[182:183], v228 offset:34816\n"
"ds_read_b64_tr_b16 v[212:213], v232 offset:49152\n"
"v_mfma_f32_16x16x32_bf16 v[12:15], v[224:227], a[156:159], v[12:15]\n"
"global_load_dword v200, v201, s[6:7] sc1\n"
"v_mfma_f32_16x16x32_bf16 v[28:31], v[224:227], a[172:175], v[28:31]\n"
"v_add_f32 v50, v50, v207\n"
"v_add_f32 v54, v54, v203\n"
"v_mfma_f32_16x16x32_bf16 v[28:31], v[120:123], a[168:171], v[28:31]\n"
"s_waitcnt vmcnt(9)\n"
"v_lshlrev_b32 v58, 16, v125\n"
"v_lshlrev_b32 v62, 16, v127\n"
"v_mfma_f32_16x16x32_bf16 v[28:31], v[116:119], a[164:167], v[28:31]\n"
"ds_read_b64_tr_b16 v[210:211], v228 offset:51200\n"
"ds_read_b64_tr_b16 v[218:219], v228 offset:59392\n"
"v_mfma_f32_16x16x32_bf16 v[28:31], v[112:115], a[160:163], v[28:31]\n"
"ds_read_b64_tr_b16 v[190:191], v228 offset:43008\n"
"v_lshlrev_b32 v60, 16, v126\n"
"v_mfma_f32_16x16x32_bf16 v[28:31], v[108:111], a[124:127], v[28:31]\n"
"s_waitcnt vmcnt(8)\n"
"ds_read_b64_tr_b16 v[192:193], v232 offset:40960\n"
"v_lshlrev_b32 v205, 16, v129\n"
"v_mfma_f32_16x16x32_bf16 v[28:31], v[104:107], a[120:123], v[28:31]\n"
"v_lshlrev_b32 v56, 16, v124\n"
"v_lshlrev_b32 v206, 16, v130\n"
"v_mfma_f32_16x16x32_bf16 v[28:31], v[100:103], a[116:119], v[28:31]\n"
"ds_read_b64_tr_b16 v[222:223], v232 offset:59392\n"
"v_add_f32 v58, v58, v205\n"
"v_mfma_f32_16x16x32_bf16 v[28:31], v[96:99], a[112:115], v[28:31]\n"
"s_waitcnt vmcnt(7)\n"
"v_lshlrev_b32 v204, 16, v128\n"
"v_add_f32 v56, v56, v204\n"
"v_mfma_f32_16x16x32_bf16 v[44:47], v[96:99], a[128:131], v[44:47]\n"
"v_lshlrev_b32 v207, 16, v131\n"
"v_lshlrev_b32 v203, 16, v133\n"
"v_mfma_f32_16x16x32_bf16 v[44:47], v[100:103], a[132:135], v[44:47]\n"
"v_add_f32 v58, v58, v203\n"
"v_lshlrev_b32 v205, 16, v135\n"
"v_mfma_f32_16x16x32_bf16 v[44:47], v[104:107], a[136:139], v[44:47]\n"
"s_waitcnt vmcnt(6)\n"
"ds_read_b64_tr_b16 v[106:107], v229 offset:43008\n"
"v_and_b32 v59, s73, v125\n"
"v_mfma_f32_16x16x32_bf16 v[44:47], v[108:111], a[140:143], v[44:47]\n"
"v_add_f32 v60, v60, v206\n"
"v_lshlrev_b32 v206, 16, v136\n"
"v_mfma_f32_16x16x32_bf16 v[44:47], v[112:115], a[176:179], v[44:47]\n"
"v_lshlrev_b32 v202, 16, v132\n"
"v_and_b32 v132, s73, v132\n"
"v_mfma_f32_16x16x32_bf16 v[44:47], v[116:119], a[180:183], v[44:47]\n"
"s_waitcnt vmcnt(5)\n"
"ds_read_b64_tr_b16 v[100:101], v233 offset:32768\n"
"v_lshlrev_b32 v204, 16, v134\n"
"v_mfma_f32_16x16x32_bf16 v[44:47], v[120:123], a[184:187], v[44:47]\n"
"ds_read_b64_tr_b16 v[220:221], v232 offset:57344\n"
"v_add_f32 v62, v62, v207\n"
"v_mfma_f32_16x16x32_bf16 v[44:47], v[224:227], a[188:191], v[44:47]\n"
"s_waitcnt lgkmcnt(3)\n"
"ds_read_b64_tr_b16 v[224:225], v233 offset:57344\n"
"v_add_f32 v62, v62, v205\n"
"v_mfma_f32_16x16x32_bf16 v[0:3], v[180:183], a[48:51], v[0:3]\n"
"s_waitcnt vmcnt(4)\n"
"ds_read_b64_tr_b16 v[114:115], v229 offset:51200\n"
"v_lshlrev_b32 v207, 16, v137\n"
"v_mfma_f32_16x16x32_bf16 v[0:3], v[184:187], a[52:55], v[0:3]\n"
"v_and_b32 v128, s73, v128\n"
"v_lshlrev_b32 v203, 16, v139\n"
"v_mfma_f32_16x16x32_bf16 v[0:3], v[188:191], a[56:59], v[0:3]\n"
"v_and_b32 v57, s73, v124\n"
"v_add_f32 v60, v60, v204\n"
"v_mfma_f32_16x16x32_bf16 v[0:3], v[192:195], a[60:63], v[0:3]\n"
"s_waitcnt vmcnt(3)\n"
"ds_read_b64_tr_b16 v[96:97], v229 offset:32768\n"
"ds_read_b64_tr_b16 v[112:113], v229 offset:49152\n"
"v_mfma_f32_16x16x32_bf16 v[0:3], v[208:211], a[0:3], v[0:3]\n"
"v_and_b32 v247, s73, v247\n"
"v_add_f32 v58, v58, v207\n"
"v_mfma_f32_16x16x32_bf16 v[0:3], v[212:215], a[4:7], v[0:3]\n"
"s_waitcnt vmcnt(2)\n"
"ds_read_b64_tr_b16 v[102:103], v233 offset:34816\n"
"v_and_b32 v134, s73, v134\n"
"v_mfma_f32_16x16x32_bf16 v[0:3], v[216:219], a[8:11], v[0:3]\n"
"s_waitcnt lgkmcnt(5)\n"
"ds_read_b64_tr_b16 v[120:121], v229 offset:57344\n"
"v_lshlrev_b32 v204, 16, v140\n"
"v_mfma_f32_16x16x32_bf16 v[0:3], v[220:223], a[12:15], v[0:3]\n"
"ds_read_b64_tr_b16 v[98:99], v229 offset:34816\n"
"v_and_b32 v61, s73, v126\n"
"v_add_f32 v57, v57, v128\n"
"v_add_f32 v56, v56, v202\n"
"v_and_b32 v135, s73, v135\n"
"v_and_b32 v136, s73, v136\n"
"v_and_b32 v137, s73, v137\n"
"v_lshlrev_b32 v202, 16, v138\n"
"v_mfma_f32_16x16x32_bf16 v[16:19], v[220:223], a[28:31], v[16:19]\n"
"ds_read_b64_tr_b16 v[122:123], v229 offset:59392\n"
"v_and_b32 v129, s73, v129\n"
"v_mfma_f32_16x16x32_bf16 v[16:19], v[216:219], a[24:27], v[16:19]\n"
"ds_read_b64_tr_b16 v[108:109], v233 offset:40960\n"
"v_add_f32 v56, v56, v206\n"
"v_mfma_f32_16x16x32_bf16 v[16:19], v[212:215], a[20:23], v[16:19]\n"
"v_add_f32 v60, v60, v202\n"
"v_lshlrev_b32 v207, 16, v143\n"
"v_mfma_f32_16x16x32_bf16 v[16:19], v[208:211], a[16:19], v[16:19]\n"
"ds_read_b64_tr_b16 v[116:117], v233 offset:49152\n"
"v_lshlrev_b32 v205, 16, v141\n"
"v_mfma_f32_16x16x32_bf16 v[16:19], v[192:195], a[76:79], v[16:19]\n"
"s_waitcnt vmcnt(0)\n"
"v_add_f32 v55, v55, v247\n"
"v_lshlrev_b32 v202, 16, v144\n"
"v_mfma_f32_16x16x32_bf16 v[16:19], v[188:191], a[72:75], v[16:19]\n"
"v_subrev_u32 v200, s21, v200\n"
"ds_read_b64_tr_b16 v[104:105], v229 offset:40960\n"
"v_mfma_f32_16x16x32_bf16 v[16:19], v[184:187], a[68:71], v[16:19]\n"
"v_cmp_lt_u32 vcc, s69, v200\n"
"v_add_f32 v62, v62, v203\n"
"v_mfma_f32_16x16x32_bf16 v[16:19], v[180:183], a[64:67], v[16:19]\n"
"ds_read_b64_tr_b16 v[118:119], v233 offset:51200\n"
"v_and_b32 v63, s73, v127\n"
"v_add_f32 v59, v59, v129\n"
"v_add_f32 v56, v56, v204\n"
"v_add_f32 v58, v58, v205\n"
"v_lshlrev_b32 v206, 16, v142\n"
"v_lshlrev_b32 v203, 16, v145\n"
"v_lshlrev_b32 v205, 16, v147\n"
"v_subrev_f32 v1, s22, v1\n"
"s_cbranch_vccnz LGR_%=\nLGRK_%=:\n"
"v_mfma_f32_16x16x32_bf16 v[32:35], v[180:183], a[80:83], v[32:35]\n"
"ds_read_b64_tr_b16 v[110:111], v233 offset:43008\n"
"s_cmp_eq_u32 s93, 0\ns_cbranch_scc1 LEP_%=\nLEPK_%=:\n"
"v_mfma_f32_16x16x32_bf16 v[32:35], v[184:187], a[84:87], v[32:35]\n"
"s_bfe_u32 s20, s89, 65536\ns_lshl_b32 s20, s20, 13\ns_add_u32 s20, s20, 65536\n"
"ds_read_b64_tr_b16 v[226:227], v233 offset:59392\n"
"v_add_f32 v57, v57, v132\n"
"v_mfma_f32_16x16x32_bf16 v[32:35], v[188:191], a[88:91], v[32:35]\n"
"buffer_load_dwordx4 v[72:75], v236, s[32:35], s20 offen sc1\n"
"v_mfma_f32_16x16x32_bf16 v[32:35], v[192:195], a[92:95], v[32:35]\n"
"v_and_b32 v131, s73, v131\n"
"v_add_f32 v58, v58, v203\n"
"v_mfma_f32_16x16x32_bf16 v[32:35], v[208:211], a[32:35], v[32:35]\n"
"s_bfe_u32 s20, s89, 65540\ns_lshl_b32 s20, s20, 13\ns_add_u32 s20, s20, 1114112\n"
"v_subrev_f32 v2, s22, v2\n"
"v_subrev_f32 v16, s22, v16\n"
"v_mfma_f32_16x16x32_bf16 v[32:35], v[212:215], a[36:39], v[32:35]\n"
"buffer_load_dwordx4 v[76:79], v236, s[32:35], s20 offen sc1\n"
"v_mfma_f32_16x16x32_bf16 v[32:35], v[216:219], a[40:43], v[32:35]\n"
"v_and_b32 v139, s73, v139\n"
"v_add_f32 v62, v62, v207\n"
"v_mfma_f32_16x16x32_bf16 v[32:35], v[220:223], a[44:47], v[32:35]\n"
"s_bfe_u32 s20, s89, 65544\ns_lshl_b32 s20, s20, 13\ns_add_u32 s20, s20, 2162688\n"
"v_add_f32 v62, v62, v205\n"
"v_lshlrev_b32 v207, 16, v149\n"
"v_mfma_f32_16x16x32_bf16 v[48:51], v[220:223], a[228:231], v[48:51]\n"
"buffer_load_dwordx4 v[80:83], v236, s[32:35], s20 offen sc1\n"
"v_mfma_f32_16x16x32_bf16 v[48:51], v[216:219], a[224:227], v[48:51]\n"
"v_and_b32 v140, s73, v140\n"
"v_and_b32 v142, s73, v142\n"
"v_mfma_f32_16x16x32_bf16 v[48:51], v[212:215], a[212:215], v[48:51]\n"
"s_bfe_u32 s20, s89, 65548\ns_lshl_b32 s20, s20, 13\ns_add_u32 s20, s20, 3211264\n"
"v_add_f32 v57, v57, v136\n"
"v_subrev_f32 v3, s22, v3\n"
"v_mfma_f32_16x16x32_bf16 v[48:51], v[208:211], a[208:211], v[48:51]\n"
"buffer_load_dwordx4 v[84:87], v236, s[32:35], s20 offen sc1\n"
"v_mfma_f32_16x16x32_bf16 v[48:51], v[192:195], a[236:239], v[48:51]\n"
"v_lshlrev_b32 v204, 16, v146\n"
"v_and_b32 v147, s73, v147\n"
"v_mfma_f32_16x16x32_bf16 v[48:51], v[188:191], a[232:235], v[48:51]\n"
"s_bfe_u32 s20, s89, 65552\ns_lshl_b32 s20, s20, 13\ns_add_u32 s20, s20, 4259840\n"
"v_and_b32 v138, s73, v138\n"
"v_and_b32 v144, s73, v144\n"
"v_mfma_f32_16x16x32_bf16 v[48:51], v[184:187], a[220:223], v[48:51]\n"
"buffer_load_dwordx4 v[88:91], v236, s[32:35], s20 offen sc1\n"
"v_mfma_f32_16x16x32_bf16 v[48:51], v[180:183], a[216:219], v[48:51]\n"
"s_waitcnt lgkmcnt(0)\n"
"v_and_b32 v133, s73, v133\n"
"v_add_f32 v58, v58, v207\n"
"v_mfma_f32_16x16x32_bf16 v[4:7], v[96:99], a[48:51], v[4:7]\n"
"s_bfe_u32 s20, s89, 65556\ns_lshl_b32 s20, s20, 13\ns_add_u32 s20, s20, 5308416\n"
"v_add_f32 v59, v59, v133\n"
"v_add_f32 v60, v60, v206\n"
"v_mfma_f32_16x16x32_bf16 v[4:7], v[100:103], a[52:55], v[4:7]\n"
"buffer_load_dwordx4 v[92:95], v236, s[32:35], s20 offen sc1\n"
"v_mfma_f32_16x16x32_bf16 v[4:7], v[104:107], a[56:59], v[4:7]\n"
"v_subrev_f32 v0, s22, v0\n"
"v_subrev_f32 v17, s22, v17\n"
"v_mfma_f32_16x16x32_bf16 v[4:7], v[108:111], a[60:63], v[4:7]\n"
"s_bfe_u32 s20, s89, 65560\ns_lshl_b32 s20, s20, 13\ns_add_u32 s20, s20, 6356992\n"
"v_add_f32 v56, v56, v202\n"
"v_cvt_pk_bf16_f32 v16, v16, v17\n"
"v_mfma_f32_16x16x32_bf16 v[4:7], v[112:115], a[0:3], v[4:7]\n"
"buffer_load_dwordx4 v[244:247], v236, s[32:35], s20 offen sc1\n"
"v_and_b32 v130, s73, v130\n"
"v_add_f32 v59, v59, v137\n"
"v_add_f32 v57, v57, v140\n"
"v_add_f32 v60, v60, v204\n"
"v_lshlrev_b32 v202, 16, v150\n"
"v_lshlrev_b32 v203, 16, v151\n"
"v_cvt_pk_bf16_f32 v0, v0, v1\n"
"v_subrev_f32 v32, s22, v32\n"
"v_mfma_f32_16x16x32_bf16 v[4:7], v[116:119], a[4:7], v[4:7]\n"
"v_add_f32 v63, v63, v131\n"
"v_lshlrev_b32 v206, 16, v148\n"
"v_mfma_f32_16x16x32_bf16 v[4:7], v[120:123], a[8:11], v[4:7]\n"
"s_bfe_u32 s20, s89, 65564\ns_lshl_b32 s20, s20, 13\ns_add_u32 s20, s20, 7405568\n"
"v_and_b32 v151, s73, v151\n"
"v_lshlrev_b32 v204, 16, v152\n"
"v_mfma_f32_16x16x32_bf16 v[4:7], v[224:227], a[12:15], v[4:7]\n"
"buffer_load_dwordx4 v[156:159], v236, s[32:35], s20 offen sc1\n"
"v_mfma_f32_16x16x32_bf16 v[20:23], v[224:227], a[28:31], v[20:23]\n"
"v_add_f32 v57, v57, v144\n"
"v_and_b32 v146, s73, v146\n"
"v_mfma_f32_16x16x32_bf16 v[20:23], v[120:123], a[24:27], v[20:23]\n"
"v_and_b32 v141, s73, v141\n"
"v_cvt_pk_bf16_f32 v1, v2, v3\n"
"v_mfma_f32_16x16x32_bf16 v[20:23], v[116:119], a[20:23], v[20:23]\n"
"v_lshlrev_b32 v207, 16, v155\n"
"v_subrev_f32 v19, s22, v19\n"
"v_mfma_f32_16x16x32_bf16 v[20:23], v[112:115], a[16:19], v[20:23]\n"
"v_add_f32 v62, v62, v203\n"
"v_subrev_f32 v18, s22, v18\n"
"v_mfma_f32_16x16x32_bf16 v[20:23], v[108:111], a[76:79], v[20:23]\n"
"ds_read_b64_tr_b16 v[210:211], v230 offset:51200\n"
"v_add_f32 v61, v61, v130\n"
"v_add_f32 v63, v63, v135\n"
"v_and_b32 v143, s73, v143\n"
"v_and_b32 v148, s73, v148\n"
"v_add_f32 v56, v56, v206\n"
"v_and_b32 v149, s73, v149\n"
"v_add_f32 v56, v56, v204\n"
"v_subrev_f32 v6, s22, v6\n"
"v_subrev_f32 v7, s22, v7\n"
"v_mfma_f32_16x16x32_bf16 v[20:23], v[104:107], a[72:75], v[20:23]\n"
"ds_read_b64_tr_b16 v[182:183], v230 offset:34816\n"
"v_subrev_f32 v35, s22, v35\n"
"v_mfma_f32_16x16x32_bf16 v[20:23], v[100:103], a[68:71], v[20:23]\n"
"v_and_b32 v155, s73, v155\n"
"v_subrev_f32 v4, s22, v4\n"
"v_mfma_f32_16x16x32_bf16 v[20:23], v[96:99], a[64:67], v[20:23]\n"
"ds_read_b64_tr_b16 v[186:187], v234 offset:34816\n"
"v_add_f32 v61, v61, v134\n"
"v_add_f32 v57, v57, v148\n"
"v_and_b32 v152, s73, v152\n"
"v_add_f32 v57, v57, v152\n"
"v_lshlrev_b32 v205, 16, v153\n"
"v_subrev_f32 v5, s22, v5\n"
"v_cvt_pk_bf16_f32 v2, v4, v5\n"
"v_cvt_pk_bf16_f32 v3, v6, v7\n"
"v_subrev_f32 v34, s22, v34\n"
"v_mfma_f32_16x16x32_bf16 v[36:39], v[96:99], a[80:83], v[36:39]\n"
"ds_read_b64_tr_b16 v[208:209], v230 offset:49152\n"
"v_and_b32 v150, s73, v150\n"
"v_mfma_f32_16x16x32_bf16 v[36:39], v[100:103], a[84:87], v[36:39]\n"
"ds_read_b64_tr_b16 v[192:193], v234 offset:40960\n"
"v_add_f32 v61, v61, v138\n"
"v_add_f32 v63, v63, v139\n"
"v_and_b32 v145, s73, v145\n"
"v_permlane16_swap_b32 v0, v2\n"
"v_permlane16_swap_b32 v1, v3\n"
"v_cvt_pk_bf16_f32 v17, v18, v19\n"
"v_mfma_f32_16x16x32_bf16 v[36:39], v[104:107], a[88:91], v[36:39]\n"
"ds_read_b64_tr_b16 v[214:215], v234 offset:51200\n"
"v_add_f32 v59, v59, v141\n"
"v_mfma_f32_16x16x32_bf16 v[36:39], v[108:111], a[92:95], v[36:39]\n"
"ds_read_b64_tr_b16 v[190:191], v230 offset:43008\n"
"v_add_f32 v61, v61, v142\n"
"v_mfma_f32_16x16x32_bf16 v[36:39], v[112:115], a[32:35], v[36:39]\n"
"ds_read_b64_tr_b16 v[184:185], v234 offset:32768\n"
"buffer_store_dwordx4 v[0:3], v238, s[36:39], 0 offen sc0 sc1\n"
"v_lshlrev_b32 v206, 16, v154\n"
"v_subrev_f32 v20, s22, v20\n"
"v_subrev_f32 v21, s22, v21\n"
"v_subrev_f32 v23, s22, v23\n"
"v_cvt_pk_bf16_f32 v18, v20, v21\n"
"v_mfma_f32_16x16x32_bf16 v[36:39], v[116:119], a[36:39], v[36:39]\n"
"ds_read_b64_tr_b16 v[194:195], v234 offset:43008\n"
"v_and_b32 v154, s73, v154\n"
"v_mfma_f32_16x16x32_bf16 v[36:39], v[120:123], a[40:43], v[36:39]\n"
"ds_read_b64_tr_b16 v[188:189], v230 offset:40960\n"
"v_and_b32 v153, s73, v153\n"
"v_mfma_f32_16x16x32_bf16 v[36:39], v[224:227], a[44:47], v[36:39]\n"
"ds_read_b64_tr_b16 v[212:213], v234 offset:49152\n"
"v_add_f32 v59, v59, v145\n"
"v_add_f32 v59, v59, v149\n"
"v_add_f32 v62, v62, v207\n"
"v_subrev_f32 v22, s22, v22\n"
"v_permlane16_swap_b32 v16, v18\n"
"v_subrev_f32 v48, s22, v48\n"
"v_subrev_f32 v50, s22, v50\n"
"v_subrev_f32 v51, s22, v51\n"
"v_mfma_f32_16x16x32_bf16 v[52:55], v[224:227], a[228:231], v[52:55]\n"
"ds_read_b64_tr_b16 v[220:221], v234 offset:57344\n"
"v_cvt_pk_bf16_f32 v19, v22, v23\n"
"v_mfma_f32_16x16x32_bf16 v[52:55], v[120:123], a[224:227], v[52:55]\n"
"v_add_f32 v60, v60, v202\n"
"v_add_f32 v59, v59, v153\n"
"v_mfma_f32_16x16x32_bf16 v[52:55], v[116:119], a[212:215], v[52:55]\n"
"ds_read_b64_tr_b16 v[180:181], v230 offset:32768\n"
"v_add_f32 v63, v63, v143\n"
"v_add_f32 v61, v61, v146\n"
"v_add_f32 v63, v63, v147\n"
"v_add_f32 v61, v61, v150\n"
"v_add_f32 v63, v63, v151\n"
"v_add_f32 v60, v60, v206\n"
"v_cvt_pk_bf16_f32 v56, v56, v57\n"
"v_permlane16_swap_b32 v17, v19\n"
"v_mfma_f32_16x16x32_bf16 v[52:55], v[112:115], a[208:211], v[52:55]\n"
"v_add_f32 v58, v58, v205\n"
"v_subrev_f32 v37, s22, v37\n"
"v_mfma_f32_16x16x32_bf16 v[52:55], v[108:111], a[236:239], v[52:55]\n"
"ds_read_b64_tr_b16 v[218:219], v230 offset:59392\n"
"v_subrev_f32 v36, s22, v36\n"
"v_mfma_f32_16x16x32_bf16 v[52:55], v[104:107], a[232:235], v[52:55]\n"
"ds_read_b64_tr_b16 v[216:217], v230 offset:57344\n"
"buffer_store_dwordx4 v[16:19], v238, s[36:39], s52 offen sc0 sc1\n"
"v_add_f32 v61, v61, v154\n"
"v_add_f32 v63, v63, v155\n"
"v_cvt_pk_bf16_f32 v57, v58, v59\n"
"v_cvt_pk_bf16_f32 v58, v60, v61\n"
"v_subrev_f32 v38, s22, v38\n"
"v_mfma_f32_16x16x32_bf16 v[52:55], v[100:103], a[220:223], v[52:55]\n"
"ds_read_b64_tr_b16 v[222:223], v234 offset:59392\n"
"v_cvt_pk_bf16_f32 v59, v62, v63\n"
"v_mfma_f32_16x16x32_bf16 v[52:55], v[96:99], a[216:219], v[52:55]\n"
"s_waitcnt lgkmcnt(3)\n"
"v_permlane16_swap_b32 v56, v58\n"
"v_mfma_f32_16x16x32_bf16 v[8:11], v[180:183], a[48:51], v[8:11]\n"
"v_permlane16_swap_b32 v57, v59\n"
"v_mfma_f32_16x16x32_bf16 v[8:11], v[184:187], a[52:55], v[8:11]\n"
"buffer_store_dwordx4 v[56:59], v253, s[36:39], 0 offen sc0 sc1\n"
"v_subrev_f32 v33, s22, v33\n"
"v_subrev_f32 v39, s22, v39\n"
"v_cvt_pk_bf16_f32 v32, v32, v33\n"
"v_cvt_pk_bf16_f32 v33, v34, v35\n"
"v_cvt_pk_bf16_f32 v34, v36, v37\n"
"v_cvt_pk_bf16_f32 v35, v38, v39\n"
"v_mfma_f32_16x16x32_bf16 v[8:11], v[188:191], a[56:59], v[8:11]\n"
"v_subrev_f32 v49, s22, v49\n"
"v_subrev_f32 v52, s22, v52\n"
"v_mfma_f32_16x16x32_bf16 v[8:11], v[192:195], a[60:63], v[8:11]\n"
"v_permlane16_swap_b32 v32, v34\n"
"v_mfma_f32_16x16x32_bf16 v[8:11], v[208:211], a[0:3], v[8:11]\n"
"v_permlane16_swap_b32 v33, v35\n"
"v_mfma_f32_16x16x32_bf16 v[8:11], v[212:215], a[4:7], v[8:11]\n"
"s_waitcnt lgkmcnt(1)\n"
"buffer_store_dwordx4 v[32:35], v238, s[36:39], s53 offen sc0 sc1\n"
"v_cvt_pk_bf16_f32 v48, v48, v49\n"
"v_mfma_f32_16x16x32_bf16 v[8:11], v[216:219], a[8:11], v[8:11]\n"
"s_waitcnt lgkmcnt(0)\n"
"ds_read_b64_tr_b16 v[110:111], v235 offset:43008\n"
"v_subrev_f32 v53, s22, v53\n"
"v_mfma_f32_16x16x32_bf16 v[8:11], v[220:223], a[12:15], v[8:11]\n"
"v_cvt_pk_bf16_f32 v49, v50, v51\n"
"v_cvt_pk_bf16_f32 v50, v52, v53\n"
"v_mfma_f32_16x16x32_bf16 v[24:27], v[220:223], a[28:31], v[24:27]\n"
"s_nop 0\n"
"v_permlane16_swap_b32 v48, v50\n"
"v_mfma_f32_16x16x32_bf16 v[24:27], v[216:219], a[24:27], v[24:27]\n"
"ds_read_b64_tr_b16 v[114:115], v231 offset:51200\n"
"v_subrev_f32 v55, s22, v55\n"
"v_mfma_f32_16x16x32_bf16 v[24:27], v[212:215], a[20:23], v[24:27]\n"
"ds_read_b64_tr_b16 v[102:103], v235 offset:34816\n"
"v_subrev_f32 v54, s22, v54\n"
"v_mfma_f32_16x16x32_bf16 v[24:27], v[208:211], a[16:19], v[24:27]\n"
"ds_read_b64_tr_b16 v[112:113], v231 offset:49152\n"
"v_cvt_pk_bf16_f32 v51, v54, v55\n"
"v_mfma_f32_16x16x32_bf16 v[24:27], v[192:195], a[76:79], v[24:27]\n"
"s_nop 0\n"
"v_permlane16_swap_b32 v49, v51\n"
"v_mfma_f32_16x16x32_bf16 v[24:27], v[188:191], a[72:75], v[24:27]\n"
"buffer_store_dwordx4 v[48:51], v252, s[36:39], 0 offen sc0 sc1\n"
"ds_read_b64_tr_b16 v[106:107], v231 offset:43008\n"
"v_mfma_f32_16x16x32_bf16 v[24:27], v[184:187], a[68:71], v[24:27]\n"
"ds_read_b64_tr_b16 v[96:97], v231 offset:32768\n"
"ds_read_b64_tr_b16 v[98:99], v231 offset:34816\n"
"v_mfma_f32_16x16x32_bf16 v[24:27], v[180:183], a[64:67], v[24:27]\n"
"ds_read_b64_tr_b16 v[100:101], v235 offset:32768\n"
"v_and_b32 v163, v160, v161\n"
"v_mfma_f32_16x16x32_bf16 v[40:43], v[180:183], a[80:83], v[40:43]\n"
"ds_read_b64_tr_b16 v[118:119], v235 offset:51200\n"
"v_xor_b32 v162, v160, v163\n"
"v_mfma_f32_16x16x32_bf16 v[40:43], v[184:187], a[84:87], v[40:43]\n"
"ds_read_b64_tr_b16 v[104:105], v231 offset:40960\n"
"v_mfma_f32_16x16x32_bf16 v[40:43], v[188:191], a[88:91], v[40:43]\n"
"ds_read_b64_tr_b16 v[108:109], v235 offset:40960\n"
"v_subrev_f32 v8, s22, v8\n"
"v_mfma_f32_16x16x32_bf16 v[40:43], v[192:195], a[92:95], v[40:43]\n"
"ds_read_b64_tr_b16 v[116:117], v235 offset:49152\n"
"v_mfma_f32_16x16x32_bf16 v[40:43], v[208:211], a[32:35], v[40:43]\n"
"ds_read_b64_tr_b16 v[122:123], v231 offset:59392\n"
"v_subrev_f32 v9, s22, v9\n"
"v_mfma_f32_16x16x32_bf16 v[40:43], v[212:215], a[36:39], v[40:43]\n"
"ds_read_b64_tr_b16 v[120:121], v231 offset:57344\n"
"v_subrev_f32 v10, s22, v10\n"
"v_mfma_f32_16x16x32_bf16 v[40:43], v[216:219], a[40:43], v[40:43]\n"
"ds_read_b64_tr_b16 v[224:225], v235 offset:57344\n"
"ds_read_b64_tr_b16 v[226:227], v235 offset:59392\n"
"v_mfma_f32_16x16x32_bf16 v[40:43], v[220:223], a[44:47], v[40:43]\n"
"s_waitcnt lgkmcnt(8)\n"
"v_subrev_f32 v11, s22, v11\n"
"v_mfma_f32_16x16x32_bf16 v[12:15], v[96:99], a[48:51], v[12:15]\n"
"v_mfma_f32_16x16x32_bf16 v[12:15], v[100:103], a[52:55], v[12:15]\n"
"s_waitcnt lgkmcnt(6)\n"
"v_mov_b32 v160, 0\n"
"v_subrev_f32 v24, s22, v24\n"
"v_mfma_f32_16x16x32_bf16 v[12:15], v[104:107], a[56:59], v[12:15]\n"
"s_waitcnt lgkmcnt(5)\n"
"v_mfma_f32_16x16x32_bf16 v[12:15], v[108:111], a[60:63], v[12:15]\n"
"v_mfma_f32_16x16x32_bf16 v[12:15], v[112:115], a[0:3], v[12:15]\n"
"s_waitcnt lgkmcnt(3)\n"
"v_cvt_pk_bf16_f32 v8, v8, v9\n"
"v_mfma_f32_16x16x32_bf16 v[12:15], v[116:119], a[4:7], v[12:15]\n"
"s_waitcnt lgkmcnt(2)\n"
"v_mfma_f32_16x16x32_bf16 v[12:15], v[120:123], a[8:11], v[12:15]\n"
"s_waitcnt lgkmcnt(0)\n"
"v_mov_b32 v161, 0\n"
"v_subrev_f32 v25, s22, v25\n"
"v_mfma_f32_16x16x32_bf16 v[12:15], v[224:227], a[12:15], v[12:15]\n"
"v_subrev_f32 v26, s22, v26\n"
"s_waitcnt vmcnt(5)\n"
"v_mfma_f32_16x16x32_bf16 v[64:67], v[162:165], v[72:75], 0\n"
"v_subrev_f32 v40, s22, v40\n"
"v_subrev_f32 v43, s22, v43\n"
"v_mfma_f32_16x16x32_bf16 v[64:67], v[162:165], v[76:79], v[64:67]\n"
"v_subrev_f32 v41, s22, v41\n"
"v_mfma_f32_16x16x32_bf16 v[64:67], v[162:165], v[80:83], v[64:67]\n"
"v_mfma_f32_16x16x32_bf16 v[64:67], v[162:165], v[84:87], v[64:67]\n"
"v_subrev_f32 v13, s22, v13\n"
"v_mfma_f32_16x16x32_bf16 v[64:67], v[162:165], v[88:91], v[64:67]\n"
"v_mfma_f32_16x16x32_bf16 v[64:67], v[162:165], v[92:95], v[64:67]\n"
"v_subrev_f32 v15, s22, v15\n"
"v_cvt_pk_bf16_f32 v40, v40, v41\n"
"v_mfma_f32_16x16x32_bf16 v[64:67], v[162:165], v[244:247], v[64:67]\n"
"v_subrev_f32 v14, s22, v14\n"
"v_cvt_pk_bf16_f32 v9, v10, v11\n"
"v_mfma_f32_16x16x32_bf16 v[64:67], v[162:165], v[156:159], v[64:67]\n"
"v_subrev_f32 v12, s22, v12\n"
"v_cvt_pk_bf16_f32 v24, v24, v25\n"
"v_mfma_f32_16x16x32_bf16 v[68:71], v[160:163], v[72:75], 0\n"
"v_cvt_pk_bf16_f32 v10, v12, v13\n"
"v_cvt_pk_bf16_f32 v11, v14, v15\n"
"v_mfma_f32_16x16x32_bf16 v[68:71], v[160:163], v[76:79], v[68:71]\n"
"s_nop 0\n"
"v_permlane16_swap_b32 v9, v11\n"
"v_mfma_f32_16x16x32_bf16 v[68:71], v[160:163], v[80:83], v[68:71]\n"
"v_cvt_pk_bf16_f32 v64, v64, v65\n"
"v_mfma_f32_16x16x32_bf16 v[68:71], v[160:163], v[84:87], v[68:71]\n"
"v_permlane16_swap_b32 v8, v10\n"
"v_mfma_f32_16x16x32_bf16 v[68:71], v[160:163], v[88:91], v[68:71]\n"
"v_subrev_f32 v42, s22, v42\n"
"v_mfma_f32_16x16x32_bf16 v[68:71], v[160:163], v[92:95], v[68:71]\n"
"buffer_store_dwordx4 v[8:11], v239, s[36:39], 0 offen sc0 sc1\n"
"v_mfma_f32_16x16x32_bf16 v[68:71], v[160:163], v[244:247], v[68:71]\n"
"v_mfma_f32_16x16x32_bf16 v[68:71], v[160:163], v[156:159], v[68:71]\n"
"v_cvt_pk_bf16_f32 v65, v66, v67\n"
"v_mfma_f32_16x16x32_bf16 v[28:31], v[224:227], a[28:31], v[28:31]\n"
"v_subrev_f32 v27, s22, v27\n"
"v_mfma_f32_16x16x32_bf16 v[28:31], v[120:123], a[24:27], v[28:31]\n"
"v_mfma_f32_16x16x32_bf16 v[28:31], v[116:119], a[20:23], v[28:31]\n"
"v_mfma_f32_16x16x32_bf16 v[28:31], v[112:115], a[16:19], v[28:31]\n"
"s_nop 1\n"
"v_cvt_pk_bf16_f32 v66, v68, v69\n"
"v_cvt_pk_bf16_f32 v25, v26, v27\n"
"v_mfma_f32_16x16x32_bf16 v[28:31], v[108:111], a[76:79], v[28:31]\n"
"v_permlane16_swap_b32 v64, v66\n"
"v_mfma_f32_16x16x32_bf16 v[28:31], v[104:107], a[72:75], v[28:31]\n"
"v_cvt_pk_bf16_f32 v67, v70, v71\n"
"v_cvt_pk_bf16_f32 v41, v42, v43\n"
"v_mfma_f32_16x16x32_bf16 v[28:31], v[100:103], a[68:71], v[28:31]\n"
"v_permlane16_swap_b32 v65, v67\n"
"v_mfma_f32_16x16x32_bf16 v[28:31], v[96:99], a[64:67], v[28:31]\n"
"buffer_store_dwordx4 v[64:67], v253, s[36:39], 0 offen offset:128 sc0 sc1\n"
"v_mfma_f32_16x16x32_bf16 v[44:47], v[96:99], a[80:83], v[44:47]\n"
"v_mfma_f32_16x16x32_bf16 v[44:47], v[100:103], a[84:87], v[44:47]\n"
"v_mfma_f32_16x16x32_bf16 v[44:47], v[104:107], a[88:91], v[44:47]\n"
"s_nop 3\n"
"v_subrev_f32 v29, s22, v29\n"
"v_subrev_f32 v30, s22, v30\n"
"v_mfma_f32_16x16x32_bf16 v[44:47], v[108:111], a[92:95], v[44:47]\n"
"v_subrev_f32 v28, s22, v28\n"
"v_subrev_f32 v31, s22, v31\n"
"v_mfma_f32_16x16x32_bf16 v[44:47], v[112:115], a[32:35], v[44:47]\n"
"v_cvt_pk_bf16_f32 v26, v28, v29\n"
"v_cvt_pk_bf16_f32 v27, v30, v31\n"
"v_mfma_f32_16x16x32_bf16 v[44:47], v[116:119], a[36:39], v[44:47]\n"
"v_permlane16_swap_b32 v24, v26\n"
"v_mfma_f32_16x16x32_bf16 v[44:47], v[120:123], a[40:43], v[44:47]\n"
"v_permlane16_swap_b32 v25, v27\n"
"v_mfma_f32_16x16x32_bf16 v[44:47], v[224:227], a[44:47], v[44:47]\n"
"buffer_store_dwordx4 v[24:27], v239, s[36:39], s52 offen sc0 sc1\n"
"s_nop 6\n"
"v_subrev_f32 v44, s22, v44\n"
"v_subrev_f32 v45, s22, v45\n"
"v_subrev_f32 v46, s22, v46\n"
"v_subrev_f32 v47, s22, v47\n"
"v_cvt_pk_bf16_f32 v42, v44, v45\n"
"v_cvt_pk_bf16_f32 v43, v46, v47\n"
"s_nop 0\n"
"v_permlane16_swap_b32 v40, v42\n"
"v_permlane16_swap_b32 v41, v43\n"
"buffer_store_dwordx4 v[40:43], v239, s[36:39], s53 offen sc0 sc1\n"
"s_branch LFIN_%=\n"
"LSW_%=:\n"
"s_add_u32 s16, s8, s95\n"
"s_addc_u32 s17, s9, 0\n"
"s_sub_u32 s18, 655360, s95\n"
"s_add_u32 s20, s8, s100\n"
"s_addc_u32 s21, s9, 0\n"
"s_sub_u32 s22, 655360, s100\n"
"s_mov_b32 s56, 0\n"
"s_mov_b32 s55, 0\n"
"s_mov_b32 s58, 0\n"
"s_mov_b32 s59, 163840\n"
"s_mov_b32 s52, 9216\n"
"s_mov_b32 s53, 18432\n"
"s_mov_b32 s54, 27648\n"
"v_lshrrev_b32 v206, 7, v242\n"
"v_mul_u32_u24 v206, 1280, v206\n"
"v_and_b32 v240, 0x7f, v240\n"
"v_add_u32 v240, v206, v240\n"
"buffer_load_dword v252, v240, s[16:19], 0 offen\n"
"s_branch LSWK_%=\n"
"LGP_%=:\n"
"s_mov_b32 s80, 0x40000\n"
"LGPa_%=:\n"
"s_sleep 1\n"
"global_load_dword v200, v201, s[6:7] sc1\n"
"s_sub_u32 s80, s80, 1\n"
"s_cmp_eq_u32 s80, 0\n"
"s_waitcnt vmcnt(0)\n"
"s_cbranch_scc1 LGPK_%=\n"
"v_subrev_u32 v200, s21, v200\n"
"v_cmp_lt_u32 vcc, s69, v200\n"
"s_nop 4\n"
"s_cbranch_vccnz LGPa_%=\n"
"s_branch LGPK_%=\n"
"LGQ_%=:\n"
"s_mov_b32 s80, 0x40000\n"
"LGQa_%=:\n"
"s_sleep 1\n"
"global_load_dword v200, v201, s[6:7] sc1\n"
"s_sub_u32 s80, s80, 1\n"
"s_cmp_eq_u32 s80, 0\n"
"s_waitcnt vmcnt(0)\n"
"s_cbranch_scc1 LGQK_%=\n"
"v_subrev_u32 v200, s21, v200\n"
"v_cmp_lt_u32 vcc, s69, v200\n"
"s_nop 4\n"
"s_cbranch_vccnz LGQa_%=\n"
"s_branch LGQK_%=\n"
"LGR_%=:\n"
"s_mov_b32 s80, 0x40000\n"
"LGRa_%=:\n"
"s_sleep 1\n"
"global_load_dword v200, v201, s[6:7] sc1\n"
"s_sub_u32 s80, s80, 1\n"
"s_cmp_eq_u32 s80, 0\n"
"s_waitcnt vmcnt(0)\n"
"s_cbranch_scc1 LGRK_%=\n"
"v_subrev_u32 v200, s21, v200\n"
"v_cmp_lt_u32 vcc, s69, v200\n"
"s_nop 4\n"
"s_cbranch_vccnz LGRa_%=\n"
"s_branch LGRK_%=\n"
"LGV_%=:\n"
"s_mov_b32 s80, 0x40000\n"
"LGVa_%=:\n"
"s_sleep 1\n"
"global_load_dword v200, v201, s[6:7] sc1\n"
"s_sub_u32 s80, s80, 1\n"
"s_cmp_eq_u32 s80, 0\n"
"s_waitcnt vmcnt(0)\n"
"s_cbranch_scc1 LGVK_%=\n"
"v_subrev_u32 v200, s21, v200\n"
"v_cmp_lt_u32 vcc, s69, v200\n"
"s_nop 4\n"
"s_cbranch_vccnz LGVa_%=\n"
"s_branch LGVK_%=\n"
"LEP_%=:\n"
"s_add_u32 s92, s92, 1\n"
"v_mov_b32 v200, s92\n"
"v_mov_b32 v255, 0\n"
"s_mov_b64 exec, 1\n"
"global_store_dword v255, v200, s[6:7] offset:128 sc1\n"
"s_mov_b64 exec, -1\n"
"s_branch LEPK_%=\n"
"LPCE0_%=:\n"
"s_memrealtime s[80:81]\n"
"s_sub_u32 s82, s67, s66\n"
"s_cmp_gt_i32 s82, 0\n"
"s_cselect_b32 s83, s67, s66\n"
"s_waitcnt lgkmcnt(0)\n"
"s_mov_b32 s63, 0\n"
"s_mov_b32 s62, 126\n"
"s_sub_u32 s82, s83, s80\n"
"s_cmp_gt_i32 s82, 3\n"
"s_cbranch_scc0 LPEE0_%=\n"
"LPDE0_%=:\n"
"s_sleep 1\n"
"s_memrealtime s[80:81]\n"
"s_waitcnt lgkmcnt(0)\n"
"s_sub_u32 s82, s83, s80\n"
"s_cmp_gt_i32 s82, 3\n"
"s_cbranch_scc1 LPDE0_%=\n"
"LPEE0_%=:\n"
"s_add_u32 s66, s66, s72\n"
"s_add_u32 s66, s66, 4\n"
"s_add_u32 s67, s80, s72\n"
"s_sub_u32 s72, s72, 0\n"
"s_max_u32 s72, s72, s62\n"
"s_branch LPKE0_%=\n"
"LPCE1_%=:\n"
"s_memrealtime s[80:81]\n"
"s_sub_u32 s82, s67, s66\n"
"s_cmp_gt_i32 s82, 0\n"
"s_cselect_b32 s83, s67, s66\n"
"s_waitcnt lgkmcnt(0)\n"
"s_sub_u32 s82, s83, s80\n"
"s_cmp_gt_i32 s82, 3\n"
"s_cbranch_scc0 LPEE1_%=\n"
"LPDE1_%=:\n"
"s_sleep 1\n"
"s_memrealtime s[80:81]\n"
"s_waitcnt lgkmcnt(0)\n"
"s_sub_u32 s82, s83, s80\n"
"s_cmp_gt_i32 s82, 3\n"
"s_cbranch_scc1 LPDE1_%=\n"
"LPEE1_%=:\n"
"s_add_u32 s66, s66, s72\n"
"s_add_u32 s66, s66, 4\n"
"s_add_u32 s67, s80, s72\n"
"s_sub_u32 s72, s72, 0\n"
"s_max_u32 s72, s72, s62\n"
"s_branch LPKE1_%=\n"
"LPCT0_%=:\n"
"s_memrealtime s[80:81]\n"
"s_sub_u32 s82, s67, s66\n"
"s_cmp_gt_i32 s82, 0\n"
"s_cselect_b32 s83, s67, s66\n"
"s_waitcnt lgkmcnt(0)\n"
"s_sub_u32 s82, s83, s80\n"
"s_cmp_gt_i32 s82, 3\n"
"s_cbranch_scc0 LPET0_%=\n"
"LPDT0_%=:\n"
"s_sleep 1\n"
"s_memrealtime s[80:81]\n"
"s_waitcnt lgkmcnt(0)\n"
"s_sub_u32 s82, s83, s80\n"
"s_cmp_gt_i32 s82, 3\n"
"s_cbranch_scc1 LPDT0_%=\n"
"LPET0_%=:\n"
"s_add_u32 s66, s66, s72\n"
"s_add_u32 s66, s66, 4\n"
"s_add_u32 s67, s80, s72\n"
"s_sub_u32 s72, s72, 0\n"
"s_max_u32 s72, s72, s62\n"
"s_branch LPKT0_%=\n"
"LPCT1_%=:\n"
"s_memrealtime s[80:81]\n"
"s_sub_u32 s82, s67, s66\n"
"s_cmp_gt_i32 s82, 0\n"
"s_cselect_b32 s83, s67, s66\n"
"s_waitcnt lgkmcnt(0)\n"
"s_sub_u32 s82, s83, s80\n"
"s_cmp_gt_i32 s82, 3\n"
"s_cbranch_scc0 LPET1_%=\n"
"LPDT1_%=:\n"
"s_sleep 1\n"
"s_memrealtime s[80:81]\n"
"s_waitcnt lgkmcnt(0)\n"
"s_sub_u32 s82, s83, s80\n"
"s_cmp_gt_i32 s82, 3\n"
"s_cbranch_scc1 LPDT1_%=\n"
"LPET1_%=:\n"
"s_add_u32 s66, s66, s72\n"
"s_add_u32 s66, s66, 4\n"
"s_add_u32 s67, s80, s72\n"
"s_sub_u32 s72, s72, 0\n"
"s_max_u32 s72, s72, s62\n"
"s_branch LPKT1_%=\n"
"LPCL0_%=:\n"
"s_memrealtime s[80:81]\n"
"s_sub_u32 s82, s67, s66\n"
"s_cmp_gt_i32 s82, 0\n"
"s_cselect_b32 s83, s67, s66\n"
"s_waitcnt lgkmcnt(0)\n"
"s_sub_u32 s82, s80, s67\n"
"s_add_u32 s63, s63, 1\n"
"s_min_i32 s81, s63, 0\n"
"s_cmp_gt_i32 s82, 20\n"
"s_cselect_b32 s63, s63, s81\n"
"s_cmp_ge_i32 s63, 2\n"
"s_cbranch_scc0 LPRL0_%=\n"
"s_mov_b32 s63, -9\n"
"s_add_u32 s62, s62, 10\n"
"s_min_u32 s62, s62, 146\n"
"s_max_u32 s72, s72, s62\n"
"s_add_u32 s83, s80, 200\n"
"s_and_b32 s83, s83, -1\n"
"LPRL0_%=:\n"
"s_sub_u32 s82, s83, s80\n"
"s_cmp_gt_i32 s82, 3\n"
"s_cbranch_scc0 LPEL0_%=\n"
"LPDL0_%=:\n"
"s_sleep 1\n"
"s_memrealtime s[80:81]\n"
"s_waitcnt lgkmcnt(0)\n"
"s_sub_u32 s82, s83, s80\n"
"s_cmp_gt_i32 s82, 3\n"
"s_cbranch_scc1 LPDL0_%=\n"
"LPEL0_%=:\n"
"s_add_u32 s66, s66, s72\n"
"s_add_u32 s66, s66, 4\n"
"s_add_u32 s67, s80, s72\n"
"s_sub_u32 s72, s72, 0\n"
"s_max_u32 s72, s72, s62\n"
"s_branch LPKL0_%=\n"
"LPCL1_%=:\n"
"s_memrealtime s[80:81]\n"
"s_sub_u32 s82, s67, s66\n"
"s_cmp_gt_i32 s82, 0\n"
"s_cselect_b32 s83, s67, s66\n"
"s_waitcnt lgkmcnt(0)\n"
"s_sub_u32 s82, s80, s67\n"
"s_add_u32 s63, s63, 1\n"
"s_min_i32 s81, s63, 0\n"
"s_cmp_gt_i32 s82, 20\n"
"s_cselect_b32 s63, s63, s81\n"
"s_cmp_ge_i32 s63, 2\n"
"s_cbranch_scc0 LPRL1_%=\n"
"s_mov_b32 s63, -9\n"
"s_add_u32 s62, s62, 10\n"
"s_min_u32 s62, s62, 146\n"
"s_max_u32 s72, s72, s62\n"
"s_add_u32 s83, s80, 200\n"
"s_and_b32 s83, s83, -1\n"
"LPRL1_%=:\n"
"s_sub_u32 s82, s83, s80\n"
"s_cmp_gt_i32 s82, 3\n"
"s_cbranch_scc0 LPEL1_%=\n"
"LPDL1_%=:\n"
"s_sleep 1\n"
"s_memrealtime s[80:81]\n"
"s_waitcnt lgkmcnt(0)\n"
"s_sub_u32 s82, s83, s80\n"
"s_cmp_gt_i32 s82, 3\n"
"s_cbranch_scc1 LPDL1_%=\n"
"LPEL1_%=:\n"
"s_add_u32 s66, s66, s72\n"
"s_add_u32 s66, s66, 4\n"
"s_add_u32 s67, s80, s72\n"
"s_sub_u32 s72, s72, 0\n"
"s_max_u32 s72, s72, s62\n"
"s_branch LPKL1_%=\n"
"LPCL2_%=:\n"
"s_memrealtime s[80:81]\n"
"s_sub_u32 s82, s67, s66\n"
"s_cmp_gt_i32 s82, 0\n"
"s_cselect_b32 s83, s67, s66\n"
"s_waitcnt lgkmcnt(0)\n"
"s_sub_u32 s82, s80, s67\n"
"s_add_u32 s63, s63, 1\n"
"s_min_i32 s81, s63, 0\n"
"s_cmp_gt_i32 s82, 20\n"
"s_cselect_b32 s63, s63, s81\n"
"s_cmp_ge_i32 s63, 2\n"
"s_cbranch_scc0 LPRL2_%=\n"
"s_mov_b32 s63, -9\n"
"s_add_u32 s62, s62, 10\n"
"s_min_u32 s62, s62, 146\n"
"s_max_u32 s72, s72, s62\n"
"s_add_u32 s83, s80, 200\n"
"s_and_b32 s83, s83, -1\n"
"LPRL2_%=:\n"
"s_sub_u32 s82, s83, s80\n"
"s_cmp_gt_i32 s82, 3\n"
"s_cbranch_scc0 LPEL2_%=\n"
"LPDL2_%=:\n"
"s_sleep 1\n"
"s_memrealtime s[80:81]\n"
"s_waitcnt lgkmcnt(0)\n"
"s_sub_u32 s82, s83, s80\n"
"s_cmp_gt_i32 s82, 3\n"
"s_cbranch_scc1 LPDL2_%=\n"
"LPEL2_%=:\n"
"s_add_u32 s66, s66, s72\n"
"s_add_u32 s66, s66, 4\n"
"s_add_u32 s67, s80, s72\n"
"s_sub_u32 s72, s72, 0\n"
"s_max_u32 s72, s72, s62\n"
"s_branch LPKL2_%=\n"
"LPCL3_%=:\n"
"s_memrealtime s[80:81]\n"
"s_sub_u32 s82, s67, s66\n"
"s_cmp_gt_i32 s82, 0\n"
"s_cselect_b32 s83, s67, s66\n"
"s_waitcnt lgkmcnt(0)\n"
"s_sub_u32 s82, s80, s67\n"
"s_add_u32 s63, s63, 1\n"
"s_min_i32 s81, s63, 0\n"
"s_cmp_gt_i32 s82, 20\n"
"s_cselect_b32 s63, s63, s81\n"
"s_cmp_ge_i32 s63, 2\n"
"s_cbranch_scc0 LPRL3_%=\n"
"s_mov_b32 s63, -9\n"
"s_add_u32 s62, s62, 10\n"
"s_min_u32 s62, s62, 146\n"
"s_max_u32 s72, s72, s62\n"
"s_add_u32 s83, s80, 200\n"
"s_and_b32 s83, s83, -1\n"
"LPRL3_%=:\n"
"s_sub_u32 s82, s83, s80\n"
"s_cmp_gt_i32 s82, 3\n"
"s_cbranch_scc0 LPEL3_%=\n"
"LPDL3_%=:\n"
"s_sleep 1\n"
"s_memrealtime s[80:81]\n"
"s_waitcnt lgkmcnt(0)\n"
"s_sub_u32 s82, s83, s80\n"
"s_cmp_gt_i32 s82, 3\n"
"s_cbranch_scc1 LPDL3_%=\n"
"LPEL3_%=:\n"
"s_add_u32 s66, s66, s72\n"
"s_add_u32 s66, s66, 4\n"
"s_add_u32 s67, s80, s72\n"
"s_sub_u32 s72, s72, 0\n"
"s_max_u32 s72, s72, s62\n"
"s_branch LPKL3_%=\n"
"LEND_%=:\n"
"LSLOW_%=:\n"
"s_waitcnt vmcnt(0) lgkmcnt(0)\n"
"s_nop 9\n"
"v_bfe_u32 v148, v242, 4, 4\n"
"v_bfe_u32 v149, v242, 8, 2\n"
"v_lshlrev_b32 v144, 8, v148\n"
"v_lshl_add_u32 v144, v149, 4, v144\n"
"v_add_u32 v144, s43, v144\n"
"v_xor_b32 v145, 64, v144\n"
"v_lshlrev_b32 v146, 1, v148\n"
"v_lshl_add_u32 v146, v149, 10, v146\n"
"v_add_u32 v146, s43, v146\n"
"v_xor_b32 v147, 64, v146\n"
"v_mov_b32 v152, 0xff7fffff\n"
"v_mov_b32 v155, 0\n"
"v_mov_b32 v0, 0\n"
"v_mov_b32 v1, 0\n"
"v_mov_b32 v2, 0\n"
"v_mov_b32 v3, 0\n"
"v_mov_b32 v4, 0\n"
"v_mov_b32 v5, 0\n"
"v_mov_b32 v6, 0\n"
"v_mov_b32 v7, 0\n"
"v_mov_b32 v8, 0\n"
"v_mov_b32 v9, 0\n"
"v_mov_b32 v10, 0\n"
"v_mov_b32 v11, 0\n"
"v_mov_b32 v12, 0\n"
"v_mov_b32 v13, 0\n"
"v_mov_b32 v14, 0\n"
"v_mov_b32 v15, 0\n"
"v_mov_b32 v16, 0\n"
"v_mov_b32 v17, 0\n"
"v_mov_b32 v18, 0\n"
"v_mov_b32 v19, 0\n"
"v_mov_b32 v20, 0\n"
"v_mov_b32 v21, 0\n"
"v_mov_b32 v22, 0\n"
"v_mov_b32 v23, 0\n"
"v_mov_b32 v24, 0\n"
"v_mov_b32 v25, 0\n"
"v_mov_b32 v26, 0\n"
"v_mov_b32 v27, 0\n"
"v_mov_b32 v28, 0\n"
"v_mov_b32 v29, 0\n"
"v_mov_b32 v30, 0\n"
"v_mov_b32 v31, 0\n"
"v_mov_b32 v156, 0xff7fffff\n"
"v_mov_b32 v159, 0\n"
"v_mov_b32 v32, 0\n"
"v_mov_b32 v33, 0\n"
"v_mov_b32 v34, 0\n"
"v_mov_b32 v35, 0\n"
"v_mov_b32 v36, 0\n"
"v_mov_b32 v37, 0\n"
"v_mov_b32 v38, 0\n"
"v_mov_b32 v39, 0\n"
"v_mov_b32 v40, 0\n"
"v_mov_b32 v41, 0\n"
"v_mov_b32 v42, 0\n"
"v_mov_b32 v43, 0\n"
"v_mov_b32 v44, 0\n"
"v_mov_b32 v45, 0\n"
"v_mov_b32 v46, 0\n"
"v_mov_b32 v47, 0\n"
"v_mov_b32 v48, 0\n"
"v_mov_b32 v49, 0\n"
"v_mov_b32 v50, 0\n"
"v_mov_b32 v51, 0\n"
"v_mov_b32 v52, 0\n"
"v_mov_b32 v53, 0\n"
"v_mov_b32 v54, 0\n"
"v_mov_b32 v55, 0\n"
"v_mov_b32 v56, 0\n"
"v_mov_b32 v57, 0\n"
"v_mov_b32 v58, 0\n"
"v_mov_b32 v59, 0\n"
"v_mov_b32 v60, 0\n"
"v_mov_b32 v61, 0\n"
"v_mov_b32 v62, 0\n"
"v_mov_b32 v63, 0\n"
"v_mov_b32 v160, 0xff7fffff\n"
"v_mov_b32 v163, 0\n"
"v_mov_b32 v64, 0\n"
"v_mov_b32 v65, 0\n"
"v_mov_b32 v66, 0\n"
"v_mov_b32 v67, 0\n"
"v_mov_b32 v68, 0\n"
"v_mov_b32 v69, 0\n"
"v_mov_b32 v70, 0\n"
"v_mov_b32 v71, 0\n"
"v_mov_b32 v72, 0\n"
"v_mov_b32 v73, 0\n"
"v_mov_b32 v74, 0\n"
"v_mov_b32 v75, 0\n"
"v_mov_b32 v76, 0\n"
"v_mov_b32 v77, 0\n"
"v_mov_b32 v78, 0\n"
"v_mov_b32 v79, 0\n"
"v_mov_b32 v80, 0\n"
"v_mov_b32 v81, 0\n"
"v_mov_b32 v82, 0\n"
"v_mov_b32 v83, 0\n"
"v_mov_b32 v84, 0\n"
"v_mov_b32 v85, 0\n"
"v_mov_b32 v86, 0\n"
"v_mov_b32 v87, 0\n"
"v_mov_b32 v88, 0\n"
"v_mov_b32 v89, 0\n"
"v_mov_b32 v90, 0\n"
"v_mov_b32 v91, 0\n"
"v_mov_b32 v92, 0\n"
"v_mov_b32 v93, 0\n"
"v_mov_b32 v94, 0\n"
"v_mov_b32 v95, 0\n"
"v_mov_b32 v164, 0xff7fffff\n"
"v_mov_b32 v167, 0\n"
"v_accvgpr_write_b32 a208, 0\n"
"v_accvgpr_write_b32 a209, 0\n"
"v_accvgpr_write_b32 a210, 0\n"
"v_accvgpr_write_b32 a211, 0\n"
"v_accvgpr_write_b32 a212, 0\n"
"v_accvgpr_write_b32 a213, 0\n"
"v_accvgpr_write_b32 a214, 0\n"
"v_accvgpr_write_b32 a215, 0\n"
"v_accvgpr_write_b32 a216, 0\n"
"v_accvgpr_write_b32 a217, 0\n"
"v_accvgpr_write_b32 a218, 0\n"
"v_accvgpr_write_b32 a219, 0\n"
"v_accvgpr_write_b32 a220, 0\n"
"v_accvgpr_write_b32 a221, 0\n"
"v_accvgpr_write_b32 a222, 0\n"
"v_accvgpr_write_b32 a223, 0\n"
"v_accvgpr_write_b32 a224, 0\n"
"v_accvgpr_write_b32 a225, 0\n"
"v_accvgpr_write_b32 a226, 0\n"
"v_accvgpr_write_b32 a227, 0\n"
"v_accvgpr_write_b32 a228, 0\n"
"v_accvgpr_write_b32 a229, 0\n"
"v_accvgpr_write_b32 a230, 0\n"
"v_accvgpr_write_b32 a231, 0\n"
"v_accvgpr_write_b32 a232, 0\n"
"v_accvgpr_write_b32 a233, 0\n"
"v_accvgpr_write_b32 a234, 0\n"
"v_accvgpr_write_b32 a235, 0\n"
"v_accvgpr_write_b32 a236, 0\n"
"v_accvgpr_write_b32 a237, 0\n"
"v_accvgpr_write_b32 a238, 0\n"
"v_accvgpr_write_b32 a239, 0\n"
"s_lshl_b32 s80, s47, 16\n"
"s_sub_u32 s80, s80, 261248\n"
"s_add_u32 s81, s80, 4096\n"
"s_mov_b32 s82, 0\n"
"LSLOOP_%=:\n"
"s_lshl_b32 s83, s82, 6\n"
"s_and_b32 s83, s83, 64\n"
"buffer_load_dwordx4 v[128:131], v144, s[84:87], s80 offen offset:0\n"
"buffer_load_dwordx4 v[132:135], v145, s[84:87], s80 offen offset:0\n"
"buffer_load_dwordx4 v[136:139], v144, s[84:87], s80 offen offset:128\n"
"buffer_load_dwordx4 v[140:143], v145, s[84:87], s80 offen offset:128\n"
"s_waitcnt vmcnt(0)\n"
"v_mfma_f32_16x16x32_bf16 v[96:99], v[128:131], a[0:3], 0\n"
"v_mfma_f32_16x16x32_bf16 v[96:99], v[132:135], a[4:7], v[96:99]\n"
"v_mfma_f32_16x16x32_bf16 v[96:99], v[136:139], a[8:11], v[96:99]\n"
"v_mfma_f32_16x16x32_bf16 v[96:99], v[140:143], a[12:15], v[96:99]\n"
"v_mfma_f32_16x16x32_bf16 v[104:107], v[128:131], a[16:19], 0\n"
"v_mfma_f32_16x16x32_bf16 v[104:107], v[132:135], a[20:23], v[104:107]\n"
"v_mfma_f32_16x16x32_bf16 v[104:107], v[136:139], a[24:27], v[104:107]\n"
"v_mfma_f32_16x16x32_bf16 v[104:107], v[140:143], a[28:31], v[104:107]\n"
"v_mfma_f32_16x16x32_bf16 v[112:115], v[128:131], a[32:35], 0\n"
"v_mfma_f32_16x16x32_bf16 v[112:115], v[132:135], a[36:39], v[112:115]\n"
"v_mfma_f32_16x16x32_bf16 v[112:115], v[136:139], a[40:43], v[112:115]\n"
"v_mfma_f32_16x16x32_bf16 v[112:115], v[140:143], a[44:47], v[112:115]\n"
"v_mfma_f32_16x16x32_bf16 v[120:123], v[128:131], a[192:195], 0\n"
"v_mfma_f32_16x16x32_bf16 v[120:123], v[132:135], a[196:199], v[120:123]\n"
"v_mfma_f32_16x16x32_bf16 v[120:123], v[136:139], a[200:203], v[120:123]\n"
"v_mfma_f32_16x16x32_bf16 v[120:123], v[140:143], a[204:207], v[120:123]\n"
"s_nop 7\n"
"buffer_load_dwordx4 v[128:131], v144, s[84:87], s81 offen offset:0\n"
"buffer_load_dwordx4 v[132:135], v145, s[84:87], s81 offen offset:0\n"
"buffer_load_dwordx4 v[136:139], v144, s[84:87], s81 offen offset:128\n"
"buffer_load_dwordx4 v[140:143], v145, s[84:87], s81 offen offset:128\n"
"s_waitcnt vmcnt(0)\n"
"v_mfma_f32_16x16x32_bf16 v[100:103], v[128:131], a[0:3], 0\n"
"v_mfma_f32_16x16x32_bf16 v[100:103], v[132:135], a[4:7], v[100:103]\n"
"v_mfma_f32_16x16x32_bf16 v[100:103], v[136:139], a[8:11], v[100:103]\n"
"v_mfma_f32_16x16x32_bf16 v[100:103], v[140:143], a[12:15], v[100:103]\n"
"v_mfma_f32_16x16x32_bf16 v[108:111], v[128:131], a[16:19], 0\n"
"v_mfma_f32_16x16x32_bf16 v[108:111], v[132:135], a[20:23], v[108:111]\n"
"v_mfma_f32_16x16x32_bf16 v[108:111], v[136:139], a[24:27], v[108:111]\n"
"v_mfma_f32_16x16x32_bf16 v[108:111], v[140:143], a[28:31], v[108:111]\n"
"v_mfma_f32_16x16x32_bf16 v[116:119], v[128:131], a[32:35], 0\n"
"v_mfma_f32_16x16x32_bf16 v[116:119], v[132:135], a[36:39], v[116:119]\n"
"v_mfma_f32_16x16x32_bf16 v[116:119], v[136:139], a[40:43], v[116:119]\n"
"v_mfma_f32_16x16x32_bf16 v[116:119], v[140:143], a[44:47], v[116:119]\n"
"v_mfma_f32_16x16x32_bf16 v[124:127], v[128:131], a[192:195], 0\n"
"v_mfma_f32_16x16x32_bf16 v[124:127], v[132:135], a[196:199], v[124:127]\n"
"v_mfma_f32_16x16x32_bf16 v[124:127], v[136:139], a[200:203], v[124:127]\n"
"v_mfma_f32_16x16x32_bf16 v[124:127], v[140:143], a[204:207], v[124:127]\n"
"s_nop 7\n"
"v_max3_f32 v153, v96, v97, v98\n"
"v_max3_f32 v153, v153, v99, v100\n"
"v_max3_f32 v153, v153, v101, v102\n"
"v_max3_f32 v153, v153, v103, v152\n"
"v_mov_b32 v154, v153\n"
"s_nop 1\n"
"v_permlane32_swap_b32 v153, v154\n"
"v_max_f32 v153, v153, v154\n"
"v_mov_b32 v154, v153\n"
"s_nop 1\n"
"v_permlane16_swap_b32 v153, v154\n"
"v_max_f32 v153, v153, v154\n"
"v_sub_f32 v154, v152, v153\n"
"v_exp_f32 v154, v154\n"
"v_mov_b32 v152, v153\n"
"v_sub_f32 v96, v96, v152\n"
"v_sub_f32 v97, v97, v152\n"
"v_sub_f32 v98, v98, v152\n"
"v_sub_f32 v99, v99, v152\n"
"v_sub_f32 v100, v100, v152\n"
"v_sub_f32 v101, v101, v152\n"
"v_sub_f32 v102, v102, v152\n"
"v_sub_f32 v103, v103, v152\n"
"v_exp_f32 v96, v96\n"
"v_exp_f32 v97, v97\n"
"v_exp_f32 v98, v98\n"
"v_exp_f32 v99, v99\n"
"v_exp_f32 v100, v100\n"
"v_exp_f32 v101, v101\n"
"v_exp_f32 v102, v102\n"
"v_exp_f32 v103, v103\n"
"v_mul_f32 v155, v155, v154\n"
"v_add_f32 v155, v155, v96\n"
"v_add_f32 v155, v155, v97\n"
"v_add_f32 v155, v155, v98\n"
"v_add_f32 v155, v155, v99\n"
"v_add_f32 v155, v155, v100\n"
"v_add_f32 v155, v155, v101\n"
"v_add_f32 v155, v155, v102\n"
"v_add_f32 v155, v155, v103\n"
"v_cvt_pk_bf16_f32 v96, v96, v97\n"
"v_cvt_pk_bf16_f32 v97, v98, v99\n"
"v_cvt_pk_bf16_f32 v98, v100, v101\n"
"v_cvt_pk_bf16_f32 v99, v102, v103\n"
"v_mov_b32_dpp v154, v154 row_ror:12 row_mask:0x2 bank_mask:0xf\n"
"s_nop 1\n"
"v_mov_b32_dpp v154, v154 row_ror:8 row_mask:0x4 bank_mask:0xf\n"
"s_nop 1\n"
"v_mov_b32_dpp v154, v154 row_ror:4 row_mask:0x8 bank_mask:0xf\n"
"s_nop 1\n"
"v_mul_f32_dpp v0, v154, v0 row_newbcast:0 row_mask:0xf bank_mask:0xf\n"
"v_mul_f32_dpp v1, v154, v1 row_newbcast:1 row_mask:0xf bank_mask:0xf\n"
"v_mul_f32_dpp v2, v154, v2 row_newbcast:2 row_mask:0xf bank_mask:0xf\n"
"v_mul_f32_dpp v3, v154, v3 row_newbcast:3 row_mask:0xf bank_mask:0xf\n"
"v_mul_f32_dpp v4, v154, v4 row_newbcast:0 row_mask:0xf bank_mask:0xf\n"
"v_mul_f32_dpp v5, v154, v5 row_newbcast:1 row_mask:0xf bank_mask:0xf\n"
"v_mul_f32_dpp v6, v154, v6 row_newbcast:2 row_mask:0xf bank_mask:0xf\n"
"v_mul_f32_dpp v7, v154, v7 row_newbcast:3 row_mask:0xf bank_mask:0xf\n"
"v_mul_f32_dpp v8, v154, v8 row_newbcast:0 row_mask:0xf bank_mask:0xf\n"
"v_mul_f32_dpp v9, v154, v9 row_newbcast:1 row_mask:0xf bank_mask:0xf\n"
"v_mul_f32_dpp v10, v154, v10 row_newbcast:2 row_mask:0xf bank_mask:0xf\n"
"v_mul_f32_dpp v11, v154, v11 row_newbcast:3 row_mask:0xf bank_mask:0xf\n"
"v_mul_f32_dpp v12, v154, v12 row_newbcast:0 row_mask:0xf bank_mask:0xf\n"
"v_mul_f32_dpp v13, v154, v13 row_newbcast:1 row_mask:0xf bank_mask:0xf\n"
"v_mul_f32_dpp v14, v154, v14 row_newbcast:2 row_mask:0xf bank_mask:0xf\n"
"v_mul_f32_dpp v15, v154, v15 row_newbcast:3 row_mask:0xf bank_mask:0xf\n"
"v_mul_f32_dpp v16, v154, v16 row_newbcast:0 row_mask:0xf bank_mask:0xf\n"
"v_mul_f32_dpp v17, v154, v17 row_newbcast:1 row_mask:0xf bank_mask:0xf\n"
"v_mul_f32_dpp v18, v154, v18 row_newbcast:2 row_mask:0xf bank_mask:0xf\n"
"v_mul_f32_dpp v19, v154, v19 row_newbcast:3 row_mask:0xf bank_mask:0xf\n"
"v_mul_f32_dpp v20, v154, v20 row_newbcast:0 row_mask:0xf bank_mask:0xf\n"
"v_mul_f32_dpp v21, v154, v21 row_newbcast:1 row_mask:0xf bank_mask:0xf\n"
"v_mul_f32_dpp v22, v154, v22 row_newbcast:2 row_mask:0xf bank_mask:0xf\n"
"v_mul_f32_dpp v23, v154, v23 row_newbcast:3 row_mask:0xf bank_mask:0xf\n"
"v_mul_f32_dpp v24, v154, v24 row_newbcast:0 row_mask:0xf bank_mask:0xf\n"
"v_mul_f32_dpp v25, v154, v25 row_newbcast:1 row_mask:0xf bank_mask:0xf\n"
"v_mul_f32_dpp v26, v154, v26 row_newbcast:2 row_mask:0xf bank_mask:0xf\n"
"v_mul_f32_dpp v27, v154, v27 row_newbcast:3 row_mask:0xf bank_mask:0xf\n"
"v_mul_f32_dpp v28, v154, v28 row_newbcast:0 row_mask:0xf bank_mask:0xf\n"
"v_mul_f32_dpp v29, v154, v29 row_newbcast:1 row_mask:0xf bank_mask:0xf\n"
"v_mul_f32_dpp v30, v154, v30 row_newbcast:2 row_mask:0xf bank_mask:0xf\n"
"v_mul_f32_dpp v31, v154, v31 row_newbcast:3 row_mask:0xf bank_mask:0xf\n"
"v_max3_f32 v157, v104, v105, v106\n"
"v_max3_f32 v157, v157, v107, v108\n"
"v_max3_f32 v157, v157, v109, v110\n"
"v_max3_f32 v157, v157, v111, v156\n"
"v_mov_b32 v158, v157\n"
"s_nop 1\n"
"v_permlane32_swap_b32 v157, v158\n"
"v_max_f32 v157, v157, v158\n"
"v_mov_b32 v158, v157\n"
"s_nop 1\n"
"v_permlane16_swap_b32 v157, v158\n"
"v_max_f32 v157, v157, v158\n"
"v_sub_f32 v158, v156, v157\n"
"v_exp_f32 v158, v158\n"
"v_mov_b32 v156, v157\n"
"v_sub_f32 v104, v104, v156\n"
"v_sub_f32 v105, v105, v156\n"
"v_sub_f32 v106, v106, v156\n"
"v_sub_f32 v107, v107, v156\n"
"v_sub_f32 v108, v108, v156\n"
"v_sub_f32 v109, v109, v156\n"
"v_sub_f32 v110, v110, v156\n"
"v_sub_f32 v111, v111, v156\n"
"v_exp_f32 v104, v104\n"
"v_exp_f32 v105, v105\n"
"v_exp_f32 v106, v106\n"
"v_exp_f32 v107, v107\n"
"v_exp_f32 v108, v108\n"
"v_exp_f32 v109, v109\n"
"v_exp_f32 v110, v110\n"
"v_exp_f32 v111, v111\n"
"v_mul_f32 v159, v159, v158\n"
"v_add_f32 v159, v159, v104\n"
"v_add_f32 v159, v159, v105\n"
"v_add_f32 v159, v159, v106\n"
"v_add_f32 v159, v159, v107\n"
"v_add_f32 v159, v159, v108\n"
"v_add_f32 v159, v159, v109\n"
"v_add_f32 v159, v159, v110\n"
"v_add_f32 v159, v159, v111\n"
"v_cvt_pk_bf16_f32 v104, v104, v105\n"
"v_cvt_pk_bf16_f32 v105, v106, v107\n"
"v_cvt_pk_bf16_f32 v106, v108, v109\n"
"v_cvt_pk_bf16_f32 v107, v110, v111\n"
"v_mov_b32_dpp v158, v158 row_ror:12 row_mask:0x2 bank_mask:0xf\n"
"s_nop 1\n"
"v_mov_b32_dpp v158, v158 row_ror:8 row_mask:0x4 bank_mask:0xf\n"
"s_nop 1\n"
"v_mov_b32_dpp v158, v158 row_ror:4 row_mask:0x8 bank_mask:0xf\n"
"s_nop 1\n"
"v_mul_f32_dpp v32, v158, v32 row_newbcast:0 row_mask:0xf bank_mask:0xf\n"
"v_mul_f32_dpp v33, v158, v33 row_newbcast:1 row_mask:0xf bank_mask:0xf\n"
"v_mul_f32_dpp v34, v158, v34 row_newbcast:2 row_mask:0xf bank_mask:0xf\n"
"v_mul_f32_dpp v35, v158, v35 row_newbcast:3 row_mask:0xf bank_mask:0xf\n"
"v_mul_f32_dpp v36, v158, v36 row_newbcast:0 row_mask:0xf bank_mask:0xf\n"
"v_mul_f32_dpp v37, v158, v37 row_newbcast:1 row_mask:0xf bank_mask:0xf\n"
"v_mul_f32_dpp v38, v158, v38 row_newbcast:2 row_mask:0xf bank_mask:0xf\n"
"v_mul_f32_dpp v39, v158, v39 row_newbcast:3 row_mask:0xf bank_mask:0xf\n"
"v_mul_f32_dpp v40, v158, v40 row_newbcast:0 row_mask:0xf bank_mask:0xf\n"
"v_mul_f32_dpp v41, v158, v41 row_newbcast:1 row_mask:0xf bank_mask:0xf\n"
"v_mul_f32_dpp v42, v158, v42 row_newbcast:2 row_mask:0xf bank_mask:0xf\n"
"v_mul_f32_dpp v43, v158, v43 row_newbcast:3 row_mask:0xf bank_mask:0xf\n"
"v_mul_f32_dpp v44, v158, v44 row_newbcast:0 row_mask:0xf bank_mask:0xf\n"
"v_mul_f32_dpp v45, v158, v45 row_newbcast:1 row_mask:0xf bank_mask:0xf\n"
"v_mul_f32_dpp v46, v158, v46 row_newbcast:2 row_mask:0xf bank_mask:0xf\n"
"v_mul_f32_dpp v47, v158, v47 row_newbcast:3 row_mask:0xf bank_mask:0xf\n"
"v_mul_f32_dpp v48, v158, v48 row_newbcast:0 row_mask:0xf bank_mask:0xf\n"
"v_mul_f32_dpp v49, v158, v49 row_newbcast:1 row_mask:0xf bank_mask:0xf\n"
"v_mul_f32_dpp v50, v158, v50 row_newbcast:2 row_mask:0xf bank_mask:0xf\n"
"v_mul_f32_dpp v51, v158, v51 row_newbcast:3 row_mask:0xf bank_mask:0xf\n"
"v_mul_f32_dpp v52, v158, v52 row_newbcast:0 row_mask:0xf bank_mask:0xf\n"
"v_mul_f32_dpp v53, v158, v53 row_newbcast:1 row_mask:0xf bank_mask:0xf\n"
"v_mul_f32_dpp v54, v158, v54 row_newbcast:2 row_mask:0xf bank_mask:0xf\n"
"v_mul_f32_dpp v55, v158, v55 row_newbcast:3 row_mask:0xf bank_mask:0xf\n"
"v_mul_f32_dpp v56, v158, v56 row_newbcast:0 row_mask:0xf bank_mask:0xf\n"
"v_mul_f32_dpp v57, v158, v57 row_newbcast:1 row_mask:0xf bank_mask:0xf\n"
"v_mul_f32_dpp v58, v158, v58 row_newbcast:2 row_mask:0xf bank_mask:0xf\n"
"v_mul_f32_dpp v59, v158, v59 row_newbcast:3 row_mask:0xf bank_mask:0xf\n"
"v_mul_f32_dpp v60, v158, v60 row_newbcast:0 row_mask:0xf bank_mask:0xf\n"
"v_mul_f32_dpp v61, v158, v61 row_newbcast:1 row_mask:0xf bank_mask:0xf\n"
"v_mul_f32_dpp v62, v158, v62 row_newbcast:2 row_mask:0xf bank_mask:0xf\n"
"v_mul_f32_dpp v63, v158, v63 row_newbcast:3 row_mask:0xf bank_mask:0xf\n"
"v_max3_f32 v161, v112, v113, v114\n"
"v_max3_f32 v161, v161, v115, v116\n"
"v_max3_f32 v161, v161, v117, v118\n"
"v_max3_f32 v161, v161, v119, v160\n"
"v_mov_b32 v162, v161\n"
"s_nop 1\n"
"v_permlane32_swap_b32 v161, v162\n"
"v_max_f32 v161, v161, v162\n"
"v_mov_b32 v162, v161\n"
"s_nop 1\n"
"v_permlane16_swap_b32 v161, v162\n"
"v_max_f32 v161, v161, v162\n"
"v_sub_f32 v162, v160, v161\n"
"v_exp_f32 v162, v162\n"
"v_mov_b32 v160, v161\n"
"v_sub_f32 v112, v112, v160\n"
"v_sub_f32 v113, v113, v160\n"
"v_sub_f32 v114, v114, v160\n"
"v_sub_f32 v115, v115, v160\n"
"v_sub_f32 v116, v116, v160\n"
"v_sub_f32 v117, v117, v160\n"
"v_sub_f32 v118, v118, v160\n"
"v_sub_f32 v119, v119, v160\n"
"v_exp_f32 v112, v112\n"
"v_exp_f32 v113, v113\n"
"v_exp_f32 v114, v114\n"
"v_exp_f32 v115, v115\n"
"v_exp_f32 v116, v116\n"
"v_exp_f32 v117, v117\n"
"v_exp_f32 v118, v118\n"
"v_exp_f32 v119, v119\n"
"v_mul_f32 v163, v163, v162\n"
"v_add_f32 v163, v163, v112\n"
"v_add_f32 v163, v163, v113\n"
"v_add_f32 v163, v163, v114\n"
"v_add_f32 v163, v163, v115\n"
"v_add_f32 v163, v163, v116\n"
"v_add_f32 v163, v163, v117\n"
"v_add_f32 v163, v163, v118\n"
"v_add_f32 v163, v163, v119\n"
"v_cvt_pk_bf16_f32 v112, v112, v113\n"
"v_cvt_pk_bf16_f32 v113, v114, v115\n"
"v_cvt_pk_bf16_f32 v114, v116, v117\n"
"v_cvt_pk_bf16_f32 v115, v118, v119\n"
"v_mov_b32_dpp v162, v162 row_ror:12 row_mask:0x2 bank_mask:0xf\n"
"s_nop 1\n"
"v_mov_b32_dpp v162, v162 row_ror:8 row_mask:0x4 bank_mask:0xf\n"
"s_nop 1\n"
"v_mov_b32_dpp v162, v162 row_ror:4 row_mask:0x8 bank_mask:0xf\n"
"s_nop 1\n"
"v_mul_f32_dpp v64, v162, v64 row_newbcast:0 row_mask:0xf bank_mask:0xf\n"
"v_mul_f32_dpp v65, v162, v65 row_newbcast:1 row_mask:0xf bank_mask:0xf\n"
"v_mul_f32_dpp v66, v162, v66 row_newbcast:2 row_mask:0xf bank_mask:0xf\n"
"v_mul_f32_dpp v67, v162, v67 row_newbcast:3 row_mask:0xf bank_mask:0xf\n"
"v_mul_f32_dpp v68, v162, v68 row_newbcast:0 row_mask:0xf bank_mask:0xf\n"
"v_mul_f32_dpp v69, v162, v69 row_newbcast:1 row_mask:0xf bank_mask:0xf\n"
"v_mul_f32_dpp v70, v162, v70 row_newbcast:2 row_mask:0xf bank_mask:0xf\n"
"v_mul_f32_dpp v71, v162, v71 row_newbcast:3 row_mask:0xf bank_mask:0xf\n"
"v_mul_f32_dpp v72, v162, v72 row_newbcast:0 row_mask:0xf bank_mask:0xf\n"
"v_mul_f32_dpp v73, v162, v73 row_newbcast:1 row_mask:0xf bank_mask:0xf\n"
"v_mul_f32_dpp v74, v162, v74 row_newbcast:2 row_mask:0xf bank_mask:0xf\n"
"v_mul_f32_dpp v75, v162, v75 row_newbcast:3 row_mask:0xf bank_mask:0xf\n"
"v_mul_f32_dpp v76, v162, v76 row_newbcast:0 row_mask:0xf bank_mask:0xf\n"
"v_mul_f32_dpp v77, v162, v77 row_newbcast:1 row_mask:0xf bank_mask:0xf\n"
"v_mul_f32_dpp v78, v162, v78 row_newbcast:2 row_mask:0xf bank_mask:0xf\n"
"v_mul_f32_dpp v79, v162, v79 row_newbcast:3 row_mask:0xf bank_mask:0xf\n"
"v_mul_f32_dpp v80, v162, v80 row_newbcast:0 row_mask:0xf bank_mask:0xf\n"
"v_mul_f32_dpp v81, v162, v81 row_newbcast:1 row_mask:0xf bank_mask:0xf\n"
"v_mul_f32_dpp v82, v162, v82 row_newbcast:2 row_mask:0xf bank_mask:0xf\n"
"v_mul_f32_dpp v83, v162, v83 row_newbcast:3 row_mask:0xf bank_mask:0xf\n"
"v_mul_f32_dpp v84, v162, v84 row_newbcast:0 row_mask:0xf bank_mask:0xf\n"
"v_mul_f32_dpp v85, v162, v85 row_newbcast:1 row_mask:0xf bank_mask:0xf\n"
"v_mul_f32_dpp v86, v162, v86 row_newbcast:2 row_mask:0xf bank_mask:0xf\n"
"v_mul_f32_dpp v87, v162, v87 row_newbcast:3 row_mask:0xf bank_mask:0xf\n"
"v_mul_f32_dpp v88, v162, v88 row_newbcast:0 row_mask:0xf bank_mask:0xf\n"
"v_mul_f32_dpp v89, v162, v89 row_newbcast:1 row_mask:0xf bank_mask:0xf\n"
"v_mul_f32_dpp v90, v162, v90 row_newbcast:2 row_mask:0xf bank_mask:0xf\n"
"v_mul_f32_dpp v91, v162, v91 row_newbcast:3 row_mask:0xf bank_mask:0xf\n"
"v_mul_f32_dpp v92, v162, v92 row_newbcast:0 row_mask:0xf bank_mask:0xf\n"
"v_mul_f32_dpp v93, v162, v93 row_newbcast:1 row_mask:0xf bank_mask:0xf\n"
"v_mul_f32_dpp v94, v162, v94 row_newbcast:2 row_mask:0xf bank_mask:0xf\n"
"v_mul_f32_dpp v95, v162, v95 row_newbcast:3 row_mask:0xf bank_mask:0xf\n"
"s_cmp_lg_u32 s83, s43\n"
"s_cbranch_scc1 LSK3A_%=\n"
"v_max3_f32 v165, v120, v121, v122\n"
"v_max3_f32 v165, v165, v123, v124\n"
"v_max3_f32 v165, v165, v125, v126\n"
"v_max3_f32 v165, v165, v127, v164\n"
"v_mov_b32 v166, v165\n"
"s_nop 1\n"
"v_permlane32_swap_b32 v165, v166\n"
"v_max_f32 v165, v165, v166\n"
"v_mov_b32 v166, v165\n"
"s_nop 1\n"
"v_permlane16_swap_b32 v165, v166\n"
"v_max_f32 v165, v165, v166\n"
"v_sub_f32 v166, v164, v165\n"
"v_exp_f32 v166, v166\n"
"v_mov_b32 v164, v165\n"
"v_sub_f32 v120, v120, v164\n"
"v_sub_f32 v121, v121, v164\n"
"v_sub_f32 v122, v122, v164\n"
"v_sub_f32 v123, v123, v164\n"
"v_sub_f32 v124, v124, v164\n"
"v_sub_f32 v125, v125, v164\n"
"v_sub_f32 v126, v126, v164\n"
"v_sub_f32 v127, v127, v164\n"
"v_exp_f32 v120, v120\n"
"v_exp_f32 v121, v121\n"
"v_exp_f32 v122, v122\n"
"v_exp_f32 v123, v123\n"
"v_exp_f32 v124, v124\n"
"v_exp_f32 v125, v125\n"
"v_exp_f32 v126, v126\n"
"v_exp_f32 v127, v127\n"
"v_mul_f32 v167, v167, v166\n"
"v_add_f32 v167, v167, v120\n"
"v_add_f32 v167, v167, v121\n"
"v_add_f32 v167, v167, v122\n"
"v_add_f32 v167, v167, v123\n"
"v_add_f32 v167, v167, v124\n"
"v_add_f32 v167, v167, v125\n"
"v_add_f32 v167, v167, v126\n"
"v_add_f32 v167, v167, v127\n"
"v_cvt_pk_bf16_f32 v120, v120, v121\n"
"v_cvt_pk_bf16_f32 v121, v122, v123\n"
"v_cvt_pk_bf16_f32 v122, v124, v125\n"
"v_cvt_pk_bf16_f32 v123, v126, v127\n"
"v_mov_b32_dpp v166, v166 row_ror:12 row_mask:0x2 bank_mask:0xf\n"
"s_nop 1\n"
"v_mov_b32_dpp v166, v166 row_ror:8 row_mask:0x4 bank_mask:0xf\n"
"s_nop 1\n"
"v_mov_b32_dpp v166, v166 row_ror:4 row_mask:0x8 bank_mask:0xf\n"
"v_accvgpr_read_b32 v124, a208\n"
"s_nop 1\n"
"v_mul_f32_dpp v124, v166, v124 row_newbcast:0 row_mask:0xf bank_mask:0xf\n"
"v_accvgpr_write_b32 a208, v124\n"
"v_accvgpr_read_b32 v125, a209\n"
"s_nop 1\n"
"v_mul_f32_dpp v125, v166, v125 row_newbcast:1 row_mask:0xf bank_mask:0xf\n"
"v_accvgpr_write_b32 a209, v125\n"
"v_accvgpr_read_b32 v126, a210\n"
"s_nop 1\n"
"v_mul_f32_dpp v126, v166, v126 row_newbcast:2 row_mask:0xf bank_mask:0xf\n"
"v_accvgpr_write_b32 a210, v126\n"
"v_accvgpr_read_b32 v127, a211\n"
"s_nop 1\n"
"v_mul_f32_dpp v127, v166, v127 row_newbcast:3 row_mask:0xf bank_mask:0xf\n"
"v_accvgpr_write_b32 a211, v127\n"
"v_accvgpr_read_b32 v124, a212\n"
"s_nop 1\n"
"v_mul_f32_dpp v124, v166, v124 row_newbcast:0 row_mask:0xf bank_mask:0xf\n"
"v_accvgpr_write_b32 a212, v124\n"
"v_accvgpr_read_b32 v125, a213\n"
"s_nop 1\n"
"v_mul_f32_dpp v125, v166, v125 row_newbcast:1 row_mask:0xf bank_mask:0xf\n"
"v_accvgpr_write_b32 a213, v125\n"
"v_accvgpr_read_b32 v126, a214\n"
"s_nop 1\n"
"v_mul_f32_dpp v126, v166, v126 row_newbcast:2 row_mask:0xf bank_mask:0xf\n"
"v_accvgpr_write_b32 a214, v126\n"
"v_accvgpr_read_b32 v127, a215\n"
"s_nop 1\n"
"v_mul_f32_dpp v127, v166, v127 row_newbcast:3 row_mask:0xf bank_mask:0xf\n"
"v_accvgpr_write_b32 a215, v127\n"
"v_accvgpr_read_b32 v124, a216\n"
"s_nop 1\n"
"v_mul_f32_dpp v124, v166, v124 row_newbcast:0 row_mask:0xf bank_mask:0xf\n"
"v_accvgpr_write_b32 a216, v124\n"
"v_accvgpr_read_b32 v125, a217\n"
"s_nop 1\n"
"v_mul_f32_dpp v125, v166, v125 row_newbcast:1 row_mask:0xf bank_mask:0xf\n"
"v_accvgpr_write_b32 a217, v125\n"
"v_accvgpr_read_b32 v126, a218\n"
"s_nop 1\n"
"v_mul_f32_dpp v126, v166, v126 row_newbcast:2 row_mask:0xf bank_mask:0xf\n"
"v_accvgpr_write_b32 a218, v126\n"
"v_accvgpr_read_b32 v127, a219\n"
"s_nop 1\n"
"v_mul_f32_dpp v127, v166, v127 row_newbcast:3 row_mask:0xf bank_mask:0xf\n"
"v_accvgpr_write_b32 a219, v127\n"
"v_accvgpr_read_b32 v124, a220\n"
"s_nop 1\n"
"v_mul_f32_dpp v124, v166, v124 row_newbcast:0 row_mask:0xf bank_mask:0xf\n"
"v_accvgpr_write_b32 a220, v124\n"
"v_accvgpr_read_b32 v125, a221\n"
"s_nop 1\n"
"v_mul_f32_dpp v125, v166, v125 row_newbcast:1 row_mask:0xf bank_mask:0xf\n"
"v_accvgpr_write_b32 a221, v125\n"
"v_accvgpr_read_b32 v126, a222\n"
"s_nop 1\n"
"v_mul_f32_dpp v126, v166, v126 row_newbcast:2 row_mask:0xf bank_mask:0xf\n"
"v_accvgpr_write_b32 a222, v126\n"
"v_accvgpr_read_b32 v127, a223\n"
"s_nop 1\n"
"v_mul_f32_dpp v127, v166, v127 row_newbcast:3 row_mask:0xf bank_mask:0xf\n"
"v_accvgpr_write_b32 a223, v127\n"
"v_accvgpr_read_b32 v124, a224\n"
"s_nop 1\n"
"v_mul_f32_dpp v124, v166, v124 row_newbcast:0 row_mask:0xf bank_mask:0xf\n"
"v_accvgpr_write_b32 a224, v124\n"
"v_accvgpr_read_b32 v125, a225\n"
"s_nop 1\n"
"v_mul_f32_dpp v125, v166, v125 row_newbcast:1 row_mask:0xf bank_mask:0xf\n"
"v_accvgpr_write_b32 a225, v125\n"
"v_accvgpr_read_b32 v126, a226\n"
"s_nop 1\n"
"v_mul_f32_dpp v126, v166, v126 row_newbcast:2 row_mask:0xf bank_mask:0xf\n"
"v_accvgpr_write_b32 a226, v126\n"
"v_accvgpr_read_b32 v127, a227\n"
"s_nop 1\n"
"v_mul_f32_dpp v127, v166, v127 row_newbcast:3 row_mask:0xf bank_mask:0xf\n"
"v_accvgpr_write_b32 a227, v127\n"
"v_accvgpr_read_b32 v124, a228\n"
"s_nop 1\n"
"v_mul_f32_dpp v124, v166, v124 row_newbcast:0 row_mask:0xf bank_mask:0xf\n"
"v_accvgpr_write_b32 a228, v124\n"
"v_accvgpr_read_b32 v125, a229\n"
"s_nop 1\n"
"v_mul_f32_dpp v125, v166, v125 row_newbcast:1 row_mask:0xf bank_mask:0xf\n"
"v_accvgpr_write_b32 a229, v125\n"
"v_accvgpr_read_b32 v126, a230\n"
"s_nop 1\n"
"v_mul_f32_dpp v126, v166, v126 row_newbcast:2 row_mask:0xf bank_mask:0xf\n"
"v_accvgpr_write_b32 a230, v126\n"
"v_accvgpr_read_b32 v127, a231\n"
"s_nop 1\n"
"v_mul_f32_dpp v127, v166, v127 row_newbcast:3 row_mask:0xf bank_mask:0xf\n"
"v_accvgpr_write_b32 a231, v127\n"
"v_accvgpr_read_b32 v124, a232\n"
"s_nop 1\n"
"v_mul_f32_dpp v124, v166, v124 row_newbcast:0 row_mask:0xf bank_mask:0xf\n"
"v_accvgpr_write_b32 a232, v124\n"
"v_accvgpr_read_b32 v125, a233\n"
"s_nop 1\n"
"v_mul_f32_dpp v125, v166, v125 row_newbcast:1 row_mask:0xf bank_mask:0xf\n"
"v_accvgpr_write_b32 a233, v125\n"
"v_accvgpr_read_b32 v126, a234\n"
"s_nop 1\n"
"v_mul_f32_dpp v126, v166, v126 row_newbcast:2 row_mask:0xf bank_mask:0xf\n"
"v_accvgpr_write_b32 a234, v126\n"
"v_accvgpr_read_b32 v127, a235\n"
"s_nop 1\n"
"v_mul_f32_dpp v127, v166, v127 row_newbcast:3 row_mask:0xf bank_mask:0xf\n"
"v_accvgpr_write_b32 a235, v127\n"
"v_accvgpr_read_b32 v124, a236\n"
"s_nop 1\n"
"v_mul_f32_dpp v124, v166, v124 row_newbcast:0 row_mask:0xf bank_mask:0xf\n"
"v_accvgpr_write_b32 a236, v124\n"
"v_accvgpr_read_b32 v125, a237\n"
"s_nop 1\n"
"v_mul_f32_dpp v125, v166, v125 row_newbcast:1 row_mask:0xf bank_mask:0xf\n"
"v_accvgpr_write_b32 a237, v125\n"
"v_accvgpr_read_b32 v126, a238\n"
"s_nop 1\n"
"v_mul_f32_dpp v126, v166, v126 row_newbcast:2 row_mask:0xf bank_mask:0xf\n"
"v_accvgpr_write_b32 a238, v126\n"
"v_accvgpr_read_b32 v127, a239\n"
"s_nop 1\n"
"v_mul_f32_dpp v127, v166, v127 row_newbcast:3 row_mask:0xf bank_mask:0xf\n"
"v_accvgpr_write_b32 a239, v127\n"
"LSK3A_%=:\n"
"s_nop 3\n"
"buffer_load_ushort v128, v146, s[88:91], s80 offen offset:0\n"
"buffer_load_ushort v129, v146, s[88:91], s80 offen offset:256\n"
"buffer_load_ushort v130, v146, s[88:91], s80 offen offset:512\n"
"buffer_load_ushort v131, v146, s[88:91], s80 offen offset:768\n"
"buffer_load_ushort v132, v146, s[88:91], s81 offen offset:0\n"
"buffer_load_ushort v133, v146, s[88:91], s81 offen offset:256\n"
"buffer_load_ushort v134, v146, s[88:91], s81 offen offset:512\n"
"buffer_load_ushort v135, v146, s[88:91], s81 offen offset:768\n"
"buffer_load_ushort v136, v146, s[88:91], s80 offen offset:32\n"
"buffer_load_ushort v137, v146, s[88:91], s80 offen offset:288\n"
"buffer_load_ushort v138, v146, s[88:91], s80 offen offset:544\n"
"buffer_load_ushort v139, v146, s[88:91], s80 offen offset:800\n"
"buffer_load_ushort v140, v146, s[88:91], s81 offen offset:32\n"
"buffer_load_ushort v141, v146, s[88:91], s81 offen offset:288\n"
"buffer_load_ushort v142, v146, s[88:91], s81 offen offset:544\n"
"buffer_load_ushort v143, v146, s[88:91], s81 offen offset:800\n"
"s_waitcnt vmcnt(0)\n"
"v_lshl_or_b32 v128, v129, 16, v128\n"
"v_lshl_or_b32 v129, v131, 16, v130\n"
"v_lshl_or_b32 v130, v133, 16, v132\n"
"v_lshl_or_b32 v131, v135, 16, v134\n"
"v_lshl_or_b32 v136, v137, 16, v136\n"
"v_lshl_or_b32 v137, v139, 16, v138\n"
"v_lshl_or_b32 v138, v141, 16, v140\n"
"v_lshl_or_b32 v139, v143, 16, v142\n"
"v_mfma_f32_16x16x32_bf16 v[0:3], v[96:99], v[128:131], v[0:3]\n"
"s_nop 0\n"
"v_mfma_f32_16x16x32_bf16 v[4:7], v[96:99], v[136:139], v[4:7]\n"
"v_mfma_f32_16x16x32_bf16 v[32:35], v[104:107], v[128:131], v[32:35]\n"
"v_mfma_f32_16x16x32_bf16 v[36:39], v[104:107], v[136:139], v[36:39]\n"
"v_mfma_f32_16x16x32_bf16 v[64:67], v[112:115], v[128:131], v[64:67]\n"
"v_mfma_f32_16x16x32_bf16 v[68:71], v[112:115], v[136:139], v[68:71]\n"
"s_cmp_lg_u32 s83, s43\n"
"s_cbranch_scc1 LSK3B0_%=\n"
"v_mfma_f32_16x16x32_bf16 a[208:211], v[120:123], v[128:131], a[208:211]\n"
"v_mfma_f32_16x16x32_bf16 a[212:215], v[120:123], v[136:139], a[212:215]\n"
"LSK3B0_%=:\n"
"s_nop 3\n"
"buffer_load_ushort v128, v147, s[88:91], s80 offen offset:0\n"
"buffer_load_ushort v129, v147, s[88:91], s80 offen offset:256\n"
"buffer_load_ushort v130, v147, s[88:91], s80 offen offset:512\n"
"buffer_load_ushort v131, v147, s[88:91], s80 offen offset:768\n"
"buffer_load_ushort v132, v147, s[88:91], s81 offen offset:0\n"
"buffer_load_ushort v133, v147, s[88:91], s81 offen offset:256\n"
"buffer_load_ushort v134, v147, s[88:91], s81 offen offset:512\n"
"buffer_load_ushort v135, v147, s[88:91], s81 offen offset:768\n"
"buffer_load_ushort v136, v147, s[88:91], s80 offen offset:32\n"
"buffer_load_ushort v137, v147, s[88:91], s80 offen offset:288\n"
"buffer_load_ushort v138, v147, s[88:91], s80 offen offset:544\n"
"buffer_load_ushort v139, v147, s[88:91], s80 offen offset:800\n"
"buffer_load_ushort v140, v147, s[88:91], s81 offen offset:32\n"
"buffer_load_ushort v141, v147, s[88:91], s81 offen offset:288\n"
"buffer_load_ushort v142, v147, s[88:91], s81 offen offset:544\n"
"buffer_load_ushort v143, v147, s[88:91], s81 offen offset:800\n"
"s_waitcnt vmcnt(0)\n"
"v_lshl_or_b32 v128, v129, 16, v128\n"
"v_lshl_or_b32 v129, v131, 16, v130\n"
"v_lshl_or_b32 v130, v133, 16, v132\n"
"v_lshl_or_b32 v131, v135, 16, v134\n"
"v_lshl_or_b32 v136, v137, 16, v136\n"
"v_lshl_or_b32 v137, v139, 16, v138\n"
"v_lshl_or_b32 v138, v141, 16, v140\n"
"v_lshl_or_b32 v139, v143, 16, v142\n"
"v_mfma_f32_16x16x32_bf16 v[8:11], v[96:99], v[128:131], v[8:11]\n"
"s_nop 0\n"
"v_mfma_f32_16x16x32_bf16 v[12:15], v[96:99], v[136:139], v[12:15]\n"
"v_mfma_f32_16x16x32_bf16 v[40:43], v[104:107], v[128:131], v[40:43]\n"
"v_mfma_f32_16x16x32_bf16 v[44:47], v[104:107], v[136:139], v[44:47]\n"
"v_mfma_f32_16x16x32_bf16 v[72:75], v[112:115], v[128:131], v[72:75]\n"
"v_mfma_f32_16x16x32_bf16 v[76:79], v[112:115], v[136:139], v[76:79]\n"
"s_cmp_lg_u32 s83, s43\n"
"s_cbranch_scc1 LSK3B1_%=\n"
"v_mfma_f32_16x16x32_bf16 a[216:219], v[120:123], v[128:131], a[216:219]\n"
"v_mfma_f32_16x16x32_bf16 a[220:223], v[120:123], v[136:139], a[220:223]\n"
"LSK3B1_%=:\n"
"s_nop 3\n"
"buffer_load_ushort v128, v146, s[88:91], s80 offen offset:128\n"
"buffer_load_ushort v129, v146, s[88:91], s80 offen offset:384\n"
"buffer_load_ushort v130, v146, s[88:91], s80 offen offset:640\n"
"buffer_load_ushort v131, v146, s[88:91], s80 offen offset:896\n"
"buffer_load_ushort v132, v146, s[88:91], s81 offen offset:128\n"
"buffer_load_ushort v133, v146, s[88:91], s81 offen offset:384\n"
"buffer_load_ushort v134, v146, s[88:91], s81 offen offset:640\n"
"buffer_load_ushort v135, v146, s[88:91], s81 offen offset:896\n"
"buffer_load_ushort v136, v146, s[88:91], s80 offen offset:160\n"
"buffer_load_ushort v137, v146, s[88:91], s80 offen offset:416\n"
"buffer_load_ushort v138, v146, s[88:91], s80 offen offset:672\n"
"buffer_load_ushort v139, v146, s[88:91], s80 offen offset:928\n"
"buffer_load_ushort v140, v146, s[88:91], s81 offen offset:160\n"
"buffer_load_ushort v141, v146, s[88:91], s81 offen offset:416\n"
"buffer_load_ushort v142, v146, s[88:91], s81 offen offset:672\n"
"buffer_load_ushort v143, v146, s[88:91], s81 offen offset:928\n"
"s_waitcnt vmcnt(0)\n"
"v_lshl_or_b32 v128, v129, 16, v128\n"
"v_lshl_or_b32 v129, v131, 16, v130\n"
"v_lshl_or_b32 v130, v133, 16, v132\n"
"v_lshl_or_b32 v131, v135, 16, v134\n"
"v_lshl_or_b32 v136, v137, 16, v136\n"
"v_lshl_or_b32 v137, v139, 16, v138\n"
"v_lshl_or_b32 v138, v141, 16, v140\n"
"v_lshl_or_b32 v139, v143, 16, v142\n"
"v_mfma_f32_16x16x32_bf16 v[16:19], v[96:99], v[128:131], v[16:19]\n"
"s_nop 0\n"
"v_mfma_f32_16x16x32_bf16 v[20:23], v[96:99], v[136:139], v[20:23]\n"
"v_mfma_f32_16x16x32_bf16 v[48:51], v[104:107], v[128:131], v[48:51]\n"
"v_mfma_f32_16x16x32_bf16 v[52:55], v[104:107], v[136:139], v[52:55]\n"
"v_mfma_f32_16x16x32_bf16 v[80:83], v[112:115], v[128:131], v[80:83]\n"
"v_mfma_f32_16x16x32_bf16 v[84:87], v[112:115], v[136:139], v[84:87]\n"
"s_cmp_lg_u32 s83, s43\n"
"s_cbranch_scc1 LSK3B2_%=\n"
"v_mfma_f32_16x16x32_bf16 a[224:227], v[120:123], v[128:131], a[224:227]\n"
"v_mfma_f32_16x16x32_bf16 a[228:231], v[120:123], v[136:139], a[228:231]\n"
"LSK3B2_%=:\n"
"s_nop 3\n"
"buffer_load_ushort v128, v147, s[88:91], s80 offen offset:128\n"
"buffer_load_ushort v129, v147, s[88:91], s80 offen offset:384\n"
"buffer_load_ushort v130, v147, s[88:91], s80 offen offset:640\n"
"buffer_load_ushort v131, v147, s[88:91], s80 offen offset:896\n"
"buffer_load_ushort v132, v147, s[88:91], s81 offen offset:128\n"
"buffer_load_ushort v133, v147, s[88:91], s81 offen offset:384\n"
"buffer_load_ushort v134, v147, s[88:91], s81 offen offset:640\n"
"buffer_load_ushort v135, v147, s[88:91], s81 offen offset:896\n"
"buffer_load_ushort v136, v147, s[88:91], s80 offen offset:160\n"
"buffer_load_ushort v137, v147, s[88:91], s80 offen offset:416\n"
"buffer_load_ushort v138, v147, s[88:91], s80 offen offset:672\n"
"buffer_load_ushort v139, v147, s[88:91], s80 offen offset:928\n"
"buffer_load_ushort v140, v147, s[88:91], s81 offen offset:160\n"
"buffer_load_ushort v141, v147, s[88:91], s81 offen offset:416\n"
"buffer_load_ushort v142, v147, s[88:91], s81 offen offset:672\n"
"buffer_load_ushort v143, v147, s[88:91], s81 offen offset:928\n"
"s_waitcnt vmcnt(0)\n"
"v_lshl_or_b32 v128, v129, 16, v128\n"
"v_lshl_or_b32 v129, v131, 16, v130\n"
"v_lshl_or_b32 v130, v133, 16, v132\n"
"v_lshl_or_b32 v131, v135, 16, v134\n"
"v_lshl_or_b32 v136, v137, 16, v136\n"
"v_lshl_or_b32 v137, v139, 16, v138\n"
"v_lshl_or_b32 v138, v141, 16, v140\n"
"v_lshl_or_b32 v139, v143, 16, v142\n"
"v_mfma_f32_16x16x32_bf16 v[24:27], v[96:99], v[128:131], v[24:27]\n"
"s_nop 0\n"
"v_mfma_f32_16x16x32_bf16 v[28:31], v[96:99], v[136:139], v[28:31]\n"
"v_mfma_f32_16x16x32_bf16 v[56:59], v[104:107], v[128:131], v[56:59]\n"
"v_mfma_f32_16x16x32_bf16 v[60:63], v[104:107], v[136:139], v[60:63]\n"
"v_mfma_f32_16x16x32_bf16 v[88:91], v[112:115], v[128:131], v[88:91]\n"
"v_mfma_f32_16x16x32_bf16 v[92:95], v[112:115], v[136:139], v[92:95]\n"
"s_cmp_lg_u32 s83, s43\n"
"s_cbranch_scc1 LSK3B3_%=\n"
"v_mfma_f32_16x16x32_bf16 a[232:235], v[120:123], v[128:131], a[232:235]\n"
"v_mfma_f32_16x16x32_bf16 a[236:239], v[120:123], v[136:139], a[236:239]\n"
"LSK3B3_%=:\n"
"s_nop 3\n"
"s_nop 9\n"
"s_add_u32 s80, s80, 8192\n"
"s_add_u32 s81, s81, 8192\n"
"s_add_u32 s82, s82, 1\n"
"s_cmp_lt_u32 s82, 32\n"
"s_cbranch_scc1 LSLOOP_%=\n"
"v_mov_b32 v154, v155\n"
"s_nop 1\n"
"v_permlane32_swap_b32 v155, v154\n"
"v_add_f32 v155, v155, v154\n"
"v_mov_b32 v154, v155\n"
"s_nop 1\n"
"v_permlane16_swap_b32 v155, v154\n"
"v_add_f32 v155, v155, v154\n"
"v_mov_b32 v196, v155\n"
"v_mov_b32 v158, v159\n"
"s_nop 1\n"
"v_permlane32_swap_b32 v159, v158\n"
"v_add_f32 v159, v159, v158\n"
"v_mov_b32 v158, v159\n"
"s_nop 1\n"
"v_permlane16_swap_b32 v159, v158\n"
"v_add_f32 v159, v159, v158\n"
"v_mov_b32 v198, v159\n"
"v_mov_b32 v162, v163\n"
"s_nop 1\n"
"v_permlane32_swap_b32 v163, v162\n"
"v_add_f32 v163, v163, v162\n"
"v_mov_b32 v162, v163\n"
"s_nop 1\n"
"v_permlane16_swap_b32 v163, v162\n"
"v_add_f32 v163, v163, v162\n"
"v_mov_b32 v200, v163\n"
"v_mov_b32 v166, v167\n"
"s_nop 1\n"
"v_permlane32_swap_b32 v167, v166\n"
"v_add_f32 v167, v167, v166\n"
"v_mov_b32 v166, v167\n"
"s_nop 1\n"
"v_permlane16_swap_b32 v167, v166\n"
"v_add_f32 v167, v167, v166\n"
"v_mov_b32 v202, v167\n"
"v_sub_f32 v192, 0, v164\n"
"v_mov_b32 v96, 0\n"
"v_mov_b32 v97, 0\n"
"v_mov_b32 v98, 0\n"
"v_mov_b32 v99, 0\n"
"v_mov_b32 v100, 0\n"
"v_mov_b32 v101, 0\n"
"v_mov_b32 v102, 0\n"
"v_mov_b32 v103, 0\n"
"v_mov_b32 v104, 0\n"
"v_mov_b32 v105, 0\n"
"v_mov_b32 v106, 0\n"
"v_mov_b32 v107, 0\n"
"v_mov_b32 v108, 0\n"
"v_mov_b32 v109, 0\n"
"v_mov_b32 v110, 0\n"
"v_mov_b32 v111, 0\n"
"v_mov_b32 v112, 0\n"
"v_mov_b32 v113, 0\n"
"v_mov_b32 v114, 0\n"
"v_mov_b32 v115, 0\n"
"v_mov_b32 v116, 0\n"
"v_mov_b32 v117, 0\n"
"v_mov_b32 v118, 0\n"
"v_mov_b32 v119, 0\n"
"v_mov_b32 v120, 0\n"
"v_mov_b32 v121, 0\n"
"v_mov_b32 v122, 0\n"
"v_mov_b32 v123, 0\n"
"v_mov_b32 v204, 0\n"
"v_mov_b32 v205, 0\n"
"s_nop 3\n"
"s_mov_b32 s68, 0\n"
"s_cmp_eq_u32 s47, 4\n"
"s_cbranch_scc1 LBACK0_%=\n"
"s_cmp_eq_u32 s47, 8\n"
"s_cbranch_scc1 LBACK1_%=\n"
"s_cmp_eq_u32 s47, 12\n"
"s_cbranch_scc1 LBACK2_%=\n"
"s_branch LBACK3_%=\n"
"LFIN_%=:\n"
    :: [kalo]"s"((unsigned)ka), [kahi]"s"((unsigned)(ka >> 32)), [wg]"s"(blockIdx.x), [wid]"s"(wid)
    : "v0","v1","v2","v3","v4","v5","v6","v7","v8","v9","v10","v11","v12","v13","v14","v15","v16","v17","v18","v19","v20","v21","v22","v23","v24","v25","v26","v27","v28","v29","v30","v31","v32","v33","v34","v35","v36","v37","v38","v39","v40","v41","v42","v43","v44","v45","v46","v47","v48","v49","v50","v51","v52","v53","v54","v55","v56","v57","v58","v59","v60","v61","v62","v63","v64","v65","v66","v67","v68","v69","v70","v71","v72","v73","v74","v75","v76","v77","v78","v79","v80","v81","v82","v83","v84","v85","v86","v87","v88","v89","v90","v91","v92","v93","v94","v95","v96","v97","v98","v99","v100","v101","v102","v103","v104","v105","v106","v107","v108","v109","v110","v111","v112","v113","v114","v115","v116","v117","v118","v119","v120","v121","v122","v123","v124","v125","v126","v127","v128","v129","v130","v131","v132","v133","v134","v135","v136","v137","v138","v139","v140","v141","v142","v143","v144","v145","v146","v147","v148","v149","v150","v151","v152","v153","v154","v155","v156","v157","v158","v159","v160","v161","v162","v163","v164","v165","v166","v167","v168","v169","v170","v171","v172","v173","v174","v175","v176","v177","v178","v179","v180","v181","v182","v183","v184","v185","v186","v187","v188","v189","v190","v191","v192","v193","v194","v195","v196","v197","v198","v199","v200","v201","v202","v203","v204","v205","v206","v207","v208","v209","v210","v211","v212","v213","v214","v215","v216","v217","v218","v219","v220","v221","v222","v223","v224","v225","v226","v227","v228","v229","v230","v231","v232","v233","v234","v235","v236","v237","v238","v239","v240","v241","v242","v243","v244","v245","v246","v247","v248","v249","v250","v251","v252","v253","v254","v255","a0","a1","a2","a3","a4","a5","a6","a7","a8","a9","a10","a11","a12","a13","a14","a15","a16","a17","a18","a19","a20","a21","a22","a23","a24","a25","a26","a27","a28","a29","a30","a31","a32","a33","a34","a35","a36","a37","a38","a39","a40","a41","a42","a43","a44","a45","a46","a47","a48","a49","a50","a51","a52","a53","a54","a55","a56","a57","a58","a59","a60","a61","a62","a63","a64","a65","a66","a67","a68","a69","a70","a71","a72","a73","a74","a75","a76","a77","a78","a79","a80","a81","a82","a83","a84","a85","a86","a87","a88","a89","a90","a91","a92","a93","a94","a95","a96","a97","a98","a99","a100","a101","a102","a103","a104","a105","a106","a107","a108","a109","a110","a111","a112","a113","a114","a115","a116","a117","a118","a119","a120","a121","a122","a123","a124","a125","a126","a127","a128","a129","a130","a131","a132","a133","a134","a135","a136","a137","a138","a139","a140","a141","a142","a143","a144","a145","a146","a147","a148","a149","a150","a151","a152","a153","a154","a155","a156","a157","a158","a159","a160","a161","a162","a163","a164","a165","a166","a167","a168","a169","a170","a171","a172","a173","a174","a175","a176","a177","a178","a179","a180","a181","a182","a183","a184","a185","a186","a187","a188","a189","a190","a191","a192","a193","a194","a195","a196","a197","a198","a199","a200","a201","a202","a203","a204","a205","a206","a207","a208","a209","a210","a211","a212","a213","a214","a215","a216","a217","a218","a219","a220","a221","a222","a223","a224","a225","a226","a227","a228","a229","a230","a231","a232","a233","a234","a235","a236","a237","a238","a239","a240","a241","a242","a243","a244","a245","a246","a247","a248","a249","a250","a251","a252","a253","a254","a255","s6","s7","s8","s9","s10","s11","s12","s13","s14","s15","s16","s17","s18","s19","s20","s21","s22","s23","s24","s25","s26","s27","s28","s29","s30","s31","s32","s33","s34","s35","s36","s37","s38","s39","s40","s41","s42","s43","s44","s45","s46","s47","s48","s49","s50","s51","s52","s53","s54","s55","s56","s57","s58","s59","s60","s61","s62","s63","s64","s65","s66","s67","s68","s69","s70","s71","s72","s73","s74","s75","s76","s77","s78","s79","s80","s81","s82","s83","s84","s85","s86","s87","s88","s89","s90","s91","s92","s93","s94","s95","s96","s97","s98","s99","s100","vcc","scc","m0","memory");
}
extern "C" void kernel_launch(void* const* d_in, const int* in_sizes, int n_in, void* d_out, int out_size, void* d_ws, size_t ws_size, hipStream_t stream) {
  char* xch = (char*)d_ws + ((ws_size - (size_t)270532608) & ~(size_t)0xfffff);
  (void)hipFuncSetAttribute((const void*)fused_attn, hipFuncAttributeMaxDynamicSharedMemorySize, 163840);
  hipLaunchKernelGGL(fused_attn, dim3(256), dim3(256), 163840, stream,
                     (const char*)d_in[0], (const char*)d_in[1], (const char*)d_in[2], (const char*)d_in[3], (const char*)d_in[4],
                     (const char*)d_in[5], (const char*)d_in[6], (const char*)d_in[7], (const char*)d_in[8], (const char*)d_in[9], xch, (char*)d_out, (char*)d_ws);
}
```

```cpp
#include <hip/hip_runtime.h>
#include <hip/hip_bf16.h>
#include <stdint.h>

__global__ __launch_bounds__(256, 1)
void fused_attn(const char* xA, const char* kA, const char* vA, const char* wA, const char* uA,
                const char* xB, const char* kB, const char* vB, const char* wB, const char* uB, char* xch, char* out, char* flags) {
  extern __shared__ __attribute__((aligned(16))) char lds[];
  const unsigned wid = __builtin_amdgcn_readfirstlane(threadIdx.x >> 6);
  const unsigned long long ka = (unsigned long long)__builtin_amdgcn_kernarg_segment_ptr();
  asm volatile(
"s_memtime s[64:65]\n"
"s_memrealtime s[66:67]\n"
"s_bfe_u32 s10, %[wg], 0x10002\n"
"s_mul_i32 s40, s10, 40\n"
"s_add_u32 s40, s40, %[kalo]\n"
"s_addc_u32 s41, %[kahi], 0\n"
"s_mov_b32 s78, %[kalo]\n"
"s_mov_b32 s79, %[kahi]\n"
"s_load_dwordx2 s[24:25], s[40:41], 0\n"
"s_load_dwordx2 s[28:29], s[40:41], 24\n"
"s_load_dwordx2 s[6:7], s[78:79], 0x60\n"
"s_load_dwordx2 s[16:17], s[40:41], 8\n"
"s_load_dwordx2 s[20:21], s[40:41], 16\n"
"s_load_dwordx2 s[8:9], s[40:41], 32\n"
"s_load_dwordx4 s[12:15], s[78:79], 0x50\n"
"s_mov_b32 s42, %[wid]\n"
"s_and_b32 s11, %[wg], 1\n"
"s_lshl_b32 s11, s11, 3\n"
"s_lshr_b32 s60, %[wg], 5\n"
"s_add_u32 s11, s11, s60\n"
"s_bfe_u32 s61, %[wg], 0x10001\n"
"s_bfe_u32 s60, %[wg], 0x20003\n"
"s_lshl2_add_u32 s61, s61, s60\n"
"s_getpc_b64 s[80:81]\n"
"LCP_%=:\n"
"s_mov_b32 s82, LEND_%=-LCP_%=\n"
"s_mov_b32 s83, 0x20000\n"
"v_mbcnt_lo_u32_b32 v2, -1, 0\n"
"v_lshlrev_b32 v2, 7, v2\n"
"s_lshr_b32 s60, %[wg], 3\n"
"s_lshl2_add_u32 s60, s60, s42\n"
"s_mul_i32 s60, s60, 640\n"
"s_mov_b64 exec, 31\n"
"buffer_load_dword v3, v2, s[80:83], s60 offen\n"
"s_mov_b64 exec, -1\n"
"s_mov_b32 s19, 0x20000\n"
"s_mov_b32 s23, 0x20000\n"
"s_mov_b32 s27, 0x20000\n"
"s_mov_b32 s31, 0x20000\n"
"s_mov_b32 s35, 0x20000\n"
"s_mov_b32 s39, 0x20000\n"
"s_mov_b32 s26, 286720\n"
"s_lshl_b32 s44, s42, 12\n"
"s_mov_b32 s50, s44\n"
"s_mul_i32 s51, s42, 7168\n"
"s_add_u32 s51, s51, 0x10000\n"
"s_mov_b32 s74, 0x8000\n"
"s_mov_b32 s58, 524288\n"
"s_mov_b32 s47, 0\n"
"s_add_u32 s60, s11, s61\n"
"s_and_b32 s60, s60, 0\n"
"s_mul_i32 s60, s60, 3\n"
"s_lshl_b32 s49, s60, 7\n"
"s_lshl_b32 s48, s60, 19\n"
"v_mbcnt_lo_u32_b32 v4, -1, 0\n"
"v_mbcnt_hi_u32_b32 v4, -1, v4\n"
"v_and_b32 v5, 15, v4\n"
"v_lshrrev_b32 v6, 4, v4\n"
"v_lshrrev_b32 v7, 3, v4\n"
"v_and_b32 v8, 7, v4\n"
"v_lshlrev_b32 v9, 1, v6\n"
"v_xor_b32 v9, v9, v8\n"
"v_lshlrev_b32 v9, 4, v9\n"
"s_mul_i32 s60, s42, 71680\n"
"v_mul_u32_u24 v10, 1280, v7\n"
"v_add3_u32 v248, v10, v9, s60\n"
"v_add_u32 v249, 9216, v248\n"
"v_add_u32 v254, 18432, v248\n"
"v_add_u32 v255, 27648, v248\n"
"v_lshl_add_u32 v240, v7, 8, v9\n"
"v_add_u32 v240, s44, v240\n"
"v_and_b32 v10, 1, v6\n"
"v_xor_b32 v10, v10, v8\n"
"v_lshlrev_b32 v10, 4, v10\n"
"v_lshl_add_u32 v10, v7, 13, v10\n"
"s_lshl_b32 s60, s42, 17\n"
"v_add_u32 v10, s60, v10\n"
"v_add_u32 v224, 1024, v10\n"
"v_add_u32 v225, 128, v10\n"
"v_xor_b32 v10, 64, v10\n"
"v_add_u32 v226, 64512, v10\n"
"v_add_u32 v227, 63616, v10\n"
"s_mul_i32 s40, s11, 286720\n"
"s_lshl_b32 s41, s61, 10\n"
"s_sub_u32 s30, 5243904, s41\n"
"s_sub_u32 s41, s41, 1024\n"
"s_ashr_i32 s60, s41, 31\n"
"s_lshl4_add_u32 s92, s10, s11\n"
"s_lshl_b32 s93, s92, 9\n"
"v_mov_b32 v0, 0\n"
"s_waitcnt lgkmcnt(0)\n"
"s_add_u32 s24, s24, s40\n"
"s_addc_u32 s25, s25, 0\n"
"s_and_b32 s25, s25, 0xffff\n"
"s_add_u32 s28, s28, s41\n"
"s_addc_u32 s29, s29, s60\n"
"s_and_b32 s29, s29, 0xffff\n"
"s_add_u32 s6, s6, s93\n"
"s_addc_u32 s7, s7, 0\n"
"buffer_load_dword v12, v248, s[24:27], s49 offen\n"
"buffer_load_dword v13, v224, s[28:31], s48 offen\n"
"global_load_dword v1, v0, s[6:7] offset:128 sc1\n"
"s_mov_b32 m0, s51\n"
"s_nop 0\n"
"buffer_load_dwordx4 v248, s[24:27], s49 offen lds\n"
"buffer_load_dwordx4 v249, s[24:27], s49 offen offset:1024 lds\n"
"buffer_load_dwordx4 v254, s[24:27], s49 offen offset:2048 lds\n"
"buffer_load_dwordx4 v255, s[24:27], s49 offen offset:3072 lds\n"
"s_add_u32 m0, s51, 4096\n"
"s_add_u32 s40, s49, 40960\n"
"buffer_load_dwordx4 v248, s[24:27], s40 offen lds\n"
"buffer_load_dwordx4 v249, s[24:27], s40 offen offset:1024 lds\n"
"buffer_load_dwordx4 v254, s[24:27], s40 offen offset:2048 lds\n"
"s_add_u32 s49, s49, 128\n"
"s_sub_u32 s41, s49, 1280\n"
"s_min_u32 s49, s49, s41\n"
"s_xor_b32 s51, s51, s74\n"
"s_mov_b32 m0, s50\n"
"s_nop 0\n"
"buffer_load_dwordx4 v224, s[28:31], s48 offen lds\n"
"buffer_load_dwordx4 v225, s[28:31], s48 offen offset:1024 lds\n"
"buffer_load_dwordx4 v226, s[28:31], s48 offen offset:2048 lds\n"
"buffer_load_dwordx4 v227, s[28:31], s48 offen offset:3072 lds\n"
"s_xor_b32 s50, s50, s74\n"
"s_lshl2_add_u32 s63, s11, s42\n"
"s_mul_i32 s63, s63, 10240\n"
"s_and_b32 s78, s42, 1\n"
"s_lshl_b32 s43, s78, 6\n"
"s_mov_b32 s77, 0x80000000\n"
"s_mov_b32 s75, 0x10000\n"
"s_mov_b32 s73, 0xffff0000\n"
"s_mov_b32 s46, 0x3e0293ee\n"
"s_mov_b32 s45, 0x63800000\n"
"s_mov_b32 s62, 9\n"
"v_lshl_add_u32 v242, v4, 4, s44\n"
"s_lshr_b32 s79, s42, 1\n"
"s_lshl4_add_u32 s76, s79, s78\n"
"s_lshl_b32 s76, s76, 10\n"
"s_add_u32 s76, s76, 0x20000\n"
"s_sub_u32 s76, s76, s44\n"
"v_lshlrev_b32 v9, 6, v6\n"
"v_xor_b32 v9, v9, v242\n"
"v_add_u32 v243, s76, v9\n"
"v_and_b32 v9, 6, v5\n"
"s_xor_b32 m0, s50, 0xc000\n"
"s_add_u32 s40, s48, 256\n"
"buffer_load_dwordx4 v224, s[28:31], s40 offen lds\n"
"v_xor_b32 v9, v9, v6\n"
"v_lshlrev_b32 v9, 4, v9\n"
"v_lshl_add_u32 v10, v5, 7, v9\n"
"s_mul_i32 s79, s42, 6144\n"
"s_add_u32 s79, s79, 0x10000\n"
"v_add_u32 v250, s79, v10\n"
"v_xor_b32 v251, 64, v250\n"
"s_lshr_b32 s79, s42, 1\n"
"s_lshl_b32 s79, s79, 11\n"
"buffer_load_dwordx4 v225, s[28:31], s40 offen offset:1024 lds\n"
"s_add_u32 s79, s79, 90112\n"
"v_add_u32 v252, s79, v10\n"
"v_xor_b32 v253, 64, v252\n"
"s_mul_i32 s79, s78, 8256\n"
"v_lshrrev_b32 v9, 5, v4\n"
"v_lshlrev_b32 v9, 11, v9\n"
"v_bfe_u32 v10, v4, 2, 3\n"
"v_lshl_add_u32 v9, v10, 7, v9\n"
"v_and_b32 v10, 3, v4\n"
"buffer_load_dwordx4 v226, s[28:31], s40 offen offset:2048 lds\n"
"v_lshl_add_u32 v9, v10, 3, v9\n"
"v_bfe_u32 v10, v4, 3, 2\n"
"v_lshl_add_u32 v9, v10, 5, v9\n"
"s_add_u32 s79, s79, 0x10000\n"
"v_xor_b32 v232, s79, v9\n"
"v_xor_b32 v233, 32, v232\n"
"v_xor_b32 v234, 64, v232\n"
"v_xor_b32 v235, 96, v232\n"
"v_xor_b32 v236, 0x2000, v232\n"
"buffer_load_dwordx4 v227, s[28:31], s40 offen offset:3072 lds\n"
"s_cmp_eq_u32 s47, 8\n"
"s_cselect_b32 s41, s52, s58\n"
"s_add_u32 s48, s48, s41\n"
"s_sub_u32 s41, s48, 5242880\n"
"s_min_u32 s48, s48, s41\n"
"v_xor_b32 v237, 0x2000, v233\n"
"v_xor_b32 v238, 0x2000, v234\n"
"v_xor_b32 v239, 0x2000, v235\n"
"v_lshlrev_b32 v9, 11, v6\n"
"v_bfe_u32 v10, v4, 2, 2\n"
"v_lshl_add_u32 v9, v10, 7, v9\n"
"v_and_b32 v10, 1, v4\n"
"v_lshl_add_u32 v9, v10, 3, v9\n"
"v_bfe_u32 v10, v4, 1, 1\n"
"s_mov_b32 m0, s50\n"
"s_nop 0\n"
"buffer_load_dwordx4 v224, s[28:31], s48 offen lds\n"
"v_lshl_add_u32 v9, v10, 5, v9\n"
"v_bfe_u32 v10, v4, 3, 1\n"
"v_lshl_add_u32 v9, v10, 4, v9\n"
"v_bfe_u32 v10, v4, 4, 1\n"
"v_lshl_add_u32 v9, v10, 6, v9\n"
"v_xor_b32 v244, s43, v9\n"
"v_xor_b32 v245, 16, v244\n"
"v_xor_b32 v246, 64, v244\n"
"v_xor_b32 v247, 0x50, v244\n"
"buffer_load_dwordx4 v225, s[28:31], s48 offen offset:1024 lds\n"
"s_lshl3_add_u32 s79, s11, s61\n"
"s_lshl_b32 s79, s79, 20\n"
"s_sub_u32 s79, s79, 896\n"
"s_ashr_i32 s60, s79, 31\n"
"s_add_u32 s16, s16, s79\n"
"s_addc_u32 s17, s17, s60\n"
"s_and_b32 s17, s17, 0xffff\n"
"s_mov_b32 s18, 1049472\n"
"s_mov_b64 s[84:85], s[16:17]\n"
"buffer_load_dwordx4 v226, s[28:31], s48 offen offset:2048 lds\n"
"s_mov_b64 s[86:87], s[18:19]\n"
"s_add_u32 s20, s20, s79\n"
"s_addc_u32 s21, s21, s60\n"
"s_and_b32 s21, s21, 0xffff\n"
"s_mov_b32 s22, 1049472\n"
"s_mov_b64 s[88:89], s[20:21]\n"
"s_mov_b64 s[90:91], s[22:23]\n"
"s_lshr_b32 s57, %[wg], 3\n"
"s_mul_i32 s57, s57, 5\n"
"buffer_load_dwordx4 v227, s[28:31], s48 offen offset:3072 lds\n"
"s_xor_b32 s50, s50, s74\n"
"s_and_b32 s57, s57, 15\n"
"s_lshl_b32 s57, s57, 14\n"
"s_mov_b32 s55, 896\n"
"s_add_u32 s56, s57, 896\n"
"s_mov_b32 s59, 16384\n"
"s_mov_b32 s54, -896\n"
"s_add_u32 s52, s11, s61\n"
"s_and_b32 s52, s52, 0\n"
"s_mul_i32 s52, s52, 3\n"
"s_mov_b32 m0, s51\n"
"s_nop 0\n"
"buffer_load_dwordx4 v248, s[24:27], s49 offen lds\n"
"s_add_u32 s52, s52, 1\n"
"s_lshl_b32 s53, s52, 7\n"
"s_lshl_b32 s52, s52, 19\n"
"s_add_u32 s52, s52, 512\n"
"s_mul_i32 s79, s61, 655360\n"
"s_add_u32 s8, s8, s79\n"
"s_addc_u32 s9, s9, 0\n"
"s_and_b32 s9, s9, 0xffff\n"
"s_add_u32 s12, s12, 0x1fffff\n"
"s_addc_u32 s13, s13, 0\n"
"buffer_load_dwordx4 v249, s[24:27], s49 offen offset:1024 lds\n"
"s_and_b32 s12, s12, 0xffe00000\n"
"s_mul_i32 s96, s42, 3\n"
"s_and_b32 s96, s96, 5\n"
"s_cmp_eq_u32 s42, 3\n"
"s_cselect_b32 s96, 5, s96\n"
"s_lshl_b32 s79, s61, 8\n"
"s_add_u32 s96, s96, s79\n"
"s_add_u32 s96, s96, 160\n"
"buffer_load_dwordx4 v254, s[24:27], s49 offen offset:2048 lds\n"
"s_lshl_b32 s96, s96, 12\n"
"s_mul_i32 s79, s92, 8388608\n"
"s_add_u32 s32, s12, s79\n"
"s_addc_u32 s33, s13, 0\n"
"s_and_b32 s33, s33, 0xffff\n"
"s_mov_b32 s34, 8388608\n"
"s_mul_i32 s79, s11, 573440\n"
"s_mul_i32 s60, s10, 1280\n"
"s_sub_u32 s38, 573440, s60\n"
"buffer_load_dwordx4 v255, s[24:27], s49 offen offset:3072 lds\n"
"s_add_u32 s79, s79, s60\n"
"s_add_u32 s36, s14, s79\n"
"s_addc_u32 s37, s15, 0\n"
"s_and_b32 s37, s37, 0xffff\n"
"s_lshl2_add_u32 s93, s61, s42\n"
"s_add_u32 s95, s61, 1\n"
"s_and_b32 s95, s95, 7\n"
"s_lshl_b32 s95, s95, 7\n"
"s_add_u32 s100, s61, 2\n"
"s_add_u32 m0, s51, 4096\n"
"s_add_u32 s40, s49, 40960\n"
"buffer_load_dwordx4 v248, s[24:27], s40 offen lds\n"
"s_and_b32 s100, s100, 7\n"
"s_lshl_b32 s100, s100, 7\n"
"s_lshl_b32 s60, s61, 7\n"
"s_add_u32 s60, s60, s43\n"
"s_mul_i32 s97, s42, 122880\n"
"s_add_u32 s97, s97, s60\n"
"s_lshr_b32 s98, s42, 1\n"
"s_add_u32 s98, s98, 12\n"
"s_mul_i32 s98, s98, 40960\n"
"buffer_load_dwordx4 v249, s[24:27], s40 offen offset:1024 lds\n"
"s_add_u32 s98, s98, s60\n"
"s_mul_i32 s60, s93, 37\n"
"s_lshr_b32 s60, s60, 8\n"
"s_mul_i32 s79, s60, 7\n"
"s_sub_u32 s79, s93, s79\n"
"s_lshl4_add_u32 s94, s60, s79\n"
"s_lshl_b32 s94, s94, 10\n"
"s_add_u32 s94, s94, 524288\n"
"s_mul_i32 s99, s60, 3\n"
"buffer_load_dwordx4 v254, s[24:27], s40 offen offset:2048 lds\n"
"s_add_u32 s49, s49, 128\n"
"s_sub_u32 s41, s49, 1280\n"
"s_min_u32 s49, s49, s41\n"
"s_xor_b32 s51, s51, s74\n"
"s_lshr_b32 s78, s79, 1\n"
"s_add_u32 s99, s99, s78\n"
"s_lshr_b32 s78, s60, 1\n"
"s_add_u32 s78, s78, 12\n"
"s_cmp_eq_u32 s79, 6\n"
"s_cselect_b32 s99, s78, s99\n"
"s_mul_i32 s99, s99, 40960\n"
"s_xor_b32 s79, s79, s60\n"
"s_and_b32 s79, s79, 1\n"
"s_lshl_b32 s79, s79, 6\n"
"s_add_u32 s99, s99, s79\n"
"s_add_u32 s99, s99, 1024\n"
"s_cmp_eq_u32 s61, 7\n"
"s_cselect_b32 s99, 0x40000000, s99\n"
"s_cselect_b32 s94, 0x40000000, s94\n"
"s_waitcnt vmcnt(27)\n"
"v_and_b32 v12, 0x7fff0000, v12\n"
"v_and_b32 v13, 0x7fff0000, v13\n"
"s_nop 0\n"
"v_max_u32_dpp v12, v12, v12 quad_perm:[1,0,3,2] row_mask:0xf bank_mask:0xf\n"
"v_max_u32_dpp v13, v13, v13 quad_perm:[1,0,3,2] row_mask:0xf bank_mask:0xf\n"
"s_nop 0\n"
"v_max_u32_dpp v12, v12, v12 quad_perm:[2,3,0,1] row_mask:0xf bank_mask:0xf\n"
"v_max_u32_dpp v13, v13, v13 quad_perm:[2,3,0,1] row_mask:0xf bank_mask:0xf\n"
"s_memtime s[80:81]\n"
"s_memrealtime s[82:83]\n"
"s_nop 0\n"
"v_max_u32_dpp v12, v12, v12 row_half_mirror row_mask:0xf bank_mask:0xf\n"
"v_max_u32_dpp v13, v13, v13 row_half_mirror row_mask:0xf bank_mask:0xf\n"
"s_nop 0\n"
"v_min_u32_dpp v12, v12, v12 row_mirror row_mask:0xf bank_mask:0xf\n"
"v_min_u32_dpp v13, v13, v13 row_mirror row_mask:0xf bank_mask:0xf\n"
"s_nop 0\n"
"v_min_u32_dpp v12, v12, v12 row_bcast:15 row_mask:0xa bank_mask:0xf\n"
"v_min_u32_dpp v13, v13, v13 row_bcast:15 row_mask:0xa bank_mask:0xf\n"
"s_nop 0\n"
"v_min_u32_dpp v12, v12, v12 row_bcast:31 row_mask:0xc bank_mask:0xf\n"
"v_min_u32_dpp v13, v13, v13 row_bcast:31 row_mask:0xc bank_mask:0xf\n"
"s_nop 0\n"
"v_mul_f32 v12, v12, v13\n"
"v_mul_f32 v12, 0x43c80000, v12\n"
"v_min_f32 v12, 0x71800000, v12\n"
"v_mul_f32 v13, s46, v12\n"
"s_nop 0\n"
"v_readlane_b32 s70, v12, 63\n"
"v_readlane_b32 s71, v12, 63\n"
"v_readlane_b32 s69, v13, 63\n"
"s_nop 1\n"
"v_mov_b64 v[228:229], s[70:71]\n"
"v_mov_b64 v[230:231], s[70:71]\n"
"s_waitcnt vmcnt(26)\n"
"v_readfirstlane_b32 s92, v1\n"
"v_mov_b64 v[0:1], s[70:71]\n"
"v_mov_b64 v[2:3], s[70:71]\n"
"s_mov_b32 s70, s46\n"
"s_xor_b32 s71, s69, s77\n"
"s_waitcnt lgkmcnt(0)\n"
"s_sub_u32 s80, s80, s64\n"
"s_sub_u32 s82, s82, s66\n"
"s_mul_i32 s80, s80, 10\n"
"s_mul_i32 s82, s82, 211\n"
"s_cmp_gt_u32 s80, s82\n"
"s_cselect_b32 s64, 1, 0\n"
"s_cselect_b32 s68, 655360, 0\n"
"s_cmp_eq_u32 s42, 0\n"
"s_cselect_b32 s64, s64, 0\n"
"s_mov_b32 s65, 0\n"
"s_add_u32 s66, s66, 2800\n"
"s_mov_b32 s67, s66\n"
"s_mov_b32 s72, 126\n"
"s_mov_b64 vcc, s[64:65]\n"
"s_nop 2\n"
"s_mul_i32 s60, s92, 0x9e3779b1\n"
"s_add_u32 s60, s60, 0x7f4a7c15\n"
"s_waitcnt vmcnt(15)\n"
"s_barrier\n"
"ds_read_b128 a[160:163], v250 offset:0\n"
"ds_read_b128 a[164:167], v251 offset:0\n"
"ds_read_b64_tr_b16 a[96:97], v244 offset:0\n"
"ds_read_b64_tr_b16 a[98:99], v244 offset:512\n"
"ds_read_b64_tr_b16 a[100:101], v244 offset:8192\n"
"ds_read_b64_tr_b16 a[102:103], v244 offset:8704\n"
"ds_read_b64_tr_b16 a[104:105], v245 offset:0\n"
"ds_read_b64_tr_b16 a[106:107], v245 offset:512\n"
"ds_read_b64_tr_b16 a[108:109], v245 offset:8192\n"
"ds_read_b64_tr_b16 a[110:111], v245 offset:8704\n"
"ds_read_b64_tr_b16 a[112:113], v246 offset:0\n"
"ds_read_b64_tr_b16 a[114:115], v246 offset:512\n"
"ds_read_b64_tr_b16 a[116:117], v246 offset:8192\n"
"ds_read_b64_tr_b16 a[118:119], v246 offset:8704\n"
"s_waitcnt lgkmcnt(10)\n"
"v_mfma_f32_16x16x32_bf16 v[112:115], a[96:99], a[160:163], v[228:231]\n"
"ds_read_b64_tr_b16 a[120:121], v247 offset:0\n"
"ds_read_b64_tr_b16 a[122:123], v247 offset:512\n"
"ds_read_b64_tr_b16 a[136:137], v245 offset:1024\n"
"ds_read_b64_tr_b16 a[138:139], v245 offset:1536\n"
"s_waitcnt lgkmcnt(12)\n"
"v_mfma_f32_16x16x32_bf16 v[112:115], a[100:103], a[164:167], v[112:115]\n"
"ds_read_b64_tr_b16 a[124:125], v247 offset:8192\n"
"ds_read_b64_tr_b16 a[126:127], v247 offset:8704\n"
"ds_read_b64_tr_b16 a[140:141], v245 offset:9216\n"
"ds_read_b64_tr_b16 a[142:143], v245 offset:9728\n"
"s_waitcnt lgkmcnt(14)\n"
"v_mfma_f32_16x16x32_bf16 v[116:119], a[104:107], a[160:163], v[228:231]\n"
"s_waitcnt lgkmcnt(12)\n"
"ds_read_b64_tr_b16 a[128:129], v244 offset:1024\n"
"ds_read_b64_tr_b16 a[130:131], v244 offset:1536\n"
"ds_read_b64_tr_b16 a[144:145], v246 offset:1024\n"
"ds_read_b64_tr_b16 a[146:147], v246 offset:1536\n"
"v_mfma_f32_16x16x32_bf16 v[116:119], a[108:111], a[164:167], v[116:119]\n"
"s_waitcnt lgkmcnt(14)\n"
"ds_read_b64_tr_b16 a[132:133], v244 offset:9216\n"
"ds_read_b64_tr_b16 a[134:135], v244 offset:9728\n"
"ds_read_b64_tr_b16 a[148:149], v246 offset:9216\n"
"ds_read_b64_tr_b16 a[150:151], v246 offset:9728\n"
"v_mfma_f32_16x16x32_bf16 v[120:123], a[112:115], a[160:163], v[228:231]\n"
"s_waitcnt lgkmcnt(15)\n"
"ds_read_b64_tr_b16 a[152:153], v247 offset:1024\n"
"ds_read_b64_tr_b16 a[154:155], v247 offset:1536\n"
"v_mfma_f32_16x16x32_bf16 v[120:123], a[116:119], a[164:167], v[120:123]\n"
"s_waitcnt lgkmcnt(15)\n"
"ds_read_b64_tr_b16 a[156:157], v247 offset:9216\n"
"ds_read_b64_tr_b16 a[158:159], v247 offset:9728\n"
"v_mfma_f32_16x16x32_bf16 v[124:127], a[120:123], a[160:163], v[228:231]\n"
"s_waitcnt lgkmcnt(14)\n"
"ds_read_b128 a[168:171], v250 offset:2048\n"
"v_mfma_f32_16x16x32_bf16 v[124:127], a[124:127], a[164:167], v[124:127]\n"
"s_waitcnt lgkmcnt(11)\n"
"ds_read_b128 a[172:175], v251 offset:2048\n"
"v_mfma_f32_16x16x32_bf16 v[128:131], a[128:131], a[160:163], v[228:231]\n"
"s_waitcnt lgkmcnt(8)\n"
"ds_read_b128 a[176:179], v250 offset:4096\n"
"v_mfma_f32_16x16x32_bf16 v[128:131], a[132:135], a[164:167], v[128:131]\n"
"ds_read_b128 a[180:183], v251 offset:4096\n"
"v_mfma_f32_16x16x32_bf16 v[132:135], a[136:139], a[160:163], v[228:231]\n"
"s_waitcnt vmcnt(11) lgkmcnt(4)\n"
"ds_read_b128 a[184:187], v252 offset:0\n"
"v_mfma_f32_16x16x32_bf16 v[132:135], a[140:143], a[164:167], v[132:135]\n"
"s_barrier\n"
"v_mfma_f32_16x16x32_bf16 v[136:139], a[144:147], a[160:163], v[228:231]\n"
"s_add_u32 s40, s48, 256\n"
"s_xor_b32 m0, s50, 0xc000\n"
"ds_read_b128 a[188:191], v253 offset:0\n"
"v_mfma_f32_16x16x32_bf16 v[136:139], a[148:151], a[164:167], v[136:139]\n"
"buffer_load_dwordx4 v224, s[28:31], s40 offen lds\n"
"v_mfma_f32_16x16x32_bf16 v[140:143], a[152:155], a[160:163], v[228:231]\n"
"ds_read_b64_tr_b16 a[192:193], v244 offset:16384\n"
"v_mfma_f32_16x16x32_bf16 v[140:143], a[156:159], a[164:167], v[140:143]\n"
"s_waitcnt lgkmcnt(5)\n"
"ds_read_b64_tr_b16 a[194:195], v244 offset:16896\n"
"v_mfma_f32_16x16x32_bf16 v[172:175], a[152:155], a[168:171], v[228:231]\n"
"ds_read_b64_tr_b16 a[196:197], v244 offset:24576\n"
"v_mfma_f32_16x16x32_bf16 v[172:175], a[156:159], a[172:175], v[172:175]\n"
"ds_read_b64_tr_b16 a[198:199], v244 offset:25088\n"
"v_mfma_f32_16x16x32_bf16 v[168:171], a[144:147], a[168:171], v[228:231]\n"
"ds_read_b64_tr_b16 a[200:201], v245 offset:16384\n"
"v_mfma_f32_16x16x32_bf16 v[168:171], a[148:151], a[172:175], v[168:171]\n"
"buffer_load_dwordx4 v225, s[28:31], s40 offen offset:1024 lds\n"
"v_mfma_f32_16x16x32_bf16 v[164:167], a[136:139], a[168:171], v[228:231]\n"
"ds_read_b64_tr_b16 a[202:203], v245 offset:16896\n"
"v_mfma_f32_16x16x32_bf16 v[164:167], a[140:143], a[172:175], v[164:167]\n"
"ds_read_b64_tr_b16 a[204:205], v245 offset:24576\n"
"v_mfma_f32_16x16x32_bf16 v[160:163], a[128:131], a[168:171], v[228:231]\n"
"ds_read_b64_tr_b16 a[206:207], v245 offset:25088\n"
"v_mfma_f32_16x16x32_bf16 v[160:163], a[132:135], a[172:175], v[160:163]\n"
"ds_read_b64_tr_b16 a[208:209], v246 offset:16384\n"
"v_mfma_f32_16x16x32_bf16 v[156:159], a[120:123], a[168:171], v[228:231]\n"
"ds_read_b64_tr_b16 a[210:211], v246 offset:16896\n"
"v_mfma_f32_16x16x32_bf16 v[156:159], a[124:127], a[172:175], v[156:159]\n"
"buffer_load_dwordx4 v226, s[28:31], s40 offen offset:2048 lds\n"
"v_mfma_f32_16x16x32_bf16 v[152:155], a[112:115], a[168:171], v[228:231]\n"
"ds_read_b64_tr_b16 a[212:213], v246 offset:24576\n"
"v_mfma_f32_16x16x32_bf16 v[152:155], a[116:119], a[172:175], v[152:155]\n"
"ds_read_b64_tr_b16 a[214:215], v246 offset:25088\n"
"v_mfma_f32_16x16x32_bf16 v[148:151], a[104:107], a[168:171], v[228:231]\n"
"ds_read_b64_tr_b16 a[216:217], v247 offset:16384\n"
"v_mfma_f32_16x16x32_bf16 v[148:151], a[108:111], a[172:175], v[148:151]\n"
"buffer_load_dwordx4 v227, s[28:31], s40 offen offset:3072 lds\n"
"v_mfma_f32_16x16x32_bf16 v[144:147], a[96:99], a[168:171], v[228:231]\n"
"s_cmp_eq_u32 s47, 8\n"
"ds_read_b64_tr_b16 a[218:219], v247 offset:16896\n"
"v_xor_b32 v250, s74, v250\n"
"v_mfma_f32_16x16x32_bf16 v[144:147], a[100:103], a[172:175], v[144:147]\n"
"s_waitcnt lgkmcnt(5)\n"
"s_cselect_b32 s41, s52, s58\n"
"ds_read_b64_tr_b16 a[220:221], v247 offset:24576\n"
"v_xor_b32 v251, s74, v251\n"
"v_mfma_f32_16x16x32_bf16 v[176:179], a[96:99], a[176:179], v[228:231]\n"
"s_add_u32 s48, s48, s41\n"
"ds_read_b64_tr_b16 a[222:223], v247 offset:25088\n"
"v_xor_b32 v252, s74, v252\n"
"v_mfma_f32_16x16x32_bf16 v[176:179], a[100:103], a[180:183], v[176:179]\n"
"s_sub_u32 s41, s48, 5242880\n"
"ds_read_b64_tr_b16 a[224:225], v244 offset:17408\n"
"v_xor_b32 v253, s74, v253\n"
"v_mfma_f32_16x16x32_bf16 v[180:183], a[104:107], a[176:179], v[228:231]\n"
"s_min_u32 s48, s48, s41\n"
"ds_read_b64_tr_b16 a[226:227], v244 offset:17920\n"
"v_mfma_f32_16x16x32_bf16 v[180:183], a[108:111], a[180:183], v[180:183]\n"
"ds_read_b64_tr_b16 a[228:229], v244 offset:25600\n"
"v_mfma_f32_16x16x32_bf16 v[184:187], a[112:115], a[176:179], v[228:231]\n"
"s_mov_b32 m0, s50\n"
"ds_read_b64_tr_b16 a[230:231], v244 offset:26112\n"
"v_xor_b32 v244, s74, v244\n"
"v_mfma_f32_16x16x32_bf16 v[184:187], a[116:119], a[180:183], v[184:187]\n"
"buffer_load_dwordx4 v224, s[28:31], s48 offen lds\n"
"v_mfma_f32_16x16x32_bf16 v[188:191], a[120:123], a[176:179], v[228:231]\n"
"ds_read_b64_tr_b16 a[232:233], v245 offset:17408\n"
"v_mfma_f32_16x16x32_bf16 v[188:191], a[124:127], a[180:183], v[188:191]\n"
"ds_read_b64_tr_b16 a[234:235], v245 offset:17920\n"
"v_mfma_f32_16x16x32_bf16 v[200:203], a[144:147], a[176:179], v[228:231]\n"
"ds_read_b64_tr_b16 a[236:237], v245 offset:25600\n"
"v_mfma_f32_16x16x32_bf16 v[200:203], a[148:151], a[180:183], v[200:203]\n"
"buffer_load_dwordx4 v225, s[28:31], s48 offen offset:1024 lds\n"
"v_mfma_f32_16x16x32_bf16 v[204:207], a[152:155], a[176:179], v[228:231]\n"
"ds_read_b64_tr_b16 a[238:239], v245 offset:26112\n"
"v_xor_b32 v245, s74, v245\n"
"v_mfma_f32_16x16x32_bf16 v[204:207], a[156:159], a[180:183], v[204:207]\n"
"ds_read_b64_tr_b16 a[240:241], v246 offset:17408\n"
"v_mfma_f32_16x16x32_bf16 v[192:195], a[128:131], a[176:179], v[228:231]\n"
"ds_read_b64_tr_b16 a[242:243], v246 offset:17920\n"
"v_mfma_f32_16x16x32_bf16 v[192:195], a[132:135], a[180:183], v[192:195]\n"
"ds_read_b64_tr_b16 a[244:245], v246 offset:25600\n"
"v_mfma_f32_16x16x32_bf16 v[196:199], a[136:139], a[176:179], v[228:231]\n"
"ds_read_b64_tr_b16 a[246:247], v246 offset:26112\n"
"v_xor_b32 v246, s74, v246\n"
"v_mfma_f32_16x16x32_bf16 v[196:199], a[140:143], a[180:183], v[196:199]\n"
"buffer_load_dwordx4 v226, s[28:31], s48 offen offset:2048 lds\n"
"v_mfma_f32_16x16x32_bf16 v[220:223], a[136:139], a[184:187], v[228:231]\n"
"ds_read_b64_tr_b16 a[248:249], v247 offset:17408\n"
"v_mfma_f32_16x16x32_bf16 v[220:223], a[140:143], a[188:191], v[220:223]\n"
"ds_read_b64_tr_b16 a[250:251], v247 offset:17920\n"
"v_mfma_f32_16x16x32_bf16 v[216:219], a[128:131], a[184:187], v[228:231]\n"
"ds_read_b64_tr_b16 a[252:253], v247 offset:25600\n"
"v_mfma_f32_16x16x32_bf16 v[216:219], a[132:135], a[188:191], v[216:219]\n"
"ds_read_b64_tr_b16 a[254:255], v247 offset:26112\n"
"v_xor_b32 v247, s74, v247\n"
"v_mfma_f32_16x16x32_bf16 v[212:215], a[104:107], a[184:187], v[228:231]\n"
"v_mfma_f32_16x16x32_bf16 v[212:215], a[108:111], a[188:191], v[212:215]\n"
"buffer_load_dwordx4 v227, s[28:31], s48 offen offset:3072 lds\n"
"v_mfma_f32_16x16x32_bf16 v[208:211], a[96:99], a[184:187], v[228:231]\n"
"s_xor_b32 s50, s50, s74\n"
"v_mfma_f32_16x16x32_bf16 v[208:211], a[100:103], a[188:191], v[208:211]\n"
"v_mfma_f32_16x16x32_bf16 v[0:3], a[192:195], a[160:163], v[228:231]\n"
"v_mfma_f32_16x16x32_bf16 v[0:3], a[196:199], a[164:167], v[0:3]\n"
"v_mfma_f32_16x16x32_bf16 v[4:7], a[200:203], a[160:163], v[228:231]\n"
"s_waitcnt vmcnt(8) lgkmcnt(0)\n"
"v_mfma_f32_16x16x32_bf16 v[4:7], a[204:207], a[164:167], v[4:7]\n"
"s_barrier\n"
"v_mfma_f32_16x16x32_bf16 v[8:11], a[208:211], a[160:163], v[228:231]\n"
"s_mov_b32 m0, s51\n"
"ds_read_b64_tr_b16 a[96:97], v244 offset:0\n"
"v_mfma_f32_16x16x32_bf16 v[8:11], a[212:215], a[164:167], v[8:11]\n"
"buffer_load_dwordx4 v248, s[24:27], s49 offen lds\n"
"v_mfma_f32_16x16x32_bf16 v[12:15], a[216:219], a[160:163], v[228:231]\n"
"ds_read_b64_tr_b16 a[98:99], v244 offset:512\n"
"v_mfma_f32_16x16x32_bf16 v[12:15], a[220:223], a[164:167], v[12:15]\n"
"ds_read_b64_tr_b16 a[100:101], v244 offset:8192\n"
"v_mfma_f32_16x16x32_bf16 v[16:19], a[224:227], a[160:163], v[228:231]\n"
"ds_read_b64_tr_b16 a[102:103], v244 offset:8704\n"
"v_mfma_f32_16x16x32_bf16 v[16:19], a[228:231], a[164:167], v[16:19]\n"
"ds_read_b64_tr_b16 a[104:105], v245 offset:0\n"
"v_mfma_f32_16x16x32_bf16 v[20:23], a[232:235], a[160:163], v[228:231]\n"
"ds_read_b64_tr_b16 a[106:107], v245 offset:512\n"
"v_mfma_f32_16x16x32_bf16 v[20:23], a[236:239], a[164:167], v[20:23]\n"
"buffer_load_dwordx4 v249, s[24:27], s49 offen offset:1024 lds\n"
"v_mfma_f32_16x16x32_bf16 v[24:27], a[240:243], a[160:163], v[228:231]\n"
"ds_read_b64_tr_b16 a[108:109], v245 offset:8192\n"
"v_mfma_f32_16x16x32_bf16 v[24:27], a[244:247], a[164:167], v[24:27]\n"
"ds_read_b64_tr_b16 a[110:111], v245 offset:8704\n"
"v_mfma_f32_16x16x32_bf16 v[28:31], a[248:251], a[160:163], v[228:231]\n"
"ds_read_b128 a[160:163], v250 offset:0\n"
"v_mfma_f32_16x16x32_bf16 v[28:31], a[252:255], a[164:167], v[28:31]\n"
"ds_read_b128 a[164:167], v251 offset:0\n"
"v_mfma_f32_16x16x32_bf16 v[60:63], a[248:251], a[168:171], v[228:231]\n"
"ds_read_b64_tr_b16 a[112:113], v246 offset:0\n"
"v_mfma_f32_16x16x32_bf16 v[60:63], a[252:255], a[172:175], v[60:63]\n"
"buffer_load_dwordx4 v254, s[24:27], s49 offen offset:2048 lds\n"
"v_mfma_f32_16x16x32_bf16 v[56:59], a[240:243], a[168:171], v[228:231]\n"
"ds_read_b64_tr_b16 a[114:115], v246 offset:512\n"
"v_mfma_f32_16x16x32_bf16 v[56:59], a[244:247], a[172:175], v[56:59]\n"
"ds_read_b64_tr_b16 a[116:117], v246 offset:8192\n"
"v_mfma_f32_16x16x32_bf16 v[52:55], a[232:235], a[168:171], v[228:231]\n"
"ds_read_b64_tr_b16 a[118:119], v246 offset:8704\n"
"v_mfma_f32_16x16x32_bf16 v[52:55], a[236:239], a[172:175], v[52:55]\n"
"ds_read_b64_tr_b16 a[120:121], v247 offset:0\n"
"v_mfma_f32_16x16x32_bf16 v[48:51], a[224:227], a[168:171], v[228:231]\n"
"ds_read_b64_tr_b16 a[122:123], v247 offset:512\n"
"v_mfma_f32_16x16x32_bf16 v[48:51], a[228:231], a[172:175], v[48:51]\n"
"buffer_load_dwordx4 v255, s[24:27], s49 offen offset:3072 lds\n"
"v_mfma_f32_16x16x32_bf16 v[44:47], a[216:219], a[168:171], v[228:231]\n"
"ds_read_b64_tr_b16 a[124:125], v247 offset:8192\n"
"v_mfma_f32_16x16x32_bf16 v[44:47], a[220:223], a[172:175], v[44:47]\n"
"ds_read_b64_tr_b16 a[126:127], v247 offset:8704\n"
"v_mfma_f32_16x16x32_bf16 v[40:43], a[208:211], a[168:171], v[228:231]\n"
"ds_read_b64_tr_b16 a[128:129], v244 offset:1024\n"
"v_mfma_f32_16x16x32_bf16 v[40:43], a[212:215], a[172:175], v[40:43]\n"
"s_add_u32 s40, s49, 40960\n"
"ds_read_b64_tr_b16 a[130:131], v244 offset:1536\n"
"v_mfma_f32_16x16x32_bf16 v[36:39], a[200:203], a[168:171], v[228:231]\n"
"s_add_u32 m0, s51, 4096\n"
"ds_read_b64_tr_b16 a[132:133], v244 offset:9216\n"
"v_mfma_f32_16x16x32_bf16 v[36:39], a[204:207], a[172:175], v[36:39]\n"
"buffer_load_dwordx4 v248, s[24:27], s40 offen lds\n"
"v_mfma_f32_16x16x32_bf16 v[32:35], a[192:195], a[168:171], v[228:231]\n"
"ds_read_b64_tr_b16 a[134:135], v244 offset:9728\n"
"v_mfma_f32_16x16x32_bf16 v[32:35], a[196:199], a[172:175], v[32:35]\n"
"ds_read_b64_tr_b16 a[136:137], v245 offset:1024\n"
"v_mfma_f32_16x16x32_bf16 v[64:67], a[192:195], a[176:179], v[228:231]\n"
"ds_read_b64_tr_b16 a[138:139], v245 offset:1536\n"
"v_mfma_f32_16x16x32_bf16 v[64:67], a[196:199], a[180:183], v[64:67]\n"
"ds_read_b64_tr_b16 a[140:141], v245 offset:9216\n"
"v_mfma_f32_16x16x32_bf16 v[68:71], a[200:203], a[176:179], v[228:231]\n"
"ds_read_b64_tr_b16 a[142:143], v245 offset:9728\n"
"v_mfma_f32_16x16x32_bf16 v[68:71], a[204:207], a[180:183], v[68:71]\n"
"buffer_load_dwordx4 v249, s[24:27], s40 offen offset:1024 lds\n"
"v_mfma_f32_16x16x32_bf16 v[72:75], a[208:211], a[176:179], v[228:231]\n"
"ds_read_b64_tr_b16 a[144:145], v246 offset:1024\n"
"v_mfma_f32_16x16x32_bf16 v[72:75], a[212:215], a[180:183], v[72:75]\n"
"ds_read_b64_tr_b16 a[146:147], v246 offset:1536\n"
"v_mfma_f32_16x16x32_bf16 v[76:79], a[216:219], a[176:179], v[228:231]\n"
"ds_read_b64_tr_b16 a[148:149], v246 offset:9216\n"
"v_mfma_f32_16x16x32_bf16 v[76:79], a[220:223], a[180:183], v[76:79]\n"
"ds_read_b64_tr_b16 a[150:151], v246 offset:9728\n"
"v_mfma_f32_16x16x32_bf16 v[88:91], a[240:243], a[176:179], v[228:231]\n"
"ds_read_b64_tr_b16 a[152:153], v247 offset:1024\n"
"v_mfma_f32_16x16x32_bf16 v[88:91], a[244:247], a[180:183], v[88:91]\n"
"buffer_load_dwordx4 v254, s[24:27], s40 offen offset:2048 lds\n"
"v_mfma_f32_16x16x32_bf16 v[92:95], a[248:251], a[176:179], v[228:231]\n"
"s_add_u32 s49, s49, 128\n"
"ds_read_b64_tr_b16 a[154:155], v247 offset:1536\n"
"v_mfma_f32_16x16x32_bf16 v[92:95], a[252:255], a[180:183], v[92:95]\n"
"s_sub_u32 s41, s49, 1280\n"
"ds_read_b64_tr_b16 a[156:157], v247 offset:9216\n"
"v_mfma_f32_16x16x32_bf16 v[80:83], a[224:227], a[176:179], v[228:231]\n"
"s_min_u32 s49, s49, s41\n"
"ds_read_b64_tr_b16 a[158:159], v247 offset:9728\n"
"v_mfma_f32_16x16x32_bf16 v[80:83], a[228:231], a[180:183], v[80:83]\n"
"s_xor_b32 s51, s51, s74\n"
"ds_read_b128 a[168:171], v250 offset:2048\n"
"v_mfma_f32_16x16x32_bf16 v[84:87], a[232:235], a[176:179], v[228:231]\n"
"ds_read_b128 a[172:175], v251 offset:2048\n"
"v_mfma_f32_16x16x32_bf16 v[84:87], a[236:239], a[180:183], v[84:87]\n"
"ds_read_b128 a[176:179], v250 offset:4096\n"
"v_mfma_f32_16x16x32_bf16 v[108:111], a[232:235], a[184:187], v[228:231]\n"
"ds_read_b128 a[180:183], v251 offset:4096\n"
"v_mfma_f32_16x16x32_bf16 v[108:111], a[236:239], a[188:191], v[108:111]\n"
"v_mfma_f32_16x16x32_bf16 v[104:107], a[224:227], a[184:187], v[228:231]\n"
"v_mfma_f32_16x16x32_bf16 v[104:107], a[228:231], a[188:191], v[104:107]\n"
"v_mfma_f32_16x16x32_bf16 v[100:103], a[200:203], a[184:187], v[228:231]\n"
"v_mfma_f32_16x16x32_bf16 v[100:103], a[204:207], a[188:191], v[100:103]\n"
"v_mfma_f32_16x16x32_bf16 v[96:99], a[192:195], a[184:187], v[228:231]\n"
"s_add_u32 s47, s47, 1\n"
"ds_read_b128 a[184:187], v252 offset:0\n"
"v_mfma_f32_16x16x32_bf16 v[96:99], a[196:199], a[188:191], v[96:99]\n"
"s_waitcnt lgkmcnt(2)\n"
"ds_read_b128 a[188:191], v253 offset:0\n"
"LPL_%=:\n"
"v_mfma_f32_16x16x32_bf16 v[112:115], a[96:99], a[160:163], v[112:115]\n"
"v_mfma_f32_16x16x32_bf16 v[112:115], a[100:103], a[164:167], v[112:115]\n"
"v_mfma_f32_16x16x32_bf16 v[116:119], a[104:107], a[160:163], v[116:119]\n"
"s_waitcnt vmcnt(11) lgkmcnt(5)\n"
"v_mfma_f32_16x16x32_bf16 v[116:119], a[108:111], a[164:167], v[116:119]\n"
"s_barrier\n"
"v_mfma_f32_16x16x32_bf16 v[120:123], a[112:115], a[160:163], v[120:123]\n"
"s_add_u32 s40, s48, 256\n"
"s_xor_b32 m0, s50, 0xc000\n"
"ds_read_b64_tr_b16 a[192:193], v244 offset:16384\n"
"v_mfma_f32_16x16x32_bf16 v[120:123], a[116:119], a[164:167], v[120:123]\n"
"buffer_load_dwordx4 v224, s[28:31], s40 offen lds\n"
"v_mfma_f32_16x16x32_bf16 v[124:127], a[120:123], a[160:163], v[124:127]\n"
"ds_read_b64_tr_b16 a[194:195], v244 offset:16896\n"
"v_mfma_f32_16x16x32_bf16 v[124:127], a[124:127], a[164:167], v[124:127]\n"
"ds_read_b64_tr_b16 a[196:197], v244 offset:24576\n"
"v_mfma_f32_16x16x32_bf16 v[128:131], a[128:131], a[160:163], v[128:131]\n"
"ds_read_b64_tr_b16 a[198:199], v244 offset:25088\n"
"v_mfma_f32_16x16x32_bf16 v[128:131], a[132:135], a[164:167], v[128:131]\n"
"ds_read_b64_tr_b16 a[200:201], v245 offset:16384\n"
"v_mfma_f32_16x16x32_bf16 v[132:135], a[136:139], a[160:163], v[132:135]\n"
"ds_read_b64_tr_b16 a[202:203], v245 offset:16896\n"
"v_mfma_f32_16x16x32_bf16 v[132:135], a[140:143], a[164:167], v[132:135]\n"
"buffer_load_dwordx4 v225, s[28:31], s40 offen offset:1024 lds\n"
"v_mfma_f32_16x16x32_bf16 v[136:139], a[144:147], a[160:163], v[136:139]\n"
"ds_read_b64_tr_b16 a[204:205], v245 offset:24576\n"
"v_mfma_f32_16x16x32_bf16 v[136:139], a[148:151], a[164:167], v[136:139]\n"
"ds_read_b64_tr_b16 a[206:207], v245 offset:25088\n"
"v_mfma_f32_16x16x32_bf16 v[140:143], a[152:155], a[160:163], v[140:143]\n"
"ds_read_b64_tr_b16 a[208:209], v246 offset:16384\n"
"v_mfma_f32_16x16x32_bf16 v[140:143], a[156:159], a[164:167], v[140:143]\n"
"ds_read_b64_tr_b16 a[210:211], v246 offset:16896\n"
"v_mfma_f32_16x16x32_bf16 v[172:175], a[152:155], a[168:171], v[172:175]\n"
"s_waitcnt lgkmcnt(6)\n"
"ds_read_b64_tr_b16 a[212:213], v246 offset:24576\n"
"v_mfma_f32_16x16x32_bf16 v[172:175], a[156:159], a[172:175], v[172:175]\n"
"buffer_load_dwordx4 v226, s[28:31], s40 offen offset:2048 lds\n"
"v_mfma_f32_16x16x32_bf16 v[168:171], a[144:147], a[168:171], v[168:171]\n"
"ds_read_b64_tr_b16 a[214:215], v246 offset:25088\n"
"v_mfma_f32_16x16x32_bf16 v[168:171], a[148:151], a[172:175], v[168:171]\n"
"ds_read_b64_tr_b16 a[216:217], v247 offset:16384\n"
"v_mfma_f32_16x16x32_bf16 v[164:167], a[136:139], a[168:171], v[164:167]\n"
"ds_read_b64_tr_b16 a[218:219], v247 offset:16896\n"
"v_mfma_f32_16x16x32_bf16 v[164:167], a[140:143], a[172:175], v[164:167]\n"
"ds_read_b64_tr_b16 a[220:221], v247 offset:24576\n"
"v_mfma_f32_16x16x32_bf16 v[160:163], a[128:131], a[168:171], v[160:163]\n"
"ds_read_b64_tr_b16 a[222:223], v247 offset:25088\n"
"v_mfma_f32_16x16x32_bf16 v[160:163], a[132:135], a[172:175], v[160:163]\n"
"buffer_load_dwordx4 v227, s[28:31], s40 offen offset:3072 lds\n"
"v_mfma_f32_16x16x32_bf16 v[156:159], a[120:123], a[168:171], v[156:159]\n"
"s_cmp_eq_u32 s47, 8\n"
"ds_read_b64_tr_b16 a[224:225], v244 offset:17408\n"
"v_mfma_f32_16x16x32_bf16 v[156:159], a[124:127], a[172:175], v[156:159]\n"
"s_cselect_b32 s41, s52, s58\n"
"ds_read_b64_tr_b16 a[226:227], v244 offset:17920\n"
"v_mfma_f32_16x16x32_bf16 v[152:155], a[112:115], a[168:171], v[152:155]\n"
"s_add_u32 s48, s48, s41\n"
"ds_read_b64_tr_b16 a[228:229], v244 offset:25600\n"
"v_mfma_f32_16x16x32_bf16 v[152:155], a[116:119], a[172:175], v[152:155]\n"
"s_sub_u32 s41, s48, 5242880\n"
"ds_read_b64_tr_b16 a[230:231], v244 offset:26112\n"
"v_mfma_f32_16x16x32_bf16 v[148:151], a[104:107], a[168:171], v[148:151]\n"
"s_min_u32 s48, s48, s41\n"
"ds_read_b64_tr_b16 a[232:233], v245 offset:17408\n"
"v_mfma_f32_16x16x32_bf16 v[148:151], a[108:111], a[172:175], v[148:151]\n"
"ds_read_b64_tr_b16 a[234:235], v245 offset:17920\n"
"v_xor_b32 v244, s74, v244\n"
"v_mfma_f32_16x16x32_bf16 v[144:147], a[96:99], a[168:171], v[144:147]\n"
"s_mov_b32 m0, s50\n"
"ds_read_b64_tr_b16 a[236:237], v245 offset:25600\n"
"v_xor_b32 v250, s74, v250\n"
"v_mfma_f32_16x16x32_bf16 v[144:147], a[100:103], a[172:175], v[144:147]\n"
"buffer_load_dwordx4 v224, s[28:31], s48 offen lds\n"
"v_mfma_f32_16x16x32_bf16 v[176:179], a[96:99], a[176:179], v[176:179]\n"
"s_waitcnt lgkmcnt(6)\n"
"ds_read_b64_tr_b16 a[238:239], v245 offset:26112\n"
"v_xor_b32 v251, s74, v251\n"
"v_mfma_f32_16x16x32_bf16 v[176:179], a[100:103], a[180:183], v[176:179]\n"
"ds_read_b64_tr_b16 a[240:241], v246 offset:17408\n"
"v_xor_b32 v245, s74, v245\n"
"v_mfma_f32_16x16x32_bf16 v[180:183], a[104:107], a[176:179], v[180:183]\n"
"ds_read_b64_tr_b16 a[242:243], v246 offset:17920\n"
"v_xor_b32 v252, s74, v252\n"
"v_mfma_f32_16x16x32_bf16 v[180:183], a[108:111], a[180:183], v[180:183]\n"
"buffer_load_dwordx4 v225, s[28:31], s48 offen offset:1024 lds\n"
"v_mfma_f32_16x16x32_bf16 v[184:187], a[112:115], a[176:179], v[184:187]\n"
"ds_read_b64_tr_b16 a[244:245], v246 offset:25600\n"
"v_xor_b32 v253, s74, v253\n"
"v_mfma_f32_16x16x32_bf16 v[184:187], a[116:119], a[180:183], v[184:187]\n"
"ds_read_b64_tr_b16 a[246:247], v246 offset:26112\n"
"v_xor_b32 v246, s74, v246\n"
"v_mfma_f32_16x16x32_bf16 v[188:191], a[120:123], a[176:179], v[188:191]\n"
"ds_read_b64_tr_b16 a[248:249], v247 offset:17408\n"
"v_mfma_f32_16x16x32_bf16 v[188:191], a[124:127], a[180:183], v[188:191]\n"
"ds_read_b64_tr_b16 a[250:251], v247 offset:17920\n"
"v_mfma_f32_16x16x32_bf16 v[200:203], a[144:147], a[176:179], v[200:203]\n"
"ds_read_b64_tr_b16 a[252:253], v247 offset:25600\n"
"v_mfma_f32_16x16x32_bf16 v[200:203], a[148:151], a[180:183], v[200:203]\n"
"buffer_load_dwordx4 v226, s[28:31], s48 offen offset:2048 lds\n"
"v_mfma_f32_16x16x32_bf16 v[204:207], a[152:155], a[176:179], v[204:207]\n"
"ds_read_b64_tr_b16 a[254:255], v247 offset:26112\n"
"v_xor_b32 v247, s74, v247\n"
"v_mfma_f32_16x16x32_bf16 v[204:207], a[156:159], a[180:183], v[204:207]\n"
"v_mfma_f32_16x16x32_bf16 v[192:195], a[128:131], a[176:179], v[192:195]\n"
"v_mfma_f32_16x16x32_bf16 v[192:195], a[132:135], a[180:183], v[192:195]\n"
"v_mfma_f32_16x16x32_bf16 v[196:199], a[136:139], a[176:179], v[196:199]\n"
"v_mfma_f32_16x16x32_bf16 v[196:199], a[140:143], a[180:183], v[196:199]\n"
"buffer_load_dwordx4 v227, s[28:31], s48 offen offset:3072 lds\n"
"v_mfma_f32_16x16x32_bf16 v[220:223], a[136:139], a[184:187], v[220:223]\n"
"s_xor_b32 s50, s50, s74\n"
"v_mfma_f32_16x16x32_bf16 v[220:223], a[140:143], a[188:191], v[220:223]\n"
"v_mfma_f32_16x16x32_bf16 v[216:219], a[128:131], a[184:187], v[216:219]\n"
"v_mfma_f32_16x16x32_bf16 v[216:219], a[132:135], a[188:191], v[216:219]\n"
"v_mfma_f32_16x16x32_bf16 v[212:215], a[104:107], a[184:187], v[212:215]\n"
"v_mfma_f32_16x16x32_bf16 v[212:215], a[108:111], a[188:191], v[212:215]\n"
"v_mfma_f32_16x16x32_bf16 v[208:211], a[96:99], a[184:187], v[208:211]\n"
"v_mfma_f32_16x16x32_bf16 v[208:211], a[100:103], a[188:191], v[208:211]\n"
"v_mfma_f32_16x16x32_bf16 v[0:3], a[192:195], a[160:163], v[0:3]\n"
"v_mfma_f32_16x16x32_bf16 v[0:3], a[196:199], a[164:167], v[0:3]\n"
"s_waitcnt lgkmcnt(0)\n"
"v_mfma_f32_16x16x32_bf16 v[4:7], a[200:203], a[160:163], v[4:7]\n"
"s_waitcnt vmcnt(8) lgkmcnt(0)\n"
"v_mfma_f32_16x16x32_bf16 v[4:7], a[204:207], a[164:167], v[4:7]\n"
"s_barrier\n"
"v_mfma_f32_16x16x32_bf16 v[8:11], a[208:211], a[160:163], v[8:11]\n"
"s_mov_b32 m0, s51\n"
"ds_read_b64_tr_b16 a[96:97], v244 offset:0\n"
"v_mfma_f32_16x16x32_bf16 v[8:11], a[212:215], a[164:167], v[8:11]\n"
"buffer_load_dwordx4 v248, s[24:27], s49 offen lds\n"
"v_mfma_f32_16x16x32_bf16 v[12:15], a[216:219], a[160:163], v[12:15]\n"
"ds_read_b64_tr_b16 a[98:99], v244 offset:512\n"
"v_mfma_f32_16x16x32_bf16 v[12:15], a[220:223], a[164:167], v[12:15]\n"
"ds_read_b64_tr_b16 a[100:101], v244 offset:8192\n"
"v_mfma_f32_16x16x32_bf16 v[16:19], a[224:227], a[160:163], v[16:19]\n"
"ds_read_b64_tr_b16 a[102:103], v244 offset:8704\n"
"v_mfma_f32_16x16x32_bf16 v[16:19], a[228:231], a[164:167], v[16:19]\n"
"ds_read_b64_tr_b16 a[104:105], v245 offset:0\n"
"v_mfma_f32_16x16x32_bf16 v[20:23], a[232:235], a[160:163], v[20:23]\n"
"ds_read_b64_tr_b16 a[106:107], v245 offset:512\n"
"v_mfma_f32_16x16x32_bf16 v[20:23], a[236:239], a[164:167], v[20:23]\n"
"buffer_load_dwordx4 v249, s[24:27], s49 offen offset:1024 lds\n"
"v_mfma_f32_16x16x32_bf16 v[24:27], a[240:243], a[160:163], v[24:27]\n"
"ds_read_b64_tr_b16 a[108:109], v245 offset:8192\n"
"v_mfma_f32_16x16x32_bf16 v[24:27], a[244:247], a[164:167], v[24:27]\n"
"ds_read_b64_tr_b16 a[110:111], v245 offset:8704\n"
"v_mfma_f32_16x16x32_bf16 v[28:31], a[248:251], a[160:163], v[28:31]\n"
"ds_read_b128 a[160:163], v250 offset:0\n"
"v_mfma_f32_16x16x32_bf16 v[28:31], a[252:255], a[164:167], v[28:31]\n"
"ds_read_b128 a[164:167], v251 offset:0\n"
"v_mfma_f32_16x16x32_bf16 v[60:63], a[248:251], a[168:171], v[60:63]\n"
"ds_read_b64_tr_b16 a[112:113], v246 offset:0\n"
"v_mfma_f32_16x16x32_bf16 v[60:63], a[252:255], a[172:175], v[60:63]\n"
"buffer_load_dwordx4 v254, s[24:27], s49 offen offset:2048 lds\n"
"v_mfma_f32_16x16x32_bf16 v[56:59], a[240:243], a[168:171], v[56:59]\n"
"ds_read_b64_tr_b16 a[114:115], v246 offset:512\n"
"v_mfma_f32_16x16x32_bf16 v[56:59], a[244:247], a[172:175], v[56:59]\n"
"ds_read_b64_tr_b16 a[116:117], v246 offset:8192\n"
"v_mfma_f32_16x16x32_bf16 v[52:55], a[232:235], a[168:171], v[52:55]\n"
"ds_read_b64_tr_b16 a[118:119], v246 offset:8704\n"
"v_mfma_f32_16x16x32_bf16 v[52:55], a[236:239], a[172:175], v[52:55]\n"
"ds_read_b64_tr_b16 a[120:121], v247 offset:0\n"
"v_mfma_f32_16x16x32_bf16 v[48:51], a[224:227], a[168:171], v[48:51]\n"
"ds_read_b64_tr_b16 a[122:123], v247 offset:512\n"
"v_mfma_f32_16x16x32_bf16 v[48:51], a[228:231], a[172:175], v[48:51]\n"
"buffer_load_dwordx4 v255, s[24:27], s49 offen offset:3072 lds\n"
"v_mfma_f32_16x16x32_bf16 v[44:47], a[216:219], a[168:171], v[44:47]\n"
"ds_read_b64_tr_b16 a[124:125], v247 offset:8192\n"
"v_mfma_f32_16x16x32_bf16 v[44:47], a[220:223], a[172:175], v[44:47]\n"
"ds_read_b64_tr_b16 a[126:127], v247 offset:8704\n"
"v_mfma_f32_16x16x32_bf16 v[40:43], a[208:211], a[168:171], v[40:43]\n"
"ds_read_b64_tr_b16 a[128:129], v244 offset:1024\n"
"v_mfma_f32_16x16x32_bf16 v[40:43], a[212:215], a[172:175], v[40:43]\n"
"s_add_u32 s40, s49, 40960\n"
"ds_read_b64_tr_b16 a[130:131], v244 offset:1536\n"
"v_mfma_f32_16x16x32_bf16 v[36:39], a[200:203], a[168:171], v[36:39]\n"
"s_add_u32 m0, s51, 4096\n"
"ds_read_b64_tr_b16 a[132:133], v244 offset:9216\n"
"v_mfma_f32_16x16x32_bf16 v[36:39], a[204:207], a[172:175], v[36:39]\n"
"buffer_load_dwordx4 v248, s[24:27], s40 offen lds\n"
"v_mfma_f32_16x16x32_bf16 v[32:35], a[192:195], a[168:171], v[32:35]\n"
"ds_read_b64_tr_b16 a[134:135], v244 offset:9728\n"
"v_mfma_f32_16x16x32_bf16 v[32:35], a[196:199], a[172:175], v[32:35]\n"
"ds_read_b64_tr_b16 a[136:137], v245 offset:1024\n"
"v_mfma_f32_16x16x32_bf16 v[64:67], a[192:195], a[176:179], v[64:67]\n"
"ds_read_b64_tr_b16 a[138:139], v245 offset:1536\n"
"v_mfma_f32_16x16x32_bf16 v[64:67], a[196:199], a[180:183], v[64:67]\n"
"ds_read_b64_tr_b16 a[140:141], v245 offset:9216\n"
"v_mfma_f32_16x16x32_bf16 v[68:71], a[200:203], a[176:179], v[68:71]\n"
"ds_read_b64_tr_b16 a[142:143], v245 offset:9728\n"
"v_mfma_f32_16x16x32_bf16 v[68:71], a[204:207], a[180:183], v[68:71]\n"
"buffer_load_dwordx4 v249, s[24:27], s40 offen offset:1024 lds\n"
"v_mfma_f32_16x16x32_bf16 v[72:75], a[208:211], a[176:179], v[72:75]\n"
"ds_read_b64_tr_b16 a[144:145], v246 offset:1024\n"
"v_mfma_f32_16x16x32_bf16 v[72:75], a[212:215], a[180:183], v[72:75]\n"
"ds_read_b64_tr_b16 a[146:147], v246 offset:1536\n"
"v_mfma_f32_16x16x32_bf16 v[76:79], a[216:219], a[176:179], v[76:79]\n"
"ds_read_b64_tr_b16 a[148:149], v246 offset:9216\n"
"v_mfma_f32_16x16x32_bf16 v[76:79], a[220:223], a[180:183], v[76:79]\n"
"ds_read_b64_tr_b16 a[150:151], v246 offset:9728\n"
"v_mfma_f32_16x16x32_bf16 v[88:91], a[240:243], a[176:179], v[88:91]\n"
"ds_read_b64_tr_b16 a[152:153], v247 offset:1024\n"
"v_mfma_f32_16x16x32_bf16 v[88:91], a[244:247], a[180:183], v[88:91]\n"
"buffer_load_dwordx4 v254, s[24:27], s40 offen offset:2048 lds\n"
"v_mfma_f32_16x16x32_bf16 v[92:95], a[248:251], a[176:179], v[92:95]\n"
"s_add_u32 s49, s49, 128\n"
"ds_read_b64_tr_b16 a[154:155], v247 offset:1536\n"
"v_mfma_f32_16x16x32_bf16 v[92:95], a[252:255], a[180:183], v[92:95]\n"
"s_sub_u32 s41, s49, 1280\n"
"ds_read_b64_tr_b16 a[156:157], v247 offset:9216\n"
"v_mfma_f32_16x16x32_bf16 v[80:83], a[224:227], a[176:179], v[80:83]\n"
"s_min_u32 s49, s49, s41\n"
"ds_read_b64_tr_b16 a[158:159], v247 offset:9728\n"
"v_mfma_f32_16x16x32_bf16 v[80:83], a[228:231], a[180:183], v[80:83]\n"
"s_xor_b32 s51, s51, s74\n"
"ds_read_b128 a[168:171], v250 offset:2048\n"
"v_mfma_f32_16x16x32_bf16 v[84:87], a[232:235], a[176:179], v[84:87]\n"
"ds_read_b128 a[172:175], v251 offset:2048\n"
"v_mfma_f32_16x16x32_bf16 v[84:87], a[236:239], a[180:183], v[84:87]\n"
"ds_read_b128 a[176:179], v250 offset:4096\n"
"v_mfma_f32_16x16x32_bf16 v[108:111], a[232:235], a[184:187], v[108:111]\n"
"ds_read_b128 a[180:183], v251 offset:4096\n"
"v_mfma_f32_16x16x32_bf16 v[108:111], a[236:239], a[188:191], v[108:111]\n"
"v_mfma_f32_16x16x32_bf16 v[104:107], a[224:227], a[184:187], v[104:107]\n"
"v_mfma_f32_16x16x32_bf16 v[104:107], a[228:231], a[188:191], v[104:107]\n"
"v_mfma_f32_16x16x32_bf16 v[100:103], a[200:203], a[184:187], v[100:103]\n"
"v_mfma_f32_16x16x32_bf16 v[100:103], a[204:207], a[188:191], v[100:103]\n"
"v_mfma_f32_16x16x32_bf16 v[96:99], a[192:195], a[184:187], v[96:99]\n"
"s_add_u32 s47, s47, 1\n"
"ds_read_b128 a[184:187], v252 offset:0\n"
"v_mfma_f32_16x16x32_bf16 v[96:99], a[196:199], a[188:191], v[96:99]\n"
"s_waitcnt lgkmcnt(2)\n"
"s_cmp_lg_u32 s47, s62\n"
"ds_read_b128 a[188:191], v253 offset:0\n"
"s_cbranch_scc1 LPL_%=\n"
"s_cmp_eq_u32 s47, 18\n"
"s_cbranch_scc1 LPT2_%=\n"
"v_mfma_f32_16x16x32_bf16 v[112:115], a[96:99], a[160:163], v[112:115]\n"
"s_add_u32 s78, s96, 8192\n"
"v_mov_b32 v241, 1\n"
"v_mfma_f32_16x16x32_bf16 v[112:115], a[100:103], a[164:167], v[112:115]\n"
"s_mov_b64 exec, 1\n"
"buffer_store_dword v241, off, s[32:35], s96 sc0 sc1\n"
"buffer_store_dword v241, off, s[32:35], s78 sc0 sc1\n"
"s_mov_b64 exec, -1\n"
"v_mfma_f32_16x16x32_bf16 v[116:119], a[104:107], a[160:163], v[116:119]\n"
"s_waitcnt vmcnt(13)\n"
"v_mfma_f32_16x16x32_bf16 v[116:119], a[108:111], a[164:167], v[116:119]\n"
"s_barrier\n"
"v_mfma_f32_16x16x32_bf16 v[120:123], a[112:115], a[160:163], v[120:123]\n"
"s_add_u32 s40, s48, 256\n"
"s_xor_b32 m0, s50, 0xc000\n"
"ds_read_b64_tr_b16 a[232:233], v245 offset:17408\n"
"v_mfma_f32_16x16x32_bf16 v[120:123], a[116:119], a[164:167], v[120:123]\n"
"buffer_load_dwordx4 v224, s[28:31], s40 offen lds\n"
"v_mfma_f32_16x16x32_bf16 v[124:127], a[120:123], a[160:163], v[124:127]\n"
"ds_read_b64_tr_b16 a[234:235], v245 offset:17920\n"
"v_mfma_f32_16x16x32_bf16 v[124:127], a[124:127], a[164:167], v[124:127]\n"
"ds_read_b64_tr_b16 a[236:237], v245 offset:25600\n"
"v_mfma_f32_16x16x32_bf16 v[128:131], a[128:131], a[160:163], v[128:131]\n"
"ds_read_b64_tr_b16 a[238:239], v245 offset:26112\n"
"v_mfma_f32_16x16x32_bf16 v[128:131], a[132:135], a[164:167], v[128:131]\n"
"ds_read_b64_tr_b16 a[224:225], v244 offset:17408\n"
"v_mfma_f32_16x16x32_bf16 v[132:135], a[136:139], a[160:163], v[132:135]\n"
"ds_read_b64_tr_b16 a[226:227], v244 offset:17920\n"
"v_mfma_f32_16x16x32_bf16 v[132:135], a[140:143], a[164:167], v[132:135]\n"
"buffer_load_dwordx4 v225, s[28:31], s40 offen offset:1024 lds\n"
"v_mfma_f32_16x16x32_bf16 v[136:139], a[144:147], a[160:163], v[136:139]\n"
"ds_read_b64_tr_b16 a[192:193], v244 offset:16384\n"
"v_mfma_f32_16x16x32_bf16 v[136:139], a[148:151], a[164:167], v[136:139]\n"
"ds_read_b64_tr_b16 a[194:195], v244 offset:16896\n"
"v_mfma_f32_16x16x32_bf16 v[140:143], a[152:155], a[160:163], v[140:143]\n"
"ds_read_b64_tr_b16 a[196:197], v244 offset:24576\n"
"v_mfma_f32_16x16x32_bf16 v[140:143], a[156:159], a[164:167], v[140:143]\n"
"s_waitcnt lgkmcnt(6)\n"
"ds_read_b64_tr_b16 a[198:199], v244 offset:25088\n"
"v_mfma_f32_16x16x32_bf16 v[220:223], a[136:139], a[184:187], v[220:223]\n"
"ds_read_b64_tr_b16 a[200:201], v245 offset:16384\n"
"v_mfma_f32_16x16x32_bf16 v[220:223], a[140:143], a[188:191], v[220:223]\n"
"buffer_load_dwordx4 v226, s[28:31], s40 offen offset:2048 lds\n"
"v_mfma_f32_16x16x32_bf16 v[216:219], a[128:131], a[184:187], v[216:219]\n"
"ds_read_b64_tr_b16 a[202:203], v245 offset:16896\n"
"v_mfma_f32_16x16x32_bf16 v[216:219], a[132:135], a[188:191], v[216:219]\n"
"ds_read_b64_tr_b16 a[204:205], v245 offset:24576\n"
"v_mfma_f32_16x16x32_bf16 v[212:215], a[104:107], a[184:187], v[212:215]\n"
"ds_read_b64_tr_b16 a[206:207], v245 offset:25088\n"
"v_mfma_f32_16x16x32_bf16 v[212:215], a[108:111], a[188:191], v[212:215]\n"
"v_pk_fma_f32 v[112:113], v[112:113], s[70:71], s[70:71] op_sel:[0,0,1] op_sel_hi:[1,0,1]\nv_pk_fma_f32 v[114:115], v[114:115], s[70:71], s[70:71] op_sel:[0,0,1] op_sel_hi:[1,0,1]\nv_pk_fma_f32 v[116:117], v[116:117], s[70:71], s[70:71] op_sel:[0,0,1] op_sel_hi:[1,0,1]\nv_pk_fma_f32 v[118:119], v[118:119], s[70:71], s[70:71] op_sel:[0,0,1] op_sel_hi:[1,0,1]\nv_pk_fma_f32 v[120:121], v[120:121], s[70:71], s[70:71] op_sel:[0,0,1] op_sel_hi:[1,0,1]\nv_pk_fma_f32 v[122:123], v[122:123], s[70:71], s[70:71] op_sel:[0,0,1] op_sel_hi:[1,0,1]\nv_pk_fma_f32 v[124:125], v[124:125], s[70:71], s[70:71] op_sel:[0,0,1] op_sel_hi:[1,0,1]\nv_pk_fma_f32 v[126:127], v[126:127], s[70:71], s[70:71] op_sel:[0,0,1] op_sel_hi:[1,0,1]\nv_pk_fma_f32 v[128:129], v[128:129], s[70:71], s[70:71] op_sel:[0,0,1] op_sel_hi:[1,0,1]\nv_pk_fma_f32 v[130:131], v[130:131], s[70:71], s[70:71] op_sel:[0,0,1] op_sel_hi:[1,0,1]\nv_pk_fma_f32 v[132:133], v[132:133], s[70:71], s[70:71] op_sel:[0,0,1] op_sel_hi:[1,0,1]\nv_pk_fma_f32 v[134:135], v[134:135], s[70:71], s[70:71] op_sel:[0,0,1] op_sel_hi:[1,0,1]\nv_pk_fma_f32 v[136:137], v[136:137], s[70:71], s[70:71] op_sel:[0,0,1] op_sel_hi:[1,0,1]\nv_pk_fma_f32 v[138:139], v[138:139], s[70:71], s[70:71] op_sel:[0,0,1] op_sel_hi:[1,0,1]\nv_pk_fma_f32 v[140:141], v[140:141], s[70:71], s[70:71] op_sel:[0,0,1] op_sel_hi:[1,0,1]\nv_pk_fma_f32 v[142:143], v[142:143], s[70:71], s[70:71] op_sel:[0,0,1] op_sel_hi:[1,0,1]\n"
"v_mfma_f32_16x16x32_bf16 v[208:211], a[96:99], a[184:187], v[208:211]\n"
"ds_read_b64_tr_b16 a[208:209], v246 offset:16384\n"
"v_cvt_pk_bf16_f32 v112, v112, v113\n"
"v_mfma_f32_16x16x32_bf16 v[208:211], a[100:103], a[188:191], v[208:211]\n"
"buffer_load_dwordx4 v227, s[28:31], s40 offen offset:3072 lds\n"
"v_mfma_f32_16x16x32_bf16 v[172:175], a[152:155], a[168:171], v[172:175]\n"
"s_cmp_eq_u32 s47, 8\n"
"ds_read_b64_tr_b16 a[210:211], v246 offset:16896\n"
"v_cvt_pk_bf16_f32 v113, v114, v115\n"
"v_mfma_f32_16x16x32_bf16 v[172:175], a[156:159], a[172:175], v[172:175]\n"
"s_cselect_b32 s41, s52, s58\n"
"ds_read_b64_tr_b16 a[212:213], v246 offset:24576\n"
"v_cvt_pk_bf16_f32 v114, v116, v117\n"
"v_mfma_f32_16x16x32_bf16 v[168:171], a[144:147], a[168:171], v[168:171]\n"
"s_add_u32 s48, s48, s41\n"
"ds_read_b64_tr_b16 a[214:215], v246 offset:25088\n"
"v_cvt_pk_bf16_f32 v115, v118, v119\n"
"v_mfma_f32_16x16x32_bf16 v[168:171], a[148:151], a[172:175], v[168:171]\n"
"s_sub_u32 s41, s48, 5242880\n"
"ds_read_b64_tr_b16 a[216:217], v247 offset:16384\n"
"v_cvt_pk_bf16_f32 v120, v120, v121\n"
"v_mfma_f32_16x16x32_bf16 v[164:167], a[136:139], a[168:171], v[164:167]\n"
"s_min_u32 s48, s48, s41\n"
"ds_read_b64_tr_b16 a[218:219], v247 offset:16896\n"
"v_cvt_pk_bf16_f32 v121, v122, v123\n"
"v_mfma_f32_16x16x32_bf16 v[164:167], a[140:143], a[172:175], v[164:167]\n"
"ds_read_b64_tr_b16 a[220:221], v247 offset:24576\n"
"v_xor_b32 v250, s74, v250\n"
"v_mfma_f32_16x16x32_bf16 v[160:163], a[128:131], a[168:171], v[160:163]\n"
"s_mov_b32 m0, s50\n"
"ds_read_b64_tr_b16 a[222:223], v247 offset:25088\n"
"v_xor_b32 v251, s74, v251\n"
"v_mfma_f32_16x16x32_bf16 v[160:163], a[132:135], a[172:175], v[160:163]\n"
"buffer_load_dwordx4 v224, s[28:31], s48 offen lds\n"
"v_mfma_f32_16x16x32_bf16 v[156:159], a[120:123], a[168:171], v[156:159]\n"
"ds_read_b64_tr_b16 a[228:229], v244 offset:25600\n"
"v_xor_b32 v245, s74, v245\n"
"v_mfma_f32_16x16x32_bf16 v[156:159], a[124:127], a[172:175], v[156:159]\n"
"s_mov_b64 s[80:81], s[8:9]\n"
"ds_read_b64_tr_b16 a[230:231], v244 offset:26112\n"
"v_xor_b32 v244, s74, v244\n"
"v_mfma_f32_16x16x32_bf16 v[152:155], a[112:115], a[168:171], v[152:155]\n"
"s_mov_b32 s82, s68\n"
"ds_read_b64_tr_b16 a[240:241], v246 offset:17408\n"
"v_cvt_pk_bf16_f32 v122, v124, v125\n"
"v_mfma_f32_16x16x32_bf16 v[152:155], a[116:119], a[172:175], v[152:155]\n"
"s_mov_b32 s83, 131072\n"
"buffer_load_dwordx4 v225, s[28:31], s48 offen offset:1024 lds\n"
"v_mfma_f32_16x16x32_bf16 v[148:151], a[104:107], a[168:171], v[148:151]\n"
"v_and_b32 v241, 0x3f0, v242\n"
"ds_read_b64_tr_b16 a[242:243], v246 offset:17920\n"
"v_cvt_pk_bf16_f32 v123, v126, v127\n"
"v_mfma_f32_16x16x32_bf16 v[148:151], a[108:111], a[172:175], v[148:151]\n"
"v_mul_u32_u24 v241, 10, v241\n"
"ds_read_b64_tr_b16 a[244:245], v246 offset:25600\n"
"v_cvt_pk_bf16_f32 v128, v128, v129\n"
"v_mfma_f32_16x16x32_bf16 v[144:147], a[96:99], a[168:171], v[144:147]\n"
"ds_read_b64_tr_b16 a[246:247], v246 offset:26112\n"
"v_xor_b32 v246, s74, v246\n"
"v_mfma_f32_16x16x32_bf16 v[144:147], a[100:103], a[172:175], v[144:147]\n"
"ds_read_b64_tr_b16 a[248:249], v247 offset:17408\n"
"v_cvt_pk_bf16_f32 v129, v130, v131\n"
"v_mfma_f32_16x16x32_bf16 v[176:179], a[96:99], a[176:179], v[176:179]\n"
"ds_read_b64_tr_b16 a[250:251], v247 offset:17920\n"
"v_cvt_pk_bf16_f32 v130, v132, v133\n"
"v_mfma_f32_16x16x32_bf16 v[176:179], a[100:103], a[180:183], v[176:179]\n"
"buffer_load_dwordx4 v226, s[28:31], s48 offen offset:2048 lds\n"
"v_mfma_f32_16x16x32_bf16 v[180:183], a[104:107], a[176:179], v[180:183]\n"
"ds_read_b64_tr_b16 a[252:253], v247 offset:25600\n"
"v_cvt_pk_bf16_f32 v131, v134, v135\n"
"v_mfma_f32_16x16x32_bf16 v[180:183], a[108:111], a[180:183], v[180:183]\n"
"ds_read_b64_tr_b16 a[254:255], v247 offset:26112\n"
"v_xor_b32 v247, s74, v247\n"
"v_mfma_f32_16x16x32_bf16 v[184:187], a[112:115], a[176:179], v[184:187]\n"
"v_permlane16_swap_b32 v112, v114\n"
"v_mfma_f32_16x16x32_bf16 v[184:187], a[116:119], a[180:183], v[184:187]\n"
"v_permlane16_swap_b32 v113, v115\n"
"v_mfma_f32_16x16x32_bf16 v[188:191], a[120:123], a[176:179], v[188:191]\n"
"v_permlane16_swap_b32 v120, v122\n"
"v_mfma_f32_16x16x32_bf16 v[188:191], a[124:127], a[180:183], v[188:191]\n"
"buffer_load_dwordx4 v227, s[28:31], s48 offen offset:3072 lds\n"
"v_mfma_f32_16x16x32_bf16 v[200:203], a[144:147], a[176:179], v[200:203]\n"
"s_xor_b32 s50, s50, s74\n"
"v_permlane16_swap_b32 v121, v123\n"
"v_mfma_f32_16x16x32_bf16 v[200:203], a[148:151], a[180:183], v[200:203]\n"
"v_permlane16_swap_b32 v128, v130\n"
"v_mfma_f32_16x16x32_bf16 v[204:207], a[152:155], a[176:179], v[204:207]\n"
"v_permlane16_swap_b32 v129, v131\n"
"v_mfma_f32_16x16x32_bf16 v[204:207], a[156:159], a[180:183], v[204:207]\n"
"v_pk_fma_f32 v[144:145], v[144:145], s[70:71], s[70:71] op_sel:[0,0,1] op_sel_hi:[1,0,1]\nv_pk_fma_f32 v[146:147], v[146:147], s[70:71], s[70:71] op_sel:[0,0,1] op_sel_hi:[1,0,1]\nv_pk_fma_f32 v[148:149], v[148:149], s[70:71], s[70:71] op_sel:[0,0,1] op_sel_hi:[1,0,1]\nv_pk_fma_f32 v[150:151], v[150:151], s[70:71], s[70:71] op_sel:[0,0,1] op_sel_hi:[1,0,1]\nv_pk_fma_f32 v[152:153], v[152:153], s[70:71], s[70:71] op_sel:[0,0,1] op_sel_hi:[1,0,1]\nv_pk_fma_f32 v[154:155], v[154:155], s[70:71], s[70:71] op_sel:[0,0,1] op_sel_hi:[1,0,1]\nv_pk_fma_f32 v[156:157], v[156:157], s[70:71], s[70:71] op_sel:[0,0,1] op_sel_hi:[1,0,1]\nv_pk_fma_f32 v[158:159], v[158:159], s[70:71], s[70:71] op_sel:[0,0,1] op_sel_hi:[1,0,1]\nv_pk_fma_f32 v[160:161], v[160:161], s[70:71], s[70:71] op_sel:[0,0,1] op_sel_hi:[1,0,1]\nv_pk_fma_f32 v[162:163], v[162:163], s[70:71], s[70:71] op_sel:[0,0,1] op_sel_hi:[1,0,1]\nv_pk_fma_f32 v[164:165], v[164:165], s[70:71], s[70:71] op_sel:[0,0,1] op_sel_hi:[1,0,1]\nv_pk_fma_f32 v[166:167], v[166:167], s[70:71], s[70:71] op_sel:[0,0,1] op_sel_hi:[1,0,1]\nv_pk_fma_f32 v[168:169], v[168:169], s[70:71], s[70:71] op_sel:[0,0,1] op_sel_hi:[1,0,1]\nv_pk_fma_f32 v[170:171], v[170:171], s[70:71], s[70:71] op_sel:[0,0,1] op_sel_hi:[1,0,1]\nv_pk_fma_f32 v[172:173], v[172:173], s[70:71], s[70:71] op_sel:[0,0,1] op_sel_hi:[1,0,1]\nv_pk_fma_f32 v[174:175], v[174:175], s[70:71], s[70:71] op_sel:[0,0,1] op_sel_hi:[1,0,1]\n"
"v_mfma_f32_16x16x32_bf16 v[192:195], a[128:131], a[176:179], v[192:195]\n"
"v_pk_fma_f32 v[208:209], v[208:209], s[70:71], s[70:71] op_sel:[0,0,1] op_sel_hi:[1,0,1]\nv_pk_fma_f32 v[210:211], v[210:211], s[70:71], s[70:71] op_sel:[0,0,1] op_sel_hi:[1,0,1]\nv_pk_fma_f32 v[212:213], v[212:213], s[70:71], s[70:71] op_sel:[0,0,1] op_sel_hi:[1,0,1]\nv_pk_fma_f32 v[214:215], v[214:215], s[70:71], s[70:71] op_sel:[0,0,1] op_sel_hi:[1,0,1]\nv_pk_fma_f32 v[216:217], v[216:217], s[70:71], s[70:71] op_sel:[0,0,1] op_sel_hi:[1,0,1]\nv_pk_fma_f32 v[218:219], v[218:219], s[70:71], s[70:71] op_sel:[0,0,1] op_sel_hi:[1,0,1]\nv_pk_fma_f32 v[220:221], v[220:221], s[70:71], s[70:71] op_sel:[0,0,1] op_sel_hi:[1,0,1]\nv_pk_fma_f32 v[222:223], v[222:223], s[70:71], s[70:71] op_sel:[0,0,1] op_sel_hi:[1,0,1]\n"
"v_mfma_f32_16x16x32_bf16 v[192:195], a[132:135], a[180:183], v[192:195]\n"
"v_accvgpr_write_b32 a0, v112\n"
"v_accvgpr_write_b32 a1, v113\n"
"v_mfma_f32_16x16x32_bf16 v[196:199], a[136:139], a[176:179], v[196:199]\n"
"v_accvgpr_write_b32 a2, v114\n"
"v_accvgpr_write_b32 a3, v115\n"
"v_mfma_f32_16x16x32_bf16 v[196:199], a[140:143], a[180:183], v[196:199]\n"
"v_accvgpr_write_b32 a4, v120\n"
"v_accvgpr_write_b32 a5, v121\n"
"v_mfma_f32_16x16x32_bf16 v[108:111], a[232:235], a[184:187], v[108:111]\n"
"s_waitcnt lgkmcnt(0)\n"
"v_accvgpr_write_b32 a6, v122\n"
"v_accvgpr_write_b32 a7, v123\n"
"v_mfma_f32_16x16x32_bf16 v[108:111], a[236:239], a[188:191], v[108:111]\n"
"v_accvgpr_write_b32 a8, v128\n"
"v_accvgpr_write_b32 a9, v129\n"
"v_mfma_f32_16x16x32_bf16 v[104:107], a[224:227], a[184:187], v[104:107]\n"
"s_waitcnt vmcnt(10)\n"
"v_accvgpr_write_b32 a10, v130\n"
"v_accvgpr_write_b32 a11, v131\n"
"v_mfma_f32_16x16x32_bf16 v[104:107], a[228:231], a[188:191], v[104:107]\n"
"s_barrier\n"
"v_mfma_f32_16x16x32_bf16 v[100:103], a[200:203], a[184:187], v[100:103]\n"
"s_mov_b32 m0, s51\n"
"ds_read_b64_tr_b16 a[96:97], v244 offset:0\n"
"v_cvt_pk_bf16_f32 v136, v136, v137\n"
"v_mfma_f32_16x16x32_bf16 v[100:103], a[204:207], a[188:191], v[100:103]\n"
"buffer_load_dwordx4 v248, s[24:27], s49 offen lds\n"
"v_mfma_f32_16x16x32_bf16 v[96:99], a[192:195], a[184:187], v[96:99]\n"
"ds_read_b64_tr_b16 a[98:99], v244 offset:512\n"
"v_cvt_pk_bf16_f32 v137, v138, v139\n"
"v_mfma_f32_16x16x32_bf16 v[96:99], a[196:199], a[188:191], v[96:99]\n"
"ds_read_b64_tr_b16 a[100:101], v244 offset:8192\n"
"v_cvt_pk_bf16_f32 v138, v140, v141\n"
"v_mfma_f32_16x16x32_bf16 v[0:3], a[192:195], a[160:163], v[0:3]\n"
"ds_read_b64_tr_b16 a[102:103], v244 offset:8704\n"
"v_cvt_pk_bf16_f32 v139, v142, v143\n"
"v_mfma_f32_16x16x32_bf16 v[0:3], a[196:199], a[164:167], v[0:3]\n"
"ds_read_b64_tr_b16 a[104:105], v245 offset:0\n"
"v_cvt_pk_bf16_f32 v168, v168, v169\n"
"v_mfma_f32_16x16x32_bf16 v[4:7], a[200:203], a[160:163], v[4:7]\n"
"ds_read_b64_tr_b16 a[106:107], v245 offset:512\n"
"v_cvt_pk_bf16_f32 v169, v170, v171\n"
"v_mfma_f32_16x16x32_bf16 v[4:7], a[204:207], a[164:167], v[4:7]\n"
"buffer_load_dwordx4 v249, s[24:27], s49 offen offset:1024 lds\n"
"v_mfma_f32_16x16x32_bf16 v[8:11], a[208:211], a[160:163], v[8:11]\n"
"ds_read_b64_tr_b16 a[108:109], v245 offset:8192\n"
"v_cvt_pk_bf16_f32 v170, v172, v173\n"
"v_mfma_f32_16x16x32_bf16 v[8:11], a[212:215], a[164:167], v[8:11]\n"
"ds_read_b64_tr_b16 a[110:111], v245 offset:8704\n"
"v_cvt_pk_bf16_f32 v171, v174, v175\n"
"v_mfma_f32_16x16x32_bf16 v[12:15], a[216:219], a[160:163], v[12:15]\n"
"ds_read_b64_tr_b16 a[112:113], v246 offset:0\n"
"v_cvt_pk_bf16_f32 v160, v160, v161\n"
"v_mfma_f32_16x16x32_bf16 v[12:15], a[220:223], a[164:167], v[12:15]\n"
"buffer_load_dwordx4 v254, s[24:27], s49 offen offset:2048 lds\n"
"v_mfma_f32_16x16x32_bf16 v[16:19], a[224:227], a[160:163], v[16:19]\n"
"ds_read_b64_tr_b16 a[114:115], v246 offset:512\n"
"v_cvt_pk_bf16_f32 v161, v162, v163\n"
"v_mfma_f32_16x16x32_bf16 v[16:19], a[228:231], a[164:167], v[16:19]\n"
"ds_read_b64_tr_b16 a[116:117], v246 offset:8192\n"
"v_cvt_pk_bf16_f32 v162, v164, v165\n"
"v_mfma_f32_16x16x32_bf16 v[20:23], a[232:235], a[160:163], v[20:23]\n"
"ds_read_b64_tr_b16 a[118:119], v246 offset:8704\n"
"v_cvt_pk_bf16_f32 v163, v166, v167\n"
"v_mfma_f32_16x16x32_bf16 v[20:23], a[236:239], a[164:167], v[20:23]\n"
"ds_read_b64_tr_b16 a[120:121], v247 offset:0\n"
"v_cvt_pk_bf16_f32 v152, v152, v153\n"
"v_mfma_f32_16x16x32_bf16 v[24:27], a[240:243], a[160:163], v[24:27]\n"
"ds_read_b64_tr_b16 a[122:123], v247 offset:512\n"
"v_cvt_pk_bf16_f32 v153, v154, v155\n"
"v_mfma_f32_16x16x32_bf16 v[24:27], a[244:247], a[164:167], v[24:27]\n"
"buffer_load_dwordx4 v255, s[24:27], s49 offen offset:3072 lds\n"
"v_mfma_f32_16x16x32_bf16 v[28:31], a[248:251], a[160:163], v[28:31]\n"
"ds_read_b128 a[160:163], v250 offset:0\n"
"v_cvt_pk_bf16_f32 v154, v156, v157\n"
"v_mfma_f32_16x16x32_bf16 v[28:31], a[252:255], a[164:167], v[28:31]\n"
"ds_read_b128 a[164:167], v251 offset:0\n"
"v_cvt_pk_bf16_f32 v155, v158, v159\n"
"v_mfma_f32_16x16x32_bf16 v[60:63], a[248:251], a[168:171], v[60:63]\n"
"ds_read_b64_tr_b16 a[124:125], v247 offset:8192\n"
"v_cvt_pk_bf16_f32 v144, v144, v145\n"
"v_mfma_f32_16x16x32_bf16 v[60:63], a[252:255], a[172:175], v[60:63]\n"
"s_add_u32 s40, s49, 40960\n"
"ds_read_b64_tr_b16 a[126:127], v247 offset:8704\n"
"v_cvt_pk_bf16_f32 v145, v146, v147\n"
"v_mfma_f32_16x16x32_bf16 v[56:59], a[240:243], a[168:171], v[56:59]\n"
"s_add_u32 m0, s51, 4096\n"
"ds_read_b64_tr_b16 a[128:129], v244 offset:1024\n"
"v_cvt_pk_bf16_f32 v146, v148, v149\n"
"v_mfma_f32_16x16x32_bf16 v[56:59], a[244:247], a[172:175], v[56:59]\n"
"buffer_load_dwordx4 v248, s[24:27], s40 offen lds\n"
"v_mfma_f32_16x16x32_bf16 v[52:55], a[232:235], a[168:171], v[52:55]\n"
"ds_read_b64_tr_b16 a[130:131], v244 offset:1536\n"
"v_cvt_pk_bf16_f32 v147, v150, v151\n"
"v_mfma_f32_16x16x32_bf16 v[52:55], a[236:239], a[172:175], v[52:55]\n"
"ds_read_b64_tr_b16 a[132:133], v244 offset:9216\n"
"v_xor_b32 v252, s74, v252\n"
"v_mfma_f32_16x16x32_bf16 v[48:51], a[224:227], a[168:171], v[48:51]\n"
"ds_read_b64_tr_b16 a[134:135], v244 offset:9728\n"
"v_cvt_pk_bf16_f32 v216, v216, v217\n"
"v_mfma_f32_16x16x32_bf16 v[48:51], a[228:231], a[172:175], v[48:51]\n"
"ds_read_b64_tr_b16 a[136:137], v245 offset:1024\n"
"v_cvt_pk_bf16_f32 v217, v218, v219\n"
"v_mfma_f32_16x16x32_bf16 v[44:47], a[216:219], a[168:171], v[44:47]\n"
"ds_read_b64_tr_b16 a[138:139], v245 offset:1536\n"
"v_cvt_pk_bf16_f32 v218, v220, v221\n"
"v_mfma_f32_16x16x32_bf16 v[44:47], a[220:223], a[172:175], v[44:47]\n"
"buffer_load_dwordx4 v249, s[24:27], s40 offen offset:1024 lds\n"
"v_mfma_f32_16x16x32_bf16 v[40:43], a[208:211], a[168:171], v[40:43]\n"
"ds_read_b64_tr_b16 a[140:141], v245 offset:9216\n"
"v_cvt_pk_bf16_f32 v219, v222, v223\n"
"v_mfma_f32_16x16x32_bf16 v[40:43], a[212:215], a[172:175], v[40:43]\n"
"ds_read_b64_tr_b16 a[142:143], v245 offset:9728\n"
"v_xor_b32 v253, s74, v253\n"
"v_mfma_f32_16x16x32_bf16 v[36:39], a[200:203], a[168:171], v[36:39]\n"
"ds_read_b64_tr_b16 a[144:145], v246 offset:1024\n"
"v_cvt_pk_bf16_f32 v208, v208, v209\n"
"v_mfma_f32_16x16x32_bf16 v[36:39], a[204:207], a[172:175], v[36:39]\n"
"buffer_load_dwordx4 v254, s[24:27], s40 offen offset:2048 lds\n"
"v_mfma_f32_16x16x32_bf16 v[32:35], a[192:195], a[168:171], v[32:35]\n"
"s_add_u32 s49, s49, 128\n"
"ds_read_b64_tr_b16 a[146:147], v246 offset:1536\n"
"v_cvt_pk_bf16_f32 v209, v210, v211\n"
"v_mfma_f32_16x16x32_bf16 v[32:35], a[196:199], a[172:175], v[32:35]\n"
"s_sub_u32 s41, s49, 1280\n"
"ds_read_b64_tr_b16 a[148:149], v246 offset:9216\n"
"v_cvt_pk_bf16_f32 v210, v212, v213\n"
"v_mfma_f32_16x16x32_bf16 v[64:67], a[192:195], a[176:179], v[64:67]\n"
"s_min_u32 s49, s49, s41\n"
"ds_read_b64_tr_b16 a[150:151], v246 offset:9728\n"
"v_cvt_pk_bf16_f32 v211, v214, v215\n"
"v_mfma_f32_16x16x32_bf16 v[64:67], a[196:199], a[180:183], v[64:67]\n"
"s_xor_b32 s51, s51, s74\n"
"v_permlane16_swap_b32 v136, v138\n"
"v_mfma_f32_16x16x32_bf16 v[68:71], a[200:203], a[176:179], v[68:71]\n"
"s_mov_b32 m0, 139264\n"
"ds_read_b64_tr_b16 a[152:153], v247 offset:1024\n"
"v_accvgpr_write_b32 a12, v136\n"
"v_mfma_f32_16x16x32_bf16 v[68:71], a[204:207], a[180:183], v[68:71]\n"
"buffer_load_dword v241, s[80:83], s63 offen lds\n"
"v_mfma_f32_16x16x32_bf16 v[72:75], a[208:211], a[176:179], v[72:75]\n"
"ds_read_b64_tr_b16 a[154:155], v247 offset:1536\n"
"v_accvgpr_write_b32 a14, v138\n"
"v_mfma_f32_16x16x32_bf16 v[72:75], a[212:215], a[180:183], v[72:75]\n"
"v_permlane16_swap_b32 v137, v139\n"
"v_mfma_f32_16x16x32_bf16 v[76:79], a[216:219], a[176:179], v[76:79]\n"
"ds_read_b64_tr_b16 a[156:157], v247 offset:9216\n"
"v_accvgpr_write_b32 a13, v137\n"
"v_mfma_f32_16x16x32_bf16 v[76:79], a[220:223], a[180:183], v[76:79]\n"
"ds_read_b64_tr_b16 a[158:159], v247 offset:9728\n"
"v_accvgpr_write_b32 a15, v139\n"
"v_mfma_f32_16x16x32_bf16 v[88:91], a[240:243], a[176:179], v[88:91]\n"
"v_permlane16_swap_b32 v168, v170\n"
"v_mfma_f32_16x16x32_bf16 v[88:91], a[244:247], a[180:183], v[88:91]\n"
"buffer_load_dword v241, s[80:83], s63 offen offset:80 lds\n"
"v_mfma_f32_16x16x32_bf16 v[92:95], a[248:251], a[176:179], v[92:95]\n"
"ds_read_b128 a[168:171], v250 offset:2048\n"
"v_accvgpr_write_b32 a28, v168\n"
"v_mfma_f32_16x16x32_bf16 v[92:95], a[252:255], a[180:183], v[92:95]\n"
"ds_read_b128 a[172:175], v251 offset:2048\n"
"v_accvgpr_write_b32 a30, v170\n"
"v_mfma_f32_16x16x32_bf16 v[80:83], a[224:227], a[176:179], v[80:83]\n"
"v_permlane16_swap_b32 v169, v171\n"
"v_mfma_f32_16x16x32_bf16 v[80:83], a[228:231], a[180:183], v[80:83]\n"
"ds_read_b128 a[184:187], v252 offset:0\n"
"v_accvgpr_write_b32 a29, v169\n"
"v_mfma_f32_16x16x32_bf16 v[84:87], a[232:235], a[176:179], v[84:87]\n"
"s_add_u32 s47, s47, 1\n"
"ds_read_b128 a[176:179], v250 offset:4096\n"
"v_accvgpr_write_b32 a31, v171\n"
"v_mfma_f32_16x16x32_bf16 v[84:87], a[236:239], a[180:183], v[84:87]\n"
"s_waitcnt lgkmcnt(4)\n"
"v_permlane16_swap_b32 v160, v162\n"
"v_mfma_f32_16x16x32_bf16 v[112:115], a[96:99], a[160:163], v[228:231]\n"
"ds_read_b128 a[180:183], v251 offset:4096\n"
"v_accvgpr_write_b32 a24, v160\n"
"v_mfma_f32_16x16x32_bf16 v[112:115], a[100:103], a[164:167], v[112:115]\n"
"ds_read_b128 a[188:191], v253 offset:0\n"
"v_accvgpr_write_b32 a26, v162\n"
"v_mfma_f32_16x16x32_bf16 v[116:119], a[104:107], a[160:163], v[228:231]\n"
"s_waitcnt vmcnt(13)\n"
"v_permlane16_swap_b32 v161, v163\n"
"v_mfma_f32_16x16x32_bf16 v[116:119], a[108:111], a[164:167], v[116:119]\n"
"s_barrier\n"
"v_mfma_f32_16x16x32_bf16 v[120:123], a[112:115], a[160:163], v[228:231]\n"
"s_add_u32 s40, s48, 256\n"
"s_xor_b32 m0, s50, 0xc000\n"
"ds_read_b64_tr_b16 a[192:193], v244 offset:16384\n"
"v_mfma_f32_16x16x32_bf16 v[120:123], a[116:119], a[164:167], v[120:123]\n"
"buffer_load_dwordx4 v224, s[28:31], s40 offen lds\n"
"v_mfma_f32_16x16x32_bf16 v[124:127], a[120:123], a[160:163], v[228:231]\n"
"ds_read_b64_tr_b16 a[194:195], v244 offset:16896\n"
"v_accvgpr_write_b32 a25, v161\n"
"v_mfma_f32_16x16x32_bf16 v[124:127], a[124:127], a[164:167], v[124:127]\n"
"ds_read_b64_tr_b16 a[196:197], v244 offset:24576\n"
"v_accvgpr_write_b32 a27, v163\n"
"v_mfma_f32_16x16x32_bf16 v[128:131], a[128:131], a[160:163], v[228:231]\n"
"v_permlane16_swap_b32 v152, v154\n"
"v_mfma_f32_16x16x32_bf16 v[128:131], a[132:135], a[164:167], v[128:131]\n"
"ds_read_b64_tr_b16 a[198:199], v244 offset:25088\n"
"v_accvgpr_write_b32 a20, v152\n"
"v_mfma_f32_16x16x32_bf16 v[132:135], a[136:139], a[160:163], v[228:231]\n"
"ds_read_b64_tr_b16 a[200:201], v245 offset:16384\n"
"v_accvgpr_write_b32 a22, v154\n"
"v_mfma_f32_16x16x32_bf16 v[132:135], a[140:143], a[164:167], v[132:135]\n"
"buffer_load_dwordx4 v225, s[28:31], s40 offen offset:1024 lds\n"
"v_mfma_f32_16x16x32_bf16 v[136:139], a[144:147], a[160:163], v[228:231]\n"
"v_permlane16_swap_b32 v153, v155\n"
"v_mfma_f32_16x16x32_bf16 v[136:139], a[148:151], a[164:167], v[136:139]\n"
"ds_read_b64_tr_b16 a[202:203], v245 offset:16896\n"
"v_accvgpr_write_b32 a21, v153\n"
"v_mfma_f32_16x16x32_bf16 v[140:143], a[152:155], a[160:163], v[228:231]\n"
"ds_read_b64_tr_b16 a[204:205], v245 offset:24576\n"
"v_accvgpr_write_b32 a23, v155\n"
"v_mfma_f32_16x16x32_bf16 v[140:143], a[156:159], a[164:167], v[140:143]\n"
"s_waitcnt lgkmcnt(4)\n"
"v_permlane16_swap_b32 v144, v146\n"
"v_mfma_f32_16x16x32_bf16 v[172:175], a[152:155], a[168:171], v[228:231]\n"
"ds_read_b64_tr_b16 a[206:207], v245 offset:25088\n"
"v_accvgpr_write_b32 a16, v144\n"
"v_mfma_f32_16x16x32_bf16 v[172:175], a[156:159], a[172:175], v[172:175]\n"
"buffer_load_dwordx4 v226, s[28:31], s40 offen offset:2048 lds\n"
"v_mfma_f32_16x16x32_bf16 v[168:171], a[144:147], a[168:171], v[228:231]\n"
"ds_read_b64_tr_b16 a[208:209], v246 offset:16384\n"
"v_accvgpr_write_b32 a18, v146\n"
"v_mfma_f32_16x16x32_bf16 v[168:171], a[148:151], a[172:175], v[168:171]\n"
"v_permlane16_swap_b32 v145, v147\n"
"v_mfma_f32_16x16x32_bf16 v[164:167], a[136:139], a[168:171], v[228:231]\n"
"ds_read_b64_tr_b16 a[210:211], v246 offset:16896\n"
"v_accvgpr_write_b32 a17, v145\n"
"v_mfma_f32_16x16x32_bf16 v[164:167], a[140:143], a[172:175], v[164:167]\n"
"ds_read_b64_tr_b16 a[212:213], v246 offset:24576\n"
"v_accvgpr_write_b32 a19, v147\n"
"v_mfma_f32_16x16x32_bf16 v[160:163], a[128:131], a[168:171], v[228:231]\n"
"v_pk_fma_f32 v[176:177], v[176:177], s[70:71], s[70:71] op_sel:[0,0,1] op_sel_hi:[1,0,1]\nv_pk_fma_f32 v[178:179], v[178:179], s[70:71], s[70:71] op_sel:[0,0,1] op_sel_hi:[1,0,1]\nv_pk_fma_f32 v[180:181], v[180:181], s[70:71], s[70:71] op_sel:[0,0,1] op_sel_hi:[1,0,1]\nv_pk_fma_f32 v[182:183], v[182:183], s[70:71], s[70:71] op_sel:[0,0,1] op_sel_hi:[1,0,1]\nv_pk_fma_f32 v[184:185], v[184:185], s[70:71], s[70:71] op_sel:[0,0,1] op_sel_hi:[1,0,1]\nv_pk_fma_f32 v[186:187], v[186:187], s[70:71], s[70:71] op_sel:[0,0,1] op_sel_hi:[1,0,1]\nv_pk_fma_f32 v[188:189], v[188:189], s[70:71], s[70:71] op_sel:[0,0,1] op_sel_hi:[1,0,1]\nv_pk_fma_f32 v[190:191], v[190:191], s[70:71], s[70:71] op_sel:[0,0,1] op_sel_hi:[1,0,1]\nv_pk_fma_f32 v[192:193], v[192:193], s[70:71], s[70:71] op_sel:[0,0,1] op_sel_hi:[1,0,1]\nv_pk_fma_f32 v[194:195], v[194:195], s[70:71], s[70:71] op_sel:[0,0,1] op_sel_hi:[1,0,1]\nv_pk_fma_f32 v[196:197], v[196:197], s[70:71], s[70:71] op_sel:[0,0,1] op_sel_hi:[1,0,1]\nv_pk_fma_f32 v[198:199], v[198:199], s[70:71], s[70:71] op_sel:[0,0,1] op_sel_hi:[1,0,1]\nv_pk_fma_f32 v[200:201], v[200:201], s[70:71], s[70:71] op_sel:[0,0,1] op_sel_hi:[1,0,1]\nv_pk_fma_f32 v[202:203], v[202:203], s[70:71], s[70:71] op_sel:[0,0,1] op_sel_hi:[1,0,1]\nv_pk_fma_f32 v[204:205], v[204:205], s[70:71], s[70:71] op_sel:[0,0,1] op_sel_hi:[1,0,1]\nv_pk_fma_f32 v[206:207], v[206:207], s[70:71], s[70:71] op_sel:[0,0,1] op_sel_hi:[1,0,1]\n"
"v_mfma_f32_16x16x32_bf16 v[160:163], a[132:135], a[172:175], v[160:163]\n"
"buffer_load_dwordx4 v227, s[28:31], s40 offen offset:3072 lds\n"
"v_mfma_f32_16x16x32_bf16 v[156:159], a[120:123], a[168:171], v[228:231]\n"
"s_cmp_eq_u32 s47, 8\n"
"ds_read_b64_tr_b16 a[214:215], v246 offset:25088\n"
"v_cvt_pk_bf16_f32 v176, v176, v177\n"
"v_mfma_f32_16x16x32_bf16 v[156:159], a[124:127], a[172:175], v[156:159]\n"
"s_cselect_b32 s41, s52, s58\n"
"ds_read_b64_tr_b16 a[216:217], v247 offset:16384\n"
"v_cvt_pk_bf16_f32 v177, v178, v179\n"
"v_mfma_f32_16x16x32_bf16 v[152:155], a[112:115], a[168:171], v[228:231]\n"
"s_add_u32 s48, s48, s41\n"
"ds_read_b64_tr_b16 a[218:219], v247 offset:16896\n"
"v_cvt_pk_bf16_f32 v178, v180, v181\n"
"v_mfma_f32_16x16x32_bf16 v[152:155], a[116:119], a[172:175], v[152:155]\n"
"s_sub_u32 s41, s48, 5242880\n"
"ds_read_b64_tr_b16 a[220:221], v247 offset:24576\n"
"v_cvt_pk_bf16_f32 v179, v182, v183\n"
"v_mfma_f32_16x16x32_bf16 v[148:151], a[104:107], a[168:171], v[228:231]\n"
"s_min_u32 s48, s48, s41\n"
"v_permlane16_swap_b32 v176, v178\n"
"v_mfma_f32_16x16x32_bf16 v[148:151], a[108:111], a[172:175], v[148:151]\n"
"v_permlane16_swap_b32 v177, v179\n"
"v_mfma_f32_16x16x32_bf16 v[144:147], a[96:99], a[168:171], v[228:231]\n"
"s_mov_b32 m0, s50\n"
"v_accvgpr_write_b32 a32, v176\n"
"v_accvgpr_write_b32 a33, v177\n"
"v_mfma_f32_16x16x32_bf16 v[144:147], a[100:103], a[172:175], v[144:147]\n"
"buffer_load_dwordx4 v224, s[28:31], s48 offen lds\n"
"v_accvgpr_write_b32 a34, v178\n"
"v_accvgpr_write_b32 a35, v179\n"
"v_mfma_f32_16x16x32_bf16 v[176:179], a[96:99], a[176:179], v[228:231]\n"
"v_permlane16_swap_b32 v216, v218\n"
"v_permlane16_swap_b32 v217, v219\n"
"v_mfma_f32_16x16x32_bf16 v[176:179], a[100:103], a[180:183], v[176:179]\n"
"ds_write_b128 v243, v[216:219] offset:2048\n"
"v_mfma_f32_16x16x32_bf16 v[180:183], a[104:107], a[176:179], v[228:231]\n"
"v_cvt_pk_bf16_f32 v184, v184, v185\n"
"v_cvt_pk_bf16_f32 v185, v186, v187\n"
"v_cvt_pk_bf16_f32 v186, v188, v189\n"
"v_cvt_pk_bf16_f32 v187, v190, v191\n"
"v_mfma_f32_16x16x32_bf16 v[180:183], a[108:111], a[180:183], v[180:183]\n"
"buffer_load_dwordx4 v225, s[28:31], s48 offen offset:1024 lds\n"
"v_permlane16_swap_b32 v184, v186\n"
"v_permlane16_swap_b32 v185, v187\n"
"v_accvgpr_write_b32 a36, v184\n"
"v_accvgpr_write_b32 a37, v185\n"
"v_accvgpr_write_b32 a38, v186\n"
"v_accvgpr_write_b32 a39, v187\n"
"v_mfma_f32_16x16x32_bf16 v[184:187], a[112:115], a[176:179], v[228:231]\n"
"v_permlane16_swap_b32 v208, v210\n"
"v_mfma_f32_16x16x32_bf16 v[184:187], a[116:119], a[180:183], v[184:187]\n"
"v_permlane16_swap_b32 v209, v211\n"
"v_mfma_f32_16x16x32_bf16 v[188:191], a[120:123], a[176:179], v[228:231]\n"
"ds_write_b128 v243, v[208:211] offset:0\n"
"v_cvt_pk_bf16_f32 v200, v200, v201\n"
"v_cvt_pk_bf16_f32 v201, v202, v203\n"
"v_cvt_pk_bf16_f32 v202, v204, v205\n"
"v_cvt_pk_bf16_f32 v203, v206, v207\n"
"v_mfma_f32_16x16x32_bf16 v[188:191], a[124:127], a[180:183], v[188:191]\n"
"v_permlane16_swap_b32 v200, v202\n"
"v_permlane16_swap_b32 v201, v203\n"
"v_accvgpr_write_b32 a44, v200\n"
"v_accvgpr_write_b32 a45, v201\n"
"v_accvgpr_write_b32 a46, v202\n"
"v_accvgpr_write_b32 a47, v203\n"
"v_mfma_f32_16x16x32_bf16 v[200:203], a[144:147], a[176:179], v[228:231]\n"
"ds_read_b64_tr_b16 a[222:223], v247 offset:25088\n"
"v_cvt_pk_bf16_f32 v192, v192, v193\n"
"v_mfma_f32_16x16x32_bf16 v[200:203], a[148:151], a[180:183], v[200:203]\n"
"buffer_load_dwordx4 v226, s[28:31], s48 offen offset:2048 lds\n"
"v_mfma_f32_16x16x32_bf16 v[204:207], a[152:155], a[176:179], v[228:231]\n"
"ds_read_b64_tr_b16 a[224:225], v244 offset:17408\n"
"v_cvt_pk_bf16_f32 v193, v194, v195\n"
"v_cvt_pk_bf16_f32 v194, v196, v197\n"
"v_cvt_pk_bf16_f32 v195, v198, v199\n"
"v_mfma_f32_16x16x32_bf16 v[204:207], a[156:159], a[180:183], v[204:207]\n"
"v_permlane16_swap_b32 v192, v194\n"
"v_permlane16_swap_b32 v193, v195\n"
"v_accvgpr_write_b32 a40, v192\n"
"v_accvgpr_write_b32 a41, v193\n"
"v_accvgpr_write_b32 a42, v194\n"
"v_accvgpr_write_b32 a43, v195\n"
"v_mfma_f32_16x16x32_bf16 v[192:195], a[128:131], a[176:179], v[228:231]\n"
"ds_read_b64_tr_b16 a[226:227], v244 offset:17920\n"
"v_xor_b32 v250, s74, v250\n"
"v_mfma_f32_16x16x32_bf16 v[192:195], a[132:135], a[180:183], v[192:195]\n"
"ds_read_b64_tr_b16 a[228:229], v244 offset:25600\n"
"v_xor_b32 v251, s74, v251\n"
"v_mfma_f32_16x16x32_bf16 v[196:199], a[136:139], a[176:179], v[228:231]\n"
"ds_read_b64_tr_b16 a[230:231], v244 offset:26112\n"
"v_xor_b32 v244, s74, v244\n"
"v_mfma_f32_16x16x32_bf16 v[196:199], a[140:143], a[180:183], v[196:199]\n"
"buffer_load_dwordx4 v227, s[28:31], s48 offen offset:3072 lds\n"
"v_mfma_f32_16x16x32_bf16 v[220:223], a[136:139], a[184:187], v[228:231]\n"
"s_xor_b32 s50, s50, s74\n"
"ds_read_b64_tr_b16 a[232:233], v245 offset:17408\n"
"ds_read_b64_tr_b16 a[234:235], v245 offset:17920\n"
"v_mfma_f32_16x16x32_bf16 v[220:223], a[140:143], a[188:191], v[220:223]\n"
"ds_read_b64_tr_b16 a[236:237], v245 offset:25600\n"
"ds_read_b64_tr_b16 a[238:239], v245 offset:26112\n"
"v_mfma_f32_16x16x32_bf16 v[216:219], a[128:131], a[184:187], v[228:231]\n"
"ds_read_b64_tr_b16 a[240:241], v246 offset:17408\n"
"ds_read_b64_tr_b16 a[242:243], v246 offset:17920\n"
"v_mfma_f32_16x16x32_bf16 v[216:219], a[132:135], a[188:191], v[216:219]\n"
"ds_read_b64_tr_b16 a[244:245], v246 offset:25600\n"
"ds_read_b64_tr_b16 a[246:247], v246 offset:26112\n"
"v_mfma_f32_16x16x32_bf16 v[212:215], a[104:107], a[184:187], v[228:231]\n"
"ds_read_b64_tr_b16 a[248:249], v247 offset:17408\n"
"ds_read_b64_tr_b16 a[250:251], v247 offset:17920\n"
"v_mfma_f32_16x16x32_bf16 v[212:215], a[108:111], a[188:191], v[212:215]\n"
"ds_read_b64_tr_b16 a[252:253], v247 offset:25600\n"
"ds_read_b64_tr_b16 a[254:255], v247 offset:26112\n"
"v_mfma_f32_16x16x32_bf16 v[208:211], a[96:99], a[184:187], v[228:231]\n"
"v_pk_fma_f32 v[0:1], v[0:1], s[70:71], s[70:71] op_sel:[0,0,1] op_sel_hi:[1,0,1]\nv_pk_fma_f32 v[2:3], v[2:3], s[70:71], s[70:71] op_sel:[0,0,1] op_sel_hi:[1,0,1]\nv_pk_fma_f32 v[4:5], v[4:5], s[70:71], s[70:71] op_sel:[0,0,1] op_sel_hi:[1,0,1]\nv_pk_fma_f32 v[6:7], v[6:7], s[70:71], s[70:71] op_sel:[0,0,1] op_sel_hi:[1,0,1]\nv_pk_fma_f32 v[8:9], v[8:9], s[70:71], s[70:71] op_sel:[0,0,1] op_sel_hi:[1,0,1]\nv_pk_fma_f32 v[10:11], v[10:11], s[70:71], s[70:71] op_sel:[0,0,1] op_sel_hi:[1,0,1]\nv_pk_fma_f32 v[12:13], v[12:13], s[70:71], s[70:71] op_sel:[0,0,1] op_sel_hi:[1,0,1]\nv_pk_fma_f32 v[14:15], v[14:15], s[70:71], s[70:71] op_sel:[0,0,1] op_sel_hi:[1,0,1]\nv_pk_fma_f32 v[16:17], v[16:17], s[70:71], s[70:71] op_sel:[0,0,1] op_sel_hi:[1,0,1]\nv_pk_fma_f32 v[18:19], v[18:19], s[70:71], s[70:71] op_sel:[0,0,1] op_sel_hi:[1,0,1]\nv_pk_fma_f32 v[20:21], v[20:21], s[70:71], s[70:71] op_sel:[0,0,1] op_sel_hi:[1,0,1]\nv_pk_fma_f32 v[22:23], v[22:23], s[70:71], s[70:71] op_sel:[0,0,1] op_sel_hi:[1,0,1]\nv_pk_fma_f32 v[24:25], v[24:25], s[70:71], s[70:71] op_sel:[0,0,1] op_sel_hi:[1,0,1]\nv_pk_fma_f32 v[26:27], v[26:27], s[70:71], s[70:71] op_sel:[0,0,1] op_sel_hi:[1,0,1]\nv_pk_fma_f32 v[28:29], v[28:29], s[70:71], s[70:71] op_sel:[0,0,1] op_sel_hi:[1,0,1]\nv_pk_fma_f32 v[30:31], v[30:31], s[70:71], s[70:71] op_sel:[0,0,1] op_sel_hi:[1,0,1]\n"
"v_cvt_pk_bf16_f32 v0, v0, v1\n"
"v_cvt_pk_bf16_f32 v1, v2, v3\n"
"v_cvt_pk_bf16_f32 v2, v4, v5\n"
"v_cvt_pk_bf16_f32 v3, v6, v7\n"
"v_mfma_f32_16x16x32_bf16 v[208:211], a[100:103], a[188:191], v[208:211]\n"
"v_permlane16_swap_b32 v0, v2\n"
"v_permlane16_swap_b32 v1, v3\n"
"v_accvgpr_write_b32 a48, v0\n"
"v_accvgpr_write_b32 a49, v1\n"
"v_accvgpr_write_b32 a50, v2\n"
"v_accvgpr_write_b32 a51, v3\n"
"v_mfma_f32_16x16x32_bf16 v[0:3], a[192:195], a[160:163], v[228:231]\n"
"s_waitcnt lgkmcnt(10)\n"
"v_cvt_pk_bf16_f32 v8, v8, v9\n"
"v_cvt_pk_bf16_f32 v9, v10, v11\n"
"v_mfma_f32_16x16x32_bf16 v[0:3], a[196:199], a[164:167], v[0:3]\n"
"v_cvt_pk_bf16_f32 v10, v12, v13\n"
"v_cvt_pk_bf16_f32 v11, v14, v15\n"
"v_mfma_f32_16x16x32_bf16 v[4:7], a[200:203], a[160:163], v[228:231]\n"
"s_waitcnt vmcnt(10) lgkmcnt(0)\n"
"v_permlane16_swap_b32 v8, v10\n"
"v_mfma_f32_16x16x32_bf16 v[4:7], a[204:207], a[164:167], v[4:7]\n"
"s_barrier\n"
"v_permlane16_swap_b32 v9, v11\n"
"v_accvgpr_write_b32 a52, v8\n"
"v_accvgpr_write_b32 a53, v9\n"
"v_accvgpr_write_b32 a54, v10\n"
"v_accvgpr_write_b32 a55, v11\n"
"v_mfma_f32_16x16x32_bf16 v[8:11], a[208:211], a[160:163], v[228:231]\n"
"s_mov_b32 m0, s51\n"
"ds_read_b64_tr_b16 a[96:97], v244 offset:0\n"
"v_cvt_pk_bf16_f32 v16, v16, v17\n"
"v_mfma_f32_16x16x32_bf16 v[8:11], a[212:215], a[164:167], v[8:11]\n"
"buffer_load_dwordx4 v248, s[24:27], s49 offen lds\n"
"v_mfma_f32_16x16x32_bf16 v[12:15], a[216:219], a[160:163], v[228:231]\n"
"ds_read_b64_tr_b16 a[98:99], v244 offset:512\n"
"v_cvt_pk_bf16_f32 v17, v18, v19\n"
"v_cvt_pk_bf16_f32 v18, v20, v21\n"
"v_cvt_pk_bf16_f32 v19, v22, v23\n"
"v_mfma_f32_16x16x32_bf16 v[12:15], a[220:223], a[164:167], v[12:15]\n"
"v_permlane16_swap_b32 v16, v18\n"
"v_permlane16_swap_b32 v17, v19\n"
"v_accvgpr_write_b32 a56, v16\n"
"v_accvgpr_write_b32 a57, v17\n"
"v_accvgpr_write_b32 a58, v18\n"
"v_accvgpr_write_b32 a59, v19\n"
"v_mfma_f32_16x16x32_bf16 v[16:19], a[224:227], a[160:163], v[228:231]\n"
"ds_read_b64_tr_b16 a[100:101], v244 offset:8192\n"
"v_cvt_pk_bf16_f32 v24, v24, v25\n"
"v_mfma_f32_16x16x32_bf16 v[16:19], a[228:231], a[164:167], v[16:19]\n"
"ds_read_b64_tr_b16 a[102:103], v244 offset:8704\n"
"v_cvt_pk_bf16_f32 v25, v26, v27\n"
"v_mfma_f32_16x16x32_bf16 v[20:23], a[232:235], a[160:163], v[228:231]\n"
"ds_read_b64_tr_b16 a[128:129], v244 offset:1024\n"
"v_cvt_pk_bf16_f32 v26, v28, v29\n"
"v_cvt_pk_bf16_f32 v27, v30, v31\n"
"v_mfma_f32_16x16x32_bf16 v[20:23], a[236:239], a[164:167], v[20:23]\n"
"buffer_load_dwordx4 v249, s[24:27], s49 offen offset:1024 lds\n"
"v_permlane16_swap_b32 v24, v26\n"
"v_permlane16_swap_b32 v25, v27\n"
"v_accvgpr_write_b32 a60, v24\n"
"v_accvgpr_write_b32 a61, v25\n"
"v_accvgpr_write_b32 a62, v26\n"
"v_accvgpr_write_b32 a63, v27\n"
"v_mfma_f32_16x16x32_bf16 v[24:27], a[240:243], a[160:163], v[228:231]\n"
"ds_read_b64_tr_b16 a[130:131], v244 offset:1536\n"
"v_xor_b32 v245, s74, v245\n"
"v_mfma_f32_16x16x32_bf16 v[24:27], a[244:247], a[164:167], v[24:27]\n"
"ds_read_b64_tr_b16 a[104:105], v245 offset:0\n"
"v_xor_b32 v246, s74, v246\n"
"v_mfma_f32_16x16x32_bf16 v[28:31], a[248:251], a[160:163], v[228:231]\n"
"v_pk_fma_f32 v[32:33], v[32:33], s[70:71], s[70:71] op_sel:[0,0,1] op_sel_hi:[1,0,1]\nv_pk_fma_f32 v[34:35], v[34:35], s[70:71], s[70:71] op_sel:[0,0,1] op_sel_hi:[1,0,1]\nv_pk_fma_f32 v[36:37], v[36:37], s[70:71], s[70:71] op_sel:[0,0,1] op_sel_hi:[1,0,1]\nv_pk_fma_f32 v[38:39], v[38:39], s[70:71], s[70:71] op_sel:[0,0,1] op_sel_hi:[1,0,1]\nv_pk_fma_f32 v[40:41], v[40:41], s[70:71], s[70:71] op_sel:[0,0,1] op_sel_hi:[1,0,1]\nv_pk_fma_f32 v[42:43], v[42:43], s[70:71], s[70:71] op_sel:[0,0,1] op_sel_hi:[1,0,1]\nv_pk_fma_f32 v[44:45], v[44:45], s[70:71], s[70:71] op_sel:[0,0,1] op_sel_hi:[1,0,1]\nv_pk_fma_f32 v[46:47], v[46:47], s[70:71], s[70:71] op_sel:[0,0,1] op_sel_hi:[1,0,1]\nv_pk_fma_f32 v[48:49], v[48:49], s[70:71], s[70:71] op_sel:[0,0,1] op_sel_hi:[1,0,1]\nv_pk_fma_f32 v[50:51], v[50:51], s[70:71], s[70:71] op_sel:[0,0,1] op_sel_hi:[1,0,1]\nv_pk_fma_f32 v[52:53], v[52:53], s[70:71], s[70:71] op_sel:[0,0,1] op_sel_hi:[1,0,1]\nv_pk_fma_f32 v[54:55], v[54:55], s[70:71], s[70:71] op_sel:[0,0,1] op_sel_hi:[1,0,1]\nv_pk_fma_f32 v[56:57], v[56:57], s[70:71], s[70:71] op_sel:[0,0,1] op_sel_hi:[1,0,1]\nv_pk_fma_f32 v[58:59], v[58:59], s[70:71], s[70:71] op_sel:[0,0,1] op_sel_hi:[1,0,1]\nv_pk_fma_f32 v[60:61], v[60:61], s[70:71], s[70:71] op_sel:[0,0,1] op_sel_hi:[1,0,1]\nv_pk_fma_f32 v[62:63], v[62:63], s[70:71], s[70:71] op_sel:[0,0,1] op_sel_hi:[1,0,1]\n"
"v_mfma_f32_16x16x32_bf16 v[28:31], a[252:255], a[164:167], v[28:31]\n"
"ds_read_b128 a[160:163], v250 offset:0\n"
"v_cvt_pk_bf16_f32 v56, v56, v57\n"
"v_cvt_pk_bf16_f32 v57, v58, v59\n"
"v_cvt_pk_bf16_f32 v58, v60, v61\n"
"v_cvt_pk_bf16_f32 v59, v62, v63\n"
"v_mfma_f32_16x16x32_bf16 v[60:63], a[248:251], a[168:171], v[228:231]\n"
"v_permlane16_swap_b32 v56, v58\n"
"v_mfma_f32_16x16x32_bf16 v[60:63], a[252:255], a[172:175], v[60:63]\n"
"buffer_load_dwordx4 v254, s[24:27], s49 offen offset:2048 lds\n"
"v_permlane16_swap_b32 v57, v59\n"
"v_accvgpr_write_b32 a76, v56\n"
"v_accvgpr_write_b32 a77, v57\n"
"v_accvgpr_write_b32 a78, v58\n"
"v_accvgpr_write_b32 a79, v59\n"
"v_mfma_f32_16x16x32_bf16 v[56:59], a[240:243], a[168:171], v[228:231]\n"
"ds_read_b128 a[164:167], v251 offset:0\n"
"v_cvt_pk_bf16_f32 v48, v48, v49\n"
"v_mfma_f32_16x16x32_bf16 v[56:59], a[244:247], a[172:175], v[56:59]\n"
"ds_read_b64_tr_b16 a[106:107], v245 offset:512\n"
"v_cvt_pk_bf16_f32 v49, v50, v51\n"
"v_cvt_pk_bf16_f32 v50, v52, v53\n"
"v_cvt_pk_bf16_f32 v51, v54, v55\n"
"v_mfma_f32_16x16x32_bf16 v[52:55], a[232:235], a[168:171], v[228:231]\n"
"v_permlane16_swap_b32 v48, v50\n"
"v_mfma_f32_16x16x32_bf16 v[52:55], a[236:239], a[172:175], v[52:55]\n"
"v_permlane16_swap_b32 v49, v51\n"
"v_accvgpr_write_b32 a72, v48\n"
"v_accvgpr_write_b32 a73, v49\n"
"v_accvgpr_write_b32 a74, v50\n"
"v_accvgpr_write_b32 a75, v51\n"
"v_mfma_f32_16x16x32_bf16 v[48:51], a[224:227], a[168:171], v[228:231]\n"
"ds_read_b64_tr_b16 a[108:109], v245 offset:8192\n"
"v_cvt_pk_bf16_f32 v40, v40, v41\n"
"v_mfma_f32_16x16x32_bf16 v[48:51], a[228:231], a[172:175], v[48:51]\n"
"buffer_load_dwordx4 v255, s[24:27], s49 offen offset:3072 lds\n"
"v_cvt_pk_bf16_f32 v41, v42, v43\n"
"v_cvt_pk_bf16_f32 v42, v44, v45\n"
"v_cvt_pk_bf16_f32 v43, v46, v47\n"
"v_mfma_f32_16x16x32_bf16 v[44:47], a[216:219], a[168:171], v[228:231]\n"
"v_pk_fma_f32 v[96:97], v[96:97], s[70:71], s[70:71] op_sel:[0,0,1] op_sel_hi:[1,0,1]\nv_pk_fma_f32 v[98:99], v[98:99], s[70:71], s[70:71] op_sel:[0,0,1] op_sel_hi:[1,0,1]\nv_pk_fma_f32 v[100:101], v[100:101], s[70:71], s[70:71] op_sel:[0,0,1] op_sel_hi:[1,0,1]\nv_pk_fma_f32 v[102:103], v[102:103], s[70:71], s[70:71] op_sel:[0,0,1] op_sel_hi:[1,0,1]\nv_pk_fma_f32 v[104:105], v[104:105], s[70:71], s[70:71] op_sel:[0,0,1] op_sel_hi:[1,0,1]\nv_pk_fma_f32 v[106:107], v[106:107], s[70:71], s[70:71] op_sel:[0,0,1] op_sel_hi:[1,0,1]\nv_pk_fma_f32 v[108:109], v[108:109], s[70:71], s[70:71] op_sel:[0,0,1] op_sel_hi:[1,0,1]\nv_pk_fma_f32 v[110:111], v[110:111], s[70:71], s[70:71] op_sel:[0,0,1] op_sel_hi:[1,0,1]\n"
"v_mfma_f32_16x16x32_bf16 v[44:47], a[220:223], a[172:175], v[44:47]\n"
"v_cvt_pk_bf16_f32 v104, v104, v105\n"
"v_cvt_pk_bf16_f32 v105, v106, v107\n"
"v_cvt_pk_bf16_f32 v106, v108, v109\n"
"v_cvt_pk_bf16_f32 v107, v110, v111\n"
"v_permlane16_swap_b32 v40, v42\n"
"v_permlane16_swap_b32 v41, v43\n"
"v_accvgpr_write_b32 a68, v40\n"
"v_accvgpr_write_b32 a69, v41\n"
"v_accvgpr_write_b32 a70, v42\n"
"v_accvgpr_write_b32 a71, v43\n"
"v_mfma_f32_16x16x32_bf16 v[40:43], a[208:211], a[168:171], v[228:231]\n"
"v_permlane16_swap_b32 v104, v106\n"
"v_permlane16_swap_b32 v105, v107\n"
"v_mfma_f32_16x16x32_bf16 v[40:43], a[212:215], a[172:175], v[40:43]\n"
"s_add_u32 s40, s49, 40960\n"
"ds_write_b128 v243, v[104:107] offset:6144\n"
"v_cvt_pk_bf16_f32 v32, v32, v33\n"
"v_cvt_pk_bf16_f32 v33, v34, v35\n"
"v_cvt_pk_bf16_f32 v34, v36, v37\n"
"v_cvt_pk_bf16_f32 v35, v38, v39\n"
"v_mfma_f32_16x16x32_bf16 v[36:39], a[200:203], a[168:171], v[228:231]\n"
"s_add_u32 m0, s51, 4096\n"
"v_permlane16_swap_b32 v32, v34\n"
"v_mfma_f32_16x16x32_bf16 v[36:39], a[204:207], a[172:175], v[36:39]\n"
"buffer_load_dwordx4 v248, s[24:27], s40 offen lds\n"
"v_permlane16_swap_b32 v33, v35\n"
"v_accvgpr_write_b32 a64, v32\n"
"v_accvgpr_write_b32 a65, v33\n"
"v_accvgpr_write_b32 a66, v34\n"
"v_accvgpr_write_b32 a67, v35\n"
"v_mfma_f32_16x16x32_bf16 v[32:35], a[192:195], a[168:171], v[228:231]\n"
"v_pk_fma_f32 v[64:65], v[64:65], s[70:71], s[70:71] op_sel:[0,0,1] op_sel_hi:[1,0,1]\nv_pk_fma_f32 v[66:67], v[66:67], s[70:71], s[70:71] op_sel:[0,0,1] op_sel_hi:[1,0,1]\nv_pk_fma_f32 v[68:69], v[68:69], s[70:71], s[70:71] op_sel:[0,0,1] op_sel_hi:[1,0,1]\nv_pk_fma_f32 v[70:71], v[70:71], s[70:71], s[70:71] op_sel:[0,0,1] op_sel_hi:[1,0,1]\nv_pk_fma_f32 v[72:73], v[72:73], s[70:71], s[70:71] op_sel:[0,0,1] op_sel_hi:[1,0,1]\nv_pk_fma_f32 v[74:75], v[74:75], s[70:71], s[70:71] op_sel:[0,0,1] op_sel_hi:[1,0,1]\nv_pk_fma_f32 v[76:77], v[76:77], s[70:71], s[70:71] op_sel:[0,0,1] op_sel_hi:[1,0,1]\nv_pk_fma_f32 v[78:79], v[78:79], s[70:71], s[70:71] op_sel:[0,0,1] op_sel_hi:[1,0,1]\nv_pk_fma_f32 v[80:81], v[80:81], s[70:71], s[70:71] op_sel:[0,0,1] op_sel_hi:[1,0,1]\nv_pk_fma_f32 v[82:83], v[82:83], s[70:71], s[70:71] op_sel:[0,0,1] op_sel_hi:[1,0,1]\nv_pk_fma_f32 v[84:85], v[84:85], s[70:71], s[70:71] op_sel:[0,0,1] op_sel_hi:[1,0,1]\nv_pk_fma_f32 v[86:87], v[86:87], s[70:71], s[70:71] op_sel:[0,0,1] op_sel_hi:[1,0,1]\nv_pk_fma_f32 v[88:89], v[88:89], s[70:71], s[70:71] op_sel:[0,0,1] op_sel_hi:[1,0,1]\nv_pk_fma_f32 v[90:91], v[90:91], s[70:71], s[70:71] op_sel:[0,0,1] op_sel_hi:[1,0,1]\nv_pk_fma_f32 v[92:93], v[92:93], s[70:71], s[70:71] op_sel:[0,0,1] op_sel_hi:[1,0,1]\nv_pk_fma_f32 v[94:95], v[94:95], s[70:71], s[70:71] op_sel:[0,0,1] op_sel_hi:[1,0,1]\n"
"v_cvt_pk_bf16_f32 v64, v64, v65\n"
"v_cvt_pk_bf16_f32 v65, v66, v67\n"
"v_cvt_pk_bf16_f32 v66, v68, v69\n"
"v_cvt_pk_bf16_f32 v67, v70, v71\n"
"v_mfma_f32_16x16x32_bf16 v[32:35], a[196:199], a[172:175], v[32:35]\n"
"v_permlane16_swap_b32 v64, v66\n"
"v_permlane16_swap_b32 v65, v67\n"
"v_accvgpr_write_b32 a80, v64\n"
"v_accvgpr_write_b32 a81, v65\n"
"v_accvgpr_write_b32 a82, v66\n"
"v_accvgpr_write_b32 a83, v67\n"
"v_mfma_f32_16x16x32_bf16 v[64:67], a[192:195], a[176:179], v[228:231]\n"
"ds_read_b64_tr_b16 a[110:111], v245 offset:8704\n"
"v_cvt_pk_bf16_f32 v96, v96, v97\n"
"v_cvt_pk_bf16_f32 v97, v98, v99\n"
"v_cvt_pk_bf16_f32 v98, v100, v101\n"
"v_cvt_pk_bf16_f32 v99, v102, v103\n"
"v_mfma_f32_16x16x32_bf16 v[64:67], a[196:199], a[180:183], v[64:67]\n"
"v_permlane16_swap_b32 v96, v98\n"
"v_permlane16_swap_b32 v97, v99\n"
"v_mfma_f32_16x16x32_bf16 v[68:71], a[200:203], a[176:179], v[228:231]\n"
"ds_write_b128 v243, v[96:99] offset:4096\n"
"v_cvt_pk_bf16_f32 v72, v72, v73\n"
"v_cvt_pk_bf16_f32 v73, v74, v75\n"
"v_cvt_pk_bf16_f32 v74, v76, v77\n"
"v_cvt_pk_bf16_f32 v75, v78, v79\n"
"v_mfma_f32_16x16x32_bf16 v[68:71], a[204:207], a[180:183], v[68:71]\n"
"buffer_load_dwordx4 v249, s[24:27], s40 offen offset:1024 lds\n"
"v_permlane16_swap_b32 v72, v74\n"
"v_permlane16_swap_b32 v73, v75\n"
"v_accvgpr_write_b32 a84, v72\n"
"v_accvgpr_write_b32 a85, v73\n"
"v_accvgpr_write_b32 a86, v74\n"
"v_accvgpr_write_b32 a87, v75\n"
"v_mfma_f32_16x16x32_bf16 v[72:75], a[208:211], a[176:179], v[228:231]\n"
"ds_read_b64_tr_b16 a[112:113], v246 offset:0\n"
"v_cvt_pk_bf16_f32 v88, v88, v89\n"
"v_mfma_f32_16x16x32_bf16 v[72:75], a[212:215], a[180:183], v[72:75]\n"
"ds_read_b64_tr_b16 a[114:115], v246 offset:512\n"
"v_cvt_pk_bf16_f32 v89, v90, v91\n"
"v_mfma_f32_16x16x32_bf16 v[76:79], a[216:219], a[176:179], v[228:231]\n"
"ds_read_b64_tr_b16 a[116:117], v246 offset:8192\n"
"ds_read_b64_tr_b16 a[118:119], v246 offset:8704\n"
"v_cvt_pk_bf16_f32 v90, v92, v93\n"
"v_cvt_pk_bf16_f32 v91, v94, v95\n"
"v_xor_b32 v247, s74, v247\n"
"v_mfma_f32_16x16x32_bf16 v[76:79], a[220:223], a[180:183], v[76:79]\n"
"ds_read_b64_tr_b16 a[120:121], v247 offset:0\n"
"ds_read_b64_tr_b16 a[122:123], v247 offset:512\n"
"v_permlane16_swap_b32 v88, v90\n"
"v_permlane16_swap_b32 v89, v91\n"
"v_accvgpr_write_b32 a92, v88\n"
"v_accvgpr_write_b32 a93, v89\n"
"v_accvgpr_write_b32 a94, v90\n"
"v_accvgpr_write_b32 a95, v91\n"
"v_mfma_f32_16x16x32_bf16 v[88:91], a[240:243], a[176:179], v[228:231]\n"
"ds_read_b64_tr_b16 a[124:125], v247 offset:8192\n"
"ds_read_b64_tr_b16 a[126:127], v247 offset:8704\n"
"v_mfma_f32_16x16x32_bf16 v[88:91], a[244:247], a[180:183], v[88:91]\n"
"buffer_load_dwordx4 v254, s[24:27], s40 offen offset:2048 lds\n"
"v_mfma_f32_16x16x32_bf16 v[92:95], a[248:251], a[176:179], v[228:231]\n"
"s_add_u32 s49, s49, 128\n"
"ds_read_b64_tr_b16 a[132:133], v244 offset:9216\n"
"v_cvt_pk_bf16_f32 v80, v80, v81\n"
"v_cvt_pk_bf16_f32 v81, v82, v83\n"
"v_cvt_pk_bf16_f32 v82, v84, v85\n"
"v_cvt_pk_bf16_f32 v83, v86, v87\n"
"v_mfma_f32_16x16x32_bf16 v[92:95], a[252:255], a[180:183], v[92:95]\n"
"s_sub_u32 s41, s49, 1280\n"
"ds_read_b64_tr_b16 a[134:135], v244 offset:9728\n"
"v_permlane16_swap_b32 v80, v82\n"
"v_permlane16_swap_b32 v81, v83\n"
"v_accvgpr_write_b32 a88, v80\n"
"v_accvgpr_write_b32 a89, v81\n"
"v_accvgpr_write_b32 a90, v82\n"
"v_accvgpr_write_b32 a91, v83\n"
"v_mfma_f32_16x16x32_bf16 v[80:83], a[224:227], a[176:179], v[228:231]\n"
"s_min_u32 s49, s49, s41\n"
"ds_read_b64_tr_b16 a[136:137], v245 offset:1024\n"
"ds_read_b64_tr_b16 a[138:139], v245 offset:1536\n"
"v_mfma_f32_16x16x32_bf16 v[80:83], a[228:231], a[180:183], v[80:83]\n"
"s_xor_b32 s51, s51, s74\n"
"ds_read_b64_tr_b16 a[140:141], v245 offset:9216\n"
"ds_read_b64_tr_b16 a[142:143], v245 offset:9728\n"
"v_mfma_f32_16x16x32_bf16 v[84:87], a[232:235], a[176:179], v[228:231]\n"
"ds_read_b64_tr_b16 a[144:145], v246 offset:1024\n"
"ds_read_b64_tr_b16 a[146:147], v246 offset:1536\n"
"v_mfma_f32_16x16x32_bf16 v[84:87], a[236:239], a[180:183], v[84:87]\n"
"ds_read_b64_tr_b16 a[148:149], v246 offset:9216\n"
"ds_read_b64_tr_b16 a[150:151], v246 offset:9728\n"
"v_mfma_f32_16x16x32_bf16 v[108:111], a[232:235], a[184:187], v[228:231]\n"
"ds_read_b64_tr_b16 a[152:153], v247 offset:1024\n"
"ds_read_b64_tr_b16 a[154:155], v247 offset:1536\n"
"v_mfma_f32_16x16x32_bf16 v[108:111], a[236:239], a[188:191], v[108:111]\n"
"ds_read_b64_tr_b16 a[156:157], v247 offset:9216\n"
"ds_read_b64_tr_b16 a[158:159], v247 offset:9728\n"
"v_mfma_f32_16x16x32_bf16 v[104:107], a[224:227], a[184:187], v[228:231]\n"
"ds_read_b128 a[168:171], v250 offset:2048\n"
"v_xor_b32 v252, s74, v252\n"
"v_mfma_f32_16x16x32_bf16 v[104:107], a[228:231], a[188:191], v[104:107]\n"
"ds_read_b128 a[172:175], v251 offset:2048\n"
"v_xor_b32 v253, s74, v253\n"
"v_mfma_f32_16x16x32_bf16 v[100:103], a[200:203], a[184:187], v[228:231]\n"
"ds_read_b128 a[176:179], v250 offset:4096\n"
"v_mfma_f32_16x16x32_bf16 v[100:103], a[204:207], a[188:191], v[100:103]\n"
"ds_read_b128 a[180:183], v251 offset:4096\n"
"v_mfma_f32_16x16x32_bf16 v[96:99], a[192:195], a[184:187], v[228:231]\n"
"s_add_u32 s47, s47, 1\n"
"ds_read_b128 a[184:187], v252 offset:0\n"
"v_mfma_f32_16x16x32_bf16 v[96:99], a[196:199], a[188:191], v[96:99]\n"
"s_waitcnt lgkmcnt(9)\n"
"ds_read_b128 a[188:191], v253 offset:0\n"
"s_mov_b32 s62, 18\n"
"s_branch LPL_%=\n"
"LPT2_%=:\n"
"v_mfma_f32_16x16x32_bf16 v[112:115], a[96:99], a[160:163], v[112:115]\n"
"s_add_u32 s78, s32, s96\ns_addc_u32 s79, s33, 0\n"
"s_mov_b64 s[80:81], 0\n"
"s_mov_b32 s82, 0\n"
"v_mfma_f32_16x16x32_bf16 v[112:115], a[100:103], a[164:167], v[112:115]\n"
"v_bfe_u32 v228, v242, 8, 2\n"
"v_and_b32 v229, 0x60, v242\n"
"v_mfma_f32_16x16x32_bf16 v[116:119], a[104:107], a[160:163], v[116:119]\n"
"s_waitcnt vmcnt(11)\n"
"s_load_dword s80, s[78:79], 0\n"
"v_lshlrev_b32 v228, 4, v228\n"
"v_mfma_f32_16x16x32_bf16 v[116:119], a[108:111], a[164:167], v[116:119]\n"
"s_barrier\n"
"v_mfma_f32_16x16x32_bf16 v[120:123], a[112:115], a[160:163], v[120:123]\n"
"s_add_u32 s40, s48, 256\n"
"s_xor_b32 m0, s50, 0xc000\n"
"ds_read_b64_tr_b16 a[192:193], v244 offset:16384\n"
"v_mfma_f32_16x16x32_bf16 v[120:123], a[116:119], a[164:167], v[120:123]\n"
"buffer_load_dwordx4 v224, s[28:31], s40 offen lds\n"
"v_mfma_f32_16x16x32_bf16 v[124:127], a[120:123], a[160:163], v[124:127]\n"
"ds_read_b64_tr_b16 a[194:195], v244 offset:16896\n"
"v_xor_b32 v228, v228, v229\n"
"v_mfma_f32_16x16x32_bf16 v[124:127], a[124:127], a[164:167], v[124:127]\n"
"ds_read_b64_tr_b16 a[196:197], v244 offset:24576\n"
"v_and_b32 v229, 0x70, v242\n"
"v_mfma_f32_16x16x32_bf16 v[128:131], a[128:131], a[160:163], v[128:131]\n"
"ds_read_b64_tr_b16 a[198:199], v244 offset:25088\n"
"v_lshl_add_u32 v228, v229, 3, v228\n"
"v_mfma_f32_16x16x32_bf16 v[128:131], a[132:135], a[164:167], v[128:131]\n"
"ds_read_b64_tr_b16 a[200:201], v245 offset:16384\n"
"v_and_b32 v229, 0x80, v242\n"
"v_mfma_f32_16x16x32_bf16 v[132:135], a[136:139], a[160:163], v[132:135]\n"
"ds_read_b64_tr_b16 a[202:203], v245 offset:16896\n"
"v_lshl_add_u32 v228, v229, 4, v228\n"
"v_mfma_f32_16x16x32_bf16 v[132:135], a[140:143], a[164:167], v[132:135]\n"
"buffer_load_dwordx4 v225, s[28:31], s40 offen offset:1024 lds\n"
"v_mfma_f32_16x16x32_bf16 v[136:139], a[144:147], a[160:163], v[136:139]\n"
"s_add_u32 s82, s82, s80\n"
"ds_read_b64_tr_b16 a[204:205], v245 offset:24576\n"
"v_mov_b32 v229, s43\n"
"v_mfma_f32_16x16x32_bf16 v[136:139], a[148:151], a[164:167], v[136:139]\n"
"s_add_u32 s82, s82, s80\n"
"ds_read_b64_tr_b16 a[206:207], v245 offset:25088\n"
"v_mul_u32_u24 v229, 0x81, v229\n"
"v_mfma_f32_16x16x32_bf16 v[140:143], a[152:155], a[160:163], v[140:143]\n"
"s_add_u32 s82, s82, s80\n"
"ds_read_b64_tr_b16 a[208:209], v246 offset:16384\n"
"v_xor_b32 v228, v228, v229\n"
"v_mfma_f32_16x16x32_bf16 v[140:143], a[156:159], a[164:167], v[140:143]\n"
"s_add_u32 s82, s82, s80\n"
"ds_read_b64_tr_b16 a[210:211], v246 offset:16896\n"
"v_xor_b32 v229, 64, v228\n"
"v_mfma_f32_16x16x32_bf16 v[172:175], a[152:155], a[168:171], v[172:175]\n"
"s_add_u32 s82, s82, s80\n"
"ds_read_b64_tr_b16 a[212:213], v246 offset:24576\n"
"v_xor_b32 v230, 0x2000, v228\n"
"v_mfma_f32_16x16x32_bf16 v[172:175], a[156:159], a[172:175], v[172:175]\n"
"s_add_u32 s82, s82, s80\n"
"ds_read_b64_tr_b16 a[214:215], v246 offset:25088\n"
"v_xor_b32 v231, 0x2000, v229\n"
"v_mfma_f32_16x16x32_bf16 v[168:171], a[144:147], a[168:171], v[168:171]\n"
"s_add_u32 s82, s82, s80\n"
"ds_read_b64_tr_b16 a[216:217], v247 offset:16384\n"
"v_bfe_i32 v241, v242, 4, 1\n"
"v_mfma_f32_16x16x32_bf16 v[168:171], a[148:151], a[172:175], v[168:171]\n"
"s_add_u32 s82, s82, s80\n"
"buffer_load_dwordx4 v226, s[28:31], s40 offen offset:2048 lds\n"
"v_mfma_f32_16x16x32_bf16 v[164:167], a[136:139], a[168:171], v[164:167]\n"
"s_add_u32 s82, s82, s80\n"
"ds_read_b64_tr_b16 a[218:219], v247 offset:16896\n"
"v_and_b32 v241, 0xaaaaaaaa, v241\n"
"v_mfma_f32_16x16x32_bf16 v[164:167], a[140:143], a[172:175], v[164:167]\n"
"s_add_u32 s82, s82, s80\n"
"ds_read_b64_tr_b16 a[220:221], v247 offset:24576\n"
"v_or_b32 v241, 0x44444444, v241\n"
"v_mfma_f32_16x16x32_bf16 v[160:163], a[128:131], a[168:171], v[160:163]\n"
"s_add_u32 s82, s82, s80\n"
"ds_read_b64_tr_b16 a[222:223], v247 offset:25088\n"
"v_bfe_u32 v248, v242, 8, 2\n"
"v_mfma_f32_16x16x32_bf16 v[160:163], a[132:135], a[172:175], v[160:163]\n"
"s_add_u32 s82, s82, s80\n"
"ds_read_b64_tr_b16 a[224:225], v244 offset:17408\n"
"v_lshlrev_b32 v248, 6, v248\n"
"v_mfma_f32_16x16x32_bf16 v[156:159], a[120:123], a[168:171], v[156:159]\n"
"s_add_u32 s82, s82, s80\n"
"ds_read_b64_tr_b16 a[226:227], v244 offset:17920\n"
"v_xor_b32 v248, v248, v242\n"
"v_mfma_f32_16x16x32_bf16 v[156:159], a[124:127], a[172:175], v[156:159]\n"
"s_add_u32 s82, s82, s80\n"
"buffer_load_dwordx4 v227, s[28:31], s40 offen offset:3072 lds\n"
"v_mfma_f32_16x16x32_bf16 v[152:155], a[112:115], a[168:171], v[152:155]\n"
"s_add_u32 s82, s82, s80\n"
"s_mov_b32 s58, 245760\n"
"ds_read_b64_tr_b16 a[228:229], v244 offset:25600\n"
"v_mfma_f32_16x16x32_bf16 v[152:155], a[116:119], a[172:175], v[152:155]\n"
"s_add_u32 s82, s82, s80\n"
"s_mov_b32 s47, 0\n"
"ds_read_b64_tr_b16 a[230:231], v244 offset:26112\n"
"v_mfma_f32_16x16x32_bf16 v[148:151], a[104:107], a[168:171], v[148:151]\n"
"s_add_u32 s82, s82, s80\n"
"s_mov_b32 s52, -896\n"
"ds_read_b64_tr_b16 a[232:233], v245 offset:17408\n"
"v_mfma_f32_16x16x32_bf16 v[148:151], a[108:111], a[172:175], v[148:151]\n"
"s_load_dword s81, s[78:79], 0x2000\n"
"s_mov_b32 s53, 0\n"
"ds_read_b64_tr_b16 a[234:235], v245 offset:17920\n"
"v_mfma_f32_16x16x32_bf16 v[144:147], a[96:99], a[168:171], v[144:147]\n"
"s_mov_b32 m0, s44\n"
"ds_read_b64_tr_b16 a[236:237], v245 offset:25600\n"
"v_xor_b32 v244, s74, v244\n"
"v_mfma_f32_16x16x32_bf16 v[144:147], a[100:103], a[172:175], v[144:147]\n"
"buffer_load_dwordx4 v240, s[16:19], s56 offen nt lds\n"
"v_mfma_f32_16x16x32_bf16 v[176:179], a[96:99], a[176:179], v[176:179]\n"
"s_waitcnt lgkmcnt(5)\n"
"ds_read_b64_tr_b16 a[238:239], v245 offset:26112\n"
"v_xor_b32 v250, s74, v250\n"
"v_mfma_f32_16x16x32_bf16 v[176:179], a[100:103], a[180:183], v[176:179]\n"
"ds_read_b64_tr_b16 a[240:241], v246 offset:17408\n"
"v_xor_b32 v251, s74, v251\n"
"v_mfma_f32_16x16x32_bf16 v[180:183], a[104:107], a[176:179], v[180:183]\n"
"ds_read_b64_tr_b16 a[242:243], v246 offset:17920\n"
"v_xor_b32 v245, s74, v245\n"
"v_mfma_f32_16x16x32_bf16 v[180:183], a[108:111], a[180:183], v[180:183]\n"
"ds_read_b64_tr_b16 a[244:245], v246 offset:25600\n"
"v_xor_b32 v252, s74, v252\n"
"v_mfma_f32_16x16x32_bf16 v[184:187], a[112:115], a[176:179], v[184:187]\n"
"ds_read_b64_tr_b16 a[246:247], v246 offset:26112\n"
"v_xor_b32 v246, s74, v246\n"
"v_mfma_f32_16x16x32_bf16 v[184:187], a[116:119], a[180:183], v[184:187]\n"
"ds_read_b64_tr_b16 a[248:249], v247 offset:17408\n"
"v_xor_b32 v253, s74, v253\n"
"v_mfma_f32_16x16x32_bf16 v[188:191], a[120:123], a[176:179], v[188:191]\n"
"s_add_u32 s40, s56, s52\n"
"ds_read_b64_tr_b16 a[250:251], v247 offset:17920\n"
"v_bfe_u32 v249, v242, 4, 2\n"
"v_mfma_f32_16x16x32_bf16 v[188:191], a[124:127], a[180:183], v[188:191]\n"
"s_sub_u32 s82, s82, s81\n"
"buffer_load_dwordx4 v240, s[16:19], s40 offen offset:1024 nt lds\n"
"v_mfma_f32_16x16x32_bf16 v[200:203], a[144:147], a[176:179], v[200:203]\n"
"s_sub_u32 s82, s82, s81\n"
"ds_read_b64_tr_b16 a[252:253], v247 offset:25600\n"
"v_mul_u32_u24 v249, 20, v249\n"
"v_mfma_f32_16x16x32_bf16 v[200:203], a[148:151], a[180:183], v[200:203]\n"
"s_sub_u32 s82, s82, s81\n"
"ds_read_b64_tr_b16 a[254:255], v247 offset:26112\n"
"v_xor_b32 v247, s74, v247\n"
"v_mfma_f32_16x16x32_bf16 v[204:207], a[152:155], a[176:179], v[204:207]\n"
"s_sub_u32 s82, s82, s81\n"
"v_mfma_f32_16x16x32_bf16 v[204:207], a[156:159], a[180:183], v[204:207]\n"
"s_sub_u32 s82, s82, s81\n"
"v_mfma_f32_16x16x32_bf16 v[192:195], a[128:131], a[176:179], v[192:195]\n"
"s_sub_u32 s82, s82, s81\n"
"s_add_u32 s40, s56, s53\n"
"v_mfma_f32_16x16x32_bf16 v[192:195], a[132:135], a[180:183], v[192:195]\n"
"s_sub_u32 s82, s82, s81\n"
"buffer_load_dwordx4 v240, s[16:19], s40 offen offset:2048 nt lds\n"
"v_mfma_f32_16x16x32_bf16 v[196:199], a[136:139], a[176:179], v[196:199]\n"
"s_sub_u32 s82, s82, s81\n"
"v_mfma_f32_16x16x32_bf16 v[196:199], a[140:143], a[180:183], v[196:199]\n"
"s_sub_u32 s82, s82, s81\n"
"v_mfma_f32_16x16x32_bf16 v[220:223], a[136:139], a[184:187], v[220:223]\n"
"s_sub_u32 s82, s82, s81\n"
"v_mfma_f32_16x16x32_bf16 v[220:223], a[140:143], a[188:191], v[220:223]\n"
"s_sub_u32 s82, s82, s81\n"
"v_mfma_f32_16x16x32_bf16 v[216:219], a[128:131], a[184:187], v[216:219]\n"
"s_sub_u32 s82, s82, s81\n"
"s_add_u32 s40, s56, s54\n"
"v_mfma_f32_16x16x32_bf16 v[216:219], a[132:135], a[188:191], v[216:219]\n"
"s_sub_u32 s82, s82, s81\n"
"buffer_load_dwordx4 v240, s[16:19], s40 offen offset:3072 nt lds\n"
"v_mfma_f32_16x16x32_bf16 v[212:215], a[104:107], a[184:187], v[212:215]\n"
"s_sub_u32 s82, s82, s81\n"
"v_mfma_f32_16x16x32_bf16 v[212:215], a[108:111], a[188:191], v[212:215]\n"
"s_sub_u32 s82, s82, s81\n"
"v_mfma_f32_16x16x32_bf16 v[208:211], a[96:99], a[184:187], v[208:211]\n"
"s_sub_u32 s82, s82, s81\n"
"v_mfma_f32_16x16x32_bf16 v[208:211], a[100:103], a[188:191], v[208:211]\n"
"s_sub_u32 s82, s82, s81\n"
"v_mfma_f32_16x16x32_bf16 v[0:3], a[192:195], a[160:163], v[0:3]\n"
"v_mfma_f32_16x16x32_bf16 v[0:3], a[196:199], a[164:167], v[0:3]\n"
"v_mfma_f32_16x16x32_bf16 v[4:7], a[200:203], a[160:163], v[4:7]\n"
"s_waitcnt vmcnt(8) lgkmcnt(0)\n"
"v_mfma_f32_16x16x32_bf16 v[4:7], a[204:207], a[164:167], v[4:7]\n"
"s_barrier\n"
"v_mfma_f32_16x16x32_bf16 v[8:11], a[208:211], a[160:163], v[8:11]\n"
"s_add_u32 m0, s44, 65536\n"
"ds_read_b64_tr_b16 a[96:97], v244 offset:0\n"
"v_mfma_f32_16x16x32_bf16 v[8:11], a[212:215], a[164:167], v[8:11]\n"
"buffer_load_dwordx4 v240, s[20:23], s56 offen nt lds\n"
"v_mfma_f32_16x16x32_bf16 v[12:15], a[216:219], a[160:163], v[12:15]\n"
"s_lshr_b32 s82, s82, 31\n"
"ds_read_b64_tr_b16 a[98:99], v244 offset:512\n"
"v_mfma_f32_16x16x32_bf16 v[12:15], a[220:223], a[164:167], v[12:15]\n"
"s_add_u32 s82, s82, s60\n"
"ds_read_b64_tr_b16 a[100:101], v244 offset:8192\n"
"v_mfma_f32_16x16x32_bf16 v[16:19], a[224:227], a[160:163], v[16:19]\n"
"s_add_u32 s82, s82, 32\n"
"ds_read_b64_tr_b16 a[102:103], v244 offset:8704\n"
"v_mfma_f32_16x16x32_bf16 v[16:19], a[228:231], a[164:167], v[16:19]\n"
"v_mov_b32 v254, s82\n"
"ds_read_b64_tr_b16 a[104:105], v245 offset:0\n"
"v_mfma_f32_16x16x32_bf16 v[20:23], a[232:235], a[160:163], v[20:23]\n"
"v_lshlrev_b32_e64 v255, 2, s93\n"
"ds_read_b64_tr_b16 a[106:107], v245 offset:512\n"
"v_mfma_f32_16x16x32_bf16 v[20:23], a[236:239], a[164:167], v[20:23]\n"
"ds_read_b64_tr_b16 a[108:109], v245 offset:8192\n"
"v_mfma_f32_16x16x32_bf16 v[24:27], a[240:243], a[160:163], v[24:27]\n"
"s_add_u32 s40, s56, s52\n"
"ds_read_b64_tr_b16 a[110:111], v245 offset:8704\n"
"v_mfma_f32_16x16x32_bf16 v[24:27], a[244:247], a[164:167], v[24:27]\n"
"buffer_load_dwordx4 v240, s[20:23], s40 offen offset:1024 nt lds\n"
"v_mfma_f32_16x16x32_bf16 v[28:31], a[248:251], a[160:163], v[28:31]\n"
"ds_read_b128 a[160:163], v250 offset:0\n"
"v_mfma_f32_16x16x32_bf16 v[28:31], a[252:255], a[164:167], v[28:31]\n"
"s_mov_b64 exec, 1\n"
"global_store_dword v255, v254, s[6:7] offset:256 sc1\n"
"s_mov_b64 exec, -1\n"
"v_mfma_f32_16x16x32_bf16 v[60:63], a[248:251], a[168:171], v[60:63]\n"
"ds_read_b128 a[164:167], v251 offset:0\n"
"v_mfma_f32_16x16x32_bf16 v[60:63], a[252:255], a[172:175], v[60:63]\n"
"ds_read_b64_tr_b16 a[112:113], v246 offset:0\n"
"v_and_b32 v254, 0x20, v242\n"
"v_mfma_f32_16x16x32_bf16 v[56:59], a[240:243], a[168:171], v[56:59]\n"
"s_add_u32 s40, s56, s53\n"
"ds_read_b64_tr_b16 a[114:115], v246 offset:512\n"
"v_add_u32 v254, -1, v254\n"
"v_mfma_f32_16x16x32_bf16 v[56:59], a[244:247], a[172:175], v[56:59]\n"
"buffer_load_dwordx4 v240, s[20:23], s40 offen offset:2048 nt lds\n"
"v_mfma_f32_16x16x32_bf16 v[52:55], a[232:235], a[168:171], v[52:55]\n"
"ds_read_b64_tr_b16 a[116:117], v246 offset:8192\n"
"v_ashrrev_i32 v254, 31, v254\n"
"v_mfma_f32_16x16x32_bf16 v[52:55], a[236:239], a[172:175], v[52:55]\n"
"ds_read_b64_tr_b16 a[118:119], v246 offset:8704\n"
"v_and_b32 v254, 0x3f803f80, v254\n"
"v_mfma_f32_16x16x32_bf16 v[48:51], a[224:227], a[168:171], v[48:51]\n"
"ds_read_b64_tr_b16 a[120:121], v247 offset:0\n"
"v_mfma_f32_16x16x32_bf16 v[48:51], a[228:231], a[172:175], v[48:51]\n"
"ds_read_b64_tr_b16 a[122:123], v247 offset:512\n"
"v_mfma_f32_16x16x32_bf16 v[44:47], a[216:219], a[168:171], v[44:47]\n"
"s_add_u32 s40, s56, s54\n"
"ds_read_b64_tr_b16 a[124:125], v247 offset:8192\n"
"v_mfma_f32_16x16x32_bf16 v[44:47], a[220:223], a[172:175], v[44:47]\n"
"buffer_load_dwordx4 v240, s[20:23], s40 offen offset:3072 nt lds\n"
"v_mfma_f32_16x16x32_bf16 v[40:43], a[208:211], a[168:171], v[40:43]\n"
"s_add_u32 s55, s55, s59\n"
"ds_read_b64_tr_b16 a[126:127], v247 offset:8704\n"
"v_mfma_f32_16x16x32_bf16 v[40:43], a[212:215], a[172:175], v[40:43]\n"
"s_add_u32 s41, s55, s57\n"
"ds_read_b64_tr_b16 a[128:129], v244 offset:1024\n"
"v_mfma_f32_16x16x32_bf16 v[36:39], a[200:203], a[168:171], v[36:39]\n"
"s_and_b32 s41, s41, s58\n"
"ds_read_b64_tr_b16 a[130:131], v244 offset:1536\n"
"v_mfma_f32_16x16x32_bf16 v[36:39], a[204:207], a[172:175], v[36:39]\n"
"s_andn2_b32 s56, s55, s58\n"
"ds_read_b64_tr_b16 a[132:133], v244 offset:9216\n"
"v_mfma_f32_16x16x32_bf16 v[32:35], a[192:195], a[168:171], v[32:35]\n"
"s_or_b32 s56, s56, s41\n"
"ds_read_b64_tr_b16 a[134:135], v244 offset:9728\n"
"v_mfma_f32_16x16x32_bf16 v[32:35], a[196:199], a[172:175], v[32:35]\n"
"ds_read_b64_tr_b16 a[136:137], v245 offset:1024\n"
"v_mfma_f32_16x16x32_bf16 v[64:67], a[192:195], a[176:179], v[64:67]\n"
"s_add_u32 m0, s44, 16384\n"
"ds_read_b64_tr_b16 a[138:139], v245 offset:1536\n"
"v_mfma_f32_16x16x32_bf16 v[64:67], a[196:199], a[180:183], v[64:67]\n"
"buffer_load_dwordx4 v240, s[16:19], s56 offen nt lds\n"
"v_mfma_f32_16x16x32_bf16 v[68:71], a[200:203], a[176:179], v[68:71]\n"
"ds_read_b64_tr_b16 a[140:141], v245 offset:9216\n"
"v_mfma_f32_16x16x32_bf16 v[68:71], a[204:207], a[180:183], v[68:71]\n"
"ds_read_b64_tr_b16 a[142:143], v245 offset:9728\n"
"v_mfma_f32_16x16x32_bf16 v[72:75], a[208:211], a[176:179], v[72:75]\n"
"ds_read_b64_tr_b16 a[144:145], v246 offset:1024\n"
"v_mfma_f32_16x16x32_bf16 v[72:75], a[212:215], a[180:183], v[72:75]\n"
"ds_read_b64_tr_b16 a[146:147], v246 offset:1536\n"
"v_mfma_f32_16x16x32_bf16 v[76:79], a[216:219], a[176:179], v[76:79]\n"
"s_add_u32 s40, s56, s52\n"
"ds_read_b64_tr_b16 a[148:149], v246 offset:9216\n"
"v_mfma_f32_16x16x32_bf16 v[76:79], a[220:223], a[180:183], v[76:79]\n"
"buffer_load_dwordx4 v240, s[16:19], s40 offen offset:1024 nt lds\n"
"v_mfma_f32_16x16x32_bf16 v[88:91], a[240:243], a[176:179], v[88:91]\n"
"ds_read_b64_tr_b16 a[150:151], v246 offset:9728\n"
"v_mfma_f32_16x16x32_bf16 v[88:91], a[244:247], a[180:183], v[88:91]\n"
"ds_read_b64_tr_b16 a[152:153], v247 offset:1024\n"
"v_mfma_f32_16x16x32_bf16 v[92:95], a[248:251], a[176:179], v[92:95]\n"
"ds_read_b64_tr_b16 a[154:155], v247 offset:1536\n"
"v_mfma_f32_16x16x32_bf16 v[92:95], a[252:255], a[180:183], v[92:95]\n"
"ds_read_b64_tr_b16 a[156:157], v247 offset:9216\n"
"v_mfma_f32_16x16x32_bf16 v[80:83], a[224:227], a[176:179], v[80:83]\n"
"s_add_u32 s40, s56, s53\n"
"ds_read_b64_tr_b16 a[158:159], v247 offset:9728\n"
"v_mfma_f32_16x16x32_bf16 v[80:83], a[228:231], a[180:183], v[80:83]\n"
"buffer_load_dwordx4 v240, s[16:19], s40 offen offset:2048 nt lds\n"
"v_mfma_f32_16x16x32_bf16 v[84:87], a[232:235], a[176:179], v[84:87]\n"
"ds_read_b128 a[168:171], v250 offset:2048\n"
"v_mfma_f32_16x16x32_bf16 v[84:87], a[236:239], a[180:183], v[84:87]\n"
"ds_read_b128 a[172:175], v251 offset:2048\n"
"v_mfma_f32_16x16x32_bf16 v[108:111], a[232:235], a[184:187], v[108:111]\n"
"ds_read_b128 a[176:179], v250 offset:4096\n"
"v_mfma_f32_16x16x32_bf16 v[108:111], a[236:239], a[188:191], v[108:111]\n"
"ds_read_b128 a[180:183], v251 offset:4096\n"
"v_mfma_f32_16x16x32_bf16 v[104:107], a[224:227], a[184:187], v[104:107]\n"
"s_add_u32 s40, s56, s54\n"
"v_mfma_f32_16x16x32_bf16 v[104:107], a[228:231], a[188:191], v[104:107]\n"
"buffer_load_dwordx4 v240, s[16:19], s40 offen offset:3072 nt lds\n"
"v_mfma_f32_16x16x32_bf16 v[100:103], a[200:203], a[184:187], v[100:103]\n"
"v_mfma_f32_16x16x32_bf16 v[100:103], a[204:207], a[188:191], v[100:103]\n"
"v_mfma_f32_16x16x32_bf16 v[96:99], a[192:195], a[184:187], v[96:99]\n"
"ds_read_b128 a[184:187], v252 offset:0\n"
"v_mfma_f32_16x16x32_bf16 v[96:99], a[196:199], a[188:191], v[96:99]\n"
"s_waitcnt lgkmcnt(3)\n"
"ds_read_b128 a[188:191], v253 offset:0\n"
"v_mfma_f32_16x16x32_bf16 v[112:115], a[96:99], a[160:163], v[112:115]\n"
"v_mfma_f32_16x16x32_bf16 v[112:115], a[100:103], a[164:167], v[112:115]\n"
"v_mfma_f32_16x16x32_bf16 v[116:119], a[104:107], a[160:163], v[116:119]\n"
"s_waitcnt vmcnt(13)\n"
"v_mfma_f32_16x16x32_bf16 v[116:119], a[108:111], a[164:167], v[116:119]\n"
"s_barrier\n"
"v_mfma_f32_16x16x32_bf16 v[120:123], a[112:115], a[160:163], v[120:123]\n"
"s_add_u32 m0, s44, 81920\n"
"ds_read_b64_tr_b16 a[232:233], v245 offset:17408\n"
"v_mfma_f32_16x16x32_bf16 v[120:123], a[116:119], a[164:167], v[120:123]\n"
"buffer_load_dwordx4 v240, s[20:23], s56 offen nt lds\n"
"v_mfma_f32_16x16x32_bf16 v[124:127], a[120:123], a[160:163], v[124:127]\n"
"ds_read_b64_tr_b16 a[234:235], v245 offset:17920\n"
"v_mfma_f32_16x16x32_bf16 v[124:127], a[124:127], a[164:167], v[124:127]\n"
"ds_read_b64_tr_b16 a[236:237], v245 offset:25600\n"
"v_mfma_f32_16x16x32_bf16 v[128:131], a[128:131], a[160:163], v[128:131]\n"
"ds_read_b64_tr_b16 a[238:239], v245 offset:26112\n"
"v_mfma_f32_16x16x32_bf16 v[128:131], a[132:135], a[164:167], v[128:131]\n"
"ds_read_b64_tr_b16 a[224:225], v244 offset:17408\n"
"v_mfma_f32_16x16x32_bf16 v[132:135], a[136:139], a[160:163], v[132:135]\n"
"ds_read_b64_tr_b16 a[226:227], v244 offset:17920\n"
"v_mfma_f32_16x16x32_bf16 v[132:135], a[140:143], a[164:167], v[132:135]\n"
"ds_read_b64_tr_b16 a[192:193], v244 offset:16384\n"
"v_mfma_f32_16x16x32_bf16 v[136:139], a[144:147], a[160:163], v[136:139]\n"
"ds_read_b64_tr_b16 a[194:195], v244 offset:16896\n"
"v_mfma_f32_16x16x32_bf16 v[136:139], a[148:151], a[164:167], v[136:139]\n"
"ds_read_b64_tr_b16 a[196:197], v244 offset:24576\n"
"v_mfma_f32_16x16x32_bf16 v[140:143], a[152:155], a[160:163], v[140:143]\n"
"ds_read_b64_tr_b16 a[198:199], v244 offset:25088\n"
"v_mfma_f32_16x16x32_bf16 v[140:143], a[156:159], a[164:167], v[140:143]\n"
"s_waitcnt lgkmcnt(7)\n"
"ds_read_b64_tr_b16 a[200:201], v245 offset:16384\n"
"v_mfma_f32_16x16x32_bf16 v[220:223], a[136:139], a[184:187], v[220:223]\n"
"ds_read_b64_tr_b16 a[202:203], v245 offset:16896\n"
"v_mfma_f32_16x16x32_bf16 v[220:223], a[140:143], a[188:191], v[220:223]\n"
"ds_read_b64_tr_b16 a[204:205], v245 offset:24576\n"
"v_mfma_f32_16x16x32_bf16 v[216:219], a[128:131], a[184:187], v[216:219]\n"
"s_add_u32 s40, s56, s52\n"
"ds_read_b64_tr_b16 a[206:207], v245 offset:25088\n"
"v_mfma_f32_16x16x32_bf16 v[216:219], a[132:135], a[188:191], v[216:219]\n"
"buffer_load_dwordx4 v240, s[20:23], s40 offen offset:1024 nt lds\n"
"v_mfma_f32_16x16x32_bf16 v[212:215], a[104:107], a[184:187], v[212:215]\n"
"ds_read_b64_tr_b16 a[208:209], v246 offset:16384\n"
"v_mfma_f32_16x16x32_bf16 v[212:215], a[108:111], a[188:191], v[212:215]\n"
"v_pk_fma_f32 v[112:113], v[112:113], s[70:71], s[70:71] op_sel:[0,0,1] op_sel_hi:[1,0,1]\nv_pk_fma_f32 v[114:115], v[114:115], s[70:71], s[70:71] op_sel:[0,0,1] op_sel_hi:[1,0,1]\nv_pk_fma_f32 v[116:117], v[116:117], s[70:71], s[70:71] op_sel:[0,0,1] op_sel_hi:[1,0,1]\nv_pk_fma_f32 v[118:119], v[118:119], s[70:71], s[70:71] op_sel:[0,0,1] op_sel_hi:[1,0,1]\nv_pk_fma_f32 v[120:121], v[120:121], s[70:71], s[70:71] op_sel:[0,0,1] op_sel_hi:[1,0,1]\nv_pk_fma_f32 v[122:123], v[122:123], s[70:71], s[70:71] op_sel:[0,0,1] op_sel_hi:[1,0,1]\nv_pk_fma_f32 v[124:125], v[124:125], s[70:71], s[70:71] op_sel:[0,0,1] op_sel_hi:[1,0,1]\nv_pk_fma_f32 v[126:127], v[126:127], s[70:71], s[70:71] op_sel:[0,0,1] op_sel_hi:[1,0,1]\nv_pk_fma_f32 v[128:129], v[128:129], s[70:71], s[70:71] op_sel:[0,0,1] op_sel_hi:[1,0,1]\nv_pk_fma_f32 v[130:131], v[130:131], s[70:71], s[70:71] op_sel:[0,0,1] op_sel_hi:[1,0,1]\nv_pk_fma_f32 v[132:133], v[132:133], s[70:71], s[70:71] op_sel:[0,0,1] op_sel_hi:[1,0,1]\nv_pk_fma_f32 v[134:135], v[134:135], s[70:71], s[70:71] op_sel:[0,0,1] op_sel_hi:[1,0,1]\nv_pk_fma_f32 v[136:137], v[136:137], s[70:71], s[70:71] op_sel:[0,0,1] op_sel_hi:[1,0,1]\nv_pk_fma_f32 v[138:139], v[138:139], s[70:71], s[70:71] op_sel:[0,0,1] op_sel_hi:[1,0,1]\nv_pk_fma_f32 v[140:141], v[140:141], s[70:71], s[70:71] op_sel:[0,0,1] op_sel_hi:[1,0,1]\nv_pk_fma_f32 v[142:143], v[142:143], s[70:71], s[70:71] op_sel:[0,0,1] op_sel_hi:[1,0,1]\n"
"v_mfma_f32_16x16x32_bf16 v[208:211], a[96:99], a[184:187], v[208:211]\n"
"ds_read_b64_tr_b16 a[210:211], v246 offset:16896\n"
"v_cvt_pk_bf16_f32 v128, v128, v129\n"
"v_mfma_f32_16x16x32_bf16 v[208:211], a[100:103], a[188:191], v[208:211]\n"
"ds_read_b64_tr_b16 a[212:213], v246 offset:24576\n"
"v_cvt_pk_bf16_f32 v129, v130, v131\n"
"v_mfma_f32_16x16x32_bf16 v[172:175], a[152:155], a[168:171], v[172:175]\n"
"ds_read_b64_tr_b16 a[214:215], v246 offset:25088\n"
"v_cvt_pk_bf16_f32 v130, v132, v133\n"
"v_mfma_f32_16x16x32_bf16 v[172:175], a[156:159], a[172:175], v[172:175]\n"
"ds_read_b64_tr_b16 a[216:217], v247 offset:16384\n"
"v_cvt_pk_bf16_f32 v131, v134, v135\n"
"v_mfma_f32_16x16x32_bf16 v[168:171], a[144:147], a[168:171], v[168:171]\n"
"ds_read_b64_tr_b16 a[218:219], v247 offset:16896\n"
"v_cvt_pk_bf16_f32 v136, v136, v137\n"
"v_mfma_f32_16x16x32_bf16 v[168:171], a[148:151], a[172:175], v[168:171]\n"
"ds_read_b64_tr_b16 a[220:221], v247 offset:24576\n"
"v_cvt_pk_bf16_f32 v137, v138, v139\n"
"v_mfma_f32_16x16x32_bf16 v[164:167], a[136:139], a[168:171], v[164:167]\n"
"ds_read_b64_tr_b16 a[222:223], v247 offset:25088\n"
"v_cvt_pk_bf16_f32 v138, v140, v141\n"
"v_mfma_f32_16x16x32_bf16 v[164:167], a[140:143], a[172:175], v[164:167]\n"
"ds_read_b64_tr_b16 a[228:229], v244 offset:25600\n"
"v_cvt_pk_bf16_f32 v139, v142, v143\n"
"v_mfma_f32_16x16x32_bf16 v[160:163], a[128:131], a[168:171], v[160:163]\n"
"s_add_u32 s40, s56, s53\n"
"ds_read_b64_tr_b16 a[230:231], v244 offset:26112\n"
"v_cvt_pk_bf16_f32 v120, v120, v121\n"
"v_mfma_f32_16x16x32_bf16 v[160:163], a[132:135], a[172:175], v[160:163]\n"
"buffer_load_dwordx4 v240, s[20:23], s40 offen offset:2048 nt lds\n"
"v_mfma_f32_16x16x32_bf16 v[156:159], a[120:123], a[168:171], v[156:159]\n"
"ds_read_b64_tr_b16 a[240:241], v246 offset:17408\n"
"v_cvt_pk_bf16_f32 v121, v122, v123\n"
"v_mfma_f32_16x16x32_bf16 v[156:159], a[124:127], a[172:175], v[156:159]\n"
"ds_read_b64_tr_b16 a[242:243], v246 offset:17920\n"
"v_cvt_pk_bf16_f32 v122, v124, v125\n"
"v_mfma_f32_16x16x32_bf16 v[152:155], a[112:115], a[168:171], v[152:155]\n"
"ds_read_b64_tr_b16 a[244:245], v246 offset:25600\n"
"v_cvt_pk_bf16_f32 v123, v126, v127\n"
"v_mfma_f32_16x16x32_bf16 v[152:155], a[116:119], a[172:175], v[152:155]\n"
"ds_read_b64_tr_b16 a[246:247], v246 offset:26112\n"
"v_cvt_pk_bf16_f32 v112, v112, v113\n"
"v_mfma_f32_16x16x32_bf16 v[148:151], a[104:107], a[168:171], v[148:151]\n"
"ds_read_b64_tr_b16 a[248:249], v247 offset:17408\n"
"v_cvt_pk_bf16_f32 v113, v114, v115\n"
"v_mfma_f32_16x16x32_bf16 v[148:151], a[108:111], a[172:175], v[148:151]\n"
"ds_read_b64_tr_b16 a[250:251], v247 offset:17920\n"
"v_cvt_pk_bf16_f32 v114, v116, v117\n"
"v_mfma_f32_16x16x32_bf16 v[144:147], a[96:99], a[168:171], v[144:147]\n"
"ds_read_b64_tr_b16 a[252:253], v247 offset:25600\n"
"v_cvt_pk_bf16_f32 v115, v118, v119\n"
"v_mfma_f32_16x16x32_bf16 v[144:147], a[100:103], a[172:175], v[144:147]\n"
"v_pk_fma_f32 v[208:209], v[208:209], s[70:71], s[70:71] op_sel:[0,0,1] op_sel_hi:[1,0,1]\nv_pk_fma_f32 v[210:211], v[210:211], s[70:71], s[70:71] op_sel:[0,0,1] op_sel_hi:[1,0,1]\nv_pk_fma_f32 v[212:213], v[212:213], s[70:71], s[70:71] op_sel:[0,0,1] op_sel_hi:[1,0,1]\nv_pk_fma_f32 v[214:215], v[214:215], s[70:71], s[70:71] op_sel:[0,0,1] op_sel_hi:[1,0,1]\nv_pk_fma_f32 v[216:217], v[216:217], s[70:71], s[70:71] op_sel:[0,0,1] op_sel_hi:[1,0,1]\nv_pk_fma_f32 v[218:219], v[218:219], s[70:71], s[70:71] op_sel:[0,0,1] op_sel_hi:[1,0,1]\nv_pk_fma_f32 v[220:221], v[220:221], s[70:71], s[70:71] op_sel:[0,0,1] op_sel_hi:[1,0,1]\nv_pk_fma_f32 v[222:223], v[222:223], s[70:71], s[70:71] op_sel:[0,0,1] op_sel_hi:[1,0,1]\n"
"v_mfma_f32_16x16x32_bf16 v[176:179], a[96:99], a[176:179], v[176:179]\n"
"ds_read_b64_tr_b16 a[254:255], v247 offset:26112\n"
"v_cvt_pk_bf16_f32 v208, v208, v209\n"
"v_mfma_f32_16x16x32_bf16 v[176:179], a[100:103], a[180:183], v[176:179]\n"
"v_permlane16_swap_b32 v128, v130\n"
"v_mfma_f32_16x16x32_bf16 v[180:183], a[104:107], a[176:179], v[180:183]\n"
"v_permlane16_swap_b32 v129, v131\n"
"v_mfma_f32_16x16x32_bf16 v[180:183], a[108:111], a[180:183], v[180:183]\n"
"v_permlane16_swap_b32 v136, v138\n"
"v_mfma_f32_16x16x32_bf16 v[184:187], a[112:115], a[176:179], v[184:187]\n"
"s_add_u32 s40, s56, s54\n"
"v_permlane16_swap_b32 v137, v139\n"
"v_mfma_f32_16x16x32_bf16 v[184:187], a[116:119], a[180:183], v[184:187]\n"
"buffer_load_dwordx4 v240, s[20:23], s40 offen offset:3072 nt lds\n"
"v_mfma_f32_16x16x32_bf16 v[188:191], a[120:123], a[176:179], v[188:191]\n"
"s_add_u32 s55, s55, s59\n"
"v_pk_fma_f32 v[144:145], v[144:145], s[70:71], s[70:71] op_sel:[0,0,1] op_sel_hi:[1,0,1]\nv_pk_fma_f32 v[146:147], v[146:147], s[70:71], s[70:71] op_sel:[0,0,1] op_sel_hi:[1,0,1]\nv_pk_fma_f32 v[148:149], v[148:149], s[70:71], s[70:71] op_sel:[0,0,1] op_sel_hi:[1,0,1]\nv_pk_fma_f32 v[150:151], v[150:151], s[70:71], s[70:71] op_sel:[0,0,1] op_sel_hi:[1,0,1]\nv_pk_fma_f32 v[152:153], v[152:153], s[70:71], s[70:71] op_sel:[0,0,1] op_sel_hi:[1,0,1]\nv_pk_fma_f32 v[154:155], v[154:155], s[70:71], s[70:71] op_sel:[0,0,1] op_sel_hi:[1,0,1]\nv_pk_fma_f32 v[156:157], v[156:157], s[70:71], s[70:71] op_sel:[0,0,1] op_sel_hi:[1,0,1]\nv_pk_fma_f32 v[158:159], v[158:159], s[70:71], s[70:71] op_sel:[0,0,1] op_sel_hi:[1,0,1]\nv_pk_fma_f32 v[160:161], v[160:161], s[70:71], s[70:71] op_sel:[0,0,1] op_sel_hi:[1,0,1]\nv_pk_fma_f32 v[162:163], v[162:163], s[70:71], s[70:71] op_sel:[0,0,1] op_sel_hi:[1,0,1]\nv_pk_fma_f32 v[164:165], v[164:165], s[70:71], s[70:71] op_sel:[0,0,1] op_sel_hi:[1,0,1]\nv_pk_fma_f32 v[166:167], v[166:167], s[70:71], s[70:71] op_sel:[0,0,1] op_sel_hi:[1,0,1]\nv_pk_fma_f32 v[168:169], v[168:169], s[70:71], s[70:71] op_sel:[0,0,1] op_sel_hi:[1,0,1]\nv_pk_fma_f32 v[170:171], v[170:171], s[70:71], s[70:71] op_sel:[0,0,1] op_sel_hi:[1,0,1]\nv_pk_fma_f32 v[172:173], v[172:173], s[70:71], s[70:71] op_sel:[0,0,1] op_sel_hi:[1,0,1]\nv_pk_fma_f32 v[174:175], v[174:175], s[70:71], s[70:71] op_sel:[0,0,1] op_sel_hi:[1,0,1]\n"
"v_mfma_f32_16x16x32_bf16 v[188:191], a[124:127], a[180:183], v[188:191]\n"
"s_add_u32 s41, s55, s57\n"
"v_permlane16_swap_b32 v112, v114\n"
"v_mfma_f32_16x16x32_bf16 v[200:203], a[144:147], a[176:179], v[200:203]\n"
"s_and_b32 s41, s41, s58\n"
"v_permlane16_swap_b32 v113, v115\n"
"v_mfma_f32_16x16x32_bf16 v[200:203], a[148:151], a[180:183], v[200:203]\n"
"s_andn2_b32 s56, s55, s58\n"
"v_permlane16_swap_b32 v120, v122\n"
"v_mfma_f32_16x16x32_bf16 v[204:207], a[152:155], a[176:179], v[204:207]\n"
"s_or_b32 s56, s56, s41\n"
"v_permlane16_swap_b32 v121, v123\n"
"v_mfma_f32_16x16x32_bf16 v[204:207], a[156:159], a[180:183], v[204:207]\n"
"v_cvt_pk_bf16_f32 v209, v210, v211\n"
"v_cvt_pk_bf16_f32 v210, v212, v213\n"
"v_mfma_f32_16x16x32_bf16 v[192:195], a[128:131], a[176:179], v[192:195]\n"
"s_nop 0\n"
"v_permlane16_swap_b32 v208, v210\n"
"v_mfma_f32_16x16x32_bf16 v[192:195], a[132:135], a[180:183], v[192:195]\n"
"v_cvt_pk_bf16_f32 v211, v214, v215\n"
"v_cvt_pk_bf16_f32 v216, v216, v217\n"
"v_mfma_f32_16x16x32_bf16 v[196:199], a[136:139], a[176:179], v[196:199]\n"
"v_permlane16_swap_b32 v209, v211\n"
"v_mfma_f32_16x16x32_bf16 v[196:199], a[140:143], a[180:183], v[196:199]\n"
"ds_write_b128 v243, v[208:211] offset:8192\n"
"v_mfma_f32_16x16x32_bf16 v[108:111], a[232:235], a[184:187], v[108:111]\n"
"s_waitcnt lgkmcnt(1)\n"
"s_add_u32 m0, s44, 32768\n"
"v_cvt_pk_bf16_f32 v217, v218, v219\n"
"v_cvt_pk_bf16_f32 v218, v220, v221\n"
"v_mfma_f32_16x16x32_bf16 v[108:111], a[236:239], a[188:191], v[108:111]\n"
"buffer_load_dwordx4 v240, s[16:19], s56 offen nt lds\n"
"v_mfma_f32_16x16x32_bf16 v[104:107], a[224:227], a[184:187], v[104:107]\n"
"s_waitcnt vmcnt(14)\n"
"v_permlane16_swap_b32 v216, v218\n"
"v_mfma_f32_16x16x32_bf16 v[104:107], a[228:231], a[188:191], v[104:107]\n"
"s_barrier\n"
"v_mfma_f32_16x16x32_bf16 v[100:103], a[200:203], a[184:187], v[100:103]\n"
"v_cvt_pk_bf16_f32 v219, v222, v223\n"
"v_cvt_pk_bf16_f32 v144, v144, v145\n"
"v_mfma_f32_16x16x32_bf16 v[100:103], a[204:207], a[188:191], v[100:103]\n"
"s_mov_b32 s74, 0x41400000\n"
"v_permlane16_swap_b32 v217, v219\n"
"v_mfma_f32_16x16x32_bf16 v[96:99], a[192:195], a[184:187], v[96:99]\n"
"ds_write_b128 v243, v[216:219] offset:10240\n"
"v_mfma_f32_16x16x32_bf16 v[96:99], a[196:199], a[188:191], v[96:99]\n"
"v_pk_fma_f32 v[176:177], v[176:177], s[70:71], s[70:71] op_sel:[0,0,1] op_sel_hi:[1,0,1]\nv_pk_fma_f32 v[178:179], v[178:179], s[70:71], s[70:71] op_sel:[0,0,1] op_sel_hi:[1,0,1]\nv_pk_fma_f32 v[180:181], v[180:181], s[70:71], s[70:71] op_sel:[0,0,1] op_sel_hi:[1,0,1]\nv_pk_fma_f32 v[182:183], v[182:183], s[70:71], s[70:71] op_sel:[0,0,1] op_sel_hi:[1,0,1]\nv_pk_fma_f32 v[184:185], v[184:185], s[70:71], s[70:71] op_sel:[0,0,1] op_sel_hi:[1,0,1]\nv_pk_fma_f32 v[186:187], v[186:187], s[70:71], s[70:71] op_sel:[0,0,1] op_sel_hi:[1,0,1]\nv_pk_fma_f32 v[188:189], v[188:189], s[70:71], s[70:71] op_sel:[0,0,1] op_sel_hi:[1,0,1]\nv_pk_fma_f32 v[190:191], v[190:191], s[70:71], s[70:71] op_sel:[0,0,1] op_sel_hi:[1,0,1]\nv_pk_fma_f32 v[192:193], v[192:193], s[70:71], s[70:71] op_sel:[0,0,1] op_sel_hi:[1,0,1]\nv_pk_fma_f32 v[194:195], v[194:195], s[70:71], s[70:71] op_sel:[0,0,1] op_sel_hi:[1,0,1]\nv_pk_fma_f32 v[196:197], v[196:197], s[70:71], s[70:71] op_sel:[0,0,1] op_sel_hi:[1,0,1]\nv_pk_fma_f32 v[198:199], v[198:199], s[70:71], s[70:71] op_sel:[0,0,1] op_sel_hi:[1,0,1]\nv_pk_fma_f32 v[200:201], v[200:201], s[70:71], s[70:71] op_sel:[0,0,1] op_sel_hi:[1,0,1]\nv_pk_fma_f32 v[202:203], v[202:203], s[70:71], s[70:71] op_sel:[0,0,1] op_sel_hi:[1,0,1]\nv_pk_fma_f32 v[204:205], v[204:205], s[70:71], s[70:71] op_sel:[0,0,1] op_sel_hi:[1,0,1]\nv_pk_fma_f32 v[206:207], v[206:207], s[70:71], s[70:71] op_sel:[0,0,1] op_sel_hi:[1,0,1]\n"
"v_mfma_f32_16x16x32_bf16 v[0:3], a[192:195], a[160:163], v[0:3]\n"
"v_cvt_pk_bf16_f32 v145, v146, v147\n"
"v_cvt_pk_bf16_f32 v146, v148, v149\n"
"v_mfma_f32_16x16x32_bf16 v[0:3], a[196:199], a[164:167], v[0:3]\n"
"s_nop 0\n"
"v_permlane16_swap_b32 v144, v146\n"
"v_mfma_f32_16x16x32_bf16 v[4:7], a[200:203], a[160:163], v[4:7]\n"
"v_bfe_u32 v149, v242, 8, 2\n"
"v_lshl_add_u32 v149, v149, 4, s43\n"
"v_mfma_f32_16x16x32_bf16 v[4:7], a[204:207], a[164:167], v[4:7]\n"
"ds_read_b128 v[208:211], v149 offset:0\n"
"v_cvt_pk_bf16_f32 v147, v150, v151\n"
"v_mfma_f32_16x16x32_bf16 v[8:11], a[208:211], a[160:163], v[8:11]\n"
"ds_read_b128 v[216:219], v149 offset:1024\n"
"v_xor_b32 v150, 64, v149\n"
"v_mfma_f32_16x16x32_bf16 v[8:11], a[212:215], a[164:167], v[8:11]\n"
"ds_read_b128 v[212:215], v150 offset:0\n"
"v_cvt_pk_bf16_f32 v176, v176, v177\n"
"v_mfma_f32_16x16x32_bf16 v[12:15], a[216:219], a[160:163], v[12:15]\n"
"s_add_u32 s40, s56, s52\n"
"ds_read_b128 v[220:223], v150 offset:1024\n"
"v_cvt_pk_bf16_f32 v177, v178, v179\n"
"v_mfma_f32_16x16x32_bf16 v[12:15], a[220:223], a[164:167], v[12:15]\n"
"buffer_load_dwordx4 v240, s[16:19], s40 offen offset:1024 nt lds\n"
"v_mfma_f32_16x16x32_bf16 v[16:19], a[224:227], a[160:163], v[16:19]\n"
"v_permlane16_swap_b32 v145, v147\n"
"v_mfma_f32_16x16x32_bf16 v[16:19], a[228:231], a[164:167], v[16:19]\n"
"v_pk_fma_f32 v[96:97], v[96:97], s[70:71], s[70:71] op_sel:[0,0,1] op_sel_hi:[1,0,1]\nv_pk_fma_f32 v[98:99], v[98:99], s[70:71], s[70:71] op_sel:[0,0,1] op_sel_hi:[1,0,1]\nv_pk_fma_f32 v[100:101], v[100:101], s[70:71], s[70:71] op_sel:[0,0,1] op_sel_hi:[1,0,1]\nv_pk_fma_f32 v[102:103], v[102:103], s[70:71], s[70:71] op_sel:[0,0,1] op_sel_hi:[1,0,1]\nv_pk_fma_f32 v[104:105], v[104:105], s[70:71], s[70:71] op_sel:[0,0,1] op_sel_hi:[1,0,1]\nv_pk_fma_f32 v[106:107], v[106:107], s[70:71], s[70:71] op_sel:[0,0,1] op_sel_hi:[1,0,1]\nv_pk_fma_f32 v[108:109], v[108:109], s[70:71], s[70:71] op_sel:[0,0,1] op_sel_hi:[1,0,1]\nv_pk_fma_f32 v[110:111], v[110:111], s[70:71], s[70:71] op_sel:[0,0,1] op_sel_hi:[1,0,1]\n"
"v_mfma_f32_16x16x32_bf16 v[20:23], a[232:235], a[160:163], v[20:23]\n"
"v_cvt_pk_bf16_f32 v178, v180, v181\n"
"v_cvt_pk_bf16_f32 v179, v182, v183\n"
"v_mfma_f32_16x16x32_bf16 v[20:23], a[236:239], a[164:167], v[20:23]\n"
"v_permlane16_swap_b32 v176, v178\n"
"v_mfma_f32_16x16x32_bf16 v[24:27], a[240:243], a[160:163], v[24:27]\n"
"v_permlane16_swap_b32 v177, v179\n"
"v_mfma_f32_16x16x32_bf16 v[24:27], a[244:247], a[164:167], v[24:27]\n"
"v_cvt_pk_bf16_f32 v184, v184, v185\n"
"v_cvt_pk_bf16_f32 v185, v186, v187\n"
"v_mfma_f32_16x16x32_bf16 v[28:31], a[248:251], a[160:163], v[28:31]\n"
"v_cvt_pk_bf16_f32 v186, v188, v189\n"
"v_cvt_pk_bf16_f32 v187, v190, v191\n"
"v_mfma_f32_16x16x32_bf16 v[28:31], a[252:255], a[164:167], v[28:31]\n"
"v_permlane16_swap_b32 v184, v186\n"
"v_mfma_f32_16x16x32_bf16 v[60:63], a[248:251], a[168:171], v[60:63]\n"
"v_permlane16_swap_b32 v185, v187\n"
"v_mfma_f32_16x16x32_bf16 v[60:63], a[252:255], a[172:175], v[60:63]\n"
"v_accvgpr_write_b32 a132, v184\n"
"v_accvgpr_write_b32 a133, v185\n"
"v_mfma_f32_16x16x32_bf16 v[56:59], a[240:243], a[168:171], v[56:59]\n"
"s_add_u32 s40, s56, s53\n"
"v_accvgpr_write_b32 a134, v186\n"
"v_accvgpr_write_b32 a135, v187\n"
"v_mfma_f32_16x16x32_bf16 v[56:59], a[244:247], a[172:175], v[56:59]\n"
"buffer_load_dwordx4 v240, s[16:19], s40 offen offset:2048 nt lds\n"
"v_mfma_f32_16x16x32_bf16 v[52:55], a[232:235], a[168:171], v[52:55]\n"
"v_xor_b32 v148, 0x400, v243\n"
"v_cvt_pk_bf16_f32 v192, v192, v193\n"
"v_mfma_f32_16x16x32_bf16 v[52:55], a[236:239], a[172:175], v[52:55]\n"
"v_pk_fma_f32 v[0:1], v[0:1], s[70:71], s[70:71] op_sel:[0,0,1] op_sel_hi:[1,0,1]\nv_pk_fma_f32 v[2:3], v[2:3], s[70:71], s[70:71] op_sel:[0,0,1] op_sel_hi:[1,0,1]\nv_pk_fma_f32 v[4:5], v[4:5], s[70:71], s[70:71] op_sel:[0,0,1] op_sel_hi:[1,0,1]\nv_pk_fma_f32 v[6:7], v[6:7], s[70:71], s[70:71] op_sel:[0,0,1] op_sel_hi:[1,0,1]\nv_pk_fma_f32 v[8:9], v[8:9], s[70:71], s[70:71] op_sel:[0,0,1] op_sel_hi:[1,0,1]\nv_pk_fma_f32 v[10:11], v[10:11], s[70:71], s[70:71] op_sel:[0,0,1] op_sel_hi:[1,0,1]\nv_pk_fma_f32 v[12:13], v[12:13], s[70:71], s[70:71] op_sel:[0,0,1] op_sel_hi:[1,0,1]\nv_pk_fma_f32 v[14:15], v[14:15], s[70:71], s[70:71] op_sel:[0,0,1] op_sel_hi:[1,0,1]\nv_pk_fma_f32 v[16:17], v[16:17], s[70:71], s[70:71] op_sel:[0,0,1] op_sel_hi:[1,0,1]\nv_pk_fma_f32 v[18:19], v[18:19], s[70:71], s[70:71] op_sel:[0,0,1] op_sel_hi:[1,0,1]\nv_pk_fma_f32 v[20:21], v[20:21], s[70:71], s[70:71] op_sel:[0,0,1] op_sel_hi:[1,0,1]\nv_pk_fma_f32 v[22:23], v[22:23], s[70:71], s[70:71] op_sel:[0,0,1] op_sel_hi:[1,0,1]\nv_pk_fma_f32 v[24:25], v[24:25], s[70:71], s[70:71] op_sel:[0,0,1] op_sel_hi:[1,0,1]\nv_pk_fma_f32 v[26:27], v[26:27], s[70:71], s[70:71] op_sel:[0,0,1] op_sel_hi:[1,0,1]\nv_pk_fma_f32 v[28:29], v[28:29], s[70:71], s[70:71] op_sel:[0,0,1] op_sel_hi:[1,0,1]\nv_pk_fma_f32 v[30:31], v[30:31], s[70:71], s[70:71] op_sel:[0,0,1] op_sel_hi:[1,0,1]\n"
"v_mfma_f32_16x16x32_bf16 v[48:51], a[224:227], a[168:171], v[48:51]\n"
"v_cvt_pk_bf16_f32 v193, v194, v195\n"
"v_cvt_pk_bf16_f32 v194, v196, v197\n"
"v_mfma_f32_16x16x32_bf16 v[48:51], a[228:231], a[172:175], v[48:51]\n"
"s_nop 0\n"
"v_permlane16_swap_b32 v192, v194\n"
"v_mfma_f32_16x16x32_bf16 v[44:47], a[216:219], a[168:171], v[44:47]\n"
"v_cvt_pk_bf16_f32 v195, v198, v199\n"
"v_accvgpr_write_b32 a136, v192\n"
"v_mfma_f32_16x16x32_bf16 v[44:47], a[220:223], a[172:175], v[44:47]\n"
"v_permlane16_swap_b32 v193, v195\n"
"v_mfma_f32_16x16x32_bf16 v[40:43], a[208:211], a[168:171], v[40:43]\n"
"v_accvgpr_write_b32 a137, v193\n"
"v_accvgpr_write_b32 a138, v194\n"
"v_mfma_f32_16x16x32_bf16 v[40:43], a[212:215], a[172:175], v[40:43]\n"
"v_accvgpr_write_b32 a139, v195\n"
"v_cvt_pk_bf16_f32 v152, v152, v153\n"
"v_mfma_f32_16x16x32_bf16 v[36:39], a[200:203], a[168:171], v[36:39]\n"
"v_cvt_pk_bf16_f32 v153, v154, v155\n"
"v_cvt_pk_bf16_f32 v154, v156, v157\n"
"v_mfma_f32_16x16x32_bf16 v[36:39], a[204:207], a[172:175], v[36:39]\n"
"s_nop 0\n"
"v_permlane16_swap_b32 v152, v154\n"
"v_mfma_f32_16x16x32_bf16 v[32:35], a[192:195], a[168:171], v[32:35]\n"
"v_cvt_pk_bf16_f32 v155, v158, v159\n"
"v_accvgpr_write_b32 a116, v152\n"
"v_mfma_f32_16x16x32_bf16 v[32:35], a[196:199], a[172:175], v[32:35]\n"
"v_permlane16_swap_b32 v153, v155\n"
"v_mfma_f32_16x16x32_bf16 v[64:67], a[192:195], a[176:179], v[64:67]\n"
"s_add_u32 s40, s56, s54\n"
"ds_read_b128 a[192:195], v243 offset:0\n"
"v_accvgpr_write_b32 a117, v153\n"
"v_mfma_f32_16x16x32_bf16 v[64:67], a[196:199], a[180:183], v[64:67]\n"
"buffer_load_dwordx4 v240, s[16:19], s40 offen offset:3072 nt lds\n"
"v_mfma_f32_16x16x32_bf16 v[68:71], a[200:203], a[176:179], v[68:71]\n"
"ds_read_b128 a[196:199], v148 offset:0\n"
"v_accvgpr_write_b32 a118, v154\n"
"v_mfma_f32_16x16x32_bf16 v[68:71], a[204:207], a[180:183], v[68:71]\n"
"ds_read_b128 a[200:203], v243 offset:2048\n"
"v_accvgpr_write_b32 a119, v155\n"
"v_mfma_f32_16x16x32_bf16 v[72:75], a[208:211], a[176:179], v[72:75]\n"
"ds_read_b128 a[204:207], v148 offset:2048\n"
"v_cvt_pk_bf16_f32 v160, v160, v161\n"
"v_mfma_f32_16x16x32_bf16 v[72:75], a[212:215], a[180:183], v[72:75]\n"
"v_pk_fma_f32 v[32:33], v[32:33], s[70:71], s[70:71] op_sel:[0,0,1] op_sel_hi:[1,0,1]\nv_pk_fma_f32 v[34:35], v[34:35], s[70:71], s[70:71] op_sel:[0,0,1] op_sel_hi:[1,0,1]\nv_pk_fma_f32 v[36:37], v[36:37], s[70:71], s[70:71] op_sel:[0,0,1] op_sel_hi:[1,0,1]\nv_pk_fma_f32 v[38:39], v[38:39], s[70:71], s[70:71] op_sel:[0,0,1] op_sel_hi:[1,0,1]\nv_pk_fma_f32 v[40:41], v[40:41], s[70:71], s[70:71] op_sel:[0,0,1] op_sel_hi:[1,0,1]\nv_pk_fma_f32 v[42:43], v[42:43], s[70:71], s[70:71] op_sel:[0,0,1] op_sel_hi:[1,0,1]\nv_pk_fma_f32 v[44:45], v[44:45], s[70:71], s[70:71] op_sel:[0,0,1] op_sel_hi:[1,0,1]\nv_pk_fma_f32 v[46:47], v[46:47], s[70:71], s[70:71] op_sel:[0,0,1] op_sel_hi:[1,0,1]\nv_pk_fma_f32 v[48:49], v[48:49], s[70:71], s[70:71] op_sel:[0,0,1] op_sel_hi:[1,0,1]\nv_pk_fma_f32 v[50:51], v[50:51], s[70:71], s[70:71] op_sel:[0,0,1] op_sel_hi:[1,0,1]\nv_pk_fma_f32 v[52:53], v[52:53], s[70:71], s[70:71] op_sel:[0,0,1] op_sel_hi:[1,0,1]\nv_pk_fma_f32 v[54:55], v[54:55], s[70:71], s[70:71] op_sel:[0,0,1] op_sel_hi:[1,0,1]\nv_pk_fma_f32 v[56:57], v[56:57], s[70:71], s[70:71] op_sel:[0,0,1] op_sel_hi:[1,0,1]\nv_pk_fma_f32 v[58:59], v[58:59], s[70:71], s[70:71] op_sel:[0,0,1] op_sel_hi:[1,0,1]\nv_pk_fma_f32 v[60:61], v[60:61], s[70:71], s[70:71] op_sel:[0,0,1] op_sel_hi:[1,0,1]\nv_pk_fma_f32 v[62:63], v[62:63], s[70:71], s[70:71] op_sel:[0,0,1] op_sel_hi:[1,0,1]\n"
"v_mfma_f32_16x16x32_bf16 v[76:79], a[216:219], a[176:179], v[76:79]\n"
"v_cvt_pk_bf16_f32 v161, v162, v163\n"
"v_cvt_pk_bf16_f32 v162, v164, v165\n"
"v_mfma_f32_16x16x32_bf16 v[76:79], a[220:223], a[180:183], v[76:79]\n"
"s_nop 0\n"
"v_permlane16_swap_b32 v160, v162\n"
"v_mfma_f32_16x16x32_bf16 v[88:91], a[240:243], a[176:179], v[88:91]\n"
"v_cvt_pk_bf16_f32 v163, v166, v167\n"
"v_accvgpr_write_b32 a120, v160\n"
"v_mfma_f32_16x16x32_bf16 v[88:91], a[244:247], a[180:183], v[88:91]\n"
"v_permlane16_swap_b32 v161, v163\n"
"v_mfma_f32_16x16x32_bf16 v[92:95], a[248:251], a[176:179], v[92:95]\n"
"v_accvgpr_write_b32 a121, v161\n"
"v_accvgpr_write_b32 a122, v162\n"
"v_mfma_f32_16x16x32_bf16 v[92:95], a[252:255], a[180:183], v[92:95]\n"
"v_accvgpr_write_b32 a123, v163\n"
"v_cvt_pk_bf16_f32 v168, v168, v169\n"
"v_mfma_f32_16x16x32_bf16 v[80:83], a[224:227], a[176:179], v[80:83]\n"
"v_cvt_pk_bf16_f32 v169, v170, v171\n"
"v_cvt_pk_bf16_f32 v170, v172, v173\n"
"v_mfma_f32_16x16x32_bf16 v[80:83], a[228:231], a[180:183], v[80:83]\n"
"s_nop 0\n"
"v_permlane16_swap_b32 v168, v170\n"
"v_mfma_f32_16x16x32_bf16 v[84:87], a[232:235], a[176:179], v[84:87]\n"
"v_cvt_pk_bf16_f32 v171, v174, v175\n"
"v_accvgpr_write_b32 a124, v168\n"
"v_mfma_f32_16x16x32_bf16 v[84:87], a[236:239], a[180:183], v[84:87]\n"
"s_waitcnt lgkmcnt(0)\n"
"v_permlane16_swap_b32 v169, v171\n"
"v_mfma_f32_16x16x32_bf16 v[180:183], v[208:211], a[0:3], 0\n"
"v_accvgpr_write_b32 a125, v169\n"
"v_accvgpr_write_b32 a126, v170\n"
"v_mfma_f32_16x16x32_bf16 v[180:183], v[212:215], a[4:7], v[180:183]\n"
"v_accvgpr_write_b32 a127, v171\n"
"v_accvgpr_write_b32 a128, v176\n"
"v_mfma_f32_16x16x32_bf16 v[180:183], v[216:219], a[8:11], v[180:183]\n"
"v_accvgpr_write_b32 a129, v177\n"
"v_accvgpr_write_b32 a130, v178\n"
"v_mfma_f32_16x16x32_bf16 v[180:183], v[220:223], a[12:15], v[180:183]\n"
"v_accvgpr_write_b32 a131, v179\n"
"v_accvgpr_write_b32 a104, v128\n"
"v_mfma_f32_16x16x32_bf16 v[184:187], v[208:211], a[16:19], 0\n"
"v_accvgpr_write_b32 a105, v129\n"
"v_accvgpr_write_b32 a108, v136\n"
"v_mfma_f32_16x16x32_bf16 v[184:187], v[212:215], a[20:23], v[184:187]\n"
"v_pk_fma_f32 v[64:65], v[64:65], s[70:71], s[70:71] op_sel:[0,0,1] op_sel_hi:[1,0,1]\nv_pk_fma_f32 v[66:67], v[66:67], s[70:71], s[70:71] op_sel:[0,0,1] op_sel_hi:[1,0,1]\nv_pk_fma_f32 v[68:69], v[68:69], s[70:71], s[70:71] op_sel:[0,0,1] op_sel_hi:[1,0,1]\nv_pk_fma_f32 v[70:71], v[70:71], s[70:71], s[70:71] op_sel:[0,0,1] op_sel_hi:[1,0,1]\nv_pk_fma_f32 v[72:73], v[72:73], s[70:71], s[70:71] op_sel:[0,0,1] op_sel_hi:[1,0,1]\nv_pk_fma_f32 v[74:75], v[74:75], s[70:71], s[70:71] op_sel:[0,0,1] op_sel_hi:[1,0,1]\nv_pk_fma_f32 v[76:77], v[76:77], s[70:71], s[70:71] op_sel:[0,0,1] op_sel_hi:[1,0,1]\nv_pk_fma_f32 v[78:79], v[78:79], s[70:71], s[70:71] op_sel:[0,0,1] op_sel_hi:[1,0,1]\nv_pk_fma_f32 v[80:81], v[80:81], s[70:71], s[70:71] op_sel:[0,0,1] op_sel_hi:[1,0,1]\nv_pk_fma_f32 v[82:83], v[82:83], s[70:71], s[70:71] op_sel:[0,0,1] op_sel_hi:[1,0,1]\nv_pk_fma_f32 v[84:85], v[84:85], s[70:71], s[70:71] op_sel:[0,0,1] op_sel_hi:[1,0,1]\nv_pk_fma_f32 v[86:87], v[86:87], s[70:71], s[70:71] op_sel:[0,0,1] op_sel_hi:[1,0,1]\nv_pk_fma_f32 v[88:89], v[88:89], s[70:71], s[70:71] op_sel:[0,0,1] op_sel_hi:[1,0,1]\nv_pk_fma_f32 v[90:91], v[90:91], s[70:71], s[70:71] op_sel:[0,0,1] op_sel_hi:[1,0,1]\nv_pk_fma_f32 v[92:93], v[92:93], s[70:71], s[70:71] op_sel:[0,0,1] op_sel_hi:[1,0,1]\nv_pk_fma_f32 v[94:95], v[94:95], s[70:71], s[70:71] op_sel:[0,0,1] op_sel_hi:[1,0,1]\n"
"v_mfma_f32_16x16x32_bf16 v[184:187], v[216:219], a[24:27], v[184:187]\n"
"v_accvgpr_write_b32 a109, v137\n"
"v_accvgpr_write_b32 a112, v144\n"
"v_mfma_f32_16x16x32_bf16 v[184:187], v[220:223], a[28:31], v[184:187]\n"
"v_accvgpr_write_b32 a113, v145\n"
"v_accvgpr_write_b32 a106, v130\n"
"v_mfma_f32_16x16x32_bf16 v[188:191], v[208:211], a[32:35], 0\n"
"v_accvgpr_write_b32 a107, v131\n"
"v_accvgpr_write_b32 a110, v138\n"
"v_mfma_f32_16x16x32_bf16 v[188:191], v[212:215], a[36:39], v[188:191]\n"
"v_sub_f32 v180, s74, v180\n"
"v_sub_f32 v181, s74, v181\n"
"v_mfma_f32_16x16x32_bf16 v[188:191], v[216:219], a[40:43], v[188:191]\n"
"v_sub_f32 v182, s74, v182\n"
"v_sub_f32 v183, s74, v183\n"
"v_mfma_f32_16x16x32_bf16 v[188:191], v[220:223], a[44:47], v[188:191]\n"
"v_accvgpr_write_b32 a111, v139\n"
"v_cvt_pk_bf16_f32 v96, v96, v97\n"
"v_mfma_f32_16x16x32_bf16 v[192:195], v[208:211], a[192:195], 0\n"
"ds_read_b128 v[208:211], v228 offset:0\n"
"v_cvt_pk_bf16_f32 v97, v98, v99\n"
"v_mfma_f32_16x16x32_bf16 v[192:195], v[212:215], a[196:199], v[192:195]\n"
"ds_read_b128 v[212:215], v229 offset:0\n"
"v_sub_f32 v184, s74, v184\n"
"v_mfma_f32_16x16x32_bf16 v[192:195], v[216:219], a[200:203], v[192:195]\n"
"ds_read_b128 v[216:219], v228 offset:1024\n"
"v_sub_f32 v185, s74, v185\n"
"v_mfma_f32_16x16x32_bf16 v[192:195], v[220:223], a[204:207], v[192:195]\n"
"s_waitcnt lgkmcnt(2)\n"
"ds_read_b128 v[220:223], v229 offset:1024\n"
"v_sub_f32 v186, s74, v186\n"
"v_mfma_f32_16x16x32_bf16 v[152:155], v[208:211], a[0:3], v[180:183]\n"
"s_waitcnt lgkmcnt(2)\n"
"v_sub_f32 v187, s74, v187\n"
"v_cvt_pk_bf16_f32 v98, v100, v101\n"
"v_mfma_f32_16x16x32_bf16 v[152:155], v[212:215], a[4:7], v[152:155]\n"
"s_waitcnt lgkmcnt(1)\n"
"v_sub_f32 v188, s74, v188\n"
"v_sub_f32 v189, s74, v189\n"
"v_mfma_f32_16x16x32_bf16 v[152:155], v[216:219], a[8:11], v[152:155]\n"
"s_waitcnt lgkmcnt(0)\n"
"v_sub_f32 v190, s74, v190\n"
"v_sub_f32 v191, s74, v191\n"
"v_mfma_f32_16x16x32_bf16 v[152:155], v[220:223], a[12:15], v[152:155]\n"
"v_permlane16_swap_b32 v96, v98\n"
"v_mfma_f32_16x16x32_bf16 v[156:159], v[216:219], a[24:27], v[184:187]\n"
"v_cvt_pk_bf16_f32 v99, v102, v103\n"
"v_cvt_pk_bf16_f32 v104, v104, v105\n"
"v_mfma_f32_16x16x32_bf16 v[156:159], v[220:223], a[28:31], v[156:159]\n"
"v_sub_f32 v192, s74, v192\n"
"v_sub_f32 v193, s74, v193\n"
"v_mfma_f32_16x16x32_bf16 v[156:159], v[208:211], a[16:19], v[156:159]\n"
"v_sub_f32 v194, s74, v194\n"
"v_sub_f32 v195, s74, v195\n"
"v_mfma_f32_16x16x32_bf16 v[156:159], v[212:215], a[20:23], v[156:159]\n"
"v_permlane16_swap_b32 v97, v99\n"
"v_mfma_f32_16x16x32_bf16 v[160:163], v[208:211], a[32:35], v[188:191]\n"
"ds_write_b128 v243, v[96:99] offset:12288\n"
"v_mfma_f32_16x16x32_bf16 v[160:163], v[212:215], a[36:39], v[160:163]\n"
"v_exp_f32 v152, v152\n"
"v_mfma_f32_16x16x32_bf16 v[160:163], v[216:219], a[40:43], v[160:163]\n"
"v_exp_f32 v153, v153\n"
"v_mfma_f32_16x16x32_bf16 v[160:163], v[220:223], a[44:47], v[160:163]\n"
"v_exp_f32 v154, v154\n"
"v_mfma_f32_16x16x32_bf16 v[164:167], v[216:219], a[200:203], v[192:195]\n"
"ds_read_b128 v[216:219], v228 offset:5120\n"
"v_cvt_pk_bf16_f32 v128, v152, v153\n"
"v_mfma_f32_16x16x32_bf16 v[164:167], v[220:223], a[204:207], v[164:167]\n"
"ds_read_b128 v[220:223], v229 offset:5120\n"
"v_cvt_pk_bf16_f32 v105, v106, v107\n"
"v_mfma_f32_16x16x32_bf16 v[164:167], v[208:211], a[192:195], v[164:167]\n"
"ds_read_b128 v[208:211], v228 offset:4096\n"
"v_cvt_pk_bf16_f32 v106, v108, v109\n"
"v_mfma_f32_16x16x32_bf16 v[164:167], v[212:215], a[196:199], v[164:167]\n"
"s_waitcnt lgkmcnt(2)\n"
"ds_read_b128 v[212:215], v229 offset:4096\n"
"v_cvt_pk_bf16_f32 v107, v110, v111\n"
"v_mfma_f32_16x16x32_bf16 v[168:171], v[216:219], a[8:11], v[180:183]\n"
"s_waitcnt lgkmcnt(2)\n"
"v_exp_f32 v155, v155\n"
"v_mfma_f32_16x16x32_bf16 v[168:171], v[220:223], a[12:15], v[168:171]\n"
"s_waitcnt lgkmcnt(1)\n"
"v_exp_f32 v156, v156\n"
"v_mfma_f32_16x16x32_bf16 v[168:171], v[208:211], a[0:3], v[168:171]\n"
"s_waitcnt lgkmcnt(0)\n"
"v_exp_f32 v157, v157\n"
"v_mfma_f32_16x16x32_bf16 v[168:171], v[212:215], a[4:7], v[168:171]\n"
"v_exp_f32 v158, v158\n"
"v_mfma_f32_16x16x32_bf16 v[172:175], v[208:211], a[16:19], v[184:187]\n"
"v_exp_f32 v159, v159\n"
"v_mfma_f32_16x16x32_bf16 v[172:175], v[212:215], a[20:23], v[172:175]\n"
"v_exp_f32 v160, v160\n"
"v_mfma_f32_16x16x32_bf16 v[172:175], v[216:219], a[24:27], v[172:175]\n"
"v_exp_f32 v161, v161\n"
"v_mfma_f32_16x16x32_bf16 v[172:175], v[220:223], a[28:31], v[172:175]\n"
"v_exp_f32 v162, v162\n"
"v_mfma_f32_16x16x32_bf16 v[176:179], v[216:219], a[40:43], v[188:191]\n"
"v_exp_f32 v163, v163\n"
"v_mfma_f32_16x16x32_bf16 v[176:179], v[220:223], a[44:47], v[176:179]\n"
"v_cvt_pk_bf16_f32 v129, v154, v155\n"
"v_cvt_pk_bf16_f32 v136, v156, v157\n"
"v_mfma_f32_16x16x32_bf16 v[176:179], v[208:211], a[32:35], v[176:179]\n"
"v_exp_f32 v164, v164\n"
"v_mfma_f32_16x16x32_bf16 v[176:179], v[212:215], a[36:39], v[176:179]\n"
"v_exp_f32 v165, v165\n"
"v_mfma_f32_16x16x32_bf16 v[152:155], v[208:211], a[192:195], v[192:195]\n"
"ds_read_b128 v[208:211], v230 offset:0\n"
"v_cvt_pk_bf16_f32 v137, v158, v159\n"
"v_mfma_f32_16x16x32_bf16 v[152:155], v[212:215], a[196:199], v[152:155]\n"
"ds_read_b128 v[212:215], v231 offset:0\n"
"v_cvt_pk_bf16_f32 v144, v160, v161\n"
"v_mfma_f32_16x16x32_bf16 v[152:155], v[216:219], a[200:203], v[152:155]\n"
"ds_read_b128 v[216:219], v230 offset:1024\n"
"v_cvt_pk_bf16_f32 v145, v162, v163\n"
"v_mfma_f32_16x16x32_bf16 v[152:155], v[220:223], a[204:207], v[152:155]\n"
"s_waitcnt lgkmcnt(2)\n"
"ds_read_b128 v[220:223], v231 offset:1024\n"
"v_cvt_pk_bf16_f32 v124, v164, v165\n"
"v_mfma_f32_16x16x32_bf16 v[156:159], v[208:211], a[0:3], v[180:183]\n"
"s_waitcnt lgkmcnt(2)\n"
"v_exp_f32 v166, v166\n"
"v_mfma_f32_16x16x32_bf16 v[156:159], v[212:215], a[4:7], v[156:159]\n"
"s_waitcnt lgkmcnt(1)\n"
"v_exp_f32 v167, v167\n"
"v_mfma_f32_16x16x32_bf16 v[156:159], v[216:219], a[8:11], v[156:159]\n"
"s_waitcnt lgkmcnt(0)\n"
"v_exp_f32 v168, v168\n"
"v_mfma_f32_16x16x32_bf16 v[156:159], v[220:223], a[12:15], v[156:159]\n"
"v_exp_f32 v169, v169\n"
"v_mfma_f32_16x16x32_bf16 v[160:163], v[216:219], a[24:27], v[184:187]\n"
"v_exp_f32 v170, v170\n"
"v_mfma_f32_16x16x32_bf16 v[160:163], v[220:223], a[28:31], v[160:163]\n"
"v_exp_f32 v171, v171\n"
"v_mfma_f32_16x16x32_bf16 v[160:163], v[208:211], a[16:19], v[160:163]\n"
"v_cvt_pk_bf16_f32 v125, v166, v167\n"
"v_cvt_pk_bf16_f32 v130, v168, v169\n"
"v_mfma_f32_16x16x32_bf16 v[160:163], v[212:215], a[20:23], v[160:163]\n"
"v_exp_f32 v172, v172\n"
"v_mfma_f32_16x16x32_bf16 v[164:167], v[208:211], a[32:35], v[188:191]\n"
"ds_read_b128 v[208:211], v230 offset:4096\n"
"v_cvt_pk_bf16_f32 v131, v170, v171\n"
"v_mfma_f32_16x16x32_bf16 v[164:167], v[212:215], a[36:39], v[164:167]\n"
"ds_read_b128 v[212:215], v231 offset:4096\n"
"v_accvgpr_write_b32 a114, v146\n"
"v_mfma_f32_16x16x32_bf16 v[164:167], v[216:219], a[40:43], v[164:167]\n"
"ds_read_b128 v[216:219], v230 offset:5120\n"
"v_accvgpr_write_b32 a115, v147\n"
"v_mfma_f32_16x16x32_bf16 v[164:167], v[220:223], a[44:47], v[164:167]\n"
"s_waitcnt lgkmcnt(2)\n"
"ds_read_b128 v[220:223], v231 offset:5120\n"
"v_cvt_pk_bf16_f32 v8, v8, v9\n"
"v_mfma_f32_16x16x32_bf16 v[168:171], v[208:211], a[0:3], v[180:183]\n"
"s_waitcnt lgkmcnt(2)\n"
"v_exp_f32 v173, v173\n"
"v_mfma_f32_16x16x32_bf16 v[168:171], v[212:215], a[4:7], v[168:171]\n"
"s_waitcnt lgkmcnt(1)\n"
"v_exp_f32 v174, v174\n"
"v_mfma_f32_16x16x32_bf16 v[168:171], v[216:219], a[8:11], v[168:171]\n"
"s_waitcnt lgkmcnt(0)\n"
"v_exp_f32 v175, v175\n"
"v_mfma_f32_16x16x32_bf16 v[168:171], v[220:223], a[12:15], v[168:171]\n"
"v_cvt_pk_bf16_f32 v138, v172, v173\n"
"v_cvt_pk_bf16_f32 v139, v174, v175\n"
"v_mfma_f32_16x16x32_bf16 v[172:175], v[216:219], a[24:27], v[184:187]\n"
"v_permlane16_swap_b32 v104, v106\n"
"v_mfma_f32_16x16x32_bf16 v[172:175], v[220:223], a[28:31], v[172:175]\n"
"v_permlane16_swap_b32 v105, v107\n"
"v_mfma_f32_16x16x32_bf16 v[172:175], v[208:211], a[16:19], v[172:175]\n"
"ds_write_b128 v243, v[104:107] offset:14336\n"
"v_exp_f32 v176, v176\n"
"v_exp_f32 v177, v177\n"
"v_exp_f32 v178, v178\n"
"v_exp_f32 v179, v179\n"
"v_mfma_f32_16x16x32_bf16 v[172:175], v[212:215], a[20:23], v[172:175]\n"
"s_cbranch_vccnz LPCE0_%=\nLPKE0_%=:\n"
"v_cvt_pk_bf16_f32 v146, v176, v177\n"
"v_cvt_pk_bf16_f32 v147, v178, v179\n"
"v_mfma_f32_16x16x32_bf16 v[176:179], v[208:211], a[32:35], v[188:191]\n"
"v_exp_f32 v152, v152\n"
"v_mfma_f32_16x16x32_bf16 v[176:179], v[212:215], a[36:39], v[176:179]\n"
"v_exp_f32 v153, v153\n"
"v_mfma_f32_16x16x32_bf16 v[176:179], v[216:219], a[40:43], v[176:179]\n"
"v_exp_f32 v154, v154\n"
"v_mfma_f32_16x16x32_bf16 v[176:179], v[220:223], a[44:47], v[176:179]\n"
"v_exp_f32 v155, v155\n"
"s_waitcnt vmcnt(8) lgkmcnt(0)\n"
"s_barrier\n"
"ds_read_b128 v[208:211], v228 offset:16384\n"
"ds_read_b128 v[212:215], v229 offset:16384\n"
"ds_read_b128 v[216:219], v228 offset:17408\n"
"ds_read_b128 v[220:223], v229 offset:17408\n"
"v_cvt_pk_bf16_f32 v126, v152, v153\n"
"v_cvt_pk_bf16_f32 v127, v154, v155\n"
"s_waitcnt lgkmcnt(3)\n"
"v_mfma_f32_16x16x32_bf16 v[152:155], v[208:211], a[0:3], v[180:183]\n"
"v_exp_f32 v156, v156\n"
"s_waitcnt lgkmcnt(2)\n"
"v_mfma_f32_16x16x32_bf16 v[152:155], v[212:215], a[4:7], v[152:155]\n"
"v_exp_f32 v157, v157\n"
"s_waitcnt lgkmcnt(1)\n"
"v_mfma_f32_16x16x32_bf16 v[152:155], v[216:219], a[8:11], v[152:155]\n"
"s_waitcnt lgkmcnt(0)\n"
"s_add_u32 m0, s44, 98304\n"
"v_exp_f32 v158, v158\n"
"v_exp_f32 v159, v159\n"
"v_mfma_f32_16x16x32_bf16 v[152:155], v[220:223], a[12:15], v[152:155]\n"
"buffer_load_dwordx4 v240, s[20:23], s56 offen nt lds\n"
"v_cvt_pk_bf16_f32 v132, v156, v157\n"
"v_cvt_pk_bf16_f32 v133, v158, v159\n"
"v_mfma_f32_16x16x32_bf16 v[156:159], v[216:219], a[24:27], v[184:187]\n"
"v_exp_f32 v160, v160\n"
"v_mfma_f32_16x16x32_bf16 v[156:159], v[220:223], a[28:31], v[156:159]\n"
"v_exp_f32 v161, v161\n"
"v_mfma_f32_16x16x32_bf16 v[156:159], v[208:211], a[16:19], v[156:159]\n"
"v_exp_f32 v162, v162\n"
"v_exp_f32 v163, v163\n"
"v_mfma_f32_16x16x32_bf16 v[156:159], v[212:215], a[20:23], v[156:159]\n"
"v_cvt_pk_bf16_f32 v140, v160, v161\n"
"v_cvt_pk_bf16_f32 v141, v162, v163\n"
"v_mfma_f32_16x16x32_bf16 v[160:163], v[208:211], a[32:35], v[188:191]\n"
"v_exp_f32 v164, v164\n"
"v_mfma_f32_16x16x32_bf16 v[160:163], v[212:215], a[36:39], v[160:163]\n"
"v_exp_f32 v165, v165\n"
"v_mfma_f32_16x16x32_bf16 v[160:163], v[216:219], a[40:43], v[160:163]\n"
"ds_read_b64_tr_b16 a[244:245], v234 offset:0\n"
"ds_read_b64_tr_b16 a[246:247], v234 offset:4096\n"
"v_exp_f32 v166, v166\n"
"v_exp_f32 v167, v167\n"
"v_mfma_f32_16x16x32_bf16 v[160:163], v[220:223], a[44:47], v[160:163]\n"
"v_cvt_pk_bf16_f32 v148, v164, v165\n"
"v_cvt_pk_bf16_f32 v149, v166, v167\n"
"v_mfma_f32_16x16x32_bf16 v[164:167], v[216:219], a[200:203], v[192:195]\n"
"ds_read_b64_tr_b16 a[248:249], v238 offset:0\n"
"ds_read_b64_tr_b16 a[250:251], v238 offset:4096\n"
"v_mfma_f32_16x16x32_bf16 v[164:167], v[220:223], a[204:207], v[164:167]\n"
"v_exp_f32 v168, v168\n"
"v_mfma_f32_16x16x32_bf16 v[164:167], v[208:211], a[192:195], v[164:167]\n"
"s_add_u32 s40, s56, s52\n"
"v_exp_f32 v169, v169\n"
"v_mfma_f32_16x16x32_bf16 v[164:167], v[212:215], a[196:199], v[164:167]\n"
"s_waitcnt lgkmcnt(2)\n"
"buffer_load_dwordx4 v240, s[20:23], s40 offen offset:1024 nt lds\n"
"v_cvt_pk_bf16_f32 v9, v10, v11\n"
"v_cvt_pk_bf16_f32 v10, v12, v13\n"
"v_cvt_pk_bf16_f32 v11, v14, v15\n"
"v_exp_f32 v170, v170\n"
"v_exp_f32 v171, v171\n"
"v_mfma_f32_16x16x32_bf16 a[216:219], v[124:127], a[244:247], 0\n"
"v_permlane16_swap_b32 v8, v10\n"
"v_permlane16_swap_b32 v9, v11\n"
"v_accvgpr_write_b32 a148, v8\n"
"v_accvgpr_write_b32 a149, v9\n"
"v_accvgpr_write_b32 a150, v10\n"
"v_accvgpr_write_b32 a151, v11\n"
"v_cvt_pk_bf16_f32 v134, v168, v169\n"
"v_cvt_pk_bf16_f32 v135, v170, v171\n"
"v_mfma_f32_16x16x32_bf16 v[8:11], v[128:131], a[244:247], 0\n"
"s_waitcnt lgkmcnt(0)\n"
"ds_read_b64_tr_b16 v[224:225], v239 offset:0\n"
"ds_read_b64_tr_b16 v[226:227], v239 offset:4096\n"
"v_cvt_pk_bf16_f32 v40, v40, v41\n"
"v_cvt_pk_bf16_f32 v41, v42, v43\n"
"v_cvt_pk_bf16_f32 v42, v44, v45\n"
"v_cvt_pk_bf16_f32 v43, v46, v47\n"
"v_exp_f32 v172, v172\n"
"v_exp_f32 v173, v173\n"
"v_exp_f32 v174, v174\n"
"v_exp_f32 v175, v175\n"
"v_mfma_f32_16x16x32_bf16 v[8:11], v[132:135], a[248:251], v[8:11]\n"
"ds_read_b64_tr_b16 a[252:253], v235 offset:0\n"
"ds_read_b64_tr_b16 a[254:255], v235 offset:4096\n"
"v_permlane16_swap_b32 v40, v42\n"
"v_permlane16_swap_b32 v41, v43\n"
"v_accvgpr_write_b32 a164, v40\n"
"v_accvgpr_write_b32 a165, v41\n"
"v_accvgpr_write_b32 a166, v42\n"
"v_accvgpr_write_b32 a167, v43\n"
"v_cvt_pk_bf16_f32 v142, v172, v173\n"
"v_cvt_pk_bf16_f32 v143, v174, v175\n"
"v_mfma_f32_16x16x32_bf16 v[40:43], v[136:139], a[244:247], 0\n"
"v_cvt_pk_bf16_f32 v72, v72, v73\n"
"v_cvt_pk_bf16_f32 v73, v74, v75\n"
"v_cvt_pk_bf16_f32 v74, v76, v77\n"
"v_cvt_pk_bf16_f32 v75, v78, v79\n"
"v_exp_f32 v176, v176\n"
"v_exp_f32 v177, v177\n"
"v_exp_f32 v178, v178\n"
"v_exp_f32 v179, v179\n"
"v_mfma_f32_16x16x32_bf16 v[40:43], v[140:143], a[248:251], v[40:43]\n"
"v_permlane16_swap_b32 v72, v74\n"
"v_permlane16_swap_b32 v73, v75\n"
"v_accvgpr_write_b32 a180, v72\n"
"v_accvgpr_write_b32 a181, v73\n"
"v_accvgpr_write_b32 a182, v74\n"
"v_accvgpr_write_b32 a183, v75\n"
"v_cvt_pk_bf16_f32 v150, v176, v177\n"
"v_cvt_pk_bf16_f32 v151, v178, v179\n"
"v_mfma_f32_16x16x32_bf16 v[72:75], v[144:147], a[244:247], 0\n"
"ds_read_b128 v[216:219], v228 offset:21504\n"
"v_cvt_pk_bf16_f32 v200, v200, v201\n"
"v_mfma_f32_16x16x32_bf16 v[72:75], v[148:151], a[248:251], v[72:75]\n"
"s_waitcnt lgkmcnt(3)\n"
"ds_read_b128 v[220:223], v229 offset:21504\n"
"v_cvt_pk_bf16_f32 v201, v202, v203\n"
"v_mfma_f32_16x16x32_bf16 v[76:79], v[148:151], v[224:227], 0\n"
"s_waitcnt lgkmcnt(2)\n"
"ds_read_b128 v[208:211], v228 offset:20480\n"
"v_cvt_pk_bf16_f32 v202, v204, v205\n"
"v_mfma_f32_16x16x32_bf16 v[76:79], v[144:147], a[252:255], v[76:79]\n"
"ds_read_b128 v[212:215], v229 offset:20480\n"
"v_cvt_pk_bf16_f32 v203, v206, v207\n"
"v_mfma_f32_16x16x32_bf16 v[44:47], v[140:143], v[224:227], 0\n"
"s_add_u32 s40, s56, s53\n"
"ds_read_b64_tr_b16 a[244:245], v234 offset:1024\n"
"v_accvgpr_write_b32 a240, v254\n"
"v_mfma_f32_16x16x32_bf16 v[44:47], v[136:139], a[252:255], v[44:47]\n"
"buffer_load_dwordx4 v240, s[20:23], s40 offen offset:2048 nt lds\n"
"v_mfma_f32_16x16x32_bf16 v[12:15], v[132:135], v[224:227], 0\n"
"v_accvgpr_write_b32 a241, v254\n"
"v_accvgpr_write_b32 a242, v254\n"
"v_mfma_f32_16x16x32_bf16 v[12:15], v[128:131], a[252:255], v[12:15]\n"
"v_accvgpr_write_b32 a243, v254\n"
"v_mov_b32 v196, 0\n"
"v_mov_b32 v197, 0\n"
"v_mov_b32 v198, 0\n"
"v_mov_b32 v199, 0\n"
"v_mfma_f32_16x16x32_bf16 a[220:223], v[124:127], a[252:255], 0\n"
"v_permlane16_swap_b32 v200, v202\n"
"v_permlane16_swap_b32 v201, v203\n"
"v_accvgpr_write_b32 a142, v202\n"
"v_accvgpr_write_b32 a143, v203\n"
"v_mov_b32 v202, 0\n"
"v_mov_b32 v203, 0\n"
"v_mov_b32 v204, 0\n"
"v_mov_b32 v205, 0\n"
"v_smfmac_f32_16x16x64_bf16 v[196:199], a[240:243], v[128:135], v241\n"
"ds_read_b64_tr_b16 a[246:247], v234 offset:5120\n"
"v_cvt_pk_bf16_f32 v24, v24, v25\n"
"v_mfma_f32_16x16x32_bf16 v[202:205], a[240:243], v[124:127], v[202:205]\n"
"s_waitcnt lgkmcnt(3)\n"
"ds_read_b64_tr_b16 a[248:249], v238 offset:1024\n"
"v_cvt_pk_bf16_f32 v25, v26, v27\n"
"v_mfma_f32_16x16x32_bf16 v[168:171], v[216:219], a[8:11], v[180:183]\n"
"ds_read_b64_tr_b16 a[250:251], v238 offset:5120\n"
"v_cvt_pk_bf16_f32 v26, v28, v29\n"
"v_mfma_f32_16x16x32_bf16 v[168:171], v[220:223], a[12:15], v[168:171]\n"
"ds_read_b64_tr_b16 v[224:225], v239 offset:1024\n"
"v_cvt_pk_bf16_f32 v27, v30, v31\n"
"v_mfma_f32_16x16x32_bf16 v[168:171], v[208:211], a[0:3], v[168:171]\n"
"s_waitcnt lgkmcnt(4)\n"
"ds_read_b64_tr_b16 v[226:227], v239 offset:5120\n"
"v_cvt_pk_bf16_f32 v56, v56, v57\n"
"v_mfma_f32_16x16x32_bf16 v[168:171], v[212:215], a[4:7], v[168:171]\n"
"ds_read_b64_tr_b16 a[252:253], v235 offset:1024\n"
"v_cvt_pk_bf16_f32 v57, v58, v59\n"
"v_mfma_f32_16x16x32_bf16 v[172:175], v[208:211], a[16:19], v[184:187]\n"
"v_exp_f32 v152, v152\n"
"v_mfma_f32_16x16x32_bf16 v[172:175], v[212:215], a[20:23], v[172:175]\n"
"ds_read_b64_tr_b16 a[254:255], v235 offset:5120\n"
"v_cvt_pk_bf16_f32 v58, v60, v61\n"
"v_mfma_f32_16x16x32_bf16 v[172:175], v[216:219], a[24:27], v[172:175]\n"
"s_add_u32 s40, s56, s54\n"
"v_exp_f32 v153, v153\n"
"v_mfma_f32_16x16x32_bf16 v[172:175], v[220:223], a[28:31], v[172:175]\n"
"buffer_load_dwordx4 v240, s[20:23], s40 offen offset:3072 nt lds\n"
"v_mfma_f32_16x16x32_bf16 v[176:179], v[216:219], a[40:43], v[188:191]\n"
"s_add_u32 s55, s55, s59\n"
"v_exp_f32 v154, v154\n"
"v_mfma_f32_16x16x32_bf16 v[176:179], v[220:223], a[44:47], v[176:179]\n"
"s_add_u32 s41, s55, s57\n"
"v_exp_f32 v155, v155\n"
"v_mfma_f32_16x16x32_bf16 v[176:179], v[208:211], a[32:35], v[176:179]\n"
"s_and_b32 s41, s41, s58\n"
"v_cvt_pk_bf16_f32 v96, v152, v153\n"
"v_cvt_pk_bf16_f32 v97, v154, v155\n"
"v_mfma_f32_16x16x32_bf16 v[176:179], v[212:215], a[36:39], v[176:179]\n"
"s_andn2_b32 s56, s55, s58\n"
"v_permlane16_swap_b32 v24, v26\n"
"v_mfma_f32_16x16x32_bf16 v[152:155], v[208:211], a[192:195], v[192:195]\n"
"s_or_b32 s56, s56, s41\n"
"ds_read_b128 v[208:211], v230 offset:16384\n"
"v_accvgpr_write_b32 a156, v24\n"
"v_mfma_f32_16x16x32_bf16 v[152:155], v[212:215], a[196:199], v[152:155]\n"
"ds_read_b128 v[212:215], v231 offset:16384\n"
"v_accvgpr_write_b32 a158, v26\n"
"v_mfma_f32_16x16x32_bf16 v[152:155], v[216:219], a[200:203], v[152:155]\n"
"ds_read_b128 v[216:219], v230 offset:17408\n"
"v_cvt_pk_bf16_f32 v59, v62, v63\n"
"v_mfma_f32_16x16x32_bf16 v[152:155], v[220:223], a[204:207], v[152:155]\n"
"s_waitcnt lgkmcnt(3)\n"
"v_permlane16_swap_b32 v25, v27\n"
"v_mfma_f32_16x16x32_bf16 a[232:235], v[124:127], a[244:247], 0\n"
"v_accvgpr_write_b32 a157, v25\n"
"v_accvgpr_write_b32 a159, v27\n"
"v_mfma_f32_16x16x32_bf16 v[24:27], v[128:131], a[244:247], 0\n"
"s_add_u32 m0, s44, 49152\n"
"v_permlane16_swap_b32 v56, v58\n"
"v_mfma_f32_16x16x32_bf16 v[24:27], v[132:135], a[248:251], v[24:27]\n"
"buffer_load_dwordx4 v240, s[16:19], s56 offen nt lds\n"
"v_permlane16_swap_b32 v57, v59\n"
"v_accvgpr_write_b32 a172, v56\n"
"v_accvgpr_write_b32 a173, v57\n"
"v_accvgpr_write_b32 a174, v58\n"
"v_accvgpr_write_b32 a175, v59\n"
"v_mfma_f32_16x16x32_bf16 v[56:59], v[136:139], a[244:247], 0\n"
"v_cvt_pk_bf16_f32 v88, v88, v89\n"
"v_cvt_pk_bf16_f32 v89, v90, v91\n"
"v_cvt_pk_bf16_f32 v90, v92, v93\n"
"v_cvt_pk_bf16_f32 v91, v94, v95\n"
"v_mfma_f32_16x16x32_bf16 v[56:59], v[140:143], a[248:251], v[56:59]\n"
"v_permlane16_swap_b32 v88, v90\n"
"v_permlane16_swap_b32 v89, v91\n"
"v_accvgpr_write_b32 a188, v88\n"
"v_accvgpr_write_b32 a189, v89\n"
"v_accvgpr_write_b32 a190, v90\n"
"v_accvgpr_write_b32 a191, v91\n"
"v_mfma_f32_16x16x32_bf16 v[88:91], v[144:147], a[244:247], 0\n"
"v_exp_f32 v156, v156\n"
"v_mfma_f32_16x16x32_bf16 v[88:91], v[148:151], a[248:251], v[88:91]\n"
"v_accvgpr_write_b32 a140, v200\n"
"v_accvgpr_write_b32 a141, v201\n"
"v_mfma_f32_16x16x32_bf16 v[92:95], v[148:151], v[224:227], 0\n"
"v_mov_b32 v200, 0\n"
"v_mov_b32 v201, 0\n"
"v_mfma_f32_16x16x32_bf16 v[92:95], v[144:147], a[252:255], v[92:95]\n"
"v_exp_f32 v157, v157\n"
"v_mfma_f32_16x16x32_bf16 v[60:63], v[140:143], v[224:227], 0\n"
"v_exp_f32 v158, v158\n"
"v_mfma_f32_16x16x32_bf16 v[60:63], v[136:139], a[252:255], v[60:63]\n"
"v_exp_f32 v159, v159\n"
"v_mfma_f32_16x16x32_bf16 v[28:31], v[132:135], v[224:227], 0\n"
"v_cvt_pk_bf16_f32 v104, v156, v157\n"
"v_cvt_pk_bf16_f32 v105, v158, v159\n"
"v_mfma_f32_16x16x32_bf16 v[28:31], v[128:131], a[252:255], v[28:31]\n"
"ds_read_b128 v[220:223], v231 offset:17408\n"
"v_accvgpr_write_b32 a96, v112\n"
"v_mfma_f32_16x16x32_bf16 a[236:239], v[124:127], a[252:255], 0\n"
"s_add_u32 s40, s56, s52\n"
"v_exp_f32 v160, v160\n"
"v_smfmac_f32_16x16x64_bf16 v[198:201], a[240:243], v[136:143], v241\n"
"s_waitcnt lgkmcnt(1)\n"
"buffer_load_dwordx4 v240, s[16:19], s40 offen offset:1024 nt lds\n"
"v_mfma_f32_16x16x32_bf16 v[156:159], v[208:211], a[0:3], v[180:183]\n"
"v_exp_f32 v161, v161\n"
"v_mfma_f32_16x16x32_bf16 v[156:159], v[212:215], a[4:7], v[156:159]\n"
"v_exp_f32 v162, v162\n"
"v_mfma_f32_16x16x32_bf16 v[156:159], v[216:219], a[8:11], v[156:159]\n"
"s_waitcnt lgkmcnt(0)\n"
"v_exp_f32 v163, v163\n"
"v_mfma_f32_16x16x32_bf16 v[156:159], v[220:223], a[12:15], v[156:159]\n"
"v_accvgpr_write_b32 a97, v113\n"
"v_cvt_pk_bf16_f32 v112, v160, v161\n"
"v_cvt_pk_bf16_f32 v113, v162, v163\n"
"v_mfma_f32_16x16x32_bf16 v[160:163], v[216:219], a[24:27], v[184:187]\n"
"v_exp_f32 v164, v164\n"
"v_mfma_f32_16x16x32_bf16 v[160:163], v[220:223], a[28:31], v[160:163]\n"
"v_exp_f32 v165, v165\n"
"v_mfma_f32_16x16x32_bf16 v[160:163], v[208:211], a[16:19], v[160:163]\n"
"ds_read_b64_tr_b16 a[244:245], v232 offset:0\n"
"ds_read_b64_tr_b16 a[246:247], v232 offset:4096\n"
"v_exp_f32 v166, v166\n"
"v_exp_f32 v167, v167\n"
"v_mfma_f32_16x16x32_bf16 v[160:163], v[212:215], a[20:23], v[160:163]\n"
"ds_read_b64_tr_b16 a[248:249], v236 offset:0\n"
"v_accvgpr_write_b32 a100, v120\n"
"v_accvgpr_write_b32 a101, v121\n"
"v_cvt_pk_bf16_f32 v120, v164, v165\n"
"v_cvt_pk_bf16_f32 v121, v166, v167\n"
"v_mfma_f32_16x16x32_bf16 v[164:167], v[208:211], a[32:35], v[188:191]\n"
"ds_read_b64_tr_b16 a[250:251], v236 offset:4096\n"
"v_cvt_pk_bf16_f32 v0, v0, v1\n"
"v_mfma_f32_16x16x32_bf16 v[164:167], v[212:215], a[36:39], v[164:167]\n"
"ds_read_b64_tr_b16 v[224:225], v237 offset:0\n"
"v_cvt_pk_bf16_f32 v1, v2, v3\n"
"v_mfma_f32_16x16x32_bf16 v[164:167], v[216:219], a[40:43], v[164:167]\n"
"ds_read_b64_tr_b16 v[226:227], v237 offset:4096\n"
"v_cvt_pk_bf16_f32 v2, v4, v5\n"
"v_mfma_f32_16x16x32_bf16 v[164:167], v[220:223], a[44:47], v[164:167]\n"
"s_waitcnt lgkmcnt(4)\n"
"v_cvt_pk_bf16_f32 v3, v6, v7\n"
"v_cvt_pk_bf16_f32 v32, v32, v33\n"
"v_mfma_f32_16x16x32_bf16 a[208:211], v[124:127], a[244:247], 0\n"
"v_permlane16_swap_b32 v0, v2\n"
"v_permlane16_swap_b32 v1, v3\n"
"v_accvgpr_write_b32 a144, v0\n"
"v_accvgpr_write_b32 a145, v1\n"
"v_accvgpr_write_b32 a146, v2\n"
"v_accvgpr_write_b32 a147, v3\n"
"v_mfma_f32_16x16x32_bf16 v[0:3], v[128:131], a[244:247], 0\n"
"s_waitcnt lgkmcnt(2)\n"
"s_add_u32 s40, s56, s53\n"
"ds_read_b64_tr_b16 a[252:253], v233 offset:0\n"
"ds_read_b64_tr_b16 a[254:255], v233 offset:4096\n"
"v_cvt_pk_bf16_f32 v33, v34, v35\n"
"v_cvt_pk_bf16_f32 v34, v36, v37\n"
"v_cvt_pk_bf16_f32 v35, v38, v39\n"
"v_mfma_f32_16x16x32_bf16 v[0:3], v[132:135], a[248:251], v[0:3]\n"
"buffer_load_dwordx4 v240, s[16:19], s40 offen offset:2048 nt lds\n"
"v_permlane16_swap_b32 v32, v34\n"
"v_permlane16_swap_b32 v33, v35\n"
"v_accvgpr_write_b32 a160, v32\n"
"v_accvgpr_write_b32 a161, v33\n"
"v_accvgpr_write_b32 a162, v34\n"
"v_accvgpr_write_b32 a163, v35\n"
"v_mfma_f32_16x16x32_bf16 v[32:35], v[136:139], a[244:247], 0\n"
"v_cvt_pk_bf16_f32 v64, v64, v65\n"
"v_cvt_pk_bf16_f32 v65, v66, v67\n"
"v_cvt_pk_bf16_f32 v66, v68, v69\n"
"v_cvt_pk_bf16_f32 v67, v70, v71\n"
"v_mfma_f32_16x16x32_bf16 v[32:35], v[140:143], a[248:251], v[32:35]\n"
"v_permlane16_swap_b32 v64, v66\n"
"v_permlane16_swap_b32 v65, v67\n"
"v_accvgpr_write_b32 a176, v64\n"
"v_accvgpr_write_b32 a177, v65\n"
"v_accvgpr_write_b32 a178, v66\n"
"v_accvgpr_write_b32 a179, v67\n"
"v_mfma_f32_16x16x32_bf16 v[64:67], v[144:147], a[244:247], 0\n"
"v_exp_f32 v168, v168\n"
"v_mfma_f32_16x16x32_bf16 v[64:67], v[148:151], a[248:251], v[64:67]\n"
"s_waitcnt lgkmcnt(2)\n"
"v_exp_f32 v169, v169\n"
"v_mfma_f32_16x16x32_bf16 v[68:71], v[148:151], v[224:227], 0\n"
"s_waitcnt lgkmcnt(0)\n"
"v_exp_f32 v170, v170\n"
"v_mfma_f32_16x16x32_bf16 v[68:71], v[144:147], a[252:255], v[68:71]\n"
"ds_read_b128 v[208:211], v230 offset:20480\n"
"v_cvt_pk_bf16_f32 v98, v168, v169\n"
"v_mfma_f32_16x16x32_bf16 v[36:39], v[140:143], v[224:227], 0\n"
"ds_read_b128 v[212:215], v231 offset:20480\n"
"v_accvgpr_write_b32 a98, v114\n"
"v_mfma_f32_16x16x32_bf16 v[36:39], v[136:139], a[252:255], v[36:39]\n"
"ds_read_b128 v[216:219], v230 offset:21504\n"
"v_accvgpr_write_b32 a99, v115\n"
"v_mfma_f32_16x16x32_bf16 v[4:7], v[132:135], v[224:227], 0\n"
"s_add_u32 s40, s56, s54\n"
"ds_read_b128 v[220:223], v231 offset:21504\n"
"v_cvt_pk_bf16_f32 v16, v16, v17\n"
"v_mfma_f32_16x16x32_bf16 v[4:7], v[128:131], a[252:255], v[4:7]\n"
"buffer_load_dwordx4 v240, s[16:19], s40 offen offset:3072 nt lds\n"
"v_mfma_f32_16x16x32_bf16 a[212:215], v[124:127], a[252:255], 0\n"
"v_exp_f32 v171, v171\n"
"v_smfmac_f32_16x16x64_bf16 v[200:203], a[240:243], v[144:151], v241\n"
"s_waitcnt lgkmcnt(3)\n"
"v_cvt_pk_bf16_f32 v99, v170, v171\n"
"v_cvt_pk_bf16_f32 v17, v18, v19\n"
"v_mfma_f32_16x16x32_bf16 v[168:171], v[208:211], a[0:3], v[180:183]\n"
"s_waitcnt lgkmcnt(2)\n"
"v_exp_f32 v172, v172\n"
"v_mfma_f32_16x16x32_bf16 v[168:171], v[212:215], a[4:7], v[168:171]\n"
"s_waitcnt lgkmcnt(1)\n"
"v_exp_f32 v173, v173\n"
"v_mfma_f32_16x16x32_bf16 v[168:171], v[216:219], a[8:11], v[168:171]\n"
"s_waitcnt lgkmcnt(0)\n"
"v_exp_f32 v174, v174\n"
"v_exp_f32 v175, v175\n"
"v_mfma_f32_16x16x32_bf16 v[168:171], v[220:223], a[12:15], v[168:171]\n"
"v_cvt_pk_bf16_f32 v106, v172, v173\n"
"v_cvt_pk_bf16_f32 v107, v174, v175\n"
"v_mfma_f32_16x16x32_bf16 v[172:175], v[216:219], a[24:27], v[184:187]\n"
"v_exp_f32 v176, v176\n"
"v_mfma_f32_16x16x32_bf16 v[172:175], v[220:223], a[28:31], v[172:175]\n"
"v_exp_f32 v177, v177\n"
"v_mfma_f32_16x16x32_bf16 v[172:175], v[208:211], a[16:19], v[172:175]\n"
"ds_read_b64_tr_b16 a[244:245], v232 offset:1024\n"
"ds_read_b64_tr_b16 a[246:247], v232 offset:5120\n"
"v_exp_f32 v178, v178\n"
"v_exp_f32 v179, v179\n"
"v_mfma_f32_16x16x32_bf16 v[172:175], v[212:215], a[20:23], v[172:175]\n"
"s_cbranch_vccnz LPCE1_%=\nLPKE1_%=:\n"
"ds_read_b64_tr_b16 a[248:249], v236 offset:1024\n"
"v_cvt_pk_bf16_f32 v114, v176, v177\n"
"v_cvt_pk_bf16_f32 v115, v178, v179\n"
"v_mfma_f32_16x16x32_bf16 v[176:179], v[208:211], a[32:35], v[188:191]\n"
"ds_read_b64_tr_b16 a[250:251], v236 offset:5120\n"
"v_cvt_pk_bf16_f32 v18, v20, v21\n"
"v_mfma_f32_16x16x32_bf16 v[176:179], v[212:215], a[36:39], v[176:179]\n"
"s_nop 0\n"
"v_permlane16_swap_b32 v16, v18\n"
"v_mfma_f32_16x16x32_bf16 v[176:179], v[216:219], a[40:43], v[176:179]\n"
"v_cvt_pk_bf16_f32 v19, v22, v23\n"
"v_accvgpr_write_b32 a152, v16\n"
"v_mfma_f32_16x16x32_bf16 v[176:179], v[220:223], a[44:47], v[176:179]\n"
"s_waitcnt lgkmcnt(2)\n"
"v_permlane16_swap_b32 v17, v19\n"
"v_mfma_f32_16x16x32_bf16 a[224:227], v[124:127], a[244:247], 0\n"
"ds_read_b64_tr_b16 v[224:225], v237 offset:1024\n"
"ds_read_b64_tr_b16 v[226:227], v237 offset:5120\n"
"v_accvgpr_write_b32 a153, v17\n"
"v_accvgpr_write_b32 a154, v18\n"
"v_accvgpr_write_b32 a155, v19\n"
"v_mfma_f32_16x16x32_bf16 v[16:19], v[128:131], a[244:247], 0\n"
"s_waitcnt lgkmcnt(2)\n"
"s_waitcnt vmcnt(8)\n"
"ds_read_b64_tr_b16 a[252:253], v233 offset:1024\n"
"ds_read_b64_tr_b16 a[254:255], v233 offset:5120\n"
"v_cvt_pk_bf16_f32 v48, v48, v49\n"
"v_cvt_pk_bf16_f32 v49, v50, v51\n"
"v_cvt_pk_bf16_f32 v50, v52, v53\n"
"v_cvt_pk_bf16_f32 v51, v54, v55\n"
"v_mfma_f32_16x16x32_bf16 v[16:19], v[132:135], a[248:251], v[16:19]\n"
"s_barrier\n"
"v_permlane16_swap_b32 v48, v50\n"
"v_permlane16_swap_b32 v49, v51\n"
"v_accvgpr_write_b32 a168, v48\n"
"v_accvgpr_write_b32 a169, v49\n"
"v_accvgpr_write_b32 a170, v50\n"
"v_accvgpr_write_b32 a171, v51\n"
"v_mfma_f32_16x16x32_bf16 v[48:51], v[136:139], a[244:247], 0\n"
"v_cvt_pk_bf16_f32 v80, v80, v81\n"
"v_cvt_pk_bf16_f32 v81, v82, v83\n"
"v_cvt_pk_bf16_f32 v82, v84, v85\n"
"v_cvt_pk_bf16_f32 v83, v86, v87\n"
"v_mfma_f32_16x16x32_bf16 v[48:51], v[140:143], a[248:251], v[48:51]\n"
"v_permlane16_swap_b32 v80, v82\n"
"v_permlane16_swap_b32 v81, v83\n"
"v_accvgpr_write_b32 a184, v80\n"
"v_accvgpr_write_b32 a185, v81\n"
"v_accvgpr_write_b32 a186, v82\n"
"v_accvgpr_write_b32 a187, v83\n"
"v_mfma_f32_16x16x32_bf16 v[80:83], v[144:147], a[244:247], 0\n"
"v_exp_f32 v152, v152\n"
"v_mfma_f32_16x16x32_bf16 v[80:83], v[148:151], a[248:251], v[80:83]\n"
"s_waitcnt lgkmcnt(2)\n"
"v_exp_f32 v153, v153\n"
"v_mfma_f32_16x16x32_bf16 v[84:87], v[148:151], v[224:227], 0\n"
"s_waitcnt lgkmcnt(0)\n"
"ds_read_b128 v[208:211], v228 offset:32768\n"
"v_accvgpr_write_b32 a102, v122\n"
"v_mfma_f32_16x16x32_bf16 v[84:87], v[144:147], a[252:255], v[84:87]\n"
"ds_read_b128 v[212:215], v229 offset:32768\n"
"v_accvgpr_write_b32 a103, v123\n"
"v_mfma_f32_16x16x32_bf16 v[52:55], v[140:143], v[224:227], 0\n"
"ds_read_b128 v[216:219], v228 offset:33792\n"
"v_bfe_u32 v254, v242, 6, 4\n"
"v_mfma_f32_16x16x32_bf16 v[52:55], v[136:139], a[252:255], v[52:55]\n"
"ds_read_b128 v[220:223], v229 offset:33792\n"
"v_xor_b32 v249, v249, v254\n"
"v_mfma_f32_16x16x32_bf16 v[20:23], v[132:135], v[224:227], 0\n"
"ds_read_b64_tr_b16 a[244:245], v234 offset:16384\n"
"ds_read_b64_tr_b16 a[246:247], v234 offset:20480\n"
"v_mfma_f32_16x16x32_bf16 v[20:23], v[128:131], a[252:255], v[20:23]\n"
"ds_read_b64_tr_b16 a[248:249], v238 offset:16384\n"
"ds_read_b64_tr_b16 a[250:251], v238 offset:20480\n"
"v_exp_f32 v154, v154\n"
"v_exp_f32 v155, v155\n"
"v_exp_f32 v156, v156\n"
"v_exp_f32 v157, v157\n"
"v_exp_f32 v158, v158\n"
"v_exp_f32 v159, v159\n"
"v_exp_f32 v162, v162\n"
"v_exp_f32 v163, v163\n"
"v_exp_f32 v166, v166\n"
"v_exp_f32 v167, v167\n"
"v_mfma_f32_16x16x32_bf16 a[228:231], v[124:127], a[252:255], 0\n"
"s_waitcnt lgkmcnt(7)\n"
"ds_read_b64_tr_b16 v[226:227], v239 offset:20480\n"
"v_lshl_add_u32 v249, v249, 4, s44\n"
"v_cvt_pk_bf16_f32 v122, v152, v153\n"
"v_cvt_pk_bf16_f32 v123, v154, v155\n"
"v_cvt_pk_bf16_f32 v100, v156, v157\n"
"v_cvt_pk_bf16_f32 v101, v158, v159\n"
"v_exp_f32 v160, v160\n"
"v_exp_f32 v161, v161\n"
"v_cvt_pk_bf16_f32 v109, v162, v163\n"
"v_exp_f32 v164, v164\n"
"v_exp_f32 v165, v165\n"
"v_cvt_pk_bf16_f32 v117, v166, v167\n"
"v_exp_f32 v169, v169\n"
"v_exp_f32 v173, v173\n"
"v_exp_f32 v175, v175\n"
"v_exp_f32 v178, v178\n"
"s_branch LAY2_%=\n"
"LAL_%=:\n"
"v_mfma_f32_16x16x32_bf16 a[224:227], v[124:127], a[244:247], a[224:227]\n"
"v_exp_f32 v169, v169\n"
"v_mfma_f32_16x16x32_bf16 v[16:19], v[128:131], a[244:247], v[16:19]\n"
"s_waitcnt vmcnt(8)\n"
"v_exp_f32 v175, v175\n"
"v_mfma_f32_16x16x32_bf16 v[16:19], v[132:135], a[248:251], v[16:19]\n"
"s_barrier\n"
"ds_read_b128 v[220:223], v229 offset:1024\n"
"v_mfma_f32_16x16x32_bf16 v[48:51], v[136:139], a[244:247], v[48:51]\n"
"ds_read_b128 v[216:219], v228 offset:1024\n"
"v_cvt_pk_bf16_f32 v109, v162, v163\n"
"v_mfma_f32_16x16x32_bf16 v[48:51], v[140:143], a[248:251], v[48:51]\n"
"ds_read_b128 v[212:215], v229 offset:0\n"
"v_mfma_f32_16x16x32_bf16 v[80:83], v[144:147], a[244:247], v[80:83]\n"
"ds_read_b128 v[208:211], v228 offset:0\n"
"v_cvt_pk_bf16_f32 v101, v158, v159\n"
"v_mfma_f32_16x16x32_bf16 v[80:83], v[148:151], a[248:251], v[80:83]\n"
"s_waitcnt lgkmcnt(4)\n"
"v_exp_f32 v178, v178\n"
"v_mfma_f32_16x16x32_bf16 v[84:87], v[148:151], v[224:227], v[84:87]\n"
"ds_read_b64_tr_b16 a[244:245], v234 offset:49152\n"
"v_cvt_pk_bf16_f32 v117, v166, v167\n"
"v_mfma_f32_16x16x32_bf16 v[84:87], v[144:147], a[252:255], v[84:87]\n"
"ds_read_b64_tr_b16 a[250:251], v238 offset:53248\n"
"v_cvt_pk_bf16_f32 v123, v154, v155\n"
"v_mfma_f32_16x16x32_bf16 v[52:55], v[140:143], v[224:227], v[52:55]\n"
"v_exp_f32 v173, v173\n"
"v_mfma_f32_16x16x32_bf16 v[52:55], v[136:139], a[252:255], v[52:55]\n"
"ds_read_b64_tr_b16 a[248:249], v238 offset:49152\n"
"v_mfma_f32_16x16x32_bf16 v[20:23], v[132:135], v[224:227], v[20:23]\n"
"v_exp_f32 v156, v156\n"
"v_mfma_f32_16x16x32_bf16 v[20:23], v[128:131], a[252:255], v[20:23]\n"
"ds_read_b64_tr_b16 v[226:227], v239 offset:53248\n"
"v_cvt_pk_bf16_f32 v100, v156, v157\n"
"v_mfma_f32_16x16x32_bf16 a[228:231], v[124:127], a[252:255], a[228:231]\n"
"s_waitcnt lgkmcnt(4)\n"
"ds_read_b64_tr_b16 a[246:247], v234 offset:53248\n"
"v_mfma_f32_16x16x32_bf16 v[152:155], v[208:211], a[0:3], v[180:183]\n"
"ds_read_b64_tr_b16 v[224:225], v239 offset:49152\n"
"v_cvt_pk_bf16_f32 v116, v164, v165\n"
"v_mfma_f32_16x16x32_bf16 v[152:155], v[212:215], a[4:7], v[152:155]\n"
"ds_read_b64_tr_b16 a[252:253], v235 offset:49152\n"
"v_cvt_pk_bf16_f32 v108, v160, v161\n"
"v_mfma_f32_16x16x32_bf16 v[152:155], v[216:219], a[8:11], v[152:155]\n"
"s_add_u32 m0, s44, 81920\n"
"ds_read_b64_tr_b16 a[254:255], v235 offset:53248\n"
"v_mfma_f32_16x16x32_bf16 v[152:155], v[220:223], a[12:15], v[152:155]\n"
"buffer_load_dwordx4 v240, s[20:23], s56 offen nt lds\n"
"v_mfma_f32_16x16x32_bf16 v[156:159], v[216:219], a[24:27], v[184:187]\n"
"v_exp_f32 v177, v177\n"
"v_mfma_f32_16x16x32_bf16 v[156:159], v[220:223], a[28:31], v[156:159]\n"
"v_exp_f32 v168, v168\n"
"v_mfma_f32_16x16x32_bf16 v[156:159], v[208:211], a[16:19], v[156:159]\n"
"v_exp_f32 v179, v179\n"
"v_mfma_f32_16x16x32_bf16 v[156:159], v[212:215], a[20:23], v[156:159]\n"
"v_exp_f32 v174, v174\n"
"v_mfma_f32_16x16x32_bf16 v[160:163], v[208:211], a[32:35], v[188:191]\n"
"v_exp_f32 v172, v172\n"
"v_mfma_f32_16x16x32_bf16 v[160:163], v[212:215], a[36:39], v[160:163]\n"
"v_exp_f32 v171, v171\n"
"v_mfma_f32_16x16x32_bf16 v[160:163], v[216:219], a[40:43], v[160:163]\n"
"v_exp_f32 v154, v154\n"
"v_mfma_f32_16x16x32_bf16 v[160:163], v[220:223], a[44:47], v[160:163]\n"
"v_exp_f32 v170, v170\n"
"v_mfma_f32_16x16x32_bf16 v[164:167], v[216:219], a[200:203], v[192:195]\n"
"ds_read_b128 v[216:219], v228 offset:5120\n"
"v_cvt_pk_bf16_f32 v103, v170, v171\n"
"v_mfma_f32_16x16x32_bf16 v[164:167], v[220:223], a[204:207], v[164:167]\n"
"v_cvt_pk_bf16_f32 v102, v168, v169\n"
"v_cvt_pk_bf16_f32 v119, v178, v179\n"
"v_mfma_f32_16x16x32_bf16 v[164:167], v[208:211], a[192:195], v[164:167]\n"
"s_add_u32 s40, s56, s52\n"
"ds_read_b128 v[208:211], v228 offset:4096\n"
"v_mfma_f32_16x16x32_bf16 v[164:167], v[212:215], a[196:199], v[164:167]\n"
"s_waitcnt lgkmcnt(2)\n"
"buffer_load_dwordx4 v240, s[20:23], s40 offen offset:1024 nt lds\n"
"v_mfma_f32_16x16x32_bf16 a[216:219], v[120:123], a[244:247], a[216:219]\n"
"v_cvt_pk_bf16_f32 v110, v172, v173\n"
"v_cvt_pk_bf16_f32 v111, v174, v175\n"
"v_mfma_f32_16x16x32_bf16 v[8:11], v[96:99], a[244:247], v[8:11]\n"
"v_exp_f32 v176, v176\n"
"v_mfma_f32_16x16x32_bf16 v[8:11], v[100:103], a[248:251], v[8:11]\n"
"v_exp_f32 v155, v155\n"
"v_mfma_f32_16x16x32_bf16 v[40:43], v[104:107], a[244:247], v[40:43]\n"
"v_exp_f32 v152, v152\n"
"v_mfma_f32_16x16x32_bf16 v[40:43], v[108:111], a[248:251], v[40:43]\n"
"ds_read_b128 v[212:215], v229 offset:4096\n"
"v_cvt_pk_bf16_f32 v118, v176, v177\n"
"v_mfma_f32_16x16x32_bf16 v[72:75], v[112:115], a[244:247], v[72:75]\n"
"v_exp_f32 v158, v158\n"
"v_mfma_f32_16x16x32_bf16 v[72:75], v[116:119], a[248:251], v[72:75]\n"
"v_exp_f32 v163, v163\n"
"v_mfma_f32_16x16x32_bf16 v[76:79], v[116:119], v[224:227], v[76:79]\n"
"ds_read_b128 v[220:223], v229 offset:5120\n"
"v_mfma_f32_16x16x32_bf16 v[76:79], v[112:115], a[252:255], v[76:79]\n"
"v_exp_f32 v159, v159\n"
"v_mfma_f32_16x16x32_bf16 v[44:47], v[108:111], v[224:227], v[44:47]\n"
"s_add_u32 s40, s56, s53\n"
"ds_read_b64_tr_b16 a[244:245], v234 offset:50176\n"
"v_mfma_f32_16x16x32_bf16 v[44:47], v[104:107], a[252:255], v[44:47]\n"
"buffer_load_dwordx4 v240, s[20:23], s40 offen offset:2048 nt lds\n"
"v_mfma_f32_16x16x32_bf16 v[12:15], v[100:103], v[224:227], v[12:15]\n"
"v_exp_f32 v153, v153\n"
"v_mfma_f32_16x16x32_bf16 v[12:15], v[96:99], a[252:255], v[12:15]\n"
"v_exp_f32 v165, v165\n"
"v_mfma_f32_16x16x32_bf16 a[220:223], v[120:123], a[252:255], a[220:223]\n"
"v_exp_f32 v164, v164\n"
"v_smfmac_f32_16x16x64_bf16 v[196:199], a[240:243], v[96:103], v241\n"
"v_cvt_pk_bf16_f32 v129, v154, v155\n"
"v_mfma_f32_16x16x32_bf16 v[202:205], a[240:243], v[120:123], v[202:205]\n"
"s_waitcnt lgkmcnt(1)\n"
"ds_read_b64_tr_b16 a[254:255], v235 offset:54272\n"
"v_mfma_f32_16x16x32_bf16 v[168:171], v[216:219], a[8:11], v[180:183]\n"
"ds_read_b64_tr_b16 a[248:249], v238 offset:50176\n"
"v_mfma_f32_16x16x32_bf16 v[168:171], v[220:223], a[12:15], v[168:171]\n"
"ds_read_b64_tr_b16 a[246:247], v234 offset:54272\n"
"v_mfma_f32_16x16x32_bf16 v[168:171], v[208:211], a[0:3], v[168:171]\n"
"ds_read_b64_tr_b16 v[224:225], v239 offset:50176\n"
"v_cvt_pk_bf16_f32 v124, v164, v165\n"
"v_mfma_f32_16x16x32_bf16 v[168:171], v[212:215], a[4:7], v[168:171]\n"
"v_exp_f32 v166, v166\n"
"v_mfma_f32_16x16x32_bf16 v[172:175], v[208:211], a[16:19], v[184:187]\n"
"ds_read_b64_tr_b16 a[252:253], v235 offset:50176\n"
"v_cvt_pk_bf16_f32 v128, v152, v153\n"
"v_mfma_f32_16x16x32_bf16 v[172:175], v[212:215], a[20:23], v[172:175]\n"
"ds_read_b64_tr_b16 v[226:227], v239 offset:54272\n"
"v_cvt_pk_bf16_f32 v137, v158, v159\n"
"v_mfma_f32_16x16x32_bf16 v[172:175], v[216:219], a[24:27], v[172:175]\n"
"s_add_u32 s40, s56, s54\n"
"ds_read_b64_tr_b16 a[250:251], v238 offset:54272\n"
"v_mfma_f32_16x16x32_bf16 v[172:175], v[220:223], a[28:31], v[172:175]\n"
"buffer_load_dwordx4 v240, s[20:23], s40 offen offset:3072 nt lds\n"
"v_mfma_f32_16x16x32_bf16 v[176:179], v[216:219], a[40:43], v[188:191]\n"
"s_add_u32 s55, s55, s59\n"
"v_exp_f32 v167, v167\n"
"v_mfma_f32_16x16x32_bf16 v[176:179], v[220:223], a[44:47], v[176:179]\n"
"s_add_u32 s41, s55, s57\n"
"v_exp_f32 v161, v161\n"
"v_mfma_f32_16x16x32_bf16 v[176:179], v[208:211], a[32:35], v[176:179]\n"
"s_and_b32 s41, s41, s58\n"
"v_exp_f32 v157, v157\n"
"v_mfma_f32_16x16x32_bf16 v[176:179], v[212:215], a[36:39], v[176:179]\n"
"s_andn2_b32 s56, s55, s58\n"
"v_exp_f32 v160, v160\n"
"v_mfma_f32_16x16x32_bf16 v[152:155], v[208:211], a[192:195], v[192:195]\n"
"s_or_b32 s56, s56, s41\n"
"ds_read_b128 v[208:211], v230 offset:0\n"
"v_mfma_f32_16x16x32_bf16 v[152:155], v[212:215], a[196:199], v[152:155]\n"
"v_exp_f32 v172, v172\n"
"v_mfma_f32_16x16x32_bf16 v[152:155], v[216:219], a[200:203], v[152:155]\n"
"ds_read_b128 v[216:219], v230 offset:1024\n"
"v_mfma_f32_16x16x32_bf16 v[152:155], v[220:223], a[204:207], v[152:155]\n"
"s_waitcnt lgkmcnt(2)\n"
"v_exp_f32 v162, v162\n"
"v_mfma_f32_16x16x32_bf16 a[232:235], v[120:123], a[244:247], a[232:235]\n"
"ds_read_b128 v[212:215], v231 offset:0\n"
"v_mfma_f32_16x16x32_bf16 v[24:27], v[96:99], a[244:247], v[24:27]\n"
"s_add_u32 m0, s44, 32768\n"
"ds_read_b128 v[220:223], v231 offset:1024\n"
"v_mfma_f32_16x16x32_bf16 v[24:27], v[100:103], a[248:251], v[24:27]\n"
"buffer_load_dwordx4 v240, s[16:19], s56 offen nt lds\n"
"v_mfma_f32_16x16x32_bf16 v[56:59], v[104:107], a[244:247], v[56:59]\n"
"v_exp_f32 v171, v171\n"
"v_mfma_f32_16x16x32_bf16 v[56:59], v[108:111], a[248:251], v[56:59]\n"
"v_exp_f32 v169, v169\n"
"v_mfma_f32_16x16x32_bf16 v[88:91], v[112:115], a[244:247], v[88:91]\n"
"v_exp_f32 v154, v154\n"
"v_mfma_f32_16x16x32_bf16 v[88:91], v[116:119], a[248:251], v[88:91]\n"
"v_exp_f32 v156, v156\n"
"v_mfma_f32_16x16x32_bf16 v[92:95], v[116:119], v[224:227], v[92:95]\n"
"ds_read_b64_tr_b16 a[244:245], v232 offset:49152\n"
"v_cvt_pk_bf16_f32 v145, v162, v163\n"
"v_mfma_f32_16x16x32_bf16 v[92:95], v[112:115], a[252:255], v[92:95]\n"
"ds_read_b64_tr_b16 a[250:251], v236 offset:53248\n"
"v_cvt_pk_bf16_f32 v136, v156, v157\n"
"v_mfma_f32_16x16x32_bf16 v[60:63], v[108:111], v[224:227], v[60:63]\n"
"ds_read_b64_tr_b16 a[246:247], v232 offset:53248\n"
"v_cvt_pk_bf16_f32 v144, v160, v161\n"
"v_mfma_f32_16x16x32_bf16 v[60:63], v[104:107], a[252:255], v[60:63]\n"
"v_exp_f32 v152, v152\n"
"v_mfma_f32_16x16x32_bf16 v[28:31], v[100:103], v[224:227], v[28:31]\n"
"ds_read_b64_tr_b16 v[224:225], v237 offset:49152\n"
"ds_read_b64_tr_b16 v[226:227], v237 offset:53248\n"
"v_mfma_f32_16x16x32_bf16 v[28:31], v[96:99], a[252:255], v[28:31]\n"
"ds_read_b64_tr_b16 a[248:249], v236 offset:49152\n"
"v_cvt_pk_bf16_f32 v125, v166, v167\n"
"v_mfma_f32_16x16x32_bf16 a[236:239], v[120:123], a[252:255], a[236:239]\n"
"s_add_u32 s40, s56, s52\n"
"ds_read_b64_tr_b16 a[252:253], v233 offset:49152\n"
"v_smfmac_f32_16x16x64_bf16 v[198:201], a[240:243], v[104:111], v241\n"
"s_waitcnt lgkmcnt(7)\n"
"buffer_load_dwordx4 v240, s[16:19], s40 offen offset:1024 nt lds\n"
"v_mfma_f32_16x16x32_bf16 v[156:159], v[208:211], a[0:3], v[180:183]\n"
"v_exp_f32 v175, v175\n"
"v_mfma_f32_16x16x32_bf16 v[156:159], v[212:215], a[4:7], v[156:159]\n"
"v_exp_f32 v173, v173\n"
"v_mfma_f32_16x16x32_bf16 v[156:159], v[216:219], a[8:11], v[156:159]\n"
"v_exp_f32 v168, v168\n"
"v_mfma_f32_16x16x32_bf16 v[156:159], v[220:223], a[12:15], v[156:159]\n"
"ds_read_b64_tr_b16 a[254:255], v233 offset:53248\n"
"v_mfma_f32_16x16x32_bf16 v[160:163], v[216:219], a[24:27], v[184:187]\n"
"v_exp_f32 v170, v170\n"
"v_mfma_f32_16x16x32_bf16 v[160:163], v[220:223], a[28:31], v[160:163]\n"
"v_exp_f32 v178, v178\n"
"v_mfma_f32_16x16x32_bf16 v[160:163], v[208:211], a[16:19], v[160:163]\n"
"v_exp_f32 v177, v177\n"
"v_mfma_f32_16x16x32_bf16 v[160:163], v[212:215], a[20:23], v[160:163]\n"
"v_exp_f32 v176, v176\n"
"v_mfma_f32_16x16x32_bf16 v[164:167], v[208:211], a[32:35], v[188:191]\n"
"ds_read_b128 v[208:211], v230 offset:4096\n"
"v_cvt_pk_bf16_f32 v131, v170, v171\n"
"v_mfma_f32_16x16x32_bf16 v[164:167], v[212:215], a[36:39], v[164:167]\n"
"ds_read_b128 v[212:215], v231 offset:4096\n"
"v_mfma_f32_16x16x32_bf16 v[164:167], v[216:219], a[40:43], v[164:167]\n"
"v_exp_f32 v153, v153\n"
"v_mfma_f32_16x16x32_bf16 v[164:167], v[220:223], a[44:47], v[164:167]\n"
"s_waitcnt lgkmcnt(2)\n"
"ds_read_b128 v[220:223], v231 offset:5120\n"
"v_cvt_pk_bf16_f32 v138, v172, v173\n"
"v_mfma_f32_16x16x32_bf16 a[208:211], v[120:123], a[244:247], a[208:211]\n"
"v_exp_f32 v174, v174\n"
"v_mfma_f32_16x16x32_bf16 v[0:3], v[96:99], a[244:247], v[0:3]\n"
"s_add_u32 s40, s56, s53\n"
"ds_read_b128 v[216:219], v230 offset:5120\n"
"v_mfma_f32_16x16x32_bf16 v[0:3], v[100:103], a[248:251], v[0:3]\n"
"buffer_load_dwordx4 v240, s[16:19], s40 offen offset:2048 nt lds\n"
"v_mfma_f32_16x16x32_bf16 v[32:35], v[104:107], a[244:247], v[32:35]\n"
"v_exp_f32 v163, v163\n"
"v_mfma_f32_16x16x32_bf16 v[32:35], v[108:111], a[248:251], v[32:35]\n"
"v_exp_f32 v161, v161\n"
"v_mfma_f32_16x16x32_bf16 v[64:67], v[112:115], a[244:247], v[64:67]\n"
"v_exp_f32 v155, v155\n"
"v_mfma_f32_16x16x32_bf16 v[64:67], v[116:119], a[248:251], v[64:67]\n"
"v_exp_f32 v166, v166\n"
"v_mfma_f32_16x16x32_bf16 v[68:71], v[116:119], v[224:227], v[68:71]\n"
"ds_read_b64_tr_b16 a[250:251], v236 offset:54272\n"
"v_cvt_pk_bf16_f32 v139, v174, v175\n"
"v_mfma_f32_16x16x32_bf16 v[68:71], v[112:115], a[252:255], v[68:71]\n"
"ds_read_b64_tr_b16 a[246:247], v232 offset:54272\n"
"v_cvt_pk_bf16_f32 v130, v168, v169\n"
"v_mfma_f32_16x16x32_bf16 v[36:39], v[108:111], v[224:227], v[36:39]\n"
"v_exp_f32 v179, v179\n"
"v_mfma_f32_16x16x32_bf16 v[36:39], v[104:107], a[252:255], v[36:39]\n"
"ds_read_b64_tr_b16 a[244:245], v232 offset:50176\n"
"v_cvt_pk_bf16_f32 v126, v152, v153\n"
"v_mfma_f32_16x16x32_bf16 v[4:7], v[100:103], v[224:227], v[4:7]\n"
"s_add_u32 s40, s56, s54\n"
"ds_read_b64_tr_b16 a[248:249], v236 offset:50176\n"
"v_mfma_f32_16x16x32_bf16 v[4:7], v[96:99], a[252:255], v[4:7]\n"
"buffer_load_dwordx4 v240, s[16:19], s40 offen offset:3072 nt lds\n"
"v_mfma_f32_16x16x32_bf16 a[212:215], v[120:123], a[252:255], a[212:215]\n"
"v_exp_f32 v162, v162\n"
"v_smfmac_f32_16x16x64_bf16 v[200:203], a[240:243], v[112:119], v241\n"
"s_waitcnt lgkmcnt(4)\n"
"v_cvt_pk_bf16_f32 v146, v176, v177\n"
"v_mfma_f32_16x16x32_bf16 v[168:171], v[208:211], a[0:3], v[180:183]\n"
"v_exp_f32 v157, v157\n"
"v_mfma_f32_16x16x32_bf16 v[168:171], v[212:215], a[4:7], v[168:171]\n"
"v_exp_f32 v165, v165\n"
"v_mfma_f32_16x16x32_bf16 v[168:171], v[216:219], a[8:11], v[168:171]\n"
"ds_read_b64_tr_b16 a[254:255], v233 offset:54272\n"
"ds_read_b64_tr_b16 v[226:227], v237 offset:54272\n"
"v_mfma_f32_16x16x32_bf16 v[168:171], v[220:223], a[12:15], v[168:171]\n"
"ds_read_b64_tr_b16 a[252:253], v233 offset:50176\n"
"v_cvt_pk_bf16_f32 v147, v178, v179\n"
"v_mfma_f32_16x16x32_bf16 v[172:175], v[216:219], a[24:27], v[184:187]\n"
"ds_read_b64_tr_b16 v[224:225], v237 offset:50176\n"
"v_mfma_f32_16x16x32_bf16 v[172:175], v[220:223], a[28:31], v[172:175]\n"
"v_exp_f32 v160, v160\n"
"v_mfma_f32_16x16x32_bf16 v[172:175], v[208:211], a[16:19], v[172:175]\n"
"v_exp_f32 v158, v158\n"
"v_mfma_f32_16x16x32_bf16 v[172:175], v[212:215], a[20:23], v[172:175]\n"
"s_cbranch_vccnz LPCL0_%=\nLPKL0_%=:\n"
"v_mfma_f32_16x16x32_bf16 v[176:179], v[208:211], a[32:35], v[188:191]\n"
"v_exp_f32 v159, v159\n"
"v_mfma_f32_16x16x32_bf16 v[176:179], v[212:215], a[36:39], v[176:179]\n"
"v_exp_f32 v167, v167\n"
"v_mfma_f32_16x16x32_bf16 v[176:179], v[216:219], a[40:43], v[176:179]\n"
"v_exp_f32 v164, v164\n"
"v_mfma_f32_16x16x32_bf16 v[176:179], v[220:223], a[44:47], v[176:179]\n"
"s_waitcnt lgkmcnt(1)\n"
"v_mfma_f32_16x16x32_bf16 a[224:227], v[120:123], a[244:247], a[224:227]\n"
"v_exp_f32 v169, v169\n"
"v_mfma_f32_16x16x32_bf16 v[16:19], v[96:99], a[244:247], v[16:19]\n"
"s_waitcnt vmcnt(8)\n"
"v_exp_f32 v175, v175\n"
"v_mfma_f32_16x16x32_bf16 v[16:19], v[100:103], a[248:251], v[16:19]\n"
"s_barrier\n"
"ds_read_b128 v[220:223], v229 offset:17408\n"
"v_mfma_f32_16x16x32_bf16 v[48:51], v[104:107], a[244:247], v[48:51]\n"
"ds_read_b128 v[216:219], v228 offset:17408\n"
"v_cvt_pk_bf16_f32 v141, v162, v163\n"
"v_mfma_f32_16x16x32_bf16 v[48:51], v[108:111], a[248:251], v[48:51]\n"
"ds_read_b128 v[212:215], v229 offset:16384\n"
"v_mfma_f32_16x16x32_bf16 v[80:83], v[112:115], a[244:247], v[80:83]\n"
"ds_read_b128 v[208:211], v228 offset:16384\n"
"v_cvt_pk_bf16_f32 v133, v158, v159\n"
"v_mfma_f32_16x16x32_bf16 v[80:83], v[116:119], a[248:251], v[80:83]\n"
"s_waitcnt lgkmcnt(4)\n"
"v_exp_f32 v178, v178\n"
"v_mfma_f32_16x16x32_bf16 v[84:87], v[116:119], v[224:227], v[84:87]\n"
"ds_read_b64_tr_b16 a[244:245], v234 offset:0\n"
"v_cvt_pk_bf16_f32 v149, v166, v167\n"
"v_mfma_f32_16x16x32_bf16 v[84:87], v[112:115], a[252:255], v[84:87]\n"
"ds_read_b64_tr_b16 a[250:251], v238 offset:4096\n"
"v_cvt_pk_bf16_f32 v127, v154, v155\n"
"v_mfma_f32_16x16x32_bf16 v[52:55], v[108:111], v[224:227], v[52:55]\n"
"v_exp_f32 v173, v173\n"
"v_mfma_f32_16x16x32_bf16 v[52:55], v[104:107], a[252:255], v[52:55]\n"
"ds_read_b64_tr_b16 a[248:249], v238 offset:0\n"
"v_mfma_f32_16x16x32_bf16 v[20:23], v[100:103], v[224:227], v[20:23]\n"
"v_exp_f32 v156, v156\n"
"v_mfma_f32_16x16x32_bf16 v[20:23], v[96:99], a[252:255], v[20:23]\n"
"ds_read_b64_tr_b16 v[226:227], v239 offset:4096\n"
"v_cvt_pk_bf16_f32 v132, v156, v157\n"
"v_mfma_f32_16x16x32_bf16 a[228:231], v[120:123], a[252:255], a[228:231]\n"
"s_waitcnt lgkmcnt(4)\n"
"ds_read_b64_tr_b16 a[246:247], v234 offset:4096\n"
"v_mfma_f32_16x16x32_bf16 v[152:155], v[208:211], a[0:3], v[180:183]\n"
"ds_read_b64_tr_b16 v[224:225], v239 offset:0\n"
"v_cvt_pk_bf16_f32 v148, v164, v165\n"
"v_mfma_f32_16x16x32_bf16 v[152:155], v[212:215], a[4:7], v[152:155]\n"
"ds_read_b64_tr_b16 a[252:253], v235 offset:0\n"
"v_cvt_pk_bf16_f32 v140, v160, v161\n"
"v_mfma_f32_16x16x32_bf16 v[152:155], v[216:219], a[8:11], v[152:155]\n"
"s_add_u32 m0, s44, 98304\n"
"ds_read_b64_tr_b16 a[254:255], v235 offset:4096\n"
"v_mfma_f32_16x16x32_bf16 v[152:155], v[220:223], a[12:15], v[152:155]\n"
"buffer_load_dwordx4 v240, s[20:23], s56 offen nt lds\n"
"v_mfma_f32_16x16x32_bf16 v[156:159], v[216:219], a[24:27], v[184:187]\n"
"v_exp_f32 v177, v177\n"
"v_mfma_f32_16x16x32_bf16 v[156:159], v[220:223], a[28:31], v[156:159]\n"
"v_exp_f32 v168, v168\n"
"v_mfma_f32_16x16x32_bf16 v[156:159], v[208:211], a[16:19], v[156:159]\n"
"v_exp_f32 v179, v179\n"
"v_mfma_f32_16x16x32_bf16 v[156:159], v[212:215], a[20:23], v[156:159]\n"
"v_exp_f32 v174, v174\n"
"v_mfma_f32_16x16x32_bf16 v[160:163], v[208:211], a[32:35], v[188:191]\n"
"v_exp_f32 v172, v172\n"
"v_mfma_f32_16x16x32_bf16 v[160:163], v[212:215], a[36:39], v[160:163]\n"
"v_exp_f32 v171, v171\n"
"v_mfma_f32_16x16x32_bf16 v[160:163], v[216:219], a[40:43], v[160:163]\n"
"v_exp_f32 v154, v154\n"
"v_mfma_f32_16x16x32_bf16 v[160:163], v[220:223], a[44:47], v[160:163]\n"
"v_exp_f32 v170, v170\n"
"v_mfma_f32_16x16x32_bf16 v[164:167], v[216:219], a[200:203], v[192:195]\n"
"ds_read_b128 v[216:219], v228 offset:21504\n"
"v_cvt_pk_bf16_f32 v135, v170, v171\n"
"v_mfma_f32_16x16x32_bf16 v[164:167], v[220:223], a[204:207], v[164:167]\n"
"v_cvt_pk_bf16_f32 v134, v168, v169\n"
"v_cvt_pk_bf16_f32 v151, v178, v179\n"
"v_mfma_f32_16x16x32_bf16 v[164:167], v[208:211], a[192:195], v[164:167]\n"
"s_add_u32 s40, s56, s52\n"
"ds_read_b128 v[208:211], v228 offset:20480\n"
"v_mfma_f32_16x16x32_bf16 v[164:167], v[212:215], a[196:199], v[164:167]\n"
"s_waitcnt lgkmcnt(2)\n"
"buffer_load_dwordx4 v240, s[20:23], s40 offen offset:1024 nt lds\n"
"v_mfma_f32_16x16x32_bf16 a[216:219], v[124:127], a[244:247], a[216:219]\n"
"v_cvt_pk_bf16_f32 v142, v172, v173\n"
"v_cvt_pk_bf16_f32 v143, v174, v175\n"
"v_mfma_f32_16x16x32_bf16 v[8:11], v[128:131], a[244:247], v[8:11]\n"
"v_exp_f32 v176, v176\n"
"v_mfma_f32_16x16x32_bf16 v[8:11], v[132:135], a[248:251], v[8:11]\n"
"v_exp_f32 v155, v155\n"
"v_mfma_f32_16x16x32_bf16 v[40:43], v[136:139], a[244:247], v[40:43]\n"
"v_exp_f32 v152, v152\n"
"v_mfma_f32_16x16x32_bf16 v[40:43], v[140:143], a[248:251], v[40:43]\n"
"ds_read_b128 v[212:215], v229 offset:20480\n"
"v_cvt_pk_bf16_f32 v150, v176, v177\n"
"v_mfma_f32_16x16x32_bf16 v[72:75], v[144:147], a[244:247], v[72:75]\n"
"v_exp_f32 v158, v158\n"
"v_mfma_f32_16x16x32_bf16 v[72:75], v[148:151], a[248:251], v[72:75]\n"
"v_exp_f32 v163, v163\n"
"v_mfma_f32_16x16x32_bf16 v[76:79], v[148:151], v[224:227], v[76:79]\n"
"ds_read_b128 v[220:223], v229 offset:21504\n"
"v_mfma_f32_16x16x32_bf16 v[76:79], v[144:147], a[252:255], v[76:79]\n"
"v_exp_f32 v159, v159\n"
"v_mfma_f32_16x16x32_bf16 v[44:47], v[140:143], v[224:227], v[44:47]\n"
"s_add_u32 s40, s56, s53\n"
"ds_read_b64_tr_b16 a[244:245], v234 offset:1024\n"
"v_mfma_f32_16x16x32_bf16 v[44:47], v[136:139], a[252:255], v[44:47]\n"
"buffer_load_dwordx4 v240, s[20:23], s40 offen offset:2048 nt lds\n"
"v_mfma_f32_16x16x32_bf16 v[12:15], v[132:135], v[224:227], v[12:15]\n"
"v_exp_f32 v153, v153\n"
"v_mfma_f32_16x16x32_bf16 v[12:15], v[128:131], a[252:255], v[12:15]\n"
"v_exp_f32 v165, v165\n"
"v_mfma_f32_16x16x32_bf16 a[220:223], v[124:127], a[252:255], a[220:223]\n"
"v_exp_f32 v164, v164\n"
"v_smfmac_f32_16x16x64_bf16 v[196:199], a[240:243], v[128:135], v241\n"
"v_cvt_pk_bf16_f32 v97, v154, v155\n"
"v_mfma_f32_16x16x32_bf16 v[202:205], a[240:243], v[124:127], v[202:205]\n"
"s_waitcnt lgkmcnt(1)\n"
"ds_read_b64_tr_b16 a[254:255], v235 offset:5120\n"
"v_mfma_f32_16x16x32_bf16 v[168:171], v[216:219], a[8:11], v[180:183]\n"
"ds_read_b64_tr_b16 a[248:249], v238 offset:1024\n"
"v_mfma_f32_16x16x32_bf16 v[168:171], v[220:223], a[12:15], v[168:171]\n"
"ds_read_b64_tr_b16 a[246:247], v234 offset:5120\n"
"v_mfma_f32_16x16x32_bf16 v[168:171], v[208:211], a[0:3], v[168:171]\n"
"ds_read_b64_tr_b16 v[224:225], v239 offset:1024\n"
"v_cvt_pk_bf16_f32 v120, v164, v165\n"
"v_mfma_f32_16x16x32_bf16 v[168:171], v[212:215], a[4:7], v[168:171]\n"
"v_exp_f32 v166, v166\n"
"v_mfma_f32_16x16x32_bf16 v[172:175], v[208:211], a[16:19], v[184:187]\n"
"ds_read_b64_tr_b16 a[252:253], v235 offset:1024\n"
"v_cvt_pk_bf16_f32 v96, v152, v153\n"
"v_mfma_f32_16x16x32_bf16 v[172:175], v[212:215], a[20:23], v[172:175]\n"
"ds_read_b64_tr_b16 v[226:227], v239 offset:5120\n"
"v_cvt_pk_bf16_f32 v105, v158, v159\n"
"v_mfma_f32_16x16x32_bf16 v[172:175], v[216:219], a[24:27], v[172:175]\n"
"s_add_u32 s40, s56, s54\n"
"ds_read_b64_tr_b16 a[250:251], v238 offset:5120\n"
"v_mfma_f32_16x16x32_bf16 v[172:175], v[220:223], a[28:31], v[172:175]\n"
"buffer_load_dwordx4 v240, s[20:23], s40 offen offset:3072 nt lds\n"
"v_mfma_f32_16x16x32_bf16 v[176:179], v[216:219], a[40:43], v[188:191]\n"
"s_add_u32 s55, s55, s59\n"
"v_exp_f32 v167, v167\n"
"v_mfma_f32_16x16x32_bf16 v[176:179], v[220:223], a[44:47], v[176:179]\n"
"s_add_u32 s41, s55, s57\n"
"v_exp_f32 v161, v161\n"
"v_mfma_f32_16x16x32_bf16 v[176:179], v[208:211], a[32:35], v[176:179]\n"
"s_and_b32 s41, s41, s58\n"
"v_exp_f32 v157, v157\n"
"v_mfma_f32_16x16x32_bf16 v[176:179], v[212:215], a[36:39], v[176:179]\n"
"s_andn2_b32 s56, s55, s58\n"
"v_exp_f32 v160, v160\n"
"v_mfma_f32_16x16x32_bf16 v[152:155], v[208:211], a[192:195], v[192:195]\n"
"s_or_b32 s56, s56, s41\n"
"ds_read_b128 v[208:211], v230 offset:16384\n"
"v_mfma_f32_16x16x32_bf16 v[152:155], v[212:215], a[196:199], v[152:155]\n"
"v_exp_f32 v172, v172\n"
"v_mfma_f32_16x16x32_bf16 v[152:155], v[216:219], a[200:203], v[152:155]\n"
"ds_read_b128 v[216:219], v230 offset:17408\n"
"v_mfma_f32_16x16x32_bf16 v[152:155], v[220:223], a[204:207], v[152:155]\n"
"s_waitcnt lgkmcnt(2)\n"
"v_exp_f32 v162, v162\n"
"v_mfma_f32_16x16x32_bf16 a[232:235], v[124:127], a[244:247], a[232:235]\n"
"ds_read_b128 v[212:215], v231 offset:16384\n"
"v_mfma_f32_16x16x32_bf16 v[24:27], v[128:131], a[244:247], v[24:27]\n"
"s_add_u32 m0, s44, 49152\n"
"ds_read_b128 v[220:223], v231 offset:17408\n"
"v_mfma_f32_16x16x32_bf16 v[24:27], v[132:135], a[248:251], v[24:27]\n"
"buffer_load_dwordx4 v240, s[16:19], s56 offen nt lds\n"
"v_mfma_f32_16x16x32_bf16 v[56:59], v[136:139], a[244:247], v[56:59]\n"
"v_exp_f32 v171, v171\n"
"v_mfma_f32_16x16x32_bf16 v[56:59], v[140:143], a[248:251], v[56:59]\n"
"v_exp_f32 v169, v169\n"
"v_mfma_f32_16x16x32_bf16 v[88:91], v[144:147], a[244:247], v[88:91]\n"
"v_exp_f32 v154, v154\n"
"v_mfma_f32_16x16x32_bf16 v[88:91], v[148:151], a[248:251], v[88:91]\n"
"v_exp_f32 v156, v156\n"
"v_mfma_f32_16x16x32_bf16 v[92:95], v[148:151], v[224:227], v[92:95]\n"
"ds_read_b64_tr_b16 a[244:245], v232 offset:0\n"
"v_cvt_pk_bf16_f32 v113, v162, v163\n"
"v_mfma_f32_16x16x32_bf16 v[92:95], v[144:147], a[252:255], v[92:95]\n"
"ds_read_b64_tr_b16 a[250:251], v236 offset:4096\n"
"v_cvt_pk_bf16_f32 v104, v156, v157\n"
"v_mfma_f32_16x16x32_bf16 v[60:63], v[140:143], v[224:227], v[60:63]\n"
"ds_read_b64_tr_b16 a[246:247], v232 offset:4096\n"
"v_cvt_pk_bf16_f32 v112, v160, v161\n"
"v_mfma_f32_16x16x32_bf16 v[60:63], v[136:139], a[252:255], v[60:63]\n"
"v_exp_f32 v152, v152\n"
"v_mfma_f32_16x16x32_bf16 v[28:31], v[132:135], v[224:227], v[28:31]\n"
"ds_read_b64_tr_b16 v[224:225], v237 offset:0\n"
"ds_read_b64_tr_b16 v[226:227], v237 offset:4096\n"
"v_mfma_f32_16x16x32_bf16 v[28:31], v[128:131], a[252:255], v[28:31]\n"
"ds_read_b64_tr_b16 a[248:249], v236 offset:0\n"
"v_cvt_pk_bf16_f32 v121, v166, v167\n"
"v_mfma_f32_16x16x32_bf16 a[236:239], v[124:127], a[252:255], a[236:239]\n"
"s_add_u32 s40, s56, s52\n"
"ds_read_b64_tr_b16 a[252:253], v233 offset:0\n"
"v_smfmac_f32_16x16x64_bf16 v[198:201], a[240:243], v[136:143], v241\n"
"s_waitcnt lgkmcnt(7)\n"
"buffer_load_dwordx4 v240, s[16:19], s40 offen offset:1024 nt lds\n"
"v_mfma_f32_16x16x32_bf16 v[156:159], v[208:211], a[0:3], v[180:183]\n"
"v_exp_f32 v175, v175\n"
"v_mfma_f32_16x16x32_bf16 v[156:159], v[212:215], a[4:7], v[156:159]\n"
"v_exp_f32 v173, v173\n"
"v_mfma_f32_16x16x32_bf16 v[156:159], v[216:219], a[8:11], v[156:159]\n"
"v_exp_f32 v168, v168\n"
"v_mfma_f32_16x16x32_bf16 v[156:159], v[220:223], a[12:15], v[156:159]\n"
"ds_read_b64_tr_b16 a[254:255], v233 offset:4096\n"
"v_mfma_f32_16x16x32_bf16 v[160:163], v[216:219], a[24:27], v[184:187]\n"
"v_exp_f32 v170, v170\n"
"v_mfma_f32_16x16x32_bf16 v[160:163], v[220:223], a[28:31], v[160:163]\n"
"v_exp_f32 v178, v178\n"
"v_mfma_f32_16x16x32_bf16 v[160:163], v[208:211], a[16:19], v[160:163]\n"
"v_exp_f32 v177, v177\n"
"v_mfma_f32_16x16x32_bf16 v[160:163], v[212:215], a[20:23], v[160:163]\n"
"v_exp_f32 v176, v176\n"
"v_mfma_f32_16x16x32_bf16 v[164:167], v[208:211], a[32:35], v[188:191]\n"
"ds_read_b128 v[208:211], v230 offset:20480\n"
"v_cvt_pk_bf16_f32 v99, v170, v171\n"
"v_mfma_f32_16x16x32_bf16 v[164:167], v[212:215], a[36:39], v[164:167]\n"
"ds_read_b128 v[212:215], v231 offset:20480\n"
"v_mfma_f32_16x16x32_bf16 v[164:167], v[216:219], a[40:43], v[164:167]\n"
"v_exp_f32 v153, v153\n"
"v_mfma_f32_16x16x32_bf16 v[164:167], v[220:223], a[44:47], v[164:167]\n"
"s_waitcnt lgkmcnt(2)\n"
"ds_read_b128 v[220:223], v231 offset:21504\n"
"v_cvt_pk_bf16_f32 v106, v172, v173\n"
"v_mfma_f32_16x16x32_bf16 a[208:211], v[124:127], a[244:247], a[208:211]\n"
"v_exp_f32 v174, v174\n"
"v_mfma_f32_16x16x32_bf16 v[0:3], v[128:131], a[244:247], v[0:3]\n"
"s_add_u32 s40, s56, s53\n"
"ds_read_b128 v[216:219], v230 offset:21504\n"
"v_mfma_f32_16x16x32_bf16 v[0:3], v[132:135], a[248:251], v[0:3]\n"
"buffer_load_dwordx4 v240, s[16:19], s40 offen offset:2048 nt lds\n"
"v_mfma_f32_16x16x32_bf16 v[32:35], v[136:139], a[244:247], v[32:35]\n"
"v_exp_f32 v163, v163\n"
"v_mfma_f32_16x16x32_bf16 v[32:35], v[140:143], a[248:251], v[32:35]\n"
"v_exp_f32 v161, v161\n"
"v_mfma_f32_16x16x32_bf16 v[64:67], v[144:147], a[244:247], v[64:67]\n"
"v_exp_f32 v155, v155\n"
"v_mfma_f32_16x16x32_bf16 v[64:67], v[148:151], a[248:251], v[64:67]\n"
"v_exp_f32 v166, v166\n"
"v_mfma_f32_16x16x32_bf16 v[68:71], v[148:151], v[224:227], v[68:71]\n"
"ds_read_b64_tr_b16 a[250:251], v236 offset:5120\n"
"v_cvt_pk_bf16_f32 v107, v174, v175\n"
"v_mfma_f32_16x16x32_bf16 v[68:71], v[144:147], a[252:255], v[68:71]\n"
"ds_read_b64_tr_b16 a[246:247], v232 offset:5120\n"
"v_cvt_pk_bf16_f32 v98, v168, v169\n"
"v_mfma_f32_16x16x32_bf16 v[36:39], v[140:143], v[224:227], v[36:39]\n"
"v_exp_f32 v179, v179\n"
"v_mfma_f32_16x16x32_bf16 v[36:39], v[136:139], a[252:255], v[36:39]\n"
"ds_read_b64_tr_b16 a[244:245], v232 offset:1024\n"
"v_cvt_pk_bf16_f32 v122, v152, v153\n"
"v_mfma_f32_16x16x32_bf16 v[4:7], v[132:135], v[224:227], v[4:7]\n"
"s_add_u32 s40, s56, s54\n"
"ds_read_b64_tr_b16 a[248:249], v236 offset:1024\n"
"v_mfma_f32_16x16x32_bf16 v[4:7], v[128:131], a[252:255], v[4:7]\n"
"buffer_load_dwordx4 v240, s[16:19], s40 offen offset:3072 nt lds\n"
"v_mfma_f32_16x16x32_bf16 a[212:215], v[124:127], a[252:255], a[212:215]\n"
"v_exp_f32 v162, v162\n"
"v_smfmac_f32_16x16x64_bf16 v[200:203], a[240:243], v[144:151], v241\n"
"s_waitcnt lgkmcnt(4)\n"
"v_cvt_pk_bf16_f32 v114, v176, v177\n"
"v_mfma_f32_16x16x32_bf16 v[168:171], v[208:211], a[0:3], v[180:183]\n"
"v_exp_f32 v157, v157\n"
"v_mfma_f32_16x16x32_bf16 v[168:171], v[212:215], a[4:7], v[168:171]\n"
"v_exp_f32 v165, v165\n"
"v_mfma_f32_16x16x32_bf16 v[168:171], v[216:219], a[8:11], v[168:171]\n"
"ds_read_b64_tr_b16 a[254:255], v233 offset:5120\n"
"ds_read_b64_tr_b16 v[226:227], v237 offset:5120\n"
"v_mfma_f32_16x16x32_bf16 v[168:171], v[220:223], a[12:15], v[168:171]\n"
"ds_read_b64_tr_b16 a[252:253], v233 offset:1024\n"
"v_cvt_pk_bf16_f32 v115, v178, v179\n"
"v_mfma_f32_16x16x32_bf16 v[172:175], v[216:219], a[24:27], v[184:187]\n"
"ds_read_b64_tr_b16 v[224:225], v237 offset:1024\n"
"v_mfma_f32_16x16x32_bf16 v[172:175], v[220:223], a[28:31], v[172:175]\n"
"v_exp_f32 v160, v160\n"
"v_mfma_f32_16x16x32_bf16 v[172:175], v[208:211], a[16:19], v[172:175]\n"
"v_exp_f32 v158, v158\n"
"v_mfma_f32_16x16x32_bf16 v[172:175], v[212:215], a[20:23], v[172:175]\n"
"s_cbranch_vccnz LPCL1_%=\nLPKL1_%=:\n"
"v_mfma_f32_16x16x32_bf16 v[176:179], v[208:211], a[32:35], v[188:191]\n"
"v_exp_f32 v159, v159\n"
"v_mfma_f32_16x16x32_bf16 v[176:179], v[212:215], a[36:39], v[176:179]\n"
"v_exp_f32 v167, v167\n"
"v_mfma_f32_16x16x32_bf16 v[176:179], v[216:219], a[40:43], v[176:179]\n"
"v_exp_f32 v164, v164\n"
"v_mfma_f32_16x16x32_bf16 v[176:179], v[220:223], a[44:47], v[176:179]\n"
"s_waitcnt lgkmcnt(1)\n"
"v_mfma_f32_16x16x32_bf16 a[224:227], v[124:127], a[244:247], a[224:227]\n"
"v_exp_f32 v169, v169\n"
"v_mfma_f32_16x16x32_bf16 v[16:19], v[128:131], a[244:247], v[16:19]\n"
"s_waitcnt vmcnt(8)\n"
"v_exp_f32 v175, v175\n"
"v_mfma_f32_16x16x32_bf16 v[16:19], v[132:135], a[248:251], v[16:19]\n"
"s_barrier\n"
"ds_read_b128 v[220:223], v229 offset:33792\n"
"v_mfma_f32_16x16x32_bf16 v[48:51], v[136:139], a[244:247], v[48:51]\n"
"ds_read_b128 v[216:219], v228 offset:33792\n"
"v_cvt_pk_bf16_f32 v109, v162, v163\n"
"v_mfma_f32_16x16x32_bf16 v[48:51], v[140:143], a[248:251], v[48:51]\n"
"ds_read_b128 v[212:215], v229 offset:32768\n"
"v_mfma_f32_16x16x32_bf16 v[80:83], v[144:147], a[244:247], v[80:83]\n"
"ds_read_b128 v[208:211], v228 offset:32768\n"
"v_cvt_pk_bf16_f32 v101, v158, v159\n"
"v_mfma_f32_16x16x32_bf16 v[80:83], v[148:151], a[248:251], v[80:83]\n"
"s_waitcnt lgkmcnt(4)\n"
"v_exp_f32 v178, v178\n"
"v_mfma_f32_16x16x32_bf16 v[84:87], v[148:151], v[224:227], v[84:87]\n"
"ds_read_b64_tr_b16 a[244:245], v234 offset:16384\n"
"v_cvt_pk_bf16_f32 v117, v166, v167\n"
"v_mfma_f32_16x16x32_bf16 v[84:87], v[144:147], a[252:255], v[84:87]\n"
"ds_read_b64_tr_b16 a[250:251], v238 offset:20480\n"
"v_cvt_pk_bf16_f32 v123, v154, v155\n"
"v_mfma_f32_16x16x32_bf16 v[52:55], v[140:143], v[224:227], v[52:55]\n"
"v_exp_f32 v173, v173\n"
"v_mfma_f32_16x16x32_bf16 v[52:55], v[136:139], a[252:255], v[52:55]\n"
"ds_read_b64_tr_b16 a[248:249], v238 offset:16384\n"
"v_mfma_f32_16x16x32_bf16 v[20:23], v[132:135], v[224:227], v[20:23]\n"
"v_exp_f32 v156, v156\n"
"v_mfma_f32_16x16x32_bf16 v[20:23], v[128:131], a[252:255], v[20:23]\n"
"ds_read_b64_tr_b16 v[226:227], v239 offset:20480\n"
"v_cvt_pk_bf16_f32 v100, v156, v157\n"
"v_mfma_f32_16x16x32_bf16 a[228:231], v[124:127], a[252:255], a[228:231]\n"
"s_waitcnt lgkmcnt(4)\n"
"ds_read_b64_tr_b16 a[246:247], v234 offset:20480\n"
"LAY2_%=:\n"
"v_mfma_f32_16x16x32_bf16 v[152:155], v[208:211], a[0:3], v[180:183]\n"
"s_waitcnt lgkmcnt(7)\n"
"ds_read_b64_tr_b16 v[224:225], v239 offset:16384\n"
"v_cvt_pk_bf16_f32 v116, v164, v165\n"
"v_mfma_f32_16x16x32_bf16 v[152:155], v[212:215], a[4:7], v[152:155]\n"
"s_waitcnt lgkmcnt(7)\n"
"ds_read_b64_tr_b16 a[252:253], v235 offset:16384\n"
"v_cvt_pk_bf16_f32 v108, v160, v161\n"
"v_mfma_f32_16x16x32_bf16 v[152:155], v[216:219], a[8:11], v[152:155]\n"
"s_waitcnt lgkmcnt(7)\n"
"s_add_u32 m0, s44, 114688\n"
"ds_read_b64_tr_b16 a[254:255], v235 offset:20480\n"
"v_mfma_f32_16x16x32_bf16 v[152:155], v[220:223], a[12:15], v[152:155]\n"
"buffer_load_dwordx4 v240, s[20:23], s56 offen nt lds\n"
"v_mfma_f32_16x16x32_bf16 v[156:159], v[216:219], a[24:27], v[184:187]\n"
"v_exp_f32 v177, v177\n"
"v_mfma_f32_16x16x32_bf16 v[156:159], v[220:223], a[28:31], v[156:159]\n"
"v_exp_f32 v168, v168\n"
"v_mfma_f32_16x16x32_bf16 v[156:159], v[208:211], a[16:19], v[156:159]\n"
"v_exp_f32 v179, v179\n"
"v_mfma_f32_16x16x32_bf16 v[156:159], v[212:215], a[20:23], v[156:159]\n"
"v_exp_f32 v174, v174\n"
"v_mfma_f32_16x16x32_bf16 v[160:163], v[208:211], a[32:35], v[188:191]\n"
"v_exp_f32 v172, v172\n"
"v_mfma_f32_16x16x32_bf16 v[160:163], v[212:215], a[36:39], v[160:163]\n"
"v_exp_f32 v171, v171\n"
"v_mfma_f32_16x16x32_bf16 v[160:163], v[216:219], a[40:43], v[160:163]\n"
"v_exp_f32 v154, v154\n"
"v_mfma_f32_16x16x32_bf16 v[160:163], v[220:223], a[44:47], v[160:163]\n"
"v_exp_f32 v170, v170\n"
"v_mfma_f32_16x16x32_bf16 v[164:167], v[216:219], a[200:203], v[192:195]\n"
"ds_read_b128 v[216:219], v228 offset:37888\n"
"v_cvt_pk_bf16_f32 v103, v170, v171\n"
"v_mfma_f32_16x16x32_bf16 v[164:167], v[220:223], a[204:207], v[164:167]\n"
"v_cvt_pk_bf16_f32 v102, v168, v169\n"
"v_cvt_pk_bf16_f32 v119, v178, v179\n"
"v_mfma_f32_16x16x32_bf16 v[164:167], v[208:211], a[192:195], v[164:167]\n"
"s_add_u32 s40, s56, s52\n"
"ds_read_b128 v[208:211], v228 offset:36864\n"
"v_mfma_f32_16x16x32_bf16 v[164:167], v[212:215], a[196:199], v[164:167]\n"
"s_waitcnt lgkmcnt(2)\n"
"buffer_load_dwordx4 v240, s[20:23], s40 offen offset:1024 nt lds\n"
"v_mfma_f32_16x16x32_bf16 a[216:219], v[120:123], a[244:247], a[216:219]\n"
"v_cvt_pk_bf16_f32 v110, v172, v173\n"
"v_cvt_pk_bf16_f32 v111, v174, v175\n"
"v_mfma_f32_16x16x32_bf16 v[8:11], v[96:99], a[244:247], v[8:11]\n"
"v_exp_f32 v176, v176\n"
"v_mfma_f32_16x16x32_bf16 v[8:11], v[100:103], a[248:251], v[8:11]\n"
"v_exp_f32 v155, v155\n"
"v_mfma_f32_16x16x32_bf16 v[40:43], v[104:107], a[244:247], v[40:43]\n"
"v_exp_f32 v152, v152\n"
"v_mfma_f32_16x16x32_bf16 v[40:43], v[108:111], a[248:251], v[40:43]\n"
"ds_read_b128 v[212:215], v229 offset:36864\n"
"v_cvt_pk_bf16_f32 v118, v176, v177\n"
"v_mfma_f32_16x16x32_bf16 v[72:75], v[112:115], a[244:247], v[72:75]\n"
"v_exp_f32 v158, v158\n"
"v_mfma_f32_16x16x32_bf16 v[72:75], v[116:119], a[248:251], v[72:75]\n"
"v_exp_f32 v163, v163\n"
"v_mfma_f32_16x16x32_bf16 v[76:79], v[116:119], v[224:227], v[76:79]\n"
"ds_read_b128 v[220:223], v229 offset:37888\n"
"v_mfma_f32_16x16x32_bf16 v[76:79], v[112:115], a[252:255], v[76:79]\n"
"v_exp_f32 v159, v159\n"
"v_mfma_f32_16x16x32_bf16 v[44:47], v[108:111], v[224:227], v[44:47]\n"
"s_add_u32 s40, s56, s53\n"
"ds_read_b64_tr_b16 a[244:245], v234 offset:17408\n"
"v_mfma_f32_16x16x32_bf16 v[44:47], v[104:107], a[252:255], v[44:47]\n"
"buffer_load_dwordx4 v240, s[20:23], s40 offen offset:2048 nt lds\n"
"v_mfma_f32_16x16x32_bf16 v[12:15], v[100:103], v[224:227], v[12:15]\n"
"v_exp_f32 v153, v153\n"
"v_mfma_f32_16x16x32_bf16 v[12:15], v[96:99], a[252:255], v[12:15]\n"
"v_exp_f32 v165, v165\n"
"v_mfma_f32_16x16x32_bf16 a[220:223], v[120:123], a[252:255], a[220:223]\n"
"v_exp_f32 v164, v164\n"
"v_smfmac_f32_16x16x64_bf16 v[196:199], a[240:243], v[96:103], v241\n"
"v_cvt_pk_bf16_f32 v129, v154, v155\n"
"v_mfma_f32_16x16x32_bf16 v[202:205], a[240:243], v[120:123], v[202:205]\n"
"s_waitcnt lgkmcnt(1)\n"
"ds_read_b64_tr_b16 a[254:255], v235 offset:21504\n"
"v_mfma_f32_16x16x32_bf16 v[168:171], v[216:219], a[8:11], v[180:183]\n"
"ds_read_b64_tr_b16 a[248:249], v238 offset:17408\n"
"v_mfma_f32_16x16x32_bf16 v[168:171], v[220:223], a[12:15], v[168:171]\n"
"ds_read_b64_tr_b16 a[246:247], v234 offset:21504\n"
"v_mfma_f32_16x16x32_bf16 v[168:171], v[208:211], a[0:3], v[168:171]\n"
"ds_read_b64_tr_b16 v[224:225], v239 offset:17408\n"
"v_cvt_pk_bf16_f32 v124, v164, v165\n"
"v_mfma_f32_16x16x32_bf16 v[168:171], v[212:215], a[4:7], v[168:171]\n"
"v_exp_f32 v166, v166\n"
"v_mfma_f32_16x16x32_bf16 v[172:175], v[208:211], a[16:19], v[184:187]\n"
"ds_read_b64_tr_b16 a[252:253], v235 offset:17408\n"
"v_cvt_pk_bf16_f32 v128, v152, v153\n"
"v_mfma_f32_16x16x32_bf16 v[172:175], v[212:215], a[20:23], v[172:175]\n"
"ds_read_b64_tr_b16 v[226:227], v239 offset:21504\n"
"v_cvt_pk_bf16_f32 v137, v158, v159\n"
"v_mfma_f32_16x16x32_bf16 v[172:175], v[216:219], a[24:27], v[172:175]\n"
"s_add_u32 s40, s56, s54\n"
"ds_read_b64_tr_b16 a[250:251], v238 offset:21504\n"
"v_mfma_f32_16x16x32_bf16 v[172:175], v[220:223], a[28:31], v[172:175]\n"
"buffer_load_dwordx4 v240, s[20:23], s40 offen offset:3072 nt lds\n"
"v_mfma_f32_16x16x32_bf16 v[176:179], v[216:219], a[40:43], v[188:191]\n"
"s_add_u32 s55, s55, s59\n"
"v_exp_f32 v167, v167\n"
"v_mfma_f32_16x16x32_bf16 v[176:179], v[220:223], a[44:47], v[176:179]\n"
"s_add_u32 s41, s55, s57\n"
"v_exp_f32 v161, v161\n"
"v_mfma_f32_16x16x32_bf16 v[176:179], v[208:211], a[32:35], v[176:179]\n"
"s_and_b32 s41, s41, s58\n"
"v_exp_f32 v157, v157\n"
"v_mfma_f32_16x16x32_bf16 v[176:179], v[212:215], a[36:39], v[176:179]\n"
"s_andn2_b32 s56, s55, s58\n"
"v_exp_f32 v160, v160\n"
"v_mfma_f32_16x16x32_bf16 v[152:155], v[208:211], a[192:195], v[192:195]\n"
"s_or_b32 s56, s56, s41\n"
"ds_read_b128 v[208:211], v230 offset:32768\n"
"v_mfma_f32_16x16x32_bf16 v[152:155], v[212:215], a[196:199], v[152:155]\n"
"s_cmp_ge_u32 s47, 15\n"
"v_exp_f32 v172, v172\n"
"v_mfma_f32_16x16x32_bf16 v[152:155], v[216:219], a[200:203], v[152:155]\n"
"s_cbranch_scc1 LSW_%=\nLSWK_%=:\n"
"ds_read_b128 v[216:219], v230 offset:33792\n"
"v_mfma_f32_16x16x32_bf16 v[152:155], v[220:223], a[204:207], v[152:155]\n"
"s_waitcnt lgkmcnt(2)\n"
"v_exp_f32 v162, v162\n"
"v_mfma_f32_16x16x32_bf16 a[232:235], v[120:123], a[244:247], a[232:235]\n"
"ds_read_b128 v[212:215], v231 offset:32768\n"
"v_mfma_f32_16x16x32_bf16 v[24:27], v[96:99], a[244:247], v[24:27]\n"
"s_mov_b32 m0, s44\n"
"ds_read_b128 v[220:223], v231 offset:33792\n"
"v_mfma_f32_16x16x32_bf16 v[24:27], v[100:103], a[248:251], v[24:27]\n"
"buffer_load_dwordx4 v240, s[16:19], s56 offen nt lds\n"
"v_mfma_f32_16x16x32_bf16 v[56:59], v[104:107], a[244:247], v[56:59]\n"
"v_exp_f32 v171, v171\n"
"v_mfma_f32_16x16x32_bf16 v[56:59], v[108:111], a[248:251], v[56:59]\n"
"v_exp_f32 v169, v169\n"
"v_mfma_f32_16x16x32_bf16 v[88:91], v[112:115], a[244:247], v[88:91]\n"
"v_exp_f32 v154, v154\n"
"v_mfma_f32_16x16x32_bf16 v[88:91], v[116:119], a[248:251], v[88:91]\n"
"v_exp_f32 v156, v156\n"
"v_mfma_f32_16x16x32_bf16 v[92:95], v[116:119], v[224:227], v[92:95]\n"
"ds_read_b64_tr_b16 a[244:245], v232 offset:16384\n"
"v_cvt_pk_bf16_f32 v145, v162, v163\n"
"v_mfma_f32_16x16x32_bf16 v[92:95], v[112:115], a[252:255], v[92:95]\n"
"ds_read_b64_tr_b16 a[250:251], v236 offset:20480\n"
"v_cvt_pk_bf16_f32 v136, v156, v157\n"
"v_mfma_f32_16x16x32_bf16 v[60:63], v[108:111], v[224:227], v[60:63]\n"
"ds_read_b64_tr_b16 a[246:247], v232 offset:20480\n"
"v_cvt_pk_bf16_f32 v144, v160, v161\n"
"v_mfma_f32_16x16x32_bf16 v[60:63], v[104:107], a[252:255], v[60:63]\n"
"v_exp_f32 v152, v152\n"
"v_mfma_f32_16x16x32_bf16 v[28:31], v[100:103], v[224:227], v[28:31]\n"
"ds_read_b64_tr_b16 v[224:225], v237 offset:16384\n"
"ds_read_b64_tr_b16 v[226:227], v237 offset:20480\n"
"v_mfma_f32_16x16x32_bf16 v[28:31], v[96:99], a[252:255], v[28:31]\n"
"ds_read_b64_tr_b16 a[248:249], v236 offset:16384\n"
"v_cvt_pk_bf16_f32 v125, v166, v167\n"
"v_mfma_f32_16x16x32_bf16 a[236:239], v[120:123], a[252:255], a[236:239]\n"
"s_add_u32 s40, s56, s52\n"
"ds_read_b64_tr_b16 a[252:253], v233 offset:16384\n"
"v_smfmac_f32_16x16x64_bf16 v[198:201], a[240:243], v[104:111], v241\n"
"s_waitcnt lgkmcnt(7)\n"
"buffer_load_dwordx4 v240, s[16:19], s40 offen offset:1024 nt lds\n"
"v_mfma_f32_16x16x32_bf16 v[156:159], v[208:211], a[0:3], v[180:183]\n"
"v_exp_f32 v175, v175\n"
"v_mfma_f32_16x16x32_bf16 v[156:159], v[212:215], a[4:7], v[156:159]\n"
"v_exp_f32 v173, v173\n"
"v_mfma_f32_16x16x32_bf16 v[156:159], v[216:219], a[8:11], v[156:159]\n"
"v_exp_f32 v168, v168\n"
"v_mfma_f32_16x16x32_bf16 v[156:159], v[220:223], a[12:15], v[156:159]\n"
"ds_read_b64_tr_b16 a[254:255], v233 offset:20480\n"
"v_mfma_f32_16x16x32_bf16 v[160:163], v[216:219], a[24:27], v[184:187]\n"
"v_exp_f32 v170, v170\n"
"v_mfma_f32_16x16x32_bf16 v[160:163], v[220:223], a[28:31], v[160:163]\n"
"v_exp_f32 v178, v178\n"
"v_mfma_f32_16x16x32_bf16 v[160:163], v[208:211], a[16:19], v[160:163]\n"
"v_exp_f32 v177, v177\n"
"v_mfma_f32_16x16x32_bf16 v[160:163], v[212:215], a[20:23], v[160:163]\n"
"v_exp_f32 v176, v176\n"
"v_mfma_f32_16x16x32_bf16 v[164:167], v[208:211], a[32:35], v[188:191]\n"
"ds_read_b128 v[208:211], v230 offset:36864\n"
"v_cvt_pk_bf16_f32 v131, v170, v171\n"
"v_mfma_f32_16x16x32_bf16 v[164:167], v[212:215], a[36:39], v[164:167]\n"
"ds_read_b128 v[212:215], v231 offset:36864\n"
"v_mfma_f32_16x16x32_bf16 v[164:167], v[216:219], a[40:43], v[164:167]\n"
"v_exp_f32 v153, v153\n"
"v_mfma_f32_16x16x32_bf16 v[164:167], v[220:223], a[44:47], v[164:167]\n"
"s_waitcnt lgkmcnt(2)\n"
"ds_read_b128 v[220:223], v231 offset:37888\n"
"v_cvt_pk_bf16_f32 v138, v172, v173\n"
"v_mfma_f32_16x16x32_bf16 a[208:211], v[120:123], a[244:247], a[208:211]\n"
"v_exp_f32 v174, v174\n"
"v_mfma_f32_16x16x32_bf16 v[0:3], v[96:99], a[244:247], v[0:3]\n"
"s_add_u32 s40, s56, s53\n"
"ds_read_b128 v[216:219], v230 offset:37888\n"
"v_mfma_f32_16x16x32_bf16 v[0:3], v[100:103], a[248:251], v[0:3]\n"
"buffer_load_dwordx4 v240, s[16:19], s40 offen offset:2048 nt lds\n"
"v_mfma_f32_16x16x32_bf16 v[32:35], v[104:107], a[244:247], v[32:35]\n"
"v_exp_f32 v163, v163\n"
"v_mfma_f32_16x16x32_bf16 v[32:35], v[108:111], a[248:251], v[32:35]\n"
"v_exp_f32 v161, v161\n"
"v_mfma_f32_16x16x32_bf16 v[64:67], v[112:115], a[244:247], v[64:67]\n"
"v_exp_f32 v155, v155\n"
"v_mfma_f32_16x16x32_bf16 v[64:67], v[116:119], a[248:251], v[64:67]\n"
"v_exp_f32 v166, v166\n"
"v_mfma_f32_16x16x32_bf16 v[68:71], v[116:119], v[224:227], v[68:71]\n"
"ds_read_b64_tr_b16 a[250:251], v236 offset:21504\n"
"v_cvt_pk_bf16_f32 v139, v174, v175\n"
"v_mfma_f32_16x16x32_bf16 v[68:71], v[112:115], a[252:255], v[68:71]\n"
"ds_read_b64_tr_b16 a[246:247], v232 offset:21504\n"
"v_cvt_pk_bf16_f32 v130, v168, v169\n"
"v_mfma_f32_16x16x32_bf16 v[36:39], v[108:111], v[224:227], v[36:39]\n"
"v_exp_f32 v179, v179\n"
"v_mfma_f32_16x16x32_bf16 v[36:39], v[104:107], a[252:255], v[36:39]\n"
"ds_read_b64_tr_b16 a[244:245], v232 offset:17408\n"
"v_cvt_pk_bf16_f32 v126, v152, v153\n"
"v_mfma_f32_16x16x32_bf16 v[4:7], v[100:103], v[224:227], v[4:7]\n"
"s_add_u32 s40, s56, s54\n"
"ds_read_b64_tr_b16 a[248:249], v236 offset:17408\n"
"v_mfma_f32_16x16x32_bf16 v[4:7], v[96:99], a[252:255], v[4:7]\n"
"buffer_load_dwordx4 v240, s[16:19], s40 offen offset:3072 nt lds\n"
"v_mfma_f32_16x16x32_bf16 a[212:215], v[120:123], a[252:255], a[212:215]\n"
"v_exp_f32 v162, v162\n"
"v_smfmac_f32_16x16x64_bf16 v[200:203], a[240:243], v[112:119], v241\n"
"s_waitcnt lgkmcnt(4)\n"
"v_cvt_pk_bf16_f32 v146, v176, v177\n"
"v_mfma_f32_16x16x32_bf16 v[168:171], v[208:211], a[0:3], v[180:183]\n"
"v_exp_f32 v157, v157\n"
"v_mfma_f32_16x16x32_bf16 v[168:171], v[212:215], a[4:7], v[168:171]\n"
"v_exp_f32 v165, v165\n"
"v_mfma_f32_16x16x32_bf16 v[168:171], v[216:219], a[8:11], v[168:171]\n"
"ds_read_b64_tr_b16 a[254:255], v233 offset:21504\n"
"ds_read_b64_tr_b16 v[226:227], v237 offset:21504\n"
"v_mfma_f32_16x16x32_bf16 v[168:171], v[220:223], a[12:15], v[168:171]\n"
"ds_read_b64_tr_b16 a[252:253], v233 offset:17408\n"
"v_cvt_pk_bf16_f32 v147, v178, v179\n"
"v_mfma_f32_16x16x32_bf16 v[172:175], v[216:219], a[24:27], v[184:187]\n"
"ds_read_b64_tr_b16 v[224:225], v237 offset:17408\n"
"v_mfma_f32_16x16x32_bf16 v[172:175], v[220:223], a[28:31], v[172:175]\n"
"v_exp_f32 v160, v160\n"
"v_mfma_f32_16x16x32_bf16 v[172:175], v[208:211], a[16:19], v[172:175]\n"
"v_exp_f32 v158, v158\n"
"v_mfma_f32_16x16x32_bf16 v[172:175], v[212:215], a[20:23], v[172:175]\n"
"s_cbranch_vccnz LPCL2_%=\nLPKL2_%=:\n"
"v_mfma_f32_16x16x32_bf16 v[176:179], v[208:211], a[32:35], v[188:191]\n"
"v_exp_f32 v159, v159\n"
"v_mfma_f32_16x16x32_bf16 v[176:179], v[212:215], a[36:39], v[176:179]\n"
"v_exp_f32 v167, v167\n"
"v_mfma_f32_16x16x32_bf16 v[176:179], v[216:219], a[40:43], v[176:179]\n"
"v_exp_f32 v164, v164\n"
"v_mfma_f32_16x16x32_bf16 v[176:179], v[220:223], a[44:47], v[176:179]\n"
"s_waitcnt lgkmcnt(1)\n"
"v_mfma_f32_16x16x32_bf16 a[224:227], v[120:123], a[244:247], a[224:227]\n"
"v_exp_f32 v169, v169\n"
"v_mfma_f32_16x16x32_bf16 v[16:19], v[96:99], a[244:247], v[16:19]\n"
"s_waitcnt vmcnt(8)\n"
"v_exp_f32 v175, v175\n"
"v_mfma_f32_16x16x32_bf16 v[16:19], v[100:103], a[248:251], v[16:19]\n"
"s_barrier\n"
"ds_read_b128 v[220:223], v229 offset:50176\n"
"v_mfma_f32_16x16x32_bf16 v[48:51], v[104:107], a[244:247], v[48:51]\n"
"ds_read_b128 v[216:219], v228 offset:50176\n"
"v_cvt_pk_bf16_f32 v141, v162, v163\n"
"v_mfma_f32_16x16x32_bf16 v[48:51], v[108:111], a[248:251], v[48:51]\n"
"ds_read_b128 v[212:215], v229 offset:49152\n"
"v_mfma_f32_16x16x32_bf16 v[80:83], v[112:115], a[244:247], v[80:83]\n"
"ds_read_b128 v[208:211], v228 offset:49152\n"
"v_cvt_pk_bf16_f32 v133, v158, v159\n"
"v_mfma_f32_16x16x32_bf16 v[80:83], v[116:119], a[248:251], v[80:83]\n"
"s_waitcnt lgkmcnt(4)\n"
"v_exp_f32 v178, v178\n"
"v_mfma_f32_16x16x32_bf16 v[84:87], v[116:119], v[224:227], v[84:87]\n"
"ds_read_b64_tr_b16 a[244:245], v234 offset:32768\n"
"v_cvt_pk_bf16_f32 v149, v166, v167\n"
"v_mfma_f32_16x16x32_bf16 v[84:87], v[112:115], a[252:255], v[84:87]\n"
"ds_read_b64_tr_b16 a[250:251], v238 offset:36864\n"
"v_cvt_pk_bf16_f32 v127, v154, v155\n"
"v_mfma_f32_16x16x32_bf16 v[52:55], v[108:111], v[224:227], v[52:55]\n"
"v_exp_f32 v173, v173\n"
"v_mfma_f32_16x16x32_bf16 v[52:55], v[104:107], a[252:255], v[52:55]\n"
"ds_read_b64_tr_b16 a[248:249], v238 offset:32768\n"
"v_mfma_f32_16x16x32_bf16 v[20:23], v[100:103], v[224:227], v[20:23]\n"
"v_exp_f32 v156, v156\n"
"v_mfma_f32_16x16x32_bf16 v[20:23], v[96:99], a[252:255], v[20:23]\n"
"ds_read_b64_tr_b16 v[226:227], v239 offset:36864\n"
"v_cvt_pk_bf16_f32 v132, v156, v157\n"
"v_mfma_f32_16x16x32_bf16 a[228:231], v[120:123], a[252:255], a[228:231]\n"
"s_waitcnt lgkmcnt(4)\n"
"ds_read_b64_tr_b16 a[246:247], v234 offset:36864\n"
"v_mfma_f32_16x16x32_bf16 v[152:155], v[208:211], a[0:3], v[180:183]\n"
"ds_read_b64_tr_b16 v[224:225], v239 offset:32768\n"
"v_cvt_pk_bf16_f32 v148, v164, v165\n"
"v_mfma_f32_16x16x32_bf16 v[152:155], v[212:215], a[4:7], v[152:155]\n"
"ds_read_b64_tr_b16 a[252:253], v235 offset:32768\n"
"v_cvt_pk_bf16_f32 v140, v160, v161\n"
"v_mfma_f32_16x16x32_bf16 v[152:155], v[216:219], a[8:11], v[152:155]\n"
"s_add_u32 m0, s44, 65536\n"
"ds_read_b64_tr_b16 a[254:255], v235 offset:36864\n"
"v_mfma_f32_16x16x32_bf16 v[152:155], v[220:223], a[12:15], v[152:155]\n"
"buffer_load_dwordx4 v240, s[20:23], s56 offen nt lds\n"
"v_mfma_f32_16x16x32_bf16 v[156:159], v[216:219], a[24:27], v[184:187]\n"
"v_exp_f32 v177, v177\n"
"v_mfma_f32_16x16x32_bf16 v[156:159], v[220:223], a[28:31], v[156:159]\n"
"v_exp_f32 v168, v168\n"
"v_mfma_f32_16x16x32_bf16 v[156:159], v[208:211], a[16:19], v[156:159]\n"
"v_exp_f32 v179, v179\n"
"v_mfma_f32_16x16x32_bf16 v[156:159], v[212:215], a[20:23], v[156:159]\n"
"v_exp_f32 v174, v174\n"
"v_mfma_f32_16x16x32_bf16 v[160:163], v[208:211], a[32:35], v[188:191]\n"
"v_exp_f32 v172, v172\n"
"v_mfma_f32_16x16x32_bf16 v[160:163], v[212:215], a[36:39], v[160:163]\n"
"v_exp_f32 v171, v171\n"
"v_mfma_f32_16x16x32_bf16 v[160:163], v[216:219], a[40:43], v[160:163]\n"
"v_exp_f32 v154, v154\n"
"v_mfma_f32_16x16x32_bf16 v[160:163], v[220:223], a[44:47], v[160:163]\n"
"v_exp_f32 v170, v170\n"
"v_mfma_f32_16x16x32_bf16 v[164:167], v[216:219], a[200:203], v[192:195]\n"
"ds_read_b128 v[216:219], v228 offset:54272\n"
"v_cvt_pk_bf16_f32 v135, v170, v171\n"
"v_mfma_f32_16x16x32_bf16 v[164:167], v[220:223], a[204:207], v[164:167]\n"
"v_cvt_pk_bf16_f32 v134, v168, v169\n"
"v_cvt_pk_bf16_f32 v151, v178, v179\n"
"v_mfma_f32_16x16x32_bf16 v[164:167], v[208:211], a[192:195], v[164:167]\n"
"s_add_u32 s40, s56, s52\n"
"ds_read_b128 v[208:211], v228 offset:53248\n"
"v_mfma_f32_16x16x32_bf16 v[164:167], v[212:215], a[196:199], v[164:167]\n"
"s_waitcnt lgkmcnt(2)\n"
"buffer_load_dwordx4 v240, s[20:23], s40 offen offset:1024 nt lds\n"
"v_mfma_f32_16x16x32_bf16 a[216:219], v[124:127], a[244:247], a[216:219]\n"
"v_cvt_pk_bf16_f32 v142, v172, v173\n"
"v_cvt_pk_bf16_f32 v143, v174, v175\n"
"v_mfma_f32_16x16x32_bf16 v[8:11], v[128:131], a[244:247], v[8:11]\n"
"v_exp_f32 v176, v176\n"
"v_mfma_f32_16x16x32_bf16 v[8:11], v[132:135], a[248:251], v[8:11]\n"
"v_exp_f32 v155, v155\n"
"v_mfma_f32_16x16x32_bf16 v[40:43], v[136:139], a[244:247], v[40:43]\n"
"v_exp_f32 v152, v152\n"
"v_mfma_f32_16x16x32_bf16 v[40:43], v[140:143], a[248:251], v[40:43]\n"
"ds_read_b128 v[212:215], v229 offset:53248\n"
"v_cvt_pk_bf16_f32 v150, v176, v177\n"
"v_mfma_f32_16x16x32_bf16 v[72:75], v[144:147], a[244:247], v[72:75]\n"
"v_exp_f32 v158, v158\n"
"v_mfma_f32_16x16x32_bf16 v[72:75], v[148:151], a[248:251], v[72:75]\n"
"v_exp_f32 v163, v163\n"
"v_mfma_f32_16x16x32_bf16 v[76:79], v[148:151], v[224:227], v[76:79]\n"
"ds_read_b128 v[220:223], v229 offset:54272\n"
"v_mfma_f32_16x16x32_bf16 v[76:79], v[144:147], a[252:255], v[76:79]\n"
"v_exp_f32 v159, v159\n"
"v_mfma_f32_16x16x32_bf16 v[44:47], v[140:143], v[224:227], v[44:47]\n"
"s_add_u32 s40, s56, s53\n"
"ds_read_b64_tr_b16 a[244:245], v234 offset:33792\n"
"v_mfma_f32_16x16x32_bf16 v[44:47], v[136:139], a[252:255], v[44:47]\n"
"buffer_load_dwordx4 v240, s[20:23], s40 offen offset:2048 nt lds\n"
"v_mfma_f32_16x16x32_bf16 v[12:15], v[132:135], v[224:227], v[12:15]\n"
"v_exp_f32 v153, v153\n"
"v_mfma_f32_16x16x32_bf16 v[12:15], v[128:131], a[252:255], v[12:15]\n"
"v_exp_f32 v165, v165\n"
"v_mfma_f32_16x16x32_bf16 a[220:223], v[124:127], a[252:255], a[220:223]\n"
"v_exp_f32 v164, v164\n"
"v_smfmac_f32_16x16x64_bf16 v[196:199], a[240:243], v[128:135], v241\n"
"v_cvt_pk_bf16_f32 v97, v154, v155\n"
"v_mfma_f32_16x16x32_bf16 v[202:205], a[240:243], v[124:127], v[202:205]\n"
"s_waitcnt lgkmcnt(1)\n"
"ds_read_b64_tr_b16 a[254:255], v235 offset:37888\n"
"v_mfma_f32_16x16x32_bf16 v[168:171], v[216:219], a[8:11], v[180:183]\n"
"ds_read_b64_tr_b16 a[248:249], v238 offset:33792\n"
"v_mfma_f32_16x16x32_bf16 v[168:171], v[220:223], a[12:15], v[168:171]\n"
"ds_read_b64_tr_b16 a[246:247], v234 offset:37888\n"
"v_mfma_f32_16x16x32_bf16 v[168:171], v[208:211], a[0:3], v[168:171]\n"
"ds_read_b64_tr_b16 v[224:225], v239 offset:33792\n"
"v_cvt_pk_bf16_f32 v120, v164, v165\n"
"v_mfma_f32_16x16x32_bf16 v[168:171], v[212:215], a[4:7], v[168:171]\n"
"v_exp_f32 v166, v166\n"
"v_mfma_f32_16x16x32_bf16 v[172:175], v[208:211], a[16:19], v[184:187]\n"
"ds_read_b64_tr_b16 a[252:253], v235 offset:33792\n"
"v_cvt_pk_bf16_f32 v96, v152, v153\n"
"v_mfma_f32_16x16x32_bf16 v[172:175], v[212:215], a[20:23], v[172:175]\n"
"ds_read_b64_tr_b16 v[226:227], v239 offset:37888\n"
"v_cvt_pk_bf16_f32 v105, v158, v159\n"
"v_mfma_f32_16x16x32_bf16 v[172:175], v[216:219], a[24:27], v[172:175]\n"
"s_add_u32 s40, s56, s54\n"
"ds_read_b64_tr_b16 a[250:251], v238 offset:37888\n"
"v_mfma_f32_16x16x32_bf16 v[172:175], v[220:223], a[28:31], v[172:175]\n"
"buffer_load_dwordx4 v240, s[20:23], s40 offen offset:3072 nt lds\n"
"v_mfma_f32_16x16x32_bf16 v[176:179], v[216:219], a[40:43], v[188:191]\n"
"s_add_u32 s55, s55, s59\n"
"v_exp_f32 v167, v167\n"
"v_mfma_f32_16x16x32_bf16 v[176:179], v[220:223], a[44:47], v[176:179]\n"
"s_add_u32 s41, s55, s57\n"
"v_exp_f32 v161, v161\n"
"v_mfma_f32_16x16x32_bf16 v[176:179], v[208:211], a[32:35], v[176:179]\n"
"s_and_b32 s41, s41, s58\n"
"v_exp_f32 v157, v157\n"
"v_mfma_f32_16x16x32_bf16 v[176:179], v[212:215], a[36:39], v[176:179]\n"
"s_andn2_b32 s56, s55, s58\n"
"v_exp_f32 v160, v160\n"
"v_mfma_f32_16x16x32_bf16 v[152:155], v[208:211], a[192:195], v[192:195]\n"
"s_or_b32 s56, s56, s41\n"
"ds_read_b128 v[208:211], v230 offset:49152\n"
"v_mfma_f32_16x16x32_bf16 v[152:155], v[212:215], a[196:199], v[152:155]\n"
"v_exp_f32 v172, v172\n"
"v_mfma_f32_16x16x32_bf16 v[152:155], v[216:219], a[200:203], v[152:155]\n"
"ds_read_b128 v[216:219], v230 offset:50176\n"
"v_mfma_f32_16x16x32_bf16 v[152:155], v[220:223], a[204:207], v[152:155]\n"
"s_waitcnt lgkmcnt(2)\n"
"v_exp_f32 v162, v162\n"
"v_mfma_f32_16x16x32_bf16 a[232:235], v[124:127], a[244:247], a[232:235]\n"
"ds_read_b128 v[212:215], v231 offset:49152\n"
"v_mfma_f32_16x16x32_bf16 v[24:27], v[128:131], a[244:247], v[24:27]\n"
"s_add_u32 m0, s44, 16384\n"
"ds_read_b128 v[220:223], v231 offset:50176\n"
"v_mfma_f32_16x16x32_bf16 v[24:27], v[132:135], a[248:251], v[24:27]\n"
"buffer_load_dwordx4 v240, s[16:19], s56 offen nt lds\n"
"v_mfma_f32_16x16x32_bf16 v[56:59], v[136:139], a[244:247], v[56:59]\n"
"v_exp_f32 v171, v171\n"
"v_mfma_f32_16x16x32_bf16 v[56:59], v[140:143], a[248:251], v[56:59]\n"
"v_exp_f32 v169, v169\n"
"v_mfma_f32_16x16x32_bf16 v[88:91], v[144:147], a[244:247], v[88:91]\n"
"v_exp_f32 v154, v154\n"
"v_mfma_f32_16x16x32_bf16 v[88:91], v[148:151], a[248:251], v[88:91]\n"
"v_exp_f32 v156, v156\n"
"v_mfma_f32_16x16x32_bf16 v[92:95], v[148:151], v[224:227], v[92:95]\n"
"ds_read_b64_tr_b16 a[244:245], v232 offset:32768\n"
"v_cvt_pk_bf16_f32 v113, v162, v163\n"
"v_mfma_f32_16x16x32_bf16 v[92:95], v[144:147], a[252:255], v[92:95]\n"
"ds_read_b64_tr_b16 a[250:251], v236 offset:36864\n"
"v_cvt_pk_bf16_f32 v104, v156, v157\n"
"v_mfma_f32_16x16x32_bf16 v[60:63], v[140:143], v[224:227], v[60:63]\n"
"ds_read_b64_tr_b16 a[246:247], v232 offset:36864\n"
"v_cvt_pk_bf16_f32 v112, v160, v161\n"
"v_mfma_f32_16x16x32_bf16 v[60:63], v[136:139], a[252:255], v[60:63]\n"
"v_exp_f32 v152, v152\n"
"v_mfma_f32_16x16x32_bf16 v[28:31], v[132:135], v[224:227], v[28:31]\n"
"ds_read_b64_tr_b16 v[224:225], v237 offset:32768\n"
"ds_read_b64_tr_b16 v[226:227], v237 offset:36864\n"
"v_mfma_f32_16x16x32_bf16 v[28:31], v[128:131], a[252:255], v[28:31]\n"
"ds_read_b64_tr_b16 a[248:249], v236 offset:32768\n"
"v_cvt_pk_bf16_f32 v121, v166, v167\n"
"v_mfma_f32_16x16x32_bf16 a[236:239], v[124:127], a[252:255], a[236:239]\n"
"s_add_u32 s40, s56, s52\n"
"ds_read_b64_tr_b16 a[252:253], v233 offset:32768\n"
"v_smfmac_f32_16x16x64_bf16 v[198:201], a[240:243], v[136:143], v241\n"
"s_waitcnt lgkmcnt(7)\n"
"buffer_load_dwordx4 v240, s[16:19], s40 offen offset:1024 nt lds\n"
"v_mfma_f32_16x16x32_bf16 v[156:159], v[208:211], a[0:3], v[180:183]\n"
"v_exp_f32 v175, v175\n"
"v_mfma_f32_16x16x32_bf16 v[156:159], v[212:215], a[4:7], v[156:159]\n"
"v_exp_f32 v173, v173\n"
"v_mfma_f32_16x16x32_bf16 v[156:159], v[216:219], a[8:11], v[156:159]\n"
"v_exp_f32 v168, v168\n"
"v_mfma_f32_16x16x32_bf16 v[156:159], v[220:223], a[12:15], v[156:159]\n"
"ds_read_b64_tr_b16 a[254:255], v233 offset:36864\n"
"v_mfma_f32_16x16x32_bf16 v[160:163], v[216:219], a[24:27], v[184:187]\n"
"v_exp_f32 v170, v170\n"
"v_mfma_f32_16x16x32_bf16 v[160:163], v[220:223], a[28:31], v[160:163]\n"
"v_exp_f32 v178, v178\n"
"v_mfma_f32_16x16x32_bf16 v[160:163], v[208:211], a[16:19], v[160:163]\n"
"v_exp_f32 v177, v177\n"
"v_mfma_f32_16x16x32_bf16 v[160:163], v[212:215], a[20:23], v[160:163]\n"
"v_exp_f32 v176, v176\n"
"v_mfma_f32_16x16x32_bf16 v[164:167], v[208:211], a[32:35], v[188:191]\n"
"ds_read_b128 v[208:211], v230 offset:53248\n"
"v_cvt_pk_bf16_f32 v99, v170, v171\n"
"v_mfma_f32_16x16x32_bf16 v[164:167], v[212:215], a[36:39], v[164:167]\n"
"ds_read_b128 v[212:215], v231 offset:53248\n"
"v_mfma_f32_16x16x32_bf16 v[164:167], v[216:219], a[40:43], v[164:167]\n"
"v_exp_f32 v153, v153\n"
"v_mfma_f32_16x16x32_bf16 v[164:167], v[220:223], a[44:47], v[164:167]\n"
"s_waitcnt lgkmcnt(2)\n"
"ds_read_b128 v[220:223], v231 offset:54272\n"
"v_cvt_pk_bf16_f32 v106, v172, v173\n"
"v_mfma_f32_16x16x32_bf16 a[208:211], v[124:127], a[244:247], a[208:211]\n"
"v_exp_f32 v174, v174\n"
"v_mfma_f32_16x16x32_bf16 v[0:3], v[128:131], a[244:247], v[0:3]\n"
"s_add_u32 s40, s56, s53\n"
"ds_read_b128 v[216:219], v230 offset:54272\n"
"v_mfma_f32_16x16x32_bf16 v[0:3], v[132:135], a[248:251], v[0:3]\n"
"buffer_load_dwordx4 v240, s[16:19], s40 offen offset:2048 nt lds\n"
"v_mfma_f32_16x16x32_bf16 v[32:35], v[136:139], a[244:247], v[32:35]\n"
"v_exp_f32 v163, v163\n"
"v_mfma_f32_16x16x32_bf16 v[32:35], v[140:143], a[248:251], v[32:35]\n"
"v_exp_f32 v161, v161\n"
"v_mfma_f32_16x16x32_bf16 v[64:67], v[144:147], a[244:247], v[64:67]\n"
"v_exp_f32 v155, v155\n"
"v_mfma_f32_16x16x32_bf16 v[64:67], v[148:151], a[248:251], v[64:67]\n"
"v_exp_f32 v166, v166\n"
"v_mfma_f32_16x16x32_bf16 v[68:71], v[148:151], v[224:227], v[68:71]\n"
"ds_read_b64_tr_b16 a[250:251], v236 offset:37888\n"
"v_cvt_pk_bf16_f32 v107, v174, v175\n"
"v_mfma_f32_16x16x32_bf16 v[68:71], v[144:147], a[252:255], v[68:71]\n"
"ds_read_b64_tr_b16 a[246:247], v232 offset:37888\n"
"v_cvt_pk_bf16_f32 v98, v168, v169\n"
"v_mfma_f32_16x16x32_bf16 v[36:39], v[140:143], v[224:227], v[36:39]\n"
"v_exp_f32 v179, v179\n"
"v_mfma_f32_16x16x32_bf16 v[36:39], v[136:139], a[252:255], v[36:39]\n"
"ds_read_b64_tr_b16 a[244:245], v232 offset:33792\n"
"v_cvt_pk_bf16_f32 v122, v152, v153\n"
"v_mfma_f32_16x16x32_bf16 v[4:7], v[132:135], v[224:227], v[4:7]\n"
"s_add_u32 s40, s56, s54\n"
"ds_read_b64_tr_b16 a[248:249], v236 offset:33792\n"
"v_mfma_f32_16x16x32_bf16 v[4:7], v[128:131], a[252:255], v[4:7]\n"
"buffer_load_dwordx4 v240, s[16:19], s40 offen offset:3072 nt lds\n"
"v_mfma_f32_16x16x32_bf16 a[212:215], v[124:127], a[252:255], a[212:215]\n"
"v_exp_f32 v162, v162\n"
"v_smfmac_f32_16x16x64_bf16 v[200:203], a[240:243], v[144:151], v241\n"
"s_waitcnt lgkmcnt(4)\n"
"v_cvt_pk_bf16_f32 v114, v176, v177\n"
"v_mfma_f32_16x16x32_bf16 v[168:171], v[208:211], a[0:3], v[180:183]\n"
"v_exp_f32 v157, v157\n"
"v_mfma_f32_16x16x32_bf16 v[168:171], v[212:215], a[4:7], v[168:171]\n"
"v_exp_f32 v165, v165\n"
"v_mfma_f32_16x16x32_bf16 v[168:171], v[216:219], a[8:11], v[168:171]\n"
"ds_read_b64_tr_b16 a[254:255], v233 offset:37888\n"
"ds_read_b64_tr_b16 v[226:227], v237 offset:37888\n"
"v_mfma_f32_16x16x32_bf16 v[168:171], v[220:223], a[12:15], v[168:171]\n"
"ds_read_b64_tr_b16 a[252:253], v233 offset:33792\n"
"v_cvt_pk_bf16_f32 v115, v178, v179\n"
"v_mfma_f32_16x16x32_bf16 v[172:175], v[216:219], a[24:27], v[184:187]\n"
"ds_read_b64_tr_b16 v[224:225], v237 offset:33792\n"
"v_mfma_f32_16x16x32_bf16 v[172:175], v[220:223], a[28:31], v[172:175]\n"
"v_exp_f32 v160, v160\n"
"v_mfma_f32_16x16x32_bf16 v[172:175], v[208:211], a[16:19], v[172:175]\n"
"v_exp_f32 v158, v158\n"
"v_mfma_f32_16x16x32_bf16 v[172:175], v[212:215], a[20:23], v[172:175]\n"
"s_cbranch_vccnz LPCL3_%=\nLPKL3_%=:\n"
"v_mfma_f32_16x16x32_bf16 v[176:179], v[208:211], a[32:35], v[188:191]\n"
"s_add_u32 s47, s47, 1\n"
"v_exp_f32 v159, v159\n"
"v_mfma_f32_16x16x32_bf16 v[176:179], v[212:215], a[36:39], v[176:179]\n"
"v_exp_f32 v167, v167\n"
"v_mfma_f32_16x16x32_bf16 v[176:179], v[216:219], a[40:43], v[176:179]\n"
"s_and_b32 s40, s47, 3\n"
"v_exp_f32 v164, v164\n"
"v_mfma_f32_16x16x32_bf16 v[176:179], v[220:223], a[44:47], v[176:179]\n"
"s_waitcnt lgkmcnt(1)\n"
"s_cbranch_scc1 LAL_%=\n"
"s_cmp_eq_u32 s47, 8\n"
"s_cbranch_scc1 LTH1_%=\n"
"s_cmp_eq_u32 s47, 12\n"
"s_cbranch_scc1 LTH2_%=\n"
"s_cmp_eq_u32 s47, 16\n"
"s_cbranch_scc1 LAX_%=\n"
"LTH0_%=:\n"
"v_mfma_f32_16x16x32_bf16 a[224:227], v[124:127], a[244:247], a[224:227]\n"
"v_exp_f32 v168, v168\n"
"v_mfma_f32_16x16x32_bf16 v[16:19], v[128:131], a[244:247], v[16:19]\n"
"s_waitcnt vmcnt(8) lgkmcnt(0)\n"
"v_exp_f32 v169, v169\n"
"v_mfma_f32_16x16x32_bf16 v[16:19], v[132:135], a[248:251], v[16:19]\n"
"s_barrier\n"
"v_mfma_f32_16x16x32_bf16 v[48:51], v[136:139], a[244:247], v[48:51]\n"
"v_exp_f32 v170, v170\n"
"v_mfma_f32_16x16x32_bf16 v[48:51], v[140:143], a[248:251], v[48:51]\n"
"v_exp_f32 v156, v156\n"
"v_cvt_pk_bf16_f32 v102, v168, v169\n"
"v_exp_f32 v173, v173\n"
"v_exp_f32 v174, v174\n"
"v_exp_f32 v179, v179\n"
"v_mfma_f32_16x16x32_bf16 v[80:83], v[144:147], a[244:247], v[80:83]\n"
"ds_read_b64_tr_b16 a[246:247], v234 offset:53248\n"
"v_cvt_pk_bf16_f32 v108, v160, v161\n"
"v_mfma_f32_16x16x32_bf16 v[80:83], v[148:151], a[248:251], v[80:83]\n"
"ds_read_b64_tr_b16 a[244:245], v234 offset:49152\n"
"v_cvt_pk_bf16_f32 v123, v154, v155\n"
"v_mfma_f32_16x16x32_bf16 v[84:87], v[148:151], v[224:227], v[84:87]\n"
"ds_read_b64_tr_b16 a[250:251], v238 offset:53248\n"
"v_cvt_pk_bf16_f32 v100, v156, v157\n"
"v_mfma_f32_16x16x32_bf16 v[84:87], v[144:147], a[252:255], v[84:87]\n"
"ds_read_b64_tr_b16 a[248:249], v238 offset:49152\n"
"v_cvt_pk_bf16_f32 v117, v166, v167\n"
"v_mfma_f32_16x16x32_bf16 v[52:55], v[140:143], v[224:227], v[52:55]\n"
"v_exp_f32 v178, v178\n"
"v_mfma_f32_16x16x32_bf16 v[52:55], v[136:139], a[252:255], v[52:55]\n"
"v_cvt_pk_bf16_f32 v101, v158, v159\n"
"v_exp_f32 v171, v171\n"
"v_exp_f32 v172, v172\n"
"v_exp_f32 v175, v175\n"
"v_exp_f32 v176, v176\n"
"v_mfma_f32_16x16x32_bf16 v[20:23], v[132:135], v[224:227], v[20:23]\n"
"v_exp_f32 v177, v177\n"
"v_mfma_f32_16x16x32_bf16 v[20:23], v[128:131], a[252:255], v[20:23]\n"
"v_cvt_pk_bf16_f32 v116, v164, v165\n"
"v_cvt_pk_bf16_f32 v103, v170, v171\n"
"v_mfma_f32_16x16x32_bf16 a[228:231], v[124:127], a[252:255], a[228:231]\n"
"s_waitcnt lgkmcnt(2)\n"
"ds_read_b64_tr_b16 v[224:225], v239 offset:49152\n"
"ds_read_b64_tr_b16 v[226:227], v239 offset:53248\n"
"v_mfma_f32_16x16x32_bf16 a[216:219], v[120:123], a[244:247], a[216:219]\n"
"s_add_u32 m0, s44, 81920\n"
"ds_read_b128 v[212:215], v229 offset:0\n"
"v_cvt_pk_bf16_f32 v110, v172, v173\n"
"v_smfmac_f32_16x16x64_bf16 v[196:199], a[240:243], v[96:103], v241\n"
"buffer_load_dwordx4 v240, s[20:23], s56 offen nt lds\n"
"v_mfma_f32_16x16x32_bf16 v[8:11], v[96:99], a[244:247], v[8:11]\n"
"s_waitcnt lgkmcnt(3)\n"
"v_cvt_pk_bf16_f32 v118, v176, v177\n"
"v_cvt_pk_bf16_f32 v119, v178, v179\n"
"v_mfma_f32_16x16x32_bf16 v[8:11], v[100:103], a[248:251], v[8:11]\n"
"ds_read_b64_tr_b16 a[254:255], v235 offset:53248\n"
"v_cvt_pk_bf16_f32 v109, v162, v163\n"
"v_smfmac_f32_16x16x64_bf16 v[200:203], a[240:243], v[112:119], v241\n"
"ds_read_b64_tr_b16 a[252:253], v235 offset:49152\n"
"v_cvt_pk_bf16_f32 v111, v174, v175\n"
"v_mfma_f32_16x16x32_bf16 v[40:43], v[104:107], a[244:247], v[40:43]\n"
"ds_read_b128 v[220:223], v229 offset:1024\n"
"v_add_f32 v196, v196, v197\n"
"v_mfma_f32_16x16x32_bf16 v[40:43], v[108:111], a[248:251], v[40:43]\n"
"v_and_b32 v245, 0x3f0, v242\n"
"v_lshrrev_b32 v245, 1, v245\n"
"v_smfmac_f32_16x16x64_bf16 v[198:201], a[240:243], v[104:111], v241\n"
"ds_read_b128 v[208:211], v228 offset:0\n"
"v_lshlrev_b32_e64 v244, 24, s43\n"
"v_mfma_f32_16x16x32_bf16 v[72:75], v[112:115], a[244:247], v[72:75]\n"
"ds_read_b64_tr_b16 a[246:247], v234 offset:54272\n"
"v_mul_f32 v244, 0x45800000, v244\n"
"v_mfma_f32_16x16x32_bf16 v[72:75], v[116:119], a[248:251], v[72:75]\n"
"ds_read_b64_tr_b16 a[244:245], v234 offset:50176\n"
"v_sub_u32 v245, v242, v245\n"
"v_mfma_f32_16x16x32_bf16 v[202:205], a[240:243], v[120:123], v[202:205]\n"
"s_waitcnt lgkmcnt(6)\n"
"ds_read_b64_tr_b16 a[250:251], v238 offset:54272\n"
"v_add_f32 v198, v198, v199\n"
"v_mfma_f32_16x16x32_bf16 v[76:79], v[116:119], v[224:227], v[76:79]\n"
"s_waitcnt lgkmcnt(5)\n"
"ds_read_b64_tr_b16 a[248:249], v238 offset:50176\n"
"v_add_f32 v254, v196, v198\n"
"v_mfma_f32_16x16x32_bf16 v[76:79], v[112:115], a[252:255], v[76:79]\n"
"v_add_f32 v200, v200, v201\n"
"v_add_f32 v254, v254, v200\n"
"v_mfma_f32_16x16x32_bf16 v[44:47], v[108:111], v[224:227], v[44:47]\n"
"v_add_f32 v244, v244, v202\n"
"v_add_f32 v254, v254, v202\n"
"v_mfma_f32_16x16x32_bf16 v[44:47], v[104:107], a[252:255], v[44:47]\n"
"v_min3_f32 v244, v244, v196, v198\n"
"v_min_f32 v244, v244, v200\n"
"v_mfma_f32_16x16x32_bf16 v[12:15], v[100:103], v[224:227], v[12:15]\n"
"s_add_u32 s40, s56, s52\n"
"v_rcp_f32 v244, v244\n"
"v_mfma_f32_16x16x32_bf16 v[12:15], v[96:99], a[252:255], v[12:15]\n"
"buffer_load_dwordx4 v240, s[20:23], s40 offen offset:1024 nt lds\n"
"v_fmac_f32 v254, 0x68800000, v244\n"
"v_xor_b32 v244, 0x11000, v242\n"
"v_mfma_f32_16x16x32_bf16 a[220:223], v[120:123], a[252:255], a[220:223]\n"
"s_waitcnt lgkmcnt(2)\n"
"v_cmp_nge_f32 vcc, s45, v254\n"
"s_cbranch_vccnz LSLOW_%=\nLBACK0_%=:\ns_mov_b64 vcc, s[64:65]\n"
"v_mfma_f32_16x16x32_bf16 a[232:235], v[120:123], a[244:247], a[232:235]\n"
"v_rcp_f32 v246, v196\n"
"v_rcp_f32 v206, v200\n"
"v_accvgpr_mov_b32 a1, a49\n"
"v_accvgpr_mov_b32 a35, a83\n"
"v_accvgpr_mov_b32 a37, a85\n"
"v_accvgpr_mov_b32 a45, a93\n"
"v_bfe_u32 v149, v242, 8, 2\n"
"v_lshl_add_u32 v149, v149, 4, s43\n"
"v_mfma_f32_16x16x32_bf16 v[24:27], v[96:99], a[244:247], v[24:27]\n"
"s_waitcnt lgkmcnt(0)\n"
"ds_read_b128 v[124:127], v149 offset:0\n"
"v_accvgpr_mov_b32 a2, a50\n"
"v_accvgpr_mov_b32 a5, a53\n"
"v_accvgpr_mov_b32 a8, a56\n"
"v_accvgpr_mov_b32 a13, a61\n"
"v_accvgpr_mov_b32 a17, a65\n"
"v_accvgpr_mov_b32 a21, a69\n"
"v_xor_b32 v148, 0x400, v243\n"
"v_xor_b32 v150, 64, v149\n"
"v_mfma_f32_16x16x32_bf16 v[24:27], v[100:103], a[248:251], v[24:27]\n"
"ds_read_b128 v[132:135], v149 offset:1024\n"
"v_mov_b32_dpp v246, v246 row_ror:12 row_mask:0x2 bank_mask:0xf\n"
"v_accvgpr_mov_b32 a0, a48\n"
"v_accvgpr_mov_b32 a4, a52\n"
"v_accvgpr_mov_b32 a7, a55\n"
"v_accvgpr_mov_b32 a14, a62\n"
"v_accvgpr_mov_b32 a22, a70\n"
"v_accvgpr_mov_b32 a27, a75\n"
"v_accvgpr_mov_b32 a32, a80\n"
"v_accvgpr_mov_b32 a42, a90\n"
"v_mfma_f32_16x16x32_bf16 v[56:59], v[104:107], a[244:247], v[56:59]\n"
"ds_read_b128 v[128:131], v150 offset:0\n"
"v_rcp_f32 v247, v198\n"
"v_mov_b32_dpp v246, v246 row_ror:8 row_mask:0x4 bank_mask:0xf\n"
"v_accvgpr_mov_b32 a9, a57\n"
"v_accvgpr_mov_b32 a25, a73\n"
"v_accvgpr_mov_b32 a31, a79\n"
"v_accvgpr_mov_b32 a33, a81\n"
"v_accvgpr_mov_b32 a41, a89\n"
"v_mfma_f32_16x16x32_bf16 v[56:59], v[108:111], a[248:251], v[56:59]\n"
"ds_read_b128 v[136:139], v150 offset:1024\n"
"v_mov_b32_dpp v247, v247 row_ror:12 row_mask:0x2 bank_mask:0xf\n"
"v_mov_b32 v253, v202\n"
"v_accvgpr_mov_b32 a3, a51\n"
"v_accvgpr_mov_b32 a6, a54\n"
"v_accvgpr_mov_b32 a12, a60\n"
"v_accvgpr_mov_b32 a16, a64\n"
"v_accvgpr_mov_b32 a24, a72\n"
"v_accvgpr_mov_b32 a26, a74\n"
"v_accvgpr_mov_b32 a30, a78\n"
"v_mfma_f32_16x16x32_bf16 v[88:91], v[112:115], a[244:247], v[88:91]\n"
"v_accvgpr_mov_b32 a34, a82\n"
"v_accvgpr_mov_b32 a43, a91\n"
"v_mfma_f32_16x16x32_bf16 v[88:91], v[116:119], a[248:251], v[88:91]\n"
"ds_read_b128 a[196:199], v148 offset:4096\n"
"v_mov_b32_dpp v246, v246 row_ror:4 row_mask:0x8 bank_mask:0xf\n"
"v_accvgpr_mov_b32 a11, a59\n"
"v_accvgpr_mov_b32 a18, a66\n"
"v_accvgpr_mov_b32 a19, a67\n"
"v_accvgpr_mov_b32 a39, a87\n"
"v_accvgpr_mov_b32 a40, a88\n"
"v_accvgpr_mov_b32 a46, a94\n"
"s_waitcnt lgkmcnt(4)\n"
"v_mfma_f32_16x16x32_bf16 v[180:183], v[124:127], a[0:3], 0\n"
"s_waitcnt lgkmcnt(2)\n"
"ds_read_b64_tr_b16 a[252:253], v235 offset:50176\n"
"v_accvgpr_mov_b32 a10, a58\n"
"v_mfma_f32_16x16x32_bf16 v[180:183], v[128:131], a[4:7], v[180:183]\n"
"ds_read_b128 a[192:195], v243 offset:4096\n"
"v_accvgpr_mov_b32 a15, a63\n"
"v_mfma_f32_16x16x32_bf16 v[180:183], v[132:135], a[8:11], v[180:183]\n"
"ds_read_b128 a[200:203], v243 offset:6144\n"
"s_waitcnt lgkmcnt(4)\n"
"v_mfma_f32_16x16x32_bf16 v[180:183], v[136:139], a[12:15], v[180:183]\n"
"ds_read_b128 a[204:207], v148 offset:6144\n"
"v_accvgpr_mov_b32 a20, a68\n"
"v_accvgpr_mov_b32 a23, a71\n"
"v_accvgpr_mov_b32 a28, a76\n"
"v_accvgpr_mov_b32 a36, a84\n"
"v_accvgpr_mov_b32 a38, a86\n"
"v_accvgpr_mov_b32 a47, a95\n"
"v_mul_f32_dpp v25, v246, v25 row_newbcast:1 row_mask:0xf bank_mask:0xf\n"
"v_mfma_f32_16x16x32_bf16 v[184:187], v[124:127], a[16:19], 0\n"
"v_accvgpr_mov_b32 a44, a92\n"
"v_mul_f32_dpp v27, v246, v27 row_newbcast:3 row_mask:0xf bank_mask:0xf\n"
"v_mfma_f32_16x16x32_bf16 v[184:187], v[128:131], a[20:23], v[184:187]\n"
"v_mov_b32 v252, v192\n"
"v_accvgpr_mov_b32 a29, a77\n"
"v_mfma_f32_16x16x32_bf16 v[184:187], v[132:135], a[24:27], v[184:187]\n"
"s_add_u32 s40, s56, s53\n"
"ds_read_b128 v[216:219], v228 offset:1024\n"
"v_sub_f32 v180, s74, v180\n"
"v_mfma_f32_16x16x32_bf16 v[184:187], v[136:139], a[28:31], v[184:187]\n"
"buffer_load_dwordx4 v240, s[20:23], s40 offen offset:2048 nt lds\n"
"v_mov_b32_dpp v206, v206 row_ror:12 row_mask:0x2 bank_mask:0xf\n"
"v_mfma_f32_16x16x32_bf16 v[188:191], v[124:127], a[32:35], 0\n"
"v_mov_b32_dpp v247, v247 row_ror:8 row_mask:0x4 bank_mask:0xf\n"
"v_mfma_f32_16x16x32_bf16 v[188:191], v[128:131], a[36:39], v[188:191]\n"
"s_nop 0\n"
"v_mov_b32_dpp v247, v247 row_ror:4 row_mask:0x8 bank_mask:0xf\n"
"v_sub_f32 v183, s74, v183\n"
"v_mfma_f32_16x16x32_bf16 v[188:191], v[132:135], a[40:43], v[188:191]\n"
"v_sub_f32 v181, s74, v181\n"
"v_mfma_f32_16x16x32_bf16 v[188:191], v[136:139], a[44:47], v[188:191]\n"
"ds_write_b128 v244, a[232:235] offset:34816\n"
"v_mov_b32_dpp v206, v206 row_ror:8 row_mask:0x4 bank_mask:0xf\n"
"v_sub_f32 v185, s74, v185\n"
"v_sub_f32 v187, s74, v187\n"
"s_waitcnt lgkmcnt(2)\n"
"v_mfma_f32_16x16x32_bf16 v[192:195], v[124:127], a[192:195], 0\n"
"v_sub_f32 v182, s74, v182\n"
"v_mfma_f32_16x16x32_bf16 v[192:195], v[128:131], a[196:199], v[192:195]\n"
"ds_read_b64_tr_b16 a[246:247], v232 offset:53248\n"
"v_mfma_f32_16x16x32_bf16 v[192:195], v[132:135], a[200:203], v[192:195]\n"
"v_mfma_f32_16x16x32_bf16 v[192:195], v[136:139], a[204:207], v[192:195]\n"
"ds_read_b64_tr_b16 v[224:225], v239 offset:50176\n"
"ds_read_b64_tr_b16 v[226:227], v239 offset:54272\n"
"v_sub_f32 v184, s74, v184\n"
"v_mul_f32_dpp v56, v247, v56 row_newbcast:0 row_mask:0xf bank_mask:0xf\n"
"v_mfma_f32_16x16x32_bf16 v[152:155], v[208:211], a[0:3], v[180:183]\n"
"v_mul_f32_dpp v45, v247, v45 row_newbcast:1 row_mask:0xf bank_mask:0xf\n"
"v_sub_f32 v186, s74, v186\n"
"v_mfma_f32_16x16x32_bf16 v[152:155], v[212:215], a[4:7], v[152:155]\n"
"v_sub_f32 v188, s74, v188\n"
"s_waitcnt lgkmcnt(4)\n"
"v_mfma_f32_16x16x32_bf16 v[152:155], v[216:219], a[8:11], v[152:155]\n"
"s_add_u32 s40, s56, s54\n"
"v_sub_f32 v195, s74, v195\n"
"v_mov_b64 v[196:197], 0\n"
"v_mfma_f32_16x16x32_bf16 v[152:155], v[220:223], a[12:15], v[152:155]\n"
"buffer_load_dwordx4 v240, s[20:23], s40 offen offset:3072 nt lds\n"
"v_mul_f32_dpp v15, v246, v15 row_newbcast:3 row_mask:0xf bank_mask:0xf\n"
"v_sub_f32 v190, s74, v190\n"
"v_sub_f32 v191, s74, v191\n"
"v_sub_f32 v192, s74, v192\n"
"v_mfma_f32_16x16x32_bf16 v[156:159], v[216:219], a[24:27], v[184:187]\n"
"s_add_u32 s55, s55, s59\n"
"v_mov_b32_dpp v206, v206 row_ror:4 row_mask:0x8 bank_mask:0xf\n"
"v_mfma_f32_16x16x32_bf16 v[156:159], v[220:223], a[28:31], v[156:159]\n"
"s_add_u32 s41, s55, s57\n"
"v_sub_f32 v189, s74, v189\n"
"v_mfma_f32_16x16x32_bf16 v[156:159], v[208:211], a[16:19], v[156:159]\n"
"s_and_b32 s41, s41, s58\n"
"v_mul_f32_dpp v41, v247, v41 row_newbcast:1 row_mask:0xf bank_mask:0xf\n"
"v_mul_f32_dpp v26, v246, v26 row_newbcast:2 row_mask:0xf bank_mask:0xf\n"
"v_mfma_f32_16x16x32_bf16 v[156:159], v[212:215], a[20:23], v[156:159]\n"
"s_andn2_b32 s56, s55, s58\n"
"ds_read_b64_tr_b16 a[254:255], v235 offset:54272\n"
"v_mul_f32_dpp v46, v247, v46 row_newbcast:2 row_mask:0xf bank_mask:0xf\n"
"v_sub_f32 v193, s74, v193\n"
"v_mul_f32_dpp v58, v247, v58 row_newbcast:2 row_mask:0xf bank_mask:0xf\n"
"v_mfma_f32_16x16x32_bf16 v[160:163], v[208:211], a[32:35], v[188:191]\n"
"s_or_b32 s56, s56, s41\n"
"v_sub_f32 v194, s74, v194\n"
"v_mfma_f32_16x16x32_bf16 v[160:163], v[212:215], a[36:39], v[160:163]\n"
"v_mfma_f32_16x16x32_bf16 v[160:163], v[216:219], a[40:43], v[160:163]\n"
"v_mfma_f32_16x16x32_bf16 v[160:163], v[220:223], a[44:47], v[160:163]\n"
"ds_write_b128 v244, a[216:219] offset:32768\n"
"v_mul_f32_dpp v8, v246, v8 row_newbcast:0 row_mask:0xf bank_mask:0xf\n"
"v_exp_f32 v154, v154\n"
"v_mfma_f32_16x16x32_bf16 v[164:167], v[216:219], a[200:203], v[192:195]\n"
"v_exp_f32 v153, v153\n"
"v_mfma_f32_16x16x32_bf16 v[164:167], v[220:223], a[204:207], v[164:167]\n"
"ds_read_b128 v[220:223], v229 offset:5120\n"
"v_mul_f32_dpp v9, v246, v9 row_newbcast:1 row_mask:0xf bank_mask:0xf\n"
"v_mfma_f32_16x16x32_bf16 v[164:167], v[208:211], a[192:195], v[164:167]\n"
"v_mfma_f32_16x16x32_bf16 v[164:167], v[212:215], a[196:199], v[164:167]\n"
"ds_read_b128 v[216:219], v228 offset:5120\n"
"v_mul_f32_dpp v40, v247, v40 row_newbcast:0 row_mask:0xf bank_mask:0xf\n"
"v_exp_f32 v155, v155\n"
"v_exp_f32 v160, v160\n"
"v_exp_f32 v163, v163\n"
"s_branch LTC_%=\n"
"LTH1_%=:\n"
"v_mfma_f32_16x16x32_bf16 a[224:227], v[124:127], a[244:247], a[224:227]\n"
"v_exp_f32 v168, v168\n"
"v_mfma_f32_16x16x32_bf16 v[16:19], v[128:131], a[244:247], v[16:19]\n"
"s_waitcnt vmcnt(8) lgkmcnt(0)\n"
"v_exp_f32 v169, v169\n"
"v_mfma_f32_16x16x32_bf16 v[16:19], v[132:135], a[248:251], v[16:19]\n"
"s_barrier\n"
"v_mfma_f32_16x16x32_bf16 v[48:51], v[136:139], a[244:247], v[48:51]\n"
"v_exp_f32 v170, v170\n"
"v_mfma_f32_16x16x32_bf16 v[48:51], v[140:143], a[248:251], v[48:51]\n"
"v_exp_f32 v156, v156\n"
"v_cvt_pk_bf16_f32 v102, v168, v169\n"
"v_exp_f32 v173, v173\n"
"v_exp_f32 v174, v174\n"
"v_exp_f32 v179, v179\n"
"v_mfma_f32_16x16x32_bf16 v[80:83], v[144:147], a[244:247], v[80:83]\n"
"ds_read_b64_tr_b16 a[246:247], v234 offset:53248\n"
"v_cvt_pk_bf16_f32 v108, v160, v161\n"
"v_mfma_f32_16x16x32_bf16 v[80:83], v[148:151], a[248:251], v[80:83]\n"
"ds_read_b64_tr_b16 a[244:245], v234 offset:49152\n"
"v_cvt_pk_bf16_f32 v123, v154, v155\n"
"v_mfma_f32_16x16x32_bf16 v[84:87], v[148:151], v[224:227], v[84:87]\n"
"ds_read_b64_tr_b16 a[250:251], v238 offset:53248\n"
"v_cvt_pk_bf16_f32 v100, v156, v157\n"
"v_mfma_f32_16x16x32_bf16 v[84:87], v[144:147], a[252:255], v[84:87]\n"
"ds_read_b64_tr_b16 a[248:249], v238 offset:49152\n"
"v_cvt_pk_bf16_f32 v117, v166, v167\n"
"v_mfma_f32_16x16x32_bf16 v[52:55], v[140:143], v[224:227], v[52:55]\n"
"v_exp_f32 v178, v178\n"
"v_mfma_f32_16x16x32_bf16 v[52:55], v[136:139], a[252:255], v[52:55]\n"
"v_cvt_pk_bf16_f32 v101, v158, v159\n"
"v_exp_f32 v171, v171\n"
"v_exp_f32 v172, v172\n"
"v_exp_f32 v175, v175\n"
"v_exp_f32 v176, v176\n"
"v_mfma_f32_16x16x32_bf16 v[20:23], v[132:135], v[224:227], v[20:23]\n"
"v_exp_f32 v177, v177\n"
"v_mfma_f32_16x16x32_bf16 v[20:23], v[128:131], a[252:255], v[20:23]\n"
"v_cvt_pk_bf16_f32 v116, v164, v165\n"
"v_cvt_pk_bf16_f32 v103, v170, v171\n"
"v_mfma_f32_16x16x32_bf16 a[228:231], v[124:127], a[252:255], a[228:231]\n"
"s_waitcnt lgkmcnt(2)\n"
"ds_read_b64_tr_b16 v[224:225], v239 offset:49152\n"
"ds_read_b64_tr_b16 v[226:227], v239 offset:53248\n"
"v_mfma_f32_16x16x32_bf16 a[216:219], v[120:123], a[244:247], a[216:219]\n"
"s_add_u32 m0, s44, 81920\n"
"ds_read_b128 v[212:215], v229 offset:0\n"
"v_cvt_pk_bf16_f32 v110, v172, v173\n"
"v_smfmac_f32_16x16x64_bf16 v[196:199], a[240:243], v[96:103], v241\n"
"buffer_load_dwordx4 v240, s[20:23], s56 offen nt lds\n"
"v_mfma_f32_16x16x32_bf16 v[8:11], v[96:99], a[244:247], v[8:11]\n"
"s_waitcnt lgkmcnt(3)\n"
"v_cvt_pk_bf16_f32 v118, v176, v177\n"
"v_cvt_pk_bf16_f32 v119, v178, v179\n"
"v_mfma_f32_16x16x32_bf16 v[8:11], v[100:103], a[248:251], v[8:11]\n"
"ds_read_b64_tr_b16 a[254:255], v235 offset:53248\n"
"v_cvt_pk_bf16_f32 v109, v162, v163\n"
"v_smfmac_f32_16x16x64_bf16 v[200:203], a[240:243], v[112:119], v241\n"
"ds_read_b64_tr_b16 a[252:253], v235 offset:49152\n"
"v_cvt_pk_bf16_f32 v111, v174, v175\n"
"v_mfma_f32_16x16x32_bf16 v[40:43], v[104:107], a[244:247], v[40:43]\n"
"ds_read_b128 v[220:223], v229 offset:1024\n"
"v_add_f32 v196, v196, v197\n"
"v_mfma_f32_16x16x32_bf16 v[40:43], v[108:111], a[248:251], v[40:43]\n"
"v_and_b32 v245, 0x3f0, v242\n"
"v_lshrrev_b32 v245, 1, v245\n"
"v_smfmac_f32_16x16x64_bf16 v[198:201], a[240:243], v[104:111], v241\n"
"ds_read_b128 v[208:211], v228 offset:0\n"
"v_lshlrev_b32_e64 v244, 24, s43\n"
"v_mfma_f32_16x16x32_bf16 v[72:75], v[112:115], a[244:247], v[72:75]\n"
"ds_read_b64_tr_b16 a[246:247], v234 offset:54272\n"
"v_mul_f32 v244, 0x45800000, v244\n"
"v_mfma_f32_16x16x32_bf16 v[72:75], v[116:119], a[248:251], v[72:75]\n"
"ds_read_b64_tr_b16 a[244:245], v234 offset:50176\n"
"v_sub_u32 v245, v242, v245\n"
"v_mfma_f32_16x16x32_bf16 v[202:205], a[240:243], v[120:123], v[202:205]\n"
"s_waitcnt lgkmcnt(6)\n"
"ds_read_b64_tr_b16 a[250:251], v238 offset:54272\n"
"v_add_f32 v198, v198, v199\n"
"v_mfma_f32_16x16x32_bf16 v[76:79], v[116:119], v[224:227], v[76:79]\n"
"s_waitcnt lgkmcnt(5)\n"
"ds_read_b64_tr_b16 a[248:249], v238 offset:50176\n"
"v_add_f32 v254, v196, v198\n"
"v_mfma_f32_16x16x32_bf16 v[76:79], v[112:115], a[252:255], v[76:79]\n"
"v_add_f32 v200, v200, v201\n"
"v_add_f32 v254, v254, v200\n"
"v_mfma_f32_16x16x32_bf16 v[44:47], v[108:111], v[224:227], v[44:47]\n"
"v_add_f32 v244, v244, v202\n"
"v_add_f32 v254, v254, v202\n"
"v_mfma_f32_16x16x32_bf16 v[44:47], v[104:107], a[252:255], v[44:47]\n"
"v_min3_f32 v244, v244, v196, v198\n"
"v_min_f32 v244, v244, v200\n"
"v_mfma_f32_16x16x32_bf16 v[12:15], v[100:103], v[224:227], v[12:15]\n"
"s_add_u32 s40, s56, s52\n"
"v_rcp_f32 v244, v244\n"
"v_mfma_f32_16x16x32_bf16 v[12:15], v[96:99], a[252:255], v[12:15]\n"
"buffer_load_dwordx4 v240, s[20:23], s40 offen offset:1024 nt lds\n"
"v_fmac_f32 v254, 0x68800000, v244\n"
"v_xor_b32 v244, 0x11000, v242\n"
"v_mfma_f32_16x16x32_bf16 a[220:223], v[120:123], a[252:255], a[220:223]\n"
"s_waitcnt lgkmcnt(2)\n"
"v_cmp_nge_f32 vcc, s45, v254\n"
"s_cbranch_vccnz LSLOW_%=\nLBACK1_%=:\ns_mov_b64 vcc, s[64:65]\n"
"v_mfma_f32_16x16x32_bf16 a[232:235], v[120:123], a[244:247], a[232:235]\n"
"v_rcp_f32 v246, v196\n"
"v_rcp_f32 v206, v200\n"
"v_accvgpr_mov_b32 a1, a97\n"
"v_accvgpr_mov_b32 a35, a131\n"
"v_accvgpr_mov_b32 a37, a133\n"
"v_accvgpr_mov_b32 a45, a141\n"
"v_bfe_u32 v149, v242, 8, 2\n"
"v_lshl_add_u32 v149, v149, 4, s43\n"
"v_mfma_f32_16x16x32_bf16 v[24:27], v[96:99], a[244:247], v[24:27]\n"
"s_waitcnt lgkmcnt(0)\n"
"ds_read_b128 v[124:127], v149 offset:0\n"
"v_accvgpr_mov_b32 a2, a98\n"
"v_accvgpr_mov_b32 a5, a101\n"
"v_accvgpr_mov_b32 a8, a104\n"
"v_accvgpr_mov_b32 a13, a109\n"
"v_accvgpr_mov_b32 a17, a113\n"
"v_accvgpr_mov_b32 a21, a117\n"
"v_xor_b32 v148, 0x400, v243\n"
"v_xor_b32 v150, 64, v149\n"
"v_mfma_f32_16x16x32_bf16 v[24:27], v[100:103], a[248:251], v[24:27]\n"
"ds_read_b128 v[132:135], v149 offset:1024\n"
"v_mov_b32_dpp v246, v246 row_ror:12 row_mask:0x2 bank_mask:0xf\n"
"v_accvgpr_mov_b32 a0, a96\n"
"v_accvgpr_mov_b32 a4, a100\n"
"v_accvgpr_mov_b32 a7, a103\n"
"v_accvgpr_mov_b32 a14, a110\n"
"v_accvgpr_mov_b32 a22, a118\n"
"v_accvgpr_mov_b32 a27, a123\n"
"v_accvgpr_mov_b32 a32, a128\n"
"v_accvgpr_mov_b32 a42, a138\n"
"v_mfma_f32_16x16x32_bf16 v[56:59], v[104:107], a[244:247], v[56:59]\n"
"ds_read_b128 v[128:131], v150 offset:0\n"
"v_rcp_f32 v247, v198\n"
"v_mov_b32_dpp v246, v246 row_ror:8 row_mask:0x4 bank_mask:0xf\n"
"v_accvgpr_mov_b32 a9, a105\n"
"v_accvgpr_mov_b32 a25, a121\n"
"v_accvgpr_mov_b32 a31, a127\n"
"v_accvgpr_mov_b32 a33, a129\n"
"v_accvgpr_mov_b32 a41, a137\n"
"v_accvgpr_mov_b32 a118, a70\n"
"v_mfma_f32_16x16x32_bf16 v[56:59], v[108:111], a[248:251], v[56:59]\n"
"ds_read_b128 v[136:139], v150 offset:1024\n"
"v_mov_b32_dpp v247, v247 row_ror:12 row_mask:0x2 bank_mask:0xf\n"
"v_mov_b32 v253, v202\n"
"v_accvgpr_mov_b32 a3, a99\n"
"v_accvgpr_mov_b32 a6, a102\n"
"v_accvgpr_mov_b32 a12, a108\n"
"v_accvgpr_mov_b32 a16, a112\n"
"v_accvgpr_mov_b32 a24, a120\n"
"v_accvgpr_mov_b32 a26, a122\n"
"v_accvgpr_mov_b32 a30, a126\n"
"v_mfma_f32_16x16x32_bf16 v[88:91], v[112:115], a[244:247], v[88:91]\n"
"v_accvgpr_mov_b32 a34, a130\n"
"v_accvgpr_mov_b32 a43, a139\n"
"v_mfma_f32_16x16x32_bf16 v[88:91], v[116:119], a[248:251], v[88:91]\n"
"s_waitcnt lgkmcnt(3)\n"
"ds_read_b128 a[196:199], v148 offset:4096\n"
"v_mov_b32_dpp v246, v246 row_ror:4 row_mask:0x8 bank_mask:0xf\n"
"v_accvgpr_mov_b32 a11, a107\n"
"v_accvgpr_mov_b32 a18, a114\n"
"v_accvgpr_mov_b32 a19, a115\n"
"v_accvgpr_mov_b32 a39, a135\n"
"v_accvgpr_mov_b32 a40, a136\n"
"v_accvgpr_mov_b32 a46, a142\n"
"v_accvgpr_mov_b32 a108, a60\n"
"v_accvgpr_mov_b32 a96, a48\n"
"v_mfma_f32_16x16x32_bf16 v[180:183], v[124:127], a[0:3], 0\n"
"s_waitcnt lgkmcnt(2)\n"
"ds_read_b64_tr_b16 a[252:253], v235 offset:50176\n"
"v_accvgpr_mov_b32 a10, a106\n"
"v_mfma_f32_16x16x32_bf16 v[180:183], v[128:131], a[4:7], v[180:183]\n"
"ds_read_b128 a[192:195], v243 offset:4096\n"
"v_accvgpr_mov_b32 a15, a111\n"
"v_mfma_f32_16x16x32_bf16 v[180:183], v[132:135], a[8:11], v[180:183]\n"
"s_waitcnt lgkmcnt(3)\n"
"ds_read_b128 a[200:203], v243 offset:6144\n"
"v_accvgpr_mov_b32 a113, a65\n"
"v_mfma_f32_16x16x32_bf16 v[180:183], v[136:139], a[12:15], v[180:183]\n"
"ds_read_b128 a[204:207], v148 offset:6144\n"
"v_accvgpr_mov_b32 a20, a116\n"
"v_accvgpr_mov_b32 a23, a119\n"
"v_accvgpr_mov_b32 a28, a124\n"
"v_accvgpr_mov_b32 a36, a132\n"
"v_accvgpr_mov_b32 a38, a134\n"
"v_accvgpr_mov_b32 a47, a143\n"
"v_accvgpr_mov_b32 a100, a52\n"
"v_mul_f32_dpp v25, v246, v25 row_newbcast:1 row_mask:0xf bank_mask:0xf\n"
"v_mfma_f32_16x16x32_bf16 v[184:187], v[124:127], a[16:19], 0\n"
"v_accvgpr_mov_b32 a44, a140\n"
"v_mul_f32_dpp v27, v246, v27 row_newbcast:3 row_mask:0xf bank_mask:0xf\n"
"v_mfma_f32_16x16x32_bf16 v[184:187], v[128:131], a[20:23], v[184:187]\n"
"v_mov_b32 v252, v192\n"
"v_accvgpr_mov_b32 a29, a125\n"
"v_mfma_f32_16x16x32_bf16 v[184:187], v[132:135], a[24:27], v[184:187]\n"
"s_add_u32 s40, s56, s53\n"
"ds_read_b128 v[216:219], v228 offset:1024\n"
"v_sub_f32 v180, s74, v180\n"
"v_mfma_f32_16x16x32_bf16 v[184:187], v[136:139], a[28:31], v[184:187]\n"
"buffer_load_dwordx4 v240, s[20:23], s40 offen offset:2048 nt lds\n"
"v_mov_b32_dpp v206, v206 row_ror:12 row_mask:0x2 bank_mask:0xf\n"
"v_accvgpr_mov_b32 a101, a53\n"
"v_accvgpr_mov_b32 a116, a68\n"
"v_accvgpr_mov_b32 a132, a84\n"
"v_accvgpr_mov_b32 a142, a94\n"
"v_accvgpr_mov_b32 a99, a51\n"
"v_accvgpr_mov_b32 a131, a83\n"
"v_accvgpr_mov_b32 a139, a91\n"
"v_mfma_f32_16x16x32_bf16 v[188:191], v[124:127], a[32:35], 0\n"
"v_mov_b32_dpp v247, v247 row_ror:8 row_mask:0x4 bank_mask:0xf\n"
"v_accvgpr_mov_b32 a115, a67\n"
"v_mfma_f32_16x16x32_bf16 v[188:191], v[128:131], a[36:39], v[188:191]\n"
"v_mov_b32_dpp v247, v247 row_ror:4 row_mask:0x8 bank_mask:0xf\n"
"v_sub_f32 v183, s74, v183\n"
"v_mfma_f32_16x16x32_bf16 v[188:191], v[132:135], a[40:43], v[188:191]\n"
"v_accvgpr_mov_b32 a102, a54\n"
"v_sub_f32 v181, s74, v181\n"
"v_mfma_f32_16x16x32_bf16 v[188:191], v[136:139], a[44:47], v[188:191]\n"
"s_waitcnt lgkmcnt(1)\n"
"ds_write_b128 v244, a[232:235] offset:34816\n"
"v_mov_b32_dpp v206, v206 row_ror:8 row_mask:0x4 bank_mask:0xf\n"
"v_accvgpr_mov_b32 a124, a76\n"
"v_accvgpr_mov_b32 a125, a77\n"
"v_sub_f32 v185, s74, v185\n"
"v_sub_f32 v187, s74, v187\n"
"v_mfma_f32_16x16x32_bf16 v[192:195], v[124:127], a[192:195], 0\n"
"v_accvgpr_mov_b32 a103, a55\n"
"v_sub_f32 v182, s74, v182\n"
"v_mfma_f32_16x16x32_bf16 v[192:195], v[128:131], a[196:199], v[192:195]\n"
"ds_read_b64_tr_b16 a[246:247], v232 offset:53248\n"
"v_accvgpr_mov_b32 a98, a50\n"
"v_mfma_f32_16x16x32_bf16 v[192:195], v[132:135], a[200:203], v[192:195]\n"
"v_accvgpr_mov_b32 a109, a61\n"
"v_accvgpr_mov_b32 a121, a73\n"
"v_mfma_f32_16x16x32_bf16 v[192:195], v[136:139], a[204:207], v[192:195]\n"
"ds_read_b64_tr_b16 v[224:225], v239 offset:50176\n"
"ds_read_b64_tr_b16 v[226:227], v239 offset:54272\n"
"v_accvgpr_mov_b32 a135, a87\n"
"v_accvgpr_mov_b32 a97, a49\n"
"v_accvgpr_mov_b32 a128, a80\n"
"v_accvgpr_mov_b32 a130, a82\n"
"v_accvgpr_mov_b32 a123, a75\n"
"v_accvgpr_mov_b32 a137, a89\n"
"v_sub_f32 v184, s74, v184\n"
"v_mul_f32_dpp v56, v247, v56 row_newbcast:0 row_mask:0xf bank_mask:0xf\n"
"v_mfma_f32_16x16x32_bf16 v[152:155], v[208:211], a[0:3], v[180:183]\n"
"v_mul_f32_dpp v45, v247, v45 row_newbcast:1 row_mask:0xf bank_mask:0xf\n"
"v_sub_f32 v186, s74, v186\n"
"v_mfma_f32_16x16x32_bf16 v[152:155], v[212:215], a[4:7], v[152:155]\n"
"s_waitcnt lgkmcnt(4)\n"
"v_accvgpr_mov_b32 a127, a79\n"
"v_sub_f32 v188, s74, v188\n"
"v_mfma_f32_16x16x32_bf16 v[152:155], v[216:219], a[8:11], v[152:155]\n"
"s_add_u32 s40, s56, s54\n"
"v_sub_f32 v195, s74, v195\n"
"v_mov_b64 v[196:197], 0\n"
"v_mfma_f32_16x16x32_bf16 v[152:155], v[220:223], a[12:15], v[152:155]\n"
"buffer_load_dwordx4 v240, s[20:23], s40 offen offset:3072 nt lds\n"
"v_accvgpr_mov_b32 a143, a95\n"
"v_accvgpr_mov_b32 a114, a66\n"
"v_accvgpr_mov_b32 a105, a57\n"
"v_mul_f32_dpp v15, v246, v15 row_newbcast:3 row_mask:0xf bank_mask:0xf\n"
"v_sub_f32 v190, s74, v190\n"
"v_sub_f32 v191, s74, v191\n"
"v_sub_f32 v192, s74, v192\n"
"v_mfma_f32_16x16x32_bf16 v[156:159], v[216:219], a[24:27], v[184:187]\n"
"s_add_u32 s55, s55, s59\n"
"v_mov_b32_dpp v206, v206 row_ror:4 row_mask:0x8 bank_mask:0xf\n"
"v_accvgpr_mov_b32 a138, a90\n"
"v_mfma_f32_16x16x32_bf16 v[156:159], v[220:223], a[28:31], v[156:159]\n"
"s_add_u32 s41, s55, s57\n"
"v_accvgpr_mov_b32 a110, a62\n"
"v_sub_f32 v189, s74, v189\n"
"v_mfma_f32_16x16x32_bf16 v[156:159], v[208:211], a[16:19], v[156:159]\n"
"s_and_b32 s41, s41, s58\n"
"v_mul_f32_dpp v41, v247, v41 row_newbcast:1 row_mask:0xf bank_mask:0xf\n"
"v_mul_f32_dpp v26, v246, v26 row_newbcast:2 row_mask:0xf bank_mask:0xf\n"
"v_mfma_f32_16x16x32_bf16 v[156:159], v[212:215], a[20:23], v[156:159]\n"
"s_andn2_b32 s56, s55, s58\n"
"ds_read_b64_tr_b16 a[254:255], v235 offset:54272\n"
"v_accvgpr_mov_b32 a119, a71\n"
"v_accvgpr_mov_b32 a133, a85\n"
"v_accvgpr_mov_b32 a126, a78\n"
"v_accvgpr_mov_b32 a140, a92\n"
"v_accvgpr_mov_b32 a106, a58\n"
"v_accvgpr_mov_b32 a120, a72\n"
"v_mul_f32_dpp v46, v247, v46 row_newbcast:2 row_mask:0xf bank_mask:0xf\n"
"v_sub_f32 v193, s74, v193\n"
"v_mul_f32_dpp v58, v247, v58 row_newbcast:2 row_mask:0xf bank_mask:0xf\n"
"v_mfma_f32_16x16x32_bf16 v[160:163], v[208:211], a[32:35], v[188:191]\n"
"s_or_b32 s56, s56, s41\n"
"v_accvgpr_mov_b32 a117, a69\n"
"v_sub_f32 v194, s74, v194\n"
"v_mfma_f32_16x16x32_bf16 v[160:163], v[212:215], a[36:39], v[160:163]\n"
"v_accvgpr_mov_b32 a111, a63\n"
"v_accvgpr_mov_b32 a122, a74\n"
"v_mfma_f32_16x16x32_bf16 v[160:163], v[216:219], a[40:43], v[160:163]\n"
"v_accvgpr_mov_b32 a107, a59\n"
"v_accvgpr_mov_b32 a136, a88\n"
"v_mfma_f32_16x16x32_bf16 v[160:163], v[220:223], a[44:47], v[160:163]\n"
"ds_write_b128 v244, a[216:219] offset:32768\n"
"v_accvgpr_mov_b32 a112, a64\n"
"v_accvgpr_mov_b32 a129, a81\n"
"v_mul_f32_dpp v8, v246, v8 row_newbcast:0 row_mask:0xf bank_mask:0xf\n"
"v_exp_f32 v154, v154\n"
"v_mfma_f32_16x16x32_bf16 v[164:167], v[216:219], a[200:203], v[192:195]\n"
"v_exp_f32 v153, v153\n"
"v_mfma_f32_16x16x32_bf16 v[164:167], v[220:223], a[204:207], v[164:167]\n"
"ds_read_b128 v[220:223], v229 offset:5120\n"
"v_mul_f32_dpp v9, v246, v9 row_newbcast:1 row_mask:0xf bank_mask:0xf\n"
"v_mfma_f32_16x16x32_bf16 v[164:167], v[208:211], a[192:195], v[164:167]\n"
"v_accvgpr_mov_b32 a141, a93\n"
"v_accvgpr_mov_b32 a104, a56\n"
"v_mfma_f32_16x16x32_bf16 v[164:167], v[212:215], a[196:199], v[164:167]\n"
"s_waitcnt lgkmcnt(2)\n"
"ds_read_b128 v[216:219], v228 offset:5120\n"
"v_accvgpr_mov_b32 a134, a86\n"
"v_mul_f32_dpp v40, v247, v40 row_newbcast:0 row_mask:0xf bank_mask:0xf\n"
"v_exp_f32 v155, v155\n"
"v_exp_f32 v160, v160\n"
"v_exp_f32 v163, v163\n"
"s_branch LTC_%=\n"
"LTH2_%=:\n"
"v_mfma_f32_16x16x32_bf16 a[224:227], v[124:127], a[244:247], a[224:227]\n"
"v_exp_f32 v168, v168\n"
"v_mfma_f32_16x16x32_bf16 v[16:19], v[128:131], a[244:247], v[16:19]\n"
"s_waitcnt vmcnt(8) lgkmcnt(0)\n"
"v_exp_f32 v169, v169\n"
"v_mfma_f32_16x16x32_bf16 v[16:19], v[132:135], a[248:251], v[16:19]\n"
"s_barrier\n"
"v_mfma_f32_16x16x32_bf16 v[48:51], v[136:139], a[244:247], v[48:51]\n"
"v_exp_f32 v170, v170\n"
"v_mfma_f32_16x16x32_bf16 v[48:51], v[140:143], a[248:251], v[48:51]\n"
"v_exp_f32 v156, v156\n"
"v_cvt_pk_bf16_f32 v102, v168, v169\n"
"v_exp_f32 v173, v173\n"
"v_exp_f32 v174, v174\n"
"v_exp_f32 v179, v179\n"
"v_mfma_f32_16x16x32_bf16 v[80:83], v[144:147], a[244:247], v[80:83]\n"
"ds_read_b64_tr_b16 a[246:247], v234 offset:53248\n"
"v_cvt_pk_bf16_f32 v108, v160, v161\n"
"v_mfma_f32_16x16x32_bf16 v[80:83], v[148:151], a[248:251], v[80:83]\n"
"ds_read_b64_tr_b16 a[244:245], v234 offset:49152\n"
"v_cvt_pk_bf16_f32 v123, v154, v155\n"
"v_mfma_f32_16x16x32_bf16 v[84:87], v[148:151], v[224:227], v[84:87]\n"
"ds_read_b64_tr_b16 a[250:251], v238 offset:53248\n"
"v_cvt_pk_bf16_f32 v100, v156, v157\n"
"v_mfma_f32_16x16x32_bf16 v[84:87], v[144:147], a[252:255], v[84:87]\n"
"ds_read_b64_tr_b16 a[248:249], v238 offset:49152\n"
"v_cvt_pk_bf16_f32 v117, v166, v167\n"
"v_mfma_f32_16x16x32_bf16 v[52:55], v[140:143], v[224:227], v[52:55]\n"
"v_exp_f32 v178, v178\n"
"v_mfma_f32_16x16x32_bf16 v[52:55], v[136:139], a[252:255], v[52:55]\n"
"v_cvt_pk_bf16_f32 v101, v158, v159\n"
"v_exp_f32 v171, v171\n"
"v_exp_f32 v172, v172\n"
"v_exp_f32 v175, v175\n"
"v_exp_f32 v176, v176\n"
"v_mfma_f32_16x16x32_bf16 v[20:23], v[132:135], v[224:227], v[20:23]\n"
"v_exp_f32 v177, v177\n"
"v_mfma_f32_16x16x32_bf16 v[20:23], v[128:131], a[252:255], v[20:23]\n"
"v_cvt_pk_bf16_f32 v116, v164, v165\n"
"v_cvt_pk_bf16_f32 v103, v170, v171\n"
"v_mfma_f32_16x16x32_bf16 a[228:231], v[124:127], a[252:255], a[228:231]\n"
"s_waitcnt lgkmcnt(2)\n"
"ds_read_b64_tr_b16 v[224:225], v239 offset:49152\n"
"ds_read_b64_tr_b16 v[226:227], v239 offset:53248\n"
"v_mfma_f32_16x16x32_bf16 a[216:219], v[120:123], a[244:247], a[216:219]\n"
"s_add_u32 m0, s44, 81920\n"
"ds_read_b128 v[212:215], v229 offset:0\n"
"v_cvt_pk_bf16_f32 v110, v172, v173\n"
"v_smfmac_f32_16x16x64_bf16 v[196:199], a[240:243], v[96:103], v241\n"
"buffer_load_dwordx4 v240, s[20:23], s56 offen nt lds\n"
"v_mfma_f32_16x16x32_bf16 v[8:11], v[96:99], a[244:247], v[8:11]\n"
"s_waitcnt lgkmcnt(3)\n"
"v_cvt_pk_bf16_f32 v118, v176, v177\n"
"v_cvt_pk_bf16_f32 v119, v178, v179\n"
"v_mfma_f32_16x16x32_bf16 v[8:11], v[100:103], a[248:251], v[8:11]\n"
"ds_read_b64_tr_b16 a[254:255], v235 offset:53248\n"
"v_cvt_pk_bf16_f32 v109, v162, v163\n"
"v_smfmac_f32_16x16x64_bf16 v[200:203], a[240:243], v[112:119], v241\n"
"ds_read_b64_tr_b16 a[252:253], v235 offset:49152\n"
"v_cvt_pk_bf16_f32 v111, v174, v175\n"
"v_mfma_f32_16x16x32_bf16 v[40:43], v[104:107], a[244:247], v[40:43]\n"
"ds_read_b128 v[220:223], v229 offset:1024\n"
"v_add_f32 v196, v196, v197\n"
"v_mfma_f32_16x16x32_bf16 v[40:43], v[108:111], a[248:251], v[40:43]\n"
"v_and_b32 v245, 0x3f0, v242\n"
"v_lshrrev_b32 v245, 1, v245\n"
"v_smfmac_f32_16x16x64_bf16 v[198:201], a[240:243], v[104:111], v241\n"
"ds_read_b128 v[208:211], v228 offset:0\n"
"v_lshlrev_b32_e64 v244, 24, s43\n"
"v_mfma_f32_16x16x32_bf16 v[72:75], v[112:115], a[244:247], v[72:75]\n"
"ds_read_b64_tr_b16 a[246:247], v234 offset:54272\n"
"v_mul_f32 v244, 0x45800000, v244\n"
"v_mfma_f32_16x16x32_bf16 v[72:75], v[116:119], a[248:251], v[72:75]\n"
"ds_read_b64_tr_b16 a[244:245], v234 offset:50176\n"
"v_sub_u32 v245, v242, v245\n"
"v_mfma_f32_16x16x32_bf16 v[202:205], a[240:243], v[120:123], v[202:205]\n"
"s_waitcnt lgkmcnt(6)\n"
"ds_read_b64_tr_b16 a[250:251], v238 offset:54272\n"
"v_add_f32 v198, v198, v199\n"
"v_mfma_f32_16x16x32_bf16 v[76:79], v[116:119], v[224:227], v[76:79]\n"
"s_waitcnt lgkmcnt(5)\n"
"ds_read_b64_tr_b16 a[248:249], v238 offset:50176\n"
"v_add_f32 v254, v196, v198\n"
"v_mfma_f32_16x16x32_bf16 v[76:79], v[112:115], a[252:255], v[76:79]\n"
"v_add_f32 v200, v200, v201\n"
"v_add_f32 v254, v254, v200\n"
"v_mfma_f32_16x16x32_bf16 v[44:47], v[108:111], v[224:227], v[44:47]\n"
"v_add_f32 v244, v244, v202\n"
"v_add_f32 v254, v254, v202\n"
"v_mfma_f32_16x16x32_bf16 v[44:47], v[104:107], a[252:255], v[44:47]\n"
"v_min3_f32 v244, v244, v196, v198\n"
"v_min_f32 v244, v244, v200\n"
"v_mfma_f32_16x16x32_bf16 v[12:15], v[100:103], v[224:227], v[12:15]\n"
"s_add_u32 s40, s56, s52\n"
"v_rcp_f32 v244, v244\n"
"v_mfma_f32_16x16x32_bf16 v[12:15], v[96:99], a[252:255], v[12:15]\n"
"buffer_load_dwordx4 v240, s[20:23], s40 offen offset:1024 nt lds\n"
"v_fmac_f32 v254, 0x68800000, v244\n"
"v_xor_b32 v244, 0x11000, v242\n"
"v_mfma_f32_16x16x32_bf16 a[220:223], v[120:123], a[252:255], a[220:223]\n"
"s_waitcnt lgkmcnt(2)\n"
"v_cmp_nge_f32 vcc, s45, v254\n"
"s_cbranch_vccnz LSLOW_%=\nLBACK2_%=:\ns_mov_b64 vcc, s[64:65]\n"
"v_mfma_f32_16x16x32_bf16 a[232:235], v[120:123], a[244:247], a[232:235]\n"
"v_rcp_f32 v246, v196\n"
"v_rcp_f32 v206, v200\n"
"v_accvgpr_mov_b32 a1, a145\n"
"v_accvgpr_mov_b32 a35, a179\n"
"v_accvgpr_mov_b32 a37, a181\n"
"v_accvgpr_mov_b32 a45, a189\n"
"v_bfe_u32 v149, v242, 8, 2\n"
"v_lshl_add_u32 v149, v149, 4, s43\n"
"v_mfma_f32_16x16x32_bf16 v[24:27], v[96:99], a[244:247], v[24:27]\n"
"s_waitcnt lgkmcnt(0)\n"
"ds_read_b128 v[124:127], v149 offset:0\n"
"v_accvgpr_mov_b32 a2, a146\n"
"v_accvgpr_mov_b32 a5, a149\n"
"v_accvgpr_mov_b32 a8, a152\n"
"v_accvgpr_mov_b32 a13, a157\n"
"v_accvgpr_mov_b32 a17, a161\n"
"v_accvgpr_mov_b32 a21, a165\n"
"v_xor_b32 v148, 0x400, v243\n"
"v_xor_b32 v150, 64, v149\n"
"v_mfma_f32_16x16x32_bf16 v[24:27], v[100:103], a[248:251], v[24:27]\n"
"ds_read_b128 v[132:135], v149 offset:1024\n"
"v_mov_b32_dpp v246, v246 row_ror:12 row_mask:0x2 bank_mask:0xf\n"
"v_accvgpr_mov_b32 a0, a144\n"
"v_accvgpr_mov_b32 a4, a148\n"
"v_accvgpr_mov_b32 a7, a151\n"
"v_accvgpr_mov_b32 a14, a158\n"
"v_accvgpr_mov_b32 a22, a166\n"
"v_accvgpr_mov_b32 a27, a171\n"
"v_accvgpr_mov_b32 a32, a176\n"
"v_accvgpr_mov_b32 a42, a186\n"
"v_mfma_f32_16x16x32_bf16 v[56:59], v[104:107], a[244:247], v[56:59]\n"
"ds_read_b128 v[128:131], v150 offset:0\n"
"v_rcp_f32 v247, v198\n"
"v_mov_b32_dpp v246, v246 row_ror:8 row_mask:0x4 bank_mask:0xf\n"
"v_accvgpr_mov_b32 a9, a153\n"
"v_accvgpr_mov_b32 a25, a169\n"
"v_accvgpr_mov_b32 a31, a175\n"
"v_accvgpr_mov_b32 a33, a177\n"
"v_accvgpr_mov_b32 a41, a185\n"
"v_accvgpr_mov_b32 a166, a70\n"
"v_mfma_f32_16x16x32_bf16 v[56:59], v[108:111], a[248:251], v[56:59]\n"
"ds_read_b128 v[136:139], v150 offset:1024\n"
"v_mov_b32_dpp v247, v247 row_ror:12 row_mask:0x2 bank_mask:0xf\n"
"v_mov_b32 v253, v202\n"
"v_accvgpr_mov_b32 a3, a147\n"
"v_accvgpr_mov_b32 a6, a150\n"
"v_accvgpr_mov_b32 a12, a156\n"
"v_accvgpr_mov_b32 a16, a160\n"
"v_accvgpr_mov_b32 a24, a168\n"
"v_accvgpr_mov_b32 a26, a170\n"
"v_accvgpr_mov_b32 a30, a174\n"
"v_mfma_f32_16x16x32_bf16 v[88:91], v[112:115], a[244:247], v[88:91]\n"
"v_accvgpr_mov_b32 a34, a178\n"
"v_accvgpr_mov_b32 a43, a187\n"
"v_mfma_f32_16x16x32_bf16 v[88:91], v[116:119], a[248:251], v[88:91]\n"
"s_waitcnt lgkmcnt(3)\n"
"ds_read_b128 a[196:199], v148 offset:4096\n"
"v_mov_b32_dpp v246, v246 row_ror:4 row_mask:0x8 bank_mask:0xf\n"
"v_accvgpr_mov_b32 a11, a155\n"
"v_accvgpr_mov_b32 a18, a162\n"
"v_accvgpr_mov_b32 a19, a163\n"
"v_accvgpr_mov_b32 a39, a183\n"
"v_accvgpr_mov_b32 a40, a184\n"
"v_accvgpr_mov_b32 a46, a190\n"
"v_accvgpr_mov_b32 a156, a60\n"
"v_accvgpr_mov_b32 a144, a48\n"
"v_mfma_f32_16x16x32_bf16 v[180:183], v[124:127], a[0:3], 0\n"
"s_waitcnt lgkmcnt(2)\n"
"ds_read_b64_tr_b16 a[252:253], v235 offset:50176\n"
"v_accvgpr_mov_b32 a10, a154\n"
"v_mfma_f32_16x16x32_bf16 v[180:183], v[128:131], a[4:7], v[180:183]\n"
"ds_read_b128 a[192:195], v243 offset:4096\n"
"v_accvgpr_mov_b32 a15, a159\n"
"v_mfma_f32_16x16x32_bf16 v[180:183], v[132:135], a[8:11], v[180:183]\n"
"s_waitcnt lgkmcnt(3)\n"
"ds_read_b128 a[200:203], v243 offset:6144\n"
"v_accvgpr_mov_b32 a161, a65\n"
"v_mfma_f32_16x16x32_bf16 v[180:183], v[136:139], a[12:15], v[180:183]\n"
"ds_read_b128 a[204:207], v148 offset:6144\n"
"v_accvgpr_mov_b32 a20, a164\n"
"v_accvgpr_mov_b32 a23, a167\n"
"v_accvgpr_mov_b32 a28, a172\n"
"v_accvgpr_mov_b32 a36, a180\n"
"v_accvgpr_mov_b32 a38, a182\n"
"v_accvgpr_mov_b32 a47, a191\n"
"v_accvgpr_mov_b32 a148, a52\n"
"v_mul_f32_dpp v25, v246, v25 row_newbcast:1 row_mask:0xf bank_mask:0xf\n"
"v_mfma_f32_16x16x32_bf16 v[184:187], v[124:127], a[16:19], 0\n"
"v_accvgpr_mov_b32 a44, a188\n"
"v_mul_f32_dpp v27, v246, v27 row_newbcast:3 row_mask:0xf bank_mask:0xf\n"
"v_mfma_f32_16x16x32_bf16 v[184:187], v[128:131], a[20:23], v[184:187]\n"
"v_mov_b32 v252, v192\n"
"v_accvgpr_mov_b32 a29, a173\n"
"v_mfma_f32_16x16x32_bf16 v[184:187], v[132:135], a[24:27], v[184:187]\n"
"s_add_u32 s40, s56, s53\n"
"ds_read_b128 v[216:219], v228 offset:1024\n"
"v_sub_f32 v180, s74, v180\n"
"v_mfma_f32_16x16x32_bf16 v[184:187], v[136:139], a[28:31], v[184:187]\n"
"buffer_load_dwordx4 v240, s[20:23], s40 offen offset:2048 nt lds\n"
"v_mov_b32_dpp v206, v206 row_ror:12 row_mask:0x2 bank_mask:0xf\n"
"v_accvgpr_mov_b32 a149, a53\n"
"v_accvgpr_mov_b32 a164, a68\n"
"v_accvgpr_mov_b32 a180, a84\n"
"v_accvgpr_mov_b32 a190, a94\n"
"v_accvgpr_mov_b32 a147, a51\n"
"v_accvgpr_mov_b32 a179, a83\n"
"v_accvgpr_mov_b32 a187, a91\n"
"v_mfma_f32_16x16x32_bf16 v[188:191], v[124:127], a[32:35], 0\n"
"v_mov_b32_dpp v247, v247 row_ror:8 row_mask:0x4 bank_mask:0xf\n"
"v_accvgpr_mov_b32 a163, a67\n"
"v_mfma_f32_16x16x32_bf16 v[188:191], v[128:131], a[36:39], v[188:191]\n"
"v_mov_b32_dpp v247, v247 row_ror:4 row_mask:0x8 bank_mask:0xf\n"
"v_sub_f32 v183, s74, v183\n"
"v_mfma_f32_16x16x32_bf16 v[188:191], v[132:135], a[40:43], v[188:191]\n"
"v_accvgpr_mov_b32 a150, a54\n"
"v_sub_f32 v181, s74, v181\n"
"v_mfma_f32_16x16x32_bf16 v[188:191], v[136:139], a[44:47], v[188:191]\n"
"s_waitcnt lgkmcnt(1)\n"
"ds_write_b128 v244, a[232:235] offset:34816\n"
"v_mov_b32_dpp v206, v206 row_ror:8 row_mask:0x4 bank_mask:0xf\n"
"v_accvgpr_mov_b32 a172, a76\n"
"v_accvgpr_mov_b32 a173, a77\n"
"v_sub_f32 v185, s74, v185\n"
"v_sub_f32 v187, s74, v187\n"
"v_mfma_f32_16x16x32_bf16 v[192:195], v[124:127], a[192:195], 0\n"
"v_accvgpr_mov_b32 a151, a55\n"
"v_sub_f32 v182, s74, v182\n"
"v_mfma_f32_16x16x32_bf16 v[192:195], v[128:131], a[196:199], v[192:195]\n"
"ds_read_b64_tr_b16 a[246:247], v232 offset:53248\n"
"v_accvgpr_mov_b32 a146, a50\n"
"v_mfma_f32_16x16x32_bf16 v[192:195], v[132:135], a[200:203], v[192:195]\n"
"v_accvgpr_mov_b32 a157, a61\n"
"v_accvgpr_mov_b32 a169, a73\n"
"v_mfma_f32_16x16x32_bf16 v[192:195], v[136:139], a[204:207], v[192:195]\n"
"ds_read_b64_tr_b16 v[224:225], v239 offset:50176\n"
"ds_read_b64_tr_b16 v[226:227], v239 offset:54272\n"
"v_accvgpr_mov_b32 a183, a87\n"
"v_accvgpr_mov_b32 a145, a49\n"
"v_accvgpr_mov_b32 a176, a80\n"
"v_accvgpr_mov_b32 a178, a82\n"
"v_accvgpr_mov_b32 a171, a75\n"
"v_accvgpr_mov_b32 a185, a89\n"
"v_sub_f32 v184, s74, v184\n"
"v_mul_f32_dpp v56, v247, v56 row_newbcast:0 row_mask:0xf bank_mask:0xf\n"
"v_mfma_f32_16x16x32_bf16 v[152:155], v[208:211], a[0:3], v[180:183]\n"
"v_mul_f32_dpp v45, v247, v45 row_newbcast:1 row_mask:0xf bank_mask:0xf\n"
"v_sub_f32 v186, s74, v186\n"
"v_mfma_f32_16x16x32_bf16 v[152:155], v[212:215], a[4:7], v[152:155]\n"
"s_waitcnt lgkmcnt(4)\n"
"v_accvgpr_mov_b32 a175, a79\n"
"v_sub_f32 v188, s74, v188\n"
"v_mfma_f32_16x16x32_bf16 v[152:155], v[216:219], a[8:11], v[152:155]\n"
"s_add_u32 s40, s56, s54\n"
"v_sub_f32 v195, s74, v195\n"
"v_mov_b64 v[196:197], 0\n"
"v_mfma_f32_16x16x32_bf16 v[152:155], v[220:223], a[12:15], v[152:155]\n"
"buffer_load_dwordx4 v240, s[20:23], s40 offen offset:3072 nt lds\n"
"v_accvgpr_mov_b32 a191, a95\n"
"v_accvgpr_mov_b32 a162, a66\n"
"v_accvgpr_mov_b32 a153, a57\n"
"v_mul_f32_dpp v15, v246, v15 row_newbcast:3 row_mask:0xf bank_mask:0xf\n"
"v_sub_f32 v190, s74, v190\n"
"v_sub_f32 v191, s74, v191\n"
"v_sub_f32 v192, s74, v192\n"
"v_mfma_f32_16x16x32_bf16 v[156:159], v[216:219], a[24:27], v[184:187]\n"
"s_add_u32 s55, s55, s59\n"
"v_mov_b32_dpp v206, v206 row_ror:4 row_mask:0x8 bank_mask:0xf\n"
"v_accvgpr_mov_b32 a186, a90\n"
"v_mfma_f32_16x16x32_bf16 v[156:159], v[220:223], a[28:31], v[156:159]\n"
"s_add_u32 s41, s55, s57\n"
"v_accvgpr_mov_b32 a158, a62\n"
"v_sub_f32 v189, s74, v189\n"
"v_mfma_f32_16x16x32_bf16 v[156:159], v[208:211], a[16:19], v[156:159]\n"
"s_and_b32 s41, s41, s58\n"
"v_mul_f32_dpp v41, v247, v41 row_newbcast:1 row_mask:0xf bank_mask:0xf\n"
"v_mul_f32_dpp v26, v246, v26 row_newbcast:2 row_mask:0xf bank_mask:0xf\n"
"v_mfma_f32_16x16x32_bf16 v[156:159], v[212:215], a[20:23], v[156:159]\n"
"s_andn2_b32 s56, s55, s58\n"
"ds_read_b64_tr_b16 a[254:255], v235 offset:54272\n"
"v_accvgpr_mov_b32 a167, a71\n"
"v_accvgpr_mov_b32 a181, a85\n"
"v_accvgpr_mov_b32 a174, a78\n"
"v_accvgpr_mov_b32 a188, a92\n"
"v_accvgpr_mov_b32 a154, a58\n"
"v_accvgpr_mov_b32 a168, a72\n"
"v_mul_f32_dpp v46, v247, v46 row_newbcast:2 row_mask:0xf bank_mask:0xf\n"
"v_sub_f32 v193, s74, v193\n"
"v_mul_f32_dpp v58, v247, v58 row_newbcast:2 row_mask:0xf bank_mask:0xf\n"
"v_mfma_f32_16x16x32_bf16 v[160:163], v[208:211], a[32:35], v[188:191]\n"
"s_or_b32 s56, s56, s41\n"
"v_accvgpr_mov_b32 a165, a69\n"
"v_sub_f32 v194, s74, v194\n"
"v_mfma_f32_16x16x32_bf16 v[160:163], v[212:215], a[36:39], v[160:163]\n"
"v_accvgpr_mov_b32 a159, a63\n"
"v_accvgpr_mov_b32 a170, a74\n"
"v_mfma_f32_16x16x32_bf16 v[160:163], v[216:219], a[40:43], v[160:163]\n"
"v_accvgpr_mov_b32 a155, a59\n"
"v_accvgpr_mov_b32 a184, a88\n"
"v_mfma_f32_16x16x32_bf16 v[160:163], v[220:223], a[44:47], v[160:163]\n"
"ds_write_b128 v244, a[216:219] offset:32768\n"
"v_accvgpr_mov_b32 a160, a64\n"
"v_accvgpr_mov_b32 a177, a81\n"
"v_mul_f32_dpp v8, v246, v8 row_newbcast:0 row_mask:0xf bank_mask:0xf\n"
"v_exp_f32 v154, v154\n"
"v_mfma_f32_16x16x32_bf16 v[164:167], v[216:219], a[200:203], v[192:195]\n"
"v_exp_f32 v153, v153\n"
"v_mfma_f32_16x16x32_bf16 v[164:167], v[220:223], a[204:207], v[164:167]\n"
"ds_read_b128 v[220:223], v229 offset:5120\n"
"v_mul_f32_dpp v9, v246, v9 row_newbcast:1 row_mask:0xf bank_mask:0xf\n"
"v_mfma_f32_16x16x32_bf16 v[164:167], v[208:211], a[192:195], v[164:167]\n"
"v_accvgpr_mov_b32 a189, a93\n"
"v_accvgpr_mov_b32 a152, a56\n"
"v_mfma_f32_16x16x32_bf16 v[164:167], v[212:215], a[196:199], v[164:167]\n"
"s_waitcnt lgkmcnt(2)\n"
"ds_read_b128 v[216:219], v228 offset:5120\n"
"v_accvgpr_mov_b32 a182, a86\n"
"v_mul_f32_dpp v40, v247, v40 row_newbcast:0 row_mask:0xf bank_mask:0xf\n"
"v_exp_f32 v155, v155\n"
"v_exp_f32 v160, v160\n"
"v_exp_f32 v163, v163\n"
"LTC_%=:\n"
"s_waitcnt lgkmcnt(3)\n"
"v_mfma_f32_16x16x32_bf16 v[92:95], v[116:119], v[224:227], v[92:95]\n"
"ds_read_b128 v[212:215], v229 offset:4096\n"
"v_mul_f32_dpp v59, v247, v59 row_newbcast:3 row_mask:0xf bank_mask:0xf\n"
"v_mfma_f32_16x16x32_bf16 v[92:95], v[112:115], a[252:255], v[92:95]\n"
"ds_read_b128 v[208:211], v228 offset:4096\n"
"v_mul_f32_dpp v79, v206, v79 row_newbcast:3 row_mask:0xf bank_mask:0xf\n"
"v_mfma_f32_16x16x32_bf16 v[60:63], v[108:111], v[224:227], v[60:63]\n"
"s_add_u32 m0, s44, 32768\n"
"v_mul_f32_dpp v10, v246, v10 row_newbcast:2 row_mask:0xf bank_mask:0xf\n"
"v_cvt_pk_bf16_f32 v129, v154, v155\n"
"v_mfma_f32_16x16x32_bf16 v[60:63], v[104:107], a[252:255], v[60:63]\n"
"buffer_load_dwordx4 v240, s[16:19], s56 offen nt lds\n"
"v_cvt_pk_bf16_f32 v40, v40, v41\n"
"v_mul_f32_dpp v72, v206, v72 row_newbcast:0 row_mask:0xf bank_mask:0xf\n"
"v_exp_f32 v152, v152\n"
"v_exp_f32 v166, v166\n"
"v_mul_f32_dpp v57, v247, v57 row_newbcast:1 row_mask:0xf bank_mask:0xf\n"
"v_mov_b64 v[200:201], 0\n"
"v_mfma_f32_16x16x32_bf16 v[28:31], v[100:103], v[224:227], v[28:31]\n"
"v_cvt_pk_bf16_f32 v8, v8, v9\n"
"v_mul_f32_dpp v42, v247, v42 row_newbcast:2 row_mask:0xf bank_mask:0xf\n"
"v_mfma_f32_16x16x32_bf16 v[28:31], v[96:99], a[252:255], v[28:31]\n"
"v_cvt_pk_bf16_f32 v128, v152, v153\n"
"v_mul_f32_dpp v24, v246, v24 row_newbcast:0 row_mask:0xf bank_mask:0xf\n"
"v_mfma_f32_16x16x32_bf16 a[236:239], v[120:123], a[252:255], a[236:239]\n"
"s_waitcnt lgkmcnt(2)\n"
"v_exp_f32 v159, v159\n"
"v_mfma_f32_16x16x32_bf16 v[168:171], v[216:219], a[8:11], v[180:183]\n"
"v_mul_f32_dpp v47, v247, v47 row_newbcast:3 row_mask:0xf bank_mask:0xf\n"
"v_mul_f32_dpp v88, v206, v88 row_newbcast:0 row_mask:0xf bank_mask:0xf\n"
"v_mfma_f32_16x16x32_bf16 v[168:171], v[220:223], a[12:15], v[168:171]\n"
"s_waitcnt lgkmcnt(0)\n"
"ds_read_b64_tr_b16 a[244:245], v232 offset:49152\n"
"v_mul_f32_dpp v75, v206, v75 row_newbcast:3 row_mask:0xf bank_mask:0xf\n"
"v_mfma_f32_16x16x32_bf16 v[168:171], v[208:211], a[0:3], v[168:171]\n"
"v_exp_f32 v158, v158\n"
"v_mfma_f32_16x16x32_bf16 v[168:171], v[212:215], a[4:7], v[168:171]\n"
"ds_write_b128 v244, a[236:239] offset:35840\n"
"v_mul_f32_dpp v43, v247, v43 row_newbcast:3 row_mask:0xf bank_mask:0xf\n"
"v_exp_f32 v162, v162\n"
"v_exp_f32 v167, v167\n"
"v_mfma_f32_16x16x32_bf16 v[172:175], v[208:211], a[16:19], v[184:187]\n"
"v_exp_f32 v165, v165\n"
"v_mfma_f32_16x16x32_bf16 v[172:175], v[212:215], a[20:23], v[172:175]\n"
"v_exp_f32 v161, v161\n"
"v_mfma_f32_16x16x32_bf16 v[172:175], v[216:219], a[24:27], v[172:175]\n"
"ds_read_b64_tr_b16 a[250:251], v236 offset:53248\n"
"v_mul_f32_dpp v77, v206, v77 row_newbcast:1 row_mask:0xf bank_mask:0xf\n"
"v_mfma_f32_16x16x32_bf16 v[172:175], v[220:223], a[28:31], v[172:175]\n"
"ds_write_b128 v244, a[220:223] offset:33792\n"
"v_mul_f32_dpp v11, v246, v11 row_newbcast:3 row_mask:0xf bank_mask:0xf\n"
"v_mul_f32_dpp v73, v206, v73 row_newbcast:1 row_mask:0xf bank_mask:0xf\n"
"v_mul_f32_dpp v94, v206, v94 row_newbcast:2 row_mask:0xf bank_mask:0xf\n"
"v_exp_f32 v171, v171\n"
"v_mfma_f32_16x16x32_bf16 v[176:179], v[216:219], a[40:43], v[188:191]\n"
"v_mul_f32_dpp v89, v206, v89 row_newbcast:1 row_mask:0xf bank_mask:0xf\n"
"v_cvt_pk_bf16_f32 v88, v88, v89\n"
"v_mfma_f32_16x16x32_bf16 v[176:179], v[220:223], a[44:47], v[176:179]\n"
"ds_read_b64_tr_b16 a[248:249], v236 offset:49152\n"
"v_xor_b32 v250, 0x1000, v245\n"
"v_mfma_f32_16x16x32_bf16 v[176:179], v[208:211], a[32:35], v[176:179]\n"
"s_add_u32 s40, s56, s52\n"
"ds_read_b64_tr_b16 a[252:253], v233 offset:49152\n"
"v_cvt_pk_bf16_f32 v144, v160, v161\n"
"v_mfma_f32_16x16x32_bf16 v[176:179], v[212:215], a[36:39], v[176:179]\n"
"buffer_load_dwordx4 v240, s[16:19], s40 offen offset:1024 nt lds\n"
"v_mul_f32_dpp v12, v246, v12 row_newbcast:0 row_mask:0xf bank_mask:0xf\n"
"v_exp_f32 v156, v156\n"
"v_exp_f32 v157, v157\n"
"v_mul_f32_dpp v60, v247, v60 row_newbcast:0 row_mask:0xf bank_mask:0xf\n"
"v_exp_f32 v169, v169\n"
"v_mfma_f32_16x16x32_bf16 v[152:155], v[208:211], a[192:195], v[192:195]\n"
"v_mul_f32_dpp v76, v206, v76 row_newbcast:0 row_mask:0xf bank_mask:0xf\n"
"v_mul_f32_dpp v29, v246, v29 row_newbcast:1 row_mask:0xf bank_mask:0xf\n"
"v_mfma_f32_16x16x32_bf16 v[152:155], v[212:215], a[196:199], v[152:155]\n"
"v_exp_f32 v164, v164\n"
"v_mfma_f32_16x16x32_bf16 v[152:155], v[216:219], a[200:203], v[152:155]\n"
"ds_read_b128 v[212:215], v231 offset:0\n"
"v_cvt_pk_bf16_f32 v56, v56, v57\n"
"v_mfma_f32_16x16x32_bf16 v[152:155], v[220:223], a[204:207], v[152:155]\n"
"s_waitcnt lgkmcnt(3)\n"
"v_cvt_pk_bf16_f32 v9, v10, v11\n"
"v_mul_f32_dpp v92, v206, v92 row_newbcast:0 row_mask:0xf bank_mask:0xf\n"
"v_mfma_f32_16x16x32_bf16 a[208:211], v[120:123], a[244:247], a[208:211]\n"
"ds_read_b128 v[208:211], v230 offset:0\n"
"v_cvt_pk_bf16_f32 v41, v42, v43\n"
"v_mul_f32_dpp v78, v206, v78 row_newbcast:2 row_mask:0xf bank_mask:0xf\n"
"v_cvt_pk_bf16_f32 v72, v72, v73\n"
"v_mul_f32_dpp v31, v246, v31 row_newbcast:3 row_mask:0xf bank_mask:0xf\n"
"v_mul_f32_dpp v61, v247, v61 row_newbcast:1 row_mask:0xf bank_mask:0xf\n"
"v_exp_f32 v177, v177\n"
"v_mfma_f32_16x16x32_bf16 v[0:3], v[96:99], a[244:247], v[0:3]\n"
"s_waitcnt lgkmcnt(3)\n"
"ds_read_b128 v[216:219], v230 offset:1024\n"
"v_cvt_pk_bf16_f32 v145, v162, v163\n"
"v_mfma_f32_16x16x32_bf16 v[0:3], v[100:103], a[248:251], v[0:3]\n"
"ds_read_b128 v[220:223], v231 offset:1024\n"
"v_cvt_pk_bf16_f32 v137, v158, v159\n"
"v_mul_f32_dpp v91, v206, v91 row_newbcast:3 row_mask:0xf bank_mask:0xf\n"
"v_exp_f32 v170, v170\n"
"v_exp_f32 v172, v172\n"
"v_exp_f32 v155, v155\n"
"v_mfma_f32_16x16x32_bf16 v[32:35], v[104:107], a[244:247], v[32:35]\n"
"v_cvt_pk_bf16_f32 v136, v156, v157\n"
"v_xor_b32 v244, 0x1000, v244\n"
"v_mfma_f32_16x16x32_bf16 v[32:35], v[108:111], a[248:251], v[32:35]\n"
"ds_read_b64_tr_b16 a[254:255], v233 offset:53248\n"
"v_mul_f32_dpp v90, v206, v90 row_newbcast:2 row_mask:0xf bank_mask:0xf\n"
"v_mfma_f32_16x16x32_bf16 v[64:67], v[112:115], a[244:247], v[64:67]\n"
"ds_read_b64_tr_b16 v[224:225], v237 offset:49152\n"
"v_mul_f32_dpp v93, v206, v93 row_newbcast:1 row_mask:0xf bank_mask:0xf\n"
"v_mfma_f32_16x16x32_bf16 v[64:67], v[116:119], a[248:251], v[64:67]\n"
"s_waitcnt lgkmcnt(4)\n"
"ds_read_b64_tr_b16 v[226:227], v237 offset:53248\n"
"v_exp_f32 v168, v168\n"
"v_exp_f32 v175, v175\n"
"v_exp_f32 v176, v176\n"
"v_exp_f32 v179, v179\n"
"v_mfma_f32_16x16x32_bf16 v[156:159], v[208:211], a[0:3], v[180:183]\n"
"v_mul_f32_dpp v13, v246, v13 row_newbcast:1 row_mask:0xf bank_mask:0xf\n"
"v_mul_f32_dpp v63, v247, v63 row_newbcast:3 row_mask:0xf bank_mask:0xf\n"
"v_mfma_f32_16x16x32_bf16 v[156:159], v[212:215], a[4:7], v[156:159]\n"
"s_waitcnt lgkmcnt(4)\n"
"ds_read_b64_tr_b16 a[246:247], v232 offset:54272\n"
"v_cvt_pk_bf16_f32 v125, v166, v167\n"
"v_mfma_f32_16x16x32_bf16 v[156:159], v[216:219], a[8:11], v[156:159]\n"
"s_waitcnt lgkmcnt(4)\n"
"s_add_u32 s40, s56, s53\n"
"v_mul_f32_dpp v74, v206, v74 row_newbcast:2 row_mask:0xf bank_mask:0xf\n"
"v_mul_f32_dpp v2, v246, v2 row_newbcast:2 row_mask:0xf bank_mask:0xf\n"
"v_mfma_f32_16x16x32_bf16 v[156:159], v[220:223], a[12:15], v[156:159]\n"
"buffer_load_dwordx4 v240, s[16:19], s40 offen offset:2048 nt lds\n"
"v_cvt_pk_bf16_f32 v124, v164, v165\n"
"v_cvt_pk_bf16_f32 v24, v24, v25\n"
"v_mul_f32_dpp v62, v247, v62 row_newbcast:2 row_mask:0xf bank_mask:0xf\n"
"v_exp_f32 v173, v173\n"
"v_exp_f32 v154, v154\n"
"v_accvgpr_read_b32 v255, a209\n"
"v_mfma_f32_16x16x32_bf16 v[160:163], v[216:219], a[24:27], v[184:187]\n"
"v_cvt_pk_bf16_f32 v57, v58, v59\n"
"v_cvt_pk_bf16_f32 v89, v90, v91\n"
"v_mfma_f32_16x16x32_bf16 v[160:163], v[220:223], a[28:31], v[160:163]\n"
"v_mul_f32_dpp v14, v246, v14 row_newbcast:2 row_mask:0xf bank_mask:0xf\n"
"v_cvt_pk_bf16_f32 v73, v74, v75\n"
"v_mfma_f32_16x16x32_bf16 v[160:163], v[208:211], a[16:19], v[160:163]\n"
"v_cvt_pk_bf16_f32 v59, v62, v63\n"
"v_cvt_pk_bf16_f32 v146, v176, v177\n"
"v_mfma_f32_16x16x32_bf16 v[160:163], v[212:215], a[20:23], v[160:163]\n"
"ds_write_b64 v250, v[252:253] offset:52224\n"
"v_cvt_pk_bf16_f32 v10, v12, v13\n"
"v_cvt_pk_bf16_f32 v130, v168, v169\n"
"v_exp_f32 v174, v174\n"
"v_exp_f32 v157, v157\n"
"v_mul_f32_dpp v0, v246, v0 row_newbcast:0 row_mask:0xf bank_mask:0xf\n"
"v_mfma_f32_16x16x32_bf16 v[164:167], v[208:211], a[32:35], v[188:191]\n"
"ds_read_b128 v[208:211], v230 offset:4096\n"
"v_cvt_pk_bf16_f32 v75, v78, v79\n"
"v_mfma_f32_16x16x32_bf16 v[164:167], v[212:215], a[36:39], v[164:167]\n"
"v_cvt_pk_bf16_f32 v11, v14, v15\n"
"v_mul_f32_dpp v44, v247, v44 row_newbcast:0 row_mask:0xf bank_mask:0xf\n"
"v_mfma_f32_16x16x32_bf16 v[164:167], v[216:219], a[40:43], v[164:167]\n"
"ds_read_b64_tr_b16 a[244:245], v232 offset:50176\n"
"ds_read_b128 v[216:219], v230 offset:5120\n"
"v_mfma_f32_16x16x32_bf16 v[164:167], v[220:223], a[44:47], v[164:167]\n"
"s_waitcnt lgkmcnt(4)\n"
"v_cvt_pk_bf16_f32 v43, v46, v47\n"
"v_exp_f32 v178, v178\n"
"v_exp_f32 v152, v152\n"
"v_exp_f32 v158, v158\n"
"v_mul_f32_dpp v3, v246, v3 row_newbcast:3 row_mask:0xf bank_mask:0xf\n"
"v_mfma_f32_16x16x32_bf16 v[68:71], v[116:119], v[224:227], v[68:71]\n"
"ds_read_b128 v[212:215], v231 offset:4096\n"
"v_cvt_pk_bf16_f32 v25, v26, v27\n"
"v_mfma_f32_16x16x32_bf16 v[68:71], v[112:115], a[252:255], v[68:71]\n"
"v_mul_f32_dpp v33, v247, v33 row_newbcast:1 row_mask:0xf bank_mask:0xf\n"
"v_mov_b64 v[202:203], 0\n"
"v_mfma_f32_16x16x32_bf16 v[36:39], v[108:111], v[224:227], v[36:39]\n"
"s_add_u32 s40, s56, s54\n"
"ds_read_b64_tr_b16 a[248:249], v236 offset:50176\n"
"ds_read_b128 v[220:223], v231 offset:5120\n"
"v_mfma_f32_16x16x32_bf16 v[36:39], v[104:107], a[252:255], v[36:39]\n"
"buffer_load_dwordx4 v240, s[16:19], s40 offen offset:3072 nt lds\n"
"v_cvt_pk_bf16_f32 v42, v44, v45\n"
"v_cvt_pk_bf16_f32 v147, v178, v179\n"
"v_exp_f32 v160, v160\n"
"v_exp_f32 v161, v161\n"
"v_mul_f32_dpp v32, v247, v32 row_newbcast:0 row_mask:0xf bank_mask:0xf\n"
"v_mfma_f32_16x16x32_bf16 v[4:7], v[100:103], v[224:227], v[4:7]\n"
"v_cvt_pk_bf16_f32 v58, v60, v61\n"
"v_mul_f32_dpp v95, v206, v95 row_newbcast:3 row_mask:0xf bank_mask:0xf\n"
"v_mfma_f32_16x16x32_bf16 v[4:7], v[96:99], a[252:255], v[4:7]\n"
"v_mul_f32_dpp v30, v246, v30 row_newbcast:2 row_mask:0xf bank_mask:0xf\n"
"v_cvt_pk_bf16_f32 v138, v172, v173\n"
"v_cvt_pk_bf16_f32 v139, v174, v175\n"
"v_exp_f32 v159, v159\n"
"v_exp_f32 v162, v162\n"
"v_mul_f32_dpp v1, v246, v1 row_newbcast:1 row_mask:0xf bank_mask:0xf\n"
"v_mul_f32_dpp v67, v206, v67 row_newbcast:3 row_mask:0xf bank_mask:0xf\n"
"v_mfma_f32_16x16x32_bf16 a[212:215], v[120:123], a[252:255], a[212:215]\n"
"s_waitcnt lgkmcnt(3)\n"
"ds_write_b128 v248, v[8:11] offset:49152\n"
"v_cvt_pk_bf16_f32 v27, v30, v31\n"
"v_cvt_pk_bf16_f32 v131, v170, v171\n"
"v_cvt_pk_bf16_f32 v0, v0, v1\n"
"v_cvt_pk_bf16_f32 v32, v32, v33\n"
"v_mul_f32_dpp v66, v206, v66 row_newbcast:2 row_mask:0xf bank_mask:0xf\n"
"v_mfma_f32_16x16x32_bf16 v[168:171], v[208:211], a[0:3], v[180:183]\n"
"s_waitcnt lgkmcnt(3)\n"
"v_cvt_pk_bf16_f32 v74, v76, v77\n"
"v_mul_f32_dpp v70, v206, v70 row_newbcast:2 row_mask:0xf bank_mask:0xf\n"
"v_mfma_f32_16x16x32_bf16 v[168:171], v[212:215], a[4:7], v[168:171]\n"
"v_mul_f32_dpp v28, v246, v28 row_newbcast:0 row_mask:0xf bank_mask:0xf\n"
"v_mul_f32_dpp v4, v246, v4 row_newbcast:0 row_mask:0xf bank_mask:0xf\n"
"v_mfma_f32_16x16x32_bf16 v[168:171], v[216:219], a[8:11], v[168:171]\n"
"s_waitcnt lgkmcnt(1)\n"
"ds_read_b64_tr_b16 a[250:251], v236 offset:54272\n"
"v_cvt_pk_bf16_f32 v26, v28, v29\n"
"v_mfma_f32_16x16x32_bf16 v[168:171], v[220:223], a[12:15], v[168:171]\n"
"ds_read_b64_tr_b16 a[54:55], v249 offset:49160\n"
"v_cvt_pk_bf16_f32 v140, v160, v161\n"
"v_mfma_f32_16x16x32_bf16 v[172:175], v[216:219], a[24:27], v[184:187]\n"
"ds_write_b128 v248, v[40:43] offset:50176\n"
"v_exp_f32 v153, v153\n"
"v_mul_f32_dpp v6, v246, v6 row_newbcast:2 row_mask:0xf bank_mask:0xf\n"
"v_mul_f32_dpp v36, v247, v36 row_newbcast:0 row_mask:0xf bank_mask:0xf\n"
"v_mul_f32_dpp v38, v247, v38 row_newbcast:2 row_mask:0xf bank_mask:0xf\n"
"v_mfma_f32_16x16x32_bf16 v[172:175], v[220:223], a[28:31], v[172:175]\n"
"ds_read_b64_tr_b16 a[252:253], v233 offset:50176\n"
"v_cvt_pk_bf16_f32 v90, v92, v93\n"
"v_mfma_f32_16x16x32_bf16 v[172:175], v[208:211], a[16:19], v[172:175]\n"
"v_cvt_pk_bf16_f32 v126, v152, v153\n"
"v_mul_f32_dpp v39, v247, v39 row_newbcast:3 row_mask:0xf bank_mask:0xf\n"
"v_mfma_f32_16x16x32_bf16 v[172:175], v[212:215], a[20:23], v[172:175]\n"
"s_cbranch_vccnz LPCT0_%=\nLPKT0_%=:\n"
"ds_read_b64_tr_b16 a[52:53], v249 offset:49152\n"
"ds_read_b64_tr_b16 v[226:227], v237 offset:54272\n"
"v_cvt_pk_bf16_f32 v1, v2, v3\n"
"v_mul_f32_dpp v64, v206, v64 row_newbcast:0 row_mask:0xf bank_mask:0xf\n"
"v_mul_f32_dpp v65, v206, v65 row_newbcast:1 row_mask:0xf bank_mask:0xf\n"
"v_exp_f32 v169, v169\n"
"v_mov_b64 v[198:199], 0\n"
"v_mfma_f32_16x16x32_bf16 v[176:179], v[208:211], a[32:35], v[188:191]\n"
"ds_read_b64_tr_b16 a[70:71], v249 offset:50184\n"
"v_mul_f32_dpp v34, v247, v34 row_newbcast:2 row_mask:0xf bank_mask:0xf\n"
"v_mfma_f32_16x16x32_bf16 v[176:179], v[212:215], a[36:39], v[176:179]\n"
"ds_read_b64_tr_b16 a[254:255], v233 offset:54272\n"
"ds_read_b64_tr_b16 v[224:225], v237 offset:50176\n"
"v_mfma_f32_16x16x32_bf16 v[176:179], v[216:219], a[40:43], v[176:179]\n"
"ds_read_b64_tr_b16 a[68:69], v249 offset:50176\n"
"v_mul_f32_dpp v69, v206, v69 row_newbcast:1 row_mask:0xf bank_mask:0xf\n"
"v_mfma_f32_16x16x32_bf16 v[176:179], v[220:223], a[44:47], v[176:179]\n"
"ds_write_b128 v248, v[72:75] offset:51200\n"
"v_mul_f32_dpp v5, v246, v5 row_newbcast:1 row_mask:0xf bank_mask:0xf\n"
"v_exp_f32 v170, v170\n"
"v_exp_f32 v171, v171\n"
"v_mfma_f32_16x16x32_bf16 a[224:227], v[120:123], a[244:247], a[224:227]\n"
"ds_write_b128 v248, v[24:27] offset:49152\n"
"v_cvt_pk_bf16_f32 v133, v158, v159\n"
"v_exp_f32 v167, v167\n"
"v_exp_f32 v168, v168\n"
"v_mfma_f32_16x16x32_bf16 v[16:19], v[96:99], a[244:247], v[16:19]\n"
"s_waitcnt vmcnt(8) lgkmcnt(3)\n"
"v_exp_f32 v156, v156\n"
"v_mfma_f32_16x16x32_bf16 v[16:19], v[100:103], a[248:251], v[16:19]\n"
"s_barrier\n"
"ds_read_b64_tr_b16 a[86:87], v249 offset:51208\n"
"ds_read_b64_tr_b16 a[60:61], v249 offset:49152\n"
"ds_read_b64 v[250:251], v245 offset:52224\n"
"v_exp_f32 v164, v164\n"
"v_mul_f32_dpp v35, v247, v35 row_newbcast:3 row_mask:0xf bank_mask:0xf\n"
"v_cvt_pk_bf16_f32 v33, v34, v35\n"
"v_mfma_f32_16x16x32_bf16 v[48:51], v[104:107], a[244:247], v[48:51]\n"
"ds_read_b128 v[216:219], v228 offset:17408\n"
"v_cvt_pk_bf16_f32 v64, v64, v65\n"
"v_mfma_f32_16x16x32_bf16 v[48:51], v[108:111], a[248:251], v[48:51]\n"
"ds_read_b128 v[208:211], v228 offset:16384\n"
"v_exp_f32 v163, v163\n"
"v_exp_f32 v165, v165\n"
"v_exp_f32 v166, v166\n"
"v_cvt_pk_bf16_f32 v35, v38, v39\n"
"v_mul_f32_dpp v68, v206, v68 row_newbcast:0 row_mask:0xf bank_mask:0xf\n"
"v_mfma_f32_16x16x32_bf16 v[80:83], v[112:115], a[244:247], v[80:83]\n"
"ds_read_b128 v[212:215], v229 offset:16384\n"
"v_cvt_pk_bf16_f32 v65, v66, v67\n"
"v_mfma_f32_16x16x32_bf16 v[80:83], v[116:119], a[248:251], v[80:83]\n"
"ds_read_b64_tr_b16 a[62:63], v249 offset:49160\n"
"ds_read_b128 v[220:223], v229 offset:17408\n"
"v_cvt_pk_bf16_f32 v91, v94, v95\n"
"v_mul_f32_dpp v7, v246, v7 row_newbcast:3 row_mask:0xf bank_mask:0xf\n"
"v_cvt_pk_bf16_f32 v2, v4, v5\n"
"v_cvt_pk_bf16_f32 v3, v6, v7\n"
"v_exp_f32 v173, v173\n"
"v_exp_f32 v176, v176\n"
"v_mfma_f32_16x16x32_bf16 v[84:87], v[116:119], v[224:227], v[84:87]\n"
"ds_read_b128 v[60:63], v244 offset:35840\n"
"v_mul_f32_dpp v71, v206, v71 row_newbcast:3 row_mask:0xf bank_mask:0xf\n"
"v_mfma_f32_16x16x32_bf16 v[84:87], v[112:115], a[252:255], v[84:87]\n"
"ds_write_b128 v248, v[56:59] offset:50176\n"
"v_cvt_pk_bf16_f32 v149, v166, v167\n"
"v_cvt_pk_bf16_f32 v66, v68, v69\n"
"v_exp_f32 v178, v178\n"
"v_mfma_f32_16x16x32_bf16 v[52:55], v[108:111], v[224:227], v[52:55]\n"
"ds_read_b64_tr_b16 a[84:85], v249 offset:51200\n"
"v_cvt_pk_bf16_f32 v67, v70, v71\n"
"v_mfma_f32_16x16x32_bf16 v[52:55], v[104:107], a[252:255], v[52:55]\n"
"s_waitcnt lgkmcnt(8)\n"
"ds_write_b128 v248, v[88:91] offset:51200\n"
"v_min_f32 v254, v252, v250\n"
"v_sub_f32 v252, v254, v252\n"
"v_sub_f32 v250, v254, v250\n"
"v_exp_f32 v252, v252\n"
"v_mfma_f32_16x16x32_bf16 v[20:23], v[100:103], v[224:227], v[20:23]\n"
"ds_read_b64_tr_b16 a[76:77], v249 offset:50176\n"
"v_mul_f32 v253, v253, v252\n"
"v_mfma_f32_16x16x32_bf16 v[20:23], v[96:99], a[252:255], v[20:23]\n"
"ds_read_b64_tr_b16 a[78:79], v249 offset:50184\n"
"ds_read_b64_tr_b16 a[92:93], v249 offset:51200\n"
"ds_read_b64_tr_b16 a[244:245], v234 offset:0\n"
"v_cvt_pk_bf16_f32 v148, v164, v165\n"
"v_mul_f32_dpp v37, v247, v37 row_newbcast:1 row_mask:0xf bank_mask:0xf\n"
"v_cvt_pk_bf16_f32 v135, v170, v171\n"
"v_exp_f32 v172, v172\n"
"v_mul_f32_dpp v17, v246, v17 row_newbcast:1 row_mask:0xf bank_mask:0xf\n"
"v_mfma_f32_16x16x32_bf16 a[228:231], v[120:123], a[252:255], a[228:231]\n"
"s_waitcnt lgkmcnt(10)\n"
"ds_write_b128 v248, v[0:3] offset:49152\n"
"v_cvt_pk_bf16_f32 v127, v154, v155\n"
"v_cvt_pk_bf16_f32 v34, v36, v37\n"
"v_exp_f32 v250, v250\n"
"v_mul_f32_dpp v18, v246, v18 row_newbcast:2 row_mask:0xf bank_mask:0xf\n"
"v_mfma_f32_16x16x32_bf16 v[152:155], v[208:211], a[0:3], v[180:183]\n"
"ds_write_b128 v248, v[32:35] offset:50176\n"
"v_cvt_pk_bf16_f32 v132, v156, v157\n"
"v_cvt_pk_bf16_f32 v141, v162, v163\n"
"v_fmac_f32 v253, v251, v250\n"
"v_rcp_f32 v253, v253\n"
"v_mfma_f32_16x16x32_bf16 v[152:155], v[212:215], a[4:7], v[152:155]\n"
"ds_read_b64_tr_b16 a[94:95], v249 offset:51208\n"
"ds_read_b64_tr_b16 a[48:49], v249 offset:49152\n"
"v_mfma_f32_16x16x32_bf16 v[152:155], v[216:219], a[8:11], v[152:155]\n"
"s_waitcnt lgkmcnt(10)\n"
"ds_write_b128 v248, v[64:67] offset:51200\n"
"v_exp_f32 v174, v174\n"
"v_mul_f32 v252, v252, v253\n"
"v_mul_f32_dpp v16, v246, v16 row_newbcast:0 row_mask:0xf bank_mask:0xf\n"
"v_mul_f32_dpp v49, v247, v49 row_newbcast:1 row_mask:0xf bank_mask:0xf\n"
"v_mfma_f32_16x16x32_bf16 v[152:155], v[220:223], a[12:15], v[152:155]\n"
"ds_read_b64_tr_b16 a[66:67], v249 offset:50184\n"
"ds_read_b64_tr_b16 a[80:81], v249 offset:51200\n"
"v_exp_f32 v175, v175\n"
"v_exp_f32 v177, v177\n"
"v_exp_f32 v179, v179\n"
"v_mul_f32 v250, v250, v253\n"
"v_mul_f32_dpp v83, v206, v83 row_newbcast:3 row_mask:0xf bank_mask:0xf\n"
"v_mfma_f32_16x16x32_bf16 v[156:159], v[216:219], a[24:27], v[184:187]\n"
"ds_read_b128 v[24:27], v244 offset:32768\n"
"ds_read_b128 v[56:59], v244 offset:34816\n"
"v_mfma_f32_16x16x32_bf16 v[156:159], v[220:223], a[28:31], v[156:159]\n"
"ds_read_b64_tr_b16 a[64:65], v249 offset:50176\n"
"v_accvgpr_read_b32 v254, a208\n"
"v_mfma_f32_16x16x32_bf16 v[156:159], v[208:211], a[16:19], v[156:159]\n"
"ds_read_b64_tr_b16 a[50:51], v249 offset:49160\n"
"ds_read_b64_tr_b16 a[82:83], v249 offset:51208\n"
"v_mfma_f32_16x16x32_bf16 v[156:159], v[212:215], a[20:23], v[156:159]\n"
"ds_read_b128 v[28:31], v244 offset:33792\n"
"v_xor_b32 v249, s75, v249\n"
"v_mfma_f32_16x16x32_bf16 v[160:163], v[208:211], a[32:35], v[188:191]\n"
"ds_read_b64_tr_b16 a[246:247], v234 offset:4096\n"
"v_cvt_pk_bf16_f32 v134, v168, v169\n"
"v_mfma_f32_16x16x32_bf16 v[160:163], v[212:215], a[36:39], v[160:163]\n"
"ds_read_b64_tr_b16 a[248:249], v238 offset:0\n"
"v_mov_b32_dpp v250, v250 row_ror:12 row_mask:0x2 bank_mask:0xf\n"
"v_mfma_f32_16x16x32_bf16 v[160:163], v[216:219], a[40:43], v[160:163]\n"
"ds_read_b64_tr_b16 a[250:251], v238 offset:4096\n"
"v_mov_b32_dpp v252, v252 row_ror:12 row_mask:0x2 bank_mask:0xf\n"
"v_mfma_f32_16x16x32_bf16 v[160:163], v[220:223], a[44:47], v[160:163]\n"
"ds_read_b64_tr_b16 v[226:227], v239 offset:4096\n"
"v_cvt_pk_bf16_f32 v142, v172, v173\n"
"v_mov_b32_dpp v250, v250 row_ror:8 row_mask:0x4 bank_mask:0xf\n"
"v_accvgpr_read_b32 v253, a210\n"
"v_exp_f32 v155, v155\n"
"v_exp_f32 v159, v159\n"
"v_mfma_f32_16x16x32_bf16 v[164:167], v[216:219], a[200:203], v[192:195]\n"
"ds_read_b64_tr_b16 a[254:255], v235 offset:4096\n"
"v_mov_b32_dpp v250, v250 row_ror:4 row_mask:0x8 bank_mask:0xf\n"
"v_mfma_f32_16x16x32_bf16 v[164:167], v[220:223], a[204:207], v[164:167]\n"
"s_waitcnt lgkmcnt(8)\n"
"v_mul_f32_dpp v27, v250, v27 row_newbcast:3 row_mask:0xf bank_mask:0xf\n"
"v_mul_f32_dpp v60, v250, v60 row_newbcast:0 row_mask:0xf bank_mask:0xf\n"
"v_mfma_f32_16x16x32_bf16 v[164:167], v[208:211], a[192:195], v[164:167]\n"
"v_cvt_pk_bf16_f32 v143, v174, v175\n"
"v_cvt_pk_bf16_f32 v150, v176, v177\n"
"v_mfma_f32_16x16x32_bf16 v[164:167], v[212:215], a[196:199], v[164:167]\n"
"s_waitcnt lgkmcnt(4)\n"
"ds_read_b128 v[220:223], v229 offset:21504\n"
"v_cvt_pk_bf16_f32 v151, v178, v179\n"
"v_mul_f32_dpp v26, v250, v26 row_newbcast:2 row_mask:0xf bank_mask:0xf\n"
"v_mul_f32_dpp v19, v246, v19 row_newbcast:3 row_mask:0xf bank_mask:0xf\n"
"v_mul_f32_dpp v84, v206, v84 row_newbcast:0 row_mask:0xf bank_mask:0xf\n"
"v_mul_f32_dpp v56, v250, v56 row_newbcast:0 row_mask:0xf bank_mask:0xf\n"
"v_exp_f32 v160, v160\n"
"v_mfma_f32_16x16x32_bf16 a[216:219], v[124:127], a[244:247], 0\n"
"ds_read_b64_tr_b16 a[252:253], v235 offset:0\n"
"ds_read_b64_tr_b16 v[224:225], v239 offset:0\n"
"v_mul_f32_dpp v24, v250, v24 row_newbcast:0 row_mask:0xf bank_mask:0xf\n"
"v_accvgpr_read_b32 v251, a211\n"
"v_mul_f32_dpp v81, v206, v81 row_newbcast:1 row_mask:0xf bank_mask:0xf\n"
"v_mul_f32_dpp v86, v206, v86 row_newbcast:2 row_mask:0xf bank_mask:0xf\n"
"v_exp_f32 v152, v152\n"
"v_exp_f32 v153, v153\n"
"v_mfma_f32_16x16x32_bf16 v[8:11], v[128:131], a[244:247], 0\n"
"s_waitcnt lgkmcnt(5)\n"
"s_add_u32 m0, s44, 98304\n"
"v_exp_f32 v156, v156\n"
"v_mfma_f32_16x16x32_bf16 v[8:11], v[132:135], a[248:251], v[8:11]\n"
"buffer_load_dwordx4 v240, s[20:23], s56 offen nt lds\n"
"v_mov_b32_dpp v252, v252 row_ror:8 row_mask:0x4 bank_mask:0xf\n"
"v_mul_f32_dpp v48, v247, v48 row_newbcast:0 row_mask:0xf bank_mask:0xf\n"
"v_mul_f32_dpp v58, v250, v58 row_newbcast:2 row_mask:0xf bank_mask:0xf\n"
"v_mul_f32_dpp v59, v250, v59 row_newbcast:3 row_mask:0xf bank_mask:0xf\n"
"v_exp_f32 v161, v161\n"
"v_exp_f32 v162, v162\n"
"v_mfma_f32_16x16x32_bf16 v[40:43], v[136:139], a[244:247], 0\n"
"v_mov_b32_dpp v252, v252 row_ror:4 row_mask:0x8 bank_mask:0xf\n"
"v_mul_f32_dpp v25, v250, v25 row_newbcast:1 row_mask:0xf bank_mask:0xf\n"
"v_mfma_f32_16x16x32_bf16 v[40:43], v[140:143], a[248:251], v[40:43]\n"
"v_fmac_f32_dpp v26, v252, v253 row_newbcast:2 row_mask:0xf bank_mask:0xf\n"
"v_mul_f32_dpp v85, v206, v85 row_newbcast:1 row_mask:0xf bank_mask:0xf\n"
"v_mfma_f32_16x16x32_bf16 v[72:75], v[144:147], a[244:247], 0\n"
"v_mul_f32_dpp v30, v250, v30 row_newbcast:2 row_mask:0xf bank_mask:0xf\n"
"v_mul_f32_dpp v57, v250, v57 row_newbcast:1 row_mask:0xf bank_mask:0xf\n"
"v_mfma_f32_16x16x32_bf16 v[72:75], v[148:151], a[248:251], v[72:75]\n"
"s_waitcnt lgkmcnt(0)\n"
"ds_read_b128 v[208:211], v228 offset:20480\n"
"v_fmac_f32_dpp v24, v252, v254 row_newbcast:0 row_mask:0xf bank_mask:0xf\n"
"v_fmac_f32_dpp v27, v252, v251 row_newbcast:3 row_mask:0xf bank_mask:0xf\n"
"v_accvgpr_read_b32 v254, a212\n"
"v_mul_f32_dpp v29, v250, v29 row_newbcast:1 row_mask:0xf bank_mask:0xf\n"
"v_accvgpr_read_b32 v253, a214\n"
"v_mul_f32_dpp v31, v250, v31 row_newbcast:3 row_mask:0xf bank_mask:0xf\n"
"v_xor_b32 v248, s75, v248\n"
"v_cvt_pk_bf16_f32 v16, v16, v17\n"
"v_cvt_pk_bf16_f32 v48, v48, v49\n"
"v_mfma_f32_16x16x32_bf16 v[76:79], v[148:151], v[224:227], 0\n"
"v_fmac_f32_dpp v25, v252, v255 row_newbcast:1 row_mask:0xf bank_mask:0xf\n"
"v_mul_f32_dpp v53, v247, v53 row_newbcast:1 row_mask:0xf bank_mask:0xf\n"
"v_mfma_f32_16x16x32_bf16 v[76:79], v[144:147], a[252:255], v[76:79]\n"
"v_mul_f32_dpp v28, v250, v28 row_newbcast:0 row_mask:0xf bank_mask:0xf\n"
"v_accvgpr_read_b32 v255, a213\n"
"v_fmac_f32_dpp v30, v252, v253 row_newbcast:2 row_mask:0xf bank_mask:0xf\n"
"v_mul_f32_dpp v51, v247, v51 row_newbcast:3 row_mask:0xf bank_mask:0xf\n"
"v_exp_f32 v157, v157\n"
"v_exp_f32 v163, v163\n"
"v_exp_f32 v164, v164\n"
"v_mfma_f32_16x16x32_bf16 v[44:47], v[140:143], v[224:227], 0\n"
"ds_read_b128 v[216:219], v228 offset:21504\n"
"v_mul_f32_dpp v54, v247, v54 row_newbcast:2 row_mask:0xf bank_mask:0xf\n"
"v_mfma_f32_16x16x32_bf16 v[44:47], v[136:139], a[252:255], v[44:47]\n"
"v_mul_f32_dpp v87, v206, v87 row_newbcast:3 row_mask:0xf bank_mask:0xf\n"
"v_accvgpr_read_b32 v253, a226\n"
"v_mfma_f32_16x16x32_bf16 v[12:15], v[132:135], v[224:227], 0\n"
"s_add_u32 s40, s56, s52\n"
"ds_read_b128 v[212:215], v229 offset:20480\n"
"v_fmac_f32_dpp v29, v252, v255 row_newbcast:1 row_mask:0xf bank_mask:0xf\n"
"v_mfma_f32_16x16x32_bf16 v[12:15], v[128:131], a[252:255], v[12:15]\n"
"buffer_load_dwordx4 v240, s[20:23], s40 offen offset:1024 nt lds\n"
"v_fmac_f32_dpp v28, v252, v254 row_newbcast:0 row_mask:0xf bank_mask:0xf\n"
"v_accvgpr_read_b32 v254, a224\n"
"v_accvgpr_read_b32 v255, a225\n"
"v_fmac_f32_dpp v58, v252, v253 row_newbcast:2 row_mask:0xf bank_mask:0xf\n"
"v_exp_f32 v154, v154\n"
"v_exp_f32 v167, v167\n"
"v_mfma_f32_16x16x32_bf16 a[220:223], v[124:127], a[252:255], 0\n"
"v_cvt_pk_bf16_f32 v24, v24, v25\n"
"v_mul_f32_dpp v61, v250, v61 row_newbcast:1 row_mask:0xf bank_mask:0xf\n"
"v_smfmac_f32_16x16x64_bf16 v[196:199], a[240:243], v[128:135], v241\n"
"v_cvt_pk_bf16_f32 v25, v26, v27\n"
"v_mul_f32_dpp v50, v247, v50 row_newbcast:2 row_mask:0xf bank_mask:0xf\n"
"v_mfma_f32_16x16x32_bf16 v[202:205], a[240:243], v[124:127], v[202:205]\n"
"s_waitcnt lgkmcnt(1)\n"
"v_accvgpr_read_b32 v253, a230\n"
"v_cvt_pk_bf16_f32 v97, v154, v155\n"
"v_mfma_f32_16x16x32_bf16 v[168:171], v[216:219], a[8:11], v[180:183]\n"
"ds_read_b64_tr_b16 a[248:249], v238 offset:1024\n"
"v_cvt_pk_bf16_f32 v26, v28, v29\n"
"v_mfma_f32_16x16x32_bf16 v[168:171], v[220:223], a[12:15], v[168:171]\n"
"v_accvgpr_read_b32 v251, a215\n"
"v_fmac_f32_dpp v57, v252, v255 row_newbcast:1 row_mask:0xf bank_mask:0xf\n"
"v_mfma_f32_16x16x32_bf16 v[168:171], v[208:211], a[0:3], v[168:171]\n"
"s_waitcnt lgkmcnt(1)\n"
"v_fmac_f32_dpp v31, v252, v251 row_newbcast:3 row_mask:0xf bank_mask:0xf\n"
"v_accvgpr_read_b32 v251, a227\n"
"v_mfma_f32_16x16x32_bf16 v[168:171], v[212:215], a[4:7], v[168:171]\n"
"ds_read_b64_tr_b16 a[250:251], v238 offset:5120\n"
"v_accvgpr_read_b32 v255, a229\n"
"v_mfma_f32_16x16x32_bf16 v[172:175], v[208:211], a[16:19], v[184:187]\n"
"ds_read_b64_tr_b16 a[244:245], v234 offset:1024\n"
"v_mul_f32_dpp v63, v250, v63 row_newbcast:3 row_mask:0xf bank_mask:0xf\n"
"v_mfma_f32_16x16x32_bf16 v[172:175], v[212:215], a[20:23], v[172:175]\n"
"v_mul_f32_dpp v55, v247, v55 row_newbcast:3 row_mask:0xf bank_mask:0xf\n"
"v_fmac_f32_dpp v59, v252, v251 row_newbcast:3 row_mask:0xf bank_mask:0xf\n"
"v_mfma_f32_16x16x32_bf16 v[172:175], v[216:219], a[24:27], v[172:175]\n"
"s_add_u32 s40, s56, s53\n"
"ds_read_b64_tr_b16 a[246:247], v234 offset:5120\n"
"ds_read_b64_tr_b16 v[224:225], v239 offset:1024\n"
"v_mfma_f32_16x16x32_bf16 v[172:175], v[220:223], a[28:31], v[172:175]\n"
"buffer_load_dwordx4 v240, s[20:23], s40 offen offset:2048 nt lds\n"
"v_cvt_pk_bf16_f32 v27, v30, v31\n"
"v_fmac_f32_dpp v56, v252, v254 row_newbcast:0 row_mask:0xf bank_mask:0xf\n"
"v_accvgpr_read_b32 v251, a231\n"
"v_fmac_f32_dpp v61, v252, v255 row_newbcast:1 row_mask:0xf bank_mask:0xf\n"
"v_cvt_pk_bf16_f32 v56, v56, v57\n"
"v_cvt_pk_bf16_f32 v113, v162, v163\n"
"v_exp_f32 v168, v168\n"
"v_mfma_f32_16x16x32_bf16 v[176:179], v[216:219], a[40:43], v[188:191]\n"
"v_mul_f32_dpp v62, v250, v62 row_newbcast:2 row_mask:0xf bank_mask:0xf\n"
"v_cvt_pk_bf16_f32 v57, v58, v59\n"
"v_mfma_f32_16x16x32_bf16 v[176:179], v[220:223], a[44:47], v[176:179]\n"
"v_fmac_f32_dpp v63, v252, v251 row_newbcast:3 row_mask:0xf bank_mask:0xf\n"
"v_cvt_pk_bf16_f32 v96, v152, v153\n"
"v_mfma_f32_16x16x32_bf16 v[176:179], v[208:211], a[32:35], v[176:179]\n"
"ds_read_b64_tr_b16 a[254:255], v235 offset:5120\n"
"v_accvgpr_read_b32 v254, a228\n"
"v_mfma_f32_16x16x32_bf16 v[176:179], v[212:215], a[36:39], v[176:179]\n"
"ds_write_b128 v243, v[24:27] offset:0\n"
"v_fmac_f32_dpp v60, v252, v254 row_newbcast:0 row_mask:0xf bank_mask:0xf\n"
"v_fmac_f32_dpp v62, v252, v253 row_newbcast:2 row_mask:0xf bank_mask:0xf\n"
"v_cvt_pk_bf16_f32 v58, v60, v61\n"
"v_cvt_pk_bf16_f32 v59, v62, v63\n"
"v_cvt_pk_bf16_f32 v112, v160, v161\n"
"v_mfma_f32_16x16x32_bf16 v[152:155], v[208:211], a[192:195], v[192:195]\n"
"ds_write_b128 v243, v[56:59] offset:2048\n"
"v_mul_f32_dpp v23, v246, v23 row_newbcast:3 row_mask:0xf bank_mask:0xf\n"
"v_exp_f32 v158, v158\n"
"v_exp_f32 v165, v165\n"
"v_mfma_f32_16x16x32_bf16 v[152:155], v[212:215], a[196:199], v[152:155]\n"
"v_exp_f32 v166, v166\n"
"v_mfma_f32_16x16x32_bf16 v[152:155], v[216:219], a[200:203], v[152:155]\n"
"ds_read_b64_tr_b16 v[226:227], v239 offset:5120\n"
"v_cvt_pk_bf16_f32 v104, v156, v157\n"
"v_mfma_f32_16x16x32_bf16 v[152:155], v[220:223], a[204:207], v[152:155]\n"
"s_waitcnt lgkmcnt(4)\n"
"ds_read_b64_tr_b16 a[252:253], v235 offset:1024\n"
"v_mul_f32_dpp v80, v206, v80 row_newbcast:0 row_mask:0xf bank_mask:0xf\n"
"v_mfma_f32_16x16x32_bf16 a[232:235], v[124:127], a[244:247], 0\n"
"ds_read_b128 v[220:223], v231 offset:17408\n"
"v_cvt_pk_bf16_f32 v80, v80, v81\n"
"v_mfma_f32_16x16x32_bf16 v[24:27], v[128:131], a[244:247], 0\n"
"s_add_u32 s40, s56, s54\n"
"v_exp_f32 v174, v174\n"
"v_mfma_f32_16x16x32_bf16 v[24:27], v[132:135], a[248:251], v[24:27]\n"
"buffer_load_dwordx4 v240, s[20:23], s40 offen offset:3072 nt lds\n"
"v_mfma_f32_16x16x32_bf16 v[56:59], v[136:139], a[244:247], 0\n"
"s_add_u32 s55, s55, s59\n"
"v_exp_f32 v172, v172\n"
"v_mfma_f32_16x16x32_bf16 v[56:59], v[140:143], a[248:251], v[56:59]\n"
"s_add_u32 s41, s55, s57\n"
"v_mul_f32_dpp v21, v246, v21 row_newbcast:1 row_mask:0xf bank_mask:0xf\n"
"v_mul_f32_dpp v52, v247, v52 row_newbcast:0 row_mask:0xf bank_mask:0xf\n"
"v_cvt_pk_bf16_f32 v49, v50, v51\n"
"v_exp_f32 v176, v176\n"
"v_mfma_f32_16x16x32_bf16 v[88:91], v[144:147], a[244:247], 0\n"
"s_and_b32 s41, s41, s58\n"
"v_exp_f32 v169, v169\n"
"v_mfma_f32_16x16x32_bf16 v[88:91], v[148:151], a[248:251], v[88:91]\n"
"s_waitcnt lgkmcnt(2)\n"
"s_andn2_b32 s56, s55, s58\n"
"ds_read_b128 v[208:211], v230 offset:16384\n"
"v_mul_f32_dpp v20, v246, v20 row_newbcast:0 row_mask:0xf bank_mask:0xf\n"
"v_mfma_f32_16x16x32_bf16 v[92:95], v[148:151], v[224:227], 0\n"
"s_waitcnt lgkmcnt(2)\n"
"s_or_b32 s56, s56, s41\n"
"ds_read_b128 v[212:215], v231 offset:16384\n"
"v_cvt_pk_bf16_f32 v98, v168, v169\n"
"v_mfma_f32_16x16x32_bf16 v[92:95], v[144:147], a[252:255], v[92:95]\n"
"ds_read_b128 v[216:219], v230 offset:17408\n"
"v_cvt_pk_bf16_f32 v105, v158, v159\n"
"v_mfma_f32_16x16x32_bf16 v[60:63], v[140:143], v[224:227], 0\n"
"s_add_u32 m0, s44, 49152\n"
"v_exp_f32 v173, v173\n"
"v_mfma_f32_16x16x32_bf16 v[60:63], v[136:139], a[252:255], v[60:63]\n"
"buffer_load_dwordx4 v240, s[16:19], s56 offen nt lds\n"
"v_mfma_f32_16x16x32_bf16 v[28:31], v[132:135], v[224:227], 0\n"
"v_exp_f32 v152, v152\n"
"v_mfma_f32_16x16x32_bf16 v[28:31], v[128:131], a[252:255], v[28:31]\n"
"v_mul_f32_dpp v22, v246, v22 row_newbcast:2 row_mask:0xf bank_mask:0xf\n"
"v_cvt_pk_bf16_f32 v50, v52, v53\n"
"v_mfma_f32_16x16x32_bf16 a[236:239], v[124:127], a[252:255], 0\n"
"ds_read_b64_tr_b16 a[246:247], v232 offset:4096\n"
"v_cvt_pk_bf16_f32 v17, v18, v19\n"
"v_smfmac_f32_16x16x64_bf16 v[198:201], a[240:243], v[136:143], v241\n"
"s_waitcnt lgkmcnt(3)\n"
"ds_read_b64_tr_b16 a[254:255], v233 offset:4096\n"
"v_cvt_pk_bf16_f32 v106, v172, v173\n"
"v_mfma_f32_16x16x32_bf16 v[156:159], v[208:211], a[0:3], v[180:183]\n"
"s_waitcnt lgkmcnt(3)\n"
"ds_read_b64_tr_b16 v[224:225], v237 offset:0\n"
"v_cvt_pk_bf16_f32 v51, v54, v55\n"
"v_mfma_f32_16x16x32_bf16 v[156:159], v[212:215], a[4:7], v[156:159]\n"
"s_waitcnt lgkmcnt(3)\n"
"ds_read_b64_tr_b16 a[244:245], v232 offset:0\n"
"v_cvt_pk_bf16_f32 v120, v164, v165\n"
"v_mfma_f32_16x16x32_bf16 v[156:159], v[216:219], a[8:11], v[156:159]\n"
"v_exp_f32 v177, v177\n"
"v_mfma_f32_16x16x32_bf16 v[156:159], v[220:223], a[12:15], v[156:159]\n"
"ds_read_b64_tr_b16 a[248:249], v236 offset:0\n"
"v_mul_f32_dpp v82, v206, v82 row_newbcast:2 row_mask:0xf bank_mask:0xf\n"
"v_mfma_f32_16x16x32_bf16 v[160:163], v[216:219], a[24:27], v[184:187]\n"
"ds_read_b64_tr_b16 a[250:251], v236 offset:4096\n"
"v_add_u32 v243, 0x1000, v243\n"
"v_mfma_f32_16x16x32_bf16 v[160:163], v[220:223], a[28:31], v[160:163]\n"
"ds_read_b64_tr_b16 v[226:227], v237 offset:4096\n"
"v_cvt_pk_bf16_f32 v121, v166, v167\n"
"v_mfma_f32_16x16x32_bf16 v[160:163], v[208:211], a[16:19], v[160:163]\n"
"s_add_u32 s40, s56, s52\n"
"ds_read_b64_tr_b16 a[252:253], v233 offset:0\n"
"v_cvt_pk_bf16_f32 v81, v82, v83\n"
"v_mfma_f32_16x16x32_bf16 v[160:163], v[212:215], a[20:23], v[160:163]\n"
"buffer_load_dwordx4 v240, s[16:19], s40 offen offset:1024 nt lds\n"
"v_mfma_f32_16x16x32_bf16 v[164:167], v[208:211], a[32:35], v[188:191]\n"
"v_exp_f32 v179, v179\n"
"v_mfma_f32_16x16x32_bf16 v[164:167], v[212:215], a[36:39], v[164:167]\n"
"v_exp_f32 v154, v154\n"
"v_mfma_f32_16x16x32_bf16 v[164:167], v[216:219], a[40:43], v[164:167]\n"
"ds_read_b128 v[212:215], v231 offset:20480\n"
"v_cvt_pk_bf16_f32 v18, v20, v21\n"
"v_mfma_f32_16x16x32_bf16 v[164:167], v[220:223], a[44:47], v[164:167]\n"
"s_waitcnt lgkmcnt(4)\n"
"v_exp_f32 v175, v175\n"
"v_mfma_f32_16x16x32_bf16 a[208:211], v[124:127], a[244:247], 0\n"
"ds_read_b128 v[208:211], v230 offset:20480\n"
"v_cvt_pk_bf16_f32 v83, v86, v87\n"
"v_mfma_f32_16x16x32_bf16 v[0:3], v[128:131], a[244:247], 0\n"
"s_waitcnt lgkmcnt(3)\n"
"v_exp_f32 v156, v156\n"
"v_mfma_f32_16x16x32_bf16 v[0:3], v[132:135], a[248:251], v[0:3]\n"
"ds_write_b128 v248, v[48:51] offset:50176\n"
"v_exp_f32 v178, v178\n"
"v_exp_f32 v157, v157\n"
"v_mfma_f32_16x16x32_bf16 v[32:35], v[136:139], a[244:247], 0\n"
"s_add_u32 s40, s56, s53\n"
"v_exp_f32 v171, v171\n"
"v_mfma_f32_16x16x32_bf16 v[32:35], v[140:143], a[248:251], v[32:35]\n"
"buffer_load_dwordx4 v240, s[16:19], s40 offen offset:2048 nt lds\n"
"v_mfma_f32_16x16x32_bf16 v[64:67], v[144:147], a[244:247], 0\n"
"ds_read_b128 v[216:219], v230 offset:21504\n"
"v_cvt_pk_bf16_f32 v19, v22, v23\n"
"v_mfma_f32_16x16x32_bf16 v[64:67], v[148:151], a[248:251], v[64:67]\n"
"v_exp_f32 v160, v160\n"
"v_mfma_f32_16x16x32_bf16 v[68:71], v[148:151], v[224:227], 0\n"
"s_waitcnt lgkmcnt(3)\n"
"ds_read_b64_tr_b16 a[72:73], v249 offset:50176\n"
"ds_read_b128 v[220:223], v231 offset:21504\n"
"v_mfma_f32_16x16x32_bf16 v[68:71], v[144:147], a[252:255], v[68:71]\n"
"ds_write_b128 v248, v[16:19] offset:49152\n"
"v_cvt_pk_bf16_f32 v82, v84, v85\n"
"v_exp_f32 v158, v158\n"
"v_mfma_f32_16x16x32_bf16 v[36:39], v[140:143], v[224:227], 0\n"
"v_exp_f32 v170, v170\n"
"v_mfma_f32_16x16x32_bf16 v[36:39], v[136:139], a[252:255], v[36:39]\n"
"ds_read_b64_tr_b16 a[244:245], v232 offset:1024\n"
"v_cvt_pk_bf16_f32 v114, v176, v177\n"
"v_mfma_f32_16x16x32_bf16 v[4:7], v[132:135], v[224:227], 0\n"
"ds_read_b64_tr_b16 a[58:59], v249 offset:49160\n"
"ds_read_b64_tr_b16 v[224:225], v237 offset:1024\n"
"v_mfma_f32_16x16x32_bf16 v[4:7], v[128:131], a[252:255], v[4:7]\n"
"ds_read_b64_tr_b16 a[74:75], v249 offset:50184\n"
"v_cvt_pk_bf16_f32 v99, v170, v171\n"
"v_mfma_f32_16x16x32_bf16 a[212:215], v[124:127], a[252:255], 0\n"
"s_add_u32 s40, s56, s54\n"
"ds_write_b128 v248, v[80:83] offset:51200\n"
"v_exp_f32 v163, v163\n"
"v_exp_f32 v167, v167\n"
"v_smfmac_f32_16x16x64_bf16 v[200:203], a[240:243], v[144:151], v241\n"
"s_waitcnt lgkmcnt(8)\n"
"buffer_load_dwordx4 v240, s[16:19], s40 offen offset:3072 nt lds\n"
"v_mfma_f32_16x16x32_bf16 v[168:171], v[208:211], a[0:3], v[180:183]\n"
"v_exp_f32 v159, v159\n"
"v_mfma_f32_16x16x32_bf16 v[168:171], v[212:215], a[4:7], v[168:171]\n"
"ds_read_b64_tr_b16 a[88:89], v249 offset:51200\n"
"ds_read_b64_tr_b16 a[246:247], v232 offset:5120\n"
"v_mfma_f32_16x16x32_bf16 v[168:171], v[216:219], a[8:11], v[168:171]\n"
"s_waitcnt lgkmcnt(7)\n"
"ds_read_b64_tr_b16 a[252:253], v233 offset:1024\n"
"v_cvt_pk_bf16_f32 v107, v174, v175\n"
"v_mfma_f32_16x16x32_bf16 v[168:171], v[220:223], a[12:15], v[168:171]\n"
"ds_read_b64_tr_b16 a[56:57], v249 offset:49152\n"
"ds_read_b64_tr_b16 a[90:91], v249 offset:51208\n"
"v_mfma_f32_16x16x32_bf16 v[172:175], v[216:219], a[24:27], v[184:187]\n"
"v_exp_f32 v162, v162\n"
"v_mfma_f32_16x16x32_bf16 v[172:175], v[220:223], a[28:31], v[172:175]\n"
"ds_read_b64_tr_b16 a[248:249], v236 offset:1024\n"
"v_xor_b32 v248, s75, v248\n"
"v_mfma_f32_16x16x32_bf16 v[172:175], v[208:211], a[16:19], v[172:175]\n"
"ds_read_b64_tr_b16 a[250:251], v236 offset:5120\n"
"v_cvt_pk_bf16_f32 v115, v178, v179\n"
"v_mfma_f32_16x16x32_bf16 v[172:175], v[212:215], a[20:23], v[172:175]\n"
"s_cbranch_vccnz LPCT1_%=\nLPKT1_%=:\n"
"v_cvt_pk_bf16_f32 v109, v162, v163\n"
"v_mfma_f32_16x16x32_bf16 v[176:179], v[208:211], a[32:35], v[188:191]\n"
"v_exp_f32 v161, v161\n"
"v_mfma_f32_16x16x32_bf16 v[176:179], v[212:215], a[36:39], v[176:179]\n"
"v_exp_f32 v166, v166\n"
"v_mfma_f32_16x16x32_bf16 v[176:179], v[216:219], a[40:43], v[176:179]\n"
"ds_read_b64_tr_b16 a[254:255], v233 offset:5120\n"
"v_cvt_pk_bf16_f32 v100, v156, v157\n"
"v_mfma_f32_16x16x32_bf16 v[176:179], v[220:223], a[44:47], v[176:179]\n"
"s_waitcnt lgkmcnt(3)\n"
"ds_read_b64_tr_b16 v[226:227], v237 offset:5120\n"
"v_mfma_f32_16x16x32_bf16 a[224:227], v[124:127], a[244:247], 0\n"
"v_exp_f32 v173, v173\n"
"v_mfma_f32_16x16x32_bf16 v[16:19], v[128:131], a[244:247], 0\n"
"s_waitcnt vmcnt(8) lgkmcnt(2)\n"
"v_exp_f32 v169, v169\n"
"v_mfma_f32_16x16x32_bf16 v[16:19], v[132:135], a[248:251], v[16:19]\n"
"s_barrier\n"
"ds_read_b128 v[208:211], v228 offset:32768\n"
"ds_read_b128 v[212:215], v229 offset:32768\n"
"v_exp_f32 v165, v165\n"
"v_mfma_f32_16x16x32_bf16 v[48:51], v[136:139], a[244:247], 0\n"
"ds_read_b128 v[216:219], v228 offset:33792\n"
"v_cvt_pk_bf16_f32 v101, v158, v159\n"
"v_mfma_f32_16x16x32_bf16 v[48:51], v[140:143], a[248:251], v[48:51]\n"
"ds_read_b128 v[220:223], v229 offset:33792\n"
"v_cvt_pk_bf16_f32 v117, v166, v167\n"
"v_mfma_f32_16x16x32_bf16 v[80:83], v[144:147], a[244:247], 0\n"
"ds_read_b64_tr_b16 a[244:245], v234 offset:16384\n"
"v_xor_b32 v249, s75, v249\n"
"v_mfma_f32_16x16x32_bf16 v[80:83], v[148:151], a[248:251], v[80:83]\n"
"s_waitcnt lgkmcnt(5)\n"
"ds_read_b64_tr_b16 a[246:247], v234 offset:20480\n"
"ds_read_b64_tr_b16 a[248:249], v238 offset:16384\n"
"ds_read_b64_tr_b16 a[250:251], v238 offset:20480\n"
"v_mfma_f32_16x16x32_bf16 v[84:87], v[148:151], v[224:227], 0\n"
"v_exp_f32 v175, v175\n"
"v_mfma_f32_16x16x32_bf16 v[84:87], v[144:147], a[252:255], v[84:87]\n"
"v_exp_f32 v153, v153\n"
"v_mfma_f32_16x16x32_bf16 v[52:55], v[140:143], v[224:227], 0\n"
"v_exp_f32 v164, v164\n"
"v_mfma_f32_16x16x32_bf16 v[52:55], v[136:139], a[252:255], v[52:55]\n"
"v_exp_f32 v155, v155\n"
"v_mfma_f32_16x16x32_bf16 v[20:23], v[132:135], v[224:227], 0\n"
"ds_read_b64_tr_b16 v[226:227], v239 offset:20480\n"
"v_cvt_pk_bf16_f32 v123, v154, v155\n"
"v_mfma_f32_16x16x32_bf16 v[20:23], v[128:131], a[252:255], v[20:23]\n"
"v_exp_f32 v178, v178\n"
"v_mfma_f32_16x16x32_bf16 a[228:231], v[124:127], a[252:255], 0\n"
"s_waitcnt lgkmcnt(4)\n"
"v_cvt_pk_bf16_f32 v122, v152, v153\n"
"s_branch LAY2_%=\n"
"LAX_%=:\n"
"v_mfma_f32_16x16x32_bf16 a[224:227], v[124:127], a[244:247], a[224:227]\n"
"v_exp_f32 v168, v168\n"
"v_mfma_f32_16x16x32_bf16 v[16:19], v[128:131], a[244:247], v[16:19]\n"
"s_waitcnt vmcnt(8) lgkmcnt(0)\n"
"v_exp_f32 v172, v172\n"
"v_mfma_f32_16x16x32_bf16 v[16:19], v[132:135], a[248:251], v[16:19]\n"
"s_barrier\n"
"v_mfma_f32_16x16x32_bf16 v[48:51], v[136:139], a[244:247], v[48:51]\n"
"v_exp_f32 v170, v170\n"
"v_mfma_f32_16x16x32_bf16 v[48:51], v[140:143], a[248:251], v[48:51]\n"
"v_exp_f32 v156, v156\n"
"v_mfma_f32_16x16x32_bf16 v[80:83], v[144:147], a[244:247], v[80:83]\n"
"v_exp_f32 v177, v177\n"
"v_mfma_f32_16x16x32_bf16 v[80:83], v[148:151], a[248:251], v[80:83]\n"
"ds_read_b64_tr_b16 a[244:245], v234 offset:49152\n"
"ds_read_b64_tr_b16 a[246:247], v234 offset:53248\n"
"v_cvt_pk_bf16_f32 v123, v154, v155\n"
"v_cvt_pk_bf16_f32 v108, v160, v161\n"
"v_exp_f32 v174, v174\n"
"v_exp_f32 v176, v176\n"
"v_exp_f32 v179, v179\n"
"v_accvgpr_read_b32 v253, a96\n"
"v_mfma_f32_16x16x32_bf16 v[84:87], v[148:151], v[224:227], v[84:87]\n"
"v_lshlrev_b32_e64 v244, 24, s43\n"
"v_and_b32 v245, 0x3f0, v242\n"
"v_mfma_f32_16x16x32_bf16 v[84:87], v[144:147], a[252:255], v[84:87]\n"
"ds_read_b64_tr_b16 a[248:249], v238 offset:49152\n"
"v_cvt_pk_bf16_f32 v101, v158, v159\n"
"v_cvt_pk_bf16_f32 v109, v162, v163\n"
"v_exp_f32 v169, v169\n"
"v_exp_f32 v171, v171\n"
"v_exp_f32 v173, v173\n"
"v_and_b32 v252, 0x7fff0000, v252\n"
"v_and_b32 v253, 0x7fff0000, v253\n"
"v_mfma_f32_16x16x32_bf16 v[52:55], v[140:143], v[224:227], v[52:55]\n"
"ds_read_b64_tr_b16 a[250:251], v238 offset:53248\n"
"v_cvt_pk_bf16_f32 v110, v172, v173\n"
"v_mfma_f32_16x16x32_bf16 v[52:55], v[136:139], a[252:255], v[52:55]\n"
"v_exp_f32 v178, v178\n"
"v_mfma_f32_16x16x32_bf16 v[20:23], v[132:135], v[224:227], v[20:23]\n"
"v_cvt_pk_bf16_f32 v100, v156, v157\n"
"v_cvt_pk_bf16_f32 v103, v170, v171\n"
"v_mfma_f32_16x16x32_bf16 v[20:23], v[128:131], a[252:255], v[20:23]\n"
"v_cvt_pk_bf16_f32 v119, v178, v179\n"
"v_max_u32_dpp v252, v252, v252 quad_perm:[1,0,3,2] row_mask:0xf bank_mask:0xf\n"
"v_mfma_f32_16x16x32_bf16 a[228:231], v[124:127], a[252:255], a[228:231]\n"
"s_waitcnt lgkmcnt(2)\n"
"ds_read_b64_tr_b16 v[226:227], v239 offset:53248\n"
"v_cvt_pk_bf16_f32 v102, v168, v169\n"
"v_mfma_f32_16x16x32_bf16 a[216:219], v[120:123], a[244:247], a[216:219]\n"
"s_mov_b64 s[28:29], s[8:9]\n"
"v_cvt_pk_bf16_f32 v116, v164, v165\n"
"v_cvt_pk_bf16_f32 v117, v166, v167\n"
"v_smfmac_f32_16x16x64_bf16 v[196:199], a[240:243], v[96:103], v241\n"
"s_mov_b32 s30, 655360\n"
"ds_read_b64_tr_b16 v[224:225], v239 offset:49152\n"
"v_cvt_pk_bf16_f32 v118, v176, v177\n"
"v_mfma_f32_16x16x32_bf16 v[8:11], v[96:99], a[244:247], v[8:11]\n"
"s_waitcnt lgkmcnt(2)\n"
"s_mov_b32 s71, s44\n"
"v_exp_f32 v175, v175\n"
"v_mfma_f32_16x16x32_bf16 v[8:11], v[100:103], a[248:251], v[8:11]\n"
"s_mov_b32 s70, s95\n"
"ds_read_b64_tr_b16 a[252:253], v235 offset:49152\n"
"ds_read_b64_tr_b16 a[254:255], v235 offset:53248\n"
"v_smfmac_f32_16x16x64_bf16 v[200:203], a[240:243], v[112:119], v241\n"
"v_cvt_pk_bf16_f32 v111, v174, v175\n"
"v_max_u32_dpp v253, v253, v253 quad_perm:[1,0,3,2] row_mask:0xf bank_mask:0xf\n"
"v_mfma_f32_16x16x32_bf16 v[40:43], v[104:107], a[244:247], v[40:43]\n"
"s_nop 0\n"
"v_max_u32_dpp v253, v253, v253 quad_perm:[2,3,0,1] row_mask:0xf bank_mask:0xf\n"
"v_add_f32 v196, v196, v197\n"
"v_mfma_f32_16x16x32_bf16 v[40:43], v[108:111], a[248:251], v[40:43]\n"
"s_add_u32 s41, s70, 327680\n"
"s_add_u32 m0, s71, 32768\n"
"v_max_u32_dpp v252, v252, v252 quad_perm:[2,3,0,1] row_mask:0xf bank_mask:0xf\n"
"v_smfmac_f32_16x16x64_bf16 v[198:201], a[240:243], v[104:111], v241\n"
"buffer_load_dwordx4 v240, s[28:31], s41 offen lds\n"
"v_mfma_f32_16x16x32_bf16 v[72:75], v[112:115], a[244:247], v[72:75]\n"
"v_max_u32_dpp v253, v253, v253 row_half_mirror row_mask:0xf bank_mask:0xf\n"
"v_mul_f32 v244, 0x45800000, v244\n"
"v_mfma_f32_16x16x32_bf16 v[72:75], v[116:119], a[248:251], v[72:75]\n"
"ds_read_b64_tr_b16 a[246:247], v234 offset:54272\n"
"v_max_u32_dpp v252, v252, v252 row_half_mirror row_mask:0xf bank_mask:0xf\n"
"v_mfma_f32_16x16x32_bf16 v[202:205], a[240:243], v[120:123], v[202:205]\n"
"s_waitcnt lgkmcnt(3)\n"
"ds_read_b64_tr_b16 a[250:251], v238 offset:54272\n"
"v_add_f32 v200, v200, v201\n"
"v_mfma_f32_16x16x32_bf16 v[76:79], v[116:119], v[224:227], v[76:79]\n"
"s_waitcnt lgkmcnt(2)\n"
"s_add_u32 s40, s41, s52\n"
"ds_read_b64_tr_b16 a[244:245], v234 offset:50176\n"
"ds_read_b64_tr_b16 a[248:249], v238 offset:50176\n"
"v_mfma_f32_16x16x32_bf16 v[76:79], v[112:115], a[252:255], v[76:79]\n"
"buffer_load_dwordx4 v240, s[28:31], s40 offen offset:1024 lds\n"
"v_mfma_f32_16x16x32_bf16 v[44:47], v[108:111], v[224:227], v[44:47]\n"
"v_add_f32 v198, v198, v199\n"
"v_add_f32 v244, v244, v202\n"
"v_mfma_f32_16x16x32_bf16 v[44:47], v[104:107], a[252:255], v[44:47]\n"
"v_min_u32_dpp v253, v253, v253 row_mirror row_mask:0xf bank_mask:0xf\n"
"v_min3_f32 v244, v244, v196, v198\n"
"v_mfma_f32_16x16x32_bf16 v[12:15], v[100:103], v[224:227], v[12:15]\n"
"s_add_u32 s40, s41, s53\n"
"ds_read_b64_tr_b16 v[224:225], v239 offset:50176\n"
"v_add_f32 v254, v196, v198\n"
"v_mfma_f32_16x16x32_bf16 v[12:15], v[96:99], a[252:255], v[12:15]\n"
"buffer_load_dwordx4 v240, s[28:31], s40 offen offset:2048 lds\n"
"v_min_u32_dpp v252, v252, v252 row_mirror row_mask:0xf bank_mask:0xf\n"
"v_add_f32 v254, v254, v200\n"
"v_min_f32 v244, v244, v200\n"
"v_rcp_f32 v244, v244\n"
"v_lshrrev_b32 v245, 1, v245\n"
"v_mfma_f32_16x16x32_bf16 a[220:223], v[120:123], a[252:255], a[220:223]\n"
"s_waitcnt lgkmcnt(2)\n"
"v_add_f32 v254, v254, v202\n"
"v_fmac_f32 v254, 0x68800000, v244\n"
"v_cmp_nge_f32 vcc, s45, v254\n"
"v_sub_u32 v245, v242, v245\n"
"s_cbranch_vccnz LSLOW_%=\nLBACK3_%=:\ns_mov_b64 vcc, s[64:65]\n"
"v_mfma_f32_16x16x32_bf16 a[232:235], v[120:123], a[244:247], a[232:235]\n"
"ds_read_b64_tr_b16 a[252:253], v235 offset:50176\n"
"ds_read_b64_tr_b16 a[254:255], v235 offset:54272\n"
"ds_read_b64_tr_b16 v[226:227], v239 offset:54272\n"
"v_rcp_f32 v246, v196\n"
"v_rcp_f32 v247, v198\n"
"v_xor_b32 v244, 0x11000, v242\n"
"v_bfe_u32 v254, v242, 9, 1\n"
"v_mfma_f32_16x16x32_bf16 v[24:27], v[96:99], a[244:247], v[24:27]\n"
"s_waitcnt lgkmcnt(4)\n"
"v_min_u32_dpp v252, v252, v252 row_bcast:15 row_mask:0xa bank_mask:0xf\n"
"v_mov_b32_dpp v246, v246 row_ror:12 row_mask:0x2 bank_mask:0xf\n"
"v_mfma_f32_16x16x32_bf16 v[24:27], v[100:103], a[248:251], v[24:27]\n"
"ds_write_b128 v244, a[220:223] offset:33792\n"
"v_min_u32_dpp v253, v253, v253 row_bcast:15 row_mask:0xa bank_mask:0xf\n"
"v_min_u32_dpp v252, v252, v252 row_bcast:31 row_mask:0xc bank_mask:0xf\n"
"v_mul_f32 v252, 0x43340000, v252\n"
"v_mov_b32_dpp v247, v247 row_ror:12 row_mask:0x2 bank_mask:0xf\n"
"v_mov_b32_dpp v246, v246 row_ror:8 row_mask:0x4 bank_mask:0xf\n"
"v_mfma_f32_16x16x32_bf16 v[56:59], v[104:107], a[244:247], v[56:59]\n"
"s_add_u32 s40, s41, s54\n"
"v_rcp_f32 v206, v200\n"
"v_mfma_f32_16x16x32_bf16 v[56:59], v[108:111], a[248:251], v[56:59]\n"
"buffer_load_dwordx4 v240, s[28:31], s40 offen offset:3072 lds\n"
"v_mfma_f32_16x16x32_bf16 v[88:91], v[112:115], a[244:247], v[88:91]\n"
"s_add_u32 s71, s44, s75\n"
"v_mov_b32_dpp v206, v206 row_ror:12 row_mask:0x2 bank_mask:0xf\n"
"v_mov_b32_dpp v246, v246 row_ror:4 row_mask:0x8 bank_mask:0xf\n"
"v_mfma_f32_16x16x32_bf16 v[88:91], v[116:119], a[248:251], v[88:91]\n"
"s_waitcnt lgkmcnt(1)\n"
"s_mov_b32 s70, s100\n"
"ds_read_b64_tr_b16 a[244:245], v232 offset:49152\n"
"ds_read_b64_tr_b16 a[246:247], v232 offset:53248\n"
"ds_read_b64_tr_b16 a[248:249], v236 offset:49152\n"
"v_mov_b32_dpp v247, v247 row_ror:8 row_mask:0x4 bank_mask:0xf\n"
"v_xor_b32 v250, 0x1000, v245\n"
"v_mul_f32_dpp v10, v246, v10 row_newbcast:2 row_mask:0xf bank_mask:0xf\n"
"v_mul_f32_dpp v12, v246, v12 row_newbcast:0 row_mask:0xf bank_mask:0xf\n"
"v_mul_f32_dpp v15, v246, v15 row_newbcast:3 row_mask:0xf bank_mask:0xf\n"
"v_mfma_f32_16x16x32_bf16 v[92:95], v[116:119], v[224:227], v[92:95]\n"
"s_add_u32 s41, s70, 163840\n"
"s_add_u32 m0, s71, 16384\n"
"ds_read_b64_tr_b16 a[250:251], v236 offset:53248\n"
"v_mfma_f32_16x16x32_bf16 v[92:95], v[112:115], a[252:255], v[92:95]\n"
"buffer_load_dwordx4 v240, s[28:31], s41 offen lds\n"
"v_mov_b32_dpp v206, v206 row_ror:8 row_mask:0x4 bank_mask:0xf\n"
"v_mov_b32_dpp v247, v247 row_ror:4 row_mask:0x8 bank_mask:0xf\n"
"v_mul_f32_dpp v11, v246, v11 row_newbcast:3 row_mask:0xf bank_mask:0xf\n"
"v_mul_f32_dpp v24, v246, v24 row_newbcast:0 row_mask:0xf bank_mask:0xf\n"
"v_mul_f32_dpp v25, v246, v25 row_newbcast:1 row_mask:0xf bank_mask:0xf\n"
"v_cvt_pk_bf16_f32 v24, v24, v25\n"
"v_mfma_f32_16x16x32_bf16 v[60:63], v[108:111], v[224:227], v[60:63]\n"
"v_min_u32_dpp v253, v253, v253 row_bcast:31 row_mask:0xc bank_mask:0xf\n"
"v_mul_f32_dpp v44, v247, v44 row_newbcast:0 row_mask:0xf bank_mask:0xf\n"
"v_mfma_f32_16x16x32_bf16 v[60:63], v[104:107], a[252:255], v[60:63]\n"
"v_mov_b32_dpp v206, v206 row_ror:4 row_mask:0x8 bank_mask:0xf\n"
"v_mul_f32_dpp v41, v247, v41 row_newbcast:1 row_mask:0xf bank_mask:0xf\n"
"v_mfma_f32_16x16x32_bf16 v[28:31], v[100:103], v[224:227], v[28:31]\n"
"s_add_u32 s40, s41, s52\n"
"ds_read_b64_tr_b16 v[224:225], v237 offset:49152\n"
"ds_read_b64_tr_b16 v[226:227], v237 offset:53248\n"
"v_mfma_f32_16x16x32_bf16 v[28:31], v[96:99], a[252:255], v[28:31]\n"
"buffer_load_dwordx4 v240, s[28:31], s40 offen offset:1024 lds\n"
"v_mul_f32_dpp v8, v246, v8 row_newbcast:0 row_mask:0xf bank_mask:0xf\n"
"v_mul_f32_dpp v13, v246, v13 row_newbcast:1 row_mask:0xf bank_mask:0xf\n"
"v_mul_f32_dpp v40, v247, v40 row_newbcast:0 row_mask:0xf bank_mask:0xf\n"
"v_mul_f32_dpp v26, v246, v26 row_newbcast:2 row_mask:0xf bank_mask:0xf\n"
"v_mul_f32_dpp v27, v246, v27 row_newbcast:3 row_mask:0xf bank_mask:0xf\n"
"v_mul_f32_dpp v94, v206, v94 row_newbcast:2 row_mask:0xf bank_mask:0xf\n"
"v_mfma_f32_16x16x32_bf16 a[236:239], v[120:123], a[252:255], a[236:239]\n"
"s_waitcnt lgkmcnt(4)\n"
"v_cvt_pk_bf16_f32 v40, v40, v41\n"
"v_mul_f32_dpp v91, v206, v91 row_newbcast:3 row_mask:0xf bank_mask:0xf\n"
"v_mfma_f32_16x16x32_bf16 a[208:211], v[120:123], a[244:247], a[208:211]\n"
"v_mul_f32_dpp v9, v246, v9 row_newbcast:1 row_mask:0xf bank_mask:0xf\n"
"v_mul_f32_dpp v43, v247, v43 row_newbcast:3 row_mask:0xf bank_mask:0xf\n"
"v_mfma_f32_16x16x32_bf16 v[0:3], v[96:99], a[244:247], v[0:3]\n"
"s_waitcnt lgkmcnt(2)\n"
"ds_write_b128 v244, a[216:219] offset:32768\n"
"v_mul_f32_dpp v42, v247, v42 row_newbcast:2 row_mask:0xf bank_mask:0xf\n"
"v_mul_f32_dpp v72, v206, v72 row_newbcast:0 row_mask:0xf bank_mask:0xf\n"
"v_mul_f32_dpp v73, v206, v73 row_newbcast:1 row_mask:0xf bank_mask:0xf\n"
"v_mul_f32_dpp v31, v246, v31 row_newbcast:3 row_mask:0xf bank_mask:0xf\n"
"v_cvt_pk_bf16_f32 v25, v26, v27\n"
"v_mfma_f32_16x16x32_bf16 v[0:3], v[100:103], a[248:251], v[0:3]\n"
"ds_read_b64_tr_b16 a[252:253], v233 offset:49152\n"
"ds_read_b64_tr_b16 a[254:255], v233 offset:53248\n"
"v_mfma_f32_16x16x32_bf16 v[32:35], v[104:107], a[244:247], v[32:35]\n"
"s_add_u32 s40, s41, s53\n"
"v_lshl_add_u32 v254, v254, 4, s43\n"
"v_mul_f32_dpp v77, v206, v77 row_newbcast:1 row_mask:0xf bank_mask:0xf\n"
"v_mfma_f32_16x16x32_bf16 v[32:35], v[108:111], a[248:251], v[32:35]\n"
"buffer_load_dwordx4 v240, s[28:31], s40 offen offset:2048 lds\n"
"v_mfma_f32_16x16x32_bf16 v[64:67], v[112:115], a[244:247], v[64:67]\n"
"v_mul_f32_dpp v76, v206, v76 row_newbcast:0 row_mask:0xf bank_mask:0xf\n"
"v_mul_f32_dpp v88, v206, v88 row_newbcast:0 row_mask:0xf bank_mask:0xf\n"
"v_mfma_f32_16x16x32_bf16 v[64:67], v[116:119], a[248:251], v[64:67]\n"
"s_waitcnt lgkmcnt(3)\n"
"ds_read_b64_tr_b16 a[244:245], v232 offset:50176\n"
"ds_read_b64_tr_b16 a[246:247], v232 offset:54272\n"
"ds_read_b64_tr_b16 a[248:249], v236 offset:50176\n"
"v_cvt_pk_bf16_f32 v8, v8, v9\n"
"v_mul_f32_dpp v46, v247, v46 row_newbcast:2 row_mask:0xf bank_mask:0xf\n"
"v_mul_f32_dpp v78, v206, v78 row_newbcast:2 row_mask:0xf bank_mask:0xf\n"
"v_mul_f32_dpp v57, v247, v57 row_newbcast:1 row_mask:0xf bank_mask:0xf\n"
"v_mul_f32_dpp v60, v247, v60 row_newbcast:0 row_mask:0xf bank_mask:0xf\n"
"v_mul_f32_dpp v89, v206, v89 row_newbcast:1 row_mask:0xf bank_mask:0xf\n"
"v_mfma_f32_16x16x32_bf16 v[68:71], v[116:119], v[224:227], v[68:71]\n"
"s_waitcnt lgkmcnt(3)\n"
"s_add_u32 s40, s41, s54\n"
"ds_write_b128 v244, a[236:239] offset:35840\n"
"v_mul_f32 v252, v252, v253\n"
"v_lshl_add_u32 v254, v254, 6, s75\n"
"v_mul_f32_dpp v56, v247, v56 row_newbcast:0 row_mask:0xf bank_mask:0xf\n"
"v_accvgpr_read_b32 v255, a209\n"
"v_mfma_f32_16x16x32_bf16 v[68:71], v[112:115], a[252:255], v[68:71]\n"
"buffer_load_dwordx4 v240, s[28:31], s40 offen offset:3072 lds\n"
"v_min_f32 v252, 0x71800000, v252\n"
"s_nop 0\n"
"v_readlane_b32 s22, v252, 63\n"
"v_mul_f32_dpp v45, v247, v45 row_newbcast:1 row_mask:0xf bank_mask:0xf\n"
"v_cvt_pk_bf16_f32 v41, v42, v43\n"
"v_mul_f32_dpp v74, v206, v74 row_newbcast:2 row_mask:0xf bank_mask:0xf\n"
"v_mul_f32_dpp v29, v246, v29 row_newbcast:1 row_mask:0xf bank_mask:0xf\n"
"v_mul_f32_dpp v58, v247, v58 row_newbcast:2 row_mask:0xf bank_mask:0xf\n"
"v_mul_f32_dpp v92, v206, v92 row_newbcast:0 row_mask:0xf bank_mask:0xf\n"
"v_mfma_f32_16x16x32_bf16 v[36:39], v[108:111], v[224:227], v[36:39]\n"
"v_readlane_b32 s23, v252, 63\n"
"v_mul_f32_dpp v1, v246, v1 row_newbcast:1 row_mask:0xf bank_mask:0xf\n"
"v_mfma_f32_16x16x32_bf16 v[36:39], v[104:107], a[252:255], v[36:39]\n"
"ds_write_b128 v244, a[232:235] offset:34816\n"
"v_mov_b32 v252, v192\n"
"v_mov_b32 v253, v202\n"
"v_sub_u32 v228, v232, v254\n"
"v_mul_f32_dpp v14, v246, v14 row_newbcast:2 row_mask:0xf bank_mask:0xf\n"
"v_cvt_pk_bf16_f32 v9, v10, v11\n"
"v_mfma_f32_16x16x32_bf16 v[4:7], v[100:103], v[224:227], v[4:7]\n"
"ds_write_b64 v250, v[252:253] offset:52224\n"
"v_sub_u32 v230, v234, v254\n"
"v_cvt_pk_bf16_f32 v42, v44, v45\n"
"v_mul_f32_dpp v75, v206, v75 row_newbcast:3 row_mask:0xf bank_mask:0xf\n"
"v_cvt_pk_bf16_f32 v72, v72, v73\n"
"v_mul_f32_dpp v30, v246, v30 row_newbcast:2 row_mask:0xf bank_mask:0xf\n"
"v_mul_f32_dpp v33, v247, v33 row_newbcast:1 row_mask:0xf bank_mask:0xf\n"
"v_mfma_f32_16x16x32_bf16 v[4:7], v[96:99], a[252:255], v[4:7]\n"
"ds_read_b64_tr_b16 a[250:251], v236 offset:54272\n"
"ds_read_b64_tr_b16 v[224:225], v237 offset:50176\n"
"ds_read_b64_tr_b16 v[226:227], v237 offset:54272\n"
"v_mul_f32_dpp v47, v247, v47 row_newbcast:3 row_mask:0xf bank_mask:0xf\n"
"v_mul_f32_dpp v79, v206, v79 row_newbcast:3 row_mask:0xf bank_mask:0xf\n"
"v_cvt_pk_bf16_f32 v27, v30, v31\n"
"v_mul_f32_dpp v63, v247, v63 row_newbcast:3 row_mask:0xf bank_mask:0xf\n"
"v_mul_f32_dpp v90, v206, v90 row_newbcast:2 row_mask:0xf bank_mask:0xf\n"
"v_mul_f32_dpp v93, v206, v93 row_newbcast:1 row_mask:0xf bank_mask:0xf\n"
"v_mov_b64 v[196:197], s[22:23]\n"
"v_mfma_f32_16x16x32_bf16 a[212:215], v[120:123], a[252:255], a[212:215]\n"
"s_waitcnt lgkmcnt(7)\n"
"ds_read_b64_tr_b16 a[252:253], v233 offset:50176\n"
"ds_read_b64_tr_b16 a[254:255], v233 offset:54272\n"
"v_cvt_pk_bf16_f32 v10, v12, v13\n"
"v_cvt_pk_bf16_f32 v43, v46, v47\n"
"v_cvt_pk_bf16_f32 v73, v74, v75\n"
"v_cvt_pk_bf16_f32 v88, v88, v89\n"
"v_cvt_pk_bf16_f32 v89, v90, v91\n"
"v_mul_f32_dpp v35, v247, v35 row_newbcast:3 row_mask:0xf bank_mask:0xf\n"
"v_mul_f32_dpp v67, v206, v67 row_newbcast:3 row_mask:0xf bank_mask:0xf\n"
"v_xor_b32 v244, 0x1000, v244\n"
"v_mfma_f32_16x16x32_bf16 a[224:227], v[120:123], a[244:247], a[224:227]\n"
"ds_write_b128 v248, v[40:43] offset:50176\n"
"v_cvt_pk_bf16_f32 v11, v14, v15\n"
"v_mul_f32_dpp v59, v247, v59 row_newbcast:3 row_mask:0xf bank_mask:0xf\n"
"v_mul_f32_dpp v39, v247, v39 row_newbcast:3 row_mask:0xf bank_mask:0xf\n"
"v_mov_b64 v[198:199], s[22:23]\n"
"v_xor_b32 v232, 0x1000, v228\n"
"v_mfma_f32_16x16x32_bf16 v[16:19], v[96:99], a[244:247], v[16:19]\n"
"s_waitcnt vmcnt(8) lgkmcnt(1)\n"
"ds_write_b128 v248, v[8:11] offset:49152\n"
"v_cvt_pk_bf16_f32 v56, v56, v57\n"
"v_mul_f32_dpp v3, v246, v3 row_newbcast:3 row_mask:0xf bank_mask:0xf\n"
"v_mul_f32_dpp v36, v247, v36 row_newbcast:0 row_mask:0xf bank_mask:0xf\n"
"v_mul_f32_dpp v64, v206, v64 row_newbcast:0 row_mask:0xf bank_mask:0xf\n"
"v_mfma_f32_16x16x32_bf16 v[16:19], v[100:103], a[248:251], v[16:19]\n"
"s_barrier\n"
"ds_read_b64_tr_b16 a[4:5], v249 offset:49152\n"
"ds_read_b64 v[250:251], v245 offset:52224\n"
"ds_read_b64_tr_b16 v[216:217], v228 offset:24576\n"
"ds_read_b64_tr_b16 v[222:223], v232 offset:26624\n"
"v_mul_f32_dpp v95, v206, v95 row_newbcast:3 row_mask:0xf bank_mask:0xf\n"
"v_mul_f32_dpp v34, v247, v34 row_newbcast:2 row_mask:0xf bank_mask:0xf\n"
"v_mul_f32_dpp v69, v206, v69 row_newbcast:1 row_mask:0xf bank_mask:0xf\n"
"v_mfma_f32_16x16x32_bf16 v[48:51], v[104:107], a[244:247], v[48:51]\n"
"ds_read_b64_tr_b16 v[212:213], v232 offset:16384\n"
"v_add_u32 v236, s76, v249\n"
"v_mfma_f32_16x16x32_bf16 v[48:51], v[108:111], a[248:251], v[48:51]\n"
"ds_read_b64_tr_b16 a[6:7], v249 offset:49160\n"
"ds_read_b64_tr_b16 v[186:187], v232 offset:2048\n"
"ds_read_b64_tr_b16 v[188:189], v228 offset:8192\n"
"ds_read_b64_tr_b16 v[194:195], v232 offset:10240\n"
"v_sub_u32 v229, v233, v254\n"
"v_cvt_pk_bf16_f32 v74, v76, v77\n"
"v_cvt_pk_bf16_f32 v75, v78, v79\n"
"v_mul_f32_dpp v62, v247, v62 row_newbcast:2 row_mask:0xf bank_mask:0xf\n"
"v_mul_f32_dpp v0, v246, v0 row_newbcast:0 row_mask:0xf bank_mask:0xf\n"
"v_mul_f32_dpp v4, v246, v4 row_newbcast:0 row_mask:0xf bank_mask:0xf\n"
"v_mfma_f32_16x16x32_bf16 v[80:83], v[112:115], a[244:247], v[80:83]\n"
"ds_write_b128 v248, v[72:75] offset:51200\n"
"v_mul_f32_dpp v28, v246, v28 row_newbcast:0 row_mask:0xf bank_mask:0xf\n"
"v_cvt_pk_bf16_f32 v26, v28, v29\n"
"v_mul_f32_dpp v7, v246, v7 row_newbcast:3 row_mask:0xf bank_mask:0xf\n"
"v_cvt_pk_bf16_f32 v0, v0, v1\n"
"v_mul_f32_dpp v65, v206, v65 row_newbcast:1 row_mask:0xf bank_mask:0xf\n"
"v_mfma_f32_16x16x32_bf16 v[80:83], v[116:119], a[248:251], v[80:83]\n"
"ds_read_b64_tr_b16 a[22:23], v249 offset:50184\n"
"ds_read_b64_tr_b16 a[36:37], v249 offset:51200\n"
"ds_read_b64_tr_b16 v[190:191], v228 offset:10240\n"
"ds_read_b64_tr_b16 v[208:209], v228 offset:16384\n"
"v_mul_f32_dpp v61, v247, v61 row_newbcast:1 row_mask:0xf bank_mask:0xf\n"
"v_cvt_pk_bf16_f32 v57, v58, v59\n"
"v_cvt_pk_bf16_f32 v91, v94, v95\n"
"v_mul_f32_dpp v2, v246, v2 row_newbcast:2 row_mask:0xf bank_mask:0xf\n"
"v_mul_f32_dpp v70, v206, v70 row_newbcast:2 row_mask:0xf bank_mask:0xf\n"
"v_mfma_f32_16x16x32_bf16 v[84:87], v[116:119], v[224:227], v[84:87]\n"
"ds_write_b128 v248, v[24:27] offset:49152\n"
"v_sub_u32 v231, v235, v254\n"
"v_mul_f32_dpp v32, v247, v32 row_newbcast:0 row_mask:0xf bank_mask:0xf\n"
"v_mul_f32_dpp v37, v247, v37 row_newbcast:1 row_mask:0xf bank_mask:0xf\n"
"v_cvt_pk_bf16_f32 v64, v64, v65\n"
"v_mul_f32_dpp v48, v247, v48 row_newbcast:0 row_mask:0xf bank_mask:0xf\n"
"v_mfma_f32_16x16x32_bf16 v[84:87], v[112:115], a[252:255], v[84:87]\n"
"ds_read_b64_tr_b16 a[20:21], v249 offset:50176\n"
"ds_read_b64_tr_b16 a[14:15], v249 offset:49160\n"
"ds_read_b64_tr_b16 v[180:181], v228 offset:0\n"
"ds_read_b64_tr_b16 v[210:211], v228 offset:18432\n"
"v_cvt_pk_bf16_f32 v90, v92, v93\n"
"v_mul_f32_dpp v6, v246, v6 row_newbcast:2 row_mask:0xf bank_mask:0xf\n"
"v_xor_b32 v233, 0x1000, v229\n"
"v_xor_b32 v237, 0x400, v236\n"
"v_mfma_f32_16x16x32_bf16 v[52:55], v[108:111], v[224:227], v[52:55]\n"
"ds_read_b64_tr_b16 a[202:203], v236 offset:2056\n"
"v_cvt_pk_bf16_f32 v58, v60, v61\n"
"v_mfma_f32_16x16x32_bf16 v[52:55], v[104:107], a[252:255], v[52:55]\n"
"s_waitcnt lgkmcnt(15)\n"
"ds_read_b64_tr_b16 a[38:39], v249 offset:51208\n"
"ds_read_b64_tr_b16 a[12:13], v249 offset:49152\n"
"ds_read_b64_tr_b16 v[182:183], v228 offset:2048\n"
"ds_read_b64_tr_b16 v[214:215], v232 offset:18432\n"
"v_cvt_pk_bf16_f32 v1, v2, v3\n"
"v_cvt_pk_bf16_f32 v32, v32, v33\n"
"v_mul_f32_dpp v66, v206, v66 row_newbcast:2 row_mask:0xf bank_mask:0xf\n"
"v_mul_f32_dpp v71, v206, v71 row_newbcast:3 row_mask:0xf bank_mask:0xf\n"
"v_min_f32 v254, v252, v250\n"
"v_sub_f32 v252, v254, v252\n"
"v_mfma_f32_16x16x32_bf16 v[20:23], v[100:103], v[224:227], v[20:23]\n"
"ds_read_b64_tr_b16 v[184:185], v232 offset:0\n"
"ds_read_b64_tr_b16 v[192:193], v232 offset:8192\n"
"v_mfma_f32_16x16x32_bf16 v[20:23], v[96:99], a[252:255], v[20:23]\n"
"ds_write_b128 v248, v[88:91] offset:51200\n"
"v_cvt_pk_bf16_f32 v59, v62, v63\n"
"v_cvt_pk_bf16_f32 v3, v6, v7\n"
"v_exp_f32 v252, v252\n"
"v_mul_f32_dpp v80, v206, v80 row_newbcast:0 row_mask:0xf bank_mask:0xf\n"
"v_mfma_f32_16x16x32_bf16 a[228:231], v[120:123], a[252:255], a[228:231]\n"
"s_waitcnt lgkmcnt(4)\n"
"ds_read_b64_tr_b16 a[44:45], v249 offset:51200\n"
"ds_read_b64_tr_b16 a[46:47], v249 offset:51208\n"
"ds_read_b64_tr_b16 v[218:219], v228 offset:26624\n"
"ds_read_b64_tr_b16 v[220:221], v232 offset:24576\n"
"v_cvt_pk_bf16_f32 v33, v34, v35\n"
"v_mul_f32_dpp v68, v206, v68 row_newbcast:0 row_mask:0xf bank_mask:0xf\n"
"v_sub_f32 v250, v254, v250\n"
"v_exp_f32 v250, v250\n"
"v_mfma_f32_16x16x32_bf16 v[124:127], v[180:183], a[96:99], v[196:199]\n"
"s_waitcnt lgkmcnt(6)\n"
"ds_write_b128 v248, v[56:59] offset:50176\n"
"v_mul_f32_dpp v5, v246, v5 row_newbcast:1 row_mask:0xf bank_mask:0xf\n"
"v_cvt_pk_bf16_f32 v2, v4, v5\n"
"v_cvt_pk_bf16_f32 v34, v36, v37\n"
"v_cvt_pk_bf16_f32 v65, v66, v67\n"
"v_cvt_pk_bf16_f32 v67, v70, v71\n"
"v_mfma_f32_16x16x32_bf16 v[124:127], v[184:187], a[100:103], v[124:127]\n"
"ds_read_b64_tr_b16 v[104:105], v229 offset:8192\n"
"ds_read_b64_tr_b16 v[118:119], v233 offset:18432\n"
"v_mfma_f32_16x16x32_bf16 v[124:127], v[188:191], a[104:107], v[124:127]\n"
"s_waitcnt lgkmcnt(8)\n"
"ds_read_b64_tr_b16 a[28:29], v249 offset:50176\n"
"ds_read_b64_tr_b16 a[30:31], v249 offset:50184\n"
"v_mfma_f32_16x16x32_bf16 v[124:127], v[192:195], a[108:111], v[124:127]\n"
"ds_write_b128 v248, v[0:3] offset:49152\n"
"v_mul_f32_dpp v38, v247, v38 row_newbcast:2 row_mask:0xf bank_mask:0xf\n"
"v_cvt_pk_bf16_f32 v35, v38, v39\n"
"v_cvt_pk_bf16_f32 v66, v68, v69\n"
"v_mul_f32_dpp v20, v246, v20 row_newbcast:0 row_mask:0xf bank_mask:0xf\n"
"v_mfma_f32_16x16x32_bf16 v[124:127], v[208:211], a[144:147], v[124:127]\n"
"ds_read_b64_tr_b16 a[2:3], v249 offset:49160\n"
"ds_read_b64_tr_b16 v[108:109], v233 offset:8192\n"
"v_mfma_f32_16x16x32_bf16 v[124:127], v[212:215], a[148:151], v[124:127]\n"
"s_waitcnt lgkmcnt(9)\n"
"ds_write_b128 v248, v[32:35] offset:50176\n"
"ds_write_b128 v248, v[64:67] offset:51200\n"
"v_mfma_f32_16x16x32_bf16 v[124:127], v[216:219], a[152:155], v[124:127]\n"
"s_waitcnt lgkmcnt(10)\n"
"ds_read_b64_tr_b16 a[16:17], v249 offset:50176\n"
"ds_read_b128 v[28:31], v244 offset:33792\n"
"v_mfma_f32_16x16x32_bf16 v[124:127], v[220:223], a[156:159], v[124:127]\n"
"ds_read_b64_tr_b16 a[0:1], v249 offset:49152\n"
"ds_read_b128 v[24:27], v244 offset:32768\n"
"v_mfma_f32_16x16x32_bf16 v[140:143], v[220:223], a[172:175], v[196:199]\n"
"ds_read_b64_tr_b16 a[34:35], v249 offset:51208\n"
"ds_read_b64_tr_b16 v[112:113], v229 offset:16384\n"
"v_mfma_f32_16x16x32_bf16 v[140:143], v[216:219], a[168:171], v[140:143]\n"
"ds_read_b64_tr_b16 a[18:19], v249 offset:50184\n"
"ds_read_b64_tr_b16 a[246:247], v237 offset:4104\n"
"v_mfma_f32_16x16x32_bf16 v[140:143], v[212:215], a[164:167], v[140:143]\n"
"ds_read_b128 v[60:63], v244 offset:35840\n"
"ds_read_b64_tr_b16 a[250:251], v236 offset:6152\n"
"v_mfma_f32_16x16x32_bf16 v[140:143], v[208:211], a[160:163], v[140:143]\n"
"ds_read_b64_tr_b16 a[32:33], v249 offset:51200\n"
"ds_read_b128 v[56:59], v244 offset:34816\n"
"v_mfma_f32_16x16x32_bf16 v[140:143], v[192:195], a[124:127], v[140:143]\n"
"ds_read_b64_tr_b16 a[194:195], v236 offset:8\n"
"ds_read_b64_tr_b16 a[242:243], v236 offset:4104\n"
"v_mfma_f32_16x16x32_bf16 v[140:143], v[188:191], a[120:123], v[140:143]\n"
"ds_read_b64_tr_b16 v[110:111], v233 offset:10240\n"
"ds_read_b64_tr_b16 a[252:253], v237 offset:6144\n"
"v_mfma_f32_16x16x32_bf16 v[140:143], v[184:187], a[116:119], v[140:143]\n"
"ds_read_b64_tr_b16 a[192:193], v236 offset:0\n"
"ds_read_b64_tr_b16 a[200:201], v236 offset:2048\n"
"v_mfma_f32_16x16x32_bf16 v[140:143], v[180:183], a[112:115], v[140:143]\n"
"ds_read_b64_tr_b16 a[198:199], v237 offset:8\n"
"ds_read_b64_tr_b16 v[102:103], v233 offset:2048\n"
"v_mfma_f32_16x16x32_bf16 v[156:159], v[180:183], a[128:131], v[196:199]\n"
"ds_read_b64_tr_b16 a[244:245], v237 offset:4096\n"
"ds_read_b64_tr_b16 a[254:255], v237 offset:6152\n"
"v_mfma_f32_16x16x32_bf16 v[156:159], v[184:187], a[132:135], v[156:159]\n"
"ds_read_b64_tr_b16 a[206:207], v237 offset:2056\n"
"ds_read_b64_tr_b16 a[248:249], v236 offset:6144\n"
"v_mfma_f32_16x16x32_bf16 v[156:159], v[188:191], a[136:139], v[156:159]\n"
"ds_read_b64_tr_b16 a[240:241], v236 offset:4096\n"
"ds_read_b64_tr_b16 a[236:237], v237 offset:10240\n"
"v_mfma_f32_16x16x32_bf16 v[156:159], v[192:195], a[140:143], v[156:159]\n"
"s_waitcnt lgkmcnt(14)\n"
"ds_read_b64_tr_b16 a[196:197], v237 offset:0\n"
"ds_read_b64_tr_b16 v[96:97], v229 offset:0\n"
"v_mfma_f32_16x16x32_bf16 v[156:159], v[208:211], a[176:179], v[156:159]\n"
"ds_read_b64_tr_b16 a[204:205], v237 offset:2048\n"
"ds_read_b64_tr_b16 v[100:101], v233 offset:0\n"
"v_mfma_f32_16x16x32_bf16 v[156:159], v[212:215], a[180:183], v[156:159]\n"
"s_add_u32 s41, s95, 491520\n"
"ds_read_b64_tr_b16 v[98:99], v229 offset:2048\n"
"ds_read_b64_tr_b16 v[106:107], v229 offset:10240\n"
"v_mfma_f32_16x16x32_bf16 v[156:159], v[216:219], a[184:187], v[156:159]\n"
"s_add_u32 m0, s44, 49152\n"
"v_xor_b32 v235, 0x1000, v231\n"
"v_mul_f32_dpp v87, v206, v87 row_newbcast:3 row_mask:0xf bank_mask:0xf\n"
"v_mfma_f32_16x16x32_bf16 v[156:159], v[220:223], a[188:191], v[156:159]\n"
"s_waitcnt lgkmcnt(10)\n"
"buffer_load_dwordx4 v240, s[28:31], s41 offen lds\n"
"v_mfma_f32_16x16x32_bf16 v[172:175], v[220:223], a[252:255], v[196:199]\n"
"s_waitcnt lgkmcnt(8)\n"
"s_add_u32 s40, s41, s52\n"
"ds_read_b64_tr_b16 v[114:115], v229 offset:18432\n"
"ds_read_b64_tr_b16 v[116:117], v233 offset:16384\n"
"v_mfma_f32_16x16x32_bf16 v[172:175], v[216:219], a[248:251], v[172:175]\n"
"buffer_load_dwordx4 v240, s[28:31], s40 offen offset:1024 lds\n"
"v_mfma_f32_16x16x32_bf16 v[172:175], v[212:215], a[244:247], v[172:175]\n"
"s_waitcnt lgkmcnt(8)\n"
"s_add_u32 s40, s41, s53\n"
"v_mul_f32 v253, v253, v252\n"
"v_xor_b32 v234, 0x1000, v230\n"
"v_mfma_f32_16x16x32_bf16 v[172:175], v[208:211], a[240:243], v[172:175]\n"
"s_waitcnt lgkmcnt(5)\n"
"buffer_load_dwordx4 v240, s[28:31], s40 offen offset:2048 lds\n"
"v_mfma_f32_16x16x32_bf16 v[172:175], v[192:195], a[204:207], v[172:175]\n"
"ds_read_b64_tr_b16 v[120:121], v229 offset:24576\n"
"ds_read_b64_tr_b16 v[226:227], v233 offset:26624\n"
"v_mfma_f32_16x16x32_bf16 v[172:175], v[188:191], a[200:203], v[172:175]\n"
"ds_read_b64_tr_b16 v[122:123], v229 offset:26624\n"
"ds_read_b64_tr_b16 v[214:215], v234 offset:18432\n"
"v_mfma_f32_16x16x32_bf16 v[172:175], v[184:187], a[196:199], v[172:175]\n"
"s_add_u32 s40, s41, s54\n"
"ds_read_b64_tr_b16 v[224:225], v233 offset:24576\n"
"v_fmac_f32 v253, v251, v250\n"
"v_mfma_f32_16x16x32_bf16 v[172:175], v[180:183], a[192:195], v[172:175]\n"
"s_waitcnt lgkmcnt(7)\n"
"buffer_load_dwordx4 v240, s[28:31], s40 offen offset:3072 lds\n"
"v_mfma_f32_16x16x32_bf16 v[128:131], v[96:99], a[96:99], v[196:199]\n"
"v_rcp_f32 v253, v253\n"
"v_mfma_f32_16x16x32_bf16 v[128:131], v[100:103], a[100:103], v[128:131]\n"
"v_mul_f32 v252, v252, v253\n"
"v_mul_f32_dpp v49, v247, v49 row_newbcast:1 row_mask:0xf bank_mask:0xf\n"
"v_mfma_f32_16x16x32_bf16 v[128:131], v[104:107], a[104:107], v[128:131]\n"
"ds_read_b64_tr_b16 v[220:221], v234 offset:24576\n"
"v_mul_f32 v250, v250, v253\n"
"v_mfma_f32_16x16x32_bf16 v[128:131], v[108:111], a[108:111], v[128:131]\n"
"s_waitcnt lgkmcnt(6)\n"
"ds_read_b64_tr_b16 v[180:181], v230 offset:0\n"
"v_mov_b32_dpp v250, v250 row_ror:12 row_mask:0x2 bank_mask:0xf\n"
"v_mfma_f32_16x16x32_bf16 v[128:131], v[112:115], a[144:147], v[128:131]\n"
"s_add_u32 s21, s60, 32\n"
"ds_read_b64_tr_b16 v[188:189], v230 offset:8192\n"
"ds_read_b64_tr_b16 v[194:195], v234 offset:10240\n"
"v_mfma_f32_16x16x32_bf16 v[128:131], v[116:119], a[148:151], v[128:131]\n"
"s_waitcnt lgkmcnt(5)\n"
"s_mov_b32 s69, 1\n"
"ds_read_b64_tr_b16 v[212:213], v234 offset:16384\n"
"v_mov_b32_dpp v250, v250 row_ror:8 row_mask:0x4 bank_mask:0xf\n"
"v_mfma_f32_16x16x32_bf16 v[128:131], v[120:123], a[152:155], v[128:131]\n"
"s_waitcnt lgkmcnt(5)\n"
"v_bfe_u32 v201, v242, 2, 7\n"
"v_add_u32 v201, 0x100, v201\n"
"ds_read_b64_tr_b16 v[192:193], v234 offset:8192\n"
"v_mfma_f32_16x16x32_bf16 v[128:131], v[224:227], a[156:159], v[128:131]\n"
"global_load_dword v200, v201, s[6:7] sc1\n"
"v_mov_b32_dpp v252, v252 row_ror:12 row_mask:0x2 bank_mask:0xf\n"
"v_mul_f32_dpp v22, v246, v22 row_newbcast:2 row_mask:0xf bank_mask:0xf\n"
"v_mul_f32_dpp v84, v206, v84 row_newbcast:0 row_mask:0xf bank_mask:0xf\n"
"v_mfma_f32_16x16x32_bf16 v[144:147], v[224:227], a[172:175], v[196:199]\n"
"v_mov_b32_dpp v250, v250 row_ror:4 row_mask:0x8 bank_mask:0xf\n"
"v_accvgpr_read_b32 v254, a208\n"
"v_mfma_f32_16x16x32_bf16 v[144:147], v[120:123], a[168:171], v[144:147]\n"
"v_mul_f32_dpp v24, v250, v24 row_newbcast:0 row_mask:0xf bank_mask:0xf\n"
"v_mul_f32_dpp v17, v246, v17 row_newbcast:1 row_mask:0xf bank_mask:0xf\n"
"v_mfma_f32_16x16x32_bf16 v[144:147], v[116:119], a[164:167], v[144:147]\n"
"v_mul_f32_dpp v29, v250, v29 row_newbcast:1 row_mask:0xf bank_mask:0xf\n"
"v_mul_f32_dpp v23, v246, v23 row_newbcast:3 row_mask:0xf bank_mask:0xf\n"
"v_mfma_f32_16x16x32_bf16 v[144:147], v[112:115], a[160:163], v[144:147]\n"
"ds_read_b64_tr_b16 v[210:211], v230 offset:18432\n"
"v_accvgpr_read_b32 v251, a211\n"
"v_mfma_f32_16x16x32_bf16 v[144:147], v[108:111], a[124:127], v[144:147]\n"
"v_mov_b32_dpp v252, v252 row_ror:8 row_mask:0x4 bank_mask:0xf\n"
"v_mul_f32_dpp v31, v250, v31 row_newbcast:3 row_mask:0xf bank_mask:0xf\n"
"v_mfma_f32_16x16x32_bf16 v[144:147], v[104:107], a[120:123], v[144:147]\n"
"v_accvgpr_read_b32 v253, a210\n"
"v_mul_f32_dpp v82, v206, v82 row_newbcast:2 row_mask:0xf bank_mask:0xf\n"
"v_mfma_f32_16x16x32_bf16 v[144:147], v[100:103], a[116:119], v[144:147]\n"
"ds_read_b64_tr_b16 a[220:221], v237 offset:8192\n"
"v_mul_f32_dpp v57, v250, v57 row_newbcast:1 row_mask:0xf bank_mask:0xf\n"
"v_mfma_f32_16x16x32_bf16 v[144:147], v[96:99], a[112:115], v[144:147]\n"
"ds_read_b64_tr_b16 v[184:185], v234 offset:0\n"
"v_mul_f32_dpp v25, v250, v25 row_newbcast:1 row_mask:0xf bank_mask:0xf\n"
"v_mfma_f32_16x16x32_bf16 v[160:163], v[96:99], a[128:131], v[196:199]\n"
"ds_read_b64_tr_b16 v[222:223], v234 offset:26624\n"
"v_mul_f32_dpp v16, v246, v16 row_newbcast:0 row_mask:0xf bank_mask:0xf\n"
"v_mfma_f32_16x16x32_bf16 v[160:163], v[100:103], a[132:135], v[160:163]\n"
"ds_read_b64_tr_b16 v[182:183], v230 offset:2048\n"
"v_mul_f32_dpp v55, v247, v55 row_newbcast:3 row_mask:0xf bank_mask:0xf\n"
"v_mfma_f32_16x16x32_bf16 v[160:163], v[104:107], a[136:139], v[160:163]\n"
"v_mov_b32_dpp v252, v252 row_ror:4 row_mask:0x8 bank_mask:0xf\n"
"v_mul_f32_dpp v81, v206, v81 row_newbcast:1 row_mask:0xf bank_mask:0xf\n"
"v_mfma_f32_16x16x32_bf16 v[160:163], v[108:111], a[140:143], v[160:163]\n"
"ds_read_b64_tr_b16 a[222:223], v237 offset:8200\n"
"v_mul_f32_dpp v19, v246, v19 row_newbcast:3 row_mask:0xf bank_mask:0xf\n"
"v_mfma_f32_16x16x32_bf16 v[160:163], v[112:115], a[176:179], v[160:163]\n"
"v_fmac_f32_dpp v24, v252, v254 row_newbcast:0 row_mask:0xf bank_mask:0xf\n"
"v_mul_f32_dpp v58, v250, v58 row_newbcast:2 row_mask:0xf bank_mask:0xf\n"
"v_mfma_f32_16x16x32_bf16 v[160:163], v[116:119], a[180:183], v[160:163]\n"
"ds_read_b64_tr_b16 v[186:187], v234 offset:2048\n"
"v_mul_f32_dpp v28, v250, v28 row_newbcast:0 row_mask:0xf bank_mask:0xf\n"
"v_mfma_f32_16x16x32_bf16 v[160:163], v[120:123], a[184:187], v[160:163]\n"
"ds_read_b64_tr_b16 v[190:191], v230 offset:10240\n"
"v_mul_f32_dpp v63, v250, v63 row_newbcast:3 row_mask:0xf bank_mask:0xf\n"
"v_mfma_f32_16x16x32_bf16 v[160:163], v[224:227], a[188:191], v[160:163]\n"
"v_fmac_f32_dpp v25, v252, v255 row_newbcast:1 row_mask:0xf bank_mask:0xf\n"
"v_accvgpr_read_b32 v255, a213\n"
"v_mfma_f32_16x16x32_bf16 v[176:179], v[224:227], a[252:255], v[196:199]\n"
"v_mul_f32_dpp v26, v250, v26 row_newbcast:2 row_mask:0xf bank_mask:0xf\n"
"v_mul_f32_dpp v59, v250, v59 row_newbcast:3 row_mask:0xf bank_mask:0xf\n"
"v_mfma_f32_16x16x32_bf16 v[176:179], v[120:123], a[248:251], v[176:179]\n"
"ds_read_b64_tr_b16 v[208:209], v230 offset:16384\n"
"v_mul_f32_dpp v27, v250, v27 row_newbcast:3 row_mask:0xf bank_mask:0xf\n"
"v_mfma_f32_16x16x32_bf16 v[176:179], v[116:119], a[244:247], v[176:179]\n"
"v_accvgpr_read_b32 v254, a212\n"
"v_mul_f32_dpp v62, v250, v62 row_newbcast:2 row_mask:0xf bank_mask:0xf\n"
"v_mfma_f32_16x16x32_bf16 v[176:179], v[112:115], a[240:243], v[176:179]\n"
"v_fmac_f32_dpp v26, v252, v253 row_newbcast:2 row_mask:0xf bank_mask:0xf\n"
"v_xor_b32 v249, s75, v249\n"
"v_mfma_f32_16x16x32_bf16 v[176:179], v[108:111], a[204:207], v[176:179]\n"
"ds_read_b64_tr_b16 v[218:219], v230 offset:26624\n"
"v_mul_f32_dpp v61, v250, v61 row_newbcast:1 row_mask:0xf bank_mask:0xf\n"
"v_mfma_f32_16x16x32_bf16 v[176:179], v[104:107], a[200:203], v[176:179]\n"
"v_fmac_f32_dpp v27, v252, v251 row_newbcast:3 row_mask:0xf bank_mask:0xf\n"
"v_fmac_f32_dpp v29, v252, v255 row_newbcast:1 row_mask:0xf bank_mask:0xf\n"
"v_mfma_f32_16x16x32_bf16 v[176:179], v[100:103], a[196:199], v[176:179]\n"
"ds_read_b64_tr_b16 v[216:217], v230 offset:24576\n"
"v_cvt_pk_bf16_f32 v16, v16, v17\n"
"v_mfma_f32_16x16x32_bf16 v[176:179], v[96:99], a[192:195], v[176:179]\n"
"s_waitcnt lgkmcnt(3)\n"
"ds_read_b64_tr_b16 v[98:99], v231 offset:2048\n"
"ds_read_b64_tr_b16 v[100:101], v235 offset:0\n"
"v_accvgpr_read_b32 v253, a214\n"
"v_mul_f32_dpp v30, v250, v30 row_newbcast:2 row_mask:0xf bank_mask:0xf\n"
"v_cvt_pk_bf16_f32 v24, v24, v25\n"
"v_mul_f32_dpp v18, v246, v18 row_newbcast:2 row_mask:0xf bank_mask:0xf\n"
"v_mul_f32_dpp v56, v250, v56 row_newbcast:0 row_mask:0xf bank_mask:0xf\n"
"v_accvgpr_read_b32 v255, a225\n"
"v_mfma_f32_16x16x32_bf16 v[132:135], v[180:183], a[96:99], v[196:199]\n"
"v_fmac_f32_dpp v30, v252, v253 row_newbcast:2 row_mask:0xf bank_mask:0xf\n"
"v_mul_f32_dpp v60, v250, v60 row_newbcast:0 row_mask:0xf bank_mask:0xf\n"
"v_mfma_f32_16x16x32_bf16 v[132:135], v[184:187], a[100:103], v[132:135]\n"
"v_mul_f32_dpp v83, v206, v83 row_newbcast:3 row_mask:0xf bank_mask:0xf\n"
"v_accvgpr_read_b32 v253, a226\n"
"v_mfma_f32_16x16x32_bf16 v[132:135], v[188:191], a[104:107], v[132:135]\n"
"ds_read_b64_tr_b16 v[108:109], v235 offset:8192\n"
"v_fmac_f32_dpp v58, v252, v253 row_newbcast:2 row_mask:0xf bank_mask:0xf\n"
"v_mfma_f32_16x16x32_bf16 v[132:135], v[192:195], a[108:111], v[132:135]\n"
"s_waitcnt lgkmcnt(5)\n"
"ds_read_b64_tr_b16 v[120:121], v231 offset:24576\n"
"v_accvgpr_read_b32 v253, a230\n"
"v_mfma_f32_16x16x32_bf16 v[132:135], v[208:211], a[144:147], v[132:135]\n"
"s_waitcnt vmcnt(0)\n"
"v_accvgpr_read_b32 v251, a215\n"
"v_mul_f32_dpp v85, v206, v85 row_newbcast:1 row_mask:0xf bank_mask:0xf\n"
"v_mfma_f32_16x16x32_bf16 v[132:135], v[212:215], a[148:151], v[132:135]\n"
"s_waitcnt lgkmcnt(4)\n"
"v_subrev_u32 v200, s21, v200\n"
"v_fmac_f32_dpp v62, v252, v253 row_newbcast:2 row_mask:0xf bank_mask:0xf\n"
"v_mfma_f32_16x16x32_bf16 v[132:135], v[216:219], a[152:155], v[132:135]\n"
"v_cmp_lt_u32 vcc, s69, v200\n"
"ds_read_b64_tr_b16 v[226:227], v235 offset:26624\n"
"v_fmac_f32_dpp v28, v252, v254 row_newbcast:0 row_mask:0xf bank_mask:0xf\n"
"v_fmac_f32_dpp v31, v252, v251 row_newbcast:3 row_mask:0xf bank_mask:0xf\n"
"v_cvt_pk_bf16_f32 v25, v26, v27\n"
"v_cvt_pk_bf16_f32 v27, v30, v31\n"
"v_accvgpr_read_b32 v254, a224\n"
"v_accvgpr_read_b32 v251, a227\n"
"v_mfma_f32_16x16x32_bf16 v[132:135], v[220:223], a[156:159], v[132:135]\n"
"ds_read_b64_tr_b16 v[96:97], v231 offset:0\n"
"v_mul_f32_dpp v21, v246, v21 row_newbcast:1 row_mask:0xf bank_mask:0xf\n"
"v_mul_f32_dpp v86, v206, v86 row_newbcast:2 row_mask:0xf bank_mask:0xf\n"
"v_fmac_f32_dpp v56, v252, v254 row_newbcast:0 row_mask:0xf bank_mask:0xf\n"
"v_fmac_f32_dpp v57, v252, v255 row_newbcast:1 row_mask:0xf bank_mask:0xf\n"
"v_fmac_f32_dpp v59, v252, v251 row_newbcast:3 row_mask:0xf bank_mask:0xf\n"
"v_accvgpr_read_b32 v255, a229\n"
"v_accvgpr_read_b32 v251, a231\n"
"v_cvt_pk_bf16_f32 v56, v56, v57\n"
"s_cbranch_vccnz LGV_%=\nLGVK_%=:\n"
"v_mfma_f32_16x16x32_bf16 v[148:151], v[220:223], a[172:175], v[196:199]\n"
"ds_read_b64_tr_b16 a[234:235], v236 offset:10248\n"
"v_cvt_pk_bf16_f32 v26, v28, v29\n"
"v_mfma_f32_16x16x32_bf16 v[148:151], v[216:219], a[168:171], v[148:151]\n"
"v_add_u32_dpp v200, v200, v200 quad_perm:[1,0,3,2] row_mask:0xf bank_mask:0xf\n"
"ds_read_b64_tr_b16 v[104:105], v231 offset:8192\n"
"v_mfma_f32_16x16x32_bf16 v[148:151], v[212:215], a[164:167], v[148:151]\n"
"v_accvgpr_read_b32 v254, a228\n"
"v_cvt_pk_bf16_f32 v57, v58, v59\n"
"v_mfma_f32_16x16x32_bf16 v[148:151], v[208:211], a[160:163], v[148:151]\n"
"v_add_u32_dpp v200, v200, v200 quad_perm:[2,3,0,1] row_mask:0xf bank_mask:0xf\n"
"ds_write_b128 v243, v[24:27] offset:0\n"
"v_cvt_pk_bf16_f32 v17, v18, v19\n"
"v_mul_f32_dpp v51, v247, v51 row_newbcast:3 row_mask:0xf bank_mask:0xf\n"
"v_fmac_f32_dpp v60, v252, v254 row_newbcast:0 row_mask:0xf bank_mask:0xf\n"
"v_fmac_f32_dpp v63, v252, v251 row_newbcast:3 row_mask:0xf bank_mask:0xf\n"
"v_mfma_f32_16x16x32_bf16 v[148:151], v[192:195], a[124:127], v[148:151]\n"
"v_fmac_f32_dpp v61, v252, v255 row_newbcast:1 row_mask:0xf bank_mask:0xf\n"
"v_cvt_pk_bf16_f32 v59, v62, v63\n"
"v_mfma_f32_16x16x32_bf16 v[148:151], v[188:191], a[120:123], v[148:151]\n"
"v_cmp_lt_u32 vcc, 1, v200\n"
"ds_read_b64_tr_b16 v[118:119], v235 offset:18432\n"
"v_mfma_f32_16x16x32_bf16 v[148:151], v[184:187], a[116:119], v[148:151]\n"
"ds_read_b64_tr_b16 v[102:103], v235 offset:2048\n"
"v_cvt_pk_bf16_f32 v58, v60, v61\n"
"v_mfma_f32_16x16x32_bf16 v[148:151], v[180:183], a[112:115], v[148:151]\n"
"ds_write_b128 v243, v[56:59] offset:2048\n"
"v_cvt_pk_bf16_f32 v80, v80, v81\n"
"v_cvt_pk_bf16_f32 v81, v82, v83\n"
"v_mfma_f32_16x16x32_bf16 v[164:167], v[180:183], a[128:131], v[196:199]\n"
"s_mov_b32 s47, 0\n"
"v_xor_b32 v248, s75, v248\n"
"v_cvt_pk_bf16_f32 v83, v86, v87\n"
"v_mfma_f32_16x16x32_bf16 v[164:167], v[184:187], a[132:135], v[164:167]\n"
"s_mov_b32 s89, vcc_lo\n"
"ds_read_b64_tr_b16 v[114:115], v231 offset:18432\n"
"ds_read_b64_tr_b16 v[122:123], v231 offset:26624\n"
"v_mfma_f32_16x16x32_bf16 v[164:167], v[188:191], a[136:139], v[164:167]\n"
"s_lshr_b32 s90, s89, s93\n"
"v_mul_f32_dpp v50, v247, v50 row_newbcast:2 row_mask:0xf bank_mask:0xf\n"
"v_cvt_pk_bf16_f32 v82, v84, v85\n"
"v_mfma_f32_16x16x32_bf16 v[164:167], v[192:195], a[140:143], v[164:167]\n"
"s_and_b32 s90, s90, 1\n"
"ds_read_b64_tr_b16 v[106:107], v231 offset:10240\n"
"ds_read_b64_tr_b16 v[110:111], v235 offset:10240\n"
"v_mfma_f32_16x16x32_bf16 v[164:167], v[208:211], a[176:179], v[164:167]\n"
"s_lshl_b32 s16, s61, 7\n"
"ds_read_b64_tr_b16 v[116:117], v235 offset:16384\n"
"ds_read_b64_tr_b16 v[224:225], v235 offset:24576\n"
"v_mfma_f32_16x16x32_bf16 v[164:167], v[212:215], a[180:183], v[164:167]\n"
"s_add_u32 s90, s90, s16\n"
"ds_read_b64_tr_b16 v[112:113], v231 offset:16384\n"
"v_mul_f32_dpp v53, v247, v53 row_newbcast:1 row_mask:0xf bank_mask:0xf\n"
"v_mfma_f32_16x16x32_bf16 v[164:167], v[216:219], a[184:187], v[164:167]\n"
"s_waitcnt vmcnt(63) lgkmcnt(7)\n"
"s_lshl_b32 s90, s90, 13\n"
"ds_read_b64_tr_b16 a[216:217], v236 offset:8192\n"
"v_mfma_f32_16x16x32_bf16 v[164:167], v[220:223], a[188:191], v[164:167]\n"
"s_barrier\n"
"s_lshl_b32 s16, s95, 9\n"
"ds_write_b128 v248, v[80:83] offset:51200\n"
"v_cvt_pk_bf16_f32 v48, v48, v49\n"
"v_mfma_f32_16x16x32_bf16 v[136:139], v[96:99], a[96:99], v[196:199]\n"
"s_add_u32 s16, s16, s90\n"
"ds_read_b64_tr_b16 a[214:215], v237 offset:12296\n"
"v_cvt_pk_bf16_f32 v18, v20, v21\n"
"v_mfma_f32_16x16x32_bf16 v[136:139], v[100:103], a[100:103], v[136:139]\n"
"s_waitcnt lgkmcnt(7)\n"
"s_add_u32 s17, s16, 4096\n"
"ds_read_b64_tr_b16 v[216:217], v228 offset:57344\n"
"v_cvt_pk_bf16_f32 v19, v22, v23\n"
"v_mfma_f32_16x16x32_bf16 v[136:139], v[104:107], a[104:107], v[136:139]\n"
"s_waitcnt lgkmcnt(7)\n"
"s_mov_b32 s72, s61\n"
"ds_read_b64_tr_b16 v[194:195], v232 offset:43008\n"
"v_cvt_pk_bf16_f32 v49, v50, v51\n"
"v_mfma_f32_16x16x32_bf16 v[136:139], v[108:111], a[108:111], v[136:139]\n"
"s_waitcnt lgkmcnt(5)\n"
"s_mov_b32 s21, s60\n"
"ds_write_b128 v248, v[16:19] offset:49152\n"
"v_mul_f32_dpp v54, v247, v54 row_newbcast:2 row_mask:0xf bank_mask:0xf\n"
"v_mfma_f32_16x16x32_bf16 v[136:139], v[112:115], a[144:147], v[136:139]\n"
"s_mov_b32 s18, s77\n"
"v_mul_f32_dpp v52, v247, v52 row_newbcast:0 row_mask:0xf bank_mask:0xf\n"
"v_cvt_pk_bf16_f32 v50, v52, v53\n"
"v_mfma_f32_16x16x32_bf16 v[136:139], v[116:119], a[148:151], v[136:139]\n"
"s_mov_b32 s19, s77\n"
"ds_read_b64_tr_b16 a[10:11], v249 offset:49160\n"
"ds_read_b64_tr_b16 a[40:41], v249 offset:51200\n"
"v_mfma_f32_16x16x32_bf16 v[136:139], v[120:123], a[152:155], v[136:139]\n"
"s_mov_b32 s69, -1\n"
"ds_read_b64_tr_b16 a[42:43], v249 offset:51208\n"
"v_cvt_pk_bf16_f32 v51, v54, v55\n"
"v_mfma_f32_16x16x32_bf16 v[136:139], v[224:227], a[156:159], v[136:139]\n"
"s_mul_i32 s88, s61, 0x10011111\n"
"ds_write_b128 v248, v[48:51] offset:50176\n"
"v_mfma_f32_16x16x32_bf16 v[152:155], v[224:227], a[172:175], v[196:199]\n"
"s_add_u32 s88, s88, 484675\n"
"v_mov_b64 v[86:87], 0\n"
"v_mfma_f32_16x16x32_bf16 v[152:155], v[120:123], a[168:171], v[152:155]\n"
"s_and_b32 s88, s88, 0x70077777\n"
"ds_read_b64_tr_b16 a[8:9], v249 offset:49152\n"
"ds_read_b64_tr_b16 v[188:189], v228 offset:40960\n"
"v_mfma_f32_16x16x32_bf16 v[152:155], v[116:119], a[164:167], v[152:155]\n"
"s_or_b32 s88, s88, 0x9800000\n"
"ds_read_b64_tr_b16 a[24:25], v249 offset:50176\n"
"ds_read_b64_tr_b16 a[26:27], v249 offset:50184\n"
"v_mfma_f32_16x16x32_bf16 v[152:155], v[112:115], a[160:163], v[152:155]\n"
"s_mov_b32 s84, s6\n"
"ds_read_b64_tr_b16 v[186:187], v232 offset:34816\n"
"v_mov_b64 v[14:15], s[22:23]\n"
"v_mfma_f32_16x16x32_bf16 v[152:155], v[108:111], a[124:127], v[152:155]\n"
"s_and_b32 s85, s7, 0xffff\n"
"ds_read_b64_tr_b16 v[180:181], v228 offset:32768\n"
"ds_read_b64_tr_b16 v[190:191], v228 offset:43008\n"
"v_mfma_f32_16x16x32_bf16 v[152:155], v[104:107], a[120:123], v[152:155]\n"
"s_mov_b32 s86, 128\n"
"ds_read_b64_tr_b16 v[210:211], v228 offset:51200\n"
"ds_read_b64_tr_b16 v[222:223], v232 offset:59392\n"
"v_mfma_f32_16x16x32_bf16 v[152:155], v[100:103], a[116:119], v[152:155]\n"
"s_mov_b32 s87, 131072\n"
"ds_read_b64_tr_b16 v[182:183], v228 offset:34816\n"
"v_mov_b64 v[52:53], s[22:23]\n"
"v_mfma_f32_16x16x32_bf16 v[152:155], v[96:99], a[112:115], v[152:155]\n"
"ds_read_b64_tr_b16 v[184:185], v232 offset:32768\n"
"ds_read_b64_tr_b16 v[214:215], v232 offset:51200\n"
"v_mfma_f32_16x16x32_bf16 v[168:171], v[96:99], a[128:131], v[196:199]\n"
"ds_read_b64_tr_b16 a[210:211], v236 offset:12296\n"
"v_and_b32 v243, 0x3f0, v242\n"
"v_mfma_f32_16x16x32_bf16 v[168:171], v[100:103], a[132:135], v[168:171]\n"
"ds_read_b64_tr_b16 a[226:227], v236 offset:14344\n"
"ds_read_b64_tr_b16 v[212:213], v232 offset:49152\n"
"v_mfma_f32_16x16x32_bf16 v[168:171], v[104:107], a[136:139], v[168:171]\n"
"ds_read_b64_tr_b16 v[192:193], v232 offset:40960\n"
"v_mov_b64 v[18:19], s[22:23]\n"
"v_mfma_f32_16x16x32_bf16 v[168:171], v[108:111], a[140:143], v[168:171]\n"
"s_waitcnt lgkmcnt(11)\n"
"ds_read_b64_tr_b16 a[218:219], v236 offset:8200\n"
"ds_read_b64_tr_b16 v[208:209], v228 offset:49152\n"
"v_mfma_f32_16x16x32_bf16 v[168:171], v[112:115], a[176:179], v[168:171]\n"
"ds_read_b64_tr_b16 v[218:219], v228 offset:59392\n"
"v_mov_b64 v[2:3], s[22:23]\n"
"v_mfma_f32_16x16x32_bf16 v[168:171], v[116:119], a[180:183], v[168:171]\n"
"ds_read_b64_tr_b16 a[224:225], v236 offset:14336\n"
"ds_read_b64_tr_b16 a[230:231], v237 offset:14344\n"
"v_mfma_f32_16x16x32_bf16 v[168:171], v[120:123], a[184:187], v[168:171]\n"
"s_add_u32 s41, s70, 327680\n"
"s_add_u32 m0, s71, 32768\n"
"ds_read_b64_tr_b16 v[220:221], v232 offset:57344\n"
"v_mfma_f32_16x16x32_bf16 v[168:171], v[224:227], a[188:191], v[168:171]\n"
"s_waitcnt lgkmcnt(10)\n"
"buffer_load_dwordx4 v240, s[28:31], s41 offen lds\n"
"v_mfma_f32_16x16x32_bf16 v[124:127], v[180:183], a[48:51], v[124:127]\n"
"v_mov_b64 v[42:43], s[22:23]\n"
"v_mov_b64 v[94:95], 0\n"
"v_mfma_f32_16x16x32_bf16 v[124:127], v[184:187], a[52:55], v[124:127]\n"
"v_bfe_u32 v254, v242, 4, 4\n"
"v_mov_b64 v[40:41], s[22:23]\n"
"v_mfma_f32_16x16x32_bf16 v[124:127], v[188:191], a[56:59], v[124:127]\n"
"s_waitcnt lgkmcnt(6)\n"
"s_add_u32 s40, s41, s52\n"
"ds_read_b64_tr_b16 a[228:229], v237 offset:14336\n"
"ds_read_b64_tr_b16 v[108:109], v233 offset:40960\n"
"v_mfma_f32_16x16x32_bf16 v[124:127], v[192:195], a[60:63], v[124:127]\n"
"s_waitcnt lgkmcnt(6)\n"
"buffer_load_dwordx4 v240, s[28:31], s40 offen offset:1024 lds\n"
"v_mfma_f32_16x16x32_bf16 v[124:127], v[208:211], a[0:3], v[124:127]\n"
"v_xor_b32 v249, s75, v249\n"
"v_mfma_f32_16x16x32_bf16 v[124:127], v[212:215], a[4:7], v[124:127]\n"
"s_waitcnt lgkmcnt(3)\n"
"ds_read_b64_tr_b16 a[208:209], v236 offset:12288\n"
"ds_read_b64_tr_b16 a[212:213], v237 offset:12288\n"
"v_mfma_f32_16x16x32_bf16 v[124:127], v[216:219], a[8:11], v[124:127]\n"
"s_waitcnt lgkmcnt(4)\n"
"s_add_u32 s40, s41, s53\n"
"ds_read_b64_tr_b16 v[98:99], v229 offset:34816\n"
"ds_read_b64_tr_b16 v[112:113], v229 offset:49152\n"
"v_mfma_f32_16x16x32_bf16 v[124:127], v[220:223], a[12:15], v[124:127]\n"
"buffer_load_dwordx4 v240, s[28:31], s40 offen offset:2048 lds\n"
"v_mfma_f32_16x16x32_bf16 v[140:143], v[220:223], a[28:31], v[140:143]\n"
"v_mul_u32_u24 v254, 2560, v254\n"
"v_bfe_u32 v255, v242, 8, 1\n"
"v_mfma_f32_16x16x32_bf16 v[140:143], v[216:219], a[24:27], v[140:143]\n"
"v_mov_b64 v[10:11], s[22:23]\n"
"v_mfma_f32_16x16x32_bf16 v[140:143], v[212:215], a[20:23], v[140:143]\n"
"ds_read_b64_tr_b16 v[104:105], v229 offset:40960\n"
"ds_read_b64_tr_b16 v[106:107], v229 offset:43008\n"
"v_mfma_f32_16x16x32_bf16 v[140:143], v[208:211], a[16:19], v[140:143]\n"
"ds_read_b64_tr_b16 v[110:111], v233 offset:43008\n"
"v_subrev_f32 v124, s22, v124\n"
"v_mfma_f32_16x16x32_bf16 v[140:143], v[192:195], a[76:79], v[140:143]\n"
"ds_read_b64_tr_b16 a[232:233], v236 offset:10240\n"
"ds_read_b64_tr_b16 v[116:117], v233 offset:49152\n"
"v_mfma_f32_16x16x32_bf16 v[140:143], v[188:191], a[72:75], v[140:143]\n"
"ds_read_b64_tr_b16 v[118:119], v233 offset:51200\n"
"v_lshl_add_u32 v254, v255, 5, v254\n"
"v_mfma_f32_16x16x32_bf16 v[140:143], v[184:187], a[68:71], v[140:143]\n"
"s_add_u32 s40, s41, s54\n"
"ds_read_b64_tr_b16 a[238:239], v237 offset:10248\n"
"ds_read_b64_tr_b16 v[114:115], v229 offset:51200\n"
"v_mfma_f32_16x16x32_bf16 v[140:143], v[180:183], a[64:67], v[140:143]\n"
"buffer_load_dwordx4 v240, s[28:31], s40 offen offset:3072 lds\n"
"v_mfma_f32_16x16x32_bf16 v[156:159], v[180:183], a[80:83], v[156:159]\n"
"s_add_u32 s41, s70, 491520\n"
"s_add_u32 m0, s71, 49152\n"
"v_subrev_f32 v125, s22, v125\n"
"v_mfma_f32_16x16x32_bf16 v[156:159], v[184:187], a[84:87], v[156:159]\n"
"buffer_load_dwordx4 v240, s[28:31], s41 offen lds\n"
"v_mfma_f32_16x16x32_bf16 v[156:159], v[188:191], a[88:91], v[156:159]\n"
"v_subrev_f32 v126, s22, v126\n"
"v_mfma_f32_16x16x32_bf16 v[156:159], v[192:195], a[92:95], v[156:159]\n"
"v_mov_b64 v[36:37], s[22:23]\n"
"v_cvt_pk_bf16_f32 v124, v124, v125\n"
"v_mfma_f32_16x16x32_bf16 v[156:159], v[208:211], a[32:35], v[156:159]\n"
"ds_read_b64_tr_b16 v[100:101], v233 offset:32768\n"
"ds_read_b64_tr_b16 v[226:227], v233 offset:59392\n"
"v_mfma_f32_16x16x32_bf16 v[156:159], v[212:215], a[36:39], v[156:159]\n"
"ds_read_b64_tr_b16 v[96:97], v229 offset:32768\n"
"v_add_u32 v236, s94, v243\n"
"v_mfma_f32_16x16x32_bf16 v[156:159], v[216:219], a[40:43], v[156:159]\n"
"s_add_u32 s40, s41, s52\n"
"ds_read_b64_tr_b16 v[102:103], v233 offset:34816\n"
"ds_read_b64_tr_b16 v[122:123], v229 offset:59392\n"
"v_mfma_f32_16x16x32_bf16 v[156:159], v[220:223], a[44:47], v[156:159]\n"
"s_waitcnt lgkmcnt(5)\n"
"buffer_load_dwordx4 v240, s[28:31], s40 offen offset:1024 lds\n"
"v_mfma_f32_16x16x32_bf16 v[172:175], v[220:223], a[228:231], v[172:175]\n"
"s_add_u32 s40, s41, s53\n"
"v_subrev_f32 v141, s22, v141\n"
"v_mfma_f32_16x16x32_bf16 v[172:175], v[216:219], a[224:227], v[172:175]\n"
"buffer_load_dwordx4 v240, s[28:31], s40 offen offset:2048 lds\n"
"v_mfma_f32_16x16x32_bf16 v[172:175], v[212:215], a[212:215], v[172:175]\n"
"v_mov_b64 v[24:25], s[22:23]\n"
"v_subrev_f32 v127, s22, v127\n"
"v_mfma_f32_16x16x32_bf16 v[172:175], v[208:211], a[208:211], v[172:175]\n"
"v_bfe_u32 v255, v242, 9, 1\n"
"v_mov_b64 v[12:13], s[22:23]\n"
"v_mfma_f32_16x16x32_bf16 v[172:175], v[192:195], a[236:239], v[172:175]\n"
"s_add_u32 s40, s41, s54\n"
"ds_read_b64_tr_b16 v[120:121], v229 offset:57344\n"
"ds_read_b64_tr_b16 v[224:225], v233 offset:57344\n"
"v_mfma_f32_16x16x32_bf16 v[172:175], v[188:191], a[232:235], v[172:175]\n"
"buffer_load_dwordx4 v240, s[28:31], s40 offen offset:3072 lds\n"
"v_mfma_f32_16x16x32_bf16 v[172:175], v[184:187], a[220:223], v[172:175]\n"
"s_lshl_b32 s62, s70, 9\n"
"v_bfe_u32 v237, v242, 4, 3\n"
"v_subrev_f32 v140, s22, v140\n"
"v_mfma_f32_16x16x32_bf16 v[172:175], v[180:183], a[216:219], v[172:175]\n"
"s_waitcnt lgkmcnt(2)\n"
"s_and_b32 s70, s88, 15\n"
"v_mfma_f32_16x16x32_bf16 v[128:131], v[96:99], a[48:51], v[128:131]\n"
"s_lshr_b32 s88, s88, 4\n"
"ds_read_b64_tr_b16 v[212:213], v234 offset:49152\n"
"v_subrev_f32 v156, s22, v156\n"
"v_mfma_f32_16x16x32_bf16 v[128:131], v[100:103], a[52:55], v[128:131]\n"
"s_lshl_b32 s70, s70, 7\n"
"ds_read_b64_tr_b16 v[192:193], v234 offset:40960\n"
"v_mov_b64 v[22:23], s[22:23]\n"
"v_mfma_f32_16x16x32_bf16 v[128:131], v[104:107], a[56:59], v[128:131]\n"
"s_xor_b32 s71, s71, s75\n"
"ds_read_b64_tr_b16 v[218:219], v230 offset:59392\n"
"v_mfma_f32_16x16x32_bf16 v[128:131], v[108:111], a[60:63], v[128:131]\n"
"v_lshlrev_b32_e64 v201, 2, s72\n"
"ds_read_b64_tr_b16 v[184:185], v234 offset:32768\n"
"v_lshl_add_u32 v254, v255, 4, v254\n"
"v_mfma_f32_16x16x32_bf16 v[128:131], v[112:115], a[0:3], v[128:131]\n"
"global_load_dword v200, v201, s[6:7] sc1\n"
"v_mfma_f32_16x16x32_bf16 v[128:131], v[116:119], a[4:7], v[128:131]\n"
"s_waitcnt lgkmcnt(4)\n"
"v_subrev_f32 v172, s22, v172\n"
"v_xor_b32 v229, s75, v229\n"
"v_mfma_f32_16x16x32_bf16 v[128:131], v[120:123], a[8:11], v[128:131]\n"
"v_subrev_f32 v143, s22, v143\n"
"v_mfma_f32_16x16x32_bf16 v[128:131], v[224:227], a[12:15], v[128:131]\n"
"ds_read_b64_tr_b16 v[182:183], v230 offset:34816\n"
"v_cvt_pk_bf16_f32 v140, v140, v141\n"
"v_mfma_f32_16x16x32_bf16 v[144:147], v[224:227], a[28:31], v[144:147]\n"
"ds_read_b64_tr_b16 v[216:217], v230 offset:57344\n"
"v_subrev_f32 v142, s22, v142\n"
"v_mfma_f32_16x16x32_bf16 v[144:147], v[120:123], a[24:27], v[144:147]\n"
"v_xor_b32 v248, s75, v248\n"
"v_mfma_f32_16x16x32_bf16 v[144:147], v[116:119], a[20:23], v[144:147]\n"
"ds_read_b64_tr_b16 v[190:191], v230 offset:43008\n"
"v_subrev_f32 v129, s22, v129\n"
"v_mfma_f32_16x16x32_bf16 v[144:147], v[112:115], a[16:19], v[144:147]\n"
"v_subrev_f32 v131, s22, v131\n"
"v_mfma_f32_16x16x32_bf16 v[144:147], v[108:111], a[76:79], v[144:147]\n"
"ds_read_b64_tr_b16 v[186:187], v234 offset:34816\n"
"v_subrev_f32 v157, s22, v157\n"
"v_mfma_f32_16x16x32_bf16 v[144:147], v[104:107], a[72:75], v[144:147]\n"
"v_mov_b64 v[0:1], s[22:23]\n"
"v_mov_b64 v[28:29], s[22:23]\n"
"v_mfma_f32_16x16x32_bf16 v[144:147], v[100:103], a[68:71], v[144:147]\n"
"ds_read_b64_tr_b16 v[194:195], v234 offset:43008\n"
"v_cvt_pk_bf16_f32 v125, v126, v127\n"
"v_mfma_f32_16x16x32_bf16 v[144:147], v[96:99], a[64:67], v[144:147]\n"
"v_cvt_pk_bf16_f32 v141, v142, v143\n"
"v_subrev_f32 v130, s22, v130\n"
"v_mfma_f32_16x16x32_bf16 v[160:163], v[96:99], a[80:83], v[160:163]\n"
"ds_read_b64_tr_b16 v[220:221], v234 offset:57344\n"
"v_mov_b64 v[6:7], s[22:23]\n"
"v_mfma_f32_16x16x32_bf16 v[160:163], v[100:103], a[84:87], v[160:163]\n"
"v_subrev_f32 v128, s22, v128\n"
"v_cvt_pk_bf16_f32 v126, v128, v129\n"
"v_mfma_f32_16x16x32_bf16 v[160:163], v[104:107], a[88:91], v[160:163]\n"
"ds_read_b64_tr_b16 v[188:189], v230 offset:40960\n"
"v_subrev_f32 v173, s22, v173\n"
"v_mfma_f32_16x16x32_bf16 v[160:163], v[108:111], a[92:95], v[160:163]\n"
"v_mov_b64 v[246:247], 0\n"
"v_subrev_f32 v159, s22, v159\n"
"v_mfma_f32_16x16x32_bf16 v[160:163], v[112:115], a[32:35], v[160:163]\n"
"ds_read_b64_tr_b16 v[180:181], v230 offset:32768\n"
"v_mfma_f32_16x16x32_bf16 v[160:163], v[116:119], a[36:39], v[160:163]\n"
"v_lshl_add_u32 v243, s44, 2, v243\n"
"v_mov_b64 v[90:91], 0\n"
"v_mfma_f32_16x16x32_bf16 v[160:163], v[120:123], a[40:43], v[160:163]\n"
"ds_read_b64_tr_b16 v[208:209], v230 offset:49152\n"
"v_cvt_pk_bf16_f32 v127, v130, v131\n"
"v_mfma_f32_16x16x32_bf16 v[160:163], v[224:227], a[44:47], v[160:163]\n"
"ds_read_b64_tr_b16 v[222:223], v234 offset:59392\n"
"buffer_store_dwordx4 v[124:127], v243, s[32:35], s16 offen sc0 sc1\n"
"v_mfma_f32_16x16x32_bf16 v[176:179], v[224:227], a[228:231], v[176:179]\n"
"v_subrev_f32 v145, s22, v145\n"
"v_subrev_f32 v146, s22, v146\n"
"v_mfma_f32_16x16x32_bf16 v[176:179], v[120:123], a[224:227], v[176:179]\n"
"ds_read_b64_tr_b16 v[210:211], v230 offset:51200\n"
"v_mfma_f32_16x16x32_bf16 v[176:179], v[116:119], a[212:215], v[176:179]\n"
"v_subrev_f32 v147, s22, v147\n"
"v_mfma_f32_16x16x32_bf16 v[176:179], v[112:115], a[208:211], v[176:179]\n"
"ds_read_b64_tr_b16 v[214:215], v234 offset:51200\n"
"v_xor_b32 v232, s75, v232\n"
"v_mfma_f32_16x16x32_bf16 v[176:179], v[108:111], a[236:239], v[176:179]\n"
"v_mov_b64 v[8:9], s[22:23]\n"
"v_cvt_pk_bf16_f32 v156, v156, v157\n"
"v_mfma_f32_16x16x32_bf16 v[176:179], v[104:107], a[232:235], v[176:179]\n"
"ds_read_b64_tr_b16 v[108:109], v235 offset:40960\n"
"v_subrev_f32 v174, s22, v174\n"
"v_mfma_f32_16x16x32_bf16 v[176:179], v[100:103], a[220:223], v[176:179]\n"
"v_mov_b64 v[16:17], s[22:23]\n"
"v_mov_b64 v[44:45], s[22:23]\n"
"v_mfma_f32_16x16x32_bf16 v[176:179], v[96:99], a[216:219], v[176:179]\n"
"s_waitcnt lgkmcnt(3)\n"
"ds_read_b64_tr_b16 v[122:123], v231 offset:59392\n"
"v_cvt_pk_bf16_f32 v143, v146, v147\n"
"v_mfma_f32_16x16x32_bf16 v[132:135], v[180:183], a[48:51], v[132:135]\n"
"v_subrev_f32 v144, s22, v144\n"
"v_subrev_f32 v160, s22, v160\n"
"v_mfma_f32_16x16x32_bf16 v[132:135], v[184:187], a[52:55], v[132:135]\n"
"ds_read_b64_tr_b16 v[100:101], v235 offset:32768\n"
"v_mov_b64 v[20:21], s[22:23]\n"
"v_mfma_f32_16x16x32_bf16 v[132:135], v[188:191], a[56:59], v[132:135]\n"
"v_mov_b64 v[30:31], s[22:23]\n"
"v_cvt_pk_bf16_f32 v142, v144, v145\n"
"v_mfma_f32_16x16x32_bf16 v[132:135], v[192:195], a[60:63], v[132:135]\n"
"s_waitcnt lgkmcnt(3)\n"
"ds_read_b64_tr_b16 v[106:107], v231 offset:43008\n"
"v_mov_b64 v[244:245], 0\n"
"v_mfma_f32_16x16x32_bf16 v[132:135], v[208:211], a[0:3], v[132:135]\n"
"ds_read_b64_tr_b16 v[226:227], v235 offset:59392\n"
"v_subrev_f32 v161, s22, v161\n"
"v_mfma_f32_16x16x32_bf16 v[132:135], v[212:215], a[4:7], v[132:135]\n"
"ds_read_b64_tr_b16 v[120:121], v231 offset:57344\n"
"v_subrev_f32 v158, s22, v158\n"
"v_mfma_f32_16x16x32_bf16 v[132:135], v[216:219], a[8:11], v[132:135]\n"
"ds_read_b64_tr_b16 v[114:115], v231 offset:51200\n"
"v_mov_b64 v[92:93], 0\n"
"v_mfma_f32_16x16x32_bf16 v[132:135], v[220:223], a[12:15], v[132:135]\n"
"buffer_store_dwordx4 v[140:143], v243, s[32:35], s16 offen offset:2048 sc0 sc1\n"
"ds_read_b64_tr_b16 v[98:99], v231 offset:34816\n"
"v_mfma_f32_16x16x32_bf16 v[148:151], v[220:223], a[28:31], v[148:151]\n"
"v_subrev_f32 v163, s22, v163\n"
"v_subrev_f32 v178, s22, v178\n"
"v_mfma_f32_16x16x32_bf16 v[148:151], v[216:219], a[24:27], v[148:151]\n"
"ds_read_b64_tr_b16 v[112:113], v231 offset:49152\n"
"v_mfma_f32_16x16x32_bf16 v[148:151], v[212:215], a[20:23], v[148:151]\n"
"v_subrev_f32 v175, s22, v175\n"
"v_subrev_f32 v177, s22, v177\n"
"v_mfma_f32_16x16x32_bf16 v[148:151], v[208:211], a[16:19], v[148:151]\n"
"ds_read_b64_tr_b16 v[110:111], v235 offset:43008\n"
"v_xor_b32 v228, s75, v228\n"
"v_mfma_f32_16x16x32_bf16 v[148:151], v[192:195], a[76:79], v[148:151]\n"
"ds_read_b64_tr_b16 v[104:105], v231 offset:40960\n"
"v_subrev_f32 v162, s22, v162\n"
"v_mfma_f32_16x16x32_bf16 v[148:151], v[188:191], a[72:75], v[148:151]\n"
"v_max_u32 v237, 1, v237\n"
"v_cvt_pk_bf16_f32 v172, v172, v173\n"
"v_mfma_f32_16x16x32_bf16 v[148:151], v[184:187], a[68:71], v[148:151]\n"
"ds_read_b64_tr_b16 v[96:97], v231 offset:32768\n"
"v_subrev_f32 v135, s22, v135\n"
"v_mfma_f32_16x16x32_bf16 v[148:151], v[180:183], a[64:67], v[148:151]\n"
"v_mov_b64 v[48:49], s[22:23]\n"
"v_cvt_pk_bf16_f32 v157, v158, v159\n"
"v_mfma_f32_16x16x32_bf16 v[164:167], v[180:183], a[80:83], v[164:167]\n"
"ds_read_b64_tr_b16 v[102:103], v235 offset:34816\n"
"v_mov_b64 v[50:51], s[22:23]\n"
"v_mfma_f32_16x16x32_bf16 v[164:167], v[184:187], a[84:87], v[164:167]\n"
"v_add_u32 v238, s97, v254\n"
"v_xor_b32 v233, s75, v233\n"
"v_mfma_f32_16x16x32_bf16 v[164:167], v[188:191], a[88:91], v[164:167]\n"
"ds_read_b64_tr_b16 v[118:119], v235 offset:51200\n"
"v_mov_b64 v[34:35], s[22:23]\n"
"v_mfma_f32_16x16x32_bf16 v[164:167], v[192:195], a[92:95], v[164:167]\n"
"ds_read_b64_tr_b16 v[116:117], v235 offset:49152\n"
"v_xor_b32 v237, s61, v237\n"
"v_mfma_f32_16x16x32_bf16 v[164:167], v[208:211], a[32:35], v[164:167]\n"
"v_cvt_pk_bf16_f32 v158, v160, v161\n"
"v_subrev_f32 v176, s22, v176\n"
"v_mfma_f32_16x16x32_bf16 v[164:167], v[212:215], a[36:39], v[164:167]\n"
"v_mov_b64 v[54:55], s[22:23]\n"
"v_cvt_pk_bf16_f32 v159, v162, v163\n"
"v_mfma_f32_16x16x32_bf16 v[164:167], v[216:219], a[40:43], v[164:167]\n"
"ds_read_b64_tr_b16 v[224:225], v235 offset:57344\n"
"v_add_u32 v252, s98, v254\n"
"v_mfma_f32_16x16x32_bf16 v[164:167], v[220:223], a[44:47], v[164:167]\n"
"s_barrier\n"
"s_waitcnt lgkmcnt(4)\n"
"v_mfma_f32_16x16x32_bf16 v[136:139], v[96:99], a[48:51], v[136:139]\n"
"s_waitcnt lgkmcnt(3)\n"
"ds_read_b64_tr_b16 v[184:185], v232 offset:0\n"
"v_cvt_pk_bf16_f32 v173, v174, v175\n"
"v_mfma_f32_16x16x32_bf16 v[136:139], v[100:103], a[52:55], v[136:139]\n"
"ds_read_b64_tr_b16 v[212:213], v232 offset:16384\n"
"v_lshlrev_b32 v237, 2, v237\n"
"v_mfma_f32_16x16x32_bf16 v[136:139], v[104:107], a[56:59], v[136:139]\n"
"ds_read_b64_tr_b16 v[194:195], v232 offset:10240\n"
"v_mov_b32 v241, s60\n"
"v_mfma_f32_16x16x32_bf16 v[136:139], v[108:111], a[60:63], v[136:139]\n"
"ds_read_b64_tr_b16 v[188:189], v228 offset:8192\n"
"v_mov_b64 v[4:5], s[22:23]\n"
"v_mfma_f32_16x16x32_bf16 v[136:139], v[112:115], a[0:3], v[136:139]\n"
"s_waitcnt lgkmcnt(5)\n"
"ds_read_b64_tr_b16 v[218:219], v228 offset:26624\n"
"v_subrev_f32 v133, s22, v133\n"
"v_mfma_f32_16x16x32_bf16 v[136:139], v[116:119], a[4:7], v[136:139]\n"
"ds_read_b64_tr_b16 v[192:193], v232 offset:8192\n"
"v_mov_b64 v[88:89], 0\n"
"v_mfma_f32_16x16x32_bf16 v[136:139], v[120:123], a[8:11], v[136:139]\n"
"s_waitcnt lgkmcnt(6)\n"
"ds_read_b64_tr_b16 v[214:215], v232 offset:18432\n"
"v_lshl_add_u32 v248, v242, 8, s93\n"
"v_mfma_f32_16x16x32_bf16 v[136:139], v[224:227], a[12:15], v[136:139]\n"
"buffer_store_dwordx4 v[156:159], v243, s[32:35], s17 offen sc0 sc1\n"
"ds_read_b64_tr_b16 v[210:211], v228 offset:18432\n"
"v_mfma_f32_16x16x32_bf16 v[152:155], v[224:227], a[28:31], v[152:155]\n"
"ds_read_b64_tr_b16 v[208:209], v228 offset:16384\n"
"v_mov_b64 v[32:33], s[22:23]\n"
"v_mfma_f32_16x16x32_bf16 v[152:155], v[120:123], a[24:27], v[152:155]\n"
"ds_read_b64_tr_b16 v[222:223], v232 offset:26624\n"
"v_subrev_f32 v179, s22, v179\n"
"v_mfma_f32_16x16x32_bf16 v[152:155], v[116:119], a[20:23], v[152:155]\n"
"ds_read_b64_tr_b16 v[220:221], v232 offset:24576\n"
"v_cvt_pk_bf16_f32 v174, v176, v177\n"
"v_mfma_f32_16x16x32_bf16 v[152:155], v[112:115], a[16:19], v[152:155]\n"
"ds_read_b64_tr_b16 v[216:217], v228 offset:24576\n"
"v_subrev_f32 v134, s22, v134\n"
"v_mfma_f32_16x16x32_bf16 v[152:155], v[108:111], a[76:79], v[152:155]\n"
"ds_read_b64_tr_b16 v[186:187], v232 offset:2048\n"
"v_mfma_f32_16x16x32_bf16 v[152:155], v[104:107], a[72:75], v[152:155]\n"
"ds_read_b64_tr_b16 v[182:183], v228 offset:2048\n"
"v_mov_b64 v[46:47], s[22:23]\n"
"v_mfma_f32_16x16x32_bf16 v[152:155], v[100:103], a[68:71], v[152:155]\n"
"ds_read_b64_tr_b16 v[180:181], v228 offset:0\n"
"v_mov_b64 v[38:39], s[22:23]\n"
"v_mfma_f32_16x16x32_bf16 v[152:155], v[96:99], a[64:67], v[152:155]\n"
"ds_read_b64_tr_b16 v[190:191], v228 offset:10240\n"
"v_add_u32 v241, 1, v241\n"
"v_mfma_f32_16x16x32_bf16 v[168:171], v[96:99], a[80:83], v[168:171]\n"
"ds_read_b64_tr_b16 v[96:97], v229 offset:0\n"
"ds_read_b64_tr_b16 v[98:99], v229 offset:2048\n"
"v_mfma_f32_16x16x32_bf16 v[168:171], v[100:103], a[84:87], v[168:171]\n"
"ds_read_b64_tr_b16 v[100:101], v233 offset:0\n"
"ds_read_b64_tr_b16 v[102:103], v233 offset:2048\n"
"v_mfma_f32_16x16x32_bf16 v[168:171], v[104:107], a[88:91], v[168:171]\n"
"ds_read_b64_tr_b16 v[104:105], v229 offset:8192\n"
"v_cvt_pk_bf16_f32 v175, v178, v179\n"
"v_mfma_f32_16x16x32_bf16 v[168:171], v[108:111], a[92:95], v[168:171]\n"
"v_add_u32 v253, s99, v254\n"
"v_mov_b64 v[26:27], s[22:23]\n"
"v_mfma_f32_16x16x32_bf16 v[168:171], v[112:115], a[32:35], v[168:171]\n"
"v_xor_b32 v239, 64, v238\n"
"v_mov_b64 v[84:85], 0\n"
"v_mfma_f32_16x16x32_bf16 v[168:171], v[116:119], a[36:39], v[168:171]\n"
"v_subrev_f32 v132, s22, v132\n"
"v_cvt_pk_bf16_f32 v132, v132, v133\n"
"v_mfma_f32_16x16x32_bf16 v[168:171], v[120:123], a[40:43], v[168:171]\n"
"s_add_u32 s47, s47, 1\n"
"v_mfma_f32_16x16x32_bf16 v[168:171], v[224:227], a[44:47], v[168:171]\n"
"s_waitcnt lgkmcnt(5)\n"
"LGL_%=:\n"
"v_mfma_f32_16x16x32_bf16 v[124:127], v[180:183], a[96:99], v[196:199]\n"
"ds_read_b64_tr_b16 v[106:107], v229 offset:10240\n"
"v_cvt_pk_bf16_f32 v133, v134, v135\n"
"v_mfma_f32_16x16x32_bf16 v[124:127], v[184:187], a[100:103], v[124:127]\n"
"ds_read_b64_tr_b16 v[108:109], v233 offset:8192\n"
"v_subrev_f32 v136, s22, v136\n"
"v_mfma_f32_16x16x32_bf16 v[124:127], v[188:191], a[104:107], v[124:127]\n"
"ds_read_b64_tr_b16 v[110:111], v233 offset:10240\n"
"v_subrev_f32 v137, s22, v137\n"
"v_mfma_f32_16x16x32_bf16 v[124:127], v[192:195], a[108:111], v[124:127]\n"
"ds_read_b64_tr_b16 v[112:113], v229 offset:16384\n"
"v_subrev_f32 v138, s22, v138\n"
"v_mfma_f32_16x16x32_bf16 v[124:127], v[208:211], a[144:147], v[124:127]\n"
"ds_read_b64_tr_b16 v[114:115], v229 offset:18432\n"
"v_subrev_f32 v139, s22, v139\n"
"v_mfma_f32_16x16x32_bf16 v[124:127], v[212:215], a[148:151], v[124:127]\n"
"s_waitcnt lgkmcnt(7)\n"
"ds_read_b64_tr_b16 v[116:117], v233 offset:16384\n"
"v_cvt_pk_bf16_f32 v134, v136, v137\n"
"v_mfma_f32_16x16x32_bf16 v[124:127], v[216:219], a[152:155], v[124:127]\n"
"ds_read_b64_tr_b16 v[118:119], v233 offset:18432\n"
"v_cvt_pk_bf16_f32 v135, v138, v139\n"
"v_mfma_f32_16x16x32_bf16 v[124:127], v[220:223], a[156:159], v[124:127]\n"
"buffer_store_dwordx4 v[172:175], v243, s[32:35], s17 offen offset:2048 sc0 sc1\n"
"v_subrev_f32 v148, s22, v148\n"
"v_mfma_f32_16x16x32_bf16 v[140:143], v[220:223], a[172:175], v[196:199]\n"
"ds_read_b64_tr_b16 v[120:121], v229 offset:24576\n"
"v_subrev_f32 v149, s22, v149\n"
"v_mfma_f32_16x16x32_bf16 v[140:143], v[216:219], a[168:171], v[140:143]\n"
"ds_read_b64_tr_b16 v[122:123], v229 offset:26624\n"
"v_subrev_f32 v150, s22, v150\n"
"v_mfma_f32_16x16x32_bf16 v[140:143], v[212:215], a[164:167], v[140:143]\n"
"ds_read_b64_tr_b16 v[224:225], v233 offset:24576\n"
"v_subrev_f32 v151, s22, v151\n"
"v_mfma_f32_16x16x32_bf16 v[140:143], v[208:211], a[160:163], v[140:143]\n"
"ds_read_b64_tr_b16 v[226:227], v233 offset:26624\n"
"v_cvt_pk_bf16_f32 v148, v148, v149\n"
"v_mfma_f32_16x16x32_bf16 v[140:143], v[192:195], a[124:127], v[140:143]\n"
"v_cvt_pk_bf16_f32 v149, v150, v151\n"
"v_subrev_f32 v152, s22, v152\n"
"v_mfma_f32_16x16x32_bf16 v[140:143], v[188:191], a[120:123], v[140:143]\n"
"s_add_u32 s41, s70, 0\n"
"v_subrev_f32 v153, s22, v153\n"
"v_subrev_f32 v154, s22, v154\n"
"v_mfma_f32_16x16x32_bf16 v[140:143], v[184:187], a[116:119], v[140:143]\n"
"s_mov_b32 m0, s71\n"
"v_subrev_f32 v155, s22, v155\n"
"v_cvt_pk_bf16_f32 v150, v152, v153\n"
"v_mfma_f32_16x16x32_bf16 v[140:143], v[180:183], a[112:115], v[140:143]\n"
"s_waitcnt vmcnt(14)\n"
"buffer_load_dwordx4 v240, s[28:31], s41 offen lds\n"
"v_mfma_f32_16x16x32_bf16 v[156:159], v[180:183], a[128:131], v[196:199]\n"
"v_xor_b32 v230, s75, v230\n"
"v_cvt_pk_bf16_f32 v151, v154, v155\n"
"v_mfma_f32_16x16x32_bf16 v[156:159], v[184:187], a[132:135], v[156:159]\n"
"v_xor_b32 v234, s75, v234\n"
"v_subrev_f32 v164, s22, v164\n"
"v_mfma_f32_16x16x32_bf16 v[156:159], v[188:191], a[136:139], v[156:159]\n"
"v_subrev_f32 v165, s22, v165\n"
"v_subrev_f32 v166, s22, v166\n"
"v_mfma_f32_16x16x32_bf16 v[156:159], v[192:195], a[140:143], v[156:159]\n"
"v_subrev_f32 v167, s22, v167\n"
"v_cvt_pk_bf16_f32 v164, v164, v165\n"
"v_mfma_f32_16x16x32_bf16 v[156:159], v[208:211], a[176:179], v[156:159]\n"
"v_cvt_pk_bf16_f32 v165, v166, v167\n"
"v_subrev_f32 v168, s22, v168\n"
"v_mfma_f32_16x16x32_bf16 v[156:159], v[212:215], a[180:183], v[156:159]\n"
"v_subrev_f32 v169, s22, v169\n"
"v_subrev_f32 v170, s22, v170\n"
"v_mfma_f32_16x16x32_bf16 v[156:159], v[216:219], a[184:187], v[156:159]\n"
"v_subrev_f32 v171, s22, v171\n"
"v_cvt_pk_bf16_f32 v166, v168, v169\n"
"v_mfma_f32_16x16x32_bf16 v[156:159], v[220:223], a[188:191], v[156:159]\n"
"buffer_store_dwordx4 v[132:135], v243, s[32:35], s16 offen offset:1024 sc0 sc1\n"
"v_cvt_pk_bf16_f32 v167, v170, v171\n"
"v_mfma_f32_16x16x32_bf16 v[172:175], v[220:223], a[252:255], v[196:199]\n"
"ds_read_b64_tr_b16 v[220:221], v234 offset:24576\n"
"v_xor_b32 v231, s75, v231\n"
"v_mfma_f32_16x16x32_bf16 v[172:175], v[216:219], a[248:251], v[172:175]\n"
"ds_read_b64_tr_b16 v[216:217], v230 offset:24576\n"
"v_xor_b32 v235, s75, v235\n"
"v_mfma_f32_16x16x32_bf16 v[172:175], v[212:215], a[244:247], v[172:175]\n"
"ds_read_b64_tr_b16 v[212:213], v234 offset:16384\n"
"v_lshlrev_b32 v202, 16, v84\n"
"v_mfma_f32_16x16x32_bf16 v[172:175], v[208:211], a[240:243], v[172:175]\n"
"ds_read_b64_tr_b16 v[208:209], v230 offset:16384\n"
"v_and_b32 v84, s73, v84\n"
"v_mfma_f32_16x16x32_bf16 v[172:175], v[192:195], a[204:207], v[172:175]\n"
"ds_read_b64_tr_b16 v[192:193], v234 offset:8192\n"
"v_add_f32 v25, v25, v84\n"
"v_mfma_f32_16x16x32_bf16 v[172:175], v[188:191], a[200:203], v[172:175]\n"
"ds_read_b64_tr_b16 v[188:189], v230 offset:8192\n"
"v_lshlrev_b32 v203, 16, v85\n"
"v_mfma_f32_16x16x32_bf16 v[172:175], v[184:187], a[196:199], v[172:175]\n"
"s_add_u32 s40, s41, s52\n"
"ds_read_b64_tr_b16 v[184:185], v234 offset:0\n"
"v_and_b32 v85, s73, v85\n"
"v_mfma_f32_16x16x32_bf16 v[172:175], v[180:183], a[192:195], v[172:175]\n"
"s_waitcnt vmcnt(13) lgkmcnt(7)\n"
"buffer_load_dwordx4 v240, s[28:31], s40 offen offset:1024 lds\n"
"v_mfma_f32_16x16x32_bf16 v[128:131], v[96:99], a[96:99], v[196:199]\n"
"s_waitcnt lgkmcnt(6)\n"
"ds_read_b64_tr_b16 v[180:181], v230 offset:0\n"
"v_add_f32 v27, v27, v85\n"
"v_mfma_f32_16x16x32_bf16 v[128:131], v[100:103], a[100:103], v[128:131]\n"
"ds_read_b64_tr_b16 v[182:183], v230 offset:2048\n"
"v_lshlrev_b32 v204, 16, v86\n"
"v_mfma_f32_16x16x32_bf16 v[128:131], v[104:107], a[104:107], v[128:131]\n"
"ds_read_b64_tr_b16 v[186:187], v234 offset:2048\n"
"v_and_b32 v86, s73, v86\n"
"v_mfma_f32_16x16x32_bf16 v[128:131], v[108:111], a[108:111], v[128:131]\n"
"ds_read_b64_tr_b16 v[190:191], v230 offset:10240\n"
"v_add_f32 v29, v29, v86\n"
"v_mfma_f32_16x16x32_bf16 v[128:131], v[112:115], a[144:147], v[128:131]\n"
"s_waitcnt vmcnt(6)\n"
"ds_read_b64_tr_b16 v[194:195], v234 offset:10240\n"
"v_lshlrev_b32 v205, 16, v87\n"
"v_mfma_f32_16x16x32_bf16 v[128:131], v[116:119], a[148:151], v[128:131]\n"
"v_subrev_u32 v200, s21, v200\n"
"ds_read_b64_tr_b16 v[210:211], v230 offset:18432\n"
"v_mfma_f32_16x16x32_bf16 v[128:131], v[120:123], a[152:155], v[128:131]\n"
"v_cmp_lt_u32 vcc, s69, v200\n"
"ds_read_b64_tr_b16 v[214:215], v234 offset:18432\n"
"v_mfma_f32_16x16x32_bf16 v[128:131], v[224:227], a[156:159], v[128:131]\n"
"ds_read_b64_tr_b16 v[218:219], v230 offset:26624\n"
"s_cbranch_vccnz LGP_%=\nLGPK_%=:\n"
"v_mfma_f32_16x16x32_bf16 v[144:147], v[224:227], a[172:175], v[196:199]\n"
"s_sub_u32 s72, s72, 1\n"
"ds_read_b64_tr_b16 v[222:223], v234 offset:26624\n"
"v_and_b32 v87, s73, v87\n"
"v_mfma_f32_16x16x32_bf16 v[144:147], v[120:123], a[168:171], v[144:147]\n"
"s_and_b32 s72, s72, 7\n"
"v_add_f32 v24, v24, v202\n"
"v_add_f32 v31, v31, v87\n"
"v_mfma_f32_16x16x32_bf16 v[144:147], v[116:119], a[164:167], v[144:147]\n"
"v_add_f32 v26, v26, v203\n"
"v_lshlrev_b32 v206, 16, v88\n"
"v_mfma_f32_16x16x32_bf16 v[144:147], v[112:115], a[160:163], v[144:147]\n"
"buffer_load_dwordx4 v[72:75], v243, s[32:35], s18 offen sc1\n"
"v_mfma_f32_16x16x32_bf16 v[144:147], v[108:111], a[124:127], v[144:147]\n"
"v_and_b32 v88, s73, v88\n"
"v_add_f32 v33, v33, v88\n"
"v_mfma_f32_16x16x32_bf16 v[144:147], v[104:107], a[120:123], v[144:147]\n"
"v_lshlrev_b32 v207, 16, v89\n"
"v_and_b32 v89, s73, v89\n"
"v_mfma_f32_16x16x32_bf16 v[144:147], v[100:103], a[116:119], v[144:147]\n"
"v_add_f32 v35, v35, v89\n"
"v_lshlrev_b32 v202, 16, v90\n"
"v_mfma_f32_16x16x32_bf16 v[144:147], v[96:99], a[112:115], v[144:147]\n"
"buffer_store_dwordx4 v[148:151], v243, s[32:35], s16 offen offset:3072 sc0 sc1\n"
"v_and_b32 v90, s73, v90\n"
"v_mfma_f32_16x16x32_bf16 v[160:163], v[96:99], a[128:131], v[196:199]\n"
"v_add_f32 v37, v37, v90\n"
"v_lshlrev_b32 v203, 16, v91\n"
"v_mfma_f32_16x16x32_bf16 v[160:163], v[100:103], a[132:135], v[160:163]\n"
"v_and_b32 v91, s73, v91\n"
"v_add_f32 v39, v39, v91\n"
"v_mfma_f32_16x16x32_bf16 v[160:163], v[104:107], a[136:139], v[160:163]\n"
"v_add_f32 v28, v28, v204\n"
"v_add_f32 v30, v30, v205\n"
"v_mfma_f32_16x16x32_bf16 v[160:163], v[108:111], a[140:143], v[160:163]\n"
"v_add_f32 v32, v32, v206\n"
"v_add_f32 v34, v34, v207\n"
"v_mfma_f32_16x16x32_bf16 v[160:163], v[112:115], a[176:179], v[160:163]\n"
"v_lshlrev_b32 v204, 16, v92\n"
"v_and_b32 v92, s73, v92\n"
"v_mfma_f32_16x16x32_bf16 v[160:163], v[116:119], a[180:183], v[160:163]\n"
"v_add_f32 v41, v41, v92\n"
"v_lshlrev_b32 v205, 16, v93\n"
"v_mfma_f32_16x16x32_bf16 v[160:163], v[120:123], a[184:187], v[160:163]\n"
"s_add_u32 s40, s41, s53\n"
"v_and_b32 v93, s73, v93\n"
"v_add_f32 v43, v43, v93\n"
"v_mfma_f32_16x16x32_bf16 v[160:163], v[224:227], a[188:191], v[160:163]\n"
"buffer_load_dwordx4 v240, s[28:31], s40 offen offset:2048 lds\n"
"v_mfma_f32_16x16x32_bf16 v[176:179], v[224:227], a[252:255], v[196:199]\n"
"ds_read_b64_tr_b16 v[224:225], v235 offset:24576\n"
"v_lshlrev_b32 v206, 16, v94\n"
"v_mfma_f32_16x16x32_bf16 v[176:179], v[120:123], a[248:251], v[176:179]\n"
"ds_read_b64_tr_b16 v[120:121], v231 offset:24576\n"
"v_and_b32 v94, s73, v94\n"
"v_mfma_f32_16x16x32_bf16 v[176:179], v[116:119], a[244:247], v[176:179]\n"
"ds_read_b64_tr_b16 v[116:117], v235 offset:16384\n"
"v_add_f32 v45, v45, v94\n"
"v_mfma_f32_16x16x32_bf16 v[176:179], v[112:115], a[240:243], v[176:179]\n"
"buffer_load_dwordx4 v[76:79], v243, s[32:35], s18 offen offset:1024 sc1\n"
"v_mfma_f32_16x16x32_bf16 v[176:179], v[108:111], a[204:207], v[176:179]\n"
"ds_read_b64_tr_b16 v[108:109], v235 offset:8192\n"
"v_lshlrev_b32 v207, 16, v95\n"
"v_mfma_f32_16x16x32_bf16 v[176:179], v[104:107], a[200:203], v[176:179]\n"
"ds_read_b64_tr_b16 v[104:105], v231 offset:8192\n"
"v_and_b32 v95, s73, v95\n"
"v_mfma_f32_16x16x32_bf16 v[176:179], v[100:103], a[196:199], v[176:179]\n"
"s_add_u32 s40, s41, s54\n"
"ds_read_b64_tr_b16 v[100:101], v235 offset:0\n"
"v_add_f32 v47, v47, v95\n"
"v_mfma_f32_16x16x32_bf16 v[176:179], v[96:99], a[192:195], v[176:179]\n"
"s_waitcnt lgkmcnt(6)\n"
"buffer_load_dwordx4 v240, s[28:31], s40 offen offset:3072 lds\n"
"v_mfma_f32_16x16x32_bf16 v[132:135], v[180:183], a[96:99], v[196:199]\n"
"ds_read_b64_tr_b16 v[96:97], v231 offset:0\n"
"v_add_f32 v36, v36, v202\n"
"v_mfma_f32_16x16x32_bf16 v[132:135], v[184:187], a[100:103], v[132:135]\n"
"ds_read_b64_tr_b16 v[98:99], v231 offset:2048\n"
"v_add_f32 v38, v38, v203\n"
"v_mfma_f32_16x16x32_bf16 v[132:135], v[188:191], a[104:107], v[132:135]\n"
"ds_read_b64_tr_b16 v[102:103], v235 offset:2048\n"
"v_add_f32 v44, v44, v206\n"
"v_mfma_f32_16x16x32_bf16 v[132:135], v[192:195], a[108:111], v[132:135]\n"
"ds_read_b64_tr_b16 v[106:107], v231 offset:10240\n"
"v_add_f32 v46, v46, v207\n"
"v_mfma_f32_16x16x32_bf16 v[132:135], v[208:211], a[144:147], v[132:135]\n"
"ds_read_b64_tr_b16 v[110:111], v235 offset:10240\n"
"v_add_f32 v40, v40, v204\n"
"v_mfma_f32_16x16x32_bf16 v[132:135], v[212:215], a[148:151], v[132:135]\n"
"ds_read_b64_tr_b16 v[112:113], v231 offset:16384\n"
"v_add_f32 v42, v42, v205\n"
"v_mfma_f32_16x16x32_bf16 v[132:135], v[216:219], a[152:155], v[132:135]\n"
"ds_read_b64_tr_b16 v[114:115], v231 offset:18432\n"
"v_mfma_f32_16x16x32_bf16 v[132:135], v[220:223], a[156:159], v[132:135]\n"
"buffer_store_dwordx4 v[164:167], v243, s[32:35], s17 offen offset:1024 sc0 sc1\n"
"v_lshlrev_b32 v206, 16, v244\n"
"v_mfma_f32_16x16x32_bf16 v[148:151], v[220:223], a[172:175], v[196:199]\n"
"ds_read_b64_tr_b16 v[118:119], v235 offset:18432\n"
"v_and_b32 v244, s73, v244\n"
"v_mfma_f32_16x16x32_bf16 v[148:151], v[216:219], a[168:171], v[148:151]\n"
"ds_read_b64_tr_b16 v[122:123], v231 offset:26624\n"
"v_add_f32 v49, v49, v244\n"
"v_mfma_f32_16x16x32_bf16 v[148:151], v[212:215], a[164:167], v[148:151]\n"
"ds_read_b64_tr_b16 v[226:227], v235 offset:26624\n"
"v_lshlrev_b32 v207, 16, v245\n"
"v_mfma_f32_16x16x32_bf16 v[148:151], v[208:211], a[160:163], v[148:151]\n"
"buffer_load_dwordx4 v[80:83], v243, s[32:35], s18 offen offset:2048 sc1\n"
"v_mfma_f32_16x16x32_bf16 v[148:151], v[192:195], a[124:127], v[148:151]\n"
"v_and_b32 v245, s73, v245\n"
"v_add_f32 v51, v51, v245\n"
"v_mfma_f32_16x16x32_bf16 v[148:151], v[188:191], a[120:123], v[148:151]\n"
"s_add_u32 s41, s70, 163840\n"
"v_lshlrev_b32 v202, 16, v246\n"
"v_and_b32 v246, s73, v246\n"
"v_mfma_f32_16x16x32_bf16 v[148:151], v[184:187], a[116:119], v[148:151]\n"
"s_add_u32 m0, s71, 16384\n"
"v_add_f32 v53, v53, v246\n"
"v_lshlrev_b32 v203, 16, v247\n"
"v_mfma_f32_16x16x32_bf16 v[148:151], v[180:183], a[112:115], v[148:151]\n"
"buffer_load_dwordx4 v240, s[28:31], s41 offen lds\n"
"v_mfma_f32_16x16x32_bf16 v[164:167], v[180:183], a[128:131], v[196:199]\n"
"v_and_b32 v247, s73, v247\n"
"v_add_f32 v55, v55, v247\n"
"v_mfma_f32_16x16x32_bf16 v[164:167], v[184:187], a[132:135], v[164:167]\n"
"v_add_f32 v48, v48, v206\n"
"v_add_f32 v50, v50, v207\n"
"v_mfma_f32_16x16x32_bf16 v[164:167], v[188:191], a[136:139], v[164:167]\n"
"v_add_f32 v52, v52, v202\n"
"v_add_f32 v54, v54, v203\n"
"v_mfma_f32_16x16x32_bf16 v[164:167], v[192:195], a[140:143], v[164:167]\n"
"v_mfma_f32_16x16x32_bf16 v[164:167], v[208:211], a[176:179], v[164:167]\n"
"v_mfma_f32_16x16x32_bf16 v[164:167], v[212:215], a[180:183], v[164:167]\n"
"v_mfma_f32_16x16x32_bf16 v[164:167], v[216:219], a[184:187], v[164:167]\n"
"s_waitcnt vmcnt(12)\n"
"v_mfma_f32_16x16x32_bf16 v[164:167], v[220:223], a[188:191], v[164:167]\n"
"s_barrier\n"
"s_waitcnt lgkmcnt(0)\n"
"v_mfma_f32_16x16x32_bf16 v[136:139], v[96:99], a[96:99], v[196:199]\n"
"ds_read_b64_tr_b16 v[180:181], v228 offset:32768\n"
"v_mfma_f32_16x16x32_bf16 v[136:139], v[100:103], a[100:103], v[136:139]\n"
"ds_read_b64_tr_b16 v[182:183], v228 offset:34816\n"
"v_mfma_f32_16x16x32_bf16 v[136:139], v[104:107], a[104:107], v[136:139]\n"
"ds_read_b64_tr_b16 v[184:185], v232 offset:32768\n"
"v_mfma_f32_16x16x32_bf16 v[136:139], v[108:111], a[108:111], v[136:139]\n"
"ds_read_b64_tr_b16 v[186:187], v232 offset:34816\n"
"v_mfma_f32_16x16x32_bf16 v[136:139], v[112:115], a[144:147], v[136:139]\n"
"ds_read_b64_tr_b16 v[188:189], v228 offset:40960\n"
"v_mfma_f32_16x16x32_bf16 v[136:139], v[116:119], a[148:151], v[136:139]\n"
"ds_read_b64_tr_b16 v[190:191], v228 offset:43008\n"
"v_mfma_f32_16x16x32_bf16 v[136:139], v[120:123], a[152:155], v[136:139]\n"
"s_add_u32 s40, s41, s52\n"
"ds_read_b64_tr_b16 v[192:193], v232 offset:40960\n"
"v_mfma_f32_16x16x32_bf16 v[136:139], v[224:227], a[156:159], v[136:139]\n"
"buffer_load_dwordx4 v240, s[28:31], s40 offen offset:1024 lds\n"
"v_mfma_f32_16x16x32_bf16 v[152:155], v[224:227], a[172:175], v[196:199]\n"
"ds_read_b64_tr_b16 v[194:195], v232 offset:43008\n"
"v_mfma_f32_16x16x32_bf16 v[152:155], v[120:123], a[168:171], v[152:155]\n"
"ds_read_b64_tr_b16 v[208:209], v228 offset:49152\n"
"v_mfma_f32_16x16x32_bf16 v[152:155], v[116:119], a[164:167], v[152:155]\n"
"ds_read_b64_tr_b16 v[210:211], v228 offset:51200\n"
"v_mfma_f32_16x16x32_bf16 v[152:155], v[112:115], a[160:163], v[152:155]\n"
"ds_read_b64_tr_b16 v[212:213], v232 offset:49152\n"
"v_mfma_f32_16x16x32_bf16 v[152:155], v[108:111], a[124:127], v[152:155]\n"
"ds_read_b64_tr_b16 v[214:215], v232 offset:51200\n"
"v_mfma_f32_16x16x32_bf16 v[152:155], v[104:107], a[120:123], v[152:155]\n"
"ds_read_b64_tr_b16 v[216:217], v228 offset:57344\n"
"v_mfma_f32_16x16x32_bf16 v[152:155], v[100:103], a[116:119], v[152:155]\n"
"s_add_u32 s40, s41, s53\n"
"ds_read_b64_tr_b16 v[218:219], v228 offset:59392\n"
"v_xor_b32 v228, s75, v228\n"
"v_mfma_f32_16x16x32_bf16 v[152:155], v[96:99], a[112:115], v[152:155]\n"
"buffer_load_dwordx4 v240, s[28:31], s40 offen offset:2048 lds\n"
"v_mfma_f32_16x16x32_bf16 v[168:171], v[96:99], a[128:131], v[196:199]\n"
"ds_read_b64_tr_b16 v[220:221], v232 offset:57344\n"
"v_mfma_f32_16x16x32_bf16 v[168:171], v[100:103], a[132:135], v[168:171]\n"
"ds_read_b64_tr_b16 v[222:223], v232 offset:59392\n"
"v_xor_b32 v232, s75, v232\n"
"v_mfma_f32_16x16x32_bf16 v[168:171], v[104:107], a[136:139], v[168:171]\n"
"ds_read_b64_tr_b16 v[96:97], v229 offset:32768\n"
"v_mfma_f32_16x16x32_bf16 v[168:171], v[108:111], a[140:143], v[168:171]\n"
"ds_read_b64_tr_b16 v[98:99], v229 offset:34816\n"
"v_mfma_f32_16x16x32_bf16 v[168:171], v[112:115], a[176:179], v[168:171]\n"
"ds_read_b64_tr_b16 v[100:101], v233 offset:32768\n"
"v_mfma_f32_16x16x32_bf16 v[168:171], v[116:119], a[180:183], v[168:171]\n"
"ds_read_b64_tr_b16 v[102:103], v233 offset:34816\n"
"v_mfma_f32_16x16x32_bf16 v[168:171], v[120:123], a[184:187], v[168:171]\n"
"s_add_u32 s40, s41, s54\n"
"ds_read_b64_tr_b16 v[104:105], v229 offset:40960\n"
"v_mfma_f32_16x16x32_bf16 v[168:171], v[224:227], a[188:191], v[168:171]\n"
"s_waitcnt lgkmcnt(7)\n"
"buffer_load_dwordx4 v240, s[28:31], s40 offen offset:3072 lds\n"
"v_mfma_f32_16x16x32_bf16 v[124:127], v[180:183], a[48:51], v[124:127]\n"
"ds_read_b64_tr_b16 v[106:107], v229 offset:43008\n"
"v_mfma_f32_16x16x32_bf16 v[124:127], v[184:187], a[52:55], v[124:127]\n"
"ds_read_b64_tr_b16 v[108:109], v233 offset:40960\n"
"v_mfma_f32_16x16x32_bf16 v[124:127], v[188:191], a[56:59], v[124:127]\n"
"ds_read_b64_tr_b16 v[110:111], v233 offset:43008\n"
"v_mfma_f32_16x16x32_bf16 v[124:127], v[192:195], a[60:63], v[124:127]\n"
"ds_read_b64_tr_b16 v[112:113], v229 offset:49152\n"
"v_mfma_f32_16x16x32_bf16 v[124:127], v[208:211], a[0:3], v[124:127]\n"
"ds_read_b64_tr_b16 v[114:115], v229 offset:51200\n"
"v_mfma_f32_16x16x32_bf16 v[124:127], v[212:215], a[4:7], v[124:127]\n"
"s_add_u32 s41, s70, 327680\n"
"ds_read_b64_tr_b16 v[116:117], v233 offset:49152\n"
"v_mfma_f32_16x16x32_bf16 v[124:127], v[216:219], a[8:11], v[124:127]\n"
"s_waitcnt lgkmcnt(6)\n"
"s_add_u32 m0, s71, 32768\n"
"ds_read_b64_tr_b16 v[118:119], v233 offset:51200\n"
"v_mfma_f32_16x16x32_bf16 v[124:127], v[220:223], a[12:15], v[124:127]\n"
"buffer_load_dwordx4 v240, s[28:31], s41 offen lds\n"
"v_mfma_f32_16x16x32_bf16 v[140:143], v[220:223], a[28:31], v[140:143]\n"
"ds_read_b64_tr_b16 v[120:121], v229 offset:57344\n"
"v_mfma_f32_16x16x32_bf16 v[140:143], v[216:219], a[24:27], v[140:143]\n"
"ds_read_b64_tr_b16 v[122:123], v229 offset:59392\n"
"v_xor_b32 v229, s75, v229\n"
"v_mfma_f32_16x16x32_bf16 v[140:143], v[212:215], a[20:23], v[140:143]\n"
"ds_read_b64_tr_b16 v[224:225], v233 offset:57344\n"
"v_mfma_f32_16x16x32_bf16 v[140:143], v[208:211], a[16:19], v[140:143]\n"
"ds_read_b64_tr_b16 v[226:227], v233 offset:59392\n"
"v_xor_b32 v233, s75, v233\n"
"v_mfma_f32_16x16x32_bf16 v[140:143], v[192:195], a[76:79], v[140:143]\n"
"v_mfma_f32_16x16x32_bf16 v[140:143], v[188:191], a[72:75], v[140:143]\n"
"v_subrev_f32 v124, s22, v124\n"
"v_subrev_f32 v125, s22, v125\n"
"v_mfma_f32_16x16x32_bf16 v[140:143], v[184:187], a[68:71], v[140:143]\n"
"s_add_u32 s40, s41, s52\n"
"v_subrev_f32 v126, s22, v126\n"
"v_subrev_f32 v127, s22, v127\n"
"v_mfma_f32_16x16x32_bf16 v[140:143], v[180:183], a[64:67], v[140:143]\n"
"buffer_load_dwordx4 v240, s[28:31], s40 offen offset:1024 lds\n"
"v_mfma_f32_16x16x32_bf16 v[156:159], v[180:183], a[80:83], v[156:159]\n"
"v_cvt_pk_bf16_f32 v124, v124, v125\n"
"v_cvt_pk_bf16_f32 v125, v126, v127\n"
"v_mfma_f32_16x16x32_bf16 v[156:159], v[184:187], a[84:87], v[156:159]\n"
"v_mfma_f32_16x16x32_bf16 v[156:159], v[188:191], a[88:91], v[156:159]\n"
"v_mfma_f32_16x16x32_bf16 v[156:159], v[192:195], a[92:95], v[156:159]\n"
"v_mfma_f32_16x16x32_bf16 v[156:159], v[208:211], a[32:35], v[156:159]\n"
"v_mfma_f32_16x16x32_bf16 v[156:159], v[212:215], a[36:39], v[156:159]\n"
"v_subrev_f32 v140, s22, v140\n"
"v_subrev_f32 v141, s22, v141\n"
"v_mfma_f32_16x16x32_bf16 v[156:159], v[216:219], a[40:43], v[156:159]\n"
"s_waitcnt vmcnt(7)\n"
"v_subrev_f32 v142, s22, v142\n"
"v_subrev_f32 v143, s22, v143\n"
"v_mfma_f32_16x16x32_bf16 v[156:159], v[220:223], a[44:47], v[156:159]\n"
"s_barrier\n"
"buffer_store_dword v241, v248, s[84:87], 0 offen sc1\n"
"v_mfma_f32_16x16x32_bf16 v[172:175], v[220:223], a[228:231], v[172:175]\n"
"ds_read_b64_tr_b16 v[220:221], v234 offset:57344\n"
"v_cvt_pk_bf16_f32 v140, v140, v141\n"
"v_mfma_f32_16x16x32_bf16 v[172:175], v[216:219], a[224:227], v[172:175]\n"
"ds_read_b64_tr_b16 v[216:217], v230 offset:57344\n"
"v_cvt_pk_bf16_f32 v141, v142, v143\n"
"v_mfma_f32_16x16x32_bf16 v[172:175], v[212:215], a[212:215], v[172:175]\n"
"ds_read_b64_tr_b16 v[212:213], v234 offset:49152\n"
"v_add_u32 v241, 1, v241\n"
"v_mfma_f32_16x16x32_bf16 v[172:175], v[208:211], a[208:211], v[172:175]\n"
"s_add_u32 s16, s90, s62\n"
"buffer_load_dwordx4 v[84:87], v243, s[32:35], s18 offen offset:3072 sc1\n"
"v_mfma_f32_16x16x32_bf16 v[172:175], v[192:195], a[236:239], v[172:175]\n"
"s_add_u32 s17, s16, 4096\n"
"ds_read_b64_tr_b16 v[192:193], v234 offset:40960\n"
"v_mfma_f32_16x16x32_bf16 v[172:175], v[188:191], a[232:235], v[172:175]\n"
"s_lshl_b32 s62, s70, 9\n"
"ds_read_b64_tr_b16 v[188:189], v230 offset:40960\n"
"v_subrev_f32 v156, s22, v156\n"
"v_mfma_f32_16x16x32_bf16 v[172:175], v[184:187], a[220:223], v[172:175]\n"
"s_add_u32 s40, s41, s53\n"
"ds_read_b64_tr_b16 v[184:185], v234 offset:32768\n"
"v_subrev_f32 v157, s22, v157\n"
"v_mfma_f32_16x16x32_bf16 v[172:175], v[180:183], a[216:219], v[172:175]\n"
"buffer_load_dwordx4 v240, s[28:31], s40 offen offset:2048 lds\n"
"v_mfma_f32_16x16x32_bf16 v[128:131], v[96:99], a[48:51], v[128:131]\n"
"ds_read_b64_tr_b16 v[180:181], v230 offset:32768\n"
"v_subrev_f32 v158, s22, v158\n"
"v_mfma_f32_16x16x32_bf16 v[128:131], v[100:103], a[52:55], v[128:131]\n"
"s_waitcnt lgkmcnt(5)\n"
"ds_read_b64_tr_b16 v[182:183], v230 offset:34816\n"
"v_subrev_f32 v159, s22, v159\n"
"v_mfma_f32_16x16x32_bf16 v[128:131], v[104:107], a[56:59], v[128:131]\n"
"ds_read_b64_tr_b16 v[186:187], v234 offset:34816\n"
"v_cvt_pk_bf16_f32 v156, v156, v157\n"
"v_mfma_f32_16x16x32_bf16 v[128:131], v[108:111], a[60:63], v[128:131]\n"
"ds_read_b64_tr_b16 v[190:191], v230 offset:43008\n"
"v_cvt_pk_bf16_f32 v157, v158, v159\n"
"v_mfma_f32_16x16x32_bf16 v[128:131], v[112:115], a[0:3], v[128:131]\n"
"ds_read_b64_tr_b16 v[194:195], v234 offset:43008\n"
"v_lshlrev_b32 v202, 16, v72\n"
"v_mfma_f32_16x16x32_bf16 v[128:131], v[116:119], a[4:7], v[128:131]\n"
"ds_read_b64_tr_b16 v[208:209], v230 offset:49152\n"
"v_and_b32 v72, s73, v72\n"
"v_mfma_f32_16x16x32_bf16 v[128:131], v[120:123], a[8:11], v[128:131]\n"
"s_add_u32 s40, s41, s54\n"
"ds_read_b64_tr_b16 v[210:211], v230 offset:51200\n"
"v_add_f32 v0, v0, v202\n"
"v_mfma_f32_16x16x32_bf16 v[128:131], v[224:227], a[12:15], v[128:131]\n"
"buffer_load_dwordx4 v240, s[28:31], s40 offen offset:3072 lds\n"
"v_mfma_f32_16x16x32_bf16 v[144:147], v[224:227], a[28:31], v[144:147]\n"
"ds_read_b64_tr_b16 v[214:215], v234 offset:51200\n"
"v_add_f32 v1, v1, v72\n"
"v_mfma_f32_16x16x32_bf16 v[144:147], v[120:123], a[24:27], v[144:147]\n"
"ds_read_b64_tr_b16 v[218:219], v230 offset:59392\n"
"v_lshlrev_b32 v203, 16, v73\n"
"v_mfma_f32_16x16x32_bf16 v[144:147], v[116:119], a[20:23], v[144:147]\n"
"ds_read_b64_tr_b16 v[222:223], v234 offset:59392\n"
"v_and_b32 v73, s73, v73\n"
"v_mfma_f32_16x16x32_bf16 v[144:147], v[112:115], a[16:19], v[144:147]\n"
"buffer_load_dwordx4 v[88:91], v243, s[32:35], s19 offen sc1\n"
"v_mfma_f32_16x16x32_bf16 v[144:147], v[108:111], a[76:79], v[144:147]\n"
"v_add_f32 v2, v2, v203\n"
"v_add_f32 v3, v3, v73\n"
"v_mfma_f32_16x16x32_bf16 v[144:147], v[104:107], a[72:75], v[144:147]\n"
"s_add_u32 s41, s70, 491520\n"
"v_subrev_f32 v128, s22, v128\n"
"v_subrev_f32 v129, s22, v129\n"
"v_mfma_f32_16x16x32_bf16 v[144:147], v[100:103], a[68:71], v[144:147]\n"
"s_add_u32 m0, s71, 49152\n"
"v_subrev_f32 v130, s22, v130\n"
"v_subrev_f32 v131, s22, v131\n"
"v_mfma_f32_16x16x32_bf16 v[144:147], v[96:99], a[64:67], v[144:147]\n"
"buffer_load_dwordx4 v240, s[28:31], s41 offen lds\n"
"v_mfma_f32_16x16x32_bf16 v[160:163], v[96:99], a[80:83], v[160:163]\n"
"s_and_b32 s70, s88, 15\n"
"v_cvt_pk_bf16_f32 v126, v128, v129\n"
"v_cvt_pk_bf16_f32 v127, v130, v131\n"
"v_mfma_f32_16x16x32_bf16 v[160:163], v[100:103], a[84:87], v[160:163]\n"
"s_lshr_b32 s88, s88, 4\n"
"v_lshlrev_b32 v204, 16, v74\n"
"v_and_b32 v74, s73, v74\n"
"v_mfma_f32_16x16x32_bf16 v[160:163], v[104:107], a[88:91], v[160:163]\n"
"s_lshl_b32 s70, s70, 7\n"
"v_add_f32 v4, v4, v204\n"
"v_add_f32 v5, v5, v74\n"
"v_mfma_f32_16x16x32_bf16 v[160:163], v[108:111], a[92:95], v[160:163]\n"
"s_xor_b32 s71, s71, s75\n"
"v_lshlrev_b32 v205, 16, v75\n"
"v_and_b32 v75, s73, v75\n"
"v_mfma_f32_16x16x32_bf16 v[160:163], v[112:115], a[32:35], v[160:163]\n"
"v_add_f32 v6, v6, v205\n"
"v_add_f32 v7, v7, v75\n"
"v_mfma_f32_16x16x32_bf16 v[160:163], v[116:119], a[36:39], v[160:163]\n"
"v_subrev_f32 v144, s22, v144\n"
"v_subrev_f32 v145, s22, v145\n"
"v_mfma_f32_16x16x32_bf16 v[160:163], v[120:123], a[40:43], v[160:163]\n"
"v_subrev_f32 v146, s22, v146\n"
"v_subrev_f32 v147, s22, v147\n"
"v_mfma_f32_16x16x32_bf16 v[160:163], v[224:227], a[44:47], v[160:163]\n"
"buffer_store_dwordx4 v[124:127], v243, s[32:35], s16 offen sc0 sc1\n"
"v_cvt_pk_bf16_f32 v142, v144, v145\n"
"v_mfma_f32_16x16x32_bf16 v[176:179], v[224:227], a[228:231], v[176:179]\n"
"ds_read_b64_tr_b16 v[224:225], v235 offset:57344\n"
"v_cvt_pk_bf16_f32 v143, v146, v147\n"
"v_mfma_f32_16x16x32_bf16 v[176:179], v[120:123], a[224:227], v[176:179]\n"
"ds_read_b64_tr_b16 v[120:121], v231 offset:57344\n"
"v_lshlrev_b32 v206, 16, v76\n"
"v_mfma_f32_16x16x32_bf16 v[176:179], v[116:119], a[212:215], v[176:179]\n"
"ds_read_b64_tr_b16 v[116:117], v235 offset:49152\n"
"v_and_b32 v76, s73, v76\n"
"v_mfma_f32_16x16x32_bf16 v[176:179], v[112:115], a[208:211], v[176:179]\n"
"buffer_load_dwordx4 v[92:95], v243, s[32:35], s19 offen offset:1024 sc1\n"
"v_mfma_f32_16x16x32_bf16 v[176:179], v[108:111], a[236:239], v[176:179]\n"
"ds_read_b64_tr_b16 v[108:109], v235 offset:40960\n"
"v_add_f32 v8, v8, v206\n"
"v_mfma_f32_16x16x32_bf16 v[176:179], v[104:107], a[232:235], v[176:179]\n"
"ds_read_b64_tr_b16 v[104:105], v231 offset:40960\n"
"v_subrev_f32 v160, s22, v160\n"
"v_mfma_f32_16x16x32_bf16 v[176:179], v[100:103], a[220:223], v[176:179]\n"
"s_add_u32 s40, s41, s52\n"
"ds_read_b64_tr_b16 v[100:101], v235 offset:32768\n"
"v_subrev_f32 v161, s22, v161\n"
"v_mfma_f32_16x16x32_bf16 v[176:179], v[96:99], a[216:219], v[176:179]\n"
"s_waitcnt vmcnt(14) lgkmcnt(6)\n"
"buffer_load_dwordx4 v240, s[28:31], s40 offen offset:1024 lds\n"
"v_mfma_f32_16x16x32_bf16 v[132:135], v[180:183], a[48:51], v[132:135]\n"
"ds_read_b64_tr_b16 v[96:97], v231 offset:32768\n"
"v_subrev_f32 v162, s22, v162\n"
"v_mfma_f32_16x16x32_bf16 v[132:135], v[184:187], a[52:55], v[132:135]\n"
"ds_read_b64_tr_b16 v[98:99], v231 offset:34816\n"
"v_subrev_f32 v163, s22, v163\n"
"v_mfma_f32_16x16x32_bf16 v[132:135], v[188:191], a[56:59], v[132:135]\n"
"ds_read_b64_tr_b16 v[102:103], v235 offset:34816\n"
"v_cvt_pk_bf16_f32 v158, v160, v161\n"
"v_mfma_f32_16x16x32_bf16 v[132:135], v[192:195], a[60:63], v[132:135]\n"
"ds_read_b64_tr_b16 v[106:107], v231 offset:43008\n"
"v_cvt_pk_bf16_f32 v159, v162, v163\n"
"v_mfma_f32_16x16x32_bf16 v[132:135], v[208:211], a[0:3], v[132:135]\n"
"ds_read_b64_tr_b16 v[110:111], v235 offset:43008\n"
"v_add_f32 v9, v9, v76\n"
"v_mfma_f32_16x16x32_bf16 v[132:135], v[212:215], a[4:7], v[132:135]\n"
"ds_read_b64_tr_b16 v[112:113], v231 offset:49152\n"
"v_lshlrev_b32 v207, 16, v77\n"
"v_mfma_f32_16x16x32_bf16 v[132:135], v[216:219], a[8:11], v[132:135]\n"
"ds_read_b64_tr_b16 v[114:115], v231 offset:51200\n"
"v_and_b32 v77, s73, v77\n"
"v_mfma_f32_16x16x32_bf16 v[132:135], v[220:223], a[12:15], v[132:135]\n"
"buffer_store_dwordx4 v[140:143], v243, s[32:35], s16 offen offset:2048 sc0 sc1\n"
"v_add_f32 v10, v10, v207\n"
"v_mfma_f32_16x16x32_bf16 v[148:151], v[220:223], a[28:31], v[148:151]\n"
"ds_read_b64_tr_b16 v[118:119], v235 offset:51200\n"
"v_add_f32 v11, v11, v77\n"
"v_mfma_f32_16x16x32_bf16 v[148:151], v[216:219], a[24:27], v[148:151]\n"
"ds_read_b64_tr_b16 v[122:123], v231 offset:59392\n"
"v_lshlrev_b32 v202, 16, v78\n"
"v_mfma_f32_16x16x32_bf16 v[148:151], v[212:215], a[20:23], v[148:151]\n"
"ds_read_b64_tr_b16 v[226:227], v235 offset:59392\n"
"v_and_b32 v78, s73, v78\n"
"v_mfma_f32_16x16x32_bf16 v[148:151], v[208:211], a[16:19], v[148:151]\n"
"v_add_f32 v12, v12, v202\n"
"v_add_f32 v13, v13, v78\n"
"v_mfma_f32_16x16x32_bf16 v[148:151], v[192:195], a[76:79], v[148:151]\n"
"v_lshlrev_b32 v203, 16, v79\n"
"v_and_b32 v79, s73, v79\n"
"v_mfma_f32_16x16x32_bf16 v[148:151], v[188:191], a[72:75], v[148:151]\n"
"v_add_f32 v14, v14, v203\n"
"v_add_f32 v15, v15, v79\n"
"v_mfma_f32_16x16x32_bf16 v[148:151], v[184:187], a[68:71], v[148:151]\n"
"s_add_u32 s40, s41, s53\n"
"v_lshlrev_b32 v204, 16, v80\n"
"v_and_b32 v80, s73, v80\n"
"v_mfma_f32_16x16x32_bf16 v[148:151], v[180:183], a[64:67], v[148:151]\n"
"buffer_load_dwordx4 v240, s[28:31], s40 offen offset:2048 lds\n"
"v_mfma_f32_16x16x32_bf16 v[164:167], v[180:183], a[80:83], v[164:167]\n"
"v_add_f32 v16, v16, v204\n"
"v_add_f32 v17, v17, v80\n"
"v_mfma_f32_16x16x32_bf16 v[164:167], v[184:187], a[84:87], v[164:167]\n"
"v_lshlrev_b32 v205, 16, v81\n"
"v_and_b32 v81, s73, v81\n"
"v_mfma_f32_16x16x32_bf16 v[164:167], v[188:191], a[88:91], v[164:167]\n"
"v_add_f32 v18, v18, v205\n"
"v_add_f32 v19, v19, v81\n"
"v_mfma_f32_16x16x32_bf16 v[164:167], v[192:195], a[92:95], v[164:167]\n"
"buffer_load_dwordx4 v[244:247], v243, s[32:35], s19 offen offset:2048 sc1\n"
"v_mfma_f32_16x16x32_bf16 v[164:167], v[208:211], a[32:35], v[164:167]\n"
"s_lshl4_add_u32 s18, s72, s61\n"
"v_lshlrev_b32 v206, 16, v82\n"
"v_and_b32 v82, s73, v82\n"
"v_mfma_f32_16x16x32_bf16 v[164:167], v[212:215], a[36:39], v[164:167]\n"
"s_lshl_b32 s19, s72, 2\n"
"v_add_f32 v20, v20, v206\n"
"v_add_f32 v21, v21, v82\n"
"v_mfma_f32_16x16x32_bf16 v[164:167], v[216:219], a[40:43], v[164:167]\n"
"s_waitcnt vmcnt(14)\n"
"s_lshr_b32 s19, s89, s19\n"
"v_lshlrev_b32 v207, 16, v83\n"
"v_mfma_f32_16x16x32_bf16 v[164:167], v[220:223], a[44:47], v[164:167]\n"
"s_barrier\n"
"s_and_b32 s19, s19, 1\n"
"s_waitcnt lgkmcnt(0)\n"
"v_mfma_f32_16x16x32_bf16 v[136:139], v[96:99], a[48:51], v[136:139]\n"
"s_lshl3_add_u32 s18, s18, s19\n"
"ds_read_b64_tr_b16 v[180:181], v228 offset:0\n"
"v_and_b32 v83, s73, v83\n"
"v_mfma_f32_16x16x32_bf16 v[136:139], v[100:103], a[52:55], v[136:139]\n"
"s_lshl_b32 s18, s18, 13\n"
"ds_read_b64_tr_b16 v[182:183], v228 offset:2048\n"
"v_add_f32 v22, v22, v207\n"
"v_mfma_f32_16x16x32_bf16 v[136:139], v[104:107], a[56:59], v[136:139]\n"
"s_add_u32 s19, s18, 4096\n"
"v_lshlrev_b32_e64 v201, 2, s72\n"
"ds_read_b64_tr_b16 v[184:185], v232 offset:0\n"
"v_mfma_f32_16x16x32_bf16 v[136:139], v[108:111], a[60:63], v[136:139]\n"
"s_mov_b32 s69, 15\n"
"global_load_dword v200, v201, s[6:7] sc1\n"
"v_mfma_f32_16x16x32_bf16 v[136:139], v[112:115], a[0:3], v[136:139]\n"
"s_add_u32 s21, s21, 1\n"
"ds_read_b64_tr_b16 v[186:187], v232 offset:2048\n"
"v_add_f32 v23, v23, v83\n"
"v_mfma_f32_16x16x32_bf16 v[136:139], v[116:119], a[4:7], v[136:139]\n"
"ds_read_b64_tr_b16 v[188:189], v228 offset:8192\n"
"v_subrev_f32 v172, s22, v172\n"
"v_mfma_f32_16x16x32_bf16 v[136:139], v[120:123], a[8:11], v[136:139]\n"
"ds_read_b64_tr_b16 v[190:191], v228 offset:10240\n"
"v_subrev_f32 v173, s22, v173\n"
"v_mfma_f32_16x16x32_bf16 v[136:139], v[224:227], a[12:15], v[136:139]\n"
"buffer_store_dwordx4 v[156:159], v243, s[32:35], s17 offen sc0 sc1\n"
"v_subrev_f32 v174, s22, v174\n"
"v_mfma_f32_16x16x32_bf16 v[152:155], v[224:227], a[28:31], v[152:155]\n"
"ds_read_b64_tr_b16 v[192:193], v232 offset:8192\n"
"v_subrev_f32 v175, s22, v175\n"
"v_mfma_f32_16x16x32_bf16 v[152:155], v[120:123], a[24:27], v[152:155]\n"
"ds_read_b64_tr_b16 v[194:195], v232 offset:10240\n"
"v_cvt_pk_bf16_f32 v172, v172, v173\n"
"v_mfma_f32_16x16x32_bf16 v[152:155], v[116:119], a[20:23], v[152:155]\n"
"ds_read_b64_tr_b16 v[208:209], v228 offset:16384\n"
"v_cvt_pk_bf16_f32 v173, v174, v175\n"
"v_mfma_f32_16x16x32_bf16 v[152:155], v[112:115], a[16:19], v[152:155]\n"
"ds_read_b64_tr_b16 v[210:211], v228 offset:18432\n"
"v_subrev_f32 v176, s22, v176\n"
"v_mfma_f32_16x16x32_bf16 v[152:155], v[108:111], a[76:79], v[152:155]\n"
"ds_read_b64_tr_b16 v[212:213], v232 offset:16384\n"
"v_subrev_f32 v177, s22, v177\n"
"v_mfma_f32_16x16x32_bf16 v[152:155], v[104:107], a[72:75], v[152:155]\n"
"ds_read_b64_tr_b16 v[214:215], v232 offset:18432\n"
"v_subrev_f32 v178, s22, v178\n"
"v_mfma_f32_16x16x32_bf16 v[152:155], v[100:103], a[68:71], v[152:155]\n"
"s_add_u32 s40, s41, s54\n"
"ds_read_b64_tr_b16 v[216:217], v228 offset:24576\n"
"v_subrev_f32 v179, s22, v179\n"
"v_mfma_f32_16x16x32_bf16 v[152:155], v[96:99], a[64:67], v[152:155]\n"
"buffer_load_dwordx4 v240, s[28:31], s40 offen offset:3072 lds\n"
"v_mfma_f32_16x16x32_bf16 v[168:171], v[96:99], a[80:83], v[168:171]\n"
"ds_read_b64_tr_b16 v[218:219], v228 offset:26624\n"
"v_cvt_pk_bf16_f32 v174, v176, v177\n"
"v_mfma_f32_16x16x32_bf16 v[168:171], v[100:103], a[84:87], v[168:171]\n"
"ds_read_b64_tr_b16 v[220:221], v232 offset:24576\n"
"v_cvt_pk_bf16_f32 v175, v178, v179\n"
"v_mfma_f32_16x16x32_bf16 v[168:171], v[104:107], a[88:91], v[168:171]\n"
"ds_read_b64_tr_b16 v[222:223], v232 offset:26624\n"
"v_subrev_f32 v132, s22, v132\n"
"v_mfma_f32_16x16x32_bf16 v[168:171], v[108:111], a[92:95], v[168:171]\n"
"ds_read_b64_tr_b16 v[96:97], v229 offset:0\n"
"v_subrev_f32 v133, s22, v133\n"
"v_mfma_f32_16x16x32_bf16 v[168:171], v[112:115], a[32:35], v[168:171]\n"
"ds_read_b64_tr_b16 v[98:99], v229 offset:2048\n"
"v_subrev_f32 v134, s22, v134\n"
"v_mfma_f32_16x16x32_bf16 v[168:171], v[116:119], a[36:39], v[168:171]\n"
"ds_read_b64_tr_b16 v[100:101], v233 offset:0\n"
"v_subrev_f32 v135, s22, v135\n"
"v_mfma_f32_16x16x32_bf16 v[168:171], v[120:123], a[40:43], v[168:171]\n"
"s_add_u32 s47, s47, 1\n"
"ds_read_b64_tr_b16 v[102:103], v233 offset:2048\n"
"v_cvt_pk_bf16_f32 v132, v132, v133\n"
"v_mfma_f32_16x16x32_bf16 v[168:171], v[224:227], a[44:47], v[168:171]\n"
"s_waitcnt lgkmcnt(7)\n"
"s_cmp_lg_u32 s47, 9\n"
"ds_read_b64_tr_b16 v[104:105], v229 offset:8192\n"
"s_cbranch_scc1 LGL_%=\n"
"v_mfma_f32_16x16x32_bf16 v[0:3], v[180:183], a[96:99], v[0:3]\n"
"v_subrev_f32 v136, s22, v136\n"
"v_subrev_f32 v138, s22, v138\n"
"v_mfma_f32_16x16x32_bf16 v[0:3], v[184:187], a[100:103], v[0:3]\n"
"v_subrev_f32 v151, s22, v151\n"
"v_subrev_f32 v139, s22, v139\n"
"v_mfma_f32_16x16x32_bf16 v[0:3], v[188:191], a[104:107], v[0:3]\n"
"v_mov_b32 v201, v237\n"
"v_cvt_pk_bf16_f32 v133, v134, v135\n"
"v_subrev_f32 v150, s22, v150\n"
"v_mfma_f32_16x16x32_bf16 v[0:3], v[192:195], a[108:111], v[0:3]\n"
"global_load_dword v200, v201, s[6:7] sc1\n"
"v_mfma_f32_16x16x32_bf16 v[0:3], v[208:211], a[144:147], v[0:3]\n"
"v_subrev_f32 v153, s22, v153\n"
"v_subrev_f32 v169, s22, v169\n"
"v_mfma_f32_16x16x32_bf16 v[0:3], v[212:215], a[148:151], v[0:3]\n"
"s_waitcnt lgkmcnt(2)\n"
"v_subrev_f32 v137, s22, v137\n"
"v_subrev_f32 v152, s22, v152\n"
"v_mfma_f32_16x16x32_bf16 v[0:3], v[216:219], a[152:155], v[0:3]\n"
"v_cvt_pk_bf16_f32 v134, v136, v137\n"
"v_subrev_f32 v168, s22, v168\n"
"v_mfma_f32_16x16x32_bf16 v[0:3], v[220:223], a[156:159], v[0:3]\n"
"buffer_store_dwordx4 v[172:175], v243, s[32:35], s17 offen offset:2048 sc0 sc1\n"
"v_cvt_pk_bf16_f32 v135, v138, v139\n"
"v_subrev_f32 v170, s22, v170\n"
"v_subrev_f32 v171, s22, v171\n"
"v_mfma_f32_16x16x32_bf16 v[16:19], v[220:223], a[172:175], v[16:19]\n"
"buffer_store_dwordx4 v[132:135], v243, s[32:35], s16 offen offset:1024 sc0 sc1\n"
"v_subrev_f32 v165, s22, v165\n"
"v_mfma_f32_16x16x32_bf16 v[16:19], v[216:219], a[168:171], v[16:19]\n"
"v_subrev_f32 v148, s22, v148\n"
"v_subrev_f32 v149, s22, v149\n"
"v_mfma_f32_16x16x32_bf16 v[16:19], v[212:215], a[164:167], v[16:19]\n"
"v_cvt_pk_bf16_f32 v148, v148, v149\n"
"v_subrev_f32 v167, s22, v167\n"
"v_mfma_f32_16x16x32_bf16 v[16:19], v[208:211], a[160:163], v[16:19]\n"
"v_cvt_pk_bf16_f32 v149, v150, v151\n"
"v_subrev_f32 v155, s22, v155\n"
"v_mfma_f32_16x16x32_bf16 v[16:19], v[192:195], a[124:127], v[16:19]\n"
"v_subrev_f32 v164, s22, v164\n"
"v_subrev_f32 v166, s22, v166\n"
"v_mfma_f32_16x16x32_bf16 v[16:19], v[188:191], a[120:123], v[16:19]\n"
"v_cvt_pk_bf16_f32 v164, v164, v165\n"
"v_subrev_f32 v154, s22, v154\n"
"v_mfma_f32_16x16x32_bf16 v[16:19], v[184:187], a[116:119], v[16:19]\n"
"v_cvt_pk_bf16_f32 v150, v152, v153\n"
"v_cvt_pk_bf16_f32 v151, v154, v155\n"
"v_mfma_f32_16x16x32_bf16 v[16:19], v[180:183], a[112:115], v[16:19]\n"
"s_waitcnt vmcnt(16)\n"
"buffer_store_dwordx4 v[148:151], v243, s[32:35], s16 offen offset:3072 sc0 sc1\n"
"v_cvt_pk_bf16_f32 v165, v166, v167\n"
"v_cvt_pk_bf16_f32 v166, v168, v169\n"
"v_cvt_pk_bf16_f32 v167, v170, v171\n"
"v_mfma_f32_16x16x32_bf16 v[32:35], v[180:183], a[128:131], v[32:35]\n"
"buffer_store_dwordx4 v[164:167], v243, s[32:35], s17 offen offset:1024 sc0 sc1\n"
"ds_read_b64_tr_b16 v[108:109], v233 offset:8192\n"
"v_mfma_f32_16x16x32_bf16 v[32:35], v[184:187], a[132:135], v[32:35]\n"
"ds_read_b64_tr_b16 v[122:123], v229 offset:26624\n"
"v_lshlrev_b32 v202, 16, v84\n"
"v_mfma_f32_16x16x32_bf16 v[32:35], v[188:191], a[136:139], v[32:35]\n"
"ds_read_b64_tr_b16 v[224:225], v233 offset:24576\n"
"v_xor_b32 v230, s75, v230\n"
"v_mfma_f32_16x16x32_bf16 v[32:35], v[192:195], a[140:143], v[32:35]\n"
"ds_read_b64_tr_b16 v[118:119], v233 offset:18432\n"
"v_bfe_u32 v161, v242, 8, 2\n"
"v_mfma_f32_16x16x32_bf16 v[32:35], v[208:211], a[176:179], v[32:35]\n"
"ds_read_b64_tr_b16 v[226:227], v233 offset:26624\n"
"v_bfe_u32 v160, v242, 6, 2\n"
"v_mfma_f32_16x16x32_bf16 v[32:35], v[212:215], a[180:183], v[32:35]\n"
"ds_read_b64_tr_b16 v[114:115], v229 offset:18432\n"
"v_mov_b32 v165, 0\n"
"v_mfma_f32_16x16x32_bf16 v[32:35], v[216:219], a[184:187], v[32:35]\n"
"ds_read_b64_tr_b16 v[120:121], v229 offset:24576\n"
"v_xor_b32 v160, v160, v161\n"
"v_mfma_f32_16x16x32_bf16 v[32:35], v[220:223], a[188:191], v[32:35]\n"
"ds_read_b64_tr_b16 v[116:117], v233 offset:16384\n"
"v_xor_b32 v234, s75, v234\n"
"v_mfma_f32_16x16x32_bf16 v[48:51], v[220:223], a[252:255], v[48:51]\n"
"ds_read_b64_tr_b16 v[110:111], v233 offset:10240\n"
"v_lshlrev_b32 v204, 16, v86\n"
"v_mfma_f32_16x16x32_bf16 v[48:51], v[216:219], a[248:251], v[48:51]\n"
"ds_read_b64_tr_b16 v[106:107], v229 offset:10240\n"
"v_lshlrev_b32 v205, 16, v87\n"
"v_mfma_f32_16x16x32_bf16 v[48:51], v[212:215], a[244:247], v[48:51]\n"
"ds_read_b64_tr_b16 v[112:113], v229 offset:16384\n"
"v_lshlrev_b32 v203, 16, v85\n"
"v_mfma_f32_16x16x32_bf16 v[48:51], v[208:211], a[240:243], v[48:51]\n"
"ds_read_b64_tr_b16 v[210:211], v230 offset:18432\n"
"v_add_f32 v24, v24, v202\n"
"v_mfma_f32_16x16x32_bf16 v[48:51], v[192:195], a[204:207], v[48:51]\n"
"ds_read_b64_tr_b16 v[220:221], v234 offset:24576\n"
"v_xor_b32 v231, s75, v231\n"
"v_mfma_f32_16x16x32_bf16 v[48:51], v[188:191], a[200:203], v[48:51]\n"
"ds_read_b64_tr_b16 v[194:195], v234 offset:10240\n"
"v_add_f32 v26, v26, v203\n"
"v_mfma_f32_16x16x32_bf16 v[48:51], v[184:187], a[196:199], v[48:51]\n"
"ds_read_b64_tr_b16 v[222:223], v234 offset:26624\n"
"v_add_f32 v28, v28, v204\n"
"v_mfma_f32_16x16x32_bf16 v[48:51], v[180:183], a[192:195], v[48:51]\n"
"s_waitcnt vmcnt(15)\n"
"ds_read_b64_tr_b16 v[180:181], v230 offset:0\n"
"v_lshlrev_b32 v202, 16, v90\n"
"v_mfma_f32_16x16x32_bf16 v[4:7], v[96:99], a[96:99], v[4:7]\n"
"s_waitcnt lgkmcnt(7)\n"
"ds_read_b64_tr_b16 v[218:219], v230 offset:26624\n"
"v_lshlrev_b32 v203, 16, v91\n"
"v_mfma_f32_16x16x32_bf16 v[4:7], v[100:103], a[100:103], v[4:7]\n"
"s_waitcnt lgkmcnt(7)\n"
"ds_read_b64_tr_b16 v[216:217], v230 offset:24576\n"
"v_and_b32 v91, s73, v91\n"
"v_mfma_f32_16x16x32_bf16 v[4:7], v[104:107], a[104:107], v[4:7]\n"
"ds_read_b64_tr_b16 v[190:191], v230 offset:10240\n"
"v_and_b32 v90, s73, v90\n"
"v_mfma_f32_16x16x32_bf16 v[4:7], v[108:111], a[108:111], v[4:7]\n"
"s_waitcnt lgkmcnt(7)\n"
"ds_read_b64_tr_b16 v[214:215], v234 offset:18432\n"
"v_add_u32 v160, -1, v160\n"
"v_mfma_f32_16x16x32_bf16 v[4:7], v[112:115], a[144:147], v[4:7]\n"
"s_waitcnt vmcnt(4)\n"
"ds_read_b64_tr_b16 v[208:209], v230 offset:16384\n"
"v_add_f32 v37, v37, v90\n"
"v_mfma_f32_16x16x32_bf16 v[4:7], v[116:119], a[148:151], v[4:7]\n"
"v_subrev_u32 v200, s21, v200\n"
"ds_read_b64_tr_b16 v[186:187], v234 offset:2048\n"
"v_mfma_f32_16x16x32_bf16 v[4:7], v[120:123], a[152:155], v[4:7]\n"
"v_cmp_lt_u32 vcc, s69, v200\n"
"ds_read_b64_tr_b16 v[192:193], v234 offset:8192\n"
"v_mfma_f32_16x16x32_bf16 v[4:7], v[224:227], a[156:159], v[4:7]\n"
"ds_read_b64_tr_b16 v[188:189], v230 offset:8192\n"
"s_cbranch_vccnz LGQ_%=\nLGQK_%=:\n"
"v_mfma_f32_16x16x32_bf16 v[20:23], v[224:227], a[172:175], v[20:23]\n"
"s_bfe_u32 s20, s89, 65536\ns_lshl_b32 s20, s20, 13\ns_add_u32 s20, s20, 0\n"
"ds_read_b64_tr_b16 v[184:185], v234 offset:0\n"
"v_add_f32 v39, v39, v91\n"
"v_mfma_f32_16x16x32_bf16 v[20:23], v[120:123], a[168:171], v[20:23]\n"
"buffer_load_dwordx4 v[124:127], v236, s[32:35], s20 offen sc1\n"
"v_mfma_f32_16x16x32_bf16 v[20:23], v[116:119], a[164:167], v[20:23]\n"
"v_add_f32 v30, v30, v205\n"
"v_ashrrev_i32 v160, 31, v160\n"
"v_mfma_f32_16x16x32_bf16 v[20:23], v[112:115], a[160:163], v[20:23]\n"
"s_bfe_u32 s20, s89, 65540\ns_lshl_b32 s20, s20, 13\ns_add_u32 s20, s20, 1048576\n"
"v_add_f32 v36, v36, v202\n"
"v_mfma_f32_16x16x32_bf16 v[20:23], v[108:111], a[124:127], v[20:23]\n"
"buffer_load_dwordx4 v[128:131], v236, s[32:35], s20 offen sc1\n"
"v_mfma_f32_16x16x32_bf16 v[20:23], v[104:107], a[120:123], v[20:23]\n"
"v_add_f32 v38, v38, v203\n"
"v_mfma_f32_16x16x32_bf16 v[20:23], v[100:103], a[116:119], v[20:23]\n"
"s_bfe_u32 s20, s89, 65544\ns_lshl_b32 s20, s20, 13\ns_add_u32 s20, s20, 2097152\n"
"v_and_b32 v84, s73, v84\n"
"v_lshlrev_b32 v207, 16, v89\n"
"v_mfma_f32_16x16x32_bf16 v[20:23], v[96:99], a[112:115], v[20:23]\n"
"buffer_load_dwordx4 v[132:135], v236, s[32:35], s20 offen sc1\n"
"v_mfma_f32_16x16x32_bf16 v[36:39], v[96:99], a[128:131], v[36:39]\n"
"v_and_b32 v85, s73, v85\n"
"v_lshlrev_b32 v204, 16, v92\n"
"v_mfma_f32_16x16x32_bf16 v[36:39], v[100:103], a[132:135], v[36:39]\n"
"s_bfe_u32 s20, s89, 65548\ns_lshl_b32 s20, s20, 13\ns_add_u32 s20, s20, 3145728\n"
"ds_read_b64_tr_b16 v[182:183], v230 offset:2048\n"
"ds_read_b64_tr_b16 v[212:213], v234 offset:16384\n"
"v_mfma_f32_16x16x32_bf16 v[36:39], v[104:107], a[136:139], v[36:39]\n"
"buffer_load_dwordx4 v[136:139], v236, s[32:35], s20 offen sc1\n"
"v_mfma_f32_16x16x32_bf16 v[36:39], v[108:111], a[140:143], v[36:39]\n"
"v_lshlrev_b32 v205, 16, v93\n"
"v_mfma_f32_16x16x32_bf16 v[36:39], v[112:115], a[176:179], v[36:39]\n"
"s_bfe_u32 s20, s89, 65552\ns_lshl_b32 s20, s20, 13\ns_add_u32 s20, s20, 4194304\n"
"v_and_b32 v160, 0x3f80, v160\n"
"v_add_f32 v40, v40, v204\n"
"v_mfma_f32_16x16x32_bf16 v[36:39], v[116:119], a[180:183], v[36:39]\n"
"buffer_load_dwordx4 v[140:143], v236, s[32:35], s20 offen sc1\n"
"v_mfma_f32_16x16x32_bf16 v[36:39], v[120:123], a[184:187], v[36:39]\n"
"v_and_b32 v161, 16, v242\n"
"v_lshlrev_b32 v206, 16, v88\n"
"v_mfma_f32_16x16x32_bf16 v[36:39], v[224:227], a[188:191], v[36:39]\n"
"s_bfe_u32 s20, s89, 65556\ns_lshl_b32 s20, s20, 13\ns_add_u32 s20, s20, 5242880\n"
"v_add_f32 v27, v27, v85\n"
"v_add_f32 v34, v34, v207\n"
"v_mfma_f32_16x16x32_bf16 v[52:55], v[224:227], a[252:255], v[52:55]\n"
"buffer_load_dwordx4 v[144:147], v236, s[32:35], s20 offen sc1\n"
"v_mfma_f32_16x16x32_bf16 v[52:55], v[120:123], a[248:251], v[52:55]\n"
"v_and_b32 v86, s73, v86\n"
"v_and_b32 v92, s73, v92\n"
"v_mfma_f32_16x16x32_bf16 v[52:55], v[116:119], a[244:247], v[52:55]\n"
"s_bfe_u32 s20, s89, 65560\ns_lshl_b32 s20, s20, 13\ns_add_u32 s20, s20, 6291456\n"
"v_xor_b32 v235, s75, v235\n"
"v_mov_b32 v164, 0\n"
"v_mfma_f32_16x16x32_bf16 v[52:55], v[112:115], a[240:243], v[52:55]\n"
"buffer_load_dwordx4 v[148:151], v236, s[32:35], s20 offen sc1\n"
"v_mfma_f32_16x16x32_bf16 v[52:55], v[108:111], a[204:207], v[52:55]\n"
"s_bfe_u32 s20, s89, 65564\ns_lshl_b32 s20, s20, 13\ns_add_u32 s20, s20, 7340032\n"
"v_and_b32 v88, s73, v88\n"
"v_lshlrev_b32 v207, 16, v95\n"
"v_mfma_f32_16x16x32_bf16 v[52:55], v[104:107], a[200:203], v[52:55]\n"
"buffer_load_dwordx4 v[152:155], v236, s[32:35], s20 offen sc1\n"
"v_mfma_f32_16x16x32_bf16 v[52:55], v[100:103], a[196:199], v[52:55]\n"
"s_waitcnt vmcnt(8)\n"
"s_add_u32 s21, s21, 1\n"
"v_add_f32 v32, v32, v206\n"
"v_mfma_f32_16x16x32_bf16 v[52:55], v[96:99], a[192:195], v[52:55]\n"
"s_barrier\n"
"s_mov_b32 s52, 40960\n"
"buffer_store_dword v241, v248, s[84:87], 0 offen sc1\n"
"s_waitcnt lgkmcnt(0)\n"
"v_mfma_f32_16x16x32_bf16 v[8:11], v[180:183], a[96:99], v[8:11]\n"
"s_mov_b32 s53, 81920\n"
"ds_read_b64_tr_b16 v[120:121], v231 offset:24576\n"
"v_lshlrev_b32 v206, 16, v94\n"
"v_mfma_f32_16x16x32_bf16 v[8:11], v[184:187], a[100:103], v[8:11]\n"
"ds_read_b64_tr_b16 v[96:97], v231 offset:0\n"
"v_and_b32 v89, s73, v89\n"
"v_mfma_f32_16x16x32_bf16 v[8:11], v[188:191], a[104:107], v[8:11]\n"
"ds_read_b64_tr_b16 v[102:103], v235 offset:2048\n"
"v_add_f32 v46, v46, v207\n"
"v_mfma_f32_16x16x32_bf16 v[8:11], v[192:195], a[108:111], v[8:11]\n"
"v_and_b32 v93, s73, v93\n"
"v_add_f32 v44, v44, v206\n"
"v_mfma_f32_16x16x32_bf16 v[8:11], v[208:211], a[144:147], v[8:11]\n"
"ds_read_b64_tr_b16 v[122:123], v231 offset:26624\n"
"v_add_f32 v43, v43, v93\n"
"v_mfma_f32_16x16x32_bf16 v[8:11], v[212:215], a[148:151], v[8:11]\n"
"ds_read_b64_tr_b16 v[116:117], v235 offset:16384\n"
"v_add_f32 v25, v25, v84\n"
"v_mfma_f32_16x16x32_bf16 v[8:11], v[216:219], a[152:155], v[8:11]\n"
"v_and_b32 v87, s73, v87\n"
"v_and_b32 v94, s73, v94\n"
"v_mfma_f32_16x16x32_bf16 v[8:11], v[220:223], a[156:159], v[8:11]\n"
"ds_read_b64_tr_b16 v[224:225], v235 offset:24576\n"
"v_lshlrev_b32 v202, 16, v246\n"
"v_mfma_f32_16x16x32_bf16 v[24:27], v[220:223], a[172:175], v[24:27]\n"
"v_add_f32 v31, v31, v87\n"
"v_add_f32 v42, v42, v205\n"
"v_mfma_f32_16x16x32_bf16 v[24:27], v[216:219], a[168:171], v[24:27]\n"
"ds_read_b64_tr_b16 v[100:101], v235 offset:0\n"
"v_lshlrev_b32 v207, 16, v245\n"
"v_mfma_f32_16x16x32_bf16 v[24:27], v[212:215], a[164:167], v[24:27]\n"
"v_add_f32 v29, v29, v86\n"
"v_add_f32 v41, v41, v92\n"
"v_mfma_f32_16x16x32_bf16 v[24:27], v[208:211], a[160:163], v[24:27]\n"
"ds_read_b64_tr_b16 v[98:99], v231 offset:2048\n"
"v_and_b32 v95, s73, v95\n"
"v_mfma_f32_16x16x32_bf16 v[24:27], v[192:195], a[124:127], v[24:27]\n"
"ds_read_b64_tr_b16 v[114:115], v231 offset:18432\n"
"v_add_f32 v33, v33, v88\n"
"v_mfma_f32_16x16x32_bf16 v[24:27], v[188:191], a[120:123], v[24:27]\n"
"ds_read_b64_tr_b16 v[110:111], v235 offset:10240\n"
"v_and_b32 v245, s73, v245\n"
"v_mfma_f32_16x16x32_bf16 v[24:27], v[184:187], a[116:119], v[24:27]\n"
"v_add_f32 v45, v45, v94\n"
"v_and_b32 v246, s73, v246\n"
"v_mfma_f32_16x16x32_bf16 v[24:27], v[180:183], a[112:115], v[24:27]\n"
"ds_read_b64_tr_b16 v[108:109], v235 offset:8192\n"
"v_lshlrev_b32 v206, 16, v244\n"
"v_mfma_f32_16x16x32_bf16 v[40:43], v[180:183], a[128:131], v[40:43]\n"
"ds_read_b64_tr_b16 v[106:107], v231 offset:10240\n"
"v_lshlrev_b32 v160, v161, v160\n"
"v_mfma_f32_16x16x32_bf16 v[40:43], v[184:187], a[132:135], v[40:43]\n"
"ds_read_b64_tr_b16 v[104:105], v231 offset:8192\n"
"v_mfma_f32_16x16x32_bf16 v[40:43], v[188:191], a[136:139], v[40:43]\n"
"v_bfe_i32 v161, v242, 5, 1\n"
"v_mfma_f32_16x16x32_bf16 v[40:43], v[192:195], a[140:143], v[40:43]\n"
"ds_read_b64_tr_b16 v[112:113], v231 offset:16384\n"
"v_and_b32 v244, s73, v244\n"
"v_mfma_f32_16x16x32_bf16 v[40:43], v[208:211], a[176:179], v[40:43]\n"
"ds_read_b64_tr_b16 v[118:119], v235 offset:18432\n"
"v_add_f32 v49, v49, v244\n"
"v_mfma_f32_16x16x32_bf16 v[40:43], v[212:215], a[180:183], v[40:43]\n"
"v_add_f32 v52, v52, v202\n"
"v_lshlrev_b32 v203, 16, v247\n"
"v_mfma_f32_16x16x32_bf16 v[40:43], v[216:219], a[184:187], v[40:43]\n"
"ds_read_b64_tr_b16 v[226:227], v235 offset:26624\n"
"v_add_f32 v48, v48, v206\n"
"v_mfma_f32_16x16x32_bf16 v[40:43], v[220:223], a[188:191], v[40:43]\n"
"s_barrier\n"
"s_waitcnt lgkmcnt(5)\n"
"v_mfma_f32_16x16x32_bf16 v[12:15], v[96:99], a[96:99], v[12:15]\n"
"ds_read_b64_tr_b16 v[194:195], v232 offset:43008\n"
"ds_read_b64_tr_b16 v[214:215], v232 offset:51200\n"
"v_mfma_f32_16x16x32_bf16 v[12:15], v[100:103], a[100:103], v[12:15]\n"
"s_waitcnt lgkmcnt(5)\n"
"ds_read_b64_tr_b16 v[186:187], v232 offset:34816\n"
"v_add_f32 v53, v53, v246\n"
"v_mfma_f32_16x16x32_bf16 v[12:15], v[104:107], a[104:107], v[12:15]\n"
"ds_read_b64_tr_b16 v[184:185], v232 offset:32768\n"
"ds_read_b64_tr_b16 v[208:209], v228 offset:49152\n"
"v_mfma_f32_16x16x32_bf16 v[12:15], v[108:111], a[108:111], v[12:15]\n"
"s_waitcnt lgkmcnt(7)\n"
"ds_read_b64_tr_b16 v[188:189], v228 offset:40960\n"
"v_add_f32 v47, v47, v95\n"
"v_mfma_f32_16x16x32_bf16 v[12:15], v[112:115], a[144:147], v[12:15]\n"
"s_waitcnt lgkmcnt(7)\n"
"ds_read_b64_tr_b16 v[180:181], v228 offset:32768\n"
"v_add_f32 v35, v35, v89\n"
"v_mfma_f32_16x16x32_bf16 v[12:15], v[116:119], a[148:151], v[12:15]\n"
"ds_read_b64_tr_b16 v[216:217], v228 offset:57344\n"
"v_add_f32 v51, v51, v245\n"
"v_mfma_f32_16x16x32_bf16 v[12:15], v[120:123], a[152:155], v[12:15]\n"
"s_waitcnt lgkmcnt(8)\n"
"ds_read_b64_tr_b16 v[182:183], v228 offset:34816\n"
"ds_read_b64_tr_b16 v[212:213], v232 offset:49152\n"
"v_mfma_f32_16x16x32_bf16 v[12:15], v[224:227], a[156:159], v[12:15]\n"
"global_load_dword v200, v201, s[6:7] sc1\n"
"v_mfma_f32_16x16x32_bf16 v[28:31], v[224:227], a[172:175], v[28:31]\n"
"v_add_f32 v50, v50, v207\n"
"v_add_f32 v54, v54, v203\n"
"v_mfma_f32_16x16x32_bf16 v[28:31], v[120:123], a[168:171], v[28:31]\n"
"s_waitcnt vmcnt(9)\n"
"v_lshlrev_b32 v58, 16, v125\n"
"v_lshlrev_b32 v62, 16, v127\n"
"v_mfma_f32_16x16x32_bf16 v[28:31], v[116:119], a[164:167], v[28:31]\n"
"ds_read_b64_tr_b16 v[210:211], v228 offset:51200\n"
"ds_read_b64_tr_b16 v[218:219], v228 offset:59392\n"
"v_mfma_f32_16x16x32_bf16 v[28:31], v[112:115], a[160:163], v[28:31]\n"
"ds_read_b64_tr_b16 v[190:191], v228 offset:43008\n"
"v_lshlrev_b32 v60, 16, v126\n"
"v_mfma_f32_16x16x32_bf16 v[28:31], v[108:111], a[124:127], v[28:31]\n"
"s_waitcnt vmcnt(8)\n"
"ds_read_b64_tr_b16 v[192:193], v232 offset:40960\n"
"v_lshlrev_b32 v205, 16, v129\n"
"v_mfma_f32_16x16x32_bf16 v[28:31], v[104:107], a[120:123], v[28:31]\n"
"v_lshlrev_b32 v56, 16, v124\n"
"v_lshlrev_b32 v206, 16, v130\n"
"v_mfma_f32_16x16x32_bf16 v[28:31], v[100:103], a[116:119], v[28:31]\n"
"ds_read_b64_tr_b16 v[222:223], v232 offset:59392\n"
"v_add_f32 v58, v58, v205\n"
"v_mfma_f32_16x16x32_bf16 v[28:31], v[96:99], a[112:115], v[28:31]\n"
"s_waitcnt vmcnt(7)\n"
"v_lshlrev_b32 v204, 16, v128\n"
"v_add_f32 v56, v56, v204\n"
"v_mfma_f32_16x16x32_bf16 v[44:47], v[96:99], a[128:131], v[44:47]\n"
"v_lshlrev_b32 v207, 16, v131\n"
"v_lshlrev_b32 v203, 16, v133\n"
"v_mfma_f32_16x16x32_bf16 v[44:47], v[100:103], a[132:135], v[44:47]\n"
"v_add_f32 v58, v58, v203\n"
"v_lshlrev_b32 v205, 16, v135\n"
"v_mfma_f32_16x16x32_bf16 v[44:47], v[104:107], a[136:139], v[44:47]\n"
"s_waitcnt vmcnt(6)\n"
"ds_read_b64_tr_b16 v[106:107], v229 offset:43008\n"
"v_and_b32 v59, s73, v125\n"
"v_mfma_f32_16x16x32_bf16 v[44:47], v[108:111], a[140:143], v[44:47]\n"
"v_add_f32 v60, v60, v206\n"
"v_lshlrev_b32 v206, 16, v136\n"
"v_mfma_f32_16x16x32_bf16 v[44:47], v[112:115], a[176:179], v[44:47]\n"
"v_lshlrev_b32 v202, 16, v132\n"
"v_and_b32 v132, s73, v132\n"
"v_mfma_f32_16x16x32_bf16 v[44:47], v[116:119], a[180:183], v[44:47]\n"
"s_waitcnt vmcnt(5)\n"
"ds_read_b64_tr_b16 v[100:101], v233 offset:32768\n"
"v_lshlrev_b32 v204, 16, v134\n"
"v_mfma_f32_16x16x32_bf16 v[44:47], v[120:123], a[184:187], v[44:47]\n"
"ds_read_b64_tr_b16 v[220:221], v232 offset:57344\n"
"v_add_f32 v62, v62, v207\n"
"v_mfma_f32_16x16x32_bf16 v[44:47], v[224:227], a[188:191], v[44:47]\n"
"s_waitcnt lgkmcnt(3)\n"
"ds_read_b64_tr_b16 v[224:225], v233 offset:57344\n"
"v_add_f32 v62, v62, v205\n"
"v_mfma_f32_16x16x32_bf16 v[0:3], v[180:183], a[48:51], v[0:3]\n"
"s_waitcnt vmcnt(4)\n"
"ds_read_b64_tr_b16 v[114:115], v229 offset:51200\n"
"v_lshlrev_b32 v207, 16, v137\n"
"v_mfma_f32_16x16x32_bf16 v[0:3], v[184:187], a[52:55], v[0:3]\n"
"v_and_b32 v128, s73, v128\n"
"v_lshlrev_b32 v203, 16, v139\n"
"v_mfma_f32_16x16x32_bf16 v[0:3], v[188:191], a[56:59], v[0:3]\n"
"v_and_b32 v57, s73, v124\n"
"v_add_f32 v60, v60, v204\n"
"v_mfma_f32_16x16x32_bf16 v[0:3], v[192:195], a[60:63], v[0:3]\n"
"s_waitcnt vmcnt(3)\n"
"ds_read_b64_tr_b16 v[96:97], v229 offset:32768\n"
"ds_read_b64_tr_b16 v[112:113], v229 offset:49152\n"
"v_mfma_f32_16x16x32_bf16 v[0:3], v[208:211], a[0:3], v[0:3]\n"
"v_and_b32 v247, s73, v247\n"
"v_add_f32 v58, v58, v207\n"
"v_mfma_f32_16x16x32_bf16 v[0:3], v[212:215], a[4:7], v[0:3]\n"
"s_waitcnt vmcnt(2)\n"
"ds_read_b64_tr_b16 v[102:103], v233 offset:34816\n"
"v_and_b32 v134, s73, v134\n"
"v_mfma_f32_16x16x32_bf16 v[0:3], v[216:219], a[8:11], v[0:3]\n"
"s_waitcnt lgkmcnt(5)\n"
"ds_read_b64_tr_b16 v[120:121], v229 offset:57344\n"
"v_lshlrev_b32 v204, 16, v140\n"
"v_mfma_f32_16x16x32_bf16 v[0:3], v[220:223], a[12:15], v[0:3]\n"
"ds_read_b64_tr_b16 v[98:99], v229 offset:34816\n"
"v_and_b32 v61, s73, v126\n"
"v_add_f32 v57, v57, v128\n"
"v_add_f32 v56, v56, v202\n"
"v_and_b32 v135, s73, v135\n"
"v_and_b32 v136, s73, v136\n"
"v_and_b32 v137, s73, v137\n"
"v_lshlrev_b32 v202, 16, v138\n"
"v_mfma_f32_16x16x32_bf16 v[16:19], v[220:223], a[28:31], v[16:19]\n"
"ds_read_b64_tr_b16 v[122:123], v229 offset:59392\n"
"v_and_b32 v129, s73, v129\n"
"v_mfma_f32_16x16x32_bf16 v[16:19], v[216:219], a[24:27], v[16:19]\n"
"ds_read_b64_tr_b16 v[108:109], v233 offset:40960\n"
"v_add_f32 v56, v56, v206\n"
"v_mfma_f32_16x16x32_bf16 v[16:19], v[212:215], a[20:23], v[16:19]\n"
"v_add_f32 v60, v60, v202\n"
"v_lshlrev_b32 v207, 16, v143\n"
"v_mfma_f32_16x16x32_bf16 v[16:19], v[208:211], a[16:19], v[16:19]\n"
"ds_read_b64_tr_b16 v[116:117], v233 offset:49152\n"
"v_lshlrev_b32 v205, 16, v141\n"
"v_mfma_f32_16x16x32_bf16 v[16:19], v[192:195], a[76:79], v[16:19]\n"
"s_waitcnt vmcnt(0)\n"
"v_add_f32 v55, v55, v247\n"
"v_lshlrev_b32 v202, 16, v144\n"
"v_mfma_f32_16x16x32_bf16 v[16:19], v[188:191], a[72:75], v[16:19]\n"
"v_subrev_u32 v200, s21, v200\n"
"ds_read_b64_tr_b16 v[104:105], v229 offset:40960\n"
"v_mfma_f32_16x16x32_bf16 v[16:19], v[184:187], a[68:71], v[16:19]\n"
"v_cmp_lt_u32 vcc, s69, v200\n"
"v_add_f32 v62, v62, v203\n"
"v_mfma_f32_16x16x32_bf16 v[16:19], v[180:183], a[64:67], v[16:19]\n"
"ds_read_b64_tr_b16 v[118:119], v233 offset:51200\n"
"v_and_b32 v63, s73, v127\n"
"v_add_f32 v59, v59, v129\n"
"v_add_f32 v56, v56, v204\n"
"v_add_f32 v58, v58, v205\n"
"v_lshlrev_b32 v206, 16, v142\n"
"v_lshlrev_b32 v203, 16, v145\n"
"v_lshlrev_b32 v205, 16, v147\n"
"v_subrev_f32 v1, s22, v1\n"
"s_cbranch_vccnz LGR_%=\nLGRK_%=:\n"
"v_mfma_f32_16x16x32_bf16 v[32:35], v[180:183], a[80:83], v[32:35]\n"
"ds_read_b64_tr_b16 v[110:111], v233 offset:43008\n"
"s_cmp_eq_u32 s93, 0\ns_cbranch_scc1 LEP_%=\nLEPK_%=:\n"
"v_mfma_f32_16x16x32_bf16 v[32:35], v[184:187], a[84:87], v[32:35]\n"
"s_bfe_u32 s20, s89, 65536\ns_lshl_b32 s20, s20, 13\ns_add_u32 s20, s20, 65536\n"
"ds_read_b64_tr_b16 v[226:227], v233 offset:59392\n"
"v_add_f32 v57, v57, v132\n"
"v_mfma_f32_16x16x32_bf16 v[32:35], v[188:191], a[88:91], v[32:35]\n"
"buffer_load_dwordx4 v[72:75], v236, s[32:35], s20 offen sc1\n"
"v_mfma_f32_16x16x32_bf16 v[32:35], v[192:195], a[92:95], v[32:35]\n"
"v_and_b32 v131, s73, v131\n"
"v_add_f32 v58, v58, v203\n"
"v_mfma_f32_16x16x32_bf16 v[32:35], v[208:211], a[32:35], v[32:35]\n"
"s_bfe_u32 s20, s89, 65540\ns_lshl_b32 s20, s20, 13\ns_add_u32 s20, s20, 1114112\n"
"v_subrev_f32 v2, s22, v2\n"
"v_subrev_f32 v16, s22, v16\n"
"v_mfma_f32_16x16x32_bf16 v[32:35], v[212:215], a[36:39], v[32:35]\n"
"buffer_load_dwordx4 v[76:79], v236, s[32:35], s20 offen sc1\n"
"v_mfma_f32_16x16x32_bf16 v[32:35], v[216:219], a[40:43], v[32:35]\n"
"v_and_b32 v139, s73, v139\n"
"v_add_f32 v62, v62, v207\n"
"v_mfma_f32_16x16x32_bf16 v[32:35], v[220:223], a[44:47], v[32:35]\n"
"s_bfe_u32 s20, s89, 65544\ns_lshl_b32 s20, s20, 13\ns_add_u32 s20, s20, 2162688\n"
"v_add_f32 v62, v62, v205\n"
"v_lshlrev_b32 v207, 16, v149\n"
"v_mfma_f32_16x16x32_bf16 v[48:51], v[220:223], a[228:231], v[48:51]\n"
"buffer_load_dwordx4 v[80:83], v236, s[32:35], s20 offen sc1\n"
"v_mfma_f32_16x16x32_bf16 v[48:51], v[216:219], a[224:227], v[48:51]\n"
"v_and_b32 v140, s73, v140\n"
"v_and_b32 v142, s73, v142\n"
"v_mfma_f32_16x16x32_bf16 v[48:51], v[212:215], a[212:215], v[48:51]\n"
"s_bfe_u32 s20, s89, 65548\ns_lshl_b32 s20, s20, 13\ns_add_u32 s20, s20, 3211264\n"
"v_add_f32 v57, v57, v136\n"
"v_subrev_f32 v3, s22, v3\n"
"v_mfma_f32_16x16x32_bf16 v[48:51], v[208:211], a[208:211], v[48:51]\n"
"buffer_load_dwordx4 v[84:87], v236, s[32:35], s20 offen sc1\n"
"v_mfma_f32_16x16x32_bf16 v[48:51], v[192:195], a[236:239], v[48:51]\n"
"v_lshlrev_b32 v204, 16, v146\n"
"v_and_b32 v147, s73, v147\n"
"v_mfma_f32_16x16x32_bf16 v[48:51], v[188:191], a[232:235], v[48:51]\n"
"s_bfe_u32 s20, s89, 65552\ns_lshl_b32 s20, s20, 13\ns_add_u32 s20, s20, 4259840\n"
"v_and_b32 v138, s73, v138\n"
"v_and_b32 v144, s73, v144\n"
"v_mfma_f32_16x16x32_bf16 v[48:51], v[184:187], a[220:223], v[48:51]\n"
"buffer_load_dwordx4 v[88:91], v236, s[32:35], s20 offen sc1\n"
"v_mfma_f32_16x16x32_bf16 v[48:51], v[180:183], a[216:219], v[48:51]\n"
"s_waitcnt lgkmcnt(0)\n"
"v_and_b32 v133, s73, v133\n"
"v_add_f32 v58, v58, v207\n"
"v_mfma_f32_16x16x32_bf16 v[4:7], v[96:99], a[48:51], v[4:7]\n"
"s_bfe_u32 s20, s89, 65556\ns_lshl_b32 s20, s20, 13\ns_add_u32 s20, s20, 5308416\n"
"v_add_f32 v59, v59, v133\n"
"v_add_f32 v60, v60, v206\n"
"v_mfma_f32_16x16x32_bf16 v[4:7], v[100:103], a[52:55], v[4:7]\n"
"buffer_load_dwordx4 v[92:95], v236, s[32:35], s20 offen sc1\n"
"v_mfma_f32_16x16x32_bf16 v[4:7], v[104:107], a[56:59], v[4:7]\n"
"v_subrev_f32 v0, s22, v0\n"
"v_subrev_f32 v17, s22, v17\n"
"v_mfma_f32_16x16x32_bf16 v[4:7], v[108:111], a[60:63], v[4:7]\n"
"s_bfe_u32 s20, s89, 65560\ns_lshl_b32 s20, s20, 13\ns_add_u32 s20, s20, 6356992\n"
"v_add_f32 v56, v56, v202\n"
"v_cvt_pk_bf16_f32 v16, v16, v17\n"
"v_mfma_f32_16x16x32_bf16 v[4:7], v[112:115], a[0:3], v[4:7]\n"
"buffer_load_dwordx4 v[244:247], v236, s[32:35], s20 offen sc1\n"
"v_and_b32 v130, s73, v130\n"
"v_add_f32 v59, v59, v137\n"
"v_add_f32 v57, v57, v140\n"
"v_add_f32 v60, v60, v204\n"
"v_lshlrev_b32 v202, 16, v150\n"
"v_lshlrev_b32 v203, 16, v151\n"
"v_cvt_pk_bf16_f32 v0, v0, v1\n"
"v_subrev_f32 v32, s22, v32\n"
"v_mfma_f32_16x16x32_bf16 v[4:7], v[116:119], a[4:7], v[4:7]\n"
"v_add_f32 v63, v63, v131\n"
"v_lshlrev_b32 v206, 16, v148\n"
"v_mfma_f32_16x16x32_bf16 v[4:7], v[120:123], a[8:11], v[4:7]\n"
"s_bfe_u32 s20, s89, 65564\ns_lshl_b32 s20, s20, 13\ns_add_u32 s20, s20, 7405568\n"
"v_and_b32 v151, s73, v151\n"
"v_lshlrev_b32 v204, 16, v152\n"
"v_mfma_f32_16x16x32_bf16 v[4:7], v[224:227], a[12:15], v[4:7]\n"
"buffer_load_dwordx4 v[156:159], v236, s[32:35], s20 offen sc1\n"
"v_mfma_f32_16x16x32_bf16 v[20:23], v[224:227], a[28:31], v[20:23]\n"
"v_add_f32 v57, v57, v144\n"
"v_and_b32 v146, s73, v146\n"
"v_mfma_f32_16x16x32_bf16 v[20:23], v[120:123], a[24:27], v[20:23]\n"
"v_and_b32 v141, s73, v141\n"
"v_cvt_pk_bf16_f32 v1, v2, v3\n"
"v_mfma_f32_16x16x32_bf16 v[20:23], v[116:119], a[20:23], v[20:23]\n"
"v_lshlrev_b32 v207, 16, v155\n"
"v_subrev_f32 v19, s22, v19\n"
"v_mfma_f32_16x16x32_bf16 v[20:23], v[112:115], a[16:19], v[20:23]\n"
"v_add_f32 v62, v62, v203\n"
"v_subrev_f32 v18, s22, v18\n"
"v_mfma_f32_16x16x32_bf16 v[20:23], v[108:111], a[76:79], v[20:23]\n"
"ds_read_b64_tr_b16 v[210:211], v230 offset:51200\n"
"v_add_f32 v61, v61, v130\n"
"v_add_f32 v63, v63, v135\n"
"v_and_b32 v143, s73, v143\n"
"v_and_b32 v148, s73, v148\n"
"v_add_f32 v56, v56, v206\n"
"v_and_b32 v149, s73, v149\n"
"v_add_f32 v56, v56, v204\n"
"v_subrev_f32 v6, s22, v6\n"
"v_subrev_f32 v7, s22, v7\n"
"v_mfma_f32_16x16x32_bf16 v[20:23], v[104:107], a[72:75], v[20:23]\n"
"ds_read_b64_tr_b16 v[182:183], v230 offset:34816\n"
"v_subrev_f32 v35, s22, v35\n"
"v_mfma_f32_16x16x32_bf16 v[20:23], v[100:103], a[68:71], v[20:23]\n"
"v_and_b32 v155, s73, v155\n"
"v_subrev_f32 v4, s22, v4\n"
"v_mfma_f32_16x16x32_bf16 v[20:23], v[96:99], a[64:67], v[20:23]\n"
"ds_read_b64_tr_b16 v[186:187], v234 offset:34816\n"
"v_add_f32 v61, v61, v134\n"
"v_add_f32 v57, v57, v148\n"
"v_and_b32 v152, s73, v152\n"
"v_add_f32 v57, v57, v152\n"
"v_lshlrev_b32 v205, 16, v153\n"
"v_subrev_f32 v5, s22, v5\n"
"v_cvt_pk_bf16_f32 v2, v4, v5\n"
"v_cvt_pk_bf16_f32 v3, v6, v7\n"
"v_subrev_f32 v34, s22, v34\n"
"v_mfma_f32_16x16x32_bf16 v[36:39], v[96:99], a[80:83], v[36:39]\n"
"ds_read_b64_tr_b16 v[208:209], v230 offset:49152\n"
"v_and_b32 v150, s73, v150\n"
"v_mfma_f32_16x16x32_bf16 v[36:39], v[100:103], a[84:87], v[36:39]\n"
"ds_read_b64_tr_b16 v[192:193], v234 offset:40960\n"
"v_add_f32 v61, v61, v138\n"
"v_add_f32 v63, v63, v139\n"
"v_and_b32 v145, s73, v145\n"
"v_permlane16_swap_b32 v0, v2\n"
"v_permlane16_swap_b32 v1, v3\n"
"v_cvt_pk_bf16_f32 v17, v18, v19\n"
"v_mfma_f32_16x16x32_bf16 v[36:39], v[104:107], a[88:91], v[36:39]\n"
"ds_read_b64_tr_b16 v[214:215], v234 offset:51200\n"
"v_add_f32 v59, v59, v141\n"
"v_mfma_f32_16x16x32_bf16 v[36:39], v[108:111], a[92:95], v[36:39]\n"
"ds_read_b64_tr_b16 v[190:191], v230 offset:43008\n"
"v_add_f32 v61, v61, v142\n"
"v_mfma_f32_16x16x32_bf16 v[36:39], v[112:115], a[32:35], v[36:39]\n"
"ds_read_b64_tr_b16 v[184:185], v234 offset:32768\n"
"buffer_store_dwordx4 v[0:3], v238, s[36:39], 0 offen sc0 sc1\n"
"v_lshlrev_b32 v206, 16, v154\n"
"v_subrev_f32 v20, s22, v20\n"
"v_subrev_f32 v21, s22, v21\n"
"v_subrev_f32 v23, s22, v23\n"
"v_cvt_pk_bf16_f32 v18, v20, v21\n"
"v_mfma_f32_16x16x32_bf16 v[36:39], v[116:119], a[36:39], v[36:39]\n"
"ds_read_b64_tr_b16 v[194:195], v234 offset:43008\n"
"v_and_b32 v154, s73, v154\n"
"v_mfma_f32_16x16x32_bf16 v[36:39], v[120:123], a[40:43], v[36:39]\n"
"ds_read_b64_tr_b16 v[188:189], v230 offset:40960\n"
"v_and_b32 v153, s73, v153\n"
"v_mfma_f32_16x16x32_bf16 v[36:39], v[224:227], a[44:47], v[36:39]\n"
"ds_read_b64_tr_b16 v[212:213], v234 offset:49152\n"
"v_add_f32 v59, v59, v145\n"
"v_add_f32 v59, v59, v149\n"
"v_add_f32 v62, v62, v207\n"
"v_subrev_f32 v22, s22, v22\n"
"v_permlane16_swap_b32 v16, v18\n"
"v_subrev_f32 v48, s22, v48\n"
"v_subrev_f32 v50, s22, v50\n"
"v_subrev_f32 v51, s22, v51\n"
"v_mfma_f32_16x16x32_bf16 v[52:55], v[224:227], a[228:231], v[52:55]\n"
"ds_read_b64_tr_b16 v[220:221], v234 offset:57344\n"
"v_cvt_pk_bf16_f32 v19, v22, v23\n"
"v_mfma_f32_16x16x32_bf16 v[52:55], v[120:123], a[224:227], v[52:55]\n"
"v_add_f32 v60, v60, v202\n"
"v_add_f32 v59, v59, v153\n"
"v_mfma_f32_16x16x32_bf16 v[52:55], v[116:119], a[212:215], v[52:55]\n"
"ds_read_b64_tr_b16 v[180:181], v230 offset:32768\n"
"v_add_f32 v63, v63, v143\n"
"v_add_f32 v61, v61, v146\n"
"v_add_f32 v63, v63, v147\n"
"v_add_f32 v61, v61, v150\n"
"v_add_f32 v63, v63, v151\n"
"v_add_f32 v60, v60, v206\n"
"v_cvt_pk_bf16_f32 v56, v56, v57\n"
"v_permlane16_swap_b32 v17, v19\n"
"v_mfma_f32_16x16x32_bf16 v[52:55], v[112:115], a[208:211], v[52:55]\n"
"v_add_f32 v58, v58, v205\n"
"v_subrev_f32 v37, s22, v37\n"
"v_mfma_f32_16x16x32_bf16 v[52:55], v[108:111], a[236:239], v[52:55]\n"
"ds_read_b64_tr_b16 v[218:219], v230 offset:59392\n"
"v_subrev_f32 v36, s22, v36\n"
"v_mfma_f32_16x16x32_bf16 v[52:55], v[104:107], a[232:235], v[52:55]\n"
"ds_read_b64_tr_b16 v[216:217], v230 offset:57344\n"
"buffer_store_dwordx4 v[16:19], v238, s[36:39], s52 offen sc0 sc1\n"
"v_add_f32 v61, v61, v154\n"
"v_add_f32 v63, v63, v155\n"
"v_cvt_pk_bf16_f32 v57, v58, v59\n"
"v_cvt_pk_bf16_f32 v58, v60, v61\n"
"v_subrev_f32 v38, s22, v38\n"
"v_mfma_f32_16x16x32_bf16 v[52:55], v[100:103], a[220:223], v[52:55]\n"
"ds_read_b64_tr_b16 v[222:223], v234 offset:59392\n"
"v_cvt_pk_bf16_f32 v59, v62, v63\n"
"v_mfma_f32_16x16x32_bf16 v[52:55], v[96:99], a[216:219], v[52:55]\n"
"s_waitcnt lgkmcnt(3)\n"
"v_permlane16_swap_b32 v56, v58\n"
"v_mfma_f32_16x16x32_bf16 v[8:11], v[180:183], a[48:51], v[8:11]\n"
"v_permlane16_swap_b32 v57, v59\n"
"v_mfma_f32_16x16x32_bf16 v[8:11], v[184:187], a[52:55], v[8:11]\n"
"buffer_store_dwordx4 v[56:59], v253, s[36:39], 0 offen sc0 sc1\n"
"v_subrev_f32 v33, s22, v33\n"
"v_subrev_f32 v39, s22, v39\n"
"v_cvt_pk_bf16_f32 v32, v32, v33\n"
"v_cvt_pk_bf16_f32 v33, v34, v35\n"
"v_cvt_pk_bf16_f32 v34, v36, v37\n"
"v_cvt_pk_bf16_f32 v35, v38, v39\n"
"v_mfma_f32_16x16x32_bf16 v[8:11], v[188:191], a[56:59], v[8:11]\n"
"v_subrev_f32 v49, s22, v49\n"
"v_subrev_f32 v52, s22, v52\n"
"v_mfma_f32_16x16x32_bf16 v[8:11], v[192:195], a[60:63], v[8:11]\n"
"v_permlane16_swap_b32 v32, v34\n"
"v_mfma_f32_16x16x32_bf16 v[8:11], v[208:211], a[0:3], v[8:11]\n"
"v_permlane16_swap_b32 v33, v35\n"
"v_mfma_f32_16x16x32_bf16 v[8:11], v[212:215], a[4:7], v[8:11]\n"
"s_waitcnt lgkmcnt(1)\n"
"buffer_store_dwordx4 v[32:35], v238, s[36:39], s53 offen sc0 sc1\n"
"v_cvt_pk_bf16_f32 v48, v48, v49\n"
"v_mfma_f32_16x16x32_bf16 v[8:11], v[216:219], a[8:11], v[8:11]\n"
"s_waitcnt lgkmcnt(0)\n"
"ds_read_b64_tr_b16 v[110:111], v235 offset:43008\n"
"v_subrev_f32 v53, s22, v53\n"
"v_mfma_f32_16x16x32_bf16 v[8:11], v[220:223], a[12:15], v[8:11]\n"
"v_cvt_pk_bf16_f32 v49, v50, v51\n"
"v_cvt_pk_bf16_f32 v50, v52, v53\n"
"v_mfma_f32_16x16x32_bf16 v[24:27], v[220:223], a[28:31], v[24:27]\n"
"s_nop 0\n"
"v_permlane16_swap_b32 v48, v50\n"
"v_mfma_f32_16x16x32_bf16 v[24:27], v[216:219], a[24:27], v[24:27]\n"
"ds_read_b64_tr_b16 v[114:115], v231 offset:51200\n"
"v_subrev_f32 v55, s22, v55\n"
"v_mfma_f32_16x16x32_bf16 v[24:27], v[212:215], a[20:23], v[24:27]\n"
"ds_read_b64_tr_b16 v[102:103], v235 offset:34816\n"
"v_subrev_f32 v54, s22, v54\n"
"v_mfma_f32_16x16x32_bf16 v[24:27], v[208:211], a[16:19], v[24:27]\n"
"ds_read_b64_tr_b16 v[112:113], v231 offset:49152\n"
"v_cvt_pk_bf16_f32 v51, v54, v55\n"
"v_mfma_f32_16x16x32_bf16 v[24:27], v[192:195], a[76:79], v[24:27]\n"
"s_nop 0\n"
"v_permlane16_swap_b32 v49, v51\n"
"v_mfma_f32_16x16x32_bf16 v[24:27], v[188:191], a[72:75], v[24:27]\n"
"buffer_store_dwordx4 v[48:51], v252, s[36:39], 0 offen sc0 sc1\n"
"ds_read_b64_tr_b16 v[106:107], v231 offset:43008\n"
"v_mfma_f32_16x16x32_bf16 v[24:27], v[184:187], a[68:71], v[24:27]\n"
"ds_read_b64_tr_b16 v[96:97], v231 offset:32768\n"
"ds_read_b64_tr_b16 v[98:99], v231 offset:34816\n"
"v_mfma_f32_16x16x32_bf16 v[24:27], v[180:183], a[64:67], v[24:27]\n"
"ds_read_b64_tr_b16 v[100:101], v235 offset:32768\n"
"v_and_b32 v163, v160, v161\n"
"v_mfma_f32_16x16x32_bf16 v[40:43], v[180:183], a[80:83], v[40:43]\n"
"ds_read_b64_tr_b16 v[118:119], v235 offset:51200\n"
"v_xor_b32 v162, v160, v163\n"
"v_mfma_f32_16x16x32_bf16 v[40:43], v[184:187], a[84:87], v[40:43]\n"
"ds_read_b64_tr_b16 v[104:105], v231 offset:40960\n"
"v_mfma_f32_16x16x32_bf16 v[40:43], v[188:191], a[88:91], v[40:43]\n"
"ds_read_b64_tr_b16 v[108:109], v235 offset:40960\n"
"v_subrev_f32 v8, s22, v8\n"
"v_mfma_f32_16x16x32_bf16 v[40:43], v[192:195], a[92:95], v[40:43]\n"
"ds_read_b64_tr_b16 v[116:117], v235 offset:49152\n"
"v_mfma_f32_16x16x32_bf16 v[40:43], v[208:211], a[32:35], v[40:43]\n"
"ds_read_b64_tr_b16 v[122:123], v231 offset:59392\n"
"v_subrev_f32 v9, s22, v9\n"
"v_mfma_f32_16x16x32_bf16 v[40:43], v[212:215], a[36:39], v[40:43]\n"
"ds_read_b64_tr_b16 v[120:121], v231 offset:57344\n"
"v_subrev_f32 v10, s22, v10\n"
"v_mfma_f32_16x16x32_bf16 v[40:43], v[216:219], a[40:43], v[40:43]\n"
"ds_read_b64_tr_b16 v[224:225], v235 offset:57344\n"
"ds_read_b64_tr_b16 v[226:227], v235 offset:59392\n"
"v_mfma_f32_16x16x32_bf16 v[40:43], v[220:223], a[44:47], v[40:43]\n"
"s_waitcnt lgkmcnt(8)\n"
"v_subrev_f32 v11, s22, v11\n"
"v_mfma_f32_16x16x32_bf16 v[12:15], v[96:99], a[48:51], v[12:15]\n"
"v_mfma_f32_16x16x32_bf16 v[12:15], v[100:103], a[52:55], v[12:15]\n"
"s_waitcnt lgkmcnt(6)\n"
"v_mov_b32 v160, 0\n"
"v_subrev_f32 v24, s22, v24\n"
"v_mfma_f32_16x16x32_bf16 v[12:15], v[104:107], a[56:59], v[12:15]\n"
"s_waitcnt lgkmcnt(5)\n"
"v_mfma_f32_16x16x32_bf16 v[12:15], v[108:111], a[60:63], v[12:15]\n"
"v_mfma_f32_16x16x32_bf16 v[12:15], v[112:115], a[0:3], v[12:15]\n"
"s_waitcnt lgkmcnt(3)\n"
"v_cvt_pk_bf16_f32 v8, v8, v9\n"
"v_mfma_f32_16x16x32_bf16 v[12:15], v[116:119], a[4:7], v[12:15]\n"
"s_waitcnt lgkmcnt(2)\n"
"v_mfma_f32_16x16x32_bf16 v[12:15], v[120:123], a[8:11], v[12:15]\n"
"s_waitcnt lgkmcnt(0)\n"
"v_mov_b32 v161, 0\n"
"v_subrev_f32 v25, s22, v25\n"
"v_mfma_f32_16x16x32_bf16 v[12:15], v[224:227], a[12:15], v[12:15]\n"
"v_subrev_f32 v26, s22, v26\n"
"s_waitcnt vmcnt(5)\n"
"v_mfma_f32_16x16x32_bf16 v[64:67], v[162:165], v[72:75], 0\n"
"v_subrev_f32 v40, s22, v40\n"
"v_subrev_f32 v43, s22, v43\n"
"v_mfma_f32_16x16x32_bf16 v[64:67], v[162:165], v[76:79], v[64:67]\n"
"v_subrev_f32 v41, s22, v41\n"
"v_mfma_f32_16x16x32_bf16 v[64:67], v[162:165], v[80:83], v[64:67]\n"
"v_mfma_f32_16x16x32_bf16 v[64:67], v[162:165], v[84:87], v[64:67]\n"
"v_subrev_f32 v13, s22, v13\n"
"v_mfma_f32_16x16x32_bf16 v[64:67], v[162:165], v[88:91], v[64:67]\n"
"v_mfma_f32_16x16x32_bf16 v[64:67], v[162:165], v[92:95], v[64:67]\n"
"v_subrev_f32 v15, s22, v15\n"
"v_cvt_pk_bf16_f32 v40, v40, v41\n"
"v_mfma_f32_16x16x32_bf16 v[64:67], v[162:165], v[244:247], v[64:67]\n"
"v_subrev_f32 v14, s22, v14\n"
"v_cvt_pk_bf16_f32 v9, v10, v11\n"
"v_mfma_f32_16x16x32_bf16 v[64:67], v[162:165], v[156:159], v[64:67]\n"
"v_subrev_f32 v12, s22, v12\n"
"v_cvt_pk_bf16_f32 v24, v24, v25\n"
"v_mfma_f32_16x16x32_bf16 v[68:71], v[160:163], v[72:75], 0\n"
"v_cvt_pk_bf16_f32 v10, v12, v13\n"
"v_cvt_pk_bf16_f32 v11, v14, v15\n"
"v_mfma_f32_16x16x32_bf16 v[68:71], v[160:163], v[76:79], v[68:71]\n"
"s_nop 0\n"
"v_permlane16_swap_b32 v9, v11\n"
"v_mfma_f32_16x16x32_bf16 v[68:71], v[160:163], v[80:83], v[68:71]\n"
"v_cvt_pk_bf16_f32 v64, v64, v65\n"
"v_mfma_f32_16x16x32_bf16 v[68:71], v[160:163], v[84:87], v[68:71]\n"
"v_permlane16_swap_b32 v8, v10\n"
"v_mfma_f32_16x16x32_bf16 v[68:71], v[160:163], v[88:91], v[68:71]\n"
"v_subrev_f32 v42, s22, v42\n"
"v_mfma_f32_16x16x32_bf16 v[68:71], v[160:163], v[92:95], v[68:71]\n"
"buffer_store_dwordx4 v[8:11], v239, s[36:39], 0 offen sc0 sc1\n"
"v_mfma_f32_16x16x32_bf16 v[68:71], v[160:163], v[244:247], v[68:71]\n"
"v_mfma_f32_16x16x32_bf16 v[68:71], v[160:163], v[156:159], v[68:71]\n"
"v_cvt_pk_bf16_f32 v65, v66, v67\n"
"v_mfma_f32_16x16x32_bf16 v[28:31], v[224:227], a[28:31], v[28:31]\n"
"v_subrev_f32 v27, s22, v27\n"
"v_mfma_f32_16x16x32_bf16 v[28:31], v[120:123], a[24:27], v[28:31]\n"
"v_mfma_f32_16x16x32_bf16 v[28:31], v[116:119], a[20:23], v[28:31]\n"
"v_mfma_f32_16x16x32_bf16 v[28:31], v[112:115], a[16:19], v[28:31]\n"
"s_nop 1\n"
"v_cvt_pk_bf16_f32 v66, v68, v69\n"
"v_cvt_pk_bf16_f32 v25, v26, v27\n"
"v_mfma_f32_16x16x32_bf16 v[28:31], v[108:111], a[76:79], v[28:31]\n"
"v_permlane16_swap_b32 v64, v66\n"
"v_mfma_f32_16x16x32_bf16 v[28:31], v[104:107], a[72:75], v[28:31]\n"
"v_cvt_pk_bf16_f32 v67, v70, v71\n"
"v_cvt_pk_bf16_f32 v41, v42, v43\n"
"v_mfma_f32_16x16x32_bf16 v[28:31], v[100:103], a[68:71], v[28:31]\n"
"v_permlane16_swap_b32 v65, v67\n"
"v_mfma_f32_16x16x32_bf16 v[28:31], v[96:99], a[64:67], v[28:31]\n"
"buffer_store_dwordx4 v[64:67], v253, s[36:39], 0 offen offset:128 sc0 sc1\n"
"v_mfma_f32_16x16x32_bf16 v[44:47], v[96:99], a[80:83], v[44:47]\n"
"v_mfma_f32_16x16x32_bf16 v[44:47], v[100:103], a[84:87], v[44:47]\n"
"v_mfma_f32_16x16x32_bf16 v[44:47], v[104:107], a[88:91], v[44:47]\n"
"s_nop 3\n"
"v_subrev_f32 v29, s22, v29\n"
"v_subrev_f32 v30, s22, v30\n"
"v_mfma_f32_16x16x32_bf16 v[44:47], v[108:111], a[92:95], v[44:47]\n"
"v_subrev_f32 v28, s22, v28\n"
"v_subrev_f32 v31, s22, v31\n"
"v_mfma_f32_16x16x32_bf16 v[44:47], v[112:115], a[32:35], v[44:47]\n"
"v_cvt_pk_bf16_f32 v26, v28, v29\n"
"v_cvt_pk_bf16_f32 v27, v30, v31\n"
"v_mfma_f32_16x16x32_bf16 v[44:47], v[116:119], a[36:39], v[44:47]\n"
"v_permlane16_swap_b32 v24, v26\n"
"v_mfma_f32_16x16x32_bf16 v[44:47], v[120:123], a[40:43], v[44:47]\n"
"v_permlane16_swap_b32 v25, v27\n"
"v_mfma_f32_16x16x32_bf16 v[44:47], v[224:227], a[44:47], v[44:47]\n"
"buffer_store_dwordx4 v[24:27], v239, s[36:39], s52 offen sc0 sc1\n"
"s_nop 6\n"
"v_subrev_f32 v44, s22, v44\n"
"v_subrev_f32 v45, s22, v45\n"
"v_subrev_f32 v46, s22, v46\n"
"v_subrev_f32 v47, s22, v47\n"
"v_cvt_pk_bf16_f32 v42, v44, v45\n"
"v_cvt_pk_bf16_f32 v43, v46, v47\n"
"s_nop 0\n"
"v_permlane16_swap_b32 v40, v42\n"
"v_permlane16_swap_b32 v41, v43\n"
"buffer_store_dwordx4 v[40:43], v239, s[36:39], s53 offen sc0 sc1\n"
"s_branch LFIN_%=\n"
"LSW_%=:\n"
"s_add_u32 s16, s8, s95\n"
"s_addc_u32 s17, s9, 0\n"
"s_sub_u32 s18, 655360, s95\n"
"s_add_u32 s20, s8, s100\n"
"s_addc_u32 s21, s9, 0\n"
"s_sub_u32 s22, 655360, s100\n"
"s_mov_b32 s56, 0\n"
"s_mov_b32 s55, 0\n"
"s_mov_b32 s58, 0\n"
"s_mov_b32 s59, 163840\n"
"s_mov_b32 s52, 9216\n"
"s_mov_b32 s53, 18432\n"
"s_mov_b32 s54, 27648\n"
"v_lshrrev_b32 v206, 7, v242\n"
"v_mul_u32_u24 v206, 1280, v206\n"
"v_and_b32 v240, 0x7f, v240\n"
"v_add_u32 v240, v206, v240\n"
"buffer_load_dword v252, v240, s[16:19], 0 offen\n"
"s_branch LSWK_%=\n"
"LGP_%=:\n"
"s_mov_b32 s80, 0x40000\n"
"LGPa_%=:\n"
"s_sleep 1\n"
"global_load_dword v200, v201, s[6:7] sc1\n"
"s_sub_u32 s80, s80, 1\n"
"s_cmp_eq_u32 s80, 0\n"
"s_waitcnt vmcnt(0)\n"
"s_cbranch_scc1 LGPK_%=\n"
"v_subrev_u32 v200, s21, v200\n"
"v_cmp_lt_u32 vcc, s69, v200\n"
"s_nop 4\n"
"s_cbranch_vccnz LGPa_%=\n"
"s_branch LGPK_%=\n"
"LGQ_%=:\n"
"s_mov_b32 s80, 0x40000\n"
"LGQa_%=:\n"
"s_sleep 1\n"
"global_load_dword v200, v201, s[6:7] sc1\n"
"s_sub_u32 s80, s80, 1\n"
"s_cmp_eq_u32 s80, 0\n"
"s_waitcnt vmcnt(0)\n"
"s_cbranch_scc1 LGQK_%=\n"
"v_subrev_u32 v200, s21, v200\n"
"v_cmp_lt_u32 vcc, s69, v200\n"
"s_nop 4\n"
"s_cbranch_vccnz LGQa_%=\n"
"s_branch LGQK_%=\n"
"LGR_%=:\n"
"s_mov_b32 s80, 0x40000\n"
"LGRa_%=:\n"
"s_sleep 1\n"
"global_load_dword v200, v201, s[6:7] sc1\n"
"s_sub_u32 s80, s80, 1\n"
"s_cmp_eq_u32 s80, 0\n"
"s_waitcnt vmcnt(0)\n"
"s_cbranch_scc1 LGRK_%=\n"
"v_subrev_u32 v200, s21, v200\n"
"v_cmp_lt_u32 vcc, s69, v200\n"
"s_nop 4\n"
"s_cbranch_vccnz LGRa_%=\n"
"s_branch LGRK_%=\n"
"LGV_%=:\n"
"s_mov_b32 s80, 0x40000\n"
"LGVa_%=:\n"
"s_sleep 1\n"
"global_load_dword v200, v201, s[6:7] sc1\n"
"s_sub_u32 s80, s80, 1\n"
"s_cmp_eq_u32 s80, 0\n"
"s_waitcnt vmcnt(0)\n"
"s_cbranch_scc1 LGVK_%=\n"
"v_subrev_u32 v200, s21, v200\n"
"v_cmp_lt_u32 vcc, s69, v200\n"
"s_nop 4\n"
"s_cbranch_vccnz LGVa_%=\n"
"s_branch LGVK_%=\n"
"LEP_%=:\n"
"s_add_u32 s92, s92, 1\n"
"v_mov_b32 v200, s92\n"
"v_mov_b32 v255, 0\n"
"s_mov_b64 exec, 1\n"
"global_store_dword v255, v200, s[6:7] offset:128 sc1\n"
"s_mov_b64 exec, -1\n"
"s_branch LEPK_%=\n"
"LPCE0_%=:\n"
"s_memrealtime s[80:81]\n"
"s_sub_u32 s82, s67, s66\n"
"s_cmp_gt_i32 s82, 0\n"
"s_cselect_b32 s83, s67, s66\n"
"s_waitcnt lgkmcnt(0)\n"
"s_mov_b32 s63, 0\n"
"s_mov_b32 s62, 126\n"
"s_sub_u32 s82, s83, s80\n"
"s_cmp_gt_i32 s82, 3\n"
"s_cbranch_scc0 LPEE0_%=\n"
"LPDE0_%=:\n"
"s_sleep 1\n"
"s_memrealtime s[80:81]\n"
"s_waitcnt lgkmcnt(0)\n"
"s_sub_u32 s82, s83, s80\n"
"s_cmp_gt_i32 s82, 3\n"
"s_cbranch_scc1 LPDE0_%=\n"
"LPEE0_%=:\n"
"s_add_u32 s66, s66, s72\n"
"s_add_u32 s66, s66, 4\n"
"s_add_u32 s67, s80, s72\n"
"s_sub_u32 s72, s72, 0\n"
"s_max_u32 s72, s72, s62\n"
"s_branch LPKE0_%=\n"
"LPCE1_%=:\n"
"s_memrealtime s[80:81]\n"
"s_sub_u32 s82, s67, s66\n"
"s_cmp_gt_i32 s82, 0\n"
"s_cselect_b32 s83, s67, s66\n"
"s_waitcnt lgkmcnt(0)\n"
"s_sub_u32 s82, s83, s80\n"
"s_cmp_gt_i32 s82, 3\n"
"s_cbranch_scc0 LPEE1_%=\n"
"LPDE1_%=:\n"
"s_sleep 1\n"
"s_memrealtime s[80:81]\n"
"s_waitcnt lgkmcnt(0)\n"
"s_sub_u32 s82, s83, s80\n"
"s_cmp_gt_i32 s82, 3\n"
"s_cbranch_scc1 LPDE1_%=\n"
"LPEE1_%=:\n"
"s_add_u32 s66, s66, s72\n"
"s_add_u32 s66, s66, 4\n"
"s_add_u32 s67, s80, s72\n"
"s_sub_u32 s72, s72, 0\n"
"s_max_u32 s72, s72, s62\n"
"s_branch LPKE1_%=\n"
"LPCT0_%=:\n"
"s_memrealtime s[80:81]\n"
"s_sub_u32 s82, s67, s66\n"
"s_cmp_gt_i32 s82, 0\n"
"s_cselect_b32 s83, s67, s66\n"
"s_waitcnt lgkmcnt(0)\n"
"s_sub_u32 s82, s83, s80\n"
"s_cmp_gt_i32 s82, 3\n"
"s_cbranch_scc0 LPET0_%=\n"
"LPDT0_%=:\n"
"s_sleep 1\n"
"s_memrealtime s[80:81]\n"
"s_waitcnt lgkmcnt(0)\n"
"s_sub_u32 s82, s83, s80\n"
"s_cmp_gt_i32 s82, 3\n"
"s_cbranch_scc1 LPDT0_%=\n"
"LPET0_%=:\n"
"s_add_u32 s66, s66, s72\n"
"s_add_u32 s66, s66, 4\n"
"s_add_u32 s67, s80, s72\n"
"s_sub_u32 s72, s72, 0\n"
"s_max_u32 s72, s72, s62\n"
"s_branch LPKT0_%=\n"
"LPCT1_%=:\n"
"s_memrealtime s[80:81]\n"
"s_sub_u32 s82, s67, s66\n"
"s_cmp_gt_i32 s82, 0\n"
"s_cselect_b32 s83, s67, s66\n"
"s_waitcnt lgkmcnt(0)\n"
"s_sub_u32 s82, s83, s80\n"
"s_cmp_gt_i32 s82, 3\n"
"s_cbranch_scc0 LPET1_%=\n"
"LPDT1_%=:\n"
"s_sleep 1\n"
"s_memrealtime s[80:81]\n"
"s_waitcnt lgkmcnt(0)\n"
"s_sub_u32 s82, s83, s80\n"
"s_cmp_gt_i32 s82, 3\n"
"s_cbranch_scc1 LPDT1_%=\n"
"LPET1_%=:\n"
"s_add_u32 s66, s66, s72\n"
"s_add_u32 s66, s66, 4\n"
"s_add_u32 s67, s80, s72\n"
"s_sub_u32 s72, s72, 0\n"
"s_max_u32 s72, s72, s62\n"
"s_branch LPKT1_%=\n"
"LPCL0_%=:\n"
"s_memrealtime s[80:81]\n"
"s_sub_u32 s82, s67, s66\n"
"s_cmp_gt_i32 s82, 0\n"
"s_cselect_b32 s83, s67, s66\n"
"s_waitcnt lgkmcnt(0)\n"
"s_sub_u32 s82, s80, s67\n"
"s_add_u32 s63, s63, 1\n"
"s_min_i32 s81, s63, 0\n"
"s_cmp_gt_i32 s82, 20\n"
"s_cselect_b32 s63, s63, s81\n"
"s_cmp_ge_i32 s63, 2\n"
"s_cbranch_scc0 LPRL0_%=\n"
"s_mov_b32 s63, -9\n"
"s_add_u32 s62, s62, 10\n"
"s_min_u32 s62, s62, 146\n"
"s_max_u32 s72, s72, s62\n"
"s_add_u32 s83, s80, 200\n"
"s_and_b32 s83, s83, -1\n"
"LPRL0_%=:\n"
"s_sub_u32 s82, s83, s80\n"
"s_cmp_gt_i32 s82, 3\n"
"s_cbranch_scc0 LPEL0_%=\n"
"LPDL0_%=:\n"
"s_sleep 1\n"
"s_memrealtime s[80:81]\n"
"s_waitcnt lgkmcnt(0)\n"
"s_sub_u32 s82, s83, s80\n"
"s_cmp_gt_i32 s82, 3\n"
"s_cbranch_scc1 LPDL0_%=\n"
"LPEL0_%=:\n"
"s_add_u32 s66, s66, s72\n"
"s_add_u32 s66, s66, 4\n"
"s_add_u32 s67, s80, s72\n"
"s_sub_u32 s72, s72, 0\n"
"s_max_u32 s72, s72, s62\n"
"s_branch LPKL0_%=\n"
"LPCL1_%=:\n"
"s_memrealtime s[80:81]\n"
"s_sub_u32 s82, s67, s66\n"
"s_cmp_gt_i32 s82, 0\n"
"s_cselect_b32 s83, s67, s66\n"
"s_waitcnt lgkmcnt(0)\n"
"s_sub_u32 s82, s80, s67\n"
"s_add_u32 s63, s63, 1\n"
"s_min_i32 s81, s63, 0\n"
"s_cmp_gt_i32 s82, 20\n"
"s_cselect_b32 s63, s63, s81\n"
"s_cmp_ge_i32 s63, 2\n"
"s_cbranch_scc0 LPRL1_%=\n"
"s_mov_b32 s63, -9\n"
"s_add_u32 s62, s62, 10\n"
"s_min_u32 s62, s62, 146\n"
"s_max_u32 s72, s72, s62\n"
"s_add_u32 s83, s80, 200\n"
"s_and_b32 s83, s83, -1\n"
"LPRL1_%=:\n"
"s_sub_u32 s82, s83, s80\n"
"s_cmp_gt_i32 s82, 3\n"
"s_cbranch_scc0 LPEL1_%=\n"
"LPDL1_%=:\n"
"s_sleep 1\n"
"s_memrealtime s[80:81]\n"
"s_waitcnt lgkmcnt(0)\n"
"s_sub_u32 s82, s83, s80\n"
"s_cmp_gt_i32 s82, 3\n"
"s_cbranch_scc1 LPDL1_%=\n"
"LPEL1_%=:\n"
"s_add_u32 s66, s66, s72\n"
"s_add_u32 s66, s66, 4\n"
"s_add_u32 s67, s80, s72\n"
"s_sub_u32 s72, s72, 0\n"
"s_max_u32 s72, s72, s62\n"
"s_branch LPKL1_%=\n"
"LPCL2_%=:\n"
"s_memrealtime s[80:81]\n"
"s_sub_u32 s82, s67, s66\n"
"s_cmp_gt_i32 s82, 0\n"
"s_cselect_b32 s83, s67, s66\n"
"s_waitcnt lgkmcnt(0)\n"
"s_sub_u32 s82, s80, s67\n"
"s_add_u32 s63, s63, 1\n"
"s_min_i32 s81, s63, 0\n"
"s_cmp_gt_i32 s82, 20\n"
"s_cselect_b32 s63, s63, s81\n"
"s_cmp_ge_i32 s63, 2\n"
"s_cbranch_scc0 LPRL2_%=\n"
"s_mov_b32 s63, -9\n"
"s_add_u32 s62, s62, 10\n"
"s_min_u32 s62, s62, 146\n"
"s_max_u32 s72, s72, s62\n"
"s_add_u32 s83, s80, 200\n"
"s_and_b32 s83, s83, -1\n"
"LPRL2_%=:\n"
"s_sub_u32 s82, s83, s80\n"
"s_cmp_gt_i32 s82, 3\n"
"s_cbranch_scc0 LPEL2_%=\n"
"LPDL2_%=:\n"
"s_sleep 1\n"
"s_memrealtime s[80:81]\n"
"s_waitcnt lgkmcnt(0)\n"
"s_sub_u32 s82, s83, s80\n"
"s_cmp_gt_i32 s82, 3\n"
"s_cbranch_scc1 LPDL2_%=\n"
"LPEL2_%=:\n"
"s_add_u32 s66, s66, s72\n"
"s_add_u32 s66, s66, 4\n"
"s_add_u32 s67, s80, s72\n"
"s_sub_u32 s72, s72, 0\n"
"s_max_u32 s72, s72, s62\n"
"s_branch LPKL2_%=\n"
"LPCL3_%=:\n"
"s_memrealtime s[80:81]\n"
"s_sub_u32 s82, s67, s66\n"
"s_cmp_gt_i32 s82, 0\n"
"s_cselect_b32 s83, s67, s66\n"
"s_waitcnt lgkmcnt(0)\n"
"s_sub_u32 s82, s80, s67\n"
"s_add_u32 s63, s63, 1\n"
"s_min_i32 s81, s63, 0\n"
"s_cmp_gt_i32 s82, 20\n"
"s_cselect_b32 s63, s63, s81\n"
"s_cmp_ge_i32 s63, 2\n"
"s_cbranch_scc0 LPRL3_%=\n"
"s_mov_b32 s63, -9\n"
"s_add_u32 s62, s62, 10\n"
"s_min_u32 s62, s62, 146\n"
"s_max_u32 s72, s72, s62\n"
"s_add_u32 s83, s80, 200\n"
"s_and_b32 s83, s83, -1\n"
"LPRL3_%=:\n"
"s_sub_u32 s82, s83, s80\n"
"s_cmp_gt_i32 s82, 3\n"
"s_cbranch_scc0 LPEL3_%=\n"
"LPDL3_%=:\n"
"s_sleep 1\n"
"s_memrealtime s[80:81]\n"
"s_waitcnt lgkmcnt(0)\n"
"s_sub_u32 s82, s83, s80\n"
"s_cmp_gt_i32 s82, 3\n"
"s_cbranch_scc1 LPDL3_%=\n"
"LPEL3_%=:\n"
"s_add_u32 s66, s66, s72\n"
"s_add_u32 s66, s66, 4\n"
"s_add_u32 s67, s80, s72\n"
"s_sub_u32 s72, s72, 0\n"
"s_max_u32 s72, s72, s62\n"
"s_branch LPKL3_%=\n"
"LEND_%=:\n"
"LSLOW_%=:\n"
"s_waitcnt vmcnt(0) lgkmcnt(0)\n"
"s_nop 9\n"
"v_bfe_u32 v148, v242, 4, 4\n"
"v_bfe_u32 v149, v242, 8, 2\n"
"v_lshlrev_b32 v144, 8, v148\n"
"v_lshl_add_u32 v144, v149, 4, v144\n"
"v_add_u32 v144, s43, v144\n"
"v_xor_b32 v145, 64, v144\n"
"v_lshlrev_b32 v146, 1, v148\n"
"v_lshl_add_u32 v146, v149, 10, v146\n"
"v_add_u32 v146, s43, v146\n"
"v_xor_b32 v147, 64, v146\n"
"v_mov_b32 v152, 0xff7fffff\n"
"v_mov_b32 v155, 0\n"
"v_mov_b32 v0, 0\n"
"v_mov_b32 v1, 0\n"
"v_mov_b32 v2, 0\n"
"v_mov_b32 v3, 0\n"
"v_mov_b32 v4, 0\n"
"v_mov_b32 v5, 0\n"
"v_mov_b32 v6, 0\n"
"v_mov_b32 v7, 0\n"
"v_mov_b32 v8, 0\n"
"v_mov_b32 v9, 0\n"
"v_mov_b32 v10, 0\n"
"v_mov_b32 v11, 0\n"
"v_mov_b32 v12, 0\n"
"v_mov_b32 v13, 0\n"
"v_mov_b32 v14, 0\n"
"v_mov_b32 v15, 0\n"
"v_mov_b32 v16, 0\n"
"v_mov_b32 v17, 0\n"
"v_mov_b32 v18, 0\n"
"v_mov_b32 v19, 0\n"
"v_mov_b32 v20, 0\n"
"v_mov_b32 v21, 0\n"
"v_mov_b32 v22, 0\n"
"v_mov_b32 v23, 0\n"
"v_mov_b32 v24, 0\n"
"v_mov_b32 v25, 0\n"
"v_mov_b32 v26, 0\n"
"v_mov_b32 v27, 0\n"
"v_mov_b32 v28, 0\n"
"v_mov_b32 v29, 0\n"
"v_mov_b32 v30, 0\n"
"v_mov_b32 v31, 0\n"
"v_mov_b32 v156, 0xff7fffff\n"
"v_mov_b32 v159, 0\n"
"v_mov_b32 v32, 0\n"
"v_mov_b32 v33, 0\n"
"v_mov_b32 v34, 0\n"
"v_mov_b32 v35, 0\n"
"v_mov_b32 v36, 0\n"
"v_mov_b32 v37, 0\n"
"v_mov_b32 v38, 0\n"
"v_mov_b32 v39, 0\n"
"v_mov_b32 v40, 0\n"
"v_mov_b32 v41, 0\n"
"v_mov_b32 v42, 0\n"
"v_mov_b32 v43, 0\n"
"v_mov_b32 v44, 0\n"
"v_mov_b32 v45, 0\n"
"v_mov_b32 v46, 0\n"
"v_mov_b32 v47, 0\n"
"v_mov_b32 v48, 0\n"
"v_mov_b32 v49, 0\n"
"v_mov_b32 v50, 0\n"
"v_mov_b32 v51, 0\n"
"v_mov_b32 v52, 0\n"
"v_mov_b32 v53, 0\n"
"v_mov_b32 v54, 0\n"
"v_mov_b32 v55, 0\n"
"v_mov_b32 v56, 0\n"
"v_mov_b32 v57, 0\n"
"v_mov_b32 v58, 0\n"
"v_mov_b32 v59, 0\n"
"v_mov_b32 v60, 0\n"
"v_mov_b32 v61, 0\n"
"v_mov_b32 v62, 0\n"
"v_mov_b32 v63, 0\n"
"v_mov_b32 v160, 0xff7fffff\n"
"v_mov_b32 v163, 0\n"
"v_mov_b32 v64, 0\n"
"v_mov_b32 v65, 0\n"
"v_mov_b32 v66, 0\n"
"v_mov_b32 v67, 0\n"
"v_mov_b32 v68, 0\n"
"v_mov_b32 v69, 0\n"
"v_mov_b32 v70, 0\n"
"v_mov_b32 v71, 0\n"
"v_mov_b32 v72, 0\n"
"v_mov_b32 v73, 0\n"
"v_mov_b32 v74, 0\n"
"v_mov_b32 v75, 0\n"
"v_mov_b32 v76, 0\n"
"v_mov_b32 v77, 0\n"
"v_mov_b32 v78, 0\n"
"v_mov_b32 v79, 0\n"
"v_mov_b32 v80, 0\n"
"v_mov_b32 v81, 0\n"
"v_mov_b32 v82, 0\n"
"v_mov_b32 v83, 0\n"
"v_mov_b32 v84, 0\n"
"v_mov_b32 v85, 0\n"
"v_mov_b32 v86, 0\n"
"v_mov_b32 v87, 0\n"
"v_mov_b32 v88, 0\n"
"v_mov_b32 v89, 0\n"
"v_mov_b32 v90, 0\n"
"v_mov_b32 v91, 0\n"
"v_mov_b32 v92, 0\n"
"v_mov_b32 v93, 0\n"
"v_mov_b32 v94, 0\n"
"v_mov_b32 v95, 0\n"
"v_mov_b32 v164, 0xff7fffff\n"
"v_mov_b32 v167, 0\n"
"v_accvgpr_write_b32 a208, 0\n"
"v_accvgpr_write_b32 a209, 0\n"
"v_accvgpr_write_b32 a210, 0\n"
"v_accvgpr_write_b32 a211, 0\n"
"v_accvgpr_write_b32 a212, 0\n"
"v_accvgpr_write_b32 a213, 0\n"
"v_accvgpr_write_b32 a214, 0\n"
"v_accvgpr_write_b32 a215, 0\n"
"v_accvgpr_write_b32 a216, 0\n"
"v_accvgpr_write_b32 a217, 0\n"
"v_accvgpr_write_b32 a218, 0\n"
"v_accvgpr_write_b32 a219, 0\n"
"v_accvgpr_write_b32 a220, 0\n"
"v_accvgpr_write_b32 a221, 0\n"
"v_accvgpr_write_b32 a222, 0\n"
"v_accvgpr_write_b32 a223, 0\n"
"v_accvgpr_write_b32 a224, 0\n"
"v_accvgpr_write_b32 a225, 0\n"
"v_accvgpr_write_b32 a226, 0\n"
"v_accvgpr_write_b32 a227, 0\n"
"v_accvgpr_write_b32 a228, 0\n"
"v_accvgpr_write_b32 a229, 0\n"
"v_accvgpr_write_b32 a230, 0\n"
"v_accvgpr_write_b32 a231, 0\n"
"v_accvgpr_write_b32 a232, 0\n"
"v_accvgpr_write_b32 a233, 0\n"
"v_accvgpr_write_b32 a234, 0\n"
"v_accvgpr_write_b32 a235, 0\n"
"v_accvgpr_write_b32 a236, 0\n"
"v_accvgpr_write_b32 a237, 0\n"
"v_accvgpr_write_b32 a238, 0\n"
"v_accvgpr_write_b32 a239, 0\n"
"s_lshl_b32 s80, s47, 16\n"
"s_sub_u32 s80, s80, 261248\n"
"s_add_u32 s81, s80, 4096\n"
"s_mov_b32 s82, 0\n"
"LSLOOP_%=:\n"
"s_lshl_b32 s83, s82, 6\n"
"s_and_b32 s83, s83, 64\n"
"buffer_load_dwordx4 v[128:131], v144, s[84:87], s80 offen offset:0\n"
"buffer_load_dwordx4 v[132:135], v145, s[84:87], s80 offen offset:0\n"
"buffer_load_dwordx4 v[136:139], v144, s[84:87], s80 offen offset:128\n"
"buffer_load_dwordx4 v[140:143], v145, s[84:87], s80 offen offset:128\n"
"s_waitcnt vmcnt(0)\n"
"v_mfma_f32_16x16x32_bf16 v[96:99], v[128:131], a[0:3], 0\n"
"v_mfma_f32_16x16x32_bf16 v[96:99], v[132:135], a[4:7], v[96:99]\n"
"v_mfma_f32_16x16x32_bf16 v[96:99], v[136:139], a[8:11], v[96:99]\n"
"v_mfma_f32_16x16x32_bf16 v[96:99], v[140:143], a[12:15], v[96:99]\n"
"v_mfma_f32_16x16x32_bf16 v[104:107], v[128:131], a[16:19], 0\n"
"v_mfma_f32_16x16x32_bf16 v[104:107], v[132:135], a[20:23], v[104:107]\n"
"v_mfma_f32_16x16x32_bf16 v[104:107], v[136:139], a[24:27], v[104:107]\n"
"v_mfma_f32_16x16x32_bf16 v[104:107], v[140:143], a[28:31], v[104:107]\n"
"v_mfma_f32_16x16x32_bf16 v[112:115], v[128:131], a[32:35], 0\n"
"v_mfma_f32_16x16x32_bf16 v[112:115], v[132:135], a[36:39], v[112:115]\n"
"v_mfma_f32_16x16x32_bf16 v[112:115], v[136:139], a[40:43], v[112:115]\n"
"v_mfma_f32_16x16x32_bf16 v[112:115], v[140:143], a[44:47], v[112:115]\n"
"v_mfma_f32_16x16x32_bf16 v[120:123], v[128:131], a[192:195], 0\n"
"v_mfma_f32_16x16x32_bf16 v[120:123], v[132:135], a[196:199], v[120:123]\n"
"v_mfma_f32_16x16x32_bf16 v[120:123], v[136:139], a[200:203], v[120:123]\n"
"v_mfma_f32_16x16x32_bf16 v[120:123], v[140:143], a[204:207], v[120:123]\n"
"s_nop 7\n"
"buffer_load_dwordx4 v[128:131], v144, s[84:87], s81 offen offset:0\n"
"buffer_load_dwordx4 v[132:135], v145, s[84:87], s81 offen offset:0\n"
"buffer_load_dwordx4 v[136:139], v144, s[84:87], s81 offen offset:128\n"
"buffer_load_dwordx4 v[140:143], v145, s[84:87], s81 offen offset:128\n"
"s_waitcnt vmcnt(0)\n"
"v_mfma_f32_16x16x32_bf16 v[100:103], v[128:131], a[0:3], 0\n"
"v_mfma_f32_16x16x32_bf16 v[100:103], v[132:135], a[4:7], v[100:103]\n"
"v_mfma_f32_16x16x32_bf16 v[100:103], v[136:139], a[8:11], v[100:103]\n"
"v_mfma_f32_16x16x32_bf16 v[100:103], v[140:143], a[12:15], v[100:103]\n"
"v_mfma_f32_16x16x32_bf16 v[108:111], v[128:131], a[16:19], 0\n"
"v_mfma_f32_16x16x32_bf16 v[108:111], v[132:135], a[20:23], v[108:111]\n"
"v_mfma_f32_16x16x32_bf16 v[108:111], v[136:139], a[24:27], v[108:111]\n"
"v_mfma_f32_16x16x32_bf16 v[108:111], v[140:143], a[28:31], v[108:111]\n"
"v_mfma_f32_16x16x32_bf16 v[116:119], v[128:131], a[32:35], 0\n"
"v_mfma_f32_16x16x32_bf16 v[116:119], v[132:135], a[36:39], v[116:119]\n"
"v_mfma_f32_16x16x32_bf16 v[116:119], v[136:139], a[40:43], v[116:119]\n"
"v_mfma_f32_16x16x32_bf16 v[116:119], v[140:143], a[44:47], v[116:119]\n"
"v_mfma_f32_16x16x32_bf16 v[124:127], v[128:131], a[192:195], 0\n"
"v_mfma_f32_16x16x32_bf16 v[124:127], v[132:135], a[196:199], v[124:127]\n"
"v_mfma_f32_16x16x32_bf16 v[124:127], v[136:139], a[200:203], v[124:127]\n"
"v_mfma_f32_16x16x32_bf16 v[124:127], v[140:143], a[204:207], v[124:127]\n"
"s_nop 7\n"
"v_max3_f32 v153, v96, v97, v98\n"
"v_max3_f32 v153, v153, v99, v100\n"
"v_max3_f32 v153, v153, v101, v102\n"
"v_max3_f32 v153, v153, v103, v152\n"
"v_mov_b32 v154, v153\n"
"s_nop 1\n"
"v_permlane32_swap_b32 v153, v154\n"
"v_max_f32 v153, v153, v154\n"
"v_mov_b32 v154, v153\n"
"s_nop 1\n"
"v_permlane16_swap_b32 v153, v154\n"
"v_max_f32 v153, v153, v154\n"
"v_sub_f32 v154, v152, v153\n"
"v_exp_f32 v154, v154\n"
"v_mov_b32 v152, v153\n"
"v_sub_f32 v96, v96, v152\n"
"v_sub_f32 v97, v97, v152\n"
"v_sub_f32 v98, v98, v152\n"
"v_sub_f32 v99, v99, v152\n"
"v_sub_f32 v100, v100, v152\n"
"v_sub_f32 v101, v101, v152\n"
"v_sub_f32 v102, v102, v152\n"
"v_sub_f32 v103, v103, v152\n"
"v_exp_f32 v96, v96\n"
"v_exp_f32 v97, v97\n"
"v_exp_f32 v98, v98\n"
"v_exp_f32 v99, v99\n"
"v_exp_f32 v100, v100\n"
"v_exp_f32 v101, v101\n"
"v_exp_f32 v102, v102\n"
"v_exp_f32 v103, v103\n"
"v_mul_f32 v155, v155, v154\n"
"v_add_f32 v155, v155, v96\n"
"v_add_f32 v155, v155, v97\n"
"v_add_f32 v155, v155, v98\n"
"v_add_f32 v155, v155, v99\n"
"v_add_f32 v155, v155, v100\n"
"v_add_f32 v155, v155, v101\n"
"v_add_f32 v155, v155, v102\n"
"v_add_f32 v155, v155, v103\n"
"v_cvt_pk_bf16_f32 v96, v96, v97\n"
"v_cvt_pk_bf16_f32 v97, v98, v99\n"
"v_cvt_pk_bf16_f32 v98, v100, v101\n"
"v_cvt_pk_bf16_f32 v99, v102, v103\n"
"v_mov_b32_dpp v154, v154 row_ror:12 row_mask:0x2 bank_mask:0xf\n"
"s_nop 1\n"
"v_mov_b32_dpp v154, v154 row_ror:8 row_mask:0x4 bank_mask:0xf\n"
"s_nop 1\n"
"v_mov_b32_dpp v154, v154 row_ror:4 row_mask:0x8 bank_mask:0xf\n"
"s_nop 1\n"
"v_mul_f32_dpp v0, v154, v0 row_newbcast:0 row_mask:0xf bank_mask:0xf\n"
"v_mul_f32_dpp v1, v154, v1 row_newbcast:1 row_mask:0xf bank_mask:0xf\n"
"v_mul_f32_dpp v2, v154, v2 row_newbcast:2 row_mask:0xf bank_mask:0xf\n"
"v_mul_f32_dpp v3, v154, v3 row_newbcast:3 row_mask:0xf bank_mask:0xf\n"
"v_mul_f32_dpp v4, v154, v4 row_newbcast:0 row_mask:0xf bank_mask:0xf\n"
"v_mul_f32_dpp v5, v154, v5 row_newbcast:1 row_mask:0xf bank_mask:0xf\n"
"v_mul_f32_dpp v6, v154, v6 row_newbcast:2 row_mask:0xf bank_mask:0xf\n"
"v_mul_f32_dpp v7, v154, v7 row_newbcast:3 row_mask:0xf bank_mask:0xf\n"
"v_mul_f32_dpp v8, v154, v8 row_newbcast:0 row_mask:0xf bank_mask:0xf\n"
"v_mul_f32_dpp v9, v154, v9 row_newbcast:1 row_mask:0xf bank_mask:0xf\n"
"v_mul_f32_dpp v10, v154, v10 row_newbcast:2 row_mask:0xf bank_mask:0xf\n"
"v_mul_f32_dpp v11, v154, v11 row_newbcast:3 row_mask:0xf bank_mask:0xf\n"
"v_mul_f32_dpp v12, v154, v12 row_newbcast:0 row_mask:0xf bank_mask:0xf\n"
"v_mul_f32_dpp v13, v154, v13 row_newbcast:1 row_mask:0xf bank_mask:0xf\n"
"v_mul_f32_dpp v14, v154, v14 row_newbcast:2 row_mask:0xf bank_mask:0xf\n"
"v_mul_f32_dpp v15, v154, v15 row_newbcast:3 row_mask:0xf bank_mask:0xf\n"
"v_mul_f32_dpp v16, v154, v16 row_newbcast:0 row_mask:0xf bank_mask:0xf\n"
"v_mul_f32_dpp v17, v154, v17 row_newbcast:1 row_mask:0xf bank_mask:0xf\n"
"v_mul_f32_dpp v18, v154, v18 row_newbcast:2 row_mask:0xf bank_mask:0xf\n"
"v_mul_f32_dpp v19, v154, v19 row_newbcast:3 row_mask:0xf bank_mask:0xf\n"
"v_mul_f32_dpp v20, v154, v20 row_newbcast:0 row_mask:0xf bank_mask:0xf\n"
"v_mul_f32_dpp v21, v154, v21 row_newbcast:1 row_mask:0xf bank_mask:0xf\n"
"v_mul_f32_dpp v22, v154, v22 row_newbcast:2 row_mask:0xf bank_mask:0xf\n"
"v_mul_f32_dpp v23, v154, v23 row_newbcast:3 row_mask:0xf bank_mask:0xf\n"
"v_mul_f32_dpp v24, v154, v24 row_newbcast:0 row_mask:0xf bank_mask:0xf\n"
"v_mul_f32_dpp v25, v154, v25 row_newbcast:1 row_mask:0xf bank_mask:0xf\n"
"v_mul_f32_dpp v26, v154, v26 row_newbcast:2 row_mask:0xf bank_mask:0xf\n"
"v_mul_f32_dpp v27, v154, v27 row_newbcast:3 row_mask:0xf bank_mask:0xf\n"
"v_mul_f32_dpp v28, v154, v28 row_newbcast:0 row_mask:0xf bank_mask:0xf\n"
"v_mul_f32_dpp v29, v154, v29 row_newbcast:1 row_mask:0xf bank_mask:0xf\n"
"v_mul_f32_dpp v30, v154, v30 row_newbcast:2 row_mask:0xf bank_mask:0xf\n"
"v_mul_f32_dpp v31, v154, v31 row_newbcast:3 row_mask:0xf bank_mask:0xf\n"
"v_max3_f32 v157, v104, v105, v106\n"
"v_max3_f32 v157, v157, v107, v108\n"
"v_max3_f32 v157, v157, v109, v110\n"
"v_max3_f32 v157, v157, v111, v156\n"
"v_mov_b32 v158, v157\n"
"s_nop 1\n"
"v_permlane32_swap_b32 v157, v158\n"
"v_max_f32 v157, v157, v158\n"
"v_mov_b32 v158, v157\n"
"s_nop 1\n"
"v_permlane16_swap_b32 v157, v158\n"
"v_max_f32 v157, v157, v158\n"
"v_sub_f32 v158, v156, v157\n"
"v_exp_f32 v158, v158\n"
"v_mov_b32 v156, v157\n"
"v_sub_f32 v104, v104, v156\n"
"v_sub_f32 v105, v105, v156\n"
"v_sub_f32 v106, v106, v156\n"
"v_sub_f32 v107, v107, v156\n"
"v_sub_f32 v108, v108, v156\n"
"v_sub_f32 v109, v109, v156\n"
"v_sub_f32 v110, v110, v156\n"
"v_sub_f32 v111, v111, v156\n"
"v_exp_f32 v104, v104\n"
"v_exp_f32 v105, v105\n"
"v_exp_f32 v106, v106\n"
"v_exp_f32 v107, v107\n"
"v_exp_f32 v108, v108\n"
"v_exp_f32 v109, v109\n"
"v_exp_f32 v110, v110\n"
"v_exp_f32 v111, v111\n"
"v_mul_f32 v159, v159, v158\n"
"v_add_f32 v159, v159, v104\n"
"v_add_f32 v159, v159, v105\n"
"v_add_f32 v159, v159, v106\n"
"v_add_f32 v159, v159, v107\n"
"v_add_f32 v159, v159, v108\n"
"v_add_f32 v159, v159, v109\n"
"v_add_f32 v159, v159, v110\n"
"v_add_f32 v159, v159, v111\n"
"v_cvt_pk_bf16_f32 v104, v104, v105\n"
"v_cvt_pk_bf16_f32 v105, v106, v107\n"
"v_cvt_pk_bf16_f32 v106, v108, v109\n"
"v_cvt_pk_bf16_f32 v107, v110, v111\n"
"v_mov_b32_dpp v158, v158 row_ror:12 row_mask:0x2 bank_mask:0xf\n"
"s_nop 1\n"
"v_mov_b32_dpp v158, v158 row_ror:8 row_mask:0x4 bank_mask:0xf\n"
"s_nop 1\n"
"v_mov_b32_dpp v158, v158 row_ror:4 row_mask:0x8 bank_mask:0xf\n"
"s_nop 1\n"
"v_mul_f32_dpp v32, v158, v32 row_newbcast:0 row_mask:0xf bank_mask:0xf\n"
"v_mul_f32_dpp v33, v158, v33 row_newbcast:1 row_mask:0xf bank_mask:0xf\n"
"v_mul_f32_dpp v34, v158, v34 row_newbcast:2 row_mask:0xf bank_mask:0xf\n"
"v_mul_f32_dpp v35, v158, v35 row_newbcast:3 row_mask:0xf bank_mask:0xf\n"
"v_mul_f32_dpp v36, v158, v36 row_newbcast:0 row_mask:0xf bank_mask:0xf\n"
"v_mul_f32_dpp v37, v158, v37 row_newbcast:1 row_mask:0xf bank_mask:0xf\n"
"v_mul_f32_dpp v38, v158, v38 row_newbcast:2 row_mask:0xf bank_mask:0xf\n"
"v_mul_f32_dpp v39, v158, v39 row_newbcast:3 row_mask:0xf bank_mask:0xf\n"
"v_mul_f32_dpp v40, v158, v40 row_newbcast:0 row_mask:0xf bank_mask:0xf\n"
"v_mul_f32_dpp v41, v158, v41 row_newbcast:1 row_mask:0xf bank_mask:0xf\n"
"v_mul_f32_dpp v42, v158, v42 row_newbcast:2 row_mask:0xf bank_mask:0xf\n"
"v_mul_f32_dpp v43, v158, v43 row_newbcast:3 row_mask:0xf bank_mask:0xf\n"
"v_mul_f32_dpp v44, v158, v44 row_newbcast:0 row_mask:0xf bank_mask:0xf\n"
"v_mul_f32_dpp v45, v158, v45 row_newbcast:1 row_mask:0xf bank_mask:0xf\n"
"v_mul_f32_dpp v46, v158, v46 row_newbcast:2 row_mask:0xf bank_mask:0xf\n"
"v_mul_f32_dpp v47, v158, v47 row_newbcast:3 row_mask:0xf bank_mask:0xf\n"
"v_mul_f32_dpp v48, v158, v48 row_newbcast:0 row_mask:0xf bank_mask:0xf\n"
"v_mul_f32_dpp v49, v158, v49 row_newbcast:1 row_mask:0xf bank_mask:0xf\n"
"v_mul_f32_dpp v50, v158, v50 row_newbcast:2 row_mask:0xf bank_mask:0xf\n"
"v_mul_f32_dpp v51, v158, v51 row_newbcast:3 row_mask:0xf bank_mask:0xf\n"
"v_mul_f32_dpp v52, v158, v52 row_newbcast:0 row_mask:0xf bank_mask:0xf\n"
"v_mul_f32_dpp v53, v158, v53 row_newbcast:1 row_mask:0xf bank_mask:0xf\n"
"v_mul_f32_dpp v54, v158, v54 row_newbcast:2 row_mask:0xf bank_mask:0xf\n"
"v_mul_f32_dpp v55, v158, v55 row_newbcast:3 row_mask:0xf bank_mask:0xf\n"
"v_mul_f32_dpp v56, v158, v56 row_newbcast:0 row_mask:0xf bank_mask:0xf\n"
"v_mul_f32_dpp v57, v158, v57 row_newbcast:1 row_mask:0xf bank_mask:0xf\n"
"v_mul_f32_dpp v58, v158, v58 row_newbcast:2 row_mask:0xf bank_mask:0xf\n"
"v_mul_f32_dpp v59, v158, v59 row_newbcast:3 row_mask:0xf bank_mask:0xf\n"
"v_mul_f32_dpp v60, v158, v60 row_newbcast:0 row_mask:0xf bank_mask:0xf\n"
"v_mul_f32_dpp v61, v158, v61 row_newbcast:1 row_mask:0xf bank_mask:0xf\n"
"v_mul_f32_dpp v62, v158, v62 row_newbcast:2 row_mask:0xf bank_mask:0xf\n"
"v_mul_f32_dpp v63, v158, v63 row_newbcast:3 row_mask:0xf bank_mask:0xf\n"
"v_max3_f32 v161, v112, v113, v114\n"
"v_max3_f32 v161, v161, v115, v116\n"
"v_max3_f32 v161, v161, v117, v118\n"
"v_max3_f32 v161, v161, v119, v160\n"
"v_mov_b32 v162, v161\n"
"s_nop 1\n"
"v_permlane32_swap_b32 v161, v162\n"
"v_max_f32 v161, v161, v162\n"
"v_mov_b32 v162, v161\n"
"s_nop 1\n"
"v_permlane16_swap_b32 v161, v162\n"
"v_max_f32 v161, v161, v162\n"
"v_sub_f32 v162, v160, v161\n"
"v_exp_f32 v162, v162\n"
"v_mov_b32 v160, v161\n"
"v_sub_f32 v112, v112, v160\n"
"v_sub_f32 v113, v113, v160\n"
"v_sub_f32 v114, v114, v160\n"
"v_sub_f32 v115, v115, v160\n"
"v_sub_f32 v116, v116, v160\n"
"v_sub_f32 v117, v117, v160\n"
"v_sub_f32 v118, v118, v160\n"
"v_sub_f32 v119, v119, v160\n"
"v_exp_f32 v112, v112\n"
"v_exp_f32 v113, v113\n"
"v_exp_f32 v114, v114\n"
"v_exp_f32 v115, v115\n"
"v_exp_f32 v116, v116\n"
"v_exp_f32 v117, v117\n"
"v_exp_f32 v118, v118\n"
"v_exp_f32 v119, v119\n"
"v_mul_f32 v163, v163, v162\n"
"v_add_f32 v163, v163, v112\n"
"v_add_f32 v163, v163, v113\n"
"v_add_f32 v163, v163, v114\n"
"v_add_f32 v163, v163, v115\n"
"v_add_f32 v163, v163, v116\n"
"v_add_f32 v163, v163, v117\n"
"v_add_f32 v163, v163, v118\n"
"v_add_f32 v163, v163, v119\n"
"v_cvt_pk_bf16_f32 v112, v112, v113\n"
"v_cvt_pk_bf16_f32 v113, v114, v115\n"
"v_cvt_pk_bf16_f32 v114, v116, v117\n"
"v_cvt_pk_bf16_f32 v115, v118, v119\n"
"v_mov_b32_dpp v162, v162 row_ror:12 row_mask:0x2 bank_mask:0xf\n"
"s_nop 1\n"
"v_mov_b32_dpp v162, v162 row_ror:8 row_mask:0x4 bank_mask:0xf\n"
"s_nop 1\n"
"v_mov_b32_dpp v162, v162 row_ror:4 row_mask:0x8 bank_mask:0xf\n"
"s_nop 1\n"
"v_mul_f32_dpp v64, v162, v64 row_newbcast:0 row_mask:0xf bank_mask:0xf\n"
"v_mul_f32_dpp v65, v162, v65 row_newbcast:1 row_mask:0xf bank_mask:0xf\n"
"v_mul_f32_dpp v66, v162, v66 row_newbcast:2 row_mask:0xf bank_mask:0xf\n"
"v_mul_f32_dpp v67, v162, v67 row_newbcast:3 row_mask:0xf bank_mask:0xf\n"
"v_mul_f32_dpp v68, v162, v68 row_newbcast:0 row_mask:0xf bank_mask:0xf\n"
"v_mul_f32_dpp v69, v162, v69 row_newbcast:1 row_mask:0xf bank_mask:0xf\n"
"v_mul_f32_dpp v70, v162, v70 row_newbcast:2 row_mask:0xf bank_mask:0xf\n"
"v_mul_f32_dpp v71, v162, v71 row_newbcast:3 row_mask:0xf bank_mask:0xf\n"
"v_mul_f32_dpp v72, v162, v72 row_newbcast:0 row_mask:0xf bank_mask:0xf\n"
"v_mul_f32_dpp v73, v162, v73 row_newbcast:1 row_mask:0xf bank_mask:0xf\n"
"v_mul_f32_dpp v74, v162, v74 row_newbcast:2 row_mask:0xf bank_mask:0xf\n"
"v_mul_f32_dpp v75, v162, v75 row_newbcast:3 row_mask:0xf bank_mask:0xf\n"
"v_mul_f32_dpp v76, v162, v76 row_newbcast:0 row_mask:0xf bank_mask:0xf\n"
"v_mul_f32_dpp v77, v162, v77 row_newbcast:1 row_mask:0xf bank_mask:0xf\n"
"v_mul_f32_dpp v78, v162, v78 row_newbcast:2 row_mask:0xf bank_mask:0xf\n"
"v_mul_f32_dpp v79, v162, v79 row_newbcast:3 row_mask:0xf bank_mask:0xf\n"
"v_mul_f32_dpp v80, v162, v80 row_newbcast:0 row_mask:0xf bank_mask:0xf\n"
"v_mul_f32_dpp v81, v162, v81 row_newbcast:1 row_mask:0xf bank_mask:0xf\n"
"v_mul_f32_dpp v82, v162, v82 row_newbcast:2 row_mask:0xf bank_mask:0xf\n"
"v_mul_f32_dpp v83, v162, v83 row_newbcast:3 row_mask:0xf bank_mask:0xf\n"
"v_mul_f32_dpp v84, v162, v84 row_newbcast:0 row_mask:0xf bank_mask:0xf\n"
"v_mul_f32_dpp v85, v162, v85 row_newbcast:1 row_mask:0xf bank_mask:0xf\n"
"v_mul_f32_dpp v86, v162, v86 row_newbcast:2 row_mask:0xf bank_mask:0xf\n"
"v_mul_f32_dpp v87, v162, v87 row_newbcast:3 row_mask:0xf bank_mask:0xf\n"
"v_mul_f32_dpp v88, v162, v88 row_newbcast:0 row_mask:0xf bank_mask:0xf\n"
"v_mul_f32_dpp v89, v162, v89 row_newbcast:1 row_mask:0xf bank_mask:0xf\n"
"v_mul_f32_dpp v90, v162, v90 row_newbcast:2 row_mask:0xf bank_mask:0xf\n"
"v_mul_f32_dpp v91, v162, v91 row_newbcast:3 row_mask:0xf bank_mask:0xf\n"
"v_mul_f32_dpp v92, v162, v92 row_newbcast:0 row_mask:0xf bank_mask:0xf\n"
"v_mul_f32_dpp v93, v162, v93 row_newbcast:1 row_mask:0xf bank_mask:0xf\n"
"v_mul_f32_dpp v94, v162, v94 row_newbcast:2 row_mask:0xf bank_mask:0xf\n"
"v_mul_f32_dpp v95, v162, v95 row_newbcast:3 row_mask:0xf bank_mask:0xf\n"
"s_cmp_lg_u32 s83, s43\n"
"s_cbranch_scc1 LSK3A_%=\n"
"v_max3_f32 v165, v120, v121, v122\n"
"v_max3_f32 v165, v165, v123, v124\n"
"v_max3_f32 v165, v165, v125, v126\n"
"v_max3_f32 v165, v165, v127, v164\n"
"v_mov_b32 v166, v165\n"
"s_nop 1\n"
"v_permlane32_swap_b32 v165, v166\n"
"v_max_f32 v165, v165, v166\n"
"v_mov_b32 v166, v165\n"
"s_nop 1\n"
"v_permlane16_swap_b32 v165, v166\n"
"v_max_f32 v165, v165, v166\n"
"v_sub_f32 v166, v164, v165\n"
"v_exp_f32 v166, v166\n"
"v_mov_b32 v164, v165\n"
"v_sub_f32 v120, v120, v164\n"
"v_sub_f32 v121, v121, v164\n"
"v_sub_f32 v122, v122, v164\n"
"v_sub_f32 v123, v123, v164\n"
"v_sub_f32 v124, v124, v164\n"
"v_sub_f32 v125, v125, v164\n"
"v_sub_f32 v126, v126, v164\n"
"v_sub_f32 v127, v127, v164\n"
"v_exp_f32 v120, v120\n"
"v_exp_f32 v121, v121\n"
"v_exp_f32 v122, v122\n"
"v_exp_f32 v123, v123\n"
"v_exp_f32 v124, v124\n"
"v_exp_f32 v125, v125\n"
"v_exp_f32 v126, v126\n"
"v_exp_f32 v127, v127\n"
"v_mul_f32 v167, v167, v166\n"
"v_add_f32 v167, v167, v120\n"
"v_add_f32 v167, v167, v121\n"
"v_add_f32 v167, v167, v122\n"
"v_add_f32 v167, v167, v123\n"
"v_add_f32 v167, v167, v124\n"
"v_add_f32 v167, v167, v125\n"
"v_add_f32 v167, v167, v126\n"
"v_add_f32 v167, v167, v127\n"
"v_cvt_pk_bf16_f32 v120, v120, v121\n"
"v_cvt_pk_bf16_f32 v121, v122, v123\n"
"v_cvt_pk_bf16_f32 v122, v124, v125\n"
"v_cvt_pk_bf16_f32 v123, v126, v127\n"
"v_mov_b32_dpp v166, v166 row_ror:12 row_mask:0x2 bank_mask:0xf\n"
"s_nop 1\n"
"v_mov_b32_dpp v166, v166 row_ror:8 row_mask:0x4 bank_mask:0xf\n"
"s_nop 1\n"
"v_mov_b32_dpp v166, v166 row_ror:4 row_mask:0x8 bank_mask:0xf\n"
"v_accvgpr_read_b32 v124, a208\n"
"s_nop 1\n"
"v_mul_f32_dpp v124, v166, v124 row_newbcast:0 row_mask:0xf bank_mask:0xf\n"
"v_accvgpr_write_b32 a208, v124\n"
"v_accvgpr_read_b32 v125, a209\n"
"s_nop 1\n"
"v_mul_f32_dpp v125, v166, v125 row_newbcast:1 row_mask:0xf bank_mask:0xf\n"
"v_accvgpr_write_b32 a209, v125\n"
"v_accvgpr_read_b32 v126, a210\n"
"s_nop 1\n"
"v_mul_f32_dpp v126, v166, v126 row_newbcast:2 row_mask:0xf bank_mask:0xf\n"
"v_accvgpr_write_b32 a210, v126\n"
"v_accvgpr_read_b32 v127, a211\n"
"s_nop 1\n"
"v_mul_f32_dpp v127, v166, v127 row_newbcast:3 row_mask:0xf bank_mask:0xf\n"
"v_accvgpr_write_b32 a211, v127\n"
"v_accvgpr_read_b32 v124, a212\n"
"s_nop 1\n"
"v_mul_f32_dpp v124, v166, v124 row_newbcast:0 row_mask:0xf bank_mask:0xf\n"
"v_accvgpr_write_b32 a212, v124\n"
"v_accvgpr_read_b32 v125, a213\n"
"s_nop 1\n"
"v_mul_f32_dpp v125, v166, v125 row_newbcast:1 row_mask:0xf bank_mask:0xf\n"
"v_accvgpr_write_b32 a213, v125\n"
"v_accvgpr_read_b32 v126, a214\n"
"s_nop 1\n"
"v_mul_f32_dpp v126, v166, v126 row_newbcast:2 row_mask:0xf bank_mask:0xf\n"
"v_accvgpr_write_b32 a214, v126\n"
"v_accvgpr_read_b32 v127, a215\n"
"s_nop 1\n"
"v_mul_f32_dpp v127, v166, v127 row_newbcast:3 row_mask:0xf bank_mask:0xf\n"
"v_accvgpr_write_b32 a215, v127\n"
"v_accvgpr_read_b32 v124, a216\n"
"s_nop 1\n"
"v_mul_f32_dpp v124, v166, v124 row_newbcast:0 row_mask:0xf bank_mask:0xf\n"
"v_accvgpr_write_b32 a216, v124\n"
"v_accvgpr_read_b32 v125, a217\n"
"s_nop 1\n"
"v_mul_f32_dpp v125, v166, v125 row_newbcast:1 row_mask:0xf bank_mask:0xf\n"
"v_accvgpr_write_b32 a217, v125\n"
"v_accvgpr_read_b32 v126, a218\n"
"s_nop 1\n"
"v_mul_f32_dpp v126, v166, v126 row_newbcast:2 row_mask:0xf bank_mask:0xf\n"
"v_accvgpr_write_b32 a218, v126\n"
"v_accvgpr_read_b32 v127, a219\n"
"s_nop 1\n"
"v_mul_f32_dpp v127, v166, v127 row_newbcast:3 row_mask:0xf bank_mask:0xf\n"
"v_accvgpr_write_b32 a219, v127\n"
"v_accvgpr_read_b32 v124, a220\n"
"s_nop 1\n"
"v_mul_f32_dpp v124, v166, v124 row_newbcast:0 row_mask:0xf bank_mask:0xf\n"
"v_accvgpr_write_b32 a220, v124\n"
"v_accvgpr_read_b32 v125, a221\n"
"s_nop 1\n"
"v_mul_f32_dpp v125, v166, v125 row_newbcast:1 row_mask:0xf bank_mask:0xf\n"
"v_accvgpr_write_b32 a221, v125\n"
"v_accvgpr_read_b32 v126, a222\n"
"s_nop 1\n"
"v_mul_f32_dpp v126, v166, v126 row_newbcast:2 row_mask:0xf bank_mask:0xf\n"
"v_accvgpr_write_b32 a222, v126\n"
"v_accvgpr_read_b32 v127, a223\n"
"s_nop 1\n"
"v_mul_f32_dpp v127, v166, v127 row_newbcast:3 row_mask:0xf bank_mask:0xf\n"
"v_accvgpr_write_b32 a223, v127\n"
"v_accvgpr_read_b32 v124, a224\n"
"s_nop 1\n"
"v_mul_f32_dpp v124, v166, v124 row_newbcast:0 row_mask:0xf bank_mask:0xf\n"
"v_accvgpr_write_b32 a224, v124\n"
"v_accvgpr_read_b32 v125, a225\n"
"s_nop 1\n"
"v_mul_f32_dpp v125, v166, v125 row_newbcast:1 row_mask:0xf bank_mask:0xf\n"
"v_accvgpr_write_b32 a225, v125\n"
"v_accvgpr_read_b32 v126, a226\n"
"s_nop 1\n"
"v_mul_f32_dpp v126, v166, v126 row_newbcast:2 row_mask:0xf bank_mask:0xf\n"
"v_accvgpr_write_b32 a226, v126\n"
"v_accvgpr_read_b32 v127, a227\n"
"s_nop 1\n"
"v_mul_f32_dpp v127, v166, v127 row_newbcast:3 row_mask:0xf bank_mask:0xf\n"
"v_accvgpr_write_b32 a227, v127\n"
"v_accvgpr_read_b32 v124, a228\n"
"s_nop 1\n"
"v_mul_f32_dpp v124, v166, v124 row_newbcast:0 row_mask:0xf bank_mask:0xf\n"
"v_accvgpr_write_b32 a228, v124\n"
"v_accvgpr_read_b32 v125, a229\n"
"s_nop 1\n"
"v_mul_f32_dpp v125, v166, v125 row_newbcast:1 row_mask:0xf bank_mask:0xf\n"
"v_accvgpr_write_b32 a229, v125\n"
"v_accvgpr_read_b32 v126, a230\n"
"s_nop 1\n"
"v_mul_f32_dpp v126, v166, v126 row_newbcast:2 row_mask:0xf bank_mask:0xf\n"
"v_accvgpr_write_b32 a230, v126\n"
"v_accvgpr_read_b32 v127, a231\n"
"s_nop 1\n"
"v_mul_f32_dpp v127, v166, v127 row_newbcast:3 row_mask:0xf bank_mask:0xf\n"
"v_accvgpr_write_b32 a231, v127\n"
"v_accvgpr_read_b32 v124, a232\n"
"s_nop 1\n"
"v_mul_f32_dpp v124, v166, v124 row_newbcast:0 row_mask:0xf bank_mask:0xf\n"
"v_accvgpr_write_b32 a232, v124\n"
"v_accvgpr_read_b32 v125, a233\n"
"s_nop 1\n"
"v_mul_f32_dpp v125, v166, v125 row_newbcast:1 row_mask:0xf bank_mask:0xf\n"
"v_accvgpr_write_b32 a233, v125\n"
"v_accvgpr_read_b32 v126, a234\n"
"s_nop 1\n"
"v_mul_f32_dpp v126, v166, v126 row_newbcast:2 row_mask:0xf bank_mask:0xf\n"
"v_accvgpr_write_b32 a234, v126\n"
"v_accvgpr_read_b32 v127, a235\n"
"s_nop 1\n"
"v_mul_f32_dpp v127, v166, v127 row_newbcast:3 row_mask:0xf bank_mask:0xf\n"
"v_accvgpr_write_b32 a235, v127\n"
"v_accvgpr_read_b32 v124, a236\n"
"s_nop 1\n"
"v_mul_f32_dpp v124, v166, v124 row_newbcast:0 row_mask:0xf bank_mask:0xf\n"
"v_accvgpr_write_b32 a236, v124\n"
"v_accvgpr_read_b32 v125, a237\n"
"s_nop 1\n"
"v_mul_f32_dpp v125, v166, v125 row_newbcast:1 row_mask:0xf bank_mask:0xf\n"
"v_accvgpr_write_b32 a237, v125\n"
"v_accvgpr_read_b32 v126, a238\n"
"s_nop 1\n"
"v_mul_f32_dpp v126, v166, v126 row_newbcast:2 row_mask:0xf bank_mask:0xf\n"
"v_accvgpr_write_b32 a238, v126\n"
"v_accvgpr_read_b32 v127, a239\n"
"s_nop 1\n"
"v_mul_f32_dpp v127, v166, v127 row_newbcast:3 row_mask:0xf bank_mask:0xf\n"
"v_accvgpr_write_b32 a239, v127\n"
"LSK3A_%=:\n"
"s_nop 3\n"
"buffer_load_ushort v128, v146, s[88:91], s80 offen offset:0\n"
"buffer_load_ushort v129, v146, s[88:91], s80 offen offset:256\n"
"buffer_load_ushort v130, v146, s[88:91], s80 offen offset:512\n"
"buffer_load_ushort v131, v146, s[88:91], s80 offen offset:768\n"
"buffer_load_ushort v132, v146, s[88:91], s81 offen offset:0\n"
"buffer_load_ushort v133, v146, s[88:91], s81 offen offset:256\n"
"buffer_load_ushort v134, v146, s[88:91], s81 offen offset:512\n"
"buffer_load_ushort v135, v146, s[88:91], s81 offen offset:768\n"
"buffer_load_ushort v136, v146, s[88:91], s80 offen offset:32\n"
"buffer_load_ushort v137, v146, s[88:91], s80 offen offset:288\n"
"buffer_load_ushort v138, v146, s[88:91], s80 offen offset:544\n"
"buffer_load_ushort v139, v146, s[88:91], s80 offen offset:800\n"
"buffer_load_ushort v140, v146, s[88:91], s81 offen offset:32\n"
"buffer_load_ushort v141, v146, s[88:91], s81 offen offset:288\n"
"buffer_load_ushort v142, v146, s[88:91], s81 offen offset:544\n"
"buffer_load_ushort v143, v146, s[88:91], s81 offen offset:800\n"
"s_waitcnt vmcnt(0)\n"
"v_lshl_or_b32 v128, v129, 16, v128\n"
"v_lshl_or_b32 v129, v131, 16, v130\n"
"v_lshl_or_b32 v130, v133, 16, v132\n"
"v_lshl_or_b32 v131, v135, 16, v134\n"
"v_lshl_or_b32 v136, v137, 16, v136\n"
"v_lshl_or_b32 v137, v139, 16, v138\n"
"v_lshl_or_b32 v138, v141, 16, v140\n"
"v_lshl_or_b32 v139, v143, 16, v142\n"
"v_mfma_f32_16x16x32_bf16 v[0:3], v[96:99], v[128:131], v[0:3]\n"
"s_nop 0\n"
"v_mfma_f32_16x16x32_bf16 v[4:7], v[96:99], v[136:139], v[4:7]\n"
"v_mfma_f32_16x16x32_bf16 v[32:35], v[104:107], v[128:131], v[32:35]\n"
"v_mfma_f32_16x16x32_bf16 v[36:39], v[104:107], v[136:139], v[36:39]\n"
"v_mfma_f32_16x16x32_bf16 v[64:67], v[112:115], v[128:131], v[64:67]\n"
"v_mfma_f32_16x16x32_bf16 v[68:71], v[112:115], v[136:139], v[68:71]\n"
"s_cmp_lg_u32 s83, s43\n"
"s_cbranch_scc1 LSK3B0_%=\n"
"v_mfma_f32_16x16x32_bf16 a[208:211], v[120:123], v[128:131], a[208:211]\n"
"v_mfma_f32_16x16x32_bf16 a[212:215], v[120:123], v[136:139], a[212:215]\n"
"LSK3B0_%=:\n"
"s_nop 3\n"
"buffer_load_ushort v128, v147, s[88:91], s80 offen offset:0\n"
"buffer_load_ushort v129, v147, s[88:91], s80 offen offset:256\n"
"buffer_load_ushort v130, v147, s[88:91], s80 offen offset:512\n"
"buffer_load_ushort v131, v147, s[88:91], s80 offen offset:768\n"
"buffer_load_ushort v132, v147, s[88:91], s81 offen offset:0\n"
"buffer_load_ushort v133, v147, s[88:91], s81 offen offset:256\n"
"buffer_load_ushort v134, v147, s[88:91], s81 offen offset:512\n"
"buffer_load_ushort v135, v147, s[88:91], s81 offen offset:768\n"
"buffer_load_ushort v136, v147, s[88:91], s80 offen offset:32\n"
"buffer_load_ushort v137, v147, s[88:91], s80 offen offset:288\n"
"buffer_load_ushort v138, v147, s[88:91], s80 offen offset:544\n"
"buffer_load_ushort v139, v147, s[88:91], s80 offen offset:800\n"
"buffer_load_ushort v140, v147, s[88:91], s81 offen offset:32\n"
"buffer_load_ushort v141, v147, s[88:91], s81 offen offset:288\n"
"buffer_load_ushort v142, v147, s[88:91], s81 offen offset:544\n"
"buffer_load_ushort v143, v147, s[88:91], s81 offen offset:800\n"
"s_waitcnt vmcnt(0)\n"
"v_lshl_or_b32 v128, v129, 16, v128\n"
"v_lshl_or_b32 v129, v131, 16, v130\n"
"v_lshl_or_b32 v130, v133, 16, v132\n"
"v_lshl_or_b32 v131, v135, 16, v134\n"
"v_lshl_or_b32 v136, v137, 16, v136\n"
"v_lshl_or_b32 v137, v139, 16, v138\n"
"v_lshl_or_b32 v138, v141, 16, v140\n"
"v_lshl_or_b32 v139, v143, 16, v142\n"
"v_mfma_f32_16x16x32_bf16 v[8:11], v[96:99], v[128:131], v[8:11]\n"
"s_nop 0\n"
"v_mfma_f32_16x16x32_bf16 v[12:15], v[96:99], v[136:139], v[12:15]\n"
"v_mfma_f32_16x16x32_bf16 v[40:43], v[104:107], v[128:131], v[40:43]\n"
"v_mfma_f32_16x16x32_bf16 v[44:47], v[104:107], v[136:139], v[44:47]\n"
"v_mfma_f32_16x16x32_bf16 v[72:75], v[112:115], v[128:131], v[72:75]\n"
"v_mfma_f32_16x16x32_bf16 v[76:79], v[112:115], v[136:139], v[76:79]\n"
"s_cmp_lg_u32 s83, s43\n"
"s_cbranch_scc1 LSK3B1_%=\n"
"v_mfma_f32_16x16x32_bf16 a[216:219], v[120:123], v[128:131], a[216:219]\n"
"v_mfma_f32_16x16x32_bf16 a[220:223], v[120:123], v[136:139], a[220:223]\n"
"LSK3B1_%=:\n"
"s_nop 3\n"
"buffer_load_ushort v128, v146, s[88:91], s80 offen offset:128\n"
"buffer_load_ushort v129, v146, s[88:91], s80 offen offset:384\n"
"buffer_load_ushort v130, v146, s[88:91], s80 offen offset:640\n"
"buffer_load_ushort v131, v146, s[88:91], s80 offen offset:896\n"
"buffer_load_ushort v132, v146, s[88:91], s81 offen offset:128\n"
"buffer_load_ushort v133, v146, s[88:91], s81 offen offset:384\n"
"buffer_load_ushort v134, v146, s[88:91], s81 offen offset:640\n"
"buffer_load_ushort v135, v146, s[88:91], s81 offen offset:896\n"
"buffer_load_ushort v136, v146, s[88:91], s80 offen offset:160\n"
"buffer_load_ushort v137, v146, s[88:91], s80 offen offset:416\n"
"buffer_load_ushort v138, v146, s[88:91], s80 offen offset:672\n"
"buffer_load_ushort v139, v146, s[88:91], s80 offen offset:928\n"
"buffer_load_ushort v140, v146, s[88:91], s81 offen offset:160\n"
"buffer_load_ushort v141, v146, s[88:91], s81 offen offset:416\n"
"buffer_load_ushort v142, v146, s[88:91], s81 offen offset:672\n"
"buffer_load_ushort v143, v146, s[88:91], s81 offen offset:928\n"
"s_waitcnt vmcnt(0)\n"
"v_lshl_or_b32 v128, v129, 16, v128\n"
"v_lshl_or_b32 v129, v131, 16, v130\n"
"v_lshl_or_b32 v130, v133, 16, v132\n"
"v_lshl_or_b32 v131, v135, 16, v134\n"
"v_lshl_or_b32 v136, v137, 16, v136\n"
"v_lshl_or_b32 v137, v139, 16, v138\n"
"v_lshl_or_b32 v138, v141, 16, v140\n"
"v_lshl_or_b32 v139, v143, 16, v142\n"
"v_mfma_f32_16x16x32_bf16 v[16:19], v[96:99], v[128:131], v[16:19]\n"
"s_nop 0\n"
"v_mfma_f32_16x16x32_bf16 v[20:23], v[96:99], v[136:139], v[20:23]\n"
"v_mfma_f32_16x16x32_bf16 v[48:51], v[104:107], v[128:131], v[48:51]\n"
"v_mfma_f32_16x16x32_bf16 v[52:55], v[104:107], v[136:139], v[52:55]\n"
"v_mfma_f32_16x16x32_bf16 v[80:83], v[112:115], v[128:131], v[80:83]\n"
"v_mfma_f32_16x16x32_bf16 v[84:87], v[112:115], v[136:139], v[84:87]\n"
"s_cmp_lg_u32 s83, s43\n"
"s_cbranch_scc1 LSK3B2_%=\n"
"v_mfma_f32_16x16x32_bf16 a[224:227], v[120:123], v[128:131], a[224:227]\n"
"v_mfma_f32_16x16x32_bf16 a[228:231], v[120:123], v[136:139], a[228:231]\n"
"LSK3B2_%=:\n"
"s_nop 3\n"
"buffer_load_ushort v128, v147, s[88:91], s80 offen offset:128\n"
"buffer_load_ushort v129, v147, s[88:91], s80 offen offset:384\n"
"buffer_load_ushort v130, v147, s[88:91], s80 offen offset:640\n"
"buffer_load_ushort v131, v147, s[88:91], s80 offen offset:896\n"
"buffer_load_ushort v132, v147, s[88:91], s81 offen offset:128\n"
"buffer_load_ushort v133, v147, s[88:91], s81 offen offset:384\n"
"buffer_load_ushort v134, v147, s[88:91], s81 offen offset:640\n"
"buffer_load_ushort v135, v147, s[88:91], s81 offen offset:896\n"
"buffer_load_ushort v136, v147, s[88:91], s80 offen offset:160\n"
"buffer_load_ushort v137, v147, s[88:91], s80 offen offset:416\n"
"buffer_load_ushort v138, v147, s[88:91], s80 offen offset:672\n"
"buffer_load_ushort v139, v147, s[88:91], s80 offen offset:928\n"
"buffer_load_ushort v140, v147, s[88:91], s81 offen offset:160\n"
"buffer_load_ushort v141, v147, s[88:91], s81 offen offset:416\n"
"buffer_load_ushort v142, v147, s[88:91], s81 offen offset:672\n"
"buffer_load_ushort v143, v147, s[88:91], s81 offen offset:928\n"
"s_waitcnt vmcnt(0)\n"
"v_lshl_or_b32 v128, v129, 16, v128\n"
"v_lshl_or_b32 v129, v131, 16, v130\n"
"v_lshl_or_b32 v130, v133, 16, v132\n"
"v_lshl_or_b32 v131, v135, 16, v134\n"
"v_lshl_or_b32 v136, v137, 16, v136\n"
"v_lshl_or_b32 v137, v139, 16, v138\n"
"v_lshl_or_b32 v138, v141, 16, v140\n"
"v_lshl_or_b32 v139, v143, 16, v142\n"
"v_mfma_f32_16x16x32_bf16 v[24:27], v[96:99], v[128:131], v[24:27]\n"
"s_nop 0\n"
"v_mfma_f32_16x16x32_bf16 v[28:31], v[96:99], v[136:139], v[28:31]\n"
"v_mfma_f32_16x16x32_bf16 v[56:59], v[104:107], v[128:131], v[56:59]\n"
"v_mfma_f32_16x16x32_bf16 v[60:63], v[104:107], v[136:139], v[60:63]\n"
"v_mfma_f32_16x16x32_bf16 v[88:91], v[112:115], v[128:131], v[88:91]\n"
"v_mfma_f32_16x16x32_bf16 v[92:95], v[112:115], v[136:139], v[92:95]\n"
"s_cmp_lg_u32 s83, s43\n"
"s_cbranch_scc1 LSK3B3_%=\n"
"v_mfma_f32_16x16x32_bf16 a[232:235], v[120:123], v[128:131], a[232:235]\n"
"v_mfma_f32_16x16x32_bf16 a[236:239], v[120:123], v[136:139], a[236:239]\n"
"LSK3B3_%=:\n"
"s_nop 3\n"
"s_nop 9\n"
"s_add_u32 s80, s80, 8192\n"
"s_add_u32 s81, s81, 8192\n"
"s_add_u32 s82, s82, 1\n"
"s_cmp_lt_u32 s82, 32\n"
"s_cbranch_scc1 LSLOOP_%=\n"
"v_mov_b32 v154, v155\n"
"s_nop 1\n"
"v_permlane32_swap_b32 v155, v154\n"
"v_add_f32 v155, v155, v154\n"
"v_mov_b32 v154, v155\n"
"s_nop 1\n"
"v_permlane16_swap_b32 v155, v154\n"
"v_add_f32 v155, v155, v154\n"
"v_mov_b32 v196, v155\n"
"v_mov_b32 v158, v159\n"
"s_nop 1\n"
"v_permlane32_swap_b32 v159, v158\n"
"v_add_f32 v159, v159, v158\n"
"v_mov_b32 v158, v159\n"
"s_nop 1\n"
"v_permlane16_swap_b32 v159, v158\n"
"v_add_f32 v159, v159, v158\n"
"v_mov_b32 v198, v159\n"
"v_mov_b32 v162, v163\n"
"s_nop 1\n"
"v_permlane32_swap_b32 v163, v162\n"
"v_add_f32 v163, v163, v162\n"
"v_mov_b32 v162, v163\n"
"s_nop 1\n"
"v_permlane16_swap_b32 v163, v162\n"
"v_add_f32 v163, v163, v162\n"
"v_mov_b32 v200, v163\n"
"v_mov_b32 v166, v167\n"
"s_nop 1\n"
"v_permlane32_swap_b32 v167, v166\n"
"v_add_f32 v167, v167, v166\n"
"v_mov_b32 v166, v167\n"
"s_nop 1\n"
"v_permlane16_swap_b32 v167, v166\n"
"v_add_f32 v167, v167, v166\n"
"v_mov_b32 v202, v167\n"
"v_sub_f32 v192, 0, v164\n"
"v_mov_b32 v96, 0\n"
"v_mov_b32 v97, 0\n"
"v_mov_b32 v98, 0\n"
"v_mov_b32 v99, 0\n"
"v_mov_b32 v100, 0\n"
"v_mov_b32 v101, 0\n"
"v_mov_b32 v102, 0\n"
"v_mov_b32 v103, 0\n"
"v_mov_b32 v104, 0\n"
"v_mov_b32 v105, 0\n"
"v_mov_b32 v106, 0\n"
"v_mov_b32 v107, 0\n"
"v_mov_b32 v108, 0\n"
"v_mov_b32 v109, 0\n"
"v_mov_b32 v110, 0\n"
"v_mov_b32 v111, 0\n"
"v_mov_b32 v112, 0\n"
"v_mov_b32 v113, 0\n"
"v_mov_b32 v114, 0\n"
"v_mov_b32 v115, 0\n"
"v_mov_b32 v116, 0\n"
"v_mov_b32 v117, 0\n"
"v_mov_b32 v118, 0\n"
"v_mov_b32 v119, 0\n"
"v_mov_b32 v120, 0\n"
"v_mov_b32 v121, 0\n"
"v_mov_b32 v122, 0\n"
"v_mov_b32 v123, 0\n"
"v_mov_b32 v204, 0\n"
"v_mov_b32 v205, 0\n"
"s_nop 3\n"
"s_mov_b32 s68, 0\n"
"s_cmp_eq_u32 s47, 4\n"
"s_cbranch_scc1 LBACK0_%=\n"
"s_cmp_eq_u32 s47, 8\n"
"s_cbranch_scc1 LBACK1_%=\n"
"s_cmp_eq_u32 s47, 12\n"
"s_cbranch_scc1 LBACK2_%=\n"
"s_branch LBACK3_%=\n"
"LFIN_%=:\n"
    :: [kalo]"s"((unsigned)ka), [kahi]"s"((unsigned)(ka >> 32)), [wg]"s"(blockIdx.x), [wid]"s"(wid)
    : "v0","v1","v2","v3","v4","v5","v6","v7","v8","v9","v10","v11","v12","v13","v14","v15","v16","v17","v18","v19","v20","v21","v22","v23","v24","v25","v26","v27","v28","v29","v30","v31","v32","v33","v34","v35","v36","v37","v38","v39","v40","v41","v42","v43","v44","v45","v46","v47","v48","v49","v50","v51","v52","v53","v54","v55","v56","v57","v58","v59","v60","v61","v62","v63","v64","v65","v66","v67","v68","v69","v70","v71","v72","v73","v74","v75","v76","v77","v78","v79","v80","v81","v82","v83","v84","v85","v86","v87","v88","v89","v90","v91","v92","v93","v94","v95","v96","v97","v98","v99","v100","v101","v102","v103","v104","v105","v106","v107","v108","v109","v110","v111","v112","v113","v114","v115","v116","v117","v118","v119","v120","v121","v122","v123","v124","v125","v126","v127","v128","v129","v130","v131","v132","v133","v134","v135","v136","v137","v138","v139","v140","v141","v142","v143","v144","v145","v146","v147","v148","v149","v150","v151","v152","v153","v154","v155","v156","v157","v158","v159","v160","v161","v162","v163","v164","v165","v166","v167","v168","v169","v170","v171","v172","v173","v174","v175","v176","v177","v178","v179","v180","v181","v182","v183","v184","v185","v186","v187","v188","v189","v190","v191","v192","v193","v194","v195","v196","v197","v198","v199","v200","v201","v202","v203","v204","v205","v206","v207","v208","v209","v210","v211","v212","v213","v214","v215","v216","v217","v218","v219","v220","v221","v222","v223","v224","v225","v226","v227","v228","v229","v230","v231","v232","v233","v234","v235","v236","v237","v238","v239","v240","v241","v242","v243","v244","v245","v246","v247","v248","v249","v250","v251","v252","v253","v254","v255","a0","a1","a2","a3","a4","a5","a6","a7","a8","a9","a10","a11","a12","a13","a14","a15","a16","a17","a18","a19","a20","a21","a22","a23","a24","a25","a26","a27","a28","a29","a30","a31","a32","a33","a34","a35","a36","a37","a38","a39","a40","a41","a42","a43","a44","a45","a46","a47","a48","a49","a50","a51","a52","a53","a54","a55","a56","a57","a58","a59","a60","a61","a62","a63","a64","a65","a66","a67","a68","a69","a70","a71","a72","a73","a74","a75","a76","a77","a78","a79","a80","a81","a82","a83","a84","a85","a86","a87","a88","a89","a90","a91","a92","a93","a94","a95","a96","a97","a98","a99","a100","a101","a102","a103","a104","a105","a106","a107","a108","a109","a110","a111","a112","a113","a114","a115","a116","a117","a118","a119","a120","a121","a122","a123","a124","a125","a126","a127","a128","a129","a130","a131","a132","a133","a134","a135","a136","a137","a138","a139","a140","a141","a142","a143","a144","a145","a146","a147","a148","a149","a150","a151","a152","a153","a154","a155","a156","a157","a158","a159","a160","a161","a162","a163","a164","a165","a166","a167","a168","a169","a170","a171","a172","a173","a174","a175","a176","a177","a178","a179","a180","a181","a182","a183","a184","a185","a186","a187","a188","a189","a190","a191","a192","a193","a194","a195","a196","a197","a198","a199","a200","a201","a202","a203","a204","a205","a206","a207","a208","a209","a210","a211","a212","a213","a214","a215","a216","a217","a218","a219","a220","a221","a222","a223","a224","a225","a226","a227","a228","a229","a230","a231","a232","a233","a234","a235","a236","a237","a238","a239","a240","a241","a242","a243","a244","a245","a246","a247","a248","a249","a250","a251","a252","a253","a254","a255","s6","s7","s8","s9","s10","s11","s12","s13","s14","s15","s16","s17","s18","s19","s20","s21","s22","s23","s24","s25","s26","s27","s28","s29","s30","s31","s32","s33","s34","s35","s36","s37","s38","s39","s40","s41","s42","s43","s44","s45","s46","s47","s48","s49","s50","s51","s52","s53","s54","s55","s56","s57","s58","s59","s60","s61","s62","s63","s64","s65","s66","s67","s68","s69","s70","s71","s72","s73","s74","s75","s76","s77","s78","s79","s80","s81","s82","s83","s84","s85","s86","s87","s88","s89","s90","s91","s92","s93","s94","s95","s96","s97","s98","s99","s100","vcc","scc","m0","memory");
}
extern "C" void kernel_launch(void* const* d_in, const int* in_sizes, int n_in, void* d_out, int out_size, void* d_ws, size_t ws_size, hipStream_t stream) {
  char* xch = (char*)d_ws + ((ws_size - (size_t)270532608) & ~(size_t)0xfffff);
  (void)hipFuncSetAttribute((const void*)fused_attn, hipFuncAttributeMaxDynamicSharedMemorySize, 163840);
  hipLaunchKernelGGL(fused_attn, dim3(256), dim3(256), 163840, stream,
                     (const char*)d_in[0], (const char*)d_in[1], (const char*)d_in[2], (const char*)d_in[3], (const char*)d_in[4],
                     (const char*)d_in[5], (const char*)d_in[6], (const char*)d_in[7], (const char*)d_in[8], (const char*)d_in[9], xch, (char*)d_out, (char*)d_ws);
}
```

```cpp
#include <hip/hip_runtime.h>
#include <hip/hip_bf16.h>
#include <stdint.h>

__global__ __launch_bounds__(256, 1)
void fused_attn(const char* xA, const char* kA, const char* vA, const char* wA, const char* uA,
                const char* xB, const char* kB, const char* vB, const char* wB, const char* uB, char* xch, char* out, char* flags) {
  extern __shared__ __attribute__((aligned(16))) char lds[];
  const unsigned wid = __builtin_amdgcn_readfirstlane(threadIdx.x >> 6);
  const unsigned long long ka = (unsigned long long)__builtin_amdgcn_kernarg_segment_ptr();
  asm volatile(
"s_memtime s[64:65]\n"
"s_memrealtime s[66:67]\n"
"s_bfe_u32 s10, %[wg], 0x10002\n"
"s_mul_i32 s40, s10, 40\n"
"s_add_u32 s40, s40, %[kalo]\n"
"s_addc_u32 s41, %[kahi], 0\n"
"s_mov_b32 s78, %[kalo]\n"
"s_mov_b32 s79, %[kahi]\n"
"s_load_dwordx2 s[24:25], s[40:41], 0\n"
"s_load_dwordx2 s[28:29], s[40:41], 24\n"
"s_load_dwordx2 s[6:7], s[78:79], 0x60\n"
"s_load_dwordx2 s[16:17], s[40:41], 8\n"
"s_load_dwordx2 s[20:21], s[40:41], 16\n"
"s_load_dwordx2 s[8:9], s[40:41], 32\n"
"s_load_dwordx4 s[12:15], s[78:79], 0x50\n"
"s_mov_b32 s42, %[wid]\n"
"s_and_b32 s11, %[wg], 1\n"
"s_lshl_b32 s11, s11, 3\n"
"s_lshr_b32 s60, %[wg], 5\n"
"s_add_u32 s11, s11, s60\n"
"s_bfe_u32 s61, %[wg], 0x10001\n"
"s_bfe_u32 s60, %[wg], 0x20003\n"
"s_lshl2_add_u32 s61, s61, s60\n"
"s_getpc_b64 s[80:81]\n"
"LCP_%=:\n"
"s_mov_b32 s82, LEND_%=-LCP_%=\n"
"s_mov_b32 s83, 0x20000\n"
"v_mbcnt_lo_u32_b32 v2, -1, 0\n"
"v_lshlrev_b32 v2, 7, v2\n"
"s_lshr_b32 s60, %[wg], 3\n"
"s_lshl2_add_u32 s60, s60, s42\n"
"s_mul_i32 s60, s60, 640\n"
"s_mov_b64 exec, 31\n"
"buffer_load_dword v3, v2, s[80:83], s60 offen\n"
"s_mov_b64 exec, -1\n"
"s_mov_b32 s19, 0x20000\n"
"s_mov_b32 s23, 0x20000\n"
"s_mov_b32 s27, 0x20000\n"
"s_mov_b32 s31, 0x20000\n"
"s_mov_b32 s35, 0x20000\n"
"s_mov_b32 s39, 0x20000\n"
"s_mov_b32 s26, 286720\n"
"s_lshl_b32 s44, s42, 12\n"
"s_mov_b32 s50, s44\n"
"s_mul_i32 s51, s42, 7168\n"
"s_add_u32 s51, s51, 0x10000\n"
"s_mov_b32 s74, 0x8000\n"
"s_mov_b32 s58, 524288\n"
"s_mov_b32 s47, 0\n"
"s_add_u32 s60, s11, s61\n"
"s_and_b32 s60, s60, 0\n"
"s_mul_i32 s60, s60, 3\n"
"s_lshl_b32 s49, s60, 7\n"
"s_lshl_b32 s48, s60, 19\n"
"v_mbcnt_lo_u32_b32 v4, -1, 0\n"
"v_mbcnt_hi_u32_b32 v4, -1, v4\n"
"v_and_b32 v5, 15, v4\n"
"v_lshrrev_b32 v6, 4, v4\n"
"v_lshrrev_b32 v7, 3, v4\n"
"v_and_b32 v8, 7, v4\n"
"v_lshlrev_b32 v9, 1, v6\n"
"v_xor_b32 v9, v9, v8\n"
"v_lshlrev_b32 v9, 4, v9\n"
"s_mul_i32 s60, s42, 71680\n"
"v_mul_u32_u24 v10, 1280, v7\n"
"v_add3_u32 v248, v10, v9, s60\n"
"v_add_u32 v249, 9216, v248\n"
"v_add_u32 v254, 18432, v248\n"
"v_add_u32 v255, 27648, v248\n"
"v_lshl_add_u32 v240, v7, 8, v9\n"
"v_add_u32 v240, s44, v240\n"
"v_and_b32 v10, 1, v6\n"
"v_xor_b32 v10, v10, v8\n"
"v_lshlrev_b32 v10, 4, v10\n"
"v_lshl_add_u32 v10, v7, 13, v10\n"
"s_lshl_b32 s60, s42, 17\n"
"v_add_u32 v10, s60, v10\n"
"v_add_u32 v224, 1024, v10\n"
"v_add_u32 v225, 128, v10\n"
"v_xor_b32 v10, 64, v10\n"
"v_add_u32 v226, 64512, v10\n"
"v_add_u32 v227, 63616, v10\n"
"s_mul_i32 s40, s11, 286720\n"
"s_lshl_b32 s41, s61, 10\n"
"s_sub_u32 s30, 5243904, s41\n"
"s_sub_u32 s41, s41, 1024\n"
"s_ashr_i32 s60, s41, 31\n"
"s_lshl4_add_u32 s92, s10, s11\n"
"s_lshl_b32 s93, s92, 9\n"
"v_mov_b32 v0, 0\n"
"s_waitcnt lgkmcnt(0)\n"
"s_add_u32 s24, s24, s40\n"
"s_addc_u32 s25, s25, 0\n"
"s_and_b32 s25, s25, 0xffff\n"
"s_add_u32 s28, s28, s41\n"
"s_addc_u32 s29, s29, s60\n"
"s_and_b32 s29, s29, 0xffff\n"
"s_add_u32 s6, s6, s93\n"
"s_addc_u32 s7, s7, 0\n"
"buffer_load_dword v12, v248, s[24:27], s49 offen\n"
"buffer_load_dword v13, v224, s[28:31], s48 offen\n"
"global_load_dword v1, v0, s[6:7] offset:128 sc1\n"
"s_mov_b32 m0, s51\n"
"s_nop 0\n"
"buffer_load_dwordx4 v248, s[24:27], s49 offen lds\n"
"buffer_load_dwordx4 v249, s[24:27], s49 offen offset:1024 lds\n"
"buffer_load_dwordx4 v254, s[24:27], s49 offen offset:2048 lds\n"
"buffer_load_dwordx4 v255, s[24:27], s49 offen offset:3072 lds\n"
"s_add_u32 m0, s51, 4096\n"
"s_add_u32 s40, s49, 40960\n"
"buffer_load_dwordx4 v248, s[24:27], s40 offen lds\n"
"buffer_load_dwordx4 v249, s[24:27], s40 offen offset:1024 lds\n"
"buffer_load_dwordx4 v254, s[24:27], s40 offen offset:2048 lds\n"
"s_add_u32 s49, s49, 128\n"
"s_sub_u32 s41, s49, 1280\n"
"s_min_u32 s49, s49, s41\n"
"s_xor_b32 s51, s51, s74\n"
"s_mov_b32 m0, s50\n"
"s_nop 0\n"
"buffer_load_dwordx4 v224, s[28:31], s48 offen lds\n"
"buffer_load_dwordx4 v225, s[28:31], s48 offen offset:1024 lds\n"
"buffer_load_dwordx4 v226, s[28:31], s48 offen offset:2048 lds\n"
"buffer_load_dwordx4 v227, s[28:31], s48 offen offset:3072 lds\n"
"s_xor_b32 s50, s50, s74\n"
"s_lshl2_add_u32 s63, s11, s42\n"
"s_mul_i32 s63, s63, 10240\n"
"s_and_b32 s78, s42, 1\n"
"s_lshl_b32 s43, s78, 6\n"
"s_mov_b32 s77, 0x80000000\n"
"s_mov_b32 s75, 0x10000\n"
"s_mov_b32 s73, 0xffff0000\n"
"s_mov_b32 s46, 0x3e0293ee\n"
"s_mov_b32 s45, 0x63800000\n"
"s_mov_b32 s62, 9\n"
"v_lshl_add_u32 v242, v4, 4, s44\n"
"s_lshr_b32 s79, s42, 1\n"
"s_lshl4_add_u32 s76, s79, s78\n"
"s_lshl_b32 s76, s76, 10\n"
"s_add_u32 s76, s76, 0x20000\n"
"s_sub_u32 s76, s76, s44\n"
"v_lshlrev_b32 v9, 6, v6\n"
"v_xor_b32 v9, v9, v242\n"
"v_add_u32 v243, s76, v9\n"
"v_and_b32 v9, 6, v5\n"
"s_xor_b32 m0, s50, 0xc000\n"
"s_add_u32 s40, s48, 256\n"
"buffer_load_dwordx4 v224, s[28:31], s40 offen lds\n"
"v_xor_b32 v9, v9, v6\n"
"v_lshlrev_b32 v9, 4, v9\n"
"v_lshl_add_u32 v10, v5, 7, v9\n"
"s_mul_i32 s79, s42, 6144\n"
"s_add_u32 s79, s79, 0x10000\n"
"v_add_u32 v246, s79, v10\n"
"v_xor_b32 v247, 64, v246\n"
"s_lshr_b32 s79, s42, 1\n"
"s_lshl_b32 s79, s79, 11\n"
"buffer_load_dwordx4 v225, s[28:31], s40 offen offset:1024 lds\n"
"s_add_u32 s79, s79, 90112\n"
"v_add_u32 v252, s79, v10\n"
"v_xor_b32 v253, 64, v252\n"
"s_xor_b32 s79, s42, 1\n"
"s_mul_i32 s79, s79, 6144\n"
"s_add_u32 s79, s79, 0x10000\n"
"v_add_u32 v250, s79, v10\n"
"v_xor_b32 v251, 64, v250\n"
"s_mul_i32 s79, s78, 8256\n"
"buffer_load_dwordx4 v226, s[28:31], s40 offen offset:2048 lds\n"
"v_lshrrev_b32 v9, 5, v4\n"
"v_lshlrev_b32 v9, 11, v9\n"
"v_bfe_u32 v10, v4, 2, 3\n"
"v_lshl_add_u32 v9, v10, 7, v9\n"
"v_and_b32 v10, 3, v4\n"
"v_lshl_add_u32 v9, v10, 3, v9\n"
"v_bfe_u32 v10, v4, 3, 2\n"
"v_lshl_add_u32 v9, v10, 5, v9\n"
"s_add_u32 s79, s79, 0x10000\n"
"buffer_load_dwordx4 v227, s[28:31], s40 offen offset:3072 lds\n"
"s_cmp_eq_u32 s47, 8\n"
"s_cselect_b32 s41, s52, s58\n"
"s_add_u32 s48, s48, s41\n"
"s_sub_u32 s41, s48, 5242880\n"
"s_min_u32 s48, s48, s41\n"
"v_xor_b32 v232, s79, v9\n"
"v_xor_b32 v233, 32, v232\n"
"v_xor_b32 v234, 64, v232\n"
"v_xor_b32 v235, 96, v232\n"
"v_xor_b32 v236, 0x2000, v232\n"
"v_xor_b32 v237, 0x2000, v233\n"
"v_xor_b32 v238, 0x2000, v234\n"
"v_xor_b32 v239, 0x2000, v235\n"
"v_lshlrev_b32 v9, 11, v6\n"
"s_mov_b32 m0, s50\n"
"s_nop 0\n"
"buffer_load_dwordx4 v224, s[28:31], s48 offen lds\n"
"v_bfe_u32 v10, v4, 2, 2\n"
"v_lshl_add_u32 v9, v10, 7, v9\n"
"v_and_b32 v10, 1, v4\n"
"v_lshl_add_u32 v9, v10, 3, v9\n"
"v_bfe_u32 v10, v4, 1, 1\n"
"v_lshl_add_u32 v9, v10, 5, v9\n"
"v_bfe_u32 v10, v4, 3, 1\n"
"v_lshl_add_u32 v9, v10, 4, v9\n"
"v_bfe_u32 v10, v4, 4, 1\n"
"buffer_load_dwordx4 v225, s[28:31], s48 offen offset:1024 lds\n"
"v_lshl_add_u32 v9, v10, 6, v9\n"
"v_xor_b32 v244, s43, v9\n"
"v_xor_b32 v245, 16, v244\n"
"s_lshl3_add_u32 s79, s11, s61\n"
"s_lshl_b32 s79, s79, 20\n"
"s_sub_u32 s79, s79, 896\n"
"s_ashr_i32 s60, s79, 31\n"
"s_add_u32 s16, s16, s79\n"
"s_addc_u32 s17, s17, s60\n"
"buffer_load_dwordx4 v226, s[28:31], s48 offen offset:2048 lds\n"
"s_and_b32 s17, s17, 0xffff\n"
"s_mov_b32 s18, 1049472\n"
"s_mov_b64 s[84:85], s[16:17]\n"
"s_mov_b64 s[86:87], s[18:19]\n"
"s_add_u32 s20, s20, s79\n"
"s_addc_u32 s21, s21, s60\n"
"s_and_b32 s21, s21, 0xffff\n"
"s_mov_b32 s22, 1049472\n"
"s_mov_b64 s[88:89], s[20:21]\n"
"buffer_load_dwordx4 v227, s[28:31], s48 offen offset:3072 lds\n"
"s_xor_b32 s50, s50, s74\n"
"s_mov_b64 s[90:91], s[22:23]\n"
"s_lshr_b32 s57, %[wg], 3\n"
"s_mul_i32 s57, s57, 5\n"
"s_and_b32 s57, s57, 15\n"
"s_lshl_b32 s57, s57, 14\n"
"s_mov_b32 s55, 896\n"
"s_add_u32 s56, s57, 896\n"
"s_mov_b32 s59, 16384\n"
"s_mov_b32 s54, -896\n"
"s_mov_b32 m0, s51\n"
"s_nop 0\n"
"buffer_load_dwordx4 v248, s[24:27], s49 offen lds\n"
"s_add_u32 s52, s11, s61\n"
"s_and_b32 s52, s52, 0\n"
"s_mul_i32 s52, s52, 3\n"
"s_add_u32 s52, s52, 1\n"
"s_lshl_b32 s53, s52, 7\n"
"s_lshl_b32 s52, s52, 19\n"
"s_add_u32 s52, s52, 512\n"
"s_mul_i32 s79, s61, 655360\n"
"s_add_u32 s8, s8, s79\n"
"s_addc_u32 s9, s9, 0\n"
"buffer_load_dwordx4 v249, s[24:27], s49 offen offset:1024 lds\n"
"s_and_b32 s9, s9, 0xffff\n"
"s_add_u32 s12, s12, 0x1fffff\n"
"s_addc_u32 s13, s13, 0\n"
"s_and_b32 s12, s12, 0xffe00000\n"
"s_mul_i32 s96, s42, 3\n"
"s_and_b32 s96, s96, 5\n"
"s_cmp_eq_u32 s42, 3\n"
"s_cselect_b32 s96, 5, s96\n"
"buffer_load_dwordx4 v254, s[24:27], s49 offen offset:2048 lds\n"
"s_lshl_b32 s79, s61, 8\n"
"s_add_u32 s96, s96, s79\n"
"s_add_u32 s96, s96, 160\n"
"s_lshl_b32 s96, s96, 12\n"
"s_mul_i32 s79, s92, 8388608\n"
"s_add_u32 s32, s12, s79\n"
"s_addc_u32 s33, s13, 0\n"
"s_and_b32 s33, s33, 0xffff\n"
"s_mov_b32 s34, 8388608\n"
"buffer_load_dwordx4 v255, s[24:27], s49 offen offset:3072 lds\n"
"s_mul_i32 s79, s11, 573440\n"
"s_mul_i32 s60, s10, 1280\n"
"s_sub_u32 s38, 573440, s60\n"
"s_add_u32 s79, s79, s60\n"
"s_add_u32 s36, s14, s79\n"
"s_addc_u32 s37, s15, 0\n"
"s_and_b32 s37, s37, 0xffff\n"
"s_lshl2_add_u32 s93, s61, s42\n"
"s_add_u32 s95, s61, 1\n"
"s_add_u32 m0, s51, 4096\n"
"s_add_u32 s40, s49, 40960\n"
"buffer_load_dwordx4 v248, s[24:27], s40 offen lds\n"
"s_and_b32 s95, s95, 7\n"
"s_lshl_b32 s95, s95, 7\n"
"s_add_u32 s100, s61, 2\n"
"s_and_b32 s100, s100, 7\n"
"s_lshl_b32 s100, s100, 7\n"
"s_lshl_b32 s60, s61, 7\n"
"s_add_u32 s60, s60, s43\n"
"s_mul_i32 s97, s42, 122880\n"
"s_add_u32 s97, s97, s60\n"
"buffer_load_dwordx4 v249, s[24:27], s40 offen offset:1024 lds\n"
"s_lshr_b32 s98, s42, 1\n"
"s_add_u32 s98, s98, 12\n"
"s_mul_i32 s98, s98, 40960\n"
"s_add_u32 s98, s98, s60\n"
"s_mul_i32 s60, s93, 37\n"
"s_lshr_b32 s60, s60, 8\n"
"s_mul_i32 s79, s60, 7\n"
"s_sub_u32 s79, s93, s79\n"
"s_lshl4_add_u32 s94, s60, s79\n"
"buffer_load_dwordx4 v254, s[24:27], s40 offen offset:2048 lds\n"
"s_add_u32 s49, s49, 128\n"
"s_sub_u32 s41, s49, 1280\n"
"s_min_u32 s49, s49, s41\n"
"s_xor_b32 s51, s51, s74\n"
"s_lshl_b32 s94, s94, 10\n"
"s_add_u32 s94, s94, 524288\n"
"s_mul_i32 s99, s60, 3\n"
"s_lshr_b32 s78, s79, 1\n"
"s_add_u32 s99, s99, s78\n"
"s_lshr_b32 s78, s60, 1\n"
"s_add_u32 s78, s78, 12\n"
"s_cmp_eq_u32 s79, 6\n"
"s_cselect_b32 s99, s78, s99\n"
"s_mul_i32 s99, s99, 40960\n"
"s_xor_b32 s79, s79, s60\n"
"s_and_b32 s79, s79, 1\n"
"s_lshl_b32 s79, s79, 6\n"
"s_add_u32 s99, s99, s79\n"
"s_add_u32 s99, s99, 1024\n"
"s_cmp_eq_u32 s61, 7\n"
"s_cselect_b32 s99, 0x40000000, s99\n"
"s_cselect_b32 s94, 0x40000000, s94\n"
"s_waitcnt vmcnt(27)\n"
"v_and_b32 v12, 0x7fff0000, v12\n"
"v_and_b32 v13, 0x7fff0000, v13\n"
"s_nop 0\n"
"v_max_u32_dpp v12, v12, v12 quad_perm:[1,0,3,2] row_mask:0xf bank_mask:0xf\n"
"v_max_u32_dpp v13, v13, v13 quad_perm:[1,0,3,2] row_mask:0xf bank_mask:0xf\n"
"s_nop 0\n"
"v_max_u32_dpp v12, v12, v12 quad_perm:[2,3,0,1] row_mask:0xf bank_mask:0xf\n"
"v_max_u32_dpp v13, v13, v13 quad_perm:[2,3,0,1] row_mask:0xf bank_mask:0xf\n"
"s_memtime s[80:81]\n"
"s_memrealtime s[82:83]\n"
"s_nop 0\n"
"v_max_u32_dpp v12, v12, v12 row_half_mirror row_mask:0xf bank_mask:0xf\n"
"v_max_u32_dpp v13, v13, v13 row_half_mirror row_mask:0xf bank_mask:0xf\n"
"s_nop 0\n"
"v_min_u32_dpp v12, v12, v12 row_mirror row_mask:0xf bank_mask:0xf\n"
"v_min_u32_dpp v13, v13, v13 row_mirror row_mask:0xf bank_mask:0xf\n"
"s_nop 0\n"
"v_min_u32_dpp v12, v12, v12 row_bcast:15 row_mask:0xa bank_mask:0xf\n"
"v_min_u32_dpp v13, v13, v13 row_bcast:15 row_mask:0xa bank_mask:0xf\n"
"s_nop 0\n"
"v_min_u32_dpp v12, v12, v12 row_bcast:31 row_mask:0xc bank_mask:0xf\n"
"v_min_u32_dpp v13, v13, v13 row_bcast:31 row_mask:0xc bank_mask:0xf\n"
"s_nop 0\n"
"v_mul_f32 v12, v12, v13\n"
"v_mul_f32 v12, 0x43c80000, v12\n"
"v_min_f32 v12, 0x71800000, v12\n"
"v_mul_f32 v13, s46, v12\n"
"s_nop 0\n"
"v_readlane_b32 s70, v12, 63\n"
"v_readlane_b32 s71, v12, 63\n"
"v_readlane_b32 s69, v13, 63\n"
"s_nop 1\n"
"v_mov_b64 v[228:229], s[70:71]\n"
"v_mov_b64 v[230:231], s[70:71]\n"
"s_waitcnt vmcnt(26)\n"
"v_readfirstlane_b32 s92, v1\n"
"v_mov_b64 v[0:1], s[70:71]\n"
"v_mov_b64 v[2:3], s[70:71]\n"
"s_mov_b32 s70, s46\n"
"s_xor_b32 s71, s69, s77\n"
"s_waitcnt lgkmcnt(0)\n"
"s_sub_u32 s80, s80, s64\n"
"s_sub_u32 s82, s82, s66\n"
"s_mul_i32 s80, s80, 10\n"
"s_mul_i32 s82, s82, 211\n"
"s_cmp_gt_u32 s80, s82\n"
"s_cselect_b32 s64, 1, 0\n"
"s_cselect_b32 s68, 655360, 0\n"
"s_cmp_eq_u32 s42, 0\n"
"s_cselect_b32 s64, s64, 0\n"
"s_mov_b32 s65, 0\n"
"s_add_u32 s66, s66, 2800\n"
"s_mov_b32 s67, s66\n"
"s_mov_b32 s72, 126\n"
"s_mov_b64 vcc, s[64:65]\n"
"s_nop 2\n"
"s_mul_i32 s60, s92, 0x9e3779b1\n"
"s_add_u32 s60, s60, 0x7f4a7c15\n"
"s_waitcnt vmcnt(15)\n"
"s_barrier\n"
"ds_read_b128 a[136:139], v246 offset:0\n"
"ds_read_b128 a[140:143], v247 offset:0\n"
"ds_read_b64_tr_b16 a[192:193], v244 offset:0\n"
"ds_read_b64_tr_b16 a[194:195], v244 offset:512\n"
"ds_read_b64_tr_b16 a[196:197], v244 offset:8192\n"
"ds_read_b64_tr_b16 a[198:199], v244 offset:8704\n"
"ds_read_b64_tr_b16 a[200:201], v245 offset:0\n"
"ds_read_b64_tr_b16 a[202:203], v245 offset:512\n"
"ds_read_b64_tr_b16 a[204:205], v245 offset:8192\n"
"ds_read_b64_tr_b16 a[206:207], v245 offset:8704\n"
"ds_read_b64_tr_b16 a[208:209], v244 offset:1024\n"
"ds_read_b64_tr_b16 a[210:211], v244 offset:1536\n"
"ds_read_b64_tr_b16 a[212:213], v244 offset:9216\n"
"ds_read_b64_tr_b16 a[214:215], v244 offset:9728\n"
"s_waitcnt lgkmcnt(10)\n"
"v_mfma_f32_16x16x32_bf16 v[112:115], a[192:195], a[136:139], v[228:231]\n"
"ds_read_b64_tr_b16 a[216:217], v245 offset:1024\n"
"ds_read_b64_tr_b16 a[218:219], v245 offset:1536\n"
"s_waitcnt lgkmcnt(10)\n"
"v_mfma_f32_16x16x32_bf16 v[112:115], a[196:199], a[140:143], v[112:115]\n"
"ds_read_b64_tr_b16 a[220:221], v245 offset:9216\n"
"ds_read_b64_tr_b16 a[222:223], v245 offset:9728\n"
"s_waitcnt lgkmcnt(10)\n"
"v_mfma_f32_16x16x32_bf16 v[116:119], a[200:203], a[136:139], v[228:231]\n"
"s_waitcnt lgkmcnt(8)\n"
"ds_read_b128 a[144:147], v246 offset:2048\n"
"v_mfma_f32_16x16x32_bf16 v[116:119], a[204:207], a[140:143], v[116:119]\n"
"s_waitcnt lgkmcnt(7)\n"
"ds_read_b128 a[148:151], v247 offset:2048\n"
"v_mfma_f32_16x16x32_bf16 v[120:123], a[208:211], a[136:139], v[228:231]\n"
"s_waitcnt lgkmcnt(6)\n"
"ds_read_b128 a[152:155], v246 offset:4096\n"
"v_mfma_f32_16x16x32_bf16 v[120:123], a[212:215], a[140:143], v[120:123]\n"
"s_waitcnt lgkmcnt(5)\n"
"ds_read_b128 a[156:159], v247 offset:4096\n"
"v_mfma_f32_16x16x32_bf16 v[124:127], a[216:219], a[136:139], v[228:231]\n"
"s_waitcnt lgkmcnt(4)\n"
"ds_read_b128 a[160:163], v252 offset:0\n"
"v_mfma_f32_16x16x32_bf16 v[124:127], a[220:223], a[140:143], v[124:127]\n"
"s_waitcnt lgkmcnt(4)\n"
"ds_read_b128 a[164:167], v253 offset:0\n"
"v_mfma_f32_16x16x32_bf16 v[140:143], a[216:219], a[144:147], v[228:231]\n"
"s_waitcnt lgkmcnt(4)\n"
"ds_read_b128 a[168:171], v250 offset:0\n"
"v_mfma_f32_16x16x32_bf16 v[140:143], a[220:223], a[148:151], v[140:143]\n"
"ds_read_b128 a[172:175], v251 offset:0\n"
"v_mfma_f32_16x16x32_bf16 v[136:139], a[208:211], a[144:147], v[228:231]\n"
"s_waitcnt vmcnt(11)\n"
"ds_read_b128 a[176:179], v250 offset:2048\n"
"v_mfma_f32_16x16x32_bf16 v[136:139], a[212:215], a[148:151], v[136:139]\n"
"s_barrier\n"
"v_mfma_f32_16x16x32_bf16 v[132:135], a[200:203], a[144:147], v[228:231]\n"
"s_add_u32 s40, s48, 256\n"
"s_xor_b32 m0, s50, 0xc000\n"
"ds_read_b128 a[180:183], v251 offset:2048\n"
"v_mfma_f32_16x16x32_bf16 v[132:135], a[204:207], a[148:151], v[132:135]\n"
"buffer_load_dwordx4 v224, s[28:31], s40 offen lds\n"
"v_mfma_f32_16x16x32_bf16 v[128:131], a[192:195], a[144:147], v[228:231]\n"
"ds_read_b128 a[184:187], v250 offset:4096\n"
"v_mfma_f32_16x16x32_bf16 v[128:131], a[196:199], a[148:151], v[128:131]\n"
"s_waitcnt lgkmcnt(5)\n"
"ds_read_b128 a[188:191], v251 offset:4096\n"
"v_mfma_f32_16x16x32_bf16 v[144:147], a[192:195], a[152:155], v[228:231]\n"
"ds_read_b64_tr_b16 a[224:225], v244 offset:16384\n"
"v_mfma_f32_16x16x32_bf16 v[144:147], a[196:199], a[156:159], v[144:147]\n"
"ds_read_b64_tr_b16 a[226:227], v244 offset:16896\n"
"v_mfma_f32_16x16x32_bf16 v[148:151], a[200:203], a[152:155], v[228:231]\n"
"ds_read_b64_tr_b16 a[228:229], v244 offset:24576\n"
"v_mfma_f32_16x16x32_bf16 v[148:151], a[204:207], a[156:159], v[148:151]\n"
"buffer_load_dwordx4 v225, s[28:31], s40 offen offset:1024 lds\n"
"v_mfma_f32_16x16x32_bf16 v[152:155], a[208:211], a[152:155], v[228:231]\n"
"ds_read_b64_tr_b16 a[230:231], v244 offset:25088\n"
"v_mfma_f32_16x16x32_bf16 v[152:155], a[212:215], a[156:159], v[152:155]\n"
"ds_read_b64_tr_b16 a[232:233], v245 offset:16384\n"
"v_mfma_f32_16x16x32_bf16 v[156:159], a[216:219], a[152:155], v[228:231]\n"
"ds_read_b64_tr_b16 a[234:235], v245 offset:16896\n"
"v_mfma_f32_16x16x32_bf16 v[156:159], a[220:223], a[156:159], v[156:159]\n"
"ds_read_b64_tr_b16 a[236:237], v245 offset:24576\n"
"v_mfma_f32_16x16x32_bf16 v[172:175], a[216:219], a[160:163], v[228:231]\n"
"ds_read_b64_tr_b16 a[238:239], v245 offset:25088\n"
"v_mfma_f32_16x16x32_bf16 v[172:175], a[220:223], a[164:167], v[172:175]\n"
"buffer_load_dwordx4 v226, s[28:31], s40 offen offset:2048 lds\n"
"v_mfma_f32_16x16x32_bf16 v[168:171], a[208:211], a[160:163], v[228:231]\n"
"ds_read_b64_tr_b16 a[240:241], v244 offset:17408\n"
"v_mfma_f32_16x16x32_bf16 v[168:171], a[212:215], a[164:167], v[168:171]\n"
"ds_read_b64_tr_b16 a[242:243], v244 offset:17920\n"
"v_mfma_f32_16x16x32_bf16 v[164:167], a[200:203], a[160:163], v[228:231]\n"
"ds_read_b64_tr_b16 a[244:245], v244 offset:25600\n"
"v_mfma_f32_16x16x32_bf16 v[164:167], a[204:207], a[164:167], v[164:167]\n"
"buffer_load_dwordx4 v227, s[28:31], s40 offen offset:3072 lds\n"
"v_mfma_f32_16x16x32_bf16 v[160:163], a[192:195], a[160:163], v[228:231]\n"
"s_cmp_eq_u32 s47, 8\n"
"ds_read_b64_tr_b16 a[246:247], v244 offset:26112\n"
"v_xor_b32 v244, s74, v244\n"
"v_mfma_f32_16x16x32_bf16 v[160:163], a[196:199], a[164:167], v[160:163]\n"
"s_waitcnt lgkmcnt(5)\n"
"s_cselect_b32 s41, s52, s58\n"
"ds_read_b64_tr_b16 a[248:249], v245 offset:17408\n"
"v_xor_b32 v246, s74, v246\n"
"v_mfma_f32_16x16x32_bf16 v[176:179], a[192:195], a[168:171], v[228:231]\n"
"s_add_u32 s48, s48, s41\n"
"ds_read_b64_tr_b16 a[250:251], v245 offset:17920\n"
"v_xor_b32 v247, s74, v247\n"
"v_mfma_f32_16x16x32_bf16 v[176:179], a[196:199], a[172:175], v[176:179]\n"
"s_sub_u32 s41, s48, 5242880\n"
"ds_read_b64_tr_b16 a[252:253], v245 offset:25600\n"
"v_xor_b32 v252, s74, v252\n"
"v_mfma_f32_16x16x32_bf16 v[180:183], a[200:203], a[168:171], v[228:231]\n"
"s_min_u32 s48, s48, s41\n"
"ds_read_b64_tr_b16 a[254:255], v245 offset:26112\n"
"v_xor_b32 v245, s74, v245\n"
"v_mfma_f32_16x16x32_bf16 v[180:183], a[204:207], a[172:175], v[180:183]\n"
"v_xor_b32 v253, s74, v253\n"
"v_xor_b32 v250, s74, v250\n"
"v_mfma_f32_16x16x32_bf16 v[184:187], a[208:211], a[168:171], v[228:231]\n"
"s_mov_b32 m0, s50\n"
"v_xor_b32 v251, s74, v251\n"
"v_mfma_f32_16x16x32_bf16 v[184:187], a[212:215], a[172:175], v[184:187]\n"
"buffer_load_dwordx4 v224, s[28:31], s48 offen lds\n"
"v_mfma_f32_16x16x32_bf16 v[188:191], a[216:219], a[168:171], v[228:231]\n"
"v_mfma_f32_16x16x32_bf16 v[188:191], a[220:223], a[172:175], v[188:191]\n"
"v_mfma_f32_16x16x32_bf16 v[204:207], a[216:219], a[176:179], v[228:231]\n"
"v_mfma_f32_16x16x32_bf16 v[204:207], a[220:223], a[180:183], v[204:207]\n"
"buffer_load_dwordx4 v225, s[28:31], s48 offen offset:1024 lds\n"
"v_mfma_f32_16x16x32_bf16 v[200:203], a[208:211], a[176:179], v[228:231]\n"
"v_mfma_f32_16x16x32_bf16 v[200:203], a[212:215], a[180:183], v[200:203]\n"
"v_mfma_f32_16x16x32_bf16 v[196:199], a[200:203], a[176:179], v[228:231]\n"
"v_mfma_f32_16x16x32_bf16 v[196:199], a[204:207], a[180:183], v[196:199]\n"
"v_mfma_f32_16x16x32_bf16 v[192:195], a[192:195], a[176:179], v[228:231]\n"
"v_mfma_f32_16x16x32_bf16 v[192:195], a[196:199], a[180:183], v[192:195]\n"
"buffer_load_dwordx4 v226, s[28:31], s48 offen offset:2048 lds\n"
"v_mfma_f32_16x16x32_bf16 v[208:211], a[192:195], a[184:187], v[228:231]\n"
"v_mfma_f32_16x16x32_bf16 v[208:211], a[196:199], a[188:191], v[208:211]\n"
"v_mfma_f32_16x16x32_bf16 v[212:215], a[200:203], a[184:187], v[228:231]\n"
"v_mfma_f32_16x16x32_bf16 v[212:215], a[204:207], a[188:191], v[212:215]\n"
"v_mfma_f32_16x16x32_bf16 v[216:219], a[208:211], a[184:187], v[228:231]\n"
"v_mfma_f32_16x16x32_bf16 v[216:219], a[212:215], a[188:191], v[216:219]\n"
"buffer_load_dwordx4 v227, s[28:31], s48 offen offset:3072 lds\n"
"v_mfma_f32_16x16x32_bf16 v[220:223], a[216:219], a[184:187], v[228:231]\n"
"s_xor_b32 s50, s50, s74\n"
"v_mfma_f32_16x16x32_bf16 v[220:223], a[220:223], a[188:191], v[220:223]\n"
"v_mfma_f32_16x16x32_bf16 v[0:3], a[224:227], a[136:139], v[228:231]\n"
"v_mfma_f32_16x16x32_bf16 v[0:3], a[228:231], a[140:143], v[0:3]\n"
"v_mfma_f32_16x16x32_bf16 v[4:7], a[232:235], a[136:139], v[228:231]\n"
"s_waitcnt vmcnt(8) lgkmcnt(0)\n"
"v_mfma_f32_16x16x32_bf16 v[4:7], a[236:239], a[140:143], v[4:7]\n"
"s_barrier\n"
"v_mfma_f32_16x16x32_bf16 v[8:11], a[240:243], a[136:139], v[228:231]\n"
"s_mov_b32 m0, s51\n"
"ds_read_b64_tr_b16 a[192:193], v244 offset:0\n"
"v_mfma_f32_16x16x32_bf16 v[8:11], a[244:247], a[140:143], v[8:11]\n"
"buffer_load_dwordx4 v248, s[24:27], s49 offen lds\n"
"v_mfma_f32_16x16x32_bf16 v[12:15], a[248:251], a[136:139], v[228:231]\n"
"ds_read_b64_tr_b16 a[194:195], v244 offset:512\n"
"v_mfma_f32_16x16x32_bf16 v[12:15], a[252:255], a[140:143], v[12:15]\n"
"ds_read_b128 a[136:139], v246 offset:0\n"
"v_mfma_f32_16x16x32_bf16 v[28:31], a[248:251], a[144:147], v[228:231]\n"
"ds_read_b64_tr_b16 a[196:197], v244 offset:8192\n"
"v_mfma_f32_16x16x32_bf16 v[28:31], a[252:255], a[148:151], v[28:31]\n"
"ds_read_b64_tr_b16 a[198:199], v244 offset:8704\n"
"v_mfma_f32_16x16x32_bf16 v[24:27], a[240:243], a[144:147], v[228:231]\n"
"ds_read_b128 a[140:143], v247 offset:0\n"
"v_mfma_f32_16x16x32_bf16 v[24:27], a[244:247], a[148:151], v[24:27]\n"
"buffer_load_dwordx4 v249, s[24:27], s49 offen offset:1024 lds\n"
"v_mfma_f32_16x16x32_bf16 v[20:23], a[232:235], a[144:147], v[228:231]\n"
"ds_read_b64_tr_b16 a[200:201], v245 offset:0\n"
"v_mfma_f32_16x16x32_bf16 v[20:23], a[236:239], a[148:151], v[20:23]\n"
"ds_read_b64_tr_b16 a[202:203], v245 offset:512\n"
"v_mfma_f32_16x16x32_bf16 v[16:19], a[224:227], a[144:147], v[228:231]\n"
"ds_read_b64_tr_b16 a[204:205], v245 offset:8192\n"
"v_mfma_f32_16x16x32_bf16 v[16:19], a[228:231], a[148:151], v[16:19]\n"
"ds_read_b64_tr_b16 a[206:207], v245 offset:8704\n"
"v_mfma_f32_16x16x32_bf16 v[32:35], a[224:227], a[152:155], v[228:231]\n"
"ds_read_b64_tr_b16 a[208:209], v244 offset:1024\n"
"v_mfma_f32_16x16x32_bf16 v[32:35], a[228:231], a[156:159], v[32:35]\n"
"buffer_load_dwordx4 v254, s[24:27], s49 offen offset:2048 lds\n"
"v_mfma_f32_16x16x32_bf16 v[36:39], a[232:235], a[152:155], v[228:231]\n"
"ds_read_b64_tr_b16 a[210:211], v244 offset:1536\n"
"v_mfma_f32_16x16x32_bf16 v[36:39], a[236:239], a[156:159], v[36:39]\n"
"ds_read_b64_tr_b16 a[212:213], v244 offset:9216\n"
"v_mfma_f32_16x16x32_bf16 v[40:43], a[240:243], a[152:155], v[228:231]\n"
"ds_read_b64_tr_b16 a[214:215], v244 offset:9728\n"
"v_mfma_f32_16x16x32_bf16 v[40:43], a[244:247], a[156:159], v[40:43]\n"
"ds_read_b64_tr_b16 a[216:217], v245 offset:1024\n"
"v_mfma_f32_16x16x32_bf16 v[44:47], a[248:251], a[152:155], v[228:231]\n"
"ds_read_b64_tr_b16 a[218:219], v245 offset:1536\n"
"v_mfma_f32_16x16x32_bf16 v[44:47], a[252:255], a[156:159], v[44:47]\n"
"buffer_load_dwordx4 v255, s[24:27], s49 offen offset:3072 lds\n"
"v_mfma_f32_16x16x32_bf16 v[60:63], a[248:251], a[160:163], v[228:231]\n"
"ds_read_b64_tr_b16 a[220:221], v245 offset:9216\n"
"v_mfma_f32_16x16x32_bf16 v[60:63], a[252:255], a[164:167], v[60:63]\n"
"ds_read_b64_tr_b16 a[222:223], v245 offset:9728\n"
"v_mfma_f32_16x16x32_bf16 v[56:59], a[240:243], a[160:163], v[228:231]\n"
"ds_read_b128 a[144:147], v246 offset:2048\n"
"v_mfma_f32_16x16x32_bf16 v[56:59], a[244:247], a[164:167], v[56:59]\n"
"s_add_u32 s40, s49, 40960\n"
"ds_read_b128 a[148:151], v247 offset:2048\n"
"v_mfma_f32_16x16x32_bf16 v[52:55], a[232:235], a[160:163], v[228:231]\n"
"s_add_u32 m0, s51, 4096\n"
"ds_read_b128 a[152:155], v246 offset:4096\n"
"v_mfma_f32_16x16x32_bf16 v[52:55], a[236:239], a[164:167], v[52:55]\n"
"buffer_load_dwordx4 v248, s[24:27], s40 offen lds\n"
"v_mfma_f32_16x16x32_bf16 v[48:51], a[224:227], a[160:163], v[228:231]\n"
"ds_read_b128 a[156:159], v247 offset:4096\n"
"v_mfma_f32_16x16x32_bf16 v[48:51], a[228:231], a[164:167], v[48:51]\n"
"ds_read_b128 a[160:163], v252 offset:0\n"
"v_mfma_f32_16x16x32_bf16 v[64:67], a[224:227], a[168:171], v[228:231]\n"
"ds_read_b128 a[164:167], v253 offset:0\n"
"v_mfma_f32_16x16x32_bf16 v[64:67], a[228:231], a[172:175], v[64:67]\n"
"v_mfma_f32_16x16x32_bf16 v[68:71], a[232:235], a[168:171], v[228:231]\n"
"v_mfma_f32_16x16x32_bf16 v[68:71], a[236:239], a[172:175], v[68:71]\n"
"buffer_load_dwordx4 v249, s[24:27], s40 offen offset:1024 lds\n"
"v_mfma_f32_16x16x32_bf16 v[72:75], a[240:243], a[168:171], v[228:231]\n"
"v_mfma_f32_16x16x32_bf16 v[72:75], a[244:247], a[172:175], v[72:75]\n"
"v_mfma_f32_16x16x32_bf16 v[76:79], a[248:251], a[168:171], v[228:231]\n"
"ds_read_b128 a[168:171], v250 offset:0\n"
"v_mfma_f32_16x16x32_bf16 v[76:79], a[252:255], a[172:175], v[76:79]\n"
"ds_read_b128 a[172:175], v251 offset:0\n"
"v_mfma_f32_16x16x32_bf16 v[92:95], a[248:251], a[176:179], v[228:231]\n"
"v_mfma_f32_16x16x32_bf16 v[92:95], a[252:255], a[180:183], v[92:95]\n"
"buffer_load_dwordx4 v254, s[24:27], s40 offen offset:2048 lds\n"
"v_mfma_f32_16x16x32_bf16 v[88:91], a[240:243], a[176:179], v[228:231]\n"
"s_add_u32 s49, s49, 128\n"
"v_mfma_f32_16x16x32_bf16 v[88:91], a[244:247], a[180:183], v[88:91]\n"
"s_sub_u32 s41, s49, 1280\n"
"v_mfma_f32_16x16x32_bf16 v[84:87], a[232:235], a[176:179], v[228:231]\n"
"s_min_u32 s49, s49, s41\n"
"v_mfma_f32_16x16x32_bf16 v[84:87], a[236:239], a[180:183], v[84:87]\n"
"s_xor_b32 s51, s51, s74\n"
"v_mfma_f32_16x16x32_bf16 v[80:83], a[224:227], a[176:179], v[228:231]\n"
"ds_read_b128 a[176:179], v250 offset:2048\n"
"v_mfma_f32_16x16x32_bf16 v[80:83], a[228:231], a[180:183], v[80:83]\n"
"ds_read_b128 a[180:183], v251 offset:2048\n"
"v_mfma_f32_16x16x32_bf16 v[96:99], a[224:227], a[184:187], v[228:231]\n"
"v_mfma_f32_16x16x32_bf16 v[96:99], a[228:231], a[188:191], v[96:99]\n"
"v_mfma_f32_16x16x32_bf16 v[100:103], a[232:235], a[184:187], v[228:231]\n"
"v_mfma_f32_16x16x32_bf16 v[100:103], a[236:239], a[188:191], v[100:103]\n"
"v_mfma_f32_16x16x32_bf16 v[104:107], a[240:243], a[184:187], v[228:231]\n"
"v_mfma_f32_16x16x32_bf16 v[104:107], a[244:247], a[188:191], v[104:107]\n"
"v_mfma_f32_16x16x32_bf16 v[108:111], a[248:251], a[184:187], v[228:231]\n"
"s_add_u32 s47, s47, 1\n"
"ds_read_b128 a[184:187], v250 offset:4096\n"
"v_mfma_f32_16x16x32_bf16 v[108:111], a[252:255], a[188:191], v[108:111]\n"
"s_waitcnt lgkmcnt(1)\n"
"ds_read_b128 a[188:191], v251 offset:4096\n"
"LPL_%=:\n"
"v_mfma_f32_16x16x32_bf16 v[112:115], a[192:195], a[136:139], v[112:115]\n"
"v_mfma_f32_16x16x32_bf16 v[112:115], a[196:199], a[140:143], v[112:115]\n"
"v_mfma_f32_16x16x32_bf16 v[116:119], a[200:203], a[136:139], v[116:119]\n"
"s_waitcnt vmcnt(11)\n"
"v_mfma_f32_16x16x32_bf16 v[116:119], a[204:207], a[140:143], v[116:119]\n"
"s_barrier\n"
"v_mfma_f32_16x16x32_bf16 v[120:123], a[208:211], a[136:139], v[120:123]\n"
"s_add_u32 s40, s48, 256\n"
"s_xor_b32 m0, s50, 0xc000\n"
"ds_read_b64_tr_b16 a[224:225], v244 offset:16384\n"
"v_mfma_f32_16x16x32_bf16 v[120:123], a[212:215], a[140:143], v[120:123]\n"
"buffer_load_dwordx4 v224, s[28:31], s40 offen lds\n"
"v_mfma_f32_16x16x32_bf16 v[124:127], a[216:219], a[136:139], v[124:127]\n"
"ds_read_b64_tr_b16 a[226:227], v244 offset:16896\n"
"v_mfma_f32_16x16x32_bf16 v[124:127], a[220:223], a[140:143], v[124:127]\n"
"ds_read_b64_tr_b16 a[228:229], v244 offset:24576\n"
"v_mfma_f32_16x16x32_bf16 v[140:143], a[216:219], a[144:147], v[140:143]\n"
"ds_read_b64_tr_b16 a[230:231], v244 offset:25088\n"
"v_mfma_f32_16x16x32_bf16 v[140:143], a[220:223], a[148:151], v[140:143]\n"
"ds_read_b64_tr_b16 a[232:233], v245 offset:16384\n"
"v_mfma_f32_16x16x32_bf16 v[136:139], a[208:211], a[144:147], v[136:139]\n"
"ds_read_b64_tr_b16 a[234:235], v245 offset:16896\n"
"v_mfma_f32_16x16x32_bf16 v[136:139], a[212:215], a[148:151], v[136:139]\n"
"buffer_load_dwordx4 v225, s[28:31], s40 offen offset:1024 lds\n"
"v_mfma_f32_16x16x32_bf16 v[132:135], a[200:203], a[144:147], v[132:135]\n"
"ds_read_b64_tr_b16 a[236:237], v245 offset:24576\n"
"v_mfma_f32_16x16x32_bf16 v[132:135], a[204:207], a[148:151], v[132:135]\n"
"ds_read_b64_tr_b16 a[238:239], v245 offset:25088\n"
"v_mfma_f32_16x16x32_bf16 v[128:131], a[192:195], a[144:147], v[128:131]\n"
"ds_read_b64_tr_b16 a[240:241], v244 offset:17408\n"
"v_mfma_f32_16x16x32_bf16 v[128:131], a[196:199], a[148:151], v[128:131]\n"
"ds_read_b64_tr_b16 a[242:243], v244 offset:17920\n"
"v_mfma_f32_16x16x32_bf16 v[144:147], a[192:195], a[152:155], v[144:147]\n"
"ds_read_b64_tr_b16 a[244:245], v244 offset:25600\n"
"v_mfma_f32_16x16x32_bf16 v[144:147], a[196:199], a[156:159], v[144:147]\n"
"buffer_load_dwordx4 v226, s[28:31], s40 offen offset:2048 lds\n"
"v_mfma_f32_16x16x32_bf16 v[148:151], a[200:203], a[152:155], v[148:151]\n"
"ds_read_b64_tr_b16 a[246:247], v244 offset:26112\n"
"v_mfma_f32_16x16x32_bf16 v[148:151], a[204:207], a[156:159], v[148:151]\n"
"ds_read_b64_tr_b16 a[248:249], v245 offset:17408\n"
"v_mfma_f32_16x16x32_bf16 v[152:155], a[208:211], a[152:155], v[152:155]\n"
"ds_read_b64_tr_b16 a[250:251], v245 offset:17920\n"
"v_mfma_f32_16x16x32_bf16 v[152:155], a[212:215], a[156:159], v[152:155]\n"
"ds_read_b64_tr_b16 a[252:253], v245 offset:25600\n"
"v_mfma_f32_16x16x32_bf16 v[156:159], a[216:219], a[152:155], v[156:159]\n"
"ds_read_b64_tr_b16 a[254:255], v245 offset:26112\n"
"v_mfma_f32_16x16x32_bf16 v[156:159], a[220:223], a[156:159], v[156:159]\n"
"buffer_load_dwordx4 v227, s[28:31], s40 offen offset:3072 lds\n"
"v_mfma_f32_16x16x32_bf16 v[172:175], a[216:219], a[160:163], v[172:175]\n"
"s_cmp_eq_u32 s47, 8\n"
"v_mfma_f32_16x16x32_bf16 v[172:175], a[220:223], a[164:167], v[172:175]\n"
"s_cselect_b32 s41, s52, s58\n"
"v_mfma_f32_16x16x32_bf16 v[168:171], a[208:211], a[160:163], v[168:171]\n"
"s_add_u32 s48, s48, s41\n"
"v_mfma_f32_16x16x32_bf16 v[168:171], a[212:215], a[164:167], v[168:171]\n"
"s_sub_u32 s41, s48, 5242880\n"
"v_mfma_f32_16x16x32_bf16 v[164:167], a[200:203], a[160:163], v[164:167]\n"
"s_min_u32 s48, s48, s41\n"
"v_mfma_f32_16x16x32_bf16 v[164:167], a[204:207], a[164:167], v[164:167]\n"
"v_xor_b32 v244, s74, v244\n"
"v_xor_b32 v246, s74, v246\n"
"v_mfma_f32_16x16x32_bf16 v[160:163], a[192:195], a[160:163], v[160:163]\n"
"s_mov_b32 m0, s50\n"
"v_xor_b32 v245, s74, v245\n"
"v_xor_b32 v247, s74, v247\n"
"v_mfma_f32_16x16x32_bf16 v[160:163], a[196:199], a[164:167], v[160:163]\n"
"buffer_load_dwordx4 v224, s[28:31], s48 offen lds\n"
"v_mfma_f32_16x16x32_bf16 v[176:179], a[192:195], a[168:171], v[176:179]\n"
"v_xor_b32 v252, s74, v252\n"
"v_xor_b32 v253, s74, v253\n"
"v_mfma_f32_16x16x32_bf16 v[176:179], a[196:199], a[172:175], v[176:179]\n"
"v_xor_b32 v250, s74, v250\n"
"v_xor_b32 v251, s74, v251\n"
"v_mfma_f32_16x16x32_bf16 v[180:183], a[200:203], a[168:171], v[180:183]\n"
"v_mfma_f32_16x16x32_bf16 v[180:183], a[204:207], a[172:175], v[180:183]\n"
"buffer_load_dwordx4 v225, s[28:31], s48 offen offset:1024 lds\n"
"v_mfma_f32_16x16x32_bf16 v[184:187], a[208:211], a[168:171], v[184:187]\n"
"v_mfma_f32_16x16x32_bf16 v[184:187], a[212:215], a[172:175], v[184:187]\n"
"v_mfma_f32_16x16x32_bf16 v[188:191], a[216:219], a[168:171], v[188:191]\n"
"v_mfma_f32_16x16x32_bf16 v[188:191], a[220:223], a[172:175], v[188:191]\n"
"v_mfma_f32_16x16x32_bf16 v[204:207], a[216:219], a[176:179], v[204:207]\n"
"s_waitcnt lgkmcnt(0)\n"
"v_mfma_f32_16x16x32_bf16 v[204:207], a[220:223], a[180:183], v[204:207]\n"
"buffer_load_dwordx4 v226, s[28:31], s48 offen offset:2048 lds\n"
"v_mfma_f32_16x16x32_bf16 v[200:203], a[208:211], a[176:179], v[200:203]\n"
"v_mfma_f32_16x16x32_bf16 v[200:203], a[212:215], a[180:183], v[200:203]\n"
"v_mfma_f32_16x16x32_bf16 v[196:199], a[200:203], a[176:179], v[196:199]\n"
"v_mfma_f32_16x16x32_bf16 v[196:199], a[204:207], a[180:183], v[196:199]\n"
"v_mfma_f32_16x16x32_bf16 v[192:195], a[192:195], a[176:179], v[192:195]\n"
"v_mfma_f32_16x16x32_bf16 v[192:195], a[196:199], a[180:183], v[192:195]\n"
"s_waitcnt lgkmcnt(0)\n"
"buffer_load_dwordx4 v227, s[28:31], s48 offen offset:3072 lds\n"
"v_mfma_f32_16x16x32_bf16 v[208:211], a[192:195], a[184:187], v[208:211]\n"
"s_xor_b32 s50, s50, s74\n"
"v_mfma_f32_16x16x32_bf16 v[208:211], a[196:199], a[188:191], v[208:211]\n"
"v_mfma_f32_16x16x32_bf16 v[212:215], a[200:203], a[184:187], v[212:215]\n"
"v_mfma_f32_16x16x32_bf16 v[212:215], a[204:207], a[188:191], v[212:215]\n"
"v_mfma_f32_16x16x32_bf16 v[216:219], a[208:211], a[184:187], v[216:219]\n"
"v_mfma_f32_16x16x32_bf16 v[216:219], a[212:215], a[188:191], v[216:219]\n"
"v_mfma_f32_16x16x32_bf16 v[220:223], a[216:219], a[184:187], v[220:223]\n"
"v_mfma_f32_16x16x32_bf16 v[220:223], a[220:223], a[188:191], v[220:223]\n"
"v_mfma_f32_16x16x32_bf16 v[0:3], a[224:227], a[136:139], v[0:3]\n"
"v_mfma_f32_16x16x32_bf16 v[0:3], a[228:231], a[140:143], v[0:3]\n"
"v_mfma_f32_16x16x32_bf16 v[4:7], a[232:235], a[136:139], v[4:7]\n"
"s_waitcnt vmcnt(8)\n"
"v_mfma_f32_16x16x32_bf16 v[4:7], a[236:239], a[140:143], v[4:7]\n"
"s_barrier\n"
"v_mfma_f32_16x16x32_bf16 v[8:11], a[240:243], a[136:139], v[8:11]\n"
"s_mov_b32 m0, s51\n"
"ds_read_b64_tr_b16 a[192:193], v244 offset:0\n"
"v_mfma_f32_16x16x32_bf16 v[8:11], a[244:247], a[140:143], v[8:11]\n"
"buffer_load_dwordx4 v248, s[24:27], s49 offen lds\n"
"v_mfma_f32_16x16x32_bf16 v[12:15], a[248:251], a[136:139], v[12:15]\n"
"ds_read_b64_tr_b16 a[194:195], v244 offset:512\n"
"v_mfma_f32_16x16x32_bf16 v[12:15], a[252:255], a[140:143], v[12:15]\n"
"ds_read_b128 a[136:139], v246 offset:0\n"
"v_mfma_f32_16x16x32_bf16 v[28:31], a[248:251], a[144:147], v[28:31]\n"
"ds_read_b64_tr_b16 a[196:197], v244 offset:8192\n"
"v_mfma_f32_16x16x32_bf16 v[28:31], a[252:255], a[148:151], v[28:31]\n"
"ds_read_b64_tr_b16 a[198:199], v244 offset:8704\n"
"v_mfma_f32_16x16x32_bf16 v[24:27], a[240:243], a[144:147], v[24:27]\n"
"ds_read_b128 a[140:143], v247 offset:0\n"
"v_mfma_f32_16x16x32_bf16 v[24:27], a[244:247], a[148:151], v[24:27]\n"
"buffer_load_dwordx4 v249, s[24:27], s49 offen offset:1024 lds\n"
"v_mfma_f32_16x16x32_bf16 v[20:23], a[232:235], a[144:147], v[20:23]\n"
"ds_read_b64_tr_b16 a[200:201], v245 offset:0\n"
"v_mfma_f32_16x16x32_bf16 v[20:23], a[236:239], a[148:151], v[20:23]\n"
"ds_read_b64_tr_b16 a[202:203], v245 offset:512\n"
"v_mfma_f32_16x16x32_bf16 v[16:19], a[224:227], a[144:147], v[16:19]\n"
"ds_read_b64_tr_b16 a[204:205], v245 offset:8192\n"
"v_mfma_f32_16x16x32_bf16 v[16:19], a[228:231], a[148:151], v[16:19]\n"
"ds_read_b64_tr_b16 a[206:207], v245 offset:8704\n"
"v_mfma_f32_16x16x32_bf16 v[32:35], a[224:227], a[152:155], v[32:35]\n"
"ds_read_b64_tr_b16 a[208:209], v244 offset:1024\n"
"v_mfma_f32_16x16x32_bf16 v[32:35], a[228:231], a[156:159], v[32:35]\n"
"buffer_load_dwordx4 v254, s[24:27], s49 offen offset:2048 lds\n"
"v_mfma_f32_16x16x32_bf16 v[36:39], a[232:235], a[152:155], v[36:39]\n"
"ds_read_b64_tr_b16 a[210:211], v244 offset:1536\n"
"v_mfma_f32_16x16x32_bf16 v[36:39], a[236:239], a[156:159], v[36:39]\n"
"ds_read_b64_tr_b16 a[212:213], v244 offset:9216\n"
"v_mfma_f32_16x16x32_bf16 v[40:43], a[240:243], a[152:155], v[40:43]\n"
"ds_read_b64_tr_b16 a[214:215], v244 offset:9728\n"
"v_mfma_f32_16x16x32_bf16 v[40:43], a[244:247], a[156:159], v[40:43]\n"
"ds_read_b64_tr_b16 a[216:217], v245 offset:1024\n"
"v_mfma_f32_16x16x32_bf16 v[44:47], a[248:251], a[152:155], v[44:47]\n"
"ds_read_b64_tr_b16 a[218:219], v245 offset:1536\n"
"v_mfma_f32_16x16x32_bf16 v[44:47], a[252:255], a[156:159], v[44:47]\n"
"buffer_load_dwordx4 v255, s[24:27], s49 offen offset:3072 lds\n"
"v_mfma_f32_16x16x32_bf16 v[60:63], a[248:251], a[160:163], v[60:63]\n"
"ds_read_b64_tr_b16 a[220:221], v245 offset:9216\n"
"v_mfma_f32_16x16x32_bf16 v[60:63], a[252:255], a[164:167], v[60:63]\n"
"ds_read_b64_tr_b16 a[222:223], v245 offset:9728\n"
"v_mfma_f32_16x16x32_bf16 v[56:59], a[240:243], a[160:163], v[56:59]\n"
"ds_read_b128 a[144:147], v246 offset:2048\n"
"v_mfma_f32_16x16x32_bf16 v[56:59], a[244:247], a[164:167], v[56:59]\n"
"s_add_u32 s40, s49, 40960\n"
"ds_read_b128 a[148:151], v247 offset:2048\n"
"v_mfma_f32_16x16x32_bf16 v[52:55], a[232:235], a[160:163], v[52:55]\n"
"s_add_u32 m0, s51, 4096\n"
"ds_read_b128 a[152:155], v246 offset:4096\n"
"v_mfma_f32_16x16x32_bf16 v[52:55], a[236:239], a[164:167], v[52:55]\n"
"buffer_load_dwordx4 v248, s[24:27], s40 offen lds\n"
"v_mfma_f32_16x16x32_bf16 v[48:51], a[224:227], a[160:163], v[48:51]\n"
"ds_read_b128 a[156:159], v247 offset:4096\n"
"v_mfma_f32_16x16x32_bf16 v[48:51], a[228:231], a[164:167], v[48:51]\n"
"ds_read_b128 a[160:163], v252 offset:0\n"
"v_mfma_f32_16x16x32_bf16 v[64:67], a[224:227], a[168:171], v[64:67]\n"
"ds_read_b128 a[164:167], v253 offset:0\n"
"v_mfma_f32_16x16x32_bf16 v[64:67], a[228:231], a[172:175], v[64:67]\n"
"v_mfma_f32_16x16x32_bf16 v[68:71], a[232:235], a[168:171], v[68:71]\n"
"v_mfma_f32_16x16x32_bf16 v[68:71], a[236:239], a[172:175], v[68:71]\n"
"buffer_load_dwordx4 v249, s[24:27], s40 offen offset:1024 lds\n"
"v_mfma_f32_16x16x32_bf16 v[72:75], a[240:243], a[168:171], v[72:75]\n"
"v_mfma_f32_16x16x32_bf16 v[72:75], a[244:247], a[172:175], v[72:75]\n"
"v_mfma_f32_16x16x32_bf16 v[76:79], a[248:251], a[168:171], v[76:79]\n"
"ds_read_b128 a[168:171], v250 offset:0\n"
"v_mfma_f32_16x16x32_bf16 v[76:79], a[252:255], a[172:175], v[76:79]\n"
"ds_read_b128 a[172:175], v251 offset:0\n"
"v_mfma_f32_16x16x32_bf16 v[92:95], a[248:251], a[176:179], v[92:95]\n"
"v_mfma_f32_16x16x32_bf16 v[92:95], a[252:255], a[180:183], v[92:95]\n"
"buffer_load_dwordx4 v254, s[24:27], s40 offen offset:2048 lds\n"
"v_mfma_f32_16x16x32_bf16 v[88:91], a[240:243], a[176:179], v[88:91]\n"
"s_add_u32 s49, s49, 128\n"
"v_mfma_f32_16x16x32_bf16 v[88:91], a[244:247], a[180:183], v[88:91]\n"
"s_sub_u32 s41, s49, 1280\n"
"v_mfma_f32_16x16x32_bf16 v[84:87], a[232:235], a[176:179], v[84:87]\n"
"s_min_u32 s49, s49, s41\n"
"v_mfma_f32_16x16x32_bf16 v[84:87], a[236:239], a[180:183], v[84:87]\n"
"s_xor_b32 s51, s51, s74\n"
"v_mfma_f32_16x16x32_bf16 v[80:83], a[224:227], a[176:179], v[80:83]\n"
"ds_read_b128 a[176:179], v250 offset:2048\n"
"v_mfma_f32_16x16x32_bf16 v[80:83], a[228:231], a[180:183], v[80:83]\n"
"ds_read_b128 a[180:183], v251 offset:2048\n"
"v_mfma_f32_16x16x32_bf16 v[96:99], a[224:227], a[184:187], v[96:99]\n"
"v_mfma_f32_16x16x32_bf16 v[96:99], a[228:231], a[188:191], v[96:99]\n"
"v_mfma_f32_16x16x32_bf16 v[100:103], a[232:235], a[184:187], v[100:103]\n"
"v_mfma_f32_16x16x32_bf16 v[100:103], a[236:239], a[188:191], v[100:103]\n"
"v_mfma_f32_16x16x32_bf16 v[104:107], a[240:243], a[184:187], v[104:107]\n"
"v_mfma_f32_16x16x32_bf16 v[104:107], a[244:247], a[188:191], v[104:107]\n"
"v_mfma_f32_16x16x32_bf16 v[108:111], a[248:251], a[184:187], v[108:111]\n"
"s_add_u32 s47, s47, 1\n"
"ds_read_b128 a[184:187], v250 offset:4096\n"
"v_mfma_f32_16x16x32_bf16 v[108:111], a[252:255], a[188:191], v[108:111]\n"
"s_waitcnt lgkmcnt(1)\n"
"s_cmp_lg_u32 s47, s62\n"
"ds_read_b128 a[188:191], v251 offset:4096\n"
"s_cbranch_scc1 LPL_%=\n"
"s_cmp_eq_u32 s47, 18\n"
"s_cbranch_scc1 LPT2_%=\n"
"v_mfma_f32_16x16x32_bf16 v[112:115], a[192:195], a[136:139], v[112:115]\n"
"s_add_u32 s78, s96, 8192\n"
"v_mov_b32 v241, 1\n"
"v_mfma_f32_16x16x32_bf16 v[112:115], a[196:199], a[140:143], v[112:115]\n"
"s_mov_b64 exec, 1\n"
"buffer_store_dword v241, off, s[32:35], s96 sc0 sc1\n"
"buffer_store_dword v241, off, s[32:35], s78 sc0 sc1\n"
"s_mov_b64 exec, -1\n"
"v_mfma_f32_16x16x32_bf16 v[116:119], a[200:203], a[136:139], v[116:119]\n"
"s_waitcnt vmcnt(13)\n"
"v_mfma_f32_16x16x32_bf16 v[116:119], a[204:207], a[140:143], v[116:119]\n"
"s_barrier\n"
"v_mfma_f32_16x16x32_bf16 v[120:123], a[208:211], a[136:139], v[120:123]\n"
"s_add_u32 s40, s48, 256\n"
"s_xor_b32 m0, s50, 0xc000\n"
"ds_read_b64_tr_b16 a[252:253], v245 offset:25600\n"
"v_mfma_f32_16x16x32_bf16 v[120:123], a[212:215], a[140:143], v[120:123]\n"
"buffer_load_dwordx4 v224, s[28:31], s40 offen lds\n"
"v_mfma_f32_16x16x32_bf16 v[124:127], a[216:219], a[136:139], v[124:127]\n"
"v_mfma_f32_16x16x32_bf16 v[124:127], a[220:223], a[140:143], v[124:127]\n"
"v_mfma_f32_16x16x32_bf16 v[188:191], a[216:219], a[168:171], v[188:191]\n"
"ds_read_b64_tr_b16 a[246:247], v244 offset:26112\n"
"v_mfma_f32_16x16x32_bf16 v[188:191], a[220:223], a[172:175], v[188:191]\n"
"ds_read_b64_tr_b16 a[254:255], v245 offset:26112\n"
"v_mfma_f32_16x16x32_bf16 v[184:187], a[208:211], a[168:171], v[184:187]\n"
"ds_read_b64_tr_b16 a[232:233], v245 offset:16384\n"
"ds_read_b64_tr_b16 a[248:249], v245 offset:17408\n"
"v_mfma_f32_16x16x32_bf16 v[184:187], a[212:215], a[172:175], v[184:187]\n"
"buffer_load_dwordx4 v225, s[28:31], s40 offen offset:1024 lds\n"
"v_mfma_f32_16x16x32_bf16 v[180:183], a[200:203], a[168:171], v[180:183]\n"
"v_mfma_f32_16x16x32_bf16 v[180:183], a[204:207], a[172:175], v[180:183]\n"
"v_mfma_f32_16x16x32_bf16 v[176:179], a[192:195], a[168:171], v[176:179]\n"
"ds_read_b64_tr_b16 a[234:235], v245 offset:16896\n"
"ds_read_b64_tr_b16 a[250:251], v245 offset:17920\n"
"v_mfma_f32_16x16x32_bf16 v[176:179], a[196:199], a[172:175], v[176:179]\n"
"ds_read_b64_tr_b16 a[244:245], v244 offset:25600\n"
"v_mfma_f32_16x16x32_bf16 v[192:195], a[192:195], a[176:179], v[192:195]\n"
"ds_read_b64_tr_b16 a[236:237], v245 offset:24576\n"
"ds_read_b64_tr_b16 a[240:241], v244 offset:17408\n"
"v_mfma_f32_16x16x32_bf16 v[192:195], a[196:199], a[180:183], v[192:195]\n"
"buffer_load_dwordx4 v226, s[28:31], s40 offen offset:2048 lds\n"
"v_mfma_f32_16x16x32_bf16 v[196:199], a[200:203], a[176:179], v[196:199]\n"
"v_mfma_f32_16x16x32_bf16 v[196:199], a[204:207], a[180:183], v[196:199]\n"
"v_mfma_f32_16x16x32_bf16 v[200:203], a[208:211], a[176:179], v[200:203]\n"
"ds_read_b64_tr_b16 a[230:231], v244 offset:25088\n"
"ds_read_b64_tr_b16 a[238:239], v245 offset:25088\n"
"v_mfma_f32_16x16x32_bf16 v[200:203], a[212:215], a[180:183], v[200:203]\n"
"ds_read_b64_tr_b16 a[224:225], v244 offset:16384\n"
"v_mfma_f32_16x16x32_bf16 v[204:207], a[216:219], a[176:179], v[204:207]\n"
"ds_read_b64_tr_b16 a[226:227], v244 offset:16896\n"
"ds_read_b64_tr_b16 a[242:243], v244 offset:17920\n"
"v_mfma_f32_16x16x32_bf16 v[204:207], a[220:223], a[180:183], v[204:207]\n"
"s_waitcnt lgkmcnt(7)\n"
"buffer_load_dwordx4 v227, s[28:31], s40 offen offset:3072 lds\n"
"v_mfma_f32_16x16x32_bf16 v[220:223], a[216:219], a[184:187], v[220:223]\n"
"s_cmp_eq_u32 s47, 8\n"
"v_mfma_f32_16x16x32_bf16 v[220:223], a[220:223], a[188:191], v[220:223]\n"
"s_cselect_b32 s41, s52, s58\n"
"s_nop 1\n"
"v_pk_fma_f32 v[176:177], v[176:177], s[70:71], s[70:71] op_sel:[0,0,1] op_sel_hi:[1,0,1]\nv_pk_fma_f32 v[178:179], v[178:179], s[70:71], s[70:71] op_sel:[0,0,1] op_sel_hi:[1,0,1]\nv_pk_fma_f32 v[180:181], v[180:181], s[70:71], s[70:71] op_sel:[0,0,1] op_sel_hi:[1,0,1]\nv_pk_fma_f32 v[182:183], v[182:183], s[70:71], s[70:71] op_sel:[0,0,1] op_sel_hi:[1,0,1]\nv_pk_fma_f32 v[184:185], v[184:185], s[70:71], s[70:71] op_sel:[0,0,1] op_sel_hi:[1,0,1]\nv_pk_fma_f32 v[186:187], v[186:187], s[70:71], s[70:71] op_sel:[0,0,1] op_sel_hi:[1,0,1]\nv_pk_fma_f32 v[188:189], v[188:189], s[70:71], s[70:71] op_sel:[0,0,1] op_sel_hi:[1,0,1]\nv_pk_fma_f32 v[190:191], v[190:191], s[70:71], s[70:71] op_sel:[0,0,1] op_sel_hi:[1,0,1]\nv_pk_fma_f32 v[192:193], v[192:193], s[70:71], s[70:71] op_sel:[0,0,1] op_sel_hi:[1,0,1]\nv_pk_fma_f32 v[194:195], v[194:195], s[70:71], s[70:71] op_sel:[0,0,1] op_sel_hi:[1,0,1]\nv_pk_fma_f32 v[196:197], v[196:197], s[70:71], s[70:71] op_sel:[0,0,1] op_sel_hi:[1,0,1]\nv_pk_fma_f32 v[198:199], v[198:199], s[70:71], s[70:71] op_sel:[0,0,1] op_sel_hi:[1,0,1]\nv_pk_fma_f32 v[200:201], v[200:201], s[70:71], s[70:71] op_sel:[0,0,1] op_sel_hi:[1,0,1]\nv_pk_fma_f32 v[202:203], v[202:203], s[70:71], s[70:71] op_sel:[0,0,1] op_sel_hi:[1,0,1]\nv_pk_fma_f32 v[204:205], v[204:205], s[70:71], s[70:71] op_sel:[0,0,1] op_sel_hi:[1,0,1]\nv_pk_fma_f32 v[206:207], v[206:207], s[70:71], s[70:71] op_sel:[0,0,1] op_sel_hi:[1,0,1]\n"
"v_mfma_f32_16x16x32_bf16 v[216:219], a[208:211], a[184:187], v[216:219]\n"
"s_add_u32 s48, s48, s41\n"
"v_cvt_pk_bf16_f32 v200, v200, v201\n"
"v_cvt_pk_bf16_f32 v201, v202, v203\n"
"v_mfma_f32_16x16x32_bf16 v[216:219], a[212:215], a[188:191], v[216:219]\n"
"s_sub_u32 s41, s48, 5242880\n"
"v_cvt_pk_bf16_f32 v176, v176, v177\n"
"v_cvt_pk_bf16_f32 v177, v178, v179\n"
"v_mfma_f32_16x16x32_bf16 v[212:215], a[200:203], a[184:187], v[212:215]\n"
"s_min_u32 s48, s48, s41\n"
"v_cvt_pk_bf16_f32 v179, v182, v183\n"
"v_cvt_pk_bf16_f32 v192, v192, v193\n"
"v_mfma_f32_16x16x32_bf16 v[212:215], a[204:207], a[188:191], v[212:215]\n"
"ds_read_b64_tr_b16 a[228:229], v244 offset:24576\n"
"v_cvt_pk_bf16_f32 v184, v184, v185\n"
"v_mfma_f32_16x16x32_bf16 v[208:211], a[192:195], a[184:187], v[208:211]\n"
"s_mov_b32 m0, s50\n"
"v_cvt_pk_bf16_f32 v178, v180, v181\n"
"v_bfe_u32 v181, v242, 9, 1\n"
"v_mfma_f32_16x16x32_bf16 v[208:211], a[196:199], a[188:191], v[208:211]\n"
"buffer_load_dwordx4 v224, s[28:31], s48 offen lds\n"
"v_mfma_f32_16x16x32_bf16 v[160:163], a[192:195], a[160:163], v[160:163]\n"
"v_cvt_pk_bf16_f32 v193, v194, v195\n"
"v_xor_b32 v246, s74, v246\n"
"v_mfma_f32_16x16x32_bf16 v[160:163], a[196:199], a[164:167], v[160:163]\n"
"s_mov_b64 s[80:81], s[8:9]\n"
"v_and_b32 v182, 0xf0, v242\n"
"v_cvt_pk_bf16_f32 v185, v186, v187\n"
"v_mfma_f32_16x16x32_bf16 v[164:167], a[200:203], a[160:163], v[164:167]\n"
"s_mov_b32 s82, s68\n"
"v_bfe_u32 v180, v242, 9, 1\n"
"v_cvt_pk_bf16_f32 v203, v206, v207\n"
"v_mfma_f32_16x16x32_bf16 v[164:167], a[204:207], a[164:167], v[164:167]\n"
"s_mov_b32 s83, 131072\n"
"buffer_load_dwordx4 v225, s[28:31], s48 offen offset:1024 lds\n"
"v_mfma_f32_16x16x32_bf16 v[168:171], a[208:211], a[160:163], v[168:171]\n"
"v_and_b32 v241, 0x3f0, v242\n"
"v_cvt_pk_bf16_f32 v186, v188, v189\n"
"v_xor_b32 v253, s74, v253\n"
"v_mfma_f32_16x16x32_bf16 v[168:171], a[212:215], a[164:167], v[168:171]\n"
"v_mul_u32_u24 v241, 10, v241\n"
"v_lshl_add_u32 v180, v180, 8, v182\n"
"v_cvt_pk_bf16_f32 v202, v204, v205\n"
"v_mfma_f32_16x16x32_bf16 v[172:175], a[216:219], a[160:163], v[172:175]\n"
"v_bfe_u32 v182, v242, 8, 1\n"
"v_xor_b32 v244, s74, v244\n"
"v_mfma_f32_16x16x32_bf16 v[172:175], a[220:223], a[164:167], v[172:175]\n"
"v_lshl_add_u32 v180, v182, 3, v180\n"
"v_xor_b32_e64 v182, s42, 1\n"
"v_mfma_f32_16x16x32_bf16 v[140:143], a[216:219], a[144:147], v[140:143]\n"
"v_lshl_add_u32 v180, v182, 10, v180\n"
"v_add_u32 v180, 94208, v180\n"
"v_mfma_f32_16x16x32_bf16 v[140:143], a[220:223], a[148:151], v[140:143]\n"
"buffer_load_dwordx4 v226, s[28:31], s48 offen offset:2048 lds\n"
"v_mfma_f32_16x16x32_bf16 v[136:139], a[208:211], a[144:147], v[136:139]\n"
"v_cvt_pk_bf16_f32 v194, v196, v197\n"
"v_cvt_pk_bf16_f32 v195, v198, v199\n"
"v_mfma_f32_16x16x32_bf16 v[136:139], a[212:215], a[148:151], v[136:139]\n"
"v_pk_fma_f32 v[208:209], v[208:209], s[70:71], s[70:71] op_sel:[0,0,1] op_sel_hi:[1,0,1]\nv_pk_fma_f32 v[210:211], v[210:211], s[70:71], s[70:71] op_sel:[0,0,1] op_sel_hi:[1,0,1]\nv_pk_fma_f32 v[212:213], v[212:213], s[70:71], s[70:71] op_sel:[0,0,1] op_sel_hi:[1,0,1]\nv_pk_fma_f32 v[214:215], v[214:215], s[70:71], s[70:71] op_sel:[0,0,1] op_sel_hi:[1,0,1]\nv_pk_fma_f32 v[216:217], v[216:217], s[70:71], s[70:71] op_sel:[0,0,1] op_sel_hi:[1,0,1]\nv_pk_fma_f32 v[218:219], v[218:219], s[70:71], s[70:71] op_sel:[0,0,1] op_sel_hi:[1,0,1]\nv_pk_fma_f32 v[220:221], v[220:221], s[70:71], s[70:71] op_sel:[0,0,1] op_sel_hi:[1,0,1]\nv_pk_fma_f32 v[222:223], v[222:223], s[70:71], s[70:71] op_sel:[0,0,1] op_sel_hi:[1,0,1]\nv_pk_fma_f32 v[160:161], v[160:161], s[70:71], s[70:71] op_sel:[0,0,1] op_sel_hi:[1,0,1]\nv_pk_fma_f32 v[162:163], v[162:163], s[70:71], s[70:71] op_sel:[0,0,1] op_sel_hi:[1,0,1]\nv_pk_fma_f32 v[164:165], v[164:165], s[70:71], s[70:71] op_sel:[0,0,1] op_sel_hi:[1,0,1]\nv_pk_fma_f32 v[166:167], v[166:167], s[70:71], s[70:71] op_sel:[0,0,1] op_sel_hi:[1,0,1]\nv_pk_fma_f32 v[168:169], v[168:169], s[70:71], s[70:71] op_sel:[0,0,1] op_sel_hi:[1,0,1]\nv_pk_fma_f32 v[170:171], v[170:171], s[70:71], s[70:71] op_sel:[0,0,1] op_sel_hi:[1,0,1]\nv_pk_fma_f32 v[172:173], v[172:173], s[70:71], s[70:71] op_sel:[0,0,1] op_sel_hi:[1,0,1]\nv_pk_fma_f32 v[174:175], v[174:175], s[70:71], s[70:71] op_sel:[0,0,1] op_sel_hi:[1,0,1]\n"
"v_mfma_f32_16x16x32_bf16 v[132:135], a[200:203], a[144:147], v[132:135]\n"
"v_cvt_pk_bf16_f32 v168, v168, v169\n"
"v_and_b32 v182, 0xf0, v242\n"
"v_mfma_f32_16x16x32_bf16 v[132:135], a[204:207], a[148:151], v[132:135]\n"
"ds_write2st64_b64 v180, v[192:193], v[194:195] offset0:88 offset1:89\n"
"v_cvt_pk_bf16_f32 v187, v190, v191\n"
"v_cvt_pk_bf16_f32 v208, v208, v209\n"
"v_mfma_f32_16x16x32_bf16 v[128:131], a[192:195], a[144:147], v[128:131]\n"
"v_cvt_pk_bf16_f32 v209, v210, v211\n"
"v_cvt_pk_bf16_f32 v169, v170, v171\n"
"v_mfma_f32_16x16x32_bf16 v[128:131], a[196:199], a[148:151], v[128:131]\n"
"buffer_load_dwordx4 v227, s[28:31], s48 offen offset:3072 lds\n"
"v_mfma_f32_16x16x32_bf16 v[144:147], a[192:195], a[152:155], v[144:147]\n"
"s_xor_b32 s50, s50, s74\n"
"v_cvt_pk_bf16_f32 v210, v212, v213\n"
"v_cvt_pk_bf16_f32 v216, v216, v217\n"
"v_mfma_f32_16x16x32_bf16 v[144:147], a[196:199], a[156:159], v[144:147]\n"
"v_xor_b32 v245, s74, v245\n"
"v_xor_b32 v247, s74, v247\n"
"v_mfma_f32_16x16x32_bf16 v[148:151], a[200:203], a[152:155], v[148:151]\n"
"v_cvt_pk_bf16_f32 v211, v214, v215\n"
"v_cvt_pk_bf16_f32 v160, v160, v161\n"
"v_mfma_f32_16x16x32_bf16 v[148:151], a[204:207], a[156:159], v[148:151]\n"
"v_pk_fma_f32 v[112:113], v[112:113], s[70:71], s[70:71] op_sel:[0,0,1] op_sel_hi:[1,0,1]\nv_pk_fma_f32 v[114:115], v[114:115], s[70:71], s[70:71] op_sel:[0,0,1] op_sel_hi:[1,0,1]\nv_pk_fma_f32 v[116:117], v[116:117], s[70:71], s[70:71] op_sel:[0,0,1] op_sel_hi:[1,0,1]\nv_pk_fma_f32 v[118:119], v[118:119], s[70:71], s[70:71] op_sel:[0,0,1] op_sel_hi:[1,0,1]\nv_pk_fma_f32 v[120:121], v[120:121], s[70:71], s[70:71] op_sel:[0,0,1] op_sel_hi:[1,0,1]\nv_pk_fma_f32 v[122:123], v[122:123], s[70:71], s[70:71] op_sel:[0,0,1] op_sel_hi:[1,0,1]\nv_pk_fma_f32 v[124:125], v[124:125], s[70:71], s[70:71] op_sel:[0,0,1] op_sel_hi:[1,0,1]\nv_pk_fma_f32 v[126:127], v[126:127], s[70:71], s[70:71] op_sel:[0,0,1] op_sel_hi:[1,0,1]\nv_pk_fma_f32 v[128:129], v[128:129], s[70:71], s[70:71] op_sel:[0,0,1] op_sel_hi:[1,0,1]\nv_pk_fma_f32 v[130:131], v[130:131], s[70:71], s[70:71] op_sel:[0,0,1] op_sel_hi:[1,0,1]\nv_pk_fma_f32 v[132:133], v[132:133], s[70:71], s[70:71] op_sel:[0,0,1] op_sel_hi:[1,0,1]\nv_pk_fma_f32 v[134:135], v[134:135], s[70:71], s[70:71] op_sel:[0,0,1] op_sel_hi:[1,0,1]\nv_pk_fma_f32 v[136:137], v[136:137], s[70:71], s[70:71] op_sel:[0,0,1] op_sel_hi:[1,0,1]\nv_pk_fma_f32 v[138:139], v[138:139], s[70:71], s[70:71] op_sel:[0,0,1] op_sel_hi:[1,0,1]\nv_pk_fma_f32 v[140:141], v[140:141], s[70:71], s[70:71] op_sel:[0,0,1] op_sel_hi:[1,0,1]\nv_pk_fma_f32 v[142:143], v[142:143], s[70:71], s[70:71] op_sel:[0,0,1] op_sel_hi:[1,0,1]\n"
"v_mfma_f32_16x16x32_bf16 v[152:155], a[208:211], a[152:155], v[152:155]\n"
"v_cvt_pk_bf16_f32 v120, v120, v121\n"
"v_lshl_add_u32 v181, v181, 8, v182\n"
"v_mfma_f32_16x16x32_bf16 v[152:155], a[212:215], a[156:159], v[152:155]\n"
"ds_write2st64_b64 v180, v[176:177], v[178:179] offset0:0 offset1:1\n"
"v_cvt_pk_bf16_f32 v217, v218, v219\n"
"v_cvt_pk_bf16_f32 v171, v174, v175\n"
"v_cvt_pk_bf16_f32 v121, v122, v123\n"
"v_mfma_f32_16x16x32_bf16 v[156:159], a[216:219], a[152:155], v[156:159]\n"
"v_xor_b32 v250, s74, v250\n"
"v_xor_b32 v252, s74, v252\n"
"v_mfma_f32_16x16x32_bf16 v[156:159], a[220:223], a[156:159], v[156:159]\n"
"s_waitcnt lgkmcnt(1)\n"
"ds_write2st64_b64 v180, v[200:201], v[202:203] offset0:96 offset1:97\n"
"v_bfe_u32 v182, v242, 8, 1\n"
"v_mfma_f32_16x16x32_bf16 v[96:99], a[224:227], a[184:187], v[96:99]\n"
"v_cvt_pk_bf16_f32 v170, v172, v173\n"
"v_cvt_pk_bf16_f32 v136, v136, v137\n"
"v_mfma_f32_16x16x32_bf16 v[96:99], a[228:231], a[188:191], v[96:99]\n"
"v_cvt_pk_bf16_f32 v128, v128, v129\n"
"v_cvt_pk_bf16_f32 v129, v130, v131\n"
"v_mfma_f32_16x16x32_bf16 v[100:103], a[232:235], a[184:187], v[100:103]\n"
"s_waitcnt vmcnt(10)\n"
"v_cvt_pk_bf16_f32 v112, v112, v113\n"
"v_cvt_pk_bf16_f32 v113, v114, v115\n"
"v_mfma_f32_16x16x32_bf16 v[100:103], a[236:239], a[188:191], v[100:103]\n"
"s_barrier\n"
"ds_write2st64_b64 v180, v[208:209], v[210:211] offset0:120 offset1:121\n"
"v_cvt_pk_bf16_f32 v219, v222, v223\n"
"v_cvt_pk_bf16_f32 v114, v116, v117\n"
"v_mfma_f32_16x16x32_bf16 v[104:107], a[240:243], a[184:187], v[104:107]\n"
"s_mov_b32 m0, s51\n"
"ds_read_b64_tr_b16 a[204:205], v245 offset:8192\n"
"v_cvt_pk_bf16_f32 v218, v220, v221\n"
"v_mfma_f32_16x16x32_bf16 v[104:107], a[244:247], a[188:191], v[104:107]\n"
"buffer_load_dword v241, s[80:83], s63 offen lds\n"
"v_mfma_f32_16x16x32_bf16 v[108:111], a[248:251], a[184:187], v[108:111]\n"
"v_permlane16_swap_b32 v168, v170\n"
"v_mfma_f32_16x16x32_bf16 v[108:111], a[252:255], a[188:191], v[108:111]\n"
"v_pk_fma_f32 v[144:145], v[144:145], s[70:71], s[70:71] op_sel:[0,0,1] op_sel_hi:[1,0,1]\nv_pk_fma_f32 v[146:147], v[146:147], s[70:71], s[70:71] op_sel:[0,0,1] op_sel_hi:[1,0,1]\nv_pk_fma_f32 v[148:149], v[148:149], s[70:71], s[70:71] op_sel:[0,0,1] op_sel_hi:[1,0,1]\nv_pk_fma_f32 v[150:151], v[150:151], s[70:71], s[70:71] op_sel:[0,0,1] op_sel_hi:[1,0,1]\nv_pk_fma_f32 v[152:153], v[152:153], s[70:71], s[70:71] op_sel:[0,0,1] op_sel_hi:[1,0,1]\nv_pk_fma_f32 v[154:155], v[154:155], s[70:71], s[70:71] op_sel:[0,0,1] op_sel_hi:[1,0,1]\nv_pk_fma_f32 v[156:157], v[156:157], s[70:71], s[70:71] op_sel:[0,0,1] op_sel_hi:[1,0,1]\nv_pk_fma_f32 v[158:159], v[158:159], s[70:71], s[70:71] op_sel:[0,0,1] op_sel_hi:[1,0,1]\n"
"v_mfma_f32_16x16x32_bf16 v[92:95], a[248:251], a[176:179], v[92:95]\n"
"ds_write2st64_b64 v180, v[184:185], v[186:187] offset0:64 offset1:65\n"
"v_permlane16_swap_b32 v112, v114\n"
"v_cvt_pk_bf16_f32 v122, v124, v125\n"
"v_lshl_add_u32 v181, v182, 9, v181\n"
"v_mfma_f32_16x16x32_bf16 v[92:95], a[252:255], a[180:183], v[92:95]\n"
"ds_write2st64_b64 v180, v[216:217], v[218:219] offset0:128 offset1:129\n"
"v_cvt_pk_bf16_f32 v130, v132, v133\n"
"v_cvt_pk_bf16_f32 v137, v138, v139\n"
"v_cvt_pk_bf16_f32 v152, v152, v153\n"
"v_mfma_f32_16x16x32_bf16 v[88:91], a[240:243], a[176:179], v[88:91]\n"
"v_cvt_pk_bf16_f32 v144, v144, v145\n"
"v_cvt_pk_bf16_f32 v145, v146, v147\n"
"v_mfma_f32_16x16x32_bf16 v[88:91], a[244:247], a[180:183], v[88:91]\n"
"buffer_load_dword v241, s[80:83], s63 offen offset:80 lds\n"
"v_mfma_f32_16x16x32_bf16 v[84:87], a[232:235], a[176:179], v[84:87]\n"
"v_cvt_pk_bf16_f32 v161, v162, v163\n"
"v_cvt_pk_bf16_f32 v115, v118, v119\n"
"v_mfma_f32_16x16x32_bf16 v[84:87], a[236:239], a[180:183], v[84:87]\n"
"v_permlane16_swap_b32 v120, v122\n"
"v_mfma_f32_16x16x32_bf16 v[80:83], a[224:227], a[176:179], v[80:83]\n"
"s_mov_b32 m0, s51\n"
"ds_read_b64_tr_b16 a[198:199], v244 offset:8704\n"
"v_xor_b32_e64 v182, s42, 0\n"
"v_mfma_f32_16x16x32_bf16 v[80:83], a[228:231], a[180:183], v[80:83]\n"
"buffer_load_dwordx4 v248, s[24:27], s49 offen lds\n"
"v_mfma_f32_16x16x32_bf16 v[64:67], a[224:227], a[168:171], v[64:67]\n"
"v_cvt_pk_bf16_f32 v123, v126, v127\n"
"v_cvt_pk_bf16_f32 v153, v154, v155\n"
"v_mfma_f32_16x16x32_bf16 v[64:67], a[228:231], a[172:175], v[64:67]\n"
"ds_read_b64_tr_b16 a[210:211], v244 offset:1536\n"
"ds_read_b64_tr_b16 a[212:213], v244 offset:9216\n"
"v_accvgpr_write_b32 a0, v112\n"
"v_permlane16_swap_b32 v121, v123\n"
"v_cvt_pk_bf16_f32 v131, v134, v135\n"
"v_cvt_pk_bf16_f32 v138, v140, v141\n"
"v_cvt_pk_bf16_f32 v146, v148, v149\n"
"v_cvt_pk_bf16_f32 v147, v150, v151\n"
"v_mfma_f32_16x16x32_bf16 v[68:71], a[232:235], a[168:171], v[68:71]\n"
"v_permlane16_swap_b32 v113, v115\n"
"v_mfma_f32_16x16x32_bf16 v[68:71], a[236:239], a[172:175], v[68:71]\n"
"ds_read_b64_tr_b16 a[194:195], v244 offset:512\n"
"ds_read_b64_tr_b16 a[220:221], v245 offset:9216\n"
"v_mfma_f32_16x16x32_bf16 v[72:75], a[240:243], a[168:171], v[72:75]\n"
"ds_read_b64_tr_b16 a[192:193], v244 offset:0\n"
"v_cvt_pk_bf16_f32 v155, v158, v159\n"
"v_mfma_f32_16x16x32_bf16 v[72:75], a[244:247], a[172:175], v[72:75]\n"
"buffer_load_dwordx4 v249, s[24:27], s49 offen offset:1024 lds\n"
"v_mfma_f32_16x16x32_bf16 v[76:79], a[248:251], a[168:171], v[76:79]\n"
"v_lshl_add_u32 v181, v182, 10, v181\n"
"v_add_u32 v181, 94208, v181\n"
"v_mfma_f32_16x16x32_bf16 v[76:79], a[252:255], a[172:175], v[76:79]\n"
"v_permlane16_swap_b32 v169, v171\n"
"v_mfma_f32_16x16x32_bf16 v[60:63], a[248:251], a[160:163], v[60:63]\n"
"ds_read_b64_tr_b16 a[200:201], v245 offset:0\n"
"ds_read_b64_tr_b16 a[208:209], v244 offset:1024\n"
"v_mfma_f32_16x16x32_bf16 v[60:63], a[252:255], a[164:167], v[60:63]\n"
"buffer_load_dwordx4 v254, s[24:27], s49 offen offset:2048 lds\n"
"v_mfma_f32_16x16x32_bf16 v[56:59], a[240:243], a[160:163], v[56:59]\n"
"v_permlane16_swap_b32 v145, v147\n"
"v_mfma_f32_16x16x32_bf16 v[56:59], a[244:247], a[164:167], v[56:59]\n"
"v_pk_fma_f32 v[96:97], v[96:97], s[70:71], s[70:71] op_sel:[0,0,1] op_sel_hi:[1,0,1]\nv_pk_fma_f32 v[98:99], v[98:99], s[70:71], s[70:71] op_sel:[0,0,1] op_sel_hi:[1,0,1]\nv_pk_fma_f32 v[100:101], v[100:101], s[70:71], s[70:71] op_sel:[0,0,1] op_sel_hi:[1,0,1]\nv_pk_fma_f32 v[102:103], v[102:103], s[70:71], s[70:71] op_sel:[0,0,1] op_sel_hi:[1,0,1]\nv_pk_fma_f32 v[104:105], v[104:105], s[70:71], s[70:71] op_sel:[0,0,1] op_sel_hi:[1,0,1]\nv_pk_fma_f32 v[106:107], v[106:107], s[70:71], s[70:71] op_sel:[0,0,1] op_sel_hi:[1,0,1]\nv_pk_fma_f32 v[108:109], v[108:109], s[70:71], s[70:71] op_sel:[0,0,1] op_sel_hi:[1,0,1]\nv_pk_fma_f32 v[110:111], v[110:111], s[70:71], s[70:71] op_sel:[0,0,1] op_sel_hi:[1,0,1]\nv_pk_fma_f32 v[80:81], v[80:81], s[70:71], s[70:71] op_sel:[0,0,1] op_sel_hi:[1,0,1]\nv_pk_fma_f32 v[82:83], v[82:83], s[70:71], s[70:71] op_sel:[0,0,1] op_sel_hi:[1,0,1]\nv_pk_fma_f32 v[84:85], v[84:85], s[70:71], s[70:71] op_sel:[0,0,1] op_sel_hi:[1,0,1]\nv_pk_fma_f32 v[86:87], v[86:87], s[70:71], s[70:71] op_sel:[0,0,1] op_sel_hi:[1,0,1]\nv_pk_fma_f32 v[88:89], v[88:89], s[70:71], s[70:71] op_sel:[0,0,1] op_sel_hi:[1,0,1]\nv_pk_fma_f32 v[90:91], v[90:91], s[70:71], s[70:71] op_sel:[0,0,1] op_sel_hi:[1,0,1]\nv_pk_fma_f32 v[92:93], v[92:93], s[70:71], s[70:71] op_sel:[0,0,1] op_sel_hi:[1,0,1]\nv_pk_fma_f32 v[94:95], v[94:95], s[70:71], s[70:71] op_sel:[0,0,1] op_sel_hi:[1,0,1]\n"
"v_mfma_f32_16x16x32_bf16 v[52:55], a[232:235], a[160:163], v[52:55]\n"
"ds_read_b64_tr_b16 a[206:207], v245 offset:8704\n"
"ds_read_b64_tr_b16 a[214:215], v244 offset:9728\n"
"v_mfma_f32_16x16x32_bf16 v[52:55], a[236:239], a[164:167], v[52:55]\n"
"ds_read_b64_tr_b16 a[218:219], v245 offset:1536\n"
"v_accvgpr_write_b32 a10, v122\n"
"v_mfma_f32_16x16x32_bf16 v[48:51], a[224:227], a[160:163], v[48:51]\n"
"s_waitcnt vmcnt(63) lgkmcnt(5)\n"
"ds_read_b64_tr_b16 a[222:223], v245 offset:9728\n"
"v_cvt_pk_bf16_f32 v162, v164, v165\n"
"v_mfma_f32_16x16x32_bf16 v[48:51], a[228:231], a[164:167], v[48:51]\n"
"s_barrier\n"
"ds_read_b128 a[20:23], v181 offset:45056\n"
"v_accvgpr_write_b32 a9, v121\n"
"v_accvgpr_write_b32 a11, v123\n"
"v_permlane16_swap_b32 v136, v138\n"
"v_permlane16_swap_b32 v144, v146\n"
"v_cvt_pk_bf16_f32 v154, v156, v157\n"
"v_mfma_f32_16x16x32_bf16 v[0:3], a[224:227], a[136:139], v[0:3]\n"
"ds_read_b64_tr_b16 a[202:203], v245 offset:512\n"
"v_add_u32 v182, s75, v181\n"
"v_mfma_f32_16x16x32_bf16 v[0:3], a[228:231], a[140:143], v[0:3]\n"
"buffer_load_dwordx4 v255, s[24:27], s49 offen offset:3072 lds\n"
"v_permlane16_swap_b32 v128, v130\n"
"v_accvgpr_write_b32 a18, v130\n"
"v_cvt_pk_bf16_f32 v139, v142, v143\n"
"v_accvgpr_write_b32 a32, v144\n"
"v_xor_b32 v251, s74, v251\n"
"v_cvt_pk_bf16_f32 v96, v96, v97\n"
"v_mfma_f32_16x16x32_bf16 v[4:7], a[232:235], a[136:139], v[4:7]\n"
"s_add_u32 s40, s49, 40960\n"
"s_add_u32 m0, s51, 4096\n"
"v_accvgpr_write_b32 a2, v114\n"
"v_mfma_f32_16x16x32_bf16 v[4:7], a[236:239], a[140:143], v[4:7]\n"
"buffer_load_dwordx4 v248, s[24:27], s40 offen lds\n"
"v_mfma_f32_16x16x32_bf16 v[8:11], a[240:243], a[136:139], v[8:11]\n"
"v_permlane16_swap_b32 v160, v162\n"
"v_mfma_f32_16x16x32_bf16 v[8:11], a[244:247], a[140:143], v[8:11]\n"
"v_pk_fma_f32 v[64:65], v[64:65], s[70:71], s[70:71] op_sel:[0,0,1] op_sel_hi:[1,0,1]\nv_pk_fma_f32 v[66:67], v[66:67], s[70:71], s[70:71] op_sel:[0,0,1] op_sel_hi:[1,0,1]\nv_pk_fma_f32 v[68:69], v[68:69], s[70:71], s[70:71] op_sel:[0,0,1] op_sel_hi:[1,0,1]\nv_pk_fma_f32 v[70:71], v[70:71], s[70:71], s[70:71] op_sel:[0,0,1] op_sel_hi:[1,0,1]\nv_pk_fma_f32 v[72:73], v[72:73], s[70:71], s[70:71] op_sel:[0,0,1] op_sel_hi:[1,0,1]\nv_pk_fma_f32 v[74:75], v[74:75], s[70:71], s[70:71] op_sel:[0,0,1] op_sel_hi:[1,0,1]\nv_pk_fma_f32 v[76:77], v[76:77], s[70:71], s[70:71] op_sel:[0,0,1] op_sel_hi:[1,0,1]\nv_pk_fma_f32 v[78:79], v[78:79], s[70:71], s[70:71] op_sel:[0,0,1] op_sel_hi:[1,0,1]\nv_pk_fma_f32 v[48:49], v[48:49], s[70:71], s[70:71] op_sel:[0,0,1] op_sel_hi:[1,0,1]\nv_pk_fma_f32 v[50:51], v[50:51], s[70:71], s[70:71] op_sel:[0,0,1] op_sel_hi:[1,0,1]\nv_pk_fma_f32 v[52:53], v[52:53], s[70:71], s[70:71] op_sel:[0,0,1] op_sel_hi:[1,0,1]\nv_pk_fma_f32 v[54:55], v[54:55], s[70:71], s[70:71] op_sel:[0,0,1] op_sel_hi:[1,0,1]\nv_pk_fma_f32 v[56:57], v[56:57], s[70:71], s[70:71] op_sel:[0,0,1] op_sel_hi:[1,0,1]\nv_pk_fma_f32 v[58:59], v[58:59], s[70:71], s[70:71] op_sel:[0,0,1] op_sel_hi:[1,0,1]\nv_pk_fma_f32 v[60:61], v[60:61], s[70:71], s[70:71] op_sel:[0,0,1] op_sel_hi:[1,0,1]\nv_pk_fma_f32 v[62:63], v[62:63], s[70:71], s[70:71] op_sel:[0,0,1] op_sel_hi:[1,0,1]\n"
"v_mfma_f32_16x16x32_bf16 v[12:15], a[248:251], a[136:139], v[12:15]\n"
"ds_read_b128 a[4:7], v181 offset:0\n"
"v_accvgpr_write_b32 a8, v120\n"
"v_mfma_f32_16x16x32_bf16 v[12:15], a[252:255], a[140:143], v[12:15]\n"
"ds_read_b64_tr_b16 a[196:197], v244 offset:8192\n"
"v_accvgpr_write_b32 a1, v113\n"
"v_mfma_f32_16x16x32_bf16 v[28:31], a[248:251], a[144:147], v[28:31]\n"
"ds_read_b128 a[44:47], v182 offset:0\n"
"v_cvt_pk_bf16_f32 v80, v80, v81\n"
"v_mfma_f32_16x16x32_bf16 v[28:31], a[252:255], a[148:151], v[28:31]\n"
"buffer_load_dwordx4 v249, s[24:27], s40 offen offset:1024 lds\n"
"v_mfma_f32_16x16x32_bf16 v[24:27], a[240:243], a[144:147], v[24:27]\n"
"ds_read_b128 a[12:15], v181 offset:32768\n"
"v_cvt_pk_bf16_f32 v88, v88, v89\n"
"v_mfma_f32_16x16x32_bf16 v[24:27], a[244:247], a[148:151], v[24:27]\n"
"v_cvt_pk_bf16_f32 v163, v166, v167\n"
"v_cvt_pk_bf16_f32 v97, v98, v99\n"
"v_mfma_f32_16x16x32_bf16 v[20:23], a[232:235], a[144:147], v[20:23]\n"
"ds_read_b128 a[36:39], v181 offset:61440\n"
"v_accvgpr_write_b32 a26, v138\n"
"v_mfma_f32_16x16x32_bf16 v[20:23], a[236:239], a[148:151], v[20:23]\n"
"buffer_load_dwordx4 v254, s[24:27], s40 offen offset:2048 lds\n"
"v_permlane16_swap_b32 v129, v131\n"
"v_accvgpr_write_b32 a16, v128\n"
"v_permlane16_swap_b32 v137, v139\n"
"v_cvt_pk_bf16_f32 v81, v82, v83\n"
"v_cvt_pk_bf16_f32 v48, v48, v49\n"
"v_mfma_f32_16x16x32_bf16 v[16:19], a[224:227], a[144:147], v[16:19]\n"
"s_add_u32 s49, s49, 128\n"
"ds_read_b64_tr_b16 a[216:217], v245 offset:1024\n"
"ds_read_b128 a[136:139], v246 offset:0\n"
"v_mfma_f32_16x16x32_bf16 v[16:19], a[228:231], a[148:151], v[16:19]\n"
"s_sub_u32 s41, s49, 1280\n"
"v_permlane16_swap_b32 v153, v155\n"
"v_mfma_f32_16x16x32_bf16 v[32:35], a[224:227], a[152:155], v[32:35]\n"
"s_min_u32 s49, s49, s41\n"
"ds_read_b128 a[160:163], v252 offset:0\n"
"v_accvgpr_write_b32 a34, v146\n"
"v_mfma_f32_16x16x32_bf16 v[32:35], a[228:231], a[156:159], v[32:35]\n"
"s_xor_b32 s51, s51, s74\n"
"ds_read_b128 a[140:143], v247 offset:0\n"
"v_accvgpr_write_b32 a25, v137\n"
"v_mfma_f32_16x16x32_bf16 v[36:39], a[232:235], a[152:155], v[36:39]\n"
"ds_read_b128 a[28:31], v181 offset:49152\n"
"v_accvgpr_write_b32 a33, v145\n"
"v_mfma_f32_16x16x32_bf16 v[36:39], a[236:239], a[156:159], v[36:39]\n"
"ds_read_b128 a[180:183], v251 offset:2048\n"
"v_pk_fma_f32 v[0:1], v[0:1], s[70:71], s[70:71] op_sel:[0,0,1] op_sel_hi:[1,0,1]\nv_pk_fma_f32 v[2:3], v[2:3], s[70:71], s[70:71] op_sel:[0,0,1] op_sel_hi:[1,0,1]\nv_pk_fma_f32 v[4:5], v[4:5], s[70:71], s[70:71] op_sel:[0,0,1] op_sel_hi:[1,0,1]\nv_pk_fma_f32 v[6:7], v[6:7], s[70:71], s[70:71] op_sel:[0,0,1] op_sel_hi:[1,0,1]\nv_pk_fma_f32 v[8:9], v[8:9], s[70:71], s[70:71] op_sel:[0,0,1] op_sel_hi:[1,0,1]\nv_pk_fma_f32 v[10:11], v[10:11], s[70:71], s[70:71] op_sel:[0,0,1] op_sel_hi:[1,0,1]\nv_pk_fma_f32 v[12:13], v[12:13], s[70:71], s[70:71] op_sel:[0,0,1] op_sel_hi:[1,0,1]\nv_pk_fma_f32 v[14:15], v[14:15], s[70:71], s[70:71] op_sel:[0,0,1] op_sel_hi:[1,0,1]\nv_pk_fma_f32 v[16:17], v[16:17], s[70:71], s[70:71] op_sel:[0,0,1] op_sel_hi:[1,0,1]\nv_pk_fma_f32 v[18:19], v[18:19], s[70:71], s[70:71] op_sel:[0,0,1] op_sel_hi:[1,0,1]\nv_pk_fma_f32 v[20:21], v[20:21], s[70:71], s[70:71] op_sel:[0,0,1] op_sel_hi:[1,0,1]\nv_pk_fma_f32 v[22:23], v[22:23], s[70:71], s[70:71] op_sel:[0,0,1] op_sel_hi:[1,0,1]\nv_pk_fma_f32 v[24:25], v[24:25], s[70:71], s[70:71] op_sel:[0,0,1] op_sel_hi:[1,0,1]\nv_pk_fma_f32 v[26:27], v[26:27], s[70:71], s[70:71] op_sel:[0,0,1] op_sel_hi:[1,0,1]\nv_pk_fma_f32 v[28:29], v[28:29], s[70:71], s[70:71] op_sel:[0,0,1] op_sel_hi:[1,0,1]\nv_pk_fma_f32 v[30:31], v[30:31], s[70:71], s[70:71] op_sel:[0,0,1] op_sel_hi:[1,0,1]\n"
"v_mfma_f32_16x16x32_bf16 v[40:43], a[240:243], a[152:155], v[40:43]\n"
"v_accvgpr_write_b32 a19, v131\n"
"v_accvgpr_write_b32 a41, v153\n"
"v_mfma_f32_16x16x32_bf16 v[40:43], a[244:247], a[156:159], v[40:43]\n"
"ds_read_b128 a[144:147], v246 offset:2048\n"
"v_permlane16_swap_b32 v161, v163\n"
"v_accvgpr_write_b32 a17, v129\n"
"v_accvgpr_write_b32 a27, v139\n"
"v_accvgpr_write_b32 a35, v147\n"
"v_permlane16_swap_b32 v152, v154\n"
"v_cvt_pk_bf16_f32 v104, v104, v105\n"
"v_mfma_f32_16x16x32_bf16 v[44:47], a[248:251], a[152:155], v[44:47]\n"
"s_add_u32 s47, s47, 1\n"
"ds_read_b128 a[148:151], v247 offset:2048\n"
"v_cvt_pk_bf16_f32 v64, v64, v65\n"
"v_mfma_f32_16x16x32_bf16 v[44:47], a[252:255], a[156:159], v[44:47]\n"
"s_waitcnt lgkmcnt(6)\n"
"v_accvgpr_write_b32 a3, v115\n"
"v_cvt_pk_bf16_f32 v56, v56, v57\n"
"v_mfma_f32_16x16x32_bf16 v[112:115], a[192:195], a[136:139], v[228:231]\n"
"s_waitcnt lgkmcnt(4)\n"
"ds_read_b128 a[156:159], v247 offset:4096\n"
"v_cvt_pk_bf16_f32 v83, v86, v87\n"
"v_mfma_f32_16x16x32_bf16 v[112:115], a[196:199], a[140:143], v[112:115]\n"
"ds_read_b128 a[152:155], v246 offset:4096\n"
"v_cvt_pk_bf16_f32 v98, v100, v101\n"
"v_mfma_f32_16x16x32_bf16 v[116:119], a[200:203], a[136:139], v[228:231]\n"
"s_waitcnt vmcnt(13) lgkmcnt(5)\n"
"v_cvt_pk_bf16_f32 v65, v66, v67\n"
"v_cvt_pk_bf16_f32 v16, v16, v17\n"
"v_mfma_f32_16x16x32_bf16 v[116:119], a[204:207], a[140:143], v[116:119]\n"
"s_barrier\n"
"ds_write_b128 v243, v[168:171] offset:2048\n"
"v_accvgpr_write_b32 a43, v155\n"
"v_cvt_pk_bf16_f32 v99, v102, v103\n"
"v_cvt_pk_bf16_f32 v8, v8, v9\n"
"v_mfma_f32_16x16x32_bf16 v[120:123], a[208:211], a[136:139], v[228:231]\n"
"s_add_u32 s40, s48, 256\n"
"s_xor_b32 m0, s50, 0xc000\n"
"v_cvt_pk_bf16_f32 v0, v0, v1\n"
"v_mfma_f32_16x16x32_bf16 v[120:123], a[212:215], a[140:143], v[120:123]\n"
"buffer_load_dwordx4 v224, s[28:31], s40 offen lds\n"
"v_mfma_f32_16x16x32_bf16 v[124:127], a[216:219], a[136:139], v[228:231]\n"
"v_accvgpr_write_b32 a24, v136\n"
"v_cvt_pk_bf16_f32 v66, v68, v69\n"
"v_mfma_f32_16x16x32_bf16 v[124:127], a[220:223], a[140:143], v[124:127]\n"
"s_waitcnt lgkmcnt(3)\n"
"v_pk_fma_f32 v[32:33], v[32:33], s[70:71], s[70:71] op_sel:[0,0,1] op_sel_hi:[1,0,1]\nv_pk_fma_f32 v[34:35], v[34:35], s[70:71], s[70:71] op_sel:[0,0,1] op_sel_hi:[1,0,1]\nv_pk_fma_f32 v[36:37], v[36:37], s[70:71], s[70:71] op_sel:[0,0,1] op_sel_hi:[1,0,1]\nv_pk_fma_f32 v[38:39], v[38:39], s[70:71], s[70:71] op_sel:[0,0,1] op_sel_hi:[1,0,1]\nv_pk_fma_f32 v[40:41], v[40:41], s[70:71], s[70:71] op_sel:[0,0,1] op_sel_hi:[1,0,1]\nv_pk_fma_f32 v[42:43], v[42:43], s[70:71], s[70:71] op_sel:[0,0,1] op_sel_hi:[1,0,1]\nv_pk_fma_f32 v[44:45], v[44:45], s[70:71], s[70:71] op_sel:[0,0,1] op_sel_hi:[1,0,1]\nv_pk_fma_f32 v[46:47], v[46:47], s[70:71], s[70:71] op_sel:[0,0,1] op_sel_hi:[1,0,1]\n"
"v_mfma_f32_16x16x32_bf16 v[140:143], a[216:219], a[144:147], v[228:231]\n"
"ds_read_b64_tr_b16 a[246:247], v244 offset:26112\n"
"v_and_b32 v102, 0xf0, v242\n"
"v_mfma_f32_16x16x32_bf16 v[140:143], a[220:223], a[148:151], v[140:143]\n"
"ds_read_b64_tr_b16 a[228:229], v244 offset:24576\n"
"v_cvt_pk_bf16_f32 v24, v24, v25\n"
"v_mfma_f32_16x16x32_bf16 v[136:139], a[208:211], a[144:147], v[228:231]\n"
"ds_read_b128 a[176:179], v250 offset:2048\n"
"ds_read_b64_tr_b16 a[236:237], v245 offset:24576\n"
"v_mfma_f32_16x16x32_bf16 v[136:139], a[212:215], a[148:151], v[136:139]\n"
"buffer_load_dwordx4 v225, s[28:31], s40 offen offset:1024 lds\n"
"v_accvgpr_write_b32 a40, v152\n"
"v_bfe_u32 v100, v242, 9, 1\n"
"v_lshl_add_u32 v100, v100, 8, v102\n"
"v_bfe_u32 v102, v242, 8, 1\n"
"v_cvt_pk_bf16_f32 v1, v2, v3\n"
"v_cvt_pk_bf16_f32 v2, v4, v5\n"
"v_cvt_pk_bf16_f32 v3, v6, v7\n"
"v_cvt_pk_bf16_f32 v40, v40, v41\n"
"v_mfma_f32_16x16x32_bf16 v[132:135], a[200:203], a[144:147], v[228:231]\n"
"v_cvt_pk_bf16_f32 v9, v10, v11\n"
"v_cvt_pk_bf16_f32 v10, v12, v13\n"
"v_mfma_f32_16x16x32_bf16 v[132:135], a[204:207], a[148:151], v[132:135]\n"
"ds_write_b128 v243, v[160:163] offset:0\n"
"v_permlane16_swap_b32 v1, v3\n"
"v_permlane16_swap_b32 v8, v10\n"
"v_bfe_u32 v101, v242, 9, 1\n"
"v_mfma_f32_16x16x32_bf16 v[128:131], a[192:195], a[144:147], v[228:231]\n"
"ds_read_b128 a[164:167], v253 offset:0\n"
"v_cvt_pk_bf16_f32 v49, v50, v51\n"
"v_mfma_f32_16x16x32_bf16 v[128:131], a[196:199], a[148:151], v[128:131]\n"
"s_waitcnt lgkmcnt(6)\n"
"ds_read_b64_tr_b16 a[244:245], v244 offset:25600\n"
"v_accvgpr_write_b32 a42, v154\n"
"v_mfma_f32_16x16x32_bf16 v[144:147], a[192:195], a[152:155], v[228:231]\n"
"ds_read_b128 a[172:175], v251 offset:0\n"
"v_cvt_pk_bf16_f32 v41, v42, v43\n"
"v_mfma_f32_16x16x32_bf16 v[144:147], a[196:199], a[156:159], v[144:147]\n"
"buffer_load_dwordx4 v226, s[28:31], s40 offen offset:2048 lds\n"
"v_mfma_f32_16x16x32_bf16 v[148:151], a[200:203], a[152:155], v[228:231]\n"
"v_cvt_pk_bf16_f32 v25, v26, v27\n"
"v_cvt_pk_bf16_f32 v43, v46, v47\n"
"v_mfma_f32_16x16x32_bf16 v[148:151], a[204:207], a[156:159], v[148:151]\n"
"v_cvt_pk_bf16_f32 v72, v72, v73\n"
"v_cvt_pk_bf16_f32 v27, v30, v31\n"
"v_mfma_f32_16x16x32_bf16 v[152:155], a[208:211], a[152:155], v[228:231]\n"
"s_nop 0\n"
"v_permlane16_swap_b32 v25, v27\n"
"v_mfma_f32_16x16x32_bf16 v[152:155], a[212:215], a[156:159], v[152:155]\n"
"ds_read_b128 a[168:171], v250 offset:0\n"
"v_cvt_pk_bf16_f32 v17, v18, v19\n"
"v_mfma_f32_16x16x32_bf16 v[156:159], a[216:219], a[152:155], v[228:231]\n"
"v_cvt_pk_bf16_f32 v89, v90, v91\n"
"v_cvt_pk_bf16_f32 v32, v32, v33\n"
"v_mfma_f32_16x16x32_bf16 v[156:159], a[220:223], a[156:159], v[156:159]\n"
"buffer_load_dwordx4 v227, s[28:31], s40 offen offset:3072 lds\n"
"v_mfma_f32_16x16x32_bf16 v[172:175], a[216:219], a[160:163], v[228:231]\n"
"s_waitcnt lgkmcnt(1)\n"
"s_cmp_eq_u32 s47, 8\n"
"v_lshl_add_u32 v100, v102, 3, v100\n"
"v_accvgpr_write_b32 a49, v1\n"
"v_mfma_f32_16x16x32_bf16 v[172:175], a[220:223], a[164:167], v[172:175]\n"
"s_cselect_b32 s41, s52, s58\n"
"v_cvt_pk_bf16_f32 v26, v28, v29\n"
"v_cvt_pk_bf16_f32 v33, v34, v35\n"
"v_mfma_f32_16x16x32_bf16 v[168:171], a[208:211], a[160:163], v[228:231]\n"
"s_add_u32 s48, s48, s41\n"
"ds_read_b64_tr_b16 a[238:239], v245 offset:25088\n"
"ds_read_b64_tr_b16 a[250:251], v245 offset:17920\n"
"v_mfma_f32_16x16x32_bf16 v[168:171], a[212:215], a[164:167], v[168:171]\n"
"s_sub_u32 s41, s48, 5242880\n"
"v_permlane16_swap_b32 v0, v2\n"
"v_mfma_f32_16x16x32_bf16 v[164:167], a[200:203], a[160:163], v[228:231]\n"
"s_min_u32 s48, s48, s41\n"
"ds_read_b128 a[184:187], v250 offset:4096\n"
"v_xor_b32_e64 v102, s42, 1\n"
"v_mfma_f32_16x16x32_bf16 v[164:167], a[204:207], a[164:167], v[164:167]\n"
"v_accvgpr_write_b32 a58, v10\n"
"v_cvt_pk_bf16_f32 v42, v44, v45\n"
"v_mfma_f32_16x16x32_bf16 v[160:163], a[192:195], a[160:163], v[228:231]\n"
"s_mov_b32 m0, s50\n"
"ds_read_b128 a[188:191], v251 offset:4096\n"
"v_lshl_add_u32 v100, v102, 10, v100\n"
"v_mfma_f32_16x16x32_bf16 v[160:163], a[196:199], a[164:167], v[160:163]\n"
"s_waitcnt lgkmcnt(4)\n"
"buffer_load_dwordx4 v224, s[28:31], s48 offen lds\n"
"v_mfma_f32_16x16x32_bf16 v[176:179], a[192:195], a[168:171], v[228:231]\n"
"v_permlane16_swap_b32 v40, v42\n"
"v_mfma_f32_16x16x32_bf16 v[176:179], a[196:199], a[172:175], v[176:179]\n"
"v_cvt_pk_bf16_f32 v11, v14, v15\n"
"v_cvt_pk_bf16_f32 v19, v22, v23\n"
"v_mfma_f32_16x16x32_bf16 v[180:183], a[200:203], a[168:171], v[228:231]\n"
"ds_read_b64_tr_b16 a[226:227], v244 offset:16896\n"
"v_cvt_pk_bf16_f32 v57, v58, v59\n"
"v_mfma_f32_16x16x32_bf16 v[180:183], a[204:207], a[172:175], v[180:183]\n"
"buffer_load_dwordx4 v225, s[28:31], s48 offen offset:1024 lds\n"
"v_mfma_f32_16x16x32_bf16 v[184:187], a[208:211], a[168:171], v[228:231]\n"
"v_cvt_pk_bf16_f32 v67, v70, v71\n"
"v_accvgpr_write_b32 a51, v3\n"
"v_mfma_f32_16x16x32_bf16 v[184:187], a[212:215], a[172:175], v[184:187]\n"
"v_permlane16_swap_b32 v9, v11\n"
"v_mfma_f32_16x16x32_bf16 v[188:191], a[216:219], a[168:171], v[228:231]\n"
"ds_read_b64_tr_b16 a[224:225], v244 offset:16384\n"
"v_accvgpr_write_b32 a56, v8\n"
"v_mfma_f32_16x16x32_bf16 v[188:191], a[220:223], a[172:175], v[188:191]\n"
"ds_read_b64_tr_b16 a[232:233], v245 offset:16384\n"
"v_cvt_pk_bf16_f32 v59, v62, v63\n"
"v_mfma_f32_16x16x32_bf16 v[204:207], a[216:219], a[176:179], v[228:231]\n"
"ds_read_b64_tr_b16 a[230:231], v244 offset:25088\n"
"v_and_b32 v102, 0xf0, v242\n"
"v_mfma_f32_16x16x32_bf16 v[204:207], a[220:223], a[180:183], v[204:207]\n"
"buffer_load_dwordx4 v226, s[28:31], s48 offen offset:2048 lds\n"
"v_mfma_f32_16x16x32_bf16 v[200:203], a[208:211], a[176:179], v[228:231]\n"
"v_cvt_pk_bf16_f32 v35, v38, v39\n"
"v_lshl_add_u32 v101, v101, 8, v102\n"
"v_mfma_f32_16x16x32_bf16 v[200:203], a[212:215], a[180:183], v[200:203]\n"
"v_cvt_pk_bf16_f32 v82, v84, v85\n"
"v_cvt_pk_bf16_f32 v73, v74, v75\n"
"v_mfma_f32_16x16x32_bf16 v[196:199], a[200:203], a[176:179], v[228:231]\n"
"ds_read_b64_tr_b16 a[248:249], v245 offset:17408\n"
"v_accvgpr_write_b32 a50, v2\n"
"v_mfma_f32_16x16x32_bf16 v[196:199], a[204:207], a[180:183], v[196:199]\n"
"ds_read_b64_tr_b16 a[252:253], v245 offset:25600\n"
"ds_read_b64_tr_b16 a[254:255], v245 offset:26112\n"
"v_mfma_f32_16x16x32_bf16 v[192:195], a[192:195], a[176:179], v[228:231]\n"
"v_accvgpr_write_b32 a48, v0\n"
"v_accvgpr_write_b32 a73, v25\n"
"v_mfma_f32_16x16x32_bf16 v[192:195], a[196:199], a[180:183], v[192:195]\n"
"s_waitcnt lgkmcnt(4)\n"
"buffer_load_dwordx4 v227, s[28:31], s48 offen offset:3072 lds\n"
"v_cvt_pk_bf16_f32 v105, v106, v107\n"
"v_cvt_pk_bf16_f32 v50, v52, v53\n"
"v_cvt_pk_bf16_f32 v51, v54, v55\n"
"v_permlane16_swap_b32 v17, v19\n"
"v_permlane16_swap_b32 v24, v26\n"
"v_mfma_f32_16x16x32_bf16 v[208:211], a[192:195], a[184:187], v[228:231]\n"
"s_xor_b32 s50, s50, s74\n"
"v_accvgpr_write_b32 a59, v11\n"
"v_accvgpr_write_b32 a67, v19\n"
"v_mfma_f32_16x16x32_bf16 v[208:211], a[196:199], a[188:191], v[208:211]\n"
"v_cvt_pk_bf16_f32 v58, v60, v61\n"
"v_cvt_pk_bf16_f32 v34, v36, v37\n"
"v_mfma_f32_16x16x32_bf16 v[212:215], a[200:203], a[184:187], v[228:231]\n"
"ds_read_b64_tr_b16 a[234:235], v245 offset:16896\n"
"ds_read_b64_tr_b16 a[242:243], v244 offset:17920\n"
"v_mfma_f32_16x16x32_bf16 v[212:215], a[204:207], a[188:191], v[212:215]\n"
"ds_read_b64_tr_b16 a[240:241], v244 offset:17408\n"
"v_accvgpr_write_b32 a75, v27\n"
"v_mfma_f32_16x16x32_bf16 v[216:219], a[208:211], a[184:187], v[228:231]\n"
"v_add_u32 v100, 94208, v100\n"
"v_accvgpr_write_b32 a72, v24\n"
"v_mfma_f32_16x16x32_bf16 v[216:219], a[212:215], a[188:191], v[216:219]\n"
"ds_write2st64_b64 v100, v[64:65], v[66:67] offset0:0 offset1:1\n"
"v_cvt_pk_bf16_f32 v90, v92, v93\n"
"v_accvgpr_write_b32 a57, v9\n"
"v_permlane16_swap_b32 v33, v35\n"
"v_mfma_f32_16x16x32_bf16 v[220:223], a[216:219], a[184:187], v[228:231]\n"
"v_accvgpr_write_b32 a83, v35\n"
"v_xor_b32 v245, s74, v245\n"
"v_mfma_f32_16x16x32_bf16 v[220:223], a[220:223], a[188:191], v[220:223]\n"
"ds_write2st64_b64 v100, v[96:97], v[98:99] offset0:120 offset1:121\n"
"v_permlane16_swap_b32 v57, v59\n"
"v_cvt_pk_bf16_f32 v18, v20, v21\n"
"v_accvgpr_write_b32 a88, v40\n"
"v_mfma_f32_16x16x32_bf16 v[0:3], a[224:227], a[136:139], v[228:231]\n"
"s_waitcnt lgkmcnt(5)\n"
"v_permlane16_swap_b32 v48, v50\n"
"v_mfma_f32_16x16x32_bf16 v[0:3], a[228:231], a[140:143], v[0:3]\n"
"s_waitcnt lgkmcnt(4)\n"
"ds_write2st64_b64 v100, v[80:81], v[82:83] offset0:88 offset1:89\n"
"v_cvt_pk_bf16_f32 v106, v108, v109\n"
"v_cvt_pk_bf16_f32 v107, v110, v111\n"
"v_accvgpr_write_b32 a74, v26\n"
"v_mfma_f32_16x16x32_bf16 v[4:7], a[232:235], a[136:139], v[228:231]\n"
"s_waitcnt vmcnt(8) lgkmcnt(3)\n"
"v_permlane16_swap_b32 v56, v58\n"
"v_mfma_f32_16x16x32_bf16 v[4:7], a[236:239], a[140:143], v[4:7]\n"
"s_barrier\n"
"ds_write2st64_b64 v100, v[104:105], v[106:107] offset0:128 offset1:129\n"
"v_cvt_pk_bf16_f32 v74, v76, v77\n"
"v_mfma_f32_16x16x32_bf16 v[8:11], a[240:243], a[136:139], v[228:231]\n"
"s_mov_b32 m0, s51\n"
"v_cvt_pk_bf16_f32 v75, v78, v79\n"
"v_accvgpr_write_b32 a65, v17\n"
"v_mfma_f32_16x16x32_bf16 v[8:11], a[244:247], a[140:143], v[8:11]\n"
"buffer_load_dwordx4 v248, s[24:27], s49 offen lds\n"
"v_mfma_f32_16x16x32_bf16 v[12:15], a[248:251], a[136:139], v[228:231]\n"
"v_permlane16_swap_b32 v32, v34\n"
"v_mfma_f32_16x16x32_bf16 v[12:15], a[252:255], a[140:143], v[12:15]\n"
"ds_write2st64_b64 v100, v[72:73], v[74:75] offset0:64 offset1:65\n"
"v_cvt_pk_bf16_f32 v91, v94, v95\n"
"v_permlane16_swap_b32 v49, v51\n"
"v_permlane16_swap_b32 v16, v18\n"
"v_mfma_f32_16x16x32_bf16 v[28:31], a[248:251], a[144:147], v[228:231]\n"
"v_permlane16_swap_b32 v41, v43\n"
"v_mfma_f32_16x16x32_bf16 v[28:31], a[252:255], a[148:151], v[28:31]\n"
"ds_write2st64_b64 v100, v[88:89], v[90:91] offset0:96 offset1:97\n"
"v_accvgpr_write_b32 a81, v33\n"
"v_mfma_f32_16x16x32_bf16 v[24:27], a[240:243], a[144:147], v[228:231]\n"
"v_accvgpr_write_b32 a64, v16\n"
"v_accvgpr_write_b32 a91, v43\n"
"v_mfma_f32_16x16x32_bf16 v[24:27], a[244:247], a[148:151], v[24:27]\n"
"buffer_load_dwordx4 v249, s[24:27], s49 offen offset:1024 lds\n"
"v_mfma_f32_16x16x32_bf16 v[20:23], a[232:235], a[144:147], v[228:231]\n"
"v_accvgpr_write_b32 a82, v34\n"
"v_accvgpr_write_b32 a90, v42\n"
"v_mfma_f32_16x16x32_bf16 v[20:23], a[236:239], a[148:151], v[20:23]\n"
"ds_write_b128 v243, v[56:59] offset:6144\n"
"v_accvgpr_write_b32 a66, v18\n"
"v_mfma_f32_16x16x32_bf16 v[16:19], a[224:227], a[144:147], v[228:231]\n"
"v_xor_b32 v244, s74, v244\n"
"v_xor_b32 v252, s74, v252\n"
"v_mfma_f32_16x16x32_bf16 v[16:19], a[228:231], a[148:151], v[16:19]\n"
"ds_read_b64_tr_b16 a[206:207], v245 offset:8704\n"
"v_accvgpr_write_b32 a80, v32\n"
"v_mfma_f32_16x16x32_bf16 v[32:35], a[224:227], a[152:155], v[228:231]\n"
"ds_write_b128 v243, v[48:51] offset:4096\n"
"v_mfma_f32_16x16x32_bf16 v[32:35], a[228:231], a[156:159], v[32:35]\n"
"buffer_load_dwordx4 v254, s[24:27], s49 offen offset:2048 lds\n"
"v_mfma_f32_16x16x32_bf16 v[36:39], a[232:235], a[152:155], v[228:231]\n"
"s_waitcnt vmcnt(63) lgkmcnt(3)\n"
"v_accvgpr_write_b32 a89, v41\n"
"v_xor_b32 v247, s74, v247\n"
"v_mfma_f32_16x16x32_bf16 v[36:39], a[236:239], a[156:159], v[36:39]\n"
"s_barrier\n"
"v_mfma_f32_16x16x32_bf16 v[40:43], a[240:243], a[152:155], v[228:231]\n"
"ds_read_b64_tr_b16 a[196:197], v244 offset:8192\n"
"ds_read_b64_tr_b16 a[200:201], v245 offset:0\n"
"v_mfma_f32_16x16x32_bf16 v[40:43], a[244:247], a[156:159], v[40:43]\n"
"v_bfe_u32 v102, v242, 8, 1\n"
"v_lshl_add_u32 v101, v102, 9, v101\n"
"v_mfma_f32_16x16x32_bf16 v[44:47], a[248:251], a[152:155], v[228:231]\n"
"ds_read_b128 a[148:151], v247 offset:2048\n"
"v_xor_b32_e64 v102, s42, 0\n"
"v_mfma_f32_16x16x32_bf16 v[44:47], a[252:255], a[156:159], v[44:47]\n"
"buffer_load_dwordx4 v255, s[24:27], s49 offen offset:3072 lds\n"
"v_mfma_f32_16x16x32_bf16 v[60:63], a[248:251], a[160:163], v[228:231]\n"
"v_lshl_add_u32 v101, v102, 10, v101\n"
"v_add_u32 v101, 94208, v101\n"
"v_mfma_f32_16x16x32_bf16 v[60:63], a[252:255], a[164:167], v[60:63]\n"
"ds_read_b64_tr_b16 a[210:211], v244 offset:1536\n"
"ds_read_b128 a[140:143], v247 offset:0\n"
"v_mfma_f32_16x16x32_bf16 v[56:59], a[240:243], a[160:163], v[228:231]\n"
"ds_read_b128 a[76:79], v101 offset:49152\n"
"v_mfma_f32_16x16x32_bf16 v[56:59], a[244:247], a[164:167], v[56:59]\n"
"s_add_u32 s40, s49, 40960\n"
"ds_read_b128 a[68:71], v101 offset:45056\n"
"v_xor_b32 v246, s74, v246\n"
"v_mfma_f32_16x16x32_bf16 v[52:55], a[232:235], a[160:163], v[228:231]\n"
"s_add_u32 m0, s51, 4096\n"
"ds_read_b64_tr_b16 a[214:215], v244 offset:9728\n"
"ds_read_b64_tr_b16 a[218:219], v245 offset:1536\n"
"v_mfma_f32_16x16x32_bf16 v[52:55], a[236:239], a[164:167], v[52:55]\n"
"buffer_load_dwordx4 v248, s[24:27], s40 offen lds\n"
"v_mfma_f32_16x16x32_bf16 v[48:51], a[224:227], a[160:163], v[228:231]\n"
"ds_read_b128 a[144:147], v246 offset:2048\n"
"ds_read_b128 a[160:163], v252 offset:0\n"
"v_mfma_f32_16x16x32_bf16 v[48:51], a[228:231], a[164:167], v[48:51]\n"
"v_xor_b32 v253, s74, v253\n"
"v_mfma_f32_16x16x32_bf16 v[64:67], a[224:227], a[168:171], v[228:231]\n"
"ds_read_b64_tr_b16 a[202:203], v245 offset:512\n"
"ds_read_b128 a[84:87], v101 offset:61440\n"
"v_mfma_f32_16x16x32_bf16 v[64:67], a[228:231], a[172:175], v[64:67]\n"
"ds_read_b128 a[156:159], v247 offset:4096\n"
"v_add_u32 v102, s75, v101\n"
"v_mfma_f32_16x16x32_bf16 v[68:71], a[232:235], a[168:171], v[228:231]\n"
"ds_read_b64_tr_b16 a[204:205], v245 offset:8192\n"
"ds_read_b128 a[92:95], v102 offset:0\n"
"v_mfma_f32_16x16x32_bf16 v[68:71], a[236:239], a[172:175], v[68:71]\n"
"buffer_load_dwordx4 v249, s[24:27], s40 offen offset:1024 lds\n"
"v_mfma_f32_16x16x32_bf16 v[72:75], a[240:243], a[168:171], v[228:231]\n"
"v_xor_b32 v250, s74, v250\n"
"v_mfma_f32_16x16x32_bf16 v[72:75], a[244:247], a[172:175], v[72:75]\n"
"ds_read_b64_tr_b16 a[216:217], v245 offset:1024\n"
"ds_read_b128 a[152:155], v246 offset:4096\n"
"v_mfma_f32_16x16x32_bf16 v[76:79], a[248:251], a[168:171], v[228:231]\n"
"ds_read_b64_tr_b16 a[192:193], v244 offset:0\n"
"ds_read_b64_tr_b16 a[208:209], v244 offset:1024\n"
"v_mfma_f32_16x16x32_bf16 v[76:79], a[252:255], a[172:175], v[76:79]\n"
"ds_read_b128 a[60:63], v101 offset:32768\n"
"v_mfma_f32_16x16x32_bf16 v[92:95], a[248:251], a[176:179], v[228:231]\n"
"ds_read_b64_tr_b16 a[198:199], v244 offset:8704\n"
"ds_read_b128 a[52:55], v101 offset:0\n"
"v_mfma_f32_16x16x32_bf16 v[92:95], a[252:255], a[180:183], v[92:95]\n"
"buffer_load_dwordx4 v254, s[24:27], s40 offen offset:2048 lds\n"
"v_mfma_f32_16x16x32_bf16 v[88:91], a[240:243], a[176:179], v[228:231]\n"
"s_add_u32 s49, s49, 128\n"
"v_xor_b32 v251, s74, v251\n"
"v_mfma_f32_16x16x32_bf16 v[88:91], a[244:247], a[180:183], v[88:91]\n"
"s_sub_u32 s41, s49, 1280\n"
"ds_read_b64_tr_b16 a[212:213], v244 offset:9216\n"
"ds_read_b128 a[168:171], v250 offset:0\n"
"v_mfma_f32_16x16x32_bf16 v[84:87], a[232:235], a[176:179], v[228:231]\n"
"s_min_u32 s49, s49, s41\n"
"ds_read_b64_tr_b16 a[222:223], v245 offset:9728\n"
"ds_read_b128 a[172:175], v251 offset:0\n"
"v_mfma_f32_16x16x32_bf16 v[84:87], a[236:239], a[180:183], v[84:87]\n"
"s_xor_b32 s51, s51, s74\n"
"ds_read_b128 a[164:167], v253 offset:0\n"
"v_mfma_f32_16x16x32_bf16 v[80:83], a[224:227], a[176:179], v[228:231]\n"
"ds_read_b64_tr_b16 a[220:221], v245 offset:9216\n"
"ds_read_b128 a[176:179], v250 offset:2048\n"
"v_mfma_f32_16x16x32_bf16 v[80:83], a[228:231], a[180:183], v[80:83]\n"
"ds_read_b128 a[136:139], v246 offset:0\n"
"v_mfma_f32_16x16x32_bf16 v[96:99], a[224:227], a[184:187], v[228:231]\n"
"ds_read_b64_tr_b16 a[194:195], v244 offset:512\n"
"ds_read_b128 a[180:183], v251 offset:2048\n"
"v_mfma_f32_16x16x32_bf16 v[96:99], a[228:231], a[188:191], v[96:99]\n"
"v_mfma_f32_16x16x32_bf16 v[100:103], a[232:235], a[184:187], v[228:231]\n"
"v_mfma_f32_16x16x32_bf16 v[100:103], a[236:239], a[188:191], v[100:103]\n"
"v_mfma_f32_16x16x32_bf16 v[104:107], a[240:243], a[184:187], v[228:231]\n"
"v_mfma_f32_16x16x32_bf16 v[104:107], a[244:247], a[188:191], v[104:107]\n"
"v_mfma_f32_16x16x32_bf16 v[108:111], a[248:251], a[184:187], v[228:231]\n"
"s_add_u32 s47, s47, 1\n"
"ds_read_b128 a[184:187], v250 offset:4096\n"
"v_mfma_f32_16x16x32_bf16 v[108:111], a[252:255], a[188:191], v[108:111]\n"
"s_waitcnt lgkmcnt(2)\n"
"ds_read_b128 a[188:191], v251 offset:4096\n"
"s_mov_b32 s62, 18\n"
"s_branch LPL_%=\n"
"LPT2_%=:\n"
"v_mfma_f32_16x16x32_bf16 v[112:115], a[192:195], a[136:139], v[112:115]\n"
"s_add_u32 s78, s32, s96\ns_addc_u32 s79, s33, 0\n"
"s_mov_b64 s[80:81], 0\n"
"s_mov_b32 s82, 0\n"
"v_mfma_f32_16x16x32_bf16 v[112:115], a[196:199], a[140:143], v[112:115]\n"
"v_bfe_u32 v228, v242, 8, 2\n"
"v_mfma_f32_16x16x32_bf16 v[116:119], a[200:203], a[136:139], v[116:119]\n"
"s_waitcnt vmcnt(11)\n"
"s_load_dword s80, s[78:79], 0\n"
"v_mfma_f32_16x16x32_bf16 v[116:119], a[204:207], a[140:143], v[116:119]\n"
"s_barrier\n"
"v_mfma_f32_16x16x32_bf16 v[120:123], a[208:211], a[136:139], v[120:123]\n"
"s_add_u32 s40, s48, 256\n"
"s_xor_b32 m0, s50, 0xc000\n"
"ds_read_b64_tr_b16 a[232:233], v245 offset:16384\n"
"v_mfma_f32_16x16x32_bf16 v[120:123], a[212:215], a[140:143], v[120:123]\n"
"buffer_load_dwordx4 v224, s[28:31], s40 offen lds\n"
"v_mfma_f32_16x16x32_bf16 v[124:127], a[216:219], a[136:139], v[124:127]\n"
"v_mfma_f32_16x16x32_bf16 v[124:127], a[220:223], a[140:143], v[124:127]\n"
"v_mfma_f32_16x16x32_bf16 v[140:143], a[216:219], a[144:147], v[140:143]\n"
"ds_read_b64_tr_b16 a[238:239], v245 offset:25088\n"
"v_mfma_f32_16x16x32_bf16 v[140:143], a[220:223], a[148:151], v[140:143]\n"
"v_mfma_f32_16x16x32_bf16 v[136:139], a[208:211], a[144:147], v[136:139]\n"
"ds_read_b64_tr_b16 a[224:225], v244 offset:16384\n"
"v_mfma_f32_16x16x32_bf16 v[136:139], a[212:215], a[148:151], v[136:139]\n"
"buffer_load_dwordx4 v225, s[28:31], s40 offen offset:1024 lds\n"
"v_mfma_f32_16x16x32_bf16 v[132:135], a[200:203], a[144:147], v[132:135]\n"
"s_add_u32 s82, s82, s80\n"
"v_mfma_f32_16x16x32_bf16 v[132:135], a[204:207], a[148:151], v[132:135]\n"
"s_add_u32 s82, s82, s80\n"
"v_and_b32 v229, 0x60, v242\n"
"v_mfma_f32_16x16x32_bf16 v[128:131], a[192:195], a[144:147], v[128:131]\n"
"s_add_u32 s82, s82, s80\n"
"ds_read_b64_tr_b16 a[236:237], v245 offset:24576\n"
"v_mfma_f32_16x16x32_bf16 v[128:131], a[196:199], a[148:151], v[128:131]\n"
"s_add_u32 s82, s82, s80\n"
"v_lshlrev_b32 v228, 4, v228\n"
"v_mfma_f32_16x16x32_bf16 v[144:147], a[192:195], a[152:155], v[144:147]\n"
"s_add_u32 s82, s82, s80\n"
"ds_read_b64_tr_b16 a[234:235], v245 offset:16896\n"
"v_mfma_f32_16x16x32_bf16 v[144:147], a[196:199], a[156:159], v[144:147]\n"
"s_add_u32 s82, s82, s80\n"
"v_mfma_f32_16x16x32_bf16 v[148:151], a[200:203], a[152:155], v[148:151]\n"
"s_add_u32 s82, s82, s80\n"
"ds_read_b64_tr_b16 a[226:227], v244 offset:16896\n"
"v_mfma_f32_16x16x32_bf16 v[148:151], a[204:207], a[156:159], v[148:151]\n"
"s_add_u32 s82, s82, s80\n"
"buffer_load_dwordx4 v226, s[28:31], s40 offen offset:2048 lds\n"
"v_mfma_f32_16x16x32_bf16 v[152:155], a[208:211], a[152:155], v[152:155]\n"
"s_add_u32 s82, s82, s80\n"
"v_mfma_f32_16x16x32_bf16 v[152:155], a[212:215], a[156:159], v[152:155]\n"
"s_add_u32 s82, s82, s80\n"
"v_mfma_f32_16x16x32_bf16 v[156:159], a[216:219], a[152:155], v[156:159]\n"
"s_add_u32 s82, s82, s80\n"
"ds_read_b64_tr_b16 a[246:247], v244 offset:26112\n"
"v_mfma_f32_16x16x32_bf16 v[156:159], a[220:223], a[156:159], v[156:159]\n"
"s_add_u32 s82, s82, s80\n"
"v_mfma_f32_16x16x32_bf16 v[172:175], a[216:219], a[160:163], v[172:175]\n"
"s_add_u32 s82, s82, s80\n"
"ds_read_b64_tr_b16 a[244:245], v244 offset:25600\n"
"v_mfma_f32_16x16x32_bf16 v[172:175], a[220:223], a[164:167], v[172:175]\n"
"s_add_u32 s82, s82, s80\n"
"buffer_load_dwordx4 v227, s[28:31], s40 offen offset:3072 lds\n"
"v_mfma_f32_16x16x32_bf16 v[168:171], a[208:211], a[160:163], v[168:171]\n"
"s_add_u32 s82, s82, s80\n"
"s_mov_b32 s58, 245760\n"
"v_mfma_f32_16x16x32_bf16 v[168:171], a[212:215], a[164:167], v[168:171]\n"
"s_add_u32 s82, s82, s80\n"
"s_mov_b32 s47, 0\n"
"v_xor_b32 v228, v228, v229\n"
"v_mfma_f32_16x16x32_bf16 v[164:167], a[200:203], a[160:163], v[164:167]\n"
"s_add_u32 s82, s82, s80\n"
"s_mov_b32 s52, -896\n"
"ds_read_b64_tr_b16 a[248:249], v245 offset:17408\n"
"v_mfma_f32_16x16x32_bf16 v[164:167], a[204:207], a[164:167], v[164:167]\n"
"s_load_dword s81, s[78:79], 0x2000\n"
"s_mov_b32 s53, 0\n"
"v_mfma_f32_16x16x32_bf16 v[160:163], a[192:195], a[160:163], v[160:163]\n"
"s_mov_b32 m0, s44\n"
"ds_read_b64_tr_b16 a[230:231], v244 offset:25088\n"
"v_mfma_f32_16x16x32_bf16 v[160:163], a[196:199], a[164:167], v[160:163]\n"
"buffer_load_dwordx4 v240, s[16:19], s56 offen nt lds\n"
"v_mfma_f32_16x16x32_bf16 v[176:179], a[192:195], a[168:171], v[176:179]\n"
"v_mfma_f32_16x16x32_bf16 v[176:179], a[196:199], a[172:175], v[176:179]\n"
"v_and_b32 v229, 0x70, v242\n"
"v_bfe_i32 v241, v242, 4, 1\n"
"v_mfma_f32_16x16x32_bf16 v[180:183], a[200:203], a[168:171], v[180:183]\n"
"ds_read_b64_tr_b16 a[228:229], v244 offset:24576\n"
"v_xor_b32 v250, s74, v250\n"
"v_mfma_f32_16x16x32_bf16 v[180:183], a[204:207], a[172:175], v[180:183]\n"
"v_mfma_f32_16x16x32_bf16 v[184:187], a[208:211], a[168:171], v[184:187]\n"
"ds_read_b64_tr_b16 a[252:253], v245 offset:25600\n"
"v_lshl_add_u32 v228, v229, 3, v228\n"
"v_mfma_f32_16x16x32_bf16 v[184:187], a[212:215], a[172:175], v[184:187]\n"
"v_mfma_f32_16x16x32_bf16 v[188:191], a[216:219], a[168:171], v[188:191]\n"
"s_add_u32 s40, s56, s52\n"
"ds_read_b64_tr_b16 a[254:255], v245 offset:26112\n"
"v_mfma_f32_16x16x32_bf16 v[188:191], a[220:223], a[172:175], v[188:191]\n"
"s_sub_u32 s82, s82, s81\n"
"buffer_load_dwordx4 v240, s[16:19], s40 offen offset:1024 nt lds\n"
"v_mfma_f32_16x16x32_bf16 v[204:207], a[216:219], a[176:179], v[204:207]\n"
"s_sub_u32 s82, s82, s81\n"
"v_mfma_f32_16x16x32_bf16 v[204:207], a[220:223], a[180:183], v[204:207]\n"
"s_sub_u32 s82, s82, s81\n"
"v_mfma_f32_16x16x32_bf16 v[200:203], a[208:211], a[176:179], v[200:203]\n"
"s_sub_u32 s82, s82, s81\n"
"ds_read_b64_tr_b16 a[242:243], v244 offset:17920\n"
"v_mfma_f32_16x16x32_bf16 v[200:203], a[212:215], a[180:183], v[200:203]\n"
"s_sub_u32 s82, s82, s81\n"
"v_mfma_f32_16x16x32_bf16 v[196:199], a[200:203], a[176:179], v[196:199]\n"
"s_sub_u32 s82, s82, s81\n"
"s_add_u32 s40, s56, s53\n"
"v_mfma_f32_16x16x32_bf16 v[196:199], a[204:207], a[180:183], v[196:199]\n"
"s_sub_u32 s82, s82, s81\n"
"buffer_load_dwordx4 v240, s[16:19], s40 offen offset:2048 nt lds\n"
"v_mfma_f32_16x16x32_bf16 v[192:195], a[192:195], a[176:179], v[192:195]\n"
"s_sub_u32 s82, s82, s81\n"
"v_mfma_f32_16x16x32_bf16 v[192:195], a[196:199], a[180:183], v[192:195]\n"
"s_waitcnt lgkmcnt(1)\n"
"s_sub_u32 s82, s82, s81\n"
"v_and_b32 v229, 0x80, v242\n"
"v_mfma_f32_16x16x32_bf16 v[208:211], a[192:195], a[184:187], v[208:211]\n"
"s_sub_u32 s82, s82, s81\n"
"ds_read_b64_tr_b16 a[250:251], v245 offset:17920\n"
"v_mfma_f32_16x16x32_bf16 v[208:211], a[196:199], a[188:191], v[208:211]\n"
"s_sub_u32 s82, s82, s81\n"
"v_mfma_f32_16x16x32_bf16 v[212:215], a[200:203], a[184:187], v[212:215]\n"
"s_sub_u32 s82, s82, s81\n"
"s_add_u32 s40, s56, s54\n"
"ds_read_b64_tr_b16 a[240:241], v244 offset:17408\n"
"v_mfma_f32_16x16x32_bf16 v[212:215], a[204:207], a[188:191], v[212:215]\n"
"s_sub_u32 s82, s82, s81\n"
"buffer_load_dwordx4 v240, s[16:19], s40 offen offset:3072 nt lds\n"
"v_mfma_f32_16x16x32_bf16 v[216:219], a[208:211], a[184:187], v[216:219]\n"
"s_sub_u32 s82, s82, s81\n"
"v_xor_b32 v244, s74, v244\n"
"v_mfma_f32_16x16x32_bf16 v[216:219], a[212:215], a[188:191], v[216:219]\n"
"s_sub_u32 s82, s82, s81\n"
"v_lshl_add_u32 v228, v229, 4, v228\n"
"v_mfma_f32_16x16x32_bf16 v[220:223], a[216:219], a[184:187], v[220:223]\n"
"s_sub_u32 s82, s82, s81\n"
"v_mfma_f32_16x16x32_bf16 v[220:223], a[220:223], a[188:191], v[220:223]\n"
"s_sub_u32 s82, s82, s81\n"
"v_mfma_f32_16x16x32_bf16 v[0:3], a[224:227], a[136:139], v[0:3]\n"
"v_mfma_f32_16x16x32_bf16 v[0:3], a[228:231], a[140:143], v[0:3]\n"
"v_mfma_f32_16x16x32_bf16 v[4:7], a[232:235], a[136:139], v[4:7]\n"
"s_waitcnt vmcnt(8) lgkmcnt(0)\n"
"v_mfma_f32_16x16x32_bf16 v[4:7], a[236:239], a[140:143], v[4:7]\n"
"s_barrier\n"
"v_mfma_f32_16x16x32_bf16 v[8:11], a[240:243], a[136:139], v[8:11]\n"
"s_add_u32 m0, s44, 65536\n"
"ds_read_b64_tr_b16 a[212:213], v244 offset:9216\n"
"v_xor_b32 v246, s74, v246\n"
"v_mfma_f32_16x16x32_bf16 v[8:11], a[244:247], a[140:143], v[8:11]\n"
"buffer_load_dwordx4 v240, s[20:23], s56 offen nt lds\n"
"v_mfma_f32_16x16x32_bf16 v[12:15], a[248:251], a[136:139], v[12:15]\n"
"s_lshr_b32 s82, s82, 31\n"
"v_xor_b32 v245, s74, v245\n"
"v_mfma_f32_16x16x32_bf16 v[12:15], a[252:255], a[140:143], v[12:15]\n"
"s_add_u32 s82, s82, s60\n"
"v_mfma_f32_16x16x32_bf16 v[28:31], a[248:251], a[144:147], v[28:31]\n"
"s_add_u32 s82, s82, 32\n"
"ds_read_b64_tr_b16 a[204:205], v245 offset:8192\n"
"v_mfma_f32_16x16x32_bf16 v[28:31], a[252:255], a[148:151], v[28:31]\n"
"v_mov_b32 v254, s82\n"
"ds_read_b128 a[136:139], v246 offset:0\n"
"v_mfma_f32_16x16x32_bf16 v[24:27], a[240:243], a[144:147], v[24:27]\n"
"v_lshlrev_b32_e64 v255, 2, s93\n"
"ds_read_b64_tr_b16 a[210:211], v244 offset:1536\n"
"v_mfma_f32_16x16x32_bf16 v[24:27], a[244:247], a[148:151], v[24:27]\n"
"ds_read_b64_tr_b16 a[208:209], v244 offset:1024\n"
"v_mfma_f32_16x16x32_bf16 v[20:23], a[232:235], a[144:147], v[20:23]\n"
"s_add_u32 s40, s56, s52\n"
"ds_read_b64_tr_b16 a[196:197], v244 offset:8192\n"
"v_xor_b32 v252, s74, v252\n"
"v_mfma_f32_16x16x32_bf16 v[20:23], a[236:239], a[148:151], v[20:23]\n"
"buffer_load_dwordx4 v240, s[20:23], s40 offen offset:1024 nt lds\n"
"v_mfma_f32_16x16x32_bf16 v[16:19], a[224:227], a[144:147], v[16:19]\n"
"v_mfma_f32_16x16x32_bf16 v[16:19], a[228:231], a[148:151], v[16:19]\n"
"s_mov_b64 exec, 1\n"
"global_store_dword v255, v254, s[6:7] offset:256 sc1\n"
"s_mov_b64 exec, -1\n"
"v_mfma_f32_16x16x32_bf16 v[32:35], a[224:227], a[152:155], v[32:35]\n"
"ds_read_b128 a[144:147], v246 offset:2048\n"
"v_mfma_f32_16x16x32_bf16 v[32:35], a[228:231], a[156:159], v[32:35]\n"
"ds_read_b64_tr_b16 a[222:223], v245 offset:9728\n"
"v_mfma_f32_16x16x32_bf16 v[36:39], a[232:235], a[152:155], v[36:39]\n"
"s_add_u32 s40, s56, s53\n"
"ds_read_b64_tr_b16 a[198:199], v244 offset:8704\n"
"v_mfma_f32_16x16x32_bf16 v[36:39], a[236:239], a[156:159], v[36:39]\n"
"buffer_load_dwordx4 v240, s[20:23], s40 offen offset:2048 nt lds\n"
"v_mfma_f32_16x16x32_bf16 v[40:43], a[240:243], a[152:155], v[40:43]\n"
"v_mov_b32 v229, s43\n"
"v_xor_b32 v251, s74, v251\n"
"v_mfma_f32_16x16x32_bf16 v[40:43], a[244:247], a[156:159], v[40:43]\n"
"v_mfma_f32_16x16x32_bf16 v[44:47], a[248:251], a[152:155], v[44:47]\n"
"ds_read_b64_tr_b16 a[206:207], v245 offset:8704\n"
"v_xor_b32 v247, s74, v247\n"
"v_mfma_f32_16x16x32_bf16 v[44:47], a[252:255], a[156:159], v[44:47]\n"
"ds_read_b64_tr_b16 a[200:201], v245 offset:0\n"
"v_mul_u32_u24 v229, 0x81, v229\n"
"v_mfma_f32_16x16x32_bf16 v[60:63], a[248:251], a[160:163], v[60:63]\n"
"s_add_u32 s40, s56, s54\n"
"ds_read_b64_tr_b16 a[192:193], v244 offset:0\n"
"v_and_b32 v241, 0xaaaaaaaa, v241\n"
"v_mfma_f32_16x16x32_bf16 v[60:63], a[252:255], a[164:167], v[60:63]\n"
"buffer_load_dwordx4 v240, s[20:23], s40 offen offset:3072 nt lds\n"
"v_mfma_f32_16x16x32_bf16 v[56:59], a[240:243], a[160:163], v[56:59]\n"
"s_add_u32 s55, s55, s59\n"
"v_mfma_f32_16x16x32_bf16 v[56:59], a[244:247], a[164:167], v[56:59]\n"
"s_add_u32 s41, s55, s57\n"
"v_mfma_f32_16x16x32_bf16 v[52:55], a[232:235], a[160:163], v[52:55]\n"
"s_and_b32 s41, s41, s58\n"
"ds_read_b64_tr_b16 a[220:221], v245 offset:9216\n"
"v_mfma_f32_16x16x32_bf16 v[52:55], a[236:239], a[164:167], v[52:55]\n"
"s_andn2_b32 s56, s55, s58\n"
"v_mfma_f32_16x16x32_bf16 v[48:51], a[224:227], a[160:163], v[48:51]\n"
"s_or_b32 s56, s56, s41\n"
"ds_read_b128 a[140:143], v247 offset:0\n"
"v_mfma_f32_16x16x32_bf16 v[48:51], a[228:231], a[164:167], v[48:51]\n"
"v_mfma_f32_16x16x32_bf16 v[64:67], a[224:227], a[168:171], v[64:67]\n"
"s_add_u32 m0, s44, 16384\n"
"ds_read_b64_tr_b16 a[216:217], v245 offset:1024\n"
"v_mfma_f32_16x16x32_bf16 v[64:67], a[228:231], a[172:175], v[64:67]\n"
"buffer_load_dwordx4 v240, s[16:19], s56 offen nt lds\n"
"v_mfma_f32_16x16x32_bf16 v[68:71], a[232:235], a[168:171], v[68:71]\n"
"v_xor_b32 v253, s74, v253\n"
"v_mfma_f32_16x16x32_bf16 v[68:71], a[236:239], a[172:175], v[68:71]\n"
"v_mfma_f32_16x16x32_bf16 v[72:75], a[240:243], a[168:171], v[72:75]\n"
"ds_read_b64_tr_b16 a[194:195], v244 offset:512\n"
"v_mfma_f32_16x16x32_bf16 v[72:75], a[244:247], a[172:175], v[72:75]\n"
"ds_read_b64_tr_b16 a[202:203], v245 offset:512\n"
"v_or_b32 v241, 0x44444444, v241\n"
"v_mfma_f32_16x16x32_bf16 v[76:79], a[248:251], a[168:171], v[76:79]\n"
"s_add_u32 s40, s56, s52\n"
"ds_read_b64_tr_b16 a[218:219], v245 offset:1536\n"
"v_mfma_f32_16x16x32_bf16 v[76:79], a[252:255], a[172:175], v[76:79]\n"
"buffer_load_dwordx4 v240, s[16:19], s40 offen offset:1024 nt lds\n"
"v_mfma_f32_16x16x32_bf16 v[92:95], a[248:251], a[176:179], v[92:95]\n"
"v_mfma_f32_16x16x32_bf16 v[92:95], a[252:255], a[180:183], v[92:95]\n"
"v_mfma_f32_16x16x32_bf16 v[88:91], a[240:243], a[176:179], v[88:91]\n"
"ds_read_b128 a[168:171], v250 offset:0\n"
"v_mfma_f32_16x16x32_bf16 v[88:91], a[244:247], a[180:183], v[88:91]\n"
"v_mfma_f32_16x16x32_bf16 v[84:87], a[232:235], a[176:179], v[84:87]\n"
"s_add_u32 s40, s56, s53\n"
"ds_read_b128 a[152:155], v246 offset:4096\n"
"v_mfma_f32_16x16x32_bf16 v[84:87], a[236:239], a[180:183], v[84:87]\n"
"buffer_load_dwordx4 v240, s[16:19], s40 offen offset:2048 nt lds\n"
"v_mfma_f32_16x16x32_bf16 v[80:83], a[224:227], a[176:179], v[80:83]\n"
"v_mfma_f32_16x16x32_bf16 v[80:83], a[228:231], a[180:183], v[80:83]\n"
"v_xor_b32 v228, v228, v229\n"
"v_mfma_f32_16x16x32_bf16 v[96:99], a[224:227], a[184:187], v[96:99]\n"
"ds_read_b128 a[176:179], v250 offset:2048\n"
"v_mfma_f32_16x16x32_bf16 v[96:99], a[228:231], a[188:191], v[96:99]\n"
"v_mfma_f32_16x16x32_bf16 v[100:103], a[232:235], a[184:187], v[100:103]\n"
"s_add_u32 s40, s56, s54\n"
"ds_read_b64_tr_b16 a[214:215], v244 offset:9728\n"
"v_mfma_f32_16x16x32_bf16 v[100:103], a[236:239], a[188:191], v[100:103]\n"
"buffer_load_dwordx4 v240, s[16:19], s40 offen offset:3072 nt lds\n"
"v_mfma_f32_16x16x32_bf16 v[104:107], a[240:243], a[184:187], v[104:107]\n"
"v_xor_b32 v230, 0x2000, v228\n"
"v_mfma_f32_16x16x32_bf16 v[104:107], a[244:247], a[188:191], v[104:107]\n"
"v_mfma_f32_16x16x32_bf16 v[108:111], a[248:251], a[184:187], v[108:111]\n"
"ds_read_b128 a[164:167], v253 offset:0\n"
"v_and_b32 v254, 0x20, v242\n"
"v_mfma_f32_16x16x32_bf16 v[108:111], a[252:255], a[188:191], v[108:111]\n"
"s_waitcnt lgkmcnt(3)\n"
"v_mfma_f32_16x16x32_bf16 v[112:115], a[192:195], a[136:139], v[112:115]\n"
"ds_read_b128 a[172:175], v251 offset:0\n"
"v_bfe_u32 v255, v242, 9, 1\n"
"v_mfma_f32_16x16x32_bf16 v[112:115], a[196:199], a[140:143], v[112:115]\n"
"v_and_b32 v249, 0xf0, v242\n"
"v_add_u32 v254, -1, v254\n"
"v_mfma_f32_16x16x32_bf16 v[116:119], a[200:203], a[136:139], v[116:119]\n"
"s_waitcnt vmcnt(13) lgkmcnt(2)\n"
"ds_read_b128 a[180:183], v251 offset:2048\n"
"v_mfma_f32_16x16x32_bf16 v[116:119], a[204:207], a[140:143], v[116:119]\n"
"s_barrier\n"
"v_mfma_f32_16x16x32_bf16 v[120:123], a[208:211], a[136:139], v[120:123]\n"
"s_add_u32 m0, s44, 81920\n"
"ds_read_b64_tr_b16 a[226:227], v244 offset:16896\n"
"v_bfe_u32 v248, v242, 9, 1\n"
"v_mfma_f32_16x16x32_bf16 v[120:123], a[212:215], a[140:143], v[120:123]\n"
"buffer_load_dwordx4 v240, s[20:23], s56 offen nt lds\n"
"v_mfma_f32_16x16x32_bf16 v[124:127], a[216:219], a[136:139], v[124:127]\n"
"v_lshl_add_u32 v255, v255, 8, v249\n"
"v_mfma_f32_16x16x32_bf16 v[124:127], a[220:223], a[140:143], v[124:127]\n"
"v_mfma_f32_16x16x32_bf16 v[188:191], a[216:219], a[168:171], v[188:191]\n"
"s_waitcnt lgkmcnt(2)\n"
"ds_read_b64_tr_b16 a[238:239], v245 offset:25088\n"
"v_mfma_f32_16x16x32_bf16 v[188:191], a[220:223], a[172:175], v[188:191]\n"
"v_xor_b32 v229, 64, v228\n"
"v_mfma_f32_16x16x32_bf16 v[184:187], a[208:211], a[168:171], v[184:187]\n"
"ds_read_b128 a[188:191], v251 offset:4096\n"
"v_ashrrev_i32 v254, 31, v254\n"
"v_mfma_f32_16x16x32_bf16 v[184:187], a[212:215], a[172:175], v[184:187]\n"
"v_mfma_f32_16x16x32_bf16 v[180:183], a[200:203], a[168:171], v[180:183]\n"
"ds_read_b128 a[184:187], v250 offset:4096\n"
"v_mfma_f32_16x16x32_bf16 v[180:183], a[204:207], a[172:175], v[180:183]\n"
"v_mfma_f32_16x16x32_bf16 v[176:179], a[192:195], a[168:171], v[176:179]\n"
"ds_read_b128 a[160:163], v252 offset:0\n"
"v_mfma_f32_16x16x32_bf16 v[176:179], a[196:199], a[172:175], v[176:179]\n"
"v_mfma_f32_16x16x32_bf16 v[192:195], a[192:195], a[176:179], v[192:195]\n"
"s_waitcnt lgkmcnt(3)\n"
"ds_read_b64_tr_b16 a[252:253], v245 offset:25600\n"
"v_mfma_f32_16x16x32_bf16 v[192:195], a[196:199], a[180:183], v[192:195]\n"
"v_mfma_f32_16x16x32_bf16 v[196:199], a[200:203], a[176:179], v[196:199]\n"
"s_add_u32 s40, s56, s52\n"
"ds_read_b128 a[148:151], v247 offset:2048\n"
"v_mfma_f32_16x16x32_bf16 v[196:199], a[204:207], a[180:183], v[196:199]\n"
"buffer_load_dwordx4 v240, s[20:23], s40 offen offset:1024 nt lds\n"
"v_mfma_f32_16x16x32_bf16 v[200:203], a[208:211], a[176:179], v[200:203]\n"
"v_bfe_u32 v249, v242, 8, 1\n"
"v_lshl_add_u32 v255, v249, 3, v255\n"
"v_mfma_f32_16x16x32_bf16 v[200:203], a[212:215], a[180:183], v[200:203]\n"
"v_and_b32 v254, 0x3f803f80, v254\n"
"v_mfma_f32_16x16x32_bf16 v[204:207], a[216:219], a[176:179], v[204:207]\n"
"ds_read_b64_tr_b16 a[240:241], v244 offset:17408\n"
"v_xor_b32_e64 v249, s42, 1\n"
"v_mfma_f32_16x16x32_bf16 v[204:207], a[220:223], a[180:183], v[204:207]\n"
"s_waitcnt lgkmcnt(3)\n"
"v_lshl_add_u32 v255, v249, 12, v255\n"
"v_mfma_f32_16x16x32_bf16 v[220:223], a[216:219], a[184:187], v[220:223]\n"
"ds_read_b64_tr_b16 a[242:243], v244 offset:17920\n"
"v_xor_b32 v231, 0x2000, v229\n"
"v_mfma_f32_16x16x32_bf16 v[220:223], a[220:223], a[188:191], v[220:223]\n"
"ds_read_b64_tr_b16 a[244:245], v244 offset:25600\n"
"v_mfma_f32_16x16x32_bf16 v[216:219], a[208:211], a[184:187], v[216:219]\n"
"ds_read_b64_tr_b16 a[232:233], v245 offset:16384\n"
"v_mfma_f32_16x16x32_bf16 v[216:219], a[212:215], a[188:191], v[216:219]\n"
"v_pk_fma_f32 v[176:177], v[176:177], s[70:71], s[70:71] op_sel:[0,0,1] op_sel_hi:[1,0,1]\nv_pk_fma_f32 v[178:179], v[178:179], s[70:71], s[70:71] op_sel:[0,0,1] op_sel_hi:[1,0,1]\nv_pk_fma_f32 v[180:181], v[180:181], s[70:71], s[70:71] op_sel:[0,0,1] op_sel_hi:[1,0,1]\nv_pk_fma_f32 v[182:183], v[182:183], s[70:71], s[70:71] op_sel:[0,0,1] op_sel_hi:[1,0,1]\nv_pk_fma_f32 v[184:185], v[184:185], s[70:71], s[70:71] op_sel:[0,0,1] op_sel_hi:[1,0,1]\nv_pk_fma_f32 v[186:187], v[186:187], s[70:71], s[70:71] op_sel:[0,0,1] op_sel_hi:[1,0,1]\nv_pk_fma_f32 v[188:189], v[188:189], s[70:71], s[70:71] op_sel:[0,0,1] op_sel_hi:[1,0,1]\nv_pk_fma_f32 v[190:191], v[190:191], s[70:71], s[70:71] op_sel:[0,0,1] op_sel_hi:[1,0,1]\nv_pk_fma_f32 v[192:193], v[192:193], s[70:71], s[70:71] op_sel:[0,0,1] op_sel_hi:[1,0,1]\nv_pk_fma_f32 v[194:195], v[194:195], s[70:71], s[70:71] op_sel:[0,0,1] op_sel_hi:[1,0,1]\nv_pk_fma_f32 v[196:197], v[196:197], s[70:71], s[70:71] op_sel:[0,0,1] op_sel_hi:[1,0,1]\nv_pk_fma_f32 v[198:199], v[198:199], s[70:71], s[70:71] op_sel:[0,0,1] op_sel_hi:[1,0,1]\nv_pk_fma_f32 v[200:201], v[200:201], s[70:71], s[70:71] op_sel:[0,0,1] op_sel_hi:[1,0,1]\nv_pk_fma_f32 v[202:203], v[202:203], s[70:71], s[70:71] op_sel:[0,0,1] op_sel_hi:[1,0,1]\nv_pk_fma_f32 v[204:205], v[204:205], s[70:71], s[70:71] op_sel:[0,0,1] op_sel_hi:[1,0,1]\nv_pk_fma_f32 v[206:207], v[206:207], s[70:71], s[70:71] op_sel:[0,0,1] op_sel_hi:[1,0,1]\n"
"v_mfma_f32_16x16x32_bf16 v[212:215], a[200:203], a[184:187], v[212:215]\n"
"ds_read_b64_tr_b16 a[234:235], v245 offset:16896\n"
"v_mfma_f32_16x16x32_bf16 v[212:215], a[204:207], a[188:191], v[212:215]\n"
"v_cvt_pk_bf16_f32 v192, v192, v193\n"
"v_cvt_pk_bf16_f32 v200, v200, v201\n"
"v_mfma_f32_16x16x32_bf16 v[208:211], a[192:195], a[184:187], v[208:211]\n"
"s_add_u32 s40, s56, s53\n"
"ds_read_b64_tr_b16 a[248:249], v245 offset:17408\n"
"v_cvt_pk_bf16_f32 v201, v202, v203\n"
"v_mfma_f32_16x16x32_bf16 v[208:211], a[196:199], a[188:191], v[208:211]\n"
"buffer_load_dwordx4 v240, s[20:23], s40 offen offset:2048 nt lds\n"
"v_mfma_f32_16x16x32_bf16 v[160:163], a[192:195], a[160:163], v[160:163]\n"
"v_cvt_pk_bf16_f32 v176, v176, v177\n"
"v_cvt_pk_bf16_f32 v184, v184, v185\n"
"v_mfma_f32_16x16x32_bf16 v[160:163], a[196:199], a[164:167], v[160:163]\n"
"v_cvt_pk_bf16_f32 v177, v178, v179\n"
"v_cvt_pk_bf16_f32 v185, v186, v187\n"
"v_mfma_f32_16x16x32_bf16 v[164:167], a[200:203], a[160:163], v[164:167]\n"
"ds_read_b64_tr_b16 a[246:247], v244 offset:26112\n"
"v_cvt_pk_bf16_f32 v193, v194, v195\n"
"v_mfma_f32_16x16x32_bf16 v[164:167], a[204:207], a[164:167], v[164:167]\n"
"v_cvt_pk_bf16_f32 v179, v182, v183\n"
"v_cvt_pk_bf16_f32 v195, v198, v199\n"
"v_mfma_f32_16x16x32_bf16 v[168:171], a[208:211], a[160:163], v[168:171]\n"
"ds_read_b64_tr_b16 a[254:255], v245 offset:26112\n"
"v_cvt_pk_bf16_f32 v187, v190, v191\n"
"v_mfma_f32_16x16x32_bf16 v[168:171], a[212:215], a[164:167], v[168:171]\n"
"ds_read_b64_tr_b16 a[230:231], v244 offset:25088\n"
"v_cvt_pk_bf16_f32 v194, v196, v197\n"
"v_mfma_f32_16x16x32_bf16 v[172:175], a[216:219], a[160:163], v[172:175]\n"
"ds_read_b64_tr_b16 a[224:225], v244 offset:16384\n"
"v_bfe_u32 v196, v242, 8, 2\n"
"v_mfma_f32_16x16x32_bf16 v[172:175], a[220:223], a[164:167], v[172:175]\n"
"v_cvt_pk_bf16_f32 v202, v204, v205\n"
"v_and_b32 v249, 0xf0, v242\n"
"v_mfma_f32_16x16x32_bf16 v[140:143], a[216:219], a[144:147], v[140:143]\n"
"s_waitcnt lgkmcnt(4)\n"
"ds_read_b64_tr_b16 a[228:229], v244 offset:24576\n"
"v_lshl_add_u32 v196, v196, 4, s43\n"
"v_mfma_f32_16x16x32_bf16 v[140:143], a[220:223], a[148:151], v[140:143]\n"
"v_lshl_add_u32 v248, v248, 8, v249\n"
"v_bfe_u32 v249, v242, 8, 1\n"
"v_mfma_f32_16x16x32_bf16 v[136:139], a[208:211], a[144:147], v[136:139]\n"
"ds_read_b128 a[156:159], v247 offset:4096\n"
"v_xor_b32 v198, 0x400, v243\n"
"v_mfma_f32_16x16x32_bf16 v[136:139], a[212:215], a[148:151], v[136:139]\n"
"v_pk_fma_f32 v[208:209], v[208:209], s[70:71], s[70:71] op_sel:[0,0,1] op_sel_hi:[1,0,1]\nv_pk_fma_f32 v[210:211], v[210:211], s[70:71], s[70:71] op_sel:[0,0,1] op_sel_hi:[1,0,1]\nv_pk_fma_f32 v[212:213], v[212:213], s[70:71], s[70:71] op_sel:[0,0,1] op_sel_hi:[1,0,1]\nv_pk_fma_f32 v[214:215], v[214:215], s[70:71], s[70:71] op_sel:[0,0,1] op_sel_hi:[1,0,1]\nv_pk_fma_f32 v[216:217], v[216:217], s[70:71], s[70:71] op_sel:[0,0,1] op_sel_hi:[1,0,1]\nv_pk_fma_f32 v[218:219], v[218:219], s[70:71], s[70:71] op_sel:[0,0,1] op_sel_hi:[1,0,1]\nv_pk_fma_f32 v[220:221], v[220:221], s[70:71], s[70:71] op_sel:[0,0,1] op_sel_hi:[1,0,1]\nv_pk_fma_f32 v[222:223], v[222:223], s[70:71], s[70:71] op_sel:[0,0,1] op_sel_hi:[1,0,1]\nv_pk_fma_f32 v[160:161], v[160:161], s[70:71], s[70:71] op_sel:[0,0,1] op_sel_hi:[1,0,1]\nv_pk_fma_f32 v[162:163], v[162:163], s[70:71], s[70:71] op_sel:[0,0,1] op_sel_hi:[1,0,1]\nv_pk_fma_f32 v[164:165], v[164:165], s[70:71], s[70:71] op_sel:[0,0,1] op_sel_hi:[1,0,1]\nv_pk_fma_f32 v[166:167], v[166:167], s[70:71], s[70:71] op_sel:[0,0,1] op_sel_hi:[1,0,1]\nv_pk_fma_f32 v[168:169], v[168:169], s[70:71], s[70:71] op_sel:[0,0,1] op_sel_hi:[1,0,1]\nv_pk_fma_f32 v[170:171], v[170:171], s[70:71], s[70:71] op_sel:[0,0,1] op_sel_hi:[1,0,1]\nv_pk_fma_f32 v[172:173], v[172:173], s[70:71], s[70:71] op_sel:[0,0,1] op_sel_hi:[1,0,1]\nv_pk_fma_f32 v[174:175], v[174:175], s[70:71], s[70:71] op_sel:[0,0,1] op_sel_hi:[1,0,1]\n"
"v_mfma_f32_16x16x32_bf16 v[132:135], a[200:203], a[144:147], v[132:135]\n"
"s_add_u32 s40, s56, s54\n"
"ds_read_b64_tr_b16 a[250:251], v245 offset:17920\n"
"v_cvt_pk_bf16_f32 v168, v168, v169\n"
"v_mfma_f32_16x16x32_bf16 v[132:135], a[204:207], a[148:151], v[132:135]\n"
"buffer_load_dwordx4 v240, s[20:23], s40 offen offset:3072 nt lds\n"
"v_mfma_f32_16x16x32_bf16 v[128:131], a[192:195], a[144:147], v[128:131]\n"
"s_add_u32 s55, s55, s59\n"
"v_cvt_pk_bf16_f32 v208, v208, v209\n"
"v_cvt_pk_bf16_f32 v169, v170, v171\n"
"v_mfma_f32_16x16x32_bf16 v[128:131], a[196:199], a[148:151], v[128:131]\n"
"s_add_u32 s41, s55, s57\n"
"v_cvt_pk_bf16_f32 v178, v180, v181\n"
"v_cvt_pk_bf16_f32 v171, v174, v175\n"
"v_mfma_f32_16x16x32_bf16 v[144:147], a[192:195], a[152:155], v[144:147]\n"
"s_waitcnt lgkmcnt(1)\n"
"s_and_b32 s41, s41, s58\n"
"v_cvt_pk_bf16_f32 v209, v210, v211\n"
"v_cvt_pk_bf16_f32 v170, v172, v173\n"
"v_mfma_f32_16x16x32_bf16 v[144:147], a[196:199], a[156:159], v[144:147]\n"
"s_andn2_b32 s56, s55, s58\n"
"ds_read_b64_tr_b16 a[236:237], v245 offset:24576\n"
"v_cvt_pk_bf16_f32 v160, v160, v161\n"
"v_mfma_f32_16x16x32_bf16 v[148:151], a[200:203], a[152:155], v[148:151]\n"
"s_or_b32 s56, s56, s41\n"
"v_cvt_pk_bf16_f32 v216, v216, v217\n"
"v_mov_b32 v205, 0\n"
"v_mfma_f32_16x16x32_bf16 v[148:151], a[204:207], a[156:159], v[148:151]\n"
"v_pk_fma_f32 v[112:113], v[112:113], s[70:71], s[70:71] op_sel:[0,0,1] op_sel_hi:[1,0,1]\nv_pk_fma_f32 v[114:115], v[114:115], s[70:71], s[70:71] op_sel:[0,0,1] op_sel_hi:[1,0,1]\nv_pk_fma_f32 v[116:117], v[116:117], s[70:71], s[70:71] op_sel:[0,0,1] op_sel_hi:[1,0,1]\nv_pk_fma_f32 v[118:119], v[118:119], s[70:71], s[70:71] op_sel:[0,0,1] op_sel_hi:[1,0,1]\nv_pk_fma_f32 v[120:121], v[120:121], s[70:71], s[70:71] op_sel:[0,0,1] op_sel_hi:[1,0,1]\nv_pk_fma_f32 v[122:123], v[122:123], s[70:71], s[70:71] op_sel:[0,0,1] op_sel_hi:[1,0,1]\nv_pk_fma_f32 v[124:125], v[124:125], s[70:71], s[70:71] op_sel:[0,0,1] op_sel_hi:[1,0,1]\nv_pk_fma_f32 v[126:127], v[126:127], s[70:71], s[70:71] op_sel:[0,0,1] op_sel_hi:[1,0,1]\nv_pk_fma_f32 v[128:129], v[128:129], s[70:71], s[70:71] op_sel:[0,0,1] op_sel_hi:[1,0,1]\nv_pk_fma_f32 v[130:131], v[130:131], s[70:71], s[70:71] op_sel:[0,0,1] op_sel_hi:[1,0,1]\nv_pk_fma_f32 v[132:133], v[132:133], s[70:71], s[70:71] op_sel:[0,0,1] op_sel_hi:[1,0,1]\nv_pk_fma_f32 v[134:135], v[134:135], s[70:71], s[70:71] op_sel:[0,0,1] op_sel_hi:[1,0,1]\nv_pk_fma_f32 v[136:137], v[136:137], s[70:71], s[70:71] op_sel:[0,0,1] op_sel_hi:[1,0,1]\nv_pk_fma_f32 v[138:139], v[138:139], s[70:71], s[70:71] op_sel:[0,0,1] op_sel_hi:[1,0,1]\nv_pk_fma_f32 v[140:141], v[140:141], s[70:71], s[70:71] op_sel:[0,0,1] op_sel_hi:[1,0,1]\nv_pk_fma_f32 v[142:143], v[142:143], s[70:71], s[70:71] op_sel:[0,0,1] op_sel_hi:[1,0,1]\n"
"v_mfma_f32_16x16x32_bf16 v[152:155], a[208:211], a[152:155], v[152:155]\n"
"v_cvt_pk_bf16_f32 v217, v218, v219\n"
"v_xor_b32 v197, 64, v196\n"
"v_mfma_f32_16x16x32_bf16 v[152:155], a[212:215], a[156:159], v[152:155]\n"
"v_cvt_pk_bf16_f32 v136, v136, v137\n"
"v_mfma_f32_16x16x32_bf16 v[156:159], a[216:219], a[152:155], v[156:159]\n"
"v_cvt_pk_bf16_f32 v186, v188, v189\n"
"v_cvt_pk_bf16_f32 v161, v162, v163\n"
"v_mfma_f32_16x16x32_bf16 v[156:159], a[220:223], a[156:159], v[156:159]\n"
"v_cvt_pk_bf16_f32 v162, v164, v165\n"
"v_cvt_pk_bf16_f32 v163, v166, v167\n"
"v_mfma_f32_16x16x32_bf16 v[96:99], a[224:227], a[184:187], v[96:99]\n"
"s_add_u32 m0, s44, 32768\n"
"v_cvt_pk_bf16_f32 v211, v214, v215\n"
"v_cvt_pk_bf16_f32 v128, v128, v129\n"
"v_mfma_f32_16x16x32_bf16 v[96:99], a[228:231], a[188:191], v[96:99]\n"
"buffer_load_dwordx4 v240, s[16:19], s56 offen nt lds\n"
"v_mfma_f32_16x16x32_bf16 v[100:103], a[232:235], a[184:187], v[100:103]\n"
"s_waitcnt vmcnt(14) lgkmcnt(0)\n"
"v_cvt_pk_bf16_f32 v120, v120, v121\n"
"v_cvt_pk_bf16_f32 v137, v138, v139\n"
"v_mfma_f32_16x16x32_bf16 v[100:103], a[236:239], a[188:191], v[100:103]\n"
"s_barrier\n"
"v_mfma_f32_16x16x32_bf16 v[104:107], a[240:243], a[184:187], v[104:107]\n"
"ds_read_b128 a[204:207], v198 offset:2048\n"
"v_cvt_pk_bf16_f32 v129, v130, v131\n"
"v_mfma_f32_16x16x32_bf16 v[104:107], a[244:247], a[188:191], v[104:107]\n"
"s_mov_b32 s74, 0x41400000\n"
"v_pk_fma_f32 v[144:145], v[144:145], s[70:71], s[70:71] op_sel:[0,0,1] op_sel_hi:[1,0,1]\nv_pk_fma_f32 v[146:147], v[146:147], s[70:71], s[70:71] op_sel:[0,0,1] op_sel_hi:[1,0,1]\nv_pk_fma_f32 v[148:149], v[148:149], s[70:71], s[70:71] op_sel:[0,0,1] op_sel_hi:[1,0,1]\nv_pk_fma_f32 v[150:151], v[150:151], s[70:71], s[70:71] op_sel:[0,0,1] op_sel_hi:[1,0,1]\nv_pk_fma_f32 v[152:153], v[152:153], s[70:71], s[70:71] op_sel:[0,0,1] op_sel_hi:[1,0,1]\nv_pk_fma_f32 v[154:155], v[154:155], s[70:71], s[70:71] op_sel:[0,0,1] op_sel_hi:[1,0,1]\nv_pk_fma_f32 v[156:157], v[156:157], s[70:71], s[70:71] op_sel:[0,0,1] op_sel_hi:[1,0,1]\nv_pk_fma_f32 v[158:159], v[158:159], s[70:71], s[70:71] op_sel:[0,0,1] op_sel_hi:[1,0,1]\n"
"v_mfma_f32_16x16x32_bf16 v[108:111], a[248:251], a[184:187], v[108:111]\n"
"v_cvt_pk_bf16_f32 v131, v134, v135\n"
"v_cvt_pk_bf16_f32 v139, v142, v143\n"
"v_mfma_f32_16x16x32_bf16 v[108:111], a[252:255], a[188:191], v[108:111]\n"
"v_permlane16_swap_b32 v129, v131\n"
"v_mfma_f32_16x16x32_bf16 v[92:95], a[248:251], a[176:179], v[92:95]\n"
"v_permlane16_swap_b32 v169, v171\n"
"v_mfma_f32_16x16x32_bf16 v[92:95], a[252:255], a[180:183], v[92:95]\n"
"ds_write2st64_b64 v255, v[192:193], v[194:195] offset0:100 offset1:101\n"
"v_mfma_f32_16x16x32_bf16 v[88:91], a[240:243], a[176:179], v[88:91]\n"
"v_permlane16_swap_b32 v160, v162\n"
"v_mfma_f32_16x16x32_bf16 v[88:91], a[244:247], a[180:183], v[88:91]\n"
"v_permlane16_swap_b32 v161, v163\n"
"v_mfma_f32_16x16x32_bf16 v[84:87], a[232:235], a[176:179], v[84:87]\n"
"v_cvt_pk_bf16_f32 v152, v152, v153\n"
"v_mov_b32 v204, 0\n"
"v_mfma_f32_16x16x32_bf16 v[84:87], a[236:239], a[180:183], v[84:87]\n"
"ds_write2st64_b64 v255, v[176:177], v[178:179] offset0:96 offset1:97\n"
"v_mfma_f32_16x16x32_bf16 v[80:83], a[224:227], a[176:179], v[80:83]\n"
"s_add_u32 s40, s56, s52\n"
"v_cvt_pk_bf16_f32 v218, v220, v221\n"
"v_cvt_pk_bf16_f32 v144, v144, v145\n"
"v_mfma_f32_16x16x32_bf16 v[80:83], a[228:231], a[180:183], v[80:83]\n"
"buffer_load_dwordx4 v240, s[16:19], s40 offen offset:1024 nt lds\n"
"v_mfma_f32_16x16x32_bf16 v[64:67], a[224:227], a[168:171], v[64:67]\n"
"v_cvt_pk_bf16_f32 v219, v222, v223\n"
"v_cvt_pk_bf16_f32 v145, v146, v147\n"
"v_mfma_f32_16x16x32_bf16 v[64:67], a[228:231], a[172:175], v[64:67]\n"
"v_cvt_pk_bf16_f32 v153, v154, v155\n"
"v_lshl_add_u32 v248, v249, 9, v248\n"
"v_mfma_f32_16x16x32_bf16 v[68:71], a[232:235], a[168:171], v[68:71]\n"
"v_permlane16_swap_b32 v137, v139\n"
"v_mfma_f32_16x16x32_bf16 v[68:71], a[236:239], a[172:175], v[68:71]\n"
"v_pk_fma_f32 v[96:97], v[96:97], s[70:71], s[70:71] op_sel:[0,0,1] op_sel_hi:[1,0,1]\nv_pk_fma_f32 v[98:99], v[98:99], s[70:71], s[70:71] op_sel:[0,0,1] op_sel_hi:[1,0,1]\nv_pk_fma_f32 v[100:101], v[100:101], s[70:71], s[70:71] op_sel:[0,0,1] op_sel_hi:[1,0,1]\nv_pk_fma_f32 v[102:103], v[102:103], s[70:71], s[70:71] op_sel:[0,0,1] op_sel_hi:[1,0,1]\nv_pk_fma_f32 v[104:105], v[104:105], s[70:71], s[70:71] op_sel:[0,0,1] op_sel_hi:[1,0,1]\nv_pk_fma_f32 v[106:107], v[106:107], s[70:71], s[70:71] op_sel:[0,0,1] op_sel_hi:[1,0,1]\nv_pk_fma_f32 v[108:109], v[108:109], s[70:71], s[70:71] op_sel:[0,0,1] op_sel_hi:[1,0,1]\nv_pk_fma_f32 v[110:111], v[110:111], s[70:71], s[70:71] op_sel:[0,0,1] op_sel_hi:[1,0,1]\nv_pk_fma_f32 v[80:81], v[80:81], s[70:71], s[70:71] op_sel:[0,0,1] op_sel_hi:[1,0,1]\nv_pk_fma_f32 v[82:83], v[82:83], s[70:71], s[70:71] op_sel:[0,0,1] op_sel_hi:[1,0,1]\nv_pk_fma_f32 v[84:85], v[84:85], s[70:71], s[70:71] op_sel:[0,0,1] op_sel_hi:[1,0,1]\nv_pk_fma_f32 v[86:87], v[86:87], s[70:71], s[70:71] op_sel:[0,0,1] op_sel_hi:[1,0,1]\nv_pk_fma_f32 v[88:89], v[88:89], s[70:71], s[70:71] op_sel:[0,0,1] op_sel_hi:[1,0,1]\nv_pk_fma_f32 v[90:91], v[90:91], s[70:71], s[70:71] op_sel:[0,0,1] op_sel_hi:[1,0,1]\nv_pk_fma_f32 v[92:93], v[92:93], s[70:71], s[70:71] op_sel:[0,0,1] op_sel_hi:[1,0,1]\nv_pk_fma_f32 v[94:95], v[94:95], s[70:71], s[70:71] op_sel:[0,0,1] op_sel_hi:[1,0,1]\n"
"v_mfma_f32_16x16x32_bf16 v[72:75], a[240:243], a[168:171], v[72:75]\n"
"v_cvt_pk_bf16_f32 v130, v132, v133\n"
"v_accvgpr_write_b32 a123, v139\n"
"v_mfma_f32_16x16x32_bf16 v[72:75], a[244:247], a[172:175], v[72:75]\n"
"ds_write2st64_b64 v255, v[184:185], v[186:187] offset0:98 offset1:99\n"
"v_mfma_f32_16x16x32_bf16 v[76:79], a[248:251], a[168:171], v[76:79]\n"
"v_cvt_pk_bf16_f32 v96, v96, v97\n"
"v_cvt_pk_bf16_f32 v104, v104, v105\n"
"v_mfma_f32_16x16x32_bf16 v[76:79], a[252:255], a[172:175], v[76:79]\n"
"v_cvt_pk_bf16_f32 v146, v148, v149\n"
"v_cvt_pk_bf16_f32 v80, v80, v81\n"
"v_mfma_f32_16x16x32_bf16 v[60:63], a[248:251], a[160:163], v[60:63]\n"
"v_cvt_pk_bf16_f32 v203, v206, v207\n"
"v_cvt_pk_bf16_f32 v155, v158, v159\n"
"v_mfma_f32_16x16x32_bf16 v[60:63], a[252:255], a[164:167], v[60:63]\n"
"v_permlane16_swap_b32 v128, v130\n"
"v_mfma_f32_16x16x32_bf16 v[56:59], a[240:243], a[160:163], v[56:59]\n"
"s_add_u32 s40, s56, s53\n"
"ds_read_b128 a[192:195], v243 offset:0\n"
"v_accvgpr_write_b32 a112, v128\n"
"v_mfma_f32_16x16x32_bf16 v[56:59], a[244:247], a[164:167], v[56:59]\n"
"buffer_load_dwordx4 v240, s[16:19], s40 offen offset:2048 nt lds\n"
"v_mfma_f32_16x16x32_bf16 v[52:55], a[232:235], a[160:163], v[52:55]\n"
"v_cvt_pk_bf16_f32 v147, v150, v151\n"
"v_cvt_pk_bf16_f32 v81, v82, v83\n"
"v_mfma_f32_16x16x32_bf16 v[52:55], a[236:239], a[164:167], v[52:55]\n"
"v_cvt_pk_bf16_f32 v210, v212, v213\n"
"v_cvt_pk_bf16_f32 v97, v98, v99\n"
"v_mfma_f32_16x16x32_bf16 v[48:51], a[224:227], a[160:163], v[48:51]\n"
"v_permlane16_swap_b32 v145, v147\n"
"v_mfma_f32_16x16x32_bf16 v[48:51], a[228:231], a[164:167], v[48:51]\n"
"v_accvgpr_write_b32 a115, v131\n"
"v_cvt_pk_bf16_f32 v154, v156, v157\n"
"v_mfma_f32_16x16x32_bf16 v[0:3], a[224:227], a[136:139], v[0:3]\n"
"ds_read_b128 v[212:215], v197 offset:0\n"
"v_cvt_pk_bf16_f32 v105, v106, v107\n"
"v_mfma_f32_16x16x32_bf16 v[0:3], a[228:231], a[140:143], v[0:3]\n"
"ds_write2st64_b64 v255, v[216:217], v[218:219] offset0:194 offset1:195\n"
"v_mfma_f32_16x16x32_bf16 v[4:7], a[232:235], a[136:139], v[4:7]\n"
"v_cvt_pk_bf16_f32 v121, v122, v123\n"
"v_cvt_pk_bf16_f32 v99, v102, v103\n"
"v_mfma_f32_16x16x32_bf16 v[4:7], a[236:239], a[140:143], v[4:7]\n"
"v_cvt_pk_bf16_f32 v122, v124, v125\n"
"v_cvt_pk_bf16_f32 v98, v100, v101\n"
"v_mfma_f32_16x16x32_bf16 v[8:11], a[240:243], a[136:139], v[8:11]\n"
"v_cvt_pk_bf16_f32 v88, v88, v89\n"
"v_xor_b32_e64 v249, s42, 0\n"
"v_mfma_f32_16x16x32_bf16 v[8:11], a[244:247], a[140:143], v[8:11]\n"
"ds_write2st64_b64 v255, v[208:209], v[210:211] offset0:192 offset1:193\n"
"v_mfma_f32_16x16x32_bf16 v[12:15], a[248:251], a[136:139], v[12:15]\n"
"v_permlane16_swap_b32 v144, v146\n"
"v_mfma_f32_16x16x32_bf16 v[12:15], a[252:255], a[140:143], v[12:15]\n"
"v_pk_fma_f32 v[64:65], v[64:65], s[70:71], s[70:71] op_sel:[0,0,1] op_sel_hi:[1,0,1]\nv_pk_fma_f32 v[66:67], v[66:67], s[70:71], s[70:71] op_sel:[0,0,1] op_sel_hi:[1,0,1]\nv_pk_fma_f32 v[68:69], v[68:69], s[70:71], s[70:71] op_sel:[0,0,1] op_sel_hi:[1,0,1]\nv_pk_fma_f32 v[70:71], v[70:71], s[70:71], s[70:71] op_sel:[0,0,1] op_sel_hi:[1,0,1]\nv_pk_fma_f32 v[72:73], v[72:73], s[70:71], s[70:71] op_sel:[0,0,1] op_sel_hi:[1,0,1]\nv_pk_fma_f32 v[74:75], v[74:75], s[70:71], s[70:71] op_sel:[0,0,1] op_sel_hi:[1,0,1]\nv_pk_fma_f32 v[76:77], v[76:77], s[70:71], s[70:71] op_sel:[0,0,1] op_sel_hi:[1,0,1]\nv_pk_fma_f32 v[78:79], v[78:79], s[70:71], s[70:71] op_sel:[0,0,1] op_sel_hi:[1,0,1]\nv_pk_fma_f32 v[48:49], v[48:49], s[70:71], s[70:71] op_sel:[0,0,1] op_sel_hi:[1,0,1]\nv_pk_fma_f32 v[50:51], v[50:51], s[70:71], s[70:71] op_sel:[0,0,1] op_sel_hi:[1,0,1]\nv_pk_fma_f32 v[52:53], v[52:53], s[70:71], s[70:71] op_sel:[0,0,1] op_sel_hi:[1,0,1]\nv_pk_fma_f32 v[54:55], v[54:55], s[70:71], s[70:71] op_sel:[0,0,1] op_sel_hi:[1,0,1]\nv_pk_fma_f32 v[56:57], v[56:57], s[70:71], s[70:71] op_sel:[0,0,1] op_sel_hi:[1,0,1]\nv_pk_fma_f32 v[58:59], v[58:59], s[70:71], s[70:71] op_sel:[0,0,1] op_sel_hi:[1,0,1]\nv_pk_fma_f32 v[60:61], v[60:61], s[70:71], s[70:71] op_sel:[0,0,1] op_sel_hi:[1,0,1]\nv_pk_fma_f32 v[62:63], v[62:63], s[70:71], s[70:71] op_sel:[0,0,1] op_sel_hi:[1,0,1]\n"
"v_mfma_f32_16x16x32_bf16 v[28:31], a[248:251], a[144:147], v[28:31]\n"
"s_add_u32 s40, s56, s54\n"
"ds_read_b128 a[200:203], v243 offset:2048\n"
"v_cvt_pk_bf16_f32 v48, v48, v49\n"
"v_mfma_f32_16x16x32_bf16 v[28:31], a[252:255], a[148:151], v[28:31]\n"
"buffer_load_dwordx4 v240, s[16:19], s40 offen offset:3072 nt lds\n"
"v_mfma_f32_16x16x32_bf16 v[24:27], a[240:243], a[144:147], v[24:27]\n"
"v_permlane16_swap_b32 v168, v170\n"
"v_mfma_f32_16x16x32_bf16 v[24:27], a[244:247], a[148:151], v[24:27]\n"
"v_permlane16_swap_b32 v152, v154\n"
"v_mfma_f32_16x16x32_bf16 v[20:23], a[232:235], a[144:147], v[20:23]\n"
"v_accvgpr_write_b32 a130, v146\n"
"v_cvt_pk_bf16_f32 v106, v108, v109\n"
"v_mfma_f32_16x16x32_bf16 v[20:23], a[236:239], a[148:151], v[20:23]\n"
"ds_read_b128 v[208:211], v196 offset:0\n"
"v_cvt_pk_bf16_f32 v89, v90, v91\n"
"v_mfma_f32_16x16x32_bf16 v[16:19], a[224:227], a[144:147], v[16:19]\n"
"ds_read_b128 v[220:223], v197 offset:1024\n"
"v_cvt_pk_bf16_f32 v138, v140, v141\n"
"v_mfma_f32_16x16x32_bf16 v[16:19], a[228:231], a[148:151], v[16:19]\n"
"v_cvt_pk_bf16_f32 v107, v110, v111\n"
"v_cvt_pk_bf16_f32 v56, v56, v57\n"
"v_mfma_f32_16x16x32_bf16 v[32:35], a[224:227], a[152:155], v[32:35]\n"
"ds_read_b128 v[216:219], v196 offset:1024\n"
"v_cvt_pk_bf16_f32 v72, v72, v73\n"
"v_mfma_f32_16x16x32_bf16 v[32:35], a[228:231], a[156:159], v[32:35]\n"
"v_permlane16_swap_b32 v153, v155\n"
"v_mfma_f32_16x16x32_bf16 v[36:39], a[232:235], a[152:155], v[36:39]\n"
"v_accvgpr_write_b32 a128, v144\n"
"v_cvt_pk_bf16_f32 v64, v64, v65\n"
"v_mfma_f32_16x16x32_bf16 v[36:39], a[236:239], a[156:159], v[36:39]\n"
"ds_write2st64_b64 v255, v[200:201], v[202:203] offset0:102 offset1:103\n"
"v_cvt_pk_bf16_f32 v123, v126, v127\n"
"v_permlane16_swap_b32 v136, v138\n"
"v_mfma_f32_16x16x32_bf16 v[40:43], a[240:243], a[152:155], v[40:43]\n"
"v_cvt_pk_bf16_f32 v49, v50, v51\n"
"v_accvgpr_write_b32 a242, v254\n"
"v_mfma_f32_16x16x32_bf16 v[40:43], a[244:247], a[156:159], v[40:43]\n"
"v_pk_fma_f32 v[0:1], v[0:1], s[70:71], s[70:71] op_sel:[0,0,1] op_sel_hi:[1,0,1]\nv_pk_fma_f32 v[2:3], v[2:3], s[70:71], s[70:71] op_sel:[0,0,1] op_sel_hi:[1,0,1]\nv_pk_fma_f32 v[4:5], v[4:5], s[70:71], s[70:71] op_sel:[0,0,1] op_sel_hi:[1,0,1]\nv_pk_fma_f32 v[6:7], v[6:7], s[70:71], s[70:71] op_sel:[0,0,1] op_sel_hi:[1,0,1]\nv_pk_fma_f32 v[8:9], v[8:9], s[70:71], s[70:71] op_sel:[0,0,1] op_sel_hi:[1,0,1]\nv_pk_fma_f32 v[10:11], v[10:11], s[70:71], s[70:71] op_sel:[0,0,1] op_sel_hi:[1,0,1]\nv_pk_fma_f32 v[12:13], v[12:13], s[70:71], s[70:71] op_sel:[0,0,1] op_sel_hi:[1,0,1]\nv_pk_fma_f32 v[14:15], v[14:15], s[70:71], s[70:71] op_sel:[0,0,1] op_sel_hi:[1,0,1]\nv_pk_fma_f32 v[16:17], v[16:17], s[70:71], s[70:71] op_sel:[0,0,1] op_sel_hi:[1,0,1]\nv_pk_fma_f32 v[18:19], v[18:19], s[70:71], s[70:71] op_sel:[0,0,1] op_sel_hi:[1,0,1]\nv_pk_fma_f32 v[20:21], v[20:21], s[70:71], s[70:71] op_sel:[0,0,1] op_sel_hi:[1,0,1]\nv_pk_fma_f32 v[22:23], v[22:23], s[70:71], s[70:71] op_sel:[0,0,1] op_sel_hi:[1,0,1]\nv_pk_fma_f32 v[24:25], v[24:25], s[70:71], s[70:71] op_sel:[0,0,1] op_sel_hi:[1,0,1]\nv_pk_fma_f32 v[26:27], v[26:27], s[70:71], s[70:71] op_sel:[0,0,1] op_sel_hi:[1,0,1]\nv_pk_fma_f32 v[28:29], v[28:29], s[70:71], s[70:71] op_sel:[0,0,1] op_sel_hi:[1,0,1]\nv_pk_fma_f32 v[30:31], v[30:31], s[70:71], s[70:71] op_sel:[0,0,1] op_sel_hi:[1,0,1]\n"
"v_mfma_f32_16x16x32_bf16 v[44:47], a[248:251], a[152:155], v[44:47]\n"
"ds_read_b128 a[196:199], v198 offset:0\n"
"v_cvt_pk_bf16_f32 v73, v74, v75\n"
"v_mfma_f32_16x16x32_bf16 v[44:47], a[252:255], a[156:159], v[44:47]\n"
"s_waitcnt lgkmcnt(3)\n"
"v_accvgpr_write_b32 a131, v147\n"
"v_cvt_pk_bf16_f32 v57, v58, v59\n"
"v_mfma_f32_16x16x32_bf16 v[180:183], v[208:211], a[0:3], 0\n"
"v_cvt_pk_bf16_f32 v50, v52, v53\n"
"v_cvt_pk_bf16_f32 v8, v8, v9\n"
"v_mfma_f32_16x16x32_bf16 v[180:183], v[212:215], a[4:7], v[180:183]\n"
"s_waitcnt lgkmcnt(2)\n"
"v_accvgpr_write_b32 a137, v153\n"
"v_cvt_pk_bf16_f32 v91, v94, v95\n"
"v_mfma_f32_16x16x32_bf16 v[180:183], v[216:219], a[8:11], v[180:183]\n"
"v_accvgpr_write_b32 a113, v129\n"
"v_cvt_pk_bf16_f32 v65, v66, v67\n"
"v_mfma_f32_16x16x32_bf16 v[180:183], v[220:223], a[12:15], v[180:183]\n"
"ds_write_b128 v243, v[160:163] offset:8192\n"
"v_cvt_pk_bf16_f32 v90, v92, v93\n"
"v_cvt_pk_bf16_f32 v66, v68, v69\n"
"v_mfma_f32_16x16x32_bf16 v[184:187], v[208:211], a[16:19], 0\n"
"v_cvt_pk_bf16_f32 v112, v112, v113\n"
"v_accvgpr_write_b32 a139, v155\n"
"v_mfma_f32_16x16x32_bf16 v[184:187], v[212:215], a[20:23], v[184:187]\n"
"v_cvt_pk_bf16_f32 v83, v86, v87\n"
"v_cvt_pk_bf16_f32 v9, v10, v11\n"
"v_mfma_f32_16x16x32_bf16 v[184:187], v[216:219], a[24:27], v[184:187]\n"
"v_cvt_pk_bf16_f32 v10, v12, v13\n"
"v_lshl_add_u32 v248, v249, 12, v248\n"
"v_mfma_f32_16x16x32_bf16 v[184:187], v[220:223], a[28:31], v[184:187]\n"
"v_pk_fma_f32 v[32:33], v[32:33], s[70:71], s[70:71] op_sel:[0,0,1] op_sel_hi:[1,0,1]\nv_pk_fma_f32 v[34:35], v[34:35], s[70:71], s[70:71] op_sel:[0,0,1] op_sel_hi:[1,0,1]\nv_pk_fma_f32 v[36:37], v[36:37], s[70:71], s[70:71] op_sel:[0,0,1] op_sel_hi:[1,0,1]\nv_pk_fma_f32 v[38:39], v[38:39], s[70:71], s[70:71] op_sel:[0,0,1] op_sel_hi:[1,0,1]\nv_pk_fma_f32 v[40:41], v[40:41], s[70:71], s[70:71] op_sel:[0,0,1] op_sel_hi:[1,0,1]\nv_pk_fma_f32 v[42:43], v[42:43], s[70:71], s[70:71] op_sel:[0,0,1] op_sel_hi:[1,0,1]\nv_pk_fma_f32 v[44:45], v[44:45], s[70:71], s[70:71] op_sel:[0,0,1] op_sel_hi:[1,0,1]\nv_pk_fma_f32 v[46:47], v[46:47], s[70:71], s[70:71] op_sel:[0,0,1] op_sel_hi:[1,0,1]\n"
"v_mfma_f32_16x16x32_bf16 v[188:191], v[208:211], a[32:35], 0\n"
"v_accvgpr_write_b32 a138, v154\n"
"v_sub_f32 v180, s74, v180\n"
"v_mfma_f32_16x16x32_bf16 v[188:191], v[212:215], a[36:39], v[188:191]\n"
"v_sub_f32 v181, s74, v181\n"
"v_sub_f32 v183, s74, v183\n"
"v_mfma_f32_16x16x32_bf16 v[188:191], v[216:219], a[40:43], v[188:191]\n"
"v_cvt_pk_bf16_f32 v67, v70, v71\n"
"v_sub_f32 v182, s74, v182\n"
"v_mfma_f32_16x16x32_bf16 v[188:191], v[220:223], a[44:47], v[188:191]\n"
"v_accvgpr_write_b32 a114, v130\n"
"v_accvgpr_write_b32 a136, v152\n"
"v_mfma_f32_16x16x32_bf16 v[192:195], v[208:211], a[192:195], 0\n"
"s_waitcnt lgkmcnt(0)\n"
"ds_read_b128 v[208:211], v228 offset:0\n"
"v_sub_f32 v186, s74, v186\n"
"v_mfma_f32_16x16x32_bf16 v[192:195], v[212:215], a[196:199], v[192:195]\n"
"ds_read_b128 v[212:215], v229 offset:0\n"
"v_sub_f32 v187, s74, v187\n"
"v_mfma_f32_16x16x32_bf16 v[192:195], v[216:219], a[200:203], v[192:195]\n"
"ds_read_b128 v[216:219], v228 offset:1024\n"
"v_sub_f32 v184, s74, v184\n"
"v_mfma_f32_16x16x32_bf16 v[192:195], v[220:223], a[204:207], v[192:195]\n"
"s_waitcnt lgkmcnt(2)\n"
"ds_read_b128 v[220:223], v229 offset:1024\n"
"v_cvt_pk_bf16_f32 v82, v84, v85\n"
"v_cvt_pk_bf16_f32 v58, v60, v61\n"
"v_permlane16_swap_b32 v8, v10\n"
"v_cvt_pk_bf16_f32 v16, v16, v17\n"
"v_cvt_pk_bf16_f32 v24, v24, v25\n"
"v_mov_b32 v196, 0\n"
"v_sub_f32 v185, s74, v185\n"
"v_sub_f32 v190, s74, v190\n"
"v_mfma_f32_16x16x32_bf16 v[152:155], v[208:211], a[0:3], v[180:183]\n"
"s_waitcnt lgkmcnt(2)\n"
"v_cvt_pk_bf16_f32 v59, v62, v63\n"
"v_sub_f32 v191, s74, v191\n"
"v_mfma_f32_16x16x32_bf16 v[152:155], v[212:215], a[4:7], v[152:155]\n"
"s_waitcnt lgkmcnt(1)\n"
"v_permlane16_swap_b32 v57, v59\n"
"v_mfma_f32_16x16x32_bf16 v[152:155], v[216:219], a[8:11], v[152:155]\n"
"s_waitcnt lgkmcnt(0)\n"
"v_cvt_pk_bf16_f32 v11, v14, v15\n"
"v_sub_f32 v193, s74, v193\n"
"v_mfma_f32_16x16x32_bf16 v[152:155], v[220:223], a[12:15], v[152:155]\n"
"ds_write_b128 v243, v[168:171] offset:10240\n"
"v_accvgpr_write_b32 a122, v138\n"
"v_permlane16_swap_b32 v9, v11\n"
"v_cvt_pk_bf16_f32 v40, v40, v41\n"
"v_sub_f32 v189, s74, v189\n"
"v_mfma_f32_16x16x32_bf16 v[156:159], v[216:219], a[24:27], v[184:187]\n"
"v_accvgpr_write_b32 a121, v137\n"
"v_sub_f32 v188, s74, v188\n"
"v_mfma_f32_16x16x32_bf16 v[156:159], v[220:223], a[28:31], v[156:159]\n"
"v_mov_b32 v199, 0\n"
"v_sub_f32 v194, s74, v194\n"
"v_mfma_f32_16x16x32_bf16 v[156:159], v[208:211], a[16:19], v[156:159]\n"
"v_permlane16_swap_b32 v56, v58\n"
"v_mfma_f32_16x16x32_bf16 v[156:159], v[212:215], a[20:23], v[156:159]\n"
"v_exp_f32 v154, v154\n"
"v_mfma_f32_16x16x32_bf16 v[160:163], v[208:211], a[32:35], v[188:191]\n"
"v_exp_f32 v153, v153\n"
"v_mfma_f32_16x16x32_bf16 v[160:163], v[212:215], a[36:39], v[160:163]\n"
"v_cvt_pk_bf16_f32 v113, v114, v115\n"
"v_accvgpr_write_b32 a129, v145\n"
"v_mfma_f32_16x16x32_bf16 v[160:163], v[216:219], a[40:43], v[160:163]\n"
"v_sub_f32 v192, s74, v192\n"
"v_sub_f32 v195, s74, v195\n"
"v_mfma_f32_16x16x32_bf16 v[160:163], v[220:223], a[44:47], v[160:163]\n"
"ds_write2st64_b64 v255, v[88:89], v[90:91] offset0:226 offset1:227\n"
"v_permlane16_swap_b32 v48, v50\n"
"v_cvt_pk_bf16_f32 v41, v42, v43\n"
"v_exp_f32 v152, v152\n"
"v_mfma_f32_16x16x32_bf16 v[164:167], v[216:219], a[200:203], v[192:195]\n"
"ds_read_b128 v[216:219], v228 offset:5120\n"
"v_cvt_pk_bf16_f32 v51, v54, v55\n"
"v_mfma_f32_16x16x32_bf16 v[164:167], v[220:223], a[204:207], v[164:167]\n"
"ds_read_b128 v[220:223], v229 offset:5120\n"
"v_accvgpr_write_b32 a241, v254\n"
"v_mfma_f32_16x16x32_bf16 v[164:167], v[208:211], a[192:195], v[164:167]\n"
"ds_read_b128 v[208:211], v228 offset:4096\n"
"v_cvt_pk_bf16_f32 v42, v44, v45\n"
"v_mfma_f32_16x16x32_bf16 v[164:167], v[212:215], a[196:199], v[164:167]\n"
"s_waitcnt lgkmcnt(2)\n"
"ds_read_b128 v[212:215], v229 offset:4096\n"
"v_cvt_pk_bf16_f32 v32, v32, v33\n"
"v_exp_f32 v155, v155\n"
"v_exp_f32 v156, v156\n"
"v_exp_f32 v157, v157\n"
"v_exp_f32 v160, v160\n"
"v_mfma_f32_16x16x32_bf16 v[168:171], v[216:219], a[8:11], v[180:183]\n"
"s_waitcnt lgkmcnt(2)\n"
"v_permlane16_swap_b32 v40, v42\n"
"v_mfma_f32_16x16x32_bf16 v[168:171], v[220:223], a[12:15], v[168:171]\n"
"s_waitcnt lgkmcnt(1)\n"
"v_accvgpr_write_b32 a120, v136\n"
"v_accvgpr_write_b32 a152, v8\n"
"v_mfma_f32_16x16x32_bf16 v[168:171], v[208:211], a[0:3], v[168:171]\n"
"s_waitcnt lgkmcnt(0)\n"
"v_exp_f32 v162, v162\n"
"v_mfma_f32_16x16x32_bf16 v[168:171], v[212:215], a[4:7], v[168:171]\n"
"ds_write2st64_b64 v255, v[80:81], v[82:83] offset0:224 offset1:225\n"
"v_cvt_pk_bf16_f32 v74, v76, v77\n"
"v_cvt_pk_bf16_f32 v129, v154, v155\n"
"v_exp_f32 v158, v158\n"
"v_mfma_f32_16x16x32_bf16 v[172:175], v[208:211], a[16:19], v[184:187]\n"
"v_cvt_pk_bf16_f32 v136, v156, v157\n"
"v_add_u32 v249, s75, v248\n"
"v_mfma_f32_16x16x32_bf16 v[172:175], v[212:215], a[20:23], v[172:175]\n"
"v_mov_b32 v197, 0\n"
"v_cvt_pk_bf16_f32 v128, v152, v153\n"
"v_mfma_f32_16x16x32_bf16 v[172:175], v[216:219], a[24:27], v[172:175]\n"
"v_exp_f32 v167, v167\n"
"v_mfma_f32_16x16x32_bf16 v[172:175], v[220:223], a[28:31], v[172:175]\n"
"ds_write2st64_b64 v255, v[96:97], v[98:99] offset0:228 offset1:229\n"
"v_cvt_pk_bf16_f32 v75, v78, v79\n"
"v_exp_f32 v168, v168\n"
"v_exp_f32 v169, v169\n"
"v_mfma_f32_16x16x32_bf16 v[176:179], v[216:219], a[40:43], v[188:191]\n"
"v_exp_f32 v166, v166\n"
"v_mfma_f32_16x16x32_bf16 v[176:179], v[220:223], a[44:47], v[176:179]\n"
"v_permlane16_swap_b32 v120, v122\n"
"v_mfma_f32_16x16x32_bf16 v[176:179], v[208:211], a[32:35], v[176:179]\n"
"v_exp_f32 v159, v159\n"
"v_mfma_f32_16x16x32_bf16 v[176:179], v[212:215], a[36:39], v[176:179]\n"
"v_permlane16_swap_b32 v49, v51\n"
"v_exp_f32 v161, v161\n"
"v_exp_f32 v163, v163\n"
"v_exp_f32 v165, v165\n"
"v_exp_f32 v174, v174\n"
"v_mfma_f32_16x16x32_bf16 v[152:155], v[208:211], a[192:195], v[192:195]\n"
"ds_read_b128 v[208:211], v230 offset:0\n"
"v_cvt_pk_bf16_f32 v145, v162, v163\n"
"v_mfma_f32_16x16x32_bf16 v[152:155], v[212:215], a[196:199], v[152:155]\n"
"ds_read_b128 v[212:215], v231 offset:0\n"
"v_cvt_pk_bf16_f32 v144, v160, v161\n"
"v_mfma_f32_16x16x32_bf16 v[152:155], v[216:219], a[200:203], v[152:155]\n"
"ds_read_b128 v[216:219], v230 offset:1024\n"
"v_cvt_pk_bf16_f32 v43, v46, v47\n"
"v_mfma_f32_16x16x32_bf16 v[152:155], v[220:223], a[204:207], v[152:155]\n"
"s_waitcnt lgkmcnt(2)\n"
"ds_read_b128 v[220:223], v231 offset:1024\n"
"v_cvt_pk_bf16_f32 v137, v158, v159\n"
"v_exp_f32 v164, v164\n"
"v_exp_f32 v170, v170\n"
"v_exp_f32 v171, v171\n"
"v_exp_f32 v173, v173\n"
"v_mfma_f32_16x16x32_bf16 v[156:159], v[208:211], a[0:3], v[180:183]\n"
"s_waitcnt lgkmcnt(2)\n"
"v_cvt_pk_bf16_f32 v125, v166, v167\n"
"v_cvt_pk_bf16_f32 v131, v170, v171\n"
"v_mfma_f32_16x16x32_bf16 v[156:159], v[212:215], a[4:7], v[156:159]\n"
"s_waitcnt lgkmcnt(1)\n"
"v_exp_f32 v176, v176\n"
"v_mfma_f32_16x16x32_bf16 v[156:159], v[216:219], a[8:11], v[156:159]\n"
"s_waitcnt lgkmcnt(0)\n"
"v_exp_f32 v172, v172\n"
"v_mfma_f32_16x16x32_bf16 v[156:159], v[220:223], a[12:15], v[156:159]\n"
"ds_write_b128 v243, v[48:51] offset:12288\n"
"v_accvgpr_write_b32 a186, v42\n"
"v_cvt_pk_bf16_f32 v130, v168, v169\n"
"v_exp_f32 v175, v175\n"
"v_mfma_f32_16x16x32_bf16 v[160:163], v[216:219], a[24:27], v[184:187]\n"
"v_permlane16_swap_b32 v41, v43\n"
"v_mfma_f32_16x16x32_bf16 v[160:163], v[220:223], a[28:31], v[160:163]\n"
"v_exp_f32 v155, v155\n"
"v_mfma_f32_16x16x32_bf16 v[160:163], v[208:211], a[16:19], v[160:163]\n"
"v_accvgpr_write_b32 a184, v40\n"
"v_cvt_pk_bf16_f32 v138, v172, v173\n"
"v_mfma_f32_16x16x32_bf16 v[160:163], v[212:215], a[20:23], v[160:163]\n"
"ds_write2st64_b64 v255, v[64:65], v[66:67] offset0:196 offset1:197\n"
"v_cvt_pk_bf16_f32 v124, v164, v165\n"
"v_exp_f32 v177, v177\n"
"v_exp_f32 v178, v178\n"
"v_mfma_f32_16x16x32_bf16 v[164:167], v[208:211], a[32:35], v[188:191]\n"
"ds_read_b128 v[208:211], v230 offset:4096\n"
"v_mov_b32 v198, 0\n"
"v_mfma_f32_16x16x32_bf16 v[164:167], v[212:215], a[36:39], v[164:167]\n"
"ds_read_b128 v[212:215], v231 offset:4096\n"
"v_cvt_pk_bf16_f32 v25, v26, v27\n"
"v_mfma_f32_16x16x32_bf16 v[164:167], v[216:219], a[40:43], v[164:167]\n"
"ds_read_b128 v[216:219], v230 offset:5120\n"
"v_accvgpr_write_b32 a187, v43\n"
"v_mfma_f32_16x16x32_bf16 v[164:167], v[220:223], a[44:47], v[164:167]\n"
"s_waitcnt lgkmcnt(2)\n"
"ds_read_b128 v[220:223], v231 offset:5120\n"
"v_mov_b32 v202, 0\n"
"v_exp_f32 v179, v179\n"
"v_exp_f32 v152, v152\n"
"v_exp_f32 v154, v154\n"
"v_exp_f32 v158, v158\n"
"v_mfma_f32_16x16x32_bf16 v[168:171], v[208:211], a[0:3], v[180:183]\n"
"s_waitcnt lgkmcnt(2)\n"
"v_exp_f32 v156, v156\n"
"v_mfma_f32_16x16x32_bf16 v[168:171], v[212:215], a[4:7], v[168:171]\n"
"s_waitcnt lgkmcnt(1)\n"
"v_exp_f32 v157, v157\n"
"v_mfma_f32_16x16x32_bf16 v[168:171], v[216:219], a[8:11], v[168:171]\n"
"s_waitcnt lgkmcnt(0)\n"
"v_exp_f32 v160, v160\n"
"v_mfma_f32_16x16x32_bf16 v[168:171], v[220:223], a[12:15], v[168:171]\n"
"ds_write2st64_b64 v255, v[72:73], v[74:75] offset0:198 offset1:199\n"
"v_cvt_pk_bf16_f32 v139, v174, v175\n"
"v_exp_f32 v153, v153\n"
"v_exp_f32 v163, v163\n"
"v_mfma_f32_16x16x32_bf16 v[172:175], v[216:219], a[24:27], v[184:187]\n"
"v_cvt_pk_bf16_f32 v33, v34, v35\n"
"v_cvt_pk_bf16_f32 v127, v154, v155\n"
"v_mfma_f32_16x16x32_bf16 v[172:175], v[220:223], a[28:31], v[172:175]\n"
"ds_write2st64_b64 v255, v[104:105], v[106:107] offset0:230 offset1:231\n"
"v_accvgpr_write_b32 a104, v120\n"
"v_accvgpr_write_b32 a153, v9\n"
"v_cvt_pk_bf16_f32 v147, v178, v179\n"
"v_exp_f32 v162, v162\n"
"v_mfma_f32_16x16x32_bf16 v[172:175], v[208:211], a[16:19], v[172:175]\n"
"v_exp_f32 v159, v159\n"
"v_mfma_f32_16x16x32_bf16 v[172:175], v[212:215], a[20:23], v[172:175]\n"
"s_cbranch_vccnz LPCE0_%=\nLPKE0_%=:\n"
"ds_write_b128 v243, v[56:59] offset:14336\n"
"v_cvt_pk_bf16_f32 v27, v30, v31\n"
"v_cvt_pk_bf16_f32 v146, v176, v177\n"
"v_exp_f32 v171, v171\n"
"v_mfma_f32_16x16x32_bf16 v[176:179], v[208:211], a[32:35], v[188:191]\n"
"v_exp_f32 v161, v161\n"
"v_mfma_f32_16x16x32_bf16 v[176:179], v[212:215], a[36:39], v[176:179]\n"
"v_accvgpr_write_b32 a185, v41\n"
"v_cvt_pk_bf16_f32 v133, v158, v159\n"
"v_mfma_f32_16x16x32_bf16 v[176:179], v[216:219], a[40:43], v[176:179]\n"
"v_accvgpr_write_b32 a243, v254\n"
"v_cvt_pk_bf16_f32 v140, v160, v161\n"
"v_mfma_f32_16x16x32_bf16 v[176:179], v[220:223], a[44:47], v[176:179]\n"
"v_cvt_pk_bf16_f32 v126, v152, v153\n"
"v_exp_f32 v165, v165\n"
"v_exp_f32 v166, v166\n"
"v_exp_f32 v168, v168\n"
"v_exp_f32 v175, v175\n"
"s_waitcnt vmcnt(8) lgkmcnt(0)\n"
"s_barrier\n"
"ds_read_b128 v[208:211], v228 offset:16384\n"
"ds_read_b128 v[212:215], v229 offset:16384\n"
"ds_read_b128 v[216:219], v228 offset:17408\n"
"ds_read_b128 v[220:223], v229 offset:17408\n"
"s_waitcnt lgkmcnt(3)\n"
"v_mfma_f32_16x16x32_bf16 v[152:155], v[208:211], a[0:3], v[180:183]\n"
"v_exp_f32 v174, v174\n"
"s_waitcnt lgkmcnt(2)\n"
"v_mfma_f32_16x16x32_bf16 v[152:155], v[212:215], a[4:7], v[152:155]\n"
"ds_read_b128 a[188:191], v249 offset:52224\n"
"v_accvgpr_write_b32 a240, v254\n"
"s_waitcnt lgkmcnt(2)\n"
"v_mfma_f32_16x16x32_bf16 v[152:155], v[216:219], a[8:11], v[152:155]\n"
"s_waitcnt lgkmcnt(1)\n"
"ds_read_b128 a[100:103], v248 offset:49152\n"
"v_cvt_pk_bf16_f32 v141, v162, v163\n"
"v_mfma_f32_16x16x32_bf16 v[152:155], v[220:223], a[12:15], v[152:155]\n"
"ds_read_b128 a[164:167], v249 offset:49152\n"
"v_cvt_pk_bf16_f32 v132, v156, v157\n"
"v_exp_f32 v167, v167\n"
"v_exp_f32 v170, v170\n"
"v_exp_f32 v177, v177\n"
"v_exp_f32 v178, v178\n"
"v_mfma_f32_16x16x32_bf16 v[156:159], v[216:219], a[24:27], v[184:187]\n"
"ds_read_b64_tr_b16 a[246:247], v234 offset:4096\n"
"ds_read_b64_tr_b16 v[224:225], v239 offset:0\n"
"v_mfma_f32_16x16x32_bf16 v[156:159], v[220:223], a[28:31], v[156:159]\n"
"ds_read_b64_tr_b16 a[248:249], v238 offset:0\n"
"v_cvt_pk_bf16_f32 v135, v170, v171\n"
"v_mfma_f32_16x16x32_bf16 v[156:159], v[208:211], a[16:19], v[156:159]\n"
"v_exp_f32 v155, v155\n"
"v_mfma_f32_16x16x32_bf16 v[156:159], v[212:215], a[20:23], v[156:159]\n"
"ds_read_b64_tr_b16 a[244:245], v234 offset:0\n"
"ds_read_b64_tr_b16 v[226:227], v239 offset:4096\n"
"v_cvt_pk_bf16_f32 v115, v118, v119\n"
"v_accvgpr_write_b32 a155, v11\n"
"v_permlane16_swap_b32 v25, v27\n"
"v_exp_f32 v164, v164\n"
"v_exp_f32 v176, v176\n"
"v_mfma_f32_16x16x32_bf16 v[160:163], v[208:211], a[32:35], v[188:191]\n"
"v_exp_f32 v172, v172\n"
"v_mfma_f32_16x16x32_bf16 v[160:163], v[212:215], a[36:39], v[160:163]\n"
"v_mov_b32 v203, 0\n"
"v_cvt_pk_bf16_f32 v149, v166, v167\n"
"v_mfma_f32_16x16x32_bf16 v[160:163], v[216:219], a[40:43], v[160:163]\n"
"ds_read_b64_tr_b16 a[250:251], v238 offset:4096\n"
"ds_read_b128 a[156:159], v249 offset:35840\n"
"v_mfma_f32_16x16x32_bf16 v[160:163], v[220:223], a[44:47], v[160:163]\n"
"ds_read_b64_tr_b16 a[252:253], v235 offset:0\n"
"v_accvgpr_write_b32 a154, v10\n"
"v_cvt_pk_bf16_f32 v148, v164, v165\n"
"v_exp_f32 v173, v173\n"
"v_cvt_pk_bf16_f32 v143, v174, v175\n"
"v_exp_f32 v179, v179\n"
"v_exp_f32 v152, v152\n"
"v_mfma_f32_16x16x32_bf16 v[164:167], v[216:219], a[200:203], v[192:195]\n"
"ds_read_b64_tr_b16 a[254:255], v235 offset:4096\n"
"v_cvt_pk_bf16_f32 v151, v178, v179\n"
"v_mfma_f32_16x16x32_bf16 v[164:167], v[220:223], a[204:207], v[164:167]\n"
"ds_read_b128 a[132:135], v249 offset:32768\n"
"v_cvt_pk_bf16_f32 v150, v176, v177\n"
"v_mfma_f32_16x16x32_bf16 v[164:167], v[208:211], a[192:195], v[164:167]\n"
"ds_read_b128 a[116:119], v248 offset:51200\n"
"v_cvt_pk_bf16_f32 v34, v36, v37\n"
"v_mfma_f32_16x16x32_bf16 v[164:167], v[212:215], a[196:199], v[164:167]\n"
"s_waitcnt lgkmcnt(6)\n"
"ds_read_b128 a[108:111], v248 offset:50176\n"
"v_exp_f32 v169, v169\n"
"v_cvt_pk_bf16_f32 v142, v172, v173\n"
"v_exp_f32 v153, v153\n"
"v_exp_f32 v154, v154\n"
"v_exp_f32 v157, v157\n"
"v_mfma_f32_16x16x32_bf16 a[216:219], v[124:127], a[244:247], 0\n"
"ds_read_b128 a[172:175], v249 offset:50176\n"
"v_cvt_pk_bf16_f32 v134, v168, v169\n"
"v_mfma_f32_16x16x32_bf16 v[8:11], v[128:131], a[244:247], 0\n"
"s_waitcnt lgkmcnt(7)\n"
"ds_read_b128 a[140:143], v249 offset:33792\n"
"v_accvgpr_write_b32 a169, v25\n"
"v_mfma_f32_16x16x32_bf16 v[8:11], v[132:135], a[248:251], v[8:11]\n"
"ds_read_b128 v[208:211], v228 offset:20480\n"
"v_cvt_pk_bf16_f32 v26, v28, v29\n"
"v_mfma_f32_16x16x32_bf16 v[40:43], v[136:139], a[244:247], 0\n"
"ds_read_b128 a[180:183], v249 offset:51200\n"
"v_cvt_pk_bf16_f32 v114, v116, v117\n"
"v_mfma_f32_16x16x32_bf16 v[40:43], v[140:143], a[248:251], v[40:43]\n"
"ds_read_b128 a[148:151], v249 offset:34816\n"
"v_cvt_pk_bf16_f32 v0, v0, v1\n"
"v_permlane16_swap_b32 v24, v26\n"
"v_permlane16_swap_b32 v32, v34\n"
"v_exp_f32 v164, v164\n"
"v_mfma_f32_16x16x32_bf16 v[72:75], v[144:147], a[244:247], 0\n"
"ds_read_b128 a[124:127], v248 offset:52224\n"
"v_accvgpr_write_b32 a106, v122\n"
"v_mfma_f32_16x16x32_bf16 v[72:75], v[148:151], a[248:251], v[72:75]\n"
"v_permlane16_swap_b32 v121, v123\n"
"v_mfma_f32_16x16x32_bf16 v[76:79], v[148:151], v[224:227], 0\n"
"s_waitcnt lgkmcnt(6)\n"
"ds_read_b128 v[216:219], v228 offset:21504\n"
"v_cvt_pk_bf16_f32 v1, v2, v3\n"
"v_mfma_f32_16x16x32_bf16 v[76:79], v[144:147], a[252:255], v[76:79]\n"
"ds_read_b128 v[212:215], v229 offset:20480\n"
"v_cvt_pk_bf16_f32 v3, v6, v7\n"
"v_accvgpr_write_b32 a171, v27\n"
"v_cvt_pk_bf16_f32 v35, v38, v39\n"
"v_mov_b32 v200, 0\n"
"v_cvt_pk_bf16_f32 v97, v154, v155\n"
"v_exp_f32 v158, v158\n"
"v_exp_f32 v160, v160\n"
"v_mfma_f32_16x16x32_bf16 v[44:47], v[140:143], v[224:227], 0\n"
"ds_read_b128 v[220:223], v229 offset:21504\n"
"ds_read_b64_tr_b16 a[244:245], v234 offset:1024\n"
"v_mfma_f32_16x16x32_bf16 v[44:47], v[136:139], a[252:255], v[44:47]\n"
"v_permlane16_swap_b32 v1, v3\n"
"v_mfma_f32_16x16x32_bf16 v[12:15], v[132:135], v[224:227], 0\n"
"v_exp_f32 v156, v156\n"
"v_mfma_f32_16x16x32_bf16 v[12:15], v[128:131], a[252:255], v[12:15]\n"
"ds_read_b64_tr_b16 a[248:249], v238 offset:1024\n"
"v_bfe_u32 v248, v242, 8, 2\n"
"v_mfma_f32_16x16x32_bf16 a[220:223], v[124:127], a[252:255], 0\n"
"s_waitcnt lgkmcnt(5)\n"
"ds_read_b64_tr_b16 a[250:251], v238 offset:5120\n"
"v_cvt_pk_bf16_f32 v17, v18, v19\n"
"v_smfmac_f32_16x16x64_bf16 v[196:199], a[240:243], v[128:135], v241\n"
"s_add_u32 m0, s44, 98304\n"
"ds_read_b64_tr_b16 a[254:255], v235 offset:5120\n"
"v_mov_b32 v201, 0\n"
"v_mfma_f32_16x16x32_bf16 v[202:205], a[240:243], v[124:127], v[202:205]\n"
"s_waitcnt lgkmcnt(6)\n"
"buffer_load_dwordx4 v240, s[20:23], s56 offen nt lds\n"
"v_mfma_f32_16x16x32_bf16 v[168:171], v[216:219], a[8:11], v[180:183]\n"
"s_waitcnt lgkmcnt(4)\n"
"v_exp_f32 v161, v161\n"
"v_mfma_f32_16x16x32_bf16 v[168:171], v[220:223], a[12:15], v[168:171]\n"
"v_accvgpr_write_b32 a176, v32\n"
"v_bfe_u32 v249, v242, 4, 2\n"
"v_mfma_f32_16x16x32_bf16 v[168:171], v[208:211], a[0:3], v[168:171]\n"
"v_exp_f32 v163, v163\n"
"v_mfma_f32_16x16x32_bf16 v[168:171], v[212:215], a[4:7], v[168:171]\n"
"ds_read_b64_tr_b16 a[246:247], v234 offset:5120\n"
"v_accvgpr_write_b32 a168, v24\n"
"v_mfma_f32_16x16x32_bf16 v[172:175], v[208:211], a[16:19], v[184:187]\n"
"ds_read_b64_tr_b16 v[224:225], v239 offset:1024\n"
"v_cvt_pk_bf16_f32 v96, v152, v153\n"
"v_mfma_f32_16x16x32_bf16 v[172:175], v[212:215], a[20:23], v[172:175]\n"
"v_exp_f32 v166, v166\n"
"v_mfma_f32_16x16x32_bf16 v[172:175], v[216:219], a[24:27], v[172:175]\n"
"s_add_u32 s40, s56, s52\n"
"ds_read_b64_tr_b16 v[226:227], v239 offset:5120\n"
"v_accvgpr_write_b32 a170, v26\n"
"v_mfma_f32_16x16x32_bf16 v[172:175], v[220:223], a[28:31], v[172:175]\n"
"buffer_load_dwordx4 v240, s[20:23], s40 offen offset:1024 nt lds\n"
"v_mfma_f32_16x16x32_bf16 v[176:179], v[216:219], a[40:43], v[188:191]\n"
"v_permlane16_swap_b32 v113, v115\n"
"v_mfma_f32_16x16x32_bf16 v[176:179], v[220:223], a[44:47], v[176:179]\n"
"v_exp_f32 v168, v168\n"
"v_mfma_f32_16x16x32_bf16 v[176:179], v[208:211], a[32:35], v[176:179]\n"
"v_cvt_pk_bf16_f32 v2, v4, v5\n"
"v_cvt_pk_bf16_f32 v104, v156, v157\n"
"v_mfma_f32_16x16x32_bf16 v[176:179], v[212:215], a[36:39], v[176:179]\n"
"ds_read_b64_tr_b16 a[252:253], v235 offset:1024\n"
"v_lshlrev_b32 v248, 6, v248\n"
"v_mfma_f32_16x16x32_bf16 v[152:155], v[208:211], a[192:195], v[192:195]\n"
"v_permlane16_swap_b32 v112, v114\n"
"v_mfma_f32_16x16x32_bf16 v[152:155], v[212:215], a[196:199], v[152:155]\n"
"v_exp_f32 v165, v165\n"
"v_mfma_f32_16x16x32_bf16 v[152:155], v[216:219], a[200:203], v[152:155]\n"
"v_exp_f32 v169, v169\n"
"v_mfma_f32_16x16x32_bf16 v[152:155], v[220:223], a[204:207], v[152:155]\n"
"s_waitcnt lgkmcnt(1)\n"
"ds_read_b128 v[208:211], v230 offset:16384\n"
"v_cvt_pk_bf16_f32 v120, v164, v165\n"
"v_mfma_f32_16x16x32_bf16 a[232:235], v[124:127], a[244:247], 0\n"
"v_accvgpr_write_b32 a147, v3\n"
"v_bfe_u32 v254, v242, 6, 4\n"
"v_mfma_f32_16x16x32_bf16 v[24:27], v[128:131], a[244:247], 0\n"
"s_add_u32 s40, s56, s53\n"
"ds_read_b128 v[212:215], v231 offset:16384\n"
"v_accvgpr_write_b32 a107, v123\n"
"v_mfma_f32_16x16x32_bf16 v[24:27], v[132:135], a[248:251], v[24:27]\n"
"buffer_load_dwordx4 v240, s[20:23], s40 offen offset:2048 nt lds\n"
"v_permlane16_swap_b32 v0, v2\n"
"v_cvt_pk_bf16_f32 v19, v22, v23\n"
"v_permlane16_swap_b32 v33, v35\n"
"v_mul_u32_u24 v249, 20, v249\n"
"v_exp_f32 v162, v162\n"
"v_mfma_f32_16x16x32_bf16 v[56:59], v[136:139], a[244:247], 0\n"
"v_exp_f32 v175, v175\n"
"v_mfma_f32_16x16x32_bf16 v[56:59], v[140:143], a[248:251], v[56:59]\n"
"v_accvgpr_write_b32 a96, v112\n"
"v_accvgpr_write_b32 a105, v121\n"
"v_exp_f32 v159, v159\n"
"v_exp_f32 v170, v170\n"
"v_exp_f32 v174, v174\n"
"v_mfma_f32_16x16x32_bf16 v[88:91], v[144:147], a[244:247], 0\n"
"ds_read_b128 v[216:219], v230 offset:17408\n"
"v_accvgpr_write_b32 a177, v33\n"
"v_mfma_f32_16x16x32_bf16 v[88:91], v[148:151], a[248:251], v[88:91]\n"
"v_exp_f32 v172, v172\n"
"v_mfma_f32_16x16x32_bf16 v[92:95], v[148:151], v[224:227], 0\n"
"s_waitcnt lgkmcnt(2)\n"
"s_add_u32 s40, s56, s54\n"
"ds_read_b128 v[220:223], v231 offset:17408\n"
"v_accvgpr_write_b32 a99, v115\n"
"v_mfma_f32_16x16x32_bf16 v[92:95], v[144:147], a[252:255], v[92:95]\n"
"buffer_load_dwordx4 v240, s[20:23], s40 offen offset:3072 nt lds\n"
"v_mfma_f32_16x16x32_bf16 v[60:63], v[140:143], v[224:227], 0\n"
"s_add_u32 s55, s55, s59\n"
"v_accvgpr_write_b32 a145, v1\n"
"v_cvt_pk_bf16_f32 v105, v158, v159\n"
"v_mfma_f32_16x16x32_bf16 v[60:63], v[136:139], a[252:255], v[60:63]\n"
"s_add_u32 s41, s55, s57\n"
"v_exp_f32 v173, v173\n"
"v_mfma_f32_16x16x32_bf16 v[28:31], v[132:135], v[224:227], 0\n"
"s_and_b32 s41, s41, s58\n"
"ds_read_b64_tr_b16 a[248:249], v236 offset:0\n"
"v_accvgpr_write_b32 a98, v114\n"
"v_mfma_f32_16x16x32_bf16 v[28:31], v[128:131], a[252:255], v[28:31]\n"
"s_andn2_b32 s56, s55, s58\n"
"ds_read_b64_tr_b16 a[246:247], v232 offset:4096\n"
"v_cvt_pk_bf16_f32 v18, v20, v21\n"
"v_cvt_pk_bf16_f32 v112, v160, v161\n"
"v_exp_f32 v177, v177\n"
"v_exp_f32 v178, v178\n"
"v_mfma_f32_16x16x32_bf16 a[236:239], v[124:127], a[252:255], 0\n"
"s_or_b32 s56, s56, s41\n"
"ds_read_b64_tr_b16 a[244:245], v232 offset:0\n"
"v_cvt_pk_bf16_f32 v107, v174, v175\n"
"v_smfmac_f32_16x16x64_bf16 v[198:201], a[240:243], v[136:143], v241\n"
"ds_read_b64_tr_b16 a[254:255], v233 offset:4096\n"
"v_accvgpr_write_b32 a97, v113\n"
"v_mfma_f32_16x16x32_bf16 v[156:159], v[208:211], a[0:3], v[180:183]\n"
"s_waitcnt lgkmcnt(4)\n"
"v_exp_f32 v167, v167\n"
"v_mfma_f32_16x16x32_bf16 v[156:159], v[212:215], a[4:7], v[156:159]\n"
"ds_read_b64_tr_b16 a[250:251], v236 offset:4096\n"
"v_cvt_pk_bf16_f32 v113, v162, v163\n"
"v_mfma_f32_16x16x32_bf16 v[156:159], v[216:219], a[8:11], v[156:159]\n"
"s_add_u32 m0, s44, 49152\n"
"v_accvgpr_write_b32 a144, v0\n"
"v_cvt_pk_bf16_f32 v98, v168, v169\n"
"v_mfma_f32_16x16x32_bf16 v[156:159], v[220:223], a[12:15], v[156:159]\n"
"buffer_load_dwordx4 v240, s[16:19], s56 offen nt lds\n"
"v_mfma_f32_16x16x32_bf16 v[160:163], v[216:219], a[24:27], v[184:187]\n"
"v_exp_f32 v176, v176\n"
"v_mfma_f32_16x16x32_bf16 v[160:163], v[220:223], a[28:31], v[160:163]\n"
"v_exp_f32 v153, v153\n"
"v_mfma_f32_16x16x32_bf16 v[160:163], v[208:211], a[16:19], v[160:163]\n"
"v_accvgpr_write_b32 a178, v34\n"
"v_cvt_pk_bf16_f32 v121, v166, v167\n"
"v_mfma_f32_16x16x32_bf16 v[160:163], v[212:215], a[20:23], v[160:163]\n"
"ds_read_b64_tr_b16 v[224:225], v237 offset:0\n"
"v_cvt_pk_bf16_f32 v106, v172, v173\n"
"v_mfma_f32_16x16x32_bf16 v[164:167], v[208:211], a[32:35], v[188:191]\n"
"v_permlane16_swap_b32 v17, v19\n"
"v_mfma_f32_16x16x32_bf16 v[164:167], v[212:215], a[36:39], v[164:167]\n"
"ds_read_b64_tr_b16 a[252:253], v233 offset:0\n"
"ds_read_b64_tr_b16 v[226:227], v237 offset:4096\n"
"v_mfma_f32_16x16x32_bf16 v[164:167], v[216:219], a[40:43], v[164:167]\n"
"v_exp_f32 v179, v179\n"
"v_mfma_f32_16x16x32_bf16 v[164:167], v[220:223], a[44:47], v[164:167]\n"
"s_waitcnt lgkmcnt(3)\n"
"ds_read_b128 v[208:211], v230 offset:20480\n"
"v_accvgpr_write_b32 a179, v35\n"
"v_mfma_f32_16x16x32_bf16 a[208:211], v[124:127], a[244:247], 0\n"
"v_accvgpr_write_b32 a146, v2\n"
"v_xor_b32 v248, v248, v242\n"
"v_mfma_f32_16x16x32_bf16 v[0:3], v[128:131], a[244:247], 0\n"
"s_add_u32 s40, s56, s52\n"
"ds_read_b128 v[212:215], v231 offset:20480\n"
"v_xor_b32 v249, v249, v254\n"
"v_mfma_f32_16x16x32_bf16 v[0:3], v[132:135], a[248:251], v[0:3]\n"
"buffer_load_dwordx4 v240, s[16:19], s40 offen offset:1024 nt lds\n"
"v_mfma_f32_16x16x32_bf16 v[32:35], v[136:139], a[244:247], 0\n"
"v_exp_f32 v158, v158\n"
"v_mfma_f32_16x16x32_bf16 v[32:35], v[140:143], a[248:251], v[32:35]\n"
"v_exp_f32 v171, v171\n"
"v_mfma_f32_16x16x32_bf16 v[64:67], v[144:147], a[244:247], 0\n"
"v_exp_f32 v166, v166\n"
"v_mfma_f32_16x16x32_bf16 v[64:67], v[148:151], a[248:251], v[64:67]\n"
"s_waitcnt lgkmcnt(2)\n"
"ds_read_b128 v[220:223], v231 offset:21504\n"
"v_lshl_add_u32 v249, v249, 4, s44\n"
"v_mfma_f32_16x16x32_bf16 v[68:71], v[148:151], v[224:227], 0\n"
"s_add_u32 s40, s56, s53\n"
"ds_read_b128 v[216:219], v230 offset:21504\n"
"v_cvt_pk_bf16_f32 v115, v178, v179\n"
"v_mfma_f32_16x16x32_bf16 v[68:71], v[144:147], a[252:255], v[68:71]\n"
"buffer_load_dwordx4 v240, s[16:19], s40 offen offset:2048 nt lds\n"
"v_mfma_f32_16x16x32_bf16 v[36:39], v[140:143], v[224:227], 0\n"
"v_accvgpr_write_b32 a161, v17\n"
"v_accvgpr_write_b32 a163, v19\n"
"v_mfma_f32_16x16x32_bf16 v[36:39], v[136:139], a[252:255], v[36:39]\n"
"v_exp_f32 v157, v157\n"
"v_mfma_f32_16x16x32_bf16 v[4:7], v[132:135], v[224:227], 0\n"
"ds_read_b64_tr_b16 a[246:247], v232 offset:5120\n"
"v_cvt_pk_bf16_f32 v114, v176, v177\n"
"v_mfma_f32_16x16x32_bf16 v[4:7], v[128:131], a[252:255], v[4:7]\n"
"v_exp_f32 v163, v163\n"
"v_mfma_f32_16x16x32_bf16 a[212:215], v[124:127], a[252:255], 0\n"
"v_permlane16_swap_b32 v16, v18\n"
"v_smfmac_f32_16x16x64_bf16 v[200:203], a[240:243], v[144:151], v241\n"
"s_waitcnt lgkmcnt(2)\n"
"ds_read_b64_tr_b16 a[248:249], v236 offset:1024\n"
"v_cvt_pk_bf16_f32 v99, v170, v171\n"
"v_mfma_f32_16x16x32_bf16 v[168:171], v[208:211], a[0:3], v[180:183]\n"
"ds_read_b64_tr_b16 a[244:245], v232 offset:1024\n"
"ds_read_b64_tr_b16 a[250:251], v236 offset:5120\n"
"v_mfma_f32_16x16x32_bf16 v[168:171], v[212:215], a[4:7], v[168:171]\n"
"s_waitcnt lgkmcnt(4)\n"
"v_exp_f32 v165, v165\n"
"v_mfma_f32_16x16x32_bf16 v[168:171], v[216:219], a[8:11], v[168:171]\n"
"s_add_u32 s40, s56, s54\n"
"v_exp_f32 v162, v162\n"
"v_mfma_f32_16x16x32_bf16 v[168:171], v[220:223], a[12:15], v[168:171]\n"
"buffer_load_dwordx4 v240, s[16:19], s40 offen offset:3072 nt lds\n"
"v_mfma_f32_16x16x32_bf16 v[172:175], v[216:219], a[24:27], v[184:187]\n"
"v_accvgpr_write_b32 a160, v16\n"
"v_accvgpr_write_b32 a162, v18\n"
"v_mfma_f32_16x16x32_bf16 v[172:175], v[220:223], a[28:31], v[172:175]\n"
"v_exp_f32 v154, v154\n"
"v_mfma_f32_16x16x32_bf16 v[172:175], v[208:211], a[16:19], v[172:175]\n"
"v_exp_f32 v167, v167\n"
"v_mfma_f32_16x16x32_bf16 v[172:175], v[212:215], a[20:23], v[172:175]\n"
"s_cbranch_vccnz LPCE1_%=\nLPKE1_%=:\n"
"ds_read_b64_tr_b16 v[224:225], v237 offset:1024\n"
"v_mfma_f32_16x16x32_bf16 v[176:179], v[208:211], a[32:35], v[188:191]\n"
"v_exp_f32 v156, v156\n"
"v_mfma_f32_16x16x32_bf16 v[176:179], v[212:215], a[36:39], v[176:179]\n"
"ds_read_b64_tr_b16 a[252:253], v233 offset:1024\n"
"ds_read_b64_tr_b16 a[254:255], v233 offset:5120\n"
"v_mfma_f32_16x16x32_bf16 v[176:179], v[216:219], a[40:43], v[176:179]\n"
"v_exp_f32 v164, v164\n"
"v_mfma_f32_16x16x32_bf16 v[176:179], v[220:223], a[44:47], v[176:179]\n"
"s_waitcnt lgkmcnt(3)\n"
"ds_read_b64_tr_b16 v[226:227], v237 offset:5120\n"
"v_cvt_pk_bf16_f32 v109, v162, v163\n"
"v_mfma_f32_16x16x32_bf16 a[224:227], v[124:127], a[244:247], 0\n"
"v_exp_f32 v159, v159\n"
"v_mfma_f32_16x16x32_bf16 v[16:19], v[128:131], a[244:247], 0\n"
"s_waitcnt vmcnt(8)\n"
"v_exp_f32 v161, v161\n"
"v_mfma_f32_16x16x32_bf16 v[16:19], v[132:135], a[248:251], v[16:19]\n"
"s_barrier\n"
"ds_read_b128 v[212:215], v229 offset:32768\n"
"v_exp_f32 v152, v152\n"
"v_exp_f32 v178, v178\n"
"v_mfma_f32_16x16x32_bf16 v[48:51], v[136:139], a[244:247], 0\n"
"ds_read_b128 v[208:211], v228 offset:32768\n"
"v_cvt_pk_bf16_f32 v100, v156, v157\n"
"v_mfma_f32_16x16x32_bf16 v[48:51], v[140:143], a[248:251], v[48:51]\n"
"ds_read_b128 v[216:219], v228 offset:33792\n"
"v_cvt_pk_bf16_f32 v117, v166, v167\n"
"v_mfma_f32_16x16x32_bf16 v[80:83], v[144:147], a[244:247], 0\n"
"ds_read_b128 v[220:223], v229 offset:33792\n"
"v_cvt_pk_bf16_f32 v101, v158, v159\n"
"v_mfma_f32_16x16x32_bf16 v[80:83], v[148:151], a[248:251], v[80:83]\n"
"s_waitcnt lgkmcnt(4)\n"
"ds_read_b64_tr_b16 a[244:245], v234 offset:16384\n"
"ds_read_b64_tr_b16 a[246:247], v234 offset:20480\n"
"ds_read_b64_tr_b16 a[248:249], v238 offset:16384\n"
"ds_read_b64_tr_b16 a[250:251], v238 offset:20480\n"
"v_mfma_f32_16x16x32_bf16 v[84:87], v[148:151], v[224:227], 0\n"
"v_exp_f32 v169, v169\n"
"v_mfma_f32_16x16x32_bf16 v[84:87], v[144:147], a[252:255], v[84:87]\n"
"v_exp_f32 v160, v160\n"
"v_mfma_f32_16x16x32_bf16 v[52:55], v[140:143], v[224:227], 0\n"
"v_exp_f32 v155, v155\n"
"v_mfma_f32_16x16x32_bf16 v[52:55], v[136:139], a[252:255], v[52:55]\n"
"v_exp_f32 v173, v173\n"
"v_mfma_f32_16x16x32_bf16 v[20:23], v[132:135], v[224:227], 0\n"
"ds_read_b64_tr_b16 v[226:227], v239 offset:20480\n"
"v_cvt_pk_bf16_f32 v123, v154, v155\n"
"v_mfma_f32_16x16x32_bf16 v[20:23], v[128:131], a[252:255], v[20:23]\n"
"v_exp_f32 v175, v175\n"
"v_mfma_f32_16x16x32_bf16 a[228:231], v[124:127], a[252:255], 0\n"
"s_waitcnt lgkmcnt(5)\n"
"v_cvt_pk_bf16_f32 v122, v152, v153\n"
"s_branch LAY2_%=\n"
"LAL_%=:\n"
"v_mfma_f32_16x16x32_bf16 a[224:227], v[124:127], a[244:247], a[224:227]\n"
"v_exp_f32 v169, v169\n"
"v_mfma_f32_16x16x32_bf16 v[16:19], v[128:131], a[244:247], v[16:19]\n"
"s_waitcnt vmcnt(8)\n"
"v_exp_f32 v175, v175\n"
"v_mfma_f32_16x16x32_bf16 v[16:19], v[132:135], a[248:251], v[16:19]\n"
"s_barrier\n"
"ds_read_b128 v[220:223], v229 offset:1024\n"
"v_mfma_f32_16x16x32_bf16 v[48:51], v[136:139], a[244:247], v[48:51]\n"
"ds_read_b128 v[216:219], v228 offset:1024\n"
"v_cvt_pk_bf16_f32 v109, v162, v163\n"
"v_mfma_f32_16x16x32_bf16 v[48:51], v[140:143], a[248:251], v[48:51]\n"
"ds_read_b128 v[212:215], v229 offset:0\n"
"v_mfma_f32_16x16x32_bf16 v[80:83], v[144:147], a[244:247], v[80:83]\n"
"ds_read_b128 v[208:211], v228 offset:0\n"
"v_cvt_pk_bf16_f32 v101, v158, v159\n"
"v_mfma_f32_16x16x32_bf16 v[80:83], v[148:151], a[248:251], v[80:83]\n"
"s_waitcnt lgkmcnt(4)\n"
"v_exp_f32 v178, v178\n"
"v_mfma_f32_16x16x32_bf16 v[84:87], v[148:151], v[224:227], v[84:87]\n"
"ds_read_b64_tr_b16 a[244:245], v234 offset:49152\n"
"v_cvt_pk_bf16_f32 v117, v166, v167\n"
"v_mfma_f32_16x16x32_bf16 v[84:87], v[144:147], a[252:255], v[84:87]\n"
"ds_read_b64_tr_b16 a[250:251], v238 offset:53248\n"
"v_cvt_pk_bf16_f32 v123, v154, v155\n"
"v_mfma_f32_16x16x32_bf16 v[52:55], v[140:143], v[224:227], v[52:55]\n"
"v_exp_f32 v173, v173\n"
"v_mfma_f32_16x16x32_bf16 v[52:55], v[136:139], a[252:255], v[52:55]\n"
"ds_read_b64_tr_b16 a[248:249], v238 offset:49152\n"
"v_mfma_f32_16x16x32_bf16 v[20:23], v[132:135], v[224:227], v[20:23]\n"
"v_exp_f32 v156, v156\n"
"v_mfma_f32_16x16x32_bf16 v[20:23], v[128:131], a[252:255], v[20:23]\n"
"ds_read_b64_tr_b16 v[226:227], v239 offset:53248\n"
"v_cvt_pk_bf16_f32 v100, v156, v157\n"
"v_mfma_f32_16x16x32_bf16 a[228:231], v[124:127], a[252:255], a[228:231]\n"
"s_waitcnt lgkmcnt(4)\n"
"ds_read_b64_tr_b16 a[246:247], v234 offset:53248\n"
"v_mfma_f32_16x16x32_bf16 v[152:155], v[208:211], a[0:3], v[180:183]\n"
"ds_read_b64_tr_b16 v[224:225], v239 offset:49152\n"
"v_cvt_pk_bf16_f32 v116, v164, v165\n"
"v_mfma_f32_16x16x32_bf16 v[152:155], v[212:215], a[4:7], v[152:155]\n"
"ds_read_b64_tr_b16 a[252:253], v235 offset:49152\n"
"v_cvt_pk_bf16_f32 v108, v160, v161\n"
"v_mfma_f32_16x16x32_bf16 v[152:155], v[216:219], a[8:11], v[152:155]\n"
"s_add_u32 m0, s44, 81920\n"
"ds_read_b64_tr_b16 a[254:255], v235 offset:53248\n"
"v_mfma_f32_16x16x32_bf16 v[152:155], v[220:223], a[12:15], v[152:155]\n"
"buffer_load_dwordx4 v240, s[20:23], s56 offen nt lds\n"
"v_mfma_f32_16x16x32_bf16 v[156:159], v[216:219], a[24:27], v[184:187]\n"
"v_exp_f32 v177, v177\n"
"v_mfma_f32_16x16x32_bf16 v[156:159], v[220:223], a[28:31], v[156:159]\n"
"v_exp_f32 v168, v168\n"
"v_mfma_f32_16x16x32_bf16 v[156:159], v[208:211], a[16:19], v[156:159]\n"
"v_exp_f32 v179, v179\n"
"v_mfma_f32_16x16x32_bf16 v[156:159], v[212:215], a[20:23], v[156:159]\n"
"v_exp_f32 v174, v174\n"
"v_mfma_f32_16x16x32_bf16 v[160:163], v[208:211], a[32:35], v[188:191]\n"
"v_exp_f32 v172, v172\n"
"v_mfma_f32_16x16x32_bf16 v[160:163], v[212:215], a[36:39], v[160:163]\n"
"v_exp_f32 v171, v171\n"
"v_mfma_f32_16x16x32_bf16 v[160:163], v[216:219], a[40:43], v[160:163]\n"
"v_exp_f32 v154, v154\n"
"v_mfma_f32_16x16x32_bf16 v[160:163], v[220:223], a[44:47], v[160:163]\n"
"v_exp_f32 v170, v170\n"
"v_mfma_f32_16x16x32_bf16 v[164:167], v[216:219], a[200:203], v[192:195]\n"
"ds_read_b128 v[216:219], v228 offset:5120\n"
"v_cvt_pk_bf16_f32 v103, v170, v171\n"
"v_mfma_f32_16x16x32_bf16 v[164:167], v[220:223], a[204:207], v[164:167]\n"
"v_cvt_pk_bf16_f32 v102, v168, v169\n"
"v_cvt_pk_bf16_f32 v119, v178, v179\n"
"v_mfma_f32_16x16x32_bf16 v[164:167], v[208:211], a[192:195], v[164:167]\n"
"s_add_u32 s40, s56, s52\n"
"ds_read_b128 v[208:211], v228 offset:4096\n"
"v_mfma_f32_16x16x32_bf16 v[164:167], v[212:215], a[196:199], v[164:167]\n"
"s_waitcnt lgkmcnt(2)\n"
"buffer_load_dwordx4 v240, s[20:23], s40 offen offset:1024 nt lds\n"
"v_mfma_f32_16x16x32_bf16 a[216:219], v[120:123], a[244:247], a[216:219]\n"
"v_cvt_pk_bf16_f32 v110, v172, v173\n"
"v_cvt_pk_bf16_f32 v111, v174, v175\n"
"v_mfma_f32_16x16x32_bf16 v[8:11], v[96:99], a[244:247], v[8:11]\n"
"v_exp_f32 v176, v176\n"
"v_mfma_f32_16x16x32_bf16 v[8:11], v[100:103], a[248:251], v[8:11]\n"
"v_exp_f32 v155, v155\n"
"v_mfma_f32_16x16x32_bf16 v[40:43], v[104:107], a[244:247], v[40:43]\n"
"v_exp_f32 v152, v152\n"
"v_mfma_f32_16x16x32_bf16 v[40:43], v[108:111], a[248:251], v[40:43]\n"
"ds_read_b128 v[212:215], v229 offset:4096\n"
"v_cvt_pk_bf16_f32 v118, v176, v177\n"
"v_mfma_f32_16x16x32_bf16 v[72:75], v[112:115], a[244:247], v[72:75]\n"
"v_exp_f32 v158, v158\n"
"v_mfma_f32_16x16x32_bf16 v[72:75], v[116:119], a[248:251], v[72:75]\n"
"v_exp_f32 v163, v163\n"
"v_mfma_f32_16x16x32_bf16 v[76:79], v[116:119], v[224:227], v[76:79]\n"
"ds_read_b128 v[220:223], v229 offset:5120\n"
"v_mfma_f32_16x16x32_bf16 v[76:79], v[112:115], a[252:255], v[76:79]\n"
"v_exp_f32 v159, v159\n"
"v_mfma_f32_16x16x32_bf16 v[44:47], v[108:111], v[224:227], v[44:47]\n"
"s_add_u32 s40, s56, s53\n"
"ds_read_b64_tr_b16 a[244:245], v234 offset:50176\n"
"v_mfma_f32_16x16x32_bf16 v[44:47], v[104:107], a[252:255], v[44:47]\n"
"buffer_load_dwordx4 v240, s[20:23], s40 offen offset:2048 nt lds\n"
"v_mfma_f32_16x16x32_bf16 v[12:15], v[100:103], v[224:227], v[12:15]\n"
"v_exp_f32 v153, v153\n"
"v_mfma_f32_16x16x32_bf16 v[12:15], v[96:99], a[252:255], v[12:15]\n"
"v_exp_f32 v165, v165\n"
"v_mfma_f32_16x16x32_bf16 a[220:223], v[120:123], a[252:255], a[220:223]\n"
"v_exp_f32 v164, v164\n"
"v_smfmac_f32_16x16x64_bf16 v[196:199], a[240:243], v[96:103], v241\n"
"v_cvt_pk_bf16_f32 v129, v154, v155\n"
"v_mfma_f32_16x16x32_bf16 v[202:205], a[240:243], v[120:123], v[202:205]\n"
"s_waitcnt lgkmcnt(1)\n"
"ds_read_b64_tr_b16 a[254:255], v235 offset:54272\n"
"v_mfma_f32_16x16x32_bf16 v[168:171], v[216:219], a[8:11], v[180:183]\n"
"ds_read_b64_tr_b16 a[248:249], v238 offset:50176\n"
"v_mfma_f32_16x16x32_bf16 v[168:171], v[220:223], a[12:15], v[168:171]\n"
"ds_read_b64_tr_b16 a[246:247], v234 offset:54272\n"
"v_mfma_f32_16x16x32_bf16 v[168:171], v[208:211], a[0:3], v[168:171]\n"
"ds_read_b64_tr_b16 v[224:225], v239 offset:50176\n"
"v_cvt_pk_bf16_f32 v124, v164, v165\n"
"v_mfma_f32_16x16x32_bf16 v[168:171], v[212:215], a[4:7], v[168:171]\n"
"v_exp_f32 v166, v166\n"
"v_mfma_f32_16x16x32_bf16 v[172:175], v[208:211], a[16:19], v[184:187]\n"
"ds_read_b64_tr_b16 a[252:253], v235 offset:50176\n"
"v_cvt_pk_bf16_f32 v128, v152, v153\n"
"v_mfma_f32_16x16x32_bf16 v[172:175], v[212:215], a[20:23], v[172:175]\n"
"ds_read_b64_tr_b16 v[226:227], v239 offset:54272\n"
"v_cvt_pk_bf16_f32 v137, v158, v159\n"
"v_mfma_f32_16x16x32_bf16 v[172:175], v[216:219], a[24:27], v[172:175]\n"
"s_add_u32 s40, s56, s54\n"
"ds_read_b64_tr_b16 a[250:251], v238 offset:54272\n"
"v_mfma_f32_16x16x32_bf16 v[172:175], v[220:223], a[28:31], v[172:175]\n"
"buffer_load_dwordx4 v240, s[20:23], s40 offen offset:3072 nt lds\n"
"v_mfma_f32_16x16x32_bf16 v[176:179], v[216:219], a[40:43], v[188:191]\n"
"s_add_u32 s55, s55, s59\n"
"v_exp_f32 v167, v167\n"
"v_mfma_f32_16x16x32_bf16 v[176:179], v[220:223], a[44:47], v[176:179]\n"
"s_add_u32 s41, s55, s57\n"
"v_exp_f32 v161, v161\n"
"v_mfma_f32_16x16x32_bf16 v[176:179], v[208:211], a[32:35], v[176:179]\n"
"s_and_b32 s41, s41, s58\n"
"v_exp_f32 v157, v157\n"
"v_mfma_f32_16x16x32_bf16 v[176:179], v[212:215], a[36:39], v[176:179]\n"
"s_andn2_b32 s56, s55, s58\n"
"v_exp_f32 v160, v160\n"
"v_mfma_f32_16x16x32_bf16 v[152:155], v[208:211], a[192:195], v[192:195]\n"
"s_or_b32 s56, s56, s41\n"
"ds_read_b128 v[208:211], v230 offset:0\n"
"v_mfma_f32_16x16x32_bf16 v[152:155], v[212:215], a[196:199], v[152:155]\n"
"v_exp_f32 v172, v172\n"
"v_mfma_f32_16x16x32_bf16 v[152:155], v[216:219], a[200:203], v[152:155]\n"
"ds_read_b128 v[216:219], v230 offset:1024\n"
"v_mfma_f32_16x16x32_bf16 v[152:155], v[220:223], a[204:207], v[152:155]\n"
"s_waitcnt lgkmcnt(2)\n"
"v_exp_f32 v162, v162\n"
"v_mfma_f32_16x16x32_bf16 a[232:235], v[120:123], a[244:247], a[232:235]\n"
"ds_read_b128 v[212:215], v231 offset:0\n"
"v_mfma_f32_16x16x32_bf16 v[24:27], v[96:99], a[244:247], v[24:27]\n"
"s_add_u32 m0, s44, 32768\n"
"ds_read_b128 v[220:223], v231 offset:1024\n"
"v_mfma_f32_16x16x32_bf16 v[24:27], v[100:103], a[248:251], v[24:27]\n"
"buffer_load_dwordx4 v240, s[16:19], s56 offen nt lds\n"
"v_mfma_f32_16x16x32_bf16 v[56:59], v[104:107], a[244:247], v[56:59]\n"
"v_exp_f32 v171, v171\n"
"v_mfma_f32_16x16x32_bf16 v[56:59], v[108:111], a[248:251], v[56:59]\n"
"v_exp_f32 v169, v169\n"
"v_mfma_f32_16x16x32_bf16 v[88:91], v[112:115], a[244:247], v[88:91]\n"
"v_exp_f32 v154, v154\n"
"v_mfma_f32_16x16x32_bf16 v[88:91], v[116:119], a[248:251], v[88:91]\n"
"v_exp_f32 v156, v156\n"
"v_mfma_f32_16x16x32_bf16 v[92:95], v[116:119], v[224:227], v[92:95]\n"
"ds_read_b64_tr_b16 a[244:245], v232 offset:49152\n"
"v_cvt_pk_bf16_f32 v145, v162, v163\n"
"v_mfma_f32_16x16x32_bf16 v[92:95], v[112:115], a[252:255], v[92:95]\n"
"ds_read_b64_tr_b16 a[250:251], v236 offset:53248\n"
"v_cvt_pk_bf16_f32 v136, v156, v157\n"
"v_mfma_f32_16x16x32_bf16 v[60:63], v[108:111], v[224:227], v[60:63]\n"
"ds_read_b64_tr_b16 a[246:247], v232 offset:53248\n"
"v_cvt_pk_bf16_f32 v144, v160, v161\n"
"v_mfma_f32_16x16x32_bf16 v[60:63], v[104:107], a[252:255], v[60:63]\n"
"v_exp_f32 v152, v152\n"
"v_mfma_f32_16x16x32_bf16 v[28:31], v[100:103], v[224:227], v[28:31]\n"
"ds_read_b64_tr_b16 v[224:225], v237 offset:49152\n"
"ds_read_b64_tr_b16 v[226:227], v237 offset:53248\n"
"v_mfma_f32_16x16x32_bf16 v[28:31], v[96:99], a[252:255], v[28:31]\n"
"ds_read_b64_tr_b16 a[248:249], v236 offset:49152\n"
"v_cvt_pk_bf16_f32 v125, v166, v167\n"
"v_mfma_f32_16x16x32_bf16 a[236:239], v[120:123], a[252:255], a[236:239]\n"
"s_add_u32 s40, s56, s52\n"
"ds_read_b64_tr_b16 a[252:253], v233 offset:49152\n"
"v_smfmac_f32_16x16x64_bf16 v[198:201], a[240:243], v[104:111], v241\n"
"s_waitcnt lgkmcnt(7)\n"
"buffer_load_dwordx4 v240, s[16:19], s40 offen offset:1024 nt lds\n"
"v_mfma_f32_16x16x32_bf16 v[156:159], v[208:211], a[0:3], v[180:183]\n"
"v_exp_f32 v175, v175\n"
"v_mfma_f32_16x16x32_bf16 v[156:159], v[212:215], a[4:7], v[156:159]\n"
"v_exp_f32 v173, v173\n"
"v_mfma_f32_16x16x32_bf16 v[156:159], v[216:219], a[8:11], v[156:159]\n"
"v_exp_f32 v168, v168\n"
"v_mfma_f32_16x16x32_bf16 v[156:159], v[220:223], a[12:15], v[156:159]\n"
"ds_read_b64_tr_b16 a[254:255], v233 offset:53248\n"
"v_mfma_f32_16x16x32_bf16 v[160:163], v[216:219], a[24:27], v[184:187]\n"
"v_exp_f32 v170, v170\n"
"v_mfma_f32_16x16x32_bf16 v[160:163], v[220:223], a[28:31], v[160:163]\n"
"v_exp_f32 v178, v178\n"
"v_mfma_f32_16x16x32_bf16 v[160:163], v[208:211], a[16:19], v[160:163]\n"
"v_exp_f32 v177, v177\n"
"v_mfma_f32_16x16x32_bf16 v[160:163], v[212:215], a[20:23], v[160:163]\n"
"v_exp_f32 v176, v176\n"
"v_mfma_f32_16x16x32_bf16 v[164:167], v[208:211], a[32:35], v[188:191]\n"
"ds_read_b128 v[208:211], v230 offset:4096\n"
"v_cvt_pk_bf16_f32 v131, v170, v171\n"
"v_mfma_f32_16x16x32_bf16 v[164:167], v[212:215], a[36:39], v[164:167]\n"
"ds_read_b128 v[212:215], v231 offset:4096\n"
"v_mfma_f32_16x16x32_bf16 v[164:167], v[216:219], a[40:43], v[164:167]\n"
"v_exp_f32 v153, v153\n"
"v_mfma_f32_16x16x32_bf16 v[164:167], v[220:223], a[44:47], v[164:167]\n"
"s_waitcnt lgkmcnt(2)\n"
"ds_read_b128 v[220:223], v231 offset:5120\n"
"v_cvt_pk_bf16_f32 v138, v172, v173\n"
"v_mfma_f32_16x16x32_bf16 a[208:211], v[120:123], a[244:247], a[208:211]\n"
"v_exp_f32 v174, v174\n"
"v_mfma_f32_16x16x32_bf16 v[0:3], v[96:99], a[244:247], v[0:3]\n"
"s_add_u32 s40, s56, s53\n"
"ds_read_b128 v[216:219], v230 offset:5120\n"
"v_mfma_f32_16x16x32_bf16 v[0:3], v[100:103], a[248:251], v[0:3]\n"
"buffer_load_dwordx4 v240, s[16:19], s40 offen offset:2048 nt lds\n"
"v_mfma_f32_16x16x32_bf16 v[32:35], v[104:107], a[244:247], v[32:35]\n"
"v_exp_f32 v163, v163\n"
"v_mfma_f32_16x16x32_bf16 v[32:35], v[108:111], a[248:251], v[32:35]\n"
"v_exp_f32 v161, v161\n"
"v_mfma_f32_16x16x32_bf16 v[64:67], v[112:115], a[244:247], v[64:67]\n"
"v_exp_f32 v155, v155\n"
"v_mfma_f32_16x16x32_bf16 v[64:67], v[116:119], a[248:251], v[64:67]\n"
"v_exp_f32 v166, v166\n"
"v_mfma_f32_16x16x32_bf16 v[68:71], v[116:119], v[224:227], v[68:71]\n"
"ds_read_b64_tr_b16 a[250:251], v236 offset:54272\n"
"v_cvt_pk_bf16_f32 v139, v174, v175\n"
"v_mfma_f32_16x16x32_bf16 v[68:71], v[112:115], a[252:255], v[68:71]\n"
"ds_read_b64_tr_b16 a[246:247], v232 offset:54272\n"
"v_cvt_pk_bf16_f32 v130, v168, v169\n"
"v_mfma_f32_16x16x32_bf16 v[36:39], v[108:111], v[224:227], v[36:39]\n"
"v_exp_f32 v179, v179\n"
"v_mfma_f32_16x16x32_bf16 v[36:39], v[104:107], a[252:255], v[36:39]\n"
"ds_read_b64_tr_b16 a[244:245], v232 offset:50176\n"
"v_cvt_pk_bf16_f32 v126, v152, v153\n"
"v_mfma_f32_16x16x32_bf16 v[4:7], v[100:103], v[224:227], v[4:7]\n"
"s_add_u32 s40, s56, s54\n"
"ds_read_b64_tr_b16 a[248:249], v236 offset:50176\n"
"v_mfma_f32_16x16x32_bf16 v[4:7], v[96:99], a[252:255], v[4:7]\n"
"buffer_load_dwordx4 v240, s[16:19], s40 offen offset:3072 nt lds\n"
"v_mfma_f32_16x16x32_bf16 a[212:215], v[120:123], a[252:255], a[212:215]\n"
"v_exp_f32 v162, v162\n"
"v_smfmac_f32_16x16x64_bf16 v[200:203], a[240:243], v[112:119], v241\n"
"s_waitcnt lgkmcnt(4)\n"
"v_cvt_pk_bf16_f32 v146, v176, v177\n"
"v_mfma_f32_16x16x32_bf16 v[168:171], v[208:211], a[0:3], v[180:183]\n"
"v_exp_f32 v157, v157\n"
"v_mfma_f32_16x16x32_bf16 v[168:171], v[212:215], a[4:7], v[168:171]\n"
"v_exp_f32 v165, v165\n"
"v_mfma_f32_16x16x32_bf16 v[168:171], v[216:219], a[8:11], v[168:171]\n"
"ds_read_b64_tr_b16 a[254:255], v233 offset:54272\n"
"ds_read_b64_tr_b16 v[226:227], v237 offset:54272\n"
"v_mfma_f32_16x16x32_bf16 v[168:171], v[220:223], a[12:15], v[168:171]\n"
"ds_read_b64_tr_b16 a[252:253], v233 offset:50176\n"
"v_cvt_pk_bf16_f32 v147, v178, v179\n"
"v_mfma_f32_16x16x32_bf16 v[172:175], v[216:219], a[24:27], v[184:187]\n"
"ds_read_b64_tr_b16 v[224:225], v237 offset:50176\n"
"v_mfma_f32_16x16x32_bf16 v[172:175], v[220:223], a[28:31], v[172:175]\n"
"v_exp_f32 v160, v160\n"
"v_mfma_f32_16x16x32_bf16 v[172:175], v[208:211], a[16:19], v[172:175]\n"
"v_exp_f32 v158, v158\n"
"v_mfma_f32_16x16x32_bf16 v[172:175], v[212:215], a[20:23], v[172:175]\n"
"s_cbranch_vccnz LPCL0_%=\nLPKL0_%=:\n"
"v_mfma_f32_16x16x32_bf16 v[176:179], v[208:211], a[32:35], v[188:191]\n"
"v_exp_f32 v159, v159\n"
"v_mfma_f32_16x16x32_bf16 v[176:179], v[212:215], a[36:39], v[176:179]\n"
"v_exp_f32 v167, v167\n"
"v_mfma_f32_16x16x32_bf16 v[176:179], v[216:219], a[40:43], v[176:179]\n"
"v_exp_f32 v164, v164\n"
"v_mfma_f32_16x16x32_bf16 v[176:179], v[220:223], a[44:47], v[176:179]\n"
"s_waitcnt lgkmcnt(1)\n"
"v_mfma_f32_16x16x32_bf16 a[224:227], v[120:123], a[244:247], a[224:227]\n"
"v_exp_f32 v169, v169\n"
"v_mfma_f32_16x16x32_bf16 v[16:19], v[96:99], a[244:247], v[16:19]\n"
"s_waitcnt vmcnt(8)\n"
"v_exp_f32 v175, v175\n"
"v_mfma_f32_16x16x32_bf16 v[16:19], v[100:103], a[248:251], v[16:19]\n"
"s_barrier\n"
"ds_read_b128 v[220:223], v229 offset:17408\n"
"v_mfma_f32_16x16x32_bf16 v[48:51], v[104:107], a[244:247], v[48:51]\n"
"ds_read_b128 v[216:219], v228 offset:17408\n"
"v_cvt_pk_bf16_f32 v141, v162, v163\n"
"v_mfma_f32_16x16x32_bf16 v[48:51], v[108:111], a[248:251], v[48:51]\n"
"ds_read_b128 v[212:215], v229 offset:16384\n"
"v_mfma_f32_16x16x32_bf16 v[80:83], v[112:115], a[244:247], v[80:83]\n"
"ds_read_b128 v[208:211], v228 offset:16384\n"
"v_cvt_pk_bf16_f32 v133, v158, v159\n"
"v_mfma_f32_16x16x32_bf16 v[80:83], v[116:119], a[248:251], v[80:83]\n"
"s_waitcnt lgkmcnt(4)\n"
"v_exp_f32 v178, v178\n"
"v_mfma_f32_16x16x32_bf16 v[84:87], v[116:119], v[224:227], v[84:87]\n"
"ds_read_b64_tr_b16 a[244:245], v234 offset:0\n"
"v_cvt_pk_bf16_f32 v149, v166, v167\n"
"v_mfma_f32_16x16x32_bf16 v[84:87], v[112:115], a[252:255], v[84:87]\n"
"ds_read_b64_tr_b16 a[250:251], v238 offset:4096\n"
"v_cvt_pk_bf16_f32 v127, v154, v155\n"
"v_mfma_f32_16x16x32_bf16 v[52:55], v[108:111], v[224:227], v[52:55]\n"
"v_exp_f32 v173, v173\n"
"v_mfma_f32_16x16x32_bf16 v[52:55], v[104:107], a[252:255], v[52:55]\n"
"ds_read_b64_tr_b16 a[248:249], v238 offset:0\n"
"v_mfma_f32_16x16x32_bf16 v[20:23], v[100:103], v[224:227], v[20:23]\n"
"v_exp_f32 v156, v156\n"
"v_mfma_f32_16x16x32_bf16 v[20:23], v[96:99], a[252:255], v[20:23]\n"
"ds_read_b64_tr_b16 v[226:227], v239 offset:4096\n"
"v_cvt_pk_bf16_f32 v132, v156, v157\n"
"v_mfma_f32_16x16x32_bf16 a[228:231], v[120:123], a[252:255], a[228:231]\n"
"s_waitcnt lgkmcnt(4)\n"
"ds_read_b64_tr_b16 a[246:247], v234 offset:4096\n"
"v_mfma_f32_16x16x32_bf16 v[152:155], v[208:211], a[0:3], v[180:183]\n"
"ds_read_b64_tr_b16 v[224:225], v239 offset:0\n"
"v_cvt_pk_bf16_f32 v148, v164, v165\n"
"v_mfma_f32_16x16x32_bf16 v[152:155], v[212:215], a[4:7], v[152:155]\n"
"ds_read_b64_tr_b16 a[252:253], v235 offset:0\n"
"v_cvt_pk_bf16_f32 v140, v160, v161\n"
"v_mfma_f32_16x16x32_bf16 v[152:155], v[216:219], a[8:11], v[152:155]\n"
"s_add_u32 m0, s44, 98304\n"
"ds_read_b64_tr_b16 a[254:255], v235 offset:4096\n"
"v_mfma_f32_16x16x32_bf16 v[152:155], v[220:223], a[12:15], v[152:155]\n"
"buffer_load_dwordx4 v240, s[20:23], s56 offen nt lds\n"
"v_mfma_f32_16x16x32_bf16 v[156:159], v[216:219], a[24:27], v[184:187]\n"
"v_exp_f32 v177, v177\n"
"v_mfma_f32_16x16x32_bf16 v[156:159], v[220:223], a[28:31], v[156:159]\n"
"v_exp_f32 v168, v168\n"
"v_mfma_f32_16x16x32_bf16 v[156:159], v[208:211], a[16:19], v[156:159]\n"
"v_exp_f32 v179, v179\n"
"v_mfma_f32_16x16x32_bf16 v[156:159], v[212:215], a[20:23], v[156:159]\n"
"v_exp_f32 v174, v174\n"
"v_mfma_f32_16x16x32_bf16 v[160:163], v[208:211], a[32:35], v[188:191]\n"
"v_exp_f32 v172, v172\n"
"v_mfma_f32_16x16x32_bf16 v[160:163], v[212:215], a[36:39], v[160:163]\n"
"v_exp_f32 v171, v171\n"
"v_mfma_f32_16x16x32_bf16 v[160:163], v[216:219], a[40:43], v[160:163]\n"
"v_exp_f32 v154, v154\n"
"v_mfma_f32_16x16x32_bf16 v[160:163], v[220:223], a[44:47], v[160:163]\n"
"v_exp_f32 v170, v170\n"
"v_mfma_f32_16x16x32_bf16 v[164:167], v[216:219], a[200:203], v[192:195]\n"
"ds_read_b128 v[216:219], v228 offset:21504\n"
"v_cvt_pk_bf16_f32 v135, v170, v171\n"
"v_mfma_f32_16x16x32_bf16 v[164:167], v[220:223], a[204:207], v[164:167]\n"
"v_cvt_pk_bf16_f32 v134, v168, v169\n"
"v_cvt_pk_bf16_f32 v151, v178, v179\n"
"v_mfma_f32_16x16x32_bf16 v[164:167], v[208:211], a[192:195], v[164:167]\n"
"s_add_u32 s40, s56, s52\n"
"ds_read_b128 v[208:211], v228 offset:20480\n"
"v_mfma_f32_16x16x32_bf16 v[164:167], v[212:215], a[196:199], v[164:167]\n"
"s_waitcnt lgkmcnt(2)\n"
"buffer_load_dwordx4 v240, s[20:23], s40 offen offset:1024 nt lds\n"
"v_mfma_f32_16x16x32_bf16 a[216:219], v[124:127], a[244:247], a[216:219]\n"
"v_cvt_pk_bf16_f32 v142, v172, v173\n"
"v_cvt_pk_bf16_f32 v143, v174, v175\n"
"v_mfma_f32_16x16x32_bf16 v[8:11], v[128:131], a[244:247], v[8:11]\n"
"v_exp_f32 v176, v176\n"
"v_mfma_f32_16x16x32_bf16 v[8:11], v[132:135], a[248:251], v[8:11]\n"
"v_exp_f32 v155, v155\n"
"v_mfma_f32_16x16x32_bf16 v[40:43], v[136:139], a[244:247], v[40:43]\n"
"v_exp_f32 v152, v152\n"
"v_mfma_f32_16x16x32_bf16 v[40:43], v[140:143], a[248:251], v[40:43]\n"
"ds_read_b128 v[212:215], v229 offset:20480\n"
"v_cvt_pk_bf16_f32 v150, v176, v177\n"
"v_mfma_f32_16x16x32_bf16 v[72:75], v[144:147], a[244:247], v[72:75]\n"
"v_exp_f32 v158, v158\n"
"v_mfma_f32_16x16x32_bf16 v[72:75], v[148:151], a[248:251], v[72:75]\n"
"v_exp_f32 v163, v163\n"
"v_mfma_f32_16x16x32_bf16 v[76:79], v[148:151], v[224:227], v[76:79]\n"
"ds_read_b128 v[220:223], v229 offset:21504\n"
"v_mfma_f32_16x16x32_bf16 v[76:79], v[144:147], a[252:255], v[76:79]\n"
"v_exp_f32 v159, v159\n"
"v_mfma_f32_16x16x32_bf16 v[44:47], v[140:143], v[224:227], v[44:47]\n"
"s_add_u32 s40, s56, s53\n"
"ds_read_b64_tr_b16 a[244:245], v234 offset:1024\n"
"v_mfma_f32_16x16x32_bf16 v[44:47], v[136:139], a[252:255], v[44:47]\n"
"buffer_load_dwordx4 v240, s[20:23], s40 offen offset:2048 nt lds\n"
"v_mfma_f32_16x16x32_bf16 v[12:15], v[132:135], v[224:227], v[12:15]\n"
"v_exp_f32 v153, v153\n"
"v_mfma_f32_16x16x32_bf16 v[12:15], v[128:131], a[252:255], v[12:15]\n"
"v_exp_f32 v165, v165\n"
"v_mfma_f32_16x16x32_bf16 a[220:223], v[124:127], a[252:255], a[220:223]\n"
"v_exp_f32 v164, v164\n"
"v_smfmac_f32_16x16x64_bf16 v[196:199], a[240:243], v[128:135], v241\n"
"v_cvt_pk_bf16_f32 v97, v154, v155\n"
"v_mfma_f32_16x16x32_bf16 v[202:205], a[240:243], v[124:127], v[202:205]\n"
"s_waitcnt lgkmcnt(1)\n"
"ds_read_b64_tr_b16 a[254:255], v235 offset:5120\n"
"v_mfma_f32_16x16x32_bf16 v[168:171], v[216:219], a[8:11], v[180:183]\n"
"ds_read_b64_tr_b16 a[248:249], v238 offset:1024\n"
"v_mfma_f32_16x16x32_bf16 v[168:171], v[220:223], a[12:15], v[168:171]\n"
"ds_read_b64_tr_b16 a[246:247], v234 offset:5120\n"
"v_mfma_f32_16x16x32_bf16 v[168:171], v[208:211], a[0:3], v[168:171]\n"
"ds_read_b64_tr_b16 v[224:225], v239 offset:1024\n"
"v_cvt_pk_bf16_f32 v120, v164, v165\n"
"v_mfma_f32_16x16x32_bf16 v[168:171], v[212:215], a[4:7], v[168:171]\n"
"v_exp_f32 v166, v166\n"
"v_mfma_f32_16x16x32_bf16 v[172:175], v[208:211], a[16:19], v[184:187]\n"
"ds_read_b64_tr_b16 a[252:253], v235 offset:1024\n"
"v_cvt_pk_bf16_f32 v96, v152, v153\n"
"v_mfma_f32_16x16x32_bf16 v[172:175], v[212:215], a[20:23], v[172:175]\n"
"ds_read_b64_tr_b16 v[226:227], v239 offset:5120\n"
"v_cvt_pk_bf16_f32 v105, v158, v159\n"
"v_mfma_f32_16x16x32_bf16 v[172:175], v[216:219], a[24:27], v[172:175]\n"
"s_add_u32 s40, s56, s54\n"
"ds_read_b64_tr_b16 a[250:251], v238 offset:5120\n"
"v_mfma_f32_16x16x32_bf16 v[172:175], v[220:223], a[28:31], v[172:175]\n"
"buffer_load_dwordx4 v240, s[20:23], s40 offen offset:3072 nt lds\n"
"v_mfma_f32_16x16x32_bf16 v[176:179], v[216:219], a[40:43], v[188:191]\n"
"s_add_u32 s55, s55, s59\n"
"v_exp_f32 v167, v167\n"
"v_mfma_f32_16x16x32_bf16 v[176:179], v[220:223], a[44:47], v[176:179]\n"
"s_add_u32 s41, s55, s57\n"
"v_exp_f32 v161, v161\n"
"v_mfma_f32_16x16x32_bf16 v[176:179], v[208:211], a[32:35], v[176:179]\n"
"s_and_b32 s41, s41, s58\n"
"v_exp_f32 v157, v157\n"
"v_mfma_f32_16x16x32_bf16 v[176:179], v[212:215], a[36:39], v[176:179]\n"
"s_andn2_b32 s56, s55, s58\n"
"v_exp_f32 v160, v160\n"
"v_mfma_f32_16x16x32_bf16 v[152:155], v[208:211], a[192:195], v[192:195]\n"
"s_or_b32 s56, s56, s41\n"
"ds_read_b128 v[208:211], v230 offset:16384\n"
"v_mfma_f32_16x16x32_bf16 v[152:155], v[212:215], a[196:199], v[152:155]\n"
"v_exp_f32 v172, v172\n"
"v_mfma_f32_16x16x32_bf16 v[152:155], v[216:219], a[200:203], v[152:155]\n"
"ds_read_b128 v[216:219], v230 offset:17408\n"
"v_mfma_f32_16x16x32_bf16 v[152:155], v[220:223], a[204:207], v[152:155]\n"
"s_waitcnt lgkmcnt(2)\n"
"v_exp_f32 v162, v162\n"
"v_mfma_f32_16x16x32_bf16 a[232:235], v[124:127], a[244:247], a[232:235]\n"
"ds_read_b128 v[212:215], v231 offset:16384\n"
"v_mfma_f32_16x16x32_bf16 v[24:27], v[128:131], a[244:247], v[24:27]\n"
"s_add_u32 m0, s44, 49152\n"
"ds_read_b128 v[220:223], v231 offset:17408\n"
"v_mfma_f32_16x16x32_bf16 v[24:27], v[132:135], a[248:251], v[24:27]\n"
"buffer_load_dwordx4 v240, s[16:19], s56 offen nt lds\n"
"v_mfma_f32_16x16x32_bf16 v[56:59], v[136:139], a[244:247], v[56:59]\n"
"v_exp_f32 v171, v171\n"
"v_mfma_f32_16x16x32_bf16 v[56:59], v[140:143], a[248:251], v[56:59]\n"
"v_exp_f32 v169, v169\n"
"v_mfma_f32_16x16x32_bf16 v[88:91], v[144:147], a[244:247], v[88:91]\n"
"v_exp_f32 v154, v154\n"
"v_mfma_f32_16x16x32_bf16 v[88:91], v[148:151], a[248:251], v[88:91]\n"
"v_exp_f32 v156, v156\n"
"v_mfma_f32_16x16x32_bf16 v[92:95], v[148:151], v[224:227], v[92:95]\n"
"ds_read_b64_tr_b16 a[244:245], v232 offset:0\n"
"v_cvt_pk_bf16_f32 v113, v162, v163\n"
"v_mfma_f32_16x16x32_bf16 v[92:95], v[144:147], a[252:255], v[92:95]\n"
"ds_read_b64_tr_b16 a[250:251], v236 offset:4096\n"
"v_cvt_pk_bf16_f32 v104, v156, v157\n"
"v_mfma_f32_16x16x32_bf16 v[60:63], v[140:143], v[224:227], v[60:63]\n"
"ds_read_b64_tr_b16 a[246:247], v232 offset:4096\n"
"v_cvt_pk_bf16_f32 v112, v160, v161\n"
"v_mfma_f32_16x16x32_bf16 v[60:63], v[136:139], a[252:255], v[60:63]\n"
"v_exp_f32 v152, v152\n"
"v_mfma_f32_16x16x32_bf16 v[28:31], v[132:135], v[224:227], v[28:31]\n"
"ds_read_b64_tr_b16 v[224:225], v237 offset:0\n"
"ds_read_b64_tr_b16 v[226:227], v237 offset:4096\n"
"v_mfma_f32_16x16x32_bf16 v[28:31], v[128:131], a[252:255], v[28:31]\n"
"ds_read_b64_tr_b16 a[248:249], v236 offset:0\n"
"v_cvt_pk_bf16_f32 v121, v166, v167\n"
"v_mfma_f32_16x16x32_bf16 a[236:239], v[124:127], a[252:255], a[236:239]\n"
"s_add_u32 s40, s56, s52\n"
"ds_read_b64_tr_b16 a[252:253], v233 offset:0\n"
"v_smfmac_f32_16x16x64_bf16 v[198:201], a[240:243], v[136:143], v241\n"
"s_waitcnt lgkmcnt(7)\n"
"buffer_load_dwordx4 v240, s[16:19], s40 offen offset:1024 nt lds\n"
"v_mfma_f32_16x16x32_bf16 v[156:159], v[208:211], a[0:3], v[180:183]\n"
"v_exp_f32 v175, v175\n"
"v_mfma_f32_16x16x32_bf16 v[156:159], v[212:215], a[4:7], v[156:159]\n"
"v_exp_f32 v173, v173\n"
"v_mfma_f32_16x16x32_bf16 v[156:159], v[216:219], a[8:11], v[156:159]\n"
"v_exp_f32 v168, v168\n"
"v_mfma_f32_16x16x32_bf16 v[156:159], v[220:223], a[12:15], v[156:159]\n"
"ds_read_b64_tr_b16 a[254:255], v233 offset:4096\n"
"v_mfma_f32_16x16x32_bf16 v[160:163], v[216:219], a[24:27], v[184:187]\n"
"v_exp_f32 v170, v170\n"
"v_mfma_f32_16x16x32_bf16 v[160:163], v[220:223], a[28:31], v[160:163]\n"
"v_exp_f32 v178, v178\n"
"v_mfma_f32_16x16x32_bf16 v[160:163], v[208:211], a[16:19], v[160:163]\n"
"v_exp_f32 v177, v177\n"
"v_mfma_f32_16x16x32_bf16 v[160:163], v[212:215], a[20:23], v[160:163]\n"
"v_exp_f32 v176, v176\n"
"v_mfma_f32_16x16x32_bf16 v[164:167], v[208:211], a[32:35], v[188:191]\n"
"ds_read_b128 v[208:211], v230 offset:20480\n"
"v_cvt_pk_bf16_f32 v99, v170, v171\n"
"v_mfma_f32_16x16x32_bf16 v[164:167], v[212:215], a[36:39], v[164:167]\n"
"ds_read_b128 v[212:215], v231 offset:20480\n"
"v_mfma_f32_16x16x32_bf16 v[164:167], v[216:219], a[40:43], v[164:167]\n"
"v_exp_f32 v153, v153\n"
"v_mfma_f32_16x16x32_bf16 v[164:167], v[220:223], a[44:47], v[164:167]\n"
"s_waitcnt lgkmcnt(2)\n"
"ds_read_b128 v[220:223], v231 offset:21504\n"
"v_cvt_pk_bf16_f32 v106, v172, v173\n"
"v_mfma_f32_16x16x32_bf16 a[208:211], v[124:127], a[244:247], a[208:211]\n"
"v_exp_f32 v174, v174\n"
"v_mfma_f32_16x16x32_bf16 v[0:3], v[128:131], a[244:247], v[0:3]\n"
"s_add_u32 s40, s56, s53\n"
"ds_read_b128 v[216:219], v230 offset:21504\n"
"v_mfma_f32_16x16x32_bf16 v[0:3], v[132:135], a[248:251], v[0:3]\n"
"buffer_load_dwordx4 v240, s[16:19], s40 offen offset:2048 nt lds\n"
"v_mfma_f32_16x16x32_bf16 v[32:35], v[136:139], a[244:247], v[32:35]\n"
"v_exp_f32 v163, v163\n"
"v_mfma_f32_16x16x32_bf16 v[32:35], v[140:143], a[248:251], v[32:35]\n"
"v_exp_f32 v161, v161\n"
"v_mfma_f32_16x16x32_bf16 v[64:67], v[144:147], a[244:247], v[64:67]\n"
"v_exp_f32 v155, v155\n"
"v_mfma_f32_16x16x32_bf16 v[64:67], v[148:151], a[248:251], v[64:67]\n"
"v_exp_f32 v166, v166\n"
"v_mfma_f32_16x16x32_bf16 v[68:71], v[148:151], v[224:227], v[68:71]\n"
"ds_read_b64_tr_b16 a[250:251], v236 offset:5120\n"
"v_cvt_pk_bf16_f32 v107, v174, v175\n"
"v_mfma_f32_16x16x32_bf16 v[68:71], v[144:147], a[252:255], v[68:71]\n"
"ds_read_b64_tr_b16 a[246:247], v232 offset:5120\n"
"v_cvt_pk_bf16_f32 v98, v168, v169\n"
"v_mfma_f32_16x16x32_bf16 v[36:39], v[140:143], v[224:227], v[36:39]\n"
"v_exp_f32 v179, v179\n"
"v_mfma_f32_16x16x32_bf16 v[36:39], v[136:139], a[252:255], v[36:39]\n"
"ds_read_b64_tr_b16 a[244:245], v232 offset:1024\n"
"v_cvt_pk_bf16_f32 v122, v152, v153\n"
"v_mfma_f32_16x16x32_bf16 v[4:7], v[132:135], v[224:227], v[4:7]\n"
"s_add_u32 s40, s56, s54\n"
"ds_read_b64_tr_b16 a[248:249], v236 offset:1024\n"
"v_mfma_f32_16x16x32_bf16 v[4:7], v[128:131], a[252:255], v[4:7]\n"
"buffer_load_dwordx4 v240, s[16:19], s40 offen offset:3072 nt lds\n"
"v_mfma_f32_16x16x32_bf16 a[212:215], v[124:127], a[252:255], a[212:215]\n"
"v_exp_f32 v162, v162\n"
"v_smfmac_f32_16x16x64_bf16 v[200:203], a[240:243], v[144:151], v241\n"
"s_waitcnt lgkmcnt(4)\n"
"v_cvt_pk_bf16_f32 v114, v176, v177\n"
"v_mfma_f32_16x16x32_bf16 v[168:171], v[208:211], a[0:3], v[180:183]\n"
"v_exp_f32 v157, v157\n"
"v_mfma_f32_16x16x32_bf16 v[168:171], v[212:215], a[4:7], v[168:171]\n"
"v_exp_f32 v165, v165\n"
"v_mfma_f32_16x16x32_bf16 v[168:171], v[216:219], a[8:11], v[168:171]\n"
"ds_read_b64_tr_b16 a[254:255], v233 offset:5120\n"
"ds_read_b64_tr_b16 v[226:227], v237 offset:5120\n"
"v_mfma_f32_16x16x32_bf16 v[168:171], v[220:223], a[12:15], v[168:171]\n"
"ds_read_b64_tr_b16 a[252:253], v233 offset:1024\n"
"v_cvt_pk_bf16_f32 v115, v178, v179\n"
"v_mfma_f32_16x16x32_bf16 v[172:175], v[216:219], a[24:27], v[184:187]\n"
"ds_read_b64_tr_b16 v[224:225], v237 offset:1024\n"
"v_mfma_f32_16x16x32_bf16 v[172:175], v[220:223], a[28:31], v[172:175]\n"
"v_exp_f32 v160, v160\n"
"v_mfma_f32_16x16x32_bf16 v[172:175], v[208:211], a[16:19], v[172:175]\n"
"v_exp_f32 v158, v158\n"
"v_mfma_f32_16x16x32_bf16 v[172:175], v[212:215], a[20:23], v[172:175]\n"
"s_cbranch_vccnz LPCL1_%=\nLPKL1_%=:\n"
"v_mfma_f32_16x16x32_bf16 v[176:179], v[208:211], a[32:35], v[188:191]\n"
"v_exp_f32 v159, v159\n"
"v_mfma_f32_16x16x32_bf16 v[176:179], v[212:215], a[36:39], v[176:179]\n"
"v_exp_f32 v167, v167\n"
"v_mfma_f32_16x16x32_bf16 v[176:179], v[216:219], a[40:43], v[176:179]\n"
"v_exp_f32 v164, v164\n"
"v_mfma_f32_16x16x32_bf16 v[176:179], v[220:223], a[44:47], v[176:179]\n"
"s_waitcnt lgkmcnt(1)\n"
"v_mfma_f32_16x16x32_bf16 a[224:227], v[124:127], a[244:247], a[224:227]\n"
"v_exp_f32 v169, v169\n"
"v_mfma_f32_16x16x32_bf16 v[16:19], v[128:131], a[244:247], v[16:19]\n"
"s_waitcnt vmcnt(8)\n"
"v_exp_f32 v175, v175\n"
"v_mfma_f32_16x16x32_bf16 v[16:19], v[132:135], a[248:251], v[16:19]\n"
"s_barrier\n"
"ds_read_b128 v[220:223], v229 offset:33792\n"
"v_mfma_f32_16x16x32_bf16 v[48:51], v[136:139], a[244:247], v[48:51]\n"
"ds_read_b128 v[216:219], v228 offset:33792\n"
"v_cvt_pk_bf16_f32 v109, v162, v163\n"
"v_mfma_f32_16x16x32_bf16 v[48:51], v[140:143], a[248:251], v[48:51]\n"
"ds_read_b128 v[212:215], v229 offset:32768\n"
"v_mfma_f32_16x16x32_bf16 v[80:83], v[144:147], a[244:247], v[80:83]\n"
"ds_read_b128 v[208:211], v228 offset:32768\n"
"v_cvt_pk_bf16_f32 v101, v158, v159\n"
"v_mfma_f32_16x16x32_bf16 v[80:83], v[148:151], a[248:251], v[80:83]\n"
"s_waitcnt lgkmcnt(4)\n"
"v_exp_f32 v178, v178\n"
"v_mfma_f32_16x16x32_bf16 v[84:87], v[148:151], v[224:227], v[84:87]\n"
"ds_read_b64_tr_b16 a[244:245], v234 offset:16384\n"
"v_cvt_pk_bf16_f32 v117, v166, v167\n"
"v_mfma_f32_16x16x32_bf16 v[84:87], v[144:147], a[252:255], v[84:87]\n"
"ds_read_b64_tr_b16 a[250:251], v238 offset:20480\n"
"v_cvt_pk_bf16_f32 v123, v154, v155\n"
"v_mfma_f32_16x16x32_bf16 v[52:55], v[140:143], v[224:227], v[52:55]\n"
"v_exp_f32 v173, v173\n"
"v_mfma_f32_16x16x32_bf16 v[52:55], v[136:139], a[252:255], v[52:55]\n"
"ds_read_b64_tr_b16 a[248:249], v238 offset:16384\n"
"v_mfma_f32_16x16x32_bf16 v[20:23], v[132:135], v[224:227], v[20:23]\n"
"v_exp_f32 v156, v156\n"
"v_mfma_f32_16x16x32_bf16 v[20:23], v[128:131], a[252:255], v[20:23]\n"
"ds_read_b64_tr_b16 v[226:227], v239 offset:20480\n"
"v_cvt_pk_bf16_f32 v100, v156, v157\n"
"v_mfma_f32_16x16x32_bf16 a[228:231], v[124:127], a[252:255], a[228:231]\n"
"s_waitcnt lgkmcnt(4)\n"
"ds_read_b64_tr_b16 a[246:247], v234 offset:20480\n"
"LAY2_%=:\n"
"v_mfma_f32_16x16x32_bf16 v[152:155], v[208:211], a[0:3], v[180:183]\n"
"ds_read_b64_tr_b16 v[224:225], v239 offset:16384\n"
"v_cvt_pk_bf16_f32 v116, v164, v165\n"
"v_mfma_f32_16x16x32_bf16 v[152:155], v[212:215], a[4:7], v[152:155]\n"
"ds_read_b64_tr_b16 a[252:253], v235 offset:16384\n"
"v_cvt_pk_bf16_f32 v108, v160, v161\n"
"v_mfma_f32_16x16x32_bf16 v[152:155], v[216:219], a[8:11], v[152:155]\n"
"s_add_u32 m0, s44, 114688\n"
"ds_read_b64_tr_b16 a[254:255], v235 offset:20480\n"
"v_mfma_f32_16x16x32_bf16 v[152:155], v[220:223], a[12:15], v[152:155]\n"
"buffer_load_dwordx4 v240, s[20:23], s56 offen nt lds\n"
"v_mfma_f32_16x16x32_bf16 v[156:159], v[216:219], a[24:27], v[184:187]\n"
"v_exp_f32 v177, v177\n"
"v_mfma_f32_16x16x32_bf16 v[156:159], v[220:223], a[28:31], v[156:159]\n"
"v_exp_f32 v168, v168\n"
"v_mfma_f32_16x16x32_bf16 v[156:159], v[208:211], a[16:19], v[156:159]\n"
"v_exp_f32 v179, v179\n"
"v_mfma_f32_16x16x32_bf16 v[156:159], v[212:215], a[20:23], v[156:159]\n"
"v_exp_f32 v174, v174\n"
"v_mfma_f32_16x16x32_bf16 v[160:163], v[208:211], a[32:35], v[188:191]\n"
"v_exp_f32 v172, v172\n"
"v_mfma_f32_16x16x32_bf16 v[160:163], v[212:215], a[36:39], v[160:163]\n"
"v_exp_f32 v171, v171\n"
"v_mfma_f32_16x16x32_bf16 v[160:163], v[216:219], a[40:43], v[160:163]\n"
"v_exp_f32 v154, v154\n"
"v_mfma_f32_16x16x32_bf16 v[160:163], v[220:223], a[44:47], v[160:163]\n"
"v_exp_f32 v170, v170\n"
"v_mfma_f32_16x16x32_bf16 v[164:167], v[216:219], a[200:203], v[192:195]\n"
"ds_read_b128 v[216:219], v228 offset:37888\n"
"v_cvt_pk_bf16_f32 v103, v170, v171\n"
"v_mfma_f32_16x16x32_bf16 v[164:167], v[220:223], a[204:207], v[164:167]\n"
"v_cvt_pk_bf16_f32 v102, v168, v169\n"
"v_cvt_pk_bf16_f32 v119, v178, v179\n"
"v_mfma_f32_16x16x32_bf16 v[164:167], v[208:211], a[192:195], v[164:167]\n"
"s_add_u32 s40, s56, s52\n"
"ds_read_b128 v[208:211], v228 offset:36864\n"
"v_mfma_f32_16x16x32_bf16 v[164:167], v[212:215], a[196:199], v[164:167]\n"
"s_waitcnt lgkmcnt(2)\n"
"buffer_load_dwordx4 v240, s[20:23], s40 offen offset:1024 nt lds\n"
"v_mfma_f32_16x16x32_bf16 a[216:219], v[120:123], a[244:247], a[216:219]\n"
"v_cvt_pk_bf16_f32 v110, v172, v173\n"
"v_cvt_pk_bf16_f32 v111, v174, v175\n"
"v_mfma_f32_16x16x32_bf16 v[8:11], v[96:99], a[244:247], v[8:11]\n"
"v_exp_f32 v176, v176\n"
"v_mfma_f32_16x16x32_bf16 v[8:11], v[100:103], a[248:251], v[8:11]\n"
"v_exp_f32 v155, v155\n"
"v_mfma_f32_16x16x32_bf16 v[40:43], v[104:107], a[244:247], v[40:43]\n"
"v_exp_f32 v152, v152\n"
"v_mfma_f32_16x16x32_bf16 v[40:43], v[108:111], a[248:251], v[40:43]\n"
"ds_read_b128 v[212:215], v229 offset:36864\n"
"v_cvt_pk_bf16_f32 v118, v176, v177\n"
"v_mfma_f32_16x16x32_bf16 v[72:75], v[112:115], a[244:247], v[72:75]\n"
"v_exp_f32 v158, v158\n"
"v_mfma_f32_16x16x32_bf16 v[72:75], v[116:119], a[248:251], v[72:75]\n"
"v_exp_f32 v163, v163\n"
"v_mfma_f32_16x16x32_bf16 v[76:79], v[116:119], v[224:227], v[76:79]\n"
"ds_read_b128 v[220:223], v229 offset:37888\n"
"v_mfma_f32_16x16x32_bf16 v[76:79], v[112:115], a[252:255], v[76:79]\n"
"v_exp_f32 v159, v159\n"
"v_mfma_f32_16x16x32_bf16 v[44:47], v[108:111], v[224:227], v[44:47]\n"
"s_add_u32 s40, s56, s53\n"
"ds_read_b64_tr_b16 a[244:245], v234 offset:17408\n"
"v_mfma_f32_16x16x32_bf16 v[44:47], v[104:107], a[252:255], v[44:47]\n"
"buffer_load_dwordx4 v240, s[20:23], s40 offen offset:2048 nt lds\n"
"v_mfma_f32_16x16x32_bf16 v[12:15], v[100:103], v[224:227], v[12:15]\n"
"v_exp_f32 v153, v153\n"
"v_mfma_f32_16x16x32_bf16 v[12:15], v[96:99], a[252:255], v[12:15]\n"
"v_exp_f32 v165, v165\n"
"v_mfma_f32_16x16x32_bf16 a[220:223], v[120:123], a[252:255], a[220:223]\n"
"v_exp_f32 v164, v164\n"
"v_smfmac_f32_16x16x64_bf16 v[196:199], a[240:243], v[96:103], v241\n"
"v_cvt_pk_bf16_f32 v129, v154, v155\n"
"v_mfma_f32_16x16x32_bf16 v[202:205], a[240:243], v[120:123], v[202:205]\n"
"s_waitcnt lgkmcnt(1)\n"
"ds_read_b64_tr_b16 a[254:255], v235 offset:21504\n"
"v_mfma_f32_16x16x32_bf16 v[168:171], v[216:219], a[8:11], v[180:183]\n"
"ds_read_b64_tr_b16 a[248:249], v238 offset:17408\n"
"v_mfma_f32_16x16x32_bf16 v[168:171], v[220:223], a[12:15], v[168:171]\n"
"ds_read_b64_tr_b16 a[246:247], v234 offset:21504\n"
"v_mfma_f32_16x16x32_bf16 v[168:171], v[208:211], a[0:3], v[168:171]\n"
"ds_read_b64_tr_b16 v[224:225], v239 offset:17408\n"
"v_cvt_pk_bf16_f32 v124, v164, v165\n"
"v_mfma_f32_16x16x32_bf16 v[168:171], v[212:215], a[4:7], v[168:171]\n"
"v_exp_f32 v166, v166\n"
"v_mfma_f32_16x16x32_bf16 v[172:175], v[208:211], a[16:19], v[184:187]\n"
"ds_read_b64_tr_b16 a[252:253], v235 offset:17408\n"
"v_cvt_pk_bf16_f32 v128, v152, v153\n"
"v_mfma_f32_16x16x32_bf16 v[172:175], v[212:215], a[20:23], v[172:175]\n"
"ds_read_b64_tr_b16 v[226:227], v239 offset:21504\n"
"v_cvt_pk_bf16_f32 v137, v158, v159\n"
"v_mfma_f32_16x16x32_bf16 v[172:175], v[216:219], a[24:27], v[172:175]\n"
"s_add_u32 s40, s56, s54\n"
"ds_read_b64_tr_b16 a[250:251], v238 offset:21504\n"
"v_mfma_f32_16x16x32_bf16 v[172:175], v[220:223], a[28:31], v[172:175]\n"
"buffer_load_dwordx4 v240, s[20:23], s40 offen offset:3072 nt lds\n"
"v_mfma_f32_16x16x32_bf16 v[176:179], v[216:219], a[40:43], v[188:191]\n"
"s_add_u32 s55, s55, s59\n"
"v_exp_f32 v167, v167\n"
"v_mfma_f32_16x16x32_bf16 v[176:179], v[220:223], a[44:47], v[176:179]\n"
"s_add_u32 s41, s55, s57\n"
"v_exp_f32 v161, v161\n"
"v_mfma_f32_16x16x32_bf16 v[176:179], v[208:211], a[32:35], v[176:179]\n"
"s_and_b32 s41, s41, s58\n"
"v_exp_f32 v157, v157\n"
"v_mfma_f32_16x16x32_bf16 v[176:179], v[212:215], a[36:39], v[176:179]\n"
"s_andn2_b32 s56, s55, s58\n"
"v_exp_f32 v160, v160\n"
"v_mfma_f32_16x16x32_bf16 v[152:155], v[208:211], a[192:195], v[192:195]\n"
"s_or_b32 s56, s56, s41\n"
"ds_read_b128 v[208:211], v230 offset:32768\n"
"v_mfma_f32_16x16x32_bf16 v[152:155], v[212:215], a[196:199], v[152:155]\n"
"s_cmp_ge_u32 s47, 15\n"
"v_exp_f32 v172, v172\n"
"v_mfma_f32_16x16x32_bf16 v[152:155], v[216:219], a[200:203], v[152:155]\n"
"s_cbranch_scc1 LSW_%=\nLSWK_%=:\n"
"ds_read_b128 v[216:219], v230 offset:33792\n"
"v_mfma_f32_16x16x32_bf16 v[152:155], v[220:223], a[204:207], v[152:155]\n"
"s_waitcnt lgkmcnt(2)\n"
"v_exp_f32 v162, v162\n"
"v_mfma_f32_16x16x32_bf16 a[232:235], v[120:123], a[244:247], a[232:235]\n"
"ds_read_b128 v[212:215], v231 offset:32768\n"
"v_mfma_f32_16x16x32_bf16 v[24:27], v[96:99], a[244:247], v[24:27]\n"
"s_mov_b32 m0, s44\n"
"ds_read_b128 v[220:223], v231 offset:33792\n"
"v_mfma_f32_16x16x32_bf16 v[24:27], v[100:103], a[248:251], v[24:27]\n"
"buffer_load_dwordx4 v240, s[16:19], s56 offen nt lds\n"
"v_mfma_f32_16x16x32_bf16 v[56:59], v[104:107], a[244:247], v[56:59]\n"
"v_exp_f32 v171, v171\n"
"v_mfma_f32_16x16x32_bf16 v[56:59], v[108:111], a[248:251], v[56:59]\n"
"v_exp_f32 v169, v169\n"
"v_mfma_f32_16x16x32_bf16 v[88:91], v[112:115], a[244:247], v[88:91]\n"
"v_exp_f32 v154, v154\n"
"v_mfma_f32_16x16x32_bf16 v[88:91], v[116:119], a[248:251], v[88:91]\n"
"v_exp_f32 v156, v156\n"
"v_mfma_f32_16x16x32_bf16 v[92:95], v[116:119], v[224:227], v[92:95]\n"
"ds_read_b64_tr_b16 a[244:245], v232 offset:16384\n"
"v_cvt_pk_bf16_f32 v145, v162, v163\n"
"v_mfma_f32_16x16x32_bf16 v[92:95], v[112:115], a[252:255], v[92:95]\n"
"ds_read_b64_tr_b16 a[250:251], v236 offset:20480\n"
"v_cvt_pk_bf16_f32 v136, v156, v157\n"
"v_mfma_f32_16x16x32_bf16 v[60:63], v[108:111], v[224:227], v[60:63]\n"
"ds_read_b64_tr_b16 a[246:247], v232 offset:20480\n"
"v_cvt_pk_bf16_f32 v144, v160, v161\n"
"v_mfma_f32_16x16x32_bf16 v[60:63], v[104:107], a[252:255], v[60:63]\n"
"v_exp_f32 v152, v152\n"
"v_mfma_f32_16x16x32_bf16 v[28:31], v[100:103], v[224:227], v[28:31]\n"
"ds_read_b64_tr_b16 v[224:225], v237 offset:16384\n"
"ds_read_b64_tr_b16 v[226:227], v237 offset:20480\n"
"v_mfma_f32_16x16x32_bf16 v[28:31], v[96:99], a[252:255], v[28:31]\n"
"ds_read_b64_tr_b16 a[248:249], v236 offset:16384\n"
"v_cvt_pk_bf16_f32 v125, v166, v167\n"
"v_mfma_f32_16x16x32_bf16 a[236:239], v[120:123], a[252:255], a[236:239]\n"
"s_add_u32 s40, s56, s52\n"
"ds_read_b64_tr_b16 a[252:253], v233 offset:16384\n"
"v_smfmac_f32_16x16x64_bf16 v[198:201], a[240:243], v[104:111], v241\n"
"s_waitcnt lgkmcnt(7)\n"
"buffer_load_dwordx4 v240, s[16:19], s40 offen offset:1024 nt lds\n"
"v_mfma_f32_16x16x32_bf16 v[156:159], v[208:211], a[0:3], v[180:183]\n"
"v_exp_f32 v175, v175\n"
"v_mfma_f32_16x16x32_bf16 v[156:159], v[212:215], a[4:7], v[156:159]\n"
"v_exp_f32 v173, v173\n"
"v_mfma_f32_16x16x32_bf16 v[156:159], v[216:219], a[8:11], v[156:159]\n"
"v_exp_f32 v168, v168\n"
"v_mfma_f32_16x16x32_bf16 v[156:159], v[220:223], a[12:15], v[156:159]\n"
"ds_read_b64_tr_b16 a[254:255], v233 offset:20480\n"
"v_mfma_f32_16x16x32_bf16 v[160:163], v[216:219], a[24:27], v[184:187]\n"
"v_exp_f32 v170, v170\n"
"v_mfma_f32_16x16x32_bf16 v[160:163], v[220:223], a[28:31], v[160:163]\n"
"v_exp_f32 v178, v178\n"
"v_mfma_f32_16x16x32_bf16 v[160:163], v[208:211], a[16:19], v[160:163]\n"
"v_exp_f32 v177, v177\n"
"v_mfma_f32_16x16x32_bf16 v[160:163], v[212:215], a[20:23], v[160:163]\n"
"v_exp_f32 v176, v176\n"
"v_mfma_f32_16x16x32_bf16 v[164:167], v[208:211], a[32:35], v[188:191]\n"
"ds_read_b128 v[208:211], v230 offset:36864\n"
"v_cvt_pk_bf16_f32 v131, v170, v171\n"
"v_mfma_f32_16x16x32_bf16 v[164:167], v[212:215], a[36:39], v[164:167]\n"
"ds_read_b128 v[212:215], v231 offset:36864\n"
"v_mfma_f32_16x16x32_bf16 v[164:167], v[216:219], a[40:43], v[164:167]\n"
"v_exp_f32 v153, v153\n"
"v_mfma_f32_16x16x32_bf16 v[164:167], v[220:223], a[44:47], v[164:167]\n"
"s_waitcnt lgkmcnt(2)\n"
"ds_read_b128 v[220:223], v231 offset:37888\n"
"v_cvt_pk_bf16_f32 v138, v172, v173\n"
"v_mfma_f32_16x16x32_bf16 a[208:211], v[120:123], a[244:247], a[208:211]\n"
"v_exp_f32 v174, v174\n"
"v_mfma_f32_16x16x32_bf16 v[0:3], v[96:99], a[244:247], v[0:3]\n"
"s_add_u32 s40, s56, s53\n"
"ds_read_b128 v[216:219], v230 offset:37888\n"
"v_mfma_f32_16x16x32_bf16 v[0:3], v[100:103], a[248:251], v[0:3]\n"
"buffer_load_dwordx4 v240, s[16:19], s40 offen offset:2048 nt lds\n"
"v_mfma_f32_16x16x32_bf16 v[32:35], v[104:107], a[244:247], v[32:35]\n"
"v_exp_f32 v163, v163\n"
"v_mfma_f32_16x16x32_bf16 v[32:35], v[108:111], a[248:251], v[32:35]\n"
"v_exp_f32 v161, v161\n"
"v_mfma_f32_16x16x32_bf16 v[64:67], v[112:115], a[244:247], v[64:67]\n"
"v_exp_f32 v155, v155\n"
"v_mfma_f32_16x16x32_bf16 v[64:67], v[116:119], a[248:251], v[64:67]\n"
"v_exp_f32 v166, v166\n"
"v_mfma_f32_16x16x32_bf16 v[68:71], v[116:119], v[224:227], v[68:71]\n"
"ds_read_b64_tr_b16 a[250:251], v236 offset:21504\n"
"v_cvt_pk_bf16_f32 v139, v174, v175\n"
"v_mfma_f32_16x16x32_bf16 v[68:71], v[112:115], a[252:255], v[68:71]\n"
"ds_read_b64_tr_b16 a[246:247], v232 offset:21504\n"
"v_cvt_pk_bf16_f32 v130, v168, v169\n"
"v_mfma_f32_16x16x32_bf16 v[36:39], v[108:111], v[224:227], v[36:39]\n"
"v_exp_f32 v179, v179\n"
"v_mfma_f32_16x16x32_bf16 v[36:39], v[104:107], a[252:255], v[36:39]\n"
"ds_read_b64_tr_b16 a[244:245], v232 offset:17408\n"
"v_cvt_pk_bf16_f32 v126, v152, v153\n"
"v_mfma_f32_16x16x32_bf16 v[4:7], v[100:103], v[224:227], v[4:7]\n"
"s_add_u32 s40, s56, s54\n"
"ds_read_b64_tr_b16 a[248:249], v236 offset:17408\n"
"v_mfma_f32_16x16x32_bf16 v[4:7], v[96:99], a[252:255], v[4:7]\n"
"buffer_load_dwordx4 v240, s[16:19], s40 offen offset:3072 nt lds\n"
"v_mfma_f32_16x16x32_bf16 a[212:215], v[120:123], a[252:255], a[212:215]\n"
"v_exp_f32 v162, v162\n"
"v_smfmac_f32_16x16x64_bf16 v[200:203], a[240:243], v[112:119], v241\n"
"s_waitcnt lgkmcnt(4)\n"
"v_cvt_pk_bf16_f32 v146, v176, v177\n"
"v_mfma_f32_16x16x32_bf16 v[168:171], v[208:211], a[0:3], v[180:183]\n"
"v_exp_f32 v157, v157\n"
"v_mfma_f32_16x16x32_bf16 v[168:171], v[212:215], a[4:7], v[168:171]\n"
"v_exp_f32 v165, v165\n"
"v_mfma_f32_16x16x32_bf16 v[168:171], v[216:219], a[8:11], v[168:171]\n"
"ds_read_b64_tr_b16 a[254:255], v233 offset:21504\n"
"ds_read_b64_tr_b16 v[226:227], v237 offset:21504\n"
"v_mfma_f32_16x16x32_bf16 v[168:171], v[220:223], a[12:15], v[168:171]\n"
"ds_read_b64_tr_b16 a[252:253], v233 offset:17408\n"
"v_cvt_pk_bf16_f32 v147, v178, v179\n"
"v_mfma_f32_16x16x32_bf16 v[172:175], v[216:219], a[24:27], v[184:187]\n"
"ds_read_b64_tr_b16 v[224:225], v237 offset:17408\n"
"v_mfma_f32_16x16x32_bf16 v[172:175], v[220:223], a[28:31], v[172:175]\n"
"v_exp_f32 v160, v160\n"
"v_mfma_f32_16x16x32_bf16 v[172:175], v[208:211], a[16:19], v[172:175]\n"
"v_exp_f32 v158, v158\n"
"v_mfma_f32_16x16x32_bf16 v[172:175], v[212:215], a[20:23], v[172:175]\n"
"s_cbranch_vccnz LPCL2_%=\nLPKL2_%=:\n"
"v_mfma_f32_16x16x32_bf16 v[176:179], v[208:211], a[32:35], v[188:191]\n"
"v_exp_f32 v159, v159\n"
"v_mfma_f32_16x16x32_bf16 v[176:179], v[212:215], a[36:39], v[176:179]\n"
"v_exp_f32 v167, v167\n"
"v_mfma_f32_16x16x32_bf16 v[176:179], v[216:219], a[40:43], v[176:179]\n"
"v_exp_f32 v164, v164\n"
"v_mfma_f32_16x16x32_bf16 v[176:179], v[220:223], a[44:47], v[176:179]\n"
"s_waitcnt lgkmcnt(1)\n"
"v_mfma_f32_16x16x32_bf16 a[224:227], v[120:123], a[244:247], a[224:227]\n"
"v_exp_f32 v169, v169\n"
"v_mfma_f32_16x16x32_bf16 v[16:19], v[96:99], a[244:247], v[16:19]\n"
"s_waitcnt vmcnt(8)\n"
"v_exp_f32 v175, v175\n"
"v_mfma_f32_16x16x32_bf16 v[16:19], v[100:103], a[248:251], v[16:19]\n"
"s_barrier\n"
"ds_read_b128 v[220:223], v229 offset:50176\n"
"v_mfma_f32_16x16x32_bf16 v[48:51], v[104:107], a[244:247], v[48:51]\n"
"ds_read_b128 v[216:219], v228 offset:50176\n"
"v_cvt_pk_bf16_f32 v141, v162, v163\n"
"v_mfma_f32_16x16x32_bf16 v[48:51], v[108:111], a[248:251], v[48:51]\n"
"ds_read_b128 v[212:215], v229 offset:49152\n"
"v_mfma_f32_16x16x32_bf16 v[80:83], v[112:115], a[244:247], v[80:83]\n"
"ds_read_b128 v[208:211], v228 offset:49152\n"
"v_cvt_pk_bf16_f32 v133, v158, v159\n"
"v_mfma_f32_16x16x32_bf16 v[80:83], v[116:119], a[248:251], v[80:83]\n"
"s_waitcnt lgkmcnt(4)\n"
"v_exp_f32 v178, v178\n"
"v_mfma_f32_16x16x32_bf16 v[84:87], v[116:119], v[224:227], v[84:87]\n"
"ds_read_b64_tr_b16 a[244:245], v234 offset:32768\n"
"v_cvt_pk_bf16_f32 v149, v166, v167\n"
"v_mfma_f32_16x16x32_bf16 v[84:87], v[112:115], a[252:255], v[84:87]\n"
"ds_read_b64_tr_b16 a[250:251], v238 offset:36864\n"
"v_cvt_pk_bf16_f32 v127, v154, v155\n"
"v_mfma_f32_16x16x32_bf16 v[52:55], v[108:111], v[224:227], v[52:55]\n"
"v_exp_f32 v173, v173\n"
"v_mfma_f32_16x16x32_bf16 v[52:55], v[104:107], a[252:255], v[52:55]\n"
"ds_read_b64_tr_b16 a[248:249], v238 offset:32768\n"
"v_mfma_f32_16x16x32_bf16 v[20:23], v[100:103], v[224:227], v[20:23]\n"
"v_exp_f32 v156, v156\n"
"v_mfma_f32_16x16x32_bf16 v[20:23], v[96:99], a[252:255], v[20:23]\n"
"ds_read_b64_tr_b16 v[226:227], v239 offset:36864\n"
"v_cvt_pk_bf16_f32 v132, v156, v157\n"
"v_mfma_f32_16x16x32_bf16 a[228:231], v[120:123], a[252:255], a[228:231]\n"
"s_waitcnt lgkmcnt(4)\n"
"ds_read_b64_tr_b16 a[246:247], v234 offset:36864\n"
"v_mfma_f32_16x16x32_bf16 v[152:155], v[208:211], a[0:3], v[180:183]\n"
"ds_read_b64_tr_b16 v[224:225], v239 offset:32768\n"
"v_cvt_pk_bf16_f32 v148, v164, v165\n"
"v_mfma_f32_16x16x32_bf16 v[152:155], v[212:215], a[4:7], v[152:155]\n"
"ds_read_b64_tr_b16 a[252:253], v235 offset:32768\n"
"v_cvt_pk_bf16_f32 v140, v160, v161\n"
"v_mfma_f32_16x16x32_bf16 v[152:155], v[216:219], a[8:11], v[152:155]\n"
"s_add_u32 m0, s44, 65536\n"
"ds_read_b64_tr_b16 a[254:255], v235 offset:36864\n"
"v_mfma_f32_16x16x32_bf16 v[152:155], v[220:223], a[12:15], v[152:155]\n"
"buffer_load_dwordx4 v240, s[20:23], s56 offen nt lds\n"
"v_mfma_f32_16x16x32_bf16 v[156:159], v[216:219], a[24:27], v[184:187]\n"
"v_exp_f32 v177, v177\n"
"v_mfma_f32_16x16x32_bf16 v[156:159], v[220:223], a[28:31], v[156:159]\n"
"v_exp_f32 v168, v168\n"
"v_mfma_f32_16x16x32_bf16 v[156:159], v[208:211], a[16:19], v[156:159]\n"
"v_exp_f32 v179, v179\n"
"v_mfma_f32_16x16x32_bf16 v[156:159], v[212:215], a[20:23], v[156:159]\n"
"v_exp_f32 v174, v174\n"
"v_mfma_f32_16x16x32_bf16 v[160:163], v[208:211], a[32:35], v[188:191]\n"
"v_exp_f32 v172, v172\n"
"v_mfma_f32_16x16x32_bf16 v[160:163], v[212:215], a[36:39], v[160:163]\n"
"v_exp_f32 v171, v171\n"
"v_mfma_f32_16x16x32_bf16 v[160:163], v[216:219], a[40:43], v[160:163]\n"
"v_exp_f32 v154, v154\n"
"v_mfma_f32_16x16x32_bf16 v[160:163], v[220:223], a[44:47], v[160:163]\n"
"v_exp_f32 v170, v170\n"
"v_mfma_f32_16x16x32_bf16 v[164:167], v[216:219], a[200:203], v[192:195]\n"
"ds_read_b128 v[216:219], v228 offset:54272\n"
"v_cvt_pk_bf16_f32 v135, v170, v171\n"
"v_mfma_f32_16x16x32_bf16 v[164:167], v[220:223], a[204:207], v[164:167]\n"
"v_cvt_pk_bf16_f32 v134, v168, v169\n"
"v_cvt_pk_bf16_f32 v151, v178, v179\n"
"v_mfma_f32_16x16x32_bf16 v[164:167], v[208:211], a[192:195], v[164:167]\n"
"s_add_u32 s40, s56, s52\n"
"ds_read_b128 v[208:211], v228 offset:53248\n"
"v_mfma_f32_16x16x32_bf16 v[164:167], v[212:215], a[196:199], v[164:167]\n"
"s_waitcnt lgkmcnt(2)\n"
"buffer_load_dwordx4 v240, s[20:23], s40 offen offset:1024 nt lds\n"
"v_mfma_f32_16x16x32_bf16 a[216:219], v[124:127], a[244:247], a[216:219]\n"
"v_cvt_pk_bf16_f32 v142, v172, v173\n"
"v_cvt_pk_bf16_f32 v143, v174, v175\n"
"v_mfma_f32_16x16x32_bf16 v[8:11], v[128:131], a[244:247], v[8:11]\n"
"v_exp_f32 v176, v176\n"
"v_mfma_f32_16x16x32_bf16 v[8:11], v[132:135], a[248:251], v[8:11]\n"
"v_exp_f32 v155, v155\n"
"v_mfma_f32_16x16x32_bf16 v[40:43], v[136:139], a[244:247], v[40:43]\n"
"v_exp_f32 v152, v152\n"
"v_mfma_f32_16x16x32_bf16 v[40:43], v[140:143], a[248:251], v[40:43]\n"
"ds_read_b128 v[212:215], v229 offset:53248\n"
"v_cvt_pk_bf16_f32 v150, v176, v177\n"
"v_mfma_f32_16x16x32_bf16 v[72:75], v[144:147], a[244:247], v[72:75]\n"
"v_exp_f32 v158, v158\n"
"v_mfma_f32_16x16x32_bf16 v[72:75], v[148:151], a[248:251], v[72:75]\n"
"v_exp_f32 v163, v163\n"
"v_mfma_f32_16x16x32_bf16 v[76:79], v[148:151], v[224:227], v[76:79]\n"
"ds_read_b128 v[220:223], v229 offset:54272\n"
"v_mfma_f32_16x16x32_bf16 v[76:79], v[144:147], a[252:255], v[76:79]\n"
"v_exp_f32 v159, v159\n"
"v_mfma_f32_16x16x32_bf16 v[44:47], v[140:143], v[224:227], v[44:47]\n"
"s_add_u32 s40, s56, s53\n"
"ds_read_b64_tr_b16 a[244:245], v234 offset:33792\n"
"v_mfma_f32_16x16x32_bf16 v[44:47], v[136:139], a[252:255], v[44:47]\n"
"buffer_load_dwordx4 v240, s[20:23], s40 offen offset:2048 nt lds\n"
"v_mfma_f32_16x16x32_bf16 v[12:15], v[132:135], v[224:227], v[12:15]\n"
"v_exp_f32 v153, v153\n"
"v_mfma_f32_16x16x32_bf16 v[12:15], v[128:131], a[252:255], v[12:15]\n"
"v_exp_f32 v165, v165\n"
"v_mfma_f32_16x16x32_bf16 a[220:223], v[124:127], a[252:255], a[220:223]\n"
"v_exp_f32 v164, v164\n"
"v_smfmac_f32_16x16x64_bf16 v[196:199], a[240:243], v[128:135], v241\n"
"v_cvt_pk_bf16_f32 v97, v154, v155\n"
"v_mfma_f32_16x16x32_bf16 v[202:205], a[240:243], v[124:127], v[202:205]\n"
"s_waitcnt lgkmcnt(1)\n"
"ds_read_b64_tr_b16 a[254:255], v235 offset:37888\n"
"v_mfma_f32_16x16x32_bf16 v[168:171], v[216:219], a[8:11], v[180:183]\n"
"ds_read_b64_tr_b16 a[248:249], v238 offset:33792\n"
"v_mfma_f32_16x16x32_bf16 v[168:171], v[220:223], a[12:15], v[168:171]\n"
"ds_read_b64_tr_b16 a[246:247], v234 offset:37888\n"
"v_mfma_f32_16x16x32_bf16 v[168:171], v[208:211], a[0:3], v[168:171]\n"
"ds_read_b64_tr_b16 v[224:225], v239 offset:33792\n"
"v_cvt_pk_bf16_f32 v120, v164, v165\n"
"v_mfma_f32_16x16x32_bf16 v[168:171], v[212:215], a[4:7], v[168:171]\n"
"v_exp_f32 v166, v166\n"
"v_mfma_f32_16x16x32_bf16 v[172:175], v[208:211], a[16:19], v[184:187]\n"
"ds_read_b64_tr_b16 a[252:253], v235 offset:33792\n"
"v_cvt_pk_bf16_f32 v96, v152, v153\n"
"v_mfma_f32_16x16x32_bf16 v[172:175], v[212:215], a[20:23], v[172:175]\n"
"ds_read_b64_tr_b16 v[226:227], v239 offset:37888\n"
"v_cvt_pk_bf16_f32 v105, v158, v159\n"
"v_mfma_f32_16x16x32_bf16 v[172:175], v[216:219], a[24:27], v[172:175]\n"
"s_add_u32 s40, s56, s54\n"
"ds_read_b64_tr_b16 a[250:251], v238 offset:37888\n"
"v_mfma_f32_16x16x32_bf16 v[172:175], v[220:223], a[28:31], v[172:175]\n"
"buffer_load_dwordx4 v240, s[20:23], s40 offen offset:3072 nt lds\n"
"v_mfma_f32_16x16x32_bf16 v[176:179], v[216:219], a[40:43], v[188:191]\n"
"s_add_u32 s55, s55, s59\n"
"v_exp_f32 v167, v167\n"
"v_mfma_f32_16x16x32_bf16 v[176:179], v[220:223], a[44:47], v[176:179]\n"
"s_add_u32 s41, s55, s57\n"
"v_exp_f32 v161, v161\n"
"v_mfma_f32_16x16x32_bf16 v[176:179], v[208:211], a[32:35], v[176:179]\n"
"s_and_b32 s41, s41, s58\n"
"v_exp_f32 v157, v157\n"
"v_mfma_f32_16x16x32_bf16 v[176:179], v[212:215], a[36:39], v[176:179]\n"
"s_andn2_b32 s56, s55, s58\n"
"v_exp_f32 v160, v160\n"
"v_mfma_f32_16x16x32_bf16 v[152:155], v[208:211], a[192:195], v[192:195]\n"
"s_or_b32 s56, s56, s41\n"
"ds_read_b128 v[208:211], v230 offset:49152\n"
"v_mfma_f32_16x16x32_bf16 v[152:155], v[212:215], a[196:199], v[152:155]\n"
"v_exp_f32 v172, v172\n"
"v_mfma_f32_16x16x32_bf16 v[152:155], v[216:219], a[200:203], v[152:155]\n"
"ds_read_b128 v[216:219], v230 offset:50176\n"
"v_mfma_f32_16x16x32_bf16 v[152:155], v[220:223], a[204:207], v[152:155]\n"
"s_waitcnt lgkmcnt(2)\n"
"v_exp_f32 v162, v162\n"
"v_mfma_f32_16x16x32_bf16 a[232:235], v[124:127], a[244:247], a[232:235]\n"
"ds_read_b128 v[212:215], v231 offset:49152\n"
"v_mfma_f32_16x16x32_bf16 v[24:27], v[128:131], a[244:247], v[24:27]\n"
"s_add_u32 m0, s44, 16384\n"
"ds_read_b128 v[220:223], v231 offset:50176\n"
"v_mfma_f32_16x16x32_bf16 v[24:27], v[132:135], a[248:251], v[24:27]\n"
"buffer_load_dwordx4 v240, s[16:19], s56 offen nt lds\n"
"v_mfma_f32_16x16x32_bf16 v[56:59], v[136:139], a[244:247], v[56:59]\n"
"v_exp_f32 v171, v171\n"
"v_mfma_f32_16x16x32_bf16 v[56:59], v[140:143], a[248:251], v[56:59]\n"
"v_exp_f32 v169, v169\n"
"v_mfma_f32_16x16x32_bf16 v[88:91], v[144:147], a[244:247], v[88:91]\n"
"v_exp_f32 v154, v154\n"
"v_mfma_f32_16x16x32_bf16 v[88:91], v[148:151], a[248:251], v[88:91]\n"
"v_exp_f32 v156, v156\n"
"v_mfma_f32_16x16x32_bf16 v[92:95], v[148:151], v[224:227], v[92:95]\n"
"ds_read_b64_tr_b16 a[244:245], v232 offset:32768\n"
"v_cvt_pk_bf16_f32 v113, v162, v163\n"
"v_mfma_f32_16x16x32_bf16 v[92:95], v[144:147], a[252:255], v[92:95]\n"
"ds_read_b64_tr_b16 a[250:251], v236 offset:36864\n"
"v_cvt_pk_bf16_f32 v104, v156, v157\n"
"v_mfma_f32_16x16x32_bf16 v[60:63], v[140:143], v[224:227], v[60:63]\n"
"ds_read_b64_tr_b16 a[246:247], v232 offset:36864\n"
"v_cvt_pk_bf16_f32 v112, v160, v161\n"
"v_mfma_f32_16x16x32_bf16 v[60:63], v[136:139], a[252:255], v[60:63]\n"
"v_exp_f32 v152, v152\n"
"v_mfma_f32_16x16x32_bf16 v[28:31], v[132:135], v[224:227], v[28:31]\n"
"ds_read_b64_tr_b16 v[224:225], v237 offset:32768\n"
"ds_read_b64_tr_b16 v[226:227], v237 offset:36864\n"
"v_mfma_f32_16x16x32_bf16 v[28:31], v[128:131], a[252:255], v[28:31]\n"
"ds_read_b64_tr_b16 a[248:249], v236 offset:32768\n"
"v_cvt_pk_bf16_f32 v121, v166, v167\n"
"v_mfma_f32_16x16x32_bf16 a[236:239], v[124:127], a[252:255], a[236:239]\n"
"s_add_u32 s40, s56, s52\n"
"ds_read_b64_tr_b16 a[252:253], v233 offset:32768\n"
"v_smfmac_f32_16x16x64_bf16 v[198:201], a[240:243], v[136:143], v241\n"
"s_waitcnt lgkmcnt(7)\n"
"buffer_load_dwordx4 v240, s[16:19], s40 offen offset:1024 nt lds\n"
"v_mfma_f32_16x16x32_bf16 v[156:159], v[208:211], a[0:3], v[180:183]\n"
"v_exp_f32 v175, v175\n"
"v_mfma_f32_16x16x32_bf16 v[156:159], v[212:215], a[4:7], v[156:159]\n"
"v_exp_f32 v173, v173\n"
"v_mfma_f32_16x16x32_bf16 v[156:159], v[216:219], a[8:11], v[156:159]\n"
"v_exp_f32 v168, v168\n"
"v_mfma_f32_16x16x32_bf16 v[156:159], v[220:223], a[12:15], v[156:159]\n"
"ds_read_b64_tr_b16 a[254:255], v233 offset:36864\n"
"v_mfma_f32_16x16x32_bf16 v[160:163], v[216:219], a[24:27], v[184:187]\n"
"v_exp_f32 v170, v170\n"
"v_mfma_f32_16x16x32_bf16 v[160:163], v[220:223], a[28:31], v[160:163]\n"
"v_exp_f32 v178, v178\n"
"v_mfma_f32_16x16x32_bf16 v[160:163], v[208:211], a[16:19], v[160:163]\n"
"v_exp_f32 v177, v177\n"
"v_mfma_f32_16x16x32_bf16 v[160:163], v[212:215], a[20:23], v[160:163]\n"
"v_exp_f32 v176, v176\n"
"v_mfma_f32_16x16x32_bf16 v[164:167], v[208:211], a[32:35], v[188:191]\n"
"ds_read_b128 v[208:211], v230 offset:53248\n"
"v_cvt_pk_bf16_f32 v99, v170, v171\n"
"v_mfma_f32_16x16x32_bf16 v[164:167], v[212:215], a[36:39], v[164:167]\n"
"ds_read_b128 v[212:215], v231 offset:53248\n"
"v_mfma_f32_16x16x32_bf16 v[164:167], v[216:219], a[40:43], v[164:167]\n"
"v_exp_f32 v153, v153\n"
"v_mfma_f32_16x16x32_bf16 v[164:167], v[220:223], a[44:47], v[164:167]\n"
"s_waitcnt lgkmcnt(2)\n"
"ds_read_b128 v[220:223], v231 offset:54272\n"
"v_cvt_pk_bf16_f32 v106, v172, v173\n"
"v_mfma_f32_16x16x32_bf16 a[208:211], v[124:127], a[244:247], a[208:211]\n"
"v_exp_f32 v174, v174\n"
"v_mfma_f32_16x16x32_bf16 v[0:3], v[128:131], a[244:247], v[0:3]\n"
"s_add_u32 s40, s56, s53\n"
"ds_read_b128 v[216:219], v230 offset:54272\n"
"v_mfma_f32_16x16x32_bf16 v[0:3], v[132:135], a[248:251], v[0:3]\n"
"buffer_load_dwordx4 v240, s[16:19], s40 offen offset:2048 nt lds\n"
"v_mfma_f32_16x16x32_bf16 v[32:35], v[136:139], a[244:247], v[32:35]\n"
"v_exp_f32 v163, v163\n"
"v_mfma_f32_16x16x32_bf16 v[32:35], v[140:143], a[248:251], v[32:35]\n"
"v_exp_f32 v161, v161\n"
"v_mfma_f32_16x16x32_bf16 v[64:67], v[144:147], a[244:247], v[64:67]\n"
"v_exp_f32 v155, v155\n"
"v_mfma_f32_16x16x32_bf16 v[64:67], v[148:151], a[248:251], v[64:67]\n"
"v_exp_f32 v166, v166\n"
"v_mfma_f32_16x16x32_bf16 v[68:71], v[148:151], v[224:227], v[68:71]\n"
"ds_read_b64_tr_b16 a[250:251], v236 offset:37888\n"
"v_cvt_pk_bf16_f32 v107, v174, v175\n"
"v_mfma_f32_16x16x32_bf16 v[68:71], v[144:147], a[252:255], v[68:71]\n"
"ds_read_b64_tr_b16 a[246:247], v232 offset:37888\n"
"v_cvt_pk_bf16_f32 v98, v168, v169\n"
"v_mfma_f32_16x16x32_bf16 v[36:39], v[140:143], v[224:227], v[36:39]\n"
"v_exp_f32 v179, v179\n"
"v_mfma_f32_16x16x32_bf16 v[36:39], v[136:139], a[252:255], v[36:39]\n"
"ds_read_b64_tr_b16 a[244:245], v232 offset:33792\n"
"v_cvt_pk_bf16_f32 v122, v152, v153\n"
"v_mfma_f32_16x16x32_bf16 v[4:7], v[132:135], v[224:227], v[4:7]\n"
"s_add_u32 s40, s56, s54\n"
"ds_read_b64_tr_b16 a[248:249], v236 offset:33792\n"
"v_mfma_f32_16x16x32_bf16 v[4:7], v[128:131], a[252:255], v[4:7]\n"
"buffer_load_dwordx4 v240, s[16:19], s40 offen offset:3072 nt lds\n"
"v_mfma_f32_16x16x32_bf16 a[212:215], v[124:127], a[252:255], a[212:215]\n"
"v_exp_f32 v162, v162\n"
"v_smfmac_f32_16x16x64_bf16 v[200:203], a[240:243], v[144:151], v241\n"
"s_waitcnt lgkmcnt(4)\n"
"v_cvt_pk_bf16_f32 v114, v176, v177\n"
"v_mfma_f32_16x16x32_bf16 v[168:171], v[208:211], a[0:3], v[180:183]\n"
"v_exp_f32 v157, v157\n"
"v_mfma_f32_16x16x32_bf16 v[168:171], v[212:215], a[4:7], v[168:171]\n"
"v_exp_f32 v165, v165\n"
"v_mfma_f32_16x16x32_bf16 v[168:171], v[216:219], a[8:11], v[168:171]\n"
"ds_read_b64_tr_b16 a[254:255], v233 offset:37888\n"
"ds_read_b64_tr_b16 v[226:227], v237 offset:37888\n"
"v_mfma_f32_16x16x32_bf16 v[168:171], v[220:223], a[12:15], v[168:171]\n"
"ds_read_b64_tr_b16 a[252:253], v233 offset:33792\n"
"v_cvt_pk_bf16_f32 v115, v178, v179\n"
"v_mfma_f32_16x16x32_bf16 v[172:175], v[216:219], a[24:27], v[184:187]\n"
"ds_read_b64_tr_b16 v[224:225], v237 offset:33792\n"
"v_mfma_f32_16x16x32_bf16 v[172:175], v[220:223], a[28:31], v[172:175]\n"
"v_exp_f32 v160, v160\n"
"v_mfma_f32_16x16x32_bf16 v[172:175], v[208:211], a[16:19], v[172:175]\n"
"v_exp_f32 v158, v158\n"
"v_mfma_f32_16x16x32_bf16 v[172:175], v[212:215], a[20:23], v[172:175]\n"
"s_cbranch_vccnz LPCL3_%=\nLPKL3_%=:\n"
"v_mfma_f32_16x16x32_bf16 v[176:179], v[208:211], a[32:35], v[188:191]\n"
"s_add_u32 s47, s47, 1\n"
"v_exp_f32 v159, v159\n"
"v_mfma_f32_16x16x32_bf16 v[176:179], v[212:215], a[36:39], v[176:179]\n"
"v_exp_f32 v167, v167\n"
"v_mfma_f32_16x16x32_bf16 v[176:179], v[216:219], a[40:43], v[176:179]\n"
"s_and_b32 s40, s47, 3\n"
"v_exp_f32 v164, v164\n"
"v_mfma_f32_16x16x32_bf16 v[176:179], v[220:223], a[44:47], v[176:179]\n"
"s_waitcnt lgkmcnt(1)\n"
"s_cbranch_scc1 LAL_%=\n"
"s_cmp_eq_u32 s47, 8\n"
"s_cbranch_scc1 LTH1_%=\n"
"s_cmp_eq_u32 s47, 12\n"
"s_cbranch_scc1 LTH2_%=\n"
"s_cmp_eq_u32 s47, 16\n"
"s_cbranch_scc1 LAX_%=\n"
"LTH0_%=:\n"
"v_mfma_f32_16x16x32_bf16 a[224:227], v[124:127], a[244:247], a[224:227]\n"
"v_exp_f32 v168, v168\n"
"v_mfma_f32_16x16x32_bf16 v[16:19], v[128:131], a[244:247], v[16:19]\n"
"s_waitcnt vmcnt(8) lgkmcnt(0)\n"
"v_exp_f32 v169, v169\n"
"v_mfma_f32_16x16x32_bf16 v[16:19], v[132:135], a[248:251], v[16:19]\n"
"s_barrier\n"
"v_mfma_f32_16x16x32_bf16 v[48:51], v[136:139], a[244:247], v[48:51]\n"
"v_exp_f32 v170, v170\n"
"v_mfma_f32_16x16x32_bf16 v[48:51], v[140:143], a[248:251], v[48:51]\n"
"v_exp_f32 v156, v156\n"
"v_cvt_pk_bf16_f32 v102, v168, v169\n"
"v_exp_f32 v173, v173\n"
"v_exp_f32 v174, v174\n"
"v_exp_f32 v179, v179\n"
"v_mfma_f32_16x16x32_bf16 v[80:83], v[144:147], a[244:247], v[80:83]\n"
"ds_read_b64_tr_b16 a[246:247], v234 offset:53248\n"
"v_cvt_pk_bf16_f32 v108, v160, v161\n"
"v_mfma_f32_16x16x32_bf16 v[80:83], v[148:151], a[248:251], v[80:83]\n"
"ds_read_b64_tr_b16 a[244:245], v234 offset:49152\n"
"v_cvt_pk_bf16_f32 v123, v154, v155\n"
"v_mfma_f32_16x16x32_bf16 v[84:87], v[148:151], v[224:227], v[84:87]\n"
"ds_read_b64_tr_b16 a[250:251], v238 offset:53248\n"
"v_cvt_pk_bf16_f32 v100, v156, v157\n"
"v_mfma_f32_16x16x32_bf16 v[84:87], v[144:147], a[252:255], v[84:87]\n"
"ds_read_b64_tr_b16 a[248:249], v238 offset:49152\n"
"v_cvt_pk_bf16_f32 v117, v166, v167\n"
"v_mfma_f32_16x16x32_bf16 v[52:55], v[140:143], v[224:227], v[52:55]\n"
"v_exp_f32 v178, v178\n"
"v_mfma_f32_16x16x32_bf16 v[52:55], v[136:139], a[252:255], v[52:55]\n"
"v_cvt_pk_bf16_f32 v101, v158, v159\n"
"v_exp_f32 v171, v171\n"
"v_exp_f32 v172, v172\n"
"v_exp_f32 v175, v175\n"
"v_exp_f32 v176, v176\n"
"v_mfma_f32_16x16x32_bf16 v[20:23], v[132:135], v[224:227], v[20:23]\n"
"v_exp_f32 v177, v177\n"
"v_mfma_f32_16x16x32_bf16 v[20:23], v[128:131], a[252:255], v[20:23]\n"
"v_cvt_pk_bf16_f32 v116, v164, v165\n"
"v_cvt_pk_bf16_f32 v103, v170, v171\n"
"v_mfma_f32_16x16x32_bf16 a[228:231], v[124:127], a[252:255], a[228:231]\n"
"s_waitcnt lgkmcnt(2)\n"
"ds_read_b64_tr_b16 v[224:225], v239 offset:49152\n"
"ds_read_b64_tr_b16 v[226:227], v239 offset:53248\n"
"v_mfma_f32_16x16x32_bf16 a[216:219], v[120:123], a[244:247], a[216:219]\n"
"s_add_u32 m0, s44, 81920\n"
"ds_read_b128 v[212:215], v229 offset:0\n"
"v_cvt_pk_bf16_f32 v110, v172, v173\n"
"v_smfmac_f32_16x16x64_bf16 v[196:199], a[240:243], v[96:103], v241\n"
"buffer_load_dwordx4 v240, s[20:23], s56 offen nt lds\n"
"v_mfma_f32_16x16x32_bf16 v[8:11], v[96:99], a[244:247], v[8:11]\n"
"s_waitcnt lgkmcnt(3)\n"
"v_cvt_pk_bf16_f32 v118, v176, v177\n"
"v_cvt_pk_bf16_f32 v119, v178, v179\n"
"v_mfma_f32_16x16x32_bf16 v[8:11], v[100:103], a[248:251], v[8:11]\n"
"ds_read_b64_tr_b16 a[254:255], v235 offset:53248\n"
"v_cvt_pk_bf16_f32 v109, v162, v163\n"
"v_smfmac_f32_16x16x64_bf16 v[200:203], a[240:243], v[112:119], v241\n"
"ds_read_b64_tr_b16 a[252:253], v235 offset:49152\n"
"v_cvt_pk_bf16_f32 v111, v174, v175\n"
"v_mfma_f32_16x16x32_bf16 v[40:43], v[104:107], a[244:247], v[40:43]\n"
"ds_read_b128 v[220:223], v229 offset:1024\n"
"v_add_f32 v196, v196, v197\n"
"v_mfma_f32_16x16x32_bf16 v[40:43], v[108:111], a[248:251], v[40:43]\n"
"v_and_b32 v245, 0x3f0, v242\n"
"v_lshrrev_b32 v245, 1, v245\n"
"v_smfmac_f32_16x16x64_bf16 v[198:201], a[240:243], v[104:111], v241\n"
"ds_read_b128 v[208:211], v228 offset:0\n"
"v_lshlrev_b32_e64 v244, 24, s43\n"
"v_mfma_f32_16x16x32_bf16 v[72:75], v[112:115], a[244:247], v[72:75]\n"
"ds_read_b64_tr_b16 a[246:247], v234 offset:54272\n"
"v_mul_f32 v244, 0x45800000, v244\n"
"v_mfma_f32_16x16x32_bf16 v[72:75], v[116:119], a[248:251], v[72:75]\n"
"ds_read_b64_tr_b16 a[244:245], v234 offset:50176\n"
"v_sub_u32 v245, v242, v245\n"
"v_mfma_f32_16x16x32_bf16 v[202:205], a[240:243], v[120:123], v[202:205]\n"
"s_waitcnt lgkmcnt(6)\n"
"ds_read_b64_tr_b16 a[250:251], v238 offset:54272\n"
"v_add_f32 v198, v198, v199\n"
"v_mfma_f32_16x16x32_bf16 v[76:79], v[116:119], v[224:227], v[76:79]\n"
"s_waitcnt lgkmcnt(5)\n"
"ds_read_b64_tr_b16 a[248:249], v238 offset:50176\n"
"v_add_f32 v254, v196, v198\n"
"v_mfma_f32_16x16x32_bf16 v[76:79], v[112:115], a[252:255], v[76:79]\n"
"v_add_f32 v200, v200, v201\n"
"v_add_f32 v254, v254, v200\n"
"v_mfma_f32_16x16x32_bf16 v[44:47], v[108:111], v[224:227], v[44:47]\n"
"v_add_f32 v244, v244, v202\n"
"v_add_f32 v254, v254, v202\n"
"v_mfma_f32_16x16x32_bf16 v[44:47], v[104:107], a[252:255], v[44:47]\n"
"v_min3_f32 v244, v244, v196, v198\n"
"v_min_f32 v244, v244, v200\n"
"v_mfma_f32_16x16x32_bf16 v[12:15], v[100:103], v[224:227], v[12:15]\n"
"s_add_u32 s40, s56, s52\n"
"v_rcp_f32 v244, v244\n"
"v_mfma_f32_16x16x32_bf16 v[12:15], v[96:99], a[252:255], v[12:15]\n"
"buffer_load_dwordx4 v240, s[20:23], s40 offen offset:1024 nt lds\n"
"v_fmac_f32 v254, 0x68800000, v244\n"
"v_xor_b32 v244, 0x11000, v242\n"
"v_mfma_f32_16x16x32_bf16 a[220:223], v[120:123], a[252:255], a[220:223]\n"
"s_waitcnt lgkmcnt(2)\n"
"v_cmp_nge_f32 vcc, s45, v254\n"
"s_cbranch_vccnz LSLOW_%=\nLBACK0_%=:\ns_mov_b64 vcc, s[64:65]\n"
"v_mfma_f32_16x16x32_bf16 a[232:235], v[120:123], a[244:247], a[232:235]\n"
"v_rcp_f32 v246, v196\n"
"v_rcp_f32 v206, v200\n"
"v_accvgpr_mov_b32 a1, a49\n"
"v_accvgpr_mov_b32 a35, a83\n"
"v_accvgpr_mov_b32 a37, a85\n"
"v_accvgpr_mov_b32 a45, a93\n"
"v_bfe_u32 v149, v242, 8, 2\n"
"v_lshl_add_u32 v149, v149, 4, s43\n"
"v_mfma_f32_16x16x32_bf16 v[24:27], v[96:99], a[244:247], v[24:27]\n"
"s_waitcnt lgkmcnt(0)\n"
"ds_read_b128 v[124:127], v149 offset:0\n"
"v_accvgpr_mov_b32 a2, a50\n"
"v_accvgpr_mov_b32 a5, a53\n"
"v_accvgpr_mov_b32 a8, a56\n"
"v_accvgpr_mov_b32 a13, a61\n"
"v_accvgpr_mov_b32 a17, a65\n"
"v_accvgpr_mov_b32 a21, a69\n"
"v_xor_b32 v148, 0x400, v243\n"
"v_xor_b32 v150, 64, v149\n"
"v_mfma_f32_16x16x32_bf16 v[24:27], v[100:103], a[248:251], v[24:27]\n"
"ds_read_b128 v[132:135], v149 offset:1024\n"
"v_mov_b32_dpp v246, v246 row_ror:12 row_mask:0x2 bank_mask:0xf\n"
"v_accvgpr_mov_b32 a0, a48\n"
"v_accvgpr_mov_b32 a4, a52\n"
"v_accvgpr_mov_b32 a7, a55\n"
"v_accvgpr_mov_b32 a14, a62\n"
"v_accvgpr_mov_b32 a22, a70\n"
"v_accvgpr_mov_b32 a27, a75\n"
"v_accvgpr_mov_b32 a32, a80\n"
"v_accvgpr_mov_b32 a42, a90\n"
"v_mfma_f32_16x16x32_bf16 v[56:59], v[104:107], a[244:247], v[56:59]\n"
"ds_read_b128 v[128:131], v150 offset:0\n"
"v_rcp_f32 v247, v198\n"
"v_mov_b32_dpp v246, v246 row_ror:8 row_mask:0x4 bank_mask:0xf\n"
"v_accvgpr_mov_b32 a9, a57\n"
"v_accvgpr_mov_b32 a25, a73\n"
"v_accvgpr_mov_b32 a31, a79\n"
"v_accvgpr_mov_b32 a33, a81\n"
"v_accvgpr_mov_b32 a41, a89\n"
"v_mfma_f32_16x16x32_bf16 v[56:59], v[108:111], a[248:251], v[56:59]\n"
"ds_read_b128 v[136:139], v150 offset:1024\n"
"v_mov_b32_dpp v247, v247 row_ror:12 row_mask:0x2 bank_mask:0xf\n"
"v_mov_b32 v253, v202\n"
"v_accvgpr_mov_b32 a3, a51\n"
"v_accvgpr_mov_b32 a6, a54\n"
"v_accvgpr_mov_b32 a12, a60\n"
"v_accvgpr_mov_b32 a16, a64\n"
"v_accvgpr_mov_b32 a24, a72\n"
"v_accvgpr_mov_b32 a26, a74\n"
"v_accvgpr_mov_b32 a30, a78\n"
"v_mfma_f32_16x16x32_bf16 v[88:91], v[112:115], a[244:247], v[88:91]\n"
"v_accvgpr_mov_b32 a34, a82\n"
"v_accvgpr_mov_b32 a43, a91\n"
"v_mfma_f32_16x16x32_bf16 v[88:91], v[116:119], a[248:251], v[88:91]\n"
"ds_read_b128 a[196:199], v148 offset:4096\n"
"v_mov_b32_dpp v246, v246 row_ror:4 row_mask:0x8 bank_mask:0xf\n"
"v_accvgpr_mov_b32 a11, a59\n"
"v_accvgpr_mov_b32 a18, a66\n"
"v_accvgpr_mov_b32 a19, a67\n"
"v_accvgpr_mov_b32 a39, a87\n"
"v_accvgpr_mov_b32 a40, a88\n"
"v_accvgpr_mov_b32 a46, a94\n"
"s_waitcnt lgkmcnt(4)\n"
"v_mfma_f32_16x16x32_bf16 v[180:183], v[124:127], a[0:3], 0\n"
"s_waitcnt lgkmcnt(2)\n"
"ds_read_b64_tr_b16 a[252:253], v235 offset:50176\n"
"v_accvgpr_mov_b32 a10, a58\n"
"v_mfma_f32_16x16x32_bf16 v[180:183], v[128:131], a[4:7], v[180:183]\n"
"ds_read_b128 a[192:195], v243 offset:4096\n"
"v_accvgpr_mov_b32 a15, a63\n"
"v_mfma_f32_16x16x32_bf16 v[180:183], v[132:135], a[8:11], v[180:183]\n"
"ds_read_b128 a[200:203], v243 offset:6144\n"
"s_waitcnt lgkmcnt(4)\n"
"v_mfma_f32_16x16x32_bf16 v[180:183], v[136:139], a[12:15], v[180:183]\n"
"ds_read_b128 a[204:207], v148 offset:6144\n"
"v_accvgpr_mov_b32 a20, a68\n"
"v_accvgpr_mov_b32 a23, a71\n"
"v_accvgpr_mov_b32 a28, a76\n"
"v_accvgpr_mov_b32 a36, a84\n"
"v_accvgpr_mov_b32 a38, a86\n"
"v_accvgpr_mov_b32 a47, a95\n"
"v_mul_f32_dpp v25, v246, v25 row_newbcast:1 row_mask:0xf bank_mask:0xf\n"
"v_mfma_f32_16x16x32_bf16 v[184:187], v[124:127], a[16:19], 0\n"
"v_accvgpr_mov_b32 a44, a92\n"
"v_mul_f32_dpp v27, v246, v27 row_newbcast:3 row_mask:0xf bank_mask:0xf\n"
"v_mfma_f32_16x16x32_bf16 v[184:187], v[128:131], a[20:23], v[184:187]\n"
"v_mov_b32 v252, v192\n"
"v_accvgpr_mov_b32 a29, a77\n"
"v_mfma_f32_16x16x32_bf16 v[184:187], v[132:135], a[24:27], v[184:187]\n"
"s_add_u32 s40, s56, s53\n"
"ds_read_b128 v[216:219], v228 offset:1024\n"
"v_sub_f32 v180, s74, v180\n"
"v_mfma_f32_16x16x32_bf16 v[184:187], v[136:139], a[28:31], v[184:187]\n"
"buffer_load_dwordx4 v240, s[20:23], s40 offen offset:2048 nt lds\n"
"v_mov_b32_dpp v206, v206 row_ror:12 row_mask:0x2 bank_mask:0xf\n"
"v_mfma_f32_16x16x32_bf16 v[188:191], v[124:127], a[32:35], 0\n"
"v_mov_b32_dpp v247, v247 row_ror:8 row_mask:0x4 bank_mask:0xf\n"
"v_mfma_f32_16x16x32_bf16 v[188:191], v[128:131], a[36:39], v[188:191]\n"
"s_nop 0\n"
"v_mov_b32_dpp v247, v247 row_ror:4 row_mask:0x8 bank_mask:0xf\n"
"v_sub_f32 v183, s74, v183\n"
"v_mfma_f32_16x16x32_bf16 v[188:191], v[132:135], a[40:43], v[188:191]\n"
"v_sub_f32 v181, s74, v181\n"
"v_mfma_f32_16x16x32_bf16 v[188:191], v[136:139], a[44:47], v[188:191]\n"
"ds_write_b128 v244, a[232:235] offset:34816\n"
"v_mov_b32_dpp v206, v206 row_ror:8 row_mask:0x4 bank_mask:0xf\n"
"v_sub_f32 v185, s74, v185\n"
"v_sub_f32 v187, s74, v187\n"
"s_waitcnt lgkmcnt(2)\n"
"v_mfma_f32_16x16x32_bf16 v[192:195], v[124:127], a[192:195], 0\n"
"v_sub_f32 v182, s74, v182\n"
"v_mfma_f32_16x16x32_bf16 v[192:195], v[128:131], a[196:199], v[192:195]\n"
"ds_read_b64_tr_b16 a[246:247], v232 offset:53248\n"
"v_mfma_f32_16x16x32_bf16 v[192:195], v[132:135], a[200:203], v[192:195]\n"
"v_mfma_f32_16x16x32_bf16 v[192:195], v[136:139], a[204:207], v[192:195]\n"
"ds_read_b64_tr_b16 v[224:225], v239 offset:50176\n"
"ds_read_b64_tr_b16 v[226:227], v239 offset:54272\n"
"v_sub_f32 v184, s74, v184\n"
"v_mul_f32_dpp v56, v247, v56 row_newbcast:0 row_mask:0xf bank_mask:0xf\n"
"v_mfma_f32_16x16x32_bf16 v[152:155], v[208:211], a[0:3], v[180:183]\n"
"v_mul_f32_dpp v45, v247, v45 row_newbcast:1 row_mask:0xf bank_mask:0xf\n"
"v_sub_f32 v186, s74, v186\n"
"v_mfma_f32_16x16x32_bf16 v[152:155], v[212:215], a[4:7], v[152:155]\n"
"v_sub_f32 v188, s74, v188\n"
"s_waitcnt lgkmcnt(4)\n"
"v_mfma_f32_16x16x32_bf16 v[152:155], v[216:219], a[8:11], v[152:155]\n"
"s_add_u32 s40, s56, s54\n"
"v_sub_f32 v195, s74, v195\n"
"v_mov_b64 v[196:197], 0\n"
"v_mfma_f32_16x16x32_bf16 v[152:155], v[220:223], a[12:15], v[152:155]\n"
"buffer_load_dwordx4 v240, s[20:23], s40 offen offset:3072 nt lds\n"
"v_mul_f32_dpp v15, v246, v15 row_newbcast:3 row_mask:0xf bank_mask:0xf\n"
"v_sub_f32 v190, s74, v190\n"
"v_sub_f32 v191, s74, v191\n"
"v_sub_f32 v192, s74, v192\n"
"v_mfma_f32_16x16x32_bf16 v[156:159], v[216:219], a[24:27], v[184:187]\n"
"s_add_u32 s55, s55, s59\n"
"v_mov_b32_dpp v206, v206 row_ror:4 row_mask:0x8 bank_mask:0xf\n"
"v_mfma_f32_16x16x32_bf16 v[156:159], v[220:223], a[28:31], v[156:159]\n"
"s_add_u32 s41, s55, s57\n"
"v_sub_f32 v189, s74, v189\n"
"v_mfma_f32_16x16x32_bf16 v[156:159], v[208:211], a[16:19], v[156:159]\n"
"s_and_b32 s41, s41, s58\n"
"v_mul_f32_dpp v41, v247, v41 row_newbcast:1 row_mask:0xf bank_mask:0xf\n"
"v_mul_f32_dpp v26, v246, v26 row_newbcast:2 row_mask:0xf bank_mask:0xf\n"
"v_mfma_f32_16x16x32_bf16 v[156:159], v[212:215], a[20:23], v[156:159]\n"
"s_andn2_b32 s56, s55, s58\n"
"ds_read_b64_tr_b16 a[254:255], v235 offset:54272\n"
"v_mul_f32_dpp v46, v247, v46 row_newbcast:2 row_mask:0xf bank_mask:0xf\n"
"v_sub_f32 v193, s74, v193\n"
"v_mul_f32_dpp v58, v247, v58 row_newbcast:2 row_mask:0xf bank_mask:0xf\n"
"v_mfma_f32_16x16x32_bf16 v[160:163], v[208:211], a[32:35], v[188:191]\n"
"s_or_b32 s56, s56, s41\n"
"v_sub_f32 v194, s74, v194\n"
"v_mfma_f32_16x16x32_bf16 v[160:163], v[212:215], a[36:39], v[160:163]\n"
"v_mfma_f32_16x16x32_bf16 v[160:163], v[216:219], a[40:43], v[160:163]\n"
"v_mfma_f32_16x16x32_bf16 v[160:163], v[220:223], a[44:47], v[160:163]\n"
"ds_write_b128 v244, a[216:219] offset:32768\n"
"v_mul_f32_dpp v8, v246, v8 row_newbcast:0 row_mask:0xf bank_mask:0xf\n"
"v_exp_f32 v154, v154\n"
"v_mfma_f32_16x16x32_bf16 v[164:167], v[216:219], a[200:203], v[192:195]\n"
"v_exp_f32 v153, v153\n"
"v_mfma_f32_16x16x32_bf16 v[164:167], v[220:223], a[204:207], v[164:167]\n"
"ds_read_b128 v[220:223], v229 offset:5120\n"
"v_mul_f32_dpp v9, v246, v9 row_newbcast:1 row_mask:0xf bank_mask:0xf\n"
"v_mfma_f32_16x16x32_bf16 v[164:167], v[208:211], a[192:195], v[164:167]\n"
"v_mfma_f32_16x16x32_bf16 v[164:167], v[212:215], a[196:199], v[164:167]\n"
"ds_read_b128 v[216:219], v228 offset:5120\n"
"v_mul_f32_dpp v40, v247, v40 row_newbcast:0 row_mask:0xf bank_mask:0xf\n"
"v_exp_f32 v155, v155\n"
"v_exp_f32 v160, v160\n"
"v_exp_f32 v163, v163\n"
"s_branch LTC_%=\n"
"LTH1_%=:\n"
"v_mfma_f32_16x16x32_bf16 a[224:227], v[124:127], a[244:247], a[224:227]\n"
"v_exp_f32 v168, v168\n"
"v_mfma_f32_16x16x32_bf16 v[16:19], v[128:131], a[244:247], v[16:19]\n"
"s_waitcnt vmcnt(8) lgkmcnt(0)\n"
"v_exp_f32 v169, v169\n"
"v_mfma_f32_16x16x32_bf16 v[16:19], v[132:135], a[248:251], v[16:19]\n"
"s_barrier\n"
"v_mfma_f32_16x16x32_bf16 v[48:51], v[136:139], a[244:247], v[48:51]\n"
"v_exp_f32 v170, v170\n"
"v_mfma_f32_16x16x32_bf16 v[48:51], v[140:143], a[248:251], v[48:51]\n"
"v_exp_f32 v156, v156\n"
"v_cvt_pk_bf16_f32 v102, v168, v169\n"
"v_exp_f32 v173, v173\n"
"v_exp_f32 v174, v174\n"
"v_exp_f32 v179, v179\n"
"v_mfma_f32_16x16x32_bf16 v[80:83], v[144:147], a[244:247], v[80:83]\n"
"ds_read_b64_tr_b16 a[246:247], v234 offset:53248\n"
"v_cvt_pk_bf16_f32 v108, v160, v161\n"
"v_mfma_f32_16x16x32_bf16 v[80:83], v[148:151], a[248:251], v[80:83]\n"
"ds_read_b64_tr_b16 a[244:245], v234 offset:49152\n"
"v_cvt_pk_bf16_f32 v123, v154, v155\n"
"v_mfma_f32_16x16x32_bf16 v[84:87], v[148:151], v[224:227], v[84:87]\n"
"ds_read_b64_tr_b16 a[250:251], v238 offset:53248\n"
"v_cvt_pk_bf16_f32 v100, v156, v157\n"
"v_mfma_f32_16x16x32_bf16 v[84:87], v[144:147], a[252:255], v[84:87]\n"
"ds_read_b64_tr_b16 a[248:249], v238 offset:49152\n"
"v_cvt_pk_bf16_f32 v117, v166, v167\n"
"v_mfma_f32_16x16x32_bf16 v[52:55], v[140:143], v[224:227], v[52:55]\n"
"v_exp_f32 v178, v178\n"
"v_mfma_f32_16x16x32_bf16 v[52:55], v[136:139], a[252:255], v[52:55]\n"
"v_cvt_pk_bf16_f32 v101, v158, v159\n"
"v_exp_f32 v171, v171\n"
"v_exp_f32 v172, v172\n"
"v_exp_f32 v175, v175\n"
"v_exp_f32 v176, v176\n"
"v_mfma_f32_16x16x32_bf16 v[20:23], v[132:135], v[224:227], v[20:23]\n"
"v_exp_f32 v177, v177\n"
"v_mfma_f32_16x16x32_bf16 v[20:23], v[128:131], a[252:255], v[20:23]\n"
"v_cvt_pk_bf16_f32 v116, v164, v165\n"
"v_cvt_pk_bf16_f32 v103, v170, v171\n"
"v_mfma_f32_16x16x32_bf16 a[228:231], v[124:127], a[252:255], a[228:231]\n"
"s_waitcnt lgkmcnt(2)\n"
"ds_read_b64_tr_b16 v[224:225], v239 offset:49152\n"
"ds_read_b64_tr_b16 v[226:227], v239 offset:53248\n"
"v_mfma_f32_16x16x32_bf16 a[216:219], v[120:123], a[244:247], a[216:219]\n"
"s_add_u32 m0, s44, 81920\n"
"ds_read_b128 v[212:215], v229 offset:0\n"
"v_cvt_pk_bf16_f32 v110, v172, v173\n"
"v_smfmac_f32_16x16x64_bf16 v[196:199], a[240:243], v[96:103], v241\n"
"buffer_load_dwordx4 v240, s[20:23], s56 offen nt lds\n"
"v_mfma_f32_16x16x32_bf16 v[8:11], v[96:99], a[244:247], v[8:11]\n"
"s_waitcnt lgkmcnt(3)\n"
"v_cvt_pk_bf16_f32 v118, v176, v177\n"
"v_cvt_pk_bf16_f32 v119, v178, v179\n"
"v_mfma_f32_16x16x32_bf16 v[8:11], v[100:103], a[248:251], v[8:11]\n"
"ds_read_b64_tr_b16 a[254:255], v235 offset:53248\n"
"v_cvt_pk_bf16_f32 v109, v162, v163\n"
"v_smfmac_f32_16x16x64_bf16 v[200:203], a[240:243], v[112:119], v241\n"
"ds_read_b64_tr_b16 a[252:253], v235 offset:49152\n"
"v_cvt_pk_bf16_f32 v111, v174, v175\n"
"v_mfma_f32_16x16x32_bf16 v[40:43], v[104:107], a[244:247], v[40:43]\n"
"ds_read_b128 v[220:223], v229 offset:1024\n"
"v_add_f32 v196, v196, v197\n"
"v_mfma_f32_16x16x32_bf16 v[40:43], v[108:111], a[248:251], v[40:43]\n"
"v_and_b32 v245, 0x3f0, v242\n"
"v_lshrrev_b32 v245, 1, v245\n"
"v_smfmac_f32_16x16x64_bf16 v[198:201], a[240:243], v[104:111], v241\n"
"ds_read_b128 v[208:211], v228 offset:0\n"
"v_lshlrev_b32_e64 v244, 24, s43\n"
"v_mfma_f32_16x16x32_bf16 v[72:75], v[112:115], a[244:247], v[72:75]\n"
"ds_read_b64_tr_b16 a[246:247], v234 offset:54272\n"
"v_mul_f32 v244, 0x45800000, v244\n"
"v_mfma_f32_16x16x32_bf16 v[72:75], v[116:119], a[248:251], v[72:75]\n"
"ds_read_b64_tr_b16 a[244:245], v234 offset:50176\n"
"v_sub_u32 v245, v242, v245\n"
"v_mfma_f32_16x16x32_bf16 v[202:205], a[240:243], v[120:123], v[202:205]\n"
"s_waitcnt lgkmcnt(6)\n"
"ds_read_b64_tr_b16 a[250:251], v238 offset:54272\n"
"v_add_f32 v198, v198, v199\n"
"v_mfma_f32_16x16x32_bf16 v[76:79], v[116:119], v[224:227], v[76:79]\n"
"s_waitcnt lgkmcnt(5)\n"
"ds_read_b64_tr_b16 a[248:249], v238 offset:50176\n"
"v_add_f32 v254, v196, v198\n"
"v_mfma_f32_16x16x32_bf16 v[76:79], v[112:115], a[252:255], v[76:79]\n"
"v_add_f32 v200, v200, v201\n"
"v_add_f32 v254, v254, v200\n"
"v_mfma_f32_16x16x32_bf16 v[44:47], v[108:111], v[224:227], v[44:47]\n"
"v_add_f32 v244, v244, v202\n"
"v_add_f32 v254, v254, v202\n"
"v_mfma_f32_16x16x32_bf16 v[44:47], v[104:107], a[252:255], v[44:47]\n"
"v_min3_f32 v244, v244, v196, v198\n"
"v_min_f32 v244, v244, v200\n"
"v_mfma_f32_16x16x32_bf16 v[12:15], v[100:103], v[224:227], v[12:15]\n"
"s_add_u32 s40, s56, s52\n"
"v_rcp_f32 v244, v244\n"
"v_mfma_f32_16x16x32_bf16 v[12:15], v[96:99], a[252:255], v[12:15]\n"
"buffer_load_dwordx4 v240, s[20:23], s40 offen offset:1024 nt lds\n"
"v_fmac_f32 v254, 0x68800000, v244\n"
"v_xor_b32 v244, 0x11000, v242\n"
"v_mfma_f32_16x16x32_bf16 a[220:223], v[120:123], a[252:255], a[220:223]\n"
"s_waitcnt lgkmcnt(2)\n"
"v_cmp_nge_f32 vcc, s45, v254\n"
"s_cbranch_vccnz LSLOW_%=\nLBACK1_%=:\ns_mov_b64 vcc, s[64:65]\n"
"v_mfma_f32_16x16x32_bf16 a[232:235], v[120:123], a[244:247], a[232:235]\n"
"v_rcp_f32 v246, v196\n"
"v_rcp_f32 v206, v200\n"
"v_accvgpr_mov_b32 a1, a97\n"
"v_accvgpr_mov_b32 a35, a131\n"
"v_accvgpr_mov_b32 a37, a133\n"
"v_accvgpr_mov_b32 a45, a141\n"
"v_bfe_u32 v149, v242, 8, 2\n"
"v_lshl_add_u32 v149, v149, 4, s43\n"
"v_mfma_f32_16x16x32_bf16 v[24:27], v[96:99], a[244:247], v[24:27]\n"
"s_waitcnt lgkmcnt(0)\n"
"ds_read_b128 v[124:127], v149 offset:0\n"
"v_accvgpr_mov_b32 a2, a98\n"
"v_accvgpr_mov_b32 a5, a101\n"
"v_accvgpr_mov_b32 a8, a104\n"
"v_accvgpr_mov_b32 a13, a109\n"
"v_accvgpr_mov_b32 a17, a113\n"
"v_accvgpr_mov_b32 a21, a117\n"
"v_xor_b32 v148, 0x400, v243\n"
"v_xor_b32 v150, 64, v149\n"
"v_mfma_f32_16x16x32_bf16 v[24:27], v[100:103], a[248:251], v[24:27]\n"
"ds_read_b128 v[132:135], v149 offset:1024\n"
"v_mov_b32_dpp v246, v246 row_ror:12 row_mask:0x2 bank_mask:0xf\n"
"v_accvgpr_mov_b32 a0, a96\n"
"v_accvgpr_mov_b32 a4, a100\n"
"v_accvgpr_mov_b32 a7, a103\n"
"v_accvgpr_mov_b32 a14, a110\n"
"v_accvgpr_mov_b32 a22, a118\n"
"v_accvgpr_mov_b32 a27, a123\n"
"v_accvgpr_mov_b32 a32, a128\n"
"v_accvgpr_mov_b32 a42, a138\n"
"v_mfma_f32_16x16x32_bf16 v[56:59], v[104:107], a[244:247], v[56:59]\n"
"ds_read_b128 v[128:131], v150 offset:0\n"
"v_rcp_f32 v247, v198\n"
"v_mov_b32_dpp v246, v246 row_ror:8 row_mask:0x4 bank_mask:0xf\n"
"v_accvgpr_mov_b32 a9, a105\n"
"v_accvgpr_mov_b32 a25, a121\n"
"v_accvgpr_mov_b32 a31, a127\n"
"v_accvgpr_mov_b32 a33, a129\n"
"v_accvgpr_mov_b32 a41, a137\n"
"v_accvgpr_mov_b32 a118, a70\n"
"v_mfma_f32_16x16x32_bf16 v[56:59], v[108:111], a[248:251], v[56:59]\n"
"ds_read_b128 v[136:139], v150 offset:1024\n"
"v_mov_b32_dpp v247, v247 row_ror:12 row_mask:0x2 bank_mask:0xf\n"
"v_mov_b32 v253, v202\n"
"v_accvgpr_mov_b32 a3, a99\n"
"v_accvgpr_mov_b32 a6, a102\n"
"v_accvgpr_mov_b32 a12, a108\n"
"v_accvgpr_mov_b32 a16, a112\n"
"v_accvgpr_mov_b32 a24, a120\n"
"v_accvgpr_mov_b32 a26, a122\n"
"v_accvgpr_mov_b32 a30, a126\n"
"v_mfma_f32_16x16x32_bf16 v[88:91], v[112:115], a[244:247], v[88:91]\n"
"v_accvgpr_mov_b32 a34, a130\n"
"v_accvgpr_mov_b32 a43, a139\n"
"v_mfma_f32_16x16x32_bf16 v[88:91], v[116:119], a[248:251], v[88:91]\n"
"s_waitcnt lgkmcnt(3)\n"
"ds_read_b128 a[196:199], v148 offset:4096\n"
"v_mov_b32_dpp v246, v246 row_ror:4 row_mask:0x8 bank_mask:0xf\n"
"v_accvgpr_mov_b32 a11, a107\n"
"v_accvgpr_mov_b32 a18, a114\n"
"v_accvgpr_mov_b32 a19, a115\n"
"v_accvgpr_mov_b32 a39, a135\n"
"v_accvgpr_mov_b32 a40, a136\n"
"v_accvgpr_mov_b32 a46, a142\n"
"v_accvgpr_mov_b32 a108, a60\n"
"v_accvgpr_mov_b32 a96, a48\n"
"v_mfma_f32_16x16x32_bf16 v[180:183], v[124:127], a[0:3], 0\n"
"s_waitcnt lgkmcnt(2)\n"
"ds_read_b64_tr_b16 a[252:253], v235 offset:50176\n"
"v_accvgpr_mov_b32 a10, a106\n"
"v_mfma_f32_16x16x32_bf16 v[180:183], v[128:131], a[4:7], v[180:183]\n"
"ds_read_b128 a[192:195], v243 offset:4096\n"
"v_accvgpr_mov_b32 a15, a111\n"
"v_mfma_f32_16x16x32_bf16 v[180:183], v[132:135], a[8:11], v[180:183]\n"
"s_waitcnt lgkmcnt(3)\n"
"ds_read_b128 a[200:203], v243 offset:6144\n"
"v_accvgpr_mov_b32 a113, a65\n"
"v_mfma_f32_16x16x32_bf16 v[180:183], v[136:139], a[12:15], v[180:183]\n"
"ds_read_b128 a[204:207], v148 offset:6144\n"
"v_accvgpr_mov_b32 a20, a116\n"
"v_accvgpr_mov_b32 a23, a119\n"
"v_accvgpr_mov_b32 a28, a124\n"
"v_accvgpr_mov_b32 a36, a132\n"
"v_accvgpr_mov_b32 a38, a134\n"
"v_accvgpr_mov_b32 a47, a143\n"
"v_accvgpr_mov_b32 a100, a52\n"
"v_mul_f32_dpp v25, v246, v25 row_newbcast:1 row_mask:0xf bank_mask:0xf\n"
"v_mfma_f32_16x16x32_bf16 v[184:187], v[124:127], a[16:19], 0\n"
"v_accvgpr_mov_b32 a44, a140\n"
"v_mul_f32_dpp v27, v246, v27 row_newbcast:3 row_mask:0xf bank_mask:0xf\n"
"v_mfma_f32_16x16x32_bf16 v[184:187], v[128:131], a[20:23], v[184:187]\n"
"v_mov_b32 v252, v192\n"
"v_accvgpr_mov_b32 a29, a125\n"
"v_mfma_f32_16x16x32_bf16 v[184:187], v[132:135], a[24:27], v[184:187]\n"
"s_add_u32 s40, s56, s53\n"
"ds_read_b128 v[216:219], v228 offset:1024\n"
"v_sub_f32 v180, s74, v180\n"
"v_mfma_f32_16x16x32_bf16 v[184:187], v[136:139], a[28:31], v[184:187]\n"
"buffer_load_dwordx4 v240, s[20:23], s40 offen offset:2048 nt lds\n"
"v_mov_b32_dpp v206, v206 row_ror:12 row_mask:0x2 bank_mask:0xf\n"
"v_accvgpr_mov_b32 a101, a53\n"
"v_accvgpr_mov_b32 a116, a68\n"
"v_accvgpr_mov_b32 a132, a84\n"
"v_accvgpr_mov_b32 a142, a94\n"
"v_accvgpr_mov_b32 a99, a51\n"
"v_accvgpr_mov_b32 a131, a83\n"
"v_accvgpr_mov_b32 a139, a91\n"
"v_mfma_f32_16x16x32_bf16 v[188:191], v[124:127], a[32:35], 0\n"
"v_mov_b32_dpp v247, v247 row_ror:8 row_mask:0x4 bank_mask:0xf\n"
"v_accvgpr_mov_b32 a115, a67\n"
"v_mfma_f32_16x16x32_bf16 v[188:191], v[128:131], a[36:39], v[188:191]\n"
"v_mov_b32_dpp v247, v247 row_ror:4 row_mask:0x8 bank_mask:0xf\n"
"v_sub_f32 v183, s74, v183\n"
"v_mfma_f32_16x16x32_bf16 v[188:191], v[132:135], a[40:43], v[188:191]\n"
"v_accvgpr_mov_b32 a102, a54\n"
"v_sub_f32 v181, s74, v181\n"
"v_mfma_f32_16x16x32_bf16 v[188:191], v[136:139], a[44:47], v[188:191]\n"
"s_waitcnt lgkmcnt(1)\n"
"ds_write_b128 v244, a[232:235] offset:34816\n"
"v_mov_b32_dpp v206, v206 row_ror:8 row_mask:0x4 bank_mask:0xf\n"
"v_accvgpr_mov_b32 a124, a76\n"
"v_accvgpr_mov_b32 a125, a77\n"
"v_sub_f32 v185, s74, v185\n"
"v_sub_f32 v187, s74, v187\n"
"v_mfma_f32_16x16x32_bf16 v[192:195], v[124:127], a[192:195], 0\n"
"v_accvgpr_mov_b32 a103, a55\n"
"v_sub_f32 v182, s74, v182\n"
"v_mfma_f32_16x16x32_bf16 v[192:195], v[128:131], a[196:199], v[192:195]\n"
"ds_read_b64_tr_b16 a[246:247], v232 offset:53248\n"
"v_accvgpr_mov_b32 a98, a50\n"
"v_mfma_f32_16x16x32_bf16 v[192:195], v[132:135], a[200:203], v[192:195]\n"
"v_accvgpr_mov_b32 a109, a61\n"
"v_accvgpr_mov_b32 a121, a73\n"
"v_mfma_f32_16x16x32_bf16 v[192:195], v[136:139], a[204:207], v[192:195]\n"
"ds_read_b64_tr_b16 v[224:225], v239 offset:50176\n"
"ds_read_b64_tr_b16 v[226:227], v239 offset:54272\n"
"v_accvgpr_mov_b32 a135, a87\n"
"v_accvgpr_mov_b32 a97, a49\n"
"v_accvgpr_mov_b32 a128, a80\n"
"v_accvgpr_mov_b32 a130, a82\n"
"v_accvgpr_mov_b32 a123, a75\n"
"v_accvgpr_mov_b32 a137, a89\n"
"v_sub_f32 v184, s74, v184\n"
"v_mul_f32_dpp v56, v247, v56 row_newbcast:0 row_mask:0xf bank_mask:0xf\n"
"v_mfma_f32_16x16x32_bf16 v[152:155], v[208:211], a[0:3], v[180:183]\n"
"v_mul_f32_dpp v45, v247, v45 row_newbcast:1 row_mask:0xf bank_mask:0xf\n"
"v_sub_f32 v186, s74, v186\n"
"v_mfma_f32_16x16x32_bf16 v[152:155], v[212:215], a[4:7], v[152:155]\n"
"s_waitcnt lgkmcnt(4)\n"
"v_accvgpr_mov_b32 a127, a79\n"
"v_sub_f32 v188, s74, v188\n"
"v_mfma_f32_16x16x32_bf16 v[152:155], v[216:219], a[8:11], v[152:155]\n"
"s_add_u32 s40, s56, s54\n"
"v_sub_f32 v195, s74, v195\n"
"v_mov_b64 v[196:197], 0\n"
"v_mfma_f32_16x16x32_bf16 v[152:155], v[220:223], a[12:15], v[152:155]\n"
"buffer_load_dwordx4 v240, s[20:23], s40 offen offset:3072 nt lds\n"
"v_accvgpr_mov_b32 a143, a95\n"
"v_accvgpr_mov_b32 a114, a66\n"
"v_accvgpr_mov_b32 a105, a57\n"
"v_mul_f32_dpp v15, v246, v15 row_newbcast:3 row_mask:0xf bank_mask:0xf\n"
"v_sub_f32 v190, s74, v190\n"
"v_sub_f32 v191, s74, v191\n"
"v_sub_f32 v192, s74, v192\n"
"v_mfma_f32_16x16x32_bf16 v[156:159], v[216:219], a[24:27], v[184:187]\n"
"s_add_u32 s55, s55, s59\n"
"v_mov_b32_dpp v206, v206 row_ror:4 row_mask:0x8 bank_mask:0xf\n"
"v_accvgpr_mov_b32 a138, a90\n"
"v_mfma_f32_16x16x32_bf16 v[156:159], v[220:223], a[28:31], v[156:159]\n"
"s_add_u32 s41, s55, s57\n"
"v_accvgpr_mov_b32 a110, a62\n"
"v_sub_f32 v189, s74, v189\n"
"v_mfma_f32_16x16x32_bf16 v[156:159], v[208:211], a[16:19], v[156:159]\n"
"s_and_b32 s41, s41, s58\n"
"v_mul_f32_dpp v41, v247, v41 row_newbcast:1 row_mask:0xf bank_mask:0xf\n"
"v_mul_f32_dpp v26, v246, v26 row_newbcast:2 row_mask:0xf bank_mask:0xf\n"
"v_mfma_f32_16x16x32_bf16 v[156:159], v[212:215], a[20:23], v[156:159]\n"
"s_andn2_b32 s56, s55, s58\n"
"ds_read_b64_tr_b16 a[254:255], v235 offset:54272\n"
"v_accvgpr_mov_b32 a119, a71\n"
"v_accvgpr_mov_b32 a133, a85\n"
"v_accvgpr_mov_b32 a126, a78\n"
"v_accvgpr_mov_b32 a140, a92\n"
"v_accvgpr_mov_b32 a106, a58\n"
"v_accvgpr_mov_b32 a120, a72\n"
"v_mul_f32_dpp v46, v247, v46 row_newbcast:2 row_mask:0xf bank_mask:0xf\n"
"v_sub_f32 v193, s74, v193\n"
"v_mul_f32_dpp v58, v247, v58 row_newbcast:2 row_mask:0xf bank_mask:0xf\n"
"v_mfma_f32_16x16x32_bf16 v[160:163], v[208:211], a[32:35], v[188:191]\n"
"s_or_b32 s56, s56, s41\n"
"v_accvgpr_mov_b32 a117, a69\n"
"v_sub_f32 v194, s74, v194\n"
"v_mfma_f32_16x16x32_bf16 v[160:163], v[212:215], a[36:39], v[160:163]\n"
"v_accvgpr_mov_b32 a111, a63\n"
"v_accvgpr_mov_b32 a122, a74\n"
"v_mfma_f32_16x16x32_bf16 v[160:163], v[216:219], a[40:43], v[160:163]\n"
"v_accvgpr_mov_b32 a107, a59\n"
"v_accvgpr_mov_b32 a136, a88\n"
"v_mfma_f32_16x16x32_bf16 v[160:163], v[220:223], a[44:47], v[160:163]\n"
"ds_write_b128 v244, a[216:219] offset:32768\n"
"v_accvgpr_mov_b32 a112, a64\n"
"v_accvgpr_mov_b32 a129, a81\n"
"v_mul_f32_dpp v8, v246, v8 row_newbcast:0 row_mask:0xf bank_mask:0xf\n"
"v_exp_f32 v154, v154\n"
"v_mfma_f32_16x16x32_bf16 v[164:167], v[216:219], a[200:203], v[192:195]\n"
"v_exp_f32 v153, v153\n"
"v_mfma_f32_16x16x32_bf16 v[164:167], v[220:223], a[204:207], v[164:167]\n"
"ds_read_b128 v[220:223], v229 offset:5120\n"
"v_mul_f32_dpp v9, v246, v9 row_newbcast:1 row_mask:0xf bank_mask:0xf\n"
"v_mfma_f32_16x16x32_bf16 v[164:167], v[208:211], a[192:195], v[164:167]\n"
"v_accvgpr_mov_b32 a141, a93\n"
"v_accvgpr_mov_b32 a104, a56\n"
"v_mfma_f32_16x16x32_bf16 v[164:167], v[212:215], a[196:199], v[164:167]\n"
"s_waitcnt lgkmcnt(2)\n"
"ds_read_b128 v[216:219], v228 offset:5120\n"
"v_accvgpr_mov_b32 a134, a86\n"
"v_mul_f32_dpp v40, v247, v40 row_newbcast:0 row_mask:0xf bank_mask:0xf\n"
"v_exp_f32 v155, v155\n"
"v_exp_f32 v160, v160\n"
"v_exp_f32 v163, v163\n"
"s_branch LTC_%=\n"
"LTH2_%=:\n"
"v_mfma_f32_16x16x32_bf16 a[224:227], v[124:127], a[244:247], a[224:227]\n"
"v_exp_f32 v168, v168\n"
"v_mfma_f32_16x16x32_bf16 v[16:19], v[128:131], a[244:247], v[16:19]\n"
"s_waitcnt vmcnt(8) lgkmcnt(0)\n"
"v_exp_f32 v169, v169\n"
"v_mfma_f32_16x16x32_bf16 v[16:19], v[132:135], a[248:251], v[16:19]\n"
"s_barrier\n"
"v_mfma_f32_16x16x32_bf16 v[48:51], v[136:139], a[244:247], v[48:51]\n"
"v_exp_f32 v170, v170\n"
"v_mfma_f32_16x16x32_bf16 v[48:51], v[140:143], a[248:251], v[48:51]\n"
"v_exp_f32 v156, v156\n"
"v_cvt_pk_bf16_f32 v102, v168, v169\n"
"v_exp_f32 v173, v173\n"
"v_exp_f32 v174, v174\n"
"v_exp_f32 v179, v179\n"
"v_mfma_f32_16x16x32_bf16 v[80:83], v[144:147], a[244:247], v[80:83]\n"
"ds_read_b64_tr_b16 a[246:247], v234 offset:53248\n"
"v_cvt_pk_bf16_f32 v108, v160, v161\n"
"v_mfma_f32_16x16x32_bf16 v[80:83], v[148:151], a[248:251], v[80:83]\n"
"ds_read_b64_tr_b16 a[244:245], v234 offset:49152\n"
"v_cvt_pk_bf16_f32 v123, v154, v155\n"
"v_mfma_f32_16x16x32_bf16 v[84:87], v[148:151], v[224:227], v[84:87]\n"
"ds_read_b64_tr_b16 a[250:251], v238 offset:53248\n"
"v_cvt_pk_bf16_f32 v100, v156, v157\n"
"v_mfma_f32_16x16x32_bf16 v[84:87], v[144:147], a[252:255], v[84:87]\n"
"ds_read_b64_tr_b16 a[248:249], v238 offset:49152\n"
"v_cvt_pk_bf16_f32 v117, v166, v167\n"
"v_mfma_f32_16x16x32_bf16 v[52:55], v[140:143], v[224:227], v[52:55]\n"
"v_exp_f32 v178, v178\n"
"v_mfma_f32_16x16x32_bf16 v[52:55], v[136:139], a[252:255], v[52:55]\n"
"v_cvt_pk_bf16_f32 v101, v158, v159\n"
"v_exp_f32 v171, v171\n"
"v_exp_f32 v172, v172\n"
"v_exp_f32 v175, v175\n"
"v_exp_f32 v176, v176\n"
"v_mfma_f32_16x16x32_bf16 v[20:23], v[132:135], v[224:227], v[20:23]\n"
"v_exp_f32 v177, v177\n"
"v_mfma_f32_16x16x32_bf16 v[20:23], v[128:131], a[252:255], v[20:23]\n"
"v_cvt_pk_bf16_f32 v116, v164, v165\n"
"v_cvt_pk_bf16_f32 v103, v170, v171\n"
"v_mfma_f32_16x16x32_bf16 a[228:231], v[124:127], a[252:255], a[228:231]\n"
"s_waitcnt lgkmcnt(2)\n"
"ds_read_b64_tr_b16 v[224:225], v239 offset:49152\n"
"ds_read_b64_tr_b16 v[226:227], v239 offset:53248\n"
"v_mfma_f32_16x16x32_bf16 a[216:219], v[120:123], a[244:247], a[216:219]\n"
"s_add_u32 m0, s44, 81920\n"
"ds_read_b128 v[212:215], v229 offset:0\n"
"v_cvt_pk_bf16_f32 v110, v172, v173\n"
"v_smfmac_f32_16x16x64_bf16 v[196:199], a[240:243], v[96:103], v241\n"
"buffer_load_dwordx4 v240, s[20:23], s56 offen nt lds\n"
"v_mfma_f32_16x16x32_bf16 v[8:11], v[96:99], a[244:247], v[8:11]\n"
"s_waitcnt lgkmcnt(3)\n"
"v_cvt_pk_bf16_f32 v118, v176, v177\n"
"v_cvt_pk_bf16_f32 v119, v178, v179\n"
"v_mfma_f32_16x16x32_bf16 v[8:11], v[100:103], a[248:251], v[8:11]\n"
"ds_read_b64_tr_b16 a[254:255], v235 offset:53248\n"
"v_cvt_pk_bf16_f32 v109, v162, v163\n"
"v_smfmac_f32_16x16x64_bf16 v[200:203], a[240:243], v[112:119], v241\n"
"ds_read_b64_tr_b16 a[252:253], v235 offset:49152\n"
"v_cvt_pk_bf16_f32 v111, v174, v175\n"
"v_mfma_f32_16x16x32_bf16 v[40:43], v[104:107], a[244:247], v[40:43]\n"
"ds_read_b128 v[220:223], v229 offset:1024\n"
"v_add_f32 v196, v196, v197\n"
"v_mfma_f32_16x16x32_bf16 v[40:43], v[108:111], a[248:251], v[40:43]\n"
"v_and_b32 v245, 0x3f0, v242\n"
"v_lshrrev_b32 v245, 1, v245\n"
"v_smfmac_f32_16x16x64_bf16 v[198:201], a[240:243], v[104:111], v241\n"
"ds_read_b128 v[208:211], v228 offset:0\n"
"v_lshlrev_b32_e64 v244, 24, s43\n"
"v_mfma_f32_16x16x32_bf16 v[72:75], v[112:115], a[244:247], v[72:75]\n"
"ds_read_b64_tr_b16 a[246:247], v234 offset:54272\n"
"v_mul_f32 v244, 0x45800000, v244\n"
"v_mfma_f32_16x16x32_bf16 v[72:75], v[116:119], a[248:251], v[72:75]\n"
"ds_read_b64_tr_b16 a[244:245], v234 offset:50176\n"
"v_sub_u32 v245, v242, v245\n"
"v_mfma_f32_16x16x32_bf16 v[202:205], a[240:243], v[120:123], v[202:205]\n"
"s_waitcnt lgkmcnt(6)\n"
"ds_read_b64_tr_b16 a[250:251], v238 offset:54272\n"
"v_add_f32 v198, v198, v199\n"
"v_mfma_f32_16x16x32_bf16 v[76:79], v[116:119], v[224:227], v[76:79]\n"
"s_waitcnt lgkmcnt(5)\n"
"ds_read_b64_tr_b16 a[248:249], v238 offset:50176\n"
"v_add_f32 v254, v196, v198\n"
"v_mfma_f32_16x16x32_bf16 v[76:79], v[112:115], a[252:255], v[76:79]\n"
"v_add_f32 v200, v200, v201\n"
"v_add_f32 v254, v254, v200\n"
"v_mfma_f32_16x16x32_bf16 v[44:47], v[108:111], v[224:227], v[44:47]\n"
"v_add_f32 v244, v244, v202\n"
"v_add_f32 v254, v254, v202\n"
"v_mfma_f32_16x16x32_bf16 v[44:47], v[104:107], a[252:255], v[44:47]\n"
"v_min3_f32 v244, v244, v196, v198\n"
"v_min_f32 v244, v244, v200\n"
"v_mfma_f32_16x16x32_bf16 v[12:15], v[100:103], v[224:227], v[12:15]\n"
"s_add_u32 s40, s56, s52\n"
"v_rcp_f32 v244, v244\n"
"v_mfma_f32_16x16x32_bf16 v[12:15], v[96:99], a[252:255], v[12:15]\n"
"buffer_load_dwordx4 v240, s[20:23], s40 offen offset:1024 nt lds\n"
"v_fmac_f32 v254, 0x68800000, v244\n"
"v_xor_b32 v244, 0x11000, v242\n"
"v_mfma_f32_16x16x32_bf16 a[220:223], v[120:123], a[252:255], a[220:223]\n"
"s_waitcnt lgkmcnt(2)\n"
"v_cmp_nge_f32 vcc, s45, v254\n"
"s_cbranch_vccnz LSLOW_%=\nLBACK2_%=:\ns_mov_b64 vcc, s[64:65]\n"
"v_mfma_f32_16x16x32_bf16 a[232:235], v[120:123], a[244:247], a[232:235]\n"
"v_rcp_f32 v246, v196\n"
"v_rcp_f32 v206, v200\n"
"v_accvgpr_mov_b32 a1, a145\n"
"v_accvgpr_mov_b32 a35, a179\n"
"v_accvgpr_mov_b32 a37, a181\n"
"v_accvgpr_mov_b32 a45, a189\n"
"v_bfe_u32 v149, v242, 8, 2\n"
"v_lshl_add_u32 v149, v149, 4, s43\n"
"v_mfma_f32_16x16x32_bf16 v[24:27], v[96:99], a[244:247], v[24:27]\n"
"s_waitcnt lgkmcnt(0)\n"
"ds_read_b128 v[124:127], v149 offset:0\n"
"v_accvgpr_mov_b32 a2, a146\n"
"v_accvgpr_mov_b32 a5, a149\n"
"v_accvgpr_mov_b32 a8, a152\n"
"v_accvgpr_mov_b32 a13, a157\n"
"v_accvgpr_mov_b32 a17, a161\n"
"v_accvgpr_mov_b32 a21, a165\n"
"v_xor_b32 v148, 0x400, v243\n"
"v_xor_b32 v150, 64, v149\n"
"v_mfma_f32_16x16x32_bf16 v[24:27], v[100:103], a[248:251], v[24:27]\n"
"ds_read_b128 v[132:135], v149 offset:1024\n"
"v_mov_b32_dpp v246, v246 row_ror:12 row_mask:0x2 bank_mask:0xf\n"
"v_accvgpr_mov_b32 a0, a144\n"
"v_accvgpr_mov_b32 a4, a148\n"
"v_accvgpr_mov_b32 a7, a151\n"
"v_accvgpr_mov_b32 a14, a158\n"
"v_accvgpr_mov_b32 a22, a166\n"
"v_accvgpr_mov_b32 a27, a171\n"
"v_accvgpr_mov_b32 a32, a176\n"
"v_accvgpr_mov_b32 a42, a186\n"
"v_mfma_f32_16x16x32_bf16 v[56:59], v[104:107], a[244:247], v[56:59]\n"
"ds_read_b128 v[128:131], v150 offset:0\n"
"v_rcp_f32 v247, v198\n"
"v_mov_b32_dpp v246, v246 row_ror:8 row_mask:0x4 bank_mask:0xf\n"
"v_accvgpr_mov_b32 a9, a153\n"
"v_accvgpr_mov_b32 a25, a169\n"
"v_accvgpr_mov_b32 a31, a175\n"
"v_accvgpr_mov_b32 a33, a177\n"
"v_accvgpr_mov_b32 a41, a185\n"
"v_accvgpr_mov_b32 a166, a70\n"
"v_mfma_f32_16x16x32_bf16 v[56:59], v[108:111], a[248:251], v[56:59]\n"
"ds_read_b128 v[136:139], v150 offset:1024\n"
"v_mov_b32_dpp v247, v247 row_ror:12 row_mask:0x2 bank_mask:0xf\n"
"v_mov_b32 v253, v202\n"
"v_accvgpr_mov_b32 a3, a147\n"
"v_accvgpr_mov_b32 a6, a150\n"
"v_accvgpr_mov_b32 a12, a156\n"
"v_accvgpr_mov_b32 a16, a160\n"
"v_accvgpr_mov_b32 a24, a168\n"
"v_accvgpr_mov_b32 a26, a170\n"
"v_accvgpr_mov_b32 a30, a174\n"
"v_mfma_f32_16x16x32_bf16 v[88:91], v[112:115], a[244:247], v[88:91]\n"
"v_accvgpr_mov_b32 a34, a178\n"
"v_accvgpr_mov_b32 a43, a187\n"
"v_mfma_f32_16x16x32_bf16 v[88:91], v[116:119], a[248:251], v[88:91]\n"
"s_waitcnt lgkmcnt(3)\n"
"ds_read_b128 a[196:199], v148 offset:4096\n"
"v_mov_b32_dpp v246, v246 row_ror:4 row_mask:0x8 bank_mask:0xf\n"
"v_accvgpr_mov_b32 a11, a155\n"
"v_accvgpr_mov_b32 a18, a162\n"
"v_accvgpr_mov_b32 a19, a163\n"
"v_accvgpr_mov_b32 a39, a183\n"
"v_accvgpr_mov_b32 a40, a184\n"
"v_accvgpr_mov_b32 a46, a190\n"
"v_accvgpr_mov_b32 a156, a60\n"
"v_accvgpr_mov_b32 a144, a48\n"
"v_mfma_f32_16x16x32_bf16 v[180:183], v[124:127], a[0:3], 0\n"
"s_waitcnt lgkmcnt(2)\n"
"ds_read_b64_tr_b16 a[252:253], v235 offset:50176\n"
"v_accvgpr_mov_b32 a10, a154\n"
"v_mfma_f32_16x16x32_bf16 v[180:183], v[128:131], a[4:7], v[180:183]\n"
"ds_read_b128 a[192:195], v243 offset:4096\n"
"v_accvgpr_mov_b32 a15, a159\n"
"v_mfma_f32_16x16x32_bf16 v[180:183], v[132:135], a[8:11], v[180:183]\n"
"s_waitcnt lgkmcnt(3)\n"
"ds_read_b128 a[200:203], v243 offset:6144\n"
"v_accvgpr_mov_b32 a161, a65\n"
"v_mfma_f32_16x16x32_bf16 v[180:183], v[136:139], a[12:15], v[180:183]\n"
"ds_read_b128 a[204:207], v148 offset:6144\n"
"v_accvgpr_mov_b32 a20, a164\n"
"v_accvgpr_mov_b32 a23, a167\n"
"v_accvgpr_mov_b32 a28, a172\n"
"v_accvgpr_mov_b32 a36, a180\n"
"v_accvgpr_mov_b32 a38, a182\n"
"v_accvgpr_mov_b32 a47, a191\n"
"v_accvgpr_mov_b32 a148, a52\n"
"v_mul_f32_dpp v25, v246, v25 row_newbcast:1 row_mask:0xf bank_mask:0xf\n"
"v_mfma_f32_16x16x32_bf16 v[184:187], v[124:127], a[16:19], 0\n"
"v_accvgpr_mov_b32 a44, a188\n"
"v_mul_f32_dpp v27, v246, v27 row_newbcast:3 row_mask:0xf bank_mask:0xf\n"
"v_mfma_f32_16x16x32_bf16 v[184:187], v[128:131], a[20:23], v[184:187]\n"
"v_mov_b32 v252, v192\n"
"v_accvgpr_mov_b32 a29, a173\n"
"v_mfma_f32_16x16x32_bf16 v[184:187], v[132:135], a[24:27], v[184:187]\n"
"s_add_u32 s40, s56, s53\n"
"ds_read_b128 v[216:219], v228 offset:1024\n"
"v_sub_f32 v180, s74, v180\n"
"v_mfma_f32_16x16x32_bf16 v[184:187], v[136:139], a[28:31], v[184:187]\n"
"buffer_load_dwordx4 v240, s[20:23], s40 offen offset:2048 nt lds\n"
"v_mov_b32_dpp v206, v206 row_ror:12 row_mask:0x2 bank_mask:0xf\n"
"v_accvgpr_mov_b32 a149, a53\n"
"v_accvgpr_mov_b32 a164, a68\n"
"v_accvgpr_mov_b32 a180, a84\n"
"v_accvgpr_mov_b32 a190, a94\n"
"v_accvgpr_mov_b32 a147, a51\n"
"v_accvgpr_mov_b32 a179, a83\n"
"v_accvgpr_mov_b32 a187, a91\n"
"v_mfma_f32_16x16x32_bf16 v[188:191], v[124:127], a[32:35], 0\n"
"v_mov_b32_dpp v247, v247 row_ror:8 row_mask:0x4 bank_mask:0xf\n"
"v_accvgpr_mov_b32 a163, a67\n"
"v_mfma_f32_16x16x32_bf16 v[188:191], v[128:131], a[36:39], v[188:191]\n"
"v_mov_b32_dpp v247, v247 row_ror:4 row_mask:0x8 bank_mask:0xf\n"
"v_sub_f32 v183, s74, v183\n"
"v_mfma_f32_16x16x32_bf16 v[188:191], v[132:135], a[40:43], v[188:191]\n"
"v_accvgpr_mov_b32 a150, a54\n"
"v_sub_f32 v181, s74, v181\n"
"v_mfma_f32_16x16x32_bf16 v[188:191], v[136:139], a[44:47], v[188:191]\n"
"s_waitcnt lgkmcnt(1)\n"
"ds_write_b128 v244, a[232:235] offset:34816\n"
"v_mov_b32_dpp v206, v206 row_ror:8 row_mask:0x4 bank_mask:0xf\n"
"v_accvgpr_mov_b32 a172, a76\n"
"v_accvgpr_mov_b32 a173, a77\n"
"v_sub_f32 v185, s74, v185\n"
"v_sub_f32 v187, s74, v187\n"
"v_mfma_f32_16x16x32_bf16 v[192:195], v[124:127], a[192:195], 0\n"
"v_accvgpr_mov_b32 a151, a55\n"
"v_sub_f32 v182, s74, v182\n"
"v_mfma_f32_16x16x32_bf16 v[192:195], v[128:131], a[196:199], v[192:195]\n"
"ds_read_b64_tr_b16 a[246:247], v232 offset:53248\n"
"v_accvgpr_mov_b32 a146, a50\n"
"v_mfma_f32_16x16x32_bf16 v[192:195], v[132:135], a[200:203], v[192:195]\n"
"v_accvgpr_mov_b32 a157, a61\n"
"v_accvgpr_mov_b32 a169, a73\n"
"v_mfma_f32_16x16x32_bf16 v[192:195], v[136:139], a[204:207], v[192:195]\n"
"ds_read_b64_tr_b16 v[224:225], v239 offset:50176\n"
"ds_read_b64_tr_b16 v[226:227], v239 offset:54272\n"
"v_accvgpr_mov_b32 a183, a87\n"
"v_accvgpr_mov_b32 a145, a49\n"
"v_accvgpr_mov_b32 a176, a80\n"
"v_accvgpr_mov_b32 a178, a82\n"
"v_accvgpr_mov_b32 a171, a75\n"
"v_accvgpr_mov_b32 a185, a89\n"
"v_sub_f32 v184, s74, v184\n"
"v_mul_f32_dpp v56, v247, v56 row_newbcast:0 row_mask:0xf bank_mask:0xf\n"
"v_mfma_f32_16x16x32_bf16 v[152:155], v[208:211], a[0:3], v[180:183]\n"
"v_mul_f32_dpp v45, v247, v45 row_newbcast:1 row_mask:0xf bank_mask:0xf\n"
"v_sub_f32 v186, s74, v186\n"
"v_mfma_f32_16x16x32_bf16 v[152:155], v[212:215], a[4:7], v[152:155]\n"
"s_waitcnt lgkmcnt(4)\n"
"v_accvgpr_mov_b32 a175, a79\n"
"v_sub_f32 v188, s74, v188\n"
"v_mfma_f32_16x16x32_bf16 v[152:155], v[216:219], a[8:11], v[152:155]\n"
"s_add_u32 s40, s56, s54\n"
"v_sub_f32 v195, s74, v195\n"
"v_mov_b64 v[196:197], 0\n"
"v_mfma_f32_16x16x32_bf16 v[152:155], v[220:223], a[12:15], v[152:155]\n"
"buffer_load_dwordx4 v240, s[20:23], s40 offen offset:3072 nt lds\n"
"v_accvgpr_mov_b32 a191, a95\n"
"v_accvgpr_mov_b32 a162, a66\n"
"v_accvgpr_mov_b32 a153, a57\n"
"v_mul_f32_dpp v15, v246, v15 row_newbcast:3 row_mask:0xf bank_mask:0xf\n"
"v_sub_f32 v190, s74, v190\n"
"v_sub_f32 v191, s74, v191\n"
"v_sub_f32 v192, s74, v192\n"
"v_mfma_f32_16x16x32_bf16 v[156:159], v[216:219], a[24:27], v[184:187]\n"
"s_add_u32 s55, s55, s59\n"
"v_mov_b32_dpp v206, v206 row_ror:4 row_mask:0x8 bank_mask:0xf\n"
"v_accvgpr_mov_b32 a186, a90\n"
"v_mfma_f32_16x16x32_bf16 v[156:159], v[220:223], a[28:31], v[156:159]\n"
"s_add_u32 s41, s55, s57\n"
"v_accvgpr_mov_b32 a158, a62\n"
"v_sub_f32 v189, s74, v189\n"
"v_mfma_f32_16x16x32_bf16 v[156:159], v[208:211], a[16:19], v[156:159]\n"
"s_and_b32 s41, s41, s58\n"
"v_mul_f32_dpp v41, v247, v41 row_newbcast:1 row_mask:0xf bank_mask:0xf\n"
"v_mul_f32_dpp v26, v246, v26 row_newbcast:2 row_mask:0xf bank_mask:0xf\n"
"v_mfma_f32_16x16x32_bf16 v[156:159], v[212:215], a[20:23], v[156:159]\n"
"s_andn2_b32 s56, s55, s58\n"
"ds_read_b64_tr_b16 a[254:255], v235 offset:54272\n"
"v_accvgpr_mov_b32 a167, a71\n"
"v_accvgpr_mov_b32 a181, a85\n"
"v_accvgpr_mov_b32 a174, a78\n"
"v_accvgpr_mov_b32 a188, a92\n"
"v_accvgpr_mov_b32 a154, a58\n"
"v_accvgpr_mov_b32 a168, a72\n"
"v_mul_f32_dpp v46, v247, v46 row_newbcast:2 row_mask:0xf bank_mask:0xf\n"
"v_sub_f32 v193, s74, v193\n"
"v_mul_f32_dpp v58, v247, v58 row_newbcast:2 row_mask:0xf bank_mask:0xf\n"
"v_mfma_f32_16x16x32_bf16 v[160:163], v[208:211], a[32:35], v[188:191]\n"
"s_or_b32 s56, s56, s41\n"
"v_accvgpr_mov_b32 a165, a69\n"
"v_sub_f32 v194, s74, v194\n"
"v_mfma_f32_16x16x32_bf16 v[160:163], v[212:215], a[36:39], v[160:163]\n"
"v_accvgpr_mov_b32 a159, a63\n"
"v_accvgpr_mov_b32 a170, a74\n"
"v_mfma_f32_16x16x32_bf16 v[160:163], v[216:219], a[40:43], v[160:163]\n"
"v_accvgpr_mov_b32 a155, a59\n"
"v_accvgpr_mov_b32 a184, a88\n"
"v_mfma_f32_16x16x32_bf16 v[160:163], v[220:223], a[44:47], v[160:163]\n"
"ds_write_b128 v244, a[216:219] offset:32768\n"
"v_accvgpr_mov_b32 a160, a64\n"
"v_accvgpr_mov_b32 a177, a81\n"
"v_mul_f32_dpp v8, v246, v8 row_newbcast:0 row_mask:0xf bank_mask:0xf\n"
"v_exp_f32 v154, v154\n"
"v_mfma_f32_16x16x32_bf16 v[164:167], v[216:219], a[200:203], v[192:195]\n"
"v_exp_f32 v153, v153\n"
"v_mfma_f32_16x16x32_bf16 v[164:167], v[220:223], a[204:207], v[164:167]\n"
"ds_read_b128 v[220:223], v229 offset:5120\n"
"v_mul_f32_dpp v9, v246, v9 row_newbcast:1 row_mask:0xf bank_mask:0xf\n"
"v_mfma_f32_16x16x32_bf16 v[164:167], v[208:211], a[192:195], v[164:167]\n"
"v_accvgpr_mov_b32 a189, a93\n"
"v_accvgpr_mov_b32 a152, a56\n"
"v_mfma_f32_16x16x32_bf16 v[164:167], v[212:215], a[196:199], v[164:167]\n"
"s_waitcnt lgkmcnt(2)\n"
"ds_read_b128 v[216:219], v228 offset:5120\n"
"v_accvgpr_mov_b32 a182, a86\n"
"v_mul_f32_dpp v40, v247, v40 row_newbcast:0 row_mask:0xf bank_mask:0xf\n"
"v_exp_f32 v155, v155\n"
"v_exp_f32 v160, v160\n"
"v_exp_f32 v163, v163\n"
"LTC_%=:\n"
"s_waitcnt lgkmcnt(3)\n"
"v_mfma_f32_16x16x32_bf16 v[92:95], v[116:119], v[224:227], v[92:95]\n"
"ds_read_b128 v[212:215], v229 offset:4096\n"
"v_mul_f32_dpp v59, v247, v59 row_newbcast:3 row_mask:0xf bank_mask:0xf\n"
"v_mfma_f32_16x16x32_bf16 v[92:95], v[112:115], a[252:255], v[92:95]\n"
"ds_read_b128 v[208:211], v228 offset:4096\n"
"v_mul_f32_dpp v79, v206, v79 row_newbcast:3 row_mask:0xf bank_mask:0xf\n"
"v_mfma_f32_16x16x32_bf16 v[60:63], v[108:111], v[224:227], v[60:63]\n"
"s_add_u32 m0, s44, 32768\n"
"v_mul_f32_dpp v10, v246, v10 row_newbcast:2 row_mask:0xf bank_mask:0xf\n"
"v_cvt_pk_bf16_f32 v129, v154, v155\n"
"v_mfma_f32_16x16x32_bf16 v[60:63], v[104:107], a[252:255], v[60:63]\n"
"buffer_load_dwordx4 v240, s[16:19], s56 offen nt lds\n"
"v_cvt_pk_bf16_f32 v40, v40, v41\n"
"v_mul_f32_dpp v72, v206, v72 row_newbcast:0 row_mask:0xf bank_mask:0xf\n"
"v_exp_f32 v152, v152\n"
"v_exp_f32 v166, v166\n"
"v_mul_f32_dpp v57, v247, v57 row_newbcast:1 row_mask:0xf bank_mask:0xf\n"
"v_mov_b64 v[200:201], 0\n"
"v_mfma_f32_16x16x32_bf16 v[28:31], v[100:103], v[224:227], v[28:31]\n"
"v_cvt_pk_bf16_f32 v8, v8, v9\n"
"v_mul_f32_dpp v42, v247, v42 row_newbcast:2 row_mask:0xf bank_mask:0xf\n"
"v_mfma_f32_16x16x32_bf16 v[28:31], v[96:99], a[252:255], v[28:31]\n"
"v_cvt_pk_bf16_f32 v128, v152, v153\n"
"v_mul_f32_dpp v24, v246, v24 row_newbcast:0 row_mask:0xf bank_mask:0xf\n"
"v_mfma_f32_16x16x32_bf16 a[236:239], v[120:123], a[252:255], a[236:239]\n"
"s_waitcnt lgkmcnt(2)\n"
"v_exp_f32 v159, v159\n"
"v_mfma_f32_16x16x32_bf16 v[168:171], v[216:219], a[8:11], v[180:183]\n"
"v_mul_f32_dpp v47, v247, v47 row_newbcast:3 row_mask:0xf bank_mask:0xf\n"
"v_mul_f32_dpp v88, v206, v88 row_newbcast:0 row_mask:0xf bank_mask:0xf\n"
"v_mfma_f32_16x16x32_bf16 v[168:171], v[220:223], a[12:15], v[168:171]\n"
"s_waitcnt lgkmcnt(0)\n"
"ds_read_b64_tr_b16 a[244:245], v232 offset:49152\n"
"v_mul_f32_dpp v75, v206, v75 row_newbcast:3 row_mask:0xf bank_mask:0xf\n"
"v_mfma_f32_16x16x32_bf16 v[168:171], v[208:211], a[0:3], v[168:171]\n"
"v_exp_f32 v158, v158\n"
"v_mfma_f32_16x16x32_bf16 v[168:171], v[212:215], a[4:7], v[168:171]\n"
"ds_write_b128 v244, a[236:239] offset:35840\n"
"v_mul_f32_dpp v43, v247, v43 row_newbcast:3 row_mask:0xf bank_mask:0xf\n"
"v_exp_f32 v162, v162\n"
"v_exp_f32 v167, v167\n"
"v_mfma_f32_16x16x32_bf16 v[172:175], v[208:211], a[16:19], v[184:187]\n"
"v_exp_f32 v165, v165\n"
"v_mfma_f32_16x16x32_bf16 v[172:175], v[212:215], a[20:23], v[172:175]\n"
"v_exp_f32 v161, v161\n"
"v_mfma_f32_16x16x32_bf16 v[172:175], v[216:219], a[24:27], v[172:175]\n"
"ds_read_b64_tr_b16 a[250:251], v236 offset:53248\n"
"v_mul_f32_dpp v77, v206, v77 row_newbcast:1 row_mask:0xf bank_mask:0xf\n"
"v_mfma_f32_16x16x32_bf16 v[172:175], v[220:223], a[28:31], v[172:175]\n"
"ds_write_b128 v244, a[220:223] offset:33792\n"
"v_mul_f32_dpp v11, v246, v11 row_newbcast:3 row_mask:0xf bank_mask:0xf\n"
"v_mul_f32_dpp v73, v206, v73 row_newbcast:1 row_mask:0xf bank_mask:0xf\n"
"v_mul_f32_dpp v94, v206, v94 row_newbcast:2 row_mask:0xf bank_mask:0xf\n"
"v_exp_f32 v171, v171\n"
"v_mfma_f32_16x16x32_bf16 v[176:179], v[216:219], a[40:43], v[188:191]\n"
"v_mul_f32_dpp v89, v206, v89 row_newbcast:1 row_mask:0xf bank_mask:0xf\n"
"v_cvt_pk_bf16_f32 v88, v88, v89\n"
"v_mfma_f32_16x16x32_bf16 v[176:179], v[220:223], a[44:47], v[176:179]\n"
"ds_read_b64_tr_b16 a[248:249], v236 offset:49152\n"
"v_xor_b32 v250, 0x1000, v245\n"
"v_mfma_f32_16x16x32_bf16 v[176:179], v[208:211], a[32:35], v[176:179]\n"
"s_add_u32 s40, s56, s52\n"
"ds_read_b64_tr_b16 a[252:253], v233 offset:49152\n"
"v_cvt_pk_bf16_f32 v144, v160, v161\n"
"v_mfma_f32_16x16x32_bf16 v[176:179], v[212:215], a[36:39], v[176:179]\n"
"buffer_load_dwordx4 v240, s[16:19], s40 offen offset:1024 nt lds\n"
"v_mul_f32_dpp v12, v246, v12 row_newbcast:0 row_mask:0xf bank_mask:0xf\n"
"v_exp_f32 v156, v156\n"
"v_exp_f32 v157, v157\n"
"v_mul_f32_dpp v60, v247, v60 row_newbcast:0 row_mask:0xf bank_mask:0xf\n"
"v_exp_f32 v169, v169\n"
"v_mfma_f32_16x16x32_bf16 v[152:155], v[208:211], a[192:195], v[192:195]\n"
"v_mul_f32_dpp v76, v206, v76 row_newbcast:0 row_mask:0xf bank_mask:0xf\n"
"v_mul_f32_dpp v29, v246, v29 row_newbcast:1 row_mask:0xf bank_mask:0xf\n"
"v_mfma_f32_16x16x32_bf16 v[152:155], v[212:215], a[196:199], v[152:155]\n"
"v_exp_f32 v164, v164\n"
"v_mfma_f32_16x16x32_bf16 v[152:155], v[216:219], a[200:203], v[152:155]\n"
"ds_read_b128 v[212:215], v231 offset:0\n"
"v_cvt_pk_bf16_f32 v56, v56, v57\n"
"v_mfma_f32_16x16x32_bf16 v[152:155], v[220:223], a[204:207], v[152:155]\n"
"s_waitcnt lgkmcnt(3)\n"
"v_cvt_pk_bf16_f32 v9, v10, v11\n"
"v_mul_f32_dpp v92, v206, v92 row_newbcast:0 row_mask:0xf bank_mask:0xf\n"
"v_mfma_f32_16x16x32_bf16 a[208:211], v[120:123], a[244:247], a[208:211]\n"
"ds_read_b128 v[208:211], v230 offset:0\n"
"v_cvt_pk_bf16_f32 v41, v42, v43\n"
"v_mul_f32_dpp v78, v206, v78 row_newbcast:2 row_mask:0xf bank_mask:0xf\n"
"v_cvt_pk_bf16_f32 v72, v72, v73\n"
"v_mul_f32_dpp v31, v246, v31 row_newbcast:3 row_mask:0xf bank_mask:0xf\n"
"v_mul_f32_dpp v61, v247, v61 row_newbcast:1 row_mask:0xf bank_mask:0xf\n"
"v_exp_f32 v177, v177\n"
"v_mfma_f32_16x16x32_bf16 v[0:3], v[96:99], a[244:247], v[0:3]\n"
"s_waitcnt lgkmcnt(3)\n"
"ds_read_b128 v[216:219], v230 offset:1024\n"
"v_cvt_pk_bf16_f32 v145, v162, v163\n"
"v_mfma_f32_16x16x32_bf16 v[0:3], v[100:103], a[248:251], v[0:3]\n"
"ds_read_b128 v[220:223], v231 offset:1024\n"
"v_cvt_pk_bf16_f32 v137, v158, v159\n"
"v_mul_f32_dpp v91, v206, v91 row_newbcast:3 row_mask:0xf bank_mask:0xf\n"
"v_exp_f32 v170, v170\n"
"v_exp_f32 v172, v172\n"
"v_exp_f32 v155, v155\n"
"v_mfma_f32_16x16x32_bf16 v[32:35], v[104:107], a[244:247], v[32:35]\n"
"v_cvt_pk_bf16_f32 v136, v156, v157\n"
"v_xor_b32 v244, 0x1000, v244\n"
"v_mfma_f32_16x16x32_bf16 v[32:35], v[108:111], a[248:251], v[32:35]\n"
"ds_read_b64_tr_b16 a[254:255], v233 offset:53248\n"
"v_mul_f32_dpp v90, v206, v90 row_newbcast:2 row_mask:0xf bank_mask:0xf\n"
"v_mfma_f32_16x16x32_bf16 v[64:67], v[112:115], a[244:247], v[64:67]\n"
"ds_read_b64_tr_b16 v[224:225], v237 offset:49152\n"
"v_mul_f32_dpp v93, v206, v93 row_newbcast:1 row_mask:0xf bank_mask:0xf\n"
"v_mfma_f32_16x16x32_bf16 v[64:67], v[116:119], a[248:251], v[64:67]\n"
"s_waitcnt lgkmcnt(4)\n"
"ds_read_b64_tr_b16 v[226:227], v237 offset:53248\n"
"v_exp_f32 v168, v168\n"
"v_exp_f32 v175, v175\n"
"v_exp_f32 v176, v176\n"
"v_exp_f32 v179, v179\n"
"v_mfma_f32_16x16x32_bf16 v[156:159], v[208:211], a[0:3], v[180:183]\n"
"v_mul_f32_dpp v13, v246, v13 row_newbcast:1 row_mask:0xf bank_mask:0xf\n"
"v_mul_f32_dpp v63, v247, v63 row_newbcast:3 row_mask:0xf bank_mask:0xf\n"
"v_mfma_f32_16x16x32_bf16 v[156:159], v[212:215], a[4:7], v[156:159]\n"
"s_waitcnt lgkmcnt(4)\n"
"ds_read_b64_tr_b16 a[246:247], v232 offset:54272\n"
"v_cvt_pk_bf16_f32 v125, v166, v167\n"
"v_mfma_f32_16x16x32_bf16 v[156:159], v[216:219], a[8:11], v[156:159]\n"
"s_waitcnt lgkmcnt(4)\n"
"s_add_u32 s40, s56, s53\n"
"v_mul_f32_dpp v74, v206, v74 row_newbcast:2 row_mask:0xf bank_mask:0xf\n"
"v_mul_f32_dpp v2, v246, v2 row_newbcast:2 row_mask:0xf bank_mask:0xf\n"
"v_mfma_f32_16x16x32_bf16 v[156:159], v[220:223], a[12:15], v[156:159]\n"
"buffer_load_dwordx4 v240, s[16:19], s40 offen offset:2048 nt lds\n"
"v_cvt_pk_bf16_f32 v124, v164, v165\n"
"v_cvt_pk_bf16_f32 v24, v24, v25\n"
"v_mul_f32_dpp v62, v247, v62 row_newbcast:2 row_mask:0xf bank_mask:0xf\n"
"v_exp_f32 v173, v173\n"
"v_exp_f32 v154, v154\n"
"v_accvgpr_read_b32 v255, a209\n"
"v_mfma_f32_16x16x32_bf16 v[160:163], v[216:219], a[24:27], v[184:187]\n"
"v_cvt_pk_bf16_f32 v57, v58, v59\n"
"v_cvt_pk_bf16_f32 v89, v90, v91\n"
"v_mfma_f32_16x16x32_bf16 v[160:163], v[220:223], a[28:31], v[160:163]\n"
"v_mul_f32_dpp v14, v246, v14 row_newbcast:2 row_mask:0xf bank_mask:0xf\n"
"v_cvt_pk_bf16_f32 v73, v74, v75\n"
"v_mfma_f32_16x16x32_bf16 v[160:163], v[208:211], a[16:19], v[160:163]\n"
"v_cvt_pk_bf16_f32 v59, v62, v63\n"
"v_cvt_pk_bf16_f32 v146, v176, v177\n"
"v_mfma_f32_16x16x32_bf16 v[160:163], v[212:215], a[20:23], v[160:163]\n"
"ds_write_b64 v250, v[252:253] offset:52224\n"
"v_cvt_pk_bf16_f32 v10, v12, v13\n"
"v_cvt_pk_bf16_f32 v130, v168, v169\n"
"v_exp_f32 v174, v174\n"
"v_exp_f32 v157, v157\n"
"v_mul_f32_dpp v0, v246, v0 row_newbcast:0 row_mask:0xf bank_mask:0xf\n"
"v_mfma_f32_16x16x32_bf16 v[164:167], v[208:211], a[32:35], v[188:191]\n"
"ds_read_b128 v[208:211], v230 offset:4096\n"
"v_cvt_pk_bf16_f32 v75, v78, v79\n"
"v_mfma_f32_16x16x32_bf16 v[164:167], v[212:215], a[36:39], v[164:167]\n"
"v_cvt_pk_bf16_f32 v11, v14, v15\n"
"v_mul_f32_dpp v44, v247, v44 row_newbcast:0 row_mask:0xf bank_mask:0xf\n"
"v_mfma_f32_16x16x32_bf16 v[164:167], v[216:219], a[40:43], v[164:167]\n"
"ds_read_b64_tr_b16 a[244:245], v232 offset:50176\n"
"ds_read_b128 v[216:219], v230 offset:5120\n"
"v_mfma_f32_16x16x32_bf16 v[164:167], v[220:223], a[44:47], v[164:167]\n"
"s_waitcnt lgkmcnt(4)\n"
"v_cvt_pk_bf16_f32 v43, v46, v47\n"
"v_exp_f32 v178, v178\n"
"v_exp_f32 v152, v152\n"
"v_exp_f32 v158, v158\n"
"v_mul_f32_dpp v3, v246, v3 row_newbcast:3 row_mask:0xf bank_mask:0xf\n"
"v_mfma_f32_16x16x32_bf16 v[68:71], v[116:119], v[224:227], v[68:71]\n"
"ds_read_b128 v[212:215], v231 offset:4096\n"
"v_cvt_pk_bf16_f32 v25, v26, v27\n"
"v_mfma_f32_16x16x32_bf16 v[68:71], v[112:115], a[252:255], v[68:71]\n"
"v_mul_f32_dpp v33, v247, v33 row_newbcast:1 row_mask:0xf bank_mask:0xf\n"
"v_mov_b64 v[202:203], 0\n"
"v_mfma_f32_16x16x32_bf16 v[36:39], v[108:111], v[224:227], v[36:39]\n"
"s_add_u32 s40, s56, s54\n"
"ds_read_b64_tr_b16 a[248:249], v236 offset:50176\n"
"ds_read_b128 v[220:223], v231 offset:5120\n"
"v_mfma_f32_16x16x32_bf16 v[36:39], v[104:107], a[252:255], v[36:39]\n"
"buffer_load_dwordx4 v240, s[16:19], s40 offen offset:3072 nt lds\n"
"v_cvt_pk_bf16_f32 v42, v44, v45\n"
"v_cvt_pk_bf16_f32 v147, v178, v179\n"
"v_exp_f32 v160, v160\n"
"v_exp_f32 v161, v161\n"
"v_mul_f32_dpp v32, v247, v32 row_newbcast:0 row_mask:0xf bank_mask:0xf\n"
"v_mfma_f32_16x16x32_bf16 v[4:7], v[100:103], v[224:227], v[4:7]\n"
"v_cvt_pk_bf16_f32 v58, v60, v61\n"
"v_mul_f32_dpp v95, v206, v95 row_newbcast:3 row_mask:0xf bank_mask:0xf\n"
"v_mfma_f32_16x16x32_bf16 v[4:7], v[96:99], a[252:255], v[4:7]\n"
"v_mul_f32_dpp v30, v246, v30 row_newbcast:2 row_mask:0xf bank_mask:0xf\n"
"v_cvt_pk_bf16_f32 v138, v172, v173\n"
"v_cvt_pk_bf16_f32 v139, v174, v175\n"
"v_exp_f32 v159, v159\n"
"v_exp_f32 v162, v162\n"
"v_mul_f32_dpp v1, v246, v1 row_newbcast:1 row_mask:0xf bank_mask:0xf\n"
"v_mul_f32_dpp v67, v206, v67 row_newbcast:3 row_mask:0xf bank_mask:0xf\n"
"v_mfma_f32_16x16x32_bf16 a[212:215], v[120:123], a[252:255], a[212:215]\n"
"s_waitcnt lgkmcnt(3)\n"
"ds_write_b128 v248, v[8:11] offset:49152\n"
"v_cvt_pk_bf16_f32 v27, v30, v31\n"
"v_cvt_pk_bf16_f32 v131, v170, v171\n"
"v_cvt_pk_bf16_f32 v0, v0, v1\n"
"v_cvt_pk_bf16_f32 v32, v32, v33\n"
"v_mul_f32_dpp v66, v206, v66 row_newbcast:2 row_mask:0xf bank_mask:0xf\n"
"v_mfma_f32_16x16x32_bf16 v[168:171], v[208:211], a[0:3], v[180:183]\n"
"s_waitcnt lgkmcnt(3)\n"
"v_cvt_pk_bf16_f32 v74, v76, v77\n"
"v_mul_f32_dpp v70, v206, v70 row_newbcast:2 row_mask:0xf bank_mask:0xf\n"
"v_mfma_f32_16x16x32_bf16 v[168:171], v[212:215], a[4:7], v[168:171]\n"
"v_mul_f32_dpp v28, v246, v28 row_newbcast:0 row_mask:0xf bank_mask:0xf\n"
"v_mul_f32_dpp v4, v246, v4 row_newbcast:0 row_mask:0xf bank_mask:0xf\n"
"v_mfma_f32_16x16x32_bf16 v[168:171], v[216:219], a[8:11], v[168:171]\n"
"s_waitcnt lgkmcnt(1)\n"
"ds_read_b64_tr_b16 a[250:251], v236 offset:54272\n"
"v_cvt_pk_bf16_f32 v26, v28, v29\n"
"v_mfma_f32_16x16x32_bf16 v[168:171], v[220:223], a[12:15], v[168:171]\n"
"ds_read_b64_tr_b16 a[54:55], v249 offset:49160\n"
"v_cvt_pk_bf16_f32 v140, v160, v161\n"
"v_mfma_f32_16x16x32_bf16 v[172:175], v[216:219], a[24:27], v[184:187]\n"
"ds_write_b128 v248, v[40:43] offset:50176\n"
"v_exp_f32 v153, v153\n"
"v_mul_f32_dpp v6, v246, v6 row_newbcast:2 row_mask:0xf bank_mask:0xf\n"
"v_mul_f32_dpp v36, v247, v36 row_newbcast:0 row_mask:0xf bank_mask:0xf\n"
"v_mul_f32_dpp v38, v247, v38 row_newbcast:2 row_mask:0xf bank_mask:0xf\n"
"v_mfma_f32_16x16x32_bf16 v[172:175], v[220:223], a[28:31], v[172:175]\n"
"ds_read_b64_tr_b16 a[252:253], v233 offset:50176\n"
"v_cvt_pk_bf16_f32 v90, v92, v93\n"
"v_mfma_f32_16x16x32_bf16 v[172:175], v[208:211], a[16:19], v[172:175]\n"
"v_cvt_pk_bf16_f32 v126, v152, v153\n"
"v_mul_f32_dpp v39, v247, v39 row_newbcast:3 row_mask:0xf bank_mask:0xf\n"
"v_mfma_f32_16x16x32_bf16 v[172:175], v[212:215], a[20:23], v[172:175]\n"
"s_cbranch_vccnz LPCT0_%=\nLPKT0_%=:\n"
"ds_read_b64_tr_b16 a[52:53], v249 offset:49152\n"
"ds_read_b64_tr_b16 v[226:227], v237 offset:54272\n"
"v_cvt_pk_bf16_f32 v1, v2, v3\n"
"v_mul_f32_dpp v64, v206, v64 row_newbcast:0 row_mask:0xf bank_mask:0xf\n"
"v_mul_f32_dpp v65, v206, v65 row_newbcast:1 row_mask:0xf bank_mask:0xf\n"
"v_exp_f32 v169, v169\n"
"v_mov_b64 v[198:199], 0\n"
"v_mfma_f32_16x16x32_bf16 v[176:179], v[208:211], a[32:35], v[188:191]\n"
"ds_read_b64_tr_b16 a[70:71], v249 offset:50184\n"
"v_mul_f32_dpp v34, v247, v34 row_newbcast:2 row_mask:0xf bank_mask:0xf\n"
"v_mfma_f32_16x16x32_bf16 v[176:179], v[212:215], a[36:39], v[176:179]\n"
"ds_read_b64_tr_b16 a[254:255], v233 offset:54272\n"
"ds_read_b64_tr_b16 v[224:225], v237 offset:50176\n"
"v_mfma_f32_16x16x32_bf16 v[176:179], v[216:219], a[40:43], v[176:179]\n"
"ds_read_b64_tr_b16 a[68:69], v249 offset:50176\n"
"v_mul_f32_dpp v69, v206, v69 row_newbcast:1 row_mask:0xf bank_mask:0xf\n"
"v_mfma_f32_16x16x32_bf16 v[176:179], v[220:223], a[44:47], v[176:179]\n"
"ds_write_b128 v248, v[72:75] offset:51200\n"
"v_mul_f32_dpp v5, v246, v5 row_newbcast:1 row_mask:0xf bank_mask:0xf\n"
"v_exp_f32 v170, v170\n"
"v_exp_f32 v171, v171\n"
"v_mfma_f32_16x16x32_bf16 a[224:227], v[120:123], a[244:247], a[224:227]\n"
"ds_write_b128 v248, v[24:27] offset:49152\n"
"v_cvt_pk_bf16_f32 v133, v158, v159\n"
"v_exp_f32 v167, v167\n"
"v_exp_f32 v168, v168\n"
"v_mfma_f32_16x16x32_bf16 v[16:19], v[96:99], a[244:247], v[16:19]\n"
"s_waitcnt vmcnt(8) lgkmcnt(3)\n"
"v_exp_f32 v156, v156\n"
"v_mfma_f32_16x16x32_bf16 v[16:19], v[100:103], a[248:251], v[16:19]\n"
"s_barrier\n"
"ds_read_b64_tr_b16 a[86:87], v249 offset:51208\n"
"ds_read_b64_tr_b16 a[60:61], v249 offset:49152\n"
"ds_read_b64 v[250:251], v245 offset:52224\n"
"v_exp_f32 v164, v164\n"
"v_mul_f32_dpp v35, v247, v35 row_newbcast:3 row_mask:0xf bank_mask:0xf\n"
"v_cvt_pk_bf16_f32 v33, v34, v35\n"
"v_mfma_f32_16x16x32_bf16 v[48:51], v[104:107], a[244:247], v[48:51]\n"
"ds_read_b128 v[216:219], v228 offset:17408\n"
"v_cvt_pk_bf16_f32 v64, v64, v65\n"
"v_mfma_f32_16x16x32_bf16 v[48:51], v[108:111], a[248:251], v[48:51]\n"
"ds_read_b128 v[208:211], v228 offset:16384\n"
"v_exp_f32 v163, v163\n"
"v_exp_f32 v165, v165\n"
"v_exp_f32 v166, v166\n"
"v_cvt_pk_bf16_f32 v35, v38, v39\n"
"v_mul_f32_dpp v68, v206, v68 row_newbcast:0 row_mask:0xf bank_mask:0xf\n"
"v_mfma_f32_16x16x32_bf16 v[80:83], v[112:115], a[244:247], v[80:83]\n"
"ds_read_b128 v[212:215], v229 offset:16384\n"
"v_cvt_pk_bf16_f32 v65, v66, v67\n"
"v_mfma_f32_16x16x32_bf16 v[80:83], v[116:119], a[248:251], v[80:83]\n"
"ds_read_b64_tr_b16 a[62:63], v249 offset:49160\n"
"ds_read_b128 v[220:223], v229 offset:17408\n"
"v_cvt_pk_bf16_f32 v91, v94, v95\n"
"v_mul_f32_dpp v7, v246, v7 row_newbcast:3 row_mask:0xf bank_mask:0xf\n"
"v_cvt_pk_bf16_f32 v2, v4, v5\n"
"v_cvt_pk_bf16_f32 v3, v6, v7\n"
"v_exp_f32 v173, v173\n"
"v_exp_f32 v176, v176\n"
"v_mfma_f32_16x16x32_bf16 v[84:87], v[116:119], v[224:227], v[84:87]\n"
"ds_read_b128 v[60:63], v244 offset:35840\n"
"v_mul_f32_dpp v71, v206, v71 row_newbcast:3 row_mask:0xf bank_mask:0xf\n"
"v_mfma_f32_16x16x32_bf16 v[84:87], v[112:115], a[252:255], v[84:87]\n"
"ds_write_b128 v248, v[56:59] offset:50176\n"
"v_cvt_pk_bf16_f32 v149, v166, v167\n"
"v_cvt_pk_bf16_f32 v66, v68, v69\n"
"v_exp_f32 v178, v178\n"
"v_mfma_f32_16x16x32_bf16 v[52:55], v[108:111], v[224:227], v[52:55]\n"
"ds_read_b64_tr_b16 a[84:85], v249 offset:51200\n"
"v_cvt_pk_bf16_f32 v67, v70, v71\n"
"v_mfma_f32_16x16x32_bf16 v[52:55], v[104:107], a[252:255], v[52:55]\n"
"s_waitcnt lgkmcnt(8)\n"
"ds_write_b128 v248, v[88:91] offset:51200\n"
"v_min_f32 v254, v252, v250\n"
"v_sub_f32 v252, v254, v252\n"
"v_sub_f32 v250, v254, v250\n"
"v_exp_f32 v252, v252\n"
"v_mfma_f32_16x16x32_bf16 v[20:23], v[100:103], v[224:227], v[20:23]\n"
"ds_read_b64_tr_b16 a[76:77], v249 offset:50176\n"
"v_mul_f32 v253, v253, v252\n"
"v_mfma_f32_16x16x32_bf16 v[20:23], v[96:99], a[252:255], v[20:23]\n"
"ds_read_b64_tr_b16 a[78:79], v249 offset:50184\n"
"ds_read_b64_tr_b16 a[92:93], v249 offset:51200\n"
"ds_read_b64_tr_b16 a[244:245], v234 offset:0\n"
"v_cvt_pk_bf16_f32 v148, v164, v165\n"
"v_mul_f32_dpp v37, v247, v37 row_newbcast:1 row_mask:0xf bank_mask:0xf\n"
"v_cvt_pk_bf16_f32 v135, v170, v171\n"
"v_exp_f32 v172, v172\n"
"v_mul_f32_dpp v17, v246, v17 row_newbcast:1 row_mask:0xf bank_mask:0xf\n"
"v_mfma_f32_16x16x32_bf16 a[228:231], v[120:123], a[252:255], a[228:231]\n"
"s_waitcnt lgkmcnt(10)\n"
"ds_write_b128 v248, v[0:3] offset:49152\n"
"v_cvt_pk_bf16_f32 v127, v154, v155\n"
"v_cvt_pk_bf16_f32 v34, v36, v37\n"
"v_exp_f32 v250, v250\n"
"v_mul_f32_dpp v18, v246, v18 row_newbcast:2 row_mask:0xf bank_mask:0xf\n"
"v_mfma_f32_16x16x32_bf16 v[152:155], v[208:211], a[0:3], v[180:183]\n"
"ds_write_b128 v248, v[32:35] offset:50176\n"
"v_cvt_pk_bf16_f32 v132, v156, v157\n"
"v_cvt_pk_bf16_f32 v141, v162, v163\n"
"v_fmac_f32 v253, v251, v250\n"
"v_rcp_f32 v253, v253\n"
"v_mfma_f32_16x16x32_bf16 v[152:155], v[212:215], a[4:7], v[152:155]\n"
"ds_read_b64_tr_b16 a[94:95], v249 offset:51208\n"
"ds_read_b64_tr_b16 a[48:49], v249 offset:49152\n"
"v_mfma_f32_16x16x32_bf16 v[152:155], v[216:219], a[8:11], v[152:155]\n"
"s_waitcnt lgkmcnt(10)\n"
"ds_write_b128 v248, v[64:67] offset:51200\n"
"v_exp_f32 v174, v174\n"
"v_mul_f32 v252, v252, v253\n"
"v_mul_f32_dpp v16, v246, v16 row_newbcast:0 row_mask:0xf bank_mask:0xf\n"
"v_mul_f32_dpp v49, v247, v49 row_newbcast:1 row_mask:0xf bank_mask:0xf\n"
"v_mfma_f32_16x16x32_bf16 v[152:155], v[220:223], a[12:15], v[152:155]\n"
"ds_read_b64_tr_b16 a[66:67], v249 offset:50184\n"
"ds_read_b64_tr_b16 a[80:81], v249 offset:51200\n"
"v_exp_f32 v175, v175\n"
"v_exp_f32 v177, v177\n"
"v_exp_f32 v179, v179\n"
"v_mul_f32 v250, v250, v253\n"
"v_mul_f32_dpp v83, v206, v83 row_newbcast:3 row_mask:0xf bank_mask:0xf\n"
"v_mfma_f32_16x16x32_bf16 v[156:159], v[216:219], a[24:27], v[184:187]\n"
"ds_read_b128 v[24:27], v244 offset:32768\n"
"ds_read_b128 v[56:59], v244 offset:34816\n"
"v_mfma_f32_16x16x32_bf16 v[156:159], v[220:223], a[28:31], v[156:159]\n"
"ds_read_b64_tr_b16 a[64:65], v249 offset:50176\n"
"v_accvgpr_read_b32 v254, a208\n"
"v_mfma_f32_16x16x32_bf16 v[156:159], v[208:211], a[16:19], v[156:159]\n"
"ds_read_b64_tr_b16 a[50:51], v249 offset:49160\n"
"ds_read_b64_tr_b16 a[82:83], v249 offset:51208\n"
"v_mfma_f32_16x16x32_bf16 v[156:159], v[212:215], a[20:23], v[156:159]\n"
"ds_read_b128 v[28:31], v244 offset:33792\n"
"v_xor_b32 v249, s75, v249\n"
"v_mfma_f32_16x16x32_bf16 v[160:163], v[208:211], a[32:35], v[188:191]\n"
"ds_read_b64_tr_b16 a[246:247], v234 offset:4096\n"
"v_cvt_pk_bf16_f32 v134, v168, v169\n"
"v_mfma_f32_16x16x32_bf16 v[160:163], v[212:215], a[36:39], v[160:163]\n"
"ds_read_b64_tr_b16 a[248:249], v238 offset:0\n"
"v_mov_b32_dpp v250, v250 row_ror:12 row_mask:0x2 bank_mask:0xf\n"
"v_mfma_f32_16x16x32_bf16 v[160:163], v[216:219], a[40:43], v[160:163]\n"
"ds_read_b64_tr_b16 a[250:251], v238 offset:4096\n"
"v_mov_b32_dpp v252, v252 row_ror:12 row_mask:0x2 bank_mask:0xf\n"
"v_mfma_f32_16x16x32_bf16 v[160:163], v[220:223], a[44:47], v[160:163]\n"
"ds_read_b64_tr_b16 v[226:227], v239 offset:4096\n"
"v_cvt_pk_bf16_f32 v142, v172, v173\n"
"v_mov_b32_dpp v250, v250 row_ror:8 row_mask:0x4 bank_mask:0xf\n"
"v_accvgpr_read_b32 v253, a210\n"
"v_exp_f32 v155, v155\n"
"v_exp_f32 v159, v159\n"
"v_mfma_f32_16x16x32_bf16 v[164:167], v[216:219], a[200:203], v[192:195]\n"
"ds_read_b64_tr_b16 a[254:255], v235 offset:4096\n"
"v_mov_b32_dpp v250, v250 row_ror:4 row_mask:0x8 bank_mask:0xf\n"
"v_mfma_f32_16x16x32_bf16 v[164:167], v[220:223], a[204:207], v[164:167]\n"
"s_waitcnt lgkmcnt(8)\n"
"v_mul_f32_dpp v27, v250, v27 row_newbcast:3 row_mask:0xf bank_mask:0xf\n"
"v_mul_f32_dpp v60, v250, v60 row_newbcast:0 row_mask:0xf bank_mask:0xf\n"
"v_mfma_f32_16x16x32_bf16 v[164:167], v[208:211], a[192:195], v[164:167]\n"
"v_cvt_pk_bf16_f32 v143, v174, v175\n"
"v_cvt_pk_bf16_f32 v150, v176, v177\n"
"v_mfma_f32_16x16x32_bf16 v[164:167], v[212:215], a[196:199], v[164:167]\n"
"s_waitcnt lgkmcnt(4)\n"
"ds_read_b128 v[220:223], v229 offset:21504\n"
"v_cvt_pk_bf16_f32 v151, v178, v179\n"
"v_mul_f32_dpp v26, v250, v26 row_newbcast:2 row_mask:0xf bank_mask:0xf\n"
"v_mul_f32_dpp v19, v246, v19 row_newbcast:3 row_mask:0xf bank_mask:0xf\n"
"v_mul_f32_dpp v84, v206, v84 row_newbcast:0 row_mask:0xf bank_mask:0xf\n"
"v_mul_f32_dpp v56, v250, v56 row_newbcast:0 row_mask:0xf bank_mask:0xf\n"
"v_exp_f32 v160, v160\n"
"v_mfma_f32_16x16x32_bf16 a[216:219], v[124:127], a[244:247], 0\n"
"ds_read_b64_tr_b16 a[252:253], v235 offset:0\n"
"ds_read_b64_tr_b16 v[224:225], v239 offset:0\n"
"v_mul_f32_dpp v24, v250, v24 row_newbcast:0 row_mask:0xf bank_mask:0xf\n"
"v_accvgpr_read_b32 v251, a211\n"
"v_mul_f32_dpp v81, v206, v81 row_newbcast:1 row_mask:0xf bank_mask:0xf\n"
"v_mul_f32_dpp v86, v206, v86 row_newbcast:2 row_mask:0xf bank_mask:0xf\n"
"v_exp_f32 v152, v152\n"
"v_exp_f32 v153, v153\n"
"v_mfma_f32_16x16x32_bf16 v[8:11], v[128:131], a[244:247], 0\n"
"s_waitcnt lgkmcnt(5)\n"
"s_add_u32 m0, s44, 98304\n"
"v_exp_f32 v156, v156\n"
"v_mfma_f32_16x16x32_bf16 v[8:11], v[132:135], a[248:251], v[8:11]\n"
"buffer_load_dwordx4 v240, s[20:23], s56 offen nt lds\n"
"v_mov_b32_dpp v252, v252 row_ror:8 row_mask:0x4 bank_mask:0xf\n"
"v_mul_f32_dpp v48, v247, v48 row_newbcast:0 row_mask:0xf bank_mask:0xf\n"
"v_mul_f32_dpp v58, v250, v58 row_newbcast:2 row_mask:0xf bank_mask:0xf\n"
"v_mul_f32_dpp v59, v250, v59 row_newbcast:3 row_mask:0xf bank_mask:0xf\n"
"v_exp_f32 v161, v161\n"
"v_exp_f32 v162, v162\n"
"v_mfma_f32_16x16x32_bf16 v[40:43], v[136:139], a[244:247], 0\n"
"v_mov_b32_dpp v252, v252 row_ror:4 row_mask:0x8 bank_mask:0xf\n"
"v_mul_f32_dpp v25, v250, v25 row_newbcast:1 row_mask:0xf bank_mask:0xf\n"
"v_mfma_f32_16x16x32_bf16 v[40:43], v[140:143], a[248:251], v[40:43]\n"
"v_fmac_f32_dpp v26, v252, v253 row_newbcast:2 row_mask:0xf bank_mask:0xf\n"
"v_mul_f32_dpp v85, v206, v85 row_newbcast:1 row_mask:0xf bank_mask:0xf\n"
"v_mfma_f32_16x16x32_bf16 v[72:75], v[144:147], a[244:247], 0\n"
"v_mul_f32_dpp v30, v250, v30 row_newbcast:2 row_mask:0xf bank_mask:0xf\n"
"v_mul_f32_dpp v57, v250, v57 row_newbcast:1 row_mask:0xf bank_mask:0xf\n"
"v_mfma_f32_16x16x32_bf16 v[72:75], v[148:151], a[248:251], v[72:75]\n"
"s_waitcnt lgkmcnt(0)\n"
"ds_read_b128 v[208:211], v228 offset:20480\n"
"v_fmac_f32_dpp v24, v252, v254 row_newbcast:0 row_mask:0xf bank_mask:0xf\n"
"v_fmac_f32_dpp v27, v252, v251 row_newbcast:3 row_mask:0xf bank_mask:0xf\n"
"v_accvgpr_read_b32 v254, a212\n"
"v_mul_f32_dpp v29, v250, v29 row_newbcast:1 row_mask:0xf bank_mask:0xf\n"
"v_accvgpr_read_b32 v253, a214\n"
"v_mul_f32_dpp v31, v250, v31 row_newbcast:3 row_mask:0xf bank_mask:0xf\n"
"v_xor_b32 v248, s75, v248\n"
"v_cvt_pk_bf16_f32 v16, v16, v17\n"
"v_cvt_pk_bf16_f32 v48, v48, v49\n"
"v_mfma_f32_16x16x32_bf16 v[76:79], v[148:151], v[224:227], 0\n"
"v_fmac_f32_dpp v25, v252, v255 row_newbcast:1 row_mask:0xf bank_mask:0xf\n"
"v_mul_f32_dpp v53, v247, v53 row_newbcast:1 row_mask:0xf bank_mask:0xf\n"
"v_mfma_f32_16x16x32_bf16 v[76:79], v[144:147], a[252:255], v[76:79]\n"
"v_mul_f32_dpp v28, v250, v28 row_newbcast:0 row_mask:0xf bank_mask:0xf\n"
"v_accvgpr_read_b32 v255, a213\n"
"v_fmac_f32_dpp v30, v252, v253 row_newbcast:2 row_mask:0xf bank_mask:0xf\n"
"v_mul_f32_dpp v51, v247, v51 row_newbcast:3 row_mask:0xf bank_mask:0xf\n"
"v_exp_f32 v157, v157\n"
"v_exp_f32 v163, v163\n"
"v_exp_f32 v164, v164\n"
"v_mfma_f32_16x16x32_bf16 v[44:47], v[140:143], v[224:227], 0\n"
"ds_read_b128 v[216:219], v228 offset:21504\n"
"v_mul_f32_dpp v54, v247, v54 row_newbcast:2 row_mask:0xf bank_mask:0xf\n"
"v_mfma_f32_16x16x32_bf16 v[44:47], v[136:139], a[252:255], v[44:47]\n"
"v_mul_f32_dpp v87, v206, v87 row_newbcast:3 row_mask:0xf bank_mask:0xf\n"
"v_accvgpr_read_b32 v253, a226\n"
"v_mfma_f32_16x16x32_bf16 v[12:15], v[132:135], v[224:227], 0\n"
"s_add_u32 s40, s56, s52\n"
"ds_read_b128 v[212:215], v229 offset:20480\n"
"v_fmac_f32_dpp v29, v252, v255 row_newbcast:1 row_mask:0xf bank_mask:0xf\n"
"v_mfma_f32_16x16x32_bf16 v[12:15], v[128:131], a[252:255], v[12:15]\n"
"buffer_load_dwordx4 v240, s[20:23], s40 offen offset:1024 nt lds\n"
"v_fmac_f32_dpp v28, v252, v254 row_newbcast:0 row_mask:0xf bank_mask:0xf\n"
"v_accvgpr_read_b32 v254, a224\n"
"v_accvgpr_read_b32 v255, a225\n"
"v_fmac_f32_dpp v58, v252, v253 row_newbcast:2 row_mask:0xf bank_mask:0xf\n"
"v_exp_f32 v154, v154\n"
"v_exp_f32 v167, v167\n"
"v_mfma_f32_16x16x32_bf16 a[220:223], v[124:127], a[252:255], 0\n"
"v_cvt_pk_bf16_f32 v24, v24, v25\n"
"v_mul_f32_dpp v61, v250, v61 row_newbcast:1 row_mask:0xf bank_mask:0xf\n"
"v_smfmac_f32_16x16x64_bf16 v[196:199], a[240:243], v[128:135], v241\n"
"v_cvt_pk_bf16_f32 v25, v26, v27\n"
"v_mul_f32_dpp v50, v247, v50 row_newbcast:2 row_mask:0xf bank_mask:0xf\n"
"v_mfma_f32_16x16x32_bf16 v[202:205], a[240:243], v[124:127], v[202:205]\n"
"s_waitcnt lgkmcnt(1)\n"
"v_accvgpr_read_b32 v253, a230\n"
"v_cvt_pk_bf16_f32 v97, v154, v155\n"
"v_mfma_f32_16x16x32_bf16 v[168:171], v[216:219], a[8:11], v[180:183]\n"
"ds_read_b64_tr_b16 a[248:249], v238 offset:1024\n"
"v_cvt_pk_bf16_f32 v26, v28, v29\n"
"v_mfma_f32_16x16x32_bf16 v[168:171], v[220:223], a[12:15], v[168:171]\n"
"v_accvgpr_read_b32 v251, a215\n"
"v_fmac_f32_dpp v57, v252, v255 row_newbcast:1 row_mask:0xf bank_mask:0xf\n"
"v_mfma_f32_16x16x32_bf16 v[168:171], v[208:211], a[0:3], v[168:171]\n"
"s_waitcnt lgkmcnt(1)\n"
"v_fmac_f32_dpp v31, v252, v251 row_newbcast:3 row_mask:0xf bank_mask:0xf\n"
"v_accvgpr_read_b32 v251, a227\n"
"v_mfma_f32_16x16x32_bf16 v[168:171], v[212:215], a[4:7], v[168:171]\n"
"ds_read_b64_tr_b16 a[250:251], v238 offset:5120\n"
"v_accvgpr_read_b32 v255, a229\n"
"v_mfma_f32_16x16x32_bf16 v[172:175], v[208:211], a[16:19], v[184:187]\n"
"ds_read_b64_tr_b16 a[244:245], v234 offset:1024\n"
"v_mul_f32_dpp v63, v250, v63 row_newbcast:3 row_mask:0xf bank_mask:0xf\n"
"v_mfma_f32_16x16x32_bf16 v[172:175], v[212:215], a[20:23], v[172:175]\n"
"v_mul_f32_dpp v55, v247, v55 row_newbcast:3 row_mask:0xf bank_mask:0xf\n"
"v_fmac_f32_dpp v59, v252, v251 row_newbcast:3 row_mask:0xf bank_mask:0xf\n"
"v_mfma_f32_16x16x32_bf16 v[172:175], v[216:219], a[24:27], v[172:175]\n"
"s_add_u32 s40, s56, s53\n"
"ds_read_b64_tr_b16 a[246:247], v234 offset:5120\n"
"ds_read_b64_tr_b16 v[224:225], v239 offset:1024\n"
"v_mfma_f32_16x16x32_bf16 v[172:175], v[220:223], a[28:31], v[172:175]\n"
"buffer_load_dwordx4 v240, s[20:23], s40 offen offset:2048 nt lds\n"
"v_cvt_pk_bf16_f32 v27, v30, v31\n"
"v_fmac_f32_dpp v56, v252, v254 row_newbcast:0 row_mask:0xf bank_mask:0xf\n"
"v_accvgpr_read_b32 v251, a231\n"
"v_fmac_f32_dpp v61, v252, v255 row_newbcast:1 row_mask:0xf bank_mask:0xf\n"
"v_cvt_pk_bf16_f32 v56, v56, v57\n"
"v_cvt_pk_bf16_f32 v113, v162, v163\n"
"v_exp_f32 v168, v168\n"
"v_mfma_f32_16x16x32_bf16 v[176:179], v[216:219], a[40:43], v[188:191]\n"
"v_mul_f32_dpp v62, v250, v62 row_newbcast:2 row_mask:0xf bank_mask:0xf\n"
"v_cvt_pk_bf16_f32 v57, v58, v59\n"
"v_mfma_f32_16x16x32_bf16 v[176:179], v[220:223], a[44:47], v[176:179]\n"
"v_fmac_f32_dpp v63, v252, v251 row_newbcast:3 row_mask:0xf bank_mask:0xf\n"
"v_cvt_pk_bf16_f32 v96, v152, v153\n"
"v_mfma_f32_16x16x32_bf16 v[176:179], v[208:211], a[32:35], v[176:179]\n"
"ds_read_b64_tr_b16 a[254:255], v235 offset:5120\n"
"v_accvgpr_read_b32 v254, a228\n"
"v_mfma_f32_16x16x32_bf16 v[176:179], v[212:215], a[36:39], v[176:179]\n"
"ds_write_b128 v243, v[24:27] offset:0\n"
"v_fmac_f32_dpp v60, v252, v254 row_newbcast:0 row_mask:0xf bank_mask:0xf\n"
"v_fmac_f32_dpp v62, v252, v253 row_newbcast:2 row_mask:0xf bank_mask:0xf\n"
"v_cvt_pk_bf16_f32 v58, v60, v61\n"
"v_cvt_pk_bf16_f32 v59, v62, v63\n"
"v_cvt_pk_bf16_f32 v112, v160, v161\n"
"v_mfma_f32_16x16x32_bf16 v[152:155], v[208:211], a[192:195], v[192:195]\n"
"ds_write_b128 v243, v[56:59] offset:2048\n"
"v_mul_f32_dpp v23, v246, v23 row_newbcast:3 row_mask:0xf bank_mask:0xf\n"
"v_exp_f32 v158, v158\n"
"v_exp_f32 v165, v165\n"
"v_mfma_f32_16x16x32_bf16 v[152:155], v[212:215], a[196:199], v[152:155]\n"
"v_exp_f32 v166, v166\n"
"v_mfma_f32_16x16x32_bf16 v[152:155], v[216:219], a[200:203], v[152:155]\n"
"ds_read_b64_tr_b16 v[226:227], v239 offset:5120\n"
"v_cvt_pk_bf16_f32 v104, v156, v157\n"
"v_mfma_f32_16x16x32_bf16 v[152:155], v[220:223], a[204:207], v[152:155]\n"
"s_waitcnt lgkmcnt(4)\n"
"ds_read_b64_tr_b16 a[252:253], v235 offset:1024\n"
"v_mul_f32_dpp v80, v206, v80 row_newbcast:0 row_mask:0xf bank_mask:0xf\n"
"v_mfma_f32_16x16x32_bf16 a[232:235], v[124:127], a[244:247], 0\n"
"ds_read_b128 v[220:223], v231 offset:17408\n"
"v_cvt_pk_bf16_f32 v80, v80, v81\n"
"v_mfma_f32_16x16x32_bf16 v[24:27], v[128:131], a[244:247], 0\n"
"s_add_u32 s40, s56, s54\n"
"v_exp_f32 v174, v174\n"
"v_mfma_f32_16x16x32_bf16 v[24:27], v[132:135], a[248:251], v[24:27]\n"
"buffer_load_dwordx4 v240, s[20:23], s40 offen offset:3072 nt lds\n"
"v_mfma_f32_16x16x32_bf16 v[56:59], v[136:139], a[244:247], 0\n"
"s_add_u32 s55, s55, s59\n"
"v_exp_f32 v172, v172\n"
"v_mfma_f32_16x16x32_bf16 v[56:59], v[140:143], a[248:251], v[56:59]\n"
"s_add_u32 s41, s55, s57\n"
"v_mul_f32_dpp v21, v246, v21 row_newbcast:1 row_mask:0xf bank_mask:0xf\n"
"v_mul_f32_dpp v52, v247, v52 row_newbcast:0 row_mask:0xf bank_mask:0xf\n"
"v_cvt_pk_bf16_f32 v49, v50, v51\n"
"v_exp_f32 v176, v176\n"
"v_mfma_f32_16x16x32_bf16 v[88:91], v[144:147], a[244:247], 0\n"
"s_and_b32 s41, s41, s58\n"
"v_exp_f32 v169, v169\n"
"v_mfma_f32_16x16x32_bf16 v[88:91], v[148:151], a[248:251], v[88:91]\n"
"s_waitcnt lgkmcnt(2)\n"
"s_andn2_b32 s56, s55, s58\n"
"ds_read_b128 v[208:211], v230 offset:16384\n"
"v_mul_f32_dpp v20, v246, v20 row_newbcast:0 row_mask:0xf bank_mask:0xf\n"
"v_mfma_f32_16x16x32_bf16 v[92:95], v[148:151], v[224:227], 0\n"
"s_waitcnt lgkmcnt(2)\n"
"s_or_b32 s56, s56, s41\n"
"ds_read_b128 v[212:215], v231 offset:16384\n"
"v_cvt_pk_bf16_f32 v98, v168, v169\n"
"v_mfma_f32_16x16x32_bf16 v[92:95], v[144:147], a[252:255], v[92:95]\n"
"ds_read_b128 v[216:219], v230 offset:17408\n"
"v_cvt_pk_bf16_f32 v105, v158, v159\n"
"v_mfma_f32_16x16x32_bf16 v[60:63], v[140:143], v[224:227], 0\n"
"s_add_u32 m0, s44, 49152\n"
"v_exp_f32 v173, v173\n"
"v_mfma_f32_16x16x32_bf16 v[60:63], v[136:139], a[252:255], v[60:63]\n"
"buffer_load_dwordx4 v240, s[16:19], s56 offen nt lds\n"
"v_mfma_f32_16x16x32_bf16 v[28:31], v[132:135], v[224:227], 0\n"
"v_exp_f32 v152, v152\n"
"v_mfma_f32_16x16x32_bf16 v[28:31], v[128:131], a[252:255], v[28:31]\n"
"v_mul_f32_dpp v22, v246, v22 row_newbcast:2 row_mask:0xf bank_mask:0xf\n"
"v_cvt_pk_bf16_f32 v50, v52, v53\n"
"v_mfma_f32_16x16x32_bf16 a[236:239], v[124:127], a[252:255], 0\n"
"ds_read_b64_tr_b16 a[246:247], v232 offset:4096\n"
"v_cvt_pk_bf16_f32 v17, v18, v19\n"
"v_smfmac_f32_16x16x64_bf16 v[198:201], a[240:243], v[136:143], v241\n"
"s_waitcnt lgkmcnt(3)\n"
"ds_read_b64_tr_b16 a[254:255], v233 offset:4096\n"
"v_cvt_pk_bf16_f32 v106, v172, v173\n"
"v_mfma_f32_16x16x32_bf16 v[156:159], v[208:211], a[0:3], v[180:183]\n"
"s_waitcnt lgkmcnt(3)\n"
"ds_read_b64_tr_b16 v[224:225], v237 offset:0\n"
"v_cvt_pk_bf16_f32 v51, v54, v55\n"
"v_mfma_f32_16x16x32_bf16 v[156:159], v[212:215], a[4:7], v[156:159]\n"
"s_waitcnt lgkmcnt(3)\n"
"ds_read_b64_tr_b16 a[244:245], v232 offset:0\n"
"v_cvt_pk_bf16_f32 v120, v164, v165\n"
"v_mfma_f32_16x16x32_bf16 v[156:159], v[216:219], a[8:11], v[156:159]\n"
"v_exp_f32 v177, v177\n"
"v_mfma_f32_16x16x32_bf16 v[156:159], v[220:223], a[12:15], v[156:159]\n"
"ds_read_b64_tr_b16 a[248:249], v236 offset:0\n"
"v_mul_f32_dpp v82, v206, v82 row_newbcast:2 row_mask:0xf bank_mask:0xf\n"
"v_mfma_f32_16x16x32_bf16 v[160:163], v[216:219], a[24:27], v[184:187]\n"
"ds_read_b64_tr_b16 a[250:251], v236 offset:4096\n"
"v_add_u32 v243, 0x1000, v243\n"
"v_mfma_f32_16x16x32_bf16 v[160:163], v[220:223], a[28:31], v[160:163]\n"
"ds_read_b64_tr_b16 v[226:227], v237 offset:4096\n"
"v_cvt_pk_bf16_f32 v121, v166, v167\n"
"v_mfma_f32_16x16x32_bf16 v[160:163], v[208:211], a[16:19], v[160:163]\n"
"s_add_u32 s40, s56, s52\n"
"ds_read_b64_tr_b16 a[252:253], v233 offset:0\n"
"v_cvt_pk_bf16_f32 v81, v82, v83\n"
"v_mfma_f32_16x16x32_bf16 v[160:163], v[212:215], a[20:23], v[160:163]\n"
"buffer_load_dwordx4 v240, s[16:19], s40 offen offset:1024 nt lds\n"
"v_mfma_f32_16x16x32_bf16 v[164:167], v[208:211], a[32:35], v[188:191]\n"
"v_exp_f32 v179, v179\n"
"v_mfma_f32_16x16x32_bf16 v[164:167], v[212:215], a[36:39], v[164:167]\n"
"v_exp_f32 v154, v154\n"
"v_mfma_f32_16x16x32_bf16 v[164:167], v[216:219], a[40:43], v[164:167]\n"
"ds_read_b128 v[212:215], v231 offset:20480\n"
"v_cvt_pk_bf16_f32 v18, v20, v21\n"
"v_mfma_f32_16x16x32_bf16 v[164:167], v[220:223], a[44:47], v[164:167]\n"
"s_waitcnt lgkmcnt(4)\n"
"v_exp_f32 v175, v175\n"
"v_mfma_f32_16x16x32_bf16 a[208:211], v[124:127], a[244:247], 0\n"
"ds_read_b128 v[208:211], v230 offset:20480\n"
"v_cvt_pk_bf16_f32 v83, v86, v87\n"
"v_mfma_f32_16x16x32_bf16 v[0:3], v[128:131], a[244:247], 0\n"
"s_waitcnt lgkmcnt(3)\n"
"v_exp_f32 v156, v156\n"
"v_mfma_f32_16x16x32_bf16 v[0:3], v[132:135], a[248:251], v[0:3]\n"
"ds_write_b128 v248, v[48:51] offset:50176\n"
"v_exp_f32 v178, v178\n"
"v_exp_f32 v157, v157\n"
"v_mfma_f32_16x16x32_bf16 v[32:35], v[136:139], a[244:247], 0\n"
"s_add_u32 s40, s56, s53\n"
"v_exp_f32 v171, v171\n"
"v_mfma_f32_16x16x32_bf16 v[32:35], v[140:143], a[248:251], v[32:35]\n"
"buffer_load_dwordx4 v240, s[16:19], s40 offen offset:2048 nt lds\n"
"v_mfma_f32_16x16x32_bf16 v[64:67], v[144:147], a[244:247], 0\n"
"ds_read_b128 v[216:219], v230 offset:21504\n"
"v_cvt_pk_bf16_f32 v19, v22, v23\n"
"v_mfma_f32_16x16x32_bf16 v[64:67], v[148:151], a[248:251], v[64:67]\n"
"v_exp_f32 v160, v160\n"
"v_mfma_f32_16x16x32_bf16 v[68:71], v[148:151], v[224:227], 0\n"
"s_waitcnt lgkmcnt(3)\n"
"ds_read_b64_tr_b16 a[72:73], v249 offset:50176\n"
"ds_read_b128 v[220:223], v231 offset:21504\n"
"v_mfma_f32_16x16x32_bf16 v[68:71], v[144:147], a[252:255], v[68:71]\n"
"ds_write_b128 v248, v[16:19] offset:49152\n"
"v_cvt_pk_bf16_f32 v82, v84, v85\n"
"v_exp_f32 v158, v158\n"
"v_mfma_f32_16x16x32_bf16 v[36:39], v[140:143], v[224:227], 0\n"
"v_exp_f32 v170, v170\n"
"v_mfma_f32_16x16x32_bf16 v[36:39], v[136:139], a[252:255], v[36:39]\n"
"ds_read_b64_tr_b16 a[244:245], v232 offset:1024\n"
"v_cvt_pk_bf16_f32 v114, v176, v177\n"
"v_mfma_f32_16x16x32_bf16 v[4:7], v[132:135], v[224:227], 0\n"
"ds_read_b64_tr_b16 a[58:59], v249 offset:49160\n"
"ds_read_b64_tr_b16 v[224:225], v237 offset:1024\n"
"v_mfma_f32_16x16x32_bf16 v[4:7], v[128:131], a[252:255], v[4:7]\n"
"ds_read_b64_tr_b16 a[74:75], v249 offset:50184\n"
"v_cvt_pk_bf16_f32 v99, v170, v171\n"
"v_mfma_f32_16x16x32_bf16 a[212:215], v[124:127], a[252:255], 0\n"
"s_add_u32 s40, s56, s54\n"
"ds_write_b128 v248, v[80:83] offset:51200\n"
"v_exp_f32 v163, v163\n"
"v_exp_f32 v167, v167\n"
"v_smfmac_f32_16x16x64_bf16 v[200:203], a[240:243], v[144:151], v241\n"
"s_waitcnt lgkmcnt(8)\n"
"buffer_load_dwordx4 v240, s[16:19], s40 offen offset:3072 nt lds\n"
"v_mfma_f32_16x16x32_bf16 v[168:171], v[208:211], a[0:3], v[180:183]\n"
"v_exp_f32 v159, v159\n"
"v_mfma_f32_16x16x32_bf16 v[168:171], v[212:215], a[4:7], v[168:171]\n"
"ds_read_b64_tr_b16 a[88:89], v249 offset:51200\n"
"ds_read_b64_tr_b16 a[246:247], v232 offset:5120\n"
"v_mfma_f32_16x16x32_bf16 v[168:171], v[216:219], a[8:11], v[168:171]\n"
"s_waitcnt lgkmcnt(7)\n"
"ds_read_b64_tr_b16 a[252:253], v233 offset:1024\n"
"v_cvt_pk_bf16_f32 v107, v174, v175\n"
"v_mfma_f32_16x16x32_bf16 v[168:171], v[220:223], a[12:15], v[168:171]\n"
"ds_read_b64_tr_b16 a[56:57], v249 offset:49152\n"
"ds_read_b64_tr_b16 a[90:91], v249 offset:51208\n"
"v_mfma_f32_16x16x32_bf16 v[172:175], v[216:219], a[24:27], v[184:187]\n"
"v_exp_f32 v162, v162\n"
"v_mfma_f32_16x16x32_bf16 v[172:175], v[220:223], a[28:31], v[172:175]\n"
"ds_read_b64_tr_b16 a[248:249], v236 offset:1024\n"
"v_xor_b32 v248, s75, v248\n"
"v_mfma_f32_16x16x32_bf16 v[172:175], v[208:211], a[16:19], v[172:175]\n"
"ds_read_b64_tr_b16 a[250:251], v236 offset:5120\n"
"v_cvt_pk_bf16_f32 v115, v178, v179\n"
"v_mfma_f32_16x16x32_bf16 v[172:175], v[212:215], a[20:23], v[172:175]\n"
"s_cbranch_vccnz LPCT1_%=\nLPKT1_%=:\n"
"v_cvt_pk_bf16_f32 v109, v162, v163\n"
"v_mfma_f32_16x16x32_bf16 v[176:179], v[208:211], a[32:35], v[188:191]\n"
"v_exp_f32 v161, v161\n"
"v_mfma_f32_16x16x32_bf16 v[176:179], v[212:215], a[36:39], v[176:179]\n"
"v_exp_f32 v166, v166\n"
"v_mfma_f32_16x16x32_bf16 v[176:179], v[216:219], a[40:43], v[176:179]\n"
"ds_read_b64_tr_b16 a[254:255], v233 offset:5120\n"
"v_cvt_pk_bf16_f32 v100, v156, v157\n"
"v_mfma_f32_16x16x32_bf16 v[176:179], v[220:223], a[44:47], v[176:179]\n"
"s_waitcnt lgkmcnt(3)\n"
"ds_read_b64_tr_b16 v[226:227], v237 offset:5120\n"
"v_mfma_f32_16x16x32_bf16 a[224:227], v[124:127], a[244:247], 0\n"
"v_exp_f32 v173, v173\n"
"v_mfma_f32_16x16x32_bf16 v[16:19], v[128:131], a[244:247], 0\n"
"s_waitcnt vmcnt(8) lgkmcnt(2)\n"
"v_exp_f32 v169, v169\n"
"v_mfma_f32_16x16x32_bf16 v[16:19], v[132:135], a[248:251], v[16:19]\n"
"s_barrier\n"
"ds_read_b128 v[208:211], v228 offset:32768\n"
"ds_read_b128 v[212:215], v229 offset:32768\n"
"v_exp_f32 v165, v165\n"
"v_mfma_f32_16x16x32_bf16 v[48:51], v[136:139], a[244:247], 0\n"
"ds_read_b128 v[216:219], v228 offset:33792\n"
"v_cvt_pk_bf16_f32 v101, v158, v159\n"
"v_mfma_f32_16x16x32_bf16 v[48:51], v[140:143], a[248:251], v[48:51]\n"
"ds_read_b128 v[220:223], v229 offset:33792\n"
"v_cvt_pk_bf16_f32 v117, v166, v167\n"
"v_mfma_f32_16x16x32_bf16 v[80:83], v[144:147], a[244:247], 0\n"
"ds_read_b64_tr_b16 a[244:245], v234 offset:16384\n"
"v_xor_b32 v249, s75, v249\n"
"v_mfma_f32_16x16x32_bf16 v[80:83], v[148:151], a[248:251], v[80:83]\n"
"s_waitcnt lgkmcnt(5)\n"
"ds_read_b64_tr_b16 a[246:247], v234 offset:20480\n"
"ds_read_b64_tr_b16 a[248:249], v238 offset:16384\n"
"ds_read_b64_tr_b16 a[250:251], v238 offset:20480\n"
"v_mfma_f32_16x16x32_bf16 v[84:87], v[148:151], v[224:227], 0\n"
"v_exp_f32 v175, v175\n"
"v_mfma_f32_16x16x32_bf16 v[84:87], v[144:147], a[252:255], v[84:87]\n"
"v_exp_f32 v153, v153\n"
"v_mfma_f32_16x16x32_bf16 v[52:55], v[140:143], v[224:227], 0\n"
"v_exp_f32 v164, v164\n"
"v_mfma_f32_16x16x32_bf16 v[52:55], v[136:139], a[252:255], v[52:55]\n"
"v_exp_f32 v155, v155\n"
"v_mfma_f32_16x16x32_bf16 v[20:23], v[132:135], v[224:227], 0\n"
"ds_read_b64_tr_b16 v[226:227], v239 offset:20480\n"
"v_cvt_pk_bf16_f32 v123, v154, v155\n"
"v_mfma_f32_16x16x32_bf16 v[20:23], v[128:131], a[252:255], v[20:23]\n"
"v_exp_f32 v178, v178\n"
"v_mfma_f32_16x16x32_bf16 a[228:231], v[124:127], a[252:255], 0\n"
"s_waitcnt lgkmcnt(4)\n"
"v_cvt_pk_bf16_f32 v122, v152, v153\n"
"s_branch LAY2_%=\n"
"LAX_%=:\n"
"v_mfma_f32_16x16x32_bf16 a[224:227], v[124:127], a[244:247], a[224:227]\n"
"v_exp_f32 v168, v168\n"
"v_mfma_f32_16x16x32_bf16 v[16:19], v[128:131], a[244:247], v[16:19]\n"
"s_waitcnt vmcnt(8) lgkmcnt(0)\n"
"v_exp_f32 v172, v172\n"
"v_mfma_f32_16x16x32_bf16 v[16:19], v[132:135], a[248:251], v[16:19]\n"
"s_barrier\n"
"v_mfma_f32_16x16x32_bf16 v[48:51], v[136:139], a[244:247], v[48:51]\n"
"v_exp_f32 v170, v170\n"
"v_mfma_f32_16x16x32_bf16 v[48:51], v[140:143], a[248:251], v[48:51]\n"
"v_exp_f32 v156, v156\n"
"v_mfma_f32_16x16x32_bf16 v[80:83], v[144:147], a[244:247], v[80:83]\n"
"v_exp_f32 v177, v177\n"
"v_mfma_f32_16x16x32_bf16 v[80:83], v[148:151], a[248:251], v[80:83]\n"
"ds_read_b64_tr_b16 a[244:245], v234 offset:49152\n"
"ds_read_b64_tr_b16 a[246:247], v234 offset:53248\n"
"v_cvt_pk_bf16_f32 v123, v154, v155\n"
"v_cvt_pk_bf16_f32 v108, v160, v161\n"
"v_exp_f32 v174, v174\n"
"v_exp_f32 v176, v176\n"
"v_exp_f32 v179, v179\n"
"v_accvgpr_read_b32 v253, a96\n"
"v_mfma_f32_16x16x32_bf16 v[84:87], v[148:151], v[224:227], v[84:87]\n"
"v_lshlrev_b32_e64 v244, 24, s43\n"
"v_and_b32 v245, 0x3f0, v242\n"
"v_mfma_f32_16x16x32_bf16 v[84:87], v[144:147], a[252:255], v[84:87]\n"
"ds_read_b64_tr_b16 a[248:249], v238 offset:49152\n"
"v_cvt_pk_bf16_f32 v101, v158, v159\n"
"v_cvt_pk_bf16_f32 v109, v162, v163\n"
"v_exp_f32 v169, v169\n"
"v_exp_f32 v171, v171\n"
"v_exp_f32 v173, v173\n"
"v_and_b32 v252, 0x7fff0000, v252\n"
"v_and_b32 v253, 0x7fff0000, v253\n"
"v_mfma_f32_16x16x32_bf16 v[52:55], v[140:143], v[224:227], v[52:55]\n"
"ds_read_b64_tr_b16 a[250:251], v238 offset:53248\n"
"v_cvt_pk_bf16_f32 v110, v172, v173\n"
"v_mfma_f32_16x16x32_bf16 v[52:55], v[136:139], a[252:255], v[52:55]\n"
"v_exp_f32 v178, v178\n"
"v_mfma_f32_16x16x32_bf16 v[20:23], v[132:135], v[224:227], v[20:23]\n"
"v_cvt_pk_bf16_f32 v100, v156, v157\n"
"v_cvt_pk_bf16_f32 v103, v170, v171\n"
"v_mfma_f32_16x16x32_bf16 v[20:23], v[128:131], a[252:255], v[20:23]\n"
"v_cvt_pk_bf16_f32 v119, v178, v179\n"
"v_max_u32_dpp v252, v252, v252 quad_perm:[1,0,3,2] row_mask:0xf bank_mask:0xf\n"
"v_mfma_f32_16x16x32_bf16 a[228:231], v[124:127], a[252:255], a[228:231]\n"
"s_waitcnt lgkmcnt(2)\n"
"ds_read_b64_tr_b16 v[226:227], v239 offset:53248\n"
"v_cvt_pk_bf16_f32 v102, v168, v169\n"
"v_mfma_f32_16x16x32_bf16 a[216:219], v[120:123], a[244:247], a[216:219]\n"
"s_mov_b64 s[28:29], s[8:9]\n"
"v_cvt_pk_bf16_f32 v116, v164, v165\n"
"v_cvt_pk_bf16_f32 v117, v166, v167\n"
"v_smfmac_f32_16x16x64_bf16 v[196:199], a[240:243], v[96:103], v241\n"
"s_mov_b32 s30, 655360\n"
"ds_read_b64_tr_b16 v[224:225], v239 offset:49152\n"
"v_cvt_pk_bf16_f32 v118, v176, v177\n"
"v_mfma_f32_16x16x32_bf16 v[8:11], v[96:99], a[244:247], v[8:11]\n"
"s_waitcnt lgkmcnt(2)\n"
"s_mov_b32 s71, s44\n"
"v_exp_f32 v175, v175\n"
"v_mfma_f32_16x16x32_bf16 v[8:11], v[100:103], a[248:251], v[8:11]\n"
"s_mov_b32 s70, s95\n"
"ds_read_b64_tr_b16 a[252:253], v235 offset:49152\n"
"ds_read_b64_tr_b16 a[254:255], v235 offset:53248\n"
"v_smfmac_f32_16x16x64_bf16 v[200:203], a[240:243], v[112:119], v241\n"
"v_cvt_pk_bf16_f32 v111, v174, v175\n"
"v_max_u32_dpp v253, v253, v253 quad_perm:[1,0,3,2] row_mask:0xf bank_mask:0xf\n"
"v_mfma_f32_16x16x32_bf16 v[40:43], v[104:107], a[244:247], v[40:43]\n"
"s_nop 0\n"
"v_max_u32_dpp v253, v253, v253 quad_perm:[2,3,0,1] row_mask:0xf bank_mask:0xf\n"
"v_add_f32 v196, v196, v197\n"
"v_mfma_f32_16x16x32_bf16 v[40:43], v[108:111], a[248:251], v[40:43]\n"
"s_add_u32 s41, s70, 327680\n"
"s_add_u32 m0, s71, 32768\n"
"v_max_u32_dpp v252, v252, v252 quad_perm:[2,3,0,1] row_mask:0xf bank_mask:0xf\n"
"v_smfmac_f32_16x16x64_bf16 v[198:201], a[240:243], v[104:111], v241\n"
"buffer_load_dwordx4 v240, s[28:31], s41 offen lds\n"
"v_mfma_f32_16x16x32_bf16 v[72:75], v[112:115], a[244:247], v[72:75]\n"
"v_max_u32_dpp v253, v253, v253 row_half_mirror row_mask:0xf bank_mask:0xf\n"
"v_mul_f32 v244, 0x45800000, v244\n"
"v_mfma_f32_16x16x32_bf16 v[72:75], v[116:119], a[248:251], v[72:75]\n"
"ds_read_b64_tr_b16 a[246:247], v234 offset:54272\n"
"v_max_u32_dpp v252, v252, v252 row_half_mirror row_mask:0xf bank_mask:0xf\n"
"v_mfma_f32_16x16x32_bf16 v[202:205], a[240:243], v[120:123], v[202:205]\n"
"s_waitcnt lgkmcnt(3)\n"
"ds_read_b64_tr_b16 a[250:251], v238 offset:54272\n"
"v_add_f32 v200, v200, v201\n"
"v_mfma_f32_16x16x32_bf16 v[76:79], v[116:119], v[224:227], v[76:79]\n"
"s_waitcnt lgkmcnt(2)\n"
"s_add_u32 s40, s41, s52\n"
"ds_read_b64_tr_b16 a[244:245], v234 offset:50176\n"
"ds_read_b64_tr_b16 a[248:249], v238 offset:50176\n"
"v_mfma_f32_16x16x32_bf16 v[76:79], v[112:115], a[252:255], v[76:79]\n"
"buffer_load_dwordx4 v240, s[28:31], s40 offen offset:1024 lds\n"
"v_mfma_f32_16x16x32_bf16 v[44:47], v[108:111], v[224:227], v[44:47]\n"
"v_add_f32 v198, v198, v199\n"
"v_add_f32 v244, v244, v202\n"
"v_mfma_f32_16x16x32_bf16 v[44:47], v[104:107], a[252:255], v[44:47]\n"
"v_min_u32_dpp v253, v253, v253 row_mirror row_mask:0xf bank_mask:0xf\n"
"v_min3_f32 v244, v244, v196, v198\n"
"v_mfma_f32_16x16x32_bf16 v[12:15], v[100:103], v[224:227], v[12:15]\n"
"s_add_u32 s40, s41, s53\n"
"ds_read_b64_tr_b16 v[224:225], v239 offset:50176\n"
"v_add_f32 v254, v196, v198\n"
"v_mfma_f32_16x16x32_bf16 v[12:15], v[96:99], a[252:255], v[12:15]\n"
"buffer_load_dwordx4 v240, s[28:31], s40 offen offset:2048 lds\n"
"v_min_u32_dpp v252, v252, v252 row_mirror row_mask:0xf bank_mask:0xf\n"
"v_add_f32 v254, v254, v200\n"
"v_min_f32 v244, v244, v200\n"
"v_rcp_f32 v244, v244\n"
"v_lshrrev_b32 v245, 1, v245\n"
"v_mfma_f32_16x16x32_bf16 a[220:223], v[120:123], a[252:255], a[220:223]\n"
"s_waitcnt lgkmcnt(2)\n"
"v_add_f32 v254, v254, v202\n"
"v_fmac_f32 v254, 0x68800000, v244\n"
"v_cmp_nge_f32 vcc, s45, v254\n"
"v_sub_u32 v245, v242, v245\n"
"s_cbranch_vccnz LSLOW_%=\nLBACK3_%=:\ns_mov_b64 vcc, s[64:65]\n"
"v_mfma_f32_16x16x32_bf16 a[232:235], v[120:123], a[244:247], a[232:235]\n"
"ds_read_b64_tr_b16 a[252:253], v235 offset:50176\n"
"ds_read_b64_tr_b16 a[254:255], v235 offset:54272\n"
"ds_read_b64_tr_b16 v[226:227], v239 offset:54272\n"
"v_rcp_f32 v246, v196\n"
"v_rcp_f32 v247, v198\n"
"v_xor_b32 v244, 0x11000, v242\n"
"v_bfe_u32 v254, v242, 9, 1\n"
"v_mfma_f32_16x16x32_bf16 v[24:27], v[96:99], a[244:247], v[24:27]\n"
"s_waitcnt lgkmcnt(4)\n"
"v_min_u32_dpp v252, v252, v252 row_bcast:15 row_mask:0xa bank_mask:0xf\n"
"v_mov_b32_dpp v246, v246 row_ror:12 row_mask:0x2 bank_mask:0xf\n"
"v_mfma_f32_16x16x32_bf16 v[24:27], v[100:103], a[248:251], v[24:27]\n"
"ds_write_b128 v244, a[220:223] offset:33792\n"
"v_min_u32_dpp v253, v253, v253 row_bcast:15 row_mask:0xa bank_mask:0xf\n"
"v_min_u32_dpp v252, v252, v252 row_bcast:31 row_mask:0xc bank_mask:0xf\n"
"v_mul_f32 v252, 0x43340000, v252\n"
"v_mov_b32_dpp v247, v247 row_ror:12 row_mask:0x2 bank_mask:0xf\n"
"v_mov_b32_dpp v246, v246 row_ror:8 row_mask:0x4 bank_mask:0xf\n"
"v_mfma_f32_16x16x32_bf16 v[56:59], v[104:107], a[244:247], v[56:59]\n"
"s_add_u32 s40, s41, s54\n"
"v_rcp_f32 v206, v200\n"
"v_mfma_f32_16x16x32_bf16 v[56:59], v[108:111], a[248:251], v[56:59]\n"
"buffer_load_dwordx4 v240, s[28:31], s40 offen offset:3072 lds\n"
"v_mfma_f32_16x16x32_bf16 v[88:91], v[112:115], a[244:247], v[88:91]\n"
"s_add_u32 s71, s44, s75\n"
"v_mov_b32_dpp v206, v206 row_ror:12 row_mask:0x2 bank_mask:0xf\n"
"v_mov_b32_dpp v246, v246 row_ror:4 row_mask:0x8 bank_mask:0xf\n"
"v_mfma_f32_16x16x32_bf16 v[88:91], v[116:119], a[248:251], v[88:91]\n"
"s_waitcnt lgkmcnt(1)\n"
"s_mov_b32 s70, s100\n"
"ds_read_b64_tr_b16 a[244:245], v232 offset:49152\n"
"ds_read_b64_tr_b16 a[246:247], v232 offset:53248\n"
"ds_read_b64_tr_b16 a[248:249], v236 offset:49152\n"
"v_mov_b32_dpp v247, v247 row_ror:8 row_mask:0x4 bank_mask:0xf\n"
"v_xor_b32 v250, 0x1000, v245\n"
"v_mul_f32_dpp v10, v246, v10 row_newbcast:2 row_mask:0xf bank_mask:0xf\n"
"v_mul_f32_dpp v12, v246, v12 row_newbcast:0 row_mask:0xf bank_mask:0xf\n"
"v_mul_f32_dpp v15, v246, v15 row_newbcast:3 row_mask:0xf bank_mask:0xf\n"
"v_mfma_f32_16x16x32_bf16 v[92:95], v[116:119], v[224:227], v[92:95]\n"
"s_add_u32 s41, s70, 163840\n"
"s_add_u32 m0, s71, 16384\n"
"ds_read_b64_tr_b16 a[250:251], v236 offset:53248\n"
"v_mfma_f32_16x16x32_bf16 v[92:95], v[112:115], a[252:255], v[92:95]\n"
"buffer_load_dwordx4 v240, s[28:31], s41 offen lds\n"
"v_mov_b32_dpp v206, v206 row_ror:8 row_mask:0x4 bank_mask:0xf\n"
"v_mov_b32_dpp v247, v247 row_ror:4 row_mask:0x8 bank_mask:0xf\n"
"v_mul_f32_dpp v11, v246, v11 row_newbcast:3 row_mask:0xf bank_mask:0xf\n"
"v_mul_f32_dpp v24, v246, v24 row_newbcast:0 row_mask:0xf bank_mask:0xf\n"
"v_mul_f32_dpp v25, v246, v25 row_newbcast:1 row_mask:0xf bank_mask:0xf\n"
"v_cvt_pk_bf16_f32 v24, v24, v25\n"
"v_mfma_f32_16x16x32_bf16 v[60:63], v[108:111], v[224:227], v[60:63]\n"
"v_min_u32_dpp v253, v253, v253 row_bcast:31 row_mask:0xc bank_mask:0xf\n"
"v_mul_f32_dpp v44, v247, v44 row_newbcast:0 row_mask:0xf bank_mask:0xf\n"
"v_mfma_f32_16x16x32_bf16 v[60:63], v[104:107], a[252:255], v[60:63]\n"
"v_mov_b32_dpp v206, v206 row_ror:4 row_mask:0x8 bank_mask:0xf\n"
"v_mul_f32_dpp v41, v247, v41 row_newbcast:1 row_mask:0xf bank_mask:0xf\n"
"v_mfma_f32_16x16x32_bf16 v[28:31], v[100:103], v[224:227], v[28:31]\n"
"s_add_u32 s40, s41, s52\n"
"ds_read_b64_tr_b16 v[224:225], v237 offset:49152\n"
"ds_read_b64_tr_b16 v[226:227], v237 offset:53248\n"
"v_mfma_f32_16x16x32_bf16 v[28:31], v[96:99], a[252:255], v[28:31]\n"
"buffer_load_dwordx4 v240, s[28:31], s40 offen offset:1024 lds\n"
"v_mul_f32_dpp v8, v246, v8 row_newbcast:0 row_mask:0xf bank_mask:0xf\n"
"v_mul_f32_dpp v13, v246, v13 row_newbcast:1 row_mask:0xf bank_mask:0xf\n"
"v_mul_f32_dpp v40, v247, v40 row_newbcast:0 row_mask:0xf bank_mask:0xf\n"
"v_mul_f32_dpp v26, v246, v26 row_newbcast:2 row_mask:0xf bank_mask:0xf\n"
"v_mul_f32_dpp v27, v246, v27 row_newbcast:3 row_mask:0xf bank_mask:0xf\n"
"v_mul_f32_dpp v94, v206, v94 row_newbcast:2 row_mask:0xf bank_mask:0xf\n"
"v_mfma_f32_16x16x32_bf16 a[236:239], v[120:123], a[252:255], a[236:239]\n"
"s_waitcnt lgkmcnt(4)\n"
"v_cvt_pk_bf16_f32 v40, v40, v41\n"
"v_mul_f32_dpp v91, v206, v91 row_newbcast:3 row_mask:0xf bank_mask:0xf\n"
"v_mfma_f32_16x16x32_bf16 a[208:211], v[120:123], a[244:247], a[208:211]\n"
"v_mul_f32_dpp v9, v246, v9 row_newbcast:1 row_mask:0xf bank_mask:0xf\n"
"v_mul_f32_dpp v43, v247, v43 row_newbcast:3 row_mask:0xf bank_mask:0xf\n"
"v_mfma_f32_16x16x32_bf16 v[0:3], v[96:99], a[244:247], v[0:3]\n"
"s_waitcnt lgkmcnt(2)\n"
"ds_write_b128 v244, a[216:219] offset:32768\n"
"v_mul_f32_dpp v42, v247, v42 row_newbcast:2 row_mask:0xf bank_mask:0xf\n"
"v_mul_f32_dpp v72, v206, v72 row_newbcast:0 row_mask:0xf bank_mask:0xf\n"
"v_mul_f32_dpp v73, v206, v73 row_newbcast:1 row_mask:0xf bank_mask:0xf\n"
"v_mul_f32_dpp v31, v246, v31 row_newbcast:3 row_mask:0xf bank_mask:0xf\n"
"v_cvt_pk_bf16_f32 v25, v26, v27\n"
"v_mfma_f32_16x16x32_bf16 v[0:3], v[100:103], a[248:251], v[0:3]\n"
"ds_read_b64_tr_b16 a[252:253], v233 offset:49152\n"
"ds_read_b64_tr_b16 a[254:255], v233 offset:53248\n"
"v_mfma_f32_16x16x32_bf16 v[32:35], v[104:107], a[244:247], v[32:35]\n"
"s_add_u32 s40, s41, s53\n"
"v_lshl_add_u32 v254, v254, 4, s43\n"
"v_mul_f32_dpp v77, v206, v77 row_newbcast:1 row_mask:0xf bank_mask:0xf\n"
"v_mfma_f32_16x16x32_bf16 v[32:35], v[108:111], a[248:251], v[32:35]\n"
"buffer_load_dwordx4 v240, s[28:31], s40 offen offset:2048 lds\n"
"v_mfma_f32_16x16x32_bf16 v[64:67], v[112:115], a[244:247], v[64:67]\n"
"v_mul_f32_dpp v76, v206, v76 row_newbcast:0 row_mask:0xf bank_mask:0xf\n"
"v_mul_f32_dpp v88, v206, v88 row_newbcast:0 row_mask:0xf bank_mask:0xf\n"
"v_mfma_f32_16x16x32_bf16 v[64:67], v[116:119], a[248:251], v[64:67]\n"
"s_waitcnt lgkmcnt(3)\n"
"ds_read_b64_tr_b16 a[244:245], v232 offset:50176\n"
"ds_read_b64_tr_b16 a[246:247], v232 offset:54272\n"
"ds_read_b64_tr_b16 a[248:249], v236 offset:50176\n"
"v_cvt_pk_bf16_f32 v8, v8, v9\n"
"v_mul_f32_dpp v46, v247, v46 row_newbcast:2 row_mask:0xf bank_mask:0xf\n"
"v_mul_f32_dpp v78, v206, v78 row_newbcast:2 row_mask:0xf bank_mask:0xf\n"
"v_mul_f32_dpp v57, v247, v57 row_newbcast:1 row_mask:0xf bank_mask:0xf\n"
"v_mul_f32_dpp v60, v247, v60 row_newbcast:0 row_mask:0xf bank_mask:0xf\n"
"v_mul_f32_dpp v89, v206, v89 row_newbcast:1 row_mask:0xf bank_mask:0xf\n"
"v_mfma_f32_16x16x32_bf16 v[68:71], v[116:119], v[224:227], v[68:71]\n"
"s_waitcnt lgkmcnt(3)\n"
"s_add_u32 s40, s41, s54\n"
"ds_write_b128 v244, a[236:239] offset:35840\n"
"v_mul_f32 v252, v252, v253\n"
"v_lshl_add_u32 v254, v254, 6, s75\n"
"v_mul_f32_dpp v56, v247, v56 row_newbcast:0 row_mask:0xf bank_mask:0xf\n"
"v_accvgpr_read_b32 v255, a209\n"
"v_mfma_f32_16x16x32_bf16 v[68:71], v[112:115], a[252:255], v[68:71]\n"
"buffer_load_dwordx4 v240, s[28:31], s40 offen offset:3072 lds\n"
"v_min_f32 v252, 0x71800000, v252\n"
"s_nop 0\n"
"v_readlane_b32 s22, v252, 63\n"
"v_mul_f32_dpp v45, v247, v45 row_newbcast:1 row_mask:0xf bank_mask:0xf\n"
"v_cvt_pk_bf16_f32 v41, v42, v43\n"
"v_mul_f32_dpp v74, v206, v74 row_newbcast:2 row_mask:0xf bank_mask:0xf\n"
"v_mul_f32_dpp v29, v246, v29 row_newbcast:1 row_mask:0xf bank_mask:0xf\n"
"v_mul_f32_dpp v58, v247, v58 row_newbcast:2 row_mask:0xf bank_mask:0xf\n"
"v_mul_f32_dpp v92, v206, v92 row_newbcast:0 row_mask:0xf bank_mask:0xf\n"
"v_mfma_f32_16x16x32_bf16 v[36:39], v[108:111], v[224:227], v[36:39]\n"
"v_readlane_b32 s23, v252, 63\n"
"v_mul_f32_dpp v1, v246, v1 row_newbcast:1 row_mask:0xf bank_mask:0xf\n"
"v_mfma_f32_16x16x32_bf16 v[36:39], v[104:107], a[252:255], v[36:39]\n"
"ds_write_b128 v244, a[232:235] offset:34816\n"
"v_mov_b32 v252, v192\n"
"v_mov_b32 v253, v202\n"
"v_sub_u32 v228, v232, v254\n"
"v_mul_f32_dpp v14, v246, v14 row_newbcast:2 row_mask:0xf bank_mask:0xf\n"
"v_cvt_pk_bf16_f32 v9, v10, v11\n"
"v_mfma_f32_16x16x32_bf16 v[4:7], v[100:103], v[224:227], v[4:7]\n"
"ds_write_b64 v250, v[252:253] offset:52224\n"
"v_sub_u32 v230, v234, v254\n"
"v_cvt_pk_bf16_f32 v42, v44, v45\n"
"v_mul_f32_dpp v75, v206, v75 row_newbcast:3 row_mask:0xf bank_mask:0xf\n"
"v_cvt_pk_bf16_f32 v72, v72, v73\n"
"v_mul_f32_dpp v30, v246, v30 row_newbcast:2 row_mask:0xf bank_mask:0xf\n"
"v_mul_f32_dpp v33, v247, v33 row_newbcast:1 row_mask:0xf bank_mask:0xf\n"
"v_mfma_f32_16x16x32_bf16 v[4:7], v[96:99], a[252:255], v[4:7]\n"
"ds_read_b64_tr_b16 a[250:251], v236 offset:54272\n"
"ds_read_b64_tr_b16 v[224:225], v237 offset:50176\n"
"ds_read_b64_tr_b16 v[226:227], v237 offset:54272\n"
"v_mul_f32_dpp v47, v247, v47 row_newbcast:3 row_mask:0xf bank_mask:0xf\n"
"v_mul_f32_dpp v79, v206, v79 row_newbcast:3 row_mask:0xf bank_mask:0xf\n"
"v_cvt_pk_bf16_f32 v27, v30, v31\n"
"v_mul_f32_dpp v63, v247, v63 row_newbcast:3 row_mask:0xf bank_mask:0xf\n"
"v_mul_f32_dpp v90, v206, v90 row_newbcast:2 row_mask:0xf bank_mask:0xf\n"
"v_mul_f32_dpp v93, v206, v93 row_newbcast:1 row_mask:0xf bank_mask:0xf\n"
"v_mov_b64 v[196:197], s[22:23]\n"
"v_mfma_f32_16x16x32_bf16 a[212:215], v[120:123], a[252:255], a[212:215]\n"
"s_waitcnt lgkmcnt(7)\n"
"ds_read_b64_tr_b16 a[252:253], v233 offset:50176\n"
"ds_read_b64_tr_b16 a[254:255], v233 offset:54272\n"
"v_cvt_pk_bf16_f32 v10, v12, v13\n"
"v_cvt_pk_bf16_f32 v43, v46, v47\n"
"v_cvt_pk_bf16_f32 v73, v74, v75\n"
"v_cvt_pk_bf16_f32 v88, v88, v89\n"
"v_cvt_pk_bf16_f32 v89, v90, v91\n"
"v_mul_f32_dpp v35, v247, v35 row_newbcast:3 row_mask:0xf bank_mask:0xf\n"
"v_mul_f32_dpp v67, v206, v67 row_newbcast:3 row_mask:0xf bank_mask:0xf\n"
"v_xor_b32 v244, 0x1000, v244\n"
"v_mfma_f32_16x16x32_bf16 a[224:227], v[120:123], a[244:247], a[224:227]\n"
"ds_write_b128 v248, v[40:43] offset:50176\n"
"v_cvt_pk_bf16_f32 v11, v14, v15\n"
"v_mul_f32_dpp v59, v247, v59 row_newbcast:3 row_mask:0xf bank_mask:0xf\n"
"v_mul_f32_dpp v39, v247, v39 row_newbcast:3 row_mask:0xf bank_mask:0xf\n"
"v_mov_b64 v[198:199], s[22:23]\n"
"v_xor_b32 v232, 0x1000, v228\n"
"v_mfma_f32_16x16x32_bf16 v[16:19], v[96:99], a[244:247], v[16:19]\n"
"s_waitcnt vmcnt(8) lgkmcnt(1)\n"
"ds_write_b128 v248, v[8:11] offset:49152\n"
"v_cvt_pk_bf16_f32 v56, v56, v57\n"
"v_mul_f32_dpp v3, v246, v3 row_newbcast:3 row_mask:0xf bank_mask:0xf\n"
"v_mul_f32_dpp v36, v247, v36 row_newbcast:0 row_mask:0xf bank_mask:0xf\n"
"v_mul_f32_dpp v64, v206, v64 row_newbcast:0 row_mask:0xf bank_mask:0xf\n"
"v_mfma_f32_16x16x32_bf16 v[16:19], v[100:103], a[248:251], v[16:19]\n"
"s_barrier\n"
"ds_read_b64_tr_b16 a[4:5], v249 offset:49152\n"
"ds_read_b64 v[250:251], v245 offset:52224\n"
"ds_read_b64_tr_b16 v[216:217], v228 offset:24576\n"
"ds_read_b64_tr_b16 v[222:223], v232 offset:26624\n"
"v_mul_f32_dpp v95, v206, v95 row_newbcast:3 row_mask:0xf bank_mask:0xf\n"
"v_mul_f32_dpp v34, v247, v34 row_newbcast:2 row_mask:0xf bank_mask:0xf\n"
"v_mul_f32_dpp v69, v206, v69 row_newbcast:1 row_mask:0xf bank_mask:0xf\n"
"v_mfma_f32_16x16x32_bf16 v[48:51], v[104:107], a[244:247], v[48:51]\n"
"ds_read_b64_tr_b16 v[212:213], v232 offset:16384\n"
"v_add_u32 v236, s76, v249\n"
"v_mfma_f32_16x16x32_bf16 v[48:51], v[108:111], a[248:251], v[48:51]\n"
"ds_read_b64_tr_b16 a[6:7], v249 offset:49160\n"
"ds_read_b64_tr_b16 v[186:187], v232 offset:2048\n"
"ds_read_b64_tr_b16 v[188:189], v228 offset:8192\n"
"ds_read_b64_tr_b16 v[194:195], v232 offset:10240\n"
"v_sub_u32 v229, v233, v254\n"
"v_cvt_pk_bf16_f32 v74, v76, v77\n"
"v_cvt_pk_bf16_f32 v75, v78, v79\n"
"v_mul_f32_dpp v62, v247, v62 row_newbcast:2 row_mask:0xf bank_mask:0xf\n"
"v_mul_f32_dpp v0, v246, v0 row_newbcast:0 row_mask:0xf bank_mask:0xf\n"
"v_mul_f32_dpp v4, v246, v4 row_newbcast:0 row_mask:0xf bank_mask:0xf\n"
"v_mfma_f32_16x16x32_bf16 v[80:83], v[112:115], a[244:247], v[80:83]\n"
"ds_write_b128 v248, v[72:75] offset:51200\n"
"v_mul_f32_dpp v28, v246, v28 row_newbcast:0 row_mask:0xf bank_mask:0xf\n"
"v_cvt_pk_bf16_f32 v26, v28, v29\n"
"v_mul_f32_dpp v7, v246, v7 row_newbcast:3 row_mask:0xf bank_mask:0xf\n"
"v_cvt_pk_bf16_f32 v0, v0, v1\n"
"v_mul_f32_dpp v65, v206, v65 row_newbcast:1 row_mask:0xf bank_mask:0xf\n"
"v_mfma_f32_16x16x32_bf16 v[80:83], v[116:119], a[248:251], v[80:83]\n"
"ds_read_b64_tr_b16 a[22:23], v249 offset:50184\n"
"ds_read_b64_tr_b16 a[36:37], v249 offset:51200\n"
"ds_read_b64_tr_b16 v[190:191], v228 offset:10240\n"
"ds_read_b64_tr_b16 v[208:209], v228 offset:16384\n"
"v_mul_f32_dpp v61, v247, v61 row_newbcast:1 row_mask:0xf bank_mask:0xf\n"
"v_cvt_pk_bf16_f32 v57, v58, v59\n"
"v_cvt_pk_bf16_f32 v91, v94, v95\n"
"v_mul_f32_dpp v2, v246, v2 row_newbcast:2 row_mask:0xf bank_mask:0xf\n"
"v_mul_f32_dpp v70, v206, v70 row_newbcast:2 row_mask:0xf bank_mask:0xf\n"
"v_mfma_f32_16x16x32_bf16 v[84:87], v[116:119], v[224:227], v[84:87]\n"
"ds_write_b128 v248, v[24:27] offset:49152\n"
"v_sub_u32 v231, v235, v254\n"
"v_mul_f32_dpp v32, v247, v32 row_newbcast:0 row_mask:0xf bank_mask:0xf\n"
"v_mul_f32_dpp v37, v247, v37 row_newbcast:1 row_mask:0xf bank_mask:0xf\n"
"v_cvt_pk_bf16_f32 v64, v64, v65\n"
"v_mul_f32_dpp v48, v247, v48 row_newbcast:0 row_mask:0xf bank_mask:0xf\n"
"v_mfma_f32_16x16x32_bf16 v[84:87], v[112:115], a[252:255], v[84:87]\n"
"ds_read_b64_tr_b16 a[20:21], v249 offset:50176\n"
"ds_read_b64_tr_b16 a[14:15], v249 offset:49160\n"
"ds_read_b64_tr_b16 v[180:181], v228 offset:0\n"
"ds_read_b64_tr_b16 v[210:211], v228 offset:18432\n"
"v_cvt_pk_bf16_f32 v90, v92, v93\n"
"v_mul_f32_dpp v6, v246, v6 row_newbcast:2 row_mask:0xf bank_mask:0xf\n"
"v_xor_b32 v233, 0x1000, v229\n"
"v_xor_b32 v237, 0x400, v236\n"
"v_mfma_f32_16x16x32_bf16 v[52:55], v[108:111], v[224:227], v[52:55]\n"
"ds_read_b64_tr_b16 a[202:203], v236 offset:2056\n"
"v_cvt_pk_bf16_f32 v58, v60, v61\n"
"v_mfma_f32_16x16x32_bf16 v[52:55], v[104:107], a[252:255], v[52:55]\n"
"s_waitcnt lgkmcnt(15)\n"
"ds_read_b64_tr_b16 a[38:39], v249 offset:51208\n"
"ds_read_b64_tr_b16 a[12:13], v249 offset:49152\n"
"ds_read_b64_tr_b16 v[182:183], v228 offset:2048\n"
"ds_read_b64_tr_b16 v[214:215], v232 offset:18432\n"
"v_cvt_pk_bf16_f32 v1, v2, v3\n"
"v_cvt_pk_bf16_f32 v32, v32, v33\n"
"v_mul_f32_dpp v66, v206, v66 row_newbcast:2 row_mask:0xf bank_mask:0xf\n"
"v_mul_f32_dpp v71, v206, v71 row_newbcast:3 row_mask:0xf bank_mask:0xf\n"
"v_min_f32 v254, v252, v250\n"
"v_sub_f32 v252, v254, v252\n"
"v_mfma_f32_16x16x32_bf16 v[20:23], v[100:103], v[224:227], v[20:23]\n"
"ds_read_b64_tr_b16 v[184:185], v232 offset:0\n"
"ds_read_b64_tr_b16 v[192:193], v232 offset:8192\n"
"v_mfma_f32_16x16x32_bf16 v[20:23], v[96:99], a[252:255], v[20:23]\n"
"ds_write_b128 v248, v[88:91] offset:51200\n"
"v_cvt_pk_bf16_f32 v59, v62, v63\n"
"v_cvt_pk_bf16_f32 v3, v6, v7\n"
"v_exp_f32 v252, v252\n"
"v_mul_f32_dpp v80, v206, v80 row_newbcast:0 row_mask:0xf bank_mask:0xf\n"
"v_mfma_f32_16x16x32_bf16 a[228:231], v[120:123], a[252:255], a[228:231]\n"
"s_waitcnt lgkmcnt(4)\n"
"ds_read_b64_tr_b16 a[44:45], v249 offset:51200\n"
"ds_read_b64_tr_b16 a[46:47], v249 offset:51208\n"
"ds_read_b64_tr_b16 v[218:219], v228 offset:26624\n"
"ds_read_b64_tr_b16 v[220:221], v232 offset:24576\n"
"v_cvt_pk_bf16_f32 v33, v34, v35\n"
"v_mul_f32_dpp v68, v206, v68 row_newbcast:0 row_mask:0xf bank_mask:0xf\n"
"v_sub_f32 v250, v254, v250\n"
"v_exp_f32 v250, v250\n"
"v_mfma_f32_16x16x32_bf16 v[124:127], v[180:183], a[96:99], v[196:199]\n"
"s_waitcnt lgkmcnt(6)\n"
"ds_write_b128 v248, v[56:59] offset:50176\n"
"v_mul_f32_dpp v5, v246, v5 row_newbcast:1 row_mask:0xf bank_mask:0xf\n"
"v_cvt_pk_bf16_f32 v2, v4, v5\n"
"v_cvt_pk_bf16_f32 v34, v36, v37\n"
"v_cvt_pk_bf16_f32 v65, v66, v67\n"
"v_cvt_pk_bf16_f32 v67, v70, v71\n"
"v_mfma_f32_16x16x32_bf16 v[124:127], v[184:187], a[100:103], v[124:127]\n"
"ds_read_b64_tr_b16 v[104:105], v229 offset:8192\n"
"ds_read_b64_tr_b16 v[118:119], v233 offset:18432\n"
"v_mfma_f32_16x16x32_bf16 v[124:127], v[188:191], a[104:107], v[124:127]\n"
"s_waitcnt lgkmcnt(8)\n"
"ds_read_b64_tr_b16 a[28:29], v249 offset:50176\n"
"ds_read_b64_tr_b16 a[30:31], v249 offset:50184\n"
"v_mfma_f32_16x16x32_bf16 v[124:127], v[192:195], a[108:111], v[124:127]\n"
"ds_write_b128 v248, v[0:3] offset:49152\n"
"v_mul_f32_dpp v38, v247, v38 row_newbcast:2 row_mask:0xf bank_mask:0xf\n"
"v_cvt_pk_bf16_f32 v35, v38, v39\n"
"v_cvt_pk_bf16_f32 v66, v68, v69\n"
"v_mul_f32_dpp v20, v246, v20 row_newbcast:0 row_mask:0xf bank_mask:0xf\n"
"v_mfma_f32_16x16x32_bf16 v[124:127], v[208:211], a[144:147], v[124:127]\n"
"ds_read_b64_tr_b16 a[2:3], v249 offset:49160\n"
"ds_read_b64_tr_b16 v[108:109], v233 offset:8192\n"
"v_mfma_f32_16x16x32_bf16 v[124:127], v[212:215], a[148:151], v[124:127]\n"
"s_waitcnt lgkmcnt(9)\n"
"ds_write_b128 v248, v[32:35] offset:50176\n"
"ds_write_b128 v248, v[64:67] offset:51200\n"
"v_mfma_f32_16x16x32_bf16 v[124:127], v[216:219], a[152:155], v[124:127]\n"
"s_waitcnt lgkmcnt(10)\n"
"ds_read_b64_tr_b16 a[16:17], v249 offset:50176\n"
"ds_read_b128 v[28:31], v244 offset:33792\n"
"v_mfma_f32_16x16x32_bf16 v[124:127], v[220:223], a[156:159], v[124:127]\n"
"ds_read_b64_tr_b16 a[0:1], v249 offset:49152\n"
"ds_read_b128 v[24:27], v244 offset:32768\n"
"v_mfma_f32_16x16x32_bf16 v[140:143], v[220:223], a[172:175], v[196:199]\n"
"ds_read_b64_tr_b16 a[34:35], v249 offset:51208\n"
"ds_read_b64_tr_b16 v[112:113], v229 offset:16384\n"
"v_mfma_f32_16x16x32_bf16 v[140:143], v[216:219], a[168:171], v[140:143]\n"
"ds_read_b64_tr_b16 a[18:19], v249 offset:50184\n"
"ds_read_b64_tr_b16 a[246:247], v237 offset:4104\n"
"v_mfma_f32_16x16x32_bf16 v[140:143], v[212:215], a[164:167], v[140:143]\n"
"ds_read_b128 v[60:63], v244 offset:35840\n"
"ds_read_b64_tr_b16 a[250:251], v236 offset:6152\n"
"v_mfma_f32_16x16x32_bf16 v[140:143], v[208:211], a[160:163], v[140:143]\n"
"ds_read_b64_tr_b16 a[32:33], v249 offset:51200\n"
"ds_read_b128 v[56:59], v244 offset:34816\n"
"v_mfma_f32_16x16x32_bf16 v[140:143], v[192:195], a[124:127], v[140:143]\n"
"ds_read_b64_tr_b16 a[194:195], v236 offset:8\n"
"ds_read_b64_tr_b16 a[242:243], v236 offset:4104\n"
"v_mfma_f32_16x16x32_bf16 v[140:143], v[188:191], a[120:123], v[140:143]\n"
"ds_read_b64_tr_b16 v[110:111], v233 offset:10240\n"
"ds_read_b64_tr_b16 a[252:253], v237 offset:6144\n"
"v_mfma_f32_16x16x32_bf16 v[140:143], v[184:187], a[116:119], v[140:143]\n"
"ds_read_b64_tr_b16 a[192:193], v236 offset:0\n"
"ds_read_b64_tr_b16 a[200:201], v236 offset:2048\n"
"v_mfma_f32_16x16x32_bf16 v[140:143], v[180:183], a[112:115], v[140:143]\n"
"ds_read_b64_tr_b16 a[198:199], v237 offset:8\n"
"ds_read_b64_tr_b16 v[102:103], v233 offset:2048\n"
"v_mfma_f32_16x16x32_bf16 v[156:159], v[180:183], a[128:131], v[196:199]\n"
"ds_read_b64_tr_b16 a[244:245], v237 offset:4096\n"
"ds_read_b64_tr_b16 a[254:255], v237 offset:6152\n"
"v_mfma_f32_16x16x32_bf16 v[156:159], v[184:187], a[132:135], v[156:159]\n"
"ds_read_b64_tr_b16 a[206:207], v237 offset:2056\n"
"ds_read_b64_tr_b16 a[248:249], v236 offset:6144\n"
"v_mfma_f32_16x16x32_bf16 v[156:159], v[188:191], a[136:139], v[156:159]\n"
"ds_read_b64_tr_b16 a[240:241], v236 offset:4096\n"
"ds_read_b64_tr_b16 a[236:237], v237 offset:10240\n"
"v_mfma_f32_16x16x32_bf16 v[156:159], v[192:195], a[140:143], v[156:159]\n"
"s_waitcnt lgkmcnt(14)\n"
"ds_read_b64_tr_b16 a[196:197], v237 offset:0\n"
"ds_read_b64_tr_b16 v[96:97], v229 offset:0\n"
"v_mfma_f32_16x16x32_bf16 v[156:159], v[208:211], a[176:179], v[156:159]\n"
"ds_read_b64_tr_b16 a[204:205], v237 offset:2048\n"
"ds_read_b64_tr_b16 v[100:101], v233 offset:0\n"
"v_mfma_f32_16x16x32_bf16 v[156:159], v[212:215], a[180:183], v[156:159]\n"
"s_add_u32 s41, s95, 491520\n"
"ds_read_b64_tr_b16 v[98:99], v229 offset:2048\n"
"ds_read_b64_tr_b16 v[106:107], v229 offset:10240\n"
"v_mfma_f32_16x16x32_bf16 v[156:159], v[216:219], a[184:187], v[156:159]\n"
"s_add_u32 m0, s44, 49152\n"
"v_xor_b32 v235, 0x1000, v231\n"
"v_mul_f32_dpp v87, v206, v87 row_newbcast:3 row_mask:0xf bank_mask:0xf\n"
"v_mfma_f32_16x16x32_bf16 v[156:159], v[220:223], a[188:191], v[156:159]\n"
"s_waitcnt lgkmcnt(10)\n"
"buffer_load_dwordx4 v240, s[28:31], s41 offen lds\n"
"v_mfma_f32_16x16x32_bf16 v[172:175], v[220:223], a[252:255], v[196:199]\n"
"s_waitcnt lgkmcnt(8)\n"
"s_add_u32 s40, s41, s52\n"
"ds_read_b64_tr_b16 v[114:115], v229 offset:18432\n"
"ds_read_b64_tr_b16 v[116:117], v233 offset:16384\n"
"v_mfma_f32_16x16x32_bf16 v[172:175], v[216:219], a[248:251], v[172:175]\n"
"buffer_load_dwordx4 v240, s[28:31], s40 offen offset:1024 lds\n"
"v_mfma_f32_16x16x32_bf16 v[172:175], v[212:215], a[244:247], v[172:175]\n"
"s_waitcnt lgkmcnt(8)\n"
"s_add_u32 s40, s41, s53\n"
"v_mul_f32 v253, v253, v252\n"
"v_xor_b32 v234, 0x1000, v230\n"
"v_mfma_f32_16x16x32_bf16 v[172:175], v[208:211], a[240:243], v[172:175]\n"
"s_waitcnt lgkmcnt(5)\n"
"buffer_load_dwordx4 v240, s[28:31], s40 offen offset:2048 lds\n"
"v_mfma_f32_16x16x32_bf16 v[172:175], v[192:195], a[204:207], v[172:175]\n"
"ds_read_b64_tr_b16 v[120:121], v229 offset:24576\n"
"ds_read_b64_tr_b16 v[226:227], v233 offset:26624\n"
"v_mfma_f32_16x16x32_bf16 v[172:175], v[188:191], a[200:203], v[172:175]\n"
"ds_read_b64_tr_b16 v[122:123], v229 offset:26624\n"
"ds_read_b64_tr_b16 v[214:215], v234 offset:18432\n"
"v_mfma_f32_16x16x32_bf16 v[172:175], v[184:187], a[196:199], v[172:175]\n"
"s_add_u32 s40, s41, s54\n"
"ds_read_b64_tr_b16 v[224:225], v233 offset:24576\n"
"v_fmac_f32 v253, v251, v250\n"
"v_mfma_f32_16x16x32_bf16 v[172:175], v[180:183], a[192:195], v[172:175]\n"
"s_waitcnt lgkmcnt(7)\n"
"buffer_load_dwordx4 v240, s[28:31], s40 offen offset:3072 lds\n"
"v_mfma_f32_16x16x32_bf16 v[128:131], v[96:99], a[96:99], v[196:199]\n"
"v_rcp_f32 v253, v253\n"
"v_mfma_f32_16x16x32_bf16 v[128:131], v[100:103], a[100:103], v[128:131]\n"
"v_mul_f32 v252, v252, v253\n"
"v_mul_f32_dpp v49, v247, v49 row_newbcast:1 row_mask:0xf bank_mask:0xf\n"
"v_mfma_f32_16x16x32_bf16 v[128:131], v[104:107], a[104:107], v[128:131]\n"
"ds_read_b64_tr_b16 v[220:221], v234 offset:24576\n"
"v_mul_f32 v250, v250, v253\n"
"v_mfma_f32_16x16x32_bf16 v[128:131], v[108:111], a[108:111], v[128:131]\n"
"s_waitcnt lgkmcnt(6)\n"
"ds_read_b64_tr_b16 v[180:181], v230 offset:0\n"
"v_mov_b32_dpp v250, v250 row_ror:12 row_mask:0x2 bank_mask:0xf\n"
"v_mfma_f32_16x16x32_bf16 v[128:131], v[112:115], a[144:147], v[128:131]\n"
"s_add_u32 s21, s60, 32\n"
"ds_read_b64_tr_b16 v[188:189], v230 offset:8192\n"
"ds_read_b64_tr_b16 v[194:195], v234 offset:10240\n"
"v_mfma_f32_16x16x32_bf16 v[128:131], v[116:119], a[148:151], v[128:131]\n"
"s_waitcnt lgkmcnt(5)\n"
"s_mov_b32 s69, 1\n"
"ds_read_b64_tr_b16 v[212:213], v234 offset:16384\n"
"v_mov_b32_dpp v250, v250 row_ror:8 row_mask:0x4 bank_mask:0xf\n"
"v_mfma_f32_16x16x32_bf16 v[128:131], v[120:123], a[152:155], v[128:131]\n"
"s_waitcnt lgkmcnt(5)\n"
"v_bfe_u32 v201, v242, 2, 7\n"
"v_add_u32 v201, 0x100, v201\n"
"ds_read_b64_tr_b16 v[192:193], v234 offset:8192\n"
"v_mfma_f32_16x16x32_bf16 v[128:131], v[224:227], a[156:159], v[128:131]\n"
"global_load_dword v200, v201, s[6:7] sc1\n"
"v_mov_b32_dpp v252, v252 row_ror:12 row_mask:0x2 bank_mask:0xf\n"
"v_mul_f32_dpp v22, v246, v22 row_newbcast:2 row_mask:0xf bank_mask:0xf\n"
"v_mul_f32_dpp v84, v206, v84 row_newbcast:0 row_mask:0xf bank_mask:0xf\n"
"v_mfma_f32_16x16x32_bf16 v[144:147], v[224:227], a[172:175], v[196:199]\n"
"v_mov_b32_dpp v250, v250 row_ror:4 row_mask:0x8 bank_mask:0xf\n"
"v_accvgpr_read_b32 v254, a208\n"
"v_mfma_f32_16x16x32_bf16 v[144:147], v[120:123], a[168:171], v[144:147]\n"
"v_mul_f32_dpp v24, v250, v24 row_newbcast:0 row_mask:0xf bank_mask:0xf\n"
"v_mul_f32_dpp v17, v246, v17 row_newbcast:1 row_mask:0xf bank_mask:0xf\n"
"v_mfma_f32_16x16x32_bf16 v[144:147], v[116:119], a[164:167], v[144:147]\n"
"v_mul_f32_dpp v29, v250, v29 row_newbcast:1 row_mask:0xf bank_mask:0xf\n"
"v_mul_f32_dpp v23, v246, v23 row_newbcast:3 row_mask:0xf bank_mask:0xf\n"
"v_mfma_f32_16x16x32_bf16 v[144:147], v[112:115], a[160:163], v[144:147]\n"
"ds_read_b64_tr_b16 v[210:211], v230 offset:18432\n"
"v_accvgpr_read_b32 v251, a211\n"
"v_mfma_f32_16x16x32_bf16 v[144:147], v[108:111], a[124:127], v[144:147]\n"
"v_mov_b32_dpp v252, v252 row_ror:8 row_mask:0x4 bank_mask:0xf\n"
"v_mul_f32_dpp v31, v250, v31 row_newbcast:3 row_mask:0xf bank_mask:0xf\n"
"v_mfma_f32_16x16x32_bf16 v[144:147], v[104:107], a[120:123], v[144:147]\n"
"v_accvgpr_read_b32 v253, a210\n"
"v_mul_f32_dpp v82, v206, v82 row_newbcast:2 row_mask:0xf bank_mask:0xf\n"
"v_mfma_f32_16x16x32_bf16 v[144:147], v[100:103], a[116:119], v[144:147]\n"
"ds_read_b64_tr_b16 a[220:221], v237 offset:8192\n"
"v_mul_f32_dpp v57, v250, v57 row_newbcast:1 row_mask:0xf bank_mask:0xf\n"
"v_mfma_f32_16x16x32_bf16 v[144:147], v[96:99], a[112:115], v[144:147]\n"
"ds_read_b64_tr_b16 v[184:185], v234 offset:0\n"
"v_mul_f32_dpp v25, v250, v25 row_newbcast:1 row_mask:0xf bank_mask:0xf\n"
"v_mfma_f32_16x16x32_bf16 v[160:163], v[96:99], a[128:131], v[196:199]\n"
"ds_read_b64_tr_b16 v[222:223], v234 offset:26624\n"
"v_mul_f32_dpp v16, v246, v16 row_newbcast:0 row_mask:0xf bank_mask:0xf\n"
"v_mfma_f32_16x16x32_bf16 v[160:163], v[100:103], a[132:135], v[160:163]\n"
"ds_read_b64_tr_b16 v[182:183], v230 offset:2048\n"
"v_mul_f32_dpp v55, v247, v55 row_newbcast:3 row_mask:0xf bank_mask:0xf\n"
"v_mfma_f32_16x16x32_bf16 v[160:163], v[104:107], a[136:139], v[160:163]\n"
"v_mov_b32_dpp v252, v252 row_ror:4 row_mask:0x8 bank_mask:0xf\n"
"v_mul_f32_dpp v81, v206, v81 row_newbcast:1 row_mask:0xf bank_mask:0xf\n"
"v_mfma_f32_16x16x32_bf16 v[160:163], v[108:111], a[140:143], v[160:163]\n"
"ds_read_b64_tr_b16 a[222:223], v237 offset:8200\n"
"v_mul_f32_dpp v19, v246, v19 row_newbcast:3 row_mask:0xf bank_mask:0xf\n"
"v_mfma_f32_16x16x32_bf16 v[160:163], v[112:115], a[176:179], v[160:163]\n"
"v_fmac_f32_dpp v24, v252, v254 row_newbcast:0 row_mask:0xf bank_mask:0xf\n"
"v_mul_f32_dpp v58, v250, v58 row_newbcast:2 row_mask:0xf bank_mask:0xf\n"
"v_mfma_f32_16x16x32_bf16 v[160:163], v[116:119], a[180:183], v[160:163]\n"
"ds_read_b64_tr_b16 v[186:187], v234 offset:2048\n"
"v_mul_f32_dpp v28, v250, v28 row_newbcast:0 row_mask:0xf bank_mask:0xf\n"
"v_mfma_f32_16x16x32_bf16 v[160:163], v[120:123], a[184:187], v[160:163]\n"
"ds_read_b64_tr_b16 v[190:191], v230 offset:10240\n"
"v_mul_f32_dpp v63, v250, v63 row_newbcast:3 row_mask:0xf bank_mask:0xf\n"
"v_mfma_f32_16x16x32_bf16 v[160:163], v[224:227], a[188:191], v[160:163]\n"
"v_fmac_f32_dpp v25, v252, v255 row_newbcast:1 row_mask:0xf bank_mask:0xf\n"
"v_accvgpr_read_b32 v255, a213\n"
"v_mfma_f32_16x16x32_bf16 v[176:179], v[224:227], a[252:255], v[196:199]\n"
"v_mul_f32_dpp v26, v250, v26 row_newbcast:2 row_mask:0xf bank_mask:0xf\n"
"v_mul_f32_dpp v59, v250, v59 row_newbcast:3 row_mask:0xf bank_mask:0xf\n"
"v_mfma_f32_16x16x32_bf16 v[176:179], v[120:123], a[248:251], v[176:179]\n"
"ds_read_b64_tr_b16 v[208:209], v230 offset:16384\n"
"v_mul_f32_dpp v27, v250, v27 row_newbcast:3 row_mask:0xf bank_mask:0xf\n"
"v_mfma_f32_16x16x32_bf16 v[176:179], v[116:119], a[244:247], v[176:179]\n"
"v_accvgpr_read_b32 v254, a212\n"
"v_mul_f32_dpp v62, v250, v62 row_newbcast:2 row_mask:0xf bank_mask:0xf\n"
"v_mfma_f32_16x16x32_bf16 v[176:179], v[112:115], a[240:243], v[176:179]\n"
"v_fmac_f32_dpp v26, v252, v253 row_newbcast:2 row_mask:0xf bank_mask:0xf\n"
"v_xor_b32 v249, s75, v249\n"
"v_mfma_f32_16x16x32_bf16 v[176:179], v[108:111], a[204:207], v[176:179]\n"
"ds_read_b64_tr_b16 v[218:219], v230 offset:26624\n"
"v_mul_f32_dpp v61, v250, v61 row_newbcast:1 row_mask:0xf bank_mask:0xf\n"
"v_mfma_f32_16x16x32_bf16 v[176:179], v[104:107], a[200:203], v[176:179]\n"
"v_fmac_f32_dpp v27, v252, v251 row_newbcast:3 row_mask:0xf bank_mask:0xf\n"
"v_fmac_f32_dpp v29, v252, v255 row_newbcast:1 row_mask:0xf bank_mask:0xf\n"
"v_mfma_f32_16x16x32_bf16 v[176:179], v[100:103], a[196:199], v[176:179]\n"
"ds_read_b64_tr_b16 v[216:217], v230 offset:24576\n"
"v_cvt_pk_bf16_f32 v16, v16, v17\n"
"v_mfma_f32_16x16x32_bf16 v[176:179], v[96:99], a[192:195], v[176:179]\n"
"s_waitcnt lgkmcnt(3)\n"
"ds_read_b64_tr_b16 v[98:99], v231 offset:2048\n"
"ds_read_b64_tr_b16 v[100:101], v235 offset:0\n"
"v_accvgpr_read_b32 v253, a214\n"
"v_mul_f32_dpp v30, v250, v30 row_newbcast:2 row_mask:0xf bank_mask:0xf\n"
"v_cvt_pk_bf16_f32 v24, v24, v25\n"
"v_mul_f32_dpp v18, v246, v18 row_newbcast:2 row_mask:0xf bank_mask:0xf\n"
"v_mul_f32_dpp v56, v250, v56 row_newbcast:0 row_mask:0xf bank_mask:0xf\n"
"v_accvgpr_read_b32 v255, a225\n"
"v_mfma_f32_16x16x32_bf16 v[132:135], v[180:183], a[96:99], v[196:199]\n"
"v_fmac_f32_dpp v30, v252, v253 row_newbcast:2 row_mask:0xf bank_mask:0xf\n"
"v_mul_f32_dpp v60, v250, v60 row_newbcast:0 row_mask:0xf bank_mask:0xf\n"
"v_mfma_f32_16x16x32_bf16 v[132:135], v[184:187], a[100:103], v[132:135]\n"
"v_mul_f32_dpp v83, v206, v83 row_newbcast:3 row_mask:0xf bank_mask:0xf\n"
"v_accvgpr_read_b32 v253, a226\n"
"v_mfma_f32_16x16x32_bf16 v[132:135], v[188:191], a[104:107], v[132:135]\n"
"ds_read_b64_tr_b16 v[108:109], v235 offset:8192\n"
"v_fmac_f32_dpp v58, v252, v253 row_newbcast:2 row_mask:0xf bank_mask:0xf\n"
"v_mfma_f32_16x16x32_bf16 v[132:135], v[192:195], a[108:111], v[132:135]\n"
"s_waitcnt lgkmcnt(5)\n"
"ds_read_b64_tr_b16 v[120:121], v231 offset:24576\n"
"v_accvgpr_read_b32 v253, a230\n"
"v_mfma_f32_16x16x32_bf16 v[132:135], v[208:211], a[144:147], v[132:135]\n"
"s_waitcnt vmcnt(0)\n"
"v_accvgpr_read_b32 v251, a215\n"
"v_mul_f32_dpp v85, v206, v85 row_newbcast:1 row_mask:0xf bank_mask:0xf\n"
"v_mfma_f32_16x16x32_bf16 v[132:135], v[212:215], a[148:151], v[132:135]\n"
"s_waitcnt lgkmcnt(4)\n"
"v_subrev_u32 v200, s21, v200\n"
"v_fmac_f32_dpp v62, v252, v253 row_newbcast:2 row_mask:0xf bank_mask:0xf\n"
"v_mfma_f32_16x16x32_bf16 v[132:135], v[216:219], a[152:155], v[132:135]\n"
"v_cmp_lt_u32 vcc, s69, v200\n"
"ds_read_b64_tr_b16 v[226:227], v235 offset:26624\n"
"v_fmac_f32_dpp v28, v252, v254 row_newbcast:0 row_mask:0xf bank_mask:0xf\n"
"v_fmac_f32_dpp v31, v252, v251 row_newbcast:3 row_mask:0xf bank_mask:0xf\n"
"v_cvt_pk_bf16_f32 v25, v26, v27\n"
"v_cvt_pk_bf16_f32 v27, v30, v31\n"
"v_accvgpr_read_b32 v254, a224\n"
"v_accvgpr_read_b32 v251, a227\n"
"v_mfma_f32_16x16x32_bf16 v[132:135], v[220:223], a[156:159], v[132:135]\n"
"ds_read_b64_tr_b16 v[96:97], v231 offset:0\n"
"v_mul_f32_dpp v21, v246, v21 row_newbcast:1 row_mask:0xf bank_mask:0xf\n"
"v_mul_f32_dpp v86, v206, v86 row_newbcast:2 row_mask:0xf bank_mask:0xf\n"
"v_fmac_f32_dpp v56, v252, v254 row_newbcast:0 row_mask:0xf bank_mask:0xf\n"
"v_fmac_f32_dpp v57, v252, v255 row_newbcast:1 row_mask:0xf bank_mask:0xf\n"
"v_fmac_f32_dpp v59, v252, v251 row_newbcast:3 row_mask:0xf bank_mask:0xf\n"
"v_accvgpr_read_b32 v255, a229\n"
"v_accvgpr_read_b32 v251, a231\n"
"v_cvt_pk_bf16_f32 v56, v56, v57\n"
"s_cbranch_vccnz LGV_%=\nLGVK_%=:\n"
"v_mfma_f32_16x16x32_bf16 v[148:151], v[220:223], a[172:175], v[196:199]\n"
"ds_read_b64_tr_b16 a[234:235], v236 offset:10248\n"
"v_cvt_pk_bf16_f32 v26, v28, v29\n"
"v_mfma_f32_16x16x32_bf16 v[148:151], v[216:219], a[168:171], v[148:151]\n"
"v_add_u32_dpp v200, v200, v200 quad_perm:[1,0,3,2] row_mask:0xf bank_mask:0xf\n"
"ds_read_b64_tr_b16 v[104:105], v231 offset:8192\n"
"v_mfma_f32_16x16x32_bf16 v[148:151], v[212:215], a[164:167], v[148:151]\n"
"v_accvgpr_read_b32 v254, a228\n"
"v_cvt_pk_bf16_f32 v57, v58, v59\n"
"v_mfma_f32_16x16x32_bf16 v[148:151], v[208:211], a[160:163], v[148:151]\n"
"v_add_u32_dpp v200, v200, v200 quad_perm:[2,3,0,1] row_mask:0xf bank_mask:0xf\n"
"ds_write_b128 v243, v[24:27] offset:0\n"
"v_cvt_pk_bf16_f32 v17, v18, v19\n"
"v_mul_f32_dpp v51, v247, v51 row_newbcast:3 row_mask:0xf bank_mask:0xf\n"
"v_fmac_f32_dpp v60, v252, v254 row_newbcast:0 row_mask:0xf bank_mask:0xf\n"
"v_fmac_f32_dpp v63, v252, v251 row_newbcast:3 row_mask:0xf bank_mask:0xf\n"
"v_mfma_f32_16x16x32_bf16 v[148:151], v[192:195], a[124:127], v[148:151]\n"
"v_fmac_f32_dpp v61, v252, v255 row_newbcast:1 row_mask:0xf bank_mask:0xf\n"
"v_cvt_pk_bf16_f32 v59, v62, v63\n"
"v_mfma_f32_16x16x32_bf16 v[148:151], v[188:191], a[120:123], v[148:151]\n"
"v_cmp_lt_u32 vcc, 1, v200\n"
"ds_read_b64_tr_b16 v[118:119], v235 offset:18432\n"
"v_mfma_f32_16x16x32_bf16 v[148:151], v[184:187], a[116:119], v[148:151]\n"
"ds_read_b64_tr_b16 v[102:103], v235 offset:2048\n"
"v_cvt_pk_bf16_f32 v58, v60, v61\n"
"v_mfma_f32_16x16x32_bf16 v[148:151], v[180:183], a[112:115], v[148:151]\n"
"ds_write_b128 v243, v[56:59] offset:2048\n"
"v_cvt_pk_bf16_f32 v80, v80, v81\n"
"v_cvt_pk_bf16_f32 v81, v82, v83\n"
"v_mfma_f32_16x16x32_bf16 v[164:167], v[180:183], a[128:131], v[196:199]\n"
"s_mov_b32 s47, 0\n"
"v_xor_b32 v248, s75, v248\n"
"v_cvt_pk_bf16_f32 v83, v86, v87\n"
"v_mfma_f32_16x16x32_bf16 v[164:167], v[184:187], a[132:135], v[164:167]\n"
"s_mov_b32 s89, vcc_lo\n"
"ds_read_b64_tr_b16 v[114:115], v231 offset:18432\n"
"ds_read_b64_tr_b16 v[122:123], v231 offset:26624\n"
"v_mfma_f32_16x16x32_bf16 v[164:167], v[188:191], a[136:139], v[164:167]\n"
"s_lshr_b32 s90, s89, s93\n"
"v_mul_f32_dpp v50, v247, v50 row_newbcast:2 row_mask:0xf bank_mask:0xf\n"
"v_cvt_pk_bf16_f32 v82, v84, v85\n"
"v_mfma_f32_16x16x32_bf16 v[164:167], v[192:195], a[140:143], v[164:167]\n"
"s_and_b32 s90, s90, 1\n"
"ds_read_b64_tr_b16 v[106:107], v231 offset:10240\n"
"ds_read_b64_tr_b16 v[110:111], v235 offset:10240\n"
"v_mfma_f32_16x16x32_bf16 v[164:167], v[208:211], a[176:179], v[164:167]\n"
"s_lshl_b32 s16, s61, 7\n"
"ds_read_b64_tr_b16 v[116:117], v235 offset:16384\n"
"ds_read_b64_tr_b16 v[224:225], v235 offset:24576\n"
"v_mfma_f32_16x16x32_bf16 v[164:167], v[212:215], a[180:183], v[164:167]\n"
"s_add_u32 s90, s90, s16\n"
"ds_read_b64_tr_b16 v[112:113], v231 offset:16384\n"
"v_mul_f32_dpp v53, v247, v53 row_newbcast:1 row_mask:0xf bank_mask:0xf\n"
"v_mfma_f32_16x16x32_bf16 v[164:167], v[216:219], a[184:187], v[164:167]\n"
"s_waitcnt vmcnt(63) lgkmcnt(7)\n"
"s_lshl_b32 s90, s90, 13\n"
"ds_read_b64_tr_b16 a[216:217], v236 offset:8192\n"
"v_mfma_f32_16x16x32_bf16 v[164:167], v[220:223], a[188:191], v[164:167]\n"
"s_barrier\n"
"s_lshl_b32 s16, s95, 9\n"
"ds_write_b128 v248, v[80:83] offset:51200\n"
"v_cvt_pk_bf16_f32 v48, v48, v49\n"
"v_mfma_f32_16x16x32_bf16 v[136:139], v[96:99], a[96:99], v[196:199]\n"
"s_add_u32 s16, s16, s90\n"
"ds_read_b64_tr_b16 a[214:215], v237 offset:12296\n"
"v_cvt_pk_bf16_f32 v18, v20, v21\n"
"v_mfma_f32_16x16x32_bf16 v[136:139], v[100:103], a[100:103], v[136:139]\n"
"s_waitcnt lgkmcnt(7)\n"
"s_add_u32 s17, s16, 4096\n"
"ds_read_b64_tr_b16 v[216:217], v228 offset:57344\n"
"v_cvt_pk_bf16_f32 v19, v22, v23\n"
"v_mfma_f32_16x16x32_bf16 v[136:139], v[104:107], a[104:107], v[136:139]\n"
"s_waitcnt lgkmcnt(7)\n"
"s_mov_b32 s72, s61\n"
"ds_read_b64_tr_b16 v[194:195], v232 offset:43008\n"
"v_cvt_pk_bf16_f32 v49, v50, v51\n"
"v_mfma_f32_16x16x32_bf16 v[136:139], v[108:111], a[108:111], v[136:139]\n"
"s_waitcnt lgkmcnt(5)\n"
"s_mov_b32 s21, s60\n"
"ds_write_b128 v248, v[16:19] offset:49152\n"
"v_mul_f32_dpp v54, v247, v54 row_newbcast:2 row_mask:0xf bank_mask:0xf\n"
"v_mfma_f32_16x16x32_bf16 v[136:139], v[112:115], a[144:147], v[136:139]\n"
"s_mov_b32 s18, s77\n"
"v_mul_f32_dpp v52, v247, v52 row_newbcast:0 row_mask:0xf bank_mask:0xf\n"
"v_cvt_pk_bf16_f32 v50, v52, v53\n"
"v_mfma_f32_16x16x32_bf16 v[136:139], v[116:119], a[148:151], v[136:139]\n"
"s_mov_b32 s19, s77\n"
"ds_read_b64_tr_b16 a[10:11], v249 offset:49160\n"
"ds_read_b64_tr_b16 a[40:41], v249 offset:51200\n"
"v_mfma_f32_16x16x32_bf16 v[136:139], v[120:123], a[152:155], v[136:139]\n"
"s_mov_b32 s69, -1\n"
"ds_read_b64_tr_b16 a[42:43], v249 offset:51208\n"
"v_cvt_pk_bf16_f32 v51, v54, v55\n"
"v_mfma_f32_16x16x32_bf16 v[136:139], v[224:227], a[156:159], v[136:139]\n"
"s_mul_i32 s88, s61, 0x10011111\n"
"ds_write_b128 v248, v[48:51] offset:50176\n"
"v_mfma_f32_16x16x32_bf16 v[152:155], v[224:227], a[172:175], v[196:199]\n"
"s_add_u32 s88, s88, 484675\n"
"v_mov_b64 v[86:87], 0\n"
"v_mfma_f32_16x16x32_bf16 v[152:155], v[120:123], a[168:171], v[152:155]\n"
"s_and_b32 s88, s88, 0x70077777\n"
"ds_read_b64_tr_b16 a[8:9], v249 offset:49152\n"
"ds_read_b64_tr_b16 v[188:189], v228 offset:40960\n"
"v_mfma_f32_16x16x32_bf16 v[152:155], v[116:119], a[164:167], v[152:155]\n"
"s_or_b32 s88, s88, 0x9800000\n"
"ds_read_b64_tr_b16 a[24:25], v249 offset:50176\n"
"ds_read_b64_tr_b16 a[26:27], v249 offset:50184\n"
"v_mfma_f32_16x16x32_bf16 v[152:155], v[112:115], a[160:163], v[152:155]\n"
"s_mov_b32 s84, s6\n"
"ds_read_b64_tr_b16 v[186:187], v232 offset:34816\n"
"v_mov_b64 v[14:15], s[22:23]\n"
"v_mfma_f32_16x16x32_bf16 v[152:155], v[108:111], a[124:127], v[152:155]\n"
"s_and_b32 s85, s7, 0xffff\n"
"ds_read_b64_tr_b16 v[180:181], v228 offset:32768\n"
"ds_read_b64_tr_b16 v[190:191], v228 offset:43008\n"
"v_mfma_f32_16x16x32_bf16 v[152:155], v[104:107], a[120:123], v[152:155]\n"
"s_mov_b32 s86, 128\n"
"ds_read_b64_tr_b16 v[210:211], v228 offset:51200\n"
"ds_read_b64_tr_b16 v[222:223], v232 offset:59392\n"
"v_mfma_f32_16x16x32_bf16 v[152:155], v[100:103], a[116:119], v[152:155]\n"
"s_mov_b32 s87, 131072\n"
"ds_read_b64_tr_b16 v[182:183], v228 offset:34816\n"
"v_mov_b64 v[52:53], s[22:23]\n"
"v_mfma_f32_16x16x32_bf16 v[152:155], v[96:99], a[112:115], v[152:155]\n"
"ds_read_b64_tr_b16 v[184:185], v232 offset:32768\n"
"ds_read_b64_tr_b16 v[214:215], v232 offset:51200\n"
"v_mfma_f32_16x16x32_bf16 v[168:171], v[96:99], a[128:131], v[196:199]\n"
"ds_read_b64_tr_b16 a[210:211], v236 offset:12296\n"
"v_and_b32 v243, 0x3f0, v242\n"
"v_mfma_f32_16x16x32_bf16 v[168:171], v[100:103], a[132:135], v[168:171]\n"
"ds_read_b64_tr_b16 a[226:227], v236 offset:14344\n"
"ds_read_b64_tr_b16 v[212:213], v232 offset:49152\n"
"v_mfma_f32_16x16x32_bf16 v[168:171], v[104:107], a[136:139], v[168:171]\n"
"ds_read_b64_tr_b16 v[192:193], v232 offset:40960\n"
"v_mov_b64 v[18:19], s[22:23]\n"
"v_mfma_f32_16x16x32_bf16 v[168:171], v[108:111], a[140:143], v[168:171]\n"
"s_waitcnt lgkmcnt(11)\n"
"ds_read_b64_tr_b16 a[218:219], v236 offset:8200\n"
"ds_read_b64_tr_b16 v[208:209], v228 offset:49152\n"
"v_mfma_f32_16x16x32_bf16 v[168:171], v[112:115], a[176:179], v[168:171]\n"
"ds_read_b64_tr_b16 v[218:219], v228 offset:59392\n"
"v_mov_b64 v[2:3], s[22:23]\n"
"v_mfma_f32_16x16x32_bf16 v[168:171], v[116:119], a[180:183], v[168:171]\n"
"ds_read_b64_tr_b16 a[224:225], v236 offset:14336\n"
"ds_read_b64_tr_b16 a[230:231], v237 offset:14344\n"
"v_mfma_f32_16x16x32_bf16 v[168:171], v[120:123], a[184:187], v[168:171]\n"
"s_add_u32 s41, s70, 327680\n"
"s_add_u32 m0, s71, 32768\n"
"ds_read_b64_tr_b16 v[220:221], v232 offset:57344\n"
"v_mfma_f32_16x16x32_bf16 v[168:171], v[224:227], a[188:191], v[168:171]\n"
"s_waitcnt lgkmcnt(10)\n"
"buffer_load_dwordx4 v240, s[28:31], s41 offen lds\n"
"v_mfma_f32_16x16x32_bf16 v[124:127], v[180:183], a[48:51], v[124:127]\n"
"v_mov_b64 v[42:43], s[22:23]\n"
"v_mov_b64 v[94:95], 0\n"
"v_mfma_f32_16x16x32_bf16 v[124:127], v[184:187], a[52:55], v[124:127]\n"
"v_bfe_u32 v254, v242, 4, 4\n"
"v_mov_b64 v[40:41], s[22:23]\n"
"v_mfma_f32_16x16x32_bf16 v[124:127], v[188:191], a[56:59], v[124:127]\n"
"s_waitcnt lgkmcnt(6)\n"
"s_add_u32 s40, s41, s52\n"
"ds_read_b64_tr_b16 a[228:229], v237 offset:14336\n"
"ds_read_b64_tr_b16 v[108:109], v233 offset:40960\n"
"v_mfma_f32_16x16x32_bf16 v[124:127], v[192:195], a[60:63], v[124:127]\n"
"s_waitcnt lgkmcnt(6)\n"
"buffer_load_dwordx4 v240, s[28:31], s40 offen offset:1024 lds\n"
"v_mfma_f32_16x16x32_bf16 v[124:127], v[208:211], a[0:3], v[124:127]\n"
"v_xor_b32 v249, s75, v249\n"
"v_mfma_f32_16x16x32_bf16 v[124:127], v[212:215], a[4:7], v[124:127]\n"
"s_waitcnt lgkmcnt(3)\n"
"ds_read_b64_tr_b16 a[208:209], v236 offset:12288\n"
"ds_read_b64_tr_b16 a[212:213], v237 offset:12288\n"
"v_mfma_f32_16x16x32_bf16 v[124:127], v[216:219], a[8:11], v[124:127]\n"
"s_waitcnt lgkmcnt(4)\n"
"s_add_u32 s40, s41, s53\n"
"ds_read_b64_tr_b16 v[98:99], v229 offset:34816\n"
"ds_read_b64_tr_b16 v[112:113], v229 offset:49152\n"
"v_mfma_f32_16x16x32_bf16 v[124:127], v[220:223], a[12:15], v[124:127]\n"
"buffer_load_dwordx4 v240, s[28:31], s40 offen offset:2048 lds\n"
"v_mfma_f32_16x16x32_bf16 v[140:143], v[220:223], a[28:31], v[140:143]\n"
"v_mul_u32_u24 v254, 2560, v254\n"
"v_bfe_u32 v255, v242, 8, 1\n"
"v_mfma_f32_16x16x32_bf16 v[140:143], v[216:219], a[24:27], v[140:143]\n"
"v_mov_b64 v[10:11], s[22:23]\n"
"v_mfma_f32_16x16x32_bf16 v[140:143], v[212:215], a[20:23], v[140:143]\n"
"ds_read_b64_tr_b16 v[104:105], v229 offset:40960\n"
"ds_read_b64_tr_b16 v[106:107], v229 offset:43008\n"
"v_mfma_f32_16x16x32_bf16 v[140:143], v[208:211], a[16:19], v[140:143]\n"
"ds_read_b64_tr_b16 v[110:111], v233 offset:43008\n"
"v_subrev_f32 v124, s22, v124\n"
"v_mfma_f32_16x16x32_bf16 v[140:143], v[192:195], a[76:79], v[140:143]\n"
"ds_read_b64_tr_b16 a[232:233], v236 offset:10240\n"
"ds_read_b64_tr_b16 v[116:117], v233 offset:49152\n"
"v_mfma_f32_16x16x32_bf16 v[140:143], v[188:191], a[72:75], v[140:143]\n"
"ds_read_b64_tr_b16 v[118:119], v233 offset:51200\n"
"v_lshl_add_u32 v254, v255, 5, v254\n"
"v_mfma_f32_16x16x32_bf16 v[140:143], v[184:187], a[68:71], v[140:143]\n"
"s_add_u32 s40, s41, s54\n"
"ds_read_b64_tr_b16 a[238:239], v237 offset:10248\n"
"ds_read_b64_tr_b16 v[114:115], v229 offset:51200\n"
"v_mfma_f32_16x16x32_bf16 v[140:143], v[180:183], a[64:67], v[140:143]\n"
"buffer_load_dwordx4 v240, s[28:31], s40 offen offset:3072 lds\n"
"v_mfma_f32_16x16x32_bf16 v[156:159], v[180:183], a[80:83], v[156:159]\n"
"s_add_u32 s41, s70, 491520\n"
"s_add_u32 m0, s71, 49152\n"
"v_subrev_f32 v125, s22, v125\n"
"v_mfma_f32_16x16x32_bf16 v[156:159], v[184:187], a[84:87], v[156:159]\n"
"buffer_load_dwordx4 v240, s[28:31], s41 offen lds\n"
"v_mfma_f32_16x16x32_bf16 v[156:159], v[188:191], a[88:91], v[156:159]\n"
"v_subrev_f32 v126, s22, v126\n"
"v_mfma_f32_16x16x32_bf16 v[156:159], v[192:195], a[92:95], v[156:159]\n"
"v_mov_b64 v[36:37], s[22:23]\n"
"v_cvt_pk_bf16_f32 v124, v124, v125\n"
"v_mfma_f32_16x16x32_bf16 v[156:159], v[208:211], a[32:35], v[156:159]\n"
"ds_read_b64_tr_b16 v[100:101], v233 offset:32768\n"
"ds_read_b64_tr_b16 v[226:227], v233 offset:59392\n"
"v_mfma_f32_16x16x32_bf16 v[156:159], v[212:215], a[36:39], v[156:159]\n"
"ds_read_b64_tr_b16 v[96:97], v229 offset:32768\n"
"v_add_u32 v236, s94, v243\n"
"v_mfma_f32_16x16x32_bf16 v[156:159], v[216:219], a[40:43], v[156:159]\n"
"s_add_u32 s40, s41, s52\n"
"ds_read_b64_tr_b16 v[102:103], v233 offset:34816\n"
"ds_read_b64_tr_b16 v[122:123], v229 offset:59392\n"
"v_mfma_f32_16x16x32_bf16 v[156:159], v[220:223], a[44:47], v[156:159]\n"
"s_waitcnt lgkmcnt(5)\n"
"buffer_load_dwordx4 v240, s[28:31], s40 offen offset:1024 lds\n"
"v_mfma_f32_16x16x32_bf16 v[172:175], v[220:223], a[228:231], v[172:175]\n"
"s_add_u32 s40, s41, s53\n"
"v_subrev_f32 v141, s22, v141\n"
"v_mfma_f32_16x16x32_bf16 v[172:175], v[216:219], a[224:227], v[172:175]\n"
"buffer_load_dwordx4 v240, s[28:31], s40 offen offset:2048 lds\n"
"v_mfma_f32_16x16x32_bf16 v[172:175], v[212:215], a[212:215], v[172:175]\n"
"v_mov_b64 v[24:25], s[22:23]\n"
"v_subrev_f32 v127, s22, v127\n"
"v_mfma_f32_16x16x32_bf16 v[172:175], v[208:211], a[208:211], v[172:175]\n"
"v_bfe_u32 v255, v242, 9, 1\n"
"v_mov_b64 v[12:13], s[22:23]\n"
"v_mfma_f32_16x16x32_bf16 v[172:175], v[192:195], a[236:239], v[172:175]\n"
"s_add_u32 s40, s41, s54\n"
"ds_read_b64_tr_b16 v[120:121], v229 offset:57344\n"
"ds_read_b64_tr_b16 v[224:225], v233 offset:57344\n"
"v_mfma_f32_16x16x32_bf16 v[172:175], v[188:191], a[232:235], v[172:175]\n"
"buffer_load_dwordx4 v240, s[28:31], s40 offen offset:3072 lds\n"
"v_mfma_f32_16x16x32_bf16 v[172:175], v[184:187], a[220:223], v[172:175]\n"
"s_lshl_b32 s62, s70, 9\n"
"v_bfe_u32 v237, v242, 4, 3\n"
"v_subrev_f32 v140, s22, v140\n"
"v_mfma_f32_16x16x32_bf16 v[172:175], v[180:183], a[216:219], v[172:175]\n"
"s_waitcnt lgkmcnt(2)\n"
"s_and_b32 s70, s88, 15\n"
"v_mfma_f32_16x16x32_bf16 v[128:131], v[96:99], a[48:51], v[128:131]\n"
"s_lshr_b32 s88, s88, 4\n"
"ds_read_b64_tr_b16 v[212:213], v234 offset:49152\n"
"v_subrev_f32 v156, s22, v156\n"
"v_mfma_f32_16x16x32_bf16 v[128:131], v[100:103], a[52:55], v[128:131]\n"
"s_lshl_b32 s70, s70, 7\n"
"ds_read_b64_tr_b16 v[192:193], v234 offset:40960\n"
"v_mov_b64 v[22:23], s[22:23]\n"
"v_mfma_f32_16x16x32_bf16 v[128:131], v[104:107], a[56:59], v[128:131]\n"
"s_xor_b32 s71, s71, s75\n"
"ds_read_b64_tr_b16 v[218:219], v230 offset:59392\n"
"v_mfma_f32_16x16x32_bf16 v[128:131], v[108:111], a[60:63], v[128:131]\n"
"v_lshlrev_b32_e64 v201, 2, s72\n"
"ds_read_b64_tr_b16 v[184:185], v234 offset:32768\n"
"v_lshl_add_u32 v254, v255, 4, v254\n"
"v_mfma_f32_16x16x32_bf16 v[128:131], v[112:115], a[0:3], v[128:131]\n"
"global_load_dword v200, v201, s[6:7] sc1\n"
"v_mfma_f32_16x16x32_bf16 v[128:131], v[116:119], a[4:7], v[128:131]\n"
"s_waitcnt lgkmcnt(4)\n"
"v_subrev_f32 v172, s22, v172\n"
"v_xor_b32 v229, s75, v229\n"
"v_mfma_f32_16x16x32_bf16 v[128:131], v[120:123], a[8:11], v[128:131]\n"
"v_subrev_f32 v143, s22, v143\n"
"v_mfma_f32_16x16x32_bf16 v[128:131], v[224:227], a[12:15], v[128:131]\n"
"ds_read_b64_tr_b16 v[182:183], v230 offset:34816\n"
"v_cvt_pk_bf16_f32 v140, v140, v141\n"
"v_mfma_f32_16x16x32_bf16 v[144:147], v[224:227], a[28:31], v[144:147]\n"
"ds_read_b64_tr_b16 v[216:217], v230 offset:57344\n"
"v_subrev_f32 v142, s22, v142\n"
"v_mfma_f32_16x16x32_bf16 v[144:147], v[120:123], a[24:27], v[144:147]\n"
"v_xor_b32 v248, s75, v248\n"
"v_mfma_f32_16x16x32_bf16 v[144:147], v[116:119], a[20:23], v[144:147]\n"
"ds_read_b64_tr_b16 v[190:191], v230 offset:43008\n"
"v_subrev_f32 v129, s22, v129\n"
"v_mfma_f32_16x16x32_bf16 v[144:147], v[112:115], a[16:19], v[144:147]\n"
"v_subrev_f32 v131, s22, v131\n"
"v_mfma_f32_16x16x32_bf16 v[144:147], v[108:111], a[76:79], v[144:147]\n"
"ds_read_b64_tr_b16 v[186:187], v234 offset:34816\n"
"v_subrev_f32 v157, s22, v157\n"
"v_mfma_f32_16x16x32_bf16 v[144:147], v[104:107], a[72:75], v[144:147]\n"
"v_mov_b64 v[0:1], s[22:23]\n"
"v_mov_b64 v[28:29], s[22:23]\n"
"v_mfma_f32_16x16x32_bf16 v[144:147], v[100:103], a[68:71], v[144:147]\n"
"ds_read_b64_tr_b16 v[194:195], v234 offset:43008\n"
"v_cvt_pk_bf16_f32 v125, v126, v127\n"
"v_mfma_f32_16x16x32_bf16 v[144:147], v[96:99], a[64:67], v[144:147]\n"
"v_cvt_pk_bf16_f32 v141, v142, v143\n"
"v_subrev_f32 v130, s22, v130\n"
"v_mfma_f32_16x16x32_bf16 v[160:163], v[96:99], a[80:83], v[160:163]\n"
"ds_read_b64_tr_b16 v[220:221], v234 offset:57344\n"
"v_mov_b64 v[6:7], s[22:23]\n"
"v_mfma_f32_16x16x32_bf16 v[160:163], v[100:103], a[84:87], v[160:163]\n"
"v_subrev_f32 v128, s22, v128\n"
"v_cvt_pk_bf16_f32 v126, v128, v129\n"
"v_mfma_f32_16x16x32_bf16 v[160:163], v[104:107], a[88:91], v[160:163]\n"
"ds_read_b64_tr_b16 v[188:189], v230 offset:40960\n"
"v_subrev_f32 v173, s22, v173\n"
"v_mfma_f32_16x16x32_bf16 v[160:163], v[108:111], a[92:95], v[160:163]\n"
"v_mov_b64 v[246:247], 0\n"
"v_subrev_f32 v159, s22, v159\n"
"v_mfma_f32_16x16x32_bf16 v[160:163], v[112:115], a[32:35], v[160:163]\n"
"ds_read_b64_tr_b16 v[180:181], v230 offset:32768\n"
"v_mfma_f32_16x16x32_bf16 v[160:163], v[116:119], a[36:39], v[160:163]\n"
"v_lshl_add_u32 v243, s44, 2, v243\n"
"v_mov_b64 v[90:91], 0\n"
"v_mfma_f32_16x16x32_bf16 v[160:163], v[120:123], a[40:43], v[160:163]\n"
"ds_read_b64_tr_b16 v[208:209], v230 offset:49152\n"
"v_cvt_pk_bf16_f32 v127, v130, v131\n"
"v_mfma_f32_16x16x32_bf16 v[160:163], v[224:227], a[44:47], v[160:163]\n"
"ds_read_b64_tr_b16 v[222:223], v234 offset:59392\n"
"buffer_store_dwordx4 v[124:127], v243, s[32:35], s16 offen sc0 sc1\n"
"v_mfma_f32_16x16x32_bf16 v[176:179], v[224:227], a[228:231], v[176:179]\n"
"v_subrev_f32 v145, s22, v145\n"
"v_subrev_f32 v146, s22, v146\n"
"v_mfma_f32_16x16x32_bf16 v[176:179], v[120:123], a[224:227], v[176:179]\n"
"ds_read_b64_tr_b16 v[210:211], v230 offset:51200\n"
"v_mfma_f32_16x16x32_bf16 v[176:179], v[116:119], a[212:215], v[176:179]\n"
"v_subrev_f32 v147, s22, v147\n"
"v_mfma_f32_16x16x32_bf16 v[176:179], v[112:115], a[208:211], v[176:179]\n"
"ds_read_b64_tr_b16 v[214:215], v234 offset:51200\n"
"v_xor_b32 v232, s75, v232\n"
"v_mfma_f32_16x16x32_bf16 v[176:179], v[108:111], a[236:239], v[176:179]\n"
"v_mov_b64 v[8:9], s[22:23]\n"
"v_cvt_pk_bf16_f32 v156, v156, v157\n"
"v_mfma_f32_16x16x32_bf16 v[176:179], v[104:107], a[232:235], v[176:179]\n"
"ds_read_b64_tr_b16 v[108:109], v235 offset:40960\n"
"v_subrev_f32 v174, s22, v174\n"
"v_mfma_f32_16x16x32_bf16 v[176:179], v[100:103], a[220:223], v[176:179]\n"
"v_mov_b64 v[16:17], s[22:23]\n"
"v_mov_b64 v[44:45], s[22:23]\n"
"v_mfma_f32_16x16x32_bf16 v[176:179], v[96:99], a[216:219], v[176:179]\n"
"s_waitcnt lgkmcnt(3)\n"
"ds_read_b64_tr_b16 v[122:123], v231 offset:59392\n"
"v_cvt_pk_bf16_f32 v143, v146, v147\n"
"v_mfma_f32_16x16x32_bf16 v[132:135], v[180:183], a[48:51], v[132:135]\n"
"v_subrev_f32 v144, s22, v144\n"
"v_subrev_f32 v160, s22, v160\n"
"v_mfma_f32_16x16x32_bf16 v[132:135], v[184:187], a[52:55], v[132:135]\n"
"ds_read_b64_tr_b16 v[100:101], v235 offset:32768\n"
"v_mov_b64 v[20:21], s[22:23]\n"
"v_mfma_f32_16x16x32_bf16 v[132:135], v[188:191], a[56:59], v[132:135]\n"
"v_mov_b64 v[30:31], s[22:23]\n"
"v_cvt_pk_bf16_f32 v142, v144, v145\n"
"v_mfma_f32_16x16x32_bf16 v[132:135], v[192:195], a[60:63], v[132:135]\n"
"s_waitcnt lgkmcnt(3)\n"
"ds_read_b64_tr_b16 v[106:107], v231 offset:43008\n"
"v_mov_b64 v[244:245], 0\n"
"v_mfma_f32_16x16x32_bf16 v[132:135], v[208:211], a[0:3], v[132:135]\n"
"ds_read_b64_tr_b16 v[226:227], v235 offset:59392\n"
"v_subrev_f32 v161, s22, v161\n"
"v_mfma_f32_16x16x32_bf16 v[132:135], v[212:215], a[4:7], v[132:135]\n"
"ds_read_b64_tr_b16 v[120:121], v231 offset:57344\n"
"v_subrev_f32 v158, s22, v158\n"
"v_mfma_f32_16x16x32_bf16 v[132:135], v[216:219], a[8:11], v[132:135]\n"
"ds_read_b64_tr_b16 v[114:115], v231 offset:51200\n"
"v_mov_b64 v[92:93], 0\n"
"v_mfma_f32_16x16x32_bf16 v[132:135], v[220:223], a[12:15], v[132:135]\n"
"buffer_store_dwordx4 v[140:143], v243, s[32:35], s16 offen offset:2048 sc0 sc1\n"
"ds_read_b64_tr_b16 v[98:99], v231 offset:34816\n"
"v_mfma_f32_16x16x32_bf16 v[148:151], v[220:223], a[28:31], v[148:151]\n"
"v_subrev_f32 v163, s22, v163\n"
"v_subrev_f32 v178, s22, v178\n"
"v_mfma_f32_16x16x32_bf16 v[148:151], v[216:219], a[24:27], v[148:151]\n"
"ds_read_b64_tr_b16 v[112:113], v231 offset:49152\n"
"v_mfma_f32_16x16x32_bf16 v[148:151], v[212:215], a[20:23], v[148:151]\n"
"v_subrev_f32 v175, s22, v175\n"
"v_subrev_f32 v177, s22, v177\n"
"v_mfma_f32_16x16x32_bf16 v[148:151], v[208:211], a[16:19], v[148:151]\n"
"ds_read_b64_tr_b16 v[110:111], v235 offset:43008\n"
"v_xor_b32 v228, s75, v228\n"
"v_mfma_f32_16x16x32_bf16 v[148:151], v[192:195], a[76:79], v[148:151]\n"
"ds_read_b64_tr_b16 v[104:105], v231 offset:40960\n"
"v_subrev_f32 v162, s22, v162\n"
"v_mfma_f32_16x16x32_bf16 v[148:151], v[188:191], a[72:75], v[148:151]\n"
"v_max_u32 v237, 1, v237\n"
"v_cvt_pk_bf16_f32 v172, v172, v173\n"
"v_mfma_f32_16x16x32_bf16 v[148:151], v[184:187], a[68:71], v[148:151]\n"
"ds_read_b64_tr_b16 v[96:97], v231 offset:32768\n"
"v_subrev_f32 v135, s22, v135\n"
"v_mfma_f32_16x16x32_bf16 v[148:151], v[180:183], a[64:67], v[148:151]\n"
"v_mov_b64 v[48:49], s[22:23]\n"
"v_cvt_pk_bf16_f32 v157, v158, v159\n"
"v_mfma_f32_16x16x32_bf16 v[164:167], v[180:183], a[80:83], v[164:167]\n"
"ds_read_b64_tr_b16 v[102:103], v235 offset:34816\n"
"v_mov_b64 v[50:51], s[22:23]\n"
"v_mfma_f32_16x16x32_bf16 v[164:167], v[184:187], a[84:87], v[164:167]\n"
"v_add_u32 v238, s97, v254\n"
"v_xor_b32 v233, s75, v233\n"
"v_mfma_f32_16x16x32_bf16 v[164:167], v[188:191], a[88:91], v[164:167]\n"
"ds_read_b64_tr_b16 v[118:119], v235 offset:51200\n"
"v_mov_b64 v[34:35], s[22:23]\n"
"v_mfma_f32_16x16x32_bf16 v[164:167], v[192:195], a[92:95], v[164:167]\n"
"ds_read_b64_tr_b16 v[116:117], v235 offset:49152\n"
"v_xor_b32 v237, s61, v237\n"
"v_mfma_f32_16x16x32_bf16 v[164:167], v[208:211], a[32:35], v[164:167]\n"
"v_cvt_pk_bf16_f32 v158, v160, v161\n"
"v_subrev_f32 v176, s22, v176\n"
"v_mfma_f32_16x16x32_bf16 v[164:167], v[212:215], a[36:39], v[164:167]\n"
"v_mov_b64 v[54:55], s[22:23]\n"
"v_cvt_pk_bf16_f32 v159, v162, v163\n"
"v_mfma_f32_16x16x32_bf16 v[164:167], v[216:219], a[40:43], v[164:167]\n"
"ds_read_b64_tr_b16 v[224:225], v235 offset:57344\n"
"v_add_u32 v252, s98, v254\n"
"v_mfma_f32_16x16x32_bf16 v[164:167], v[220:223], a[44:47], v[164:167]\n"
"s_barrier\n"
"s_waitcnt lgkmcnt(4)\n"
"v_mfma_f32_16x16x32_bf16 v[136:139], v[96:99], a[48:51], v[136:139]\n"
"s_waitcnt lgkmcnt(3)\n"
"ds_read_b64_tr_b16 v[184:185], v232 offset:0\n"
"v_cvt_pk_bf16_f32 v173, v174, v175\n"
"v_mfma_f32_16x16x32_bf16 v[136:139], v[100:103], a[52:55], v[136:139]\n"
"ds_read_b64_tr_b16 v[212:213], v232 offset:16384\n"
"v_lshlrev_b32 v237, 2, v237\n"
"v_mfma_f32_16x16x32_bf16 v[136:139], v[104:107], a[56:59], v[136:139]\n"
"ds_read_b64_tr_b16 v[194:195], v232 offset:10240\n"
"v_mov_b32 v241, s60\n"
"v_mfma_f32_16x16x32_bf16 v[136:139], v[108:111], a[60:63], v[136:139]\n"
"ds_read_b64_tr_b16 v[188:189], v228 offset:8192\n"
"v_mov_b64 v[4:5], s[22:23]\n"
"v_mfma_f32_16x16x32_bf16 v[136:139], v[112:115], a[0:3], v[136:139]\n"
"s_waitcnt lgkmcnt(5)\n"
"ds_read_b64_tr_b16 v[218:219], v228 offset:26624\n"
"v_subrev_f32 v133, s22, v133\n"
"v_mfma_f32_16x16x32_bf16 v[136:139], v[116:119], a[4:7], v[136:139]\n"
"ds_read_b64_tr_b16 v[192:193], v232 offset:8192\n"
"v_mov_b64 v[88:89], 0\n"
"v_mfma_f32_16x16x32_bf16 v[136:139], v[120:123], a[8:11], v[136:139]\n"
"s_waitcnt lgkmcnt(6)\n"
"ds_read_b64_tr_b16 v[214:215], v232 offset:18432\n"
"v_lshl_add_u32 v248, v242, 8, s93\n"
"v_mfma_f32_16x16x32_bf16 v[136:139], v[224:227], a[12:15], v[136:139]\n"
"buffer_store_dwordx4 v[156:159], v243, s[32:35], s17 offen sc0 sc1\n"
"ds_read_b64_tr_b16 v[210:211], v228 offset:18432\n"
"v_mfma_f32_16x16x32_bf16 v[152:155], v[224:227], a[28:31], v[152:155]\n"
"ds_read_b64_tr_b16 v[208:209], v228 offset:16384\n"
"v_mov_b64 v[32:33], s[22:23]\n"
"v_mfma_f32_16x16x32_bf16 v[152:155], v[120:123], a[24:27], v[152:155]\n"
"ds_read_b64_tr_b16 v[222:223], v232 offset:26624\n"
"v_subrev_f32 v179, s22, v179\n"
"v_mfma_f32_16x16x32_bf16 v[152:155], v[116:119], a[20:23], v[152:155]\n"
"ds_read_b64_tr_b16 v[220:221], v232 offset:24576\n"
"v_cvt_pk_bf16_f32 v174, v176, v177\n"
"v_mfma_f32_16x16x32_bf16 v[152:155], v[112:115], a[16:19], v[152:155]\n"
"ds_read_b64_tr_b16 v[216:217], v228 offset:24576\n"
"v_subrev_f32 v134, s22, v134\n"
"v_mfma_f32_16x16x32_bf16 v[152:155], v[108:111], a[76:79], v[152:155]\n"
"ds_read_b64_tr_b16 v[186:187], v232 offset:2048\n"
"v_mfma_f32_16x16x32_bf16 v[152:155], v[104:107], a[72:75], v[152:155]\n"
"ds_read_b64_tr_b16 v[182:183], v228 offset:2048\n"
"v_mov_b64 v[46:47], s[22:23]\n"
"v_mfma_f32_16x16x32_bf16 v[152:155], v[100:103], a[68:71], v[152:155]\n"
"ds_read_b64_tr_b16 v[180:181], v228 offset:0\n"
"v_mov_b64 v[38:39], s[22:23]\n"
"v_mfma_f32_16x16x32_bf16 v[152:155], v[96:99], a[64:67], v[152:155]\n"
"ds_read_b64_tr_b16 v[190:191], v228 offset:10240\n"
"v_add_u32 v241, 1, v241\n"
"v_mfma_f32_16x16x32_bf16 v[168:171], v[96:99], a[80:83], v[168:171]\n"
"ds_read_b64_tr_b16 v[96:97], v229 offset:0\n"
"ds_read_b64_tr_b16 v[98:99], v229 offset:2048\n"
"v_mfma_f32_16x16x32_bf16 v[168:171], v[100:103], a[84:87], v[168:171]\n"
"ds_read_b64_tr_b16 v[100:101], v233 offset:0\n"
"ds_read_b64_tr_b16 v[102:103], v233 offset:2048\n"
"v_mfma_f32_16x16x32_bf16 v[168:171], v[104:107], a[88:91], v[168:171]\n"
"ds_read_b64_tr_b16 v[104:105], v229 offset:8192\n"
"v_cvt_pk_bf16_f32 v175, v178, v179\n"
"v_mfma_f32_16x16x32_bf16 v[168:171], v[108:111], a[92:95], v[168:171]\n"
"v_add_u32 v253, s99, v254\n"
"v_mov_b64 v[26:27], s[22:23]\n"
"v_mfma_f32_16x16x32_bf16 v[168:171], v[112:115], a[32:35], v[168:171]\n"
"v_xor_b32 v239, 64, v238\n"
"v_mov_b64 v[84:85], 0\n"
"v_mfma_f32_16x16x32_bf16 v[168:171], v[116:119], a[36:39], v[168:171]\n"
"v_subrev_f32 v132, s22, v132\n"
"v_cvt_pk_bf16_f32 v132, v132, v133\n"
"v_mfma_f32_16x16x32_bf16 v[168:171], v[120:123], a[40:43], v[168:171]\n"
"s_add_u32 s47, s47, 1\n"
"v_mfma_f32_16x16x32_bf16 v[168:171], v[224:227], a[44:47], v[168:171]\n"
"s_waitcnt lgkmcnt(5)\n"
"LGL_%=:\n"
"v_mfma_f32_16x16x32_bf16 v[124:127], v[180:183], a[96:99], v[196:199]\n"
"ds_read_b64_tr_b16 v[106:107], v229 offset:10240\n"
"v_cvt_pk_bf16_f32 v133, v134, v135\n"
"v_mfma_f32_16x16x32_bf16 v[124:127], v[184:187], a[100:103], v[124:127]\n"
"ds_read_b64_tr_b16 v[108:109], v233 offset:8192\n"
"v_subrev_f32 v136, s22, v136\n"
"v_mfma_f32_16x16x32_bf16 v[124:127], v[188:191], a[104:107], v[124:127]\n"
"ds_read_b64_tr_b16 v[110:111], v233 offset:10240\n"
"v_subrev_f32 v137, s22, v137\n"
"v_mfma_f32_16x16x32_bf16 v[124:127], v[192:195], a[108:111], v[124:127]\n"
"ds_read_b64_tr_b16 v[112:113], v229 offset:16384\n"
"v_subrev_f32 v138, s22, v138\n"
"v_mfma_f32_16x16x32_bf16 v[124:127], v[208:211], a[144:147], v[124:127]\n"
"ds_read_b64_tr_b16 v[114:115], v229 offset:18432\n"
"v_subrev_f32 v139, s22, v139\n"
"v_mfma_f32_16x16x32_bf16 v[124:127], v[212:215], a[148:151], v[124:127]\n"
"s_waitcnt lgkmcnt(7)\n"
"ds_read_b64_tr_b16 v[116:117], v233 offset:16384\n"
"v_cvt_pk_bf16_f32 v134, v136, v137\n"
"v_mfma_f32_16x16x32_bf16 v[124:127], v[216:219], a[152:155], v[124:127]\n"
"ds_read_b64_tr_b16 v[118:119], v233 offset:18432\n"
"v_cvt_pk_bf16_f32 v135, v138, v139\n"
"v_mfma_f32_16x16x32_bf16 v[124:127], v[220:223], a[156:159], v[124:127]\n"
"buffer_store_dwordx4 v[172:175], v243, s[32:35], s17 offen offset:2048 sc0 sc1\n"
"v_subrev_f32 v148, s22, v148\n"
"v_mfma_f32_16x16x32_bf16 v[140:143], v[220:223], a[172:175], v[196:199]\n"
"ds_read_b64_tr_b16 v[120:121], v229 offset:24576\n"
"v_subrev_f32 v149, s22, v149\n"
"v_mfma_f32_16x16x32_bf16 v[140:143], v[216:219], a[168:171], v[140:143]\n"
"ds_read_b64_tr_b16 v[122:123], v229 offset:26624\n"
"v_subrev_f32 v150, s22, v150\n"
"v_mfma_f32_16x16x32_bf16 v[140:143], v[212:215], a[164:167], v[140:143]\n"
"ds_read_b64_tr_b16 v[224:225], v233 offset:24576\n"
"v_subrev_f32 v151, s22, v151\n"
"v_mfma_f32_16x16x32_bf16 v[140:143], v[208:211], a[160:163], v[140:143]\n"
"ds_read_b64_tr_b16 v[226:227], v233 offset:26624\n"
"v_cvt_pk_bf16_f32 v148, v148, v149\n"
"v_mfma_f32_16x16x32_bf16 v[140:143], v[192:195], a[124:127], v[140:143]\n"
"v_cvt_pk_bf16_f32 v149, v150, v151\n"
"v_subrev_f32 v152, s22, v152\n"
"v_mfma_f32_16x16x32_bf16 v[140:143], v[188:191], a[120:123], v[140:143]\n"
"s_add_u32 s41, s70, 0\n"
"v_subrev_f32 v153, s22, v153\n"
"v_subrev_f32 v154, s22, v154\n"
"v_mfma_f32_16x16x32_bf16 v[140:143], v[184:187], a[116:119], v[140:143]\n"
"s_mov_b32 m0, s71\n"
"v_subrev_f32 v155, s22, v155\n"
"v_cvt_pk_bf16_f32 v150, v152, v153\n"
"v_mfma_f32_16x16x32_bf16 v[140:143], v[180:183], a[112:115], v[140:143]\n"
"s_waitcnt vmcnt(14)\n"
"buffer_load_dwordx4 v240, s[28:31], s41 offen lds\n"
"v_mfma_f32_16x16x32_bf16 v[156:159], v[180:183], a[128:131], v[196:199]\n"
"v_xor_b32 v230, s75, v230\n"
"v_cvt_pk_bf16_f32 v151, v154, v155\n"
"v_mfma_f32_16x16x32_bf16 v[156:159], v[184:187], a[132:135], v[156:159]\n"
"v_xor_b32 v234, s75, v234\n"
"v_subrev_f32 v164, s22, v164\n"
"v_mfma_f32_16x16x32_bf16 v[156:159], v[188:191], a[136:139], v[156:159]\n"
"v_subrev_f32 v165, s22, v165\n"
"v_subrev_f32 v166, s22, v166\n"
"v_mfma_f32_16x16x32_bf16 v[156:159], v[192:195], a[140:143], v[156:159]\n"
"v_subrev_f32 v167, s22, v167\n"
"v_cvt_pk_bf16_f32 v164, v164, v165\n"
"v_mfma_f32_16x16x32_bf16 v[156:159], v[208:211], a[176:179], v[156:159]\n"
"v_cvt_pk_bf16_f32 v165, v166, v167\n"
"v_subrev_f32 v168, s22, v168\n"
"v_mfma_f32_16x16x32_bf16 v[156:159], v[212:215], a[180:183], v[156:159]\n"
"v_subrev_f32 v169, s22, v169\n"
"v_subrev_f32 v170, s22, v170\n"
"v_mfma_f32_16x16x32_bf16 v[156:159], v[216:219], a[184:187], v[156:159]\n"
"v_subrev_f32 v171, s22, v171\n"
"v_cvt_pk_bf16_f32 v166, v168, v169\n"
"v_mfma_f32_16x16x32_bf16 v[156:159], v[220:223], a[188:191], v[156:159]\n"
"buffer_store_dwordx4 v[132:135], v243, s[32:35], s16 offen offset:1024 sc0 sc1\n"
"v_cvt_pk_bf16_f32 v167, v170, v171\n"
"v_mfma_f32_16x16x32_bf16 v[172:175], v[220:223], a[252:255], v[196:199]\n"
"ds_read_b64_tr_b16 v[220:221], v234 offset:24576\n"
"v_xor_b32 v231, s75, v231\n"
"v_mfma_f32_16x16x32_bf16 v[172:175], v[216:219], a[248:251], v[172:175]\n"
"ds_read_b64_tr_b16 v[216:217], v230 offset:24576\n"
"v_xor_b32 v235, s75, v235\n"
"v_mfma_f32_16x16x32_bf16 v[172:175], v[212:215], a[244:247], v[172:175]\n"
"ds_read_b64_tr_b16 v[212:213], v234 offset:16384\n"
"v_lshlrev_b32 v202, 16, v84\n"
"v_mfma_f32_16x16x32_bf16 v[172:175], v[208:211], a[240:243], v[172:175]\n"
"ds_read_b64_tr_b16 v[208:209], v230 offset:16384\n"
"v_and_b32 v84, s73, v84\n"
"v_mfma_f32_16x16x32_bf16 v[172:175], v[192:195], a[204:207], v[172:175]\n"
"ds_read_b64_tr_b16 v[192:193], v234 offset:8192\n"
"v_add_f32 v25, v25, v84\n"
"v_mfma_f32_16x16x32_bf16 v[172:175], v[188:191], a[200:203], v[172:175]\n"
"ds_read_b64_tr_b16 v[188:189], v230 offset:8192\n"
"v_lshlrev_b32 v203, 16, v85\n"
"v_mfma_f32_16x16x32_bf16 v[172:175], v[184:187], a[196:199], v[172:175]\n"
"s_add_u32 s40, s41, s52\n"
"ds_read_b64_tr_b16 v[184:185], v234 offset:0\n"
"v_and_b32 v85, s73, v85\n"
"v_mfma_f32_16x16x32_bf16 v[172:175], v[180:183], a[192:195], v[172:175]\n"
"s_waitcnt vmcnt(13) lgkmcnt(7)\n"
"buffer_load_dwordx4 v240, s[28:31], s40 offen offset:1024 lds\n"
"v_mfma_f32_16x16x32_bf16 v[128:131], v[96:99], a[96:99], v[196:199]\n"
"s_waitcnt lgkmcnt(6)\n"
"ds_read_b64_tr_b16 v[180:181], v230 offset:0\n"
"v_add_f32 v27, v27, v85\n"
"v_mfma_f32_16x16x32_bf16 v[128:131], v[100:103], a[100:103], v[128:131]\n"
"ds_read_b64_tr_b16 v[182:183], v230 offset:2048\n"
"v_lshlrev_b32 v204, 16, v86\n"
"v_mfma_f32_16x16x32_bf16 v[128:131], v[104:107], a[104:107], v[128:131]\n"
"ds_read_b64_tr_b16 v[186:187], v234 offset:2048\n"
"v_and_b32 v86, s73, v86\n"
"v_mfma_f32_16x16x32_bf16 v[128:131], v[108:111], a[108:111], v[128:131]\n"
"ds_read_b64_tr_b16 v[190:191], v230 offset:10240\n"
"v_add_f32 v29, v29, v86\n"
"v_mfma_f32_16x16x32_bf16 v[128:131], v[112:115], a[144:147], v[128:131]\n"
"s_waitcnt vmcnt(6)\n"
"ds_read_b64_tr_b16 v[194:195], v234 offset:10240\n"
"v_lshlrev_b32 v205, 16, v87\n"
"v_mfma_f32_16x16x32_bf16 v[128:131], v[116:119], a[148:151], v[128:131]\n"
"v_subrev_u32 v200, s21, v200\n"
"ds_read_b64_tr_b16 v[210:211], v230 offset:18432\n"
"v_mfma_f32_16x16x32_bf16 v[128:131], v[120:123], a[152:155], v[128:131]\n"
"v_cmp_lt_u32 vcc, s69, v200\n"
"ds_read_b64_tr_b16 v[214:215], v234 offset:18432\n"
"v_mfma_f32_16x16x32_bf16 v[128:131], v[224:227], a[156:159], v[128:131]\n"
"ds_read_b64_tr_b16 v[218:219], v230 offset:26624\n"
"s_cbranch_vccnz LGP_%=\nLGPK_%=:\n"
"v_mfma_f32_16x16x32_bf16 v[144:147], v[224:227], a[172:175], v[196:199]\n"
"s_sub_u32 s72, s72, 1\n"
"ds_read_b64_tr_b16 v[222:223], v234 offset:26624\n"
"v_and_b32 v87, s73, v87\n"
"v_mfma_f32_16x16x32_bf16 v[144:147], v[120:123], a[168:171], v[144:147]\n"
"s_and_b32 s72, s72, 7\n"
"v_add_f32 v24, v24, v202\n"
"v_add_f32 v31, v31, v87\n"
"v_mfma_f32_16x16x32_bf16 v[144:147], v[116:119], a[164:167], v[144:147]\n"
"v_add_f32 v26, v26, v203\n"
"v_lshlrev_b32 v206, 16, v88\n"
"v_mfma_f32_16x16x32_bf16 v[144:147], v[112:115], a[160:163], v[144:147]\n"
"buffer_load_dwordx4 v[72:75], v243, s[32:35], s18 offen sc1\n"
"v_mfma_f32_16x16x32_bf16 v[144:147], v[108:111], a[124:127], v[144:147]\n"
"v_and_b32 v88, s73, v88\n"
"v_add_f32 v33, v33, v88\n"
"v_mfma_f32_16x16x32_bf16 v[144:147], v[104:107], a[120:123], v[144:147]\n"
"v_lshlrev_b32 v207, 16, v89\n"
"v_and_b32 v89, s73, v89\n"
"v_mfma_f32_16x16x32_bf16 v[144:147], v[100:103], a[116:119], v[144:147]\n"
"v_add_f32 v35, v35, v89\n"
"v_lshlrev_b32 v202, 16, v90\n"
"v_mfma_f32_16x16x32_bf16 v[144:147], v[96:99], a[112:115], v[144:147]\n"
"buffer_store_dwordx4 v[148:151], v243, s[32:35], s16 offen offset:3072 sc0 sc1\n"
"v_and_b32 v90, s73, v90\n"
"v_mfma_f32_16x16x32_bf16 v[160:163], v[96:99], a[128:131], v[196:199]\n"
"v_add_f32 v37, v37, v90\n"
"v_lshlrev_b32 v203, 16, v91\n"
"v_mfma_f32_16x16x32_bf16 v[160:163], v[100:103], a[132:135], v[160:163]\n"
"v_and_b32 v91, s73, v91\n"
"v_add_f32 v39, v39, v91\n"
"v_mfma_f32_16x16x32_bf16 v[160:163], v[104:107], a[136:139], v[160:163]\n"
"v_add_f32 v28, v28, v204\n"
"v_add_f32 v30, v30, v205\n"
"v_mfma_f32_16x16x32_bf16 v[160:163], v[108:111], a[140:143], v[160:163]\n"
"v_add_f32 v32, v32, v206\n"
"v_add_f32 v34, v34, v207\n"
"v_mfma_f32_16x16x32_bf16 v[160:163], v[112:115], a[176:179], v[160:163]\n"
"v_lshlrev_b32 v204, 16, v92\n"
"v_and_b32 v92, s73, v92\n"
"v_mfma_f32_16x16x32_bf16 v[160:163], v[116:119], a[180:183], v[160:163]\n"
"v_add_f32 v41, v41, v92\n"
"v_lshlrev_b32 v205, 16, v93\n"
"v_mfma_f32_16x16x32_bf16 v[160:163], v[120:123], a[184:187], v[160:163]\n"
"s_add_u32 s40, s41, s53\n"
"v_and_b32 v93, s73, v93\n"
"v_add_f32 v43, v43, v93\n"
"v_mfma_f32_16x16x32_bf16 v[160:163], v[224:227], a[188:191], v[160:163]\n"
"buffer_load_dwordx4 v240, s[28:31], s40 offen offset:2048 lds\n"
"v_mfma_f32_16x16x32_bf16 v[176:179], v[224:227], a[252:255], v[196:199]\n"
"ds_read_b64_tr_b16 v[224:225], v235 offset:24576\n"
"v_lshlrev_b32 v206, 16, v94\n"
"v_mfma_f32_16x16x32_bf16 v[176:179], v[120:123], a[248:251], v[176:179]\n"
"ds_read_b64_tr_b16 v[120:121], v231 offset:24576\n"
"v_and_b32 v94, s73, v94\n"
"v_mfma_f32_16x16x32_bf16 v[176:179], v[116:119], a[244:247], v[176:179]\n"
"ds_read_b64_tr_b16 v[116:117], v235 offset:16384\n"
"v_add_f32 v45, v45, v94\n"
"v_mfma_f32_16x16x32_bf16 v[176:179], v[112:115], a[240:243], v[176:179]\n"
"buffer_load_dwordx4 v[76:79], v243, s[32:35], s18 offen offset:1024 sc1\n"
"v_mfma_f32_16x16x32_bf16 v[176:179], v[108:111], a[204:207], v[176:179]\n"
"ds_read_b64_tr_b16 v[108:109], v235 offset:8192\n"
"v_lshlrev_b32 v207, 16, v95\n"
"v_mfma_f32_16x16x32_bf16 v[176:179], v[104:107], a[200:203], v[176:179]\n"
"ds_read_b64_tr_b16 v[104:105], v231 offset:8192\n"
"v_and_b32 v95, s73, v95\n"
"v_mfma_f32_16x16x32_bf16 v[176:179], v[100:103], a[196:199], v[176:179]\n"
"s_add_u32 s40, s41, s54\n"
"ds_read_b64_tr_b16 v[100:101], v235 offset:0\n"
"v_add_f32 v47, v47, v95\n"
"v_mfma_f32_16x16x32_bf16 v[176:179], v[96:99], a[192:195], v[176:179]\n"
"s_waitcnt lgkmcnt(6)\n"
"buffer_load_dwordx4 v240, s[28:31], s40 offen offset:3072 lds\n"
"v_mfma_f32_16x16x32_bf16 v[132:135], v[180:183], a[96:99], v[196:199]\n"
"ds_read_b64_tr_b16 v[96:97], v231 offset:0\n"
"v_add_f32 v36, v36, v202\n"
"v_mfma_f32_16x16x32_bf16 v[132:135], v[184:187], a[100:103], v[132:135]\n"
"ds_read_b64_tr_b16 v[98:99], v231 offset:2048\n"
"v_add_f32 v38, v38, v203\n"
"v_mfma_f32_16x16x32_bf16 v[132:135], v[188:191], a[104:107], v[132:135]\n"
"ds_read_b64_tr_b16 v[102:103], v235 offset:2048\n"
"v_add_f32 v44, v44, v206\n"
"v_mfma_f32_16x16x32_bf16 v[132:135], v[192:195], a[108:111], v[132:135]\n"
"ds_read_b64_tr_b16 v[106:107], v231 offset:10240\n"
"v_add_f32 v46, v46, v207\n"
"v_mfma_f32_16x16x32_bf16 v[132:135], v[208:211], a[144:147], v[132:135]\n"
"ds_read_b64_tr_b16 v[110:111], v235 offset:10240\n"
"v_add_f32 v40, v40, v204\n"
"v_mfma_f32_16x16x32_bf16 v[132:135], v[212:215], a[148:151], v[132:135]\n"
"ds_read_b64_tr_b16 v[112:113], v231 offset:16384\n"
"v_add_f32 v42, v42, v205\n"
"v_mfma_f32_16x16x32_bf16 v[132:135], v[216:219], a[152:155], v[132:135]\n"
"ds_read_b64_tr_b16 v[114:115], v231 offset:18432\n"
"v_mfma_f32_16x16x32_bf16 v[132:135], v[220:223], a[156:159], v[132:135]\n"
"buffer_store_dwordx4 v[164:167], v243, s[32:35], s17 offen offset:1024 sc0 sc1\n"
"v_lshlrev_b32 v206, 16, v244\n"
"v_mfma_f32_16x16x32_bf16 v[148:151], v[220:223], a[172:175], v[196:199]\n"
"ds_read_b64_tr_b16 v[118:119], v235 offset:18432\n"
"v_and_b32 v244, s73, v244\n"
"v_mfma_f32_16x16x32_bf16 v[148:151], v[216:219], a[168:171], v[148:151]\n"
"ds_read_b64_tr_b16 v[122:123], v231 offset:26624\n"
"v_add_f32 v49, v49, v244\n"
"v_mfma_f32_16x16x32_bf16 v[148:151], v[212:215], a[164:167], v[148:151]\n"
"ds_read_b64_tr_b16 v[226:227], v235 offset:26624\n"
"v_lshlrev_b32 v207, 16, v245\n"
"v_mfma_f32_16x16x32_bf16 v[148:151], v[208:211], a[160:163], v[148:151]\n"
"buffer_load_dwordx4 v[80:83], v243, s[32:35], s18 offen offset:2048 sc1\n"
"v_mfma_f32_16x16x32_bf16 v[148:151], v[192:195], a[124:127], v[148:151]\n"
"v_and_b32 v245, s73, v245\n"
"v_add_f32 v51, v51, v245\n"
"v_mfma_f32_16x16x32_bf16 v[148:151], v[188:191], a[120:123], v[148:151]\n"
"s_add_u32 s41, s70, 163840\n"
"v_lshlrev_b32 v202, 16, v246\n"
"v_and_b32 v246, s73, v246\n"
"v_mfma_f32_16x16x32_bf16 v[148:151], v[184:187], a[116:119], v[148:151]\n"
"s_add_u32 m0, s71, 16384\n"
"v_add_f32 v53, v53, v246\n"
"v_lshlrev_b32 v203, 16, v247\n"
"v_mfma_f32_16x16x32_bf16 v[148:151], v[180:183], a[112:115], v[148:151]\n"
"buffer_load_dwordx4 v240, s[28:31], s41 offen lds\n"
"v_mfma_f32_16x16x32_bf16 v[164:167], v[180:183], a[128:131], v[196:199]\n"
"v_and_b32 v247, s73, v247\n"
"v_add_f32 v55, v55, v247\n"
"v_mfma_f32_16x16x32_bf16 v[164:167], v[184:187], a[132:135], v[164:167]\n"
"v_add_f32 v48, v48, v206\n"
"v_add_f32 v50, v50, v207\n"
"v_mfma_f32_16x16x32_bf16 v[164:167], v[188:191], a[136:139], v[164:167]\n"
"v_add_f32 v52, v52, v202\n"
"v_add_f32 v54, v54, v203\n"
"v_mfma_f32_16x16x32_bf16 v[164:167], v[192:195], a[140:143], v[164:167]\n"
"v_mfma_f32_16x16x32_bf16 v[164:167], v[208:211], a[176:179], v[164:167]\n"
"v_mfma_f32_16x16x32_bf16 v[164:167], v[212:215], a[180:183], v[164:167]\n"
"v_mfma_f32_16x16x32_bf16 v[164:167], v[216:219], a[184:187], v[164:167]\n"
"s_waitcnt vmcnt(12)\n"
"v_mfma_f32_16x16x32_bf16 v[164:167], v[220:223], a[188:191], v[164:167]\n"
"s_barrier\n"
"s_waitcnt lgkmcnt(0)\n"
"v_mfma_f32_16x16x32_bf16 v[136:139], v[96:99], a[96:99], v[196:199]\n"
"ds_read_b64_tr_b16 v[180:181], v228 offset:32768\n"
"v_mfma_f32_16x16x32_bf16 v[136:139], v[100:103], a[100:103], v[136:139]\n"
"ds_read_b64_tr_b16 v[182:183], v228 offset:34816\n"
"v_mfma_f32_16x16x32_bf16 v[136:139], v[104:107], a[104:107], v[136:139]\n"
"ds_read_b64_tr_b16 v[184:185], v232 offset:32768\n"
"v_mfma_f32_16x16x32_bf16 v[136:139], v[108:111], a[108:111], v[136:139]\n"
"ds_read_b64_tr_b16 v[186:187], v232 offset:34816\n"
"v_mfma_f32_16x16x32_bf16 v[136:139], v[112:115], a[144:147], v[136:139]\n"
"ds_read_b64_tr_b16 v[188:189], v228 offset:40960\n"
"v_mfma_f32_16x16x32_bf16 v[136:139], v[116:119], a[148:151], v[136:139]\n"
"ds_read_b64_tr_b16 v[190:191], v228 offset:43008\n"
"v_mfma_f32_16x16x32_bf16 v[136:139], v[120:123], a[152:155], v[136:139]\n"
"s_add_u32 s40, s41, s52\n"
"ds_read_b64_tr_b16 v[192:193], v232 offset:40960\n"
"v_mfma_f32_16x16x32_bf16 v[136:139], v[224:227], a[156:159], v[136:139]\n"
"buffer_load_dwordx4 v240, s[28:31], s40 offen offset:1024 lds\n"
"v_mfma_f32_16x16x32_bf16 v[152:155], v[224:227], a[172:175], v[196:199]\n"
"ds_read_b64_tr_b16 v[194:195], v232 offset:43008\n"
"v_mfma_f32_16x16x32_bf16 v[152:155], v[120:123], a[168:171], v[152:155]\n"
"ds_read_b64_tr_b16 v[208:209], v228 offset:49152\n"
"v_mfma_f32_16x16x32_bf16 v[152:155], v[116:119], a[164:167], v[152:155]\n"
"ds_read_b64_tr_b16 v[210:211], v228 offset:51200\n"
"v_mfma_f32_16x16x32_bf16 v[152:155], v[112:115], a[160:163], v[152:155]\n"
"ds_read_b64_tr_b16 v[212:213], v232 offset:49152\n"
"v_mfma_f32_16x16x32_bf16 v[152:155], v[108:111], a[124:127], v[152:155]\n"
"ds_read_b64_tr_b16 v[214:215], v232 offset:51200\n"
"v_mfma_f32_16x16x32_bf16 v[152:155], v[104:107], a[120:123], v[152:155]\n"
"ds_read_b64_tr_b16 v[216:217], v228 offset:57344\n"
"v_mfma_f32_16x16x32_bf16 v[152:155], v[100:103], a[116:119], v[152:155]\n"
"s_add_u32 s40, s41, s53\n"
"ds_read_b64_tr_b16 v[218:219], v228 offset:59392\n"
"v_xor_b32 v228, s75, v228\n"
"v_mfma_f32_16x16x32_bf16 v[152:155], v[96:99], a[112:115], v[152:155]\n"
"buffer_load_dwordx4 v240, s[28:31], s40 offen offset:2048 lds\n"
"v_mfma_f32_16x16x32_bf16 v[168:171], v[96:99], a[128:131], v[196:199]\n"
"ds_read_b64_tr_b16 v[220:221], v232 offset:57344\n"
"v_mfma_f32_16x16x32_bf16 v[168:171], v[100:103], a[132:135], v[168:171]\n"
"ds_read_b64_tr_b16 v[222:223], v232 offset:59392\n"
"v_xor_b32 v232, s75, v232\n"
"v_mfma_f32_16x16x32_bf16 v[168:171], v[104:107], a[136:139], v[168:171]\n"
"ds_read_b64_tr_b16 v[96:97], v229 offset:32768\n"
"v_mfma_f32_16x16x32_bf16 v[168:171], v[108:111], a[140:143], v[168:171]\n"
"ds_read_b64_tr_b16 v[98:99], v229 offset:34816\n"
"v_mfma_f32_16x16x32_bf16 v[168:171], v[112:115], a[176:179], v[168:171]\n"
"ds_read_b64_tr_b16 v[100:101], v233 offset:32768\n"
"v_mfma_f32_16x16x32_bf16 v[168:171], v[116:119], a[180:183], v[168:171]\n"
"ds_read_b64_tr_b16 v[102:103], v233 offset:34816\n"
"v_mfma_f32_16x16x32_bf16 v[168:171], v[120:123], a[184:187], v[168:171]\n"
"s_add_u32 s40, s41, s54\n"
"ds_read_b64_tr_b16 v[104:105], v229 offset:40960\n"
"v_mfma_f32_16x16x32_bf16 v[168:171], v[224:227], a[188:191], v[168:171]\n"
"s_waitcnt lgkmcnt(7)\n"
"buffer_load_dwordx4 v240, s[28:31], s40 offen offset:3072 lds\n"
"v_mfma_f32_16x16x32_bf16 v[124:127], v[180:183], a[48:51], v[124:127]\n"
"ds_read_b64_tr_b16 v[106:107], v229 offset:43008\n"
"v_mfma_f32_16x16x32_bf16 v[124:127], v[184:187], a[52:55], v[124:127]\n"
"ds_read_b64_tr_b16 v[108:109], v233 offset:40960\n"
"v_mfma_f32_16x16x32_bf16 v[124:127], v[188:191], a[56:59], v[124:127]\n"
"ds_read_b64_tr_b16 v[110:111], v233 offset:43008\n"
"v_mfma_f32_16x16x32_bf16 v[124:127], v[192:195], a[60:63], v[124:127]\n"
"ds_read_b64_tr_b16 v[112:113], v229 offset:49152\n"
"v_mfma_f32_16x16x32_bf16 v[124:127], v[208:211], a[0:3], v[124:127]\n"
"ds_read_b64_tr_b16 v[114:115], v229 offset:51200\n"
"v_mfma_f32_16x16x32_bf16 v[124:127], v[212:215], a[4:7], v[124:127]\n"
"s_add_u32 s41, s70, 327680\n"
"ds_read_b64_tr_b16 v[116:117], v233 offset:49152\n"
"v_mfma_f32_16x16x32_bf16 v[124:127], v[216:219], a[8:11], v[124:127]\n"
"s_waitcnt lgkmcnt(6)\n"
"s_add_u32 m0, s71, 32768\n"
"ds_read_b64_tr_b16 v[118:119], v233 offset:51200\n"
"v_mfma_f32_16x16x32_bf16 v[124:127], v[220:223], a[12:15], v[124:127]\n"
"buffer_load_dwordx4 v240, s[28:31], s41 offen lds\n"
"v_mfma_f32_16x16x32_bf16 v[140:143], v[220:223], a[28:31], v[140:143]\n"
"ds_read_b64_tr_b16 v[120:121], v229 offset:57344\n"
"v_mfma_f32_16x16x32_bf16 v[140:143], v[216:219], a[24:27], v[140:143]\n"
"ds_read_b64_tr_b16 v[122:123], v229 offset:59392\n"
"v_xor_b32 v229, s75, v229\n"
"v_mfma_f32_16x16x32_bf16 v[140:143], v[212:215], a[20:23], v[140:143]\n"
"ds_read_b64_tr_b16 v[224:225], v233 offset:57344\n"
"v_mfma_f32_16x16x32_bf16 v[140:143], v[208:211], a[16:19], v[140:143]\n"
"ds_read_b64_tr_b16 v[226:227], v233 offset:59392\n"
"v_xor_b32 v233, s75, v233\n"
"v_mfma_f32_16x16x32_bf16 v[140:143], v[192:195], a[76:79], v[140:143]\n"
"v_mfma_f32_16x16x32_bf16 v[140:143], v[188:191], a[72:75], v[140:143]\n"
"v_subrev_f32 v124, s22, v124\n"
"v_subrev_f32 v125, s22, v125\n"
"v_mfma_f32_16x16x32_bf16 v[140:143], v[184:187], a[68:71], v[140:143]\n"
"s_add_u32 s40, s41, s52\n"
"v_subrev_f32 v126, s22, v126\n"
"v_subrev_f32 v127, s22, v127\n"
"v_mfma_f32_16x16x32_bf16 v[140:143], v[180:183], a[64:67], v[140:143]\n"
"buffer_load_dwordx4 v240, s[28:31], s40 offen offset:1024 lds\n"
"v_mfma_f32_16x16x32_bf16 v[156:159], v[180:183], a[80:83], v[156:159]\n"
"v_cvt_pk_bf16_f32 v124, v124, v125\n"
"v_cvt_pk_bf16_f32 v125, v126, v127\n"
"v_mfma_f32_16x16x32_bf16 v[156:159], v[184:187], a[84:87], v[156:159]\n"
"v_mfma_f32_16x16x32_bf16 v[156:159], v[188:191], a[88:91], v[156:159]\n"
"v_mfma_f32_16x16x32_bf16 v[156:159], v[192:195], a[92:95], v[156:159]\n"
"v_mfma_f32_16x16x32_bf16 v[156:159], v[208:211], a[32:35], v[156:159]\n"
"v_mfma_f32_16x16x32_bf16 v[156:159], v[212:215], a[36:39], v[156:159]\n"
"v_subrev_f32 v140, s22, v140\n"
"v_subrev_f32 v141, s22, v141\n"
"v_mfma_f32_16x16x32_bf16 v[156:159], v[216:219], a[40:43], v[156:159]\n"
"s_waitcnt vmcnt(7)\n"
"v_subrev_f32 v142, s22, v142\n"
"v_subrev_f32 v143, s22, v143\n"
"v_mfma_f32_16x16x32_bf16 v[156:159], v[220:223], a[44:47], v[156:159]\n"
"s_barrier\n"
"buffer_store_dword v241, v248, s[84:87], 0 offen sc1\n"
"v_mfma_f32_16x16x32_bf16 v[172:175], v[220:223], a[228:231], v[172:175]\n"
"ds_read_b64_tr_b16 v[220:221], v234 offset:57344\n"
"v_cvt_pk_bf16_f32 v140, v140, v141\n"
"v_mfma_f32_16x16x32_bf16 v[172:175], v[216:219], a[224:227], v[172:175]\n"
"ds_read_b64_tr_b16 v[216:217], v230 offset:57344\n"
"v_cvt_pk_bf16_f32 v141, v142, v143\n"
"v_mfma_f32_16x16x32_bf16 v[172:175], v[212:215], a[212:215], v[172:175]\n"
"ds_read_b64_tr_b16 v[212:213], v234 offset:49152\n"
"v_add_u32 v241, 1, v241\n"
"v_mfma_f32_16x16x32_bf16 v[172:175], v[208:211], a[208:211], v[172:175]\n"
"s_add_u32 s16, s90, s62\n"
"buffer_load_dwordx4 v[84:87], v243, s[32:35], s18 offen offset:3072 sc1\n"
"v_mfma_f32_16x16x32_bf16 v[172:175], v[192:195], a[236:239], v[172:175]\n"
"s_add_u32 s17, s16, 4096\n"
"ds_read_b64_tr_b16 v[192:193], v234 offset:40960\n"
"v_mfma_f32_16x16x32_bf16 v[172:175], v[188:191], a[232:235], v[172:175]\n"
"s_lshl_b32 s62, s70, 9\n"
"ds_read_b64_tr_b16 v[188:189], v230 offset:40960\n"
"v_subrev_f32 v156, s22, v156\n"
"v_mfma_f32_16x16x32_bf16 v[172:175], v[184:187], a[220:223], v[172:175]\n"
"s_add_u32 s40, s41, s53\n"
"ds_read_b64_tr_b16 v[184:185], v234 offset:32768\n"
"v_subrev_f32 v157, s22, v157\n"
"v_mfma_f32_16x16x32_bf16 v[172:175], v[180:183], a[216:219], v[172:175]\n"
"buffer_load_dwordx4 v240, s[28:31], s40 offen offset:2048 lds\n"
"v_mfma_f32_16x16x32_bf16 v[128:131], v[96:99], a[48:51], v[128:131]\n"
"ds_read_b64_tr_b16 v[180:181], v230 offset:32768\n"
"v_subrev_f32 v158, s22, v158\n"
"v_mfma_f32_16x16x32_bf16 v[128:131], v[100:103], a[52:55], v[128:131]\n"
"s_waitcnt lgkmcnt(5)\n"
"ds_read_b64_tr_b16 v[182:183], v230 offset:34816\n"
"v_subrev_f32 v159, s22, v159\n"
"v_mfma_f32_16x16x32_bf16 v[128:131], v[104:107], a[56:59], v[128:131]\n"
"ds_read_b64_tr_b16 v[186:187], v234 offset:34816\n"
"v_cvt_pk_bf16_f32 v156, v156, v157\n"
"v_mfma_f32_16x16x32_bf16 v[128:131], v[108:111], a[60:63], v[128:131]\n"
"ds_read_b64_tr_b16 v[190:191], v230 offset:43008\n"
"v_cvt_pk_bf16_f32 v157, v158, v159\n"
"v_mfma_f32_16x16x32_bf16 v[128:131], v[112:115], a[0:3], v[128:131]\n"
"ds_read_b64_tr_b16 v[194:195], v234 offset:43008\n"
"v_lshlrev_b32 v202, 16, v72\n"
"v_mfma_f32_16x16x32_bf16 v[128:131], v[116:119], a[4:7], v[128:131]\n"
"ds_read_b64_tr_b16 v[208:209], v230 offset:49152\n"
"v_and_b32 v72, s73, v72\n"
"v_mfma_f32_16x16x32_bf16 v[128:131], v[120:123], a[8:11], v[128:131]\n"
"s_add_u32 s40, s41, s54\n"
"ds_read_b64_tr_b16 v[210:211], v230 offset:51200\n"
"v_add_f32 v0, v0, v202\n"
"v_mfma_f32_16x16x32_bf16 v[128:131], v[224:227], a[12:15], v[128:131]\n"
"buffer_load_dwordx4 v240, s[28:31], s40 offen offset:3072 lds\n"
"v_mfma_f32_16x16x32_bf16 v[144:147], v[224:227], a[28:31], v[144:147]\n"
"ds_read_b64_tr_b16 v[214:215], v234 offset:51200\n"
"v_add_f32 v1, v1, v72\n"
"v_mfma_f32_16x16x32_bf16 v[144:147], v[120:123], a[24:27], v[144:147]\n"
"ds_read_b64_tr_b16 v[218:219], v230 offset:59392\n"
"v_lshlrev_b32 v203, 16, v73\n"
"v_mfma_f32_16x16x32_bf16 v[144:147], v[116:119], a[20:23], v[144:147]\n"
"ds_read_b64_tr_b16 v[222:223], v234 offset:59392\n"
"v_and_b32 v73, s73, v73\n"
"v_mfma_f32_16x16x32_bf16 v[144:147], v[112:115], a[16:19], v[144:147]\n"
"buffer_load_dwordx4 v[88:91], v243, s[32:35], s19 offen sc1\n"
"v_mfma_f32_16x16x32_bf16 v[144:147], v[108:111], a[76:79], v[144:147]\n"
"v_add_f32 v2, v2, v203\n"
"v_add_f32 v3, v3, v73\n"
"v_mfma_f32_16x16x32_bf16 v[144:147], v[104:107], a[72:75], v[144:147]\n"
"s_add_u32 s41, s70, 491520\n"
"v_subrev_f32 v128, s22, v128\n"
"v_subrev_f32 v129, s22, v129\n"
"v_mfma_f32_16x16x32_bf16 v[144:147], v[100:103], a[68:71], v[144:147]\n"
"s_add_u32 m0, s71, 49152\n"
"v_subrev_f32 v130, s22, v130\n"
"v_subrev_f32 v131, s22, v131\n"
"v_mfma_f32_16x16x32_bf16 v[144:147], v[96:99], a[64:67], v[144:147]\n"
"buffer_load_dwordx4 v240, s[28:31], s41 offen lds\n"
"v_mfma_f32_16x16x32_bf16 v[160:163], v[96:99], a[80:83], v[160:163]\n"
"s_and_b32 s70, s88, 15\n"
"v_cvt_pk_bf16_f32 v126, v128, v129\n"
"v_cvt_pk_bf16_f32 v127, v130, v131\n"
"v_mfma_f32_16x16x32_bf16 v[160:163], v[100:103], a[84:87], v[160:163]\n"
"s_lshr_b32 s88, s88, 4\n"
"v_lshlrev_b32 v204, 16, v74\n"
"v_and_b32 v74, s73, v74\n"
"v_mfma_f32_16x16x32_bf16 v[160:163], v[104:107], a[88:91], v[160:163]\n"
"s_lshl_b32 s70, s70, 7\n"
"v_add_f32 v4, v4, v204\n"
"v_add_f32 v5, v5, v74\n"
"v_mfma_f32_16x16x32_bf16 v[160:163], v[108:111], a[92:95], v[160:163]\n"
"s_xor_b32 s71, s71, s75\n"
"v_lshlrev_b32 v205, 16, v75\n"
"v_and_b32 v75, s73, v75\n"
"v_mfma_f32_16x16x32_bf16 v[160:163], v[112:115], a[32:35], v[160:163]\n"
"v_add_f32 v6, v6, v205\n"
"v_add_f32 v7, v7, v75\n"
"v_mfma_f32_16x16x32_bf16 v[160:163], v[116:119], a[36:39], v[160:163]\n"
"v_subrev_f32 v144, s22, v144\n"
"v_subrev_f32 v145, s22, v145\n"
"v_mfma_f32_16x16x32_bf16 v[160:163], v[120:123], a[40:43], v[160:163]\n"
"v_subrev_f32 v146, s22, v146\n"
"v_subrev_f32 v147, s22, v147\n"
"v_mfma_f32_16x16x32_bf16 v[160:163], v[224:227], a[44:47], v[160:163]\n"
"buffer_store_dwordx4 v[124:127], v243, s[32:35], s16 offen sc0 sc1\n"
"v_cvt_pk_bf16_f32 v142, v144, v145\n"
"v_mfma_f32_16x16x32_bf16 v[176:179], v[224:227], a[228:231], v[176:179]\n"
"ds_read_b64_tr_b16 v[224:225], v235 offset:57344\n"
"v_cvt_pk_bf16_f32 v143, v146, v147\n"
"v_mfma_f32_16x16x32_bf16 v[176:179], v[120:123], a[224:227], v[176:179]\n"
"ds_read_b64_tr_b16 v[120:121], v231 offset:57344\n"
"v_lshlrev_b32 v206, 16, v76\n"
"v_mfma_f32_16x16x32_bf16 v[176:179], v[116:119], a[212:215], v[176:179]\n"
"ds_read_b64_tr_b16 v[116:117], v235 offset:49152\n"
"v_and_b32 v76, s73, v76\n"
"v_mfma_f32_16x16x32_bf16 v[176:179], v[112:115], a[208:211], v[176:179]\n"
"buffer_load_dwordx4 v[92:95], v243, s[32:35], s19 offen offset:1024 sc1\n"
"v_mfma_f32_16x16x32_bf16 v[176:179], v[108:111], a[236:239], v[176:179]\n"
"ds_read_b64_tr_b16 v[108:109], v235 offset:40960\n"
"v_add_f32 v8, v8, v206\n"
"v_mfma_f32_16x16x32_bf16 v[176:179], v[104:107], a[232:235], v[176:179]\n"
"ds_read_b64_tr_b16 v[104:105], v231 offset:40960\n"
"v_subrev_f32 v160, s22, v160\n"
"v_mfma_f32_16x16x32_bf16 v[176:179], v[100:103], a[220:223], v[176:179]\n"
"s_add_u32 s40, s41, s52\n"
"ds_read_b64_tr_b16 v[100:101], v235 offset:32768\n"
"v_subrev_f32 v161, s22, v161\n"
"v_mfma_f32_16x16x32_bf16 v[176:179], v[96:99], a[216:219], v[176:179]\n"
"s_waitcnt vmcnt(14) lgkmcnt(6)\n"
"buffer_load_dwordx4 v240, s[28:31], s40 offen offset:1024 lds\n"
"v_mfma_f32_16x16x32_bf16 v[132:135], v[180:183], a[48:51], v[132:135]\n"
"ds_read_b64_tr_b16 v[96:97], v231 offset:32768\n"
"v_subrev_f32 v162, s22, v162\n"
"v_mfma_f32_16x16x32_bf16 v[132:135], v[184:187], a[52:55], v[132:135]\n"
"ds_read_b64_tr_b16 v[98:99], v231 offset:34816\n"
"v_subrev_f32 v163, s22, v163\n"
"v_mfma_f32_16x16x32_bf16 v[132:135], v[188:191], a[56:59], v[132:135]\n"
"ds_read_b64_tr_b16 v[102:103], v235 offset:34816\n"
"v_cvt_pk_bf16_f32 v158, v160, v161\n"
"v_mfma_f32_16x16x32_bf16 v[132:135], v[192:195], a[60:63], v[132:135]\n"
"ds_read_b64_tr_b16 v[106:107], v231 offset:43008\n"
"v_cvt_pk_bf16_f32 v159, v162, v163\n"
"v_mfma_f32_16x16x32_bf16 v[132:135], v[208:211], a[0:3], v[132:135]\n"
"ds_read_b64_tr_b16 v[110:111], v235 offset:43008\n"
"v_add_f32 v9, v9, v76\n"
"v_mfma_f32_16x16x32_bf16 v[132:135], v[212:215], a[4:7], v[132:135]\n"
"ds_read_b64_tr_b16 v[112:113], v231 offset:49152\n"
"v_lshlrev_b32 v207, 16, v77\n"
"v_mfma_f32_16x16x32_bf16 v[132:135], v[216:219], a[8:11], v[132:135]\n"
"ds_read_b64_tr_b16 v[114:115], v231 offset:51200\n"
"v_and_b32 v77, s73, v77\n"
"v_mfma_f32_16x16x32_bf16 v[132:135], v[220:223], a[12:15], v[132:135]\n"
"buffer_store_dwordx4 v[140:143], v243, s[32:35], s16 offen offset:2048 sc0 sc1\n"
"v_add_f32 v10, v10, v207\n"
"v_mfma_f32_16x16x32_bf16 v[148:151], v[220:223], a[28:31], v[148:151]\n"
"ds_read_b64_tr_b16 v[118:119], v235 offset:51200\n"
"v_add_f32 v11, v11, v77\n"
"v_mfma_f32_16x16x32_bf16 v[148:151], v[216:219], a[24:27], v[148:151]\n"
"ds_read_b64_tr_b16 v[122:123], v231 offset:59392\n"
"v_lshlrev_b32 v202, 16, v78\n"
"v_mfma_f32_16x16x32_bf16 v[148:151], v[212:215], a[20:23], v[148:151]\n"
"ds_read_b64_tr_b16 v[226:227], v235 offset:59392\n"
"v_and_b32 v78, s73, v78\n"
"v_mfma_f32_16x16x32_bf16 v[148:151], v[208:211], a[16:19], v[148:151]\n"
"v_add_f32 v12, v12, v202\n"
"v_add_f32 v13, v13, v78\n"
"v_mfma_f32_16x16x32_bf16 v[148:151], v[192:195], a[76:79], v[148:151]\n"
"v_lshlrev_b32 v203, 16, v79\n"
"v_and_b32 v79, s73, v79\n"
"v_mfma_f32_16x16x32_bf16 v[148:151], v[188:191], a[72:75], v[148:151]\n"
"v_add_f32 v14, v14, v203\n"
"v_add_f32 v15, v15, v79\n"
"v_mfma_f32_16x16x32_bf16 v[148:151], v[184:187], a[68:71], v[148:151]\n"
"s_add_u32 s40, s41, s53\n"
"v_lshlrev_b32 v204, 16, v80\n"
"v_and_b32 v80, s73, v80\n"
"v_mfma_f32_16x16x32_bf16 v[148:151], v[180:183], a[64:67], v[148:151]\n"
"buffer_load_dwordx4 v240, s[28:31], s40 offen offset:2048 lds\n"
"v_mfma_f32_16x16x32_bf16 v[164:167], v[180:183], a[80:83], v[164:167]\n"
"v_add_f32 v16, v16, v204\n"
"v_add_f32 v17, v17, v80\n"
"v_mfma_f32_16x16x32_bf16 v[164:167], v[184:187], a[84:87], v[164:167]\n"
"v_lshlrev_b32 v205, 16, v81\n"
"v_and_b32 v81, s73, v81\n"
"v_mfma_f32_16x16x32_bf16 v[164:167], v[188:191], a[88:91], v[164:167]\n"
"v_add_f32 v18, v18, v205\n"
"v_add_f32 v19, v19, v81\n"
"v_mfma_f32_16x16x32_bf16 v[164:167], v[192:195], a[92:95], v[164:167]\n"
"buffer_load_dwordx4 v[244:247], v243, s[32:35], s19 offen offset:2048 sc1\n"
"v_mfma_f32_16x16x32_bf16 v[164:167], v[208:211], a[32:35], v[164:167]\n"
"s_lshl4_add_u32 s18, s72, s61\n"
"v_lshlrev_b32 v206, 16, v82\n"
"v_and_b32 v82, s73, v82\n"
"v_mfma_f32_16x16x32_bf16 v[164:167], v[212:215], a[36:39], v[164:167]\n"
"s_lshl_b32 s19, s72, 2\n"
"v_add_f32 v20, v20, v206\n"
"v_add_f32 v21, v21, v82\n"
"v_mfma_f32_16x16x32_bf16 v[164:167], v[216:219], a[40:43], v[164:167]\n"
"s_waitcnt vmcnt(14)\n"
"s_lshr_b32 s19, s89, s19\n"
"v_lshlrev_b32 v207, 16, v83\n"
"v_mfma_f32_16x16x32_bf16 v[164:167], v[220:223], a[44:47], v[164:167]\n"
"s_barrier\n"
"s_and_b32 s19, s19, 1\n"
"s_waitcnt lgkmcnt(0)\n"
"v_mfma_f32_16x16x32_bf16 v[136:139], v[96:99], a[48:51], v[136:139]\n"
"s_lshl3_add_u32 s18, s18, s19\n"
"ds_read_b64_tr_b16 v[180:181], v228 offset:0\n"
"v_and_b32 v83, s73, v83\n"
"v_mfma_f32_16x16x32_bf16 v[136:139], v[100:103], a[52:55], v[136:139]\n"
"s_lshl_b32 s18, s18, 13\n"
"ds_read_b64_tr_b16 v[182:183], v228 offset:2048\n"
"v_add_f32 v22, v22, v207\n"
"v_mfma_f32_16x16x32_bf16 v[136:139], v[104:107], a[56:59], v[136:139]\n"
"s_add_u32 s19, s18, 4096\n"
"v_lshlrev_b32_e64 v201, 2, s72\n"
"ds_read_b64_tr_b16 v[184:185], v232 offset:0\n"
"v_mfma_f32_16x16x32_bf16 v[136:139], v[108:111], a[60:63], v[136:139]\n"
"s_mov_b32 s69, 15\n"
"global_load_dword v200, v201, s[6:7] sc1\n"
"v_mfma_f32_16x16x32_bf16 v[136:139], v[112:115], a[0:3], v[136:139]\n"
"s_add_u32 s21, s21, 1\n"
"ds_read_b64_tr_b16 v[186:187], v232 offset:2048\n"
"v_add_f32 v23, v23, v83\n"
"v_mfma_f32_16x16x32_bf16 v[136:139], v[116:119], a[4:7], v[136:139]\n"
"ds_read_b64_tr_b16 v[188:189], v228 offset:8192\n"
"v_subrev_f32 v172, s22, v172\n"
"v_mfma_f32_16x16x32_bf16 v[136:139], v[120:123], a[8:11], v[136:139]\n"
"ds_read_b64_tr_b16 v[190:191], v228 offset:10240\n"
"v_subrev_f32 v173, s22, v173\n"
"v_mfma_f32_16x16x32_bf16 v[136:139], v[224:227], a[12:15], v[136:139]\n"
"buffer_store_dwordx4 v[156:159], v243, s[32:35], s17 offen sc0 sc1\n"
"v_subrev_f32 v174, s22, v174\n"
"v_mfma_f32_16x16x32_bf16 v[152:155], v[224:227], a[28:31], v[152:155]\n"
"ds_read_b64_tr_b16 v[192:193], v232 offset:8192\n"
"v_subrev_f32 v175, s22, v175\n"
"v_mfma_f32_16x16x32_bf16 v[152:155], v[120:123], a[24:27], v[152:155]\n"
"ds_read_b64_tr_b16 v[194:195], v232 offset:10240\n"
"v_cvt_pk_bf16_f32 v172, v172, v173\n"
"v_mfma_f32_16x16x32_bf16 v[152:155], v[116:119], a[20:23], v[152:155]\n"
"ds_read_b64_tr_b16 v[208:209], v228 offset:16384\n"
"v_cvt_pk_bf16_f32 v173, v174, v175\n"
"v_mfma_f32_16x16x32_bf16 v[152:155], v[112:115], a[16:19], v[152:155]\n"
"ds_read_b64_tr_b16 v[210:211], v228 offset:18432\n"
"v_subrev_f32 v176, s22, v176\n"
"v_mfma_f32_16x16x32_bf16 v[152:155], v[108:111], a[76:79], v[152:155]\n"
"ds_read_b64_tr_b16 v[212:213], v232 offset:16384\n"
"v_subrev_f32 v177, s22, v177\n"
"v_mfma_f32_16x16x32_bf16 v[152:155], v[104:107], a[72:75], v[152:155]\n"
"ds_read_b64_tr_b16 v[214:215], v232 offset:18432\n"
"v_subrev_f32 v178, s22, v178\n"
"v_mfma_f32_16x16x32_bf16 v[152:155], v[100:103], a[68:71], v[152:155]\n"
"s_add_u32 s40, s41, s54\n"
"ds_read_b64_tr_b16 v[216:217], v228 offset:24576\n"
"v_subrev_f32 v179, s22, v179\n"
"v_mfma_f32_16x16x32_bf16 v[152:155], v[96:99], a[64:67], v[152:155]\n"
"buffer_load_dwordx4 v240, s[28:31], s40 offen offset:3072 lds\n"
"v_mfma_f32_16x16x32_bf16 v[168:171], v[96:99], a[80:83], v[168:171]\n"
"ds_read_b64_tr_b16 v[218:219], v228 offset:26624\n"
"v_cvt_pk_bf16_f32 v174, v176, v177\n"
"v_mfma_f32_16x16x32_bf16 v[168:171], v[100:103], a[84:87], v[168:171]\n"
"ds_read_b64_tr_b16 v[220:221], v232 offset:24576\n"
"v_cvt_pk_bf16_f32 v175, v178, v179\n"
"v_mfma_f32_16x16x32_bf16 v[168:171], v[104:107], a[88:91], v[168:171]\n"
"ds_read_b64_tr_b16 v[222:223], v232 offset:26624\n"
"v_subrev_f32 v132, s22, v132\n"
"v_mfma_f32_16x16x32_bf16 v[168:171], v[108:111], a[92:95], v[168:171]\n"
"ds_read_b64_tr_b16 v[96:97], v229 offset:0\n"
"v_subrev_f32 v133, s22, v133\n"
"v_mfma_f32_16x16x32_bf16 v[168:171], v[112:115], a[32:35], v[168:171]\n"
"ds_read_b64_tr_b16 v[98:99], v229 offset:2048\n"
"v_subrev_f32 v134, s22, v134\n"
"v_mfma_f32_16x16x32_bf16 v[168:171], v[116:119], a[36:39], v[168:171]\n"
"ds_read_b64_tr_b16 v[100:101], v233 offset:0\n"
"v_subrev_f32 v135, s22, v135\n"
"v_mfma_f32_16x16x32_bf16 v[168:171], v[120:123], a[40:43], v[168:171]\n"
"s_add_u32 s47, s47, 1\n"
"ds_read_b64_tr_b16 v[102:103], v233 offset:2048\n"
"v_cvt_pk_bf16_f32 v132, v132, v133\n"
"v_mfma_f32_16x16x32_bf16 v[168:171], v[224:227], a[44:47], v[168:171]\n"
"s_waitcnt lgkmcnt(7)\n"
"s_cmp_lg_u32 s47, 9\n"
"ds_read_b64_tr_b16 v[104:105], v229 offset:8192\n"
"s_cbranch_scc1 LGL_%=\n"
"v_mfma_f32_16x16x32_bf16 v[0:3], v[180:183], a[96:99], v[0:3]\n"
"v_subrev_f32 v136, s22, v136\n"
"v_subrev_f32 v138, s22, v138\n"
"v_mfma_f32_16x16x32_bf16 v[0:3], v[184:187], a[100:103], v[0:3]\n"
"v_subrev_f32 v151, s22, v151\n"
"v_subrev_f32 v139, s22, v139\n"
"v_mfma_f32_16x16x32_bf16 v[0:3], v[188:191], a[104:107], v[0:3]\n"
"v_mov_b32 v201, v237\n"
"v_cvt_pk_bf16_f32 v133, v134, v135\n"
"v_subrev_f32 v150, s22, v150\n"
"v_mfma_f32_16x16x32_bf16 v[0:3], v[192:195], a[108:111], v[0:3]\n"
"global_load_dword v200, v201, s[6:7] sc1\n"
"v_mfma_f32_16x16x32_bf16 v[0:3], v[208:211], a[144:147], v[0:3]\n"
"v_subrev_f32 v153, s22, v153\n"
"v_subrev_f32 v169, s22, v169\n"
"v_mfma_f32_16x16x32_bf16 v[0:3], v[212:215], a[148:151], v[0:3]\n"
"s_waitcnt lgkmcnt(2)\n"
"v_subrev_f32 v137, s22, v137\n"
"v_subrev_f32 v152, s22, v152\n"
"v_mfma_f32_16x16x32_bf16 v[0:3], v[216:219], a[152:155], v[0:3]\n"
"v_cvt_pk_bf16_f32 v134, v136, v137\n"
"v_subrev_f32 v168, s22, v168\n"
"v_mfma_f32_16x16x32_bf16 v[0:3], v[220:223], a[156:159], v[0:3]\n"
"buffer_store_dwordx4 v[172:175], v243, s[32:35], s17 offen offset:2048 sc0 sc1\n"
"v_cvt_pk_bf16_f32 v135, v138, v139\n"
"v_subrev_f32 v170, s22, v170\n"
"v_subrev_f32 v171, s22, v171\n"
"v_mfma_f32_16x16x32_bf16 v[16:19], v[220:223], a[172:175], v[16:19]\n"
"buffer_store_dwordx4 v[132:135], v243, s[32:35], s16 offen offset:1024 sc0 sc1\n"
"v_subrev_f32 v165, s22, v165\n"
"v_mfma_f32_16x16x32_bf16 v[16:19], v[216:219], a[168:171], v[16:19]\n"
"v_subrev_f32 v148, s22, v148\n"
"v_subrev_f32 v149, s22, v149\n"
"v_mfma_f32_16x16x32_bf16 v[16:19], v[212:215], a[164:167], v[16:19]\n"
"v_cvt_pk_bf16_f32 v148, v148, v149\n"
"v_subrev_f32 v167, s22, v167\n"
"v_mfma_f32_16x16x32_bf16 v[16:19], v[208:211], a[160:163], v[16:19]\n"
"v_cvt_pk_bf16_f32 v149, v150, v151\n"
"v_subrev_f32 v155, s22, v155\n"
"v_mfma_f32_16x16x32_bf16 v[16:19], v[192:195], a[124:127], v[16:19]\n"
"v_subrev_f32 v164, s22, v164\n"
"v_subrev_f32 v166, s22, v166\n"
"v_mfma_f32_16x16x32_bf16 v[16:19], v[188:191], a[120:123], v[16:19]\n"
"v_cvt_pk_bf16_f32 v164, v164, v165\n"
"v_subrev_f32 v154, s22, v154\n"
"v_mfma_f32_16x16x32_bf16 v[16:19], v[184:187], a[116:119], v[16:19]\n"
"v_cvt_pk_bf16_f32 v150, v152, v153\n"
"v_cvt_pk_bf16_f32 v151, v154, v155\n"
"v_mfma_f32_16x16x32_bf16 v[16:19], v[180:183], a[112:115], v[16:19]\n"
"s_waitcnt vmcnt(16)\n"
"buffer_store_dwordx4 v[148:151], v243, s[32:35], s16 offen offset:3072 sc0 sc1\n"
"v_cvt_pk_bf16_f32 v165, v166, v167\n"
"v_cvt_pk_bf16_f32 v166, v168, v169\n"
"v_cvt_pk_bf16_f32 v167, v170, v171\n"
"v_mfma_f32_16x16x32_bf16 v[32:35], v[180:183], a[128:131], v[32:35]\n"
"buffer_store_dwordx4 v[164:167], v243, s[32:35], s17 offen offset:1024 sc0 sc1\n"
"ds_read_b64_tr_b16 v[108:109], v233 offset:8192\n"
"v_mfma_f32_16x16x32_bf16 v[32:35], v[184:187], a[132:135], v[32:35]\n"
"ds_read_b64_tr_b16 v[122:123], v229 offset:26624\n"
"v_lshlrev_b32 v202, 16, v84\n"
"v_mfma_f32_16x16x32_bf16 v[32:35], v[188:191], a[136:139], v[32:35]\n"
"ds_read_b64_tr_b16 v[224:225], v233 offset:24576\n"
"v_xor_b32 v230, s75, v230\n"
"v_mfma_f32_16x16x32_bf16 v[32:35], v[192:195], a[140:143], v[32:35]\n"
"ds_read_b64_tr_b16 v[118:119], v233 offset:18432\n"
"v_bfe_u32 v161, v242, 8, 2\n"
"v_mfma_f32_16x16x32_bf16 v[32:35], v[208:211], a[176:179], v[32:35]\n"
"ds_read_b64_tr_b16 v[226:227], v233 offset:26624\n"
"v_bfe_u32 v160, v242, 6, 2\n"
"v_mfma_f32_16x16x32_bf16 v[32:35], v[212:215], a[180:183], v[32:35]\n"
"ds_read_b64_tr_b16 v[114:115], v229 offset:18432\n"
"v_mov_b32 v165, 0\n"
"v_mfma_f32_16x16x32_bf16 v[32:35], v[216:219], a[184:187], v[32:35]\n"
"ds_read_b64_tr_b16 v[120:121], v229 offset:24576\n"
"v_xor_b32 v160, v160, v161\n"
"v_mfma_f32_16x16x32_bf16 v[32:35], v[220:223], a[188:191], v[32:35]\n"
"ds_read_b64_tr_b16 v[116:117], v233 offset:16384\n"
"v_xor_b32 v234, s75, v234\n"
"v_mfma_f32_16x16x32_bf16 v[48:51], v[220:223], a[252:255], v[48:51]\n"
"ds_read_b64_tr_b16 v[110:111], v233 offset:10240\n"
"v_lshlrev_b32 v204, 16, v86\n"
"v_mfma_f32_16x16x32_bf16 v[48:51], v[216:219], a[248:251], v[48:51]\n"
"ds_read_b64_tr_b16 v[106:107], v229 offset:10240\n"
"v_lshlrev_b32 v205, 16, v87\n"
"v_mfma_f32_16x16x32_bf16 v[48:51], v[212:215], a[244:247], v[48:51]\n"
"ds_read_b64_tr_b16 v[112:113], v229 offset:16384\n"
"v_lshlrev_b32 v203, 16, v85\n"
"v_mfma_f32_16x16x32_bf16 v[48:51], v[208:211], a[240:243], v[48:51]\n"
"ds_read_b64_tr_b16 v[210:211], v230 offset:18432\n"
"v_add_f32 v24, v24, v202\n"
"v_mfma_f32_16x16x32_bf16 v[48:51], v[192:195], a[204:207], v[48:51]\n"
"ds_read_b64_tr_b16 v[220:221], v234 offset:24576\n"
"v_xor_b32 v231, s75, v231\n"
"v_mfma_f32_16x16x32_bf16 v[48:51], v[188:191], a[200:203], v[48:51]\n"
"ds_read_b64_tr_b16 v[194:195], v234 offset:10240\n"
"v_add_f32 v26, v26, v203\n"
"v_mfma_f32_16x16x32_bf16 v[48:51], v[184:187], a[196:199], v[48:51]\n"
"ds_read_b64_tr_b16 v[222:223], v234 offset:26624\n"
"v_add_f32 v28, v28, v204\n"
"v_mfma_f32_16x16x32_bf16 v[48:51], v[180:183], a[192:195], v[48:51]\n"
"s_waitcnt vmcnt(15)\n"
"ds_read_b64_tr_b16 v[180:181], v230 offset:0\n"
"v_lshlrev_b32 v202, 16, v90\n"
"v_mfma_f32_16x16x32_bf16 v[4:7], v[96:99], a[96:99], v[4:7]\n"
"s_waitcnt lgkmcnt(7)\n"
"ds_read_b64_tr_b16 v[218:219], v230 offset:26624\n"
"v_lshlrev_b32 v203, 16, v91\n"
"v_mfma_f32_16x16x32_bf16 v[4:7], v[100:103], a[100:103], v[4:7]\n"
"s_waitcnt lgkmcnt(7)\n"
"ds_read_b64_tr_b16 v[216:217], v230 offset:24576\n"
"v_and_b32 v91, s73, v91\n"
"v_mfma_f32_16x16x32_bf16 v[4:7], v[104:107], a[104:107], v[4:7]\n"
"ds_read_b64_tr_b16 v[190:191], v230 offset:10240\n"
"v_and_b32 v90, s73, v90\n"
"v_mfma_f32_16x16x32_bf16 v[4:7], v[108:111], a[108:111], v[4:7]\n"
"s_waitcnt lgkmcnt(7)\n"
"ds_read_b64_tr_b16 v[214:215], v234 offset:18432\n"
"v_add_u32 v160, -1, v160\n"
"v_mfma_f32_16x16x32_bf16 v[4:7], v[112:115], a[144:147], v[4:7]\n"
"s_waitcnt vmcnt(4)\n"
"ds_read_b64_tr_b16 v[208:209], v230 offset:16384\n"
"v_add_f32 v37, v37, v90\n"
"v_mfma_f32_16x16x32_bf16 v[4:7], v[116:119], a[148:151], v[4:7]\n"
"v_subrev_u32 v200, s21, v200\n"
"ds_read_b64_tr_b16 v[186:187], v234 offset:2048\n"
"v_mfma_f32_16x16x32_bf16 v[4:7], v[120:123], a[152:155], v[4:7]\n"
"v_cmp_lt_u32 vcc, s69, v200\n"
"ds_read_b64_tr_b16 v[192:193], v234 offset:8192\n"
"v_mfma_f32_16x16x32_bf16 v[4:7], v[224:227], a[156:159], v[4:7]\n"
"ds_read_b64_tr_b16 v[188:189], v230 offset:8192\n"
"s_cbranch_vccnz LGQ_%=\nLGQK_%=:\n"
"v_mfma_f32_16x16x32_bf16 v[20:23], v[224:227], a[172:175], v[20:23]\n"
"s_bfe_u32 s20, s89, 65536\ns_lshl_b32 s20, s20, 13\ns_add_u32 s20, s20, 0\n"
"ds_read_b64_tr_b16 v[184:185], v234 offset:0\n"
"v_add_f32 v39, v39, v91\n"
"v_mfma_f32_16x16x32_bf16 v[20:23], v[120:123], a[168:171], v[20:23]\n"
"buffer_load_dwordx4 v[124:127], v236, s[32:35], s20 offen sc1\n"
"v_mfma_f32_16x16x32_bf16 v[20:23], v[116:119], a[164:167], v[20:23]\n"
"v_add_f32 v30, v30, v205\n"
"v_ashrrev_i32 v160, 31, v160\n"
"v_mfma_f32_16x16x32_bf16 v[20:23], v[112:115], a[160:163], v[20:23]\n"
"s_bfe_u32 s20, s89, 65540\ns_lshl_b32 s20, s20, 13\ns_add_u32 s20, s20, 1048576\n"
"v_add_f32 v36, v36, v202\n"
"v_mfma_f32_16x16x32_bf16 v[20:23], v[108:111], a[124:127], v[20:23]\n"
"buffer_load_dwordx4 v[128:131], v236, s[32:35], s20 offen sc1\n"
"v_mfma_f32_16x16x32_bf16 v[20:23], v[104:107], a[120:123], v[20:23]\n"
"v_add_f32 v38, v38, v203\n"
"v_mfma_f32_16x16x32_bf16 v[20:23], v[100:103], a[116:119], v[20:23]\n"
"s_bfe_u32 s20, s89, 65544\ns_lshl_b32 s20, s20, 13\ns_add_u32 s20, s20, 2097152\n"
"v_and_b32 v84, s73, v84\n"
"v_lshlrev_b32 v207, 16, v89\n"
"v_mfma_f32_16x16x32_bf16 v[20:23], v[96:99], a[112:115], v[20:23]\n"
"buffer_load_dwordx4 v[132:135], v236, s[32:35], s20 offen sc1\n"
"v_mfma_f32_16x16x32_bf16 v[36:39], v[96:99], a[128:131], v[36:39]\n"
"v_and_b32 v85, s73, v85\n"
"v_lshlrev_b32 v204, 16, v92\n"
"v_mfma_f32_16x16x32_bf16 v[36:39], v[100:103], a[132:135], v[36:39]\n"
"s_bfe_u32 s20, s89, 65548\ns_lshl_b32 s20, s20, 13\ns_add_u32 s20, s20, 3145728\n"
"ds_read_b64_tr_b16 v[182:183], v230 offset:2048\n"
"ds_read_b64_tr_b16 v[212:213], v234 offset:16384\n"
"v_mfma_f32_16x16x32_bf16 v[36:39], v[104:107], a[136:139], v[36:39]\n"
"buffer_load_dwordx4 v[136:139], v236, s[32:35], s20 offen sc1\n"
"v_mfma_f32_16x16x32_bf16 v[36:39], v[108:111], a[140:143], v[36:39]\n"
"v_lshlrev_b32 v205, 16, v93\n"
"v_mfma_f32_16x16x32_bf16 v[36:39], v[112:115], a[176:179], v[36:39]\n"
"s_bfe_u32 s20, s89, 65552\ns_lshl_b32 s20, s20, 13\ns_add_u32 s20, s20, 4194304\n"
"v_and_b32 v160, 0x3f80, v160\n"
"v_add_f32 v40, v40, v204\n"
"v_mfma_f32_16x16x32_bf16 v[36:39], v[116:119], a[180:183], v[36:39]\n"
"buffer_load_dwordx4 v[140:143], v236, s[32:35], s20 offen sc1\n"
"v_mfma_f32_16x16x32_bf16 v[36:39], v[120:123], a[184:187], v[36:39]\n"
"v_and_b32 v161, 16, v242\n"
"v_lshlrev_b32 v206, 16, v88\n"
"v_mfma_f32_16x16x32_bf16 v[36:39], v[224:227], a[188:191], v[36:39]\n"
"s_bfe_u32 s20, s89, 65556\ns_lshl_b32 s20, s20, 13\ns_add_u32 s20, s20, 5242880\n"
"v_add_f32 v27, v27, v85\n"
"v_add_f32 v34, v34, v207\n"
"v_mfma_f32_16x16x32_bf16 v[52:55], v[224:227], a[252:255], v[52:55]\n"
"buffer_load_dwordx4 v[144:147], v236, s[32:35], s20 offen sc1\n"
"v_mfma_f32_16x16x32_bf16 v[52:55], v[120:123], a[248:251], v[52:55]\n"
"v_and_b32 v86, s73, v86\n"
"v_and_b32 v92, s73, v92\n"
"v_mfma_f32_16x16x32_bf16 v[52:55], v[116:119], a[244:247], v[52:55]\n"
"s_bfe_u32 s20, s89, 65560\ns_lshl_b32 s20, s20, 13\ns_add_u32 s20, s20, 6291456\n"
"v_xor_b32 v235, s75, v235\n"
"v_mov_b32 v164, 0\n"
"v_mfma_f32_16x16x32_bf16 v[52:55], v[112:115], a[240:243], v[52:55]\n"
"buffer_load_dwordx4 v[148:151], v236, s[32:35], s20 offen sc1\n"
"v_mfma_f32_16x16x32_bf16 v[52:55], v[108:111], a[204:207], v[52:55]\n"
"s_bfe_u32 s20, s89, 65564\ns_lshl_b32 s20, s20, 13\ns_add_u32 s20, s20, 7340032\n"
"v_and_b32 v88, s73, v88\n"
"v_lshlrev_b32 v207, 16, v95\n"
"v_mfma_f32_16x16x32_bf16 v[52:55], v[104:107], a[200:203], v[52:55]\n"
"buffer_load_dwordx4 v[152:155], v236, s[32:35], s20 offen sc1\n"
"v_mfma_f32_16x16x32_bf16 v[52:55], v[100:103], a[196:199], v[52:55]\n"
"s_waitcnt vmcnt(8)\n"
"s_add_u32 s21, s21, 1\n"
"v_add_f32 v32, v32, v206\n"
"v_mfma_f32_16x16x32_bf16 v[52:55], v[96:99], a[192:195], v[52:55]\n"
"s_barrier\n"
"s_mov_b32 s52, 40960\n"
"buffer_store_dword v241, v248, s[84:87], 0 offen sc1\n"
"s_waitcnt lgkmcnt(0)\n"
"v_mfma_f32_16x16x32_bf16 v[8:11], v[180:183], a[96:99], v[8:11]\n"
"s_mov_b32 s53, 81920\n"
"ds_read_b64_tr_b16 v[120:121], v231 offset:24576\n"
"v_lshlrev_b32 v206, 16, v94\n"
"v_mfma_f32_16x16x32_bf16 v[8:11], v[184:187], a[100:103], v[8:11]\n"
"ds_read_b64_tr_b16 v[96:97], v231 offset:0\n"
"v_and_b32 v89, s73, v89\n"
"v_mfma_f32_16x16x32_bf16 v[8:11], v[188:191], a[104:107], v[8:11]\n"
"ds_read_b64_tr_b16 v[102:103], v235 offset:2048\n"
"v_add_f32 v46, v46, v207\n"
"v_mfma_f32_16x16x32_bf16 v[8:11], v[192:195], a[108:111], v[8:11]\n"
"v_and_b32 v93, s73, v93\n"
"v_add_f32 v44, v44, v206\n"
"v_mfma_f32_16x16x32_bf16 v[8:11], v[208:211], a[144:147], v[8:11]\n"
"ds_read_b64_tr_b16 v[122:123], v231 offset:26624\n"
"v_add_f32 v43, v43, v93\n"
"v_mfma_f32_16x16x32_bf16 v[8:11], v[212:215], a[148:151], v[8:11]\n"
"ds_read_b64_tr_b16 v[116:117], v235 offset:16384\n"
"v_add_f32 v25, v25, v84\n"
"v_mfma_f32_16x16x32_bf16 v[8:11], v[216:219], a[152:155], v[8:11]\n"
"v_and_b32 v87, s73, v87\n"
"v_and_b32 v94, s73, v94\n"
"v_mfma_f32_16x16x32_bf16 v[8:11], v[220:223], a[156:159], v[8:11]\n"
"ds_read_b64_tr_b16 v[224:225], v235 offset:24576\n"
"v_lshlrev_b32 v202, 16, v246\n"
"v_mfma_f32_16x16x32_bf16 v[24:27], v[220:223], a[172:175], v[24:27]\n"
"v_add_f32 v31, v31, v87\n"
"v_add_f32 v42, v42, v205\n"
"v_mfma_f32_16x16x32_bf16 v[24:27], v[216:219], a[168:171], v[24:27]\n"
"ds_read_b64_tr_b16 v[100:101], v235 offset:0\n"
"v_lshlrev_b32 v207, 16, v245\n"
"v_mfma_f32_16x16x32_bf16 v[24:27], v[212:215], a[164:167], v[24:27]\n"
"v_add_f32 v29, v29, v86\n"
"v_add_f32 v41, v41, v92\n"
"v_mfma_f32_16x16x32_bf16 v[24:27], v[208:211], a[160:163], v[24:27]\n"
"ds_read_b64_tr_b16 v[98:99], v231 offset:2048\n"
"v_and_b32 v95, s73, v95\n"
"v_mfma_f32_16x16x32_bf16 v[24:27], v[192:195], a[124:127], v[24:27]\n"
"ds_read_b64_tr_b16 v[114:115], v231 offset:18432\n"
"v_add_f32 v33, v33, v88\n"
"v_mfma_f32_16x16x32_bf16 v[24:27], v[188:191], a[120:123], v[24:27]\n"
"ds_read_b64_tr_b16 v[110:111], v235 offset:10240\n"
"v_and_b32 v245, s73, v245\n"
"v_mfma_f32_16x16x32_bf16 v[24:27], v[184:187], a[116:119], v[24:27]\n"
"v_add_f32 v45, v45, v94\n"
"v_and_b32 v246, s73, v246\n"
"v_mfma_f32_16x16x32_bf16 v[24:27], v[180:183], a[112:115], v[24:27]\n"
"ds_read_b64_tr_b16 v[108:109], v235 offset:8192\n"
"v_lshlrev_b32 v206, 16, v244\n"
"v_mfma_f32_16x16x32_bf16 v[40:43], v[180:183], a[128:131], v[40:43]\n"
"ds_read_b64_tr_b16 v[106:107], v231 offset:10240\n"
"v_lshlrev_b32 v160, v161, v160\n"
"v_mfma_f32_16x16x32_bf16 v[40:43], v[184:187], a[132:135], v[40:43]\n"
"ds_read_b64_tr_b16 v[104:105], v231 offset:8192\n"
"v_mfma_f32_16x16x32_bf16 v[40:43], v[188:191], a[136:139], v[40:43]\n"
"v_bfe_i32 v161, v242, 5, 1\n"
"v_mfma_f32_16x16x32_bf16 v[40:43], v[192:195], a[140:143], v[40:43]\n"
"ds_read_b64_tr_b16 v[112:113], v231 offset:16384\n"
"v_and_b32 v244, s73, v244\n"
"v_mfma_f32_16x16x32_bf16 v[40:43], v[208:211], a[176:179], v[40:43]\n"
"ds_read_b64_tr_b16 v[118:119], v235 offset:18432\n"
"v_add_f32 v49, v49, v244\n"
"v_mfma_f32_16x16x32_bf16 v[40:43], v[212:215], a[180:183], v[40:43]\n"
"v_add_f32 v52, v52, v202\n"
"v_lshlrev_b32 v203, 16, v247\n"
"v_mfma_f32_16x16x32_bf16 v[40:43], v[216:219], a[184:187], v[40:43]\n"
"ds_read_b64_tr_b16 v[226:227], v235 offset:26624\n"
"v_add_f32 v48, v48, v206\n"
"v_mfma_f32_16x16x32_bf16 v[40:43], v[220:223], a[188:191], v[40:43]\n"
"s_barrier\n"
"s_waitcnt lgkmcnt(5)\n"
"v_mfma_f32_16x16x32_bf16 v[12:15], v[96:99], a[96:99], v[12:15]\n"
"ds_read_b64_tr_b16 v[194:195], v232 offset:43008\n"
"ds_read_b64_tr_b16 v[214:215], v232 offset:51200\n"
"v_mfma_f32_16x16x32_bf16 v[12:15], v[100:103], a[100:103], v[12:15]\n"
"s_waitcnt lgkmcnt(5)\n"
"ds_read_b64_tr_b16 v[186:187], v232 offset:34816\n"
"v_add_f32 v53, v53, v246\n"
"v_mfma_f32_16x16x32_bf16 v[12:15], v[104:107], a[104:107], v[12:15]\n"
"ds_read_b64_tr_b16 v[184:185], v232 offset:32768\n"
"ds_read_b64_tr_b16 v[208:209], v228 offset:49152\n"
"v_mfma_f32_16x16x32_bf16 v[12:15], v[108:111], a[108:111], v[12:15]\n"
"s_waitcnt lgkmcnt(7)\n"
"ds_read_b64_tr_b16 v[188:189], v228 offset:40960\n"
"v_add_f32 v47, v47, v95\n"
"v_mfma_f32_16x16x32_bf16 v[12:15], v[112:115], a[144:147], v[12:15]\n"
"s_waitcnt lgkmcnt(7)\n"
"ds_read_b64_tr_b16 v[180:181], v228 offset:32768\n"
"v_add_f32 v35, v35, v89\n"
"v_mfma_f32_16x16x32_bf16 v[12:15], v[116:119], a[148:151], v[12:15]\n"
"ds_read_b64_tr_b16 v[216:217], v228 offset:57344\n"
"v_add_f32 v51, v51, v245\n"
"v_mfma_f32_16x16x32_bf16 v[12:15], v[120:123], a[152:155], v[12:15]\n"
"s_waitcnt lgkmcnt(8)\n"
"ds_read_b64_tr_b16 v[182:183], v228 offset:34816\n"
"ds_read_b64_tr_b16 v[212:213], v232 offset:49152\n"
"v_mfma_f32_16x16x32_bf16 v[12:15], v[224:227], a[156:159], v[12:15]\n"
"global_load_dword v200, v201, s[6:7] sc1\n"
"v_mfma_f32_16x16x32_bf16 v[28:31], v[224:227], a[172:175], v[28:31]\n"
"v_add_f32 v50, v50, v207\n"
"v_add_f32 v54, v54, v203\n"
"v_mfma_f32_16x16x32_bf16 v[28:31], v[120:123], a[168:171], v[28:31]\n"
"s_waitcnt vmcnt(9)\n"
"v_lshlrev_b32 v58, 16, v125\n"
"v_lshlrev_b32 v62, 16, v127\n"
"v_mfma_f32_16x16x32_bf16 v[28:31], v[116:119], a[164:167], v[28:31]\n"
"ds_read_b64_tr_b16 v[210:211], v228 offset:51200\n"
"ds_read_b64_tr_b16 v[218:219], v228 offset:59392\n"
"v_mfma_f32_16x16x32_bf16 v[28:31], v[112:115], a[160:163], v[28:31]\n"
"ds_read_b64_tr_b16 v[190:191], v228 offset:43008\n"
"v_lshlrev_b32 v60, 16, v126\n"
"v_mfma_f32_16x16x32_bf16 v[28:31], v[108:111], a[124:127], v[28:31]\n"
"s_waitcnt vmcnt(8)\n"
"ds_read_b64_tr_b16 v[192:193], v232 offset:40960\n"
"v_lshlrev_b32 v205, 16, v129\n"
"v_mfma_f32_16x16x32_bf16 v[28:31], v[104:107], a[120:123], v[28:31]\n"
"v_lshlrev_b32 v56, 16, v124\n"
"v_lshlrev_b32 v206, 16, v130\n"
"v_mfma_f32_16x16x32_bf16 v[28:31], v[100:103], a[116:119], v[28:31]\n"
"ds_read_b64_tr_b16 v[222:223], v232 offset:59392\n"
"v_add_f32 v58, v58, v205\n"
"v_mfma_f32_16x16x32_bf16 v[28:31], v[96:99], a[112:115], v[28:31]\n"
"s_waitcnt vmcnt(7)\n"
"v_lshlrev_b32 v204, 16, v128\n"
"v_add_f32 v56, v56, v204\n"
"v_mfma_f32_16x16x32_bf16 v[44:47], v[96:99], a[128:131], v[44:47]\n"
"v_lshlrev_b32 v207, 16, v131\n"
"v_lshlrev_b32 v203, 16, v133\n"
"v_mfma_f32_16x16x32_bf16 v[44:47], v[100:103], a[132:135], v[44:47]\n"
"v_add_f32 v58, v58, v203\n"
"v_lshlrev_b32 v205, 16, v135\n"
"v_mfma_f32_16x16x32_bf16 v[44:47], v[104:107], a[136:139], v[44:47]\n"
"s_waitcnt vmcnt(6)\n"
"ds_read_b64_tr_b16 v[106:107], v229 offset:43008\n"
"v_and_b32 v59, s73, v125\n"
"v_mfma_f32_16x16x32_bf16 v[44:47], v[108:111], a[140:143], v[44:47]\n"
"v_add_f32 v60, v60, v206\n"
"v_lshlrev_b32 v206, 16, v136\n"
"v_mfma_f32_16x16x32_bf16 v[44:47], v[112:115], a[176:179], v[44:47]\n"
"v_lshlrev_b32 v202, 16, v132\n"
"v_and_b32 v132, s73, v132\n"
"v_mfma_f32_16x16x32_bf16 v[44:47], v[116:119], a[180:183], v[44:47]\n"
"s_waitcnt vmcnt(5)\n"
"ds_read_b64_tr_b16 v[100:101], v233 offset:32768\n"
"v_lshlrev_b32 v204, 16, v134\n"
"v_mfma_f32_16x16x32_bf16 v[44:47], v[120:123], a[184:187], v[44:47]\n"
"ds_read_b64_tr_b16 v[220:221], v232 offset:57344\n"
"v_add_f32 v62, v62, v207\n"
"v_mfma_f32_16x16x32_bf16 v[44:47], v[224:227], a[188:191], v[44:47]\n"
"s_waitcnt lgkmcnt(3)\n"
"ds_read_b64_tr_b16 v[224:225], v233 offset:57344\n"
"v_add_f32 v62, v62, v205\n"
"v_mfma_f32_16x16x32_bf16 v[0:3], v[180:183], a[48:51], v[0:3]\n"
"s_waitcnt vmcnt(4)\n"
"ds_read_b64_tr_b16 v[114:115], v229 offset:51200\n"
"v_lshlrev_b32 v207, 16, v137\n"
"v_mfma_f32_16x16x32_bf16 v[0:3], v[184:187], a[52:55], v[0:3]\n"
"v_and_b32 v128, s73, v128\n"
"v_lshlrev_b32 v203, 16, v139\n"
"v_mfma_f32_16x16x32_bf16 v[0:3], v[188:191], a[56:59], v[0:3]\n"
"v_and_b32 v57, s73, v124\n"
"v_add_f32 v60, v60, v204\n"
"v_mfma_f32_16x16x32_bf16 v[0:3], v[192:195], a[60:63], v[0:3]\n"
"s_waitcnt vmcnt(3)\n"
"ds_read_b64_tr_b16 v[96:97], v229 offset:32768\n"
"ds_read_b64_tr_b16 v[112:113], v229 offset:49152\n"
"v_mfma_f32_16x16x32_bf16 v[0:3], v[208:211], a[0:3], v[0:3]\n"
"v_and_b32 v247, s73, v247\n"
"v_add_f32 v58, v58, v207\n"
"v_mfma_f32_16x16x32_bf16 v[0:3], v[212:215], a[4:7], v[0:3]\n"
"s_waitcnt vmcnt(2)\n"
"ds_read_b64_tr_b16 v[102:103], v233 offset:34816\n"
"v_and_b32 v134, s73, v134\n"
"v_mfma_f32_16x16x32_bf16 v[0:3], v[216:219], a[8:11], v[0:3]\n"
"s_waitcnt lgkmcnt(5)\n"
"ds_read_b64_tr_b16 v[120:121], v229 offset:57344\n"
"v_lshlrev_b32 v204, 16, v140\n"
"v_mfma_f32_16x16x32_bf16 v[0:3], v[220:223], a[12:15], v[0:3]\n"
"ds_read_b64_tr_b16 v[98:99], v229 offset:34816\n"
"v_and_b32 v61, s73, v126\n"
"v_add_f32 v57, v57, v128\n"
"v_add_f32 v56, v56, v202\n"
"v_and_b32 v135, s73, v135\n"
"v_and_b32 v136, s73, v136\n"
"v_and_b32 v137, s73, v137\n"
"v_lshlrev_b32 v202, 16, v138\n"
"v_mfma_f32_16x16x32_bf16 v[16:19], v[220:223], a[28:31], v[16:19]\n"
"ds_read_b64_tr_b16 v[122:123], v229 offset:59392\n"
"v_and_b32 v129, s73, v129\n"
"v_mfma_f32_16x16x32_bf16 v[16:19], v[216:219], a[24:27], v[16:19]\n"
"ds_read_b64_tr_b16 v[108:109], v233 offset:40960\n"
"v_add_f32 v56, v56, v206\n"
"v_mfma_f32_16x16x32_bf16 v[16:19], v[212:215], a[20:23], v[16:19]\n"
"v_add_f32 v60, v60, v202\n"
"v_lshlrev_b32 v207, 16, v143\n"
"v_mfma_f32_16x16x32_bf16 v[16:19], v[208:211], a[16:19], v[16:19]\n"
"ds_read_b64_tr_b16 v[116:117], v233 offset:49152\n"
"v_lshlrev_b32 v205, 16, v141\n"
"v_mfma_f32_16x16x32_bf16 v[16:19], v[192:195], a[76:79], v[16:19]\n"
"s_waitcnt vmcnt(0)\n"
"v_add_f32 v55, v55, v247\n"
"v_lshlrev_b32 v202, 16, v144\n"
"v_mfma_f32_16x16x32_bf16 v[16:19], v[188:191], a[72:75], v[16:19]\n"
"v_subrev_u32 v200, s21, v200\n"
"ds_read_b64_tr_b16 v[104:105], v229 offset:40960\n"
"v_mfma_f32_16x16x32_bf16 v[16:19], v[184:187], a[68:71], v[16:19]\n"
"v_cmp_lt_u32 vcc, s69, v200\n"
"v_add_f32 v62, v62, v203\n"
"v_mfma_f32_16x16x32_bf16 v[16:19], v[180:183], a[64:67], v[16:19]\n"
"ds_read_b64_tr_b16 v[118:119], v233 offset:51200\n"
"v_and_b32 v63, s73, v127\n"
"v_add_f32 v59, v59, v129\n"
"v_add_f32 v56, v56, v204\n"
"v_add_f32 v58, v58, v205\n"
"v_lshlrev_b32 v206, 16, v142\n"
"v_lshlrev_b32 v203, 16, v145\n"
"v_lshlrev_b32 v205, 16, v147\n"
"v_subrev_f32 v1, s22, v1\n"
"s_cbranch_vccnz LGR_%=\nLGRK_%=:\n"
"v_mfma_f32_16x16x32_bf16 v[32:35], v[180:183], a[80:83], v[32:35]\n"
"ds_read_b64_tr_b16 v[110:111], v233 offset:43008\n"
"s_cmp_eq_u32 s93, 0\ns_cbranch_scc1 LEP_%=\nLEPK_%=:\n"
"v_mfma_f32_16x16x32_bf16 v[32:35], v[184:187], a[84:87], v[32:35]\n"
"s_bfe_u32 s20, s89, 65536\ns_lshl_b32 s20, s20, 13\ns_add_u32 s20, s20, 65536\n"
"ds_read_b64_tr_b16 v[226:227], v233 offset:59392\n"
"v_add_f32 v57, v57, v132\n"
"v_mfma_f32_16x16x32_bf16 v[32:35], v[188:191], a[88:91], v[32:35]\n"
"buffer_load_dwordx4 v[72:75], v236, s[32:35], s20 offen sc1\n"
"v_mfma_f32_16x16x32_bf16 v[32:35], v[192:195], a[92:95], v[32:35]\n"
"v_and_b32 v131, s73, v131\n"
"v_add_f32 v58, v58, v203\n"
"v_mfma_f32_16x16x32_bf16 v[32:35], v[208:211], a[32:35], v[32:35]\n"
"s_bfe_u32 s20, s89, 65540\ns_lshl_b32 s20, s20, 13\ns_add_u32 s20, s20, 1114112\n"
"v_subrev_f32 v2, s22, v2\n"
"v_subrev_f32 v16, s22, v16\n"
"v_mfma_f32_16x16x32_bf16 v[32:35], v[212:215], a[36:39], v[32:35]\n"
"buffer_load_dwordx4 v[76:79], v236, s[32:35], s20 offen sc1\n"
"v_mfma_f32_16x16x32_bf16 v[32:35], v[216:219], a[40:43], v[32:35]\n"
"v_and_b32 v139, s73, v139\n"
"v_add_f32 v62, v62, v207\n"
"v_mfma_f32_16x16x32_bf16 v[32:35], v[220:223], a[44:47], v[32:35]\n"
"s_bfe_u32 s20, s89, 65544\ns_lshl_b32 s20, s20, 13\ns_add_u32 s20, s20, 2162688\n"
"v_add_f32 v62, v62, v205\n"
"v_lshlrev_b32 v207, 16, v149\n"
"v_mfma_f32_16x16x32_bf16 v[48:51], v[220:223], a[228:231], v[48:51]\n"
"buffer_load_dwordx4 v[80:83], v236, s[32:35], s20 offen sc1\n"
"v_mfma_f32_16x16x32_bf16 v[48:51], v[216:219], a[224:227], v[48:51]\n"
"v_and_b32 v140, s73, v140\n"
"v_and_b32 v142, s73, v142\n"
"v_mfma_f32_16x16x32_bf16 v[48:51], v[212:215], a[212:215], v[48:51]\n"
"s_bfe_u32 s20, s89, 65548\ns_lshl_b32 s20, s20, 13\ns_add_u32 s20, s20, 3211264\n"
"v_add_f32 v57, v57, v136\n"
"v_subrev_f32 v3, s22, v3\n"
"v_mfma_f32_16x16x32_bf16 v[48:51], v[208:211], a[208:211], v[48:51]\n"
"buffer_load_dwordx4 v[84:87], v236, s[32:35], s20 offen sc1\n"
"v_mfma_f32_16x16x32_bf16 v[48:51], v[192:195], a[236:239], v[48:51]\n"
"v_lshlrev_b32 v204, 16, v146\n"
"v_and_b32 v147, s73, v147\n"
"v_mfma_f32_16x16x32_bf16 v[48:51], v[188:191], a[232:235], v[48:51]\n"
"s_bfe_u32 s20, s89, 65552\ns_lshl_b32 s20, s20, 13\ns_add_u32 s20, s20, 4259840\n"
"v_and_b32 v138, s73, v138\n"
"v_and_b32 v144, s73, v144\n"
"v_mfma_f32_16x16x32_bf16 v[48:51], v[184:187], a[220:223], v[48:51]\n"
"buffer_load_dwordx4 v[88:91], v236, s[32:35], s20 offen sc1\n"
"v_mfma_f32_16x16x32_bf16 v[48:51], v[180:183], a[216:219], v[48:51]\n"
"s_waitcnt lgkmcnt(0)\n"
"v_and_b32 v133, s73, v133\n"
"v_add_f32 v58, v58, v207\n"
"v_mfma_f32_16x16x32_bf16 v[4:7], v[96:99], a[48:51], v[4:7]\n"
"s_bfe_u32 s20, s89, 65556\ns_lshl_b32 s20, s20, 13\ns_add_u32 s20, s20, 5308416\n"
"v_add_f32 v59, v59, v133\n"
"v_add_f32 v60, v60, v206\n"
"v_mfma_f32_16x16x32_bf16 v[4:7], v[100:103], a[52:55], v[4:7]\n"
"buffer_load_dwordx4 v[92:95], v236, s[32:35], s20 offen sc1\n"
"v_mfma_f32_16x16x32_bf16 v[4:7], v[104:107], a[56:59], v[4:7]\n"
"v_subrev_f32 v0, s22, v0\n"
"v_subrev_f32 v17, s22, v17\n"
"v_mfma_f32_16x16x32_bf16 v[4:7], v[108:111], a[60:63], v[4:7]\n"
"s_bfe_u32 s20, s89, 65560\ns_lshl_b32 s20, s20, 13\ns_add_u32 s20, s20, 6356992\n"
"v_add_f32 v56, v56, v202\n"
"v_cvt_pk_bf16_f32 v16, v16, v17\n"
"v_mfma_f32_16x16x32_bf16 v[4:7], v[112:115], a[0:3], v[4:7]\n"
"buffer_load_dwordx4 v[244:247], v236, s[32:35], s20 offen sc1\n"
"v_and_b32 v130, s73, v130\n"
"v_add_f32 v59, v59, v137\n"
"v_add_f32 v57, v57, v140\n"
"v_add_f32 v60, v60, v204\n"
"v_lshlrev_b32 v202, 16, v150\n"
"v_lshlrev_b32 v203, 16, v151\n"
"v_cvt_pk_bf16_f32 v0, v0, v1\n"
"v_subrev_f32 v32, s22, v32\n"
"v_mfma_f32_16x16x32_bf16 v[4:7], v[116:119], a[4:7], v[4:7]\n"
"v_add_f32 v63, v63, v131\n"
"v_lshlrev_b32 v206, 16, v148\n"
"v_mfma_f32_16x16x32_bf16 v[4:7], v[120:123], a[8:11], v[4:7]\n"
"s_bfe_u32 s20, s89, 65564\ns_lshl_b32 s20, s20, 13\ns_add_u32 s20, s20, 7405568\n"
"v_and_b32 v151, s73, v151\n"
"v_lshlrev_b32 v204, 16, v152\n"
"v_mfma_f32_16x16x32_bf16 v[4:7], v[224:227], a[12:15], v[4:7]\n"
"buffer_load_dwordx4 v[156:159], v236, s[32:35], s20 offen sc1\n"
"v_mfma_f32_16x16x32_bf16 v[20:23], v[224:227], a[28:31], v[20:23]\n"
"v_add_f32 v57, v57, v144\n"
"v_and_b32 v146, s73, v146\n"
"v_mfma_f32_16x16x32_bf16 v[20:23], v[120:123], a[24:27], v[20:23]\n"
"v_and_b32 v141, s73, v141\n"
"v_cvt_pk_bf16_f32 v1, v2, v3\n"
"v_mfma_f32_16x16x32_bf16 v[20:23], v[116:119], a[20:23], v[20:23]\n"
"v_lshlrev_b32 v207, 16, v155\n"
"v_subrev_f32 v19, s22, v19\n"
"v_mfma_f32_16x16x32_bf16 v[20:23], v[112:115], a[16:19], v[20:23]\n"
"v_add_f32 v62, v62, v203\n"
"v_subrev_f32 v18, s22, v18\n"
"v_mfma_f32_16x16x32_bf16 v[20:23], v[108:111], a[76:79], v[20:23]\n"
"ds_read_b64_tr_b16 v[210:211], v230 offset:51200\n"
"v_add_f32 v61, v61, v130\n"
"v_add_f32 v63, v63, v135\n"
"v_and_b32 v143, s73, v143\n"
"v_and_b32 v148, s73, v148\n"
"v_add_f32 v56, v56, v206\n"
"v_and_b32 v149, s73, v149\n"
"v_add_f32 v56, v56, v204\n"
"v_subrev_f32 v6, s22, v6\n"
"v_subrev_f32 v7, s22, v7\n"
"v_mfma_f32_16x16x32_bf16 v[20:23], v[104:107], a[72:75], v[20:23]\n"
"ds_read_b64_tr_b16 v[182:183], v230 offset:34816\n"
"v_subrev_f32 v35, s22, v35\n"
"v_mfma_f32_16x16x32_bf16 v[20:23], v[100:103], a[68:71], v[20:23]\n"
"v_and_b32 v155, s73, v155\n"
"v_subrev_f32 v4, s22, v4\n"
"v_mfma_f32_16x16x32_bf16 v[20:23], v[96:99], a[64:67], v[20:23]\n"
"ds_read_b64_tr_b16 v[186:187], v234 offset:34816\n"
"v_add_f32 v61, v61, v134\n"
"v_add_f32 v57, v57, v148\n"
"v_and_b32 v152, s73, v152\n"
"v_add_f32 v57, v57, v152\n"
"v_lshlrev_b32 v205, 16, v153\n"
"v_subrev_f32 v5, s22, v5\n"
"v_cvt_pk_bf16_f32 v2, v4, v5\n"
"v_cvt_pk_bf16_f32 v3, v6, v7\n"
"v_subrev_f32 v34, s22, v34\n"
"v_mfma_f32_16x16x32_bf16 v[36:39], v[96:99], a[80:83], v[36:39]\n"
"ds_read_b64_tr_b16 v[208:209], v230 offset:49152\n"
"v_and_b32 v150, s73, v150\n"
"v_mfma_f32_16x16x32_bf16 v[36:39], v[100:103], a[84:87], v[36:39]\n"
"ds_read_b64_tr_b16 v[192:193], v234 offset:40960\n"
"v_add_f32 v61, v61, v138\n"
"v_add_f32 v63, v63, v139\n"
"v_and_b32 v145, s73, v145\n"
"v_permlane16_swap_b32 v0, v2\n"
"v_permlane16_swap_b32 v1, v3\n"
"v_cvt_pk_bf16_f32 v17, v18, v19\n"
"v_mfma_f32_16x16x32_bf16 v[36:39], v[104:107], a[88:91], v[36:39]\n"
"ds_read_b64_tr_b16 v[214:215], v234 offset:51200\n"
"v_add_f32 v59, v59, v141\n"
"v_mfma_f32_16x16x32_bf16 v[36:39], v[108:111], a[92:95], v[36:39]\n"
"ds_read_b64_tr_b16 v[190:191], v230 offset:43008\n"
"v_add_f32 v61, v61, v142\n"
"v_mfma_f32_16x16x32_bf16 v[36:39], v[112:115], a[32:35], v[36:39]\n"
"ds_read_b64_tr_b16 v[184:185], v234 offset:32768\n"
"buffer_store_dwordx4 v[0:3], v238, s[36:39], 0 offen sc0 sc1\n"
"v_lshlrev_b32 v206, 16, v154\n"
"v_subrev_f32 v20, s22, v20\n"
"v_subrev_f32 v21, s22, v21\n"
"v_subrev_f32 v23, s22, v23\n"
"v_cvt_pk_bf16_f32 v18, v20, v21\n"
"v_mfma_f32_16x16x32_bf16 v[36:39], v[116:119], a[36:39], v[36:39]\n"
"ds_read_b64_tr_b16 v[194:195], v234 offset:43008\n"
"v_and_b32 v154, s73, v154\n"
"v_mfma_f32_16x16x32_bf16 v[36:39], v[120:123], a[40:43], v[36:39]\n"
"ds_read_b64_tr_b16 v[188:189], v230 offset:40960\n"
"v_and_b32 v153, s73, v153\n"
"v_mfma_f32_16x16x32_bf16 v[36:39], v[224:227], a[44:47], v[36:39]\n"
"ds_read_b64_tr_b16 v[212:213], v234 offset:49152\n"
"v_add_f32 v59, v59, v145\n"
"v_add_f32 v59, v59, v149\n"
"v_add_f32 v62, v62, v207\n"
"v_subrev_f32 v22, s22, v22\n"
"v_permlane16_swap_b32 v16, v18\n"
"v_subrev_f32 v48, s22, v48\n"
"v_subrev_f32 v50, s22, v50\n"
"v_subrev_f32 v51, s22, v51\n"
"v_mfma_f32_16x16x32_bf16 v[52:55], v[224:227], a[228:231], v[52:55]\n"
"ds_read_b64_tr_b16 v[220:221], v234 offset:57344\n"
"v_cvt_pk_bf16_f32 v19, v22, v23\n"
"v_mfma_f32_16x16x32_bf16 v[52:55], v[120:123], a[224:227], v[52:55]\n"
"v_add_f32 v60, v60, v202\n"
"v_add_f32 v59, v59, v153\n"
"v_mfma_f32_16x16x32_bf16 v[52:55], v[116:119], a[212:215], v[52:55]\n"
"ds_read_b64_tr_b16 v[180:181], v230 offset:32768\n"
"v_add_f32 v63, v63, v143\n"
"v_add_f32 v61, v61, v146\n"
"v_add_f32 v63, v63, v147\n"
"v_add_f32 v61, v61, v150\n"
"v_add_f32 v63, v63, v151\n"
"v_add_f32 v60, v60, v206\n"
"v_cvt_pk_bf16_f32 v56, v56, v57\n"
"v_permlane16_swap_b32 v17, v19\n"
"v_mfma_f32_16x16x32_bf16 v[52:55], v[112:115], a[208:211], v[52:55]\n"
"v_add_f32 v58, v58, v205\n"
"v_subrev_f32 v37, s22, v37\n"
"v_mfma_f32_16x16x32_bf16 v[52:55], v[108:111], a[236:239], v[52:55]\n"
"ds_read_b64_tr_b16 v[218:219], v230 offset:59392\n"
"v_subrev_f32 v36, s22, v36\n"
"v_mfma_f32_16x16x32_bf16 v[52:55], v[104:107], a[232:235], v[52:55]\n"
"ds_read_b64_tr_b16 v[216:217], v230 offset:57344\n"
"buffer_store_dwordx4 v[16:19], v238, s[36:39], s52 offen sc0 sc1\n"
"v_add_f32 v61, v61, v154\n"
"v_add_f32 v63, v63, v155\n"
"v_cvt_pk_bf16_f32 v57, v58, v59\n"
"v_cvt_pk_bf16_f32 v58, v60, v61\n"
"v_subrev_f32 v38, s22, v38\n"
"v_mfma_f32_16x16x32_bf16 v[52:55], v[100:103], a[220:223], v[52:55]\n"
"ds_read_b64_tr_b16 v[222:223], v234 offset:59392\n"
"v_cvt_pk_bf16_f32 v59, v62, v63\n"
"v_mfma_f32_16x16x32_bf16 v[52:55], v[96:99], a[216:219], v[52:55]\n"
"s_waitcnt lgkmcnt(3)\n"
"v_permlane16_swap_b32 v56, v58\n"
"v_mfma_f32_16x16x32_bf16 v[8:11], v[180:183], a[48:51], v[8:11]\n"
"v_permlane16_swap_b32 v57, v59\n"
"v_mfma_f32_16x16x32_bf16 v[8:11], v[184:187], a[52:55], v[8:11]\n"
"buffer_store_dwordx4 v[56:59], v253, s[36:39], 0 offen sc0 sc1\n"
"v_subrev_f32 v33, s22, v33\n"
"v_subrev_f32 v39, s22, v39\n"
"v_cvt_pk_bf16_f32 v32, v32, v33\n"
"v_cvt_pk_bf16_f32 v33, v34, v35\n"
"v_cvt_pk_bf16_f32 v34, v36, v37\n"
"v_cvt_pk_bf16_f32 v35, v38, v39\n"
"v_mfma_f32_16x16x32_bf16 v[8:11], v[188:191], a[56:59], v[8:11]\n"
"v_subrev_f32 v49, s22, v49\n"
"v_subrev_f32 v52, s22, v52\n"
"v_mfma_f32_16x16x32_bf16 v[8:11], v[192:195], a[60:63], v[8:11]\n"
"v_permlane16_swap_b32 v32, v34\n"
"v_mfma_f32_16x16x32_bf16 v[8:11], v[208:211], a[0:3], v[8:11]\n"
"v_permlane16_swap_b32 v33, v35\n"
"v_mfma_f32_16x16x32_bf16 v[8:11], v[212:215], a[4:7], v[8:11]\n"
"s_waitcnt lgkmcnt(1)\n"
"buffer_store_dwordx4 v[32:35], v238, s[36:39], s53 offen sc0 sc1\n"
"v_cvt_pk_bf16_f32 v48, v48, v49\n"
"v_mfma_f32_16x16x32_bf16 v[8:11], v[216:219], a[8:11], v[8:11]\n"
"s_waitcnt lgkmcnt(0)\n"
"ds_read_b64_tr_b16 v[110:111], v235 offset:43008\n"
"v_subrev_f32 v53, s22, v53\n"
"v_mfma_f32_16x16x32_bf16 v[8:11], v[220:223], a[12:15], v[8:11]\n"
"v_cvt_pk_bf16_f32 v49, v50, v51\n"
"v_cvt_pk_bf16_f32 v50, v52, v53\n"
"v_mfma_f32_16x16x32_bf16 v[24:27], v[220:223], a[28:31], v[24:27]\n"
"s_nop 0\n"
"v_permlane16_swap_b32 v48, v50\n"
"v_mfma_f32_16x16x32_bf16 v[24:27], v[216:219], a[24:27], v[24:27]\n"
"ds_read_b64_tr_b16 v[114:115], v231 offset:51200\n"
"v_subrev_f32 v55, s22, v55\n"
"v_mfma_f32_16x16x32_bf16 v[24:27], v[212:215], a[20:23], v[24:27]\n"
"ds_read_b64_tr_b16 v[102:103], v235 offset:34816\n"
"v_subrev_f32 v54, s22, v54\n"
"v_mfma_f32_16x16x32_bf16 v[24:27], v[208:211], a[16:19], v[24:27]\n"
"ds_read_b64_tr_b16 v[112:113], v231 offset:49152\n"
"v_cvt_pk_bf16_f32 v51, v54, v55\n"
"v_mfma_f32_16x16x32_bf16 v[24:27], v[192:195], a[76:79], v[24:27]\n"
"s_nop 0\n"
"v_permlane16_swap_b32 v49, v51\n"
"v_mfma_f32_16x16x32_bf16 v[24:27], v[188:191], a[72:75], v[24:27]\n"
"buffer_store_dwordx4 v[48:51], v252, s[36:39], 0 offen sc0 sc1\n"
"ds_read_b64_tr_b16 v[106:107], v231 offset:43008\n"
"v_mfma_f32_16x16x32_bf16 v[24:27], v[184:187], a[68:71], v[24:27]\n"
"ds_read_b64_tr_b16 v[96:97], v231 offset:32768\n"
"ds_read_b64_tr_b16 v[98:99], v231 offset:34816\n"
"v_mfma_f32_16x16x32_bf16 v[24:27], v[180:183], a[64:67], v[24:27]\n"
"ds_read_b64_tr_b16 v[100:101], v235 offset:32768\n"
"v_and_b32 v163, v160, v161\n"
"v_mfma_f32_16x16x32_bf16 v[40:43], v[180:183], a[80:83], v[40:43]\n"
"ds_read_b64_tr_b16 v[118:119], v235 offset:51200\n"
"v_xor_b32 v162, v160, v163\n"
"v_mfma_f32_16x16x32_bf16 v[40:43], v[184:187], a[84:87], v[40:43]\n"
"ds_read_b64_tr_b16 v[104:105], v231 offset:40960\n"
"v_mfma_f32_16x16x32_bf16 v[40:43], v[188:191], a[88:91], v[40:43]\n"
"ds_read_b64_tr_b16 v[108:109], v235 offset:40960\n"
"v_subrev_f32 v8, s22, v8\n"
"v_mfma_f32_16x16x32_bf16 v[40:43], v[192:195], a[92:95], v[40:43]\n"
"ds_read_b64_tr_b16 v[116:117], v235 offset:49152\n"
"v_mfma_f32_16x16x32_bf16 v[40:43], v[208:211], a[32:35], v[40:43]\n"
"ds_read_b64_tr_b16 v[122:123], v231 offset:59392\n"
"v_subrev_f32 v9, s22, v9\n"
"v_mfma_f32_16x16x32_bf16 v[40:43], v[212:215], a[36:39], v[40:43]\n"
"ds_read_b64_tr_b16 v[120:121], v231 offset:57344\n"
"v_subrev_f32 v10, s22, v10\n"
"v_mfma_f32_16x16x32_bf16 v[40:43], v[216:219], a[40:43], v[40:43]\n"
"ds_read_b64_tr_b16 v[224:225], v235 offset:57344\n"
"ds_read_b64_tr_b16 v[226:227], v235 offset:59392\n"
"v_mfma_f32_16x16x32_bf16 v[40:43], v[220:223], a[44:47], v[40:43]\n"
"s_waitcnt lgkmcnt(8)\n"
"v_subrev_f32 v11, s22, v11\n"
"v_mfma_f32_16x16x32_bf16 v[12:15], v[96:99], a[48:51], v[12:15]\n"
"v_mfma_f32_16x16x32_bf16 v[12:15], v[100:103], a[52:55], v[12:15]\n"
"s_waitcnt lgkmcnt(6)\n"
"v_mov_b32 v160, 0\n"
"v_subrev_f32 v24, s22, v24\n"
"v_mfma_f32_16x16x32_bf16 v[12:15], v[104:107], a[56:59], v[12:15]\n"
"s_waitcnt lgkmcnt(5)\n"
"v_mfma_f32_16x16x32_bf16 v[12:15], v[108:111], a[60:63], v[12:15]\n"
"v_mfma_f32_16x16x32_bf16 v[12:15], v[112:115], a[0:3], v[12:15]\n"
"s_waitcnt lgkmcnt(3)\n"
"v_cvt_pk_bf16_f32 v8, v8, v9\n"
"v_mfma_f32_16x16x32_bf16 v[12:15], v[116:119], a[4:7], v[12:15]\n"
"s_waitcnt lgkmcnt(2)\n"
"v_mfma_f32_16x16x32_bf16 v[12:15], v[120:123], a[8:11], v[12:15]\n"
"s_waitcnt lgkmcnt(0)\n"
"v_mov_b32 v161, 0\n"
"v_subrev_f32 v25, s22, v25\n"
"v_mfma_f32_16x16x32_bf16 v[12:15], v[224:227], a[12:15], v[12:15]\n"
"v_subrev_f32 v26, s22, v26\n"
"s_waitcnt vmcnt(5)\n"
"v_mfma_f32_16x16x32_bf16 v[64:67], v[162:165], v[72:75], 0\n"
"v_subrev_f32 v40, s22, v40\n"
"v_subrev_f32 v43, s22, v43\n"
"v_mfma_f32_16x16x32_bf16 v[64:67], v[162:165], v[76:79], v[64:67]\n"
"v_subrev_f32 v41, s22, v41\n"
"v_mfma_f32_16x16x32_bf16 v[64:67], v[162:165], v[80:83], v[64:67]\n"
"v_mfma_f32_16x16x32_bf16 v[64:67], v[162:165], v[84:87], v[64:67]\n"
"v_subrev_f32 v13, s22, v13\n"
"v_mfma_f32_16x16x32_bf16 v[64:67], v[162:165], v[88:91], v[64:67]\n"
"v_mfma_f32_16x16x32_bf16 v[64:67], v[162:165], v[92:95], v[64:67]\n"
"v_subrev_f32 v15, s22, v15\n"
"v_cvt_pk_bf16_f32 v40, v40, v41\n"
"v_mfma_f32_16x16x32_bf16 v[64:67], v[162:165], v[244:247], v[64:67]\n"
"v_subrev_f32 v14, s22, v14\n"
"v_cvt_pk_bf16_f32 v9, v10, v11\n"
"v_mfma_f32_16x16x32_bf16 v[64:67], v[162:165], v[156:159], v[64:67]\n"
"v_subrev_f32 v12, s22, v12\n"
"v_cvt_pk_bf16_f32 v24, v24, v25\n"
"v_mfma_f32_16x16x32_bf16 v[68:71], v[160:163], v[72:75], 0\n"
"v_cvt_pk_bf16_f32 v10, v12, v13\n"
"v_cvt_pk_bf16_f32 v11, v14, v15\n"
"v_mfma_f32_16x16x32_bf16 v[68:71], v[160:163], v[76:79], v[68:71]\n"
"s_nop 0\n"
"v_permlane16_swap_b32 v9, v11\n"
"v_mfma_f32_16x16x32_bf16 v[68:71], v[160:163], v[80:83], v[68:71]\n"
"v_cvt_pk_bf16_f32 v64, v64, v65\n"
"v_mfma_f32_16x16x32_bf16 v[68:71], v[160:163], v[84:87], v[68:71]\n"
"v_permlane16_swap_b32 v8, v10\n"
"v_mfma_f32_16x16x32_bf16 v[68:71], v[160:163], v[88:91], v[68:71]\n"
"v_subrev_f32 v42, s22, v42\n"
"v_mfma_f32_16x16x32_bf16 v[68:71], v[160:163], v[92:95], v[68:71]\n"
"buffer_store_dwordx4 v[8:11], v239, s[36:39], 0 offen sc0 sc1\n"
"v_mfma_f32_16x16x32_bf16 v[68:71], v[160:163], v[244:247], v[68:71]\n"
"v_mfma_f32_16x16x32_bf16 v[68:71], v[160:163], v[156:159], v[68:71]\n"
"v_cvt_pk_bf16_f32 v65, v66, v67\n"
"v_mfma_f32_16x16x32_bf16 v[28:31], v[224:227], a[28:31], v[28:31]\n"
"v_subrev_f32 v27, s22, v27\n"
"v_mfma_f32_16x16x32_bf16 v[28:31], v[120:123], a[24:27], v[28:31]\n"
"v_mfma_f32_16x16x32_bf16 v[28:31], v[116:119], a[20:23], v[28:31]\n"
"v_mfma_f32_16x16x32_bf16 v[28:31], v[112:115], a[16:19], v[28:31]\n"
"s_nop 1\n"
"v_cvt_pk_bf16_f32 v66, v68, v69\n"
"v_cvt_pk_bf16_f32 v25, v26, v27\n"
"v_mfma_f32_16x16x32_bf16 v[28:31], v[108:111], a[76:79], v[28:31]\n"
"v_permlane16_swap_b32 v64, v66\n"
"v_mfma_f32_16x16x32_bf16 v[28:31], v[104:107], a[72:75], v[28:31]\n"
"v_cvt_pk_bf16_f32 v67, v70, v71\n"
"v_cvt_pk_bf16_f32 v41, v42, v43\n"
"v_mfma_f32_16x16x32_bf16 v[28:31], v[100:103], a[68:71], v[28:31]\n"
"v_permlane16_swap_b32 v65, v67\n"
"v_mfma_f32_16x16x32_bf16 v[28:31], v[96:99], a[64:67], v[28:31]\n"
"buffer_store_dwordx4 v[64:67], v253, s[36:39], 0 offen offset:128 sc0 sc1\n"
"v_mfma_f32_16x16x32_bf16 v[44:47], v[96:99], a[80:83], v[44:47]\n"
"v_mfma_f32_16x16x32_bf16 v[44:47], v[100:103], a[84:87], v[44:47]\n"
"v_mfma_f32_16x16x32_bf16 v[44:47], v[104:107], a[88:91], v[44:47]\n"
"s_nop 3\n"
"v_subrev_f32 v29, s22, v29\n"
"v_subrev_f32 v30, s22, v30\n"
"v_mfma_f32_16x16x32_bf16 v[44:47], v[108:111], a[92:95], v[44:47]\n"
"v_subrev_f32 v28, s22, v28\n"
"v_subrev_f32 v31, s22, v31\n"
"v_mfma_f32_16x16x32_bf16 v[44:47], v[112:115], a[32:35], v[44:47]\n"
"v_cvt_pk_bf16_f32 v26, v28, v29\n"
"v_cvt_pk_bf16_f32 v27, v30, v31\n"
"v_mfma_f32_16x16x32_bf16 v[44:47], v[116:119], a[36:39], v[44:47]\n"
"v_permlane16_swap_b32 v24, v26\n"
"v_mfma_f32_16x16x32_bf16 v[44:47], v[120:123], a[40:43], v[44:47]\n"
"v_permlane16_swap_b32 v25, v27\n"
"v_mfma_f32_16x16x32_bf16 v[44:47], v[224:227], a[44:47], v[44:47]\n"
"buffer_store_dwordx4 v[24:27], v239, s[36:39], s52 offen sc0 sc1\n"
"s_nop 6\n"
"v_subrev_f32 v44, s22, v44\n"
"v_subrev_f32 v45, s22, v45\n"
"v_subrev_f32 v46, s22, v46\n"
"v_subrev_f32 v47, s22, v47\n"
"v_cvt_pk_bf16_f32 v42, v44, v45\n"
"v_cvt_pk_bf16_f32 v43, v46, v47\n"
"s_nop 0\n"
"v_permlane16_swap_b32 v40, v42\n"
"v_permlane16_swap_b32 v41, v43\n"
"buffer_store_dwordx4 v[40:43], v239, s[36:39], s53 offen sc0 sc1\n"
"s_branch LFIN_%=\n"
"LSW_%=:\n"
"s_add_u32 s16, s8, s95\n"
"s_addc_u32 s17, s9, 0\n"
"s_sub_u32 s18, 655360, s95\n"
"s_add_u32 s20, s8, s100\n"
"s_addc_u32 s21, s9, 0\n"
"s_sub_u32 s22, 655360, s100\n"
"s_mov_b32 s56, 0\n"
"s_mov_b32 s55, 0\n"
"s_mov_b32 s58, 0\n"
"s_mov_b32 s59, 163840\n"
"s_mov_b32 s52, 9216\n"
"s_mov_b32 s53, 18432\n"
"s_mov_b32 s54, 27648\n"
"v_lshrrev_b32 v206, 7, v242\n"
"v_mul_u32_u24 v206, 1280, v206\n"
"v_and_b32 v240, 0x7f, v240\n"
"v_add_u32 v240, v206, v240\n"
"buffer_load_dword v252, v240, s[16:19], 0 offen\n"
"s_branch LSWK_%=\n"
"LGP_%=:\n"
"s_mov_b32 s80, 0x40000\n"
"LGPa_%=:\n"
"s_sleep 1\n"
"global_load_dword v200, v201, s[6:7] sc1\n"
"s_sub_u32 s80, s80, 1\n"
"s_cmp_eq_u32 s80, 0\n"
"s_waitcnt vmcnt(0)\n"
"s_cbranch_scc1 LGPK_%=\n"
"v_subrev_u32 v200, s21, v200\n"
"v_cmp_lt_u32 vcc, s69, v200\n"
"s_nop 4\n"
"s_cbranch_vccnz LGPa_%=\n"
"s_branch LGPK_%=\n"
"LGQ_%=:\n"
"s_mov_b32 s80, 0x40000\n"
"LGQa_%=:\n"
"s_sleep 1\n"
"global_load_dword v200, v201, s[6:7] sc1\n"
"s_sub_u32 s80, s80, 1\n"
"s_cmp_eq_u32 s80, 0\n"
"s_waitcnt vmcnt(0)\n"
"s_cbranch_scc1 LGQK_%=\n"
"v_subrev_u32 v200, s21, v200\n"
"v_cmp_lt_u32 vcc, s69, v200\n"
"s_nop 4\n"
"s_cbranch_vccnz LGQa_%=\n"
"s_branch LGQK_%=\n"
"LGR_%=:\n"
"s_mov_b32 s80, 0x40000\n"
"LGRa_%=:\n"
"s_sleep 1\n"
"global_load_dword v200, v201, s[6:7] sc1\n"
"s_sub_u32 s80, s80, 1\n"
"s_cmp_eq_u32 s80, 0\n"
"s_waitcnt vmcnt(0)\n"
"s_cbranch_scc1 LGRK_%=\n"
"v_subrev_u32 v200, s21, v200\n"
"v_cmp_lt_u32 vcc, s69, v200\n"
"s_nop 4\n"
"s_cbranch_vccnz LGRa_%=\n"
"s_branch LGRK_%=\n"
"LGV_%=:\n"
"s_mov_b32 s80, 0x40000\n"
"LGVa_%=:\n"
"s_sleep 1\n"
"global_load_dword v200, v201, s[6:7] sc1\n"
"s_sub_u32 s80, s80, 1\n"
"s_cmp_eq_u32 s80, 0\n"
"s_waitcnt vmcnt(0)\n"
"s_cbranch_scc1 LGVK_%=\n"
"v_subrev_u32 v200, s21, v200\n"
"v_cmp_lt_u32 vcc, s69, v200\n"
"s_nop 4\n"
"s_cbranch_vccnz LGVa_%=\n"
"s_branch LGVK_%=\n"
"LEP_%=:\n"
"s_add_u32 s92, s92, 1\n"
"v_mov_b32 v200, s92\n"
"v_mov_b32 v255, 0\n"
"s_mov_b64 exec, 1\n"
"global_store_dword v255, v200, s[6:7] offset:128 sc1\n"
"s_mov_b64 exec, -1\n"
"s_branch LEPK_%=\n"
"LPCE0_%=:\n"
"s_memrealtime s[80:81]\n"
"s_sub_u32 s82, s67, s66\n"
"s_cmp_gt_i32 s82, 0\n"
"s_cselect_b32 s83, s67, s66\n"
"s_waitcnt lgkmcnt(0)\n"
"s_mov_b32 s63, 0\n"
"s_mov_b32 s62, 126\n"
"s_sub_u32 s82, s83, s80\n"
"s_cmp_gt_i32 s82, 3\n"
"s_cbranch_scc0 LPEE0_%=\n"
"LPDE0_%=:\n"
"s_sleep 1\n"
"s_memrealtime s[80:81]\n"
"s_waitcnt lgkmcnt(0)\n"
"s_sub_u32 s82, s83, s80\n"
"s_cmp_gt_i32 s82, 3\n"
"s_cbranch_scc1 LPDE0_%=\n"
"LPEE0_%=:\n"
"s_add_u32 s66, s66, s72\n"
"s_add_u32 s66, s66, 4\n"
"s_add_u32 s67, s80, s72\n"
"s_sub_u32 s72, s72, 0\n"
"s_max_u32 s72, s72, s62\n"
"s_branch LPKE0_%=\n"
"LPCE1_%=:\n"
"s_memrealtime s[80:81]\n"
"s_sub_u32 s82, s67, s66\n"
"s_cmp_gt_i32 s82, 0\n"
"s_cselect_b32 s83, s67, s66\n"
"s_waitcnt lgkmcnt(0)\n"
"s_sub_u32 s82, s83, s80\n"
"s_cmp_gt_i32 s82, 3\n"
"s_cbranch_scc0 LPEE1_%=\n"
"LPDE1_%=:\n"
"s_sleep 1\n"
"s_memrealtime s[80:81]\n"
"s_waitcnt lgkmcnt(0)\n"
"s_sub_u32 s82, s83, s80\n"
"s_cmp_gt_i32 s82, 3\n"
"s_cbranch_scc1 LPDE1_%=\n"
"LPEE1_%=:\n"
"s_add_u32 s66, s66, s72\n"
"s_add_u32 s66, s66, 4\n"
"s_add_u32 s67, s80, s72\n"
"s_sub_u32 s72, s72, 0\n"
"s_max_u32 s72, s72, s62\n"
"s_branch LPKE1_%=\n"
"LPCT0_%=:\n"
"s_memrealtime s[80:81]\n"
"s_sub_u32 s82, s67, s66\n"
"s_cmp_gt_i32 s82, 0\n"
"s_cselect_b32 s83, s67, s66\n"
"s_waitcnt lgkmcnt(0)\n"
"s_sub_u32 s82, s83, s80\n"
"s_cmp_gt_i32 s82, 3\n"
"s_cbranch_scc0 LPET0_%=\n"
"LPDT0_%=:\n"
"s_sleep 1\n"
"s_memrealtime s[80:81]\n"
"s_waitcnt lgkmcnt(0)\n"
"s_sub_u32 s82, s83, s80\n"
"s_cmp_gt_i32 s82, 3\n"
"s_cbranch_scc1 LPDT0_%=\n"
"LPET0_%=:\n"
"s_add_u32 s66, s66, s72\n"
"s_add_u32 s66, s66, 4\n"
"s_add_u32 s67, s80, s72\n"
"s_sub_u32 s72, s72, 0\n"
"s_max_u32 s72, s72, s62\n"
"s_branch LPKT0_%=\n"
"LPCT1_%=:\n"
"s_memrealtime s[80:81]\n"
"s_sub_u32 s82, s67, s66\n"
"s_cmp_gt_i32 s82, 0\n"
"s_cselect_b32 s83, s67, s66\n"
"s_waitcnt lgkmcnt(0)\n"
"s_sub_u32 s82, s83, s80\n"
"s_cmp_gt_i32 s82, 3\n"
"s_cbranch_scc0 LPET1_%=\n"
"LPDT1_%=:\n"
"s_sleep 1\n"
"s_memrealtime s[80:81]\n"
"s_waitcnt lgkmcnt(0)\n"
"s_sub_u32 s82, s83, s80\n"
"s_cmp_gt_i32 s82, 3\n"
"s_cbranch_scc1 LPDT1_%=\n"
"LPET1_%=:\n"
"s_add_u32 s66, s66, s72\n"
"s_add_u32 s66, s66, 4\n"
"s_add_u32 s67, s80, s72\n"
"s_sub_u32 s72, s72, 0\n"
"s_max_u32 s72, s72, s62\n"
"s_branch LPKT1_%=\n"
"LPCL0_%=:\n"
"s_memrealtime s[80:81]\n"
"s_sub_u32 s82, s67, s66\n"
"s_cmp_gt_i32 s82, 0\n"
"s_cselect_b32 s83, s67, s66\n"
"s_waitcnt lgkmcnt(0)\n"
"s_sub_u32 s82, s80, s67\n"
"s_add_u32 s63, s63, 1\n"
"s_min_i32 s81, s63, 0\n"
"s_cmp_gt_i32 s82, 20\n"
"s_cselect_b32 s63, s63, s81\n"
"s_cmp_ge_i32 s63, 2\n"
"s_cbranch_scc0 LPRL0_%=\n"
"s_mov_b32 s63, -9\n"
"s_add_u32 s62, s62, 10\n"
"s_min_u32 s62, s62, 146\n"
"s_max_u32 s72, s72, s62\n"
"s_add_u32 s83, s80, 200\n"
"s_and_b32 s83, s83, -1\n"
"LPRL0_%=:\n"
"s_sub_u32 s82, s83, s80\n"
"s_cmp_gt_i32 s82, 3\n"
"s_cbranch_scc0 LPEL0_%=\n"
"LPDL0_%=:\n"
"s_sleep 1\n"
"s_memrealtime s[80:81]\n"
"s_waitcnt lgkmcnt(0)\n"
"s_sub_u32 s82, s83, s80\n"
"s_cmp_gt_i32 s82, 3\n"
"s_cbranch_scc1 LPDL0_%=\n"
"LPEL0_%=:\n"
"s_add_u32 s66, s66, s72\n"
"s_add_u32 s66, s66, 4\n"
"s_add_u32 s67, s80, s72\n"
"s_sub_u32 s72, s72, 0\n"
"s_max_u32 s72, s72, s62\n"
"s_branch LPKL0_%=\n"
"LPCL1_%=:\n"
"s_memrealtime s[80:81]\n"
"s_sub_u32 s82, s67, s66\n"
"s_cmp_gt_i32 s82, 0\n"
"s_cselect_b32 s83, s67, s66\n"
"s_waitcnt lgkmcnt(0)\n"
"s_sub_u32 s82, s80, s67\n"
"s_add_u32 s63, s63, 1\n"
"s_min_i32 s81, s63, 0\n"
"s_cmp_gt_i32 s82, 20\n"
"s_cselect_b32 s63, s63, s81\n"
"s_cmp_ge_i32 s63, 2\n"
"s_cbranch_scc0 LPRL1_%=\n"
"s_mov_b32 s63, -9\n"
"s_add_u32 s62, s62, 10\n"
"s_min_u32 s62, s62, 146\n"
"s_max_u32 s72, s72, s62\n"
"s_add_u32 s83, s80, 200\n"
"s_and_b32 s83, s83, -1\n"
"LPRL1_%=:\n"
"s_sub_u32 s82, s83, s80\n"
"s_cmp_gt_i32 s82, 3\n"
"s_cbranch_scc0 LPEL1_%=\n"
"LPDL1_%=:\n"
"s_sleep 1\n"
"s_memrealtime s[80:81]\n"
"s_waitcnt lgkmcnt(0)\n"
"s_sub_u32 s82, s83, s80\n"
"s_cmp_gt_i32 s82, 3\n"
"s_cbranch_scc1 LPDL1_%=\n"
"LPEL1_%=:\n"
"s_add_u32 s66, s66, s72\n"
"s_add_u32 s66, s66, 4\n"
"s_add_u32 s67, s80, s72\n"
"s_sub_u32 s72, s72, 0\n"
"s_max_u32 s72, s72, s62\n"
"s_branch LPKL1_%=\n"
"LPCL2_%=:\n"
"s_memrealtime s[80:81]\n"
"s_sub_u32 s82, s67, s66\n"
"s_cmp_gt_i32 s82, 0\n"
"s_cselect_b32 s83, s67, s66\n"
"s_waitcnt lgkmcnt(0)\n"
"s_sub_u32 s82, s80, s67\n"
"s_add_u32 s63, s63, 1\n"
"s_min_i32 s81, s63, 0\n"
"s_cmp_gt_i32 s82, 20\n"
"s_cselect_b32 s63, s63, s81\n"
"s_cmp_ge_i32 s63, 2\n"
"s_cbranch_scc0 LPRL2_%=\n"
"s_mov_b32 s63, -9\n"
"s_add_u32 s62, s62, 10\n"
"s_min_u32 s62, s62, 146\n"
"s_max_u32 s72, s72, s62\n"
"s_add_u32 s83, s80, 200\n"
"s_and_b32 s83, s83, -1\n"
"LPRL2_%=:\n"
"s_sub_u32 s82, s83, s80\n"
"s_cmp_gt_i32 s82, 3\n"
"s_cbranch_scc0 LPEL2_%=\n"
"LPDL2_%=:\n"
"s_sleep 1\n"
"s_memrealtime s[80:81]\n"
"s_waitcnt lgkmcnt(0)\n"
"s_sub_u32 s82, s83, s80\n"
"s_cmp_gt_i32 s82, 3\n"
"s_cbranch_scc1 LPDL2_%=\n"
"LPEL2_%=:\n"
"s_add_u32 s66, s66, s72\n"
"s_add_u32 s66, s66, 4\n"
"s_add_u32 s67, s80, s72\n"
"s_sub_u32 s72, s72, 0\n"
"s_max_u32 s72, s72, s62\n"
"s_branch LPKL2_%=\n"
"LPCL3_%=:\n"
"s_memrealtime s[80:81]\n"
"s_sub_u32 s82, s67, s66\n"
"s_cmp_gt_i32 s82, 0\n"
"s_cselect_b32 s83, s67, s66\n"
"s_waitcnt lgkmcnt(0)\n"
"s_sub_u32 s82, s80, s67\n"
"s_add_u32 s63, s63, 1\n"
"s_min_i32 s81, s63, 0\n"
"s_cmp_gt_i32 s82, 20\n"
"s_cselect_b32 s63, s63, s81\n"
"s_cmp_ge_i32 s63, 2\n"
"s_cbranch_scc0 LPRL3_%=\n"
"s_mov_b32 s63, -9\n"
"s_add_u32 s62, s62, 10\n"
"s_min_u32 s62, s62, 146\n"
"s_max_u32 s72, s72, s62\n"
"s_add_u32 s83, s80, 200\n"
"s_and_b32 s83, s83, -1\n"
"LPRL3_%=:\n"
"s_sub_u32 s82, s83, s80\n"
"s_cmp_gt_i32 s82, 3\n"
"s_cbranch_scc0 LPEL3_%=\n"
"LPDL3_%=:\n"
"s_sleep 1\n"
"s_memrealtime s[80:81]\n"
"s_waitcnt lgkmcnt(0)\n"
"s_sub_u32 s82, s83, s80\n"
"s_cmp_gt_i32 s82, 3\n"
"s_cbranch_scc1 LPDL3_%=\n"
"LPEL3_%=:\n"
"s_add_u32 s66, s66, s72\n"
"s_add_u32 s66, s66, 4\n"
"s_add_u32 s67, s80, s72\n"
"s_sub_u32 s72, s72, 0\n"
"s_max_u32 s72, s72, s62\n"
"s_branch LPKL3_%=\n"
"LEND_%=:\n"
"LSLOW_%=:\n"
"s_waitcnt vmcnt(0) lgkmcnt(0)\n"
"s_nop 9\n"
"v_bfe_u32 v148, v242, 4, 4\n"
"v_bfe_u32 v149, v242, 8, 2\n"
"v_lshlrev_b32 v144, 8, v148\n"
"v_lshl_add_u32 v144, v149, 4, v144\n"
"v_add_u32 v144, s43, v144\n"
"v_xor_b32 v145, 64, v144\n"
"v_lshlrev_b32 v146, 1, v148\n"
"v_lshl_add_u32 v146, v149, 10, v146\n"
"v_add_u32 v146, s43, v146\n"
"v_xor_b32 v147, 64, v146\n"
"v_mov_b32 v152, 0xff7fffff\n"
"v_mov_b32 v155, 0\n"
"v_mov_b32 v0, 0\n"
"v_mov_b32 v1, 0\n"
"v_mov_b32 v2, 0\n"
"v_mov_b32 v3, 0\n"
"v_mov_b32 v4, 0\n"
"v_mov_b32 v5, 0\n"
"v_mov_b32 v6, 0\n"
"v_mov_b32 v7, 0\n"
"v_mov_b32 v8, 0\n"
"v_mov_b32 v9, 0\n"
"v_mov_b32 v10, 0\n"
"v_mov_b32 v11, 0\n"
"v_mov_b32 v12, 0\n"
"v_mov_b32 v13, 0\n"
"v_mov_b32 v14, 0\n"
"v_mov_b32 v15, 0\n"
"v_mov_b32 v16, 0\n"
"v_mov_b32 v17, 0\n"
"v_mov_b32 v18, 0\n"
"v_mov_b32 v19, 0\n"
"v_mov_b32 v20, 0\n"
"v_mov_b32 v21, 0\n"
"v_mov_b32 v22, 0\n"
"v_mov_b32 v23, 0\n"
"v_mov_b32 v24, 0\n"
"v_mov_b32 v25, 0\n"
"v_mov_b32 v26, 0\n"
"v_mov_b32 v27, 0\n"
"v_mov_b32 v28, 0\n"
"v_mov_b32 v29, 0\n"
"v_mov_b32 v30, 0\n"
"v_mov_b32 v31, 0\n"
"v_mov_b32 v156, 0xff7fffff\n"
"v_mov_b32 v159, 0\n"
"v_mov_b32 v32, 0\n"
"v_mov_b32 v33, 0\n"
"v_mov_b32 v34, 0\n"
"v_mov_b32 v35, 0\n"
"v_mov_b32 v36, 0\n"
"v_mov_b32 v37, 0\n"
"v_mov_b32 v38, 0\n"
"v_mov_b32 v39, 0\n"
"v_mov_b32 v40, 0\n"
"v_mov_b32 v41, 0\n"
"v_mov_b32 v42, 0\n"
"v_mov_b32 v43, 0\n"
"v_mov_b32 v44, 0\n"
"v_mov_b32 v45, 0\n"
"v_mov_b32 v46, 0\n"
"v_mov_b32 v47, 0\n"
"v_mov_b32 v48, 0\n"
"v_mov_b32 v49, 0\n"
"v_mov_b32 v50, 0\n"
"v_mov_b32 v51, 0\n"
"v_mov_b32 v52, 0\n"
"v_mov_b32 v53, 0\n"
"v_mov_b32 v54, 0\n"
"v_mov_b32 v55, 0\n"
"v_mov_b32 v56, 0\n"
"v_mov_b32 v57, 0\n"
"v_mov_b32 v58, 0\n"
"v_mov_b32 v59, 0\n"
"v_mov_b32 v60, 0\n"
"v_mov_b32 v61, 0\n"
"v_mov_b32 v62, 0\n"
"v_mov_b32 v63, 0\n"
"v_mov_b32 v160, 0xff7fffff\n"
"v_mov_b32 v163, 0\n"
"v_mov_b32 v64, 0\n"
"v_mov_b32 v65, 0\n"
"v_mov_b32 v66, 0\n"
"v_mov_b32 v67, 0\n"
"v_mov_b32 v68, 0\n"
"v_mov_b32 v69, 0\n"
"v_mov_b32 v70, 0\n"
"v_mov_b32 v71, 0\n"
"v_mov_b32 v72, 0\n"
"v_mov_b32 v73, 0\n"
"v_mov_b32 v74, 0\n"
"v_mov_b32 v75, 0\n"
"v_mov_b32 v76, 0\n"
"v_mov_b32 v77, 0\n"
"v_mov_b32 v78, 0\n"
"v_mov_b32 v79, 0\n"
"v_mov_b32 v80, 0\n"
"v_mov_b32 v81, 0\n"
"v_mov_b32 v82, 0\n"
"v_mov_b32 v83, 0\n"
"v_mov_b32 v84, 0\n"
"v_mov_b32 v85, 0\n"
"v_mov_b32 v86, 0\n"
"v_mov_b32 v87, 0\n"
"v_mov_b32 v88, 0\n"
"v_mov_b32 v89, 0\n"
"v_mov_b32 v90, 0\n"
"v_mov_b32 v91, 0\n"
"v_mov_b32 v92, 0\n"
"v_mov_b32 v93, 0\n"
"v_mov_b32 v94, 0\n"
"v_mov_b32 v95, 0\n"
"v_mov_b32 v164, 0xff7fffff\n"
"v_mov_b32 v167, 0\n"
"v_accvgpr_write_b32 a208, 0\n"
"v_accvgpr_write_b32 a209, 0\n"
"v_accvgpr_write_b32 a210, 0\n"
"v_accvgpr_write_b32 a211, 0\n"
"v_accvgpr_write_b32 a212, 0\n"
"v_accvgpr_write_b32 a213, 0\n"
"v_accvgpr_write_b32 a214, 0\n"
"v_accvgpr_write_b32 a215, 0\n"
"v_accvgpr_write_b32 a216, 0\n"
"v_accvgpr_write_b32 a217, 0\n"
"v_accvgpr_write_b32 a218, 0\n"
"v_accvgpr_write_b32 a219, 0\n"
"v_accvgpr_write_b32 a220, 0\n"
"v_accvgpr_write_b32 a221, 0\n"
"v_accvgpr_write_b32 a222, 0\n"
"v_accvgpr_write_b32 a223, 0\n"
"v_accvgpr_write_b32 a224, 0\n"
"v_accvgpr_write_b32 a225, 0\n"
"v_accvgpr_write_b32 a226, 0\n"
"v_accvgpr_write_b32 a227, 0\n"
"v_accvgpr_write_b32 a228, 0\n"
"v_accvgpr_write_b32 a229, 0\n"
"v_accvgpr_write_b32 a230, 0\n"
"v_accvgpr_write_b32 a231, 0\n"
"v_accvgpr_write_b32 a232, 0\n"
"v_accvgpr_write_b32 a233, 0\n"
"v_accvgpr_write_b32 a234, 0\n"
"v_accvgpr_write_b32 a235, 0\n"
"v_accvgpr_write_b32 a236, 0\n"
"v_accvgpr_write_b32 a237, 0\n"
"v_accvgpr_write_b32 a238, 0\n"
"v_accvgpr_write_b32 a239, 0\n"
"s_lshl_b32 s80, s47, 16\n"
"s_sub_u32 s80, s80, 261248\n"
"s_add_u32 s81, s80, 4096\n"
"s_mov_b32 s82, 0\n"
"LSLOOP_%=:\n"
"s_lshl_b32 s83, s82, 6\n"
"s_and_b32 s83, s83, 64\n"
"buffer_load_dwordx4 v[128:131], v144, s[84:87], s80 offen offset:0\n"
"buffer_load_dwordx4 v[132:135], v145, s[84:87], s80 offen offset:0\n"
"buffer_load_dwordx4 v[136:139], v144, s[84:87], s80 offen offset:128\n"
"buffer_load_dwordx4 v[140:143], v145, s[84:87], s80 offen offset:128\n"
"s_waitcnt vmcnt(0)\n"
"v_mfma_f32_16x16x32_bf16 v[96:99], v[128:131], a[0:3], 0\n"
"v_mfma_f32_16x16x32_bf16 v[96:99], v[132:135], a[4:7], v[96:99]\n"
"v_mfma_f32_16x16x32_bf16 v[96:99], v[136:139], a[8:11], v[96:99]\n"
"v_mfma_f32_16x16x32_bf16 v[96:99], v[140:143], a[12:15], v[96:99]\n"
"v_mfma_f32_16x16x32_bf16 v[104:107], v[128:131], a[16:19], 0\n"
"v_mfma_f32_16x16x32_bf16 v[104:107], v[132:135], a[20:23], v[104:107]\n"
"v_mfma_f32_16x16x32_bf16 v[104:107], v[136:139], a[24:27], v[104:107]\n"
"v_mfma_f32_16x16x32_bf16 v[104:107], v[140:143], a[28:31], v[104:107]\n"
"v_mfma_f32_16x16x32_bf16 v[112:115], v[128:131], a[32:35], 0\n"
"v_mfma_f32_16x16x32_bf16 v[112:115], v[132:135], a[36:39], v[112:115]\n"
"v_mfma_f32_16x16x32_bf16 v[112:115], v[136:139], a[40:43], v[112:115]\n"
"v_mfma_f32_16x16x32_bf16 v[112:115], v[140:143], a[44:47], v[112:115]\n"
"v_mfma_f32_16x16x32_bf16 v[120:123], v[128:131], a[192:195], 0\n"
"v_mfma_f32_16x16x32_bf16 v[120:123], v[132:135], a[196:199], v[120:123]\n"
"v_mfma_f32_16x16x32_bf16 v[120:123], v[136:139], a[200:203], v[120:123]\n"
"v_mfma_f32_16x16x32_bf16 v[120:123], v[140:143], a[204:207], v[120:123]\n"
"s_nop 7\n"
"buffer_load_dwordx4 v[128:131], v144, s[84:87], s81 offen offset:0\n"
"buffer_load_dwordx4 v[132:135], v145, s[84:87], s81 offen offset:0\n"
"buffer_load_dwordx4 v[136:139], v144, s[84:87], s81 offen offset:128\n"
"buffer_load_dwordx4 v[140:143], v145, s[84:87], s81 offen offset:128\n"
"s_waitcnt vmcnt(0)\n"
"v_mfma_f32_16x16x32_bf16 v[100:103], v[128:131], a[0:3], 0\n"
"v_mfma_f32_16x16x32_bf16 v[100:103], v[132:135], a[4:7], v[100:103]\n"
"v_mfma_f32_16x16x32_bf16 v[100:103], v[136:139], a[8:11], v[100:103]\n"
"v_mfma_f32_16x16x32_bf16 v[100:103], v[140:143], a[12:15], v[100:103]\n"
"v_mfma_f32_16x16x32_bf16 v[108:111], v[128:131], a[16:19], 0\n"
"v_mfma_f32_16x16x32_bf16 v[108:111], v[132:135], a[20:23], v[108:111]\n"
"v_mfma_f32_16x16x32_bf16 v[108:111], v[136:139], a[24:27], v[108:111]\n"
"v_mfma_f32_16x16x32_bf16 v[108:111], v[140:143], a[28:31], v[108:111]\n"
"v_mfma_f32_16x16x32_bf16 v[116:119], v[128:131], a[32:35], 0\n"
"v_mfma_f32_16x16x32_bf16 v[116:119], v[132:135], a[36:39], v[116:119]\n"
"v_mfma_f32_16x16x32_bf16 v[116:119], v[136:139], a[40:43], v[116:119]\n"
"v_mfma_f32_16x16x32_bf16 v[116:119], v[140:143], a[44:47], v[116:119]\n"
"v_mfma_f32_16x16x32_bf16 v[124:127], v[128:131], a[192:195], 0\n"
"v_mfma_f32_16x16x32_bf16 v[124:127], v[132:135], a[196:199], v[124:127]\n"
"v_mfma_f32_16x16x32_bf16 v[124:127], v[136:139], a[200:203], v[124:127]\n"
"v_mfma_f32_16x16x32_bf16 v[124:127], v[140:143], a[204:207], v[124:127]\n"
"s_nop 7\n"
"v_max3_f32 v153, v96, v97, v98\n"
"v_max3_f32 v153, v153, v99, v100\n"
"v_max3_f32 v153, v153, v101, v102\n"
"v_max3_f32 v153, v153, v103, v152\n"
"v_mov_b32 v154, v153\n"
"s_nop 1\n"
"v_permlane32_swap_b32 v153, v154\n"
"v_max_f32 v153, v153, v154\n"
"v_mov_b32 v154, v153\n"
"s_nop 1\n"
"v_permlane16_swap_b32 v153, v154\n"
"v_max_f32 v153, v153, v154\n"
"v_sub_f32 v154, v152, v153\n"
"v_exp_f32 v154, v154\n"
"v_mov_b32 v152, v153\n"
"v_sub_f32 v96, v96, v152\n"
"v_sub_f32 v97, v97, v152\n"
"v_sub_f32 v98, v98, v152\n"
"v_sub_f32 v99, v99, v152\n"
"v_sub_f32 v100, v100, v152\n"
"v_sub_f32 v101, v101, v152\n"
"v_sub_f32 v102, v102, v152\n"
"v_sub_f32 v103, v103, v152\n"
"v_exp_f32 v96, v96\n"
"v_exp_f32 v97, v97\n"
"v_exp_f32 v98, v98\n"
"v_exp_f32 v99, v99\n"
"v_exp_f32 v100, v100\n"
"v_exp_f32 v101, v101\n"
"v_exp_f32 v102, v102\n"
"v_exp_f32 v103, v103\n"
"v_mul_f32 v155, v155, v154\n"
"v_add_f32 v155, v155, v96\n"
"v_add_f32 v155, v155, v97\n"
"v_add_f32 v155, v155, v98\n"
"v_add_f32 v155, v155, v99\n"
"v_add_f32 v155, v155, v100\n"
"v_add_f32 v155, v155, v101\n"
"v_add_f32 v155, v155, v102\n"
"v_add_f32 v155, v155, v103\n"
"v_cvt_pk_bf16_f32 v96, v96, v97\n"
"v_cvt_pk_bf16_f32 v97, v98, v99\n"
"v_cvt_pk_bf16_f32 v98, v100, v101\n"
"v_cvt_pk_bf16_f32 v99, v102, v103\n"
"v_mov_b32_dpp v154, v154 row_ror:12 row_mask:0x2 bank_mask:0xf\n"
"s_nop 1\n"
"v_mov_b32_dpp v154, v154 row_ror:8 row_mask:0x4 bank_mask:0xf\n"
"s_nop 1\n"
"v_mov_b32_dpp v154, v154 row_ror:4 row_mask:0x8 bank_mask:0xf\n"
"s_nop 1\n"
"v_mul_f32_dpp v0, v154, v0 row_newbcast:0 row_mask:0xf bank_mask:0xf\n"
"v_mul_f32_dpp v1, v154, v1 row_newbcast:1 row_mask:0xf bank_mask:0xf\n"
"v_mul_f32_dpp v2, v154, v2 row_newbcast:2 row_mask:0xf bank_mask:0xf\n"
"v_mul_f32_dpp v3, v154, v3 row_newbcast:3 row_mask:0xf bank_mask:0xf\n"
"v_mul_f32_dpp v4, v154, v4 row_newbcast:0 row_mask:0xf bank_mask:0xf\n"
"v_mul_f32_dpp v5, v154, v5 row_newbcast:1 row_mask:0xf bank_mask:0xf\n"
"v_mul_f32_dpp v6, v154, v6 row_newbcast:2 row_mask:0xf bank_mask:0xf\n"
"v_mul_f32_dpp v7, v154, v7 row_newbcast:3 row_mask:0xf bank_mask:0xf\n"
"v_mul_f32_dpp v8, v154, v8 row_newbcast:0 row_mask:0xf bank_mask:0xf\n"
"v_mul_f32_dpp v9, v154, v9 row_newbcast:1 row_mask:0xf bank_mask:0xf\n"
"v_mul_f32_dpp v10, v154, v10 row_newbcast:2 row_mask:0xf bank_mask:0xf\n"
"v_mul_f32_dpp v11, v154, v11 row_newbcast:3 row_mask:0xf bank_mask:0xf\n"
"v_mul_f32_dpp v12, v154, v12 row_newbcast:0 row_mask:0xf bank_mask:0xf\n"
"v_mul_f32_dpp v13, v154, v13 row_newbcast:1 row_mask:0xf bank_mask:0xf\n"
"v_mul_f32_dpp v14, v154, v14 row_newbcast:2 row_mask:0xf bank_mask:0xf\n"
"v_mul_f32_dpp v15, v154, v15 row_newbcast:3 row_mask:0xf bank_mask:0xf\n"
"v_mul_f32_dpp v16, v154, v16 row_newbcast:0 row_mask:0xf bank_mask:0xf\n"
"v_mul_f32_dpp v17, v154, v17 row_newbcast:1 row_mask:0xf bank_mask:0xf\n"
"v_mul_f32_dpp v18, v154, v18 row_newbcast:2 row_mask:0xf bank_mask:0xf\n"
"v_mul_f32_dpp v19, v154, v19 row_newbcast:3 row_mask:0xf bank_mask:0xf\n"
"v_mul_f32_dpp v20, v154, v20 row_newbcast:0 row_mask:0xf bank_mask:0xf\n"
"v_mul_f32_dpp v21, v154, v21 row_newbcast:1 row_mask:0xf bank_mask:0xf\n"
"v_mul_f32_dpp v22, v154, v22 row_newbcast:2 row_mask:0xf bank_mask:0xf\n"
"v_mul_f32_dpp v23, v154, v23 row_newbcast:3 row_mask:0xf bank_mask:0xf\n"
"v_mul_f32_dpp v24, v154, v24 row_newbcast:0 row_mask:0xf bank_mask:0xf\n"
"v_mul_f32_dpp v25, v154, v25 row_newbcast:1 row_mask:0xf bank_mask:0xf\n"
"v_mul_f32_dpp v26, v154, v26 row_newbcast:2 row_mask:0xf bank_mask:0xf\n"
"v_mul_f32_dpp v27, v154, v27 row_newbcast:3 row_mask:0xf bank_mask:0xf\n"
"v_mul_f32_dpp v28, v154, v28 row_newbcast:0 row_mask:0xf bank_mask:0xf\n"
"v_mul_f32_dpp v29, v154, v29 row_newbcast:1 row_mask:0xf bank_mask:0xf\n"
"v_mul_f32_dpp v30, v154, v30 row_newbcast:2 row_mask:0xf bank_mask:0xf\n"
"v_mul_f32_dpp v31, v154, v31 row_newbcast:3 row_mask:0xf bank_mask:0xf\n"
"v_max3_f32 v157, v104, v105, v106\n"
"v_max3_f32 v157, v157, v107, v108\n"
"v_max3_f32 v157, v157, v109, v110\n"
"v_max3_f32 v157, v157, v111, v156\n"
"v_mov_b32 v158, v157\n"
"s_nop 1\n"
"v_permlane32_swap_b32 v157, v158\n"
"v_max_f32 v157, v157, v158\n"
"v_mov_b32 v158, v157\n"
"s_nop 1\n"
"v_permlane16_swap_b32 v157, v158\n"
"v_max_f32 v157, v157, v158\n"
"v_sub_f32 v158, v156, v157\n"
"v_exp_f32 v158, v158\n"
"v_mov_b32 v156, v157\n"
"v_sub_f32 v104, v104, v156\n"
"v_sub_f32 v105, v105, v156\n"
"v_sub_f32 v106, v106, v156\n"
"v_sub_f32 v107, v107, v156\n"
"v_sub_f32 v108, v108, v156\n"
"v_sub_f32 v109, v109, v156\n"
"v_sub_f32 v110, v110, v156\n"
"v_sub_f32 v111, v111, v156\n"
"v_exp_f32 v104, v104\n"
"v_exp_f32 v105, v105\n"
"v_exp_f32 v106, v106\n"
"v_exp_f32 v107, v107\n"
"v_exp_f32 v108, v108\n"
"v_exp_f32 v109, v109\n"
"v_exp_f32 v110, v110\n"
"v_exp_f32 v111, v111\n"
"v_mul_f32 v159, v159, v158\n"
"v_add_f32 v159, v159, v104\n"
"v_add_f32 v159, v159, v105\n"
"v_add_f32 v159, v159, v106\n"
"v_add_f32 v159, v159, v107\n"
"v_add_f32 v159, v159, v108\n"
"v_add_f32 v159, v159, v109\n"
"v_add_f32 v159, v159, v110\n"
"v_add_f32 v159, v159, v111\n"
"v_cvt_pk_bf16_f32 v104, v104, v105\n"
"v_cvt_pk_bf16_f32 v105, v106, v107\n"
"v_cvt_pk_bf16_f32 v106, v108, v109\n"
"v_cvt_pk_bf16_f32 v107, v110, v111\n"
"v_mov_b32_dpp v158, v158 row_ror:12 row_mask:0x2 bank_mask:0xf\n"
"s_nop 1\n"
"v_mov_b32_dpp v158, v158 row_ror:8 row_mask:0x4 bank_mask:0xf\n"
"s_nop 1\n"
"v_mov_b32_dpp v158, v158 row_ror:4 row_mask:0x8 bank_mask:0xf\n"
"s_nop 1\n"
"v_mul_f32_dpp v32, v158, v32 row_newbcast:0 row_mask:0xf bank_mask:0xf\n"
"v_mul_f32_dpp v33, v158, v33 row_newbcast:1 row_mask:0xf bank_mask:0xf\n"
"v_mul_f32_dpp v34, v158, v34 row_newbcast:2 row_mask:0xf bank_mask:0xf\n"
"v_mul_f32_dpp v35, v158, v35 row_newbcast:3 row_mask:0xf bank_mask:0xf\n"
"v_mul_f32_dpp v36, v158, v36 row_newbcast:0 row_mask:0xf bank_mask:0xf\n"
"v_mul_f32_dpp v37, v158, v37 row_newbcast:1 row_mask:0xf bank_mask:0xf\n"
"v_mul_f32_dpp v38, v158, v38 row_newbcast:2 row_mask:0xf bank_mask:0xf\n"
"v_mul_f32_dpp v39, v158, v39 row_newbcast:3 row_mask:0xf bank_mask:0xf\n"
"v_mul_f32_dpp v40, v158, v40 row_newbcast:0 row_mask:0xf bank_mask:0xf\n"
"v_mul_f32_dpp v41, v158, v41 row_newbcast:1 row_mask:0xf bank_mask:0xf\n"
"v_mul_f32_dpp v42, v158, v42 row_newbcast:2 row_mask:0xf bank_mask:0xf\n"
"v_mul_f32_dpp v43, v158, v43 row_newbcast:3 row_mask:0xf bank_mask:0xf\n"
"v_mul_f32_dpp v44, v158, v44 row_newbcast:0 row_mask:0xf bank_mask:0xf\n"
"v_mul_f32_dpp v45, v158, v45 row_newbcast:1 row_mask:0xf bank_mask:0xf\n"
"v_mul_f32_dpp v46, v158, v46 row_newbcast:2 row_mask:0xf bank_mask:0xf\n"
"v_mul_f32_dpp v47, v158, v47 row_newbcast:3 row_mask:0xf bank_mask:0xf\n"
"v_mul_f32_dpp v48, v158, v48 row_newbcast:0 row_mask:0xf bank_mask:0xf\n"
"v_mul_f32_dpp v49, v158, v49 row_newbcast:1 row_mask:0xf bank_mask:0xf\n"
"v_mul_f32_dpp v50, v158, v50 row_newbcast:2 row_mask:0xf bank_mask:0xf\n"
"v_mul_f32_dpp v51, v158, v51 row_newbcast:3 row_mask:0xf bank_mask:0xf\n"
"v_mul_f32_dpp v52, v158, v52 row_newbcast:0 row_mask:0xf bank_mask:0xf\n"
"v_mul_f32_dpp v53, v158, v53 row_newbcast:1 row_mask:0xf bank_mask:0xf\n"
"v_mul_f32_dpp v54, v158, v54 row_newbcast:2 row_mask:0xf bank_mask:0xf\n"
"v_mul_f32_dpp v55, v158, v55 row_newbcast:3 row_mask:0xf bank_mask:0xf\n"
"v_mul_f32_dpp v56, v158, v56 row_newbcast:0 row_mask:0xf bank_mask:0xf\n"
"v_mul_f32_dpp v57, v158, v57 row_newbcast:1 row_mask:0xf bank_mask:0xf\n"
"v_mul_f32_dpp v58, v158, v58 row_newbcast:2 row_mask:0xf bank_mask:0xf\n"
"v_mul_f32_dpp v59, v158, v59 row_newbcast:3 row_mask:0xf bank_mask:0xf\n"
"v_mul_f32_dpp v60, v158, v60 row_newbcast:0 row_mask:0xf bank_mask:0xf\n"
"v_mul_f32_dpp v61, v158, v61 row_newbcast:1 row_mask:0xf bank_mask:0xf\n"
"v_mul_f32_dpp v62, v158, v62 row_newbcast:2 row_mask:0xf bank_mask:0xf\n"
"v_mul_f32_dpp v63, v158, v63 row_newbcast:3 row_mask:0xf bank_mask:0xf\n"
"v_max3_f32 v161, v112, v113, v114\n"
"v_max3_f32 v161, v161, v115, v116\n"
"v_max3_f32 v161, v161, v117, v118\n"
"v_max3_f32 v161, v161, v119, v160\n"
"v_mov_b32 v162, v161\n"
"s_nop 1\n"
"v_permlane32_swap_b32 v161, v162\n"
"v_max_f32 v161, v161, v162\n"
"v_mov_b32 v162, v161\n"
"s_nop 1\n"
"v_permlane16_swap_b32 v161, v162\n"
"v_max_f32 v161, v161, v162\n"
"v_sub_f32 v162, v160, v161\n"
"v_exp_f32 v162, v162\n"
"v_mov_b32 v160, v161\n"
"v_sub_f32 v112, v112, v160\n"
"v_sub_f32 v113, v113, v160\n"
"v_sub_f32 v114, v114, v160\n"
"v_sub_f32 v115, v115, v160\n"
"v_sub_f32 v116, v116, v160\n"
"v_sub_f32 v117, v117, v160\n"
"v_sub_f32 v118, v118, v160\n"
"v_sub_f32 v119, v119, v160\n"
"v_exp_f32 v112, v112\n"
"v_exp_f32 v113, v113\n"
"v_exp_f32 v114, v114\n"
"v_exp_f32 v115, v115\n"
"v_exp_f32 v116, v116\n"
"v_exp_f32 v117, v117\n"
"v_exp_f32 v118, v118\n"
"v_exp_f32 v119, v119\n"
"v_mul_f32 v163, v163, v162\n"
"v_add_f32 v163, v163, v112\n"
"v_add_f32 v163, v163, v113\n"
"v_add_f32 v163, v163, v114\n"
"v_add_f32 v163, v163, v115\n"
"v_add_f32 v163, v163, v116\n"
"v_add_f32 v163, v163, v117\n"
"v_add_f32 v163, v163, v118\n"
"v_add_f32 v163, v163, v119\n"
"v_cvt_pk_bf16_f32 v112, v112, v113\n"
"v_cvt_pk_bf16_f32 v113, v114, v115\n"
"v_cvt_pk_bf16_f32 v114, v116, v117\n"
"v_cvt_pk_bf16_f32 v115, v118, v119\n"
"v_mov_b32_dpp v162, v162 row_ror:12 row_mask:0x2 bank_mask:0xf\n"
"s_nop 1\n"
"v_mov_b32_dpp v162, v162 row_ror:8 row_mask:0x4 bank_mask:0xf\n"
"s_nop 1\n"
"v_mov_b32_dpp v162, v162 row_ror:4 row_mask:0x8 bank_mask:0xf\n"
"s_nop 1\n"
"v_mul_f32_dpp v64, v162, v64 row_newbcast:0 row_mask:0xf bank_mask:0xf\n"
"v_mul_f32_dpp v65, v162, v65 row_newbcast:1 row_mask:0xf bank_mask:0xf\n"
"v_mul_f32_dpp v66, v162, v66 row_newbcast:2 row_mask:0xf bank_mask:0xf\n"
"v_mul_f32_dpp v67, v162, v67 row_newbcast:3 row_mask:0xf bank_mask:0xf\n"
"v_mul_f32_dpp v68, v162, v68 row_newbcast:0 row_mask:0xf bank_mask:0xf\n"
"v_mul_f32_dpp v69, v162, v69 row_newbcast:1 row_mask:0xf bank_mask:0xf\n"
"v_mul_f32_dpp v70, v162, v70 row_newbcast:2 row_mask:0xf bank_mask:0xf\n"
"v_mul_f32_dpp v71, v162, v71 row_newbcast:3 row_mask:0xf bank_mask:0xf\n"
"v_mul_f32_dpp v72, v162, v72 row_newbcast:0 row_mask:0xf bank_mask:0xf\n"
"v_mul_f32_dpp v73, v162, v73 row_newbcast:1 row_mask:0xf bank_mask:0xf\n"
"v_mul_f32_dpp v74, v162, v74 row_newbcast:2 row_mask:0xf bank_mask:0xf\n"
"v_mul_f32_dpp v75, v162, v75 row_newbcast:3 row_mask:0xf bank_mask:0xf\n"
"v_mul_f32_dpp v76, v162, v76 row_newbcast:0 row_mask:0xf bank_mask:0xf\n"
"v_mul_f32_dpp v77, v162, v77 row_newbcast:1 row_mask:0xf bank_mask:0xf\n"
"v_mul_f32_dpp v78, v162, v78 row_newbcast:2 row_mask:0xf bank_mask:0xf\n"
"v_mul_f32_dpp v79, v162, v79 row_newbcast:3 row_mask:0xf bank_mask:0xf\n"
"v_mul_f32_dpp v80, v162, v80 row_newbcast:0 row_mask:0xf bank_mask:0xf\n"
"v_mul_f32_dpp v81, v162, v81 row_newbcast:1 row_mask:0xf bank_mask:0xf\n"
"v_mul_f32_dpp v82, v162, v82 row_newbcast:2 row_mask:0xf bank_mask:0xf\n"
"v_mul_f32_dpp v83, v162, v83 row_newbcast:3 row_mask:0xf bank_mask:0xf\n"
"v_mul_f32_dpp v84, v162, v84 row_newbcast:0 row_mask:0xf bank_mask:0xf\n"
"v_mul_f32_dpp v85, v162, v85 row_newbcast:1 row_mask:0xf bank_mask:0xf\n"
"v_mul_f32_dpp v86, v162, v86 row_newbcast:2 row_mask:0xf bank_mask:0xf\n"
"v_mul_f32_dpp v87, v162, v87 row_newbcast:3 row_mask:0xf bank_mask:0xf\n"
"v_mul_f32_dpp v88, v162, v88 row_newbcast:0 row_mask:0xf bank_mask:0xf\n"
"v_mul_f32_dpp v89, v162, v89 row_newbcast:1 row_mask:0xf bank_mask:0xf\n"
"v_mul_f32_dpp v90, v162, v90 row_newbcast:2 row_mask:0xf bank_mask:0xf\n"
"v_mul_f32_dpp v91, v162, v91 row_newbcast:3 row_mask:0xf bank_mask:0xf\n"
"v_mul_f32_dpp v92, v162, v92 row_newbcast:0 row_mask:0xf bank_mask:0xf\n"
"v_mul_f32_dpp v93, v162, v93 row_newbcast:1 row_mask:0xf bank_mask:0xf\n"
"v_mul_f32_dpp v94, v162, v94 row_newbcast:2 row_mask:0xf bank_mask:0xf\n"
"v_mul_f32_dpp v95, v162, v95 row_newbcast:3 row_mask:0xf bank_mask:0xf\n"
"s_cmp_lg_u32 s83, s43\n"
"s_cbranch_scc1 LSK3A_%=\n"
"v_max3_f32 v165, v120, v121, v122\n"
"v_max3_f32 v165, v165, v123, v124\n"
"v_max3_f32 v165, v165, v125, v126\n"
"v_max3_f32 v165, v165, v127, v164\n"
"v_mov_b32 v166, v165\n"
"s_nop 1\n"
"v_permlane32_swap_b32 v165, v166\n"
"v_max_f32 v165, v165, v166\n"
"v_mov_b32 v166, v165\n"
"s_nop 1\n"
"v_permlane16_swap_b32 v165, v166\n"
"v_max_f32 v165, v165, v166\n"
"v_sub_f32 v166, v164, v165\n"
"v_exp_f32 v166, v166\n"
"v_mov_b32 v164, v165\n"
"v_sub_f32 v120, v120, v164\n"
"v_sub_f32 v121, v121, v164\n"
"v_sub_f32 v122, v122, v164\n"
"v_sub_f32 v123, v123, v164\n"
"v_sub_f32 v124, v124, v164\n"
"v_sub_f32 v125, v125, v164\n"
"v_sub_f32 v126, v126, v164\n"
"v_sub_f32 v127, v127, v164\n"
"v_exp_f32 v120, v120\n"
"v_exp_f32 v121, v121\n"
"v_exp_f32 v122, v122\n"
"v_exp_f32 v123, v123\n"
"v_exp_f32 v124, v124\n"
"v_exp_f32 v125, v125\n"
"v_exp_f32 v126, v126\n"
"v_exp_f32 v127, v127\n"
"v_mul_f32 v167, v167, v166\n"
"v_add_f32 v167, v167, v120\n"
"v_add_f32 v167, v167, v121\n"
"v_add_f32 v167, v167, v122\n"
"v_add_f32 v167, v167, v123\n"
"v_add_f32 v167, v167, v124\n"
"v_add_f32 v167, v167, v125\n"
"v_add_f32 v167, v167, v126\n"
"v_add_f32 v167, v167, v127\n"
"v_cvt_pk_bf16_f32 v120, v120, v121\n"
"v_cvt_pk_bf16_f32 v121, v122, v123\n"
"v_cvt_pk_bf16_f32 v122, v124, v125\n"
"v_cvt_pk_bf16_f32 v123, v126, v127\n"
"v_mov_b32_dpp v166, v166 row_ror:12 row_mask:0x2 bank_mask:0xf\n"
"s_nop 1\n"
"v_mov_b32_dpp v166, v166 row_ror:8 row_mask:0x4 bank_mask:0xf\n"
"s_nop 1\n"
"v_mov_b32_dpp v166, v166 row_ror:4 row_mask:0x8 bank_mask:0xf\n"
"v_accvgpr_read_b32 v124, a208\n"
"s_nop 1\n"
"v_mul_f32_dpp v124, v166, v124 row_newbcast:0 row_mask:0xf bank_mask:0xf\n"
"v_accvgpr_write_b32 a208, v124\n"
"v_accvgpr_read_b32 v125, a209\n"
"s_nop 1\n"
"v_mul_f32_dpp v125, v166, v125 row_newbcast:1 row_mask:0xf bank_mask:0xf\n"
"v_accvgpr_write_b32 a209, v125\n"
"v_accvgpr_read_b32 v126, a210\n"
"s_nop 1\n"
"v_mul_f32_dpp v126, v166, v126 row_newbcast:2 row_mask:0xf bank_mask:0xf\n"
"v_accvgpr_write_b32 a210, v126\n"
"v_accvgpr_read_b32 v127, a211\n"
"s_nop 1\n"
"v_mul_f32_dpp v127, v166, v127 row_newbcast:3 row_mask:0xf bank_mask:0xf\n"
"v_accvgpr_write_b32 a211, v127\n"
"v_accvgpr_read_b32 v124, a212\n"
"s_nop 1\n"
"v_mul_f32_dpp v124, v166, v124 row_newbcast:0 row_mask:0xf bank_mask:0xf\n"
"v_accvgpr_write_b32 a212, v124\n"
"v_accvgpr_read_b32 v125, a213\n"
"s_nop 1\n"
"v_mul_f32_dpp v125, v166, v125 row_newbcast:1 row_mask:0xf bank_mask:0xf\n"
"v_accvgpr_write_b32 a213, v125\n"
"v_accvgpr_read_b32 v126, a214\n"
"s_nop 1\n"
"v_mul_f32_dpp v126, v166, v126 row_newbcast:2 row_mask:0xf bank_mask:0xf\n"
"v_accvgpr_write_b32 a214, v126\n"
"v_accvgpr_read_b32 v127, a215\n"
"s_nop 1\n"
"v_mul_f32_dpp v127, v166, v127 row_newbcast:3 row_mask:0xf bank_mask:0xf\n"
"v_accvgpr_write_b32 a215, v127\n"
"v_accvgpr_read_b32 v124, a216\n"
"s_nop 1\n"
"v_mul_f32_dpp v124, v166, v124 row_newbcast:0 row_mask:0xf bank_mask:0xf\n"
"v_accvgpr_write_b32 a216, v124\n"
"v_accvgpr_read_b32 v125, a217\n"
"s_nop 1\n"
"v_mul_f32_dpp v125, v166, v125 row_newbcast:1 row_mask:0xf bank_mask:0xf\n"
"v_accvgpr_write_b32 a217, v125\n"
"v_accvgpr_read_b32 v126, a218\n"
"s_nop 1\n"
"v_mul_f32_dpp v126, v166, v126 row_newbcast:2 row_mask:0xf bank_mask:0xf\n"
"v_accvgpr_write_b32 a218, v126\n"
"v_accvgpr_read_b32 v127, a219\n"
"s_nop 1\n"
"v_mul_f32_dpp v127, v166, v127 row_newbcast:3 row_mask:0xf bank_mask:0xf\n"
"v_accvgpr_write_b32 a219, v127\n"
"v_accvgpr_read_b32 v124, a220\n"
"s_nop 1\n"
"v_mul_f32_dpp v124, v166, v124 row_newbcast:0 row_mask:0xf bank_mask:0xf\n"
"v_accvgpr_write_b32 a220, v124\n"
"v_accvgpr_read_b32 v125, a221\n"
"s_nop 1\n"
"v_mul_f32_dpp v125, v166, v125 row_newbcast:1 row_mask:0xf bank_mask:0xf\n"
"v_accvgpr_write_b32 a221, v125\n"
"v_accvgpr_read_b32 v126, a222\n"
"s_nop 1\n"
"v_mul_f32_dpp v126, v166, v126 row_newbcast:2 row_mask:0xf bank_mask:0xf\n"
"v_accvgpr_write_b32 a222, v126\n"
"v_accvgpr_read_b32 v127, a223\n"
"s_nop 1\n"
"v_mul_f32_dpp v127, v166, v127 row_newbcast:3 row_mask:0xf bank_mask:0xf\n"
"v_accvgpr_write_b32 a223, v127\n"
"v_accvgpr_read_b32 v124, a224\n"
"s_nop 1\n"
"v_mul_f32_dpp v124, v166, v124 row_newbcast:0 row_mask:0xf bank_mask:0xf\n"
"v_accvgpr_write_b32 a224, v124\n"
"v_accvgpr_read_b32 v125, a225\n"
"s_nop 1\n"
"v_mul_f32_dpp v125, v166, v125 row_newbcast:1 row_mask:0xf bank_mask:0xf\n"
"v_accvgpr_write_b32 a225, v125\n"
"v_accvgpr_read_b32 v126, a226\n"
"s_nop 1\n"
"v_mul_f32_dpp v126, v166, v126 row_newbcast:2 row_mask:0xf bank_mask:0xf\n"
"v_accvgpr_write_b32 a226, v126\n"
"v_accvgpr_read_b32 v127, a227\n"
"s_nop 1\n"
"v_mul_f32_dpp v127, v166, v127 row_newbcast:3 row_mask:0xf bank_mask:0xf\n"
"v_accvgpr_write_b32 a227, v127\n"
"v_accvgpr_read_b32 v124, a228\n"
"s_nop 1\n"
"v_mul_f32_dpp v124, v166, v124 row_newbcast:0 row_mask:0xf bank_mask:0xf\n"
"v_accvgpr_write_b32 a228, v124\n"
"v_accvgpr_read_b32 v125, a229\n"
"s_nop 1\n"
"v_mul_f32_dpp v125, v166, v125 row_newbcast:1 row_mask:0xf bank_mask:0xf\n"
"v_accvgpr_write_b32 a229, v125\n"
"v_accvgpr_read_b32 v126, a230\n"
"s_nop 1\n"
"v_mul_f32_dpp v126, v166, v126 row_newbcast:2 row_mask:0xf bank_mask:0xf\n"
"v_accvgpr_write_b32 a230, v126\n"
"v_accvgpr_read_b32 v127, a231\n"
"s_nop 1\n"
"v_mul_f32_dpp v127, v166, v127 row_newbcast:3 row_mask:0xf bank_mask:0xf\n"
"v_accvgpr_write_b32 a231, v127\n"
"v_accvgpr_read_b32 v124, a232\n"
"s_nop 1\n"
"v_mul_f32_dpp v124, v166, v124 row_newbcast:0 row_mask:0xf bank_mask:0xf\n"
"v_accvgpr_write_b32 a232, v124\n"
"v_accvgpr_read_b32 v125, a233\n"
"s_nop 1\n"
"v_mul_f32_dpp v125, v166, v125 row_newbcast:1 row_mask:0xf bank_mask:0xf\n"
"v_accvgpr_write_b32 a233, v125\n"
"v_accvgpr_read_b32 v126, a234\n"
"s_nop 1\n"
"v_mul_f32_dpp v126, v166, v126 row_newbcast:2 row_mask:0xf bank_mask:0xf\n"
"v_accvgpr_write_b32 a234, v126\n"
"v_accvgpr_read_b32 v127, a235\n"
"s_nop 1\n"
"v_mul_f32_dpp v127, v166, v127 row_newbcast:3 row_mask:0xf bank_mask:0xf\n"
"v_accvgpr_write_b32 a235, v127\n"
"v_accvgpr_read_b32 v124, a236\n"
"s_nop 1\n"
"v_mul_f32_dpp v124, v166, v124 row_newbcast:0 row_mask:0xf bank_mask:0xf\n"
"v_accvgpr_write_b32 a236, v124\n"
"v_accvgpr_read_b32 v125, a237\n"
"s_nop 1\n"
"v_mul_f32_dpp v125, v166, v125 row_newbcast:1 row_mask:0xf bank_mask:0xf\n"
"v_accvgpr_write_b32 a237, v125\n"
"v_accvgpr_read_b32 v126, a238\n"
"s_nop 1\n"
"v_mul_f32_dpp v126, v166, v126 row_newbcast:2 row_mask:0xf bank_mask:0xf\n"
"v_accvgpr_write_b32 a238, v126\n"
"v_accvgpr_read_b32 v127, a239\n"
"s_nop 1\n"
"v_mul_f32_dpp v127, v166, v127 row_newbcast:3 row_mask:0xf bank_mask:0xf\n"
"v_accvgpr_write_b32 a239, v127\n"
"LSK3A_%=:\n"
"s_nop 3\n"
"buffer_load_ushort v128, v146, s[88:91], s80 offen offset:0\n"
"buffer_load_ushort v129, v146, s[88:91], s80 offen offset:256\n"
"buffer_load_ushort v130, v146, s[88:91], s80 offen offset:512\n"
"buffer_load_ushort v131, v146, s[88:91], s80 offen offset:768\n"
"buffer_load_ushort v132, v146, s[88:91], s81 offen offset:0\n"
"buffer_load_ushort v133, v146, s[88:91], s81 offen offset:256\n"
"buffer_load_ushort v134, v146, s[88:91], s81 offen offset:512\n"
"buffer_load_ushort v135, v146, s[88:91], s81 offen offset:768\n"
"buffer_load_ushort v136, v146, s[88:91], s80 offen offset:32\n"
"buffer_load_ushort v137, v146, s[88:91], s80 offen offset:288\n"
"buffer_load_ushort v138, v146, s[88:91], s80 offen offset:544\n"
"buffer_load_ushort v139, v146, s[88:91], s80 offen offset:800\n"
"buffer_load_ushort v140, v146, s[88:91], s81 offen offset:32\n"
"buffer_load_ushort v141, v146, s[88:91], s81 offen offset:288\n"
"buffer_load_ushort v142, v146, s[88:91], s81 offen offset:544\n"
"buffer_load_ushort v143, v146, s[88:91], s81 offen offset:800\n"
"s_waitcnt vmcnt(0)\n"
"v_lshl_or_b32 v128, v129, 16, v128\n"
"v_lshl_or_b32 v129, v131, 16, v130\n"
"v_lshl_or_b32 v130, v133, 16, v132\n"
"v_lshl_or_b32 v131, v135, 16, v134\n"
"v_lshl_or_b32 v136, v137, 16, v136\n"
"v_lshl_or_b32 v137, v139, 16, v138\n"
"v_lshl_or_b32 v138, v141, 16, v140\n"
"v_lshl_or_b32 v139, v143, 16, v142\n"
"v_mfma_f32_16x16x32_bf16 v[0:3], v[96:99], v[128:131], v[0:3]\n"
"s_nop 0\n"
"v_mfma_f32_16x16x32_bf16 v[4:7], v[96:99], v[136:139], v[4:7]\n"
"v_mfma_f32_16x16x32_bf16 v[32:35], v[104:107], v[128:131], v[32:35]\n"
"v_mfma_f32_16x16x32_bf16 v[36:39], v[104:107], v[136:139], v[36:39]\n"
"v_mfma_f32_16x16x32_bf16 v[64:67], v[112:115], v[128:131], v[64:67]\n"
"v_mfma_f32_16x16x32_bf16 v[68:71], v[112:115], v[136:139], v[68:71]\n"
"s_cmp_lg_u32 s83, s43\n"
"s_cbranch_scc1 LSK3B0_%=\n"
"v_mfma_f32_16x16x32_bf16 a[208:211], v[120:123], v[128:131], a[208:211]\n"
"v_mfma_f32_16x16x32_bf16 a[212:215], v[120:123], v[136:139], a[212:215]\n"
"LSK3B0_%=:\n"
"s_nop 3\n"
"buffer_load_ushort v128, v147, s[88:91], s80 offen offset:0\n"
"buffer_load_ushort v129, v147, s[88:91], s80 offen offset:256\n"
"buffer_load_ushort v130, v147, s[88:91], s80 offen offset:512\n"
"buffer_load_ushort v131, v147, s[88:91], s80 offen offset:768\n"
"buffer_load_ushort v132, v147, s[88:91], s81 offen offset:0\n"
"buffer_load_ushort v133, v147, s[88:91], s81 offen offset:256\n"
"buffer_load_ushort v134, v147, s[88:91], s81 offen offset:512\n"
"buffer_load_ushort v135, v147, s[88:91], s81 offen offset:768\n"
"buffer_load_ushort v136, v147, s[88:91], s80 offen offset:32\n"
"buffer_load_ushort v137, v147, s[88:91], s80 offen offset:288\n"
"buffer_load_ushort v138, v147, s[88:91], s80 offen offset:544\n"
"buffer_load_ushort v139, v147, s[88:91], s80 offen offset:800\n"
"buffer_load_ushort v140, v147, s[88:91], s81 offen offset:32\n"
"buffer_load_ushort v141, v147, s[88:91], s81 offen offset:288\n"
"buffer_load_ushort v142, v147, s[88:91], s81 offen offset:544\n"
"buffer_load_ushort v143, v147, s[88:91], s81 offen offset:800\n"
"s_waitcnt vmcnt(0)\n"
"v_lshl_or_b32 v128, v129, 16, v128\n"
"v_lshl_or_b32 v129, v131, 16, v130\n"
"v_lshl_or_b32 v130, v133, 16, v132\n"
"v_lshl_or_b32 v131, v135, 16, v134\n"
"v_lshl_or_b32 v136, v137, 16, v136\n"
"v_lshl_or_b32 v137, v139, 16, v138\n"
"v_lshl_or_b32 v138, v141, 16, v140\n"
"v_lshl_or_b32 v139, v143, 16, v142\n"
"v_mfma_f32_16x16x32_bf16 v[8:11], v[96:99], v[128:131], v[8:11]\n"
"s_nop 0\n"
"v_mfma_f32_16x16x32_bf16 v[12:15], v[96:99], v[136:139], v[12:15]\n"
"v_mfma_f32_16x16x32_bf16 v[40:43], v[104:107], v[128:131], v[40:43]\n"
"v_mfma_f32_16x16x32_bf16 v[44:47], v[104:107], v[136:139], v[44:47]\n"
"v_mfma_f32_16x16x32_bf16 v[72:75], v[112:115], v[128:131], v[72:75]\n"
"v_mfma_f32_16x16x32_bf16 v[76:79], v[112:115], v[136:139], v[76:79]\n"
"s_cmp_lg_u32 s83, s43\n"
"s_cbranch_scc1 LSK3B1_%=\n"
"v_mfma_f32_16x16x32_bf16 a[216:219], v[120:123], v[128:131], a[216:219]\n"
"v_mfma_f32_16x16x32_bf16 a[220:223], v[120:123], v[136:139], a[220:223]\n"
"LSK3B1_%=:\n"
"s_nop 3\n"
"buffer_load_ushort v128, v146, s[88:91], s80 offen offset:128\n"
"buffer_load_ushort v129, v146, s[88:91], s80 offen offset:384\n"
"buffer_load_ushort v130, v146, s[88:91], s80 offen offset:640\n"
"buffer_load_ushort v131, v146, s[88:91], s80 offen offset:896\n"
"buffer_load_ushort v132, v146, s[88:91], s81 offen offset:128\n"
"buffer_load_ushort v133, v146, s[88:91], s81 offen offset:384\n"
"buffer_load_ushort v134, v146, s[88:91], s81 offen offset:640\n"
"buffer_load_ushort v135, v146, s[88:91], s81 offen offset:896\n"
"buffer_load_ushort v136, v146, s[88:91], s80 offen offset:160\n"
"buffer_load_ushort v137, v146, s[88:91], s80 offen offset:416\n"
"buffer_load_ushort v138, v146, s[88:91], s80 offen offset:672\n"
"buffer_load_ushort v139, v146, s[88:91], s80 offen offset:928\n"
"buffer_load_ushort v140, v146, s[88:91], s81 offen offset:160\n"
"buffer_load_ushort v141, v146, s[88:91], s81 offen offset:416\n"
"buffer_load_ushort v142, v146, s[88:91], s81 offen offset:672\n"
"buffer_load_ushort v143, v146, s[88:91], s81 offen offset:928\n"
"s_waitcnt vmcnt(0)\n"
"v_lshl_or_b32 v128, v129, 16, v128\n"
"v_lshl_or_b32 v129, v131, 16, v130\n"
"v_lshl_or_b32 v130, v133, 16, v132\n"
"v_lshl_or_b32 v131, v135, 16, v134\n"
"v_lshl_or_b32 v136, v137, 16, v136\n"
"v_lshl_or_b32 v137, v139, 16, v138\n"
"v_lshl_or_b32 v138, v141, 16, v140\n"
"v_lshl_or_b32 v139, v143, 16, v142\n"
"v_mfma_f32_16x16x32_bf16 v[16:19], v[96:99], v[128:131], v[16:19]\n"
"s_nop 0\n"
"v_mfma_f32_16x16x32_bf16 v[20:23], v[96:99], v[136:139], v[20:23]\n"
"v_mfma_f32_16x16x32_bf16 v[48:51], v[104:107], v[128:131], v[48:51]\n"
"v_mfma_f32_16x16x32_bf16 v[52:55], v[104:107], v[136:139], v[52:55]\n"
"v_mfma_f32_16x16x32_bf16 v[80:83], v[112:115], v[128:131], v[80:83]\n"
"v_mfma_f32_16x16x32_bf16 v[84:87], v[112:115], v[136:139], v[84:87]\n"
"s_cmp_lg_u32 s83, s43\n"
"s_cbranch_scc1 LSK3B2_%=\n"
"v_mfma_f32_16x16x32_bf16 a[224:227], v[120:123], v[128:131], a[224:227]\n"
"v_mfma_f32_16x16x32_bf16 a[228:231], v[120:123], v[136:139], a[228:231]\n"
"LSK3B2_%=:\n"
"s_nop 3\n"
"buffer_load_ushort v128, v147, s[88:91], s80 offen offset:128\n"
"buffer_load_ushort v129, v147, s[88:91], s80 offen offset:384\n"
"buffer_load_ushort v130, v147, s[88:91], s80 offen offset:640\n"
"buffer_load_ushort v131, v147, s[88:91], s80 offen offset:896\n"
"buffer_load_ushort v132, v147, s[88:91], s81 offen offset:128\n"
"buffer_load_ushort v133, v147, s[88:91], s81 offen offset:384\n"
"buffer_load_ushort v134, v147, s[88:91], s81 offen offset:640\n"
"buffer_load_ushort v135, v147, s[88:91], s81 offen offset:896\n"
"buffer_load_ushort v136, v147, s[88:91], s80 offen offset:160\n"
"buffer_load_ushort v137, v147, s[88:91], s80 offen offset:416\n"
"buffer_load_ushort v138, v147, s[88:91], s80 offen offset:672\n"
"buffer_load_ushort v139, v147, s[88:91], s80 offen offset:928\n"
"buffer_load_ushort v140, v147, s[88:91], s81 offen offset:160\n"
"buffer_load_ushort v141, v147, s[88:91], s81 offen offset:416\n"
"buffer_load_ushort v142, v147, s[88:91], s81 offen offset:672\n"
"buffer_load_ushort v143, v147, s[88:91], s81 offen offset:928\n"
"s_waitcnt vmcnt(0)\n"
"v_lshl_or_b32 v128, v129, 16, v128\n"
"v_lshl_or_b32 v129, v131, 16, v130\n"
"v_lshl_or_b32 v130, v133, 16, v132\n"
"v_lshl_or_b32 v131, v135, 16, v134\n"
"v_lshl_or_b32 v136, v137, 16, v136\n"
"v_lshl_or_b32 v137, v139, 16, v138\n"
"v_lshl_or_b32 v138, v141, 16, v140\n"
"v_lshl_or_b32 v139, v143, 16, v142\n"
"v_mfma_f32_16x16x32_bf16 v[24:27], v[96:99], v[128:131], v[24:27]\n"
"s_nop 0\n"
"v_mfma_f32_16x16x32_bf16 v[28:31], v[96:99], v[136:139], v[28:31]\n"
"v_mfma_f32_16x16x32_bf16 v[56:59], v[104:107], v[128:131], v[56:59]\n"
"v_mfma_f32_16x16x32_bf16 v[60:63], v[104:107], v[136:139], v[60:63]\n"
"v_mfma_f32_16x16x32_bf16 v[88:91], v[112:115], v[128:131], v[88:91]\n"
"v_mfma_f32_16x16x32_bf16 v[92:95], v[112:115], v[136:139], v[92:95]\n"
"s_cmp_lg_u32 s83, s43\n"
"s_cbranch_scc1 LSK3B3_%=\n"
"v_mfma_f32_16x16x32_bf16 a[232:235], v[120:123], v[128:131], a[232:235]\n"
"v_mfma_f32_16x16x32_bf16 a[236:239], v[120:123], v[136:139], a[236:239]\n"
"LSK3B3_%=:\n"
"s_nop 3\n"
"s_nop 9\n"
"s_add_u32 s80, s80, 8192\n"
"s_add_u32 s81, s81, 8192\n"
"s_add_u32 s82, s82, 1\n"
"s_cmp_lt_u32 s82, 32\n"
"s_cbranch_scc1 LSLOOP_%=\n"
"v_mov_b32 v154, v155\n"
"s_nop 1\n"
"v_permlane32_swap_b32 v155, v154\n"
"v_add_f32 v155, v155, v154\n"
"v_mov_b32 v154, v155\n"
"s_nop 1\n"
"v_permlane16_swap_b32 v155, v154\n"
"v_add_f32 v155, v155, v154\n"
"v_mov_b32 v196, v155\n"
"v_mov_b32 v158, v159\n"
"s_nop 1\n"
"v_permlane32_swap_b32 v159, v158\n"
"v_add_f32 v159, v159, v158\n"
"v_mov_b32 v158, v159\n"
"s_nop 1\n"
"v_permlane16_swap_b32 v159, v158\n"
"v_add_f32 v159, v159, v158\n"
"v_mov_b32 v198, v159\n"
"v_mov_b32 v162, v163\n"
"s_nop 1\n"
"v_permlane32_swap_b32 v163, v162\n"
"v_add_f32 v163, v163, v162\n"
"v_mov_b32 v162, v163\n"
"s_nop 1\n"
"v_permlane16_swap_b32 v163, v162\n"
"v_add_f32 v163, v163, v162\n"
"v_mov_b32 v200, v163\n"
"v_mov_b32 v166, v167\n"
"s_nop 1\n"
"v_permlane32_swap_b32 v167, v166\n"
"v_add_f32 v167, v167, v166\n"
"v_mov_b32 v166, v167\n"
"s_nop 1\n"
"v_permlane16_swap_b32 v167, v166\n"
"v_add_f32 v167, v167, v166\n"
"v_mov_b32 v202, v167\n"
"v_sub_f32 v192, 0, v164\n"
"v_mov_b32 v96, 0\n"
"v_mov_b32 v97, 0\n"
"v_mov_b32 v98, 0\n"
"v_mov_b32 v99, 0\n"
"v_mov_b32 v100, 0\n"
"v_mov_b32 v101, 0\n"
"v_mov_b32 v102, 0\n"
"v_mov_b32 v103, 0\n"
"v_mov_b32 v104, 0\n"
"v_mov_b32 v105, 0\n"
"v_mov_b32 v106, 0\n"
"v_mov_b32 v107, 0\n"
"v_mov_b32 v108, 0\n"
"v_mov_b32 v109, 0\n"
"v_mov_b32 v110, 0\n"
"v_mov_b32 v111, 0\n"
"v_mov_b32 v112, 0\n"
"v_mov_b32 v113, 0\n"
"v_mov_b32 v114, 0\n"
"v_mov_b32 v115, 0\n"
"v_mov_b32 v116, 0\n"
"v_mov_b32 v117, 0\n"
"v_mov_b32 v118, 0\n"
"v_mov_b32 v119, 0\n"
"v_mov_b32 v120, 0\n"
"v_mov_b32 v121, 0\n"
"v_mov_b32 v122, 0\n"
"v_mov_b32 v123, 0\n"
"v_mov_b32 v204, 0\n"
"v_mov_b32 v205, 0\n"
"s_nop 3\n"
"s_mov_b32 s68, 0\n"
"s_cmp_eq_u32 s47, 4\n"
"s_cbranch_scc1 LBACK0_%=\n"
"s_cmp_eq_u32 s47, 8\n"
"s_cbranch_scc1 LBACK1_%=\n"
"s_cmp_eq_u32 s47, 12\n"
"s_cbranch_scc1 LBACK2_%=\n"
"s_branch LBACK3_%=\n"
"LFIN_%=:\n"
    :: [kalo]"s"((unsigned)ka), [kahi]"s"((unsigned)(ka >> 32)), [wg]"s"(blockIdx.x), [wid]"s"(wid)
    : "v0","v1","v2","v3","v4","v5","v6","v7","v8","v9","v10","v11","v12","v13","v14","v15","v16","v17","v18","v19","v20","v21","v22","v23","v24","v25","v26","v27","v28","v29","v30","v31","v32","v33","v34","v35","v36","v37","v38","v39","v40","v41","v42","v43","v44","v45","v46","v47","v48","v49","v50","v51","v52","v53","v54","v55","v56","v57","v58","v59","v60","v61","v62","v63","v64","v65","v66","v67","v68","v69","v70","v71","v72","v73","v74","v75","v76","v77","v78","v79","v80","v81","v82","v83","v84","v85","v86","v87","v88","v89","v90","v91","v92","v93","v94","v95","v96","v97","v98","v99","v100","v101","v102","v103","v104","v105","v106","v107","v108","v109","v110","v111","v112","v113","v114","v115","v116","v117","v118","v119","v120","v121","v122","v123","v124","v125","v126","v127","v128","v129","v130","v131","v132","v133","v134","v135","v136","v137","v138","v139","v140","v141","v142","v143","v144","v145","v146","v147","v148","v149","v150","v151","v152","v153","v154","v155","v156","v157","v158","v159","v160","v161","v162","v163","v164","v165","v166","v167","v168","v169","v170","v171","v172","v173","v174","v175","v176","v177","v178","v179","v180","v181","v182","v183","v184","v185","v186","v187","v188","v189","v190","v191","v192","v193","v194","v195","v196","v197","v198","v199","v200","v201","v202","v203","v204","v205","v206","v207","v208","v209","v210","v211","v212","v213","v214","v215","v216","v217","v218","v219","v220","v221","v222","v223","v224","v225","v226","v227","v228","v229","v230","v231","v232","v233","v234","v235","v236","v237","v238","v239","v240","v241","v242","v243","v244","v245","v246","v247","v248","v249","v250","v251","v252","v253","v254","v255","a0","a1","a2","a3","a4","a5","a6","a7","a8","a9","a10","a11","a12","a13","a14","a15","a16","a17","a18","a19","a20","a21","a22","a23","a24","a25","a26","a27","a28","a29","a30","a31","a32","a33","a34","a35","a36","a37","a38","a39","a40","a41","a42","a43","a44","a45","a46","a47","a48","a49","a50","a51","a52","a53","a54","a55","a56","a57","a58","a59","a60","a61","a62","a63","a64","a65","a66","a67","a68","a69","a70","a71","a72","a73","a74","a75","a76","a77","a78","a79","a80","a81","a82","a83","a84","a85","a86","a87","a88","a89","a90","a91","a92","a93","a94","a95","a96","a97","a98","a99","a100","a101","a102","a103","a104","a105","a106","a107","a108","a109","a110","a111","a112","a113","a114","a115","a116","a117","a118","a119","a120","a121","a122","a123","a124","a125","a126","a127","a128","a129","a130","a131","a132","a133","a134","a135","a136","a137","a138","a139","a140","a141","a142","a143","a144","a145","a146","a147","a148","a149","a150","a151","a152","a153","a154","a155","a156","a157","a158","a159","a160","a161","a162","a163","a164","a165","a166","a167","a168","a169","a170","a171","a172","a173","a174","a175","a176","a177","a178","a179","a180","a181","a182","a183","a184","a185","a186","a187","a188","a189","a190","a191","a192","a193","a194","a195","a196","a197","a198","a199","a200","a201","a202","a203","a204","a205","a206","a207","a208","a209","a210","a211","a212","a213","a214","a215","a216","a217","a218","a219","a220","a221","a222","a223","a224","a225","a226","a227","a228","a229","a230","a231","a232","a233","a234","a235","a236","a237","a238","a239","a240","a241","a242","a243","a244","a245","a246","a247","a248","a249","a250","a251","a252","a253","a254","a255","s6","s7","s8","s9","s10","s11","s12","s13","s14","s15","s16","s17","s18","s19","s20","s21","s22","s23","s24","s25","s26","s27","s28","s29","s30","s31","s32","s33","s34","s35","s36","s37","s38","s39","s40","s41","s42","s43","s44","s45","s46","s47","s48","s49","s50","s51","s52","s53","s54","s55","s56","s57","s58","s59","s60","s61","s62","s63","s64","s65","s66","s67","s68","s69","s70","s71","s72","s73","s74","s75","s76","s77","s78","s79","s80","s81","s82","s83","s84","s85","s86","s87","s88","s89","s90","s91","s92","s93","s94","s95","s96","s97","s98","s99","s100","vcc","scc","m0","memory");
}
extern "C" void kernel_launch(void* const* d_in, const int* in_sizes, int n_in, void* d_out, int out_size, void* d_ws, size_t ws_size, hipStream_t stream) {
  char* xch = (char*)d_ws + ((ws_size - (size_t)270532608) & ~(size_t)0xfffff);
  (void)hipFuncSetAttribute((const void*)fused_attn, hipFuncAttributeMaxDynamicSharedMemorySize, 163840);
  hipLaunchKernelGGL(fused_attn, dim3(256), dim3(256), 163840, stream,
                     (const char*)d_in[0], (const char*)d_in[1], (const char*)d_in[2], (const char*)d_in[3], (const char*)d_in[4],
                     (const char*)d_in[5], (const char*)d_in[6], (const char*)d_in[7], (const char*)d_in[8], (const char*)d_in[9], xch, (char*)d_out, (char*)d_ws);
}
```

```cpp
#include <hip/hip_runtime.h>
#include <hip/hip_bf16.h>
#include <stdint.h>

__global__ __launch_bounds__(256, 1)
void fused_attn(const char* xA, const char* kA, const char* vA, const char* wA, const char* uA,
                const char* xB, const char* kB, const char* vB, const char* wB, const char* uB, char* xch, char* out, char* flags) {
  extern __shared__ __attribute__((aligned(16))) char lds[];
  const unsigned wid = __builtin_amdgcn_readfirstlane(threadIdx.x >> 6);
  const unsigned long long ka = (unsigned long long)__builtin_amdgcn_kernarg_segment_ptr();
  asm volatile(
"s_memtime s[64:65]\n"
"s_memrealtime s[66:67]\n"
"s_bfe_u32 s10, %[wg], 0x10002\n"
"s_mul_i32 s40, s10, 40\n"
"s_add_u32 s40, s40, %[kalo]\n"
"s_addc_u32 s41, %[kahi], 0\n"
"s_mov_b32 s78, %[kalo]\n"
"s_mov_b32 s79, %[kahi]\n"
"s_load_dwordx2 s[24:25], s[40:41], 0\n"
"s_load_dwordx2 s[28:29], s[40:41], 24\n"
"s_load_dwordx2 s[6:7], s[78:79], 0x60\n"
"s_load_dwordx2 s[16:17], s[40:41], 8\n"
"s_load_dwordx2 s[20:21], s[40:41], 16\n"
"s_load_dwordx2 s[8:9], s[40:41], 32\n"
"s_load_dwordx4 s[12:15], s[78:79], 0x50\n"
"s_mov_b32 s42, %[wid]\n"
"s_and_b32 s11, %[wg], 1\n"
"s_lshl_b32 s11, s11, 3\n"
"s_lshr_b32 s60, %[wg], 5\n"
"s_add_u32 s11, s11, s60\n"
"s_bfe_u32 s61, %[wg], 0x10001\n"
"s_bfe_u32 s60, %[wg], 0x20003\n"
"s_lshl2_add_u32 s61, s61, s60\n"
"s_getpc_b64 s[80:81]\n"
"LCP_%=:\n"
"s_mov_b32 s82, LEND_%=-LCP_%=\n"
"s_mov_b32 s83, 0x20000\n"
"v_mbcnt_lo_u32_b32 v2, -1, 0\n"
"v_lshlrev_b32 v2, 7, v2\n"
"s_lshr_b32 s60, %[wg], 3\n"
"s_lshl2_add_u32 s60, s60, s42\n"
"s_mul_i32 s60, s60, 640\n"
"s_mov_b64 exec, 31\n"
"buffer_load_dword v3, v2, s[80:83], s60 offen\n"
"s_mov_b64 exec, -1\n"
"s_mov_b32 s19, 0x20000\n"
"s_mov_b32 s23, 0x20000\n"
"s_mov_b32 s27, 0x20000\n"
"s_mov_b32 s31, 0x20000\n"
"s_mov_b32 s35, 0x20000\n"
"s_mov_b32 s39, 0x20000\n"
"s_mov_b32 s26, 286720\n"
"s_lshl_b32 s44, s42, 12\n"
"s_mov_b32 s50, s44\n"
"s_mul_i32 s51, s42, 7168\n"
"s_add_u32 s51, s51, 0x10000\n"
"s_mov_b32 s74, 0x8000\n"
"s_mov_b32 s58, 524288\n"
"s_mov_b32 s47, 0\n"
"s_add_u32 s60, s11, s61\n"
"s_and_b32 s60, s60, 0\n"
"s_mul_i32 s60, s60, 3\n"
"s_lshl_b32 s49, s60, 7\n"
"s_lshl_b32 s48, s60, 19\n"
"v_mbcnt_lo_u32_b32 v4, -1, 0\n"
"v_mbcnt_hi_u32_b32 v4, -1, v4\n"
"v_and_b32 v5, 15, v4\n"
"v_lshrrev_b32 v6, 4, v4\n"
"v_lshrrev_b32 v7, 3, v4\n"
"v_and_b32 v8, 7, v4\n"
"v_lshlrev_b32 v9, 1, v6\n"
"v_xor_b32 v9, v9, v8\n"
"v_lshlrev_b32 v9, 4, v9\n"
"s_mul_i32 s60, s42, 71680\n"
"v_mul_u32_u24 v10, 1280, v7\n"
"v_add3_u32 v248, v10, v9, s60\n"
"v_add_u32 v249, 9216, v248\n"
"v_add_u32 v254, 18432, v248\n"
"v_add_u32 v255, 27648, v248\n"
"v_lshl_add_u32 v240, v7, 8, v9\n"
"v_add_u32 v240, s44, v240\n"
"v_and_b32 v10, 1, v6\n"
"v_xor_b32 v10, v10, v8\n"
"v_lshlrev_b32 v10, 4, v10\n"
"v_lshl_add_u32 v10, v7, 13, v10\n"
"s_lshl_b32 s60, s42, 17\n"
"v_add_u32 v10, s60, v10\n"
"v_add_u32 v224, 1024, v10\n"
"v_add_u32 v225, 128, v10\n"
"v_xor_b32 v10, 64, v10\n"
"v_add_u32 v226, 64512, v10\n"
"v_add_u32 v227, 63616, v10\n"
"s_mul_i32 s40, s11, 286720\n"
"s_lshl_b32 s41, s61, 10\n"
"s_sub_u32 s30, 5243904, s41\n"
"s_sub_u32 s41, s41, 1024\n"
"s_ashr_i32 s60, s41, 31\n"
"s_lshl4_add_u32 s92, s10, s11\n"
"s_lshl_b32 s93, s92, 9\n"
"v_mov_b32 v0, 0\n"
"s_waitcnt lgkmcnt(0)\n"
"s_add_u32 s24, s24, s40\n"
"s_addc_u32 s25, s25, 0\n"
"s_and_b32 s25, s25, 0xffff\n"
"s_add_u32 s28, s28, s41\n"
"s_addc_u32 s29, s29, s60\n"
"s_and_b32 s29, s29, 0xffff\n"
"s_add_u32 s6, s6, s93\n"
"s_addc_u32 s7, s7, 0\n"
"buffer_load_dword v12, v248, s[24:27], s49 offen\n"
"buffer_load_dword v13, v224, s[28:31], s48 offen\n"
"global_load_dword v1, v0, s[6:7] offset:128 sc1\n"
"s_mov_b32 m0, s51\n"
"s_nop 0\n"
"buffer_load_dwordx4 v248, s[24:27], s49 offen lds\n"
"buffer_load_dwordx4 v249, s[24:27], s49 offen offset:1024 lds\n"
"buffer_load_dwordx4 v254, s[24:27], s49 offen offset:2048 lds\n"
"buffer_load_dwordx4 v255, s[24:27], s49 offen offset:3072 lds\n"
"s_add_u32 m0, s51, 4096\n"
"s_add_u32 s40, s49, 40960\n"
"buffer_load_dwordx4 v248, s[24:27], s40 offen lds\n"
"buffer_load_dwordx4 v249, s[24:27], s40 offen offset:1024 lds\n"
"buffer_load_dwordx4 v254, s[24:27], s40 offen offset:2048 lds\n"
"s_add_u32 s49, s49, 128\n"
"s_sub_u32 s41, s49, 1280\n"
"s_min_u32 s49, s49, s41\n"
"s_xor_b32 s51, s51, s74\n"
"s_mov_b32 m0, s50\n"
"s_nop 0\n"
"buffer_load_dwordx4 v224, s[28:31], s48 offen lds\n"
"buffer_load_dwordx4 v225, s[28:31], s48 offen offset:1024 lds\n"
"buffer_load_dwordx4 v226, s[28:31], s48 offen offset:2048 lds\n"
"buffer_load_dwordx4 v227, s[28:31], s48 offen offset:3072 lds\n"
"s_xor_b32 s50, s50, s74\n"
"s_lshl2_add_u32 s63, s11, s42\n"
"s_mul_i32 s63, s63, 10240\n"
"s_and_b32 s78, s42, 1\n"
"s_lshl_b32 s43, s78, 6\n"
"s_mov_b32 s77, 0x80000000\n"
"s_mov_b32 s75, 0x10000\n"
"s_mov_b32 s73, 0xffff0000\n"
"s_mov_b32 s46, 0x3e0293ee\n"
"s_mov_b32 s45, 0x63800000\n"
"s_mov_b32 s62, 9\n"
"v_lshl_add_u32 v242, v4, 4, s44\n"
"s_lshr_b32 s79, s42, 1\n"
"s_lshl4_add_u32 s76, s79, s78\n"
"s_lshl_b32 s76, s76, 10\n"
"s_add_u32 s76, s76, 0x20000\n"
"s_sub_u32 s76, s76, s44\n"
"v_lshlrev_b32 v9, 6, v6\n"
"v_xor_b32 v9, v9, v242\n"
"v_add_u32 v243, s76, v9\n"
"v_and_b32 v9, 6, v5\n"
"s_xor_b32 m0, s50, 0xc000\n"
"s_add_u32 s40, s48, 256\n"
"buffer_load_dwordx4 v224, s[28:31], s40 offen lds\n"
"v_xor_b32 v9, v9, v6\n"
"v_lshlrev_b32 v9, 4, v9\n"
"v_lshl_add_u32 v10, v5, 7, v9\n"
"s_mul_i32 s79, s42, 6144\n"
"s_add_u32 s79, s79, 0x10000\n"
"v_add_u32 v246, s79, v10\n"
"v_xor_b32 v247, 64, v246\n"
"s_lshr_b32 s79, s42, 1\n"
"s_lshl_b32 s79, s79, 11\n"
"buffer_load_dwordx4 v225, s[28:31], s40 offen offset:1024 lds\n"
"s_add_u32 s79, s79, 90112\n"
"v_add_u32 v252, s79, v10\n"
"v_xor_b32 v253, 64, v252\n"
"s_xor_b32 s79, s42, 1\n"
"s_mul_i32 s79, s79, 6144\n"
"s_add_u32 s79, s79, 0x10000\n"
"v_add_u32 v250, s79, v10\n"
"v_xor_b32 v251, 64, v250\n"
"s_mul_i32 s79, s78, 8256\n"
"buffer_load_dwordx4 v226, s[28:31], s40 offen offset:2048 lds\n"
"v_lshrrev_b32 v9, 5, v4\n"
"v_lshlrev_b32 v9, 11, v9\n"
"v_bfe_u32 v10, v4, 2, 3\n"
"v_lshl_add_u32 v9, v10, 7, v9\n"
"v_and_b32 v10, 3, v4\n"
"v_lshl_add_u32 v9, v10, 3, v9\n"
"v_bfe_u32 v10, v4, 3, 2\n"
"v_lshl_add_u32 v9, v10, 5, v9\n"
"s_add_u32 s79, s79, 0x10000\n"
"buffer_load_dwordx4 v227, s[28:31], s40 offen offset:3072 lds\n"
"s_cmp_eq_u32 s47, 8\n"
"s_cselect_b32 s41, s52, s58\n"
"s_add_u32 s48, s48, s41\n"
"s_sub_u32 s41, s48, 5242880\n"
"s_min_u32 s48, s48, s41\n"
"v_xor_b32 v232, s79, v9\n"
"v_xor_b32 v233, 32, v232\n"
"v_xor_b32 v234, 64, v232\n"
"v_xor_b32 v235, 96, v232\n"
"v_xor_b32 v236, 0x2000, v232\n"
"v_xor_b32 v237, 0x2000, v233\n"
"v_xor_b32 v238, 0x2000, v234\n"
"v_xor_b32 v239, 0x2000, v235\n"
"v_lshlrev_b32 v9, 11, v6\n"
"s_mov_b32 m0, s50\n"
"s_nop 0\n"
"buffer_load_dwordx4 v224, s[28:31], s48 offen lds\n"
"v_bfe_u32 v10, v4, 2, 2\n"
"v_lshl_add_u32 v9, v10, 7, v9\n"
"v_and_b32 v10, 1, v4\n"
"v_lshl_add_u32 v9, v10, 3, v9\n"
"v_bfe_u32 v10, v4, 1, 1\n"
"v_lshl_add_u32 v9, v10, 5, v9\n"
"v_bfe_u32 v10, v4, 3, 1\n"
"v_lshl_add_u32 v9, v10, 4, v9\n"
"v_bfe_u32 v10, v4, 4, 1\n"
"buffer_load_dwordx4 v225, s[28:31], s48 offen offset:1024 lds\n"
"v_lshl_add_u32 v9, v10, 6, v9\n"
"v_xor_b32 v244, s43, v9\n"
"v_xor_b32 v245, 16, v244\n"
"s_lshl3_add_u32 s79, s11, s61\n"
"s_lshl_b32 s79, s79, 20\n"
"s_sub_u32 s79, s79, 896\n"
"s_ashr_i32 s60, s79, 31\n"
"s_add_u32 s16, s16, s79\n"
"s_addc_u32 s17, s17, s60\n"
"buffer_load_dwordx4 v226, s[28:31], s48 offen offset:2048 lds\n"
"s_and_b32 s17, s17, 0xffff\n"
"s_mov_b32 s18, 1049472\n"
"s_mov_b64 s[84:85], s[16:17]\n"
"s_mov_b64 s[86:87], s[18:19]\n"
"s_add_u32 s20, s20, s79\n"
"s_addc_u32 s21, s21, s60\n"
"s_and_b32 s21, s21, 0xffff\n"
"s_mov_b32 s22, 1049472\n"
"s_mov_b64 s[88:89], s[20:21]\n"
"buffer_load_dwordx4 v227, s[28:31], s48 offen offset:3072 lds\n"
"s_xor_b32 s50, s50, s74\n"
"s_mov_b64 s[90:91], s[22:23]\n"
"s_lshr_b32 s57, %[wg], 3\n"
"s_mul_i32 s57, s57, 5\n"
"s_and_b32 s57, s57, 15\n"
"s_lshl_b32 s57, s57, 14\n"
"s_mov_b32 s55, 896\n"
"s_add_u32 s56, s57, 896\n"
"s_mov_b32 s59, 16384\n"
"s_mov_b32 s54, -896\n"
"s_mov_b32 m0, s51\n"
"s_nop 0\n"
"buffer_load_dwordx4 v248, s[24:27], s49 offen lds\n"
"s_add_u32 s52, s11, s61\n"
"s_and_b32 s52, s52, 0\n"
"s_mul_i32 s52, s52, 3\n"
"s_add_u32 s52, s52, 1\n"
"s_lshl_b32 s53, s52, 7\n"
"s_lshl_b32 s52, s52, 19\n"
"s_add_u32 s52, s52, 512\n"
"s_mul_i32 s79, s61, 655360\n"
"s_add_u32 s8, s8, s79\n"
"s_addc_u32 s9, s9, 0\n"
"buffer_load_dwordx4 v249, s[24:27], s49 offen offset:1024 lds\n"
"s_and_b32 s9, s9, 0xffff\n"
"s_add_u32 s12, s12, 0x1fffff\n"
"s_addc_u32 s13, s13, 0\n"
"s_and_b32 s12, s12, 0xffe00000\n"
"s_mul_i32 s96, s42, 3\n"
"s_and_b32 s96, s96, 5\n"
"s_cmp_eq_u32 s42, 3\n"
"s_cselect_b32 s96, 5, s96\n"
"buffer_load_dwordx4 v254, s[24:27], s49 offen offset:2048 lds\n"
"s_lshl_b32 s79, s61, 8\n"
"s_add_u32 s96, s96, s79\n"
"s_add_u32 s96, s96, 160\n"
"s_lshl_b32 s96, s96, 12\n"
"s_mul_i32 s79, s92, 8388608\n"
"s_add_u32 s32, s12, s79\n"
"s_addc_u32 s33, s13, 0\n"
"s_and_b32 s33, s33, 0xffff\n"
"s_mov_b32 s34, 8388608\n"
"buffer_load_dwordx4 v255, s[24:27], s49 offen offset:3072 lds\n"
"s_mul_i32 s79, s11, 573440\n"
"s_mul_i32 s60, s10, 1280\n"
"s_sub_u32 s38, 573440, s60\n"
"s_add_u32 s79, s79, s60\n"
"s_add_u32 s36, s14, s79\n"
"s_addc_u32 s37, s15, 0\n"
"s_and_b32 s37, s37, 0xffff\n"
"s_lshl2_add_u32 s93, s61, s42\n"
"s_add_u32 s95, s61, 1\n"
"s_add_u32 m0, s51, 4096\n"
"s_add_u32 s40, s49, 40960\n"
"buffer_load_dwordx4 v248, s[24:27], s40 offen lds\n"
"s_and_b32 s95, s95, 7\n"
"s_lshl_b32 s95, s95, 7\n"
"s_add_u32 s100, s61, 2\n"
"s_and_b32 s100, s100, 7\n"
"s_lshl_b32 s100, s100, 7\n"
"s_lshl_b32 s60, s61, 7\n"
"s_add_u32 s60, s60, s43\n"
"s_mul_i32 s97, s42, 122880\n"
"s_add_u32 s97, s97, s60\n"
"buffer_load_dwordx4 v249, s[24:27], s40 offen offset:1024 lds\n"
"s_lshr_b32 s98, s42, 1\n"
"s_add_u32 s98, s98, 12\n"
"s_mul_i32 s98, s98, 40960\n"
"s_add_u32 s98, s98, s60\n"
"s_mul_i32 s60, s93, 37\n"
"s_lshr_b32 s60, s60, 8\n"
"s_mul_i32 s79, s60, 7\n"
"s_sub_u32 s79, s93, s79\n"
"s_lshl4_add_u32 s94, s60, s79\n"
"buffer_load_dwordx4 v254, s[24:27], s40 offen offset:2048 lds\n"
"s_add_u32 s49, s49, 128\n"
"s_sub_u32 s41, s49, 1280\n"
"s_min_u32 s49, s49, s41\n"
"s_xor_b32 s51, s51, s74\n"
"s_lshl_b32 s94, s94, 10\n"
"s_add_u32 s94, s94, 524288\n"
"s_mul_i32 s99, s60, 3\n"
"s_lshr_b32 s78, s79, 1\n"
"s_add_u32 s99, s99, s78\n"
"s_lshr_b32 s78, s60, 1\n"
"s_add_u32 s78, s78, 12\n"
"s_cmp_eq_u32 s79, 6\n"
"s_cselect_b32 s99, s78, s99\n"
"s_mul_i32 s99, s99, 40960\n"
"s_xor_b32 s79, s79, s60\n"
"s_and_b32 s79, s79, 1\n"
"s_lshl_b32 s79, s79, 6\n"
"s_add_u32 s99, s99, s79\n"
"s_add_u32 s99, s99, 1024\n"
"s_cmp_eq_u32 s61, 7\n"
"s_cselect_b32 s99, 0x40000000, s99\n"
"s_cselect_b32 s94, 0x40000000, s94\n"
"s_waitcnt vmcnt(27)\n"
"v_and_b32 v12, 0x7fff0000, v12\n"
"v_and_b32 v13, 0x7fff0000, v13\n"
"s_nop 0\n"
"v_max_u32_dpp v12, v12, v12 quad_perm:[1,0,3,2] row_mask:0xf bank_mask:0xf\n"
"v_max_u32_dpp v13, v13, v13 quad_perm:[1,0,3,2] row_mask:0xf bank_mask:0xf\n"
"s_nop 0\n"
"v_max_u32_dpp v12, v12, v12 quad_perm:[2,3,0,1] row_mask:0xf bank_mask:0xf\n"
"v_max_u32_dpp v13, v13, v13 quad_perm:[2,3,0,1] row_mask:0xf bank_mask:0xf\n"
"s_memtime s[80:81]\n"
"s_memrealtime s[82:83]\n"
"s_nop 0\n"
"v_max_u32_dpp v12, v12, v12 row_half_mirror row_mask:0xf bank_mask:0xf\n"
"v_max_u32_dpp v13, v13, v13 row_half_mirror row_mask:0xf bank_mask:0xf\n"
"s_nop 0\n"
"v_min_u32_dpp v12, v12, v12 row_mirror row_mask:0xf bank_mask:0xf\n"
"v_min_u32_dpp v13, v13, v13 row_mirror row_mask:0xf bank_mask:0xf\n"
"s_nop 0\n"
"v_min_u32_dpp v12, v12, v12 row_bcast:15 row_mask:0xa bank_mask:0xf\n"
"v_min_u32_dpp v13, v13, v13 row_bcast:15 row_mask:0xa bank_mask:0xf\n"
"s_nop 0\n"
"v_min_u32_dpp v12, v12, v12 row_bcast:31 row_mask:0xc bank_mask:0xf\n"
"v_min_u32_dpp v13, v13, v13 row_bcast:31 row_mask:0xc bank_mask:0xf\n"
"s_nop 0\n"
"v_mul_f32 v12, v12, v13\n"
"v_mul_f32 v12, 0x43c80000, v12\n"
"v_min_f32 v12, 0x71800000, v12\n"
"v_mul_f32 v13, s46, v12\n"
"s_nop 0\n"
"v_readlane_b32 s70, v12, 63\n"
"v_readlane_b32 s71, v12, 63\n"
"v_readlane_b32 s69, v13, 63\n"
"s_nop 1\n"
"v_mov_b64 v[228:229], s[70:71]\n"
"v_mov_b64 v[230:231], s[70:71]\n"
"s_waitcnt vmcnt(26)\n"
"v_readfirstlane_b32 s92, v1\n"
"v_mov_b64 v[0:1], s[70:71]\n"
"v_mov_b64 v[2:3], s[70:71]\n"
"s_mov_b32 s70, s46\n"
"s_xor_b32 s71, s69, s77\n"
"s_waitcnt lgkmcnt(0)\n"
"s_sub_u32 s80, s80, s64\n"
"s_sub_u32 s82, s82, s66\n"
"s_mul_i32 s80, s80, 10\n"
"s_mul_i32 s82, s82, 211\n"
"s_cmp_gt_u32 s80, s82\n"
"s_cselect_b32 s64, 1, 0\n"
"s_cselect_b32 s68, 655360, 0\n"
"s_cmp_eq_u32 s42, 0\n"
"s_cselect_b32 s64, s64, 0\n"
"s_mov_b32 s65, 0\n"
"s_add_u32 s66, s66, 2800\n"
"s_mov_b32 s67, s66\n"
"s_mov_b32 s72, 126\n"
"s_mov_b64 vcc, s[64:65]\n"
"s_nop 2\n"
"s_mul_i32 s60, s92, 0x9e3779b1\n"
"s_add_u32 s60, s60, 0x7f4a7c15\n"
"s_waitcnt vmcnt(15)\n"
"s_barrier\n"
"ds_read_b128 a[136:139], v246 offset:0\n"
"ds_read_b128 a[140:143], v247 offset:0\n"
"ds_read_b64_tr_b16 a[192:193], v244 offset:0\n"
"ds_read_b64_tr_b16 a[194:195], v244 offset:512\n"
"ds_read_b64_tr_b16 a[196:197], v244 offset:8192\n"
"ds_read_b64_tr_b16 a[198:199], v244 offset:8704\n"
"ds_read_b64_tr_b16 a[200:201], v245 offset:0\n"
"ds_read_b64_tr_b16 a[202:203], v245 offset:512\n"
"ds_read_b64_tr_b16 a[204:205], v245 offset:8192\n"
"ds_read_b64_tr_b16 a[206:207], v245 offset:8704\n"
"ds_read_b64_tr_b16 a[208:209], v244 offset:1024\n"
"ds_read_b64_tr_b16 a[210:211], v244 offset:1536\n"
"ds_read_b64_tr_b16 a[212:213], v244 offset:9216\n"
"ds_read_b64_tr_b16 a[214:215], v244 offset:9728\n"
"s_waitcnt lgkmcnt(10)\n"
"v_mfma_f32_16x16x32_bf16 v[112:115], a[192:195], a[136:139], v[228:231]\n"
"ds_read_b64_tr_b16 a[216:217], v245 offset:1024\n"
"ds_read_b64_tr_b16 a[218:219], v245 offset:1536\n"
"s_waitcnt lgkmcnt(10)\n"
"v_mfma_f32_16x16x32_bf16 v[112:115], a[196:199], a[140:143], v[112:115]\n"
"ds_read_b64_tr_b16 a[220:221], v245 offset:9216\n"
"ds_read_b64_tr_b16 a[222:223], v245 offset:9728\n"
"s_waitcnt lgkmcnt(10)\n"
"v_mfma_f32_16x16x32_bf16 v[116:119], a[200:203], a[136:139], v[228:231]\n"
"s_waitcnt lgkmcnt(8)\n"
"ds_read_b128 a[144:147], v246 offset:2048\n"
"v_mfma_f32_16x16x32_bf16 v[116:119], a[204:207], a[140:143], v[116:119]\n"
"s_waitcnt lgkmcnt(7)\n"
"ds_read_b128 a[148:151], v247 offset:2048\n"
"v_mfma_f32_16x16x32_bf16 v[120:123], a[208:211], a[136:139], v[228:231]\n"
"s_waitcnt lgkmcnt(6)\n"
"ds_read_b128 a[152:155], v246 offset:4096\n"
"v_mfma_f32_16x16x32_bf16 v[120:123], a[212:215], a[140:143], v[120:123]\n"
"s_waitcnt lgkmcnt(5)\n"
"ds_read_b128 a[156:159], v247 offset:4096\n"
"v_mfma_f32_16x16x32_bf16 v[124:127], a[216:219], a[136:139], v[228:231]\n"
"s_waitcnt lgkmcnt(4)\n"
"ds_read_b128 a[160:163], v252 offset:0\n"
"v_mfma_f32_16x16x32_bf16 v[124:127], a[220:223], a[140:143], v[124:127]\n"
"s_waitcnt lgkmcnt(4)\n"
"ds_read_b128 a[164:167], v253 offset:0\n"
"v_mfma_f32_16x16x32_bf16 v[140:143], a[216:219], a[144:147], v[228:231]\n"
"s_waitcnt lgkmcnt(4)\n"
"ds_read_b128 a[168:171], v250 offset:0\n"
"v_mfma_f32_16x16x32_bf16 v[140:143], a[220:223], a[148:151], v[140:143]\n"
"ds_read_b128 a[172:175], v251 offset:0\n"
"v_mfma_f32_16x16x32_bf16 v[136:139], a[208:211], a[144:147], v[228:231]\n"
"s_waitcnt vmcnt(11)\n"
"ds_read_b128 a[176:179], v250 offset:2048\n"
"v_mfma_f32_16x16x32_bf16 v[136:139], a[212:215], a[148:151], v[136:139]\n"
"s_barrier\n"
"v_mfma_f32_16x16x32_bf16 v[132:135], a[200:203], a[144:147], v[228:231]\n"
"s_add_u32 s40, s48, 256\n"
"s_xor_b32 m0, s50, 0xc000\n"
"ds_read_b128 a[180:183], v251 offset:2048\n"
"v_mfma_f32_16x16x32_bf16 v[132:135], a[204:207], a[148:151], v[132:135]\n"
"buffer_load_dwordx4 v224, s[28:31], s40 offen lds\n"
"v_mfma_f32_16x16x32_bf16 v[128:131], a[192:195], a[144:147], v[228:231]\n"
"ds_read_b128 a[184:187], v250 offset:4096\n"
"v_mfma_f32_16x16x32_bf16 v[128:131], a[196:199], a[148:151], v[128:131]\n"
"s_waitcnt lgkmcnt(5)\n"
"ds_read_b128 a[188:191], v251 offset:4096\n"
"v_mfma_f32_16x16x32_bf16 v[144:147], a[192:195], a[152:155], v[228:231]\n"
"ds_read_b64_tr_b16 a[224:225], v244 offset:16384\n"
"v_mfma_f32_16x16x32_bf16 v[144:147], a[196:199], a[156:159], v[144:147]\n"
"ds_read_b64_tr_b16 a[226:227], v244 offset:16896\n"
"v_mfma_f32_16x16x32_bf16 v[148:151], a[200:203], a[152:155], v[228:231]\n"
"ds_read_b64_tr_b16 a[228:229], v244 offset:24576\n"
"v_mfma_f32_16x16x32_bf16 v[148:151], a[204:207], a[156:159], v[148:151]\n"
"buffer_load_dwordx4 v225, s[28:31], s40 offen offset:1024 lds\n"
"v_mfma_f32_16x16x32_bf16 v[152:155], a[208:211], a[152:155], v[228:231]\n"
"ds_read_b64_tr_b16 a[230:231], v244 offset:25088\n"
"v_mfma_f32_16x16x32_bf16 v[152:155], a[212:215], a[156:159], v[152:155]\n"
"ds_read_b64_tr_b16 a[232:233], v245 offset:16384\n"
"v_mfma_f32_16x16x32_bf16 v[156:159], a[216:219], a[152:155], v[228:231]\n"
"ds_read_b64_tr_b16 a[234:235], v245 offset:16896\n"
"v_mfma_f32_16x16x32_bf16 v[156:159], a[220:223], a[156:159], v[156:159]\n"
"ds_read_b64_tr_b16 a[236:237], v245 offset:24576\n"
"v_mfma_f32_16x16x32_bf16 v[172:175], a[216:219], a[160:163], v[228:231]\n"
"ds_read_b64_tr_b16 a[238:239], v245 offset:25088\n"
"v_mfma_f32_16x16x32_bf16 v[172:175], a[220:223], a[164:167], v[172:175]\n"
"buffer_load_dwordx4 v226, s[28:31], s40 offen offset:2048 lds\n"
"v_mfma_f32_16x16x32_bf16 v[168:171], a[208:211], a[160:163], v[228:231]\n"
"ds_read_b64_tr_b16 a[240:241], v244 offset:17408\n"
"v_mfma_f32_16x16x32_bf16 v[168:171], a[212:215], a[164:167], v[168:171]\n"
"ds_read_b64_tr_b16 a[242:243], v244 offset:17920\n"
"v_mfma_f32_16x16x32_bf16 v[164:167], a[200:203], a[160:163], v[228:231]\n"
"ds_read_b64_tr_b16 a[244:245], v244 offset:25600\n"
"v_mfma_f32_16x16x32_bf16 v[164:167], a[204:207], a[164:167], v[164:167]\n"
"buffer_load_dwordx4 v227, s[28:31], s40 offen offset:3072 lds\n"
"v_mfma_f32_16x16x32_bf16 v[160:163], a[192:195], a[160:163], v[228:231]\n"
"s_cmp_eq_u32 s47, 8\n"
"ds_read_b64_tr_b16 a[246:247], v244 offset:26112\n"
"v_xor_b32 v244, s74, v244\n"
"v_mfma_f32_16x16x32_bf16 v[160:163], a[196:199], a[164:167], v[160:163]\n"
"s_waitcnt lgkmcnt(5)\n"
"s_cselect_b32 s41, s52, s58\n"
"ds_read_b64_tr_b16 a[248:249], v245 offset:17408\n"
"v_xor_b32 v246, s74, v246\n"
"v_mfma_f32_16x16x32_bf16 v[176:179], a[192:195], a[168:171], v[228:231]\n"
"s_add_u32 s48, s48, s41\n"
"ds_read_b64_tr_b16 a[250:251], v245 offset:17920\n"
"v_xor_b32 v247, s74, v247\n"
"v_mfma_f32_16x16x32_bf16 v[176:179], a[196:199], a[172:175], v[176:179]\n"
"s_sub_u32 s41, s48, 5242880\n"
"ds_read_b64_tr_b16 a[252:253], v245 offset:25600\n"
"v_xor_b32 v252, s74, v252\n"
"v_mfma_f32_16x16x32_bf16 v[180:183], a[200:203], a[168:171], v[228:231]\n"
"s_min_u32 s48, s48, s41\n"
"ds_read_b64_tr_b16 a[254:255], v245 offset:26112\n"
"v_xor_b32 v245, s74, v245\n"
"v_mfma_f32_16x16x32_bf16 v[180:183], a[204:207], a[172:175], v[180:183]\n"
"v_xor_b32 v253, s74, v253\n"
"v_xor_b32 v250, s74, v250\n"
"v_mfma_f32_16x16x32_bf16 v[184:187], a[208:211], a[168:171], v[228:231]\n"
"s_mov_b32 m0, s50\n"
"v_xor_b32 v251, s74, v251\n"
"v_mfma_f32_16x16x32_bf16 v[184:187], a[212:215], a[172:175], v[184:187]\n"
"buffer_load_dwordx4 v224, s[28:31], s48 offen lds\n"
"v_mfma_f32_16x16x32_bf16 v[188:191], a[216:219], a[168:171], v[228:231]\n"
"v_mfma_f32_16x16x32_bf16 v[188:191], a[220:223], a[172:175], v[188:191]\n"
"v_mfma_f32_16x16x32_bf16 v[204:207], a[216:219], a[176:179], v[228:231]\n"
"v_mfma_f32_16x16x32_bf16 v[204:207], a[220:223], a[180:183], v[204:207]\n"
"buffer_load_dwordx4 v225, s[28:31], s48 offen offset:1024 lds\n"
"v_mfma_f32_16x16x32_bf16 v[200:203], a[208:211], a[176:179], v[228:231]\n"
"v_mfma_f32_16x16x32_bf16 v[200:203], a[212:215], a[180:183], v[200:203]\n"
"v_mfma_f32_16x16x32_bf16 v[196:199], a[200:203], a[176:179], v[228:231]\n"
"v_mfma_f32_16x16x32_bf16 v[196:199], a[204:207], a[180:183], v[196:199]\n"
"v_mfma_f32_16x16x32_bf16 v[192:195], a[192:195], a[176:179], v[228:231]\n"
"v_mfma_f32_16x16x32_bf16 v[192:195], a[196:199], a[180:183], v[192:195]\n"
"buffer_load_dwordx4 v226, s[28:31], s48 offen offset:2048 lds\n"
"v_mfma_f32_16x16x32_bf16 v[208:211], a[192:195], a[184:187], v[228:231]\n"
"v_mfma_f32_16x16x32_bf16 v[208:211], a[196:199], a[188:191], v[208:211]\n"
"v_mfma_f32_16x16x32_bf16 v[212:215], a[200:203], a[184:187], v[228:231]\n"
"v_mfma_f32_16x16x32_bf16 v[212:215], a[204:207], a[188:191], v[212:215]\n"
"v_mfma_f32_16x16x32_bf16 v[216:219], a[208:211], a[184:187], v[228:231]\n"
"v_mfma_f32_16x16x32_bf16 v[216:219], a[212:215], a[188:191], v[216:219]\n"
"buffer_load_dwordx4 v227, s[28:31], s48 offen offset:3072 lds\n"
"v_mfma_f32_16x16x32_bf16 v[220:223], a[216:219], a[184:187], v[228:231]\n"
"s_xor_b32 s50, s50, s74\n"
"v_mfma_f32_16x16x32_bf16 v[220:223], a[220:223], a[188:191], v[220:223]\n"
"v_mfma_f32_16x16x32_bf16 v[0:3], a[224:227], a[136:139], v[228:231]\n"
"v_mfma_f32_16x16x32_bf16 v[0:3], a[228:231], a[140:143], v[0:3]\n"
"v_mfma_f32_16x16x32_bf16 v[4:7], a[232:235], a[136:139], v[228:231]\n"
"s_waitcnt vmcnt(8) lgkmcnt(0)\n"
"v_mfma_f32_16x16x32_bf16 v[4:7], a[236:239], a[140:143], v[4:7]\n"
"s_barrier\n"
"v_mfma_f32_16x16x32_bf16 v[8:11], a[240:243], a[136:139], v[228:231]\n"
"s_mov_b32 m0, s51\n"
"ds_read_b64_tr_b16 a[192:193], v244 offset:0\n"
"v_mfma_f32_16x16x32_bf16 v[8:11], a[244:247], a[140:143], v[8:11]\n"
"buffer_load_dwordx4 v248, s[24:27], s49 offen lds\n"
"v_mfma_f32_16x16x32_bf16 v[12:15], a[248:251], a[136:139], v[228:231]\n"
"ds_read_b64_tr_b16 a[194:195], v244 offset:512\n"
"v_mfma_f32_16x16x32_bf16 v[12:15], a[252:255], a[140:143], v[12:15]\n"
"ds_read_b128 a[136:139], v246 offset:0\n"
"v_mfma_f32_16x16x32_bf16 v[28:31], a[248:251], a[144:147], v[228:231]\n"
"ds_read_b64_tr_b16 a[196:197], v244 offset:8192\n"
"v_mfma_f32_16x16x32_bf16 v[28:31], a[252:255], a[148:151], v[28:31]\n"
"ds_read_b64_tr_b16 a[198:199], v244 offset:8704\n"
"v_mfma_f32_16x16x32_bf16 v[24:27], a[240:243], a[144:147], v[228:231]\n"
"ds_read_b128 a[140:143], v247 offset:0\n"
"v_mfma_f32_16x16x32_bf16 v[24:27], a[244:247], a[148:151], v[24:27]\n"
"buffer_load_dwordx4 v249, s[24:27], s49 offen offset:1024 lds\n"
"v_mfma_f32_16x16x32_bf16 v[20:23], a[232:235], a[144:147], v[228:231]\n"
"ds_read_b64_tr_b16 a[200:201], v245 offset:0\n"
"v_mfma_f32_16x16x32_bf16 v[20:23], a[236:239], a[148:151], v[20:23]\n"
"ds_read_b64_tr_b16 a[202:203], v245 offset:512\n"
"v_mfma_f32_16x16x32_bf16 v[16:19], a[224:227], a[144:147], v[228:231]\n"
"ds_read_b64_tr_b16 a[204:205], v245 offset:8192\n"
"v_mfma_f32_16x16x32_bf16 v[16:19], a[228:231], a[148:151], v[16:19]\n"
"ds_read_b64_tr_b16 a[206:207], v245 offset:8704\n"
"v_mfma_f32_16x16x32_bf16 v[32:35], a[224:227], a[152:155], v[228:231]\n"
"ds_read_b64_tr_b16 a[208:209], v244 offset:1024\n"
"v_mfma_f32_16x16x32_bf16 v[32:35], a[228:231], a[156:159], v[32:35]\n"
"buffer_load_dwordx4 v254, s[24:27], s49 offen offset:2048 lds\n"
"v_mfma_f32_16x16x32_bf16 v[36:39], a[232:235], a[152:155], v[228:231]\n"
"ds_read_b64_tr_b16 a[210:211], v244 offset:1536\n"
"v_mfma_f32_16x16x32_bf16 v[36:39], a[236:239], a[156:159], v[36:39]\n"
"ds_read_b64_tr_b16 a[212:213], v244 offset:9216\n"
"v_mfma_f32_16x16x32_bf16 v[40:43], a[240:243], a[152:155], v[228:231]\n"
"ds_read_b64_tr_b16 a[214:215], v244 offset:9728\n"
"v_mfma_f32_16x16x32_bf16 v[40:43], a[244:247], a[156:159], v[40:43]\n"
"ds_read_b64_tr_b16 a[216:217], v245 offset:1024\n"
"v_mfma_f32_16x16x32_bf16 v[44:47], a[248:251], a[152:155], v[228:231]\n"
"ds_read_b64_tr_b16 a[218:219], v245 offset:1536\n"
"v_mfma_f32_16x16x32_bf16 v[44:47], a[252:255], a[156:159], v[44:47]\n"
"buffer_load_dwordx4 v255, s[24:27], s49 offen offset:3072 lds\n"
"v_mfma_f32_16x16x32_bf16 v[60:63], a[248:251], a[160:163], v[228:231]\n"
"ds_read_b64_tr_b16 a[220:221], v245 offset:9216\n"
"v_mfma_f32_16x16x32_bf16 v[60:63], a[252:255], a[164:167], v[60:63]\n"
"ds_read_b64_tr_b16 a[222:223], v245 offset:9728\n"
"v_mfma_f32_16x16x32_bf16 v[56:59], a[240:243], a[160:163], v[228:231]\n"
"ds_read_b128 a[144:147], v246 offset:2048\n"
"v_mfma_f32_16x16x32_bf16 v[56:59], a[244:247], a[164:167], v[56:59]\n"
"s_add_u32 s40, s49, 40960\n"
"ds_read_b128 a[148:151], v247 offset:2048\n"
"v_mfma_f32_16x16x32_bf16 v[52:55], a[232:235], a[160:163], v[228:231]\n"
"s_add_u32 m0, s51, 4096\n"
"ds_read_b128 a[152:155], v246 offset:4096\n"
"v_mfma_f32_16x16x32_bf16 v[52:55], a[236:239], a[164:167], v[52:55]\n"
"buffer_load_dwordx4 v248, s[24:27], s40 offen lds\n"
"v_mfma_f32_16x16x32_bf16 v[48:51], a[224:227], a[160:163], v[228:231]\n"
"ds_read_b128 a[156:159], v247 offset:4096\n"
"v_mfma_f32_16x16x32_bf16 v[48:51], a[228:231], a[164:167], v[48:51]\n"
"ds_read_b128 a[160:163], v252 offset:0\n"
"v_mfma_f32_16x16x32_bf16 v[64:67], a[224:227], a[168:171], v[228:231]\n"
"ds_read_b128 a[164:167], v253 offset:0\n"
"v_mfma_f32_16x16x32_bf16 v[64:67], a[228:231], a[172:175], v[64:67]\n"
"v_mfma_f32_16x16x32_bf16 v[68:71], a[232:235], a[168:171], v[228:231]\n"
"v_mfma_f32_16x16x32_bf16 v[68:71], a[236:239], a[172:175], v[68:71]\n"
"buffer_load_dwordx4 v249, s[24:27], s40 offen offset:1024 lds\n"
"v_mfma_f32_16x16x32_bf16 v[72:75], a[240:243], a[168:171], v[228:231]\n"
"v_mfma_f32_16x16x32_bf16 v[72:75], a[244:247], a[172:175], v[72:75]\n"
"v_mfma_f32_16x16x32_bf16 v[76:79], a[248:251], a[168:171], v[228:231]\n"
"ds_read_b128 a[168:171], v250 offset:0\n"
"v_mfma_f32_16x16x32_bf16 v[76:79], a[252:255], a[172:175], v[76:79]\n"
"ds_read_b128 a[172:175], v251 offset:0\n"
"v_mfma_f32_16x16x32_bf16 v[92:95], a[248:251], a[176:179], v[228:231]\n"
"v_mfma_f32_16x16x32_bf16 v[92:95], a[252:255], a[180:183], v[92:95]\n"
"buffer_load_dwordx4 v254, s[24:27], s40 offen offset:2048 lds\n"
"v_mfma_f32_16x16x32_bf16 v[88:91], a[240:243], a[176:179], v[228:231]\n"
"s_add_u32 s49, s49, 128\n"
"v_mfma_f32_16x16x32_bf16 v[88:91], a[244:247], a[180:183], v[88:91]\n"
"s_sub_u32 s41, s49, 1280\n"
"v_mfma_f32_16x16x32_bf16 v[84:87], a[232:235], a[176:179], v[228:231]\n"
"s_min_u32 s49, s49, s41\n"
"v_mfma_f32_16x16x32_bf16 v[84:87], a[236:239], a[180:183], v[84:87]\n"
"s_xor_b32 s51, s51, s74\n"
"v_mfma_f32_16x16x32_bf16 v[80:83], a[224:227], a[176:179], v[228:231]\n"
"ds_read_b128 a[176:179], v250 offset:2048\n"
"v_mfma_f32_16x16x32_bf16 v[80:83], a[228:231], a[180:183], v[80:83]\n"
"ds_read_b128 a[180:183], v251 offset:2048\n"
"v_mfma_f32_16x16x32_bf16 v[96:99], a[224:227], a[184:187], v[228:231]\n"
"v_mfma_f32_16x16x32_bf16 v[96:99], a[228:231], a[188:191], v[96:99]\n"
"v_mfma_f32_16x16x32_bf16 v[100:103], a[232:235], a[184:187], v[228:231]\n"
"v_mfma_f32_16x16x32_bf16 v[100:103], a[236:239], a[188:191], v[100:103]\n"
"v_mfma_f32_16x16x32_bf16 v[104:107], a[240:243], a[184:187], v[228:231]\n"
"v_mfma_f32_16x16x32_bf16 v[104:107], a[244:247], a[188:191], v[104:107]\n"
"v_mfma_f32_16x16x32_bf16 v[108:111], a[248:251], a[184:187], v[228:231]\n"
"s_add_u32 s47, s47, 1\n"
"ds_read_b128 a[184:187], v250 offset:4096\n"
"v_mfma_f32_16x16x32_bf16 v[108:111], a[252:255], a[188:191], v[108:111]\n"
"s_waitcnt lgkmcnt(1)\n"
"ds_read_b128 a[188:191], v251 offset:4096\n"
"LPL_%=:\n"
"v_mfma_f32_16x16x32_bf16 v[112:115], a[192:195], a[136:139], v[112:115]\n"
"v_mfma_f32_16x16x32_bf16 v[112:115], a[196:199], a[140:143], v[112:115]\n"
"v_mfma_f32_16x16x32_bf16 v[116:119], a[200:203], a[136:139], v[116:119]\n"
"s_waitcnt vmcnt(11)\n"
"v_mfma_f32_16x16x32_bf16 v[116:119], a[204:207], a[140:143], v[116:119]\n"
"s_barrier\n"
"v_mfma_f32_16x16x32_bf16 v[120:123], a[208:211], a[136:139], v[120:123]\n"
"s_add_u32 s40, s48, 256\n"
"s_xor_b32 m0, s50, 0xc000\n"
"ds_read_b64_tr_b16 a[224:225], v244 offset:16384\n"
"v_mfma_f32_16x16x32_bf16 v[120:123], a[212:215], a[140:143], v[120:123]\n"
"buffer_load_dwordx4 v224, s[28:31], s40 offen lds\n"
"v_mfma_f32_16x16x32_bf16 v[124:127], a[216:219], a[136:139], v[124:127]\n"
"ds_read_b64_tr_b16 a[226:227], v244 offset:16896\n"
"v_mfma_f32_16x16x32_bf16 v[124:127], a[220:223], a[140:143], v[124:127]\n"
"ds_read_b64_tr_b16 a[228:229], v244 offset:24576\n"
"v_mfma_f32_16x16x32_bf16 v[140:143], a[216:219], a[144:147], v[140:143]\n"
"ds_read_b64_tr_b16 a[230:231], v244 offset:25088\n"
"v_mfma_f32_16x16x32_bf16 v[140:143], a[220:223], a[148:151], v[140:143]\n"
"ds_read_b64_tr_b16 a[232:233], v245 offset:16384\n"
"v_mfma_f32_16x16x32_bf16 v[136:139], a[208:211], a[144:147], v[136:139]\n"
"ds_read_b64_tr_b16 a[234:235], v245 offset:16896\n"
"v_mfma_f32_16x16x32_bf16 v[136:139], a[212:215], a[148:151], v[136:139]\n"
"buffer_load_dwordx4 v225, s[28:31], s40 offen offset:1024 lds\n"
"v_mfma_f32_16x16x32_bf16 v[132:135], a[200:203], a[144:147], v[132:135]\n"
"ds_read_b64_tr_b16 a[236:237], v245 offset:24576\n"
"v_mfma_f32_16x16x32_bf16 v[132:135], a[204:207], a[148:151], v[132:135]\n"
"ds_read_b64_tr_b16 a[238:239], v245 offset:25088\n"
"v_mfma_f32_16x16x32_bf16 v[128:131], a[192:195], a[144:147], v[128:131]\n"
"ds_read_b64_tr_b16 a[240:241], v244 offset:17408\n"
"v_mfma_f32_16x16x32_bf16 v[128:131], a[196:199], a[148:151], v[128:131]\n"
"ds_read_b64_tr_b16 a[242:243], v244 offset:17920\n"
"v_mfma_f32_16x16x32_bf16 v[144:147], a[192:195], a[152:155], v[144:147]\n"
"ds_read_b64_tr_b16 a[244:245], v244 offset:25600\n"
"v_mfma_f32_16x16x32_bf16 v[144:147], a[196:199], a[156:159], v[144:147]\n"
"buffer_load_dwordx4 v226, s[28:31], s40 offen offset:2048 lds\n"
"v_mfma_f32_16x16x32_bf16 v[148:151], a[200:203], a[152:155], v[148:151]\n"
"ds_read_b64_tr_b16 a[246:247], v244 offset:26112\n"
"v_mfma_f32_16x16x32_bf16 v[148:151], a[204:207], a[156:159], v[148:151]\n"
"ds_read_b64_tr_b16 a[248:249], v245 offset:17408\n"
"v_mfma_f32_16x16x32_bf16 v[152:155], a[208:211], a[152:155], v[152:155]\n"
"ds_read_b64_tr_b16 a[250:251], v245 offset:17920\n"
"v_mfma_f32_16x16x32_bf16 v[152:155], a[212:215], a[156:159], v[152:155]\n"
"ds_read_b64_tr_b16 a[252:253], v245 offset:25600\n"
"v_mfma_f32_16x16x32_bf16 v[156:159], a[216:219], a[152:155], v[156:159]\n"
"ds_read_b64_tr_b16 a[254:255], v245 offset:26112\n"
"v_mfma_f32_16x16x32_bf16 v[156:159], a[220:223], a[156:159], v[156:159]\n"
"buffer_load_dwordx4 v227, s[28:31], s40 offen offset:3072 lds\n"
"v_mfma_f32_16x16x32_bf16 v[172:175], a[216:219], a[160:163], v[172:175]\n"
"s_cmp_eq_u32 s47, 8\n"
"v_mfma_f32_16x16x32_bf16 v[172:175], a[220:223], a[164:167], v[172:175]\n"
"s_cselect_b32 s41, s52, s58\n"
"v_mfma_f32_16x16x32_bf16 v[168:171], a[208:211], a[160:163], v[168:171]\n"
"s_add_u32 s48, s48, s41\n"
"v_mfma_f32_16x16x32_bf16 v[168:171], a[212:215], a[164:167], v[168:171]\n"
"s_sub_u32 s41, s48, 5242880\n"
"v_mfma_f32_16x16x32_bf16 v[164:167], a[200:203], a[160:163], v[164:167]\n"
"s_min_u32 s48, s48, s41\n"
"v_mfma_f32_16x16x32_bf16 v[164:167], a[204:207], a[164:167], v[164:167]\n"
"v_xor_b32 v244, s74, v244\n"
"v_xor_b32 v246, s74, v246\n"
"v_mfma_f32_16x16x32_bf16 v[160:163], a[192:195], a[160:163], v[160:163]\n"
"s_mov_b32 m0, s50\n"
"v_xor_b32 v245, s74, v245\n"
"v_xor_b32 v247, s74, v247\n"
"v_mfma_f32_16x16x32_bf16 v[160:163], a[196:199], a[164:167], v[160:163]\n"
"buffer_load_dwordx4 v224, s[28:31], s48 offen lds\n"
"v_mfma_f32_16x16x32_bf16 v[176:179], a[192:195], a[168:171], v[176:179]\n"
"v_xor_b32 v252, s74, v252\n"
"v_xor_b32 v253, s74, v253\n"
"v_mfma_f32_16x16x32_bf16 v[176:179], a[196:199], a[172:175], v[176:179]\n"
"v_xor_b32 v250, s74, v250\n"
"v_xor_b32 v251, s74, v251\n"
"v_mfma_f32_16x16x32_bf16 v[180:183], a[200:203], a[168:171], v[180:183]\n"
"v_mfma_f32_16x16x32_bf16 v[180:183], a[204:207], a[172:175], v[180:183]\n"
"buffer_load_dwordx4 v225, s[28:31], s48 offen offset:1024 lds\n"
"v_mfma_f32_16x16x32_bf16 v[184:187], a[208:211], a[168:171], v[184:187]\n"
"v_mfma_f32_16x16x32_bf16 v[184:187], a[212:215], a[172:175], v[184:187]\n"
"v_mfma_f32_16x16x32_bf16 v[188:191], a[216:219], a[168:171], v[188:191]\n"
"v_mfma_f32_16x16x32_bf16 v[188:191], a[220:223], a[172:175], v[188:191]\n"
"v_mfma_f32_16x16x32_bf16 v[204:207], a[216:219], a[176:179], v[204:207]\n"
"v_mfma_f32_16x16x32_bf16 v[204:207], a[220:223], a[180:183], v[204:207]\n"
"buffer_load_dwordx4 v226, s[28:31], s48 offen offset:2048 lds\n"
"v_mfma_f32_16x16x32_bf16 v[200:203], a[208:211], a[176:179], v[200:203]\n"
"v_mfma_f32_16x16x32_bf16 v[200:203], a[212:215], a[180:183], v[200:203]\n"
"v_mfma_f32_16x16x32_bf16 v[196:199], a[200:203], a[176:179], v[196:199]\n"
"v_mfma_f32_16x16x32_bf16 v[196:199], a[204:207], a[180:183], v[196:199]\n"
"v_mfma_f32_16x16x32_bf16 v[192:195], a[192:195], a[176:179], v[192:195]\n"
"v_mfma_f32_16x16x32_bf16 v[192:195], a[196:199], a[180:183], v[192:195]\n"
"s_waitcnt lgkmcnt(0)\n"
"buffer_load_dwordx4 v227, s[28:31], s48 offen offset:3072 lds\n"
"v_mfma_f32_16x16x32_bf16 v[208:211], a[192:195], a[184:187], v[208:211]\n"
"s_xor_b32 s50, s50, s74\n"
"v_mfma_f32_16x16x32_bf16 v[208:211], a[196:199], a[188:191], v[208:211]\n"
"v_mfma_f32_16x16x32_bf16 v[212:215], a[200:203], a[184:187], v[212:215]\n"
"v_mfma_f32_16x16x32_bf16 v[212:215], a[204:207], a[188:191], v[212:215]\n"
"v_mfma_f32_16x16x32_bf16 v[216:219], a[208:211], a[184:187], v[216:219]\n"
"v_mfma_f32_16x16x32_bf16 v[216:219], a[212:215], a[188:191], v[216:219]\n"
"v_mfma_f32_16x16x32_bf16 v[220:223], a[216:219], a[184:187], v[220:223]\n"
"v_mfma_f32_16x16x32_bf16 v[220:223], a[220:223], a[188:191], v[220:223]\n"
"v_mfma_f32_16x16x32_bf16 v[0:3], a[224:227], a[136:139], v[0:3]\n"
"v_mfma_f32_16x16x32_bf16 v[0:3], a[228:231], a[140:143], v[0:3]\n"
"v_mfma_f32_16x16x32_bf16 v[4:7], a[232:235], a[136:139], v[4:7]\n"
"s_waitcnt vmcnt(8)\n"
"v_mfma_f32_16x16x32_bf16 v[4:7], a[236:239], a[140:143], v[4:7]\n"
"s_barrier\n"
"v_mfma_f32_16x16x32_bf16 v[8:11], a[240:243], a[136:139], v[8:11]\n"
"s_mov_b32 m0, s51\n"
"ds_read_b64_tr_b16 a[192:193], v244 offset:0\n"
"v_mfma_f32_16x16x32_bf16 v[8:11], a[244:247], a[140:143], v[8:11]\n"
"buffer_load_dwordx4 v248, s[24:27], s49 offen lds\n"
"v_mfma_f32_16x16x32_bf16 v[12:15], a[248:251], a[136:139], v[12:15]\n"
"ds_read_b64_tr_b16 a[194:195], v244 offset:512\n"
"v_mfma_f32_16x16x32_bf16 v[12:15], a[252:255], a[140:143], v[12:15]\n"
"ds_read_b128 a[136:139], v246 offset:0\n"
"v_mfma_f32_16x16x32_bf16 v[28:31], a[248:251], a[144:147], v[28:31]\n"
"ds_read_b64_tr_b16 a[196:197], v244 offset:8192\n"
"v_mfma_f32_16x16x32_bf16 v[28:31], a[252:255], a[148:151], v[28:31]\n"
"ds_read_b64_tr_b16 a[198:199], v244 offset:8704\n"
"v_mfma_f32_16x16x32_bf16 v[24:27], a[240:243], a[144:147], v[24:27]\n"
"ds_read_b128 a[140:143], v247 offset:0\n"
"v_mfma_f32_16x16x32_bf16 v[24:27], a[244:247], a[148:151], v[24:27]\n"
"buffer_load_dwordx4 v249, s[24:27], s49 offen offset:1024 lds\n"
"v_mfma_f32_16x16x32_bf16 v[20:23], a[232:235], a[144:147], v[20:23]\n"
"ds_read_b64_tr_b16 a[200:201], v245 offset:0\n"
"v_mfma_f32_16x16x32_bf16 v[20:23], a[236:239], a[148:151], v[20:23]\n"
"ds_read_b64_tr_b16 a[202:203], v245 offset:512\n"
"v_mfma_f32_16x16x32_bf16 v[16:19], a[224:227], a[144:147], v[16:19]\n"
"ds_read_b64_tr_b16 a[204:205], v245 offset:8192\n"
"v_mfma_f32_16x16x32_bf16 v[16:19], a[228:231], a[148:151], v[16:19]\n"
"ds_read_b64_tr_b16 a[206:207], v245 offset:8704\n"
"v_mfma_f32_16x16x32_bf16 v[32:35], a[224:227], a[152:155], v[32:35]\n"
"ds_read_b64_tr_b16 a[208:209], v244 offset:1024\n"
"v_mfma_f32_16x16x32_bf16 v[32:35], a[228:231], a[156:159], v[32:35]\n"
"buffer_load_dwordx4 v254, s[24:27], s49 offen offset:2048 lds\n"
"v_mfma_f32_16x16x32_bf16 v[36:39], a[232:235], a[152:155], v[36:39]\n"
"ds_read_b64_tr_b16 a[210:211], v244 offset:1536\n"
"v_mfma_f32_16x16x32_bf16 v[36:39], a[236:239], a[156:159], v[36:39]\n"
"ds_read_b64_tr_b16 a[212:213], v244 offset:9216\n"
"v_mfma_f32_16x16x32_bf16 v[40:43], a[240:243], a[152:155], v[40:43]\n"
"ds_read_b64_tr_b16 a[214:215], v244 offset:9728\n"
"v_mfma_f32_16x16x32_bf16 v[40:43], a[244:247], a[156:159], v[40:43]\n"
"ds_read_b64_tr_b16 a[216:217], v245 offset:1024\n"
"v_mfma_f32_16x16x32_bf16 v[44:47], a[248:251], a[152:155], v[44:47]\n"
"ds_read_b64_tr_b16 a[218:219], v245 offset:1536\n"
"v_mfma_f32_16x16x32_bf16 v[44:47], a[252:255], a[156:159], v[44:47]\n"
"buffer_load_dwordx4 v255, s[24:27], s49 offen offset:3072 lds\n"
"v_mfma_f32_16x16x32_bf16 v[60:63], a[248:251], a[160:163], v[60:63]\n"
"ds_read_b64_tr_b16 a[220:221], v245 offset:9216\n"
"v_mfma_f32_16x16x32_bf16 v[60:63], a[252:255], a[164:167], v[60:63]\n"
"ds_read_b64_tr_b16 a[222:223], v245 offset:9728\n"
"v_mfma_f32_16x16x32_bf16 v[56:59], a[240:243], a[160:163], v[56:59]\n"
"ds_read_b128 a[144:147], v246 offset:2048\n"
"v_mfma_f32_16x16x32_bf16 v[56:59], a[244:247], a[164:167], v[56:59]\n"
"s_add_u32 s40, s49, 40960\n"
"ds_read_b128 a[148:151], v247 offset:2048\n"
"v_mfma_f32_16x16x32_bf16 v[52:55], a[232:235], a[160:163], v[52:55]\n"
"s_add_u32 m0, s51, 4096\n"
"ds_read_b128 a[152:155], v246 offset:4096\n"
"v_mfma_f32_16x16x32_bf16 v[52:55], a[236:239], a[164:167], v[52:55]\n"
"buffer_load_dwordx4 v248, s[24:27], s40 offen lds\n"
"v_mfma_f32_16x16x32_bf16 v[48:51], a[224:227], a[160:163], v[48:51]\n"
"ds_read_b128 a[156:159], v247 offset:4096\n"
"v_mfma_f32_16x16x32_bf16 v[48:51], a[228:231], a[164:167], v[48:51]\n"
"ds_read_b128 a[160:163], v252 offset:0\n"
"v_mfma_f32_16x16x32_bf16 v[64:67], a[224:227], a[168:171], v[64:67]\n"
"ds_read_b128 a[164:167], v253 offset:0\n"
"v_mfma_f32_16x16x32_bf16 v[64:67], a[228:231], a[172:175], v[64:67]\n"
"v_mfma_f32_16x16x32_bf16 v[68:71], a[232:235], a[168:171], v[68:71]\n"
"v_mfma_f32_16x16x32_bf16 v[68:71], a[236:239], a[172:175], v[68:71]\n"
"buffer_load_dwordx4 v249, s[24:27], s40 offen offset:1024 lds\n"
"v_mfma_f32_16x16x32_bf16 v[72:75], a[240:243], a[168:171], v[72:75]\n"
"v_mfma_f32_16x16x32_bf16 v[72:75], a[244:247], a[172:175], v[72:75]\n"
"v_mfma_f32_16x16x32_bf16 v[76:79], a[248:251], a[168:171], v[76:79]\n"
"ds_read_b128 a[168:171], v250 offset:0\n"
"v_mfma_f32_16x16x32_bf16 v[76:79], a[252:255], a[172:175], v[76:79]\n"
"ds_read_b128 a[172:175], v251 offset:0\n"
"v_mfma_f32_16x16x32_bf16 v[92:95], a[248:251], a[176:179], v[92:95]\n"
"v_mfma_f32_16x16x32_bf16 v[92:95], a[252:255], a[180:183], v[92:95]\n"
"buffer_load_dwordx4 v254, s[24:27], s40 offen offset:2048 lds\n"
"v_mfma_f32_16x16x32_bf16 v[88:91], a[240:243], a[176:179], v[88:91]\n"
"s_add_u32 s49, s49, 128\n"
"v_mfma_f32_16x16x32_bf16 v[88:91], a[244:247], a[180:183], v[88:91]\n"
"s_sub_u32 s41, s49, 1280\n"
"v_mfma_f32_16x16x32_bf16 v[84:87], a[232:235], a[176:179], v[84:87]\n"
"s_min_u32 s49, s49, s41\n"
"v_mfma_f32_16x16x32_bf16 v[84:87], a[236:239], a[180:183], v[84:87]\n"
"s_xor_b32 s51, s51, s74\n"
"v_mfma_f32_16x16x32_bf16 v[80:83], a[224:227], a[176:179], v[80:83]\n"
"ds_read_b128 a[176:179], v250 offset:2048\n"
"v_mfma_f32_16x16x32_bf16 v[80:83], a[228:231], a[180:183], v[80:83]\n"
"ds_read_b128 a[180:183], v251 offset:2048\n"
"v_mfma_f32_16x16x32_bf16 v[96:99], a[224:227], a[184:187], v[96:99]\n"
"v_mfma_f32_16x16x32_bf16 v[96:99], a[228:231], a[188:191], v[96:99]\n"
"v_mfma_f32_16x16x32_bf16 v[100:103], a[232:235], a[184:187], v[100:103]\n"
"v_mfma_f32_16x16x32_bf16 v[100:103], a[236:239], a[188:191], v[100:103]\n"
"v_mfma_f32_16x16x32_bf16 v[104:107], a[240:243], a[184:187], v[104:107]\n"
"v_mfma_f32_16x16x32_bf16 v[104:107], a[244:247], a[188:191], v[104:107]\n"
"v_mfma_f32_16x16x32_bf16 v[108:111], a[248:251], a[184:187], v[108:111]\n"
"s_add_u32 s47, s47, 1\n"
"ds_read_b128 a[184:187], v250 offset:4096\n"
"v_mfma_f32_16x16x32_bf16 v[108:111], a[252:255], a[188:191], v[108:111]\n"
"s_waitcnt lgkmcnt(1)\n"
"s_cmp_lg_u32 s47, s62\n"
"ds_read_b128 a[188:191], v251 offset:4096\n"
"s_cbranch_scc1 LPL_%=\n"
"s_cmp_eq_u32 s47, 18\n"
"s_cbranch_scc1 LPT2_%=\n"
"v_mfma_f32_16x16x32_bf16 v[112:115], a[192:195], a[136:139], v[112:115]\n"
"s_add_u32 s78, s96, 8192\n"
"v_mov_b32 v241, 1\n"
"v_mfma_f32_16x16x32_bf16 v[112:115], a[196:199], a[140:143], v[112:115]\n"
"s_mov_b64 exec, 1\n"
"buffer_store_dword v241, off, s[32:35], s96 sc0 sc1\n"
"buffer_store_dword v241, off, s[32:35], s78 sc0 sc1\n"
"s_mov_b64 exec, -1\n"
"v_mfma_f32_16x16x32_bf16 v[116:119], a[200:203], a[136:139], v[116:119]\n"
"s_waitcnt vmcnt(13)\n"
"v_mfma_f32_16x16x32_bf16 v[116:119], a[204:207], a[140:143], v[116:119]\n"
"s_barrier\n"
"v_mfma_f32_16x16x32_bf16 v[120:123], a[208:211], a[136:139], v[120:123]\n"
"s_add_u32 s40, s48, 256\n"
"s_xor_b32 m0, s50, 0xc000\n"
"ds_read_b64_tr_b16 a[242:243], v244 offset:17920\n"
"v_mfma_f32_16x16x32_bf16 v[120:123], a[212:215], a[140:143], v[120:123]\n"
"buffer_load_dwordx4 v224, s[28:31], s40 offen lds\n"
"v_mfma_f32_16x16x32_bf16 v[124:127], a[216:219], a[136:139], v[124:127]\n"
"v_mfma_f32_16x16x32_bf16 v[124:127], a[220:223], a[140:143], v[124:127]\n"
"v_mfma_f32_16x16x32_bf16 v[188:191], a[216:219], a[168:171], v[188:191]\n"
"ds_read_b64_tr_b16 a[230:231], v244 offset:25088\n"
"ds_read_b64_tr_b16 a[236:237], v245 offset:24576\n"
"v_mfma_f32_16x16x32_bf16 v[188:191], a[220:223], a[172:175], v[188:191]\n"
"ds_read_b64_tr_b16 a[226:227], v244 offset:16896\n"
"v_mfma_f32_16x16x32_bf16 v[184:187], a[208:211], a[168:171], v[184:187]\n"
"ds_read_b64_tr_b16 a[246:247], v244 offset:26112\n"
"ds_read_b64_tr_b16 a[248:249], v245 offset:17408\n"
"v_mfma_f32_16x16x32_bf16 v[184:187], a[212:215], a[172:175], v[184:187]\n"
"buffer_load_dwordx4 v225, s[28:31], s40 offen offset:1024 lds\n"
"v_mfma_f32_16x16x32_bf16 v[180:183], a[200:203], a[168:171], v[180:183]\n"
"v_mfma_f32_16x16x32_bf16 v[180:183], a[204:207], a[172:175], v[180:183]\n"
"v_mfma_f32_16x16x32_bf16 v[176:179], a[192:195], a[168:171], v[176:179]\n"
"ds_read_b64_tr_b16 a[224:225], v244 offset:16384\n"
"ds_read_b64_tr_b16 a[228:229], v244 offset:24576\n"
"v_mfma_f32_16x16x32_bf16 v[176:179], a[196:199], a[172:175], v[176:179]\n"
"ds_read_b64_tr_b16 a[244:245], v244 offset:25600\n"
"v_mfma_f32_16x16x32_bf16 v[192:195], a[192:195], a[176:179], v[192:195]\n"
"ds_read_b64_tr_b16 a[252:253], v245 offset:25600\n"
"ds_read_b64_tr_b16 a[254:255], v245 offset:26112\n"
"v_mfma_f32_16x16x32_bf16 v[192:195], a[196:199], a[180:183], v[192:195]\n"
"buffer_load_dwordx4 v226, s[28:31], s40 offen offset:2048 lds\n"
"v_mfma_f32_16x16x32_bf16 v[196:199], a[200:203], a[176:179], v[196:199]\n"
"v_mfma_f32_16x16x32_bf16 v[196:199], a[204:207], a[180:183], v[196:199]\n"
"v_mfma_f32_16x16x32_bf16 v[200:203], a[208:211], a[176:179], v[200:203]\n"
"ds_read_b64_tr_b16 a[234:235], v245 offset:16896\n"
"v_mfma_f32_16x16x32_bf16 v[200:203], a[212:215], a[180:183], v[200:203]\n"
"ds_read_b64_tr_b16 a[250:251], v245 offset:17920\n"
"v_mfma_f32_16x16x32_bf16 v[204:207], a[216:219], a[176:179], v[204:207]\n"
"ds_read_b64_tr_b16 a[232:233], v245 offset:16384\n"
"ds_read_b64_tr_b16 a[238:239], v245 offset:25088\n"
"v_mfma_f32_16x16x32_bf16 v[204:207], a[220:223], a[180:183], v[204:207]\n"
"s_waitcnt lgkmcnt(6)\n"
"buffer_load_dwordx4 v227, s[28:31], s40 offen offset:3072 lds\n"
"v_mfma_f32_16x16x32_bf16 v[220:223], a[216:219], a[184:187], v[220:223]\n"
"s_cmp_eq_u32 s47, 8\n"
"v_mfma_f32_16x16x32_bf16 v[220:223], a[220:223], a[188:191], v[220:223]\n"
"s_cselect_b32 s41, s52, s58\n"
"s_nop 1\n"
"v_pk_fma_f32 v[176:177], v[176:177], s[70:71], s[70:71] op_sel:[0,0,1] op_sel_hi:[1,0,1]\nv_pk_fma_f32 v[178:179], v[178:179], s[70:71], s[70:71] op_sel:[0,0,1] op_sel_hi:[1,0,1]\nv_pk_fma_f32 v[180:181], v[180:181], s[70:71], s[70:71] op_sel:[0,0,1] op_sel_hi:[1,0,1]\nv_pk_fma_f32 v[182:183], v[182:183], s[70:71], s[70:71] op_sel:[0,0,1] op_sel_hi:[1,0,1]\nv_pk_fma_f32 v[184:185], v[184:185], s[70:71], s[70:71] op_sel:[0,0,1] op_sel_hi:[1,0,1]\nv_pk_fma_f32 v[186:187], v[186:187], s[70:71], s[70:71] op_sel:[0,0,1] op_sel_hi:[1,0,1]\nv_pk_fma_f32 v[188:189], v[188:189], s[70:71], s[70:71] op_sel:[0,0,1] op_sel_hi:[1,0,1]\nv_pk_fma_f32 v[190:191], v[190:191], s[70:71], s[70:71] op_sel:[0,0,1] op_sel_hi:[1,0,1]\nv_pk_fma_f32 v[192:193], v[192:193], s[70:71], s[70:71] op_sel:[0,0,1] op_sel_hi:[1,0,1]\nv_pk_fma_f32 v[194:195], v[194:195], s[70:71], s[70:71] op_sel:[0,0,1] op_sel_hi:[1,0,1]\nv_pk_fma_f32 v[196:197], v[196:197], s[70:71], s[70:71] op_sel:[0,0,1] op_sel_hi:[1,0,1]\nv_pk_fma_f32 v[198:199], v[198:199], s[70:71], s[70:71] op_sel:[0,0,1] op_sel_hi:[1,0,1]\nv_pk_fma_f32 v[200:201], v[200:201], s[70:71], s[70:71] op_sel:[0,0,1] op_sel_hi:[1,0,1]\nv_pk_fma_f32 v[202:203], v[202:203], s[70:71], s[70:71] op_sel:[0,0,1] op_sel_hi:[1,0,1]\nv_pk_fma_f32 v[204:205], v[204:205], s[70:71], s[70:71] op_sel:[0,0,1] op_sel_hi:[1,0,1]\nv_pk_fma_f32 v[206:207], v[206:207], s[70:71], s[70:71] op_sel:[0,0,1] op_sel_hi:[1,0,1]\n"
"v_mfma_f32_16x16x32_bf16 v[216:219], a[208:211], a[184:187], v[216:219]\n"
"s_add_u32 s48, s48, s41\n"
"v_cvt_pk_bf16_f32 v176, v176, v177\n"
"v_cvt_pk_bf16_f32 v177, v178, v179\n"
"v_mfma_f32_16x16x32_bf16 v[216:219], a[212:215], a[188:191], v[216:219]\n"
"s_sub_u32 s41, s48, 5242880\n"
"v_cvt_pk_bf16_f32 v192, v192, v193\n"
"v_cvt_pk_bf16_f32 v193, v194, v195\n"
"v_mfma_f32_16x16x32_bf16 v[212:215], a[200:203], a[184:187], v[212:215]\n"
"s_min_u32 s48, s48, s41\n"
"v_cvt_pk_bf16_f32 v179, v182, v183\n"
"v_and_b32 v182, 0xf0, v242\n"
"v_mfma_f32_16x16x32_bf16 v[212:215], a[204:207], a[188:191], v[212:215]\n"
"v_cvt_pk_bf16_f32 v178, v180, v181\n"
"v_xor_b32 v251, s74, v251\n"
"v_mfma_f32_16x16x32_bf16 v[208:211], a[192:195], a[184:187], v[208:211]\n"
"s_mov_b32 m0, s50\n"
"ds_read_b64_tr_b16 a[240:241], v244 offset:17408\n"
"v_cvt_pk_bf16_f32 v195, v198, v199\n"
"v_mfma_f32_16x16x32_bf16 v[208:211], a[196:199], a[188:191], v[208:211]\n"
"buffer_load_dwordx4 v224, s[28:31], s48 offen lds\n"
"v_mfma_f32_16x16x32_bf16 v[160:163], a[192:195], a[160:163], v[160:163]\n"
"v_bfe_u32 v180, v242, 9, 1\n"
"v_lshl_add_u32 v180, v180, 8, v182\n"
"v_mfma_f32_16x16x32_bf16 v[160:163], a[196:199], a[164:167], v[160:163]\n"
"s_mov_b64 s[80:81], s[8:9]\n"
"v_bfe_u32 v182, v242, 8, 1\n"
"v_cvt_pk_bf16_f32 v194, v196, v197\n"
"v_mfma_f32_16x16x32_bf16 v[164:167], a[200:203], a[160:163], v[164:167]\n"
"s_mov_b32 s82, s68\n"
"v_cvt_pk_bf16_f32 v184, v184, v185\n"
"v_bfe_u32 v181, v242, 9, 1\n"
"v_mfma_f32_16x16x32_bf16 v[164:167], a[204:207], a[164:167], v[164:167]\n"
"s_mov_b32 s83, 131072\n"
"buffer_load_dwordx4 v225, s[28:31], s48 offen offset:1024 lds\n"
"v_mfma_f32_16x16x32_bf16 v[168:171], a[208:211], a[160:163], v[168:171]\n"
"v_and_b32 v241, 0x3f0, v242\n"
"v_lshl_add_u32 v180, v182, 3, v180\n"
"v_xor_b32 v245, s74, v245\n"
"v_mfma_f32_16x16x32_bf16 v[168:171], a[212:215], a[164:167], v[168:171]\n"
"v_mul_u32_u24 v241, 10, v241\n"
"v_xor_b32 v244, s74, v244\n"
"v_xor_b32 v253, s74, v253\n"
"v_mfma_f32_16x16x32_bf16 v[172:175], a[216:219], a[160:163], v[172:175]\n"
"v_xor_b32_e64 v182, s42, 1\n"
"v_lshl_add_u32 v180, v182, 10, v180\n"
"v_mfma_f32_16x16x32_bf16 v[172:175], a[220:223], a[164:167], v[172:175]\n"
"v_cvt_pk_bf16_f32 v185, v186, v187\n"
"v_xor_b32 v252, s74, v252\n"
"v_mfma_f32_16x16x32_bf16 v[140:143], a[216:219], a[144:147], v[140:143]\n"
"v_add_u32 v180, 94208, v180\n"
"v_cvt_pk_bf16_f32 v200, v200, v201\n"
"v_mfma_f32_16x16x32_bf16 v[140:143], a[220:223], a[148:151], v[140:143]\n"
"buffer_load_dwordx4 v226, s[28:31], s48 offen offset:2048 lds\n"
"v_mfma_f32_16x16x32_bf16 v[136:139], a[208:211], a[144:147], v[136:139]\n"
"v_cvt_pk_bf16_f32 v187, v190, v191\n"
"v_and_b32 v182, 0xf0, v242\n"
"v_mfma_f32_16x16x32_bf16 v[136:139], a[212:215], a[148:151], v[136:139]\n"
"v_pk_fma_f32 v[208:209], v[208:209], s[70:71], s[70:71] op_sel:[0,0,1] op_sel_hi:[1,0,1]\nv_pk_fma_f32 v[210:211], v[210:211], s[70:71], s[70:71] op_sel:[0,0,1] op_sel_hi:[1,0,1]\nv_pk_fma_f32 v[212:213], v[212:213], s[70:71], s[70:71] op_sel:[0,0,1] op_sel_hi:[1,0,1]\nv_pk_fma_f32 v[214:215], v[214:215], s[70:71], s[70:71] op_sel:[0,0,1] op_sel_hi:[1,0,1]\nv_pk_fma_f32 v[216:217], v[216:217], s[70:71], s[70:71] op_sel:[0,0,1] op_sel_hi:[1,0,1]\nv_pk_fma_f32 v[218:219], v[218:219], s[70:71], s[70:71] op_sel:[0,0,1] op_sel_hi:[1,0,1]\nv_pk_fma_f32 v[220:221], v[220:221], s[70:71], s[70:71] op_sel:[0,0,1] op_sel_hi:[1,0,1]\nv_pk_fma_f32 v[222:223], v[222:223], s[70:71], s[70:71] op_sel:[0,0,1] op_sel_hi:[1,0,1]\nv_pk_fma_f32 v[160:161], v[160:161], s[70:71], s[70:71] op_sel:[0,0,1] op_sel_hi:[1,0,1]\nv_pk_fma_f32 v[162:163], v[162:163], s[70:71], s[70:71] op_sel:[0,0,1] op_sel_hi:[1,0,1]\nv_pk_fma_f32 v[164:165], v[164:165], s[70:71], s[70:71] op_sel:[0,0,1] op_sel_hi:[1,0,1]\nv_pk_fma_f32 v[166:167], v[166:167], s[70:71], s[70:71] op_sel:[0,0,1] op_sel_hi:[1,0,1]\nv_pk_fma_f32 v[168:169], v[168:169], s[70:71], s[70:71] op_sel:[0,0,1] op_sel_hi:[1,0,1]\nv_pk_fma_f32 v[170:171], v[170:171], s[70:71], s[70:71] op_sel:[0,0,1] op_sel_hi:[1,0,1]\nv_pk_fma_f32 v[172:173], v[172:173], s[70:71], s[70:71] op_sel:[0,0,1] op_sel_hi:[1,0,1]\nv_pk_fma_f32 v[174:175], v[174:175], s[70:71], s[70:71] op_sel:[0,0,1] op_sel_hi:[1,0,1]\n"
"v_mfma_f32_16x16x32_bf16 v[132:135], a[200:203], a[144:147], v[132:135]\n"
"v_cvt_pk_bf16_f32 v216, v216, v217\n"
"v_lshl_add_u32 v181, v181, 8, v182\n"
"v_mfma_f32_16x16x32_bf16 v[132:135], a[204:207], a[148:151], v[132:135]\n"
"ds_write2st64_b64 v180, v[192:193], v[194:195] offset0:88 offset1:89\n"
"v_cvt_pk_bf16_f32 v217, v218, v219\n"
"v_mfma_f32_16x16x32_bf16 v[128:131], a[192:195], a[144:147], v[128:131]\n"
"v_cvt_pk_bf16_f32 v218, v220, v221\n"
"v_bfe_u32 v182, v242, 8, 1\n"
"v_mfma_f32_16x16x32_bf16 v[128:131], a[196:199], a[148:151], v[128:131]\n"
"buffer_load_dwordx4 v227, s[28:31], s48 offen offset:3072 lds\n"
"v_mfma_f32_16x16x32_bf16 v[144:147], a[192:195], a[152:155], v[144:147]\n"
"s_xor_b32 s50, s50, s74\n"
"v_cvt_pk_bf16_f32 v201, v202, v203\n"
"v_cvt_pk_bf16_f32 v202, v204, v205\n"
"v_mfma_f32_16x16x32_bf16 v[144:147], a[196:199], a[156:159], v[144:147]\n"
"v_xor_b32 v246, s74, v246\n"
"v_lshl_add_u32 v181, v182, 9, v181\n"
"v_mfma_f32_16x16x32_bf16 v[148:151], a[200:203], a[152:155], v[148:151]\n"
"v_cvt_pk_bf16_f32 v219, v222, v223\n"
"v_cvt_pk_bf16_f32 v168, v168, v169\n"
"v_mfma_f32_16x16x32_bf16 v[148:151], a[204:207], a[156:159], v[148:151]\n"
"ds_write2st64_b64 v180, v[216:217], v[218:219] offset0:128 offset1:129\n"
"v_pk_fma_f32 v[112:113], v[112:113], s[70:71], s[70:71] op_sel:[0,0,1] op_sel_hi:[1,0,1]\nv_pk_fma_f32 v[114:115], v[114:115], s[70:71], s[70:71] op_sel:[0,0,1] op_sel_hi:[1,0,1]\nv_pk_fma_f32 v[116:117], v[116:117], s[70:71], s[70:71] op_sel:[0,0,1] op_sel_hi:[1,0,1]\nv_pk_fma_f32 v[118:119], v[118:119], s[70:71], s[70:71] op_sel:[0,0,1] op_sel_hi:[1,0,1]\nv_pk_fma_f32 v[120:121], v[120:121], s[70:71], s[70:71] op_sel:[0,0,1] op_sel_hi:[1,0,1]\nv_pk_fma_f32 v[122:123], v[122:123], s[70:71], s[70:71] op_sel:[0,0,1] op_sel_hi:[1,0,1]\nv_pk_fma_f32 v[124:125], v[124:125], s[70:71], s[70:71] op_sel:[0,0,1] op_sel_hi:[1,0,1]\nv_pk_fma_f32 v[126:127], v[126:127], s[70:71], s[70:71] op_sel:[0,0,1] op_sel_hi:[1,0,1]\nv_pk_fma_f32 v[128:129], v[128:129], s[70:71], s[70:71] op_sel:[0,0,1] op_sel_hi:[1,0,1]\nv_pk_fma_f32 v[130:131], v[130:131], s[70:71], s[70:71] op_sel:[0,0,1] op_sel_hi:[1,0,1]\nv_pk_fma_f32 v[132:133], v[132:133], s[70:71], s[70:71] op_sel:[0,0,1] op_sel_hi:[1,0,1]\nv_pk_fma_f32 v[134:135], v[134:135], s[70:71], s[70:71] op_sel:[0,0,1] op_sel_hi:[1,0,1]\nv_pk_fma_f32 v[136:137], v[136:137], s[70:71], s[70:71] op_sel:[0,0,1] op_sel_hi:[1,0,1]\nv_pk_fma_f32 v[138:139], v[138:139], s[70:71], s[70:71] op_sel:[0,0,1] op_sel_hi:[1,0,1]\nv_pk_fma_f32 v[140:141], v[140:141], s[70:71], s[70:71] op_sel:[0,0,1] op_sel_hi:[1,0,1]\nv_pk_fma_f32 v[142:143], v[142:143], s[70:71], s[70:71] op_sel:[0,0,1] op_sel_hi:[1,0,1]\n"
"v_cvt_pk_bf16_f32 v120, v120, v121\n"
"v_mfma_f32_16x16x32_bf16 v[152:155], a[208:211], a[152:155], v[152:155]\n"
"v_cvt_pk_bf16_f32 v203, v206, v207\n"
"v_cvt_pk_bf16_f32 v121, v122, v123\n"
"v_mfma_f32_16x16x32_bf16 v[152:155], a[212:215], a[156:159], v[152:155]\n"
"v_cvt_pk_bf16_f32 v123, v126, v127\n"
"v_xor_b32_e64 v182, s42, 0\n"
"v_mfma_f32_16x16x32_bf16 v[156:159], a[216:219], a[152:155], v[156:159]\n"
"v_cvt_pk_bf16_f32 v186, v188, v189\n"
"v_cvt_pk_bf16_f32 v112, v112, v113\n"
"v_mfma_f32_16x16x32_bf16 v[156:159], a[220:223], a[156:159], v[156:159]\n"
"v_cvt_pk_bf16_f32 v128, v128, v129\n"
"v_cvt_pk_bf16_f32 v129, v130, v131\n"
"v_mfma_f32_16x16x32_bf16 v[96:99], a[224:227], a[184:187], v[96:99]\n"
"v_cvt_pk_bf16_f32 v208, v208, v209\n"
"v_cvt_pk_bf16_f32 v160, v160, v161\n"
"v_mfma_f32_16x16x32_bf16 v[96:99], a[228:231], a[188:191], v[96:99]\n"
"s_waitcnt lgkmcnt(1)\n"
"ds_write2st64_b64 v180, v[200:201], v[202:203] offset0:96 offset1:97\n"
"v_cvt_pk_bf16_f32 v209, v210, v211\n"
"v_cvt_pk_bf16_f32 v210, v212, v213\n"
"v_cvt_pk_bf16_f32 v211, v214, v215\n"
"v_cvt_pk_bf16_f32 v169, v170, v171\n"
"v_lshl_add_u32 v181, v182, 10, v181\n"
"v_mfma_f32_16x16x32_bf16 v[100:103], a[232:235], a[184:187], v[100:103]\n"
"s_waitcnt vmcnt(10)\n"
"v_permlane16_swap_b32 v121, v123\n"
"v_mfma_f32_16x16x32_bf16 v[100:103], a[236:239], a[188:191], v[100:103]\n"
"s_barrier\n"
"ds_write2st64_b64 v180, v[176:177], v[178:179] offset0:0 offset1:1\n"
"v_pk_fma_f32 v[144:145], v[144:145], s[70:71], s[70:71] op_sel:[0,0,1] op_sel_hi:[1,0,1]\nv_pk_fma_f32 v[146:147], v[146:147], s[70:71], s[70:71] op_sel:[0,0,1] op_sel_hi:[1,0,1]\nv_pk_fma_f32 v[148:149], v[148:149], s[70:71], s[70:71] op_sel:[0,0,1] op_sel_hi:[1,0,1]\nv_pk_fma_f32 v[150:151], v[150:151], s[70:71], s[70:71] op_sel:[0,0,1] op_sel_hi:[1,0,1]\nv_pk_fma_f32 v[152:153], v[152:153], s[70:71], s[70:71] op_sel:[0,0,1] op_sel_hi:[1,0,1]\nv_pk_fma_f32 v[154:155], v[154:155], s[70:71], s[70:71] op_sel:[0,0,1] op_sel_hi:[1,0,1]\nv_pk_fma_f32 v[156:157], v[156:157], s[70:71], s[70:71] op_sel:[0,0,1] op_sel_hi:[1,0,1]\nv_pk_fma_f32 v[158:159], v[158:159], s[70:71], s[70:71] op_sel:[0,0,1] op_sel_hi:[1,0,1]\n"
"v_mfma_f32_16x16x32_bf16 v[104:107], a[240:243], a[184:187], v[104:107]\n"
"s_mov_b32 m0, s51\n"
"v_accvgpr_write_b32 a11, v123\n"
"v_cvt_pk_bf16_f32 v130, v132, v133\n"
"v_mfma_f32_16x16x32_bf16 v[104:107], a[244:247], a[188:191], v[104:107]\n"
"buffer_load_dword v241, s[80:83], s63 offen lds\n"
"v_mfma_f32_16x16x32_bf16 v[108:111], a[248:251], a[184:187], v[108:111]\n"
"v_cvt_pk_bf16_f32 v144, v144, v145\n"
"v_cvt_pk_bf16_f32 v152, v152, v153\n"
"v_mfma_f32_16x16x32_bf16 v[108:111], a[252:255], a[188:191], v[108:111]\n"
"v_cvt_pk_bf16_f32 v131, v134, v135\n"
"v_cvt_pk_bf16_f32 v153, v154, v155\n"
"v_mfma_f32_16x16x32_bf16 v[92:95], a[248:251], a[176:179], v[92:95]\n"
"ds_read_b64_tr_b16 a[214:215], v244 offset:9728\n"
"v_cvt_pk_bf16_f32 v161, v162, v163\n"
"v_mfma_f32_16x16x32_bf16 v[92:95], a[252:255], a[180:183], v[92:95]\n"
"ds_read_b64_tr_b16 a[218:219], v245 offset:1536\n"
"v_cvt_pk_bf16_f32 v145, v146, v147\n"
"v_mfma_f32_16x16x32_bf16 v[88:91], a[240:243], a[176:179], v[88:91]\n"
"ds_read_b64_tr_b16 a[208:209], v244 offset:1024\n"
"v_cvt_pk_bf16_f32 v146, v148, v149\n"
"v_mfma_f32_16x16x32_bf16 v[88:91], a[244:247], a[180:183], v[88:91]\n"
"buffer_load_dword v241, s[80:83], s63 offen offset:80 lds\n"
"v_mfma_f32_16x16x32_bf16 v[84:87], a[232:235], a[176:179], v[84:87]\n"
"v_cvt_pk_bf16_f32 v136, v136, v137\n"
"v_cvt_pk_bf16_f32 v137, v138, v139\n"
"v_mfma_f32_16x16x32_bf16 v[84:87], a[236:239], a[180:183], v[84:87]\n"
"v_cvt_pk_bf16_f32 v113, v114, v115\n"
"v_cvt_pk_bf16_f32 v114, v116, v117\n"
"v_mfma_f32_16x16x32_bf16 v[80:83], a[224:227], a[176:179], v[80:83]\n"
"s_mov_b32 m0, s51\n"
"v_cvt_pk_bf16_f32 v138, v140, v141\n"
"v_cvt_pk_bf16_f32 v139, v142, v143\n"
"v_mfma_f32_16x16x32_bf16 v[80:83], a[228:231], a[180:183], v[80:83]\n"
"buffer_load_dwordx4 v248, s[24:27], s49 offen lds\n"
"v_mfma_f32_16x16x32_bf16 v[64:67], a[224:227], a[168:171], v[64:67]\n"
"v_cvt_pk_bf16_f32 v115, v118, v119\n"
"v_cvt_pk_bf16_f32 v154, v156, v157\n"
"v_mfma_f32_16x16x32_bf16 v[64:67], a[228:231], a[172:175], v[64:67]\n"
"ds_write2st64_b64 v180, v[184:185], v[186:187] offset0:64 offset1:65\n"
"v_accvgpr_write_b32 a9, v121\n"
"v_permlane16_swap_b32 v144, v146\n"
"v_permlane16_swap_b32 v152, v154\n"
"v_add_u32 v181, 94208, v181\n"
"v_mfma_f32_16x16x32_bf16 v[68:71], a[232:235], a[168:171], v[68:71]\n"
"v_permlane16_swap_b32 v136, v138\n"
"v_mfma_f32_16x16x32_bf16 v[68:71], a[236:239], a[172:175], v[68:71]\n"
"ds_write2st64_b64 v180, v[208:209], v[210:211] offset0:120 offset1:121\n"
"v_cvt_pk_bf16_f32 v170, v172, v173\n"
"v_cvt_pk_bf16_f32 v122, v124, v125\n"
"v_permlane16_swap_b32 v128, v130\n"
"v_cvt_pk_bf16_f32 v147, v150, v151\n"
"v_cvt_pk_bf16_f32 v155, v158, v159\n"
"v_accvgpr_write_b32 a40, v152\n"
"v_xor_b32 v247, s74, v247\n"
"v_pk_fma_f32 v[96:97], v[96:97], s[70:71], s[70:71] op_sel:[0,0,1] op_sel_hi:[1,0,1]\nv_pk_fma_f32 v[98:99], v[98:99], s[70:71], s[70:71] op_sel:[0,0,1] op_sel_hi:[1,0,1]\nv_pk_fma_f32 v[100:101], v[100:101], s[70:71], s[70:71] op_sel:[0,0,1] op_sel_hi:[1,0,1]\nv_pk_fma_f32 v[102:103], v[102:103], s[70:71], s[70:71] op_sel:[0,0,1] op_sel_hi:[1,0,1]\nv_pk_fma_f32 v[104:105], v[104:105], s[70:71], s[70:71] op_sel:[0,0,1] op_sel_hi:[1,0,1]\nv_pk_fma_f32 v[106:107], v[106:107], s[70:71], s[70:71] op_sel:[0,0,1] op_sel_hi:[1,0,1]\nv_pk_fma_f32 v[108:109], v[108:109], s[70:71], s[70:71] op_sel:[0,0,1] op_sel_hi:[1,0,1]\nv_pk_fma_f32 v[110:111], v[110:111], s[70:71], s[70:71] op_sel:[0,0,1] op_sel_hi:[1,0,1]\nv_pk_fma_f32 v[80:81], v[80:81], s[70:71], s[70:71] op_sel:[0,0,1] op_sel_hi:[1,0,1]\nv_pk_fma_f32 v[82:83], v[82:83], s[70:71], s[70:71] op_sel:[0,0,1] op_sel_hi:[1,0,1]\nv_pk_fma_f32 v[84:85], v[84:85], s[70:71], s[70:71] op_sel:[0,0,1] op_sel_hi:[1,0,1]\nv_pk_fma_f32 v[86:87], v[86:87], s[70:71], s[70:71] op_sel:[0,0,1] op_sel_hi:[1,0,1]\nv_pk_fma_f32 v[88:89], v[88:89], s[70:71], s[70:71] op_sel:[0,0,1] op_sel_hi:[1,0,1]\nv_pk_fma_f32 v[90:91], v[90:91], s[70:71], s[70:71] op_sel:[0,0,1] op_sel_hi:[1,0,1]\nv_pk_fma_f32 v[92:93], v[92:93], s[70:71], s[70:71] op_sel:[0,0,1] op_sel_hi:[1,0,1]\nv_pk_fma_f32 v[94:95], v[94:95], s[70:71], s[70:71] op_sel:[0,0,1] op_sel_hi:[1,0,1]\n"
"v_mfma_f32_16x16x32_bf16 v[72:75], a[240:243], a[168:171], v[72:75]\n"
"ds_read_b64_tr_b16 a[192:193], v244 offset:0\n"
"ds_read_b128 a[188:191], v251 offset:4096\n"
"v_mfma_f32_16x16x32_bf16 v[72:75], a[244:247], a[172:175], v[72:75]\n"
"buffer_load_dwordx4 v249, s[24:27], s49 offen offset:1024 lds\n"
"v_mfma_f32_16x16x32_bf16 v[76:79], a[248:251], a[168:171], v[76:79]\n"
"v_cvt_pk_bf16_f32 v171, v174, v175\n"
"v_cvt_pk_bf16_f32 v96, v96, v97\n"
"v_mfma_f32_16x16x32_bf16 v[76:79], a[252:255], a[172:175], v[76:79]\n"
"v_permlane16_swap_b32 v137, v139\n"
"v_mfma_f32_16x16x32_bf16 v[60:63], a[248:251], a[160:163], v[60:63]\n"
"v_permlane16_swap_b32 v145, v147\n"
"v_mfma_f32_16x16x32_bf16 v[60:63], a[252:255], a[164:167], v[60:63]\n"
"buffer_load_dwordx4 v254, s[24:27], s49 offen offset:2048 lds\n"
"v_mfma_f32_16x16x32_bf16 v[56:59], a[240:243], a[160:163], v[56:59]\n"
"v_permlane16_swap_b32 v113, v115\n"
"v_mfma_f32_16x16x32_bf16 v[56:59], a[244:247], a[164:167], v[56:59]\n"
"v_accvgpr_write_b32 a34, v146\n"
"v_accvgpr_write_b32 a42, v154\n"
"v_mfma_f32_16x16x32_bf16 v[52:55], a[232:235], a[160:163], v[52:55]\n"
"ds_read_b64_tr_b16 a[202:203], v245 offset:512\n"
"ds_read_b64_tr_b16 a[210:211], v244 offset:1536\n"
"v_mfma_f32_16x16x32_bf16 v[52:55], a[236:239], a[164:167], v[52:55]\n"
"ds_read_b64_tr_b16 a[198:199], v244 offset:8704\n"
"v_cvt_pk_bf16_f32 v80, v80, v81\n"
"v_mfma_f32_16x16x32_bf16 v[48:51], a[224:227], a[160:163], v[48:51]\n"
"s_waitcnt vmcnt(63) lgkmcnt(3)\n"
"ds_read_b64_tr_b16 a[194:195], v244 offset:512\n"
"ds_read_b64_tr_b16 a[200:201], v245 offset:0\n"
"v_mfma_f32_16x16x32_bf16 v[48:51], a[228:231], a[164:167], v[48:51]\n"
"s_barrier\n"
"ds_read_b64_tr_b16 a[222:223], v245 offset:9728\n"
"v_permlane16_swap_b32 v169, v171\n"
"v_permlane16_swap_b32 v112, v114\n"
"v_accvgpr_write_b32 a1, v113\n"
"v_permlane16_swap_b32 v120, v122\n"
"v_permlane16_swap_b32 v129, v131\n"
"v_accvgpr_write_b32 a17, v129\n"
"v_mfma_f32_16x16x32_bf16 v[0:3], a[224:227], a[136:139], v[0:3]\n"
"ds_read_b64_tr_b16 a[216:217], v245 offset:1024\n"
"ds_read_b64_tr_b16 a[220:221], v245 offset:9216\n"
"v_mfma_f32_16x16x32_bf16 v[0:3], a[228:231], a[140:143], v[0:3]\n"
"buffer_load_dwordx4 v255, s[24:27], s49 offen offset:3072 lds\n"
"v_mfma_f32_16x16x32_bf16 v[4:7], a[232:235], a[136:139], v[4:7]\n"
"s_add_u32 s40, s49, 40960\n"
"s_add_u32 m0, s51, 4096\n"
"v_accvgpr_write_b32 a26, v138\n"
"v_mfma_f32_16x16x32_bf16 v[4:7], a[236:239], a[140:143], v[4:7]\n"
"buffer_load_dwordx4 v248, s[24:27], s40 offen lds\n"
"v_permlane16_swap_b32 v168, v170\n"
"v_accvgpr_write_b32 a0, v112\n"
"v_accvgpr_write_b32 a33, v145\n"
"v_pk_fma_f32 v[64:65], v[64:65], s[70:71], s[70:71] op_sel:[0,0,1] op_sel_hi:[1,0,1]\nv_pk_fma_f32 v[66:67], v[66:67], s[70:71], s[70:71] op_sel:[0,0,1] op_sel_hi:[1,0,1]\nv_pk_fma_f32 v[68:69], v[68:69], s[70:71], s[70:71] op_sel:[0,0,1] op_sel_hi:[1,0,1]\nv_pk_fma_f32 v[70:71], v[70:71], s[70:71], s[70:71] op_sel:[0,0,1] op_sel_hi:[1,0,1]\nv_pk_fma_f32 v[72:73], v[72:73], s[70:71], s[70:71] op_sel:[0,0,1] op_sel_hi:[1,0,1]\nv_pk_fma_f32 v[74:75], v[74:75], s[70:71], s[70:71] op_sel:[0,0,1] op_sel_hi:[1,0,1]\nv_pk_fma_f32 v[76:77], v[76:77], s[70:71], s[70:71] op_sel:[0,0,1] op_sel_hi:[1,0,1]\nv_pk_fma_f32 v[78:79], v[78:79], s[70:71], s[70:71] op_sel:[0,0,1] op_sel_hi:[1,0,1]\nv_pk_fma_f32 v[48:49], v[48:49], s[70:71], s[70:71] op_sel:[0,0,1] op_sel_hi:[1,0,1]\nv_pk_fma_f32 v[50:51], v[50:51], s[70:71], s[70:71] op_sel:[0,0,1] op_sel_hi:[1,0,1]\nv_pk_fma_f32 v[52:53], v[52:53], s[70:71], s[70:71] op_sel:[0,0,1] op_sel_hi:[1,0,1]\nv_pk_fma_f32 v[54:55], v[54:55], s[70:71], s[70:71] op_sel:[0,0,1] op_sel_hi:[1,0,1]\nv_pk_fma_f32 v[56:57], v[56:57], s[70:71], s[70:71] op_sel:[0,0,1] op_sel_hi:[1,0,1]\nv_pk_fma_f32 v[58:59], v[58:59], s[70:71], s[70:71] op_sel:[0,0,1] op_sel_hi:[1,0,1]\nv_pk_fma_f32 v[60:61], v[60:61], s[70:71], s[70:71] op_sel:[0,0,1] op_sel_hi:[1,0,1]\nv_pk_fma_f32 v[62:63], v[62:63], s[70:71], s[70:71] op_sel:[0,0,1] op_sel_hi:[1,0,1]\n"
"v_mfma_f32_16x16x32_bf16 v[8:11], a[240:243], a[136:139], v[8:11]\n"
"v_permlane16_swap_b32 v153, v155\n"
"v_mfma_f32_16x16x32_bf16 v[8:11], a[244:247], a[140:143], v[8:11]\n"
"ds_read_b128 a[36:39], v181 offset:61440\n"
"v_cvt_pk_bf16_f32 v104, v104, v105\n"
"v_mfma_f32_16x16x32_bf16 v[12:15], a[248:251], a[136:139], v[12:15]\n"
"ds_read_b128 a[4:7], v181 offset:0\n"
"v_accvgpr_write_b32 a32, v144\n"
"v_mfma_f32_16x16x32_bf16 v[12:15], a[252:255], a[140:143], v[12:15]\n"
"ds_read_b64_tr_b16 a[206:207], v245 offset:8704\n"
"v_accvgpr_write_b32 a19, v131\n"
"v_mfma_f32_16x16x32_bf16 v[28:31], a[248:251], a[144:147], v[28:31]\n"
"ds_read_b64_tr_b16 a[204:205], v245 offset:8192\n"
"ds_read_b128 a[172:175], v251 offset:0\n"
"v_mfma_f32_16x16x32_bf16 v[28:31], a[252:255], a[148:151], v[28:31]\n"
"buffer_load_dwordx4 v249, s[24:27], s40 offen offset:1024 lds\n"
"v_mfma_f32_16x16x32_bf16 v[24:27], a[240:243], a[144:147], v[24:27]\n"
"v_accvgpr_write_b32 a16, v128\n"
"v_cvt_pk_bf16_f32 v81, v82, v83\n"
"v_mfma_f32_16x16x32_bf16 v[24:27], a[244:247], a[148:151], v[24:27]\n"
"ds_read_b128 a[136:139], v246 offset:0\n"
"v_accvgpr_write_b32 a24, v136\n"
"v_mfma_f32_16x16x32_bf16 v[20:23], a[232:235], a[144:147], v[20:23]\n"
"ds_read_b64_tr_b16 a[212:213], v244 offset:9216\n"
"ds_read_b128 a[28:31], v181 offset:49152\n"
"v_mfma_f32_16x16x32_bf16 v[20:23], a[236:239], a[148:151], v[20:23]\n"
"buffer_load_dwordx4 v254, s[24:27], s40 offen offset:2048 lds\n"
"v_mfma_f32_16x16x32_bf16 v[16:19], a[224:227], a[144:147], v[16:19]\n"
"s_add_u32 s49, s49, 128\n"
"v_accvgpr_write_b32 a2, v114\n"
"v_add_u32 v182, s75, v181\n"
"v_mfma_f32_16x16x32_bf16 v[16:19], a[228:231], a[148:151], v[16:19]\n"
"s_sub_u32 s41, s49, 1280\n"
"v_cvt_pk_bf16_f32 v163, v166, v167\n"
"v_cvt_pk_bf16_f32 v97, v98, v99\n"
"v_mfma_f32_16x16x32_bf16 v[32:35], a[224:227], a[152:155], v[32:35]\n"
"s_min_u32 s49, s49, s41\n"
"ds_read_b128 a[140:143], v247 offset:0\n"
"v_accvgpr_write_b32 a3, v115\n"
"v_mfma_f32_16x16x32_bf16 v[32:35], a[228:231], a[156:159], v[32:35]\n"
"s_xor_b32 s51, s51, s74\n"
"ds_read_b128 a[44:47], v182 offset:0\n"
"v_cvt_pk_bf16_f32 v105, v106, v107\n"
"v_mfma_f32_16x16x32_bf16 v[36:39], a[232:235], a[152:155], v[36:39]\n"
"ds_read_b128 a[12:15], v181 offset:32768\n"
"v_cvt_pk_bf16_f32 v72, v72, v73\n"
"v_mfma_f32_16x16x32_bf16 v[36:39], a[236:239], a[156:159], v[36:39]\n"
"ds_read_b64_tr_b16 a[196:197], v244 offset:8192\n"
"ds_read_b128 a[20:23], v181 offset:45056\n"
"v_accvgpr_write_b32 a10, v122\n"
"v_accvgpr_write_b32 a18, v130\n"
"v_xor_b32 v250, s74, v250\n"
"v_cvt_pk_bf16_f32 v88, v88, v89\n"
"v_pk_fma_f32 v[0:1], v[0:1], s[70:71], s[70:71] op_sel:[0,0,1] op_sel_hi:[1,0,1]\nv_pk_fma_f32 v[2:3], v[2:3], s[70:71], s[70:71] op_sel:[0,0,1] op_sel_hi:[1,0,1]\nv_pk_fma_f32 v[4:5], v[4:5], s[70:71], s[70:71] op_sel:[0,0,1] op_sel_hi:[1,0,1]\nv_pk_fma_f32 v[6:7], v[6:7], s[70:71], s[70:71] op_sel:[0,0,1] op_sel_hi:[1,0,1]\nv_pk_fma_f32 v[8:9], v[8:9], s[70:71], s[70:71] op_sel:[0,0,1] op_sel_hi:[1,0,1]\nv_pk_fma_f32 v[10:11], v[10:11], s[70:71], s[70:71] op_sel:[0,0,1] op_sel_hi:[1,0,1]\nv_pk_fma_f32 v[12:13], v[12:13], s[70:71], s[70:71] op_sel:[0,0,1] op_sel_hi:[1,0,1]\nv_pk_fma_f32 v[14:15], v[14:15], s[70:71], s[70:71] op_sel:[0,0,1] op_sel_hi:[1,0,1]\nv_pk_fma_f32 v[16:17], v[16:17], s[70:71], s[70:71] op_sel:[0,0,1] op_sel_hi:[1,0,1]\nv_pk_fma_f32 v[18:19], v[18:19], s[70:71], s[70:71] op_sel:[0,0,1] op_sel_hi:[1,0,1]\nv_pk_fma_f32 v[20:21], v[20:21], s[70:71], s[70:71] op_sel:[0,0,1] op_sel_hi:[1,0,1]\nv_pk_fma_f32 v[22:23], v[22:23], s[70:71], s[70:71] op_sel:[0,0,1] op_sel_hi:[1,0,1]\nv_pk_fma_f32 v[24:25], v[24:25], s[70:71], s[70:71] op_sel:[0,0,1] op_sel_hi:[1,0,1]\nv_pk_fma_f32 v[26:27], v[26:27], s[70:71], s[70:71] op_sel:[0,0,1] op_sel_hi:[1,0,1]\nv_pk_fma_f32 v[28:29], v[28:29], s[70:71], s[70:71] op_sel:[0,0,1] op_sel_hi:[1,0,1]\nv_pk_fma_f32 v[30:31], v[30:31], s[70:71], s[70:71] op_sel:[0,0,1] op_sel_hi:[1,0,1]\n"
"v_cvt_pk_bf16_f32 v0, v0, v1\n"
"v_mfma_f32_16x16x32_bf16 v[40:43], a[240:243], a[152:155], v[40:43]\n"
"ds_read_b128 a[160:163], v252 offset:0\n"
"v_cvt_pk_bf16_f32 v16, v16, v17\n"
"v_mfma_f32_16x16x32_bf16 v[40:43], a[244:247], a[156:159], v[40:43]\n"
"v_accvgpr_write_b32 a35, v147\n"
"v_cvt_pk_bf16_f32 v82, v84, v85\n"
"v_mfma_f32_16x16x32_bf16 v[44:47], a[248:251], a[152:155], v[44:47]\n"
"s_add_u32 s47, s47, 1\n"
"ds_read_b128 a[168:171], v250 offset:0\n"
"v_cvt_pk_bf16_f32 v1, v2, v3\n"
"v_mfma_f32_16x16x32_bf16 v[44:47], a[252:255], a[156:159], v[44:47]\n"
"s_waitcnt lgkmcnt(7)\n"
"ds_read_b128 a[144:147], v246 offset:2048\n"
"v_cvt_pk_bf16_f32 v48, v48, v49\n"
"v_mfma_f32_16x16x32_bf16 v[112:115], a[192:195], a[136:139], v[228:231]\n"
"s_waitcnt lgkmcnt(4)\n"
"ds_read_b128 a[180:183], v251 offset:2048\n"
"v_accvgpr_write_b32 a8, v120\n"
"v_mfma_f32_16x16x32_bf16 v[112:115], a[196:199], a[140:143], v[112:115]\n"
"ds_read_b128 a[148:151], v247 offset:2048\n"
"v_cvt_pk_bf16_f32 v8, v8, v9\n"
"v_mfma_f32_16x16x32_bf16 v[116:119], a[200:203], a[136:139], v[228:231]\n"
"s_waitcnt vmcnt(13) lgkmcnt(5)\n"
"v_cvt_pk_bf16_f32 v162, v164, v165\n"
"v_cvt_pk_bf16_f32 v2, v4, v5\n"
"v_mfma_f32_16x16x32_bf16 v[116:119], a[204:207], a[140:143], v[116:119]\n"
"s_barrier\n"
"ds_read_b128 a[152:155], v246 offset:4096\n"
"v_permlane16_swap_b32 v160, v162\n"
"v_permlane16_swap_b32 v161, v163\n"
"v_accvgpr_write_b32 a27, v139\n"
"v_accvgpr_write_b32 a41, v153\n"
"v_accvgpr_write_b32 a43, v155\n"
"v_permlane16_swap_b32 v0, v2\n"
"v_cvt_pk_bf16_f32 v17, v18, v19\n"
"v_pk_fma_f32 v[32:33], v[32:33], s[70:71], s[70:71] op_sel:[0,0,1] op_sel_hi:[1,0,1]\nv_pk_fma_f32 v[34:35], v[34:35], s[70:71], s[70:71] op_sel:[0,0,1] op_sel_hi:[1,0,1]\nv_pk_fma_f32 v[36:37], v[36:37], s[70:71], s[70:71] op_sel:[0,0,1] op_sel_hi:[1,0,1]\nv_pk_fma_f32 v[38:39], v[38:39], s[70:71], s[70:71] op_sel:[0,0,1] op_sel_hi:[1,0,1]\nv_pk_fma_f32 v[40:41], v[40:41], s[70:71], s[70:71] op_sel:[0,0,1] op_sel_hi:[1,0,1]\nv_pk_fma_f32 v[42:43], v[42:43], s[70:71], s[70:71] op_sel:[0,0,1] op_sel_hi:[1,0,1]\nv_pk_fma_f32 v[44:45], v[44:45], s[70:71], s[70:71] op_sel:[0,0,1] op_sel_hi:[1,0,1]\nv_pk_fma_f32 v[46:47], v[46:47], s[70:71], s[70:71] op_sel:[0,0,1] op_sel_hi:[1,0,1]\n"
"v_mfma_f32_16x16x32_bf16 v[120:123], a[208:211], a[136:139], v[228:231]\n"
"s_add_u32 s40, s48, 256\n"
"s_xor_b32 m0, s50, 0xc000\n"
"v_cvt_pk_bf16_f32 v99, v102, v103\n"
"v_mfma_f32_16x16x32_bf16 v[120:123], a[212:215], a[140:143], v[120:123]\n"
"buffer_load_dwordx4 v224, s[28:31], s40 offen lds\n"
"v_mfma_f32_16x16x32_bf16 v[124:127], a[216:219], a[136:139], v[228:231]\n"
"v_cvt_pk_bf16_f32 v73, v74, v75\n"
"v_cvt_pk_bf16_f32 v9, v10, v11\n"
"v_mfma_f32_16x16x32_bf16 v[124:127], a[220:223], a[140:143], v[124:127]\n"
"s_waitcnt lgkmcnt(3)\n"
"v_cvt_pk_bf16_f32 v98, v100, v101\n"
"v_cvt_pk_bf16_f32 v18, v20, v21\n"
"v_mfma_f32_16x16x32_bf16 v[140:143], a[216:219], a[144:147], v[228:231]\n"
"s_waitcnt lgkmcnt(1)\n"
"ds_read_b128 a[156:159], v247 offset:4096\n"
"v_accvgpr_write_b32 a25, v137\n"
"v_mfma_f32_16x16x32_bf16 v[140:143], a[220:223], a[148:151], v[140:143]\n"
"ds_read_b64_tr_b16 a[240:241], v244 offset:17408\n"
"v_cvt_pk_bf16_f32 v11, v14, v15\n"
"v_mfma_f32_16x16x32_bf16 v[136:139], a[208:211], a[144:147], v[228:231]\n"
"ds_read_b128 a[164:167], v253 offset:0\n"
"v_cvt_pk_bf16_f32 v19, v22, v23\n"
"v_mfma_f32_16x16x32_bf16 v[136:139], a[212:215], a[148:151], v[136:139]\n"
"buffer_load_dwordx4 v225, s[28:31], s40 offen offset:1024 lds\n"
"v_mfma_f32_16x16x32_bf16 v[132:135], a[200:203], a[144:147], v[228:231]\n"
"v_cvt_pk_bf16_f32 v24, v24, v25\n"
"v_cvt_pk_bf16_f32 v32, v32, v33\n"
"v_mfma_f32_16x16x32_bf16 v[132:135], a[204:207], a[148:151], v[132:135]\n"
"ds_write_b128 v243, v[160:163] offset:0\n"
"v_bfe_u32 v100, v242, 9, 1\n"
"v_cvt_pk_bf16_f32 v75, v78, v79\n"
"v_cvt_pk_bf16_f32 v49, v50, v51\n"
"v_permlane16_swap_b32 v9, v11\n"
"v_permlane16_swap_b32 v17, v19\n"
"v_mfma_f32_16x16x32_bf16 v[128:131], a[192:195], a[144:147], v[228:231]\n"
"ds_read_b64_tr_b16 a[232:233], v245 offset:16384\n"
"v_cvt_pk_bf16_f32 v10, v12, v13\n"
"v_mfma_f32_16x16x32_bf16 v[128:131], a[196:199], a[148:151], v[128:131]\n"
"s_waitcnt lgkmcnt(5)\n"
"ds_write_b128 v243, v[168:171] offset:2048\n"
"v_cvt_pk_bf16_f32 v74, v76, v77\n"
"v_permlane16_swap_b32 v16, v18\n"
"v_cvt_pk_bf16_f32 v25, v26, v27\n"
"v_cvt_pk_bf16_f32 v33, v34, v35\n"
"v_bfe_u32 v101, v242, 9, 1\n"
"v_mfma_f32_16x16x32_bf16 v[144:147], a[192:195], a[152:155], v[228:231]\n"
"s_waitcnt lgkmcnt(5)\n"
"ds_read_b64_tr_b16 a[248:249], v245 offset:17408\n"
"v_and_b32 v102, 0xf0, v242\n"
"v_mfma_f32_16x16x32_bf16 v[144:147], a[196:199], a[156:159], v[144:147]\n"
"buffer_load_dwordx4 v226, s[28:31], s40 offen offset:2048 lds\n"
"v_mfma_f32_16x16x32_bf16 v[148:151], a[200:203], a[152:155], v[228:231]\n"
"v_permlane16_swap_b32 v8, v10\n"
"v_mfma_f32_16x16x32_bf16 v[148:151], a[204:207], a[156:159], v[148:151]\n"
"v_lshl_add_u32 v100, v100, 8, v102\n"
"v_cvt_pk_bf16_f32 v56, v56, v57\n"
"v_mfma_f32_16x16x32_bf16 v[152:155], a[208:211], a[152:155], v[228:231]\n"
"ds_read_b64_tr_b16 a[228:229], v244 offset:24576\n"
"ds_read_b64_tr_b16 a[246:247], v244 offset:26112\n"
"v_mfma_f32_16x16x32_bf16 v[152:155], a[212:215], a[156:159], v[152:155]\n"
"ds_read_b64_tr_b16 a[244:245], v244 offset:25600\n"
"v_accvgpr_write_b32 a59, v11\n"
"v_mfma_f32_16x16x32_bf16 v[156:159], a[216:219], a[152:155], v[228:231]\n"
"ds_read_b64_tr_b16 a[242:243], v244 offset:17920\n"
"v_accvgpr_write_b32 a50, v2\n"
"v_mfma_f32_16x16x32_bf16 v[156:159], a[220:223], a[156:159], v[156:159]\n"
"buffer_load_dwordx4 v227, s[28:31], s40 offen offset:3072 lds\n"
"v_mfma_f32_16x16x32_bf16 v[172:175], a[216:219], a[160:163], v[228:231]\n"
"s_waitcnt lgkmcnt(4)\n"
"s_cmp_eq_u32 s47, 8\n"
"v_cvt_pk_bf16_f32 v57, v58, v59\n"
"v_cvt_pk_bf16_f32 v3, v6, v7\n"
"v_mfma_f32_16x16x32_bf16 v[172:175], a[220:223], a[164:167], v[172:175]\n"
"s_cselect_b32 s41, s52, s58\n"
"v_cvt_pk_bf16_f32 v89, v90, v91\n"
"v_accvgpr_write_b32 a58, v10\n"
"v_mfma_f32_16x16x32_bf16 v[168:171], a[208:211], a[160:163], v[228:231]\n"
"s_add_u32 s48, s48, s41\n"
"ds_read_b128 a[184:187], v250 offset:4096\n"
"v_cvt_pk_bf16_f32 v83, v86, v87\n"
"v_mfma_f32_16x16x32_bf16 v[168:171], a[212:215], a[164:167], v[168:171]\n"
"s_sub_u32 s41, s48, 5242880\n"
"ds_read_b64_tr_b16 a[250:251], v245 offset:17920\n"
"v_cvt_pk_bf16_f32 v26, v28, v29\n"
"v_mfma_f32_16x16x32_bf16 v[164:167], a[200:203], a[160:163], v[228:231]\n"
"s_min_u32 s48, s48, s41\n"
"ds_read_b64_tr_b16 a[230:231], v244 offset:25088\n"
"v_cvt_pk_bf16_f32 v59, v62, v63\n"
"v_mfma_f32_16x16x32_bf16 v[164:167], a[204:207], a[164:167], v[164:167]\n"
"ds_read_b64_tr_b16 a[236:237], v245 offset:24576\n"
"v_permlane16_swap_b32 v1, v3\n"
"v_accvgpr_write_b32 a57, v9\n"
"v_cvt_pk_bf16_f32 v27, v30, v31\n"
"v_mfma_f32_16x16x32_bf16 v[160:163], a[192:195], a[160:163], v[228:231]\n"
"s_mov_b32 m0, s50\n"
"ds_read_b128 a[176:179], v250 offset:2048\n"
"v_cvt_pk_bf16_f32 v40, v40, v41\n"
"v_mfma_f32_16x16x32_bf16 v[160:163], a[196:199], a[164:167], v[160:163]\n"
"buffer_load_dwordx4 v224, s[28:31], s48 offen lds\n"
"v_mfma_f32_16x16x32_bf16 v[176:179], a[192:195], a[168:171], v[228:231]\n"
"v_cvt_pk_bf16_f32 v107, v110, v111\n"
"v_accvgpr_write_b32 a64, v16\n"
"v_mfma_f32_16x16x32_bf16 v[176:179], a[196:199], a[172:175], v[176:179]\n"
"v_cvt_pk_bf16_f32 v64, v64, v65\n"
"v_accvgpr_write_b32 a67, v19\n"
"v_mfma_f32_16x16x32_bf16 v[180:183], a[200:203], a[168:171], v[228:231]\n"
"ds_read_b64_tr_b16 a[238:239], v245 offset:25088\n"
"v_bfe_u32 v102, v242, 8, 1\n"
"v_mfma_f32_16x16x32_bf16 v[180:183], a[204:207], a[172:175], v[180:183]\n"
"buffer_load_dwordx4 v225, s[28:31], s48 offen offset:1024 lds\n"
"v_mfma_f32_16x16x32_bf16 v[184:187], a[208:211], a[168:171], v[228:231]\n"
"v_lshl_add_u32 v100, v102, 3, v100\n"
"v_cvt_pk_bf16_f32 v65, v66, v67\n"
"v_mfma_f32_16x16x32_bf16 v[184:187], a[212:215], a[172:175], v[184:187]\n"
"v_permlane16_swap_b32 v24, v26\n"
"v_mfma_f32_16x16x32_bf16 v[188:191], a[216:219], a[168:171], v[228:231]\n"
"v_accvgpr_write_b32 a72, v24\n"
"v_cvt_pk_bf16_f32 v34, v36, v37\n"
"v_mfma_f32_16x16x32_bf16 v[188:191], a[220:223], a[172:175], v[188:191]\n"
"s_waitcnt lgkmcnt(1)\n"
"v_cvt_pk_bf16_f32 v58, v60, v61\n"
"v_cvt_pk_bf16_f32 v41, v42, v43\n"
"v_mfma_f32_16x16x32_bf16 v[204:207], a[216:219], a[176:179], v[228:231]\n"
"v_xor_b32_e64 v102, s42, 1\n"
"v_cvt_pk_bf16_f32 v51, v54, v55\n"
"v_mfma_f32_16x16x32_bf16 v[204:207], a[220:223], a[180:183], v[204:207]\n"
"buffer_load_dwordx4 v226, s[28:31], s48 offen offset:2048 lds\n"
"v_mfma_f32_16x16x32_bf16 v[200:203], a[208:211], a[176:179], v[228:231]\n"
"v_lshl_add_u32 v100, v102, 10, v100\n"
"v_cvt_pk_bf16_f32 v35, v38, v39\n"
"v_mfma_f32_16x16x32_bf16 v[200:203], a[212:215], a[180:183], v[200:203]\n"
"v_add_u32 v100, 94208, v100\n"
"v_accvgpr_write_b32 a65, v17\n"
"v_mfma_f32_16x16x32_bf16 v[196:199], a[200:203], a[176:179], v[228:231]\n"
"ds_read_b64_tr_b16 a[224:225], v244 offset:16384\n"
"ds_read_b64_tr_b16 a[226:227], v244 offset:16896\n"
"v_mfma_f32_16x16x32_bf16 v[196:199], a[204:207], a[180:183], v[196:199]\n"
"v_permlane16_swap_b32 v32, v34\n"
"v_mfma_f32_16x16x32_bf16 v[192:195], a[192:195], a[176:179], v[228:231]\n"
"ds_read_b64_tr_b16 a[252:253], v245 offset:25600\n"
"v_cvt_pk_bf16_f32 v106, v108, v109\n"
"v_mfma_f32_16x16x32_bf16 v[192:195], a[196:199], a[180:183], v[192:195]\n"
"buffer_load_dwordx4 v227, s[28:31], s48 offen offset:3072 lds\n"
"v_mfma_f32_16x16x32_bf16 v[208:211], a[192:195], a[184:187], v[228:231]\n"
"s_xor_b32 s50, s50, s74\n"
"v_accvgpr_write_b32 a51, v3\n"
"v_accvgpr_write_b32 a56, v8\n"
"v_mfma_f32_16x16x32_bf16 v[208:211], a[196:199], a[188:191], v[208:211]\n"
"v_accvgpr_write_b32 a48, v0\n"
"v_cvt_pk_bf16_f32 v43, v46, v47\n"
"v_mfma_f32_16x16x32_bf16 v[212:215], a[200:203], a[184:187], v[228:231]\n"
"ds_read_b64_tr_b16 a[254:255], v245 offset:26112\n"
"v_cvt_pk_bf16_f32 v66, v68, v69\n"
"v_mfma_f32_16x16x32_bf16 v[212:215], a[204:207], a[188:191], v[212:215]\n"
"ds_write2st64_b64 v100, v[104:105], v[106:107] offset0:128 offset1:129\n"
"v_permlane16_swap_b32 v25, v27\n"
"v_permlane16_swap_b32 v41, v43\n"
"v_mfma_f32_16x16x32_bf16 v[216:219], a[208:211], a[184:187], v[228:231]\n"
"ds_read_b64_tr_b16 a[234:235], v245 offset:16896\n"
"v_cvt_pk_bf16_f32 v42, v44, v45\n"
"v_mfma_f32_16x16x32_bf16 v[216:219], a[212:215], a[188:191], v[216:219]\n"
"ds_write2st64_b64 v100, v[72:73], v[74:75] offset0:64 offset1:65\n"
"v_cvt_pk_bf16_f32 v90, v92, v93\n"
"v_mfma_f32_16x16x32_bf16 v[220:223], a[216:219], a[184:187], v[228:231]\n"
"v_accvgpr_write_b32 a49, v1\n"
"v_and_b32 v102, 0xf0, v242\n"
"v_mfma_f32_16x16x32_bf16 v[220:223], a[220:223], a[188:191], v[220:223]\n"
"s_waitcnt lgkmcnt(4)\n"
"ds_write2st64_b64 v100, v[80:81], v[82:83] offset0:88 offset1:89\n"
"v_cvt_pk_bf16_f32 v91, v94, v95\n"
"v_mfma_f32_16x16x32_bf16 v[0:3], a[224:227], a[136:139], v[228:231]\n"
"v_accvgpr_write_b32 a74, v26\n"
"v_xor_b32 v245, s74, v245\n"
"v_mfma_f32_16x16x32_bf16 v[0:3], a[228:231], a[140:143], v[0:3]\n"
"s_waitcnt lgkmcnt(2)\n"
"ds_write2st64_b64 v100, v[88:89], v[90:91] offset0:96 offset1:97\n"
"v_permlane16_swap_b32 v40, v42\n"
"v_mfma_f32_16x16x32_bf16 v[4:7], a[232:235], a[136:139], v[228:231]\n"
"s_waitcnt vmcnt(8)\n"
"v_cvt_pk_bf16_f32 v67, v70, v71\n"
"v_accvgpr_write_b32 a75, v27\n"
"v_mfma_f32_16x16x32_bf16 v[4:7], a[236:239], a[140:143], v[4:7]\n"
"s_barrier\n"
"ds_write2st64_b64 v100, v[64:65], v[66:67] offset0:0 offset1:1\n"
"v_permlane16_swap_b32 v56, v58\n"
"v_mfma_f32_16x16x32_bf16 v[8:11], a[240:243], a[136:139], v[228:231]\n"
"s_mov_b32 m0, s51\n"
"ds_read_b64_tr_b16 a[218:219], v245 offset:1536\n"
"v_xor_b32 v244, s74, v244\n"
"v_mfma_f32_16x16x32_bf16 v[8:11], a[244:247], a[140:143], v[8:11]\n"
"buffer_load_dwordx4 v248, s[24:27], s49 offen lds\n"
"v_mfma_f32_16x16x32_bf16 v[12:15], a[248:251], a[136:139], v[228:231]\n"
"v_accvgpr_write_b32 a82, v34\n"
"v_accvgpr_write_b32 a88, v40\n"
"v_mfma_f32_16x16x32_bf16 v[12:15], a[252:255], a[140:143], v[12:15]\n"
"v_permlane16_swap_b32 v57, v59\n"
"v_mfma_f32_16x16x32_bf16 v[28:31], a[248:251], a[144:147], v[228:231]\n"
"v_accvgpr_write_b32 a73, v25\n"
"v_accvgpr_write_b32 a91, v43\n"
"v_mfma_f32_16x16x32_bf16 v[28:31], a[252:255], a[148:151], v[28:31]\n"
"ds_write2st64_b64 v100, v[96:97], v[98:99] offset0:120 offset1:121\n"
"v_accvgpr_write_b32 a66, v18\n"
"v_mfma_f32_16x16x32_bf16 v[24:27], a[240:243], a[144:147], v[228:231]\n"
"v_permlane16_swap_b32 v33, v35\n"
"v_mfma_f32_16x16x32_bf16 v[24:27], a[244:247], a[148:151], v[24:27]\n"
"buffer_load_dwordx4 v249, s[24:27], s49 offen offset:1024 lds\n"
"v_mfma_f32_16x16x32_bf16 v[20:23], a[232:235], a[144:147], v[228:231]\n"
"v_accvgpr_write_b32 a80, v32\n"
"v_accvgpr_write_b32 a90, v42\n"
"v_mfma_f32_16x16x32_bf16 v[20:23], a[236:239], a[148:151], v[20:23]\n"
"v_cvt_pk_bf16_f32 v50, v52, v53\n"
"v_accvgpr_write_b32 a81, v33\n"
"v_mfma_f32_16x16x32_bf16 v[16:19], a[224:227], a[144:147], v[228:231]\n"
"ds_read_b64_tr_b16 a[210:211], v244 offset:1536\n"
"v_accvgpr_write_b32 a83, v35\n"
"v_mfma_f32_16x16x32_bf16 v[16:19], a[228:231], a[148:151], v[16:19]\n"
"ds_write_b128 v243, v[56:59] offset:6144\n"
"v_mfma_f32_16x16x32_bf16 v[32:35], a[224:227], a[152:155], v[228:231]\n"
"v_permlane16_swap_b32 v48, v50\n"
"v_mfma_f32_16x16x32_bf16 v[32:35], a[228:231], a[156:159], v[32:35]\n"
"buffer_load_dwordx4 v254, s[24:27], s49 offen offset:2048 lds\n"
"v_mfma_f32_16x16x32_bf16 v[36:39], a[232:235], a[152:155], v[228:231]\n"
"s_waitcnt vmcnt(63) lgkmcnt(2)\n"
"v_permlane16_swap_b32 v49, v51\n"
"v_mfma_f32_16x16x32_bf16 v[36:39], a[236:239], a[156:159], v[36:39]\n"
"s_barrier\n"
"ds_write_b128 v243, v[48:51] offset:4096\n"
"v_accvgpr_write_b32 a89, v41\n"
"v_mfma_f32_16x16x32_bf16 v[40:43], a[240:243], a[152:155], v[228:231]\n"
"v_xor_b32 v246, s74, v246\n"
"v_xor_b32 v247, s74, v247\n"
"v_mfma_f32_16x16x32_bf16 v[40:43], a[244:247], a[156:159], v[40:43]\n"
"ds_read_b128 a[148:151], v247 offset:2048\n"
"v_lshl_add_u32 v101, v101, 8, v102\n"
"v_mfma_f32_16x16x32_bf16 v[44:47], a[248:251], a[152:155], v[228:231]\n"
"ds_read_b128 a[140:143], v247 offset:0\n"
"v_bfe_u32 v102, v242, 8, 1\n"
"v_mfma_f32_16x16x32_bf16 v[44:47], a[252:255], a[156:159], v[44:47]\n"
"buffer_load_dwordx4 v255, s[24:27], s49 offen offset:3072 lds\n"
"v_mfma_f32_16x16x32_bf16 v[60:63], a[248:251], a[160:163], v[228:231]\n"
"v_xor_b32 v250, s74, v250\n"
"v_lshl_add_u32 v101, v102, 9, v101\n"
"v_mfma_f32_16x16x32_bf16 v[60:63], a[252:255], a[164:167], v[60:63]\n"
"ds_read_b64_tr_b16 a[192:193], v244 offset:0\n"
"ds_read_b128 a[156:159], v247 offset:4096\n"
"v_mfma_f32_16x16x32_bf16 v[56:59], a[240:243], a[160:163], v[228:231]\n"
"ds_read_b64_tr_b16 a[214:215], v244 offset:9728\n"
"ds_read_b64_tr_b16 a[220:221], v245 offset:9216\n"
"v_mfma_f32_16x16x32_bf16 v[56:59], a[244:247], a[164:167], v[56:59]\n"
"s_add_u32 s40, s49, 40960\n"
"ds_read_b128 a[144:147], v246 offset:2048\n"
"v_xor_b32_e64 v102, s42, 0\n"
"v_mfma_f32_16x16x32_bf16 v[52:55], a[232:235], a[160:163], v[228:231]\n"
"s_add_u32 m0, s51, 4096\n"
"ds_read_b64_tr_b16 a[200:201], v245 offset:0\n"
"ds_read_b64_tr_b16 a[216:217], v245 offset:1024\n"
"v_mfma_f32_16x16x32_bf16 v[52:55], a[236:239], a[164:167], v[52:55]\n"
"buffer_load_dwordx4 v248, s[24:27], s40 offen lds\n"
"v_mfma_f32_16x16x32_bf16 v[48:51], a[224:227], a[160:163], v[228:231]\n"
"v_lshl_add_u32 v101, v102, 10, v101\n"
"v_add_u32 v101, 94208, v101\n"
"v_mfma_f32_16x16x32_bf16 v[48:51], a[228:231], a[164:167], v[48:51]\n"
"ds_read_b64_tr_b16 a[222:223], v245 offset:9728\n"
"ds_read_b128 a[152:155], v246 offset:4096\n"
"v_mfma_f32_16x16x32_bf16 v[64:67], a[224:227], a[168:171], v[228:231]\n"
"ds_read_b128 a[68:71], v101 offset:45056\n"
"v_mfma_f32_16x16x32_bf16 v[64:67], a[228:231], a[172:175], v[64:67]\n"
"ds_read_b128 a[76:79], v101 offset:49152\n"
"v_xor_b32 v251, s74, v251\n"
"v_mfma_f32_16x16x32_bf16 v[68:71], a[232:235], a[168:171], v[228:231]\n"
"ds_read_b64_tr_b16 a[206:207], v245 offset:8704\n"
"ds_read_b128 a[136:139], v246 offset:0\n"
"v_mfma_f32_16x16x32_bf16 v[68:71], a[236:239], a[172:175], v[68:71]\n"
"buffer_load_dwordx4 v249, s[24:27], s40 offen offset:1024 lds\n"
"v_mfma_f32_16x16x32_bf16 v[72:75], a[240:243], a[168:171], v[228:231]\n"
"ds_read_b64_tr_b16 a[208:209], v244 offset:1024\n"
"ds_read_b128 a[60:63], v101 offset:32768\n"
"v_mfma_f32_16x16x32_bf16 v[72:75], a[244:247], a[172:175], v[72:75]\n"
"v_xor_b32 v252, s74, v252\n"
"v_add_u32 v102, s75, v101\n"
"v_mfma_f32_16x16x32_bf16 v[76:79], a[248:251], a[168:171], v[228:231]\n"
"ds_read_b128 a[52:55], v101 offset:0\n"
"ds_read_b128 a[84:87], v101 offset:61440\n"
"v_mfma_f32_16x16x32_bf16 v[76:79], a[252:255], a[172:175], v[76:79]\n"
"ds_read_b64_tr_b16 a[204:205], v245 offset:8192\n"
"v_xor_b32 v253, s74, v253\n"
"v_mfma_f32_16x16x32_bf16 v[92:95], a[248:251], a[176:179], v[228:231]\n"
"ds_read_b128 a[92:95], v102 offset:0\n"
"ds_read_b128 a[172:175], v251 offset:0\n"
"v_mfma_f32_16x16x32_bf16 v[92:95], a[252:255], a[180:183], v[92:95]\n"
"buffer_load_dwordx4 v254, s[24:27], s40 offen offset:2048 lds\n"
"v_mfma_f32_16x16x32_bf16 v[88:91], a[240:243], a[176:179], v[228:231]\n"
"s_add_u32 s49, s49, 128\n"
"ds_read_b64_tr_b16 a[196:197], v244 offset:8192\n"
"ds_read_b128 a[160:163], v252 offset:0\n"
"v_mfma_f32_16x16x32_bf16 v[88:91], a[244:247], a[180:183], v[88:91]\n"
"s_sub_u32 s41, s49, 1280\n"
"ds_read_b64_tr_b16 a[194:195], v244 offset:512\n"
"ds_read_b64_tr_b16 a[202:203], v245 offset:512\n"
"v_mfma_f32_16x16x32_bf16 v[84:87], a[232:235], a[176:179], v[228:231]\n"
"s_min_u32 s49, s49, s41\n"
"ds_read_b64_tr_b16 a[198:199], v244 offset:8704\n"
"ds_read_b128 a[164:167], v253 offset:0\n"
"v_mfma_f32_16x16x32_bf16 v[84:87], a[236:239], a[180:183], v[84:87]\n"
"s_xor_b32 s51, s51, s74\n"
"ds_read_b128 a[168:171], v250 offset:0\n"
"v_mfma_f32_16x16x32_bf16 v[80:83], a[224:227], a[176:179], v[228:231]\n"
"ds_read_b64_tr_b16 a[212:213], v244 offset:9216\n"
"ds_read_b128 a[176:179], v250 offset:2048\n"
"v_mfma_f32_16x16x32_bf16 v[80:83], a[228:231], a[180:183], v[80:83]\n"
"ds_read_b128 a[180:183], v251 offset:2048\n"
"v_mfma_f32_16x16x32_bf16 v[96:99], a[224:227], a[184:187], v[228:231]\n"
"v_mfma_f32_16x16x32_bf16 v[96:99], a[228:231], a[188:191], v[96:99]\n"
"v_mfma_f32_16x16x32_bf16 v[100:103], a[232:235], a[184:187], v[228:231]\n"
"v_mfma_f32_16x16x32_bf16 v[100:103], a[236:239], a[188:191], v[100:103]\n"
"v_mfma_f32_16x16x32_bf16 v[104:107], a[240:243], a[184:187], v[228:231]\n"
"v_mfma_f32_16x16x32_bf16 v[104:107], a[244:247], a[188:191], v[104:107]\n"
"v_mfma_f32_16x16x32_bf16 v[108:111], a[248:251], a[184:187], v[228:231]\n"
"s_add_u32 s47, s47, 1\n"
"ds_read_b128 a[184:187], v250 offset:4096\n"
"v_mfma_f32_16x16x32_bf16 v[108:111], a[252:255], a[188:191], v[108:111]\n"
"s_waitcnt lgkmcnt(1)\n"
"ds_read_b128 a[188:191], v251 offset:4096\n"
"s_mov_b32 s62, 18\n"
"s_branch LPL_%=\n"
"LPT2_%=:\n"
"v_mfma_f32_16x16x32_bf16 v[112:115], a[192:195], a[136:139], v[112:115]\n"
"s_add_u32 s78, s32, s96\ns_addc_u32 s79, s33, 0\n"
"s_mov_b64 s[80:81], 0\n"
"s_mov_b32 s82, 0\n"
"v_mfma_f32_16x16x32_bf16 v[112:115], a[196:199], a[140:143], v[112:115]\n"
"v_mfma_f32_16x16x32_bf16 v[116:119], a[200:203], a[136:139], v[116:119]\n"
"s_waitcnt vmcnt(11)\n"
"s_load_dword s80, s[78:79], 0\n"
"v_mfma_f32_16x16x32_bf16 v[116:119], a[204:207], a[140:143], v[116:119]\n"
"s_barrier\n"
"v_mfma_f32_16x16x32_bf16 v[120:123], a[208:211], a[136:139], v[120:123]\n"
"s_add_u32 s40, s48, 256\n"
"s_xor_b32 m0, s50, 0xc000\n"
"ds_read_b64_tr_b16 a[254:255], v245 offset:26112\n"
"v_mfma_f32_16x16x32_bf16 v[120:123], a[212:215], a[140:143], v[120:123]\n"
"buffer_load_dwordx4 v224, s[28:31], s40 offen lds\n"
"v_mfma_f32_16x16x32_bf16 v[124:127], a[216:219], a[136:139], v[124:127]\n"
"v_mfma_f32_16x16x32_bf16 v[124:127], a[220:223], a[140:143], v[124:127]\n"
"v_mfma_f32_16x16x32_bf16 v[140:143], a[216:219], a[144:147], v[140:143]\n"
"ds_read_b64_tr_b16 a[246:247], v244 offset:26112\n"
"v_mfma_f32_16x16x32_bf16 v[140:143], a[220:223], a[148:151], v[140:143]\n"
"v_mfma_f32_16x16x32_bf16 v[136:139], a[208:211], a[144:147], v[136:139]\n"
"ds_read_b64_tr_b16 a[226:227], v244 offset:16896\n"
"v_mfma_f32_16x16x32_bf16 v[136:139], a[212:215], a[148:151], v[136:139]\n"
"buffer_load_dwordx4 v225, s[28:31], s40 offen offset:1024 lds\n"
"v_mfma_f32_16x16x32_bf16 v[132:135], a[200:203], a[144:147], v[132:135]\n"
"s_add_u32 s82, s82, s80\n"
"v_mfma_f32_16x16x32_bf16 v[132:135], a[204:207], a[148:151], v[132:135]\n"
"s_add_u32 s82, s82, s80\n"
"v_mfma_f32_16x16x32_bf16 v[128:131], a[192:195], a[144:147], v[128:131]\n"
"s_add_u32 s82, s82, s80\n"
"ds_read_b64_tr_b16 a[244:245], v244 offset:25600\n"
"v_mfma_f32_16x16x32_bf16 v[128:131], a[196:199], a[148:151], v[128:131]\n"
"s_add_u32 s82, s82, s80\n"
"v_mfma_f32_16x16x32_bf16 v[144:147], a[192:195], a[152:155], v[144:147]\n"
"s_add_u32 s82, s82, s80\n"
"ds_read_b64_tr_b16 a[236:237], v245 offset:24576\n"
"v_mfma_f32_16x16x32_bf16 v[144:147], a[196:199], a[156:159], v[144:147]\n"
"s_add_u32 s82, s82, s80\n"
"v_mfma_f32_16x16x32_bf16 v[148:151], a[200:203], a[152:155], v[148:151]\n"
"s_add_u32 s82, s82, s80\n"
"ds_read_b64_tr_b16 a[224:225], v244 offset:16384\n"
"v_mfma_f32_16x16x32_bf16 v[148:151], a[204:207], a[156:159], v[148:151]\n"
"s_add_u32 s82, s82, s80\n"
"buffer_load_dwordx4 v226, s[28:31], s40 offen offset:2048 lds\n"
"v_mfma_f32_16x16x32_bf16 v[152:155], a[208:211], a[152:155], v[152:155]\n"
"s_add_u32 s82, s82, s80\n"
"v_mfma_f32_16x16x32_bf16 v[152:155], a[212:215], a[156:159], v[152:155]\n"
"s_add_u32 s82, s82, s80\n"
"v_mfma_f32_16x16x32_bf16 v[156:159], a[216:219], a[152:155], v[156:159]\n"
"s_add_u32 s82, s82, s80\n"
"ds_read_b64_tr_b16 a[234:235], v245 offset:16896\n"
"v_mfma_f32_16x16x32_bf16 v[156:159], a[220:223], a[156:159], v[156:159]\n"
"s_add_u32 s82, s82, s80\n"
"v_mfma_f32_16x16x32_bf16 v[172:175], a[216:219], a[160:163], v[172:175]\n"
"s_add_u32 s82, s82, s80\n"
"ds_read_b64_tr_b16 a[250:251], v245 offset:17920\n"
"v_mfma_f32_16x16x32_bf16 v[172:175], a[220:223], a[164:167], v[172:175]\n"
"s_add_u32 s82, s82, s80\n"
"buffer_load_dwordx4 v227, s[28:31], s40 offen offset:3072 lds\n"
"v_mfma_f32_16x16x32_bf16 v[168:171], a[208:211], a[160:163], v[168:171]\n"
"s_add_u32 s82, s82, s80\n"
"s_mov_b32 s58, 245760\n"
"v_mfma_f32_16x16x32_bf16 v[168:171], a[212:215], a[164:167], v[168:171]\n"
"s_add_u32 s82, s82, s80\n"
"s_mov_b32 s47, 0\n"
"v_mfma_f32_16x16x32_bf16 v[164:167], a[200:203], a[160:163], v[164:167]\n"
"s_add_u32 s82, s82, s80\n"
"s_mov_b32 s52, -896\n"
"ds_read_b64_tr_b16 a[252:253], v245 offset:25600\n"
"v_mfma_f32_16x16x32_bf16 v[164:167], a[204:207], a[164:167], v[164:167]\n"
"s_load_dword s81, s[78:79], 0x2000\n"
"s_mov_b32 s53, 0\n"
"v_mfma_f32_16x16x32_bf16 v[160:163], a[192:195], a[160:163], v[160:163]\n"
"s_mov_b32 m0, s44\n"
"ds_read_b64_tr_b16 a[248:249], v245 offset:17408\n"
"v_mfma_f32_16x16x32_bf16 v[160:163], a[196:199], a[164:167], v[160:163]\n"
"buffer_load_dwordx4 v240, s[16:19], s56 offen nt lds\n"
"v_mfma_f32_16x16x32_bf16 v[176:179], a[192:195], a[168:171], v[176:179]\n"
"v_mfma_f32_16x16x32_bf16 v[176:179], a[196:199], a[172:175], v[176:179]\n"
"v_mfma_f32_16x16x32_bf16 v[180:183], a[200:203], a[168:171], v[180:183]\n"
"ds_read_b64_tr_b16 a[230:231], v244 offset:25088\n"
"v_mfma_f32_16x16x32_bf16 v[180:183], a[204:207], a[172:175], v[180:183]\n"
"v_mfma_f32_16x16x32_bf16 v[184:187], a[208:211], a[168:171], v[184:187]\n"
"ds_read_b64_tr_b16 a[238:239], v245 offset:25088\n"
"v_mfma_f32_16x16x32_bf16 v[184:187], a[212:215], a[172:175], v[184:187]\n"
"v_mfma_f32_16x16x32_bf16 v[188:191], a[216:219], a[168:171], v[188:191]\n"
"s_add_u32 s40, s56, s52\n"
"ds_read_b64_tr_b16 a[228:229], v244 offset:24576\n"
"v_mfma_f32_16x16x32_bf16 v[188:191], a[220:223], a[172:175], v[188:191]\n"
"s_sub_u32 s82, s82, s81\n"
"buffer_load_dwordx4 v240, s[16:19], s40 offen offset:1024 nt lds\n"
"v_mfma_f32_16x16x32_bf16 v[204:207], a[216:219], a[176:179], v[204:207]\n"
"s_sub_u32 s82, s82, s81\n"
"v_xor_b32 v250, s74, v250\n"
"v_mfma_f32_16x16x32_bf16 v[204:207], a[220:223], a[180:183], v[204:207]\n"
"s_sub_u32 s82, s82, s81\n"
"v_mfma_f32_16x16x32_bf16 v[200:203], a[208:211], a[176:179], v[200:203]\n"
"s_sub_u32 s82, s82, s81\n"
"ds_read_b64_tr_b16 a[232:233], v245 offset:16384\n"
"v_mfma_f32_16x16x32_bf16 v[200:203], a[212:215], a[180:183], v[200:203]\n"
"s_sub_u32 s82, s82, s81\n"
"v_mfma_f32_16x16x32_bf16 v[196:199], a[200:203], a[176:179], v[196:199]\n"
"s_sub_u32 s82, s82, s81\n"
"s_add_u32 s40, s56, s53\n"
"ds_read_b64_tr_b16 a[240:241], v244 offset:17408\n"
"v_mfma_f32_16x16x32_bf16 v[196:199], a[204:207], a[180:183], v[196:199]\n"
"s_sub_u32 s82, s82, s81\n"
"buffer_load_dwordx4 v240, s[16:19], s40 offen offset:2048 nt lds\n"
"v_mfma_f32_16x16x32_bf16 v[192:195], a[192:195], a[176:179], v[192:195]\n"
"s_sub_u32 s82, s82, s81\n"
"v_and_b32 v229, 0x60, v242\n"
"v_mfma_f32_16x16x32_bf16 v[192:195], a[196:199], a[180:183], v[192:195]\n"
"s_waitcnt lgkmcnt(2)\n"
"s_sub_u32 s82, s82, s81\n"
"v_mfma_f32_16x16x32_bf16 v[208:211], a[192:195], a[184:187], v[208:211]\n"
"s_sub_u32 s82, s82, s81\n"
"ds_read_b64_tr_b16 a[242:243], v244 offset:17920\n"
"v_mfma_f32_16x16x32_bf16 v[208:211], a[196:199], a[188:191], v[208:211]\n"
"s_sub_u32 s82, s82, s81\n"
"v_xor_b32 v246, s74, v246\n"
"v_mfma_f32_16x16x32_bf16 v[212:215], a[200:203], a[184:187], v[212:215]\n"
"s_sub_u32 s82, s82, s81\n"
"s_add_u32 s40, s56, s54\n"
"v_mfma_f32_16x16x32_bf16 v[212:215], a[204:207], a[188:191], v[212:215]\n"
"s_sub_u32 s82, s82, s81\n"
"buffer_load_dwordx4 v240, s[16:19], s40 offen offset:3072 nt lds\n"
"v_mfma_f32_16x16x32_bf16 v[216:219], a[208:211], a[184:187], v[216:219]\n"
"s_sub_u32 s82, s82, s81\n"
"v_bfe_u32 v228, v242, 8, 2\n"
"v_mfma_f32_16x16x32_bf16 v[216:219], a[212:215], a[188:191], v[216:219]\n"
"s_sub_u32 s82, s82, s81\n"
"v_mfma_f32_16x16x32_bf16 v[220:223], a[216:219], a[184:187], v[220:223]\n"
"s_sub_u32 s82, s82, s81\n"
"v_mfma_f32_16x16x32_bf16 v[220:223], a[220:223], a[188:191], v[220:223]\n"
"s_sub_u32 s82, s82, s81\n"
"v_mfma_f32_16x16x32_bf16 v[0:3], a[224:227], a[136:139], v[0:3]\n"
"v_mfma_f32_16x16x32_bf16 v[0:3], a[228:231], a[140:143], v[0:3]\n"
"s_waitcnt lgkmcnt(0)\n"
"v_mfma_f32_16x16x32_bf16 v[4:7], a[232:235], a[136:139], v[4:7]\n"
"s_waitcnt vmcnt(8)\n"
"v_xor_b32 v245, s74, v245\n"
"v_mfma_f32_16x16x32_bf16 v[4:7], a[236:239], a[140:143], v[4:7]\n"
"s_barrier\n"
"v_mfma_f32_16x16x32_bf16 v[8:11], a[240:243], a[136:139], v[8:11]\n"
"s_add_u32 m0, s44, 65536\n"
"ds_read_b64_tr_b16 a[204:205], v245 offset:8192\n"
"v_mfma_f32_16x16x32_bf16 v[8:11], a[244:247], a[140:143], v[8:11]\n"
"buffer_load_dwordx4 v240, s[20:23], s56 offen nt lds\n"
"v_mfma_f32_16x16x32_bf16 v[12:15], a[248:251], a[136:139], v[12:15]\n"
"s_lshr_b32 s82, s82, 31\n"
"v_xor_b32 v244, s74, v244\n"
"v_mfma_f32_16x16x32_bf16 v[12:15], a[252:255], a[140:143], v[12:15]\n"
"s_add_u32 s82, s82, s60\n"
"v_xor_b32 v247, s74, v247\n"
"v_mfma_f32_16x16x32_bf16 v[28:31], a[248:251], a[144:147], v[28:31]\n"
"s_add_u32 s82, s82, 32\n"
"ds_read_b64_tr_b16 a[208:209], v244 offset:1024\n"
"v_mfma_f32_16x16x32_bf16 v[28:31], a[252:255], a[148:151], v[28:31]\n"
"v_mov_b32 v254, s82\n"
"ds_read_b64_tr_b16 a[196:197], v244 offset:8192\n"
"v_mfma_f32_16x16x32_bf16 v[24:27], a[240:243], a[144:147], v[24:27]\n"
"v_lshlrev_b32_e64 v255, 2, s93\n"
"ds_read_b64_tr_b16 a[210:211], v244 offset:1536\n"
"v_mfma_f32_16x16x32_bf16 v[24:27], a[244:247], a[148:151], v[24:27]\n"
"ds_read_b64_tr_b16 a[222:223], v245 offset:9728\n"
"v_xor_b32 v251, s74, v251\n"
"v_mfma_f32_16x16x32_bf16 v[20:23], a[232:235], a[144:147], v[20:23]\n"
"s_add_u32 s40, s56, s52\n"
"ds_read_b64_tr_b16 a[202:203], v245 offset:512\n"
"v_mfma_f32_16x16x32_bf16 v[20:23], a[236:239], a[148:151], v[20:23]\n"
"buffer_load_dwordx4 v240, s[20:23], s40 offen offset:1024 nt lds\n"
"v_mfma_f32_16x16x32_bf16 v[16:19], a[224:227], a[144:147], v[16:19]\n"
"v_mfma_f32_16x16x32_bf16 v[16:19], a[228:231], a[148:151], v[16:19]\n"
"s_mov_b64 exec, 1\n"
"global_store_dword v255, v254, s[6:7] offset:256 sc1\n"
"s_mov_b64 exec, -1\n"
"v_mfma_f32_16x16x32_bf16 v[32:35], a[224:227], a[152:155], v[32:35]\n"
"ds_read_b64_tr_b16 a[192:193], v244 offset:0\n"
"v_bfe_i32 v241, v242, 4, 1\n"
"v_mfma_f32_16x16x32_bf16 v[32:35], a[228:231], a[156:159], v[32:35]\n"
"v_mfma_f32_16x16x32_bf16 v[36:39], a[232:235], a[152:155], v[36:39]\n"
"s_add_u32 s40, s56, s53\n"
"ds_read_b128 a[136:139], v246 offset:0\n"
"v_mfma_f32_16x16x32_bf16 v[36:39], a[236:239], a[156:159], v[36:39]\n"
"buffer_load_dwordx4 v240, s[20:23], s40 offen offset:2048 nt lds\n"
"v_mfma_f32_16x16x32_bf16 v[40:43], a[240:243], a[152:155], v[40:43]\n"
"v_mfma_f32_16x16x32_bf16 v[40:43], a[244:247], a[156:159], v[40:43]\n"
"v_mfma_f32_16x16x32_bf16 v[44:47], a[248:251], a[152:155], v[44:47]\n"
"ds_read_b128 a[140:143], v247 offset:0\n"
"v_mfma_f32_16x16x32_bf16 v[44:47], a[252:255], a[156:159], v[44:47]\n"
"v_mfma_f32_16x16x32_bf16 v[60:63], a[248:251], a[160:163], v[60:63]\n"
"s_add_u32 s40, s56, s54\n"
"ds_read_b64_tr_b16 a[194:195], v244 offset:512\n"
"v_mfma_f32_16x16x32_bf16 v[60:63], a[252:255], a[164:167], v[60:63]\n"
"buffer_load_dwordx4 v240, s[20:23], s40 offen offset:3072 nt lds\n"
"v_mfma_f32_16x16x32_bf16 v[56:59], a[240:243], a[160:163], v[56:59]\n"
"s_add_u32 s55, s55, s59\n"
"v_mfma_f32_16x16x32_bf16 v[56:59], a[244:247], a[164:167], v[56:59]\n"
"s_add_u32 s41, s55, s57\n"
"v_mfma_f32_16x16x32_bf16 v[52:55], a[232:235], a[160:163], v[52:55]\n"
"s_and_b32 s41, s41, s58\n"
"ds_read_b64_tr_b16 a[218:219], v245 offset:1536\n"
"v_mfma_f32_16x16x32_bf16 v[52:55], a[236:239], a[164:167], v[52:55]\n"
"s_andn2_b32 s56, s55, s58\n"
"ds_read_b64_tr_b16 a[200:201], v245 offset:0\n"
"v_lshlrev_b32 v228, 4, v228\n"
"v_mfma_f32_16x16x32_bf16 v[48:51], a[224:227], a[160:163], v[48:51]\n"
"s_or_b32 s56, s56, s41\n"
"ds_read_b64_tr_b16 a[212:213], v244 offset:9216\n"
"v_mfma_f32_16x16x32_bf16 v[48:51], a[228:231], a[164:167], v[48:51]\n"
"ds_read_b64_tr_b16 a[198:199], v244 offset:8704\n"
"v_mfma_f32_16x16x32_bf16 v[64:67], a[224:227], a[168:171], v[64:67]\n"
"s_add_u32 m0, s44, 16384\n"
"ds_read_b64_tr_b16 a[214:215], v244 offset:9728\n"
"v_mfma_f32_16x16x32_bf16 v[64:67], a[228:231], a[172:175], v[64:67]\n"
"buffer_load_dwordx4 v240, s[16:19], s56 offen nt lds\n"
"v_mfma_f32_16x16x32_bf16 v[68:71], a[232:235], a[168:171], v[68:71]\n"
"v_mfma_f32_16x16x32_bf16 v[68:71], a[236:239], a[172:175], v[68:71]\n"
"v_mfma_f32_16x16x32_bf16 v[72:75], a[240:243], a[168:171], v[72:75]\n"
"ds_read_b64_tr_b16 a[216:217], v245 offset:1024\n"
"v_and_b32 v254, 0x20, v242\n"
"v_mfma_f32_16x16x32_bf16 v[72:75], a[244:247], a[172:175], v[72:75]\n"
"ds_read_b64_tr_b16 a[220:221], v245 offset:9216\n"
"v_mfma_f32_16x16x32_bf16 v[76:79], a[248:251], a[168:171], v[76:79]\n"
"s_add_u32 s40, s56, s52\n"
"ds_read_b64_tr_b16 a[206:207], v245 offset:8704\n"
"v_mfma_f32_16x16x32_bf16 v[76:79], a[252:255], a[172:175], v[76:79]\n"
"buffer_load_dwordx4 v240, s[16:19], s40 offen offset:1024 nt lds\n"
"v_mfma_f32_16x16x32_bf16 v[92:95], a[248:251], a[176:179], v[92:95]\n"
"v_xor_b32 v228, v228, v229\n"
"v_mfma_f32_16x16x32_bf16 v[92:95], a[252:255], a[180:183], v[92:95]\n"
"v_and_b32 v229, 0x70, v242\n"
"v_mfma_f32_16x16x32_bf16 v[88:91], a[240:243], a[176:179], v[88:91]\n"
"ds_read_b128 a[172:175], v251 offset:0\n"
"v_mfma_f32_16x16x32_bf16 v[88:91], a[244:247], a[180:183], v[88:91]\n"
"v_lshl_add_u32 v228, v229, 3, v228\n"
"v_mfma_f32_16x16x32_bf16 v[84:87], a[232:235], a[176:179], v[84:87]\n"
"s_add_u32 s40, s56, s53\n"
"ds_read_b128 a[168:171], v250 offset:0\n"
"v_add_u32 v254, -1, v254\n"
"v_mfma_f32_16x16x32_bf16 v[84:87], a[236:239], a[180:183], v[84:87]\n"
"buffer_load_dwordx4 v240, s[16:19], s40 offen offset:2048 nt lds\n"
"v_mfma_f32_16x16x32_bf16 v[80:83], a[224:227], a[176:179], v[80:83]\n"
"v_mfma_f32_16x16x32_bf16 v[80:83], a[228:231], a[180:183], v[80:83]\n"
"v_mfma_f32_16x16x32_bf16 v[96:99], a[224:227], a[184:187], v[96:99]\n"
"ds_read_b128 a[152:155], v246 offset:4096\n"
"v_mfma_f32_16x16x32_bf16 v[96:99], a[228:231], a[188:191], v[96:99]\n"
"v_mfma_f32_16x16x32_bf16 v[100:103], a[232:235], a[184:187], v[100:103]\n"
"s_add_u32 s40, s56, s54\n"
"ds_read_b128 a[144:147], v246 offset:2048\n"
"v_and_b32 v241, 0xaaaaaaaa, v241\n"
"v_mfma_f32_16x16x32_bf16 v[100:103], a[236:239], a[188:191], v[100:103]\n"
"buffer_load_dwordx4 v240, s[16:19], s40 offen offset:3072 nt lds\n"
"v_mfma_f32_16x16x32_bf16 v[104:107], a[240:243], a[184:187], v[104:107]\n"
"v_mfma_f32_16x16x32_bf16 v[104:107], a[244:247], a[188:191], v[104:107]\n"
"v_mfma_f32_16x16x32_bf16 v[108:111], a[248:251], a[184:187], v[108:111]\n"
"ds_read_b128 a[180:183], v251 offset:2048\n"
"v_mfma_f32_16x16x32_bf16 v[108:111], a[252:255], a[188:191], v[108:111]\n"
"s_waitcnt lgkmcnt(3)\n"
"v_and_b32 v229, 0x80, v242\n"
"v_mfma_f32_16x16x32_bf16 v[112:115], a[192:195], a[136:139], v[112:115]\n"
"ds_read_b128 a[176:179], v250 offset:2048\n"
"v_and_b32 v249, 0xf0, v242\n"
"v_mfma_f32_16x16x32_bf16 v[112:115], a[196:199], a[140:143], v[112:115]\n"
"v_xor_b32 v253, s74, v253\n"
"v_bfe_u32 v255, v242, 9, 1\n"
"v_mfma_f32_16x16x32_bf16 v[116:119], a[200:203], a[136:139], v[116:119]\n"
"s_waitcnt vmcnt(13)\n"
"ds_read_b128 a[184:187], v250 offset:4096\n"
"v_lshl_add_u32 v255, v255, 8, v249\n"
"v_mfma_f32_16x16x32_bf16 v[116:119], a[204:207], a[140:143], v[116:119]\n"
"s_barrier\n"
"v_mfma_f32_16x16x32_bf16 v[120:123], a[208:211], a[136:139], v[120:123]\n"
"s_add_u32 m0, s44, 81920\n"
"ds_read_b64_tr_b16 a[238:239], v245 offset:25088\n"
"v_mfma_f32_16x16x32_bf16 v[120:123], a[212:215], a[140:143], v[120:123]\n"
"buffer_load_dwordx4 v240, s[20:23], s56 offen nt lds\n"
"v_mfma_f32_16x16x32_bf16 v[124:127], a[216:219], a[136:139], v[124:127]\n"
"v_ashrrev_i32 v254, 31, v254\n"
"v_bfe_u32 v248, v242, 9, 1\n"
"v_mfma_f32_16x16x32_bf16 v[124:127], a[220:223], a[140:143], v[124:127]\n"
"v_lshl_add_u32 v228, v229, 4, v228\n"
"v_bfe_u32 v249, v242, 8, 1\n"
"v_mfma_f32_16x16x32_bf16 v[188:191], a[216:219], a[168:171], v[188:191]\n"
"ds_read_b64_tr_b16 a[248:249], v245 offset:17408\n"
"v_or_b32 v241, 0x44444444, v241\n"
"v_mfma_f32_16x16x32_bf16 v[188:191], a[220:223], a[172:175], v[188:191]\n"
"ds_read_b64_tr_b16 a[242:243], v244 offset:17920\n"
"v_mfma_f32_16x16x32_bf16 v[184:187], a[208:211], a[168:171], v[184:187]\n"
"v_lshl_add_u32 v255, v249, 3, v255\n"
"v_xor_b32_e64 v249, s42, 1\n"
"v_mfma_f32_16x16x32_bf16 v[184:187], a[212:215], a[172:175], v[184:187]\n"
"ds_read_b128 a[188:191], v251 offset:4096\n"
"v_mov_b32 v229, s43\n"
"v_mfma_f32_16x16x32_bf16 v[180:183], a[200:203], a[168:171], v[180:183]\n"
"v_mfma_f32_16x16x32_bf16 v[180:183], a[204:207], a[172:175], v[180:183]\n"
"ds_read_b128 a[148:151], v247 offset:2048\n"
"v_mfma_f32_16x16x32_bf16 v[176:179], a[192:195], a[168:171], v[176:179]\n"
"v_lshl_add_u32 v255, v249, 12, v255\n"
"v_mfma_f32_16x16x32_bf16 v[176:179], a[196:199], a[172:175], v[176:179]\n"
"s_waitcnt lgkmcnt(4)\n"
"ds_read_b64_tr_b16 a[224:225], v244 offset:16384\n"
"v_xor_b32 v252, s74, v252\n"
"v_mfma_f32_16x16x32_bf16 v[192:195], a[192:195], a[176:179], v[192:195]\n"
"ds_read_b64_tr_b16 a[250:251], v245 offset:17920\n"
"v_mul_u32_u24 v229, 0x81, v229\n"
"v_mfma_f32_16x16x32_bf16 v[192:195], a[196:199], a[180:183], v[192:195]\n"
"ds_read_b64_tr_b16 a[236:237], v245 offset:24576\n"
"v_mfma_f32_16x16x32_bf16 v[196:199], a[200:203], a[176:179], v[196:199]\n"
"s_add_u32 s40, s56, s52\n"
"ds_read_b64_tr_b16 a[226:227], v244 offset:16896\n"
"v_mfma_f32_16x16x32_bf16 v[196:199], a[204:207], a[180:183], v[196:199]\n"
"buffer_load_dwordx4 v240, s[20:23], s40 offen offset:1024 nt lds\n"
"v_mfma_f32_16x16x32_bf16 v[200:203], a[208:211], a[176:179], v[200:203]\n"
"v_and_b32 v249, 0xf0, v242\n"
"v_mfma_f32_16x16x32_bf16 v[200:203], a[212:215], a[180:183], v[200:203]\n"
"v_mfma_f32_16x16x32_bf16 v[204:207], a[216:219], a[176:179], v[204:207]\n"
"ds_read_b128 a[164:167], v253 offset:0\n"
"v_mfma_f32_16x16x32_bf16 v[204:207], a[220:223], a[180:183], v[204:207]\n"
"v_mfma_f32_16x16x32_bf16 v[220:223], a[216:219], a[184:187], v[220:223]\n"
"s_waitcnt lgkmcnt(3)\n"
"ds_read_b64_tr_b16 a[234:235], v245 offset:16896\n"
"v_lshl_add_u32 v248, v248, 8, v249\n"
"v_mfma_f32_16x16x32_bf16 v[220:223], a[220:223], a[188:191], v[220:223]\n"
"v_mfma_f32_16x16x32_bf16 v[216:219], a[208:211], a[184:187], v[216:219]\n"
"ds_read_b128 a[160:163], v252 offset:0\n"
"v_xor_b32 v228, v228, v229\n"
"v_mfma_f32_16x16x32_bf16 v[216:219], a[212:215], a[188:191], v[216:219]\n"
"v_pk_fma_f32 v[176:177], v[176:177], s[70:71], s[70:71] op_sel:[0,0,1] op_sel_hi:[1,0,1]\nv_pk_fma_f32 v[178:179], v[178:179], s[70:71], s[70:71] op_sel:[0,0,1] op_sel_hi:[1,0,1]\nv_pk_fma_f32 v[180:181], v[180:181], s[70:71], s[70:71] op_sel:[0,0,1] op_sel_hi:[1,0,1]\nv_pk_fma_f32 v[182:183], v[182:183], s[70:71], s[70:71] op_sel:[0,0,1] op_sel_hi:[1,0,1]\nv_pk_fma_f32 v[184:185], v[184:185], s[70:71], s[70:71] op_sel:[0,0,1] op_sel_hi:[1,0,1]\nv_pk_fma_f32 v[186:187], v[186:187], s[70:71], s[70:71] op_sel:[0,0,1] op_sel_hi:[1,0,1]\nv_pk_fma_f32 v[188:189], v[188:189], s[70:71], s[70:71] op_sel:[0,0,1] op_sel_hi:[1,0,1]\nv_pk_fma_f32 v[190:191], v[190:191], s[70:71], s[70:71] op_sel:[0,0,1] op_sel_hi:[1,0,1]\nv_pk_fma_f32 v[192:193], v[192:193], s[70:71], s[70:71] op_sel:[0,0,1] op_sel_hi:[1,0,1]\nv_pk_fma_f32 v[194:195], v[194:195], s[70:71], s[70:71] op_sel:[0,0,1] op_sel_hi:[1,0,1]\nv_pk_fma_f32 v[196:197], v[196:197], s[70:71], s[70:71] op_sel:[0,0,1] op_sel_hi:[1,0,1]\nv_pk_fma_f32 v[198:199], v[198:199], s[70:71], s[70:71] op_sel:[0,0,1] op_sel_hi:[1,0,1]\nv_pk_fma_f32 v[200:201], v[200:201], s[70:71], s[70:71] op_sel:[0,0,1] op_sel_hi:[1,0,1]\nv_pk_fma_f32 v[202:203], v[202:203], s[70:71], s[70:71] op_sel:[0,0,1] op_sel_hi:[1,0,1]\nv_pk_fma_f32 v[204:205], v[204:205], s[70:71], s[70:71] op_sel:[0,0,1] op_sel_hi:[1,0,1]\nv_pk_fma_f32 v[206:207], v[206:207], s[70:71], s[70:71] op_sel:[0,0,1] op_sel_hi:[1,0,1]\n"
"v_mfma_f32_16x16x32_bf16 v[212:215], a[200:203], a[184:187], v[212:215]\n"
"ds_read_b64_tr_b16 a[252:253], v245 offset:25600\n"
"v_cvt_pk_bf16_f32 v176, v176, v177\n"
"v_mfma_f32_16x16x32_bf16 v[212:215], a[204:207], a[188:191], v[212:215]\n"
"v_cvt_pk_bf16_f32 v177, v178, v179\n"
"v_cvt_pk_bf16_f32 v192, v192, v193\n"
"v_mfma_f32_16x16x32_bf16 v[208:211], a[192:195], a[184:187], v[208:211]\n"
"s_add_u32 s40, s56, s53\n"
"ds_read_b128 a[156:159], v247 offset:4096\n"
"v_cvt_pk_bf16_f32 v184, v184, v185\n"
"v_mfma_f32_16x16x32_bf16 v[208:211], a[196:199], a[188:191], v[208:211]\n"
"s_waitcnt lgkmcnt(2)\n"
"buffer_load_dwordx4 v240, s[20:23], s40 offen offset:2048 nt lds\n"
"v_mfma_f32_16x16x32_bf16 v[160:163], a[192:195], a[160:163], v[160:163]\n"
"v_mfma_f32_16x16x32_bf16 v[160:163], a[196:199], a[164:167], v[160:163]\n"
"v_xor_b32 v230, 0x2000, v228\n"
"v_mfma_f32_16x16x32_bf16 v[164:167], a[200:203], a[160:163], v[164:167]\n"
"ds_read_b64_tr_b16 a[244:245], v244 offset:25600\n"
"v_cvt_pk_bf16_f32 v185, v186, v187\n"
"v_mfma_f32_16x16x32_bf16 v[164:167], a[204:207], a[164:167], v[164:167]\n"
"v_cvt_pk_bf16_f32 v193, v194, v195\n"
"v_cvt_pk_bf16_f32 v200, v200, v201\n"
"v_mfma_f32_16x16x32_bf16 v[168:171], a[208:211], a[160:163], v[168:171]\n"
"ds_read_b64_tr_b16 a[240:241], v244 offset:17408\n"
"v_cvt_pk_bf16_f32 v195, v198, v199\n"
"v_mfma_f32_16x16x32_bf16 v[168:171], a[212:215], a[164:167], v[168:171]\n"
"v_cvt_pk_bf16_f32 v194, v196, v197\n"
"v_xor_b32 v198, 0x400, v243\n"
"v_mfma_f32_16x16x32_bf16 v[172:175], a[216:219], a[160:163], v[172:175]\n"
"ds_read_b64_tr_b16 a[254:255], v245 offset:26112\n"
"v_mfma_f32_16x16x32_bf16 v[172:175], a[220:223], a[164:167], v[172:175]\n"
"v_cvt_pk_bf16_f32 v201, v202, v203\n"
"v_and_b32 v254, 0x3f803f80, v254\n"
"v_mfma_f32_16x16x32_bf16 v[140:143], a[216:219], a[144:147], v[140:143]\n"
"ds_read_b64_tr_b16 a[246:247], v244 offset:26112\n"
"v_bfe_u32 v196, v242, 8, 2\n"
"v_mfma_f32_16x16x32_bf16 v[140:143], a[220:223], a[148:151], v[140:143]\n"
"v_xor_b32 v229, 64, v228\n"
"v_cvt_pk_bf16_f32 v186, v188, v189\n"
"v_mfma_f32_16x16x32_bf16 v[136:139], a[208:211], a[144:147], v[136:139]\n"
"ds_read_b64_tr_b16 a[228:229], v244 offset:24576\n"
"v_bfe_u32 v249, v242, 8, 1\n"
"v_mfma_f32_16x16x32_bf16 v[136:139], a[212:215], a[148:151], v[136:139]\n"
"v_pk_fma_f32 v[208:209], v[208:209], s[70:71], s[70:71] op_sel:[0,0,1] op_sel_hi:[1,0,1]\nv_pk_fma_f32 v[210:211], v[210:211], s[70:71], s[70:71] op_sel:[0,0,1] op_sel_hi:[1,0,1]\nv_pk_fma_f32 v[212:213], v[212:213], s[70:71], s[70:71] op_sel:[0,0,1] op_sel_hi:[1,0,1]\nv_pk_fma_f32 v[214:215], v[214:215], s[70:71], s[70:71] op_sel:[0,0,1] op_sel_hi:[1,0,1]\nv_pk_fma_f32 v[216:217], v[216:217], s[70:71], s[70:71] op_sel:[0,0,1] op_sel_hi:[1,0,1]\nv_pk_fma_f32 v[218:219], v[218:219], s[70:71], s[70:71] op_sel:[0,0,1] op_sel_hi:[1,0,1]\nv_pk_fma_f32 v[220:221], v[220:221], s[70:71], s[70:71] op_sel:[0,0,1] op_sel_hi:[1,0,1]\nv_pk_fma_f32 v[222:223], v[222:223], s[70:71], s[70:71] op_sel:[0,0,1] op_sel_hi:[1,0,1]\nv_pk_fma_f32 v[160:161], v[160:161], s[70:71], s[70:71] op_sel:[0,0,1] op_sel_hi:[1,0,1]\nv_pk_fma_f32 v[162:163], v[162:163], s[70:71], s[70:71] op_sel:[0,0,1] op_sel_hi:[1,0,1]\nv_pk_fma_f32 v[164:165], v[164:165], s[70:71], s[70:71] op_sel:[0,0,1] op_sel_hi:[1,0,1]\nv_pk_fma_f32 v[166:167], v[166:167], s[70:71], s[70:71] op_sel:[0,0,1] op_sel_hi:[1,0,1]\nv_pk_fma_f32 v[168:169], v[168:169], s[70:71], s[70:71] op_sel:[0,0,1] op_sel_hi:[1,0,1]\nv_pk_fma_f32 v[170:171], v[170:171], s[70:71], s[70:71] op_sel:[0,0,1] op_sel_hi:[1,0,1]\nv_pk_fma_f32 v[172:173], v[172:173], s[70:71], s[70:71] op_sel:[0,0,1] op_sel_hi:[1,0,1]\nv_pk_fma_f32 v[174:175], v[174:175], s[70:71], s[70:71] op_sel:[0,0,1] op_sel_hi:[1,0,1]\n"
"v_mfma_f32_16x16x32_bf16 v[132:135], a[200:203], a[144:147], v[132:135]\n"
"s_add_u32 s40, s56, s54\n"
"v_cvt_pk_bf16_f32 v168, v168, v169\n"
"v_cvt_pk_bf16_f32 v169, v170, v171\n"
"v_mfma_f32_16x16x32_bf16 v[132:135], a[204:207], a[148:151], v[132:135]\n"
"buffer_load_dwordx4 v240, s[20:23], s40 offen offset:3072 nt lds\n"
"v_mfma_f32_16x16x32_bf16 v[128:131], a[192:195], a[144:147], v[128:131]\n"
"s_add_u32 s55, s55, s59\n"
"v_cvt_pk_bf16_f32 v216, v216, v217\n"
"v_cvt_pk_bf16_f32 v171, v174, v175\n"
"v_mfma_f32_16x16x32_bf16 v[128:131], a[196:199], a[148:151], v[128:131]\n"
"s_add_u32 s41, s55, s57\n"
"v_cvt_pk_bf16_f32 v179, v182, v183\n"
"v_cvt_pk_bf16_f32 v160, v160, v161\n"
"v_mfma_f32_16x16x32_bf16 v[144:147], a[192:195], a[152:155], v[144:147]\n"
"s_waitcnt lgkmcnt(1)\n"
"s_and_b32 s41, s41, s58\n"
"ds_read_b64_tr_b16 a[230:231], v244 offset:25088\n"
"v_cvt_pk_bf16_f32 v161, v162, v163\n"
"v_mfma_f32_16x16x32_bf16 v[144:147], a[196:199], a[156:159], v[144:147]\n"
"s_andn2_b32 s56, s55, s58\n"
"v_cvt_pk_bf16_f32 v202, v204, v205\n"
"v_cvt_pk_bf16_f32 v217, v218, v219\n"
"v_mfma_f32_16x16x32_bf16 v[148:151], a[200:203], a[152:155], v[148:151]\n"
"s_or_b32 s56, s56, s41\n"
"ds_read_b64_tr_b16 a[232:233], v245 offset:16384\n"
"v_cvt_pk_bf16_f32 v219, v222, v223\n"
"v_mfma_f32_16x16x32_bf16 v[148:151], a[204:207], a[156:159], v[148:151]\n"
"v_pk_fma_f32 v[112:113], v[112:113], s[70:71], s[70:71] op_sel:[0,0,1] op_sel_hi:[1,0,1]\nv_pk_fma_f32 v[114:115], v[114:115], s[70:71], s[70:71] op_sel:[0,0,1] op_sel_hi:[1,0,1]\nv_pk_fma_f32 v[116:117], v[116:117], s[70:71], s[70:71] op_sel:[0,0,1] op_sel_hi:[1,0,1]\nv_pk_fma_f32 v[118:119], v[118:119], s[70:71], s[70:71] op_sel:[0,0,1] op_sel_hi:[1,0,1]\nv_pk_fma_f32 v[120:121], v[120:121], s[70:71], s[70:71] op_sel:[0,0,1] op_sel_hi:[1,0,1]\nv_pk_fma_f32 v[122:123], v[122:123], s[70:71], s[70:71] op_sel:[0,0,1] op_sel_hi:[1,0,1]\nv_pk_fma_f32 v[124:125], v[124:125], s[70:71], s[70:71] op_sel:[0,0,1] op_sel_hi:[1,0,1]\nv_pk_fma_f32 v[126:127], v[126:127], s[70:71], s[70:71] op_sel:[0,0,1] op_sel_hi:[1,0,1]\nv_pk_fma_f32 v[128:129], v[128:129], s[70:71], s[70:71] op_sel:[0,0,1] op_sel_hi:[1,0,1]\nv_pk_fma_f32 v[130:131], v[130:131], s[70:71], s[70:71] op_sel:[0,0,1] op_sel_hi:[1,0,1]\nv_pk_fma_f32 v[132:133], v[132:133], s[70:71], s[70:71] op_sel:[0,0,1] op_sel_hi:[1,0,1]\nv_pk_fma_f32 v[134:135], v[134:135], s[70:71], s[70:71] op_sel:[0,0,1] op_sel_hi:[1,0,1]\nv_pk_fma_f32 v[136:137], v[136:137], s[70:71], s[70:71] op_sel:[0,0,1] op_sel_hi:[1,0,1]\nv_pk_fma_f32 v[138:139], v[138:139], s[70:71], s[70:71] op_sel:[0,0,1] op_sel_hi:[1,0,1]\nv_pk_fma_f32 v[140:141], v[140:141], s[70:71], s[70:71] op_sel:[0,0,1] op_sel_hi:[1,0,1]\nv_pk_fma_f32 v[142:143], v[142:143], s[70:71], s[70:71] op_sel:[0,0,1] op_sel_hi:[1,0,1]\n"
"v_mfma_f32_16x16x32_bf16 v[152:155], a[208:211], a[152:155], v[152:155]\n"
"v_cvt_pk_bf16_f32 v163, v166, v167\n"
"v_cvt_pk_bf16_f32 v120, v120, v121\n"
"v_mfma_f32_16x16x32_bf16 v[152:155], a[212:215], a[156:159], v[152:155]\n"
"v_cvt_pk_bf16_f32 v208, v208, v209\n"
"v_cvt_pk_bf16_f32 v218, v220, v221\n"
"v_mfma_f32_16x16x32_bf16 v[156:159], a[216:219], a[152:155], v[156:159]\n"
"v_cvt_pk_bf16_f32 v162, v164, v165\n"
"v_cvt_pk_bf16_f32 v170, v172, v173\n"
"v_mfma_f32_16x16x32_bf16 v[156:159], a[220:223], a[156:159], v[156:159]\n"
"v_permlane16_swap_b32 v169, v171\n"
"v_mfma_f32_16x16x32_bf16 v[96:99], a[224:227], a[184:187], v[96:99]\n"
"s_waitcnt lgkmcnt(1)\n"
"s_add_u32 m0, s44, 32768\n"
"v_cvt_pk_bf16_f32 v209, v210, v211\n"
"v_cvt_pk_bf16_f32 v128, v128, v129\n"
"v_mfma_f32_16x16x32_bf16 v[96:99], a[228:231], a[188:191], v[96:99]\n"
"s_waitcnt lgkmcnt(0)\n"
"buffer_load_dwordx4 v240, s[16:19], s56 offen nt lds\n"
"v_mfma_f32_16x16x32_bf16 v[100:103], a[232:235], a[184:187], v[100:103]\n"
"s_waitcnt vmcnt(14)\n"
"v_mov_b32 v205, 0\n"
"v_mfma_f32_16x16x32_bf16 v[100:103], a[236:239], a[188:191], v[100:103]\n"
"s_barrier\n"
"v_mfma_f32_16x16x32_bf16 v[104:107], a[240:243], a[184:187], v[104:107]\n"
"v_permlane16_swap_b32 v160, v162\n"
"v_mfma_f32_16x16x32_bf16 v[104:107], a[244:247], a[188:191], v[104:107]\n"
"s_mov_b32 s74, 0x41400000\n"
"ds_read_b128 a[204:207], v198 offset:2048\n"
"v_xor_b32 v231, 0x2000, v229\n"
"v_mfma_f32_16x16x32_bf16 v[108:111], a[248:251], a[184:187], v[108:111]\n"
"v_permlane16_swap_b32 v168, v170\n"
"v_mfma_f32_16x16x32_bf16 v[108:111], a[252:255], a[188:191], v[108:111]\n"
"ds_write2st64_b64 v255, v[192:193], v[194:195] offset0:100 offset1:101\n"
"v_mfma_f32_16x16x32_bf16 v[92:95], a[248:251], a[176:179], v[92:95]\n"
"v_cvt_pk_bf16_f32 v129, v130, v131\n"
"v_cvt_pk_bf16_f32 v136, v136, v137\n"
"v_mfma_f32_16x16x32_bf16 v[92:95], a[252:255], a[180:183], v[92:95]\n"
"v_pk_fma_f32 v[144:145], v[144:145], s[70:71], s[70:71] op_sel:[0,0,1] op_sel_hi:[1,0,1]\nv_pk_fma_f32 v[146:147], v[146:147], s[70:71], s[70:71] op_sel:[0,0,1] op_sel_hi:[1,0,1]\nv_pk_fma_f32 v[148:149], v[148:149], s[70:71], s[70:71] op_sel:[0,0,1] op_sel_hi:[1,0,1]\nv_pk_fma_f32 v[150:151], v[150:151], s[70:71], s[70:71] op_sel:[0,0,1] op_sel_hi:[1,0,1]\nv_pk_fma_f32 v[152:153], v[152:153], s[70:71], s[70:71] op_sel:[0,0,1] op_sel_hi:[1,0,1]\nv_pk_fma_f32 v[154:155], v[154:155], s[70:71], s[70:71] op_sel:[0,0,1] op_sel_hi:[1,0,1]\nv_pk_fma_f32 v[156:157], v[156:157], s[70:71], s[70:71] op_sel:[0,0,1] op_sel_hi:[1,0,1]\nv_pk_fma_f32 v[158:159], v[158:159], s[70:71], s[70:71] op_sel:[0,0,1] op_sel_hi:[1,0,1]\n"
"v_mfma_f32_16x16x32_bf16 v[88:91], a[240:243], a[176:179], v[88:91]\n"
"ds_read_b128 a[192:195], v243 offset:0\n"
"v_cvt_pk_bf16_f32 v187, v190, v191\n"
"v_mfma_f32_16x16x32_bf16 v[88:91], a[244:247], a[180:183], v[88:91]\n"
"ds_write2st64_b64 v255, v[184:185], v[186:187] offset0:98 offset1:99\n"
"v_cvt_pk_bf16_f32 v178, v180, v181\n"
"v_mfma_f32_16x16x32_bf16 v[84:87], a[232:235], a[176:179], v[84:87]\n"
"v_cvt_pk_bf16_f32 v210, v212, v213\n"
"v_lshl_add_u32 v196, v196, 4, s43\n"
"v_mfma_f32_16x16x32_bf16 v[84:87], a[236:239], a[180:183], v[84:87]\n"
"ds_write2st64_b64 v255, v[216:217], v[218:219] offset0:194 offset1:195\n"
"v_mfma_f32_16x16x32_bf16 v[80:83], a[224:227], a[176:179], v[80:83]\n"
"s_add_u32 s40, s56, s52\n"
"v_permlane16_swap_b32 v161, v163\n"
"v_mfma_f32_16x16x32_bf16 v[80:83], a[228:231], a[180:183], v[80:83]\n"
"buffer_load_dwordx4 v240, s[16:19], s40 offen offset:1024 nt lds\n"
"v_mfma_f32_16x16x32_bf16 v[64:67], a[224:227], a[168:171], v[64:67]\n"
"v_cvt_pk_bf16_f32 v211, v214, v215\n"
"v_cvt_pk_bf16_f32 v137, v138, v139\n"
"v_mfma_f32_16x16x32_bf16 v[64:67], a[228:231], a[172:175], v[64:67]\n"
"v_cvt_pk_bf16_f32 v131, v134, v135\n"
"v_cvt_pk_bf16_f32 v144, v144, v145\n"
"v_mfma_f32_16x16x32_bf16 v[68:71], a[232:235], a[168:171], v[68:71]\n"
"v_xor_b32 v197, 64, v196\n"
"v_cvt_pk_bf16_f32 v130, v132, v133\n"
"v_mfma_f32_16x16x32_bf16 v[68:71], a[236:239], a[172:175], v[68:71]\n"
"v_pk_fma_f32 v[96:97], v[96:97], s[70:71], s[70:71] op_sel:[0,0,1] op_sel_hi:[1,0,1]\nv_pk_fma_f32 v[98:99], v[98:99], s[70:71], s[70:71] op_sel:[0,0,1] op_sel_hi:[1,0,1]\nv_pk_fma_f32 v[100:101], v[100:101], s[70:71], s[70:71] op_sel:[0,0,1] op_sel_hi:[1,0,1]\nv_pk_fma_f32 v[102:103], v[102:103], s[70:71], s[70:71] op_sel:[0,0,1] op_sel_hi:[1,0,1]\nv_pk_fma_f32 v[104:105], v[104:105], s[70:71], s[70:71] op_sel:[0,0,1] op_sel_hi:[1,0,1]\nv_pk_fma_f32 v[106:107], v[106:107], s[70:71], s[70:71] op_sel:[0,0,1] op_sel_hi:[1,0,1]\nv_pk_fma_f32 v[108:109], v[108:109], s[70:71], s[70:71] op_sel:[0,0,1] op_sel_hi:[1,0,1]\nv_pk_fma_f32 v[110:111], v[110:111], s[70:71], s[70:71] op_sel:[0,0,1] op_sel_hi:[1,0,1]\nv_pk_fma_f32 v[80:81], v[80:81], s[70:71], s[70:71] op_sel:[0,0,1] op_sel_hi:[1,0,1]\nv_pk_fma_f32 v[82:83], v[82:83], s[70:71], s[70:71] op_sel:[0,0,1] op_sel_hi:[1,0,1]\nv_pk_fma_f32 v[84:85], v[84:85], s[70:71], s[70:71] op_sel:[0,0,1] op_sel_hi:[1,0,1]\nv_pk_fma_f32 v[86:87], v[86:87], s[70:71], s[70:71] op_sel:[0,0,1] op_sel_hi:[1,0,1]\nv_pk_fma_f32 v[88:89], v[88:89], s[70:71], s[70:71] op_sel:[0,0,1] op_sel_hi:[1,0,1]\nv_pk_fma_f32 v[90:91], v[90:91], s[70:71], s[70:71] op_sel:[0,0,1] op_sel_hi:[1,0,1]\nv_pk_fma_f32 v[92:93], v[92:93], s[70:71], s[70:71] op_sel:[0,0,1] op_sel_hi:[1,0,1]\nv_pk_fma_f32 v[94:95], v[94:95], s[70:71], s[70:71] op_sel:[0,0,1] op_sel_hi:[1,0,1]\n"
"v_mfma_f32_16x16x32_bf16 v[72:75], a[240:243], a[168:171], v[72:75]\n"
"v_cvt_pk_bf16_f32 v152, v152, v153\n"
"v_cvt_pk_bf16_f32 v80, v80, v81\n"
"v_mfma_f32_16x16x32_bf16 v[72:75], a[244:247], a[172:175], v[72:75]\n"
"v_cvt_pk_bf16_f32 v153, v154, v155\n"
"v_cvt_pk_bf16_f32 v81, v82, v83\n"
"v_mfma_f32_16x16x32_bf16 v[76:79], a[248:251], a[168:171], v[76:79]\n"
"v_cvt_pk_bf16_f32 v139, v142, v143\n"
"v_cvt_pk_bf16_f32 v104, v104, v105\n"
"v_mfma_f32_16x16x32_bf16 v[76:79], a[252:255], a[172:175], v[76:79]\n"
"ds_read_b128 v[212:215], v197 offset:0\n"
"v_cvt_pk_bf16_f32 v96, v96, v97\n"
"v_mfma_f32_16x16x32_bf16 v[60:63], a[248:251], a[160:163], v[60:63]\n"
"v_cvt_pk_bf16_f32 v138, v140, v141\n"
"v_cvt_pk_bf16_f32 v145, v146, v147\n"
"v_mfma_f32_16x16x32_bf16 v[60:63], a[252:255], a[164:167], v[60:63]\n"
"ds_write_b128 v243, v[168:171] offset:10240\n"
"v_mfma_f32_16x16x32_bf16 v[56:59], a[240:243], a[160:163], v[56:59]\n"
"s_add_u32 s40, s56, s53\n"
"v_cvt_pk_bf16_f32 v121, v122, v123\n"
"v_cvt_pk_bf16_f32 v155, v158, v159\n"
"v_mfma_f32_16x16x32_bf16 v[56:59], a[244:247], a[164:167], v[56:59]\n"
"buffer_load_dwordx4 v240, s[16:19], s40 offen offset:2048 nt lds\n"
"v_mfma_f32_16x16x32_bf16 v[52:55], a[232:235], a[160:163], v[52:55]\n"
"v_permlane16_swap_b32 v137, v139\n"
"v_mfma_f32_16x16x32_bf16 v[52:55], a[236:239], a[164:167], v[52:55]\n"
"v_permlane16_swap_b32 v128, v130\n"
"v_mfma_f32_16x16x32_bf16 v[48:51], a[224:227], a[160:163], v[48:51]\n"
"v_cvt_pk_bf16_f32 v122, v124, v125\n"
"v_cvt_pk_bf16_f32 v147, v150, v151\n"
"v_mfma_f32_16x16x32_bf16 v[48:51], a[228:231], a[164:167], v[48:51]\n"
"v_cvt_pk_bf16_f32 v83, v86, v87\n"
"v_lshl_add_u32 v248, v249, 9, v248\n"
"v_mfma_f32_16x16x32_bf16 v[0:3], a[224:227], a[136:139], v[0:3]\n"
"ds_read_b128 v[216:219], v196 offset:1024\n"
"v_cvt_pk_bf16_f32 v154, v156, v157\n"
"v_mfma_f32_16x16x32_bf16 v[0:3], a[228:231], a[140:143], v[0:3]\n"
"ds_write_b128 v243, v[160:163] offset:8192\n"
"v_mfma_f32_16x16x32_bf16 v[4:7], a[232:235], a[136:139], v[4:7]\n"
"v_permlane16_swap_b32 v136, v138\n"
"v_mfma_f32_16x16x32_bf16 v[4:7], a[236:239], a[140:143], v[4:7]\n"
"v_permlane16_swap_b32 v129, v131\n"
"v_mfma_f32_16x16x32_bf16 v[8:11], a[240:243], a[136:139], v[8:11]\n"
"ds_read_b128 a[200:203], v243 offset:2048\n"
"v_cvt_pk_bf16_f32 v105, v106, v107\n"
"v_mfma_f32_16x16x32_bf16 v[8:11], a[244:247], a[140:143], v[8:11]\n"
"v_accvgpr_write_b32 a120, v136\n"
"v_cvt_pk_bf16_f32 v146, v148, v149\n"
"v_mfma_f32_16x16x32_bf16 v[12:15], a[248:251], a[136:139], v[12:15]\n"
"v_permlane16_swap_b32 v152, v154\n"
"v_mfma_f32_16x16x32_bf16 v[12:15], a[252:255], a[140:143], v[12:15]\n"
"ds_write2st64_b64 v255, v[208:209], v[210:211] offset0:192 offset1:193\n"
"v_pk_fma_f32 v[64:65], v[64:65], s[70:71], s[70:71] op_sel:[0,0,1] op_sel_hi:[1,0,1]\nv_pk_fma_f32 v[66:67], v[66:67], s[70:71], s[70:71] op_sel:[0,0,1] op_sel_hi:[1,0,1]\nv_pk_fma_f32 v[68:69], v[68:69], s[70:71], s[70:71] op_sel:[0,0,1] op_sel_hi:[1,0,1]\nv_pk_fma_f32 v[70:71], v[70:71], s[70:71], s[70:71] op_sel:[0,0,1] op_sel_hi:[1,0,1]\nv_pk_fma_f32 v[72:73], v[72:73], s[70:71], s[70:71] op_sel:[0,0,1] op_sel_hi:[1,0,1]\nv_pk_fma_f32 v[74:75], v[74:75], s[70:71], s[70:71] op_sel:[0,0,1] op_sel_hi:[1,0,1]\nv_pk_fma_f32 v[76:77], v[76:77], s[70:71], s[70:71] op_sel:[0,0,1] op_sel_hi:[1,0,1]\nv_pk_fma_f32 v[78:79], v[78:79], s[70:71], s[70:71] op_sel:[0,0,1] op_sel_hi:[1,0,1]\nv_pk_fma_f32 v[48:49], v[48:49], s[70:71], s[70:71] op_sel:[0,0,1] op_sel_hi:[1,0,1]\nv_pk_fma_f32 v[50:51], v[50:51], s[70:71], s[70:71] op_sel:[0,0,1] op_sel_hi:[1,0,1]\nv_pk_fma_f32 v[52:53], v[52:53], s[70:71], s[70:71] op_sel:[0,0,1] op_sel_hi:[1,0,1]\nv_pk_fma_f32 v[54:55], v[54:55], s[70:71], s[70:71] op_sel:[0,0,1] op_sel_hi:[1,0,1]\nv_pk_fma_f32 v[56:57], v[56:57], s[70:71], s[70:71] op_sel:[0,0,1] op_sel_hi:[1,0,1]\nv_pk_fma_f32 v[58:59], v[58:59], s[70:71], s[70:71] op_sel:[0,0,1] op_sel_hi:[1,0,1]\nv_pk_fma_f32 v[60:61], v[60:61], s[70:71], s[70:71] op_sel:[0,0,1] op_sel_hi:[1,0,1]\nv_pk_fma_f32 v[62:63], v[62:63], s[70:71], s[70:71] op_sel:[0,0,1] op_sel_hi:[1,0,1]\n"
"v_mfma_f32_16x16x32_bf16 v[28:31], a[248:251], a[144:147], v[28:31]\n"
"s_add_u32 s40, s56, s54\n"
"v_permlane16_swap_b32 v145, v147\n"
"v_mfma_f32_16x16x32_bf16 v[28:31], a[252:255], a[148:151], v[28:31]\n"
"buffer_load_dwordx4 v240, s[16:19], s40 offen offset:3072 nt lds\n"
"v_mfma_f32_16x16x32_bf16 v[24:27], a[240:243], a[144:147], v[24:27]\n"
"v_cvt_pk_bf16_f32 v203, v206, v207\n"
"v_cvt_pk_bf16_f32 v72, v72, v73\n"
"v_mfma_f32_16x16x32_bf16 v[24:27], a[244:247], a[148:151], v[24:27]\n"
"v_cvt_pk_bf16_f32 v97, v98, v99\n"
"v_cvt_pk_bf16_f32 v64, v64, v65\n"
"v_mfma_f32_16x16x32_bf16 v[20:23], a[232:235], a[144:147], v[20:23]\n"
"v_cvt_pk_bf16_f32 v106, v108, v109\n"
"v_cvt_pk_bf16_f32 v56, v56, v57\n"
"v_mfma_f32_16x16x32_bf16 v[20:23], a[236:239], a[148:151], v[20:23]\n"
"v_accvgpr_write_b32 a112, v128\n"
"v_accvgpr_write_b32 a131, v147\n"
"v_mfma_f32_16x16x32_bf16 v[16:19], a[224:227], a[144:147], v[16:19]\n"
"ds_read_b128 v[208:211], v196 offset:0\n"
"v_cvt_pk_bf16_f32 v88, v88, v89\n"
"v_mfma_f32_16x16x32_bf16 v[16:19], a[228:231], a[148:151], v[16:19]\n"
"v_accvgpr_write_b32 a113, v129\n"
"v_cvt_pk_bf16_f32 v82, v84, v85\n"
"v_mfma_f32_16x16x32_bf16 v[32:35], a[224:227], a[152:155], v[32:35]\n"
"v_accvgpr_write_b32 a114, v130\n"
"v_cvt_pk_bf16_f32 v57, v58, v59\n"
"v_mfma_f32_16x16x32_bf16 v[32:35], a[228:231], a[156:159], v[32:35]\n"
"v_cvt_pk_bf16_f32 v73, v74, v75\n"
"v_xor_b32_e64 v249, s42, 0\n"
"v_mfma_f32_16x16x32_bf16 v[36:39], a[232:235], a[152:155], v[36:39]\n"
"ds_read_b128 v[220:223], v197 offset:1024\n"
"v_lshl_add_u32 v248, v249, 12, v248\n"
"v_mfma_f32_16x16x32_bf16 v[36:39], a[236:239], a[156:159], v[36:39]\n"
"v_cvt_pk_bf16_f32 v89, v90, v91\n"
"v_cvt_pk_bf16_f32 v65, v66, v67\n"
"v_mfma_f32_16x16x32_bf16 v[40:43], a[240:243], a[152:155], v[40:43]\n"
"v_permlane16_swap_b32 v120, v122\n"
"v_mfma_f32_16x16x32_bf16 v[40:43], a[244:247], a[156:159], v[40:43]\n"
"ds_write2st64_b64 v255, v[200:201], v[202:203] offset0:102 offset1:103\n"
"v_pk_fma_f32 v[0:1], v[0:1], s[70:71], s[70:71] op_sel:[0,0,1] op_sel_hi:[1,0,1]\nv_pk_fma_f32 v[2:3], v[2:3], s[70:71], s[70:71] op_sel:[0,0,1] op_sel_hi:[1,0,1]\nv_pk_fma_f32 v[4:5], v[4:5], s[70:71], s[70:71] op_sel:[0,0,1] op_sel_hi:[1,0,1]\nv_pk_fma_f32 v[6:7], v[6:7], s[70:71], s[70:71] op_sel:[0,0,1] op_sel_hi:[1,0,1]\nv_pk_fma_f32 v[8:9], v[8:9], s[70:71], s[70:71] op_sel:[0,0,1] op_sel_hi:[1,0,1]\nv_pk_fma_f32 v[10:11], v[10:11], s[70:71], s[70:71] op_sel:[0,0,1] op_sel_hi:[1,0,1]\nv_pk_fma_f32 v[12:13], v[12:13], s[70:71], s[70:71] op_sel:[0,0,1] op_sel_hi:[1,0,1]\nv_pk_fma_f32 v[14:15], v[14:15], s[70:71], s[70:71] op_sel:[0,0,1] op_sel_hi:[1,0,1]\nv_pk_fma_f32 v[16:17], v[16:17], s[70:71], s[70:71] op_sel:[0,0,1] op_sel_hi:[1,0,1]\nv_pk_fma_f32 v[18:19], v[18:19], s[70:71], s[70:71] op_sel:[0,0,1] op_sel_hi:[1,0,1]\nv_pk_fma_f32 v[20:21], v[20:21], s[70:71], s[70:71] op_sel:[0,0,1] op_sel_hi:[1,0,1]\nv_pk_fma_f32 v[22:23], v[22:23], s[70:71], s[70:71] op_sel:[0,0,1] op_sel_hi:[1,0,1]\nv_pk_fma_f32 v[24:25], v[24:25], s[70:71], s[70:71] op_sel:[0,0,1] op_sel_hi:[1,0,1]\nv_pk_fma_f32 v[26:27], v[26:27], s[70:71], s[70:71] op_sel:[0,0,1] op_sel_hi:[1,0,1]\nv_pk_fma_f32 v[28:29], v[28:29], s[70:71], s[70:71] op_sel:[0,0,1] op_sel_hi:[1,0,1]\nv_pk_fma_f32 v[30:31], v[30:31], s[70:71], s[70:71] op_sel:[0,0,1] op_sel_hi:[1,0,1]\n"
"v_mfma_f32_16x16x32_bf16 v[44:47], a[248:251], a[152:155], v[44:47]\n"
"v_accvgpr_write_b32 a138, v154\n"
"v_cvt_pk_bf16_f32 v91, v94, v95\n"
"v_mfma_f32_16x16x32_bf16 v[44:47], a[252:255], a[156:159], v[44:47]\n"
"s_waitcnt lgkmcnt(2)\n"
"v_accvgpr_write_b32 a122, v138\n"
"v_accvgpr_write_b32 a242, v254\n"
"v_mfma_f32_16x16x32_bf16 v[180:183], v[208:211], a[0:3], 0\n"
"v_permlane16_swap_b32 v144, v146\n"
"v_mfma_f32_16x16x32_bf16 v[180:183], v[212:215], a[4:7], v[180:183]\n"
"v_permlane16_swap_b32 v153, v155\n"
"v_mfma_f32_16x16x32_bf16 v[180:183], v[216:219], a[8:11], v[180:183]\n"
"s_waitcnt lgkmcnt(1)\n"
"ds_read_b128 a[196:199], v198 offset:0\n"
"v_cvt_pk_bf16_f32 v90, v92, v93\n"
"v_mfma_f32_16x16x32_bf16 v[180:183], v[220:223], a[12:15], v[180:183]\n"
"ds_write2st64_b64 v255, v[80:81], v[82:83] offset0:224 offset1:225\n"
"v_pk_fma_f32 v[32:33], v[32:33], s[70:71], s[70:71] op_sel:[0,0,1] op_sel_hi:[1,0,1]\nv_pk_fma_f32 v[34:35], v[34:35], s[70:71], s[70:71] op_sel:[0,0,1] op_sel_hi:[1,0,1]\nv_pk_fma_f32 v[36:37], v[36:37], s[70:71], s[70:71] op_sel:[0,0,1] op_sel_hi:[1,0,1]\nv_pk_fma_f32 v[38:39], v[38:39], s[70:71], s[70:71] op_sel:[0,0,1] op_sel_hi:[1,0,1]\nv_pk_fma_f32 v[40:41], v[40:41], s[70:71], s[70:71] op_sel:[0,0,1] op_sel_hi:[1,0,1]\nv_pk_fma_f32 v[42:43], v[42:43], s[70:71], s[70:71] op_sel:[0,0,1] op_sel_hi:[1,0,1]\nv_pk_fma_f32 v[44:45], v[44:45], s[70:71], s[70:71] op_sel:[0,0,1] op_sel_hi:[1,0,1]\nv_pk_fma_f32 v[46:47], v[46:47], s[70:71], s[70:71] op_sel:[0,0,1] op_sel_hi:[1,0,1]\n"
"v_mfma_f32_16x16x32_bf16 v[184:187], v[208:211], a[16:19], 0\n"
"v_accvgpr_write_b32 a139, v155\n"
"v_cvt_pk_bf16_f32 v48, v48, v49\n"
"v_mfma_f32_16x16x32_bf16 v[184:187], v[212:215], a[20:23], v[184:187]\n"
"v_accvgpr_write_b32 a123, v139\n"
"v_cvt_pk_bf16_f32 v49, v50, v51\n"
"v_mfma_f32_16x16x32_bf16 v[184:187], v[216:219], a[24:27], v[184:187]\n"
"v_accvgpr_write_b32 a136, v152\n"
"v_cvt_pk_bf16_f32 v40, v40, v41\n"
"v_mfma_f32_16x16x32_bf16 v[184:187], v[220:223], a[28:31], v[184:187]\n"
"ds_write2st64_b64 v255, v[176:177], v[178:179] offset0:96 offset1:97\n"
"v_sub_f32 v180, s74, v180\n"
"v_sub_f32 v181, s74, v181\n"
"v_mfma_f32_16x16x32_bf16 v[188:191], v[208:211], a[32:35], 0\n"
"v_sub_f32 v182, s74, v182\n"
"v_sub_f32 v183, s74, v183\n"
"v_mfma_f32_16x16x32_bf16 v[188:191], v[212:215], a[36:39], v[188:191]\n"
"v_accvgpr_write_b32 a128, v144\n"
"v_cvt_pk_bf16_f32 v74, v76, v77\n"
"v_mfma_f32_16x16x32_bf16 v[188:191], v[216:219], a[40:43], v[188:191]\n"
"v_cvt_pk_bf16_f32 v107, v110, v111\n"
"v_cvt_pk_bf16_f32 v8, v8, v9\n"
"v_mfma_f32_16x16x32_bf16 v[188:191], v[220:223], a[44:47], v[188:191]\n"
"v_cvt_pk_bf16_f32 v98, v100, v101\n"
"v_mov_b32 v199, 0\n"
"v_mfma_f32_16x16x32_bf16 v[192:195], v[208:211], a[192:195], 0\n"
"s_waitcnt lgkmcnt(1)\n"
"ds_read_b128 v[208:211], v228 offset:0\n"
"v_accvgpr_write_b32 a137, v153\n"
"v_mfma_f32_16x16x32_bf16 v[192:195], v[212:215], a[196:199], v[192:195]\n"
"ds_read_b128 v[212:215], v229 offset:0\n"
"v_sub_f32 v185, s74, v185\n"
"v_mfma_f32_16x16x32_bf16 v[192:195], v[216:219], a[200:203], v[192:195]\n"
"ds_read_b128 v[216:219], v228 offset:1024\n"
"v_sub_f32 v190, s74, v190\n"
"v_mfma_f32_16x16x32_bf16 v[192:195], v[220:223], a[204:207], v[192:195]\n"
"s_waitcnt lgkmcnt(2)\n"
"ds_read_b128 v[220:223], v229 offset:1024\n"
"v_mov_b32 v204, 0\n"
"v_mfma_f32_16x16x32_bf16 v[152:155], v[208:211], a[0:3], v[180:183]\n"
"s_waitcnt lgkmcnt(2)\n"
"v_sub_f32 v184, s74, v184\n"
"v_sub_f32 v188, s74, v188\n"
"v_mfma_f32_16x16x32_bf16 v[152:155], v[212:215], a[4:7], v[152:155]\n"
"s_waitcnt lgkmcnt(1)\n"
"v_sub_f32 v186, s74, v186\n"
"v_sub_f32 v187, s74, v187\n"
"v_mfma_f32_16x16x32_bf16 v[152:155], v[216:219], a[8:11], v[152:155]\n"
"s_waitcnt lgkmcnt(0)\n"
"v_cvt_pk_bf16_f32 v99, v102, v103\n"
"v_cvt_pk_bf16_f32 v9, v10, v11\n"
"v_mfma_f32_16x16x32_bf16 v[152:155], v[220:223], a[12:15], v[152:155]\n"
"ds_write2st64_b64 v255, v[88:89], v[90:91] offset0:226 offset1:227\n"
"v_accvgpr_write_b32 a121, v137\n"
"v_cvt_pk_bf16_f32 v58, v60, v61\n"
"v_sub_f32 v191, s74, v191\n"
"v_mfma_f32_16x16x32_bf16 v[156:159], v[216:219], a[24:27], v[184:187]\n"
"v_cvt_pk_bf16_f32 v24, v24, v25\n"
"v_sub_f32 v193, s74, v193\n"
"v_mfma_f32_16x16x32_bf16 v[156:159], v[220:223], a[28:31], v[156:159]\n"
"v_sub_f32 v189, s74, v189\n"
"v_sub_f32 v194, s74, v194\n"
"v_mfma_f32_16x16x32_bf16 v[156:159], v[208:211], a[16:19], v[156:159]\n"
"v_cvt_pk_bf16_f32 v50, v52, v53\n"
"v_sub_f32 v192, s74, v192\n"
"v_mfma_f32_16x16x32_bf16 v[156:159], v[212:215], a[20:23], v[156:159]\n"
"v_exp_f32 v154, v154\n"
"v_mfma_f32_16x16x32_bf16 v[160:163], v[208:211], a[32:35], v[188:191]\n"
"v_cvt_pk_bf16_f32 v11, v14, v15\n"
"v_sub_f32 v195, s74, v195\n"
"v_mfma_f32_16x16x32_bf16 v[160:163], v[212:215], a[36:39], v[160:163]\n"
"v_exp_f32 v152, v152\n"
"v_mfma_f32_16x16x32_bf16 v[160:163], v[216:219], a[40:43], v[160:163]\n"
"s_nop 0\n"
"v_exp_f32 v158, v158\n"
"v_mfma_f32_16x16x32_bf16 v[160:163], v[220:223], a[44:47], v[160:163]\n"
"v_accvgpr_write_b32 a129, v145\n"
"v_cvt_pk_bf16_f32 v41, v42, v43\n"
"v_mfma_f32_16x16x32_bf16 v[164:167], v[216:219], a[200:203], v[192:195]\n"
"ds_read_b128 v[216:219], v228 offset:5120\n"
"v_cvt_pk_bf16_f32 v25, v26, v27\n"
"v_mfma_f32_16x16x32_bf16 v[164:167], v[220:223], a[204:207], v[164:167]\n"
"ds_read_b128 v[220:223], v229 offset:5120\n"
"v_cvt_pk_bf16_f32 v66, v68, v69\n"
"v_mfma_f32_16x16x32_bf16 v[164:167], v[208:211], a[192:195], v[164:167]\n"
"ds_read_b128 v[208:211], v228 offset:4096\n"
"v_cvt_pk_bf16_f32 v123, v126, v127\n"
"v_mfma_f32_16x16x32_bf16 v[164:167], v[212:215], a[196:199], v[164:167]\n"
"s_waitcnt lgkmcnt(2)\n"
"ds_read_b128 v[212:215], v229 offset:4096\n"
"v_cvt_pk_bf16_f32 v59, v62, v63\n"
"v_mfma_f32_16x16x32_bf16 v[168:171], v[216:219], a[8:11], v[180:183]\n"
"s_waitcnt lgkmcnt(2)\n"
"v_exp_f32 v155, v155\n"
"v_mfma_f32_16x16x32_bf16 v[168:171], v[220:223], a[12:15], v[168:171]\n"
"s_waitcnt lgkmcnt(1)\n"
"v_exp_f32 v157, v157\n"
"v_mfma_f32_16x16x32_bf16 v[168:171], v[208:211], a[0:3], v[168:171]\n"
"s_waitcnt lgkmcnt(0)\n"
"v_permlane16_swap_b32 v57, v59\n"
"v_mfma_f32_16x16x32_bf16 v[168:171], v[212:215], a[4:7], v[168:171]\n"
"ds_write2st64_b64 v255, v[96:97], v[98:99] offset0:228 offset1:229\n"
"v_cvt_pk_bf16_f32 v51, v54, v55\n"
"v_exp_f32 v160, v160\n"
"v_exp_f32 v161, v161\n"
"v_mfma_f32_16x16x32_bf16 v[172:175], v[208:211], a[16:19], v[184:187]\n"
"v_exp_f32 v159, v159\n"
"v_mfma_f32_16x16x32_bf16 v[172:175], v[212:215], a[20:23], v[172:175]\n"
"v_exp_f32 v156, v156\n"
"v_mfma_f32_16x16x32_bf16 v[172:175], v[216:219], a[24:27], v[172:175]\n"
"v_exp_f32 v153, v153\n"
"v_mfma_f32_16x16x32_bf16 v[172:175], v[220:223], a[28:31], v[172:175]\n"
"ds_write2st64_b64 v255, v[104:105], v[106:107] offset0:230 offset1:231\n"
"v_cvt_pk_bf16_f32 v75, v78, v79\n"
"v_accvgpr_write_b32 a240, v254\n"
"v_accvgpr_write_b32 a243, v254\n"
"v_cvt_pk_bf16_f32 v136, v156, v157\n"
"v_cvt_pk_bf16_f32 v144, v160, v161\n"
"v_exp_f32 v162, v162\n"
"v_exp_f32 v164, v164\n"
"v_exp_f32 v167, v167\n"
"v_mfma_f32_16x16x32_bf16 v[176:179], v[216:219], a[40:43], v[188:191]\n"
"v_exp_f32 v169, v169\n"
"v_mfma_f32_16x16x32_bf16 v[176:179], v[220:223], a[44:47], v[176:179]\n"
"v_permlane16_swap_b32 v48, v50\n"
"v_mfma_f32_16x16x32_bf16 v[176:179], v[208:211], a[32:35], v[176:179]\n"
"v_cvt_pk_bf16_f32 v128, v152, v153\n"
"v_cvt_pk_bf16_f32 v129, v154, v155\n"
"v_mfma_f32_16x16x32_bf16 v[176:179], v[212:215], a[36:39], v[176:179]\n"
"v_accvgpr_write_b32 a115, v131\n"
"v_exp_f32 v163, v163\n"
"v_exp_f32 v165, v165\n"
"v_exp_f32 v166, v166\n"
"v_exp_f32 v168, v168\n"
"v_exp_f32 v170, v170\n"
"v_exp_f32 v173, v173\n"
"v_add_u32 v249, s75, v248\n"
"v_mfma_f32_16x16x32_bf16 v[152:155], v[208:211], a[192:195], v[192:195]\n"
"ds_read_b128 v[208:211], v230 offset:0\n"
"v_cvt_pk_bf16_f32 v67, v70, v71\n"
"v_mfma_f32_16x16x32_bf16 v[152:155], v[212:215], a[196:199], v[152:155]\n"
"ds_read_b128 v[212:215], v231 offset:0\n"
"v_cvt_pk_bf16_f32 v145, v162, v163\n"
"v_mfma_f32_16x16x32_bf16 v[152:155], v[216:219], a[200:203], v[152:155]\n"
"ds_read_b128 v[216:219], v230 offset:1024\n"
"v_mov_b32 v196, 0\n"
"v_mfma_f32_16x16x32_bf16 v[152:155], v[220:223], a[204:207], v[152:155]\n"
"s_waitcnt lgkmcnt(2)\n"
"ds_read_b128 v[220:223], v231 offset:1024\n"
"v_cvt_pk_bf16_f32 v137, v158, v159\n"
"v_mfma_f32_16x16x32_bf16 v[156:159], v[208:211], a[0:3], v[180:183]\n"
"s_waitcnt lgkmcnt(2)\n"
"v_permlane16_swap_b32 v49, v51\n"
"v_mfma_f32_16x16x32_bf16 v[156:159], v[212:215], a[4:7], v[156:159]\n"
"s_waitcnt lgkmcnt(1)\n"
"v_exp_f32 v178, v178\n"
"v_mfma_f32_16x16x32_bf16 v[156:159], v[216:219], a[8:11], v[156:159]\n"
"s_waitcnt lgkmcnt(0)\n"
"v_exp_f32 v174, v174\n"
"v_mfma_f32_16x16x32_bf16 v[156:159], v[220:223], a[12:15], v[156:159]\n"
"ds_write2st64_b64 v255, v[72:73], v[74:75] offset0:198 offset1:199\n"
"v_cvt_pk_bf16_f32 v112, v112, v113\n"
"v_permlane16_swap_b32 v56, v58\n"
"v_permlane16_swap_b32 v9, v11\n"
"v_cvt_pk_bf16_f32 v27, v30, v31\n"
"v_cvt_pk_bf16_f32 v125, v166, v167\n"
"v_cvt_pk_bf16_f32 v130, v168, v169\n"
"v_exp_f32 v171, v171\n"
"v_exp_f32 v172, v172\n"
"v_mfma_f32_16x16x32_bf16 v[160:163], v[216:219], a[24:27], v[184:187]\n"
"v_exp_f32 v179, v179\n"
"v_mfma_f32_16x16x32_bf16 v[160:163], v[220:223], a[28:31], v[160:163]\n"
"v_exp_f32 v154, v154\n"
"v_mfma_f32_16x16x32_bf16 v[160:163], v[208:211], a[16:19], v[160:163]\n"
"v_cvt_pk_bf16_f32 v124, v164, v165\n"
"v_cvt_pk_bf16_f32 v138, v172, v173\n"
"v_mfma_f32_16x16x32_bf16 v[160:163], v[212:215], a[20:23], v[160:163]\n"
"ds_write2st64_b64 v255, v[64:65], v[66:67] offset0:196 offset1:197\n"
"v_accvgpr_write_b32 a241, v254\n"
"v_cvt_pk_bf16_f32 v131, v170, v171\n"
"v_exp_f32 v176, v176\n"
"v_exp_f32 v157, v157\n"
"v_mfma_f32_16x16x32_bf16 v[164:167], v[208:211], a[32:35], v[188:191]\n"
"ds_read_b128 v[208:211], v230 offset:4096\n"
"v_cvt_pk_bf16_f32 v43, v46, v47\n"
"v_mfma_f32_16x16x32_bf16 v[164:167], v[212:215], a[36:39], v[164:167]\n"
"ds_read_b128 v[212:215], v231 offset:4096\n"
"v_cvt_pk_bf16_f32 v113, v114, v115\n"
"v_mfma_f32_16x16x32_bf16 v[164:167], v[216:219], a[40:43], v[164:167]\n"
"ds_read_b128 v[216:219], v230 offset:5120\n"
"v_cvt_pk_bf16_f32 v32, v32, v33\n"
"v_mfma_f32_16x16x32_bf16 v[164:167], v[220:223], a[44:47], v[164:167]\n"
"s_waitcnt lgkmcnt(2)\n"
"ds_read_b128 v[220:223], v231 offset:5120\n"
"v_accvgpr_write_b32 a130, v146\n"
"v_permlane16_swap_b32 v41, v43\n"
"v_exp_f32 v175, v175\n"
"v_exp_f32 v177, v177\n"
"v_exp_f32 v162, v162\n"
"v_mfma_f32_16x16x32_bf16 v[168:171], v[208:211], a[0:3], v[180:183]\n"
"s_waitcnt lgkmcnt(2)\n"
"v_cvt_pk_bf16_f32 v10, v12, v13\n"
"v_cvt_pk_bf16_f32 v139, v174, v175\n"
"v_mfma_f32_16x16x32_bf16 v[168:171], v[212:215], a[4:7], v[168:171]\n"
"s_waitcnt lgkmcnt(1)\n"
"v_exp_f32 v158, v158\n"
"v_mfma_f32_16x16x32_bf16 v[168:171], v[216:219], a[8:11], v[168:171]\n"
"s_waitcnt lgkmcnt(0)\n"
"v_cvt_pk_bf16_f32 v26, v28, v29\n"
"v_cvt_pk_bf16_f32 v147, v178, v179\n"
"v_mfma_f32_16x16x32_bf16 v[168:171], v[220:223], a[12:15], v[168:171]\n"
"ds_write_b128 v243, v[48:51] offset:12288\n"
"v_accvgpr_write_b32 a155, v11\n"
"v_accvgpr_write_b32 a187, v43\n"
"v_exp_f32 v153, v153\n"
"v_exp_f32 v156, v156\n"
"v_mfma_f32_16x16x32_bf16 v[172:175], v[216:219], a[24:27], v[184:187]\n"
"v_exp_f32 v155, v155\n"
"v_mfma_f32_16x16x32_bf16 v[172:175], v[220:223], a[28:31], v[172:175]\n"
"v_exp_f32 v160, v160\n"
"v_mfma_f32_16x16x32_bf16 v[172:175], v[208:211], a[16:19], v[172:175]\n"
"v_cvt_pk_bf16_f32 v42, v44, v45\n"
"v_cvt_pk_bf16_f32 v146, v176, v177\n"
"v_mfma_f32_16x16x32_bf16 v[172:175], v[212:215], a[20:23], v[172:175]\n"
"s_cbranch_vccnz LPCE0_%=\nLPKE0_%=:\n"
"ds_write_b128 v243, v[56:59] offset:14336\n"
"v_permlane16_swap_b32 v24, v26\n"
"v_cvt_pk_bf16_f32 v127, v154, v155\n"
"v_exp_f32 v163, v163\n"
"v_exp_f32 v164, v164\n"
"v_exp_f32 v165, v165\n"
"v_exp_f32 v166, v166\n"
"v_exp_f32 v167, v167\n"
"v_mfma_f32_16x16x32_bf16 v[176:179], v[208:211], a[32:35], v[188:191]\n"
"v_exp_f32 v152, v152\n"
"v_mfma_f32_16x16x32_bf16 v[176:179], v[212:215], a[36:39], v[176:179]\n"
"v_cvt_pk_bf16_f32 v0, v0, v1\n"
"v_cvt_pk_bf16_f32 v132, v156, v157\n"
"v_mfma_f32_16x16x32_bf16 v[176:179], v[216:219], a[40:43], v[176:179]\n"
"v_accvgpr_write_b32 a153, v9\n"
"v_accvgpr_write_b32 a170, v26\n"
"v_mfma_f32_16x16x32_bf16 v[176:179], v[220:223], a[44:47], v[176:179]\n"
"v_cvt_pk_bf16_f32 v1, v2, v3\n"
"v_cvt_pk_bf16_f32 v16, v16, v17\n"
"v_cvt_pk_bf16_f32 v126, v152, v153\n"
"v_exp_f32 v159, v159\n"
"v_exp_f32 v168, v168\n"
"v_exp_f32 v169, v169\n"
"v_exp_f32 v170, v170\n"
"v_exp_f32 v173, v173\n"
"s_waitcnt vmcnt(8) lgkmcnt(0)\n"
"s_barrier\n"
"ds_read_b128 v[208:211], v228 offset:16384\n"
"ds_read_b128 v[212:215], v229 offset:16384\n"
"ds_read_b128 v[216:219], v228 offset:17408\n"
"ds_read_b128 v[220:223], v229 offset:17408\n"
"s_waitcnt lgkmcnt(3)\n"
"v_mfma_f32_16x16x32_bf16 v[152:155], v[208:211], a[0:3], v[180:183]\n"
"v_exp_f32 v177, v177\n"
"s_waitcnt lgkmcnt(2)\n"
"v_mfma_f32_16x16x32_bf16 v[152:155], v[212:215], a[4:7], v[152:155]\n"
"ds_read_b128 a[140:143], v249 offset:33792\n"
"v_cvt_pk_bf16_f32 v141, v162, v163\n"
"s_waitcnt lgkmcnt(2)\n"
"v_mfma_f32_16x16x32_bf16 v[152:155], v[216:219], a[8:11], v[152:155]\n"
"s_waitcnt lgkmcnt(1)\n"
"v_exp_f32 v175, v175\n"
"v_mfma_f32_16x16x32_bf16 v[152:155], v[220:223], a[12:15], v[152:155]\n"
"ds_read_b64_tr_b16 a[246:247], v234 offset:4096\n"
"ds_read_b64_tr_b16 a[250:251], v238 offset:4096\n"
"v_permlane16_swap_b32 v8, v10\n"
"v_cvt_pk_bf16_f32 v33, v34, v35\n"
"v_cvt_pk_bf16_f32 v34, v36, v37\n"
"v_cvt_pk_bf16_f32 v133, v158, v159\n"
"v_exp_f32 v161, v161\n"
"v_exp_f32 v176, v176\n"
"v_mfma_f32_16x16x32_bf16 v[156:159], v[216:219], a[24:27], v[184:187]\n"
"ds_read_b128 a[148:151], v249 offset:34816\n"
"v_mov_b32 v203, 0\n"
"v_mfma_f32_16x16x32_bf16 v[156:159], v[220:223], a[28:31], v[156:159]\n"
"ds_read_b64_tr_b16 a[244:245], v234 offset:0\n"
"v_accvgpr_write_b32 a154, v10\n"
"v_mfma_f32_16x16x32_bf16 v[156:159], v[208:211], a[16:19], v[156:159]\n"
"ds_read_b64_tr_b16 a[248:249], v238 offset:0\n"
"ds_read_b128 a[172:175], v249 offset:50176\n"
"v_mfma_f32_16x16x32_bf16 v[156:159], v[212:215], a[20:23], v[156:159]\n"
"ds_read_b64_tr_b16 v[226:227], v239 offset:4096\n"
"v_cvt_pk_bf16_f32 v115, v118, v119\n"
"v_permlane16_swap_b32 v40, v42\n"
"v_cvt_pk_bf16_f32 v140, v160, v161\n"
"v_cvt_pk_bf16_f32 v148, v164, v165\n"
"v_cvt_pk_bf16_f32 v149, v166, v167\n"
"v_exp_f32 v171, v171\n"
"v_exp_f32 v172, v172\n"
"v_exp_f32 v174, v174\n"
"v_exp_f32 v178, v178\n"
"v_exp_f32 v152, v152\n"
"v_mfma_f32_16x16x32_bf16 v[160:163], v[208:211], a[32:35], v[188:191]\n"
"ds_read_b128 a[132:135], v249 offset:32768\n"
"v_cvt_pk_bf16_f32 v114, v116, v117\n"
"v_mfma_f32_16x16x32_bf16 v[160:163], v[212:215], a[36:39], v[160:163]\n"
"ds_read_b128 a[100:103], v248 offset:49152\n"
"v_cvt_pk_bf16_f32 v143, v174, v175\n"
"v_mfma_f32_16x16x32_bf16 v[160:163], v[216:219], a[40:43], v[160:163]\n"
"ds_read_b128 a[116:119], v248 offset:51200\n"
"v_cvt_pk_bf16_f32 v150, v176, v177\n"
"v_mfma_f32_16x16x32_bf16 v[160:163], v[220:223], a[44:47], v[160:163]\n"
"v_mov_b32 v201, 0\n"
"v_cvt_pk_bf16_f32 v134, v168, v169\n"
"v_mfma_f32_16x16x32_bf16 v[164:167], v[216:219], a[200:203], v[192:195]\n"
"ds_read_b64_tr_b16 a[254:255], v235 offset:4096\n"
"v_accvgpr_write_b32 a185, v41\n"
"v_mfma_f32_16x16x32_bf16 v[164:167], v[220:223], a[204:207], v[164:167]\n"
"ds_read_b64_tr_b16 a[252:253], v235 offset:0\n"
"ds_read_b128 a[164:167], v249 offset:49152\n"
"v_mfma_f32_16x16x32_bf16 v[164:167], v[208:211], a[192:195], v[164:167]\n"
"ds_read_b64_tr_b16 v[224:225], v239 offset:0\n"
"v_accvgpr_write_b32 a186, v42\n"
"v_mfma_f32_16x16x32_bf16 v[164:167], v[212:215], a[196:199], v[164:167]\n"
"s_waitcnt lgkmcnt(7)\n"
"ds_read_b128 v[220:223], v229 offset:21504\n"
"v_cvt_pk_bf16_f32 v3, v6, v7\n"
"v_mov_b32 v197, 0\n"
"v_cvt_pk_bf16_f32 v135, v170, v171\n"
"v_exp_f32 v179, v179\n"
"v_exp_f32 v154, v154\n"
"v_exp_f32 v156, v156\n"
"v_exp_f32 v160, v160\n"
"v_mfma_f32_16x16x32_bf16 a[216:219], v[124:127], a[244:247], 0\n"
"ds_read_b128 a[156:159], v249 offset:35840\n"
"v_accvgpr_write_b32 a152, v8\n"
"v_mfma_f32_16x16x32_bf16 v[8:11], v[128:131], a[244:247], 0\n"
"ds_read_b128 a[108:111], v248 offset:50176\n"
"v_accvgpr_write_b32 a184, v40\n"
"v_mfma_f32_16x16x32_bf16 v[8:11], v[132:135], a[248:251], v[8:11]\n"
"ds_read_b128 a[180:183], v249 offset:51200\n"
"v_cvt_pk_bf16_f32 v142, v172, v173\n"
"v_mfma_f32_16x16x32_bf16 v[40:43], v[136:139], a[244:247], 0\n"
"ds_read_b128 a[124:127], v248 offset:52224\n"
"v_bfe_u32 v248, v242, 8, 2\n"
"v_mfma_f32_16x16x32_bf16 v[40:43], v[140:143], a[248:251], v[40:43]\n"
"ds_read_b128 a[188:191], v249 offset:52224\n"
"v_cvt_pk_bf16_f32 v151, v178, v179\n"
"v_mfma_f32_16x16x32_bf16 v[72:75], v[144:147], a[244:247], 0\n"
"v_permlane16_swap_b32 v113, v115\n"
"v_mfma_f32_16x16x32_bf16 v[72:75], v[148:151], a[248:251], v[72:75]\n"
"s_waitcnt lgkmcnt(6)\n"
"ds_read_b128 v[208:211], v228 offset:20480\n"
"ds_read_b64_tr_b16 a[250:251], v238 offset:5120\n"
"v_accvgpr_write_b32 a97, v113\n"
"v_mov_b32 v202, 0\n"
"v_exp_f32 v153, v153\n"
"v_exp_f32 v155, v155\n"
"v_exp_f32 v162, v162\n"
"v_exp_f32 v166, v166\n"
"v_mfma_f32_16x16x32_bf16 v[76:79], v[148:151], v[224:227], 0\n"
"v_cvt_pk_bf16_f32 v17, v18, v19\n"
"v_mov_b32 v198, 0\n"
"v_mfma_f32_16x16x32_bf16 v[76:79], v[144:147], a[252:255], v[76:79]\n"
"ds_read_b128 v[216:219], v228 offset:21504\n"
"v_accvgpr_write_b32 a104, v120\n"
"v_mfma_f32_16x16x32_bf16 v[44:47], v[140:143], v[224:227], 0\n"
"v_cvt_pk_bf16_f32 v2, v4, v5\n"
"v_cvt_pk_bf16_f32 v35, v38, v39\n"
"v_mfma_f32_16x16x32_bf16 v[44:47], v[136:139], a[252:255], v[44:47]\n"
"ds_read_b128 v[212:215], v229 offset:20480\n"
"v_permlane16_swap_b32 v112, v114\n"
"v_permlane16_swap_b32 v1, v3\n"
"v_permlane16_swap_b32 v33, v35\n"
"v_exp_f32 v157, v157\n"
"v_exp_f32 v158, v158\n"
"v_mfma_f32_16x16x32_bf16 v[12:15], v[132:135], v[224:227], 0\n"
"ds_read_b64_tr_b16 a[244:245], v234 offset:1024\n"
"v_cvt_pk_bf16_f32 v96, v152, v153\n"
"v_mfma_f32_16x16x32_bf16 v[12:15], v[128:131], a[252:255], v[12:15]\n"
"v_exp_f32 v159, v159\n"
"v_mfma_f32_16x16x32_bf16 a[220:223], v[124:127], a[252:255], 0\n"
"s_waitcnt lgkmcnt(5)\n"
"v_exp_f32 v165, v165\n"
"v_smfmac_f32_16x16x64_bf16 v[196:199], a[240:243], v[128:135], v241\n"
"s_add_u32 m0, s44, 98304\n"
"ds_read_b64_tr_b16 a[246:247], v234 offset:5120\n"
"ds_read_b64_tr_b16 a[248:249], v238 offset:1024\n"
"v_mfma_f32_16x16x32_bf16 v[202:205], a[240:243], v[124:127], v[202:205]\n"
"s_waitcnt lgkmcnt(4)\n"
"buffer_load_dwordx4 v240, s[20:23], s56 offen nt lds\n"
"v_mfma_f32_16x16x32_bf16 v[168:171], v[216:219], a[8:11], v[180:183]\n"
"v_permlane16_swap_b32 v25, v27\n"
"v_mfma_f32_16x16x32_bf16 v[168:171], v[220:223], a[12:15], v[168:171]\n"
"v_accvgpr_write_b32 a99, v115\n"
"v_cvt_pk_bf16_f32 v97, v154, v155\n"
"v_mfma_f32_16x16x32_bf16 v[168:171], v[208:211], a[0:3], v[168:171]\n"
"s_waitcnt lgkmcnt(3)\n"
"v_exp_f32 v163, v163\n"
"v_mfma_f32_16x16x32_bf16 v[168:171], v[212:215], a[4:7], v[168:171]\n"
"v_cvt_pk_bf16_f32 v18, v20, v21\n"
"v_accvgpr_write_b32 a171, v27\n"
"v_mfma_f32_16x16x32_bf16 v[172:175], v[208:211], a[16:19], v[184:187]\n"
"ds_read_b64_tr_b16 a[254:255], v235 offset:5120\n"
"v_cvt_pk_bf16_f32 v113, v162, v163\n"
"v_mfma_f32_16x16x32_bf16 v[172:175], v[212:215], a[20:23], v[172:175]\n"
"ds_read_b64_tr_b16 v[224:225], v239 offset:1024\n"
"v_cvt_pk_bf16_f32 v19, v22, v23\n"
"v_mfma_f32_16x16x32_bf16 v[172:175], v[216:219], a[24:27], v[172:175]\n"
"s_add_u32 s40, s56, s52\n"
"v_exp_f32 v167, v167\n"
"v_mfma_f32_16x16x32_bf16 v[172:175], v[220:223], a[28:31], v[172:175]\n"
"buffer_load_dwordx4 v240, s[20:23], s40 offen offset:1024 nt lds\n"
"v_mfma_f32_16x16x32_bf16 v[176:179], v[216:219], a[40:43], v[188:191]\n"
"v_accvgpr_write_b32 a145, v1\n"
"v_accvgpr_write_b32 a168, v24\n"
"v_mfma_f32_16x16x32_bf16 v[176:179], v[220:223], a[44:47], v[176:179]\n"
"v_exp_f32 v171, v171\n"
"v_mfma_f32_16x16x32_bf16 v[176:179], v[208:211], a[32:35], v[176:179]\n"
"v_exp_f32 v169, v169\n"
"v_mfma_f32_16x16x32_bf16 v[176:179], v[212:215], a[36:39], v[176:179]\n"
"v_exp_f32 v170, v170\n"
"v_mfma_f32_16x16x32_bf16 v[152:155], v[208:211], a[192:195], v[192:195]\n"
"ds_read_b64_tr_b16 a[252:253], v235 offset:1024\n"
"ds_read_b64_tr_b16 v[226:227], v239 offset:5120\n"
"v_mfma_f32_16x16x32_bf16 v[152:155], v[212:215], a[196:199], v[152:155]\n"
"v_mov_b32 v200, 0\n"
"v_cvt_pk_bf16_f32 v104, v156, v157\n"
"v_mfma_f32_16x16x32_bf16 v[152:155], v[216:219], a[200:203], v[152:155]\n"
"ds_read_b128 v[208:211], v230 offset:16384\n"
"v_cvt_pk_bf16_f32 v105, v158, v159\n"
"v_mfma_f32_16x16x32_bf16 v[152:155], v[220:223], a[204:207], v[152:155]\n"
"s_waitcnt lgkmcnt(3)\n"
"v_permlane16_swap_b32 v0, v2\n"
"v_mfma_f32_16x16x32_bf16 a[232:235], v[124:127], a[244:247], 0\n"
"ds_read_b128 v[212:215], v231 offset:16384\n"
"v_accvgpr_write_b32 a169, v25\n"
"v_mfma_f32_16x16x32_bf16 v[24:27], v[128:131], a[244:247], 0\n"
"s_add_u32 s40, s56, s53\n"
"ds_read_b128 v[220:223], v231 offset:17408\n"
"v_accvgpr_write_b32 a146, v2\n"
"v_mfma_f32_16x16x32_bf16 v[24:27], v[132:135], a[248:251], v[24:27]\n"
"buffer_load_dwordx4 v240, s[20:23], s40 offen offset:2048 nt lds\n"
"v_mfma_f32_16x16x32_bf16 v[56:59], v[136:139], a[244:247], 0\n"
"v_exp_f32 v164, v164\n"
"v_mfma_f32_16x16x32_bf16 v[56:59], v[140:143], a[248:251], v[56:59]\n"
"v_accvgpr_write_b32 a96, v112\n"
"v_accvgpr_write_b32 a179, v35\n"
"v_mfma_f32_16x16x32_bf16 v[88:91], v[144:147], a[244:247], 0\n"
"v_exp_f32 v179, v179\n"
"v_mfma_f32_16x16x32_bf16 v[88:91], v[148:151], a[248:251], v[88:91]\n"
"s_waitcnt lgkmcnt(2)\n"
"v_permlane16_swap_b32 v16, v18\n"
"v_mfma_f32_16x16x32_bf16 v[92:95], v[148:151], v[224:227], 0\n"
"s_add_u32 s40, s56, s54\n"
"ds_read_b128 v[216:219], v230 offset:17408\n"
"v_cvt_pk_bf16_f32 v99, v170, v171\n"
"v_mfma_f32_16x16x32_bf16 v[92:95], v[144:147], a[252:255], v[92:95]\n"
"buffer_load_dwordx4 v240, s[20:23], s40 offen offset:3072 nt lds\n"
"v_mfma_f32_16x16x32_bf16 v[60:63], v[140:143], v[224:227], 0\n"
"s_add_u32 s55, s55, s59\n"
"v_exp_f32 v161, v161\n"
"v_mfma_f32_16x16x32_bf16 v[60:63], v[136:139], a[252:255], v[60:63]\n"
"s_add_u32 s41, s55, s57\n"
"v_permlane16_swap_b32 v121, v123\n"
"v_mfma_f32_16x16x32_bf16 v[28:31], v[132:135], v[224:227], 0\n"
"s_and_b32 s41, s41, s58\n"
"v_permlane16_swap_b32 v32, v34\n"
"v_mfma_f32_16x16x32_bf16 v[28:31], v[128:131], a[252:255], v[28:31]\n"
"s_andn2_b32 s56, s55, s58\n"
"v_exp_f32 v176, v176\n"
"v_mfma_f32_16x16x32_bf16 a[236:239], v[124:127], a[252:255], 0\n"
"s_or_b32 s56, s56, s41\n"
"ds_read_b64_tr_b16 a[250:251], v236 offset:4096\n"
"ds_read_b64_tr_b16 a[252:253], v233 offset:0\n"
"v_smfmac_f32_16x16x64_bf16 v[198:201], a[240:243], v[136:143], v241\n"
"ds_read_b64_tr_b16 a[254:255], v233 offset:4096\n"
"v_accvgpr_write_b32 a176, v32\n"
"v_mfma_f32_16x16x32_bf16 v[156:159], v[208:211], a[0:3], v[180:183]\n"
"s_waitcnt lgkmcnt(3)\n"
"ds_read_b64_tr_b16 a[244:245], v232 offset:0\n"
"ds_read_b64_tr_b16 a[248:249], v236 offset:0\n"
"v_mfma_f32_16x16x32_bf16 v[156:159], v[212:215], a[4:7], v[156:159]\n"
"v_cvt_pk_bf16_f32 v112, v160, v161\n"
"v_cvt_pk_bf16_f32 v120, v164, v165\n"
"v_mfma_f32_16x16x32_bf16 v[156:159], v[216:219], a[8:11], v[156:159]\n"
"s_add_u32 m0, s44, 49152\n"
"ds_read_b64_tr_b16 a[246:247], v232 offset:4096\n"
"ds_read_b64_tr_b16 v[224:225], v237 offset:0\n"
"v_mfma_f32_16x16x32_bf16 v[156:159], v[220:223], a[12:15], v[156:159]\n"
"buffer_load_dwordx4 v240, s[16:19], s56 offen nt lds\n"
"v_mfma_f32_16x16x32_bf16 v[160:163], v[216:219], a[24:27], v[184:187]\n"
"v_accvgpr_write_b32 a105, v121\n"
"v_accvgpr_write_b32 a144, v0\n"
"v_mfma_f32_16x16x32_bf16 v[160:163], v[220:223], a[28:31], v[160:163]\n"
"v_accvgpr_write_b32 a178, v34\n"
"v_cvt_pk_bf16_f32 v121, v166, v167\n"
"v_mfma_f32_16x16x32_bf16 v[160:163], v[208:211], a[16:19], v[160:163]\n"
"ds_read_b64_tr_b16 v[226:227], v237 offset:4096\n"
"v_accvgpr_write_b32 a98, v114\n"
"v_mfma_f32_16x16x32_bf16 v[160:163], v[212:215], a[20:23], v[160:163]\n"
"v_exp_f32 v172, v172\n"
"v_mfma_f32_16x16x32_bf16 v[164:167], v[208:211], a[32:35], v[188:191]\n"
"ds_read_b128 v[208:211], v230 offset:20480\n"
"v_accvgpr_write_b32 a147, v3\n"
"v_mfma_f32_16x16x32_bf16 v[164:167], v[212:215], a[36:39], v[164:167]\n"
"v_exp_f32 v168, v168\n"
"v_mfma_f32_16x16x32_bf16 v[164:167], v[216:219], a[40:43], v[164:167]\n"
"v_exp_f32 v174, v174\n"
"v_mfma_f32_16x16x32_bf16 v[164:167], v[220:223], a[44:47], v[164:167]\n"
"s_waitcnt lgkmcnt(2)\n"
"ds_read_b128 v[216:219], v230 offset:21504\n"
"v_accvgpr_write_b32 a177, v33\n"
"v_mfma_f32_16x16x32_bf16 a[208:211], v[124:127], a[244:247], 0\n"
"v_exp_f32 v177, v177\n"
"v_mfma_f32_16x16x32_bf16 v[0:3], v[128:131], a[244:247], 0\n"
"s_add_u32 s40, s56, s52\n"
"v_exp_f32 v154, v154\n"
"v_mfma_f32_16x16x32_bf16 v[0:3], v[132:135], a[248:251], v[0:3]\n"
"buffer_load_dwordx4 v240, s[16:19], s40 offen offset:1024 nt lds\n"
"v_mfma_f32_16x16x32_bf16 v[32:35], v[136:139], a[244:247], 0\n"
"v_exp_f32 v178, v178\n"
"v_mfma_f32_16x16x32_bf16 v[32:35], v[140:143], a[248:251], v[32:35]\n"
"v_permlane16_swap_b32 v17, v19\n"
"v_mfma_f32_16x16x32_bf16 v[64:67], v[144:147], a[244:247], 0\n"
"ds_read_b128 v[212:215], v231 offset:20480\n"
"v_cvt_pk_bf16_f32 v98, v168, v169\n"
"v_mfma_f32_16x16x32_bf16 v[64:67], v[148:151], a[248:251], v[64:67]\n"
"s_waitcnt lgkmcnt(2)\n"
"v_exp_f32 v162, v162\n"
"v_mfma_f32_16x16x32_bf16 v[68:71], v[148:151], v[224:227], 0\n"
"s_add_u32 s40, s56, s53\n"
"ds_read_b128 v[220:223], v231 offset:21504\n"
"v_bfe_u32 v254, v242, 6, 4\n"
"v_mfma_f32_16x16x32_bf16 v[68:71], v[144:147], a[252:255], v[68:71]\n"
"buffer_load_dwordx4 v240, s[16:19], s40 offen offset:2048 nt lds\n"
"v_mfma_f32_16x16x32_bf16 v[36:39], v[140:143], v[224:227], 0\n"
"v_exp_f32 v156, v156\n"
"v_mfma_f32_16x16x32_bf16 v[36:39], v[136:139], a[252:255], v[36:39]\n"
"v_exp_f32 v163, v163\n"
"v_mfma_f32_16x16x32_bf16 v[4:7], v[132:135], v[224:227], 0\n"
"v_exp_f32 v173, v173\n"
"v_mfma_f32_16x16x32_bf16 v[4:7], v[128:131], a[252:255], v[4:7]\n"
"ds_read_b64_tr_b16 a[250:251], v236 offset:5120\n"
"v_lshlrev_b32 v248, 6, v248\n"
"v_exp_f32 v175, v175\n"
"v_cvt_pk_bf16_f32 v114, v176, v177\n"
"v_cvt_pk_bf16_f32 v115, v178, v179\n"
"v_exp_f32 v152, v152\n"
"v_exp_f32 v155, v155\n"
"v_exp_f32 v157, v157\n"
"v_exp_f32 v161, v161\n"
"v_mfma_f32_16x16x32_bf16 a[212:215], v[124:127], a[252:255], 0\n"
"v_exp_f32 v160, v160\n"
"v_smfmac_f32_16x16x64_bf16 v[200:203], a[240:243], v[144:151], v241\n"
"ds_read_b64_tr_b16 a[246:247], v232 offset:5120\n"
"v_cvt_pk_bf16_f32 v107, v174, v175\n"
"v_mfma_f32_16x16x32_bf16 v[168:171], v[208:211], a[0:3], v[180:183]\n"
"s_waitcnt lgkmcnt(2)\n"
"ds_read_b64_tr_b16 a[244:245], v232 offset:1024\n"
"v_cvt_pk_bf16_f32 v106, v172, v173\n"
"v_mfma_f32_16x16x32_bf16 v[168:171], v[212:215], a[4:7], v[168:171]\n"
"v_accvgpr_write_b32 a163, v19\n"
"v_bfe_u32 v249, v242, 4, 2\n"
"v_mfma_f32_16x16x32_bf16 v[168:171], v[216:219], a[8:11], v[168:171]\n"
"s_add_u32 s40, s56, s54\n"
"ds_read_b64_tr_b16 a[248:249], v236 offset:1024\n"
"v_accvgpr_write_b32 a162, v18\n"
"v_mfma_f32_16x16x32_bf16 v[168:171], v[220:223], a[12:15], v[168:171]\n"
"buffer_load_dwordx4 v240, s[16:19], s40 offen offset:3072 nt lds\n"
"v_mfma_f32_16x16x32_bf16 v[172:175], v[216:219], a[24:27], v[184:187]\n"
"v_exp_f32 v158, v158\n"
"v_mfma_f32_16x16x32_bf16 v[172:175], v[220:223], a[28:31], v[172:175]\n"
"v_exp_f32 v164, v164\n"
"v_mfma_f32_16x16x32_bf16 v[172:175], v[208:211], a[16:19], v[172:175]\n"
"ds_read_b64_tr_b16 a[254:255], v233 offset:5120\n"
"v_accvgpr_write_b32 a161, v17\n"
"v_mfma_f32_16x16x32_bf16 v[172:175], v[212:215], a[20:23], v[172:175]\n"
"s_cbranch_vccnz LPCE1_%=\nLPKE1_%=:\n"
"v_cvt_pk_bf16_f32 v100, v156, v157\n"
"v_mfma_f32_16x16x32_bf16 v[176:179], v[208:211], a[32:35], v[188:191]\n"
"ds_read_b64_tr_b16 a[252:253], v233 offset:1024\n"
"v_accvgpr_write_b32 a160, v16\n"
"v_mfma_f32_16x16x32_bf16 v[176:179], v[212:215], a[36:39], v[176:179]\n"
"ds_read_b64_tr_b16 v[224:225], v237 offset:1024\n"
"ds_read_b64_tr_b16 v[226:227], v237 offset:5120\n"
"v_mfma_f32_16x16x32_bf16 v[176:179], v[216:219], a[40:43], v[176:179]\n"
"v_exp_f32 v159, v159\n"
"v_mfma_f32_16x16x32_bf16 v[176:179], v[220:223], a[44:47], v[176:179]\n"
"s_waitcnt lgkmcnt(4)\n"
"v_accvgpr_write_b32 a107, v123\n"
"v_mul_u32_u24 v249, 20, v249\n"
"v_mfma_f32_16x16x32_bf16 a[224:227], v[124:127], a[244:247], 0\n"
"v_exp_f32 v166, v166\n"
"v_mfma_f32_16x16x32_bf16 v[16:19], v[128:131], a[244:247], 0\n"
"s_waitcnt vmcnt(8)\n"
"v_xor_b32 v249, v249, v254\n"
"v_cvt_pk_bf16_f32 v109, v162, v163\n"
"v_mfma_f32_16x16x32_bf16 v[16:19], v[132:135], a[248:251], v[16:19]\n"
"s_barrier\n"
"ds_read_b128 v[216:219], v228 offset:33792\n"
"ds_read_b128 v[220:223], v229 offset:33792\n"
"v_cvt_pk_bf16_f32 v101, v158, v159\n"
"v_exp_f32 v167, v167\n"
"v_exp_f32 v169, v169\n"
"v_exp_f32 v178, v178\n"
"v_mfma_f32_16x16x32_bf16 v[48:51], v[136:139], a[244:247], 0\n"
"v_exp_f32 v165, v165\n"
"v_mfma_f32_16x16x32_bf16 v[48:51], v[140:143], a[248:251], v[48:51]\n"
"ds_read_b128 v[208:211], v228 offset:32768\n"
"ds_read_b128 v[212:215], v229 offset:32768\n"
"v_mfma_f32_16x16x32_bf16 v[80:83], v[144:147], a[244:247], 0\n"
"ds_read_b64_tr_b16 a[244:245], v234 offset:16384\n"
"ds_read_b64_tr_b16 a[246:247], v234 offset:20480\n"
"v_mfma_f32_16x16x32_bf16 v[80:83], v[148:151], a[248:251], v[80:83]\n"
"s_waitcnt lgkmcnt(6)\n"
"ds_read_b64_tr_b16 a[248:249], v238 offset:16384\n"
"ds_read_b64_tr_b16 a[250:251], v238 offset:20480\n"
"v_mfma_f32_16x16x32_bf16 v[84:87], v[148:151], v[224:227], 0\n"
"v_exp_f32 v173, v173\n"
"v_mfma_f32_16x16x32_bf16 v[84:87], v[144:147], a[252:255], v[84:87]\n"
"v_exp_f32 v175, v175\n"
"v_mfma_f32_16x16x32_bf16 v[52:55], v[140:143], v[224:227], 0\n"
"v_xor_b32 v248, v248, v242\n"
"v_lshl_add_u32 v249, v249, 4, s44\n"
"v_mfma_f32_16x16x32_bf16 v[52:55], v[136:139], a[252:255], v[52:55]\n"
"v_exp_f32 v153, v153\n"
"v_mfma_f32_16x16x32_bf16 v[20:23], v[132:135], v[224:227], 0\n"
"ds_read_b64_tr_b16 v[226:227], v239 offset:20480\n"
"v_cvt_pk_bf16_f32 v117, v166, v167\n"
"v_mfma_f32_16x16x32_bf16 v[20:23], v[128:131], a[252:255], v[20:23]\n"
"v_accvgpr_write_b32 a106, v122\n"
"v_cvt_pk_bf16_f32 v122, v152, v153\n"
"v_mfma_f32_16x16x32_bf16 a[228:231], v[124:127], a[252:255], 0\n"
"s_waitcnt lgkmcnt(3)\n"
"v_cvt_pk_bf16_f32 v123, v154, v155\n"
"s_branch LAY2_%=\n"
"LAL_%=:\n"
"v_mfma_f32_16x16x32_bf16 a[224:227], v[124:127], a[244:247], a[224:227]\n"
"v_exp_f32 v169, v169\n"
"v_mfma_f32_16x16x32_bf16 v[16:19], v[128:131], a[244:247], v[16:19]\n"
"s_waitcnt vmcnt(8)\n"
"v_exp_f32 v175, v175\n"
"v_mfma_f32_16x16x32_bf16 v[16:19], v[132:135], a[248:251], v[16:19]\n"
"s_barrier\n"
"ds_read_b128 v[220:223], v229 offset:1024\n"
"v_mfma_f32_16x16x32_bf16 v[48:51], v[136:139], a[244:247], v[48:51]\n"
"ds_read_b128 v[216:219], v228 offset:1024\n"
"v_cvt_pk_bf16_f32 v109, v162, v163\n"
"v_mfma_f32_16x16x32_bf16 v[48:51], v[140:143], a[248:251], v[48:51]\n"
"ds_read_b128 v[212:215], v229 offset:0\n"
"v_mfma_f32_16x16x32_bf16 v[80:83], v[144:147], a[244:247], v[80:83]\n"
"ds_read_b128 v[208:211], v228 offset:0\n"
"v_cvt_pk_bf16_f32 v101, v158, v159\n"
"v_mfma_f32_16x16x32_bf16 v[80:83], v[148:151], a[248:251], v[80:83]\n"
"s_waitcnt lgkmcnt(4)\n"
"v_exp_f32 v178, v178\n"
"v_mfma_f32_16x16x32_bf16 v[84:87], v[148:151], v[224:227], v[84:87]\n"
"ds_read_b64_tr_b16 a[244:245], v234 offset:49152\n"
"v_cvt_pk_bf16_f32 v117, v166, v167\n"
"v_mfma_f32_16x16x32_bf16 v[84:87], v[144:147], a[252:255], v[84:87]\n"
"ds_read_b64_tr_b16 a[250:251], v238 offset:53248\n"
"v_cvt_pk_bf16_f32 v123, v154, v155\n"
"v_mfma_f32_16x16x32_bf16 v[52:55], v[140:143], v[224:227], v[52:55]\n"
"v_exp_f32 v173, v173\n"
"v_mfma_f32_16x16x32_bf16 v[52:55], v[136:139], a[252:255], v[52:55]\n"
"ds_read_b64_tr_b16 a[248:249], v238 offset:49152\n"
"v_mfma_f32_16x16x32_bf16 v[20:23], v[132:135], v[224:227], v[20:23]\n"
"v_exp_f32 v156, v156\n"
"v_mfma_f32_16x16x32_bf16 v[20:23], v[128:131], a[252:255], v[20:23]\n"
"ds_read_b64_tr_b16 v[226:227], v239 offset:53248\n"
"v_cvt_pk_bf16_f32 v100, v156, v157\n"
"v_mfma_f32_16x16x32_bf16 a[228:231], v[124:127], a[252:255], a[228:231]\n"
"s_waitcnt lgkmcnt(4)\n"
"ds_read_b64_tr_b16 a[246:247], v234 offset:53248\n"
"v_mfma_f32_16x16x32_bf16 v[152:155], v[208:211], a[0:3], v[180:183]\n"
"ds_read_b64_tr_b16 v[224:225], v239 offset:49152\n"
"v_cvt_pk_bf16_f32 v116, v164, v165\n"
"v_mfma_f32_16x16x32_bf16 v[152:155], v[212:215], a[4:7], v[152:155]\n"
"ds_read_b64_tr_b16 a[252:253], v235 offset:49152\n"
"v_cvt_pk_bf16_f32 v108, v160, v161\n"
"v_mfma_f32_16x16x32_bf16 v[152:155], v[216:219], a[8:11], v[152:155]\n"
"s_add_u32 m0, s44, 81920\n"
"ds_read_b64_tr_b16 a[254:255], v235 offset:53248\n"
"v_mfma_f32_16x16x32_bf16 v[152:155], v[220:223], a[12:15], v[152:155]\n"
"buffer_load_dwordx4 v240, s[20:23], s56 offen nt lds\n"
"v_mfma_f32_16x16x32_bf16 v[156:159], v[216:219], a[24:27], v[184:187]\n"
"v_exp_f32 v177, v177\n"
"v_mfma_f32_16x16x32_bf16 v[156:159], v[220:223], a[28:31], v[156:159]\n"
"v_exp_f32 v168, v168\n"
"v_mfma_f32_16x16x32_bf16 v[156:159], v[208:211], a[16:19], v[156:159]\n"
"v_exp_f32 v179, v179\n"
"v_mfma_f32_16x16x32_bf16 v[156:159], v[212:215], a[20:23], v[156:159]\n"
"v_exp_f32 v174, v174\n"
"v_mfma_f32_16x16x32_bf16 v[160:163], v[208:211], a[32:35], v[188:191]\n"
"v_exp_f32 v172, v172\n"
"v_mfma_f32_16x16x32_bf16 v[160:163], v[212:215], a[36:39], v[160:163]\n"
"v_exp_f32 v171, v171\n"
"v_mfma_f32_16x16x32_bf16 v[160:163], v[216:219], a[40:43], v[160:163]\n"
"v_exp_f32 v154, v154\n"
"v_mfma_f32_16x16x32_bf16 v[160:163], v[220:223], a[44:47], v[160:163]\n"
"v_exp_f32 v170, v170\n"
"v_mfma_f32_16x16x32_bf16 v[164:167], v[216:219], a[200:203], v[192:195]\n"
"ds_read_b128 v[216:219], v228 offset:5120\n"
"v_cvt_pk_bf16_f32 v103, v170, v171\n"
"v_mfma_f32_16x16x32_bf16 v[164:167], v[220:223], a[204:207], v[164:167]\n"
"v_cvt_pk_bf16_f32 v102, v168, v169\n"
"v_cvt_pk_bf16_f32 v119, v178, v179\n"
"v_mfma_f32_16x16x32_bf16 v[164:167], v[208:211], a[192:195], v[164:167]\n"
"s_add_u32 s40, s56, s52\n"
"ds_read_b128 v[208:211], v228 offset:4096\n"
"v_mfma_f32_16x16x32_bf16 v[164:167], v[212:215], a[196:199], v[164:167]\n"
"s_waitcnt lgkmcnt(2)\n"
"buffer_load_dwordx4 v240, s[20:23], s40 offen offset:1024 nt lds\n"
"v_mfma_f32_16x16x32_bf16 a[216:219], v[120:123], a[244:247], a[216:219]\n"
"v_cvt_pk_bf16_f32 v110, v172, v173\n"
"v_cvt_pk_bf16_f32 v111, v174, v175\n"
"v_mfma_f32_16x16x32_bf16 v[8:11], v[96:99], a[244:247], v[8:11]\n"
"v_exp_f32 v176, v176\n"
"v_mfma_f32_16x16x32_bf16 v[8:11], v[100:103], a[248:251], v[8:11]\n"
"v_exp_f32 v155, v155\n"
"v_mfma_f32_16x16x32_bf16 v[40:43], v[104:107], a[244:247], v[40:43]\n"
"v_exp_f32 v152, v152\n"
"v_mfma_f32_16x16x32_bf16 v[40:43], v[108:111], a[248:251], v[40:43]\n"
"ds_read_b128 v[212:215], v229 offset:4096\n"
"v_cvt_pk_bf16_f32 v118, v176, v177\n"
"v_mfma_f32_16x16x32_bf16 v[72:75], v[112:115], a[244:247], v[72:75]\n"
"v_exp_f32 v158, v158\n"
"v_mfma_f32_16x16x32_bf16 v[72:75], v[116:119], a[248:251], v[72:75]\n"
"v_exp_f32 v163, v163\n"
"v_mfma_f32_16x16x32_bf16 v[76:79], v[116:119], v[224:227], v[76:79]\n"
"ds_read_b128 v[220:223], v229 offset:5120\n"
"v_mfma_f32_16x16x32_bf16 v[76:79], v[112:115], a[252:255], v[76:79]\n"
"v_exp_f32 v159, v159\n"
"v_mfma_f32_16x16x32_bf16 v[44:47], v[108:111], v[224:227], v[44:47]\n"
"s_add_u32 s40, s56, s53\n"
"ds_read_b64_tr_b16 a[244:245], v234 offset:50176\n"
"v_mfma_f32_16x16x32_bf16 v[44:47], v[104:107], a[252:255], v[44:47]\n"
"buffer_load_dwordx4 v240, s[20:23], s40 offen offset:2048 nt lds\n"
"v_mfma_f32_16x16x32_bf16 v[12:15], v[100:103], v[224:227], v[12:15]\n"
"v_exp_f32 v153, v153\n"
"v_mfma_f32_16x16x32_bf16 v[12:15], v[96:99], a[252:255], v[12:15]\n"
"v_exp_f32 v165, v165\n"
"v_mfma_f32_16x16x32_bf16 a[220:223], v[120:123], a[252:255], a[220:223]\n"
"v_exp_f32 v164, v164\n"
"v_smfmac_f32_16x16x64_bf16 v[196:199], a[240:243], v[96:103], v241\n"
"v_cvt_pk_bf16_f32 v129, v154, v155\n"
"v_mfma_f32_16x16x32_bf16 v[202:205], a[240:243], v[120:123], v[202:205]\n"
"s_waitcnt lgkmcnt(1)\n"
"ds_read_b64_tr_b16 a[254:255], v235 offset:54272\n"
"v_mfma_f32_16x16x32_bf16 v[168:171], v[216:219], a[8:11], v[180:183]\n"
"ds_read_b64_tr_b16 a[248:249], v238 offset:50176\n"
"v_mfma_f32_16x16x32_bf16 v[168:171], v[220:223], a[12:15], v[168:171]\n"
"ds_read_b64_tr_b16 a[246:247], v234 offset:54272\n"
"v_mfma_f32_16x16x32_bf16 v[168:171], v[208:211], a[0:3], v[168:171]\n"
"ds_read_b64_tr_b16 v[224:225], v239 offset:50176\n"
"v_cvt_pk_bf16_f32 v124, v164, v165\n"
"v_mfma_f32_16x16x32_bf16 v[168:171], v[212:215], a[4:7], v[168:171]\n"
"v_exp_f32 v166, v166\n"
"v_mfma_f32_16x16x32_bf16 v[172:175], v[208:211], a[16:19], v[184:187]\n"
"ds_read_b64_tr_b16 a[252:253], v235 offset:50176\n"
"v_cvt_pk_bf16_f32 v128, v152, v153\n"
"v_mfma_f32_16x16x32_bf16 v[172:175], v[212:215], a[20:23], v[172:175]\n"
"ds_read_b64_tr_b16 v[226:227], v239 offset:54272\n"
"v_cvt_pk_bf16_f32 v137, v158, v159\n"
"v_mfma_f32_16x16x32_bf16 v[172:175], v[216:219], a[24:27], v[172:175]\n"
"s_add_u32 s40, s56, s54\n"
"ds_read_b64_tr_b16 a[250:251], v238 offset:54272\n"
"v_mfma_f32_16x16x32_bf16 v[172:175], v[220:223], a[28:31], v[172:175]\n"
"buffer_load_dwordx4 v240, s[20:23], s40 offen offset:3072 nt lds\n"
"v_mfma_f32_16x16x32_bf16 v[176:179], v[216:219], a[40:43], v[188:191]\n"
"s_add_u32 s55, s55, s59\n"
"v_exp_f32 v167, v167\n"
"v_mfma_f32_16x16x32_bf16 v[176:179], v[220:223], a[44:47], v[176:179]\n"
"s_add_u32 s41, s55, s57\n"
"v_exp_f32 v161, v161\n"
"v_mfma_f32_16x16x32_bf16 v[176:179], v[208:211], a[32:35], v[176:179]\n"
"s_and_b32 s41, s41, s58\n"
"v_exp_f32 v157, v157\n"
"v_mfma_f32_16x16x32_bf16 v[176:179], v[212:215], a[36:39], v[176:179]\n"
"s_andn2_b32 s56, s55, s58\n"
"v_exp_f32 v160, v160\n"
"v_mfma_f32_16x16x32_bf16 v[152:155], v[208:211], a[192:195], v[192:195]\n"
"s_or_b32 s56, s56, s41\n"
"ds_read_b128 v[208:211], v230 offset:0\n"
"v_mfma_f32_16x16x32_bf16 v[152:155], v[212:215], a[196:199], v[152:155]\n"
"v_exp_f32 v172, v172\n"
"v_mfma_f32_16x16x32_bf16 v[152:155], v[216:219], a[200:203], v[152:155]\n"
"ds_read_b128 v[216:219], v230 offset:1024\n"
"v_mfma_f32_16x16x32_bf16 v[152:155], v[220:223], a[204:207], v[152:155]\n"
"s_waitcnt lgkmcnt(2)\n"
"v_exp_f32 v162, v162\n"
"v_mfma_f32_16x16x32_bf16 a[232:235], v[120:123], a[244:247], a[232:235]\n"
"ds_read_b128 v[212:215], v231 offset:0\n"
"v_mfma_f32_16x16x32_bf16 v[24:27], v[96:99], a[244:247], v[24:27]\n"
"s_add_u32 m0, s44, 32768\n"
"ds_read_b128 v[220:223], v231 offset:1024\n"
"v_mfma_f32_16x16x32_bf16 v[24:27], v[100:103], a[248:251], v[24:27]\n"
"buffer_load_dwordx4 v240, s[16:19], s56 offen nt lds\n"
"v_mfma_f32_16x16x32_bf16 v[56:59], v[104:107], a[244:247], v[56:59]\n"
"v_exp_f32 v171, v171\n"
"v_mfma_f32_16x16x32_bf16 v[56:59], v[108:111], a[248:251], v[56:59]\n"
"v_exp_f32 v169, v169\n"
"v_mfma_f32_16x16x32_bf16 v[88:91], v[112:115], a[244:247], v[88:91]\n"
"v_exp_f32 v154, v154\n"
"v_mfma_f32_16x16x32_bf16 v[88:91], v[116:119], a[248:251], v[88:91]\n"
"v_exp_f32 v156, v156\n"
"v_mfma_f32_16x16x32_bf16 v[92:95], v[116:119], v[224:227], v[92:95]\n"
"ds_read_b64_tr_b16 a[244:245], v232 offset:49152\n"
"v_cvt_pk_bf16_f32 v145, v162, v163\n"
"v_mfma_f32_16x16x32_bf16 v[92:95], v[112:115], a[252:255], v[92:95]\n"
"ds_read_b64_tr_b16 a[250:251], v236 offset:53248\n"
"v_cvt_pk_bf16_f32 v136, v156, v157\n"
"v_mfma_f32_16x16x32_bf16 v[60:63], v[108:111], v[224:227], v[60:63]\n"
"ds_read_b64_tr_b16 a[246:247], v232 offset:53248\n"
"v_cvt_pk_bf16_f32 v144, v160, v161\n"
"v_mfma_f32_16x16x32_bf16 v[60:63], v[104:107], a[252:255], v[60:63]\n"
"v_exp_f32 v152, v152\n"
"v_mfma_f32_16x16x32_bf16 v[28:31], v[100:103], v[224:227], v[28:31]\n"
"ds_read_b64_tr_b16 v[224:225], v237 offset:49152\n"
"ds_read_b64_tr_b16 v[226:227], v237 offset:53248\n"
"v_mfma_f32_16x16x32_bf16 v[28:31], v[96:99], a[252:255], v[28:31]\n"
"ds_read_b64_tr_b16 a[248:249], v236 offset:49152\n"
"v_cvt_pk_bf16_f32 v125, v166, v167\n"
"v_mfma_f32_16x16x32_bf16 a[236:239], v[120:123], a[252:255], a[236:239]\n"
"s_add_u32 s40, s56, s52\n"
"ds_read_b64_tr_b16 a[252:253], v233 offset:49152\n"
"v_smfmac_f32_16x16x64_bf16 v[198:201], a[240:243], v[104:111], v241\n"
"s_waitcnt lgkmcnt(7)\n"
"buffer_load_dwordx4 v240, s[16:19], s40 offen offset:1024 nt lds\n"
"v_mfma_f32_16x16x32_bf16 v[156:159], v[208:211], a[0:3], v[180:183]\n"
"v_exp_f32 v175, v175\n"
"v_mfma_f32_16x16x32_bf16 v[156:159], v[212:215], a[4:7], v[156:159]\n"
"v_exp_f32 v173, v173\n"
"v_mfma_f32_16x16x32_bf16 v[156:159], v[216:219], a[8:11], v[156:159]\n"
"v_exp_f32 v168, v168\n"
"v_mfma_f32_16x16x32_bf16 v[156:159], v[220:223], a[12:15], v[156:159]\n"
"ds_read_b64_tr_b16 a[254:255], v233 offset:53248\n"
"v_mfma_f32_16x16x32_bf16 v[160:163], v[216:219], a[24:27], v[184:187]\n"
"v_exp_f32 v170, v170\n"
"v_mfma_f32_16x16x32_bf16 v[160:163], v[220:223], a[28:31], v[160:163]\n"
"v_exp_f32 v178, v178\n"
"v_mfma_f32_16x16x32_bf16 v[160:163], v[208:211], a[16:19], v[160:163]\n"
"v_exp_f32 v177, v177\n"
"v_mfma_f32_16x16x32_bf16 v[160:163], v[212:215], a[20:23], v[160:163]\n"
"v_exp_f32 v176, v176\n"
"v_mfma_f32_16x16x32_bf16 v[164:167], v[208:211], a[32:35], v[188:191]\n"
"ds_read_b128 v[208:211], v230 offset:4096\n"
"v_cvt_pk_bf16_f32 v131, v170, v171\n"
"v_mfma_f32_16x16x32_bf16 v[164:167], v[212:215], a[36:39], v[164:167]\n"
"ds_read_b128 v[212:215], v231 offset:4096\n"
"v_mfma_f32_16x16x32_bf16 v[164:167], v[216:219], a[40:43], v[164:167]\n"
"v_exp_f32 v153, v153\n"
"v_mfma_f32_16x16x32_bf16 v[164:167], v[220:223], a[44:47], v[164:167]\n"
"s_waitcnt lgkmcnt(2)\n"
"ds_read_b128 v[220:223], v231 offset:5120\n"
"v_cvt_pk_bf16_f32 v138, v172, v173\n"
"v_mfma_f32_16x16x32_bf16 a[208:211], v[120:123], a[244:247], a[208:211]\n"
"v_exp_f32 v174, v174\n"
"v_mfma_f32_16x16x32_bf16 v[0:3], v[96:99], a[244:247], v[0:3]\n"
"s_add_u32 s40, s56, s53\n"
"ds_read_b128 v[216:219], v230 offset:5120\n"
"v_mfma_f32_16x16x32_bf16 v[0:3], v[100:103], a[248:251], v[0:3]\n"
"buffer_load_dwordx4 v240, s[16:19], s40 offen offset:2048 nt lds\n"
"v_mfma_f32_16x16x32_bf16 v[32:35], v[104:107], a[244:247], v[32:35]\n"
"v_exp_f32 v163, v163\n"
"v_mfma_f32_16x16x32_bf16 v[32:35], v[108:111], a[248:251], v[32:35]\n"
"v_exp_f32 v161, v161\n"
"v_mfma_f32_16x16x32_bf16 v[64:67], v[112:115], a[244:247], v[64:67]\n"
"v_exp_f32 v155, v155\n"
"v_mfma_f32_16x16x32_bf16 v[64:67], v[116:119], a[248:251], v[64:67]\n"
"v_exp_f32 v166, v166\n"
"v_mfma_f32_16x16x32_bf16 v[68:71], v[116:119], v[224:227], v[68:71]\n"
"ds_read_b64_tr_b16 a[250:251], v236 offset:54272\n"
"v_cvt_pk_bf16_f32 v139, v174, v175\n"
"v_mfma_f32_16x16x32_bf16 v[68:71], v[112:115], a[252:255], v[68:71]\n"
"ds_read_b64_tr_b16 a[246:247], v232 offset:54272\n"
"v_cvt_pk_bf16_f32 v130, v168, v169\n"
"v_mfma_f32_16x16x32_bf16 v[36:39], v[108:111], v[224:227], v[36:39]\n"
"v_exp_f32 v179, v179\n"
"v_mfma_f32_16x16x32_bf16 v[36:39], v[104:107], a[252:255], v[36:39]\n"
"ds_read_b64_tr_b16 a[244:245], v232 offset:50176\n"
"v_cvt_pk_bf16_f32 v126, v152, v153\n"
"v_mfma_f32_16x16x32_bf16 v[4:7], v[100:103], v[224:227], v[4:7]\n"
"s_add_u32 s40, s56, s54\n"
"ds_read_b64_tr_b16 a[248:249], v236 offset:50176\n"
"v_mfma_f32_16x16x32_bf16 v[4:7], v[96:99], a[252:255], v[4:7]\n"
"buffer_load_dwordx4 v240, s[16:19], s40 offen offset:3072 nt lds\n"
"v_mfma_f32_16x16x32_bf16 a[212:215], v[120:123], a[252:255], a[212:215]\n"
"v_exp_f32 v162, v162\n"
"v_smfmac_f32_16x16x64_bf16 v[200:203], a[240:243], v[112:119], v241\n"
"s_waitcnt lgkmcnt(4)\n"
"v_cvt_pk_bf16_f32 v146, v176, v177\n"
"v_mfma_f32_16x16x32_bf16 v[168:171], v[208:211], a[0:3], v[180:183]\n"
"v_exp_f32 v157, v157\n"
"v_mfma_f32_16x16x32_bf16 v[168:171], v[212:215], a[4:7], v[168:171]\n"
"v_exp_f32 v165, v165\n"
"v_mfma_f32_16x16x32_bf16 v[168:171], v[216:219], a[8:11], v[168:171]\n"
"ds_read_b64_tr_b16 a[254:255], v233 offset:54272\n"
"ds_read_b64_tr_b16 v[226:227], v237 offset:54272\n"
"v_mfma_f32_16x16x32_bf16 v[168:171], v[220:223], a[12:15], v[168:171]\n"
"ds_read_b64_tr_b16 a[252:253], v233 offset:50176\n"
"v_cvt_pk_bf16_f32 v147, v178, v179\n"
"v_mfma_f32_16x16x32_bf16 v[172:175], v[216:219], a[24:27], v[184:187]\n"
"ds_read_b64_tr_b16 v[224:225], v237 offset:50176\n"
"v_mfma_f32_16x16x32_bf16 v[172:175], v[220:223], a[28:31], v[172:175]\n"
"v_exp_f32 v160, v160\n"
"v_mfma_f32_16x16x32_bf16 v[172:175], v[208:211], a[16:19], v[172:175]\n"
"v_exp_f32 v158, v158\n"
"v_mfma_f32_16x16x32_bf16 v[172:175], v[212:215], a[20:23], v[172:175]\n"
"s_cbranch_vccnz LPCL0_%=\nLPKL0_%=:\n"
"v_mfma_f32_16x16x32_bf16 v[176:179], v[208:211], a[32:35], v[188:191]\n"
"v_exp_f32 v159, v159\n"
"v_mfma_f32_16x16x32_bf16 v[176:179], v[212:215], a[36:39], v[176:179]\n"
"v_exp_f32 v167, v167\n"
"v_mfma_f32_16x16x32_bf16 v[176:179], v[216:219], a[40:43], v[176:179]\n"
"v_exp_f32 v164, v164\n"
"v_mfma_f32_16x16x32_bf16 v[176:179], v[220:223], a[44:47], v[176:179]\n"
"s_waitcnt lgkmcnt(1)\n"
"v_mfma_f32_16x16x32_bf16 a[224:227], v[120:123], a[244:247], a[224:227]\n"
"v_exp_f32 v169, v169\n"
"v_mfma_f32_16x16x32_bf16 v[16:19], v[96:99], a[244:247], v[16:19]\n"
"s_waitcnt vmcnt(8)\n"
"v_exp_f32 v175, v175\n"
"v_mfma_f32_16x16x32_bf16 v[16:19], v[100:103], a[248:251], v[16:19]\n"
"s_barrier\n"
"ds_read_b128 v[220:223], v229 offset:17408\n"
"v_mfma_f32_16x16x32_bf16 v[48:51], v[104:107], a[244:247], v[48:51]\n"
"ds_read_b128 v[216:219], v228 offset:17408\n"
"v_cvt_pk_bf16_f32 v141, v162, v163\n"
"v_mfma_f32_16x16x32_bf16 v[48:51], v[108:111], a[248:251], v[48:51]\n"
"ds_read_b128 v[212:215], v229 offset:16384\n"
"v_mfma_f32_16x16x32_bf16 v[80:83], v[112:115], a[244:247], v[80:83]\n"
"ds_read_b128 v[208:211], v228 offset:16384\n"
"v_cvt_pk_bf16_f32 v133, v158, v159\n"
"v_mfma_f32_16x16x32_bf16 v[80:83], v[116:119], a[248:251], v[80:83]\n"
"s_waitcnt lgkmcnt(4)\n"
"v_exp_f32 v178, v178\n"
"v_mfma_f32_16x16x32_bf16 v[84:87], v[116:119], v[224:227], v[84:87]\n"
"ds_read_b64_tr_b16 a[244:245], v234 offset:0\n"
"v_cvt_pk_bf16_f32 v149, v166, v167\n"
"v_mfma_f32_16x16x32_bf16 v[84:87], v[112:115], a[252:255], v[84:87]\n"
"ds_read_b64_tr_b16 a[250:251], v238 offset:4096\n"
"v_cvt_pk_bf16_f32 v127, v154, v155\n"
"v_mfma_f32_16x16x32_bf16 v[52:55], v[108:111], v[224:227], v[52:55]\n"
"v_exp_f32 v173, v173\n"
"v_mfma_f32_16x16x32_bf16 v[52:55], v[104:107], a[252:255], v[52:55]\n"
"ds_read_b64_tr_b16 a[248:249], v238 offset:0\n"
"v_mfma_f32_16x16x32_bf16 v[20:23], v[100:103], v[224:227], v[20:23]\n"
"v_exp_f32 v156, v156\n"
"v_mfma_f32_16x16x32_bf16 v[20:23], v[96:99], a[252:255], v[20:23]\n"
"ds_read_b64_tr_b16 v[226:227], v239 offset:4096\n"
"v_cvt_pk_bf16_f32 v132, v156, v157\n"
"v_mfma_f32_16x16x32_bf16 a[228:231], v[120:123], a[252:255], a[228:231]\n"
"s_waitcnt lgkmcnt(4)\n"
"ds_read_b64_tr_b16 a[246:247], v234 offset:4096\n"
"v_mfma_f32_16x16x32_bf16 v[152:155], v[208:211], a[0:3], v[180:183]\n"
"ds_read_b64_tr_b16 v[224:225], v239 offset:0\n"
"v_cvt_pk_bf16_f32 v148, v164, v165\n"
"v_mfma_f32_16x16x32_bf16 v[152:155], v[212:215], a[4:7], v[152:155]\n"
"ds_read_b64_tr_b16 a[252:253], v235 offset:0\n"
"v_cvt_pk_bf16_f32 v140, v160, v161\n"
"v_mfma_f32_16x16x32_bf16 v[152:155], v[216:219], a[8:11], v[152:155]\n"
"s_add_u32 m0, s44, 98304\n"
"ds_read_b64_tr_b16 a[254:255], v235 offset:4096\n"
"v_mfma_f32_16x16x32_bf16 v[152:155], v[220:223], a[12:15], v[152:155]\n"
"buffer_load_dwordx4 v240, s[20:23], s56 offen nt lds\n"
"v_mfma_f32_16x16x32_bf16 v[156:159], v[216:219], a[24:27], v[184:187]\n"
"v_exp_f32 v177, v177\n"
"v_mfma_f32_16x16x32_bf16 v[156:159], v[220:223], a[28:31], v[156:159]\n"
"v_exp_f32 v168, v168\n"
"v_mfma_f32_16x16x32_bf16 v[156:159], v[208:211], a[16:19], v[156:159]\n"
"v_exp_f32 v179, v179\n"
"v_mfma_f32_16x16x32_bf16 v[156:159], v[212:215], a[20:23], v[156:159]\n"
"v_exp_f32 v174, v174\n"
"v_mfma_f32_16x16x32_bf16 v[160:163], v[208:211], a[32:35], v[188:191]\n"
"v_exp_f32 v172, v172\n"
"v_mfma_f32_16x16x32_bf16 v[160:163], v[212:215], a[36:39], v[160:163]\n"
"v_exp_f32 v171, v171\n"
"v_mfma_f32_16x16x32_bf16 v[160:163], v[216:219], a[40:43], v[160:163]\n"
"v_exp_f32 v154, v154\n"
"v_mfma_f32_16x16x32_bf16 v[160:163], v[220:223], a[44:47], v[160:163]\n"
"v_exp_f32 v170, v170\n"
"v_mfma_f32_16x16x32_bf16 v[164:167], v[216:219], a[200:203], v[192:195]\n"
"ds_read_b128 v[216:219], v228 offset:21504\n"
"v_cvt_pk_bf16_f32 v135, v170, v171\n"
"v_mfma_f32_16x16x32_bf16 v[164:167], v[220:223], a[204:207], v[164:167]\n"
"v_cvt_pk_bf16_f32 v134, v168, v169\n"
"v_cvt_pk_bf16_f32 v151, v178, v179\n"
"v_mfma_f32_16x16x32_bf16 v[164:167], v[208:211], a[192:195], v[164:167]\n"
"s_add_u32 s40, s56, s52\n"
"ds_read_b128 v[208:211], v228 offset:20480\n"
"v_mfma_f32_16x16x32_bf16 v[164:167], v[212:215], a[196:199], v[164:167]\n"
"s_waitcnt lgkmcnt(2)\n"
"buffer_load_dwordx4 v240, s[20:23], s40 offen offset:1024 nt lds\n"
"v_mfma_f32_16x16x32_bf16 a[216:219], v[124:127], a[244:247], a[216:219]\n"
"v_cvt_pk_bf16_f32 v142, v172, v173\n"
"v_cvt_pk_bf16_f32 v143, v174, v175\n"
"v_mfma_f32_16x16x32_bf16 v[8:11], v[128:131], a[244:247], v[8:11]\n"
"v_exp_f32 v176, v176\n"
"v_mfma_f32_16x16x32_bf16 v[8:11], v[132:135], a[248:251], v[8:11]\n"
"v_exp_f32 v155, v155\n"
"v_mfma_f32_16x16x32_bf16 v[40:43], v[136:139], a[244:247], v[40:43]\n"
"v_exp_f32 v152, v152\n"
"v_mfma_f32_16x16x32_bf16 v[40:43], v[140:143], a[248:251], v[40:43]\n"
"ds_read_b128 v[212:215], v229 offset:20480\n"
"v_cvt_pk_bf16_f32 v150, v176, v177\n"
"v_mfma_f32_16x16x32_bf16 v[72:75], v[144:147], a[244:247], v[72:75]\n"
"v_exp_f32 v158, v158\n"
"v_mfma_f32_16x16x32_bf16 v[72:75], v[148:151], a[248:251], v[72:75]\n"
"v_exp_f32 v163, v163\n"
"v_mfma_f32_16x16x32_bf16 v[76:79], v[148:151], v[224:227], v[76:79]\n"
"ds_read_b128 v[220:223], v229 offset:21504\n"
"v_mfma_f32_16x16x32_bf16 v[76:79], v[144:147], a[252:255], v[76:79]\n"
"v_exp_f32 v159, v159\n"
"v_mfma_f32_16x16x32_bf16 v[44:47], v[140:143], v[224:227], v[44:47]\n"
"s_add_u32 s40, s56, s53\n"
"ds_read_b64_tr_b16 a[244:245], v234 offset:1024\n"
"v_mfma_f32_16x16x32_bf16 v[44:47], v[136:139], a[252:255], v[44:47]\n"
"buffer_load_dwordx4 v240, s[20:23], s40 offen offset:2048 nt lds\n"
"v_mfma_f32_16x16x32_bf16 v[12:15], v[132:135], v[224:227], v[12:15]\n"
"v_exp_f32 v153, v153\n"
"v_mfma_f32_16x16x32_bf16 v[12:15], v[128:131], a[252:255], v[12:15]\n"
"v_exp_f32 v165, v165\n"
"v_mfma_f32_16x16x32_bf16 a[220:223], v[124:127], a[252:255], a[220:223]\n"
"v_exp_f32 v164, v164\n"
"v_smfmac_f32_16x16x64_bf16 v[196:199], a[240:243], v[128:135], v241\n"
"v_cvt_pk_bf16_f32 v97, v154, v155\n"
"v_mfma_f32_16x16x32_bf16 v[202:205], a[240:243], v[124:127], v[202:205]\n"
"s_waitcnt lgkmcnt(1)\n"
"ds_read_b64_tr_b16 a[254:255], v235 offset:5120\n"
"v_mfma_f32_16x16x32_bf16 v[168:171], v[216:219], a[8:11], v[180:183]\n"
"ds_read_b64_tr_b16 a[248:249], v238 offset:1024\n"
"v_mfma_f32_16x16x32_bf16 v[168:171], v[220:223], a[12:15], v[168:171]\n"
"ds_read_b64_tr_b16 a[246:247], v234 offset:5120\n"
"v_mfma_f32_16x16x32_bf16 v[168:171], v[208:211], a[0:3], v[168:171]\n"
"ds_read_b64_tr_b16 v[224:225], v239 offset:1024\n"
"v_cvt_pk_bf16_f32 v120, v164, v165\n"
"v_mfma_f32_16x16x32_bf16 v[168:171], v[212:215], a[4:7], v[168:171]\n"
"v_exp_f32 v166, v166\n"
"v_mfma_f32_16x16x32_bf16 v[172:175], v[208:211], a[16:19], v[184:187]\n"
"ds_read_b64_tr_b16 a[252:253], v235 offset:1024\n"
"v_cvt_pk_bf16_f32 v96, v152, v153\n"
"v_mfma_f32_16x16x32_bf16 v[172:175], v[212:215], a[20:23], v[172:175]\n"
"ds_read_b64_tr_b16 v[226:227], v239 offset:5120\n"
"v_cvt_pk_bf16_f32 v105, v158, v159\n"
"v_mfma_f32_16x16x32_bf16 v[172:175], v[216:219], a[24:27], v[172:175]\n"
"s_add_u32 s40, s56, s54\n"
"ds_read_b64_tr_b16 a[250:251], v238 offset:5120\n"
"v_mfma_f32_16x16x32_bf16 v[172:175], v[220:223], a[28:31], v[172:175]\n"
"buffer_load_dwordx4 v240, s[20:23], s40 offen offset:3072 nt lds\n"
"v_mfma_f32_16x16x32_bf16 v[176:179], v[216:219], a[40:43], v[188:191]\n"
"s_add_u32 s55, s55, s59\n"
"v_exp_f32 v167, v167\n"
"v_mfma_f32_16x16x32_bf16 v[176:179], v[220:223], a[44:47], v[176:179]\n"
"s_add_u32 s41, s55, s57\n"
"v_exp_f32 v161, v161\n"
"v_mfma_f32_16x16x32_bf16 v[176:179], v[208:211], a[32:35], v[176:179]\n"
"s_and_b32 s41, s41, s58\n"
"v_exp_f32 v157, v157\n"
"v_mfma_f32_16x16x32_bf16 v[176:179], v[212:215], a[36:39], v[176:179]\n"
"s_andn2_b32 s56, s55, s58\n"
"v_exp_f32 v160, v160\n"
"v_mfma_f32_16x16x32_bf16 v[152:155], v[208:211], a[192:195], v[192:195]\n"
"s_or_b32 s56, s56, s41\n"
"ds_read_b128 v[208:211], v230 offset:16384\n"
"v_mfma_f32_16x16x32_bf16 v[152:155], v[212:215], a[196:199], v[152:155]\n"
"v_exp_f32 v172, v172\n"
"v_mfma_f32_16x16x32_bf16 v[152:155], v[216:219], a[200:203], v[152:155]\n"
"ds_read_b128 v[216:219], v230 offset:17408\n"
"v_mfma_f32_16x16x32_bf16 v[152:155], v[220:223], a[204:207], v[152:155]\n"
"s_waitcnt lgkmcnt(2)\n"
"v_exp_f32 v162, v162\n"
"v_mfma_f32_16x16x32_bf16 a[232:235], v[124:127], a[244:247], a[232:235]\n"
"ds_read_b128 v[212:215], v231 offset:16384\n"
"v_mfma_f32_16x16x32_bf16 v[24:27], v[128:131], a[244:247], v[24:27]\n"
"s_add_u32 m0, s44, 49152\n"
"ds_read_b128 v[220:223], v231 offset:17408\n"
"v_mfma_f32_16x16x32_bf16 v[24:27], v[132:135], a[248:251], v[24:27]\n"
"buffer_load_dwordx4 v240, s[16:19], s56 offen nt lds\n"
"v_mfma_f32_16x16x32_bf16 v[56:59], v[136:139], a[244:247], v[56:59]\n"
"v_exp_f32 v171, v171\n"
"v_mfma_f32_16x16x32_bf16 v[56:59], v[140:143], a[248:251], v[56:59]\n"
"v_exp_f32 v169, v169\n"
"v_mfma_f32_16x16x32_bf16 v[88:91], v[144:147], a[244:247], v[88:91]\n"
"v_exp_f32 v154, v154\n"
"v_mfma_f32_16x16x32_bf16 v[88:91], v[148:151], a[248:251], v[88:91]\n"
"v_exp_f32 v156, v156\n"
"v_mfma_f32_16x16x32_bf16 v[92:95], v[148:151], v[224:227], v[92:95]\n"
"ds_read_b64_tr_b16 a[244:245], v232 offset:0\n"
"v_cvt_pk_bf16_f32 v113, v162, v163\n"
"v_mfma_f32_16x16x32_bf16 v[92:95], v[144:147], a[252:255], v[92:95]\n"
"ds_read_b64_tr_b16 a[250:251], v236 offset:4096\n"
"v_cvt_pk_bf16_f32 v104, v156, v157\n"
"v_mfma_f32_16x16x32_bf16 v[60:63], v[140:143], v[224:227], v[60:63]\n"
"ds_read_b64_tr_b16 a[246:247], v232 offset:4096\n"
"v_cvt_pk_bf16_f32 v112, v160, v161\n"
"v_mfma_f32_16x16x32_bf16 v[60:63], v[136:139], a[252:255], v[60:63]\n"
"v_exp_f32 v152, v152\n"
"v_mfma_f32_16x16x32_bf16 v[28:31], v[132:135], v[224:227], v[28:31]\n"
"ds_read_b64_tr_b16 v[224:225], v237 offset:0\n"
"ds_read_b64_tr_b16 v[226:227], v237 offset:4096\n"
"v_mfma_f32_16x16x32_bf16 v[28:31], v[128:131], a[252:255], v[28:31]\n"
"ds_read_b64_tr_b16 a[248:249], v236 offset:0\n"
"v_cvt_pk_bf16_f32 v121, v166, v167\n"
"v_mfma_f32_16x16x32_bf16 a[236:239], v[124:127], a[252:255], a[236:239]\n"
"s_add_u32 s40, s56, s52\n"
"ds_read_b64_tr_b16 a[252:253], v233 offset:0\n"
"v_smfmac_f32_16x16x64_bf16 v[198:201], a[240:243], v[136:143], v241\n"
"s_waitcnt lgkmcnt(7)\n"
"buffer_load_dwordx4 v240, s[16:19], s40 offen offset:1024 nt lds\n"
"v_mfma_f32_16x16x32_bf16 v[156:159], v[208:211], a[0:3], v[180:183]\n"
"v_exp_f32 v175, v175\n"
"v_mfma_f32_16x16x32_bf16 v[156:159], v[212:215], a[4:7], v[156:159]\n"
"v_exp_f32 v173, v173\n"
"v_mfma_f32_16x16x32_bf16 v[156:159], v[216:219], a[8:11], v[156:159]\n"
"v_exp_f32 v168, v168\n"
"v_mfma_f32_16x16x32_bf16 v[156:159], v[220:223], a[12:15], v[156:159]\n"
"ds_read_b64_tr_b16 a[254:255], v233 offset:4096\n"
"v_mfma_f32_16x16x32_bf16 v[160:163], v[216:219], a[24:27], v[184:187]\n"
"v_exp_f32 v170, v170\n"
"v_mfma_f32_16x16x32_bf16 v[160:163], v[220:223], a[28:31], v[160:163]\n"
"v_exp_f32 v178, v178\n"
"v_mfma_f32_16x16x32_bf16 v[160:163], v[208:211], a[16:19], v[160:163]\n"
"v_exp_f32 v177, v177\n"
"v_mfma_f32_16x16x32_bf16 v[160:163], v[212:215], a[20:23], v[160:163]\n"
"v_exp_f32 v176, v176\n"
"v_mfma_f32_16x16x32_bf16 v[164:167], v[208:211], a[32:35], v[188:191]\n"
"ds_read_b128 v[208:211], v230 offset:20480\n"
"v_cvt_pk_bf16_f32 v99, v170, v171\n"
"v_mfma_f32_16x16x32_bf16 v[164:167], v[212:215], a[36:39], v[164:167]\n"
"ds_read_b128 v[212:215], v231 offset:20480\n"
"v_mfma_f32_16x16x32_bf16 v[164:167], v[216:219], a[40:43], v[164:167]\n"
"v_exp_f32 v153, v153\n"
"v_mfma_f32_16x16x32_bf16 v[164:167], v[220:223], a[44:47], v[164:167]\n"
"s_waitcnt lgkmcnt(2)\n"
"ds_read_b128 v[220:223], v231 offset:21504\n"
"v_cvt_pk_bf16_f32 v106, v172, v173\n"
"v_mfma_f32_16x16x32_bf16 a[208:211], v[124:127], a[244:247], a[208:211]\n"
"v_exp_f32 v174, v174\n"
"v_mfma_f32_16x16x32_bf16 v[0:3], v[128:131], a[244:247], v[0:3]\n"
"s_add_u32 s40, s56, s53\n"
"ds_read_b128 v[216:219], v230 offset:21504\n"
"v_mfma_f32_16x16x32_bf16 v[0:3], v[132:135], a[248:251], v[0:3]\n"
"buffer_load_dwordx4 v240, s[16:19], s40 offen offset:2048 nt lds\n"
"v_mfma_f32_16x16x32_bf16 v[32:35], v[136:139], a[244:247], v[32:35]\n"
"v_exp_f32 v163, v163\n"
"v_mfma_f32_16x16x32_bf16 v[32:35], v[140:143], a[248:251], v[32:35]\n"
"v_exp_f32 v161, v161\n"
"v_mfma_f32_16x16x32_bf16 v[64:67], v[144:147], a[244:247], v[64:67]\n"
"v_exp_f32 v155, v155\n"
"v_mfma_f32_16x16x32_bf16 v[64:67], v[148:151], a[248:251], v[64:67]\n"
"v_exp_f32 v166, v166\n"
"v_mfma_f32_16x16x32_bf16 v[68:71], v[148:151], v[224:227], v[68:71]\n"
"ds_read_b64_tr_b16 a[250:251], v236 offset:5120\n"
"v_cvt_pk_bf16_f32 v107, v174, v175\n"
"v_mfma_f32_16x16x32_bf16 v[68:71], v[144:147], a[252:255], v[68:71]\n"
"ds_read_b64_tr_b16 a[246:247], v232 offset:5120\n"
"v_cvt_pk_bf16_f32 v98, v168, v169\n"
"v_mfma_f32_16x16x32_bf16 v[36:39], v[140:143], v[224:227], v[36:39]\n"
"v_exp_f32 v179, v179\n"
"v_mfma_f32_16x16x32_bf16 v[36:39], v[136:139], a[252:255], v[36:39]\n"
"ds_read_b64_tr_b16 a[244:245], v232 offset:1024\n"
"v_cvt_pk_bf16_f32 v122, v152, v153\n"
"v_mfma_f32_16x16x32_bf16 v[4:7], v[132:135], v[224:227], v[4:7]\n"
"s_add_u32 s40, s56, s54\n"
"ds_read_b64_tr_b16 a[248:249], v236 offset:1024\n"
"v_mfma_f32_16x16x32_bf16 v[4:7], v[128:131], a[252:255], v[4:7]\n"
"buffer_load_dwordx4 v240, s[16:19], s40 offen offset:3072 nt lds\n"
"v_mfma_f32_16x16x32_bf16 a[212:215], v[124:127], a[252:255], a[212:215]\n"
"v_exp_f32 v162, v162\n"
"v_smfmac_f32_16x16x64_bf16 v[200:203], a[240:243], v[144:151], v241\n"
"s_waitcnt lgkmcnt(4)\n"
"v_cvt_pk_bf16_f32 v114, v176, v177\n"
"v_mfma_f32_16x16x32_bf16 v[168:171], v[208:211], a[0:3], v[180:183]\n"
"v_exp_f32 v157, v157\n"
"v_mfma_f32_16x16x32_bf16 v[168:171], v[212:215], a[4:7], v[168:171]\n"
"v_exp_f32 v165, v165\n"
"v_mfma_f32_16x16x32_bf16 v[168:171], v[216:219], a[8:11], v[168:171]\n"
"ds_read_b64_tr_b16 a[254:255], v233 offset:5120\n"
"ds_read_b64_tr_b16 v[226:227], v237 offset:5120\n"
"v_mfma_f32_16x16x32_bf16 v[168:171], v[220:223], a[12:15], v[168:171]\n"
"ds_read_b64_tr_b16 a[252:253], v233 offset:1024\n"
"v_cvt_pk_bf16_f32 v115, v178, v179\n"
"v_mfma_f32_16x16x32_bf16 v[172:175], v[216:219], a[24:27], v[184:187]\n"
"ds_read_b64_tr_b16 v[224:225], v237 offset:1024\n"
"v_mfma_f32_16x16x32_bf16 v[172:175], v[220:223], a[28:31], v[172:175]\n"
"v_exp_f32 v160, v160\n"
"v_mfma_f32_16x16x32_bf16 v[172:175], v[208:211], a[16:19], v[172:175]\n"
"v_exp_f32 v158, v158\n"
"v_mfma_f32_16x16x32_bf16 v[172:175], v[212:215], a[20:23], v[172:175]\n"
"s_cbranch_vccnz LPCL1_%=\nLPKL1_%=:\n"
"v_mfma_f32_16x16x32_bf16 v[176:179], v[208:211], a[32:35], v[188:191]\n"
"v_exp_f32 v159, v159\n"
"v_mfma_f32_16x16x32_bf16 v[176:179], v[212:215], a[36:39], v[176:179]\n"
"v_exp_f32 v167, v167\n"
"v_mfma_f32_16x16x32_bf16 v[176:179], v[216:219], a[40:43], v[176:179]\n"
"v_exp_f32 v164, v164\n"
"v_mfma_f32_16x16x32_bf16 v[176:179], v[220:223], a[44:47], v[176:179]\n"
"s_waitcnt lgkmcnt(1)\n"
"v_mfma_f32_16x16x32_bf16 a[224:227], v[124:127], a[244:247], a[224:227]\n"
"v_exp_f32 v169, v169\n"
"v_mfma_f32_16x16x32_bf16 v[16:19], v[128:131], a[244:247], v[16:19]\n"
"s_waitcnt vmcnt(8)\n"
"v_exp_f32 v175, v175\n"
"v_mfma_f32_16x16x32_bf16 v[16:19], v[132:135], a[248:251], v[16:19]\n"
"s_barrier\n"
"ds_read_b128 v[220:223], v229 offset:33792\n"
"v_mfma_f32_16x16x32_bf16 v[48:51], v[136:139], a[244:247], v[48:51]\n"
"ds_read_b128 v[216:219], v228 offset:33792\n"
"v_cvt_pk_bf16_f32 v109, v162, v163\n"
"v_mfma_f32_16x16x32_bf16 v[48:51], v[140:143], a[248:251], v[48:51]\n"
"ds_read_b128 v[212:215], v229 offset:32768\n"
"v_mfma_f32_16x16x32_bf16 v[80:83], v[144:147], a[244:247], v[80:83]\n"
"ds_read_b128 v[208:211], v228 offset:32768\n"
"v_cvt_pk_bf16_f32 v101, v158, v159\n"
"v_mfma_f32_16x16x32_bf16 v[80:83], v[148:151], a[248:251], v[80:83]\n"
"s_waitcnt lgkmcnt(4)\n"
"v_exp_f32 v178, v178\n"
"v_mfma_f32_16x16x32_bf16 v[84:87], v[148:151], v[224:227], v[84:87]\n"
"ds_read_b64_tr_b16 a[244:245], v234 offset:16384\n"
"v_cvt_pk_bf16_f32 v117, v166, v167\n"
"v_mfma_f32_16x16x32_bf16 v[84:87], v[144:147], a[252:255], v[84:87]\n"
"ds_read_b64_tr_b16 a[250:251], v238 offset:20480\n"
"v_cvt_pk_bf16_f32 v123, v154, v155\n"
"v_mfma_f32_16x16x32_bf16 v[52:55], v[140:143], v[224:227], v[52:55]\n"
"v_exp_f32 v173, v173\n"
"v_mfma_f32_16x16x32_bf16 v[52:55], v[136:139], a[252:255], v[52:55]\n"
"ds_read_b64_tr_b16 a[248:249], v238 offset:16384\n"
"v_mfma_f32_16x16x32_bf16 v[20:23], v[132:135], v[224:227], v[20:23]\n"
"v_exp_f32 v156, v156\n"
"v_mfma_f32_16x16x32_bf16 v[20:23], v[128:131], a[252:255], v[20:23]\n"
"ds_read_b64_tr_b16 v[226:227], v239 offset:20480\n"
"v_cvt_pk_bf16_f32 v100, v156, v157\n"
"v_mfma_f32_16x16x32_bf16 a[228:231], v[124:127], a[252:255], a[228:231]\n"
"s_waitcnt lgkmcnt(4)\n"
"ds_read_b64_tr_b16 a[246:247], v234 offset:20480\n"
"LAY2_%=:\n"
"v_mfma_f32_16x16x32_bf16 v[152:155], v[208:211], a[0:3], v[180:183]\n"
"ds_read_b64_tr_b16 v[224:225], v239 offset:16384\n"
"v_cvt_pk_bf16_f32 v116, v164, v165\n"
"v_mfma_f32_16x16x32_bf16 v[152:155], v[212:215], a[4:7], v[152:155]\n"
"ds_read_b64_tr_b16 a[252:253], v235 offset:16384\n"
"v_cvt_pk_bf16_f32 v108, v160, v161\n"
"v_mfma_f32_16x16x32_bf16 v[152:155], v[216:219], a[8:11], v[152:155]\n"
"s_add_u32 m0, s44, 114688\n"
"ds_read_b64_tr_b16 a[254:255], v235 offset:20480\n"
"v_mfma_f32_16x16x32_bf16 v[152:155], v[220:223], a[12:15], v[152:155]\n"
"buffer_load_dwordx4 v240, s[20:23], s56 offen nt lds\n"
"v_mfma_f32_16x16x32_bf16 v[156:159], v[216:219], a[24:27], v[184:187]\n"
"v_exp_f32 v177, v177\n"
"v_mfma_f32_16x16x32_bf16 v[156:159], v[220:223], a[28:31], v[156:159]\n"
"v_exp_f32 v168, v168\n"
"v_mfma_f32_16x16x32_bf16 v[156:159], v[208:211], a[16:19], v[156:159]\n"
"v_exp_f32 v179, v179\n"
"v_mfma_f32_16x16x32_bf16 v[156:159], v[212:215], a[20:23], v[156:159]\n"
"v_exp_f32 v174, v174\n"
"v_mfma_f32_16x16x32_bf16 v[160:163], v[208:211], a[32:35], v[188:191]\n"
"v_exp_f32 v172, v172\n"
"v_mfma_f32_16x16x32_bf16 v[160:163], v[212:215], a[36:39], v[160:163]\n"
"v_exp_f32 v171, v171\n"
"v_mfma_f32_16x16x32_bf16 v[160:163], v[216:219], a[40:43], v[160:163]\n"
"v_exp_f32 v154, v154\n"
"v_mfma_f32_16x16x32_bf16 v[160:163], v[220:223], a[44:47], v[160:163]\n"
"v_exp_f32 v170, v170\n"
"v_mfma_f32_16x16x32_bf16 v[164:167], v[216:219], a[200:203], v[192:195]\n"
"ds_read_b128 v[216:219], v228 offset:37888\n"
"v_cvt_pk_bf16_f32 v103, v170, v171\n"
"v_mfma_f32_16x16x32_bf16 v[164:167], v[220:223], a[204:207], v[164:167]\n"
"v_cvt_pk_bf16_f32 v102, v168, v169\n"
"v_cvt_pk_bf16_f32 v119, v178, v179\n"
"v_mfma_f32_16x16x32_bf16 v[164:167], v[208:211], a[192:195], v[164:167]\n"
"s_add_u32 s40, s56, s52\n"
"ds_read_b128 v[208:211], v228 offset:36864\n"
"v_mfma_f32_16x16x32_bf16 v[164:167], v[212:215], a[196:199], v[164:167]\n"
"s_waitcnt lgkmcnt(2)\n"
"buffer_load_dwordx4 v240, s[20:23], s40 offen offset:1024 nt lds\n"
"v_mfma_f32_16x16x32_bf16 a[216:219], v[120:123], a[244:247], a[216:219]\n"
"v_cvt_pk_bf16_f32 v110, v172, v173\n"
"v_cvt_pk_bf16_f32 v111, v174, v175\n"
"v_mfma_f32_16x16x32_bf16 v[8:11], v[96:99], a[244:247], v[8:11]\n"
"s_waitcnt lgkmcnt(2)\n"
"v_exp_f32 v176, v176\n"
"v_mfma_f32_16x16x32_bf16 v[8:11], v[100:103], a[248:251], v[8:11]\n"
"v_exp_f32 v155, v155\n"
"v_mfma_f32_16x16x32_bf16 v[40:43], v[104:107], a[244:247], v[40:43]\n"
"v_exp_f32 v152, v152\n"
"v_mfma_f32_16x16x32_bf16 v[40:43], v[108:111], a[248:251], v[40:43]\n"
"ds_read_b128 v[212:215], v229 offset:36864\n"
"v_cvt_pk_bf16_f32 v118, v176, v177\n"
"v_mfma_f32_16x16x32_bf16 v[72:75], v[112:115], a[244:247], v[72:75]\n"
"v_exp_f32 v158, v158\n"
"v_mfma_f32_16x16x32_bf16 v[72:75], v[116:119], a[248:251], v[72:75]\n"
"v_exp_f32 v163, v163\n"
"v_mfma_f32_16x16x32_bf16 v[76:79], v[116:119], v[224:227], v[76:79]\n"
"ds_read_b128 v[220:223], v229 offset:37888\n"
"v_mfma_f32_16x16x32_bf16 v[76:79], v[112:115], a[252:255], v[76:79]\n"
"v_exp_f32 v159, v159\n"
"v_mfma_f32_16x16x32_bf16 v[44:47], v[108:111], v[224:227], v[44:47]\n"
"s_add_u32 s40, s56, s53\n"
"ds_read_b64_tr_b16 a[244:245], v234 offset:17408\n"
"v_mfma_f32_16x16x32_bf16 v[44:47], v[104:107], a[252:255], v[44:47]\n"
"buffer_load_dwordx4 v240, s[20:23], s40 offen offset:2048 nt lds\n"
"v_mfma_f32_16x16x32_bf16 v[12:15], v[100:103], v[224:227], v[12:15]\n"
"v_exp_f32 v153, v153\n"
"v_mfma_f32_16x16x32_bf16 v[12:15], v[96:99], a[252:255], v[12:15]\n"
"v_exp_f32 v165, v165\n"
"v_mfma_f32_16x16x32_bf16 a[220:223], v[120:123], a[252:255], a[220:223]\n"
"v_exp_f32 v164, v164\n"
"v_smfmac_f32_16x16x64_bf16 v[196:199], a[240:243], v[96:103], v241\n"
"v_cvt_pk_bf16_f32 v129, v154, v155\n"
"v_mfma_f32_16x16x32_bf16 v[202:205], a[240:243], v[120:123], v[202:205]\n"
"s_waitcnt lgkmcnt(1)\n"
"ds_read_b64_tr_b16 a[254:255], v235 offset:21504\n"
"v_mfma_f32_16x16x32_bf16 v[168:171], v[216:219], a[8:11], v[180:183]\n"
"ds_read_b64_tr_b16 a[248:249], v238 offset:17408\n"
"v_mfma_f32_16x16x32_bf16 v[168:171], v[220:223], a[12:15], v[168:171]\n"
"ds_read_b64_tr_b16 a[246:247], v234 offset:21504\n"
"v_mfma_f32_16x16x32_bf16 v[168:171], v[208:211], a[0:3], v[168:171]\n"
"ds_read_b64_tr_b16 v[224:225], v239 offset:17408\n"
"v_cvt_pk_bf16_f32 v124, v164, v165\n"
"v_mfma_f32_16x16x32_bf16 v[168:171], v[212:215], a[4:7], v[168:171]\n"
"v_exp_f32 v166, v166\n"
"v_mfma_f32_16x16x32_bf16 v[172:175], v[208:211], a[16:19], v[184:187]\n"
"ds_read_b64_tr_b16 a[252:253], v235 offset:17408\n"
"v_cvt_pk_bf16_f32 v128, v152, v153\n"
"v_mfma_f32_16x16x32_bf16 v[172:175], v[212:215], a[20:23], v[172:175]\n"
"ds_read_b64_tr_b16 v[226:227], v239 offset:21504\n"
"v_cvt_pk_bf16_f32 v137, v158, v159\n"
"v_mfma_f32_16x16x32_bf16 v[172:175], v[216:219], a[24:27], v[172:175]\n"
"s_add_u32 s40, s56, s54\n"
"ds_read_b64_tr_b16 a[250:251], v238 offset:21504\n"
"v_mfma_f32_16x16x32_bf16 v[172:175], v[220:223], a[28:31], v[172:175]\n"
"buffer_load_dwordx4 v240, s[20:23], s40 offen offset:3072 nt lds\n"
"v_mfma_f32_16x16x32_bf16 v[176:179], v[216:219], a[40:43], v[188:191]\n"
"s_add_u32 s55, s55, s59\n"
"v_exp_f32 v167, v167\n"
"v_mfma_f32_16x16x32_bf16 v[176:179], v[220:223], a[44:47], v[176:179]\n"
"s_add_u32 s41, s55, s57\n"
"v_exp_f32 v161, v161\n"
"v_mfma_f32_16x16x32_bf16 v[176:179], v[208:211], a[32:35], v[176:179]\n"
"s_and_b32 s41, s41, s58\n"
"v_exp_f32 v157, v157\n"
"v_mfma_f32_16x16x32_bf16 v[176:179], v[212:215], a[36:39], v[176:179]\n"
"s_andn2_b32 s56, s55, s58\n"
"v_exp_f32 v160, v160\n"
"v_mfma_f32_16x16x32_bf16 v[152:155], v[208:211], a[192:195], v[192:195]\n"
"s_or_b32 s56, s56, s41\n"
"ds_read_b128 v[208:211], v230 offset:32768\n"
"v_mfma_f32_16x16x32_bf16 v[152:155], v[212:215], a[196:199], v[152:155]\n"
"s_cmp_ge_u32 s47, 15\n"
"v_exp_f32 v172, v172\n"
"v_mfma_f32_16x16x32_bf16 v[152:155], v[216:219], a[200:203], v[152:155]\n"
"s_cbranch_scc1 LSW_%=\nLSWK_%=:\n"
"ds_read_b128 v[216:219], v230 offset:33792\n"
"v_mfma_f32_16x16x32_bf16 v[152:155], v[220:223], a[204:207], v[152:155]\n"
"s_waitcnt lgkmcnt(2)\n"
"v_exp_f32 v162, v162\n"
"v_mfma_f32_16x16x32_bf16 a[232:235], v[120:123], a[244:247], a[232:235]\n"
"ds_read_b128 v[212:215], v231 offset:32768\n"
"v_mfma_f32_16x16x32_bf16 v[24:27], v[96:99], a[244:247], v[24:27]\n"
"s_mov_b32 m0, s44\n"
"ds_read_b128 v[220:223], v231 offset:33792\n"
"v_mfma_f32_16x16x32_bf16 v[24:27], v[100:103], a[248:251], v[24:27]\n"
"buffer_load_dwordx4 v240, s[16:19], s56 offen nt lds\n"
"v_mfma_f32_16x16x32_bf16 v[56:59], v[104:107], a[244:247], v[56:59]\n"
"v_exp_f32 v171, v171\n"
"v_mfma_f32_16x16x32_bf16 v[56:59], v[108:111], a[248:251], v[56:59]\n"
"v_exp_f32 v169, v169\n"
"v_mfma_f32_16x16x32_bf16 v[88:91], v[112:115], a[244:247], v[88:91]\n"
"v_exp_f32 v154, v154\n"
"v_mfma_f32_16x16x32_bf16 v[88:91], v[116:119], a[248:251], v[88:91]\n"
"v_exp_f32 v156, v156\n"
"v_mfma_f32_16x16x32_bf16 v[92:95], v[116:119], v[224:227], v[92:95]\n"
"ds_read_b64_tr_b16 a[244:245], v232 offset:16384\n"
"v_cvt_pk_bf16_f32 v145, v162, v163\n"
"v_mfma_f32_16x16x32_bf16 v[92:95], v[112:115], a[252:255], v[92:95]\n"
"ds_read_b64_tr_b16 a[250:251], v236 offset:20480\n"
"v_cvt_pk_bf16_f32 v136, v156, v157\n"
"v_mfma_f32_16x16x32_bf16 v[60:63], v[108:111], v[224:227], v[60:63]\n"
"ds_read_b64_tr_b16 a[246:247], v232 offset:20480\n"
"v_cvt_pk_bf16_f32 v144, v160, v161\n"
"v_mfma_f32_16x16x32_bf16 v[60:63], v[104:107], a[252:255], v[60:63]\n"
"v_exp_f32 v152, v152\n"
"v_mfma_f32_16x16x32_bf16 v[28:31], v[100:103], v[224:227], v[28:31]\n"
"ds_read_b64_tr_b16 v[224:225], v237 offset:16384\n"
"ds_read_b64_tr_b16 v[226:227], v237 offset:20480\n"
"v_mfma_f32_16x16x32_bf16 v[28:31], v[96:99], a[252:255], v[28:31]\n"
"ds_read_b64_tr_b16 a[248:249], v236 offset:16384\n"
"v_cvt_pk_bf16_f32 v125, v166, v167\n"
"v_mfma_f32_16x16x32_bf16 a[236:239], v[120:123], a[252:255], a[236:239]\n"
"s_add_u32 s40, s56, s52\n"
"ds_read_b64_tr_b16 a[252:253], v233 offset:16384\n"
"v_smfmac_f32_16x16x64_bf16 v[198:201], a[240:243], v[104:111], v241\n"
"s_waitcnt lgkmcnt(7)\n"
"buffer_load_dwordx4 v240, s[16:19], s40 offen offset:1024 nt lds\n"
"v_mfma_f32_16x16x32_bf16 v[156:159], v[208:211], a[0:3], v[180:183]\n"
"v_exp_f32 v175, v175\n"
"v_mfma_f32_16x16x32_bf16 v[156:159], v[212:215], a[4:7], v[156:159]\n"
"v_exp_f32 v173, v173\n"
"v_mfma_f32_16x16x32_bf16 v[156:159], v[216:219], a[8:11], v[156:159]\n"
"v_exp_f32 v168, v168\n"
"v_mfma_f32_16x16x32_bf16 v[156:159], v[220:223], a[12:15], v[156:159]\n"
"ds_read_b64_tr_b16 a[254:255], v233 offset:20480\n"
"v_mfma_f32_16x16x32_bf16 v[160:163], v[216:219], a[24:27], v[184:187]\n"
"v_exp_f32 v170, v170\n"
"v_mfma_f32_16x16x32_bf16 v[160:163], v[220:223], a[28:31], v[160:163]\n"
"v_exp_f32 v178, v178\n"
"v_mfma_f32_16x16x32_bf16 v[160:163], v[208:211], a[16:19], v[160:163]\n"
"v_exp_f32 v177, v177\n"
"v_mfma_f32_16x16x32_bf16 v[160:163], v[212:215], a[20:23], v[160:163]\n"
"v_exp_f32 v176, v176\n"
"v_mfma_f32_16x16x32_bf16 v[164:167], v[208:211], a[32:35], v[188:191]\n"
"ds_read_b128 v[208:211], v230 offset:36864\n"
"v_cvt_pk_bf16_f32 v131, v170, v171\n"
"v_mfma_f32_16x16x32_bf16 v[164:167], v[212:215], a[36:39], v[164:167]\n"
"ds_read_b128 v[212:215], v231 offset:36864\n"
"v_mfma_f32_16x16x32_bf16 v[164:167], v[216:219], a[40:43], v[164:167]\n"
"v_exp_f32 v153, v153\n"
"v_mfma_f32_16x16x32_bf16 v[164:167], v[220:223], a[44:47], v[164:167]\n"
"s_waitcnt lgkmcnt(2)\n"
"ds_read_b128 v[220:223], v231 offset:37888\n"
"v_cvt_pk_bf16_f32 v138, v172, v173\n"
"v_mfma_f32_16x16x32_bf16 a[208:211], v[120:123], a[244:247], a[208:211]\n"
"v_exp_f32 v174, v174\n"
"v_mfma_f32_16x16x32_bf16 v[0:3], v[96:99], a[244:247], v[0:3]\n"
"s_add_u32 s40, s56, s53\n"
"ds_read_b128 v[216:219], v230 offset:37888\n"
"v_mfma_f32_16x16x32_bf16 v[0:3], v[100:103], a[248:251], v[0:3]\n"
"buffer_load_dwordx4 v240, s[16:19], s40 offen offset:2048 nt lds\n"
"v_mfma_f32_16x16x32_bf16 v[32:35], v[104:107], a[244:247], v[32:35]\n"
"v_exp_f32 v163, v163\n"
"v_mfma_f32_16x16x32_bf16 v[32:35], v[108:111], a[248:251], v[32:35]\n"
"v_exp_f32 v161, v161\n"
"v_mfma_f32_16x16x32_bf16 v[64:67], v[112:115], a[244:247], v[64:67]\n"
"v_exp_f32 v155, v155\n"
"v_mfma_f32_16x16x32_bf16 v[64:67], v[116:119], a[248:251], v[64:67]\n"
"v_exp_f32 v166, v166\n"
"v_mfma_f32_16x16x32_bf16 v[68:71], v[116:119], v[224:227], v[68:71]\n"
"ds_read_b64_tr_b16 a[250:251], v236 offset:21504\n"
"v_cvt_pk_bf16_f32 v139, v174, v175\n"
"v_mfma_f32_16x16x32_bf16 v[68:71], v[112:115], a[252:255], v[68:71]\n"
"ds_read_b64_tr_b16 a[246:247], v232 offset:21504\n"
"v_cvt_pk_bf16_f32 v130, v168, v169\n"
"v_mfma_f32_16x16x32_bf16 v[36:39], v[108:111], v[224:227], v[36:39]\n"
"v_exp_f32 v179, v179\n"
"v_mfma_f32_16x16x32_bf16 v[36:39], v[104:107], a[252:255], v[36:39]\n"
"ds_read_b64_tr_b16 a[244:245], v232 offset:17408\n"
"v_cvt_pk_bf16_f32 v126, v152, v153\n"
"v_mfma_f32_16x16x32_bf16 v[4:7], v[100:103], v[224:227], v[4:7]\n"
"s_add_u32 s40, s56, s54\n"
"ds_read_b64_tr_b16 a[248:249], v236 offset:17408\n"
"v_mfma_f32_16x16x32_bf16 v[4:7], v[96:99], a[252:255], v[4:7]\n"
"buffer_load_dwordx4 v240, s[16:19], s40 offen offset:3072 nt lds\n"
"v_mfma_f32_16x16x32_bf16 a[212:215], v[120:123], a[252:255], a[212:215]\n"
"v_exp_f32 v162, v162\n"
"v_smfmac_f32_16x16x64_bf16 v[200:203], a[240:243], v[112:119], v241\n"
"s_waitcnt lgkmcnt(4)\n"
"v_cvt_pk_bf16_f32 v146, v176, v177\n"
"v_mfma_f32_16x16x32_bf16 v[168:171], v[208:211], a[0:3], v[180:183]\n"
"v_exp_f32 v157, v157\n"
"v_mfma_f32_16x16x32_bf16 v[168:171], v[212:215], a[4:7], v[168:171]\n"
"v_exp_f32 v165, v165\n"
"v_mfma_f32_16x16x32_bf16 v[168:171], v[216:219], a[8:11], v[168:171]\n"
"ds_read_b64_tr_b16 a[254:255], v233 offset:21504\n"
"ds_read_b64_tr_b16 v[226:227], v237 offset:21504\n"
"v_mfma_f32_16x16x32_bf16 v[168:171], v[220:223], a[12:15], v[168:171]\n"
"ds_read_b64_tr_b16 a[252:253], v233 offset:17408\n"
"v_cvt_pk_bf16_f32 v147, v178, v179\n"
"v_mfma_f32_16x16x32_bf16 v[172:175], v[216:219], a[24:27], v[184:187]\n"
"ds_read_b64_tr_b16 v[224:225], v237 offset:17408\n"
"v_mfma_f32_16x16x32_bf16 v[172:175], v[220:223], a[28:31], v[172:175]\n"
"v_exp_f32 v160, v160\n"
"v_mfma_f32_16x16x32_bf16 v[172:175], v[208:211], a[16:19], v[172:175]\n"
"v_exp_f32 v158, v158\n"
"v_mfma_f32_16x16x32_bf16 v[172:175], v[212:215], a[20:23], v[172:175]\n"
"s_cbranch_vccnz LPCL2_%=\nLPKL2_%=:\n"
"v_mfma_f32_16x16x32_bf16 v[176:179], v[208:211], a[32:35], v[188:191]\n"
"v_exp_f32 v159, v159\n"
"v_mfma_f32_16x16x32_bf16 v[176:179], v[212:215], a[36:39], v[176:179]\n"
"v_exp_f32 v167, v167\n"
"v_mfma_f32_16x16x32_bf16 v[176:179], v[216:219], a[40:43], v[176:179]\n"
"v_exp_f32 v164, v164\n"
"v_mfma_f32_16x16x32_bf16 v[176:179], v[220:223], a[44:47], v[176:179]\n"
"s_waitcnt lgkmcnt(1)\n"
"v_mfma_f32_16x16x32_bf16 a[224:227], v[120:123], a[244:247], a[224:227]\n"
"v_exp_f32 v169, v169\n"
"v_mfma_f32_16x16x32_bf16 v[16:19], v[96:99], a[244:247], v[16:19]\n"
"s_waitcnt vmcnt(8)\n"
"v_exp_f32 v175, v175\n"
"v_mfma_f32_16x16x32_bf16 v[16:19], v[100:103], a[248:251], v[16:19]\n"
"s_barrier\n"
"ds_read_b128 v[220:223], v229 offset:50176\n"
"v_mfma_f32_16x16x32_bf16 v[48:51], v[104:107], a[244:247], v[48:51]\n"
"ds_read_b128 v[216:219], v228 offset:50176\n"
"v_cvt_pk_bf16_f32 v141, v162, v163\n"
"v_mfma_f32_16x16x32_bf16 v[48:51], v[108:111], a[248:251], v[48:51]\n"
"ds_read_b128 v[212:215], v229 offset:49152\n"
"v_mfma_f32_16x16x32_bf16 v[80:83], v[112:115], a[244:247], v[80:83]\n"
"ds_read_b128 v[208:211], v228 offset:49152\n"
"v_cvt_pk_bf16_f32 v133, v158, v159\n"
"v_mfma_f32_16x16x32_bf16 v[80:83], v[116:119], a[248:251], v[80:83]\n"
"s_waitcnt lgkmcnt(4)\n"
"v_exp_f32 v178, v178\n"
"v_mfma_f32_16x16x32_bf16 v[84:87], v[116:119], v[224:227], v[84:87]\n"
"ds_read_b64_tr_b16 a[244:245], v234 offset:32768\n"
"v_cvt_pk_bf16_f32 v149, v166, v167\n"
"v_mfma_f32_16x16x32_bf16 v[84:87], v[112:115], a[252:255], v[84:87]\n"
"ds_read_b64_tr_b16 a[250:251], v238 offset:36864\n"
"v_cvt_pk_bf16_f32 v127, v154, v155\n"
"v_mfma_f32_16x16x32_bf16 v[52:55], v[108:111], v[224:227], v[52:55]\n"
"v_exp_f32 v173, v173\n"
"v_mfma_f32_16x16x32_bf16 v[52:55], v[104:107], a[252:255], v[52:55]\n"
"ds_read_b64_tr_b16 a[248:249], v238 offset:32768\n"
"v_mfma_f32_16x16x32_bf16 v[20:23], v[100:103], v[224:227], v[20:23]\n"
"v_exp_f32 v156, v156\n"
"v_mfma_f32_16x16x32_bf16 v[20:23], v[96:99], a[252:255], v[20:23]\n"
"ds_read_b64_tr_b16 v[226:227], v239 offset:36864\n"
"v_cvt_pk_bf16_f32 v132, v156, v157\n"
"v_mfma_f32_16x16x32_bf16 a[228:231], v[120:123], a[252:255], a[228:231]\n"
"s_waitcnt lgkmcnt(4)\n"
"ds_read_b64_tr_b16 a[246:247], v234 offset:36864\n"
"v_mfma_f32_16x16x32_bf16 v[152:155], v[208:211], a[0:3], v[180:183]\n"
"ds_read_b64_tr_b16 v[224:225], v239 offset:32768\n"
"v_cvt_pk_bf16_f32 v148, v164, v165\n"
"v_mfma_f32_16x16x32_bf16 v[152:155], v[212:215], a[4:7], v[152:155]\n"
"ds_read_b64_tr_b16 a[252:253], v235 offset:32768\n"
"v_cvt_pk_bf16_f32 v140, v160, v161\n"
"v_mfma_f32_16x16x32_bf16 v[152:155], v[216:219], a[8:11], v[152:155]\n"
"s_add_u32 m0, s44, 65536\n"
"ds_read_b64_tr_b16 a[254:255], v235 offset:36864\n"
"v_mfma_f32_16x16x32_bf16 v[152:155], v[220:223], a[12:15], v[152:155]\n"
"buffer_load_dwordx4 v240, s[20:23], s56 offen nt lds\n"
"v_mfma_f32_16x16x32_bf16 v[156:159], v[216:219], a[24:27], v[184:187]\n"
"v_exp_f32 v177, v177\n"
"v_mfma_f32_16x16x32_bf16 v[156:159], v[220:223], a[28:31], v[156:159]\n"
"v_exp_f32 v168, v168\n"
"v_mfma_f32_16x16x32_bf16 v[156:159], v[208:211], a[16:19], v[156:159]\n"
"v_exp_f32 v179, v179\n"
"v_mfma_f32_16x16x32_bf16 v[156:159], v[212:215], a[20:23], v[156:159]\n"
"v_exp_f32 v174, v174\n"
"v_mfma_f32_16x16x32_bf16 v[160:163], v[208:211], a[32:35], v[188:191]\n"
"v_exp_f32 v172, v172\n"
"v_mfma_f32_16x16x32_bf16 v[160:163], v[212:215], a[36:39], v[160:163]\n"
"v_exp_f32 v171, v171\n"
"v_mfma_f32_16x16x32_bf16 v[160:163], v[216:219], a[40:43], v[160:163]\n"
"v_exp_f32 v154, v154\n"
"v_mfma_f32_16x16x32_bf16 v[160:163], v[220:223], a[44:47], v[160:163]\n"
"v_exp_f32 v170, v170\n"
"v_mfma_f32_16x16x32_bf16 v[164:167], v[216:219], a[200:203], v[192:195]\n"
"ds_read_b128 v[216:219], v228 offset:54272\n"
"v_cvt_pk_bf16_f32 v135, v170, v171\n"
"v_mfma_f32_16x16x32_bf16 v[164:167], v[220:223], a[204:207], v[164:167]\n"
"v_cvt_pk_bf16_f32 v134, v168, v169\n"
"v_cvt_pk_bf16_f32 v151, v178, v179\n"
"v_mfma_f32_16x16x32_bf16 v[164:167], v[208:211], a[192:195], v[164:167]\n"
"s_add_u32 s40, s56, s52\n"
"ds_read_b128 v[208:211], v228 offset:53248\n"
"v_mfma_f32_16x16x32_bf16 v[164:167], v[212:215], a[196:199], v[164:167]\n"
"s_waitcnt lgkmcnt(2)\n"
"buffer_load_dwordx4 v240, s[20:23], s40 offen offset:1024 nt lds\n"
"v_mfma_f32_16x16x32_bf16 a[216:219], v[124:127], a[244:247], a[216:219]\n"
"v_cvt_pk_bf16_f32 v142, v172, v173\n"
"v_cvt_pk_bf16_f32 v143, v174, v175\n"
"v_mfma_f32_16x16x32_bf16 v[8:11], v[128:131], a[244:247], v[8:11]\n"
"v_exp_f32 v176, v176\n"
"v_mfma_f32_16x16x32_bf16 v[8:11], v[132:135], a[248:251], v[8:11]\n"
"v_exp_f32 v155, v155\n"
"v_mfma_f32_16x16x32_bf16 v[40:43], v[136:139], a[244:247], v[40:43]\n"
"v_exp_f32 v152, v152\n"
"v_mfma_f32_16x16x32_bf16 v[40:43], v[140:143], a[248:251], v[40:43]\n"
"ds_read_b128 v[212:215], v229 offset:53248\n"
"v_cvt_pk_bf16_f32 v150, v176, v177\n"
"v_mfma_f32_16x16x32_bf16 v[72:75], v[144:147], a[244:247], v[72:75]\n"
"v_exp_f32 v158, v158\n"
"v_mfma_f32_16x16x32_bf16 v[72:75], v[148:151], a[248:251], v[72:75]\n"
"v_exp_f32 v163, v163\n"
"v_mfma_f32_16x16x32_bf16 v[76:79], v[148:151], v[224:227], v[76:79]\n"
"ds_read_b128 v[220:223], v229 offset:54272\n"
"v_mfma_f32_16x16x32_bf16 v[76:79], v[144:147], a[252:255], v[76:79]\n"
"v_exp_f32 v159, v159\n"
"v_mfma_f32_16x16x32_bf16 v[44:47], v[140:143], v[224:227], v[44:47]\n"
"s_add_u32 s40, s56, s53\n"
"ds_read_b64_tr_b16 a[244:245], v234 offset:33792\n"
"v_mfma_f32_16x16x32_bf16 v[44:47], v[136:139], a[252:255], v[44:47]\n"
"buffer_load_dwordx4 v240, s[20:23], s40 offen offset:2048 nt lds\n"
"v_mfma_f32_16x16x32_bf16 v[12:15], v[132:135], v[224:227], v[12:15]\n"
"v_exp_f32 v153, v153\n"
"v_mfma_f32_16x16x32_bf16 v[12:15], v[128:131], a[252:255], v[12:15]\n"
"v_exp_f32 v165, v165\n"
"v_mfma_f32_16x16x32_bf16 a[220:223], v[124:127], a[252:255], a[220:223]\n"
"v_exp_f32 v164, v164\n"
"v_smfmac_f32_16x16x64_bf16 v[196:199], a[240:243], v[128:135], v241\n"
"v_cvt_pk_bf16_f32 v97, v154, v155\n"
"v_mfma_f32_16x16x32_bf16 v[202:205], a[240:243], v[124:127], v[202:205]\n"
"s_waitcnt lgkmcnt(1)\n"
"ds_read_b64_tr_b16 a[254:255], v235 offset:37888\n"
"v_mfma_f32_16x16x32_bf16 v[168:171], v[216:219], a[8:11], v[180:183]\n"
"ds_read_b64_tr_b16 a[248:249], v238 offset:33792\n"
"v_mfma_f32_16x16x32_bf16 v[168:171], v[220:223], a[12:15], v[168:171]\n"
"ds_read_b64_tr_b16 a[246:247], v234 offset:37888\n"
"v_mfma_f32_16x16x32_bf16 v[168:171], v[208:211], a[0:3], v[168:171]\n"
"ds_read_b64_tr_b16 v[224:225], v239 offset:33792\n"
"v_cvt_pk_bf16_f32 v120, v164, v165\n"
"v_mfma_f32_16x16x32_bf16 v[168:171], v[212:215], a[4:7], v[168:171]\n"
"v_exp_f32 v166, v166\n"
"v_mfma_f32_16x16x32_bf16 v[172:175], v[208:211], a[16:19], v[184:187]\n"
"ds_read_b64_tr_b16 a[252:253], v235 offset:33792\n"
"v_cvt_pk_bf16_f32 v96, v152, v153\n"
"v_mfma_f32_16x16x32_bf16 v[172:175], v[212:215], a[20:23], v[172:175]\n"
"ds_read_b64_tr_b16 v[226:227], v239 offset:37888\n"
"v_cvt_pk_bf16_f32 v105, v158, v159\n"
"v_mfma_f32_16x16x32_bf16 v[172:175], v[216:219], a[24:27], v[172:175]\n"
"s_add_u32 s40, s56, s54\n"
"ds_read_b64_tr_b16 a[250:251], v238 offset:37888\n"
"v_mfma_f32_16x16x32_bf16 v[172:175], v[220:223], a[28:31], v[172:175]\n"
"buffer_load_dwordx4 v240, s[20:23], s40 offen offset:3072 nt lds\n"
"v_mfma_f32_16x16x32_bf16 v[176:179], v[216:219], a[40:43], v[188:191]\n"
"s_add_u32 s55, s55, s59\n"
"v_exp_f32 v167, v167\n"
"v_mfma_f32_16x16x32_bf16 v[176:179], v[220:223], a[44:47], v[176:179]\n"
"s_add_u32 s41, s55, s57\n"
"v_exp_f32 v161, v161\n"
"v_mfma_f32_16x16x32_bf16 v[176:179], v[208:211], a[32:35], v[176:179]\n"
"s_and_b32 s41, s41, s58\n"
"v_exp_f32 v157, v157\n"
"v_mfma_f32_16x16x32_bf16 v[176:179], v[212:215], a[36:39], v[176:179]\n"
"s_andn2_b32 s56, s55, s58\n"
"v_exp_f32 v160, v160\n"
"v_mfma_f32_16x16x32_bf16 v[152:155], v[208:211], a[192:195], v[192:195]\n"
"s_or_b32 s56, s56, s41\n"
"ds_read_b128 v[208:211], v230 offset:49152\n"
"v_mfma_f32_16x16x32_bf16 v[152:155], v[212:215], a[196:199], v[152:155]\n"
"v_exp_f32 v172, v172\n"
"v_mfma_f32_16x16x32_bf16 v[152:155], v[216:219], a[200:203], v[152:155]\n"
"ds_read_b128 v[216:219], v230 offset:50176\n"
"v_mfma_f32_16x16x32_bf16 v[152:155], v[220:223], a[204:207], v[152:155]\n"
"s_waitcnt lgkmcnt(2)\n"
"v_exp_f32 v162, v162\n"
"v_mfma_f32_16x16x32_bf16 a[232:235], v[124:127], a[244:247], a[232:235]\n"
"ds_read_b128 v[212:215], v231 offset:49152\n"
"v_mfma_f32_16x16x32_bf16 v[24:27], v[128:131], a[244:247], v[24:27]\n"
"s_add_u32 m0, s44, 16384\n"
"ds_read_b128 v[220:223], v231 offset:50176\n"
"v_mfma_f32_16x16x32_bf16 v[24:27], v[132:135], a[248:251], v[24:27]\n"
"buffer_load_dwordx4 v240, s[16:19], s56 offen nt lds\n"
"v_mfma_f32_16x16x32_bf16 v[56:59], v[136:139], a[244:247], v[56:59]\n"
"v_exp_f32 v171, v171\n"
"v_mfma_f32_16x16x32_bf16 v[56:59], v[140:143], a[248:251], v[56:59]\n"
"v_exp_f32 v169, v169\n"
"v_mfma_f32_16x16x32_bf16 v[88:91], v[144:147], a[244:247], v[88:91]\n"
"v_exp_f32 v154, v154\n"
"v_mfma_f32_16x16x32_bf16 v[88:91], v[148:151], a[248:251], v[88:91]\n"
"v_exp_f32 v156, v156\n"
"v_mfma_f32_16x16x32_bf16 v[92:95], v[148:151], v[224:227], v[92:95]\n"
"ds_read_b64_tr_b16 a[244:245], v232 offset:32768\n"
"v_cvt_pk_bf16_f32 v113, v162, v163\n"
"v_mfma_f32_16x16x32_bf16 v[92:95], v[144:147], a[252:255], v[92:95]\n"
"ds_read_b64_tr_b16 a[250:251], v236 offset:36864\n"
"v_cvt_pk_bf16_f32 v104, v156, v157\n"
"v_mfma_f32_16x16x32_bf16 v[60:63], v[140:143], v[224:227], v[60:63]\n"
"ds_read_b64_tr_b16 a[246:247], v232 offset:36864\n"
"v_cvt_pk_bf16_f32 v112, v160, v161\n"
"v_mfma_f32_16x16x32_bf16 v[60:63], v[136:139], a[252:255], v[60:63]\n"
"v_exp_f32 v152, v152\n"
"v_mfma_f32_16x16x32_bf16 v[28:31], v[132:135], v[224:227], v[28:31]\n"
"ds_read_b64_tr_b16 v[224:225], v237 offset:32768\n"
"ds_read_b64_tr_b16 v[226:227], v237 offset:36864\n"
"v_mfma_f32_16x16x32_bf16 v[28:31], v[128:131], a[252:255], v[28:31]\n"
"ds_read_b64_tr_b16 a[248:249], v236 offset:32768\n"
"v_cvt_pk_bf16_f32 v121, v166, v167\n"
"v_mfma_f32_16x16x32_bf16 a[236:239], v[124:127], a[252:255], a[236:239]\n"
"s_add_u32 s40, s56, s52\n"
"ds_read_b64_tr_b16 a[252:253], v233 offset:32768\n"
"v_smfmac_f32_16x16x64_bf16 v[198:201], a[240:243], v[136:143], v241\n"
"s_waitcnt lgkmcnt(7)\n"
"buffer_load_dwordx4 v240, s[16:19], s40 offen offset:1024 nt lds\n"
"v_mfma_f32_16x16x32_bf16 v[156:159], v[208:211], a[0:3], v[180:183]\n"
"v_exp_f32 v175, v175\n"
"v_mfma_f32_16x16x32_bf16 v[156:159], v[212:215], a[4:7], v[156:159]\n"
"v_exp_f32 v173, v173\n"
"v_mfma_f32_16x16x32_bf16 v[156:159], v[216:219], a[8:11], v[156:159]\n"
"v_exp_f32 v168, v168\n"
"v_mfma_f32_16x16x32_bf16 v[156:159], v[220:223], a[12:15], v[156:159]\n"
"ds_read_b64_tr_b16 a[254:255], v233 offset:36864\n"
"v_mfma_f32_16x16x32_bf16 v[160:163], v[216:219], a[24:27], v[184:187]\n"
"v_exp_f32 v170, v170\n"
"v_mfma_f32_16x16x32_bf16 v[160:163], v[220:223], a[28:31], v[160:163]\n"
"v_exp_f32 v178, v178\n"
"v_mfma_f32_16x16x32_bf16 v[160:163], v[208:211], a[16:19], v[160:163]\n"
"v_exp_f32 v177, v177\n"
"v_mfma_f32_16x16x32_bf16 v[160:163], v[212:215], a[20:23], v[160:163]\n"
"v_exp_f32 v176, v176\n"
"v_mfma_f32_16x16x32_bf16 v[164:167], v[208:211], a[32:35], v[188:191]\n"
"ds_read_b128 v[208:211], v230 offset:53248\n"
"v_cvt_pk_bf16_f32 v99, v170, v171\n"
"v_mfma_f32_16x16x32_bf16 v[164:167], v[212:215], a[36:39], v[164:167]\n"
"ds_read_b128 v[212:215], v231 offset:53248\n"
"v_mfma_f32_16x16x32_bf16 v[164:167], v[216:219], a[40:43], v[164:167]\n"
"v_exp_f32 v153, v153\n"
"v_mfma_f32_16x16x32_bf16 v[164:167], v[220:223], a[44:47], v[164:167]\n"
"s_waitcnt lgkmcnt(2)\n"
"ds_read_b128 v[220:223], v231 offset:54272\n"
"v_cvt_pk_bf16_f32 v106, v172, v173\n"
"v_mfma_f32_16x16x32_bf16 a[208:211], v[124:127], a[244:247], a[208:211]\n"
"v_exp_f32 v174, v174\n"
"v_mfma_f32_16x16x32_bf16 v[0:3], v[128:131], a[244:247], v[0:3]\n"
"s_add_u32 s40, s56, s53\n"
"ds_read_b128 v[216:219], v230 offset:54272\n"
"v_mfma_f32_16x16x32_bf16 v[0:3], v[132:135], a[248:251], v[0:3]\n"
"buffer_load_dwordx4 v240, s[16:19], s40 offen offset:2048 nt lds\n"
"v_mfma_f32_16x16x32_bf16 v[32:35], v[136:139], a[244:247], v[32:35]\n"
"v_exp_f32 v163, v163\n"
"v_mfma_f32_16x16x32_bf16 v[32:35], v[140:143], a[248:251], v[32:35]\n"
"v_exp_f32 v161, v161\n"
"v_mfma_f32_16x16x32_bf16 v[64:67], v[144:147], a[244:247], v[64:67]\n"
"v_exp_f32 v155, v155\n"
"v_mfma_f32_16x16x32_bf16 v[64:67], v[148:151], a[248:251], v[64:67]\n"
"v_exp_f32 v166, v166\n"
"v_mfma_f32_16x16x32_bf16 v[68:71], v[148:151], v[224:227], v[68:71]\n"
"ds_read_b64_tr_b16 a[250:251], v236 offset:37888\n"
"v_cvt_pk_bf16_f32 v107, v174, v175\n"
"v_mfma_f32_16x16x32_bf16 v[68:71], v[144:147], a[252:255], v[68:71]\n"
"ds_read_b64_tr_b16 a[246:247], v232 offset:37888\n"
"v_cvt_pk_bf16_f32 v98, v168, v169\n"
"v_mfma_f32_16x16x32_bf16 v[36:39], v[140:143], v[224:227], v[36:39]\n"
"v_exp_f32 v179, v179\n"
"v_mfma_f32_16x16x32_bf16 v[36:39], v[136:139], a[252:255], v[36:39]\n"
"ds_read_b64_tr_b16 a[244:245], v232 offset:33792\n"
"v_cvt_pk_bf16_f32 v122, v152, v153\n"
"v_mfma_f32_16x16x32_bf16 v[4:7], v[132:135], v[224:227], v[4:7]\n"
"s_add_u32 s40, s56, s54\n"
"ds_read_b64_tr_b16 a[248:249], v236 offset:33792\n"
"v_mfma_f32_16x16x32_bf16 v[4:7], v[128:131], a[252:255], v[4:7]\n"
"buffer_load_dwordx4 v240, s[16:19], s40 offen offset:3072 nt lds\n"
"v_mfma_f32_16x16x32_bf16 a[212:215], v[124:127], a[252:255], a[212:215]\n"
"v_exp_f32 v162, v162\n"
"v_smfmac_f32_16x16x64_bf16 v[200:203], a[240:243], v[144:151], v241\n"
"s_waitcnt lgkmcnt(4)\n"
"v_cvt_pk_bf16_f32 v114, v176, v177\n"
"v_mfma_f32_16x16x32_bf16 v[168:171], v[208:211], a[0:3], v[180:183]\n"
"v_exp_f32 v157, v157\n"
"v_mfma_f32_16x16x32_bf16 v[168:171], v[212:215], a[4:7], v[168:171]\n"
"v_exp_f32 v165, v165\n"
"v_mfma_f32_16x16x32_bf16 v[168:171], v[216:219], a[8:11], v[168:171]\n"
"ds_read_b64_tr_b16 a[254:255], v233 offset:37888\n"
"ds_read_b64_tr_b16 v[226:227], v237 offset:37888\n"
"v_mfma_f32_16x16x32_bf16 v[168:171], v[220:223], a[12:15], v[168:171]\n"
"ds_read_b64_tr_b16 a[252:253], v233 offset:33792\n"
"v_cvt_pk_bf16_f32 v115, v178, v179\n"
"v_mfma_f32_16x16x32_bf16 v[172:175], v[216:219], a[24:27], v[184:187]\n"
"ds_read_b64_tr_b16 v[224:225], v237 offset:33792\n"
"v_mfma_f32_16x16x32_bf16 v[172:175], v[220:223], a[28:31], v[172:175]\n"
"v_exp_f32 v160, v160\n"
"v_mfma_f32_16x16x32_bf16 v[172:175], v[208:211], a[16:19], v[172:175]\n"
"v_exp_f32 v158, v158\n"
"v_mfma_f32_16x16x32_bf16 v[172:175], v[212:215], a[20:23], v[172:175]\n"
"s_cbranch_vccnz LPCL3_%=\nLPKL3_%=:\n"
"v_mfma_f32_16x16x32_bf16 v[176:179], v[208:211], a[32:35], v[188:191]\n"
"s_add_u32 s47, s47, 1\n"
"v_exp_f32 v159, v159\n"
"v_mfma_f32_16x16x32_bf16 v[176:179], v[212:215], a[36:39], v[176:179]\n"
"v_exp_f32 v167, v167\n"
"v_mfma_f32_16x16x32_bf16 v[176:179], v[216:219], a[40:43], v[176:179]\n"
"s_and_b32 s40, s47, 3\n"
"v_exp_f32 v164, v164\n"
"v_mfma_f32_16x16x32_bf16 v[176:179], v[220:223], a[44:47], v[176:179]\n"
"s_waitcnt lgkmcnt(1)\n"
"s_cbranch_scc1 LAL_%=\n"
"s_cmp_eq_u32 s47, 8\n"
"s_cbranch_scc1 LTH1_%=\n"
"s_cmp_eq_u32 s47, 12\n"
"s_cbranch_scc1 LTH2_%=\n"
"s_cmp_eq_u32 s47, 16\n"
"s_cbranch_scc1 LAX_%=\n"
"LTH0_%=:\n"
"v_mfma_f32_16x16x32_bf16 a[224:227], v[124:127], a[244:247], a[224:227]\n"
"v_exp_f32 v168, v168\n"
"v_mfma_f32_16x16x32_bf16 v[16:19], v[128:131], a[244:247], v[16:19]\n"
"s_waitcnt vmcnt(8) lgkmcnt(0)\n"
"v_exp_f32 v169, v169\n"
"v_mfma_f32_16x16x32_bf16 v[16:19], v[132:135], a[248:251], v[16:19]\n"
"s_barrier\n"
"v_mfma_f32_16x16x32_bf16 v[48:51], v[136:139], a[244:247], v[48:51]\n"
"v_exp_f32 v172, v172\n"
"v_mfma_f32_16x16x32_bf16 v[48:51], v[140:143], a[248:251], v[48:51]\n"
"ds_read_b128 v[216:219], v228 offset:1024\n"
"v_cvt_pk_bf16_f32 v102, v168, v169\n"
"v_mfma_f32_16x16x32_bf16 v[80:83], v[144:147], a[244:247], v[80:83]\n"
"v_exp_f32 v176, v176\n"
"v_mfma_f32_16x16x32_bf16 v[80:83], v[148:151], a[248:251], v[80:83]\n"
"ds_read_b64_tr_b16 a[244:245], v234 offset:49152\n"
"ds_read_b64_tr_b16 a[246:247], v234 offset:53248\n"
"v_cvt_pk_bf16_f32 v123, v154, v155\n"
"v_cvt_pk_bf16_f32 v117, v166, v167\n"
"v_exp_f32 v170, v170\n"
"v_exp_f32 v175, v175\n"
"v_exp_f32 v177, v177\n"
"v_mfma_f32_16x16x32_bf16 v[84:87], v[148:151], v[224:227], v[84:87]\n"
"v_exp_f32 v156, v156\n"
"v_mfma_f32_16x16x32_bf16 v[84:87], v[144:147], a[252:255], v[84:87]\n"
"ds_read_b64_tr_b16 a[248:249], v238 offset:49152\n"
"ds_read_b64_tr_b16 a[250:251], v238 offset:53248\n"
"v_mfma_f32_16x16x32_bf16 v[52:55], v[140:143], v[224:227], v[52:55]\n"
"v_cvt_pk_bf16_f32 v101, v158, v159\n"
"v_cvt_pk_bf16_f32 v118, v176, v177\n"
"v_mfma_f32_16x16x32_bf16 v[52:55], v[136:139], a[252:255], v[52:55]\n"
"v_cvt_pk_bf16_f32 v100, v156, v157\n"
"v_exp_f32 v173, v173\n"
"v_exp_f32 v174, v174\n"
"v_exp_f32 v178, v178\n"
"v_exp_f32 v179, v179\n"
"v_mfma_f32_16x16x32_bf16 v[20:23], v[132:135], v[224:227], v[20:23]\n"
"v_exp_f32 v171, v171\n"
"v_mfma_f32_16x16x32_bf16 v[20:23], v[128:131], a[252:255], v[20:23]\n"
"ds_read_b64_tr_b16 v[224:225], v239 offset:49152\n"
"v_cvt_pk_bf16_f32 v103, v170, v171\n"
"v_mfma_f32_16x16x32_bf16 a[228:231], v[124:127], a[252:255], a[228:231]\n"
"s_waitcnt lgkmcnt(3)\n"
"ds_read_b64_tr_b16 a[252:253], v235 offset:49152\n"
"v_cvt_pk_bf16_f32 v119, v178, v179\n"
"v_mfma_f32_16x16x32_bf16 a[216:219], v[120:123], a[244:247], a[216:219]\n"
"s_add_u32 m0, s44, 81920\n"
"ds_read_b64_tr_b16 a[254:255], v235 offset:53248\n"
"ds_read_b64_tr_b16 v[226:227], v239 offset:53248\n"
"v_smfmac_f32_16x16x64_bf16 v[196:199], a[240:243], v[96:103], v241\n"
"buffer_load_dwordx4 v240, s[20:23], s56 offen nt lds\n"
"v_mfma_f32_16x16x32_bf16 v[8:11], v[96:99], a[244:247], v[8:11]\n"
"s_waitcnt lgkmcnt(4)\n"
"v_cvt_pk_bf16_f32 v109, v162, v163\n"
"v_cvt_pk_bf16_f32 v116, v164, v165\n"
"v_mfma_f32_16x16x32_bf16 v[8:11], v[100:103], a[248:251], v[8:11]\n"
"v_cvt_pk_bf16_f32 v110, v172, v173\n"
"v_cvt_pk_bf16_f32 v111, v174, v175\n"
"v_smfmac_f32_16x16x64_bf16 v[200:203], a[240:243], v[112:119], v241\n"
"ds_read_b128 v[220:223], v229 offset:1024\n"
"v_cvt_pk_bf16_f32 v108, v160, v161\n"
"v_mfma_f32_16x16x32_bf16 v[40:43], v[104:107], a[244:247], v[40:43]\n"
"v_add_f32 v196, v196, v197\n"
"v_and_b32 v245, 0x3f0, v242\n"
"v_mfma_f32_16x16x32_bf16 v[40:43], v[108:111], a[248:251], v[40:43]\n"
"ds_read_b128 v[212:215], v229 offset:0\n"
"v_lshrrev_b32 v245, 1, v245\n"
"v_smfmac_f32_16x16x64_bf16 v[198:201], a[240:243], v[104:111], v241\n"
"v_lshlrev_b32_e64 v244, 24, s43\n"
"v_sub_u32 v245, v242, v245\n"
"v_mfma_f32_16x16x32_bf16 v[72:75], v[112:115], a[244:247], v[72:75]\n"
"ds_read_b64_tr_b16 a[244:245], v234 offset:50176\n"
"ds_read_b64_tr_b16 a[246:247], v234 offset:54272\n"
"v_mfma_f32_16x16x32_bf16 v[72:75], v[116:119], a[248:251], v[72:75]\n"
"ds_read_b128 v[208:211], v228 offset:0\n"
"v_mul_f32 v244, 0x45800000, v244\n"
"v_mfma_f32_16x16x32_bf16 v[202:205], a[240:243], v[120:123], v[202:205]\n"
"s_waitcnt lgkmcnt(5)\n"
"ds_read_b64_tr_b16 a[250:251], v238 offset:54272\n"
"v_add_f32 v198, v198, v199\n"
"v_mfma_f32_16x16x32_bf16 v[76:79], v[116:119], v[224:227], v[76:79]\n"
"v_add_f32 v200, v200, v201\n"
"v_add_f32 v254, v196, v198\n"
"v_mfma_f32_16x16x32_bf16 v[76:79], v[112:115], a[252:255], v[76:79]\n"
"ds_read_b64_tr_b16 a[248:249], v238 offset:50176\n"
"v_add_f32 v254, v254, v200\n"
"v_mfma_f32_16x16x32_bf16 v[44:47], v[108:111], v[224:227], v[44:47]\n"
"v_add_f32 v244, v244, v202\n"
"v_min3_f32 v244, v244, v196, v198\n"
"v_mfma_f32_16x16x32_bf16 v[44:47], v[104:107], a[252:255], v[44:47]\n"
"v_min_f32 v244, v244, v200\n"
"v_rcp_f32 v244, v244\n"
"v_mfma_f32_16x16x32_bf16 v[12:15], v[100:103], v[224:227], v[12:15]\n"
"s_add_u32 s40, s56, s52\n"
"v_add_f32 v254, v254, v202\n"
"v_fmac_f32 v254, 0x68800000, v244\n"
"v_mfma_f32_16x16x32_bf16 v[12:15], v[96:99], a[252:255], v[12:15]\n"
"buffer_load_dwordx4 v240, s[20:23], s40 offen offset:1024 nt lds\n"
"v_mfma_f32_16x16x32_bf16 a[220:223], v[120:123], a[252:255], a[220:223]\n"
"s_waitcnt lgkmcnt(2)\n"
"v_cmp_nge_f32 vcc, s45, v254\n"
"s_cbranch_vccnz LSLOW_%=\nLBACK0_%=:\ns_mov_b64 vcc, s[64:65]\n"
"v_mfma_f32_16x16x32_bf16 a[232:235], v[120:123], a[244:247], a[232:235]\n"
"v_rcp_f32 v246, v196\n"
"v_rcp_f32 v206, v200\n"
"v_mov_b32 v252, v192\n"
"v_accvgpr_mov_b32 a9, a57\n"
"v_accvgpr_mov_b32 a10, a58\n"
"v_accvgpr_mov_b32 a28, a76\n"
"v_accvgpr_mov_b32 a31, a79\n"
"v_accvgpr_mov_b32 a33, a81\n"
"v_accvgpr_mov_b32 a40, a88\n"
"v_bfe_u32 v149, v242, 8, 2\n"
"v_lshl_add_u32 v149, v149, 4, s43\n"
"v_xor_b32 v150, 64, v149\n"
"v_mfma_f32_16x16x32_bf16 v[24:27], v[96:99], a[244:247], v[24:27]\n"
"s_waitcnt lgkmcnt(0)\n"
"ds_read_b128 v[128:131], v150 offset:0\n"
"v_accvgpr_mov_b32 a12, a60\n"
"v_mfma_f32_16x16x32_bf16 v[24:27], v[100:103], a[248:251], v[24:27]\n"
"ds_read_b128 v[124:127], v149 offset:0\n"
"v_accvgpr_mov_b32 a0, a48\n"
"v_accvgpr_mov_b32 a3, a51\n"
"v_accvgpr_mov_b32 a4, a52\n"
"v_accvgpr_mov_b32 a18, a66\n"
"v_accvgpr_mov_b32 a21, a69\n"
"v_accvgpr_mov_b32 a35, a83\n"
"v_accvgpr_mov_b32 a38, a86\n"
"v_accvgpr_mov_b32 a47, a95\n"
"v_mfma_f32_16x16x32_bf16 v[56:59], v[104:107], a[244:247], v[56:59]\n"
"ds_read_b128 v[132:135], v149 offset:1024\n"
"v_rcp_f32 v247, v198\n"
"v_accvgpr_mov_b32 a1, a49\n"
"v_accvgpr_mov_b32 a2, a50\n"
"v_accvgpr_mov_b32 a11, a59\n"
"v_accvgpr_mov_b32 a13, a61\n"
"v_accvgpr_mov_b32 a14, a62\n"
"v_accvgpr_mov_b32 a16, a64\n"
"v_accvgpr_mov_b32 a20, a68\n"
"v_accvgpr_mov_b32 a22, a70\n"
"v_accvgpr_mov_b32 a25, a73\n"
"v_accvgpr_mov_b32 a36, a84\n"
"v_xor_b32 v148, 0x400, v243\n"
"v_mfma_f32_16x16x32_bf16 v[56:59], v[108:111], a[248:251], v[56:59]\n"
"ds_read_b128 v[136:139], v150 offset:1024\n"
"v_mov_b32_dpp v246, v246 row_ror:12 row_mask:0x2 bank_mask:0xf\n"
"v_accvgpr_mov_b32 a6, a54\n"
"v_accvgpr_mov_b32 a7, a55\n"
"v_accvgpr_mov_b32 a8, a56\n"
"v_accvgpr_mov_b32 a24, a72\n"
"v_accvgpr_mov_b32 a29, a77\n"
"v_accvgpr_mov_b32 a34, a82\n"
"v_accvgpr_mov_b32 a39, a87\n"
"v_accvgpr_mov_b32 a43, a91\n"
"v_accvgpr_mov_b32 a45, a93\n"
"v_mfma_f32_16x16x32_bf16 v[88:91], v[112:115], a[244:247], v[88:91]\n"
"v_accvgpr_mov_b32 a19, a67\n"
"v_mfma_f32_16x16x32_bf16 v[88:91], v[116:119], a[248:251], v[88:91]\n"
"ds_read_b128 a[192:195], v243 offset:4096\n"
"v_accvgpr_mov_b32 a5, a53\n"
"v_accvgpr_mov_b32 a17, a65\n"
"v_accvgpr_mov_b32 a23, a71\n"
"v_accvgpr_mov_b32 a26, a74\n"
"v_accvgpr_mov_b32 a27, a75\n"
"v_accvgpr_mov_b32 a37, a85\n"
"v_accvgpr_mov_b32 a46, a94\n"
"s_waitcnt lgkmcnt(3)\n"
"v_mfma_f32_16x16x32_bf16 v[180:183], v[124:127], a[0:3], 0\n"
"ds_read_b128 a[196:199], v148 offset:4096\n"
"v_accvgpr_mov_b32 a15, a63\n"
"v_mfma_f32_16x16x32_bf16 v[180:183], v[128:131], a[4:7], v[180:183]\n"
"s_waitcnt lgkmcnt(3)\n"
"v_mfma_f32_16x16x32_bf16 v[180:183], v[132:135], a[8:11], v[180:183]\n"
"v_accvgpr_mov_b32 a41, a89\n"
"s_waitcnt lgkmcnt(2)\n"
"v_mfma_f32_16x16x32_bf16 v[180:183], v[136:139], a[12:15], v[180:183]\n"
"ds_read_b128 a[204:207], v148 offset:6144\n"
"v_mov_b32_dpp v246, v246 row_ror:8 row_mask:0x4 bank_mask:0xf\n"
"v_mfma_f32_16x16x32_bf16 v[184:187], v[124:127], a[16:19], 0\n"
"v_accvgpr_mov_b32 a32, a80\n"
"v_mfma_f32_16x16x32_bf16 v[184:187], v[128:131], a[20:23], v[184:187]\n"
"ds_read_b128 a[200:203], v243 offset:6144\n"
"v_accvgpr_mov_b32 a30, a78\n"
"v_mfma_f32_16x16x32_bf16 v[184:187], v[132:135], a[24:27], v[184:187]\n"
"s_add_u32 s40, s56, s53\n"
"v_sub_f32 v181, s74, v181\n"
"v_mfma_f32_16x16x32_bf16 v[184:187], v[136:139], a[28:31], v[184:187]\n"
"buffer_load_dwordx4 v240, s[20:23], s40 offen offset:2048 nt lds\n"
"v_mov_b32_dpp v247, v247 row_ror:12 row_mask:0x2 bank_mask:0xf\n"
"v_mov_b32_dpp v246, v246 row_ror:4 row_mask:0x8 bank_mask:0xf\n"
"v_accvgpr_mov_b32 a42, a90\n"
"v_sub_f32 v180, s74, v180\n"
"v_sub_f32 v183, s74, v183\n"
"v_mfma_f32_16x16x32_bf16 v[188:191], v[124:127], a[32:35], 0\n"
"v_mfma_f32_16x16x32_bf16 v[188:191], v[128:131], a[36:39], v[188:191]\n"
"v_accvgpr_mov_b32 a44, a92\n"
"v_mfma_f32_16x16x32_bf16 v[188:191], v[132:135], a[40:43], v[188:191]\n"
"v_sub_f32 v187, s74, v187\n"
"v_mfma_f32_16x16x32_bf16 v[188:191], v[136:139], a[44:47], v[188:191]\n"
"s_waitcnt lgkmcnt(1)\n"
"ds_read_b64_tr_b16 a[250:251], v236 offset:53248\n"
"v_sub_f32 v186, s74, v186\n"
"v_mfma_f32_16x16x32_bf16 v[192:195], v[124:127], a[192:195], 0\n"
"v_mfma_f32_16x16x32_bf16 v[192:195], v[128:131], a[196:199], v[192:195]\n"
"v_mul_f32_dpp v14, v246, v14 row_newbcast:2 row_mask:0xf bank_mask:0xf\n"
"s_waitcnt lgkmcnt(1)\n"
"v_mfma_f32_16x16x32_bf16 v[192:195], v[132:135], a[200:203], v[192:195]\n"
"ds_read_b64_tr_b16 a[252:253], v235 offset:50176\n"
"v_sub_f32 v182, s74, v182\n"
"v_mfma_f32_16x16x32_bf16 v[192:195], v[136:139], a[204:207], v[192:195]\n"
"ds_read_b64_tr_b16 v[226:227], v239 offset:54272\n"
"ds_read_b64_tr_b16 a[248:249], v236 offset:49152\n"
"v_xor_b32 v244, 0x11000, v242\n"
"v_mul_f32_dpp v12, v246, v12 row_newbcast:0 row_mask:0xf bank_mask:0xf\n"
"v_sub_f32 v185, s74, v185\n"
"v_sub_f32 v188, s74, v188\n"
"v_sub_f32 v189, s74, v189\n"
"v_sub_f32 v190, s74, v190\n"
"v_mul_f32_dpp v27, v246, v27 row_newbcast:3 row_mask:0xf bank_mask:0xf\n"
"v_mfma_f32_16x16x32_bf16 v[152:155], v[208:211], a[0:3], v[180:183]\n"
"v_mul_f32_dpp v10, v246, v10 row_newbcast:2 row_mask:0xf bank_mask:0xf\n"
"v_mfma_f32_16x16x32_bf16 v[152:155], v[212:215], a[4:7], v[152:155]\n"
"v_sub_f32 v184, s74, v184\n"
"v_mfma_f32_16x16x32_bf16 v[152:155], v[216:219], a[8:11], v[152:155]\n"
"s_add_u32 s40, s56, s54\n"
"v_sub_f32 v194, s74, v194\n"
"v_mfma_f32_16x16x32_bf16 v[152:155], v[220:223], a[12:15], v[152:155]\n"
"buffer_load_dwordx4 v240, s[20:23], s40 offen offset:3072 nt lds\n"
"v_mov_b32_dpp v206, v206 row_ror:12 row_mask:0x2 bank_mask:0xf\n"
"v_sub_f32 v191, s74, v191\n"
"v_sub_f32 v192, s74, v192\n"
"v_sub_f32 v193, s74, v193\n"
"v_sub_f32 v195, s74, v195\n"
"v_mfma_f32_16x16x32_bf16 v[156:159], v[216:219], a[24:27], v[184:187]\n"
"s_add_u32 s55, s55, s59\n"
"v_mov_b32_dpp v206, v206 row_ror:8 row_mask:0x4 bank_mask:0xf\n"
"v_mfma_f32_16x16x32_bf16 v[156:159], v[220:223], a[28:31], v[156:159]\n"
"s_add_u32 s41, s55, s57\n"
"v_mfma_f32_16x16x32_bf16 v[156:159], v[208:211], a[16:19], v[156:159]\n"
"s_and_b32 s41, s41, s58\n"
"ds_read_b64_tr_b16 v[224:225], v239 offset:50176\n"
"v_mov_b32_dpp v247, v247 row_ror:8 row_mask:0x4 bank_mask:0xf\n"
"v_mfma_f32_16x16x32_bf16 v[156:159], v[212:215], a[20:23], v[156:159]\n"
"s_andn2_b32 s56, s55, s58\n"
"v_mfma_f32_16x16x32_bf16 v[160:163], v[208:211], a[32:35], v[188:191]\n"
"s_or_b32 s56, s56, s41\n"
"ds_read_b64_tr_b16 a[254:255], v235 offset:54272\n"
"v_mfma_f32_16x16x32_bf16 v[160:163], v[212:215], a[36:39], v[160:163]\n"
"v_xor_b32 v250, 0x1000, v245\n"
"v_mfma_f32_16x16x32_bf16 v[160:163], v[216:219], a[40:43], v[160:163]\n"
"ds_read_b64_tr_b16 a[244:245], v232 offset:49152\n"
"v_mfma_f32_16x16x32_bf16 v[160:163], v[220:223], a[44:47], v[160:163]\n"
"v_mul_f32_dpp v25, v246, v25 row_newbcast:1 row_mask:0xf bank_mask:0xf\n"
"v_mfma_f32_16x16x32_bf16 v[164:167], v[216:219], a[200:203], v[192:195]\n"
"ds_read_b128 v[216:219], v228 offset:5120\n"
"v_mfma_f32_16x16x32_bf16 v[164:167], v[220:223], a[204:207], v[164:167]\n"
"v_mfma_f32_16x16x32_bf16 v[164:167], v[208:211], a[192:195], v[164:167]\n"
"ds_read_b128 v[220:223], v229 offset:5120\n"
"v_mfma_f32_16x16x32_bf16 v[164:167], v[212:215], a[196:199], v[164:167]\n"
"ds_read_b128 v[208:211], v228 offset:4096\n"
"v_mov_b32_dpp v206, v206 row_ror:4 row_mask:0x8 bank_mask:0xf\n"
"v_exp_f32 v161, v161\n"
"v_exp_f32 v162, v162\n"
"s_branch LTC_%=\n"
"LTH1_%=:\n"
"v_mfma_f32_16x16x32_bf16 a[224:227], v[124:127], a[244:247], a[224:227]\n"
"v_exp_f32 v168, v168\n"
"v_mfma_f32_16x16x32_bf16 v[16:19], v[128:131], a[244:247], v[16:19]\n"
"s_waitcnt vmcnt(8) lgkmcnt(0)\n"
"v_exp_f32 v169, v169\n"
"v_mfma_f32_16x16x32_bf16 v[16:19], v[132:135], a[248:251], v[16:19]\n"
"s_barrier\n"
"v_mfma_f32_16x16x32_bf16 v[48:51], v[136:139], a[244:247], v[48:51]\n"
"v_exp_f32 v172, v172\n"
"v_mfma_f32_16x16x32_bf16 v[48:51], v[140:143], a[248:251], v[48:51]\n"
"ds_read_b128 v[216:219], v228 offset:1024\n"
"v_cvt_pk_bf16_f32 v102, v168, v169\n"
"v_mfma_f32_16x16x32_bf16 v[80:83], v[144:147], a[244:247], v[80:83]\n"
"v_exp_f32 v176, v176\n"
"v_mfma_f32_16x16x32_bf16 v[80:83], v[148:151], a[248:251], v[80:83]\n"
"ds_read_b64_tr_b16 a[244:245], v234 offset:49152\n"
"ds_read_b64_tr_b16 a[246:247], v234 offset:53248\n"
"v_cvt_pk_bf16_f32 v123, v154, v155\n"
"v_cvt_pk_bf16_f32 v117, v166, v167\n"
"v_exp_f32 v170, v170\n"
"v_exp_f32 v175, v175\n"
"v_exp_f32 v177, v177\n"
"v_mfma_f32_16x16x32_bf16 v[84:87], v[148:151], v[224:227], v[84:87]\n"
"v_exp_f32 v156, v156\n"
"v_mfma_f32_16x16x32_bf16 v[84:87], v[144:147], a[252:255], v[84:87]\n"
"ds_read_b64_tr_b16 a[248:249], v238 offset:49152\n"
"ds_read_b64_tr_b16 a[250:251], v238 offset:53248\n"
"v_mfma_f32_16x16x32_bf16 v[52:55], v[140:143], v[224:227], v[52:55]\n"
"v_cvt_pk_bf16_f32 v101, v158, v159\n"
"v_cvt_pk_bf16_f32 v118, v176, v177\n"
"v_mfma_f32_16x16x32_bf16 v[52:55], v[136:139], a[252:255], v[52:55]\n"
"v_cvt_pk_bf16_f32 v100, v156, v157\n"
"v_exp_f32 v173, v173\n"
"v_exp_f32 v174, v174\n"
"v_exp_f32 v178, v178\n"
"v_exp_f32 v179, v179\n"
"v_mfma_f32_16x16x32_bf16 v[20:23], v[132:135], v[224:227], v[20:23]\n"
"v_exp_f32 v171, v171\n"
"v_mfma_f32_16x16x32_bf16 v[20:23], v[128:131], a[252:255], v[20:23]\n"
"ds_read_b64_tr_b16 v[224:225], v239 offset:49152\n"
"v_cvt_pk_bf16_f32 v103, v170, v171\n"
"v_mfma_f32_16x16x32_bf16 a[228:231], v[124:127], a[252:255], a[228:231]\n"
"s_waitcnt lgkmcnt(3)\n"
"ds_read_b64_tr_b16 a[252:253], v235 offset:49152\n"
"v_cvt_pk_bf16_f32 v119, v178, v179\n"
"v_mfma_f32_16x16x32_bf16 a[216:219], v[120:123], a[244:247], a[216:219]\n"
"s_add_u32 m0, s44, 81920\n"
"ds_read_b64_tr_b16 a[254:255], v235 offset:53248\n"
"ds_read_b64_tr_b16 v[226:227], v239 offset:53248\n"
"v_smfmac_f32_16x16x64_bf16 v[196:199], a[240:243], v[96:103], v241\n"
"buffer_load_dwordx4 v240, s[20:23], s56 offen nt lds\n"
"v_mfma_f32_16x16x32_bf16 v[8:11], v[96:99], a[244:247], v[8:11]\n"
"s_waitcnt lgkmcnt(4)\n"
"v_cvt_pk_bf16_f32 v109, v162, v163\n"
"v_cvt_pk_bf16_f32 v116, v164, v165\n"
"v_mfma_f32_16x16x32_bf16 v[8:11], v[100:103], a[248:251], v[8:11]\n"
"v_cvt_pk_bf16_f32 v110, v172, v173\n"
"v_cvt_pk_bf16_f32 v111, v174, v175\n"
"v_smfmac_f32_16x16x64_bf16 v[200:203], a[240:243], v[112:119], v241\n"
"ds_read_b128 v[220:223], v229 offset:1024\n"
"v_cvt_pk_bf16_f32 v108, v160, v161\n"
"v_mfma_f32_16x16x32_bf16 v[40:43], v[104:107], a[244:247], v[40:43]\n"
"v_add_f32 v196, v196, v197\n"
"v_and_b32 v245, 0x3f0, v242\n"
"v_mfma_f32_16x16x32_bf16 v[40:43], v[108:111], a[248:251], v[40:43]\n"
"ds_read_b128 v[212:215], v229 offset:0\n"
"v_lshrrev_b32 v245, 1, v245\n"
"v_smfmac_f32_16x16x64_bf16 v[198:201], a[240:243], v[104:111], v241\n"
"v_lshlrev_b32_e64 v244, 24, s43\n"
"v_sub_u32 v245, v242, v245\n"
"v_mfma_f32_16x16x32_bf16 v[72:75], v[112:115], a[244:247], v[72:75]\n"
"ds_read_b64_tr_b16 a[244:245], v234 offset:50176\n"
"ds_read_b64_tr_b16 a[246:247], v234 offset:54272\n"
"v_mfma_f32_16x16x32_bf16 v[72:75], v[116:119], a[248:251], v[72:75]\n"
"ds_read_b128 v[208:211], v228 offset:0\n"
"v_mul_f32 v244, 0x45800000, v244\n"
"v_mfma_f32_16x16x32_bf16 v[202:205], a[240:243], v[120:123], v[202:205]\n"
"s_waitcnt lgkmcnt(5)\n"
"ds_read_b64_tr_b16 a[250:251], v238 offset:54272\n"
"v_add_f32 v198, v198, v199\n"
"v_mfma_f32_16x16x32_bf16 v[76:79], v[116:119], v[224:227], v[76:79]\n"
"v_add_f32 v200, v200, v201\n"
"v_add_f32 v254, v196, v198\n"
"v_mfma_f32_16x16x32_bf16 v[76:79], v[112:115], a[252:255], v[76:79]\n"
"ds_read_b64_tr_b16 a[248:249], v238 offset:50176\n"
"v_add_f32 v254, v254, v200\n"
"v_mfma_f32_16x16x32_bf16 v[44:47], v[108:111], v[224:227], v[44:47]\n"
"v_add_f32 v244, v244, v202\n"
"v_min3_f32 v244, v244, v196, v198\n"
"v_mfma_f32_16x16x32_bf16 v[44:47], v[104:107], a[252:255], v[44:47]\n"
"v_min_f32 v244, v244, v200\n"
"v_rcp_f32 v244, v244\n"
"v_mfma_f32_16x16x32_bf16 v[12:15], v[100:103], v[224:227], v[12:15]\n"
"s_add_u32 s40, s56, s52\n"
"v_add_f32 v254, v254, v202\n"
"v_fmac_f32 v254, 0x68800000, v244\n"
"v_mfma_f32_16x16x32_bf16 v[12:15], v[96:99], a[252:255], v[12:15]\n"
"buffer_load_dwordx4 v240, s[20:23], s40 offen offset:1024 nt lds\n"
"v_mfma_f32_16x16x32_bf16 a[220:223], v[120:123], a[252:255], a[220:223]\n"
"s_waitcnt lgkmcnt(2)\n"
"v_cmp_nge_f32 vcc, s45, v254\n"
"s_cbranch_vccnz LSLOW_%=\nLBACK1_%=:\ns_mov_b64 vcc, s[64:65]\n"
"v_mfma_f32_16x16x32_bf16 a[232:235], v[120:123], a[244:247], a[232:235]\n"
"v_rcp_f32 v246, v196\n"
"v_rcp_f32 v206, v200\n"
"v_mov_b32 v252, v192\n"
"v_accvgpr_mov_b32 a9, a105\n"
"v_accvgpr_mov_b32 a10, a106\n"
"v_accvgpr_mov_b32 a28, a124\n"
"v_accvgpr_mov_b32 a31, a127\n"
"v_accvgpr_mov_b32 a33, a129\n"
"v_accvgpr_mov_b32 a40, a136\n"
"v_bfe_u32 v149, v242, 8, 2\n"
"v_lshl_add_u32 v149, v149, 4, s43\n"
"v_xor_b32 v150, 64, v149\n"
"v_accvgpr_mov_b32 a105, a57\n"
"v_mfma_f32_16x16x32_bf16 v[24:27], v[96:99], a[244:247], v[24:27]\n"
"s_waitcnt lgkmcnt(0)\n"
"ds_read_b128 v[128:131], v150 offset:0\n"
"v_accvgpr_mov_b32 a12, a108\n"
"v_mfma_f32_16x16x32_bf16 v[24:27], v[100:103], a[248:251], v[24:27]\n"
"ds_read_b128 v[124:127], v149 offset:0\n"
"v_accvgpr_mov_b32 a0, a96\n"
"v_accvgpr_mov_b32 a3, a99\n"
"v_accvgpr_mov_b32 a4, a100\n"
"v_accvgpr_mov_b32 a18, a114\n"
"v_accvgpr_mov_b32 a21, a117\n"
"v_accvgpr_mov_b32 a35, a131\n"
"v_accvgpr_mov_b32 a38, a134\n"
"v_accvgpr_mov_b32 a47, a143\n"
"v_accvgpr_mov_b32 a99, a51\n"
"v_mfma_f32_16x16x32_bf16 v[56:59], v[104:107], a[244:247], v[56:59]\n"
"ds_read_b128 v[132:135], v149 offset:1024\n"
"v_rcp_f32 v247, v198\n"
"v_accvgpr_mov_b32 a1, a97\n"
"v_accvgpr_mov_b32 a2, a98\n"
"v_accvgpr_mov_b32 a11, a107\n"
"v_accvgpr_mov_b32 a13, a109\n"
"v_accvgpr_mov_b32 a14, a110\n"
"v_accvgpr_mov_b32 a16, a112\n"
"v_accvgpr_mov_b32 a20, a116\n"
"v_accvgpr_mov_b32 a22, a118\n"
"v_accvgpr_mov_b32 a25, a121\n"
"v_accvgpr_mov_b32 a36, a132\n"
"v_xor_b32 v148, 0x400, v243\n"
"v_mfma_f32_16x16x32_bf16 v[56:59], v[108:111], a[248:251], v[56:59]\n"
"ds_read_b128 v[136:139], v150 offset:1024\n"
"v_mov_b32_dpp v246, v246 row_ror:12 row_mask:0x2 bank_mask:0xf\n"
"v_accvgpr_mov_b32 a6, a102\n"
"v_accvgpr_mov_b32 a7, a103\n"
"v_accvgpr_mov_b32 a8, a104\n"
"v_accvgpr_mov_b32 a24, a120\n"
"v_accvgpr_mov_b32 a29, a125\n"
"v_accvgpr_mov_b32 a34, a130\n"
"v_accvgpr_mov_b32 a39, a135\n"
"v_accvgpr_mov_b32 a43, a139\n"
"v_accvgpr_mov_b32 a45, a141\n"
"v_accvgpr_mov_b32 a103, a55\n"
"v_mfma_f32_16x16x32_bf16 v[88:91], v[112:115], a[244:247], v[88:91]\n"
"v_accvgpr_mov_b32 a19, a115\n"
"v_accvgpr_mov_b32 a129, a81\n"
"v_mfma_f32_16x16x32_bf16 v[88:91], v[116:119], a[248:251], v[88:91]\n"
"s_waitcnt lgkmcnt(2)\n"
"ds_read_b128 a[192:195], v243 offset:4096\n"
"v_accvgpr_mov_b32 a5, a101\n"
"v_accvgpr_mov_b32 a17, a113\n"
"v_accvgpr_mov_b32 a23, a119\n"
"v_accvgpr_mov_b32 a26, a122\n"
"v_accvgpr_mov_b32 a27, a123\n"
"v_accvgpr_mov_b32 a37, a133\n"
"v_accvgpr_mov_b32 a46, a142\n"
"v_accvgpr_mov_b32 a101, a53\n"
"v_accvgpr_mov_b32 a124, a76\n"
"v_accvgpr_mov_b32 a121, a73\n"
"v_mfma_f32_16x16x32_bf16 v[180:183], v[124:127], a[0:3], 0\n"
"ds_read_b128 a[196:199], v148 offset:4096\n"
"v_accvgpr_mov_b32 a15, a111\n"
"v_mfma_f32_16x16x32_bf16 v[180:183], v[128:131], a[4:7], v[180:183]\n"
"s_waitcnt lgkmcnt(3)\n"
"v_accvgpr_mov_b32 a117, a69\n"
"v_accvgpr_mov_b32 a119, a71\n"
"v_mfma_f32_16x16x32_bf16 v[180:183], v[132:135], a[8:11], v[180:183]\n"
"s_waitcnt lgkmcnt(2)\n"
"v_accvgpr_mov_b32 a41, a137\n"
"v_accvgpr_mov_b32 a123, a75\n"
"v_mfma_f32_16x16x32_bf16 v[180:183], v[136:139], a[12:15], v[180:183]\n"
"ds_read_b128 a[204:207], v148 offset:6144\n"
"v_mov_b32_dpp v246, v246 row_ror:8 row_mask:0x4 bank_mask:0xf\n"
"v_mfma_f32_16x16x32_bf16 v[184:187], v[124:127], a[16:19], 0\n"
"v_accvgpr_mov_b32 a32, a128\n"
"v_accvgpr_mov_b32 a112, a64\n"
"v_mfma_f32_16x16x32_bf16 v[184:187], v[128:131], a[20:23], v[184:187]\n"
"ds_read_b128 a[200:203], v243 offset:6144\n"
"v_accvgpr_mov_b32 a30, a126\n"
"v_mfma_f32_16x16x32_bf16 v[184:187], v[132:135], a[24:27], v[184:187]\n"
"s_add_u32 s40, s56, s53\n"
"v_accvgpr_mov_b32 a104, a56\n"
"v_sub_f32 v181, s74, v181\n"
"v_mfma_f32_16x16x32_bf16 v[184:187], v[136:139], a[28:31], v[184:187]\n"
"buffer_load_dwordx4 v240, s[20:23], s40 offen offset:2048 nt lds\n"
"v_mov_b32_dpp v247, v247 row_ror:12 row_mask:0x2 bank_mask:0xf\n"
"v_mov_b32_dpp v246, v246 row_ror:4 row_mask:0x8 bank_mask:0xf\n"
"v_accvgpr_mov_b32 a42, a138\n"
"v_accvgpr_mov_b32 a133, a85\n"
"v_accvgpr_mov_b32 a111, a63\n"
"v_accvgpr_mov_b32 a143, a95\n"
"v_accvgpr_mov_b32 a130, a82\n"
"v_sub_f32 v180, s74, v180\n"
"v_sub_f32 v183, s74, v183\n"
"v_mfma_f32_16x16x32_bf16 v[188:191], v[124:127], a[32:35], 0\n"
"v_accvgpr_mov_b32 a120, a72\n"
"v_accvgpr_mov_b32 a139, a91\n"
"v_mfma_f32_16x16x32_bf16 v[188:191], v[128:131], a[36:39], v[188:191]\n"
"v_accvgpr_mov_b32 a44, a140\n"
"v_accvgpr_mov_b32 a132, a84\n"
"v_mfma_f32_16x16x32_bf16 v[188:191], v[132:135], a[40:43], v[188:191]\n"
"v_accvgpr_mov_b32 a108, a60\n"
"v_sub_f32 v187, s74, v187\n"
"v_mfma_f32_16x16x32_bf16 v[188:191], v[136:139], a[44:47], v[188:191]\n"
"s_waitcnt lgkmcnt(1)\n"
"ds_read_b64_tr_b16 a[250:251], v236 offset:53248\n"
"v_sub_f32 v186, s74, v186\n"
"v_mfma_f32_16x16x32_bf16 v[192:195], v[124:127], a[192:195], 0\n"
"v_accvgpr_mov_b32 a115, a67\n"
"v_accvgpr_mov_b32 a122, a74\n"
"v_mfma_f32_16x16x32_bf16 v[192:195], v[128:131], a[196:199], v[192:195]\n"
"s_waitcnt lgkmcnt(1)\n"
"v_accvgpr_mov_b32 a102, a54\n"
"v_mul_f32_dpp v14, v246, v14 row_newbcast:2 row_mask:0xf bank_mask:0xf\n"
"v_mfma_f32_16x16x32_bf16 v[192:195], v[132:135], a[200:203], v[192:195]\n"
"ds_read_b64_tr_b16 a[252:253], v235 offset:50176\n"
"v_sub_f32 v182, s74, v182\n"
"v_mfma_f32_16x16x32_bf16 v[192:195], v[136:139], a[204:207], v[192:195]\n"
"ds_read_b64_tr_b16 v[226:227], v239 offset:54272\n"
"ds_read_b64_tr_b16 a[248:249], v236 offset:49152\n"
"v_xor_b32 v244, 0x11000, v242\n"
"v_accvgpr_mov_b32 a100, a52\n"
"v_accvgpr_mov_b32 a142, a94\n"
"v_mul_f32_dpp v12, v246, v12 row_newbcast:0 row_mask:0xf bank_mask:0xf\n"
"v_sub_f32 v185, s74, v185\n"
"v_sub_f32 v188, s74, v188\n"
"v_sub_f32 v189, s74, v189\n"
"v_sub_f32 v190, s74, v190\n"
"v_mul_f32_dpp v27, v246, v27 row_newbcast:3 row_mask:0xf bank_mask:0xf\n"
"v_mfma_f32_16x16x32_bf16 v[152:155], v[208:211], a[0:3], v[180:183]\n"
"v_accvgpr_mov_b32 a135, a87\n"
"v_mul_f32_dpp v10, v246, v10 row_newbcast:2 row_mask:0xf bank_mask:0xf\n"
"v_mfma_f32_16x16x32_bf16 v[152:155], v[212:215], a[4:7], v[152:155]\n"
"v_accvgpr_mov_b32 a126, a78\n"
"v_sub_f32 v184, s74, v184\n"
"v_mfma_f32_16x16x32_bf16 v[152:155], v[216:219], a[8:11], v[152:155]\n"
"s_add_u32 s40, s56, s54\n"
"v_accvgpr_mov_b32 a110, a62\n"
"v_sub_f32 v194, s74, v194\n"
"v_mfma_f32_16x16x32_bf16 v[152:155], v[220:223], a[12:15], v[152:155]\n"
"buffer_load_dwordx4 v240, s[20:23], s40 offen offset:3072 nt lds\n"
"v_mov_b32_dpp v206, v206 row_ror:12 row_mask:0x2 bank_mask:0xf\n"
"v_accvgpr_mov_b32 a134, a86\n"
"v_accvgpr_mov_b32 a113, a65\n"
"v_accvgpr_mov_b32 a128, a80\n"
"v_accvgpr_mov_b32 a107, a59\n"
"v_sub_f32 v191, s74, v191\n"
"v_sub_f32 v192, s74, v192\n"
"v_sub_f32 v193, s74, v193\n"
"v_sub_f32 v195, s74, v195\n"
"v_mfma_f32_16x16x32_bf16 v[156:159], v[216:219], a[24:27], v[184:187]\n"
"s_add_u32 s55, s55, s59\n"
"v_mov_b32_dpp v206, v206 row_ror:8 row_mask:0x4 bank_mask:0xf\n"
"v_accvgpr_mov_b32 a138, a90\n"
"v_mfma_f32_16x16x32_bf16 v[156:159], v[220:223], a[28:31], v[156:159]\n"
"s_add_u32 s41, s55, s57\n"
"v_accvgpr_mov_b32 a140, a92\n"
"v_accvgpr_mov_b32 a97, a49\n"
"v_mfma_f32_16x16x32_bf16 v[156:159], v[208:211], a[16:19], v[156:159]\n"
"s_and_b32 s41, s41, s58\n"
"ds_read_b64_tr_b16 v[224:225], v239 offset:50176\n"
"v_mov_b32_dpp v247, v247 row_ror:8 row_mask:0x4 bank_mask:0xf\n"
"v_mfma_f32_16x16x32_bf16 v[156:159], v[212:215], a[20:23], v[156:159]\n"
"s_andn2_b32 s56, s55, s58\n"
"v_accvgpr_mov_b32 a96, a48\n"
"v_accvgpr_mov_b32 a98, a50\n"
"v_mfma_f32_16x16x32_bf16 v[160:163], v[208:211], a[32:35], v[188:191]\n"
"s_or_b32 s56, s56, s41\n"
"ds_read_b64_tr_b16 a[254:255], v235 offset:54272\n"
"v_accvgpr_mov_b32 a114, a66\n"
"v_mfma_f32_16x16x32_bf16 v[160:163], v[212:215], a[36:39], v[160:163]\n"
"v_xor_b32 v250, 0x1000, v245\n"
"v_accvgpr_mov_b32 a141, a93\n"
"v_mfma_f32_16x16x32_bf16 v[160:163], v[216:219], a[40:43], v[160:163]\n"
"ds_read_b64_tr_b16 a[244:245], v232 offset:49152\n"
"v_accvgpr_mov_b32 a118, a70\n"
"v_mfma_f32_16x16x32_bf16 v[160:163], v[220:223], a[44:47], v[160:163]\n"
"v_accvgpr_mov_b32 a127, a79\n"
"v_mul_f32_dpp v25, v246, v25 row_newbcast:1 row_mask:0xf bank_mask:0xf\n"
"v_mfma_f32_16x16x32_bf16 v[164:167], v[216:219], a[200:203], v[192:195]\n"
"ds_read_b128 v[216:219], v228 offset:5120\n"
"v_accvgpr_mov_b32 a116, a68\n"
"v_mfma_f32_16x16x32_bf16 v[164:167], v[220:223], a[204:207], v[164:167]\n"
"v_accvgpr_mov_b32 a125, a77\n"
"v_accvgpr_mov_b32 a106, a58\n"
"v_mfma_f32_16x16x32_bf16 v[164:167], v[208:211], a[192:195], v[164:167]\n"
"ds_read_b128 v[220:223], v229 offset:5120\n"
"v_accvgpr_mov_b32 a136, a88\n"
"v_mfma_f32_16x16x32_bf16 v[164:167], v[212:215], a[196:199], v[164:167]\n"
"s_waitcnt lgkmcnt(4)\n"
"ds_read_b128 v[208:211], v228 offset:4096\n"
"v_mov_b32_dpp v206, v206 row_ror:4 row_mask:0x8 bank_mask:0xf\n"
"v_accvgpr_mov_b32 a109, a61\n"
"v_accvgpr_mov_b32 a131, a83\n"
"v_accvgpr_mov_b32 a137, a89\n"
"v_exp_f32 v161, v161\n"
"v_exp_f32 v162, v162\n"
"s_branch LTC_%=\n"
"LTH2_%=:\n"
"v_mfma_f32_16x16x32_bf16 a[224:227], v[124:127], a[244:247], a[224:227]\n"
"v_exp_f32 v168, v168\n"
"v_mfma_f32_16x16x32_bf16 v[16:19], v[128:131], a[244:247], v[16:19]\n"
"s_waitcnt vmcnt(8) lgkmcnt(0)\n"
"v_exp_f32 v169, v169\n"
"v_mfma_f32_16x16x32_bf16 v[16:19], v[132:135], a[248:251], v[16:19]\n"
"s_barrier\n"
"v_mfma_f32_16x16x32_bf16 v[48:51], v[136:139], a[244:247], v[48:51]\n"
"v_exp_f32 v172, v172\n"
"v_mfma_f32_16x16x32_bf16 v[48:51], v[140:143], a[248:251], v[48:51]\n"
"ds_read_b128 v[216:219], v228 offset:1024\n"
"v_cvt_pk_bf16_f32 v102, v168, v169\n"
"v_mfma_f32_16x16x32_bf16 v[80:83], v[144:147], a[244:247], v[80:83]\n"
"v_exp_f32 v176, v176\n"
"v_mfma_f32_16x16x32_bf16 v[80:83], v[148:151], a[248:251], v[80:83]\n"
"ds_read_b64_tr_b16 a[244:245], v234 offset:49152\n"
"ds_read_b64_tr_b16 a[246:247], v234 offset:53248\n"
"v_cvt_pk_bf16_f32 v123, v154, v155\n"
"v_cvt_pk_bf16_f32 v117, v166, v167\n"
"v_exp_f32 v170, v170\n"
"v_exp_f32 v175, v175\n"
"v_exp_f32 v177, v177\n"
"v_mfma_f32_16x16x32_bf16 v[84:87], v[148:151], v[224:227], v[84:87]\n"
"v_exp_f32 v156, v156\n"
"v_mfma_f32_16x16x32_bf16 v[84:87], v[144:147], a[252:255], v[84:87]\n"
"ds_read_b64_tr_b16 a[248:249], v238 offset:49152\n"
"ds_read_b64_tr_b16 a[250:251], v238 offset:53248\n"
"v_mfma_f32_16x16x32_bf16 v[52:55], v[140:143], v[224:227], v[52:55]\n"
"v_cvt_pk_bf16_f32 v101, v158, v159\n"
"v_cvt_pk_bf16_f32 v118, v176, v177\n"
"v_mfma_f32_16x16x32_bf16 v[52:55], v[136:139], a[252:255], v[52:55]\n"
"v_cvt_pk_bf16_f32 v100, v156, v157\n"
"v_exp_f32 v173, v173\n"
"v_exp_f32 v174, v174\n"
"v_exp_f32 v178, v178\n"
"v_exp_f32 v179, v179\n"
"v_mfma_f32_16x16x32_bf16 v[20:23], v[132:135], v[224:227], v[20:23]\n"
"v_exp_f32 v171, v171\n"
"v_mfma_f32_16x16x32_bf16 v[20:23], v[128:131], a[252:255], v[20:23]\n"
"ds_read_b64_tr_b16 v[224:225], v239 offset:49152\n"
"v_cvt_pk_bf16_f32 v103, v170, v171\n"
"v_mfma_f32_16x16x32_bf16 a[228:231], v[124:127], a[252:255], a[228:231]\n"
"s_waitcnt lgkmcnt(3)\n"
"ds_read_b64_tr_b16 a[252:253], v235 offset:49152\n"
"v_cvt_pk_bf16_f32 v119, v178, v179\n"
"v_mfma_f32_16x16x32_bf16 a[216:219], v[120:123], a[244:247], a[216:219]\n"
"s_add_u32 m0, s44, 81920\n"
"ds_read_b64_tr_b16 a[254:255], v235 offset:53248\n"
"ds_read_b64_tr_b16 v[226:227], v239 offset:53248\n"
"v_smfmac_f32_16x16x64_bf16 v[196:199], a[240:243], v[96:103], v241\n"
"buffer_load_dwordx4 v240, s[20:23], s56 offen nt lds\n"
"v_mfma_f32_16x16x32_bf16 v[8:11], v[96:99], a[244:247], v[8:11]\n"
"s_waitcnt lgkmcnt(4)\n"
"v_cvt_pk_bf16_f32 v109, v162, v163\n"
"v_cvt_pk_bf16_f32 v116, v164, v165\n"
"v_mfma_f32_16x16x32_bf16 v[8:11], v[100:103], a[248:251], v[8:11]\n"
"v_cvt_pk_bf16_f32 v110, v172, v173\n"
"v_cvt_pk_bf16_f32 v111, v174, v175\n"
"v_smfmac_f32_16x16x64_bf16 v[200:203], a[240:243], v[112:119], v241\n"
"ds_read_b128 v[220:223], v229 offset:1024\n"
"v_cvt_pk_bf16_f32 v108, v160, v161\n"
"v_mfma_f32_16x16x32_bf16 v[40:43], v[104:107], a[244:247], v[40:43]\n"
"v_add_f32 v196, v196, v197\n"
"v_and_b32 v245, 0x3f0, v242\n"
"v_mfma_f32_16x16x32_bf16 v[40:43], v[108:111], a[248:251], v[40:43]\n"
"ds_read_b128 v[212:215], v229 offset:0\n"
"v_lshrrev_b32 v245, 1, v245\n"
"v_smfmac_f32_16x16x64_bf16 v[198:201], a[240:243], v[104:111], v241\n"
"v_lshlrev_b32_e64 v244, 24, s43\n"
"v_sub_u32 v245, v242, v245\n"
"v_mfma_f32_16x16x32_bf16 v[72:75], v[112:115], a[244:247], v[72:75]\n"
"ds_read_b64_tr_b16 a[244:245], v234 offset:50176\n"
"ds_read_b64_tr_b16 a[246:247], v234 offset:54272\n"
"v_mfma_f32_16x16x32_bf16 v[72:75], v[116:119], a[248:251], v[72:75]\n"
"ds_read_b128 v[208:211], v228 offset:0\n"
"v_mul_f32 v244, 0x45800000, v244\n"
"v_mfma_f32_16x16x32_bf16 v[202:205], a[240:243], v[120:123], v[202:205]\n"
"s_waitcnt lgkmcnt(5)\n"
"ds_read_b64_tr_b16 a[250:251], v238 offset:54272\n"
"v_add_f32 v198, v198, v199\n"
"v_mfma_f32_16x16x32_bf16 v[76:79], v[116:119], v[224:227], v[76:79]\n"
"v_add_f32 v200, v200, v201\n"
"v_add_f32 v254, v196, v198\n"
"v_mfma_f32_16x16x32_bf16 v[76:79], v[112:115], a[252:255], v[76:79]\n"
"ds_read_b64_tr_b16 a[248:249], v238 offset:50176\n"
"v_add_f32 v254, v254, v200\n"
"v_mfma_f32_16x16x32_bf16 v[44:47], v[108:111], v[224:227], v[44:47]\n"
"v_add_f32 v244, v244, v202\n"
"v_min3_f32 v244, v244, v196, v198\n"
"v_mfma_f32_16x16x32_bf16 v[44:47], v[104:107], a[252:255], v[44:47]\n"
"v_min_f32 v244, v244, v200\n"
"v_rcp_f32 v244, v244\n"
"v_mfma_f32_16x16x32_bf16 v[12:15], v[100:103], v[224:227], v[12:15]\n"
"s_add_u32 s40, s56, s52\n"
"v_add_f32 v254, v254, v202\n"
"v_fmac_f32 v254, 0x68800000, v244\n"
"v_mfma_f32_16x16x32_bf16 v[12:15], v[96:99], a[252:255], v[12:15]\n"
"buffer_load_dwordx4 v240, s[20:23], s40 offen offset:1024 nt lds\n"
"v_mfma_f32_16x16x32_bf16 a[220:223], v[120:123], a[252:255], a[220:223]\n"
"s_waitcnt lgkmcnt(2)\n"
"v_cmp_nge_f32 vcc, s45, v254\n"
"s_cbranch_vccnz LSLOW_%=\nLBACK2_%=:\ns_mov_b64 vcc, s[64:65]\n"
"v_mfma_f32_16x16x32_bf16 a[232:235], v[120:123], a[244:247], a[232:235]\n"
"v_rcp_f32 v246, v196\n"
"v_rcp_f32 v206, v200\n"
"v_mov_b32 v252, v192\n"
"v_accvgpr_mov_b32 a9, a153\n"
"v_accvgpr_mov_b32 a10, a154\n"
"v_accvgpr_mov_b32 a28, a172\n"
"v_accvgpr_mov_b32 a31, a175\n"
"v_accvgpr_mov_b32 a33, a177\n"
"v_accvgpr_mov_b32 a40, a184\n"
"v_bfe_u32 v149, v242, 8, 2\n"
"v_lshl_add_u32 v149, v149, 4, s43\n"
"v_xor_b32 v150, 64, v149\n"
"v_accvgpr_mov_b32 a153, a57\n"
"v_mfma_f32_16x16x32_bf16 v[24:27], v[96:99], a[244:247], v[24:27]\n"
"s_waitcnt lgkmcnt(0)\n"
"ds_read_b128 v[128:131], v150 offset:0\n"
"v_accvgpr_mov_b32 a12, a156\n"
"v_mfma_f32_16x16x32_bf16 v[24:27], v[100:103], a[248:251], v[24:27]\n"
"ds_read_b128 v[124:127], v149 offset:0\n"
"v_accvgpr_mov_b32 a0, a144\n"
"v_accvgpr_mov_b32 a3, a147\n"
"v_accvgpr_mov_b32 a4, a148\n"
"v_accvgpr_mov_b32 a18, a162\n"
"v_accvgpr_mov_b32 a21, a165\n"
"v_accvgpr_mov_b32 a35, a179\n"
"v_accvgpr_mov_b32 a38, a182\n"
"v_accvgpr_mov_b32 a47, a191\n"
"v_accvgpr_mov_b32 a147, a51\n"
"v_mfma_f32_16x16x32_bf16 v[56:59], v[104:107], a[244:247], v[56:59]\n"
"ds_read_b128 v[132:135], v149 offset:1024\n"
"v_rcp_f32 v247, v198\n"
"v_accvgpr_mov_b32 a1, a145\n"
"v_accvgpr_mov_b32 a2, a146\n"
"v_accvgpr_mov_b32 a11, a155\n"
"v_accvgpr_mov_b32 a13, a157\n"
"v_accvgpr_mov_b32 a14, a158\n"
"v_accvgpr_mov_b32 a16, a160\n"
"v_accvgpr_mov_b32 a20, a164\n"
"v_accvgpr_mov_b32 a22, a166\n"
"v_accvgpr_mov_b32 a25, a169\n"
"v_accvgpr_mov_b32 a36, a180\n"
"v_xor_b32 v148, 0x400, v243\n"
"v_mfma_f32_16x16x32_bf16 v[56:59], v[108:111], a[248:251], v[56:59]\n"
"ds_read_b128 v[136:139], v150 offset:1024\n"
"v_mov_b32_dpp v246, v246 row_ror:12 row_mask:0x2 bank_mask:0xf\n"
"v_accvgpr_mov_b32 a6, a150\n"
"v_accvgpr_mov_b32 a7, a151\n"
"v_accvgpr_mov_b32 a8, a152\n"
"v_accvgpr_mov_b32 a24, a168\n"
"v_accvgpr_mov_b32 a29, a173\n"
"v_accvgpr_mov_b32 a34, a178\n"
"v_accvgpr_mov_b32 a39, a183\n"
"v_accvgpr_mov_b32 a43, a187\n"
"v_accvgpr_mov_b32 a45, a189\n"
"v_accvgpr_mov_b32 a151, a55\n"
"v_mfma_f32_16x16x32_bf16 v[88:91], v[112:115], a[244:247], v[88:91]\n"
"v_accvgpr_mov_b32 a19, a163\n"
"v_accvgpr_mov_b32 a177, a81\n"
"v_mfma_f32_16x16x32_bf16 v[88:91], v[116:119], a[248:251], v[88:91]\n"
"s_waitcnt lgkmcnt(2)\n"
"ds_read_b128 a[192:195], v243 offset:4096\n"
"v_accvgpr_mov_b32 a5, a149\n"
"v_accvgpr_mov_b32 a17, a161\n"
"v_accvgpr_mov_b32 a23, a167\n"
"v_accvgpr_mov_b32 a26, a170\n"
"v_accvgpr_mov_b32 a27, a171\n"
"v_accvgpr_mov_b32 a37, a181\n"
"v_accvgpr_mov_b32 a46, a190\n"
"v_accvgpr_mov_b32 a149, a53\n"
"v_accvgpr_mov_b32 a172, a76\n"
"v_accvgpr_mov_b32 a169, a73\n"
"v_mfma_f32_16x16x32_bf16 v[180:183], v[124:127], a[0:3], 0\n"
"ds_read_b128 a[196:199], v148 offset:4096\n"
"v_accvgpr_mov_b32 a15, a159\n"
"v_mfma_f32_16x16x32_bf16 v[180:183], v[128:131], a[4:7], v[180:183]\n"
"s_waitcnt lgkmcnt(3)\n"
"v_accvgpr_mov_b32 a165, a69\n"
"v_accvgpr_mov_b32 a167, a71\n"
"v_mfma_f32_16x16x32_bf16 v[180:183], v[132:135], a[8:11], v[180:183]\n"
"s_waitcnt lgkmcnt(2)\n"
"v_accvgpr_mov_b32 a41, a185\n"
"v_accvgpr_mov_b32 a171, a75\n"
"v_mfma_f32_16x16x32_bf16 v[180:183], v[136:139], a[12:15], v[180:183]\n"
"ds_read_b128 a[204:207], v148 offset:6144\n"
"v_mov_b32_dpp v246, v246 row_ror:8 row_mask:0x4 bank_mask:0xf\n"
"v_mfma_f32_16x16x32_bf16 v[184:187], v[124:127], a[16:19], 0\n"
"v_accvgpr_mov_b32 a32, a176\n"
"v_accvgpr_mov_b32 a160, a64\n"
"v_mfma_f32_16x16x32_bf16 v[184:187], v[128:131], a[20:23], v[184:187]\n"
"ds_read_b128 a[200:203], v243 offset:6144\n"
"v_accvgpr_mov_b32 a30, a174\n"
"v_mfma_f32_16x16x32_bf16 v[184:187], v[132:135], a[24:27], v[184:187]\n"
"s_add_u32 s40, s56, s53\n"
"v_accvgpr_mov_b32 a152, a56\n"
"v_sub_f32 v181, s74, v181\n"
"v_mfma_f32_16x16x32_bf16 v[184:187], v[136:139], a[28:31], v[184:187]\n"
"buffer_load_dwordx4 v240, s[20:23], s40 offen offset:2048 nt lds\n"
"v_mov_b32_dpp v247, v247 row_ror:12 row_mask:0x2 bank_mask:0xf\n"
"v_mov_b32_dpp v246, v246 row_ror:4 row_mask:0x8 bank_mask:0xf\n"
"v_accvgpr_mov_b32 a42, a186\n"
"v_accvgpr_mov_b32 a181, a85\n"
"v_accvgpr_mov_b32 a159, a63\n"
"v_accvgpr_mov_b32 a191, a95\n"
"v_accvgpr_mov_b32 a178, a82\n"
"v_sub_f32 v180, s74, v180\n"
"v_sub_f32 v183, s74, v183\n"
"v_mfma_f32_16x16x32_bf16 v[188:191], v[124:127], a[32:35], 0\n"
"v_accvgpr_mov_b32 a168, a72\n"
"v_accvgpr_mov_b32 a187, a91\n"
"v_mfma_f32_16x16x32_bf16 v[188:191], v[128:131], a[36:39], v[188:191]\n"
"v_accvgpr_mov_b32 a44, a188\n"
"v_accvgpr_mov_b32 a180, a84\n"
"v_mfma_f32_16x16x32_bf16 v[188:191], v[132:135], a[40:43], v[188:191]\n"
"v_accvgpr_mov_b32 a156, a60\n"
"v_sub_f32 v187, s74, v187\n"
"v_mfma_f32_16x16x32_bf16 v[188:191], v[136:139], a[44:47], v[188:191]\n"
"s_waitcnt lgkmcnt(1)\n"
"ds_read_b64_tr_b16 a[250:251], v236 offset:53248\n"
"v_sub_f32 v186, s74, v186\n"
"v_mfma_f32_16x16x32_bf16 v[192:195], v[124:127], a[192:195], 0\n"
"v_accvgpr_mov_b32 a163, a67\n"
"v_accvgpr_mov_b32 a170, a74\n"
"v_mfma_f32_16x16x32_bf16 v[192:195], v[128:131], a[196:199], v[192:195]\n"
"s_waitcnt lgkmcnt(1)\n"
"v_accvgpr_mov_b32 a150, a54\n"
"v_mul_f32_dpp v14, v246, v14 row_newbcast:2 row_mask:0xf bank_mask:0xf\n"
"v_mfma_f32_16x16x32_bf16 v[192:195], v[132:135], a[200:203], v[192:195]\n"
"ds_read_b64_tr_b16 a[252:253], v235 offset:50176\n"
"v_sub_f32 v182, s74, v182\n"
"v_mfma_f32_16x16x32_bf16 v[192:195], v[136:139], a[204:207], v[192:195]\n"
"ds_read_b64_tr_b16 v[226:227], v239 offset:54272\n"
"ds_read_b64_tr_b16 a[248:249], v236 offset:49152\n"
"v_xor_b32 v244, 0x11000, v242\n"
"v_accvgpr_mov_b32 a148, a52\n"
"v_accvgpr_mov_b32 a190, a94\n"
"v_mul_f32_dpp v12, v246, v12 row_newbcast:0 row_mask:0xf bank_mask:0xf\n"
"v_sub_f32 v185, s74, v185\n"
"v_sub_f32 v188, s74, v188\n"
"v_sub_f32 v189, s74, v189\n"
"v_sub_f32 v190, s74, v190\n"
"v_mul_f32_dpp v27, v246, v27 row_newbcast:3 row_mask:0xf bank_mask:0xf\n"
"v_mfma_f32_16x16x32_bf16 v[152:155], v[208:211], a[0:3], v[180:183]\n"
"v_accvgpr_mov_b32 a183, a87\n"
"v_mul_f32_dpp v10, v246, v10 row_newbcast:2 row_mask:0xf bank_mask:0xf\n"
"v_mfma_f32_16x16x32_bf16 v[152:155], v[212:215], a[4:7], v[152:155]\n"
"v_accvgpr_mov_b32 a174, a78\n"
"v_sub_f32 v184, s74, v184\n"
"v_mfma_f32_16x16x32_bf16 v[152:155], v[216:219], a[8:11], v[152:155]\n"
"s_add_u32 s40, s56, s54\n"
"v_accvgpr_mov_b32 a158, a62\n"
"v_sub_f32 v194, s74, v194\n"
"v_mfma_f32_16x16x32_bf16 v[152:155], v[220:223], a[12:15], v[152:155]\n"
"buffer_load_dwordx4 v240, s[20:23], s40 offen offset:3072 nt lds\n"
"v_mov_b32_dpp v206, v206 row_ror:12 row_mask:0x2 bank_mask:0xf\n"
"v_accvgpr_mov_b32 a182, a86\n"
"v_accvgpr_mov_b32 a161, a65\n"
"v_accvgpr_mov_b32 a176, a80\n"
"v_accvgpr_mov_b32 a155, a59\n"
"v_sub_f32 v191, s74, v191\n"
"v_sub_f32 v192, s74, v192\n"
"v_sub_f32 v193, s74, v193\n"
"v_sub_f32 v195, s74, v195\n"
"v_mfma_f32_16x16x32_bf16 v[156:159], v[216:219], a[24:27], v[184:187]\n"
"s_add_u32 s55, s55, s59\n"
"v_mov_b32_dpp v206, v206 row_ror:8 row_mask:0x4 bank_mask:0xf\n"
"v_accvgpr_mov_b32 a186, a90\n"
"v_mfma_f32_16x16x32_bf16 v[156:159], v[220:223], a[28:31], v[156:159]\n"
"s_add_u32 s41, s55, s57\n"
"v_accvgpr_mov_b32 a188, a92\n"
"v_accvgpr_mov_b32 a145, a49\n"
"v_mfma_f32_16x16x32_bf16 v[156:159], v[208:211], a[16:19], v[156:159]\n"
"s_and_b32 s41, s41, s58\n"
"ds_read_b64_tr_b16 v[224:225], v239 offset:50176\n"
"v_mov_b32_dpp v247, v247 row_ror:8 row_mask:0x4 bank_mask:0xf\n"
"v_mfma_f32_16x16x32_bf16 v[156:159], v[212:215], a[20:23], v[156:159]\n"
"s_andn2_b32 s56, s55, s58\n"
"v_accvgpr_mov_b32 a144, a48\n"
"v_accvgpr_mov_b32 a146, a50\n"
"v_mfma_f32_16x16x32_bf16 v[160:163], v[208:211], a[32:35], v[188:191]\n"
"s_or_b32 s56, s56, s41\n"
"ds_read_b64_tr_b16 a[254:255], v235 offset:54272\n"
"v_accvgpr_mov_b32 a162, a66\n"
"v_mfma_f32_16x16x32_bf16 v[160:163], v[212:215], a[36:39], v[160:163]\n"
"v_xor_b32 v250, 0x1000, v245\n"
"v_accvgpr_mov_b32 a189, a93\n"
"v_mfma_f32_16x16x32_bf16 v[160:163], v[216:219], a[40:43], v[160:163]\n"
"ds_read_b64_tr_b16 a[244:245], v232 offset:49152\n"
"v_accvgpr_mov_b32 a166, a70\n"
"v_mfma_f32_16x16x32_bf16 v[160:163], v[220:223], a[44:47], v[160:163]\n"
"v_accvgpr_mov_b32 a175, a79\n"
"v_mul_f32_dpp v25, v246, v25 row_newbcast:1 row_mask:0xf bank_mask:0xf\n"
"v_mfma_f32_16x16x32_bf16 v[164:167], v[216:219], a[200:203], v[192:195]\n"
"ds_read_b128 v[216:219], v228 offset:5120\n"
"v_accvgpr_mov_b32 a164, a68\n"
"v_mfma_f32_16x16x32_bf16 v[164:167], v[220:223], a[204:207], v[164:167]\n"
"v_accvgpr_mov_b32 a173, a77\n"
"v_accvgpr_mov_b32 a154, a58\n"
"v_mfma_f32_16x16x32_bf16 v[164:167], v[208:211], a[192:195], v[164:167]\n"
"ds_read_b128 v[220:223], v229 offset:5120\n"
"v_accvgpr_mov_b32 a184, a88\n"
"v_mfma_f32_16x16x32_bf16 v[164:167], v[212:215], a[196:199], v[164:167]\n"
"s_waitcnt lgkmcnt(4)\n"
"ds_read_b128 v[208:211], v228 offset:4096\n"
"v_mov_b32_dpp v206, v206 row_ror:4 row_mask:0x8 bank_mask:0xf\n"
"v_accvgpr_mov_b32 a157, a61\n"
"v_accvgpr_mov_b32 a179, a83\n"
"v_accvgpr_mov_b32 a185, a89\n"
"v_exp_f32 v161, v161\n"
"v_exp_f32 v162, v162\n"
"LTC_%=:\n"
"s_waitcnt lgkmcnt(5)\n"
"v_mfma_f32_16x16x32_bf16 v[92:95], v[116:119], v[224:227], v[92:95]\n"
"s_waitcnt lgkmcnt(4)\n"
"ds_read_b128 v[212:215], v229 offset:4096\n"
"v_mul_f32_dpp v11, v246, v11 row_newbcast:3 row_mask:0xf bank_mask:0xf\n"
"v_mfma_f32_16x16x32_bf16 v[92:95], v[112:115], a[252:255], v[92:95]\n"
"v_mov_b32_dpp v247, v247 row_ror:4 row_mask:0x8 bank_mask:0xf\n"
"v_mov_b32 v253, v202\n"
"v_mfma_f32_16x16x32_bf16 v[60:63], v[108:111], v[224:227], v[60:63]\n"
"s_add_u32 m0, s44, 32768\n"
"ds_read_b64_tr_b16 a[246:247], v232 offset:53248\n"
"v_mul_f32_dpp v46, v247, v46 row_newbcast:2 row_mask:0xf bank_mask:0xf\n"
"v_mfma_f32_16x16x32_bf16 v[60:63], v[104:107], a[252:255], v[60:63]\n"
"buffer_load_dwordx4 v240, s[16:19], s56 offen nt lds\n"
"v_mfma_f32_16x16x32_bf16 v[28:31], v[100:103], v[224:227], v[28:31]\n"
"v_exp_f32 v154, v154\n"
"v_mfma_f32_16x16x32_bf16 v[28:31], v[96:99], a[252:255], v[28:31]\n"
"v_mul_f32_dpp v13, v246, v13 row_newbcast:1 row_mask:0xf bank_mask:0xf\n"
"v_mul_f32_dpp v26, v246, v26 row_newbcast:2 row_mask:0xf bank_mask:0xf\n"
"v_mfma_f32_16x16x32_bf16 a[236:239], v[120:123], a[252:255], a[236:239]\n"
"s_waitcnt lgkmcnt(3)\n"
"ds_write_b128 v244, a[216:219] offset:32768\n"
"v_mul_f32_dpp v15, v246, v15 row_newbcast:3 row_mask:0xf bank_mask:0xf\n"
"v_exp_f32 v155, v155\n"
"v_exp_f32 v156, v156\n"
"v_exp_f32 v158, v158\n"
"v_exp_f32 v160, v160\n"
"v_exp_f32 v163, v163\n"
"v_exp_f32 v166, v166\n"
"v_mul_f32_dpp v24, v246, v24 row_newbcast:0 row_mask:0xf bank_mask:0xf\n"
"v_mfma_f32_16x16x32_bf16 v[168:171], v[216:219], a[8:11], v[180:183]\n"
"v_exp_f32 v159, v159\n"
"v_mfma_f32_16x16x32_bf16 v[168:171], v[220:223], a[12:15], v[168:171]\n"
"s_waitcnt lgkmcnt(2)\n"
"ds_read_b64_tr_b16 v[226:227], v237 offset:53248\n"
"v_mul_f32_dpp v74, v206, v74 row_newbcast:2 row_mask:0xf bank_mask:0xf\n"
"v_mfma_f32_16x16x32_bf16 v[168:171], v[208:211], a[0:3], v[168:171]\n"
"v_exp_f32 v153, v153\n"
"v_mfma_f32_16x16x32_bf16 v[168:171], v[212:215], a[4:7], v[168:171]\n"
"v_mul_f32_dpp v41, v247, v41 row_newbcast:1 row_mask:0xf bank_mask:0xf\n"
"v_cvt_pk_bf16_f32 v144, v160, v161\n"
"v_mfma_f32_16x16x32_bf16 v[172:175], v[208:211], a[16:19], v[184:187]\n"
"v_exp_f32 v157, v157\n"
"v_mfma_f32_16x16x32_bf16 v[172:175], v[212:215], a[20:23], v[172:175]\n"
"v_exp_f32 v152, v152\n"
"v_mfma_f32_16x16x32_bf16 v[172:175], v[216:219], a[24:27], v[172:175]\n"
"v_exp_f32 v164, v164\n"
"v_mfma_f32_16x16x32_bf16 v[172:175], v[220:223], a[28:31], v[172:175]\n"
"ds_read_b64_tr_b16 a[254:255], v233 offset:53248\n"
"v_mul_f32_dpp v9, v246, v9 row_newbcast:1 row_mask:0xf bank_mask:0xf\n"
"v_mfma_f32_16x16x32_bf16 v[176:179], v[216:219], a[40:43], v[188:191]\n"
"ds_read_b64_tr_b16 v[224:225], v237 offset:49152\n"
"v_mul_f32_dpp v73, v206, v73 row_newbcast:1 row_mask:0xf bank_mask:0xf\n"
"v_mfma_f32_16x16x32_bf16 v[176:179], v[220:223], a[44:47], v[176:179]\n"
"v_cvt_pk_bf16_f32 v128, v152, v153\n"
"v_cvt_pk_bf16_f32 v129, v154, v155\n"
"v_mfma_f32_16x16x32_bf16 v[176:179], v[208:211], a[32:35], v[176:179]\n"
"s_add_u32 s40, s56, s52\n"
"v_mul_f32_dpp v43, v247, v43 row_newbcast:3 row_mask:0xf bank_mask:0xf\n"
"v_cvt_pk_bf16_f32 v24, v24, v25\n"
"v_mfma_f32_16x16x32_bf16 v[176:179], v[212:215], a[36:39], v[176:179]\n"
"buffer_load_dwordx4 v240, s[16:19], s40 offen offset:1024 nt lds\n"
"v_mfma_f32_16x16x32_bf16 v[152:155], v[208:211], a[192:195], v[192:195]\n"
"v_cvt_pk_bf16_f32 v137, v158, v159\n"
"v_mul_f32_dpp v62, v247, v62 row_newbcast:2 row_mask:0xf bank_mask:0xf\n"
"v_mfma_f32_16x16x32_bf16 v[152:155], v[212:215], a[196:199], v[152:155]\n"
"v_cvt_pk_bf16_f32 v145, v162, v163\n"
"v_mul_f32_dpp v90, v206, v90 row_newbcast:2 row_mask:0xf bank_mask:0xf\n"
"v_mfma_f32_16x16x32_bf16 v[152:155], v[216:219], a[200:203], v[152:155]\n"
"ds_read_b128 v[212:215], v231 offset:0\n"
"v_mul_f32_dpp v40, v247, v40 row_newbcast:0 row_mask:0xf bank_mask:0xf\n"
"v_mfma_f32_16x16x32_bf16 v[152:155], v[220:223], a[204:207], v[152:155]\n"
"s_waitcnt lgkmcnt(2)\n"
"ds_read_b128 v[208:211], v230 offset:0\n"
"ds_read_b128 v[220:223], v231 offset:1024\n"
"v_mfma_f32_16x16x32_bf16 a[208:211], v[120:123], a[244:247], a[208:211]\n"
"v_exp_f32 v173, v173\n"
"v_mfma_f32_16x16x32_bf16 v[0:3], v[96:99], a[244:247], v[0:3]\n"
"ds_read_b128 v[216:219], v230 offset:1024\n"
"v_mul_f32_dpp v72, v206, v72 row_newbcast:0 row_mask:0xf bank_mask:0xf\n"
"v_mfma_f32_16x16x32_bf16 v[0:3], v[100:103], a[248:251], v[0:3]\n"
"v_exp_f32 v178, v178\n"
"v_mfma_f32_16x16x32_bf16 v[32:35], v[104:107], a[244:247], v[32:35]\n"
"v_exp_f32 v171, v171\n"
"v_mfma_f32_16x16x32_bf16 v[32:35], v[108:111], a[248:251], v[32:35]\n"
"ds_write_b128 v244, a[220:223] offset:33792\n"
"v_exp_f32 v165, v165\n"
"v_exp_f32 v167, v167\n"
"v_mul_f32_dpp v59, v247, v59 row_newbcast:3 row_mask:0xf bank_mask:0xf\n"
"v_exp_f32 v168, v168\n"
"v_mfma_f32_16x16x32_bf16 v[64:67], v[112:115], a[244:247], v[64:67]\n"
"v_exp_f32 v172, v172\n"
"v_mfma_f32_16x16x32_bf16 v[64:67], v[116:119], a[248:251], v[64:67]\n"
"s_waitcnt lgkmcnt(3)\n"
"ds_write_b64 v250, v[252:253] offset:52224\n"
"v_mul_f32_dpp v42, v247, v42 row_newbcast:2 row_mask:0xf bank_mask:0xf\n"
"v_mul_f32_dpp v75, v206, v75 row_newbcast:3 row_mask:0xf bank_mask:0xf\n"
"v_cvt_pk_bf16_f32 v136, v156, v157\n"
"v_cvt_pk_bf16_f32 v125, v166, v167\n"
"v_mul_f32_dpp v29, v246, v29 row_newbcast:1 row_mask:0xf bank_mask:0xf\n"
"v_mul_f32_dpp v57, v247, v57 row_newbcast:1 row_mask:0xf bank_mask:0xf\n"
"v_exp_f32 v155, v155\n"
"v_mul_f32_dpp v0, v246, v0 row_newbcast:0 row_mask:0xf bank_mask:0xf\n"
"v_mul_f32_dpp v1, v246, v1 row_newbcast:1 row_mask:0xf bank_mask:0xf\n"
"v_mfma_f32_16x16x32_bf16 v[156:159], v[208:211], a[0:3], v[180:183]\n"
"v_mul_f32_dpp v45, v247, v45 row_newbcast:1 row_mask:0xf bank_mask:0xf\n"
"v_mul_f32_dpp v89, v206, v89 row_newbcast:1 row_mask:0xf bank_mask:0xf\n"
"v_mfma_f32_16x16x32_bf16 v[156:159], v[212:215], a[4:7], v[156:159]\n"
"s_waitcnt lgkmcnt(2)\n"
"v_mul_f32_dpp v8, v246, v8 row_newbcast:0 row_mask:0xf bank_mask:0xf\n"
"v_mul_f32_dpp v30, v246, v30 row_newbcast:2 row_mask:0xf bank_mask:0xf\n"
"v_mfma_f32_16x16x32_bf16 v[156:159], v[216:219], a[8:11], v[156:159]\n"
"s_add_u32 s40, s56, s53\n"
"ds_read_b64_tr_b16 a[252:253], v233 offset:49152\n"
"v_mul_f32_dpp v56, v247, v56 row_newbcast:0 row_mask:0xf bank_mask:0xf\n"
"v_mfma_f32_16x16x32_bf16 v[156:159], v[220:223], a[12:15], v[156:159]\n"
"buffer_load_dwordx4 v240, s[16:19], s40 offen offset:2048 nt lds\n"
"v_mul_f32_dpp v44, v247, v44 row_newbcast:0 row_mask:0xf bank_mask:0xf\n"
"v_mul_f32_dpp v76, v206, v76 row_newbcast:0 row_mask:0xf bank_mask:0xf\n"
"v_mul_f32_dpp v88, v206, v88 row_newbcast:0 row_mask:0xf bank_mask:0xf\n"
"v_exp_f32 v170, v170\n"
"v_exp_f32 v174, v174\n"
"v_exp_f32 v175, v175\n"
"v_exp_f32 v176, v176\n"
"v_exp_f32 v152, v152\n"
"v_exp_f32 v154, v154\n"
"v_mul_f32_dpp v66, v206, v66 row_newbcast:2 row_mask:0xf bank_mask:0xf\n"
"v_mfma_f32_16x16x32_bf16 v[160:163], v[216:219], a[24:27], v[184:187]\n"
"v_mul_f32_dpp v92, v206, v92 row_newbcast:0 row_mask:0xf bank_mask:0xf\n"
"v_cvt_pk_bf16_f32 v0, v0, v1\n"
"v_mfma_f32_16x16x32_bf16 v[160:163], v[220:223], a[28:31], v[160:163]\n"
"v_mul_f32_dpp v95, v206, v95 row_newbcast:3 row_mask:0xf bank_mask:0xf\n"
"v_cvt_pk_bf16_f32 v131, v170, v171\n"
"v_mfma_f32_16x16x32_bf16 v[160:163], v[208:211], a[16:19], v[160:163]\n"
"v_mul_f32_dpp v58, v247, v58 row_newbcast:2 row_mask:0xf bank_mask:0xf\n"
"v_cvt_pk_bf16_f32 v139, v174, v175\n"
"v_mfma_f32_16x16x32_bf16 v[160:163], v[212:215], a[20:23], v[160:163]\n"
"ds_write_b128 v244, a[236:239] offset:35840\n"
"v_cvt_pk_bf16_f32 v124, v164, v165\n"
"v_mul_f32_dpp v94, v206, v94 row_newbcast:2 row_mask:0xf bank_mask:0xf\n"
"v_exp_f32 v169, v169\n"
"v_exp_f32 v156, v156\n"
"v_mfma_f32_16x16x32_bf16 v[164:167], v[208:211], a[32:35], v[188:191]\n"
"v_exp_f32 v157, v157\n"
"v_mfma_f32_16x16x32_bf16 v[164:167], v[212:215], a[36:39], v[164:167]\n"
"ds_read_b128 v[208:211], v230 offset:4096\n"
"v_cvt_pk_bf16_f32 v8, v8, v9\n"
"v_mfma_f32_16x16x32_bf16 v[164:167], v[216:219], a[40:43], v[164:167]\n"
"v_cvt_pk_bf16_f32 v40, v40, v41\n"
"v_mul_f32_dpp v60, v247, v60 row_newbcast:0 row_mask:0xf bank_mask:0xf\n"
"v_mfma_f32_16x16x32_bf16 v[164:167], v[220:223], a[44:47], v[164:167]\n"
"ds_read_b64_tr_b16 a[244:245], v232 offset:50176\n"
"ds_read_b64_tr_b16 a[248:249], v236 offset:50176\n"
"v_mul_f32_dpp v79, v206, v79 row_newbcast:3 row_mask:0xf bank_mask:0xf\n"
"v_cvt_pk_bf16_f32 v25, v26, v27\n"
"v_mul_f32_dpp v61, v247, v61 row_newbcast:1 row_mask:0xf bank_mask:0xf\n"
"v_exp_f32 v177, v177\n"
"v_exp_f32 v179, v179\n"
"v_exp_f32 v160, v160\n"
"v_exp_f32 v161, v161\n"
"v_mul_f32_dpp v33, v247, v33 row_newbcast:1 row_mask:0xf bank_mask:0xf\n"
"v_mfma_f32_16x16x32_bf16 v[68:71], v[116:119], v[224:227], v[68:71]\n"
"s_waitcnt lgkmcnt(4)\n"
"ds_read_b128 v[220:223], v231 offset:5120\n"
"v_mul_f32_dpp v77, v206, v77 row_newbcast:1 row_mask:0xf bank_mask:0xf\n"
"v_mfma_f32_16x16x32_bf16 v[68:71], v[112:115], a[252:255], v[68:71]\n"
"ds_read_b128 v[212:215], v231 offset:4096\n"
"v_mul_f32_dpp v32, v247, v32 row_newbcast:0 row_mask:0xf bank_mask:0xf\n"
"v_mfma_f32_16x16x32_bf16 v[36:39], v[108:111], v[224:227], v[36:39]\n"
"s_add_u32 s40, s56, s54\n"
"ds_read_b128 v[216:219], v230 offset:5120\n"
"v_mul_f32_dpp v2, v246, v2 row_newbcast:2 row_mask:0xf bank_mask:0xf\n"
"v_mfma_f32_16x16x32_bf16 v[36:39], v[104:107], a[252:255], v[36:39]\n"
"buffer_load_dwordx4 v240, s[16:19], s40 offen offset:3072 nt lds\n"
"v_mul_f32_dpp v47, v247, v47 row_newbcast:3 row_mask:0xf bank_mask:0xf\n"
"v_cvt_pk_bf16_f32 v41, v42, v43\n"
"v_mul_f32_dpp v31, v246, v31 row_newbcast:3 row_mask:0xf bank_mask:0xf\n"
"v_cvt_pk_bf16_f32 v147, v178, v179\n"
"v_exp_f32 v153, v153\n"
"v_exp_f32 v159, v159\n"
"v_exp_f32 v165, v165\n"
"v_mfma_f32_16x16x32_bf16 v[4:7], v[100:103], v[224:227], v[4:7]\n"
"v_cvt_pk_bf16_f32 v56, v56, v57\n"
"v_cvt_pk_bf16_f32 v138, v172, v173\n"
"v_mfma_f32_16x16x32_bf16 v[4:7], v[96:99], a[252:255], v[4:7]\n"
"ds_write_b128 v244, a[232:235] offset:34816\n"
"v_cvt_pk_bf16_f32 v9, v10, v11\n"
"v_mul_f32_dpp v63, v247, v63 row_newbcast:3 row_mask:0xf bank_mask:0xf\n"
"v_cvt_pk_bf16_f32 v130, v168, v169\n"
"v_cvt_pk_bf16_f32 v126, v152, v153\n"
"v_mul_f32_dpp v64, v206, v64 row_newbcast:0 row_mask:0xf bank_mask:0xf\n"
"v_mfma_f32_16x16x32_bf16 a[212:215], v[120:123], a[252:255], a[212:215]\n"
"s_waitcnt lgkmcnt(6)\n"
"v_cvt_pk_bf16_f32 v88, v88, v89\n"
"v_cvt_pk_bf16_f32 v146, v176, v177\n"
"v_mfma_f32_16x16x32_bf16 v[168:171], v[208:211], a[0:3], v[180:183]\n"
"s_waitcnt lgkmcnt(2)\n"
"ds_read_b64_tr_b16 a[246:247], v232 offset:54272\n"
"v_cvt_pk_bf16_f32 v43, v46, v47\n"
"v_mfma_f32_16x16x32_bf16 v[168:171], v[212:215], a[4:7], v[168:171]\n"
"s_waitcnt lgkmcnt(2)\n"
"ds_read_b64_tr_b16 v[224:225], v237 offset:50176\n"
"v_cvt_pk_bf16_f32 v57, v58, v59\n"
"v_mfma_f32_16x16x32_bf16 v[168:171], v[216:219], a[8:11], v[168:171]\n"
"v_exp_f32 v158, v158\n"
"v_mfma_f32_16x16x32_bf16 v[168:171], v[220:223], a[12:15], v[168:171]\n"
"ds_read_b64_tr_b16 a[250:251], v236 offset:54272\n"
"ds_read_b64_tr_b16 a[252:253], v233 offset:50176\n"
"v_cvt_pk_bf16_f32 v42, v44, v45\n"
"v_cvt_pk_bf16_f32 v72, v72, v73\n"
"v_cvt_pk_bf16_f32 v73, v74, v75\n"
"v_mul_f32_dpp v28, v246, v28 row_newbcast:0 row_mask:0xf bank_mask:0xf\n"
"v_mul_f32_dpp v91, v206, v91 row_newbcast:3 row_mask:0xf bank_mask:0xf\n"
"v_exp_f32 v167, v167\n"
"v_mul_f32_dpp v34, v247, v34 row_newbcast:2 row_mask:0xf bank_mask:0xf\n"
"v_mul_f32_dpp v35, v247, v35 row_newbcast:3 row_mask:0xf bank_mask:0xf\n"
"v_mul_f32_dpp v65, v206, v65 row_newbcast:1 row_mask:0xf bank_mask:0xf\n"
"v_mul_f32_dpp v68, v206, v68 row_newbcast:0 row_mask:0xf bank_mask:0xf\n"
"v_mfma_f32_16x16x32_bf16 v[172:175], v[216:219], a[24:27], v[184:187]\n"
"v_cvt_pk_bf16_f32 v10, v12, v13\n"
"v_xor_b32 v244, 0x1000, v244\n"
"v_mfma_f32_16x16x32_bf16 v[172:175], v[220:223], a[28:31], v[172:175]\n"
"ds_read_b64_tr_b16 v[226:227], v237 offset:54272\n"
"v_cvt_pk_bf16_f32 v127, v154, v155\n"
"v_mfma_f32_16x16x32_bf16 v[172:175], v[208:211], a[16:19], v[172:175]\n"
"v_cvt_pk_bf16_f32 v59, v62, v63\n"
"v_mul_f32_dpp v67, v206, v67 row_newbcast:3 row_mask:0xf bank_mask:0xf\n"
"v_mfma_f32_16x16x32_bf16 v[172:175], v[212:215], a[20:23], v[172:175]\n"
"s_cbranch_vccnz LPCT0_%=\nLPKT0_%=:\n"
"ds_write_b128 v248, v[40:43] offset:50176\n"
"v_cvt_pk_bf16_f32 v11, v14, v15\n"
"v_mul_f32_dpp v78, v206, v78 row_newbcast:2 row_mask:0xf bank_mask:0xf\n"
"v_mul_f32_dpp v93, v206, v93 row_newbcast:1 row_mask:0xf bank_mask:0xf\n"
"v_exp_f32 v164, v164\n"
"v_mul_f32_dpp v3, v246, v3 row_newbcast:3 row_mask:0xf bank_mask:0xf\n"
"v_mul_f32_dpp v36, v247, v36 row_newbcast:0 row_mask:0xf bank_mask:0xf\n"
"v_mul_f32_dpp v39, v247, v39 row_newbcast:3 row_mask:0xf bank_mask:0xf\n"
"v_mul_f32_dpp v70, v206, v70 row_newbcast:2 row_mask:0xf bank_mask:0xf\n"
"v_mov_b64 v[198:199], 0\n"
"v_mfma_f32_16x16x32_bf16 v[176:179], v[208:211], a[32:35], v[188:191]\n"
"ds_read_b64_tr_b16 a[68:69], v249 offset:50176\n"
"ds_read_b64_tr_b16 a[254:255], v233 offset:54272\n"
"v_mfma_f32_16x16x32_bf16 v[176:179], v[212:215], a[36:39], v[176:179]\n"
"ds_read_b64_tr_b16 a[70:71], v249 offset:50184\n"
"v_cvt_pk_bf16_f32 v74, v76, v77\n"
"v_mfma_f32_16x16x32_bf16 v[176:179], v[216:219], a[40:43], v[176:179]\n"
"v_mul_f32_dpp v69, v206, v69 row_newbcast:1 row_mask:0xf bank_mask:0xf\n"
"v_mul_f32_dpp v71, v206, v71 row_newbcast:3 row_mask:0xf bank_mask:0xf\n"
"v_mfma_f32_16x16x32_bf16 v[176:179], v[220:223], a[44:47], v[176:179]\n"
"s_waitcnt lgkmcnt(5)\n"
"ds_write_b128 v248, v[8:11] offset:49152\n"
"v_cvt_pk_bf16_f32 v75, v78, v79\n"
"v_cvt_pk_bf16_f32 v27, v30, v31\n"
"v_mul_f32_dpp v38, v247, v38 row_newbcast:2 row_mask:0xf bank_mask:0xf\n"
"v_cvt_pk_bf16_f32 v32, v32, v33\n"
"v_accvgpr_read_b32 v255, a209\n"
"v_mfma_f32_16x16x32_bf16 a[224:227], v[120:123], a[244:247], a[224:227]\n"
"ds_write_b128 v248, v[72:75] offset:51200\n"
"v_cvt_pk_bf16_f32 v89, v90, v91\n"
"v_mul_f32_dpp v5, v246, v5 row_newbcast:1 row_mask:0xf bank_mask:0xf\n"
"v_exp_f32 v170, v170\n"
"v_exp_f32 v175, v175\n"
"v_mfma_f32_16x16x32_bf16 v[16:19], v[96:99], a[244:247], v[16:19]\n"
"s_waitcnt vmcnt(8) lgkmcnt(3)\n"
"ds_read_b64_tr_b16 a[84:85], v249 offset:51200\n"
"v_mul_f32_dpp v37, v247, v37 row_newbcast:1 row_mask:0xf bank_mask:0xf\n"
"v_mfma_f32_16x16x32_bf16 v[16:19], v[100:103], a[248:251], v[16:19]\n"
"s_barrier\n"
"ds_read_b128 v[212:215], v229 offset:16384\n"
"ds_read_b64 v[250:251], v245 offset:52224\n"
"v_cvt_pk_bf16_f32 v26, v28, v29\n"
"v_cvt_pk_bf16_f32 v58, v60, v61\n"
"v_mul_f32_dpp v6, v246, v6 row_newbcast:2 row_mask:0xf bank_mask:0xf\n"
"v_mul_f32_dpp v7, v246, v7 row_newbcast:3 row_mask:0xf bank_mask:0xf\n"
"v_exp_f32 v172, v172\n"
"v_exp_f32 v177, v177\n"
"v_mfma_f32_16x16x32_bf16 v[48:51], v[104:107], a[244:247], v[48:51]\n"
"ds_read_b64_tr_b16 a[86:87], v249 offset:51208\n"
"ds_read_b128 v[208:211], v228 offset:16384\n"
"v_mfma_f32_16x16x32_bf16 v[48:51], v[108:111], a[248:251], v[48:51]\n"
"ds_read_b128 v[216:219], v228 offset:17408\n"
"v_cvt_pk_bf16_f32 v91, v94, v95\n"
"v_mfma_f32_16x16x32_bf16 v[80:83], v[112:115], a[244:247], v[80:83]\n"
"ds_read_b64_tr_b16 a[54:55], v249 offset:49160\n"
"v_cvt_pk_bf16_f32 v90, v92, v93\n"
"v_mfma_f32_16x16x32_bf16 v[80:83], v[116:119], a[248:251], v[80:83]\n"
"ds_read_b64_tr_b16 a[52:53], v249 offset:49152\n"
"ds_read_b128 v[28:31], v244 offset:33792\n"
"v_mfma_f32_16x16x32_bf16 v[84:87], v[116:119], v[224:227], v[84:87]\n"
"ds_read_b128 v[220:223], v229 offset:17408\n"
"v_cvt_pk_bf16_f32 v33, v34, v35\n"
"v_mfma_f32_16x16x32_bf16 v[84:87], v[112:115], a[252:255], v[84:87]\n"
"ds_write_b128 v248, v[56:59] offset:50176\n"
"v_cvt_pk_bf16_f32 v1, v2, v3\n"
"v_cvt_pk_bf16_f32 v64, v64, v65\n"
"v_exp_f32 v171, v171\n"
"v_exp_f32 v176, v176\n"
"v_mov_b64 v[202:203], 0\n"
"v_mfma_f32_16x16x32_bf16 v[52:55], v[108:111], v[224:227], v[52:55]\n"
"s_waitcnt lgkmcnt(8)\n"
"ds_write_b128 v248, v[24:27] offset:49152\n"
"v_cvt_pk_bf16_f32 v65, v66, v67\n"
"v_cvt_pk_bf16_f32 v66, v68, v69\n"
"v_cvt_pk_bf16_f32 v67, v70, v71\n"
"v_exp_f32 v169, v169\n"
"v_min_f32 v254, v252, v250\n"
"v_mfma_f32_16x16x32_bf16 v[52:55], v[104:107], a[252:255], v[52:55]\n"
"ds_write_b128 v248, v[88:91] offset:51200\n"
"v_cvt_pk_bf16_f32 v132, v156, v157\n"
"v_exp_f32 v162, v162\n"
"v_exp_f32 v166, v166\n"
"v_cvt_pk_bf16_f32 v3, v6, v7\n"
"v_exp_f32 v174, v174\n"
"v_mfma_f32_16x16x32_bf16 v[20:23], v[100:103], v[224:227], v[20:23]\n"
"ds_read_b64_tr_b16 a[76:77], v249 offset:50176\n"
"ds_read_b64_tr_b16 a[250:251], v238 offset:4096\n"
"v_mfma_f32_16x16x32_bf16 v[20:23], v[96:99], a[252:255], v[20:23]\n"
"ds_read_b128 v[24:27], v244 offset:32768\n"
"v_sub_f32 v252, v254, v252\n"
"v_mfma_f32_16x16x32_bf16 a[228:231], v[120:123], a[252:255], a[228:231]\n"
"s_waitcnt lgkmcnt(9)\n"
"ds_read_b64_tr_b16 a[62:63], v249 offset:49160\n"
"ds_read_b64_tr_b16 a[92:93], v249 offset:51200\n"
"ds_read_b64_tr_b16 a[94:95], v249 offset:51208\n"
"v_exp_f32 v163, v163\n"
"v_mul_f32_dpp v4, v246, v4 row_newbcast:0 row_mask:0xf bank_mask:0xf\n"
"v_cvt_pk_bf16_f32 v34, v36, v37\n"
"v_exp_f32 v178, v178\n"
"v_sub_f32 v250, v254, v250\n"
"v_exp_f32 v252, v252\n"
"v_mfma_f32_16x16x32_bf16 v[152:155], v[208:211], a[0:3], v[180:183]\n"
"ds_read_b64_tr_b16 a[78:79], v249 offset:50184\n"
"ds_read_b128 v[60:63], v244 offset:35840\n"
"v_mfma_f32_16x16x32_bf16 v[152:155], v[212:215], a[4:7], v[152:155]\n"
"ds_write_b128 v248, v[64:67] offset:51200\n"
"v_cvt_pk_bf16_f32 v133, v158, v159\n"
"v_cvt_pk_bf16_f32 v2, v4, v5\n"
"v_cvt_pk_bf16_f32 v35, v38, v39\n"
"v_exp_f32 v250, v250\n"
"v_mul_f32 v253, v253, v252\n"
"v_mfma_f32_16x16x32_bf16 v[152:155], v[216:219], a[8:11], v[152:155]\n"
"s_waitcnt lgkmcnt(11)\n"
"ds_read_b64_tr_b16 a[60:61], v249 offset:49152\n"
"ds_read_b64_tr_b16 a[82:83], v249 offset:51208\n"
"v_mfma_f32_16x16x32_bf16 v[152:155], v[220:223], a[12:15], v[152:155]\n"
"ds_write_b128 v248, v[0:3] offset:49152\n"
"ds_write_b128 v248, v[32:35] offset:50176\n"
"v_mul_f32_dpp v50, v247, v50 row_newbcast:2 row_mask:0xf bank_mask:0xf\n"
"v_mfma_f32_16x16x32_bf16 v[156:159], v[216:219], a[24:27], v[184:187]\n"
"ds_read_b64_tr_b16 a[66:67], v249 offset:50184\n"
"ds_read_b64_tr_b16 a[80:81], v249 offset:51200\n"
"v_mfma_f32_16x16x32_bf16 v[156:159], v[220:223], a[28:31], v[156:159]\n"
"ds_read_b64_tr_b16 a[48:49], v249 offset:49152\n"
"ds_read_b128 v[56:59], v244 offset:34816\n"
"v_mfma_f32_16x16x32_bf16 v[156:159], v[208:211], a[16:19], v[156:159]\n"
"ds_read_b64_tr_b16 v[224:225], v239 offset:0\n"
"v_cvt_pk_bf16_f32 v143, v174, v175\n"
"v_mfma_f32_16x16x32_bf16 v[156:159], v[212:215], a[20:23], v[156:159]\n"
"ds_read_b64_tr_b16 a[50:51], v249 offset:49160\n"
"ds_read_b64_tr_b16 a[64:65], v249 offset:50176\n"
"ds_read_b64_tr_b16 a[244:245], v234 offset:0\n"
"ds_read_b64_tr_b16 a[246:247], v234 offset:4096\n"
"v_cvt_pk_bf16_f32 v140, v160, v161\n"
"v_cvt_pk_bf16_f32 v141, v162, v163\n"
"v_cvt_pk_bf16_f32 v135, v170, v171\n"
"v_cvt_pk_bf16_f32 v150, v176, v177\n"
"v_fmac_f32 v253, v251, v250\n"
"v_xor_b32 v248, s75, v248\n"
"v_mfma_f32_16x16x32_bf16 v[160:163], v[208:211], a[32:35], v[188:191]\n"
"ds_read_b64_tr_b16 a[248:249], v238 offset:0\n"
"v_mul_f32_dpp v51, v247, v51 row_newbcast:3 row_mask:0xf bank_mask:0xf\n"
"v_mfma_f32_16x16x32_bf16 v[160:163], v[212:215], a[36:39], v[160:163]\n"
"v_cvt_pk_bf16_f32 v148, v164, v165\n"
"v_cvt_pk_bf16_f32 v149, v166, v167\n"
"v_mfma_f32_16x16x32_bf16 v[160:163], v[216:219], a[40:43], v[160:163]\n"
"v_rcp_f32 v253, v253\n"
"v_mfma_f32_16x16x32_bf16 v[160:163], v[220:223], a[44:47], v[160:163]\n"
"ds_read_b64_tr_b16 a[252:253], v235 offset:0\n"
"v_mul_f32 v250, v250, v253\n"
"v_mfma_f32_16x16x32_bf16 v[164:167], v[216:219], a[200:203], v[192:195]\n"
"ds_read_b64_tr_b16 v[226:227], v239 offset:4096\n"
"v_mul_f32 v252, v252, v253\n"
"v_mfma_f32_16x16x32_bf16 v[164:167], v[220:223], a[204:207], v[164:167]\n"
"v_exp_f32 v168, v168\n"
"v_mfma_f32_16x16x32_bf16 v[164:167], v[208:211], a[192:195], v[164:167]\n"
"ds_read_b128 v[216:219], v228 offset:21504\n"
"v_mov_b32_dpp v250, v250 row_ror:12 row_mask:0x2 bank_mask:0xf\n"
"v_mfma_f32_16x16x32_bf16 v[164:167], v[212:215], a[196:199], v[164:167]\n"
"s_waitcnt lgkmcnt(4)\n"
"ds_read_b64_tr_b16 a[254:255], v235 offset:4096\n"
"v_exp_f32 v173, v173\n"
"v_mov_b32_dpp v252, v252 row_ror:12 row_mask:0x2 bank_mask:0xf\n"
"v_accvgpr_read_b32 v254, a208\n"
"v_mul_f32_dpp v22, v246, v22 row_newbcast:2 row_mask:0xf bank_mask:0xf\n"
"v_mul_f32_dpp v80, v206, v80 row_newbcast:0 row_mask:0xf bank_mask:0xf\n"
"v_exp_f32 v156, v156\n"
"v_mfma_f32_16x16x32_bf16 a[216:219], v[124:127], a[244:247], 0\n"
"ds_read_b128 v[220:223], v229 offset:21504\n"
"v_cvt_pk_bf16_f32 v134, v168, v169\n"
"v_cvt_pk_bf16_f32 v142, v172, v173\n"
"v_mov_b32_dpp v250, v250 row_ror:8 row_mask:0x4 bank_mask:0xf\n"
"v_accvgpr_read_b32 v251, a211\n"
"v_mul_f32_dpp v84, v206, v84 row_newbcast:0 row_mask:0xf bank_mask:0xf\n"
"v_mov_b64 v[196:197], 0\n"
"v_exp_f32 v154, v154\n"
"v_exp_f32 v157, v157\n"
"v_mfma_f32_16x16x32_bf16 v[8:11], v[128:131], a[244:247], 0\n"
"s_waitcnt lgkmcnt(5)\n"
"s_add_u32 m0, s44, 98304\n"
"v_exp_f32 v179, v179\n"
"v_mfma_f32_16x16x32_bf16 v[8:11], v[132:135], a[248:251], v[8:11]\n"
"buffer_load_dwordx4 v240, s[20:23], s56 offen nt lds\n"
"v_mfma_f32_16x16x32_bf16 v[40:43], v[136:139], a[244:247], 0\n"
"v_exp_f32 v159, v159\n"
"v_mfma_f32_16x16x32_bf16 v[40:43], v[140:143], a[248:251], v[40:43]\n"
"v_cvt_pk_bf16_f32 v151, v178, v179\n"
"v_mov_b32_dpp v252, v252 row_ror:8 row_mask:0x4 bank_mask:0xf\n"
"v_mfma_f32_16x16x32_bf16 v[72:75], v[144:147], a[244:247], 0\n"
"v_mov_b32_dpp v250, v250 row_ror:4 row_mask:0x8 bank_mask:0xf\n"
"v_mul_f32_dpp v23, v246, v23 row_newbcast:3 row_mask:0xf bank_mask:0xf\n"
"v_mfma_f32_16x16x32_bf16 v[72:75], v[148:151], a[248:251], v[72:75]\n"
"s_waitcnt lgkmcnt(2)\n"
"ds_read_b128 v[212:215], v229 offset:20480\n"
"v_accvgpr_read_b32 v253, a210\n"
"v_mul_f32_dpp v27, v250, v27 row_newbcast:3 row_mask:0xf bank_mask:0xf\n"
"v_mul_f32_dpp v21, v246, v21 row_newbcast:1 row_mask:0xf bank_mask:0xf\n"
"v_mul_f32_dpp v86, v206, v86 row_newbcast:2 row_mask:0xf bank_mask:0xf\n"
"v_mul_f32_dpp v57, v250, v57 row_newbcast:1 row_mask:0xf bank_mask:0xf\n"
"v_exp_f32 v158, v158\n"
"v_exp_f32 v161, v161\n"
"v_mfma_f32_16x16x32_bf16 v[76:79], v[148:151], v[224:227], 0\n"
"s_waitcnt lgkmcnt(2)\n"
"ds_read_b128 v[208:211], v228 offset:20480\n"
"v_mov_b32_dpp v252, v252 row_ror:4 row_mask:0x8 bank_mask:0xf\n"
"v_mfma_f32_16x16x32_bf16 v[76:79], v[144:147], a[252:255], v[76:79]\n"
"ds_read_b64_tr_b16 a[248:249], v238 offset:1024\n"
"v_mul_f32_dpp v24, v250, v24 row_newbcast:0 row_mask:0xf bank_mask:0xf\n"
"v_mfma_f32_16x16x32_bf16 v[44:47], v[140:143], v[224:227], 0\n"
"v_mul_f32_dpp v26, v250, v26 row_newbcast:2 row_mask:0xf bank_mask:0xf\n"
"v_mul_f32_dpp v30, v250, v30 row_newbcast:2 row_mask:0xf bank_mask:0xf\n"
"v_mfma_f32_16x16x32_bf16 v[44:47], v[136:139], a[252:255], v[44:47]\n"
"ds_read_b64_tr_b16 a[244:245], v234 offset:1024\n"
"v_fmac_f32_dpp v26, v252, v253 row_newbcast:2 row_mask:0xf bank_mask:0xf\n"
"v_mfma_f32_16x16x32_bf16 v[12:15], v[132:135], v[224:227], 0\n"
"s_add_u32 s40, s56, s52\n"
"ds_read_b64_tr_b16 v[224:225], v239 offset:1024\n"
"v_mul_f32_dpp v25, v250, v25 row_newbcast:1 row_mask:0xf bank_mask:0xf\n"
"v_mfma_f32_16x16x32_bf16 v[12:15], v[128:131], a[252:255], v[12:15]\n"
"buffer_load_dwordx4 v240, s[20:23], s40 offen offset:1024 nt lds\n"
"v_fmac_f32_dpp v24, v252, v254 row_newbcast:0 row_mask:0xf bank_mask:0xf\n"
"v_fmac_f32_dpp v25, v252, v255 row_newbcast:1 row_mask:0xf bank_mask:0xf\n"
"v_accvgpr_read_b32 v254, a212\n"
"v_mul_f32_dpp v31, v250, v31 row_newbcast:3 row_mask:0xf bank_mask:0xf\n"
"v_mul_f32_dpp v62, v250, v62 row_newbcast:2 row_mask:0xf bank_mask:0xf\n"
"v_mul_f32_dpp v63, v250, v63 row_newbcast:3 row_mask:0xf bank_mask:0xf\n"
"v_exp_f32 v163, v163\n"
"v_mfma_f32_16x16x32_bf16 a[220:223], v[124:127], a[252:255], 0\n"
"v_mul_f32_dpp v28, v250, v28 row_newbcast:0 row_mask:0xf bank_mask:0xf\n"
"v_mul_f32_dpp v60, v250, v60 row_newbcast:0 row_mask:0xf bank_mask:0xf\n"
"v_smfmac_f32_16x16x64_bf16 v[196:199], a[240:243], v[128:135], v241\n"
"v_accvgpr_read_b32 v253, a214\n"
"v_mul_f32_dpp v53, v247, v53 row_newbcast:1 row_mask:0xf bank_mask:0xf\n"
"v_mfma_f32_16x16x32_bf16 v[202:205], a[240:243], v[124:127], v[202:205]\n"
"ds_read_b64_tr_b16 a[250:251], v238 offset:5120\n"
"v_fmac_f32_dpp v28, v252, v254 row_newbcast:0 row_mask:0xf bank_mask:0xf\n"
"v_mfma_f32_16x16x32_bf16 v[168:171], v[216:219], a[8:11], v[180:183]\n"
"s_waitcnt lgkmcnt(3)\n"
"v_exp_f32 v155, v155\n"
"v_mfma_f32_16x16x32_bf16 v[168:171], v[220:223], a[12:15], v[168:171]\n"
"ds_read_b64_tr_b16 a[246:247], v234 offset:5120\n"
"v_mul_f32_dpp v85, v206, v85 row_newbcast:1 row_mask:0xf bank_mask:0xf\n"
"v_mfma_f32_16x16x32_bf16 v[168:171], v[208:211], a[0:3], v[168:171]\n"
"v_mul_f32_dpp v29, v250, v29 row_newbcast:1 row_mask:0xf bank_mask:0xf\n"
"v_mul_f32_dpp v58, v250, v58 row_newbcast:2 row_mask:0xf bank_mask:0xf\n"
"v_mfma_f32_16x16x32_bf16 v[168:171], v[212:215], a[4:7], v[168:171]\n"
"v_fmac_f32_dpp v27, v252, v251 row_newbcast:3 row_mask:0xf bank_mask:0xf\n"
"v_accvgpr_read_b32 v255, a213\n"
"v_accvgpr_read_b32 v251, a215\n"
"v_fmac_f32_dpp v30, v252, v253 row_newbcast:2 row_mask:0xf bank_mask:0xf\n"
"v_cvt_pk_bf16_f32 v24, v24, v25\n"
"v_cvt_pk_bf16_f32 v25, v26, v27\n"
"v_accvgpr_read_b32 v254, a224\n"
"v_mul_f32_dpp v61, v250, v61 row_newbcast:1 row_mask:0xf bank_mask:0xf\n"
"v_exp_f32 v165, v165\n"
"v_mfma_f32_16x16x32_bf16 v[172:175], v[208:211], a[16:19], v[184:187]\n"
"ds_read_b64_tr_b16 v[226:227], v239 offset:5120\n"
"v_accvgpr_read_b32 v253, a226\n"
"v_mfma_f32_16x16x32_bf16 v[172:175], v[212:215], a[20:23], v[172:175]\n"
"v_fmac_f32_dpp v29, v252, v255 row_newbcast:1 row_mask:0xf bank_mask:0xf\n"
"v_mul_f32_dpp v56, v250, v56 row_newbcast:0 row_mask:0xf bank_mask:0xf\n"
"v_mfma_f32_16x16x32_bf16 v[172:175], v[216:219], a[24:27], v[172:175]\n"
"s_add_u32 s40, s56, s53\n"
"ds_read_b64_tr_b16 a[254:255], v235 offset:5120\n"
"v_mul_f32_dpp v59, v250, v59 row_newbcast:3 row_mask:0xf bank_mask:0xf\n"
"v_mfma_f32_16x16x32_bf16 v[172:175], v[220:223], a[28:31], v[172:175]\n"
"buffer_load_dwordx4 v240, s[20:23], s40 offen offset:2048 nt lds\n"
"v_fmac_f32_dpp v31, v252, v251 row_newbcast:3 row_mask:0xf bank_mask:0xf\n"
"v_mul_f32_dpp v16, v246, v16 row_newbcast:0 row_mask:0xf bank_mask:0xf\n"
"v_mul_f32_dpp v48, v247, v48 row_newbcast:0 row_mask:0xf bank_mask:0xf\n"
"v_mul_f32_dpp v49, v247, v49 row_newbcast:1 row_mask:0xf bank_mask:0xf\n"
"v_accvgpr_read_b32 v255, a225\n"
"v_accvgpr_read_b32 v251, a227\n"
"v_fmac_f32_dpp v56, v252, v254 row_newbcast:0 row_mask:0xf bank_mask:0xf\n"
"v_exp_f32 v167, v167\n"
"v_exp_f32 v171, v171\n"
"v_mfma_f32_16x16x32_bf16 v[176:179], v[216:219], a[40:43], v[188:191]\n"
"v_cvt_pk_bf16_f32 v26, v28, v29\n"
"v_mul_f32_dpp v81, v206, v81 row_newbcast:1 row_mask:0xf bank_mask:0xf\n"
"v_fmac_f32_dpp v57, v252, v255 row_newbcast:1 row_mask:0xf bank_mask:0xf\n"
"v_fmac_f32_dpp v58, v252, v253 row_newbcast:2 row_mask:0xf bank_mask:0xf\n"
"v_fmac_f32_dpp v59, v252, v251 row_newbcast:3 row_mask:0xf bank_mask:0xf\n"
"v_accvgpr_read_b32 v254, a228\n"
"v_accvgpr_read_b32 v255, a229\n"
"v_accvgpr_read_b32 v253, a230\n"
"v_accvgpr_read_b32 v251, a231\n"
"v_cvt_pk_bf16_f32 v56, v56, v57\n"
"v_exp_f32 v152, v152\n"
"v_exp_f32 v153, v153\n"
"v_cvt_pk_bf16_f32 v97, v154, v155\n"
"v_mfma_f32_16x16x32_bf16 v[176:179], v[220:223], a[44:47], v[176:179]\n"
"v_mul_f32_dpp v54, v247, v54 row_newbcast:2 row_mask:0xf bank_mask:0xf\n"
"v_fmac_f32_dpp v60, v252, v254 row_newbcast:0 row_mask:0xf bank_mask:0xf\n"
"v_mfma_f32_16x16x32_bf16 v[176:179], v[208:211], a[32:35], v[176:179]\n"
"v_cvt_pk_bf16_f32 v27, v30, v31\n"
"v_mul_f32_dpp v20, v246, v20 row_newbcast:0 row_mask:0xf bank_mask:0xf\n"
"v_mfma_f32_16x16x32_bf16 v[176:179], v[212:215], a[36:39], v[176:179]\n"
"ds_write_b128 v243, v[24:27] offset:0\n"
"v_mul_f32_dpp v87, v206, v87 row_newbcast:3 row_mask:0xf bank_mask:0xf\n"
"v_fmac_f32_dpp v61, v252, v255 row_newbcast:1 row_mask:0xf bank_mask:0xf\n"
"v_fmac_f32_dpp v62, v252, v253 row_newbcast:2 row_mask:0xf bank_mask:0xf\n"
"v_fmac_f32_dpp v63, v252, v251 row_newbcast:3 row_mask:0xf bank_mask:0xf\n"
"v_cvt_pk_bf16_f32 v57, v58, v59\n"
"v_cvt_pk_bf16_f32 v58, v60, v61\n"
"v_cvt_pk_bf16_f32 v59, v62, v63\n"
"v_cvt_pk_bf16_f32 v96, v152, v153\n"
"v_mfma_f32_16x16x32_bf16 v[152:155], v[208:211], a[192:195], v[192:195]\n"
"ds_write_b128 v243, v[56:59] offset:2048\n"
"v_cvt_pk_bf16_f32 v105, v158, v159\n"
"v_exp_f32 v162, v162\n"
"v_exp_f32 v172, v172\n"
"v_mfma_f32_16x16x32_bf16 v[152:155], v[212:215], a[196:199], v[152:155]\n"
"ds_read_b64_tr_b16 a[252:253], v235 offset:1024\n"
"v_mul_f32_dpp v18, v246, v18 row_newbcast:2 row_mask:0xf bank_mask:0xf\n"
"v_mfma_f32_16x16x32_bf16 v[152:155], v[216:219], a[200:203], v[152:155]\n"
"v_exp_f32 v174, v174\n"
"v_mfma_f32_16x16x32_bf16 v[152:155], v[220:223], a[204:207], v[152:155]\n"
"s_waitcnt lgkmcnt(3)\n"
"ds_read_b128 v[212:215], v231 offset:16384\n"
"v_mul_f32_dpp v55, v247, v55 row_newbcast:3 row_mask:0xf bank_mask:0xf\n"
"v_mfma_f32_16x16x32_bf16 a[232:235], v[124:127], a[244:247], 0\n"
"v_exp_f32 v169, v169\n"
"v_mfma_f32_16x16x32_bf16 v[24:27], v[128:131], a[244:247], 0\n"
"s_add_u32 s40, s56, s54\n"
"ds_read_b128 v[208:211], v230 offset:16384\n"
"v_mul_f32_dpp v17, v246, v17 row_newbcast:1 row_mask:0xf bank_mask:0xf\n"
"v_mfma_f32_16x16x32_bf16 v[24:27], v[132:135], a[248:251], v[24:27]\n"
"buffer_load_dwordx4 v240, s[20:23], s40 offen offset:3072 nt lds\n"
"v_mfma_f32_16x16x32_bf16 v[56:59], v[136:139], a[244:247], 0\n"
"s_add_u32 s55, s55, s59\n"
"v_exp_f32 v173, v173\n"
"v_mfma_f32_16x16x32_bf16 v[56:59], v[140:143], a[248:251], v[56:59]\n"
"s_add_u32 s41, s55, s57\n"
"v_exp_f32 v160, v160\n"
"v_mfma_f32_16x16x32_bf16 v[88:91], v[144:147], a[244:247], 0\n"
"s_and_b32 s41, s41, s58\n"
"v_exp_f32 v166, v166\n"
"v_mfma_f32_16x16x32_bf16 v[88:91], v[148:151], a[248:251], v[88:91]\n"
"s_andn2_b32 s56, s55, s58\n"
"ds_read_b64_tr_b16 a[248:249], v236 offset:0\n"
"v_mov_b64 v[200:201], 0\n"
"v_mfma_f32_16x16x32_bf16 v[92:95], v[148:151], v[224:227], 0\n"
"s_waitcnt lgkmcnt(2)\n"
"s_or_b32 s56, s56, s41\n"
"ds_read_b128 v[220:223], v231 offset:17408\n"
"v_cvt_pk_bf16_f32 v16, v16, v17\n"
"v_mfma_f32_16x16x32_bf16 v[92:95], v[144:147], a[252:255], v[92:95]\n"
"ds_read_b128 v[216:219], v230 offset:17408\n"
"v_exp_f32 v164, v164\n"
"v_exp_f32 v178, v178\n"
"v_exp_f32 v179, v179\n"
"v_mfma_f32_16x16x32_bf16 v[60:63], v[140:143], v[224:227], 0\n"
"s_add_u32 m0, s44, 49152\n"
"ds_read_b64_tr_b16 a[244:245], v232 offset:0\n"
"v_mul_f32_dpp v19, v246, v19 row_newbcast:3 row_mask:0xf bank_mask:0xf\n"
"v_mfma_f32_16x16x32_bf16 v[60:63], v[136:139], a[252:255], v[60:63]\n"
"buffer_load_dwordx4 v240, s[16:19], s56 offen nt lds\n"
"v_mfma_f32_16x16x32_bf16 v[28:31], v[132:135], v[224:227], 0\n"
"v_mul_f32_dpp v82, v206, v82 row_newbcast:2 row_mask:0xf bank_mask:0xf\n"
"v_cvt_pk_bf16_f32 v112, v160, v161\n"
"v_mfma_f32_16x16x32_bf16 v[28:31], v[128:131], a[252:255], v[28:31]\n"
"v_exp_f32 v154, v154\n"
"v_mfma_f32_16x16x32_bf16 a[236:239], v[124:127], a[252:255], 0\n"
"v_cvt_pk_bf16_f32 v17, v18, v19\n"
"v_cvt_pk_bf16_f32 v104, v156, v157\n"
"v_smfmac_f32_16x16x64_bf16 v[198:201], a[240:243], v[136:143], v241\n"
"s_waitcnt lgkmcnt(3)\n"
"ds_read_b64_tr_b16 v[226:227], v237 offset:4096\n"
"v_cvt_pk_bf16_f32 v48, v48, v49\n"
"v_mfma_f32_16x16x32_bf16 v[156:159], v[208:211], a[0:3], v[180:183]\n"
"v_mul_f32_dpp v52, v247, v52 row_newbcast:0 row_mask:0xf bank_mask:0xf\n"
"v_cvt_pk_bf16_f32 v120, v164, v165\n"
"v_mfma_f32_16x16x32_bf16 v[156:159], v[212:215], a[4:7], v[156:159]\n"
"s_waitcnt lgkmcnt(2)\n"
"ds_read_b64_tr_b16 v[224:225], v237 offset:0\n"
"v_cvt_pk_bf16_f32 v113, v162, v163\n"
"v_mfma_f32_16x16x32_bf16 v[156:159], v[216:219], a[8:11], v[156:159]\n"
"v_exp_f32 v176, v176\n"
"v_mfma_f32_16x16x32_bf16 v[156:159], v[220:223], a[12:15], v[156:159]\n"
"ds_read_b64_tr_b16 a[246:247], v232 offset:4096\n"
"ds_read_b64_tr_b16 a[254:255], v233 offset:4096\n"
"v_mfma_f32_16x16x32_bf16 v[160:163], v[216:219], a[24:27], v[184:187]\n"
"ds_read_b64_tr_b16 a[250:251], v236 offset:4096\n"
"v_cvt_pk_bf16_f32 v49, v50, v51\n"
"v_mfma_f32_16x16x32_bf16 v[160:163], v[220:223], a[28:31], v[160:163]\n"
"v_exp_f32 v168, v168\n"
"v_mfma_f32_16x16x32_bf16 v[160:163], v[208:211], a[16:19], v[160:163]\n"
"s_add_u32 s40, s56, s52\n"
"ds_read_b64_tr_b16 a[252:253], v233 offset:0\n"
"v_cvt_pk_bf16_f32 v121, v166, v167\n"
"v_mfma_f32_16x16x32_bf16 v[160:163], v[212:215], a[20:23], v[160:163]\n"
"buffer_load_dwordx4 v240, s[16:19], s40 offen offset:1024 nt lds\n"
"v_mfma_f32_16x16x32_bf16 v[164:167], v[208:211], a[32:35], v[188:191]\n"
"v_cvt_pk_bf16_f32 v51, v54, v55\n"
"v_add_u32 v243, 0x1000, v243\n"
"v_mfma_f32_16x16x32_bf16 v[164:167], v[212:215], a[36:39], v[164:167]\n"
"v_exp_f32 v177, v177\n"
"v_mfma_f32_16x16x32_bf16 v[164:167], v[216:219], a[40:43], v[164:167]\n"
"ds_read_b128 v[208:211], v230 offset:20480\n"
"v_cvt_pk_bf16_f32 v115, v178, v179\n"
"v_mfma_f32_16x16x32_bf16 v[164:167], v[220:223], a[44:47], v[164:167]\n"
"s_waitcnt lgkmcnt(3)\n"
"v_cvt_pk_bf16_f32 v50, v52, v53\n"
"v_mul_f32_dpp v83, v206, v83 row_newbcast:3 row_mask:0xf bank_mask:0xf\n"
"v_mfma_f32_16x16x32_bf16 a[208:211], v[124:127], a[244:247], 0\n"
"v_cvt_pk_bf16_f32 v19, v22, v23\n"
"v_cvt_pk_bf16_f32 v114, v176, v177\n"
"v_mfma_f32_16x16x32_bf16 v[0:3], v[128:131], a[244:247], 0\n"
"s_waitcnt lgkmcnt(2)\n"
"ds_read_b128 v[216:219], v230 offset:21504\n"
"v_xor_b32 v249, s75, v249\n"
"v_mfma_f32_16x16x32_bf16 v[0:3], v[132:135], a[248:251], v[0:3]\n"
"v_exp_f32 v175, v175\n"
"v_mfma_f32_16x16x32_bf16 v[32:35], v[136:139], a[244:247], 0\n"
"s_add_u32 s40, s56, s53\n"
"ds_read_b128 v[212:215], v231 offset:20480\n"
"v_cvt_pk_bf16_f32 v18, v20, v21\n"
"v_mfma_f32_16x16x32_bf16 v[32:35], v[140:143], a[248:251], v[32:35]\n"
"buffer_load_dwordx4 v240, s[16:19], s40 offen offset:2048 nt lds\n"
"v_mfma_f32_16x16x32_bf16 v[64:67], v[144:147], a[244:247], 0\n"
"v_cvt_pk_bf16_f32 v98, v168, v169\n"
"v_cvt_pk_bf16_f32 v107, v174, v175\n"
"v_mfma_f32_16x16x32_bf16 v[64:67], v[148:151], a[248:251], v[64:67]\n"
"v_exp_f32 v153, v153\n"
"v_mfma_f32_16x16x32_bf16 v[68:71], v[148:151], v[224:227], 0\n"
"s_waitcnt lgkmcnt(2)\n"
"ds_read_b128 v[220:223], v231 offset:21504\n"
"v_cvt_pk_bf16_f32 v106, v172, v173\n"
"v_mfma_f32_16x16x32_bf16 v[68:71], v[144:147], a[252:255], v[68:71]\n"
"ds_write_b128 v248, v[48:51] offset:50176\n"
"v_cvt_pk_bf16_f32 v80, v80, v81\n"
"v_cvt_pk_bf16_f32 v81, v82, v83\n"
"v_exp_f32 v170, v170\n"
"v_exp_f32 v159, v159\n"
"v_mfma_f32_16x16x32_bf16 v[36:39], v[140:143], v[224:227], 0\n"
"v_exp_f32 v167, v167\n"
"v_mfma_f32_16x16x32_bf16 v[36:39], v[136:139], a[252:255], v[36:39]\n"
"ds_read_b64_tr_b16 a[74:75], v249 offset:50184\n"
"ds_read_b64_tr_b16 a[244:245], v232 offset:1024\n"
"v_mfma_f32_16x16x32_bf16 v[4:7], v[132:135], v[224:227], 0\n"
"ds_read_b64_tr_b16 a[72:73], v249 offset:50176\n"
"v_cvt_pk_bf16_f32 v83, v86, v87\n"
"v_mfma_f32_16x16x32_bf16 v[4:7], v[128:131], a[252:255], v[4:7]\n"
"v_exp_f32 v166, v166\n"
"v_mfma_f32_16x16x32_bf16 a[212:215], v[124:127], a[252:255], 0\n"
"s_add_u32 s40, s56, s54\n"
"ds_write_b128 v248, v[16:19] offset:49152\n"
"v_cvt_pk_bf16_f32 v99, v170, v171\n"
"v_exp_f32 v161, v161\n"
"v_exp_f32 v164, v164\n"
"v_smfmac_f32_16x16x64_bf16 v[200:203], a[240:243], v[144:151], v241\n"
"buffer_load_dwordx4 v240, s[16:19], s40 offen offset:3072 nt lds\n"
"v_mfma_f32_16x16x32_bf16 v[168:171], v[208:211], a[0:3], v[180:183]\n"
"s_waitcnt lgkmcnt(5)\n"
"ds_read_b64_tr_b16 a[58:59], v249 offset:49160\n"
"ds_read_b64_tr_b16 a[246:247], v232 offset:5120\n"
"v_mfma_f32_16x16x32_bf16 v[168:171], v[212:215], a[4:7], v[168:171]\n"
"ds_read_b64_tr_b16 a[56:57], v249 offset:49152\n"
"v_cvt_pk_bf16_f32 v82, v84, v85\n"
"v_mfma_f32_16x16x32_bf16 v[168:171], v[216:219], a[8:11], v[168:171]\n"
"ds_write_b128 v248, v[80:83] offset:51200\n"
"v_exp_f32 v156, v156\n"
"v_exp_f32 v158, v158\n"
"v_exp_f32 v165, v165\n"
"v_mfma_f32_16x16x32_bf16 v[168:171], v[220:223], a[12:15], v[168:171]\n"
"ds_read_b64_tr_b16 a[88:89], v249 offset:51200\n"
"ds_read_b64_tr_b16 a[90:91], v249 offset:51208\n"
"v_mfma_f32_16x16x32_bf16 v[172:175], v[216:219], a[24:27], v[184:187]\n"
"ds_read_b64_tr_b16 a[248:249], v236 offset:1024\n"
"ds_read_b64_tr_b16 a[250:251], v236 offset:5120\n"
"v_mfma_f32_16x16x32_bf16 v[172:175], v[220:223], a[28:31], v[172:175]\n"
"v_xor_b32 v248, s75, v248\n"
"v_cvt_pk_bf16_f32 v117, v166, v167\n"
"v_mfma_f32_16x16x32_bf16 v[172:175], v[208:211], a[16:19], v[172:175]\n"
"ds_read_b64_tr_b16 v[226:227], v237 offset:5120\n"
"v_mfma_f32_16x16x32_bf16 v[172:175], v[212:215], a[20:23], v[172:175]\n"
"s_cbranch_vccnz LPCT1_%=\nLPKT1_%=:\n"
"ds_read_b64_tr_b16 a[254:255], v233 offset:5120\n"
"v_mfma_f32_16x16x32_bf16 v[176:179], v[208:211], a[32:35], v[188:191]\n"
"v_exp_f32 v163, v163\n"
"v_mfma_f32_16x16x32_bf16 v[176:179], v[212:215], a[36:39], v[176:179]\n"
"ds_read_b64_tr_b16 v[224:225], v237 offset:1024\n"
"v_cvt_pk_bf16_f32 v101, v158, v159\n"
"v_mfma_f32_16x16x32_bf16 v[176:179], v[216:219], a[40:43], v[176:179]\n"
"v_exp_f32 v175, v175\n"
"v_mfma_f32_16x16x32_bf16 v[176:179], v[220:223], a[44:47], v[176:179]\n"
"s_waitcnt lgkmcnt(5)\n"
"ds_read_b64_tr_b16 a[252:253], v233 offset:1024\n"
"v_mfma_f32_16x16x32_bf16 a[224:227], v[124:127], a[244:247], 0\n"
"v_exp_f32 v160, v160\n"
"v_mfma_f32_16x16x32_bf16 v[16:19], v[128:131], a[244:247], 0\n"
"s_waitcnt vmcnt(8) lgkmcnt(4)\n"
"v_exp_f32 v173, v173\n"
"v_mfma_f32_16x16x32_bf16 v[16:19], v[132:135], a[248:251], v[16:19]\n"
"s_barrier\n"
"ds_read_b128 v[216:219], v228 offset:33792\n"
"v_exp_f32 v155, v155\n"
"v_exp_f32 v157, v157\n"
"v_mfma_f32_16x16x32_bf16 v[48:51], v[136:139], a[244:247], 0\n"
"ds_read_b128 v[220:223], v229 offset:33792\n"
"v_cvt_pk_bf16_f32 v100, v156, v157\n"
"v_mfma_f32_16x16x32_bf16 v[48:51], v[140:143], a[248:251], v[48:51]\n"
"ds_read_b128 v[208:211], v228 offset:32768\n"
"ds_read_b128 v[212:215], v229 offset:32768\n"
"v_mfma_f32_16x16x32_bf16 v[80:83], v[144:147], a[244:247], 0\n"
"ds_read_b64_tr_b16 a[244:245], v234 offset:16384\n"
"ds_read_b64_tr_b16 a[246:247], v234 offset:20480\n"
"v_mfma_f32_16x16x32_bf16 v[80:83], v[148:151], a[248:251], v[80:83]\n"
"s_waitcnt lgkmcnt(7)\n"
"ds_read_b64_tr_b16 a[248:249], v238 offset:16384\n"
"ds_read_b64_tr_b16 a[250:251], v238 offset:20480\n"
"v_mfma_f32_16x16x32_bf16 v[84:87], v[148:151], v[224:227], 0\n"
"s_waitcnt lgkmcnt(8)\n"
"v_exp_f32 v162, v162\n"
"v_mfma_f32_16x16x32_bf16 v[84:87], v[144:147], a[252:255], v[84:87]\n"
"v_exp_f32 v152, v152\n"
"v_mfma_f32_16x16x32_bf16 v[52:55], v[140:143], v[224:227], 0\n"
"v_exp_f32 v178, v178\n"
"v_mfma_f32_16x16x32_bf16 v[52:55], v[136:139], a[252:255], v[52:55]\n"
"v_cvt_pk_bf16_f32 v122, v152, v153\n"
"v_cvt_pk_bf16_f32 v109, v162, v163\n"
"v_mfma_f32_16x16x32_bf16 v[20:23], v[132:135], v[224:227], 0\n"
"ds_read_b64_tr_b16 v[226:227], v239 offset:20480\n"
"v_cvt_pk_bf16_f32 v123, v154, v155\n"
"v_mfma_f32_16x16x32_bf16 v[20:23], v[128:131], a[252:255], v[20:23]\n"
"v_exp_f32 v169, v169\n"
"v_mfma_f32_16x16x32_bf16 a[228:231], v[124:127], a[252:255], 0\n"
"s_waitcnt lgkmcnt(3)\n"
"v_xor_b32 v249, s75, v249\n"
"s_branch LAY2_%=\n"
"LAX_%=:\n"
"v_mfma_f32_16x16x32_bf16 a[224:227], v[124:127], a[244:247], a[224:227]\n"
"v_exp_f32 v168, v168\n"
"v_mfma_f32_16x16x32_bf16 v[16:19], v[128:131], a[244:247], v[16:19]\n"
"s_waitcnt vmcnt(8) lgkmcnt(0)\n"
"v_exp_f32 v169, v169\n"
"v_mfma_f32_16x16x32_bf16 v[16:19], v[132:135], a[248:251], v[16:19]\n"
"s_barrier\n"
"v_mfma_f32_16x16x32_bf16 v[48:51], v[136:139], a[244:247], v[48:51]\n"
"v_exp_f32 v175, v175\n"
"v_mfma_f32_16x16x32_bf16 v[48:51], v[140:143], a[248:251], v[48:51]\n"
"v_exp_f32 v178, v178\n"
"v_mfma_f32_16x16x32_bf16 v[80:83], v[144:147], a[244:247], v[80:83]\n"
"ds_read_b64_tr_b16 a[246:247], v234 offset:53248\n"
"v_cvt_pk_bf16_f32 v109, v162, v163\n"
"v_mfma_f32_16x16x32_bf16 v[80:83], v[148:151], a[248:251], v[80:83]\n"
"ds_read_b64_tr_b16 a[244:245], v234 offset:49152\n"
"v_cvt_pk_bf16_f32 v123, v154, v155\n"
"v_cvt_pk_bf16_f32 v116, v164, v165\n"
"v_exp_f32 v174, v174\n"
"v_exp_f32 v176, v176\n"
"v_exp_f32 v177, v177\n"
"v_exp_f32 v179, v179\n"
"v_accvgpr_read_b32 v253, a96\n"
"v_mfma_f32_16x16x32_bf16 v[84:87], v[148:151], v[224:227], v[84:87]\n"
"ds_read_b64_tr_b16 a[248:249], v238 offset:49152\n"
"v_cvt_pk_bf16_f32 v111, v174, v175\n"
"v_mfma_f32_16x16x32_bf16 v[84:87], v[144:147], a[252:255], v[84:87]\n"
"ds_read_b64_tr_b16 a[250:251], v238 offset:53248\n"
"v_exp_f32 v156, v156\n"
"v_cvt_pk_bf16_f32 v101, v158, v159\n"
"v_cvt_pk_bf16_f32 v108, v160, v161\n"
"v_exp_f32 v170, v170\n"
"v_exp_f32 v171, v171\n"
"v_cvt_pk_bf16_f32 v118, v176, v177\n"
"v_and_b32 v245, 0x3f0, v242\n"
"v_mfma_f32_16x16x32_bf16 v[52:55], v[140:143], v[224:227], v[52:55]\n"
"v_exp_f32 v172, v172\n"
"v_mfma_f32_16x16x32_bf16 v[52:55], v[136:139], a[252:255], v[52:55]\n"
"v_cvt_pk_bf16_f32 v119, v178, v179\n"
"v_and_b32 v253, 0x7fff0000, v253\n"
"v_mfma_f32_16x16x32_bf16 v[20:23], v[132:135], v[224:227], v[20:23]\n"
"v_cvt_pk_bf16_f32 v102, v168, v169\n"
"v_cvt_pk_bf16_f32 v103, v170, v171\n"
"v_mfma_f32_16x16x32_bf16 v[20:23], v[128:131], a[252:255], v[20:23]\n"
"v_cvt_pk_bf16_f32 v100, v156, v157\n"
"v_lshlrev_b32_e64 v244, 24, s43\n"
"v_mfma_f32_16x16x32_bf16 a[228:231], v[124:127], a[252:255], a[228:231]\n"
"s_waitcnt lgkmcnt(2)\n"
"ds_read_b64_tr_b16 v[224:225], v239 offset:49152\n"
"v_cvt_pk_bf16_f32 v117, v166, v167\n"
"v_mfma_f32_16x16x32_bf16 a[216:219], v[120:123], a[244:247], a[216:219]\n"
"s_mov_b64 s[28:29], s[8:9]\n"
"v_exp_f32 v173, v173\n"
"v_smfmac_f32_16x16x64_bf16 v[196:199], a[240:243], v[96:103], v241\n"
"s_mov_b32 s30, 655360\n"
"ds_read_b64_tr_b16 a[254:255], v235 offset:53248\n"
"ds_read_b64_tr_b16 v[226:227], v239 offset:53248\n"
"v_mfma_f32_16x16x32_bf16 v[8:11], v[96:99], a[244:247], v[8:11]\n"
"s_waitcnt lgkmcnt(3)\n"
"s_mov_b32 s71, s44\n"
"ds_read_b64_tr_b16 a[252:253], v235 offset:49152\n"
"v_max_u32_dpp v253, v253, v253 quad_perm:[1,0,3,2] row_mask:0xf bank_mask:0xf\n"
"v_mfma_f32_16x16x32_bf16 v[8:11], v[100:103], a[248:251], v[8:11]\n"
"s_mov_b32 s70, s95\n"
"v_and_b32 v252, 0x7fff0000, v252\n"
"v_max_u32_dpp v253, v253, v253 quad_perm:[2,3,0,1] row_mask:0xf bank_mask:0xf\n"
"v_smfmac_f32_16x16x64_bf16 v[200:203], a[240:243], v[112:119], v241\n"
"v_cvt_pk_bf16_f32 v110, v172, v173\n"
"v_max_u32_dpp v252, v252, v252 quad_perm:[1,0,3,2] row_mask:0xf bank_mask:0xf\n"
"v_mfma_f32_16x16x32_bf16 v[40:43], v[104:107], a[244:247], v[40:43]\n"
"s_nop 0\n"
"v_max_u32_dpp v252, v252, v252 quad_perm:[2,3,0,1] row_mask:0xf bank_mask:0xf\n"
"v_max_u32_dpp v253, v253, v253 row_half_mirror row_mask:0xf bank_mask:0xf\n"
"v_mfma_f32_16x16x32_bf16 v[40:43], v[108:111], a[248:251], v[40:43]\n"
"s_add_u32 s41, s70, 327680\n"
"s_add_u32 m0, s71, 32768\n"
"v_add_f32 v196, v196, v197\n"
"v_smfmac_f32_16x16x64_bf16 v[198:201], a[240:243], v[104:111], v241\n"
"buffer_load_dwordx4 v240, s[28:31], s41 offen lds\n"
"v_mfma_f32_16x16x32_bf16 v[72:75], v[112:115], a[244:247], v[72:75]\n"
"v_min_u32_dpp v253, v253, v253 row_mirror row_mask:0xf bank_mask:0xf\n"
"v_lshrrev_b32 v245, 1, v245\n"
"v_mfma_f32_16x16x32_bf16 v[72:75], v[116:119], a[248:251], v[72:75]\n"
"v_max_u32_dpp v252, v252, v252 row_half_mirror row_mask:0xf bank_mask:0xf\n"
"v_min_u32_dpp v253, v253, v253 row_bcast:15 row_mask:0xa bank_mask:0xf\n"
"v_mfma_f32_16x16x32_bf16 v[202:205], a[240:243], v[120:123], v[202:205]\n"
"s_waitcnt lgkmcnt(0)\n"
"ds_read_b64_tr_b16 a[244:245], v234 offset:50176\n"
"ds_read_b64_tr_b16 a[248:249], v238 offset:50176\n"
"v_mfma_f32_16x16x32_bf16 v[76:79], v[116:119], v[224:227], v[76:79]\n"
"s_add_u32 s40, s41, s52\n"
"ds_read_b64_tr_b16 a[246:247], v234 offset:54272\n"
"ds_read_b64_tr_b16 a[250:251], v238 offset:54272\n"
"v_mfma_f32_16x16x32_bf16 v[76:79], v[112:115], a[252:255], v[76:79]\n"
"buffer_load_dwordx4 v240, s[28:31], s40 offen offset:1024 lds\n"
"v_mfma_f32_16x16x32_bf16 v[44:47], v[108:111], v[224:227], v[44:47]\n"
"v_min_u32_dpp v252, v252, v252 row_mirror row_mask:0xf bank_mask:0xf\n"
"v_mul_f32 v244, 0x45800000, v244\n"
"v_mfma_f32_16x16x32_bf16 v[44:47], v[104:107], a[252:255], v[44:47]\n"
"v_add_f32 v198, v198, v199\n"
"v_add_f32 v244, v244, v202\n"
"v_mfma_f32_16x16x32_bf16 v[12:15], v[100:103], v[224:227], v[12:15]\n"
"s_add_u32 s40, s41, s53\n"
"ds_read_b64_tr_b16 v[224:225], v239 offset:50176\n"
"ds_read_b64_tr_b16 v[226:227], v239 offset:54272\n"
"v_mfma_f32_16x16x32_bf16 v[12:15], v[96:99], a[252:255], v[12:15]\n"
"buffer_load_dwordx4 v240, s[28:31], s40 offen offset:2048 lds\n"
"v_min_u32_dpp v252, v252, v252 row_bcast:15 row_mask:0xa bank_mask:0xf\n"
"v_add_f32 v200, v200, v201\n"
"v_min3_f32 v244, v244, v196, v198\n"
"v_min_f32 v244, v244, v200\n"
"v_rcp_f32 v244, v244\n"
"v_sub_u32 v245, v242, v245\n"
"v_mfma_f32_16x16x32_bf16 a[220:223], v[120:123], a[252:255], a[220:223]\n"
"s_waitcnt lgkmcnt(3)\n"
"v_min_u32_dpp v253, v253, v253 row_bcast:31 row_mask:0xc bank_mask:0xf\n"
"v_add_f32 v254, v196, v198\n"
"v_add_f32 v254, v254, v200\n"
"v_add_f32 v254, v254, v202\n"
"v_fmac_f32 v254, 0x68800000, v244\n"
"v_cmp_nge_f32 vcc, s45, v254\n"
"s_cbranch_vccnz LSLOW_%=\nLBACK3_%=:\ns_mov_b64 vcc, s[64:65]\n"
"v_mfma_f32_16x16x32_bf16 a[232:235], v[120:123], a[244:247], a[232:235]\n"
"ds_read_b64_tr_b16 a[252:253], v235 offset:50176\n"
"ds_read_b64_tr_b16 a[254:255], v235 offset:54272\n"
"v_min_u32_dpp v252, v252, v252 row_bcast:31 row_mask:0xc bank_mask:0xf\n"
"v_rcp_f32 v246, v196\n"
"v_xor_b32 v244, 0x11000, v242\n"
"v_bfe_u32 v254, v242, 9, 1\n"
"v_mfma_f32_16x16x32_bf16 v[24:27], v[96:99], a[244:247], v[24:27]\n"
"s_waitcnt lgkmcnt(4)\n"
"v_rcp_f32 v247, v198\n"
"v_mfma_f32_16x16x32_bf16 v[24:27], v[100:103], a[248:251], v[24:27]\n"
"ds_write_b128 v244, a[216:219] offset:32768\n"
"v_rcp_f32 v206, v200\n"
"v_mov_b32_dpp v246, v246 row_ror:12 row_mask:0x2 bank_mask:0xf\n"
"v_mov_b32_dpp v247, v247 row_ror:12 row_mask:0x2 bank_mask:0xf\n"
"v_mfma_f32_16x16x32_bf16 v[56:59], v[104:107], a[244:247], v[56:59]\n"
"s_add_u32 s40, s41, s54\n"
"v_mov_b32_dpp v246, v246 row_ror:8 row_mask:0x4 bank_mask:0xf\n"
"v_lshl_add_u32 v254, v254, 4, s43\n"
"v_mfma_f32_16x16x32_bf16 v[56:59], v[108:111], a[248:251], v[56:59]\n"
"buffer_load_dwordx4 v240, s[28:31], s40 offen offset:3072 lds\n"
"v_mfma_f32_16x16x32_bf16 v[88:91], v[112:115], a[244:247], v[88:91]\n"
"s_add_u32 s71, s44, s75\n"
"v_mul_f32 v252, 0x43340000, v252\n"
"v_mov_b32_dpp v246, v246 row_ror:4 row_mask:0x8 bank_mask:0xf\n"
"v_mfma_f32_16x16x32_bf16 v[88:91], v[116:119], a[248:251], v[88:91]\n"
"s_waitcnt lgkmcnt(3)\n"
"s_mov_b32 s70, s100\n"
"ds_read_b64_tr_b16 a[244:245], v232 offset:49152\n"
"ds_read_b64_tr_b16 a[246:247], v232 offset:53248\n"
"ds_read_b64_tr_b16 a[248:249], v236 offset:49152\n"
"ds_read_b64_tr_b16 a[250:251], v236 offset:53248\n"
"v_mov_b32_dpp v247, v247 row_ror:8 row_mask:0x4 bank_mask:0xf\n"
"v_lshl_add_u32 v254, v254, 6, s75\n"
"v_mul_f32_dpp v9, v246, v9 row_newbcast:1 row_mask:0xf bank_mask:0xf\n"
"v_mul_f32_dpp v10, v246, v10 row_newbcast:2 row_mask:0xf bank_mask:0xf\n"
"v_mul_f32_dpp v11, v246, v11 row_newbcast:3 row_mask:0xf bank_mask:0xf\n"
"v_mul_f32_dpp v24, v246, v24 row_newbcast:0 row_mask:0xf bank_mask:0xf\n"
"v_mfma_f32_16x16x32_bf16 v[92:95], v[116:119], v[224:227], v[92:95]\n"
"s_waitcnt lgkmcnt(5)\n"
"s_add_u32 s41, s70, 163840\n"
"s_add_u32 m0, s71, 16384\n"
"v_mul_f32_dpp v14, v246, v14 row_newbcast:2 row_mask:0xf bank_mask:0xf\n"
"v_mfma_f32_16x16x32_bf16 v[92:95], v[112:115], a[252:255], v[92:95]\n"
"buffer_load_dwordx4 v240, s[28:31], s41 offen lds\n"
"v_mfma_f32_16x16x32_bf16 v[60:63], v[108:111], v[224:227], v[60:63]\n"
"v_mul_f32 v252, v252, v253\n"
"v_mov_b32_dpp v206, v206 row_ror:12 row_mask:0x2 bank_mask:0xf\n"
"v_mfma_f32_16x16x32_bf16 v[60:63], v[104:107], a[252:255], v[60:63]\n"
"ds_write_b128 v244, a[220:223] offset:33792\n"
"v_mov_b32_dpp v247, v247 row_ror:4 row_mask:0x8 bank_mask:0xf\n"
"v_sub_u32 v228, v232, v254\n"
"v_mul_f32_dpp v8, v246, v8 row_newbcast:0 row_mask:0xf bank_mask:0xf\n"
"v_mul_f32_dpp v25, v246, v25 row_newbcast:1 row_mask:0xf bank_mask:0xf\n"
"v_mul_f32_dpp v26, v246, v26 row_newbcast:2 row_mask:0xf bank_mask:0xf\n"
"v_mfma_f32_16x16x32_bf16 v[28:31], v[100:103], v[224:227], v[28:31]\n"
"s_add_u32 s40, s41, s52\n"
"v_min_f32 v252, 0x71800000, v252\n"
"v_mul_f32_dpp v12, v246, v12 row_newbcast:0 row_mask:0xf bank_mask:0xf\n"
"v_mfma_f32_16x16x32_bf16 v[28:31], v[96:99], a[252:255], v[28:31]\n"
"buffer_load_dwordx4 v240, s[28:31], s40 offen offset:1024 lds\n"
"v_mfma_f32_16x16x32_bf16 a[236:239], v[120:123], a[252:255], a[236:239]\n"
"s_waitcnt lgkmcnt(3)\n"
"ds_read_b64_tr_b16 a[252:253], v233 offset:49152\n"
"ds_read_b64_tr_b16 a[254:255], v233 offset:53248\n"
"ds_read_b64_tr_b16 v[224:225], v237 offset:49152\n"
"ds_read_b64_tr_b16 v[226:227], v237 offset:53248\n"
"v_readlane_b32 s23, v252, 63\n"
"v_mov_b32_dpp v206, v206 row_ror:8 row_mask:0x4 bank_mask:0xf\n"
"v_cvt_pk_bf16_f32 v8, v8, v9\n"
"v_mul_f32_dpp v47, v247, v47 row_newbcast:3 row_mask:0xf bank_mask:0xf\n"
"v_mul_f32_dpp v58, v247, v58 row_newbcast:2 row_mask:0xf bank_mask:0xf\n"
"v_mul_f32_dpp v60, v247, v60 row_newbcast:0 row_mask:0xf bank_mask:0xf\n"
"v_mfma_f32_16x16x32_bf16 a[208:211], v[120:123], a[244:247], a[208:211]\n"
"ds_write_b128 v244, a[232:235] offset:34816\n"
"v_readlane_b32 s22, v252, 63\n"
"v_mov_b32_dpp v206, v206 row_ror:4 row_mask:0x8 bank_mask:0xf\n"
"v_mul_f32_dpp v15, v246, v15 row_newbcast:3 row_mask:0xf bank_mask:0xf\n"
"v_cvt_pk_bf16_f32 v9, v10, v11\n"
"v_mul_f32_dpp v41, v247, v41 row_newbcast:1 row_mask:0xf bank_mask:0xf\n"
"v_mfma_f32_16x16x32_bf16 v[0:3], v[96:99], a[244:247], v[0:3]\n"
"s_waitcnt lgkmcnt(6)\n"
"v_sub_u32 v231, v235, v254\n"
"v_mov_b64 v[196:197], s[22:23]\n"
"v_mfma_f32_16x16x32_bf16 v[0:3], v[100:103], a[248:251], v[0:3]\n"
"ds_write_b128 v244, a[236:239] offset:35840\n"
"v_mul_f32_dpp v42, v247, v42 row_newbcast:2 row_mask:0xf bank_mask:0xf\n"
"v_mul_f32_dpp v75, v206, v75 row_newbcast:3 row_mask:0xf bank_mask:0xf\n"
"v_mul_f32_dpp v77, v206, v77 row_newbcast:1 row_mask:0xf bank_mask:0xf\n"
"v_cvt_pk_bf16_f32 v24, v24, v25\n"
"v_mul_f32_dpp v63, v247, v63 row_newbcast:3 row_mask:0xf bank_mask:0xf\n"
"v_mfma_f32_16x16x32_bf16 v[32:35], v[104:107], a[244:247], v[32:35]\n"
"s_add_u32 s40, s41, s53\n"
"v_xor_b32 v250, 0x1000, v245\n"
"v_mul_f32_dpp v94, v206, v94 row_newbcast:2 row_mask:0xf bank_mask:0xf\n"
"v_mfma_f32_16x16x32_bf16 v[32:35], v[108:111], a[248:251], v[32:35]\n"
"buffer_load_dwordx4 v240, s[28:31], s40 offen offset:2048 lds\n"
"v_mfma_f32_16x16x32_bf16 v[64:67], v[112:115], a[244:247], v[64:67]\n"
"v_mul_f32_dpp v13, v246, v13 row_newbcast:1 row_mask:0xf bank_mask:0xf\n"
"v_mul_f32_dpp v30, v246, v30 row_newbcast:2 row_mask:0xf bank_mask:0xf\n"
"v_mfma_f32_16x16x32_bf16 v[64:67], v[116:119], a[248:251], v[64:67]\n"
"s_waitcnt lgkmcnt(2)\n"
"ds_read_b64_tr_b16 a[246:247], v232 offset:54272\n"
"ds_read_b64_tr_b16 a[248:249], v236 offset:50176\n"
"v_mov_b32 v252, v192\n"
"v_cvt_pk_bf16_f32 v11, v14, v15\n"
"v_mul_f32_dpp v40, v247, v40 row_newbcast:0 row_mask:0xf bank_mask:0xf\n"
"v_mul_f32_dpp v43, v247, v43 row_newbcast:3 row_mask:0xf bank_mask:0xf\n"
"v_cvt_pk_bf16_f32 v40, v40, v41\n"
"v_mul_f32_dpp v78, v206, v78 row_newbcast:2 row_mask:0xf bank_mask:0xf\n"
"v_mul_f32_dpp v31, v246, v31 row_newbcast:3 row_mask:0xf bank_mask:0xf\n"
"v_mul_f32_dpp v92, v206, v92 row_newbcast:0 row_mask:0xf bank_mask:0xf\n"
"v_mfma_f32_16x16x32_bf16 v[68:71], v[116:119], v[224:227], v[68:71]\n"
"s_add_u32 s40, s41, s54\n"
"ds_read_b64_tr_b16 a[244:245], v232 offset:50176\n"
"ds_read_b64_tr_b16 a[250:251], v236 offset:54272\n"
"v_mfma_f32_16x16x32_bf16 v[68:71], v[112:115], a[252:255], v[68:71]\n"
"buffer_load_dwordx4 v240, s[28:31], s40 offen offset:3072 lds\n"
"v_mov_b32 v253, v202\n"
"v_mul_f32_dpp v44, v247, v44 row_newbcast:0 row_mask:0xf bank_mask:0xf\n"
"v_mul_f32_dpp v45, v247, v45 row_newbcast:1 row_mask:0xf bank_mask:0xf\n"
"v_mul_f32_dpp v46, v247, v46 row_newbcast:2 row_mask:0xf bank_mask:0xf\n"
"v_cvt_pk_bf16_f32 v41, v42, v43\n"
"v_mul_f32_dpp v72, v206, v72 row_newbcast:0 row_mask:0xf bank_mask:0xf\n"
"v_mul_f32_dpp v73, v206, v73 row_newbcast:1 row_mask:0xf bank_mask:0xf\n"
"v_mul_f32_dpp v2, v246, v2 row_newbcast:2 row_mask:0xf bank_mask:0xf\n"
"v_mfma_f32_16x16x32_bf16 v[36:39], v[108:111], v[224:227], v[36:39]\n"
"v_mul_f32_dpp v74, v206, v74 row_newbcast:2 row_mask:0xf bank_mask:0xf\n"
"v_add_u32 v236, s76, v249\n"
"v_mfma_f32_16x16x32_bf16 v[36:39], v[104:107], a[252:255], v[36:39]\n"
"ds_write_b64 v250, v[252:253] offset:52224\n"
"v_cvt_pk_bf16_f32 v10, v12, v13\n"
"v_mul_f32_dpp v76, v206, v76 row_newbcast:0 row_mask:0xf bank_mask:0xf\n"
"v_mul_f32_dpp v79, v206, v79 row_newbcast:3 row_mask:0xf bank_mask:0xf\n"
"v_mul_f32_dpp v93, v206, v93 row_newbcast:1 row_mask:0xf bank_mask:0xf\n"
"v_mul_f32_dpp v3, v246, v3 row_newbcast:3 row_mask:0xf bank_mask:0xf\n"
"v_mul_f32_dpp v33, v247, v33 row_newbcast:1 row_mask:0xf bank_mask:0xf\n"
"v_xor_b32 v232, 0x1000, v228\n"
"v_mfma_f32_16x16x32_bf16 v[4:7], v[100:103], v[224:227], v[4:7]\n"
"ds_read_b64_tr_b16 v[224:225], v237 offset:50176\n"
"ds_read_b64_tr_b16 v[226:227], v237 offset:54272\n"
"v_mfma_f32_16x16x32_bf16 v[4:7], v[96:99], a[252:255], v[4:7]\n"
"ds_write_b128 v248, v[8:11] offset:49152\n"
"v_cvt_pk_bf16_f32 v42, v44, v45\n"
"v_mul_f32_dpp v56, v247, v56 row_newbcast:0 row_mask:0xf bank_mask:0xf\n"
"v_mul_f32_dpp v61, v247, v61 row_newbcast:1 row_mask:0xf bank_mask:0xf\n"
"v_mul_f32_dpp v32, v247, v32 row_newbcast:0 row_mask:0xf bank_mask:0xf\n"
"v_mul_f32_dpp v35, v247, v35 row_newbcast:3 row_mask:0xf bank_mask:0xf\n"
"v_mfma_f32_16x16x32_bf16 a[212:215], v[120:123], a[252:255], a[212:215]\n"
"s_waitcnt lgkmcnt(5)\n"
"ds_read_b64_tr_b16 a[252:253], v233 offset:50176\n"
"ds_read_b64_tr_b16 a[254:255], v233 offset:54272\n"
"v_mfma_f32_16x16x32_bf16 a[224:227], v[120:123], a[244:247], a[224:227]\n"
"ds_read_b64_tr_b16 a[4:5], v249 offset:49152\n"
"v_mul_f32_dpp v62, v247, v62 row_newbcast:2 row_mask:0xf bank_mask:0xf\n"
"v_mfma_f32_16x16x32_bf16 v[16:19], v[96:99], a[244:247], v[16:19]\n"
"s_waitcnt vmcnt(8) lgkmcnt(1)\n"
"ds_read_b64_tr_b16 a[6:7], v249 offset:49160\n"
"v_cvt_pk_bf16_f32 v43, v46, v47\n"
"v_mfma_f32_16x16x32_bf16 v[16:19], v[100:103], a[248:251], v[16:19]\n"
"s_barrier\n"
"ds_read_b64_tr_b16 v[182:183], v228 offset:2048\n"
"ds_read_b64_tr_b16 v[186:187], v232 offset:2048\n"
"ds_read_b64_tr_b16 v[188:189], v228 offset:8192\n"
"ds_read_b64_tr_b16 v[208:209], v228 offset:16384\n"
"v_mul_f32_dpp v28, v246, v28 row_newbcast:0 row_mask:0xf bank_mask:0xf\n"
"v_mul_f32_dpp v57, v247, v57 row_newbcast:1 row_mask:0xf bank_mask:0xf\n"
"v_mul_f32_dpp v36, v247, v36 row_newbcast:0 row_mask:0xf bank_mask:0xf\n"
"v_mul_f32_dpp v38, v247, v38 row_newbcast:2 row_mask:0xf bank_mask:0xf\n"
"v_mfma_f32_16x16x32_bf16 v[48:51], v[104:107], a[244:247], v[48:51]\n"
"ds_read_b64_tr_b16 v[210:211], v228 offset:18432\n"
"v_mul_f32_dpp v90, v206, v90 row_newbcast:2 row_mask:0xf bank_mask:0xf\n"
"v_mfma_f32_16x16x32_bf16 v[48:51], v[108:111], a[248:251], v[48:51]\n"
"ds_read_b64_tr_b16 v[212:213], v232 offset:16384\n"
"ds_read_b64_tr_b16 v[218:219], v228 offset:26624\n"
"ds_read_b64_tr_b16 v[222:223], v232 offset:26624\n"
"ds_read_b64_tr_b16 a[202:203], v236 offset:2056\n"
"v_cvt_pk_bf16_f32 v72, v72, v73\n"
"v_cvt_pk_bf16_f32 v73, v74, v75\n"
"v_cvt_pk_bf16_f32 v56, v56, v57\n"
"v_mul_f32_dpp v0, v246, v0 row_newbcast:0 row_mask:0xf bank_mask:0xf\n"
"v_mul_f32_dpp v6, v246, v6 row_newbcast:2 row_mask:0xf bank_mask:0xf\n"
"v_mul_f32_dpp v64, v206, v64 row_newbcast:0 row_mask:0xf bank_mask:0xf\n"
"v_mfma_f32_16x16x32_bf16 v[80:83], v[112:115], a[244:247], v[80:83]\n"
"ds_write_b128 v248, v[40:43] offset:50176\n"
"v_cvt_pk_bf16_f32 v74, v76, v77\n"
"v_mul_f32_dpp v27, v246, v27 row_newbcast:3 row_mask:0xf bank_mask:0xf\n"
"v_mul_f32_dpp v59, v247, v59 row_newbcast:3 row_mask:0xf bank_mask:0xf\n"
"v_mul_f32_dpp v88, v206, v88 row_newbcast:0 row_mask:0xf bank_mask:0xf\n"
"v_mul_f32_dpp v66, v206, v66 row_newbcast:2 row_mask:0xf bank_mask:0xf\n"
"v_mfma_f32_16x16x32_bf16 v[80:83], v[116:119], a[248:251], v[80:83]\n"
"ds_read_b64_tr_b16 a[22:23], v249 offset:50184\n"
"ds_read_b64_tr_b16 v[180:181], v228 offset:0\n"
"ds_read_b64_tr_b16 v[184:185], v232 offset:0\n"
"ds_read_b64_tr_b16 v[192:193], v232 offset:8192\n"
"v_cvt_pk_bf16_f32 v75, v78, v79\n"
"v_mul_f32_dpp v29, v246, v29 row_newbcast:1 row_mask:0xf bank_mask:0xf\n"
"v_mul_f32_dpp v89, v206, v89 row_newbcast:1 row_mask:0xf bank_mask:0xf\n"
"v_mul_f32_dpp v34, v247, v34 row_newbcast:2 row_mask:0xf bank_mask:0xf\n"
"v_mul_f32_dpp v39, v247, v39 row_newbcast:3 row_mask:0xf bank_mask:0xf\n"
"v_mul_f32_dpp v68, v206, v68 row_newbcast:0 row_mask:0xf bank_mask:0xf\n"
"v_mfma_f32_16x16x32_bf16 v[84:87], v[116:119], v[224:227], v[84:87]\n"
"ds_write_b128 v248, v[72:75] offset:51200\n"
"v_cvt_pk_bf16_f32 v25, v26, v27\n"
"v_cvt_pk_bf16_f32 v26, v28, v29\n"
"v_cvt_pk_bf16_f32 v88, v88, v89\n"
"v_mul_f32_dpp v1, v246, v1 row_newbcast:1 row_mask:0xf bank_mask:0xf\n"
"v_mul_f32_dpp v65, v206, v65 row_newbcast:1 row_mask:0xf bank_mask:0xf\n"
"v_mfma_f32_16x16x32_bf16 v[84:87], v[112:115], a[252:255], v[84:87]\n"
"ds_read_b64_tr_b16 v[194:195], v232 offset:10240\n"
"ds_read_b64_tr_b16 v[214:215], v232 offset:18432\n"
"ds_read_b64_tr_b16 a[250:251], v236 offset:6152\n"
"v_cvt_pk_bf16_f32 v57, v58, v59\n"
"v_mul_f32_dpp v91, v206, v91 row_newbcast:3 row_mask:0xf bank_mask:0xf\n"
"v_mul_f32_dpp v95, v206, v95 row_newbcast:3 row_mask:0xf bank_mask:0xf\n"
"v_mul_f32_dpp v4, v246, v4 row_newbcast:0 row_mask:0xf bank_mask:0xf\n"
"v_mul_f32_dpp v5, v246, v5 row_newbcast:1 row_mask:0xf bank_mask:0xf\n"
"v_cvt_pk_bf16_f32 v0, v0, v1\n"
"v_cvt_pk_bf16_f32 v32, v32, v33\n"
"v_cvt_pk_bf16_f32 v64, v64, v65\n"
"v_mfma_f32_16x16x32_bf16 v[52:55], v[108:111], v[224:227], v[52:55]\n"
"ds_read_b64_tr_b16 v[190:191], v228 offset:10240\n"
"ds_read_b64_tr_b16 v[220:221], v232 offset:24576\n"
"v_mfma_f32_16x16x32_bf16 v[52:55], v[104:107], a[252:255], v[52:55]\n"
"ds_read_b64_tr_b16 a[20:21], v249 offset:50176\n"
"ds_read_b64_tr_b16 a[36:37], v249 offset:51200\n"
"ds_read_b64_tr_b16 a[38:39], v249 offset:51208\n"
"ds_read_b64_tr_b16 v[216:217], v228 offset:24576\n"
"v_cvt_pk_bf16_f32 v27, v30, v31\n"
"v_cvt_pk_bf16_f32 v58, v60, v61\n"
"v_mul_f32_dpp v7, v246, v7 row_newbcast:3 row_mask:0xf bank_mask:0xf\n"
"v_cvt_pk_bf16_f32 v1, v2, v3\n"
"v_mul_f32_dpp v69, v206, v69 row_newbcast:1 row_mask:0xf bank_mask:0xf\n"
"v_xor_b32 v244, 0x1000, v244\n"
"v_mfma_f32_16x16x32_bf16 v[20:23], v[100:103], v[224:227], v[20:23]\n"
"ds_write_b128 v248, v[24:27] offset:49152\n"
"v_sub_u32 v229, v233, v254\n"
"v_cvt_pk_bf16_f32 v59, v62, v63\n"
"v_cvt_pk_bf16_f32 v89, v90, v91\n"
"v_cvt_pk_bf16_f32 v91, v94, v95\n"
"v_cvt_pk_bf16_f32 v2, v4, v5\n"
"v_mfma_f32_16x16x32_bf16 v[20:23], v[96:99], a[252:255], v[20:23]\n"
"ds_write_b128 v248, v[56:59] offset:50176\n"
"v_cvt_pk_bf16_f32 v90, v92, v93\n"
"v_cvt_pk_bf16_f32 v33, v34, v35\n"
"v_mul_f32_dpp v67, v206, v67 row_newbcast:3 row_mask:0xf bank_mask:0xf\n"
"v_mul_f32_dpp v71, v206, v71 row_newbcast:3 row_mask:0xf bank_mask:0xf\n"
"v_mov_b64 v[198:199], s[22:23]\n"
"v_mfma_f32_16x16x32_bf16 a[228:231], v[120:123], a[252:255], a[228:231]\n"
"s_waitcnt lgkmcnt(14)\n"
"ds_write_b128 v248, v[88:91] offset:51200\n"
"v_cvt_pk_bf16_f32 v3, v6, v7\n"
"v_mul_f32_dpp v37, v247, v37 row_newbcast:1 row_mask:0xf bank_mask:0xf\n"
"v_cvt_pk_bf16_f32 v34, v36, v37\n"
"v_mul_f32_dpp v70, v206, v70 row_newbcast:2 row_mask:0xf bank_mask:0xf\n"
"v_xor_b32 v237, 0x400, v236\n"
"v_mfma_f32_16x16x32_bf16 v[124:127], v[180:183], a[96:99], v[196:199]\n"
"s_waitcnt lgkmcnt(13)\n"
"ds_read_b64_tr_b16 a[30:31], v249 offset:50184\n"
"ds_read_b64_tr_b16 a[44:45], v249 offset:51200\n"
"v_mfma_f32_16x16x32_bf16 v[124:127], v[184:187], a[100:103], v[124:127]\n"
"s_waitcnt lgkmcnt(10)\n"
"ds_read_b64_tr_b16 a[28:29], v249 offset:50176\n"
"ds_read_b64_tr_b16 a[244:245], v237 offset:4096\n"
"v_mfma_f32_16x16x32_bf16 v[124:127], v[188:191], a[104:107], v[124:127]\n"
"ds_read_b64_tr_b16 a[12:13], v249 offset:49152\n"
"ds_read_b64_tr_b16 a[14:15], v249 offset:49160\n"
"v_mfma_f32_16x16x32_bf16 v[124:127], v[192:195], a[108:111], v[124:127]\n"
"ds_write_b128 v248, v[0:3] offset:49152\n"
"v_cvt_pk_bf16_f32 v35, v38, v39\n"
"v_cvt_pk_bf16_f32 v65, v66, v67\n"
"v_cvt_pk_bf16_f32 v66, v68, v69\n"
"v_cvt_pk_bf16_f32 v67, v70, v71\n"
"v_mfma_f32_16x16x32_bf16 v[124:127], v[208:211], a[144:147], v[124:127]\n"
"ds_read_b64_tr_b16 a[46:47], v249 offset:51208\n"
"ds_read_b128 v[60:63], v244 offset:35840\n"
"v_mfma_f32_16x16x32_bf16 v[124:127], v[212:215], a[148:151], v[124:127]\n"
"s_waitcnt lgkmcnt(11)\n"
"ds_write_b128 v248, v[32:35] offset:50176\n"
"ds_write_b128 v248, v[64:67] offset:51200\n"
"v_mfma_f32_16x16x32_bf16 v[124:127], v[216:219], a[152:155], v[124:127]\n"
"ds_read_b64_tr_b16 a[2:3], v249 offset:49160\n"
"ds_read_b128 v[28:31], v244 offset:33792\n"
"v_mfma_f32_16x16x32_bf16 v[124:127], v[220:223], a[156:159], v[124:127]\n"
"ds_read_b64_tr_b16 a[16:17], v249 offset:50176\n"
"ds_read_b64_tr_b16 a[34:35], v249 offset:51208\n"
"ds_read_b64 v[250:251], v245 offset:52224\n"
"ds_read_b128 v[56:59], v244 offset:34816\n"
"ds_read_b64_tr_b16 a[204:205], v237 offset:2048\n"
"v_sub_u32 v230, v234, v254\n"
"v_xor_b32 v233, 0x1000, v229\n"
"v_mul_f32_dpp v22, v246, v22 row_newbcast:2 row_mask:0xf bank_mask:0xf\n"
"v_mfma_f32_16x16x32_bf16 v[140:143], v[220:223], a[172:175], v[196:199]\n"
"ds_read_b64_tr_b16 a[0:1], v249 offset:49152\n"
"ds_read_b64_tr_b16 a[246:247], v237 offset:4104\n"
"v_mfma_f32_16x16x32_bf16 v[140:143], v[216:219], a[168:171], v[140:143]\n"
"ds_read_b64_tr_b16 a[32:33], v249 offset:51200\n"
"ds_read_b128 v[24:27], v244 offset:32768\n"
"v_mfma_f32_16x16x32_bf16 v[140:143], v[212:215], a[164:167], v[140:143]\n"
"ds_read_b64_tr_b16 a[18:19], v249 offset:50184\n"
"ds_read_b64_tr_b16 a[248:249], v236 offset:6144\n"
"v_mfma_f32_16x16x32_bf16 v[140:143], v[208:211], a[160:163], v[140:143]\n"
"ds_read_b64_tr_b16 v[110:111], v233 offset:10240\n"
"ds_read_b64_tr_b16 a[254:255], v237 offset:6152\n"
"v_mfma_f32_16x16x32_bf16 v[140:143], v[192:195], a[124:127], v[140:143]\n"
"ds_read_b64_tr_b16 v[108:109], v233 offset:8192\n"
"ds_read_b64_tr_b16 a[252:253], v237 offset:6144\n"
"v_mfma_f32_16x16x32_bf16 v[140:143], v[188:191], a[120:123], v[140:143]\n"
"ds_read_b64_tr_b16 v[226:227], v233 offset:26624\n"
"ds_read_b64_tr_b16 a[242:243], v236 offset:4104\n"
"v_mfma_f32_16x16x32_bf16 v[140:143], v[184:187], a[116:119], v[140:143]\n"
"ds_read_b64_tr_b16 a[206:207], v237 offset:2056\n"
"ds_read_b64_tr_b16 v[98:99], v229 offset:2048\n"
"v_mfma_f32_16x16x32_bf16 v[140:143], v[180:183], a[112:115], v[140:143]\n"
"ds_read_b64_tr_b16 a[194:195], v236 offset:8\n"
"ds_read_b64_tr_b16 a[196:197], v237 offset:0\n"
"v_mfma_f32_16x16x32_bf16 v[156:159], v[180:183], a[128:131], v[196:199]\n"
"ds_read_b64_tr_b16 v[102:103], v233 offset:2048\n"
"ds_read_b64_tr_b16 v[106:107], v229 offset:10240\n"
"v_mfma_f32_16x16x32_bf16 v[156:159], v[184:187], a[132:135], v[156:159]\n"
"ds_read_b64_tr_b16 a[200:201], v236 offset:2048\n"
"ds_read_b64_tr_b16 v[224:225], v233 offset:24576\n"
"v_mfma_f32_16x16x32_bf16 v[156:159], v[188:191], a[136:139], v[156:159]\n"
"ds_read_b64_tr_b16 v[120:121], v229 offset:24576\n"
"ds_read_b64_tr_b16 a[240:241], v236 offset:4096\n"
"v_mfma_f32_16x16x32_bf16 v[156:159], v[192:195], a[140:143], v[156:159]\n"
"s_waitcnt lgkmcnt(14)\n"
"ds_read_b64_tr_b16 v[96:97], v229 offset:0\n"
"v_xor_b32 v234, 0x1000, v230\n"
"v_mfma_f32_16x16x32_bf16 v[156:159], v[208:211], a[176:179], v[156:159]\n"
"ds_read_b64_tr_b16 a[198:199], v237 offset:8\n"
"ds_read_b64_tr_b16 v[114:115], v229 offset:18432\n"
"v_mfma_f32_16x16x32_bf16 v[156:159], v[212:215], a[180:183], v[156:159]\n"
"s_add_u32 s41, s95, 491520\n"
"ds_read_b64_tr_b16 a[192:193], v236 offset:0\n"
"v_mul_f32_dpp v54, v247, v54 row_newbcast:2 row_mask:0xf bank_mask:0xf\n"
"v_mfma_f32_16x16x32_bf16 v[156:159], v[216:219], a[184:187], v[156:159]\n"
"s_add_u32 m0, s44, 49152\n"
"ds_read_b64_tr_b16 v[100:101], v233 offset:0\n"
"ds_read_b64_tr_b16 v[118:119], v233 offset:18432\n"
"v_mfma_f32_16x16x32_bf16 v[156:159], v[220:223], a[188:191], v[156:159]\n"
"s_waitcnt lgkmcnt(12)\n"
"buffer_load_dwordx4 v240, s[28:31], s41 offen lds\n"
"v_mfma_f32_16x16x32_bf16 v[172:175], v[220:223], a[252:255], v[196:199]\n"
"s_add_u32 s40, s41, s52\n"
"ds_read_b64_tr_b16 v[104:105], v229 offset:8192\n"
"ds_read_b64_tr_b16 v[116:117], v233 offset:16384\n"
"v_mfma_f32_16x16x32_bf16 v[172:175], v[216:219], a[248:251], v[172:175]\n"
"buffer_load_dwordx4 v240, s[28:31], s40 offen offset:1024 lds\n"
"v_mfma_f32_16x16x32_bf16 v[172:175], v[212:215], a[244:247], v[172:175]\n"
"s_waitcnt lgkmcnt(8)\n"
"s_add_u32 s40, s41, s53\n"
"ds_read_b64_tr_b16 v[112:113], v229 offset:16384\n"
"ds_read_b64_tr_b16 v[122:123], v229 offset:26624\n"
"v_mfma_f32_16x16x32_bf16 v[172:175], v[208:211], a[240:243], v[172:175]\n"
"buffer_load_dwordx4 v240, s[28:31], s40 offen offset:2048 lds\n"
"v_mfma_f32_16x16x32_bf16 v[172:175], v[192:195], a[204:207], v[172:175]\n"
"v_min_f32 v254, v252, v250\n"
"v_sub_f32 v250, v254, v250\n"
"v_mfma_f32_16x16x32_bf16 v[172:175], v[188:191], a[200:203], v[172:175]\n"
"s_waitcnt lgkmcnt(6)\n"
"v_sub_f32 v252, v254, v252\n"
"v_mul_f32_dpp v86, v206, v86 row_newbcast:2 row_mask:0xf bank_mask:0xf\n"
"v_mfma_f32_16x16x32_bf16 v[172:175], v[184:187], a[196:199], v[172:175]\n"
"s_add_u32 s40, s41, s54\n"
"ds_read_b64_tr_b16 v[192:193], v234 offset:8192\n"
"v_mul_f32_dpp v49, v247, v49 row_newbcast:1 row_mask:0xf bank_mask:0xf\n"
"v_mfma_f32_16x16x32_bf16 v[172:175], v[180:183], a[192:195], v[172:175]\n"
"buffer_load_dwordx4 v240, s[28:31], s40 offen offset:3072 lds\n"
"v_mfma_f32_16x16x32_bf16 v[128:131], v[96:99], a[96:99], v[196:199]\n"
"s_waitcnt lgkmcnt(3)\n"
"v_xor_b32 v248, s75, v248\n"
"v_mul_f32_dpp v23, v246, v23 row_newbcast:3 row_mask:0xf bank_mask:0xf\n"
"v_mfma_f32_16x16x32_bf16 v[128:131], v[100:103], a[100:103], v[128:131]\n"
"v_exp_f32 v252, v252\n"
"v_mfma_f32_16x16x32_bf16 v[128:131], v[104:107], a[104:107], v[128:131]\n"
"ds_read_b64_tr_b16 v[208:209], v230 offset:16384\n"
"v_mul_f32 v253, v253, v252\n"
"v_mfma_f32_16x16x32_bf16 v[128:131], v[108:111], a[108:111], v[128:131]\n"
"s_waitcnt lgkmcnt(2)\n"
"ds_read_b64_tr_b16 a[238:239], v237 offset:10248\n"
"ds_read_b64_tr_b16 v[210:211], v230 offset:18432\n"
"v_mfma_f32_16x16x32_bf16 v[128:131], v[112:115], a[144:147], v[128:131]\n"
"s_add_u32 s21, s60, 32\n"
"v_exp_f32 v250, v250\n"
"v_mfma_f32_16x16x32_bf16 v[128:131], v[116:119], a[148:151], v[128:131]\n"
"s_mov_b32 s69, 1\n"
"ds_read_b64_tr_b16 v[180:181], v230 offset:0\n"
"v_mul_f32_dpp v16, v246, v16 row_newbcast:0 row_mask:0xf bank_mask:0xf\n"
"v_mfma_f32_16x16x32_bf16 v[128:131], v[120:123], a[152:155], v[128:131]\n"
"v_bfe_u32 v201, v242, 2, 7\n"
"v_add_u32 v201, 0x100, v201\n"
"ds_read_b64_tr_b16 v[220:221], v234 offset:24576\n"
"v_mfma_f32_16x16x32_bf16 v[128:131], v[224:227], a[156:159], v[128:131]\n"
"global_load_dword v200, v201, s[6:7] sc1\n"
"v_mfma_f32_16x16x32_bf16 v[144:147], v[224:227], a[172:175], v[196:199]\n"
"v_accvgpr_read_b32 v254, a208\n"
"v_accvgpr_read_b32 v255, a209\n"
"v_mfma_f32_16x16x32_bf16 v[144:147], v[120:123], a[168:171], v[144:147]\n"
"v_mul_f32_dpp v48, v247, v48 row_newbcast:0 row_mask:0xf bank_mask:0xf\n"
"v_mul_f32_dpp v80, v206, v80 row_newbcast:0 row_mask:0xf bank_mask:0xf\n"
"v_mfma_f32_16x16x32_bf16 v[144:147], v[116:119], a[164:167], v[144:147]\n"
"ds_read_b64_tr_b16 v[188:189], v230 offset:8192\n"
"v_fmac_f32 v253, v251, v250\n"
"v_mfma_f32_16x16x32_bf16 v[144:147], v[112:115], a[160:163], v[144:147]\n"
"v_mul_f32_dpp v19, v246, v19 row_newbcast:3 row_mask:0xf bank_mask:0xf\n"
"v_mul_f32_dpp v21, v246, v21 row_newbcast:1 row_mask:0xf bank_mask:0xf\n"
"v_mfma_f32_16x16x32_bf16 v[144:147], v[108:111], a[124:127], v[144:147]\n"
"v_rcp_f32 v253, v253\n"
"v_mfma_f32_16x16x32_bf16 v[144:147], v[104:107], a[120:123], v[144:147]\n"
"ds_read_b64_tr_b16 v[218:219], v230 offset:26624\n"
"v_mul_f32 v250, v250, v253\n"
"v_mfma_f32_16x16x32_bf16 v[144:147], v[100:103], a[116:119], v[144:147]\n"
"s_nop 0\n"
"v_mov_b32_dpp v250, v250 row_ror:12 row_mask:0x2 bank_mask:0xf\n"
"v_mul_f32_dpp v52, v247, v52 row_newbcast:0 row_mask:0xf bank_mask:0xf\n"
"v_mfma_f32_16x16x32_bf16 v[144:147], v[96:99], a[112:115], v[144:147]\n"
"ds_read_b64_tr_b16 a[216:217], v236 offset:8192\n"
"ds_read_b64_tr_b16 v[182:183], v230 offset:2048\n"
"v_mul_f32 v252, v252, v253\n"
"v_mov_b32_dpp v250, v250 row_ror:8 row_mask:0x4 bank_mask:0xf\n"
"v_accvgpr_read_b32 v251, a211\n"
"v_mul_f32_dpp v17, v246, v17 row_newbcast:1 row_mask:0xf bank_mask:0xf\n"
"v_mul_f32_dpp v55, v247, v55 row_newbcast:3 row_mask:0xf bank_mask:0xf\n"
"v_mfma_f32_16x16x32_bf16 v[160:163], v[96:99], a[128:131], v[196:199]\n"
"v_mov_b32_dpp v250, v250 row_ror:4 row_mask:0x8 bank_mask:0xf\n"
"v_mul_f32_dpp v81, v206, v81 row_newbcast:1 row_mask:0xf bank_mask:0xf\n"
"v_mfma_f32_16x16x32_bf16 v[160:163], v[100:103], a[132:135], v[160:163]\n"
"v_xor_b32 v235, 0x1000, v231\n"
"v_mul_f32_dpp v57, v250, v57 row_newbcast:1 row_mask:0xf bank_mask:0xf\n"
"v_mfma_f32_16x16x32_bf16 v[160:163], v[104:107], a[136:139], v[160:163]\n"
"v_mul_f32_dpp v31, v250, v31 row_newbcast:3 row_mask:0xf bank_mask:0xf\n"
"v_mul_f32_dpp v58, v250, v58 row_newbcast:2 row_mask:0xf bank_mask:0xf\n"
"v_mfma_f32_16x16x32_bf16 v[160:163], v[108:111], a[140:143], v[160:163]\n"
"ds_read_b64_tr_b16 v[194:195], v234 offset:10240\n"
"v_mov_b32_dpp v252, v252 row_ror:12 row_mask:0x2 bank_mask:0xf\n"
"v_mfma_f32_16x16x32_bf16 v[160:163], v[112:115], a[176:179], v[160:163]\n"
"s_nop 0\n"
"v_mov_b32_dpp v252, v252 row_ror:8 row_mask:0x4 bank_mask:0xf\n"
"v_mul_f32_dpp v18, v246, v18 row_newbcast:2 row_mask:0xf bank_mask:0xf\n"
"v_mfma_f32_16x16x32_bf16 v[160:163], v[116:119], a[180:183], v[160:163]\n"
"ds_read_b64_tr_b16 a[218:219], v236 offset:8200\n"
"ds_read_b64_tr_b16 v[184:185], v234 offset:0\n"
"v_mfma_f32_16x16x32_bf16 v[160:163], v[120:123], a[184:187], v[160:163]\n"
"v_mul_f32_dpp v27, v250, v27 row_newbcast:3 row_mask:0xf bank_mask:0xf\n"
"v_cvt_pk_bf16_f32 v16, v16, v17\n"
"v_mfma_f32_16x16x32_bf16 v[160:163], v[224:227], a[188:191], v[160:163]\n"
"ds_read_b64_tr_b16 v[186:187], v234 offset:2048\n"
"ds_read_b64_tr_b16 v[190:191], v230 offset:10240\n"
"v_mov_b32_dpp v252, v252 row_ror:4 row_mask:0x8 bank_mask:0xf\n"
"v_mul_f32_dpp v26, v250, v26 row_newbcast:2 row_mask:0xf bank_mask:0xf\n"
"v_mul_f32_dpp v28, v250, v28 row_newbcast:0 row_mask:0xf bank_mask:0xf\n"
"v_mul_f32_dpp v59, v250, v59 row_newbcast:3 row_mask:0xf bank_mask:0xf\n"
"v_mul_f32_dpp v63, v250, v63 row_newbcast:3 row_mask:0xf bank_mask:0xf\n"
"v_mfma_f32_16x16x32_bf16 v[176:179], v[224:227], a[252:255], v[196:199]\n"
"ds_read_b64_tr_b16 v[212:213], v234 offset:16384\n"
"v_mul_f32_dpp v30, v250, v30 row_newbcast:2 row_mask:0xf bank_mask:0xf\n"
"v_mfma_f32_16x16x32_bf16 v[176:179], v[120:123], a[248:251], v[176:179]\n"
"v_mul_f32_dpp v24, v250, v24 row_newbcast:0 row_mask:0xf bank_mask:0xf\n"
"v_fmac_f32_dpp v27, v252, v251 row_newbcast:3 row_mask:0xf bank_mask:0xf\n"
"v_mfma_f32_16x16x32_bf16 v[176:179], v[116:119], a[244:247], v[176:179]\n"
"ds_read_b64_tr_b16 v[216:217], v230 offset:24576\n"
"v_accvgpr_read_b32 v253, a210\n"
"v_mfma_f32_16x16x32_bf16 v[176:179], v[112:115], a[240:243], v[176:179]\n"
"ds_read_b64_tr_b16 v[222:223], v234 offset:26624\n"
"v_mul_f32_dpp v25, v250, v25 row_newbcast:1 row_mask:0xf bank_mask:0xf\n"
"v_mfma_f32_16x16x32_bf16 v[176:179], v[108:111], a[204:207], v[176:179]\n"
"ds_read_b64_tr_b16 a[236:237], v237 offset:10240\n"
"v_mul_f32_dpp v29, v250, v29 row_newbcast:1 row_mask:0xf bank_mask:0xf\n"
"v_mfma_f32_16x16x32_bf16 v[176:179], v[104:107], a[200:203], v[176:179]\n"
"ds_read_b64_tr_b16 v[214:215], v234 offset:18432\n"
"v_fmac_f32_dpp v25, v252, v255 row_newbcast:1 row_mask:0xf bank_mask:0xf\n"
"v_mfma_f32_16x16x32_bf16 v[176:179], v[100:103], a[196:199], v[176:179]\n"
"v_accvgpr_read_b32 v255, a213\n"
"v_accvgpr_read_b32 v251, a215\n"
"v_mfma_f32_16x16x32_bf16 v[176:179], v[96:99], a[192:195], v[176:179]\n"
"s_waitcnt lgkmcnt(5)\n"
"ds_read_b64_tr_b16 v[112:113], v231 offset:16384\n"
"ds_read_b64_tr_b16 v[122:123], v231 offset:26624\n"
"v_fmac_f32_dpp v24, v252, v254 row_newbcast:0 row_mask:0xf bank_mask:0xf\n"
"v_fmac_f32_dpp v26, v252, v253 row_newbcast:2 row_mask:0xf bank_mask:0xf\n"
"v_accvgpr_read_b32 v254, a212\n"
"v_accvgpr_read_b32 v253, a214\n"
"v_fmac_f32_dpp v31, v252, v251 row_newbcast:3 row_mask:0xf bank_mask:0xf\n"
"v_accvgpr_read_b32 v251, a227\n"
"v_mfma_f32_16x16x32_bf16 v[132:135], v[180:183], a[96:99], v[196:199]\n"
"ds_read_b64_tr_b16 a[222:223], v237 offset:8200\n"
"v_cvt_pk_bf16_f32 v24, v24, v25\n"
"v_mfma_f32_16x16x32_bf16 v[132:135], v[184:187], a[100:103], v[132:135]\n"
"ds_read_b64_tr_b16 v[120:121], v231 offset:24576\n"
"v_mul_f32_dpp v56, v250, v56 row_newbcast:0 row_mask:0xf bank_mask:0xf\n"
"v_mfma_f32_16x16x32_bf16 v[132:135], v[188:191], a[104:107], v[132:135]\n"
"v_fmac_f32_dpp v28, v252, v254 row_newbcast:0 row_mask:0xf bank_mask:0xf\n"
"v_mul_f32_dpp v60, v250, v60 row_newbcast:0 row_mask:0xf bank_mask:0xf\n"
"v_mfma_f32_16x16x32_bf16 v[132:135], v[192:195], a[108:111], v[132:135]\n"
"v_fmac_f32_dpp v29, v252, v255 row_newbcast:1 row_mask:0xf bank_mask:0xf\n"
"v_accvgpr_read_b32 v255, a225\n"
"v_mfma_f32_16x16x32_bf16 v[132:135], v[208:211], a[144:147], v[132:135]\n"
"s_waitcnt lgkmcnt(4)\n"
"s_waitcnt vmcnt(0)\n"
"v_fmac_f32_dpp v30, v252, v253 row_newbcast:2 row_mask:0xf bank_mask:0xf\n"
"v_accvgpr_read_b32 v254, a224\n"
"v_mfma_f32_16x16x32_bf16 v[132:135], v[212:215], a[148:151], v[132:135]\n"
"v_subrev_u32 v200, s21, v200\n"
"ds_read_b64_tr_b16 v[226:227], v235 offset:26624\n"
"v_mfma_f32_16x16x32_bf16 v[132:135], v[216:219], a[152:155], v[132:135]\n"
"v_cmp_lt_u32 vcc, s69, v200\n"
"v_fmac_f32_dpp v59, v252, v251 row_newbcast:3 row_mask:0xf bank_mask:0xf\n"
"v_mfma_f32_16x16x32_bf16 v[132:135], v[220:223], a[156:159], v[132:135]\n"
"ds_read_b64_tr_b16 v[102:103], v235 offset:2048\n"
"ds_read_b64_tr_b16 v[110:111], v235 offset:10240\n"
"v_cvt_pk_bf16_f32 v17, v18, v19\n"
"v_accvgpr_read_b32 v253, a226\n"
"v_fmac_f32_dpp v56, v252, v254 row_newbcast:0 row_mask:0xf bank_mask:0xf\n"
"v_fmac_f32_dpp v57, v252, v255 row_newbcast:1 row_mask:0xf bank_mask:0xf\n"
"v_accvgpr_read_b32 v254, a228\n"
"v_accvgpr_read_b32 v255, a229\n"
"v_mul_f32_dpp v61, v250, v61 row_newbcast:1 row_mask:0xf bank_mask:0xf\n"
"s_cbranch_vccnz LGV_%=\nLGVK_%=:\n"
"v_mfma_f32_16x16x32_bf16 v[148:151], v[220:223], a[172:175], v[196:199]\n"
"ds_read_b64_tr_b16 v[100:101], v235 offset:0\n"
"v_cvt_pk_bf16_f32 v25, v26, v27\n"
"v_mfma_f32_16x16x32_bf16 v[148:151], v[216:219], a[168:171], v[148:151]\n"
"v_add_u32_dpp v200, v200, v200 quad_perm:[1,0,3,2] row_mask:0xf bank_mask:0xf\n"
"ds_read_b64_tr_b16 v[104:105], v231 offset:8192\n"
"v_mfma_f32_16x16x32_bf16 v[148:151], v[212:215], a[164:167], v[148:151]\n"
"ds_read_b64_tr_b16 v[98:99], v231 offset:2048\n"
"v_fmac_f32_dpp v58, v252, v253 row_newbcast:2 row_mask:0xf bank_mask:0xf\n"
"v_mfma_f32_16x16x32_bf16 v[148:151], v[208:211], a[160:163], v[148:151]\n"
"v_add_u32_dpp v200, v200, v200 quad_perm:[2,3,0,1] row_mask:0xf bank_mask:0xf\n"
"v_accvgpr_read_b32 v251, a231\n"
"v_mfma_f32_16x16x32_bf16 v[148:151], v[192:195], a[124:127], v[148:151]\n"
"ds_read_b64_tr_b16 v[96:97], v231 offset:0\n"
"ds_read_b64_tr_b16 v[116:117], v235 offset:16384\n"
"v_cvt_pk_bf16_f32 v26, v28, v29\n"
"v_cvt_pk_bf16_f32 v27, v30, v31\n"
"v_accvgpr_read_b32 v253, a230\n"
"v_mul_f32_dpp v62, v250, v62 row_newbcast:2 row_mask:0xf bank_mask:0xf\n"
"v_fmac_f32_dpp v60, v252, v254 row_newbcast:0 row_mask:0xf bank_mask:0xf\n"
"v_cvt_pk_bf16_f32 v56, v56, v57\n"
"v_mfma_f32_16x16x32_bf16 v[148:151], v[188:191], a[120:123], v[148:151]\n"
"v_cmp_lt_u32 vcc, 1, v200\n"
"ds_write_b128 v243, v[24:27] offset:0\n"
"v_fmac_f32_dpp v61, v252, v255 row_newbcast:1 row_mask:0xf bank_mask:0xf\n"
"v_fmac_f32_dpp v62, v252, v253 row_newbcast:2 row_mask:0xf bank_mask:0xf\n"
"v_cvt_pk_bf16_f32 v57, v58, v59\n"
"v_cvt_pk_bf16_f32 v58, v60, v61\n"
"v_mfma_f32_16x16x32_bf16 v[148:151], v[184:187], a[116:119], v[148:151]\n"
"v_fmac_f32_dpp v63, v252, v251 row_newbcast:3 row_mask:0xf bank_mask:0xf\n"
"v_cvt_pk_bf16_f32 v59, v62, v63\n"
"v_mfma_f32_16x16x32_bf16 v[148:151], v[180:183], a[112:115], v[148:151]\n"
"ds_write_b128 v243, v[56:59] offset:2048\n"
"v_mul_f32_dpp v50, v247, v50 row_newbcast:2 row_mask:0xf bank_mask:0xf\n"
"v_cvt_pk_bf16_f32 v48, v48, v49\n"
"v_mfma_f32_16x16x32_bf16 v[164:167], v[180:183], a[128:131], v[196:199]\n"
"s_mov_b32 s47, 0\n"
"v_xor_b32 v249, s75, v249\n"
"v_mul_f32_dpp v20, v246, v20 row_newbcast:0 row_mask:0xf bank_mask:0xf\n"
"v_mfma_f32_16x16x32_bf16 v[164:167], v[184:187], a[132:135], v[164:167]\n"
"s_mov_b32 s89, vcc_lo\n"
"ds_read_b64_tr_b16 v[106:107], v231 offset:10240\n"
"ds_read_b64_tr_b16 v[224:225], v235 offset:24576\n"
"v_mfma_f32_16x16x32_bf16 v[164:167], v[188:191], a[136:139], v[164:167]\n"
"s_lshr_b32 s90, s89, s93\n"
"ds_read_b64_tr_b16 v[114:115], v231 offset:18432\n"
"v_cvt_pk_bf16_f32 v19, v22, v23\n"
"v_mfma_f32_16x16x32_bf16 v[164:167], v[192:195], a[140:143], v[164:167]\n"
"s_and_b32 s90, s90, 1\n"
"ds_read_b64_tr_b16 v[108:109], v235 offset:8192\n"
"v_mul_f32_dpp v87, v206, v87 row_newbcast:3 row_mask:0xf bank_mask:0xf\n"
"v_mfma_f32_16x16x32_bf16 v[164:167], v[208:211], a[176:179], v[164:167]\n"
"s_lshl_b32 s16, s61, 7\n"
"v_cvt_pk_bf16_f32 v18, v20, v21\n"
"v_mul_f32_dpp v51, v247, v51 row_newbcast:3 row_mask:0xf bank_mask:0xf\n"
"v_mfma_f32_16x16x32_bf16 v[164:167], v[212:215], a[180:183], v[164:167]\n"
"s_add_u32 s90, s90, s16\n"
"ds_read_b64_tr_b16 v[118:119], v235 offset:18432\n"
"v_mul_f32_dpp v85, v206, v85 row_newbcast:1 row_mask:0xf bank_mask:0xf\n"
"v_mfma_f32_16x16x32_bf16 v[164:167], v[216:219], a[184:187], v[164:167]\n"
"s_waitcnt vmcnt(63) lgkmcnt(5)\n"
"s_lshl_b32 s90, s90, 13\n"
"v_cvt_pk_bf16_f32 v80, v80, v81\n"
"v_mfma_f32_16x16x32_bf16 v[164:167], v[220:223], a[188:191], v[164:167]\n"
"s_barrier\n"
"s_lshl_b32 s16, s95, 9\n"
"ds_write_b128 v248, v[16:19] offset:49152\n"
"v_mfma_f32_16x16x32_bf16 v[136:139], v[96:99], a[96:99], v[196:199]\n"
"s_add_u32 s16, s16, s90\n"
"v_mul_f32_dpp v53, v247, v53 row_newbcast:1 row_mask:0xf bank_mask:0xf\n"
"v_cvt_pk_bf16_f32 v49, v50, v51\n"
"v_mfma_f32_16x16x32_bf16 v[136:139], v[100:103], a[100:103], v[136:139]\n"
"s_waitcnt lgkmcnt(4)\n"
"s_add_u32 s17, s16, 4096\n"
"ds_read_b64_tr_b16 a[228:229], v237 offset:14336\n"
"v_cvt_pk_bf16_f32 v50, v52, v53\n"
"v_mfma_f32_16x16x32_bf16 v[136:139], v[104:107], a[104:107], v[136:139]\n"
"s_waitcnt lgkmcnt(3)\n"
"s_mov_b32 s72, s61\n"
"ds_read_b64_tr_b16 a[10:11], v249 offset:49160\n"
"v_mov_b64 v[90:91], 0\n"
"v_mfma_f32_16x16x32_bf16 v[136:139], v[108:111], a[108:111], v[136:139]\n"
"s_mov_b32 s21, s60\n"
"ds_read_b64_tr_b16 v[220:221], v232 offset:57344\n"
"v_mul_f32_dpp v84, v206, v84 row_newbcast:0 row_mask:0xf bank_mask:0xf\n"
"v_mfma_f32_16x16x32_bf16 v[136:139], v[112:115], a[144:147], v[136:139]\n"
"s_waitcnt lgkmcnt(4)\n"
"s_mov_b32 s18, s77\n"
"ds_read_b64_tr_b16 a[8:9], v249 offset:49152\n"
"v_mul_f32_dpp v83, v206, v83 row_newbcast:3 row_mask:0xf bank_mask:0xf\n"
"v_mfma_f32_16x16x32_bf16 v[136:139], v[116:119], a[148:151], v[136:139]\n"
"s_mov_b32 s19, s77\n"
"ds_read_b64_tr_b16 v[180:181], v228 offset:32768\n"
"v_cvt_pk_bf16_f32 v51, v54, v55\n"
"v_mfma_f32_16x16x32_bf16 v[136:139], v[120:123], a[152:155], v[136:139]\n"
"s_mov_b32 s69, -1\n"
"ds_read_b64_tr_b16 v[182:183], v228 offset:34816\n"
"v_mul_f32_dpp v82, v206, v82 row_newbcast:2 row_mask:0xf bank_mask:0xf\n"
"v_mfma_f32_16x16x32_bf16 v[136:139], v[224:227], a[156:159], v[136:139]\n"
"s_mul_i32 s88, s61, 0x10011111\n"
"ds_read_b64_tr_b16 v[212:213], v232 offset:49152\n"
"v_cvt_pk_bf16_f32 v81, v82, v83\n"
"v_mfma_f32_16x16x32_bf16 v[152:155], v[224:227], a[172:175], v[196:199]\n"
"s_add_u32 s88, s88, 484675\n"
"ds_read_b64_tr_b16 v[186:187], v232 offset:34816\n"
"v_cvt_pk_bf16_f32 v83, v86, v87\n"
"v_mfma_f32_16x16x32_bf16 v[152:155], v[120:123], a[168:171], v[152:155]\n"
"s_and_b32 s88, s88, 0x70077777\n"
"ds_read_b64_tr_b16 v[216:217], v228 offset:57344\n"
"v_cvt_pk_bf16_f32 v82, v84, v85\n"
"v_mfma_f32_16x16x32_bf16 v[152:155], v[116:119], a[164:167], v[152:155]\n"
"s_or_b32 s88, s88, 0x9800000\n"
"ds_write_b128 v248, v[48:51] offset:50176\n"
"ds_write_b128 v248, v[80:83] offset:51200\n"
"v_mfma_f32_16x16x32_bf16 v[152:155], v[112:115], a[160:163], v[152:155]\n"
"s_mov_b32 s84, s6\n"
"ds_read_b64_tr_b16 a[24:25], v249 offset:50176\n"
"ds_read_b64_tr_b16 a[26:27], v249 offset:50184\n"
"ds_read_b64_tr_b16 a[40:41], v249 offset:51200\n"
"ds_read_b64_tr_b16 a[42:43], v249 offset:51208\n"
"v_mfma_f32_16x16x32_bf16 v[152:155], v[108:111], a[124:127], v[152:155]\n"
"s_and_b32 s85, s7, 0xffff\n"
"ds_read_b64_tr_b16 v[188:189], v228 offset:40960\n"
"v_bfe_u32 v254, v242, 4, 4\n"
"v_mfma_f32_16x16x32_bf16 v[152:155], v[104:107], a[120:123], v[152:155]\n"
"s_mov_b32 s86, 128\n"
"ds_read_b64_tr_b16 v[184:185], v232 offset:32768\n"
"ds_read_b64_tr_b16 v[222:223], v232 offset:59392\n"
"v_mfma_f32_16x16x32_bf16 v[152:155], v[100:103], a[116:119], v[152:155]\n"
"s_mov_b32 s87, 131072\n"
"ds_read_b64_tr_b16 a[224:225], v236 offset:14336\n"
"v_mfma_f32_16x16x32_bf16 v[152:155], v[96:99], a[112:115], v[152:155]\n"
"ds_read_b64_tr_b16 v[192:193], v232 offset:40960\n"
"ds_read_b64_tr_b16 v[214:215], v232 offset:51200\n"
"v_mfma_f32_16x16x32_bf16 v[168:171], v[96:99], a[128:131], v[196:199]\n"
"ds_read_b64_tr_b16 v[190:191], v228 offset:43008\n"
"ds_read_b64_tr_b16 v[218:219], v228 offset:59392\n"
"v_mfma_f32_16x16x32_bf16 v[168:171], v[100:103], a[132:135], v[168:171]\n"
"ds_read_b64_tr_b16 v[194:195], v232 offset:43008\n"
"ds_read_b64_tr_b16 v[210:211], v228 offset:51200\n"
"v_mfma_f32_16x16x32_bf16 v[168:171], v[104:107], a[136:139], v[168:171]\n"
"ds_read_b64_tr_b16 a[234:235], v236 offset:10248\n"
"ds_read_b64_tr_b16 v[208:209], v228 offset:49152\n"
"v_mfma_f32_16x16x32_bf16 v[168:171], v[108:111], a[140:143], v[168:171]\n"
"s_waitcnt lgkmcnt(12)\n"
"ds_read_b64_tr_b16 a[212:213], v237 offset:12288\n"
"ds_read_b64_tr_b16 a[230:231], v237 offset:14344\n"
"v_mfma_f32_16x16x32_bf16 v[168:171], v[112:115], a[176:179], v[168:171]\n"
"ds_read_b64_tr_b16 a[232:233], v236 offset:10240\n"
"ds_read_b64_tr_b16 a[214:215], v237 offset:12296\n"
"v_mfma_f32_16x16x32_bf16 v[168:171], v[116:119], a[180:183], v[168:171]\n"
"ds_read_b64_tr_b16 v[96:97], v229 offset:32768\n"
"ds_read_b64_tr_b16 v[100:101], v233 offset:32768\n"
"v_mfma_f32_16x16x32_bf16 v[168:171], v[120:123], a[184:187], v[168:171]\n"
"s_add_u32 s41, s70, 327680\n"
"s_add_u32 m0, s71, 32768\n"
"ds_read_b64_tr_b16 v[104:105], v229 offset:40960\n"
"v_mfma_f32_16x16x32_bf16 v[168:171], v[224:227], a[188:191], v[168:171]\n"
"buffer_load_dwordx4 v240, s[28:31], s41 offen lds\n"
"v_mfma_f32_16x16x32_bf16 v[124:127], v[180:183], a[48:51], v[124:127]\n"
"s_waitcnt lgkmcnt(11)\n"
"v_and_b32 v243, 0x3f0, v242\n"
"v_mov_b64 v[246:247], 0\n"
"v_mfma_f32_16x16x32_bf16 v[124:127], v[184:187], a[52:55], v[124:127]\n"
"v_bfe_u32 v255, v242, 8, 1\n"
"v_mfma_f32_16x16x32_bf16 v[124:127], v[188:191], a[56:59], v[124:127]\n"
"s_waitcnt lgkmcnt(7)\n"
"s_add_u32 s40, s41, s52\n"
"ds_read_b64_tr_b16 a[210:211], v236 offset:12296\n"
"ds_read_b64_tr_b16 v[98:99], v229 offset:34816\n"
"v_mfma_f32_16x16x32_bf16 v[124:127], v[192:195], a[60:63], v[124:127]\n"
"buffer_load_dwordx4 v240, s[28:31], s40 offen offset:1024 lds\n"
"v_mfma_f32_16x16x32_bf16 v[124:127], v[208:211], a[0:3], v[124:127]\n"
"v_xor_b32 v248, s75, v248\n"
"v_mov_b64 v[52:53], s[22:23]\n"
"v_mfma_f32_16x16x32_bf16 v[124:127], v[212:215], a[4:7], v[124:127]\n"
"v_mov_b64 v[6:7], s[22:23]\n"
"v_mfma_f32_16x16x32_bf16 v[124:127], v[216:219], a[8:11], v[124:127]\n"
"s_add_u32 s40, s41, s53\n"
"ds_read_b64_tr_b16 v[102:103], v233 offset:34816\n"
"ds_read_b64_tr_b16 v[110:111], v233 offset:43008\n"
"v_mfma_f32_16x16x32_bf16 v[124:127], v[220:223], a[12:15], v[124:127]\n"
"buffer_load_dwordx4 v240, s[28:31], s40 offen offset:2048 lds\n"
"v_mfma_f32_16x16x32_bf16 v[140:143], v[220:223], a[28:31], v[140:143]\n"
"v_mov_b64 v[2:3], s[22:23]\n"
"v_mov_b64 v[10:11], s[22:23]\n"
"v_mfma_f32_16x16x32_bf16 v[140:143], v[216:219], a[24:27], v[140:143]\n"
"v_mfma_f32_16x16x32_bf16 v[140:143], v[212:215], a[20:23], v[140:143]\n"
"ds_read_b64_tr_b16 a[220:221], v237 offset:8192\n"
"ds_read_b64_tr_b16 v[116:117], v233 offset:49152\n"
"v_mfma_f32_16x16x32_bf16 v[140:143], v[208:211], a[16:19], v[140:143]\n"
"ds_read_b64_tr_b16 a[208:209], v236 offset:12288\n"
"v_mul_u32_u24 v254, 2560, v254\n"
"v_mfma_f32_16x16x32_bf16 v[140:143], v[192:195], a[76:79], v[140:143]\n"
"ds_read_b64_tr_b16 v[122:123], v229 offset:59392\n"
"v_subrev_f32 v124, s22, v124\n"
"v_mfma_f32_16x16x32_bf16 v[140:143], v[188:191], a[72:75], v[140:143]\n"
"ds_read_b64_tr_b16 a[226:227], v236 offset:14344\n"
"v_mov_b64 v[18:19], s[22:23]\n"
"v_mfma_f32_16x16x32_bf16 v[140:143], v[184:187], a[68:71], v[140:143]\n"
"s_add_u32 s40, s41, s54\n"
"ds_read_b64_tr_b16 v[112:113], v229 offset:49152\n"
"ds_read_b64_tr_b16 v[120:121], v229 offset:57344\n"
"v_mfma_f32_16x16x32_bf16 v[140:143], v[180:183], a[64:67], v[140:143]\n"
"buffer_load_dwordx4 v240, s[28:31], s40 offen offset:3072 lds\n"
"v_mfma_f32_16x16x32_bf16 v[156:159], v[180:183], a[80:83], v[156:159]\n"
"s_add_u32 s41, s70, 491520\n"
"s_add_u32 m0, s71, 49152\n"
"v_lshl_add_u32 v254, v255, 5, v254\n"
"v_mfma_f32_16x16x32_bf16 v[156:159], v[184:187], a[84:87], v[156:159]\n"
"buffer_load_dwordx4 v240, s[28:31], s41 offen lds\n"
"v_mfma_f32_16x16x32_bf16 v[156:159], v[188:191], a[88:91], v[156:159]\n"
"v_subrev_f32 v125, s22, v125\n"
"v_cvt_pk_bf16_f32 v124, v124, v125\n"
"v_mfma_f32_16x16x32_bf16 v[156:159], v[192:195], a[92:95], v[156:159]\n"
"v_bfe_u32 v255, v242, 9, 1\n"
"v_mfma_f32_16x16x32_bf16 v[156:159], v[208:211], a[32:35], v[156:159]\n"
"ds_read_b64_tr_b16 v[106:107], v229 offset:43008\n"
"v_lshl_add_u32 v254, v255, 4, v254\n"
"v_mfma_f32_16x16x32_bf16 v[156:159], v[212:215], a[36:39], v[156:159]\n"
"ds_read_b64_tr_b16 v[114:115], v229 offset:51200\n"
"v_subrev_f32 v142, s22, v142\n"
"v_mfma_f32_16x16x32_bf16 v[156:159], v[216:219], a[40:43], v[156:159]\n"
"s_add_u32 s40, s41, s52\n"
"ds_read_b64_tr_b16 v[108:109], v233 offset:40960\n"
"ds_read_b64_tr_b16 v[118:119], v233 offset:51200\n"
"v_mfma_f32_16x16x32_bf16 v[156:159], v[220:223], a[44:47], v[156:159]\n"
"s_waitcnt lgkmcnt(4)\n"
"buffer_load_dwordx4 v240, s[28:31], s40 offen offset:1024 lds\n"
"v_mfma_f32_16x16x32_bf16 v[172:175], v[220:223], a[228:231], v[172:175]\n"
"s_add_u32 s40, s41, s53\n"
"v_mov_b64 v[32:33], s[22:23]\n"
"v_mov_b64 v[34:35], s[22:23]\n"
"v_mfma_f32_16x16x32_bf16 v[172:175], v[216:219], a[224:227], v[172:175]\n"
"buffer_load_dwordx4 v240, s[28:31], s40 offen offset:2048 lds\n"
"v_mfma_f32_16x16x32_bf16 v[172:175], v[212:215], a[212:215], v[172:175]\n"
"v_mov_b64 v[88:89], 0\n"
"v_subrev_f32 v141, s22, v141\n"
"v_mfma_f32_16x16x32_bf16 v[172:175], v[208:211], a[208:211], v[172:175]\n"
"v_add_u32 v236, s94, v243\n"
"v_mov_b32 v241, s60\n"
"v_mfma_f32_16x16x32_bf16 v[172:175], v[192:195], a[236:239], v[172:175]\n"
"s_add_u32 s40, s41, s54\n"
"ds_read_b64_tr_b16 v[224:225], v233 offset:57344\n"
"ds_read_b64_tr_b16 v[226:227], v233 offset:59392\n"
"v_mfma_f32_16x16x32_bf16 v[172:175], v[188:191], a[232:235], v[172:175]\n"
"buffer_load_dwordx4 v240, s[28:31], s40 offen offset:3072 lds\n"
"v_mfma_f32_16x16x32_bf16 v[172:175], v[184:187], a[220:223], v[172:175]\n"
"s_lshl_b32 s62, s70, 9\n"
"v_xor_b32 v228, s75, v228\n"
"v_mfma_f32_16x16x32_bf16 v[172:175], v[180:183], a[216:219], v[172:175]\n"
"s_and_b32 s70, s88, 15\n"
"v_mov_b64 v[26:27], s[22:23]\n"
"v_mfma_f32_16x16x32_bf16 v[128:131], v[96:99], a[48:51], v[128:131]\n"
"s_lshr_b32 s88, s88, 4\n"
"ds_read_b64_tr_b16 v[186:187], v234 offset:34816\n"
"v_mfma_f32_16x16x32_bf16 v[128:131], v[100:103], a[52:55], v[128:131]\n"
"s_waitcnt lgkmcnt(3)\n"
"s_lshl_b32 s70, s70, 7\n"
"ds_read_b64_tr_b16 v[180:181], v230 offset:32768\n"
"v_subrev_f32 v143, s22, v143\n"
"v_mfma_f32_16x16x32_bf16 v[128:131], v[104:107], a[56:59], v[128:131]\n"
"s_xor_b32 s71, s71, s75\n"
"v_mov_b64 v[86:87], 0\n"
"v_subrev_f32 v127, s22, v127\n"
"v_mfma_f32_16x16x32_bf16 v[128:131], v[108:111], a[60:63], v[128:131]\n"
"v_lshlrev_b32_e64 v201, 2, s72\n"
"ds_read_b64_tr_b16 v[214:215], v234 offset:51200\n"
"v_subrev_f32 v173, s22, v173\n"
"v_mfma_f32_16x16x32_bf16 v[128:131], v[112:115], a[0:3], v[128:131]\n"
"global_load_dword v200, v201, s[6:7] sc1\n"
"v_mfma_f32_16x16x32_bf16 v[128:131], v[116:119], a[4:7], v[128:131]\n"
"v_mov_b64 v[28:29], s[22:23]\n"
"v_subrev_f32 v140, s22, v140\n"
"v_mfma_f32_16x16x32_bf16 v[128:131], v[120:123], a[8:11], v[128:131]\n"
"s_waitcnt lgkmcnt(3)\n"
"v_mov_b64 v[38:39], s[22:23]\n"
"v_add_u32 v241, 1, v241\n"
"v_mfma_f32_16x16x32_bf16 v[128:131], v[224:227], a[12:15], v[128:131]\n"
"ds_read_b64_tr_b16 v[208:209], v230 offset:49152\n"
"v_subrev_f32 v126, s22, v126\n"
"v_mfma_f32_16x16x32_bf16 v[144:147], v[224:227], a[28:31], v[144:147]\n"
"v_mov_b64 v[94:95], 0\n"
"v_cvt_pk_bf16_f32 v140, v140, v141\n"
"v_mfma_f32_16x16x32_bf16 v[144:147], v[120:123], a[24:27], v[144:147]\n"
"ds_read_b64_tr_b16 v[192:193], v234 offset:40960\n"
"v_subrev_f32 v175, s22, v175\n"
"v_mfma_f32_16x16x32_bf16 v[144:147], v[116:119], a[20:23], v[144:147]\n"
"ds_read_b64_tr_b16 v[222:223], v234 offset:59392\n"
"v_subrev_f32 v129, s22, v129\n"
"v_mfma_f32_16x16x32_bf16 v[144:147], v[112:115], a[16:19], v[144:147]\n"
"ds_read_b64_tr_b16 v[188:189], v230 offset:40960\n"
"v_lshl_add_u32 v248, v242, 8, s93\n"
"v_mfma_f32_16x16x32_bf16 v[144:147], v[108:111], a[76:79], v[144:147]\n"
"ds_read_b64_tr_b16 v[182:183], v230 offset:34816\n"
"v_cvt_pk_bf16_f32 v125, v126, v127\n"
"v_mfma_f32_16x16x32_bf16 v[144:147], v[104:107], a[72:75], v[144:147]\n"
"v_mov_b64 v[8:9], s[22:23]\n"
"v_subrev_f32 v130, s22, v130\n"
"v_mfma_f32_16x16x32_bf16 v[144:147], v[100:103], a[68:71], v[144:147]\n"
"ds_read_b64_tr_b16 v[212:213], v234 offset:49152\n"
"v_subrev_f32 v172, s22, v172\n"
"v_mfma_f32_16x16x32_bf16 v[144:147], v[96:99], a[64:67], v[144:147]\n"
"ds_read_b64_tr_b16 v[220:221], v234 offset:57344\n"
"v_mfma_f32_16x16x32_bf16 v[160:163], v[96:99], a[80:83], v[160:163]\n"
"ds_read_b64_tr_b16 v[216:217], v230 offset:57344\n"
"v_add_u32 v238, s97, v254\n"
"v_mfma_f32_16x16x32_bf16 v[160:163], v[100:103], a[84:87], v[160:163]\n"
"v_lshl_add_u32 v243, s44, 2, v243\n"
"v_bfe_u32 v237, v242, 4, 3\n"
"v_mfma_f32_16x16x32_bf16 v[160:163], v[104:107], a[88:91], v[160:163]\n"
"ds_read_b64_tr_b16 v[190:191], v230 offset:43008\n"
"v_subrev_f32 v128, s22, v128\n"
"v_mfma_f32_16x16x32_bf16 v[160:163], v[108:111], a[92:95], v[160:163]\n"
"v_max_u32 v237, 1, v237\n"
"v_subrev_f32 v156, s22, v156\n"
"v_mfma_f32_16x16x32_bf16 v[160:163], v[112:115], a[32:35], v[160:163]\n"
"ds_read_b64_tr_b16 v[184:185], v234 offset:32768\n"
"v_subrev_f32 v159, s22, v159\n"
"v_mfma_f32_16x16x32_bf16 v[160:163], v[116:119], a[36:39], v[160:163]\n"
"v_subrev_f32 v131, s22, v131\n"
"v_cvt_pk_bf16_f32 v126, v128, v129\n"
"v_mfma_f32_16x16x32_bf16 v[160:163], v[120:123], a[40:43], v[160:163]\n"
"ds_read_b64_tr_b16 v[194:195], v234 offset:43008\n"
"v_cvt_pk_bf16_f32 v127, v130, v131\n"
"v_mfma_f32_16x16x32_bf16 v[160:163], v[224:227], a[44:47], v[160:163]\n"
"buffer_store_dwordx4 v[124:127], v243, s[32:35], s16 offen sc0 sc1\n"
"v_subrev_f32 v147, s22, v147\n"
"v_mfma_f32_16x16x32_bf16 v[176:179], v[224:227], a[228:231], v[176:179]\n"
"ds_read_b64_tr_b16 v[218:219], v230 offset:59392\n"
"v_mfma_f32_16x16x32_bf16 v[176:179], v[120:123], a[224:227], v[176:179]\n"
"v_subrev_f32 v157, s22, v157\n"
"v_subrev_f32 v146, s22, v146\n"
"v_mfma_f32_16x16x32_bf16 v[176:179], v[116:119], a[212:215], v[176:179]\n"
"ds_read_b64_tr_b16 v[210:211], v230 offset:51200\n"
"v_mfma_f32_16x16x32_bf16 v[176:179], v[112:115], a[208:211], v[176:179]\n"
"v_cvt_pk_bf16_f32 v141, v142, v143\n"
"v_subrev_f32 v145, s22, v145\n"
"v_mfma_f32_16x16x32_bf16 v[176:179], v[108:111], a[236:239], v[176:179]\n"
"ds_read_b64_tr_b16 v[226:227], v235 offset:59392\n"
"v_subrev_f32 v158, s22, v158\n"
"v_mfma_f32_16x16x32_bf16 v[176:179], v[104:107], a[232:235], v[176:179]\n"
"v_subrev_f32 v174, s22, v174\n"
"v_mfma_f32_16x16x32_bf16 v[176:179], v[100:103], a[220:223], v[176:179]\n"
"ds_read_b64_tr_b16 v[100:101], v235 offset:32768\n"
"v_mov_b64 v[36:37], s[22:23]\n"
"v_mfma_f32_16x16x32_bf16 v[176:179], v[96:99], a[216:219], v[176:179]\n"
"s_waitcnt lgkmcnt(4)\n"
"v_subrev_f32 v144, s22, v144\n"
"v_subrev_f32 v162, s22, v162\n"
"v_mfma_f32_16x16x32_bf16 v[132:135], v[180:183], a[48:51], v[132:135]\n"
"ds_read_b64_tr_b16 v[106:107], v231 offset:43008\n"
"v_subrev_f32 v160, s22, v160\n"
"v_mfma_f32_16x16x32_bf16 v[132:135], v[184:187], a[52:55], v[132:135]\n"
"ds_read_b64_tr_b16 v[96:97], v231 offset:32768\n"
"v_mfma_f32_16x16x32_bf16 v[132:135], v[188:191], a[56:59], v[132:135]\n"
"v_cvt_pk_bf16_f32 v142, v144, v145\n"
"v_cvt_pk_bf16_f32 v143, v146, v147\n"
"v_mfma_f32_16x16x32_bf16 v[132:135], v[192:195], a[60:63], v[132:135]\n"
"s_waitcnt lgkmcnt(4)\n"
"ds_read_b64_tr_b16 v[112:113], v231 offset:49152\n"
"v_mov_b64 v[50:51], s[22:23]\n"
"v_mfma_f32_16x16x32_bf16 v[132:135], v[208:211], a[0:3], v[132:135]\n"
"v_xor_b32 v237, s61, v237\n"
"v_cvt_pk_bf16_f32 v172, v172, v173\n"
"v_mfma_f32_16x16x32_bf16 v[132:135], v[212:215], a[4:7], v[132:135]\n"
"ds_read_b64_tr_b16 v[116:117], v235 offset:49152\n"
"v_xor_b32 v233, s75, v233\n"
"v_mfma_f32_16x16x32_bf16 v[132:135], v[216:219], a[8:11], v[132:135]\n"
"v_mov_b64 v[30:31], s[22:23]\n"
"v_subrev_f32 v179, s22, v179\n"
"v_mfma_f32_16x16x32_bf16 v[132:135], v[220:223], a[12:15], v[132:135]\n"
"buffer_store_dwordx4 v[140:143], v243, s[32:35], s16 offen offset:2048 sc0 sc1\n"
"ds_read_b64_tr_b16 v[120:121], v231 offset:57344\n"
"v_mfma_f32_16x16x32_bf16 v[148:151], v[220:223], a[28:31], v[148:151]\n"
"v_cvt_pk_bf16_f32 v156, v156, v157\n"
"v_subrev_f32 v161, s22, v161\n"
"v_mfma_f32_16x16x32_bf16 v[148:151], v[216:219], a[24:27], v[148:151]\n"
"ds_read_b64_tr_b16 v[110:111], v235 offset:43008\n"
"v_subrev_f32 v178, s22, v178\n"
"v_mfma_f32_16x16x32_bf16 v[148:151], v[212:215], a[20:23], v[148:151]\n"
"ds_read_b64_tr_b16 v[102:103], v235 offset:34816\n"
"v_mov_b64 v[48:49], s[22:23]\n"
"v_mfma_f32_16x16x32_bf16 v[148:151], v[208:211], a[16:19], v[148:151]\n"
"v_add_u32 v253, s99, v254\n"
"v_subrev_f32 v132, s22, v132\n"
"v_mfma_f32_16x16x32_bf16 v[148:151], v[192:195], a[76:79], v[148:151]\n"
"ds_read_b64_tr_b16 v[98:99], v231 offset:34816\n"
"v_cvt_pk_bf16_f32 v157, v158, v159\n"
"v_mfma_f32_16x16x32_bf16 v[148:151], v[188:191], a[72:75], v[148:151]\n"
"ds_read_b64_tr_b16 v[108:109], v235 offset:40960\n"
"v_subrev_f32 v135, s22, v135\n"
"v_mfma_f32_16x16x32_bf16 v[148:151], v[184:187], a[68:71], v[148:151]\n"
"v_mfma_f32_16x16x32_bf16 v[148:151], v[180:183], a[64:67], v[148:151]\n"
"ds_read_b64_tr_b16 v[118:119], v235 offset:51200\n"
"v_mfma_f32_16x16x32_bf16 v[164:167], v[180:183], a[80:83], v[164:167]\n"
"ds_read_b64_tr_b16 v[104:105], v231 offset:40960\n"
"v_subrev_f32 v163, s22, v163\n"
"v_mfma_f32_16x16x32_bf16 v[164:167], v[184:187], a[84:87], v[164:167]\n"
"ds_read_b64_tr_b16 v[114:115], v231 offset:51200\n"
"v_mov_b64 v[84:85], 0\n"
"v_mfma_f32_16x16x32_bf16 v[164:167], v[188:191], a[88:91], v[164:167]\n"
"v_cvt_pk_bf16_f32 v158, v160, v161\n"
"v_subrev_f32 v133, s22, v133\n"
"v_mfma_f32_16x16x32_bf16 v[164:167], v[192:195], a[92:95], v[164:167]\n"
"ds_read_b64_tr_b16 v[122:123], v231 offset:59392\n"
"v_mov_b64 v[14:15], s[22:23]\n"
"v_mfma_f32_16x16x32_bf16 v[164:167], v[208:211], a[32:35], v[164:167]\n"
"v_mov_b64 v[24:25], s[22:23]\n"
"v_xor_b32 v232, s75, v232\n"
"v_mfma_f32_16x16x32_bf16 v[164:167], v[212:215], a[36:39], v[164:167]\n"
"ds_read_b64_tr_b16 v[224:225], v235 offset:57344\n"
"v_cvt_pk_bf16_f32 v159, v162, v163\n"
"v_mfma_f32_16x16x32_bf16 v[164:167], v[216:219], a[40:43], v[164:167]\n"
"v_cvt_pk_bf16_f32 v173, v174, v175\n"
"v_mfma_f32_16x16x32_bf16 v[164:167], v[220:223], a[44:47], v[164:167]\n"
"s_barrier\n"
"s_waitcnt lgkmcnt(4)\n"
"v_mfma_f32_16x16x32_bf16 v[136:139], v[96:99], a[48:51], v[136:139]\n"
"ds_read_b64_tr_b16 v[214:215], v232 offset:18432\n"
"v_cvt_pk_bf16_f32 v175, v178, v179\n"
"v_mfma_f32_16x16x32_bf16 v[136:139], v[100:103], a[52:55], v[136:139]\n"
"s_waitcnt lgkmcnt(3)\n"
"ds_read_b64_tr_b16 v[218:219], v228 offset:26624\n"
"v_mov_b64 v[0:1], s[22:23]\n"
"v_mfma_f32_16x16x32_bf16 v[136:139], v[104:107], a[56:59], v[136:139]\n"
"ds_read_b64_tr_b16 v[222:223], v232 offset:26624\n"
"v_mov_b64 v[46:47], s[22:23]\n"
"v_mfma_f32_16x16x32_bf16 v[136:139], v[108:111], a[60:63], v[136:139]\n"
"ds_read_b64_tr_b16 v[216:217], v228 offset:24576\n"
"v_mov_b64 v[44:45], s[22:23]\n"
"v_mfma_f32_16x16x32_bf16 v[136:139], v[112:115], a[0:3], v[136:139]\n"
"ds_read_b64_tr_b16 v[212:213], v232 offset:16384\n"
"v_lshlrev_b32 v237, 2, v237\n"
"v_mfma_f32_16x16x32_bf16 v[136:139], v[116:119], a[4:7], v[136:139]\n"
"s_waitcnt lgkmcnt(5)\n"
"ds_read_b64_tr_b16 v[180:181], v228 offset:0\n"
"v_cvt_pk_bf16_f32 v132, v132, v133\n"
"v_mfma_f32_16x16x32_bf16 v[136:139], v[120:123], a[8:11], v[136:139]\n"
"ds_read_b64_tr_b16 v[190:191], v228 offset:10240\n"
"v_xor_b32 v229, s75, v229\n"
"v_mfma_f32_16x16x32_bf16 v[136:139], v[224:227], a[12:15], v[136:139]\n"
"buffer_store_dwordx4 v[156:159], v243, s[32:35], s17 offen sc0 sc1\n"
"ds_read_b64_tr_b16 v[186:187], v232 offset:2048\n"
"v_mfma_f32_16x16x32_bf16 v[152:155], v[224:227], a[28:31], v[152:155]\n"
"ds_read_b64_tr_b16 v[220:221], v232 offset:24576\n"
"v_xor_b32 v239, 64, v238\n"
"v_mfma_f32_16x16x32_bf16 v[152:155], v[120:123], a[24:27], v[152:155]\n"
"ds_read_b64_tr_b16 v[210:211], v228 offset:18432\n"
"v_mfma_f32_16x16x32_bf16 v[152:155], v[116:119], a[20:23], v[152:155]\n"
"ds_read_b64_tr_b16 v[208:209], v228 offset:16384\n"
"v_subrev_f32 v134, s22, v134\n"
"v_mfma_f32_16x16x32_bf16 v[152:155], v[112:115], a[16:19], v[152:155]\n"
"ds_read_b64_tr_b16 v[194:195], v232 offset:10240\n"
"v_mov_b64 v[42:43], s[22:23]\n"
"v_mfma_f32_16x16x32_bf16 v[152:155], v[108:111], a[76:79], v[152:155]\n"
"ds_read_b64_tr_b16 v[182:183], v228 offset:2048\n"
"v_add_u32 v252, s98, v254\n"
"v_mfma_f32_16x16x32_bf16 v[152:155], v[104:107], a[72:75], v[152:155]\n"
"ds_read_b64_tr_b16 v[192:193], v232 offset:8192\n"
"v_xor_b32 v249, s75, v249\n"
"v_mfma_f32_16x16x32_bf16 v[152:155], v[100:103], a[68:71], v[152:155]\n"
"ds_read_b64_tr_b16 v[184:185], v232 offset:0\n"
"ds_read_b64_tr_b16 v[188:189], v228 offset:8192\n"
"v_mfma_f32_16x16x32_bf16 v[152:155], v[96:99], a[64:67], v[152:155]\n"
"v_mov_b64 v[4:5], s[22:23]\n"
"v_subrev_f32 v176, s22, v176\n"
"v_mfma_f32_16x16x32_bf16 v[168:171], v[96:99], a[80:83], v[168:171]\n"
"ds_read_b64_tr_b16 v[96:97], v229 offset:0\n"
"ds_read_b64_tr_b16 v[98:99], v229 offset:2048\n"
"v_mfma_f32_16x16x32_bf16 v[168:171], v[100:103], a[84:87], v[168:171]\n"
"ds_read_b64_tr_b16 v[100:101], v233 offset:0\n"
"ds_read_b64_tr_b16 v[102:103], v233 offset:2048\n"
"v_mfma_f32_16x16x32_bf16 v[168:171], v[104:107], a[88:91], v[168:171]\n"
"ds_read_b64_tr_b16 v[104:105], v229 offset:8192\n"
"v_subrev_f32 v177, s22, v177\n"
"v_mfma_f32_16x16x32_bf16 v[168:171], v[108:111], a[92:95], v[168:171]\n"
"v_mov_b64 v[12:13], s[22:23]\n"
"v_mov_b64 v[22:23], s[22:23]\n"
"v_mfma_f32_16x16x32_bf16 v[168:171], v[112:115], a[32:35], v[168:171]\n"
"v_mov_b64 v[40:41], s[22:23]\n"
"v_cvt_pk_bf16_f32 v174, v176, v177\n"
"v_mfma_f32_16x16x32_bf16 v[168:171], v[116:119], a[36:39], v[168:171]\n"
"v_mov_b64 v[16:17], s[22:23]\n"
"v_mfma_f32_16x16x32_bf16 v[168:171], v[120:123], a[40:43], v[168:171]\n"
"s_add_u32 s47, s47, 1\n"
"v_mov_b64 v[20:21], s[22:23]\n"
"v_mov_b64 v[92:93], 0\n"
"v_mfma_f32_16x16x32_bf16 v[168:171], v[224:227], a[44:47], v[168:171]\n"
"s_waitcnt lgkmcnt(5)\n"
"v_mov_b64 v[54:55], s[22:23]\n"
"v_mov_b64 v[244:245], 0\n"
"LGL_%=:\n"
"v_mfma_f32_16x16x32_bf16 v[124:127], v[180:183], a[96:99], v[196:199]\n"
"ds_read_b64_tr_b16 v[106:107], v229 offset:10240\n"
"v_cvt_pk_bf16_f32 v133, v134, v135\n"
"v_mfma_f32_16x16x32_bf16 v[124:127], v[184:187], a[100:103], v[124:127]\n"
"ds_read_b64_tr_b16 v[108:109], v233 offset:8192\n"
"v_subrev_f32 v136, s22, v136\n"
"v_mfma_f32_16x16x32_bf16 v[124:127], v[188:191], a[104:107], v[124:127]\n"
"ds_read_b64_tr_b16 v[110:111], v233 offset:10240\n"
"v_subrev_f32 v137, s22, v137\n"
"v_mfma_f32_16x16x32_bf16 v[124:127], v[192:195], a[108:111], v[124:127]\n"
"ds_read_b64_tr_b16 v[112:113], v229 offset:16384\n"
"v_subrev_f32 v138, s22, v138\n"
"v_mfma_f32_16x16x32_bf16 v[124:127], v[208:211], a[144:147], v[124:127]\n"
"ds_read_b64_tr_b16 v[114:115], v229 offset:18432\n"
"v_subrev_f32 v139, s22, v139\n"
"v_mfma_f32_16x16x32_bf16 v[124:127], v[212:215], a[148:151], v[124:127]\n"
"s_waitcnt lgkmcnt(7)\n"
"ds_read_b64_tr_b16 v[116:117], v233 offset:16384\n"
"v_cvt_pk_bf16_f32 v134, v136, v137\n"
"v_mfma_f32_16x16x32_bf16 v[124:127], v[216:219], a[152:155], v[124:127]\n"
"ds_read_b64_tr_b16 v[118:119], v233 offset:18432\n"
"v_cvt_pk_bf16_f32 v135, v138, v139\n"
"v_mfma_f32_16x16x32_bf16 v[124:127], v[220:223], a[156:159], v[124:127]\n"
"buffer_store_dwordx4 v[172:175], v243, s[32:35], s17 offen offset:2048 sc0 sc1\n"
"v_subrev_f32 v148, s22, v148\n"
"v_mfma_f32_16x16x32_bf16 v[140:143], v[220:223], a[172:175], v[196:199]\n"
"ds_read_b64_tr_b16 v[120:121], v229 offset:24576\n"
"v_subrev_f32 v149, s22, v149\n"
"v_mfma_f32_16x16x32_bf16 v[140:143], v[216:219], a[168:171], v[140:143]\n"
"ds_read_b64_tr_b16 v[122:123], v229 offset:26624\n"
"v_subrev_f32 v150, s22, v150\n"
"v_mfma_f32_16x16x32_bf16 v[140:143], v[212:215], a[164:167], v[140:143]\n"
"ds_read_b64_tr_b16 v[224:225], v233 offset:24576\n"
"v_subrev_f32 v151, s22, v151\n"
"v_mfma_f32_16x16x32_bf16 v[140:143], v[208:211], a[160:163], v[140:143]\n"
"ds_read_b64_tr_b16 v[226:227], v233 offset:26624\n"
"v_cvt_pk_bf16_f32 v148, v148, v149\n"
"v_mfma_f32_16x16x32_bf16 v[140:143], v[192:195], a[124:127], v[140:143]\n"
"v_cvt_pk_bf16_f32 v149, v150, v151\n"
"v_subrev_f32 v152, s22, v152\n"
"v_mfma_f32_16x16x32_bf16 v[140:143], v[188:191], a[120:123], v[140:143]\n"
"s_add_u32 s41, s70, 0\n"
"v_subrev_f32 v153, s22, v153\n"
"v_subrev_f32 v154, s22, v154\n"
"v_mfma_f32_16x16x32_bf16 v[140:143], v[184:187], a[116:119], v[140:143]\n"
"s_mov_b32 m0, s71\n"
"v_subrev_f32 v155, s22, v155\n"
"v_cvt_pk_bf16_f32 v150, v152, v153\n"
"v_mfma_f32_16x16x32_bf16 v[140:143], v[180:183], a[112:115], v[140:143]\n"
"s_waitcnt vmcnt(14)\n"
"buffer_load_dwordx4 v240, s[28:31], s41 offen lds\n"
"v_mfma_f32_16x16x32_bf16 v[156:159], v[180:183], a[128:131], v[196:199]\n"
"v_xor_b32 v230, s75, v230\n"
"v_cvt_pk_bf16_f32 v151, v154, v155\n"
"v_mfma_f32_16x16x32_bf16 v[156:159], v[184:187], a[132:135], v[156:159]\n"
"v_xor_b32 v234, s75, v234\n"
"v_subrev_f32 v164, s22, v164\n"
"v_mfma_f32_16x16x32_bf16 v[156:159], v[188:191], a[136:139], v[156:159]\n"
"v_subrev_f32 v165, s22, v165\n"
"v_subrev_f32 v166, s22, v166\n"
"v_mfma_f32_16x16x32_bf16 v[156:159], v[192:195], a[140:143], v[156:159]\n"
"v_subrev_f32 v167, s22, v167\n"
"v_cvt_pk_bf16_f32 v164, v164, v165\n"
"v_mfma_f32_16x16x32_bf16 v[156:159], v[208:211], a[176:179], v[156:159]\n"
"v_cvt_pk_bf16_f32 v165, v166, v167\n"
"v_subrev_f32 v168, s22, v168\n"
"v_mfma_f32_16x16x32_bf16 v[156:159], v[212:215], a[180:183], v[156:159]\n"
"v_subrev_f32 v169, s22, v169\n"
"v_subrev_f32 v170, s22, v170\n"
"v_mfma_f32_16x16x32_bf16 v[156:159], v[216:219], a[184:187], v[156:159]\n"
"v_subrev_f32 v171, s22, v171\n"
"v_cvt_pk_bf16_f32 v166, v168, v169\n"
"v_mfma_f32_16x16x32_bf16 v[156:159], v[220:223], a[188:191], v[156:159]\n"
"buffer_store_dwordx4 v[132:135], v243, s[32:35], s16 offen offset:1024 sc0 sc1\n"
"v_cvt_pk_bf16_f32 v167, v170, v171\n"
"v_mfma_f32_16x16x32_bf16 v[172:175], v[220:223], a[252:255], v[196:199]\n"
"ds_read_b64_tr_b16 v[220:221], v234 offset:24576\n"
"v_xor_b32 v231, s75, v231\n"
"v_mfma_f32_16x16x32_bf16 v[172:175], v[216:219], a[248:251], v[172:175]\n"
"ds_read_b64_tr_b16 v[216:217], v230 offset:24576\n"
"v_xor_b32 v235, s75, v235\n"
"v_mfma_f32_16x16x32_bf16 v[172:175], v[212:215], a[244:247], v[172:175]\n"
"ds_read_b64_tr_b16 v[212:213], v234 offset:16384\n"
"v_lshlrev_b32 v202, 16, v84\n"
"v_mfma_f32_16x16x32_bf16 v[172:175], v[208:211], a[240:243], v[172:175]\n"
"ds_read_b64_tr_b16 v[208:209], v230 offset:16384\n"
"v_and_b32 v84, s73, v84\n"
"v_mfma_f32_16x16x32_bf16 v[172:175], v[192:195], a[204:207], v[172:175]\n"
"ds_read_b64_tr_b16 v[192:193], v234 offset:8192\n"
"v_add_f32 v25, v25, v84\n"
"v_mfma_f32_16x16x32_bf16 v[172:175], v[188:191], a[200:203], v[172:175]\n"
"ds_read_b64_tr_b16 v[188:189], v230 offset:8192\n"
"v_lshlrev_b32 v203, 16, v85\n"
"v_mfma_f32_16x16x32_bf16 v[172:175], v[184:187], a[196:199], v[172:175]\n"
"s_add_u32 s40, s41, s52\n"
"ds_read_b64_tr_b16 v[184:185], v234 offset:0\n"
"v_and_b32 v85, s73, v85\n"
"v_mfma_f32_16x16x32_bf16 v[172:175], v[180:183], a[192:195], v[172:175]\n"
"s_waitcnt vmcnt(13) lgkmcnt(7)\n"
"buffer_load_dwordx4 v240, s[28:31], s40 offen offset:1024 lds\n"
"v_mfma_f32_16x16x32_bf16 v[128:131], v[96:99], a[96:99], v[196:199]\n"
"s_waitcnt lgkmcnt(6)\n"
"ds_read_b64_tr_b16 v[180:181], v230 offset:0\n"
"v_add_f32 v27, v27, v85\n"
"v_mfma_f32_16x16x32_bf16 v[128:131], v[100:103], a[100:103], v[128:131]\n"
"ds_read_b64_tr_b16 v[182:183], v230 offset:2048\n"
"v_lshlrev_b32 v204, 16, v86\n"
"v_mfma_f32_16x16x32_bf16 v[128:131], v[104:107], a[104:107], v[128:131]\n"
"ds_read_b64_tr_b16 v[186:187], v234 offset:2048\n"
"v_and_b32 v86, s73, v86\n"
"v_mfma_f32_16x16x32_bf16 v[128:131], v[108:111], a[108:111], v[128:131]\n"
"ds_read_b64_tr_b16 v[190:191], v230 offset:10240\n"
"v_add_f32 v29, v29, v86\n"
"v_mfma_f32_16x16x32_bf16 v[128:131], v[112:115], a[144:147], v[128:131]\n"
"s_waitcnt vmcnt(6)\n"
"ds_read_b64_tr_b16 v[194:195], v234 offset:10240\n"
"v_lshlrev_b32 v205, 16, v87\n"
"v_mfma_f32_16x16x32_bf16 v[128:131], v[116:119], a[148:151], v[128:131]\n"
"v_subrev_u32 v200, s21, v200\n"
"ds_read_b64_tr_b16 v[210:211], v230 offset:18432\n"
"v_mfma_f32_16x16x32_bf16 v[128:131], v[120:123], a[152:155], v[128:131]\n"
"v_cmp_lt_u32 vcc, s69, v200\n"
"ds_read_b64_tr_b16 v[214:215], v234 offset:18432\n"
"v_mfma_f32_16x16x32_bf16 v[128:131], v[224:227], a[156:159], v[128:131]\n"
"ds_read_b64_tr_b16 v[218:219], v230 offset:26624\n"
"s_cbranch_vccnz LGP_%=\nLGPK_%=:\n"
"v_mfma_f32_16x16x32_bf16 v[144:147], v[224:227], a[172:175], v[196:199]\n"
"s_sub_u32 s72, s72, 1\n"
"ds_read_b64_tr_b16 v[222:223], v234 offset:26624\n"
"v_and_b32 v87, s73, v87\n"
"v_mfma_f32_16x16x32_bf16 v[144:147], v[120:123], a[168:171], v[144:147]\n"
"s_and_b32 s72, s72, 7\n"
"v_add_f32 v24, v24, v202\n"
"v_add_f32 v31, v31, v87\n"
"v_mfma_f32_16x16x32_bf16 v[144:147], v[116:119], a[164:167], v[144:147]\n"
"v_add_f32 v26, v26, v203\n"
"v_lshlrev_b32 v206, 16, v88\n"
"v_mfma_f32_16x16x32_bf16 v[144:147], v[112:115], a[160:163], v[144:147]\n"
"buffer_load_dwordx4 v[72:75], v243, s[32:35], s18 offen sc1\n"
"v_mfma_f32_16x16x32_bf16 v[144:147], v[108:111], a[124:127], v[144:147]\n"
"v_and_b32 v88, s73, v88\n"
"v_add_f32 v33, v33, v88\n"
"v_mfma_f32_16x16x32_bf16 v[144:147], v[104:107], a[120:123], v[144:147]\n"
"v_lshlrev_b32 v207, 16, v89\n"
"v_and_b32 v89, s73, v89\n"
"v_mfma_f32_16x16x32_bf16 v[144:147], v[100:103], a[116:119], v[144:147]\n"
"v_add_f32 v35, v35, v89\n"
"v_lshlrev_b32 v202, 16, v90\n"
"v_mfma_f32_16x16x32_bf16 v[144:147], v[96:99], a[112:115], v[144:147]\n"
"buffer_store_dwordx4 v[148:151], v243, s[32:35], s16 offen offset:3072 sc0 sc1\n"
"v_and_b32 v90, s73, v90\n"
"v_mfma_f32_16x16x32_bf16 v[160:163], v[96:99], a[128:131], v[196:199]\n"
"v_add_f32 v37, v37, v90\n"
"v_lshlrev_b32 v203, 16, v91\n"
"v_mfma_f32_16x16x32_bf16 v[160:163], v[100:103], a[132:135], v[160:163]\n"
"v_and_b32 v91, s73, v91\n"
"v_add_f32 v39, v39, v91\n"
"v_mfma_f32_16x16x32_bf16 v[160:163], v[104:107], a[136:139], v[160:163]\n"
"v_add_f32 v28, v28, v204\n"
"v_add_f32 v30, v30, v205\n"
"v_mfma_f32_16x16x32_bf16 v[160:163], v[108:111], a[140:143], v[160:163]\n"
"v_add_f32 v32, v32, v206\n"
"v_add_f32 v34, v34, v207\n"
"v_mfma_f32_16x16x32_bf16 v[160:163], v[112:115], a[176:179], v[160:163]\n"
"v_lshlrev_b32 v204, 16, v92\n"
"v_and_b32 v92, s73, v92\n"
"v_mfma_f32_16x16x32_bf16 v[160:163], v[116:119], a[180:183], v[160:163]\n"
"v_add_f32 v41, v41, v92\n"
"v_lshlrev_b32 v205, 16, v93\n"
"v_mfma_f32_16x16x32_bf16 v[160:163], v[120:123], a[184:187], v[160:163]\n"
"s_add_u32 s40, s41, s53\n"
"v_and_b32 v93, s73, v93\n"
"v_add_f32 v43, v43, v93\n"
"v_mfma_f32_16x16x32_bf16 v[160:163], v[224:227], a[188:191], v[160:163]\n"
"buffer_load_dwordx4 v240, s[28:31], s40 offen offset:2048 lds\n"
"v_mfma_f32_16x16x32_bf16 v[176:179], v[224:227], a[252:255], v[196:199]\n"
"ds_read_b64_tr_b16 v[224:225], v235 offset:24576\n"
"v_lshlrev_b32 v206, 16, v94\n"
"v_mfma_f32_16x16x32_bf16 v[176:179], v[120:123], a[248:251], v[176:179]\n"
"ds_read_b64_tr_b16 v[120:121], v231 offset:24576\n"
"v_and_b32 v94, s73, v94\n"
"v_mfma_f32_16x16x32_bf16 v[176:179], v[116:119], a[244:247], v[176:179]\n"
"ds_read_b64_tr_b16 v[116:117], v235 offset:16384\n"
"v_add_f32 v45, v45, v94\n"
"v_mfma_f32_16x16x32_bf16 v[176:179], v[112:115], a[240:243], v[176:179]\n"
"buffer_load_dwordx4 v[76:79], v243, s[32:35], s18 offen offset:1024 sc1\n"
"v_mfma_f32_16x16x32_bf16 v[176:179], v[108:111], a[204:207], v[176:179]\n"
"ds_read_b64_tr_b16 v[108:109], v235 offset:8192\n"
"v_lshlrev_b32 v207, 16, v95\n"
"v_mfma_f32_16x16x32_bf16 v[176:179], v[104:107], a[200:203], v[176:179]\n"
"ds_read_b64_tr_b16 v[104:105], v231 offset:8192\n"
"v_and_b32 v95, s73, v95\n"
"v_mfma_f32_16x16x32_bf16 v[176:179], v[100:103], a[196:199], v[176:179]\n"
"s_add_u32 s40, s41, s54\n"
"ds_read_b64_tr_b16 v[100:101], v235 offset:0\n"
"v_add_f32 v47, v47, v95\n"
"v_mfma_f32_16x16x32_bf16 v[176:179], v[96:99], a[192:195], v[176:179]\n"
"s_waitcnt lgkmcnt(6)\n"
"buffer_load_dwordx4 v240, s[28:31], s40 offen offset:3072 lds\n"
"v_mfma_f32_16x16x32_bf16 v[132:135], v[180:183], a[96:99], v[196:199]\n"
"ds_read_b64_tr_b16 v[96:97], v231 offset:0\n"
"v_add_f32 v36, v36, v202\n"
"v_mfma_f32_16x16x32_bf16 v[132:135], v[184:187], a[100:103], v[132:135]\n"
"ds_read_b64_tr_b16 v[98:99], v231 offset:2048\n"
"v_add_f32 v38, v38, v203\n"
"v_mfma_f32_16x16x32_bf16 v[132:135], v[188:191], a[104:107], v[132:135]\n"
"ds_read_b64_tr_b16 v[102:103], v235 offset:2048\n"
"v_add_f32 v44, v44, v206\n"
"v_mfma_f32_16x16x32_bf16 v[132:135], v[192:195], a[108:111], v[132:135]\n"
"ds_read_b64_tr_b16 v[106:107], v231 offset:10240\n"
"v_add_f32 v46, v46, v207\n"
"v_mfma_f32_16x16x32_bf16 v[132:135], v[208:211], a[144:147], v[132:135]\n"
"ds_read_b64_tr_b16 v[110:111], v235 offset:10240\n"
"v_add_f32 v40, v40, v204\n"
"v_mfma_f32_16x16x32_bf16 v[132:135], v[212:215], a[148:151], v[132:135]\n"
"ds_read_b64_tr_b16 v[112:113], v231 offset:16384\n"
"v_add_f32 v42, v42, v205\n"
"v_mfma_f32_16x16x32_bf16 v[132:135], v[216:219], a[152:155], v[132:135]\n"
"ds_read_b64_tr_b16 v[114:115], v231 offset:18432\n"
"v_mfma_f32_16x16x32_bf16 v[132:135], v[220:223], a[156:159], v[132:135]\n"
"buffer_store_dwordx4 v[164:167], v243, s[32:35], s17 offen offset:1024 sc0 sc1\n"
"v_lshlrev_b32 v206, 16, v244\n"
"v_mfma_f32_16x16x32_bf16 v[148:151], v[220:223], a[172:175], v[196:199]\n"
"ds_read_b64_tr_b16 v[118:119], v235 offset:18432\n"
"v_and_b32 v244, s73, v244\n"
"v_mfma_f32_16x16x32_bf16 v[148:151], v[216:219], a[168:171], v[148:151]\n"
"ds_read_b64_tr_b16 v[122:123], v231 offset:26624\n"
"v_add_f32 v49, v49, v244\n"
"v_mfma_f32_16x16x32_bf16 v[148:151], v[212:215], a[164:167], v[148:151]\n"
"ds_read_b64_tr_b16 v[226:227], v235 offset:26624\n"
"v_lshlrev_b32 v207, 16, v245\n"
"v_mfma_f32_16x16x32_bf16 v[148:151], v[208:211], a[160:163], v[148:151]\n"
"buffer_load_dwordx4 v[80:83], v243, s[32:35], s18 offen offset:2048 sc1\n"
"v_mfma_f32_16x16x32_bf16 v[148:151], v[192:195], a[124:127], v[148:151]\n"
"v_and_b32 v245, s73, v245\n"
"v_add_f32 v51, v51, v245\n"
"v_mfma_f32_16x16x32_bf16 v[148:151], v[188:191], a[120:123], v[148:151]\n"
"s_add_u32 s41, s70, 163840\n"
"v_lshlrev_b32 v202, 16, v246\n"
"v_and_b32 v246, s73, v246\n"
"v_mfma_f32_16x16x32_bf16 v[148:151], v[184:187], a[116:119], v[148:151]\n"
"s_add_u32 m0, s71, 16384\n"
"v_add_f32 v53, v53, v246\n"
"v_lshlrev_b32 v203, 16, v247\n"
"v_mfma_f32_16x16x32_bf16 v[148:151], v[180:183], a[112:115], v[148:151]\n"
"buffer_load_dwordx4 v240, s[28:31], s41 offen lds\n"
"v_mfma_f32_16x16x32_bf16 v[164:167], v[180:183], a[128:131], v[196:199]\n"
"v_and_b32 v247, s73, v247\n"
"v_add_f32 v55, v55, v247\n"
"v_mfma_f32_16x16x32_bf16 v[164:167], v[184:187], a[132:135], v[164:167]\n"
"v_add_f32 v48, v48, v206\n"
"v_add_f32 v50, v50, v207\n"
"v_mfma_f32_16x16x32_bf16 v[164:167], v[188:191], a[136:139], v[164:167]\n"
"v_add_f32 v52, v52, v202\n"
"v_add_f32 v54, v54, v203\n"
"v_mfma_f32_16x16x32_bf16 v[164:167], v[192:195], a[140:143], v[164:167]\n"
"v_mfma_f32_16x16x32_bf16 v[164:167], v[208:211], a[176:179], v[164:167]\n"
"v_mfma_f32_16x16x32_bf16 v[164:167], v[212:215], a[180:183], v[164:167]\n"
"v_mfma_f32_16x16x32_bf16 v[164:167], v[216:219], a[184:187], v[164:167]\n"
"s_waitcnt vmcnt(12)\n"
"v_mfma_f32_16x16x32_bf16 v[164:167], v[220:223], a[188:191], v[164:167]\n"
"s_barrier\n"
"s_waitcnt lgkmcnt(0)\n"
"v_mfma_f32_16x16x32_bf16 v[136:139], v[96:99], a[96:99], v[196:199]\n"
"ds_read_b64_tr_b16 v[180:181], v228 offset:32768\n"
"v_mfma_f32_16x16x32_bf16 v[136:139], v[100:103], a[100:103], v[136:139]\n"
"ds_read_b64_tr_b16 v[182:183], v228 offset:34816\n"
"v_mfma_f32_16x16x32_bf16 v[136:139], v[104:107], a[104:107], v[136:139]\n"
"ds_read_b64_tr_b16 v[184:185], v232 offset:32768\n"
"v_mfma_f32_16x16x32_bf16 v[136:139], v[108:111], a[108:111], v[136:139]\n"
"ds_read_b64_tr_b16 v[186:187], v232 offset:34816\n"
"v_mfma_f32_16x16x32_bf16 v[136:139], v[112:115], a[144:147], v[136:139]\n"
"ds_read_b64_tr_b16 v[188:189], v228 offset:40960\n"
"v_mfma_f32_16x16x32_bf16 v[136:139], v[116:119], a[148:151], v[136:139]\n"
"ds_read_b64_tr_b16 v[190:191], v228 offset:43008\n"
"v_mfma_f32_16x16x32_bf16 v[136:139], v[120:123], a[152:155], v[136:139]\n"
"s_add_u32 s40, s41, s52\n"
"ds_read_b64_tr_b16 v[192:193], v232 offset:40960\n"
"v_mfma_f32_16x16x32_bf16 v[136:139], v[224:227], a[156:159], v[136:139]\n"
"buffer_load_dwordx4 v240, s[28:31], s40 offen offset:1024 lds\n"
"v_mfma_f32_16x16x32_bf16 v[152:155], v[224:227], a[172:175], v[196:199]\n"
"ds_read_b64_tr_b16 v[194:195], v232 offset:43008\n"
"v_mfma_f32_16x16x32_bf16 v[152:155], v[120:123], a[168:171], v[152:155]\n"
"ds_read_b64_tr_b16 v[208:209], v228 offset:49152\n"
"v_mfma_f32_16x16x32_bf16 v[152:155], v[116:119], a[164:167], v[152:155]\n"
"ds_read_b64_tr_b16 v[210:211], v228 offset:51200\n"
"v_mfma_f32_16x16x32_bf16 v[152:155], v[112:115], a[160:163], v[152:155]\n"
"ds_read_b64_tr_b16 v[212:213], v232 offset:49152\n"
"v_mfma_f32_16x16x32_bf16 v[152:155], v[108:111], a[124:127], v[152:155]\n"
"ds_read_b64_tr_b16 v[214:215], v232 offset:51200\n"
"v_mfma_f32_16x16x32_bf16 v[152:155], v[104:107], a[120:123], v[152:155]\n"
"ds_read_b64_tr_b16 v[216:217], v228 offset:57344\n"
"v_mfma_f32_16x16x32_bf16 v[152:155], v[100:103], a[116:119], v[152:155]\n"
"s_add_u32 s40, s41, s53\n"
"ds_read_b64_tr_b16 v[218:219], v228 offset:59392\n"
"v_xor_b32 v228, s75, v228\n"
"v_mfma_f32_16x16x32_bf16 v[152:155], v[96:99], a[112:115], v[152:155]\n"
"buffer_load_dwordx4 v240, s[28:31], s40 offen offset:2048 lds\n"
"v_mfma_f32_16x16x32_bf16 v[168:171], v[96:99], a[128:131], v[196:199]\n"
"ds_read_b64_tr_b16 v[220:221], v232 offset:57344\n"
"v_mfma_f32_16x16x32_bf16 v[168:171], v[100:103], a[132:135], v[168:171]\n"
"ds_read_b64_tr_b16 v[222:223], v232 offset:59392\n"
"v_xor_b32 v232, s75, v232\n"
"v_mfma_f32_16x16x32_bf16 v[168:171], v[104:107], a[136:139], v[168:171]\n"
"ds_read_b64_tr_b16 v[96:97], v229 offset:32768\n"
"v_mfma_f32_16x16x32_bf16 v[168:171], v[108:111], a[140:143], v[168:171]\n"
"ds_read_b64_tr_b16 v[98:99], v229 offset:34816\n"
"v_mfma_f32_16x16x32_bf16 v[168:171], v[112:115], a[176:179], v[168:171]\n"
"ds_read_b64_tr_b16 v[100:101], v233 offset:32768\n"
"v_mfma_f32_16x16x32_bf16 v[168:171], v[116:119], a[180:183], v[168:171]\n"
"ds_read_b64_tr_b16 v[102:103], v233 offset:34816\n"
"v_mfma_f32_16x16x32_bf16 v[168:171], v[120:123], a[184:187], v[168:171]\n"
"s_add_u32 s40, s41, s54\n"
"ds_read_b64_tr_b16 v[104:105], v229 offset:40960\n"
"v_mfma_f32_16x16x32_bf16 v[168:171], v[224:227], a[188:191], v[168:171]\n"
"s_waitcnt lgkmcnt(7)\n"
"buffer_load_dwordx4 v240, s[28:31], s40 offen offset:3072 lds\n"
"v_mfma_f32_16x16x32_bf16 v[124:127], v[180:183], a[48:51], v[124:127]\n"
"ds_read_b64_tr_b16 v[106:107], v229 offset:43008\n"
"v_mfma_f32_16x16x32_bf16 v[124:127], v[184:187], a[52:55], v[124:127]\n"
"ds_read_b64_tr_b16 v[108:109], v233 offset:40960\n"
"v_mfma_f32_16x16x32_bf16 v[124:127], v[188:191], a[56:59], v[124:127]\n"
"ds_read_b64_tr_b16 v[110:111], v233 offset:43008\n"
"v_mfma_f32_16x16x32_bf16 v[124:127], v[192:195], a[60:63], v[124:127]\n"
"ds_read_b64_tr_b16 v[112:113], v229 offset:49152\n"
"v_mfma_f32_16x16x32_bf16 v[124:127], v[208:211], a[0:3], v[124:127]\n"
"ds_read_b64_tr_b16 v[114:115], v229 offset:51200\n"
"v_mfma_f32_16x16x32_bf16 v[124:127], v[212:215], a[4:7], v[124:127]\n"
"s_add_u32 s41, s70, 327680\n"
"ds_read_b64_tr_b16 v[116:117], v233 offset:49152\n"
"v_mfma_f32_16x16x32_bf16 v[124:127], v[216:219], a[8:11], v[124:127]\n"
"s_waitcnt lgkmcnt(6)\n"
"s_add_u32 m0, s71, 32768\n"
"ds_read_b64_tr_b16 v[118:119], v233 offset:51200\n"
"v_mfma_f32_16x16x32_bf16 v[124:127], v[220:223], a[12:15], v[124:127]\n"
"buffer_load_dwordx4 v240, s[28:31], s41 offen lds\n"
"v_mfma_f32_16x16x32_bf16 v[140:143], v[220:223], a[28:31], v[140:143]\n"
"ds_read_b64_tr_b16 v[120:121], v229 offset:57344\n"
"v_mfma_f32_16x16x32_bf16 v[140:143], v[216:219], a[24:27], v[140:143]\n"
"ds_read_b64_tr_b16 v[122:123], v229 offset:59392\n"
"v_xor_b32 v229, s75, v229\n"
"v_mfma_f32_16x16x32_bf16 v[140:143], v[212:215], a[20:23], v[140:143]\n"
"ds_read_b64_tr_b16 v[224:225], v233 offset:57344\n"
"v_mfma_f32_16x16x32_bf16 v[140:143], v[208:211], a[16:19], v[140:143]\n"
"ds_read_b64_tr_b16 v[226:227], v233 offset:59392\n"
"v_xor_b32 v233, s75, v233\n"
"v_mfma_f32_16x16x32_bf16 v[140:143], v[192:195], a[76:79], v[140:143]\n"
"v_mfma_f32_16x16x32_bf16 v[140:143], v[188:191], a[72:75], v[140:143]\n"
"v_subrev_f32 v124, s22, v124\n"
"v_subrev_f32 v125, s22, v125\n"
"v_mfma_f32_16x16x32_bf16 v[140:143], v[184:187], a[68:71], v[140:143]\n"
"s_add_u32 s40, s41, s52\n"
"v_subrev_f32 v126, s22, v126\n"
"v_subrev_f32 v127, s22, v127\n"
"v_mfma_f32_16x16x32_bf16 v[140:143], v[180:183], a[64:67], v[140:143]\n"
"buffer_load_dwordx4 v240, s[28:31], s40 offen offset:1024 lds\n"
"v_mfma_f32_16x16x32_bf16 v[156:159], v[180:183], a[80:83], v[156:159]\n"
"v_cvt_pk_bf16_f32 v124, v124, v125\n"
"v_cvt_pk_bf16_f32 v125, v126, v127\n"
"v_mfma_f32_16x16x32_bf16 v[156:159], v[184:187], a[84:87], v[156:159]\n"
"v_mfma_f32_16x16x32_bf16 v[156:159], v[188:191], a[88:91], v[156:159]\n"
"v_mfma_f32_16x16x32_bf16 v[156:159], v[192:195], a[92:95], v[156:159]\n"
"v_mfma_f32_16x16x32_bf16 v[156:159], v[208:211], a[32:35], v[156:159]\n"
"v_mfma_f32_16x16x32_bf16 v[156:159], v[212:215], a[36:39], v[156:159]\n"
"v_subrev_f32 v140, s22, v140\n"
"v_subrev_f32 v141, s22, v141\n"
"v_mfma_f32_16x16x32_bf16 v[156:159], v[216:219], a[40:43], v[156:159]\n"
"s_waitcnt vmcnt(7)\n"
"v_subrev_f32 v142, s22, v142\n"
"v_subrev_f32 v143, s22, v143\n"
"v_mfma_f32_16x16x32_bf16 v[156:159], v[220:223], a[44:47], v[156:159]\n"
"s_barrier\n"
"buffer_store_dword v241, v248, s[84:87], 0 offen sc1\n"
"v_mfma_f32_16x16x32_bf16 v[172:175], v[220:223], a[228:231], v[172:175]\n"
"ds_read_b64_tr_b16 v[220:221], v234 offset:57344\n"
"v_cvt_pk_bf16_f32 v140, v140, v141\n"
"v_mfma_f32_16x16x32_bf16 v[172:175], v[216:219], a[224:227], v[172:175]\n"
"ds_read_b64_tr_b16 v[216:217], v230 offset:57344\n"
"v_cvt_pk_bf16_f32 v141, v142, v143\n"
"v_mfma_f32_16x16x32_bf16 v[172:175], v[212:215], a[212:215], v[172:175]\n"
"ds_read_b64_tr_b16 v[212:213], v234 offset:49152\n"
"v_add_u32 v241, 1, v241\n"
"v_mfma_f32_16x16x32_bf16 v[172:175], v[208:211], a[208:211], v[172:175]\n"
"s_add_u32 s16, s90, s62\n"
"buffer_load_dwordx4 v[84:87], v243, s[32:35], s18 offen offset:3072 sc1\n"
"v_mfma_f32_16x16x32_bf16 v[172:175], v[192:195], a[236:239], v[172:175]\n"
"s_add_u32 s17, s16, 4096\n"
"ds_read_b64_tr_b16 v[192:193], v234 offset:40960\n"
"v_mfma_f32_16x16x32_bf16 v[172:175], v[188:191], a[232:235], v[172:175]\n"
"s_lshl_b32 s62, s70, 9\n"
"ds_read_b64_tr_b16 v[188:189], v230 offset:40960\n"
"v_subrev_f32 v156, s22, v156\n"
"v_mfma_f32_16x16x32_bf16 v[172:175], v[184:187], a[220:223], v[172:175]\n"
"s_add_u32 s40, s41, s53\n"
"ds_read_b64_tr_b16 v[184:185], v234 offset:32768\n"
"v_subrev_f32 v157, s22, v157\n"
"v_mfma_f32_16x16x32_bf16 v[172:175], v[180:183], a[216:219], v[172:175]\n"
"buffer_load_dwordx4 v240, s[28:31], s40 offen offset:2048 lds\n"
"v_mfma_f32_16x16x32_bf16 v[128:131], v[96:99], a[48:51], v[128:131]\n"
"ds_read_b64_tr_b16 v[180:181], v230 offset:32768\n"
"v_subrev_f32 v158, s22, v158\n"
"v_mfma_f32_16x16x32_bf16 v[128:131], v[100:103], a[52:55], v[128:131]\n"
"s_waitcnt lgkmcnt(5)\n"
"ds_read_b64_tr_b16 v[182:183], v230 offset:34816\n"
"v_subrev_f32 v159, s22, v159\n"
"v_mfma_f32_16x16x32_bf16 v[128:131], v[104:107], a[56:59], v[128:131]\n"
"ds_read_b64_tr_b16 v[186:187], v234 offset:34816\n"
"v_cvt_pk_bf16_f32 v156, v156, v157\n"
"v_mfma_f32_16x16x32_bf16 v[128:131], v[108:111], a[60:63], v[128:131]\n"
"ds_read_b64_tr_b16 v[190:191], v230 offset:43008\n"
"v_cvt_pk_bf16_f32 v157, v158, v159\n"
"v_mfma_f32_16x16x32_bf16 v[128:131], v[112:115], a[0:3], v[128:131]\n"
"ds_read_b64_tr_b16 v[194:195], v234 offset:43008\n"
"v_lshlrev_b32 v202, 16, v72\n"
"v_mfma_f32_16x16x32_bf16 v[128:131], v[116:119], a[4:7], v[128:131]\n"
"ds_read_b64_tr_b16 v[208:209], v230 offset:49152\n"
"v_and_b32 v72, s73, v72\n"
"v_mfma_f32_16x16x32_bf16 v[128:131], v[120:123], a[8:11], v[128:131]\n"
"s_add_u32 s40, s41, s54\n"
"ds_read_b64_tr_b16 v[210:211], v230 offset:51200\n"
"v_add_f32 v0, v0, v202\n"
"v_mfma_f32_16x16x32_bf16 v[128:131], v[224:227], a[12:15], v[128:131]\n"
"buffer_load_dwordx4 v240, s[28:31], s40 offen offset:3072 lds\n"
"v_mfma_f32_16x16x32_bf16 v[144:147], v[224:227], a[28:31], v[144:147]\n"
"ds_read_b64_tr_b16 v[214:215], v234 offset:51200\n"
"v_add_f32 v1, v1, v72\n"
"v_mfma_f32_16x16x32_bf16 v[144:147], v[120:123], a[24:27], v[144:147]\n"
"ds_read_b64_tr_b16 v[218:219], v230 offset:59392\n"
"v_lshlrev_b32 v203, 16, v73\n"
"v_mfma_f32_16x16x32_bf16 v[144:147], v[116:119], a[20:23], v[144:147]\n"
"ds_read_b64_tr_b16 v[222:223], v234 offset:59392\n"
"v_and_b32 v73, s73, v73\n"
"v_mfma_f32_16x16x32_bf16 v[144:147], v[112:115], a[16:19], v[144:147]\n"
"buffer_load_dwordx4 v[88:91], v243, s[32:35], s19 offen sc1\n"
"v_mfma_f32_16x16x32_bf16 v[144:147], v[108:111], a[76:79], v[144:147]\n"
"v_add_f32 v2, v2, v203\n"
"v_add_f32 v3, v3, v73\n"
"v_mfma_f32_16x16x32_bf16 v[144:147], v[104:107], a[72:75], v[144:147]\n"
"s_add_u32 s41, s70, 491520\n"
"v_subrev_f32 v128, s22, v128\n"
"v_subrev_f32 v129, s22, v129\n"
"v_mfma_f32_16x16x32_bf16 v[144:147], v[100:103], a[68:71], v[144:147]\n"
"s_add_u32 m0, s71, 49152\n"
"v_subrev_f32 v130, s22, v130\n"
"v_subrev_f32 v131, s22, v131\n"
"v_mfma_f32_16x16x32_bf16 v[144:147], v[96:99], a[64:67], v[144:147]\n"
"buffer_load_dwordx4 v240, s[28:31], s41 offen lds\n"
"v_mfma_f32_16x16x32_bf16 v[160:163], v[96:99], a[80:83], v[160:163]\n"
"s_and_b32 s70, s88, 15\n"
"v_cvt_pk_bf16_f32 v126, v128, v129\n"
"v_cvt_pk_bf16_f32 v127, v130, v131\n"
"v_mfma_f32_16x16x32_bf16 v[160:163], v[100:103], a[84:87], v[160:163]\n"
"s_lshr_b32 s88, s88, 4\n"
"v_lshlrev_b32 v204, 16, v74\n"
"v_and_b32 v74, s73, v74\n"
"v_mfma_f32_16x16x32_bf16 v[160:163], v[104:107], a[88:91], v[160:163]\n"
"s_lshl_b32 s70, s70, 7\n"
"v_add_f32 v4, v4, v204\n"
"v_add_f32 v5, v5, v74\n"
"v_mfma_f32_16x16x32_bf16 v[160:163], v[108:111], a[92:95], v[160:163]\n"
"s_xor_b32 s71, s71, s75\n"
"v_lshlrev_b32 v205, 16, v75\n"
"v_and_b32 v75, s73, v75\n"
"v_mfma_f32_16x16x32_bf16 v[160:163], v[112:115], a[32:35], v[160:163]\n"
"v_add_f32 v6, v6, v205\n"
"v_add_f32 v7, v7, v75\n"
"v_mfma_f32_16x16x32_bf16 v[160:163], v[116:119], a[36:39], v[160:163]\n"
"v_subrev_f32 v144, s22, v144\n"
"v_subrev_f32 v145, s22, v145\n"
"v_mfma_f32_16x16x32_bf16 v[160:163], v[120:123], a[40:43], v[160:163]\n"
"v_subrev_f32 v146, s22, v146\n"
"v_subrev_f32 v147, s22, v147\n"
"v_mfma_f32_16x16x32_bf16 v[160:163], v[224:227], a[44:47], v[160:163]\n"
"buffer_store_dwordx4 v[124:127], v243, s[32:35], s16 offen sc0 sc1\n"
"v_cvt_pk_bf16_f32 v142, v144, v145\n"
"v_mfma_f32_16x16x32_bf16 v[176:179], v[224:227], a[228:231], v[176:179]\n"
"ds_read_b64_tr_b16 v[224:225], v235 offset:57344\n"
"v_cvt_pk_bf16_f32 v143, v146, v147\n"
"v_mfma_f32_16x16x32_bf16 v[176:179], v[120:123], a[224:227], v[176:179]\n"
"ds_read_b64_tr_b16 v[120:121], v231 offset:57344\n"
"v_lshlrev_b32 v206, 16, v76\n"
"v_mfma_f32_16x16x32_bf16 v[176:179], v[116:119], a[212:215], v[176:179]\n"
"ds_read_b64_tr_b16 v[116:117], v235 offset:49152\n"
"v_and_b32 v76, s73, v76\n"
"v_mfma_f32_16x16x32_bf16 v[176:179], v[112:115], a[208:211], v[176:179]\n"
"buffer_load_dwordx4 v[92:95], v243, s[32:35], s19 offen offset:1024 sc1\n"
"v_mfma_f32_16x16x32_bf16 v[176:179], v[108:111], a[236:239], v[176:179]\n"
"ds_read_b64_tr_b16 v[108:109], v235 offset:40960\n"
"v_add_f32 v8, v8, v206\n"
"v_mfma_f32_16x16x32_bf16 v[176:179], v[104:107], a[232:235], v[176:179]\n"
"ds_read_b64_tr_b16 v[104:105], v231 offset:40960\n"
"v_subrev_f32 v160, s22, v160\n"
"v_mfma_f32_16x16x32_bf16 v[176:179], v[100:103], a[220:223], v[176:179]\n"
"s_add_u32 s40, s41, s52\n"
"ds_read_b64_tr_b16 v[100:101], v235 offset:32768\n"
"v_subrev_f32 v161, s22, v161\n"
"v_mfma_f32_16x16x32_bf16 v[176:179], v[96:99], a[216:219], v[176:179]\n"
"s_waitcnt vmcnt(14) lgkmcnt(6)\n"
"buffer_load_dwordx4 v240, s[28:31], s40 offen offset:1024 lds\n"
"v_mfma_f32_16x16x32_bf16 v[132:135], v[180:183], a[48:51], v[132:135]\n"
"ds_read_b64_tr_b16 v[96:97], v231 offset:32768\n"
"v_subrev_f32 v162, s22, v162\n"
"v_mfma_f32_16x16x32_bf16 v[132:135], v[184:187], a[52:55], v[132:135]\n"
"ds_read_b64_tr_b16 v[98:99], v231 offset:34816\n"
"v_subrev_f32 v163, s22, v163\n"
"v_mfma_f32_16x16x32_bf16 v[132:135], v[188:191], a[56:59], v[132:135]\n"
"ds_read_b64_tr_b16 v[102:103], v235 offset:34816\n"
"v_cvt_pk_bf16_f32 v158, v160, v161\n"
"v_mfma_f32_16x16x32_bf16 v[132:135], v[192:195], a[60:63], v[132:135]\n"
"ds_read_b64_tr_b16 v[106:107], v231 offset:43008\n"
"v_cvt_pk_bf16_f32 v159, v162, v163\n"
"v_mfma_f32_16x16x32_bf16 v[132:135], v[208:211], a[0:3], v[132:135]\n"
"ds_read_b64_tr_b16 v[110:111], v235 offset:43008\n"
"v_add_f32 v9, v9, v76\n"
"v_mfma_f32_16x16x32_bf16 v[132:135], v[212:215], a[4:7], v[132:135]\n"
"ds_read_b64_tr_b16 v[112:113], v231 offset:49152\n"
"v_lshlrev_b32 v207, 16, v77\n"
"v_mfma_f32_16x16x32_bf16 v[132:135], v[216:219], a[8:11], v[132:135]\n"
"ds_read_b64_tr_b16 v[114:115], v231 offset:51200\n"
"v_and_b32 v77, s73, v77\n"
"v_mfma_f32_16x16x32_bf16 v[132:135], v[220:223], a[12:15], v[132:135]\n"
"buffer_store_dwordx4 v[140:143], v243, s[32:35], s16 offen offset:2048 sc0 sc1\n"
"v_add_f32 v10, v10, v207\n"
"v_mfma_f32_16x16x32_bf16 v[148:151], v[220:223], a[28:31], v[148:151]\n"
"ds_read_b64_tr_b16 v[118:119], v235 offset:51200\n"
"v_add_f32 v11, v11, v77\n"
"v_mfma_f32_16x16x32_bf16 v[148:151], v[216:219], a[24:27], v[148:151]\n"
"ds_read_b64_tr_b16 v[122:123], v231 offset:59392\n"
"v_lshlrev_b32 v202, 16, v78\n"
"v_mfma_f32_16x16x32_bf16 v[148:151], v[212:215], a[20:23], v[148:151]\n"
"ds_read_b64_tr_b16 v[226:227], v235 offset:59392\n"
"v_and_b32 v78, s73, v78\n"
"v_mfma_f32_16x16x32_bf16 v[148:151], v[208:211], a[16:19], v[148:151]\n"
"v_add_f32 v12, v12, v202\n"
"v_add_f32 v13, v13, v78\n"
"v_mfma_f32_16x16x32_bf16 v[148:151], v[192:195], a[76:79], v[148:151]\n"
"v_lshlrev_b32 v203, 16, v79\n"
"v_and_b32 v79, s73, v79\n"
"v_mfma_f32_16x16x32_bf16 v[148:151], v[188:191], a[72:75], v[148:151]\n"
"v_add_f32 v14, v14, v203\n"
"v_add_f32 v15, v15, v79\n"
"v_mfma_f32_16x16x32_bf16 v[148:151], v[184:187], a[68:71], v[148:151]\n"
"s_add_u32 s40, s41, s53\n"
"v_lshlrev_b32 v204, 16, v80\n"
"v_and_b32 v80, s73, v80\n"
"v_mfma_f32_16x16x32_bf16 v[148:151], v[180:183], a[64:67], v[148:151]\n"
"buffer_load_dwordx4 v240, s[28:31], s40 offen offset:2048 lds\n"
"v_mfma_f32_16x16x32_bf16 v[164:167], v[180:183], a[80:83], v[164:167]\n"
"v_add_f32 v16, v16, v204\n"
"v_add_f32 v17, v17, v80\n"
"v_mfma_f32_16x16x32_bf16 v[164:167], v[184:187], a[84:87], v[164:167]\n"
"v_lshlrev_b32 v205, 16, v81\n"
"v_and_b32 v81, s73, v81\n"
"v_mfma_f32_16x16x32_bf16 v[164:167], v[188:191], a[88:91], v[164:167]\n"
"v_add_f32 v18, v18, v205\n"
"v_add_f32 v19, v19, v81\n"
"v_mfma_f32_16x16x32_bf16 v[164:167], v[192:195], a[92:95], v[164:167]\n"
"buffer_load_dwordx4 v[244:247], v243, s[32:35], s19 offen offset:2048 sc1\n"
"v_mfma_f32_16x16x32_bf16 v[164:167], v[208:211], a[32:35], v[164:167]\n"
"s_lshl4_add_u32 s18, s72, s61\n"
"v_lshlrev_b32 v206, 16, v82\n"
"v_and_b32 v82, s73, v82\n"
"v_mfma_f32_16x16x32_bf16 v[164:167], v[212:215], a[36:39], v[164:167]\n"
"s_lshl_b32 s19, s72, 2\n"
"v_add_f32 v20, v20, v206\n"
"v_add_f32 v21, v21, v82\n"
"v_mfma_f32_16x16x32_bf16 v[164:167], v[216:219], a[40:43], v[164:167]\n"
"s_waitcnt vmcnt(14)\n"
"s_lshr_b32 s19, s89, s19\n"
"v_lshlrev_b32 v207, 16, v83\n"
"v_mfma_f32_16x16x32_bf16 v[164:167], v[220:223], a[44:47], v[164:167]\n"
"s_barrier\n"
"s_and_b32 s19, s19, 1\n"
"s_waitcnt lgkmcnt(0)\n"
"v_mfma_f32_16x16x32_bf16 v[136:139], v[96:99], a[48:51], v[136:139]\n"
"s_lshl3_add_u32 s18, s18, s19\n"
"ds_read_b64_tr_b16 v[180:181], v228 offset:0\n"
"v_and_b32 v83, s73, v83\n"
"v_mfma_f32_16x16x32_bf16 v[136:139], v[100:103], a[52:55], v[136:139]\n"
"s_lshl_b32 s18, s18, 13\n"
"ds_read_b64_tr_b16 v[182:183], v228 offset:2048\n"
"v_add_f32 v22, v22, v207\n"
"v_mfma_f32_16x16x32_bf16 v[136:139], v[104:107], a[56:59], v[136:139]\n"
"s_add_u32 s19, s18, 4096\n"
"v_lshlrev_b32_e64 v201, 2, s72\n"
"ds_read_b64_tr_b16 v[184:185], v232 offset:0\n"
"v_mfma_f32_16x16x32_bf16 v[136:139], v[108:111], a[60:63], v[136:139]\n"
"s_mov_b32 s69, 15\n"
"global_load_dword v200, v201, s[6:7] sc1\n"
"v_mfma_f32_16x16x32_bf16 v[136:139], v[112:115], a[0:3], v[136:139]\n"
"s_add_u32 s21, s21, 1\n"
"ds_read_b64_tr_b16 v[186:187], v232 offset:2048\n"
"v_add_f32 v23, v23, v83\n"
"v_mfma_f32_16x16x32_bf16 v[136:139], v[116:119], a[4:7], v[136:139]\n"
"ds_read_b64_tr_b16 v[188:189], v228 offset:8192\n"
"v_subrev_f32 v172, s22, v172\n"
"v_mfma_f32_16x16x32_bf16 v[136:139], v[120:123], a[8:11], v[136:139]\n"
"ds_read_b64_tr_b16 v[190:191], v228 offset:10240\n"
"v_subrev_f32 v173, s22, v173\n"
"v_mfma_f32_16x16x32_bf16 v[136:139], v[224:227], a[12:15], v[136:139]\n"
"buffer_store_dwordx4 v[156:159], v243, s[32:35], s17 offen sc0 sc1\n"
"v_subrev_f32 v174, s22, v174\n"
"v_mfma_f32_16x16x32_bf16 v[152:155], v[224:227], a[28:31], v[152:155]\n"
"ds_read_b64_tr_b16 v[192:193], v232 offset:8192\n"
"v_subrev_f32 v175, s22, v175\n"
"v_mfma_f32_16x16x32_bf16 v[152:155], v[120:123], a[24:27], v[152:155]\n"
"ds_read_b64_tr_b16 v[194:195], v232 offset:10240\n"
"v_cvt_pk_bf16_f32 v172, v172, v173\n"
"v_mfma_f32_16x16x32_bf16 v[152:155], v[116:119], a[20:23], v[152:155]\n"
"ds_read_b64_tr_b16 v[208:209], v228 offset:16384\n"
"v_cvt_pk_bf16_f32 v173, v174, v175\n"
"v_mfma_f32_16x16x32_bf16 v[152:155], v[112:115], a[16:19], v[152:155]\n"
"ds_read_b64_tr_b16 v[210:211], v228 offset:18432\n"
"v_subrev_f32 v176, s22, v176\n"
"v_mfma_f32_16x16x32_bf16 v[152:155], v[108:111], a[76:79], v[152:155]\n"
"ds_read_b64_tr_b16 v[212:213], v232 offset:16384\n"
"v_subrev_f32 v177, s22, v177\n"
"v_mfma_f32_16x16x32_bf16 v[152:155], v[104:107], a[72:75], v[152:155]\n"
"ds_read_b64_tr_b16 v[214:215], v232 offset:18432\n"
"v_subrev_f32 v178, s22, v178\n"
"v_mfma_f32_16x16x32_bf16 v[152:155], v[100:103], a[68:71], v[152:155]\n"
"s_add_u32 s40, s41, s54\n"
"ds_read_b64_tr_b16 v[216:217], v228 offset:24576\n"
"v_subrev_f32 v179, s22, v179\n"
"v_mfma_f32_16x16x32_bf16 v[152:155], v[96:99], a[64:67], v[152:155]\n"
"buffer_load_dwordx4 v240, s[28:31], s40 offen offset:3072 lds\n"
"v_mfma_f32_16x16x32_bf16 v[168:171], v[96:99], a[80:83], v[168:171]\n"
"ds_read_b64_tr_b16 v[218:219], v228 offset:26624\n"
"v_cvt_pk_bf16_f32 v174, v176, v177\n"
"v_mfma_f32_16x16x32_bf16 v[168:171], v[100:103], a[84:87], v[168:171]\n"
"ds_read_b64_tr_b16 v[220:221], v232 offset:24576\n"
"v_cvt_pk_bf16_f32 v175, v178, v179\n"
"v_mfma_f32_16x16x32_bf16 v[168:171], v[104:107], a[88:91], v[168:171]\n"
"ds_read_b64_tr_b16 v[222:223], v232 offset:26624\n"
"v_subrev_f32 v132, s22, v132\n"
"v_mfma_f32_16x16x32_bf16 v[168:171], v[108:111], a[92:95], v[168:171]\n"
"ds_read_b64_tr_b16 v[96:97], v229 offset:0\n"
"v_subrev_f32 v133, s22, v133\n"
"v_mfma_f32_16x16x32_bf16 v[168:171], v[112:115], a[32:35], v[168:171]\n"
"ds_read_b64_tr_b16 v[98:99], v229 offset:2048\n"
"v_subrev_f32 v134, s22, v134\n"
"v_mfma_f32_16x16x32_bf16 v[168:171], v[116:119], a[36:39], v[168:171]\n"
"ds_read_b64_tr_b16 v[100:101], v233 offset:0\n"
"v_subrev_f32 v135, s22, v135\n"
"v_mfma_f32_16x16x32_bf16 v[168:171], v[120:123], a[40:43], v[168:171]\n"
"s_add_u32 s47, s47, 1\n"
"ds_read_b64_tr_b16 v[102:103], v233 offset:2048\n"
"v_cvt_pk_bf16_f32 v132, v132, v133\n"
"v_mfma_f32_16x16x32_bf16 v[168:171], v[224:227], a[44:47], v[168:171]\n"
"s_waitcnt lgkmcnt(7)\n"
"s_cmp_lg_u32 s47, 9\n"
"ds_read_b64_tr_b16 v[104:105], v229 offset:8192\n"
"s_cbranch_scc1 LGL_%=\n"
"v_mfma_f32_16x16x32_bf16 v[0:3], v[180:183], a[96:99], v[0:3]\n"
"v_xor_b32 v235, s75, v235\n"
"v_subrev_f32 v136, s22, v136\n"
"v_mfma_f32_16x16x32_bf16 v[0:3], v[184:187], a[100:103], v[0:3]\n"
"ds_read_b64_tr_b16 v[114:115], v229 offset:18432\n"
"v_cvt_pk_bf16_f32 v133, v134, v135\n"
"v_mfma_f32_16x16x32_bf16 v[0:3], v[188:191], a[104:107], v[0:3]\n"
"v_mov_b32 v201, v237\n"
"v_subrev_f32 v138, s22, v138\n"
"v_subrev_f32 v139, s22, v139\n"
"v_mfma_f32_16x16x32_bf16 v[0:3], v[192:195], a[108:111], v[0:3]\n"
"global_load_dword v200, v201, s[6:7] sc1\n"
"v_mfma_f32_16x16x32_bf16 v[0:3], v[208:211], a[144:147], v[0:3]\n"
"v_subrev_f32 v167, s22, v167\n"
"v_cvt_pk_bf16_f32 v135, v138, v139\n"
"v_mfma_f32_16x16x32_bf16 v[0:3], v[212:215], a[148:151], v[0:3]\n"
"s_waitcnt lgkmcnt(3)\n"
"v_subrev_f32 v148, s22, v148\n"
"v_subrev_f32 v137, s22, v137\n"
"v_mfma_f32_16x16x32_bf16 v[0:3], v[216:219], a[152:155], v[0:3]\n"
"v_subrev_f32 v165, s22, v165\n"
"v_subrev_f32 v168, s22, v168\n"
"v_mfma_f32_16x16x32_bf16 v[0:3], v[220:223], a[156:159], v[0:3]\n"
"buffer_store_dwordx4 v[172:175], v243, s[32:35], s17 offen offset:2048 sc0 sc1\n"
"v_subrev_f32 v151, s22, v151\n"
"v_cvt_pk_bf16_f32 v134, v136, v137\n"
"v_mfma_f32_16x16x32_bf16 v[16:19], v[220:223], a[172:175], v[16:19]\n"
"buffer_store_dwordx4 v[132:135], v243, s[32:35], s16 offen offset:1024 sc0 sc1\n"
"v_subrev_f32 v149, s22, v149\n"
"v_subrev_f32 v150, s22, v150\n"
"v_subrev_f32 v164, s22, v164\n"
"v_cvt_pk_bf16_f32 v164, v164, v165\n"
"v_subrev_f32 v153, s22, v153\n"
"v_subrev_f32 v155, s22, v155\n"
"v_mfma_f32_16x16x32_bf16 v[16:19], v[216:219], a[168:171], v[16:19]\n"
"v_cvt_pk_bf16_f32 v148, v148, v149\n"
"v_subrev_f32 v154, s22, v154\n"
"v_mfma_f32_16x16x32_bf16 v[16:19], v[212:215], a[164:167], v[16:19]\n"
"v_cvt_pk_bf16_f32 v149, v150, v151\n"
"v_subrev_f32 v170, s22, v170\n"
"v_mfma_f32_16x16x32_bf16 v[16:19], v[208:211], a[160:163], v[16:19]\n"
"v_subrev_f32 v166, s22, v166\n"
"v_subrev_f32 v171, s22, v171\n"
"v_mfma_f32_16x16x32_bf16 v[16:19], v[192:195], a[124:127], v[16:19]\n"
"v_cvt_pk_bf16_f32 v165, v166, v167\n"
"v_cvt_pk_bf16_f32 v167, v170, v171\n"
"v_mfma_f32_16x16x32_bf16 v[16:19], v[188:191], a[120:123], v[16:19]\n"
"v_subrev_f32 v152, s22, v152\n"
"v_cvt_pk_bf16_f32 v151, v154, v155\n"
"v_mfma_f32_16x16x32_bf16 v[16:19], v[184:187], a[116:119], v[16:19]\n"
"v_cvt_pk_bf16_f32 v150, v152, v153\n"
"v_subrev_f32 v169, s22, v169\n"
"v_mfma_f32_16x16x32_bf16 v[16:19], v[180:183], a[112:115], v[16:19]\n"
"s_waitcnt vmcnt(16)\n"
"buffer_store_dwordx4 v[148:151], v243, s[32:35], s16 offen offset:3072 sc0 sc1\n"
"v_cvt_pk_bf16_f32 v166, v168, v169\n"
"v_mfma_f32_16x16x32_bf16 v[32:35], v[180:183], a[128:131], v[32:35]\n"
"buffer_store_dwordx4 v[164:167], v243, s[32:35], s17 offen offset:1024 sc0 sc1\n"
"ds_read_b64_tr_b16 v[120:121], v229 offset:24576\n"
"v_mfma_f32_16x16x32_bf16 v[32:35], v[184:187], a[132:135], v[32:35]\n"
"ds_read_b64_tr_b16 v[224:225], v233 offset:24576\n"
"v_xor_b32 v234, s75, v234\n"
"v_mfma_f32_16x16x32_bf16 v[32:35], v[188:191], a[136:139], v[32:35]\n"
"ds_read_b64_tr_b16 v[122:123], v229 offset:26624\n"
"v_lshlrev_b32 v203, 16, v85\n"
"v_mfma_f32_16x16x32_bf16 v[32:35], v[192:195], a[140:143], v[32:35]\n"
"ds_read_b64_tr_b16 v[108:109], v233 offset:8192\n"
"v_lshlrev_b32 v202, 16, v84\n"
"v_mfma_f32_16x16x32_bf16 v[32:35], v[208:211], a[176:179], v[32:35]\n"
"ds_read_b64_tr_b16 v[226:227], v233 offset:26624\n"
"v_and_b32 v84, s73, v84\n"
"v_mfma_f32_16x16x32_bf16 v[32:35], v[212:215], a[180:183], v[32:35]\n"
"ds_read_b64_tr_b16 v[110:111], v233 offset:10240\n"
"v_bfe_u32 v161, v242, 8, 2\n"
"v_mfma_f32_16x16x32_bf16 v[32:35], v[216:219], a[184:187], v[32:35]\n"
"ds_read_b64_tr_b16 v[106:107], v229 offset:10240\n"
"v_lshlrev_b32 v205, 16, v87\n"
"v_mfma_f32_16x16x32_bf16 v[32:35], v[220:223], a[188:191], v[32:35]\n"
"v_xor_b32 v230, s75, v230\n"
"v_lshlrev_b32 v204, 16, v86\n"
"v_mfma_f32_16x16x32_bf16 v[48:51], v[220:223], a[252:255], v[48:51]\n"
"ds_read_b64_tr_b16 v[220:221], v234 offset:24576\n"
"v_add_f32 v26, v26, v203\n"
"v_mfma_f32_16x16x32_bf16 v[48:51], v[216:219], a[248:251], v[48:51]\n"
"ds_read_b64_tr_b16 v[112:113], v229 offset:16384\n"
"v_bfe_u32 v160, v242, 6, 2\n"
"v_mfma_f32_16x16x32_bf16 v[48:51], v[212:215], a[244:247], v[48:51]\n"
"ds_read_b64_tr_b16 v[116:117], v233 offset:16384\n"
"v_add_f32 v24, v24, v202\n"
"v_mfma_f32_16x16x32_bf16 v[48:51], v[208:211], a[240:243], v[48:51]\n"
"ds_read_b64_tr_b16 v[118:119], v233 offset:18432\n"
"v_add_f32 v25, v25, v84\n"
"v_mfma_f32_16x16x32_bf16 v[48:51], v[192:195], a[204:207], v[48:51]\n"
"ds_read_b64_tr_b16 v[210:211], v230 offset:18432\n"
"v_add_f32 v28, v28, v204\n"
"v_mfma_f32_16x16x32_bf16 v[48:51], v[188:191], a[200:203], v[48:51]\n"
"ds_read_b64_tr_b16 v[188:189], v230 offset:8192\n"
"v_and_b32 v86, s73, v86\n"
"v_mfma_f32_16x16x32_bf16 v[48:51], v[184:187], a[196:199], v[48:51]\n"
"ds_read_b64_tr_b16 v[194:195], v234 offset:10240\n"
"v_mov_b32 v164, 0\n"
"v_mfma_f32_16x16x32_bf16 v[48:51], v[180:183], a[192:195], v[48:51]\n"
"s_waitcnt vmcnt(15)\n"
"v_xor_b32 v160, v160, v161\n"
"v_lshlrev_b32 v202, 16, v90\n"
"v_mfma_f32_16x16x32_bf16 v[4:7], v[96:99], a[96:99], v[4:7]\n"
"s_waitcnt lgkmcnt(6)\n"
"ds_read_b64_tr_b16 v[222:223], v234 offset:26624\n"
"v_lshlrev_b32 v203, 16, v91\n"
"v_mfma_f32_16x16x32_bf16 v[4:7], v[100:103], a[100:103], v[4:7]\n"
"ds_read_b64_tr_b16 v[184:185], v234 offset:0\n"
"v_lshlrev_b32 v206, 16, v88\n"
"v_mfma_f32_16x16x32_bf16 v[4:7], v[104:107], a[104:107], v[4:7]\n"
"ds_read_b64_tr_b16 v[216:217], v230 offset:24576\n"
"v_mfma_f32_16x16x32_bf16 v[4:7], v[108:111], a[108:111], v[4:7]\n"
"s_waitcnt lgkmcnt(6)\n"
"ds_read_b64_tr_b16 v[218:219], v230 offset:26624\n"
"v_and_b32 v91, s73, v91\n"
"v_mfma_f32_16x16x32_bf16 v[4:7], v[112:115], a[144:147], v[4:7]\n"
"s_waitcnt vmcnt(4)\n"
"ds_read_b64_tr_b16 v[190:191], v230 offset:10240\n"
"v_lshlrev_b32 v207, 16, v89\n"
"v_mfma_f32_16x16x32_bf16 v[4:7], v[116:119], a[148:151], v[4:7]\n"
"v_subrev_u32 v200, s21, v200\n"
"ds_read_b64_tr_b16 v[208:209], v230 offset:16384\n"
"v_mfma_f32_16x16x32_bf16 v[4:7], v[120:123], a[152:155], v[4:7]\n"
"v_cmp_lt_u32 vcc, s69, v200\n"
"ds_read_b64_tr_b16 v[182:183], v230 offset:2048\n"
"v_mfma_f32_16x16x32_bf16 v[4:7], v[224:227], a[156:159], v[4:7]\n"
"ds_read_b64_tr_b16 v[180:181], v230 offset:0\n"
"s_cbranch_vccnz LGQ_%=\nLGQK_%=:\n"
"v_mfma_f32_16x16x32_bf16 v[20:23], v[224:227], a[172:175], v[20:23]\n"
"s_bfe_u32 s20, s89, 65536\ns_lshl_b32 s20, s20, 13\ns_add_u32 s20, s20, 0\n"
"ds_read_b64_tr_b16 v[214:215], v234 offset:18432\n"
"v_and_b32 v85, s73, v85\n"
"v_mfma_f32_16x16x32_bf16 v[20:23], v[120:123], a[168:171], v[20:23]\n"
"buffer_load_dwordx4 v[124:127], v236, s[32:35], s20 offen sc1\n"
"v_mfma_f32_16x16x32_bf16 v[20:23], v[116:119], a[164:167], v[20:23]\n"
"v_and_b32 v90, s73, v90\n"
"v_add_f32 v37, v37, v90\n"
"v_mfma_f32_16x16x32_bf16 v[20:23], v[112:115], a[160:163], v[20:23]\n"
"s_bfe_u32 s20, s89, 65540\ns_lshl_b32 s20, s20, 13\ns_add_u32 s20, s20, 1048576\n"
"v_add_f32 v36, v36, v202\n"
"v_add_f32 v38, v38, v203\n"
"v_mfma_f32_16x16x32_bf16 v[20:23], v[108:111], a[124:127], v[20:23]\n"
"buffer_load_dwordx4 v[128:131], v236, s[32:35], s20 offen sc1\n"
"v_mfma_f32_16x16x32_bf16 v[20:23], v[104:107], a[120:123], v[20:23]\n"
"v_add_u32 v160, -1, v160\n"
"v_add_f32 v39, v39, v91\n"
"v_mfma_f32_16x16x32_bf16 v[20:23], v[100:103], a[116:119], v[20:23]\n"
"s_bfe_u32 s20, s89, 65544\ns_lshl_b32 s20, s20, 13\ns_add_u32 s20, s20, 2097152\n"
"v_add_f32 v32, v32, v206\n"
"v_mfma_f32_16x16x32_bf16 v[20:23], v[96:99], a[112:115], v[20:23]\n"
"buffer_load_dwordx4 v[132:135], v236, s[32:35], s20 offen sc1\n"
"v_mfma_f32_16x16x32_bf16 v[36:39], v[96:99], a[128:131], v[36:39]\n"
"v_lshlrev_b32 v204, 16, v92\n"
"v_add_f32 v40, v40, v204\n"
"v_mfma_f32_16x16x32_bf16 v[36:39], v[100:103], a[132:135], v[36:39]\n"
"s_bfe_u32 s20, s89, 65548\ns_lshl_b32 s20, s20, 13\ns_add_u32 s20, s20, 3145728\n"
"ds_read_b64_tr_b16 v[212:213], v234 offset:16384\n"
"v_add_f32 v30, v30, v205\n"
"v_mfma_f32_16x16x32_bf16 v[36:39], v[104:107], a[136:139], v[36:39]\n"
"buffer_load_dwordx4 v[136:139], v236, s[32:35], s20 offen sc1\n"
"v_mfma_f32_16x16x32_bf16 v[36:39], v[108:111], a[140:143], v[36:39]\n"
"v_and_b32 v92, s73, v92\n"
"v_lshlrev_b32 v206, 16, v94\n"
"v_mfma_f32_16x16x32_bf16 v[36:39], v[112:115], a[176:179], v[36:39]\n"
"s_bfe_u32 s20, s89, 65552\ns_lshl_b32 s20, s20, 13\ns_add_u32 s20, s20, 4194304\n"
"ds_read_b64_tr_b16 v[186:187], v234 offset:2048\n"
"ds_read_b64_tr_b16 v[192:193], v234 offset:8192\n"
"v_mfma_f32_16x16x32_bf16 v[36:39], v[116:119], a[180:183], v[36:39]\n"
"buffer_load_dwordx4 v[140:143], v236, s[32:35], s20 offen sc1\n"
"v_mfma_f32_16x16x32_bf16 v[36:39], v[120:123], a[184:187], v[36:39]\n"
"v_and_b32 v94, s73, v94\n"
"v_mfma_f32_16x16x32_bf16 v[36:39], v[224:227], a[188:191], v[36:39]\n"
"s_bfe_u32 s20, s89, 65556\ns_lshl_b32 s20, s20, 13\ns_add_u32 s20, s20, 5242880\n"
"v_add_f32 v34, v34, v207\n"
"v_lshlrev_b32 v205, 16, v93\n"
"v_mfma_f32_16x16x32_bf16 v[52:55], v[224:227], a[252:255], v[52:55]\n"
"buffer_load_dwordx4 v[144:147], v236, s[32:35], s20 offen sc1\n"
"v_mfma_f32_16x16x32_bf16 v[52:55], v[120:123], a[248:251], v[52:55]\n"
"v_ashrrev_i32 v160, 31, v160\n"
"v_add_f32 v44, v44, v206\n"
"v_mfma_f32_16x16x32_bf16 v[52:55], v[116:119], a[244:247], v[52:55]\n"
"s_bfe_u32 s20, s89, 65560\ns_lshl_b32 s20, s20, 13\ns_add_u32 s20, s20, 6291456\n"
"v_xor_b32 v231, s75, v231\n"
"v_lshlrev_b32 v207, 16, v95\n"
"v_mfma_f32_16x16x32_bf16 v[52:55], v[112:115], a[240:243], v[52:55]\n"
"buffer_load_dwordx4 v[148:151], v236, s[32:35], s20 offen sc1\n"
"v_mfma_f32_16x16x32_bf16 v[52:55], v[108:111], a[204:207], v[52:55]\n"
"s_bfe_u32 s20, s89, 65564\ns_lshl_b32 s20, s20, 13\ns_add_u32 s20, s20, 7340032\n"
"v_and_b32 v87, s73, v87\n"
"v_and_b32 v95, s73, v95\n"
"v_mfma_f32_16x16x32_bf16 v[52:55], v[104:107], a[200:203], v[52:55]\n"
"buffer_load_dwordx4 v[152:155], v236, s[32:35], s20 offen sc1\n"
"v_mfma_f32_16x16x32_bf16 v[52:55], v[100:103], a[196:199], v[52:55]\n"
"s_waitcnt vmcnt(8)\n"
"s_add_u32 s21, s21, 1\n"
"v_and_b32 v88, s73, v88\n"
"v_mfma_f32_16x16x32_bf16 v[52:55], v[96:99], a[192:195], v[52:55]\n"
"s_barrier\n"
"s_mov_b32 s52, 40960\n"
"buffer_store_dword v241, v248, s[84:87], 0 offen sc1\n"
"s_waitcnt lgkmcnt(0)\n"
"v_mfma_f32_16x16x32_bf16 v[8:11], v[180:183], a[96:99], v[8:11]\n"
"s_mov_b32 s53, 81920\n"
"ds_read_b64_tr_b16 v[98:99], v231 offset:2048\n"
"v_add_f32 v42, v42, v205\n"
"v_mfma_f32_16x16x32_bf16 v[8:11], v[184:187], a[100:103], v[8:11]\n"
"ds_read_b64_tr_b16 v[112:113], v231 offset:16384\n"
"v_add_f32 v31, v31, v87\n"
"v_mfma_f32_16x16x32_bf16 v[8:11], v[188:191], a[104:107], v[8:11]\n"
"v_add_f32 v29, v29, v86\n"
"v_and_b32 v161, 16, v242\n"
"v_mfma_f32_16x16x32_bf16 v[8:11], v[192:195], a[108:111], v[8:11]\n"
"ds_read_b64_tr_b16 v[118:119], v235 offset:18432\n"
"v_and_b32 v160, 0x3f80, v160\n"
"v_mfma_f32_16x16x32_bf16 v[8:11], v[208:211], a[144:147], v[8:11]\n"
"ds_read_b64_tr_b16 v[102:103], v235 offset:2048\n"
"v_add_f32 v27, v27, v85\n"
"v_mfma_f32_16x16x32_bf16 v[8:11], v[212:215], a[148:151], v[8:11]\n"
"ds_read_b64_tr_b16 v[106:107], v231 offset:10240\n"
"v_lshlrev_b32 v160, v161, v160\n"
"v_mfma_f32_16x16x32_bf16 v[8:11], v[216:219], a[152:155], v[8:11]\n"
"ds_read_b64_tr_b16 v[96:97], v231 offset:0\n"
"v_and_b32 v93, s73, v93\n"
"v_mfma_f32_16x16x32_bf16 v[8:11], v[220:223], a[156:159], v[8:11]\n"
"ds_read_b64_tr_b16 v[108:109], v235 offset:8192\n"
"v_add_f32 v41, v41, v92\n"
"v_mfma_f32_16x16x32_bf16 v[24:27], v[220:223], a[172:175], v[24:27]\n"
"v_bfe_i32 v161, v242, 5, 1\n"
"v_lshlrev_b32 v206, 16, v244\n"
"v_mfma_f32_16x16x32_bf16 v[24:27], v[216:219], a[168:171], v[24:27]\n"
"ds_read_b64_tr_b16 v[114:115], v231 offset:18432\n"
"v_mfma_f32_16x16x32_bf16 v[24:27], v[212:215], a[164:167], v[24:27]\n"
"ds_read_b64_tr_b16 v[104:105], v231 offset:8192\n"
"v_lshlrev_b32 v203, 16, v247\n"
"v_mfma_f32_16x16x32_bf16 v[24:27], v[208:211], a[160:163], v[24:27]\n"
"ds_read_b64_tr_b16 v[116:117], v235 offset:16384\n"
"v_add_f32 v48, v48, v206\n"
"v_mfma_f32_16x16x32_bf16 v[24:27], v[192:195], a[124:127], v[24:27]\n"
"ds_read_b64_tr_b16 v[110:111], v235 offset:10240\n"
"v_and_b32 v89, s73, v89\n"
"v_mfma_f32_16x16x32_bf16 v[24:27], v[188:191], a[120:123], v[24:27]\n"
"v_add_f32 v45, v45, v94\n"
"v_add_f32 v47, v47, v95\n"
"v_mfma_f32_16x16x32_bf16 v[24:27], v[184:187], a[116:119], v[24:27]\n"
"ds_read_b64_tr_b16 v[100:101], v235 offset:0\n"
"v_add_f32 v43, v43, v93\n"
"v_mfma_f32_16x16x32_bf16 v[24:27], v[180:183], a[112:115], v[24:27]\n"
"ds_read_b64_tr_b16 v[122:123], v231 offset:26624\n"
"v_add_f32 v46, v46, v207\n"
"v_mfma_f32_16x16x32_bf16 v[40:43], v[180:183], a[128:131], v[40:43]\n"
"v_add_f32 v33, v33, v88\n"
"v_and_b32 v244, s73, v244\n"
"v_mfma_f32_16x16x32_bf16 v[40:43], v[184:187], a[132:135], v[40:43]\n"
"ds_read_b64_tr_b16 v[120:121], v231 offset:24576\n"
"v_lshlrev_b32 v207, 16, v245\n"
"v_mfma_f32_16x16x32_bf16 v[40:43], v[188:191], a[136:139], v[40:43]\n"
"v_and_b32 v163, v160, v161\n"
"v_lshlrev_b32 v202, 16, v246\n"
"v_mfma_f32_16x16x32_bf16 v[40:43], v[192:195], a[140:143], v[40:43]\n"
"ds_read_b64_tr_b16 v[224:225], v235 offset:24576\n"
"v_and_b32 v246, s73, v246\n"
"v_mfma_f32_16x16x32_bf16 v[40:43], v[208:211], a[176:179], v[40:43]\n"
"v_add_f32 v52, v52, v202\n"
"v_and_b32 v247, s73, v247\n"
"v_mfma_f32_16x16x32_bf16 v[40:43], v[212:215], a[180:183], v[40:43]\n"
"ds_read_b64_tr_b16 v[226:227], v235 offset:26624\n"
"v_and_b32 v245, s73, v245\n"
"v_mfma_f32_16x16x32_bf16 v[40:43], v[216:219], a[184:187], v[40:43]\n"
"v_mov_b32 v161, 0\n"
"v_add_f32 v54, v54, v203\n"
"v_mfma_f32_16x16x32_bf16 v[40:43], v[220:223], a[188:191], v[40:43]\n"
"s_barrier\n"
"s_waitcnt lgkmcnt(3)\n"
"v_mfma_f32_16x16x32_bf16 v[12:15], v[96:99], a[96:99], v[12:15]\n"
"ds_read_b64_tr_b16 v[184:185], v232 offset:32768\n"
"v_add_f32 v49, v49, v244\n"
"v_mfma_f32_16x16x32_bf16 v[12:15], v[100:103], a[100:103], v[12:15]\n"
"ds_read_b64_tr_b16 v[216:217], v228 offset:57344\n"
"v_mfma_f32_16x16x32_bf16 v[12:15], v[104:107], a[104:107], v[12:15]\n"
"ds_read_b64_tr_b16 v[188:189], v228 offset:40960\n"
"v_add_f32 v35, v35, v89\n"
"v_mfma_f32_16x16x32_bf16 v[12:15], v[108:111], a[108:111], v[12:15]\n"
"ds_read_b64_tr_b16 v[190:191], v228 offset:43008\n"
"v_mfma_f32_16x16x32_bf16 v[12:15], v[112:115], a[144:147], v[12:15]\n"
"ds_read_b64_tr_b16 v[192:193], v232 offset:40960\n"
"v_add_f32 v53, v53, v246\n"
"v_mfma_f32_16x16x32_bf16 v[12:15], v[116:119], a[148:151], v[12:15]\n"
"s_waitcnt lgkmcnt(5)\n"
"ds_read_b64_tr_b16 v[212:213], v232 offset:49152\n"
"v_add_f32 v51, v51, v245\n"
"v_mfma_f32_16x16x32_bf16 v[12:15], v[120:123], a[152:155], v[12:15]\n"
"ds_read_b64_tr_b16 v[182:183], v228 offset:34816\n"
"v_xor_b32 v162, v160, v163\n"
"v_mfma_f32_16x16x32_bf16 v[12:15], v[224:227], a[156:159], v[12:15]\n"
"global_load_dword v200, v201, s[6:7] sc1\n"
"v_mfma_f32_16x16x32_bf16 v[28:31], v[224:227], a[172:175], v[28:31]\n"
"v_add_f32 v50, v50, v207\n"
"v_add_f32 v55, v55, v247\n"
"v_mfma_f32_16x16x32_bf16 v[28:31], v[120:123], a[168:171], v[28:31]\n"
"s_waitcnt vmcnt(9)\n"
"v_lshlrev_b32 v58, 16, v125\n"
"v_lshlrev_b32 v62, 16, v127\n"
"v_mfma_f32_16x16x32_bf16 v[28:31], v[116:119], a[164:167], v[28:31]\n"
"ds_read_b64_tr_b16 v[186:187], v232 offset:34816\n"
"ds_read_b64_tr_b16 v[214:215], v232 offset:51200\n"
"v_mfma_f32_16x16x32_bf16 v[28:31], v[112:115], a[160:163], v[28:31]\n"
"ds_read_b64_tr_b16 v[194:195], v232 offset:43008\n"
"v_and_b32 v61, s73, v126\n"
"v_mfma_f32_16x16x32_bf16 v[28:31], v[108:111], a[124:127], v[28:31]\n"
"s_waitcnt vmcnt(8)\n"
"ds_read_b64_tr_b16 v[210:211], v228 offset:51200\n"
"v_and_b32 v57, s73, v124\n"
"v_mfma_f32_16x16x32_bf16 v[28:31], v[104:107], a[120:123], v[28:31]\n"
"ds_read_b64_tr_b16 v[222:223], v232 offset:59392\n"
"v_lshlrev_b32 v56, 16, v124\n"
"v_mfma_f32_16x16x32_bf16 v[28:31], v[100:103], a[116:119], v[28:31]\n"
"ds_read_b64_tr_b16 v[180:181], v228 offset:32768\n"
"v_lshlrev_b32 v205, 16, v129\n"
"v_mfma_f32_16x16x32_bf16 v[28:31], v[96:99], a[112:115], v[28:31]\n"
"s_waitcnt vmcnt(7)\n"
"v_and_b32 v59, s73, v125\n"
"v_lshlrev_b32 v206, 16, v130\n"
"v_mfma_f32_16x16x32_bf16 v[44:47], v[96:99], a[128:131], v[44:47]\n"
"ds_read_b64_tr_b16 v[220:221], v232 offset:57344\n"
"v_lshlrev_b32 v204, 16, v128\n"
"v_mfma_f32_16x16x32_bf16 v[44:47], v[100:103], a[132:135], v[44:47]\n"
"ds_read_b64_tr_b16 v[208:209], v228 offset:49152\n"
"v_and_b32 v130, s73, v130\n"
"v_mfma_f32_16x16x32_bf16 v[44:47], v[104:107], a[136:139], v[44:47]\n"
"s_waitcnt vmcnt(6)\n"
"ds_read_b64_tr_b16 v[102:103], v233 offset:34816\n"
"v_add_f32 v58, v58, v205\n"
"v_mfma_f32_16x16x32_bf16 v[44:47], v[108:111], a[140:143], v[44:47]\n"
"v_lshlrev_b32 v207, 16, v131\n"
"v_add_f32 v62, v62, v207\n"
"v_mfma_f32_16x16x32_bf16 v[44:47], v[112:115], a[176:179], v[44:47]\n"
"ds_read_b64_tr_b16 v[218:219], v228 offset:59392\n"
"v_and_b32 v131, s73, v131\n"
"v_mfma_f32_16x16x32_bf16 v[44:47], v[116:119], a[180:183], v[44:47]\n"
"s_waitcnt vmcnt(5)\n"
"ds_read_b64_tr_b16 v[96:97], v229 offset:32768\n"
"v_lshlrev_b32 v205, 16, v135\n"
"v_mfma_f32_16x16x32_bf16 v[44:47], v[120:123], a[184:187], v[44:47]\n"
"v_lshlrev_b32 v202, 16, v132\n"
"v_lshlrev_b32 v207, 16, v137\n"
"v_mfma_f32_16x16x32_bf16 v[44:47], v[224:227], a[188:191], v[44:47]\n"
"s_waitcnt lgkmcnt(5)\n"
"ds_read_b64_tr_b16 v[120:121], v229 offset:57344\n"
"v_add_f32 v56, v56, v204\n"
"v_mfma_f32_16x16x32_bf16 v[0:3], v[180:183], a[48:51], v[0:3]\n"
"s_waitcnt vmcnt(4)\n"
"ds_read_b64_tr_b16 v[116:117], v233 offset:49152\n"
"v_add_f32 v61, v61, v130\n"
"v_mfma_f32_16x16x32_bf16 v[0:3], v[184:187], a[52:55], v[0:3]\n"
"v_lshlrev_b32 v60, 16, v126\n"
"v_lshlrev_b32 v204, 16, v134\n"
"v_mfma_f32_16x16x32_bf16 v[0:3], v[188:191], a[56:59], v[0:3]\n"
"ds_read_b64_tr_b16 v[110:111], v233 offset:43008\n"
"v_add_f32 v56, v56, v202\n"
"v_mfma_f32_16x16x32_bf16 v[0:3], v[192:195], a[60:63], v[0:3]\n"
"s_waitcnt lgkmcnt(5)\n"
"s_waitcnt vmcnt(3)\n"
"ds_read_b64_tr_b16 v[112:113], v229 offset:49152\n"
"v_and_b32 v63, s73, v127\n"
"v_mfma_f32_16x16x32_bf16 v[0:3], v[208:211], a[0:3], v[0:3]\n"
"ds_read_b64_tr_b16 v[224:225], v233 offset:57344\n"
"v_and_b32 v134, s73, v134\n"
"v_mfma_f32_16x16x32_bf16 v[0:3], v[212:215], a[4:7], v[0:3]\n"
"s_waitcnt lgkmcnt(5)\n"
"s_waitcnt vmcnt(2)\n"
"ds_read_b64_tr_b16 v[108:109], v233 offset:40960\n"
"v_and_b32 v129, s73, v129\n"
"v_mfma_f32_16x16x32_bf16 v[0:3], v[216:219], a[8:11], v[0:3]\n"
"v_add_f32 v60, v60, v206\n"
"v_add_f32 v60, v60, v204\n"
"v_mfma_f32_16x16x32_bf16 v[0:3], v[220:223], a[12:15], v[0:3]\n"
"ds_read_b64_tr_b16 v[98:99], v229 offset:34816\n"
"ds_read_b64_tr_b16 v[122:123], v229 offset:59392\n"
"v_mfma_f32_16x16x32_bf16 v[16:19], v[220:223], a[28:31], v[16:19]\n"
"v_mov_b32 v165, 0\n"
"v_add_f32 v59, v59, v129\n"
"v_mfma_f32_16x16x32_bf16 v[16:19], v[216:219], a[24:27], v[16:19]\n"
"v_add_f32 v62, v62, v205\n"
"v_lshlrev_b32 v206, 16, v136\n"
"v_mfma_f32_16x16x32_bf16 v[16:19], v[212:215], a[20:23], v[16:19]\n"
"ds_read_b64_tr_b16 v[104:105], v229 offset:40960\n"
"ds_read_b64_tr_b16 v[226:227], v233 offset:59392\n"
"v_mfma_f32_16x16x32_bf16 v[16:19], v[208:211], a[16:19], v[16:19]\n"
"v_add_f32 v56, v56, v206\n"
"v_lshlrev_b32 v202, 16, v138\n"
"v_mfma_f32_16x16x32_bf16 v[16:19], v[192:195], a[76:79], v[16:19]\n"
"s_waitcnt vmcnt(0)\n"
"ds_read_b64_tr_b16 v[118:119], v233 offset:51200\n"
"v_subrev_f32 v2, s22, v2\n"
"v_mfma_f32_16x16x32_bf16 v[16:19], v[188:191], a[72:75], v[16:19]\n"
"v_subrev_u32 v200, s21, v200\n"
"ds_read_b64_tr_b16 v[114:115], v229 offset:51200\n"
"v_mfma_f32_16x16x32_bf16 v[16:19], v[184:187], a[68:71], v[16:19]\n"
"v_cmp_lt_u32 vcc, s69, v200\n"
"ds_read_b64_tr_b16 v[100:101], v233 offset:32768\n"
"v_mfma_f32_16x16x32_bf16 v[16:19], v[180:183], a[64:67], v[16:19]\n"
"v_lshlrev_b32 v206, 16, v142\n"
"s_cbranch_vccnz LGR_%=\nLGRK_%=:\n"
"v_mfma_f32_16x16x32_bf16 v[32:35], v[180:183], a[80:83], v[32:35]\n"
"v_subrev_f32 v3, s22, v3\n"
"s_cmp_eq_u32 s93, 0\ns_cbranch_scc1 LEP_%=\nLEPK_%=:\n"
"v_mfma_f32_16x16x32_bf16 v[32:35], v[184:187], a[84:87], v[32:35]\n"
"s_bfe_u32 s20, s89, 65536\ns_lshl_b32 s20, s20, 13\ns_add_u32 s20, s20, 65536\n"
"ds_read_b64_tr_b16 v[106:107], v229 offset:43008\n"
"v_and_b32 v128, s73, v128\n"
"v_mfma_f32_16x16x32_bf16 v[32:35], v[188:191], a[88:91], v[32:35]\n"
"buffer_load_dwordx4 v[72:75], v236, s[32:35], s20 offen sc1\n"
"v_mfma_f32_16x16x32_bf16 v[32:35], v[192:195], a[92:95], v[32:35]\n"
"v_lshlrev_b32 v203, 16, v133\n"
"v_lshlrev_b32 v205, 16, v141\n"
"v_mfma_f32_16x16x32_bf16 v[32:35], v[208:211], a[32:35], v[32:35]\n"
"s_bfe_u32 s20, s89, 65540\ns_lshl_b32 s20, s20, 13\ns_add_u32 s20, s20, 1114112\n"
"v_lshlrev_b32 v204, 16, v140\n"
"v_add_f32 v56, v56, v204\n"
"v_mfma_f32_16x16x32_bf16 v[32:35], v[212:215], a[36:39], v[32:35]\n"
"buffer_load_dwordx4 v[76:79], v236, s[32:35], s20 offen sc1\n"
"v_mfma_f32_16x16x32_bf16 v[32:35], v[216:219], a[40:43], v[32:35]\n"
"v_and_b32 v136, s73, v136\n"
"v_subrev_f32 v17, s22, v17\n"
"v_mfma_f32_16x16x32_bf16 v[32:35], v[220:223], a[44:47], v[32:35]\n"
"s_bfe_u32 s20, s89, 65544\ns_lshl_b32 s20, s20, 13\ns_add_u32 s20, s20, 2162688\n"
"v_add_f32 v61, v61, v134\n"
"v_and_b32 v141, s73, v141\n"
"v_mfma_f32_16x16x32_bf16 v[48:51], v[220:223], a[228:231], v[48:51]\n"
"buffer_load_dwordx4 v[80:83], v236, s[32:35], s20 offen sc1\n"
"v_mfma_f32_16x16x32_bf16 v[48:51], v[216:219], a[224:227], v[48:51]\n"
"v_add_f32 v58, v58, v203\n"
"v_subrev_f32 v19, s22, v19\n"
"v_mfma_f32_16x16x32_bf16 v[48:51], v[212:215], a[212:215], v[48:51]\n"
"s_bfe_u32 s20, s89, 65548\ns_lshl_b32 s20, s20, 13\ns_add_u32 s20, s20, 3211264\n"
"v_and_b32 v140, s73, v140\n"
"v_lshlrev_b32 v204, 16, v146\n"
"v_mfma_f32_16x16x32_bf16 v[48:51], v[208:211], a[208:211], v[48:51]\n"
"buffer_load_dwordx4 v[84:87], v236, s[32:35], s20 offen sc1\n"
"v_mfma_f32_16x16x32_bf16 v[48:51], v[192:195], a[236:239], v[48:51]\n"
"v_add_f32 v57, v57, v128\n"
"v_subrev_f32 v34, s22, v34\n"
"v_mfma_f32_16x16x32_bf16 v[48:51], v[188:191], a[232:235], v[48:51]\n"
"s_bfe_u32 s20, s89, 65552\ns_lshl_b32 s20, s20, 13\ns_add_u32 s20, s20, 4259840\n"
"ds_read_b64_tr_b16 v[214:215], v234 offset:51200\n"
"v_and_b32 v132, s73, v132\n"
"v_mfma_f32_16x16x32_bf16 v[48:51], v[184:187], a[220:223], v[48:51]\n"
"buffer_load_dwordx4 v[88:91], v236, s[32:35], s20 offen sc1\n"
"v_mfma_f32_16x16x32_bf16 v[48:51], v[180:183], a[216:219], v[48:51]\n"
"s_waitcnt lgkmcnt(1)\n"
"v_add_f32 v60, v60, v202\n"
"v_add_f32 v60, v60, v206\n"
"v_mfma_f32_16x16x32_bf16 v[4:7], v[96:99], a[48:51], v[4:7]\n"
"s_bfe_u32 s20, s89, 65556\ns_lshl_b32 s20, s20, 13\ns_add_u32 s20, s20, 5308416\n"
"v_lshlrev_b32 v206, 16, v148\n"
"v_subrev_f32 v35, s22, v35\n"
"v_mfma_f32_16x16x32_bf16 v[4:7], v[100:103], a[52:55], v[4:7]\n"
"buffer_load_dwordx4 v[92:95], v236, s[32:35], s20 offen sc1\n"
"v_mfma_f32_16x16x32_bf16 v[4:7], v[104:107], a[56:59], v[4:7]\n"
"v_add_f32 v60, v60, v204\n"
"v_subrev_f32 v0, s22, v0\n"
"v_mfma_f32_16x16x32_bf16 v[4:7], v[108:111], a[60:63], v[4:7]\n"
"s_bfe_u32 s20, s89, 65560\ns_lshl_b32 s20, s20, 13\ns_add_u32 s20, s20, 6356992\n"
"v_add_f32 v57, v57, v132\n"
"v_subrev_f32 v33, s22, v33\n"
"v_mfma_f32_16x16x32_bf16 v[4:7], v[112:115], a[0:3], v[4:7]\n"
"buffer_load_dwordx4 v[244:247], v236, s[32:35], s20 offen sc1\n"
"v_mfma_f32_16x16x32_bf16 v[4:7], v[116:119], a[4:7], v[4:7]\n"
"v_and_b32 v138, s73, v138\n"
"v_subrev_f32 v18, s22, v18\n"
"v_mfma_f32_16x16x32_bf16 v[4:7], v[120:123], a[8:11], v[4:7]\n"
"s_bfe_u32 s20, s89, 65564\ns_lshl_b32 s20, s20, 13\ns_add_u32 s20, s20, 7405568\n"
"v_and_b32 v135, s73, v135\n"
"v_subrev_f32 v49, s22, v49\n"
"v_mfma_f32_16x16x32_bf16 v[4:7], v[224:227], a[12:15], v[4:7]\n"
"buffer_load_dwordx4 v[156:159], v236, s[32:35], s20 offen sc1\n"
"v_add_f32 v58, v58, v207\n"
"v_lshlrev_b32 v203, 16, v139\n"
"v_add_f32 v58, v58, v205\n"
"v_and_b32 v142, s73, v142\n"
"v_lshlrev_b32 v202, 16, v144\n"
"v_add_f32 v56, v56, v202\n"
"v_add_f32 v56, v56, v206\n"
"v_subrev_f32 v1, s22, v1\n"
"v_mfma_f32_16x16x32_bf16 v[20:23], v[224:227], a[28:31], v[20:23]\n"
"v_add_f32 v63, v63, v131\n"
"v_lshlrev_b32 v204, 16, v152\n"
"v_mfma_f32_16x16x32_bf16 v[20:23], v[120:123], a[24:27], v[20:23]\n"
"v_and_b32 v133, s73, v133\n"
"v_add_f32 v59, v59, v133\n"
"v_mfma_f32_16x16x32_bf16 v[20:23], v[116:119], a[20:23], v[20:23]\n"
"ds_read_b64_tr_b16 v[218:219], v230 offset:59392\n"
"v_and_b32 v137, s73, v137\n"
"v_mfma_f32_16x16x32_bf16 v[20:23], v[112:115], a[16:19], v[20:23]\n"
"v_and_b32 v148, s73, v148\n"
"v_subrev_f32 v16, s22, v16\n"
"v_mfma_f32_16x16x32_bf16 v[20:23], v[108:111], a[76:79], v[20:23]\n"
"v_add_f32 v62, v62, v203\n"
"v_subrev_f32 v6, s22, v6\n"
"v_mfma_f32_16x16x32_bf16 v[20:23], v[104:107], a[72:75], v[20:23]\n"
"v_add_f32 v57, v57, v136\n"
"v_lshlrev_b32 v205, 16, v147\n"
"v_mfma_f32_16x16x32_bf16 v[20:23], v[100:103], a[68:71], v[20:23]\n"
"ds_read_b64_tr_b16 v[194:195], v234 offset:43008\n"
"v_subrev_f32 v32, s22, v32\n"
"v_mfma_f32_16x16x32_bf16 v[20:23], v[96:99], a[64:67], v[20:23]\n"
"ds_read_b64_tr_b16 v[184:185], v234 offset:32768\n"
"v_add_f32 v59, v59, v137\n"
"v_and_b32 v139, s73, v139\n"
"v_add_f32 v59, v59, v141\n"
"v_lshlrev_b32 v207, 16, v143\n"
"v_lshlrev_b32 v203, 16, v145\n"
"v_and_b32 v145, s73, v145\n"
"v_add_f32 v58, v58, v203\n"
"v_add_f32 v59, v59, v145\n"
"v_and_b32 v147, s73, v147\n"
"v_lshlrev_b32 v203, 16, v151\n"
"v_subrev_f32 v4, s22, v4\n"
"v_subrev_f32 v5, s22, v5\n"
"v_subrev_f32 v7, s22, v7\n"
"v_cvt_pk_bf16_f32 v0, v0, v1\n"
"v_cvt_pk_bf16_f32 v1, v2, v3\n"
"v_cvt_pk_bf16_f32 v3, v6, v7\n"
"v_mfma_f32_16x16x32_bf16 v[36:39], v[96:99], a[80:83], v[36:39]\n"
"v_add_f32 v56, v56, v204\n"
"v_cvt_pk_bf16_f32 v2, v4, v5\n"
"v_mfma_f32_16x16x32_bf16 v[36:39], v[100:103], a[84:87], v[36:39]\n"
"ds_read_b64_tr_b16 v[192:193], v234 offset:40960\n"
"v_add_f32 v61, v61, v138\n"
"v_mfma_f32_16x16x32_bf16 v[36:39], v[104:107], a[88:91], v[36:39]\n"
"v_add_f32 v57, v57, v140\n"
"v_subrev_f32 v23, s22, v23\n"
"v_mfma_f32_16x16x32_bf16 v[36:39], v[108:111], a[92:95], v[36:39]\n"
"ds_read_b64_tr_b16 v[180:181], v230 offset:32768\n"
"v_and_b32 v144, s73, v144\n"
"v_mfma_f32_16x16x32_bf16 v[36:39], v[112:115], a[32:35], v[36:39]\n"
"ds_read_b64_tr_b16 v[210:211], v230 offset:51200\n"
"v_add_f32 v62, v62, v207\n"
"v_add_f32 v62, v62, v205\n"
"v_lshlrev_b32 v207, 16, v149\n"
"v_add_f32 v58, v58, v207\n"
"v_lshlrev_b32 v202, 16, v150\n"
"v_permlane16_swap_b32 v0, v2\n"
"v_permlane16_swap_b32 v1, v3\n"
"v_subrev_f32 v20, s22, v20\n"
"v_cvt_pk_bf16_f32 v16, v16, v17\n"
"v_mfma_f32_16x16x32_bf16 v[36:39], v[116:119], a[36:39], v[36:39]\n"
"ds_read_b64_tr_b16 v[186:187], v234 offset:34816\n"
"v_add_f32 v63, v63, v135\n"
"v_mfma_f32_16x16x32_bf16 v[36:39], v[120:123], a[40:43], v[36:39]\n"
"ds_read_b64_tr_b16 v[188:189], v230 offset:40960\n"
"buffer_store_dwordx4 v[0:3], v238, s[36:39], 0 offen sc0 sc1\n"
"v_and_b32 v143, s73, v143\n"
"v_and_b32 v146, s73, v146\n"
"v_and_b32 v149, s73, v149\n"
"v_lshlrev_b32 v205, 16, v153\n"
"v_and_b32 v153, s73, v153\n"
"v_lshlrev_b32 v206, 16, v154\n"
"v_lshlrev_b32 v207, 16, v155\n"
"v_subrev_f32 v21, s22, v21\n"
"v_subrev_f32 v22, s22, v22\n"
"v_cvt_pk_bf16_f32 v17, v18, v19\n"
"v_cvt_pk_bf16_f32 v18, v20, v21\n"
"v_cvt_pk_bf16_f32 v32, v32, v33\n"
"v_mfma_f32_16x16x32_bf16 v[36:39], v[224:227], a[44:47], v[36:39]\n"
"ds_read_b64_tr_b16 v[182:183], v230 offset:34816\n"
"v_add_f32 v63, v63, v139\n"
"v_add_f32 v61, v61, v142\n"
"v_add_f32 v57, v57, v144\n"
"v_add_f32 v57, v57, v148\n"
"v_add_f32 v59, v59, v149\n"
"v_cvt_pk_bf16_f32 v19, v22, v23\n"
"v_permlane16_swap_b32 v16, v18\n"
"v_cvt_pk_bf16_f32 v33, v34, v35\n"
"v_mfma_f32_16x16x32_bf16 v[52:55], v[224:227], a[228:231], v[52:55]\n"
"ds_read_b64_tr_b16 v[190:191], v230 offset:43008\n"
"v_add_f32 v59, v59, v153\n"
"v_mfma_f32_16x16x32_bf16 v[52:55], v[120:123], a[224:227], v[52:55]\n"
"v_permlane16_swap_b32 v17, v19\n"
"v_mfma_f32_16x16x32_bf16 v[52:55], v[116:119], a[212:215], v[52:55]\n"
"ds_read_b64_tr_b16 v[208:209], v230 offset:49152\n"
"v_subrev_f32 v36, s22, v36\n"
"v_mfma_f32_16x16x32_bf16 v[52:55], v[112:115], a[208:211], v[52:55]\n"
"ds_read_b64_tr_b16 v[222:223], v234 offset:59392\n"
"buffer_store_dwordx4 v[16:19], v238, s[36:39], s52 offen sc0 sc1\n"
"v_add_f32 v63, v63, v143\n"
"v_add_f32 v60, v60, v202\n"
"v_add_f32 v62, v62, v203\n"
"v_and_b32 v152, s73, v152\n"
"v_subrev_f32 v38, s22, v38\n"
"v_subrev_f32 v39, s22, v39\n"
"v_mfma_f32_16x16x32_bf16 v[52:55], v[108:111], a[236:239], v[52:55]\n"
"v_and_b32 v155, s73, v155\n"
"v_cvt_pk_bf16_f32 v35, v38, v39\n"
"v_mfma_f32_16x16x32_bf16 v[52:55], v[104:107], a[232:235], v[52:55]\n"
"ds_read_b64_tr_b16 v[216:217], v230 offset:57344\n"
"v_subrev_f32 v37, s22, v37\n"
"v_mfma_f32_16x16x32_bf16 v[52:55], v[100:103], a[220:223], v[52:55]\n"
"v_and_b32 v150, s73, v150\n"
"v_and_b32 v154, s73, v154\n"
"v_mfma_f32_16x16x32_bf16 v[52:55], v[96:99], a[216:219], v[52:55]\n"
"s_waitcnt lgkmcnt(4)\n"
"ds_read_b64_tr_b16 v[212:213], v234 offset:49152\n"
"ds_read_b64_tr_b16 v[220:221], v234 offset:57344\n"
"v_add_f32 v63, v63, v147\n"
"v_and_b32 v151, s73, v151\n"
"v_add_f32 v63, v63, v151\n"
"v_add_f32 v57, v57, v152\n"
"v_add_f32 v58, v58, v205\n"
"v_add_f32 v60, v60, v206\n"
"v_add_f32 v62, v62, v207\n"
"v_cvt_pk_bf16_f32 v34, v36, v37\n"
"v_permlane16_swap_b32 v33, v35\n"
"v_mfma_f32_16x16x32_bf16 v[8:11], v[180:183], a[48:51], v[8:11]\n"
"v_cvt_pk_bf16_f32 v56, v56, v57\n"
"v_cvt_pk_bf16_f32 v57, v58, v59\n"
"v_mfma_f32_16x16x32_bf16 v[8:11], v[184:187], a[52:55], v[8:11]\n"
"s_waitcnt lgkmcnt(5)\n"
"v_permlane16_swap_b32 v32, v34\n"
"v_mfma_f32_16x16x32_bf16 v[8:11], v[188:191], a[56:59], v[8:11]\n"
"buffer_store_dwordx4 v[32:35], v238, s[36:39], s53 offen sc0 sc1\n"
"ds_read_b64_tr_b16 v[120:121], v231 offset:57344\n"
"v_add_f32 v61, v61, v146\n"
"v_add_f32 v61, v61, v150\n"
"v_add_f32 v61, v61, v154\n"
"v_add_f32 v63, v63, v155\n"
"v_cvt_pk_bf16_f32 v58, v60, v61\n"
"v_cvt_pk_bf16_f32 v59, v62, v63\n"
"v_mfma_f32_16x16x32_bf16 v[8:11], v[192:195], a[60:63], v[8:11]\n"
"s_waitcnt lgkmcnt(4)\n"
"v_permlane16_swap_b32 v57, v59\n"
"v_mfma_f32_16x16x32_bf16 v[8:11], v[208:211], a[0:3], v[8:11]\n"
"s_waitcnt lgkmcnt(2)\n"
"v_permlane16_swap_b32 v56, v58\n"
"v_mfma_f32_16x16x32_bf16 v[8:11], v[212:215], a[4:7], v[8:11]\n"
"buffer_store_dwordx4 v[56:59], v253, s[36:39], 0 offen sc0 sc1\n"
"v_subrev_f32 v48, s22, v48\n"
"v_subrev_f32 v50, s22, v50\n"
"v_subrev_f32 v51, s22, v51\n"
"v_mfma_f32_16x16x32_bf16 v[8:11], v[216:219], a[8:11], v[8:11]\n"
"s_waitcnt lgkmcnt(1)\n"
"ds_read_b64_tr_b16 v[112:113], v231 offset:49152\n"
"v_subrev_f32 v54, s22, v54\n"
"v_mfma_f32_16x16x32_bf16 v[8:11], v[220:223], a[12:15], v[8:11]\n"
"ds_read_b64_tr_b16 v[100:101], v235 offset:32768\n"
"v_subrev_f32 v52, s22, v52\n"
"v_mfma_f32_16x16x32_bf16 v[24:27], v[220:223], a[28:31], v[24:27]\n"
"ds_read_b64_tr_b16 v[102:103], v235 offset:34816\n"
"v_subrev_f32 v53, s22, v53\n"
"v_mfma_f32_16x16x32_bf16 v[24:27], v[216:219], a[24:27], v[24:27]\n"
"v_cvt_pk_bf16_f32 v48, v48, v49\n"
"v_cvt_pk_bf16_f32 v49, v50, v51\n"
"v_mfma_f32_16x16x32_bf16 v[24:27], v[212:215], a[20:23], v[24:27]\n"
"ds_read_b64_tr_b16 v[96:97], v231 offset:32768\n"
"v_cvt_pk_bf16_f32 v50, v52, v53\n"
"v_mfma_f32_16x16x32_bf16 v[24:27], v[208:211], a[16:19], v[24:27]\n"
"s_nop 0\n"
"v_permlane16_swap_b32 v48, v50\n"
"v_mfma_f32_16x16x32_bf16 v[24:27], v[192:195], a[76:79], v[24:27]\n"
"ds_read_b64_tr_b16 v[116:117], v235 offset:49152\n"
"v_subrev_f32 v55, s22, v55\n"
"v_mfma_f32_16x16x32_bf16 v[24:27], v[188:191], a[72:75], v[24:27]\n"
"ds_read_b64_tr_b16 v[98:99], v231 offset:34816\n"
"v_cvt_pk_bf16_f32 v51, v54, v55\n"
"v_mfma_f32_16x16x32_bf16 v[24:27], v[184:187], a[68:71], v[24:27]\n"
"s_nop 0\n"
"v_permlane16_swap_b32 v49, v51\n"
"v_mfma_f32_16x16x32_bf16 v[24:27], v[180:183], a[64:67], v[24:27]\n"
"buffer_store_dwordx4 v[48:51], v252, s[36:39], 0 offen sc0 sc1\n"
"ds_read_b64_tr_b16 v[104:105], v231 offset:40960\n"
"v_mfma_f32_16x16x32_bf16 v[40:43], v[180:183], a[80:83], v[40:43]\n"
"ds_read_b64_tr_b16 v[106:107], v231 offset:43008\n"
"ds_read_b64_tr_b16 v[108:109], v235 offset:40960\n"
"v_mfma_f32_16x16x32_bf16 v[40:43], v[184:187], a[84:87], v[40:43]\n"
"ds_read_b64_tr_b16 v[110:111], v235 offset:43008\n"
"v_mfma_f32_16x16x32_bf16 v[40:43], v[188:191], a[88:91], v[40:43]\n"
"ds_read_b64_tr_b16 v[114:115], v231 offset:51200\n"
"v_subrev_f32 v10, s22, v10\n"
"v_mfma_f32_16x16x32_bf16 v[40:43], v[192:195], a[92:95], v[40:43]\n"
"ds_read_b64_tr_b16 v[118:119], v235 offset:51200\n"
"v_mfma_f32_16x16x32_bf16 v[40:43], v[208:211], a[32:35], v[40:43]\n"
"ds_read_b64_tr_b16 v[122:123], v231 offset:59392\n"
"v_subrev_f32 v24, s22, v24\n"
"v_mfma_f32_16x16x32_bf16 v[40:43], v[212:215], a[36:39], v[40:43]\n"
"ds_read_b64_tr_b16 v[224:225], v235 offset:57344\n"
"ds_read_b64_tr_b16 v[226:227], v235 offset:59392\n"
"v_mfma_f32_16x16x32_bf16 v[40:43], v[216:219], a[40:43], v[40:43]\n"
"v_mfma_f32_16x16x32_bf16 v[40:43], v[220:223], a[44:47], v[40:43]\n"
"s_waitcnt lgkmcnt(8)\n"
"v_mov_b32 v160, 0\n"
"v_subrev_f32 v11, s22, v11\n"
"v_mfma_f32_16x16x32_bf16 v[12:15], v[96:99], a[48:51], v[12:15]\n"
"v_subrev_f32 v8, s22, v8\n"
"v_subrev_f32 v9, s22, v9\n"
"v_mfma_f32_16x16x32_bf16 v[12:15], v[100:103], a[52:55], v[12:15]\n"
"s_waitcnt lgkmcnt(5)\n"
"v_cvt_pk_bf16_f32 v8, v8, v9\n"
"v_mfma_f32_16x16x32_bf16 v[12:15], v[104:107], a[56:59], v[12:15]\n"
"v_subrev_f32 v27, s22, v27\n"
"v_mfma_f32_16x16x32_bf16 v[12:15], v[108:111], a[60:63], v[12:15]\n"
"s_waitcnt lgkmcnt(3)\n"
"v_mfma_f32_16x16x32_bf16 v[12:15], v[112:115], a[0:3], v[12:15]\n"
"v_mfma_f32_16x16x32_bf16 v[12:15], v[116:119], a[4:7], v[12:15]\n"
"s_waitcnt lgkmcnt(0)\n"
"v_mfma_f32_16x16x32_bf16 v[12:15], v[120:123], a[8:11], v[12:15]\n"
"v_subrev_f32 v40, s22, v40\n"
"v_mfma_f32_16x16x32_bf16 v[12:15], v[224:227], a[12:15], v[12:15]\n"
"s_waitcnt vmcnt(5)\n"
"v_mfma_f32_16x16x32_bf16 v[64:67], v[162:165], v[72:75], 0\n"
"v_mfma_f32_16x16x32_bf16 v[64:67], v[162:165], v[76:79], v[64:67]\n"
"v_cvt_pk_bf16_f32 v9, v10, v11\n"
"v_mfma_f32_16x16x32_bf16 v[64:67], v[162:165], v[80:83], v[64:67]\n"
"s_nop 2\n"
"v_subrev_f32 v12, s22, v12\n"
"v_mfma_f32_16x16x32_bf16 v[64:67], v[162:165], v[84:87], v[64:67]\n"
"v_subrev_f32 v25, s22, v25\n"
"v_mfma_f32_16x16x32_bf16 v[64:67], v[162:165], v[88:91], v[64:67]\n"
"v_subrev_f32 v13, s22, v13\n"
"v_mfma_f32_16x16x32_bf16 v[64:67], v[162:165], v[92:95], v[64:67]\n"
"v_subrev_f32 v15, s22, v15\n"
"v_mfma_f32_16x16x32_bf16 v[64:67], v[162:165], v[244:247], v[64:67]\n"
"v_subrev_f32 v14, s22, v14\n"
"v_mfma_f32_16x16x32_bf16 v[64:67], v[162:165], v[156:159], v[64:67]\n"
"v_subrev_f32 v42, s22, v42\n"
"v_mfma_f32_16x16x32_bf16 v[68:71], v[160:163], v[72:75], 0\n"
"v_cvt_pk_bf16_f32 v11, v14, v15\n"
"v_subrev_f32 v41, s22, v41\n"
"v_mfma_f32_16x16x32_bf16 v[68:71], v[160:163], v[76:79], v[68:71]\n"
"v_cvt_pk_bf16_f32 v10, v12, v13\n"
"v_cvt_pk_bf16_f32 v24, v24, v25\n"
"v_mfma_f32_16x16x32_bf16 v[68:71], v[160:163], v[80:83], v[68:71]\n"
"v_mfma_f32_16x16x32_bf16 v[68:71], v[160:163], v[84:87], v[68:71]\n"
"v_permlane16_swap_b32 v9, v11\n"
"v_mfma_f32_16x16x32_bf16 v[68:71], v[160:163], v[88:91], v[68:71]\n"
"v_mfma_f32_16x16x32_bf16 v[68:71], v[160:163], v[92:95], v[68:71]\n"
"v_cvt_pk_bf16_f32 v64, v64, v65\n"
"v_cvt_pk_bf16_f32 v40, v40, v41\n"
"v_mfma_f32_16x16x32_bf16 v[68:71], v[160:163], v[244:247], v[68:71]\n"
"v_permlane16_swap_b32 v8, v10\n"
"v_mfma_f32_16x16x32_bf16 v[68:71], v[160:163], v[156:159], v[68:71]\n"
"buffer_store_dwordx4 v[8:11], v239, s[36:39], 0 offen sc0 sc1\n"
"v_mfma_f32_16x16x32_bf16 v[28:31], v[224:227], a[28:31], v[28:31]\n"
"v_mfma_f32_16x16x32_bf16 v[28:31], v[120:123], a[24:27], v[28:31]\n"
"v_cvt_pk_bf16_f32 v65, v66, v67\n"
"v_subrev_f32 v43, s22, v43\n"
"v_mfma_f32_16x16x32_bf16 v[28:31], v[116:119], a[20:23], v[28:31]\n"
"v_mfma_f32_16x16x32_bf16 v[28:31], v[112:115], a[16:19], v[28:31]\n"
"v_mfma_f32_16x16x32_bf16 v[28:31], v[108:111], a[76:79], v[28:31]\n"
"v_cvt_pk_bf16_f32 v66, v68, v69\n"
"v_cvt_pk_bf16_f32 v67, v70, v71\n"
"v_mfma_f32_16x16x32_bf16 v[28:31], v[104:107], a[72:75], v[28:31]\n"
"v_permlane16_swap_b32 v64, v66\n"
"v_mfma_f32_16x16x32_bf16 v[28:31], v[100:103], a[68:71], v[28:31]\n"
"v_permlane16_swap_b32 v65, v67\n"
"v_mfma_f32_16x16x32_bf16 v[28:31], v[96:99], a[64:67], v[28:31]\n"
"buffer_store_dwordx4 v[64:67], v253, s[36:39], 0 offen offset:128 sc0 sc1\n"
"v_cvt_pk_bf16_f32 v41, v42, v43\n"
"v_mfma_f32_16x16x32_bf16 v[44:47], v[96:99], a[80:83], v[44:47]\n"
"v_subrev_f32 v26, s22, v26\n"
"v_cvt_pk_bf16_f32 v25, v26, v27\n"
"v_mfma_f32_16x16x32_bf16 v[44:47], v[100:103], a[84:87], v[44:47]\n"
"v_mfma_f32_16x16x32_bf16 v[44:47], v[104:107], a[88:91], v[44:47]\n"
"s_nop 0\n"
"v_subrev_f32 v30, s22, v30\n"
"v_subrev_f32 v31, s22, v31\n"
"v_mfma_f32_16x16x32_bf16 v[44:47], v[108:111], a[92:95], v[44:47]\n"
"v_subrev_f32 v28, s22, v28\n"
"v_subrev_f32 v29, s22, v29\n"
"v_mfma_f32_16x16x32_bf16 v[44:47], v[112:115], a[32:35], v[44:47]\n"
"v_cvt_pk_bf16_f32 v26, v28, v29\n"
"v_cvt_pk_bf16_f32 v27, v30, v31\n"
"v_mfma_f32_16x16x32_bf16 v[44:47], v[116:119], a[36:39], v[44:47]\n"
"s_nop 0\n"
"v_permlane16_swap_b32 v25, v27\n"
"v_mfma_f32_16x16x32_bf16 v[44:47], v[120:123], a[40:43], v[44:47]\n"
"v_permlane16_swap_b32 v24, v26\n"
"v_mfma_f32_16x16x32_bf16 v[44:47], v[224:227], a[44:47], v[44:47]\n"
"buffer_store_dwordx4 v[24:27], v239, s[36:39], s52 offen sc0 sc1\n"
"s_nop 6\n"
"v_subrev_f32 v44, s22, v44\n"
"v_subrev_f32 v45, s22, v45\n"
"v_subrev_f32 v46, s22, v46\n"
"v_subrev_f32 v47, s22, v47\n"
"v_cvt_pk_bf16_f32 v42, v44, v45\n"
"v_cvt_pk_bf16_f32 v43, v46, v47\n"
"s_nop 0\n"
"v_permlane16_swap_b32 v40, v42\n"
"v_permlane16_swap_b32 v41, v43\n"
"buffer_store_dwordx4 v[40:43], v239, s[36:39], s53 offen sc0 sc1\n"
"s_branch LFIN_%=\n"
"LSW_%=:\n"
"s_add_u32 s16, s8, s95\n"
"s_addc_u32 s17, s9, 0\n"
"s_sub_u32 s18, 655360, s95\n"
"s_add_u32 s20, s8, s100\n"
"s_addc_u32 s21, s9, 0\n"
"s_sub_u32 s22, 655360, s100\n"
"s_mov_b32 s56, 0\n"
"s_mov_b32 s55, 0\n"
"s_mov_b32 s58, 0\n"
"s_mov_b32 s59, 163840\n"
"s_mov_b32 s52, 9216\n"
"s_mov_b32 s53, 18432\n"
"s_mov_b32 s54, 27648\n"
"v_lshrrev_b32 v206, 7, v242\n"
"v_mul_u32_u24 v206, 1280, v206\n"
"v_and_b32 v240, 0x7f, v240\n"
"v_add_u32 v240, v206, v240\n"
"buffer_load_dword v252, v240, s[16:19], 0 offen\n"
"s_branch LSWK_%=\n"
"LGP_%=:\n"
"s_mov_b32 s80, 0x40000\n"
"LGPa_%=:\n"
"s_sleep 1\n"
"global_load_dword v200, v201, s[6:7] sc1\n"
"s_sub_u32 s80, s80, 1\n"
"s_cmp_eq_u32 s80, 0\n"
"s_waitcnt vmcnt(0)\n"
"s_cbranch_scc1 LGPK_%=\n"
"v_subrev_u32 v200, s21, v200\n"
"v_cmp_lt_u32 vcc, s69, v200\n"
"s_nop 4\n"
"s_cbranch_vccnz LGPa_%=\n"
"s_branch LGPK_%=\n"
"LGQ_%=:\n"
"s_mov_b32 s80, 0x40000\n"
"LGQa_%=:\n"
"s_sleep 1\n"
"global_load_dword v200, v201, s[6:7] sc1\n"
"s_sub_u32 s80, s80, 1\n"
"s_cmp_eq_u32 s80, 0\n"
"s_waitcnt vmcnt(0)\n"
"s_cbranch_scc1 LGQK_%=\n"
"v_subrev_u32 v200, s21, v200\n"
"v_cmp_lt_u32 vcc, s69, v200\n"
"s_nop 4\n"
"s_cbranch_vccnz LGQa_%=\n"
"s_branch LGQK_%=\n"
"LGR_%=:\n"
"s_mov_b32 s80, 0x40000\n"
"LGRa_%=:\n"
"s_sleep 1\n"
"global_load_dword v200, v201, s[6:7] sc1\n"
"s_sub_u32 s80, s80, 1\n"
"s_cmp_eq_u32 s80, 0\n"
"s_waitcnt vmcnt(0)\n"
"s_cbranch_scc1 LGRK_%=\n"
"v_subrev_u32 v200, s21, v200\n"
"v_cmp_lt_u32 vcc, s69, v200\n"
"s_nop 4\n"
"s_cbranch_vccnz LGRa_%=\n"
"s_branch LGRK_%=\n"
"LGV_%=:\n"
"s_mov_b32 s80, 0x40000\n"
"LGVa_%=:\n"
"s_sleep 1\n"
"global_load_dword v200, v201, s[6:7] sc1\n"
"s_sub_u32 s80, s80, 1\n"
"s_cmp_eq_u32 s80, 0\n"
"s_waitcnt vmcnt(0)\n"
"s_cbranch_scc1 LGVK_%=\n"
"v_subrev_u32 v200, s21, v200\n"
"v_cmp_lt_u32 vcc, s69, v200\n"
"s_nop 4\n"
"s_cbranch_vccnz LGVa_%=\n"
"s_branch LGVK_%=\n"
"LEP_%=:\n"
"s_add_u32 s92, s92, 1\n"
"v_mov_b32 v200, s92\n"
"v_mov_b32 v255, 0\n"
"s_mov_b64 exec, 1\n"
"global_store_dword v255, v200, s[6:7] offset:128 sc1\n"
"s_mov_b64 exec, -1\n"
"s_branch LEPK_%=\n"
"LPCE0_%=:\n"
"s_memrealtime s[80:81]\n"
"s_sub_u32 s82, s67, s66\n"
"s_cmp_gt_i32 s82, 0\n"
"s_cselect_b32 s83, s67, s66\n"
"s_waitcnt lgkmcnt(0)\n"
"s_mov_b32 s63, 0\n"
"s_mov_b32 s62, 126\n"
"s_sub_u32 s82, s83, s80\n"
"s_cmp_gt_i32 s82, 3\n"
"s_cbranch_scc0 LPEE0_%=\n"
"LPDE0_%=:\n"
"s_sleep 1\n"
"s_memrealtime s[80:81]\n"
"s_waitcnt lgkmcnt(0)\n"
"s_sub_u32 s82, s83, s80\n"
"s_cmp_gt_i32 s82, 3\n"
"s_cbranch_scc1 LPDE0_%=\n"
"LPEE0_%=:\n"
"s_add_u32 s66, s66, s72\n"
"s_add_u32 s66, s66, 4\n"
"s_add_u32 s67, s80, s72\n"
"s_sub_u32 s72, s72, 0\n"
"s_max_u32 s72, s72, s62\n"
"s_branch LPKE0_%=\n"
"LPCE1_%=:\n"
"s_memrealtime s[80:81]\n"
"s_sub_u32 s82, s67, s66\n"
"s_cmp_gt_i32 s82, 0\n"
"s_cselect_b32 s83, s67, s66\n"
"s_waitcnt lgkmcnt(0)\n"
"s_sub_u32 s82, s83, s80\n"
"s_cmp_gt_i32 s82, 3\n"
"s_cbranch_scc0 LPEE1_%=\n"
"LPDE1_%=:\n"
"s_sleep 1\n"
"s_memrealtime s[80:81]\n"
"s_waitcnt lgkmcnt(0)\n"
"s_sub_u32 s82, s83, s80\n"
"s_cmp_gt_i32 s82, 3\n"
"s_cbranch_scc1 LPDE1_%=\n"
"LPEE1_%=:\n"
"s_add_u32 s66, s66, s72\n"
"s_add_u32 s66, s66, 4\n"
"s_add_u32 s67, s80, s72\n"
"s_sub_u32 s72, s72, 0\n"
"s_max_u32 s72, s72, s62\n"
"s_branch LPKE1_%=\n"
"LPCT0_%=:\n"
"s_memrealtime s[80:81]\n"
"s_sub_u32 s82, s67, s66\n"
"s_cmp_gt_i32 s82, 0\n"
"s_cselect_b32 s83, s67, s66\n"
"s_waitcnt lgkmcnt(0)\n"
"s_sub_u32 s82, s83, s80\n"
"s_cmp_gt_i32 s82, 3\n"
"s_cbranch_scc0 LPET0_%=\n"
"LPDT0_%=:\n"
"s_sleep 1\n"
"s_memrealtime s[80:81]\n"
"s_waitcnt lgkmcnt(0)\n"
"s_sub_u32 s82, s83, s80\n"
"s_cmp_gt_i32 s82, 3\n"
"s_cbranch_scc1 LPDT0_%=\n"
"LPET0_%=:\n"
"s_add_u32 s66, s66, s72\n"
"s_add_u32 s66, s66, 4\n"
"s_add_u32 s67, s80, s72\n"
"s_sub_u32 s72, s72, 0\n"
"s_max_u32 s72, s72, s62\n"
"s_branch LPKT0_%=\n"
"LPCT1_%=:\n"
"s_memrealtime s[80:81]\n"
"s_sub_u32 s82, s67, s66\n"
"s_cmp_gt_i32 s82, 0\n"
"s_cselect_b32 s83, s67, s66\n"
"s_waitcnt lgkmcnt(0)\n"
"s_sub_u32 s82, s83, s80\n"
"s_cmp_gt_i32 s82, 3\n"
"s_cbranch_scc0 LPET1_%=\n"
"LPDT1_%=:\n"
"s_sleep 1\n"
"s_memrealtime s[80:81]\n"
"s_waitcnt lgkmcnt(0)\n"
"s_sub_u32 s82, s83, s80\n"
"s_cmp_gt_i32 s82, 3\n"
"s_cbranch_scc1 LPDT1_%=\n"
"LPET1_%=:\n"
"s_add_u32 s66, s66, s72\n"
"s_add_u32 s66, s66, 4\n"
"s_add_u32 s67, s80, s72\n"
"s_sub_u32 s72, s72, 0\n"
"s_max_u32 s72, s72, s62\n"
"s_branch LPKT1_%=\n"
"LPCL0_%=:\n"
"s_memrealtime s[80:81]\n"
"s_sub_u32 s82, s67, s66\n"
"s_cmp_gt_i32 s82, 0\n"
"s_cselect_b32 s83, s67, s66\n"
"s_waitcnt lgkmcnt(0)\n"
"s_sub_u32 s82, s80, s67\n"
"s_add_u32 s63, s63, 1\n"
"s_min_i32 s81, s63, 0\n"
"s_cmp_gt_i32 s82, 20\n"
"s_cselect_b32 s63, s63, s81\n"
"s_cmp_ge_i32 s63, 2\n"
"s_cbranch_scc0 LPRL0_%=\n"
"s_mov_b32 s63, -9\n"
"s_add_u32 s62, s62, 10\n"
"s_min_u32 s62, s62, 146\n"
"s_max_u32 s72, s72, s62\n"
"s_add_u32 s83, s80, 200\n"
"s_and_b32 s83, s83, -1\n"
"LPRL0_%=:\n"
"s_sub_u32 s82, s83, s80\n"
"s_cmp_gt_i32 s82, 3\n"
"s_cbranch_scc0 LPEL0_%=\n"
"LPDL0_%=:\n"
"s_sleep 1\n"
"s_memrealtime s[80:81]\n"
"s_waitcnt lgkmcnt(0)\n"
"s_sub_u32 s82, s83, s80\n"
"s_cmp_gt_i32 s82, 3\n"
"s_cbranch_scc1 LPDL0_%=\n"
"LPEL0_%=:\n"
"s_add_u32 s66, s66, s72\n"
"s_add_u32 s66, s66, 4\n"
"s_add_u32 s67, s80, s72\n"
"s_sub_u32 s72, s72, 0\n"
"s_max_u32 s72, s72, s62\n"
"s_branch LPKL0_%=\n"
"LPCL1_%=:\n"
"s_memrealtime s[80:81]\n"
"s_sub_u32 s82, s67, s66\n"
"s_cmp_gt_i32 s82, 0\n"
"s_cselect_b32 s83, s67, s66\n"
"s_waitcnt lgkmcnt(0)\n"
"s_sub_u32 s82, s80, s67\n"
"s_add_u32 s63, s63, 1\n"
"s_min_i32 s81, s63, 0\n"
"s_cmp_gt_i32 s82, 20\n"
"s_cselect_b32 s63, s63, s81\n"
"s_cmp_ge_i32 s63, 2\n"
"s_cbranch_scc0 LPRL1_%=\n"
"s_mov_b32 s63, -9\n"
"s_add_u32 s62, s62, 10\n"
"s_min_u32 s62, s62, 146\n"
"s_max_u32 s72, s72, s62\n"
"s_add_u32 s83, s80, 200\n"
"s_and_b32 s83, s83, -1\n"
"LPRL1_%=:\n"
"s_sub_u32 s82, s83, s80\n"
"s_cmp_gt_i32 s82, 3\n"
"s_cbranch_scc0 LPEL1_%=\n"
"LPDL1_%=:\n"
"s_sleep 1\n"
"s_memrealtime s[80:81]\n"
"s_waitcnt lgkmcnt(0)\n"
"s_sub_u32 s82, s83, s80\n"
"s_cmp_gt_i32 s82, 3\n"
"s_cbranch_scc1 LPDL1_%=\n"
"LPEL1_%=:\n"
"s_add_u32 s66, s66, s72\n"
"s_add_u32 s66, s66, 4\n"
"s_add_u32 s67, s80, s72\n"
"s_sub_u32 s72, s72, 0\n"
"s_max_u32 s72, s72, s62\n"
"s_branch LPKL1_%=\n"
"LPCL2_%=:\n"
"s_memrealtime s[80:81]\n"
"s_sub_u32 s82, s67, s66\n"
"s_cmp_gt_i32 s82, 0\n"
"s_cselect_b32 s83, s67, s66\n"
"s_waitcnt lgkmcnt(0)\n"
"s_sub_u32 s82, s80, s67\n"
"s_add_u32 s63, s63, 1\n"
"s_min_i32 s81, s63, 0\n"
"s_cmp_gt_i32 s82, 20\n"
"s_cselect_b32 s63, s63, s81\n"
"s_cmp_ge_i32 s63, 2\n"
"s_cbranch_scc0 LPRL2_%=\n"
"s_mov_b32 s63, -9\n"
"s_add_u32 s62, s62, 10\n"
"s_min_u32 s62, s62, 146\n"
"s_max_u32 s72, s72, s62\n"
"s_add_u32 s83, s80, 200\n"
"s_and_b32 s83, s83, -1\n"
"LPRL2_%=:\n"
"s_sub_u32 s82, s83, s80\n"
"s_cmp_gt_i32 s82, 3\n"
"s_cbranch_scc0 LPEL2_%=\n"
"LPDL2_%=:\n"
"s_sleep 1\n"
"s_memrealtime s[80:81]\n"
"s_waitcnt lgkmcnt(0)\n"
"s_sub_u32 s82, s83, s80\n"
"s_cmp_gt_i32 s82, 3\n"
"s_cbranch_scc1 LPDL2_%=\n"
"LPEL2_%=:\n"
"s_add_u32 s66, s66, s72\n"
"s_add_u32 s66, s66, 4\n"
"s_add_u32 s67, s80, s72\n"
"s_sub_u32 s72, s72, 0\n"
"s_max_u32 s72, s72, s62\n"
"s_branch LPKL2_%=\n"
"LPCL3_%=:\n"
"s_memrealtime s[80:81]\n"
"s_sub_u32 s82, s67, s66\n"
"s_cmp_gt_i32 s82, 0\n"
"s_cselect_b32 s83, s67, s66\n"
"s_waitcnt lgkmcnt(0)\n"
"s_sub_u32 s82, s80, s67\n"
"s_add_u32 s63, s63, 1\n"
"s_min_i32 s81, s63, 0\n"
"s_cmp_gt_i32 s82, 20\n"
"s_cselect_b32 s63, s63, s81\n"
"s_cmp_ge_i32 s63, 2\n"
"s_cbranch_scc0 LPRL3_%=\n"
"s_mov_b32 s63, -9\n"
"s_add_u32 s62, s62, 10\n"
"s_min_u32 s62, s62, 146\n"
"s_max_u32 s72, s72, s62\n"
"s_add_u32 s83, s80, 200\n"
"s_and_b32 s83, s83, -1\n"
"LPRL3_%=:\n"
"s_sub_u32 s82, s83, s80\n"
"s_cmp_gt_i32 s82, 3\n"
"s_cbranch_scc0 LPEL3_%=\n"
"LPDL3_%=:\n"
"s_sleep 1\n"
"s_memrealtime s[80:81]\n"
"s_waitcnt lgkmcnt(0)\n"
"s_sub_u32 s82, s83, s80\n"
"s_cmp_gt_i32 s82, 3\n"
"s_cbranch_scc1 LPDL3_%=\n"
"LPEL3_%=:\n"
"s_add_u32 s66, s66, s72\n"
"s_add_u32 s66, s66, 4\n"
"s_add_u32 s67, s80, s72\n"
"s_sub_u32 s72, s72, 0\n"
"s_max_u32 s72, s72, s62\n"
"s_branch LPKL3_%=\n"
"LEND_%=:\n"
"LSLOW_%=:\n"
"s_waitcnt vmcnt(0) lgkmcnt(0)\n"
"s_nop 9\n"
"v_bfe_u32 v148, v242, 4, 4\n"
"v_bfe_u32 v149, v242, 8, 2\n"
"v_lshlrev_b32 v144, 8, v148\n"
"v_lshl_add_u32 v144, v149, 4, v144\n"
"v_add_u32 v144, s43, v144\n"
"v_xor_b32 v145, 64, v144\n"
"v_lshlrev_b32 v146, 1, v148\n"
"v_lshl_add_u32 v146, v149, 10, v146\n"
"v_add_u32 v146, s43, v146\n"
"v_xor_b32 v147, 64, v146\n"
"v_mov_b32 v152, 0xff7fffff\n"
"v_mov_b32 v155, 0\n"
"v_mov_b32 v0, 0\n"
"v_mov_b32 v1, 0\n"
"v_mov_b32 v2, 0\n"
"v_mov_b32 v3, 0\n"
"v_mov_b32 v4, 0\n"
"v_mov_b32 v5, 0\n"
"v_mov_b32 v6, 0\n"
"v_mov_b32 v7, 0\n"
"v_mov_b32 v8, 0\n"
"v_mov_b32 v9, 0\n"
"v_mov_b32 v10, 0\n"
"v_mov_b32 v11, 0\n"
"v_mov_b32 v12, 0\n"
"v_mov_b32 v13, 0\n"
"v_mov_b32 v14, 0\n"
"v_mov_b32 v15, 0\n"
"v_mov_b32 v16, 0\n"
"v_mov_b32 v17, 0\n"
"v_mov_b32 v18, 0\n"
"v_mov_b32 v19, 0\n"
"v_mov_b32 v20, 0\n"
"v_mov_b32 v21, 0\n"
"v_mov_b32 v22, 0\n"
"v_mov_b32 v23, 0\n"
"v_mov_b32 v24, 0\n"
"v_mov_b32 v25, 0\n"
"v_mov_b32 v26, 0\n"
"v_mov_b32 v27, 0\n"
"v_mov_b32 v28, 0\n"
"v_mov_b32 v29, 0\n"
"v_mov_b32 v30, 0\n"
"v_mov_b32 v31, 0\n"
"v_mov_b32 v156, 0xff7fffff\n"
"v_mov_b32 v159, 0\n"
"v_mov_b32 v32, 0\n"
"v_mov_b32 v33, 0\n"
"v_mov_b32 v34, 0\n"
"v_mov_b32 v35, 0\n"
"v_mov_b32 v36, 0\n"
"v_mov_b32 v37, 0\n"
"v_mov_b32 v38, 0\n"
"v_mov_b32 v39, 0\n"
"v_mov_b32 v40, 0\n"
"v_mov_b32 v41, 0\n"
"v_mov_b32 v42, 0\n"
"v_mov_b32 v43, 0\n"
"v_mov_b32 v44, 0\n"
"v_mov_b32 v45, 0\n"
"v_mov_b32 v46, 0\n"
"v_mov_b32 v47, 0\n"
"v_mov_b32 v48, 0\n"
"v_mov_b32 v49, 0\n"
"v_mov_b32 v50, 0\n"
"v_mov_b32 v51, 0\n"
"v_mov_b32 v52, 0\n"
"v_mov_b32 v53, 0\n"
"v_mov_b32 v54, 0\n"
"v_mov_b32 v55, 0\n"
"v_mov_b32 v56, 0\n"
"v_mov_b32 v57, 0\n"
"v_mov_b32 v58, 0\n"
"v_mov_b32 v59, 0\n"
"v_mov_b32 v60, 0\n"
"v_mov_b32 v61, 0\n"
"v_mov_b32 v62, 0\n"
"v_mov_b32 v63, 0\n"
"v_mov_b32 v160, 0xff7fffff\n"
"v_mov_b32 v163, 0\n"
"v_mov_b32 v64, 0\n"
"v_mov_b32 v65, 0\n"
"v_mov_b32 v66, 0\n"
"v_mov_b32 v67, 0\n"
"v_mov_b32 v68, 0\n"
"v_mov_b32 v69, 0\n"
"v_mov_b32 v70, 0\n"
"v_mov_b32 v71, 0\n"
"v_mov_b32 v72, 0\n"
"v_mov_b32 v73, 0\n"
"v_mov_b32 v74, 0\n"
"v_mov_b32 v75, 0\n"
"v_mov_b32 v76, 0\n"
"v_mov_b32 v77, 0\n"
"v_mov_b32 v78, 0\n"
"v_mov_b32 v79, 0\n"
"v_mov_b32 v80, 0\n"
"v_mov_b32 v81, 0\n"
"v_mov_b32 v82, 0\n"
"v_mov_b32 v83, 0\n"
"v_mov_b32 v84, 0\n"
"v_mov_b32 v85, 0\n"
"v_mov_b32 v86, 0\n"
"v_mov_b32 v87, 0\n"
"v_mov_b32 v88, 0\n"
"v_mov_b32 v89, 0\n"
"v_mov_b32 v90, 0\n"
"v_mov_b32 v91, 0\n"
"v_mov_b32 v92, 0\n"
"v_mov_b32 v93, 0\n"
"v_mov_b32 v94, 0\n"
"v_mov_b32 v95, 0\n"
"v_mov_b32 v164, 0xff7fffff\n"
"v_mov_b32 v167, 0\n"
"v_accvgpr_write_b32 a208, 0\n"
"v_accvgpr_write_b32 a209, 0\n"
"v_accvgpr_write_b32 a210, 0\n"
"v_accvgpr_write_b32 a211, 0\n"
"v_accvgpr_write_b32 a212, 0\n"
"v_accvgpr_write_b32 a213, 0\n"
"v_accvgpr_write_b32 a214, 0\n"
"v_accvgpr_write_b32 a215, 0\n"
"v_accvgpr_write_b32 a216, 0\n"
"v_accvgpr_write_b32 a217, 0\n"
"v_accvgpr_write_b32 a218, 0\n"
"v_accvgpr_write_b32 a219, 0\n"
"v_accvgpr_write_b32 a220, 0\n"
"v_accvgpr_write_b32 a221, 0\n"
"v_accvgpr_write_b32 a222, 0\n"
"v_accvgpr_write_b32 a223, 0\n"
"v_accvgpr_write_b32 a224, 0\n"
"v_accvgpr_write_b32 a225, 0\n"
"v_accvgpr_write_b32 a226, 0\n"
"v_accvgpr_write_b32 a227, 0\n"
"v_accvgpr_write_b32 a228, 0\n"
"v_accvgpr_write_b32 a229, 0\n"
"v_accvgpr_write_b32 a230, 0\n"
"v_accvgpr_write_b32 a231, 0\n"
"v_accvgpr_write_b32 a232, 0\n"
"v_accvgpr_write_b32 a233, 0\n"
"v_accvgpr_write_b32 a234, 0\n"
"v_accvgpr_write_b32 a235, 0\n"
"v_accvgpr_write_b32 a236, 0\n"
"v_accvgpr_write_b32 a237, 0\n"
"v_accvgpr_write_b32 a238, 0\n"
"v_accvgpr_write_b32 a239, 0\n"
"s_lshl_b32 s80, s47, 16\n"
"s_sub_u32 s80, s80, 261248\n"
"s_add_u32 s81, s80, 4096\n"
"s_mov_b32 s82, 0\n"
"LSLOOP_%=:\n"
"s_lshl_b32 s83, s82, 6\n"
"s_and_b32 s83, s83, 64\n"
"buffer_load_dwordx4 v[128:131], v144, s[84:87], s80 offen offset:0\n"
"buffer_load_dwordx4 v[132:135], v145, s[84:87], s80 offen offset:0\n"
"buffer_load_dwordx4 v[136:139], v144, s[84:87], s80 offen offset:128\n"
"buffer_load_dwordx4 v[140:143], v145, s[84:87], s80 offen offset:128\n"
"s_waitcnt vmcnt(0)\n"
"v_mfma_f32_16x16x32_bf16 v[96:99], v[128:131], a[0:3], 0\n"
"v_mfma_f32_16x16x32_bf16 v[96:99], v[132:135], a[4:7], v[96:99]\n"
"v_mfma_f32_16x16x32_bf16 v[96:99], v[136:139], a[8:11], v[96:99]\n"
"v_mfma_f32_16x16x32_bf16 v[96:99], v[140:143], a[12:15], v[96:99]\n"
"v_mfma_f32_16x16x32_bf16 v[104:107], v[128:131], a[16:19], 0\n"
"v_mfma_f32_16x16x32_bf16 v[104:107], v[132:135], a[20:23], v[104:107]\n"
"v_mfma_f32_16x16x32_bf16 v[104:107], v[136:139], a[24:27], v[104:107]\n"
"v_mfma_f32_16x16x32_bf16 v[104:107], v[140:143], a[28:31], v[104:107]\n"
"v_mfma_f32_16x16x32_bf16 v[112:115], v[128:131], a[32:35], 0\n"
"v_mfma_f32_16x16x32_bf16 v[112:115], v[132:135], a[36:39], v[112:115]\n"
"v_mfma_f32_16x16x32_bf16 v[112:115], v[136:139], a[40:43], v[112:115]\n"
"v_mfma_f32_16x16x32_bf16 v[112:115], v[140:143], a[44:47], v[112:115]\n"
"v_mfma_f32_16x16x32_bf16 v[120:123], v[128:131], a[192:195], 0\n"
"v_mfma_f32_16x16x32_bf16 v[120:123], v[132:135], a[196:199], v[120:123]\n"
"v_mfma_f32_16x16x32_bf16 v[120:123], v[136:139], a[200:203], v[120:123]\n"
"v_mfma_f32_16x16x32_bf16 v[120:123], v[140:143], a[204:207], v[120:123]\n"
"s_nop 7\n"
"buffer_load_dwordx4 v[128:131], v144, s[84:87], s81 offen offset:0\n"
"buffer_load_dwordx4 v[132:135], v145, s[84:87], s81 offen offset:0\n"
"buffer_load_dwordx4 v[136:139], v144, s[84:87], s81 offen offset:128\n"
"buffer_load_dwordx4 v[140:143], v145, s[84:87], s81 offen offset:128\n"
"s_waitcnt vmcnt(0)\n"
"v_mfma_f32_16x16x32_bf16 v[100:103], v[128:131], a[0:3], 0\n"
"v_mfma_f32_16x16x32_bf16 v[100:103], v[132:135], a[4:7], v[100:103]\n"
"v_mfma_f32_16x16x32_bf16 v[100:103], v[136:139], a[8:11], v[100:103]\n"
"v_mfma_f32_16x16x32_bf16 v[100:103], v[140:143], a[12:15], v[100:103]\n"
"v_mfma_f32_16x16x32_bf16 v[108:111], v[128:131], a[16:19], 0\n"
"v_mfma_f32_16x16x32_bf16 v[108:111], v[132:135], a[20:23], v[108:111]\n"
"v_mfma_f32_16x16x32_bf16 v[108:111], v[136:139], a[24:27], v[108:111]\n"
"v_mfma_f32_16x16x32_bf16 v[108:111], v[140:143], a[28:31], v[108:111]\n"
"v_mfma_f32_16x16x32_bf16 v[116:119], v[128:131], a[32:35], 0\n"
"v_mfma_f32_16x16x32_bf16 v[116:119], v[132:135], a[36:39], v[116:119]\n"
"v_mfma_f32_16x16x32_bf16 v[116:119], v[136:139], a[40:43], v[116:119]\n"
"v_mfma_f32_16x16x32_bf16 v[116:119], v[140:143], a[44:47], v[116:119]\n"
"v_mfma_f32_16x16x32_bf16 v[124:127], v[128:131], a[192:195], 0\n"
"v_mfma_f32_16x16x32_bf16 v[124:127], v[132:135], a[196:199], v[124:127]\n"
"v_mfma_f32_16x16x32_bf16 v[124:127], v[136:139], a[200:203], v[124:127]\n"
"v_mfma_f32_16x16x32_bf16 v[124:127], v[140:143], a[204:207], v[124:127]\n"
"s_nop 7\n"
"v_max3_f32 v153, v96, v97, v98\n"
"v_max3_f32 v153, v153, v99, v100\n"
"v_max3_f32 v153, v153, v101, v102\n"
"v_max3_f32 v153, v153, v103, v152\n"
"v_mov_b32 v154, v153\n"
"s_nop 1\n"
"v_permlane32_swap_b32 v153, v154\n"
"v_max_f32 v153, v153, v154\n"
"v_mov_b32 v154, v153\n"
"s_nop 1\n"
"v_permlane16_swap_b32 v153, v154\n"
"v_max_f32 v153, v153, v154\n"
"v_sub_f32 v154, v152, v153\n"
"v_exp_f32 v154, v154\n"
"v_mov_b32 v152, v153\n"
"v_sub_f32 v96, v96, v152\n"
"v_sub_f32 v97, v97, v152\n"
"v_sub_f32 v98, v98, v152\n"
"v_sub_f32 v99, v99, v152\n"
"v_sub_f32 v100, v100, v152\n"
"v_sub_f32 v101, v101, v152\n"
"v_sub_f32 v102, v102, v152\n"
"v_sub_f32 v103, v103, v152\n"
"v_exp_f32 v96, v96\n"
"v_exp_f32 v97, v97\n"
"v_exp_f32 v98, v98\n"
"v_exp_f32 v99, v99\n"
"v_exp_f32 v100, v100\n"
"v_exp_f32 v101, v101\n"
"v_exp_f32 v102, v102\n"
"v_exp_f32 v103, v103\n"
"v_mul_f32 v155, v155, v154\n"
"v_add_f32 v155, v155, v96\n"
"v_add_f32 v155, v155, v97\n"
"v_add_f32 v155, v155, v98\n"
"v_add_f32 v155, v155, v99\n"
"v_add_f32 v155, v155, v100\n"
"v_add_f32 v155, v155, v101\n"
"v_add_f32 v155, v155, v102\n"
"v_add_f32 v155, v155, v103\n"
"v_cvt_pk_bf16_f32 v96, v96, v97\n"
"v_cvt_pk_bf16_f32 v97, v98, v99\n"
"v_cvt_pk_bf16_f32 v98, v100, v101\n"
"v_cvt_pk_bf16_f32 v99, v102, v103\n"
"v_mov_b32_dpp v154, v154 row_ror:12 row_mask:0x2 bank_mask:0xf\n"
"s_nop 1\n"
"v_mov_b32_dpp v154, v154 row_ror:8 row_mask:0x4 bank_mask:0xf\n"
"s_nop 1\n"
"v_mov_b32_dpp v154, v154 row_ror:4 row_mask:0x8 bank_mask:0xf\n"
"s_nop 1\n"
"v_mul_f32_dpp v0, v154, v0 row_newbcast:0 row_mask:0xf bank_mask:0xf\n"
"v_mul_f32_dpp v1, v154, v1 row_newbcast:1 row_mask:0xf bank_mask:0xf\n"
"v_mul_f32_dpp v2, v154, v2 row_newbcast:2 row_mask:0xf bank_mask:0xf\n"
"v_mul_f32_dpp v3, v154, v3 row_newbcast:3 row_mask:0xf bank_mask:0xf\n"
"v_mul_f32_dpp v4, v154, v4 row_newbcast:0 row_mask:0xf bank_mask:0xf\n"
"v_mul_f32_dpp v5, v154, v5 row_newbcast:1 row_mask:0xf bank_mask:0xf\n"
"v_mul_f32_dpp v6, v154, v6 row_newbcast:2 row_mask:0xf bank_mask:0xf\n"
"v_mul_f32_dpp v7, v154, v7 row_newbcast:3 row_mask:0xf bank_mask:0xf\n"
"v_mul_f32_dpp v8, v154, v8 row_newbcast:0 row_mask:0xf bank_mask:0xf\n"
"v_mul_f32_dpp v9, v154, v9 row_newbcast:1 row_mask:0xf bank_mask:0xf\n"
"v_mul_f32_dpp v10, v154, v10 row_newbcast:2 row_mask:0xf bank_mask:0xf\n"
"v_mul_f32_dpp v11, v154, v11 row_newbcast:3 row_mask:0xf bank_mask:0xf\n"
"v_mul_f32_dpp v12, v154, v12 row_newbcast:0 row_mask:0xf bank_mask:0xf\n"
"v_mul_f32_dpp v13, v154, v13 row_newbcast:1 row_mask:0xf bank_mask:0xf\n"
"v_mul_f32_dpp v14, v154, v14 row_newbcast:2 row_mask:0xf bank_mask:0xf\n"
"v_mul_f32_dpp v15, v154, v15 row_newbcast:3 row_mask:0xf bank_mask:0xf\n"
"v_mul_f32_dpp v16, v154, v16 row_newbcast:0 row_mask:0xf bank_mask:0xf\n"
"v_mul_f32_dpp v17, v154, v17 row_newbcast:1 row_mask:0xf bank_mask:0xf\n"
"v_mul_f32_dpp v18, v154, v18 row_newbcast:2 row_mask:0xf bank_mask:0xf\n"
"v_mul_f32_dpp v19, v154, v19 row_newbcast:3 row_mask:0xf bank_mask:0xf\n"
"v_mul_f32_dpp v20, v154, v20 row_newbcast:0 row_mask:0xf bank_mask:0xf\n"
"v_mul_f32_dpp v21, v154, v21 row_newbcast:1 row_mask:0xf bank_mask:0xf\n"
"v_mul_f32_dpp v22, v154, v22 row_newbcast:2 row_mask:0xf bank_mask:0xf\n"
"v_mul_f32_dpp v23, v154, v23 row_newbcast:3 row_mask:0xf bank_mask:0xf\n"
"v_mul_f32_dpp v24, v154, v24 row_newbcast:0 row_mask:0xf bank_mask:0xf\n"
"v_mul_f32_dpp v25, v154, v25 row_newbcast:1 row_mask:0xf bank_mask:0xf\n"
"v_mul_f32_dpp v26, v154, v26 row_newbcast:2 row_mask:0xf bank_mask:0xf\n"
"v_mul_f32_dpp v27, v154, v27 row_newbcast:3 row_mask:0xf bank_mask:0xf\n"
"v_mul_f32_dpp v28, v154, v28 row_newbcast:0 row_mask:0xf bank_mask:0xf\n"
"v_mul_f32_dpp v29, v154, v29 row_newbcast:1 row_mask:0xf bank_mask:0xf\n"
"v_mul_f32_dpp v30, v154, v30 row_newbcast:2 row_mask:0xf bank_mask:0xf\n"
"v_mul_f32_dpp v31, v154, v31 row_newbcast:3 row_mask:0xf bank_mask:0xf\n"
"v_max3_f32 v157, v104, v105, v106\n"
"v_max3_f32 v157, v157, v107, v108\n"
"v_max3_f32 v157, v157, v109, v110\n"
"v_max3_f32 v157, v157, v111, v156\n"
"v_mov_b32 v158, v157\n"
"s_nop 1\n"
"v_permlane32_swap_b32 v157, v158\n"
"v_max_f32 v157, v157, v158\n"
"v_mov_b32 v158, v157\n"
"s_nop 1\n"
"v_permlane16_swap_b32 v157, v158\n"
"v_max_f32 v157, v157, v158\n"
"v_sub_f32 v158, v156, v157\n"
"v_exp_f32 v158, v158\n"
"v_mov_b32 v156, v157\n"
"v_sub_f32 v104, v104, v156\n"
"v_sub_f32 v105, v105, v156\n"
"v_sub_f32 v106, v106, v156\n"
"v_sub_f32 v107, v107, v156\n"
"v_sub_f32 v108, v108, v156\n"
"v_sub_f32 v109, v109, v156\n"
"v_sub_f32 v110, v110, v156\n"
"v_sub_f32 v111, v111, v156\n"
"v_exp_f32 v104, v104\n"
"v_exp_f32 v105, v105\n"
"v_exp_f32 v106, v106\n"
"v_exp_f32 v107, v107\n"
"v_exp_f32 v108, v108\n"
"v_exp_f32 v109, v109\n"
"v_exp_f32 v110, v110\n"
"v_exp_f32 v111, v111\n"
"v_mul_f32 v159, v159, v158\n"
"v_add_f32 v159, v159, v104\n"
"v_add_f32 v159, v159, v105\n"
"v_add_f32 v159, v159, v106\n"
"v_add_f32 v159, v159, v107\n"
"v_add_f32 v159, v159, v108\n"
"v_add_f32 v159, v159, v109\n"
"v_add_f32 v159, v159, v110\n"
"v_add_f32 v159, v159, v111\n"
"v_cvt_pk_bf16_f32 v104, v104, v105\n"
"v_cvt_pk_bf16_f32 v105, v106, v107\n"
"v_cvt_pk_bf16_f32 v106, v108, v109\n"
"v_cvt_pk_bf16_f32 v107, v110, v111\n"
"v_mov_b32_dpp v158, v158 row_ror:12 row_mask:0x2 bank_mask:0xf\n"
"s_nop 1\n"
"v_mov_b32_dpp v158, v158 row_ror:8 row_mask:0x4 bank_mask:0xf\n"
"s_nop 1\n"
"v_mov_b32_dpp v158, v158 row_ror:4 row_mask:0x8 bank_mask:0xf\n"
"s_nop 1\n"
"v_mul_f32_dpp v32, v158, v32 row_newbcast:0 row_mask:0xf bank_mask:0xf\n"
"v_mul_f32_dpp v33, v158, v33 row_newbcast:1 row_mask:0xf bank_mask:0xf\n"
"v_mul_f32_dpp v34, v158, v34 row_newbcast:2 row_mask:0xf bank_mask:0xf\n"
"v_mul_f32_dpp v35, v158, v35 row_newbcast:3 row_mask:0xf bank_mask:0xf\n"
"v_mul_f32_dpp v36, v158, v36 row_newbcast:0 row_mask:0xf bank_mask:0xf\n"
"v_mul_f32_dpp v37, v158, v37 row_newbcast:1 row_mask:0xf bank_mask:0xf\n"
"v_mul_f32_dpp v38, v158, v38 row_newbcast:2 row_mask:0xf bank_mask:0xf\n"
"v_mul_f32_dpp v39, v158, v39 row_newbcast:3 row_mask:0xf bank_mask:0xf\n"
"v_mul_f32_dpp v40, v158, v40 row_newbcast:0 row_mask:0xf bank_mask:0xf\n"
"v_mul_f32_dpp v41, v158, v41 row_newbcast:1 row_mask:0xf bank_mask:0xf\n"
"v_mul_f32_dpp v42, v158, v42 row_newbcast:2 row_mask:0xf bank_mask:0xf\n"
"v_mul_f32_dpp v43, v158, v43 row_newbcast:3 row_mask:0xf bank_mask:0xf\n"
"v_mul_f32_dpp v44, v158, v44 row_newbcast:0 row_mask:0xf bank_mask:0xf\n"
"v_mul_f32_dpp v45, v158, v45 row_newbcast:1 row_mask:0xf bank_mask:0xf\n"
"v_mul_f32_dpp v46, v158, v46 row_newbcast:2 row_mask:0xf bank_mask:0xf\n"
"v_mul_f32_dpp v47, v158, v47 row_newbcast:3 row_mask:0xf bank_mask:0xf\n"
"v_mul_f32_dpp v48, v158, v48 row_newbcast:0 row_mask:0xf bank_mask:0xf\n"
"v_mul_f32_dpp v49, v158, v49 row_newbcast:1 row_mask:0xf bank_mask:0xf\n"
"v_mul_f32_dpp v50, v158, v50 row_newbcast:2 row_mask:0xf bank_mask:0xf\n"
"v_mul_f32_dpp v51, v158, v51 row_newbcast:3 row_mask:0xf bank_mask:0xf\n"
"v_mul_f32_dpp v52, v158, v52 row_newbcast:0 row_mask:0xf bank_mask:0xf\n"
"v_mul_f32_dpp v53, v158, v53 row_newbcast:1 row_mask:0xf bank_mask:0xf\n"
"v_mul_f32_dpp v54, v158, v54 row_newbcast:2 row_mask:0xf bank_mask:0xf\n"
"v_mul_f32_dpp v55, v158, v55 row_newbcast:3 row_mask:0xf bank_mask:0xf\n"
"v_mul_f32_dpp v56, v158, v56 row_newbcast:0 row_mask:0xf bank_mask:0xf\n"
"v_mul_f32_dpp v57, v158, v57 row_newbcast:1 row_mask:0xf bank_mask:0xf\n"
"v_mul_f32_dpp v58, v158, v58 row_newbcast:2 row_mask:0xf bank_mask:0xf\n"
"v_mul_f32_dpp v59, v158, v59 row_newbcast:3 row_mask:0xf bank_mask:0xf\n"
"v_mul_f32_dpp v60, v158, v60 row_newbcast:0 row_mask:0xf bank_mask:0xf\n"
"v_mul_f32_dpp v61, v158, v61 row_newbcast:1 row_mask:0xf bank_mask:0xf\n"
"v_mul_f32_dpp v62, v158, v62 row_newbcast:2 row_mask:0xf bank_mask:0xf\n"
"v_mul_f32_dpp v63, v158, v63 row_newbcast:3 row_mask:0xf bank_mask:0xf\n"
"v_max3_f32 v161, v112, v113, v114\n"
"v_max3_f32 v161, v161, v115, v116\n"
"v_max3_f32 v161, v161, v117, v118\n"
"v_max3_f32 v161, v161, v119, v160\n"
"v_mov_b32 v162, v161\n"
"s_nop 1\n"
"v_permlane32_swap_b32 v161, v162\n"
"v_max_f32 v161, v161, v162\n"
"v_mov_b32 v162, v161\n"
"s_nop 1\n"
"v_permlane16_swap_b32 v161, v162\n"
"v_max_f32 v161, v161, v162\n"
"v_sub_f32 v162, v160, v161\n"
"v_exp_f32 v162, v162\n"
"v_mov_b32 v160, v161\n"
"v_sub_f32 v112, v112, v160\n"
"v_sub_f32 v113, v113, v160\n"
"v_sub_f32 v114, v114, v160\n"
"v_sub_f32 v115, v115, v160\n"
"v_sub_f32 v116, v116, v160\n"
"v_sub_f32 v117, v117, v160\n"
"v_sub_f32 v118, v118, v160\n"
"v_sub_f32 v119, v119, v160\n"
"v_exp_f32 v112, v112\n"
"v_exp_f32 v113, v113\n"
"v_exp_f32 v114, v114\n"
"v_exp_f32 v115, v115\n"
"v_exp_f32 v116, v116\n"
"v_exp_f32 v117, v117\n"
"v_exp_f32 v118, v118\n"
"v_exp_f32 v119, v119\n"
"v_mul_f32 v163, v163, v162\n"
"v_add_f32 v163, v163, v112\n"
"v_add_f32 v163, v163, v113\n"
"v_add_f32 v163, v163, v114\n"
"v_add_f32 v163, v163, v115\n"
"v_add_f32 v163, v163, v116\n"
"v_add_f32 v163, v163, v117\n"
"v_add_f32 v163, v163, v118\n"
"v_add_f32 v163, v163, v119\n"
"v_cvt_pk_bf16_f32 v112, v112, v113\n"
"v_cvt_pk_bf16_f32 v113, v114, v115\n"
"v_cvt_pk_bf16_f32 v114, v116, v117\n"
"v_cvt_pk_bf16_f32 v115, v118, v119\n"
"v_mov_b32_dpp v162, v162 row_ror:12 row_mask:0x2 bank_mask:0xf\n"
"s_nop 1\n"
"v_mov_b32_dpp v162, v162 row_ror:8 row_mask:0x4 bank_mask:0xf\n"
"s_nop 1\n"
"v_mov_b32_dpp v162, v162 row_ror:4 row_mask:0x8 bank_mask:0xf\n"
"s_nop 1\n"
"v_mul_f32_dpp v64, v162, v64 row_newbcast:0 row_mask:0xf bank_mask:0xf\n"
"v_mul_f32_dpp v65, v162, v65 row_newbcast:1 row_mask:0xf bank_mask:0xf\n"
"v_mul_f32_dpp v66, v162, v66 row_newbcast:2 row_mask:0xf bank_mask:0xf\n"
"v_mul_f32_dpp v67, v162, v67 row_newbcast:3 row_mask:0xf bank_mask:0xf\n"
"v_mul_f32_dpp v68, v162, v68 row_newbcast:0 row_mask:0xf bank_mask:0xf\n"
"v_mul_f32_dpp v69, v162, v69 row_newbcast:1 row_mask:0xf bank_mask:0xf\n"
"v_mul_f32_dpp v70, v162, v70 row_newbcast:2 row_mask:0xf bank_mask:0xf\n"
"v_mul_f32_dpp v71, v162, v71 row_newbcast:3 row_mask:0xf bank_mask:0xf\n"
"v_mul_f32_dpp v72, v162, v72 row_newbcast:0 row_mask:0xf bank_mask:0xf\n"
"v_mul_f32_dpp v73, v162, v73 row_newbcast:1 row_mask:0xf bank_mask:0xf\n"
"v_mul_f32_dpp v74, v162, v74 row_newbcast:2 row_mask:0xf bank_mask:0xf\n"
"v_mul_f32_dpp v75, v162, v75 row_newbcast:3 row_mask:0xf bank_mask:0xf\n"
"v_mul_f32_dpp v76, v162, v76 row_newbcast:0 row_mask:0xf bank_mask:0xf\n"
"v_mul_f32_dpp v77, v162, v77 row_newbcast:1 row_mask:0xf bank_mask:0xf\n"
"v_mul_f32_dpp v78, v162, v78 row_newbcast:2 row_mask:0xf bank_mask:0xf\n"
"v_mul_f32_dpp v79, v162, v79 row_newbcast:3 row_mask:0xf bank_mask:0xf\n"
"v_mul_f32_dpp v80, v162, v80 row_newbcast:0 row_mask:0xf bank_mask:0xf\n"
"v_mul_f32_dpp v81, v162, v81 row_newbcast:1 row_mask:0xf bank_mask:0xf\n"
"v_mul_f32_dpp v82, v162, v82 row_newbcast:2 row_mask:0xf bank_mask:0xf\n"
"v_mul_f32_dpp v83, v162, v83 row_newbcast:3 row_mask:0xf bank_mask:0xf\n"
"v_mul_f32_dpp v84, v162, v84 row_newbcast:0 row_mask:0xf bank_mask:0xf\n"
"v_mul_f32_dpp v85, v162, v85 row_newbcast:1 row_mask:0xf bank_mask:0xf\n"
"v_mul_f32_dpp v86, v162, v86 row_newbcast:2 row_mask:0xf bank_mask:0xf\n"
"v_mul_f32_dpp v87, v162, v87 row_newbcast:3 row_mask:0xf bank_mask:0xf\n"
"v_mul_f32_dpp v88, v162, v88 row_newbcast:0 row_mask:0xf bank_mask:0xf\n"
"v_mul_f32_dpp v89, v162, v89 row_newbcast:1 row_mask:0xf bank_mask:0xf\n"
"v_mul_f32_dpp v90, v162, v90 row_newbcast:2 row_mask:0xf bank_mask:0xf\n"
"v_mul_f32_dpp v91, v162, v91 row_newbcast:3 row_mask:0xf bank_mask:0xf\n"
"v_mul_f32_dpp v92, v162, v92 row_newbcast:0 row_mask:0xf bank_mask:0xf\n"
"v_mul_f32_dpp v93, v162, v93 row_newbcast:1 row_mask:0xf bank_mask:0xf\n"
"v_mul_f32_dpp v94, v162, v94 row_newbcast:2 row_mask:0xf bank_mask:0xf\n"
"v_mul_f32_dpp v95, v162, v95 row_newbcast:3 row_mask:0xf bank_mask:0xf\n"
"s_cmp_lg_u32 s83, s43\n"
"s_cbranch_scc1 LSK3A_%=\n"
"v_max3_f32 v165, v120, v121, v122\n"
"v_max3_f32 v165, v165, v123, v124\n"
"v_max3_f32 v165, v165, v125, v126\n"
"v_max3_f32 v165, v165, v127, v164\n"
"v_mov_b32 v166, v165\n"
"s_nop 1\n"
"v_permlane32_swap_b32 v165, v166\n"
"v_max_f32 v165, v165, v166\n"
"v_mov_b32 v166, v165\n"
"s_nop 1\n"
"v_permlane16_swap_b32 v165, v166\n"
"v_max_f32 v165, v165, v166\n"
"v_sub_f32 v166, v164, v165\n"
"v_exp_f32 v166, v166\n"
"v_mov_b32 v164, v165\n"
"v_sub_f32 v120, v120, v164\n"
"v_sub_f32 v121, v121, v164\n"
"v_sub_f32 v122, v122, v164\n"
"v_sub_f32 v123, v123, v164\n"
"v_sub_f32 v124, v124, v164\n"
"v_sub_f32 v125, v125, v164\n"
"v_sub_f32 v126, v126, v164\n"
"v_sub_f32 v127, v127, v164\n"
"v_exp_f32 v120, v120\n"
"v_exp_f32 v121, v121\n"
"v_exp_f32 v122, v122\n"
"v_exp_f32 v123, v123\n"
"v_exp_f32 v124, v124\n"
"v_exp_f32 v125, v125\n"
"v_exp_f32 v126, v126\n"
"v_exp_f32 v127, v127\n"
"v_mul_f32 v167, v167, v166\n"
"v_add_f32 v167, v167, v120\n"
"v_add_f32 v167, v167, v121\n"
"v_add_f32 v167, v167, v122\n"
"v_add_f32 v167, v167, v123\n"
"v_add_f32 v167, v167, v124\n"
"v_add_f32 v167, v167, v125\n"
"v_add_f32 v167, v167, v126\n"
"v_add_f32 v167, v167, v127\n"
"v_cvt_pk_bf16_f32 v120, v120, v121\n"
"v_cvt_pk_bf16_f32 v121, v122, v123\n"
"v_cvt_pk_bf16_f32 v122, v124, v125\n"
"v_cvt_pk_bf16_f32 v123, v126, v127\n"
"v_mov_b32_dpp v166, v166 row_ror:12 row_mask:0x2 bank_mask:0xf\n"
"s_nop 1\n"
"v_mov_b32_dpp v166, v166 row_ror:8 row_mask:0x4 bank_mask:0xf\n"
"s_nop 1\n"
"v_mov_b32_dpp v166, v166 row_ror:4 row_mask:0x8 bank_mask:0xf\n"
"v_accvgpr_read_b32 v124, a208\n"
"s_nop 1\n"
"v_mul_f32_dpp v124, v166, v124 row_newbcast:0 row_mask:0xf bank_mask:0xf\n"
"v_accvgpr_write_b32 a208, v124\n"
"v_accvgpr_read_b32 v125, a209\n"
"s_nop 1\n"
"v_mul_f32_dpp v125, v166, v125 row_newbcast:1 row_mask:0xf bank_mask:0xf\n"
"v_accvgpr_write_b32 a209, v125\n"
"v_accvgpr_read_b32 v126, a210\n"
"s_nop 1\n"
"v_mul_f32_dpp v126, v166, v126 row_newbcast:2 row_mask:0xf bank_mask:0xf\n"
"v_accvgpr_write_b32 a210, v126\n"
"v_accvgpr_read_b32 v127, a211\n"
"s_nop 1\n"
"v_mul_f32_dpp v127, v166, v127 row_newbcast:3 row_mask:0xf bank_mask:0xf\n"
"v_accvgpr_write_b32 a211, v127\n"
"v_accvgpr_read_b32 v124, a212\n"
"s_nop 1\n"
"v_mul_f32_dpp v124, v166, v124 row_newbcast:0 row_mask:0xf bank_mask:0xf\n"
"v_accvgpr_write_b32 a212, v124\n"
"v_accvgpr_read_b32 v125, a213\n"
"s_nop 1\n"
"v_mul_f32_dpp v125, v166, v125 row_newbcast:1 row_mask:0xf bank_mask:0xf\n"
"v_accvgpr_write_b32 a213, v125\n"
"v_accvgpr_read_b32 v126, a214\n"
"s_nop 1\n"
"v_mul_f32_dpp v126, v166, v126 row_newbcast:2 row_mask:0xf bank_mask:0xf\n"
"v_accvgpr_write_b32 a214, v126\n"
"v_accvgpr_read_b32 v127, a215\n"
"s_nop 1\n"
"v_mul_f32_dpp v127, v166, v127 row_newbcast:3 row_mask:0xf bank_mask:0xf\n"
"v_accvgpr_write_b32 a215, v127\n"
"v_accvgpr_read_b32 v124, a216\n"
"s_nop 1\n"
"v_mul_f32_dpp v124, v166, v124 row_newbcast:0 row_mask:0xf bank_mask:0xf\n"
"v_accvgpr_write_b32 a216, v124\n"
"v_accvgpr_read_b32 v125, a217\n"
"s_nop 1\n"
"v_mul_f32_dpp v125, v166, v125 row_newbcast:1 row_mask:0xf bank_mask:0xf\n"
"v_accvgpr_write_b32 a217, v125\n"
"v_accvgpr_read_b32 v126, a218\n"
"s_nop 1\n"
"v_mul_f32_dpp v126, v166, v126 row_newbcast:2 row_mask:0xf bank_mask:0xf\n"
"v_accvgpr_write_b32 a218, v126\n"
"v_accvgpr_read_b32 v127, a219\n"
"s_nop 1\n"
"v_mul_f32_dpp v127, v166, v127 row_newbcast:3 row_mask:0xf bank_mask:0xf\n"
"v_accvgpr_write_b32 a219, v127\n"
"v_accvgpr_read_b32 v124, a220\n"
"s_nop 1\n"
"v_mul_f32_dpp v124, v166, v124 row_newbcast:0 row_mask:0xf bank_mask:0xf\n"
"v_accvgpr_write_b32 a220, v124\n"
"v_accvgpr_read_b32 v125, a221\n"
"s_nop 1\n"
"v_mul_f32_dpp v125, v166, v125 row_newbcast:1 row_mask:0xf bank_mask:0xf\n"
"v_accvgpr_write_b32 a221, v125\n"
"v_accvgpr_read_b32 v126, a222\n"
"s_nop 1\n"
"v_mul_f32_dpp v126, v166, v126 row_newbcast:2 row_mask:0xf bank_mask:0xf\n"
"v_accvgpr_write_b32 a222, v126\n"
"v_accvgpr_read_b32 v127, a223\n"
"s_nop 1\n"
"v_mul_f32_dpp v127, v166, v127 row_newbcast:3 row_mask:0xf bank_mask:0xf\n"
"v_accvgpr_write_b32 a223, v127\n"
"v_accvgpr_read_b32 v124, a224\n"
"s_nop 1\n"
"v_mul_f32_dpp v124, v166, v124 row_newbcast:0 row_mask:0xf bank_mask:0xf\n"
"v_accvgpr_write_b32 a224, v124\n"
"v_accvgpr_read_b32 v125, a225\n"
"s_nop 1\n"
"v_mul_f32_dpp v125, v166, v125 row_newbcast:1 row_mask:0xf bank_mask:0xf\n"
"v_accvgpr_write_b32 a225, v125\n"
"v_accvgpr_read_b32 v126, a226\n"
"s_nop 1\n"
"v_mul_f32_dpp v126, v166, v126 row_newbcast:2 row_mask:0xf bank_mask:0xf\n"
"v_accvgpr_write_b32 a226, v126\n"
"v_accvgpr_read_b32 v127, a227\n"
"s_nop 1\n"
"v_mul_f32_dpp v127, v166, v127 row_newbcast:3 row_mask:0xf bank_mask:0xf\n"
"v_accvgpr_write_b32 a227, v127\n"
"v_accvgpr_read_b32 v124, a228\n"
"s_nop 1\n"
"v_mul_f32_dpp v124, v166, v124 row_newbcast:0 row_mask:0xf bank_mask:0xf\n"
"v_accvgpr_write_b32 a228, v124\n"
"v_accvgpr_read_b32 v125, a229\n"
"s_nop 1\n"
"v_mul_f32_dpp v125, v166, v125 row_newbcast:1 row_mask:0xf bank_mask:0xf\n"
"v_accvgpr_write_b32 a229, v125\n"
"v_accvgpr_read_b32 v126, a230\n"
"s_nop 1\n"
"v_mul_f32_dpp v126, v166, v126 row_newbcast:2 row_mask:0xf bank_mask:0xf\n"
"v_accvgpr_write_b32 a230, v126\n"
"v_accvgpr_read_b32 v127, a231\n"
"s_nop 1\n"
"v_mul_f32_dpp v127, v166, v127 row_newbcast:3 row_mask:0xf bank_mask:0xf\n"
"v_accvgpr_write_b32 a231, v127\n"
"v_accvgpr_read_b32 v124, a232\n"
"s_nop 1\n"
"v_mul_f32_dpp v124, v166, v124 row_newbcast:0 row_mask:0xf bank_mask:0xf\n"
"v_accvgpr_write_b32 a232, v124\n"
"v_accvgpr_read_b32 v125, a233\n"
"s_nop 1\n"
"v_mul_f32_dpp v125, v166, v125 row_newbcast:1 row_mask:0xf bank_mask:0xf\n"
"v_accvgpr_write_b32 a233, v125\n"
"v_accvgpr_read_b32 v126, a234\n"
"s_nop 1\n"
"v_mul_f32_dpp v126, v166, v126 row_newbcast:2 row_mask:0xf bank_mask:0xf\n"
"v_accvgpr_write_b32 a234, v126\n"
"v_accvgpr_read_b32 v127, a235\n"
"s_nop 1\n"
"v_mul_f32_dpp v127, v166, v127 row_newbcast:3 row_mask:0xf bank_mask:0xf\n"
"v_accvgpr_write_b32 a235, v127\n"
"v_accvgpr_read_b32 v124, a236\n"
"s_nop 1\n"
"v_mul_f32_dpp v124, v166, v124 row_newbcast:0 row_mask:0xf bank_mask:0xf\n"
"v_accvgpr_write_b32 a236, v124\n"
"v_accvgpr_read_b32 v125, a237\n"
"s_nop 1\n"
"v_mul_f32_dpp v125, v166, v125 row_newbcast:1 row_mask:0xf bank_mask:0xf\n"
"v_accvgpr_write_b32 a237, v125\n"
"v_accvgpr_read_b32 v126, a238\n"
"s_nop 1\n"
"v_mul_f32_dpp v126, v166, v126 row_newbcast:2 row_mask:0xf bank_mask:0xf\n"
"v_accvgpr_write_b32 a238, v126\n"
"v_accvgpr_read_b32 v127, a239\n"
"s_nop 1\n"
"v_mul_f32_dpp v127, v166, v127 row_newbcast:3 row_mask:0xf bank_mask:0xf\n"
"v_accvgpr_write_b32 a239, v127\n"
"LSK3A_%=:\n"
"s_nop 3\n"
"buffer_load_ushort v128, v146, s[88:91], s80 offen offset:0\n"
"buffer_load_ushort v129, v146, s[88:91], s80 offen offset:256\n"
"buffer_load_ushort v130, v146, s[88:91], s80 offen offset:512\n"
"buffer_load_ushort v131, v146, s[88:91], s80 offen offset:768\n"
"buffer_load_ushort v132, v146, s[88:91], s81 offen offset:0\n"
"buffer_load_ushort v133, v146, s[88:91], s81 offen offset:256\n"
"buffer_load_ushort v134, v146, s[88:91], s81 offen offset:512\n"
"buffer_load_ushort v135, v146, s[88:91], s81 offen offset:768\n"
"buffer_load_ushort v136, v146, s[88:91], s80 offen offset:32\n"
"buffer_load_ushort v137, v146, s[88:91], s80 offen offset:288\n"
"buffer_load_ushort v138, v146, s[88:91], s80 offen offset:544\n"
"buffer_load_ushort v139, v146, s[88:91], s80 offen offset:800\n"
"buffer_load_ushort v140, v146, s[88:91], s81 offen offset:32\n"
"buffer_load_ushort v141, v146, s[88:91], s81 offen offset:288\n"
"buffer_load_ushort v142, v146, s[88:91], s81 offen offset:544\n"
"buffer_load_ushort v143, v146, s[88:91], s81 offen offset:800\n"
"s_waitcnt vmcnt(0)\n"
"v_lshl_or_b32 v128, v129, 16, v128\n"
"v_lshl_or_b32 v129, v131, 16, v130\n"
"v_lshl_or_b32 v130, v133, 16, v132\n"
"v_lshl_or_b32 v131, v135, 16, v134\n"
"v_lshl_or_b32 v136, v137, 16, v136\n"
"v_lshl_or_b32 v137, v139, 16, v138\n"
"v_lshl_or_b32 v138, v141, 16, v140\n"
"v_lshl_or_b32 v139, v143, 16, v142\n"
"v_mfma_f32_16x16x32_bf16 v[0:3], v[96:99], v[128:131], v[0:3]\n"
"s_nop 0\n"
"v_mfma_f32_16x16x32_bf16 v[4:7], v[96:99], v[136:139], v[4:7]\n"
"v_mfma_f32_16x16x32_bf16 v[32:35], v[104:107], v[128:131], v[32:35]\n"
"v_mfma_f32_16x16x32_bf16 v[36:39], v[104:107], v[136:139], v[36:39]\n"
"v_mfma_f32_16x16x32_bf16 v[64:67], v[112:115], v[128:131], v[64:67]\n"
"v_mfma_f32_16x16x32_bf16 v[68:71], v[112:115], v[136:139], v[68:71]\n"
"s_cmp_lg_u32 s83, s43\n"
"s_cbranch_scc1 LSK3B0_%=\n"
"v_mfma_f32_16x16x32_bf16 a[208:211], v[120:123], v[128:131], a[208:211]\n"
"v_mfma_f32_16x16x32_bf16 a[212:215], v[120:123], v[136:139], a[212:215]\n"
"LSK3B0_%=:\n"
"s_nop 3\n"
"buffer_load_ushort v128, v147, s[88:91], s80 offen offset:0\n"
"buffer_load_ushort v129, v147, s[88:91], s80 offen offset:256\n"
"buffer_load_ushort v130, v147, s[88:91], s80 offen offset:512\n"
"buffer_load_ushort v131, v147, s[88:91], s80 offen offset:768\n"
"buffer_load_ushort v132, v147, s[88:91], s81 offen offset:0\n"
"buffer_load_ushort v133, v147, s[88:91], s81 offen offset:256\n"
"buffer_load_ushort v134, v147, s[88:91], s81 offen offset:512\n"
"buffer_load_ushort v135, v147, s[88:91], s81 offen offset:768\n"
"buffer_load_ushort v136, v147, s[88:91], s80 offen offset:32\n"
"buffer_load_ushort v137, v147, s[88:91], s80 offen offset:288\n"
"buffer_load_ushort v138, v147, s[88:91], s80 offen offset:544\n"
"buffer_load_ushort v139, v147, s[88:91], s80 offen offset:800\n"
"buffer_load_ushort v140, v147, s[88:91], s81 offen offset:32\n"
"buffer_load_ushort v141, v147, s[88:91], s81 offen offset:288\n"
"buffer_load_ushort v142, v147, s[88:91], s81 offen offset:544\n"
"buffer_load_ushort v143, v147, s[88:91], s81 offen offset:800\n"
"s_waitcnt vmcnt(0)\n"
"v_lshl_or_b32 v128, v129, 16, v128\n"
"v_lshl_or_b32 v129, v131, 16, v130\n"
"v_lshl_or_b32 v130, v133, 16, v132\n"
"v_lshl_or_b32 v131, v135, 16, v134\n"
"v_lshl_or_b32 v136, v137, 16, v136\n"
"v_lshl_or_b32 v137, v139, 16, v138\n"
"v_lshl_or_b32 v138, v141, 16, v140\n"
"v_lshl_or_b32 v139, v143, 16, v142\n"
"v_mfma_f32_16x16x32_bf16 v[8:11], v[96:99], v[128:131], v[8:11]\n"
"s_nop 0\n"
"v_mfma_f32_16x16x32_bf16 v[12:15], v[96:99], v[136:139], v[12:15]\n"
"v_mfma_f32_16x16x32_bf16 v[40:43], v[104:107], v[128:131], v[40:43]\n"
"v_mfma_f32_16x16x32_bf16 v[44:47], v[104:107], v[136:139], v[44:47]\n"
"v_mfma_f32_16x16x32_bf16 v[72:75], v[112:115], v[128:131], v[72:75]\n"
"v_mfma_f32_16x16x32_bf16 v[76:79], v[112:115], v[136:139], v[76:79]\n"
"s_cmp_lg_u32 s83, s43\n"
"s_cbranch_scc1 LSK3B1_%=\n"
"v_mfma_f32_16x16x32_bf16 a[216:219], v[120:123], v[128:131], a[216:219]\n"
"v_mfma_f32_16x16x32_bf16 a[220:223], v[120:123], v[136:139], a[220:223]\n"
"LSK3B1_%=:\n"
"s_nop 3\n"
"buffer_load_ushort v128, v146, s[88:91], s80 offen offset:128\n"
"buffer_load_ushort v129, v146, s[88:91], s80 offen offset:384\n"
"buffer_load_ushort v130, v146, s[88:91], s80 offen offset:640\n"
"buffer_load_ushort v131, v146, s[88:91], s80 offen offset:896\n"
"buffer_load_ushort v132, v146, s[88:91], s81 offen offset:128\n"
"buffer_load_ushort v133, v146, s[88:91], s81 offen offset:384\n"
"buffer_load_ushort v134, v146, s[88:91], s81 offen offset:640\n"
"buffer_load_ushort v135, v146, s[88:91], s81 offen offset:896\n"
"buffer_load_ushort v136, v146, s[88:91], s80 offen offset:160\n"
"buffer_load_ushort v137, v146, s[88:91], s80 offen offset:416\n"
"buffer_load_ushort v138, v146, s[88:91], s80 offen offset:672\n"
"buffer_load_ushort v139, v146, s[88:91], s80 offen offset:928\n"
"buffer_load_ushort v140, v146, s[88:91], s81 offen offset:160\n"
"buffer_load_ushort v141, v146, s[88:91], s81 offen offset:416\n"
"buffer_load_ushort v142, v146, s[88:91], s81 offen offset:672\n"
"buffer_load_ushort v143, v146, s[88:91], s81 offen offset:928\n"
"s_waitcnt vmcnt(0)\n"
"v_lshl_or_b32 v128, v129, 16, v128\n"
"v_lshl_or_b32 v129, v131, 16, v130\n"
"v_lshl_or_b32 v130, v133, 16, v132\n"
"v_lshl_or_b32 v131, v135, 16, v134\n"
"v_lshl_or_b32 v136, v137, 16, v136\n"
"v_lshl_or_b32 v137, v139, 16, v138\n"
"v_lshl_or_b32 v138, v141, 16, v140\n"
"v_lshl_or_b32 v139, v143, 16, v142\n"
"v_mfma_f32_16x16x32_bf16 v[16:19], v[96:99], v[128:131], v[16:19]\n"
"s_nop 0\n"
"v_mfma_f32_16x16x32_bf16 v[20:23], v[96:99], v[136:139], v[20:23]\n"
"v_mfma_f32_16x16x32_bf16 v[48:51], v[104:107], v[128:131], v[48:51]\n"
"v_mfma_f32_16x16x32_bf16 v[52:55], v[104:107], v[136:139], v[52:55]\n"
"v_mfma_f32_16x16x32_bf16 v[80:83], v[112:115], v[128:131], v[80:83]\n"
"v_mfma_f32_16x16x32_bf16 v[84:87], v[112:115], v[136:139], v[84:87]\n"
"s_cmp_lg_u32 s83, s43\n"
"s_cbranch_scc1 LSK3B2_%=\n"
"v_mfma_f32_16x16x32_bf16 a[224:227], v[120:123], v[128:131], a[224:227]\n"
"v_mfma_f32_16x16x32_bf16 a[228:231], v[120:123], v[136:139], a[228:231]\n"
"LSK3B2_%=:\n"
"s_nop 3\n"
"buffer_load_ushort v128, v147, s[88:91], s80 offen offset:128\n"
"buffer_load_ushort v129, v147, s[88:91], s80 offen offset:384\n"
"buffer_load_ushort v130, v147, s[88:91], s80 offen offset:640\n"
"buffer_load_ushort v131, v147, s[88:91], s80 offen offset:896\n"
"buffer_load_ushort v132, v147, s[88:91], s81 offen offset:128\n"
"buffer_load_ushort v133, v147, s[88:91], s81 offen offset:384\n"
"buffer_load_ushort v134, v147, s[88:91], s81 offen offset:640\n"
"buffer_load_ushort v135, v147, s[88:91], s81 offen offset:896\n"
"buffer_load_ushort v136, v147, s[88:91], s80 offen offset:160\n"
"buffer_load_ushort v137, v147, s[88:91], s80 offen offset:416\n"
"buffer_load_ushort v138, v147, s[88:91], s80 offen offset:672\n"
"buffer_load_ushort v139, v147, s[88:91], s80 offen offset:928\n"
"buffer_load_ushort v140, v147, s[88:91], s81 offen offset:160\n"
"buffer_load_ushort v141, v147, s[88:91], s81 offen offset:416\n"
"buffer_load_ushort v142, v147, s[88:91], s81 offen offset:672\n"
"buffer_load_ushort v143, v147, s[88:91], s81 offen offset:928\n"
"s_waitcnt vmcnt(0)\n"
"v_lshl_or_b32 v128, v129, 16, v128\n"
"v_lshl_or_b32 v129, v131, 16, v130\n"
"v_lshl_or_b32 v130, v133, 16, v132\n"
"v_lshl_or_b32 v131, v135, 16, v134\n"
"v_lshl_or_b32 v136, v137, 16, v136\n"
"v_lshl_or_b32 v137, v139, 16, v138\n"
"v_lshl_or_b32 v138, v141, 16, v140\n"
"v_lshl_or_b32 v139, v143, 16, v142\n"
"v_mfma_f32_16x16x32_bf16 v[24:27], v[96:99], v[128:131], v[24:27]\n"
"s_nop 0\n"
"v_mfma_f32_16x16x32_bf16 v[28:31], v[96:99], v[136:139], v[28:31]\n"
"v_mfma_f32_16x16x32_bf16 v[56:59], v[104:107], v[128:131], v[56:59]\n"
"v_mfma_f32_16x16x32_bf16 v[60:63], v[104:107], v[136:139], v[60:63]\n"
"v_mfma_f32_16x16x32_bf16 v[88:91], v[112:115], v[128:131], v[88:91]\n"
"v_mfma_f32_16x16x32_bf16 v[92:95], v[112:115], v[136:139], v[92:95]\n"
"s_cmp_lg_u32 s83, s43\n"
"s_cbranch_scc1 LSK3B3_%=\n"
"v_mfma_f32_16x16x32_bf16 a[232:235], v[120:123], v[128:131], a[232:235]\n"
"v_mfma_f32_16x16x32_bf16 a[236:239], v[120:123], v[136:139], a[236:239]\n"
"LSK3B3_%=:\n"
"s_nop 3\n"
"s_nop 9\n"
"s_add_u32 s80, s80, 8192\n"
"s_add_u32 s81, s81, 8192\n"
"s_add_u32 s82, s82, 1\n"
"s_cmp_lt_u32 s82, 32\n"
"s_cbranch_scc1 LSLOOP_%=\n"
"v_mov_b32 v154, v155\n"
"s_nop 1\n"
"v_permlane32_swap_b32 v155, v154\n"
"v_add_f32 v155, v155, v154\n"
"v_mov_b32 v154, v155\n"
"s_nop 1\n"
"v_permlane16_swap_b32 v155, v154\n"
"v_add_f32 v155, v155, v154\n"
"v_mov_b32 v196, v155\n"
"v_mov_b32 v158, v159\n"
"s_nop 1\n"
"v_permlane32_swap_b32 v159, v158\n"
"v_add_f32 v159, v159, v158\n"
"v_mov_b32 v158, v159\n"
"s_nop 1\n"
"v_permlane16_swap_b32 v159, v158\n"
"v_add_f32 v159, v159, v158\n"
"v_mov_b32 v198, v159\n"
"v_mov_b32 v162, v163\n"
"s_nop 1\n"
"v_permlane32_swap_b32 v163, v162\n"
"v_add_f32 v163, v163, v162\n"
"v_mov_b32 v162, v163\n"
"s_nop 1\n"
"v_permlane16_swap_b32 v163, v162\n"
"v_add_f32 v163, v163, v162\n"
"v_mov_b32 v200, v163\n"
"v_mov_b32 v166, v167\n"
"s_nop 1\n"
"v_permlane32_swap_b32 v167, v166\n"
"v_add_f32 v167, v167, v166\n"
"v_mov_b32 v166, v167\n"
"s_nop 1\n"
"v_permlane16_swap_b32 v167, v166\n"
"v_add_f32 v167, v167, v166\n"
"v_mov_b32 v202, v167\n"
"v_sub_f32 v192, 0, v164\n"
"v_mov_b32 v96, 0\n"
"v_mov_b32 v97, 0\n"
"v_mov_b32 v98, 0\n"
"v_mov_b32 v99, 0\n"
"v_mov_b32 v100, 0\n"
"v_mov_b32 v101, 0\n"
"v_mov_b32 v102, 0\n"
"v_mov_b32 v103, 0\n"
"v_mov_b32 v104, 0\n"
"v_mov_b32 v105, 0\n"
"v_mov_b32 v106, 0\n"
"v_mov_b32 v107, 0\n"
"v_mov_b32 v108, 0\n"
"v_mov_b32 v109, 0\n"
"v_mov_b32 v110, 0\n"
"v_mov_b32 v111, 0\n"
"v_mov_b32 v112, 0\n"
"v_mov_b32 v113, 0\n"
"v_mov_b32 v114, 0\n"
"v_mov_b32 v115, 0\n"
"v_mov_b32 v116, 0\n"
"v_mov_b32 v117, 0\n"
"v_mov_b32 v118, 0\n"
"v_mov_b32 v119, 0\n"
"v_mov_b32 v120, 0\n"
"v_mov_b32 v121, 0\n"
"v_mov_b32 v122, 0\n"
"v_mov_b32 v123, 0\n"
"v_mov_b32 v204, 0\n"
"v_mov_b32 v205, 0\n"
"s_nop 3\n"
"s_mov_b32 s68, 0\n"
"s_cmp_eq_u32 s47, 4\n"
"s_cbranch_scc1 LBACK0_%=\n"
"s_cmp_eq_u32 s47, 8\n"
"s_cbranch_scc1 LBACK1_%=\n"
"s_cmp_eq_u32 s47, 12\n"
"s_cbranch_scc1 LBACK2_%=\n"
"s_branch LBACK3_%=\n"
"LFIN_%=:\n"
    :: [kalo]"s"((unsigned)ka), [kahi]"s"((unsigned)(ka >> 32)), [wg]"s"(blockIdx.x), [wid]"s"(wid)
    : "v0","v1","v2","v3","v4","v5","v6","v7","v8","v9","v10","v11","v12","v13","v14","v15","v16","v17","v18","v19","v20","v21","v22","v23","v24","v25","v26","v27","v28","v29","v30","v31","v32","v33","v34","v35","v36","v37","v38","v39","v40","v41","v42","v43","v44","v45","v46","v47","v48","v49","v50","v51","v52","v53","v54","v55","v56","v57","v58","v59","v60","v61","v62","v63","v64","v65","v66","v67","v68","v69","v70","v71","v72","v73","v74","v75","v76","v77","v78","v79","v80","v81","v82","v83","v84","v85","v86","v87","v88","v89","v90","v91","v92","v93","v94","v95","v96","v97","v98","v99","v100","v101","v102","v103","v104","v105","v106","v107","v108","v109","v110","v111","v112","v113","v114","v115","v116","v117","v118","v119","v120","v121","v122","v123","v124","v125","v126","v127","v128","v129","v130","v131","v132","v133","v134","v135","v136","v137","v138","v139","v140","v141","v142","v143","v144","v145","v146","v147","v148","v149","v150","v151","v152","v153","v154","v155","v156","v157","v158","v159","v160","v161","v162","v163","v164","v165","v166","v167","v168","v169","v170","v171","v172","v173","v174","v175","v176","v177","v178","v179","v180","v181","v182","v183","v184","v185","v186","v187","v188","v189","v190","v191","v192","v193","v194","v195","v196","v197","v198","v199","v200","v201","v202","v203","v204","v205","v206","v207","v208","v209","v210","v211","v212","v213","v214","v215","v216","v217","v218","v219","v220","v221","v222","v223","v224","v225","v226","v227","v228","v229","v230","v231","v232","v233","v234","v235","v236","v237","v238","v239","v240","v241","v242","v243","v244","v245","v246","v247","v248","v249","v250","v251","v252","v253","v254","v255","a0","a1","a2","a3","a4","a5","a6","a7","a8","a9","a10","a11","a12","a13","a14","a15","a16","a17","a18","a19","a20","a21","a22","a23","a24","a25","a26","a27","a28","a29","a30","a31","a32","a33","a34","a35","a36","a37","a38","a39","a40","a41","a42","a43","a44","a45","a46","a47","a48","a49","a50","a51","a52","a53","a54","a55","a56","a57","a58","a59","a60","a61","a62","a63","a64","a65","a66","a67","a68","a69","a70","a71","a72","a73","a74","a75","a76","a77","a78","a79","a80","a81","a82","a83","a84","a85","a86","a87","a88","a89","a90","a91","a92","a93","a94","a95","a96","a97","a98","a99","a100","a101","a102","a103","a104","a105","a106","a107","a108","a109","a110","a111","a112","a113","a114","a115","a116","a117","a118","a119","a120","a121","a122","a123","a124","a125","a126","a127","a128","a129","a130","a131","a132","a133","a134","a135","a136","a137","a138","a139","a140","a141","a142","a143","a144","a145","a146","a147","a148","a149","a150","a151","a152","a153","a154","a155","a156","a157","a158","a159","a160","a161","a162","a163","a164","a165","a166","a167","a168","a169","a170","a171","a172","a173","a174","a175","a176","a177","a178","a179","a180","a181","a182","a183","a184","a185","a186","a187","a188","a189","a190","a191","a192","a193","a194","a195","a196","a197","a198","a199","a200","a201","a202","a203","a204","a205","a206","a207","a208","a209","a210","a211","a212","a213","a214","a215","a216","a217","a218","a219","a220","a221","a222","a223","a224","a225","a226","a227","a228","a229","a230","a231","a232","a233","a234","a235","a236","a237","a238","a239","a240","a241","a242","a243","a244","a245","a246","a247","a248","a249","a250","a251","a252","a253","a254","a255","s6","s7","s8","s9","s10","s11","s12","s13","s14","s15","s16","s17","s18","s19","s20","s21","s22","s23","s24","s25","s26","s27","s28","s29","s30","s31","s32","s33","s34","s35","s36","s37","s38","s39","s40","s41","s42","s43","s44","s45","s46","s47","s48","s49","s50","s51","s52","s53","s54","s55","s56","s57","s58","s59","s60","s61","s62","s63","s64","s65","s66","s67","s68","s69","s70","s71","s72","s73","s74","s75","s76","s77","s78","s79","s80","s81","s82","s83","s84","s85","s86","s87","s88","s89","s90","s91","s92","s93","s94","s95","s96","s97","s98","s99","s100","vcc","scc","m0","memory");
}
extern "C" void kernel_launch(void* const* d_in, const int* in_sizes, int n_in, void* d_out, int out_size, void* d_ws, size_t ws_size, hipStream_t stream) {
  char* xch = (char*)d_ws + ((ws_size - (size_t)270532608) & ~(size_t)0xfffff);
  (void)hipFuncSetAttribute((const void*)fused_attn, hipFuncAttributeMaxDynamicSharedMemorySize, 163840);
  hipLaunchKernelGGL(fused_attn, dim3(256), dim3(256), 163840, stream,
                     (const char*)d_in[0], (const char*)d_in[1], (const char*)d_in[2], (const char*)d_in[3], (const char*)d_in[4],
                     (const char*)d_in[5], (const char*)d_in[6], (const char*)d_in[7], (const char*)d_in[8], (const char*)d_in[9], xch, (char*)d_out, (char*)d_ws);
}
```

```cpp
#include <hip/hip_runtime.h>
#include <hip/hip_bf16.h>
#include <stdint.h>

__global__ __launch_bounds__(256, 1)
void fused_attn(const char* xA, const char* kA, const char* vA, const char* wA, const char* uA,
                const char* xB, const char* kB, const char* vB, const char* wB, const char* uB, char* xch, char* out, char* flags) {
  extern __shared__ __attribute__((aligned(16))) char lds[];
  const unsigned wid = __builtin_amdgcn_readfirstlane(threadIdx.x >> 6);
  const unsigned long long ka = (unsigned long long)__builtin_amdgcn_kernarg_segment_ptr();
  asm volatile(
"s_memtime s[64:65]\n"
"s_memrealtime s[66:67]\n"
"s_bfe_u32 s10, %[wg], 0x10002\n"
"s_mul_i32 s40, s10, 40\n"
"s_add_u32 s40, s40, %[kalo]\n"
"s_addc_u32 s41, %[kahi], 0\n"
"s_mov_b32 s78, %[kalo]\n"
"s_mov_b32 s79, %[kahi]\n"
"s_load_dwordx2 s[24:25], s[40:41], 0\n"
"s_load_dwordx2 s[28:29], s[40:41], 24\n"
"s_load_dwordx2 s[6:7], s[78:79], 0x60\n"
"s_load_dwordx2 s[16:17], s[40:41], 8\n"
"s_load_dwordx2 s[20:21], s[40:41], 16\n"
"s_load_dwordx2 s[8:9], s[40:41], 32\n"
"s_load_dwordx4 s[12:15], s[78:79], 0x50\n"
"s_mov_b32 s42, %[wid]\n"
"s_and_b32 s11, %[wg], 1\n"
"s_lshl_b32 s11, s11, 3\n"
"s_lshr_b32 s60, %[wg], 5\n"
"s_add_u32 s11, s11, s60\n"
"s_bfe_u32 s61, %[wg], 0x10001\n"
"s_bfe_u32 s60, %[wg], 0x20003\n"
"s_lshl2_add_u32 s61, s61, s60\n"
"s_getpc_b64 s[80:81]\n"
"LCP_%=:\n"
"s_mov_b32 s82, LEND_%=-LCP_%=\n"
"s_mov_b32 s83, 0x20000\n"
"v_mbcnt_lo_u32_b32 v2, -1, 0\n"
"v_lshlrev_b32 v2, 7, v2\n"
"s_lshr_b32 s60, %[wg], 3\n"
"s_lshl2_add_u32 s60, s60, s42\n"
"s_mul_i32 s60, s60, 640\n"
"s_mov_b64 exec, 31\n"
"buffer_load_dword v3, v2, s[80:83], s60 offen\n"
"s_mov_b64 exec, -1\n"
"s_mov_b32 s19, 0x20000\n"
"s_mov_b32 s23, 0x20000\n"
"s_mov_b32 s27, 0x20000\n"
"s_mov_b32 s31, 0x20000\n"
"s_mov_b32 s35, 0x20000\n"
"s_mov_b32 s39, 0x20000\n"
"s_mov_b32 s26, 286720\n"
"s_lshl_b32 s44, s42, 12\n"
"s_mov_b32 s50, s44\n"
"s_mul_i32 s51, s42, 7168\n"
"s_add_u32 s51, s51, 0x10000\n"
"s_mov_b32 s74, 0x8000\n"
"s_mov_b32 s58, 524288\n"
"s_mov_b32 s47, 0\n"
"s_add_u32 s60, s11, s61\n"
"s_and_b32 s60, s60, 0\n"
"s_mul_i32 s60, s60, 3\n"
"s_lshl_b32 s49, s60, 7\n"
"s_lshl_b32 s48, s60, 19\n"
"v_mbcnt_lo_u32_b32 v4, -1, 0\n"
"v_mbcnt_hi_u32_b32 v4, -1, v4\n"
"v_and_b32 v5, 15, v4\n"
"v_lshrrev_b32 v6, 4, v4\n"
"v_lshrrev_b32 v7, 3, v4\n"
"v_and_b32 v8, 7, v4\n"
"v_lshlrev_b32 v9, 1, v6\n"
"v_xor_b32 v9, v9, v8\n"
"v_lshlrev_b32 v9, 4, v9\n"
"s_mul_i32 s60, s42, 71680\n"
"v_mul_u32_u24 v10, 1280, v7\n"
"v_add3_u32 v248, v10, v9, s60\n"
"v_add_u32 v249, 9216, v248\n"
"v_add_u32 v254, 18432, v248\n"
"v_add_u32 v255, 27648, v248\n"
"v_lshl_add_u32 v240, v7, 8, v9\n"
"v_add_u32 v240, s44, v240\n"
"v_and_b32 v10, 1, v6\n"
"v_xor_b32 v10, v10, v8\n"
"v_lshlrev_b32 v10, 4, v10\n"
"v_lshl_add_u32 v10, v7, 13, v10\n"
"s_lshl_b32 s60, s42, 17\n"
"v_add_u32 v10, s60, v10\n"
"v_add_u32 v224, 1024, v10\n"
"v_add_u32 v225, 128, v10\n"
"v_xor_b32 v10, 64, v10\n"
"v_add_u32 v226, 64512, v10\n"
"v_add_u32 v227, 63616, v10\n"
"s_mul_i32 s40, s11, 286720\n"
"s_lshl_b32 s41, s61, 10\n"
"s_sub_u32 s30, 5243904, s41\n"
"s_sub_u32 s41, s41, 1024\n"
"s_ashr_i32 s60, s41, 31\n"
"s_lshl4_add_u32 s92, s10, s11\n"
"s_lshl_b32 s93, s92, 9\n"
"v_mov_b32 v0, 0\n"
"s_waitcnt lgkmcnt(0)\n"
"s_add_u32 s24, s24, s40\n"
"s_addc_u32 s25, s25, 0\n"
"s_and_b32 s25, s25, 0xffff\n"
"s_add_u32 s28, s28, s41\n"
"s_addc_u32 s29, s29, s60\n"
"s_and_b32 s29, s29, 0xffff\n"
"s_add_u32 s6, s6, s93\n"
"s_addc_u32 s7, s7, 0\n"
"buffer_load_dword v12, v248, s[24:27], s49 offen\n"
"buffer_load_dword v13, v224, s[28:31], s48 offen\n"
"global_load_dword v1, v0, s[6:7] offset:128 sc1\n"
"s_mov_b32 m0, s51\n"
"s_nop 0\n"
"buffer_load_dwordx4 v248, s[24:27], s49 offen lds\n"
"buffer_load_dwordx4 v249, s[24:27], s49 offen offset:1024 lds\n"
"buffer_load_dwordx4 v254, s[24:27], s49 offen offset:2048 lds\n"
"buffer_load_dwordx4 v255, s[24:27], s49 offen offset:3072 lds\n"
"s_add_u32 m0, s51, 4096\n"
"s_add_u32 s40, s49, 40960\n"
"buffer_load_dwordx4 v248, s[24:27], s40 offen lds\n"
"buffer_load_dwordx4 v249, s[24:27], s40 offen offset:1024 lds\n"
"buffer_load_dwordx4 v254, s[24:27], s40 offen offset:2048 lds\n"
"s_add_u32 s49, s49, 128\n"
"s_sub_u32 s41, s49, 1280\n"
"s_min_u32 s49, s49, s41\n"
"s_xor_b32 s51, s51, s74\n"
"s_mov_b32 m0, s50\n"
"s_nop 0\n"
"buffer_load_dwordx4 v224, s[28:31], s48 offen lds\n"
"buffer_load_dwordx4 v225, s[28:31], s48 offen offset:1024 lds\n"
"buffer_load_dwordx4 v226, s[28:31], s48 offen offset:2048 lds\n"
"buffer_load_dwordx4 v227, s[28:31], s48 offen offset:3072 lds\n"
"s_xor_b32 s50, s50, s74\n"
"s_lshl2_add_u32 s63, s11, s42\n"
"s_mul_i32 s63, s63, 10240\n"
"s_and_b32 s78, s42, 1\n"
"s_lshl_b32 s43, s78, 6\n"
"s_mov_b32 s77, 0x80000000\n"
"s_mov_b32 s75, 0x10000\n"
"s_mov_b32 s73, 0xffff0000\n"
"s_mov_b32 s46, 0x3e0293ee\n"
"s_mov_b32 s45, 0x63800000\n"
"s_mov_b32 s62, 9\n"
"v_lshl_add_u32 v242, v4, 4, s44\n"
"s_lshr_b32 s79, s42, 1\n"
"s_lshl4_add_u32 s76, s79, s78\n"
"s_lshl_b32 s76, s76, 10\n"
"s_add_u32 s76, s76, 0x20000\n"
"s_sub_u32 s76, s76, s44\n"
"v_lshlrev_b32 v9, 6, v6\n"
"v_xor_b32 v9, v9, v242\n"
"v_add_u32 v243, s76, v9\n"
"v_and_b32 v9, 6, v5\n"
"s_xor_b32 m0, s50, 0xc000\n"
"s_add_u32 s40, s48, 256\n"
"buffer_load_dwordx4 v224, s[28:31], s40 offen lds\n"
"v_xor_b32 v9, v9, v6\n"
"v_lshlrev_b32 v9, 4, v9\n"
"v_lshl_add_u32 v10, v5, 7, v9\n"
"s_mul_i32 s79, s42, 6144\n"
"s_add_u32 s79, s79, 0x10000\n"
"v_add_u32 v246, s79, v10\n"
"v_xor_b32 v247, 64, v246\n"
"s_lshr_b32 s79, s42, 1\n"
"s_lshl_b32 s79, s79, 11\n"
"buffer_load_dwordx4 v225, s[28:31], s40 offen offset:1024 lds\n"
"s_add_u32 s79, s79, 90112\n"
"v_add_u32 v252, s79, v10\n"
"v_xor_b32 v253, 64, v252\n"
"s_xor_b32 s79, s42, 1\n"
"s_mul_i32 s79, s79, 6144\n"
"s_add_u32 s79, s79, 0x10000\n"
"v_add_u32 v250, s79, v10\n"
"v_xor_b32 v251, 64, v250\n"
"s_mul_i32 s79, s78, 8256\n"
"buffer_load_dwordx4 v226, s[28:31], s40 offen offset:2048 lds\n"
"v_lshrrev_b32 v9, 5, v4\n"
"v_lshlrev_b32 v9, 11, v9\n"
"v_bfe_u32 v10, v4, 2, 3\n"
"v_lshl_add_u32 v9, v10, 7, v9\n"
"v_and_b32 v10, 3, v4\n"
"v_lshl_add_u32 v9, v10, 3, v9\n"
"v_bfe_u32 v10, v4, 3, 2\n"
"v_lshl_add_u32 v9, v10, 5, v9\n"
"s_add_u32 s79, s79, 0x10000\n"
"buffer_load_dwordx4 v227, s[28:31], s40 offen offset:3072 lds\n"
"s_cmp_eq_u32 s47, 8\n"
"s_cselect_b32 s41, s52, s58\n"
"s_add_u32 s48, s48, s41\n"
"s_sub_u32 s41, s48, 5242880\n"
"s_min_u32 s48, s48, s41\n"
"v_xor_b32 v232, s79, v9\n"
"v_xor_b32 v233, 32, v232\n"
"v_xor_b32 v234, 64, v232\n"
"v_xor_b32 v235, 96, v232\n"
"v_xor_b32 v236, 0x2000, v232\n"
"v_xor_b32 v237, 0x2000, v233\n"
"v_xor_b32 v238, 0x2000, v234\n"
"v_xor_b32 v239, 0x2000, v235\n"
"v_lshlrev_b32 v9, 11, v6\n"
"s_mov_b32 m0, s50\n"
"s_nop 0\n"
"buffer_load_dwordx4 v224, s[28:31], s48 offen lds\n"
"v_bfe_u32 v10, v4, 2, 2\n"
"v_lshl_add_u32 v9, v10, 7, v9\n"
"v_and_b32 v10, 1, v4\n"
"v_lshl_add_u32 v9, v10, 3, v9\n"
"v_bfe_u32 v10, v4, 1, 1\n"
"v_lshl_add_u32 v9, v10, 5, v9\n"
"v_bfe_u32 v10, v4, 3, 1\n"
"v_lshl_add_u32 v9, v10, 4, v9\n"
"v_bfe_u32 v10, v4, 4, 1\n"
"buffer_load_dwordx4 v225, s[28:31], s48 offen offset:1024 lds\n"
"v_lshl_add_u32 v9, v10, 6, v9\n"
"v_xor_b32 v244, s43, v9\n"
"v_xor_b32 v245, 16, v244\n"
"s_lshl3_add_u32 s79, s11, s61\n"
"s_lshl_b32 s79, s79, 20\n"
"s_sub_u32 s79, s79, 896\n"
"s_ashr_i32 s60, s79, 31\n"
"s_add_u32 s16, s16, s79\n"
"s_addc_u32 s17, s17, s60\n"
"buffer_load_dwordx4 v226, s[28:31], s48 offen offset:2048 lds\n"
"s_and_b32 s17, s17, 0xffff\n"
"s_mov_b32 s18, 1049472\n"
"s_mov_b64 s[84:85], s[16:17]\n"
"s_mov_b64 s[86:87], s[18:19]\n"
"s_add_u32 s20, s20, s79\n"
"s_addc_u32 s21, s21, s60\n"
"s_and_b32 s21, s21, 0xffff\n"
"s_mov_b32 s22, 1049472\n"
"s_mov_b64 s[88:89], s[20:21]\n"
"buffer_load_dwordx4 v227, s[28:31], s48 offen offset:3072 lds\n"
"s_xor_b32 s50, s50, s74\n"
"s_mov_b64 s[90:91], s[22:23]\n"
"s_lshr_b32 s57, %[wg], 3\n"
"s_mul_i32 s57, s57, 5\n"
"s_and_b32 s57, s57, 15\n"
"s_lshl_b32 s57, s57, 14\n"
"s_mov_b32 s55, 896\n"
"s_add_u32 s56, s57, 896\n"
"s_mov_b32 s59, 16384\n"
"s_mov_b32 s54, -896\n"
"s_mov_b32 m0, s51\n"
"s_nop 0\n"
"buffer_load_dwordx4 v248, s[24:27], s49 offen lds\n"
"s_add_u32 s52, s11, s61\n"
"s_and_b32 s52, s52, 0\n"
"s_mul_i32 s52, s52, 3\n"
"s_add_u32 s52, s52, 1\n"
"s_lshl_b32 s53, s52, 7\n"
"s_lshl_b32 s52, s52, 19\n"
"s_add_u32 s52, s52, 512\n"
"s_mul_i32 s79, s61, 655360\n"
"s_add_u32 s8, s8, s79\n"
"s_addc_u32 s9, s9, 0\n"
"buffer_load_dwordx4 v249, s[24:27], s49 offen offset:1024 lds\n"
"s_and_b32 s9, s9, 0xffff\n"
"s_add_u32 s12, s12, 0x1fffff\n"
"s_addc_u32 s13, s13, 0\n"
"s_and_b32 s12, s12, 0xffe00000\n"
"s_mul_i32 s96, s42, 3\n"
"s_and_b32 s96, s96, 5\n"
"s_cmp_eq_u32 s42, 3\n"
"s_cselect_b32 s96, 5, s96\n"
"buffer_load_dwordx4 v254, s[24:27], s49 offen offset:2048 lds\n"
"s_lshl_b32 s79, s61, 8\n"
"s_add_u32 s96, s96, s79\n"
"s_add_u32 s96, s96, 160\n"
"s_lshl_b32 s96, s96, 12\n"
"s_mul_i32 s79, s92, 8388608\n"
"s_add_u32 s32, s12, s79\n"
"s_addc_u32 s33, s13, 0\n"
"s_and_b32 s33, s33, 0xffff\n"
"s_mov_b32 s34, 8388608\n"
"buffer_load_dwordx4 v255, s[24:27], s49 offen offset:3072 lds\n"
"s_mul_i32 s79, s11, 573440\n"
"s_mul_i32 s60, s10, 1280\n"
"s_sub_u32 s38, 573440, s60\n"
"s_add_u32 s79, s79, s60\n"
"s_add_u32 s36, s14, s79\n"
"s_addc_u32 s37, s15, 0\n"
"s_and_b32 s37, s37, 0xffff\n"
"s_lshl2_add_u32 s93, s61, s42\n"
"s_add_u32 s95, s61, 1\n"
"s_add_u32 m0, s51, 4096\n"
"s_add_u32 s40, s49, 40960\n"
"buffer_load_dwordx4 v248, s[24:27], s40 offen lds\n"
"s_and_b32 s95, s95, 7\n"
"s_lshl_b32 s95, s95, 7\n"
"s_add_u32 s100, s61, 2\n"
"s_and_b32 s100, s100, 7\n"
"s_lshl_b32 s100, s100, 7\n"
"s_lshl_b32 s60, s61, 7\n"
"s_add_u32 s60, s60, s43\n"
"s_mul_i32 s97, s42, 122880\n"
"s_add_u32 s97, s97, s60\n"
"buffer_load_dwordx4 v249, s[24:27], s40 offen offset:1024 lds\n"
"s_lshr_b32 s98, s42, 1\n"
"s_add_u32 s98, s98, 12\n"
"s_mul_i32 s98, s98, 40960\n"
"s_add_u32 s98, s98, s60\n"
"s_mul_i32 s60, s93, 37\n"
"s_lshr_b32 s60, s60, 8\n"
"s_mul_i32 s79, s60, 7\n"
"s_sub_u32 s79, s93, s79\n"
"s_lshl4_add_u32 s94, s60, s79\n"
"buffer_load_dwordx4 v254, s[24:27], s40 offen offset:2048 lds\n"
"s_add_u32 s49, s49, 128\n"
"s_sub_u32 s41, s49, 1280\n"
"s_min_u32 s49, s49, s41\n"
"s_xor_b32 s51, s51, s74\n"
"s_lshl_b32 s94, s94, 10\n"
"s_add_u32 s94, s94, 524288\n"
"s_mul_i32 s99, s60, 3\n"
"s_lshr_b32 s78, s79, 1\n"
"s_add_u32 s99, s99, s78\n"
"s_lshr_b32 s78, s60, 1\n"
"s_add_u32 s78, s78, 12\n"
"s_cmp_eq_u32 s79, 6\n"
"s_cselect_b32 s99, s78, s99\n"
"s_mul_i32 s99, s99, 40960\n"
"s_xor_b32 s79, s79, s60\n"
"s_and_b32 s79, s79, 1\n"
"s_lshl_b32 s79, s79, 6\n"
"s_add_u32 s99, s99, s79\n"
"s_add_u32 s99, s99, 1024\n"
"s_cmp_eq_u32 s61, 7\n"
"s_cselect_b32 s99, 0x40000000, s99\n"
"s_cselect_b32 s94, 0x40000000, s94\n"
"s_waitcnt vmcnt(27)\n"
"v_and_b32 v12, 0x7fff0000, v12\n"
"v_and_b32 v13, 0x7fff0000, v13\n"
"s_nop 0\n"
"v_max_u32_dpp v12, v12, v12 quad_perm:[1,0,3,2] row_mask:0xf bank_mask:0xf\n"
"v_max_u32_dpp v13, v13, v13 quad_perm:[1,0,3,2] row_mask:0xf bank_mask:0xf\n"
"s_nop 0\n"
"v_max_u32_dpp v12, v12, v12 quad_perm:[2,3,0,1] row_mask:0xf bank_mask:0xf\n"
"v_max_u32_dpp v13, v13, v13 quad_perm:[2,3,0,1] row_mask:0xf bank_mask:0xf\n"
"s_memtime s[80:81]\n"
"s_memrealtime s[82:83]\n"
"s_nop 0\n"
"v_max_u32_dpp v12, v12, v12 row_half_mirror row_mask:0xf bank_mask:0xf\n"
"v_max_u32_dpp v13, v13, v13 row_half_mirror row_mask:0xf bank_mask:0xf\n"
"s_nop 0\n"
"v_min_u32_dpp v12, v12, v12 row_mirror row_mask:0xf bank_mask:0xf\n"
"v_min_u32_dpp v13, v13, v13 row_mirror row_mask:0xf bank_mask:0xf\n"
"s_nop 0\n"
"v_min_u32_dpp v12, v12, v12 row_bcast:15 row_mask:0xa bank_mask:0xf\n"
"v_min_u32_dpp v13, v13, v13 row_bcast:15 row_mask:0xa bank_mask:0xf\n"
"s_nop 0\n"
"v_min_u32_dpp v12, v12, v12 row_bcast:31 row_mask:0xc bank_mask:0xf\n"
"v_min_u32_dpp v13, v13, v13 row_bcast:31 row_mask:0xc bank_mask:0xf\n"
"s_nop 0\n"
"v_mul_f32 v12, v12, v13\n"
"v_mul_f32 v12, 0x43c80000, v12\n"
"v_min_f32 v12, 0x71800000, v12\n"
"v_mul_f32 v13, s46, v12\n"
"s_nop 0\n"
"v_readlane_b32 s70, v12, 63\n"
"v_readlane_b32 s71, v12, 63\n"
"v_readlane_b32 s69, v13, 63\n"
"s_nop 1\n"
"v_mov_b64 v[228:229], s[70:71]\n"
"v_mov_b64 v[230:231], s[70:71]\n"
"s_waitcnt vmcnt(26)\n"
"v_readfirstlane_b32 s92, v1\n"
"v_mov_b64 v[0:1], s[70:71]\n"
"v_mov_b64 v[2:3], s[70:71]\n"
"s_mov_b32 s70, s46\n"
"s_xor_b32 s71, s69, s77\n"
"s_waitcnt lgkmcnt(0)\n"
"s_sub_u32 s80, s80, s64\n"
"s_sub_u32 s82, s82, s66\n"
"s_mul_i32 s80, s80, 10\n"
"s_mul_i32 s82, s82, 211\n"
"s_cmp_gt_u32 s80, s82\n"
"s_cselect_b32 s64, 1, 0\n"
"s_cselect_b32 s68, 655360, 0\n"
"s_cmp_eq_u32 s42, 0\n"
"s_cselect_b32 s64, s64, 0\n"
"s_mov_b32 s65, 0\n"
"s_add_u32 s66, s66, 2800\n"
"s_mov_b32 s67, s66\n"
"s_mov_b32 s72, 126\n"
"s_mov_b64 vcc, s[64:65]\n"
"s_sub_u32 s63, s63, s44\n"
"s_add_u32 s80, s8, s63\n"
"s_addc_u32 s81, s9, 0\n"
"s_add_u32 s82, s44, 704\n"
"s_cmp_lg_u32 s68, 0\n"
"s_cselect_b32 s82, s82, 0\n"
"s_mov_b32 s83, 0x20000\n"
"s_nop 2\n"
"s_mul_i32 s60, s92, 0x9e3779b1\n"
"s_add_u32 s60, s60, 0x7f4a7c15\n"
"s_waitcnt vmcnt(15)\n"
"s_barrier\n"
"ds_read_b128 a[136:139], v246 offset:0\n"
"ds_read_b128 a[140:143], v247 offset:0\n"
"ds_read_b64_tr_b16 a[192:193], v244 offset:0\n"
"ds_read_b64_tr_b16 a[194:195], v244 offset:512\n"
"ds_read_b64_tr_b16 a[196:197], v244 offset:8192\n"
"ds_read_b64_tr_b16 a[198:199], v244 offset:8704\n"
"ds_read_b64_tr_b16 a[200:201], v245 offset:0\n"
"ds_read_b64_tr_b16 a[202:203], v245 offset:512\n"
"ds_read_b64_tr_b16 a[204:205], v245 offset:8192\n"
"ds_read_b64_tr_b16 a[206:207], v245 offset:8704\n"
"ds_read_b64_tr_b16 a[208:209], v244 offset:1024\n"
"ds_read_b64_tr_b16 a[210:211], v244 offset:1536\n"
"ds_read_b64_tr_b16 a[212:213], v244 offset:9216\n"
"ds_read_b64_tr_b16 a[214:215], v244 offset:9728\n"
"s_waitcnt lgkmcnt(10)\n"
"v_mfma_f32_16x16x32_bf16 v[112:115], a[192:195], a[136:139], v[228:231]\n"
"ds_read_b64_tr_b16 a[216:217], v245 offset:1024\n"
"ds_read_b64_tr_b16 a[218:219], v245 offset:1536\n"
"s_waitcnt lgkmcnt(10)\n"
"v_mfma_f32_16x16x32_bf16 v[112:115], a[196:199], a[140:143], v[112:115]\n"
"ds_read_b64_tr_b16 a[220:221], v245 offset:9216\n"
"ds_read_b64_tr_b16 a[222:223], v245 offset:9728\n"
"s_waitcnt lgkmcnt(10)\n"
"v_mfma_f32_16x16x32_bf16 v[116:119], a[200:203], a[136:139], v[228:231]\n"
"s_waitcnt lgkmcnt(8)\n"
"ds_read_b128 a[144:147], v246 offset:2048\n"
"v_mfma_f32_16x16x32_bf16 v[116:119], a[204:207], a[140:143], v[116:119]\n"
"s_waitcnt lgkmcnt(7)\n"
"ds_read_b128 a[148:151], v247 offset:2048\n"
"v_mfma_f32_16x16x32_bf16 v[120:123], a[208:211], a[136:139], v[228:231]\n"
"s_waitcnt lgkmcnt(6)\n"
"ds_read_b128 a[152:155], v246 offset:4096\n"
"v_mfma_f32_16x16x32_bf16 v[120:123], a[212:215], a[140:143], v[120:123]\n"
"s_waitcnt lgkmcnt(5)\n"
"ds_read_b128 a[156:159], v247 offset:4096\n"
"v_mfma_f32_16x16x32_bf16 v[124:127], a[216:219], a[136:139], v[228:231]\n"
"s_waitcnt lgkmcnt(4)\n"
"ds_read_b128 a[160:163], v252 offset:0\n"
"v_mfma_f32_16x16x32_bf16 v[124:127], a[220:223], a[140:143], v[124:127]\n"
"s_waitcnt lgkmcnt(4)\n"
"ds_read_b128 a[164:167], v253 offset:0\n"
"v_mfma_f32_16x16x32_bf16 v[140:143], a[216:219], a[144:147], v[228:231]\n"
"s_waitcnt lgkmcnt(4)\n"
"ds_read_b128 a[168:171], v250 offset:0\n"
"v_mfma_f32_16x16x32_bf16 v[140:143], a[220:223], a[148:151], v[140:143]\n"
"ds_read_b128 a[172:175], v251 offset:0\n"
"v_mfma_f32_16x16x32_bf16 v[136:139], a[208:211], a[144:147], v[228:231]\n"
"s_waitcnt vmcnt(11)\n"
"ds_read_b128 a[176:179], v250 offset:2048\n"
"v_mfma_f32_16x16x32_bf16 v[136:139], a[212:215], a[148:151], v[136:139]\n"
"s_barrier\n"
"v_mfma_f32_16x16x32_bf16 v[132:135], a[200:203], a[144:147], v[228:231]\n"
"s_add_u32 s40, s48, 256\n"
"s_xor_b32 m0, s50, 0xc000\n"
"ds_read_b128 a[180:183], v251 offset:2048\n"
"v_mfma_f32_16x16x32_bf16 v[132:135], a[204:207], a[148:151], v[132:135]\n"
"buffer_load_dwordx4 v224, s[28:31], s40 offen lds\n"
"v_mfma_f32_16x16x32_bf16 v[128:131], a[192:195], a[144:147], v[228:231]\n"
"ds_read_b128 a[184:187], v250 offset:4096\n"
"v_mfma_f32_16x16x32_bf16 v[128:131], a[196:199], a[148:151], v[128:131]\n"
"s_waitcnt lgkmcnt(5)\n"
"ds_read_b128 a[188:191], v251 offset:4096\n"
"v_mfma_f32_16x16x32_bf16 v[144:147], a[192:195], a[152:155], v[228:231]\n"
"ds_read_b64_tr_b16 a[224:225], v244 offset:16384\n"
"v_mfma_f32_16x16x32_bf16 v[144:147], a[196:199], a[156:159], v[144:147]\n"
"ds_read_b64_tr_b16 a[226:227], v244 offset:16896\n"
"v_mfma_f32_16x16x32_bf16 v[148:151], a[200:203], a[152:155], v[228:231]\n"
"ds_read_b64_tr_b16 a[228:229], v244 offset:24576\n"
"v_mfma_f32_16x16x32_bf16 v[148:151], a[204:207], a[156:159], v[148:151]\n"
"buffer_load_dwordx4 v225, s[28:31], s40 offen offset:1024 lds\n"
"v_mfma_f32_16x16x32_bf16 v[152:155], a[208:211], a[152:155], v[228:231]\n"
"ds_read_b64_tr_b16 a[230:231], v244 offset:25088\n"
"v_mfma_f32_16x16x32_bf16 v[152:155], a[212:215], a[156:159], v[152:155]\n"
"ds_read_b64_tr_b16 a[232:233], v245 offset:16384\n"
"v_mfma_f32_16x16x32_bf16 v[156:159], a[216:219], a[152:155], v[228:231]\n"
"ds_read_b64_tr_b16 a[234:235], v245 offset:16896\n"
"v_mfma_f32_16x16x32_bf16 v[156:159], a[220:223], a[156:159], v[156:159]\n"
"ds_read_b64_tr_b16 a[236:237], v245 offset:24576\n"
"v_mfma_f32_16x16x32_bf16 v[172:175], a[216:219], a[160:163], v[228:231]\n"
"ds_read_b64_tr_b16 a[238:239], v245 offset:25088\n"
"v_mfma_f32_16x16x32_bf16 v[172:175], a[220:223], a[164:167], v[172:175]\n"
"buffer_load_dwordx4 v226, s[28:31], s40 offen offset:2048 lds\n"
"v_mfma_f32_16x16x32_bf16 v[168:171], a[208:211], a[160:163], v[228:231]\n"
"ds_read_b64_tr_b16 a[240:241], v244 offset:17408\n"
"v_mfma_f32_16x16x32_bf16 v[168:171], a[212:215], a[164:167], v[168:171]\n"
"ds_read_b64_tr_b16 a[242:243], v244 offset:17920\n"
"v_mfma_f32_16x16x32_bf16 v[164:167], a[200:203], a[160:163], v[228:231]\n"
"ds_read_b64_tr_b16 a[244:245], v244 offset:25600\n"
"v_mfma_f32_16x16x32_bf16 v[164:167], a[204:207], a[164:167], v[164:167]\n"
"buffer_load_dwordx4 v227, s[28:31], s40 offen offset:3072 lds\n"
"v_mfma_f32_16x16x32_bf16 v[160:163], a[192:195], a[160:163], v[228:231]\n"
"s_cmp_eq_u32 s47, 8\n"
"ds_read_b64_tr_b16 a[246:247], v244 offset:26112\n"
"v_xor_b32 v244, s74, v244\n"
"v_mfma_f32_16x16x32_bf16 v[160:163], a[196:199], a[164:167], v[160:163]\n"
"s_waitcnt lgkmcnt(5)\n"
"s_cselect_b32 s41, s52, s58\n"
"ds_read_b64_tr_b16 a[248:249], v245 offset:17408\n"
"v_xor_b32 v246, s74, v246\n"
"v_mfma_f32_16x16x32_bf16 v[176:179], a[192:195], a[168:171], v[228:231]\n"
"s_add_u32 s48, s48, s41\n"
"ds_read_b64_tr_b16 a[250:251], v245 offset:17920\n"
"v_xor_b32 v247, s74, v247\n"
"v_mfma_f32_16x16x32_bf16 v[176:179], a[196:199], a[172:175], v[176:179]\n"
"s_sub_u32 s41, s48, 5242880\n"
"ds_read_b64_tr_b16 a[252:253], v245 offset:25600\n"
"v_xor_b32 v252, s74, v252\n"
"v_mfma_f32_16x16x32_bf16 v[180:183], a[200:203], a[168:171], v[228:231]\n"
"s_min_u32 s48, s48, s41\n"
"ds_read_b64_tr_b16 a[254:255], v245 offset:26112\n"
"v_xor_b32 v245, s74, v245\n"
"v_mfma_f32_16x16x32_bf16 v[180:183], a[204:207], a[172:175], v[180:183]\n"
"v_xor_b32 v253, s74, v253\n"
"v_xor_b32 v250, s74, v250\n"
"v_mfma_f32_16x16x32_bf16 v[184:187], a[208:211], a[168:171], v[228:231]\n"
"s_mov_b32 m0, s50\n"
"v_xor_b32 v251, s74, v251\n"
"v_mfma_f32_16x16x32_bf16 v[184:187], a[212:215], a[172:175], v[184:187]\n"
"buffer_load_dwordx4 v224, s[28:31], s48 offen lds\n"
"v_mfma_f32_16x16x32_bf16 v[188:191], a[216:219], a[168:171], v[228:231]\n"
"v_mfma_f32_16x16x32_bf16 v[188:191], a[220:223], a[172:175], v[188:191]\n"
"v_mfma_f32_16x16x32_bf16 v[204:207], a[216:219], a[176:179], v[228:231]\n"
"v_mfma_f32_16x16x32_bf16 v[204:207], a[220:223], a[180:183], v[204:207]\n"
"buffer_load_dwordx4 v225, s[28:31], s48 offen offset:1024 lds\n"
"v_mfma_f32_16x16x32_bf16 v[200:203], a[208:211], a[176:179], v[228:231]\n"
"v_mfma_f32_16x16x32_bf16 v[200:203], a[212:215], a[180:183], v[200:203]\n"
"v_mfma_f32_16x16x32_bf16 v[196:199], a[200:203], a[176:179], v[228:231]\n"
"v_mfma_f32_16x16x32_bf16 v[196:199], a[204:207], a[180:183], v[196:199]\n"
"v_mfma_f32_16x16x32_bf16 v[192:195], a[192:195], a[176:179], v[228:231]\n"
"v_mfma_f32_16x16x32_bf16 v[192:195], a[196:199], a[180:183], v[192:195]\n"
"buffer_load_dwordx4 v226, s[28:31], s48 offen offset:2048 lds\n"
"v_mfma_f32_16x16x32_bf16 v[208:211], a[192:195], a[184:187], v[228:231]\n"
"v_mfma_f32_16x16x32_bf16 v[208:211], a[196:199], a[188:191], v[208:211]\n"
"v_mfma_f32_16x16x32_bf16 v[212:215], a[200:203], a[184:187], v[228:231]\n"
"v_mfma_f32_16x16x32_bf16 v[212:215], a[204:207], a[188:191], v[212:215]\n"
"v_mfma_f32_16x16x32_bf16 v[216:219], a[208:211], a[184:187], v[228:231]\n"
"v_mfma_f32_16x16x32_bf16 v[216:219], a[212:215], a[188:191], v[216:219]\n"
"buffer_load_dwordx4 v227, s[28:31], s48 offen offset:3072 lds\n"
"v_mfma_f32_16x16x32_bf16 v[220:223], a[216:219], a[184:187], v[228:231]\n"
"s_xor_b32 s50, s50, s74\n"
"v_mfma_f32_16x16x32_bf16 v[220:223], a[220:223], a[188:191], v[220:223]\n"
"v_mfma_f32_16x16x32_bf16 v[0:3], a[224:227], a[136:139], v[228:231]\n"
"v_mfma_f32_16x16x32_bf16 v[0:3], a[228:231], a[140:143], v[0:3]\n"
"v_mfma_f32_16x16x32_bf16 v[4:7], a[232:235], a[136:139], v[228:231]\n"
"s_waitcnt vmcnt(8) lgkmcnt(0)\n"
"v_mfma_f32_16x16x32_bf16 v[4:7], a[236:239], a[140:143], v[4:7]\n"
"s_barrier\n"
"v_mfma_f32_16x16x32_bf16 v[8:11], a[240:243], a[136:139], v[228:231]\n"
"s_mov_b32 m0, s51\n"
"ds_read_b64_tr_b16 a[192:193], v244 offset:0\n"
"v_mfma_f32_16x16x32_bf16 v[8:11], a[244:247], a[140:143], v[8:11]\n"
"buffer_load_dwordx4 v248, s[24:27], s49 offen lds\n"
"v_mfma_f32_16x16x32_bf16 v[12:15], a[248:251], a[136:139], v[228:231]\n"
"ds_read_b64_tr_b16 a[194:195], v244 offset:512\n"
"v_mfma_f32_16x16x32_bf16 v[12:15], a[252:255], a[140:143], v[12:15]\n"
"ds_read_b128 a[136:139], v246 offset:0\n"
"v_mfma_f32_16x16x32_bf16 v[28:31], a[248:251], a[144:147], v[228:231]\n"
"ds_read_b64_tr_b16 a[196:197], v244 offset:8192\n"
"v_mfma_f32_16x16x32_bf16 v[28:31], a[252:255], a[148:151], v[28:31]\n"
"ds_read_b64_tr_b16 a[198:199], v244 offset:8704\n"
"v_mfma_f32_16x16x32_bf16 v[24:27], a[240:243], a[144:147], v[228:231]\n"
"ds_read_b128 a[140:143], v247 offset:0\n"
"v_mfma_f32_16x16x32_bf16 v[24:27], a[244:247], a[148:151], v[24:27]\n"
"buffer_load_dwordx4 v249, s[24:27], s49 offen offset:1024 lds\n"
"v_mfma_f32_16x16x32_bf16 v[20:23], a[232:235], a[144:147], v[228:231]\n"
"ds_read_b64_tr_b16 a[200:201], v245 offset:0\n"
"v_mfma_f32_16x16x32_bf16 v[20:23], a[236:239], a[148:151], v[20:23]\n"
"ds_read_b64_tr_b16 a[202:203], v245 offset:512\n"
"v_mfma_f32_16x16x32_bf16 v[16:19], a[224:227], a[144:147], v[228:231]\n"
"ds_read_b64_tr_b16 a[204:205], v245 offset:8192\n"
"v_mfma_f32_16x16x32_bf16 v[16:19], a[228:231], a[148:151], v[16:19]\n"
"ds_read_b64_tr_b16 a[206:207], v245 offset:8704\n"
"v_mfma_f32_16x16x32_bf16 v[32:35], a[224:227], a[152:155], v[228:231]\n"
"ds_read_b64_tr_b16 a[208:209], v244 offset:1024\n"
"v_mfma_f32_16x16x32_bf16 v[32:35], a[228:231], a[156:159], v[32:35]\n"
"buffer_load_dwordx4 v254, s[24:27], s49 offen offset:2048 lds\n"
"v_mfma_f32_16x16x32_bf16 v[36:39], a[232:235], a[152:155], v[228:231]\n"
"ds_read_b64_tr_b16 a[210:211], v244 offset:1536\n"
"v_mfma_f32_16x16x32_bf16 v[36:39], a[236:239], a[156:159], v[36:39]\n"
"ds_read_b64_tr_b16 a[212:213], v244 offset:9216\n"
"v_mfma_f32_16x16x32_bf16 v[40:43], a[240:243], a[152:155], v[228:231]\n"
"ds_read_b64_tr_b16 a[214:215], v244 offset:9728\n"
"v_mfma_f32_16x16x32_bf16 v[40:43], a[244:247], a[156:159], v[40:43]\n"
"ds_read_b64_tr_b16 a[216:217], v245 offset:1024\n"
"v_mfma_f32_16x16x32_bf16 v[44:47], a[248:251], a[152:155], v[228:231]\n"
"ds_read_b64_tr_b16 a[218:219], v245 offset:1536\n"
"v_mfma_f32_16x16x32_bf16 v[44:47], a[252:255], a[156:159], v[44:47]\n"
"buffer_load_dwordx4 v255, s[24:27], s49 offen offset:3072 lds\n"
"v_mfma_f32_16x16x32_bf16 v[60:63], a[248:251], a[160:163], v[228:231]\n"
"ds_read_b64_tr_b16 a[220:221], v245 offset:9216\n"
"v_mfma_f32_16x16x32_bf16 v[60:63], a[252:255], a[164:167], v[60:63]\n"
"ds_read_b64_tr_b16 a[222:223], v245 offset:9728\n"
"v_mfma_f32_16x16x32_bf16 v[56:59], a[240:243], a[160:163], v[228:231]\n"
"ds_read_b128 a[144:147], v246 offset:2048\n"
"v_mfma_f32_16x16x32_bf16 v[56:59], a[244:247], a[164:167], v[56:59]\n"
"s_add_u32 s40, s49, 40960\n"
"ds_read_b128 a[148:151], v247 offset:2048\n"
"v_mfma_f32_16x16x32_bf16 v[52:55], a[232:235], a[160:163], v[228:231]\n"
"s_add_u32 m0, s51, 4096\n"
"ds_read_b128 a[152:155], v246 offset:4096\n"
"v_mfma_f32_16x16x32_bf16 v[52:55], a[236:239], a[164:167], v[52:55]\n"
"buffer_load_dwordx4 v248, s[24:27], s40 offen lds\n"
"v_mfma_f32_16x16x32_bf16 v[48:51], a[224:227], a[160:163], v[228:231]\n"
"ds_read_b128 a[156:159], v247 offset:4096\n"
"v_mfma_f32_16x16x32_bf16 v[48:51], a[228:231], a[164:167], v[48:51]\n"
"ds_read_b128 a[160:163], v252 offset:0\n"
"v_mfma_f32_16x16x32_bf16 v[64:67], a[224:227], a[168:171], v[228:231]\n"
"ds_read_b128 a[164:167], v253 offset:0\n"
"v_mfma_f32_16x16x32_bf16 v[64:67], a[228:231], a[172:175], v[64:67]\n"
"v_mfma_f32_16x16x32_bf16 v[68:71], a[232:235], a[168:171], v[228:231]\n"
"v_mfma_f32_16x16x32_bf16 v[68:71], a[236:239], a[172:175], v[68:71]\n"
"buffer_load_dwordx4 v249, s[24:27], s40 offen offset:1024 lds\n"
"v_mfma_f32_16x16x32_bf16 v[72:75], a[240:243], a[168:171], v[228:231]\n"
"v_mfma_f32_16x16x32_bf16 v[72:75], a[244:247], a[172:175], v[72:75]\n"
"v_mfma_f32_16x16x32_bf16 v[76:79], a[248:251], a[168:171], v[228:231]\n"
"ds_read_b128 a[168:171], v250 offset:0\n"
"v_mfma_f32_16x16x32_bf16 v[76:79], a[252:255], a[172:175], v[76:79]\n"
"ds_read_b128 a[172:175], v251 offset:0\n"
"v_mfma_f32_16x16x32_bf16 v[92:95], a[248:251], a[176:179], v[228:231]\n"
"v_mfma_f32_16x16x32_bf16 v[92:95], a[252:255], a[180:183], v[92:95]\n"
"buffer_load_dwordx4 v254, s[24:27], s40 offen offset:2048 lds\n"
"v_mfma_f32_16x16x32_bf16 v[88:91], a[240:243], a[176:179], v[228:231]\n"
"s_add_u32 s49, s49, 128\n"
"v_mfma_f32_16x16x32_bf16 v[88:91], a[244:247], a[180:183], v[88:91]\n"
"s_sub_u32 s41, s49, 1280\n"
"v_mfma_f32_16x16x32_bf16 v[84:87], a[232:235], a[176:179], v[228:231]\n"
"s_min_u32 s49, s49, s41\n"
"v_mfma_f32_16x16x32_bf16 v[84:87], a[236:239], a[180:183], v[84:87]\n"
"s_xor_b32 s51, s51, s74\n"
"v_mfma_f32_16x16x32_bf16 v[80:83], a[224:227], a[176:179], v[228:231]\n"
"ds_read_b128 a[176:179], v250 offset:2048\n"
"v_mfma_f32_16x16x32_bf16 v[80:83], a[228:231], a[180:183], v[80:83]\n"
"ds_read_b128 a[180:183], v251 offset:2048\n"
"v_mfma_f32_16x16x32_bf16 v[96:99], a[224:227], a[184:187], v[228:231]\n"
"v_mfma_f32_16x16x32_bf16 v[96:99], a[228:231], a[188:191], v[96:99]\n"
"v_mfma_f32_16x16x32_bf16 v[100:103], a[232:235], a[184:187], v[228:231]\n"
"v_mfma_f32_16x16x32_bf16 v[100:103], a[236:239], a[188:191], v[100:103]\n"
"v_mfma_f32_16x16x32_bf16 v[104:107], a[240:243], a[184:187], v[228:231]\n"
"v_mfma_f32_16x16x32_bf16 v[104:107], a[244:247], a[188:191], v[104:107]\n"
"v_mfma_f32_16x16x32_bf16 v[108:111], a[248:251], a[184:187], v[228:231]\n"
"s_add_u32 s47, s47, 1\n"
"ds_read_b128 a[184:187], v250 offset:4096\n"
"v_mfma_f32_16x16x32_bf16 v[108:111], a[252:255], a[188:191], v[108:111]\n"
"s_waitcnt lgkmcnt(1)\n"
"ds_read_b128 a[188:191], v251 offset:4096\n"
"LPE_%=:\n"
"s_cmp_lg_u32 s68, 0\n"
"s_cbranch_scc1 LPN_%=\n"
"LPL_%=:\n"
"v_mfma_f32_16x16x32_bf16 v[112:115], a[192:195], a[136:139], v[112:115]\n"
"v_mfma_f32_16x16x32_bf16 v[112:115], a[196:199], a[140:143], v[112:115]\n"
"v_mfma_f32_16x16x32_bf16 v[116:119], a[200:203], a[136:139], v[116:119]\n"
"s_waitcnt vmcnt(11)\n"
"v_mfma_f32_16x16x32_bf16 v[116:119], a[204:207], a[140:143], v[116:119]\n"
"s_barrier\n"
"v_mfma_f32_16x16x32_bf16 v[120:123], a[208:211], a[136:139], v[120:123]\n"
"s_add_u32 s40, s48, 256\n"
"s_xor_b32 m0, s50, 0xc000\n"
"ds_read_b64_tr_b16 a[224:225], v244 offset:16384\n"
"v_mfma_f32_16x16x32_bf16 v[120:123], a[212:215], a[140:143], v[120:123]\n"
"buffer_load_dwordx4 v224, s[28:31], s40 offen lds\n"
"v_mfma_f32_16x16x32_bf16 v[124:127], a[216:219], a[136:139], v[124:127]\n"
"ds_read_b64_tr_b16 a[226:227], v244 offset:16896\n"
"v_mfma_f32_16x16x32_bf16 v[124:127], a[220:223], a[140:143], v[124:127]\n"
"ds_read_b64_tr_b16 a[228:229], v244 offset:24576\n"
"v_mfma_f32_16x16x32_bf16 v[140:143], a[216:219], a[144:147], v[140:143]\n"
"ds_read_b64_tr_b16 a[230:231], v244 offset:25088\n"
"v_mfma_f32_16x16x32_bf16 v[140:143], a[220:223], a[148:151], v[140:143]\n"
"ds_read_b64_tr_b16 a[232:233], v245 offset:16384\n"
"v_mfma_f32_16x16x32_bf16 v[136:139], a[208:211], a[144:147], v[136:139]\n"
"ds_read_b64_tr_b16 a[234:235], v245 offset:16896\n"
"v_mfma_f32_16x16x32_bf16 v[136:139], a[212:215], a[148:151], v[136:139]\n"
"buffer_load_dwordx4 v225, s[28:31], s40 offen offset:1024 lds\n"
"v_mfma_f32_16x16x32_bf16 v[132:135], a[200:203], a[144:147], v[132:135]\n"
"ds_read_b64_tr_b16 a[236:237], v245 offset:24576\n"
"v_mfma_f32_16x16x32_bf16 v[132:135], a[204:207], a[148:151], v[132:135]\n"
"ds_read_b64_tr_b16 a[238:239], v245 offset:25088\n"
"v_mfma_f32_16x16x32_bf16 v[128:131], a[192:195], a[144:147], v[128:131]\n"
"ds_read_b64_tr_b16 a[240:241], v244 offset:17408\n"
"v_mfma_f32_16x16x32_bf16 v[128:131], a[196:199], a[148:151], v[128:131]\n"
"ds_read_b64_tr_b16 a[242:243], v244 offset:17920\n"
"v_mfma_f32_16x16x32_bf16 v[144:147], a[192:195], a[152:155], v[144:147]\n"
"ds_read_b64_tr_b16 a[244:245], v244 offset:25600\n"
"v_mfma_f32_16x16x32_bf16 v[144:147], a[196:199], a[156:159], v[144:147]\n"
"buffer_load_dwordx4 v226, s[28:31], s40 offen offset:2048 lds\n"
"v_mfma_f32_16x16x32_bf16 v[148:151], a[200:203], a[152:155], v[148:151]\n"
"ds_read_b64_tr_b16 a[246:247], v244 offset:26112\n"
"v_mfma_f32_16x16x32_bf16 v[148:151], a[204:207], a[156:159], v[148:151]\n"
"ds_read_b64_tr_b16 a[248:249], v245 offset:17408\n"
"v_mfma_f32_16x16x32_bf16 v[152:155], a[208:211], a[152:155], v[152:155]\n"
"ds_read_b64_tr_b16 a[250:251], v245 offset:17920\n"
"v_mfma_f32_16x16x32_bf16 v[152:155], a[212:215], a[156:159], v[152:155]\n"
"ds_read_b64_tr_b16 a[252:253], v245 offset:25600\n"
"v_mfma_f32_16x16x32_bf16 v[156:159], a[216:219], a[152:155], v[156:159]\n"
"ds_read_b64_tr_b16 a[254:255], v245 offset:26112\n"
"v_mfma_f32_16x16x32_bf16 v[156:159], a[220:223], a[156:159], v[156:159]\n"
"buffer_load_dwordx4 v227, s[28:31], s40 offen offset:3072 lds\n"
"v_mfma_f32_16x16x32_bf16 v[172:175], a[216:219], a[160:163], v[172:175]\n"
"s_cmp_eq_u32 s47, 8\n"
"v_mfma_f32_16x16x32_bf16 v[172:175], a[220:223], a[164:167], v[172:175]\n"
"s_cselect_b32 s41, s52, s58\n"
"v_mfma_f32_16x16x32_bf16 v[168:171], a[208:211], a[160:163], v[168:171]\n"
"s_add_u32 s48, s48, s41\n"
"v_mfma_f32_16x16x32_bf16 v[168:171], a[212:215], a[164:167], v[168:171]\n"
"s_sub_u32 s41, s48, 5242880\n"
"v_mfma_f32_16x16x32_bf16 v[164:167], a[200:203], a[160:163], v[164:167]\n"
"s_min_u32 s48, s48, s41\n"
"v_mfma_f32_16x16x32_bf16 v[164:167], a[204:207], a[164:167], v[164:167]\n"
"v_xor_b32 v244, s74, v244\n"
"v_xor_b32 v246, s74, v246\n"
"v_mfma_f32_16x16x32_bf16 v[160:163], a[192:195], a[160:163], v[160:163]\n"
"s_mov_b32 m0, s50\n"
"v_xor_b32 v245, s74, v245\n"
"v_xor_b32 v247, s74, v247\n"
"v_mfma_f32_16x16x32_bf16 v[160:163], a[196:199], a[164:167], v[160:163]\n"
"buffer_load_dwordx4 v224, s[28:31], s48 offen lds\n"
"v_mfma_f32_16x16x32_bf16 v[176:179], a[192:195], a[168:171], v[176:179]\n"
"v_xor_b32 v252, s74, v252\n"
"v_xor_b32 v253, s74, v253\n"
"v_mfma_f32_16x16x32_bf16 v[176:179], a[196:199], a[172:175], v[176:179]\n"
"v_xor_b32 v250, s74, v250\n"
"v_xor_b32 v251, s74, v251\n"
"v_mfma_f32_16x16x32_bf16 v[180:183], a[200:203], a[168:171], v[180:183]\n"
"v_mfma_f32_16x16x32_bf16 v[180:183], a[204:207], a[172:175], v[180:183]\n"
"buffer_load_dwordx4 v225, s[28:31], s48 offen offset:1024 lds\n"
"v_mfma_f32_16x16x32_bf16 v[184:187], a[208:211], a[168:171], v[184:187]\n"
"v_mfma_f32_16x16x32_bf16 v[184:187], a[212:215], a[172:175], v[184:187]\n"
"v_mfma_f32_16x16x32_bf16 v[188:191], a[216:219], a[168:171], v[188:191]\n"
"v_mfma_f32_16x16x32_bf16 v[188:191], a[220:223], a[172:175], v[188:191]\n"
"v_mfma_f32_16x16x32_bf16 v[204:207], a[216:219], a[176:179], v[204:207]\n"
"v_mfma_f32_16x16x32_bf16 v[204:207], a[220:223], a[180:183], v[204:207]\n"
"buffer_load_dwordx4 v226, s[28:31], s48 offen offset:2048 lds\n"
"v_mfma_f32_16x16x32_bf16 v[200:203], a[208:211], a[176:179], v[200:203]\n"
"v_mfma_f32_16x16x32_bf16 v[200:203], a[212:215], a[180:183], v[200:203]\n"
"v_mfma_f32_16x16x32_bf16 v[196:199], a[200:203], a[176:179], v[196:199]\n"
"v_mfma_f32_16x16x32_bf16 v[196:199], a[204:207], a[180:183], v[196:199]\n"
"v_mfma_f32_16x16x32_bf16 v[192:195], a[192:195], a[176:179], v[192:195]\n"
"v_mfma_f32_16x16x32_bf16 v[192:195], a[196:199], a[180:183], v[192:195]\n"
"s_waitcnt lgkmcnt(0)\n"
"buffer_load_dwordx4 v227, s[28:31], s48 offen offset:3072 lds\n"
"v_mfma_f32_16x16x32_bf16 v[208:211], a[192:195], a[184:187], v[208:211]\n"
"s_xor_b32 s50, s50, s74\n"
"v_mfma_f32_16x16x32_bf16 v[208:211], a[196:199], a[188:191], v[208:211]\n"
"v_mfma_f32_16x16x32_bf16 v[212:215], a[200:203], a[184:187], v[212:215]\n"
"v_mfma_f32_16x16x32_bf16 v[212:215], a[204:207], a[188:191], v[212:215]\n"
"v_mfma_f32_16x16x32_bf16 v[216:219], a[208:211], a[184:187], v[216:219]\n"
"v_mfma_f32_16x16x32_bf16 v[216:219], a[212:215], a[188:191], v[216:219]\n"
"v_mfma_f32_16x16x32_bf16 v[220:223], a[216:219], a[184:187], v[220:223]\n"
"v_mfma_f32_16x16x32_bf16 v[220:223], a[220:223], a[188:191], v[220:223]\n"
"v_mfma_f32_16x16x32_bf16 v[0:3], a[224:227], a[136:139], v[0:3]\n"
"v_mfma_f32_16x16x32_bf16 v[0:3], a[228:231], a[140:143], v[0:3]\n"
"v_mfma_f32_16x16x32_bf16 v[4:7], a[232:235], a[136:139], v[4:7]\n"
"s_waitcnt vmcnt(8)\n"
"v_mfma_f32_16x16x32_bf16 v[4:7], a[236:239], a[140:143], v[4:7]\n"
"s_barrier\n"
"v_mfma_f32_16x16x32_bf16 v[8:11], a[240:243], a[136:139], v[8:11]\n"
"s_mov_b32 m0, s51\n"
"ds_read_b64_tr_b16 a[192:193], v244 offset:0\n"
"v_mfma_f32_16x16x32_bf16 v[8:11], a[244:247], a[140:143], v[8:11]\n"
"buffer_load_dwordx4 v248, s[24:27], s49 offen lds\n"
"v_mfma_f32_16x16x32_bf16 v[12:15], a[248:251], a[136:139], v[12:15]\n"
"ds_read_b64_tr_b16 a[194:195], v244 offset:512\n"
"v_mfma_f32_16x16x32_bf16 v[12:15], a[252:255], a[140:143], v[12:15]\n"
"ds_read_b128 a[136:139], v246 offset:0\n"
"v_mfma_f32_16x16x32_bf16 v[28:31], a[248:251], a[144:147], v[28:31]\n"
"ds_read_b64_tr_b16 a[196:197], v244 offset:8192\n"
"v_mfma_f32_16x16x32_bf16 v[28:31], a[252:255], a[148:151], v[28:31]\n"
"ds_read_b64_tr_b16 a[198:199], v244 offset:8704\n"
"v_mfma_f32_16x16x32_bf16 v[24:27], a[240:243], a[144:147], v[24:27]\n"
"ds_read_b128 a[140:143], v247 offset:0\n"
"v_mfma_f32_16x16x32_bf16 v[24:27], a[244:247], a[148:151], v[24:27]\n"
"buffer_load_dwordx4 v249, s[24:27], s49 offen offset:1024 lds\n"
"v_mfma_f32_16x16x32_bf16 v[20:23], a[232:235], a[144:147], v[20:23]\n"
"ds_read_b64_tr_b16 a[200:201], v245 offset:0\n"
"v_mfma_f32_16x16x32_bf16 v[20:23], a[236:239], a[148:151], v[20:23]\n"
"ds_read_b64_tr_b16 a[202:203], v245 offset:512\n"
"v_mfma_f32_16x16x32_bf16 v[16:19], a[224:227], a[144:147], v[16:19]\n"
"ds_read_b64_tr_b16 a[204:205], v245 offset:8192\n"
"v_mfma_f32_16x16x32_bf16 v[16:19], a[228:231], a[148:151], v[16:19]\n"
"ds_read_b64_tr_b16 a[206:207], v245 offset:8704\n"
"v_mfma_f32_16x16x32_bf16 v[32:35], a[224:227], a[152:155], v[32:35]\n"
"ds_read_b64_tr_b16 a[208:209], v244 offset:1024\n"
"v_mfma_f32_16x16x32_bf16 v[32:35], a[228:231], a[156:159], v[32:35]\n"
"buffer_load_dwordx4 v254, s[24:27], s49 offen offset:2048 lds\n"
"v_mfma_f32_16x16x32_bf16 v[36:39], a[232:235], a[152:155], v[36:39]\n"
"ds_read_b64_tr_b16 a[210:211], v244 offset:1536\n"
"v_mfma_f32_16x16x32_bf16 v[36:39], a[236:239], a[156:159], v[36:39]\n"
"ds_read_b64_tr_b16 a[212:213], v244 offset:9216\n"
"v_mfma_f32_16x16x32_bf16 v[40:43], a[240:243], a[152:155], v[40:43]\n"
"ds_read_b64_tr_b16 a[214:215], v244 offset:9728\n"
"v_mfma_f32_16x16x32_bf16 v[40:43], a[244:247], a[156:159], v[40:43]\n"
"ds_read_b64_tr_b16 a[216:217], v245 offset:1024\n"
"v_mfma_f32_16x16x32_bf16 v[44:47], a[248:251], a[152:155], v[44:47]\n"
"ds_read_b64_tr_b16 a[218:219], v245 offset:1536\n"
"v_mfma_f32_16x16x32_bf16 v[44:47], a[252:255], a[156:159], v[44:47]\n"
"buffer_load_dwordx4 v255, s[24:27], s49 offen offset:3072 lds\n"
"v_mfma_f32_16x16x32_bf16 v[60:63], a[248:251], a[160:163], v[60:63]\n"
"ds_read_b64_tr_b16 a[220:221], v245 offset:9216\n"
"v_mfma_f32_16x16x32_bf16 v[60:63], a[252:255], a[164:167], v[60:63]\n"
"ds_read_b64_tr_b16 a[222:223], v245 offset:9728\n"
"v_mfma_f32_16x16x32_bf16 v[56:59], a[240:243], a[160:163], v[56:59]\n"
"ds_read_b128 a[144:147], v246 offset:2048\n"
"v_mfma_f32_16x16x32_bf16 v[56:59], a[244:247], a[164:167], v[56:59]\n"
"s_add_u32 s40, s49, 40960\n"
"ds_read_b128 a[148:151], v247 offset:2048\n"
"v_mfma_f32_16x16x32_bf16 v[52:55], a[232:235], a[160:163], v[52:55]\n"
"s_add_u32 m0, s51, 4096\n"
"ds_read_b128 a[152:155], v246 offset:4096\n"
"v_mfma_f32_16x16x32_bf16 v[52:55], a[236:239], a[164:167], v[52:55]\n"
"buffer_load_dwordx4 v248, s[24:27], s40 offen lds\n"
"v_mfma_f32_16x16x32_bf16 v[48:51], a[224:227], a[160:163], v[48:51]\n"
"ds_read_b128 a[156:159], v247 offset:4096\n"
"v_mfma_f32_16x16x32_bf16 v[48:51], a[228:231], a[164:167], v[48:51]\n"
"ds_read_b128 a[160:163], v252 offset:0\n"
"v_mfma_f32_16x16x32_bf16 v[64:67], a[224:227], a[168:171], v[64:67]\n"
"ds_read_b128 a[164:167], v253 offset:0\n"
"v_mfma_f32_16x16x32_bf16 v[64:67], a[228:231], a[172:175], v[64:67]\n"
"v_mfma_f32_16x16x32_bf16 v[68:71], a[232:235], a[168:171], v[68:71]\n"
"v_mfma_f32_16x16x32_bf16 v[68:71], a[236:239], a[172:175], v[68:71]\n"
"buffer_load_dwordx4 v249, s[24:27], s40 offen offset:1024 lds\n"
"v_mfma_f32_16x16x32_bf16 v[72:75], a[240:243], a[168:171], v[72:75]\n"
"v_mfma_f32_16x16x32_bf16 v[72:75], a[244:247], a[172:175], v[72:75]\n"
"v_mfma_f32_16x16x32_bf16 v[76:79], a[248:251], a[168:171], v[76:79]\n"
"ds_read_b128 a[168:171], v250 offset:0\n"
"v_mfma_f32_16x16x32_bf16 v[76:79], a[252:255], a[172:175], v[76:79]\n"
"ds_read_b128 a[172:175], v251 offset:0\n"
"v_mfma_f32_16x16x32_bf16 v[92:95], a[248:251], a[176:179], v[92:95]\n"
"v_mfma_f32_16x16x32_bf16 v[92:95], a[252:255], a[180:183], v[92:95]\n"
"buffer_load_dwordx4 v254, s[24:27], s40 offen offset:2048 lds\n"
"v_mfma_f32_16x16x32_bf16 v[88:91], a[240:243], a[176:179], v[88:91]\n"
"s_add_u32 s49, s49, 128\n"
"v_mfma_f32_16x16x32_bf16 v[88:91], a[244:247], a[180:183], v[88:91]\n"
"s_sub_u32 s41, s49, 1280\n"
"v_mfma_f32_16x16x32_bf16 v[84:87], a[232:235], a[176:179], v[84:87]\n"
"s_min_u32 s49, s49, s41\n"
"v_mfma_f32_16x16x32_bf16 v[84:87], a[236:239], a[180:183], v[84:87]\n"
"s_xor_b32 s51, s51, s74\n"
"v_mfma_f32_16x16x32_bf16 v[80:83], a[224:227], a[176:179], v[80:83]\n"
"ds_read_b128 a[176:179], v250 offset:2048\n"
"v_mfma_f32_16x16x32_bf16 v[80:83], a[228:231], a[180:183], v[80:83]\n"
"ds_read_b128 a[180:183], v251 offset:2048\n"
"v_mfma_f32_16x16x32_bf16 v[96:99], a[224:227], a[184:187], v[96:99]\n"
"v_mfma_f32_16x16x32_bf16 v[96:99], a[228:231], a[188:191], v[96:99]\n"
"v_mfma_f32_16x16x32_bf16 v[100:103], a[232:235], a[184:187], v[100:103]\n"
"v_mfma_f32_16x16x32_bf16 v[100:103], a[236:239], a[188:191], v[100:103]\n"
"v_mfma_f32_16x16x32_bf16 v[104:107], a[240:243], a[184:187], v[104:107]\n"
"v_mfma_f32_16x16x32_bf16 v[104:107], a[244:247], a[188:191], v[104:107]\n"
"v_mfma_f32_16x16x32_bf16 v[108:111], a[248:251], a[184:187], v[108:111]\n"
"s_add_u32 s47, s47, 1\n"
"ds_read_b128 a[184:187], v250 offset:4096\n"
"v_mfma_f32_16x16x32_bf16 v[108:111], a[252:255], a[188:191], v[108:111]\n"
"s_waitcnt lgkmcnt(1)\n"
"s_cmp_lg_u32 s47, s62\n"
"ds_read_b128 a[188:191], v251 offset:4096\n"
"s_cbranch_scc1 LPL_%=\n"
"LPX_%=:\n"
"s_cmp_eq_u32 s47, 18\n"
"s_cbranch_scc1 LPT2_%=\n"
"v_mfma_f32_16x16x32_bf16 v[112:115], a[192:195], a[136:139], v[112:115]\n"
"s_add_u32 s78, s96, 8192\n"
"v_mov_b32 v241, 1\n"
"v_mfma_f32_16x16x32_bf16 v[112:115], a[196:199], a[140:143], v[112:115]\n"
"s_mov_b64 exec, 1\n"
"buffer_store_dword v241, off, s[32:35], s96 sc0 sc1\n"
"buffer_store_dword v241, off, s[32:35], s78 sc0 sc1\n"
"s_mov_b64 exec, -1\n"
"v_mfma_f32_16x16x32_bf16 v[116:119], a[200:203], a[136:139], v[116:119]\n"
"s_waitcnt vmcnt(13)\n"
"v_mfma_f32_16x16x32_bf16 v[116:119], a[204:207], a[140:143], v[116:119]\n"
"s_barrier\n"
"v_mfma_f32_16x16x32_bf16 v[120:123], a[208:211], a[136:139], v[120:123]\n"
"s_add_u32 s40, s48, 256\n"
"s_xor_b32 m0, s50, 0xc000\n"
"ds_read_b64_tr_b16 a[238:239], v245 offset:25088\n"
"v_mfma_f32_16x16x32_bf16 v[120:123], a[212:215], a[140:143], v[120:123]\n"
"buffer_load_dwordx4 v224, s[28:31], s40 offen lds\n"
"v_mfma_f32_16x16x32_bf16 v[124:127], a[216:219], a[136:139], v[124:127]\n"
"v_mfma_f32_16x16x32_bf16 v[124:127], a[220:223], a[140:143], v[124:127]\n"
"v_mfma_f32_16x16x32_bf16 v[188:191], a[216:219], a[168:171], v[188:191]\n"
"ds_read_b64_tr_b16 a[254:255], v245 offset:26112\n"
"v_mfma_f32_16x16x32_bf16 v[188:191], a[220:223], a[172:175], v[188:191]\n"
"ds_read_b64_tr_b16 a[252:253], v245 offset:25600\n"
"v_mfma_f32_16x16x32_bf16 v[184:187], a[208:211], a[168:171], v[184:187]\n"
"ds_read_b64_tr_b16 a[224:225], v244 offset:16384\n"
"ds_read_b64_tr_b16 a[228:229], v244 offset:24576\n"
"v_mfma_f32_16x16x32_bf16 v[184:187], a[212:215], a[172:175], v[184:187]\n"
"buffer_load_dwordx4 v225, s[28:31], s40 offen offset:1024 lds\n"
"v_mfma_f32_16x16x32_bf16 v[180:183], a[200:203], a[168:171], v[180:183]\n"
"v_mfma_f32_16x16x32_bf16 v[180:183], a[204:207], a[172:175], v[180:183]\n"
"v_mfma_f32_16x16x32_bf16 v[176:179], a[192:195], a[168:171], v[176:179]\n"
"ds_read_b64_tr_b16 a[230:231], v244 offset:25088\n"
"v_mfma_f32_16x16x32_bf16 v[176:179], a[196:199], a[172:175], v[176:179]\n"
"ds_read_b64_tr_b16 a[244:245], v244 offset:25600\n"
"v_mfma_f32_16x16x32_bf16 v[192:195], a[192:195], a[176:179], v[192:195]\n"
"ds_read_b64_tr_b16 a[226:227], v244 offset:16896\n"
"ds_read_b64_tr_b16 a[250:251], v245 offset:17920\n"
"v_mfma_f32_16x16x32_bf16 v[192:195], a[196:199], a[180:183], v[192:195]\n"
"buffer_load_dwordx4 v226, s[28:31], s40 offen offset:2048 lds\n"
"v_mfma_f32_16x16x32_bf16 v[196:199], a[200:203], a[176:179], v[196:199]\n"
"v_mfma_f32_16x16x32_bf16 v[196:199], a[204:207], a[180:183], v[196:199]\n"
"v_mfma_f32_16x16x32_bf16 v[200:203], a[208:211], a[176:179], v[200:203]\n"
"ds_read_b64_tr_b16 a[236:237], v245 offset:24576\n"
"ds_read_b64_tr_b16 a[246:247], v244 offset:26112\n"
"v_mfma_f32_16x16x32_bf16 v[200:203], a[212:215], a[180:183], v[200:203]\n"
"ds_read_b64_tr_b16 a[240:241], v244 offset:17408\n"
"v_mfma_f32_16x16x32_bf16 v[204:207], a[216:219], a[176:179], v[204:207]\n"
"ds_read_b64_tr_b16 a[242:243], v244 offset:17920\n"
"ds_read_b64_tr_b16 a[248:249], v245 offset:17408\n"
"v_mfma_f32_16x16x32_bf16 v[204:207], a[220:223], a[180:183], v[204:207]\n"
"s_waitcnt lgkmcnt(7)\n"
"buffer_load_dwordx4 v227, s[28:31], s40 offen offset:3072 lds\n"
"v_mfma_f32_16x16x32_bf16 v[220:223], a[216:219], a[184:187], v[220:223]\n"
"s_cmp_eq_u32 s47, 8\n"
"v_mfma_f32_16x16x32_bf16 v[220:223], a[220:223], a[188:191], v[220:223]\n"
"s_cselect_b32 s41, s52, s58\n"
"s_nop 1\n"
"v_pk_fma_f32 v[176:177], v[176:177], s[70:71], s[70:71] op_sel:[0,0,1] op_sel_hi:[1,0,1]\nv_pk_fma_f32 v[178:179], v[178:179], s[70:71], s[70:71] op_sel:[0,0,1] op_sel_hi:[1,0,1]\nv_pk_fma_f32 v[180:181], v[180:181], s[70:71], s[70:71] op_sel:[0,0,1] op_sel_hi:[1,0,1]\nv_pk_fma_f32 v[182:183], v[182:183], s[70:71], s[70:71] op_sel:[0,0,1] op_sel_hi:[1,0,1]\nv_pk_fma_f32 v[184:185], v[184:185], s[70:71], s[70:71] op_sel:[0,0,1] op_sel_hi:[1,0,1]\nv_pk_fma_f32 v[186:187], v[186:187], s[70:71], s[70:71] op_sel:[0,0,1] op_sel_hi:[1,0,1]\nv_pk_fma_f32 v[188:189], v[188:189], s[70:71], s[70:71] op_sel:[0,0,1] op_sel_hi:[1,0,1]\nv_pk_fma_f32 v[190:191], v[190:191], s[70:71], s[70:71] op_sel:[0,0,1] op_sel_hi:[1,0,1]\nv_pk_fma_f32 v[192:193], v[192:193], s[70:71], s[70:71] op_sel:[0,0,1] op_sel_hi:[1,0,1]\nv_pk_fma_f32 v[194:195], v[194:195], s[70:71], s[70:71] op_sel:[0,0,1] op_sel_hi:[1,0,1]\nv_pk_fma_f32 v[196:197], v[196:197], s[70:71], s[70:71] op_sel:[0,0,1] op_sel_hi:[1,0,1]\nv_pk_fma_f32 v[198:199], v[198:199], s[70:71], s[70:71] op_sel:[0,0,1] op_sel_hi:[1,0,1]\nv_pk_fma_f32 v[200:201], v[200:201], s[70:71], s[70:71] op_sel:[0,0,1] op_sel_hi:[1,0,1]\nv_pk_fma_f32 v[202:203], v[202:203], s[70:71], s[70:71] op_sel:[0,0,1] op_sel_hi:[1,0,1]\nv_pk_fma_f32 v[204:205], v[204:205], s[70:71], s[70:71] op_sel:[0,0,1] op_sel_hi:[1,0,1]\nv_pk_fma_f32 v[206:207], v[206:207], s[70:71], s[70:71] op_sel:[0,0,1] op_sel_hi:[1,0,1]\n"
"v_mfma_f32_16x16x32_bf16 v[216:219], a[208:211], a[184:187], v[216:219]\n"
"s_add_u32 s48, s48, s41\n"
"v_cvt_pk_bf16_f32 v192, v192, v193\n"
"v_cvt_pk_bf16_f32 v193, v194, v195\n"
"v_mfma_f32_16x16x32_bf16 v[216:219], a[212:215], a[188:191], v[216:219]\n"
"s_sub_u32 s41, s48, 5242880\n"
"v_cvt_pk_bf16_f32 v176, v176, v177\n"
"v_cvt_pk_bf16_f32 v177, v178, v179\n"
"v_mfma_f32_16x16x32_bf16 v[212:215], a[200:203], a[184:187], v[212:215]\n"
"s_min_u32 s48, s48, s41\n"
"v_cvt_pk_bf16_f32 v178, v180, v181\n"
"v_cvt_pk_bf16_f32 v179, v182, v183\n"
"v_mfma_f32_16x16x32_bf16 v[212:215], a[204:207], a[188:191], v[212:215]\n"
"ds_read_b64_tr_b16 a[234:235], v245 offset:16896\n"
"v_xor_b32 v246, s74, v246\n"
"v_mfma_f32_16x16x32_bf16 v[208:211], a[192:195], a[184:187], v[208:211]\n"
"s_mov_b32 m0, s50\n"
"v_and_b32 v182, 0xf0, v242\n"
"v_cvt_pk_bf16_f32 v200, v200, v201\n"
"v_mfma_f32_16x16x32_bf16 v[208:211], a[196:199], a[188:191], v[208:211]\n"
"buffer_load_dwordx4 v224, s[28:31], s48 offen lds\n"
"v_mfma_f32_16x16x32_bf16 v[160:163], a[192:195], a[160:163], v[160:163]\n"
"v_bfe_u32 v180, v242, 9, 1\n"
"v_lshl_add_u32 v180, v180, 8, v182\n"
"v_mfma_f32_16x16x32_bf16 v[160:163], a[196:199], a[164:167], v[160:163]\n"
"v_bfe_u32 v182, v242, 8, 1\n"
"v_cvt_pk_bf16_f32 v184, v184, v185\n"
"v_mfma_f32_16x16x32_bf16 v[164:167], a[200:203], a[160:163], v[164:167]\n"
"ds_read_b64_tr_b16 a[232:233], v245 offset:16384\n"
"v_xor_b32 v247, s74, v247\n"
"v_mfma_f32_16x16x32_bf16 v[164:167], a[204:207], a[164:167], v[164:167]\n"
"buffer_load_dwordx4 v225, s[28:31], s48 offen offset:1024 lds\n"
"v_mfma_f32_16x16x32_bf16 v[168:171], a[208:211], a[160:163], v[168:171]\n"
"v_cvt_pk_bf16_f32 v201, v202, v203\n"
"v_xor_b32 v253, s74, v253\n"
"v_mfma_f32_16x16x32_bf16 v[168:171], a[212:215], a[164:167], v[168:171]\n"
"v_cvt_pk_bf16_f32 v194, v196, v197\n"
"v_xor_b32 v244, s74, v244\n"
"v_mfma_f32_16x16x32_bf16 v[172:175], a[216:219], a[160:163], v[172:175]\n"
"v_lshl_add_u32 v180, v182, 3, v180\n"
"v_cvt_pk_bf16_f32 v185, v186, v187\n"
"v_mfma_f32_16x16x32_bf16 v[172:175], a[220:223], a[164:167], v[172:175]\n"
"v_cvt_pk_bf16_f32 v186, v188, v189\n"
"v_cvt_pk_bf16_f32 v187, v190, v191\n"
"v_mfma_f32_16x16x32_bf16 v[140:143], a[216:219], a[144:147], v[140:143]\n"
"v_cvt_pk_bf16_f32 v203, v206, v207\n"
"v_bfe_u32 v181, v242, 9, 1\n"
"v_mfma_f32_16x16x32_bf16 v[140:143], a[220:223], a[148:151], v[140:143]\n"
"buffer_load_dwordx4 v226, s[28:31], s48 offen offset:2048 lds\n"
"v_mfma_f32_16x16x32_bf16 v[136:139], a[208:211], a[144:147], v[136:139]\n"
"v_xor_b32_e64 v182, s42, 1\n"
"v_lshl_add_u32 v180, v182, 10, v180\n"
"v_mfma_f32_16x16x32_bf16 v[136:139], a[212:215], a[148:151], v[136:139]\n"
"v_cvt_pk_bf16_f32 v202, v204, v205\n"
"v_xor_b32 v252, s74, v252\n"
"v_mfma_f32_16x16x32_bf16 v[132:135], a[200:203], a[144:147], v[132:135]\n"
"v_add_u32 v180, 94208, v180\n"
"v_and_b32 v182, 0xf0, v242\n"
"v_mfma_f32_16x16x32_bf16 v[132:135], a[204:207], a[148:151], v[132:135]\n"
"ds_write2st64_b64 v180, v[184:185], v[186:187] offset0:64 offset1:65\n"
"v_pk_fma_f32 v[208:209], v[208:209], s[70:71], s[70:71] op_sel:[0,0,1] op_sel_hi:[1,0,1]\nv_pk_fma_f32 v[210:211], v[210:211], s[70:71], s[70:71] op_sel:[0,0,1] op_sel_hi:[1,0,1]\nv_pk_fma_f32 v[212:213], v[212:213], s[70:71], s[70:71] op_sel:[0,0,1] op_sel_hi:[1,0,1]\nv_pk_fma_f32 v[214:215], v[214:215], s[70:71], s[70:71] op_sel:[0,0,1] op_sel_hi:[1,0,1]\nv_pk_fma_f32 v[216:217], v[216:217], s[70:71], s[70:71] op_sel:[0,0,1] op_sel_hi:[1,0,1]\nv_pk_fma_f32 v[218:219], v[218:219], s[70:71], s[70:71] op_sel:[0,0,1] op_sel_hi:[1,0,1]\nv_pk_fma_f32 v[220:221], v[220:221], s[70:71], s[70:71] op_sel:[0,0,1] op_sel_hi:[1,0,1]\nv_pk_fma_f32 v[222:223], v[222:223], s[70:71], s[70:71] op_sel:[0,0,1] op_sel_hi:[1,0,1]\nv_pk_fma_f32 v[160:161], v[160:161], s[70:71], s[70:71] op_sel:[0,0,1] op_sel_hi:[1,0,1]\nv_pk_fma_f32 v[162:163], v[162:163], s[70:71], s[70:71] op_sel:[0,0,1] op_sel_hi:[1,0,1]\nv_pk_fma_f32 v[164:165], v[164:165], s[70:71], s[70:71] op_sel:[0,0,1] op_sel_hi:[1,0,1]\nv_pk_fma_f32 v[166:167], v[166:167], s[70:71], s[70:71] op_sel:[0,0,1] op_sel_hi:[1,0,1]\nv_pk_fma_f32 v[168:169], v[168:169], s[70:71], s[70:71] op_sel:[0,0,1] op_sel_hi:[1,0,1]\nv_pk_fma_f32 v[170:171], v[170:171], s[70:71], s[70:71] op_sel:[0,0,1] op_sel_hi:[1,0,1]\nv_pk_fma_f32 v[172:173], v[172:173], s[70:71], s[70:71] op_sel:[0,0,1] op_sel_hi:[1,0,1]\nv_pk_fma_f32 v[174:175], v[174:175], s[70:71], s[70:71] op_sel:[0,0,1] op_sel_hi:[1,0,1]\n"
"v_cvt_pk_bf16_f32 v216, v216, v217\n"
"v_mfma_f32_16x16x32_bf16 v[128:131], a[192:195], a[144:147], v[128:131]\n"
"v_cvt_pk_bf16_f32 v168, v168, v169\n"
"v_xor_b32 v250, s74, v250\n"
"v_mfma_f32_16x16x32_bf16 v[128:131], a[196:199], a[148:151], v[128:131]\n"
"buffer_load_dwordx4 v227, s[28:31], s48 offen offset:3072 lds\n"
"v_mfma_f32_16x16x32_bf16 v[144:147], a[192:195], a[152:155], v[144:147]\n"
"s_xor_b32 s50, s50, s74\n"
"v_cvt_pk_bf16_f32 v208, v208, v209\n"
"v_xor_b32 v251, s74, v251\n"
"v_mfma_f32_16x16x32_bf16 v[144:147], a[196:199], a[156:159], v[144:147]\n"
"v_cvt_pk_bf16_f32 v217, v218, v219\n"
"v_cvt_pk_bf16_f32 v169, v170, v171\n"
"v_mfma_f32_16x16x32_bf16 v[148:151], a[200:203], a[152:155], v[148:151]\n"
"v_cvt_pk_bf16_f32 v195, v198, v199\n"
"v_cvt_pk_bf16_f32 v209, v210, v211\n"
"v_mfma_f32_16x16x32_bf16 v[148:151], a[204:207], a[156:159], v[148:151]\n"
"ds_write2st64_b64 v180, v[192:193], v[194:195] offset0:88 offset1:89\n"
"v_pk_fma_f32 v[112:113], v[112:113], s[70:71], s[70:71] op_sel:[0,0,1] op_sel_hi:[1,0,1]\nv_pk_fma_f32 v[114:115], v[114:115], s[70:71], s[70:71] op_sel:[0,0,1] op_sel_hi:[1,0,1]\nv_pk_fma_f32 v[116:117], v[116:117], s[70:71], s[70:71] op_sel:[0,0,1] op_sel_hi:[1,0,1]\nv_pk_fma_f32 v[118:119], v[118:119], s[70:71], s[70:71] op_sel:[0,0,1] op_sel_hi:[1,0,1]\nv_pk_fma_f32 v[120:121], v[120:121], s[70:71], s[70:71] op_sel:[0,0,1] op_sel_hi:[1,0,1]\nv_pk_fma_f32 v[122:123], v[122:123], s[70:71], s[70:71] op_sel:[0,0,1] op_sel_hi:[1,0,1]\nv_pk_fma_f32 v[124:125], v[124:125], s[70:71], s[70:71] op_sel:[0,0,1] op_sel_hi:[1,0,1]\nv_pk_fma_f32 v[126:127], v[126:127], s[70:71], s[70:71] op_sel:[0,0,1] op_sel_hi:[1,0,1]\nv_pk_fma_f32 v[128:129], v[128:129], s[70:71], s[70:71] op_sel:[0,0,1] op_sel_hi:[1,0,1]\nv_pk_fma_f32 v[130:131], v[130:131], s[70:71], s[70:71] op_sel:[0,0,1] op_sel_hi:[1,0,1]\nv_pk_fma_f32 v[132:133], v[132:133], s[70:71], s[70:71] op_sel:[0,0,1] op_sel_hi:[1,0,1]\nv_pk_fma_f32 v[134:135], v[134:135], s[70:71], s[70:71] op_sel:[0,0,1] op_sel_hi:[1,0,1]\nv_pk_fma_f32 v[136:137], v[136:137], s[70:71], s[70:71] op_sel:[0,0,1] op_sel_hi:[1,0,1]\nv_pk_fma_f32 v[138:139], v[138:139], s[70:71], s[70:71] op_sel:[0,0,1] op_sel_hi:[1,0,1]\nv_pk_fma_f32 v[140:141], v[140:141], s[70:71], s[70:71] op_sel:[0,0,1] op_sel_hi:[1,0,1]\nv_pk_fma_f32 v[142:143], v[142:143], s[70:71], s[70:71] op_sel:[0,0,1] op_sel_hi:[1,0,1]\n"
"v_mfma_f32_16x16x32_bf16 v[152:155], a[208:211], a[152:155], v[152:155]\n"
"v_cvt_pk_bf16_f32 v218, v220, v221\n"
"v_cvt_pk_bf16_f32 v136, v136, v137\n"
"v_mfma_f32_16x16x32_bf16 v[152:155], a[212:215], a[156:159], v[152:155]\n"
"v_cvt_pk_bf16_f32 v137, v138, v139\n"
"v_cvt_pk_bf16_f32 v139, v142, v143\n"
"v_mfma_f32_16x16x32_bf16 v[156:159], a[216:219], a[152:155], v[156:159]\n"
"v_cvt_pk_bf16_f32 v128, v128, v129\n"
"v_cvt_pk_bf16_f32 v138, v140, v141\n"
"v_mfma_f32_16x16x32_bf16 v[156:159], a[220:223], a[156:159], v[156:159]\n"
"s_waitcnt lgkmcnt(1)\n"
"v_cvt_pk_bf16_f32 v112, v112, v113\n"
"v_xor_b32 v245, s74, v245\n"
"v_mfma_f32_16x16x32_bf16 v[96:99], a[224:227], a[184:187], v[96:99]\n"
"v_cvt_pk_bf16_f32 v120, v120, v121\n"
"v_cvt_pk_bf16_f32 v129, v130, v131\n"
"v_mfma_f32_16x16x32_bf16 v[96:99], a[228:231], a[188:191], v[96:99]\n"
"ds_write2st64_b64 v180, v[200:201], v[202:203] offset0:96 offset1:97\n"
"v_cvt_pk_bf16_f32 v219, v222, v223\n"
"v_cvt_pk_bf16_f32 v170, v172, v173\n"
"v_cvt_pk_bf16_f32 v131, v134, v135\n"
"v_lshl_add_u32 v181, v181, 8, v182\n"
"v_bfe_u32 v182, v242, 8, 1\n"
"v_lshl_add_u32 v181, v182, 9, v181\n"
"v_mfma_f32_16x16x32_bf16 v[100:103], a[232:235], a[184:187], v[100:103]\n"
"s_waitcnt vmcnt(10)\n"
"v_permlane16_swap_b32 v136, v138\n"
"v_mfma_f32_16x16x32_bf16 v[100:103], a[236:239], a[188:191], v[100:103]\n"
"s_barrier\n"
"ds_write2st64_b64 v180, v[216:217], v[218:219] offset0:128 offset1:129\n"
"v_pk_fma_f32 v[144:145], v[144:145], s[70:71], s[70:71] op_sel:[0,0,1] op_sel_hi:[1,0,1]\nv_pk_fma_f32 v[146:147], v[146:147], s[70:71], s[70:71] op_sel:[0,0,1] op_sel_hi:[1,0,1]\nv_pk_fma_f32 v[148:149], v[148:149], s[70:71], s[70:71] op_sel:[0,0,1] op_sel_hi:[1,0,1]\nv_pk_fma_f32 v[150:151], v[150:151], s[70:71], s[70:71] op_sel:[0,0,1] op_sel_hi:[1,0,1]\nv_pk_fma_f32 v[152:153], v[152:153], s[70:71], s[70:71] op_sel:[0,0,1] op_sel_hi:[1,0,1]\nv_pk_fma_f32 v[154:155], v[154:155], s[70:71], s[70:71] op_sel:[0,0,1] op_sel_hi:[1,0,1]\nv_pk_fma_f32 v[156:157], v[156:157], s[70:71], s[70:71] op_sel:[0,0,1] op_sel_hi:[1,0,1]\nv_pk_fma_f32 v[158:159], v[158:159], s[70:71], s[70:71] op_sel:[0,0,1] op_sel_hi:[1,0,1]\n"
"v_cvt_pk_bf16_f32 v144, v144, v145\n"
"v_mfma_f32_16x16x32_bf16 v[104:107], a[240:243], a[184:187], v[104:107]\n"
"s_mov_b32 m0, s51\n"
"v_cvt_pk_bf16_f32 v171, v174, v175\n"
"v_cvt_pk_bf16_f32 v121, v122, v123\n"
"v_mfma_f32_16x16x32_bf16 v[104:107], a[244:247], a[188:191], v[104:107]\n"
"buffer_load_dwordx4 v248, s[24:27], s49 offen lds\n"
"v_mfma_f32_16x16x32_bf16 v[108:111], a[248:251], a[184:187], v[108:111]\n"
"v_xor_b32_e64 v182, s42, 0\n"
"v_lshl_add_u32 v181, v182, 10, v181\n"
"v_mfma_f32_16x16x32_bf16 v[108:111], a[252:255], a[188:191], v[108:111]\n"
"ds_read_b64_tr_b16 a[218:219], v245 offset:1536\n"
"v_cvt_pk_bf16_f32 v211, v214, v215\n"
"v_mfma_f32_16x16x32_bf16 v[92:95], a[248:251], a[176:179], v[92:95]\n"
"v_cvt_pk_bf16_f32 v160, v160, v161\n"
"v_cvt_pk_bf16_f32 v145, v146, v147\n"
"v_mfma_f32_16x16x32_bf16 v[92:95], a[252:255], a[180:183], v[92:95]\n"
"ds_write2st64_b64 v180, v[176:177], v[178:179] offset0:0 offset1:1\n"
"v_cvt_pk_bf16_f32 v122, v124, v125\n"
"v_cvt_pk_bf16_f32 v123, v126, v127\n"
"v_cvt_pk_bf16_f32 v146, v148, v149\n"
"v_cvt_pk_bf16_f32 v152, v152, v153\n"
"v_cvt_pk_bf16_f32 v153, v154, v155\n"
"v_cvt_pk_bf16_f32 v155, v158, v159\n"
"v_mfma_f32_16x16x32_bf16 v[88:91], a[240:243], a[176:179], v[88:91]\n"
"v_cvt_pk_bf16_f32 v210, v212, v213\n"
"v_cvt_pk_bf16_f32 v113, v114, v115\n"
"v_mfma_f32_16x16x32_bf16 v[88:91], a[244:247], a[180:183], v[88:91]\n"
"buffer_load_dwordx4 v249, s[24:27], s49 offen offset:1024 lds\n"
"v_mfma_f32_16x16x32_bf16 v[84:87], a[232:235], a[176:179], v[84:87]\n"
"v_cvt_pk_bf16_f32 v161, v162, v163\n"
"v_cvt_pk_bf16_f32 v154, v156, v157\n"
"v_mfma_f32_16x16x32_bf16 v[84:87], a[236:239], a[180:183], v[84:87]\n"
"s_nop 0\n"
"v_permlane16_swap_b32 v152, v154\n"
"v_mfma_f32_16x16x32_bf16 v[80:83], a[224:227], a[176:179], v[80:83]\n"
"ds_read_b64_tr_b16 a[196:197], v244 offset:8192\n"
"ds_read_b64_tr_b16 a[214:215], v244 offset:9728\n"
"v_mfma_f32_16x16x32_bf16 v[80:83], a[228:231], a[180:183], v[80:83]\n"
"ds_write2st64_b64 v180, v[208:209], v[210:211] offset0:120 offset1:121\n"
"v_cvt_pk_bf16_f32 v114, v116, v117\n"
"v_permlane16_swap_b32 v137, v139\n"
"v_permlane16_swap_b32 v144, v146\n"
"v_mfma_f32_16x16x32_bf16 v[64:67], a[224:227], a[168:171], v[64:67]\n"
"v_permlane16_swap_b32 v121, v123\n"
"v_mfma_f32_16x16x32_bf16 v[64:67], a[228:231], a[172:175], v[64:67]\n"
"buffer_load_dwordx4 v254, s[24:27], s49 offen offset:2048 lds\n"
"v_mfma_f32_16x16x32_bf16 v[68:71], a[232:235], a[168:171], v[68:71]\n"
"v_permlane16_swap_b32 v120, v122\n"
"v_mfma_f32_16x16x32_bf16 v[68:71], a[236:239], a[172:175], v[68:71]\n"
"v_cvt_pk_bf16_f32 v115, v118, v119\n"
"v_accvgpr_write_b32 a24, v136\n"
"v_mfma_f32_16x16x32_bf16 v[72:75], a[240:243], a[168:171], v[72:75]\n"
"ds_read_b64_tr_b16 a[206:207], v245 offset:8704\n"
"v_add_u32 v181, 94208, v181\n"
"v_mfma_f32_16x16x32_bf16 v[72:75], a[244:247], a[172:175], v[72:75]\n"
"ds_read_b64_tr_b16 a[216:217], v245 offset:1024\n"
"v_permlane16_swap_b32 v112, v114\n"
"v_permlane16_swap_b32 v153, v155\n"
"v_pk_fma_f32 v[96:97], v[96:97], s[70:71], s[70:71] op_sel:[0,0,1] op_sel_hi:[1,0,1]\nv_pk_fma_f32 v[98:99], v[98:99], s[70:71], s[70:71] op_sel:[0,0,1] op_sel_hi:[1,0,1]\nv_pk_fma_f32 v[100:101], v[100:101], s[70:71], s[70:71] op_sel:[0,0,1] op_sel_hi:[1,0,1]\nv_pk_fma_f32 v[102:103], v[102:103], s[70:71], s[70:71] op_sel:[0,0,1] op_sel_hi:[1,0,1]\nv_pk_fma_f32 v[104:105], v[104:105], s[70:71], s[70:71] op_sel:[0,0,1] op_sel_hi:[1,0,1]\nv_pk_fma_f32 v[106:107], v[106:107], s[70:71], s[70:71] op_sel:[0,0,1] op_sel_hi:[1,0,1]\nv_pk_fma_f32 v[108:109], v[108:109], s[70:71], s[70:71] op_sel:[0,0,1] op_sel_hi:[1,0,1]\nv_pk_fma_f32 v[110:111], v[110:111], s[70:71], s[70:71] op_sel:[0,0,1] op_sel_hi:[1,0,1]\nv_pk_fma_f32 v[80:81], v[80:81], s[70:71], s[70:71] op_sel:[0,0,1] op_sel_hi:[1,0,1]\nv_pk_fma_f32 v[82:83], v[82:83], s[70:71], s[70:71] op_sel:[0,0,1] op_sel_hi:[1,0,1]\nv_pk_fma_f32 v[84:85], v[84:85], s[70:71], s[70:71] op_sel:[0,0,1] op_sel_hi:[1,0,1]\nv_pk_fma_f32 v[86:87], v[86:87], s[70:71], s[70:71] op_sel:[0,0,1] op_sel_hi:[1,0,1]\nv_pk_fma_f32 v[88:89], v[88:89], s[70:71], s[70:71] op_sel:[0,0,1] op_sel_hi:[1,0,1]\nv_pk_fma_f32 v[90:91], v[90:91], s[70:71], s[70:71] op_sel:[0,0,1] op_sel_hi:[1,0,1]\nv_pk_fma_f32 v[92:93], v[92:93], s[70:71], s[70:71] op_sel:[0,0,1] op_sel_hi:[1,0,1]\nv_pk_fma_f32 v[94:95], v[94:95], s[70:71], s[70:71] op_sel:[0,0,1] op_sel_hi:[1,0,1]\n"
"v_mfma_f32_16x16x32_bf16 v[76:79], a[248:251], a[168:171], v[76:79]\n"
"ds_read_b64_tr_b16 a[208:209], v244 offset:1024\n"
"v_cvt_pk_bf16_f32 v80, v80, v81\n"
"v_mfma_f32_16x16x32_bf16 v[76:79], a[252:255], a[172:175], v[76:79]\n"
"buffer_load_dwordx4 v255, s[24:27], s49 offen offset:3072 lds\n"
"v_mfma_f32_16x16x32_bf16 v[60:63], a[248:251], a[160:163], v[60:63]\n"
"ds_read_b64_tr_b16 a[192:193], v244 offset:0\n"
"ds_read_b64_tr_b16 a[222:223], v245 offset:9728\n"
"v_mfma_f32_16x16x32_bf16 v[60:63], a[252:255], a[164:167], v[60:63]\n"
"v_permlane16_swap_b32 v168, v170\n"
"v_mfma_f32_16x16x32_bf16 v[56:59], a[240:243], a[160:163], v[56:59]\n"
"v_permlane16_swap_b32 v113, v115\n"
"v_mfma_f32_16x16x32_bf16 v[56:59], a[244:247], a[164:167], v[56:59]\n"
"s_add_u32 s40, s49, 40960\n"
"ds_read_b64_tr_b16 a[210:211], v244 offset:1536\n"
"v_add_u32 v182, s75, v181\n"
"v_mfma_f32_16x16x32_bf16 v[52:55], a[232:235], a[160:163], v[52:55]\n"
"s_add_u32 m0, s51, 4096\n"
"v_cvt_pk_bf16_f32 v130, v132, v133\n"
"v_accvgpr_write_b32 a25, v137\n"
"v_mfma_f32_16x16x32_bf16 v[52:55], a[236:239], a[164:167], v[52:55]\n"
"buffer_load_dwordx4 v248, s[24:27], s40 offen lds\n"
"v_mfma_f32_16x16x32_bf16 v[48:51], a[224:227], a[160:163], v[48:51]\n"
"s_waitcnt vmcnt(63) lgkmcnt(3)\n"
"v_permlane16_swap_b32 v129, v131\n"
"v_mfma_f32_16x16x32_bf16 v[48:51], a[228:231], a[164:167], v[48:51]\n"
"s_barrier\n"
"v_mfma_f32_16x16x32_bf16 v[0:3], a[224:227], a[136:139], v[0:3]\n"
"ds_read_b128 a[36:39], v181 offset:61440\n"
"v_accvgpr_write_b32 a43, v155\n"
"v_mfma_f32_16x16x32_bf16 v[0:3], a[228:231], a[140:143], v[0:3]\n"
"ds_read_b128 a[28:31], v181 offset:49152\n"
"v_accvgpr_write_b32 a10, v122\n"
"v_mfma_f32_16x16x32_bf16 v[4:7], a[232:235], a[136:139], v[4:7]\n"
"v_cvt_pk_bf16_f32 v96, v96, v97\n"
"v_cvt_pk_bf16_f32 v104, v104, v105\n"
"v_mfma_f32_16x16x32_bf16 v[4:7], a[236:239], a[140:143], v[4:7]\n"
"buffer_load_dwordx4 v249, s[24:27], s40 offen offset:1024 lds\n"
"v_mfma_f32_16x16x32_bf16 v[8:11], a[240:243], a[136:139], v[8:11]\n"
"v_cvt_pk_bf16_f32 v97, v98, v99\n"
"v_cvt_pk_bf16_f32 v99, v102, v103\n"
"v_mfma_f32_16x16x32_bf16 v[8:11], a[244:247], a[140:143], v[8:11]\n"
"ds_read_b64_tr_b16 a[220:221], v245 offset:9216\n"
"ds_read_b128 a[12:15], v181 offset:32768\n"
"v_mfma_f32_16x16x32_bf16 v[12:15], a[248:251], a[136:139], v[12:15]\n"
"ds_read_b128 a[164:167], v253 offset:0\n"
"v_accvgpr_write_b32 a32, v144\n"
"v_mfma_f32_16x16x32_bf16 v[12:15], a[252:255], a[140:143], v[12:15]\n"
"ds_read_b128 a[140:143], v247 offset:0\n"
"v_accvgpr_write_b32 a3, v115\n"
"v_mfma_f32_16x16x32_bf16 v[28:31], a[248:251], a[144:147], v[28:31]\n"
"ds_read_b64_tr_b16 a[194:195], v244 offset:512\n"
"v_cvt_pk_bf16_f32 v162, v164, v165\n"
"v_mfma_f32_16x16x32_bf16 v[28:31], a[252:255], a[148:151], v[28:31]\n"
"buffer_load_dwordx4 v254, s[24:27], s40 offen offset:2048 lds\n"
"v_mfma_f32_16x16x32_bf16 v[24:27], a[240:243], a[144:147], v[24:27]\n"
"s_add_u32 s49, s49, 128\n"
"v_cvt_pk_bf16_f32 v147, v150, v151\n"
"v_accvgpr_write_b32 a41, v153\n"
"v_mfma_f32_16x16x32_bf16 v[24:27], a[244:247], a[148:151], v[24:27]\n"
"s_sub_u32 s41, s49, 1280\n"
"v_cvt_pk_bf16_f32 v98, v100, v101\n"
"v_cvt_pk_bf16_f32 v88, v88, v89\n"
"v_mfma_f32_16x16x32_bf16 v[20:23], a[232:235], a[144:147], v[20:23]\n"
"s_min_u32 s49, s49, s41\n"
"ds_read_b128 a[136:139], v246 offset:0\n"
"v_accvgpr_write_b32 a19, v131\n"
"v_mfma_f32_16x16x32_bf16 v[20:23], a[236:239], a[148:151], v[20:23]\n"
"s_xor_b32 s51, s51, s74\n"
"ds_read_b64_tr_b16 a[198:199], v244 offset:8704\n"
"ds_read_b128 a[44:47], v182 offset:0\n"
"v_mfma_f32_16x16x32_bf16 v[16:19], a[224:227], a[144:147], v[16:19]\n"
"ds_read_b64_tr_b16 a[212:213], v244 offset:9216\n"
"v_cvt_pk_bf16_f32 v163, v166, v167\n"
"v_mfma_f32_16x16x32_bf16 v[16:19], a[228:231], a[148:151], v[16:19]\n"
"ds_read_b128 a[148:151], v247 offset:2048\n"
"v_accvgpr_write_b32 a11, v123\n"
"v_mfma_f32_16x16x32_bf16 v[32:35], a[224:227], a[152:155], v[32:35]\n"
"ds_read_b128 a[4:7], v181 offset:0\n"
"v_and_b32 v102, 0xf0, v242\n"
"v_mfma_f32_16x16x32_bf16 v[32:35], a[228:231], a[156:159], v[32:35]\n"
"ds_read_b64_tr_b16 a[202:203], v245 offset:512\n"
"ds_read_b64_tr_b16 a[204:205], v245 offset:8192\n"
"v_mfma_f32_16x16x32_bf16 v[36:39], a[232:235], a[152:155], v[36:39]\n"
"ds_read_b64_tr_b16 a[200:201], v245 offset:0\n"
"v_cvt_pk_bf16_f32 v89, v90, v91\n"
"v_mfma_f32_16x16x32_bf16 v[36:39], a[236:239], a[156:159], v[36:39]\n"
"ds_read_b128 a[20:23], v181 offset:45056\n"
"v_accvgpr_write_b32 a27, v139\n"
"v_mfma_f32_16x16x32_bf16 v[40:43], a[240:243], a[152:155], v[40:43]\n"
"v_accvgpr_write_b32 a0, v112\n"
"v_accvgpr_write_b32 a1, v113\n"
"v_mfma_f32_16x16x32_bf16 v[40:43], a[244:247], a[156:159], v[40:43]\n"
"ds_read_b128 a[180:183], v251 offset:2048\n"
"v_accvgpr_write_b32 a2, v114\n"
"v_mfma_f32_16x16x32_bf16 v[44:47], a[248:251], a[152:155], v[44:47]\n"
"s_add_u32 s47, s47, 1\n"
"ds_read_b128 a[144:147], v246 offset:2048\n"
"v_accvgpr_write_b32 a17, v129\n"
"v_mfma_f32_16x16x32_bf16 v[44:47], a[252:255], a[156:159], v[44:47]\n"
"s_waitcnt lgkmcnt(7)\n"
"ds_read_b128 a[160:163], v252 offset:0\n"
"v_permlane16_swap_b32 v161, v163\n"
"v_permlane16_swap_b32 v169, v171\n"
"v_accvgpr_write_b32 a9, v121\n"
"v_permlane16_swap_b32 v128, v130\n"
"v_permlane16_swap_b32 v145, v147\n"
"v_accvgpr_write_b32 a42, v154\n"
"v_bfe_u32 v100, v242, 9, 1\n"
"v_cvt_pk_bf16_f32 v91, v94, v95\n"
"v_pk_fma_f32 v[0:1], v[0:1], s[70:71], s[70:71] op_sel:[0,0,1] op_sel_hi:[1,0,1]\nv_pk_fma_f32 v[2:3], v[2:3], s[70:71], s[70:71] op_sel:[0,0,1] op_sel_hi:[1,0,1]\nv_pk_fma_f32 v[4:5], v[4:5], s[70:71], s[70:71] op_sel:[0,0,1] op_sel_hi:[1,0,1]\nv_pk_fma_f32 v[6:7], v[6:7], s[70:71], s[70:71] op_sel:[0,0,1] op_sel_hi:[1,0,1]\nv_pk_fma_f32 v[8:9], v[8:9], s[70:71], s[70:71] op_sel:[0,0,1] op_sel_hi:[1,0,1]\nv_pk_fma_f32 v[10:11], v[10:11], s[70:71], s[70:71] op_sel:[0,0,1] op_sel_hi:[1,0,1]\nv_pk_fma_f32 v[12:13], v[12:13], s[70:71], s[70:71] op_sel:[0,0,1] op_sel_hi:[1,0,1]\nv_pk_fma_f32 v[14:15], v[14:15], s[70:71], s[70:71] op_sel:[0,0,1] op_sel_hi:[1,0,1]\nv_pk_fma_f32 v[16:17], v[16:17], s[70:71], s[70:71] op_sel:[0,0,1] op_sel_hi:[1,0,1]\nv_pk_fma_f32 v[18:19], v[18:19], s[70:71], s[70:71] op_sel:[0,0,1] op_sel_hi:[1,0,1]\nv_pk_fma_f32 v[20:21], v[20:21], s[70:71], s[70:71] op_sel:[0,0,1] op_sel_hi:[1,0,1]\nv_pk_fma_f32 v[22:23], v[22:23], s[70:71], s[70:71] op_sel:[0,0,1] op_sel_hi:[1,0,1]\nv_pk_fma_f32 v[24:25], v[24:25], s[70:71], s[70:71] op_sel:[0,0,1] op_sel_hi:[1,0,1]\nv_pk_fma_f32 v[26:27], v[26:27], s[70:71], s[70:71] op_sel:[0,0,1] op_sel_hi:[1,0,1]\nv_pk_fma_f32 v[28:29], v[28:29], s[70:71], s[70:71] op_sel:[0,0,1] op_sel_hi:[1,0,1]\nv_pk_fma_f32 v[30:31], v[30:31], s[70:71], s[70:71] op_sel:[0,0,1] op_sel_hi:[1,0,1]\n"
"v_cvt_pk_bf16_f32 v16, v16, v17\n"
"v_cvt_pk_bf16_f32 v24, v24, v25\n"
"v_cvt_pk_bf16_f32 v25, v26, v27\n"
"v_mfma_f32_16x16x32_bf16 v[112:115], a[192:195], a[136:139], v[228:231]\n"
"v_cvt_pk_bf16_f32 v0, v0, v1\n"
"v_cvt_pk_bf16_f32 v8, v8, v9\n"
"v_mfma_f32_16x16x32_bf16 v[112:115], a[196:199], a[140:143], v[112:115]\n"
"s_waitcnt lgkmcnt(4)\n"
"ds_write_b128 v243, v[168:171] offset:2048\n"
"v_permlane16_swap_b32 v160, v162\n"
"v_accvgpr_write_b32 a8, v120\n"
"v_cvt_pk_bf16_f32 v81, v82, v83\n"
"v_pk_fma_f32 v[64:65], v[64:65], s[70:71], s[70:71] op_sel:[0,0,1] op_sel_hi:[1,0,1]\nv_pk_fma_f32 v[66:67], v[66:67], s[70:71], s[70:71] op_sel:[0,0,1] op_sel_hi:[1,0,1]\nv_pk_fma_f32 v[68:69], v[68:69], s[70:71], s[70:71] op_sel:[0,0,1] op_sel_hi:[1,0,1]\nv_pk_fma_f32 v[70:71], v[70:71], s[70:71], s[70:71] op_sel:[0,0,1] op_sel_hi:[1,0,1]\nv_pk_fma_f32 v[72:73], v[72:73], s[70:71], s[70:71] op_sel:[0,0,1] op_sel_hi:[1,0,1]\nv_pk_fma_f32 v[74:75], v[74:75], s[70:71], s[70:71] op_sel:[0,0,1] op_sel_hi:[1,0,1]\nv_pk_fma_f32 v[76:77], v[76:77], s[70:71], s[70:71] op_sel:[0,0,1] op_sel_hi:[1,0,1]\nv_pk_fma_f32 v[78:79], v[78:79], s[70:71], s[70:71] op_sel:[0,0,1] op_sel_hi:[1,0,1]\nv_pk_fma_f32 v[48:49], v[48:49], s[70:71], s[70:71] op_sel:[0,0,1] op_sel_hi:[1,0,1]\nv_pk_fma_f32 v[50:51], v[50:51], s[70:71], s[70:71] op_sel:[0,0,1] op_sel_hi:[1,0,1]\nv_pk_fma_f32 v[52:53], v[52:53], s[70:71], s[70:71] op_sel:[0,0,1] op_sel_hi:[1,0,1]\nv_pk_fma_f32 v[54:55], v[54:55], s[70:71], s[70:71] op_sel:[0,0,1] op_sel_hi:[1,0,1]\nv_pk_fma_f32 v[56:57], v[56:57], s[70:71], s[70:71] op_sel:[0,0,1] op_sel_hi:[1,0,1]\nv_pk_fma_f32 v[58:59], v[58:59], s[70:71], s[70:71] op_sel:[0,0,1] op_sel_hi:[1,0,1]\nv_pk_fma_f32 v[60:61], v[60:61], s[70:71], s[70:71] op_sel:[0,0,1] op_sel_hi:[1,0,1]\nv_pk_fma_f32 v[62:63], v[62:63], s[70:71], s[70:71] op_sel:[0,0,1] op_sel_hi:[1,0,1]\n"
"v_cvt_pk_bf16_f32 v64, v64, v65\n"
"v_mfma_f32_16x16x32_bf16 v[116:119], a[200:203], a[136:139], v[228:231]\n"
"s_waitcnt vmcnt(11) lgkmcnt(4)\n"
"v_accvgpr_write_b32 a18, v130\n"
"v_accvgpr_write_b32 a26, v138\n"
"v_mfma_f32_16x16x32_bf16 v[116:119], a[204:207], a[140:143], v[116:119]\n"
"s_barrier\n"
"ds_write_b128 v243, v[160:163] offset:0\n"
"v_accvgpr_write_b32 a16, v128\n"
"v_accvgpr_write_b32 a33, v145\n"
"v_cvt_pk_bf16_f32 v1, v2, v3\n"
"v_pk_fma_f32 v[32:33], v[32:33], s[70:71], s[70:71] op_sel:[0,0,1] op_sel_hi:[1,0,1]\nv_pk_fma_f32 v[34:35], v[34:35], s[70:71], s[70:71] op_sel:[0,0,1] op_sel_hi:[1,0,1]\nv_pk_fma_f32 v[36:37], v[36:37], s[70:71], s[70:71] op_sel:[0,0,1] op_sel_hi:[1,0,1]\nv_pk_fma_f32 v[38:39], v[38:39], s[70:71], s[70:71] op_sel:[0,0,1] op_sel_hi:[1,0,1]\nv_pk_fma_f32 v[40:41], v[40:41], s[70:71], s[70:71] op_sel:[0,0,1] op_sel_hi:[1,0,1]\nv_pk_fma_f32 v[42:43], v[42:43], s[70:71], s[70:71] op_sel:[0,0,1] op_sel_hi:[1,0,1]\nv_pk_fma_f32 v[44:45], v[44:45], s[70:71], s[70:71] op_sel:[0,0,1] op_sel_hi:[1,0,1]\nv_pk_fma_f32 v[46:47], v[46:47], s[70:71], s[70:71] op_sel:[0,0,1] op_sel_hi:[1,0,1]\n"
"v_cvt_pk_bf16_f32 v32, v32, v33\n"
"v_mfma_f32_16x16x32_bf16 v[120:123], a[208:211], a[136:139], v[228:231]\n"
"s_add_u32 s40, s48, 256\n"
"s_xor_b32 m0, s50, 0xc000\n"
"ds_read_b128 a[152:155], v246 offset:4096\n"
"v_mfma_f32_16x16x32_bf16 v[120:123], a[212:215], a[140:143], v[120:123]\n"
"buffer_load_dwordx4 v224, s[28:31], s40 offen lds\n"
"v_mfma_f32_16x16x32_bf16 v[124:127], a[216:219], a[136:139], v[228:231]\n"
"v_accvgpr_write_b32 a40, v152\n"
"v_cvt_pk_bf16_f32 v40, v40, v41\n"
"v_mfma_f32_16x16x32_bf16 v[124:127], a[220:223], a[140:143], v[124:127]\n"
"s_waitcnt lgkmcnt(3)\n"
"ds_read_b128 a[168:171], v250 offset:0\n"
"v_cvt_pk_bf16_f32 v105, v106, v107\n"
"v_mfma_f32_16x16x32_bf16 v[140:143], a[216:219], a[144:147], v[228:231]\n"
"ds_read_b128 a[156:159], v247 offset:4096\n"
"v_accvgpr_write_b32 a34, v146\n"
"v_mfma_f32_16x16x32_bf16 v[140:143], a[220:223], a[148:151], v[140:143]\n"
"ds_read_b128 a[172:175], v251 offset:0\n"
"v_cvt_pk_bf16_f32 v2, v4, v5\n"
"v_mfma_f32_16x16x32_bf16 v[136:139], a[208:211], a[144:147], v[228:231]\n"
"ds_read_b64_tr_b16 a[224:225], v244 offset:16384\n"
"v_lshl_add_u32 v100, v100, 8, v102\n"
"v_mfma_f32_16x16x32_bf16 v[136:139], a[212:215], a[148:151], v[136:139]\n"
"buffer_load_dwordx4 v225, s[28:31], s40 offen offset:1024 lds\n"
"v_mfma_f32_16x16x32_bf16 v[132:135], a[200:203], a[144:147], v[228:231]\n"
"v_bfe_u32 v102, v242, 8, 1\n"
"v_cvt_pk_bf16_f32 v41, v42, v43\n"
"v_mfma_f32_16x16x32_bf16 v[132:135], a[204:207], a[148:151], v[132:135]\n"
"v_accvgpr_write_b32 a35, v147\n"
"v_cvt_pk_bf16_f32 v65, v66, v67\n"
"v_mfma_f32_16x16x32_bf16 v[128:131], a[192:195], a[144:147], v[228:231]\n"
"ds_read_b64_tr_b16 a[234:235], v245 offset:16896\n"
"v_cvt_pk_bf16_f32 v72, v72, v73\n"
"v_mfma_f32_16x16x32_bf16 v[128:131], a[196:199], a[148:151], v[128:131]\n"
"s_waitcnt lgkmcnt(4)\n"
"ds_read_b64_tr_b16 a[236:237], v245 offset:24576\n"
"v_bfe_u32 v101, v242, 9, 1\n"
"v_mfma_f32_16x16x32_bf16 v[144:147], a[192:195], a[152:155], v[228:231]\n"
"s_waitcnt lgkmcnt(4)\n"
"ds_read_b64_tr_b16 a[226:227], v244 offset:16896\n"
"v_cvt_pk_bf16_f32 v9, v10, v11\n"
"v_mfma_f32_16x16x32_bf16 v[144:147], a[196:199], a[156:159], v[144:147]\n"
"buffer_load_dwordx4 v226, s[28:31], s40 offen offset:2048 lds\n"
"v_mfma_f32_16x16x32_bf16 v[148:151], a[200:203], a[152:155], v[228:231]\n"
"v_cvt_pk_bf16_f32 v3, v6, v7\n"
"v_cvt_pk_bf16_f32 v17, v18, v19\n"
"v_mfma_f32_16x16x32_bf16 v[148:151], a[204:207], a[156:159], v[148:151]\n"
"v_cvt_pk_bf16_f32 v66, v68, v69\n"
"v_cvt_pk_bf16_f32 v73, v74, v75\n"
"v_mfma_f32_16x16x32_bf16 v[152:155], a[208:211], a[152:155], v[228:231]\n"
"ds_read_b64_tr_b16 a[252:253], v245 offset:25600\n"
"v_lshl_add_u32 v100, v102, 3, v100\n"
"v_mfma_f32_16x16x32_bf16 v[152:155], a[212:215], a[156:159], v[152:155]\n"
"ds_read_b64_tr_b16 a[244:245], v244 offset:25600\n"
"ds_read_b64_tr_b16 a[250:251], v245 offset:17920\n"
"v_mfma_f32_16x16x32_bf16 v[156:159], a[216:219], a[152:155], v[228:231]\n"
"v_cvt_pk_bf16_f32 v48, v48, v49\n"
"v_cvt_pk_bf16_f32 v43, v46, v47\n"
"v_mfma_f32_16x16x32_bf16 v[156:159], a[220:223], a[156:159], v[156:159]\n"
"buffer_load_dwordx4 v227, s[28:31], s40 offen offset:3072 lds\n"
"v_mfma_f32_16x16x32_bf16 v[172:175], a[216:219], a[160:163], v[228:231]\n"
"s_cmp_eq_u32 s47, 8\n"
"v_cvt_pk_bf16_f32 v75, v78, v79\n"
"v_cvt_pk_bf16_f32 v27, v30, v31\n"
"v_mfma_f32_16x16x32_bf16 v[172:175], a[220:223], a[164:167], v[172:175]\n"
"s_cselect_b32 s41, s52, s58\n"
"v_cvt_pk_bf16_f32 v56, v56, v57\n"
"v_permlane16_swap_b32 v1, v3\n"
"v_cvt_pk_bf16_f32 v11, v14, v15\n"
"v_cvt_pk_bf16_f32 v26, v28, v29\n"
"v_cvt_pk_bf16_f32 v33, v34, v35\n"
"v_mfma_f32_16x16x32_bf16 v[168:171], a[208:211], a[160:163], v[228:231]\n"
"s_add_u32 s48, s48, s41\n"
"ds_read_b128 a[176:179], v250 offset:2048\n"
"v_cvt_pk_bf16_f32 v83, v86, v87\n"
"v_mfma_f32_16x16x32_bf16 v[168:171], a[212:215], a[164:167], v[168:171]\n"
"s_sub_u32 s41, s48, 5242880\n"
"ds_read_b64_tr_b16 a[230:231], v244 offset:25088\n"
"ds_read_b64_tr_b16 a[248:249], v245 offset:17408\n"
"v_mfma_f32_16x16x32_bf16 v[164:167], a[200:203], a[160:163], v[228:231]\n"
"s_min_u32 s48, s48, s41\n"
"ds_read_b64_tr_b16 a[232:233], v245 offset:16384\n"
"v_xor_b32_e64 v102, s42, 1\n"
"v_mfma_f32_16x16x32_bf16 v[164:167], a[204:207], a[164:167], v[164:167]\n"
"ds_read_b128 a[184:187], v250 offset:4096\n"
"v_lshl_add_u32 v100, v102, 10, v100\n"
"v_mfma_f32_16x16x32_bf16 v[160:163], a[192:195], a[160:163], v[228:231]\n"
"s_mov_b32 m0, s50\n"
"v_permlane16_swap_b32 v9, v11\n"
"v_mfma_f32_16x16x32_bf16 v[160:163], a[196:199], a[164:167], v[160:163]\n"
"buffer_load_dwordx4 v224, s[28:31], s48 offen lds\n"
"v_mfma_f32_16x16x32_bf16 v[176:179], a[192:195], a[168:171], v[228:231]\n"
"s_waitcnt lgkmcnt(5)\n"
"v_cvt_pk_bf16_f32 v49, v50, v51\n"
"v_cvt_pk_bf16_f32 v34, v36, v37\n"
"v_mfma_f32_16x16x32_bf16 v[176:179], a[196:199], a[172:175], v[176:179]\n"
"v_cvt_pk_bf16_f32 v10, v12, v13\n"
"v_and_b32 v102, 0xf0, v242\n"
"v_mfma_f32_16x16x32_bf16 v[180:183], a[200:203], a[168:171], v[228:231]\n"
"ds_read_b64_tr_b16 a[240:241], v244 offset:17408\n"
"ds_read_b64_tr_b16 a[254:255], v245 offset:26112\n"
"v_mfma_f32_16x16x32_bf16 v[180:183], a[204:207], a[172:175], v[180:183]\n"
"buffer_load_dwordx4 v225, s[28:31], s48 offen offset:1024 lds\n"
"v_mfma_f32_16x16x32_bf16 v[184:187], a[208:211], a[168:171], v[228:231]\n"
"v_cvt_pk_bf16_f32 v90, v92, v93\n"
"v_cvt_pk_bf16_f32 v42, v44, v45\n"
"v_mfma_f32_16x16x32_bf16 v[184:187], a[212:215], a[172:175], v[184:187]\n"
"v_permlane16_swap_b32 v0, v2\n"
"v_mfma_f32_16x16x32_bf16 v[188:191], a[216:219], a[168:171], v[228:231]\n"
"ds_read_b128 a[188:191], v251 offset:4096\n"
"v_accvgpr_write_b32 a51, v3\n"
"v_mfma_f32_16x16x32_bf16 v[188:191], a[220:223], a[172:175], v[188:191]\n"
"s_waitcnt lgkmcnt(3)\n"
"ds_read_b64_tr_b16 a[238:239], v245 offset:25088\n"
"v_cvt_pk_bf16_f32 v67, v70, v71\n"
"v_mfma_f32_16x16x32_bf16 v[204:207], a[216:219], a[176:179], v[228:231]\n"
"ds_read_b64_tr_b16 a[246:247], v244 offset:26112\n"
"v_add_u32 v100, 94208, v100\n"
"v_mfma_f32_16x16x32_bf16 v[204:207], a[220:223], a[180:183], v[204:207]\n"
"buffer_load_dwordx4 v226, s[28:31], s48 offen offset:2048 lds\n"
"v_mfma_f32_16x16x32_bf16 v[200:203], a[208:211], a[176:179], v[228:231]\n"
"v_accvgpr_write_b32 a50, v2\n"
"v_lshl_add_u32 v101, v101, 8, v102\n"
"v_mfma_f32_16x16x32_bf16 v[200:203], a[212:215], a[180:183], v[200:203]\n"
"v_permlane16_swap_b32 v24, v26\n"
"v_mfma_f32_16x16x32_bf16 v[196:199], a[200:203], a[176:179], v[228:231]\n"
"ds_read_b64_tr_b16 a[228:229], v244 offset:24576\n"
"v_cvt_pk_bf16_f32 v106, v108, v109\n"
"v_mfma_f32_16x16x32_bf16 v[196:199], a[204:207], a[180:183], v[196:199]\n"
"ds_write2st64_b64 v100, v[96:97], v[98:99] offset0:120 offset1:121\n"
"v_accvgpr_write_b32 a49, v1\n"
"v_cvt_pk_bf16_f32 v19, v22, v23\n"
"v_permlane16_swap_b32 v25, v27\n"
"v_cvt_pk_bf16_f32 v35, v38, v39\n"
"v_mfma_f32_16x16x32_bf16 v[192:195], a[192:195], a[176:179], v[228:231]\n"
"v_cvt_pk_bf16_f32 v74, v76, v77\n"
"v_cvt_pk_bf16_f32 v18, v20, v21\n"
"v_mfma_f32_16x16x32_bf16 v[192:195], a[196:199], a[180:183], v[192:195]\n"
"buffer_load_dwordx4 v227, s[28:31], s48 offen offset:3072 lds\n"
"v_mfma_f32_16x16x32_bf16 v[208:211], a[192:195], a[184:187], v[228:231]\n"
"s_waitcnt lgkmcnt(2)\n"
"s_xor_b32 s50, s50, s74\n"
"v_permlane16_swap_b32 v33, v35\n"
"v_mfma_f32_16x16x32_bf16 v[208:211], a[196:199], a[188:191], v[208:211]\n"
"v_permlane16_swap_b32 v40, v42\n"
"v_mfma_f32_16x16x32_bf16 v[212:215], a[200:203], a[184:187], v[228:231]\n"
"v_permlane16_swap_b32 v17, v19\n"
"v_mfma_f32_16x16x32_bf16 v[212:215], a[204:207], a[188:191], v[212:215]\n"
"ds_read_b64_tr_b16 a[242:243], v244 offset:17920\n"
"v_cvt_pk_bf16_f32 v107, v110, v111\n"
"v_mfma_f32_16x16x32_bf16 v[216:219], a[208:211], a[184:187], v[228:231]\n"
"v_permlane16_swap_b32 v16, v18\n"
"v_mfma_f32_16x16x32_bf16 v[216:219], a[212:215], a[188:191], v[216:219]\n"
"ds_write2st64_b64 v100, v[104:105], v[106:107] offset0:128 offset1:129\n"
"v_cvt_pk_bf16_f32 v82, v84, v85\n"
"v_accvgpr_write_b32 a75, v27\n"
"v_permlane16_swap_b32 v41, v43\n"
"v_mfma_f32_16x16x32_bf16 v[220:223], a[216:219], a[184:187], v[228:231]\n"
"v_accvgpr_write_b32 a48, v0\n"
"v_accvgpr_write_b32 a64, v16\n"
"v_mfma_f32_16x16x32_bf16 v[220:223], a[220:223], a[188:191], v[220:223]\n"
"ds_write2st64_b64 v100, v[80:81], v[82:83] offset0:88 offset1:89\n"
"v_cvt_pk_bf16_f32 v57, v58, v59\n"
"v_cvt_pk_bf16_f32 v58, v60, v61\n"
"v_permlane16_swap_b32 v8, v10\n"
"v_accvgpr_write_b32 a57, v9\n"
"v_mfma_f32_16x16x32_bf16 v[0:3], a[224:227], a[136:139], v[228:231]\n"
"s_waitcnt lgkmcnt(3)\n"
"v_cvt_pk_bf16_f32 v50, v52, v53\n"
"v_xor_b32 v245, s74, v245\n"
"v_mfma_f32_16x16x32_bf16 v[0:3], a[228:231], a[140:143], v[0:3]\n"
"ds_write2st64_b64 v100, v[72:73], v[74:75] offset0:64 offset1:65\n"
"v_cvt_pk_bf16_f32 v59, v62, v63\n"
"v_accvgpr_write_b32 a58, v10\n"
"v_accvgpr_write_b32 a66, v18\n"
"v_accvgpr_write_b32 a81, v33\n"
"v_mfma_f32_16x16x32_bf16 v[4:7], a[232:235], a[136:139], v[228:231]\n"
"s_waitcnt vmcnt(8) lgkmcnt(3)\n"
"v_permlane16_swap_b32 v57, v59\n"
"v_mfma_f32_16x16x32_bf16 v[4:7], a[236:239], a[140:143], v[4:7]\n"
"s_barrier\n"
"ds_write2st64_b64 v100, v[88:89], v[90:91] offset0:96 offset1:97\n"
"v_accvgpr_write_b32 a56, v8\n"
"v_accvgpr_write_b32 a59, v11\n"
"v_accvgpr_write_b32 a72, v24\n"
"v_mfma_f32_16x16x32_bf16 v[8:11], a[240:243], a[136:139], v[228:231]\n"
"s_mov_b32 m0, s51\n"
"ds_read_b64_tr_b16 a[202:203], v245 offset:512\n"
"v_accvgpr_write_b32 a73, v25\n"
"v_mfma_f32_16x16x32_bf16 v[8:11], a[244:247], a[140:143], v[8:11]\n"
"buffer_load_dwordx4 v248, s[24:27], s49 offen lds\n"
"v_mfma_f32_16x16x32_bf16 v[12:15], a[248:251], a[136:139], v[228:231]\n"
"v_accvgpr_write_b32 a65, v17\n"
"v_accvgpr_write_b32 a67, v19\n"
"v_mfma_f32_16x16x32_bf16 v[12:15], a[252:255], a[140:143], v[12:15]\n"
"v_permlane16_swap_b32 v56, v58\n"
"v_mfma_f32_16x16x32_bf16 v[28:31], a[248:251], a[144:147], v[228:231]\n"
"v_accvgpr_write_b32 a74, v26\n"
"v_accvgpr_write_b32 a83, v35\n"
"v_mfma_f32_16x16x32_bf16 v[28:31], a[252:255], a[148:151], v[28:31]\n"
"ds_write2st64_b64 v100, v[64:65], v[66:67] offset0:0 offset1:1\n"
"v_permlane16_swap_b32 v32, v34\n"
"v_accvgpr_write_b32 a88, v40\n"
"v_mfma_f32_16x16x32_bf16 v[24:27], a[240:243], a[144:147], v[228:231]\n"
"v_accvgpr_write_b32 a89, v41\n"
"v_accvgpr_write_b32 a90, v42\n"
"v_mfma_f32_16x16x32_bf16 v[24:27], a[244:247], a[148:151], v[24:27]\n"
"buffer_load_dwordx4 v249, s[24:27], s49 offen offset:1024 lds\n"
"v_mfma_f32_16x16x32_bf16 v[20:23], a[232:235], a[144:147], v[228:231]\n"
"v_accvgpr_write_b32 a82, v34\n"
"v_accvgpr_write_b32 a91, v43\n"
"v_mfma_f32_16x16x32_bf16 v[20:23], a[236:239], a[148:151], v[20:23]\n"
"v_cvt_pk_bf16_f32 v51, v54, v55\n"
"v_accvgpr_write_b32 a80, v32\n"
"v_mfma_f32_16x16x32_bf16 v[16:19], a[224:227], a[144:147], v[228:231]\n"
"ds_read_b64_tr_b16 a[216:217], v245 offset:1024\n"
"v_xor_b32 v247, s74, v247\n"
"v_mfma_f32_16x16x32_bf16 v[16:19], a[228:231], a[148:151], v[16:19]\n"
"ds_write_b128 v243, v[56:59] offset:6144\n"
"v_mfma_f32_16x16x32_bf16 v[32:35], a[224:227], a[152:155], v[228:231]\n"
"v_permlane16_swap_b32 v48, v50\n"
"v_mfma_f32_16x16x32_bf16 v[32:35], a[228:231], a[156:159], v[32:35]\n"
"buffer_load_dwordx4 v254, s[24:27], s49 offen offset:2048 lds\n"
"v_mfma_f32_16x16x32_bf16 v[36:39], a[232:235], a[152:155], v[228:231]\n"
"s_waitcnt vmcnt(63) lgkmcnt(2)\n"
"v_permlane16_swap_b32 v49, v51\n"
"v_mfma_f32_16x16x32_bf16 v[36:39], a[236:239], a[156:159], v[36:39]\n"
"s_barrier\n"
"v_mfma_f32_16x16x32_bf16 v[40:43], a[240:243], a[152:155], v[228:231]\n"
"ds_read_b64_tr_b16 a[204:205], v245 offset:8192\n"
"ds_read_b128 a[140:143], v247 offset:0\n"
"v_mfma_f32_16x16x32_bf16 v[40:43], a[244:247], a[156:159], v[40:43]\n"
"ds_write_b128 v243, v[48:51] offset:4096\n"
"v_mfma_f32_16x16x32_bf16 v[44:47], a[248:251], a[152:155], v[228:231]\n"
"v_bfe_u32 v102, v242, 8, 1\n"
"v_lshl_add_u32 v101, v102, 9, v101\n"
"v_mfma_f32_16x16x32_bf16 v[44:47], a[252:255], a[156:159], v[44:47]\n"
"buffer_load_dwordx4 v255, s[24:27], s49 offen offset:3072 lds\n"
"v_mfma_f32_16x16x32_bf16 v[60:63], a[248:251], a[160:163], v[228:231]\n"
"v_xor_b32 v244, s74, v244\n"
"v_xor_b32_e64 v102, s42, 0\n"
"v_mfma_f32_16x16x32_bf16 v[60:63], a[252:255], a[164:167], v[60:63]\n"
"v_lshl_add_u32 v101, v102, 10, v101\n"
"v_add_u32 v101, 94208, v101\n"
"v_mfma_f32_16x16x32_bf16 v[56:59], a[240:243], a[160:163], v[228:231]\n"
"ds_read_b64_tr_b16 a[198:199], v244 offset:8704\n"
"ds_read_b64_tr_b16 a[220:221], v245 offset:9216\n"
"v_mfma_f32_16x16x32_bf16 v[56:59], a[244:247], a[164:167], v[56:59]\n"
"s_add_u32 s40, s49, 40960\n"
"ds_read_b128 a[52:55], v101 offset:0\n"
"v_xor_b32 v253, s74, v253\n"
"v_mfma_f32_16x16x32_bf16 v[52:55], a[232:235], a[160:163], v[228:231]\n"
"s_add_u32 m0, s51, 4096\n"
"ds_read_b64_tr_b16 a[206:207], v245 offset:8704\n"
"ds_read_b128 a[76:79], v101 offset:49152\n"
"v_mfma_f32_16x16x32_bf16 v[52:55], a[236:239], a[164:167], v[52:55]\n"
"buffer_load_dwordx4 v248, s[24:27], s40 offen lds\n"
"v_mfma_f32_16x16x32_bf16 v[48:51], a[224:227], a[160:163], v[228:231]\n"
"v_xor_b32 v246, s74, v246\n"
"v_add_u32 v102, s75, v101\n"
"v_mfma_f32_16x16x32_bf16 v[48:51], a[228:231], a[164:167], v[48:51]\n"
"ds_read_b64_tr_b16 a[192:193], v244 offset:0\n"
"ds_read_b128 a[68:71], v101 offset:45056\n"
"v_mfma_f32_16x16x32_bf16 v[64:67], a[224:227], a[168:171], v[228:231]\n"
"ds_read_b64_tr_b16 a[212:213], v244 offset:9216\n"
"ds_read_b128 a[164:167], v253 offset:0\n"
"v_mfma_f32_16x16x32_bf16 v[64:67], a[228:231], a[172:175], v[64:67]\n"
"ds_read_b64_tr_b16 a[208:209], v244 offset:1024\n"
"ds_read_b64_tr_b16 a[218:219], v245 offset:1536\n"
"v_mfma_f32_16x16x32_bf16 v[68:71], a[232:235], a[168:171], v[228:231]\n"
"ds_read_b64_tr_b16 a[200:201], v245 offset:0\n"
"ds_read_b128 a[84:87], v101 offset:61440\n"
"v_mfma_f32_16x16x32_bf16 v[68:71], a[236:239], a[172:175], v[68:71]\n"
"buffer_load_dwordx4 v249, s[24:27], s40 offen offset:1024 lds\n"
"v_mfma_f32_16x16x32_bf16 v[72:75], a[240:243], a[168:171], v[228:231]\n"
"ds_read_b64_tr_b16 a[214:215], v244 offset:9728\n"
"ds_read_b128 a[60:63], v101 offset:32768\n"
"v_mfma_f32_16x16x32_bf16 v[72:75], a[244:247], a[172:175], v[72:75]\n"
"v_xor_b32 v250, s74, v250\n"
"v_xor_b32 v251, s74, v251\n"
"v_mfma_f32_16x16x32_bf16 v[76:79], a[248:251], a[168:171], v[228:231]\n"
"ds_read_b128 a[92:95], v102 offset:0\n"
"ds_read_b128 a[168:171], v250 offset:0\n"
"v_mfma_f32_16x16x32_bf16 v[76:79], a[252:255], a[172:175], v[76:79]\n"
"ds_read_b128 a[152:155], v246 offset:4096\n"
"v_xor_b32 v252, s74, v252\n"
"v_mfma_f32_16x16x32_bf16 v[92:95], a[248:251], a[176:179], v[228:231]\n"
"ds_read_b64_tr_b16 a[222:223], v245 offset:9728\n"
"ds_read_b128 a[160:163], v252 offset:0\n"
"v_mfma_f32_16x16x32_bf16 v[92:95], a[252:255], a[180:183], v[92:95]\n"
"buffer_load_dwordx4 v254, s[24:27], s40 offen offset:2048 lds\n"
"v_mfma_f32_16x16x32_bf16 v[88:91], a[240:243], a[176:179], v[228:231]\n"
"s_add_u32 s49, s49, 128\n"
"ds_read_b128 a[144:147], v246 offset:2048\n"
"ds_read_b128 a[148:151], v247 offset:2048\n"
"v_mfma_f32_16x16x32_bf16 v[88:91], a[244:247], a[180:183], v[88:91]\n"
"s_sub_u32 s41, s49, 1280\n"
"ds_read_b128 a[136:139], v246 offset:0\n"
"v_mfma_f32_16x16x32_bf16 v[84:87], a[232:235], a[176:179], v[228:231]\n"
"s_min_u32 s49, s49, s41\n"
"ds_read_b64_tr_b16 a[196:197], v244 offset:8192\n"
"ds_read_b64_tr_b16 a[210:211], v244 offset:1536\n"
"v_mfma_f32_16x16x32_bf16 v[84:87], a[236:239], a[180:183], v[84:87]\n"
"s_xor_b32 s51, s51, s74\n"
"ds_read_b64_tr_b16 a[194:195], v244 offset:512\n"
"ds_read_b128 a[156:159], v247 offset:4096\n"
"v_mfma_f32_16x16x32_bf16 v[80:83], a[224:227], a[176:179], v[228:231]\n"
"ds_read_b128 a[176:179], v250 offset:2048\n"
"v_mfma_f32_16x16x32_bf16 v[80:83], a[228:231], a[180:183], v[80:83]\n"
"ds_read_b128 a[172:175], v251 offset:0\n"
"ds_read_b128 a[180:183], v251 offset:2048\n"
"v_mfma_f32_16x16x32_bf16 v[96:99], a[224:227], a[184:187], v[228:231]\n"
"v_mfma_f32_16x16x32_bf16 v[96:99], a[228:231], a[188:191], v[96:99]\n"
"v_mfma_f32_16x16x32_bf16 v[100:103], a[232:235], a[184:187], v[228:231]\n"
"v_mfma_f32_16x16x32_bf16 v[100:103], a[236:239], a[188:191], v[100:103]\n"
"v_mfma_f32_16x16x32_bf16 v[104:107], a[240:243], a[184:187], v[228:231]\n"
"v_mfma_f32_16x16x32_bf16 v[104:107], a[244:247], a[188:191], v[104:107]\n"
"v_mfma_f32_16x16x32_bf16 v[108:111], a[248:251], a[184:187], v[228:231]\n"
"s_add_u32 s47, s47, 1\n"
"ds_read_b128 a[184:187], v250 offset:4096\n"
"v_mfma_f32_16x16x32_bf16 v[108:111], a[252:255], a[188:191], v[108:111]\n"
"s_waitcnt lgkmcnt(1)\n"
"ds_read_b128 a[188:191], v251 offset:4096\n"
"s_mov_b32 s62, 18\n"
"s_branch LPE_%=\n"
"LPT2_%=:\n"
"v_mfma_f32_16x16x32_bf16 v[112:115], a[192:195], a[136:139], v[112:115]\n"
"s_add_u32 s78, s32, s96\ns_addc_u32 s79, s33, 0\n"
"s_mov_b64 s[80:81], 0\n"
"s_mov_b32 s82, 0\n"
"v_mfma_f32_16x16x32_bf16 v[112:115], a[196:199], a[140:143], v[112:115]\n"
"v_mfma_f32_16x16x32_bf16 v[116:119], a[200:203], a[136:139], v[116:119]\n"
"s_waitcnt vmcnt(11)\n"
"s_load_dword s80, s[78:79], 0\n"
"v_mfma_f32_16x16x32_bf16 v[116:119], a[204:207], a[140:143], v[116:119]\n"
"s_barrier\n"
"v_mfma_f32_16x16x32_bf16 v[120:123], a[208:211], a[136:139], v[120:123]\n"
"s_add_u32 s40, s48, 256\n"
"s_xor_b32 m0, s50, 0xc000\n"
"ds_read_b64_tr_b16 a[254:255], v245 offset:26112\n"
"v_mfma_f32_16x16x32_bf16 v[120:123], a[212:215], a[140:143], v[120:123]\n"
"buffer_load_dwordx4 v224, s[28:31], s40 offen lds\n"
"v_mfma_f32_16x16x32_bf16 v[124:127], a[216:219], a[136:139], v[124:127]\n"
"v_mfma_f32_16x16x32_bf16 v[124:127], a[220:223], a[140:143], v[124:127]\n"
"v_mfma_f32_16x16x32_bf16 v[140:143], a[216:219], a[144:147], v[140:143]\n"
"ds_read_b64_tr_b16 a[246:247], v244 offset:26112\n"
"v_mfma_f32_16x16x32_bf16 v[140:143], a[220:223], a[148:151], v[140:143]\n"
"v_mfma_f32_16x16x32_bf16 v[136:139], a[208:211], a[144:147], v[136:139]\n"
"ds_read_b64_tr_b16 a[226:227], v244 offset:16896\n"
"v_mfma_f32_16x16x32_bf16 v[136:139], a[212:215], a[148:151], v[136:139]\n"
"buffer_load_dwordx4 v225, s[28:31], s40 offen offset:1024 lds\n"
"v_mfma_f32_16x16x32_bf16 v[132:135], a[200:203], a[144:147], v[132:135]\n"
"s_add_u32 s82, s82, s80\n"
"v_mfma_f32_16x16x32_bf16 v[132:135], a[204:207], a[148:151], v[132:135]\n"
"s_add_u32 s82, s82, s80\n"
"v_mfma_f32_16x16x32_bf16 v[128:131], a[192:195], a[144:147], v[128:131]\n"
"s_add_u32 s82, s82, s80\n"
"ds_read_b64_tr_b16 a[244:245], v244 offset:25600\n"
"v_mfma_f32_16x16x32_bf16 v[128:131], a[196:199], a[148:151], v[128:131]\n"
"s_add_u32 s82, s82, s80\n"
"v_mfma_f32_16x16x32_bf16 v[144:147], a[192:195], a[152:155], v[144:147]\n"
"s_add_u32 s82, s82, s80\n"
"ds_read_b64_tr_b16 a[236:237], v245 offset:24576\n"
"v_mfma_f32_16x16x32_bf16 v[144:147], a[196:199], a[156:159], v[144:147]\n"
"s_add_u32 s82, s82, s80\n"
"v_mfma_f32_16x16x32_bf16 v[148:151], a[200:203], a[152:155], v[148:151]\n"
"s_add_u32 s82, s82, s80\n"
"ds_read_b64_tr_b16 a[224:225], v244 offset:16384\n"
"v_mfma_f32_16x16x32_bf16 v[148:151], a[204:207], a[156:159], v[148:151]\n"
"s_add_u32 s82, s82, s80\n"
"buffer_load_dwordx4 v226, s[28:31], s40 offen offset:2048 lds\n"
"v_mfma_f32_16x16x32_bf16 v[152:155], a[208:211], a[152:155], v[152:155]\n"
"s_add_u32 s82, s82, s80\n"
"v_mfma_f32_16x16x32_bf16 v[152:155], a[212:215], a[156:159], v[152:155]\n"
"s_add_u32 s82, s82, s80\n"
"v_mfma_f32_16x16x32_bf16 v[156:159], a[216:219], a[152:155], v[156:159]\n"
"s_add_u32 s82, s82, s80\n"
"ds_read_b64_tr_b16 a[234:235], v245 offset:16896\n"
"v_mfma_f32_16x16x32_bf16 v[156:159], a[220:223], a[156:159], v[156:159]\n"
"s_add_u32 s82, s82, s80\n"
"v_mfma_f32_16x16x32_bf16 v[172:175], a[216:219], a[160:163], v[172:175]\n"
"s_add_u32 s82, s82, s80\n"
"ds_read_b64_tr_b16 a[250:251], v245 offset:17920\n"
"v_mfma_f32_16x16x32_bf16 v[172:175], a[220:223], a[164:167], v[172:175]\n"
"s_add_u32 s82, s82, s80\n"
"buffer_load_dwordx4 v227, s[28:31], s40 offen offset:3072 lds\n"
"v_mfma_f32_16x16x32_bf16 v[168:171], a[208:211], a[160:163], v[168:171]\n"
"s_add_u32 s82, s82, s80\n"
"s_mov_b32 s58, 245760\n"
"v_mfma_f32_16x16x32_bf16 v[168:171], a[212:215], a[164:167], v[168:171]\n"
"s_add_u32 s82, s82, s80\n"
"s_mov_b32 s47, 0\n"
"v_mfma_f32_16x16x32_bf16 v[164:167], a[200:203], a[160:163], v[164:167]\n"
"s_add_u32 s82, s82, s80\n"
"s_mov_b32 s52, -896\n"
"ds_read_b64_tr_b16 a[252:253], v245 offset:25600\n"
"v_mfma_f32_16x16x32_bf16 v[164:167], a[204:207], a[164:167], v[164:167]\n"
"s_load_dword s81, s[78:79], 0x2000\n"
"s_mov_b32 s53, 0\n"
"v_mfma_f32_16x16x32_bf16 v[160:163], a[192:195], a[160:163], v[160:163]\n"
"s_mov_b32 m0, s44\n"
"ds_read_b64_tr_b16 a[248:249], v245 offset:17408\n"
"v_mfma_f32_16x16x32_bf16 v[160:163], a[196:199], a[164:167], v[160:163]\n"
"buffer_load_dwordx4 v240, s[16:19], s56 offen nt lds\n"
"v_mfma_f32_16x16x32_bf16 v[176:179], a[192:195], a[168:171], v[176:179]\n"
"v_mfma_f32_16x16x32_bf16 v[176:179], a[196:199], a[172:175], v[176:179]\n"
"v_mfma_f32_16x16x32_bf16 v[180:183], a[200:203], a[168:171], v[180:183]\n"
"ds_read_b64_tr_b16 a[230:231], v244 offset:25088\n"
"v_mfma_f32_16x16x32_bf16 v[180:183], a[204:207], a[172:175], v[180:183]\n"
"v_mfma_f32_16x16x32_bf16 v[184:187], a[208:211], a[168:171], v[184:187]\n"
"ds_read_b64_tr_b16 a[238:239], v245 offset:25088\n"
"v_mfma_f32_16x16x32_bf16 v[184:187], a[212:215], a[172:175], v[184:187]\n"
"v_mfma_f32_16x16x32_bf16 v[188:191], a[216:219], a[168:171], v[188:191]\n"
"s_add_u32 s40, s56, s52\n"
"ds_read_b64_tr_b16 a[228:229], v244 offset:24576\n"
"v_mfma_f32_16x16x32_bf16 v[188:191], a[220:223], a[172:175], v[188:191]\n"
"s_sub_u32 s82, s82, s81\n"
"buffer_load_dwordx4 v240, s[16:19], s40 offen offset:1024 nt lds\n"
"v_mfma_f32_16x16x32_bf16 v[204:207], a[216:219], a[176:179], v[204:207]\n"
"s_sub_u32 s82, s82, s81\n"
"v_xor_b32 v250, s74, v250\n"
"v_mfma_f32_16x16x32_bf16 v[204:207], a[220:223], a[180:183], v[204:207]\n"
"s_sub_u32 s82, s82, s81\n"
"v_mfma_f32_16x16x32_bf16 v[200:203], a[208:211], a[176:179], v[200:203]\n"
"s_sub_u32 s82, s82, s81\n"
"ds_read_b64_tr_b16 a[232:233], v245 offset:16384\n"
"v_mfma_f32_16x16x32_bf16 v[200:203], a[212:215], a[180:183], v[200:203]\n"
"s_sub_u32 s82, s82, s81\n"
"v_mfma_f32_16x16x32_bf16 v[196:199], a[200:203], a[176:179], v[196:199]\n"
"s_sub_u32 s82, s82, s81\n"
"s_add_u32 s40, s56, s53\n"
"ds_read_b64_tr_b16 a[240:241], v244 offset:17408\n"
"v_mfma_f32_16x16x32_bf16 v[196:199], a[204:207], a[180:183], v[196:199]\n"
"s_sub_u32 s82, s82, s81\n"
"buffer_load_dwordx4 v240, s[16:19], s40 offen offset:2048 nt lds\n"
"v_mfma_f32_16x16x32_bf16 v[192:195], a[192:195], a[176:179], v[192:195]\n"
"s_sub_u32 s82, s82, s81\n"
"v_and_b32 v229, 0x60, v242\n"
"v_mfma_f32_16x16x32_bf16 v[192:195], a[196:199], a[180:183], v[192:195]\n"
"s_waitcnt lgkmcnt(2)\n"
"s_sub_u32 s82, s82, s81\n"
"v_mfma_f32_16x16x32_bf16 v[208:211], a[192:195], a[184:187], v[208:211]\n"
"s_sub_u32 s82, s82, s81\n"
"ds_read_b64_tr_b16 a[242:243], v244 offset:17920\n"
"v_mfma_f32_16x16x32_bf16 v[208:211], a[196:199], a[188:191], v[208:211]\n"
"s_sub_u32 s82, s82, s81\n"
"v_xor_b32 v246, s74, v246\n"
"v_mfma_f32_16x16x32_bf16 v[212:215], a[200:203], a[184:187], v[212:215]\n"
"s_sub_u32 s82, s82, s81\n"
"s_add_u32 s40, s56, s54\n"
"v_mfma_f32_16x16x32_bf16 v[212:215], a[204:207], a[188:191], v[212:215]\n"
"s_sub_u32 s82, s82, s81\n"
"buffer_load_dwordx4 v240, s[16:19], s40 offen offset:3072 nt lds\n"
"v_mfma_f32_16x16x32_bf16 v[216:219], a[208:211], a[184:187], v[216:219]\n"
"s_sub_u32 s82, s82, s81\n"
"v_bfe_u32 v228, v242, 8, 2\n"
"v_mfma_f32_16x16x32_bf16 v[216:219], a[212:215], a[188:191], v[216:219]\n"
"s_sub_u32 s82, s82, s81\n"
"v_mfma_f32_16x16x32_bf16 v[220:223], a[216:219], a[184:187], v[220:223]\n"
"s_sub_u32 s82, s82, s81\n"
"v_mfma_f32_16x16x32_bf16 v[220:223], a[220:223], a[188:191], v[220:223]\n"
"s_sub_u32 s82, s82, s81\n"
"v_mfma_f32_16x16x32_bf16 v[0:3], a[224:227], a[136:139], v[0:3]\n"
"v_mfma_f32_16x16x32_bf16 v[0:3], a[228:231], a[140:143], v[0:3]\n"
"s_waitcnt lgkmcnt(0)\n"
"v_mfma_f32_16x16x32_bf16 v[4:7], a[232:235], a[136:139], v[4:7]\n"
"s_waitcnt vmcnt(8)\n"
"v_xor_b32 v245, s74, v245\n"
"v_mfma_f32_16x16x32_bf16 v[4:7], a[236:239], a[140:143], v[4:7]\n"
"s_barrier\n"
"v_mfma_f32_16x16x32_bf16 v[8:11], a[240:243], a[136:139], v[8:11]\n"
"s_add_u32 m0, s44, 65536\n"
"ds_read_b64_tr_b16 a[204:205], v245 offset:8192\n"
"v_mfma_f32_16x16x32_bf16 v[8:11], a[244:247], a[140:143], v[8:11]\n"
"buffer_load_dwordx4 v240, s[20:23], s56 offen nt lds\n"
"v_mfma_f32_16x16x32_bf16 v[12:15], a[248:251], a[136:139], v[12:15]\n"
"s_lshr_b32 s82, s82, 31\n"
"v_xor_b32 v244, s74, v244\n"
"v_mfma_f32_16x16x32_bf16 v[12:15], a[252:255], a[140:143], v[12:15]\n"
"s_add_u32 s82, s82, s60\n"
"v_xor_b32 v247, s74, v247\n"
"v_mfma_f32_16x16x32_bf16 v[28:31], a[248:251], a[144:147], v[28:31]\n"
"s_add_u32 s82, s82, 32\n"
"ds_read_b64_tr_b16 a[208:209], v244 offset:1024\n"
"v_mfma_f32_16x16x32_bf16 v[28:31], a[252:255], a[148:151], v[28:31]\n"
"v_mov_b32 v254, s82\n"
"ds_read_b64_tr_b16 a[196:197], v244 offset:8192\n"
"v_mfma_f32_16x16x32_bf16 v[24:27], a[240:243], a[144:147], v[24:27]\n"
"v_lshlrev_b32_e64 v255, 2, s93\n"
"ds_read_b64_tr_b16 a[210:211], v244 offset:1536\n"
"v_mfma_f32_16x16x32_bf16 v[24:27], a[244:247], a[148:151], v[24:27]\n"
"ds_read_b64_tr_b16 a[222:223], v245 offset:9728\n"
"v_xor_b32 v251, s74, v251\n"
"v_mfma_f32_16x16x32_bf16 v[20:23], a[232:235], a[144:147], v[20:23]\n"
"s_add_u32 s40, s56, s52\n"
"ds_read_b64_tr_b16 a[202:203], v245 offset:512\n"
"v_mfma_f32_16x16x32_bf16 v[20:23], a[236:239], a[148:151], v[20:23]\n"
"buffer_load_dwordx4 v240, s[20:23], s40 offen offset:1024 nt lds\n"
"v_mfma_f32_16x16x32_bf16 v[16:19], a[224:227], a[144:147], v[16:19]\n"
"v_mfma_f32_16x16x32_bf16 v[16:19], a[228:231], a[148:151], v[16:19]\n"
"s_mov_b64 exec, 1\n"
"global_store_dword v255, v254, s[6:7] offset:256 sc1\n"
"s_mov_b64 exec, -1\n"
"v_mfma_f32_16x16x32_bf16 v[32:35], a[224:227], a[152:155], v[32:35]\n"
"ds_read_b64_tr_b16 a[192:193], v244 offset:0\n"
"v_bfe_i32 v241, v242, 4, 1\n"
"v_mfma_f32_16x16x32_bf16 v[32:35], a[228:231], a[156:159], v[32:35]\n"
"v_mfma_f32_16x16x32_bf16 v[36:39], a[232:235], a[152:155], v[36:39]\n"
"s_add_u32 s40, s56, s53\n"
"ds_read_b128 a[136:139], v246 offset:0\n"
"v_mfma_f32_16x16x32_bf16 v[36:39], a[236:239], a[156:159], v[36:39]\n"
"buffer_load_dwordx4 v240, s[20:23], s40 offen offset:2048 nt lds\n"
"v_mfma_f32_16x16x32_bf16 v[40:43], a[240:243], a[152:155], v[40:43]\n"
"v_mfma_f32_16x16x32_bf16 v[40:43], a[244:247], a[156:159], v[40:43]\n"
"v_mfma_f32_16x16x32_bf16 v[44:47], a[248:251], a[152:155], v[44:47]\n"
"ds_read_b128 a[140:143], v247 offset:0\n"
"v_mfma_f32_16x16x32_bf16 v[44:47], a[252:255], a[156:159], v[44:47]\n"
"v_mfma_f32_16x16x32_bf16 v[60:63], a[248:251], a[160:163], v[60:63]\n"
"s_add_u32 s40, s56, s54\n"
"ds_read_b64_tr_b16 a[194:195], v244 offset:512\n"
"v_mfma_f32_16x16x32_bf16 v[60:63], a[252:255], a[164:167], v[60:63]\n"
"buffer_load_dwordx4 v240, s[20:23], s40 offen offset:3072 nt lds\n"
"v_mfma_f32_16x16x32_bf16 v[56:59], a[240:243], a[160:163], v[56:59]\n"
"s_add_u32 s55, s55, s59\n"
"v_mfma_f32_16x16x32_bf16 v[56:59], a[244:247], a[164:167], v[56:59]\n"
"s_add_u32 s41, s55, s57\n"
"v_mfma_f32_16x16x32_bf16 v[52:55], a[232:235], a[160:163], v[52:55]\n"
"s_and_b32 s41, s41, s58\n"
"ds_read_b64_tr_b16 a[218:219], v245 offset:1536\n"
"v_mfma_f32_16x16x32_bf16 v[52:55], a[236:239], a[164:167], v[52:55]\n"
"s_andn2_b32 s56, s55, s58\n"
"ds_read_b64_tr_b16 a[200:201], v245 offset:0\n"
"v_lshlrev_b32 v228, 4, v228\n"
"v_mfma_f32_16x16x32_bf16 v[48:51], a[224:227], a[160:163], v[48:51]\n"
"s_or_b32 s56, s56, s41\n"
"ds_read_b64_tr_b16 a[212:213], v244 offset:9216\n"
"v_mfma_f32_16x16x32_bf16 v[48:51], a[228:231], a[164:167], v[48:51]\n"
"ds_read_b64_tr_b16 a[198:199], v244 offset:8704\n"
"v_mfma_f32_16x16x32_bf16 v[64:67], a[224:227], a[168:171], v[64:67]\n"
"s_add_u32 m0, s44, 16384\n"
"ds_read_b64_tr_b16 a[214:215], v244 offset:9728\n"
"v_mfma_f32_16x16x32_bf16 v[64:67], a[228:231], a[172:175], v[64:67]\n"
"buffer_load_dwordx4 v240, s[16:19], s56 offen nt lds\n"
"v_mfma_f32_16x16x32_bf16 v[68:71], a[232:235], a[168:171], v[68:71]\n"
"v_mfma_f32_16x16x32_bf16 v[68:71], a[236:239], a[172:175], v[68:71]\n"
"v_mfma_f32_16x16x32_bf16 v[72:75], a[240:243], a[168:171], v[72:75]\n"
"ds_read_b64_tr_b16 a[216:217], v245 offset:1024\n"
"v_and_b32 v254, 0x20, v242\n"
"v_mfma_f32_16x16x32_bf16 v[72:75], a[244:247], a[172:175], v[72:75]\n"
"ds_read_b64_tr_b16 a[220:221], v245 offset:9216\n"
"v_mfma_f32_16x16x32_bf16 v[76:79], a[248:251], a[168:171], v[76:79]\n"
"s_add_u32 s40, s56, s52\n"
"ds_read_b64_tr_b16 a[206:207], v245 offset:8704\n"
"v_mfma_f32_16x16x32_bf16 v[76:79], a[252:255], a[172:175], v[76:79]\n"
"buffer_load_dwordx4 v240, s[16:19], s40 offen offset:1024 nt lds\n"
"v_mfma_f32_16x16x32_bf16 v[92:95], a[248:251], a[176:179], v[92:95]\n"
"v_xor_b32 v228, v228, v229\n"
"v_mfma_f32_16x16x32_bf16 v[92:95], a[252:255], a[180:183], v[92:95]\n"
"v_and_b32 v229, 0x70, v242\n"
"v_mfma_f32_16x16x32_bf16 v[88:91], a[240:243], a[176:179], v[88:91]\n"
"ds_read_b128 a[172:175], v251 offset:0\n"
"v_mfma_f32_16x16x32_bf16 v[88:91], a[244:247], a[180:183], v[88:91]\n"
"v_lshl_add_u32 v228, v229, 3, v228\n"
"v_mfma_f32_16x16x32_bf16 v[84:87], a[232:235], a[176:179], v[84:87]\n"
"s_add_u32 s40, s56, s53\n"
"ds_read_b128 a[168:171], v250 offset:0\n"
"v_add_u32 v254, -1, v254\n"
"v_mfma_f32_16x16x32_bf16 v[84:87], a[236:239], a[180:183], v[84:87]\n"
"buffer_load_dwordx4 v240, s[16:19], s40 offen offset:2048 nt lds\n"
"v_mfma_f32_16x16x32_bf16 v[80:83], a[224:227], a[176:179], v[80:83]\n"
"v_mfma_f32_16x16x32_bf16 v[80:83], a[228:231], a[180:183], v[80:83]\n"
"v_mfma_f32_16x16x32_bf16 v[96:99], a[224:227], a[184:187], v[96:99]\n"
"ds_read_b128 a[152:155], v246 offset:4096\n"
"v_mfma_f32_16x16x32_bf16 v[96:99], a[228:231], a[188:191], v[96:99]\n"
"v_mfma_f32_16x16x32_bf16 v[100:103], a[232:235], a[184:187], v[100:103]\n"
"s_add_u32 s40, s56, s54\n"
"ds_read_b128 a[144:147], v246 offset:2048\n"
"v_and_b32 v241, 0xaaaaaaaa, v241\n"
"v_mfma_f32_16x16x32_bf16 v[100:103], a[236:239], a[188:191], v[100:103]\n"
"buffer_load_dwordx4 v240, s[16:19], s40 offen offset:3072 nt lds\n"
"v_mfma_f32_16x16x32_bf16 v[104:107], a[240:243], a[184:187], v[104:107]\n"
"v_mfma_f32_16x16x32_bf16 v[104:107], a[244:247], a[188:191], v[104:107]\n"
"v_mfma_f32_16x16x32_bf16 v[108:111], a[248:251], a[184:187], v[108:111]\n"
"ds_read_b128 a[180:183], v251 offset:2048\n"
"v_mfma_f32_16x16x32_bf16 v[108:111], a[252:255], a[188:191], v[108:111]\n"
"s_waitcnt lgkmcnt(3)\n"
"v_and_b32 v229, 0x80, v242\n"
"v_mfma_f32_16x16x32_bf16 v[112:115], a[192:195], a[136:139], v[112:115]\n"
"ds_read_b128 a[176:179], v250 offset:2048\n"
"v_and_b32 v249, 0xf0, v242\n"
"v_mfma_f32_16x16x32_bf16 v[112:115], a[196:199], a[140:143], v[112:115]\n"
"v_xor_b32 v253, s74, v253\n"
"v_bfe_u32 v255, v242, 9, 1\n"
"v_mfma_f32_16x16x32_bf16 v[116:119], a[200:203], a[136:139], v[116:119]\n"
"s_waitcnt vmcnt(13)\n"
"ds_read_b128 a[184:187], v250 offset:4096\n"
"v_lshl_add_u32 v255, v255, 8, v249\n"
"v_mfma_f32_16x16x32_bf16 v[116:119], a[204:207], a[140:143], v[116:119]\n"
"s_barrier\n"
"v_mfma_f32_16x16x32_bf16 v[120:123], a[208:211], a[136:139], v[120:123]\n"
"s_add_u32 m0, s44, 81920\n"
"ds_read_b64_tr_b16 a[238:239], v245 offset:25088\n"
"v_mfma_f32_16x16x32_bf16 v[120:123], a[212:215], a[140:143], v[120:123]\n"
"buffer_load_dwordx4 v240, s[20:23], s56 offen nt lds\n"
"v_mfma_f32_16x16x32_bf16 v[124:127], a[216:219], a[136:139], v[124:127]\n"
"v_ashrrev_i32 v254, 31, v254\n"
"v_bfe_u32 v248, v242, 9, 1\n"
"v_mfma_f32_16x16x32_bf16 v[124:127], a[220:223], a[140:143], v[124:127]\n"
"v_lshl_add_u32 v228, v229, 4, v228\n"
"v_bfe_u32 v249, v242, 8, 1\n"
"v_mfma_f32_16x16x32_bf16 v[188:191], a[216:219], a[168:171], v[188:191]\n"
"ds_read_b64_tr_b16 a[248:249], v245 offset:17408\n"
"v_or_b32 v241, 0x44444444, v241\n"
"v_mfma_f32_16x16x32_bf16 v[188:191], a[220:223], a[172:175], v[188:191]\n"
"ds_read_b64_tr_b16 a[242:243], v244 offset:17920\n"
"v_mfma_f32_16x16x32_bf16 v[184:187], a[208:211], a[168:171], v[184:187]\n"
"v_lshl_add_u32 v255, v249, 3, v255\n"
"v_xor_b32_e64 v249, s42, 1\n"
"v_mfma_f32_16x16x32_bf16 v[184:187], a[212:215], a[172:175], v[184:187]\n"
"ds_read_b128 a[188:191], v251 offset:4096\n"
"v_mov_b32 v229, s43\n"
"v_mfma_f32_16x16x32_bf16 v[180:183], a[200:203], a[168:171], v[180:183]\n"
"v_mfma_f32_16x16x32_bf16 v[180:183], a[204:207], a[172:175], v[180:183]\n"
"ds_read_b128 a[148:151], v247 offset:2048\n"
"v_mfma_f32_16x16x32_bf16 v[176:179], a[192:195], a[168:171], v[176:179]\n"
"v_lshl_add_u32 v255, v249, 12, v255\n"
"v_mfma_f32_16x16x32_bf16 v[176:179], a[196:199], a[172:175], v[176:179]\n"
"s_waitcnt lgkmcnt(4)\n"
"ds_read_b64_tr_b16 a[224:225], v244 offset:16384\n"
"v_xor_b32 v252, s74, v252\n"
"v_mfma_f32_16x16x32_bf16 v[192:195], a[192:195], a[176:179], v[192:195]\n"
"ds_read_b64_tr_b16 a[250:251], v245 offset:17920\n"
"v_mul_u32_u24 v229, 0x81, v229\n"
"v_mfma_f32_16x16x32_bf16 v[192:195], a[196:199], a[180:183], v[192:195]\n"
"ds_read_b64_tr_b16 a[236:237], v245 offset:24576\n"
"v_mfma_f32_16x16x32_bf16 v[196:199], a[200:203], a[176:179], v[196:199]\n"
"s_add_u32 s40, s56, s52\n"
"ds_read_b64_tr_b16 a[226:227], v244 offset:16896\n"
"v_mfma_f32_16x16x32_bf16 v[196:199], a[204:207], a[180:183], v[196:199]\n"
"buffer_load_dwordx4 v240, s[20:23], s40 offen offset:1024 nt lds\n"
"v_mfma_f32_16x16x32_bf16 v[200:203], a[208:211], a[176:179], v[200:203]\n"
"v_and_b32 v249, 0xf0, v242\n"
"v_mfma_f32_16x16x32_bf16 v[200:203], a[212:215], a[180:183], v[200:203]\n"
"v_mfma_f32_16x16x32_bf16 v[204:207], a[216:219], a[176:179], v[204:207]\n"
"ds_read_b128 a[164:167], v253 offset:0\n"
"v_mfma_f32_16x16x32_bf16 v[204:207], a[220:223], a[180:183], v[204:207]\n"
"v_mfma_f32_16x16x32_bf16 v[220:223], a[216:219], a[184:187], v[220:223]\n"
"s_waitcnt lgkmcnt(3)\n"
"ds_read_b64_tr_b16 a[234:235], v245 offset:16896\n"
"v_lshl_add_u32 v248, v248, 8, v249\n"
"v_mfma_f32_16x16x32_bf16 v[220:223], a[220:223], a[188:191], v[220:223]\n"
"v_mfma_f32_16x16x32_bf16 v[216:219], a[208:211], a[184:187], v[216:219]\n"
"ds_read_b128 a[160:163], v252 offset:0\n"
"v_xor_b32 v228, v228, v229\n"
"v_mfma_f32_16x16x32_bf16 v[216:219], a[212:215], a[188:191], v[216:219]\n"
"v_pk_fma_f32 v[176:177], v[176:177], s[70:71], s[70:71] op_sel:[0,0,1] op_sel_hi:[1,0,1]\nv_pk_fma_f32 v[178:179], v[178:179], s[70:71], s[70:71] op_sel:[0,0,1] op_sel_hi:[1,0,1]\nv_pk_fma_f32 v[180:181], v[180:181], s[70:71], s[70:71] op_sel:[0,0,1] op_sel_hi:[1,0,1]\nv_pk_fma_f32 v[182:183], v[182:183], s[70:71], s[70:71] op_sel:[0,0,1] op_sel_hi:[1,0,1]\nv_pk_fma_f32 v[184:185], v[184:185], s[70:71], s[70:71] op_sel:[0,0,1] op_sel_hi:[1,0,1]\nv_pk_fma_f32 v[186:187], v[186:187], s[70:71], s[70:71] op_sel:[0,0,1] op_sel_hi:[1,0,1]\nv_pk_fma_f32 v[188:189], v[188:189], s[70:71], s[70:71] op_sel:[0,0,1] op_sel_hi:[1,0,1]\nv_pk_fma_f32 v[190:191], v[190:191], s[70:71], s[70:71] op_sel:[0,0,1] op_sel_hi:[1,0,1]\nv_pk_fma_f32 v[192:193], v[192:193], s[70:71], s[70:71] op_sel:[0,0,1] op_sel_hi:[1,0,1]\nv_pk_fma_f32 v[194:195], v[194:195], s[70:71], s[70:71] op_sel:[0,0,1] op_sel_hi:[1,0,1]\nv_pk_fma_f32 v[196:197], v[196:197], s[70:71], s[70:71] op_sel:[0,0,1] op_sel_hi:[1,0,1]\nv_pk_fma_f32 v[198:199], v[198:199], s[70:71], s[70:71] op_sel:[0,0,1] op_sel_hi:[1,0,1]\nv_pk_fma_f32 v[200:201], v[200:201], s[70:71], s[70:71] op_sel:[0,0,1] op_sel_hi:[1,0,1]\nv_pk_fma_f32 v[202:203], v[202:203], s[70:71], s[70:71] op_sel:[0,0,1] op_sel_hi:[1,0,1]\nv_pk_fma_f32 v[204:205], v[204:205], s[70:71], s[70:71] op_sel:[0,0,1] op_sel_hi:[1,0,1]\nv_pk_fma_f32 v[206:207], v[206:207], s[70:71], s[70:71] op_sel:[0,0,1] op_sel_hi:[1,0,1]\n"
"v_mfma_f32_16x16x32_bf16 v[212:215], a[200:203], a[184:187], v[212:215]\n"
"ds_read_b64_tr_b16 a[252:253], v245 offset:25600\n"
"v_cvt_pk_bf16_f32 v176, v176, v177\n"
"v_mfma_f32_16x16x32_bf16 v[212:215], a[204:207], a[188:191], v[212:215]\n"
"v_cvt_pk_bf16_f32 v177, v178, v179\n"
"v_cvt_pk_bf16_f32 v192, v192, v193\n"
"v_mfma_f32_16x16x32_bf16 v[208:211], a[192:195], a[184:187], v[208:211]\n"
"s_add_u32 s40, s56, s53\n"
"ds_read_b128 a[156:159], v247 offset:4096\n"
"v_cvt_pk_bf16_f32 v184, v184, v185\n"
"v_mfma_f32_16x16x32_bf16 v[208:211], a[196:199], a[188:191], v[208:211]\n"
"s_waitcnt lgkmcnt(2)\n"
"buffer_load_dwordx4 v240, s[20:23], s40 offen offset:2048 nt lds\n"
"v_mfma_f32_16x16x32_bf16 v[160:163], a[192:195], a[160:163], v[160:163]\n"
"v_mfma_f32_16x16x32_bf16 v[160:163], a[196:199], a[164:167], v[160:163]\n"
"v_xor_b32 v230, 0x2000, v228\n"
"v_mfma_f32_16x16x32_bf16 v[164:167], a[200:203], a[160:163], v[164:167]\n"
"ds_read_b64_tr_b16 a[244:245], v244 offset:25600\n"
"v_cvt_pk_bf16_f32 v185, v186, v187\n"
"v_mfma_f32_16x16x32_bf16 v[164:167], a[204:207], a[164:167], v[164:167]\n"
"v_cvt_pk_bf16_f32 v193, v194, v195\n"
"v_cvt_pk_bf16_f32 v200, v200, v201\n"
"v_mfma_f32_16x16x32_bf16 v[168:171], a[208:211], a[160:163], v[168:171]\n"
"ds_read_b64_tr_b16 a[240:241], v244 offset:17408\n"
"v_cvt_pk_bf16_f32 v195, v198, v199\n"
"v_mfma_f32_16x16x32_bf16 v[168:171], a[212:215], a[164:167], v[168:171]\n"
"v_cvt_pk_bf16_f32 v194, v196, v197\n"
"v_xor_b32 v198, 0x400, v243\n"
"v_mfma_f32_16x16x32_bf16 v[172:175], a[216:219], a[160:163], v[172:175]\n"
"ds_read_b64_tr_b16 a[254:255], v245 offset:26112\n"
"v_mfma_f32_16x16x32_bf16 v[172:175], a[220:223], a[164:167], v[172:175]\n"
"v_cvt_pk_bf16_f32 v201, v202, v203\n"
"v_and_b32 v254, 0x3f803f80, v254\n"
"v_mfma_f32_16x16x32_bf16 v[140:143], a[216:219], a[144:147], v[140:143]\n"
"ds_read_b64_tr_b16 a[246:247], v244 offset:26112\n"
"v_bfe_u32 v196, v242, 8, 2\n"
"v_mfma_f32_16x16x32_bf16 v[140:143], a[220:223], a[148:151], v[140:143]\n"
"v_xor_b32 v229, 64, v228\n"
"v_cvt_pk_bf16_f32 v186, v188, v189\n"
"v_mfma_f32_16x16x32_bf16 v[136:139], a[208:211], a[144:147], v[136:139]\n"
"ds_read_b64_tr_b16 a[228:229], v244 offset:24576\n"
"v_bfe_u32 v249, v242, 8, 1\n"
"v_mfma_f32_16x16x32_bf16 v[136:139], a[212:215], a[148:151], v[136:139]\n"
"v_pk_fma_f32 v[208:209], v[208:209], s[70:71], s[70:71] op_sel:[0,0,1] op_sel_hi:[1,0,1]\nv_pk_fma_f32 v[210:211], v[210:211], s[70:71], s[70:71] op_sel:[0,0,1] op_sel_hi:[1,0,1]\nv_pk_fma_f32 v[212:213], v[212:213], s[70:71], s[70:71] op_sel:[0,0,1] op_sel_hi:[1,0,1]\nv_pk_fma_f32 v[214:215], v[214:215], s[70:71], s[70:71] op_sel:[0,0,1] op_sel_hi:[1,0,1]\nv_pk_fma_f32 v[216:217], v[216:217], s[70:71], s[70:71] op_sel:[0,0,1] op_sel_hi:[1,0,1]\nv_pk_fma_f32 v[218:219], v[218:219], s[70:71], s[70:71] op_sel:[0,0,1] op_sel_hi:[1,0,1]\nv_pk_fma_f32 v[220:221], v[220:221], s[70:71], s[70:71] op_sel:[0,0,1] op_sel_hi:[1,0,1]\nv_pk_fma_f32 v[222:223], v[222:223], s[70:71], s[70:71] op_sel:[0,0,1] op_sel_hi:[1,0,1]\nv_pk_fma_f32 v[160:161], v[160:161], s[70:71], s[70:71] op_sel:[0,0,1] op_sel_hi:[1,0,1]\nv_pk_fma_f32 v[162:163], v[162:163], s[70:71], s[70:71] op_sel:[0,0,1] op_sel_hi:[1,0,1]\nv_pk_fma_f32 v[164:165], v[164:165], s[70:71], s[70:71] op_sel:[0,0,1] op_sel_hi:[1,0,1]\nv_pk_fma_f32 v[166:167], v[166:167], s[70:71], s[70:71] op_sel:[0,0,1] op_sel_hi:[1,0,1]\nv_pk_fma_f32 v[168:169], v[168:169], s[70:71], s[70:71] op_sel:[0,0,1] op_sel_hi:[1,0,1]\nv_pk_fma_f32 v[170:171], v[170:171], s[70:71], s[70:71] op_sel:[0,0,1] op_sel_hi:[1,0,1]\nv_pk_fma_f32 v[172:173], v[172:173], s[70:71], s[70:71] op_sel:[0,0,1] op_sel_hi:[1,0,1]\nv_pk_fma_f32 v[174:175], v[174:175], s[70:71], s[70:71] op_sel:[0,0,1] op_sel_hi:[1,0,1]\n"
"v_mfma_f32_16x16x32_bf16 v[132:135], a[200:203], a[144:147], v[132:135]\n"
"s_add_u32 s40, s56, s54\n"
"v_cvt_pk_bf16_f32 v168, v168, v169\n"
"v_cvt_pk_bf16_f32 v169, v170, v171\n"
"v_mfma_f32_16x16x32_bf16 v[132:135], a[204:207], a[148:151], v[132:135]\n"
"buffer_load_dwordx4 v240, s[20:23], s40 offen offset:3072 nt lds\n"
"v_mfma_f32_16x16x32_bf16 v[128:131], a[192:195], a[144:147], v[128:131]\n"
"s_add_u32 s55, s55, s59\n"
"v_cvt_pk_bf16_f32 v216, v216, v217\n"
"v_cvt_pk_bf16_f32 v171, v174, v175\n"
"v_mfma_f32_16x16x32_bf16 v[128:131], a[196:199], a[148:151], v[128:131]\n"
"s_add_u32 s41, s55, s57\n"
"v_cvt_pk_bf16_f32 v179, v182, v183\n"
"v_cvt_pk_bf16_f32 v160, v160, v161\n"
"v_mfma_f32_16x16x32_bf16 v[144:147], a[192:195], a[152:155], v[144:147]\n"
"s_waitcnt lgkmcnt(1)\n"
"s_and_b32 s41, s41, s58\n"
"ds_read_b64_tr_b16 a[230:231], v244 offset:25088\n"
"v_cvt_pk_bf16_f32 v161, v162, v163\n"
"v_mfma_f32_16x16x32_bf16 v[144:147], a[196:199], a[156:159], v[144:147]\n"
"s_andn2_b32 s56, s55, s58\n"
"v_cvt_pk_bf16_f32 v202, v204, v205\n"
"v_cvt_pk_bf16_f32 v217, v218, v219\n"
"v_mfma_f32_16x16x32_bf16 v[148:151], a[200:203], a[152:155], v[148:151]\n"
"s_or_b32 s56, s56, s41\n"
"ds_read_b64_tr_b16 a[232:233], v245 offset:16384\n"
"v_cvt_pk_bf16_f32 v219, v222, v223\n"
"v_mfma_f32_16x16x32_bf16 v[148:151], a[204:207], a[156:159], v[148:151]\n"
"v_pk_fma_f32 v[112:113], v[112:113], s[70:71], s[70:71] op_sel:[0,0,1] op_sel_hi:[1,0,1]\nv_pk_fma_f32 v[114:115], v[114:115], s[70:71], s[70:71] op_sel:[0,0,1] op_sel_hi:[1,0,1]\nv_pk_fma_f32 v[116:117], v[116:117], s[70:71], s[70:71] op_sel:[0,0,1] op_sel_hi:[1,0,1]\nv_pk_fma_f32 v[118:119], v[118:119], s[70:71], s[70:71] op_sel:[0,0,1] op_sel_hi:[1,0,1]\nv_pk_fma_f32 v[120:121], v[120:121], s[70:71], s[70:71] op_sel:[0,0,1] op_sel_hi:[1,0,1]\nv_pk_fma_f32 v[122:123], v[122:123], s[70:71], s[70:71] op_sel:[0,0,1] op_sel_hi:[1,0,1]\nv_pk_fma_f32 v[124:125], v[124:125], s[70:71], s[70:71] op_sel:[0,0,1] op_sel_hi:[1,0,1]\nv_pk_fma_f32 v[126:127], v[126:127], s[70:71], s[70:71] op_sel:[0,0,1] op_sel_hi:[1,0,1]\nv_pk_fma_f32 v[128:129], v[128:129], s[70:71], s[70:71] op_sel:[0,0,1] op_sel_hi:[1,0,1]\nv_pk_fma_f32 v[130:131], v[130:131], s[70:71], s[70:71] op_sel:[0,0,1] op_sel_hi:[1,0,1]\nv_pk_fma_f32 v[132:133], v[132:133], s[70:71], s[70:71] op_sel:[0,0,1] op_sel_hi:[1,0,1]\nv_pk_fma_f32 v[134:135], v[134:135], s[70:71], s[70:71] op_sel:[0,0,1] op_sel_hi:[1,0,1]\nv_pk_fma_f32 v[136:137], v[136:137], s[70:71], s[70:71] op_sel:[0,0,1] op_sel_hi:[1,0,1]\nv_pk_fma_f32 v[138:139], v[138:139], s[70:71], s[70:71] op_sel:[0,0,1] op_sel_hi:[1,0,1]\nv_pk_fma_f32 v[140:141], v[140:141], s[70:71], s[70:71] op_sel:[0,0,1] op_sel_hi:[1,0,1]\nv_pk_fma_f32 v[142:143], v[142:143], s[70:71], s[70:71] op_sel:[0,0,1] op_sel_hi:[1,0,1]\n"
"v_mfma_f32_16x16x32_bf16 v[152:155], a[208:211], a[152:155], v[152:155]\n"
"v_cvt_pk_bf16_f32 v163, v166, v167\n"
"v_cvt_pk_bf16_f32 v120, v120, v121\n"
"v_mfma_f32_16x16x32_bf16 v[152:155], a[212:215], a[156:159], v[152:155]\n"
"v_cvt_pk_bf16_f32 v208, v208, v209\n"
"v_cvt_pk_bf16_f32 v218, v220, v221\n"
"v_mfma_f32_16x16x32_bf16 v[156:159], a[216:219], a[152:155], v[156:159]\n"
"v_cvt_pk_bf16_f32 v162, v164, v165\n"
"v_cvt_pk_bf16_f32 v170, v172, v173\n"
"v_mfma_f32_16x16x32_bf16 v[156:159], a[220:223], a[156:159], v[156:159]\n"
"v_permlane16_swap_b32 v169, v171\n"
"v_mfma_f32_16x16x32_bf16 v[96:99], a[224:227], a[184:187], v[96:99]\n"
"s_waitcnt lgkmcnt(1)\n"
"s_add_u32 m0, s44, 32768\n"
"v_cvt_pk_bf16_f32 v209, v210, v211\n"
"v_cvt_pk_bf16_f32 v128, v128, v129\n"
"v_mfma_f32_16x16x32_bf16 v[96:99], a[228:231], a[188:191], v[96:99]\n"
"s_waitcnt lgkmcnt(0)\n"
"buffer_load_dwordx4 v240, s[16:19], s56 offen nt lds\n"
"v_mfma_f32_16x16x32_bf16 v[100:103], a[232:235], a[184:187], v[100:103]\n"
"s_waitcnt vmcnt(14)\n"
"v_mov_b32 v205, 0\n"
"v_mfma_f32_16x16x32_bf16 v[100:103], a[236:239], a[188:191], v[100:103]\n"
"s_barrier\n"
"v_mfma_f32_16x16x32_bf16 v[104:107], a[240:243], a[184:187], v[104:107]\n"
"v_permlane16_swap_b32 v160, v162\n"
"v_mfma_f32_16x16x32_bf16 v[104:107], a[244:247], a[188:191], v[104:107]\n"
"s_mov_b32 s74, 0x41400000\n"
"ds_read_b128 a[204:207], v198 offset:2048\n"
"v_xor_b32 v231, 0x2000, v229\n"
"v_mfma_f32_16x16x32_bf16 v[108:111], a[248:251], a[184:187], v[108:111]\n"
"v_permlane16_swap_b32 v168, v170\n"
"v_mfma_f32_16x16x32_bf16 v[108:111], a[252:255], a[188:191], v[108:111]\n"
"ds_write2st64_b64 v255, v[192:193], v[194:195] offset0:100 offset1:101\n"
"v_mfma_f32_16x16x32_bf16 v[92:95], a[248:251], a[176:179], v[92:95]\n"
"v_cvt_pk_bf16_f32 v129, v130, v131\n"
"v_cvt_pk_bf16_f32 v136, v136, v137\n"
"v_mfma_f32_16x16x32_bf16 v[92:95], a[252:255], a[180:183], v[92:95]\n"
"v_pk_fma_f32 v[144:145], v[144:145], s[70:71], s[70:71] op_sel:[0,0,1] op_sel_hi:[1,0,1]\nv_pk_fma_f32 v[146:147], v[146:147], s[70:71], s[70:71] op_sel:[0,0,1] op_sel_hi:[1,0,1]\nv_pk_fma_f32 v[148:149], v[148:149], s[70:71], s[70:71] op_sel:[0,0,1] op_sel_hi:[1,0,1]\nv_pk_fma_f32 v[150:151], v[150:151], s[70:71], s[70:71] op_sel:[0,0,1] op_sel_hi:[1,0,1]\nv_pk_fma_f32 v[152:153], v[152:153], s[70:71], s[70:71] op_sel:[0,0,1] op_sel_hi:[1,0,1]\nv_pk_fma_f32 v[154:155], v[154:155], s[70:71], s[70:71] op_sel:[0,0,1] op_sel_hi:[1,0,1]\nv_pk_fma_f32 v[156:157], v[156:157], s[70:71], s[70:71] op_sel:[0,0,1] op_sel_hi:[1,0,1]\nv_pk_fma_f32 v[158:159], v[158:159], s[70:71], s[70:71] op_sel:[0,0,1] op_sel_hi:[1,0,1]\n"
"v_mfma_f32_16x16x32_bf16 v[88:91], a[240:243], a[176:179], v[88:91]\n"
"ds_read_b128 a[192:195], v243 offset:0\n"
"v_cvt_pk_bf16_f32 v187, v190, v191\n"
"v_mfma_f32_16x16x32_bf16 v[88:91], a[244:247], a[180:183], v[88:91]\n"
"ds_write2st64_b64 v255, v[184:185], v[186:187] offset0:98 offset1:99\n"
"v_cvt_pk_bf16_f32 v178, v180, v181\n"
"v_mfma_f32_16x16x32_bf16 v[84:87], a[232:235], a[176:179], v[84:87]\n"
"v_cvt_pk_bf16_f32 v210, v212, v213\n"
"v_lshl_add_u32 v196, v196, 4, s43\n"
"v_mfma_f32_16x16x32_bf16 v[84:87], a[236:239], a[180:183], v[84:87]\n"
"ds_write2st64_b64 v255, v[216:217], v[218:219] offset0:194 offset1:195\n"
"v_mfma_f32_16x16x32_bf16 v[80:83], a[224:227], a[176:179], v[80:83]\n"
"s_add_u32 s40, s56, s52\n"
"v_permlane16_swap_b32 v161, v163\n"
"v_mfma_f32_16x16x32_bf16 v[80:83], a[228:231], a[180:183], v[80:83]\n"
"buffer_load_dwordx4 v240, s[16:19], s40 offen offset:1024 nt lds\n"
"v_mfma_f32_16x16x32_bf16 v[64:67], a[224:227], a[168:171], v[64:67]\n"
"v_cvt_pk_bf16_f32 v211, v214, v215\n"
"v_cvt_pk_bf16_f32 v137, v138, v139\n"
"v_mfma_f32_16x16x32_bf16 v[64:67], a[228:231], a[172:175], v[64:67]\n"
"v_cvt_pk_bf16_f32 v131, v134, v135\n"
"v_cvt_pk_bf16_f32 v144, v144, v145\n"
"v_mfma_f32_16x16x32_bf16 v[68:71], a[232:235], a[168:171], v[68:71]\n"
"v_xor_b32 v197, 64, v196\n"
"v_cvt_pk_bf16_f32 v130, v132, v133\n"
"v_mfma_f32_16x16x32_bf16 v[68:71], a[236:239], a[172:175], v[68:71]\n"
"v_pk_fma_f32 v[96:97], v[96:97], s[70:71], s[70:71] op_sel:[0,0,1] op_sel_hi:[1,0,1]\nv_pk_fma_f32 v[98:99], v[98:99], s[70:71], s[70:71] op_sel:[0,0,1] op_sel_hi:[1,0,1]\nv_pk_fma_f32 v[100:101], v[100:101], s[70:71], s[70:71] op_sel:[0,0,1] op_sel_hi:[1,0,1]\nv_pk_fma_f32 v[102:103], v[102:103], s[70:71], s[70:71] op_sel:[0,0,1] op_sel_hi:[1,0,1]\nv_pk_fma_f32 v[104:105], v[104:105], s[70:71], s[70:71] op_sel:[0,0,1] op_sel_hi:[1,0,1]\nv_pk_fma_f32 v[106:107], v[106:107], s[70:71], s[70:71] op_sel:[0,0,1] op_sel_hi:[1,0,1]\nv_pk_fma_f32 v[108:109], v[108:109], s[70:71], s[70:71] op_sel:[0,0,1] op_sel_hi:[1,0,1]\nv_pk_fma_f32 v[110:111], v[110:111], s[70:71], s[70:71] op_sel:[0,0,1] op_sel_hi:[1,0,1]\nv_pk_fma_f32 v[80:81], v[80:81], s[70:71], s[70:71] op_sel:[0,0,1] op_sel_hi:[1,0,1]\nv_pk_fma_f32 v[82:83], v[82:83], s[70:71], s[70:71] op_sel:[0,0,1] op_sel_hi:[1,0,1]\nv_pk_fma_f32 v[84:85], v[84:85], s[70:71], s[70:71] op_sel:[0,0,1] op_sel_hi:[1,0,1]\nv_pk_fma_f32 v[86:87], v[86:87], s[70:71], s[70:71] op_sel:[0,0,1] op_sel_hi:[1,0,1]\nv_pk_fma_f32 v[88:89], v[88:89], s[70:71], s[70:71] op_sel:[0,0,1] op_sel_hi:[1,0,1]\nv_pk_fma_f32 v[90:91], v[90:91], s[70:71], s[70:71] op_sel:[0,0,1] op_sel_hi:[1,0,1]\nv_pk_fma_f32 v[92:93], v[92:93], s[70:71], s[70:71] op_sel:[0,0,1] op_sel_hi:[1,0,1]\nv_pk_fma_f32 v[94:95], v[94:95], s[70:71], s[70:71] op_sel:[0,0,1] op_sel_hi:[1,0,1]\n"
"v_mfma_f32_16x16x32_bf16 v[72:75], a[240:243], a[168:171], v[72:75]\n"
"v_cvt_pk_bf16_f32 v152, v152, v153\n"
"v_cvt_pk_bf16_f32 v80, v80, v81\n"
"v_mfma_f32_16x16x32_bf16 v[72:75], a[244:247], a[172:175], v[72:75]\n"
"v_cvt_pk_bf16_f32 v153, v154, v155\n"
"v_cvt_pk_bf16_f32 v81, v82, v83\n"
"v_mfma_f32_16x16x32_bf16 v[76:79], a[248:251], a[168:171], v[76:79]\n"
"v_cvt_pk_bf16_f32 v139, v142, v143\n"
"v_cvt_pk_bf16_f32 v104, v104, v105\n"
"v_mfma_f32_16x16x32_bf16 v[76:79], a[252:255], a[172:175], v[76:79]\n"
"ds_read_b128 v[212:215], v197 offset:0\n"
"v_cvt_pk_bf16_f32 v96, v96, v97\n"
"v_mfma_f32_16x16x32_bf16 v[60:63], a[248:251], a[160:163], v[60:63]\n"
"v_cvt_pk_bf16_f32 v138, v140, v141\n"
"v_cvt_pk_bf16_f32 v145, v146, v147\n"
"v_mfma_f32_16x16x32_bf16 v[60:63], a[252:255], a[164:167], v[60:63]\n"
"ds_write_b128 v243, v[168:171] offset:10240\n"
"v_mfma_f32_16x16x32_bf16 v[56:59], a[240:243], a[160:163], v[56:59]\n"
"s_add_u32 s40, s56, s53\n"
"v_cvt_pk_bf16_f32 v121, v122, v123\n"
"v_cvt_pk_bf16_f32 v155, v158, v159\n"
"v_mfma_f32_16x16x32_bf16 v[56:59], a[244:247], a[164:167], v[56:59]\n"
"buffer_load_dwordx4 v240, s[16:19], s40 offen offset:2048 nt lds\n"
"v_mfma_f32_16x16x32_bf16 v[52:55], a[232:235], a[160:163], v[52:55]\n"
"v_permlane16_swap_b32 v137, v139\n"
"v_mfma_f32_16x16x32_bf16 v[52:55], a[236:239], a[164:167], v[52:55]\n"
"v_permlane16_swap_b32 v128, v130\n"
"v_mfma_f32_16x16x32_bf16 v[48:51], a[224:227], a[160:163], v[48:51]\n"
"v_cvt_pk_bf16_f32 v122, v124, v125\n"
"v_cvt_pk_bf16_f32 v147, v150, v151\n"
"v_mfma_f32_16x16x32_bf16 v[48:51], a[228:231], a[164:167], v[48:51]\n"
"v_cvt_pk_bf16_f32 v83, v86, v87\n"
"v_lshl_add_u32 v248, v249, 9, v248\n"
"v_mfma_f32_16x16x32_bf16 v[0:3], a[224:227], a[136:139], v[0:3]\n"
"ds_read_b128 v[216:219], v196 offset:1024\n"
"v_cvt_pk_bf16_f32 v154, v156, v157\n"
"v_mfma_f32_16x16x32_bf16 v[0:3], a[228:231], a[140:143], v[0:3]\n"
"ds_write_b128 v243, v[160:163] offset:8192\n"
"v_mfma_f32_16x16x32_bf16 v[4:7], a[232:235], a[136:139], v[4:7]\n"
"v_permlane16_swap_b32 v136, v138\n"
"v_mfma_f32_16x16x32_bf16 v[4:7], a[236:239], a[140:143], v[4:7]\n"
"v_permlane16_swap_b32 v129, v131\n"
"v_mfma_f32_16x16x32_bf16 v[8:11], a[240:243], a[136:139], v[8:11]\n"
"ds_read_b128 a[200:203], v243 offset:2048\n"
"v_cvt_pk_bf16_f32 v105, v106, v107\n"
"v_mfma_f32_16x16x32_bf16 v[8:11], a[244:247], a[140:143], v[8:11]\n"
"v_accvgpr_write_b32 a120, v136\n"
"v_cvt_pk_bf16_f32 v146, v148, v149\n"
"v_mfma_f32_16x16x32_bf16 v[12:15], a[248:251], a[136:139], v[12:15]\n"
"v_permlane16_swap_b32 v152, v154\n"
"v_mfma_f32_16x16x32_bf16 v[12:15], a[252:255], a[140:143], v[12:15]\n"
"ds_write2st64_b64 v255, v[208:209], v[210:211] offset0:192 offset1:193\n"
"v_pk_fma_f32 v[64:65], v[64:65], s[70:71], s[70:71] op_sel:[0,0,1] op_sel_hi:[1,0,1]\nv_pk_fma_f32 v[66:67], v[66:67], s[70:71], s[70:71] op_sel:[0,0,1] op_sel_hi:[1,0,1]\nv_pk_fma_f32 v[68:69], v[68:69], s[70:71], s[70:71] op_sel:[0,0,1] op_sel_hi:[1,0,1]\nv_pk_fma_f32 v[70:71], v[70:71], s[70:71], s[70:71] op_sel:[0,0,1] op_sel_hi:[1,0,1]\nv_pk_fma_f32 v[72:73], v[72:73], s[70:71], s[70:71] op_sel:[0,0,1] op_sel_hi:[1,0,1]\nv_pk_fma_f32 v[74:75], v[74:75], s[70:71], s[70:71] op_sel:[0,0,1] op_sel_hi:[1,0,1]\nv_pk_fma_f32 v[76:77], v[76:77], s[70:71], s[70:71] op_sel:[0,0,1] op_sel_hi:[1,0,1]\nv_pk_fma_f32 v[78:79], v[78:79], s[70:71], s[70:71] op_sel:[0,0,1] op_sel_hi:[1,0,1]\nv_pk_fma_f32 v[48:49], v[48:49], s[70:71], s[70:71] op_sel:[0,0,1] op_sel_hi:[1,0,1]\nv_pk_fma_f32 v[50:51], v[50:51], s[70:71], s[70:71] op_sel:[0,0,1] op_sel_hi:[1,0,1]\nv_pk_fma_f32 v[52:53], v[52:53], s[70:71], s[70:71] op_sel:[0,0,1] op_sel_hi:[1,0,1]\nv_pk_fma_f32 v[54:55], v[54:55], s[70:71], s[70:71] op_sel:[0,0,1] op_sel_hi:[1,0,1]\nv_pk_fma_f32 v[56:57], v[56:57], s[70:71], s[70:71] op_sel:[0,0,1] op_sel_hi:[1,0,1]\nv_pk_fma_f32 v[58:59], v[58:59], s[70:71], s[70:71] op_sel:[0,0,1] op_sel_hi:[1,0,1]\nv_pk_fma_f32 v[60:61], v[60:61], s[70:71], s[70:71] op_sel:[0,0,1] op_sel_hi:[1,0,1]\nv_pk_fma_f32 v[62:63], v[62:63], s[70:71], s[70:71] op_sel:[0,0,1] op_sel_hi:[1,0,1]\n"
"v_mfma_f32_16x16x32_bf16 v[28:31], a[248:251], a[144:147], v[28:31]\n"
"s_add_u32 s40, s56, s54\n"
"v_permlane16_swap_b32 v145, v147\n"
"v_mfma_f32_16x16x32_bf16 v[28:31], a[252:255], a[148:151], v[28:31]\n"
"buffer_load_dwordx4 v240, s[16:19], s40 offen offset:3072 nt lds\n"
"v_mfma_f32_16x16x32_bf16 v[24:27], a[240:243], a[144:147], v[24:27]\n"
"v_cvt_pk_bf16_f32 v203, v206, v207\n"
"v_cvt_pk_bf16_f32 v72, v72, v73\n"
"v_mfma_f32_16x16x32_bf16 v[24:27], a[244:247], a[148:151], v[24:27]\n"
"v_cvt_pk_bf16_f32 v97, v98, v99\n"
"v_cvt_pk_bf16_f32 v64, v64, v65\n"
"v_mfma_f32_16x16x32_bf16 v[20:23], a[232:235], a[144:147], v[20:23]\n"
"v_cvt_pk_bf16_f32 v106, v108, v109\n"
"v_cvt_pk_bf16_f32 v56, v56, v57\n"
"v_mfma_f32_16x16x32_bf16 v[20:23], a[236:239], a[148:151], v[20:23]\n"
"v_accvgpr_write_b32 a112, v128\n"
"v_accvgpr_write_b32 a131, v147\n"
"v_mfma_f32_16x16x32_bf16 v[16:19], a[224:227], a[144:147], v[16:19]\n"
"ds_read_b128 v[208:211], v196 offset:0\n"
"v_cvt_pk_bf16_f32 v88, v88, v89\n"
"v_mfma_f32_16x16x32_bf16 v[16:19], a[228:231], a[148:151], v[16:19]\n"
"v_accvgpr_write_b32 a113, v129\n"
"v_cvt_pk_bf16_f32 v82, v84, v85\n"
"v_mfma_f32_16x16x32_bf16 v[32:35], a[224:227], a[152:155], v[32:35]\n"
"v_accvgpr_write_b32 a114, v130\n"
"v_cvt_pk_bf16_f32 v57, v58, v59\n"
"v_mfma_f32_16x16x32_bf16 v[32:35], a[228:231], a[156:159], v[32:35]\n"
"v_cvt_pk_bf16_f32 v73, v74, v75\n"
"v_xor_b32_e64 v249, s42, 0\n"
"v_mfma_f32_16x16x32_bf16 v[36:39], a[232:235], a[152:155], v[36:39]\n"
"ds_read_b128 v[220:223], v197 offset:1024\n"
"v_lshl_add_u32 v248, v249, 12, v248\n"
"v_mfma_f32_16x16x32_bf16 v[36:39], a[236:239], a[156:159], v[36:39]\n"
"v_cvt_pk_bf16_f32 v89, v90, v91\n"
"v_cvt_pk_bf16_f32 v65, v66, v67\n"
"v_mfma_f32_16x16x32_bf16 v[40:43], a[240:243], a[152:155], v[40:43]\n"
"v_permlane16_swap_b32 v120, v122\n"
"v_mfma_f32_16x16x32_bf16 v[40:43], a[244:247], a[156:159], v[40:43]\n"
"ds_write2st64_b64 v255, v[200:201], v[202:203] offset0:102 offset1:103\n"
"v_pk_fma_f32 v[0:1], v[0:1], s[70:71], s[70:71] op_sel:[0,0,1] op_sel_hi:[1,0,1]\nv_pk_fma_f32 v[2:3], v[2:3], s[70:71], s[70:71] op_sel:[0,0,1] op_sel_hi:[1,0,1]\nv_pk_fma_f32 v[4:5], v[4:5], s[70:71], s[70:71] op_sel:[0,0,1] op_sel_hi:[1,0,1]\nv_pk_fma_f32 v[6:7], v[6:7], s[70:71], s[70:71] op_sel:[0,0,1] op_sel_hi:[1,0,1]\nv_pk_fma_f32 v[8:9], v[8:9], s[70:71], s[70:71] op_sel:[0,0,1] op_sel_hi:[1,0,1]\nv_pk_fma_f32 v[10:11], v[10:11], s[70:71], s[70:71] op_sel:[0,0,1] op_sel_hi:[1,0,1]\nv_pk_fma_f32 v[12:13], v[12:13], s[70:71], s[70:71] op_sel:[0,0,1] op_sel_hi:[1,0,1]\nv_pk_fma_f32 v[14:15], v[14:15], s[70:71], s[70:71] op_sel:[0,0,1] op_sel_hi:[1,0,1]\nv_pk_fma_f32 v[16:17], v[16:17], s[70:71], s[70:71] op_sel:[0,0,1] op_sel_hi:[1,0,1]\nv_pk_fma_f32 v[18:19], v[18:19], s[70:71], s[70:71] op_sel:[0,0,1] op_sel_hi:[1,0,1]\nv_pk_fma_f32 v[20:21], v[20:21], s[70:71], s[70:71] op_sel:[0,0,1] op_sel_hi:[1,0,1]\nv_pk_fma_f32 v[22:23], v[22:23], s[70:71], s[70:71] op_sel:[0,0,1] op_sel_hi:[1,0,1]\nv_pk_fma_f32 v[24:25], v[24:25], s[70:71], s[70:71] op_sel:[0,0,1] op_sel_hi:[1,0,1]\nv_pk_fma_f32 v[26:27], v[26:27], s[70:71], s[70:71] op_sel:[0,0,1] op_sel_hi:[1,0,1]\nv_pk_fma_f32 v[28:29], v[28:29], s[70:71], s[70:71] op_sel:[0,0,1] op_sel_hi:[1,0,1]\nv_pk_fma_f32 v[30:31], v[30:31], s[70:71], s[70:71] op_sel:[0,0,1] op_sel_hi:[1,0,1]\n"
"v_mfma_f32_16x16x32_bf16 v[44:47], a[248:251], a[152:155], v[44:47]\n"
"v_accvgpr_write_b32 a138, v154\n"
"v_cvt_pk_bf16_f32 v91, v94, v95\n"
"v_mfma_f32_16x16x32_bf16 v[44:47], a[252:255], a[156:159], v[44:47]\n"
"s_waitcnt lgkmcnt(2)\n"
"v_accvgpr_write_b32 a122, v138\n"
"v_accvgpr_write_b32 a242, v254\n"
"v_mfma_f32_16x16x32_bf16 v[180:183], v[208:211], a[0:3], 0\n"
"v_permlane16_swap_b32 v144, v146\n"
"v_mfma_f32_16x16x32_bf16 v[180:183], v[212:215], a[4:7], v[180:183]\n"
"v_permlane16_swap_b32 v153, v155\n"
"v_mfma_f32_16x16x32_bf16 v[180:183], v[216:219], a[8:11], v[180:183]\n"
"s_waitcnt lgkmcnt(1)\n"
"ds_read_b128 a[196:199], v198 offset:0\n"
"v_cvt_pk_bf16_f32 v90, v92, v93\n"
"v_mfma_f32_16x16x32_bf16 v[180:183], v[220:223], a[12:15], v[180:183]\n"
"ds_write2st64_b64 v255, v[80:81], v[82:83] offset0:224 offset1:225\n"
"v_pk_fma_f32 v[32:33], v[32:33], s[70:71], s[70:71] op_sel:[0,0,1] op_sel_hi:[1,0,1]\nv_pk_fma_f32 v[34:35], v[34:35], s[70:71], s[70:71] op_sel:[0,0,1] op_sel_hi:[1,0,1]\nv_pk_fma_f32 v[36:37], v[36:37], s[70:71], s[70:71] op_sel:[0,0,1] op_sel_hi:[1,0,1]\nv_pk_fma_f32 v[38:39], v[38:39], s[70:71], s[70:71] op_sel:[0,0,1] op_sel_hi:[1,0,1]\nv_pk_fma_f32 v[40:41], v[40:41], s[70:71], s[70:71] op_sel:[0,0,1] op_sel_hi:[1,0,1]\nv_pk_fma_f32 v[42:43], v[42:43], s[70:71], s[70:71] op_sel:[0,0,1] op_sel_hi:[1,0,1]\nv_pk_fma_f32 v[44:45], v[44:45], s[70:71], s[70:71] op_sel:[0,0,1] op_sel_hi:[1,0,1]\nv_pk_fma_f32 v[46:47], v[46:47], s[70:71], s[70:71] op_sel:[0,0,1] op_sel_hi:[1,0,1]\n"
"v_mfma_f32_16x16x32_bf16 v[184:187], v[208:211], a[16:19], 0\n"
"v_accvgpr_write_b32 a139, v155\n"
"v_cvt_pk_bf16_f32 v48, v48, v49\n"
"v_mfma_f32_16x16x32_bf16 v[184:187], v[212:215], a[20:23], v[184:187]\n"
"v_accvgpr_write_b32 a123, v139\n"
"v_cvt_pk_bf16_f32 v49, v50, v51\n"
"v_mfma_f32_16x16x32_bf16 v[184:187], v[216:219], a[24:27], v[184:187]\n"
"v_accvgpr_write_b32 a136, v152\n"
"v_cvt_pk_bf16_f32 v40, v40, v41\n"
"v_mfma_f32_16x16x32_bf16 v[184:187], v[220:223], a[28:31], v[184:187]\n"
"ds_write2st64_b64 v255, v[176:177], v[178:179] offset0:96 offset1:97\n"
"v_sub_f32 v180, s74, v180\n"
"v_sub_f32 v181, s74, v181\n"
"v_mfma_f32_16x16x32_bf16 v[188:191], v[208:211], a[32:35], 0\n"
"v_sub_f32 v182, s74, v182\n"
"v_sub_f32 v183, s74, v183\n"
"v_mfma_f32_16x16x32_bf16 v[188:191], v[212:215], a[36:39], v[188:191]\n"
"v_accvgpr_write_b32 a128, v144\n"
"v_cvt_pk_bf16_f32 v74, v76, v77\n"
"v_mfma_f32_16x16x32_bf16 v[188:191], v[216:219], a[40:43], v[188:191]\n"
"v_cvt_pk_bf16_f32 v107, v110, v111\n"
"v_cvt_pk_bf16_f32 v8, v8, v9\n"
"v_mfma_f32_16x16x32_bf16 v[188:191], v[220:223], a[44:47], v[188:191]\n"
"v_cvt_pk_bf16_f32 v98, v100, v101\n"
"v_mov_b32 v199, 0\n"
"v_mfma_f32_16x16x32_bf16 v[192:195], v[208:211], a[192:195], 0\n"
"s_waitcnt lgkmcnt(1)\n"
"ds_read_b128 v[208:211], v228 offset:0\n"
"v_accvgpr_write_b32 a137, v153\n"
"v_mfma_f32_16x16x32_bf16 v[192:195], v[212:215], a[196:199], v[192:195]\n"
"ds_read_b128 v[212:215], v229 offset:0\n"
"v_sub_f32 v185, s74, v185\n"
"v_mfma_f32_16x16x32_bf16 v[192:195], v[216:219], a[200:203], v[192:195]\n"
"ds_read_b128 v[216:219], v228 offset:1024\n"
"v_sub_f32 v190, s74, v190\n"
"v_mfma_f32_16x16x32_bf16 v[192:195], v[220:223], a[204:207], v[192:195]\n"
"s_waitcnt lgkmcnt(2)\n"
"ds_read_b128 v[220:223], v229 offset:1024\n"
"v_mov_b32 v204, 0\n"
"v_mfma_f32_16x16x32_bf16 v[152:155], v[208:211], a[0:3], v[180:183]\n"
"s_waitcnt lgkmcnt(2)\n"
"v_sub_f32 v184, s74, v184\n"
"v_sub_f32 v188, s74, v188\n"
"v_mfma_f32_16x16x32_bf16 v[152:155], v[212:215], a[4:7], v[152:155]\n"
"s_waitcnt lgkmcnt(1)\n"
"v_sub_f32 v186, s74, v186\n"
"v_sub_f32 v187, s74, v187\n"
"v_mfma_f32_16x16x32_bf16 v[152:155], v[216:219], a[8:11], v[152:155]\n"
"s_waitcnt lgkmcnt(0)\n"
"v_cvt_pk_bf16_f32 v99, v102, v103\n"
"v_cvt_pk_bf16_f32 v9, v10, v11\n"
"v_mfma_f32_16x16x32_bf16 v[152:155], v[220:223], a[12:15], v[152:155]\n"
"ds_write2st64_b64 v255, v[88:89], v[90:91] offset0:226 offset1:227\n"
"v_accvgpr_write_b32 a121, v137\n"
"v_cvt_pk_bf16_f32 v58, v60, v61\n"
"v_sub_f32 v191, s74, v191\n"
"v_mfma_f32_16x16x32_bf16 v[156:159], v[216:219], a[24:27], v[184:187]\n"
"v_cvt_pk_bf16_f32 v24, v24, v25\n"
"v_sub_f32 v193, s74, v193\n"
"v_mfma_f32_16x16x32_bf16 v[156:159], v[220:223], a[28:31], v[156:159]\n"
"v_sub_f32 v189, s74, v189\n"
"v_sub_f32 v194, s74, v194\n"
"v_mfma_f32_16x16x32_bf16 v[156:159], v[208:211], a[16:19], v[156:159]\n"
"v_cvt_pk_bf16_f32 v50, v52, v53\n"
"v_sub_f32 v192, s74, v192\n"
"v_mfma_f32_16x16x32_bf16 v[156:159], v[212:215], a[20:23], v[156:159]\n"
"v_exp_f32 v154, v154\n"
"v_mfma_f32_16x16x32_bf16 v[160:163], v[208:211], a[32:35], v[188:191]\n"
"v_cvt_pk_bf16_f32 v11, v14, v15\n"
"v_sub_f32 v195, s74, v195\n"
"v_mfma_f32_16x16x32_bf16 v[160:163], v[212:215], a[36:39], v[160:163]\n"
"v_exp_f32 v152, v152\n"
"v_mfma_f32_16x16x32_bf16 v[160:163], v[216:219], a[40:43], v[160:163]\n"
"s_nop 0\n"
"v_exp_f32 v158, v158\n"
"v_mfma_f32_16x16x32_bf16 v[160:163], v[220:223], a[44:47], v[160:163]\n"
"v_accvgpr_write_b32 a129, v145\n"
"v_cvt_pk_bf16_f32 v41, v42, v43\n"
"v_mfma_f32_16x16x32_bf16 v[164:167], v[216:219], a[200:203], v[192:195]\n"
"ds_read_b128 v[216:219], v228 offset:5120\n"
"v_cvt_pk_bf16_f32 v25, v26, v27\n"
"v_mfma_f32_16x16x32_bf16 v[164:167], v[220:223], a[204:207], v[164:167]\n"
"ds_read_b128 v[220:223], v229 offset:5120\n"
"v_cvt_pk_bf16_f32 v66, v68, v69\n"
"v_mfma_f32_16x16x32_bf16 v[164:167], v[208:211], a[192:195], v[164:167]\n"
"ds_read_b128 v[208:211], v228 offset:4096\n"
"v_cvt_pk_bf16_f32 v123, v126, v127\n"
"v_mfma_f32_16x16x32_bf16 v[164:167], v[212:215], a[196:199], v[164:167]\n"
"s_waitcnt lgkmcnt(2)\n"
"ds_read_b128 v[212:215], v229 offset:4096\n"
"v_cvt_pk_bf16_f32 v59, v62, v63\n"
"v_mfma_f32_16x16x32_bf16 v[168:171], v[216:219], a[8:11], v[180:183]\n"
"s_waitcnt lgkmcnt(2)\n"
"v_exp_f32 v155, v155\n"
"v_mfma_f32_16x16x32_bf16 v[168:171], v[220:223], a[12:15], v[168:171]\n"
"s_waitcnt lgkmcnt(1)\n"
"v_exp_f32 v157, v157\n"
"v_mfma_f32_16x16x32_bf16 v[168:171], v[208:211], a[0:3], v[168:171]\n"
"s_waitcnt lgkmcnt(0)\n"
"v_permlane16_swap_b32 v57, v59\n"
"v_mfma_f32_16x16x32_bf16 v[168:171], v[212:215], a[4:7], v[168:171]\n"
"ds_write2st64_b64 v255, v[96:97], v[98:99] offset0:228 offset1:229\n"
"v_cvt_pk_bf16_f32 v51, v54, v55\n"
"v_exp_f32 v160, v160\n"
"v_exp_f32 v161, v161\n"
"v_mfma_f32_16x16x32_bf16 v[172:175], v[208:211], a[16:19], v[184:187]\n"
"v_exp_f32 v159, v159\n"
"v_mfma_f32_16x16x32_bf16 v[172:175], v[212:215], a[20:23], v[172:175]\n"
"v_exp_f32 v156, v156\n"
"v_mfma_f32_16x16x32_bf16 v[172:175], v[216:219], a[24:27], v[172:175]\n"
"v_exp_f32 v153, v153\n"
"v_mfma_f32_16x16x32_bf16 v[172:175], v[220:223], a[28:31], v[172:175]\n"
"ds_write2st64_b64 v255, v[104:105], v[106:107] offset0:230 offset1:231\n"
"v_cvt_pk_bf16_f32 v75, v78, v79\n"
"v_accvgpr_write_b32 a240, v254\n"
"v_accvgpr_write_b32 a243, v254\n"
"v_cvt_pk_bf16_f32 v136, v156, v157\n"
"v_cvt_pk_bf16_f32 v144, v160, v161\n"
"v_exp_f32 v162, v162\n"
"v_exp_f32 v164, v164\n"
"v_exp_f32 v167, v167\n"
"v_mfma_f32_16x16x32_bf16 v[176:179], v[216:219], a[40:43], v[188:191]\n"
"v_exp_f32 v169, v169\n"
"v_mfma_f32_16x16x32_bf16 v[176:179], v[220:223], a[44:47], v[176:179]\n"
"v_permlane16_swap_b32 v48, v50\n"
"v_mfma_f32_16x16x32_bf16 v[176:179], v[208:211], a[32:35], v[176:179]\n"
"v_cvt_pk_bf16_f32 v128, v152, v153\n"
"v_cvt_pk_bf16_f32 v129, v154, v155\n"
"v_mfma_f32_16x16x32_bf16 v[176:179], v[212:215], a[36:39], v[176:179]\n"
"v_accvgpr_write_b32 a115, v131\n"
"v_exp_f32 v163, v163\n"
"v_exp_f32 v165, v165\n"
"v_exp_f32 v166, v166\n"
"v_exp_f32 v168, v168\n"
"v_exp_f32 v170, v170\n"
"v_exp_f32 v173, v173\n"
"v_add_u32 v249, s75, v248\n"
"v_mfma_f32_16x16x32_bf16 v[152:155], v[208:211], a[192:195], v[192:195]\n"
"ds_read_b128 v[208:211], v230 offset:0\n"
"v_cvt_pk_bf16_f32 v67, v70, v71\n"
"v_mfma_f32_16x16x32_bf16 v[152:155], v[212:215], a[196:199], v[152:155]\n"
"ds_read_b128 v[212:215], v231 offset:0\n"
"v_cvt_pk_bf16_f32 v145, v162, v163\n"
"v_mfma_f32_16x16x32_bf16 v[152:155], v[216:219], a[200:203], v[152:155]\n"
"ds_read_b128 v[216:219], v230 offset:1024\n"
"v_mov_b32 v196, 0\n"
"v_mfma_f32_16x16x32_bf16 v[152:155], v[220:223], a[204:207], v[152:155]\n"
"s_waitcnt lgkmcnt(2)\n"
"ds_read_b128 v[220:223], v231 offset:1024\n"
"v_cvt_pk_bf16_f32 v137, v158, v159\n"
"v_mfma_f32_16x16x32_bf16 v[156:159], v[208:211], a[0:3], v[180:183]\n"
"s_waitcnt lgkmcnt(2)\n"
"v_permlane16_swap_b32 v49, v51\n"
"v_mfma_f32_16x16x32_bf16 v[156:159], v[212:215], a[4:7], v[156:159]\n"
"s_waitcnt lgkmcnt(1)\n"
"v_exp_f32 v178, v178\n"
"v_mfma_f32_16x16x32_bf16 v[156:159], v[216:219], a[8:11], v[156:159]\n"
"s_waitcnt lgkmcnt(0)\n"
"v_exp_f32 v174, v174\n"
"v_mfma_f32_16x16x32_bf16 v[156:159], v[220:223], a[12:15], v[156:159]\n"
"ds_write2st64_b64 v255, v[72:73], v[74:75] offset0:198 offset1:199\n"
"v_cvt_pk_bf16_f32 v112, v112, v113\n"
"v_permlane16_swap_b32 v56, v58\n"
"v_permlane16_swap_b32 v9, v11\n"
"v_cvt_pk_bf16_f32 v27, v30, v31\n"
"v_cvt_pk_bf16_f32 v125, v166, v167\n"
"v_cvt_pk_bf16_f32 v130, v168, v169\n"
"v_exp_f32 v171, v171\n"
"v_exp_f32 v172, v172\n"
"v_mfma_f32_16x16x32_bf16 v[160:163], v[216:219], a[24:27], v[184:187]\n"
"v_exp_f32 v179, v179\n"
"v_mfma_f32_16x16x32_bf16 v[160:163], v[220:223], a[28:31], v[160:163]\n"
"v_exp_f32 v154, v154\n"
"v_mfma_f32_16x16x32_bf16 v[160:163], v[208:211], a[16:19], v[160:163]\n"
"v_cvt_pk_bf16_f32 v124, v164, v165\n"
"v_cvt_pk_bf16_f32 v138, v172, v173\n"
"v_mfma_f32_16x16x32_bf16 v[160:163], v[212:215], a[20:23], v[160:163]\n"
"ds_write2st64_b64 v255, v[64:65], v[66:67] offset0:196 offset1:197\n"
"v_accvgpr_write_b32 a241, v254\n"
"v_cvt_pk_bf16_f32 v131, v170, v171\n"
"v_exp_f32 v176, v176\n"
"v_exp_f32 v157, v157\n"
"v_mfma_f32_16x16x32_bf16 v[164:167], v[208:211], a[32:35], v[188:191]\n"
"ds_read_b128 v[208:211], v230 offset:4096\n"
"v_cvt_pk_bf16_f32 v43, v46, v47\n"
"v_mfma_f32_16x16x32_bf16 v[164:167], v[212:215], a[36:39], v[164:167]\n"
"ds_read_b128 v[212:215], v231 offset:4096\n"
"v_cvt_pk_bf16_f32 v113, v114, v115\n"
"v_mfma_f32_16x16x32_bf16 v[164:167], v[216:219], a[40:43], v[164:167]\n"
"ds_read_b128 v[216:219], v230 offset:5120\n"
"v_cvt_pk_bf16_f32 v32, v32, v33\n"
"v_mfma_f32_16x16x32_bf16 v[164:167], v[220:223], a[44:47], v[164:167]\n"
"s_waitcnt lgkmcnt(2)\n"
"ds_read_b128 v[220:223], v231 offset:5120\n"
"v_accvgpr_write_b32 a130, v146\n"
"v_permlane16_swap_b32 v41, v43\n"
"v_exp_f32 v175, v175\n"
"v_exp_f32 v177, v177\n"
"v_exp_f32 v162, v162\n"
"v_mfma_f32_16x16x32_bf16 v[168:171], v[208:211], a[0:3], v[180:183]\n"
"s_waitcnt lgkmcnt(2)\n"
"v_cvt_pk_bf16_f32 v10, v12, v13\n"
"v_cvt_pk_bf16_f32 v139, v174, v175\n"
"v_mfma_f32_16x16x32_bf16 v[168:171], v[212:215], a[4:7], v[168:171]\n"
"s_waitcnt lgkmcnt(1)\n"
"v_exp_f32 v158, v158\n"
"v_mfma_f32_16x16x32_bf16 v[168:171], v[216:219], a[8:11], v[168:171]\n"
"s_waitcnt lgkmcnt(0)\n"
"v_cvt_pk_bf16_f32 v26, v28, v29\n"
"v_cvt_pk_bf16_f32 v147, v178, v179\n"
"v_mfma_f32_16x16x32_bf16 v[168:171], v[220:223], a[12:15], v[168:171]\n"
"ds_write_b128 v243, v[48:51] offset:12288\n"
"v_accvgpr_write_b32 a155, v11\n"
"v_accvgpr_write_b32 a187, v43\n"
"v_exp_f32 v153, v153\n"
"v_exp_f32 v156, v156\n"
"v_mfma_f32_16x16x32_bf16 v[172:175], v[216:219], a[24:27], v[184:187]\n"
"v_exp_f32 v155, v155\n"
"v_mfma_f32_16x16x32_bf16 v[172:175], v[220:223], a[28:31], v[172:175]\n"
"v_exp_f32 v160, v160\n"
"v_mfma_f32_16x16x32_bf16 v[172:175], v[208:211], a[16:19], v[172:175]\n"
"v_cvt_pk_bf16_f32 v42, v44, v45\n"
"v_cvt_pk_bf16_f32 v146, v176, v177\n"
"v_mfma_f32_16x16x32_bf16 v[172:175], v[212:215], a[20:23], v[172:175]\n"
"s_cbranch_vccnz LPCE0_%=\nLPKE0_%=:\n"
"ds_write_b128 v243, v[56:59] offset:14336\n"
"v_permlane16_swap_b32 v24, v26\n"
"v_cvt_pk_bf16_f32 v127, v154, v155\n"
"v_exp_f32 v163, v163\n"
"v_exp_f32 v164, v164\n"
"v_exp_f32 v165, v165\n"
"v_exp_f32 v166, v166\n"
"v_exp_f32 v167, v167\n"
"v_mfma_f32_16x16x32_bf16 v[176:179], v[208:211], a[32:35], v[188:191]\n"
"v_exp_f32 v152, v152\n"
"v_mfma_f32_16x16x32_bf16 v[176:179], v[212:215], a[36:39], v[176:179]\n"
"v_cvt_pk_bf16_f32 v0, v0, v1\n"
"v_cvt_pk_bf16_f32 v132, v156, v157\n"
"v_mfma_f32_16x16x32_bf16 v[176:179], v[216:219], a[40:43], v[176:179]\n"
"v_accvgpr_write_b32 a153, v9\n"
"v_accvgpr_write_b32 a170, v26\n"
"v_mfma_f32_16x16x32_bf16 v[176:179], v[220:223], a[44:47], v[176:179]\n"
"v_cvt_pk_bf16_f32 v1, v2, v3\n"
"v_cvt_pk_bf16_f32 v16, v16, v17\n"
"v_cvt_pk_bf16_f32 v126, v152, v153\n"
"v_exp_f32 v159, v159\n"
"v_exp_f32 v168, v168\n"
"v_exp_f32 v169, v169\n"
"v_exp_f32 v170, v170\n"
"v_exp_f32 v173, v173\n"
"s_waitcnt vmcnt(8) lgkmcnt(0)\n"
"s_barrier\n"
"ds_read_b128 v[208:211], v228 offset:16384\n"
"ds_read_b128 v[212:215], v229 offset:16384\n"
"ds_read_b128 v[216:219], v228 offset:17408\n"
"ds_read_b128 v[220:223], v229 offset:17408\n"
"s_waitcnt lgkmcnt(3)\n"
"v_mfma_f32_16x16x32_bf16 v[152:155], v[208:211], a[0:3], v[180:183]\n"
"v_exp_f32 v177, v177\n"
"s_waitcnt lgkmcnt(2)\n"
"v_mfma_f32_16x16x32_bf16 v[152:155], v[212:215], a[4:7], v[152:155]\n"
"ds_read_b128 a[140:143], v249 offset:33792\n"
"v_cvt_pk_bf16_f32 v141, v162, v163\n"
"s_waitcnt lgkmcnt(2)\n"
"v_mfma_f32_16x16x32_bf16 v[152:155], v[216:219], a[8:11], v[152:155]\n"
"s_waitcnt lgkmcnt(1)\n"
"v_exp_f32 v175, v175\n"
"v_mfma_f32_16x16x32_bf16 v[152:155], v[220:223], a[12:15], v[152:155]\n"
"ds_read_b64_tr_b16 a[246:247], v234 offset:4096\n"
"ds_read_b64_tr_b16 a[250:251], v238 offset:4096\n"
"v_permlane16_swap_b32 v8, v10\n"
"v_cvt_pk_bf16_f32 v33, v34, v35\n"
"v_cvt_pk_bf16_f32 v34, v36, v37\n"
"v_cvt_pk_bf16_f32 v133, v158, v159\n"
"v_exp_f32 v161, v161\n"
"v_exp_f32 v176, v176\n"
"v_mfma_f32_16x16x32_bf16 v[156:159], v[216:219], a[24:27], v[184:187]\n"
"ds_read_b128 a[148:151], v249 offset:34816\n"
"v_mov_b32 v203, 0\n"
"v_mfma_f32_16x16x32_bf16 v[156:159], v[220:223], a[28:31], v[156:159]\n"
"ds_read_b64_tr_b16 a[244:245], v234 offset:0\n"
"v_accvgpr_write_b32 a154, v10\n"
"v_mfma_f32_16x16x32_bf16 v[156:159], v[208:211], a[16:19], v[156:159]\n"
"ds_read_b64_tr_b16 a[248:249], v238 offset:0\n"
"ds_read_b128 a[172:175], v249 offset:50176\n"
"v_mfma_f32_16x16x32_bf16 v[156:159], v[212:215], a[20:23], v[156:159]\n"
"ds_read_b64_tr_b16 v[226:227], v239 offset:4096\n"
"v_cvt_pk_bf16_f32 v115, v118, v119\n"
"v_permlane16_swap_b32 v40, v42\n"
"v_cvt_pk_bf16_f32 v140, v160, v161\n"
"v_cvt_pk_bf16_f32 v148, v164, v165\n"
"v_cvt_pk_bf16_f32 v149, v166, v167\n"
"v_exp_f32 v171, v171\n"
"v_exp_f32 v172, v172\n"
"v_exp_f32 v174, v174\n"
"v_exp_f32 v178, v178\n"
"v_exp_f32 v152, v152\n"
"v_mfma_f32_16x16x32_bf16 v[160:163], v[208:211], a[32:35], v[188:191]\n"
"ds_read_b128 a[132:135], v249 offset:32768\n"
"v_cvt_pk_bf16_f32 v114, v116, v117\n"
"v_mfma_f32_16x16x32_bf16 v[160:163], v[212:215], a[36:39], v[160:163]\n"
"ds_read_b128 a[100:103], v248 offset:49152\n"
"v_cvt_pk_bf16_f32 v143, v174, v175\n"
"v_mfma_f32_16x16x32_bf16 v[160:163], v[216:219], a[40:43], v[160:163]\n"
"ds_read_b128 a[116:119], v248 offset:51200\n"
"v_cvt_pk_bf16_f32 v150, v176, v177\n"
"v_mfma_f32_16x16x32_bf16 v[160:163], v[220:223], a[44:47], v[160:163]\n"
"v_mov_b32 v201, 0\n"
"v_cvt_pk_bf16_f32 v134, v168, v169\n"
"v_mfma_f32_16x16x32_bf16 v[164:167], v[216:219], a[200:203], v[192:195]\n"
"ds_read_b64_tr_b16 a[254:255], v235 offset:4096\n"
"v_accvgpr_write_b32 a185, v41\n"
"v_mfma_f32_16x16x32_bf16 v[164:167], v[220:223], a[204:207], v[164:167]\n"
"ds_read_b64_tr_b16 a[252:253], v235 offset:0\n"
"ds_read_b128 a[164:167], v249 offset:49152\n"
"v_mfma_f32_16x16x32_bf16 v[164:167], v[208:211], a[192:195], v[164:167]\n"
"ds_read_b64_tr_b16 v[224:225], v239 offset:0\n"
"v_accvgpr_write_b32 a186, v42\n"
"v_mfma_f32_16x16x32_bf16 v[164:167], v[212:215], a[196:199], v[164:167]\n"
"s_waitcnt lgkmcnt(7)\n"
"ds_read_b128 v[220:223], v229 offset:21504\n"
"v_cvt_pk_bf16_f32 v3, v6, v7\n"
"v_mov_b32 v197, 0\n"
"v_cvt_pk_bf16_f32 v135, v170, v171\n"
"v_exp_f32 v179, v179\n"
"v_exp_f32 v154, v154\n"
"v_exp_f32 v156, v156\n"
"v_exp_f32 v160, v160\n"
"v_mfma_f32_16x16x32_bf16 a[216:219], v[124:127], a[244:247], 0\n"
"ds_read_b128 a[156:159], v249 offset:35840\n"
"v_accvgpr_write_b32 a152, v8\n"
"v_mfma_f32_16x16x32_bf16 v[8:11], v[128:131], a[244:247], 0\n"
"ds_read_b128 a[108:111], v248 offset:50176\n"
"v_accvgpr_write_b32 a184, v40\n"
"v_mfma_f32_16x16x32_bf16 v[8:11], v[132:135], a[248:251], v[8:11]\n"
"ds_read_b128 a[180:183], v249 offset:51200\n"
"v_cvt_pk_bf16_f32 v142, v172, v173\n"
"v_mfma_f32_16x16x32_bf16 v[40:43], v[136:139], a[244:247], 0\n"
"ds_read_b128 a[124:127], v248 offset:52224\n"
"v_bfe_u32 v248, v242, 8, 2\n"
"v_mfma_f32_16x16x32_bf16 v[40:43], v[140:143], a[248:251], v[40:43]\n"
"ds_read_b128 a[188:191], v249 offset:52224\n"
"v_cvt_pk_bf16_f32 v151, v178, v179\n"
"v_mfma_f32_16x16x32_bf16 v[72:75], v[144:147], a[244:247], 0\n"
"v_permlane16_swap_b32 v113, v115\n"
"v_mfma_f32_16x16x32_bf16 v[72:75], v[148:151], a[248:251], v[72:75]\n"
"s_waitcnt lgkmcnt(6)\n"
"ds_read_b128 v[208:211], v228 offset:20480\n"
"ds_read_b64_tr_b16 a[250:251], v238 offset:5120\n"
"v_accvgpr_write_b32 a97, v113\n"
"v_mov_b32 v202, 0\n"
"v_exp_f32 v153, v153\n"
"v_exp_f32 v155, v155\n"
"v_exp_f32 v162, v162\n"
"v_exp_f32 v166, v166\n"
"v_mfma_f32_16x16x32_bf16 v[76:79], v[148:151], v[224:227], 0\n"
"v_cvt_pk_bf16_f32 v17, v18, v19\n"
"v_mov_b32 v198, 0\n"
"v_mfma_f32_16x16x32_bf16 v[76:79], v[144:147], a[252:255], v[76:79]\n"
"ds_read_b128 v[216:219], v228 offset:21504\n"
"v_accvgpr_write_b32 a104, v120\n"
"v_mfma_f32_16x16x32_bf16 v[44:47], v[140:143], v[224:227], 0\n"
"v_cvt_pk_bf16_f32 v2, v4, v5\n"
"v_cvt_pk_bf16_f32 v35, v38, v39\n"
"v_mfma_f32_16x16x32_bf16 v[44:47], v[136:139], a[252:255], v[44:47]\n"
"ds_read_b128 v[212:215], v229 offset:20480\n"
"v_permlane16_swap_b32 v112, v114\n"
"v_permlane16_swap_b32 v1, v3\n"
"v_permlane16_swap_b32 v33, v35\n"
"v_exp_f32 v157, v157\n"
"v_exp_f32 v158, v158\n"
"v_mfma_f32_16x16x32_bf16 v[12:15], v[132:135], v[224:227], 0\n"
"ds_read_b64_tr_b16 a[244:245], v234 offset:1024\n"
"v_cvt_pk_bf16_f32 v96, v152, v153\n"
"v_mfma_f32_16x16x32_bf16 v[12:15], v[128:131], a[252:255], v[12:15]\n"
"v_exp_f32 v159, v159\n"
"v_mfma_f32_16x16x32_bf16 a[220:223], v[124:127], a[252:255], 0\n"
"s_waitcnt lgkmcnt(5)\n"
"v_exp_f32 v165, v165\n"
"v_smfmac_f32_16x16x64_bf16 v[196:199], a[240:243], v[128:135], v241\n"
"s_add_u32 m0, s44, 98304\n"
"ds_read_b64_tr_b16 a[246:247], v234 offset:5120\n"
"ds_read_b64_tr_b16 a[248:249], v238 offset:1024\n"
"v_mfma_f32_16x16x32_bf16 v[202:205], a[240:243], v[124:127], v[202:205]\n"
"s_waitcnt lgkmcnt(4)\n"
"buffer_load_dwordx4 v240, s[20:23], s56 offen nt lds\n"
"v_mfma_f32_16x16x32_bf16 v[168:171], v[216:219], a[8:11], v[180:183]\n"
"v_permlane16_swap_b32 v25, v27\n"
"v_mfma_f32_16x16x32_bf16 v[168:171], v[220:223], a[12:15], v[168:171]\n"
"v_accvgpr_write_b32 a99, v115\n"
"v_cvt_pk_bf16_f32 v97, v154, v155\n"
"v_mfma_f32_16x16x32_bf16 v[168:171], v[208:211], a[0:3], v[168:171]\n"
"s_waitcnt lgkmcnt(3)\n"
"v_exp_f32 v163, v163\n"
"v_mfma_f32_16x16x32_bf16 v[168:171], v[212:215], a[4:7], v[168:171]\n"
"v_cvt_pk_bf16_f32 v18, v20, v21\n"
"v_accvgpr_write_b32 a171, v27\n"
"v_mfma_f32_16x16x32_bf16 v[172:175], v[208:211], a[16:19], v[184:187]\n"
"ds_read_b64_tr_b16 a[254:255], v235 offset:5120\n"
"v_cvt_pk_bf16_f32 v113, v162, v163\n"
"v_mfma_f32_16x16x32_bf16 v[172:175], v[212:215], a[20:23], v[172:175]\n"
"ds_read_b64_tr_b16 v[224:225], v239 offset:1024\n"
"v_cvt_pk_bf16_f32 v19, v22, v23\n"
"v_mfma_f32_16x16x32_bf16 v[172:175], v[216:219], a[24:27], v[172:175]\n"
"s_add_u32 s40, s56, s52\n"
"v_exp_f32 v167, v167\n"
"v_mfma_f32_16x16x32_bf16 v[172:175], v[220:223], a[28:31], v[172:175]\n"
"buffer_load_dwordx4 v240, s[20:23], s40 offen offset:1024 nt lds\n"
"v_mfma_f32_16x16x32_bf16 v[176:179], v[216:219], a[40:43], v[188:191]\n"
"v_accvgpr_write_b32 a145, v1\n"
"v_accvgpr_write_b32 a168, v24\n"
"v_mfma_f32_16x16x32_bf16 v[176:179], v[220:223], a[44:47], v[176:179]\n"
"v_exp_f32 v171, v171\n"
"v_mfma_f32_16x16x32_bf16 v[176:179], v[208:211], a[32:35], v[176:179]\n"
"v_exp_f32 v169, v169\n"
"v_mfma_f32_16x16x32_bf16 v[176:179], v[212:215], a[36:39], v[176:179]\n"
"v_exp_f32 v170, v170\n"
"v_mfma_f32_16x16x32_bf16 v[152:155], v[208:211], a[192:195], v[192:195]\n"
"ds_read_b64_tr_b16 a[252:253], v235 offset:1024\n"
"ds_read_b64_tr_b16 v[226:227], v239 offset:5120\n"
"v_mfma_f32_16x16x32_bf16 v[152:155], v[212:215], a[196:199], v[152:155]\n"
"v_mov_b32 v200, 0\n"
"v_cvt_pk_bf16_f32 v104, v156, v157\n"
"v_mfma_f32_16x16x32_bf16 v[152:155], v[216:219], a[200:203], v[152:155]\n"
"ds_read_b128 v[208:211], v230 offset:16384\n"
"v_cvt_pk_bf16_f32 v105, v158, v159\n"
"v_mfma_f32_16x16x32_bf16 v[152:155], v[220:223], a[204:207], v[152:155]\n"
"s_waitcnt lgkmcnt(3)\n"
"v_permlane16_swap_b32 v0, v2\n"
"v_mfma_f32_16x16x32_bf16 a[232:235], v[124:127], a[244:247], 0\n"
"ds_read_b128 v[212:215], v231 offset:16384\n"
"v_accvgpr_write_b32 a169, v25\n"
"v_mfma_f32_16x16x32_bf16 v[24:27], v[128:131], a[244:247], 0\n"
"s_add_u32 s40, s56, s53\n"
"ds_read_b128 v[220:223], v231 offset:17408\n"
"v_accvgpr_write_b32 a146, v2\n"
"v_mfma_f32_16x16x32_bf16 v[24:27], v[132:135], a[248:251], v[24:27]\n"
"buffer_load_dwordx4 v240, s[20:23], s40 offen offset:2048 nt lds\n"
"v_mfma_f32_16x16x32_bf16 v[56:59], v[136:139], a[244:247], 0\n"
"v_exp_f32 v164, v164\n"
"v_mfma_f32_16x16x32_bf16 v[56:59], v[140:143], a[248:251], v[56:59]\n"
"v_accvgpr_write_b32 a96, v112\n"
"v_accvgpr_write_b32 a179, v35\n"
"v_mfma_f32_16x16x32_bf16 v[88:91], v[144:147], a[244:247], 0\n"
"v_exp_f32 v179, v179\n"
"v_mfma_f32_16x16x32_bf16 v[88:91], v[148:151], a[248:251], v[88:91]\n"
"s_waitcnt lgkmcnt(2)\n"
"v_permlane16_swap_b32 v16, v18\n"
"v_mfma_f32_16x16x32_bf16 v[92:95], v[148:151], v[224:227], 0\n"
"s_add_u32 s40, s56, s54\n"
"ds_read_b128 v[216:219], v230 offset:17408\n"
"v_cvt_pk_bf16_f32 v99, v170, v171\n"
"v_mfma_f32_16x16x32_bf16 v[92:95], v[144:147], a[252:255], v[92:95]\n"
"buffer_load_dwordx4 v240, s[20:23], s40 offen offset:3072 nt lds\n"
"v_mfma_f32_16x16x32_bf16 v[60:63], v[140:143], v[224:227], 0\n"
"s_add_u32 s55, s55, s59\n"
"v_exp_f32 v161, v161\n"
"v_mfma_f32_16x16x32_bf16 v[60:63], v[136:139], a[252:255], v[60:63]\n"
"s_add_u32 s41, s55, s57\n"
"v_permlane16_swap_b32 v121, v123\n"
"v_mfma_f32_16x16x32_bf16 v[28:31], v[132:135], v[224:227], 0\n"
"s_and_b32 s41, s41, s58\n"
"v_permlane16_swap_b32 v32, v34\n"
"v_mfma_f32_16x16x32_bf16 v[28:31], v[128:131], a[252:255], v[28:31]\n"
"s_andn2_b32 s56, s55, s58\n"
"v_exp_f32 v176, v176\n"
"v_mfma_f32_16x16x32_bf16 a[236:239], v[124:127], a[252:255], 0\n"
"s_or_b32 s56, s56, s41\n"
"ds_read_b64_tr_b16 a[250:251], v236 offset:4096\n"
"ds_read_b64_tr_b16 a[252:253], v233 offset:0\n"
"v_smfmac_f32_16x16x64_bf16 v[198:201], a[240:243], v[136:143], v241\n"
"ds_read_b64_tr_b16 a[254:255], v233 offset:4096\n"
"v_accvgpr_write_b32 a176, v32\n"
"v_mfma_f32_16x16x32_bf16 v[156:159], v[208:211], a[0:3], v[180:183]\n"
"s_waitcnt lgkmcnt(3)\n"
"ds_read_b64_tr_b16 a[244:245], v232 offset:0\n"
"ds_read_b64_tr_b16 a[248:249], v236 offset:0\n"
"v_mfma_f32_16x16x32_bf16 v[156:159], v[212:215], a[4:7], v[156:159]\n"
"v_cvt_pk_bf16_f32 v112, v160, v161\n"
"v_cvt_pk_bf16_f32 v120, v164, v165\n"
"v_mfma_f32_16x16x32_bf16 v[156:159], v[216:219], a[8:11], v[156:159]\n"
"s_add_u32 m0, s44, 49152\n"
"ds_read_b64_tr_b16 a[246:247], v232 offset:4096\n"
"ds_read_b64_tr_b16 v[224:225], v237 offset:0\n"
"v_mfma_f32_16x16x32_bf16 v[156:159], v[220:223], a[12:15], v[156:159]\n"
"buffer_load_dwordx4 v240, s[16:19], s56 offen nt lds\n"
"v_mfma_f32_16x16x32_bf16 v[160:163], v[216:219], a[24:27], v[184:187]\n"
"v_accvgpr_write_b32 a105, v121\n"
"v_accvgpr_write_b32 a144, v0\n"
"v_mfma_f32_16x16x32_bf16 v[160:163], v[220:223], a[28:31], v[160:163]\n"
"v_accvgpr_write_b32 a178, v34\n"
"v_cvt_pk_bf16_f32 v121, v166, v167\n"
"v_mfma_f32_16x16x32_bf16 v[160:163], v[208:211], a[16:19], v[160:163]\n"
"ds_read_b64_tr_b16 v[226:227], v237 offset:4096\n"
"v_accvgpr_write_b32 a98, v114\n"
"v_mfma_f32_16x16x32_bf16 v[160:163], v[212:215], a[20:23], v[160:163]\n"
"v_exp_f32 v172, v172\n"
"v_mfma_f32_16x16x32_bf16 v[164:167], v[208:211], a[32:35], v[188:191]\n"
"ds_read_b128 v[208:211], v230 offset:20480\n"
"v_accvgpr_write_b32 a147, v3\n"
"v_mfma_f32_16x16x32_bf16 v[164:167], v[212:215], a[36:39], v[164:167]\n"
"v_exp_f32 v168, v168\n"
"v_mfma_f32_16x16x32_bf16 v[164:167], v[216:219], a[40:43], v[164:167]\n"
"v_exp_f32 v174, v174\n"
"v_mfma_f32_16x16x32_bf16 v[164:167], v[220:223], a[44:47], v[164:167]\n"
"s_waitcnt lgkmcnt(2)\n"
"ds_read_b128 v[216:219], v230 offset:21504\n"
"v_accvgpr_write_b32 a177, v33\n"
"v_mfma_f32_16x16x32_bf16 a[208:211], v[124:127], a[244:247], 0\n"
"v_exp_f32 v177, v177\n"
"v_mfma_f32_16x16x32_bf16 v[0:3], v[128:131], a[244:247], 0\n"
"s_add_u32 s40, s56, s52\n"
"v_exp_f32 v154, v154\n"
"v_mfma_f32_16x16x32_bf16 v[0:3], v[132:135], a[248:251], v[0:3]\n"
"buffer_load_dwordx4 v240, s[16:19], s40 offen offset:1024 nt lds\n"
"v_mfma_f32_16x16x32_bf16 v[32:35], v[136:139], a[244:247], 0\n"
"v_exp_f32 v178, v178\n"
"v_mfma_f32_16x16x32_bf16 v[32:35], v[140:143], a[248:251], v[32:35]\n"
"v_permlane16_swap_b32 v17, v19\n"
"v_mfma_f32_16x16x32_bf16 v[64:67], v[144:147], a[244:247], 0\n"
"ds_read_b128 v[212:215], v231 offset:20480\n"
"v_cvt_pk_bf16_f32 v98, v168, v169\n"
"v_mfma_f32_16x16x32_bf16 v[64:67], v[148:151], a[248:251], v[64:67]\n"
"s_waitcnt lgkmcnt(2)\n"
"v_exp_f32 v162, v162\n"
"v_mfma_f32_16x16x32_bf16 v[68:71], v[148:151], v[224:227], 0\n"
"s_add_u32 s40, s56, s53\n"
"ds_read_b128 v[220:223], v231 offset:21504\n"
"v_bfe_u32 v254, v242, 6, 4\n"
"v_mfma_f32_16x16x32_bf16 v[68:71], v[144:147], a[252:255], v[68:71]\n"
"buffer_load_dwordx4 v240, s[16:19], s40 offen offset:2048 nt lds\n"
"v_mfma_f32_16x16x32_bf16 v[36:39], v[140:143], v[224:227], 0\n"
"v_exp_f32 v156, v156\n"
"v_mfma_f32_16x16x32_bf16 v[36:39], v[136:139], a[252:255], v[36:39]\n"
"v_exp_f32 v163, v163\n"
"v_mfma_f32_16x16x32_bf16 v[4:7], v[132:135], v[224:227], 0\n"
"v_exp_f32 v173, v173\n"
"v_mfma_f32_16x16x32_bf16 v[4:7], v[128:131], a[252:255], v[4:7]\n"
"ds_read_b64_tr_b16 a[250:251], v236 offset:5120\n"
"v_lshlrev_b32 v248, 6, v248\n"
"v_exp_f32 v175, v175\n"
"v_cvt_pk_bf16_f32 v114, v176, v177\n"
"v_cvt_pk_bf16_f32 v115, v178, v179\n"
"v_exp_f32 v152, v152\n"
"v_exp_f32 v155, v155\n"
"v_exp_f32 v157, v157\n"
"v_exp_f32 v161, v161\n"
"v_mfma_f32_16x16x32_bf16 a[212:215], v[124:127], a[252:255], 0\n"
"v_exp_f32 v160, v160\n"
"v_smfmac_f32_16x16x64_bf16 v[200:203], a[240:243], v[144:151], v241\n"
"ds_read_b64_tr_b16 a[246:247], v232 offset:5120\n"
"v_cvt_pk_bf16_f32 v107, v174, v175\n"
"v_mfma_f32_16x16x32_bf16 v[168:171], v[208:211], a[0:3], v[180:183]\n"
"s_waitcnt lgkmcnt(2)\n"
"ds_read_b64_tr_b16 a[244:245], v232 offset:1024\n"
"v_cvt_pk_bf16_f32 v106, v172, v173\n"
"v_mfma_f32_16x16x32_bf16 v[168:171], v[212:215], a[4:7], v[168:171]\n"
"v_accvgpr_write_b32 a163, v19\n"
"v_bfe_u32 v249, v242, 4, 2\n"
"v_mfma_f32_16x16x32_bf16 v[168:171], v[216:219], a[8:11], v[168:171]\n"
"s_add_u32 s40, s56, s54\n"
"ds_read_b64_tr_b16 a[248:249], v236 offset:1024\n"
"v_accvgpr_write_b32 a162, v18\n"
"v_mfma_f32_16x16x32_bf16 v[168:171], v[220:223], a[12:15], v[168:171]\n"
"buffer_load_dwordx4 v240, s[16:19], s40 offen offset:3072 nt lds\n"
"v_mfma_f32_16x16x32_bf16 v[172:175], v[216:219], a[24:27], v[184:187]\n"
"v_exp_f32 v158, v158\n"
"v_mfma_f32_16x16x32_bf16 v[172:175], v[220:223], a[28:31], v[172:175]\n"
"v_exp_f32 v164, v164\n"
"v_mfma_f32_16x16x32_bf16 v[172:175], v[208:211], a[16:19], v[172:175]\n"
"ds_read_b64_tr_b16 a[254:255], v233 offset:5120\n"
"v_accvgpr_write_b32 a161, v17\n"
"v_mfma_f32_16x16x32_bf16 v[172:175], v[212:215], a[20:23], v[172:175]\n"
"s_cbranch_vccnz LPCE1_%=\nLPKE1_%=:\n"
"v_cvt_pk_bf16_f32 v100, v156, v157\n"
"v_mfma_f32_16x16x32_bf16 v[176:179], v[208:211], a[32:35], v[188:191]\n"
"ds_read_b64_tr_b16 a[252:253], v233 offset:1024\n"
"v_accvgpr_write_b32 a160, v16\n"
"v_mfma_f32_16x16x32_bf16 v[176:179], v[212:215], a[36:39], v[176:179]\n"
"ds_read_b64_tr_b16 v[224:225], v237 offset:1024\n"
"ds_read_b64_tr_b16 v[226:227], v237 offset:5120\n"
"v_mfma_f32_16x16x32_bf16 v[176:179], v[216:219], a[40:43], v[176:179]\n"
"v_exp_f32 v159, v159\n"
"v_mfma_f32_16x16x32_bf16 v[176:179], v[220:223], a[44:47], v[176:179]\n"
"s_waitcnt lgkmcnt(4)\n"
"v_accvgpr_write_b32 a107, v123\n"
"v_mul_u32_u24 v249, 20, v249\n"
"v_mfma_f32_16x16x32_bf16 a[224:227], v[124:127], a[244:247], 0\n"
"v_exp_f32 v166, v166\n"
"v_mfma_f32_16x16x32_bf16 v[16:19], v[128:131], a[244:247], 0\n"
"s_waitcnt vmcnt(8)\n"
"v_xor_b32 v249, v249, v254\n"
"v_cvt_pk_bf16_f32 v109, v162, v163\n"
"v_mfma_f32_16x16x32_bf16 v[16:19], v[132:135], a[248:251], v[16:19]\n"
"s_barrier\n"
"ds_read_b128 v[216:219], v228 offset:33792\n"
"ds_read_b128 v[220:223], v229 offset:33792\n"
"v_cvt_pk_bf16_f32 v101, v158, v159\n"
"v_exp_f32 v167, v167\n"
"v_exp_f32 v169, v169\n"
"v_exp_f32 v178, v178\n"
"v_mfma_f32_16x16x32_bf16 v[48:51], v[136:139], a[244:247], 0\n"
"v_exp_f32 v165, v165\n"
"v_mfma_f32_16x16x32_bf16 v[48:51], v[140:143], a[248:251], v[48:51]\n"
"ds_read_b128 v[208:211], v228 offset:32768\n"
"ds_read_b128 v[212:215], v229 offset:32768\n"
"v_mfma_f32_16x16x32_bf16 v[80:83], v[144:147], a[244:247], 0\n"
"ds_read_b64_tr_b16 a[244:245], v234 offset:16384\n"
"ds_read_b64_tr_b16 a[246:247], v234 offset:20480\n"
"v_mfma_f32_16x16x32_bf16 v[80:83], v[148:151], a[248:251], v[80:83]\n"
"s_waitcnt lgkmcnt(6)\n"
"ds_read_b64_tr_b16 a[248:249], v238 offset:16384\n"
"ds_read_b64_tr_b16 a[250:251], v238 offset:20480\n"
"v_mfma_f32_16x16x32_bf16 v[84:87], v[148:151], v[224:227], 0\n"
"v_exp_f32 v173, v173\n"
"v_mfma_f32_16x16x32_bf16 v[84:87], v[144:147], a[252:255], v[84:87]\n"
"v_exp_f32 v175, v175\n"
"v_mfma_f32_16x16x32_bf16 v[52:55], v[140:143], v[224:227], 0\n"
"v_xor_b32 v248, v248, v242\n"
"v_lshl_add_u32 v249, v249, 4, s44\n"
"v_mfma_f32_16x16x32_bf16 v[52:55], v[136:139], a[252:255], v[52:55]\n"
"v_exp_f32 v153, v153\n"
"v_mfma_f32_16x16x32_bf16 v[20:23], v[132:135], v[224:227], 0\n"
"ds_read_b64_tr_b16 v[226:227], v239 offset:20480\n"
"v_cvt_pk_bf16_f32 v117, v166, v167\n"
"v_mfma_f32_16x16x32_bf16 v[20:23], v[128:131], a[252:255], v[20:23]\n"
"v_accvgpr_write_b32 a106, v122\n"
"v_cvt_pk_bf16_f32 v122, v152, v153\n"
"v_mfma_f32_16x16x32_bf16 a[228:231], v[124:127], a[252:255], 0\n"
"s_waitcnt lgkmcnt(3)\n"
"v_cvt_pk_bf16_f32 v123, v154, v155\n"
"s_branch LAY2_%=\n"
"LAL_%=:\n"
"v_mfma_f32_16x16x32_bf16 a[224:227], v[124:127], a[244:247], a[224:227]\n"
"v_exp_f32 v169, v169\n"
"v_mfma_f32_16x16x32_bf16 v[16:19], v[128:131], a[244:247], v[16:19]\n"
"s_waitcnt vmcnt(8)\n"
"v_exp_f32 v175, v175\n"
"v_mfma_f32_16x16x32_bf16 v[16:19], v[132:135], a[248:251], v[16:19]\n"
"s_barrier\n"
"ds_read_b128 v[220:223], v229 offset:1024\n"
"v_mfma_f32_16x16x32_bf16 v[48:51], v[136:139], a[244:247], v[48:51]\n"
"ds_read_b128 v[216:219], v228 offset:1024\n"
"v_cvt_pk_bf16_f32 v109, v162, v163\n"
"v_mfma_f32_16x16x32_bf16 v[48:51], v[140:143], a[248:251], v[48:51]\n"
"ds_read_b128 v[212:215], v229 offset:0\n"
"v_mfma_f32_16x16x32_bf16 v[80:83], v[144:147], a[244:247], v[80:83]\n"
"ds_read_b128 v[208:211], v228 offset:0\n"
"v_cvt_pk_bf16_f32 v101, v158, v159\n"
"v_mfma_f32_16x16x32_bf16 v[80:83], v[148:151], a[248:251], v[80:83]\n"
"s_waitcnt lgkmcnt(4)\n"
"v_exp_f32 v178, v178\n"
"v_mfma_f32_16x16x32_bf16 v[84:87], v[148:151], v[224:227], v[84:87]\n"
"ds_read_b64_tr_b16 a[244:245], v234 offset:49152\n"
"v_cvt_pk_bf16_f32 v117, v166, v167\n"
"v_mfma_f32_16x16x32_bf16 v[84:87], v[144:147], a[252:255], v[84:87]\n"
"ds_read_b64_tr_b16 a[250:251], v238 offset:53248\n"
"v_cvt_pk_bf16_f32 v123, v154, v155\n"
"v_mfma_f32_16x16x32_bf16 v[52:55], v[140:143], v[224:227], v[52:55]\n"
"v_exp_f32 v173, v173\n"
"v_mfma_f32_16x16x32_bf16 v[52:55], v[136:139], a[252:255], v[52:55]\n"
"ds_read_b64_tr_b16 a[248:249], v238 offset:49152\n"
"v_mfma_f32_16x16x32_bf16 v[20:23], v[132:135], v[224:227], v[20:23]\n"
"v_exp_f32 v156, v156\n"
"v_mfma_f32_16x16x32_bf16 v[20:23], v[128:131], a[252:255], v[20:23]\n"
"ds_read_b64_tr_b16 v[226:227], v239 offset:53248\n"
"v_cvt_pk_bf16_f32 v100, v156, v157\n"
"v_mfma_f32_16x16x32_bf16 a[228:231], v[124:127], a[252:255], a[228:231]\n"
"s_waitcnt lgkmcnt(4)\n"
"ds_read_b64_tr_b16 a[246:247], v234 offset:53248\n"
"v_mfma_f32_16x16x32_bf16 v[152:155], v[208:211], a[0:3], v[180:183]\n"
"ds_read_b64_tr_b16 v[224:225], v239 offset:49152\n"
"v_cvt_pk_bf16_f32 v116, v164, v165\n"
"v_mfma_f32_16x16x32_bf16 v[152:155], v[212:215], a[4:7], v[152:155]\n"
"ds_read_b64_tr_b16 a[252:253], v235 offset:49152\n"
"v_cvt_pk_bf16_f32 v108, v160, v161\n"
"v_mfma_f32_16x16x32_bf16 v[152:155], v[216:219], a[8:11], v[152:155]\n"
"s_add_u32 m0, s44, 81920\n"
"ds_read_b64_tr_b16 a[254:255], v235 offset:53248\n"
"v_mfma_f32_16x16x32_bf16 v[152:155], v[220:223], a[12:15], v[152:155]\n"
"buffer_load_dwordx4 v240, s[20:23], s56 offen nt lds\n"
"v_mfma_f32_16x16x32_bf16 v[156:159], v[216:219], a[24:27], v[184:187]\n"
"v_exp_f32 v177, v177\n"
"v_mfma_f32_16x16x32_bf16 v[156:159], v[220:223], a[28:31], v[156:159]\n"
"v_exp_f32 v168, v168\n"
"v_mfma_f32_16x16x32_bf16 v[156:159], v[208:211], a[16:19], v[156:159]\n"
"v_exp_f32 v179, v179\n"
"v_mfma_f32_16x16x32_bf16 v[156:159], v[212:215], a[20:23], v[156:159]\n"
"v_exp_f32 v174, v174\n"
"v_mfma_f32_16x16x32_bf16 v[160:163], v[208:211], a[32:35], v[188:191]\n"
"v_exp_f32 v172, v172\n"
"v_mfma_f32_16x16x32_bf16 v[160:163], v[212:215], a[36:39], v[160:163]\n"
"v_exp_f32 v171, v171\n"
"v_mfma_f32_16x16x32_bf16 v[160:163], v[216:219], a[40:43], v[160:163]\n"
"v_exp_f32 v154, v154\n"
"v_mfma_f32_16x16x32_bf16 v[160:163], v[220:223], a[44:47], v[160:163]\n"
"v_exp_f32 v170, v170\n"
"v_mfma_f32_16x16x32_bf16 v[164:167], v[216:219], a[200:203], v[192:195]\n"
"ds_read_b128 v[216:219], v228 offset:5120\n"
"v_cvt_pk_bf16_f32 v103, v170, v171\n"
"v_mfma_f32_16x16x32_bf16 v[164:167], v[220:223], a[204:207], v[164:167]\n"
"v_cvt_pk_bf16_f32 v102, v168, v169\n"
"v_cvt_pk_bf16_f32 v119, v178, v179\n"
"v_mfma_f32_16x16x32_bf16 v[164:167], v[208:211], a[192:195], v[164:167]\n"
"s_add_u32 s40, s56, s52\n"
"ds_read_b128 v[208:211], v228 offset:4096\n"
"v_mfma_f32_16x16x32_bf16 v[164:167], v[212:215], a[196:199], v[164:167]\n"
"s_waitcnt lgkmcnt(2)\n"
"buffer_load_dwordx4 v240, s[20:23], s40 offen offset:1024 nt lds\n"
"v_mfma_f32_16x16x32_bf16 a[216:219], v[120:123], a[244:247], a[216:219]\n"
"v_cvt_pk_bf16_f32 v110, v172, v173\n"
"v_cvt_pk_bf16_f32 v111, v174, v175\n"
"v_mfma_f32_16x16x32_bf16 v[8:11], v[96:99], a[244:247], v[8:11]\n"
"v_exp_f32 v176, v176\n"
"v_mfma_f32_16x16x32_bf16 v[8:11], v[100:103], a[248:251], v[8:11]\n"
"v_exp_f32 v155, v155\n"
"v_mfma_f32_16x16x32_bf16 v[40:43], v[104:107], a[244:247], v[40:43]\n"
"v_exp_f32 v152, v152\n"
"v_mfma_f32_16x16x32_bf16 v[40:43], v[108:111], a[248:251], v[40:43]\n"
"ds_read_b128 v[212:215], v229 offset:4096\n"
"v_cvt_pk_bf16_f32 v118, v176, v177\n"
"v_mfma_f32_16x16x32_bf16 v[72:75], v[112:115], a[244:247], v[72:75]\n"
"v_exp_f32 v158, v158\n"
"v_mfma_f32_16x16x32_bf16 v[72:75], v[116:119], a[248:251], v[72:75]\n"
"v_exp_f32 v163, v163\n"
"v_mfma_f32_16x16x32_bf16 v[76:79], v[116:119], v[224:227], v[76:79]\n"
"ds_read_b128 v[220:223], v229 offset:5120\n"
"v_mfma_f32_16x16x32_bf16 v[76:79], v[112:115], a[252:255], v[76:79]\n"
"v_exp_f32 v159, v159\n"
"v_mfma_f32_16x16x32_bf16 v[44:47], v[108:111], v[224:227], v[44:47]\n"
"s_add_u32 s40, s56, s53\n"
"ds_read_b64_tr_b16 a[244:245], v234 offset:50176\n"
"v_mfma_f32_16x16x32_bf16 v[44:47], v[104:107], a[252:255], v[44:47]\n"
"buffer_load_dwordx4 v240, s[20:23], s40 offen offset:2048 nt lds\n"
"v_mfma_f32_16x16x32_bf16 v[12:15], v[100:103], v[224:227], v[12:15]\n"
"v_exp_f32 v153, v153\n"
"v_mfma_f32_16x16x32_bf16 v[12:15], v[96:99], a[252:255], v[12:15]\n"
"v_exp_f32 v165, v165\n"
"v_mfma_f32_16x16x32_bf16 a[220:223], v[120:123], a[252:255], a[220:223]\n"
"v_exp_f32 v164, v164\n"
"v_smfmac_f32_16x16x64_bf16 v[196:199], a[240:243], v[96:103], v241\n"
"v_cvt_pk_bf16_f32 v129, v154, v155\n"
"v_mfma_f32_16x16x32_bf16 v[202:205], a[240:243], v[120:123], v[202:205]\n"
"s_waitcnt lgkmcnt(1)\n"
"ds_read_b64_tr_b16 a[254:255], v235 offset:54272\n"
"v_mfma_f32_16x16x32_bf16 v[168:171], v[216:219], a[8:11], v[180:183]\n"
"ds_read_b64_tr_b16 a[248:249], v238 offset:50176\n"
"v_mfma_f32_16x16x32_bf16 v[168:171], v[220:223], a[12:15], v[168:171]\n"
"ds_read_b64_tr_b16 a[246:247], v234 offset:54272\n"
"v_mfma_f32_16x16x32_bf16 v[168:171], v[208:211], a[0:3], v[168:171]\n"
"ds_read_b64_tr_b16 v[224:225], v239 offset:50176\n"
"v_cvt_pk_bf16_f32 v124, v164, v165\n"
"v_mfma_f32_16x16x32_bf16 v[168:171], v[212:215], a[4:7], v[168:171]\n"
"v_exp_f32 v166, v166\n"
"v_mfma_f32_16x16x32_bf16 v[172:175], v[208:211], a[16:19], v[184:187]\n"
"ds_read_b64_tr_b16 a[252:253], v235 offset:50176\n"
"v_cvt_pk_bf16_f32 v128, v152, v153\n"
"v_mfma_f32_16x16x32_bf16 v[172:175], v[212:215], a[20:23], v[172:175]\n"
"ds_read_b64_tr_b16 v[226:227], v239 offset:54272\n"
"v_cvt_pk_bf16_f32 v137, v158, v159\n"
"v_mfma_f32_16x16x32_bf16 v[172:175], v[216:219], a[24:27], v[172:175]\n"
"s_add_u32 s40, s56, s54\n"
"ds_read_b64_tr_b16 a[250:251], v238 offset:54272\n"
"v_mfma_f32_16x16x32_bf16 v[172:175], v[220:223], a[28:31], v[172:175]\n"
"buffer_load_dwordx4 v240, s[20:23], s40 offen offset:3072 nt lds\n"
"v_mfma_f32_16x16x32_bf16 v[176:179], v[216:219], a[40:43], v[188:191]\n"
"s_add_u32 s55, s55, s59\n"
"v_exp_f32 v167, v167\n"
"v_mfma_f32_16x16x32_bf16 v[176:179], v[220:223], a[44:47], v[176:179]\n"
"s_add_u32 s41, s55, s57\n"
"v_exp_f32 v161, v161\n"
"v_mfma_f32_16x16x32_bf16 v[176:179], v[208:211], a[32:35], v[176:179]\n"
"s_and_b32 s41, s41, s58\n"
"v_exp_f32 v157, v157\n"
"v_mfma_f32_16x16x32_bf16 v[176:179], v[212:215], a[36:39], v[176:179]\n"
"s_andn2_b32 s56, s55, s58\n"
"v_exp_f32 v160, v160\n"
"v_mfma_f32_16x16x32_bf16 v[152:155], v[208:211], a[192:195], v[192:195]\n"
"s_or_b32 s56, s56, s41\n"
"ds_read_b128 v[208:211], v230 offset:0\n"
"v_mfma_f32_16x16x32_bf16 v[152:155], v[212:215], a[196:199], v[152:155]\n"
"v_exp_f32 v172, v172\n"
"v_mfma_f32_16x16x32_bf16 v[152:155], v[216:219], a[200:203], v[152:155]\n"
"ds_read_b128 v[216:219], v230 offset:1024\n"
"v_mfma_f32_16x16x32_bf16 v[152:155], v[220:223], a[204:207], v[152:155]\n"
"s_waitcnt lgkmcnt(2)\n"
"v_exp_f32 v162, v162\n"
"v_mfma_f32_16x16x32_bf16 a[232:235], v[120:123], a[244:247], a[232:235]\n"
"ds_read_b128 v[212:215], v231 offset:0\n"
"v_mfma_f32_16x16x32_bf16 v[24:27], v[96:99], a[244:247], v[24:27]\n"
"s_add_u32 m0, s44, 32768\n"
"ds_read_b128 v[220:223], v231 offset:1024\n"
"v_mfma_f32_16x16x32_bf16 v[24:27], v[100:103], a[248:251], v[24:27]\n"
"buffer_load_dwordx4 v240, s[16:19], s56 offen nt lds\n"
"v_mfma_f32_16x16x32_bf16 v[56:59], v[104:107], a[244:247], v[56:59]\n"
"v_exp_f32 v171, v171\n"
"v_mfma_f32_16x16x32_bf16 v[56:59], v[108:111], a[248:251], v[56:59]\n"
"v_exp_f32 v169, v169\n"
"v_mfma_f32_16x16x32_bf16 v[88:91], v[112:115], a[244:247], v[88:91]\n"
"v_exp_f32 v154, v154\n"
"v_mfma_f32_16x16x32_bf16 v[88:91], v[116:119], a[248:251], v[88:91]\n"
"v_exp_f32 v156, v156\n"
"v_mfma_f32_16x16x32_bf16 v[92:95], v[116:119], v[224:227], v[92:95]\n"
"ds_read_b64_tr_b16 a[244:245], v232 offset:49152\n"
"v_cvt_pk_bf16_f32 v145, v162, v163\n"
"v_mfma_f32_16x16x32_bf16 v[92:95], v[112:115], a[252:255], v[92:95]\n"
"ds_read_b64_tr_b16 a[250:251], v236 offset:53248\n"
"v_cvt_pk_bf16_f32 v136, v156, v157\n"
"v_mfma_f32_16x16x32_bf16 v[60:63], v[108:111], v[224:227], v[60:63]\n"
"ds_read_b64_tr_b16 a[246:247], v232 offset:53248\n"
"v_cvt_pk_bf16_f32 v144, v160, v161\n"
"v_mfma_f32_16x16x32_bf16 v[60:63], v[104:107], a[252:255], v[60:63]\n"
"v_exp_f32 v152, v152\n"
"v_mfma_f32_16x16x32_bf16 v[28:31], v[100:103], v[224:227], v[28:31]\n"
"ds_read_b64_tr_b16 v[224:225], v237 offset:49152\n"
"ds_read_b64_tr_b16 v[226:227], v237 offset:53248\n"
"v_mfma_f32_16x16x32_bf16 v[28:31], v[96:99], a[252:255], v[28:31]\n"
"ds_read_b64_tr_b16 a[248:249], v236 offset:49152\n"
"v_cvt_pk_bf16_f32 v125, v166, v167\n"
"v_mfma_f32_16x16x32_bf16 a[236:239], v[120:123], a[252:255], a[236:239]\n"
"s_add_u32 s40, s56, s52\n"
"ds_read_b64_tr_b16 a[252:253], v233 offset:49152\n"
"v_smfmac_f32_16x16x64_bf16 v[198:201], a[240:243], v[104:111], v241\n"
"s_waitcnt lgkmcnt(7)\n"
"buffer_load_dwordx4 v240, s[16:19], s40 offen offset:1024 nt lds\n"
"v_mfma_f32_16x16x32_bf16 v[156:159], v[208:211], a[0:3], v[180:183]\n"
"v_exp_f32 v175, v175\n"
"v_mfma_f32_16x16x32_bf16 v[156:159], v[212:215], a[4:7], v[156:159]\n"
"v_exp_f32 v173, v173\n"
"v_mfma_f32_16x16x32_bf16 v[156:159], v[216:219], a[8:11], v[156:159]\n"
"v_exp_f32 v168, v168\n"
"v_mfma_f32_16x16x32_bf16 v[156:159], v[220:223], a[12:15], v[156:159]\n"
"ds_read_b64_tr_b16 a[254:255], v233 offset:53248\n"
"v_mfma_f32_16x16x32_bf16 v[160:163], v[216:219], a[24:27], v[184:187]\n"
"v_exp_f32 v170, v170\n"
"v_mfma_f32_16x16x32_bf16 v[160:163], v[220:223], a[28:31], v[160:163]\n"
"v_exp_f32 v178, v178\n"
"v_mfma_f32_16x16x32_bf16 v[160:163], v[208:211], a[16:19], v[160:163]\n"
"v_exp_f32 v177, v177\n"
"v_mfma_f32_16x16x32_bf16 v[160:163], v[212:215], a[20:23], v[160:163]\n"
"v_exp_f32 v176, v176\n"
"v_mfma_f32_16x16x32_bf16 v[164:167], v[208:211], a[32:35], v[188:191]\n"
"ds_read_b128 v[208:211], v230 offset:4096\n"
"v_cvt_pk_bf16_f32 v131, v170, v171\n"
"v_mfma_f32_16x16x32_bf16 v[164:167], v[212:215], a[36:39], v[164:167]\n"
"ds_read_b128 v[212:215], v231 offset:4096\n"
"v_mfma_f32_16x16x32_bf16 v[164:167], v[216:219], a[40:43], v[164:167]\n"
"v_exp_f32 v153, v153\n"
"v_mfma_f32_16x16x32_bf16 v[164:167], v[220:223], a[44:47], v[164:167]\n"
"s_waitcnt lgkmcnt(2)\n"
"ds_read_b128 v[220:223], v231 offset:5120\n"
"v_cvt_pk_bf16_f32 v138, v172, v173\n"
"v_mfma_f32_16x16x32_bf16 a[208:211], v[120:123], a[244:247], a[208:211]\n"
"v_exp_f32 v174, v174\n"
"v_mfma_f32_16x16x32_bf16 v[0:3], v[96:99], a[244:247], v[0:3]\n"
"s_add_u32 s40, s56, s53\n"
"ds_read_b128 v[216:219], v230 offset:5120\n"
"v_mfma_f32_16x16x32_bf16 v[0:3], v[100:103], a[248:251], v[0:3]\n"
"buffer_load_dwordx4 v240, s[16:19], s40 offen offset:2048 nt lds\n"
"v_mfma_f32_16x16x32_bf16 v[32:35], v[104:107], a[244:247], v[32:35]\n"
"v_exp_f32 v163, v163\n"
"v_mfma_f32_16x16x32_bf16 v[32:35], v[108:111], a[248:251], v[32:35]\n"
"v_exp_f32 v161, v161\n"
"v_mfma_f32_16x16x32_bf16 v[64:67], v[112:115], a[244:247], v[64:67]\n"
"v_exp_f32 v155, v155\n"
"v_mfma_f32_16x16x32_bf16 v[64:67], v[116:119], a[248:251], v[64:67]\n"
"v_exp_f32 v166, v166\n"
"v_mfma_f32_16x16x32_bf16 v[68:71], v[116:119], v[224:227], v[68:71]\n"
"ds_read_b64_tr_b16 a[250:251], v236 offset:54272\n"
"v_cvt_pk_bf16_f32 v139, v174, v175\n"
"v_mfma_f32_16x16x32_bf16 v[68:71], v[112:115], a[252:255], v[68:71]\n"
"ds_read_b64_tr_b16 a[246:247], v232 offset:54272\n"
"v_cvt_pk_bf16_f32 v130, v168, v169\n"
"v_mfma_f32_16x16x32_bf16 v[36:39], v[108:111], v[224:227], v[36:39]\n"
"v_exp_f32 v179, v179\n"
"v_mfma_f32_16x16x32_bf16 v[36:39], v[104:107], a[252:255], v[36:39]\n"
"ds_read_b64_tr_b16 a[244:245], v232 offset:50176\n"
"v_cvt_pk_bf16_f32 v126, v152, v153\n"
"v_mfma_f32_16x16x32_bf16 v[4:7], v[100:103], v[224:227], v[4:7]\n"
"s_add_u32 s40, s56, s54\n"
"ds_read_b64_tr_b16 a[248:249], v236 offset:50176\n"
"v_mfma_f32_16x16x32_bf16 v[4:7], v[96:99], a[252:255], v[4:7]\n"
"buffer_load_dwordx4 v240, s[16:19], s40 offen offset:3072 nt lds\n"
"v_mfma_f32_16x16x32_bf16 a[212:215], v[120:123], a[252:255], a[212:215]\n"
"v_exp_f32 v162, v162\n"
"v_smfmac_f32_16x16x64_bf16 v[200:203], a[240:243], v[112:119], v241\n"
"s_waitcnt lgkmcnt(4)\n"
"v_cvt_pk_bf16_f32 v146, v176, v177\n"
"v_mfma_f32_16x16x32_bf16 v[168:171], v[208:211], a[0:3], v[180:183]\n"
"v_exp_f32 v157, v157\n"
"v_mfma_f32_16x16x32_bf16 v[168:171], v[212:215], a[4:7], v[168:171]\n"
"v_exp_f32 v165, v165\n"
"v_mfma_f32_16x16x32_bf16 v[168:171], v[216:219], a[8:11], v[168:171]\n"
"ds_read_b64_tr_b16 a[254:255], v233 offset:54272\n"
"ds_read_b64_tr_b16 v[226:227], v237 offset:54272\n"
"v_mfma_f32_16x16x32_bf16 v[168:171], v[220:223], a[12:15], v[168:171]\n"
"ds_read_b64_tr_b16 a[252:253], v233 offset:50176\n"
"v_cvt_pk_bf16_f32 v147, v178, v179\n"
"v_mfma_f32_16x16x32_bf16 v[172:175], v[216:219], a[24:27], v[184:187]\n"
"ds_read_b64_tr_b16 v[224:225], v237 offset:50176\n"
"v_mfma_f32_16x16x32_bf16 v[172:175], v[220:223], a[28:31], v[172:175]\n"
"v_exp_f32 v160, v160\n"
"v_mfma_f32_16x16x32_bf16 v[172:175], v[208:211], a[16:19], v[172:175]\n"
"v_exp_f32 v158, v158\n"
"v_mfma_f32_16x16x32_bf16 v[172:175], v[212:215], a[20:23], v[172:175]\n"
"s_cbranch_vccnz LPCL0_%=\nLPKL0_%=:\n"
"v_mfma_f32_16x16x32_bf16 v[176:179], v[208:211], a[32:35], v[188:191]\n"
"v_exp_f32 v159, v159\n"
"v_mfma_f32_16x16x32_bf16 v[176:179], v[212:215], a[36:39], v[176:179]\n"
"v_exp_f32 v167, v167\n"
"v_mfma_f32_16x16x32_bf16 v[176:179], v[216:219], a[40:43], v[176:179]\n"
"v_exp_f32 v164, v164\n"
"v_mfma_f32_16x16x32_bf16 v[176:179], v[220:223], a[44:47], v[176:179]\n"
"s_waitcnt lgkmcnt(1)\n"
"v_mfma_f32_16x16x32_bf16 a[224:227], v[120:123], a[244:247], a[224:227]\n"
"v_exp_f32 v169, v169\n"
"v_mfma_f32_16x16x32_bf16 v[16:19], v[96:99], a[244:247], v[16:19]\n"
"s_waitcnt vmcnt(8)\n"
"v_exp_f32 v175, v175\n"
"v_mfma_f32_16x16x32_bf16 v[16:19], v[100:103], a[248:251], v[16:19]\n"
"s_barrier\n"
"ds_read_b128 v[220:223], v229 offset:17408\n"
"v_mfma_f32_16x16x32_bf16 v[48:51], v[104:107], a[244:247], v[48:51]\n"
"ds_read_b128 v[216:219], v228 offset:17408\n"
"v_cvt_pk_bf16_f32 v141, v162, v163\n"
"v_mfma_f32_16x16x32_bf16 v[48:51], v[108:111], a[248:251], v[48:51]\n"
"ds_read_b128 v[212:215], v229 offset:16384\n"
"v_mfma_f32_16x16x32_bf16 v[80:83], v[112:115], a[244:247], v[80:83]\n"
"ds_read_b128 v[208:211], v228 offset:16384\n"
"v_cvt_pk_bf16_f32 v133, v158, v159\n"
"v_mfma_f32_16x16x32_bf16 v[80:83], v[116:119], a[248:251], v[80:83]\n"
"s_waitcnt lgkmcnt(4)\n"
"v_exp_f32 v178, v178\n"
"v_mfma_f32_16x16x32_bf16 v[84:87], v[116:119], v[224:227], v[84:87]\n"
"ds_read_b64_tr_b16 a[244:245], v234 offset:0\n"
"v_cvt_pk_bf16_f32 v149, v166, v167\n"
"v_mfma_f32_16x16x32_bf16 v[84:87], v[112:115], a[252:255], v[84:87]\n"
"ds_read_b64_tr_b16 a[250:251], v238 offset:4096\n"
"v_cvt_pk_bf16_f32 v127, v154, v155\n"
"v_mfma_f32_16x16x32_bf16 v[52:55], v[108:111], v[224:227], v[52:55]\n"
"v_exp_f32 v173, v173\n"
"v_mfma_f32_16x16x32_bf16 v[52:55], v[104:107], a[252:255], v[52:55]\n"
"ds_read_b64_tr_b16 a[248:249], v238 offset:0\n"
"v_mfma_f32_16x16x32_bf16 v[20:23], v[100:103], v[224:227], v[20:23]\n"
"v_exp_f32 v156, v156\n"
"v_mfma_f32_16x16x32_bf16 v[20:23], v[96:99], a[252:255], v[20:23]\n"
"ds_read_b64_tr_b16 v[226:227], v239 offset:4096\n"
"v_cvt_pk_bf16_f32 v132, v156, v157\n"
"v_mfma_f32_16x16x32_bf16 a[228:231], v[120:123], a[252:255], a[228:231]\n"
"s_waitcnt lgkmcnt(4)\n"
"ds_read_b64_tr_b16 a[246:247], v234 offset:4096\n"
"v_mfma_f32_16x16x32_bf16 v[152:155], v[208:211], a[0:3], v[180:183]\n"
"ds_read_b64_tr_b16 v[224:225], v239 offset:0\n"
"v_cvt_pk_bf16_f32 v148, v164, v165\n"
"v_mfma_f32_16x16x32_bf16 v[152:155], v[212:215], a[4:7], v[152:155]\n"
"ds_read_b64_tr_b16 a[252:253], v235 offset:0\n"
"v_cvt_pk_bf16_f32 v140, v160, v161\n"
"v_mfma_f32_16x16x32_bf16 v[152:155], v[216:219], a[8:11], v[152:155]\n"
"s_add_u32 m0, s44, 98304\n"
"ds_read_b64_tr_b16 a[254:255], v235 offset:4096\n"
"v_mfma_f32_16x16x32_bf16 v[152:155], v[220:223], a[12:15], v[152:155]\n"
"buffer_load_dwordx4 v240, s[20:23], s56 offen nt lds\n"
"v_mfma_f32_16x16x32_bf16 v[156:159], v[216:219], a[24:27], v[184:187]\n"
"v_exp_f32 v177, v177\n"
"v_mfma_f32_16x16x32_bf16 v[156:159], v[220:223], a[28:31], v[156:159]\n"
"v_exp_f32 v168, v168\n"
"v_mfma_f32_16x16x32_bf16 v[156:159], v[208:211], a[16:19], v[156:159]\n"
"v_exp_f32 v179, v179\n"
"v_mfma_f32_16x16x32_bf16 v[156:159], v[212:215], a[20:23], v[156:159]\n"
"v_exp_f32 v174, v174\n"
"v_mfma_f32_16x16x32_bf16 v[160:163], v[208:211], a[32:35], v[188:191]\n"
"v_exp_f32 v172, v172\n"
"v_mfma_f32_16x16x32_bf16 v[160:163], v[212:215], a[36:39], v[160:163]\n"
"v_exp_f32 v171, v171\n"
"v_mfma_f32_16x16x32_bf16 v[160:163], v[216:219], a[40:43], v[160:163]\n"
"v_exp_f32 v154, v154\n"
"v_mfma_f32_16x16x32_bf16 v[160:163], v[220:223], a[44:47], v[160:163]\n"
"v_exp_f32 v170, v170\n"
"v_mfma_f32_16x16x32_bf16 v[164:167], v[216:219], a[200:203], v[192:195]\n"
"ds_read_b128 v[216:219], v228 offset:21504\n"
"v_cvt_pk_bf16_f32 v135, v170, v171\n"
"v_mfma_f32_16x16x32_bf16 v[164:167], v[220:223], a[204:207], v[164:167]\n"
"v_cvt_pk_bf16_f32 v134, v168, v169\n"
"v_cvt_pk_bf16_f32 v151, v178, v179\n"
"v_mfma_f32_16x16x32_bf16 v[164:167], v[208:211], a[192:195], v[164:167]\n"
"s_add_u32 s40, s56, s52\n"
"ds_read_b128 v[208:211], v228 offset:20480\n"
"v_mfma_f32_16x16x32_bf16 v[164:167], v[212:215], a[196:199], v[164:167]\n"
"s_waitcnt lgkmcnt(2)\n"
"buffer_load_dwordx4 v240, s[20:23], s40 offen offset:1024 nt lds\n"
"v_mfma_f32_16x16x32_bf16 a[216:219], v[124:127], a[244:247], a[216:219]\n"
"v_cvt_pk_bf16_f32 v142, v172, v173\n"
"v_cvt_pk_bf16_f32 v143, v174, v175\n"
"v_mfma_f32_16x16x32_bf16 v[8:11], v[128:131], a[244:247], v[8:11]\n"
"v_exp_f32 v176, v176\n"
"v_mfma_f32_16x16x32_bf16 v[8:11], v[132:135], a[248:251], v[8:11]\n"
"v_exp_f32 v155, v155\n"
"v_mfma_f32_16x16x32_bf16 v[40:43], v[136:139], a[244:247], v[40:43]\n"
"v_exp_f32 v152, v152\n"
"v_mfma_f32_16x16x32_bf16 v[40:43], v[140:143], a[248:251], v[40:43]\n"
"ds_read_b128 v[212:215], v229 offset:20480\n"
"v_cvt_pk_bf16_f32 v150, v176, v177\n"
"v_mfma_f32_16x16x32_bf16 v[72:75], v[144:147], a[244:247], v[72:75]\n"
"v_exp_f32 v158, v158\n"
"v_mfma_f32_16x16x32_bf16 v[72:75], v[148:151], a[248:251], v[72:75]\n"
"v_exp_f32 v163, v163\n"
"v_mfma_f32_16x16x32_bf16 v[76:79], v[148:151], v[224:227], v[76:79]\n"
"ds_read_b128 v[220:223], v229 offset:21504\n"
"v_mfma_f32_16x16x32_bf16 v[76:79], v[144:147], a[252:255], v[76:79]\n"
"v_exp_f32 v159, v159\n"
"v_mfma_f32_16x16x32_bf16 v[44:47], v[140:143], v[224:227], v[44:47]\n"
"s_add_u32 s40, s56, s53\n"
"ds_read_b64_tr_b16 a[244:245], v234 offset:1024\n"
"v_mfma_f32_16x16x32_bf16 v[44:47], v[136:139], a[252:255], v[44:47]\n"
"buffer_load_dwordx4 v240, s[20:23], s40 offen offset:2048 nt lds\n"
"v_mfma_f32_16x16x32_bf16 v[12:15], v[132:135], v[224:227], v[12:15]\n"
"v_exp_f32 v153, v153\n"
"v_mfma_f32_16x16x32_bf16 v[12:15], v[128:131], a[252:255], v[12:15]\n"
"v_exp_f32 v165, v165\n"
"v_mfma_f32_16x16x32_bf16 a[220:223], v[124:127], a[252:255], a[220:223]\n"
"v_exp_f32 v164, v164\n"
"v_smfmac_f32_16x16x64_bf16 v[196:199], a[240:243], v[128:135], v241\n"
"v_cvt_pk_bf16_f32 v97, v154, v155\n"
"v_mfma_f32_16x16x32_bf16 v[202:205], a[240:243], v[124:127], v[202:205]\n"
"s_waitcnt lgkmcnt(1)\n"
"ds_read_b64_tr_b16 a[254:255], v235 offset:5120\n"
"v_mfma_f32_16x16x32_bf16 v[168:171], v[216:219], a[8:11], v[180:183]\n"
"ds_read_b64_tr_b16 a[248:249], v238 offset:1024\n"
"v_mfma_f32_16x16x32_bf16 v[168:171], v[220:223], a[12:15], v[168:171]\n"
"ds_read_b64_tr_b16 a[246:247], v234 offset:5120\n"
"v_mfma_f32_16x16x32_bf16 v[168:171], v[208:211], a[0:3], v[168:171]\n"
"ds_read_b64_tr_b16 v[224:225], v239 offset:1024\n"
"v_cvt_pk_bf16_f32 v120, v164, v165\n"
"v_mfma_f32_16x16x32_bf16 v[168:171], v[212:215], a[4:7], v[168:171]\n"
"v_exp_f32 v166, v166\n"
"v_mfma_f32_16x16x32_bf16 v[172:175], v[208:211], a[16:19], v[184:187]\n"
"ds_read_b64_tr_b16 a[252:253], v235 offset:1024\n"
"v_cvt_pk_bf16_f32 v96, v152, v153\n"
"v_mfma_f32_16x16x32_bf16 v[172:175], v[212:215], a[20:23], v[172:175]\n"
"ds_read_b64_tr_b16 v[226:227], v239 offset:5120\n"
"v_cvt_pk_bf16_f32 v105, v158, v159\n"
"v_mfma_f32_16x16x32_bf16 v[172:175], v[216:219], a[24:27], v[172:175]\n"
"s_add_u32 s40, s56, s54\n"
"ds_read_b64_tr_b16 a[250:251], v238 offset:5120\n"
"v_mfma_f32_16x16x32_bf16 v[172:175], v[220:223], a[28:31], v[172:175]\n"
"buffer_load_dwordx4 v240, s[20:23], s40 offen offset:3072 nt lds\n"
"v_mfma_f32_16x16x32_bf16 v[176:179], v[216:219], a[40:43], v[188:191]\n"
"s_add_u32 s55, s55, s59\n"
"v_exp_f32 v167, v167\n"
"v_mfma_f32_16x16x32_bf16 v[176:179], v[220:223], a[44:47], v[176:179]\n"
"s_add_u32 s41, s55, s57\n"
"v_exp_f32 v161, v161\n"
"v_mfma_f32_16x16x32_bf16 v[176:179], v[208:211], a[32:35], v[176:179]\n"
"s_and_b32 s41, s41, s58\n"
"v_exp_f32 v157, v157\n"
"v_mfma_f32_16x16x32_bf16 v[176:179], v[212:215], a[36:39], v[176:179]\n"
"s_andn2_b32 s56, s55, s58\n"
"v_exp_f32 v160, v160\n"
"v_mfma_f32_16x16x32_bf16 v[152:155], v[208:211], a[192:195], v[192:195]\n"
"s_or_b32 s56, s56, s41\n"
"ds_read_b128 v[208:211], v230 offset:16384\n"
"v_mfma_f32_16x16x32_bf16 v[152:155], v[212:215], a[196:199], v[152:155]\n"
"v_exp_f32 v172, v172\n"
"v_mfma_f32_16x16x32_bf16 v[152:155], v[216:219], a[200:203], v[152:155]\n"
"ds_read_b128 v[216:219], v230 offset:17408\n"
"v_mfma_f32_16x16x32_bf16 v[152:155], v[220:223], a[204:207], v[152:155]\n"
"s_waitcnt lgkmcnt(2)\n"
"v_exp_f32 v162, v162\n"
"v_mfma_f32_16x16x32_bf16 a[232:235], v[124:127], a[244:247], a[232:235]\n"
"ds_read_b128 v[212:215], v231 offset:16384\n"
"v_mfma_f32_16x16x32_bf16 v[24:27], v[128:131], a[244:247], v[24:27]\n"
"s_add_u32 m0, s44, 49152\n"
"ds_read_b128 v[220:223], v231 offset:17408\n"
"v_mfma_f32_16x16x32_bf16 v[24:27], v[132:135], a[248:251], v[24:27]\n"
"buffer_load_dwordx4 v240, s[16:19], s56 offen nt lds\n"
"v_mfma_f32_16x16x32_bf16 v[56:59], v[136:139], a[244:247], v[56:59]\n"
"v_exp_f32 v171, v171\n"
"v_mfma_f32_16x16x32_bf16 v[56:59], v[140:143], a[248:251], v[56:59]\n"
"v_exp_f32 v169, v169\n"
"v_mfma_f32_16x16x32_bf16 v[88:91], v[144:147], a[244:247], v[88:91]\n"
"v_exp_f32 v154, v154\n"
"v_mfma_f32_16x16x32_bf16 v[88:91], v[148:151], a[248:251], v[88:91]\n"
"v_exp_f32 v156, v156\n"
"v_mfma_f32_16x16x32_bf16 v[92:95], v[148:151], v[224:227], v[92:95]\n"
"ds_read_b64_tr_b16 a[244:245], v232 offset:0\n"
"v_cvt_pk_bf16_f32 v113, v162, v163\n"
"v_mfma_f32_16x16x32_bf16 v[92:95], v[144:147], a[252:255], v[92:95]\n"
"ds_read_b64_tr_b16 a[250:251], v236 offset:4096\n"
"v_cvt_pk_bf16_f32 v104, v156, v157\n"
"v_mfma_f32_16x16x32_bf16 v[60:63], v[140:143], v[224:227], v[60:63]\n"
"ds_read_b64_tr_b16 a[246:247], v232 offset:4096\n"
"v_cvt_pk_bf16_f32 v112, v160, v161\n"
"v_mfma_f32_16x16x32_bf16 v[60:63], v[136:139], a[252:255], v[60:63]\n"
"v_exp_f32 v152, v152\n"
"v_mfma_f32_16x16x32_bf16 v[28:31], v[132:135], v[224:227], v[28:31]\n"
"ds_read_b64_tr_b16 v[224:225], v237 offset:0\n"
"ds_read_b64_tr_b16 v[226:227], v237 offset:4096\n"
"v_mfma_f32_16x16x32_bf16 v[28:31], v[128:131], a[252:255], v[28:31]\n"
"ds_read_b64_tr_b16 a[248:249], v236 offset:0\n"
"v_cvt_pk_bf16_f32 v121, v166, v167\n"
"v_mfma_f32_16x16x32_bf16 a[236:239], v[124:127], a[252:255], a[236:239]\n"
"s_add_u32 s40, s56, s52\n"
"ds_read_b64_tr_b16 a[252:253], v233 offset:0\n"
"v_smfmac_f32_16x16x64_bf16 v[198:201], a[240:243], v[136:143], v241\n"
"s_waitcnt lgkmcnt(7)\n"
"buffer_load_dwordx4 v240, s[16:19], s40 offen offset:1024 nt lds\n"
"v_mfma_f32_16x16x32_bf16 v[156:159], v[208:211], a[0:3], v[180:183]\n"
"v_exp_f32 v175, v175\n"
"v_mfma_f32_16x16x32_bf16 v[156:159], v[212:215], a[4:7], v[156:159]\n"
"v_exp_f32 v173, v173\n"
"v_mfma_f32_16x16x32_bf16 v[156:159], v[216:219], a[8:11], v[156:159]\n"
"v_exp_f32 v168, v168\n"
"v_mfma_f32_16x16x32_bf16 v[156:159], v[220:223], a[12:15], v[156:159]\n"
"ds_read_b64_tr_b16 a[254:255], v233 offset:4096\n"
"v_mfma_f32_16x16x32_bf16 v[160:163], v[216:219], a[24:27], v[184:187]\n"
"v_exp_f32 v170, v170\n"
"v_mfma_f32_16x16x32_bf16 v[160:163], v[220:223], a[28:31], v[160:163]\n"
"v_exp_f32 v178, v178\n"
"v_mfma_f32_16x16x32_bf16 v[160:163], v[208:211], a[16:19], v[160:163]\n"
"v_exp_f32 v177, v177\n"
"v_mfma_f32_16x16x32_bf16 v[160:163], v[212:215], a[20:23], v[160:163]\n"
"v_exp_f32 v176, v176\n"
"v_mfma_f32_16x16x32_bf16 v[164:167], v[208:211], a[32:35], v[188:191]\n"
"ds_read_b128 v[208:211], v230 offset:20480\n"
"v_cvt_pk_bf16_f32 v99, v170, v171\n"
"v_mfma_f32_16x16x32_bf16 v[164:167], v[212:215], a[36:39], v[164:167]\n"
"ds_read_b128 v[212:215], v231 offset:20480\n"
"v_mfma_f32_16x16x32_bf16 v[164:167], v[216:219], a[40:43], v[164:167]\n"
"v_exp_f32 v153, v153\n"
"v_mfma_f32_16x16x32_bf16 v[164:167], v[220:223], a[44:47], v[164:167]\n"
"s_waitcnt lgkmcnt(2)\n"
"ds_read_b128 v[220:223], v231 offset:21504\n"
"v_cvt_pk_bf16_f32 v106, v172, v173\n"
"v_mfma_f32_16x16x32_bf16 a[208:211], v[124:127], a[244:247], a[208:211]\n"
"v_exp_f32 v174, v174\n"
"v_mfma_f32_16x16x32_bf16 v[0:3], v[128:131], a[244:247], v[0:3]\n"
"s_add_u32 s40, s56, s53\n"
"ds_read_b128 v[216:219], v230 offset:21504\n"
"v_mfma_f32_16x16x32_bf16 v[0:3], v[132:135], a[248:251], v[0:3]\n"
"buffer_load_dwordx4 v240, s[16:19], s40 offen offset:2048 nt lds\n"
"v_mfma_f32_16x16x32_bf16 v[32:35], v[136:139], a[244:247], v[32:35]\n"
"v_exp_f32 v163, v163\n"
"v_mfma_f32_16x16x32_bf16 v[32:35], v[140:143], a[248:251], v[32:35]\n"
"v_exp_f32 v161, v161\n"
"v_mfma_f32_16x16x32_bf16 v[64:67], v[144:147], a[244:247], v[64:67]\n"
"v_exp_f32 v155, v155\n"
"v_mfma_f32_16x16x32_bf16 v[64:67], v[148:151], a[248:251], v[64:67]\n"
"v_exp_f32 v166, v166\n"
"v_mfma_f32_16x16x32_bf16 v[68:71], v[148:151], v[224:227], v[68:71]\n"
"ds_read_b64_tr_b16 a[250:251], v236 offset:5120\n"
"v_cvt_pk_bf16_f32 v107, v174, v175\n"
"v_mfma_f32_16x16x32_bf16 v[68:71], v[144:147], a[252:255], v[68:71]\n"
"ds_read_b64_tr_b16 a[246:247], v232 offset:5120\n"
"v_cvt_pk_bf16_f32 v98, v168, v169\n"
"v_mfma_f32_16x16x32_bf16 v[36:39], v[140:143], v[224:227], v[36:39]\n"
"v_exp_f32 v179, v179\n"
"v_mfma_f32_16x16x32_bf16 v[36:39], v[136:139], a[252:255], v[36:39]\n"
"ds_read_b64_tr_b16 a[244:245], v232 offset:1024\n"
"v_cvt_pk_bf16_f32 v122, v152, v153\n"
"v_mfma_f32_16x16x32_bf16 v[4:7], v[132:135], v[224:227], v[4:7]\n"
"s_add_u32 s40, s56, s54\n"
"ds_read_b64_tr_b16 a[248:249], v236 offset:1024\n"
"v_mfma_f32_16x16x32_bf16 v[4:7], v[128:131], a[252:255], v[4:7]\n"
"buffer_load_dwordx4 v240, s[16:19], s40 offen offset:3072 nt lds\n"
"v_mfma_f32_16x16x32_bf16 a[212:215], v[124:127], a[252:255], a[212:215]\n"
"v_exp_f32 v162, v162\n"
"v_smfmac_f32_16x16x64_bf16 v[200:203], a[240:243], v[144:151], v241\n"
"s_waitcnt lgkmcnt(4)\n"
"v_cvt_pk_bf16_f32 v114, v176, v177\n"
"v_mfma_f32_16x16x32_bf16 v[168:171], v[208:211], a[0:3], v[180:183]\n"
"v_exp_f32 v157, v157\n"
"v_mfma_f32_16x16x32_bf16 v[168:171], v[212:215], a[4:7], v[168:171]\n"
"v_exp_f32 v165, v165\n"
"v_mfma_f32_16x16x32_bf16 v[168:171], v[216:219], a[8:11], v[168:171]\n"
"ds_read_b64_tr_b16 a[254:255], v233 offset:5120\n"
"ds_read_b64_tr_b16 v[226:227], v237 offset:5120\n"
"v_mfma_f32_16x16x32_bf16 v[168:171], v[220:223], a[12:15], v[168:171]\n"
"ds_read_b64_tr_b16 a[252:253], v233 offset:1024\n"
"v_cvt_pk_bf16_f32 v115, v178, v179\n"
"v_mfma_f32_16x16x32_bf16 v[172:175], v[216:219], a[24:27], v[184:187]\n"
"ds_read_b64_tr_b16 v[224:225], v237 offset:1024\n"
"v_mfma_f32_16x16x32_bf16 v[172:175], v[220:223], a[28:31], v[172:175]\n"
"v_exp_f32 v160, v160\n"
"v_mfma_f32_16x16x32_bf16 v[172:175], v[208:211], a[16:19], v[172:175]\n"
"v_exp_f32 v158, v158\n"
"v_mfma_f32_16x16x32_bf16 v[172:175], v[212:215], a[20:23], v[172:175]\n"
"s_cbranch_vccnz LPCL1_%=\nLPKL1_%=:\n"
"v_mfma_f32_16x16x32_bf16 v[176:179], v[208:211], a[32:35], v[188:191]\n"
"v_exp_f32 v159, v159\n"
"v_mfma_f32_16x16x32_bf16 v[176:179], v[212:215], a[36:39], v[176:179]\n"
"v_exp_f32 v167, v167\n"
"v_mfma_f32_16x16x32_bf16 v[176:179], v[216:219], a[40:43], v[176:179]\n"
"v_exp_f32 v164, v164\n"
"v_mfma_f32_16x16x32_bf16 v[176:179], v[220:223], a[44:47], v[176:179]\n"
"s_waitcnt lgkmcnt(1)\n"
"v_mfma_f32_16x16x32_bf16 a[224:227], v[124:127], a[244:247], a[224:227]\n"
"v_exp_f32 v169, v169\n"
"v_mfma_f32_16x16x32_bf16 v[16:19], v[128:131], a[244:247], v[16:19]\n"
"s_waitcnt vmcnt(8)\n"
"v_exp_f32 v175, v175\n"
"v_mfma_f32_16x16x32_bf16 v[16:19], v[132:135], a[248:251], v[16:19]\n"
"s_barrier\n"
"ds_read_b128 v[220:223], v229 offset:33792\n"
"v_mfma_f32_16x16x32_bf16 v[48:51], v[136:139], a[244:247], v[48:51]\n"
"ds_read_b128 v[216:219], v228 offset:33792\n"
"v_cvt_pk_bf16_f32 v109, v162, v163\n"
"v_mfma_f32_16x16x32_bf16 v[48:51], v[140:143], a[248:251], v[48:51]\n"
"ds_read_b128 v[212:215], v229 offset:32768\n"
"v_mfma_f32_16x16x32_bf16 v[80:83], v[144:147], a[244:247], v[80:83]\n"
"ds_read_b128 v[208:211], v228 offset:32768\n"
"v_cvt_pk_bf16_f32 v101, v158, v159\n"
"v_mfma_f32_16x16x32_bf16 v[80:83], v[148:151], a[248:251], v[80:83]\n"
"s_waitcnt lgkmcnt(4)\n"
"v_exp_f32 v178, v178\n"
"v_mfma_f32_16x16x32_bf16 v[84:87], v[148:151], v[224:227], v[84:87]\n"
"ds_read_b64_tr_b16 a[244:245], v234 offset:16384\n"
"v_cvt_pk_bf16_f32 v117, v166, v167\n"
"v_mfma_f32_16x16x32_bf16 v[84:87], v[144:147], a[252:255], v[84:87]\n"
"ds_read_b64_tr_b16 a[250:251], v238 offset:20480\n"
"v_cvt_pk_bf16_f32 v123, v154, v155\n"
"v_mfma_f32_16x16x32_bf16 v[52:55], v[140:143], v[224:227], v[52:55]\n"
"v_exp_f32 v173, v173\n"
"v_mfma_f32_16x16x32_bf16 v[52:55], v[136:139], a[252:255], v[52:55]\n"
"ds_read_b64_tr_b16 a[248:249], v238 offset:16384\n"
"v_mfma_f32_16x16x32_bf16 v[20:23], v[132:135], v[224:227], v[20:23]\n"
"v_exp_f32 v156, v156\n"
"v_mfma_f32_16x16x32_bf16 v[20:23], v[128:131], a[252:255], v[20:23]\n"
"ds_read_b64_tr_b16 v[226:227], v239 offset:20480\n"
"v_cvt_pk_bf16_f32 v100, v156, v157\n"
"v_mfma_f32_16x16x32_bf16 a[228:231], v[124:127], a[252:255], a[228:231]\n"
"s_waitcnt lgkmcnt(4)\n"
"ds_read_b64_tr_b16 a[246:247], v234 offset:20480\n"
"LAY2_%=:\n"
"v_mfma_f32_16x16x32_bf16 v[152:155], v[208:211], a[0:3], v[180:183]\n"
"ds_read_b64_tr_b16 v[224:225], v239 offset:16384\n"
"v_cvt_pk_bf16_f32 v116, v164, v165\n"
"v_mfma_f32_16x16x32_bf16 v[152:155], v[212:215], a[4:7], v[152:155]\n"
"ds_read_b64_tr_b16 a[252:253], v235 offset:16384\n"
"v_cvt_pk_bf16_f32 v108, v160, v161\n"
"v_mfma_f32_16x16x32_bf16 v[152:155], v[216:219], a[8:11], v[152:155]\n"
"s_add_u32 m0, s44, 114688\n"
"ds_read_b64_tr_b16 a[254:255], v235 offset:20480\n"
"v_mfma_f32_16x16x32_bf16 v[152:155], v[220:223], a[12:15], v[152:155]\n"
"buffer_load_dwordx4 v240, s[20:23], s56 offen nt lds\n"
"v_mfma_f32_16x16x32_bf16 v[156:159], v[216:219], a[24:27], v[184:187]\n"
"v_exp_f32 v177, v177\n"
"v_mfma_f32_16x16x32_bf16 v[156:159], v[220:223], a[28:31], v[156:159]\n"
"v_exp_f32 v168, v168\n"
"v_mfma_f32_16x16x32_bf16 v[156:159], v[208:211], a[16:19], v[156:159]\n"
"v_exp_f32 v179, v179\n"
"v_mfma_f32_16x16x32_bf16 v[156:159], v[212:215], a[20:23], v[156:159]\n"
"v_exp_f32 v174, v174\n"
"v_mfma_f32_16x16x32_bf16 v[160:163], v[208:211], a[32:35], v[188:191]\n"
"v_exp_f32 v172, v172\n"
"v_mfma_f32_16x16x32_bf16 v[160:163], v[212:215], a[36:39], v[160:163]\n"
"v_exp_f32 v171, v171\n"
"v_mfma_f32_16x16x32_bf16 v[160:163], v[216:219], a[40:43], v[160:163]\n"
"v_exp_f32 v154, v154\n"
"v_mfma_f32_16x16x32_bf16 v[160:163], v[220:223], a[44:47], v[160:163]\n"
"v_exp_f32 v170, v170\n"
"v_mfma_f32_16x16x32_bf16 v[164:167], v[216:219], a[200:203], v[192:195]\n"
"ds_read_b128 v[216:219], v228 offset:37888\n"
"v_cvt_pk_bf16_f32 v103, v170, v171\n"
"v_mfma_f32_16x16x32_bf16 v[164:167], v[220:223], a[204:207], v[164:167]\n"
"v_cvt_pk_bf16_f32 v102, v168, v169\n"
"v_cvt_pk_bf16_f32 v119, v178, v179\n"
"v_mfma_f32_16x16x32_bf16 v[164:167], v[208:211], a[192:195], v[164:167]\n"
"s_add_u32 s40, s56, s52\n"
"ds_read_b128 v[208:211], v228 offset:36864\n"
"v_mfma_f32_16x16x32_bf16 v[164:167], v[212:215], a[196:199], v[164:167]\n"
"s_waitcnt lgkmcnt(2)\n"
"buffer_load_dwordx4 v240, s[20:23], s40 offen offset:1024 nt lds\n"
"v_mfma_f32_16x16x32_bf16 a[216:219], v[120:123], a[244:247], a[216:219]\n"
"v_cvt_pk_bf16_f32 v110, v172, v173\n"
"v_cvt_pk_bf16_f32 v111, v174, v175\n"
"v_mfma_f32_16x16x32_bf16 v[8:11], v[96:99], a[244:247], v[8:11]\n"
"s_waitcnt lgkmcnt(2)\n"
"v_exp_f32 v176, v176\n"
"v_mfma_f32_16x16x32_bf16 v[8:11], v[100:103], a[248:251], v[8:11]\n"
"v_exp_f32 v155, v155\n"
"v_mfma_f32_16x16x32_bf16 v[40:43], v[104:107], a[244:247], v[40:43]\n"
"v_exp_f32 v152, v152\n"
"v_mfma_f32_16x16x32_bf16 v[40:43], v[108:111], a[248:251], v[40:43]\n"
"ds_read_b128 v[212:215], v229 offset:36864\n"
"v_cvt_pk_bf16_f32 v118, v176, v177\n"
"v_mfma_f32_16x16x32_bf16 v[72:75], v[112:115], a[244:247], v[72:75]\n"
"v_exp_f32 v158, v158\n"
"v_mfma_f32_16x16x32_bf16 v[72:75], v[116:119], a[248:251], v[72:75]\n"
"v_exp_f32 v163, v163\n"
"v_mfma_f32_16x16x32_bf16 v[76:79], v[116:119], v[224:227], v[76:79]\n"
"ds_read_b128 v[220:223], v229 offset:37888\n"
"v_mfma_f32_16x16x32_bf16 v[76:79], v[112:115], a[252:255], v[76:79]\n"
"v_exp_f32 v159, v159\n"
"v_mfma_f32_16x16x32_bf16 v[44:47], v[108:111], v[224:227], v[44:47]\n"
"s_add_u32 s40, s56, s53\n"
"ds_read_b64_tr_b16 a[244:245], v234 offset:17408\n"
"v_mfma_f32_16x16x32_bf16 v[44:47], v[104:107], a[252:255], v[44:47]\n"
"buffer_load_dwordx4 v240, s[20:23], s40 offen offset:2048 nt lds\n"
"v_mfma_f32_16x16x32_bf16 v[12:15], v[100:103], v[224:227], v[12:15]\n"
"v_exp_f32 v153, v153\n"
"v_mfma_f32_16x16x32_bf16 v[12:15], v[96:99], a[252:255], v[12:15]\n"
"v_exp_f32 v165, v165\n"
"v_mfma_f32_16x16x32_bf16 a[220:223], v[120:123], a[252:255], a[220:223]\n"
"v_exp_f32 v164, v164\n"
"v_smfmac_f32_16x16x64_bf16 v[196:199], a[240:243], v[96:103], v241\n"
"v_cvt_pk_bf16_f32 v129, v154, v155\n"
"v_mfma_f32_16x16x32_bf16 v[202:205], a[240:243], v[120:123], v[202:205]\n"
"s_waitcnt lgkmcnt(1)\n"
"ds_read_b64_tr_b16 a[254:255], v235 offset:21504\n"
"v_mfma_f32_16x16x32_bf16 v[168:171], v[216:219], a[8:11], v[180:183]\n"
"ds_read_b64_tr_b16 a[248:249], v238 offset:17408\n"
"v_mfma_f32_16x16x32_bf16 v[168:171], v[220:223], a[12:15], v[168:171]\n"
"ds_read_b64_tr_b16 a[246:247], v234 offset:21504\n"
"v_mfma_f32_16x16x32_bf16 v[168:171], v[208:211], a[0:3], v[168:171]\n"
"ds_read_b64_tr_b16 v[224:225], v239 offset:17408\n"
"v_cvt_pk_bf16_f32 v124, v164, v165\n"
"v_mfma_f32_16x16x32_bf16 v[168:171], v[212:215], a[4:7], v[168:171]\n"
"v_exp_f32 v166, v166\n"
"v_mfma_f32_16x16x32_bf16 v[172:175], v[208:211], a[16:19], v[184:187]\n"
"ds_read_b64_tr_b16 a[252:253], v235 offset:17408\n"
"v_cvt_pk_bf16_f32 v128, v152, v153\n"
"v_mfma_f32_16x16x32_bf16 v[172:175], v[212:215], a[20:23], v[172:175]\n"
"ds_read_b64_tr_b16 v[226:227], v239 offset:21504\n"
"v_cvt_pk_bf16_f32 v137, v158, v159\n"
"v_mfma_f32_16x16x32_bf16 v[172:175], v[216:219], a[24:27], v[172:175]\n"
"s_add_u32 s40, s56, s54\n"
"ds_read_b64_tr_b16 a[250:251], v238 offset:21504\n"
"v_mfma_f32_16x16x32_bf16 v[172:175], v[220:223], a[28:31], v[172:175]\n"
"buffer_load_dwordx4 v240, s[20:23], s40 offen offset:3072 nt lds\n"
"v_mfma_f32_16x16x32_bf16 v[176:179], v[216:219], a[40:43], v[188:191]\n"
"s_add_u32 s55, s55, s59\n"
"v_exp_f32 v167, v167\n"
"v_mfma_f32_16x16x32_bf16 v[176:179], v[220:223], a[44:47], v[176:179]\n"
"s_add_u32 s41, s55, s57\n"
"v_exp_f32 v161, v161\n"
"v_mfma_f32_16x16x32_bf16 v[176:179], v[208:211], a[32:35], v[176:179]\n"
"s_and_b32 s41, s41, s58\n"
"v_exp_f32 v157, v157\n"
"v_mfma_f32_16x16x32_bf16 v[176:179], v[212:215], a[36:39], v[176:179]\n"
"s_andn2_b32 s56, s55, s58\n"
"v_exp_f32 v160, v160\n"
"v_mfma_f32_16x16x32_bf16 v[152:155], v[208:211], a[192:195], v[192:195]\n"
"s_or_b32 s56, s56, s41\n"
"ds_read_b128 v[208:211], v230 offset:32768\n"
"v_mfma_f32_16x16x32_bf16 v[152:155], v[212:215], a[196:199], v[152:155]\n"
"s_cmp_ge_u32 s47, 15\n"
"v_exp_f32 v172, v172\n"
"v_mfma_f32_16x16x32_bf16 v[152:155], v[216:219], a[200:203], v[152:155]\n"
"s_cbranch_scc1 LSW_%=\nLSWK_%=:\n"
"ds_read_b128 v[216:219], v230 offset:33792\n"
"v_mfma_f32_16x16x32_bf16 v[152:155], v[220:223], a[204:207], v[152:155]\n"
"s_waitcnt lgkmcnt(2)\n"
"v_exp_f32 v162, v162\n"
"v_mfma_f32_16x16x32_bf16 a[232:235], v[120:123], a[244:247], a[232:235]\n"
"ds_read_b128 v[212:215], v231 offset:32768\n"
"v_mfma_f32_16x16x32_bf16 v[24:27], v[96:99], a[244:247], v[24:27]\n"
"s_mov_b32 m0, s44\n"
"ds_read_b128 v[220:223], v231 offset:33792\n"
"v_mfma_f32_16x16x32_bf16 v[24:27], v[100:103], a[248:251], v[24:27]\n"
"buffer_load_dwordx4 v240, s[16:19], s56 offen nt lds\n"
"v_mfma_f32_16x16x32_bf16 v[56:59], v[104:107], a[244:247], v[56:59]\n"
"v_exp_f32 v171, v171\n"
"v_mfma_f32_16x16x32_bf16 v[56:59], v[108:111], a[248:251], v[56:59]\n"
"v_exp_f32 v169, v169\n"
"v_mfma_f32_16x16x32_bf16 v[88:91], v[112:115], a[244:247], v[88:91]\n"
"v_exp_f32 v154, v154\n"
"v_mfma_f32_16x16x32_bf16 v[88:91], v[116:119], a[248:251], v[88:91]\n"
"v_exp_f32 v156, v156\n"
"v_mfma_f32_16x16x32_bf16 v[92:95], v[116:119], v[224:227], v[92:95]\n"
"ds_read_b64_tr_b16 a[244:245], v232 offset:16384\n"
"v_cvt_pk_bf16_f32 v145, v162, v163\n"
"v_mfma_f32_16x16x32_bf16 v[92:95], v[112:115], a[252:255], v[92:95]\n"
"ds_read_b64_tr_b16 a[250:251], v236 offset:20480\n"
"v_cvt_pk_bf16_f32 v136, v156, v157\n"
"v_mfma_f32_16x16x32_bf16 v[60:63], v[108:111], v[224:227], v[60:63]\n"
"ds_read_b64_tr_b16 a[246:247], v232 offset:20480\n"
"v_cvt_pk_bf16_f32 v144, v160, v161\n"
"v_mfma_f32_16x16x32_bf16 v[60:63], v[104:107], a[252:255], v[60:63]\n"
"v_exp_f32 v152, v152\n"
"v_mfma_f32_16x16x32_bf16 v[28:31], v[100:103], v[224:227], v[28:31]\n"
"ds_read_b64_tr_b16 v[224:225], v237 offset:16384\n"
"ds_read_b64_tr_b16 v[226:227], v237 offset:20480\n"
"v_mfma_f32_16x16x32_bf16 v[28:31], v[96:99], a[252:255], v[28:31]\n"
"ds_read_b64_tr_b16 a[248:249], v236 offset:16384\n"
"v_cvt_pk_bf16_f32 v125, v166, v167\n"
"v_mfma_f32_16x16x32_bf16 a[236:239], v[120:123], a[252:255], a[236:239]\n"
"s_add_u32 s40, s56, s52\n"
"ds_read_b64_tr_b16 a[252:253], v233 offset:16384\n"
"v_smfmac_f32_16x16x64_bf16 v[198:201], a[240:243], v[104:111], v241\n"
"s_waitcnt lgkmcnt(7)\n"
"buffer_load_dwordx4 v240, s[16:19], s40 offen offset:1024 nt lds\n"
"v_mfma_f32_16x16x32_bf16 v[156:159], v[208:211], a[0:3], v[180:183]\n"
"v_exp_f32 v175, v175\n"
"v_mfma_f32_16x16x32_bf16 v[156:159], v[212:215], a[4:7], v[156:159]\n"
"v_exp_f32 v173, v173\n"
"v_mfma_f32_16x16x32_bf16 v[156:159], v[216:219], a[8:11], v[156:159]\n"
"v_exp_f32 v168, v168\n"
"v_mfma_f32_16x16x32_bf16 v[156:159], v[220:223], a[12:15], v[156:159]\n"
"ds_read_b64_tr_b16 a[254:255], v233 offset:20480\n"
"v_mfma_f32_16x16x32_bf16 v[160:163], v[216:219], a[24:27], v[184:187]\n"
"v_exp_f32 v170, v170\n"
"v_mfma_f32_16x16x32_bf16 v[160:163], v[220:223], a[28:31], v[160:163]\n"
"v_exp_f32 v178, v178\n"
"v_mfma_f32_16x16x32_bf16 v[160:163], v[208:211], a[16:19], v[160:163]\n"
"v_exp_f32 v177, v177\n"
"v_mfma_f32_16x16x32_bf16 v[160:163], v[212:215], a[20:23], v[160:163]\n"
"v_exp_f32 v176, v176\n"
"v_mfma_f32_16x16x32_bf16 v[164:167], v[208:211], a[32:35], v[188:191]\n"
"ds_read_b128 v[208:211], v230 offset:36864\n"
"v_cvt_pk_bf16_f32 v131, v170, v171\n"
"v_mfma_f32_16x16x32_bf16 v[164:167], v[212:215], a[36:39], v[164:167]\n"
"ds_read_b128 v[212:215], v231 offset:36864\n"
"v_mfma_f32_16x16x32_bf16 v[164:167], v[216:219], a[40:43], v[164:167]\n"
"v_exp_f32 v153, v153\n"
"v_mfma_f32_16x16x32_bf16 v[164:167], v[220:223], a[44:47], v[164:167]\n"
"s_waitcnt lgkmcnt(2)\n"
"ds_read_b128 v[220:223], v231 offset:37888\n"
"v_cvt_pk_bf16_f32 v138, v172, v173\n"
"v_mfma_f32_16x16x32_bf16 a[208:211], v[120:123], a[244:247], a[208:211]\n"
"v_exp_f32 v174, v174\n"
"v_mfma_f32_16x16x32_bf16 v[0:3], v[96:99], a[244:247], v[0:3]\n"
"s_add_u32 s40, s56, s53\n"
"ds_read_b128 v[216:219], v230 offset:37888\n"
"v_mfma_f32_16x16x32_bf16 v[0:3], v[100:103], a[248:251], v[0:3]\n"
"buffer_load_dwordx4 v240, s[16:19], s40 offen offset:2048 nt lds\n"
"v_mfma_f32_16x16x32_bf16 v[32:35], v[104:107], a[244:247], v[32:35]\n"
"v_exp_f32 v163, v163\n"
"v_mfma_f32_16x16x32_bf16 v[32:35], v[108:111], a[248:251], v[32:35]\n"
"v_exp_f32 v161, v161\n"
"v_mfma_f32_16x16x32_bf16 v[64:67], v[112:115], a[244:247], v[64:67]\n"
"v_exp_f32 v155, v155\n"
"v_mfma_f32_16x16x32_bf16 v[64:67], v[116:119], a[248:251], v[64:67]\n"
"v_exp_f32 v166, v166\n"
"v_mfma_f32_16x16x32_bf16 v[68:71], v[116:119], v[224:227], v[68:71]\n"
"ds_read_b64_tr_b16 a[250:251], v236 offset:21504\n"
"v_cvt_pk_bf16_f32 v139, v174, v175\n"
"v_mfma_f32_16x16x32_bf16 v[68:71], v[112:115], a[252:255], v[68:71]\n"
"ds_read_b64_tr_b16 a[246:247], v232 offset:21504\n"
"v_cvt_pk_bf16_f32 v130, v168, v169\n"
"v_mfma_f32_16x16x32_bf16 v[36:39], v[108:111], v[224:227], v[36:39]\n"
"v_exp_f32 v179, v179\n"
"v_mfma_f32_16x16x32_bf16 v[36:39], v[104:107], a[252:255], v[36:39]\n"
"ds_read_b64_tr_b16 a[244:245], v232 offset:17408\n"
"v_cvt_pk_bf16_f32 v126, v152, v153\n"
"v_mfma_f32_16x16x32_bf16 v[4:7], v[100:103], v[224:227], v[4:7]\n"
"s_add_u32 s40, s56, s54\n"
"ds_read_b64_tr_b16 a[248:249], v236 offset:17408\n"
"v_mfma_f32_16x16x32_bf16 v[4:7], v[96:99], a[252:255], v[4:7]\n"
"buffer_load_dwordx4 v240, s[16:19], s40 offen offset:3072 nt lds\n"
"v_mfma_f32_16x16x32_bf16 a[212:215], v[120:123], a[252:255], a[212:215]\n"
"v_exp_f32 v162, v162\n"
"v_smfmac_f32_16x16x64_bf16 v[200:203], a[240:243], v[112:119], v241\n"
"s_waitcnt lgkmcnt(4)\n"
"v_cvt_pk_bf16_f32 v146, v176, v177\n"
"v_mfma_f32_16x16x32_bf16 v[168:171], v[208:211], a[0:3], v[180:183]\n"
"v_exp_f32 v157, v157\n"
"v_mfma_f32_16x16x32_bf16 v[168:171], v[212:215], a[4:7], v[168:171]\n"
"v_exp_f32 v165, v165\n"
"v_mfma_f32_16x16x32_bf16 v[168:171], v[216:219], a[8:11], v[168:171]\n"
"ds_read_b64_tr_b16 a[254:255], v233 offset:21504\n"
"ds_read_b64_tr_b16 v[226:227], v237 offset:21504\n"
"v_mfma_f32_16x16x32_bf16 v[168:171], v[220:223], a[12:15], v[168:171]\n"
"ds_read_b64_tr_b16 a[252:253], v233 offset:17408\n"
"v_cvt_pk_bf16_f32 v147, v178, v179\n"
"v_mfma_f32_16x16x32_bf16 v[172:175], v[216:219], a[24:27], v[184:187]\n"
"ds_read_b64_tr_b16 v[224:225], v237 offset:17408\n"
"v_mfma_f32_16x16x32_bf16 v[172:175], v[220:223], a[28:31], v[172:175]\n"
"v_exp_f32 v160, v160\n"
"v_mfma_f32_16x16x32_bf16 v[172:175], v[208:211], a[16:19], v[172:175]\n"
"v_exp_f32 v158, v158\n"
"v_mfma_f32_16x16x32_bf16 v[172:175], v[212:215], a[20:23], v[172:175]\n"
"s_cbranch_vccnz LPCL2_%=\nLPKL2_%=:\n"
"v_mfma_f32_16x16x32_bf16 v[176:179], v[208:211], a[32:35], v[188:191]\n"
"v_exp_f32 v159, v159\n"
"v_mfma_f32_16x16x32_bf16 v[176:179], v[212:215], a[36:39], v[176:179]\n"
"v_exp_f32 v167, v167\n"
"v_mfma_f32_16x16x32_bf16 v[176:179], v[216:219], a[40:43], v[176:179]\n"
"v_exp_f32 v164, v164\n"
"v_mfma_f32_16x16x32_bf16 v[176:179], v[220:223], a[44:47], v[176:179]\n"
"s_waitcnt lgkmcnt(1)\n"
"v_mfma_f32_16x16x32_bf16 a[224:227], v[120:123], a[244:247], a[224:227]\n"
"v_exp_f32 v169, v169\n"
"v_mfma_f32_16x16x32_bf16 v[16:19], v[96:99], a[244:247], v[16:19]\n"
"s_waitcnt vmcnt(8)\n"
"v_exp_f32 v175, v175\n"
"v_mfma_f32_16x16x32_bf16 v[16:19], v[100:103], a[248:251], v[16:19]\n"
"s_barrier\n"
"ds_read_b128 v[220:223], v229 offset:50176\n"
"v_mfma_f32_16x16x32_bf16 v[48:51], v[104:107], a[244:247], v[48:51]\n"
"ds_read_b128 v[216:219], v228 offset:50176\n"
"v_cvt_pk_bf16_f32 v141, v162, v163\n"
"v_mfma_f32_16x16x32_bf16 v[48:51], v[108:111], a[248:251], v[48:51]\n"
"ds_read_b128 v[212:215], v229 offset:49152\n"
"v_mfma_f32_16x16x32_bf16 v[80:83], v[112:115], a[244:247], v[80:83]\n"
"ds_read_b128 v[208:211], v228 offset:49152\n"
"v_cvt_pk_bf16_f32 v133, v158, v159\n"
"v_mfma_f32_16x16x32_bf16 v[80:83], v[116:119], a[248:251], v[80:83]\n"
"s_waitcnt lgkmcnt(4)\n"
"v_exp_f32 v178, v178\n"
"v_mfma_f32_16x16x32_bf16 v[84:87], v[116:119], v[224:227], v[84:87]\n"
"ds_read_b64_tr_b16 a[244:245], v234 offset:32768\n"
"v_cvt_pk_bf16_f32 v149, v166, v167\n"
"v_mfma_f32_16x16x32_bf16 v[84:87], v[112:115], a[252:255], v[84:87]\n"
"ds_read_b64_tr_b16 a[250:251], v238 offset:36864\n"
"v_cvt_pk_bf16_f32 v127, v154, v155\n"
"v_mfma_f32_16x16x32_bf16 v[52:55], v[108:111], v[224:227], v[52:55]\n"
"v_exp_f32 v173, v173\n"
"v_mfma_f32_16x16x32_bf16 v[52:55], v[104:107], a[252:255], v[52:55]\n"
"ds_read_b64_tr_b16 a[248:249], v238 offset:32768\n"
"v_mfma_f32_16x16x32_bf16 v[20:23], v[100:103], v[224:227], v[20:23]\n"
"v_exp_f32 v156, v156\n"
"v_mfma_f32_16x16x32_bf16 v[20:23], v[96:99], a[252:255], v[20:23]\n"
"ds_read_b64_tr_b16 v[226:227], v239 offset:36864\n"
"v_cvt_pk_bf16_f32 v132, v156, v157\n"
"v_mfma_f32_16x16x32_bf16 a[228:231], v[120:123], a[252:255], a[228:231]\n"
"s_waitcnt lgkmcnt(4)\n"
"ds_read_b64_tr_b16 a[246:247], v234 offset:36864\n"
"v_mfma_f32_16x16x32_bf16 v[152:155], v[208:211], a[0:3], v[180:183]\n"
"ds_read_b64_tr_b16 v[224:225], v239 offset:32768\n"
"v_cvt_pk_bf16_f32 v148, v164, v165\n"
"v_mfma_f32_16x16x32_bf16 v[152:155], v[212:215], a[4:7], v[152:155]\n"
"ds_read_b64_tr_b16 a[252:253], v235 offset:32768\n"
"v_cvt_pk_bf16_f32 v140, v160, v161\n"
"v_mfma_f32_16x16x32_bf16 v[152:155], v[216:219], a[8:11], v[152:155]\n"
"s_add_u32 m0, s44, 65536\n"
"ds_read_b64_tr_b16 a[254:255], v235 offset:36864\n"
"v_mfma_f32_16x16x32_bf16 v[152:155], v[220:223], a[12:15], v[152:155]\n"
"buffer_load_dwordx4 v240, s[20:23], s56 offen nt lds\n"
"v_mfma_f32_16x16x32_bf16 v[156:159], v[216:219], a[24:27], v[184:187]\n"
"v_exp_f32 v177, v177\n"
"v_mfma_f32_16x16x32_bf16 v[156:159], v[220:223], a[28:31], v[156:159]\n"
"v_exp_f32 v168, v168\n"
"v_mfma_f32_16x16x32_bf16 v[156:159], v[208:211], a[16:19], v[156:159]\n"
"v_exp_f32 v179, v179\n"
"v_mfma_f32_16x16x32_bf16 v[156:159], v[212:215], a[20:23], v[156:159]\n"
"v_exp_f32 v174, v174\n"
"v_mfma_f32_16x16x32_bf16 v[160:163], v[208:211], a[32:35], v[188:191]\n"
"v_exp_f32 v172, v172\n"
"v_mfma_f32_16x16x32_bf16 v[160:163], v[212:215], a[36:39], v[160:163]\n"
"v_exp_f32 v171, v171\n"
"v_mfma_f32_16x16x32_bf16 v[160:163], v[216:219], a[40:43], v[160:163]\n"
"v_exp_f32 v154, v154\n"
"v_mfma_f32_16x16x32_bf16 v[160:163], v[220:223], a[44:47], v[160:163]\n"
"v_exp_f32 v170, v170\n"
"v_mfma_f32_16x16x32_bf16 v[164:167], v[216:219], a[200:203], v[192:195]\n"
"ds_read_b128 v[216:219], v228 offset:54272\n"
"v_cvt_pk_bf16_f32 v135, v170, v171\n"
"v_mfma_f32_16x16x32_bf16 v[164:167], v[220:223], a[204:207], v[164:167]\n"
"v_cvt_pk_bf16_f32 v134, v168, v169\n"
"v_cvt_pk_bf16_f32 v151, v178, v179\n"
"v_mfma_f32_16x16x32_bf16 v[164:167], v[208:211], a[192:195], v[164:167]\n"
"s_add_u32 s40, s56, s52\n"
"ds_read_b128 v[208:211], v228 offset:53248\n"
"v_mfma_f32_16x16x32_bf16 v[164:167], v[212:215], a[196:199], v[164:167]\n"
"s_waitcnt lgkmcnt(2)\n"
"buffer_load_dwordx4 v240, s[20:23], s40 offen offset:1024 nt lds\n"
"v_mfma_f32_16x16x32_bf16 a[216:219], v[124:127], a[244:247], a[216:219]\n"
"v_cvt_pk_bf16_f32 v142, v172, v173\n"
"v_cvt_pk_bf16_f32 v143, v174, v175\n"
"v_mfma_f32_16x16x32_bf16 v[8:11], v[128:131], a[244:247], v[8:11]\n"
"v_exp_f32 v176, v176\n"
"v_mfma_f32_16x16x32_bf16 v[8:11], v[132:135], a[248:251], v[8:11]\n"
"v_exp_f32 v155, v155\n"
"v_mfma_f32_16x16x32_bf16 v[40:43], v[136:139], a[244:247], v[40:43]\n"
"v_exp_f32 v152, v152\n"
"v_mfma_f32_16x16x32_bf16 v[40:43], v[140:143], a[248:251], v[40:43]\n"
"ds_read_b128 v[212:215], v229 offset:53248\n"
"v_cvt_pk_bf16_f32 v150, v176, v177\n"
"v_mfma_f32_16x16x32_bf16 v[72:75], v[144:147], a[244:247], v[72:75]\n"
"v_exp_f32 v158, v158\n"
"v_mfma_f32_16x16x32_bf16 v[72:75], v[148:151], a[248:251], v[72:75]\n"
"v_exp_f32 v163, v163\n"
"v_mfma_f32_16x16x32_bf16 v[76:79], v[148:151], v[224:227], v[76:79]\n"
"ds_read_b128 v[220:223], v229 offset:54272\n"
"v_mfma_f32_16x16x32_bf16 v[76:79], v[144:147], a[252:255], v[76:79]\n"
"v_exp_f32 v159, v159\n"
"v_mfma_f32_16x16x32_bf16 v[44:47], v[140:143], v[224:227], v[44:47]\n"
"s_add_u32 s40, s56, s53\n"
"ds_read_b64_tr_b16 a[244:245], v234 offset:33792\n"
"v_mfma_f32_16x16x32_bf16 v[44:47], v[136:139], a[252:255], v[44:47]\n"
"buffer_load_dwordx4 v240, s[20:23], s40 offen offset:2048 nt lds\n"
"v_mfma_f32_16x16x32_bf16 v[12:15], v[132:135], v[224:227], v[12:15]\n"
"v_exp_f32 v153, v153\n"
"v_mfma_f32_16x16x32_bf16 v[12:15], v[128:131], a[252:255], v[12:15]\n"
"v_exp_f32 v165, v165\n"
"v_mfma_f32_16x16x32_bf16 a[220:223], v[124:127], a[252:255], a[220:223]\n"
"v_exp_f32 v164, v164\n"
"v_smfmac_f32_16x16x64_bf16 v[196:199], a[240:243], v[128:135], v241\n"
"v_cvt_pk_bf16_f32 v97, v154, v155\n"
"v_mfma_f32_16x16x32_bf16 v[202:205], a[240:243], v[124:127], v[202:205]\n"
"s_waitcnt lgkmcnt(1)\n"
"ds_read_b64_tr_b16 a[254:255], v235 offset:37888\n"
"v_mfma_f32_16x16x32_bf16 v[168:171], v[216:219], a[8:11], v[180:183]\n"
"ds_read_b64_tr_b16 a[248:249], v238 offset:33792\n"
"v_mfma_f32_16x16x32_bf16 v[168:171], v[220:223], a[12:15], v[168:171]\n"
"ds_read_b64_tr_b16 a[246:247], v234 offset:37888\n"
"v_mfma_f32_16x16x32_bf16 v[168:171], v[208:211], a[0:3], v[168:171]\n"
"ds_read_b64_tr_b16 v[224:225], v239 offset:33792\n"
"v_cvt_pk_bf16_f32 v120, v164, v165\n"
"v_mfma_f32_16x16x32_bf16 v[168:171], v[212:215], a[4:7], v[168:171]\n"
"v_exp_f32 v166, v166\n"
"v_mfma_f32_16x16x32_bf16 v[172:175], v[208:211], a[16:19], v[184:187]\n"
"ds_read_b64_tr_b16 a[252:253], v235 offset:33792\n"
"v_cvt_pk_bf16_f32 v96, v152, v153\n"
"v_mfma_f32_16x16x32_bf16 v[172:175], v[212:215], a[20:23], v[172:175]\n"
"ds_read_b64_tr_b16 v[226:227], v239 offset:37888\n"
"v_cvt_pk_bf16_f32 v105, v158, v159\n"
"v_mfma_f32_16x16x32_bf16 v[172:175], v[216:219], a[24:27], v[172:175]\n"
"s_add_u32 s40, s56, s54\n"
"ds_read_b64_tr_b16 a[250:251], v238 offset:37888\n"
"v_mfma_f32_16x16x32_bf16 v[172:175], v[220:223], a[28:31], v[172:175]\n"
"buffer_load_dwordx4 v240, s[20:23], s40 offen offset:3072 nt lds\n"
"v_mfma_f32_16x16x32_bf16 v[176:179], v[216:219], a[40:43], v[188:191]\n"
"s_add_u32 s55, s55, s59\n"
"v_exp_f32 v167, v167\n"
"v_mfma_f32_16x16x32_bf16 v[176:179], v[220:223], a[44:47], v[176:179]\n"
"s_add_u32 s41, s55, s57\n"
"v_exp_f32 v161, v161\n"
"v_mfma_f32_16x16x32_bf16 v[176:179], v[208:211], a[32:35], v[176:179]\n"
"s_and_b32 s41, s41, s58\n"
"v_exp_f32 v157, v157\n"
"v_mfma_f32_16x16x32_bf16 v[176:179], v[212:215], a[36:39], v[176:179]\n"
"s_andn2_b32 s56, s55, s58\n"
"v_exp_f32 v160, v160\n"
"v_mfma_f32_16x16x32_bf16 v[152:155], v[208:211], a[192:195], v[192:195]\n"
"s_or_b32 s56, s56, s41\n"
"ds_read_b128 v[208:211], v230 offset:49152\n"
"v_mfma_f32_16x16x32_bf16 v[152:155], v[212:215], a[196:199], v[152:155]\n"
"v_exp_f32 v172, v172\n"
"v_mfma_f32_16x16x32_bf16 v[152:155], v[216:219], a[200:203], v[152:155]\n"
"ds_read_b128 v[216:219], v230 offset:50176\n"
"v_mfma_f32_16x16x32_bf16 v[152:155], v[220:223], a[204:207], v[152:155]\n"
"s_waitcnt lgkmcnt(2)\n"
"v_exp_f32 v162, v162\n"
"v_mfma_f32_16x16x32_bf16 a[232:235], v[124:127], a[244:247], a[232:235]\n"
"ds_read_b128 v[212:215], v231 offset:49152\n"
"v_mfma_f32_16x16x32_bf16 v[24:27], v[128:131], a[244:247], v[24:27]\n"
"s_add_u32 m0, s44, 16384\n"
"ds_read_b128 v[220:223], v231 offset:50176\n"
"v_mfma_f32_16x16x32_bf16 v[24:27], v[132:135], a[248:251], v[24:27]\n"
"buffer_load_dwordx4 v240, s[16:19], s56 offen nt lds\n"
"v_mfma_f32_16x16x32_bf16 v[56:59], v[136:139], a[244:247], v[56:59]\n"
"v_exp_f32 v171, v171\n"
"v_mfma_f32_16x16x32_bf16 v[56:59], v[140:143], a[248:251], v[56:59]\n"
"v_exp_f32 v169, v169\n"
"v_mfma_f32_16x16x32_bf16 v[88:91], v[144:147], a[244:247], v[88:91]\n"
"v_exp_f32 v154, v154\n"
"v_mfma_f32_16x16x32_bf16 v[88:91], v[148:151], a[248:251], v[88:91]\n"
"v_exp_f32 v156, v156\n"
"v_mfma_f32_16x16x32_bf16 v[92:95], v[148:151], v[224:227], v[92:95]\n"
"ds_read_b64_tr_b16 a[244:245], v232 offset:32768\n"
"v_cvt_pk_bf16_f32 v113, v162, v163\n"
"v_mfma_f32_16x16x32_bf16 v[92:95], v[144:147], a[252:255], v[92:95]\n"
"ds_read_b64_tr_b16 a[250:251], v236 offset:36864\n"
"v_cvt_pk_bf16_f32 v104, v156, v157\n"
"v_mfma_f32_16x16x32_bf16 v[60:63], v[140:143], v[224:227], v[60:63]\n"
"ds_read_b64_tr_b16 a[246:247], v232 offset:36864\n"
"v_cvt_pk_bf16_f32 v112, v160, v161\n"
"v_mfma_f32_16x16x32_bf16 v[60:63], v[136:139], a[252:255], v[60:63]\n"
"v_exp_f32 v152, v152\n"
"v_mfma_f32_16x16x32_bf16 v[28:31], v[132:135], v[224:227], v[28:31]\n"
"ds_read_b64_tr_b16 v[224:225], v237 offset:32768\n"
"ds_read_b64_tr_b16 v[226:227], v237 offset:36864\n"
"v_mfma_f32_16x16x32_bf16 v[28:31], v[128:131], a[252:255], v[28:31]\n"
"ds_read_b64_tr_b16 a[248:249], v236 offset:32768\n"
"v_cvt_pk_bf16_f32 v121, v166, v167\n"
"v_mfma_f32_16x16x32_bf16 a[236:239], v[124:127], a[252:255], a[236:239]\n"
"s_add_u32 s40, s56, s52\n"
"ds_read_b64_tr_b16 a[252:253], v233 offset:32768\n"
"v_smfmac_f32_16x16x64_bf16 v[198:201], a[240:243], v[136:143], v241\n"
"s_waitcnt lgkmcnt(7)\n"
"buffer_load_dwordx4 v240, s[16:19], s40 offen offset:1024 nt lds\n"
"v_mfma_f32_16x16x32_bf16 v[156:159], v[208:211], a[0:3], v[180:183]\n"
"v_exp_f32 v175, v175\n"
"v_mfma_f32_16x16x32_bf16 v[156:159], v[212:215], a[4:7], v[156:159]\n"
"v_exp_f32 v173, v173\n"
"v_mfma_f32_16x16x32_bf16 v[156:159], v[216:219], a[8:11], v[156:159]\n"
"v_exp_f32 v168, v168\n"
"v_mfma_f32_16x16x32_bf16 v[156:159], v[220:223], a[12:15], v[156:159]\n"
"ds_read_b64_tr_b16 a[254:255], v233 offset:36864\n"
"v_mfma_f32_16x16x32_bf16 v[160:163], v[216:219], a[24:27], v[184:187]\n"
"v_exp_f32 v170, v170\n"
"v_mfma_f32_16x16x32_bf16 v[160:163], v[220:223], a[28:31], v[160:163]\n"
"v_exp_f32 v178, v178\n"
"v_mfma_f32_16x16x32_bf16 v[160:163], v[208:211], a[16:19], v[160:163]\n"
"v_exp_f32 v177, v177\n"
"v_mfma_f32_16x16x32_bf16 v[160:163], v[212:215], a[20:23], v[160:163]\n"
"v_exp_f32 v176, v176\n"
"v_mfma_f32_16x16x32_bf16 v[164:167], v[208:211], a[32:35], v[188:191]\n"
"ds_read_b128 v[208:211], v230 offset:53248\n"
"v_cvt_pk_bf16_f32 v99, v170, v171\n"
"v_mfma_f32_16x16x32_bf16 v[164:167], v[212:215], a[36:39], v[164:167]\n"
"ds_read_b128 v[212:215], v231 offset:53248\n"
"v_mfma_f32_16x16x32_bf16 v[164:167], v[216:219], a[40:43], v[164:167]\n"
"v_exp_f32 v153, v153\n"
"v_mfma_f32_16x16x32_bf16 v[164:167], v[220:223], a[44:47], v[164:167]\n"
"s_waitcnt lgkmcnt(2)\n"
"ds_read_b128 v[220:223], v231 offset:54272\n"
"v_cvt_pk_bf16_f32 v106, v172, v173\n"
"v_mfma_f32_16x16x32_bf16 a[208:211], v[124:127], a[244:247], a[208:211]\n"
"v_exp_f32 v174, v174\n"
"v_mfma_f32_16x16x32_bf16 v[0:3], v[128:131], a[244:247], v[0:3]\n"
"s_add_u32 s40, s56, s53\n"
"ds_read_b128 v[216:219], v230 offset:54272\n"
"v_mfma_f32_16x16x32_bf16 v[0:3], v[132:135], a[248:251], v[0:3]\n"
"buffer_load_dwordx4 v240, s[16:19], s40 offen offset:2048 nt lds\n"
"v_mfma_f32_16x16x32_bf16 v[32:35], v[136:139], a[244:247], v[32:35]\n"
"v_exp_f32 v163, v163\n"
"v_mfma_f32_16x16x32_bf16 v[32:35], v[140:143], a[248:251], v[32:35]\n"
"v_exp_f32 v161, v161\n"
"v_mfma_f32_16x16x32_bf16 v[64:67], v[144:147], a[244:247], v[64:67]\n"
"v_exp_f32 v155, v155\n"
"v_mfma_f32_16x16x32_bf16 v[64:67], v[148:151], a[248:251], v[64:67]\n"
"v_exp_f32 v166, v166\n"
"v_mfma_f32_16x16x32_bf16 v[68:71], v[148:151], v[224:227], v[68:71]\n"
"ds_read_b64_tr_b16 a[250:251], v236 offset:37888\n"
"v_cvt_pk_bf16_f32 v107, v174, v175\n"
"v_mfma_f32_16x16x32_bf16 v[68:71], v[144:147], a[252:255], v[68:71]\n"
"ds_read_b64_tr_b16 a[246:247], v232 offset:37888\n"
"v_cvt_pk_bf16_f32 v98, v168, v169\n"
"v_mfma_f32_16x16x32_bf16 v[36:39], v[140:143], v[224:227], v[36:39]\n"
"v_exp_f32 v179, v179\n"
"v_mfma_f32_16x16x32_bf16 v[36:39], v[136:139], a[252:255], v[36:39]\n"
"ds_read_b64_tr_b16 a[244:245], v232 offset:33792\n"
"v_cvt_pk_bf16_f32 v122, v152, v153\n"
"v_mfma_f32_16x16x32_bf16 v[4:7], v[132:135], v[224:227], v[4:7]\n"
"s_add_u32 s40, s56, s54\n"
"ds_read_b64_tr_b16 a[248:249], v236 offset:33792\n"
"v_mfma_f32_16x16x32_bf16 v[4:7], v[128:131], a[252:255], v[4:7]\n"
"buffer_load_dwordx4 v240, s[16:19], s40 offen offset:3072 nt lds\n"
"v_mfma_f32_16x16x32_bf16 a[212:215], v[124:127], a[252:255], a[212:215]\n"
"v_exp_f32 v162, v162\n"
"v_smfmac_f32_16x16x64_bf16 v[200:203], a[240:243], v[144:151], v241\n"
"s_waitcnt lgkmcnt(4)\n"
"v_cvt_pk_bf16_f32 v114, v176, v177\n"
"v_mfma_f32_16x16x32_bf16 v[168:171], v[208:211], a[0:3], v[180:183]\n"
"v_exp_f32 v157, v157\n"
"v_mfma_f32_16x16x32_bf16 v[168:171], v[212:215], a[4:7], v[168:171]\n"
"v_exp_f32 v165, v165\n"
"v_mfma_f32_16x16x32_bf16 v[168:171], v[216:219], a[8:11], v[168:171]\n"
"ds_read_b64_tr_b16 a[254:255], v233 offset:37888\n"
"ds_read_b64_tr_b16 v[226:227], v237 offset:37888\n"
"v_mfma_f32_16x16x32_bf16 v[168:171], v[220:223], a[12:15], v[168:171]\n"
"ds_read_b64_tr_b16 a[252:253], v233 offset:33792\n"
"v_cvt_pk_bf16_f32 v115, v178, v179\n"
"v_mfma_f32_16x16x32_bf16 v[172:175], v[216:219], a[24:27], v[184:187]\n"
"ds_read_b64_tr_b16 v[224:225], v237 offset:33792\n"
"v_mfma_f32_16x16x32_bf16 v[172:175], v[220:223], a[28:31], v[172:175]\n"
"v_exp_f32 v160, v160\n"
"v_mfma_f32_16x16x32_bf16 v[172:175], v[208:211], a[16:19], v[172:175]\n"
"v_exp_f32 v158, v158\n"
"v_mfma_f32_16x16x32_bf16 v[172:175], v[212:215], a[20:23], v[172:175]\n"
"s_cbranch_vccnz LPCL3_%=\nLPKL3_%=:\n"
"v_mfma_f32_16x16x32_bf16 v[176:179], v[208:211], a[32:35], v[188:191]\n"
"s_add_u32 s47, s47, 1\n"
"v_exp_f32 v159, v159\n"
"v_mfma_f32_16x16x32_bf16 v[176:179], v[212:215], a[36:39], v[176:179]\n"
"v_exp_f32 v167, v167\n"
"v_mfma_f32_16x16x32_bf16 v[176:179], v[216:219], a[40:43], v[176:179]\n"
"s_and_b32 s40, s47, 3\n"
"v_exp_f32 v164, v164\n"
"v_mfma_f32_16x16x32_bf16 v[176:179], v[220:223], a[44:47], v[176:179]\n"
"s_waitcnt lgkmcnt(1)\n"
"s_cbranch_scc1 LAL_%=\n"
"s_cmp_eq_u32 s47, 8\n"
"s_cbranch_scc1 LTH1_%=\n"
"s_cmp_eq_u32 s47, 12\n"
"s_cbranch_scc1 LTH2_%=\n"
"s_cmp_eq_u32 s47, 16\n"
"s_cbranch_scc1 LAX_%=\n"
"LTH0_%=:\n"
"v_mfma_f32_16x16x32_bf16 a[224:227], v[124:127], a[244:247], a[224:227]\n"
"v_exp_f32 v168, v168\n"
"v_mfma_f32_16x16x32_bf16 v[16:19], v[128:131], a[244:247], v[16:19]\n"
"s_waitcnt vmcnt(8) lgkmcnt(0)\n"
"v_exp_f32 v169, v169\n"
"v_mfma_f32_16x16x32_bf16 v[16:19], v[132:135], a[248:251], v[16:19]\n"
"s_barrier\n"
"v_mfma_f32_16x16x32_bf16 v[48:51], v[136:139], a[244:247], v[48:51]\n"
"v_exp_f32 v172, v172\n"
"v_mfma_f32_16x16x32_bf16 v[48:51], v[140:143], a[248:251], v[48:51]\n"
"ds_read_b128 v[216:219], v228 offset:1024\n"
"v_cvt_pk_bf16_f32 v102, v168, v169\n"
"v_mfma_f32_16x16x32_bf16 v[80:83], v[144:147], a[244:247], v[80:83]\n"
"v_exp_f32 v176, v176\n"
"v_mfma_f32_16x16x32_bf16 v[80:83], v[148:151], a[248:251], v[80:83]\n"
"ds_read_b64_tr_b16 a[244:245], v234 offset:49152\n"
"ds_read_b64_tr_b16 a[246:247], v234 offset:53248\n"
"v_cvt_pk_bf16_f32 v123, v154, v155\n"
"v_cvt_pk_bf16_f32 v117, v166, v167\n"
"v_exp_f32 v170, v170\n"
"v_exp_f32 v175, v175\n"
"v_exp_f32 v177, v177\n"
"v_mfma_f32_16x16x32_bf16 v[84:87], v[148:151], v[224:227], v[84:87]\n"
"v_exp_f32 v156, v156\n"
"v_mfma_f32_16x16x32_bf16 v[84:87], v[144:147], a[252:255], v[84:87]\n"
"ds_read_b64_tr_b16 a[248:249], v238 offset:49152\n"
"ds_read_b64_tr_b16 a[250:251], v238 offset:53248\n"
"v_mfma_f32_16x16x32_bf16 v[52:55], v[140:143], v[224:227], v[52:55]\n"
"v_cvt_pk_bf16_f32 v101, v158, v159\n"
"v_cvt_pk_bf16_f32 v118, v176, v177\n"
"v_mfma_f32_16x16x32_bf16 v[52:55], v[136:139], a[252:255], v[52:55]\n"
"v_cvt_pk_bf16_f32 v100, v156, v157\n"
"v_exp_f32 v173, v173\n"
"v_exp_f32 v174, v174\n"
"v_exp_f32 v178, v178\n"
"v_exp_f32 v179, v179\n"
"v_mfma_f32_16x16x32_bf16 v[20:23], v[132:135], v[224:227], v[20:23]\n"
"v_exp_f32 v171, v171\n"
"v_mfma_f32_16x16x32_bf16 v[20:23], v[128:131], a[252:255], v[20:23]\n"
"ds_read_b64_tr_b16 v[224:225], v239 offset:49152\n"
"v_cvt_pk_bf16_f32 v103, v170, v171\n"
"v_mfma_f32_16x16x32_bf16 a[228:231], v[124:127], a[252:255], a[228:231]\n"
"s_waitcnt lgkmcnt(3)\n"
"ds_read_b64_tr_b16 a[252:253], v235 offset:49152\n"
"v_cvt_pk_bf16_f32 v119, v178, v179\n"
"v_mfma_f32_16x16x32_bf16 a[216:219], v[120:123], a[244:247], a[216:219]\n"
"s_add_u32 m0, s44, 81920\n"
"ds_read_b64_tr_b16 a[254:255], v235 offset:53248\n"
"ds_read_b64_tr_b16 v[226:227], v239 offset:53248\n"
"v_smfmac_f32_16x16x64_bf16 v[196:199], a[240:243], v[96:103], v241\n"
"buffer_load_dwordx4 v240, s[20:23], s56 offen nt lds\n"
"v_mfma_f32_16x16x32_bf16 v[8:11], v[96:99], a[244:247], v[8:11]\n"
"s_waitcnt lgkmcnt(4)\n"
"v_cvt_pk_bf16_f32 v109, v162, v163\n"
"v_cvt_pk_bf16_f32 v116, v164, v165\n"
"v_mfma_f32_16x16x32_bf16 v[8:11], v[100:103], a[248:251], v[8:11]\n"
"v_cvt_pk_bf16_f32 v110, v172, v173\n"
"v_cvt_pk_bf16_f32 v111, v174, v175\n"
"v_smfmac_f32_16x16x64_bf16 v[200:203], a[240:243], v[112:119], v241\n"
"ds_read_b128 v[220:223], v229 offset:1024\n"
"v_cvt_pk_bf16_f32 v108, v160, v161\n"
"v_mfma_f32_16x16x32_bf16 v[40:43], v[104:107], a[244:247], v[40:43]\n"
"v_add_f32 v196, v196, v197\n"
"v_and_b32 v245, 0x3f0, v242\n"
"v_mfma_f32_16x16x32_bf16 v[40:43], v[108:111], a[248:251], v[40:43]\n"
"ds_read_b128 v[212:215], v229 offset:0\n"
"v_lshrrev_b32 v245, 1, v245\n"
"v_smfmac_f32_16x16x64_bf16 v[198:201], a[240:243], v[104:111], v241\n"
"v_lshlrev_b32_e64 v244, 24, s43\n"
"v_sub_u32 v245, v242, v245\n"
"v_mfma_f32_16x16x32_bf16 v[72:75], v[112:115], a[244:247], v[72:75]\n"
"ds_read_b64_tr_b16 a[244:245], v234 offset:50176\n"
"ds_read_b64_tr_b16 a[246:247], v234 offset:54272\n"
"v_mfma_f32_16x16x32_bf16 v[72:75], v[116:119], a[248:251], v[72:75]\n"
"ds_read_b128 v[208:211], v228 offset:0\n"
"v_mul_f32 v244, 0x45800000, v244\n"
"v_mfma_f32_16x16x32_bf16 v[202:205], a[240:243], v[120:123], v[202:205]\n"
"s_waitcnt lgkmcnt(5)\n"
"ds_read_b64_tr_b16 a[250:251], v238 offset:54272\n"
"v_add_f32 v198, v198, v199\n"
"v_mfma_f32_16x16x32_bf16 v[76:79], v[116:119], v[224:227], v[76:79]\n"
"v_add_f32 v200, v200, v201\n"
"v_add_f32 v254, v196, v198\n"
"v_mfma_f32_16x16x32_bf16 v[76:79], v[112:115], a[252:255], v[76:79]\n"
"ds_read_b64_tr_b16 a[248:249], v238 offset:50176\n"
"v_add_f32 v254, v254, v200\n"
"v_mfma_f32_16x16x32_bf16 v[44:47], v[108:111], v[224:227], v[44:47]\n"
"v_add_f32 v244, v244, v202\n"
"v_min3_f32 v244, v244, v196, v198\n"
"v_mfma_f32_16x16x32_bf16 v[44:47], v[104:107], a[252:255], v[44:47]\n"
"v_min_f32 v244, v244, v200\n"
"v_rcp_f32 v244, v244\n"
"v_mfma_f32_16x16x32_bf16 v[12:15], v[100:103], v[224:227], v[12:15]\n"
"s_add_u32 s40, s56, s52\n"
"v_add_f32 v254, v254, v202\n"
"v_fmac_f32 v254, 0x68800000, v244\n"
"v_mfma_f32_16x16x32_bf16 v[12:15], v[96:99], a[252:255], v[12:15]\n"
"buffer_load_dwordx4 v240, s[20:23], s40 offen offset:1024 nt lds\n"
"v_mfma_f32_16x16x32_bf16 a[220:223], v[120:123], a[252:255], a[220:223]\n"
"s_waitcnt lgkmcnt(2)\n"
"v_cmp_nge_f32 vcc, s45, v254\n"
"s_cbranch_vccnz LSLOW_%=\nLBACK0_%=:\ns_mov_b64 vcc, s[64:65]\n"
"v_mfma_f32_16x16x32_bf16 a[232:235], v[120:123], a[244:247], a[232:235]\n"
"v_rcp_f32 v246, v196\n"
"v_rcp_f32 v206, v200\n"
"v_mov_b32 v252, v192\n"
"v_accvgpr_mov_b32 a9, a57\n"
"v_accvgpr_mov_b32 a10, a58\n"
"v_accvgpr_mov_b32 a28, a76\n"
"v_accvgpr_mov_b32 a31, a79\n"
"v_accvgpr_mov_b32 a33, a81\n"
"v_accvgpr_mov_b32 a40, a88\n"
"v_bfe_u32 v149, v242, 8, 2\n"
"v_lshl_add_u32 v149, v149, 4, s43\n"
"v_xor_b32 v150, 64, v149\n"
"v_mfma_f32_16x16x32_bf16 v[24:27], v[96:99], a[244:247], v[24:27]\n"
"s_waitcnt lgkmcnt(0)\n"
"ds_read_b128 v[128:131], v150 offset:0\n"
"v_accvgpr_mov_b32 a12, a60\n"
"v_mfma_f32_16x16x32_bf16 v[24:27], v[100:103], a[248:251], v[24:27]\n"
"ds_read_b128 v[124:127], v149 offset:0\n"
"v_accvgpr_mov_b32 a0, a48\n"
"v_accvgpr_mov_b32 a3, a51\n"
"v_accvgpr_mov_b32 a4, a52\n"
"v_accvgpr_mov_b32 a18, a66\n"
"v_accvgpr_mov_b32 a21, a69\n"
"v_accvgpr_mov_b32 a35, a83\n"
"v_accvgpr_mov_b32 a38, a86\n"
"v_accvgpr_mov_b32 a47, a95\n"
"v_mfma_f32_16x16x32_bf16 v[56:59], v[104:107], a[244:247], v[56:59]\n"
"ds_read_b128 v[132:135], v149 offset:1024\n"
"v_rcp_f32 v247, v198\n"
"v_accvgpr_mov_b32 a1, a49\n"
"v_accvgpr_mov_b32 a2, a50\n"
"v_accvgpr_mov_b32 a11, a59\n"
"v_accvgpr_mov_b32 a13, a61\n"
"v_accvgpr_mov_b32 a14, a62\n"
"v_accvgpr_mov_b32 a16, a64\n"
"v_accvgpr_mov_b32 a20, a68\n"
"v_accvgpr_mov_b32 a22, a70\n"
"v_accvgpr_mov_b32 a25, a73\n"
"v_accvgpr_mov_b32 a36, a84\n"
"v_xor_b32 v148, 0x400, v243\n"
"v_mfma_f32_16x16x32_bf16 v[56:59], v[108:111], a[248:251], v[56:59]\n"
"ds_read_b128 v[136:139], v150 offset:1024\n"
"v_mov_b32_dpp v246, v246 row_ror:12 row_mask:0x2 bank_mask:0xf\n"
"v_accvgpr_mov_b32 a6, a54\n"
"v_accvgpr_mov_b32 a7, a55\n"
"v_accvgpr_mov_b32 a8, a56\n"
"v_accvgpr_mov_b32 a24, a72\n"
"v_accvgpr_mov_b32 a29, a77\n"
"v_accvgpr_mov_b32 a34, a82\n"
"v_accvgpr_mov_b32 a39, a87\n"
"v_accvgpr_mov_b32 a43, a91\n"
"v_accvgpr_mov_b32 a45, a93\n"
"v_mfma_f32_16x16x32_bf16 v[88:91], v[112:115], a[244:247], v[88:91]\n"
"v_accvgpr_mov_b32 a19, a67\n"
"v_mfma_f32_16x16x32_bf16 v[88:91], v[116:119], a[248:251], v[88:91]\n"
"ds_read_b128 a[192:195], v243 offset:4096\n"
"v_accvgpr_mov_b32 a5, a53\n"
"v_accvgpr_mov_b32 a17, a65\n"
"v_accvgpr_mov_b32 a23, a71\n"
"v_accvgpr_mov_b32 a26, a74\n"
"v_accvgpr_mov_b32 a27, a75\n"
"v_accvgpr_mov_b32 a37, a85\n"
"v_accvgpr_mov_b32 a46, a94\n"
"s_waitcnt lgkmcnt(3)\n"
"v_mfma_f32_16x16x32_bf16 v[180:183], v[124:127], a[0:3], 0\n"
"ds_read_b128 a[196:199], v148 offset:4096\n"
"v_accvgpr_mov_b32 a15, a63\n"
"v_mfma_f32_16x16x32_bf16 v[180:183], v[128:131], a[4:7], v[180:183]\n"
"s_waitcnt lgkmcnt(3)\n"
"v_mfma_f32_16x16x32_bf16 v[180:183], v[132:135], a[8:11], v[180:183]\n"
"v_accvgpr_mov_b32 a41, a89\n"
"s_waitcnt lgkmcnt(2)\n"
"v_mfma_f32_16x16x32_bf16 v[180:183], v[136:139], a[12:15], v[180:183]\n"
"ds_read_b128 a[204:207], v148 offset:6144\n"
"v_mov_b32_dpp v246, v246 row_ror:8 row_mask:0x4 bank_mask:0xf\n"
"v_mfma_f32_16x16x32_bf16 v[184:187], v[124:127], a[16:19], 0\n"
"v_accvgpr_mov_b32 a32, a80\n"
"v_mfma_f32_16x16x32_bf16 v[184:187], v[128:131], a[20:23], v[184:187]\n"
"ds_read_b128 a[200:203], v243 offset:6144\n"
"v_accvgpr_mov_b32 a30, a78\n"
"v_mfma_f32_16x16x32_bf16 v[184:187], v[132:135], a[24:27], v[184:187]\n"
"s_add_u32 s40, s56, s53\n"
"v_sub_f32 v181, s74, v181\n"
"v_mfma_f32_16x16x32_bf16 v[184:187], v[136:139], a[28:31], v[184:187]\n"
"buffer_load_dwordx4 v240, s[20:23], s40 offen offset:2048 nt lds\n"
"v_mov_b32_dpp v247, v247 row_ror:12 row_mask:0x2 bank_mask:0xf\n"
"v_mov_b32_dpp v246, v246 row_ror:4 row_mask:0x8 bank_mask:0xf\n"
"v_accvgpr_mov_b32 a42, a90\n"
"v_sub_f32 v180, s74, v180\n"
"v_sub_f32 v183, s74, v183\n"
"v_mfma_f32_16x16x32_bf16 v[188:191], v[124:127], a[32:35], 0\n"
"v_mfma_f32_16x16x32_bf16 v[188:191], v[128:131], a[36:39], v[188:191]\n"
"v_accvgpr_mov_b32 a44, a92\n"
"v_mfma_f32_16x16x32_bf16 v[188:191], v[132:135], a[40:43], v[188:191]\n"
"v_sub_f32 v187, s74, v187\n"
"v_mfma_f32_16x16x32_bf16 v[188:191], v[136:139], a[44:47], v[188:191]\n"
"s_waitcnt lgkmcnt(1)\n"
"ds_read_b64_tr_b16 a[250:251], v236 offset:53248\n"
"v_sub_f32 v186, s74, v186\n"
"v_mfma_f32_16x16x32_bf16 v[192:195], v[124:127], a[192:195], 0\n"
"v_mfma_f32_16x16x32_bf16 v[192:195], v[128:131], a[196:199], v[192:195]\n"
"v_mul_f32_dpp v14, v246, v14 row_newbcast:2 row_mask:0xf bank_mask:0xf\n"
"s_waitcnt lgkmcnt(1)\n"
"v_mfma_f32_16x16x32_bf16 v[192:195], v[132:135], a[200:203], v[192:195]\n"
"ds_read_b64_tr_b16 a[252:253], v235 offset:50176\n"
"v_sub_f32 v182, s74, v182\n"
"v_mfma_f32_16x16x32_bf16 v[192:195], v[136:139], a[204:207], v[192:195]\n"
"ds_read_b64_tr_b16 v[226:227], v239 offset:54272\n"
"ds_read_b64_tr_b16 a[248:249], v236 offset:49152\n"
"v_xor_b32 v244, 0x11000, v242\n"
"v_mul_f32_dpp v12, v246, v12 row_newbcast:0 row_mask:0xf bank_mask:0xf\n"
"v_sub_f32 v185, s74, v185\n"
"v_sub_f32 v188, s74, v188\n"
"v_sub_f32 v189, s74, v189\n"
"v_sub_f32 v190, s74, v190\n"
"v_mul_f32_dpp v27, v246, v27 row_newbcast:3 row_mask:0xf bank_mask:0xf\n"
"v_mfma_f32_16x16x32_bf16 v[152:155], v[208:211], a[0:3], v[180:183]\n"
"v_mul_f32_dpp v10, v246, v10 row_newbcast:2 row_mask:0xf bank_mask:0xf\n"
"v_mfma_f32_16x16x32_bf16 v[152:155], v[212:215], a[4:7], v[152:155]\n"
"v_sub_f32 v184, s74, v184\n"
"v_mfma_f32_16x16x32_bf16 v[152:155], v[216:219], a[8:11], v[152:155]\n"
"s_add_u32 s40, s56, s54\n"
"v_sub_f32 v194, s74, v194\n"
"v_mfma_f32_16x16x32_bf16 v[152:155], v[220:223], a[12:15], v[152:155]\n"
"buffer_load_dwordx4 v240, s[20:23], s40 offen offset:3072 nt lds\n"
"v_mov_b32_dpp v206, v206 row_ror:12 row_mask:0x2 bank_mask:0xf\n"
"v_sub_f32 v191, s74, v191\n"
"v_sub_f32 v192, s74, v192\n"
"v_sub_f32 v193, s74, v193\n"
"v_sub_f32 v195, s74, v195\n"
"v_mfma_f32_16x16x32_bf16 v[156:159], v[216:219], a[24:27], v[184:187]\n"
"s_add_u32 s55, s55, s59\n"
"v_mov_b32_dpp v206, v206 row_ror:8 row_mask:0x4 bank_mask:0xf\n"
"v_mfma_f32_16x16x32_bf16 v[156:159], v[220:223], a[28:31], v[156:159]\n"
"s_add_u32 s41, s55, s57\n"
"v_mfma_f32_16x16x32_bf16 v[156:159], v[208:211], a[16:19], v[156:159]\n"
"s_and_b32 s41, s41, s58\n"
"ds_read_b64_tr_b16 v[224:225], v239 offset:50176\n"
"v_mov_b32_dpp v247, v247 row_ror:8 row_mask:0x4 bank_mask:0xf\n"
"v_mfma_f32_16x16x32_bf16 v[156:159], v[212:215], a[20:23], v[156:159]\n"
"s_andn2_b32 s56, s55, s58\n"
"v_mfma_f32_16x16x32_bf16 v[160:163], v[208:211], a[32:35], v[188:191]\n"
"s_or_b32 s56, s56, s41\n"
"ds_read_b64_tr_b16 a[254:255], v235 offset:54272\n"
"v_mfma_f32_16x16x32_bf16 v[160:163], v[212:215], a[36:39], v[160:163]\n"
"v_xor_b32 v250, 0x1000, v245\n"
"v_mfma_f32_16x16x32_bf16 v[160:163], v[216:219], a[40:43], v[160:163]\n"
"ds_read_b64_tr_b16 a[244:245], v232 offset:49152\n"
"v_mfma_f32_16x16x32_bf16 v[160:163], v[220:223], a[44:47], v[160:163]\n"
"v_mul_f32_dpp v25, v246, v25 row_newbcast:1 row_mask:0xf bank_mask:0xf\n"
"v_mfma_f32_16x16x32_bf16 v[164:167], v[216:219], a[200:203], v[192:195]\n"
"ds_read_b128 v[216:219], v228 offset:5120\n"
"v_mfma_f32_16x16x32_bf16 v[164:167], v[220:223], a[204:207], v[164:167]\n"
"v_mfma_f32_16x16x32_bf16 v[164:167], v[208:211], a[192:195], v[164:167]\n"
"ds_read_b128 v[220:223], v229 offset:5120\n"
"v_mfma_f32_16x16x32_bf16 v[164:167], v[212:215], a[196:199], v[164:167]\n"
"ds_read_b128 v[208:211], v228 offset:4096\n"
"v_mov_b32_dpp v206, v206 row_ror:4 row_mask:0x8 bank_mask:0xf\n"
"v_exp_f32 v161, v161\n"
"v_exp_f32 v162, v162\n"
"s_branch LTC_%=\n"
"LTH1_%=:\n"
"v_mfma_f32_16x16x32_bf16 a[224:227], v[124:127], a[244:247], a[224:227]\n"
"v_exp_f32 v168, v168\n"
"v_mfma_f32_16x16x32_bf16 v[16:19], v[128:131], a[244:247], v[16:19]\n"
"s_waitcnt vmcnt(8) lgkmcnt(0)\n"
"v_exp_f32 v169, v169\n"
"v_mfma_f32_16x16x32_bf16 v[16:19], v[132:135], a[248:251], v[16:19]\n"
"s_barrier\n"
"v_mfma_f32_16x16x32_bf16 v[48:51], v[136:139], a[244:247], v[48:51]\n"
"v_exp_f32 v172, v172\n"
"v_mfma_f32_16x16x32_bf16 v[48:51], v[140:143], a[248:251], v[48:51]\n"
"ds_read_b128 v[216:219], v228 offset:1024\n"
"v_cvt_pk_bf16_f32 v102, v168, v169\n"
"v_mfma_f32_16x16x32_bf16 v[80:83], v[144:147], a[244:247], v[80:83]\n"
"v_exp_f32 v176, v176\n"
"v_mfma_f32_16x16x32_bf16 v[80:83], v[148:151], a[248:251], v[80:83]\n"
"ds_read_b64_tr_b16 a[244:245], v234 offset:49152\n"
"ds_read_b64_tr_b16 a[246:247], v234 offset:53248\n"
"v_cvt_pk_bf16_f32 v123, v154, v155\n"
"v_cvt_pk_bf16_f32 v117, v166, v167\n"
"v_exp_f32 v170, v170\n"
"v_exp_f32 v175, v175\n"
"v_exp_f32 v177, v177\n"
"v_mfma_f32_16x16x32_bf16 v[84:87], v[148:151], v[224:227], v[84:87]\n"
"v_exp_f32 v156, v156\n"
"v_mfma_f32_16x16x32_bf16 v[84:87], v[144:147], a[252:255], v[84:87]\n"
"ds_read_b64_tr_b16 a[248:249], v238 offset:49152\n"
"ds_read_b64_tr_b16 a[250:251], v238 offset:53248\n"
"v_mfma_f32_16x16x32_bf16 v[52:55], v[140:143], v[224:227], v[52:55]\n"
"v_cvt_pk_bf16_f32 v101, v158, v159\n"
"v_cvt_pk_bf16_f32 v118, v176, v177\n"
"v_mfma_f32_16x16x32_bf16 v[52:55], v[136:139], a[252:255], v[52:55]\n"
"v_cvt_pk_bf16_f32 v100, v156, v157\n"
"v_exp_f32 v173, v173\n"
"v_exp_f32 v174, v174\n"
"v_exp_f32 v178, v178\n"
"v_exp_f32 v179, v179\n"
"v_mfma_f32_16x16x32_bf16 v[20:23], v[132:135], v[224:227], v[20:23]\n"
"v_exp_f32 v171, v171\n"
"v_mfma_f32_16x16x32_bf16 v[20:23], v[128:131], a[252:255], v[20:23]\n"
"ds_read_b64_tr_b16 v[224:225], v239 offset:49152\n"
"v_cvt_pk_bf16_f32 v103, v170, v171\n"
"v_mfma_f32_16x16x32_bf16 a[228:231], v[124:127], a[252:255], a[228:231]\n"
"s_waitcnt lgkmcnt(3)\n"
"ds_read_b64_tr_b16 a[252:253], v235 offset:49152\n"
"v_cvt_pk_bf16_f32 v119, v178, v179\n"
"v_mfma_f32_16x16x32_bf16 a[216:219], v[120:123], a[244:247], a[216:219]\n"
"s_add_u32 m0, s44, 81920\n"
"ds_read_b64_tr_b16 a[254:255], v235 offset:53248\n"
"ds_read_b64_tr_b16 v[226:227], v239 offset:53248\n"
"v_smfmac_f32_16x16x64_bf16 v[196:199], a[240:243], v[96:103], v241\n"
"buffer_load_dwordx4 v240, s[20:23], s56 offen nt lds\n"
"v_mfma_f32_16x16x32_bf16 v[8:11], v[96:99], a[244:247], v[8:11]\n"
"s_waitcnt lgkmcnt(4)\n"
"v_cvt_pk_bf16_f32 v109, v162, v163\n"
"v_cvt_pk_bf16_f32 v116, v164, v165\n"
"v_mfma_f32_16x16x32_bf16 v[8:11], v[100:103], a[248:251], v[8:11]\n"
"v_cvt_pk_bf16_f32 v110, v172, v173\n"
"v_cvt_pk_bf16_f32 v111, v174, v175\n"
"v_smfmac_f32_16x16x64_bf16 v[200:203], a[240:243], v[112:119], v241\n"
"ds_read_b128 v[220:223], v229 offset:1024\n"
"v_cvt_pk_bf16_f32 v108, v160, v161\n"
"v_mfma_f32_16x16x32_bf16 v[40:43], v[104:107], a[244:247], v[40:43]\n"
"v_add_f32 v196, v196, v197\n"
"v_and_b32 v245, 0x3f0, v242\n"
"v_mfma_f32_16x16x32_bf16 v[40:43], v[108:111], a[248:251], v[40:43]\n"
"ds_read_b128 v[212:215], v229 offset:0\n"
"v_lshrrev_b32 v245, 1, v245\n"
"v_smfmac_f32_16x16x64_bf16 v[198:201], a[240:243], v[104:111], v241\n"
"v_lshlrev_b32_e64 v244, 24, s43\n"
"v_sub_u32 v245, v242, v245\n"
"v_mfma_f32_16x16x32_bf16 v[72:75], v[112:115], a[244:247], v[72:75]\n"
"ds_read_b64_tr_b16 a[244:245], v234 offset:50176\n"
"ds_read_b64_tr_b16 a[246:247], v234 offset:54272\n"
"v_mfma_f32_16x16x32_bf16 v[72:75], v[116:119], a[248:251], v[72:75]\n"
"ds_read_b128 v[208:211], v228 offset:0\n"
"v_mul_f32 v244, 0x45800000, v244\n"
"v_mfma_f32_16x16x32_bf16 v[202:205], a[240:243], v[120:123], v[202:205]\n"
"s_waitcnt lgkmcnt(5)\n"
"ds_read_b64_tr_b16 a[250:251], v238 offset:54272\n"
"v_add_f32 v198, v198, v199\n"
"v_mfma_f32_16x16x32_bf16 v[76:79], v[116:119], v[224:227], v[76:79]\n"
"v_add_f32 v200, v200, v201\n"
"v_add_f32 v254, v196, v198\n"
"v_mfma_f32_16x16x32_bf16 v[76:79], v[112:115], a[252:255], v[76:79]\n"
"ds_read_b64_tr_b16 a[248:249], v238 offset:50176\n"
"v_add_f32 v254, v254, v200\n"
"v_mfma_f32_16x16x32_bf16 v[44:47], v[108:111], v[224:227], v[44:47]\n"
"v_add_f32 v244, v244, v202\n"
"v_min3_f32 v244, v244, v196, v198\n"
"v_mfma_f32_16x16x32_bf16 v[44:47], v[104:107], a[252:255], v[44:47]\n"
"v_min_f32 v244, v244, v200\n"
"v_rcp_f32 v244, v244\n"
"v_mfma_f32_16x16x32_bf16 v[12:15], v[100:103], v[224:227], v[12:15]\n"
"s_add_u32 s40, s56, s52\n"
"v_add_f32 v254, v254, v202\n"
"v_fmac_f32 v254, 0x68800000, v244\n"
"v_mfma_f32_16x16x32_bf16 v[12:15], v[96:99], a[252:255], v[12:15]\n"
"buffer_load_dwordx4 v240, s[20:23], s40 offen offset:1024 nt lds\n"
"v_mfma_f32_16x16x32_bf16 a[220:223], v[120:123], a[252:255], a[220:223]\n"
"s_waitcnt lgkmcnt(2)\n"
"v_cmp_nge_f32 vcc, s45, v254\n"
"s_cbranch_vccnz LSLOW_%=\nLBACK1_%=:\ns_mov_b64 vcc, s[64:65]\n"
"v_mfma_f32_16x16x32_bf16 a[232:235], v[120:123], a[244:247], a[232:235]\n"
"v_rcp_f32 v246, v196\n"
"v_rcp_f32 v206, v200\n"
"v_mov_b32 v252, v192\n"
"v_accvgpr_mov_b32 a9, a105\n"
"v_accvgpr_mov_b32 a10, a106\n"
"v_accvgpr_mov_b32 a28, a124\n"
"v_accvgpr_mov_b32 a31, a127\n"
"v_accvgpr_mov_b32 a33, a129\n"
"v_accvgpr_mov_b32 a40, a136\n"
"v_bfe_u32 v149, v242, 8, 2\n"
"v_lshl_add_u32 v149, v149, 4, s43\n"
"v_xor_b32 v150, 64, v149\n"
"v_accvgpr_mov_b32 a105, a57\n"
"v_mfma_f32_16x16x32_bf16 v[24:27], v[96:99], a[244:247], v[24:27]\n"
"s_waitcnt lgkmcnt(0)\n"
"ds_read_b128 v[128:131], v150 offset:0\n"
"v_accvgpr_mov_b32 a12, a108\n"
"v_mfma_f32_16x16x32_bf16 v[24:27], v[100:103], a[248:251], v[24:27]\n"
"ds_read_b128 v[124:127], v149 offset:0\n"
"v_accvgpr_mov_b32 a0, a96\n"
"v_accvgpr_mov_b32 a3, a99\n"
"v_accvgpr_mov_b32 a4, a100\n"
"v_accvgpr_mov_b32 a18, a114\n"
"v_accvgpr_mov_b32 a21, a117\n"
"v_accvgpr_mov_b32 a35, a131\n"
"v_accvgpr_mov_b32 a38, a134\n"
"v_accvgpr_mov_b32 a47, a143\n"
"v_accvgpr_mov_b32 a99, a51\n"
"v_mfma_f32_16x16x32_bf16 v[56:59], v[104:107], a[244:247], v[56:59]\n"
"ds_read_b128 v[132:135], v149 offset:1024\n"
"v_rcp_f32 v247, v198\n"
"v_accvgpr_mov_b32 a1, a97\n"
"v_accvgpr_mov_b32 a2, a98\n"
"v_accvgpr_mov_b32 a11, a107\n"
"v_accvgpr_mov_b32 a13, a109\n"
"v_accvgpr_mov_b32 a14, a110\n"
"v_accvgpr_mov_b32 a16, a112\n"
"v_accvgpr_mov_b32 a20, a116\n"
"v_accvgpr_mov_b32 a22, a118\n"
"v_accvgpr_mov_b32 a25, a121\n"
"v_accvgpr_mov_b32 a36, a132\n"
"v_xor_b32 v148, 0x400, v243\n"
"v_mfma_f32_16x16x32_bf16 v[56:59], v[108:111], a[248:251], v[56:59]\n"
"ds_read_b128 v[136:139], v150 offset:1024\n"
"v_mov_b32_dpp v246, v246 row_ror:12 row_mask:0x2 bank_mask:0xf\n"
"v_accvgpr_mov_b32 a6, a102\n"
"v_accvgpr_mov_b32 a7, a103\n"
"v_accvgpr_mov_b32 a8, a104\n"
"v_accvgpr_mov_b32 a24, a120\n"
"v_accvgpr_mov_b32 a29, a125\n"
"v_accvgpr_mov_b32 a34, a130\n"
"v_accvgpr_mov_b32 a39, a135\n"
"v_accvgpr_mov_b32 a43, a139\n"
"v_accvgpr_mov_b32 a45, a141\n"
"v_accvgpr_mov_b32 a103, a55\n"
"v_mfma_f32_16x16x32_bf16 v[88:91], v[112:115], a[244:247], v[88:91]\n"
"v_accvgpr_mov_b32 a19, a115\n"
"v_accvgpr_mov_b32 a129, a81\n"
"v_mfma_f32_16x16x32_bf16 v[88:91], v[116:119], a[248:251], v[88:91]\n"
"s_waitcnt lgkmcnt(2)\n"
"ds_read_b128 a[192:195], v243 offset:4096\n"
"v_accvgpr_mov_b32 a5, a101\n"
"v_accvgpr_mov_b32 a17, a113\n"
"v_accvgpr_mov_b32 a23, a119\n"
"v_accvgpr_mov_b32 a26, a122\n"
"v_accvgpr_mov_b32 a27, a123\n"
"v_accvgpr_mov_b32 a37, a133\n"
"v_accvgpr_mov_b32 a46, a142\n"
"v_accvgpr_mov_b32 a101, a53\n"
"v_accvgpr_mov_b32 a124, a76\n"
"v_accvgpr_mov_b32 a121, a73\n"
"v_mfma_f32_16x16x32_bf16 v[180:183], v[124:127], a[0:3], 0\n"
"ds_read_b128 a[196:199], v148 offset:4096\n"
"v_accvgpr_mov_b32 a15, a111\n"
"v_mfma_f32_16x16x32_bf16 v[180:183], v[128:131], a[4:7], v[180:183]\n"
"s_waitcnt lgkmcnt(3)\n"
"v_accvgpr_mov_b32 a117, a69\n"
"v_accvgpr_mov_b32 a119, a71\n"
"v_mfma_f32_16x16x32_bf16 v[180:183], v[132:135], a[8:11], v[180:183]\n"
"s_waitcnt lgkmcnt(2)\n"
"v_accvgpr_mov_b32 a41, a137\n"
"v_accvgpr_mov_b32 a123, a75\n"
"v_mfma_f32_16x16x32_bf16 v[180:183], v[136:139], a[12:15], v[180:183]\n"
"ds_read_b128 a[204:207], v148 offset:6144\n"
"v_mov_b32_dpp v246, v246 row_ror:8 row_mask:0x4 bank_mask:0xf\n"
"v_mfma_f32_16x16x32_bf16 v[184:187], v[124:127], a[16:19], 0\n"
"v_accvgpr_mov_b32 a32, a128\n"
"v_accvgpr_mov_b32 a112, a64\n"
"v_mfma_f32_16x16x32_bf16 v[184:187], v[128:131], a[20:23], v[184:187]\n"
"ds_read_b128 a[200:203], v243 offset:6144\n"
"v_accvgpr_mov_b32 a30, a126\n"
"v_mfma_f32_16x16x32_bf16 v[184:187], v[132:135], a[24:27], v[184:187]\n"
"s_add_u32 s40, s56, s53\n"
"v_accvgpr_mov_b32 a104, a56\n"
"v_sub_f32 v181, s74, v181\n"
"v_mfma_f32_16x16x32_bf16 v[184:187], v[136:139], a[28:31], v[184:187]\n"
"buffer_load_dwordx4 v240, s[20:23], s40 offen offset:2048 nt lds\n"
"v_mov_b32_dpp v247, v247 row_ror:12 row_mask:0x2 bank_mask:0xf\n"
"v_mov_b32_dpp v246, v246 row_ror:4 row_mask:0x8 bank_mask:0xf\n"
"v_accvgpr_mov_b32 a42, a138\n"
"v_accvgpr_mov_b32 a133, a85\n"
"v_accvgpr_mov_b32 a111, a63\n"
"v_accvgpr_mov_b32 a143, a95\n"
"v_accvgpr_mov_b32 a130, a82\n"
"v_sub_f32 v180, s74, v180\n"
"v_sub_f32 v183, s74, v183\n"
"v_mfma_f32_16x16x32_bf16 v[188:191], v[124:127], a[32:35], 0\n"
"v_accvgpr_mov_b32 a120, a72\n"
"v_accvgpr_mov_b32 a139, a91\n"
"v_mfma_f32_16x16x32_bf16 v[188:191], v[128:131], a[36:39], v[188:191]\n"
"v_accvgpr_mov_b32 a44, a140\n"
"v_accvgpr_mov_b32 a132, a84\n"
"v_mfma_f32_16x16x32_bf16 v[188:191], v[132:135], a[40:43], v[188:191]\n"
"v_accvgpr_mov_b32 a108, a60\n"
"v_sub_f32 v187, s74, v187\n"
"v_mfma_f32_16x16x32_bf16 v[188:191], v[136:139], a[44:47], v[188:191]\n"
"s_waitcnt lgkmcnt(1)\n"
"ds_read_b64_tr_b16 a[250:251], v236 offset:53248\n"
"v_sub_f32 v186, s74, v186\n"
"v_mfma_f32_16x16x32_bf16 v[192:195], v[124:127], a[192:195], 0\n"
"v_accvgpr_mov_b32 a115, a67\n"
"v_accvgpr_mov_b32 a122, a74\n"
"v_mfma_f32_16x16x32_bf16 v[192:195], v[128:131], a[196:199], v[192:195]\n"
"s_waitcnt lgkmcnt(1)\n"
"v_accvgpr_mov_b32 a102, a54\n"
"v_mul_f32_dpp v14, v246, v14 row_newbcast:2 row_mask:0xf bank_mask:0xf\n"
"v_mfma_f32_16x16x32_bf16 v[192:195], v[132:135], a[200:203], v[192:195]\n"
"ds_read_b64_tr_b16 a[252:253], v235 offset:50176\n"
"v_sub_f32 v182, s74, v182\n"
"v_mfma_f32_16x16x32_bf16 v[192:195], v[136:139], a[204:207], v[192:195]\n"
"ds_read_b64_tr_b16 v[226:227], v239 offset:54272\n"
"ds_read_b64_tr_b16 a[248:249], v236 offset:49152\n"
"v_xor_b32 v244, 0x11000, v242\n"
"v_accvgpr_mov_b32 a100, a52\n"
"v_accvgpr_mov_b32 a142, a94\n"
"v_mul_f32_dpp v12, v246, v12 row_newbcast:0 row_mask:0xf bank_mask:0xf\n"
"v_sub_f32 v185, s74, v185\n"
"v_sub_f32 v188, s74, v188\n"
"v_sub_f32 v189, s74, v189\n"
"v_sub_f32 v190, s74, v190\n"
"v_mul_f32_dpp v27, v246, v27 row_newbcast:3 row_mask:0xf bank_mask:0xf\n"
"v_mfma_f32_16x16x32_bf16 v[152:155], v[208:211], a[0:3], v[180:183]\n"
"v_accvgpr_mov_b32 a135, a87\n"
"v_mul_f32_dpp v10, v246, v10 row_newbcast:2 row_mask:0xf bank_mask:0xf\n"
"v_mfma_f32_16x16x32_bf16 v[152:155], v[212:215], a[4:7], v[152:155]\n"
"v_accvgpr_mov_b32 a126, a78\n"
"v_sub_f32 v184, s74, v184\n"
"v_mfma_f32_16x16x32_bf16 v[152:155], v[216:219], a[8:11], v[152:155]\n"
"s_add_u32 s40, s56, s54\n"
"v_accvgpr_mov_b32 a110, a62\n"
"v_sub_f32 v194, s74, v194\n"
"v_mfma_f32_16x16x32_bf16 v[152:155], v[220:223], a[12:15], v[152:155]\n"
"buffer_load_dwordx4 v240, s[20:23], s40 offen offset:3072 nt lds\n"
"v_mov_b32_dpp v206, v206 row_ror:12 row_mask:0x2 bank_mask:0xf\n"
"v_accvgpr_mov_b32 a134, a86\n"
"v_accvgpr_mov_b32 a113, a65\n"
"v_accvgpr_mov_b32 a128, a80\n"
"v_accvgpr_mov_b32 a107, a59\n"
"v_sub_f32 v191, s74, v191\n"
"v_sub_f32 v192, s74, v192\n"
"v_sub_f32 v193, s74, v193\n"
"v_sub_f32 v195, s74, v195\n"
"v_mfma_f32_16x16x32_bf16 v[156:159], v[216:219], a[24:27], v[184:187]\n"
"s_add_u32 s55, s55, s59\n"
"v_mov_b32_dpp v206, v206 row_ror:8 row_mask:0x4 bank_mask:0xf\n"
"v_accvgpr_mov_b32 a138, a90\n"
"v_mfma_f32_16x16x32_bf16 v[156:159], v[220:223], a[28:31], v[156:159]\n"
"s_add_u32 s41, s55, s57\n"
"v_accvgpr_mov_b32 a140, a92\n"
"v_accvgpr_mov_b32 a97, a49\n"
"v_mfma_f32_16x16x32_bf16 v[156:159], v[208:211], a[16:19], v[156:159]\n"
"s_and_b32 s41, s41, s58\n"
"ds_read_b64_tr_b16 v[224:225], v239 offset:50176\n"
"v_mov_b32_dpp v247, v247 row_ror:8 row_mask:0x4 bank_mask:0xf\n"
"v_mfma_f32_16x16x32_bf16 v[156:159], v[212:215], a[20:23], v[156:159]\n"
"s_andn2_b32 s56, s55, s58\n"
"v_accvgpr_mov_b32 a96, a48\n"
"v_accvgpr_mov_b32 a98, a50\n"
"v_mfma_f32_16x16x32_bf16 v[160:163], v[208:211], a[32:35], v[188:191]\n"
"s_or_b32 s56, s56, s41\n"
"ds_read_b64_tr_b16 a[254:255], v235 offset:54272\n"
"v_accvgpr_mov_b32 a114, a66\n"
"v_mfma_f32_16x16x32_bf16 v[160:163], v[212:215], a[36:39], v[160:163]\n"
"v_xor_b32 v250, 0x1000, v245\n"
"v_accvgpr_mov_b32 a141, a93\n"
"v_mfma_f32_16x16x32_bf16 v[160:163], v[216:219], a[40:43], v[160:163]\n"
"ds_read_b64_tr_b16 a[244:245], v232 offset:49152\n"
"v_accvgpr_mov_b32 a118, a70\n"
"v_mfma_f32_16x16x32_bf16 v[160:163], v[220:223], a[44:47], v[160:163]\n"
"v_accvgpr_mov_b32 a127, a79\n"
"v_mul_f32_dpp v25, v246, v25 row_newbcast:1 row_mask:0xf bank_mask:0xf\n"
"v_mfma_f32_16x16x32_bf16 v[164:167], v[216:219], a[200:203], v[192:195]\n"
"ds_read_b128 v[216:219], v228 offset:5120\n"
"v_accvgpr_mov_b32 a116, a68\n"
"v_mfma_f32_16x16x32_bf16 v[164:167], v[220:223], a[204:207], v[164:167]\n"
"v_accvgpr_mov_b32 a125, a77\n"
"v_accvgpr_mov_b32 a106, a58\n"
"v_mfma_f32_16x16x32_bf16 v[164:167], v[208:211], a[192:195], v[164:167]\n"
"ds_read_b128 v[220:223], v229 offset:5120\n"
"v_accvgpr_mov_b32 a136, a88\n"
"v_mfma_f32_16x16x32_bf16 v[164:167], v[212:215], a[196:199], v[164:167]\n"
"s_waitcnt lgkmcnt(4)\n"
"ds_read_b128 v[208:211], v228 offset:4096\n"
"v_mov_b32_dpp v206, v206 row_ror:4 row_mask:0x8 bank_mask:0xf\n"
"v_accvgpr_mov_b32 a109, a61\n"
"v_accvgpr_mov_b32 a131, a83\n"
"v_accvgpr_mov_b32 a137, a89\n"
"v_exp_f32 v161, v161\n"
"v_exp_f32 v162, v162\n"
"s_branch LTC_%=\n"
"LTH2_%=:\n"
"v_mfma_f32_16x16x32_bf16 a[224:227], v[124:127], a[244:247], a[224:227]\n"
"v_exp_f32 v168, v168\n"
"v_mfma_f32_16x16x32_bf16 v[16:19], v[128:131], a[244:247], v[16:19]\n"
"s_waitcnt vmcnt(8) lgkmcnt(0)\n"
"v_exp_f32 v169, v169\n"
"v_mfma_f32_16x16x32_bf16 v[16:19], v[132:135], a[248:251], v[16:19]\n"
"s_barrier\n"
"v_mfma_f32_16x16x32_bf16 v[48:51], v[136:139], a[244:247], v[48:51]\n"
"v_exp_f32 v172, v172\n"
"v_mfma_f32_16x16x32_bf16 v[48:51], v[140:143], a[248:251], v[48:51]\n"
"ds_read_b128 v[216:219], v228 offset:1024\n"
"v_cvt_pk_bf16_f32 v102, v168, v169\n"
"v_mfma_f32_16x16x32_bf16 v[80:83], v[144:147], a[244:247], v[80:83]\n"
"v_exp_f32 v176, v176\n"
"v_mfma_f32_16x16x32_bf16 v[80:83], v[148:151], a[248:251], v[80:83]\n"
"ds_read_b64_tr_b16 a[244:245], v234 offset:49152\n"
"ds_read_b64_tr_b16 a[246:247], v234 offset:53248\n"
"v_cvt_pk_bf16_f32 v123, v154, v155\n"
"v_cvt_pk_bf16_f32 v117, v166, v167\n"
"v_exp_f32 v170, v170\n"
"v_exp_f32 v175, v175\n"
"v_exp_f32 v177, v177\n"
"v_mfma_f32_16x16x32_bf16 v[84:87], v[148:151], v[224:227], v[84:87]\n"
"v_exp_f32 v156, v156\n"
"v_mfma_f32_16x16x32_bf16 v[84:87], v[144:147], a[252:255], v[84:87]\n"
"ds_read_b64_tr_b16 a[248:249], v238 offset:49152\n"
"ds_read_b64_tr_b16 a[250:251], v238 offset:53248\n"
"v_mfma_f32_16x16x32_bf16 v[52:55], v[140:143], v[224:227], v[52:55]\n"
"v_cvt_pk_bf16_f32 v101, v158, v159\n"
"v_cvt_pk_bf16_f32 v118, v176, v177\n"
"v_mfma_f32_16x16x32_bf16 v[52:55], v[136:139], a[252:255], v[52:55]\n"
"v_cvt_pk_bf16_f32 v100, v156, v157\n"
"v_exp_f32 v173, v173\n"
"v_exp_f32 v174, v174\n"
"v_exp_f32 v178, v178\n"
"v_exp_f32 v179, v179\n"
"v_mfma_f32_16x16x32_bf16 v[20:23], v[132:135], v[224:227], v[20:23]\n"
"v_exp_f32 v171, v171\n"
"v_mfma_f32_16x16x32_bf16 v[20:23], v[128:131], a[252:255], v[20:23]\n"
"ds_read_b64_tr_b16 v[224:225], v239 offset:49152\n"
"v_cvt_pk_bf16_f32 v103, v170, v171\n"
"v_mfma_f32_16x16x32_bf16 a[228:231], v[124:127], a[252:255], a[228:231]\n"
"s_waitcnt lgkmcnt(3)\n"
"ds_read_b64_tr_b16 a[252:253], v235 offset:49152\n"
"v_cvt_pk_bf16_f32 v119, v178, v179\n"
"v_mfma_f32_16x16x32_bf16 a[216:219], v[120:123], a[244:247], a[216:219]\n"
"s_add_u32 m0, s44, 81920\n"
"ds_read_b64_tr_b16 a[254:255], v235 offset:53248\n"
"ds_read_b64_tr_b16 v[226:227], v239 offset:53248\n"
"v_smfmac_f32_16x16x64_bf16 v[196:199], a[240:243], v[96:103], v241\n"
"buffer_load_dwordx4 v240, s[20:23], s56 offen nt lds\n"
"v_mfma_f32_16x16x32_bf16 v[8:11], v[96:99], a[244:247], v[8:11]\n"
"s_waitcnt lgkmcnt(4)\n"
"v_cvt_pk_bf16_f32 v109, v162, v163\n"
"v_cvt_pk_bf16_f32 v116, v164, v165\n"
"v_mfma_f32_16x16x32_bf16 v[8:11], v[100:103], a[248:251], v[8:11]\n"
"v_cvt_pk_bf16_f32 v110, v172, v173\n"
"v_cvt_pk_bf16_f32 v111, v174, v175\n"
"v_smfmac_f32_16x16x64_bf16 v[200:203], a[240:243], v[112:119], v241\n"
"ds_read_b128 v[220:223], v229 offset:1024\n"
"v_cvt_pk_bf16_f32 v108, v160, v161\n"
"v_mfma_f32_16x16x32_bf16 v[40:43], v[104:107], a[244:247], v[40:43]\n"
"v_add_f32 v196, v196, v197\n"
"v_and_b32 v245, 0x3f0, v242\n"
"v_mfma_f32_16x16x32_bf16 v[40:43], v[108:111], a[248:251], v[40:43]\n"
"ds_read_b128 v[212:215], v229 offset:0\n"
"v_lshrrev_b32 v245, 1, v245\n"
"v_smfmac_f32_16x16x64_bf16 v[198:201], a[240:243], v[104:111], v241\n"
"v_lshlrev_b32_e64 v244, 24, s43\n"
"v_sub_u32 v245, v242, v245\n"
"v_mfma_f32_16x16x32_bf16 v[72:75], v[112:115], a[244:247], v[72:75]\n"
"ds_read_b64_tr_b16 a[244:245], v234 offset:50176\n"
"ds_read_b64_tr_b16 a[246:247], v234 offset:54272\n"
"v_mfma_f32_16x16x32_bf16 v[72:75], v[116:119], a[248:251], v[72:75]\n"
"ds_read_b128 v[208:211], v228 offset:0\n"
"v_mul_f32 v244, 0x45800000, v244\n"
"v_mfma_f32_16x16x32_bf16 v[202:205], a[240:243], v[120:123], v[202:205]\n"
"s_waitcnt lgkmcnt(5)\n"
"ds_read_b64_tr_b16 a[250:251], v238 offset:54272\n"
"v_add_f32 v198, v198, v199\n"
"v_mfma_f32_16x16x32_bf16 v[76:79], v[116:119], v[224:227], v[76:79]\n"
"v_add_f32 v200, v200, v201\n"
"v_add_f32 v254, v196, v198\n"
"v_mfma_f32_16x16x32_bf16 v[76:79], v[112:115], a[252:255], v[76:79]\n"
"ds_read_b64_tr_b16 a[248:249], v238 offset:50176\n"
"v_add_f32 v254, v254, v200\n"
"v_mfma_f32_16x16x32_bf16 v[44:47], v[108:111], v[224:227], v[44:47]\n"
"v_add_f32 v244, v244, v202\n"
"v_min3_f32 v244, v244, v196, v198\n"
"v_mfma_f32_16x16x32_bf16 v[44:47], v[104:107], a[252:255], v[44:47]\n"
"v_min_f32 v244, v244, v200\n"
"v_rcp_f32 v244, v244\n"
"v_mfma_f32_16x16x32_bf16 v[12:15], v[100:103], v[224:227], v[12:15]\n"
"s_add_u32 s40, s56, s52\n"
"v_add_f32 v254, v254, v202\n"
"v_fmac_f32 v254, 0x68800000, v244\n"
"v_mfma_f32_16x16x32_bf16 v[12:15], v[96:99], a[252:255], v[12:15]\n"
"buffer_load_dwordx4 v240, s[20:23], s40 offen offset:1024 nt lds\n"
"v_mfma_f32_16x16x32_bf16 a[220:223], v[120:123], a[252:255], a[220:223]\n"
"s_waitcnt lgkmcnt(2)\n"
"v_cmp_nge_f32 vcc, s45, v254\n"
"s_cbranch_vccnz LSLOW_%=\nLBACK2_%=:\ns_mov_b64 vcc, s[64:65]\n"
"v_mfma_f32_16x16x32_bf16 a[232:235], v[120:123], a[244:247], a[232:235]\n"
"v_rcp_f32 v246, v196\n"
"v_rcp_f32 v206, v200\n"
"v_mov_b32 v252, v192\n"
"v_accvgpr_mov_b32 a9, a153\n"
"v_accvgpr_mov_b32 a10, a154\n"
"v_accvgpr_mov_b32 a28, a172\n"
"v_accvgpr_mov_b32 a31, a175\n"
"v_accvgpr_mov_b32 a33, a177\n"
"v_accvgpr_mov_b32 a40, a184\n"
"v_bfe_u32 v149, v242, 8, 2\n"
"v_lshl_add_u32 v149, v149, 4, s43\n"
"v_xor_b32 v150, 64, v149\n"
"v_accvgpr_mov_b32 a153, a57\n"
"v_mfma_f32_16x16x32_bf16 v[24:27], v[96:99], a[244:247], v[24:27]\n"
"s_waitcnt lgkmcnt(0)\n"
"ds_read_b128 v[128:131], v150 offset:0\n"
"v_accvgpr_mov_b32 a12, a156\n"
"v_mfma_f32_16x16x32_bf16 v[24:27], v[100:103], a[248:251], v[24:27]\n"
"ds_read_b128 v[124:127], v149 offset:0\n"
"v_accvgpr_mov_b32 a0, a144\n"
"v_accvgpr_mov_b32 a3, a147\n"
"v_accvgpr_mov_b32 a4, a148\n"
"v_accvgpr_mov_b32 a18, a162\n"
"v_accvgpr_mov_b32 a21, a165\n"
"v_accvgpr_mov_b32 a35, a179\n"
"v_accvgpr_mov_b32 a38, a182\n"
"v_accvgpr_mov_b32 a47, a191\n"
"v_accvgpr_mov_b32 a147, a51\n"
"v_mfma_f32_16x16x32_bf16 v[56:59], v[104:107], a[244:247], v[56:59]\n"
"ds_read_b128 v[132:135], v149 offset:1024\n"
"v_rcp_f32 v247, v198\n"
"v_accvgpr_mov_b32 a1, a145\n"
"v_accvgpr_mov_b32 a2, a146\n"
"v_accvgpr_mov_b32 a11, a155\n"
"v_accvgpr_mov_b32 a13, a157\n"
"v_accvgpr_mov_b32 a14, a158\n"
"v_accvgpr_mov_b32 a16, a160\n"
"v_accvgpr_mov_b32 a20, a164\n"
"v_accvgpr_mov_b32 a22, a166\n"
"v_accvgpr_mov_b32 a25, a169\n"
"v_accvgpr_mov_b32 a36, a180\n"
"v_xor_b32 v148, 0x400, v243\n"
"v_mfma_f32_16x16x32_bf16 v[56:59], v[108:111], a[248:251], v[56:59]\n"
"ds_read_b128 v[136:139], v150 offset:1024\n"
"v_mov_b32_dpp v246, v246 row_ror:12 row_mask:0x2 bank_mask:0xf\n"
"v_accvgpr_mov_b32 a6, a150\n"
"v_accvgpr_mov_b32 a7, a151\n"
"v_accvgpr_mov_b32 a8, a152\n"
"v_accvgpr_mov_b32 a24, a168\n"
"v_accvgpr_mov_b32 a29, a173\n"
"v_accvgpr_mov_b32 a34, a178\n"
"v_accvgpr_mov_b32 a39, a183\n"
"v_accvgpr_mov_b32 a43, a187\n"
"v_accvgpr_mov_b32 a45, a189\n"
"v_accvgpr_mov_b32 a151, a55\n"
"v_mfma_f32_16x16x32_bf16 v[88:91], v[112:115], a[244:247], v[88:91]\n"
"v_accvgpr_mov_b32 a19, a163\n"
"v_accvgpr_mov_b32 a177, a81\n"
"v_mfma_f32_16x16x32_bf16 v[88:91], v[116:119], a[248:251], v[88:91]\n"
"s_waitcnt lgkmcnt(2)\n"
"ds_read_b128 a[192:195], v243 offset:4096\n"
"v_accvgpr_mov_b32 a5, a149\n"
"v_accvgpr_mov_b32 a17, a161\n"
"v_accvgpr_mov_b32 a23, a167\n"
"v_accvgpr_mov_b32 a26, a170\n"
"v_accvgpr_mov_b32 a27, a171\n"
"v_accvgpr_mov_b32 a37, a181\n"
"v_accvgpr_mov_b32 a46, a190\n"
"v_accvgpr_mov_b32 a149, a53\n"
"v_accvgpr_mov_b32 a172, a76\n"
"v_accvgpr_mov_b32 a169, a73\n"
"v_mfma_f32_16x16x32_bf16 v[180:183], v[124:127], a[0:3], 0\n"
"ds_read_b128 a[196:199], v148 offset:4096\n"
"v_accvgpr_mov_b32 a15, a159\n"
"v_mfma_f32_16x16x32_bf16 v[180:183], v[128:131], a[4:7], v[180:183]\n"
"s_waitcnt lgkmcnt(3)\n"
"v_accvgpr_mov_b32 a165, a69\n"
"v_accvgpr_mov_b32 a167, a71\n"
"v_mfma_f32_16x16x32_bf16 v[180:183], v[132:135], a[8:11], v[180:183]\n"
"s_waitcnt lgkmcnt(2)\n"
"v_accvgpr_mov_b32 a41, a185\n"
"v_accvgpr_mov_b32 a171, a75\n"
"v_mfma_f32_16x16x32_bf16 v[180:183], v[136:139], a[12:15], v[180:183]\n"
"ds_read_b128 a[204:207], v148 offset:6144\n"
"v_mov_b32_dpp v246, v246 row_ror:8 row_mask:0x4 bank_mask:0xf\n"
"v_mfma_f32_16x16x32_bf16 v[184:187], v[124:127], a[16:19], 0\n"
"v_accvgpr_mov_b32 a32, a176\n"
"v_accvgpr_mov_b32 a160, a64\n"
"v_mfma_f32_16x16x32_bf16 v[184:187], v[128:131], a[20:23], v[184:187]\n"
"ds_read_b128 a[200:203], v243 offset:6144\n"
"v_accvgpr_mov_b32 a30, a174\n"
"v_mfma_f32_16x16x32_bf16 v[184:187], v[132:135], a[24:27], v[184:187]\n"
"s_add_u32 s40, s56, s53\n"
"v_accvgpr_mov_b32 a152, a56\n"
"v_sub_f32 v181, s74, v181\n"
"v_mfma_f32_16x16x32_bf16 v[184:187], v[136:139], a[28:31], v[184:187]\n"
"buffer_load_dwordx4 v240, s[20:23], s40 offen offset:2048 nt lds\n"
"v_mov_b32_dpp v247, v247 row_ror:12 row_mask:0x2 bank_mask:0xf\n"
"v_mov_b32_dpp v246, v246 row_ror:4 row_mask:0x8 bank_mask:0xf\n"
"v_accvgpr_mov_b32 a42, a186\n"
"v_accvgpr_mov_b32 a181, a85\n"
"v_accvgpr_mov_b32 a159, a63\n"
"v_accvgpr_mov_b32 a191, a95\n"
"v_accvgpr_mov_b32 a178, a82\n"
"v_sub_f32 v180, s74, v180\n"
"v_sub_f32 v183, s74, v183\n"
"v_mfma_f32_16x16x32_bf16 v[188:191], v[124:127], a[32:35], 0\n"
"v_accvgpr_mov_b32 a168, a72\n"
"v_accvgpr_mov_b32 a187, a91\n"
"v_mfma_f32_16x16x32_bf16 v[188:191], v[128:131], a[36:39], v[188:191]\n"
"v_accvgpr_mov_b32 a44, a188\n"
"v_accvgpr_mov_b32 a180, a84\n"
"v_mfma_f32_16x16x32_bf16 v[188:191], v[132:135], a[40:43], v[188:191]\n"
"v_accvgpr_mov_b32 a156, a60\n"
"v_sub_f32 v187, s74, v187\n"
"v_mfma_f32_16x16x32_bf16 v[188:191], v[136:139], a[44:47], v[188:191]\n"
"s_waitcnt lgkmcnt(1)\n"
"ds_read_b64_tr_b16 a[250:251], v236 offset:53248\n"
"v_sub_f32 v186, s74, v186\n"
"v_mfma_f32_16x16x32_bf16 v[192:195], v[124:127], a[192:195], 0\n"
"v_accvgpr_mov_b32 a163, a67\n"
"v_accvgpr_mov_b32 a170, a74\n"
"v_mfma_f32_16x16x32_bf16 v[192:195], v[128:131], a[196:199], v[192:195]\n"
"s_waitcnt lgkmcnt(1)\n"
"v_accvgpr_mov_b32 a150, a54\n"
"v_mul_f32_dpp v14, v246, v14 row_newbcast:2 row_mask:0xf bank_mask:0xf\n"
"v_mfma_f32_16x16x32_bf16 v[192:195], v[132:135], a[200:203], v[192:195]\n"
"ds_read_b64_tr_b16 a[252:253], v235 offset:50176\n"
"v_sub_f32 v182, s74, v182\n"
"v_mfma_f32_16x16x32_bf16 v[192:195], v[136:139], a[204:207], v[192:195]\n"
"ds_read_b64_tr_b16 v[226:227], v239 offset:54272\n"
"ds_read_b64_tr_b16 a[248:249], v236 offset:49152\n"
"v_xor_b32 v244, 0x11000, v242\n"
"v_accvgpr_mov_b32 a148, a52\n"
"v_accvgpr_mov_b32 a190, a94\n"
"v_mul_f32_dpp v12, v246, v12 row_newbcast:0 row_mask:0xf bank_mask:0xf\n"
"v_sub_f32 v185, s74, v185\n"
"v_sub_f32 v188, s74, v188\n"
"v_sub_f32 v189, s74, v189\n"
"v_sub_f32 v190, s74, v190\n"
"v_mul_f32_dpp v27, v246, v27 row_newbcast:3 row_mask:0xf bank_mask:0xf\n"
"v_mfma_f32_16x16x32_bf16 v[152:155], v[208:211], a[0:3], v[180:183]\n"
"v_accvgpr_mov_b32 a183, a87\n"
"v_mul_f32_dpp v10, v246, v10 row_newbcast:2 row_mask:0xf bank_mask:0xf\n"
"v_mfma_f32_16x16x32_bf16 v[152:155], v[212:215], a[4:7], v[152:155]\n"
"v_accvgpr_mov_b32 a174, a78\n"
"v_sub_f32 v184, s74, v184\n"
"v_mfma_f32_16x16x32_bf16 v[152:155], v[216:219], a[8:11], v[152:155]\n"
"s_add_u32 s40, s56, s54\n"
"v_accvgpr_mov_b32 a158, a62\n"
"v_sub_f32 v194, s74, v194\n"
"v_mfma_f32_16x16x32_bf16 v[152:155], v[220:223], a[12:15], v[152:155]\n"
"buffer_load_dwordx4 v240, s[20:23], s40 offen offset:3072 nt lds\n"
"v_mov_b32_dpp v206, v206 row_ror:12 row_mask:0x2 bank_mask:0xf\n"
"v_accvgpr_mov_b32 a182, a86\n"
"v_accvgpr_mov_b32 a161, a65\n"
"v_accvgpr_mov_b32 a176, a80\n"
"v_accvgpr_mov_b32 a155, a59\n"
"v_sub_f32 v191, s74, v191\n"
"v_sub_f32 v192, s74, v192\n"
"v_sub_f32 v193, s74, v193\n"
"v_sub_f32 v195, s74, v195\n"
"v_mfma_f32_16x16x32_bf16 v[156:159], v[216:219], a[24:27], v[184:187]\n"
"s_add_u32 s55, s55, s59\n"
"v_mov_b32_dpp v206, v206 row_ror:8 row_mask:0x4 bank_mask:0xf\n"
"v_accvgpr_mov_b32 a186, a90\n"
"v_mfma_f32_16x16x32_bf16 v[156:159], v[220:223], a[28:31], v[156:159]\n"
"s_add_u32 s41, s55, s57\n"
"v_accvgpr_mov_b32 a188, a92\n"
"v_accvgpr_mov_b32 a145, a49\n"
"v_mfma_f32_16x16x32_bf16 v[156:159], v[208:211], a[16:19], v[156:159]\n"
"s_and_b32 s41, s41, s58\n"
"ds_read_b64_tr_b16 v[224:225], v239 offset:50176\n"
"v_mov_b32_dpp v247, v247 row_ror:8 row_mask:0x4 bank_mask:0xf\n"
"v_mfma_f32_16x16x32_bf16 v[156:159], v[212:215], a[20:23], v[156:159]\n"
"s_andn2_b32 s56, s55, s58\n"
"v_accvgpr_mov_b32 a144, a48\n"
"v_accvgpr_mov_b32 a146, a50\n"
"v_mfma_f32_16x16x32_bf16 v[160:163], v[208:211], a[32:35], v[188:191]\n"
"s_or_b32 s56, s56, s41\n"
"ds_read_b64_tr_b16 a[254:255], v235 offset:54272\n"
"v_accvgpr_mov_b32 a162, a66\n"
"v_mfma_f32_16x16x32_bf16 v[160:163], v[212:215], a[36:39], v[160:163]\n"
"v_xor_b32 v250, 0x1000, v245\n"
"v_accvgpr_mov_b32 a189, a93\n"
"v_mfma_f32_16x16x32_bf16 v[160:163], v[216:219], a[40:43], v[160:163]\n"
"ds_read_b64_tr_b16 a[244:245], v232 offset:49152\n"
"v_accvgpr_mov_b32 a166, a70\n"
"v_mfma_f32_16x16x32_bf16 v[160:163], v[220:223], a[44:47], v[160:163]\n"
"v_accvgpr_mov_b32 a175, a79\n"
"v_mul_f32_dpp v25, v246, v25 row_newbcast:1 row_mask:0xf bank_mask:0xf\n"
"v_mfma_f32_16x16x32_bf16 v[164:167], v[216:219], a[200:203], v[192:195]\n"
"ds_read_b128 v[216:219], v228 offset:5120\n"
"v_accvgpr_mov_b32 a164, a68\n"
"v_mfma_f32_16x16x32_bf16 v[164:167], v[220:223], a[204:207], v[164:167]\n"
"v_accvgpr_mov_b32 a173, a77\n"
"v_accvgpr_mov_b32 a154, a58\n"
"v_mfma_f32_16x16x32_bf16 v[164:167], v[208:211], a[192:195], v[164:167]\n"
"ds_read_b128 v[220:223], v229 offset:5120\n"
"v_accvgpr_mov_b32 a184, a88\n"
"v_mfma_f32_16x16x32_bf16 v[164:167], v[212:215], a[196:199], v[164:167]\n"
"s_waitcnt lgkmcnt(4)\n"
"ds_read_b128 v[208:211], v228 offset:4096\n"
"v_mov_b32_dpp v206, v206 row_ror:4 row_mask:0x8 bank_mask:0xf\n"
"v_accvgpr_mov_b32 a157, a61\n"
"v_accvgpr_mov_b32 a179, a83\n"
"v_accvgpr_mov_b32 a185, a89\n"
"v_exp_f32 v161, v161\n"
"v_exp_f32 v162, v162\n"
"LTC_%=:\n"
"s_waitcnt lgkmcnt(5)\n"
"v_mfma_f32_16x16x32_bf16 v[92:95], v[116:119], v[224:227], v[92:95]\n"
"s_waitcnt lgkmcnt(4)\n"
"ds_read_b128 v[212:215], v229 offset:4096\n"
"v_mul_f32_dpp v11, v246, v11 row_newbcast:3 row_mask:0xf bank_mask:0xf\n"
"v_mfma_f32_16x16x32_bf16 v[92:95], v[112:115], a[252:255], v[92:95]\n"
"v_mov_b32_dpp v247, v247 row_ror:4 row_mask:0x8 bank_mask:0xf\n"
"v_mov_b32 v253, v202\n"
"v_mfma_f32_16x16x32_bf16 v[60:63], v[108:111], v[224:227], v[60:63]\n"
"s_add_u32 m0, s44, 32768\n"
"ds_read_b64_tr_b16 a[246:247], v232 offset:53248\n"
"v_mul_f32_dpp v46, v247, v46 row_newbcast:2 row_mask:0xf bank_mask:0xf\n"
"v_mfma_f32_16x16x32_bf16 v[60:63], v[104:107], a[252:255], v[60:63]\n"
"buffer_load_dwordx4 v240, s[16:19], s56 offen nt lds\n"
"v_mfma_f32_16x16x32_bf16 v[28:31], v[100:103], v[224:227], v[28:31]\n"
"v_exp_f32 v154, v154\n"
"v_mfma_f32_16x16x32_bf16 v[28:31], v[96:99], a[252:255], v[28:31]\n"
"v_mul_f32_dpp v13, v246, v13 row_newbcast:1 row_mask:0xf bank_mask:0xf\n"
"v_mul_f32_dpp v26, v246, v26 row_newbcast:2 row_mask:0xf bank_mask:0xf\n"
"v_mfma_f32_16x16x32_bf16 a[236:239], v[120:123], a[252:255], a[236:239]\n"
"s_waitcnt lgkmcnt(3)\n"
"ds_write_b128 v244, a[216:219] offset:32768\n"
"v_mul_f32_dpp v15, v246, v15 row_newbcast:3 row_mask:0xf bank_mask:0xf\n"
"v_exp_f32 v155, v155\n"
"v_exp_f32 v156, v156\n"
"v_exp_f32 v158, v158\n"
"v_exp_f32 v160, v160\n"
"v_exp_f32 v163, v163\n"
"v_exp_f32 v166, v166\n"
"v_mul_f32_dpp v24, v246, v24 row_newbcast:0 row_mask:0xf bank_mask:0xf\n"
"v_mfma_f32_16x16x32_bf16 v[168:171], v[216:219], a[8:11], v[180:183]\n"
"v_exp_f32 v159, v159\n"
"v_mfma_f32_16x16x32_bf16 v[168:171], v[220:223], a[12:15], v[168:171]\n"
"s_waitcnt lgkmcnt(2)\n"
"ds_read_b64_tr_b16 v[226:227], v237 offset:53248\n"
"v_mul_f32_dpp v74, v206, v74 row_newbcast:2 row_mask:0xf bank_mask:0xf\n"
"v_mfma_f32_16x16x32_bf16 v[168:171], v[208:211], a[0:3], v[168:171]\n"
"v_exp_f32 v153, v153\n"
"v_mfma_f32_16x16x32_bf16 v[168:171], v[212:215], a[4:7], v[168:171]\n"
"v_mul_f32_dpp v41, v247, v41 row_newbcast:1 row_mask:0xf bank_mask:0xf\n"
"v_cvt_pk_bf16_f32 v144, v160, v161\n"
"v_mfma_f32_16x16x32_bf16 v[172:175], v[208:211], a[16:19], v[184:187]\n"
"v_exp_f32 v157, v157\n"
"v_mfma_f32_16x16x32_bf16 v[172:175], v[212:215], a[20:23], v[172:175]\n"
"v_exp_f32 v152, v152\n"
"v_mfma_f32_16x16x32_bf16 v[172:175], v[216:219], a[24:27], v[172:175]\n"
"v_exp_f32 v164, v164\n"
"v_mfma_f32_16x16x32_bf16 v[172:175], v[220:223], a[28:31], v[172:175]\n"
"ds_read_b64_tr_b16 a[254:255], v233 offset:53248\n"
"v_mul_f32_dpp v9, v246, v9 row_newbcast:1 row_mask:0xf bank_mask:0xf\n"
"v_mfma_f32_16x16x32_bf16 v[176:179], v[216:219], a[40:43], v[188:191]\n"
"ds_read_b64_tr_b16 v[224:225], v237 offset:49152\n"
"v_mul_f32_dpp v73, v206, v73 row_newbcast:1 row_mask:0xf bank_mask:0xf\n"
"v_mfma_f32_16x16x32_bf16 v[176:179], v[220:223], a[44:47], v[176:179]\n"
"v_cvt_pk_bf16_f32 v128, v152, v153\n"
"v_cvt_pk_bf16_f32 v129, v154, v155\n"
"v_mfma_f32_16x16x32_bf16 v[176:179], v[208:211], a[32:35], v[176:179]\n"
"s_add_u32 s40, s56, s52\n"
"v_mul_f32_dpp v43, v247, v43 row_newbcast:3 row_mask:0xf bank_mask:0xf\n"
"v_cvt_pk_bf16_f32 v24, v24, v25\n"
"v_mfma_f32_16x16x32_bf16 v[176:179], v[212:215], a[36:39], v[176:179]\n"
"buffer_load_dwordx4 v240, s[16:19], s40 offen offset:1024 nt lds\n"
"v_mfma_f32_16x16x32_bf16 v[152:155], v[208:211], a[192:195], v[192:195]\n"
"v_cvt_pk_bf16_f32 v137, v158, v159\n"
"v_mul_f32_dpp v62, v247, v62 row_newbcast:2 row_mask:0xf bank_mask:0xf\n"
"v_mfma_f32_16x16x32_bf16 v[152:155], v[212:215], a[196:199], v[152:155]\n"
"v_cvt_pk_bf16_f32 v145, v162, v163\n"
"v_mul_f32_dpp v90, v206, v90 row_newbcast:2 row_mask:0xf bank_mask:0xf\n"
"v_mfma_f32_16x16x32_bf16 v[152:155], v[216:219], a[200:203], v[152:155]\n"
"ds_read_b128 v[212:215], v231 offset:0\n"
"v_mul_f32_dpp v40, v247, v40 row_newbcast:0 row_mask:0xf bank_mask:0xf\n"
"v_mfma_f32_16x16x32_bf16 v[152:155], v[220:223], a[204:207], v[152:155]\n"
"s_waitcnt lgkmcnt(2)\n"
"ds_read_b128 v[208:211], v230 offset:0\n"
"ds_read_b128 v[220:223], v231 offset:1024\n"
"v_mfma_f32_16x16x32_bf16 a[208:211], v[120:123], a[244:247], a[208:211]\n"
"v_exp_f32 v173, v173\n"
"v_mfma_f32_16x16x32_bf16 v[0:3], v[96:99], a[244:247], v[0:3]\n"
"ds_read_b128 v[216:219], v230 offset:1024\n"
"v_mul_f32_dpp v72, v206, v72 row_newbcast:0 row_mask:0xf bank_mask:0xf\n"
"v_mfma_f32_16x16x32_bf16 v[0:3], v[100:103], a[248:251], v[0:3]\n"
"v_exp_f32 v178, v178\n"
"v_mfma_f32_16x16x32_bf16 v[32:35], v[104:107], a[244:247], v[32:35]\n"
"v_exp_f32 v171, v171\n"
"v_mfma_f32_16x16x32_bf16 v[32:35], v[108:111], a[248:251], v[32:35]\n"
"ds_write_b128 v244, a[220:223] offset:33792\n"
"v_exp_f32 v165, v165\n"
"v_exp_f32 v167, v167\n"
"v_mul_f32_dpp v59, v247, v59 row_newbcast:3 row_mask:0xf bank_mask:0xf\n"
"v_exp_f32 v168, v168\n"
"v_mfma_f32_16x16x32_bf16 v[64:67], v[112:115], a[244:247], v[64:67]\n"
"v_exp_f32 v172, v172\n"
"v_mfma_f32_16x16x32_bf16 v[64:67], v[116:119], a[248:251], v[64:67]\n"
"s_waitcnt lgkmcnt(3)\n"
"ds_write_b64 v250, v[252:253] offset:52224\n"
"v_mul_f32_dpp v42, v247, v42 row_newbcast:2 row_mask:0xf bank_mask:0xf\n"
"v_mul_f32_dpp v75, v206, v75 row_newbcast:3 row_mask:0xf bank_mask:0xf\n"
"v_cvt_pk_bf16_f32 v136, v156, v157\n"
"v_cvt_pk_bf16_f32 v125, v166, v167\n"
"v_mul_f32_dpp v29, v246, v29 row_newbcast:1 row_mask:0xf bank_mask:0xf\n"
"v_mul_f32_dpp v57, v247, v57 row_newbcast:1 row_mask:0xf bank_mask:0xf\n"
"v_exp_f32 v155, v155\n"
"v_mul_f32_dpp v0, v246, v0 row_newbcast:0 row_mask:0xf bank_mask:0xf\n"
"v_mul_f32_dpp v1, v246, v1 row_newbcast:1 row_mask:0xf bank_mask:0xf\n"
"v_mfma_f32_16x16x32_bf16 v[156:159], v[208:211], a[0:3], v[180:183]\n"
"v_mul_f32_dpp v45, v247, v45 row_newbcast:1 row_mask:0xf bank_mask:0xf\n"
"v_mul_f32_dpp v89, v206, v89 row_newbcast:1 row_mask:0xf bank_mask:0xf\n"
"v_mfma_f32_16x16x32_bf16 v[156:159], v[212:215], a[4:7], v[156:159]\n"
"s_waitcnt lgkmcnt(2)\n"
"v_mul_f32_dpp v8, v246, v8 row_newbcast:0 row_mask:0xf bank_mask:0xf\n"
"v_mul_f32_dpp v30, v246, v30 row_newbcast:2 row_mask:0xf bank_mask:0xf\n"
"v_mfma_f32_16x16x32_bf16 v[156:159], v[216:219], a[8:11], v[156:159]\n"
"s_add_u32 s40, s56, s53\n"
"ds_read_b64_tr_b16 a[252:253], v233 offset:49152\n"
"v_mul_f32_dpp v56, v247, v56 row_newbcast:0 row_mask:0xf bank_mask:0xf\n"
"v_mfma_f32_16x16x32_bf16 v[156:159], v[220:223], a[12:15], v[156:159]\n"
"buffer_load_dwordx4 v240, s[16:19], s40 offen offset:2048 nt lds\n"
"v_mul_f32_dpp v44, v247, v44 row_newbcast:0 row_mask:0xf bank_mask:0xf\n"
"v_mul_f32_dpp v76, v206, v76 row_newbcast:0 row_mask:0xf bank_mask:0xf\n"
"v_mul_f32_dpp v88, v206, v88 row_newbcast:0 row_mask:0xf bank_mask:0xf\n"
"v_exp_f32 v170, v170\n"
"v_exp_f32 v174, v174\n"
"v_exp_f32 v175, v175\n"
"v_exp_f32 v176, v176\n"
"v_exp_f32 v152, v152\n"
"v_exp_f32 v154, v154\n"
"v_mul_f32_dpp v66, v206, v66 row_newbcast:2 row_mask:0xf bank_mask:0xf\n"
"v_mfma_f32_16x16x32_bf16 v[160:163], v[216:219], a[24:27], v[184:187]\n"
"v_mul_f32_dpp v92, v206, v92 row_newbcast:0 row_mask:0xf bank_mask:0xf\n"
"v_cvt_pk_bf16_f32 v0, v0, v1\n"
"v_mfma_f32_16x16x32_bf16 v[160:163], v[220:223], a[28:31], v[160:163]\n"
"v_mul_f32_dpp v95, v206, v95 row_newbcast:3 row_mask:0xf bank_mask:0xf\n"
"v_cvt_pk_bf16_f32 v131, v170, v171\n"
"v_mfma_f32_16x16x32_bf16 v[160:163], v[208:211], a[16:19], v[160:163]\n"
"v_mul_f32_dpp v58, v247, v58 row_newbcast:2 row_mask:0xf bank_mask:0xf\n"
"v_cvt_pk_bf16_f32 v139, v174, v175\n"
"v_mfma_f32_16x16x32_bf16 v[160:163], v[212:215], a[20:23], v[160:163]\n"
"ds_write_b128 v244, a[236:239] offset:35840\n"
"v_cvt_pk_bf16_f32 v124, v164, v165\n"
"v_mul_f32_dpp v94, v206, v94 row_newbcast:2 row_mask:0xf bank_mask:0xf\n"
"v_exp_f32 v169, v169\n"
"v_exp_f32 v156, v156\n"
"v_mfma_f32_16x16x32_bf16 v[164:167], v[208:211], a[32:35], v[188:191]\n"
"v_exp_f32 v157, v157\n"
"v_mfma_f32_16x16x32_bf16 v[164:167], v[212:215], a[36:39], v[164:167]\n"
"ds_read_b128 v[208:211], v230 offset:4096\n"
"v_cvt_pk_bf16_f32 v8, v8, v9\n"
"v_mfma_f32_16x16x32_bf16 v[164:167], v[216:219], a[40:43], v[164:167]\n"
"v_cvt_pk_bf16_f32 v40, v40, v41\n"
"v_mul_f32_dpp v60, v247, v60 row_newbcast:0 row_mask:0xf bank_mask:0xf\n"
"v_mfma_f32_16x16x32_bf16 v[164:167], v[220:223], a[44:47], v[164:167]\n"
"ds_read_b64_tr_b16 a[244:245], v232 offset:50176\n"
"ds_read_b64_tr_b16 a[248:249], v236 offset:50176\n"
"v_mul_f32_dpp v79, v206, v79 row_newbcast:3 row_mask:0xf bank_mask:0xf\n"
"v_cvt_pk_bf16_f32 v25, v26, v27\n"
"v_mul_f32_dpp v61, v247, v61 row_newbcast:1 row_mask:0xf bank_mask:0xf\n"
"v_exp_f32 v177, v177\n"
"v_exp_f32 v179, v179\n"
"v_exp_f32 v160, v160\n"
"v_exp_f32 v161, v161\n"
"v_mul_f32_dpp v33, v247, v33 row_newbcast:1 row_mask:0xf bank_mask:0xf\n"
"v_mfma_f32_16x16x32_bf16 v[68:71], v[116:119], v[224:227], v[68:71]\n"
"s_waitcnt lgkmcnt(4)\n"
"ds_read_b128 v[220:223], v231 offset:5120\n"
"v_mul_f32_dpp v77, v206, v77 row_newbcast:1 row_mask:0xf bank_mask:0xf\n"
"v_mfma_f32_16x16x32_bf16 v[68:71], v[112:115], a[252:255], v[68:71]\n"
"ds_read_b128 v[212:215], v231 offset:4096\n"
"v_mul_f32_dpp v32, v247, v32 row_newbcast:0 row_mask:0xf bank_mask:0xf\n"
"v_mfma_f32_16x16x32_bf16 v[36:39], v[108:111], v[224:227], v[36:39]\n"
"s_add_u32 s40, s56, s54\n"
"ds_read_b128 v[216:219], v230 offset:5120\n"
"v_mul_f32_dpp v2, v246, v2 row_newbcast:2 row_mask:0xf bank_mask:0xf\n"
"v_mfma_f32_16x16x32_bf16 v[36:39], v[104:107], a[252:255], v[36:39]\n"
"buffer_load_dwordx4 v240, s[16:19], s40 offen offset:3072 nt lds\n"
"v_mul_f32_dpp v47, v247, v47 row_newbcast:3 row_mask:0xf bank_mask:0xf\n"
"v_cvt_pk_bf16_f32 v41, v42, v43\n"
"v_mul_f32_dpp v31, v246, v31 row_newbcast:3 row_mask:0xf bank_mask:0xf\n"
"v_cvt_pk_bf16_f32 v147, v178, v179\n"
"v_exp_f32 v153, v153\n"
"v_exp_f32 v159, v159\n"
"v_exp_f32 v165, v165\n"
"v_mfma_f32_16x16x32_bf16 v[4:7], v[100:103], v[224:227], v[4:7]\n"
"v_cvt_pk_bf16_f32 v56, v56, v57\n"
"v_cvt_pk_bf16_f32 v138, v172, v173\n"
"v_mfma_f32_16x16x32_bf16 v[4:7], v[96:99], a[252:255], v[4:7]\n"
"ds_write_b128 v244, a[232:235] offset:34816\n"
"v_cvt_pk_bf16_f32 v9, v10, v11\n"
"v_mul_f32_dpp v63, v247, v63 row_newbcast:3 row_mask:0xf bank_mask:0xf\n"
"v_cvt_pk_bf16_f32 v130, v168, v169\n"
"v_cvt_pk_bf16_f32 v126, v152, v153\n"
"v_mul_f32_dpp v64, v206, v64 row_newbcast:0 row_mask:0xf bank_mask:0xf\n"
"v_mfma_f32_16x16x32_bf16 a[212:215], v[120:123], a[252:255], a[212:215]\n"
"s_waitcnt lgkmcnt(6)\n"
"v_cvt_pk_bf16_f32 v88, v88, v89\n"
"v_cvt_pk_bf16_f32 v146, v176, v177\n"
"v_mfma_f32_16x16x32_bf16 v[168:171], v[208:211], a[0:3], v[180:183]\n"
"s_waitcnt lgkmcnt(2)\n"
"ds_read_b64_tr_b16 a[246:247], v232 offset:54272\n"
"v_cvt_pk_bf16_f32 v43, v46, v47\n"
"v_mfma_f32_16x16x32_bf16 v[168:171], v[212:215], a[4:7], v[168:171]\n"
"s_waitcnt lgkmcnt(2)\n"
"ds_read_b64_tr_b16 v[224:225], v237 offset:50176\n"
"v_cvt_pk_bf16_f32 v57, v58, v59\n"
"v_mfma_f32_16x16x32_bf16 v[168:171], v[216:219], a[8:11], v[168:171]\n"
"v_exp_f32 v158, v158\n"
"v_mfma_f32_16x16x32_bf16 v[168:171], v[220:223], a[12:15], v[168:171]\n"
"ds_read_b64_tr_b16 a[250:251], v236 offset:54272\n"
"ds_read_b64_tr_b16 a[252:253], v233 offset:50176\n"
"v_cvt_pk_bf16_f32 v42, v44, v45\n"
"v_cvt_pk_bf16_f32 v72, v72, v73\n"
"v_cvt_pk_bf16_f32 v73, v74, v75\n"
"v_mul_f32_dpp v28, v246, v28 row_newbcast:0 row_mask:0xf bank_mask:0xf\n"
"v_mul_f32_dpp v91, v206, v91 row_newbcast:3 row_mask:0xf bank_mask:0xf\n"
"v_exp_f32 v167, v167\n"
"v_mul_f32_dpp v34, v247, v34 row_newbcast:2 row_mask:0xf bank_mask:0xf\n"
"v_mul_f32_dpp v35, v247, v35 row_newbcast:3 row_mask:0xf bank_mask:0xf\n"
"v_mul_f32_dpp v65, v206, v65 row_newbcast:1 row_mask:0xf bank_mask:0xf\n"
"v_mul_f32_dpp v68, v206, v68 row_newbcast:0 row_mask:0xf bank_mask:0xf\n"
"v_mfma_f32_16x16x32_bf16 v[172:175], v[216:219], a[24:27], v[184:187]\n"
"v_cvt_pk_bf16_f32 v10, v12, v13\n"
"v_xor_b32 v244, 0x1000, v244\n"
"v_mfma_f32_16x16x32_bf16 v[172:175], v[220:223], a[28:31], v[172:175]\n"
"ds_read_b64_tr_b16 v[226:227], v237 offset:54272\n"
"v_cvt_pk_bf16_f32 v127, v154, v155\n"
"v_mfma_f32_16x16x32_bf16 v[172:175], v[208:211], a[16:19], v[172:175]\n"
"v_cvt_pk_bf16_f32 v59, v62, v63\n"
"v_mul_f32_dpp v67, v206, v67 row_newbcast:3 row_mask:0xf bank_mask:0xf\n"
"v_mfma_f32_16x16x32_bf16 v[172:175], v[212:215], a[20:23], v[172:175]\n"
"s_cbranch_vccnz LPCT0_%=\nLPKT0_%=:\n"
"ds_write_b128 v248, v[40:43] offset:50176\n"
"v_cvt_pk_bf16_f32 v11, v14, v15\n"
"v_mul_f32_dpp v78, v206, v78 row_newbcast:2 row_mask:0xf bank_mask:0xf\n"
"v_mul_f32_dpp v93, v206, v93 row_newbcast:1 row_mask:0xf bank_mask:0xf\n"
"v_exp_f32 v164, v164\n"
"v_mul_f32_dpp v3, v246, v3 row_newbcast:3 row_mask:0xf bank_mask:0xf\n"
"v_mul_f32_dpp v36, v247, v36 row_newbcast:0 row_mask:0xf bank_mask:0xf\n"
"v_mul_f32_dpp v39, v247, v39 row_newbcast:3 row_mask:0xf bank_mask:0xf\n"
"v_mul_f32_dpp v70, v206, v70 row_newbcast:2 row_mask:0xf bank_mask:0xf\n"
"v_mov_b64 v[198:199], 0\n"
"v_mfma_f32_16x16x32_bf16 v[176:179], v[208:211], a[32:35], v[188:191]\n"
"ds_read_b64_tr_b16 a[68:69], v249 offset:50176\n"
"ds_read_b64_tr_b16 a[254:255], v233 offset:54272\n"
"v_mfma_f32_16x16x32_bf16 v[176:179], v[212:215], a[36:39], v[176:179]\n"
"ds_read_b64_tr_b16 a[70:71], v249 offset:50184\n"
"v_cvt_pk_bf16_f32 v74, v76, v77\n"
"v_mfma_f32_16x16x32_bf16 v[176:179], v[216:219], a[40:43], v[176:179]\n"
"v_mul_f32_dpp v69, v206, v69 row_newbcast:1 row_mask:0xf bank_mask:0xf\n"
"v_mul_f32_dpp v71, v206, v71 row_newbcast:3 row_mask:0xf bank_mask:0xf\n"
"v_mfma_f32_16x16x32_bf16 v[176:179], v[220:223], a[44:47], v[176:179]\n"
"s_waitcnt lgkmcnt(5)\n"
"ds_write_b128 v248, v[8:11] offset:49152\n"
"v_cvt_pk_bf16_f32 v75, v78, v79\n"
"v_cvt_pk_bf16_f32 v27, v30, v31\n"
"v_mul_f32_dpp v38, v247, v38 row_newbcast:2 row_mask:0xf bank_mask:0xf\n"
"v_cvt_pk_bf16_f32 v32, v32, v33\n"
"v_accvgpr_read_b32 v255, a209\n"
"v_mfma_f32_16x16x32_bf16 a[224:227], v[120:123], a[244:247], a[224:227]\n"
"ds_write_b128 v248, v[72:75] offset:51200\n"
"v_cvt_pk_bf16_f32 v89, v90, v91\n"
"v_mul_f32_dpp v5, v246, v5 row_newbcast:1 row_mask:0xf bank_mask:0xf\n"
"v_exp_f32 v170, v170\n"
"v_exp_f32 v175, v175\n"
"v_mfma_f32_16x16x32_bf16 v[16:19], v[96:99], a[244:247], v[16:19]\n"
"s_waitcnt vmcnt(8) lgkmcnt(3)\n"
"ds_read_b64_tr_b16 a[84:85], v249 offset:51200\n"
"v_mul_f32_dpp v37, v247, v37 row_newbcast:1 row_mask:0xf bank_mask:0xf\n"
"v_mfma_f32_16x16x32_bf16 v[16:19], v[100:103], a[248:251], v[16:19]\n"
"s_barrier\n"
"ds_read_b128 v[212:215], v229 offset:16384\n"
"ds_read_b64 v[250:251], v245 offset:52224\n"
"v_cvt_pk_bf16_f32 v26, v28, v29\n"
"v_cvt_pk_bf16_f32 v58, v60, v61\n"
"v_mul_f32_dpp v6, v246, v6 row_newbcast:2 row_mask:0xf bank_mask:0xf\n"
"v_mul_f32_dpp v7, v246, v7 row_newbcast:3 row_mask:0xf bank_mask:0xf\n"
"v_exp_f32 v172, v172\n"
"v_exp_f32 v177, v177\n"
"v_mfma_f32_16x16x32_bf16 v[48:51], v[104:107], a[244:247], v[48:51]\n"
"ds_read_b64_tr_b16 a[86:87], v249 offset:51208\n"
"ds_read_b128 v[208:211], v228 offset:16384\n"
"v_mfma_f32_16x16x32_bf16 v[48:51], v[108:111], a[248:251], v[48:51]\n"
"ds_read_b128 v[216:219], v228 offset:17408\n"
"v_cvt_pk_bf16_f32 v91, v94, v95\n"
"v_mfma_f32_16x16x32_bf16 v[80:83], v[112:115], a[244:247], v[80:83]\n"
"ds_read_b64_tr_b16 a[54:55], v249 offset:49160\n"
"v_cvt_pk_bf16_f32 v90, v92, v93\n"
"v_mfma_f32_16x16x32_bf16 v[80:83], v[116:119], a[248:251], v[80:83]\n"
"ds_read_b64_tr_b16 a[52:53], v249 offset:49152\n"
"ds_read_b128 v[28:31], v244 offset:33792\n"
"v_mfma_f32_16x16x32_bf16 v[84:87], v[116:119], v[224:227], v[84:87]\n"
"ds_read_b128 v[220:223], v229 offset:17408\n"
"v_cvt_pk_bf16_f32 v33, v34, v35\n"
"v_mfma_f32_16x16x32_bf16 v[84:87], v[112:115], a[252:255], v[84:87]\n"
"ds_write_b128 v248, v[56:59] offset:50176\n"
"v_cvt_pk_bf16_f32 v1, v2, v3\n"
"v_cvt_pk_bf16_f32 v64, v64, v65\n"
"v_exp_f32 v171, v171\n"
"v_exp_f32 v176, v176\n"
"v_mov_b64 v[202:203], 0\n"
"v_mfma_f32_16x16x32_bf16 v[52:55], v[108:111], v[224:227], v[52:55]\n"
"s_waitcnt lgkmcnt(8)\n"
"ds_write_b128 v248, v[24:27] offset:49152\n"
"v_cvt_pk_bf16_f32 v65, v66, v67\n"
"v_cvt_pk_bf16_f32 v66, v68, v69\n"
"v_cvt_pk_bf16_f32 v67, v70, v71\n"
"v_exp_f32 v169, v169\n"
"v_min_f32 v254, v252, v250\n"
"v_mfma_f32_16x16x32_bf16 v[52:55], v[104:107], a[252:255], v[52:55]\n"
"ds_write_b128 v248, v[88:91] offset:51200\n"
"v_cvt_pk_bf16_f32 v132, v156, v157\n"
"v_exp_f32 v162, v162\n"
"v_exp_f32 v166, v166\n"
"v_cvt_pk_bf16_f32 v3, v6, v7\n"
"v_exp_f32 v174, v174\n"
"v_mfma_f32_16x16x32_bf16 v[20:23], v[100:103], v[224:227], v[20:23]\n"
"ds_read_b64_tr_b16 a[76:77], v249 offset:50176\n"
"ds_read_b64_tr_b16 a[250:251], v238 offset:4096\n"
"v_mfma_f32_16x16x32_bf16 v[20:23], v[96:99], a[252:255], v[20:23]\n"
"ds_read_b128 v[24:27], v244 offset:32768\n"
"v_sub_f32 v252, v254, v252\n"
"v_mfma_f32_16x16x32_bf16 a[228:231], v[120:123], a[252:255], a[228:231]\n"
"s_waitcnt lgkmcnt(9)\n"
"ds_read_b64_tr_b16 a[62:63], v249 offset:49160\n"
"ds_read_b64_tr_b16 a[92:93], v249 offset:51200\n"
"ds_read_b64_tr_b16 a[94:95], v249 offset:51208\n"
"v_exp_f32 v163, v163\n"
"v_mul_f32_dpp v4, v246, v4 row_newbcast:0 row_mask:0xf bank_mask:0xf\n"
"v_cvt_pk_bf16_f32 v34, v36, v37\n"
"v_exp_f32 v178, v178\n"
"v_sub_f32 v250, v254, v250\n"
"v_exp_f32 v252, v252\n"
"v_mfma_f32_16x16x32_bf16 v[152:155], v[208:211], a[0:3], v[180:183]\n"
"ds_read_b64_tr_b16 a[78:79], v249 offset:50184\n"
"ds_read_b128 v[60:63], v244 offset:35840\n"
"v_mfma_f32_16x16x32_bf16 v[152:155], v[212:215], a[4:7], v[152:155]\n"
"ds_write_b128 v248, v[64:67] offset:51200\n"
"v_cvt_pk_bf16_f32 v133, v158, v159\n"
"v_cvt_pk_bf16_f32 v2, v4, v5\n"
"v_cvt_pk_bf16_f32 v35, v38, v39\n"
"v_exp_f32 v250, v250\n"
"v_mul_f32 v253, v253, v252\n"
"v_mfma_f32_16x16x32_bf16 v[152:155], v[216:219], a[8:11], v[152:155]\n"
"s_waitcnt lgkmcnt(11)\n"
"ds_read_b64_tr_b16 a[60:61], v249 offset:49152\n"
"ds_read_b64_tr_b16 a[82:83], v249 offset:51208\n"
"v_mfma_f32_16x16x32_bf16 v[152:155], v[220:223], a[12:15], v[152:155]\n"
"ds_write_b128 v248, v[0:3] offset:49152\n"
"ds_write_b128 v248, v[32:35] offset:50176\n"
"v_mul_f32_dpp v50, v247, v50 row_newbcast:2 row_mask:0xf bank_mask:0xf\n"
"v_mfma_f32_16x16x32_bf16 v[156:159], v[216:219], a[24:27], v[184:187]\n"
"ds_read_b64_tr_b16 a[66:67], v249 offset:50184\n"
"ds_read_b64_tr_b16 a[80:81], v249 offset:51200\n"
"v_mfma_f32_16x16x32_bf16 v[156:159], v[220:223], a[28:31], v[156:159]\n"
"ds_read_b64_tr_b16 a[48:49], v249 offset:49152\n"
"ds_read_b128 v[56:59], v244 offset:34816\n"
"v_mfma_f32_16x16x32_bf16 v[156:159], v[208:211], a[16:19], v[156:159]\n"
"ds_read_b64_tr_b16 v[224:225], v239 offset:0\n"
"v_cvt_pk_bf16_f32 v143, v174, v175\n"
"v_mfma_f32_16x16x32_bf16 v[156:159], v[212:215], a[20:23], v[156:159]\n"
"ds_read_b64_tr_b16 a[50:51], v249 offset:49160\n"
"ds_read_b64_tr_b16 a[64:65], v249 offset:50176\n"
"ds_read_b64_tr_b16 a[244:245], v234 offset:0\n"
"ds_read_b64_tr_b16 a[246:247], v234 offset:4096\n"
"v_cvt_pk_bf16_f32 v140, v160, v161\n"
"v_cvt_pk_bf16_f32 v141, v162, v163\n"
"v_cvt_pk_bf16_f32 v135, v170, v171\n"
"v_cvt_pk_bf16_f32 v150, v176, v177\n"
"v_fmac_f32 v253, v251, v250\n"
"v_xor_b32 v248, s75, v248\n"
"v_mfma_f32_16x16x32_bf16 v[160:163], v[208:211], a[32:35], v[188:191]\n"
"ds_read_b64_tr_b16 a[248:249], v238 offset:0\n"
"v_mul_f32_dpp v51, v247, v51 row_newbcast:3 row_mask:0xf bank_mask:0xf\n"
"v_mfma_f32_16x16x32_bf16 v[160:163], v[212:215], a[36:39], v[160:163]\n"
"v_cvt_pk_bf16_f32 v148, v164, v165\n"
"v_cvt_pk_bf16_f32 v149, v166, v167\n"
"v_mfma_f32_16x16x32_bf16 v[160:163], v[216:219], a[40:43], v[160:163]\n"
"v_rcp_f32 v253, v253\n"
"v_mfma_f32_16x16x32_bf16 v[160:163], v[220:223], a[44:47], v[160:163]\n"
"ds_read_b64_tr_b16 a[252:253], v235 offset:0\n"
"v_mul_f32 v250, v250, v253\n"
"v_mfma_f32_16x16x32_bf16 v[164:167], v[216:219], a[200:203], v[192:195]\n"
"ds_read_b64_tr_b16 v[226:227], v239 offset:4096\n"
"v_mul_f32 v252, v252, v253\n"
"v_mfma_f32_16x16x32_bf16 v[164:167], v[220:223], a[204:207], v[164:167]\n"
"v_exp_f32 v168, v168\n"
"v_mfma_f32_16x16x32_bf16 v[164:167], v[208:211], a[192:195], v[164:167]\n"
"ds_read_b128 v[216:219], v228 offset:21504\n"
"v_mov_b32_dpp v250, v250 row_ror:12 row_mask:0x2 bank_mask:0xf\n"
"v_mfma_f32_16x16x32_bf16 v[164:167], v[212:215], a[196:199], v[164:167]\n"
"s_waitcnt lgkmcnt(4)\n"
"ds_read_b64_tr_b16 a[254:255], v235 offset:4096\n"
"v_exp_f32 v173, v173\n"
"v_mov_b32_dpp v252, v252 row_ror:12 row_mask:0x2 bank_mask:0xf\n"
"v_accvgpr_read_b32 v254, a208\n"
"v_mul_f32_dpp v22, v246, v22 row_newbcast:2 row_mask:0xf bank_mask:0xf\n"
"v_mul_f32_dpp v80, v206, v80 row_newbcast:0 row_mask:0xf bank_mask:0xf\n"
"v_exp_f32 v156, v156\n"
"v_mfma_f32_16x16x32_bf16 a[216:219], v[124:127], a[244:247], 0\n"
"ds_read_b128 v[220:223], v229 offset:21504\n"
"v_cvt_pk_bf16_f32 v134, v168, v169\n"
"v_cvt_pk_bf16_f32 v142, v172, v173\n"
"v_mov_b32_dpp v250, v250 row_ror:8 row_mask:0x4 bank_mask:0xf\n"
"v_accvgpr_read_b32 v251, a211\n"
"v_mul_f32_dpp v84, v206, v84 row_newbcast:0 row_mask:0xf bank_mask:0xf\n"
"v_mov_b64 v[196:197], 0\n"
"v_exp_f32 v154, v154\n"
"v_exp_f32 v157, v157\n"
"v_mfma_f32_16x16x32_bf16 v[8:11], v[128:131], a[244:247], 0\n"
"s_waitcnt lgkmcnt(5)\n"
"s_add_u32 m0, s44, 98304\n"
"v_exp_f32 v179, v179\n"
"v_mfma_f32_16x16x32_bf16 v[8:11], v[132:135], a[248:251], v[8:11]\n"
"buffer_load_dwordx4 v240, s[20:23], s56 offen nt lds\n"
"v_mfma_f32_16x16x32_bf16 v[40:43], v[136:139], a[244:247], 0\n"
"v_exp_f32 v159, v159\n"
"v_mfma_f32_16x16x32_bf16 v[40:43], v[140:143], a[248:251], v[40:43]\n"
"v_cvt_pk_bf16_f32 v151, v178, v179\n"
"v_mov_b32_dpp v252, v252 row_ror:8 row_mask:0x4 bank_mask:0xf\n"
"v_mfma_f32_16x16x32_bf16 v[72:75], v[144:147], a[244:247], 0\n"
"v_mov_b32_dpp v250, v250 row_ror:4 row_mask:0x8 bank_mask:0xf\n"
"v_mul_f32_dpp v23, v246, v23 row_newbcast:3 row_mask:0xf bank_mask:0xf\n"
"v_mfma_f32_16x16x32_bf16 v[72:75], v[148:151], a[248:251], v[72:75]\n"
"s_waitcnt lgkmcnt(2)\n"
"ds_read_b128 v[212:215], v229 offset:20480\n"
"v_accvgpr_read_b32 v253, a210\n"
"v_mul_f32_dpp v27, v250, v27 row_newbcast:3 row_mask:0xf bank_mask:0xf\n"
"v_mul_f32_dpp v21, v246, v21 row_newbcast:1 row_mask:0xf bank_mask:0xf\n"
"v_mul_f32_dpp v86, v206, v86 row_newbcast:2 row_mask:0xf bank_mask:0xf\n"
"v_mul_f32_dpp v57, v250, v57 row_newbcast:1 row_mask:0xf bank_mask:0xf\n"
"v_exp_f32 v158, v158\n"
"v_exp_f32 v161, v161\n"
"v_mfma_f32_16x16x32_bf16 v[76:79], v[148:151], v[224:227], 0\n"
"s_waitcnt lgkmcnt(2)\n"
"ds_read_b128 v[208:211], v228 offset:20480\n"
"v_mov_b32_dpp v252, v252 row_ror:4 row_mask:0x8 bank_mask:0xf\n"
"v_mfma_f32_16x16x32_bf16 v[76:79], v[144:147], a[252:255], v[76:79]\n"
"ds_read_b64_tr_b16 a[248:249], v238 offset:1024\n"
"v_mul_f32_dpp v24, v250, v24 row_newbcast:0 row_mask:0xf bank_mask:0xf\n"
"v_mfma_f32_16x16x32_bf16 v[44:47], v[140:143], v[224:227], 0\n"
"v_mul_f32_dpp v26, v250, v26 row_newbcast:2 row_mask:0xf bank_mask:0xf\n"
"v_mul_f32_dpp v30, v250, v30 row_newbcast:2 row_mask:0xf bank_mask:0xf\n"
"v_mfma_f32_16x16x32_bf16 v[44:47], v[136:139], a[252:255], v[44:47]\n"
"ds_read_b64_tr_b16 a[244:245], v234 offset:1024\n"
"v_fmac_f32_dpp v26, v252, v253 row_newbcast:2 row_mask:0xf bank_mask:0xf\n"
"v_mfma_f32_16x16x32_bf16 v[12:15], v[132:135], v[224:227], 0\n"
"s_add_u32 s40, s56, s52\n"
"ds_read_b64_tr_b16 v[224:225], v239 offset:1024\n"
"v_mul_f32_dpp v25, v250, v25 row_newbcast:1 row_mask:0xf bank_mask:0xf\n"
"v_mfma_f32_16x16x32_bf16 v[12:15], v[128:131], a[252:255], v[12:15]\n"
"buffer_load_dwordx4 v240, s[20:23], s40 offen offset:1024 nt lds\n"
"v_fmac_f32_dpp v24, v252, v254 row_newbcast:0 row_mask:0xf bank_mask:0xf\n"
"v_fmac_f32_dpp v25, v252, v255 row_newbcast:1 row_mask:0xf bank_mask:0xf\n"
"v_accvgpr_read_b32 v254, a212\n"
"v_mul_f32_dpp v31, v250, v31 row_newbcast:3 row_mask:0xf bank_mask:0xf\n"
"v_mul_f32_dpp v62, v250, v62 row_newbcast:2 row_mask:0xf bank_mask:0xf\n"
"v_mul_f32_dpp v63, v250, v63 row_newbcast:3 row_mask:0xf bank_mask:0xf\n"
"v_exp_f32 v163, v163\n"
"v_mfma_f32_16x16x32_bf16 a[220:223], v[124:127], a[252:255], 0\n"
"v_mul_f32_dpp v28, v250, v28 row_newbcast:0 row_mask:0xf bank_mask:0xf\n"
"v_mul_f32_dpp v60, v250, v60 row_newbcast:0 row_mask:0xf bank_mask:0xf\n"
"v_smfmac_f32_16x16x64_bf16 v[196:199], a[240:243], v[128:135], v241\n"
"v_accvgpr_read_b32 v253, a214\n"
"v_mul_f32_dpp v53, v247, v53 row_newbcast:1 row_mask:0xf bank_mask:0xf\n"
"v_mfma_f32_16x16x32_bf16 v[202:205], a[240:243], v[124:127], v[202:205]\n"
"ds_read_b64_tr_b16 a[250:251], v238 offset:5120\n"
"v_fmac_f32_dpp v28, v252, v254 row_newbcast:0 row_mask:0xf bank_mask:0xf\n"
"v_mfma_f32_16x16x32_bf16 v[168:171], v[216:219], a[8:11], v[180:183]\n"
"s_waitcnt lgkmcnt(3)\n"
"v_exp_f32 v155, v155\n"
"v_mfma_f32_16x16x32_bf16 v[168:171], v[220:223], a[12:15], v[168:171]\n"
"ds_read_b64_tr_b16 a[246:247], v234 offset:5120\n"
"v_mul_f32_dpp v85, v206, v85 row_newbcast:1 row_mask:0xf bank_mask:0xf\n"
"v_mfma_f32_16x16x32_bf16 v[168:171], v[208:211], a[0:3], v[168:171]\n"
"v_mul_f32_dpp v29, v250, v29 row_newbcast:1 row_mask:0xf bank_mask:0xf\n"
"v_mul_f32_dpp v58, v250, v58 row_newbcast:2 row_mask:0xf bank_mask:0xf\n"
"v_mfma_f32_16x16x32_bf16 v[168:171], v[212:215], a[4:7], v[168:171]\n"
"v_fmac_f32_dpp v27, v252, v251 row_newbcast:3 row_mask:0xf bank_mask:0xf\n"
"v_accvgpr_read_b32 v255, a213\n"
"v_accvgpr_read_b32 v251, a215\n"
"v_fmac_f32_dpp v30, v252, v253 row_newbcast:2 row_mask:0xf bank_mask:0xf\n"
"v_cvt_pk_bf16_f32 v24, v24, v25\n"
"v_cvt_pk_bf16_f32 v25, v26, v27\n"
"v_accvgpr_read_b32 v254, a224\n"
"v_mul_f32_dpp v61, v250, v61 row_newbcast:1 row_mask:0xf bank_mask:0xf\n"
"v_exp_f32 v165, v165\n"
"v_mfma_f32_16x16x32_bf16 v[172:175], v[208:211], a[16:19], v[184:187]\n"
"ds_read_b64_tr_b16 v[226:227], v239 offset:5120\n"
"v_accvgpr_read_b32 v253, a226\n"
"v_mfma_f32_16x16x32_bf16 v[172:175], v[212:215], a[20:23], v[172:175]\n"
"v_fmac_f32_dpp v29, v252, v255 row_newbcast:1 row_mask:0xf bank_mask:0xf\n"
"v_mul_f32_dpp v56, v250, v56 row_newbcast:0 row_mask:0xf bank_mask:0xf\n"
"v_mfma_f32_16x16x32_bf16 v[172:175], v[216:219], a[24:27], v[172:175]\n"
"s_add_u32 s40, s56, s53\n"
"ds_read_b64_tr_b16 a[254:255], v235 offset:5120\n"
"v_mul_f32_dpp v59, v250, v59 row_newbcast:3 row_mask:0xf bank_mask:0xf\n"
"v_mfma_f32_16x16x32_bf16 v[172:175], v[220:223], a[28:31], v[172:175]\n"
"buffer_load_dwordx4 v240, s[20:23], s40 offen offset:2048 nt lds\n"
"v_fmac_f32_dpp v31, v252, v251 row_newbcast:3 row_mask:0xf bank_mask:0xf\n"
"v_mul_f32_dpp v16, v246, v16 row_newbcast:0 row_mask:0xf bank_mask:0xf\n"
"v_mul_f32_dpp v48, v247, v48 row_newbcast:0 row_mask:0xf bank_mask:0xf\n"
"v_mul_f32_dpp v49, v247, v49 row_newbcast:1 row_mask:0xf bank_mask:0xf\n"
"v_accvgpr_read_b32 v255, a225\n"
"v_accvgpr_read_b32 v251, a227\n"
"v_fmac_f32_dpp v56, v252, v254 row_newbcast:0 row_mask:0xf bank_mask:0xf\n"
"v_exp_f32 v167, v167\n"
"v_exp_f32 v171, v171\n"
"v_mfma_f32_16x16x32_bf16 v[176:179], v[216:219], a[40:43], v[188:191]\n"
"v_cvt_pk_bf16_f32 v26, v28, v29\n"
"v_mul_f32_dpp v81, v206, v81 row_newbcast:1 row_mask:0xf bank_mask:0xf\n"
"v_fmac_f32_dpp v57, v252, v255 row_newbcast:1 row_mask:0xf bank_mask:0xf\n"
"v_fmac_f32_dpp v58, v252, v253 row_newbcast:2 row_mask:0xf bank_mask:0xf\n"
"v_fmac_f32_dpp v59, v252, v251 row_newbcast:3 row_mask:0xf bank_mask:0xf\n"
"v_accvgpr_read_b32 v254, a228\n"
"v_accvgpr_read_b32 v255, a229\n"
"v_accvgpr_read_b32 v253, a230\n"
"v_accvgpr_read_b32 v251, a231\n"
"v_cvt_pk_bf16_f32 v56, v56, v57\n"
"v_exp_f32 v152, v152\n"
"v_exp_f32 v153, v153\n"
"v_cvt_pk_bf16_f32 v97, v154, v155\n"
"v_mfma_f32_16x16x32_bf16 v[176:179], v[220:223], a[44:47], v[176:179]\n"
"v_mul_f32_dpp v54, v247, v54 row_newbcast:2 row_mask:0xf bank_mask:0xf\n"
"v_fmac_f32_dpp v60, v252, v254 row_newbcast:0 row_mask:0xf bank_mask:0xf\n"
"v_mfma_f32_16x16x32_bf16 v[176:179], v[208:211], a[32:35], v[176:179]\n"
"v_cvt_pk_bf16_f32 v27, v30, v31\n"
"v_mul_f32_dpp v20, v246, v20 row_newbcast:0 row_mask:0xf bank_mask:0xf\n"
"v_mfma_f32_16x16x32_bf16 v[176:179], v[212:215], a[36:39], v[176:179]\n"
"ds_write_b128 v243, v[24:27] offset:0\n"
"v_mul_f32_dpp v87, v206, v87 row_newbcast:3 row_mask:0xf bank_mask:0xf\n"
"v_fmac_f32_dpp v61, v252, v255 row_newbcast:1 row_mask:0xf bank_mask:0xf\n"
"v_fmac_f32_dpp v62, v252, v253 row_newbcast:2 row_mask:0xf bank_mask:0xf\n"
"v_fmac_f32_dpp v63, v252, v251 row_newbcast:3 row_mask:0xf bank_mask:0xf\n"
"v_cvt_pk_bf16_f32 v57, v58, v59\n"
"v_cvt_pk_bf16_f32 v58, v60, v61\n"
"v_cvt_pk_bf16_f32 v59, v62, v63\n"
"v_cvt_pk_bf16_f32 v96, v152, v153\n"
"v_mfma_f32_16x16x32_bf16 v[152:155], v[208:211], a[192:195], v[192:195]\n"
"ds_write_b128 v243, v[56:59] offset:2048\n"
"v_cvt_pk_bf16_f32 v105, v158, v159\n"
"v_exp_f32 v162, v162\n"
"v_exp_f32 v172, v172\n"
"v_mfma_f32_16x16x32_bf16 v[152:155], v[212:215], a[196:199], v[152:155]\n"
"ds_read_b64_tr_b16 a[252:253], v235 offset:1024\n"
"v_mul_f32_dpp v18, v246, v18 row_newbcast:2 row_mask:0xf bank_mask:0xf\n"
"v_mfma_f32_16x16x32_bf16 v[152:155], v[216:219], a[200:203], v[152:155]\n"
"v_exp_f32 v174, v174\n"
"v_mfma_f32_16x16x32_bf16 v[152:155], v[220:223], a[204:207], v[152:155]\n"
"s_waitcnt lgkmcnt(3)\n"
"ds_read_b128 v[212:215], v231 offset:16384\n"
"v_mul_f32_dpp v55, v247, v55 row_newbcast:3 row_mask:0xf bank_mask:0xf\n"
"v_mfma_f32_16x16x32_bf16 a[232:235], v[124:127], a[244:247], 0\n"
"v_exp_f32 v169, v169\n"
"v_mfma_f32_16x16x32_bf16 v[24:27], v[128:131], a[244:247], 0\n"
"s_add_u32 s40, s56, s54\n"
"ds_read_b128 v[208:211], v230 offset:16384\n"
"v_mul_f32_dpp v17, v246, v17 row_newbcast:1 row_mask:0xf bank_mask:0xf\n"
"v_mfma_f32_16x16x32_bf16 v[24:27], v[132:135], a[248:251], v[24:27]\n"
"buffer_load_dwordx4 v240, s[20:23], s40 offen offset:3072 nt lds\n"
"v_mfma_f32_16x16x32_bf16 v[56:59], v[136:139], a[244:247], 0\n"
"s_add_u32 s55, s55, s59\n"
"v_exp_f32 v173, v173\n"
"v_mfma_f32_16x16x32_bf16 v[56:59], v[140:143], a[248:251], v[56:59]\n"
"s_add_u32 s41, s55, s57\n"
"v_exp_f32 v160, v160\n"
"v_mfma_f32_16x16x32_bf16 v[88:91], v[144:147], a[244:247], 0\n"
"s_and_b32 s41, s41, s58\n"
"v_exp_f32 v166, v166\n"
"v_mfma_f32_16x16x32_bf16 v[88:91], v[148:151], a[248:251], v[88:91]\n"
"s_andn2_b32 s56, s55, s58\n"
"ds_read_b64_tr_b16 a[248:249], v236 offset:0\n"
"v_mov_b64 v[200:201], 0\n"
"v_mfma_f32_16x16x32_bf16 v[92:95], v[148:151], v[224:227], 0\n"
"s_waitcnt lgkmcnt(2)\n"
"s_or_b32 s56, s56, s41\n"
"ds_read_b128 v[220:223], v231 offset:17408\n"
"v_cvt_pk_bf16_f32 v16, v16, v17\n"
"v_mfma_f32_16x16x32_bf16 v[92:95], v[144:147], a[252:255], v[92:95]\n"
"ds_read_b128 v[216:219], v230 offset:17408\n"
"v_exp_f32 v164, v164\n"
"v_exp_f32 v178, v178\n"
"v_exp_f32 v179, v179\n"
"v_mfma_f32_16x16x32_bf16 v[60:63], v[140:143], v[224:227], 0\n"
"s_add_u32 m0, s44, 49152\n"
"ds_read_b64_tr_b16 a[244:245], v232 offset:0\n"
"v_mul_f32_dpp v19, v246, v19 row_newbcast:3 row_mask:0xf bank_mask:0xf\n"
"v_mfma_f32_16x16x32_bf16 v[60:63], v[136:139], a[252:255], v[60:63]\n"
"buffer_load_dwordx4 v240, s[16:19], s56 offen nt lds\n"
"v_mfma_f32_16x16x32_bf16 v[28:31], v[132:135], v[224:227], 0\n"
"v_mul_f32_dpp v82, v206, v82 row_newbcast:2 row_mask:0xf bank_mask:0xf\n"
"v_cvt_pk_bf16_f32 v112, v160, v161\n"
"v_mfma_f32_16x16x32_bf16 v[28:31], v[128:131], a[252:255], v[28:31]\n"
"v_exp_f32 v154, v154\n"
"v_mfma_f32_16x16x32_bf16 a[236:239], v[124:127], a[252:255], 0\n"
"v_cvt_pk_bf16_f32 v17, v18, v19\n"
"v_cvt_pk_bf16_f32 v104, v156, v157\n"
"v_smfmac_f32_16x16x64_bf16 v[198:201], a[240:243], v[136:143], v241\n"
"s_waitcnt lgkmcnt(3)\n"
"ds_read_b64_tr_b16 v[226:227], v237 offset:4096\n"
"v_cvt_pk_bf16_f32 v48, v48, v49\n"
"v_mfma_f32_16x16x32_bf16 v[156:159], v[208:211], a[0:3], v[180:183]\n"
"v_mul_f32_dpp v52, v247, v52 row_newbcast:0 row_mask:0xf bank_mask:0xf\n"
"v_cvt_pk_bf16_f32 v120, v164, v165\n"
"v_mfma_f32_16x16x32_bf16 v[156:159], v[212:215], a[4:7], v[156:159]\n"
"s_waitcnt lgkmcnt(2)\n"
"ds_read_b64_tr_b16 v[224:225], v237 offset:0\n"
"v_cvt_pk_bf16_f32 v113, v162, v163\n"
"v_mfma_f32_16x16x32_bf16 v[156:159], v[216:219], a[8:11], v[156:159]\n"
"v_exp_f32 v176, v176\n"
"v_mfma_f32_16x16x32_bf16 v[156:159], v[220:223], a[12:15], v[156:159]\n"
"ds_read_b64_tr_b16 a[246:247], v232 offset:4096\n"
"ds_read_b64_tr_b16 a[254:255], v233 offset:4096\n"
"v_mfma_f32_16x16x32_bf16 v[160:163], v[216:219], a[24:27], v[184:187]\n"
"ds_read_b64_tr_b16 a[250:251], v236 offset:4096\n"
"v_cvt_pk_bf16_f32 v49, v50, v51\n"
"v_mfma_f32_16x16x32_bf16 v[160:163], v[220:223], a[28:31], v[160:163]\n"
"v_exp_f32 v168, v168\n"
"v_mfma_f32_16x16x32_bf16 v[160:163], v[208:211], a[16:19], v[160:163]\n"
"s_add_u32 s40, s56, s52\n"
"ds_read_b64_tr_b16 a[252:253], v233 offset:0\n"
"v_cvt_pk_bf16_f32 v121, v166, v167\n"
"v_mfma_f32_16x16x32_bf16 v[160:163], v[212:215], a[20:23], v[160:163]\n"
"buffer_load_dwordx4 v240, s[16:19], s40 offen offset:1024 nt lds\n"
"v_mfma_f32_16x16x32_bf16 v[164:167], v[208:211], a[32:35], v[188:191]\n"
"v_cvt_pk_bf16_f32 v51, v54, v55\n"
"v_add_u32 v243, 0x1000, v243\n"
"v_mfma_f32_16x16x32_bf16 v[164:167], v[212:215], a[36:39], v[164:167]\n"
"v_exp_f32 v177, v177\n"
"v_mfma_f32_16x16x32_bf16 v[164:167], v[216:219], a[40:43], v[164:167]\n"
"ds_read_b128 v[208:211], v230 offset:20480\n"
"v_cvt_pk_bf16_f32 v115, v178, v179\n"
"v_mfma_f32_16x16x32_bf16 v[164:167], v[220:223], a[44:47], v[164:167]\n"
"s_waitcnt lgkmcnt(3)\n"
"v_cvt_pk_bf16_f32 v50, v52, v53\n"
"v_mul_f32_dpp v83, v206, v83 row_newbcast:3 row_mask:0xf bank_mask:0xf\n"
"v_mfma_f32_16x16x32_bf16 a[208:211], v[124:127], a[244:247], 0\n"
"v_cvt_pk_bf16_f32 v19, v22, v23\n"
"v_cvt_pk_bf16_f32 v114, v176, v177\n"
"v_mfma_f32_16x16x32_bf16 v[0:3], v[128:131], a[244:247], 0\n"
"s_waitcnt lgkmcnt(2)\n"
"ds_read_b128 v[216:219], v230 offset:21504\n"
"v_xor_b32 v249, s75, v249\n"
"v_mfma_f32_16x16x32_bf16 v[0:3], v[132:135], a[248:251], v[0:3]\n"
"v_exp_f32 v175, v175\n"
"v_mfma_f32_16x16x32_bf16 v[32:35], v[136:139], a[244:247], 0\n"
"s_add_u32 s40, s56, s53\n"
"ds_read_b128 v[212:215], v231 offset:20480\n"
"v_cvt_pk_bf16_f32 v18, v20, v21\n"
"v_mfma_f32_16x16x32_bf16 v[32:35], v[140:143], a[248:251], v[32:35]\n"
"buffer_load_dwordx4 v240, s[16:19], s40 offen offset:2048 nt lds\n"
"v_mfma_f32_16x16x32_bf16 v[64:67], v[144:147], a[244:247], 0\n"
"v_cvt_pk_bf16_f32 v98, v168, v169\n"
"v_cvt_pk_bf16_f32 v107, v174, v175\n"
"v_mfma_f32_16x16x32_bf16 v[64:67], v[148:151], a[248:251], v[64:67]\n"
"v_exp_f32 v153, v153\n"
"v_mfma_f32_16x16x32_bf16 v[68:71], v[148:151], v[224:227], 0\n"
"s_waitcnt lgkmcnt(2)\n"
"ds_read_b128 v[220:223], v231 offset:21504\n"
"v_cvt_pk_bf16_f32 v106, v172, v173\n"
"v_mfma_f32_16x16x32_bf16 v[68:71], v[144:147], a[252:255], v[68:71]\n"
"ds_write_b128 v248, v[48:51] offset:50176\n"
"v_cvt_pk_bf16_f32 v80, v80, v81\n"
"v_cvt_pk_bf16_f32 v81, v82, v83\n"
"v_exp_f32 v170, v170\n"
"v_exp_f32 v159, v159\n"
"v_mfma_f32_16x16x32_bf16 v[36:39], v[140:143], v[224:227], 0\n"
"v_exp_f32 v167, v167\n"
"v_mfma_f32_16x16x32_bf16 v[36:39], v[136:139], a[252:255], v[36:39]\n"
"ds_read_b64_tr_b16 a[74:75], v249 offset:50184\n"
"ds_read_b64_tr_b16 a[244:245], v232 offset:1024\n"
"v_mfma_f32_16x16x32_bf16 v[4:7], v[132:135], v[224:227], 0\n"
"ds_read_b64_tr_b16 a[72:73], v249 offset:50176\n"
"v_cvt_pk_bf16_f32 v83, v86, v87\n"
"v_mfma_f32_16x16x32_bf16 v[4:7], v[128:131], a[252:255], v[4:7]\n"
"v_exp_f32 v166, v166\n"
"v_mfma_f32_16x16x32_bf16 a[212:215], v[124:127], a[252:255], 0\n"
"s_add_u32 s40, s56, s54\n"
"ds_write_b128 v248, v[16:19] offset:49152\n"
"v_cvt_pk_bf16_f32 v99, v170, v171\n"
"v_exp_f32 v161, v161\n"
"v_exp_f32 v164, v164\n"
"v_smfmac_f32_16x16x64_bf16 v[200:203], a[240:243], v[144:151], v241\n"
"buffer_load_dwordx4 v240, s[16:19], s40 offen offset:3072 nt lds\n"
"v_mfma_f32_16x16x32_bf16 v[168:171], v[208:211], a[0:3], v[180:183]\n"
"s_waitcnt lgkmcnt(5)\n"
"ds_read_b64_tr_b16 a[58:59], v249 offset:49160\n"
"ds_read_b64_tr_b16 a[246:247], v232 offset:5120\n"
"v_mfma_f32_16x16x32_bf16 v[168:171], v[212:215], a[4:7], v[168:171]\n"
"ds_read_b64_tr_b16 a[56:57], v249 offset:49152\n"
"v_cvt_pk_bf16_f32 v82, v84, v85\n"
"v_mfma_f32_16x16x32_bf16 v[168:171], v[216:219], a[8:11], v[168:171]\n"
"ds_write_b128 v248, v[80:83] offset:51200\n"
"v_exp_f32 v156, v156\n"
"v_exp_f32 v158, v158\n"
"v_exp_f32 v165, v165\n"
"v_mfma_f32_16x16x32_bf16 v[168:171], v[220:223], a[12:15], v[168:171]\n"
"ds_read_b64_tr_b16 a[88:89], v249 offset:51200\n"
"ds_read_b64_tr_b16 a[90:91], v249 offset:51208\n"
"v_mfma_f32_16x16x32_bf16 v[172:175], v[216:219], a[24:27], v[184:187]\n"
"ds_read_b64_tr_b16 a[248:249], v236 offset:1024\n"
"ds_read_b64_tr_b16 a[250:251], v236 offset:5120\n"
"v_mfma_f32_16x16x32_bf16 v[172:175], v[220:223], a[28:31], v[172:175]\n"
"v_xor_b32 v248, s75, v248\n"
"v_cvt_pk_bf16_f32 v117, v166, v167\n"
"v_mfma_f32_16x16x32_bf16 v[172:175], v[208:211], a[16:19], v[172:175]\n"
"ds_read_b64_tr_b16 v[226:227], v237 offset:5120\n"
"v_mfma_f32_16x16x32_bf16 v[172:175], v[212:215], a[20:23], v[172:175]\n"
"s_cbranch_vccnz LPCT1_%=\nLPKT1_%=:\n"
"ds_read_b64_tr_b16 a[254:255], v233 offset:5120\n"
"v_mfma_f32_16x16x32_bf16 v[176:179], v[208:211], a[32:35], v[188:191]\n"
"v_exp_f32 v163, v163\n"
"v_mfma_f32_16x16x32_bf16 v[176:179], v[212:215], a[36:39], v[176:179]\n"
"ds_read_b64_tr_b16 v[224:225], v237 offset:1024\n"
"v_cvt_pk_bf16_f32 v101, v158, v159\n"
"v_mfma_f32_16x16x32_bf16 v[176:179], v[216:219], a[40:43], v[176:179]\n"
"v_exp_f32 v175, v175\n"
"v_mfma_f32_16x16x32_bf16 v[176:179], v[220:223], a[44:47], v[176:179]\n"
"s_waitcnt lgkmcnt(5)\n"
"ds_read_b64_tr_b16 a[252:253], v233 offset:1024\n"
"v_mfma_f32_16x16x32_bf16 a[224:227], v[124:127], a[244:247], 0\n"
"v_exp_f32 v160, v160\n"
"v_mfma_f32_16x16x32_bf16 v[16:19], v[128:131], a[244:247], 0\n"
"s_waitcnt vmcnt(8) lgkmcnt(4)\n"
"v_exp_f32 v173, v173\n"
"v_mfma_f32_16x16x32_bf16 v[16:19], v[132:135], a[248:251], v[16:19]\n"
"s_barrier\n"
"ds_read_b128 v[216:219], v228 offset:33792\n"
"v_exp_f32 v155, v155\n"
"v_exp_f32 v157, v157\n"
"v_mfma_f32_16x16x32_bf16 v[48:51], v[136:139], a[244:247], 0\n"
"ds_read_b128 v[220:223], v229 offset:33792\n"
"v_cvt_pk_bf16_f32 v100, v156, v157\n"
"v_mfma_f32_16x16x32_bf16 v[48:51], v[140:143], a[248:251], v[48:51]\n"
"ds_read_b128 v[208:211], v228 offset:32768\n"
"ds_read_b128 v[212:215], v229 offset:32768\n"
"v_mfma_f32_16x16x32_bf16 v[80:83], v[144:147], a[244:247], 0\n"
"ds_read_b64_tr_b16 a[244:245], v234 offset:16384\n"
"ds_read_b64_tr_b16 a[246:247], v234 offset:20480\n"
"v_mfma_f32_16x16x32_bf16 v[80:83], v[148:151], a[248:251], v[80:83]\n"
"s_waitcnt lgkmcnt(7)\n"
"ds_read_b64_tr_b16 a[248:249], v238 offset:16384\n"
"ds_read_b64_tr_b16 a[250:251], v238 offset:20480\n"
"v_mfma_f32_16x16x32_bf16 v[84:87], v[148:151], v[224:227], 0\n"
"s_waitcnt lgkmcnt(8)\n"
"v_exp_f32 v162, v162\n"
"v_mfma_f32_16x16x32_bf16 v[84:87], v[144:147], a[252:255], v[84:87]\n"
"v_exp_f32 v152, v152\n"
"v_mfma_f32_16x16x32_bf16 v[52:55], v[140:143], v[224:227], 0\n"
"v_exp_f32 v178, v178\n"
"v_mfma_f32_16x16x32_bf16 v[52:55], v[136:139], a[252:255], v[52:55]\n"
"v_cvt_pk_bf16_f32 v122, v152, v153\n"
"v_cvt_pk_bf16_f32 v109, v162, v163\n"
"v_mfma_f32_16x16x32_bf16 v[20:23], v[132:135], v[224:227], 0\n"
"ds_read_b64_tr_b16 v[226:227], v239 offset:20480\n"
"v_cvt_pk_bf16_f32 v123, v154, v155\n"
"v_mfma_f32_16x16x32_bf16 v[20:23], v[128:131], a[252:255], v[20:23]\n"
"v_exp_f32 v169, v169\n"
"v_mfma_f32_16x16x32_bf16 a[228:231], v[124:127], a[252:255], 0\n"
"s_waitcnt lgkmcnt(3)\n"
"v_xor_b32 v249, s75, v249\n"
"s_branch LAY2_%=\n"
"LAX_%=:\n"
"v_mfma_f32_16x16x32_bf16 a[224:227], v[124:127], a[244:247], a[224:227]\n"
"v_exp_f32 v168, v168\n"
"v_mfma_f32_16x16x32_bf16 v[16:19], v[128:131], a[244:247], v[16:19]\n"
"s_waitcnt vmcnt(8) lgkmcnt(0)\n"
"v_exp_f32 v169, v169\n"
"v_mfma_f32_16x16x32_bf16 v[16:19], v[132:135], a[248:251], v[16:19]\n"
"s_barrier\n"
"v_mfma_f32_16x16x32_bf16 v[48:51], v[136:139], a[244:247], v[48:51]\n"
"v_exp_f32 v175, v175\n"
"v_mfma_f32_16x16x32_bf16 v[48:51], v[140:143], a[248:251], v[48:51]\n"
"v_exp_f32 v178, v178\n"
"v_mfma_f32_16x16x32_bf16 v[80:83], v[144:147], a[244:247], v[80:83]\n"
"ds_read_b64_tr_b16 a[246:247], v234 offset:53248\n"
"v_cvt_pk_bf16_f32 v109, v162, v163\n"
"v_mfma_f32_16x16x32_bf16 v[80:83], v[148:151], a[248:251], v[80:83]\n"
"ds_read_b64_tr_b16 a[244:245], v234 offset:49152\n"
"v_cvt_pk_bf16_f32 v123, v154, v155\n"
"v_cvt_pk_bf16_f32 v116, v164, v165\n"
"v_exp_f32 v174, v174\n"
"v_exp_f32 v176, v176\n"
"v_exp_f32 v177, v177\n"
"v_exp_f32 v179, v179\n"
"v_accvgpr_read_b32 v253, a96\n"
"v_mfma_f32_16x16x32_bf16 v[84:87], v[148:151], v[224:227], v[84:87]\n"
"ds_read_b64_tr_b16 a[248:249], v238 offset:49152\n"
"v_cvt_pk_bf16_f32 v111, v174, v175\n"
"v_mfma_f32_16x16x32_bf16 v[84:87], v[144:147], a[252:255], v[84:87]\n"
"ds_read_b64_tr_b16 a[250:251], v238 offset:53248\n"
"v_exp_f32 v156, v156\n"
"v_cvt_pk_bf16_f32 v101, v158, v159\n"
"v_cvt_pk_bf16_f32 v108, v160, v161\n"
"v_exp_f32 v170, v170\n"
"v_exp_f32 v171, v171\n"
"v_cvt_pk_bf16_f32 v118, v176, v177\n"
"v_and_b32 v245, 0x3f0, v242\n"
"v_mfma_f32_16x16x32_bf16 v[52:55], v[140:143], v[224:227], v[52:55]\n"
"v_exp_f32 v172, v172\n"
"v_mfma_f32_16x16x32_bf16 v[52:55], v[136:139], a[252:255], v[52:55]\n"
"v_cvt_pk_bf16_f32 v119, v178, v179\n"
"v_and_b32 v253, 0x7fff0000, v253\n"
"v_mfma_f32_16x16x32_bf16 v[20:23], v[132:135], v[224:227], v[20:23]\n"
"v_cvt_pk_bf16_f32 v102, v168, v169\n"
"v_cvt_pk_bf16_f32 v103, v170, v171\n"
"v_mfma_f32_16x16x32_bf16 v[20:23], v[128:131], a[252:255], v[20:23]\n"
"v_cvt_pk_bf16_f32 v100, v156, v157\n"
"v_lshlrev_b32_e64 v244, 24, s43\n"
"v_mfma_f32_16x16x32_bf16 a[228:231], v[124:127], a[252:255], a[228:231]\n"
"s_waitcnt lgkmcnt(2)\n"
"ds_read_b64_tr_b16 v[224:225], v239 offset:49152\n"
"v_cvt_pk_bf16_f32 v117, v166, v167\n"
"v_mfma_f32_16x16x32_bf16 a[216:219], v[120:123], a[244:247], a[216:219]\n"
"s_mov_b64 s[28:29], s[8:9]\n"
"v_exp_f32 v173, v173\n"
"v_smfmac_f32_16x16x64_bf16 v[196:199], a[240:243], v[96:103], v241\n"
"s_mov_b32 s30, 655360\n"
"ds_read_b64_tr_b16 a[254:255], v235 offset:53248\n"
"ds_read_b64_tr_b16 v[226:227], v239 offset:53248\n"
"v_mfma_f32_16x16x32_bf16 v[8:11], v[96:99], a[244:247], v[8:11]\n"
"s_waitcnt lgkmcnt(3)\n"
"s_mov_b32 s71, s44\n"
"ds_read_b64_tr_b16 a[252:253], v235 offset:49152\n"
"v_max_u32_dpp v253, v253, v253 quad_perm:[1,0,3,2] row_mask:0xf bank_mask:0xf\n"
"v_mfma_f32_16x16x32_bf16 v[8:11], v[100:103], a[248:251], v[8:11]\n"
"s_mov_b32 s70, s95\n"
"v_and_b32 v252, 0x7fff0000, v252\n"
"v_max_u32_dpp v253, v253, v253 quad_perm:[2,3,0,1] row_mask:0xf bank_mask:0xf\n"
"v_smfmac_f32_16x16x64_bf16 v[200:203], a[240:243], v[112:119], v241\n"
"v_cvt_pk_bf16_f32 v110, v172, v173\n"
"v_max_u32_dpp v252, v252, v252 quad_perm:[1,0,3,2] row_mask:0xf bank_mask:0xf\n"
"v_mfma_f32_16x16x32_bf16 v[40:43], v[104:107], a[244:247], v[40:43]\n"
"s_nop 0\n"
"v_max_u32_dpp v252, v252, v252 quad_perm:[2,3,0,1] row_mask:0xf bank_mask:0xf\n"
"v_max_u32_dpp v253, v253, v253 row_half_mirror row_mask:0xf bank_mask:0xf\n"
"v_mfma_f32_16x16x32_bf16 v[40:43], v[108:111], a[248:251], v[40:43]\n"
"s_add_u32 s41, s70, 327680\n"
"s_add_u32 m0, s71, 32768\n"
"v_add_f32 v196, v196, v197\n"
"v_smfmac_f32_16x16x64_bf16 v[198:201], a[240:243], v[104:111], v241\n"
"buffer_load_dwordx4 v240, s[28:31], s41 offen lds\n"
"v_mfma_f32_16x16x32_bf16 v[72:75], v[112:115], a[244:247], v[72:75]\n"
"v_min_u32_dpp v253, v253, v253 row_mirror row_mask:0xf bank_mask:0xf\n"
"v_lshrrev_b32 v245, 1, v245\n"
"v_mfma_f32_16x16x32_bf16 v[72:75], v[116:119], a[248:251], v[72:75]\n"
"v_max_u32_dpp v252, v252, v252 row_half_mirror row_mask:0xf bank_mask:0xf\n"
"v_min_u32_dpp v253, v253, v253 row_bcast:15 row_mask:0xa bank_mask:0xf\n"
"v_mfma_f32_16x16x32_bf16 v[202:205], a[240:243], v[120:123], v[202:205]\n"
"s_waitcnt lgkmcnt(0)\n"
"ds_read_b64_tr_b16 a[244:245], v234 offset:50176\n"
"ds_read_b64_tr_b16 a[248:249], v238 offset:50176\n"
"v_mfma_f32_16x16x32_bf16 v[76:79], v[116:119], v[224:227], v[76:79]\n"
"s_add_u32 s40, s41, s52\n"
"ds_read_b64_tr_b16 a[246:247], v234 offset:54272\n"
"ds_read_b64_tr_b16 a[250:251], v238 offset:54272\n"
"v_mfma_f32_16x16x32_bf16 v[76:79], v[112:115], a[252:255], v[76:79]\n"
"buffer_load_dwordx4 v240, s[28:31], s40 offen offset:1024 lds\n"
"v_mfma_f32_16x16x32_bf16 v[44:47], v[108:111], v[224:227], v[44:47]\n"
"v_min_u32_dpp v252, v252, v252 row_mirror row_mask:0xf bank_mask:0xf\n"
"v_mul_f32 v244, 0x45800000, v244\n"
"v_mfma_f32_16x16x32_bf16 v[44:47], v[104:107], a[252:255], v[44:47]\n"
"v_add_f32 v198, v198, v199\n"
"v_add_f32 v244, v244, v202\n"
"v_mfma_f32_16x16x32_bf16 v[12:15], v[100:103], v[224:227], v[12:15]\n"
"s_add_u32 s40, s41, s53\n"
"ds_read_b64_tr_b16 v[224:225], v239 offset:50176\n"
"ds_read_b64_tr_b16 v[226:227], v239 offset:54272\n"
"v_mfma_f32_16x16x32_bf16 v[12:15], v[96:99], a[252:255], v[12:15]\n"
"buffer_load_dwordx4 v240, s[28:31], s40 offen offset:2048 lds\n"
"v_min_u32_dpp v252, v252, v252 row_bcast:15 row_mask:0xa bank_mask:0xf\n"
"v_add_f32 v200, v200, v201\n"
"v_min3_f32 v244, v244, v196, v198\n"
"v_min_f32 v244, v244, v200\n"
"v_rcp_f32 v244, v244\n"
"v_sub_u32 v245, v242, v245\n"
"v_mfma_f32_16x16x32_bf16 a[220:223], v[120:123], a[252:255], a[220:223]\n"
"s_waitcnt lgkmcnt(3)\n"
"v_min_u32_dpp v253, v253, v253 row_bcast:31 row_mask:0xc bank_mask:0xf\n"
"v_add_f32 v254, v196, v198\n"
"v_add_f32 v254, v254, v200\n"
"v_add_f32 v254, v254, v202\n"
"v_fmac_f32 v254, 0x68800000, v244\n"
"v_cmp_nge_f32 vcc, s45, v254\n"
"s_cbranch_vccnz LSLOW_%=\nLBACK3_%=:\ns_mov_b64 vcc, s[64:65]\n"
"v_mfma_f32_16x16x32_bf16 a[232:235], v[120:123], a[244:247], a[232:235]\n"
"ds_read_b64_tr_b16 a[252:253], v235 offset:50176\n"
"ds_read_b64_tr_b16 a[254:255], v235 offset:54272\n"
"v_min_u32_dpp v252, v252, v252 row_bcast:31 row_mask:0xc bank_mask:0xf\n"
"v_rcp_f32 v246, v196\n"
"v_xor_b32 v244, 0x11000, v242\n"
"v_bfe_u32 v254, v242, 9, 1\n"
"v_mfma_f32_16x16x32_bf16 v[24:27], v[96:99], a[244:247], v[24:27]\n"
"s_waitcnt lgkmcnt(4)\n"
"v_rcp_f32 v247, v198\n"
"v_mfma_f32_16x16x32_bf16 v[24:27], v[100:103], a[248:251], v[24:27]\n"
"ds_write_b128 v244, a[216:219] offset:32768\n"
"v_rcp_f32 v206, v200\n"
"v_mov_b32_dpp v246, v246 row_ror:12 row_mask:0x2 bank_mask:0xf\n"
"v_mov_b32_dpp v247, v247 row_ror:12 row_mask:0x2 bank_mask:0xf\n"
"v_mfma_f32_16x16x32_bf16 v[56:59], v[104:107], a[244:247], v[56:59]\n"
"s_add_u32 s40, s41, s54\n"
"v_mov_b32_dpp v246, v246 row_ror:8 row_mask:0x4 bank_mask:0xf\n"
"v_lshl_add_u32 v254, v254, 4, s43\n"
"v_mfma_f32_16x16x32_bf16 v[56:59], v[108:111], a[248:251], v[56:59]\n"
"buffer_load_dwordx4 v240, s[28:31], s40 offen offset:3072 lds\n"
"v_mfma_f32_16x16x32_bf16 v[88:91], v[112:115], a[244:247], v[88:91]\n"
"s_add_u32 s71, s44, s75\n"
"v_mul_f32 v252, 0x43340000, v252\n"
"v_mov_b32_dpp v246, v246 row_ror:4 row_mask:0x8 bank_mask:0xf\n"
"v_mfma_f32_16x16x32_bf16 v[88:91], v[116:119], a[248:251], v[88:91]\n"
"s_waitcnt lgkmcnt(3)\n"
"s_mov_b32 s70, s100\n"
"ds_read_b64_tr_b16 a[244:245], v232 offset:49152\n"
"ds_read_b64_tr_b16 a[246:247], v232 offset:53248\n"
"ds_read_b64_tr_b16 a[248:249], v236 offset:49152\n"
"ds_read_b64_tr_b16 a[250:251], v236 offset:53248\n"
"v_mov_b32_dpp v247, v247 row_ror:8 row_mask:0x4 bank_mask:0xf\n"
"v_lshl_add_u32 v254, v254, 6, s75\n"
"v_mul_f32_dpp v9, v246, v9 row_newbcast:1 row_mask:0xf bank_mask:0xf\n"
"v_mul_f32_dpp v10, v246, v10 row_newbcast:2 row_mask:0xf bank_mask:0xf\n"
"v_mul_f32_dpp v11, v246, v11 row_newbcast:3 row_mask:0xf bank_mask:0xf\n"
"v_mul_f32_dpp v24, v246, v24 row_newbcast:0 row_mask:0xf bank_mask:0xf\n"
"v_mfma_f32_16x16x32_bf16 v[92:95], v[116:119], v[224:227], v[92:95]\n"
"s_waitcnt lgkmcnt(5)\n"
"s_add_u32 s41, s70, 163840\n"
"s_add_u32 m0, s71, 16384\n"
"v_mul_f32_dpp v14, v246, v14 row_newbcast:2 row_mask:0xf bank_mask:0xf\n"
"v_mfma_f32_16x16x32_bf16 v[92:95], v[112:115], a[252:255], v[92:95]\n"
"buffer_load_dwordx4 v240, s[28:31], s41 offen lds\n"
"v_mfma_f32_16x16x32_bf16 v[60:63], v[108:111], v[224:227], v[60:63]\n"
"v_mul_f32 v252, v252, v253\n"
"v_mov_b32_dpp v206, v206 row_ror:12 row_mask:0x2 bank_mask:0xf\n"
"v_mfma_f32_16x16x32_bf16 v[60:63], v[104:107], a[252:255], v[60:63]\n"
"ds_write_b128 v244, a[220:223] offset:33792\n"
"v_mov_b32_dpp v247, v247 row_ror:4 row_mask:0x8 bank_mask:0xf\n"
"v_sub_u32 v228, v232, v254\n"
"v_mul_f32_dpp v8, v246, v8 row_newbcast:0 row_mask:0xf bank_mask:0xf\n"
"v_mul_f32_dpp v25, v246, v25 row_newbcast:1 row_mask:0xf bank_mask:0xf\n"
"v_mul_f32_dpp v26, v246, v26 row_newbcast:2 row_mask:0xf bank_mask:0xf\n"
"v_mfma_f32_16x16x32_bf16 v[28:31], v[100:103], v[224:227], v[28:31]\n"
"s_add_u32 s40, s41, s52\n"
"v_min_f32 v252, 0x71800000, v252\n"
"v_mul_f32_dpp v12, v246, v12 row_newbcast:0 row_mask:0xf bank_mask:0xf\n"
"v_mfma_f32_16x16x32_bf16 v[28:31], v[96:99], a[252:255], v[28:31]\n"
"buffer_load_dwordx4 v240, s[28:31], s40 offen offset:1024 lds\n"
"v_mfma_f32_16x16x32_bf16 a[236:239], v[120:123], a[252:255], a[236:239]\n"
"s_waitcnt lgkmcnt(3)\n"
"ds_read_b64_tr_b16 a[252:253], v233 offset:49152\n"
"ds_read_b64_tr_b16 a[254:255], v233 offset:53248\n"
"ds_read_b64_tr_b16 v[224:225], v237 offset:49152\n"
"ds_read_b64_tr_b16 v[226:227], v237 offset:53248\n"
"v_readlane_b32 s23, v252, 63\n"
"v_mov_b32_dpp v206, v206 row_ror:8 row_mask:0x4 bank_mask:0xf\n"
"v_cvt_pk_bf16_f32 v8, v8, v9\n"
"v_mul_f32_dpp v47, v247, v47 row_newbcast:3 row_mask:0xf bank_mask:0xf\n"
"v_mul_f32_dpp v58, v247, v58 row_newbcast:2 row_mask:0xf bank_mask:0xf\n"
"v_mul_f32_dpp v60, v247, v60 row_newbcast:0 row_mask:0xf bank_mask:0xf\n"
"v_mfma_f32_16x16x32_bf16 a[208:211], v[120:123], a[244:247], a[208:211]\n"
"ds_write_b128 v244, a[232:235] offset:34816\n"
"v_readlane_b32 s22, v252, 63\n"
"v_mov_b32_dpp v206, v206 row_ror:4 row_mask:0x8 bank_mask:0xf\n"
"v_mul_f32_dpp v15, v246, v15 row_newbcast:3 row_mask:0xf bank_mask:0xf\n"
"v_cvt_pk_bf16_f32 v9, v10, v11\n"
"v_mul_f32_dpp v41, v247, v41 row_newbcast:1 row_mask:0xf bank_mask:0xf\n"
"v_mfma_f32_16x16x32_bf16 v[0:3], v[96:99], a[244:247], v[0:3]\n"
"s_waitcnt lgkmcnt(6)\n"
"v_sub_u32 v231, v235, v254\n"
"v_mov_b64 v[196:197], s[22:23]\n"
"v_mfma_f32_16x16x32_bf16 v[0:3], v[100:103], a[248:251], v[0:3]\n"
"ds_write_b128 v244, a[236:239] offset:35840\n"
"v_mul_f32_dpp v42, v247, v42 row_newbcast:2 row_mask:0xf bank_mask:0xf\n"
"v_mul_f32_dpp v75, v206, v75 row_newbcast:3 row_mask:0xf bank_mask:0xf\n"
"v_mul_f32_dpp v77, v206, v77 row_newbcast:1 row_mask:0xf bank_mask:0xf\n"
"v_cvt_pk_bf16_f32 v24, v24, v25\n"
"v_mul_f32_dpp v63, v247, v63 row_newbcast:3 row_mask:0xf bank_mask:0xf\n"
"v_mfma_f32_16x16x32_bf16 v[32:35], v[104:107], a[244:247], v[32:35]\n"
"s_add_u32 s40, s41, s53\n"
"v_xor_b32 v250, 0x1000, v245\n"
"v_mul_f32_dpp v94, v206, v94 row_newbcast:2 row_mask:0xf bank_mask:0xf\n"
"v_mfma_f32_16x16x32_bf16 v[32:35], v[108:111], a[248:251], v[32:35]\n"
"buffer_load_dwordx4 v240, s[28:31], s40 offen offset:2048 lds\n"
"v_mfma_f32_16x16x32_bf16 v[64:67], v[112:115], a[244:247], v[64:67]\n"
"v_mul_f32_dpp v13, v246, v13 row_newbcast:1 row_mask:0xf bank_mask:0xf\n"
"v_mul_f32_dpp v30, v246, v30 row_newbcast:2 row_mask:0xf bank_mask:0xf\n"
"v_mfma_f32_16x16x32_bf16 v[64:67], v[116:119], a[248:251], v[64:67]\n"
"s_waitcnt lgkmcnt(2)\n"
"ds_read_b64_tr_b16 a[246:247], v232 offset:54272\n"
"ds_read_b64_tr_b16 a[248:249], v236 offset:50176\n"
"v_mov_b32 v252, v192\n"
"v_cvt_pk_bf16_f32 v11, v14, v15\n"
"v_mul_f32_dpp v40, v247, v40 row_newbcast:0 row_mask:0xf bank_mask:0xf\n"
"v_mul_f32_dpp v43, v247, v43 row_newbcast:3 row_mask:0xf bank_mask:0xf\n"
"v_cvt_pk_bf16_f32 v40, v40, v41\n"
"v_mul_f32_dpp v78, v206, v78 row_newbcast:2 row_mask:0xf bank_mask:0xf\n"
"v_mul_f32_dpp v31, v246, v31 row_newbcast:3 row_mask:0xf bank_mask:0xf\n"
"v_mul_f32_dpp v92, v206, v92 row_newbcast:0 row_mask:0xf bank_mask:0xf\n"
"v_mfma_f32_16x16x32_bf16 v[68:71], v[116:119], v[224:227], v[68:71]\n"
"s_add_u32 s40, s41, s54\n"
"ds_read_b64_tr_b16 a[244:245], v232 offset:50176\n"
"ds_read_b64_tr_b16 a[250:251], v236 offset:54272\n"
"v_mfma_f32_16x16x32_bf16 v[68:71], v[112:115], a[252:255], v[68:71]\n"
"buffer_load_dwordx4 v240, s[28:31], s40 offen offset:3072 lds\n"
"v_mov_b32 v253, v202\n"
"v_mul_f32_dpp v44, v247, v44 row_newbcast:0 row_mask:0xf bank_mask:0xf\n"
"v_mul_f32_dpp v45, v247, v45 row_newbcast:1 row_mask:0xf bank_mask:0xf\n"
"v_mul_f32_dpp v46, v247, v46 row_newbcast:2 row_mask:0xf bank_mask:0xf\n"
"v_cvt_pk_bf16_f32 v41, v42, v43\n"
"v_mul_f32_dpp v72, v206, v72 row_newbcast:0 row_mask:0xf bank_mask:0xf\n"
"v_mul_f32_dpp v73, v206, v73 row_newbcast:1 row_mask:0xf bank_mask:0xf\n"
"v_mul_f32_dpp v2, v246, v2 row_newbcast:2 row_mask:0xf bank_mask:0xf\n"
"v_mfma_f32_16x16x32_bf16 v[36:39], v[108:111], v[224:227], v[36:39]\n"
"v_mul_f32_dpp v74, v206, v74 row_newbcast:2 row_mask:0xf bank_mask:0xf\n"
"v_add_u32 v236, s76, v249\n"
"v_mfma_f32_16x16x32_bf16 v[36:39], v[104:107], a[252:255], v[36:39]\n"
"ds_write_b64 v250, v[252:253] offset:52224\n"
"v_cvt_pk_bf16_f32 v10, v12, v13\n"
"v_mul_f32_dpp v76, v206, v76 row_newbcast:0 row_mask:0xf bank_mask:0xf\n"
"v_mul_f32_dpp v79, v206, v79 row_newbcast:3 row_mask:0xf bank_mask:0xf\n"
"v_mul_f32_dpp v93, v206, v93 row_newbcast:1 row_mask:0xf bank_mask:0xf\n"
"v_mul_f32_dpp v3, v246, v3 row_newbcast:3 row_mask:0xf bank_mask:0xf\n"
"v_mul_f32_dpp v33, v247, v33 row_newbcast:1 row_mask:0xf bank_mask:0xf\n"
"v_xor_b32 v232, 0x1000, v228\n"
"v_mfma_f32_16x16x32_bf16 v[4:7], v[100:103], v[224:227], v[4:7]\n"
"ds_read_b64_tr_b16 v[224:225], v237 offset:50176\n"
"ds_read_b64_tr_b16 v[226:227], v237 offset:54272\n"
"v_mfma_f32_16x16x32_bf16 v[4:7], v[96:99], a[252:255], v[4:7]\n"
"ds_write_b128 v248, v[8:11] offset:49152\n"
"v_cvt_pk_bf16_f32 v42, v44, v45\n"
"v_mul_f32_dpp v56, v247, v56 row_newbcast:0 row_mask:0xf bank_mask:0xf\n"
"v_mul_f32_dpp v61, v247, v61 row_newbcast:1 row_mask:0xf bank_mask:0xf\n"
"v_mul_f32_dpp v32, v247, v32 row_newbcast:0 row_mask:0xf bank_mask:0xf\n"
"v_mul_f32_dpp v35, v247, v35 row_newbcast:3 row_mask:0xf bank_mask:0xf\n"
"v_mfma_f32_16x16x32_bf16 a[212:215], v[120:123], a[252:255], a[212:215]\n"
"s_waitcnt lgkmcnt(5)\n"
"ds_read_b64_tr_b16 a[252:253], v233 offset:50176\n"
"ds_read_b64_tr_b16 a[254:255], v233 offset:54272\n"
"v_mfma_f32_16x16x32_bf16 a[224:227], v[120:123], a[244:247], a[224:227]\n"
"ds_read_b64_tr_b16 a[4:5], v249 offset:49152\n"
"v_mul_f32_dpp v62, v247, v62 row_newbcast:2 row_mask:0xf bank_mask:0xf\n"
"v_mfma_f32_16x16x32_bf16 v[16:19], v[96:99], a[244:247], v[16:19]\n"
"s_waitcnt vmcnt(8) lgkmcnt(1)\n"
"ds_read_b64_tr_b16 a[6:7], v249 offset:49160\n"
"v_cvt_pk_bf16_f32 v43, v46, v47\n"
"v_mfma_f32_16x16x32_bf16 v[16:19], v[100:103], a[248:251], v[16:19]\n"
"s_barrier\n"
"ds_read_b64_tr_b16 v[182:183], v228 offset:2048\n"
"ds_read_b64_tr_b16 v[186:187], v232 offset:2048\n"
"ds_read_b64_tr_b16 v[188:189], v228 offset:8192\n"
"ds_read_b64_tr_b16 v[208:209], v228 offset:16384\n"
"v_mul_f32_dpp v28, v246, v28 row_newbcast:0 row_mask:0xf bank_mask:0xf\n"
"v_mul_f32_dpp v57, v247, v57 row_newbcast:1 row_mask:0xf bank_mask:0xf\n"
"v_mul_f32_dpp v36, v247, v36 row_newbcast:0 row_mask:0xf bank_mask:0xf\n"
"v_mul_f32_dpp v38, v247, v38 row_newbcast:2 row_mask:0xf bank_mask:0xf\n"
"v_mfma_f32_16x16x32_bf16 v[48:51], v[104:107], a[244:247], v[48:51]\n"
"ds_read_b64_tr_b16 v[210:211], v228 offset:18432\n"
"v_mul_f32_dpp v90, v206, v90 row_newbcast:2 row_mask:0xf bank_mask:0xf\n"
"v_mfma_f32_16x16x32_bf16 v[48:51], v[108:111], a[248:251], v[48:51]\n"
"ds_read_b64_tr_b16 v[212:213], v232 offset:16384\n"
"ds_read_b64_tr_b16 v[218:219], v228 offset:26624\n"
"ds_read_b64_tr_b16 v[222:223], v232 offset:26624\n"
"ds_read_b64_tr_b16 a[202:203], v236 offset:2056\n"
"v_cvt_pk_bf16_f32 v72, v72, v73\n"
"v_cvt_pk_bf16_f32 v73, v74, v75\n"
"v_cvt_pk_bf16_f32 v56, v56, v57\n"
"v_mul_f32_dpp v0, v246, v0 row_newbcast:0 row_mask:0xf bank_mask:0xf\n"
"v_mul_f32_dpp v6, v246, v6 row_newbcast:2 row_mask:0xf bank_mask:0xf\n"
"v_mul_f32_dpp v64, v206, v64 row_newbcast:0 row_mask:0xf bank_mask:0xf\n"
"v_mfma_f32_16x16x32_bf16 v[80:83], v[112:115], a[244:247], v[80:83]\n"
"ds_write_b128 v248, v[40:43] offset:50176\n"
"v_cvt_pk_bf16_f32 v74, v76, v77\n"
"v_mul_f32_dpp v27, v246, v27 row_newbcast:3 row_mask:0xf bank_mask:0xf\n"
"v_mul_f32_dpp v59, v247, v59 row_newbcast:3 row_mask:0xf bank_mask:0xf\n"
"v_mul_f32_dpp v88, v206, v88 row_newbcast:0 row_mask:0xf bank_mask:0xf\n"
"v_mul_f32_dpp v66, v206, v66 row_newbcast:2 row_mask:0xf bank_mask:0xf\n"
"v_mfma_f32_16x16x32_bf16 v[80:83], v[116:119], a[248:251], v[80:83]\n"
"ds_read_b64_tr_b16 a[22:23], v249 offset:50184\n"
"ds_read_b64_tr_b16 v[180:181], v228 offset:0\n"
"ds_read_b64_tr_b16 v[184:185], v232 offset:0\n"
"ds_read_b64_tr_b16 v[192:193], v232 offset:8192\n"
"v_cvt_pk_bf16_f32 v75, v78, v79\n"
"v_mul_f32_dpp v29, v246, v29 row_newbcast:1 row_mask:0xf bank_mask:0xf\n"
"v_mul_f32_dpp v89, v206, v89 row_newbcast:1 row_mask:0xf bank_mask:0xf\n"
"v_mul_f32_dpp v34, v247, v34 row_newbcast:2 row_mask:0xf bank_mask:0xf\n"
"v_mul_f32_dpp v39, v247, v39 row_newbcast:3 row_mask:0xf bank_mask:0xf\n"
"v_mul_f32_dpp v68, v206, v68 row_newbcast:0 row_mask:0xf bank_mask:0xf\n"
"v_mfma_f32_16x16x32_bf16 v[84:87], v[116:119], v[224:227], v[84:87]\n"
"ds_write_b128 v248, v[72:75] offset:51200\n"
"v_cvt_pk_bf16_f32 v25, v26, v27\n"
"v_cvt_pk_bf16_f32 v26, v28, v29\n"
"v_cvt_pk_bf16_f32 v88, v88, v89\n"
"v_mul_f32_dpp v1, v246, v1 row_newbcast:1 row_mask:0xf bank_mask:0xf\n"
"v_mul_f32_dpp v65, v206, v65 row_newbcast:1 row_mask:0xf bank_mask:0xf\n"
"v_mfma_f32_16x16x32_bf16 v[84:87], v[112:115], a[252:255], v[84:87]\n"
"ds_read_b64_tr_b16 v[194:195], v232 offset:10240\n"
"ds_read_b64_tr_b16 v[214:215], v232 offset:18432\n"
"ds_read_b64_tr_b16 a[250:251], v236 offset:6152\n"
"v_cvt_pk_bf16_f32 v57, v58, v59\n"
"v_mul_f32_dpp v91, v206, v91 row_newbcast:3 row_mask:0xf bank_mask:0xf\n"
"v_mul_f32_dpp v95, v206, v95 row_newbcast:3 row_mask:0xf bank_mask:0xf\n"
"v_mul_f32_dpp v4, v246, v4 row_newbcast:0 row_mask:0xf bank_mask:0xf\n"
"v_mul_f32_dpp v5, v246, v5 row_newbcast:1 row_mask:0xf bank_mask:0xf\n"
"v_cvt_pk_bf16_f32 v0, v0, v1\n"
"v_cvt_pk_bf16_f32 v32, v32, v33\n"
"v_cvt_pk_bf16_f32 v64, v64, v65\n"
"v_mfma_f32_16x16x32_bf16 v[52:55], v[108:111], v[224:227], v[52:55]\n"
"ds_read_b64_tr_b16 v[190:191], v228 offset:10240\n"
"ds_read_b64_tr_b16 v[220:221], v232 offset:24576\n"
"v_mfma_f32_16x16x32_bf16 v[52:55], v[104:107], a[252:255], v[52:55]\n"
"ds_read_b64_tr_b16 a[20:21], v249 offset:50176\n"
"ds_read_b64_tr_b16 a[36:37], v249 offset:51200\n"
"ds_read_b64_tr_b16 a[38:39], v249 offset:51208\n"
"ds_read_b64_tr_b16 v[216:217], v228 offset:24576\n"
"v_cvt_pk_bf16_f32 v27, v30, v31\n"
"v_cvt_pk_bf16_f32 v58, v60, v61\n"
"v_mul_f32_dpp v7, v246, v7 row_newbcast:3 row_mask:0xf bank_mask:0xf\n"
"v_cvt_pk_bf16_f32 v1, v2, v3\n"
"v_mul_f32_dpp v69, v206, v69 row_newbcast:1 row_mask:0xf bank_mask:0xf\n"
"v_xor_b32 v244, 0x1000, v244\n"
"v_mfma_f32_16x16x32_bf16 v[20:23], v[100:103], v[224:227], v[20:23]\n"
"ds_write_b128 v248, v[24:27] offset:49152\n"
"v_sub_u32 v229, v233, v254\n"
"v_cvt_pk_bf16_f32 v59, v62, v63\n"
"v_cvt_pk_bf16_f32 v89, v90, v91\n"
"v_cvt_pk_bf16_f32 v91, v94, v95\n"
"v_cvt_pk_bf16_f32 v2, v4, v5\n"
"v_mfma_f32_16x16x32_bf16 v[20:23], v[96:99], a[252:255], v[20:23]\n"
"ds_write_b128 v248, v[56:59] offset:50176\n"
"v_cvt_pk_bf16_f32 v90, v92, v93\n"
"v_cvt_pk_bf16_f32 v33, v34, v35\n"
"v_mul_f32_dpp v67, v206, v67 row_newbcast:3 row_mask:0xf bank_mask:0xf\n"
"v_mul_f32_dpp v71, v206, v71 row_newbcast:3 row_mask:0xf bank_mask:0xf\n"
"v_mov_b64 v[198:199], s[22:23]\n"
"v_mfma_f32_16x16x32_bf16 a[228:231], v[120:123], a[252:255], a[228:231]\n"
"s_waitcnt lgkmcnt(14)\n"
"ds_write_b128 v248, v[88:91] offset:51200\n"
"v_cvt_pk_bf16_f32 v3, v6, v7\n"
"v_mul_f32_dpp v37, v247, v37 row_newbcast:1 row_mask:0xf bank_mask:0xf\n"
"v_cvt_pk_bf16_f32 v34, v36, v37\n"
"v_mul_f32_dpp v70, v206, v70 row_newbcast:2 row_mask:0xf bank_mask:0xf\n"
"v_xor_b32 v237, 0x400, v236\n"
"v_mfma_f32_16x16x32_bf16 v[124:127], v[180:183], a[96:99], v[196:199]\n"
"s_waitcnt lgkmcnt(13)\n"
"ds_read_b64_tr_b16 a[30:31], v249 offset:50184\n"
"ds_read_b64_tr_b16 a[44:45], v249 offset:51200\n"
"v_mfma_f32_16x16x32_bf16 v[124:127], v[184:187], a[100:103], v[124:127]\n"
"s_waitcnt lgkmcnt(10)\n"
"ds_read_b64_tr_b16 a[28:29], v249 offset:50176\n"
"ds_read_b64_tr_b16 a[244:245], v237 offset:4096\n"
"v_mfma_f32_16x16x32_bf16 v[124:127], v[188:191], a[104:107], v[124:127]\n"
"ds_read_b64_tr_b16 a[12:13], v249 offset:49152\n"
"ds_read_b64_tr_b16 a[14:15], v249 offset:49160\n"
"v_mfma_f32_16x16x32_bf16 v[124:127], v[192:195], a[108:111], v[124:127]\n"
"ds_write_b128 v248, v[0:3] offset:49152\n"
"v_cvt_pk_bf16_f32 v35, v38, v39\n"
"v_cvt_pk_bf16_f32 v65, v66, v67\n"
"v_cvt_pk_bf16_f32 v66, v68, v69\n"
"v_cvt_pk_bf16_f32 v67, v70, v71\n"
"v_mfma_f32_16x16x32_bf16 v[124:127], v[208:211], a[144:147], v[124:127]\n"
"ds_read_b64_tr_b16 a[46:47], v249 offset:51208\n"
"ds_read_b128 v[60:63], v244 offset:35840\n"
"v_mfma_f32_16x16x32_bf16 v[124:127], v[212:215], a[148:151], v[124:127]\n"
"s_waitcnt lgkmcnt(11)\n"
"ds_write_b128 v248, v[32:35] offset:50176\n"
"ds_write_b128 v248, v[64:67] offset:51200\n"
"v_mfma_f32_16x16x32_bf16 v[124:127], v[216:219], a[152:155], v[124:127]\n"
"ds_read_b64_tr_b16 a[2:3], v249 offset:49160\n"
"ds_read_b128 v[28:31], v244 offset:33792\n"
"v_mfma_f32_16x16x32_bf16 v[124:127], v[220:223], a[156:159], v[124:127]\n"
"ds_read_b64_tr_b16 a[16:17], v249 offset:50176\n"
"ds_read_b64_tr_b16 a[34:35], v249 offset:51208\n"
"ds_read_b64 v[250:251], v245 offset:52224\n"
"ds_read_b128 v[56:59], v244 offset:34816\n"
"ds_read_b64_tr_b16 a[204:205], v237 offset:2048\n"
"v_sub_u32 v230, v234, v254\n"
"v_xor_b32 v233, 0x1000, v229\n"
"v_mul_f32_dpp v22, v246, v22 row_newbcast:2 row_mask:0xf bank_mask:0xf\n"
"v_mfma_f32_16x16x32_bf16 v[140:143], v[220:223], a[172:175], v[196:199]\n"
"ds_read_b64_tr_b16 a[0:1], v249 offset:49152\n"
"ds_read_b64_tr_b16 a[246:247], v237 offset:4104\n"
"v_mfma_f32_16x16x32_bf16 v[140:143], v[216:219], a[168:171], v[140:143]\n"
"ds_read_b64_tr_b16 a[32:33], v249 offset:51200\n"
"ds_read_b128 v[24:27], v244 offset:32768\n"
"v_mfma_f32_16x16x32_bf16 v[140:143], v[212:215], a[164:167], v[140:143]\n"
"ds_read_b64_tr_b16 a[18:19], v249 offset:50184\n"
"ds_read_b64_tr_b16 a[248:249], v236 offset:6144\n"
"v_mfma_f32_16x16x32_bf16 v[140:143], v[208:211], a[160:163], v[140:143]\n"
"ds_read_b64_tr_b16 v[110:111], v233 offset:10240\n"
"ds_read_b64_tr_b16 a[254:255], v237 offset:6152\n"
"v_mfma_f32_16x16x32_bf16 v[140:143], v[192:195], a[124:127], v[140:143]\n"
"ds_read_b64_tr_b16 v[108:109], v233 offset:8192\n"
"ds_read_b64_tr_b16 a[252:253], v237 offset:6144\n"
"v_mfma_f32_16x16x32_bf16 v[140:143], v[188:191], a[120:123], v[140:143]\n"
"ds_read_b64_tr_b16 v[226:227], v233 offset:26624\n"
"ds_read_b64_tr_b16 a[242:243], v236 offset:4104\n"
"v_mfma_f32_16x16x32_bf16 v[140:143], v[184:187], a[116:119], v[140:143]\n"
"ds_read_b64_tr_b16 a[206:207], v237 offset:2056\n"
"ds_read_b64_tr_b16 v[98:99], v229 offset:2048\n"
"v_mfma_f32_16x16x32_bf16 v[140:143], v[180:183], a[112:115], v[140:143]\n"
"ds_read_b64_tr_b16 a[194:195], v236 offset:8\n"
"ds_read_b64_tr_b16 a[196:197], v237 offset:0\n"
"v_mfma_f32_16x16x32_bf16 v[156:159], v[180:183], a[128:131], v[196:199]\n"
"ds_read_b64_tr_b16 v[102:103], v233 offset:2048\n"
"ds_read_b64_tr_b16 v[106:107], v229 offset:10240\n"
"v_mfma_f32_16x16x32_bf16 v[156:159], v[184:187], a[132:135], v[156:159]\n"
"ds_read_b64_tr_b16 a[200:201], v236 offset:2048\n"
"ds_read_b64_tr_b16 v[224:225], v233 offset:24576\n"
"v_mfma_f32_16x16x32_bf16 v[156:159], v[188:191], a[136:139], v[156:159]\n"
"ds_read_b64_tr_b16 v[120:121], v229 offset:24576\n"
"ds_read_b64_tr_b16 a[240:241], v236 offset:4096\n"
"v_mfma_f32_16x16x32_bf16 v[156:159], v[192:195], a[140:143], v[156:159]\n"
"s_waitcnt lgkmcnt(14)\n"
"ds_read_b64_tr_b16 v[96:97], v229 offset:0\n"
"v_xor_b32 v234, 0x1000, v230\n"
"v_mfma_f32_16x16x32_bf16 v[156:159], v[208:211], a[176:179], v[156:159]\n"
"ds_read_b64_tr_b16 a[198:199], v237 offset:8\n"
"ds_read_b64_tr_b16 v[114:115], v229 offset:18432\n"
"v_mfma_f32_16x16x32_bf16 v[156:159], v[212:215], a[180:183], v[156:159]\n"
"s_add_u32 s41, s95, 491520\n"
"ds_read_b64_tr_b16 a[192:193], v236 offset:0\n"
"v_mul_f32_dpp v54, v247, v54 row_newbcast:2 row_mask:0xf bank_mask:0xf\n"
"v_mfma_f32_16x16x32_bf16 v[156:159], v[216:219], a[184:187], v[156:159]\n"
"s_add_u32 m0, s44, 49152\n"
"ds_read_b64_tr_b16 v[100:101], v233 offset:0\n"
"ds_read_b64_tr_b16 v[118:119], v233 offset:18432\n"
"v_mfma_f32_16x16x32_bf16 v[156:159], v[220:223], a[188:191], v[156:159]\n"
"s_waitcnt lgkmcnt(12)\n"
"buffer_load_dwordx4 v240, s[28:31], s41 offen lds\n"
"v_mfma_f32_16x16x32_bf16 v[172:175], v[220:223], a[252:255], v[196:199]\n"
"s_add_u32 s40, s41, s52\n"
"ds_read_b64_tr_b16 v[104:105], v229 offset:8192\n"
"ds_read_b64_tr_b16 v[116:117], v233 offset:16384\n"
"v_mfma_f32_16x16x32_bf16 v[172:175], v[216:219], a[248:251], v[172:175]\n"
"buffer_load_dwordx4 v240, s[28:31], s40 offen offset:1024 lds\n"
"v_mfma_f32_16x16x32_bf16 v[172:175], v[212:215], a[244:247], v[172:175]\n"
"s_waitcnt lgkmcnt(8)\n"
"s_add_u32 s40, s41, s53\n"
"ds_read_b64_tr_b16 v[112:113], v229 offset:16384\n"
"ds_read_b64_tr_b16 v[122:123], v229 offset:26624\n"
"v_mfma_f32_16x16x32_bf16 v[172:175], v[208:211], a[240:243], v[172:175]\n"
"buffer_load_dwordx4 v240, s[28:31], s40 offen offset:2048 lds\n"
"v_mfma_f32_16x16x32_bf16 v[172:175], v[192:195], a[204:207], v[172:175]\n"
"v_min_f32 v254, v252, v250\n"
"v_sub_f32 v250, v254, v250\n"
"v_mfma_f32_16x16x32_bf16 v[172:175], v[188:191], a[200:203], v[172:175]\n"
"s_waitcnt lgkmcnt(6)\n"
"v_sub_f32 v252, v254, v252\n"
"v_mul_f32_dpp v86, v206, v86 row_newbcast:2 row_mask:0xf bank_mask:0xf\n"
"v_mfma_f32_16x16x32_bf16 v[172:175], v[184:187], a[196:199], v[172:175]\n"
"s_add_u32 s40, s41, s54\n"
"ds_read_b64_tr_b16 v[192:193], v234 offset:8192\n"
"v_mul_f32_dpp v49, v247, v49 row_newbcast:1 row_mask:0xf bank_mask:0xf\n"
"v_mfma_f32_16x16x32_bf16 v[172:175], v[180:183], a[192:195], v[172:175]\n"
"buffer_load_dwordx4 v240, s[28:31], s40 offen offset:3072 lds\n"
"v_mfma_f32_16x16x32_bf16 v[128:131], v[96:99], a[96:99], v[196:199]\n"
"s_waitcnt lgkmcnt(3)\n"
"v_xor_b32 v248, s75, v248\n"
"v_mul_f32_dpp v23, v246, v23 row_newbcast:3 row_mask:0xf bank_mask:0xf\n"
"v_mfma_f32_16x16x32_bf16 v[128:131], v[100:103], a[100:103], v[128:131]\n"
"v_exp_f32 v252, v252\n"
"v_mfma_f32_16x16x32_bf16 v[128:131], v[104:107], a[104:107], v[128:131]\n"
"ds_read_b64_tr_b16 v[208:209], v230 offset:16384\n"
"v_mul_f32 v253, v253, v252\n"
"v_mfma_f32_16x16x32_bf16 v[128:131], v[108:111], a[108:111], v[128:131]\n"
"s_waitcnt lgkmcnt(2)\n"
"ds_read_b64_tr_b16 a[238:239], v237 offset:10248\n"
"ds_read_b64_tr_b16 v[210:211], v230 offset:18432\n"
"v_mfma_f32_16x16x32_bf16 v[128:131], v[112:115], a[144:147], v[128:131]\n"
"s_add_u32 s21, s60, 32\n"
"v_exp_f32 v250, v250\n"
"v_mfma_f32_16x16x32_bf16 v[128:131], v[116:119], a[148:151], v[128:131]\n"
"s_mov_b32 s69, 1\n"
"ds_read_b64_tr_b16 v[180:181], v230 offset:0\n"
"v_mul_f32_dpp v16, v246, v16 row_newbcast:0 row_mask:0xf bank_mask:0xf\n"
"v_mfma_f32_16x16x32_bf16 v[128:131], v[120:123], a[152:155], v[128:131]\n"
"v_bfe_u32 v201, v242, 2, 7\n"
"v_add_u32 v201, 0x100, v201\n"
"ds_read_b64_tr_b16 v[220:221], v234 offset:24576\n"
"v_mfma_f32_16x16x32_bf16 v[128:131], v[224:227], a[156:159], v[128:131]\n"
"global_load_dword v200, v201, s[6:7] sc1\n"
"v_mfma_f32_16x16x32_bf16 v[144:147], v[224:227], a[172:175], v[196:199]\n"
"v_accvgpr_read_b32 v254, a208\n"
"v_accvgpr_read_b32 v255, a209\n"
"v_mfma_f32_16x16x32_bf16 v[144:147], v[120:123], a[168:171], v[144:147]\n"
"v_mul_f32_dpp v48, v247, v48 row_newbcast:0 row_mask:0xf bank_mask:0xf\n"
"v_mul_f32_dpp v80, v206, v80 row_newbcast:0 row_mask:0xf bank_mask:0xf\n"
"v_mfma_f32_16x16x32_bf16 v[144:147], v[116:119], a[164:167], v[144:147]\n"
"ds_read_b64_tr_b16 v[188:189], v230 offset:8192\n"
"v_fmac_f32 v253, v251, v250\n"
"v_mfma_f32_16x16x32_bf16 v[144:147], v[112:115], a[160:163], v[144:147]\n"
"v_mul_f32_dpp v19, v246, v19 row_newbcast:3 row_mask:0xf bank_mask:0xf\n"
"v_mul_f32_dpp v21, v246, v21 row_newbcast:1 row_mask:0xf bank_mask:0xf\n"
"v_mfma_f32_16x16x32_bf16 v[144:147], v[108:111], a[124:127], v[144:147]\n"
"v_rcp_f32 v253, v253\n"
"v_mfma_f32_16x16x32_bf16 v[144:147], v[104:107], a[120:123], v[144:147]\n"
"ds_read_b64_tr_b16 v[218:219], v230 offset:26624\n"
"v_mul_f32 v250, v250, v253\n"
"v_mfma_f32_16x16x32_bf16 v[144:147], v[100:103], a[116:119], v[144:147]\n"
"s_nop 0\n"
"v_mov_b32_dpp v250, v250 row_ror:12 row_mask:0x2 bank_mask:0xf\n"
"v_mul_f32_dpp v52, v247, v52 row_newbcast:0 row_mask:0xf bank_mask:0xf\n"
"v_mfma_f32_16x16x32_bf16 v[144:147], v[96:99], a[112:115], v[144:147]\n"
"ds_read_b64_tr_b16 a[216:217], v236 offset:8192\n"
"ds_read_b64_tr_b16 v[182:183], v230 offset:2048\n"
"v_mul_f32 v252, v252, v253\n"
"v_mov_b32_dpp v250, v250 row_ror:8 row_mask:0x4 bank_mask:0xf\n"
"v_accvgpr_read_b32 v251, a211\n"
"v_mul_f32_dpp v17, v246, v17 row_newbcast:1 row_mask:0xf bank_mask:0xf\n"
"v_mul_f32_dpp v55, v247, v55 row_newbcast:3 row_mask:0xf bank_mask:0xf\n"
"v_mfma_f32_16x16x32_bf16 v[160:163], v[96:99], a[128:131], v[196:199]\n"
"v_mov_b32_dpp v250, v250 row_ror:4 row_mask:0x8 bank_mask:0xf\n"
"v_mul_f32_dpp v81, v206, v81 row_newbcast:1 row_mask:0xf bank_mask:0xf\n"
"v_mfma_f32_16x16x32_bf16 v[160:163], v[100:103], a[132:135], v[160:163]\n"
"v_xor_b32 v235, 0x1000, v231\n"
"v_mul_f32_dpp v57, v250, v57 row_newbcast:1 row_mask:0xf bank_mask:0xf\n"
"v_mfma_f32_16x16x32_bf16 v[160:163], v[104:107], a[136:139], v[160:163]\n"
"v_mul_f32_dpp v31, v250, v31 row_newbcast:3 row_mask:0xf bank_mask:0xf\n"
"v_mul_f32_dpp v58, v250, v58 row_newbcast:2 row_mask:0xf bank_mask:0xf\n"
"v_mfma_f32_16x16x32_bf16 v[160:163], v[108:111], a[140:143], v[160:163]\n"
"ds_read_b64_tr_b16 v[194:195], v234 offset:10240\n"
"v_mov_b32_dpp v252, v252 row_ror:12 row_mask:0x2 bank_mask:0xf\n"
"v_mfma_f32_16x16x32_bf16 v[160:163], v[112:115], a[176:179], v[160:163]\n"
"s_nop 0\n"
"v_mov_b32_dpp v252, v252 row_ror:8 row_mask:0x4 bank_mask:0xf\n"
"v_mul_f32_dpp v18, v246, v18 row_newbcast:2 row_mask:0xf bank_mask:0xf\n"
"v_mfma_f32_16x16x32_bf16 v[160:163], v[116:119], a[180:183], v[160:163]\n"
"ds_read_b64_tr_b16 a[218:219], v236 offset:8200\n"
"ds_read_b64_tr_b16 v[184:185], v234 offset:0\n"
"v_mfma_f32_16x16x32_bf16 v[160:163], v[120:123], a[184:187], v[160:163]\n"
"v_mul_f32_dpp v27, v250, v27 row_newbcast:3 row_mask:0xf bank_mask:0xf\n"
"v_cvt_pk_bf16_f32 v16, v16, v17\n"
"v_mfma_f32_16x16x32_bf16 v[160:163], v[224:227], a[188:191], v[160:163]\n"
"ds_read_b64_tr_b16 v[186:187], v234 offset:2048\n"
"ds_read_b64_tr_b16 v[190:191], v230 offset:10240\n"
"v_mov_b32_dpp v252, v252 row_ror:4 row_mask:0x8 bank_mask:0xf\n"
"v_mul_f32_dpp v26, v250, v26 row_newbcast:2 row_mask:0xf bank_mask:0xf\n"
"v_mul_f32_dpp v28, v250, v28 row_newbcast:0 row_mask:0xf bank_mask:0xf\n"
"v_mul_f32_dpp v59, v250, v59 row_newbcast:3 row_mask:0xf bank_mask:0xf\n"
"v_mul_f32_dpp v63, v250, v63 row_newbcast:3 row_mask:0xf bank_mask:0xf\n"
"v_mfma_f32_16x16x32_bf16 v[176:179], v[224:227], a[252:255], v[196:199]\n"
"ds_read_b64_tr_b16 v[212:213], v234 offset:16384\n"
"v_mul_f32_dpp v30, v250, v30 row_newbcast:2 row_mask:0xf bank_mask:0xf\n"
"v_mfma_f32_16x16x32_bf16 v[176:179], v[120:123], a[248:251], v[176:179]\n"
"v_mul_f32_dpp v24, v250, v24 row_newbcast:0 row_mask:0xf bank_mask:0xf\n"
"v_fmac_f32_dpp v27, v252, v251 row_newbcast:3 row_mask:0xf bank_mask:0xf\n"
"v_mfma_f32_16x16x32_bf16 v[176:179], v[116:119], a[244:247], v[176:179]\n"
"ds_read_b64_tr_b16 v[216:217], v230 offset:24576\n"
"v_accvgpr_read_b32 v253, a210\n"
"v_mfma_f32_16x16x32_bf16 v[176:179], v[112:115], a[240:243], v[176:179]\n"
"ds_read_b64_tr_b16 v[222:223], v234 offset:26624\n"
"v_mul_f32_dpp v25, v250, v25 row_newbcast:1 row_mask:0xf bank_mask:0xf\n"
"v_mfma_f32_16x16x32_bf16 v[176:179], v[108:111], a[204:207], v[176:179]\n"
"ds_read_b64_tr_b16 a[236:237], v237 offset:10240\n"
"v_mul_f32_dpp v29, v250, v29 row_newbcast:1 row_mask:0xf bank_mask:0xf\n"
"v_mfma_f32_16x16x32_bf16 v[176:179], v[104:107], a[200:203], v[176:179]\n"
"ds_read_b64_tr_b16 v[214:215], v234 offset:18432\n"
"v_fmac_f32_dpp v25, v252, v255 row_newbcast:1 row_mask:0xf bank_mask:0xf\n"
"v_mfma_f32_16x16x32_bf16 v[176:179], v[100:103], a[196:199], v[176:179]\n"
"v_accvgpr_read_b32 v255, a213\n"
"v_accvgpr_read_b32 v251, a215\n"
"v_mfma_f32_16x16x32_bf16 v[176:179], v[96:99], a[192:195], v[176:179]\n"
"s_waitcnt lgkmcnt(5)\n"
"ds_read_b64_tr_b16 v[112:113], v231 offset:16384\n"
"ds_read_b64_tr_b16 v[122:123], v231 offset:26624\n"
"v_fmac_f32_dpp v24, v252, v254 row_newbcast:0 row_mask:0xf bank_mask:0xf\n"
"v_fmac_f32_dpp v26, v252, v253 row_newbcast:2 row_mask:0xf bank_mask:0xf\n"
"v_accvgpr_read_b32 v254, a212\n"
"v_accvgpr_read_b32 v253, a214\n"
"v_fmac_f32_dpp v31, v252, v251 row_newbcast:3 row_mask:0xf bank_mask:0xf\n"
"v_accvgpr_read_b32 v251, a227\n"
"v_mfma_f32_16x16x32_bf16 v[132:135], v[180:183], a[96:99], v[196:199]\n"
"ds_read_b64_tr_b16 a[222:223], v237 offset:8200\n"
"v_cvt_pk_bf16_f32 v24, v24, v25\n"
"v_mfma_f32_16x16x32_bf16 v[132:135], v[184:187], a[100:103], v[132:135]\n"
"ds_read_b64_tr_b16 v[120:121], v231 offset:24576\n"
"v_mul_f32_dpp v56, v250, v56 row_newbcast:0 row_mask:0xf bank_mask:0xf\n"
"v_mfma_f32_16x16x32_bf16 v[132:135], v[188:191], a[104:107], v[132:135]\n"
"v_fmac_f32_dpp v28, v252, v254 row_newbcast:0 row_mask:0xf bank_mask:0xf\n"
"v_mul_f32_dpp v60, v250, v60 row_newbcast:0 row_mask:0xf bank_mask:0xf\n"
"v_mfma_f32_16x16x32_bf16 v[132:135], v[192:195], a[108:111], v[132:135]\n"
"v_fmac_f32_dpp v29, v252, v255 row_newbcast:1 row_mask:0xf bank_mask:0xf\n"
"v_accvgpr_read_b32 v255, a225\n"
"v_mfma_f32_16x16x32_bf16 v[132:135], v[208:211], a[144:147], v[132:135]\n"
"s_waitcnt lgkmcnt(4)\n"
"s_waitcnt vmcnt(0)\n"
"v_fmac_f32_dpp v30, v252, v253 row_newbcast:2 row_mask:0xf bank_mask:0xf\n"
"v_accvgpr_read_b32 v254, a224\n"
"v_mfma_f32_16x16x32_bf16 v[132:135], v[212:215], a[148:151], v[132:135]\n"
"v_subrev_u32 v200, s21, v200\n"
"ds_read_b64_tr_b16 v[226:227], v235 offset:26624\n"
"v_mfma_f32_16x16x32_bf16 v[132:135], v[216:219], a[152:155], v[132:135]\n"
"v_cmp_lt_u32 vcc, s69, v200\n"
"v_fmac_f32_dpp v59, v252, v251 row_newbcast:3 row_mask:0xf bank_mask:0xf\n"
"v_mfma_f32_16x16x32_bf16 v[132:135], v[220:223], a[156:159], v[132:135]\n"
"ds_read_b64_tr_b16 v[102:103], v235 offset:2048\n"
"ds_read_b64_tr_b16 v[110:111], v235 offset:10240\n"
"v_cvt_pk_bf16_f32 v17, v18, v19\n"
"v_accvgpr_read_b32 v253, a226\n"
"v_fmac_f32_dpp v56, v252, v254 row_newbcast:0 row_mask:0xf bank_mask:0xf\n"
"v_fmac_f32_dpp v57, v252, v255 row_newbcast:1 row_mask:0xf bank_mask:0xf\n"
"v_accvgpr_read_b32 v254, a228\n"
"v_accvgpr_read_b32 v255, a229\n"
"v_mul_f32_dpp v61, v250, v61 row_newbcast:1 row_mask:0xf bank_mask:0xf\n"
"s_cbranch_vccnz LGV_%=\nLGVK_%=:\n"
"v_mfma_f32_16x16x32_bf16 v[148:151], v[220:223], a[172:175], v[196:199]\n"
"ds_read_b64_tr_b16 v[100:101], v235 offset:0\n"
"v_cvt_pk_bf16_f32 v25, v26, v27\n"
"v_mfma_f32_16x16x32_bf16 v[148:151], v[216:219], a[168:171], v[148:151]\n"
"v_add_u32_dpp v200, v200, v200 quad_perm:[1,0,3,2] row_mask:0xf bank_mask:0xf\n"
"ds_read_b64_tr_b16 v[104:105], v231 offset:8192\n"
"v_mfma_f32_16x16x32_bf16 v[148:151], v[212:215], a[164:167], v[148:151]\n"
"ds_read_b64_tr_b16 v[98:99], v231 offset:2048\n"
"v_fmac_f32_dpp v58, v252, v253 row_newbcast:2 row_mask:0xf bank_mask:0xf\n"
"v_mfma_f32_16x16x32_bf16 v[148:151], v[208:211], a[160:163], v[148:151]\n"
"v_add_u32_dpp v200, v200, v200 quad_perm:[2,3,0,1] row_mask:0xf bank_mask:0xf\n"
"v_accvgpr_read_b32 v251, a231\n"
"v_mfma_f32_16x16x32_bf16 v[148:151], v[192:195], a[124:127], v[148:151]\n"
"ds_read_b64_tr_b16 v[96:97], v231 offset:0\n"
"ds_read_b64_tr_b16 v[116:117], v235 offset:16384\n"
"v_cvt_pk_bf16_f32 v26, v28, v29\n"
"v_cvt_pk_bf16_f32 v27, v30, v31\n"
"v_accvgpr_read_b32 v253, a230\n"
"v_mul_f32_dpp v62, v250, v62 row_newbcast:2 row_mask:0xf bank_mask:0xf\n"
"v_fmac_f32_dpp v60, v252, v254 row_newbcast:0 row_mask:0xf bank_mask:0xf\n"
"v_cvt_pk_bf16_f32 v56, v56, v57\n"
"v_mfma_f32_16x16x32_bf16 v[148:151], v[188:191], a[120:123], v[148:151]\n"
"v_cmp_lt_u32 vcc, 1, v200\n"
"ds_write_b128 v243, v[24:27] offset:0\n"
"v_fmac_f32_dpp v61, v252, v255 row_newbcast:1 row_mask:0xf bank_mask:0xf\n"
"v_fmac_f32_dpp v62, v252, v253 row_newbcast:2 row_mask:0xf bank_mask:0xf\n"
"v_cvt_pk_bf16_f32 v57, v58, v59\n"
"v_cvt_pk_bf16_f32 v58, v60, v61\n"
"v_mfma_f32_16x16x32_bf16 v[148:151], v[184:187], a[116:119], v[148:151]\n"
"v_fmac_f32_dpp v63, v252, v251 row_newbcast:3 row_mask:0xf bank_mask:0xf\n"
"v_cvt_pk_bf16_f32 v59, v62, v63\n"
"v_mfma_f32_16x16x32_bf16 v[148:151], v[180:183], a[112:115], v[148:151]\n"
"ds_write_b128 v243, v[56:59] offset:2048\n"
"v_mul_f32_dpp v50, v247, v50 row_newbcast:2 row_mask:0xf bank_mask:0xf\n"
"v_cvt_pk_bf16_f32 v48, v48, v49\n"
"v_mfma_f32_16x16x32_bf16 v[164:167], v[180:183], a[128:131], v[196:199]\n"
"s_mov_b32 s47, 0\n"
"v_xor_b32 v249, s75, v249\n"
"v_mul_f32_dpp v20, v246, v20 row_newbcast:0 row_mask:0xf bank_mask:0xf\n"
"v_mfma_f32_16x16x32_bf16 v[164:167], v[184:187], a[132:135], v[164:167]\n"
"s_mov_b32 s89, vcc_lo\n"
"ds_read_b64_tr_b16 v[106:107], v231 offset:10240\n"
"ds_read_b64_tr_b16 v[224:225], v235 offset:24576\n"
"v_mfma_f32_16x16x32_bf16 v[164:167], v[188:191], a[136:139], v[164:167]\n"
"s_lshr_b32 s90, s89, s93\n"
"ds_read_b64_tr_b16 v[114:115], v231 offset:18432\n"
"v_cvt_pk_bf16_f32 v19, v22, v23\n"
"v_mfma_f32_16x16x32_bf16 v[164:167], v[192:195], a[140:143], v[164:167]\n"
"s_and_b32 s90, s90, 1\n"
"ds_read_b64_tr_b16 v[108:109], v235 offset:8192\n"
"v_mul_f32_dpp v87, v206, v87 row_newbcast:3 row_mask:0xf bank_mask:0xf\n"
"v_mfma_f32_16x16x32_bf16 v[164:167], v[208:211], a[176:179], v[164:167]\n"
"s_lshl_b32 s16, s61, 7\n"
"v_cvt_pk_bf16_f32 v18, v20, v21\n"
"v_mul_f32_dpp v51, v247, v51 row_newbcast:3 row_mask:0xf bank_mask:0xf\n"
"v_mfma_f32_16x16x32_bf16 v[164:167], v[212:215], a[180:183], v[164:167]\n"
"s_add_u32 s90, s90, s16\n"
"ds_read_b64_tr_b16 v[118:119], v235 offset:18432\n"
"v_mul_f32_dpp v85, v206, v85 row_newbcast:1 row_mask:0xf bank_mask:0xf\n"
"v_mfma_f32_16x16x32_bf16 v[164:167], v[216:219], a[184:187], v[164:167]\n"
"s_waitcnt vmcnt(63) lgkmcnt(5)\n"
"s_lshl_b32 s90, s90, 13\n"
"v_cvt_pk_bf16_f32 v80, v80, v81\n"
"v_mfma_f32_16x16x32_bf16 v[164:167], v[220:223], a[188:191], v[164:167]\n"
"s_barrier\n"
"s_lshl_b32 s16, s95, 9\n"
"ds_write_b128 v248, v[16:19] offset:49152\n"
"v_mfma_f32_16x16x32_bf16 v[136:139], v[96:99], a[96:99], v[196:199]\n"
"s_add_u32 s16, s16, s90\n"
"v_mul_f32_dpp v53, v247, v53 row_newbcast:1 row_mask:0xf bank_mask:0xf\n"
"v_cvt_pk_bf16_f32 v49, v50, v51\n"
"v_mfma_f32_16x16x32_bf16 v[136:139], v[100:103], a[100:103], v[136:139]\n"
"s_waitcnt lgkmcnt(4)\n"
"s_add_u32 s17, s16, 4096\n"
"ds_read_b64_tr_b16 a[228:229], v237 offset:14336\n"
"v_cvt_pk_bf16_f32 v50, v52, v53\n"
"v_mfma_f32_16x16x32_bf16 v[136:139], v[104:107], a[104:107], v[136:139]\n"
"s_waitcnt lgkmcnt(3)\n"
"s_mov_b32 s72, s61\n"
"ds_read_b64_tr_b16 a[10:11], v249 offset:49160\n"
"v_mov_b64 v[90:91], 0\n"
"v_mfma_f32_16x16x32_bf16 v[136:139], v[108:111], a[108:111], v[136:139]\n"
"s_mov_b32 s21, s60\n"
"ds_read_b64_tr_b16 v[220:221], v232 offset:57344\n"
"v_mul_f32_dpp v84, v206, v84 row_newbcast:0 row_mask:0xf bank_mask:0xf\n"
"v_mfma_f32_16x16x32_bf16 v[136:139], v[112:115], a[144:147], v[136:139]\n"
"s_waitcnt lgkmcnt(4)\n"
"s_mov_b32 s18, s77\n"
"ds_read_b64_tr_b16 a[8:9], v249 offset:49152\n"
"v_mul_f32_dpp v83, v206, v83 row_newbcast:3 row_mask:0xf bank_mask:0xf\n"
"v_mfma_f32_16x16x32_bf16 v[136:139], v[116:119], a[148:151], v[136:139]\n"
"s_mov_b32 s19, s77\n"
"ds_read_b64_tr_b16 v[180:181], v228 offset:32768\n"
"v_cvt_pk_bf16_f32 v51, v54, v55\n"
"v_mfma_f32_16x16x32_bf16 v[136:139], v[120:123], a[152:155], v[136:139]\n"
"s_mov_b32 s69, -1\n"
"ds_read_b64_tr_b16 v[182:183], v228 offset:34816\n"
"v_mul_f32_dpp v82, v206, v82 row_newbcast:2 row_mask:0xf bank_mask:0xf\n"
"v_mfma_f32_16x16x32_bf16 v[136:139], v[224:227], a[156:159], v[136:139]\n"
"s_mul_i32 s88, s61, 0x10011111\n"
"ds_read_b64_tr_b16 v[212:213], v232 offset:49152\n"
"v_cvt_pk_bf16_f32 v81, v82, v83\n"
"v_mfma_f32_16x16x32_bf16 v[152:155], v[224:227], a[172:175], v[196:199]\n"
"s_add_u32 s88, s88, 484675\n"
"ds_read_b64_tr_b16 v[186:187], v232 offset:34816\n"
"v_cvt_pk_bf16_f32 v83, v86, v87\n"
"v_mfma_f32_16x16x32_bf16 v[152:155], v[120:123], a[168:171], v[152:155]\n"
"s_and_b32 s88, s88, 0x70077777\n"
"ds_read_b64_tr_b16 v[216:217], v228 offset:57344\n"
"v_cvt_pk_bf16_f32 v82, v84, v85\n"
"v_mfma_f32_16x16x32_bf16 v[152:155], v[116:119], a[164:167], v[152:155]\n"
"s_or_b32 s88, s88, 0x9800000\n"
"ds_write_b128 v248, v[48:51] offset:50176\n"
"ds_write_b128 v248, v[80:83] offset:51200\n"
"v_mfma_f32_16x16x32_bf16 v[152:155], v[112:115], a[160:163], v[152:155]\n"
"s_mov_b32 s84, s6\n"
"ds_read_b64_tr_b16 a[24:25], v249 offset:50176\n"
"ds_read_b64_tr_b16 a[26:27], v249 offset:50184\n"
"ds_read_b64_tr_b16 a[40:41], v249 offset:51200\n"
"ds_read_b64_tr_b16 a[42:43], v249 offset:51208\n"
"v_mfma_f32_16x16x32_bf16 v[152:155], v[108:111], a[124:127], v[152:155]\n"
"s_and_b32 s85, s7, 0xffff\n"
"ds_read_b64_tr_b16 v[188:189], v228 offset:40960\n"
"v_bfe_u32 v254, v242, 4, 4\n"
"v_mfma_f32_16x16x32_bf16 v[152:155], v[104:107], a[120:123], v[152:155]\n"
"s_mov_b32 s86, 128\n"
"ds_read_b64_tr_b16 v[184:185], v232 offset:32768\n"
"ds_read_b64_tr_b16 v[222:223], v232 offset:59392\n"
"v_mfma_f32_16x16x32_bf16 v[152:155], v[100:103], a[116:119], v[152:155]\n"
"s_mov_b32 s87, 131072\n"
"ds_read_b64_tr_b16 a[224:225], v236 offset:14336\n"
"v_mfma_f32_16x16x32_bf16 v[152:155], v[96:99], a[112:115], v[152:155]\n"
"ds_read_b64_tr_b16 v[192:193], v232 offset:40960\n"
"ds_read_b64_tr_b16 v[214:215], v232 offset:51200\n"
"v_mfma_f32_16x16x32_bf16 v[168:171], v[96:99], a[128:131], v[196:199]\n"
"ds_read_b64_tr_b16 v[190:191], v228 offset:43008\n"
"ds_read_b64_tr_b16 v[218:219], v228 offset:59392\n"
"v_mfma_f32_16x16x32_bf16 v[168:171], v[100:103], a[132:135], v[168:171]\n"
"ds_read_b64_tr_b16 v[194:195], v232 offset:43008\n"
"ds_read_b64_tr_b16 v[210:211], v228 offset:51200\n"
"v_mfma_f32_16x16x32_bf16 v[168:171], v[104:107], a[136:139], v[168:171]\n"
"ds_read_b64_tr_b16 a[234:235], v236 offset:10248\n"
"ds_read_b64_tr_b16 v[208:209], v228 offset:49152\n"
"v_mfma_f32_16x16x32_bf16 v[168:171], v[108:111], a[140:143], v[168:171]\n"
"s_waitcnt lgkmcnt(12)\n"
"ds_read_b64_tr_b16 a[212:213], v237 offset:12288\n"
"ds_read_b64_tr_b16 a[230:231], v237 offset:14344\n"
"v_mfma_f32_16x16x32_bf16 v[168:171], v[112:115], a[176:179], v[168:171]\n"
"ds_read_b64_tr_b16 a[232:233], v236 offset:10240\n"
"ds_read_b64_tr_b16 a[214:215], v237 offset:12296\n"
"v_mfma_f32_16x16x32_bf16 v[168:171], v[116:119], a[180:183], v[168:171]\n"
"ds_read_b64_tr_b16 v[96:97], v229 offset:32768\n"
"ds_read_b64_tr_b16 v[100:101], v233 offset:32768\n"
"v_mfma_f32_16x16x32_bf16 v[168:171], v[120:123], a[184:187], v[168:171]\n"
"s_add_u32 s41, s70, 327680\n"
"s_add_u32 m0, s71, 32768\n"
"ds_read_b64_tr_b16 v[104:105], v229 offset:40960\n"
"v_mfma_f32_16x16x32_bf16 v[168:171], v[224:227], a[188:191], v[168:171]\n"
"buffer_load_dwordx4 v240, s[28:31], s41 offen lds\n"
"v_mfma_f32_16x16x32_bf16 v[124:127], v[180:183], a[48:51], v[124:127]\n"
"s_waitcnt lgkmcnt(11)\n"
"v_and_b32 v243, 0x3f0, v242\n"
"v_mov_b64 v[246:247], 0\n"
"v_mfma_f32_16x16x32_bf16 v[124:127], v[184:187], a[52:55], v[124:127]\n"
"v_bfe_u32 v255, v242, 8, 1\n"
"v_mfma_f32_16x16x32_bf16 v[124:127], v[188:191], a[56:59], v[124:127]\n"
"s_waitcnt lgkmcnt(7)\n"
"s_add_u32 s40, s41, s52\n"
"ds_read_b64_tr_b16 a[210:211], v236 offset:12296\n"
"ds_read_b64_tr_b16 v[98:99], v229 offset:34816\n"
"v_mfma_f32_16x16x32_bf16 v[124:127], v[192:195], a[60:63], v[124:127]\n"
"buffer_load_dwordx4 v240, s[28:31], s40 offen offset:1024 lds\n"
"v_mfma_f32_16x16x32_bf16 v[124:127], v[208:211], a[0:3], v[124:127]\n"
"v_xor_b32 v248, s75, v248\n"
"v_mov_b64 v[52:53], s[22:23]\n"
"v_mfma_f32_16x16x32_bf16 v[124:127], v[212:215], a[4:7], v[124:127]\n"
"v_mov_b64 v[6:7], s[22:23]\n"
"v_mfma_f32_16x16x32_bf16 v[124:127], v[216:219], a[8:11], v[124:127]\n"
"s_add_u32 s40, s41, s53\n"
"ds_read_b64_tr_b16 v[102:103], v233 offset:34816\n"
"ds_read_b64_tr_b16 v[110:111], v233 offset:43008\n"
"v_mfma_f32_16x16x32_bf16 v[124:127], v[220:223], a[12:15], v[124:127]\n"
"buffer_load_dwordx4 v240, s[28:31], s40 offen offset:2048 lds\n"
"v_mfma_f32_16x16x32_bf16 v[140:143], v[220:223], a[28:31], v[140:143]\n"
"v_mov_b64 v[2:3], s[22:23]\n"
"v_mov_b64 v[10:11], s[22:23]\n"
"v_mfma_f32_16x16x32_bf16 v[140:143], v[216:219], a[24:27], v[140:143]\n"
"v_mfma_f32_16x16x32_bf16 v[140:143], v[212:215], a[20:23], v[140:143]\n"
"ds_read_b64_tr_b16 a[220:221], v237 offset:8192\n"
"ds_read_b64_tr_b16 v[116:117], v233 offset:49152\n"
"v_mfma_f32_16x16x32_bf16 v[140:143], v[208:211], a[16:19], v[140:143]\n"
"ds_read_b64_tr_b16 a[208:209], v236 offset:12288\n"
"v_mul_u32_u24 v254, 2560, v254\n"
"v_mfma_f32_16x16x32_bf16 v[140:143], v[192:195], a[76:79], v[140:143]\n"
"ds_read_b64_tr_b16 v[122:123], v229 offset:59392\n"
"v_subrev_f32 v124, s22, v124\n"
"v_mfma_f32_16x16x32_bf16 v[140:143], v[188:191], a[72:75], v[140:143]\n"
"ds_read_b64_tr_b16 a[226:227], v236 offset:14344\n"
"v_mov_b64 v[18:19], s[22:23]\n"
"v_mfma_f32_16x16x32_bf16 v[140:143], v[184:187], a[68:71], v[140:143]\n"
"s_add_u32 s40, s41, s54\n"
"ds_read_b64_tr_b16 v[112:113], v229 offset:49152\n"
"ds_read_b64_tr_b16 v[120:121], v229 offset:57344\n"
"v_mfma_f32_16x16x32_bf16 v[140:143], v[180:183], a[64:67], v[140:143]\n"
"buffer_load_dwordx4 v240, s[28:31], s40 offen offset:3072 lds\n"
"v_mfma_f32_16x16x32_bf16 v[156:159], v[180:183], a[80:83], v[156:159]\n"
"s_add_u32 s41, s70, 491520\n"
"s_add_u32 m0, s71, 49152\n"
"v_lshl_add_u32 v254, v255, 5, v254\n"
"v_mfma_f32_16x16x32_bf16 v[156:159], v[184:187], a[84:87], v[156:159]\n"
"buffer_load_dwordx4 v240, s[28:31], s41 offen lds\n"
"v_mfma_f32_16x16x32_bf16 v[156:159], v[188:191], a[88:91], v[156:159]\n"
"v_subrev_f32 v125, s22, v125\n"
"v_cvt_pk_bf16_f32 v124, v124, v125\n"
"v_mfma_f32_16x16x32_bf16 v[156:159], v[192:195], a[92:95], v[156:159]\n"
"v_bfe_u32 v255, v242, 9, 1\n"
"v_mfma_f32_16x16x32_bf16 v[156:159], v[208:211], a[32:35], v[156:159]\n"
"ds_read_b64_tr_b16 v[106:107], v229 offset:43008\n"
"v_lshl_add_u32 v254, v255, 4, v254\n"
"v_mfma_f32_16x16x32_bf16 v[156:159], v[212:215], a[36:39], v[156:159]\n"
"ds_read_b64_tr_b16 v[114:115], v229 offset:51200\n"
"v_subrev_f32 v142, s22, v142\n"
"v_mfma_f32_16x16x32_bf16 v[156:159], v[216:219], a[40:43], v[156:159]\n"
"s_add_u32 s40, s41, s52\n"
"ds_read_b64_tr_b16 v[108:109], v233 offset:40960\n"
"ds_read_b64_tr_b16 v[118:119], v233 offset:51200\n"
"v_mfma_f32_16x16x32_bf16 v[156:159], v[220:223], a[44:47], v[156:159]\n"
"s_waitcnt lgkmcnt(4)\n"
"buffer_load_dwordx4 v240, s[28:31], s40 offen offset:1024 lds\n"
"v_mfma_f32_16x16x32_bf16 v[172:175], v[220:223], a[228:231], v[172:175]\n"
"s_add_u32 s40, s41, s53\n"
"v_mov_b64 v[32:33], s[22:23]\n"
"v_mov_b64 v[34:35], s[22:23]\n"
"v_mfma_f32_16x16x32_bf16 v[172:175], v[216:219], a[224:227], v[172:175]\n"
"buffer_load_dwordx4 v240, s[28:31], s40 offen offset:2048 lds\n"
"v_mfma_f32_16x16x32_bf16 v[172:175], v[212:215], a[212:215], v[172:175]\n"
"v_mov_b64 v[88:89], 0\n"
"v_subrev_f32 v141, s22, v141\n"
"v_mfma_f32_16x16x32_bf16 v[172:175], v[208:211], a[208:211], v[172:175]\n"
"v_add_u32 v236, s94, v243\n"
"v_mov_b32 v241, s60\n"
"v_mfma_f32_16x16x32_bf16 v[172:175], v[192:195], a[236:239], v[172:175]\n"
"s_add_u32 s40, s41, s54\n"
"ds_read_b64_tr_b16 v[224:225], v233 offset:57344\n"
"ds_read_b64_tr_b16 v[226:227], v233 offset:59392\n"
"v_mfma_f32_16x16x32_bf16 v[172:175], v[188:191], a[232:235], v[172:175]\n"
"buffer_load_dwordx4 v240, s[28:31], s40 offen offset:3072 lds\n"
"v_mfma_f32_16x16x32_bf16 v[172:175], v[184:187], a[220:223], v[172:175]\n"
"s_lshl_b32 s62, s70, 9\n"
"v_xor_b32 v228, s75, v228\n"
"v_mfma_f32_16x16x32_bf16 v[172:175], v[180:183], a[216:219], v[172:175]\n"
"s_and_b32 s70, s88, 15\n"
"v_mov_b64 v[26:27], s[22:23]\n"
"v_mfma_f32_16x16x32_bf16 v[128:131], v[96:99], a[48:51], v[128:131]\n"
"s_lshr_b32 s88, s88, 4\n"
"ds_read_b64_tr_b16 v[186:187], v234 offset:34816\n"
"v_mfma_f32_16x16x32_bf16 v[128:131], v[100:103], a[52:55], v[128:131]\n"
"s_waitcnt lgkmcnt(3)\n"
"s_lshl_b32 s70, s70, 7\n"
"ds_read_b64_tr_b16 v[180:181], v230 offset:32768\n"
"v_subrev_f32 v143, s22, v143\n"
"v_mfma_f32_16x16x32_bf16 v[128:131], v[104:107], a[56:59], v[128:131]\n"
"s_xor_b32 s71, s71, s75\n"
"v_mov_b64 v[86:87], 0\n"
"v_subrev_f32 v127, s22, v127\n"
"v_mfma_f32_16x16x32_bf16 v[128:131], v[108:111], a[60:63], v[128:131]\n"
"v_lshlrev_b32_e64 v201, 2, s72\n"
"ds_read_b64_tr_b16 v[214:215], v234 offset:51200\n"
"v_subrev_f32 v173, s22, v173\n"
"v_mfma_f32_16x16x32_bf16 v[128:131], v[112:115], a[0:3], v[128:131]\n"
"global_load_dword v200, v201, s[6:7] sc1\n"
"v_mfma_f32_16x16x32_bf16 v[128:131], v[116:119], a[4:7], v[128:131]\n"
"v_mov_b64 v[28:29], s[22:23]\n"
"v_subrev_f32 v140, s22, v140\n"
"v_mfma_f32_16x16x32_bf16 v[128:131], v[120:123], a[8:11], v[128:131]\n"
"s_waitcnt lgkmcnt(3)\n"
"v_mov_b64 v[38:39], s[22:23]\n"
"v_add_u32 v241, 1, v241\n"
"v_mfma_f32_16x16x32_bf16 v[128:131], v[224:227], a[12:15], v[128:131]\n"
"ds_read_b64_tr_b16 v[208:209], v230 offset:49152\n"
"v_subrev_f32 v126, s22, v126\n"
"v_mfma_f32_16x16x32_bf16 v[144:147], v[224:227], a[28:31], v[144:147]\n"
"v_mov_b64 v[94:95], 0\n"
"v_cvt_pk_bf16_f32 v140, v140, v141\n"
"v_mfma_f32_16x16x32_bf16 v[144:147], v[120:123], a[24:27], v[144:147]\n"
"ds_read_b64_tr_b16 v[192:193], v234 offset:40960\n"
"v_subrev_f32 v175, s22, v175\n"
"v_mfma_f32_16x16x32_bf16 v[144:147], v[116:119], a[20:23], v[144:147]\n"
"ds_read_b64_tr_b16 v[222:223], v234 offset:59392\n"
"v_subrev_f32 v129, s22, v129\n"
"v_mfma_f32_16x16x32_bf16 v[144:147], v[112:115], a[16:19], v[144:147]\n"
"ds_read_b64_tr_b16 v[188:189], v230 offset:40960\n"
"v_lshl_add_u32 v248, v242, 8, s93\n"
"v_mfma_f32_16x16x32_bf16 v[144:147], v[108:111], a[76:79], v[144:147]\n"
"ds_read_b64_tr_b16 v[182:183], v230 offset:34816\n"
"v_cvt_pk_bf16_f32 v125, v126, v127\n"
"v_mfma_f32_16x16x32_bf16 v[144:147], v[104:107], a[72:75], v[144:147]\n"
"v_mov_b64 v[8:9], s[22:23]\n"
"v_subrev_f32 v130, s22, v130\n"
"v_mfma_f32_16x16x32_bf16 v[144:147], v[100:103], a[68:71], v[144:147]\n"
"ds_read_b64_tr_b16 v[212:213], v234 offset:49152\n"
"v_subrev_f32 v172, s22, v172\n"
"v_mfma_f32_16x16x32_bf16 v[144:147], v[96:99], a[64:67], v[144:147]\n"
"ds_read_b64_tr_b16 v[220:221], v234 offset:57344\n"
"v_mfma_f32_16x16x32_bf16 v[160:163], v[96:99], a[80:83], v[160:163]\n"
"ds_read_b64_tr_b16 v[216:217], v230 offset:57344\n"
"v_add_u32 v238, s97, v254\n"
"v_mfma_f32_16x16x32_bf16 v[160:163], v[100:103], a[84:87], v[160:163]\n"
"v_lshl_add_u32 v243, s44, 2, v243\n"
"v_bfe_u32 v237, v242, 4, 3\n"
"v_mfma_f32_16x16x32_bf16 v[160:163], v[104:107], a[88:91], v[160:163]\n"
"ds_read_b64_tr_b16 v[190:191], v230 offset:43008\n"
"v_subrev_f32 v128, s22, v128\n"
"v_mfma_f32_16x16x32_bf16 v[160:163], v[108:111], a[92:95], v[160:163]\n"
"v_max_u32 v237, 1, v237\n"
"v_subrev_f32 v156, s22, v156\n"
"v_mfma_f32_16x16x32_bf16 v[160:163], v[112:115], a[32:35], v[160:163]\n"
"ds_read_b64_tr_b16 v[184:185], v234 offset:32768\n"
"v_subrev_f32 v159, s22, v159\n"
"v_mfma_f32_16x16x32_bf16 v[160:163], v[116:119], a[36:39], v[160:163]\n"
"v_subrev_f32 v131, s22, v131\n"
"v_cvt_pk_bf16_f32 v126, v128, v129\n"
"v_mfma_f32_16x16x32_bf16 v[160:163], v[120:123], a[40:43], v[160:163]\n"
"ds_read_b64_tr_b16 v[194:195], v234 offset:43008\n"
"v_cvt_pk_bf16_f32 v127, v130, v131\n"
"v_mfma_f32_16x16x32_bf16 v[160:163], v[224:227], a[44:47], v[160:163]\n"
"buffer_store_dwordx4 v[124:127], v243, s[32:35], s16 offen sc0 sc1\n"
"v_subrev_f32 v147, s22, v147\n"
"v_mfma_f32_16x16x32_bf16 v[176:179], v[224:227], a[228:231], v[176:179]\n"
"ds_read_b64_tr_b16 v[218:219], v230 offset:59392\n"
"v_mfma_f32_16x16x32_bf16 v[176:179], v[120:123], a[224:227], v[176:179]\n"
"v_subrev_f32 v157, s22, v157\n"
"v_subrev_f32 v146, s22, v146\n"
"v_mfma_f32_16x16x32_bf16 v[176:179], v[116:119], a[212:215], v[176:179]\n"
"ds_read_b64_tr_b16 v[210:211], v230 offset:51200\n"
"v_mfma_f32_16x16x32_bf16 v[176:179], v[112:115], a[208:211], v[176:179]\n"
"v_cvt_pk_bf16_f32 v141, v142, v143\n"
"v_subrev_f32 v145, s22, v145\n"
"v_mfma_f32_16x16x32_bf16 v[176:179], v[108:111], a[236:239], v[176:179]\n"
"ds_read_b64_tr_b16 v[226:227], v235 offset:59392\n"
"v_subrev_f32 v158, s22, v158\n"
"v_mfma_f32_16x16x32_bf16 v[176:179], v[104:107], a[232:235], v[176:179]\n"
"v_subrev_f32 v174, s22, v174\n"
"v_mfma_f32_16x16x32_bf16 v[176:179], v[100:103], a[220:223], v[176:179]\n"
"ds_read_b64_tr_b16 v[100:101], v235 offset:32768\n"
"v_mov_b64 v[36:37], s[22:23]\n"
"v_mfma_f32_16x16x32_bf16 v[176:179], v[96:99], a[216:219], v[176:179]\n"
"s_waitcnt lgkmcnt(4)\n"
"v_subrev_f32 v144, s22, v144\n"
"v_subrev_f32 v162, s22, v162\n"
"v_mfma_f32_16x16x32_bf16 v[132:135], v[180:183], a[48:51], v[132:135]\n"
"ds_read_b64_tr_b16 v[106:107], v231 offset:43008\n"
"v_subrev_f32 v160, s22, v160\n"
"v_mfma_f32_16x16x32_bf16 v[132:135], v[184:187], a[52:55], v[132:135]\n"
"ds_read_b64_tr_b16 v[96:97], v231 offset:32768\n"
"v_mfma_f32_16x16x32_bf16 v[132:135], v[188:191], a[56:59], v[132:135]\n"
"v_cvt_pk_bf16_f32 v142, v144, v145\n"
"v_cvt_pk_bf16_f32 v143, v146, v147\n"
"v_mfma_f32_16x16x32_bf16 v[132:135], v[192:195], a[60:63], v[132:135]\n"
"s_waitcnt lgkmcnt(4)\n"
"ds_read_b64_tr_b16 v[112:113], v231 offset:49152\n"
"v_mov_b64 v[50:51], s[22:23]\n"
"v_mfma_f32_16x16x32_bf16 v[132:135], v[208:211], a[0:3], v[132:135]\n"
"v_xor_b32 v237, s61, v237\n"
"v_cvt_pk_bf16_f32 v172, v172, v173\n"
"v_mfma_f32_16x16x32_bf16 v[132:135], v[212:215], a[4:7], v[132:135]\n"
"ds_read_b64_tr_b16 v[116:117], v235 offset:49152\n"
"v_xor_b32 v233, s75, v233\n"
"v_mfma_f32_16x16x32_bf16 v[132:135], v[216:219], a[8:11], v[132:135]\n"
"v_mov_b64 v[30:31], s[22:23]\n"
"v_subrev_f32 v179, s22, v179\n"
"v_mfma_f32_16x16x32_bf16 v[132:135], v[220:223], a[12:15], v[132:135]\n"
"buffer_store_dwordx4 v[140:143], v243, s[32:35], s16 offen offset:2048 sc0 sc1\n"
"ds_read_b64_tr_b16 v[120:121], v231 offset:57344\n"
"v_mfma_f32_16x16x32_bf16 v[148:151], v[220:223], a[28:31], v[148:151]\n"
"v_cvt_pk_bf16_f32 v156, v156, v157\n"
"v_subrev_f32 v161, s22, v161\n"
"v_mfma_f32_16x16x32_bf16 v[148:151], v[216:219], a[24:27], v[148:151]\n"
"ds_read_b64_tr_b16 v[110:111], v235 offset:43008\n"
"v_subrev_f32 v178, s22, v178\n"
"v_mfma_f32_16x16x32_bf16 v[148:151], v[212:215], a[20:23], v[148:151]\n"
"ds_read_b64_tr_b16 v[102:103], v235 offset:34816\n"
"v_mov_b64 v[48:49], s[22:23]\n"
"v_mfma_f32_16x16x32_bf16 v[148:151], v[208:211], a[16:19], v[148:151]\n"
"v_add_u32 v253, s99, v254\n"
"v_subrev_f32 v132, s22, v132\n"
"v_mfma_f32_16x16x32_bf16 v[148:151], v[192:195], a[76:79], v[148:151]\n"
"ds_read_b64_tr_b16 v[98:99], v231 offset:34816\n"
"v_cvt_pk_bf16_f32 v157, v158, v159\n"
"v_mfma_f32_16x16x32_bf16 v[148:151], v[188:191], a[72:75], v[148:151]\n"
"ds_read_b64_tr_b16 v[108:109], v235 offset:40960\n"
"v_subrev_f32 v135, s22, v135\n"
"v_mfma_f32_16x16x32_bf16 v[148:151], v[184:187], a[68:71], v[148:151]\n"
"v_mfma_f32_16x16x32_bf16 v[148:151], v[180:183], a[64:67], v[148:151]\n"
"ds_read_b64_tr_b16 v[118:119], v235 offset:51200\n"
"v_mfma_f32_16x16x32_bf16 v[164:167], v[180:183], a[80:83], v[164:167]\n"
"ds_read_b64_tr_b16 v[104:105], v231 offset:40960\n"
"v_subrev_f32 v163, s22, v163\n"
"v_mfma_f32_16x16x32_bf16 v[164:167], v[184:187], a[84:87], v[164:167]\n"
"ds_read_b64_tr_b16 v[114:115], v231 offset:51200\n"
"v_mov_b64 v[84:85], 0\n"
"v_mfma_f32_16x16x32_bf16 v[164:167], v[188:191], a[88:91], v[164:167]\n"
"v_cvt_pk_bf16_f32 v158, v160, v161\n"
"v_subrev_f32 v133, s22, v133\n"
"v_mfma_f32_16x16x32_bf16 v[164:167], v[192:195], a[92:95], v[164:167]\n"
"ds_read_b64_tr_b16 v[122:123], v231 offset:59392\n"
"v_mov_b64 v[14:15], s[22:23]\n"
"v_mfma_f32_16x16x32_bf16 v[164:167], v[208:211], a[32:35], v[164:167]\n"
"v_mov_b64 v[24:25], s[22:23]\n"
"v_xor_b32 v232, s75, v232\n"
"v_mfma_f32_16x16x32_bf16 v[164:167], v[212:215], a[36:39], v[164:167]\n"
"ds_read_b64_tr_b16 v[224:225], v235 offset:57344\n"
"v_cvt_pk_bf16_f32 v159, v162, v163\n"
"v_mfma_f32_16x16x32_bf16 v[164:167], v[216:219], a[40:43], v[164:167]\n"
"v_cvt_pk_bf16_f32 v173, v174, v175\n"
"v_mfma_f32_16x16x32_bf16 v[164:167], v[220:223], a[44:47], v[164:167]\n"
"s_barrier\n"
"s_waitcnt lgkmcnt(4)\n"
"v_mfma_f32_16x16x32_bf16 v[136:139], v[96:99], a[48:51], v[136:139]\n"
"ds_read_b64_tr_b16 v[214:215], v232 offset:18432\n"
"v_cvt_pk_bf16_f32 v175, v178, v179\n"
"v_mfma_f32_16x16x32_bf16 v[136:139], v[100:103], a[52:55], v[136:139]\n"
"s_waitcnt lgkmcnt(3)\n"
"ds_read_b64_tr_b16 v[218:219], v228 offset:26624\n"
"v_mov_b64 v[0:1], s[22:23]\n"
"v_mfma_f32_16x16x32_bf16 v[136:139], v[104:107], a[56:59], v[136:139]\n"
"ds_read_b64_tr_b16 v[222:223], v232 offset:26624\n"
"v_mov_b64 v[46:47], s[22:23]\n"
"v_mfma_f32_16x16x32_bf16 v[136:139], v[108:111], a[60:63], v[136:139]\n"
"ds_read_b64_tr_b16 v[216:217], v228 offset:24576\n"
"v_mov_b64 v[44:45], s[22:23]\n"
"v_mfma_f32_16x16x32_bf16 v[136:139], v[112:115], a[0:3], v[136:139]\n"
"ds_read_b64_tr_b16 v[212:213], v232 offset:16384\n"
"v_lshlrev_b32 v237, 2, v237\n"
"v_mfma_f32_16x16x32_bf16 v[136:139], v[116:119], a[4:7], v[136:139]\n"
"s_waitcnt lgkmcnt(5)\n"
"ds_read_b64_tr_b16 v[180:181], v228 offset:0\n"
"v_cvt_pk_bf16_f32 v132, v132, v133\n"
"v_mfma_f32_16x16x32_bf16 v[136:139], v[120:123], a[8:11], v[136:139]\n"
"ds_read_b64_tr_b16 v[190:191], v228 offset:10240\n"
"v_xor_b32 v229, s75, v229\n"
"v_mfma_f32_16x16x32_bf16 v[136:139], v[224:227], a[12:15], v[136:139]\n"
"buffer_store_dwordx4 v[156:159], v243, s[32:35], s17 offen sc0 sc1\n"
"ds_read_b64_tr_b16 v[186:187], v232 offset:2048\n"
"v_mfma_f32_16x16x32_bf16 v[152:155], v[224:227], a[28:31], v[152:155]\n"
"ds_read_b64_tr_b16 v[220:221], v232 offset:24576\n"
"v_xor_b32 v239, 64, v238\n"
"v_mfma_f32_16x16x32_bf16 v[152:155], v[120:123], a[24:27], v[152:155]\n"
"ds_read_b64_tr_b16 v[210:211], v228 offset:18432\n"
"v_mfma_f32_16x16x32_bf16 v[152:155], v[116:119], a[20:23], v[152:155]\n"
"ds_read_b64_tr_b16 v[208:209], v228 offset:16384\n"
"v_subrev_f32 v134, s22, v134\n"
"v_mfma_f32_16x16x32_bf16 v[152:155], v[112:115], a[16:19], v[152:155]\n"
"ds_read_b64_tr_b16 v[194:195], v232 offset:10240\n"
"v_mov_b64 v[42:43], s[22:23]\n"
"v_mfma_f32_16x16x32_bf16 v[152:155], v[108:111], a[76:79], v[152:155]\n"
"ds_read_b64_tr_b16 v[182:183], v228 offset:2048\n"
"v_add_u32 v252, s98, v254\n"
"v_mfma_f32_16x16x32_bf16 v[152:155], v[104:107], a[72:75], v[152:155]\n"
"ds_read_b64_tr_b16 v[192:193], v232 offset:8192\n"
"v_xor_b32 v249, s75, v249\n"
"v_mfma_f32_16x16x32_bf16 v[152:155], v[100:103], a[68:71], v[152:155]\n"
"ds_read_b64_tr_b16 v[184:185], v232 offset:0\n"
"ds_read_b64_tr_b16 v[188:189], v228 offset:8192\n"
"v_mfma_f32_16x16x32_bf16 v[152:155], v[96:99], a[64:67], v[152:155]\n"
"v_mov_b64 v[4:5], s[22:23]\n"
"v_subrev_f32 v176, s22, v176\n"
"v_mfma_f32_16x16x32_bf16 v[168:171], v[96:99], a[80:83], v[168:171]\n"
"ds_read_b64_tr_b16 v[96:97], v229 offset:0\n"
"ds_read_b64_tr_b16 v[98:99], v229 offset:2048\n"
"v_mfma_f32_16x16x32_bf16 v[168:171], v[100:103], a[84:87], v[168:171]\n"
"ds_read_b64_tr_b16 v[100:101], v233 offset:0\n"
"ds_read_b64_tr_b16 v[102:103], v233 offset:2048\n"
"v_mfma_f32_16x16x32_bf16 v[168:171], v[104:107], a[88:91], v[168:171]\n"
"ds_read_b64_tr_b16 v[104:105], v229 offset:8192\n"
"v_subrev_f32 v177, s22, v177\n"
"v_mfma_f32_16x16x32_bf16 v[168:171], v[108:111], a[92:95], v[168:171]\n"
"v_mov_b64 v[12:13], s[22:23]\n"
"v_mov_b64 v[22:23], s[22:23]\n"
"v_mfma_f32_16x16x32_bf16 v[168:171], v[112:115], a[32:35], v[168:171]\n"
"v_mov_b64 v[40:41], s[22:23]\n"
"v_cvt_pk_bf16_f32 v174, v176, v177\n"
"v_mfma_f32_16x16x32_bf16 v[168:171], v[116:119], a[36:39], v[168:171]\n"
"v_mov_b64 v[16:17], s[22:23]\n"
"v_mfma_f32_16x16x32_bf16 v[168:171], v[120:123], a[40:43], v[168:171]\n"
"s_add_u32 s47, s47, 1\n"
"v_mov_b64 v[20:21], s[22:23]\n"
"v_mov_b64 v[92:93], 0\n"
"v_mfma_f32_16x16x32_bf16 v[168:171], v[224:227], a[44:47], v[168:171]\n"
"s_waitcnt lgkmcnt(5)\n"
"v_mov_b64 v[54:55], s[22:23]\n"
"v_mov_b64 v[244:245], 0\n"
"LGL_%=:\n"
"v_mfma_f32_16x16x32_bf16 v[124:127], v[180:183], a[96:99], v[196:199]\n"
"ds_read_b64_tr_b16 v[106:107], v229 offset:10240\n"
"v_cvt_pk_bf16_f32 v133, v134, v135\n"
"v_mfma_f32_16x16x32_bf16 v[124:127], v[184:187], a[100:103], v[124:127]\n"
"ds_read_b64_tr_b16 v[108:109], v233 offset:8192\n"
"v_subrev_f32 v136, s22, v136\n"
"v_mfma_f32_16x16x32_bf16 v[124:127], v[188:191], a[104:107], v[124:127]\n"
"ds_read_b64_tr_b16 v[110:111], v233 offset:10240\n"
"v_subrev_f32 v137, s22, v137\n"
"v_mfma_f32_16x16x32_bf16 v[124:127], v[192:195], a[108:111], v[124:127]\n"
"ds_read_b64_tr_b16 v[112:113], v229 offset:16384\n"
"v_subrev_f32 v138, s22, v138\n"
"v_mfma_f32_16x16x32_bf16 v[124:127], v[208:211], a[144:147], v[124:127]\n"
"ds_read_b64_tr_b16 v[114:115], v229 offset:18432\n"
"v_subrev_f32 v139, s22, v139\n"
"v_mfma_f32_16x16x32_bf16 v[124:127], v[212:215], a[148:151], v[124:127]\n"
"s_waitcnt lgkmcnt(7)\n"
"ds_read_b64_tr_b16 v[116:117], v233 offset:16384\n"
"v_cvt_pk_bf16_f32 v134, v136, v137\n"
"v_mfma_f32_16x16x32_bf16 v[124:127], v[216:219], a[152:155], v[124:127]\n"
"ds_read_b64_tr_b16 v[118:119], v233 offset:18432\n"
"v_cvt_pk_bf16_f32 v135, v138, v139\n"
"v_mfma_f32_16x16x32_bf16 v[124:127], v[220:223], a[156:159], v[124:127]\n"
"buffer_store_dwordx4 v[172:175], v243, s[32:35], s17 offen offset:2048 sc0 sc1\n"
"v_subrev_f32 v148, s22, v148\n"
"v_mfma_f32_16x16x32_bf16 v[140:143], v[220:223], a[172:175], v[196:199]\n"
"ds_read_b64_tr_b16 v[120:121], v229 offset:24576\n"
"v_subrev_f32 v149, s22, v149\n"
"v_mfma_f32_16x16x32_bf16 v[140:143], v[216:219], a[168:171], v[140:143]\n"
"ds_read_b64_tr_b16 v[122:123], v229 offset:26624\n"
"v_subrev_f32 v150, s22, v150\n"
"v_mfma_f32_16x16x32_bf16 v[140:143], v[212:215], a[164:167], v[140:143]\n"
"ds_read_b64_tr_b16 v[224:225], v233 offset:24576\n"
"v_subrev_f32 v151, s22, v151\n"
"v_mfma_f32_16x16x32_bf16 v[140:143], v[208:211], a[160:163], v[140:143]\n"
"ds_read_b64_tr_b16 v[226:227], v233 offset:26624\n"
"v_cvt_pk_bf16_f32 v148, v148, v149\n"
"v_mfma_f32_16x16x32_bf16 v[140:143], v[192:195], a[124:127], v[140:143]\n"
"v_cvt_pk_bf16_f32 v149, v150, v151\n"
"v_subrev_f32 v152, s22, v152\n"
"v_mfma_f32_16x16x32_bf16 v[140:143], v[188:191], a[120:123], v[140:143]\n"
"s_add_u32 s41, s70, 0\n"
"v_subrev_f32 v153, s22, v153\n"
"v_subrev_f32 v154, s22, v154\n"
"v_mfma_f32_16x16x32_bf16 v[140:143], v[184:187], a[116:119], v[140:143]\n"
"s_mov_b32 m0, s71\n"
"v_subrev_f32 v155, s22, v155\n"
"v_cvt_pk_bf16_f32 v150, v152, v153\n"
"v_mfma_f32_16x16x32_bf16 v[140:143], v[180:183], a[112:115], v[140:143]\n"
"s_waitcnt vmcnt(14)\n"
"buffer_load_dwordx4 v240, s[28:31], s41 offen lds\n"
"v_mfma_f32_16x16x32_bf16 v[156:159], v[180:183], a[128:131], v[196:199]\n"
"v_xor_b32 v230, s75, v230\n"
"v_cvt_pk_bf16_f32 v151, v154, v155\n"
"v_mfma_f32_16x16x32_bf16 v[156:159], v[184:187], a[132:135], v[156:159]\n"
"v_xor_b32 v234, s75, v234\n"
"v_subrev_f32 v164, s22, v164\n"
"v_mfma_f32_16x16x32_bf16 v[156:159], v[188:191], a[136:139], v[156:159]\n"
"v_subrev_f32 v165, s22, v165\n"
"v_subrev_f32 v166, s22, v166\n"
"v_mfma_f32_16x16x32_bf16 v[156:159], v[192:195], a[140:143], v[156:159]\n"
"v_subrev_f32 v167, s22, v167\n"
"v_cvt_pk_bf16_f32 v164, v164, v165\n"
"v_mfma_f32_16x16x32_bf16 v[156:159], v[208:211], a[176:179], v[156:159]\n"
"v_cvt_pk_bf16_f32 v165, v166, v167\n"
"v_subrev_f32 v168, s22, v168\n"
"v_mfma_f32_16x16x32_bf16 v[156:159], v[212:215], a[180:183], v[156:159]\n"
"v_subrev_f32 v169, s22, v169\n"
"v_subrev_f32 v170, s22, v170\n"
"v_mfma_f32_16x16x32_bf16 v[156:159], v[216:219], a[184:187], v[156:159]\n"
"v_subrev_f32 v171, s22, v171\n"
"v_cvt_pk_bf16_f32 v166, v168, v169\n"
"v_mfma_f32_16x16x32_bf16 v[156:159], v[220:223], a[188:191], v[156:159]\n"
"buffer_store_dwordx4 v[132:135], v243, s[32:35], s16 offen offset:1024 sc0 sc1\n"
"v_cvt_pk_bf16_f32 v167, v170, v171\n"
"v_mfma_f32_16x16x32_bf16 v[172:175], v[220:223], a[252:255], v[196:199]\n"
"ds_read_b64_tr_b16 v[220:221], v234 offset:24576\n"
"v_xor_b32 v231, s75, v231\n"
"v_mfma_f32_16x16x32_bf16 v[172:175], v[216:219], a[248:251], v[172:175]\n"
"ds_read_b64_tr_b16 v[216:217], v230 offset:24576\n"
"v_xor_b32 v235, s75, v235\n"
"v_mfma_f32_16x16x32_bf16 v[172:175], v[212:215], a[244:247], v[172:175]\n"
"ds_read_b64_tr_b16 v[212:213], v234 offset:16384\n"
"v_lshlrev_b32 v202, 16, v84\n"
"v_mfma_f32_16x16x32_bf16 v[172:175], v[208:211], a[240:243], v[172:175]\n"
"ds_read_b64_tr_b16 v[208:209], v230 offset:16384\n"
"v_and_b32 v84, s73, v84\n"
"v_mfma_f32_16x16x32_bf16 v[172:175], v[192:195], a[204:207], v[172:175]\n"
"ds_read_b64_tr_b16 v[192:193], v234 offset:8192\n"
"v_add_f32 v25, v25, v84\n"
"v_mfma_f32_16x16x32_bf16 v[172:175], v[188:191], a[200:203], v[172:175]\n"
"ds_read_b64_tr_b16 v[188:189], v230 offset:8192\n"
"v_lshlrev_b32 v203, 16, v85\n"
"v_mfma_f32_16x16x32_bf16 v[172:175], v[184:187], a[196:199], v[172:175]\n"
"s_add_u32 s40, s41, s52\n"
"ds_read_b64_tr_b16 v[184:185], v234 offset:0\n"
"v_and_b32 v85, s73, v85\n"
"v_mfma_f32_16x16x32_bf16 v[172:175], v[180:183], a[192:195], v[172:175]\n"
"s_waitcnt vmcnt(13) lgkmcnt(7)\n"
"buffer_load_dwordx4 v240, s[28:31], s40 offen offset:1024 lds\n"
"v_mfma_f32_16x16x32_bf16 v[128:131], v[96:99], a[96:99], v[196:199]\n"
"s_waitcnt lgkmcnt(6)\n"
"ds_read_b64_tr_b16 v[180:181], v230 offset:0\n"
"v_add_f32 v27, v27, v85\n"
"v_mfma_f32_16x16x32_bf16 v[128:131], v[100:103], a[100:103], v[128:131]\n"
"ds_read_b64_tr_b16 v[182:183], v230 offset:2048\n"
"v_lshlrev_b32 v204, 16, v86\n"
"v_mfma_f32_16x16x32_bf16 v[128:131], v[104:107], a[104:107], v[128:131]\n"
"ds_read_b64_tr_b16 v[186:187], v234 offset:2048\n"
"v_and_b32 v86, s73, v86\n"
"v_mfma_f32_16x16x32_bf16 v[128:131], v[108:111], a[108:111], v[128:131]\n"
"ds_read_b64_tr_b16 v[190:191], v230 offset:10240\n"
"v_add_f32 v29, v29, v86\n"
"v_mfma_f32_16x16x32_bf16 v[128:131], v[112:115], a[144:147], v[128:131]\n"
"s_waitcnt vmcnt(6)\n"
"ds_read_b64_tr_b16 v[194:195], v234 offset:10240\n"
"v_lshlrev_b32 v205, 16, v87\n"
"v_mfma_f32_16x16x32_bf16 v[128:131], v[116:119], a[148:151], v[128:131]\n"
"v_subrev_u32 v200, s21, v200\n"
"ds_read_b64_tr_b16 v[210:211], v230 offset:18432\n"
"v_mfma_f32_16x16x32_bf16 v[128:131], v[120:123], a[152:155], v[128:131]\n"
"v_cmp_lt_u32 vcc, s69, v200\n"
"ds_read_b64_tr_b16 v[214:215], v234 offset:18432\n"
"v_mfma_f32_16x16x32_bf16 v[128:131], v[224:227], a[156:159], v[128:131]\n"
"ds_read_b64_tr_b16 v[218:219], v230 offset:26624\n"
"s_cbranch_vccnz LGP_%=\nLGPK_%=:\n"
"v_mfma_f32_16x16x32_bf16 v[144:147], v[224:227], a[172:175], v[196:199]\n"
"s_sub_u32 s72, s72, 1\n"
"ds_read_b64_tr_b16 v[222:223], v234 offset:26624\n"
"v_and_b32 v87, s73, v87\n"
"v_mfma_f32_16x16x32_bf16 v[144:147], v[120:123], a[168:171], v[144:147]\n"
"s_and_b32 s72, s72, 7\n"
"v_add_f32 v24, v24, v202\n"
"v_add_f32 v31, v31, v87\n"
"v_mfma_f32_16x16x32_bf16 v[144:147], v[116:119], a[164:167], v[144:147]\n"
"v_add_f32 v26, v26, v203\n"
"v_lshlrev_b32 v206, 16, v88\n"
"v_mfma_f32_16x16x32_bf16 v[144:147], v[112:115], a[160:163], v[144:147]\n"
"buffer_load_dwordx4 v[72:75], v243, s[32:35], s18 offen sc1\n"
"v_mfma_f32_16x16x32_bf16 v[144:147], v[108:111], a[124:127], v[144:147]\n"
"v_and_b32 v88, s73, v88\n"
"v_add_f32 v33, v33, v88\n"
"v_mfma_f32_16x16x32_bf16 v[144:147], v[104:107], a[120:123], v[144:147]\n"
"v_lshlrev_b32 v207, 16, v89\n"
"v_and_b32 v89, s73, v89\n"
"v_mfma_f32_16x16x32_bf16 v[144:147], v[100:103], a[116:119], v[144:147]\n"
"v_add_f32 v35, v35, v89\n"
"v_lshlrev_b32 v202, 16, v90\n"
"v_mfma_f32_16x16x32_bf16 v[144:147], v[96:99], a[112:115], v[144:147]\n"
"buffer_store_dwordx4 v[148:151], v243, s[32:35], s16 offen offset:3072 sc0 sc1\n"
"v_and_b32 v90, s73, v90\n"
"v_mfma_f32_16x16x32_bf16 v[160:163], v[96:99], a[128:131], v[196:199]\n"
"v_add_f32 v37, v37, v90\n"
"v_lshlrev_b32 v203, 16, v91\n"
"v_mfma_f32_16x16x32_bf16 v[160:163], v[100:103], a[132:135], v[160:163]\n"
"v_and_b32 v91, s73, v91\n"
"v_add_f32 v39, v39, v91\n"
"v_mfma_f32_16x16x32_bf16 v[160:163], v[104:107], a[136:139], v[160:163]\n"
"v_add_f32 v28, v28, v204\n"
"v_add_f32 v30, v30, v205\n"
"v_mfma_f32_16x16x32_bf16 v[160:163], v[108:111], a[140:143], v[160:163]\n"
"v_add_f32 v32, v32, v206\n"
"v_add_f32 v34, v34, v207\n"
"v_mfma_f32_16x16x32_bf16 v[160:163], v[112:115], a[176:179], v[160:163]\n"
"v_lshlrev_b32 v204, 16, v92\n"
"v_and_b32 v92, s73, v92\n"
"v_mfma_f32_16x16x32_bf16 v[160:163], v[116:119], a[180:183], v[160:163]\n"
"v_add_f32 v41, v41, v92\n"
"v_lshlrev_b32 v205, 16, v93\n"
"v_mfma_f32_16x16x32_bf16 v[160:163], v[120:123], a[184:187], v[160:163]\n"
"s_add_u32 s40, s41, s53\n"
"v_and_b32 v93, s73, v93\n"
"v_add_f32 v43, v43, v93\n"
"v_mfma_f32_16x16x32_bf16 v[160:163], v[224:227], a[188:191], v[160:163]\n"
"buffer_load_dwordx4 v240, s[28:31], s40 offen offset:2048 lds\n"
"v_mfma_f32_16x16x32_bf16 v[176:179], v[224:227], a[252:255], v[196:199]\n"
"ds_read_b64_tr_b16 v[224:225], v235 offset:24576\n"
"v_lshlrev_b32 v206, 16, v94\n"
"v_mfma_f32_16x16x32_bf16 v[176:179], v[120:123], a[248:251], v[176:179]\n"
"ds_read_b64_tr_b16 v[120:121], v231 offset:24576\n"
"v_and_b32 v94, s73, v94\n"
"v_mfma_f32_16x16x32_bf16 v[176:179], v[116:119], a[244:247], v[176:179]\n"
"ds_read_b64_tr_b16 v[116:117], v235 offset:16384\n"
"v_add_f32 v45, v45, v94\n"
"v_mfma_f32_16x16x32_bf16 v[176:179], v[112:115], a[240:243], v[176:179]\n"
"buffer_load_dwordx4 v[76:79], v243, s[32:35], s18 offen offset:1024 sc1\n"
"v_mfma_f32_16x16x32_bf16 v[176:179], v[108:111], a[204:207], v[176:179]\n"
"ds_read_b64_tr_b16 v[108:109], v235 offset:8192\n"
"v_lshlrev_b32 v207, 16, v95\n"
"v_mfma_f32_16x16x32_bf16 v[176:179], v[104:107], a[200:203], v[176:179]\n"
"ds_read_b64_tr_b16 v[104:105], v231 offset:8192\n"
"v_and_b32 v95, s73, v95\n"
"v_mfma_f32_16x16x32_bf16 v[176:179], v[100:103], a[196:199], v[176:179]\n"
"s_add_u32 s40, s41, s54\n"
"ds_read_b64_tr_b16 v[100:101], v235 offset:0\n"
"v_add_f32 v47, v47, v95\n"
"v_mfma_f32_16x16x32_bf16 v[176:179], v[96:99], a[192:195], v[176:179]\n"
"s_waitcnt lgkmcnt(6)\n"
"buffer_load_dwordx4 v240, s[28:31], s40 offen offset:3072 lds\n"
"v_mfma_f32_16x16x32_bf16 v[132:135], v[180:183], a[96:99], v[196:199]\n"
"ds_read_b64_tr_b16 v[96:97], v231 offset:0\n"
"v_add_f32 v36, v36, v202\n"
"v_mfma_f32_16x16x32_bf16 v[132:135], v[184:187], a[100:103], v[132:135]\n"
"ds_read_b64_tr_b16 v[98:99], v231 offset:2048\n"
"v_add_f32 v38, v38, v203\n"
"v_mfma_f32_16x16x32_bf16 v[132:135], v[188:191], a[104:107], v[132:135]\n"
"ds_read_b64_tr_b16 v[102:103], v235 offset:2048\n"
"v_add_f32 v44, v44, v206\n"
"v_mfma_f32_16x16x32_bf16 v[132:135], v[192:195], a[108:111], v[132:135]\n"
"ds_read_b64_tr_b16 v[106:107], v231 offset:10240\n"
"v_add_f32 v46, v46, v207\n"
"v_mfma_f32_16x16x32_bf16 v[132:135], v[208:211], a[144:147], v[132:135]\n"
"ds_read_b64_tr_b16 v[110:111], v235 offset:10240\n"
"v_add_f32 v40, v40, v204\n"
"v_mfma_f32_16x16x32_bf16 v[132:135], v[212:215], a[148:151], v[132:135]\n"
"ds_read_b64_tr_b16 v[112:113], v231 offset:16384\n"
"v_add_f32 v42, v42, v205\n"
"v_mfma_f32_16x16x32_bf16 v[132:135], v[216:219], a[152:155], v[132:135]\n"
"ds_read_b64_tr_b16 v[114:115], v231 offset:18432\n"
"v_mfma_f32_16x16x32_bf16 v[132:135], v[220:223], a[156:159], v[132:135]\n"
"buffer_store_dwordx4 v[164:167], v243, s[32:35], s17 offen offset:1024 sc0 sc1\n"
"v_lshlrev_b32 v206, 16, v244\n"
"v_mfma_f32_16x16x32_bf16 v[148:151], v[220:223], a[172:175], v[196:199]\n"
"ds_read_b64_tr_b16 v[118:119], v235 offset:18432\n"
"v_and_b32 v244, s73, v244\n"
"v_mfma_f32_16x16x32_bf16 v[148:151], v[216:219], a[168:171], v[148:151]\n"
"ds_read_b64_tr_b16 v[122:123], v231 offset:26624\n"
"v_add_f32 v49, v49, v244\n"
"v_mfma_f32_16x16x32_bf16 v[148:151], v[212:215], a[164:167], v[148:151]\n"
"ds_read_b64_tr_b16 v[226:227], v235 offset:26624\n"
"v_lshlrev_b32 v207, 16, v245\n"
"v_mfma_f32_16x16x32_bf16 v[148:151], v[208:211], a[160:163], v[148:151]\n"
"buffer_load_dwordx4 v[80:83], v243, s[32:35], s18 offen offset:2048 sc1\n"
"v_mfma_f32_16x16x32_bf16 v[148:151], v[192:195], a[124:127], v[148:151]\n"
"v_and_b32 v245, s73, v245\n"
"v_add_f32 v51, v51, v245\n"
"v_mfma_f32_16x16x32_bf16 v[148:151], v[188:191], a[120:123], v[148:151]\n"
"s_add_u32 s41, s70, 163840\n"
"v_lshlrev_b32 v202, 16, v246\n"
"v_and_b32 v246, s73, v246\n"
"v_mfma_f32_16x16x32_bf16 v[148:151], v[184:187], a[116:119], v[148:151]\n"
"s_add_u32 m0, s71, 16384\n"
"v_add_f32 v53, v53, v246\n"
"v_lshlrev_b32 v203, 16, v247\n"
"v_mfma_f32_16x16x32_bf16 v[148:151], v[180:183], a[112:115], v[148:151]\n"
"buffer_load_dwordx4 v240, s[28:31], s41 offen lds\n"
"v_mfma_f32_16x16x32_bf16 v[164:167], v[180:183], a[128:131], v[196:199]\n"
"v_and_b32 v247, s73, v247\n"
"v_add_f32 v55, v55, v247\n"
"v_mfma_f32_16x16x32_bf16 v[164:167], v[184:187], a[132:135], v[164:167]\n"
"v_add_f32 v48, v48, v206\n"
"v_add_f32 v50, v50, v207\n"
"v_mfma_f32_16x16x32_bf16 v[164:167], v[188:191], a[136:139], v[164:167]\n"
"v_add_f32 v52, v52, v202\n"
"v_add_f32 v54, v54, v203\n"
"v_mfma_f32_16x16x32_bf16 v[164:167], v[192:195], a[140:143], v[164:167]\n"
"v_mfma_f32_16x16x32_bf16 v[164:167], v[208:211], a[176:179], v[164:167]\n"
"v_mfma_f32_16x16x32_bf16 v[164:167], v[212:215], a[180:183], v[164:167]\n"
"v_mfma_f32_16x16x32_bf16 v[164:167], v[216:219], a[184:187], v[164:167]\n"
"s_waitcnt vmcnt(12)\n"
"v_mfma_f32_16x16x32_bf16 v[164:167], v[220:223], a[188:191], v[164:167]\n"
"s_barrier\n"
"s_waitcnt lgkmcnt(0)\n"
"v_mfma_f32_16x16x32_bf16 v[136:139], v[96:99], a[96:99], v[196:199]\n"
"ds_read_b64_tr_b16 v[180:181], v228 offset:32768\n"
"v_mfma_f32_16x16x32_bf16 v[136:139], v[100:103], a[100:103], v[136:139]\n"
"ds_read_b64_tr_b16 v[182:183], v228 offset:34816\n"
"v_mfma_f32_16x16x32_bf16 v[136:139], v[104:107], a[104:107], v[136:139]\n"
"ds_read_b64_tr_b16 v[184:185], v232 offset:32768\n"
"v_mfma_f32_16x16x32_bf16 v[136:139], v[108:111], a[108:111], v[136:139]\n"
"ds_read_b64_tr_b16 v[186:187], v232 offset:34816\n"
"v_mfma_f32_16x16x32_bf16 v[136:139], v[112:115], a[144:147], v[136:139]\n"
"ds_read_b64_tr_b16 v[188:189], v228 offset:40960\n"
"v_mfma_f32_16x16x32_bf16 v[136:139], v[116:119], a[148:151], v[136:139]\n"
"ds_read_b64_tr_b16 v[190:191], v228 offset:43008\n"
"v_mfma_f32_16x16x32_bf16 v[136:139], v[120:123], a[152:155], v[136:139]\n"
"s_add_u32 s40, s41, s52\n"
"ds_read_b64_tr_b16 v[192:193], v232 offset:40960\n"
"v_mfma_f32_16x16x32_bf16 v[136:139], v[224:227], a[156:159], v[136:139]\n"
"buffer_load_dwordx4 v240, s[28:31], s40 offen offset:1024 lds\n"
"v_mfma_f32_16x16x32_bf16 v[152:155], v[224:227], a[172:175], v[196:199]\n"
"ds_read_b64_tr_b16 v[194:195], v232 offset:43008\n"
"v_mfma_f32_16x16x32_bf16 v[152:155], v[120:123], a[168:171], v[152:155]\n"
"ds_read_b64_tr_b16 v[208:209], v228 offset:49152\n"
"v_mfma_f32_16x16x32_bf16 v[152:155], v[116:119], a[164:167], v[152:155]\n"
"ds_read_b64_tr_b16 v[210:211], v228 offset:51200\n"
"v_mfma_f32_16x16x32_bf16 v[152:155], v[112:115], a[160:163], v[152:155]\n"
"ds_read_b64_tr_b16 v[212:213], v232 offset:49152\n"
"v_mfma_f32_16x16x32_bf16 v[152:155], v[108:111], a[124:127], v[152:155]\n"
"ds_read_b64_tr_b16 v[214:215], v232 offset:51200\n"
"v_mfma_f32_16x16x32_bf16 v[152:155], v[104:107], a[120:123], v[152:155]\n"
"ds_read_b64_tr_b16 v[216:217], v228 offset:57344\n"
"v_mfma_f32_16x16x32_bf16 v[152:155], v[100:103], a[116:119], v[152:155]\n"
"s_add_u32 s40, s41, s53\n"
"ds_read_b64_tr_b16 v[218:219], v228 offset:59392\n"
"v_xor_b32 v228, s75, v228\n"
"v_mfma_f32_16x16x32_bf16 v[152:155], v[96:99], a[112:115], v[152:155]\n"
"buffer_load_dwordx4 v240, s[28:31], s40 offen offset:2048 lds\n"
"v_mfma_f32_16x16x32_bf16 v[168:171], v[96:99], a[128:131], v[196:199]\n"
"ds_read_b64_tr_b16 v[220:221], v232 offset:57344\n"
"v_mfma_f32_16x16x32_bf16 v[168:171], v[100:103], a[132:135], v[168:171]\n"
"ds_read_b64_tr_b16 v[222:223], v232 offset:59392\n"
"v_xor_b32 v232, s75, v232\n"
"v_mfma_f32_16x16x32_bf16 v[168:171], v[104:107], a[136:139], v[168:171]\n"
"ds_read_b64_tr_b16 v[96:97], v229 offset:32768\n"
"v_mfma_f32_16x16x32_bf16 v[168:171], v[108:111], a[140:143], v[168:171]\n"
"ds_read_b64_tr_b16 v[98:99], v229 offset:34816\n"
"v_mfma_f32_16x16x32_bf16 v[168:171], v[112:115], a[176:179], v[168:171]\n"
"ds_read_b64_tr_b16 v[100:101], v233 offset:32768\n"
"v_mfma_f32_16x16x32_bf16 v[168:171], v[116:119], a[180:183], v[168:171]\n"
"ds_read_b64_tr_b16 v[102:103], v233 offset:34816\n"
"v_mfma_f32_16x16x32_bf16 v[168:171], v[120:123], a[184:187], v[168:171]\n"
"s_add_u32 s40, s41, s54\n"
"ds_read_b64_tr_b16 v[104:105], v229 offset:40960\n"
"v_mfma_f32_16x16x32_bf16 v[168:171], v[224:227], a[188:191], v[168:171]\n"
"s_waitcnt lgkmcnt(7)\n"
"buffer_load_dwordx4 v240, s[28:31], s40 offen offset:3072 lds\n"
"v_mfma_f32_16x16x32_bf16 v[124:127], v[180:183], a[48:51], v[124:127]\n"
"ds_read_b64_tr_b16 v[106:107], v229 offset:43008\n"
"v_mfma_f32_16x16x32_bf16 v[124:127], v[184:187], a[52:55], v[124:127]\n"
"ds_read_b64_tr_b16 v[108:109], v233 offset:40960\n"
"v_mfma_f32_16x16x32_bf16 v[124:127], v[188:191], a[56:59], v[124:127]\n"
"ds_read_b64_tr_b16 v[110:111], v233 offset:43008\n"
"v_mfma_f32_16x16x32_bf16 v[124:127], v[192:195], a[60:63], v[124:127]\n"
"ds_read_b64_tr_b16 v[112:113], v229 offset:49152\n"
"v_mfma_f32_16x16x32_bf16 v[124:127], v[208:211], a[0:3], v[124:127]\n"
"ds_read_b64_tr_b16 v[114:115], v229 offset:51200\n"
"v_mfma_f32_16x16x32_bf16 v[124:127], v[212:215], a[4:7], v[124:127]\n"
"s_add_u32 s41, s70, 327680\n"
"ds_read_b64_tr_b16 v[116:117], v233 offset:49152\n"
"v_mfma_f32_16x16x32_bf16 v[124:127], v[216:219], a[8:11], v[124:127]\n"
"s_waitcnt lgkmcnt(6)\n"
"s_add_u32 m0, s71, 32768\n"
"ds_read_b64_tr_b16 v[118:119], v233 offset:51200\n"
"v_mfma_f32_16x16x32_bf16 v[124:127], v[220:223], a[12:15], v[124:127]\n"
"buffer_load_dwordx4 v240, s[28:31], s41 offen lds\n"
"v_mfma_f32_16x16x32_bf16 v[140:143], v[220:223], a[28:31], v[140:143]\n"
"ds_read_b64_tr_b16 v[120:121], v229 offset:57344\n"
"v_mfma_f32_16x16x32_bf16 v[140:143], v[216:219], a[24:27], v[140:143]\n"
"ds_read_b64_tr_b16 v[122:123], v229 offset:59392\n"
"v_xor_b32 v229, s75, v229\n"
"v_mfma_f32_16x16x32_bf16 v[140:143], v[212:215], a[20:23], v[140:143]\n"
"ds_read_b64_tr_b16 v[224:225], v233 offset:57344\n"
"v_mfma_f32_16x16x32_bf16 v[140:143], v[208:211], a[16:19], v[140:143]\n"
"ds_read_b64_tr_b16 v[226:227], v233 offset:59392\n"
"v_xor_b32 v233, s75, v233\n"
"v_mfma_f32_16x16x32_bf16 v[140:143], v[192:195], a[76:79], v[140:143]\n"
"v_mfma_f32_16x16x32_bf16 v[140:143], v[188:191], a[72:75], v[140:143]\n"
"v_subrev_f32 v124, s22, v124\n"
"v_subrev_f32 v125, s22, v125\n"
"v_mfma_f32_16x16x32_bf16 v[140:143], v[184:187], a[68:71], v[140:143]\n"
"s_add_u32 s40, s41, s52\n"
"v_subrev_f32 v126, s22, v126\n"
"v_subrev_f32 v127, s22, v127\n"
"v_mfma_f32_16x16x32_bf16 v[140:143], v[180:183], a[64:67], v[140:143]\n"
"buffer_load_dwordx4 v240, s[28:31], s40 offen offset:1024 lds\n"
"v_mfma_f32_16x16x32_bf16 v[156:159], v[180:183], a[80:83], v[156:159]\n"
"v_cvt_pk_bf16_f32 v124, v124, v125\n"
"v_cvt_pk_bf16_f32 v125, v126, v127\n"
"v_mfma_f32_16x16x32_bf16 v[156:159], v[184:187], a[84:87], v[156:159]\n"
"v_mfma_f32_16x16x32_bf16 v[156:159], v[188:191], a[88:91], v[156:159]\n"
"v_mfma_f32_16x16x32_bf16 v[156:159], v[192:195], a[92:95], v[156:159]\n"
"v_mfma_f32_16x16x32_bf16 v[156:159], v[208:211], a[32:35], v[156:159]\n"
"v_mfma_f32_16x16x32_bf16 v[156:159], v[212:215], a[36:39], v[156:159]\n"
"v_subrev_f32 v140, s22, v140\n"
"v_subrev_f32 v141, s22, v141\n"
"v_mfma_f32_16x16x32_bf16 v[156:159], v[216:219], a[40:43], v[156:159]\n"
"s_waitcnt vmcnt(7)\n"
"v_subrev_f32 v142, s22, v142\n"
"v_subrev_f32 v143, s22, v143\n"
"v_mfma_f32_16x16x32_bf16 v[156:159], v[220:223], a[44:47], v[156:159]\n"
"s_barrier\n"
"buffer_store_dword v241, v248, s[84:87], 0 offen sc1\n"
"v_mfma_f32_16x16x32_bf16 v[172:175], v[220:223], a[228:231], v[172:175]\n"
"ds_read_b64_tr_b16 v[220:221], v234 offset:57344\n"
"v_cvt_pk_bf16_f32 v140, v140, v141\n"
"v_mfma_f32_16x16x32_bf16 v[172:175], v[216:219], a[224:227], v[172:175]\n"
"ds_read_b64_tr_b16 v[216:217], v230 offset:57344\n"
"v_cvt_pk_bf16_f32 v141, v142, v143\n"
"v_mfma_f32_16x16x32_bf16 v[172:175], v[212:215], a[212:215], v[172:175]\n"
"ds_read_b64_tr_b16 v[212:213], v234 offset:49152\n"
"v_add_u32 v241, 1, v241\n"
"v_mfma_f32_16x16x32_bf16 v[172:175], v[208:211], a[208:211], v[172:175]\n"
"s_add_u32 s16, s90, s62\n"
"buffer_load_dwordx4 v[84:87], v243, s[32:35], s18 offen offset:3072 sc1\n"
"v_mfma_f32_16x16x32_bf16 v[172:175], v[192:195], a[236:239], v[172:175]\n"
"s_add_u32 s17, s16, 4096\n"
"ds_read_b64_tr_b16 v[192:193], v234 offset:40960\n"
"v_mfma_f32_16x16x32_bf16 v[172:175], v[188:191], a[232:235], v[172:175]\n"
"s_lshl_b32 s62, s70, 9\n"
"ds_read_b64_tr_b16 v[188:189], v230 offset:40960\n"
"v_subrev_f32 v156, s22, v156\n"
"v_mfma_f32_16x16x32_bf16 v[172:175], v[184:187], a[220:223], v[172:175]\n"
"s_add_u32 s40, s41, s53\n"
"ds_read_b64_tr_b16 v[184:185], v234 offset:32768\n"
"v_subrev_f32 v157, s22, v157\n"
"v_mfma_f32_16x16x32_bf16 v[172:175], v[180:183], a[216:219], v[172:175]\n"
"buffer_load_dwordx4 v240, s[28:31], s40 offen offset:2048 lds\n"
"v_mfma_f32_16x16x32_bf16 v[128:131], v[96:99], a[48:51], v[128:131]\n"
"ds_read_b64_tr_b16 v[180:181], v230 offset:32768\n"
"v_subrev_f32 v158, s22, v158\n"
"v_mfma_f32_16x16x32_bf16 v[128:131], v[100:103], a[52:55], v[128:131]\n"
"s_waitcnt lgkmcnt(5)\n"
"ds_read_b64_tr_b16 v[182:183], v230 offset:34816\n"
"v_subrev_f32 v159, s22, v159\n"
"v_mfma_f32_16x16x32_bf16 v[128:131], v[104:107], a[56:59], v[128:131]\n"
"ds_read_b64_tr_b16 v[186:187], v234 offset:34816\n"
"v_cvt_pk_bf16_f32 v156, v156, v157\n"
"v_mfma_f32_16x16x32_bf16 v[128:131], v[108:111], a[60:63], v[128:131]\n"
"ds_read_b64_tr_b16 v[190:191], v230 offset:43008\n"
"v_cvt_pk_bf16_f32 v157, v158, v159\n"
"v_mfma_f32_16x16x32_bf16 v[128:131], v[112:115], a[0:3], v[128:131]\n"
"ds_read_b64_tr_b16 v[194:195], v234 offset:43008\n"
"v_lshlrev_b32 v202, 16, v72\n"
"v_mfma_f32_16x16x32_bf16 v[128:131], v[116:119], a[4:7], v[128:131]\n"
"ds_read_b64_tr_b16 v[208:209], v230 offset:49152\n"
"v_and_b32 v72, s73, v72\n"
"v_mfma_f32_16x16x32_bf16 v[128:131], v[120:123], a[8:11], v[128:131]\n"
"s_add_u32 s40, s41, s54\n"
"ds_read_b64_tr_b16 v[210:211], v230 offset:51200\n"
"v_add_f32 v0, v0, v202\n"
"v_mfma_f32_16x16x32_bf16 v[128:131], v[224:227], a[12:15], v[128:131]\n"
"buffer_load_dwordx4 v240, s[28:31], s40 offen offset:3072 lds\n"
"v_mfma_f32_16x16x32_bf16 v[144:147], v[224:227], a[28:31], v[144:147]\n"
"ds_read_b64_tr_b16 v[214:215], v234 offset:51200\n"
"v_add_f32 v1, v1, v72\n"
"v_mfma_f32_16x16x32_bf16 v[144:147], v[120:123], a[24:27], v[144:147]\n"
"ds_read_b64_tr_b16 v[218:219], v230 offset:59392\n"
"v_lshlrev_b32 v203, 16, v73\n"
"v_mfma_f32_16x16x32_bf16 v[144:147], v[116:119], a[20:23], v[144:147]\n"
"ds_read_b64_tr_b16 v[222:223], v234 offset:59392\n"
"v_and_b32 v73, s73, v73\n"
"v_mfma_f32_16x16x32_bf16 v[144:147], v[112:115], a[16:19], v[144:147]\n"
"buffer_load_dwordx4 v[88:91], v243, s[32:35], s19 offen sc1\n"
"v_mfma_f32_16x16x32_bf16 v[144:147], v[108:111], a[76:79], v[144:147]\n"
"v_add_f32 v2, v2, v203\n"
"v_add_f32 v3, v3, v73\n"
"v_mfma_f32_16x16x32_bf16 v[144:147], v[104:107], a[72:75], v[144:147]\n"
"s_add_u32 s41, s70, 491520\n"
"v_subrev_f32 v128, s22, v128\n"
"v_subrev_f32 v129, s22, v129\n"
"v_mfma_f32_16x16x32_bf16 v[144:147], v[100:103], a[68:71], v[144:147]\n"
"s_add_u32 m0, s71, 49152\n"
"v_subrev_f32 v130, s22, v130\n"
"v_subrev_f32 v131, s22, v131\n"
"v_mfma_f32_16x16x32_bf16 v[144:147], v[96:99], a[64:67], v[144:147]\n"
"buffer_load_dwordx4 v240, s[28:31], s41 offen lds\n"
"v_mfma_f32_16x16x32_bf16 v[160:163], v[96:99], a[80:83], v[160:163]\n"
"s_and_b32 s70, s88, 15\n"
"v_cvt_pk_bf16_f32 v126, v128, v129\n"
"v_cvt_pk_bf16_f32 v127, v130, v131\n"
"v_mfma_f32_16x16x32_bf16 v[160:163], v[100:103], a[84:87], v[160:163]\n"
"s_lshr_b32 s88, s88, 4\n"
"v_lshlrev_b32 v204, 16, v74\n"
"v_and_b32 v74, s73, v74\n"
"v_mfma_f32_16x16x32_bf16 v[160:163], v[104:107], a[88:91], v[160:163]\n"
"s_lshl_b32 s70, s70, 7\n"
"v_add_f32 v4, v4, v204\n"
"v_add_f32 v5, v5, v74\n"
"v_mfma_f32_16x16x32_bf16 v[160:163], v[108:111], a[92:95], v[160:163]\n"
"s_xor_b32 s71, s71, s75\n"
"v_lshlrev_b32 v205, 16, v75\n"
"v_and_b32 v75, s73, v75\n"
"v_mfma_f32_16x16x32_bf16 v[160:163], v[112:115], a[32:35], v[160:163]\n"
"v_add_f32 v6, v6, v205\n"
"v_add_f32 v7, v7, v75\n"
"v_mfma_f32_16x16x32_bf16 v[160:163], v[116:119], a[36:39], v[160:163]\n"
"v_subrev_f32 v144, s22, v144\n"
"v_subrev_f32 v145, s22, v145\n"
"v_mfma_f32_16x16x32_bf16 v[160:163], v[120:123], a[40:43], v[160:163]\n"
"v_subrev_f32 v146, s22, v146\n"
"v_subrev_f32 v147, s22, v147\n"
"v_mfma_f32_16x16x32_bf16 v[160:163], v[224:227], a[44:47], v[160:163]\n"
"buffer_store_dwordx4 v[124:127], v243, s[32:35], s16 offen sc0 sc1\n"
"v_cvt_pk_bf16_f32 v142, v144, v145\n"
"v_mfma_f32_16x16x32_bf16 v[176:179], v[224:227], a[228:231], v[176:179]\n"
"ds_read_b64_tr_b16 v[224:225], v235 offset:57344\n"
"v_cvt_pk_bf16_f32 v143, v146, v147\n"
"v_mfma_f32_16x16x32_bf16 v[176:179], v[120:123], a[224:227], v[176:179]\n"
"ds_read_b64_tr_b16 v[120:121], v231 offset:57344\n"
"v_lshlrev_b32 v206, 16, v76\n"
"v_mfma_f32_16x16x32_bf16 v[176:179], v[116:119], a[212:215], v[176:179]\n"
"ds_read_b64_tr_b16 v[116:117], v235 offset:49152\n"
"v_and_b32 v76, s73, v76\n"
"v_mfma_f32_16x16x32_bf16 v[176:179], v[112:115], a[208:211], v[176:179]\n"
"buffer_load_dwordx4 v[92:95], v243, s[32:35], s19 offen offset:1024 sc1\n"
"v_mfma_f32_16x16x32_bf16 v[176:179], v[108:111], a[236:239], v[176:179]\n"
"ds_read_b64_tr_b16 v[108:109], v235 offset:40960\n"
"v_add_f32 v8, v8, v206\n"
"v_mfma_f32_16x16x32_bf16 v[176:179], v[104:107], a[232:235], v[176:179]\n"
"ds_read_b64_tr_b16 v[104:105], v231 offset:40960\n"
"v_subrev_f32 v160, s22, v160\n"
"v_mfma_f32_16x16x32_bf16 v[176:179], v[100:103], a[220:223], v[176:179]\n"
"s_add_u32 s40, s41, s52\n"
"ds_read_b64_tr_b16 v[100:101], v235 offset:32768\n"
"v_subrev_f32 v161, s22, v161\n"
"v_mfma_f32_16x16x32_bf16 v[176:179], v[96:99], a[216:219], v[176:179]\n"
"s_waitcnt vmcnt(14) lgkmcnt(6)\n"
"buffer_load_dwordx4 v240, s[28:31], s40 offen offset:1024 lds\n"
"v_mfma_f32_16x16x32_bf16 v[132:135], v[180:183], a[48:51], v[132:135]\n"
"ds_read_b64_tr_b16 v[96:97], v231 offset:32768\n"
"v_subrev_f32 v162, s22, v162\n"
"v_mfma_f32_16x16x32_bf16 v[132:135], v[184:187], a[52:55], v[132:135]\n"
"ds_read_b64_tr_b16 v[98:99], v231 offset:34816\n"
"v_subrev_f32 v163, s22, v163\n"
"v_mfma_f32_16x16x32_bf16 v[132:135], v[188:191], a[56:59], v[132:135]\n"
"ds_read_b64_tr_b16 v[102:103], v235 offset:34816\n"
"v_cvt_pk_bf16_f32 v158, v160, v161\n"
"v_mfma_f32_16x16x32_bf16 v[132:135], v[192:195], a[60:63], v[132:135]\n"
"ds_read_b64_tr_b16 v[106:107], v231 offset:43008\n"
"v_cvt_pk_bf16_f32 v159, v162, v163\n"
"v_mfma_f32_16x16x32_bf16 v[132:135], v[208:211], a[0:3], v[132:135]\n"
"ds_read_b64_tr_b16 v[110:111], v235 offset:43008\n"
"v_add_f32 v9, v9, v76\n"
"v_mfma_f32_16x16x32_bf16 v[132:135], v[212:215], a[4:7], v[132:135]\n"
"ds_read_b64_tr_b16 v[112:113], v231 offset:49152\n"
"v_lshlrev_b32 v207, 16, v77\n"
"v_mfma_f32_16x16x32_bf16 v[132:135], v[216:219], a[8:11], v[132:135]\n"
"ds_read_b64_tr_b16 v[114:115], v231 offset:51200\n"
"v_and_b32 v77, s73, v77\n"
"v_mfma_f32_16x16x32_bf16 v[132:135], v[220:223], a[12:15], v[132:135]\n"
"buffer_store_dwordx4 v[140:143], v243, s[32:35], s16 offen offset:2048 sc0 sc1\n"
"v_add_f32 v10, v10, v207\n"
"v_mfma_f32_16x16x32_bf16 v[148:151], v[220:223], a[28:31], v[148:151]\n"
"ds_read_b64_tr_b16 v[118:119], v235 offset:51200\n"
"v_add_f32 v11, v11, v77\n"
"v_mfma_f32_16x16x32_bf16 v[148:151], v[216:219], a[24:27], v[148:151]\n"
"ds_read_b64_tr_b16 v[122:123], v231 offset:59392\n"
"v_lshlrev_b32 v202, 16, v78\n"
"v_mfma_f32_16x16x32_bf16 v[148:151], v[212:215], a[20:23], v[148:151]\n"
"ds_read_b64_tr_b16 v[226:227], v235 offset:59392\n"
"v_and_b32 v78, s73, v78\n"
"v_mfma_f32_16x16x32_bf16 v[148:151], v[208:211], a[16:19], v[148:151]\n"
"v_add_f32 v12, v12, v202\n"
"v_add_f32 v13, v13, v78\n"
"v_mfma_f32_16x16x32_bf16 v[148:151], v[192:195], a[76:79], v[148:151]\n"
"v_lshlrev_b32 v203, 16, v79\n"
"v_and_b32 v79, s73, v79\n"
"v_mfma_f32_16x16x32_bf16 v[148:151], v[188:191], a[72:75], v[148:151]\n"
"v_add_f32 v14, v14, v203\n"
"v_add_f32 v15, v15, v79\n"
"v_mfma_f32_16x16x32_bf16 v[148:151], v[184:187], a[68:71], v[148:151]\n"
"s_add_u32 s40, s41, s53\n"
"v_lshlrev_b32 v204, 16, v80\n"
"v_and_b32 v80, s73, v80\n"
"v_mfma_f32_16x16x32_bf16 v[148:151], v[180:183], a[64:67], v[148:151]\n"
"buffer_load_dwordx4 v240, s[28:31], s40 offen offset:2048 lds\n"
"v_mfma_f32_16x16x32_bf16 v[164:167], v[180:183], a[80:83], v[164:167]\n"
"v_add_f32 v16, v16, v204\n"
"v_add_f32 v17, v17, v80\n"
"v_mfma_f32_16x16x32_bf16 v[164:167], v[184:187], a[84:87], v[164:167]\n"
"v_lshlrev_b32 v205, 16, v81\n"
"v_and_b32 v81, s73, v81\n"
"v_mfma_f32_16x16x32_bf16 v[164:167], v[188:191], a[88:91], v[164:167]\n"
"v_add_f32 v18, v18, v205\n"
"v_add_f32 v19, v19, v81\n"
"v_mfma_f32_16x16x32_bf16 v[164:167], v[192:195], a[92:95], v[164:167]\n"
"buffer_load_dwordx4 v[244:247], v243, s[32:35], s19 offen offset:2048 sc1\n"
"v_mfma_f32_16x16x32_bf16 v[164:167], v[208:211], a[32:35], v[164:167]\n"
"s_lshl4_add_u32 s18, s72, s61\n"
"v_lshlrev_b32 v206, 16, v82\n"
"v_and_b32 v82, s73, v82\n"
"v_mfma_f32_16x16x32_bf16 v[164:167], v[212:215], a[36:39], v[164:167]\n"
"s_lshl_b32 s19, s72, 2\n"
"v_add_f32 v20, v20, v206\n"
"v_add_f32 v21, v21, v82\n"
"v_mfma_f32_16x16x32_bf16 v[164:167], v[216:219], a[40:43], v[164:167]\n"
"s_waitcnt vmcnt(14)\n"
"s_lshr_b32 s19, s89, s19\n"
"v_lshlrev_b32 v207, 16, v83\n"
"v_mfma_f32_16x16x32_bf16 v[164:167], v[220:223], a[44:47], v[164:167]\n"
"s_barrier\n"
"s_and_b32 s19, s19, 1\n"
"s_waitcnt lgkmcnt(0)\n"
"v_mfma_f32_16x16x32_bf16 v[136:139], v[96:99], a[48:51], v[136:139]\n"
"s_lshl3_add_u32 s18, s18, s19\n"
"ds_read_b64_tr_b16 v[180:181], v228 offset:0\n"
"v_and_b32 v83, s73, v83\n"
"v_mfma_f32_16x16x32_bf16 v[136:139], v[100:103], a[52:55], v[136:139]\n"
"s_lshl_b32 s18, s18, 13\n"
"ds_read_b64_tr_b16 v[182:183], v228 offset:2048\n"
"v_add_f32 v22, v22, v207\n"
"v_mfma_f32_16x16x32_bf16 v[136:139], v[104:107], a[56:59], v[136:139]\n"
"s_add_u32 s19, s18, 4096\n"
"v_lshlrev_b32_e64 v201, 2, s72\n"
"ds_read_b64_tr_b16 v[184:185], v232 offset:0\n"
"v_mfma_f32_16x16x32_bf16 v[136:139], v[108:111], a[60:63], v[136:139]\n"
"s_mov_b32 s69, 15\n"
"global_load_dword v200, v201, s[6:7] sc1\n"
"v_mfma_f32_16x16x32_bf16 v[136:139], v[112:115], a[0:3], v[136:139]\n"
"s_add_u32 s21, s21, 1\n"
"ds_read_b64_tr_b16 v[186:187], v232 offset:2048\n"
"v_add_f32 v23, v23, v83\n"
"v_mfma_f32_16x16x32_bf16 v[136:139], v[116:119], a[4:7], v[136:139]\n"
"ds_read_b64_tr_b16 v[188:189], v228 offset:8192\n"
"v_subrev_f32 v172, s22, v172\n"
"v_mfma_f32_16x16x32_bf16 v[136:139], v[120:123], a[8:11], v[136:139]\n"
"ds_read_b64_tr_b16 v[190:191], v228 offset:10240\n"
"v_subrev_f32 v173, s22, v173\n"
"v_mfma_f32_16x16x32_bf16 v[136:139], v[224:227], a[12:15], v[136:139]\n"
"buffer_store_dwordx4 v[156:159], v243, s[32:35], s17 offen sc0 sc1\n"
"v_subrev_f32 v174, s22, v174\n"
"v_mfma_f32_16x16x32_bf16 v[152:155], v[224:227], a[28:31], v[152:155]\n"
"ds_read_b64_tr_b16 v[192:193], v232 offset:8192\n"
"v_subrev_f32 v175, s22, v175\n"
"v_mfma_f32_16x16x32_bf16 v[152:155], v[120:123], a[24:27], v[152:155]\n"
"ds_read_b64_tr_b16 v[194:195], v232 offset:10240\n"
"v_cvt_pk_bf16_f32 v172, v172, v173\n"
"v_mfma_f32_16x16x32_bf16 v[152:155], v[116:119], a[20:23], v[152:155]\n"
"ds_read_b64_tr_b16 v[208:209], v228 offset:16384\n"
"v_cvt_pk_bf16_f32 v173, v174, v175\n"
"v_mfma_f32_16x16x32_bf16 v[152:155], v[112:115], a[16:19], v[152:155]\n"
"ds_read_b64_tr_b16 v[210:211], v228 offset:18432\n"
"v_subrev_f32 v176, s22, v176\n"
"v_mfma_f32_16x16x32_bf16 v[152:155], v[108:111], a[76:79], v[152:155]\n"
"ds_read_b64_tr_b16 v[212:213], v232 offset:16384\n"
"v_subrev_f32 v177, s22, v177\n"
"v_mfma_f32_16x16x32_bf16 v[152:155], v[104:107], a[72:75], v[152:155]\n"
"ds_read_b64_tr_b16 v[214:215], v232 offset:18432\n"
"v_subrev_f32 v178, s22, v178\n"
"v_mfma_f32_16x16x32_bf16 v[152:155], v[100:103], a[68:71], v[152:155]\n"
"s_add_u32 s40, s41, s54\n"
"ds_read_b64_tr_b16 v[216:217], v228 offset:24576\n"
"v_subrev_f32 v179, s22, v179\n"
"v_mfma_f32_16x16x32_bf16 v[152:155], v[96:99], a[64:67], v[152:155]\n"
"buffer_load_dwordx4 v240, s[28:31], s40 offen offset:3072 lds\n"
"v_mfma_f32_16x16x32_bf16 v[168:171], v[96:99], a[80:83], v[168:171]\n"
"ds_read_b64_tr_b16 v[218:219], v228 offset:26624\n"
"v_cvt_pk_bf16_f32 v174, v176, v177\n"
"v_mfma_f32_16x16x32_bf16 v[168:171], v[100:103], a[84:87], v[168:171]\n"
"ds_read_b64_tr_b16 v[220:221], v232 offset:24576\n"
"v_cvt_pk_bf16_f32 v175, v178, v179\n"
"v_mfma_f32_16x16x32_bf16 v[168:171], v[104:107], a[88:91], v[168:171]\n"
"ds_read_b64_tr_b16 v[222:223], v232 offset:26624\n"
"v_subrev_f32 v132, s22, v132\n"
"v_mfma_f32_16x16x32_bf16 v[168:171], v[108:111], a[92:95], v[168:171]\n"
"ds_read_b64_tr_b16 v[96:97], v229 offset:0\n"
"v_subrev_f32 v133, s22, v133\n"
"v_mfma_f32_16x16x32_bf16 v[168:171], v[112:115], a[32:35], v[168:171]\n"
"ds_read_b64_tr_b16 v[98:99], v229 offset:2048\n"
"v_subrev_f32 v134, s22, v134\n"
"v_mfma_f32_16x16x32_bf16 v[168:171], v[116:119], a[36:39], v[168:171]\n"
"ds_read_b64_tr_b16 v[100:101], v233 offset:0\n"
"v_subrev_f32 v135, s22, v135\n"
"v_mfma_f32_16x16x32_bf16 v[168:171], v[120:123], a[40:43], v[168:171]\n"
"s_add_u32 s47, s47, 1\n"
"ds_read_b64_tr_b16 v[102:103], v233 offset:2048\n"
"v_cvt_pk_bf16_f32 v132, v132, v133\n"
"v_mfma_f32_16x16x32_bf16 v[168:171], v[224:227], a[44:47], v[168:171]\n"
"s_waitcnt lgkmcnt(7)\n"
"s_cmp_lg_u32 s47, 9\n"
"ds_read_b64_tr_b16 v[104:105], v229 offset:8192\n"
"s_cbranch_scc1 LGL_%=\n"
"v_mfma_f32_16x16x32_bf16 v[0:3], v[180:183], a[96:99], v[0:3]\n"
"v_xor_b32 v235, s75, v235\n"
"v_subrev_f32 v136, s22, v136\n"
"v_mfma_f32_16x16x32_bf16 v[0:3], v[184:187], a[100:103], v[0:3]\n"
"ds_read_b64_tr_b16 v[114:115], v229 offset:18432\n"
"v_cvt_pk_bf16_f32 v133, v134, v135\n"
"v_mfma_f32_16x16x32_bf16 v[0:3], v[188:191], a[104:107], v[0:3]\n"
"v_mov_b32 v201, v237\n"
"v_subrev_f32 v138, s22, v138\n"
"v_subrev_f32 v139, s22, v139\n"
"v_mfma_f32_16x16x32_bf16 v[0:3], v[192:195], a[108:111], v[0:3]\n"
"global_load_dword v200, v201, s[6:7] sc1\n"
"v_mfma_f32_16x16x32_bf16 v[0:3], v[208:211], a[144:147], v[0:3]\n"
"v_subrev_f32 v167, s22, v167\n"
"v_cvt_pk_bf16_f32 v135, v138, v139\n"
"v_mfma_f32_16x16x32_bf16 v[0:3], v[212:215], a[148:151], v[0:3]\n"
"s_waitcnt lgkmcnt(3)\n"
"v_subrev_f32 v148, s22, v148\n"
"v_subrev_f32 v137, s22, v137\n"
"v_mfma_f32_16x16x32_bf16 v[0:3], v[216:219], a[152:155], v[0:3]\n"
"v_subrev_f32 v165, s22, v165\n"
"v_subrev_f32 v168, s22, v168\n"
"v_mfma_f32_16x16x32_bf16 v[0:3], v[220:223], a[156:159], v[0:3]\n"
"buffer_store_dwordx4 v[172:175], v243, s[32:35], s17 offen offset:2048 sc0 sc1\n"
"v_subrev_f32 v151, s22, v151\n"
"v_cvt_pk_bf16_f32 v134, v136, v137\n"
"v_mfma_f32_16x16x32_bf16 v[16:19], v[220:223], a[172:175], v[16:19]\n"
"buffer_store_dwordx4 v[132:135], v243, s[32:35], s16 offen offset:1024 sc0 sc1\n"
"v_subrev_f32 v149, s22, v149\n"
"v_subrev_f32 v150, s22, v150\n"
"v_subrev_f32 v164, s22, v164\n"
"v_cvt_pk_bf16_f32 v164, v164, v165\n"
"v_subrev_f32 v153, s22, v153\n"
"v_subrev_f32 v155, s22, v155\n"
"v_mfma_f32_16x16x32_bf16 v[16:19], v[216:219], a[168:171], v[16:19]\n"
"v_cvt_pk_bf16_f32 v148, v148, v149\n"
"v_subrev_f32 v154, s22, v154\n"
"v_mfma_f32_16x16x32_bf16 v[16:19], v[212:215], a[164:167], v[16:19]\n"
"v_cvt_pk_bf16_f32 v149, v150, v151\n"
"v_subrev_f32 v170, s22, v170\n"
"v_mfma_f32_16x16x32_bf16 v[16:19], v[208:211], a[160:163], v[16:19]\n"
"v_subrev_f32 v166, s22, v166\n"
"v_subrev_f32 v171, s22, v171\n"
"v_mfma_f32_16x16x32_bf16 v[16:19], v[192:195], a[124:127], v[16:19]\n"
"v_cvt_pk_bf16_f32 v165, v166, v167\n"
"v_cvt_pk_bf16_f32 v167, v170, v171\n"
"v_mfma_f32_16x16x32_bf16 v[16:19], v[188:191], a[120:123], v[16:19]\n"
"v_subrev_f32 v152, s22, v152\n"
"v_cvt_pk_bf16_f32 v151, v154, v155\n"
"v_mfma_f32_16x16x32_bf16 v[16:19], v[184:187], a[116:119], v[16:19]\n"
"v_cvt_pk_bf16_f32 v150, v152, v153\n"
"v_subrev_f32 v169, s22, v169\n"
"v_mfma_f32_16x16x32_bf16 v[16:19], v[180:183], a[112:115], v[16:19]\n"
"s_waitcnt vmcnt(16)\n"
"buffer_store_dwordx4 v[148:151], v243, s[32:35], s16 offen offset:3072 sc0 sc1\n"
"v_cvt_pk_bf16_f32 v166, v168, v169\n"
"v_mfma_f32_16x16x32_bf16 v[32:35], v[180:183], a[128:131], v[32:35]\n"
"buffer_store_dwordx4 v[164:167], v243, s[32:35], s17 offen offset:1024 sc0 sc1\n"
"ds_read_b64_tr_b16 v[120:121], v229 offset:24576\n"
"v_mfma_f32_16x16x32_bf16 v[32:35], v[184:187], a[132:135], v[32:35]\n"
"ds_read_b64_tr_b16 v[224:225], v233 offset:24576\n"
"v_xor_b32 v234, s75, v234\n"
"v_mfma_f32_16x16x32_bf16 v[32:35], v[188:191], a[136:139], v[32:35]\n"
"ds_read_b64_tr_b16 v[122:123], v229 offset:26624\n"
"v_lshlrev_b32 v203, 16, v85\n"
"v_mfma_f32_16x16x32_bf16 v[32:35], v[192:195], a[140:143], v[32:35]\n"
"ds_read_b64_tr_b16 v[108:109], v233 offset:8192\n"
"v_lshlrev_b32 v202, 16, v84\n"
"v_mfma_f32_16x16x32_bf16 v[32:35], v[208:211], a[176:179], v[32:35]\n"
"ds_read_b64_tr_b16 v[226:227], v233 offset:26624\n"
"v_and_b32 v84, s73, v84\n"
"v_mfma_f32_16x16x32_bf16 v[32:35], v[212:215], a[180:183], v[32:35]\n"
"ds_read_b64_tr_b16 v[110:111], v233 offset:10240\n"
"v_bfe_u32 v161, v242, 8, 2\n"
"v_mfma_f32_16x16x32_bf16 v[32:35], v[216:219], a[184:187], v[32:35]\n"
"ds_read_b64_tr_b16 v[106:107], v229 offset:10240\n"
"v_lshlrev_b32 v205, 16, v87\n"
"v_mfma_f32_16x16x32_bf16 v[32:35], v[220:223], a[188:191], v[32:35]\n"
"v_xor_b32 v230, s75, v230\n"
"v_lshlrev_b32 v204, 16, v86\n"
"v_mfma_f32_16x16x32_bf16 v[48:51], v[220:223], a[252:255], v[48:51]\n"
"ds_read_b64_tr_b16 v[220:221], v234 offset:24576\n"
"v_add_f32 v26, v26, v203\n"
"v_mfma_f32_16x16x32_bf16 v[48:51], v[216:219], a[248:251], v[48:51]\n"
"ds_read_b64_tr_b16 v[112:113], v229 offset:16384\n"
"v_bfe_u32 v160, v242, 6, 2\n"
"v_mfma_f32_16x16x32_bf16 v[48:51], v[212:215], a[244:247], v[48:51]\n"
"ds_read_b64_tr_b16 v[116:117], v233 offset:16384\n"
"v_add_f32 v24, v24, v202\n"
"v_mfma_f32_16x16x32_bf16 v[48:51], v[208:211], a[240:243], v[48:51]\n"
"ds_read_b64_tr_b16 v[118:119], v233 offset:18432\n"
"v_add_f32 v25, v25, v84\n"
"v_mfma_f32_16x16x32_bf16 v[48:51], v[192:195], a[204:207], v[48:51]\n"
"ds_read_b64_tr_b16 v[210:211], v230 offset:18432\n"
"v_add_f32 v28, v28, v204\n"
"v_mfma_f32_16x16x32_bf16 v[48:51], v[188:191], a[200:203], v[48:51]\n"
"ds_read_b64_tr_b16 v[188:189], v230 offset:8192\n"
"v_and_b32 v86, s73, v86\n"
"v_mfma_f32_16x16x32_bf16 v[48:51], v[184:187], a[196:199], v[48:51]\n"
"ds_read_b64_tr_b16 v[194:195], v234 offset:10240\n"
"v_mov_b32 v164, 0\n"
"v_mfma_f32_16x16x32_bf16 v[48:51], v[180:183], a[192:195], v[48:51]\n"
"s_waitcnt vmcnt(15)\n"
"v_xor_b32 v160, v160, v161\n"
"v_lshlrev_b32 v202, 16, v90\n"
"v_mfma_f32_16x16x32_bf16 v[4:7], v[96:99], a[96:99], v[4:7]\n"
"s_waitcnt lgkmcnt(6)\n"
"ds_read_b64_tr_b16 v[222:223], v234 offset:26624\n"
"v_lshlrev_b32 v203, 16, v91\n"
"v_mfma_f32_16x16x32_bf16 v[4:7], v[100:103], a[100:103], v[4:7]\n"
"ds_read_b64_tr_b16 v[184:185], v234 offset:0\n"
"v_lshlrev_b32 v206, 16, v88\n"
"v_mfma_f32_16x16x32_bf16 v[4:7], v[104:107], a[104:107], v[4:7]\n"
"ds_read_b64_tr_b16 v[216:217], v230 offset:24576\n"
"v_mfma_f32_16x16x32_bf16 v[4:7], v[108:111], a[108:111], v[4:7]\n"
"s_waitcnt lgkmcnt(6)\n"
"ds_read_b64_tr_b16 v[218:219], v230 offset:26624\n"
"v_and_b32 v91, s73, v91\n"
"v_mfma_f32_16x16x32_bf16 v[4:7], v[112:115], a[144:147], v[4:7]\n"
"s_waitcnt vmcnt(4)\n"
"ds_read_b64_tr_b16 v[190:191], v230 offset:10240\n"
"v_lshlrev_b32 v207, 16, v89\n"
"v_mfma_f32_16x16x32_bf16 v[4:7], v[116:119], a[148:151], v[4:7]\n"
"v_subrev_u32 v200, s21, v200\n"
"ds_read_b64_tr_b16 v[208:209], v230 offset:16384\n"
"v_mfma_f32_16x16x32_bf16 v[4:7], v[120:123], a[152:155], v[4:7]\n"
"v_cmp_lt_u32 vcc, s69, v200\n"
"ds_read_b64_tr_b16 v[182:183], v230 offset:2048\n"
"v_mfma_f32_16x16x32_bf16 v[4:7], v[224:227], a[156:159], v[4:7]\n"
"ds_read_b64_tr_b16 v[180:181], v230 offset:0\n"
"s_cbranch_vccnz LGQ_%=\nLGQK_%=:\n"
"v_mfma_f32_16x16x32_bf16 v[20:23], v[224:227], a[172:175], v[20:23]\n"
"s_bfe_u32 s20, s89, 65536\ns_lshl_b32 s20, s20, 13\ns_add_u32 s20, s20, 0\n"
"ds_read_b64_tr_b16 v[214:215], v234 offset:18432\n"
"v_and_b32 v85, s73, v85\n"
"v_mfma_f32_16x16x32_bf16 v[20:23], v[120:123], a[168:171], v[20:23]\n"
"buffer_load_dwordx4 v[124:127], v236, s[32:35], s20 offen sc1\n"
"v_mfma_f32_16x16x32_bf16 v[20:23], v[116:119], a[164:167], v[20:23]\n"
"v_and_b32 v90, s73, v90\n"
"v_add_f32 v37, v37, v90\n"
"v_mfma_f32_16x16x32_bf16 v[20:23], v[112:115], a[160:163], v[20:23]\n"
"s_bfe_u32 s20, s89, 65540\ns_lshl_b32 s20, s20, 13\ns_add_u32 s20, s20, 1048576\n"
"v_add_f32 v36, v36, v202\n"
"v_add_f32 v38, v38, v203\n"
"v_mfma_f32_16x16x32_bf16 v[20:23], v[108:111], a[124:127], v[20:23]\n"
"buffer_load_dwordx4 v[128:131], v236, s[32:35], s20 offen sc1\n"
"v_mfma_f32_16x16x32_bf16 v[20:23], v[104:107], a[120:123], v[20:23]\n"
"v_add_u32 v160, -1, v160\n"
"v_add_f32 v39, v39, v91\n"
"v_mfma_f32_16x16x32_bf16 v[20:23], v[100:103], a[116:119], v[20:23]\n"
"s_bfe_u32 s20, s89, 65544\ns_lshl_b32 s20, s20, 13\ns_add_u32 s20, s20, 2097152\n"
"v_add_f32 v32, v32, v206\n"
"v_mfma_f32_16x16x32_bf16 v[20:23], v[96:99], a[112:115], v[20:23]\n"
"buffer_load_dwordx4 v[132:135], v236, s[32:35], s20 offen sc1\n"
"v_mfma_f32_16x16x32_bf16 v[36:39], v[96:99], a[128:131], v[36:39]\n"
"v_lshlrev_b32 v204, 16, v92\n"
"v_add_f32 v40, v40, v204\n"
"v_mfma_f32_16x16x32_bf16 v[36:39], v[100:103], a[132:135], v[36:39]\n"
"s_bfe_u32 s20, s89, 65548\ns_lshl_b32 s20, s20, 13\ns_add_u32 s20, s20, 3145728\n"
"ds_read_b64_tr_b16 v[212:213], v234 offset:16384\n"
"v_add_f32 v30, v30, v205\n"
"v_mfma_f32_16x16x32_bf16 v[36:39], v[104:107], a[136:139], v[36:39]\n"
"buffer_load_dwordx4 v[136:139], v236, s[32:35], s20 offen sc1\n"
"v_mfma_f32_16x16x32_bf16 v[36:39], v[108:111], a[140:143], v[36:39]\n"
"v_and_b32 v92, s73, v92\n"
"v_lshlrev_b32 v206, 16, v94\n"
"v_mfma_f32_16x16x32_bf16 v[36:39], v[112:115], a[176:179], v[36:39]\n"
"s_bfe_u32 s20, s89, 65552\ns_lshl_b32 s20, s20, 13\ns_add_u32 s20, s20, 4194304\n"
"ds_read_b64_tr_b16 v[186:187], v234 offset:2048\n"
"ds_read_b64_tr_b16 v[192:193], v234 offset:8192\n"
"v_mfma_f32_16x16x32_bf16 v[36:39], v[116:119], a[180:183], v[36:39]\n"
"buffer_load_dwordx4 v[140:143], v236, s[32:35], s20 offen sc1\n"
"v_mfma_f32_16x16x32_bf16 v[36:39], v[120:123], a[184:187], v[36:39]\n"
"v_and_b32 v94, s73, v94\n"
"v_mfma_f32_16x16x32_bf16 v[36:39], v[224:227], a[188:191], v[36:39]\n"
"s_bfe_u32 s20, s89, 65556\ns_lshl_b32 s20, s20, 13\ns_add_u32 s20, s20, 5242880\n"
"v_add_f32 v34, v34, v207\n"
"v_lshlrev_b32 v205, 16, v93\n"
"v_mfma_f32_16x16x32_bf16 v[52:55], v[224:227], a[252:255], v[52:55]\n"
"buffer_load_dwordx4 v[144:147], v236, s[32:35], s20 offen sc1\n"
"v_mfma_f32_16x16x32_bf16 v[52:55], v[120:123], a[248:251], v[52:55]\n"
"v_ashrrev_i32 v160, 31, v160\n"
"v_add_f32 v44, v44, v206\n"
"v_mfma_f32_16x16x32_bf16 v[52:55], v[116:119], a[244:247], v[52:55]\n"
"s_bfe_u32 s20, s89, 65560\ns_lshl_b32 s20, s20, 13\ns_add_u32 s20, s20, 6291456\n"
"v_xor_b32 v231, s75, v231\n"
"v_lshlrev_b32 v207, 16, v95\n"
"v_mfma_f32_16x16x32_bf16 v[52:55], v[112:115], a[240:243], v[52:55]\n"
"buffer_load_dwordx4 v[148:151], v236, s[32:35], s20 offen sc1\n"
"v_mfma_f32_16x16x32_bf16 v[52:55], v[108:111], a[204:207], v[52:55]\n"
"s_bfe_u32 s20, s89, 65564\ns_lshl_b32 s20, s20, 13\ns_add_u32 s20, s20, 7340032\n"
"v_and_b32 v87, s73, v87\n"
"v_and_b32 v95, s73, v95\n"
"v_mfma_f32_16x16x32_bf16 v[52:55], v[104:107], a[200:203], v[52:55]\n"
"buffer_load_dwordx4 v[152:155], v236, s[32:35], s20 offen sc1\n"
"v_mfma_f32_16x16x32_bf16 v[52:55], v[100:103], a[196:199], v[52:55]\n"
"s_waitcnt vmcnt(8)\n"
"s_add_u32 s21, s21, 1\n"
"v_and_b32 v88, s73, v88\n"
"v_mfma_f32_16x16x32_bf16 v[52:55], v[96:99], a[192:195], v[52:55]\n"
"s_barrier\n"
"s_mov_b32 s52, 40960\n"
"buffer_store_dword v241, v248, s[84:87], 0 offen sc1\n"
"s_waitcnt lgkmcnt(0)\n"
"v_mfma_f32_16x16x32_bf16 v[8:11], v[180:183], a[96:99], v[8:11]\n"
"s_mov_b32 s53, 81920\n"
"ds_read_b64_tr_b16 v[98:99], v231 offset:2048\n"
"v_add_f32 v42, v42, v205\n"
"v_mfma_f32_16x16x32_bf16 v[8:11], v[184:187], a[100:103], v[8:11]\n"
"ds_read_b64_tr_b16 v[112:113], v231 offset:16384\n"
"v_add_f32 v31, v31, v87\n"
"v_mfma_f32_16x16x32_bf16 v[8:11], v[188:191], a[104:107], v[8:11]\n"
"v_add_f32 v29, v29, v86\n"
"v_and_b32 v161, 16, v242\n"
"v_mfma_f32_16x16x32_bf16 v[8:11], v[192:195], a[108:111], v[8:11]\n"
"ds_read_b64_tr_b16 v[118:119], v235 offset:18432\n"
"v_and_b32 v160, 0x3f80, v160\n"
"v_mfma_f32_16x16x32_bf16 v[8:11], v[208:211], a[144:147], v[8:11]\n"
"ds_read_b64_tr_b16 v[102:103], v235 offset:2048\n"
"v_add_f32 v27, v27, v85\n"
"v_mfma_f32_16x16x32_bf16 v[8:11], v[212:215], a[148:151], v[8:11]\n"
"ds_read_b64_tr_b16 v[106:107], v231 offset:10240\n"
"v_lshlrev_b32 v160, v161, v160\n"
"v_mfma_f32_16x16x32_bf16 v[8:11], v[216:219], a[152:155], v[8:11]\n"
"ds_read_b64_tr_b16 v[96:97], v231 offset:0\n"
"v_and_b32 v93, s73, v93\n"
"v_mfma_f32_16x16x32_bf16 v[8:11], v[220:223], a[156:159], v[8:11]\n"
"ds_read_b64_tr_b16 v[108:109], v235 offset:8192\n"
"v_add_f32 v41, v41, v92\n"
"v_mfma_f32_16x16x32_bf16 v[24:27], v[220:223], a[172:175], v[24:27]\n"
"v_bfe_i32 v161, v242, 5, 1\n"
"v_lshlrev_b32 v206, 16, v244\n"
"v_mfma_f32_16x16x32_bf16 v[24:27], v[216:219], a[168:171], v[24:27]\n"
"ds_read_b64_tr_b16 v[114:115], v231 offset:18432\n"
"v_mfma_f32_16x16x32_bf16 v[24:27], v[212:215], a[164:167], v[24:27]\n"
"ds_read_b64_tr_b16 v[104:105], v231 offset:8192\n"
"v_lshlrev_b32 v203, 16, v247\n"
"v_mfma_f32_16x16x32_bf16 v[24:27], v[208:211], a[160:163], v[24:27]\n"
"ds_read_b64_tr_b16 v[116:117], v235 offset:16384\n"
"v_add_f32 v48, v48, v206\n"
"v_mfma_f32_16x16x32_bf16 v[24:27], v[192:195], a[124:127], v[24:27]\n"
"ds_read_b64_tr_b16 v[110:111], v235 offset:10240\n"
"v_and_b32 v89, s73, v89\n"
"v_mfma_f32_16x16x32_bf16 v[24:27], v[188:191], a[120:123], v[24:27]\n"
"v_add_f32 v45, v45, v94\n"
"v_add_f32 v47, v47, v95\n"
"v_mfma_f32_16x16x32_bf16 v[24:27], v[184:187], a[116:119], v[24:27]\n"
"ds_read_b64_tr_b16 v[100:101], v235 offset:0\n"
"v_add_f32 v43, v43, v93\n"
"v_mfma_f32_16x16x32_bf16 v[24:27], v[180:183], a[112:115], v[24:27]\n"
"ds_read_b64_tr_b16 v[122:123], v231 offset:26624\n"
"v_add_f32 v46, v46, v207\n"
"v_mfma_f32_16x16x32_bf16 v[40:43], v[180:183], a[128:131], v[40:43]\n"
"v_add_f32 v33, v33, v88\n"
"v_and_b32 v244, s73, v244\n"
"v_mfma_f32_16x16x32_bf16 v[40:43], v[184:187], a[132:135], v[40:43]\n"
"ds_read_b64_tr_b16 v[120:121], v231 offset:24576\n"
"v_lshlrev_b32 v207, 16, v245\n"
"v_mfma_f32_16x16x32_bf16 v[40:43], v[188:191], a[136:139], v[40:43]\n"
"v_and_b32 v163, v160, v161\n"
"v_lshlrev_b32 v202, 16, v246\n"
"v_mfma_f32_16x16x32_bf16 v[40:43], v[192:195], a[140:143], v[40:43]\n"
"ds_read_b64_tr_b16 v[224:225], v235 offset:24576\n"
"v_and_b32 v246, s73, v246\n"
"v_mfma_f32_16x16x32_bf16 v[40:43], v[208:211], a[176:179], v[40:43]\n"
"v_add_f32 v52, v52, v202\n"
"v_and_b32 v247, s73, v247\n"
"v_mfma_f32_16x16x32_bf16 v[40:43], v[212:215], a[180:183], v[40:43]\n"
"ds_read_b64_tr_b16 v[226:227], v235 offset:26624\n"
"v_and_b32 v245, s73, v245\n"
"v_mfma_f32_16x16x32_bf16 v[40:43], v[216:219], a[184:187], v[40:43]\n"
"v_mov_b32 v161, 0\n"
"v_add_f32 v54, v54, v203\n"
"v_mfma_f32_16x16x32_bf16 v[40:43], v[220:223], a[188:191], v[40:43]\n"
"s_barrier\n"
"s_waitcnt lgkmcnt(3)\n"
"v_mfma_f32_16x16x32_bf16 v[12:15], v[96:99], a[96:99], v[12:15]\n"
"ds_read_b64_tr_b16 v[184:185], v232 offset:32768\n"
"v_add_f32 v49, v49, v244\n"
"v_mfma_f32_16x16x32_bf16 v[12:15], v[100:103], a[100:103], v[12:15]\n"
"ds_read_b64_tr_b16 v[216:217], v228 offset:57344\n"
"v_mfma_f32_16x16x32_bf16 v[12:15], v[104:107], a[104:107], v[12:15]\n"
"ds_read_b64_tr_b16 v[188:189], v228 offset:40960\n"
"v_add_f32 v35, v35, v89\n"
"v_mfma_f32_16x16x32_bf16 v[12:15], v[108:111], a[108:111], v[12:15]\n"
"ds_read_b64_tr_b16 v[190:191], v228 offset:43008\n"
"v_mfma_f32_16x16x32_bf16 v[12:15], v[112:115], a[144:147], v[12:15]\n"
"ds_read_b64_tr_b16 v[192:193], v232 offset:40960\n"
"v_add_f32 v53, v53, v246\n"
"v_mfma_f32_16x16x32_bf16 v[12:15], v[116:119], a[148:151], v[12:15]\n"
"s_waitcnt lgkmcnt(5)\n"
"ds_read_b64_tr_b16 v[212:213], v232 offset:49152\n"
"v_add_f32 v51, v51, v245\n"
"v_mfma_f32_16x16x32_bf16 v[12:15], v[120:123], a[152:155], v[12:15]\n"
"ds_read_b64_tr_b16 v[182:183], v228 offset:34816\n"
"v_xor_b32 v162, v160, v163\n"
"v_mfma_f32_16x16x32_bf16 v[12:15], v[224:227], a[156:159], v[12:15]\n"
"global_load_dword v200, v201, s[6:7] sc1\n"
"v_mfma_f32_16x16x32_bf16 v[28:31], v[224:227], a[172:175], v[28:31]\n"
"v_add_f32 v50, v50, v207\n"
"v_add_f32 v55, v55, v247\n"
"v_mfma_f32_16x16x32_bf16 v[28:31], v[120:123], a[168:171], v[28:31]\n"
"s_waitcnt vmcnt(9)\n"
"v_lshlrev_b32 v58, 16, v125\n"
"v_lshlrev_b32 v62, 16, v127\n"
"v_mfma_f32_16x16x32_bf16 v[28:31], v[116:119], a[164:167], v[28:31]\n"
"ds_read_b64_tr_b16 v[186:187], v232 offset:34816\n"
"ds_read_b64_tr_b16 v[214:215], v232 offset:51200\n"
"v_mfma_f32_16x16x32_bf16 v[28:31], v[112:115], a[160:163], v[28:31]\n"
"ds_read_b64_tr_b16 v[194:195], v232 offset:43008\n"
"v_and_b32 v61, s73, v126\n"
"v_mfma_f32_16x16x32_bf16 v[28:31], v[108:111], a[124:127], v[28:31]\n"
"s_waitcnt vmcnt(8)\n"
"ds_read_b64_tr_b16 v[210:211], v228 offset:51200\n"
"v_and_b32 v57, s73, v124\n"
"v_mfma_f32_16x16x32_bf16 v[28:31], v[104:107], a[120:123], v[28:31]\n"
"ds_read_b64_tr_b16 v[222:223], v232 offset:59392\n"
"v_lshlrev_b32 v56, 16, v124\n"
"v_mfma_f32_16x16x32_bf16 v[28:31], v[100:103], a[116:119], v[28:31]\n"
"ds_read_b64_tr_b16 v[180:181], v228 offset:32768\n"
"v_lshlrev_b32 v205, 16, v129\n"
"v_mfma_f32_16x16x32_bf16 v[28:31], v[96:99], a[112:115], v[28:31]\n"
"s_waitcnt vmcnt(7)\n"
"v_and_b32 v59, s73, v125\n"
"v_lshlrev_b32 v206, 16, v130\n"
"v_mfma_f32_16x16x32_bf16 v[44:47], v[96:99], a[128:131], v[44:47]\n"
"ds_read_b64_tr_b16 v[220:221], v232 offset:57344\n"
"v_lshlrev_b32 v204, 16, v128\n"
"v_mfma_f32_16x16x32_bf16 v[44:47], v[100:103], a[132:135], v[44:47]\n"
"ds_read_b64_tr_b16 v[208:209], v228 offset:49152\n"
"v_and_b32 v130, s73, v130\n"
"v_mfma_f32_16x16x32_bf16 v[44:47], v[104:107], a[136:139], v[44:47]\n"
"s_waitcnt vmcnt(6)\n"
"ds_read_b64_tr_b16 v[102:103], v233 offset:34816\n"
"v_add_f32 v58, v58, v205\n"
"v_mfma_f32_16x16x32_bf16 v[44:47], v[108:111], a[140:143], v[44:47]\n"
"v_lshlrev_b32 v207, 16, v131\n"
"v_add_f32 v62, v62, v207\n"
"v_mfma_f32_16x16x32_bf16 v[44:47], v[112:115], a[176:179], v[44:47]\n"
"ds_read_b64_tr_b16 v[218:219], v228 offset:59392\n"
"v_and_b32 v131, s73, v131\n"
"v_mfma_f32_16x16x32_bf16 v[44:47], v[116:119], a[180:183], v[44:47]\n"
"s_waitcnt vmcnt(5)\n"
"ds_read_b64_tr_b16 v[96:97], v229 offset:32768\n"
"v_lshlrev_b32 v205, 16, v135\n"
"v_mfma_f32_16x16x32_bf16 v[44:47], v[120:123], a[184:187], v[44:47]\n"
"v_lshlrev_b32 v202, 16, v132\n"
"v_lshlrev_b32 v207, 16, v137\n"
"v_mfma_f32_16x16x32_bf16 v[44:47], v[224:227], a[188:191], v[44:47]\n"
"s_waitcnt lgkmcnt(5)\n"
"ds_read_b64_tr_b16 v[120:121], v229 offset:57344\n"
"v_add_f32 v56, v56, v204\n"
"v_mfma_f32_16x16x32_bf16 v[0:3], v[180:183], a[48:51], v[0:3]\n"
"s_waitcnt vmcnt(4)\n"
"ds_read_b64_tr_b16 v[116:117], v233 offset:49152\n"
"v_add_f32 v61, v61, v130\n"
"v_mfma_f32_16x16x32_bf16 v[0:3], v[184:187], a[52:55], v[0:3]\n"
"v_lshlrev_b32 v60, 16, v126\n"
"v_lshlrev_b32 v204, 16, v134\n"
"v_mfma_f32_16x16x32_bf16 v[0:3], v[188:191], a[56:59], v[0:3]\n"
"ds_read_b64_tr_b16 v[110:111], v233 offset:43008\n"
"v_add_f32 v56, v56, v202\n"
"v_mfma_f32_16x16x32_bf16 v[0:3], v[192:195], a[60:63], v[0:3]\n"
"s_waitcnt lgkmcnt(5)\n"
"s_waitcnt vmcnt(3)\n"
"ds_read_b64_tr_b16 v[112:113], v229 offset:49152\n"
"v_and_b32 v63, s73, v127\n"
"v_mfma_f32_16x16x32_bf16 v[0:3], v[208:211], a[0:3], v[0:3]\n"
"ds_read_b64_tr_b16 v[224:225], v233 offset:57344\n"
"v_and_b32 v134, s73, v134\n"
"v_mfma_f32_16x16x32_bf16 v[0:3], v[212:215], a[4:7], v[0:3]\n"
"s_waitcnt lgkmcnt(5)\n"
"s_waitcnt vmcnt(2)\n"
"ds_read_b64_tr_b16 v[108:109], v233 offset:40960\n"
"v_and_b32 v129, s73, v129\n"
"v_mfma_f32_16x16x32_bf16 v[0:3], v[216:219], a[8:11], v[0:3]\n"
"v_add_f32 v60, v60, v206\n"
"v_add_f32 v60, v60, v204\n"
"v_mfma_f32_16x16x32_bf16 v[0:3], v[220:223], a[12:15], v[0:3]\n"
"ds_read_b64_tr_b16 v[98:99], v229 offset:34816\n"
"ds_read_b64_tr_b16 v[122:123], v229 offset:59392\n"
"v_mfma_f32_16x16x32_bf16 v[16:19], v[220:223], a[28:31], v[16:19]\n"
"v_mov_b32 v165, 0\n"
"v_add_f32 v59, v59, v129\n"
"v_mfma_f32_16x16x32_bf16 v[16:19], v[216:219], a[24:27], v[16:19]\n"
"v_add_f32 v62, v62, v205\n"
"v_lshlrev_b32 v206, 16, v136\n"
"v_mfma_f32_16x16x32_bf16 v[16:19], v[212:215], a[20:23], v[16:19]\n"
"ds_read_b64_tr_b16 v[104:105], v229 offset:40960\n"
"ds_read_b64_tr_b16 v[226:227], v233 offset:59392\n"
"v_mfma_f32_16x16x32_bf16 v[16:19], v[208:211], a[16:19], v[16:19]\n"
"v_add_f32 v56, v56, v206\n"
"v_lshlrev_b32 v202, 16, v138\n"
"v_mfma_f32_16x16x32_bf16 v[16:19], v[192:195], a[76:79], v[16:19]\n"
"s_waitcnt vmcnt(0)\n"
"ds_read_b64_tr_b16 v[118:119], v233 offset:51200\n"
"v_subrev_f32 v2, s22, v2\n"
"v_mfma_f32_16x16x32_bf16 v[16:19], v[188:191], a[72:75], v[16:19]\n"
"v_subrev_u32 v200, s21, v200\n"
"ds_read_b64_tr_b16 v[114:115], v229 offset:51200\n"
"v_mfma_f32_16x16x32_bf16 v[16:19], v[184:187], a[68:71], v[16:19]\n"
"v_cmp_lt_u32 vcc, s69, v200\n"
"ds_read_b64_tr_b16 v[100:101], v233 offset:32768\n"
"v_mfma_f32_16x16x32_bf16 v[16:19], v[180:183], a[64:67], v[16:19]\n"
"v_lshlrev_b32 v206, 16, v142\n"
"s_cbranch_vccnz LGR_%=\nLGRK_%=:\n"
"v_mfma_f32_16x16x32_bf16 v[32:35], v[180:183], a[80:83], v[32:35]\n"
"v_subrev_f32 v3, s22, v3\n"
"s_cmp_eq_u32 s93, 0\ns_cbranch_scc1 LEP_%=\nLEPK_%=:\n"
"v_mfma_f32_16x16x32_bf16 v[32:35], v[184:187], a[84:87], v[32:35]\n"
"s_bfe_u32 s20, s89, 65536\ns_lshl_b32 s20, s20, 13\ns_add_u32 s20, s20, 65536\n"
"ds_read_b64_tr_b16 v[106:107], v229 offset:43008\n"
"v_and_b32 v128, s73, v128\n"
"v_mfma_f32_16x16x32_bf16 v[32:35], v[188:191], a[88:91], v[32:35]\n"
"buffer_load_dwordx4 v[72:75], v236, s[32:35], s20 offen sc1\n"
"v_mfma_f32_16x16x32_bf16 v[32:35], v[192:195], a[92:95], v[32:35]\n"
"v_lshlrev_b32 v203, 16, v133\n"
"v_lshlrev_b32 v205, 16, v141\n"
"v_mfma_f32_16x16x32_bf16 v[32:35], v[208:211], a[32:35], v[32:35]\n"
"s_bfe_u32 s20, s89, 65540\ns_lshl_b32 s20, s20, 13\ns_add_u32 s20, s20, 1114112\n"
"v_lshlrev_b32 v204, 16, v140\n"
"v_add_f32 v56, v56, v204\n"
"v_mfma_f32_16x16x32_bf16 v[32:35], v[212:215], a[36:39], v[32:35]\n"
"buffer_load_dwordx4 v[76:79], v236, s[32:35], s20 offen sc1\n"
"v_mfma_f32_16x16x32_bf16 v[32:35], v[216:219], a[40:43], v[32:35]\n"
"v_and_b32 v136, s73, v136\n"
"v_subrev_f32 v17, s22, v17\n"
"v_mfma_f32_16x16x32_bf16 v[32:35], v[220:223], a[44:47], v[32:35]\n"
"s_bfe_u32 s20, s89, 65544\ns_lshl_b32 s20, s20, 13\ns_add_u32 s20, s20, 2162688\n"
"v_add_f32 v61, v61, v134\n"
"v_and_b32 v141, s73, v141\n"
"v_mfma_f32_16x16x32_bf16 v[48:51], v[220:223], a[228:231], v[48:51]\n"
"buffer_load_dwordx4 v[80:83], v236, s[32:35], s20 offen sc1\n"
"v_mfma_f32_16x16x32_bf16 v[48:51], v[216:219], a[224:227], v[48:51]\n"
"v_add_f32 v58, v58, v203\n"
"v_subrev_f32 v19, s22, v19\n"
"v_mfma_f32_16x16x32_bf16 v[48:51], v[212:215], a[212:215], v[48:51]\n"
"s_bfe_u32 s20, s89, 65548\ns_lshl_b32 s20, s20, 13\ns_add_u32 s20, s20, 3211264\n"
"v_and_b32 v140, s73, v140\n"
"v_lshlrev_b32 v204, 16, v146\n"
"v_mfma_f32_16x16x32_bf16 v[48:51], v[208:211], a[208:211], v[48:51]\n"
"buffer_load_dwordx4 v[84:87], v236, s[32:35], s20 offen sc1\n"
"v_mfma_f32_16x16x32_bf16 v[48:51], v[192:195], a[236:239], v[48:51]\n"
"v_add_f32 v57, v57, v128\n"
"v_subrev_f32 v34, s22, v34\n"
"v_mfma_f32_16x16x32_bf16 v[48:51], v[188:191], a[232:235], v[48:51]\n"
"s_bfe_u32 s20, s89, 65552\ns_lshl_b32 s20, s20, 13\ns_add_u32 s20, s20, 4259840\n"
"ds_read_b64_tr_b16 v[214:215], v234 offset:51200\n"
"v_and_b32 v132, s73, v132\n"
"v_mfma_f32_16x16x32_bf16 v[48:51], v[184:187], a[220:223], v[48:51]\n"
"buffer_load_dwordx4 v[88:91], v236, s[32:35], s20 offen sc1\n"
"v_mfma_f32_16x16x32_bf16 v[48:51], v[180:183], a[216:219], v[48:51]\n"
"s_waitcnt lgkmcnt(1)\n"
"v_add_f32 v60, v60, v202\n"
"v_add_f32 v60, v60, v206\n"
"v_mfma_f32_16x16x32_bf16 v[4:7], v[96:99], a[48:51], v[4:7]\n"
"s_bfe_u32 s20, s89, 65556\ns_lshl_b32 s20, s20, 13\ns_add_u32 s20, s20, 5308416\n"
"v_lshlrev_b32 v206, 16, v148\n"
"v_subrev_f32 v35, s22, v35\n"
"v_mfma_f32_16x16x32_bf16 v[4:7], v[100:103], a[52:55], v[4:7]\n"
"buffer_load_dwordx4 v[92:95], v236, s[32:35], s20 offen sc1\n"
"v_mfma_f32_16x16x32_bf16 v[4:7], v[104:107], a[56:59], v[4:7]\n"
"v_add_f32 v60, v60, v204\n"
"v_subrev_f32 v0, s22, v0\n"
"v_mfma_f32_16x16x32_bf16 v[4:7], v[108:111], a[60:63], v[4:7]\n"
"s_bfe_u32 s20, s89, 65560\ns_lshl_b32 s20, s20, 13\ns_add_u32 s20, s20, 6356992\n"
"v_add_f32 v57, v57, v132\n"
"v_subrev_f32 v33, s22, v33\n"
"v_mfma_f32_16x16x32_bf16 v[4:7], v[112:115], a[0:3], v[4:7]\n"
"buffer_load_dwordx4 v[244:247], v236, s[32:35], s20 offen sc1\n"
"v_mfma_f32_16x16x32_bf16 v[4:7], v[116:119], a[4:7], v[4:7]\n"
"v_and_b32 v138, s73, v138\n"
"v_subrev_f32 v18, s22, v18\n"
"v_mfma_f32_16x16x32_bf16 v[4:7], v[120:123], a[8:11], v[4:7]\n"
"s_bfe_u32 s20, s89, 65564\ns_lshl_b32 s20, s20, 13\ns_add_u32 s20, s20, 7405568\n"
"v_and_b32 v135, s73, v135\n"
"v_subrev_f32 v49, s22, v49\n"
"v_mfma_f32_16x16x32_bf16 v[4:7], v[224:227], a[12:15], v[4:7]\n"
"buffer_load_dwordx4 v[156:159], v236, s[32:35], s20 offen sc1\n"
"v_add_f32 v58, v58, v207\n"
"v_lshlrev_b32 v203, 16, v139\n"
"v_add_f32 v58, v58, v205\n"
"v_and_b32 v142, s73, v142\n"
"v_lshlrev_b32 v202, 16, v144\n"
"v_add_f32 v56, v56, v202\n"
"v_add_f32 v56, v56, v206\n"
"v_subrev_f32 v1, s22, v1\n"
"v_mfma_f32_16x16x32_bf16 v[20:23], v[224:227], a[28:31], v[20:23]\n"
"v_add_f32 v63, v63, v131\n"
"v_lshlrev_b32 v204, 16, v152\n"
"v_mfma_f32_16x16x32_bf16 v[20:23], v[120:123], a[24:27], v[20:23]\n"
"v_and_b32 v133, s73, v133\n"
"v_add_f32 v59, v59, v133\n"
"v_mfma_f32_16x16x32_bf16 v[20:23], v[116:119], a[20:23], v[20:23]\n"
"ds_read_b64_tr_b16 v[218:219], v230 offset:59392\n"
"v_and_b32 v137, s73, v137\n"
"v_mfma_f32_16x16x32_bf16 v[20:23], v[112:115], a[16:19], v[20:23]\n"
"v_and_b32 v148, s73, v148\n"
"v_subrev_f32 v16, s22, v16\n"
"v_mfma_f32_16x16x32_bf16 v[20:23], v[108:111], a[76:79], v[20:23]\n"
"v_add_f32 v62, v62, v203\n"
"v_subrev_f32 v6, s22, v6\n"
"v_mfma_f32_16x16x32_bf16 v[20:23], v[104:107], a[72:75], v[20:23]\n"
"v_add_f32 v57, v57, v136\n"
"v_lshlrev_b32 v205, 16, v147\n"
"v_mfma_f32_16x16x32_bf16 v[20:23], v[100:103], a[68:71], v[20:23]\n"
"ds_read_b64_tr_b16 v[194:195], v234 offset:43008\n"
"v_subrev_f32 v32, s22, v32\n"
"v_mfma_f32_16x16x32_bf16 v[20:23], v[96:99], a[64:67], v[20:23]\n"
"ds_read_b64_tr_b16 v[184:185], v234 offset:32768\n"
"v_add_f32 v59, v59, v137\n"
"v_and_b32 v139, s73, v139\n"
"v_add_f32 v59, v59, v141\n"
"v_lshlrev_b32 v207, 16, v143\n"
"v_lshlrev_b32 v203, 16, v145\n"
"v_and_b32 v145, s73, v145\n"
"v_add_f32 v58, v58, v203\n"
"v_add_f32 v59, v59, v145\n"
"v_and_b32 v147, s73, v147\n"
"v_lshlrev_b32 v203, 16, v151\n"
"v_subrev_f32 v4, s22, v4\n"
"v_subrev_f32 v5, s22, v5\n"
"v_subrev_f32 v7, s22, v7\n"
"v_cvt_pk_bf16_f32 v0, v0, v1\n"
"v_cvt_pk_bf16_f32 v1, v2, v3\n"
"v_cvt_pk_bf16_f32 v3, v6, v7\n"
"v_mfma_f32_16x16x32_bf16 v[36:39], v[96:99], a[80:83], v[36:39]\n"
"v_add_f32 v56, v56, v204\n"
"v_cvt_pk_bf16_f32 v2, v4, v5\n"
"v_mfma_f32_16x16x32_bf16 v[36:39], v[100:103], a[84:87], v[36:39]\n"
"ds_read_b64_tr_b16 v[192:193], v234 offset:40960\n"
"v_add_f32 v61, v61, v138\n"
"v_mfma_f32_16x16x32_bf16 v[36:39], v[104:107], a[88:91], v[36:39]\n"
"v_add_f32 v57, v57, v140\n"
"v_subrev_f32 v23, s22, v23\n"
"v_mfma_f32_16x16x32_bf16 v[36:39], v[108:111], a[92:95], v[36:39]\n"
"ds_read_b64_tr_b16 v[180:181], v230 offset:32768\n"
"v_and_b32 v144, s73, v144\n"
"v_mfma_f32_16x16x32_bf16 v[36:39], v[112:115], a[32:35], v[36:39]\n"
"ds_read_b64_tr_b16 v[210:211], v230 offset:51200\n"
"v_add_f32 v62, v62, v207\n"
"v_add_f32 v62, v62, v205\n"
"v_lshlrev_b32 v207, 16, v149\n"
"v_add_f32 v58, v58, v207\n"
"v_lshlrev_b32 v202, 16, v150\n"
"v_permlane16_swap_b32 v0, v2\n"
"v_permlane16_swap_b32 v1, v3\n"
"v_subrev_f32 v20, s22, v20\n"
"v_cvt_pk_bf16_f32 v16, v16, v17\n"
"v_mfma_f32_16x16x32_bf16 v[36:39], v[116:119], a[36:39], v[36:39]\n"
"ds_read_b64_tr_b16 v[186:187], v234 offset:34816\n"
"v_add_f32 v63, v63, v135\n"
"v_mfma_f32_16x16x32_bf16 v[36:39], v[120:123], a[40:43], v[36:39]\n"
"ds_read_b64_tr_b16 v[188:189], v230 offset:40960\n"
"buffer_store_dwordx4 v[0:3], v238, s[36:39], 0 offen sc0 sc1\n"
"v_and_b32 v143, s73, v143\n"
"v_and_b32 v146, s73, v146\n"
"v_and_b32 v149, s73, v149\n"
"v_lshlrev_b32 v205, 16, v153\n"
"v_and_b32 v153, s73, v153\n"
"v_lshlrev_b32 v206, 16, v154\n"
"v_lshlrev_b32 v207, 16, v155\n"
"v_subrev_f32 v21, s22, v21\n"
"v_subrev_f32 v22, s22, v22\n"
"v_cvt_pk_bf16_f32 v17, v18, v19\n"
"v_cvt_pk_bf16_f32 v18, v20, v21\n"
"v_cvt_pk_bf16_f32 v32, v32, v33\n"
"v_mfma_f32_16x16x32_bf16 v[36:39], v[224:227], a[44:47], v[36:39]\n"
"ds_read_b64_tr_b16 v[182:183], v230 offset:34816\n"
"v_add_f32 v63, v63, v139\n"
"v_add_f32 v61, v61, v142\n"
"v_add_f32 v57, v57, v144\n"
"v_add_f32 v57, v57, v148\n"
"v_add_f32 v59, v59, v149\n"
"v_cvt_pk_bf16_f32 v19, v22, v23\n"
"v_permlane16_swap_b32 v16, v18\n"
"v_cvt_pk_bf16_f32 v33, v34, v35\n"
"v_mfma_f32_16x16x32_bf16 v[52:55], v[224:227], a[228:231], v[52:55]\n"
"ds_read_b64_tr_b16 v[190:191], v230 offset:43008\n"
"v_add_f32 v59, v59, v153\n"
"v_mfma_f32_16x16x32_bf16 v[52:55], v[120:123], a[224:227], v[52:55]\n"
"v_permlane16_swap_b32 v17, v19\n"
"v_mfma_f32_16x16x32_bf16 v[52:55], v[116:119], a[212:215], v[52:55]\n"
"ds_read_b64_tr_b16 v[208:209], v230 offset:49152\n"
"v_subrev_f32 v36, s22, v36\n"
"v_mfma_f32_16x16x32_bf16 v[52:55], v[112:115], a[208:211], v[52:55]\n"
"ds_read_b64_tr_b16 v[222:223], v234 offset:59392\n"
"buffer_store_dwordx4 v[16:19], v238, s[36:39], s52 offen sc0 sc1\n"
"v_add_f32 v63, v63, v143\n"
"v_add_f32 v60, v60, v202\n"
"v_add_f32 v62, v62, v203\n"
"v_and_b32 v152, s73, v152\n"
"v_subrev_f32 v38, s22, v38\n"
"v_subrev_f32 v39, s22, v39\n"
"v_mfma_f32_16x16x32_bf16 v[52:55], v[108:111], a[236:239], v[52:55]\n"
"v_and_b32 v155, s73, v155\n"
"v_cvt_pk_bf16_f32 v35, v38, v39\n"
"v_mfma_f32_16x16x32_bf16 v[52:55], v[104:107], a[232:235], v[52:55]\n"
"ds_read_b64_tr_b16 v[216:217], v230 offset:57344\n"
"v_subrev_f32 v37, s22, v37\n"
"v_mfma_f32_16x16x32_bf16 v[52:55], v[100:103], a[220:223], v[52:55]\n"
"v_and_b32 v150, s73, v150\n"
"v_and_b32 v154, s73, v154\n"
"v_mfma_f32_16x16x32_bf16 v[52:55], v[96:99], a[216:219], v[52:55]\n"
"s_waitcnt lgkmcnt(4)\n"
"ds_read_b64_tr_b16 v[212:213], v234 offset:49152\n"
"ds_read_b64_tr_b16 v[220:221], v234 offset:57344\n"
"v_add_f32 v63, v63, v147\n"
"v_and_b32 v151, s73, v151\n"
"v_add_f32 v63, v63, v151\n"
"v_add_f32 v57, v57, v152\n"
"v_add_f32 v58, v58, v205\n"
"v_add_f32 v60, v60, v206\n"
"v_add_f32 v62, v62, v207\n"
"v_cvt_pk_bf16_f32 v34, v36, v37\n"
"v_permlane16_swap_b32 v33, v35\n"
"v_mfma_f32_16x16x32_bf16 v[8:11], v[180:183], a[48:51], v[8:11]\n"
"v_cvt_pk_bf16_f32 v56, v56, v57\n"
"v_cvt_pk_bf16_f32 v57, v58, v59\n"
"v_mfma_f32_16x16x32_bf16 v[8:11], v[184:187], a[52:55], v[8:11]\n"
"s_waitcnt lgkmcnt(5)\n"
"v_permlane16_swap_b32 v32, v34\n"
"v_mfma_f32_16x16x32_bf16 v[8:11], v[188:191], a[56:59], v[8:11]\n"
"buffer_store_dwordx4 v[32:35], v238, s[36:39], s53 offen sc0 sc1\n"
"ds_read_b64_tr_b16 v[120:121], v231 offset:57344\n"
"v_add_f32 v61, v61, v146\n"
"v_add_f32 v61, v61, v150\n"
"v_add_f32 v61, v61, v154\n"
"v_add_f32 v63, v63, v155\n"
"v_cvt_pk_bf16_f32 v58, v60, v61\n"
"v_cvt_pk_bf16_f32 v59, v62, v63\n"
"v_mfma_f32_16x16x32_bf16 v[8:11], v[192:195], a[60:63], v[8:11]\n"
"s_waitcnt lgkmcnt(4)\n"
"v_permlane16_swap_b32 v57, v59\n"
"v_mfma_f32_16x16x32_bf16 v[8:11], v[208:211], a[0:3], v[8:11]\n"
"s_waitcnt lgkmcnt(2)\n"
"v_permlane16_swap_b32 v56, v58\n"
"v_mfma_f32_16x16x32_bf16 v[8:11], v[212:215], a[4:7], v[8:11]\n"
"buffer_store_dwordx4 v[56:59], v253, s[36:39], 0 offen sc0 sc1\n"
"v_subrev_f32 v48, s22, v48\n"
"v_subrev_f32 v50, s22, v50\n"
"v_subrev_f32 v51, s22, v51\n"
"v_mfma_f32_16x16x32_bf16 v[8:11], v[216:219], a[8:11], v[8:11]\n"
"s_waitcnt lgkmcnt(1)\n"
"ds_read_b64_tr_b16 v[112:113], v231 offset:49152\n"
"v_subrev_f32 v54, s22, v54\n"
"v_mfma_f32_16x16x32_bf16 v[8:11], v[220:223], a[12:15], v[8:11]\n"
"ds_read_b64_tr_b16 v[100:101], v235 offset:32768\n"
"v_subrev_f32 v52, s22, v52\n"
"v_mfma_f32_16x16x32_bf16 v[24:27], v[220:223], a[28:31], v[24:27]\n"
"ds_read_b64_tr_b16 v[102:103], v235 offset:34816\n"
"v_subrev_f32 v53, s22, v53\n"
"v_mfma_f32_16x16x32_bf16 v[24:27], v[216:219], a[24:27], v[24:27]\n"
"v_cvt_pk_bf16_f32 v48, v48, v49\n"
"v_cvt_pk_bf16_f32 v49, v50, v51\n"
"v_mfma_f32_16x16x32_bf16 v[24:27], v[212:215], a[20:23], v[24:27]\n"
"ds_read_b64_tr_b16 v[96:97], v231 offset:32768\n"
"v_cvt_pk_bf16_f32 v50, v52, v53\n"
"v_mfma_f32_16x16x32_bf16 v[24:27], v[208:211], a[16:19], v[24:27]\n"
"s_nop 0\n"
"v_permlane16_swap_b32 v48, v50\n"
"v_mfma_f32_16x16x32_bf16 v[24:27], v[192:195], a[76:79], v[24:27]\n"
"ds_read_b64_tr_b16 v[116:117], v235 offset:49152\n"
"v_subrev_f32 v55, s22, v55\n"
"v_mfma_f32_16x16x32_bf16 v[24:27], v[188:191], a[72:75], v[24:27]\n"
"ds_read_b64_tr_b16 v[98:99], v231 offset:34816\n"
"v_cvt_pk_bf16_f32 v51, v54, v55\n"
"v_mfma_f32_16x16x32_bf16 v[24:27], v[184:187], a[68:71], v[24:27]\n"
"s_nop 0\n"
"v_permlane16_swap_b32 v49, v51\n"
"v_mfma_f32_16x16x32_bf16 v[24:27], v[180:183], a[64:67], v[24:27]\n"
"buffer_store_dwordx4 v[48:51], v252, s[36:39], 0 offen sc0 sc1\n"
"ds_read_b64_tr_b16 v[104:105], v231 offset:40960\n"
"v_mfma_f32_16x16x32_bf16 v[40:43], v[180:183], a[80:83], v[40:43]\n"
"ds_read_b64_tr_b16 v[106:107], v231 offset:43008\n"
"ds_read_b64_tr_b16 v[108:109], v235 offset:40960\n"
"v_mfma_f32_16x16x32_bf16 v[40:43], v[184:187], a[84:87], v[40:43]\n"
"ds_read_b64_tr_b16 v[110:111], v235 offset:43008\n"
"v_mfma_f32_16x16x32_bf16 v[40:43], v[188:191], a[88:91], v[40:43]\n"
"ds_read_b64_tr_b16 v[114:115], v231 offset:51200\n"
"v_subrev_f32 v10, s22, v10\n"
"v_mfma_f32_16x16x32_bf16 v[40:43], v[192:195], a[92:95], v[40:43]\n"
"ds_read_b64_tr_b16 v[118:119], v235 offset:51200\n"
"v_mfma_f32_16x16x32_bf16 v[40:43], v[208:211], a[32:35], v[40:43]\n"
"ds_read_b64_tr_b16 v[122:123], v231 offset:59392\n"
"v_subrev_f32 v24, s22, v24\n"
"v_mfma_f32_16x16x32_bf16 v[40:43], v[212:215], a[36:39], v[40:43]\n"
"ds_read_b64_tr_b16 v[224:225], v235 offset:57344\n"
"ds_read_b64_tr_b16 v[226:227], v235 offset:59392\n"
"v_mfma_f32_16x16x32_bf16 v[40:43], v[216:219], a[40:43], v[40:43]\n"
"v_mfma_f32_16x16x32_bf16 v[40:43], v[220:223], a[44:47], v[40:43]\n"
"s_waitcnt lgkmcnt(8)\n"
"v_mov_b32 v160, 0\n"
"v_subrev_f32 v11, s22, v11\n"
"v_mfma_f32_16x16x32_bf16 v[12:15], v[96:99], a[48:51], v[12:15]\n"
"v_subrev_f32 v8, s22, v8\n"
"v_subrev_f32 v9, s22, v9\n"
"v_mfma_f32_16x16x32_bf16 v[12:15], v[100:103], a[52:55], v[12:15]\n"
"s_waitcnt lgkmcnt(5)\n"
"v_cvt_pk_bf16_f32 v8, v8, v9\n"
"v_mfma_f32_16x16x32_bf16 v[12:15], v[104:107], a[56:59], v[12:15]\n"
"v_subrev_f32 v27, s22, v27\n"
"v_mfma_f32_16x16x32_bf16 v[12:15], v[108:111], a[60:63], v[12:15]\n"
"s_waitcnt lgkmcnt(3)\n"
"v_mfma_f32_16x16x32_bf16 v[12:15], v[112:115], a[0:3], v[12:15]\n"
"v_mfma_f32_16x16x32_bf16 v[12:15], v[116:119], a[4:7], v[12:15]\n"
"s_waitcnt lgkmcnt(0)\n"
"v_mfma_f32_16x16x32_bf16 v[12:15], v[120:123], a[8:11], v[12:15]\n"
"v_subrev_f32 v40, s22, v40\n"
"v_mfma_f32_16x16x32_bf16 v[12:15], v[224:227], a[12:15], v[12:15]\n"
"s_waitcnt vmcnt(5)\n"
"v_mfma_f32_16x16x32_bf16 v[64:67], v[162:165], v[72:75], 0\n"
"v_mfma_f32_16x16x32_bf16 v[64:67], v[162:165], v[76:79], v[64:67]\n"
"v_cvt_pk_bf16_f32 v9, v10, v11\n"
"v_mfma_f32_16x16x32_bf16 v[64:67], v[162:165], v[80:83], v[64:67]\n"
"s_nop 2\n"
"v_subrev_f32 v12, s22, v12\n"
"v_mfma_f32_16x16x32_bf16 v[64:67], v[162:165], v[84:87], v[64:67]\n"
"v_subrev_f32 v25, s22, v25\n"
"v_mfma_f32_16x16x32_bf16 v[64:67], v[162:165], v[88:91], v[64:67]\n"
"v_subrev_f32 v13, s22, v13\n"
"v_mfma_f32_16x16x32_bf16 v[64:67], v[162:165], v[92:95], v[64:67]\n"
"v_subrev_f32 v15, s22, v15\n"
"v_mfma_f32_16x16x32_bf16 v[64:67], v[162:165], v[244:247], v[64:67]\n"
"v_subrev_f32 v14, s22, v14\n"
"v_mfma_f32_16x16x32_bf16 v[64:67], v[162:165], v[156:159], v[64:67]\n"
"v_subrev_f32 v42, s22, v42\n"
"v_mfma_f32_16x16x32_bf16 v[68:71], v[160:163], v[72:75], 0\n"
"v_cvt_pk_bf16_f32 v11, v14, v15\n"
"v_subrev_f32 v41, s22, v41\n"
"v_mfma_f32_16x16x32_bf16 v[68:71], v[160:163], v[76:79], v[68:71]\n"
"v_cvt_pk_bf16_f32 v10, v12, v13\n"
"v_cvt_pk_bf16_f32 v24, v24, v25\n"
"v_mfma_f32_16x16x32_bf16 v[68:71], v[160:163], v[80:83], v[68:71]\n"
"v_mfma_f32_16x16x32_bf16 v[68:71], v[160:163], v[84:87], v[68:71]\n"
"v_permlane16_swap_b32 v9, v11\n"
"v_mfma_f32_16x16x32_bf16 v[68:71], v[160:163], v[88:91], v[68:71]\n"
"v_mfma_f32_16x16x32_bf16 v[68:71], v[160:163], v[92:95], v[68:71]\n"
"v_cvt_pk_bf16_f32 v64, v64, v65\n"
"v_cvt_pk_bf16_f32 v40, v40, v41\n"
"v_mfma_f32_16x16x32_bf16 v[68:71], v[160:163], v[244:247], v[68:71]\n"
"v_permlane16_swap_b32 v8, v10\n"
"v_mfma_f32_16x16x32_bf16 v[68:71], v[160:163], v[156:159], v[68:71]\n"
"buffer_store_dwordx4 v[8:11], v239, s[36:39], 0 offen sc0 sc1\n"
"v_mfma_f32_16x16x32_bf16 v[28:31], v[224:227], a[28:31], v[28:31]\n"
"v_mfma_f32_16x16x32_bf16 v[28:31], v[120:123], a[24:27], v[28:31]\n"
"v_cvt_pk_bf16_f32 v65, v66, v67\n"
"v_subrev_f32 v43, s22, v43\n"
"v_mfma_f32_16x16x32_bf16 v[28:31], v[116:119], a[20:23], v[28:31]\n"
"v_mfma_f32_16x16x32_bf16 v[28:31], v[112:115], a[16:19], v[28:31]\n"
"v_mfma_f32_16x16x32_bf16 v[28:31], v[108:111], a[76:79], v[28:31]\n"
"v_cvt_pk_bf16_f32 v66, v68, v69\n"
"v_cvt_pk_bf16_f32 v67, v70, v71\n"
"v_mfma_f32_16x16x32_bf16 v[28:31], v[104:107], a[72:75], v[28:31]\n"
"v_permlane16_swap_b32 v64, v66\n"
"v_mfma_f32_16x16x32_bf16 v[28:31], v[100:103], a[68:71], v[28:31]\n"
"v_permlane16_swap_b32 v65, v67\n"
"v_mfma_f32_16x16x32_bf16 v[28:31], v[96:99], a[64:67], v[28:31]\n"
"buffer_store_dwordx4 v[64:67], v253, s[36:39], 0 offen offset:128 sc0 sc1\n"
"v_cvt_pk_bf16_f32 v41, v42, v43\n"
"v_mfma_f32_16x16x32_bf16 v[44:47], v[96:99], a[80:83], v[44:47]\n"
"v_subrev_f32 v26, s22, v26\n"
"v_cvt_pk_bf16_f32 v25, v26, v27\n"
"v_mfma_f32_16x16x32_bf16 v[44:47], v[100:103], a[84:87], v[44:47]\n"
"v_mfma_f32_16x16x32_bf16 v[44:47], v[104:107], a[88:91], v[44:47]\n"
"s_nop 0\n"
"v_subrev_f32 v30, s22, v30\n"
"v_subrev_f32 v31, s22, v31\n"
"v_mfma_f32_16x16x32_bf16 v[44:47], v[108:111], a[92:95], v[44:47]\n"
"v_subrev_f32 v28, s22, v28\n"
"v_subrev_f32 v29, s22, v29\n"
"v_mfma_f32_16x16x32_bf16 v[44:47], v[112:115], a[32:35], v[44:47]\n"
"v_cvt_pk_bf16_f32 v26, v28, v29\n"
"v_cvt_pk_bf16_f32 v27, v30, v31\n"
"v_mfma_f32_16x16x32_bf16 v[44:47], v[116:119], a[36:39], v[44:47]\n"
"s_nop 0\n"
"v_permlane16_swap_b32 v25, v27\n"
"v_mfma_f32_16x16x32_bf16 v[44:47], v[120:123], a[40:43], v[44:47]\n"
"v_permlane16_swap_b32 v24, v26\n"
"v_mfma_f32_16x16x32_bf16 v[44:47], v[224:227], a[44:47], v[44:47]\n"
"buffer_store_dwordx4 v[24:27], v239, s[36:39], s52 offen sc0 sc1\n"
"s_nop 6\n"
"v_subrev_f32 v44, s22, v44\n"
"v_subrev_f32 v45, s22, v45\n"
"v_subrev_f32 v46, s22, v46\n"
"v_subrev_f32 v47, s22, v47\n"
"v_cvt_pk_bf16_f32 v42, v44, v45\n"
"v_cvt_pk_bf16_f32 v43, v46, v47\n"
"s_nop 0\n"
"v_permlane16_swap_b32 v40, v42\n"
"v_permlane16_swap_b32 v41, v43\n"
"buffer_store_dwordx4 v[40:43], v239, s[36:39], s53 offen sc0 sc1\n"
"s_branch LFIN_%=\n"
"LSW_%=:\n"
"s_add_u32 s16, s8, s95\n"
"s_addc_u32 s17, s9, 0\n"
"s_sub_u32 s18, 655360, s95\n"
"s_add_u32 s20, s8, s100\n"
"s_addc_u32 s21, s9, 0\n"
"s_sub_u32 s22, 655360, s100\n"
"s_mov_b32 s56, 0\n"
"s_mov_b32 s55, 0\n"
"s_mov_b32 s58, 0\n"
"s_mov_b32 s59, 163840\n"
"s_mov_b32 s52, 9216\n"
"s_mov_b32 s53, 18432\n"
"s_mov_b32 s54, 27648\n"
"v_lshrrev_b32 v206, 7, v242\n"
"v_mul_u32_u24 v206, 1280, v206\n"
"v_and_b32 v240, 0x7f, v240\n"
"v_add_u32 v240, v206, v240\n"
"buffer_load_dword v252, v240, s[16:19], 0 offen\n"
"s_branch LSWK_%=\n"
"LGP_%=:\n"
"s_mov_b32 s80, 0x40000\n"
"LGPa_%=:\n"
"s_sleep 1\n"
"global_load_dword v200, v201, s[6:7] sc1\n"
"s_sub_u32 s80, s80, 1\n"
"s_cmp_eq_u32 s80, 0\n"
"s_waitcnt vmcnt(0)\n"
"s_cbranch_scc1 LGPK_%=\n"
"v_subrev_u32 v200, s21, v200\n"
"v_cmp_lt_u32 vcc, s69, v200\n"
"s_nop 4\n"
"s_cbranch_vccnz LGPa_%=\n"
"s_branch LGPK_%=\n"
"LGQ_%=:\n"
"s_mov_b32 s80, 0x40000\n"
"LGQa_%=:\n"
"s_sleep 1\n"
"global_load_dword v200, v201, s[6:7] sc1\n"
"s_sub_u32 s80, s80, 1\n"
"s_cmp_eq_u32 s80, 0\n"
"s_waitcnt vmcnt(0)\n"
"s_cbranch_scc1 LGQK_%=\n"
"v_subrev_u32 v200, s21, v200\n"
"v_cmp_lt_u32 vcc, s69, v200\n"
"s_nop 4\n"
"s_cbranch_vccnz LGQa_%=\n"
"s_branch LGQK_%=\n"
"LGR_%=:\n"
"s_mov_b32 s80, 0x40000\n"
"LGRa_%=:\n"
"s_sleep 1\n"
"global_load_dword v200, v201, s[6:7] sc1\n"
"s_sub_u32 s80, s80, 1\n"
"s_cmp_eq_u32 s80, 0\n"
"s_waitcnt vmcnt(0)\n"
"s_cbranch_scc1 LGRK_%=\n"
"v_subrev_u32 v200, s21, v200\n"
"v_cmp_lt_u32 vcc, s69, v200\n"
"s_nop 4\n"
"s_cbranch_vccnz LGRa_%=\n"
"s_branch LGRK_%=\n"
"LGV_%=:\n"
"s_mov_b32 s80, 0x40000\n"
"LGVa_%=:\n"
"s_sleep 1\n"
"global_load_dword v200, v201, s[6:7] sc1\n"
"s_sub_u32 s80, s80, 1\n"
"s_cmp_eq_u32 s80, 0\n"
"s_waitcnt vmcnt(0)\n"
"s_cbranch_scc1 LGVK_%=\n"
"v_subrev_u32 v200, s21, v200\n"
"v_cmp_lt_u32 vcc, s69, v200\n"
"s_nop 4\n"
"s_cbranch_vccnz LGVa_%=\n"
"s_branch LGVK_%=\n"
"LEP_%=:\n"
"s_add_u32 s92, s92, 1\n"
"v_mov_b32 v200, s92\n"
"v_mov_b32 v255, 0\n"
"s_mov_b64 exec, 1\n"
"global_store_dword v255, v200, s[6:7] offset:128 sc1\n"
"s_mov_b64 exec, -1\n"
"s_branch LEPK_%=\n"
"LPCE0_%=:\n"
"s_memrealtime s[80:81]\n"
"s_sub_u32 s82, s67, s66\n"
"s_cmp_gt_i32 s82, 0\n"
"s_cselect_b32 s83, s67, s66\n"
"s_waitcnt lgkmcnt(0)\n"
"s_mov_b32 s63, 0\n"
"s_mov_b32 s62, 126\n"
"s_sub_u32 s82, s83, s80\n"
"s_cmp_gt_i32 s82, 3\n"
"s_cbranch_scc0 LPEE0_%=\n"
"LPDE0_%=:\n"
"s_sleep 1\n"
"s_memrealtime s[80:81]\n"
"s_waitcnt lgkmcnt(0)\n"
"s_sub_u32 s82, s83, s80\n"
"s_cmp_gt_i32 s82, 3\n"
"s_cbranch_scc1 LPDE0_%=\n"
"LPEE0_%=:\n"
"s_add_u32 s66, s66, s72\n"
"s_add_u32 s66, s66, 4\n"
"s_add_u32 s67, s80, s72\n"
"s_sub_u32 s72, s72, 0\n"
"s_max_u32 s72, s72, s62\n"
"s_branch LPKE0_%=\n"
"LPCE1_%=:\n"
"s_memrealtime s[80:81]\n"
"s_sub_u32 s82, s67, s66\n"
"s_cmp_gt_i32 s82, 0\n"
"s_cselect_b32 s83, s67, s66\n"
"s_waitcnt lgkmcnt(0)\n"
"s_sub_u32 s82, s83, s80\n"
"s_cmp_gt_i32 s82, 3\n"
"s_cbranch_scc0 LPEE1_%=\n"
"LPDE1_%=:\n"
"s_sleep 1\n"
"s_memrealtime s[80:81]\n"
"s_waitcnt lgkmcnt(0)\n"
"s_sub_u32 s82, s83, s80\n"
"s_cmp_gt_i32 s82, 3\n"
"s_cbranch_scc1 LPDE1_%=\n"
"LPEE1_%=:\n"
"s_add_u32 s66, s66, s72\n"
"s_add_u32 s66, s66, 4\n"
"s_add_u32 s67, s80, s72\n"
"s_sub_u32 s72, s72, 0\n"
"s_max_u32 s72, s72, s62\n"
"s_branch LPKE1_%=\n"
"LPCT0_%=:\n"
"s_memrealtime s[80:81]\n"
"s_sub_u32 s82, s67, s66\n"
"s_cmp_gt_i32 s82, 0\n"
"s_cselect_b32 s83, s67, s66\n"
"s_waitcnt lgkmcnt(0)\n"
"s_sub_u32 s82, s83, s80\n"
"s_cmp_gt_i32 s82, 3\n"
"s_cbranch_scc0 LPET0_%=\n"
"LPDT0_%=:\n"
"s_sleep 1\n"
"s_memrealtime s[80:81]\n"
"s_waitcnt lgkmcnt(0)\n"
"s_sub_u32 s82, s83, s80\n"
"s_cmp_gt_i32 s82, 3\n"
"s_cbranch_scc1 LPDT0_%=\n"
"LPET0_%=:\n"
"s_add_u32 s66, s66, s72\n"
"s_add_u32 s66, s66, 4\n"
"s_add_u32 s67, s80, s72\n"
"s_sub_u32 s72, s72, 0\n"
"s_max_u32 s72, s72, s62\n"
"s_branch LPKT0_%=\n"
"LPCT1_%=:\n"
"s_memrealtime s[80:81]\n"
"s_sub_u32 s82, s67, s66\n"
"s_cmp_gt_i32 s82, 0\n"
"s_cselect_b32 s83, s67, s66\n"
"s_waitcnt lgkmcnt(0)\n"
"s_sub_u32 s82, s83, s80\n"
"s_cmp_gt_i32 s82, 3\n"
"s_cbranch_scc0 LPET1_%=\n"
"LPDT1_%=:\n"
"s_sleep 1\n"
"s_memrealtime s[80:81]\n"
"s_waitcnt lgkmcnt(0)\n"
"s_sub_u32 s82, s83, s80\n"
"s_cmp_gt_i32 s82, 3\n"
"s_cbranch_scc1 LPDT1_%=\n"
"LPET1_%=:\n"
"s_add_u32 s66, s66, s72\n"
"s_add_u32 s66, s66, 4\n"
"s_add_u32 s67, s80, s72\n"
"s_sub_u32 s72, s72, 0\n"
"s_max_u32 s72, s72, s62\n"
"s_branch LPKT1_%=\n"
"LPCL0_%=:\n"
"s_memrealtime s[80:81]\n"
"s_sub_u32 s82, s67, s66\n"
"s_cmp_gt_i32 s82, 0\n"
"s_cselect_b32 s83, s67, s66\n"
"s_waitcnt lgkmcnt(0)\n"
"s_sub_u32 s82, s80, s67\n"
"s_add_u32 s63, s63, 1\n"
"s_min_i32 s81, s63, 0\n"
"s_cmp_gt_i32 s82, 20\n"
"s_cselect_b32 s63, s63, s81\n"
"s_cmp_ge_i32 s63, 2\n"
"s_cbranch_scc0 LPRL0_%=\n"
"s_mov_b32 s63, -9\n"
"s_add_u32 s62, s62, 10\n"
"s_min_u32 s62, s62, 146\n"
"s_max_u32 s72, s72, s62\n"
"s_add_u32 s83, s80, 200\n"
"s_and_b32 s83, s83, -1\n"
"LPRL0_%=:\n"
"s_sub_u32 s82, s83, s80\n"
"s_cmp_gt_i32 s82, 3\n"
"s_cbranch_scc0 LPEL0_%=\n"
"LPDL0_%=:\n"
"s_sleep 1\n"
"s_memrealtime s[80:81]\n"
"s_waitcnt lgkmcnt(0)\n"
"s_sub_u32 s82, s83, s80\n"
"s_cmp_gt_i32 s82, 3\n"
"s_cbranch_scc1 LPDL0_%=\n"
"LPEL0_%=:\n"
"s_add_u32 s66, s66, s72\n"
"s_add_u32 s66, s66, 4\n"
"s_add_u32 s67, s80, s72\n"
"s_sub_u32 s72, s72, 0\n"
"s_max_u32 s72, s72, s62\n"
"s_branch LPKL0_%=\n"
"LPCL1_%=:\n"
"s_memrealtime s[80:81]\n"
"s_sub_u32 s82, s67, s66\n"
"s_cmp_gt_i32 s82, 0\n"
"s_cselect_b32 s83, s67, s66\n"
"s_waitcnt lgkmcnt(0)\n"
"s_sub_u32 s82, s80, s67\n"
"s_add_u32 s63, s63, 1\n"
"s_min_i32 s81, s63, 0\n"
"s_cmp_gt_i32 s82, 20\n"
"s_cselect_b32 s63, s63, s81\n"
"s_cmp_ge_i32 s63, 2\n"
"s_cbranch_scc0 LPRL1_%=\n"
"s_mov_b32 s63, -9\n"
"s_add_u32 s62, s62, 10\n"
"s_min_u32 s62, s62, 146\n"
"s_max_u32 s72, s72, s62\n"
"s_add_u32 s83, s80, 200\n"
"s_and_b32 s83, s83, -1\n"
"LPRL1_%=:\n"
"s_sub_u32 s82, s83, s80\n"
"s_cmp_gt_i32 s82, 3\n"
"s_cbranch_scc0 LPEL1_%=\n"
"LPDL1_%=:\n"
"s_sleep 1\n"
"s_memrealtime s[80:81]\n"
"s_waitcnt lgkmcnt(0)\n"
"s_sub_u32 s82, s83, s80\n"
"s_cmp_gt_i32 s82, 3\n"
"s_cbranch_scc1 LPDL1_%=\n"
"LPEL1_%=:\n"
"s_add_u32 s66, s66, s72\n"
"s_add_u32 s66, s66, 4\n"
"s_add_u32 s67, s80, s72\n"
"s_sub_u32 s72, s72, 0\n"
"s_max_u32 s72, s72, s62\n"
"s_branch LPKL1_%=\n"
"LPCL2_%=:\n"
"s_memrealtime s[80:81]\n"
"s_sub_u32 s82, s67, s66\n"
"s_cmp_gt_i32 s82, 0\n"
"s_cselect_b32 s83, s67, s66\n"
"s_waitcnt lgkmcnt(0)\n"
"s_sub_u32 s82, s80, s67\n"
"s_add_u32 s63, s63, 1\n"
"s_min_i32 s81, s63, 0\n"
"s_cmp_gt_i32 s82, 20\n"
"s_cselect_b32 s63, s63, s81\n"
"s_cmp_ge_i32 s63, 2\n"
"s_cbranch_scc0 LPRL2_%=\n"
"s_mov_b32 s63, -9\n"
"s_add_u32 s62, s62, 10\n"
"s_min_u32 s62, s62, 146\n"
"s_max_u32 s72, s72, s62\n"
"s_add_u32 s83, s80, 200\n"
"s_and_b32 s83, s83, -1\n"
"LPRL2_%=:\n"
"s_sub_u32 s82, s83, s80\n"
"s_cmp_gt_i32 s82, 3\n"
"s_cbranch_scc0 LPEL2_%=\n"
"LPDL2_%=:\n"
"s_sleep 1\n"
"s_memrealtime s[80:81]\n"
"s_waitcnt lgkmcnt(0)\n"
"s_sub_u32 s82, s83, s80\n"
"s_cmp_gt_i32 s82, 3\n"
"s_cbranch_scc1 LPDL2_%=\n"
"LPEL2_%=:\n"
"s_add_u32 s66, s66, s72\n"
"s_add_u32 s66, s66, 4\n"
"s_add_u32 s67, s80, s72\n"
"s_sub_u32 s72, s72, 0\n"
"s_max_u32 s72, s72, s62\n"
"s_branch LPKL2_%=\n"
"LPCL3_%=:\n"
"s_memrealtime s[80:81]\n"
"s_sub_u32 s82, s67, s66\n"
"s_cmp_gt_i32 s82, 0\n"
"s_cselect_b32 s83, s67, s66\n"
"s_waitcnt lgkmcnt(0)\n"
"s_sub_u32 s82, s80, s67\n"
"s_add_u32 s63, s63, 1\n"
"s_min_i32 s81, s63, 0\n"
"s_cmp_gt_i32 s82, 20\n"
"s_cselect_b32 s63, s63, s81\n"
"s_cmp_ge_i32 s63, 2\n"
"s_cbranch_scc0 LPRL3_%=\n"
"s_mov_b32 s63, -9\n"
"s_add_u32 s62, s62, 10\n"
"s_min_u32 s62, s62, 146\n"
"s_max_u32 s72, s72, s62\n"
"s_add_u32 s83, s80, 200\n"
"s_and_b32 s83, s83, -1\n"
"LPRL3_%=:\n"
"s_sub_u32 s82, s83, s80\n"
"s_cmp_gt_i32 s82, 3\n"
"s_cbranch_scc0 LPEL3_%=\n"
"LPDL3_%=:\n"
"s_sleep 1\n"
"s_memrealtime s[80:81]\n"
"s_waitcnt lgkmcnt(0)\n"
"s_sub_u32 s82, s83, s80\n"
"s_cmp_gt_i32 s82, 3\n"
"s_cbranch_scc1 LPDL3_%=\n"
"LPEL3_%=:\n"
"s_add_u32 s66, s66, s72\n"
"s_add_u32 s66, s66, 4\n"
"s_add_u32 s67, s80, s72\n"
"s_sub_u32 s72, s72, 0\n"
"s_max_u32 s72, s72, s62\n"
"s_branch LPKL3_%=\n"
"LPN_%=:\n"
"v_mfma_f32_16x16x32_bf16 v[112:115], a[192:195], a[136:139], v[112:115]\n"
"v_mfma_f32_16x16x32_bf16 v[112:115], a[196:199], a[140:143], v[112:115]\n"
"v_mfma_f32_16x16x32_bf16 v[116:119], a[200:203], a[136:139], v[116:119]\n"
"s_waitcnt vmcnt(11)\n"
"v_mfma_f32_16x16x32_bf16 v[116:119], a[204:207], a[140:143], v[116:119]\n"
"s_barrier\n"
"v_mfma_f32_16x16x32_bf16 v[120:123], a[208:211], a[136:139], v[120:123]\n"
"s_add_u32 s40, s48, 256\n"
"s_xor_b32 m0, s50, 0xc000\n"
"ds_read_b64_tr_b16 a[224:225], v244 offset:16384\n"
"v_mfma_f32_16x16x32_bf16 v[120:123], a[212:215], a[140:143], v[120:123]\n"
"buffer_load_dwordx4 v224, s[28:31], s40 offen nt lds\n"
"v_mfma_f32_16x16x32_bf16 v[124:127], a[216:219], a[136:139], v[124:127]\n"
"ds_read_b64_tr_b16 a[226:227], v244 offset:16896\n"
"v_mfma_f32_16x16x32_bf16 v[124:127], a[220:223], a[140:143], v[124:127]\n"
"ds_read_b64_tr_b16 a[228:229], v244 offset:24576\n"
"v_mfma_f32_16x16x32_bf16 v[140:143], a[216:219], a[144:147], v[140:143]\n"
"ds_read_b64_tr_b16 a[230:231], v244 offset:25088\n"
"v_mfma_f32_16x16x32_bf16 v[140:143], a[220:223], a[148:151], v[140:143]\n"
"ds_read_b64_tr_b16 a[232:233], v245 offset:16384\n"
"v_mfma_f32_16x16x32_bf16 v[136:139], a[208:211], a[144:147], v[136:139]\n"
"ds_read_b64_tr_b16 a[234:235], v245 offset:16896\n"
"v_mfma_f32_16x16x32_bf16 v[136:139], a[212:215], a[148:151], v[136:139]\n"
"buffer_load_dwordx4 v225, s[28:31], s40 offen offset:1024 nt lds\n"
"v_mfma_f32_16x16x32_bf16 v[132:135], a[200:203], a[144:147], v[132:135]\n"
"ds_read_b64_tr_b16 a[236:237], v245 offset:24576\n"
"v_mfma_f32_16x16x32_bf16 v[132:135], a[204:207], a[148:151], v[132:135]\n"
"ds_read_b64_tr_b16 a[238:239], v245 offset:25088\n"
"v_mfma_f32_16x16x32_bf16 v[128:131], a[192:195], a[144:147], v[128:131]\n"
"ds_read_b64_tr_b16 a[240:241], v244 offset:17408\n"
"v_mfma_f32_16x16x32_bf16 v[128:131], a[196:199], a[148:151], v[128:131]\n"
"ds_read_b64_tr_b16 a[242:243], v244 offset:17920\n"
"v_mfma_f32_16x16x32_bf16 v[144:147], a[192:195], a[152:155], v[144:147]\n"
"ds_read_b64_tr_b16 a[244:245], v244 offset:25600\n"
"v_mfma_f32_16x16x32_bf16 v[144:147], a[196:199], a[156:159], v[144:147]\n"
"buffer_load_dwordx4 v226, s[28:31], s40 offen offset:2048 nt lds\n"
"v_mfma_f32_16x16x32_bf16 v[148:151], a[200:203], a[152:155], v[148:151]\n"
"ds_read_b64_tr_b16 a[246:247], v244 offset:26112\n"
"v_mfma_f32_16x16x32_bf16 v[148:151], a[204:207], a[156:159], v[148:151]\n"
"ds_read_b64_tr_b16 a[248:249], v245 offset:17408\n"
"v_mfma_f32_16x16x32_bf16 v[152:155], a[208:211], a[152:155], v[152:155]\n"
"ds_read_b64_tr_b16 a[250:251], v245 offset:17920\n"
"v_mfma_f32_16x16x32_bf16 v[152:155], a[212:215], a[156:159], v[152:155]\n"
"ds_read_b64_tr_b16 a[252:253], v245 offset:25600\n"
"v_mfma_f32_16x16x32_bf16 v[156:159], a[216:219], a[152:155], v[156:159]\n"
"ds_read_b64_tr_b16 a[254:255], v245 offset:26112\n"
"v_mfma_f32_16x16x32_bf16 v[156:159], a[220:223], a[156:159], v[156:159]\n"
"buffer_load_dwordx4 v227, s[28:31], s40 offen offset:3072 nt lds\n"
"v_mfma_f32_16x16x32_bf16 v[172:175], a[216:219], a[160:163], v[172:175]\n"
"s_cmp_eq_u32 s47, 8\n"
"v_mfma_f32_16x16x32_bf16 v[172:175], a[220:223], a[164:167], v[172:175]\n"
"s_cselect_b32 s41, s52, s58\n"
"v_mfma_f32_16x16x32_bf16 v[168:171], a[208:211], a[160:163], v[168:171]\n"
"s_add_u32 s48, s48, s41\n"
"v_mfma_f32_16x16x32_bf16 v[168:171], a[212:215], a[164:167], v[168:171]\n"
"s_sub_u32 s41, s48, 5242880\n"
"v_mfma_f32_16x16x32_bf16 v[164:167], a[200:203], a[160:163], v[164:167]\n"
"s_min_u32 s48, s48, s41\n"
"v_mfma_f32_16x16x32_bf16 v[164:167], a[204:207], a[164:167], v[164:167]\n"
"v_xor_b32 v244, s74, v244\n"
"v_xor_b32 v246, s74, v246\n"
"v_mfma_f32_16x16x32_bf16 v[160:163], a[192:195], a[160:163], v[160:163]\n"
"s_mov_b32 m0, s50\n"
"v_xor_b32 v245, s74, v245\n"
"v_xor_b32 v247, s74, v247\n"
"v_mfma_f32_16x16x32_bf16 v[160:163], a[196:199], a[164:167], v[160:163]\n"
"buffer_load_dwordx4 v224, s[28:31], s48 offen nt lds\n"
"v_mfma_f32_16x16x32_bf16 v[176:179], a[192:195], a[168:171], v[176:179]\n"
"v_xor_b32 v252, s74, v252\n"
"v_xor_b32 v253, s74, v253\n"
"v_mfma_f32_16x16x32_bf16 v[176:179], a[196:199], a[172:175], v[176:179]\n"
"v_xor_b32 v250, s74, v250\n"
"v_xor_b32 v251, s74, v251\n"
"v_mfma_f32_16x16x32_bf16 v[180:183], a[200:203], a[168:171], v[180:183]\n"
"v_mfma_f32_16x16x32_bf16 v[180:183], a[204:207], a[172:175], v[180:183]\n"
"buffer_load_dwordx4 v225, s[28:31], s48 offen offset:1024 nt lds\n"
"v_mfma_f32_16x16x32_bf16 v[184:187], a[208:211], a[168:171], v[184:187]\n"
"v_mfma_f32_16x16x32_bf16 v[184:187], a[212:215], a[172:175], v[184:187]\n"
"v_mfma_f32_16x16x32_bf16 v[188:191], a[216:219], a[168:171], v[188:191]\n"
"v_mfma_f32_16x16x32_bf16 v[188:191], a[220:223], a[172:175], v[188:191]\n"
"v_mfma_f32_16x16x32_bf16 v[204:207], a[216:219], a[176:179], v[204:207]\n"
"v_mfma_f32_16x16x32_bf16 v[204:207], a[220:223], a[180:183], v[204:207]\n"
"buffer_load_dwordx4 v226, s[28:31], s48 offen offset:2048 nt lds\n"
"v_mfma_f32_16x16x32_bf16 v[200:203], a[208:211], a[176:179], v[200:203]\n"
"v_mfma_f32_16x16x32_bf16 v[200:203], a[212:215], a[180:183], v[200:203]\n"
"v_mfma_f32_16x16x32_bf16 v[196:199], a[200:203], a[176:179], v[196:199]\n"
"v_mfma_f32_16x16x32_bf16 v[196:199], a[204:207], a[180:183], v[196:199]\n"
"v_mfma_f32_16x16x32_bf16 v[192:195], a[192:195], a[176:179], v[192:195]\n"
"v_mfma_f32_16x16x32_bf16 v[192:195], a[196:199], a[180:183], v[192:195]\n"
"s_waitcnt lgkmcnt(0)\n"
"buffer_load_dwordx4 v227, s[28:31], s48 offen offset:3072 nt lds\n"
"v_mfma_f32_16x16x32_bf16 v[208:211], a[192:195], a[184:187], v[208:211]\n"
"s_xor_b32 s50, s50, s74\n"
"v_mfma_f32_16x16x32_bf16 v[208:211], a[196:199], a[188:191], v[208:211]\n"
"v_mfma_f32_16x16x32_bf16 v[212:215], a[200:203], a[184:187], v[212:215]\n"
"v_mfma_f32_16x16x32_bf16 v[212:215], a[204:207], a[188:191], v[212:215]\n"
"v_mfma_f32_16x16x32_bf16 v[216:219], a[208:211], a[184:187], v[216:219]\n"
"v_mfma_f32_16x16x32_bf16 v[216:219], a[212:215], a[188:191], v[216:219]\n"
"v_mfma_f32_16x16x32_bf16 v[220:223], a[216:219], a[184:187], v[220:223]\n"
"v_mfma_f32_16x16x32_bf16 v[220:223], a[220:223], a[188:191], v[220:223]\n"
"v_mfma_f32_16x16x32_bf16 v[0:3], a[224:227], a[136:139], v[0:3]\n"
"v_mfma_f32_16x16x32_bf16 v[0:3], a[228:231], a[140:143], v[0:3]\n"
"v_mfma_f32_16x16x32_bf16 v[4:7], a[232:235], a[136:139], v[4:7]\n"
"s_waitcnt vmcnt(8)\n"
"v_mfma_f32_16x16x32_bf16 v[4:7], a[236:239], a[140:143], v[4:7]\n"
"s_barrier\n"
"v_mfma_f32_16x16x32_bf16 v[8:11], a[240:243], a[136:139], v[8:11]\n"
"s_mov_b32 m0, s51\n"
"ds_read_b64_tr_b16 a[192:193], v244 offset:0\n"
"v_mfma_f32_16x16x32_bf16 v[8:11], a[244:247], a[140:143], v[8:11]\n"
"buffer_load_dword v242, s[80:83], 0 offen lds\n"
"s_add_u32 s80, s80, 680\n"
"s_addc_u32 s81, s81, 0\n"
"buffer_load_dwordx4 v248, s[24:27], s49 offen nt lds\n"
"v_mfma_f32_16x16x32_bf16 v[12:15], a[248:251], a[136:139], v[12:15]\n"
"ds_read_b64_tr_b16 a[194:195], v244 offset:512\n"
"v_mfma_f32_16x16x32_bf16 v[12:15], a[252:255], a[140:143], v[12:15]\n"
"ds_read_b128 a[136:139], v246 offset:0\n"
"v_mfma_f32_16x16x32_bf16 v[28:31], a[248:251], a[144:147], v[28:31]\n"
"ds_read_b64_tr_b16 a[196:197], v244 offset:8192\n"
"v_mfma_f32_16x16x32_bf16 v[28:31], a[252:255], a[148:151], v[28:31]\n"
"ds_read_b64_tr_b16 a[198:199], v244 offset:8704\n"
"v_mfma_f32_16x16x32_bf16 v[24:27], a[240:243], a[144:147], v[24:27]\n"
"ds_read_b128 a[140:143], v247 offset:0\n"
"v_mfma_f32_16x16x32_bf16 v[24:27], a[244:247], a[148:151], v[24:27]\n"
"buffer_load_dwordx4 v249, s[24:27], s49 offen offset:1024 nt lds\n"
"v_mfma_f32_16x16x32_bf16 v[20:23], a[232:235], a[144:147], v[20:23]\n"
"ds_read_b64_tr_b16 a[200:201], v245 offset:0\n"
"v_mfma_f32_16x16x32_bf16 v[20:23], a[236:239], a[148:151], v[20:23]\n"
"ds_read_b64_tr_b16 a[202:203], v245 offset:512\n"
"v_mfma_f32_16x16x32_bf16 v[16:19], a[224:227], a[144:147], v[16:19]\n"
"ds_read_b64_tr_b16 a[204:205], v245 offset:8192\n"
"v_mfma_f32_16x16x32_bf16 v[16:19], a[228:231], a[148:151], v[16:19]\n"
"ds_read_b64_tr_b16 a[206:207], v245 offset:8704\n"
"v_mfma_f32_16x16x32_bf16 v[32:35], a[224:227], a[152:155], v[32:35]\n"
"ds_read_b64_tr_b16 a[208:209], v244 offset:1024\n"
"v_mfma_f32_16x16x32_bf16 v[32:35], a[228:231], a[156:159], v[32:35]\n"
"buffer_load_dwordx4 v254, s[24:27], s49 offen offset:2048 nt lds\n"
"v_mfma_f32_16x16x32_bf16 v[36:39], a[232:235], a[152:155], v[36:39]\n"
"ds_read_b64_tr_b16 a[210:211], v244 offset:1536\n"
"v_mfma_f32_16x16x32_bf16 v[36:39], a[236:239], a[156:159], v[36:39]\n"
"ds_read_b64_tr_b16 a[212:213], v244 offset:9216\n"
"v_mfma_f32_16x16x32_bf16 v[40:43], a[240:243], a[152:155], v[40:43]\n"
"ds_read_b64_tr_b16 a[214:215], v244 offset:9728\n"
"v_mfma_f32_16x16x32_bf16 v[40:43], a[244:247], a[156:159], v[40:43]\n"
"ds_read_b64_tr_b16 a[216:217], v245 offset:1024\n"
"v_mfma_f32_16x16x32_bf16 v[44:47], a[248:251], a[152:155], v[44:47]\n"
"ds_read_b64_tr_b16 a[218:219], v245 offset:1536\n"
"v_mfma_f32_16x16x32_bf16 v[44:47], a[252:255], a[156:159], v[44:47]\n"
"buffer_load_dwordx4 v255, s[24:27], s49 offen offset:3072 nt lds\n"
"v_mfma_f32_16x16x32_bf16 v[60:63], a[248:251], a[160:163], v[60:63]\n"
"ds_read_b64_tr_b16 a[220:221], v245 offset:9216\n"
"v_mfma_f32_16x16x32_bf16 v[60:63], a[252:255], a[164:167], v[60:63]\n"
"ds_read_b64_tr_b16 a[222:223], v245 offset:9728\n"
"v_mfma_f32_16x16x32_bf16 v[56:59], a[240:243], a[160:163], v[56:59]\n"
"ds_read_b128 a[144:147], v246 offset:2048\n"
"v_mfma_f32_16x16x32_bf16 v[56:59], a[244:247], a[164:167], v[56:59]\n"
"s_add_u32 s40, s49, 40960\n"
"ds_read_b128 a[148:151], v247 offset:2048\n"
"v_mfma_f32_16x16x32_bf16 v[52:55], a[232:235], a[160:163], v[52:55]\n"
"s_add_u32 m0, s51, 4096\n"
"ds_read_b128 a[152:155], v246 offset:4096\n"
"v_mfma_f32_16x16x32_bf16 v[52:55], a[236:239], a[164:167], v[52:55]\n"
"buffer_load_dwordx4 v248, s[24:27], s40 offen nt lds\n"
"v_mfma_f32_16x16x32_bf16 v[48:51], a[224:227], a[160:163], v[48:51]\n"
"ds_read_b128 a[156:159], v247 offset:4096\n"
"v_mfma_f32_16x16x32_bf16 v[48:51], a[228:231], a[164:167], v[48:51]\n"
"ds_read_b128 a[160:163], v252 offset:0\n"
"v_mfma_f32_16x16x32_bf16 v[64:67], a[224:227], a[168:171], v[64:67]\n"
"ds_read_b128 a[164:167], v253 offset:0\n"
"v_mfma_f32_16x16x32_bf16 v[64:67], a[228:231], a[172:175], v[64:67]\n"
"v_mfma_f32_16x16x32_bf16 v[68:71], a[232:235], a[168:171], v[68:71]\n"
"v_mfma_f32_16x16x32_bf16 v[68:71], a[236:239], a[172:175], v[68:71]\n"
"buffer_load_dwordx4 v249, s[24:27], s40 offen offset:1024 nt lds\n"
"v_mfma_f32_16x16x32_bf16 v[72:75], a[240:243], a[168:171], v[72:75]\n"
"v_mfma_f32_16x16x32_bf16 v[72:75], a[244:247], a[172:175], v[72:75]\n"
"v_mfma_f32_16x16x32_bf16 v[76:79], a[248:251], a[168:171], v[76:79]\n"
"ds_read_b128 a[168:171], v250 offset:0\n"
"v_mfma_f32_16x16x32_bf16 v[76:79], a[252:255], a[172:175], v[76:79]\n"
"ds_read_b128 a[172:175], v251 offset:0\n"
"v_mfma_f32_16x16x32_bf16 v[92:95], a[248:251], a[176:179], v[92:95]\n"
"v_mfma_f32_16x16x32_bf16 v[92:95], a[252:255], a[180:183], v[92:95]\n"
"buffer_load_dwordx4 v254, s[24:27], s40 offen offset:2048 nt lds\n"
"v_mfma_f32_16x16x32_bf16 v[88:91], a[240:243], a[176:179], v[88:91]\n"
"s_add_u32 s49, s49, 128\n"
"v_mfma_f32_16x16x32_bf16 v[88:91], a[244:247], a[180:183], v[88:91]\n"
"s_sub_u32 s41, s49, 1280\n"
"v_mfma_f32_16x16x32_bf16 v[84:87], a[232:235], a[176:179], v[84:87]\n"
"s_min_u32 s49, s49, s41\n"
"v_mfma_f32_16x16x32_bf16 v[84:87], a[236:239], a[180:183], v[84:87]\n"
"s_xor_b32 s51, s51, s74\n"
"v_mfma_f32_16x16x32_bf16 v[80:83], a[224:227], a[176:179], v[80:83]\n"
"ds_read_b128 a[176:179], v250 offset:2048\n"
"v_mfma_f32_16x16x32_bf16 v[80:83], a[228:231], a[180:183], v[80:83]\n"
"ds_read_b128 a[180:183], v251 offset:2048\n"
"v_mfma_f32_16x16x32_bf16 v[96:99], a[224:227], a[184:187], v[96:99]\n"
"v_mfma_f32_16x16x32_bf16 v[96:99], a[228:231], a[188:191], v[96:99]\n"
"v_mfma_f32_16x16x32_bf16 v[100:103], a[232:235], a[184:187], v[100:103]\n"
"v_mfma_f32_16x16x32_bf16 v[100:103], a[236:239], a[188:191], v[100:103]\n"
"v_mfma_f32_16x16x32_bf16 v[104:107], a[240:243], a[184:187], v[104:107]\n"
"v_mfma_f32_16x16x32_bf16 v[104:107], a[244:247], a[188:191], v[104:107]\n"
"v_mfma_f32_16x16x32_bf16 v[108:111], a[248:251], a[184:187], v[108:111]\n"
"s_add_u32 s47, s47, 1\n"
"ds_read_b128 a[184:187], v250 offset:4096\n"
"v_mfma_f32_16x16x32_bf16 v[108:111], a[252:255], a[188:191], v[108:111]\n"
"s_waitcnt lgkmcnt(1)\n"
"s_cmp_lg_u32 s47, s62\n"
"ds_read_b128 a[188:191], v251 offset:4096\n"
"s_cbranch_scc1 LPN_%=\n"
"s_branch LPX_%=\n"
"LEND_%=:\n"
"LSLOW_%=:\n"
"s_waitcnt vmcnt(0) lgkmcnt(0)\n"
"s_nop 9\n"
"v_bfe_u32 v148, v242, 4, 4\n"
"v_bfe_u32 v149, v242, 8, 2\n"
"v_lshlrev_b32 v144, 8, v148\n"
"v_lshl_add_u32 v144, v149, 4, v144\n"
"v_add_u32 v144, s43, v144\n"
"v_xor_b32 v145, 64, v144\n"
"v_lshlrev_b32 v146, 1, v148\n"
"v_lshl_add_u32 v146, v149, 10, v146\n"
"v_add_u32 v146, s43, v146\n"
"v_xor_b32 v147, 64, v146\n"
"v_mov_b32 v152, 0xff7fffff\n"
"v_mov_b32 v155, 0\n"
"v_mov_b32 v0, 0\n"
"v_mov_b32 v1, 0\n"
"v_mov_b32 v2, 0\n"
"v_mov_b32 v3, 0\n"
"v_mov_b32 v4, 0\n"
"v_mov_b32 v5, 0\n"
"v_mov_b32 v6, 0\n"
"v_mov_b32 v7, 0\n"
"v_mov_b32 v8, 0\n"
"v_mov_b32 v9, 0\n"
"v_mov_b32 v10, 0\n"
"v_mov_b32 v11, 0\n"
"v_mov_b32 v12, 0\n"
"v_mov_b32 v13, 0\n"
"v_mov_b32 v14, 0\n"
"v_mov_b32 v15, 0\n"
"v_mov_b32 v16, 0\n"
"v_mov_b32 v17, 0\n"
"v_mov_b32 v18, 0\n"
"v_mov_b32 v19, 0\n"
"v_mov_b32 v20, 0\n"
"v_mov_b32 v21, 0\n"
"v_mov_b32 v22, 0\n"
"v_mov_b32 v23, 0\n"
"v_mov_b32 v24, 0\n"
"v_mov_b32 v25, 0\n"
"v_mov_b32 v26, 0\n"
"v_mov_b32 v27, 0\n"
"v_mov_b32 v28, 0\n"
"v_mov_b32 v29, 0\n"
"v_mov_b32 v30, 0\n"
"v_mov_b32 v31, 0\n"
"v_mov_b32 v156, 0xff7fffff\n"
"v_mov_b32 v159, 0\n"
"v_mov_b32 v32, 0\n"
"v_mov_b32 v33, 0\n"
"v_mov_b32 v34, 0\n"
"v_mov_b32 v35, 0\n"
"v_mov_b32 v36, 0\n"
"v_mov_b32 v37, 0\n"
"v_mov_b32 v38, 0\n"
"v_mov_b32 v39, 0\n"
"v_mov_b32 v40, 0\n"
"v_mov_b32 v41, 0\n"
"v_mov_b32 v42, 0\n"
"v_mov_b32 v43, 0\n"
"v_mov_b32 v44, 0\n"
"v_mov_b32 v45, 0\n"
"v_mov_b32 v46, 0\n"
"v_mov_b32 v47, 0\n"
"v_mov_b32 v48, 0\n"
"v_mov_b32 v49, 0\n"
"v_mov_b32 v50, 0\n"
"v_mov_b32 v51, 0\n"
"v_mov_b32 v52, 0\n"
"v_mov_b32 v53, 0\n"
"v_mov_b32 v54, 0\n"
"v_mov_b32 v55, 0\n"
"v_mov_b32 v56, 0\n"
"v_mov_b32 v57, 0\n"
"v_mov_b32 v58, 0\n"
"v_mov_b32 v59, 0\n"
"v_mov_b32 v60, 0\n"
"v_mov_b32 v61, 0\n"
"v_mov_b32 v62, 0\n"
"v_mov_b32 v63, 0\n"
"v_mov_b32 v160, 0xff7fffff\n"
"v_mov_b32 v163, 0\n"
"v_mov_b32 v64, 0\n"
"v_mov_b32 v65, 0\n"
"v_mov_b32 v66, 0\n"
"v_mov_b32 v67, 0\n"
"v_mov_b32 v68, 0\n"
"v_mov_b32 v69, 0\n"
"v_mov_b32 v70, 0\n"
"v_mov_b32 v71, 0\n"
"v_mov_b32 v72, 0\n"
"v_mov_b32 v73, 0\n"
"v_mov_b32 v74, 0\n"
"v_mov_b32 v75, 0\n"
"v_mov_b32 v76, 0\n"
"v_mov_b32 v77, 0\n"
"v_mov_b32 v78, 0\n"
"v_mov_b32 v79, 0\n"
"v_mov_b32 v80, 0\n"
"v_mov_b32 v81, 0\n"
"v_mov_b32 v82, 0\n"
"v_mov_b32 v83, 0\n"
"v_mov_b32 v84, 0\n"
"v_mov_b32 v85, 0\n"
"v_mov_b32 v86, 0\n"
"v_mov_b32 v87, 0\n"
"v_mov_b32 v88, 0\n"
"v_mov_b32 v89, 0\n"
"v_mov_b32 v90, 0\n"
"v_mov_b32 v91, 0\n"
"v_mov_b32 v92, 0\n"
"v_mov_b32 v93, 0\n"
"v_mov_b32 v94, 0\n"
"v_mov_b32 v95, 0\n"
"v_mov_b32 v164, 0xff7fffff\n"
"v_mov_b32 v167, 0\n"
"v_accvgpr_write_b32 a208, 0\n"
"v_accvgpr_write_b32 a209, 0\n"
"v_accvgpr_write_b32 a210, 0\n"
"v_accvgpr_write_b32 a211, 0\n"
"v_accvgpr_write_b32 a212, 0\n"
"v_accvgpr_write_b32 a213, 0\n"
"v_accvgpr_write_b32 a214, 0\n"
"v_accvgpr_write_b32 a215, 0\n"
"v_accvgpr_write_b32 a216, 0\n"
"v_accvgpr_write_b32 a217, 0\n"
"v_accvgpr_write_b32 a218, 0\n"
"v_accvgpr_write_b32 a219, 0\n"
"v_accvgpr_write_b32 a220, 0\n"
"v_accvgpr_write_b32 a221, 0\n"
"v_accvgpr_write_b32 a222, 0\n"
"v_accvgpr_write_b32 a223, 0\n"
"v_accvgpr_write_b32 a224, 0\n"
"v_accvgpr_write_b32 a225, 0\n"
"v_accvgpr_write_b32 a226, 0\n"
"v_accvgpr_write_b32 a227, 0\n"
"v_accvgpr_write_b32 a228, 0\n"
"v_accvgpr_write_b32 a229, 0\n"
"v_accvgpr_write_b32 a230, 0\n"
"v_accvgpr_write_b32 a231, 0\n"
"v_accvgpr_write_b32 a232, 0\n"
"v_accvgpr_write_b32 a233, 0\n"
"v_accvgpr_write_b32 a234, 0\n"
"v_accvgpr_write_b32 a235, 0\n"
"v_accvgpr_write_b32 a236, 0\n"
"v_accvgpr_write_b32 a237, 0\n"
"v_accvgpr_write_b32 a238, 0\n"
"v_accvgpr_write_b32 a239, 0\n"
"s_lshl_b32 s80, s47, 16\n"
"s_sub_u32 s80, s80, 261248\n"
"s_add_u32 s81, s80, 4096\n"
"s_mov_b32 s82, 0\n"
"LSLOOP_%=:\n"
"s_lshl_b32 s83, s82, 6\n"
"s_and_b32 s83, s83, 64\n"
"buffer_load_dwordx4 v[128:131], v144, s[84:87], s80 offen offset:0\n"
"buffer_load_dwordx4 v[132:135], v145, s[84:87], s80 offen offset:0\n"
"buffer_load_dwordx4 v[136:139], v144, s[84:87], s80 offen offset:128\n"
"buffer_load_dwordx4 v[140:143], v145, s[84:87], s80 offen offset:128\n"
"s_waitcnt vmcnt(0)\n"
"v_mfma_f32_16x16x32_bf16 v[96:99], v[128:131], a[0:3], 0\n"
"v_mfma_f32_16x16x32_bf16 v[96:99], v[132:135], a[4:7], v[96:99]\n"
"v_mfma_f32_16x16x32_bf16 v[96:99], v[136:139], a[8:11], v[96:99]\n"
"v_mfma_f32_16x16x32_bf16 v[96:99], v[140:143], a[12:15], v[96:99]\n"
"v_mfma_f32_16x16x32_bf16 v[104:107], v[128:131], a[16:19], 0\n"
"v_mfma_f32_16x16x32_bf16 v[104:107], v[132:135], a[20:23], v[104:107]\n"
"v_mfma_f32_16x16x32_bf16 v[104:107], v[136:139], a[24:27], v[104:107]\n"
"v_mfma_f32_16x16x32_bf16 v[104:107], v[140:143], a[28:31], v[104:107]\n"
"v_mfma_f32_16x16x32_bf16 v[112:115], v[128:131], a[32:35], 0\n"
"v_mfma_f32_16x16x32_bf16 v[112:115], v[132:135], a[36:39], v[112:115]\n"
"v_mfma_f32_16x16x32_bf16 v[112:115], v[136:139], a[40:43], v[112:115]\n"
"v_mfma_f32_16x16x32_bf16 v[112:115], v[140:143], a[44:47], v[112:115]\n"
"v_mfma_f32_16x16x32_bf16 v[120:123], v[128:131], a[192:195], 0\n"
"v_mfma_f32_16x16x32_bf16 v[120:123], v[132:135], a[196:199], v[120:123]\n"
"v_mfma_f32_16x16x32_bf16 v[120:123], v[136:139], a[200:203], v[120:123]\n"
"v_mfma_f32_16x16x32_bf16 v[120:123], v[140:143], a[204:207], v[120:123]\n"
"s_nop 7\n"
"buffer_load_dwordx4 v[128:131], v144, s[84:87], s81 offen offset:0\n"
"buffer_load_dwordx4 v[132:135], v145, s[84:87], s81 offen offset:0\n"
"buffer_load_dwordx4 v[136:139], v144, s[84:87], s81 offen offset:128\n"
"buffer_load_dwordx4 v[140:143], v145, s[84:87], s81 offen offset:128\n"
"s_waitcnt vmcnt(0)\n"
"v_mfma_f32_16x16x32_bf16 v[100:103], v[128:131], a[0:3], 0\n"
"v_mfma_f32_16x16x32_bf16 v[100:103], v[132:135], a[4:7], v[100:103]\n"
"v_mfma_f32_16x16x32_bf16 v[100:103], v[136:139], a[8:11], v[100:103]\n"
"v_mfma_f32_16x16x32_bf16 v[100:103], v[140:143], a[12:15], v[100:103]\n"
"v_mfma_f32_16x16x32_bf16 v[108:111], v[128:131], a[16:19], 0\n"
"v_mfma_f32_16x16x32_bf16 v[108:111], v[132:135], a[20:23], v[108:111]\n"
"v_mfma_f32_16x16x32_bf16 v[108:111], v[136:139], a[24:27], v[108:111]\n"
"v_mfma_f32_16x16x32_bf16 v[108:111], v[140:143], a[28:31], v[108:111]\n"
"v_mfma_f32_16x16x32_bf16 v[116:119], v[128:131], a[32:35], 0\n"
"v_mfma_f32_16x16x32_bf16 v[116:119], v[132:135], a[36:39], v[116:119]\n"
"v_mfma_f32_16x16x32_bf16 v[116:119], v[136:139], a[40:43], v[116:119]\n"
"v_mfma_f32_16x16x32_bf16 v[116:119], v[140:143], a[44:47], v[116:119]\n"
"v_mfma_f32_16x16x32_bf16 v[124:127], v[128:131], a[192:195], 0\n"
"v_mfma_f32_16x16x32_bf16 v[124:127], v[132:135], a[196:199], v[124:127]\n"
"v_mfma_f32_16x16x32_bf16 v[124:127], v[136:139], a[200:203], v[124:127]\n"
"v_mfma_f32_16x16x32_bf16 v[124:127], v[140:143], a[204:207], v[124:127]\n"
"s_nop 7\n"
"v_max3_f32 v153, v96, v97, v98\n"
"v_max3_f32 v153, v153, v99, v100\n"
"v_max3_f32 v153, v153, v101, v102\n"
"v_max3_f32 v153, v153, v103, v152\n"
"v_mov_b32 v154, v153\n"
"s_nop 1\n"
"v_permlane32_swap_b32 v153, v154\n"
"v_max_f32 v153, v153, v154\n"
"v_mov_b32 v154, v153\n"
"s_nop 1\n"
"v_permlane16_swap_b32 v153, v154\n"
"v_max_f32 v153, v153, v154\n"
"v_sub_f32 v154, v152, v153\n"
"v_exp_f32 v154, v154\n"
"v_mov_b32 v152, v153\n"
"v_sub_f32 v96, v96, v152\n"
"v_sub_f32 v97, v97, v152\n"
"v_sub_f32 v98, v98, v152\n"
"v_sub_f32 v99, v99, v152\n"
"v_sub_f32 v100, v100, v152\n"
"v_sub_f32 v101, v101, v152\n"
"v_sub_f32 v102, v102, v152\n"
"v_sub_f32 v103, v103, v152\n"
"v_exp_f32 v96, v96\n"
"v_exp_f32 v97, v97\n"
"v_exp_f32 v98, v98\n"
"v_exp_f32 v99, v99\n"
"v_exp_f32 v100, v100\n"
"v_exp_f32 v101, v101\n"
"v_exp_f32 v102, v102\n"
"v_exp_f32 v103, v103\n"
"v_mul_f32 v155, v155, v154\n"
"v_add_f32 v155, v155, v96\n"
"v_add_f32 v155, v155, v97\n"
"v_add_f32 v155, v155, v98\n"
"v_add_f32 v155, v155, v99\n"
"v_add_f32 v155, v155, v100\n"
"v_add_f32 v155, v155, v101\n"
"v_add_f32 v155, v155, v102\n"
"v_add_f32 v155, v155, v103\n"
"v_cvt_pk_bf16_f32 v96, v96, v97\n"
"v_cvt_pk_bf16_f32 v97, v98, v99\n"
"v_cvt_pk_bf16_f32 v98, v100, v101\n"
"v_cvt_pk_bf16_f32 v99, v102, v103\n"
"v_mov_b32_dpp v154, v154 row_ror:12 row_mask:0x2 bank_mask:0xf\n"
"s_nop 1\n"
"v_mov_b32_dpp v154, v154 row_ror:8 row_mask:0x4 bank_mask:0xf\n"
"s_nop 1\n"
"v_mov_b32_dpp v154, v154 row_ror:4 row_mask:0x8 bank_mask:0xf\n"
"s_nop 1\n"
"v_mul_f32_dpp v0, v154, v0 row_newbcast:0 row_mask:0xf bank_mask:0xf\n"
"v_mul_f32_dpp v1, v154, v1 row_newbcast:1 row_mask:0xf bank_mask:0xf\n"
"v_mul_f32_dpp v2, v154, v2 row_newbcast:2 row_mask:0xf bank_mask:0xf\n"
"v_mul_f32_dpp v3, v154, v3 row_newbcast:3 row_mask:0xf bank_mask:0xf\n"
"v_mul_f32_dpp v4, v154, v4 row_newbcast:0 row_mask:0xf bank_mask:0xf\n"
"v_mul_f32_dpp v5, v154, v5 row_newbcast:1 row_mask:0xf bank_mask:0xf\n"
"v_mul_f32_dpp v6, v154, v6 row_newbcast:2 row_mask:0xf bank_mask:0xf\n"
"v_mul_f32_dpp v7, v154, v7 row_newbcast:3 row_mask:0xf bank_mask:0xf\n"
"v_mul_f32_dpp v8, v154, v8 row_newbcast:0 row_mask:0xf bank_mask:0xf\n"
"v_mul_f32_dpp v9, v154, v9 row_newbcast:1 row_mask:0xf bank_mask:0xf\n"
"v_mul_f32_dpp v10, v154, v10 row_newbcast:2 row_mask:0xf bank_mask:0xf\n"
"v_mul_f32_dpp v11, v154, v11 row_newbcast:3 row_mask:0xf bank_mask:0xf\n"
"v_mul_f32_dpp v12, v154, v12 row_newbcast:0 row_mask:0xf bank_mask:0xf\n"
"v_mul_f32_dpp v13, v154, v13 row_newbcast:1 row_mask:0xf bank_mask:0xf\n"
"v_mul_f32_dpp v14, v154, v14 row_newbcast:2 row_mask:0xf bank_mask:0xf\n"
"v_mul_f32_dpp v15, v154, v15 row_newbcast:3 row_mask:0xf bank_mask:0xf\n"
"v_mul_f32_dpp v16, v154, v16 row_newbcast:0 row_mask:0xf bank_mask:0xf\n"
"v_mul_f32_dpp v17, v154, v17 row_newbcast:1 row_mask:0xf bank_mask:0xf\n"
"v_mul_f32_dpp v18, v154, v18 row_newbcast:2 row_mask:0xf bank_mask:0xf\n"
"v_mul_f32_dpp v19, v154, v19 row_newbcast:3 row_mask:0xf bank_mask:0xf\n"
"v_mul_f32_dpp v20, v154, v20 row_newbcast:0 row_mask:0xf bank_mask:0xf\n"
"v_mul_f32_dpp v21, v154, v21 row_newbcast:1 row_mask:0xf bank_mask:0xf\n"
"v_mul_f32_dpp v22, v154, v22 row_newbcast:2 row_mask:0xf bank_mask:0xf\n"
"v_mul_f32_dpp v23, v154, v23 row_newbcast:3 row_mask:0xf bank_mask:0xf\n"
"v_mul_f32_dpp v24, v154, v24 row_newbcast:0 row_mask:0xf bank_mask:0xf\n"
"v_mul_f32_dpp v25, v154, v25 row_newbcast:1 row_mask:0xf bank_mask:0xf\n"
"v_mul_f32_dpp v26, v154, v26 row_newbcast:2 row_mask:0xf bank_mask:0xf\n"
"v_mul_f32_dpp v27, v154, v27 row_newbcast:3 row_mask:0xf bank_mask:0xf\n"
"v_mul_f32_dpp v28, v154, v28 row_newbcast:0 row_mask:0xf bank_mask:0xf\n"
"v_mul_f32_dpp v29, v154, v29 row_newbcast:1 row_mask:0xf bank_mask:0xf\n"
"v_mul_f32_dpp v30, v154, v30 row_newbcast:2 row_mask:0xf bank_mask:0xf\n"
"v_mul_f32_dpp v31, v154, v31 row_newbcast:3 row_mask:0xf bank_mask:0xf\n"
"v_max3_f32 v157, v104, v105, v106\n"
"v_max3_f32 v157, v157, v107, v108\n"
"v_max3_f32 v157, v157, v109, v110\n"
"v_max3_f32 v157, v157, v111, v156\n"
"v_mov_b32 v158, v157\n"
"s_nop 1\n"
"v_permlane32_swap_b32 v157, v158\n"
"v_max_f32 v157, v157, v158\n"
"v_mov_b32 v158, v157\n"
"s_nop 1\n"
"v_permlane16_swap_b32 v157, v158\n"
"v_max_f32 v157, v157, v158\n"
"v_sub_f32 v158, v156, v157\n"
"v_exp_f32 v158, v158\n"
"v_mov_b32 v156, v157\n"
"v_sub_f32 v104, v104, v156\n"
"v_sub_f32 v105, v105, v156\n"
"v_sub_f32 v106, v106, v156\n"
"v_sub_f32 v107, v107, v156\n"
"v_sub_f32 v108, v108, v156\n"
"v_sub_f32 v109, v109, v156\n"
"v_sub_f32 v110, v110, v156\n"
"v_sub_f32 v111, v111, v156\n"
"v_exp_f32 v104, v104\n"
"v_exp_f32 v105, v105\n"
"v_exp_f32 v106, v106\n"
"v_exp_f32 v107, v107\n"
"v_exp_f32 v108, v108\n"
"v_exp_f32 v109, v109\n"
"v_exp_f32 v110, v110\n"
"v_exp_f32 v111, v111\n"
"v_mul_f32 v159, v159, v158\n"
"v_add_f32 v159, v159, v104\n"
"v_add_f32 v159, v159, v105\n"
"v_add_f32 v159, v159, v106\n"
"v_add_f32 v159, v159, v107\n"
"v_add_f32 v159, v159, v108\n"
"v_add_f32 v159, v159, v109\n"
"v_add_f32 v159, v159, v110\n"
"v_add_f32 v159, v159, v111\n"
"v_cvt_pk_bf16_f32 v104, v104, v105\n"
"v_cvt_pk_bf16_f32 v105, v106, v107\n"
"v_cvt_pk_bf16_f32 v106, v108, v109\n"
"v_cvt_pk_bf16_f32 v107, v110, v111\n"
"v_mov_b32_dpp v158, v158 row_ror:12 row_mask:0x2 bank_mask:0xf\n"
"s_nop 1\n"
"v_mov_b32_dpp v158, v158 row_ror:8 row_mask:0x4 bank_mask:0xf\n"
"s_nop 1\n"
"v_mov_b32_dpp v158, v158 row_ror:4 row_mask:0x8 bank_mask:0xf\n"
"s_nop 1\n"
"v_mul_f32_dpp v32, v158, v32 row_newbcast:0 row_mask:0xf bank_mask:0xf\n"
"v_mul_f32_dpp v33, v158, v33 row_newbcast:1 row_mask:0xf bank_mask:0xf\n"
"v_mul_f32_dpp v34, v158, v34 row_newbcast:2 row_mask:0xf bank_mask:0xf\n"
"v_mul_f32_dpp v35, v158, v35 row_newbcast:3 row_mask:0xf bank_mask:0xf\n"
"v_mul_f32_dpp v36, v158, v36 row_newbcast:0 row_mask:0xf bank_mask:0xf\n"
"v_mul_f32_dpp v37, v158, v37 row_newbcast:1 row_mask:0xf bank_mask:0xf\n"
"v_mul_f32_dpp v38, v158, v38 row_newbcast:2 row_mask:0xf bank_mask:0xf\n"
"v_mul_f32_dpp v39, v158, v39 row_newbcast:3 row_mask:0xf bank_mask:0xf\n"
"v_mul_f32_dpp v40, v158, v40 row_newbcast:0 row_mask:0xf bank_mask:0xf\n"
"v_mul_f32_dpp v41, v158, v41 row_newbcast:1 row_mask:0xf bank_mask:0xf\n"
"v_mul_f32_dpp v42, v158, v42 row_newbcast:2 row_mask:0xf bank_mask:0xf\n"
"v_mul_f32_dpp v43, v158, v43 row_newbcast:3 row_mask:0xf bank_mask:0xf\n"
"v_mul_f32_dpp v44, v158, v44 row_newbcast:0 row_mask:0xf bank_mask:0xf\n"
"v_mul_f32_dpp v45, v158, v45 row_newbcast:1 row_mask:0xf bank_mask:0xf\n"
"v_mul_f32_dpp v46, v158, v46 row_newbcast:2 row_mask:0xf bank_mask:0xf\n"
"v_mul_f32_dpp v47, v158, v47 row_newbcast:3 row_mask:0xf bank_mask:0xf\n"
"v_mul_f32_dpp v48, v158, v48 row_newbcast:0 row_mask:0xf bank_mask:0xf\n"
"v_mul_f32_dpp v49, v158, v49 row_newbcast:1 row_mask:0xf bank_mask:0xf\n"
"v_mul_f32_dpp v50, v158, v50 row_newbcast:2 row_mask:0xf bank_mask:0xf\n"
"v_mul_f32_dpp v51, v158, v51 row_newbcast:3 row_mask:0xf bank_mask:0xf\n"
"v_mul_f32_dpp v52, v158, v52 row_newbcast:0 row_mask:0xf bank_mask:0xf\n"
"v_mul_f32_dpp v53, v158, v53 row_newbcast:1 row_mask:0xf bank_mask:0xf\n"
"v_mul_f32_dpp v54, v158, v54 row_newbcast:2 row_mask:0xf bank_mask:0xf\n"
"v_mul_f32_dpp v55, v158, v55 row_newbcast:3 row_mask:0xf bank_mask:0xf\n"
"v_mul_f32_dpp v56, v158, v56 row_newbcast:0 row_mask:0xf bank_mask:0xf\n"
"v_mul_f32_dpp v57, v158, v57 row_newbcast:1 row_mask:0xf bank_mask:0xf\n"
"v_mul_f32_dpp v58, v158, v58 row_newbcast:2 row_mask:0xf bank_mask:0xf\n"
"v_mul_f32_dpp v59, v158, v59 row_newbcast:3 row_mask:0xf bank_mask:0xf\n"
"v_mul_f32_dpp v60, v158, v60 row_newbcast:0 row_mask:0xf bank_mask:0xf\n"
"v_mul_f32_dpp v61, v158, v61 row_newbcast:1 row_mask:0xf bank_mask:0xf\n"
"v_mul_f32_dpp v62, v158, v62 row_newbcast:2 row_mask:0xf bank_mask:0xf\n"
"v_mul_f32_dpp v63, v158, v63 row_newbcast:3 row_mask:0xf bank_mask:0xf\n"
"v_max3_f32 v161, v112, v113, v114\n"
"v_max3_f32 v161, v161, v115, v116\n"
"v_max3_f32 v161, v161, v117, v118\n"
"v_max3_f32 v161, v161, v119, v160\n"
"v_mov_b32 v162, v161\n"
"s_nop 1\n"
"v_permlane32_swap_b32 v161, v162\n"
"v_max_f32 v161, v161, v162\n"
"v_mov_b32 v162, v161\n"
"s_nop 1\n"
"v_permlane16_swap_b32 v161, v162\n"
"v_max_f32 v161, v161, v162\n"
"v_sub_f32 v162, v160, v161\n"
"v_exp_f32 v162, v162\n"
"v_mov_b32 v160, v161\n"
"v_sub_f32 v112, v112, v160\n"
"v_sub_f32 v113, v113, v160\n"
"v_sub_f32 v114, v114, v160\n"
"v_sub_f32 v115, v115, v160\n"
"v_sub_f32 v116, v116, v160\n"
"v_sub_f32 v117, v117, v160\n"
"v_sub_f32 v118, v118, v160\n"
"v_sub_f32 v119, v119, v160\n"
"v_exp_f32 v112, v112\n"
"v_exp_f32 v113, v113\n"
"v_exp_f32 v114, v114\n"
"v_exp_f32 v115, v115\n"
"v_exp_f32 v116, v116\n"
"v_exp_f32 v117, v117\n"
"v_exp_f32 v118, v118\n"
"v_exp_f32 v119, v119\n"
"v_mul_f32 v163, v163, v162\n"
"v_add_f32 v163, v163, v112\n"
"v_add_f32 v163, v163, v113\n"
"v_add_f32 v163, v163, v114\n"
"v_add_f32 v163, v163, v115\n"
"v_add_f32 v163, v163, v116\n"
"v_add_f32 v163, v163, v117\n"
"v_add_f32 v163, v163, v118\n"
"v_add_f32 v163, v163, v119\n"
"v_cvt_pk_bf16_f32 v112, v112, v113\n"
"v_cvt_pk_bf16_f32 v113, v114, v115\n"
"v_cvt_pk_bf16_f32 v114, v116, v117\n"
"v_cvt_pk_bf16_f32 v115, v118, v119\n"
"v_mov_b32_dpp v162, v162 row_ror:12 row_mask:0x2 bank_mask:0xf\n"
"s_nop 1\n"
"v_mov_b32_dpp v162, v162 row_ror:8 row_mask:0x4 bank_mask:0xf\n"
"s_nop 1\n"
"v_mov_b32_dpp v162, v162 row_ror:4 row_mask:0x8 bank_mask:0xf\n"
"s_nop 1\n"
"v_mul_f32_dpp v64, v162, v64 row_newbcast:0 row_mask:0xf bank_mask:0xf\n"
"v_mul_f32_dpp v65, v162, v65 row_newbcast:1 row_mask:0xf bank_mask:0xf\n"
"v_mul_f32_dpp v66, v162, v66 row_newbcast:2 row_mask:0xf bank_mask:0xf\n"
"v_mul_f32_dpp v67, v162, v67 row_newbcast:3 row_mask:0xf bank_mask:0xf\n"
"v_mul_f32_dpp v68, v162, v68 row_newbcast:0 row_mask:0xf bank_mask:0xf\n"
"v_mul_f32_dpp v69, v162, v69 row_newbcast:1 row_mask:0xf bank_mask:0xf\n"
"v_mul_f32_dpp v70, v162, v70 row_newbcast:2 row_mask:0xf bank_mask:0xf\n"
"v_mul_f32_dpp v71, v162, v71 row_newbcast:3 row_mask:0xf bank_mask:0xf\n"
"v_mul_f32_dpp v72, v162, v72 row_newbcast:0 row_mask:0xf bank_mask:0xf\n"
"v_mul_f32_dpp v73, v162, v73 row_newbcast:1 row_mask:0xf bank_mask:0xf\n"
"v_mul_f32_dpp v74, v162, v74 row_newbcast:2 row_mask:0xf bank_mask:0xf\n"
"v_mul_f32_dpp v75, v162, v75 row_newbcast:3 row_mask:0xf bank_mask:0xf\n"
"v_mul_f32_dpp v76, v162, v76 row_newbcast:0 row_mask:0xf bank_mask:0xf\n"
"v_mul_f32_dpp v77, v162, v77 row_newbcast:1 row_mask:0xf bank_mask:0xf\n"
"v_mul_f32_dpp v78, v162, v78 row_newbcast:2 row_mask:0xf bank_mask:0xf\n"
"v_mul_f32_dpp v79, v162, v79 row_newbcast:3 row_mask:0xf bank_mask:0xf\n"
"v_mul_f32_dpp v80, v162, v80 row_newbcast:0 row_mask:0xf bank_mask:0xf\n"
"v_mul_f32_dpp v81, v162, v81 row_newbcast:1 row_mask:0xf bank_mask:0xf\n"
"v_mul_f32_dpp v82, v162, v82 row_newbcast:2 row_mask:0xf bank_mask:0xf\n"
"v_mul_f32_dpp v83, v162, v83 row_newbcast:3 row_mask:0xf bank_mask:0xf\n"
"v_mul_f32_dpp v84, v162, v84 row_newbcast:0 row_mask:0xf bank_mask:0xf\n"
"v_mul_f32_dpp v85, v162, v85 row_newbcast:1 row_mask:0xf bank_mask:0xf\n"
"v_mul_f32_dpp v86, v162, v86 row_newbcast:2 row_mask:0xf bank_mask:0xf\n"
"v_mul_f32_dpp v87, v162, v87 row_newbcast:3 row_mask:0xf bank_mask:0xf\n"
"v_mul_f32_dpp v88, v162, v88 row_newbcast:0 row_mask:0xf bank_mask:0xf\n"
"v_mul_f32_dpp v89, v162, v89 row_newbcast:1 row_mask:0xf bank_mask:0xf\n"
"v_mul_f32_dpp v90, v162, v90 row_newbcast:2 row_mask:0xf bank_mask:0xf\n"
"v_mul_f32_dpp v91, v162, v91 row_newbcast:3 row_mask:0xf bank_mask:0xf\n"
"v_mul_f32_dpp v92, v162, v92 row_newbcast:0 row_mask:0xf bank_mask:0xf\n"
"v_mul_f32_dpp v93, v162, v93 row_newbcast:1 row_mask:0xf bank_mask:0xf\n"
"v_mul_f32_dpp v94, v162, v94 row_newbcast:2 row_mask:0xf bank_mask:0xf\n"
"v_mul_f32_dpp v95, v162, v95 row_newbcast:3 row_mask:0xf bank_mask:0xf\n"
"s_cmp_lg_u32 s83, s43\n"
"s_cbranch_scc1 LSK3A_%=\n"
"v_max3_f32 v165, v120, v121, v122\n"
"v_max3_f32 v165, v165, v123, v124\n"
"v_max3_f32 v165, v165, v125, v126\n"
"v_max3_f32 v165, v165, v127, v164\n"
"v_mov_b32 v166, v165\n"
"s_nop 1\n"
"v_permlane32_swap_b32 v165, v166\n"
"v_max_f32 v165, v165, v166\n"
"v_mov_b32 v166, v165\n"
"s_nop 1\n"
"v_permlane16_swap_b32 v165, v166\n"
"v_max_f32 v165, v165, v166\n"
"v_sub_f32 v166, v164, v165\n"
"v_exp_f32 v166, v166\n"
"v_mov_b32 v164, v165\n"
"v_sub_f32 v120, v120, v164\n"
"v_sub_f32 v121, v121, v164\n"
"v_sub_f32 v122, v122, v164\n"
"v_sub_f32 v123, v123, v164\n"
"v_sub_f32 v124, v124, v164\n"
"v_sub_f32 v125, v125, v164\n"
"v_sub_f32 v126, v126, v164\n"
"v_sub_f32 v127, v127, v164\n"
"v_exp_f32 v120, v120\n"
"v_exp_f32 v121, v121\n"
"v_exp_f32 v122, v122\n"
"v_exp_f32 v123, v123\n"
"v_exp_f32 v124, v124\n"
"v_exp_f32 v125, v125\n"
"v_exp_f32 v126, v126\n"
"v_exp_f32 v127, v127\n"
"v_mul_f32 v167, v167, v166\n"
"v_add_f32 v167, v167, v120\n"
"v_add_f32 v167, v167, v121\n"
"v_add_f32 v167, v167, v122\n"
"v_add_f32 v167, v167, v123\n"
"v_add_f32 v167, v167, v124\n"
"v_add_f32 v167, v167, v125\n"
"v_add_f32 v167, v167, v126\n"
"v_add_f32 v167, v167, v127\n"
"v_cvt_pk_bf16_f32 v120, v120, v121\n"
"v_cvt_pk_bf16_f32 v121, v122, v123\n"
"v_cvt_pk_bf16_f32 v122, v124, v125\n"
"v_cvt_pk_bf16_f32 v123, v126, v127\n"
"v_mov_b32_dpp v166, v166 row_ror:12 row_mask:0x2 bank_mask:0xf\n"
"s_nop 1\n"
"v_mov_b32_dpp v166, v166 row_ror:8 row_mask:0x4 bank_mask:0xf\n"
"s_nop 1\n"
"v_mov_b32_dpp v166, v166 row_ror:4 row_mask:0x8 bank_mask:0xf\n"
"v_accvgpr_read_b32 v124, a208\n"
"s_nop 1\n"
"v_mul_f32_dpp v124, v166, v124 row_newbcast:0 row_mask:0xf bank_mask:0xf\n"
"v_accvgpr_write_b32 a208, v124\n"
"v_accvgpr_read_b32 v125, a209\n"
"s_nop 1\n"
"v_mul_f32_dpp v125, v166, v125 row_newbcast:1 row_mask:0xf bank_mask:0xf\n"
"v_accvgpr_write_b32 a209, v125\n"
"v_accvgpr_read_b32 v126, a210\n"
"s_nop 1\n"
"v_mul_f32_dpp v126, v166, v126 row_newbcast:2 row_mask:0xf bank_mask:0xf\n"
"v_accvgpr_write_b32 a210, v126\n"
"v_accvgpr_read_b32 v127, a211\n"
"s_nop 1\n"
"v_mul_f32_dpp v127, v166, v127 row_newbcast:3 row_mask:0xf bank_mask:0xf\n"
"v_accvgpr_write_b32 a211, v127\n"
"v_accvgpr_read_b32 v124, a212\n"
"s_nop 1\n"
"v_mul_f32_dpp v124, v166, v124 row_newbcast:0 row_mask:0xf bank_mask:0xf\n"
"v_accvgpr_write_b32 a212, v124\n"
"v_accvgpr_read_b32 v125, a213\n"
"s_nop 1\n"
"v_mul_f32_dpp v125, v166, v125 row_newbcast:1 row_mask:0xf bank_mask:0xf\n"
"v_accvgpr_write_b32 a213, v125\n"
"v_accvgpr_read_b32 v126, a214\n"
"s_nop 1\n"
"v_mul_f32_dpp v126, v166, v126 row_newbcast:2 row_mask:0xf bank_mask:0xf\n"
"v_accvgpr_write_b32 a214, v126\n"
"v_accvgpr_read_b32 v127, a215\n"
"s_nop 1\n"
"v_mul_f32_dpp v127, v166, v127 row_newbcast:3 row_mask:0xf bank_mask:0xf\n"
"v_accvgpr_write_b32 a215, v127\n"
"v_accvgpr_read_b32 v124, a216\n"
"s_nop 1\n"
"v_mul_f32_dpp v124, v166, v124 row_newbcast:0 row_mask:0xf bank_mask:0xf\n"
"v_accvgpr_write_b32 a216, v124\n"
"v_accvgpr_read_b32 v125, a217\n"
"s_nop 1\n"
"v_mul_f32_dpp v125, v166, v125 row_newbcast:1 row_mask:0xf bank_mask:0xf\n"
"v_accvgpr_write_b32 a217, v125\n"
"v_accvgpr_read_b32 v126, a218\n"
"s_nop 1\n"
"v_mul_f32_dpp v126, v166, v126 row_newbcast:2 row_mask:0xf bank_mask:0xf\n"
"v_accvgpr_write_b32 a218, v126\n"
"v_accvgpr_read_b32 v127, a219\n"
"s_nop 1\n"
"v_mul_f32_dpp v127, v166, v127 row_newbcast:3 row_mask:0xf bank_mask:0xf\n"
"v_accvgpr_write_b32 a219, v127\n"
"v_accvgpr_read_b32 v124, a220\n"
"s_nop 1\n"
"v_mul_f32_dpp v124, v166, v124 row_newbcast:0 row_mask:0xf bank_mask:0xf\n"
"v_accvgpr_write_b32 a220, v124\n"
"v_accvgpr_read_b32 v125, a221\n"
"s_nop 1\n"
"v_mul_f32_dpp v125, v166, v125 row_newbcast:1 row_mask:0xf bank_mask:0xf\n"
"v_accvgpr_write_b32 a221, v125\n"
"v_accvgpr_read_b32 v126, a222\n"
"s_nop 1\n"
"v_mul_f32_dpp v126, v166, v126 row_newbcast:2 row_mask:0xf bank_mask:0xf\n"
"v_accvgpr_write_b32 a222, v126\n"
"v_accvgpr_read_b32 v127, a223\n"
"s_nop 1\n"
"v_mul_f32_dpp v127, v166, v127 row_newbcast:3 row_mask:0xf bank_mask:0xf\n"
"v_accvgpr_write_b32 a223, v127\n"
"v_accvgpr_read_b32 v124, a224\n"
"s_nop 1\n"
"v_mul_f32_dpp v124, v166, v124 row_newbcast:0 row_mask:0xf bank_mask:0xf\n"
"v_accvgpr_write_b32 a224, v124\n"
"v_accvgpr_read_b32 v125, a225\n"
"s_nop 1\n"
"v_mul_f32_dpp v125, v166, v125 row_newbcast:1 row_mask:0xf bank_mask:0xf\n"
"v_accvgpr_write_b32 a225, v125\n"
"v_accvgpr_read_b32 v126, a226\n"
"s_nop 1\n"
"v_mul_f32_dpp v126, v166, v126 row_newbcast:2 row_mask:0xf bank_mask:0xf\n"
"v_accvgpr_write_b32 a226, v126\n"
"v_accvgpr_read_b32 v127, a227\n"
"s_nop 1\n"
"v_mul_f32_dpp v127, v166, v127 row_newbcast:3 row_mask:0xf bank_mask:0xf\n"
"v_accvgpr_write_b32 a227, v127\n"
"v_accvgpr_read_b32 v124, a228\n"
"s_nop 1\n"
"v_mul_f32_dpp v124, v166, v124 row_newbcast:0 row_mask:0xf bank_mask:0xf\n"
"v_accvgpr_write_b32 a228, v124\n"
"v_accvgpr_read_b32 v125, a229\n"
"s_nop 1\n"
"v_mul_f32_dpp v125, v166, v125 row_newbcast:1 row_mask:0xf bank_mask:0xf\n"
"v_accvgpr_write_b32 a229, v125\n"
"v_accvgpr_read_b32 v126, a230\n"
"s_nop 1\n"
"v_mul_f32_dpp v126, v166, v126 row_newbcast:2 row_mask:0xf bank_mask:0xf\n"
"v_accvgpr_write_b32 a230, v126\n"
"v_accvgpr_read_b32 v127, a231\n"
"s_nop 1\n"
"v_mul_f32_dpp v127, v166, v127 row_newbcast:3 row_mask:0xf bank_mask:0xf\n"
"v_accvgpr_write_b32 a231, v127\n"
"v_accvgpr_read_b32 v124, a232\n"
"s_nop 1\n"
"v_mul_f32_dpp v124, v166, v124 row_newbcast:0 row_mask:0xf bank_mask:0xf\n"
"v_accvgpr_write_b32 a232, v124\n"
"v_accvgpr_read_b32 v125, a233\n"
"s_nop 1\n"
"v_mul_f32_dpp v125, v166, v125 row_newbcast:1 row_mask:0xf bank_mask:0xf\n"
"v_accvgpr_write_b32 a233, v125\n"
"v_accvgpr_read_b32 v126, a234\n"
"s_nop 1\n"
"v_mul_f32_dpp v126, v166, v126 row_newbcast:2 row_mask:0xf bank_mask:0xf\n"
"v_accvgpr_write_b32 a234, v126\n"
"v_accvgpr_read_b32 v127, a235\n"
"s_nop 1\n"
"v_mul_f32_dpp v127, v166, v127 row_newbcast:3 row_mask:0xf bank_mask:0xf\n"
"v_accvgpr_write_b32 a235, v127\n"
"v_accvgpr_read_b32 v124, a236\n"
"s_nop 1\n"
"v_mul_f32_dpp v124, v166, v124 row_newbcast:0 row_mask:0xf bank_mask:0xf\n"
"v_accvgpr_write_b32 a236, v124\n"
"v_accvgpr_read_b32 v125, a237\n"
"s_nop 1\n"
"v_mul_f32_dpp v125, v166, v125 row_newbcast:1 row_mask:0xf bank_mask:0xf\n"
"v_accvgpr_write_b32 a237, v125\n"
"v_accvgpr_read_b32 v126, a238\n"
"s_nop 1\n"
"v_mul_f32_dpp v126, v166, v126 row_newbcast:2 row_mask:0xf bank_mask:0xf\n"
"v_accvgpr_write_b32 a238, v126\n"
"v_accvgpr_read_b32 v127, a239\n"
"s_nop 1\n"
"v_mul_f32_dpp v127, v166, v127 row_newbcast:3 row_mask:0xf bank_mask:0xf\n"
"v_accvgpr_write_b32 a239, v127\n"
"LSK3A_%=:\n"
"s_nop 3\n"
"buffer_load_ushort v128, v146, s[88:91], s80 offen offset:0\n"
"buffer_load_ushort v129, v146, s[88:91], s80 offen offset:256\n"
"buffer_load_ushort v130, v146, s[88:91], s80 offen offset:512\n"
"buffer_load_ushort v131, v146, s[88:91], s80 offen offset:768\n"
"buffer_load_ushort v132, v146, s[88:91], s81 offen offset:0\n"
"buffer_load_ushort v133, v146, s[88:91], s81 offen offset:256\n"
"buffer_load_ushort v134, v146, s[88:91], s81 offen offset:512\n"
"buffer_load_ushort v135, v146, s[88:91], s81 offen offset:768\n"
"buffer_load_ushort v136, v146, s[88:91], s80 offen offset:32\n"
"buffer_load_ushort v137, v146, s[88:91], s80 offen offset:288\n"
"buffer_load_ushort v138, v146, s[88:91], s80 offen offset:544\n"
"buffer_load_ushort v139, v146, s[88:91], s80 offen offset:800\n"
"buffer_load_ushort v140, v146, s[88:91], s81 offen offset:32\n"
"buffer_load_ushort v141, v146, s[88:91], s81 offen offset:288\n"
"buffer_load_ushort v142, v146, s[88:91], s81 offen offset:544\n"
"buffer_load_ushort v143, v146, s[88:91], s81 offen offset:800\n"
"s_waitcnt vmcnt(0)\n"
"v_lshl_or_b32 v128, v129, 16, v128\n"
"v_lshl_or_b32 v129, v131, 16, v130\n"
"v_lshl_or_b32 v130, v133, 16, v132\n"
"v_lshl_or_b32 v131, v135, 16, v134\n"
"v_lshl_or_b32 v136, v137, 16, v136\n"
"v_lshl_or_b32 v137, v139, 16, v138\n"
"v_lshl_or_b32 v138, v141, 16, v140\n"
"v_lshl_or_b32 v139, v143, 16, v142\n"
"v_mfma_f32_16x16x32_bf16 v[0:3], v[96:99], v[128:131], v[0:3]\n"
"s_nop 0\n"
"v_mfma_f32_16x16x32_bf16 v[4:7], v[96:99], v[136:139], v[4:7]\n"
"v_mfma_f32_16x16x32_bf16 v[32:35], v[104:107], v[128:131], v[32:35]\n"
"v_mfma_f32_16x16x32_bf16 v[36:39], v[104:107], v[136:139], v[36:39]\n"
"v_mfma_f32_16x16x32_bf16 v[64:67], v[112:115], v[128:131], v[64:67]\n"
"v_mfma_f32_16x16x32_bf16 v[68:71], v[112:115], v[136:139], v[68:71]\n"
"s_cmp_lg_u32 s83, s43\n"
"s_cbranch_scc1 LSK3B0_%=\n"
"v_mfma_f32_16x16x32_bf16 a[208:211], v[120:123], v[128:131], a[208:211]\n"
"v_mfma_f32_16x16x32_bf16 a[212:215], v[120:123], v[136:139], a[212:215]\n"
"LSK3B0_%=:\n"
"s_nop 3\n"
"buffer_load_ushort v128, v147, s[88:91], s80 offen offset:0\n"
"buffer_load_ushort v129, v147, s[88:91], s80 offen offset:256\n"
"buffer_load_ushort v130, v147, s[88:91], s80 offen offset:512\n"
"buffer_load_ushort v131, v147, s[88:91], s80 offen offset:768\n"
"buffer_load_ushort v132, v147, s[88:91], s81 offen offset:0\n"
"buffer_load_ushort v133, v147, s[88:91], s81 offen offset:256\n"
"buffer_load_ushort v134, v147, s[88:91], s81 offen offset:512\n"
"buffer_load_ushort v135, v147, s[88:91], s81 offen offset:768\n"
"buffer_load_ushort v136, v147, s[88:91], s80 offen offset:32\n"
"buffer_load_ushort v137, v147, s[88:91], s80 offen offset:288\n"
"buffer_load_ushort v138, v147, s[88:91], s80 offen offset:544\n"
"buffer_load_ushort v139, v147, s[88:91], s80 offen offset:800\n"
"buffer_load_ushort v140, v147, s[88:91], s81 offen offset:32\n"
"buffer_load_ushort v141, v147, s[88:91], s81 offen offset:288\n"
"buffer_load_ushort v142, v147, s[88:91], s81 offen offset:544\n"
"buffer_load_ushort v143, v147, s[88:91], s81 offen offset:800\n"
"s_waitcnt vmcnt(0)\n"
"v_lshl_or_b32 v128, v129, 16, v128\n"
"v_lshl_or_b32 v129, v131, 16, v130\n"
"v_lshl_or_b32 v130, v133, 16, v132\n"
"v_lshl_or_b32 v131, v135, 16, v134\n"
"v_lshl_or_b32 v136, v137, 16, v136\n"
"v_lshl_or_b32 v137, v139, 16, v138\n"
"v_lshl_or_b32 v138, v141, 16, v140\n"
"v_lshl_or_b32 v139, v143, 16, v142\n"
"v_mfma_f32_16x16x32_bf16 v[8:11], v[96:99], v[128:131], v[8:11]\n"
"s_nop 0\n"
"v_mfma_f32_16x16x32_bf16 v[12:15], v[96:99], v[136:139], v[12:15]\n"
"v_mfma_f32_16x16x32_bf16 v[40:43], v[104:107], v[128:131], v[40:43]\n"
"v_mfma_f32_16x16x32_bf16 v[44:47], v[104:107], v[136:139], v[44:47]\n"
"v_mfma_f32_16x16x32_bf16 v[72:75], v[112:115], v[128:131], v[72:75]\n"
"v_mfma_f32_16x16x32_bf16 v[76:79], v[112:115], v[136:139], v[76:79]\n"
"s_cmp_lg_u32 s83, s43\n"
"s_cbranch_scc1 LSK3B1_%=\n"
"v_mfma_f32_16x16x32_bf16 a[216:219], v[120:123], v[128:131], a[216:219]\n"
"v_mfma_f32_16x16x32_bf16 a[220:223], v[120:123], v[136:139], a[220:223]\n"
"LSK3B1_%=:\n"
"s_nop 3\n"
"buffer_load_ushort v128, v146, s[88:91], s80 offen offset:128\n"
"buffer_load_ushort v129, v146, s[88:91], s80 offen offset:384\n"
"buffer_load_ushort v130, v146, s[88:91], s80 offen offset:640\n"
"buffer_load_ushort v131, v146, s[88:91], s80 offen offset:896\n"
"buffer_load_ushort v132, v146, s[88:91], s81 offen offset:128\n"
"buffer_load_ushort v133, v146, s[88:91], s81 offen offset:384\n"
"buffer_load_ushort v134, v146, s[88:91], s81 offen offset:640\n"
"buffer_load_ushort v135, v146, s[88:91], s81 offen offset:896\n"
"buffer_load_ushort v136, v146, s[88:91], s80 offen offset:160\n"
"buffer_load_ushort v137, v146, s[88:91], s80 offen offset:416\n"
"buffer_load_ushort v138, v146, s[88:91], s80 offen offset:672\n"
"buffer_load_ushort v139, v146, s[88:91], s80 offen offset:928\n"
"buffer_load_ushort v140, v146, s[88:91], s81 offen offset:160\n"
"buffer_load_ushort v141, v146, s[88:91], s81 offen offset:416\n"
"buffer_load_ushort v142, v146, s[88:91], s81 offen offset:672\n"
"buffer_load_ushort v143, v146, s[88:91], s81 offen offset:928\n"
"s_waitcnt vmcnt(0)\n"
"v_lshl_or_b32 v128, v129, 16, v128\n"
"v_lshl_or_b32 v129, v131, 16, v130\n"
"v_lshl_or_b32 v130, v133, 16, v132\n"
"v_lshl_or_b32 v131, v135, 16, v134\n"
"v_lshl_or_b32 v136, v137, 16, v136\n"
"v_lshl_or_b32 v137, v139, 16, v138\n"
"v_lshl_or_b32 v138, v141, 16, v140\n"
"v_lshl_or_b32 v139, v143, 16, v142\n"
"v_mfma_f32_16x16x32_bf16 v[16:19], v[96:99], v[128:131], v[16:19]\n"
"s_nop 0\n"
"v_mfma_f32_16x16x32_bf16 v[20:23], v[96:99], v[136:139], v[20:23]\n"
"v_mfma_f32_16x16x32_bf16 v[48:51], v[104:107], v[128:131], v[48:51]\n"
"v_mfma_f32_16x16x32_bf16 v[52:55], v[104:107], v[136:139], v[52:55]\n"
"v_mfma_f32_16x16x32_bf16 v[80:83], v[112:115], v[128:131], v[80:83]\n"
"v_mfma_f32_16x16x32_bf16 v[84:87], v[112:115], v[136:139], v[84:87]\n"
"s_cmp_lg_u32 s83, s43\n"
"s_cbranch_scc1 LSK3B2_%=\n"
"v_mfma_f32_16x16x32_bf16 a[224:227], v[120:123], v[128:131], a[224:227]\n"
"v_mfma_f32_16x16x32_bf16 a[228:231], v[120:123], v[136:139], a[228:231]\n"
"LSK3B2_%=:\n"
"s_nop 3\n"
"buffer_load_ushort v128, v147, s[88:91], s80 offen offset:128\n"
"buffer_load_ushort v129, v147, s[88:91], s80 offen offset:384\n"
"buffer_load_ushort v130, v147, s[88:91], s80 offen offset:640\n"
"buffer_load_ushort v131, v147, s[88:91], s80 offen offset:896\n"
"buffer_load_ushort v132, v147, s[88:91], s81 offen offset:128\n"
"buffer_load_ushort v133, v147, s[88:91], s81 offen offset:384\n"
"buffer_load_ushort v134, v147, s[88:91], s81 offen offset:640\n"
"buffer_load_ushort v135, v147, s[88:91], s81 offen offset:896\n"
"buffer_load_ushort v136, v147, s[88:91], s80 offen offset:160\n"
"buffer_load_ushort v137, v147, s[88:91], s80 offen offset:416\n"
"buffer_load_ushort v138, v147, s[88:91], s80 offen offset:672\n"
"buffer_load_ushort v139, v147, s[88:91], s80 offen offset:928\n"
"buffer_load_ushort v140, v147, s[88:91], s81 offen offset:160\n"
"buffer_load_ushort v141, v147, s[88:91], s81 offen offset:416\n"
"buffer_load_ushort v142, v147, s[88:91], s81 offen offset:672\n"
"buffer_load_ushort v143, v147, s[88:91], s81 offen offset:928\n"
"s_waitcnt vmcnt(0)\n"
"v_lshl_or_b32 v128, v129, 16, v128\n"
"v_lshl_or_b32 v129, v131, 16, v130\n"
"v_lshl_or_b32 v130, v133, 16, v132\n"
"v_lshl_or_b32 v131, v135, 16, v134\n"
"v_lshl_or_b32 v136, v137, 16, v136\n"
"v_lshl_or_b32 v137, v139, 16, v138\n"
"v_lshl_or_b32 v138, v141, 16, v140\n"
"v_lshl_or_b32 v139, v143, 16, v142\n"
"v_mfma_f32_16x16x32_bf16 v[24:27], v[96:99], v[128:131], v[24:27]\n"
"s_nop 0\n"
"v_mfma_f32_16x16x32_bf16 v[28:31], v[96:99], v[136:139], v[28:31]\n"
"v_mfma_f32_16x16x32_bf16 v[56:59], v[104:107], v[128:131], v[56:59]\n"
"v_mfma_f32_16x16x32_bf16 v[60:63], v[104:107], v[136:139], v[60:63]\n"
"v_mfma_f32_16x16x32_bf16 v[88:91], v[112:115], v[128:131], v[88:91]\n"
"v_mfma_f32_16x16x32_bf16 v[92:95], v[112:115], v[136:139], v[92:95]\n"
"s_cmp_lg_u32 s83, s43\n"
"s_cbranch_scc1 LSK3B3_%=\n"
"v_mfma_f32_16x16x32_bf16 a[232:235], v[120:123], v[128:131], a[232:235]\n"
"v_mfma_f32_16x16x32_bf16 a[236:239], v[120:123], v[136:139], a[236:239]\n"
"LSK3B3_%=:\n"
"s_nop 3\n"
"s_nop 9\n"
"s_add_u32 s80, s80, 8192\n"
"s_add_u32 s81, s81, 8192\n"
"s_add_u32 s82, s82, 1\n"
"s_cmp_lt_u32 s82, 32\n"
"s_cbranch_scc1 LSLOOP_%=\n"
"v_mov_b32 v154, v155\n"
"s_nop 1\n"
"v_permlane32_swap_b32 v155, v154\n"
"v_add_f32 v155, v155, v154\n"
"v_mov_b32 v154, v155\n"
"s_nop 1\n"
"v_permlane16_swap_b32 v155, v154\n"
"v_add_f32 v155, v155, v154\n"
"v_mov_b32 v196, v155\n"
"v_mov_b32 v158, v159\n"
"s_nop 1\n"
"v_permlane32_swap_b32 v159, v158\n"
"v_add_f32 v159, v159, v158\n"
"v_mov_b32 v158, v159\n"
"s_nop 1\n"
"v_permlane16_swap_b32 v159, v158\n"
"v_add_f32 v159, v159, v158\n"
"v_mov_b32 v198, v159\n"
"v_mov_b32 v162, v163\n"
"s_nop 1\n"
"v_permlane32_swap_b32 v163, v162\n"
"v_add_f32 v163, v163, v162\n"
"v_mov_b32 v162, v163\n"
"s_nop 1\n"
"v_permlane16_swap_b32 v163, v162\n"
"v_add_f32 v163, v163, v162\n"
"v_mov_b32 v200, v163\n"
"v_mov_b32 v166, v167\n"
"s_nop 1\n"
"v_permlane32_swap_b32 v167, v166\n"
"v_add_f32 v167, v167, v166\n"
"v_mov_b32 v166, v167\n"
"s_nop 1\n"
"v_permlane16_swap_b32 v167, v166\n"
"v_add_f32 v167, v167, v166\n"
"v_mov_b32 v202, v167\n"
"v_sub_f32 v192, 0, v164\n"
"v_mov_b32 v96, 0\n"
"v_mov_b32 v97, 0\n"
"v_mov_b32 v98, 0\n"
"v_mov_b32 v99, 0\n"
"v_mov_b32 v100, 0\n"
"v_mov_b32 v101, 0\n"
"v_mov_b32 v102, 0\n"
"v_mov_b32 v103, 0\n"
"v_mov_b32 v104, 0\n"
"v_mov_b32 v105, 0\n"
"v_mov_b32 v106, 0\n"
"v_mov_b32 v107, 0\n"
"v_mov_b32 v108, 0\n"
"v_mov_b32 v109, 0\n"
"v_mov_b32 v110, 0\n"
"v_mov_b32 v111, 0\n"
"v_mov_b32 v112, 0\n"
"v_mov_b32 v113, 0\n"
"v_mov_b32 v114, 0\n"
"v_mov_b32 v115, 0\n"
"v_mov_b32 v116, 0\n"
"v_mov_b32 v117, 0\n"
"v_mov_b32 v118, 0\n"
"v_mov_b32 v119, 0\n"
"v_mov_b32 v120, 0\n"
"v_mov_b32 v121, 0\n"
"v_mov_b32 v122, 0\n"
"v_mov_b32 v123, 0\n"
"v_mov_b32 v204, 0\n"
"v_mov_b32 v205, 0\n"
"s_nop 3\n"
"s_mov_b32 s68, 0\n"
"s_cmp_eq_u32 s47, 4\n"
"s_cbranch_scc1 LBACK0_%=\n"
"s_cmp_eq_u32 s47, 8\n"
"s_cbranch_scc1 LBACK1_%=\n"
"s_cmp_eq_u32 s47, 12\n"
"s_cbranch_scc1 LBACK2_%=\n"
"s_branch LBACK3_%=\n"
"LFIN_%=:\n"
    :: [kalo]"s"((unsigned)ka), [kahi]"s"((unsigned)(ka >> 32)), [wg]"s"(blockIdx.x), [wid]"s"(wid)
    : "v0","v1","v2","v3","v4","v5","v6","v7","v8","v9","v10","v11","v12","v13","v14","v15","v16","v17","v18","v19","v20","v21","v22","v23","v24","v25","v26","v27","v28","v29","v30","v31","v32","v33","v34","v35","v36","v37","v38","v39","v40","v41","v42","v43","v44","v45","v46","v47","v48","v49","v50","v51","v52","v53","v54","v55","v56","v57","v58","v59","v60","v61","v62","v63","v64","v65","v66","v67","v68","v69","v70","v71","v72","v73","v74","v75","v76","v77","v78","v79","v80","v81","v82","v83","v84","v85","v86","v87","v88","v89","v90","v91","v92","v93","v94","v95","v96","v97","v98","v99","v100","v101","v102","v103","v104","v105","v106","v107","v108","v109","v110","v111","v112","v113","v114","v115","v116","v117","v118","v119","v120","v121","v122","v123","v124","v125","v126","v127","v128","v129","v130","v131","v132","v133","v134","v135","v136","v137","v138","v139","v140","v141","v142","v143","v144","v145","v146","v147","v148","v149","v150","v151","v152","v153","v154","v155","v156","v157","v158","v159","v160","v161","v162","v163","v164","v165","v166","v167","v168","v169","v170","v171","v172","v173","v174","v175","v176","v177","v178","v179","v180","v181","v182","v183","v184","v185","v186","v187","v188","v189","v190","v191","v192","v193","v194","v195","v196","v197","v198","v199","v200","v201","v202","v203","v204","v205","v206","v207","v208","v209","v210","v211","v212","v213","v214","v215","v216","v217","v218","v219","v220","v221","v222","v223","v224","v225","v226","v227","v228","v229","v230","v231","v232","v233","v234","v235","v236","v237","v238","v239","v240","v241","v242","v243","v244","v245","v246","v247","v248","v249","v250","v251","v252","v253","v254","v255","a0","a1","a2","a3","a4","a5","a6","a7","a8","a9","a10","a11","a12","a13","a14","a15","a16","a17","a18","a19","a20","a21","a22","a23","a24","a25","a26","a27","a28","a29","a30","a31","a32","a33","a34","a35","a36","a37","a38","a39","a40","a41","a42","a43","a44","a45","a46","a47","a48","a49","a50","a51","a52","a53","a54","a55","a56","a57","a58","a59","a60","a61","a62","a63","a64","a65","a66","a67","a68","a69","a70","a71","a72","a73","a74","a75","a76","a77","a78","a79","a80","a81","a82","a83","a84","a85","a86","a87","a88","a89","a90","a91","a92","a93","a94","a95","a96","a97","a98","a99","a100","a101","a102","a103","a104","a105","a106","a107","a108","a109","a110","a111","a112","a113","a114","a115","a116","a117","a118","a119","a120","a121","a122","a123","a124","a125","a126","a127","a128","a129","a130","a131","a132","a133","a134","a135","a136","a137","a138","a139","a140","a141","a142","a143","a144","a145","a146","a147","a148","a149","a150","a151","a152","a153","a154","a155","a156","a157","a158","a159","a160","a161","a162","a163","a164","a165","a166","a167","a168","a169","a170","a171","a172","a173","a174","a175","a176","a177","a178","a179","a180","a181","a182","a183","a184","a185","a186","a187","a188","a189","a190","a191","a192","a193","a194","a195","a196","a197","a198","a199","a200","a201","a202","a203","a204","a205","a206","a207","a208","a209","a210","a211","a212","a213","a214","a215","a216","a217","a218","a219","a220","a221","a222","a223","a224","a225","a226","a227","a228","a229","a230","a231","a232","a233","a234","a235","a236","a237","a238","a239","a240","a241","a242","a243","a244","a245","a246","a247","a248","a249","a250","a251","a252","a253","a254","a255","s6","s7","s8","s9","s10","s11","s12","s13","s14","s15","s16","s17","s18","s19","s20","s21","s22","s23","s24","s25","s26","s27","s28","s29","s30","s31","s32","s33","s34","s35","s36","s37","s38","s39","s40","s41","s42","s43","s44","s45","s46","s47","s48","s49","s50","s51","s52","s53","s54","s55","s56","s57","s58","s59","s60","s61","s62","s63","s64","s65","s66","s67","s68","s69","s70","s71","s72","s73","s74","s75","s76","s77","s78","s79","s80","s81","s82","s83","s84","s85","s86","s87","s88","s89","s90","s91","s92","s93","s94","s95","s96","s97","s98","s99","s100","vcc","scc","m0","memory");
}
extern "C" void kernel_launch(void* const* d_in, const int* in_sizes, int n_in, void* d_out, int out_size, void* d_ws, size_t ws_size, hipStream_t stream) {
  char* xch = (char*)d_ws + ((ws_size - (size_t)270532608) & ~(size_t)0xfffff);
  (void)hipFuncSetAttribute((const void*)fused_attn, hipFuncAttributeMaxDynamicSharedMemorySize, 163840);
  hipLaunchKernelGGL(fused_attn, dim3(256), dim3(256), 163840, stream,
                     (const char*)d_in[0], (const char*)d_in[1], (const char*)d_in[2], (const char*)d_in[3], (const char*)d_in[4],
                     (const char*)d_in[5], (const char*)d_in[6], (const char*)d_in[7], (const char*)d_in[8], (const char*)d_in[9], xch, (char*)d_out, (char*)d_ws);
}
```

```cpp
#include <hip/hip_runtime.h>
#include <hip/hip_bf16.h>
#include <stdint.h>

__global__ __launch_bounds__(256, 1)
void fused_attn(const char* xA, const char* kA, const char* vA, const char* wA, const char* uA,
                const char* xB, const char* kB, const char* vB, const char* wB, const char* uB, char* xch, char* out, char* flags) {
  extern __shared__ __attribute__((aligned(16))) char lds[];
  const unsigned wid = __builtin_amdgcn_readfirstlane(threadIdx.x >> 6);
  const unsigned long long ka = (unsigned long long)__builtin_amdgcn_kernarg_segment_ptr();
  asm volatile(
"s_memtime s[64:65]\n"
"s_memrealtime s[66:67]\n"
"s_bfe_u32 s10, %[wg], 0x10002\n"
"s_mul_i32 s40, s10, 40\n"
"s_add_u32 s40, s40, %[kalo]\n"
"s_addc_u32 s41, %[kahi], 0\n"
"s_mov_b32 s78, %[kalo]\n"
"s_mov_b32 s79, %[kahi]\n"
"s_load_dwordx2 s[24:25], s[40:41], 0\n"
"s_load_dwordx2 s[28:29], s[40:41], 24\n"
"s_load_dwordx2 s[6:7], s[78:79], 0x60\n"
"s_load_dwordx2 s[16:17], s[40:41], 8\n"
"s_load_dwordx2 s[20:21], s[40:41], 16\n"
"s_load_dwordx2 s[8:9], s[40:41], 32\n"
"s_load_dwordx4 s[12:15], s[78:79], 0x50\n"
"s_mov_b32 s42, %[wid]\n"
"s_and_b32 s11, %[wg], 1\n"
"s_lshl_b32 s11, s11, 3\n"
"s_lshr_b32 s60, %[wg], 5\n"
"s_add_u32 s11, s11, s60\n"
"s_bfe_u32 s61, %[wg], 0x10001\n"
"s_bfe_u32 s60, %[wg], 0x20003\n"
"s_lshl2_add_u32 s61, s61, s60\n"
"s_getpc_b64 s[80:81]\n"
"LCP_%=:\n"
"s_mov_b32 s82, LEND_%=-LCP_%=\n"
"s_mov_b32 s83, 0x20000\n"
"v_mbcnt_lo_u32_b32 v2, -1, 0\n"
"v_lshlrev_b32 v2, 7, v2\n"
"s_lshr_b32 s60, %[wg], 3\n"
"s_lshl2_add_u32 s60, s60, s42\n"
"s_mul_i32 s60, s60, 640\n"
"s_mov_b64 exec, 31\n"
"buffer_load_dword v3, v2, s[80:83], s60 offen\n"
"s_mov_b64 exec, -1\n"
"s_mov_b32 s19, 0x20000\n"
"s_mov_b32 s23, 0x20000\n"
"s_mov_b32 s27, 0x20000\n"
"s_mov_b32 s31, 0x20000\n"
"s_mov_b32 s35, 0x20000\n"
"s_mov_b32 s39, 0x20000\n"
"s_mov_b32 s26, 286720\n"
"s_lshl_b32 s44, s42, 12\n"
"s_mov_b32 s50, s44\n"
"s_mul_i32 s51, s42, 7168\n"
"s_add_u32 s51, s51, 0x10000\n"
"s_mov_b32 s74, 0x8000\n"
"s_mov_b32 s58, 524288\n"
"s_mov_b32 s47, 0\n"
"s_add_u32 s60, s11, s61\n"
"s_and_b32 s60, s60, 0\n"
"s_mul_i32 s60, s60, 3\n"
"s_lshl_b32 s49, s60, 7\n"
"s_lshl_b32 s48, s60, 19\n"
"v_mbcnt_lo_u32_b32 v4, -1, 0\n"
"v_mbcnt_hi_u32_b32 v4, -1, v4\n"
"v_and_b32 v5, 15, v4\n"
"v_lshrrev_b32 v6, 4, v4\n"
"v_lshrrev_b32 v7, 3, v4\n"
"v_and_b32 v8, 7, v4\n"
"v_lshlrev_b32 v9, 1, v6\n"
"v_xor_b32 v9, v9, v8\n"
"v_lshlrev_b32 v9, 4, v9\n"
"s_mul_i32 s60, s42, 71680\n"
"v_mul_u32_u24 v10, 1280, v7\n"
"v_add3_u32 v248, v10, v9, s60\n"
"v_add_u32 v249, 9216, v248\n"
"v_add_u32 v254, 18432, v248\n"
"v_add_u32 v255, 27648, v248\n"
"v_lshl_add_u32 v240, v7, 8, v9\n"
"v_add_u32 v240, s44, v240\n"
"v_and_b32 v10, 1, v6\n"
"v_xor_b32 v10, v10, v8\n"
"v_lshlrev_b32 v10, 4, v10\n"
"v_lshl_add_u32 v10, v7, 13, v10\n"
"s_lshl_b32 s60, s42, 17\n"
"v_add_u32 v10, s60, v10\n"
"v_add_u32 v224, 1024, v10\n"
"v_add_u32 v225, 128, v10\n"
"v_xor_b32 v10, 64, v10\n"
"v_add_u32 v226, 64512, v10\n"
"v_add_u32 v227, 63616, v10\n"
"s_mul_i32 s40, s11, 286720\n"
"s_lshl_b32 s41, s61, 10\n"
"s_sub_u32 s30, 5243904, s41\n"
"s_sub_u32 s41, s41, 1024\n"
"s_ashr_i32 s60, s41, 31\n"
"s_lshl4_add_u32 s92, s10, s11\n"
"s_lshl_b32 s93, s92, 9\n"
"v_mov_b32 v0, 0\n"
"s_waitcnt lgkmcnt(0)\n"
"s_add_u32 s24, s24, s40\n"
"s_addc_u32 s25, s25, 0\n"
"s_and_b32 s25, s25, 0xffff\n"
"s_add_u32 s28, s28, s41\n"
"s_addc_u32 s29, s29, s60\n"
"s_and_b32 s29, s29, 0xffff\n"
"s_add_u32 s6, s6, s93\n"
"s_addc_u32 s7, s7, 0\n"
"buffer_load_dword v12, v248, s[24:27], s49 offen\n"
"buffer_load_dword v13, v224, s[28:31], s48 offen\n"
"global_load_dword v1, v0, s[6:7] offset:128 sc1\n"
"s_mov_b32 m0, s51\n"
"s_nop 0\n"
"buffer_load_dwordx4 v248, s[24:27], s49 offen lds\n"
"buffer_load_dwordx4 v249, s[24:27], s49 offen offset:1024 lds\n"
"buffer_load_dwordx4 v254, s[24:27], s49 offen offset:2048 lds\n"
"buffer_load_dwordx4 v255, s[24:27], s49 offen offset:3072 lds\n"
"s_add_u32 m0, s51, 4096\n"
"s_add_u32 s40, s49, 40960\n"
"buffer_load_dwordx4 v248, s[24:27], s40 offen lds\n"
"buffer_load_dwordx4 v249, s[24:27], s40 offen offset:1024 lds\n"
"buffer_load_dwordx4 v254, s[24:27], s40 offen offset:2048 lds\n"
"s_add_u32 s49, s49, 128\n"
"s_sub_u32 s41, s49, 1280\n"
"s_min_u32 s49, s49, s41\n"
"s_xor_b32 s51, s51, s74\n"
"s_mov_b32 m0, s50\n"
"s_nop 0\n"
"buffer_load_dwordx4 v224, s[28:31], s48 offen lds\n"
"buffer_load_dwordx4 v225, s[28:31], s48 offen offset:1024 lds\n"
"buffer_load_dwordx4 v226, s[28:31], s48 offen offset:2048 lds\n"
"buffer_load_dwordx4 v227, s[28:31], s48 offen offset:3072 lds\n"
"s_xor_b32 s50, s50, s74\n"
"s_lshl2_add_u32 s63, s11, s42\n"
"s_mul_i32 s63, s63, 10240\n"
"s_and_b32 s78, s42, 1\n"
"s_lshl_b32 s43, s78, 6\n"
"s_mov_b32 s77, 0x80000000\n"
"s_mov_b32 s75, 0x10000\n"
"s_mov_b32 s73, 0xffff0000\n"
"s_mov_b32 s46, 0x3e0293ee\n"
"s_mov_b32 s45, 0x63800000\n"
"s_mov_b32 s62, 9\n"
"v_lshl_add_u32 v242, v4, 4, s44\n"
"s_lshr_b32 s79, s42, 1\n"
"s_lshl4_add_u32 s76, s79, s78\n"
"s_lshl_b32 s76, s76, 10\n"
"s_add_u32 s76, s76, 0x20000\n"
"s_sub_u32 s76, s76, s44\n"
"v_lshlrev_b32 v9, 6, v6\n"
"v_xor_b32 v9, v9, v242\n"
"v_add_u32 v243, s76, v9\n"
"v_and_b32 v9, 6, v5\n"
"s_xor_b32 m0, s50, 0xc000\n"
"s_add_u32 s40, s48, 256\n"
"buffer_load_dwordx4 v224, s[28:31], s40 offen lds\n"
"v_xor_b32 v9, v9, v6\n"
"v_lshlrev_b32 v9, 4, v9\n"
"v_lshl_add_u32 v10, v5, 7, v9\n"
"s_mul_i32 s79, s42, 6144\n"
"s_add_u32 s79, s79, 0x10000\n"
"v_add_u32 v246, s79, v10\n"
"v_xor_b32 v247, 64, v246\n"
"s_lshr_b32 s79, s42, 1\n"
"s_lshl_b32 s79, s79, 11\n"
"buffer_load_dwordx4 v225, s[28:31], s40 offen offset:1024 lds\n"
"s_add_u32 s79, s79, 90112\n"
"v_add_u32 v252, s79, v10\n"
"v_xor_b32 v253, 64, v252\n"
"s_xor_b32 s79, s42, 1\n"
"s_mul_i32 s79, s79, 6144\n"
"s_add_u32 s79, s79, 0x10000\n"
"v_add_u32 v250, s79, v10\n"
"v_xor_b32 v251, 64, v250\n"
"s_mul_i32 s79, s78, 8256\n"
"buffer_load_dwordx4 v226, s[28:31], s40 offen offset:2048 lds\n"
"v_lshrrev_b32 v9, 5, v4\n"
"v_lshlrev_b32 v9, 11, v9\n"
"v_bfe_u32 v10, v4, 2, 3\n"
"v_lshl_add_u32 v9, v10, 7, v9\n"
"v_and_b32 v10, 3, v4\n"
"v_lshl_add_u32 v9, v10, 3, v9\n"
"v_bfe_u32 v10, v4, 3, 2\n"
"v_lshl_add_u32 v9, v10, 5, v9\n"
"s_add_u32 s79, s79, 0x10000\n"
"buffer_load_dwordx4 v227, s[28:31], s40 offen offset:3072 lds\n"
"s_cmp_eq_u32 s47, 8\n"
"s_cselect_b32 s41, s52, s58\n"
"s_add_u32 s48, s48, s41\n"
"s_sub_u32 s41, s48, 5242880\n"
"s_min_u32 s48, s48, s41\n"
"v_xor_b32 v232, s79, v9\n"
"v_xor_b32 v233, 32, v232\n"
"v_xor_b32 v234, 64, v232\n"
"v_xor_b32 v235, 96, v232\n"
"v_xor_b32 v236, 0x2000, v232\n"
"v_xor_b32 v237, 0x2000, v233\n"
"v_xor_b32 v238, 0x2000, v234\n"
"v_xor_b32 v239, 0x2000, v235\n"
"v_lshlrev_b32 v9, 11, v6\n"
"s_mov_b32 m0, s50\n"
"s_nop 0\n"
"buffer_load_dwordx4 v224, s[28:31], s48 offen lds\n"
"v_bfe_u32 v10, v4, 2, 2\n"
"v_lshl_add_u32 v9, v10, 7, v9\n"
"v_and_b32 v10, 1, v4\n"
"v_lshl_add_u32 v9, v10, 3, v9\n"
"v_bfe_u32 v10, v4, 1, 1\n"
"v_lshl_add_u32 v9, v10, 5, v9\n"
"v_bfe_u32 v10, v4, 3, 1\n"
"v_lshl_add_u32 v9, v10, 4, v9\n"
"v_bfe_u32 v10, v4, 4, 1\n"
"buffer_load_dwordx4 v225, s[28:31], s48 offen offset:1024 lds\n"
"v_lshl_add_u32 v9, v10, 6, v9\n"
"v_xor_b32 v244, s43, v9\n"
"v_xor_b32 v245, 16, v244\n"
"s_lshl3_add_u32 s79, s11, s61\n"
"s_lshl_b32 s79, s79, 20\n"
"s_sub_u32 s79, s79, 896\n"
"s_ashr_i32 s60, s79, 31\n"
"s_add_u32 s16, s16, s79\n"
"s_addc_u32 s17, s17, s60\n"
"buffer_load_dwordx4 v226, s[28:31], s48 offen offset:2048 lds\n"
"s_and_b32 s17, s17, 0xffff\n"
"s_mov_b32 s18, 1049472\n"
"s_mov_b64 s[84:85], s[16:17]\n"
"s_mov_b64 s[86:87], s[18:19]\n"
"s_add_u32 s20, s20, s79\n"
"s_addc_u32 s21, s21, s60\n"
"s_and_b32 s21, s21, 0xffff\n"
"s_mov_b32 s22, 1049472\n"
"s_mov_b64 s[88:89], s[20:21]\n"
"buffer_load_dwordx4 v227, s[28:31], s48 offen offset:3072 lds\n"
"s_xor_b32 s50, s50, s74\n"
"s_mov_b64 s[90:91], s[22:23]\n"
"s_lshr_b32 s57, %[wg], 3\n"
"s_mul_i32 s57, s57, 5\n"
"s_and_b32 s57, s57, 15\n"
"s_lshl_b32 s57, s57, 14\n"
"s_mov_b32 s55, 896\n"
"s_add_u32 s56, s57, 896\n"
"s_mov_b32 s59, 16384\n"
"s_mov_b32 s54, -896\n"
"s_mov_b32 m0, s51\n"
"s_nop 0\n"
"buffer_load_dwordx4 v248, s[24:27], s49 offen lds\n"
"s_add_u32 s52, s11, s61\n"
"s_and_b32 s52, s52, 0\n"
"s_mul_i32 s52, s52, 3\n"
"s_add_u32 s52, s52, 1\n"
"s_lshl_b32 s53, s52, 7\n"
"s_lshl_b32 s52, s52, 19\n"
"s_add_u32 s52, s52, 512\n"
"s_mul_i32 s79, s61, 655360\n"
"s_add_u32 s8, s8, s79\n"
"s_addc_u32 s9, s9, 0\n"
"buffer_load_dwordx4 v249, s[24:27], s49 offen offset:1024 lds\n"
"s_and_b32 s9, s9, 0xffff\n"
"s_add_u32 s12, s12, 0x1fffff\n"
"s_addc_u32 s13, s13, 0\n"
"s_and_b32 s12, s12, 0xffe00000\n"
"s_mul_i32 s96, s42, 3\n"
"s_and_b32 s96, s96, 5\n"
"s_cmp_eq_u32 s42, 3\n"
"s_cselect_b32 s96, 5, s96\n"
"buffer_load_dwordx4 v254, s[24:27], s49 offen offset:2048 lds\n"
"s_lshl_b32 s79, s61, 8\n"
"s_add_u32 s96, s96, s79\n"
"s_add_u32 s96, s96, 160\n"
"s_lshl_b32 s96, s96, 12\n"
"s_mul_i32 s79, s92, 8388608\n"
"s_add_u32 s32, s12, s79\n"
"s_addc_u32 s33, s13, 0\n"
"s_and_b32 s33, s33, 0xffff\n"
"s_mov_b32 s34, 8388608\n"
"buffer_load_dwordx4 v255, s[24:27], s49 offen offset:3072 lds\n"
"s_mul_i32 s79, s11, 573440\n"
"s_mul_i32 s60, s10, 1280\n"
"s_sub_u32 s38, 573440, s60\n"
"s_add_u32 s79, s79, s60\n"
"s_add_u32 s36, s14, s79\n"
"s_addc_u32 s37, s15, 0\n"
"s_and_b32 s37, s37, 0xffff\n"
"s_lshl2_add_u32 s93, s61, s42\n"
"s_add_u32 s95, s61, 1\n"
"s_add_u32 m0, s51, 4096\n"
"s_add_u32 s40, s49, 40960\n"
"buffer_load_dwordx4 v248, s[24:27], s40 offen lds\n"
"s_and_b32 s95, s95, 7\n"
"s_lshl_b32 s95, s95, 7\n"
"s_add_u32 s100, s61, 2\n"
"s_and_b32 s100, s100, 7\n"
"s_lshl_b32 s100, s100, 7\n"
"s_lshl_b32 s60, s61, 7\n"
"s_add_u32 s60, s60, s43\n"
"s_mul_i32 s97, s42, 122880\n"
"s_add_u32 s97, s97, s60\n"
"buffer_load_dwordx4 v249, s[24:27], s40 offen offset:1024 lds\n"
"s_lshr_b32 s98, s42, 1\n"
"s_add_u32 s98, s98, 12\n"
"s_mul_i32 s98, s98, 40960\n"
"s_add_u32 s98, s98, s60\n"
"s_mul_i32 s60, s93, 37\n"
"s_lshr_b32 s60, s60, 8\n"
"s_mul_i32 s79, s60, 7\n"
"s_sub_u32 s79, s93, s79\n"
"s_lshl4_add_u32 s94, s60, s79\n"
"buffer_load_dwordx4 v254, s[24:27], s40 offen offset:2048 lds\n"
"s_add_u32 s49, s49, 128\n"
"s_sub_u32 s41, s49, 1280\n"
"s_min_u32 s49, s49, s41\n"
"s_xor_b32 s51, s51, s74\n"
"s_lshl_b32 s94, s94, 10\n"
"s_add_u32 s94, s94, 524288\n"
"s_mul_i32 s99, s60, 3\n"
"s_lshr_b32 s78, s79, 1\n"
"s_add_u32 s99, s99, s78\n"
"s_lshr_b32 s78, s60, 1\n"
"s_add_u32 s78, s78, 12\n"
"s_cmp_eq_u32 s79, 6\n"
"s_cselect_b32 s99, s78, s99\n"
"s_mul_i32 s99, s99, 40960\n"
"s_xor_b32 s79, s79, s60\n"
"s_and_b32 s79, s79, 1\n"
"s_lshl_b32 s79, s79, 6\n"
"s_add_u32 s99, s99, s79\n"
"s_add_u32 s99, s99, 1024\n"
"s_cmp_eq_u32 s61, 7\n"
"s_cselect_b32 s99, 0x40000000, s99\n"
"s_cselect_b32 s94, 0x40000000, s94\n"
"s_waitcnt vmcnt(27)\n"
"v_and_b32 v12, 0x7fff0000, v12\n"
"v_and_b32 v13, 0x7fff0000, v13\n"
"s_nop 0\n"
"v_max_u32_dpp v12, v12, v12 quad_perm:[1,0,3,2] row_mask:0xf bank_mask:0xf\n"
"v_max_u32_dpp v13, v13, v13 quad_perm:[1,0,3,2] row_mask:0xf bank_mask:0xf\n"
"s_nop 0\n"
"v_max_u32_dpp v12, v12, v12 quad_perm:[2,3,0,1] row_mask:0xf bank_mask:0xf\n"
"v_max_u32_dpp v13, v13, v13 quad_perm:[2,3,0,1] row_mask:0xf bank_mask:0xf\n"
"s_memtime s[80:81]\n"
"s_memrealtime s[82:83]\n"
"s_nop 0\n"
"v_max_u32_dpp v12, v12, v12 row_half_mirror row_mask:0xf bank_mask:0xf\n"
"v_max_u32_dpp v13, v13, v13 row_half_mirror row_mask:0xf bank_mask:0xf\n"
"s_nop 0\n"
"v_min_u32_dpp v12, v12, v12 row_mirror row_mask:0xf bank_mask:0xf\n"
"v_min_u32_dpp v13, v13, v13 row_mirror row_mask:0xf bank_mask:0xf\n"
"s_nop 0\n"
"v_min_u32_dpp v12, v12, v12 row_bcast:15 row_mask:0xa bank_mask:0xf\n"
"v_min_u32_dpp v13, v13, v13 row_bcast:15 row_mask:0xa bank_mask:0xf\n"
"s_nop 0\n"
"v_min_u32_dpp v12, v12, v12 row_bcast:31 row_mask:0xc bank_mask:0xf\n"
"v_min_u32_dpp v13, v13, v13 row_bcast:31 row_mask:0xc bank_mask:0xf\n"
"s_nop 0\n"
"v_mul_f32 v12, v12, v13\n"
"v_mul_f32 v12, 0x43c80000, v12\n"
"v_min_f32 v12, 0x71800000, v12\n"
"v_mul_f32 v13, s46, v12\n"
"s_nop 0\n"
"v_readlane_b32 s70, v12, 63\n"
"v_readlane_b32 s71, v12, 63\n"
"v_readlane_b32 s69, v13, 63\n"
"s_nop 1\n"
"v_mov_b64 v[228:229], s[70:71]\n"
"v_mov_b64 v[230:231], s[70:71]\n"
"s_waitcnt vmcnt(26)\n"
"v_readfirstlane_b32 s92, v1\n"
"v_mov_b64 v[0:1], s[70:71]\n"
"v_mov_b64 v[2:3], s[70:71]\n"
"s_mov_b32 s70, s46\n"
"s_xor_b32 s71, s69, s77\n"
"s_waitcnt lgkmcnt(0)\n"
"s_sub_u32 s80, s80, s64\n"
"s_sub_u32 s82, s82, s66\n"
"s_mul_i32 s80, s80, 10\n"
"s_mul_i32 s82, s82, 211\n"
"s_cmp_gt_u32 s80, s82\n"
"s_cselect_b32 s64, 1, 0\n"
"s_cselect_b32 s68, 655360, 0\n"
"s_cmp_eq_u32 s42, 0\n"
"s_cselect_b32 s64, s64, 0\n"
"s_mov_b32 s65, 0\n"
"s_add_u32 s66, s66, 2800\n"
"s_mov_b32 s67, s66\n"
"s_mov_b32 s72, 122\n"
"s_mov_b64 vcc, s[64:65]\n"
"s_sub_u32 s63, s63, s44\n"
"s_add_u32 s80, s8, s63\n"
"s_addc_u32 s81, s9, 0\n"
"s_add_u32 s82, s44, 704\n"
"s_cmp_lg_u32 s68, 0\n"
"s_cselect_b32 s82, s82, 0\n"
"s_mov_b32 s83, 0x20000\n"
"s_nop 2\n"
"s_mul_i32 s60, s92, 0x9e3779b1\n"
"s_add_u32 s60, s60, 0x7f4a7c15\n"
"s_waitcnt vmcnt(15)\n"
"s_barrier\n"
"ds_read_b128 a[136:139], v246 offset:0\n"
"ds_read_b128 a[140:143], v247 offset:0\n"
"ds_read_b64_tr_b16 a[192:193], v244 offset:0\n"
"ds_read_b64_tr_b16 a[194:195], v244 offset:512\n"
"ds_read_b64_tr_b16 a[196:197], v244 offset:8192\n"
"ds_read_b64_tr_b16 a[198:199], v244 offset:8704\n"
"ds_read_b64_tr_b16 a[200:201], v245 offset:0\n"
"ds_read_b64_tr_b16 a[202:203], v245 offset:512\n"
"ds_read_b64_tr_b16 a[204:205], v245 offset:8192\n"
"ds_read_b64_tr_b16 a[206:207], v245 offset:8704\n"
"ds_read_b64_tr_b16 a[208:209], v244 offset:1024\n"
"ds_read_b64_tr_b16 a[210:211], v244 offset:1536\n"
"ds_read_b64_tr_b16 a[212:213], v244 offset:9216\n"
"ds_read_b64_tr_b16 a[214:215], v244 offset:9728\n"
"s_waitcnt lgkmcnt(10)\n"
"v_mfma_f32_16x16x32_bf16 v[112:115], a[192:195], a[136:139], v[228:231]\n"
"ds_read_b64_tr_b16 a[216:217], v245 offset:1024\n"
"ds_read_b64_tr_b16 a[218:219], v245 offset:1536\n"
"s_waitcnt lgkmcnt(10)\n"
"v_mfma_f32_16x16x32_bf16 v[112:115], a[196:199], a[140:143], v[112:115]\n"
"ds_read_b64_tr_b16 a[220:221], v245 offset:9216\n"
"ds_read_b64_tr_b16 a[222:223], v245 offset:9728\n"
"s_waitcnt lgkmcnt(10)\n"
"v_mfma_f32_16x16x32_bf16 v[116:119], a[200:203], a[136:139], v[228:231]\n"
"s_waitcnt lgkmcnt(8)\n"
"ds_read_b128 a[144:147], v246 offset:2048\n"
"v_mfma_f32_16x16x32_bf16 v[116:119], a[204:207], a[140:143], v[116:119]\n"
"s_waitcnt lgkmcnt(7)\n"
"ds_read_b128 a[148:151], v247 offset:2048\n"
"v_mfma_f32_16x16x32_bf16 v[120:123], a[208:211], a[136:139], v[228:231]\n"
"s_waitcnt lgkmcnt(6)\n"
"ds_read_b128 a[152:155], v246 offset:4096\n"
"v_mfma_f32_16x16x32_bf16 v[120:123], a[212:215], a[140:143], v[120:123]\n"
"s_waitcnt lgkmcnt(5)\n"
"ds_read_b128 a[156:159], v247 offset:4096\n"
"v_mfma_f32_16x16x32_bf16 v[124:127], a[216:219], a[136:139], v[228:231]\n"
"s_waitcnt lgkmcnt(4)\n"
"ds_read_b128 a[160:163], v252 offset:0\n"
"v_mfma_f32_16x16x32_bf16 v[124:127], a[220:223], a[140:143], v[124:127]\n"
"s_waitcnt lgkmcnt(4)\n"
"ds_read_b128 a[164:167], v253 offset:0\n"
"v_mfma_f32_16x16x32_bf16 v[140:143], a[216:219], a[144:147], v[228:231]\n"
"s_waitcnt lgkmcnt(4)\n"
"ds_read_b128 a[168:171], v250 offset:0\n"
"v_mfma_f32_16x16x32_bf16 v[140:143], a[220:223], a[148:151], v[140:143]\n"
"ds_read_b128 a[172:175], v251 offset:0\n"
"v_mfma_f32_16x16x32_bf16 v[136:139], a[208:211], a[144:147], v[228:231]\n"
"s_waitcnt vmcnt(11)\n"
"ds_read_b128 a[176:179], v250 offset:2048\n"
"v_mfma_f32_16x16x32_bf16 v[136:139], a[212:215], a[148:151], v[136:139]\n"
"s_barrier\n"
"v_mfma_f32_16x16x32_bf16 v[132:135], a[200:203], a[144:147], v[228:231]\n"
"s_add_u32 s40, s48, 256\n"
"s_xor_b32 m0, s50, 0xc000\n"
"ds_read_b128 a[180:183], v251 offset:2048\n"
"v_mfma_f32_16x16x32_bf16 v[132:135], a[204:207], a[148:151], v[132:135]\n"
"buffer_load_dwordx4 v224, s[28:31], s40 offen lds\n"
"v_mfma_f32_16x16x32_bf16 v[128:131], a[192:195], a[144:147], v[228:231]\n"
"ds_read_b128 a[184:187], v250 offset:4096\n"
"v_mfma_f32_16x16x32_bf16 v[128:131], a[196:199], a[148:151], v[128:131]\n"
"s_waitcnt lgkmcnt(5)\n"
"ds_read_b128 a[188:191], v251 offset:4096\n"
"v_mfma_f32_16x16x32_bf16 v[144:147], a[192:195], a[152:155], v[228:231]\n"
"ds_read_b64_tr_b16 a[224:225], v244 offset:16384\n"
"v_mfma_f32_16x16x32_bf16 v[144:147], a[196:199], a[156:159], v[144:147]\n"
"ds_read_b64_tr_b16 a[226:227], v244 offset:16896\n"
"v_mfma_f32_16x16x32_bf16 v[148:151], a[200:203], a[152:155], v[228:231]\n"
"ds_read_b64_tr_b16 a[228:229], v244 offset:24576\n"
"v_mfma_f32_16x16x32_bf16 v[148:151], a[204:207], a[156:159], v[148:151]\n"
"buffer_load_dwordx4 v225, s[28:31], s40 offen offset:1024 lds\n"
"v_mfma_f32_16x16x32_bf16 v[152:155], a[208:211], a[152:155], v[228:231]\n"
"ds_read_b64_tr_b16 a[230:231], v244 offset:25088\n"
"v_mfma_f32_16x16x32_bf16 v[152:155], a[212:215], a[156:159], v[152:155]\n"
"ds_read_b64_tr_b16 a[232:233], v245 offset:16384\n"
"v_mfma_f32_16x16x32_bf16 v[156:159], a[216:219], a[152:155], v[228:231]\n"
"ds_read_b64_tr_b16 a[234:235], v245 offset:16896\n"
"v_mfma_f32_16x16x32_bf16 v[156:159], a[220:223], a[156:159], v[156:159]\n"
"ds_read_b64_tr_b16 a[236:237], v245 offset:24576\n"
"v_mfma_f32_16x16x32_bf16 v[172:175], a[216:219], a[160:163], v[228:231]\n"
"ds_read_b64_tr_b16 a[238:239], v245 offset:25088\n"
"v_mfma_f32_16x16x32_bf16 v[172:175], a[220:223], a[164:167], v[172:175]\n"
"buffer_load_dwordx4 v226, s[28:31], s40 offen offset:2048 lds\n"
"v_mfma_f32_16x16x32_bf16 v[168:171], a[208:211], a[160:163], v[228:231]\n"
"ds_read_b64_tr_b16 a[240:241], v244 offset:17408\n"
"v_mfma_f32_16x16x32_bf16 v[168:171], a[212:215], a[164:167], v[168:171]\n"
"ds_read_b64_tr_b16 a[242:243], v244 offset:17920\n"
"v_mfma_f32_16x16x32_bf16 v[164:167], a[200:203], a[160:163], v[228:231]\n"
"ds_read_b64_tr_b16 a[244:245], v244 offset:25600\n"
"v_mfma_f32_16x16x32_bf16 v[164:167], a[204:207], a[164:167], v[164:167]\n"
"buffer_load_dwordx4 v227, s[28:31], s40 offen offset:3072 lds\n"
"v_mfma_f32_16x16x32_bf16 v[160:163], a[192:195], a[160:163], v[228:231]\n"
"s_cmp_eq_u32 s47, 8\n"
"ds_read_b64_tr_b16 a[246:247], v244 offset:26112\n"
"v_xor_b32 v244, s74, v244\n"
"v_mfma_f32_16x16x32_bf16 v[160:163], a[196:199], a[164:167], v[160:163]\n"
"s_waitcnt lgkmcnt(5)\n"
"s_cselect_b32 s41, s52, s58\n"
"ds_read_b64_tr_b16 a[248:249], v245 offset:17408\n"
"v_xor_b32 v246, s74, v246\n"
"v_mfma_f32_16x16x32_bf16 v[176:179], a[192:195], a[168:171], v[228:231]\n"
"s_add_u32 s48, s48, s41\n"
"ds_read_b64_tr_b16 a[250:251], v245 offset:17920\n"
"v_xor_b32 v247, s74, v247\n"
"v_mfma_f32_16x16x32_bf16 v[176:179], a[196:199], a[172:175], v[176:179]\n"
"s_sub_u32 s41, s48, 5242880\n"
"ds_read_b64_tr_b16 a[252:253], v245 offset:25600\n"
"v_xor_b32 v252, s74, v252\n"
"v_mfma_f32_16x16x32_bf16 v[180:183], a[200:203], a[168:171], v[228:231]\n"
"s_min_u32 s48, s48, s41\n"
"ds_read_b64_tr_b16 a[254:255], v245 offset:26112\n"
"v_xor_b32 v245, s74, v245\n"
"v_mfma_f32_16x16x32_bf16 v[180:183], a[204:207], a[172:175], v[180:183]\n"
"v_xor_b32 v253, s74, v253\n"
"v_xor_b32 v250, s74, v250\n"
"v_mfma_f32_16x16x32_bf16 v[184:187], a[208:211], a[168:171], v[228:231]\n"
"s_mov_b32 m0, s50\n"
"v_xor_b32 v251, s74, v251\n"
"v_mfma_f32_16x16x32_bf16 v[184:187], a[212:215], a[172:175], v[184:187]\n"
"buffer_load_dwordx4 v224, s[28:31], s48 offen lds\n"
"v_mfma_f32_16x16x32_bf16 v[188:191], a[216:219], a[168:171], v[228:231]\n"
"v_mfma_f32_16x16x32_bf16 v[188:191], a[220:223], a[172:175], v[188:191]\n"
"v_mfma_f32_16x16x32_bf16 v[204:207], a[216:219], a[176:179], v[228:231]\n"
"v_mfma_f32_16x16x32_bf16 v[204:207], a[220:223], a[180:183], v[204:207]\n"
"buffer_load_dwordx4 v225, s[28:31], s48 offen offset:1024 lds\n"
"v_mfma_f32_16x16x32_bf16 v[200:203], a[208:211], a[176:179], v[228:231]\n"
"v_mfma_f32_16x16x32_bf16 v[200:203], a[212:215], a[180:183], v[200:203]\n"
"v_mfma_f32_16x16x32_bf16 v[196:199], a[200:203], a[176:179], v[228:231]\n"
"v_mfma_f32_16x16x32_bf16 v[196:199], a[204:207], a[180:183], v[196:199]\n"
"v_mfma_f32_16x16x32_bf16 v[192:195], a[192:195], a[176:179], v[228:231]\n"
"v_mfma_f32_16x16x32_bf16 v[192:195], a[196:199], a[180:183], v[192:195]\n"
"buffer_load_dwordx4 v226, s[28:31], s48 offen offset:2048 lds\n"
"v_mfma_f32_16x16x32_bf16 v[208:211], a[192:195], a[184:187], v[228:231]\n"
"v_mfma_f32_16x16x32_bf16 v[208:211], a[196:199], a[188:191], v[208:211]\n"
"v_mfma_f32_16x16x32_bf16 v[212:215], a[200:203], a[184:187], v[228:231]\n"
"v_mfma_f32_16x16x32_bf16 v[212:215], a[204:207], a[188:191], v[212:215]\n"
"v_mfma_f32_16x16x32_bf16 v[216:219], a[208:211], a[184:187], v[228:231]\n"
"v_mfma_f32_16x16x32_bf16 v[216:219], a[212:215], a[188:191], v[216:219]\n"
"buffer_load_dwordx4 v227, s[28:31], s48 offen offset:3072 lds\n"
"v_mfma_f32_16x16x32_bf16 v[220:223], a[216:219], a[184:187], v[228:231]\n"
"s_xor_b32 s50, s50, s74\n"
"v_mfma_f32_16x16x32_bf16 v[220:223], a[220:223], a[188:191], v[220:223]\n"
"v_mfma_f32_16x16x32_bf16 v[0:3], a[224:227], a[136:139], v[228:231]\n"
"v_mfma_f32_16x16x32_bf16 v[0:3], a[228:231], a[140:143], v[0:3]\n"
"v_mfma_f32_16x16x32_bf16 v[4:7], a[232:235], a[136:139], v[228:231]\n"
"s_waitcnt vmcnt(8) lgkmcnt(0)\n"
"v_mfma_f32_16x16x32_bf16 v[4:7], a[236:239], a[140:143], v[4:7]\n"
"s_barrier\n"
"v_mfma_f32_16x16x32_bf16 v[8:11], a[240:243], a[136:139], v[228:231]\n"
"s_mov_b32 m0, s51\n"
"ds_read_b64_tr_b16 a[192:193], v244 offset:0\n"
"v_mfma_f32_16x16x32_bf16 v[8:11], a[244:247], a[140:143], v[8:11]\n"
"buffer_load_dwordx4 v248, s[24:27], s49 offen lds\n"
"v_mfma_f32_16x16x32_bf16 v[12:15], a[248:251], a[136:139], v[228:231]\n"
"ds_read_b64_tr_b16 a[194:195], v244 offset:512\n"
"v_mfma_f32_16x16x32_bf16 v[12:15], a[252:255], a[140:143], v[12:15]\n"
"ds_read_b128 a[136:139], v246 offset:0\n"
"v_mfma_f32_16x16x32_bf16 v[28:31], a[248:251], a[144:147], v[228:231]\n"
"ds_read_b64_tr_b16 a[196:197], v244 offset:8192\n"
"v_mfma_f32_16x16x32_bf16 v[28:31], a[252:255], a[148:151], v[28:31]\n"
"ds_read_b64_tr_b16 a[198:199], v244 offset:8704\n"
"v_mfma_f32_16x16x32_bf16 v[24:27], a[240:243], a[144:147], v[228:231]\n"
"ds_read_b128 a[140:143], v247 offset:0\n"
"v_mfma_f32_16x16x32_bf16 v[24:27], a[244:247], a[148:151], v[24:27]\n"
"buffer_load_dwordx4 v249, s[24:27], s49 offen offset:1024 lds\n"
"v_mfma_f32_16x16x32_bf16 v[20:23], a[232:235], a[144:147], v[228:231]\n"
"ds_read_b64_tr_b16 a[200:201], v245 offset:0\n"
"v_mfma_f32_16x16x32_bf16 v[20:23], a[236:239], a[148:151], v[20:23]\n"
"ds_read_b64_tr_b16 a[202:203], v245 offset:512\n"
"v_mfma_f32_16x16x32_bf16 v[16:19], a[224:227], a[144:147], v[228:231]\n"
"ds_read_b64_tr_b16 a[204:205], v245 offset:8192\n"
"v_mfma_f32_16x16x32_bf16 v[16:19], a[228:231], a[148:151], v[16:19]\n"
"ds_read_b64_tr_b16 a[206:207], v245 offset:8704\n"
"v_mfma_f32_16x16x32_bf16 v[32:35], a[224:227], a[152:155], v[228:231]\n"
"ds_read_b64_tr_b16 a[208:209], v244 offset:1024\n"
"v_mfma_f32_16x16x32_bf16 v[32:35], a[228:231], a[156:159], v[32:35]\n"
"buffer_load_dwordx4 v254, s[24:27], s49 offen offset:2048 lds\n"
"v_mfma_f32_16x16x32_bf16 v[36:39], a[232:235], a[152:155], v[228:231]\n"
"ds_read_b64_tr_b16 a[210:211], v244 offset:1536\n"
"v_mfma_f32_16x16x32_bf16 v[36:39], a[236:239], a[156:159], v[36:39]\n"
"ds_read_b64_tr_b16 a[212:213], v244 offset:9216\n"
"v_mfma_f32_16x16x32_bf16 v[40:43], a[240:243], a[152:155], v[228:231]\n"
"ds_read_b64_tr_b16 a[214:215], v244 offset:9728\n"
"v_mfma_f32_16x16x32_bf16 v[40:43], a[244:247], a[156:159], v[40:43]\n"
"ds_read_b64_tr_b16 a[216:217], v245 offset:1024\n"
"v_mfma_f32_16x16x32_bf16 v[44:47], a[248:251], a[152:155], v[228:231]\n"
"ds_read_b64_tr_b16 a[218:219], v245 offset:1536\n"
"v_mfma_f32_16x16x32_bf16 v[44:47], a[252:255], a[156:159], v[44:47]\n"
"buffer_load_dwordx4 v255, s[24:27], s49 offen offset:3072 lds\n"
"v_mfma_f32_16x16x32_bf16 v[60:63], a[248:251], a[160:163], v[228:231]\n"
"ds_read_b64_tr_b16 a[220:221], v245 offset:9216\n"
"v_mfma_f32_16x16x32_bf16 v[60:63], a[252:255], a[164:167], v[60:63]\n"
"ds_read_b64_tr_b16 a[222:223], v245 offset:9728\n"
"v_mfma_f32_16x16x32_bf16 v[56:59], a[240:243], a[160:163], v[228:231]\n"
"ds_read_b128 a[144:147], v246 offset:2048\n"
"v_mfma_f32_16x16x32_bf16 v[56:59], a[244:247], a[164:167], v[56:59]\n"
"s_add_u32 s40, s49, 40960\n"
"ds_read_b128 a[148:151], v247 offset:2048\n"
"v_mfma_f32_16x16x32_bf16 v[52:55], a[232:235], a[160:163], v[228:231]\n"
"s_add_u32 m0, s51, 4096\n"
"ds_read_b128 a[152:155], v246 offset:4096\n"
"v_mfma_f32_16x16x32_bf16 v[52:55], a[236:239], a[164:167], v[52:55]\n"
"buffer_load_dwordx4 v248, s[24:27], s40 offen lds\n"
"v_mfma_f32_16x16x32_bf16 v[48:51], a[224:227], a[160:163], v[228:231]\n"
"ds_read_b128 a[156:159], v247 offset:4096\n"
"v_mfma_f32_16x16x32_bf16 v[48:51], a[228:231], a[164:167], v[48:51]\n"
"ds_read_b128 a[160:163], v252 offset:0\n"
"v_mfma_f32_16x16x32_bf16 v[64:67], a[224:227], a[168:171], v[228:231]\n"
"ds_read_b128 a[164:167], v253 offset:0\n"
"v_mfma_f32_16x16x32_bf16 v[64:67], a[228:231], a[172:175], v[64:67]\n"
"v_mfma_f32_16x16x32_bf16 v[68:71], a[232:235], a[168:171], v[228:231]\n"
"v_mfma_f32_16x16x32_bf16 v[68:71], a[236:239], a[172:175], v[68:71]\n"
"buffer_load_dwordx4 v249, s[24:27], s40 offen offset:1024 lds\n"
"v_mfma_f32_16x16x32_bf16 v[72:75], a[240:243], a[168:171], v[228:231]\n"
"v_mfma_f32_16x16x32_bf16 v[72:75], a[244:247], a[172:175], v[72:75]\n"
"v_mfma_f32_16x16x32_bf16 v[76:79], a[248:251], a[168:171], v[228:231]\n"
"ds_read_b128 a[168:171], v250 offset:0\n"
"v_mfma_f32_16x16x32_bf16 v[76:79], a[252:255], a[172:175], v[76:79]\n"
"ds_read_b128 a[172:175], v251 offset:0\n"
"v_mfma_f32_16x16x32_bf16 v[92:95], a[248:251], a[176:179], v[228:231]\n"
"v_mfma_f32_16x16x32_bf16 v[92:95], a[252:255], a[180:183], v[92:95]\n"
"buffer_load_dwordx4 v254, s[24:27], s40 offen offset:2048 lds\n"
"v_mfma_f32_16x16x32_bf16 v[88:91], a[240:243], a[176:179], v[228:231]\n"
"s_add_u32 s49, s49, 128\n"
"v_mfma_f32_16x16x32_bf16 v[88:91], a[244:247], a[180:183], v[88:91]\n"
"s_sub_u32 s41, s49, 1280\n"
"v_mfma_f32_16x16x32_bf16 v[84:87], a[232:235], a[176:179], v[228:231]\n"
"s_min_u32 s49, s49, s41\n"
"v_mfma_f32_16x16x32_bf16 v[84:87], a[236:239], a[180:183], v[84:87]\n"
"s_xor_b32 s51, s51, s74\n"
"v_mfma_f32_16x16x32_bf16 v[80:83], a[224:227], a[176:179], v[228:231]\n"
"ds_read_b128 a[176:179], v250 offset:2048\n"
"v_mfma_f32_16x16x32_bf16 v[80:83], a[228:231], a[180:183], v[80:83]\n"
"ds_read_b128 a[180:183], v251 offset:2048\n"
"v_mfma_f32_16x16x32_bf16 v[96:99], a[224:227], a[184:187], v[228:231]\n"
"v_mfma_f32_16x16x32_bf16 v[96:99], a[228:231], a[188:191], v[96:99]\n"
"v_mfma_f32_16x16x32_bf16 v[100:103], a[232:235], a[184:187], v[228:231]\n"
"v_mfma_f32_16x16x32_bf16 v[100:103], a[236:239], a[188:191], v[100:103]\n"
"v_mfma_f32_16x16x32_bf16 v[104:107], a[240:243], a[184:187], v[228:231]\n"
"v_mfma_f32_16x16x32_bf16 v[104:107], a[244:247], a[188:191], v[104:107]\n"
"v_mfma_f32_16x16x32_bf16 v[108:111], a[248:251], a[184:187], v[228:231]\n"
"s_add_u32 s47, s47, 1\n"
"ds_read_b128 a[184:187], v250 offset:4096\n"
"v_mfma_f32_16x16x32_bf16 v[108:111], a[252:255], a[188:191], v[108:111]\n"
"s_waitcnt lgkmcnt(1)\n"
"ds_read_b128 a[188:191], v251 offset:4096\n"
"LPE_%=:\n"
"s_cmp_lg_u32 s68, 0\n"
"s_cbranch_scc1 LPN_%=\n"
"LPL_%=:\n"
"v_mfma_f32_16x16x32_bf16 v[112:115], a[192:195], a[136:139], v[112:115]\n"
"v_mfma_f32_16x16x32_bf16 v[112:115], a[196:199], a[140:143], v[112:115]\n"
"v_mfma_f32_16x16x32_bf16 v[116:119], a[200:203], a[136:139], v[116:119]\n"
"s_waitcnt vmcnt(11)\n"
"v_mfma_f32_16x16x32_bf16 v[116:119], a[204:207], a[140:143], v[116:119]\n"
"s_barrier\n"
"v_mfma_f32_16x16x32_bf16 v[120:123], a[208:211], a[136:139], v[120:123]\n"
"s_add_u32 s40, s48, 256\n"
"s_xor_b32 m0, s50, 0xc000\n"
"ds_read_b64_tr_b16 a[224:225], v244 offset:16384\n"
"v_mfma_f32_16x16x32_bf16 v[120:123], a[212:215], a[140:143], v[120:123]\n"
"buffer_load_dwordx4 v224, s[28:31], s40 offen lds\n"
"v_mfma_f32_16x16x32_bf16 v[124:127], a[216:219], a[136:139], v[124:127]\n"
"ds_read_b64_tr_b16 a[226:227], v244 offset:16896\n"
"v_mfma_f32_16x16x32_bf16 v[124:127], a[220:223], a[140:143], v[124:127]\n"
"ds_read_b64_tr_b16 a[228:229], v244 offset:24576\n"
"v_mfma_f32_16x16x32_bf16 v[140:143], a[216:219], a[144:147], v[140:143]\n"
"ds_read_b64_tr_b16 a[230:231], v244 offset:25088\n"
"v_mfma_f32_16x16x32_bf16 v[140:143], a[220:223], a[148:151], v[140:143]\n"
"ds_read_b64_tr_b16 a[232:233], v245 offset:16384\n"
"v_mfma_f32_16x16x32_bf16 v[136:139], a[208:211], a[144:147], v[136:139]\n"
"ds_read_b64_tr_b16 a[234:235], v245 offset:16896\n"
"v_mfma_f32_16x16x32_bf16 v[136:139], a[212:215], a[148:151], v[136:139]\n"
"buffer_load_dwordx4 v225, s[28:31], s40 offen offset:1024 lds\n"
"v_mfma_f32_16x16x32_bf16 v[132:135], a[200:203], a[144:147], v[132:135]\n"
"ds_read_b64_tr_b16 a[236:237], v245 offset:24576\n"
"v_mfma_f32_16x16x32_bf16 v[132:135], a[204:207], a[148:151], v[132:135]\n"
"ds_read_b64_tr_b16 a[238:239], v245 offset:25088\n"
"v_mfma_f32_16x16x32_bf16 v[128:131], a[192:195], a[144:147], v[128:131]\n"
"ds_read_b64_tr_b16 a[240:241], v244 offset:17408\n"
"v_mfma_f32_16x16x32_bf16 v[128:131], a[196:199], a[148:151], v[128:131]\n"
"ds_read_b64_tr_b16 a[242:243], v244 offset:17920\n"
"v_mfma_f32_16x16x32_bf16 v[144:147], a[192:195], a[152:155], v[144:147]\n"
"ds_read_b64_tr_b16 a[244:245], v244 offset:25600\n"
"v_mfma_f32_16x16x32_bf16 v[144:147], a[196:199], a[156:159], v[144:147]\n"
"buffer_load_dwordx4 v226, s[28:31], s40 offen offset:2048 lds\n"
"v_mfma_f32_16x16x32_bf16 v[148:151], a[200:203], a[152:155], v[148:151]\n"
"ds_read_b64_tr_b16 a[246:247], v244 offset:26112\n"
"v_mfma_f32_16x16x32_bf16 v[148:151], a[204:207], a[156:159], v[148:151]\n"
"ds_read_b64_tr_b16 a[248:249], v245 offset:17408\n"
"v_mfma_f32_16x16x32_bf16 v[152:155], a[208:211], a[152:155], v[152:155]\n"
"ds_read_b64_tr_b16 a[250:251], v245 offset:17920\n"
"v_mfma_f32_16x16x32_bf16 v[152:155], a[212:215], a[156:159], v[152:155]\n"
"ds_read_b64_tr_b16 a[252:253], v245 offset:25600\n"
"v_mfma_f32_16x16x32_bf16 v[156:159], a[216:219], a[152:155], v[156:159]\n"
"ds_read_b64_tr_b16 a[254:255], v245 offset:26112\n"
"v_mfma_f32_16x16x32_bf16 v[156:159], a[220:223], a[156:159], v[156:159]\n"
"buffer_load_dwordx4 v227, s[28:31], s40 offen offset:3072 lds\n"
"v_mfma_f32_16x16x32_bf16 v[172:175], a[216:219], a[160:163], v[172:175]\n"
"s_cmp_eq_u32 s47, 8\n"
"v_mfma_f32_16x16x32_bf16 v[172:175], a[220:223], a[164:167], v[172:175]\n"
"s_cselect_b32 s41, s52, s58\n"
"v_mfma_f32_16x16x32_bf16 v[168:171], a[208:211], a[160:163], v[168:171]\n"
"s_add_u32 s48, s48, s41\n"
"v_mfma_f32_16x16x32_bf16 v[168:171], a[212:215], a[164:167], v[168:171]\n"
"s_sub_u32 s41, s48, 5242880\n"
"v_mfma_f32_16x16x32_bf16 v[164:167], a[200:203], a[160:163], v[164:167]\n"
"s_min_u32 s48, s48, s41\n"
"v_mfma_f32_16x16x32_bf16 v[164:167], a[204:207], a[164:167], v[164:167]\n"
"v_xor_b32 v244, s74, v244\n"
"v_xor_b32 v246, s74, v246\n"
"v_mfma_f32_16x16x32_bf16 v[160:163], a[192:195], a[160:163], v[160:163]\n"
"s_mov_b32 m0, s50\n"
"v_xor_b32 v245, s74, v245\n"
"v_xor_b32 v247, s74, v247\n"
"v_mfma_f32_16x16x32_bf16 v[160:163], a[196:199], a[164:167], v[160:163]\n"
"buffer_load_dwordx4 v224, s[28:31], s48 offen lds\n"
"v_mfma_f32_16x16x32_bf16 v[176:179], a[192:195], a[168:171], v[176:179]\n"
"v_xor_b32 v252, s74, v252\n"
"v_xor_b32 v253, s74, v253\n"
"v_mfma_f32_16x16x32_bf16 v[176:179], a[196:199], a[172:175], v[176:179]\n"
"v_xor_b32 v250, s74, v250\n"
"v_xor_b32 v251, s74, v251\n"
"v_mfma_f32_16x16x32_bf16 v[180:183], a[200:203], a[168:171], v[180:183]\n"
"v_mfma_f32_16x16x32_bf16 v[180:183], a[204:207], a[172:175], v[180:183]\n"
"buffer_load_dwordx4 v225, s[28:31], s48 offen offset:1024 lds\n"
"v_mfma_f32_16x16x32_bf16 v[184:187], a[208:211], a[168:171], v[184:187]\n"
"v_mfma_f32_16x16x32_bf16 v[184:187], a[212:215], a[172:175], v[184:187]\n"
"v_mfma_f32_16x16x32_bf16 v[188:191], a[216:219], a[168:171], v[188:191]\n"
"v_mfma_f32_16x16x32_bf16 v[188:191], a[220:223], a[172:175], v[188:191]\n"
"v_mfma_f32_16x16x32_bf16 v[204:207], a[216:219], a[176:179], v[204:207]\n"
"v_mfma_f32_16x16x32_bf16 v[204:207], a[220:223], a[180:183], v[204:207]\n"
"buffer_load_dwordx4 v226, s[28:31], s48 offen offset:2048 lds\n"
"v_mfma_f32_16x16x32_bf16 v[200:203], a[208:211], a[176:179], v[200:203]\n"
"v_mfma_f32_16x16x32_bf16 v[200:203], a[212:215], a[180:183], v[200:203]\n"
"v_mfma_f32_16x16x32_bf16 v[196:199], a[200:203], a[176:179], v[196:199]\n"
"v_mfma_f32_16x16x32_bf16 v[196:199], a[204:207], a[180:183], v[196:199]\n"
"v_mfma_f32_16x16x32_bf16 v[192:195], a[192:195], a[176:179], v[192:195]\n"
"v_mfma_f32_16x16x32_bf16 v[192:195], a[196:199], a[180:183], v[192:195]\n"
"s_waitcnt lgkmcnt(0)\n"
"buffer_load_dwordx4 v227, s[28:31], s48 offen offset:3072 lds\n"
"v_mfma_f32_16x16x32_bf16 v[208:211], a[192:195], a[184:187], v[208:211]\n"
"s_xor_b32 s50, s50, s74\n"
"v_mfma_f32_16x16x32_bf16 v[208:211], a[196:199], a[188:191], v[208:211]\n"
"v_mfma_f32_16x16x32_bf16 v[212:215], a[200:203], a[184:187], v[212:215]\n"
"v_mfma_f32_16x16x32_bf16 v[212:215], a[204:207], a[188:191], v[212:215]\n"
"v_mfma_f32_16x16x32_bf16 v[216:219], a[208:211], a[184:187], v[216:219]\n"
"v_mfma_f32_16x16x32_bf16 v[216:219], a[212:215], a[188:191], v[216:219]\n"
"v_mfma_f32_16x16x32_bf16 v[220:223], a[216:219], a[184:187], v[220:223]\n"
"v_mfma_f32_16x16x32_bf16 v[220:223], a[220:223], a[188:191], v[220:223]\n"
"v_mfma_f32_16x16x32_bf16 v[0:3], a[224:227], a[136:139], v[0:3]\n"
"v_mfma_f32_16x16x32_bf16 v[0:3], a[228:231], a[140:143], v[0:3]\n"
"v_mfma_f32_16x16x32_bf16 v[4:7], a[232:235], a[136:139], v[4:7]\n"
"s_waitcnt vmcnt(8)\n"
"v_mfma_f32_16x16x32_bf16 v[4:7], a[236:239], a[140:143], v[4:7]\n"
"s_barrier\n"
"v_mfma_f32_16x16x32_bf16 v[8:11], a[240:243], a[136:139], v[8:11]\n"
"s_mov_b32 m0, s51\n"
"ds_read_b64_tr_b16 a[192:193], v244 offset:0\n"
"v_mfma_f32_16x16x32_bf16 v[8:11], a[244:247], a[140:143], v[8:11]\n"
"buffer_load_dwordx4 v248, s[24:27], s49 offen lds\n"
"v_mfma_f32_16x16x32_bf16 v[12:15], a[248:251], a[136:139], v[12:15]\n"
"ds_read_b64_tr_b16 a[194:195], v244 offset:512\n"
"v_mfma_f32_16x16x32_bf16 v[12:15], a[252:255], a[140:143], v[12:15]\n"
"ds_read_b128 a[136:139], v246 offset:0\n"
"v_mfma_f32_16x16x32_bf16 v[28:31], a[248:251], a[144:147], v[28:31]\n"
"ds_read_b64_tr_b16 a[196:197], v244 offset:8192\n"
"v_mfma_f32_16x16x32_bf16 v[28:31], a[252:255], a[148:151], v[28:31]\n"
"ds_read_b64_tr_b16 a[198:199], v244 offset:8704\n"
"v_mfma_f32_16x16x32_bf16 v[24:27], a[240:243], a[144:147], v[24:27]\n"
"ds_read_b128 a[140:143], v247 offset:0\n"
"v_mfma_f32_16x16x32_bf16 v[24:27], a[244:247], a[148:151], v[24:27]\n"
"buffer_load_dwordx4 v249, s[24:27], s49 offen offset:1024 lds\n"
"v_mfma_f32_16x16x32_bf16 v[20:23], a[232:235], a[144:147], v[20:23]\n"
"ds_read_b64_tr_b16 a[200:201], v245 offset:0\n"
"v_mfma_f32_16x16x32_bf16 v[20:23], a[236:239], a[148:151], v[20:23]\n"
"ds_read_b64_tr_b16 a[202:203], v245 offset:512\n"
"v_mfma_f32_16x16x32_bf16 v[16:19], a[224:227], a[144:147], v[16:19]\n"
"ds_read_b64_tr_b16 a[204:205], v245 offset:8192\n"
"v_mfma_f32_16x16x32_bf16 v[16:19], a[228:231], a[148:151], v[16:19]\n"
"ds_read_b64_tr_b16 a[206:207], v245 offset:8704\n"
"v_mfma_f32_16x16x32_bf16 v[32:35], a[224:227], a[152:155], v[32:35]\n"
"ds_read_b64_tr_b16 a[208:209], v244 offset:1024\n"
"v_mfma_f32_16x16x32_bf16 v[32:35], a[228:231], a[156:159], v[32:35]\n"
"buffer_load_dwordx4 v254, s[24:27], s49 offen offset:2048 lds\n"
"v_mfma_f32_16x16x32_bf16 v[36:39], a[232:235], a[152:155], v[36:39]\n"
"ds_read_b64_tr_b16 a[210:211], v244 offset:1536\n"
"v_mfma_f32_16x16x32_bf16 v[36:39], a[236:239], a[156:159], v[36:39]\n"
"ds_read_b64_tr_b16 a[212:213], v244 offset:9216\n"
"v_mfma_f32_16x16x32_bf16 v[40:43], a[240:243], a[152:155], v[40:43]\n"
"ds_read_b64_tr_b16 a[214:215], v244 offset:9728\n"
"v_mfma_f32_16x16x32_bf16 v[40:43], a[244:247], a[156:159], v[40:43]\n"
"ds_read_b64_tr_b16 a[216:217], v245 offset:1024\n"
"v_mfma_f32_16x16x32_bf16 v[44:47], a[248:251], a[152:155], v[44:47]\n"
"ds_read_b64_tr_b16 a[218:219], v245 offset:1536\n"
"v_mfma_f32_16x16x32_bf16 v[44:47], a[252:255], a[156:159], v[44:47]\n"
"buffer_load_dwordx4 v255, s[24:27], s49 offen offset:3072 lds\n"
"v_mfma_f32_16x16x32_bf16 v[60:63], a[248:251], a[160:163], v[60:63]\n"
"ds_read_b64_tr_b16 a[220:221], v245 offset:9216\n"
"v_mfma_f32_16x16x32_bf16 v[60:63], a[252:255], a[164:167], v[60:63]\n"
"ds_read_b64_tr_b16 a[222:223], v245 offset:9728\n"
"v_mfma_f32_16x16x32_bf16 v[56:59], a[240:243], a[160:163], v[56:59]\n"
"ds_read_b128 a[144:147], v246 offset:2048\n"
"v_mfma_f32_16x16x32_bf16 v[56:59], a[244:247], a[164:167], v[56:59]\n"
"s_add_u32 s40, s49, 40960\n"
"ds_read_b128 a[148:151], v247 offset:2048\n"
"v_mfma_f32_16x16x32_bf16 v[52:55], a[232:235], a[160:163], v[52:55]\n"
"s_add_u32 m0, s51, 4096\n"
"ds_read_b128 a[152:155], v246 offset:4096\n"
"v_mfma_f32_16x16x32_bf16 v[52:55], a[236:239], a[164:167], v[52:55]\n"
"buffer_load_dwordx4 v248, s[24:27], s40 offen lds\n"
"v_mfma_f32_16x16x32_bf16 v[48:51], a[224:227], a[160:163], v[48:51]\n"
"ds_read_b128 a[156:159], v247 offset:4096\n"
"v_mfma_f32_16x16x32_bf16 v[48:51], a[228:231], a[164:167], v[48:51]\n"
"ds_read_b128 a[160:163], v252 offset:0\n"
"v_mfma_f32_16x16x32_bf16 v[64:67], a[224:227], a[168:171], v[64:67]\n"
"ds_read_b128 a[164:167], v253 offset:0\n"
"v_mfma_f32_16x16x32_bf16 v[64:67], a[228:231], a[172:175], v[64:67]\n"
"v_mfma_f32_16x16x32_bf16 v[68:71], a[232:235], a[168:171], v[68:71]\n"
"v_mfma_f32_16x16x32_bf16 v[68:71], a[236:239], a[172:175], v[68:71]\n"
"buffer_load_dwordx4 v249, s[24:27], s40 offen offset:1024 lds\n"
"v_mfma_f32_16x16x32_bf16 v[72:75], a[240:243], a[168:171], v[72:75]\n"
"v_mfma_f32_16x16x32_bf16 v[72:75], a[244:247], a[172:175], v[72:75]\n"
"v_mfma_f32_16x16x32_bf16 v[76:79], a[248:251], a[168:171], v[76:79]\n"
"ds_read_b128 a[168:171], v250 offset:0\n"
"v_mfma_f32_16x16x32_bf16 v[76:79], a[252:255], a[172:175], v[76:79]\n"
"ds_read_b128 a[172:175], v251 offset:0\n"
"v_mfma_f32_16x16x32_bf16 v[92:95], a[248:251], a[176:179], v[92:95]\n"
"v_mfma_f32_16x16x32_bf16 v[92:95], a[252:255], a[180:183], v[92:95]\n"
"buffer_load_dwordx4 v254, s[24:27], s40 offen offset:2048 lds\n"
"v_mfma_f32_16x16x32_bf16 v[88:91], a[240:243], a[176:179], v[88:91]\n"
"s_add_u32 s49, s49, 128\n"
"v_mfma_f32_16x16x32_bf16 v[88:91], a[244:247], a[180:183], v[88:91]\n"
"s_sub_u32 s41, s49, 1280\n"
"v_mfma_f32_16x16x32_bf16 v[84:87], a[232:235], a[176:179], v[84:87]\n"
"s_min_u32 s49, s49, s41\n"
"v_mfma_f32_16x16x32_bf16 v[84:87], a[236:239], a[180:183], v[84:87]\n"
"s_xor_b32 s51, s51, s74\n"
"v_mfma_f32_16x16x32_bf16 v[80:83], a[224:227], a[176:179], v[80:83]\n"
"ds_read_b128 a[176:179], v250 offset:2048\n"
"v_mfma_f32_16x16x32_bf16 v[80:83], a[228:231], a[180:183], v[80:83]\n"
"ds_read_b128 a[180:183], v251 offset:2048\n"
"v_mfma_f32_16x16x32_bf16 v[96:99], a[224:227], a[184:187], v[96:99]\n"
"v_mfma_f32_16x16x32_bf16 v[96:99], a[228:231], a[188:191], v[96:99]\n"
"v_mfma_f32_16x16x32_bf16 v[100:103], a[232:235], a[184:187], v[100:103]\n"
"v_mfma_f32_16x16x32_bf16 v[100:103], a[236:239], a[188:191], v[100:103]\n"
"v_mfma_f32_16x16x32_bf16 v[104:107], a[240:243], a[184:187], v[104:107]\n"
"v_mfma_f32_16x16x32_bf16 v[104:107], a[244:247], a[188:191], v[104:107]\n"
"v_mfma_f32_16x16x32_bf16 v[108:111], a[248:251], a[184:187], v[108:111]\n"
"s_add_u32 s47, s47, 1\n"
"ds_read_b128 a[184:187], v250 offset:4096\n"
"v_mfma_f32_16x16x32_bf16 v[108:111], a[252:255], a[188:191], v[108:111]\n"
"s_waitcnt lgkmcnt(1)\n"
"s_cmp_lg_u32 s47, s62\n"
"ds_read_b128 a[188:191], v251 offset:4096\n"
"s_cbranch_scc1 LPL_%=\n"
"LPX_%=:\n"
"s_cmp_eq_u32 s47, 18\n"
"s_cbranch_scc1 LPT2_%=\n"
"v_mfma_f32_16x16x32_bf16 v[112:115], a[192:195], a[136:139], v[112:115]\n"
"s_add_u32 s78, s96, 8192\n"
"v_mov_b32 v241, 1\n"
"v_mfma_f32_16x16x32_bf16 v[112:115], a[196:199], a[140:143], v[112:115]\n"
"s_mov_b64 exec, 1\n"
"buffer_store_dword v241, off, s[32:35], s96 sc0 sc1\n"
"buffer_store_dword v241, off, s[32:35], s78 sc0 sc1\n"
"s_mov_b64 exec, -1\n"
"v_mfma_f32_16x16x32_bf16 v[116:119], a[200:203], a[136:139], v[116:119]\n"
"s_waitcnt vmcnt(13)\n"
"v_mfma_f32_16x16x32_bf16 v[116:119], a[204:207], a[140:143], v[116:119]\n"
"s_barrier\n"
"v_mfma_f32_16x16x32_bf16 v[120:123], a[208:211], a[136:139], v[120:123]\n"
"s_add_u32 s40, s48, 256\n"
"s_xor_b32 m0, s50, 0xc000\n"
"ds_read_b64_tr_b16 a[238:239], v245 offset:25088\n"
"v_mfma_f32_16x16x32_bf16 v[120:123], a[212:215], a[140:143], v[120:123]\n"
"buffer_load_dwordx4 v224, s[28:31], s40 offen lds\n"
"v_mfma_f32_16x16x32_bf16 v[124:127], a[216:219], a[136:139], v[124:127]\n"
"v_mfma_f32_16x16x32_bf16 v[124:127], a[220:223], a[140:143], v[124:127]\n"
"v_mfma_f32_16x16x32_bf16 v[188:191], a[216:219], a[168:171], v[188:191]\n"
"ds_read_b64_tr_b16 a[254:255], v245 offset:26112\n"
"v_mfma_f32_16x16x32_bf16 v[188:191], a[220:223], a[172:175], v[188:191]\n"
"ds_read_b64_tr_b16 a[252:253], v245 offset:25600\n"
"v_mfma_f32_16x16x32_bf16 v[184:187], a[208:211], a[168:171], v[184:187]\n"
"ds_read_b64_tr_b16 a[224:225], v244 offset:16384\n"
"ds_read_b64_tr_b16 a[228:229], v244 offset:24576\n"
"v_mfma_f32_16x16x32_bf16 v[184:187], a[212:215], a[172:175], v[184:187]\n"
"buffer_load_dwordx4 v225, s[28:31], s40 offen offset:1024 lds\n"
"v_mfma_f32_16x16x32_bf16 v[180:183], a[200:203], a[168:171], v[180:183]\n"
"v_mfma_f32_16x16x32_bf16 v[180:183], a[204:207], a[172:175], v[180:183]\n"
"v_mfma_f32_16x16x32_bf16 v[176:179], a[192:195], a[168:171], v[176:179]\n"
"ds_read_b64_tr_b16 a[230:231], v244 offset:25088\n"
"v_mfma_f32_16x16x32_bf16 v[176:179], a[196:199], a[172:175], v[176:179]\n"
"ds_read_b64_tr_b16 a[244:245], v244 offset:25600\n"
"v_mfma_f32_16x16x32_bf16 v[192:195], a[192:195], a[176:179], v[192:195]\n"
"ds_read_b64_tr_b16 a[226:227], v244 offset:16896\n"
"ds_read_b64_tr_b16 a[250:251], v245 offset:17920\n"
"v_mfma_f32_16x16x32_bf16 v[192:195], a[196:199], a[180:183], v[192:195]\n"
"buffer_load_dwordx4 v226, s[28:31], s40 offen offset:2048 lds\n"
"v_mfma_f32_16x16x32_bf16 v[196:199], a[200:203], a[176:179], v[196:199]\n"
"v_mfma_f32_16x16x32_bf16 v[196:199], a[204:207], a[180:183], v[196:199]\n"
"v_mfma_f32_16x16x32_bf16 v[200:203], a[208:211], a[176:179], v[200:203]\n"
"ds_read_b64_tr_b16 a[236:237], v245 offset:24576\n"
"ds_read_b64_tr_b16 a[246:247], v244 offset:26112\n"
"v_mfma_f32_16x16x32_bf16 v[200:203], a[212:215], a[180:183], v[200:203]\n"
"ds_read_b64_tr_b16 a[240:241], v244 offset:17408\n"
"v_mfma_f32_16x16x32_bf16 v[204:207], a[216:219], a[176:179], v[204:207]\n"
"ds_read_b64_tr_b16 a[242:243], v244 offset:17920\n"
"ds_read_b64_tr_b16 a[248:249], v245 offset:17408\n"
"v_mfma_f32_16x16x32_bf16 v[204:207], a[220:223], a[180:183], v[204:207]\n"
"s_waitcnt lgkmcnt(7)\n"
"buffer_load_dwordx4 v227, s[28:31], s40 offen offset:3072 lds\n"
"v_mfma_f32_16x16x32_bf16 v[220:223], a[216:219], a[184:187], v[220:223]\n"
"s_cmp_eq_u32 s47, 8\n"
"v_mfma_f32_16x16x32_bf16 v[220:223], a[220:223], a[188:191], v[220:223]\n"
"s_cselect_b32 s41, s52, s58\n"
"s_nop 1\n"
"v_pk_fma_f32 v[176:177], v[176:177], s[70:71], s[70:71] op_sel:[0,0,1] op_sel_hi:[1,0,1]\nv_pk_fma_f32 v[178:179], v[178:179], s[70:71], s[70:71] op_sel:[0,0,1] op_sel_hi:[1,0,1]\nv_pk_fma_f32 v[180:181], v[180:181], s[70:71], s[70:71] op_sel:[0,0,1] op_sel_hi:[1,0,1]\nv_pk_fma_f32 v[182:183], v[182:183], s[70:71], s[70:71] op_sel:[0,0,1] op_sel_hi:[1,0,1]\nv_pk_fma_f32 v[184:185], v[184:185], s[70:71], s[70:71] op_sel:[0,0,1] op_sel_hi:[1,0,1]\nv_pk_fma_f32 v[186:187], v[186:187], s[70:71], s[70:71] op_sel:[0,0,1] op_sel_hi:[1,0,1]\nv_pk_fma_f32 v[188:189], v[188:189], s[70:71], s[70:71] op_sel:[0,0,1] op_sel_hi:[1,0,1]\nv_pk_fma_f32 v[190:191], v[190:191], s[70:71], s[70:71] op_sel:[0,0,1] op_sel_hi:[1,0,1]\nv_pk_fma_f32 v[192:193], v[192:193], s[70:71], s[70:71] op_sel:[0,0,1] op_sel_hi:[1,0,1]\nv_pk_fma_f32 v[194:195], v[194:195], s[70:71], s[70:71] op_sel:[0,0,1] op_sel_hi:[1,0,1]\nv_pk_fma_f32 v[196:197], v[196:197], s[70:71], s[70:71] op_sel:[0,0,1] op_sel_hi:[1,0,1]\nv_pk_fma_f32 v[198:199], v[198:199], s[70:71], s[70:71] op_sel:[0,0,1] op_sel_hi:[1,0,1]\nv_pk_fma_f32 v[200:201], v[200:201], s[70:71], s[70:71] op_sel:[0,0,1] op_sel_hi:[1,0,1]\nv_pk_fma_f32 v[202:203], v[202:203], s[70:71], s[70:71] op_sel:[0,0,1] op_sel_hi:[1,0,1]\nv_pk_fma_f32 v[204:205], v[204:205], s[70:71], s[70:71] op_sel:[0,0,1] op_sel_hi:[1,0,1]\nv_pk_fma_f32 v[206:207], v[206:207], s[70:71], s[70:71] op_sel:[0,0,1] op_sel_hi:[1,0,1]\n"
"v_mfma_f32_16x16x32_bf16 v[216:219], a[208:211], a[184:187], v[216:219]\n"
"s_add_u32 s48, s48, s41\n"
"v_cvt_pk_bf16_f32 v192, v192, v193\n"
"v_cvt_pk_bf16_f32 v193, v194, v195\n"
"v_mfma_f32_16x16x32_bf16 v[216:219], a[212:215], a[188:191], v[216:219]\n"
"s_sub_u32 s41, s48, 5242880\n"
"v_cvt_pk_bf16_f32 v176, v176, v177\n"
"v_cvt_pk_bf16_f32 v177, v178, v179\n"
"v_mfma_f32_16x16x32_bf16 v[212:215], a[200:203], a[184:187], v[212:215]\n"
"s_min_u32 s48, s48, s41\n"
"v_cvt_pk_bf16_f32 v178, v180, v181\n"
"v_cvt_pk_bf16_f32 v179, v182, v183\n"
"v_mfma_f32_16x16x32_bf16 v[212:215], a[204:207], a[188:191], v[212:215]\n"
"ds_read_b64_tr_b16 a[234:235], v245 offset:16896\n"
"v_xor_b32 v246, s74, v246\n"
"v_mfma_f32_16x16x32_bf16 v[208:211], a[192:195], a[184:187], v[208:211]\n"
"s_mov_b32 m0, s50\n"
"v_and_b32 v182, 0xf0, v242\n"
"v_cvt_pk_bf16_f32 v200, v200, v201\n"
"v_mfma_f32_16x16x32_bf16 v[208:211], a[196:199], a[188:191], v[208:211]\n"
"buffer_load_dwordx4 v224, s[28:31], s48 offen lds\n"
"v_mfma_f32_16x16x32_bf16 v[160:163], a[192:195], a[160:163], v[160:163]\n"
"v_bfe_u32 v180, v242, 9, 1\n"
"v_lshl_add_u32 v180, v180, 8, v182\n"
"v_mfma_f32_16x16x32_bf16 v[160:163], a[196:199], a[164:167], v[160:163]\n"
"v_bfe_u32 v182, v242, 8, 1\n"
"v_cvt_pk_bf16_f32 v184, v184, v185\n"
"v_mfma_f32_16x16x32_bf16 v[164:167], a[200:203], a[160:163], v[164:167]\n"
"ds_read_b64_tr_b16 a[232:233], v245 offset:16384\n"
"v_xor_b32 v247, s74, v247\n"
"v_mfma_f32_16x16x32_bf16 v[164:167], a[204:207], a[164:167], v[164:167]\n"
"buffer_load_dwordx4 v225, s[28:31], s48 offen offset:1024 lds\n"
"v_mfma_f32_16x16x32_bf16 v[168:171], a[208:211], a[160:163], v[168:171]\n"
"v_cvt_pk_bf16_f32 v201, v202, v203\n"
"v_xor_b32 v253, s74, v253\n"
"v_mfma_f32_16x16x32_bf16 v[168:171], a[212:215], a[164:167], v[168:171]\n"
"v_cvt_pk_bf16_f32 v194, v196, v197\n"
"v_xor_b32 v244, s74, v244\n"
"v_mfma_f32_16x16x32_bf16 v[172:175], a[216:219], a[160:163], v[172:175]\n"
"v_lshl_add_u32 v180, v182, 3, v180\n"
"v_cvt_pk_bf16_f32 v185, v186, v187\n"
"v_mfma_f32_16x16x32_bf16 v[172:175], a[220:223], a[164:167], v[172:175]\n"
"v_cvt_pk_bf16_f32 v186, v188, v189\n"
"v_cvt_pk_bf16_f32 v187, v190, v191\n"
"v_mfma_f32_16x16x32_bf16 v[140:143], a[216:219], a[144:147], v[140:143]\n"
"v_cvt_pk_bf16_f32 v203, v206, v207\n"
"v_bfe_u32 v181, v242, 9, 1\n"
"v_mfma_f32_16x16x32_bf16 v[140:143], a[220:223], a[148:151], v[140:143]\n"
"buffer_load_dwordx4 v226, s[28:31], s48 offen offset:2048 lds\n"
"v_mfma_f32_16x16x32_bf16 v[136:139], a[208:211], a[144:147], v[136:139]\n"
"v_xor_b32_e64 v182, s42, 1\n"
"v_lshl_add_u32 v180, v182, 10, v180\n"
"v_mfma_f32_16x16x32_bf16 v[136:139], a[212:215], a[148:151], v[136:139]\n"
"v_cvt_pk_bf16_f32 v202, v204, v205\n"
"v_xor_b32 v252, s74, v252\n"
"v_mfma_f32_16x16x32_bf16 v[132:135], a[200:203], a[144:147], v[132:135]\n"
"v_add_u32 v180, 94208, v180\n"
"v_and_b32 v182, 0xf0, v242\n"
"v_mfma_f32_16x16x32_bf16 v[132:135], a[204:207], a[148:151], v[132:135]\n"
"ds_write2st64_b64 v180, v[184:185], v[186:187] offset0:64 offset1:65\n"
"v_pk_fma_f32 v[208:209], v[208:209], s[70:71], s[70:71] op_sel:[0,0,1] op_sel_hi:[1,0,1]\nv_pk_fma_f32 v[210:211], v[210:211], s[70:71], s[70:71] op_sel:[0,0,1] op_sel_hi:[1,0,1]\nv_pk_fma_f32 v[212:213], v[212:213], s[70:71], s[70:71] op_sel:[0,0,1] op_sel_hi:[1,0,1]\nv_pk_fma_f32 v[214:215], v[214:215], s[70:71], s[70:71] op_sel:[0,0,1] op_sel_hi:[1,0,1]\nv_pk_fma_f32 v[216:217], v[216:217], s[70:71], s[70:71] op_sel:[0,0,1] op_sel_hi:[1,0,1]\nv_pk_fma_f32 v[218:219], v[218:219], s[70:71], s[70:71] op_sel:[0,0,1] op_sel_hi:[1,0,1]\nv_pk_fma_f32 v[220:221], v[220:221], s[70:71], s[70:71] op_sel:[0,0,1] op_sel_hi:[1,0,1]\nv_pk_fma_f32 v[222:223], v[222:223], s[70:71], s[70:71] op_sel:[0,0,1] op_sel_hi:[1,0,1]\nv_pk_fma_f32 v[160:161], v[160:161], s[70:71], s[70:71] op_sel:[0,0,1] op_sel_hi:[1,0,1]\nv_pk_fma_f32 v[162:163], v[162:163], s[70:71], s[70:71] op_sel:[0,0,1] op_sel_hi:[1,0,1]\nv_pk_fma_f32 v[164:165], v[164:165], s[70:71], s[70:71] op_sel:[0,0,1] op_sel_hi:[1,0,1]\nv_pk_fma_f32 v[166:167], v[166:167], s[70:71], s[70:71] op_sel:[0,0,1] op_sel_hi:[1,0,1]\nv_pk_fma_f32 v[168:169], v[168:169], s[70:71], s[70:71] op_sel:[0,0,1] op_sel_hi:[1,0,1]\nv_pk_fma_f32 v[170:171], v[170:171], s[70:71], s[70:71] op_sel:[0,0,1] op_sel_hi:[1,0,1]\nv_pk_fma_f32 v[172:173], v[172:173], s[70:71], s[70:71] op_sel:[0,0,1] op_sel_hi:[1,0,1]\nv_pk_fma_f32 v[174:175], v[174:175], s[70:71], s[70:71] op_sel:[0,0,1] op_sel_hi:[1,0,1]\n"
"v_cvt_pk_bf16_f32 v216, v216, v217\n"
"v_mfma_f32_16x16x32_bf16 v[128:131], a[192:195], a[144:147], v[128:131]\n"
"v_cvt_pk_bf16_f32 v168, v168, v169\n"
"v_xor_b32 v250, s74, v250\n"
"v_mfma_f32_16x16x32_bf16 v[128:131], a[196:199], a[148:151], v[128:131]\n"
"buffer_load_dwordx4 v227, s[28:31], s48 offen offset:3072 lds\n"
"v_mfma_f32_16x16x32_bf16 v[144:147], a[192:195], a[152:155], v[144:147]\n"
"s_xor_b32 s50, s50, s74\n"
"v_cvt_pk_bf16_f32 v208, v208, v209\n"
"v_xor_b32 v251, s74, v251\n"
"v_mfma_f32_16x16x32_bf16 v[144:147], a[196:199], a[156:159], v[144:147]\n"
"v_cvt_pk_bf16_f32 v217, v218, v219\n"
"v_cvt_pk_bf16_f32 v169, v170, v171\n"
"v_mfma_f32_16x16x32_bf16 v[148:151], a[200:203], a[152:155], v[148:151]\n"
"v_cvt_pk_bf16_f32 v195, v198, v199\n"
"v_cvt_pk_bf16_f32 v209, v210, v211\n"
"v_mfma_f32_16x16x32_bf16 v[148:151], a[204:207], a[156:159], v[148:151]\n"
"ds_write2st64_b64 v180, v[192:193], v[194:195] offset0:88 offset1:89\n"
"v_pk_fma_f32 v[112:113], v[112:113], s[70:71], s[70:71] op_sel:[0,0,1] op_sel_hi:[1,0,1]\nv_pk_fma_f32 v[114:115], v[114:115], s[70:71], s[70:71] op_sel:[0,0,1] op_sel_hi:[1,0,1]\nv_pk_fma_f32 v[116:117], v[116:117], s[70:71], s[70:71] op_sel:[0,0,1] op_sel_hi:[1,0,1]\nv_pk_fma_f32 v[118:119], v[118:119], s[70:71], s[70:71] op_sel:[0,0,1] op_sel_hi:[1,0,1]\nv_pk_fma_f32 v[120:121], v[120:121], s[70:71], s[70:71] op_sel:[0,0,1] op_sel_hi:[1,0,1]\nv_pk_fma_f32 v[122:123], v[122:123], s[70:71], s[70:71] op_sel:[0,0,1] op_sel_hi:[1,0,1]\nv_pk_fma_f32 v[124:125], v[124:125], s[70:71], s[70:71] op_sel:[0,0,1] op_sel_hi:[1,0,1]\nv_pk_fma_f32 v[126:127], v[126:127], s[70:71], s[70:71] op_sel:[0,0,1] op_sel_hi:[1,0,1]\nv_pk_fma_f32 v[128:129], v[128:129], s[70:71], s[70:71] op_sel:[0,0,1] op_sel_hi:[1,0,1]\nv_pk_fma_f32 v[130:131], v[130:131], s[70:71], s[70:71] op_sel:[0,0,1] op_sel_hi:[1,0,1]\nv_pk_fma_f32 v[132:133], v[132:133], s[70:71], s[70:71] op_sel:[0,0,1] op_sel_hi:[1,0,1]\nv_pk_fma_f32 v[134:135], v[134:135], s[70:71], s[70:71] op_sel:[0,0,1] op_sel_hi:[1,0,1]\nv_pk_fma_f32 v[136:137], v[136:137], s[70:71], s[70:71] op_sel:[0,0,1] op_sel_hi:[1,0,1]\nv_pk_fma_f32 v[138:139], v[138:139], s[70:71], s[70:71] op_sel:[0,0,1] op_sel_hi:[1,0,1]\nv_pk_fma_f32 v[140:141], v[140:141], s[70:71], s[70:71] op_sel:[0,0,1] op_sel_hi:[1,0,1]\nv_pk_fma_f32 v[142:143], v[142:143], s[70:71], s[70:71] op_sel:[0,0,1] op_sel_hi:[1,0,1]\n"
"v_mfma_f32_16x16x32_bf16 v[152:155], a[208:211], a[152:155], v[152:155]\n"
"v_cvt_pk_bf16_f32 v218, v220, v221\n"
"v_cvt_pk_bf16_f32 v136, v136, v137\n"
"v_mfma_f32_16x16x32_bf16 v[152:155], a[212:215], a[156:159], v[152:155]\n"
"v_cvt_pk_bf16_f32 v137, v138, v139\n"
"v_cvt_pk_bf16_f32 v139, v142, v143\n"
"v_mfma_f32_16x16x32_bf16 v[156:159], a[216:219], a[152:155], v[156:159]\n"
"v_cvt_pk_bf16_f32 v128, v128, v129\n"
"v_cvt_pk_bf16_f32 v138, v140, v141\n"
"v_mfma_f32_16x16x32_bf16 v[156:159], a[220:223], a[156:159], v[156:159]\n"
"s_waitcnt lgkmcnt(1)\n"
"v_cvt_pk_bf16_f32 v112, v112, v113\n"
"v_xor_b32 v245, s74, v245\n"
"v_mfma_f32_16x16x32_bf16 v[96:99], a[224:227], a[184:187], v[96:99]\n"
"v_cvt_pk_bf16_f32 v120, v120, v121\n"
"v_cvt_pk_bf16_f32 v129, v130, v131\n"
"v_mfma_f32_16x16x32_bf16 v[96:99], a[228:231], a[188:191], v[96:99]\n"
"ds_write2st64_b64 v180, v[200:201], v[202:203] offset0:96 offset1:97\n"
"v_cvt_pk_bf16_f32 v219, v222, v223\n"
"v_cvt_pk_bf16_f32 v170, v172, v173\n"
"v_cvt_pk_bf16_f32 v131, v134, v135\n"
"v_lshl_add_u32 v181, v181, 8, v182\n"
"v_bfe_u32 v182, v242, 8, 1\n"
"v_lshl_add_u32 v181, v182, 9, v181\n"
"v_mfma_f32_16x16x32_bf16 v[100:103], a[232:235], a[184:187], v[100:103]\n"
"s_waitcnt vmcnt(10)\n"
"v_permlane16_swap_b32 v136, v138\n"
"v_mfma_f32_16x16x32_bf16 v[100:103], a[236:239], a[188:191], v[100:103]\n"
"s_barrier\n"
"ds_write2st64_b64 v180, v[216:217], v[218:219] offset0:128 offset1:129\n"
"v_pk_fma_f32 v[144:145], v[144:145], s[70:71], s[70:71] op_sel:[0,0,1] op_sel_hi:[1,0,1]\nv_pk_fma_f32 v[146:147], v[146:147], s[70:71], s[70:71] op_sel:[0,0,1] op_sel_hi:[1,0,1]\nv_pk_fma_f32 v[148:149], v[148:149], s[70:71], s[70:71] op_sel:[0,0,1] op_sel_hi:[1,0,1]\nv_pk_fma_f32 v[150:151], v[150:151], s[70:71], s[70:71] op_sel:[0,0,1] op_sel_hi:[1,0,1]\nv_pk_fma_f32 v[152:153], v[152:153], s[70:71], s[70:71] op_sel:[0,0,1] op_sel_hi:[1,0,1]\nv_pk_fma_f32 v[154:155], v[154:155], s[70:71], s[70:71] op_sel:[0,0,1] op_sel_hi:[1,0,1]\nv_pk_fma_f32 v[156:157], v[156:157], s[70:71], s[70:71] op_sel:[0,0,1] op_sel_hi:[1,0,1]\nv_pk_fma_f32 v[158:159], v[158:159], s[70:71], s[70:71] op_sel:[0,0,1] op_sel_hi:[1,0,1]\n"
"v_cvt_pk_bf16_f32 v144, v144, v145\n"
"v_mfma_f32_16x16x32_bf16 v[104:107], a[240:243], a[184:187], v[104:107]\n"
"s_mov_b32 m0, s51\n"
"v_cvt_pk_bf16_f32 v171, v174, v175\n"
"v_cvt_pk_bf16_f32 v121, v122, v123\n"
"v_mfma_f32_16x16x32_bf16 v[104:107], a[244:247], a[188:191], v[104:107]\n"
"buffer_load_dwordx4 v248, s[24:27], s49 offen lds\n"
"v_mfma_f32_16x16x32_bf16 v[108:111], a[248:251], a[184:187], v[108:111]\n"
"v_xor_b32_e64 v182, s42, 0\n"
"v_lshl_add_u32 v181, v182, 10, v181\n"
"v_mfma_f32_16x16x32_bf16 v[108:111], a[252:255], a[188:191], v[108:111]\n"
"ds_read_b64_tr_b16 a[218:219], v245 offset:1536\n"
"v_cvt_pk_bf16_f32 v211, v214, v215\n"
"v_mfma_f32_16x16x32_bf16 v[92:95], a[248:251], a[176:179], v[92:95]\n"
"v_cvt_pk_bf16_f32 v160, v160, v161\n"
"v_cvt_pk_bf16_f32 v145, v146, v147\n"
"v_mfma_f32_16x16x32_bf16 v[92:95], a[252:255], a[180:183], v[92:95]\n"
"ds_write2st64_b64 v180, v[176:177], v[178:179] offset0:0 offset1:1\n"
"v_cvt_pk_bf16_f32 v122, v124, v125\n"
"v_cvt_pk_bf16_f32 v123, v126, v127\n"
"v_cvt_pk_bf16_f32 v146, v148, v149\n"
"v_cvt_pk_bf16_f32 v152, v152, v153\n"
"v_cvt_pk_bf16_f32 v153, v154, v155\n"
"v_cvt_pk_bf16_f32 v155, v158, v159\n"
"v_mfma_f32_16x16x32_bf16 v[88:91], a[240:243], a[176:179], v[88:91]\n"
"v_cvt_pk_bf16_f32 v210, v212, v213\n"
"v_cvt_pk_bf16_f32 v113, v114, v115\n"
"v_mfma_f32_16x16x32_bf16 v[88:91], a[244:247], a[180:183], v[88:91]\n"
"buffer_load_dwordx4 v249, s[24:27], s49 offen offset:1024 lds\n"
"v_mfma_f32_16x16x32_bf16 v[84:87], a[232:235], a[176:179], v[84:87]\n"
"v_cvt_pk_bf16_f32 v161, v162, v163\n"
"v_cvt_pk_bf16_f32 v154, v156, v157\n"
"v_mfma_f32_16x16x32_bf16 v[84:87], a[236:239], a[180:183], v[84:87]\n"
"s_nop 0\n"
"v_permlane16_swap_b32 v152, v154\n"
"v_mfma_f32_16x16x32_bf16 v[80:83], a[224:227], a[176:179], v[80:83]\n"
"ds_read_b64_tr_b16 a[196:197], v244 offset:8192\n"
"ds_read_b64_tr_b16 a[214:215], v244 offset:9728\n"
"v_mfma_f32_16x16x32_bf16 v[80:83], a[228:231], a[180:183], v[80:83]\n"
"ds_write2st64_b64 v180, v[208:209], v[210:211] offset0:120 offset1:121\n"
"v_cvt_pk_bf16_f32 v114, v116, v117\n"
"v_permlane16_swap_b32 v137, v139\n"
"v_permlane16_swap_b32 v144, v146\n"
"v_mfma_f32_16x16x32_bf16 v[64:67], a[224:227], a[168:171], v[64:67]\n"
"v_permlane16_swap_b32 v121, v123\n"
"v_mfma_f32_16x16x32_bf16 v[64:67], a[228:231], a[172:175], v[64:67]\n"
"buffer_load_dwordx4 v254, s[24:27], s49 offen offset:2048 lds\n"
"v_mfma_f32_16x16x32_bf16 v[68:71], a[232:235], a[168:171], v[68:71]\n"
"v_permlane16_swap_b32 v120, v122\n"
"v_mfma_f32_16x16x32_bf16 v[68:71], a[236:239], a[172:175], v[68:71]\n"
"v_cvt_pk_bf16_f32 v115, v118, v119\n"
"v_accvgpr_write_b32 a24, v136\n"
"v_mfma_f32_16x16x32_bf16 v[72:75], a[240:243], a[168:171], v[72:75]\n"
"ds_read_b64_tr_b16 a[206:207], v245 offset:8704\n"
"v_add_u32 v181, 94208, v181\n"
"v_mfma_f32_16x16x32_bf16 v[72:75], a[244:247], a[172:175], v[72:75]\n"
"ds_read_b64_tr_b16 a[216:217], v245 offset:1024\n"
"v_permlane16_swap_b32 v112, v114\n"
"v_permlane16_swap_b32 v153, v155\n"
"v_pk_fma_f32 v[96:97], v[96:97], s[70:71], s[70:71] op_sel:[0,0,1] op_sel_hi:[1,0,1]\nv_pk_fma_f32 v[98:99], v[98:99], s[70:71], s[70:71] op_sel:[0,0,1] op_sel_hi:[1,0,1]\nv_pk_fma_f32 v[100:101], v[100:101], s[70:71], s[70:71] op_sel:[0,0,1] op_sel_hi:[1,0,1]\nv_pk_fma_f32 v[102:103], v[102:103], s[70:71], s[70:71] op_sel:[0,0,1] op_sel_hi:[1,0,1]\nv_pk_fma_f32 v[104:105], v[104:105], s[70:71], s[70:71] op_sel:[0,0,1] op_sel_hi:[1,0,1]\nv_pk_fma_f32 v[106:107], v[106:107], s[70:71], s[70:71] op_sel:[0,0,1] op_sel_hi:[1,0,1]\nv_pk_fma_f32 v[108:109], v[108:109], s[70:71], s[70:71] op_sel:[0,0,1] op_sel_hi:[1,0,1]\nv_pk_fma_f32 v[110:111], v[110:111], s[70:71], s[70:71] op_sel:[0,0,1] op_sel_hi:[1,0,1]\nv_pk_fma_f32 v[80:81], v[80:81], s[70:71], s[70:71] op_sel:[0,0,1] op_sel_hi:[1,0,1]\nv_pk_fma_f32 v[82:83], v[82:83], s[70:71], s[70:71] op_sel:[0,0,1] op_sel_hi:[1,0,1]\nv_pk_fma_f32 v[84:85], v[84:85], s[70:71], s[70:71] op_sel:[0,0,1] op_sel_hi:[1,0,1]\nv_pk_fma_f32 v[86:87], v[86:87], s[70:71], s[70:71] op_sel:[0,0,1] op_sel_hi:[1,0,1]\nv_pk_fma_f32 v[88:89], v[88:89], s[70:71], s[70:71] op_sel:[0,0,1] op_sel_hi:[1,0,1]\nv_pk_fma_f32 v[90:91], v[90:91], s[70:71], s[70:71] op_sel:[0,0,1] op_sel_hi:[1,0,1]\nv_pk_fma_f32 v[92:93], v[92:93], s[70:71], s[70:71] op_sel:[0,0,1] op_sel_hi:[1,0,1]\nv_pk_fma_f32 v[94:95], v[94:95], s[70:71], s[70:71] op_sel:[0,0,1] op_sel_hi:[1,0,1]\n"
"v_mfma_f32_16x16x32_bf16 v[76:79], a[248:251], a[168:171], v[76:79]\n"
"ds_read_b64_tr_b16 a[208:209], v244 offset:1024\n"
"v_cvt_pk_bf16_f32 v80, v80, v81\n"
"v_mfma_f32_16x16x32_bf16 v[76:79], a[252:255], a[172:175], v[76:79]\n"
"buffer_load_dwordx4 v255, s[24:27], s49 offen offset:3072 lds\n"
"v_mfma_f32_16x16x32_bf16 v[60:63], a[248:251], a[160:163], v[60:63]\n"
"ds_read_b64_tr_b16 a[192:193], v244 offset:0\n"
"ds_read_b64_tr_b16 a[222:223], v245 offset:9728\n"
"v_mfma_f32_16x16x32_bf16 v[60:63], a[252:255], a[164:167], v[60:63]\n"
"v_permlane16_swap_b32 v168, v170\n"
"v_mfma_f32_16x16x32_bf16 v[56:59], a[240:243], a[160:163], v[56:59]\n"
"v_permlane16_swap_b32 v113, v115\n"
"v_mfma_f32_16x16x32_bf16 v[56:59], a[244:247], a[164:167], v[56:59]\n"
"s_add_u32 s40, s49, 40960\n"
"ds_read_b64_tr_b16 a[210:211], v244 offset:1536\n"
"v_add_u32 v182, s75, v181\n"
"v_mfma_f32_16x16x32_bf16 v[52:55], a[232:235], a[160:163], v[52:55]\n"
"s_add_u32 m0, s51, 4096\n"
"v_cvt_pk_bf16_f32 v130, v132, v133\n"
"v_accvgpr_write_b32 a25, v137\n"
"v_mfma_f32_16x16x32_bf16 v[52:55], a[236:239], a[164:167], v[52:55]\n"
"buffer_load_dwordx4 v248, s[24:27], s40 offen lds\n"
"v_mfma_f32_16x16x32_bf16 v[48:51], a[224:227], a[160:163], v[48:51]\n"
"s_waitcnt vmcnt(63) lgkmcnt(3)\n"
"v_permlane16_swap_b32 v129, v131\n"
"v_mfma_f32_16x16x32_bf16 v[48:51], a[228:231], a[164:167], v[48:51]\n"
"s_barrier\n"
"v_mfma_f32_16x16x32_bf16 v[0:3], a[224:227], a[136:139], v[0:3]\n"
"ds_read_b128 a[36:39], v181 offset:61440\n"
"v_accvgpr_write_b32 a43, v155\n"
"v_mfma_f32_16x16x32_bf16 v[0:3], a[228:231], a[140:143], v[0:3]\n"
"ds_read_b128 a[28:31], v181 offset:49152\n"
"v_accvgpr_write_b32 a10, v122\n"
"v_mfma_f32_16x16x32_bf16 v[4:7], a[232:235], a[136:139], v[4:7]\n"
"v_cvt_pk_bf16_f32 v96, v96, v97\n"
"v_cvt_pk_bf16_f32 v104, v104, v105\n"
"v_mfma_f32_16x16x32_bf16 v[4:7], a[236:239], a[140:143], v[4:7]\n"
"buffer_load_dwordx4 v249, s[24:27], s40 offen offset:1024 lds\n"
"v_mfma_f32_16x16x32_bf16 v[8:11], a[240:243], a[136:139], v[8:11]\n"
"v_cvt_pk_bf16_f32 v97, v98, v99\n"
"v_cvt_pk_bf16_f32 v99, v102, v103\n"
"v_mfma_f32_16x16x32_bf16 v[8:11], a[244:247], a[140:143], v[8:11]\n"
"ds_read_b64_tr_b16 a[220:221], v245 offset:9216\n"
"ds_read_b128 a[12:15], v181 offset:32768\n"
"v_mfma_f32_16x16x32_bf16 v[12:15], a[248:251], a[136:139], v[12:15]\n"
"ds_read_b128 a[164:167], v253 offset:0\n"
"v_accvgpr_write_b32 a32, v144\n"
"v_mfma_f32_16x16x32_bf16 v[12:15], a[252:255], a[140:143], v[12:15]\n"
"ds_read_b128 a[140:143], v247 offset:0\n"
"v_accvgpr_write_b32 a3, v115\n"
"v_mfma_f32_16x16x32_bf16 v[28:31], a[248:251], a[144:147], v[28:31]\n"
"ds_read_b64_tr_b16 a[194:195], v244 offset:512\n"
"v_cvt_pk_bf16_f32 v162, v164, v165\n"
"v_mfma_f32_16x16x32_bf16 v[28:31], a[252:255], a[148:151], v[28:31]\n"
"buffer_load_dwordx4 v254, s[24:27], s40 offen offset:2048 lds\n"
"v_mfma_f32_16x16x32_bf16 v[24:27], a[240:243], a[144:147], v[24:27]\n"
"s_add_u32 s49, s49, 128\n"
"v_cvt_pk_bf16_f32 v147, v150, v151\n"
"v_accvgpr_write_b32 a41, v153\n"
"v_mfma_f32_16x16x32_bf16 v[24:27], a[244:247], a[148:151], v[24:27]\n"
"s_sub_u32 s41, s49, 1280\n"
"v_cvt_pk_bf16_f32 v98, v100, v101\n"
"v_cvt_pk_bf16_f32 v88, v88, v89\n"
"v_mfma_f32_16x16x32_bf16 v[20:23], a[232:235], a[144:147], v[20:23]\n"
"s_min_u32 s49, s49, s41\n"
"ds_read_b128 a[136:139], v246 offset:0\n"
"v_accvgpr_write_b32 a19, v131\n"
"v_mfma_f32_16x16x32_bf16 v[20:23], a[236:239], a[148:151], v[20:23]\n"
"s_xor_b32 s51, s51, s74\n"
"ds_read_b64_tr_b16 a[198:199], v244 offset:8704\n"
"ds_read_b128 a[44:47], v182 offset:0\n"
"v_mfma_f32_16x16x32_bf16 v[16:19], a[224:227], a[144:147], v[16:19]\n"
"ds_read_b64_tr_b16 a[212:213], v244 offset:9216\n"
"v_cvt_pk_bf16_f32 v163, v166, v167\n"
"v_mfma_f32_16x16x32_bf16 v[16:19], a[228:231], a[148:151], v[16:19]\n"
"ds_read_b128 a[148:151], v247 offset:2048\n"
"v_accvgpr_write_b32 a11, v123\n"
"v_mfma_f32_16x16x32_bf16 v[32:35], a[224:227], a[152:155], v[32:35]\n"
"ds_read_b128 a[4:7], v181 offset:0\n"
"v_and_b32 v102, 0xf0, v242\n"
"v_mfma_f32_16x16x32_bf16 v[32:35], a[228:231], a[156:159], v[32:35]\n"
"ds_read_b64_tr_b16 a[202:203], v245 offset:512\n"
"ds_read_b64_tr_b16 a[204:205], v245 offset:8192\n"
"v_mfma_f32_16x16x32_bf16 v[36:39], a[232:235], a[152:155], v[36:39]\n"
"ds_read_b64_tr_b16 a[200:201], v245 offset:0\n"
"v_cvt_pk_bf16_f32 v89, v90, v91\n"
"v_mfma_f32_16x16x32_bf16 v[36:39], a[236:239], a[156:159], v[36:39]\n"
"ds_read_b128 a[20:23], v181 offset:45056\n"
"v_accvgpr_write_b32 a27, v139\n"
"v_mfma_f32_16x16x32_bf16 v[40:43], a[240:243], a[152:155], v[40:43]\n"
"v_accvgpr_write_b32 a0, v112\n"
"v_accvgpr_write_b32 a1, v113\n"
"v_mfma_f32_16x16x32_bf16 v[40:43], a[244:247], a[156:159], v[40:43]\n"
"ds_read_b128 a[180:183], v251 offset:2048\n"
"v_accvgpr_write_b32 a2, v114\n"
"v_mfma_f32_16x16x32_bf16 v[44:47], a[248:251], a[152:155], v[44:47]\n"
"s_add_u32 s47, s47, 1\n"
"ds_read_b128 a[144:147], v246 offset:2048\n"
"v_accvgpr_write_b32 a17, v129\n"
"v_mfma_f32_16x16x32_bf16 v[44:47], a[252:255], a[156:159], v[44:47]\n"
"s_waitcnt lgkmcnt(7)\n"
"ds_read_b128 a[160:163], v252 offset:0\n"
"v_permlane16_swap_b32 v161, v163\n"
"v_permlane16_swap_b32 v169, v171\n"
"v_accvgpr_write_b32 a9, v121\n"
"v_permlane16_swap_b32 v128, v130\n"
"v_permlane16_swap_b32 v145, v147\n"
"v_accvgpr_write_b32 a42, v154\n"
"v_bfe_u32 v100, v242, 9, 1\n"
"v_cvt_pk_bf16_f32 v91, v94, v95\n"
"v_pk_fma_f32 v[0:1], v[0:1], s[70:71], s[70:71] op_sel:[0,0,1] op_sel_hi:[1,0,1]\nv_pk_fma_f32 v[2:3], v[2:3], s[70:71], s[70:71] op_sel:[0,0,1] op_sel_hi:[1,0,1]\nv_pk_fma_f32 v[4:5], v[4:5], s[70:71], s[70:71] op_sel:[0,0,1] op_sel_hi:[1,0,1]\nv_pk_fma_f32 v[6:7], v[6:7], s[70:71], s[70:71] op_sel:[0,0,1] op_sel_hi:[1,0,1]\nv_pk_fma_f32 v[8:9], v[8:9], s[70:71], s[70:71] op_sel:[0,0,1] op_sel_hi:[1,0,1]\nv_pk_fma_f32 v[10:11], v[10:11], s[70:71], s[70:71] op_sel:[0,0,1] op_sel_hi:[1,0,1]\nv_pk_fma_f32 v[12:13], v[12:13], s[70:71], s[70:71] op_sel:[0,0,1] op_sel_hi:[1,0,1]\nv_pk_fma_f32 v[14:15], v[14:15], s[70:71], s[70:71] op_sel:[0,0,1] op_sel_hi:[1,0,1]\nv_pk_fma_f32 v[16:17], v[16:17], s[70:71], s[70:71] op_sel:[0,0,1] op_sel_hi:[1,0,1]\nv_pk_fma_f32 v[18:19], v[18:19], s[70:71], s[70:71] op_sel:[0,0,1] op_sel_hi:[1,0,1]\nv_pk_fma_f32 v[20:21], v[20:21], s[70:71], s[70:71] op_sel:[0,0,1] op_sel_hi:[1,0,1]\nv_pk_fma_f32 v[22:23], v[22:23], s[70:71], s[70:71] op_sel:[0,0,1] op_sel_hi:[1,0,1]\nv_pk_fma_f32 v[24:25], v[24:25], s[70:71], s[70:71] op_sel:[0,0,1] op_sel_hi:[1,0,1]\nv_pk_fma_f32 v[26:27], v[26:27], s[70:71], s[70:71] op_sel:[0,0,1] op_sel_hi:[1,0,1]\nv_pk_fma_f32 v[28:29], v[28:29], s[70:71], s[70:71] op_sel:[0,0,1] op_sel_hi:[1,0,1]\nv_pk_fma_f32 v[30:31], v[30:31], s[70:71], s[70:71] op_sel:[0,0,1] op_sel_hi:[1,0,1]\n"
"v_cvt_pk_bf16_f32 v16, v16, v17\n"
"v_cvt_pk_bf16_f32 v24, v24, v25\n"
"v_cvt_pk_bf16_f32 v25, v26, v27\n"
"v_mfma_f32_16x16x32_bf16 v[112:115], a[192:195], a[136:139], v[228:231]\n"
"v_cvt_pk_bf16_f32 v0, v0, v1\n"
"v_cvt_pk_bf16_f32 v8, v8, v9\n"
"v_mfma_f32_16x16x32_bf16 v[112:115], a[196:199], a[140:143], v[112:115]\n"
"s_waitcnt lgkmcnt(4)\n"
"ds_write_b128 v243, v[168:171] offset:2048\n"
"v_permlane16_swap_b32 v160, v162\n"
"v_accvgpr_write_b32 a8, v120\n"
"v_cvt_pk_bf16_f32 v81, v82, v83\n"
"v_pk_fma_f32 v[64:65], v[64:65], s[70:71], s[70:71] op_sel:[0,0,1] op_sel_hi:[1,0,1]\nv_pk_fma_f32 v[66:67], v[66:67], s[70:71], s[70:71] op_sel:[0,0,1] op_sel_hi:[1,0,1]\nv_pk_fma_f32 v[68:69], v[68:69], s[70:71], s[70:71] op_sel:[0,0,1] op_sel_hi:[1,0,1]\nv_pk_fma_f32 v[70:71], v[70:71], s[70:71], s[70:71] op_sel:[0,0,1] op_sel_hi:[1,0,1]\nv_pk_fma_f32 v[72:73], v[72:73], s[70:71], s[70:71] op_sel:[0,0,1] op_sel_hi:[1,0,1]\nv_pk_fma_f32 v[74:75], v[74:75], s[70:71], s[70:71] op_sel:[0,0,1] op_sel_hi:[1,0,1]\nv_pk_fma_f32 v[76:77], v[76:77], s[70:71], s[70:71] op_sel:[0,0,1] op_sel_hi:[1,0,1]\nv_pk_fma_f32 v[78:79], v[78:79], s[70:71], s[70:71] op_sel:[0,0,1] op_sel_hi:[1,0,1]\nv_pk_fma_f32 v[48:49], v[48:49], s[70:71], s[70:71] op_sel:[0,0,1] op_sel_hi:[1,0,1]\nv_pk_fma_f32 v[50:51], v[50:51], s[70:71], s[70:71] op_sel:[0,0,1] op_sel_hi:[1,0,1]\nv_pk_fma_f32 v[52:53], v[52:53], s[70:71], s[70:71] op_sel:[0,0,1] op_sel_hi:[1,0,1]\nv_pk_fma_f32 v[54:55], v[54:55], s[70:71], s[70:71] op_sel:[0,0,1] op_sel_hi:[1,0,1]\nv_pk_fma_f32 v[56:57], v[56:57], s[70:71], s[70:71] op_sel:[0,0,1] op_sel_hi:[1,0,1]\nv_pk_fma_f32 v[58:59], v[58:59], s[70:71], s[70:71] op_sel:[0,0,1] op_sel_hi:[1,0,1]\nv_pk_fma_f32 v[60:61], v[60:61], s[70:71], s[70:71] op_sel:[0,0,1] op_sel_hi:[1,0,1]\nv_pk_fma_f32 v[62:63], v[62:63], s[70:71], s[70:71] op_sel:[0,0,1] op_sel_hi:[1,0,1]\n"
"v_cvt_pk_bf16_f32 v64, v64, v65\n"
"v_mfma_f32_16x16x32_bf16 v[116:119], a[200:203], a[136:139], v[228:231]\n"
"s_waitcnt vmcnt(11) lgkmcnt(4)\n"
"v_accvgpr_write_b32 a18, v130\n"
"v_accvgpr_write_b32 a26, v138\n"
"v_mfma_f32_16x16x32_bf16 v[116:119], a[204:207], a[140:143], v[116:119]\n"
"s_barrier\n"
"ds_write_b128 v243, v[160:163] offset:0\n"
"v_accvgpr_write_b32 a16, v128\n"
"v_accvgpr_write_b32 a33, v145\n"
"v_cvt_pk_bf16_f32 v1, v2, v3\n"
"v_pk_fma_f32 v[32:33], v[32:33], s[70:71], s[70:71] op_sel:[0,0,1] op_sel_hi:[1,0,1]\nv_pk_fma_f32 v[34:35], v[34:35], s[70:71], s[70:71] op_sel:[0,0,1] op_sel_hi:[1,0,1]\nv_pk_fma_f32 v[36:37], v[36:37], s[70:71], s[70:71] op_sel:[0,0,1] op_sel_hi:[1,0,1]\nv_pk_fma_f32 v[38:39], v[38:39], s[70:71], s[70:71] op_sel:[0,0,1] op_sel_hi:[1,0,1]\nv_pk_fma_f32 v[40:41], v[40:41], s[70:71], s[70:71] op_sel:[0,0,1] op_sel_hi:[1,0,1]\nv_pk_fma_f32 v[42:43], v[42:43], s[70:71], s[70:71] op_sel:[0,0,1] op_sel_hi:[1,0,1]\nv_pk_fma_f32 v[44:45], v[44:45], s[70:71], s[70:71] op_sel:[0,0,1] op_sel_hi:[1,0,1]\nv_pk_fma_f32 v[46:47], v[46:47], s[70:71], s[70:71] op_sel:[0,0,1] op_sel_hi:[1,0,1]\n"
"v_cvt_pk_bf16_f32 v32, v32, v33\n"
"v_mfma_f32_16x16x32_bf16 v[120:123], a[208:211], a[136:139], v[228:231]\n"
"s_add_u32 s40, s48, 256\n"
"s_xor_b32 m0, s50, 0xc000\n"
"ds_read_b128 a[152:155], v246 offset:4096\n"
"v_mfma_f32_16x16x32_bf16 v[120:123], a[212:215], a[140:143], v[120:123]\n"
"buffer_load_dwordx4 v224, s[28:31], s40 offen lds\n"
"v_mfma_f32_16x16x32_bf16 v[124:127], a[216:219], a[136:139], v[228:231]\n"
"v_accvgpr_write_b32 a40, v152\n"
"v_cvt_pk_bf16_f32 v40, v40, v41\n"
"v_mfma_f32_16x16x32_bf16 v[124:127], a[220:223], a[140:143], v[124:127]\n"
"s_waitcnt lgkmcnt(3)\n"
"ds_read_b128 a[168:171], v250 offset:0\n"
"v_cvt_pk_bf16_f32 v105, v106, v107\n"
"v_mfma_f32_16x16x32_bf16 v[140:143], a[216:219], a[144:147], v[228:231]\n"
"ds_read_b128 a[156:159], v247 offset:4096\n"
"v_accvgpr_write_b32 a34, v146\n"
"v_mfma_f32_16x16x32_bf16 v[140:143], a[220:223], a[148:151], v[140:143]\n"
"ds_read_b128 a[172:175], v251 offset:0\n"
"v_cvt_pk_bf16_f32 v2, v4, v5\n"
"v_mfma_f32_16x16x32_bf16 v[136:139], a[208:211], a[144:147], v[228:231]\n"
"ds_read_b64_tr_b16 a[224:225], v244 offset:16384\n"
"v_lshl_add_u32 v100, v100, 8, v102\n"
"v_mfma_f32_16x16x32_bf16 v[136:139], a[212:215], a[148:151], v[136:139]\n"
"buffer_load_dwordx4 v225, s[28:31], s40 offen offset:1024 lds\n"
"v_mfma_f32_16x16x32_bf16 v[132:135], a[200:203], a[144:147], v[228:231]\n"
"v_bfe_u32 v102, v242, 8, 1\n"
"v_cvt_pk_bf16_f32 v41, v42, v43\n"
"v_mfma_f32_16x16x32_bf16 v[132:135], a[204:207], a[148:151], v[132:135]\n"
"v_accvgpr_write_b32 a35, v147\n"
"v_cvt_pk_bf16_f32 v65, v66, v67\n"
"v_mfma_f32_16x16x32_bf16 v[128:131], a[192:195], a[144:147], v[228:231]\n"
"ds_read_b64_tr_b16 a[234:235], v245 offset:16896\n"
"v_cvt_pk_bf16_f32 v72, v72, v73\n"
"v_mfma_f32_16x16x32_bf16 v[128:131], a[196:199], a[148:151], v[128:131]\n"
"s_waitcnt lgkmcnt(4)\n"
"ds_read_b64_tr_b16 a[236:237], v245 offset:24576\n"
"v_bfe_u32 v101, v242, 9, 1\n"
"v_mfma_f32_16x16x32_bf16 v[144:147], a[192:195], a[152:155], v[228:231]\n"
"s_waitcnt lgkmcnt(4)\n"
"ds_read_b64_tr_b16 a[226:227], v244 offset:16896\n"
"v_cvt_pk_bf16_f32 v9, v10, v11\n"
"v_mfma_f32_16x16x32_bf16 v[144:147], a[196:199], a[156:159], v[144:147]\n"
"buffer_load_dwordx4 v226, s[28:31], s40 offen offset:2048 lds\n"
"v_mfma_f32_16x16x32_bf16 v[148:151], a[200:203], a[152:155], v[228:231]\n"
"v_cvt_pk_bf16_f32 v3, v6, v7\n"
"v_cvt_pk_bf16_f32 v17, v18, v19\n"
"v_mfma_f32_16x16x32_bf16 v[148:151], a[204:207], a[156:159], v[148:151]\n"
"v_cvt_pk_bf16_f32 v66, v68, v69\n"
"v_cvt_pk_bf16_f32 v73, v74, v75\n"
"v_mfma_f32_16x16x32_bf16 v[152:155], a[208:211], a[152:155], v[228:231]\n"
"ds_read_b64_tr_b16 a[252:253], v245 offset:25600\n"
"v_lshl_add_u32 v100, v102, 3, v100\n"
"v_mfma_f32_16x16x32_bf16 v[152:155], a[212:215], a[156:159], v[152:155]\n"
"ds_read_b64_tr_b16 a[244:245], v244 offset:25600\n"
"ds_read_b64_tr_b16 a[250:251], v245 offset:17920\n"
"v_mfma_f32_16x16x32_bf16 v[156:159], a[216:219], a[152:155], v[228:231]\n"
"v_cvt_pk_bf16_f32 v48, v48, v49\n"
"v_cvt_pk_bf16_f32 v43, v46, v47\n"
"v_mfma_f32_16x16x32_bf16 v[156:159], a[220:223], a[156:159], v[156:159]\n"
"buffer_load_dwordx4 v227, s[28:31], s40 offen offset:3072 lds\n"
"v_mfma_f32_16x16x32_bf16 v[172:175], a[216:219], a[160:163], v[228:231]\n"
"s_cmp_eq_u32 s47, 8\n"
"v_cvt_pk_bf16_f32 v75, v78, v79\n"
"v_cvt_pk_bf16_f32 v27, v30, v31\n"
"v_mfma_f32_16x16x32_bf16 v[172:175], a[220:223], a[164:167], v[172:175]\n"
"s_cselect_b32 s41, s52, s58\n"
"v_cvt_pk_bf16_f32 v56, v56, v57\n"
"v_permlane16_swap_b32 v1, v3\n"
"v_cvt_pk_bf16_f32 v11, v14, v15\n"
"v_cvt_pk_bf16_f32 v26, v28, v29\n"
"v_cvt_pk_bf16_f32 v33, v34, v35\n"
"v_mfma_f32_16x16x32_bf16 v[168:171], a[208:211], a[160:163], v[228:231]\n"
"s_add_u32 s48, s48, s41\n"
"ds_read_b128 a[176:179], v250 offset:2048\n"
"v_cvt_pk_bf16_f32 v83, v86, v87\n"
"v_mfma_f32_16x16x32_bf16 v[168:171], a[212:215], a[164:167], v[168:171]\n"
"s_sub_u32 s41, s48, 5242880\n"
"ds_read_b64_tr_b16 a[230:231], v244 offset:25088\n"
"ds_read_b64_tr_b16 a[248:249], v245 offset:17408\n"
"v_mfma_f32_16x16x32_bf16 v[164:167], a[200:203], a[160:163], v[228:231]\n"
"s_min_u32 s48, s48, s41\n"
"ds_read_b64_tr_b16 a[232:233], v245 offset:16384\n"
"v_xor_b32_e64 v102, s42, 1\n"
"v_mfma_f32_16x16x32_bf16 v[164:167], a[204:207], a[164:167], v[164:167]\n"
"ds_read_b128 a[184:187], v250 offset:4096\n"
"v_lshl_add_u32 v100, v102, 10, v100\n"
"v_mfma_f32_16x16x32_bf16 v[160:163], a[192:195], a[160:163], v[228:231]\n"
"s_mov_b32 m0, s50\n"
"v_permlane16_swap_b32 v9, v11\n"
"v_mfma_f32_16x16x32_bf16 v[160:163], a[196:199], a[164:167], v[160:163]\n"
"buffer_load_dwordx4 v224, s[28:31], s48 offen lds\n"
"v_mfma_f32_16x16x32_bf16 v[176:179], a[192:195], a[168:171], v[228:231]\n"
"s_waitcnt lgkmcnt(5)\n"
"v_cvt_pk_bf16_f32 v49, v50, v51\n"
"v_cvt_pk_bf16_f32 v34, v36, v37\n"
"v_mfma_f32_16x16x32_bf16 v[176:179], a[196:199], a[172:175], v[176:179]\n"
"v_cvt_pk_bf16_f32 v10, v12, v13\n"
"v_and_b32 v102, 0xf0, v242\n"
"v_mfma_f32_16x16x32_bf16 v[180:183], a[200:203], a[168:171], v[228:231]\n"
"ds_read_b64_tr_b16 a[240:241], v244 offset:17408\n"
"ds_read_b64_tr_b16 a[254:255], v245 offset:26112\n"
"v_mfma_f32_16x16x32_bf16 v[180:183], a[204:207], a[172:175], v[180:183]\n"
"buffer_load_dwordx4 v225, s[28:31], s48 offen offset:1024 lds\n"
"v_mfma_f32_16x16x32_bf16 v[184:187], a[208:211], a[168:171], v[228:231]\n"
"v_cvt_pk_bf16_f32 v90, v92, v93\n"
"v_cvt_pk_bf16_f32 v42, v44, v45\n"
"v_mfma_f32_16x16x32_bf16 v[184:187], a[212:215], a[172:175], v[184:187]\n"
"v_permlane16_swap_b32 v0, v2\n"
"v_mfma_f32_16x16x32_bf16 v[188:191], a[216:219], a[168:171], v[228:231]\n"
"ds_read_b128 a[188:191], v251 offset:4096\n"
"v_accvgpr_write_b32 a51, v3\n"
"v_mfma_f32_16x16x32_bf16 v[188:191], a[220:223], a[172:175], v[188:191]\n"
"s_waitcnt lgkmcnt(3)\n"
"ds_read_b64_tr_b16 a[238:239], v245 offset:25088\n"
"v_cvt_pk_bf16_f32 v67, v70, v71\n"
"v_mfma_f32_16x16x32_bf16 v[204:207], a[216:219], a[176:179], v[228:231]\n"
"ds_read_b64_tr_b16 a[246:247], v244 offset:26112\n"
"v_add_u32 v100, 94208, v100\n"
"v_mfma_f32_16x16x32_bf16 v[204:207], a[220:223], a[180:183], v[204:207]\n"
"buffer_load_dwordx4 v226, s[28:31], s48 offen offset:2048 lds\n"
"v_mfma_f32_16x16x32_bf16 v[200:203], a[208:211], a[176:179], v[228:231]\n"
"v_accvgpr_write_b32 a50, v2\n"
"v_lshl_add_u32 v101, v101, 8, v102\n"
"v_mfma_f32_16x16x32_bf16 v[200:203], a[212:215], a[180:183], v[200:203]\n"
"v_permlane16_swap_b32 v24, v26\n"
"v_mfma_f32_16x16x32_bf16 v[196:199], a[200:203], a[176:179], v[228:231]\n"
"ds_read_b64_tr_b16 a[228:229], v244 offset:24576\n"
"v_cvt_pk_bf16_f32 v106, v108, v109\n"
"v_mfma_f32_16x16x32_bf16 v[196:199], a[204:207], a[180:183], v[196:199]\n"
"ds_write2st64_b64 v100, v[96:97], v[98:99] offset0:120 offset1:121\n"
"v_accvgpr_write_b32 a49, v1\n"
"v_cvt_pk_bf16_f32 v19, v22, v23\n"
"v_permlane16_swap_b32 v25, v27\n"
"v_cvt_pk_bf16_f32 v35, v38, v39\n"
"v_mfma_f32_16x16x32_bf16 v[192:195], a[192:195], a[176:179], v[228:231]\n"
"v_cvt_pk_bf16_f32 v74, v76, v77\n"
"v_cvt_pk_bf16_f32 v18, v20, v21\n"
"v_mfma_f32_16x16x32_bf16 v[192:195], a[196:199], a[180:183], v[192:195]\n"
"buffer_load_dwordx4 v227, s[28:31], s48 offen offset:3072 lds\n"
"v_mfma_f32_16x16x32_bf16 v[208:211], a[192:195], a[184:187], v[228:231]\n"
"s_waitcnt lgkmcnt(2)\n"
"s_xor_b32 s50, s50, s74\n"
"v_permlane16_swap_b32 v33, v35\n"
"v_mfma_f32_16x16x32_bf16 v[208:211], a[196:199], a[188:191], v[208:211]\n"
"v_permlane16_swap_b32 v40, v42\n"
"v_mfma_f32_16x16x32_bf16 v[212:215], a[200:203], a[184:187], v[228:231]\n"
"v_permlane16_swap_b32 v17, v19\n"
"v_mfma_f32_16x16x32_bf16 v[212:215], a[204:207], a[188:191], v[212:215]\n"
"ds_read_b64_tr_b16 a[242:243], v244 offset:17920\n"
"v_cvt_pk_bf16_f32 v107, v110, v111\n"
"v_mfma_f32_16x16x32_bf16 v[216:219], a[208:211], a[184:187], v[228:231]\n"
"v_permlane16_swap_b32 v16, v18\n"
"v_mfma_f32_16x16x32_bf16 v[216:219], a[212:215], a[188:191], v[216:219]\n"
"ds_write2st64_b64 v100, v[104:105], v[106:107] offset0:128 offset1:129\n"
"v_cvt_pk_bf16_f32 v82, v84, v85\n"
"v_accvgpr_write_b32 a75, v27\n"
"v_permlane16_swap_b32 v41, v43\n"
"v_mfma_f32_16x16x32_bf16 v[220:223], a[216:219], a[184:187], v[228:231]\n"
"v_accvgpr_write_b32 a48, v0\n"
"v_accvgpr_write_b32 a64, v16\n"
"v_mfma_f32_16x16x32_bf16 v[220:223], a[220:223], a[188:191], v[220:223]\n"
"ds_write2st64_b64 v100, v[80:81], v[82:83] offset0:88 offset1:89\n"
"v_cvt_pk_bf16_f32 v57, v58, v59\n"
"v_cvt_pk_bf16_f32 v58, v60, v61\n"
"v_permlane16_swap_b32 v8, v10\n"
"v_accvgpr_write_b32 a57, v9\n"
"v_mfma_f32_16x16x32_bf16 v[0:3], a[224:227], a[136:139], v[228:231]\n"
"s_waitcnt lgkmcnt(3)\n"
"v_cvt_pk_bf16_f32 v50, v52, v53\n"
"v_xor_b32 v245, s74, v245\n"
"v_mfma_f32_16x16x32_bf16 v[0:3], a[228:231], a[140:143], v[0:3]\n"
"ds_write2st64_b64 v100, v[72:73], v[74:75] offset0:64 offset1:65\n"
"v_cvt_pk_bf16_f32 v59, v62, v63\n"
"v_accvgpr_write_b32 a58, v10\n"
"v_accvgpr_write_b32 a66, v18\n"
"v_accvgpr_write_b32 a81, v33\n"
"v_mfma_f32_16x16x32_bf16 v[4:7], a[232:235], a[136:139], v[228:231]\n"
"s_waitcnt vmcnt(8) lgkmcnt(3)\n"
"v_permlane16_swap_b32 v57, v59\n"
"v_mfma_f32_16x16x32_bf16 v[4:7], a[236:239], a[140:143], v[4:7]\n"
"s_barrier\n"
"ds_write2st64_b64 v100, v[88:89], v[90:91] offset0:96 offset1:97\n"
"v_accvgpr_write_b32 a56, v8\n"
"v_accvgpr_write_b32 a59, v11\n"
"v_accvgpr_write_b32 a72, v24\n"
"v_mfma_f32_16x16x32_bf16 v[8:11], a[240:243], a[136:139], v[228:231]\n"
"s_mov_b32 m0, s51\n"
"ds_read_b64_tr_b16 a[202:203], v245 offset:512\n"
"v_accvgpr_write_b32 a73, v25\n"
"v_mfma_f32_16x16x32_bf16 v[8:11], a[244:247], a[140:143], v[8:11]\n"
"buffer_load_dwordx4 v248, s[24:27], s49 offen lds\n"
"v_mfma_f32_16x16x32_bf16 v[12:15], a[248:251], a[136:139], v[228:231]\n"
"v_accvgpr_write_b32 a65, v17\n"
"v_accvgpr_write_b32 a67, v19\n"
"v_mfma_f32_16x16x32_bf16 v[12:15], a[252:255], a[140:143], v[12:15]\n"
"v_permlane16_swap_b32 v56, v58\n"
"v_mfma_f32_16x16x32_bf16 v[28:31], a[248:251], a[144:147], v[228:231]\n"
"v_accvgpr_write_b32 a74, v26\n"
"v_accvgpr_write_b32 a83, v35\n"
"v_mfma_f32_16x16x32_bf16 v[28:31], a[252:255], a[148:151], v[28:31]\n"
"ds_write2st64_b64 v100, v[64:65], v[66:67] offset0:0 offset1:1\n"
"v_permlane16_swap_b32 v32, v34\n"
"v_accvgpr_write_b32 a88, v40\n"
"v_mfma_f32_16x16x32_bf16 v[24:27], a[240:243], a[144:147], v[228:231]\n"
"v_accvgpr_write_b32 a89, v41\n"
"v_accvgpr_write_b32 a90, v42\n"
"v_mfma_f32_16x16x32_bf16 v[24:27], a[244:247], a[148:151], v[24:27]\n"
"buffer_load_dwordx4 v249, s[24:27], s49 offen offset:1024 lds\n"
"v_mfma_f32_16x16x32_bf16 v[20:23], a[232:235], a[144:147], v[228:231]\n"
"v_accvgpr_write_b32 a82, v34\n"
"v_accvgpr_write_b32 a91, v43\n"
"v_mfma_f32_16x16x32_bf16 v[20:23], a[236:239], a[148:151], v[20:23]\n"
"v_cvt_pk_bf16_f32 v51, v54, v55\n"
"v_accvgpr_write_b32 a80, v32\n"
"v_mfma_f32_16x16x32_bf16 v[16:19], a[224:227], a[144:147], v[228:231]\n"
"ds_read_b64_tr_b16 a[216:217], v245 offset:1024\n"
"v_xor_b32 v247, s74, v247\n"
"v_mfma_f32_16x16x32_bf16 v[16:19], a[228:231], a[148:151], v[16:19]\n"
"ds_write_b128 v243, v[56:59] offset:6144\n"
"v_mfma_f32_16x16x32_bf16 v[32:35], a[224:227], a[152:155], v[228:231]\n"
"v_permlane16_swap_b32 v48, v50\n"
"v_mfma_f32_16x16x32_bf16 v[32:35], a[228:231], a[156:159], v[32:35]\n"
"buffer_load_dwordx4 v254, s[24:27], s49 offen offset:2048 lds\n"
"v_mfma_f32_16x16x32_bf16 v[36:39], a[232:235], a[152:155], v[228:231]\n"
"s_waitcnt vmcnt(63) lgkmcnt(2)\n"
"v_permlane16_swap_b32 v49, v51\n"
"v_mfma_f32_16x16x32_bf16 v[36:39], a[236:239], a[156:159], v[36:39]\n"
"s_barrier\n"
"v_mfma_f32_16x16x32_bf16 v[40:43], a[240:243], a[152:155], v[228:231]\n"
"ds_read_b64_tr_b16 a[204:205], v245 offset:8192\n"
"ds_read_b128 a[140:143], v247 offset:0\n"
"v_mfma_f32_16x16x32_bf16 v[40:43], a[244:247], a[156:159], v[40:43]\n"
"ds_write_b128 v243, v[48:51] offset:4096\n"
"v_mfma_f32_16x16x32_bf16 v[44:47], a[248:251], a[152:155], v[228:231]\n"
"v_bfe_u32 v102, v242, 8, 1\n"
"v_lshl_add_u32 v101, v102, 9, v101\n"
"v_mfma_f32_16x16x32_bf16 v[44:47], a[252:255], a[156:159], v[44:47]\n"
"buffer_load_dwordx4 v255, s[24:27], s49 offen offset:3072 lds\n"
"v_mfma_f32_16x16x32_bf16 v[60:63], a[248:251], a[160:163], v[228:231]\n"
"v_xor_b32 v244, s74, v244\n"
"v_xor_b32_e64 v102, s42, 0\n"
"v_mfma_f32_16x16x32_bf16 v[60:63], a[252:255], a[164:167], v[60:63]\n"
"v_lshl_add_u32 v101, v102, 10, v101\n"
"v_add_u32 v101, 94208, v101\n"
"v_mfma_f32_16x16x32_bf16 v[56:59], a[240:243], a[160:163], v[228:231]\n"
"ds_read_b64_tr_b16 a[198:199], v244 offset:8704\n"
"ds_read_b64_tr_b16 a[220:221], v245 offset:9216\n"
"v_mfma_f32_16x16x32_bf16 v[56:59], a[244:247], a[164:167], v[56:59]\n"
"s_add_u32 s40, s49, 40960\n"
"ds_read_b128 a[52:55], v101 offset:0\n"
"v_xor_b32 v253, s74, v253\n"
"v_mfma_f32_16x16x32_bf16 v[52:55], a[232:235], a[160:163], v[228:231]\n"
"s_add_u32 m0, s51, 4096\n"
"ds_read_b64_tr_b16 a[206:207], v245 offset:8704\n"
"ds_read_b128 a[76:79], v101 offset:49152\n"
"v_mfma_f32_16x16x32_bf16 v[52:55], a[236:239], a[164:167], v[52:55]\n"
"buffer_load_dwordx4 v248, s[24:27], s40 offen lds\n"
"v_mfma_f32_16x16x32_bf16 v[48:51], a[224:227], a[160:163], v[228:231]\n"
"v_xor_b32 v246, s74, v246\n"
"v_add_u32 v102, s75, v101\n"
"v_mfma_f32_16x16x32_bf16 v[48:51], a[228:231], a[164:167], v[48:51]\n"
"ds_read_b64_tr_b16 a[192:193], v244 offset:0\n"
"ds_read_b128 a[68:71], v101 offset:45056\n"
"v_mfma_f32_16x16x32_bf16 v[64:67], a[224:227], a[168:171], v[228:231]\n"
"ds_read_b64_tr_b16 a[212:213], v244 offset:9216\n"
"ds_read_b128 a[164:167], v253 offset:0\n"
"v_mfma_f32_16x16x32_bf16 v[64:67], a[228:231], a[172:175], v[64:67]\n"
"ds_read_b64_tr_b16 a[208:209], v244 offset:1024\n"
"ds_read_b64_tr_b16 a[218:219], v245 offset:1536\n"
"v_mfma_f32_16x16x32_bf16 v[68:71], a[232:235], a[168:171], v[228:231]\n"
"ds_read_b64_tr_b16 a[200:201], v245 offset:0\n"
"ds_read_b128 a[84:87], v101 offset:61440\n"
"v_mfma_f32_16x16x32_bf16 v[68:71], a[236:239], a[172:175], v[68:71]\n"
"buffer_load_dwordx4 v249, s[24:27], s40 offen offset:1024 lds\n"
"v_mfma_f32_16x16x32_bf16 v[72:75], a[240:243], a[168:171], v[228:231]\n"
"ds_read_b64_tr_b16 a[214:215], v244 offset:9728\n"
"ds_read_b128 a[60:63], v101 offset:32768\n"
"v_mfma_f32_16x16x32_bf16 v[72:75], a[244:247], a[172:175], v[72:75]\n"
"v_xor_b32 v250, s74, v250\n"
"v_xor_b32 v251, s74, v251\n"
"v_mfma_f32_16x16x32_bf16 v[76:79], a[248:251], a[168:171], v[228:231]\n"
"ds_read_b128 a[92:95], v102 offset:0\n"
"ds_read_b128 a[168:171], v250 offset:0\n"
"v_mfma_f32_16x16x32_bf16 v[76:79], a[252:255], a[172:175], v[76:79]\n"
"ds_read_b128 a[152:155], v246 offset:4096\n"
"v_xor_b32 v252, s74, v252\n"
"v_mfma_f32_16x16x32_bf16 v[92:95], a[248:251], a[176:179], v[228:231]\n"
"ds_read_b64_tr_b16 a[222:223], v245 offset:9728\n"
"ds_read_b128 a[160:163], v252 offset:0\n"
"v_mfma_f32_16x16x32_bf16 v[92:95], a[252:255], a[180:183], v[92:95]\n"
"buffer_load_dwordx4 v254, s[24:27], s40 offen offset:2048 lds\n"
"v_mfma_f32_16x16x32_bf16 v[88:91], a[240:243], a[176:179], v[228:231]\n"
"s_add_u32 s49, s49, 128\n"
"ds_read_b128 a[144:147], v246 offset:2048\n"
"ds_read_b128 a[148:151], v247 offset:2048\n"
"v_mfma_f32_16x16x32_bf16 v[88:91], a[244:247], a[180:183], v[88:91]\n"
"s_sub_u32 s41, s49, 1280\n"
"ds_read_b128 a[136:139], v246 offset:0\n"
"v_mfma_f32_16x16x32_bf16 v[84:87], a[232:235], a[176:179], v[228:231]\n"
"s_min_u32 s49, s49, s41\n"
"ds_read_b64_tr_b16 a[196:197], v244 offset:8192\n"
"ds_read_b64_tr_b16 a[210:211], v244 offset:1536\n"
"v_mfma_f32_16x16x32_bf16 v[84:87], a[236:239], a[180:183], v[84:87]\n"
"s_xor_b32 s51, s51, s74\n"
"ds_read_b64_tr_b16 a[194:195], v244 offset:512\n"
"ds_read_b128 a[156:159], v247 offset:4096\n"
"v_mfma_f32_16x16x32_bf16 v[80:83], a[224:227], a[176:179], v[228:231]\n"
"ds_read_b128 a[176:179], v250 offset:2048\n"
"v_mfma_f32_16x16x32_bf16 v[80:83], a[228:231], a[180:183], v[80:83]\n"
"ds_read_b128 a[172:175], v251 offset:0\n"
"ds_read_b128 a[180:183], v251 offset:2048\n"
"v_mfma_f32_16x16x32_bf16 v[96:99], a[224:227], a[184:187], v[228:231]\n"
"v_mfma_f32_16x16x32_bf16 v[96:99], a[228:231], a[188:191], v[96:99]\n"
"v_mfma_f32_16x16x32_bf16 v[100:103], a[232:235], a[184:187], v[228:231]\n"
"v_mfma_f32_16x16x32_bf16 v[100:103], a[236:239], a[188:191], v[100:103]\n"
"v_mfma_f32_16x16x32_bf16 v[104:107], a[240:243], a[184:187], v[228:231]\n"
"v_mfma_f32_16x16x32_bf16 v[104:107], a[244:247], a[188:191], v[104:107]\n"
"v_mfma_f32_16x16x32_bf16 v[108:111], a[248:251], a[184:187], v[228:231]\n"
"s_add_u32 s47, s47, 1\n"
"ds_read_b128 a[184:187], v250 offset:4096\n"
"v_mfma_f32_16x16x32_bf16 v[108:111], a[252:255], a[188:191], v[108:111]\n"
"s_waitcnt lgkmcnt(1)\n"
"ds_read_b128 a[188:191], v251 offset:4096\n"
"s_mov_b32 s62, 18\n"
"s_branch LPE_%=\n"
"LPT2_%=:\n"
"v_mfma_f32_16x16x32_bf16 v[112:115], a[192:195], a[136:139], v[112:115]\n"
"s_add_u32 s78, s32, s96\ns_addc_u32 s79, s33, 0\n"
"s_mov_b64 s[80:81], 0\n"
"s_mov_b32 s82, 0\n"
"v_mfma_f32_16x16x32_bf16 v[112:115], a[196:199], a[140:143], v[112:115]\n"
"v_mfma_f32_16x16x32_bf16 v[116:119], a[200:203], a[136:139], v[116:119]\n"
"s_waitcnt vmcnt(11)\n"
"s_load_dword s80, s[78:79], 0\n"
"v_mfma_f32_16x16x32_bf16 v[116:119], a[204:207], a[140:143], v[116:119]\n"
"s_barrier\n"
"v_mfma_f32_16x16x32_bf16 v[120:123], a[208:211], a[136:139], v[120:123]\n"
"s_add_u32 s40, s48, 256\n"
"s_xor_b32 m0, s50, 0xc000\n"
"ds_read_b64_tr_b16 a[254:255], v245 offset:26112\n"
"v_mfma_f32_16x16x32_bf16 v[120:123], a[212:215], a[140:143], v[120:123]\n"
"buffer_load_dwordx4 v224, s[28:31], s40 offen lds\n"
"v_mfma_f32_16x16x32_bf16 v[124:127], a[216:219], a[136:139], v[124:127]\n"
"v_mfma_f32_16x16x32_bf16 v[124:127], a[220:223], a[140:143], v[124:127]\n"
"v_mfma_f32_16x16x32_bf16 v[140:143], a[216:219], a[144:147], v[140:143]\n"
"ds_read_b64_tr_b16 a[246:247], v244 offset:26112\n"
"v_mfma_f32_16x16x32_bf16 v[140:143], a[220:223], a[148:151], v[140:143]\n"
"v_mfma_f32_16x16x32_bf16 v[136:139], a[208:211], a[144:147], v[136:139]\n"
"ds_read_b64_tr_b16 a[226:227], v244 offset:16896\n"
"v_mfma_f32_16x16x32_bf16 v[136:139], a[212:215], a[148:151], v[136:139]\n"
"buffer_load_dwordx4 v225, s[28:31], s40 offen offset:1024 lds\n"
"v_mfma_f32_16x16x32_bf16 v[132:135], a[200:203], a[144:147], v[132:135]\n"
"s_add_u32 s82, s82, s80\n"
"v_mfma_f32_16x16x32_bf16 v[132:135], a[204:207], a[148:151], v[132:135]\n"
"s_add_u32 s82, s82, s80\n"
"v_mfma_f32_16x16x32_bf16 v[128:131], a[192:195], a[144:147], v[128:131]\n"
"s_add_u32 s82, s82, s80\n"
"ds_read_b64_tr_b16 a[244:245], v244 offset:25600\n"
"v_mfma_f32_16x16x32_bf16 v[128:131], a[196:199], a[148:151], v[128:131]\n"
"s_add_u32 s82, s82, s80\n"
"v_mfma_f32_16x16x32_bf16 v[144:147], a[192:195], a[152:155], v[144:147]\n"
"s_add_u32 s82, s82, s80\n"
"ds_read_b64_tr_b16 a[236:237], v245 offset:24576\n"
"v_mfma_f32_16x16x32_bf16 v[144:147], a[196:199], a[156:159], v[144:147]\n"
"s_add_u32 s82, s82, s80\n"
"v_mfma_f32_16x16x32_bf16 v[148:151], a[200:203], a[152:155], v[148:151]\n"
"s_add_u32 s82, s82, s80\n"
"ds_read_b64_tr_b16 a[224:225], v244 offset:16384\n"
"v_mfma_f32_16x16x32_bf16 v[148:151], a[204:207], a[156:159], v[148:151]\n"
"s_add_u32 s82, s82, s80\n"
"buffer_load_dwordx4 v226, s[28:31], s40 offen offset:2048 lds\n"
"v_mfma_f32_16x16x32_bf16 v[152:155], a[208:211], a[152:155], v[152:155]\n"
"s_add_u32 s82, s82, s80\n"
"v_mfma_f32_16x16x32_bf16 v[152:155], a[212:215], a[156:159], v[152:155]\n"
"s_add_u32 s82, s82, s80\n"
"v_mfma_f32_16x16x32_bf16 v[156:159], a[216:219], a[152:155], v[156:159]\n"
"s_add_u32 s82, s82, s80\n"
"ds_read_b64_tr_b16 a[234:235], v245 offset:16896\n"
"v_mfma_f32_16x16x32_bf16 v[156:159], a[220:223], a[156:159], v[156:159]\n"
"s_add_u32 s82, s82, s80\n"
"v_mfma_f32_16x16x32_bf16 v[172:175], a[216:219], a[160:163], v[172:175]\n"
"s_add_u32 s82, s82, s80\n"
"ds_read_b64_tr_b16 a[250:251], v245 offset:17920\n"
"v_mfma_f32_16x16x32_bf16 v[172:175], a[220:223], a[164:167], v[172:175]\n"
"s_add_u32 s82, s82, s80\n"
"buffer_load_dwordx4 v227, s[28:31], s40 offen offset:3072 lds\n"
"v_mfma_f32_16x16x32_bf16 v[168:171], a[208:211], a[160:163], v[168:171]\n"
"s_add_u32 s82, s82, s80\n"
"s_mov_b32 s58, 245760\n"
"v_mfma_f32_16x16x32_bf16 v[168:171], a[212:215], a[164:167], v[168:171]\n"
"s_add_u32 s82, s82, s80\n"
"s_mov_b32 s47, 0\n"
"v_mfma_f32_16x16x32_bf16 v[164:167], a[200:203], a[160:163], v[164:167]\n"
"s_add_u32 s82, s82, s80\n"
"s_mov_b32 s52, -896\n"
"ds_read_b64_tr_b16 a[252:253], v245 offset:25600\n"
"v_mfma_f32_16x16x32_bf16 v[164:167], a[204:207], a[164:167], v[164:167]\n"
"s_load_dword s81, s[78:79], 0x2000\n"
"s_mov_b32 s53, 0\n"
"v_mfma_f32_16x16x32_bf16 v[160:163], a[192:195], a[160:163], v[160:163]\n"
"s_mov_b32 m0, s44\n"
"ds_read_b64_tr_b16 a[248:249], v245 offset:17408\n"
"v_mfma_f32_16x16x32_bf16 v[160:163], a[196:199], a[164:167], v[160:163]\n"
"buffer_load_dwordx4 v240, s[16:19], s56 offen nt lds\n"
"v_mfma_f32_16x16x32_bf16 v[176:179], a[192:195], a[168:171], v[176:179]\n"
"v_mfma_f32_16x16x32_bf16 v[176:179], a[196:199], a[172:175], v[176:179]\n"
"v_mfma_f32_16x16x32_bf16 v[180:183], a[200:203], a[168:171], v[180:183]\n"
"ds_read_b64_tr_b16 a[230:231], v244 offset:25088\n"
"v_mfma_f32_16x16x32_bf16 v[180:183], a[204:207], a[172:175], v[180:183]\n"
"v_mfma_f32_16x16x32_bf16 v[184:187], a[208:211], a[168:171], v[184:187]\n"
"ds_read_b64_tr_b16 a[238:239], v245 offset:25088\n"
"v_mfma_f32_16x16x32_bf16 v[184:187], a[212:215], a[172:175], v[184:187]\n"
"v_mfma_f32_16x16x32_bf16 v[188:191], a[216:219], a[168:171], v[188:191]\n"
"s_add_u32 s40, s56, s52\n"
"ds_read_b64_tr_b16 a[228:229], v244 offset:24576\n"
"v_mfma_f32_16x16x32_bf16 v[188:191], a[220:223], a[172:175], v[188:191]\n"
"s_sub_u32 s82, s82, s81\n"
"buffer_load_dwordx4 v240, s[16:19], s40 offen offset:1024 nt lds\n"
"v_mfma_f32_16x16x32_bf16 v[204:207], a[216:219], a[176:179], v[204:207]\n"
"s_sub_u32 s82, s82, s81\n"
"v_xor_b32 v250, s74, v250\n"
"v_mfma_f32_16x16x32_bf16 v[204:207], a[220:223], a[180:183], v[204:207]\n"
"s_sub_u32 s82, s82, s81\n"
"v_mfma_f32_16x16x32_bf16 v[200:203], a[208:211], a[176:179], v[200:203]\n"
"s_sub_u32 s82, s82, s81\n"
"ds_read_b64_tr_b16 a[232:233], v245 offset:16384\n"
"v_mfma_f32_16x16x32_bf16 v[200:203], a[212:215], a[180:183], v[200:203]\n"
"s_sub_u32 s82, s82, s81\n"
"v_mfma_f32_16x16x32_bf16 v[196:199], a[200:203], a[176:179], v[196:199]\n"
"s_sub_u32 s82, s82, s81\n"
"s_add_u32 s40, s56, s53\n"
"ds_read_b64_tr_b16 a[240:241], v244 offset:17408\n"
"v_mfma_f32_16x16x32_bf16 v[196:199], a[204:207], a[180:183], v[196:199]\n"
"s_sub_u32 s82, s82, s81\n"
"buffer_load_dwordx4 v240, s[16:19], s40 offen offset:2048 nt lds\n"
"v_mfma_f32_16x16x32_bf16 v[192:195], a[192:195], a[176:179], v[192:195]\n"
"s_sub_u32 s82, s82, s81\n"
"v_and_b32 v229, 0x60, v242\n"
"v_mfma_f32_16x16x32_bf16 v[192:195], a[196:199], a[180:183], v[192:195]\n"
"s_waitcnt lgkmcnt(2)\n"
"s_sub_u32 s82, s82, s81\n"
"v_mfma_f32_16x16x32_bf16 v[208:211], a[192:195], a[184:187], v[208:211]\n"
"s_sub_u32 s82, s82, s81\n"
"ds_read_b64_tr_b16 a[242:243], v244 offset:17920\n"
"v_mfma_f32_16x16x32_bf16 v[208:211], a[196:199], a[188:191], v[208:211]\n"
"s_sub_u32 s82, s82, s81\n"
"v_xor_b32 v246, s74, v246\n"
"v_mfma_f32_16x16x32_bf16 v[212:215], a[200:203], a[184:187], v[212:215]\n"
"s_sub_u32 s82, s82, s81\n"
"s_add_u32 s40, s56, s54\n"
"v_mfma_f32_16x16x32_bf16 v[212:215], a[204:207], a[188:191], v[212:215]\n"
"s_sub_u32 s82, s82, s81\n"
"buffer_load_dwordx4 v240, s[16:19], s40 offen offset:3072 nt lds\n"
"v_mfma_f32_16x16x32_bf16 v[216:219], a[208:211], a[184:187], v[216:219]\n"
"s_sub_u32 s82, s82, s81\n"
"v_bfe_u32 v228, v242, 8, 2\n"
"v_mfma_f32_16x16x32_bf16 v[216:219], a[212:215], a[188:191], v[216:219]\n"
"s_sub_u32 s82, s82, s81\n"
"v_mfma_f32_16x16x32_bf16 v[220:223], a[216:219], a[184:187], v[220:223]\n"
"s_sub_u32 s82, s82, s81\n"
"v_mfma_f32_16x16x32_bf16 v[220:223], a[220:223], a[188:191], v[220:223]\n"
"s_sub_u32 s82, s82, s81\n"
"v_mfma_f32_16x16x32_bf16 v[0:3], a[224:227], a[136:139], v[0:3]\n"
"v_mfma_f32_16x16x32_bf16 v[0:3], a[228:231], a[140:143], v[0:3]\n"
"s_waitcnt lgkmcnt(0)\n"
"v_mfma_f32_16x16x32_bf16 v[4:7], a[232:235], a[136:139], v[4:7]\n"
"s_waitcnt vmcnt(8)\n"
"v_xor_b32 v245, s74, v245\n"
"v_mfma_f32_16x16x32_bf16 v[4:7], a[236:239], a[140:143], v[4:7]\n"
"s_barrier\n"
"v_mfma_f32_16x16x32_bf16 v[8:11], a[240:243], a[136:139], v[8:11]\n"
"s_add_u32 m0, s44, 65536\n"
"ds_read_b64_tr_b16 a[204:205], v245 offset:8192\n"
"v_mfma_f32_16x16x32_bf16 v[8:11], a[244:247], a[140:143], v[8:11]\n"
"buffer_load_dwordx4 v240, s[20:23], s56 offen nt lds\n"
"v_mfma_f32_16x16x32_bf16 v[12:15], a[248:251], a[136:139], v[12:15]\n"
"s_lshr_b32 s82, s82, 31\n"
"v_xor_b32 v244, s74, v244\n"
"v_mfma_f32_16x16x32_bf16 v[12:15], a[252:255], a[140:143], v[12:15]\n"
"s_add_u32 s82, s82, s60\n"
"v_xor_b32 v247, s74, v247\n"
"v_mfma_f32_16x16x32_bf16 v[28:31], a[248:251], a[144:147], v[28:31]\n"
"s_add_u32 s82, s82, 32\n"
"ds_read_b64_tr_b16 a[208:209], v244 offset:1024\n"
"v_mfma_f32_16x16x32_bf16 v[28:31], a[252:255], a[148:151], v[28:31]\n"
"v_mov_b32 v254, s82\n"
"ds_read_b64_tr_b16 a[196:197], v244 offset:8192\n"
"v_mfma_f32_16x16x32_bf16 v[24:27], a[240:243], a[144:147], v[24:27]\n"
"v_lshlrev_b32_e64 v255, 2, s93\n"
"ds_read_b64_tr_b16 a[210:211], v244 offset:1536\n"
"v_mfma_f32_16x16x32_bf16 v[24:27], a[244:247], a[148:151], v[24:27]\n"
"ds_read_b64_tr_b16 a[222:223], v245 offset:9728\n"
"v_xor_b32 v251, s74, v251\n"
"v_mfma_f32_16x16x32_bf16 v[20:23], a[232:235], a[144:147], v[20:23]\n"
"s_add_u32 s40, s56, s52\n"
"ds_read_b64_tr_b16 a[202:203], v245 offset:512\n"
"v_mfma_f32_16x16x32_bf16 v[20:23], a[236:239], a[148:151], v[20:23]\n"
"buffer_load_dwordx4 v240, s[20:23], s40 offen offset:1024 nt lds\n"
"v_mfma_f32_16x16x32_bf16 v[16:19], a[224:227], a[144:147], v[16:19]\n"
"v_mfma_f32_16x16x32_bf16 v[16:19], a[228:231], a[148:151], v[16:19]\n"
"s_mov_b64 exec, 1\n"
"global_store_dword v255, v254, s[6:7] offset:256 sc1\n"
"s_mov_b64 exec, -1\n"
"v_mfma_f32_16x16x32_bf16 v[32:35], a[224:227], a[152:155], v[32:35]\n"
"ds_read_b64_tr_b16 a[192:193], v244 offset:0\n"
"v_bfe_i32 v241, v242, 4, 1\n"
"v_mfma_f32_16x16x32_bf16 v[32:35], a[228:231], a[156:159], v[32:35]\n"
"v_mfma_f32_16x16x32_bf16 v[36:39], a[232:235], a[152:155], v[36:39]\n"
"s_add_u32 s40, s56, s53\n"
"ds_read_b128 a[136:139], v246 offset:0\n"
"v_mfma_f32_16x16x32_bf16 v[36:39], a[236:239], a[156:159], v[36:39]\n"
"buffer_load_dwordx4 v240, s[20:23], s40 offen offset:2048 nt lds\n"
"v_mfma_f32_16x16x32_bf16 v[40:43], a[240:243], a[152:155], v[40:43]\n"
"v_mfma_f32_16x16x32_bf16 v[40:43], a[244:247], a[156:159], v[40:43]\n"
"v_mfma_f32_16x16x32_bf16 v[44:47], a[248:251], a[152:155], v[44:47]\n"
"ds_read_b128 a[140:143], v247 offset:0\n"
"v_mfma_f32_16x16x32_bf16 v[44:47], a[252:255], a[156:159], v[44:47]\n"
"v_mfma_f32_16x16x32_bf16 v[60:63], a[248:251], a[160:163], v[60:63]\n"
"s_add_u32 s40, s56, s54\n"
"ds_read_b64_tr_b16 a[194:195], v244 offset:512\n"
"v_mfma_f32_16x16x32_bf16 v[60:63], a[252:255], a[164:167], v[60:63]\n"
"buffer_load_dwordx4 v240, s[20:23], s40 offen offset:3072 nt lds\n"
"v_mfma_f32_16x16x32_bf16 v[56:59], a[240:243], a[160:163], v[56:59]\n"
"s_add_u32 s55, s55, s59\n"
"v_mfma_f32_16x16x32_bf16 v[56:59], a[244:247], a[164:167], v[56:59]\n"
"s_add_u32 s41, s55, s57\n"
"v_mfma_f32_16x16x32_bf16 v[52:55], a[232:235], a[160:163], v[52:55]\n"
"s_and_b32 s41, s41, s58\n"
"ds_read_b64_tr_b16 a[218:219], v245 offset:1536\n"
"v_mfma_f32_16x16x32_bf16 v[52:55], a[236:239], a[164:167], v[52:55]\n"
"s_andn2_b32 s56, s55, s58\n"
"ds_read_b64_tr_b16 a[200:201], v245 offset:0\n"
"v_lshlrev_b32 v228, 4, v228\n"
"v_mfma_f32_16x16x32_bf16 v[48:51], a[224:227], a[160:163], v[48:51]\n"
"s_or_b32 s56, s56, s41\n"
"ds_read_b64_tr_b16 a[212:213], v244 offset:9216\n"
"v_mfma_f32_16x16x32_bf16 v[48:51], a[228:231], a[164:167], v[48:51]\n"
"ds_read_b64_tr_b16 a[198:199], v244 offset:8704\n"
"v_mfma_f32_16x16x32_bf16 v[64:67], a[224:227], a[168:171], v[64:67]\n"
"s_add_u32 m0, s44, 16384\n"
"ds_read_b64_tr_b16 a[214:215], v244 offset:9728\n"
"v_mfma_f32_16x16x32_bf16 v[64:67], a[228:231], a[172:175], v[64:67]\n"
"buffer_load_dwordx4 v240, s[16:19], s56 offen nt lds\n"
"v_mfma_f32_16x16x32_bf16 v[68:71], a[232:235], a[168:171], v[68:71]\n"
"v_mfma_f32_16x16x32_bf16 v[68:71], a[236:239], a[172:175], v[68:71]\n"
"v_mfma_f32_16x16x32_bf16 v[72:75], a[240:243], a[168:171], v[72:75]\n"
"ds_read_b64_tr_b16 a[216:217], v245 offset:1024\n"
"v_and_b32 v254, 0x20, v242\n"
"v_mfma_f32_16x16x32_bf16 v[72:75], a[244:247], a[172:175], v[72:75]\n"
"ds_read_b64_tr_b16 a[220:221], v245 offset:9216\n"
"v_mfma_f32_16x16x32_bf16 v[76:79], a[248:251], a[168:171], v[76:79]\n"
"s_add_u32 s40, s56, s52\n"
"ds_read_b64_tr_b16 a[206:207], v245 offset:8704\n"
"v_mfma_f32_16x16x32_bf16 v[76:79], a[252:255], a[172:175], v[76:79]\n"
"buffer_load_dwordx4 v240, s[16:19], s40 offen offset:1024 nt lds\n"
"v_mfma_f32_16x16x32_bf16 v[92:95], a[248:251], a[176:179], v[92:95]\n"
"v_xor_b32 v228, v228, v229\n"
"v_mfma_f32_16x16x32_bf16 v[92:95], a[252:255], a[180:183], v[92:95]\n"
"v_and_b32 v229, 0x70, v242\n"
"v_mfma_f32_16x16x32_bf16 v[88:91], a[240:243], a[176:179], v[88:91]\n"
"ds_read_b128 a[172:175], v251 offset:0\n"
"v_mfma_f32_16x16x32_bf16 v[88:91], a[244:247], a[180:183], v[88:91]\n"
"v_lshl_add_u32 v228, v229, 3, v228\n"
"v_mfma_f32_16x16x32_bf16 v[84:87], a[232:235], a[176:179], v[84:87]\n"
"s_add_u32 s40, s56, s53\n"
"ds_read_b128 a[168:171], v250 offset:0\n"
"v_add_u32 v254, -1, v254\n"
"v_mfma_f32_16x16x32_bf16 v[84:87], a[236:239], a[180:183], v[84:87]\n"
"buffer_load_dwordx4 v240, s[16:19], s40 offen offset:2048 nt lds\n"
"v_mfma_f32_16x16x32_bf16 v[80:83], a[224:227], a[176:179], v[80:83]\n"
"v_mfma_f32_16x16x32_bf16 v[80:83], a[228:231], a[180:183], v[80:83]\n"
"v_mfma_f32_16x16x32_bf16 v[96:99], a[224:227], a[184:187], v[96:99]\n"
"ds_read_b128 a[152:155], v246 offset:4096\n"
"v_mfma_f32_16x16x32_bf16 v[96:99], a[228:231], a[188:191], v[96:99]\n"
"v_mfma_f32_16x16x32_bf16 v[100:103], a[232:235], a[184:187], v[100:103]\n"
"s_add_u32 s40, s56, s54\n"
"ds_read_b128 a[144:147], v246 offset:2048\n"
"v_and_b32 v241, 0xaaaaaaaa, v241\n"
"v_mfma_f32_16x16x32_bf16 v[100:103], a[236:239], a[188:191], v[100:103]\n"
"buffer_load_dwordx4 v240, s[16:19], s40 offen offset:3072 nt lds\n"
"v_mfma_f32_16x16x32_bf16 v[104:107], a[240:243], a[184:187], v[104:107]\n"
"v_mfma_f32_16x16x32_bf16 v[104:107], a[244:247], a[188:191], v[104:107]\n"
"v_mfma_f32_16x16x32_bf16 v[108:111], a[248:251], a[184:187], v[108:111]\n"
"ds_read_b128 a[180:183], v251 offset:2048\n"
"v_mfma_f32_16x16x32_bf16 v[108:111], a[252:255], a[188:191], v[108:111]\n"
"s_waitcnt lgkmcnt(3)\n"
"v_and_b32 v229, 0x80, v242\n"
"v_mfma_f32_16x16x32_bf16 v[112:115], a[192:195], a[136:139], v[112:115]\n"
"ds_read_b128 a[176:179], v250 offset:2048\n"
"v_and_b32 v249, 0xf0, v242\n"
"v_mfma_f32_16x16x32_bf16 v[112:115], a[196:199], a[140:143], v[112:115]\n"
"v_xor_b32 v253, s74, v253\n"
"v_bfe_u32 v255, v242, 9, 1\n"
"v_mfma_f32_16x16x32_bf16 v[116:119], a[200:203], a[136:139], v[116:119]\n"
"s_waitcnt vmcnt(13)\n"
"ds_read_b128 a[184:187], v250 offset:4096\n"
"v_lshl_add_u32 v255, v255, 8, v249\n"
"v_mfma_f32_16x16x32_bf16 v[116:119], a[204:207], a[140:143], v[116:119]\n"
"s_barrier\n"
"v_mfma_f32_16x16x32_bf16 v[120:123], a[208:211], a[136:139], v[120:123]\n"
"s_add_u32 m0, s44, 81920\n"
"ds_read_b64_tr_b16 a[238:239], v245 offset:25088\n"
"v_mfma_f32_16x16x32_bf16 v[120:123], a[212:215], a[140:143], v[120:123]\n"
"buffer_load_dwordx4 v240, s[20:23], s56 offen nt lds\n"
"v_mfma_f32_16x16x32_bf16 v[124:127], a[216:219], a[136:139], v[124:127]\n"
"v_ashrrev_i32 v254, 31, v254\n"
"v_bfe_u32 v248, v242, 9, 1\n"
"v_mfma_f32_16x16x32_bf16 v[124:127], a[220:223], a[140:143], v[124:127]\n"
"v_lshl_add_u32 v228, v229, 4, v228\n"
"v_bfe_u32 v249, v242, 8, 1\n"
"v_mfma_f32_16x16x32_bf16 v[188:191], a[216:219], a[168:171], v[188:191]\n"
"ds_read_b64_tr_b16 a[248:249], v245 offset:17408\n"
"v_or_b32 v241, 0x44444444, v241\n"
"v_mfma_f32_16x16x32_bf16 v[188:191], a[220:223], a[172:175], v[188:191]\n"
"ds_read_b64_tr_b16 a[242:243], v244 offset:17920\n"
"v_mfma_f32_16x16x32_bf16 v[184:187], a[208:211], a[168:171], v[184:187]\n"
"v_lshl_add_u32 v255, v249, 3, v255\n"
"v_xor_b32_e64 v249, s42, 1\n"
"v_mfma_f32_16x16x32_bf16 v[184:187], a[212:215], a[172:175], v[184:187]\n"
"ds_read_b128 a[188:191], v251 offset:4096\n"
"v_mov_b32 v229, s43\n"
"v_mfma_f32_16x16x32_bf16 v[180:183], a[200:203], a[168:171], v[180:183]\n"
"v_mfma_f32_16x16x32_bf16 v[180:183], a[204:207], a[172:175], v[180:183]\n"
"ds_read_b128 a[148:151], v247 offset:2048\n"
"v_mfma_f32_16x16x32_bf16 v[176:179], a[192:195], a[168:171], v[176:179]\n"
"v_lshl_add_u32 v255, v249, 12, v255\n"
"v_mfma_f32_16x16x32_bf16 v[176:179], a[196:199], a[172:175], v[176:179]\n"
"s_waitcnt lgkmcnt(4)\n"
"ds_read_b64_tr_b16 a[224:225], v244 offset:16384\n"
"v_xor_b32 v252, s74, v252\n"
"v_mfma_f32_16x16x32_bf16 v[192:195], a[192:195], a[176:179], v[192:195]\n"
"ds_read_b64_tr_b16 a[250:251], v245 offset:17920\n"
"v_mul_u32_u24 v229, 0x81, v229\n"
"v_mfma_f32_16x16x32_bf16 v[192:195], a[196:199], a[180:183], v[192:195]\n"
"ds_read_b64_tr_b16 a[236:237], v245 offset:24576\n"
"v_mfma_f32_16x16x32_bf16 v[196:199], a[200:203], a[176:179], v[196:199]\n"
"s_add_u32 s40, s56, s52\n"
"ds_read_b64_tr_b16 a[226:227], v244 offset:16896\n"
"v_mfma_f32_16x16x32_bf16 v[196:199], a[204:207], a[180:183], v[196:199]\n"
"buffer_load_dwordx4 v240, s[20:23], s40 offen offset:1024 nt lds\n"
"v_mfma_f32_16x16x32_bf16 v[200:203], a[208:211], a[176:179], v[200:203]\n"
"v_and_b32 v249, 0xf0, v242\n"
"v_mfma_f32_16x16x32_bf16 v[200:203], a[212:215], a[180:183], v[200:203]\n"
"v_mfma_f32_16x16x32_bf16 v[204:207], a[216:219], a[176:179], v[204:207]\n"
"ds_read_b128 a[164:167], v253 offset:0\n"
"v_mfma_f32_16x16x32_bf16 v[204:207], a[220:223], a[180:183], v[204:207]\n"
"v_mfma_f32_16x16x32_bf16 v[220:223], a[216:219], a[184:187], v[220:223]\n"
"s_waitcnt lgkmcnt(3)\n"
"ds_read_b64_tr_b16 a[234:235], v245 offset:16896\n"
"v_lshl_add_u32 v248, v248, 8, v249\n"
"v_mfma_f32_16x16x32_bf16 v[220:223], a[220:223], a[188:191], v[220:223]\n"
"v_mfma_f32_16x16x32_bf16 v[216:219], a[208:211], a[184:187], v[216:219]\n"
"ds_read_b128 a[160:163], v252 offset:0\n"
"v_xor_b32 v228, v228, v229\n"
"v_mfma_f32_16x16x32_bf16 v[216:219], a[212:215], a[188:191], v[216:219]\n"
"v_pk_fma_f32 v[176:177], v[176:177], s[70:71], s[70:71] op_sel:[0,0,1] op_sel_hi:[1,0,1]\nv_pk_fma_f32 v[178:179], v[178:179], s[70:71], s[70:71] op_sel:[0,0,1] op_sel_hi:[1,0,1]\nv_pk_fma_f32 v[180:181], v[180:181], s[70:71], s[70:71] op_sel:[0,0,1] op_sel_hi:[1,0,1]\nv_pk_fma_f32 v[182:183], v[182:183], s[70:71], s[70:71] op_sel:[0,0,1] op_sel_hi:[1,0,1]\nv_pk_fma_f32 v[184:185], v[184:185], s[70:71], s[70:71] op_sel:[0,0,1] op_sel_hi:[1,0,1]\nv_pk_fma_f32 v[186:187], v[186:187], s[70:71], s[70:71] op_sel:[0,0,1] op_sel_hi:[1,0,1]\nv_pk_fma_f32 v[188:189], v[188:189], s[70:71], s[70:71] op_sel:[0,0,1] op_sel_hi:[1,0,1]\nv_pk_fma_f32 v[190:191], v[190:191], s[70:71], s[70:71] op_sel:[0,0,1] op_sel_hi:[1,0,1]\nv_pk_fma_f32 v[192:193], v[192:193], s[70:71], s[70:71] op_sel:[0,0,1] op_sel_hi:[1,0,1]\nv_pk_fma_f32 v[194:195], v[194:195], s[70:71], s[70:71] op_sel:[0,0,1] op_sel_hi:[1,0,1]\nv_pk_fma_f32 v[196:197], v[196:197], s[70:71], s[70:71] op_sel:[0,0,1] op_sel_hi:[1,0,1]\nv_pk_fma_f32 v[198:199], v[198:199], s[70:71], s[70:71] op_sel:[0,0,1] op_sel_hi:[1,0,1]\nv_pk_fma_f32 v[200:201], v[200:201], s[70:71], s[70:71] op_sel:[0,0,1] op_sel_hi:[1,0,1]\nv_pk_fma_f32 v[202:203], v[202:203], s[70:71], s[70:71] op_sel:[0,0,1] op_sel_hi:[1,0,1]\nv_pk_fma_f32 v[204:205], v[204:205], s[70:71], s[70:71] op_sel:[0,0,1] op_sel_hi:[1,0,1]\nv_pk_fma_f32 v[206:207], v[206:207], s[70:71], s[70:71] op_sel:[0,0,1] op_sel_hi:[1,0,1]\n"
"v_mfma_f32_16x16x32_bf16 v[212:215], a[200:203], a[184:187], v[212:215]\n"
"ds_read_b64_tr_b16 a[252:253], v245 offset:25600\n"
"v_cvt_pk_bf16_f32 v176, v176, v177\n"
"v_mfma_f32_16x16x32_bf16 v[212:215], a[204:207], a[188:191], v[212:215]\n"
"v_cvt_pk_bf16_f32 v177, v178, v179\n"
"v_cvt_pk_bf16_f32 v192, v192, v193\n"
"v_mfma_f32_16x16x32_bf16 v[208:211], a[192:195], a[184:187], v[208:211]\n"
"s_add_u32 s40, s56, s53\n"
"ds_read_b128 a[156:159], v247 offset:4096\n"
"v_cvt_pk_bf16_f32 v184, v184, v185\n"
"v_mfma_f32_16x16x32_bf16 v[208:211], a[196:199], a[188:191], v[208:211]\n"
"s_waitcnt lgkmcnt(2)\n"
"buffer_load_dwordx4 v240, s[20:23], s40 offen offset:2048 nt lds\n"
"v_mfma_f32_16x16x32_bf16 v[160:163], a[192:195], a[160:163], v[160:163]\n"
"v_mfma_f32_16x16x32_bf16 v[160:163], a[196:199], a[164:167], v[160:163]\n"
"v_xor_b32 v230, 0x2000, v228\n"
"v_mfma_f32_16x16x32_bf16 v[164:167], a[200:203], a[160:163], v[164:167]\n"
"ds_read_b64_tr_b16 a[244:245], v244 offset:25600\n"
"v_cvt_pk_bf16_f32 v185, v186, v187\n"
"v_mfma_f32_16x16x32_bf16 v[164:167], a[204:207], a[164:167], v[164:167]\n"
"v_cvt_pk_bf16_f32 v193, v194, v195\n"
"v_cvt_pk_bf16_f32 v200, v200, v201\n"
"v_mfma_f32_16x16x32_bf16 v[168:171], a[208:211], a[160:163], v[168:171]\n"
"ds_read_b64_tr_b16 a[240:241], v244 offset:17408\n"
"v_cvt_pk_bf16_f32 v195, v198, v199\n"
"v_mfma_f32_16x16x32_bf16 v[168:171], a[212:215], a[164:167], v[168:171]\n"
"v_cvt_pk_bf16_f32 v194, v196, v197\n"
"v_xor_b32 v198, 0x400, v243\n"
"v_mfma_f32_16x16x32_bf16 v[172:175], a[216:219], a[160:163], v[172:175]\n"
"ds_read_b64_tr_b16 a[254:255], v245 offset:26112\n"
"v_mfma_f32_16x16x32_bf16 v[172:175], a[220:223], a[164:167], v[172:175]\n"
"v_cvt_pk_bf16_f32 v201, v202, v203\n"
"v_and_b32 v254, 0x3f803f80, v254\n"
"v_mfma_f32_16x16x32_bf16 v[140:143], a[216:219], a[144:147], v[140:143]\n"
"ds_read_b64_tr_b16 a[246:247], v244 offset:26112\n"
"v_bfe_u32 v196, v242, 8, 2\n"
"v_mfma_f32_16x16x32_bf16 v[140:143], a[220:223], a[148:151], v[140:143]\n"
"v_xor_b32 v229, 64, v228\n"
"v_cvt_pk_bf16_f32 v186, v188, v189\n"
"v_mfma_f32_16x16x32_bf16 v[136:139], a[208:211], a[144:147], v[136:139]\n"
"ds_read_b64_tr_b16 a[228:229], v244 offset:24576\n"
"v_bfe_u32 v249, v242, 8, 1\n"
"v_mfma_f32_16x16x32_bf16 v[136:139], a[212:215], a[148:151], v[136:139]\n"
"v_pk_fma_f32 v[208:209], v[208:209], s[70:71], s[70:71] op_sel:[0,0,1] op_sel_hi:[1,0,1]\nv_pk_fma_f32 v[210:211], v[210:211], s[70:71], s[70:71] op_sel:[0,0,1] op_sel_hi:[1,0,1]\nv_pk_fma_f32 v[212:213], v[212:213], s[70:71], s[70:71] op_sel:[0,0,1] op_sel_hi:[1,0,1]\nv_pk_fma_f32 v[214:215], v[214:215], s[70:71], s[70:71] op_sel:[0,0,1] op_sel_hi:[1,0,1]\nv_pk_fma_f32 v[216:217], v[216:217], s[70:71], s[70:71] op_sel:[0,0,1] op_sel_hi:[1,0,1]\nv_pk_fma_f32 v[218:219], v[218:219], s[70:71], s[70:71] op_sel:[0,0,1] op_sel_hi:[1,0,1]\nv_pk_fma_f32 v[220:221], v[220:221], s[70:71], s[70:71] op_sel:[0,0,1] op_sel_hi:[1,0,1]\nv_pk_fma_f32 v[222:223], v[222:223], s[70:71], s[70:71] op_sel:[0,0,1] op_sel_hi:[1,0,1]\nv_pk_fma_f32 v[160:161], v[160:161], s[70:71], s[70:71] op_sel:[0,0,1] op_sel_hi:[1,0,1]\nv_pk_fma_f32 v[162:163], v[162:163], s[70:71], s[70:71] op_sel:[0,0,1] op_sel_hi:[1,0,1]\nv_pk_fma_f32 v[164:165], v[164:165], s[70:71], s[70:71] op_sel:[0,0,1] op_sel_hi:[1,0,1]\nv_pk_fma_f32 v[166:167], v[166:167], s[70:71], s[70:71] op_sel:[0,0,1] op_sel_hi:[1,0,1]\nv_pk_fma_f32 v[168:169], v[168:169], s[70:71], s[70:71] op_sel:[0,0,1] op_sel_hi:[1,0,1]\nv_pk_fma_f32 v[170:171], v[170:171], s[70:71], s[70:71] op_sel:[0,0,1] op_sel_hi:[1,0,1]\nv_pk_fma_f32 v[172:173], v[172:173], s[70:71], s[70:71] op_sel:[0,0,1] op_sel_hi:[1,0,1]\nv_pk_fma_f32 v[174:175], v[174:175], s[70:71], s[70:71] op_sel:[0,0,1] op_sel_hi:[1,0,1]\n"
"v_mfma_f32_16x16x32_bf16 v[132:135], a[200:203], a[144:147], v[132:135]\n"
"s_add_u32 s40, s56, s54\n"
"v_cvt_pk_bf16_f32 v168, v168, v169\n"
"v_cvt_pk_bf16_f32 v169, v170, v171\n"
"v_mfma_f32_16x16x32_bf16 v[132:135], a[204:207], a[148:151], v[132:135]\n"
"buffer_load_dwordx4 v240, s[20:23], s40 offen offset:3072 nt lds\n"
"v_mfma_f32_16x16x32_bf16 v[128:131], a[192:195], a[144:147], v[128:131]\n"
"s_add_u32 s55, s55, s59\n"
"v_cvt_pk_bf16_f32 v216, v216, v217\n"
"v_cvt_pk_bf16_f32 v171, v174, v175\n"
"v_mfma_f32_16x16x32_bf16 v[128:131], a[196:199], a[148:151], v[128:131]\n"
"s_add_u32 s41, s55, s57\n"
"v_cvt_pk_bf16_f32 v179, v182, v183\n"
"v_cvt_pk_bf16_f32 v160, v160, v161\n"
"v_mfma_f32_16x16x32_bf16 v[144:147], a[192:195], a[152:155], v[144:147]\n"
"s_waitcnt lgkmcnt(1)\n"
"s_and_b32 s41, s41, s58\n"
"ds_read_b64_tr_b16 a[230:231], v244 offset:25088\n"
"v_cvt_pk_bf16_f32 v161, v162, v163\n"
"v_mfma_f32_16x16x32_bf16 v[144:147], a[196:199], a[156:159], v[144:147]\n"
"s_andn2_b32 s56, s55, s58\n"
"v_cvt_pk_bf16_f32 v202, v204, v205\n"
"v_cvt_pk_bf16_f32 v217, v218, v219\n"
"v_mfma_f32_16x16x32_bf16 v[148:151], a[200:203], a[152:155], v[148:151]\n"
"s_or_b32 s56, s56, s41\n"
"ds_read_b64_tr_b16 a[232:233], v245 offset:16384\n"
"v_cvt_pk_bf16_f32 v219, v222, v223\n"
"v_mfma_f32_16x16x32_bf16 v[148:151], a[204:207], a[156:159], v[148:151]\n"
"v_pk_fma_f32 v[112:113], v[112:113], s[70:71], s[70:71] op_sel:[0,0,1] op_sel_hi:[1,0,1]\nv_pk_fma_f32 v[114:115], v[114:115], s[70:71], s[70:71] op_sel:[0,0,1] op_sel_hi:[1,0,1]\nv_pk_fma_f32 v[116:117], v[116:117], s[70:71], s[70:71] op_sel:[0,0,1] op_sel_hi:[1,0,1]\nv_pk_fma_f32 v[118:119], v[118:119], s[70:71], s[70:71] op_sel:[0,0,1] op_sel_hi:[1,0,1]\nv_pk_fma_f32 v[120:121], v[120:121], s[70:71], s[70:71] op_sel:[0,0,1] op_sel_hi:[1,0,1]\nv_pk_fma_f32 v[122:123], v[122:123], s[70:71], s[70:71] op_sel:[0,0,1] op_sel_hi:[1,0,1]\nv_pk_fma_f32 v[124:125], v[124:125], s[70:71], s[70:71] op_sel:[0,0,1] op_sel_hi:[1,0,1]\nv_pk_fma_f32 v[126:127], v[126:127], s[70:71], s[70:71] op_sel:[0,0,1] op_sel_hi:[1,0,1]\nv_pk_fma_f32 v[128:129], v[128:129], s[70:71], s[70:71] op_sel:[0,0,1] op_sel_hi:[1,0,1]\nv_pk_fma_f32 v[130:131], v[130:131], s[70:71], s[70:71] op_sel:[0,0,1] op_sel_hi:[1,0,1]\nv_pk_fma_f32 v[132:133], v[132:133], s[70:71], s[70:71] op_sel:[0,0,1] op_sel_hi:[1,0,1]\nv_pk_fma_f32 v[134:135], v[134:135], s[70:71], s[70:71] op_sel:[0,0,1] op_sel_hi:[1,0,1]\nv_pk_fma_f32 v[136:137], v[136:137], s[70:71], s[70:71] op_sel:[0,0,1] op_sel_hi:[1,0,1]\nv_pk_fma_f32 v[138:139], v[138:139], s[70:71], s[70:71] op_sel:[0,0,1] op_sel_hi:[1,0,1]\nv_pk_fma_f32 v[140:141], v[140:141], s[70:71], s[70:71] op_sel:[0,0,1] op_sel_hi:[1,0,1]\nv_pk_fma_f32 v[142:143], v[142:143], s[70:71], s[70:71] op_sel:[0,0,1] op_sel_hi:[1,0,1]\n"
"v_mfma_f32_16x16x32_bf16 v[152:155], a[208:211], a[152:155], v[152:155]\n"
"v_cvt_pk_bf16_f32 v163, v166, v167\n"
"v_cvt_pk_bf16_f32 v120, v120, v121\n"
"v_mfma_f32_16x16x32_bf16 v[152:155], a[212:215], a[156:159], v[152:155]\n"
"v_cvt_pk_bf16_f32 v208, v208, v209\n"
"v_cvt_pk_bf16_f32 v218, v220, v221\n"
"v_mfma_f32_16x16x32_bf16 v[156:159], a[216:219], a[152:155], v[156:159]\n"
"v_cvt_pk_bf16_f32 v162, v164, v165\n"
"v_cvt_pk_bf16_f32 v170, v172, v173\n"
"v_mfma_f32_16x16x32_bf16 v[156:159], a[220:223], a[156:159], v[156:159]\n"
"v_permlane16_swap_b32 v169, v171\n"
"v_mfma_f32_16x16x32_bf16 v[96:99], a[224:227], a[184:187], v[96:99]\n"
"s_waitcnt lgkmcnt(1)\n"
"s_add_u32 m0, s44, 32768\n"
"v_cvt_pk_bf16_f32 v209, v210, v211\n"
"v_cvt_pk_bf16_f32 v128, v128, v129\n"
"v_mfma_f32_16x16x32_bf16 v[96:99], a[228:231], a[188:191], v[96:99]\n"
"s_waitcnt lgkmcnt(0)\n"
"buffer_load_dwordx4 v240, s[16:19], s56 offen nt lds\n"
"v_mfma_f32_16x16x32_bf16 v[100:103], a[232:235], a[184:187], v[100:103]\n"
"s_waitcnt vmcnt(14)\n"
"v_mov_b32 v205, 0\n"
"v_mfma_f32_16x16x32_bf16 v[100:103], a[236:239], a[188:191], v[100:103]\n"
"s_barrier\n"
"v_mfma_f32_16x16x32_bf16 v[104:107], a[240:243], a[184:187], v[104:107]\n"
"v_permlane16_swap_b32 v160, v162\n"
"v_mfma_f32_16x16x32_bf16 v[104:107], a[244:247], a[188:191], v[104:107]\n"
"s_mov_b32 s74, 0x41400000\n"
"ds_read_b128 a[204:207], v198 offset:2048\n"
"v_xor_b32 v231, 0x2000, v229\n"
"v_mfma_f32_16x16x32_bf16 v[108:111], a[248:251], a[184:187], v[108:111]\n"
"v_permlane16_swap_b32 v168, v170\n"
"v_mfma_f32_16x16x32_bf16 v[108:111], a[252:255], a[188:191], v[108:111]\n"
"ds_write2st64_b64 v255, v[192:193], v[194:195] offset0:100 offset1:101\n"
"v_mfma_f32_16x16x32_bf16 v[92:95], a[248:251], a[176:179], v[92:95]\n"
"v_cvt_pk_bf16_f32 v129, v130, v131\n"
"v_cvt_pk_bf16_f32 v136, v136, v137\n"
"v_mfma_f32_16x16x32_bf16 v[92:95], a[252:255], a[180:183], v[92:95]\n"
"v_pk_fma_f32 v[144:145], v[144:145], s[70:71], s[70:71] op_sel:[0,0,1] op_sel_hi:[1,0,1]\nv_pk_fma_f32 v[146:147], v[146:147], s[70:71], s[70:71] op_sel:[0,0,1] op_sel_hi:[1,0,1]\nv_pk_fma_f32 v[148:149], v[148:149], s[70:71], s[70:71] op_sel:[0,0,1] op_sel_hi:[1,0,1]\nv_pk_fma_f32 v[150:151], v[150:151], s[70:71], s[70:71] op_sel:[0,0,1] op_sel_hi:[1,0,1]\nv_pk_fma_f32 v[152:153], v[152:153], s[70:71], s[70:71] op_sel:[0,0,1] op_sel_hi:[1,0,1]\nv_pk_fma_f32 v[154:155], v[154:155], s[70:71], s[70:71] op_sel:[0,0,1] op_sel_hi:[1,0,1]\nv_pk_fma_f32 v[156:157], v[156:157], s[70:71], s[70:71] op_sel:[0,0,1] op_sel_hi:[1,0,1]\nv_pk_fma_f32 v[158:159], v[158:159], s[70:71], s[70:71] op_sel:[0,0,1] op_sel_hi:[1,0,1]\n"
"v_mfma_f32_16x16x32_bf16 v[88:91], a[240:243], a[176:179], v[88:91]\n"
"ds_read_b128 a[192:195], v243 offset:0\n"
"v_cvt_pk_bf16_f32 v187, v190, v191\n"
"v_mfma_f32_16x16x32_bf16 v[88:91], a[244:247], a[180:183], v[88:91]\n"
"ds_write2st64_b64 v255, v[184:185], v[186:187] offset0:98 offset1:99\n"
"v_cvt_pk_bf16_f32 v178, v180, v181\n"
"v_mfma_f32_16x16x32_bf16 v[84:87], a[232:235], a[176:179], v[84:87]\n"
"v_cvt_pk_bf16_f32 v210, v212, v213\n"
"v_lshl_add_u32 v196, v196, 4, s43\n"
"v_mfma_f32_16x16x32_bf16 v[84:87], a[236:239], a[180:183], v[84:87]\n"
"ds_write2st64_b64 v255, v[216:217], v[218:219] offset0:194 offset1:195\n"
"v_mfma_f32_16x16x32_bf16 v[80:83], a[224:227], a[176:179], v[80:83]\n"
"s_add_u32 s40, s56, s52\n"
"v_permlane16_swap_b32 v161, v163\n"
"v_mfma_f32_16x16x32_bf16 v[80:83], a[228:231], a[180:183], v[80:83]\n"
"buffer_load_dwordx4 v240, s[16:19], s40 offen offset:1024 nt lds\n"
"v_mfma_f32_16x16x32_bf16 v[64:67], a[224:227], a[168:171], v[64:67]\n"
"v_cvt_pk_bf16_f32 v211, v214, v215\n"
"v_cvt_pk_bf16_f32 v137, v138, v139\n"
"v_mfma_f32_16x16x32_bf16 v[64:67], a[228:231], a[172:175], v[64:67]\n"
"v_cvt_pk_bf16_f32 v131, v134, v135\n"
"v_cvt_pk_bf16_f32 v144, v144, v145\n"
"v_mfma_f32_16x16x32_bf16 v[68:71], a[232:235], a[168:171], v[68:71]\n"
"v_xor_b32 v197, 64, v196\n"
"v_cvt_pk_bf16_f32 v130, v132, v133\n"
"v_mfma_f32_16x16x32_bf16 v[68:71], a[236:239], a[172:175], v[68:71]\n"
"v_pk_fma_f32 v[96:97], v[96:97], s[70:71], s[70:71] op_sel:[0,0,1] op_sel_hi:[1,0,1]\nv_pk_fma_f32 v[98:99], v[98:99], s[70:71], s[70:71] op_sel:[0,0,1] op_sel_hi:[1,0,1]\nv_pk_fma_f32 v[100:101], v[100:101], s[70:71], s[70:71] op_sel:[0,0,1] op_sel_hi:[1,0,1]\nv_pk_fma_f32 v[102:103], v[102:103], s[70:71], s[70:71] op_sel:[0,0,1] op_sel_hi:[1,0,1]\nv_pk_fma_f32 v[104:105], v[104:105], s[70:71], s[70:71] op_sel:[0,0,1] op_sel_hi:[1,0,1]\nv_pk_fma_f32 v[106:107], v[106:107], s[70:71], s[70:71] op_sel:[0,0,1] op_sel_hi:[1,0,1]\nv_pk_fma_f32 v[108:109], v[108:109], s[70:71], s[70:71] op_sel:[0,0,1] op_sel_hi:[1,0,1]\nv_pk_fma_f32 v[110:111], v[110:111], s[70:71], s[70:71] op_sel:[0,0,1] op_sel_hi:[1,0,1]\nv_pk_fma_f32 v[80:81], v[80:81], s[70:71], s[70:71] op_sel:[0,0,1] op_sel_hi:[1,0,1]\nv_pk_fma_f32 v[82:83], v[82:83], s[70:71], s[70:71] op_sel:[0,0,1] op_sel_hi:[1,0,1]\nv_pk_fma_f32 v[84:85], v[84:85], s[70:71], s[70:71] op_sel:[0,0,1] op_sel_hi:[1,0,1]\nv_pk_fma_f32 v[86:87], v[86:87], s[70:71], s[70:71] op_sel:[0,0,1] op_sel_hi:[1,0,1]\nv_pk_fma_f32 v[88:89], v[88:89], s[70:71], s[70:71] op_sel:[0,0,1] op_sel_hi:[1,0,1]\nv_pk_fma_f32 v[90:91], v[90:91], s[70:71], s[70:71] op_sel:[0,0,1] op_sel_hi:[1,0,1]\nv_pk_fma_f32 v[92:93], v[92:93], s[70:71], s[70:71] op_sel:[0,0,1] op_sel_hi:[1,0,1]\nv_pk_fma_f32 v[94:95], v[94:95], s[70:71], s[70:71] op_sel:[0,0,1] op_sel_hi:[1,0,1]\n"
"v_mfma_f32_16x16x32_bf16 v[72:75], a[240:243], a[168:171], v[72:75]\n"
"v_cvt_pk_bf16_f32 v152, v152, v153\n"
"v_cvt_pk_bf16_f32 v80, v80, v81\n"
"v_mfma_f32_16x16x32_bf16 v[72:75], a[244:247], a[172:175], v[72:75]\n"
"v_cvt_pk_bf16_f32 v153, v154, v155\n"
"v_cvt_pk_bf16_f32 v81, v82, v83\n"
"v_mfma_f32_16x16x32_bf16 v[76:79], a[248:251], a[168:171], v[76:79]\n"
"v_cvt_pk_bf16_f32 v139, v142, v143\n"
"v_cvt_pk_bf16_f32 v104, v104, v105\n"
"v_mfma_f32_16x16x32_bf16 v[76:79], a[252:255], a[172:175], v[76:79]\n"
"ds_read_b128 v[212:215], v197 offset:0\n"
"v_cvt_pk_bf16_f32 v96, v96, v97\n"
"v_mfma_f32_16x16x32_bf16 v[60:63], a[248:251], a[160:163], v[60:63]\n"
"v_cvt_pk_bf16_f32 v138, v140, v141\n"
"v_cvt_pk_bf16_f32 v145, v146, v147\n"
"v_mfma_f32_16x16x32_bf16 v[60:63], a[252:255], a[164:167], v[60:63]\n"
"ds_write_b128 v243, v[168:171] offset:10240\n"
"v_mfma_f32_16x16x32_bf16 v[56:59], a[240:243], a[160:163], v[56:59]\n"
"s_add_u32 s40, s56, s53\n"
"v_cvt_pk_bf16_f32 v121, v122, v123\n"
"v_cvt_pk_bf16_f32 v155, v158, v159\n"
"v_mfma_f32_16x16x32_bf16 v[56:59], a[244:247], a[164:167], v[56:59]\n"
"buffer_load_dwordx4 v240, s[16:19], s40 offen offset:2048 nt lds\n"
"v_mfma_f32_16x16x32_bf16 v[52:55], a[232:235], a[160:163], v[52:55]\n"
"v_permlane16_swap_b32 v137, v139\n"
"v_mfma_f32_16x16x32_bf16 v[52:55], a[236:239], a[164:167], v[52:55]\n"
"v_permlane16_swap_b32 v128, v130\n"
"v_mfma_f32_16x16x32_bf16 v[48:51], a[224:227], a[160:163], v[48:51]\n"
"v_cvt_pk_bf16_f32 v122, v124, v125\n"
"v_cvt_pk_bf16_f32 v147, v150, v151\n"
"v_mfma_f32_16x16x32_bf16 v[48:51], a[228:231], a[164:167], v[48:51]\n"
"v_cvt_pk_bf16_f32 v83, v86, v87\n"
"v_lshl_add_u32 v248, v249, 9, v248\n"
"v_mfma_f32_16x16x32_bf16 v[0:3], a[224:227], a[136:139], v[0:3]\n"
"ds_read_b128 v[216:219], v196 offset:1024\n"
"v_cvt_pk_bf16_f32 v154, v156, v157\n"
"v_mfma_f32_16x16x32_bf16 v[0:3], a[228:231], a[140:143], v[0:3]\n"
"ds_write_b128 v243, v[160:163] offset:8192\n"
"v_mfma_f32_16x16x32_bf16 v[4:7], a[232:235], a[136:139], v[4:7]\n"
"v_permlane16_swap_b32 v136, v138\n"
"v_mfma_f32_16x16x32_bf16 v[4:7], a[236:239], a[140:143], v[4:7]\n"
"v_permlane16_swap_b32 v129, v131\n"
"v_mfma_f32_16x16x32_bf16 v[8:11], a[240:243], a[136:139], v[8:11]\n"
"ds_read_b128 a[200:203], v243 offset:2048\n"
"v_cvt_pk_bf16_f32 v105, v106, v107\n"
"v_mfma_f32_16x16x32_bf16 v[8:11], a[244:247], a[140:143], v[8:11]\n"
"v_accvgpr_write_b32 a120, v136\n"
"v_cvt_pk_bf16_f32 v146, v148, v149\n"
"v_mfma_f32_16x16x32_bf16 v[12:15], a[248:251], a[136:139], v[12:15]\n"
"v_permlane16_swap_b32 v152, v154\n"
"v_mfma_f32_16x16x32_bf16 v[12:15], a[252:255], a[140:143], v[12:15]\n"
"ds_write2st64_b64 v255, v[208:209], v[210:211] offset0:192 offset1:193\n"
"v_pk_fma_f32 v[64:65], v[64:65], s[70:71], s[70:71] op_sel:[0,0,1] op_sel_hi:[1,0,1]\nv_pk_fma_f32 v[66:67], v[66:67], s[70:71], s[70:71] op_sel:[0,0,1] op_sel_hi:[1,0,1]\nv_pk_fma_f32 v[68:69], v[68:69], s[70:71], s[70:71] op_sel:[0,0,1] op_sel_hi:[1,0,1]\nv_pk_fma_f32 v[70:71], v[70:71], s[70:71], s[70:71] op_sel:[0,0,1] op_sel_hi:[1,0,1]\nv_pk_fma_f32 v[72:73], v[72:73], s[70:71], s[70:71] op_sel:[0,0,1] op_sel_hi:[1,0,1]\nv_pk_fma_f32 v[74:75], v[74:75], s[70:71], s[70:71] op_sel:[0,0,1] op_sel_hi:[1,0,1]\nv_pk_fma_f32 v[76:77], v[76:77], s[70:71], s[70:71] op_sel:[0,0,1] op_sel_hi:[1,0,1]\nv_pk_fma_f32 v[78:79], v[78:79], s[70:71], s[70:71] op_sel:[0,0,1] op_sel_hi:[1,0,1]\nv_pk_fma_f32 v[48:49], v[48:49], s[70:71], s[70:71] op_sel:[0,0,1] op_sel_hi:[1,0,1]\nv_pk_fma_f32 v[50:51], v[50:51], s[70:71], s[70:71] op_sel:[0,0,1] op_sel_hi:[1,0,1]\nv_pk_fma_f32 v[52:53], v[52:53], s[70:71], s[70:71] op_sel:[0,0,1] op_sel_hi:[1,0,1]\nv_pk_fma_f32 v[54:55], v[54:55], s[70:71], s[70:71] op_sel:[0,0,1] op_sel_hi:[1,0,1]\nv_pk_fma_f32 v[56:57], v[56:57], s[70:71], s[70:71] op_sel:[0,0,1] op_sel_hi:[1,0,1]\nv_pk_fma_f32 v[58:59], v[58:59], s[70:71], s[70:71] op_sel:[0,0,1] op_sel_hi:[1,0,1]\nv_pk_fma_f32 v[60:61], v[60:61], s[70:71], s[70:71] op_sel:[0,0,1] op_sel_hi:[1,0,1]\nv_pk_fma_f32 v[62:63], v[62:63], s[70:71], s[70:71] op_sel:[0,0,1] op_sel_hi:[1,0,1]\n"
"v_mfma_f32_16x16x32_bf16 v[28:31], a[248:251], a[144:147], v[28:31]\n"
"s_add_u32 s40, s56, s54\n"
"v_permlane16_swap_b32 v145, v147\n"
"v_mfma_f32_16x16x32_bf16 v[28:31], a[252:255], a[148:151], v[28:31]\n"
"buffer_load_dwordx4 v240, s[16:19], s40 offen offset:3072 nt lds\n"
"v_mfma_f32_16x16x32_bf16 v[24:27], a[240:243], a[144:147], v[24:27]\n"
"v_cvt_pk_bf16_f32 v203, v206, v207\n"
"v_cvt_pk_bf16_f32 v72, v72, v73\n"
"v_mfma_f32_16x16x32_bf16 v[24:27], a[244:247], a[148:151], v[24:27]\n"
"v_cvt_pk_bf16_f32 v97, v98, v99\n"
"v_cvt_pk_bf16_f32 v64, v64, v65\n"
"v_mfma_f32_16x16x32_bf16 v[20:23], a[232:235], a[144:147], v[20:23]\n"
"v_cvt_pk_bf16_f32 v106, v108, v109\n"
"v_cvt_pk_bf16_f32 v56, v56, v57\n"
"v_mfma_f32_16x16x32_bf16 v[20:23], a[236:239], a[148:151], v[20:23]\n"
"v_accvgpr_write_b32 a112, v128\n"
"v_accvgpr_write_b32 a131, v147\n"
"v_mfma_f32_16x16x32_bf16 v[16:19], a[224:227], a[144:147], v[16:19]\n"
"ds_read_b128 v[208:211], v196 offset:0\n"
"v_cvt_pk_bf16_f32 v88, v88, v89\n"
"v_mfma_f32_16x16x32_bf16 v[16:19], a[228:231], a[148:151], v[16:19]\n"
"v_accvgpr_write_b32 a113, v129\n"
"v_cvt_pk_bf16_f32 v82, v84, v85\n"
"v_mfma_f32_16x16x32_bf16 v[32:35], a[224:227], a[152:155], v[32:35]\n"
"v_accvgpr_write_b32 a114, v130\n"
"v_cvt_pk_bf16_f32 v57, v58, v59\n"
"v_mfma_f32_16x16x32_bf16 v[32:35], a[228:231], a[156:159], v[32:35]\n"
"v_cvt_pk_bf16_f32 v73, v74, v75\n"
"v_xor_b32_e64 v249, s42, 0\n"
"v_mfma_f32_16x16x32_bf16 v[36:39], a[232:235], a[152:155], v[36:39]\n"
"ds_read_b128 v[220:223], v197 offset:1024\n"
"v_lshl_add_u32 v248, v249, 12, v248\n"
"v_mfma_f32_16x16x32_bf16 v[36:39], a[236:239], a[156:159], v[36:39]\n"
"v_cvt_pk_bf16_f32 v89, v90, v91\n"
"v_cvt_pk_bf16_f32 v65, v66, v67\n"
"v_mfma_f32_16x16x32_bf16 v[40:43], a[240:243], a[152:155], v[40:43]\n"
"v_permlane16_swap_b32 v120, v122\n"
"v_mfma_f32_16x16x32_bf16 v[40:43], a[244:247], a[156:159], v[40:43]\n"
"ds_write2st64_b64 v255, v[200:201], v[202:203] offset0:102 offset1:103\n"
"v_pk_fma_f32 v[0:1], v[0:1], s[70:71], s[70:71] op_sel:[0,0,1] op_sel_hi:[1,0,1]\nv_pk_fma_f32 v[2:3], v[2:3], s[70:71], s[70:71] op_sel:[0,0,1] op_sel_hi:[1,0,1]\nv_pk_fma_f32 v[4:5], v[4:5], s[70:71], s[70:71] op_sel:[0,0,1] op_sel_hi:[1,0,1]\nv_pk_fma_f32 v[6:7], v[6:7], s[70:71], s[70:71] op_sel:[0,0,1] op_sel_hi:[1,0,1]\nv_pk_fma_f32 v[8:9], v[8:9], s[70:71], s[70:71] op_sel:[0,0,1] op_sel_hi:[1,0,1]\nv_pk_fma_f32 v[10:11], v[10:11], s[70:71], s[70:71] op_sel:[0,0,1] op_sel_hi:[1,0,1]\nv_pk_fma_f32 v[12:13], v[12:13], s[70:71], s[70:71] op_sel:[0,0,1] op_sel_hi:[1,0,1]\nv_pk_fma_f32 v[14:15], v[14:15], s[70:71], s[70:71] op_sel:[0,0,1] op_sel_hi:[1,0,1]\nv_pk_fma_f32 v[16:17], v[16:17], s[70:71], s[70:71] op_sel:[0,0,1] op_sel_hi:[1,0,1]\nv_pk_fma_f32 v[18:19], v[18:19], s[70:71], s[70:71] op_sel:[0,0,1] op_sel_hi:[1,0,1]\nv_pk_fma_f32 v[20:21], v[20:21], s[70:71], s[70:71] op_sel:[0,0,1] op_sel_hi:[1,0,1]\nv_pk_fma_f32 v[22:23], v[22:23], s[70:71], s[70:71] op_sel:[0,0,1] op_sel_hi:[1,0,1]\nv_pk_fma_f32 v[24:25], v[24:25], s[70:71], s[70:71] op_sel:[0,0,1] op_sel_hi:[1,0,1]\nv_pk_fma_f32 v[26:27], v[26:27], s[70:71], s[70:71] op_sel:[0,0,1] op_sel_hi:[1,0,1]\nv_pk_fma_f32 v[28:29], v[28:29], s[70:71], s[70:71] op_sel:[0,0,1] op_sel_hi:[1,0,1]\nv_pk_fma_f32 v[30:31], v[30:31], s[70:71], s[70:71] op_sel:[0,0,1] op_sel_hi:[1,0,1]\n"
"v_mfma_f32_16x16x32_bf16 v[44:47], a[248:251], a[152:155], v[44:47]\n"
"v_accvgpr_write_b32 a138, v154\n"
"v_cvt_pk_bf16_f32 v91, v94, v95\n"
"v_mfma_f32_16x16x32_bf16 v[44:47], a[252:255], a[156:159], v[44:47]\n"
"s_waitcnt lgkmcnt(2)\n"
"v_accvgpr_write_b32 a122, v138\n"
"v_accvgpr_write_b32 a242, v254\n"
"v_mfma_f32_16x16x32_bf16 v[180:183], v[208:211], a[0:3], 0\n"
"v_permlane16_swap_b32 v144, v146\n"
"v_mfma_f32_16x16x32_bf16 v[180:183], v[212:215], a[4:7], v[180:183]\n"
"v_permlane16_swap_b32 v153, v155\n"
"v_mfma_f32_16x16x32_bf16 v[180:183], v[216:219], a[8:11], v[180:183]\n"
"s_waitcnt lgkmcnt(1)\n"
"ds_read_b128 a[196:199], v198 offset:0\n"
"v_cvt_pk_bf16_f32 v90, v92, v93\n"
"v_mfma_f32_16x16x32_bf16 v[180:183], v[220:223], a[12:15], v[180:183]\n"
"ds_write2st64_b64 v255, v[80:81], v[82:83] offset0:224 offset1:225\n"
"v_pk_fma_f32 v[32:33], v[32:33], s[70:71], s[70:71] op_sel:[0,0,1] op_sel_hi:[1,0,1]\nv_pk_fma_f32 v[34:35], v[34:35], s[70:71], s[70:71] op_sel:[0,0,1] op_sel_hi:[1,0,1]\nv_pk_fma_f32 v[36:37], v[36:37], s[70:71], s[70:71] op_sel:[0,0,1] op_sel_hi:[1,0,1]\nv_pk_fma_f32 v[38:39], v[38:39], s[70:71], s[70:71] op_sel:[0,0,1] op_sel_hi:[1,0,1]\nv_pk_fma_f32 v[40:41], v[40:41], s[70:71], s[70:71] op_sel:[0,0,1] op_sel_hi:[1,0,1]\nv_pk_fma_f32 v[42:43], v[42:43], s[70:71], s[70:71] op_sel:[0,0,1] op_sel_hi:[1,0,1]\nv_pk_fma_f32 v[44:45], v[44:45], s[70:71], s[70:71] op_sel:[0,0,1] op_sel_hi:[1,0,1]\nv_pk_fma_f32 v[46:47], v[46:47], s[70:71], s[70:71] op_sel:[0,0,1] op_sel_hi:[1,0,1]\n"
"v_mfma_f32_16x16x32_bf16 v[184:187], v[208:211], a[16:19], 0\n"
"v_accvgpr_write_b32 a139, v155\n"
"v_cvt_pk_bf16_f32 v48, v48, v49\n"
"v_mfma_f32_16x16x32_bf16 v[184:187], v[212:215], a[20:23], v[184:187]\n"
"v_accvgpr_write_b32 a123, v139\n"
"v_cvt_pk_bf16_f32 v49, v50, v51\n"
"v_mfma_f32_16x16x32_bf16 v[184:187], v[216:219], a[24:27], v[184:187]\n"
"v_accvgpr_write_b32 a136, v152\n"
"v_cvt_pk_bf16_f32 v40, v40, v41\n"
"v_mfma_f32_16x16x32_bf16 v[184:187], v[220:223], a[28:31], v[184:187]\n"
"ds_write2st64_b64 v255, v[176:177], v[178:179] offset0:96 offset1:97\n"
"v_sub_f32 v180, s74, v180\n"
"v_sub_f32 v181, s74, v181\n"
"v_mfma_f32_16x16x32_bf16 v[188:191], v[208:211], a[32:35], 0\n"
"v_sub_f32 v182, s74, v182\n"
"v_sub_f32 v183, s74, v183\n"
"v_mfma_f32_16x16x32_bf16 v[188:191], v[212:215], a[36:39], v[188:191]\n"
"v_accvgpr_write_b32 a128, v144\n"
"v_cvt_pk_bf16_f32 v74, v76, v77\n"
"v_mfma_f32_16x16x32_bf16 v[188:191], v[216:219], a[40:43], v[188:191]\n"
"v_cvt_pk_bf16_f32 v107, v110, v111\n"
"v_cvt_pk_bf16_f32 v8, v8, v9\n"
"v_mfma_f32_16x16x32_bf16 v[188:191], v[220:223], a[44:47], v[188:191]\n"
"v_cvt_pk_bf16_f32 v98, v100, v101\n"
"v_mov_b32 v199, 0\n"
"v_mfma_f32_16x16x32_bf16 v[192:195], v[208:211], a[192:195], 0\n"
"s_waitcnt lgkmcnt(1)\n"
"ds_read_b128 v[208:211], v228 offset:0\n"
"v_accvgpr_write_b32 a137, v153\n"
"v_mfma_f32_16x16x32_bf16 v[192:195], v[212:215], a[196:199], v[192:195]\n"
"ds_read_b128 v[212:215], v229 offset:0\n"
"v_sub_f32 v185, s74, v185\n"
"v_mfma_f32_16x16x32_bf16 v[192:195], v[216:219], a[200:203], v[192:195]\n"
"ds_read_b128 v[216:219], v228 offset:1024\n"
"v_sub_f32 v190, s74, v190\n"
"v_mfma_f32_16x16x32_bf16 v[192:195], v[220:223], a[204:207], v[192:195]\n"
"s_waitcnt lgkmcnt(2)\n"
"ds_read_b128 v[220:223], v229 offset:1024\n"
"v_mov_b32 v204, 0\n"
"v_mfma_f32_16x16x32_bf16 v[152:155], v[208:211], a[0:3], v[180:183]\n"
"s_waitcnt lgkmcnt(2)\n"
"v_sub_f32 v184, s74, v184\n"
"v_sub_f32 v188, s74, v188\n"
"v_mfma_f32_16x16x32_bf16 v[152:155], v[212:215], a[4:7], v[152:155]\n"
"s_waitcnt lgkmcnt(1)\n"
"v_sub_f32 v186, s74, v186\n"
"v_sub_f32 v187, s74, v187\n"
"v_mfma_f32_16x16x32_bf16 v[152:155], v[216:219], a[8:11], v[152:155]\n"
"s_waitcnt lgkmcnt(0)\n"
"v_cvt_pk_bf16_f32 v99, v102, v103\n"
"v_cvt_pk_bf16_f32 v9, v10, v11\n"
"v_mfma_f32_16x16x32_bf16 v[152:155], v[220:223], a[12:15], v[152:155]\n"
"ds_write2st64_b64 v255, v[88:89], v[90:91] offset0:226 offset1:227\n"
"v_accvgpr_write_b32 a121, v137\n"
"v_cvt_pk_bf16_f32 v58, v60, v61\n"
"v_sub_f32 v191, s74, v191\n"
"v_mfma_f32_16x16x32_bf16 v[156:159], v[216:219], a[24:27], v[184:187]\n"
"v_cvt_pk_bf16_f32 v24, v24, v25\n"
"v_sub_f32 v193, s74, v193\n"
"v_mfma_f32_16x16x32_bf16 v[156:159], v[220:223], a[28:31], v[156:159]\n"
"v_sub_f32 v189, s74, v189\n"
"v_sub_f32 v194, s74, v194\n"
"v_mfma_f32_16x16x32_bf16 v[156:159], v[208:211], a[16:19], v[156:159]\n"
"v_cvt_pk_bf16_f32 v50, v52, v53\n"
"v_sub_f32 v192, s74, v192\n"
"v_mfma_f32_16x16x32_bf16 v[156:159], v[212:215], a[20:23], v[156:159]\n"
"v_exp_f32 v154, v154\n"
"v_mfma_f32_16x16x32_bf16 v[160:163], v[208:211], a[32:35], v[188:191]\n"
"v_cvt_pk_bf16_f32 v11, v14, v15\n"
"v_sub_f32 v195, s74, v195\n"
"v_mfma_f32_16x16x32_bf16 v[160:163], v[212:215], a[36:39], v[160:163]\n"
"v_exp_f32 v152, v152\n"
"v_mfma_f32_16x16x32_bf16 v[160:163], v[216:219], a[40:43], v[160:163]\n"
"s_nop 0\n"
"v_exp_f32 v158, v158\n"
"v_mfma_f32_16x16x32_bf16 v[160:163], v[220:223], a[44:47], v[160:163]\n"
"v_accvgpr_write_b32 a129, v145\n"
"v_cvt_pk_bf16_f32 v41, v42, v43\n"
"v_mfma_f32_16x16x32_bf16 v[164:167], v[216:219], a[200:203], v[192:195]\n"
"ds_read_b128 v[216:219], v228 offset:5120\n"
"v_cvt_pk_bf16_f32 v25, v26, v27\n"
"v_mfma_f32_16x16x32_bf16 v[164:167], v[220:223], a[204:207], v[164:167]\n"
"ds_read_b128 v[220:223], v229 offset:5120\n"
"v_cvt_pk_bf16_f32 v66, v68, v69\n"
"v_mfma_f32_16x16x32_bf16 v[164:167], v[208:211], a[192:195], v[164:167]\n"
"ds_read_b128 v[208:211], v228 offset:4096\n"
"v_cvt_pk_bf16_f32 v123, v126, v127\n"
"v_mfma_f32_16x16x32_bf16 v[164:167], v[212:215], a[196:199], v[164:167]\n"
"s_waitcnt lgkmcnt(2)\n"
"ds_read_b128 v[212:215], v229 offset:4096\n"
"v_cvt_pk_bf16_f32 v59, v62, v63\n"
"v_mfma_f32_16x16x32_bf16 v[168:171], v[216:219], a[8:11], v[180:183]\n"
"s_waitcnt lgkmcnt(2)\n"
"v_exp_f32 v155, v155\n"
"v_mfma_f32_16x16x32_bf16 v[168:171], v[220:223], a[12:15], v[168:171]\n"
"s_waitcnt lgkmcnt(1)\n"
"v_exp_f32 v157, v157\n"
"v_mfma_f32_16x16x32_bf16 v[168:171], v[208:211], a[0:3], v[168:171]\n"
"s_waitcnt lgkmcnt(0)\n"
"v_permlane16_swap_b32 v57, v59\n"
"v_mfma_f32_16x16x32_bf16 v[168:171], v[212:215], a[4:7], v[168:171]\n"
"ds_write2st64_b64 v255, v[96:97], v[98:99] offset0:228 offset1:229\n"
"v_cvt_pk_bf16_f32 v51, v54, v55\n"
"v_exp_f32 v160, v160\n"
"v_exp_f32 v161, v161\n"
"v_mfma_f32_16x16x32_bf16 v[172:175], v[208:211], a[16:19], v[184:187]\n"
"v_exp_f32 v159, v159\n"
"v_mfma_f32_16x16x32_bf16 v[172:175], v[212:215], a[20:23], v[172:175]\n"
"v_exp_f32 v156, v156\n"
"v_mfma_f32_16x16x32_bf16 v[172:175], v[216:219], a[24:27], v[172:175]\n"
"v_exp_f32 v153, v153\n"
"v_mfma_f32_16x16x32_bf16 v[172:175], v[220:223], a[28:31], v[172:175]\n"
"ds_write2st64_b64 v255, v[104:105], v[106:107] offset0:230 offset1:231\n"
"v_cvt_pk_bf16_f32 v75, v78, v79\n"
"v_accvgpr_write_b32 a240, v254\n"
"v_accvgpr_write_b32 a243, v254\n"
"v_cvt_pk_bf16_f32 v136, v156, v157\n"
"v_cvt_pk_bf16_f32 v144, v160, v161\n"
"v_exp_f32 v162, v162\n"
"v_exp_f32 v164, v164\n"
"v_exp_f32 v167, v167\n"
"v_mfma_f32_16x16x32_bf16 v[176:179], v[216:219], a[40:43], v[188:191]\n"
"v_exp_f32 v169, v169\n"
"v_mfma_f32_16x16x32_bf16 v[176:179], v[220:223], a[44:47], v[176:179]\n"
"v_permlane16_swap_b32 v48, v50\n"
"v_mfma_f32_16x16x32_bf16 v[176:179], v[208:211], a[32:35], v[176:179]\n"
"v_cvt_pk_bf16_f32 v128, v152, v153\n"
"v_cvt_pk_bf16_f32 v129, v154, v155\n"
"v_mfma_f32_16x16x32_bf16 v[176:179], v[212:215], a[36:39], v[176:179]\n"
"v_accvgpr_write_b32 a115, v131\n"
"v_exp_f32 v163, v163\n"
"v_exp_f32 v165, v165\n"
"v_exp_f32 v166, v166\n"
"v_exp_f32 v168, v168\n"
"v_exp_f32 v170, v170\n"
"v_exp_f32 v173, v173\n"
"v_add_u32 v249, s75, v248\n"
"v_mfma_f32_16x16x32_bf16 v[152:155], v[208:211], a[192:195], v[192:195]\n"
"ds_read_b128 v[208:211], v230 offset:0\n"
"v_cvt_pk_bf16_f32 v67, v70, v71\n"
"v_mfma_f32_16x16x32_bf16 v[152:155], v[212:215], a[196:199], v[152:155]\n"
"ds_read_b128 v[212:215], v231 offset:0\n"
"v_cvt_pk_bf16_f32 v145, v162, v163\n"
"v_mfma_f32_16x16x32_bf16 v[152:155], v[216:219], a[200:203], v[152:155]\n"
"ds_read_b128 v[216:219], v230 offset:1024\n"
"v_mov_b32 v196, 0\n"
"v_mfma_f32_16x16x32_bf16 v[152:155], v[220:223], a[204:207], v[152:155]\n"
"s_waitcnt lgkmcnt(2)\n"
"ds_read_b128 v[220:223], v231 offset:1024\n"
"v_cvt_pk_bf16_f32 v137, v158, v159\n"
"v_mfma_f32_16x16x32_bf16 v[156:159], v[208:211], a[0:3], v[180:183]\n"
"s_waitcnt lgkmcnt(2)\n"
"v_permlane16_swap_b32 v49, v51\n"
"v_mfma_f32_16x16x32_bf16 v[156:159], v[212:215], a[4:7], v[156:159]\n"
"s_waitcnt lgkmcnt(1)\n"
"v_exp_f32 v178, v178\n"
"v_mfma_f32_16x16x32_bf16 v[156:159], v[216:219], a[8:11], v[156:159]\n"
"s_waitcnt lgkmcnt(0)\n"
"v_exp_f32 v174, v174\n"
"v_mfma_f32_16x16x32_bf16 v[156:159], v[220:223], a[12:15], v[156:159]\n"
"ds_write2st64_b64 v255, v[72:73], v[74:75] offset0:198 offset1:199\n"
"v_cvt_pk_bf16_f32 v112, v112, v113\n"
"v_permlane16_swap_b32 v56, v58\n"
"v_permlane16_swap_b32 v9, v11\n"
"v_cvt_pk_bf16_f32 v27, v30, v31\n"
"v_cvt_pk_bf16_f32 v125, v166, v167\n"
"v_cvt_pk_bf16_f32 v130, v168, v169\n"
"v_exp_f32 v171, v171\n"
"v_exp_f32 v172, v172\n"
"v_mfma_f32_16x16x32_bf16 v[160:163], v[216:219], a[24:27], v[184:187]\n"
"v_exp_f32 v179, v179\n"
"v_mfma_f32_16x16x32_bf16 v[160:163], v[220:223], a[28:31], v[160:163]\n"
"v_exp_f32 v154, v154\n"
"v_mfma_f32_16x16x32_bf16 v[160:163], v[208:211], a[16:19], v[160:163]\n"
"v_cvt_pk_bf16_f32 v124, v164, v165\n"
"v_cvt_pk_bf16_f32 v138, v172, v173\n"
"v_mfma_f32_16x16x32_bf16 v[160:163], v[212:215], a[20:23], v[160:163]\n"
"ds_write2st64_b64 v255, v[64:65], v[66:67] offset0:196 offset1:197\n"
"v_accvgpr_write_b32 a241, v254\n"
"v_cvt_pk_bf16_f32 v131, v170, v171\n"
"v_exp_f32 v176, v176\n"
"v_exp_f32 v157, v157\n"
"v_mfma_f32_16x16x32_bf16 v[164:167], v[208:211], a[32:35], v[188:191]\n"
"ds_read_b128 v[208:211], v230 offset:4096\n"
"v_cvt_pk_bf16_f32 v43, v46, v47\n"
"v_mfma_f32_16x16x32_bf16 v[164:167], v[212:215], a[36:39], v[164:167]\n"
"ds_read_b128 v[212:215], v231 offset:4096\n"
"v_cvt_pk_bf16_f32 v113, v114, v115\n"
"v_mfma_f32_16x16x32_bf16 v[164:167], v[216:219], a[40:43], v[164:167]\n"
"ds_read_b128 v[216:219], v230 offset:5120\n"
"v_cvt_pk_bf16_f32 v32, v32, v33\n"
"v_mfma_f32_16x16x32_bf16 v[164:167], v[220:223], a[44:47], v[164:167]\n"
"s_waitcnt lgkmcnt(2)\n"
"ds_read_b128 v[220:223], v231 offset:5120\n"
"v_accvgpr_write_b32 a130, v146\n"
"v_permlane16_swap_b32 v41, v43\n"
"v_exp_f32 v175, v175\n"
"v_exp_f32 v177, v177\n"
"v_exp_f32 v162, v162\n"
"v_mfma_f32_16x16x32_bf16 v[168:171], v[208:211], a[0:3], v[180:183]\n"
"s_waitcnt lgkmcnt(2)\n"
"v_cvt_pk_bf16_f32 v10, v12, v13\n"
"v_cvt_pk_bf16_f32 v139, v174, v175\n"
"v_mfma_f32_16x16x32_bf16 v[168:171], v[212:215], a[4:7], v[168:171]\n"
"s_waitcnt lgkmcnt(1)\n"
"v_exp_f32 v158, v158\n"
"v_mfma_f32_16x16x32_bf16 v[168:171], v[216:219], a[8:11], v[168:171]\n"
"s_waitcnt lgkmcnt(0)\n"
"v_cvt_pk_bf16_f32 v26, v28, v29\n"
"v_cvt_pk_bf16_f32 v147, v178, v179\n"
"v_mfma_f32_16x16x32_bf16 v[168:171], v[220:223], a[12:15], v[168:171]\n"
"ds_write_b128 v243, v[48:51] offset:12288\n"
"v_accvgpr_write_b32 a155, v11\n"
"v_accvgpr_write_b32 a187, v43\n"
"v_exp_f32 v153, v153\n"
"v_exp_f32 v156, v156\n"
"v_mfma_f32_16x16x32_bf16 v[172:175], v[216:219], a[24:27], v[184:187]\n"
"v_exp_f32 v155, v155\n"
"v_mfma_f32_16x16x32_bf16 v[172:175], v[220:223], a[28:31], v[172:175]\n"
"v_exp_f32 v160, v160\n"
"v_mfma_f32_16x16x32_bf16 v[172:175], v[208:211], a[16:19], v[172:175]\n"
"v_cvt_pk_bf16_f32 v42, v44, v45\n"
"v_cvt_pk_bf16_f32 v146, v176, v177\n"
"v_mfma_f32_16x16x32_bf16 v[172:175], v[212:215], a[20:23], v[172:175]\n"
"s_cbranch_vccnz LPCE0_%=\nLPKE0_%=:\n"
"ds_write_b128 v243, v[56:59] offset:14336\n"
"v_permlane16_swap_b32 v24, v26\n"
"v_cvt_pk_bf16_f32 v127, v154, v155\n"
"v_exp_f32 v163, v163\n"
"v_exp_f32 v164, v164\n"
"v_exp_f32 v165, v165\n"
"v_exp_f32 v166, v166\n"
"v_exp_f32 v167, v167\n"
"v_mfma_f32_16x16x32_bf16 v[176:179], v[208:211], a[32:35], v[188:191]\n"
"v_exp_f32 v152, v152\n"
"v_mfma_f32_16x16x32_bf16 v[176:179], v[212:215], a[36:39], v[176:179]\n"
"v_cvt_pk_bf16_f32 v0, v0, v1\n"
"v_cvt_pk_bf16_f32 v132, v156, v157\n"
"v_mfma_f32_16x16x32_bf16 v[176:179], v[216:219], a[40:43], v[176:179]\n"
"v_accvgpr_write_b32 a153, v9\n"
"v_accvgpr_write_b32 a170, v26\n"
"v_mfma_f32_16x16x32_bf16 v[176:179], v[220:223], a[44:47], v[176:179]\n"
"v_cvt_pk_bf16_f32 v1, v2, v3\n"
"v_cvt_pk_bf16_f32 v16, v16, v17\n"
"v_cvt_pk_bf16_f32 v126, v152, v153\n"
"v_exp_f32 v159, v159\n"
"v_exp_f32 v168, v168\n"
"v_exp_f32 v169, v169\n"
"v_exp_f32 v170, v170\n"
"v_exp_f32 v173, v173\n"
"s_waitcnt vmcnt(8) lgkmcnt(0)\n"
"s_barrier\n"
"ds_read_b128 v[208:211], v228 offset:16384\n"
"ds_read_b128 v[212:215], v229 offset:16384\n"
"ds_read_b128 v[216:219], v228 offset:17408\n"
"ds_read_b128 v[220:223], v229 offset:17408\n"
"s_waitcnt lgkmcnt(3)\n"
"v_mfma_f32_16x16x32_bf16 v[152:155], v[208:211], a[0:3], v[180:183]\n"
"v_exp_f32 v177, v177\n"
"s_waitcnt lgkmcnt(2)\n"
"v_mfma_f32_16x16x32_bf16 v[152:155], v[212:215], a[4:7], v[152:155]\n"
"ds_read_b128 a[140:143], v249 offset:33792\n"
"v_cvt_pk_bf16_f32 v141, v162, v163\n"
"s_waitcnt lgkmcnt(2)\n"
"v_mfma_f32_16x16x32_bf16 v[152:155], v[216:219], a[8:11], v[152:155]\n"
"s_waitcnt lgkmcnt(1)\n"
"v_exp_f32 v175, v175\n"
"v_mfma_f32_16x16x32_bf16 v[152:155], v[220:223], a[12:15], v[152:155]\n"
"ds_read_b64_tr_b16 a[246:247], v234 offset:4096\n"
"ds_read_b64_tr_b16 a[250:251], v238 offset:4096\n"
"v_permlane16_swap_b32 v8, v10\n"
"v_cvt_pk_bf16_f32 v33, v34, v35\n"
"v_cvt_pk_bf16_f32 v34, v36, v37\n"
"v_cvt_pk_bf16_f32 v133, v158, v159\n"
"v_exp_f32 v161, v161\n"
"v_exp_f32 v176, v176\n"
"v_mfma_f32_16x16x32_bf16 v[156:159], v[216:219], a[24:27], v[184:187]\n"
"ds_read_b128 a[148:151], v249 offset:34816\n"
"v_mov_b32 v203, 0\n"
"v_mfma_f32_16x16x32_bf16 v[156:159], v[220:223], a[28:31], v[156:159]\n"
"ds_read_b64_tr_b16 a[244:245], v234 offset:0\n"
"v_accvgpr_write_b32 a154, v10\n"
"v_mfma_f32_16x16x32_bf16 v[156:159], v[208:211], a[16:19], v[156:159]\n"
"ds_read_b64_tr_b16 a[248:249], v238 offset:0\n"
"ds_read_b128 a[172:175], v249 offset:50176\n"
"v_mfma_f32_16x16x32_bf16 v[156:159], v[212:215], a[20:23], v[156:159]\n"
"ds_read_b64_tr_b16 v[226:227], v239 offset:4096\n"
"v_cvt_pk_bf16_f32 v115, v118, v119\n"
"v_permlane16_swap_b32 v40, v42\n"
"v_cvt_pk_bf16_f32 v140, v160, v161\n"
"v_cvt_pk_bf16_f32 v148, v164, v165\n"
"v_cvt_pk_bf16_f32 v149, v166, v167\n"
"v_exp_f32 v171, v171\n"
"v_exp_f32 v172, v172\n"
"v_exp_f32 v174, v174\n"
"v_exp_f32 v178, v178\n"
"v_exp_f32 v152, v152\n"
"v_mfma_f32_16x16x32_bf16 v[160:163], v[208:211], a[32:35], v[188:191]\n"
"ds_read_b128 a[132:135], v249 offset:32768\n"
"v_cvt_pk_bf16_f32 v114, v116, v117\n"
"v_mfma_f32_16x16x32_bf16 v[160:163], v[212:215], a[36:39], v[160:163]\n"
"ds_read_b128 a[100:103], v248 offset:49152\n"
"v_cvt_pk_bf16_f32 v143, v174, v175\n"
"v_mfma_f32_16x16x32_bf16 v[160:163], v[216:219], a[40:43], v[160:163]\n"
"ds_read_b128 a[116:119], v248 offset:51200\n"
"v_cvt_pk_bf16_f32 v150, v176, v177\n"
"v_mfma_f32_16x16x32_bf16 v[160:163], v[220:223], a[44:47], v[160:163]\n"
"v_mov_b32 v201, 0\n"
"v_cvt_pk_bf16_f32 v134, v168, v169\n"
"v_mfma_f32_16x16x32_bf16 v[164:167], v[216:219], a[200:203], v[192:195]\n"
"ds_read_b64_tr_b16 a[254:255], v235 offset:4096\n"
"v_accvgpr_write_b32 a185, v41\n"
"v_mfma_f32_16x16x32_bf16 v[164:167], v[220:223], a[204:207], v[164:167]\n"
"ds_read_b64_tr_b16 a[252:253], v235 offset:0\n"
"ds_read_b128 a[164:167], v249 offset:49152\n"
"v_mfma_f32_16x16x32_bf16 v[164:167], v[208:211], a[192:195], v[164:167]\n"
"ds_read_b64_tr_b16 v[224:225], v239 offset:0\n"
"v_accvgpr_write_b32 a186, v42\n"
"v_mfma_f32_16x16x32_bf16 v[164:167], v[212:215], a[196:199], v[164:167]\n"
"s_waitcnt lgkmcnt(7)\n"
"ds_read_b128 v[220:223], v229 offset:21504\n"
"v_cvt_pk_bf16_f32 v3, v6, v7\n"
"v_mov_b32 v197, 0\n"
"v_cvt_pk_bf16_f32 v135, v170, v171\n"
"v_exp_f32 v179, v179\n"
"v_exp_f32 v154, v154\n"
"v_exp_f32 v156, v156\n"
"v_exp_f32 v160, v160\n"
"v_mfma_f32_16x16x32_bf16 a[216:219], v[124:127], a[244:247], 0\n"
"ds_read_b128 a[156:159], v249 offset:35840\n"
"v_accvgpr_write_b32 a152, v8\n"
"v_mfma_f32_16x16x32_bf16 v[8:11], v[128:131], a[244:247], 0\n"
"ds_read_b128 a[108:111], v248 offset:50176\n"
"v_accvgpr_write_b32 a184, v40\n"
"v_mfma_f32_16x16x32_bf16 v[8:11], v[132:135], a[248:251], v[8:11]\n"
"ds_read_b128 a[180:183], v249 offset:51200\n"
"v_cvt_pk_bf16_f32 v142, v172, v173\n"
"v_mfma_f32_16x16x32_bf16 v[40:43], v[136:139], a[244:247], 0\n"
"ds_read_b128 a[124:127], v248 offset:52224\n"
"v_bfe_u32 v248, v242, 8, 2\n"
"v_mfma_f32_16x16x32_bf16 v[40:43], v[140:143], a[248:251], v[40:43]\n"
"ds_read_b128 a[188:191], v249 offset:52224\n"
"v_cvt_pk_bf16_f32 v151, v178, v179\n"
"v_mfma_f32_16x16x32_bf16 v[72:75], v[144:147], a[244:247], 0\n"
"v_permlane16_swap_b32 v113, v115\n"
"v_mfma_f32_16x16x32_bf16 v[72:75], v[148:151], a[248:251], v[72:75]\n"
"s_waitcnt lgkmcnt(6)\n"
"ds_read_b128 v[208:211], v228 offset:20480\n"
"ds_read_b64_tr_b16 a[250:251], v238 offset:5120\n"
"v_accvgpr_write_b32 a97, v113\n"
"v_mov_b32 v202, 0\n"
"v_exp_f32 v153, v153\n"
"v_exp_f32 v155, v155\n"
"v_exp_f32 v162, v162\n"
"v_exp_f32 v166, v166\n"
"v_mfma_f32_16x16x32_bf16 v[76:79], v[148:151], v[224:227], 0\n"
"v_cvt_pk_bf16_f32 v17, v18, v19\n"
"v_mov_b32 v198, 0\n"
"v_mfma_f32_16x16x32_bf16 v[76:79], v[144:147], a[252:255], v[76:79]\n"
"ds_read_b128 v[216:219], v228 offset:21504\n"
"v_accvgpr_write_b32 a104, v120\n"
"v_mfma_f32_16x16x32_bf16 v[44:47], v[140:143], v[224:227], 0\n"
"v_cvt_pk_bf16_f32 v2, v4, v5\n"
"v_cvt_pk_bf16_f32 v35, v38, v39\n"
"v_mfma_f32_16x16x32_bf16 v[44:47], v[136:139], a[252:255], v[44:47]\n"
"ds_read_b128 v[212:215], v229 offset:20480\n"
"v_permlane16_swap_b32 v112, v114\n"
"v_permlane16_swap_b32 v1, v3\n"
"v_permlane16_swap_b32 v33, v35\n"
"v_exp_f32 v157, v157\n"
"v_exp_f32 v158, v158\n"
"v_mfma_f32_16x16x32_bf16 v[12:15], v[132:135], v[224:227], 0\n"
"ds_read_b64_tr_b16 a[244:245], v234 offset:1024\n"
"v_cvt_pk_bf16_f32 v96, v152, v153\n"
"v_mfma_f32_16x16x32_bf16 v[12:15], v[128:131], a[252:255], v[12:15]\n"
"v_exp_f32 v159, v159\n"
"v_mfma_f32_16x16x32_bf16 a[220:223], v[124:127], a[252:255], 0\n"
"s_waitcnt lgkmcnt(5)\n"
"v_exp_f32 v165, v165\n"
"v_smfmac_f32_16x16x64_bf16 v[196:199], a[240:243], v[128:135], v241\n"
"s_add_u32 m0, s44, 98304\n"
"ds_read_b64_tr_b16 a[246:247], v234 offset:5120\n"
"ds_read_b64_tr_b16 a[248:249], v238 offset:1024\n"
"v_mfma_f32_16x16x32_bf16 v[202:205], a[240:243], v[124:127], v[202:205]\n"
"s_waitcnt lgkmcnt(4)\n"
"buffer_load_dwordx4 v240, s[20:23], s56 offen nt lds\n"
"v_mfma_f32_16x16x32_bf16 v[168:171], v[216:219], a[8:11], v[180:183]\n"
"v_permlane16_swap_b32 v25, v27\n"
"v_mfma_f32_16x16x32_bf16 v[168:171], v[220:223], a[12:15], v[168:171]\n"
"v_accvgpr_write_b32 a99, v115\n"
"v_cvt_pk_bf16_f32 v97, v154, v155\n"
"v_mfma_f32_16x16x32_bf16 v[168:171], v[208:211], a[0:3], v[168:171]\n"
"s_waitcnt lgkmcnt(3)\n"
"v_exp_f32 v163, v163\n"
"v_mfma_f32_16x16x32_bf16 v[168:171], v[212:215], a[4:7], v[168:171]\n"
"v_cvt_pk_bf16_f32 v18, v20, v21\n"
"v_accvgpr_write_b32 a171, v27\n"
"v_mfma_f32_16x16x32_bf16 v[172:175], v[208:211], a[16:19], v[184:187]\n"
"ds_read_b64_tr_b16 a[254:255], v235 offset:5120\n"
"v_cvt_pk_bf16_f32 v113, v162, v163\n"
"v_mfma_f32_16x16x32_bf16 v[172:175], v[212:215], a[20:23], v[172:175]\n"
"ds_read_b64_tr_b16 v[224:225], v239 offset:1024\n"
"v_cvt_pk_bf16_f32 v19, v22, v23\n"
"v_mfma_f32_16x16x32_bf16 v[172:175], v[216:219], a[24:27], v[172:175]\n"
"s_add_u32 s40, s56, s52\n"
"v_exp_f32 v167, v167\n"
"v_mfma_f32_16x16x32_bf16 v[172:175], v[220:223], a[28:31], v[172:175]\n"
"buffer_load_dwordx4 v240, s[20:23], s40 offen offset:1024 nt lds\n"
"v_mfma_f32_16x16x32_bf16 v[176:179], v[216:219], a[40:43], v[188:191]\n"
"v_accvgpr_write_b32 a145, v1\n"
"v_accvgpr_write_b32 a168, v24\n"
"v_mfma_f32_16x16x32_bf16 v[176:179], v[220:223], a[44:47], v[176:179]\n"
"v_exp_f32 v171, v171\n"
"v_mfma_f32_16x16x32_bf16 v[176:179], v[208:211], a[32:35], v[176:179]\n"
"v_exp_f32 v169, v169\n"
"v_mfma_f32_16x16x32_bf16 v[176:179], v[212:215], a[36:39], v[176:179]\n"
"v_exp_f32 v170, v170\n"
"v_mfma_f32_16x16x32_bf16 v[152:155], v[208:211], a[192:195], v[192:195]\n"
"ds_read_b64_tr_b16 a[252:253], v235 offset:1024\n"
"ds_read_b64_tr_b16 v[226:227], v239 offset:5120\n"
"v_mfma_f32_16x16x32_bf16 v[152:155], v[212:215], a[196:199], v[152:155]\n"
"v_mov_b32 v200, 0\n"
"v_cvt_pk_bf16_f32 v104, v156, v157\n"
"v_mfma_f32_16x16x32_bf16 v[152:155], v[216:219], a[200:203], v[152:155]\n"
"ds_read_b128 v[208:211], v230 offset:16384\n"
"v_cvt_pk_bf16_f32 v105, v158, v159\n"
"v_mfma_f32_16x16x32_bf16 v[152:155], v[220:223], a[204:207], v[152:155]\n"
"s_waitcnt lgkmcnt(3)\n"
"v_permlane16_swap_b32 v0, v2\n"
"v_mfma_f32_16x16x32_bf16 a[232:235], v[124:127], a[244:247], 0\n"
"ds_read_b128 v[212:215], v231 offset:16384\n"
"v_accvgpr_write_b32 a169, v25\n"
"v_mfma_f32_16x16x32_bf16 v[24:27], v[128:131], a[244:247], 0\n"
"s_add_u32 s40, s56, s53\n"
"ds_read_b128 v[220:223], v231 offset:17408\n"
"v_accvgpr_write_b32 a146, v2\n"
"v_mfma_f32_16x16x32_bf16 v[24:27], v[132:135], a[248:251], v[24:27]\n"
"buffer_load_dwordx4 v240, s[20:23], s40 offen offset:2048 nt lds\n"
"v_mfma_f32_16x16x32_bf16 v[56:59], v[136:139], a[244:247], 0\n"
"v_exp_f32 v164, v164\n"
"v_mfma_f32_16x16x32_bf16 v[56:59], v[140:143], a[248:251], v[56:59]\n"
"v_accvgpr_write_b32 a96, v112\n"
"v_accvgpr_write_b32 a179, v35\n"
"v_mfma_f32_16x16x32_bf16 v[88:91], v[144:147], a[244:247], 0\n"
"v_exp_f32 v179, v179\n"
"v_mfma_f32_16x16x32_bf16 v[88:91], v[148:151], a[248:251], v[88:91]\n"
"s_waitcnt lgkmcnt(2)\n"
"v_permlane16_swap_b32 v16, v18\n"
"v_mfma_f32_16x16x32_bf16 v[92:95], v[148:151], v[224:227], 0\n"
"s_add_u32 s40, s56, s54\n"
"ds_read_b128 v[216:219], v230 offset:17408\n"
"v_cvt_pk_bf16_f32 v99, v170, v171\n"
"v_mfma_f32_16x16x32_bf16 v[92:95], v[144:147], a[252:255], v[92:95]\n"
"buffer_load_dwordx4 v240, s[20:23], s40 offen offset:3072 nt lds\n"
"v_mfma_f32_16x16x32_bf16 v[60:63], v[140:143], v[224:227], 0\n"
"s_add_u32 s55, s55, s59\n"
"v_exp_f32 v161, v161\n"
"v_mfma_f32_16x16x32_bf16 v[60:63], v[136:139], a[252:255], v[60:63]\n"
"s_add_u32 s41, s55, s57\n"
"v_permlane16_swap_b32 v121, v123\n"
"v_mfma_f32_16x16x32_bf16 v[28:31], v[132:135], v[224:227], 0\n"
"s_and_b32 s41, s41, s58\n"
"v_permlane16_swap_b32 v32, v34\n"
"v_mfma_f32_16x16x32_bf16 v[28:31], v[128:131], a[252:255], v[28:31]\n"
"s_andn2_b32 s56, s55, s58\n"
"v_exp_f32 v176, v176\n"
"v_mfma_f32_16x16x32_bf16 a[236:239], v[124:127], a[252:255], 0\n"
"s_or_b32 s56, s56, s41\n"
"ds_read_b64_tr_b16 a[250:251], v236 offset:4096\n"
"ds_read_b64_tr_b16 a[252:253], v233 offset:0\n"
"v_smfmac_f32_16x16x64_bf16 v[198:201], a[240:243], v[136:143], v241\n"
"ds_read_b64_tr_b16 a[254:255], v233 offset:4096\n"
"v_accvgpr_write_b32 a176, v32\n"
"v_mfma_f32_16x16x32_bf16 v[156:159], v[208:211], a[0:3], v[180:183]\n"
"s_waitcnt lgkmcnt(3)\n"
"ds_read_b64_tr_b16 a[244:245], v232 offset:0\n"
"ds_read_b64_tr_b16 a[248:249], v236 offset:0\n"
"v_mfma_f32_16x16x32_bf16 v[156:159], v[212:215], a[4:7], v[156:159]\n"
"v_cvt_pk_bf16_f32 v112, v160, v161\n"
"v_cvt_pk_bf16_f32 v120, v164, v165\n"
"v_mfma_f32_16x16x32_bf16 v[156:159], v[216:219], a[8:11], v[156:159]\n"
"s_add_u32 m0, s44, 49152\n"
"ds_read_b64_tr_b16 a[246:247], v232 offset:4096\n"
"ds_read_b64_tr_b16 v[224:225], v237 offset:0\n"
"v_mfma_f32_16x16x32_bf16 v[156:159], v[220:223], a[12:15], v[156:159]\n"
"buffer_load_dwordx4 v240, s[16:19], s56 offen nt lds\n"
"v_mfma_f32_16x16x32_bf16 v[160:163], v[216:219], a[24:27], v[184:187]\n"
"v_accvgpr_write_b32 a105, v121\n"
"v_accvgpr_write_b32 a144, v0\n"
"v_mfma_f32_16x16x32_bf16 v[160:163], v[220:223], a[28:31], v[160:163]\n"
"v_accvgpr_write_b32 a178, v34\n"
"v_cvt_pk_bf16_f32 v121, v166, v167\n"
"v_mfma_f32_16x16x32_bf16 v[160:163], v[208:211], a[16:19], v[160:163]\n"
"ds_read_b64_tr_b16 v[226:227], v237 offset:4096\n"
"v_accvgpr_write_b32 a98, v114\n"
"v_mfma_f32_16x16x32_bf16 v[160:163], v[212:215], a[20:23], v[160:163]\n"
"v_exp_f32 v172, v172\n"
"v_mfma_f32_16x16x32_bf16 v[164:167], v[208:211], a[32:35], v[188:191]\n"
"ds_read_b128 v[208:211], v230 offset:20480\n"
"v_accvgpr_write_b32 a147, v3\n"
"v_mfma_f32_16x16x32_bf16 v[164:167], v[212:215], a[36:39], v[164:167]\n"
"v_exp_f32 v168, v168\n"
"v_mfma_f32_16x16x32_bf16 v[164:167], v[216:219], a[40:43], v[164:167]\n"
"v_exp_f32 v174, v174\n"
"v_mfma_f32_16x16x32_bf16 v[164:167], v[220:223], a[44:47], v[164:167]\n"
"s_waitcnt lgkmcnt(2)\n"
"ds_read_b128 v[216:219], v230 offset:21504\n"
"v_accvgpr_write_b32 a177, v33\n"
"v_mfma_f32_16x16x32_bf16 a[208:211], v[124:127], a[244:247], 0\n"
"v_exp_f32 v177, v177\n"
"v_mfma_f32_16x16x32_bf16 v[0:3], v[128:131], a[244:247], 0\n"
"s_add_u32 s40, s56, s52\n"
"v_exp_f32 v154, v154\n"
"v_mfma_f32_16x16x32_bf16 v[0:3], v[132:135], a[248:251], v[0:3]\n"
"buffer_load_dwordx4 v240, s[16:19], s40 offen offset:1024 nt lds\n"
"v_mfma_f32_16x16x32_bf16 v[32:35], v[136:139], a[244:247], 0\n"
"v_exp_f32 v178, v178\n"
"v_mfma_f32_16x16x32_bf16 v[32:35], v[140:143], a[248:251], v[32:35]\n"
"v_permlane16_swap_b32 v17, v19\n"
"v_mfma_f32_16x16x32_bf16 v[64:67], v[144:147], a[244:247], 0\n"
"ds_read_b128 v[212:215], v231 offset:20480\n"
"v_cvt_pk_bf16_f32 v98, v168, v169\n"
"v_mfma_f32_16x16x32_bf16 v[64:67], v[148:151], a[248:251], v[64:67]\n"
"s_waitcnt lgkmcnt(2)\n"
"v_exp_f32 v162, v162\n"
"v_mfma_f32_16x16x32_bf16 v[68:71], v[148:151], v[224:227], 0\n"
"s_add_u32 s40, s56, s53\n"
"ds_read_b128 v[220:223], v231 offset:21504\n"
"v_bfe_u32 v254, v242, 6, 4\n"
"v_mfma_f32_16x16x32_bf16 v[68:71], v[144:147], a[252:255], v[68:71]\n"
"buffer_load_dwordx4 v240, s[16:19], s40 offen offset:2048 nt lds\n"
"v_mfma_f32_16x16x32_bf16 v[36:39], v[140:143], v[224:227], 0\n"
"v_exp_f32 v156, v156\n"
"v_mfma_f32_16x16x32_bf16 v[36:39], v[136:139], a[252:255], v[36:39]\n"
"v_exp_f32 v163, v163\n"
"v_mfma_f32_16x16x32_bf16 v[4:7], v[132:135], v[224:227], 0\n"
"v_exp_f32 v173, v173\n"
"v_mfma_f32_16x16x32_bf16 v[4:7], v[128:131], a[252:255], v[4:7]\n"
"ds_read_b64_tr_b16 a[250:251], v236 offset:5120\n"
"v_lshlrev_b32 v248, 6, v248\n"
"v_exp_f32 v175, v175\n"
"v_cvt_pk_bf16_f32 v114, v176, v177\n"
"v_cvt_pk_bf16_f32 v115, v178, v179\n"
"v_exp_f32 v152, v152\n"
"v_exp_f32 v155, v155\n"
"v_exp_f32 v157, v157\n"
"v_exp_f32 v161, v161\n"
"v_mfma_f32_16x16x32_bf16 a[212:215], v[124:127], a[252:255], 0\n"
"v_exp_f32 v160, v160\n"
"v_smfmac_f32_16x16x64_bf16 v[200:203], a[240:243], v[144:151], v241\n"
"ds_read_b64_tr_b16 a[246:247], v232 offset:5120\n"
"v_cvt_pk_bf16_f32 v107, v174, v175\n"
"v_mfma_f32_16x16x32_bf16 v[168:171], v[208:211], a[0:3], v[180:183]\n"
"s_waitcnt lgkmcnt(2)\n"
"ds_read_b64_tr_b16 a[244:245], v232 offset:1024\n"
"v_cvt_pk_bf16_f32 v106, v172, v173\n"
"v_mfma_f32_16x16x32_bf16 v[168:171], v[212:215], a[4:7], v[168:171]\n"
"v_accvgpr_write_b32 a163, v19\n"
"v_bfe_u32 v249, v242, 4, 2\n"
"v_mfma_f32_16x16x32_bf16 v[168:171], v[216:219], a[8:11], v[168:171]\n"
"s_add_u32 s40, s56, s54\n"
"ds_read_b64_tr_b16 a[248:249], v236 offset:1024\n"
"v_accvgpr_write_b32 a162, v18\n"
"v_mfma_f32_16x16x32_bf16 v[168:171], v[220:223], a[12:15], v[168:171]\n"
"buffer_load_dwordx4 v240, s[16:19], s40 offen offset:3072 nt lds\n"
"v_mfma_f32_16x16x32_bf16 v[172:175], v[216:219], a[24:27], v[184:187]\n"
"v_exp_f32 v158, v158\n"
"v_mfma_f32_16x16x32_bf16 v[172:175], v[220:223], a[28:31], v[172:175]\n"
"v_exp_f32 v164, v164\n"
"v_mfma_f32_16x16x32_bf16 v[172:175], v[208:211], a[16:19], v[172:175]\n"
"ds_read_b64_tr_b16 a[254:255], v233 offset:5120\n"
"v_accvgpr_write_b32 a161, v17\n"
"v_mfma_f32_16x16x32_bf16 v[172:175], v[212:215], a[20:23], v[172:175]\n"
"s_cbranch_vccnz LPCE1_%=\nLPKE1_%=:\n"
"v_cvt_pk_bf16_f32 v100, v156, v157\n"
"v_mfma_f32_16x16x32_bf16 v[176:179], v[208:211], a[32:35], v[188:191]\n"
"ds_read_b64_tr_b16 a[252:253], v233 offset:1024\n"
"v_accvgpr_write_b32 a160, v16\n"
"v_mfma_f32_16x16x32_bf16 v[176:179], v[212:215], a[36:39], v[176:179]\n"
"ds_read_b64_tr_b16 v[224:225], v237 offset:1024\n"
"ds_read_b64_tr_b16 v[226:227], v237 offset:5120\n"
"v_mfma_f32_16x16x32_bf16 v[176:179], v[216:219], a[40:43], v[176:179]\n"
"v_exp_f32 v159, v159\n"
"v_mfma_f32_16x16x32_bf16 v[176:179], v[220:223], a[44:47], v[176:179]\n"
"s_waitcnt lgkmcnt(4)\n"
"v_accvgpr_write_b32 a107, v123\n"
"v_mul_u32_u24 v249, 20, v249\n"
"v_mfma_f32_16x16x32_bf16 a[224:227], v[124:127], a[244:247], 0\n"
"v_exp_f32 v166, v166\n"
"v_mfma_f32_16x16x32_bf16 v[16:19], v[128:131], a[244:247], 0\n"
"s_waitcnt vmcnt(8)\n"
"v_xor_b32 v249, v249, v254\n"
"v_cvt_pk_bf16_f32 v109, v162, v163\n"
"v_mfma_f32_16x16x32_bf16 v[16:19], v[132:135], a[248:251], v[16:19]\n"
"s_barrier\n"
"ds_read_b128 v[216:219], v228 offset:33792\n"
"ds_read_b128 v[220:223], v229 offset:33792\n"
"v_cvt_pk_bf16_f32 v101, v158, v159\n"
"v_exp_f32 v167, v167\n"
"v_exp_f32 v169, v169\n"
"v_exp_f32 v178, v178\n"
"v_mfma_f32_16x16x32_bf16 v[48:51], v[136:139], a[244:247], 0\n"
"v_exp_f32 v165, v165\n"
"v_mfma_f32_16x16x32_bf16 v[48:51], v[140:143], a[248:251], v[48:51]\n"
"ds_read_b128 v[208:211], v228 offset:32768\n"
"ds_read_b128 v[212:215], v229 offset:32768\n"
"v_mfma_f32_16x16x32_bf16 v[80:83], v[144:147], a[244:247], 0\n"
"ds_read_b64_tr_b16 a[244:245], v234 offset:16384\n"
"ds_read_b64_tr_b16 a[246:247], v234 offset:20480\n"
"v_mfma_f32_16x16x32_bf16 v[80:83], v[148:151], a[248:251], v[80:83]\n"
"s_waitcnt lgkmcnt(6)\n"
"ds_read_b64_tr_b16 a[248:249], v238 offset:16384\n"
"ds_read_b64_tr_b16 a[250:251], v238 offset:20480\n"
"v_mfma_f32_16x16x32_bf16 v[84:87], v[148:151], v[224:227], 0\n"
"v_exp_f32 v173, v173\n"
"v_mfma_f32_16x16x32_bf16 v[84:87], v[144:147], a[252:255], v[84:87]\n"
"v_exp_f32 v175, v175\n"
"v_mfma_f32_16x16x32_bf16 v[52:55], v[140:143], v[224:227], 0\n"
"v_xor_b32 v248, v248, v242\n"
"v_lshl_add_u32 v249, v249, 4, s44\n"
"v_mfma_f32_16x16x32_bf16 v[52:55], v[136:139], a[252:255], v[52:55]\n"
"v_exp_f32 v153, v153\n"
"v_mfma_f32_16x16x32_bf16 v[20:23], v[132:135], v[224:227], 0\n"
"ds_read_b64_tr_b16 v[226:227], v239 offset:20480\n"
"v_cvt_pk_bf16_f32 v117, v166, v167\n"
"v_mfma_f32_16x16x32_bf16 v[20:23], v[128:131], a[252:255], v[20:23]\n"
"v_accvgpr_write_b32 a106, v122\n"
"v_cvt_pk_bf16_f32 v122, v152, v153\n"
"v_mfma_f32_16x16x32_bf16 a[228:231], v[124:127], a[252:255], 0\n"
"s_waitcnt lgkmcnt(3)\n"
"v_cvt_pk_bf16_f32 v123, v154, v155\n"
"s_branch LAY2_%=\n"
"LAL_%=:\n"
"v_mfma_f32_16x16x32_bf16 a[224:227], v[124:127], a[244:247], a[224:227]\n"
"v_exp_f32 v169, v169\n"
"v_mfma_f32_16x16x32_bf16 v[16:19], v[128:131], a[244:247], v[16:19]\n"
"s_waitcnt vmcnt(8)\n"
"v_exp_f32 v175, v175\n"
"v_mfma_f32_16x16x32_bf16 v[16:19], v[132:135], a[248:251], v[16:19]\n"
"s_barrier\n"
"ds_read_b128 v[220:223], v229 offset:1024\n"
"v_mfma_f32_16x16x32_bf16 v[48:51], v[136:139], a[244:247], v[48:51]\n"
"ds_read_b128 v[216:219], v228 offset:1024\n"
"v_cvt_pk_bf16_f32 v109, v162, v163\n"
"v_mfma_f32_16x16x32_bf16 v[48:51], v[140:143], a[248:251], v[48:51]\n"
"ds_read_b128 v[212:215], v229 offset:0\n"
"v_mfma_f32_16x16x32_bf16 v[80:83], v[144:147], a[244:247], v[80:83]\n"
"ds_read_b128 v[208:211], v228 offset:0\n"
"v_cvt_pk_bf16_f32 v101, v158, v159\n"
"v_mfma_f32_16x16x32_bf16 v[80:83], v[148:151], a[248:251], v[80:83]\n"
"s_waitcnt lgkmcnt(4)\n"
"v_exp_f32 v178, v178\n"
"v_mfma_f32_16x16x32_bf16 v[84:87], v[148:151], v[224:227], v[84:87]\n"
"ds_read_b64_tr_b16 a[244:245], v234 offset:49152\n"
"v_cvt_pk_bf16_f32 v117, v166, v167\n"
"v_mfma_f32_16x16x32_bf16 v[84:87], v[144:147], a[252:255], v[84:87]\n"
"ds_read_b64_tr_b16 a[250:251], v238 offset:53248\n"
"v_cvt_pk_bf16_f32 v123, v154, v155\n"
"v_mfma_f32_16x16x32_bf16 v[52:55], v[140:143], v[224:227], v[52:55]\n"
"v_exp_f32 v173, v173\n"
"v_mfma_f32_16x16x32_bf16 v[52:55], v[136:139], a[252:255], v[52:55]\n"
"ds_read_b64_tr_b16 a[248:249], v238 offset:49152\n"
"v_mfma_f32_16x16x32_bf16 v[20:23], v[132:135], v[224:227], v[20:23]\n"
"v_exp_f32 v156, v156\n"
"v_mfma_f32_16x16x32_bf16 v[20:23], v[128:131], a[252:255], v[20:23]\n"
"ds_read_b64_tr_b16 v[226:227], v239 offset:53248\n"
"v_cvt_pk_bf16_f32 v100, v156, v157\n"
"v_mfma_f32_16x16x32_bf16 a[228:231], v[124:127], a[252:255], a[228:231]\n"
"s_waitcnt lgkmcnt(4)\n"
"ds_read_b64_tr_b16 a[246:247], v234 offset:53248\n"
"v_mfma_f32_16x16x32_bf16 v[152:155], v[208:211], a[0:3], v[180:183]\n"
"ds_read_b64_tr_b16 v[224:225], v239 offset:49152\n"
"v_cvt_pk_bf16_f32 v116, v164, v165\n"
"v_mfma_f32_16x16x32_bf16 v[152:155], v[212:215], a[4:7], v[152:155]\n"
"ds_read_b64_tr_b16 a[252:253], v235 offset:49152\n"
"v_cvt_pk_bf16_f32 v108, v160, v161\n"
"v_mfma_f32_16x16x32_bf16 v[152:155], v[216:219], a[8:11], v[152:155]\n"
"s_add_u32 m0, s44, 81920\n"
"ds_read_b64_tr_b16 a[254:255], v235 offset:53248\n"
"v_mfma_f32_16x16x32_bf16 v[152:155], v[220:223], a[12:15], v[152:155]\n"
"buffer_load_dwordx4 v240, s[20:23], s56 offen nt lds\n"
"v_mfma_f32_16x16x32_bf16 v[156:159], v[216:219], a[24:27], v[184:187]\n"
"v_exp_f32 v177, v177\n"
"v_mfma_f32_16x16x32_bf16 v[156:159], v[220:223], a[28:31], v[156:159]\n"
"v_exp_f32 v168, v168\n"
"v_mfma_f32_16x16x32_bf16 v[156:159], v[208:211], a[16:19], v[156:159]\n"
"v_exp_f32 v179, v179\n"
"v_mfma_f32_16x16x32_bf16 v[156:159], v[212:215], a[20:23], v[156:159]\n"
"v_exp_f32 v174, v174\n"
"v_mfma_f32_16x16x32_bf16 v[160:163], v[208:211], a[32:35], v[188:191]\n"
"v_exp_f32 v172, v172\n"
"v_mfma_f32_16x16x32_bf16 v[160:163], v[212:215], a[36:39], v[160:163]\n"
"v_exp_f32 v171, v171\n"
"v_mfma_f32_16x16x32_bf16 v[160:163], v[216:219], a[40:43], v[160:163]\n"
"v_exp_f32 v154, v154\n"
"v_mfma_f32_16x16x32_bf16 v[160:163], v[220:223], a[44:47], v[160:163]\n"
"v_exp_f32 v170, v170\n"
"v_mfma_f32_16x16x32_bf16 v[164:167], v[216:219], a[200:203], v[192:195]\n"
"ds_read_b128 v[216:219], v228 offset:5120\n"
"v_cvt_pk_bf16_f32 v103, v170, v171\n"
"v_mfma_f32_16x16x32_bf16 v[164:167], v[220:223], a[204:207], v[164:167]\n"
"v_cvt_pk_bf16_f32 v102, v168, v169\n"
"v_cvt_pk_bf16_f32 v119, v178, v179\n"
"v_mfma_f32_16x16x32_bf16 v[164:167], v[208:211], a[192:195], v[164:167]\n"
"s_add_u32 s40, s56, s52\n"
"ds_read_b128 v[208:211], v228 offset:4096\n"
"v_mfma_f32_16x16x32_bf16 v[164:167], v[212:215], a[196:199], v[164:167]\n"
"s_waitcnt lgkmcnt(2)\n"
"buffer_load_dwordx4 v240, s[20:23], s40 offen offset:1024 nt lds\n"
"v_mfma_f32_16x16x32_bf16 a[216:219], v[120:123], a[244:247], a[216:219]\n"
"v_cvt_pk_bf16_f32 v110, v172, v173\n"
"v_cvt_pk_bf16_f32 v111, v174, v175\n"
"v_mfma_f32_16x16x32_bf16 v[8:11], v[96:99], a[244:247], v[8:11]\n"
"v_exp_f32 v176, v176\n"
"v_mfma_f32_16x16x32_bf16 v[8:11], v[100:103], a[248:251], v[8:11]\n"
"v_exp_f32 v155, v155\n"
"v_mfma_f32_16x16x32_bf16 v[40:43], v[104:107], a[244:247], v[40:43]\n"
"v_exp_f32 v152, v152\n"
"v_mfma_f32_16x16x32_bf16 v[40:43], v[108:111], a[248:251], v[40:43]\n"
"ds_read_b128 v[212:215], v229 offset:4096\n"
"v_cvt_pk_bf16_f32 v118, v176, v177\n"
"v_mfma_f32_16x16x32_bf16 v[72:75], v[112:115], a[244:247], v[72:75]\n"
"v_exp_f32 v158, v158\n"
"v_mfma_f32_16x16x32_bf16 v[72:75], v[116:119], a[248:251], v[72:75]\n"
"v_exp_f32 v163, v163\n"
"v_mfma_f32_16x16x32_bf16 v[76:79], v[116:119], v[224:227], v[76:79]\n"
"ds_read_b128 v[220:223], v229 offset:5120\n"
"v_mfma_f32_16x16x32_bf16 v[76:79], v[112:115], a[252:255], v[76:79]\n"
"v_exp_f32 v159, v159\n"
"v_mfma_f32_16x16x32_bf16 v[44:47], v[108:111], v[224:227], v[44:47]\n"
"s_add_u32 s40, s56, s53\n"
"ds_read_b64_tr_b16 a[244:245], v234 offset:50176\n"
"v_mfma_f32_16x16x32_bf16 v[44:47], v[104:107], a[252:255], v[44:47]\n"
"buffer_load_dwordx4 v240, s[20:23], s40 offen offset:2048 nt lds\n"
"v_mfma_f32_16x16x32_bf16 v[12:15], v[100:103], v[224:227], v[12:15]\n"
"v_exp_f32 v153, v153\n"
"v_mfma_f32_16x16x32_bf16 v[12:15], v[96:99], a[252:255], v[12:15]\n"
"v_exp_f32 v165, v165\n"
"v_mfma_f32_16x16x32_bf16 a[220:223], v[120:123], a[252:255], a[220:223]\n"
"v_exp_f32 v164, v164\n"
"v_smfmac_f32_16x16x64_bf16 v[196:199], a[240:243], v[96:103], v241\n"
"v_cvt_pk_bf16_f32 v129, v154, v155\n"
"v_mfma_f32_16x16x32_bf16 v[202:205], a[240:243], v[120:123], v[202:205]\n"
"s_waitcnt lgkmcnt(1)\n"
"ds_read_b64_tr_b16 a[254:255], v235 offset:54272\n"
"v_mfma_f32_16x16x32_bf16 v[168:171], v[216:219], a[8:11], v[180:183]\n"
"ds_read_b64_tr_b16 a[248:249], v238 offset:50176\n"
"v_mfma_f32_16x16x32_bf16 v[168:171], v[220:223], a[12:15], v[168:171]\n"
"ds_read_b64_tr_b16 a[246:247], v234 offset:54272\n"
"v_mfma_f32_16x16x32_bf16 v[168:171], v[208:211], a[0:3], v[168:171]\n"
"ds_read_b64_tr_b16 v[224:225], v239 offset:50176\n"
"v_cvt_pk_bf16_f32 v124, v164, v165\n"
"v_mfma_f32_16x16x32_bf16 v[168:171], v[212:215], a[4:7], v[168:171]\n"
"v_exp_f32 v166, v166\n"
"v_mfma_f32_16x16x32_bf16 v[172:175], v[208:211], a[16:19], v[184:187]\n"
"ds_read_b64_tr_b16 a[252:253], v235 offset:50176\n"
"v_cvt_pk_bf16_f32 v128, v152, v153\n"
"v_mfma_f32_16x16x32_bf16 v[172:175], v[212:215], a[20:23], v[172:175]\n"
"ds_read_b64_tr_b16 v[226:227], v239 offset:54272\n"
"v_cvt_pk_bf16_f32 v137, v158, v159\n"
"v_mfma_f32_16x16x32_bf16 v[172:175], v[216:219], a[24:27], v[172:175]\n"
"s_add_u32 s40, s56, s54\n"
"ds_read_b64_tr_b16 a[250:251], v238 offset:54272\n"
"v_mfma_f32_16x16x32_bf16 v[172:175], v[220:223], a[28:31], v[172:175]\n"
"buffer_load_dwordx4 v240, s[20:23], s40 offen offset:3072 nt lds\n"
"v_mfma_f32_16x16x32_bf16 v[176:179], v[216:219], a[40:43], v[188:191]\n"
"s_add_u32 s55, s55, s59\n"
"v_exp_f32 v167, v167\n"
"v_mfma_f32_16x16x32_bf16 v[176:179], v[220:223], a[44:47], v[176:179]\n"
"s_add_u32 s41, s55, s57\n"
"v_exp_f32 v161, v161\n"
"v_mfma_f32_16x16x32_bf16 v[176:179], v[208:211], a[32:35], v[176:179]\n"
"s_and_b32 s41, s41, s58\n"
"v_exp_f32 v157, v157\n"
"v_mfma_f32_16x16x32_bf16 v[176:179], v[212:215], a[36:39], v[176:179]\n"
"s_andn2_b32 s56, s55, s58\n"
"v_exp_f32 v160, v160\n"
"v_mfma_f32_16x16x32_bf16 v[152:155], v[208:211], a[192:195], v[192:195]\n"
"s_or_b32 s56, s56, s41\n"
"ds_read_b128 v[208:211], v230 offset:0\n"
"v_mfma_f32_16x16x32_bf16 v[152:155], v[212:215], a[196:199], v[152:155]\n"
"v_exp_f32 v172, v172\n"
"v_mfma_f32_16x16x32_bf16 v[152:155], v[216:219], a[200:203], v[152:155]\n"
"ds_read_b128 v[216:219], v230 offset:1024\n"
"v_mfma_f32_16x16x32_bf16 v[152:155], v[220:223], a[204:207], v[152:155]\n"
"s_waitcnt lgkmcnt(2)\n"
"v_exp_f32 v162, v162\n"
"v_mfma_f32_16x16x32_bf16 a[232:235], v[120:123], a[244:247], a[232:235]\n"
"ds_read_b128 v[212:215], v231 offset:0\n"
"v_mfma_f32_16x16x32_bf16 v[24:27], v[96:99], a[244:247], v[24:27]\n"
"s_add_u32 m0, s44, 32768\n"
"ds_read_b128 v[220:223], v231 offset:1024\n"
"v_mfma_f32_16x16x32_bf16 v[24:27], v[100:103], a[248:251], v[24:27]\n"
"buffer_load_dwordx4 v240, s[16:19], s56 offen nt lds\n"
"v_mfma_f32_16x16x32_bf16 v[56:59], v[104:107], a[244:247], v[56:59]\n"
"v_exp_f32 v171, v171\n"
"v_mfma_f32_16x16x32_bf16 v[56:59], v[108:111], a[248:251], v[56:59]\n"
"v_exp_f32 v169, v169\n"
"v_mfma_f32_16x16x32_bf16 v[88:91], v[112:115], a[244:247], v[88:91]\n"
"v_exp_f32 v154, v154\n"
"v_mfma_f32_16x16x32_bf16 v[88:91], v[116:119], a[248:251], v[88:91]\n"
"v_exp_f32 v156, v156\n"
"v_mfma_f32_16x16x32_bf16 v[92:95], v[116:119], v[224:227], v[92:95]\n"
"ds_read_b64_tr_b16 a[244:245], v232 offset:49152\n"
"v_cvt_pk_bf16_f32 v145, v162, v163\n"
"v_mfma_f32_16x16x32_bf16 v[92:95], v[112:115], a[252:255], v[92:95]\n"
"ds_read_b64_tr_b16 a[250:251], v236 offset:53248\n"
"v_cvt_pk_bf16_f32 v136, v156, v157\n"
"v_mfma_f32_16x16x32_bf16 v[60:63], v[108:111], v[224:227], v[60:63]\n"
"ds_read_b64_tr_b16 a[246:247], v232 offset:53248\n"
"v_cvt_pk_bf16_f32 v144, v160, v161\n"
"v_mfma_f32_16x16x32_bf16 v[60:63], v[104:107], a[252:255], v[60:63]\n"
"v_exp_f32 v152, v152\n"
"v_mfma_f32_16x16x32_bf16 v[28:31], v[100:103], v[224:227], v[28:31]\n"
"ds_read_b64_tr_b16 v[224:225], v237 offset:49152\n"
"ds_read_b64_tr_b16 v[226:227], v237 offset:53248\n"
"v_mfma_f32_16x16x32_bf16 v[28:31], v[96:99], a[252:255], v[28:31]\n"
"ds_read_b64_tr_b16 a[248:249], v236 offset:49152\n"
"v_cvt_pk_bf16_f32 v125, v166, v167\n"
"v_mfma_f32_16x16x32_bf16 a[236:239], v[120:123], a[252:255], a[236:239]\n"
"s_add_u32 s40, s56, s52\n"
"ds_read_b64_tr_b16 a[252:253], v233 offset:49152\n"
"v_smfmac_f32_16x16x64_bf16 v[198:201], a[240:243], v[104:111], v241\n"
"s_waitcnt lgkmcnt(7)\n"
"buffer_load_dwordx4 v240, s[16:19], s40 offen offset:1024 nt lds\n"
"v_mfma_f32_16x16x32_bf16 v[156:159], v[208:211], a[0:3], v[180:183]\n"
"v_exp_f32 v175, v175\n"
"v_mfma_f32_16x16x32_bf16 v[156:159], v[212:215], a[4:7], v[156:159]\n"
"v_exp_f32 v173, v173\n"
"v_mfma_f32_16x16x32_bf16 v[156:159], v[216:219], a[8:11], v[156:159]\n"
"v_exp_f32 v168, v168\n"
"v_mfma_f32_16x16x32_bf16 v[156:159], v[220:223], a[12:15], v[156:159]\n"
"ds_read_b64_tr_b16 a[254:255], v233 offset:53248\n"
"v_mfma_f32_16x16x32_bf16 v[160:163], v[216:219], a[24:27], v[184:187]\n"
"v_exp_f32 v170, v170\n"
"v_mfma_f32_16x16x32_bf16 v[160:163], v[220:223], a[28:31], v[160:163]\n"
"v_exp_f32 v178, v178\n"
"v_mfma_f32_16x16x32_bf16 v[160:163], v[208:211], a[16:19], v[160:163]\n"
"v_exp_f32 v177, v177\n"
"v_mfma_f32_16x16x32_bf16 v[160:163], v[212:215], a[20:23], v[160:163]\n"
"v_exp_f32 v176, v176\n"
"v_mfma_f32_16x16x32_bf16 v[164:167], v[208:211], a[32:35], v[188:191]\n"
"ds_read_b128 v[208:211], v230 offset:4096\n"
"v_cvt_pk_bf16_f32 v131, v170, v171\n"
"v_mfma_f32_16x16x32_bf16 v[164:167], v[212:215], a[36:39], v[164:167]\n"
"ds_read_b128 v[212:215], v231 offset:4096\n"
"v_mfma_f32_16x16x32_bf16 v[164:167], v[216:219], a[40:43], v[164:167]\n"
"v_exp_f32 v153, v153\n"
"v_mfma_f32_16x16x32_bf16 v[164:167], v[220:223], a[44:47], v[164:167]\n"
"s_waitcnt lgkmcnt(2)\n"
"ds_read_b128 v[220:223], v231 offset:5120\n"
"v_cvt_pk_bf16_f32 v138, v172, v173\n"
"v_mfma_f32_16x16x32_bf16 a[208:211], v[120:123], a[244:247], a[208:211]\n"
"v_exp_f32 v174, v174\n"
"v_mfma_f32_16x16x32_bf16 v[0:3], v[96:99], a[244:247], v[0:3]\n"
"s_add_u32 s40, s56, s53\n"
"ds_read_b128 v[216:219], v230 offset:5120\n"
"v_mfma_f32_16x16x32_bf16 v[0:3], v[100:103], a[248:251], v[0:3]\n"
"buffer_load_dwordx4 v240, s[16:19], s40 offen offset:2048 nt lds\n"
"v_mfma_f32_16x16x32_bf16 v[32:35], v[104:107], a[244:247], v[32:35]\n"
"v_exp_f32 v163, v163\n"
"v_mfma_f32_16x16x32_bf16 v[32:35], v[108:111], a[248:251], v[32:35]\n"
"v_exp_f32 v161, v161\n"
"v_mfma_f32_16x16x32_bf16 v[64:67], v[112:115], a[244:247], v[64:67]\n"
"v_exp_f32 v155, v155\n"
"v_mfma_f32_16x16x32_bf16 v[64:67], v[116:119], a[248:251], v[64:67]\n"
"v_exp_f32 v166, v166\n"
"v_mfma_f32_16x16x32_bf16 v[68:71], v[116:119], v[224:227], v[68:71]\n"
"ds_read_b64_tr_b16 a[250:251], v236 offset:54272\n"
"v_cvt_pk_bf16_f32 v139, v174, v175\n"
"v_mfma_f32_16x16x32_bf16 v[68:71], v[112:115], a[252:255], v[68:71]\n"
"ds_read_b64_tr_b16 a[246:247], v232 offset:54272\n"
"v_cvt_pk_bf16_f32 v130, v168, v169\n"
"v_mfma_f32_16x16x32_bf16 v[36:39], v[108:111], v[224:227], v[36:39]\n"
"v_exp_f32 v179, v179\n"
"v_mfma_f32_16x16x32_bf16 v[36:39], v[104:107], a[252:255], v[36:39]\n"
"ds_read_b64_tr_b16 a[244:245], v232 offset:50176\n"
"v_cvt_pk_bf16_f32 v126, v152, v153\n"
"v_mfma_f32_16x16x32_bf16 v[4:7], v[100:103], v[224:227], v[4:7]\n"
"s_add_u32 s40, s56, s54\n"
"ds_read_b64_tr_b16 a[248:249], v236 offset:50176\n"
"v_mfma_f32_16x16x32_bf16 v[4:7], v[96:99], a[252:255], v[4:7]\n"
"buffer_load_dwordx4 v240, s[16:19], s40 offen offset:3072 nt lds\n"
"v_mfma_f32_16x16x32_bf16 a[212:215], v[120:123], a[252:255], a[212:215]\n"
"v_exp_f32 v162, v162\n"
"v_smfmac_f32_16x16x64_bf16 v[200:203], a[240:243], v[112:119], v241\n"
"s_waitcnt lgkmcnt(4)\n"
"v_cvt_pk_bf16_f32 v146, v176, v177\n"
"v_mfma_f32_16x16x32_bf16 v[168:171], v[208:211], a[0:3], v[180:183]\n"
"v_exp_f32 v157, v157\n"
"v_mfma_f32_16x16x32_bf16 v[168:171], v[212:215], a[4:7], v[168:171]\n"
"v_exp_f32 v165, v165\n"
"v_mfma_f32_16x16x32_bf16 v[168:171], v[216:219], a[8:11], v[168:171]\n"
"ds_read_b64_tr_b16 a[254:255], v233 offset:54272\n"
"ds_read_b64_tr_b16 v[226:227], v237 offset:54272\n"
"v_mfma_f32_16x16x32_bf16 v[168:171], v[220:223], a[12:15], v[168:171]\n"
"ds_read_b64_tr_b16 a[252:253], v233 offset:50176\n"
"v_cvt_pk_bf16_f32 v147, v178, v179\n"
"v_mfma_f32_16x16x32_bf16 v[172:175], v[216:219], a[24:27], v[184:187]\n"
"ds_read_b64_tr_b16 v[224:225], v237 offset:50176\n"
"v_mfma_f32_16x16x32_bf16 v[172:175], v[220:223], a[28:31], v[172:175]\n"
"v_exp_f32 v160, v160\n"
"v_mfma_f32_16x16x32_bf16 v[172:175], v[208:211], a[16:19], v[172:175]\n"
"v_exp_f32 v158, v158\n"
"v_mfma_f32_16x16x32_bf16 v[172:175], v[212:215], a[20:23], v[172:175]\n"
"s_cbranch_vccnz LPCL0_%=\nLPKL0_%=:\n"
"v_mfma_f32_16x16x32_bf16 v[176:179], v[208:211], a[32:35], v[188:191]\n"
"v_exp_f32 v159, v159\n"
"v_mfma_f32_16x16x32_bf16 v[176:179], v[212:215], a[36:39], v[176:179]\n"
"v_exp_f32 v167, v167\n"
"v_mfma_f32_16x16x32_bf16 v[176:179], v[216:219], a[40:43], v[176:179]\n"
"v_exp_f32 v164, v164\n"
"v_mfma_f32_16x16x32_bf16 v[176:179], v[220:223], a[44:47], v[176:179]\n"
"s_waitcnt lgkmcnt(1)\n"
"v_mfma_f32_16x16x32_bf16 a[224:227], v[120:123], a[244:247], a[224:227]\n"
"v_exp_f32 v169, v169\n"
"v_mfma_f32_16x16x32_bf16 v[16:19], v[96:99], a[244:247], v[16:19]\n"
"s_waitcnt vmcnt(8)\n"
"v_exp_f32 v175, v175\n"
"v_mfma_f32_16x16x32_bf16 v[16:19], v[100:103], a[248:251], v[16:19]\n"
"s_barrier\n"
"ds_read_b128 v[220:223], v229 offset:17408\n"
"v_mfma_f32_16x16x32_bf16 v[48:51], v[104:107], a[244:247], v[48:51]\n"
"ds_read_b128 v[216:219], v228 offset:17408\n"
"v_cvt_pk_bf16_f32 v141, v162, v163\n"
"v_mfma_f32_16x16x32_bf16 v[48:51], v[108:111], a[248:251], v[48:51]\n"
"ds_read_b128 v[212:215], v229 offset:16384\n"
"v_mfma_f32_16x16x32_bf16 v[80:83], v[112:115], a[244:247], v[80:83]\n"
"ds_read_b128 v[208:211], v228 offset:16384\n"
"v_cvt_pk_bf16_f32 v133, v158, v159\n"
"v_mfma_f32_16x16x32_bf16 v[80:83], v[116:119], a[248:251], v[80:83]\n"
"s_waitcnt lgkmcnt(4)\n"
"v_exp_f32 v178, v178\n"
"v_mfma_f32_16x16x32_bf16 v[84:87], v[116:119], v[224:227], v[84:87]\n"
"ds_read_b64_tr_b16 a[244:245], v234 offset:0\n"
"v_cvt_pk_bf16_f32 v149, v166, v167\n"
"v_mfma_f32_16x16x32_bf16 v[84:87], v[112:115], a[252:255], v[84:87]\n"
"ds_read_b64_tr_b16 a[250:251], v238 offset:4096\n"
"v_cvt_pk_bf16_f32 v127, v154, v155\n"
"v_mfma_f32_16x16x32_bf16 v[52:55], v[108:111], v[224:227], v[52:55]\n"
"v_exp_f32 v173, v173\n"
"v_mfma_f32_16x16x32_bf16 v[52:55], v[104:107], a[252:255], v[52:55]\n"
"ds_read_b64_tr_b16 a[248:249], v238 offset:0\n"
"v_mfma_f32_16x16x32_bf16 v[20:23], v[100:103], v[224:227], v[20:23]\n"
"v_exp_f32 v156, v156\n"
"v_mfma_f32_16x16x32_bf16 v[20:23], v[96:99], a[252:255], v[20:23]\n"
"ds_read_b64_tr_b16 v[226:227], v239 offset:4096\n"
"v_cvt_pk_bf16_f32 v132, v156, v157\n"
"v_mfma_f32_16x16x32_bf16 a[228:231], v[120:123], a[252:255], a[228:231]\n"
"s_waitcnt lgkmcnt(4)\n"
"ds_read_b64_tr_b16 a[246:247], v234 offset:4096\n"
"v_mfma_f32_16x16x32_bf16 v[152:155], v[208:211], a[0:3], v[180:183]\n"
"ds_read_b64_tr_b16 v[224:225], v239 offset:0\n"
"v_cvt_pk_bf16_f32 v148, v164, v165\n"
"v_mfma_f32_16x16x32_bf16 v[152:155], v[212:215], a[4:7], v[152:155]\n"
"ds_read_b64_tr_b16 a[252:253], v235 offset:0\n"
"v_cvt_pk_bf16_f32 v140, v160, v161\n"
"v_mfma_f32_16x16x32_bf16 v[152:155], v[216:219], a[8:11], v[152:155]\n"
"s_add_u32 m0, s44, 98304\n"
"ds_read_b64_tr_b16 a[254:255], v235 offset:4096\n"
"v_mfma_f32_16x16x32_bf16 v[152:155], v[220:223], a[12:15], v[152:155]\n"
"buffer_load_dwordx4 v240, s[20:23], s56 offen nt lds\n"
"v_mfma_f32_16x16x32_bf16 v[156:159], v[216:219], a[24:27], v[184:187]\n"
"v_exp_f32 v177, v177\n"
"v_mfma_f32_16x16x32_bf16 v[156:159], v[220:223], a[28:31], v[156:159]\n"
"v_exp_f32 v168, v168\n"
"v_mfma_f32_16x16x32_bf16 v[156:159], v[208:211], a[16:19], v[156:159]\n"
"v_exp_f32 v179, v179\n"
"v_mfma_f32_16x16x32_bf16 v[156:159], v[212:215], a[20:23], v[156:159]\n"
"v_exp_f32 v174, v174\n"
"v_mfma_f32_16x16x32_bf16 v[160:163], v[208:211], a[32:35], v[188:191]\n"
"v_exp_f32 v172, v172\n"
"v_mfma_f32_16x16x32_bf16 v[160:163], v[212:215], a[36:39], v[160:163]\n"
"v_exp_f32 v171, v171\n"
"v_mfma_f32_16x16x32_bf16 v[160:163], v[216:219], a[40:43], v[160:163]\n"
"v_exp_f32 v154, v154\n"
"v_mfma_f32_16x16x32_bf16 v[160:163], v[220:223], a[44:47], v[160:163]\n"
"v_exp_f32 v170, v170\n"
"v_mfma_f32_16x16x32_bf16 v[164:167], v[216:219], a[200:203], v[192:195]\n"
"ds_read_b128 v[216:219], v228 offset:21504\n"
"v_cvt_pk_bf16_f32 v135, v170, v171\n"
"v_mfma_f32_16x16x32_bf16 v[164:167], v[220:223], a[204:207], v[164:167]\n"
"v_cvt_pk_bf16_f32 v134, v168, v169\n"
"v_cvt_pk_bf16_f32 v151, v178, v179\n"
"v_mfma_f32_16x16x32_bf16 v[164:167], v[208:211], a[192:195], v[164:167]\n"
"s_add_u32 s40, s56, s52\n"
"ds_read_b128 v[208:211], v228 offset:20480\n"
"v_mfma_f32_16x16x32_bf16 v[164:167], v[212:215], a[196:199], v[164:167]\n"
"s_waitcnt lgkmcnt(2)\n"
"buffer_load_dwordx4 v240, s[20:23], s40 offen offset:1024 nt lds\n"
"v_mfma_f32_16x16x32_bf16 a[216:219], v[124:127], a[244:247], a[216:219]\n"
"v_cvt_pk_bf16_f32 v142, v172, v173\n"
"v_cvt_pk_bf16_f32 v143, v174, v175\n"
"v_mfma_f32_16x16x32_bf16 v[8:11], v[128:131], a[244:247], v[8:11]\n"
"v_exp_f32 v176, v176\n"
"v_mfma_f32_16x16x32_bf16 v[8:11], v[132:135], a[248:251], v[8:11]\n"
"v_exp_f32 v155, v155\n"
"v_mfma_f32_16x16x32_bf16 v[40:43], v[136:139], a[244:247], v[40:43]\n"
"v_exp_f32 v152, v152\n"
"v_mfma_f32_16x16x32_bf16 v[40:43], v[140:143], a[248:251], v[40:43]\n"
"ds_read_b128 v[212:215], v229 offset:20480\n"
"v_cvt_pk_bf16_f32 v150, v176, v177\n"
"v_mfma_f32_16x16x32_bf16 v[72:75], v[144:147], a[244:247], v[72:75]\n"
"v_exp_f32 v158, v158\n"
"v_mfma_f32_16x16x32_bf16 v[72:75], v[148:151], a[248:251], v[72:75]\n"
"v_exp_f32 v163, v163\n"
"v_mfma_f32_16x16x32_bf16 v[76:79], v[148:151], v[224:227], v[76:79]\n"
"ds_read_b128 v[220:223], v229 offset:21504\n"
"v_mfma_f32_16x16x32_bf16 v[76:79], v[144:147], a[252:255], v[76:79]\n"
"v_exp_f32 v159, v159\n"
"v_mfma_f32_16x16x32_bf16 v[44:47], v[140:143], v[224:227], v[44:47]\n"
"s_add_u32 s40, s56, s53\n"
"ds_read_b64_tr_b16 a[244:245], v234 offset:1024\n"
"v_mfma_f32_16x16x32_bf16 v[44:47], v[136:139], a[252:255], v[44:47]\n"
"buffer_load_dwordx4 v240, s[20:23], s40 offen offset:2048 nt lds\n"
"v_mfma_f32_16x16x32_bf16 v[12:15], v[132:135], v[224:227], v[12:15]\n"
"v_exp_f32 v153, v153\n"
"v_mfma_f32_16x16x32_bf16 v[12:15], v[128:131], a[252:255], v[12:15]\n"
"v_exp_f32 v165, v165\n"
"v_mfma_f32_16x16x32_bf16 a[220:223], v[124:127], a[252:255], a[220:223]\n"
"v_exp_f32 v164, v164\n"
"v_smfmac_f32_16x16x64_bf16 v[196:199], a[240:243], v[128:135], v241\n"
"v_cvt_pk_bf16_f32 v97, v154, v155\n"
"v_mfma_f32_16x16x32_bf16 v[202:205], a[240:243], v[124:127], v[202:205]\n"
"s_waitcnt lgkmcnt(1)\n"
"ds_read_b64_tr_b16 a[254:255], v235 offset:5120\n"
"v_mfma_f32_16x16x32_bf16 v[168:171], v[216:219], a[8:11], v[180:183]\n"
"ds_read_b64_tr_b16 a[248:249], v238 offset:1024\n"
"v_mfma_f32_16x16x32_bf16 v[168:171], v[220:223], a[12:15], v[168:171]\n"
"ds_read_b64_tr_b16 a[246:247], v234 offset:5120\n"
"v_mfma_f32_16x16x32_bf16 v[168:171], v[208:211], a[0:3], v[168:171]\n"
"ds_read_b64_tr_b16 v[224:225], v239 offset:1024\n"
"v_cvt_pk_bf16_f32 v120, v164, v165\n"
"v_mfma_f32_16x16x32_bf16 v[168:171], v[212:215], a[4:7], v[168:171]\n"
"v_exp_f32 v166, v166\n"
"v_mfma_f32_16x16x32_bf16 v[172:175], v[208:211], a[16:19], v[184:187]\n"
"ds_read_b64_tr_b16 a[252:253], v235 offset:1024\n"
"v_cvt_pk_bf16_f32 v96, v152, v153\n"
"v_mfma_f32_16x16x32_bf16 v[172:175], v[212:215], a[20:23], v[172:175]\n"
"ds_read_b64_tr_b16 v[226:227], v239 offset:5120\n"
"v_cvt_pk_bf16_f32 v105, v158, v159\n"
"v_mfma_f32_16x16x32_bf16 v[172:175], v[216:219], a[24:27], v[172:175]\n"
"s_add_u32 s40, s56, s54\n"
"ds_read_b64_tr_b16 a[250:251], v238 offset:5120\n"
"v_mfma_f32_16x16x32_bf16 v[172:175], v[220:223], a[28:31], v[172:175]\n"
"buffer_load_dwordx4 v240, s[20:23], s40 offen offset:3072 nt lds\n"
"v_mfma_f32_16x16x32_bf16 v[176:179], v[216:219], a[40:43], v[188:191]\n"
"s_add_u32 s55, s55, s59\n"
"v_exp_f32 v167, v167\n"
"v_mfma_f32_16x16x32_bf16 v[176:179], v[220:223], a[44:47], v[176:179]\n"
"s_add_u32 s41, s55, s57\n"
"v_exp_f32 v161, v161\n"
"v_mfma_f32_16x16x32_bf16 v[176:179], v[208:211], a[32:35], v[176:179]\n"
"s_and_b32 s41, s41, s58\n"
"v_exp_f32 v157, v157\n"
"v_mfma_f32_16x16x32_bf16 v[176:179], v[212:215], a[36:39], v[176:179]\n"
"s_andn2_b32 s56, s55, s58\n"
"v_exp_f32 v160, v160\n"
"v_mfma_f32_16x16x32_bf16 v[152:155], v[208:211], a[192:195], v[192:195]\n"
"s_or_b32 s56, s56, s41\n"
"ds_read_b128 v[208:211], v230 offset:16384\n"
"v_mfma_f32_16x16x32_bf16 v[152:155], v[212:215], a[196:199], v[152:155]\n"
"v_exp_f32 v172, v172\n"
"v_mfma_f32_16x16x32_bf16 v[152:155], v[216:219], a[200:203], v[152:155]\n"
"ds_read_b128 v[216:219], v230 offset:17408\n"
"v_mfma_f32_16x16x32_bf16 v[152:155], v[220:223], a[204:207], v[152:155]\n"
"s_waitcnt lgkmcnt(2)\n"
"v_exp_f32 v162, v162\n"
"v_mfma_f32_16x16x32_bf16 a[232:235], v[124:127], a[244:247], a[232:235]\n"
"ds_read_b128 v[212:215], v231 offset:16384\n"
"v_mfma_f32_16x16x32_bf16 v[24:27], v[128:131], a[244:247], v[24:27]\n"
"s_add_u32 m0, s44, 49152\n"
"ds_read_b128 v[220:223], v231 offset:17408\n"
"v_mfma_f32_16x16x32_bf16 v[24:27], v[132:135], a[248:251], v[24:27]\n"
"buffer_load_dwordx4 v240, s[16:19], s56 offen nt lds\n"
"v_mfma_f32_16x16x32_bf16 v[56:59], v[136:139], a[244:247], v[56:59]\n"
"v_exp_f32 v171, v171\n"
"v_mfma_f32_16x16x32_bf16 v[56:59], v[140:143], a[248:251], v[56:59]\n"
"v_exp_f32 v169, v169\n"
"v_mfma_f32_16x16x32_bf16 v[88:91], v[144:147], a[244:247], v[88:91]\n"
"v_exp_f32 v154, v154\n"
"v_mfma_f32_16x16x32_bf16 v[88:91], v[148:151], a[248:251], v[88:91]\n"
"v_exp_f32 v156, v156\n"
"v_mfma_f32_16x16x32_bf16 v[92:95], v[148:151], v[224:227], v[92:95]\n"
"ds_read_b64_tr_b16 a[244:245], v232 offset:0\n"
"v_cvt_pk_bf16_f32 v113, v162, v163\n"
"v_mfma_f32_16x16x32_bf16 v[92:95], v[144:147], a[252:255], v[92:95]\n"
"ds_read_b64_tr_b16 a[250:251], v236 offset:4096\n"
"v_cvt_pk_bf16_f32 v104, v156, v157\n"
"v_mfma_f32_16x16x32_bf16 v[60:63], v[140:143], v[224:227], v[60:63]\n"
"ds_read_b64_tr_b16 a[246:247], v232 offset:4096\n"
"v_cvt_pk_bf16_f32 v112, v160, v161\n"
"v_mfma_f32_16x16x32_bf16 v[60:63], v[136:139], a[252:255], v[60:63]\n"
"v_exp_f32 v152, v152\n"
"v_mfma_f32_16x16x32_bf16 v[28:31], v[132:135], v[224:227], v[28:31]\n"
"ds_read_b64_tr_b16 v[224:225], v237 offset:0\n"
"ds_read_b64_tr_b16 v[226:227], v237 offset:4096\n"
"v_mfma_f32_16x16x32_bf16 v[28:31], v[128:131], a[252:255], v[28:31]\n"
"ds_read_b64_tr_b16 a[248:249], v236 offset:0\n"
"v_cvt_pk_bf16_f32 v121, v166, v167\n"
"v_mfma_f32_16x16x32_bf16 a[236:239], v[124:127], a[252:255], a[236:239]\n"
"s_add_u32 s40, s56, s52\n"
"ds_read_b64_tr_b16 a[252:253], v233 offset:0\n"
"v_smfmac_f32_16x16x64_bf16 v[198:201], a[240:243], v[136:143], v241\n"
"s_waitcnt lgkmcnt(7)\n"
"buffer_load_dwordx4 v240, s[16:19], s40 offen offset:1024 nt lds\n"
"v_mfma_f32_16x16x32_bf16 v[156:159], v[208:211], a[0:3], v[180:183]\n"
"v_exp_f32 v175, v175\n"
"v_mfma_f32_16x16x32_bf16 v[156:159], v[212:215], a[4:7], v[156:159]\n"
"v_exp_f32 v173, v173\n"
"v_mfma_f32_16x16x32_bf16 v[156:159], v[216:219], a[8:11], v[156:159]\n"
"v_exp_f32 v168, v168\n"
"v_mfma_f32_16x16x32_bf16 v[156:159], v[220:223], a[12:15], v[156:159]\n"
"ds_read_b64_tr_b16 a[254:255], v233 offset:4096\n"
"v_mfma_f32_16x16x32_bf16 v[160:163], v[216:219], a[24:27], v[184:187]\n"
"v_exp_f32 v170, v170\n"
"v_mfma_f32_16x16x32_bf16 v[160:163], v[220:223], a[28:31], v[160:163]\n"
"v_exp_f32 v178, v178\n"
"v_mfma_f32_16x16x32_bf16 v[160:163], v[208:211], a[16:19], v[160:163]\n"
"v_exp_f32 v177, v177\n"
"v_mfma_f32_16x16x32_bf16 v[160:163], v[212:215], a[20:23], v[160:163]\n"
"v_exp_f32 v176, v176\n"
"v_mfma_f32_16x16x32_bf16 v[164:167], v[208:211], a[32:35], v[188:191]\n"
"ds_read_b128 v[208:211], v230 offset:20480\n"
"v_cvt_pk_bf16_f32 v99, v170, v171\n"
"v_mfma_f32_16x16x32_bf16 v[164:167], v[212:215], a[36:39], v[164:167]\n"
"ds_read_b128 v[212:215], v231 offset:20480\n"
"v_mfma_f32_16x16x32_bf16 v[164:167], v[216:219], a[40:43], v[164:167]\n"
"v_exp_f32 v153, v153\n"
"v_mfma_f32_16x16x32_bf16 v[164:167], v[220:223], a[44:47], v[164:167]\n"
"s_waitcnt lgkmcnt(2)\n"
"ds_read_b128 v[220:223], v231 offset:21504\n"
"v_cvt_pk_bf16_f32 v106, v172, v173\n"
"v_mfma_f32_16x16x32_bf16 a[208:211], v[124:127], a[244:247], a[208:211]\n"
"v_exp_f32 v174, v174\n"
"v_mfma_f32_16x16x32_bf16 v[0:3], v[128:131], a[244:247], v[0:3]\n"
"s_add_u32 s40, s56, s53\n"
"ds_read_b128 v[216:219], v230 offset:21504\n"
"v_mfma_f32_16x16x32_bf16 v[0:3], v[132:135], a[248:251], v[0:3]\n"
"buffer_load_dwordx4 v240, s[16:19], s40 offen offset:2048 nt lds\n"
"v_mfma_f32_16x16x32_bf16 v[32:35], v[136:139], a[244:247], v[32:35]\n"
"v_exp_f32 v163, v163\n"
"v_mfma_f32_16x16x32_bf16 v[32:35], v[140:143], a[248:251], v[32:35]\n"
"v_exp_f32 v161, v161\n"
"v_mfma_f32_16x16x32_bf16 v[64:67], v[144:147], a[244:247], v[64:67]\n"
"v_exp_f32 v155, v155\n"
"v_mfma_f32_16x16x32_bf16 v[64:67], v[148:151], a[248:251], v[64:67]\n"
"v_exp_f32 v166, v166\n"
"v_mfma_f32_16x16x32_bf16 v[68:71], v[148:151], v[224:227], v[68:71]\n"
"ds_read_b64_tr_b16 a[250:251], v236 offset:5120\n"
"v_cvt_pk_bf16_f32 v107, v174, v175\n"
"v_mfma_f32_16x16x32_bf16 v[68:71], v[144:147], a[252:255], v[68:71]\n"
"ds_read_b64_tr_b16 a[246:247], v232 offset:5120\n"
"v_cvt_pk_bf16_f32 v98, v168, v169\n"
"v_mfma_f32_16x16x32_bf16 v[36:39], v[140:143], v[224:227], v[36:39]\n"
"v_exp_f32 v179, v179\n"
"v_mfma_f32_16x16x32_bf16 v[36:39], v[136:139], a[252:255], v[36:39]\n"
"ds_read_b64_tr_b16 a[244:245], v232 offset:1024\n"
"v_cvt_pk_bf16_f32 v122, v152, v153\n"
"v_mfma_f32_16x16x32_bf16 v[4:7], v[132:135], v[224:227], v[4:7]\n"
"s_add_u32 s40, s56, s54\n"
"ds_read_b64_tr_b16 a[248:249], v236 offset:1024\n"
"v_mfma_f32_16x16x32_bf16 v[4:7], v[128:131], a[252:255], v[4:7]\n"
"buffer_load_dwordx4 v240, s[16:19], s40 offen offset:3072 nt lds\n"
"v_mfma_f32_16x16x32_bf16 a[212:215], v[124:127], a[252:255], a[212:215]\n"
"v_exp_f32 v162, v162\n"
"v_smfmac_f32_16x16x64_bf16 v[200:203], a[240:243], v[144:151], v241\n"
"s_waitcnt lgkmcnt(4)\n"
"v_cvt_pk_bf16_f32 v114, v176, v177\n"
"v_mfma_f32_16x16x32_bf16 v[168:171], v[208:211], a[0:3], v[180:183]\n"
"v_exp_f32 v157, v157\n"
"v_mfma_f32_16x16x32_bf16 v[168:171], v[212:215], a[4:7], v[168:171]\n"
"v_exp_f32 v165, v165\n"
"v_mfma_f32_16x16x32_bf16 v[168:171], v[216:219], a[8:11], v[168:171]\n"
"ds_read_b64_tr_b16 a[254:255], v233 offset:5120\n"
"ds_read_b64_tr_b16 v[226:227], v237 offset:5120\n"
"v_mfma_f32_16x16x32_bf16 v[168:171], v[220:223], a[12:15], v[168:171]\n"
"ds_read_b64_tr_b16 a[252:253], v233 offset:1024\n"
"v_cvt_pk_bf16_f32 v115, v178, v179\n"
"v_mfma_f32_16x16x32_bf16 v[172:175], v[216:219], a[24:27], v[184:187]\n"
"ds_read_b64_tr_b16 v[224:225], v237 offset:1024\n"
"v_mfma_f32_16x16x32_bf16 v[172:175], v[220:223], a[28:31], v[172:175]\n"
"v_exp_f32 v160, v160\n"
"v_mfma_f32_16x16x32_bf16 v[172:175], v[208:211], a[16:19], v[172:175]\n"
"v_exp_f32 v158, v158\n"
"v_mfma_f32_16x16x32_bf16 v[172:175], v[212:215], a[20:23], v[172:175]\n"
"s_cbranch_vccnz LPCL1_%=\nLPKL1_%=:\n"
"v_mfma_f32_16x16x32_bf16 v[176:179], v[208:211], a[32:35], v[188:191]\n"
"v_exp_f32 v159, v159\n"
"v_mfma_f32_16x16x32_bf16 v[176:179], v[212:215], a[36:39], v[176:179]\n"
"v_exp_f32 v167, v167\n"
"v_mfma_f32_16x16x32_bf16 v[176:179], v[216:219], a[40:43], v[176:179]\n"
"v_exp_f32 v164, v164\n"
"v_mfma_f32_16x16x32_bf16 v[176:179], v[220:223], a[44:47], v[176:179]\n"
"s_waitcnt lgkmcnt(1)\n"
"v_mfma_f32_16x16x32_bf16 a[224:227], v[124:127], a[244:247], a[224:227]\n"
"v_exp_f32 v169, v169\n"
"v_mfma_f32_16x16x32_bf16 v[16:19], v[128:131], a[244:247], v[16:19]\n"
"s_waitcnt vmcnt(8)\n"
"v_exp_f32 v175, v175\n"
"v_mfma_f32_16x16x32_bf16 v[16:19], v[132:135], a[248:251], v[16:19]\n"
"s_barrier\n"
"ds_read_b128 v[220:223], v229 offset:33792\n"
"v_mfma_f32_16x16x32_bf16 v[48:51], v[136:139], a[244:247], v[48:51]\n"
"ds_read_b128 v[216:219], v228 offset:33792\n"
"v_cvt_pk_bf16_f32 v109, v162, v163\n"
"v_mfma_f32_16x16x32_bf16 v[48:51], v[140:143], a[248:251], v[48:51]\n"
"ds_read_b128 v[212:215], v229 offset:32768\n"
"v_mfma_f32_16x16x32_bf16 v[80:83], v[144:147], a[244:247], v[80:83]\n"
"ds_read_b128 v[208:211], v228 offset:32768\n"
"v_cvt_pk_bf16_f32 v101, v158, v159\n"
"v_mfma_f32_16x16x32_bf16 v[80:83], v[148:151], a[248:251], v[80:83]\n"
"s_waitcnt lgkmcnt(4)\n"
"v_exp_f32 v178, v178\n"
"v_mfma_f32_16x16x32_bf16 v[84:87], v[148:151], v[224:227], v[84:87]\n"
"ds_read_b64_tr_b16 a[244:245], v234 offset:16384\n"
"v_cvt_pk_bf16_f32 v117, v166, v167\n"
"v_mfma_f32_16x16x32_bf16 v[84:87], v[144:147], a[252:255], v[84:87]\n"
"ds_read_b64_tr_b16 a[250:251], v238 offset:20480\n"
"v_cvt_pk_bf16_f32 v123, v154, v155\n"
"v_mfma_f32_16x16x32_bf16 v[52:55], v[140:143], v[224:227], v[52:55]\n"
"v_exp_f32 v173, v173\n"
"v_mfma_f32_16x16x32_bf16 v[52:55], v[136:139], a[252:255], v[52:55]\n"
"ds_read_b64_tr_b16 a[248:249], v238 offset:16384\n"
"v_mfma_f32_16x16x32_bf16 v[20:23], v[132:135], v[224:227], v[20:23]\n"
"v_exp_f32 v156, v156\n"
"v_mfma_f32_16x16x32_bf16 v[20:23], v[128:131], a[252:255], v[20:23]\n"
"ds_read_b64_tr_b16 v[226:227], v239 offset:20480\n"
"v_cvt_pk_bf16_f32 v100, v156, v157\n"
"v_mfma_f32_16x16x32_bf16 a[228:231], v[124:127], a[252:255], a[228:231]\n"
"s_waitcnt lgkmcnt(4)\n"
"ds_read_b64_tr_b16 a[246:247], v234 offset:20480\n"
"LAY2_%=:\n"
"v_mfma_f32_16x16x32_bf16 v[152:155], v[208:211], a[0:3], v[180:183]\n"
"ds_read_b64_tr_b16 v[224:225], v239 offset:16384\n"
"v_cvt_pk_bf16_f32 v116, v164, v165\n"
"v_mfma_f32_16x16x32_bf16 v[152:155], v[212:215], a[4:7], v[152:155]\n"
"ds_read_b64_tr_b16 a[252:253], v235 offset:16384\n"
"v_cvt_pk_bf16_f32 v108, v160, v161\n"
"v_mfma_f32_16x16x32_bf16 v[152:155], v[216:219], a[8:11], v[152:155]\n"
"s_add_u32 m0, s44, 114688\n"
"ds_read_b64_tr_b16 a[254:255], v235 offset:20480\n"
"v_mfma_f32_16x16x32_bf16 v[152:155], v[220:223], a[12:15], v[152:155]\n"
"buffer_load_dwordx4 v240, s[20:23], s56 offen nt lds\n"
"v_mfma_f32_16x16x32_bf16 v[156:159], v[216:219], a[24:27], v[184:187]\n"
"v_exp_f32 v177, v177\n"
"v_mfma_f32_16x16x32_bf16 v[156:159], v[220:223], a[28:31], v[156:159]\n"
"v_exp_f32 v168, v168\n"
"v_mfma_f32_16x16x32_bf16 v[156:159], v[208:211], a[16:19], v[156:159]\n"
"v_exp_f32 v179, v179\n"
"v_mfma_f32_16x16x32_bf16 v[156:159], v[212:215], a[20:23], v[156:159]\n"
"v_exp_f32 v174, v174\n"
"v_mfma_f32_16x16x32_bf16 v[160:163], v[208:211], a[32:35], v[188:191]\n"
"v_exp_f32 v172, v172\n"
"v_mfma_f32_16x16x32_bf16 v[160:163], v[212:215], a[36:39], v[160:163]\n"
"v_exp_f32 v171, v171\n"
"v_mfma_f32_16x16x32_bf16 v[160:163], v[216:219], a[40:43], v[160:163]\n"
"v_exp_f32 v154, v154\n"
"v_mfma_f32_16x16x32_bf16 v[160:163], v[220:223], a[44:47], v[160:163]\n"
"v_exp_f32 v170, v170\n"
"v_mfma_f32_16x16x32_bf16 v[164:167], v[216:219], a[200:203], v[192:195]\n"
"ds_read_b128 v[216:219], v228 offset:37888\n"
"v_cvt_pk_bf16_f32 v103, v170, v171\n"
"v_mfma_f32_16x16x32_bf16 v[164:167], v[220:223], a[204:207], v[164:167]\n"
"v_cvt_pk_bf16_f32 v102, v168, v169\n"
"v_cvt_pk_bf16_f32 v119, v178, v179\n"
"v_mfma_f32_16x16x32_bf16 v[164:167], v[208:211], a[192:195], v[164:167]\n"
"s_add_u32 s40, s56, s52\n"
"ds_read_b128 v[208:211], v228 offset:36864\n"
"v_mfma_f32_16x16x32_bf16 v[164:167], v[212:215], a[196:199], v[164:167]\n"
"s_waitcnt lgkmcnt(2)\n"
"buffer_load_dwordx4 v240, s[20:23], s40 offen offset:1024 nt lds\n"
"v_mfma_f32_16x16x32_bf16 a[216:219], v[120:123], a[244:247], a[216:219]\n"
"v_cvt_pk_bf16_f32 v110, v172, v173\n"
"v_cvt_pk_bf16_f32 v111, v174, v175\n"
"v_mfma_f32_16x16x32_bf16 v[8:11], v[96:99], a[244:247], v[8:11]\n"
"s_waitcnt lgkmcnt(2)\n"
"v_exp_f32 v176, v176\n"
"v_mfma_f32_16x16x32_bf16 v[8:11], v[100:103], a[248:251], v[8:11]\n"
"v_exp_f32 v155, v155\n"
"v_mfma_f32_16x16x32_bf16 v[40:43], v[104:107], a[244:247], v[40:43]\n"
"v_exp_f32 v152, v152\n"
"v_mfma_f32_16x16x32_bf16 v[40:43], v[108:111], a[248:251], v[40:43]\n"
"ds_read_b128 v[212:215], v229 offset:36864\n"
"v_cvt_pk_bf16_f32 v118, v176, v177\n"
"v_mfma_f32_16x16x32_bf16 v[72:75], v[112:115], a[244:247], v[72:75]\n"
"v_exp_f32 v158, v158\n"
"v_mfma_f32_16x16x32_bf16 v[72:75], v[116:119], a[248:251], v[72:75]\n"
"v_exp_f32 v163, v163\n"
"v_mfma_f32_16x16x32_bf16 v[76:79], v[116:119], v[224:227], v[76:79]\n"
"ds_read_b128 v[220:223], v229 offset:37888\n"
"v_mfma_f32_16x16x32_bf16 v[76:79], v[112:115], a[252:255], v[76:79]\n"
"v_exp_f32 v159, v159\n"
"v_mfma_f32_16x16x32_bf16 v[44:47], v[108:111], v[224:227], v[44:47]\n"
"s_add_u32 s40, s56, s53\n"
"ds_read_b64_tr_b16 a[244:245], v234 offset:17408\n"
"v_mfma_f32_16x16x32_bf16 v[44:47], v[104:107], a[252:255], v[44:47]\n"
"buffer_load_dwordx4 v240, s[20:23], s40 offen offset:2048 nt lds\n"
"v_mfma_f32_16x16x32_bf16 v[12:15], v[100:103], v[224:227], v[12:15]\n"
"v_exp_f32 v153, v153\n"
"v_mfma_f32_16x16x32_bf16 v[12:15], v[96:99], a[252:255], v[12:15]\n"
"v_exp_f32 v165, v165\n"
"v_mfma_f32_16x16x32_bf16 a[220:223], v[120:123], a[252:255], a[220:223]\n"
"v_exp_f32 v164, v164\n"
"v_smfmac_f32_16x16x64_bf16 v[196:199], a[240:243], v[96:103], v241\n"
"v_cvt_pk_bf16_f32 v129, v154, v155\n"
"v_mfma_f32_16x16x32_bf16 v[202:205], a[240:243], v[120:123], v[202:205]\n"
"s_waitcnt lgkmcnt(1)\n"
"ds_read_b64_tr_b16 a[254:255], v235 offset:21504\n"
"v_mfma_f32_16x16x32_bf16 v[168:171], v[216:219], a[8:11], v[180:183]\n"
"ds_read_b64_tr_b16 a[248:249], v238 offset:17408\n"
"v_mfma_f32_16x16x32_bf16 v[168:171], v[220:223], a[12:15], v[168:171]\n"
"ds_read_b64_tr_b16 a[246:247], v234 offset:21504\n"
"v_mfma_f32_16x16x32_bf16 v[168:171], v[208:211], a[0:3], v[168:171]\n"
"ds_read_b64_tr_b16 v[224:225], v239 offset:17408\n"
"v_cvt_pk_bf16_f32 v124, v164, v165\n"
"v_mfma_f32_16x16x32_bf16 v[168:171], v[212:215], a[4:7], v[168:171]\n"
"v_exp_f32 v166, v166\n"
"v_mfma_f32_16x16x32_bf16 v[172:175], v[208:211], a[16:19], v[184:187]\n"
"ds_read_b64_tr_b16 a[252:253], v235 offset:17408\n"
"v_cvt_pk_bf16_f32 v128, v152, v153\n"
"v_mfma_f32_16x16x32_bf16 v[172:175], v[212:215], a[20:23], v[172:175]\n"
"ds_read_b64_tr_b16 v[226:227], v239 offset:21504\n"
"v_cvt_pk_bf16_f32 v137, v158, v159\n"
"v_mfma_f32_16x16x32_bf16 v[172:175], v[216:219], a[24:27], v[172:175]\n"
"s_add_u32 s40, s56, s54\n"
"ds_read_b64_tr_b16 a[250:251], v238 offset:21504\n"
"v_mfma_f32_16x16x32_bf16 v[172:175], v[220:223], a[28:31], v[172:175]\n"
"buffer_load_dwordx4 v240, s[20:23], s40 offen offset:3072 nt lds\n"
"v_mfma_f32_16x16x32_bf16 v[176:179], v[216:219], a[40:43], v[188:191]\n"
"s_add_u32 s55, s55, s59\n"
"v_exp_f32 v167, v167\n"
"v_mfma_f32_16x16x32_bf16 v[176:179], v[220:223], a[44:47], v[176:179]\n"
"s_add_u32 s41, s55, s57\n"
"v_exp_f32 v161, v161\n"
"v_mfma_f32_16x16x32_bf16 v[176:179], v[208:211], a[32:35], v[176:179]\n"
"s_and_b32 s41, s41, s58\n"
"v_exp_f32 v157, v157\n"
"v_mfma_f32_16x16x32_bf16 v[176:179], v[212:215], a[36:39], v[176:179]\n"
"s_andn2_b32 s56, s55, s58\n"
"v_exp_f32 v160, v160\n"
"v_mfma_f32_16x16x32_bf16 v[152:155], v[208:211], a[192:195], v[192:195]\n"
"s_or_b32 s56, s56, s41\n"
"ds_read_b128 v[208:211], v230 offset:32768\n"
"v_mfma_f32_16x16x32_bf16 v[152:155], v[212:215], a[196:199], v[152:155]\n"
"s_cmp_ge_u32 s47, 15\n"
"v_exp_f32 v172, v172\n"
"v_mfma_f32_16x16x32_bf16 v[152:155], v[216:219], a[200:203], v[152:155]\n"
"s_cbranch_scc1 LSW_%=\nLSWK_%=:\n"
"ds_read_b128 v[216:219], v230 offset:33792\n"
"v_mfma_f32_16x16x32_bf16 v[152:155], v[220:223], a[204:207], v[152:155]\n"
"s_waitcnt lgkmcnt(2)\n"
"v_exp_f32 v162, v162\n"
"v_mfma_f32_16x16x32_bf16 a[232:235], v[120:123], a[244:247], a[232:235]\n"
"ds_read_b128 v[212:215], v231 offset:32768\n"
"v_mfma_f32_16x16x32_bf16 v[24:27], v[96:99], a[244:247], v[24:27]\n"
"s_mov_b32 m0, s44\n"
"ds_read_b128 v[220:223], v231 offset:33792\n"
"v_mfma_f32_16x16x32_bf16 v[24:27], v[100:103], a[248:251], v[24:27]\n"
"buffer_load_dwordx4 v240, s[16:19], s56 offen nt lds\n"
"v_mfma_f32_16x16x32_bf16 v[56:59], v[104:107], a[244:247], v[56:59]\n"
"v_exp_f32 v171, v171\n"
"v_mfma_f32_16x16x32_bf16 v[56:59], v[108:111], a[248:251], v[56:59]\n"
"v_exp_f32 v169, v169\n"
"v_mfma_f32_16x16x32_bf16 v[88:91], v[112:115], a[244:247], v[88:91]\n"
"v_exp_f32 v154, v154\n"
"v_mfma_f32_16x16x32_bf16 v[88:91], v[116:119], a[248:251], v[88:91]\n"
"v_exp_f32 v156, v156\n"
"v_mfma_f32_16x16x32_bf16 v[92:95], v[116:119], v[224:227], v[92:95]\n"
"ds_read_b64_tr_b16 a[244:245], v232 offset:16384\n"
"v_cvt_pk_bf16_f32 v145, v162, v163\n"
"v_mfma_f32_16x16x32_bf16 v[92:95], v[112:115], a[252:255], v[92:95]\n"
"ds_read_b64_tr_b16 a[250:251], v236 offset:20480\n"
"v_cvt_pk_bf16_f32 v136, v156, v157\n"
"v_mfma_f32_16x16x32_bf16 v[60:63], v[108:111], v[224:227], v[60:63]\n"
"ds_read_b64_tr_b16 a[246:247], v232 offset:20480\n"
"v_cvt_pk_bf16_f32 v144, v160, v161\n"
"v_mfma_f32_16x16x32_bf16 v[60:63], v[104:107], a[252:255], v[60:63]\n"
"v_exp_f32 v152, v152\n"
"v_mfma_f32_16x16x32_bf16 v[28:31], v[100:103], v[224:227], v[28:31]\n"
"ds_read_b64_tr_b16 v[224:225], v237 offset:16384\n"
"ds_read_b64_tr_b16 v[226:227], v237 offset:20480\n"
"v_mfma_f32_16x16x32_bf16 v[28:31], v[96:99], a[252:255], v[28:31]\n"
"ds_read_b64_tr_b16 a[248:249], v236 offset:16384\n"
"v_cvt_pk_bf16_f32 v125, v166, v167\n"
"v_mfma_f32_16x16x32_bf16 a[236:239], v[120:123], a[252:255], a[236:239]\n"
"s_add_u32 s40, s56, s52\n"
"ds_read_b64_tr_b16 a[252:253], v233 offset:16384\n"
"v_smfmac_f32_16x16x64_bf16 v[198:201], a[240:243], v[104:111], v241\n"
"s_waitcnt lgkmcnt(7)\n"
"buffer_load_dwordx4 v240, s[16:19], s40 offen offset:1024 nt lds\n"
"v_mfma_f32_16x16x32_bf16 v[156:159], v[208:211], a[0:3], v[180:183]\n"
"v_exp_f32 v175, v175\n"
"v_mfma_f32_16x16x32_bf16 v[156:159], v[212:215], a[4:7], v[156:159]\n"
"v_exp_f32 v173, v173\n"
"v_mfma_f32_16x16x32_bf16 v[156:159], v[216:219], a[8:11], v[156:159]\n"
"v_exp_f32 v168, v168\n"
"v_mfma_f32_16x16x32_bf16 v[156:159], v[220:223], a[12:15], v[156:159]\n"
"ds_read_b64_tr_b16 a[254:255], v233 offset:20480\n"
"v_mfma_f32_16x16x32_bf16 v[160:163], v[216:219], a[24:27], v[184:187]\n"
"v_exp_f32 v170, v170\n"
"v_mfma_f32_16x16x32_bf16 v[160:163], v[220:223], a[28:31], v[160:163]\n"
"v_exp_f32 v178, v178\n"
"v_mfma_f32_16x16x32_bf16 v[160:163], v[208:211], a[16:19], v[160:163]\n"
"v_exp_f32 v177, v177\n"
"v_mfma_f32_16x16x32_bf16 v[160:163], v[212:215], a[20:23], v[160:163]\n"
"v_exp_f32 v176, v176\n"
"v_mfma_f32_16x16x32_bf16 v[164:167], v[208:211], a[32:35], v[188:191]\n"
"ds_read_b128 v[208:211], v230 offset:36864\n"
"v_cvt_pk_bf16_f32 v131, v170, v171\n"
"v_mfma_f32_16x16x32_bf16 v[164:167], v[212:215], a[36:39], v[164:167]\n"
"ds_read_b128 v[212:215], v231 offset:36864\n"
"v_mfma_f32_16x16x32_bf16 v[164:167], v[216:219], a[40:43], v[164:167]\n"
"v_exp_f32 v153, v153\n"
"v_mfma_f32_16x16x32_bf16 v[164:167], v[220:223], a[44:47], v[164:167]\n"
"s_waitcnt lgkmcnt(2)\n"
"ds_read_b128 v[220:223], v231 offset:37888\n"
"v_cvt_pk_bf16_f32 v138, v172, v173\n"
"v_mfma_f32_16x16x32_bf16 a[208:211], v[120:123], a[244:247], a[208:211]\n"
"v_exp_f32 v174, v174\n"
"v_mfma_f32_16x16x32_bf16 v[0:3], v[96:99], a[244:247], v[0:3]\n"
"s_add_u32 s40, s56, s53\n"
"ds_read_b128 v[216:219], v230 offset:37888\n"
"v_mfma_f32_16x16x32_bf16 v[0:3], v[100:103], a[248:251], v[0:3]\n"
"buffer_load_dwordx4 v240, s[16:19], s40 offen offset:2048 nt lds\n"
"v_mfma_f32_16x16x32_bf16 v[32:35], v[104:107], a[244:247], v[32:35]\n"
"v_exp_f32 v163, v163\n"
"v_mfma_f32_16x16x32_bf16 v[32:35], v[108:111], a[248:251], v[32:35]\n"
"v_exp_f32 v161, v161\n"
"v_mfma_f32_16x16x32_bf16 v[64:67], v[112:115], a[244:247], v[64:67]\n"
"v_exp_f32 v155, v155\n"
"v_mfma_f32_16x16x32_bf16 v[64:67], v[116:119], a[248:251], v[64:67]\n"
"v_exp_f32 v166, v166\n"
"v_mfma_f32_16x16x32_bf16 v[68:71], v[116:119], v[224:227], v[68:71]\n"
"ds_read_b64_tr_b16 a[250:251], v236 offset:21504\n"
"v_cvt_pk_bf16_f32 v139, v174, v175\n"
"v_mfma_f32_16x16x32_bf16 v[68:71], v[112:115], a[252:255], v[68:71]\n"
"ds_read_b64_tr_b16 a[246:247], v232 offset:21504\n"
"v_cvt_pk_bf16_f32 v130, v168, v169\n"
"v_mfma_f32_16x16x32_bf16 v[36:39], v[108:111], v[224:227], v[36:39]\n"
"v_exp_f32 v179, v179\n"
"v_mfma_f32_16x16x32_bf16 v[36:39], v[104:107], a[252:255], v[36:39]\n"
"ds_read_b64_tr_b16 a[244:245], v232 offset:17408\n"
"v_cvt_pk_bf16_f32 v126, v152, v153\n"
"v_mfma_f32_16x16x32_bf16 v[4:7], v[100:103], v[224:227], v[4:7]\n"
"s_add_u32 s40, s56, s54\n"
"ds_read_b64_tr_b16 a[248:249], v236 offset:17408\n"
"v_mfma_f32_16x16x32_bf16 v[4:7], v[96:99], a[252:255], v[4:7]\n"
"buffer_load_dwordx4 v240, s[16:19], s40 offen offset:3072 nt lds\n"
"v_mfma_f32_16x16x32_bf16 a[212:215], v[120:123], a[252:255], a[212:215]\n"
"v_exp_f32 v162, v162\n"
"v_smfmac_f32_16x16x64_bf16 v[200:203], a[240:243], v[112:119], v241\n"
"s_waitcnt lgkmcnt(4)\n"
"v_cvt_pk_bf16_f32 v146, v176, v177\n"
"v_mfma_f32_16x16x32_bf16 v[168:171], v[208:211], a[0:3], v[180:183]\n"
"v_exp_f32 v157, v157\n"
"v_mfma_f32_16x16x32_bf16 v[168:171], v[212:215], a[4:7], v[168:171]\n"
"v_exp_f32 v165, v165\n"
"v_mfma_f32_16x16x32_bf16 v[168:171], v[216:219], a[8:11], v[168:171]\n"
"ds_read_b64_tr_b16 a[254:255], v233 offset:21504\n"
"ds_read_b64_tr_b16 v[226:227], v237 offset:21504\n"
"v_mfma_f32_16x16x32_bf16 v[168:171], v[220:223], a[12:15], v[168:171]\n"
"ds_read_b64_tr_b16 a[252:253], v233 offset:17408\n"
"v_cvt_pk_bf16_f32 v147, v178, v179\n"
"v_mfma_f32_16x16x32_bf16 v[172:175], v[216:219], a[24:27], v[184:187]\n"
"ds_read_b64_tr_b16 v[224:225], v237 offset:17408\n"
"v_mfma_f32_16x16x32_bf16 v[172:175], v[220:223], a[28:31], v[172:175]\n"
"v_exp_f32 v160, v160\n"
"v_mfma_f32_16x16x32_bf16 v[172:175], v[208:211], a[16:19], v[172:175]\n"
"v_exp_f32 v158, v158\n"
"v_mfma_f32_16x16x32_bf16 v[172:175], v[212:215], a[20:23], v[172:175]\n"
"s_cbranch_vccnz LPCL2_%=\nLPKL2_%=:\n"
"v_mfma_f32_16x16x32_bf16 v[176:179], v[208:211], a[32:35], v[188:191]\n"
"v_exp_f32 v159, v159\n"
"v_mfma_f32_16x16x32_bf16 v[176:179], v[212:215], a[36:39], v[176:179]\n"
"v_exp_f32 v167, v167\n"
"v_mfma_f32_16x16x32_bf16 v[176:179], v[216:219], a[40:43], v[176:179]\n"
"v_exp_f32 v164, v164\n"
"v_mfma_f32_16x16x32_bf16 v[176:179], v[220:223], a[44:47], v[176:179]\n"
"s_waitcnt lgkmcnt(1)\n"
"v_mfma_f32_16x16x32_bf16 a[224:227], v[120:123], a[244:247], a[224:227]\n"
"v_exp_f32 v169, v169\n"
"v_mfma_f32_16x16x32_bf16 v[16:19], v[96:99], a[244:247], v[16:19]\n"
"s_waitcnt vmcnt(8)\n"
"v_exp_f32 v175, v175\n"
"v_mfma_f32_16x16x32_bf16 v[16:19], v[100:103], a[248:251], v[16:19]\n"
"s_barrier\n"
"ds_read_b128 v[220:223], v229 offset:50176\n"
"v_mfma_f32_16x16x32_bf16 v[48:51], v[104:107], a[244:247], v[48:51]\n"
"ds_read_b128 v[216:219], v228 offset:50176\n"
"v_cvt_pk_bf16_f32 v141, v162, v163\n"
"v_mfma_f32_16x16x32_bf16 v[48:51], v[108:111], a[248:251], v[48:51]\n"
"ds_read_b128 v[212:215], v229 offset:49152\n"
"v_mfma_f32_16x16x32_bf16 v[80:83], v[112:115], a[244:247], v[80:83]\n"
"ds_read_b128 v[208:211], v228 offset:49152\n"
"v_cvt_pk_bf16_f32 v133, v158, v159\n"
"v_mfma_f32_16x16x32_bf16 v[80:83], v[116:119], a[248:251], v[80:83]\n"
"s_waitcnt lgkmcnt(4)\n"
"v_exp_f32 v178, v178\n"
"v_mfma_f32_16x16x32_bf16 v[84:87], v[116:119], v[224:227], v[84:87]\n"
"ds_read_b64_tr_b16 a[244:245], v234 offset:32768\n"
"v_cvt_pk_bf16_f32 v149, v166, v167\n"
"v_mfma_f32_16x16x32_bf16 v[84:87], v[112:115], a[252:255], v[84:87]\n"
"ds_read_b64_tr_b16 a[250:251], v238 offset:36864\n"
"v_cvt_pk_bf16_f32 v127, v154, v155\n"
"v_mfma_f32_16x16x32_bf16 v[52:55], v[108:111], v[224:227], v[52:55]\n"
"v_exp_f32 v173, v173\n"
"v_mfma_f32_16x16x32_bf16 v[52:55], v[104:107], a[252:255], v[52:55]\n"
"ds_read_b64_tr_b16 a[248:249], v238 offset:32768\n"
"v_mfma_f32_16x16x32_bf16 v[20:23], v[100:103], v[224:227], v[20:23]\n"
"v_exp_f32 v156, v156\n"
"v_mfma_f32_16x16x32_bf16 v[20:23], v[96:99], a[252:255], v[20:23]\n"
"ds_read_b64_tr_b16 v[226:227], v239 offset:36864\n"
"v_cvt_pk_bf16_f32 v132, v156, v157\n"
"v_mfma_f32_16x16x32_bf16 a[228:231], v[120:123], a[252:255], a[228:231]\n"
"s_waitcnt lgkmcnt(4)\n"
"ds_read_b64_tr_b16 a[246:247], v234 offset:36864\n"
"v_mfma_f32_16x16x32_bf16 v[152:155], v[208:211], a[0:3], v[180:183]\n"
"ds_read_b64_tr_b16 v[224:225], v239 offset:32768\n"
"v_cvt_pk_bf16_f32 v148, v164, v165\n"
"v_mfma_f32_16x16x32_bf16 v[152:155], v[212:215], a[4:7], v[152:155]\n"
"ds_read_b64_tr_b16 a[252:253], v235 offset:32768\n"
"v_cvt_pk_bf16_f32 v140, v160, v161\n"
"v_mfma_f32_16x16x32_bf16 v[152:155], v[216:219], a[8:11], v[152:155]\n"
"s_add_u32 m0, s44, 65536\n"
"ds_read_b64_tr_b16 a[254:255], v235 offset:36864\n"
"v_mfma_f32_16x16x32_bf16 v[152:155], v[220:223], a[12:15], v[152:155]\n"
"buffer_load_dwordx4 v240, s[20:23], s56 offen nt lds\n"
"v_mfma_f32_16x16x32_bf16 v[156:159], v[216:219], a[24:27], v[184:187]\n"
"v_exp_f32 v177, v177\n"
"v_mfma_f32_16x16x32_bf16 v[156:159], v[220:223], a[28:31], v[156:159]\n"
"v_exp_f32 v168, v168\n"
"v_mfma_f32_16x16x32_bf16 v[156:159], v[208:211], a[16:19], v[156:159]\n"
"v_exp_f32 v179, v179\n"
"v_mfma_f32_16x16x32_bf16 v[156:159], v[212:215], a[20:23], v[156:159]\n"
"v_exp_f32 v174, v174\n"
"v_mfma_f32_16x16x32_bf16 v[160:163], v[208:211], a[32:35], v[188:191]\n"
"v_exp_f32 v172, v172\n"
"v_mfma_f32_16x16x32_bf16 v[160:163], v[212:215], a[36:39], v[160:163]\n"
"v_exp_f32 v171, v171\n"
"v_mfma_f32_16x16x32_bf16 v[160:163], v[216:219], a[40:43], v[160:163]\n"
"v_exp_f32 v154, v154\n"
"v_mfma_f32_16x16x32_bf16 v[160:163], v[220:223], a[44:47], v[160:163]\n"
"v_exp_f32 v170, v170\n"
"v_mfma_f32_16x16x32_bf16 v[164:167], v[216:219], a[200:203], v[192:195]\n"
"ds_read_b128 v[216:219], v228 offset:54272\n"
"v_cvt_pk_bf16_f32 v135, v170, v171\n"
"v_mfma_f32_16x16x32_bf16 v[164:167], v[220:223], a[204:207], v[164:167]\n"
"v_cvt_pk_bf16_f32 v134, v168, v169\n"
"v_cvt_pk_bf16_f32 v151, v178, v179\n"
"v_mfma_f32_16x16x32_bf16 v[164:167], v[208:211], a[192:195], v[164:167]\n"
"s_add_u32 s40, s56, s52\n"
"ds_read_b128 v[208:211], v228 offset:53248\n"
"v_mfma_f32_16x16x32_bf16 v[164:167], v[212:215], a[196:199], v[164:167]\n"
"s_waitcnt lgkmcnt(2)\n"
"buffer_load_dwordx4 v240, s[20:23], s40 offen offset:1024 nt lds\n"
"v_mfma_f32_16x16x32_bf16 a[216:219], v[124:127], a[244:247], a[216:219]\n"
"v_cvt_pk_bf16_f32 v142, v172, v173\n"
"v_cvt_pk_bf16_f32 v143, v174, v175\n"
"v_mfma_f32_16x16x32_bf16 v[8:11], v[128:131], a[244:247], v[8:11]\n"
"v_exp_f32 v176, v176\n"
"v_mfma_f32_16x16x32_bf16 v[8:11], v[132:135], a[248:251], v[8:11]\n"
"v_exp_f32 v155, v155\n"
"v_mfma_f32_16x16x32_bf16 v[40:43], v[136:139], a[244:247], v[40:43]\n"
"v_exp_f32 v152, v152\n"
"v_mfma_f32_16x16x32_bf16 v[40:43], v[140:143], a[248:251], v[40:43]\n"
"ds_read_b128 v[212:215], v229 offset:53248\n"
"v_cvt_pk_bf16_f32 v150, v176, v177\n"
"v_mfma_f32_16x16x32_bf16 v[72:75], v[144:147], a[244:247], v[72:75]\n"
"v_exp_f32 v158, v158\n"
"v_mfma_f32_16x16x32_bf16 v[72:75], v[148:151], a[248:251], v[72:75]\n"
"v_exp_f32 v163, v163\n"
"v_mfma_f32_16x16x32_bf16 v[76:79], v[148:151], v[224:227], v[76:79]\n"
"ds_read_b128 v[220:223], v229 offset:54272\n"
"v_mfma_f32_16x16x32_bf16 v[76:79], v[144:147], a[252:255], v[76:79]\n"
"v_exp_f32 v159, v159\n"
"v_mfma_f32_16x16x32_bf16 v[44:47], v[140:143], v[224:227], v[44:47]\n"
"s_add_u32 s40, s56, s53\n"
"ds_read_b64_tr_b16 a[244:245], v234 offset:33792\n"
"v_mfma_f32_16x16x32_bf16 v[44:47], v[136:139], a[252:255], v[44:47]\n"
"buffer_load_dwordx4 v240, s[20:23], s40 offen offset:2048 nt lds\n"
"v_mfma_f32_16x16x32_bf16 v[12:15], v[132:135], v[224:227], v[12:15]\n"
"v_exp_f32 v153, v153\n"
"v_mfma_f32_16x16x32_bf16 v[12:15], v[128:131], a[252:255], v[12:15]\n"
"v_exp_f32 v165, v165\n"
"v_mfma_f32_16x16x32_bf16 a[220:223], v[124:127], a[252:255], a[220:223]\n"
"v_exp_f32 v164, v164\n"
"v_smfmac_f32_16x16x64_bf16 v[196:199], a[240:243], v[128:135], v241\n"
"v_cvt_pk_bf16_f32 v97, v154, v155\n"
"v_mfma_f32_16x16x32_bf16 v[202:205], a[240:243], v[124:127], v[202:205]\n"
"s_waitcnt lgkmcnt(1)\n"
"ds_read_b64_tr_b16 a[254:255], v235 offset:37888\n"
"v_mfma_f32_16x16x32_bf16 v[168:171], v[216:219], a[8:11], v[180:183]\n"
"ds_read_b64_tr_b16 a[248:249], v238 offset:33792\n"
"v_mfma_f32_16x16x32_bf16 v[168:171], v[220:223], a[12:15], v[168:171]\n"
"ds_read_b64_tr_b16 a[246:247], v234 offset:37888\n"
"v_mfma_f32_16x16x32_bf16 v[168:171], v[208:211], a[0:3], v[168:171]\n"
"ds_read_b64_tr_b16 v[224:225], v239 offset:33792\n"
"v_cvt_pk_bf16_f32 v120, v164, v165\n"
"v_mfma_f32_16x16x32_bf16 v[168:171], v[212:215], a[4:7], v[168:171]\n"
"v_exp_f32 v166, v166\n"
"v_mfma_f32_16x16x32_bf16 v[172:175], v[208:211], a[16:19], v[184:187]\n"
"ds_read_b64_tr_b16 a[252:253], v235 offset:33792\n"
"v_cvt_pk_bf16_f32 v96, v152, v153\n"
"v_mfma_f32_16x16x32_bf16 v[172:175], v[212:215], a[20:23], v[172:175]\n"
"ds_read_b64_tr_b16 v[226:227], v239 offset:37888\n"
"v_cvt_pk_bf16_f32 v105, v158, v159\n"
"v_mfma_f32_16x16x32_bf16 v[172:175], v[216:219], a[24:27], v[172:175]\n"
"s_add_u32 s40, s56, s54\n"
"ds_read_b64_tr_b16 a[250:251], v238 offset:37888\n"
"v_mfma_f32_16x16x32_bf16 v[172:175], v[220:223], a[28:31], v[172:175]\n"
"buffer_load_dwordx4 v240, s[20:23], s40 offen offset:3072 nt lds\n"
"v_mfma_f32_16x16x32_bf16 v[176:179], v[216:219], a[40:43], v[188:191]\n"
"s_add_u32 s55, s55, s59\n"
"v_exp_f32 v167, v167\n"
"v_mfma_f32_16x16x32_bf16 v[176:179], v[220:223], a[44:47], v[176:179]\n"
"s_add_u32 s41, s55, s57\n"
"v_exp_f32 v161, v161\n"
"v_mfma_f32_16x16x32_bf16 v[176:179], v[208:211], a[32:35], v[176:179]\n"
"s_and_b32 s41, s41, s58\n"
"v_exp_f32 v157, v157\n"
"v_mfma_f32_16x16x32_bf16 v[176:179], v[212:215], a[36:39], v[176:179]\n"
"s_andn2_b32 s56, s55, s58\n"
"v_exp_f32 v160, v160\n"
"v_mfma_f32_16x16x32_bf16 v[152:155], v[208:211], a[192:195], v[192:195]\n"
"s_or_b32 s56, s56, s41\n"
"ds_read_b128 v[208:211], v230 offset:49152\n"
"v_mfma_f32_16x16x32_bf16 v[152:155], v[212:215], a[196:199], v[152:155]\n"
"v_exp_f32 v172, v172\n"
"v_mfma_f32_16x16x32_bf16 v[152:155], v[216:219], a[200:203], v[152:155]\n"
"ds_read_b128 v[216:219], v230 offset:50176\n"
"v_mfma_f32_16x16x32_bf16 v[152:155], v[220:223], a[204:207], v[152:155]\n"
"s_waitcnt lgkmcnt(2)\n"
"v_exp_f32 v162, v162\n"
"v_mfma_f32_16x16x32_bf16 a[232:235], v[124:127], a[244:247], a[232:235]\n"
"ds_read_b128 v[212:215], v231 offset:49152\n"
"v_mfma_f32_16x16x32_bf16 v[24:27], v[128:131], a[244:247], v[24:27]\n"
"s_add_u32 m0, s44, 16384\n"
"ds_read_b128 v[220:223], v231 offset:50176\n"
"v_mfma_f32_16x16x32_bf16 v[24:27], v[132:135], a[248:251], v[24:27]\n"
"buffer_load_dwordx4 v240, s[16:19], s56 offen nt lds\n"
"v_mfma_f32_16x16x32_bf16 v[56:59], v[136:139], a[244:247], v[56:59]\n"
"v_exp_f32 v171, v171\n"
"v_mfma_f32_16x16x32_bf16 v[56:59], v[140:143], a[248:251], v[56:59]\n"
"v_exp_f32 v169, v169\n"
"v_mfma_f32_16x16x32_bf16 v[88:91], v[144:147], a[244:247], v[88:91]\n"
"v_exp_f32 v154, v154\n"
"v_mfma_f32_16x16x32_bf16 v[88:91], v[148:151], a[248:251], v[88:91]\n"
"v_exp_f32 v156, v156\n"
"v_mfma_f32_16x16x32_bf16 v[92:95], v[148:151], v[224:227], v[92:95]\n"
"ds_read_b64_tr_b16 a[244:245], v232 offset:32768\n"
"v_cvt_pk_bf16_f32 v113, v162, v163\n"
"v_mfma_f32_16x16x32_bf16 v[92:95], v[144:147], a[252:255], v[92:95]\n"
"ds_read_b64_tr_b16 a[250:251], v236 offset:36864\n"
"v_cvt_pk_bf16_f32 v104, v156, v157\n"
"v_mfma_f32_16x16x32_bf16 v[60:63], v[140:143], v[224:227], v[60:63]\n"
"ds_read_b64_tr_b16 a[246:247], v232 offset:36864\n"
"v_cvt_pk_bf16_f32 v112, v160, v161\n"
"v_mfma_f32_16x16x32_bf16 v[60:63], v[136:139], a[252:255], v[60:63]\n"
"v_exp_f32 v152, v152\n"
"v_mfma_f32_16x16x32_bf16 v[28:31], v[132:135], v[224:227], v[28:31]\n"
"ds_read_b64_tr_b16 v[224:225], v237 offset:32768\n"
"ds_read_b64_tr_b16 v[226:227], v237 offset:36864\n"
"v_mfma_f32_16x16x32_bf16 v[28:31], v[128:131], a[252:255], v[28:31]\n"
"ds_read_b64_tr_b16 a[248:249], v236 offset:32768\n"
"v_cvt_pk_bf16_f32 v121, v166, v167\n"
"v_mfma_f32_16x16x32_bf16 a[236:239], v[124:127], a[252:255], a[236:239]\n"
"s_add_u32 s40, s56, s52\n"
"ds_read_b64_tr_b16 a[252:253], v233 offset:32768\n"
"v_smfmac_f32_16x16x64_bf16 v[198:201], a[240:243], v[136:143], v241\n"
"s_waitcnt lgkmcnt(7)\n"
"buffer_load_dwordx4 v240, s[16:19], s40 offen offset:1024 nt lds\n"
"v_mfma_f32_16x16x32_bf16 v[156:159], v[208:211], a[0:3], v[180:183]\n"
"v_exp_f32 v175, v175\n"
"v_mfma_f32_16x16x32_bf16 v[156:159], v[212:215], a[4:7], v[156:159]\n"
"v_exp_f32 v173, v173\n"
"v_mfma_f32_16x16x32_bf16 v[156:159], v[216:219], a[8:11], v[156:159]\n"
"v_exp_f32 v168, v168\n"
"v_mfma_f32_16x16x32_bf16 v[156:159], v[220:223], a[12:15], v[156:159]\n"
"ds_read_b64_tr_b16 a[254:255], v233 offset:36864\n"
"v_mfma_f32_16x16x32_bf16 v[160:163], v[216:219], a[24:27], v[184:187]\n"
"v_exp_f32 v170, v170\n"
"v_mfma_f32_16x16x32_bf16 v[160:163], v[220:223], a[28:31], v[160:163]\n"
"v_exp_f32 v178, v178\n"
"v_mfma_f32_16x16x32_bf16 v[160:163], v[208:211], a[16:19], v[160:163]\n"
"v_exp_f32 v177, v177\n"
"v_mfma_f32_16x16x32_bf16 v[160:163], v[212:215], a[20:23], v[160:163]\n"
"v_exp_f32 v176, v176\n"
"v_mfma_f32_16x16x32_bf16 v[164:167], v[208:211], a[32:35], v[188:191]\n"
"ds_read_b128 v[208:211], v230 offset:53248\n"
"v_cvt_pk_bf16_f32 v99, v170, v171\n"
"v_mfma_f32_16x16x32_bf16 v[164:167], v[212:215], a[36:39], v[164:167]\n"
"ds_read_b128 v[212:215], v231 offset:53248\n"
"v_mfma_f32_16x16x32_bf16 v[164:167], v[216:219], a[40:43], v[164:167]\n"
"v_exp_f32 v153, v153\n"
"v_mfma_f32_16x16x32_bf16 v[164:167], v[220:223], a[44:47], v[164:167]\n"
"s_waitcnt lgkmcnt(2)\n"
"ds_read_b128 v[220:223], v231 offset:54272\n"
"v_cvt_pk_bf16_f32 v106, v172, v173\n"
"v_mfma_f32_16x16x32_bf16 a[208:211], v[124:127], a[244:247], a[208:211]\n"
"v_exp_f32 v174, v174\n"
"v_mfma_f32_16x16x32_bf16 v[0:3], v[128:131], a[244:247], v[0:3]\n"
"s_add_u32 s40, s56, s53\n"
"ds_read_b128 v[216:219], v230 offset:54272\n"
"v_mfma_f32_16x16x32_bf16 v[0:3], v[132:135], a[248:251], v[0:3]\n"
"buffer_load_dwordx4 v240, s[16:19], s40 offen offset:2048 nt lds\n"
"v_mfma_f32_16x16x32_bf16 v[32:35], v[136:139], a[244:247], v[32:35]\n"
"v_exp_f32 v163, v163\n"
"v_mfma_f32_16x16x32_bf16 v[32:35], v[140:143], a[248:251], v[32:35]\n"
"v_exp_f32 v161, v161\n"
"v_mfma_f32_16x16x32_bf16 v[64:67], v[144:147], a[244:247], v[64:67]\n"
"v_exp_f32 v155, v155\n"
"v_mfma_f32_16x16x32_bf16 v[64:67], v[148:151], a[248:251], v[64:67]\n"
"v_exp_f32 v166, v166\n"
"v_mfma_f32_16x16x32_bf16 v[68:71], v[148:151], v[224:227], v[68:71]\n"
"ds_read_b64_tr_b16 a[250:251], v236 offset:37888\n"
"v_cvt_pk_bf16_f32 v107, v174, v175\n"
"v_mfma_f32_16x16x32_bf16 v[68:71], v[144:147], a[252:255], v[68:71]\n"
"ds_read_b64_tr_b16 a[246:247], v232 offset:37888\n"
"v_cvt_pk_bf16_f32 v98, v168, v169\n"
"v_mfma_f32_16x16x32_bf16 v[36:39], v[140:143], v[224:227], v[36:39]\n"
"v_exp_f32 v179, v179\n"
"v_mfma_f32_16x16x32_bf16 v[36:39], v[136:139], a[252:255], v[36:39]\n"
"ds_read_b64_tr_b16 a[244:245], v232 offset:33792\n"
"v_cvt_pk_bf16_f32 v122, v152, v153\n"
"v_mfma_f32_16x16x32_bf16 v[4:7], v[132:135], v[224:227], v[4:7]\n"
"s_add_u32 s40, s56, s54\n"
"ds_read_b64_tr_b16 a[248:249], v236 offset:33792\n"
"v_mfma_f32_16x16x32_bf16 v[4:7], v[128:131], a[252:255], v[4:7]\n"
"buffer_load_dwordx4 v240, s[16:19], s40 offen offset:3072 nt lds\n"
"v_mfma_f32_16x16x32_bf16 a[212:215], v[124:127], a[252:255], a[212:215]\n"
"v_exp_f32 v162, v162\n"
"v_smfmac_f32_16x16x64_bf16 v[200:203], a[240:243], v[144:151], v241\n"
"s_waitcnt lgkmcnt(4)\n"
"v_cvt_pk_bf16_f32 v114, v176, v177\n"
"v_mfma_f32_16x16x32_bf16 v[168:171], v[208:211], a[0:3], v[180:183]\n"
"v_exp_f32 v157, v157\n"
"v_mfma_f32_16x16x32_bf16 v[168:171], v[212:215], a[4:7], v[168:171]\n"
"v_exp_f32 v165, v165\n"
"v_mfma_f32_16x16x32_bf16 v[168:171], v[216:219], a[8:11], v[168:171]\n"
"ds_read_b64_tr_b16 a[254:255], v233 offset:37888\n"
"ds_read_b64_tr_b16 v[226:227], v237 offset:37888\n"
"v_mfma_f32_16x16x32_bf16 v[168:171], v[220:223], a[12:15], v[168:171]\n"
"ds_read_b64_tr_b16 a[252:253], v233 offset:33792\n"
"v_cvt_pk_bf16_f32 v115, v178, v179\n"
"v_mfma_f32_16x16x32_bf16 v[172:175], v[216:219], a[24:27], v[184:187]\n"
"ds_read_b64_tr_b16 v[224:225], v237 offset:33792\n"
"v_mfma_f32_16x16x32_bf16 v[172:175], v[220:223], a[28:31], v[172:175]\n"
"v_exp_f32 v160, v160\n"
"v_mfma_f32_16x16x32_bf16 v[172:175], v[208:211], a[16:19], v[172:175]\n"
"v_exp_f32 v158, v158\n"
"v_mfma_f32_16x16x32_bf16 v[172:175], v[212:215], a[20:23], v[172:175]\n"
"s_cbranch_vccnz LPCL3_%=\nLPKL3_%=:\n"
"v_mfma_f32_16x16x32_bf16 v[176:179], v[208:211], a[32:35], v[188:191]\n"
"s_add_u32 s47, s47, 1\n"
"v_exp_f32 v159, v159\n"
"v_mfma_f32_16x16x32_bf16 v[176:179], v[212:215], a[36:39], v[176:179]\n"
"v_exp_f32 v167, v167\n"
"v_mfma_f32_16x16x32_bf16 v[176:179], v[216:219], a[40:43], v[176:179]\n"
"s_and_b32 s40, s47, 3\n"
"v_exp_f32 v164, v164\n"
"v_mfma_f32_16x16x32_bf16 v[176:179], v[220:223], a[44:47], v[176:179]\n"
"s_waitcnt lgkmcnt(1)\n"
"s_cbranch_scc1 LAL_%=\n"
"s_cmp_eq_u32 s47, 8\n"
"s_cbranch_scc1 LTH1_%=\n"
"s_cmp_eq_u32 s47, 12\n"
"s_cbranch_scc1 LTH2_%=\n"
"s_cmp_eq_u32 s47, 16\n"
"s_cbranch_scc1 LAX_%=\n"
"LTH0_%=:\n"
"v_mfma_f32_16x16x32_bf16 a[224:227], v[124:127], a[244:247], a[224:227]\n"
"v_exp_f32 v168, v168\n"
"v_mfma_f32_16x16x32_bf16 v[16:19], v[128:131], a[244:247], v[16:19]\n"
"s_waitcnt vmcnt(8) lgkmcnt(0)\n"
"v_exp_f32 v169, v169\n"
"v_mfma_f32_16x16x32_bf16 v[16:19], v[132:135], a[248:251], v[16:19]\n"
"s_barrier\n"
"v_mfma_f32_16x16x32_bf16 v[48:51], v[136:139], a[244:247], v[48:51]\n"
"v_exp_f32 v172, v172\n"
"v_mfma_f32_16x16x32_bf16 v[48:51], v[140:143], a[248:251], v[48:51]\n"
"ds_read_b128 v[216:219], v228 offset:1024\n"
"v_cvt_pk_bf16_f32 v102, v168, v169\n"
"v_mfma_f32_16x16x32_bf16 v[80:83], v[144:147], a[244:247], v[80:83]\n"
"v_exp_f32 v176, v176\n"
"v_mfma_f32_16x16x32_bf16 v[80:83], v[148:151], a[248:251], v[80:83]\n"
"ds_read_b64_tr_b16 a[244:245], v234 offset:49152\n"
"ds_read_b64_tr_b16 a[246:247], v234 offset:53248\n"
"v_cvt_pk_bf16_f32 v123, v154, v155\n"
"v_cvt_pk_bf16_f32 v117, v166, v167\n"
"v_exp_f32 v170, v170\n"
"v_exp_f32 v175, v175\n"
"v_exp_f32 v177, v177\n"
"v_mfma_f32_16x16x32_bf16 v[84:87], v[148:151], v[224:227], v[84:87]\n"
"v_exp_f32 v156, v156\n"
"v_mfma_f32_16x16x32_bf16 v[84:87], v[144:147], a[252:255], v[84:87]\n"
"ds_read_b64_tr_b16 a[248:249], v238 offset:49152\n"
"ds_read_b64_tr_b16 a[250:251], v238 offset:53248\n"
"v_mfma_f32_16x16x32_bf16 v[52:55], v[140:143], v[224:227], v[52:55]\n"
"v_cvt_pk_bf16_f32 v101, v158, v159\n"
"v_cvt_pk_bf16_f32 v118, v176, v177\n"
"v_mfma_f32_16x16x32_bf16 v[52:55], v[136:139], a[252:255], v[52:55]\n"
"v_cvt_pk_bf16_f32 v100, v156, v157\n"
"v_exp_f32 v173, v173\n"
"v_exp_f32 v174, v174\n"
"v_exp_f32 v178, v178\n"
"v_exp_f32 v179, v179\n"
"v_mfma_f32_16x16x32_bf16 v[20:23], v[132:135], v[224:227], v[20:23]\n"
"v_exp_f32 v171, v171\n"
"v_mfma_f32_16x16x32_bf16 v[20:23], v[128:131], a[252:255], v[20:23]\n"
"ds_read_b64_tr_b16 v[224:225], v239 offset:49152\n"
"v_cvt_pk_bf16_f32 v103, v170, v171\n"
"v_mfma_f32_16x16x32_bf16 a[228:231], v[124:127], a[252:255], a[228:231]\n"
"s_waitcnt lgkmcnt(3)\n"
"ds_read_b64_tr_b16 a[252:253], v235 offset:49152\n"
"v_cvt_pk_bf16_f32 v119, v178, v179\n"
"v_mfma_f32_16x16x32_bf16 a[216:219], v[120:123], a[244:247], a[216:219]\n"
"s_add_u32 m0, s44, 81920\n"
"ds_read_b64_tr_b16 a[254:255], v235 offset:53248\n"
"ds_read_b64_tr_b16 v[226:227], v239 offset:53248\n"
"v_smfmac_f32_16x16x64_bf16 v[196:199], a[240:243], v[96:103], v241\n"
"buffer_load_dwordx4 v240, s[20:23], s56 offen nt lds\n"
"v_mfma_f32_16x16x32_bf16 v[8:11], v[96:99], a[244:247], v[8:11]\n"
"s_waitcnt lgkmcnt(4)\n"
"v_cvt_pk_bf16_f32 v109, v162, v163\n"
"v_cvt_pk_bf16_f32 v116, v164, v165\n"
"v_mfma_f32_16x16x32_bf16 v[8:11], v[100:103], a[248:251], v[8:11]\n"
"v_cvt_pk_bf16_f32 v110, v172, v173\n"
"v_cvt_pk_bf16_f32 v111, v174, v175\n"
"v_smfmac_f32_16x16x64_bf16 v[200:203], a[240:243], v[112:119], v241\n"
"ds_read_b128 v[220:223], v229 offset:1024\n"
"v_cvt_pk_bf16_f32 v108, v160, v161\n"
"v_mfma_f32_16x16x32_bf16 v[40:43], v[104:107], a[244:247], v[40:43]\n"
"v_add_f32 v196, v196, v197\n"
"v_and_b32 v245, 0x3f0, v242\n"
"v_mfma_f32_16x16x32_bf16 v[40:43], v[108:111], a[248:251], v[40:43]\n"
"ds_read_b128 v[212:215], v229 offset:0\n"
"v_lshrrev_b32 v245, 1, v245\n"
"v_smfmac_f32_16x16x64_bf16 v[198:201], a[240:243], v[104:111], v241\n"
"v_lshlrev_b32_e64 v244, 24, s43\n"
"v_sub_u32 v245, v242, v245\n"
"v_mfma_f32_16x16x32_bf16 v[72:75], v[112:115], a[244:247], v[72:75]\n"
"ds_read_b64_tr_b16 a[244:245], v234 offset:50176\n"
"ds_read_b64_tr_b16 a[246:247], v234 offset:54272\n"
"v_mfma_f32_16x16x32_bf16 v[72:75], v[116:119], a[248:251], v[72:75]\n"
"ds_read_b128 v[208:211], v228 offset:0\n"
"v_mul_f32 v244, 0x45800000, v244\n"
"v_mfma_f32_16x16x32_bf16 v[202:205], a[240:243], v[120:123], v[202:205]\n"
"s_waitcnt lgkmcnt(5)\n"
"ds_read_b64_tr_b16 a[250:251], v238 offset:54272\n"
"v_add_f32 v198, v198, v199\n"
"v_mfma_f32_16x16x32_bf16 v[76:79], v[116:119], v[224:227], v[76:79]\n"
"v_add_f32 v200, v200, v201\n"
"v_add_f32 v254, v196, v198\n"
"v_mfma_f32_16x16x32_bf16 v[76:79], v[112:115], a[252:255], v[76:79]\n"
"ds_read_b64_tr_b16 a[248:249], v238 offset:50176\n"
"v_add_f32 v254, v254, v200\n"
"v_mfma_f32_16x16x32_bf16 v[44:47], v[108:111], v[224:227], v[44:47]\n"
"v_add_f32 v244, v244, v202\n"
"v_min3_f32 v244, v244, v196, v198\n"
"v_mfma_f32_16x16x32_bf16 v[44:47], v[104:107], a[252:255], v[44:47]\n"
"v_min_f32 v244, v244, v200\n"
"v_rcp_f32 v244, v244\n"
"v_mfma_f32_16x16x32_bf16 v[12:15], v[100:103], v[224:227], v[12:15]\n"
"s_add_u32 s40, s56, s52\n"
"v_add_f32 v254, v254, v202\n"
"v_fmac_f32 v254, 0x68800000, v244\n"
"v_mfma_f32_16x16x32_bf16 v[12:15], v[96:99], a[252:255], v[12:15]\n"
"buffer_load_dwordx4 v240, s[20:23], s40 offen offset:1024 nt lds\n"
"v_mfma_f32_16x16x32_bf16 a[220:223], v[120:123], a[252:255], a[220:223]\n"
"s_waitcnt lgkmcnt(2)\n"
"v_cmp_nge_f32 vcc, s45, v254\n"
"s_cbranch_vccnz LSLOW_%=\nLBACK0_%=:\ns_mov_b64 vcc, s[64:65]\n"
"v_mfma_f32_16x16x32_bf16 a[232:235], v[120:123], a[244:247], a[232:235]\n"
"v_rcp_f32 v246, v196\n"
"v_rcp_f32 v206, v200\n"
"v_mov_b32 v252, v192\n"
"v_accvgpr_mov_b32 a9, a57\n"
"v_accvgpr_mov_b32 a10, a58\n"
"v_accvgpr_mov_b32 a28, a76\n"
"v_accvgpr_mov_b32 a31, a79\n"
"v_accvgpr_mov_b32 a33, a81\n"
"v_accvgpr_mov_b32 a40, a88\n"
"v_bfe_u32 v149, v242, 8, 2\n"
"v_lshl_add_u32 v149, v149, 4, s43\n"
"v_xor_b32 v150, 64, v149\n"
"v_mfma_f32_16x16x32_bf16 v[24:27], v[96:99], a[244:247], v[24:27]\n"
"s_waitcnt lgkmcnt(0)\n"
"ds_read_b128 v[128:131], v150 offset:0\n"
"v_accvgpr_mov_b32 a12, a60\n"
"v_mfma_f32_16x16x32_bf16 v[24:27], v[100:103], a[248:251], v[24:27]\n"
"ds_read_b128 v[124:127], v149 offset:0\n"
"v_accvgpr_mov_b32 a0, a48\n"
"v_accvgpr_mov_b32 a3, a51\n"
"v_accvgpr_mov_b32 a4, a52\n"
"v_accvgpr_mov_b32 a18, a66\n"
"v_accvgpr_mov_b32 a21, a69\n"
"v_accvgpr_mov_b32 a35, a83\n"
"v_accvgpr_mov_b32 a38, a86\n"
"v_accvgpr_mov_b32 a47, a95\n"
"v_mfma_f32_16x16x32_bf16 v[56:59], v[104:107], a[244:247], v[56:59]\n"
"ds_read_b128 v[132:135], v149 offset:1024\n"
"v_rcp_f32 v247, v198\n"
"v_accvgpr_mov_b32 a1, a49\n"
"v_accvgpr_mov_b32 a2, a50\n"
"v_accvgpr_mov_b32 a11, a59\n"
"v_accvgpr_mov_b32 a13, a61\n"
"v_accvgpr_mov_b32 a14, a62\n"
"v_accvgpr_mov_b32 a16, a64\n"
"v_accvgpr_mov_b32 a20, a68\n"
"v_accvgpr_mov_b32 a22, a70\n"
"v_accvgpr_mov_b32 a25, a73\n"
"v_accvgpr_mov_b32 a36, a84\n"
"v_xor_b32 v148, 0x400, v243\n"
"v_mfma_f32_16x16x32_bf16 v[56:59], v[108:111], a[248:251], v[56:59]\n"
"ds_read_b128 v[136:139], v150 offset:1024\n"
"v_mov_b32_dpp v246, v246 row_ror:12 row_mask:0x2 bank_mask:0xf\n"
"v_accvgpr_mov_b32 a6, a54\n"
"v_accvgpr_mov_b32 a7, a55\n"
"v_accvgpr_mov_b32 a8, a56\n"
"v_accvgpr_mov_b32 a24, a72\n"
"v_accvgpr_mov_b32 a29, a77\n"
"v_accvgpr_mov_b32 a34, a82\n"
"v_accvgpr_mov_b32 a39, a87\n"
"v_accvgpr_mov_b32 a43, a91\n"
"v_accvgpr_mov_b32 a45, a93\n"
"v_mfma_f32_16x16x32_bf16 v[88:91], v[112:115], a[244:247], v[88:91]\n"
"v_accvgpr_mov_b32 a19, a67\n"
"v_mfma_f32_16x16x32_bf16 v[88:91], v[116:119], a[248:251], v[88:91]\n"
"ds_read_b128 a[192:195], v243 offset:4096\n"
"v_accvgpr_mov_b32 a5, a53\n"
"v_accvgpr_mov_b32 a17, a65\n"
"v_accvgpr_mov_b32 a23, a71\n"
"v_accvgpr_mov_b32 a26, a74\n"
"v_accvgpr_mov_b32 a27, a75\n"
"v_accvgpr_mov_b32 a37, a85\n"
"v_accvgpr_mov_b32 a46, a94\n"
"s_waitcnt lgkmcnt(3)\n"
"v_mfma_f32_16x16x32_bf16 v[180:183], v[124:127], a[0:3], 0\n"
"ds_read_b128 a[196:199], v148 offset:4096\n"
"v_accvgpr_mov_b32 a15, a63\n"
"v_mfma_f32_16x16x32_bf16 v[180:183], v[128:131], a[4:7], v[180:183]\n"
"s_waitcnt lgkmcnt(3)\n"
"v_mfma_f32_16x16x32_bf16 v[180:183], v[132:135], a[8:11], v[180:183]\n"
"v_accvgpr_mov_b32 a41, a89\n"
"s_waitcnt lgkmcnt(2)\n"
"v_mfma_f32_16x16x32_bf16 v[180:183], v[136:139], a[12:15], v[180:183]\n"
"ds_read_b128 a[204:207], v148 offset:6144\n"
"v_mov_b32_dpp v246, v246 row_ror:8 row_mask:0x4 bank_mask:0xf\n"
"v_mfma_f32_16x16x32_bf16 v[184:187], v[124:127], a[16:19], 0\n"
"v_accvgpr_mov_b32 a32, a80\n"
"v_mfma_f32_16x16x32_bf16 v[184:187], v[128:131], a[20:23], v[184:187]\n"
"ds_read_b128 a[200:203], v243 offset:6144\n"
"v_accvgpr_mov_b32 a30, a78\n"
"v_mfma_f32_16x16x32_bf16 v[184:187], v[132:135], a[24:27], v[184:187]\n"
"s_add_u32 s40, s56, s53\n"
"v_sub_f32 v181, s74, v181\n"
"v_mfma_f32_16x16x32_bf16 v[184:187], v[136:139], a[28:31], v[184:187]\n"
"buffer_load_dwordx4 v240, s[20:23], s40 offen offset:2048 nt lds\n"
"v_mov_b32_dpp v247, v247 row_ror:12 row_mask:0x2 bank_mask:0xf\n"
"v_mov_b32_dpp v246, v246 row_ror:4 row_mask:0x8 bank_mask:0xf\n"
"v_accvgpr_mov_b32 a42, a90\n"
"v_sub_f32 v180, s74, v180\n"
"v_sub_f32 v183, s74, v183\n"
"v_mfma_f32_16x16x32_bf16 v[188:191], v[124:127], a[32:35], 0\n"
"v_mfma_f32_16x16x32_bf16 v[188:191], v[128:131], a[36:39], v[188:191]\n"
"v_accvgpr_mov_b32 a44, a92\n"
"v_mfma_f32_16x16x32_bf16 v[188:191], v[132:135], a[40:43], v[188:191]\n"
"v_sub_f32 v187, s74, v187\n"
"v_mfma_f32_16x16x32_bf16 v[188:191], v[136:139], a[44:47], v[188:191]\n"
"s_waitcnt lgkmcnt(1)\n"
"ds_read_b64_tr_b16 a[250:251], v236 offset:53248\n"
"v_sub_f32 v186, s74, v186\n"
"v_mfma_f32_16x16x32_bf16 v[192:195], v[124:127], a[192:195], 0\n"
"v_mfma_f32_16x16x32_bf16 v[192:195], v[128:131], a[196:199], v[192:195]\n"
"v_mul_f32_dpp v14, v246, v14 row_newbcast:2 row_mask:0xf bank_mask:0xf\n"
"s_waitcnt lgkmcnt(1)\n"
"v_mfma_f32_16x16x32_bf16 v[192:195], v[132:135], a[200:203], v[192:195]\n"
"ds_read_b64_tr_b16 a[252:253], v235 offset:50176\n"
"v_sub_f32 v182, s74, v182\n"
"v_mfma_f32_16x16x32_bf16 v[192:195], v[136:139], a[204:207], v[192:195]\n"
"ds_read_b64_tr_b16 v[226:227], v239 offset:54272\n"
"ds_read_b64_tr_b16 a[248:249], v236 offset:49152\n"
"v_xor_b32 v244, 0x11000, v242\n"
"v_mul_f32_dpp v12, v246, v12 row_newbcast:0 row_mask:0xf bank_mask:0xf\n"
"v_sub_f32 v185, s74, v185\n"
"v_sub_f32 v188, s74, v188\n"
"v_sub_f32 v189, s74, v189\n"
"v_sub_f32 v190, s74, v190\n"
"v_mul_f32_dpp v27, v246, v27 row_newbcast:3 row_mask:0xf bank_mask:0xf\n"
"v_mfma_f32_16x16x32_bf16 v[152:155], v[208:211], a[0:3], v[180:183]\n"
"v_mul_f32_dpp v10, v246, v10 row_newbcast:2 row_mask:0xf bank_mask:0xf\n"
"v_mfma_f32_16x16x32_bf16 v[152:155], v[212:215], a[4:7], v[152:155]\n"
"v_sub_f32 v184, s74, v184\n"
"v_mfma_f32_16x16x32_bf16 v[152:155], v[216:219], a[8:11], v[152:155]\n"
"s_add_u32 s40, s56, s54\n"
"v_sub_f32 v194, s74, v194\n"
"v_mfma_f32_16x16x32_bf16 v[152:155], v[220:223], a[12:15], v[152:155]\n"
"buffer_load_dwordx4 v240, s[20:23], s40 offen offset:3072 nt lds\n"
"v_mov_b32_dpp v206, v206 row_ror:12 row_mask:0x2 bank_mask:0xf\n"
"v_sub_f32 v191, s74, v191\n"
"v_sub_f32 v192, s74, v192\n"
"v_sub_f32 v193, s74, v193\n"
"v_sub_f32 v195, s74, v195\n"
"v_mfma_f32_16x16x32_bf16 v[156:159], v[216:219], a[24:27], v[184:187]\n"
"s_add_u32 s55, s55, s59\n"
"v_mov_b32_dpp v206, v206 row_ror:8 row_mask:0x4 bank_mask:0xf\n"
"v_mfma_f32_16x16x32_bf16 v[156:159], v[220:223], a[28:31], v[156:159]\n"
"s_add_u32 s41, s55, s57\n"
"v_mfma_f32_16x16x32_bf16 v[156:159], v[208:211], a[16:19], v[156:159]\n"
"s_and_b32 s41, s41, s58\n"
"ds_read_b64_tr_b16 v[224:225], v239 offset:50176\n"
"v_mov_b32_dpp v247, v247 row_ror:8 row_mask:0x4 bank_mask:0xf\n"
"v_mfma_f32_16x16x32_bf16 v[156:159], v[212:215], a[20:23], v[156:159]\n"
"s_andn2_b32 s56, s55, s58\n"
"v_mfma_f32_16x16x32_bf16 v[160:163], v[208:211], a[32:35], v[188:191]\n"
"s_or_b32 s56, s56, s41\n"
"ds_read_b64_tr_b16 a[254:255], v235 offset:54272\n"
"v_mfma_f32_16x16x32_bf16 v[160:163], v[212:215], a[36:39], v[160:163]\n"
"v_xor_b32 v250, 0x1000, v245\n"
"v_mfma_f32_16x16x32_bf16 v[160:163], v[216:219], a[40:43], v[160:163]\n"
"ds_read_b64_tr_b16 a[244:245], v232 offset:49152\n"
"v_mfma_f32_16x16x32_bf16 v[160:163], v[220:223], a[44:47], v[160:163]\n"
"v_mul_f32_dpp v25, v246, v25 row_newbcast:1 row_mask:0xf bank_mask:0xf\n"
"v_mfma_f32_16x16x32_bf16 v[164:167], v[216:219], a[200:203], v[192:195]\n"
"ds_read_b128 v[216:219], v228 offset:5120\n"
"v_mfma_f32_16x16x32_bf16 v[164:167], v[220:223], a[204:207], v[164:167]\n"
"v_mfma_f32_16x16x32_bf16 v[164:167], v[208:211], a[192:195], v[164:167]\n"
"ds_read_b128 v[220:223], v229 offset:5120\n"
"v_mfma_f32_16x16x32_bf16 v[164:167], v[212:215], a[196:199], v[164:167]\n"
"ds_read_b128 v[208:211], v228 offset:4096\n"
"v_mov_b32_dpp v206, v206 row_ror:4 row_mask:0x8 bank_mask:0xf\n"
"v_exp_f32 v161, v161\n"
"v_exp_f32 v162, v162\n"
"s_branch LTC_%=\n"
"LTH1_%=:\n"
"v_mfma_f32_16x16x32_bf16 a[224:227], v[124:127], a[244:247], a[224:227]\n"
"v_exp_f32 v168, v168\n"
"v_mfma_f32_16x16x32_bf16 v[16:19], v[128:131], a[244:247], v[16:19]\n"
"s_waitcnt vmcnt(8) lgkmcnt(0)\n"
"v_exp_f32 v169, v169\n"
"v_mfma_f32_16x16x32_bf16 v[16:19], v[132:135], a[248:251], v[16:19]\n"
"s_barrier\n"
"v_mfma_f32_16x16x32_bf16 v[48:51], v[136:139], a[244:247], v[48:51]\n"
"v_exp_f32 v172, v172\n"
"v_mfma_f32_16x16x32_bf16 v[48:51], v[140:143], a[248:251], v[48:51]\n"
"ds_read_b128 v[216:219], v228 offset:1024\n"
"v_cvt_pk_bf16_f32 v102, v168, v169\n"
"v_mfma_f32_16x16x32_bf16 v[80:83], v[144:147], a[244:247], v[80:83]\n"
"v_exp_f32 v176, v176\n"
"v_mfma_f32_16x16x32_bf16 v[80:83], v[148:151], a[248:251], v[80:83]\n"
"ds_read_b64_tr_b16 a[244:245], v234 offset:49152\n"
"ds_read_b64_tr_b16 a[246:247], v234 offset:53248\n"
"v_cvt_pk_bf16_f32 v123, v154, v155\n"
"v_cvt_pk_bf16_f32 v117, v166, v167\n"
"v_exp_f32 v170, v170\n"
"v_exp_f32 v175, v175\n"
"v_exp_f32 v177, v177\n"
"v_mfma_f32_16x16x32_bf16 v[84:87], v[148:151], v[224:227], v[84:87]\n"
"v_exp_f32 v156, v156\n"
"v_mfma_f32_16x16x32_bf16 v[84:87], v[144:147], a[252:255], v[84:87]\n"
"ds_read_b64_tr_b16 a[248:249], v238 offset:49152\n"
"ds_read_b64_tr_b16 a[250:251], v238 offset:53248\n"
"v_mfma_f32_16x16x32_bf16 v[52:55], v[140:143], v[224:227], v[52:55]\n"
"v_cvt_pk_bf16_f32 v101, v158, v159\n"
"v_cvt_pk_bf16_f32 v118, v176, v177\n"
"v_mfma_f32_16x16x32_bf16 v[52:55], v[136:139], a[252:255], v[52:55]\n"
"v_cvt_pk_bf16_f32 v100, v156, v157\n"
"v_exp_f32 v173, v173\n"
"v_exp_f32 v174, v174\n"
"v_exp_f32 v178, v178\n"
"v_exp_f32 v179, v179\n"
"v_mfma_f32_16x16x32_bf16 v[20:23], v[132:135], v[224:227], v[20:23]\n"
"v_exp_f32 v171, v171\n"
"v_mfma_f32_16x16x32_bf16 v[20:23], v[128:131], a[252:255], v[20:23]\n"
"ds_read_b64_tr_b16 v[224:225], v239 offset:49152\n"
"v_cvt_pk_bf16_f32 v103, v170, v171\n"
"v_mfma_f32_16x16x32_bf16 a[228:231], v[124:127], a[252:255], a[228:231]\n"
"s_waitcnt lgkmcnt(3)\n"
"ds_read_b64_tr_b16 a[252:253], v235 offset:49152\n"
"v_cvt_pk_bf16_f32 v119, v178, v179\n"
"v_mfma_f32_16x16x32_bf16 a[216:219], v[120:123], a[244:247], a[216:219]\n"
"s_add_u32 m0, s44, 81920\n"
"ds_read_b64_tr_b16 a[254:255], v235 offset:53248\n"
"ds_read_b64_tr_b16 v[226:227], v239 offset:53248\n"
"v_smfmac_f32_16x16x64_bf16 v[196:199], a[240:243], v[96:103], v241\n"
"buffer_load_dwordx4 v240, s[20:23], s56 offen nt lds\n"
"v_mfma_f32_16x16x32_bf16 v[8:11], v[96:99], a[244:247], v[8:11]\n"
"s_waitcnt lgkmcnt(4)\n"
"v_cvt_pk_bf16_f32 v109, v162, v163\n"
"v_cvt_pk_bf16_f32 v116, v164, v165\n"
"v_mfma_f32_16x16x32_bf16 v[8:11], v[100:103], a[248:251], v[8:11]\n"
"v_cvt_pk_bf16_f32 v110, v172, v173\n"
"v_cvt_pk_bf16_f32 v111, v174, v175\n"
"v_smfmac_f32_16x16x64_bf16 v[200:203], a[240:243], v[112:119], v241\n"
"ds_read_b128 v[220:223], v229 offset:1024\n"
"v_cvt_pk_bf16_f32 v108, v160, v161\n"
"v_mfma_f32_16x16x32_bf16 v[40:43], v[104:107], a[244:247], v[40:43]\n"
"v_add_f32 v196, v196, v197\n"
"v_and_b32 v245, 0x3f0, v242\n"
"v_mfma_f32_16x16x32_bf16 v[40:43], v[108:111], a[248:251], v[40:43]\n"
"ds_read_b128 v[212:215], v229 offset:0\n"
"v_lshrrev_b32 v245, 1, v245\n"
"v_smfmac_f32_16x16x64_bf16 v[198:201], a[240:243], v[104:111], v241\n"
"v_lshlrev_b32_e64 v244, 24, s43\n"
"v_sub_u32 v245, v242, v245\n"
"v_mfma_f32_16x16x32_bf16 v[72:75], v[112:115], a[244:247], v[72:75]\n"
"ds_read_b64_tr_b16 a[244:245], v234 offset:50176\n"
"ds_read_b64_tr_b16 a[246:247], v234 offset:54272\n"
"v_mfma_f32_16x16x32_bf16 v[72:75], v[116:119], a[248:251], v[72:75]\n"
"ds_read_b128 v[208:211], v228 offset:0\n"
"v_mul_f32 v244, 0x45800000, v244\n"
"v_mfma_f32_16x16x32_bf16 v[202:205], a[240:243], v[120:123], v[202:205]\n"
"s_waitcnt lgkmcnt(5)\n"
"ds_read_b64_tr_b16 a[250:251], v238 offset:54272\n"
"v_add_f32 v198, v198, v199\n"
"v_mfma_f32_16x16x32_bf16 v[76:79], v[116:119], v[224:227], v[76:79]\n"
"v_add_f32 v200, v200, v201\n"
"v_add_f32 v254, v196, v198\n"
"v_mfma_f32_16x16x32_bf16 v[76:79], v[112:115], a[252:255], v[76:79]\n"
"ds_read_b64_tr_b16 a[248:249], v238 offset:50176\n"
"v_add_f32 v254, v254, v200\n"
"v_mfma_f32_16x16x32_bf16 v[44:47], v[108:111], v[224:227], v[44:47]\n"
"v_add_f32 v244, v244, v202\n"
"v_min3_f32 v244, v244, v196, v198\n"
"v_mfma_f32_16x16x32_bf16 v[44:47], v[104:107], a[252:255], v[44:47]\n"
"v_min_f32 v244, v244, v200\n"
"v_rcp_f32 v244, v244\n"
"v_mfma_f32_16x16x32_bf16 v[12:15], v[100:103], v[224:227], v[12:15]\n"
"s_add_u32 s40, s56, s52\n"
"v_add_f32 v254, v254, v202\n"
"v_fmac_f32 v254, 0x68800000, v244\n"
"v_mfma_f32_16x16x32_bf16 v[12:15], v[96:99], a[252:255], v[12:15]\n"
"buffer_load_dwordx4 v240, s[20:23], s40 offen offset:1024 nt lds\n"
"v_mfma_f32_16x16x32_bf16 a[220:223], v[120:123], a[252:255], a[220:223]\n"
"s_waitcnt lgkmcnt(2)\n"
"v_cmp_nge_f32 vcc, s45, v254\n"
"s_cbranch_vccnz LSLOW_%=\nLBACK1_%=:\ns_mov_b64 vcc, s[64:65]\n"
"v_mfma_f32_16x16x32_bf16 a[232:235], v[120:123], a[244:247], a[232:235]\n"
"v_rcp_f32 v246, v196\n"
"v_rcp_f32 v206, v200\n"
"v_mov_b32 v252, v192\n"
"v_accvgpr_mov_b32 a9, a105\n"
"v_accvgpr_mov_b32 a10, a106\n"
"v_accvgpr_mov_b32 a28, a124\n"
"v_accvgpr_mov_b32 a31, a127\n"
"v_accvgpr_mov_b32 a33, a129\n"
"v_accvgpr_mov_b32 a40, a136\n"
"v_bfe_u32 v149, v242, 8, 2\n"
"v_lshl_add_u32 v149, v149, 4, s43\n"
"v_xor_b32 v150, 64, v149\n"
"v_accvgpr_mov_b32 a105, a57\n"
"v_mfma_f32_16x16x32_bf16 v[24:27], v[96:99], a[244:247], v[24:27]\n"
"s_waitcnt lgkmcnt(0)\n"
"ds_read_b128 v[128:131], v150 offset:0\n"
"v_accvgpr_mov_b32 a12, a108\n"
"v_mfma_f32_16x16x32_bf16 v[24:27], v[100:103], a[248:251], v[24:27]\n"
"ds_read_b128 v[124:127], v149 offset:0\n"
"v_accvgpr_mov_b32 a0, a96\n"
"v_accvgpr_mov_b32 a3, a99\n"
"v_accvgpr_mov_b32 a4, a100\n"
"v_accvgpr_mov_b32 a18, a114\n"
"v_accvgpr_mov_b32 a21, a117\n"
"v_accvgpr_mov_b32 a35, a131\n"
"v_accvgpr_mov_b32 a38, a134\n"
"v_accvgpr_mov_b32 a47, a143\n"
"v_accvgpr_mov_b32 a99, a51\n"
"v_mfma_f32_16x16x32_bf16 v[56:59], v[104:107], a[244:247], v[56:59]\n"
"ds_read_b128 v[132:135], v149 offset:1024\n"
"v_rcp_f32 v247, v198\n"
"v_accvgpr_mov_b32 a1, a97\n"
"v_accvgpr_mov_b32 a2, a98\n"
"v_accvgpr_mov_b32 a11, a107\n"
"v_accvgpr_mov_b32 a13, a109\n"
"v_accvgpr_mov_b32 a14, a110\n"
"v_accvgpr_mov_b32 a16, a112\n"
"v_accvgpr_mov_b32 a20, a116\n"
"v_accvgpr_mov_b32 a22, a118\n"
"v_accvgpr_mov_b32 a25, a121\n"
"v_accvgpr_mov_b32 a36, a132\n"
"v_xor_b32 v148, 0x400, v243\n"
"v_mfma_f32_16x16x32_bf16 v[56:59], v[108:111], a[248:251], v[56:59]\n"
"ds_read_b128 v[136:139], v150 offset:1024\n"
"v_mov_b32_dpp v246, v246 row_ror:12 row_mask:0x2 bank_mask:0xf\n"
"v_accvgpr_mov_b32 a6, a102\n"
"v_accvgpr_mov_b32 a7, a103\n"
"v_accvgpr_mov_b32 a8, a104\n"
"v_accvgpr_mov_b32 a24, a120\n"
"v_accvgpr_mov_b32 a29, a125\n"
"v_accvgpr_mov_b32 a34, a130\n"
"v_accvgpr_mov_b32 a39, a135\n"
"v_accvgpr_mov_b32 a43, a139\n"
"v_accvgpr_mov_b32 a45, a141\n"
"v_accvgpr_mov_b32 a103, a55\n"
"v_mfma_f32_16x16x32_bf16 v[88:91], v[112:115], a[244:247], v[88:91]\n"
"v_accvgpr_mov_b32 a19, a115\n"
"v_accvgpr_mov_b32 a129, a81\n"
"v_mfma_f32_16x16x32_bf16 v[88:91], v[116:119], a[248:251], v[88:91]\n"
"s_waitcnt lgkmcnt(2)\n"
"ds_read_b128 a[192:195], v243 offset:4096\n"
"v_accvgpr_mov_b32 a5, a101\n"
"v_accvgpr_mov_b32 a17, a113\n"
"v_accvgpr_mov_b32 a23, a119\n"
"v_accvgpr_mov_b32 a26, a122\n"
"v_accvgpr_mov_b32 a27, a123\n"
"v_accvgpr_mov_b32 a37, a133\n"
"v_accvgpr_mov_b32 a46, a142\n"
"v_accvgpr_mov_b32 a101, a53\n"
"v_accvgpr_mov_b32 a124, a76\n"
"v_accvgpr_mov_b32 a121, a73\n"
"v_mfma_f32_16x16x32_bf16 v[180:183], v[124:127], a[0:3], 0\n"
"ds_read_b128 a[196:199], v148 offset:4096\n"
"v_accvgpr_mov_b32 a15, a111\n"
"v_mfma_f32_16x16x32_bf16 v[180:183], v[128:131], a[4:7], v[180:183]\n"
"s_waitcnt lgkmcnt(3)\n"
"v_accvgpr_mov_b32 a117, a69\n"
"v_accvgpr_mov_b32 a119, a71\n"
"v_mfma_f32_16x16x32_bf16 v[180:183], v[132:135], a[8:11], v[180:183]\n"
"s_waitcnt lgkmcnt(2)\n"
"v_accvgpr_mov_b32 a41, a137\n"
"v_accvgpr_mov_b32 a123, a75\n"
"v_mfma_f32_16x16x32_bf16 v[180:183], v[136:139], a[12:15], v[180:183]\n"
"ds_read_b128 a[204:207], v148 offset:6144\n"
"v_mov_b32_dpp v246, v246 row_ror:8 row_mask:0x4 bank_mask:0xf\n"
"v_mfma_f32_16x16x32_bf16 v[184:187], v[124:127], a[16:19], 0\n"
"v_accvgpr_mov_b32 a32, a128\n"
"v_accvgpr_mov_b32 a112, a64\n"
"v_mfma_f32_16x16x32_bf16 v[184:187], v[128:131], a[20:23], v[184:187]\n"
"ds_read_b128 a[200:203], v243 offset:6144\n"
"v_accvgpr_mov_b32 a30, a126\n"
"v_mfma_f32_16x16x32_bf16 v[184:187], v[132:135], a[24:27], v[184:187]\n"
"s_add_u32 s40, s56, s53\n"
"v_accvgpr_mov_b32 a104, a56\n"
"v_sub_f32 v181, s74, v181\n"
"v_mfma_f32_16x16x32_bf16 v[184:187], v[136:139], a[28:31], v[184:187]\n"
"buffer_load_dwordx4 v240, s[20:23], s40 offen offset:2048 nt lds\n"
"v_mov_b32_dpp v247, v247 row_ror:12 row_mask:0x2 bank_mask:0xf\n"
"v_mov_b32_dpp v246, v246 row_ror:4 row_mask:0x8 bank_mask:0xf\n"
"v_accvgpr_mov_b32 a42, a138\n"
"v_accvgpr_mov_b32 a133, a85\n"
"v_accvgpr_mov_b32 a111, a63\n"
"v_accvgpr_mov_b32 a143, a95\n"
"v_accvgpr_mov_b32 a130, a82\n"
"v_sub_f32 v180, s74, v180\n"
"v_sub_f32 v183, s74, v183\n"
"v_mfma_f32_16x16x32_bf16 v[188:191], v[124:127], a[32:35], 0\n"
"v_accvgpr_mov_b32 a120, a72\n"
"v_accvgpr_mov_b32 a139, a91\n"
"v_mfma_f32_16x16x32_bf16 v[188:191], v[128:131], a[36:39], v[188:191]\n"
"v_accvgpr_mov_b32 a44, a140\n"
"v_accvgpr_mov_b32 a132, a84\n"
"v_mfma_f32_16x16x32_bf16 v[188:191], v[132:135], a[40:43], v[188:191]\n"
"v_accvgpr_mov_b32 a108, a60\n"
"v_sub_f32 v187, s74, v187\n"
"v_mfma_f32_16x16x32_bf16 v[188:191], v[136:139], a[44:47], v[188:191]\n"
"s_waitcnt lgkmcnt(1)\n"
"ds_read_b64_tr_b16 a[250:251], v236 offset:53248\n"
"v_sub_f32 v186, s74, v186\n"
"v_mfma_f32_16x16x32_bf16 v[192:195], v[124:127], a[192:195], 0\n"
"v_accvgpr_mov_b32 a115, a67\n"
"v_accvgpr_mov_b32 a122, a74\n"
"v_mfma_f32_16x16x32_bf16 v[192:195], v[128:131], a[196:199], v[192:195]\n"
"s_waitcnt lgkmcnt(1)\n"
"v_accvgpr_mov_b32 a102, a54\n"
"v_mul_f32_dpp v14, v246, v14 row_newbcast:2 row_mask:0xf bank_mask:0xf\n"
"v_mfma_f32_16x16x32_bf16 v[192:195], v[132:135], a[200:203], v[192:195]\n"
"ds_read_b64_tr_b16 a[252:253], v235 offset:50176\n"
"v_sub_f32 v182, s74, v182\n"
"v_mfma_f32_16x16x32_bf16 v[192:195], v[136:139], a[204:207], v[192:195]\n"
"ds_read_b64_tr_b16 v[226:227], v239 offset:54272\n"
"ds_read_b64_tr_b16 a[248:249], v236 offset:49152\n"
"v_xor_b32 v244, 0x11000, v242\n"
"v_accvgpr_mov_b32 a100, a52\n"
"v_accvgpr_mov_b32 a142, a94\n"
"v_mul_f32_dpp v12, v246, v12 row_newbcast:0 row_mask:0xf bank_mask:0xf\n"
"v_sub_f32 v185, s74, v185\n"
"v_sub_f32 v188, s74, v188\n"
"v_sub_f32 v189, s74, v189\n"
"v_sub_f32 v190, s74, v190\n"
"v_mul_f32_dpp v27, v246, v27 row_newbcast:3 row_mask:0xf bank_mask:0xf\n"
"v_mfma_f32_16x16x32_bf16 v[152:155], v[208:211], a[0:3], v[180:183]\n"
"v_accvgpr_mov_b32 a135, a87\n"
"v_mul_f32_dpp v10, v246, v10 row_newbcast:2 row_mask:0xf bank_mask:0xf\n"
"v_mfma_f32_16x16x32_bf16 v[152:155], v[212:215], a[4:7], v[152:155]\n"
"v_accvgpr_mov_b32 a126, a78\n"
"v_sub_f32 v184, s74, v184\n"
"v_mfma_f32_16x16x32_bf16 v[152:155], v[216:219], a[8:11], v[152:155]\n"
"s_add_u32 s40, s56, s54\n"
"v_accvgpr_mov_b32 a110, a62\n"
"v_sub_f32 v194, s74, v194\n"
"v_mfma_f32_16x16x32_bf16 v[152:155], v[220:223], a[12:15], v[152:155]\n"
"buffer_load_dwordx4 v240, s[20:23], s40 offen offset:3072 nt lds\n"
"v_mov_b32_dpp v206, v206 row_ror:12 row_mask:0x2 bank_mask:0xf\n"
"v_accvgpr_mov_b32 a134, a86\n"
"v_accvgpr_mov_b32 a113, a65\n"
"v_accvgpr_mov_b32 a128, a80\n"
"v_accvgpr_mov_b32 a107, a59\n"
"v_sub_f32 v191, s74, v191\n"
"v_sub_f32 v192, s74, v192\n"
"v_sub_f32 v193, s74, v193\n"
"v_sub_f32 v195, s74, v195\n"
"v_mfma_f32_16x16x32_bf16 v[156:159], v[216:219], a[24:27], v[184:187]\n"
"s_add_u32 s55, s55, s59\n"
"v_mov_b32_dpp v206, v206 row_ror:8 row_mask:0x4 bank_mask:0xf\n"
"v_accvgpr_mov_b32 a138, a90\n"
"v_mfma_f32_16x16x32_bf16 v[156:159], v[220:223], a[28:31], v[156:159]\n"
"s_add_u32 s41, s55, s57\n"
"v_accvgpr_mov_b32 a140, a92\n"
"v_accvgpr_mov_b32 a97, a49\n"
"v_mfma_f32_16x16x32_bf16 v[156:159], v[208:211], a[16:19], v[156:159]\n"
"s_and_b32 s41, s41, s58\n"
"ds_read_b64_tr_b16 v[224:225], v239 offset:50176\n"
"v_mov_b32_dpp v247, v247 row_ror:8 row_mask:0x4 bank_mask:0xf\n"
"v_mfma_f32_16x16x32_bf16 v[156:159], v[212:215], a[20:23], v[156:159]\n"
"s_andn2_b32 s56, s55, s58\n"
"v_accvgpr_mov_b32 a96, a48\n"
"v_accvgpr_mov_b32 a98, a50\n"
"v_mfma_f32_16x16x32_bf16 v[160:163], v[208:211], a[32:35], v[188:191]\n"
"s_or_b32 s56, s56, s41\n"
"ds_read_b64_tr_b16 a[254:255], v235 offset:54272\n"
"v_accvgpr_mov_b32 a114, a66\n"
"v_mfma_f32_16x16x32_bf16 v[160:163], v[212:215], a[36:39], v[160:163]\n"
"v_xor_b32 v250, 0x1000, v245\n"
"v_accvgpr_mov_b32 a141, a93\n"
"v_mfma_f32_16x16x32_bf16 v[160:163], v[216:219], a[40:43], v[160:163]\n"
"ds_read_b64_tr_b16 a[244:245], v232 offset:49152\n"
"v_accvgpr_mov_b32 a118, a70\n"
"v_mfma_f32_16x16x32_bf16 v[160:163], v[220:223], a[44:47], v[160:163]\n"
"v_accvgpr_mov_b32 a127, a79\n"
"v_mul_f32_dpp v25, v246, v25 row_newbcast:1 row_mask:0xf bank_mask:0xf\n"
"v_mfma_f32_16x16x32_bf16 v[164:167], v[216:219], a[200:203], v[192:195]\n"
"ds_read_b128 v[216:219], v228 offset:5120\n"
"v_accvgpr_mov_b32 a116, a68\n"
"v_mfma_f32_16x16x32_bf16 v[164:167], v[220:223], a[204:207], v[164:167]\n"
"v_accvgpr_mov_b32 a125, a77\n"
"v_accvgpr_mov_b32 a106, a58\n"
"v_mfma_f32_16x16x32_bf16 v[164:167], v[208:211], a[192:195], v[164:167]\n"
"ds_read_b128 v[220:223], v229 offset:5120\n"
"v_accvgpr_mov_b32 a136, a88\n"
"v_mfma_f32_16x16x32_bf16 v[164:167], v[212:215], a[196:199], v[164:167]\n"
"s_waitcnt lgkmcnt(4)\n"
"ds_read_b128 v[208:211], v228 offset:4096\n"
"v_mov_b32_dpp v206, v206 row_ror:4 row_mask:0x8 bank_mask:0xf\n"
"v_accvgpr_mov_b32 a109, a61\n"
"v_accvgpr_mov_b32 a131, a83\n"
"v_accvgpr_mov_b32 a137, a89\n"
"v_exp_f32 v161, v161\n"
"v_exp_f32 v162, v162\n"
"s_branch LTC_%=\n"
"LTH2_%=:\n"
"v_mfma_f32_16x16x32_bf16 a[224:227], v[124:127], a[244:247], a[224:227]\n"
"v_exp_f32 v168, v168\n"
"v_mfma_f32_16x16x32_bf16 v[16:19], v[128:131], a[244:247], v[16:19]\n"
"s_waitcnt vmcnt(8) lgkmcnt(0)\n"
"v_exp_f32 v169, v169\n"
"v_mfma_f32_16x16x32_bf16 v[16:19], v[132:135], a[248:251], v[16:19]\n"
"s_barrier\n"
"v_mfma_f32_16x16x32_bf16 v[48:51], v[136:139], a[244:247], v[48:51]\n"
"v_exp_f32 v172, v172\n"
"v_mfma_f32_16x16x32_bf16 v[48:51], v[140:143], a[248:251], v[48:51]\n"
"ds_read_b128 v[216:219], v228 offset:1024\n"
"v_cvt_pk_bf16_f32 v102, v168, v169\n"
"v_mfma_f32_16x16x32_bf16 v[80:83], v[144:147], a[244:247], v[80:83]\n"
"v_exp_f32 v176, v176\n"
"v_mfma_f32_16x16x32_bf16 v[80:83], v[148:151], a[248:251], v[80:83]\n"
"ds_read_b64_tr_b16 a[244:245], v234 offset:49152\n"
"ds_read_b64_tr_b16 a[246:247], v234 offset:53248\n"
"v_cvt_pk_bf16_f32 v123, v154, v155\n"
"v_cvt_pk_bf16_f32 v117, v166, v167\n"
"v_exp_f32 v170, v170\n"
"v_exp_f32 v175, v175\n"
"v_exp_f32 v177, v177\n"
"v_mfma_f32_16x16x32_bf16 v[84:87], v[148:151], v[224:227], v[84:87]\n"
"v_exp_f32 v156, v156\n"
"v_mfma_f32_16x16x32_bf16 v[84:87], v[144:147], a[252:255], v[84:87]\n"
"ds_read_b64_tr_b16 a[248:249], v238 offset:49152\n"
"ds_read_b64_tr_b16 a[250:251], v238 offset:53248\n"
"v_mfma_f32_16x16x32_bf16 v[52:55], v[140:143], v[224:227], v[52:55]\n"
"v_cvt_pk_bf16_f32 v101, v158, v159\n"
"v_cvt_pk_bf16_f32 v118, v176, v177\n"
"v_mfma_f32_16x16x32_bf16 v[52:55], v[136:139], a[252:255], v[52:55]\n"
"v_cvt_pk_bf16_f32 v100, v156, v157\n"
"v_exp_f32 v173, v173\n"
"v_exp_f32 v174, v174\n"
"v_exp_f32 v178, v178\n"
"v_exp_f32 v179, v179\n"
"v_mfma_f32_16x16x32_bf16 v[20:23], v[132:135], v[224:227], v[20:23]\n"
"v_exp_f32 v171, v171\n"
"v_mfma_f32_16x16x32_bf16 v[20:23], v[128:131], a[252:255], v[20:23]\n"
"ds_read_b64_tr_b16 v[224:225], v239 offset:49152\n"
"v_cvt_pk_bf16_f32 v103, v170, v171\n"
"v_mfma_f32_16x16x32_bf16 a[228:231], v[124:127], a[252:255], a[228:231]\n"
"s_waitcnt lgkmcnt(3)\n"
"ds_read_b64_tr_b16 a[252:253], v235 offset:49152\n"
"v_cvt_pk_bf16_f32 v119, v178, v179\n"
"v_mfma_f32_16x16x32_bf16 a[216:219], v[120:123], a[244:247], a[216:219]\n"
"s_add_u32 m0, s44, 81920\n"
"ds_read_b64_tr_b16 a[254:255], v235 offset:53248\n"
"ds_read_b64_tr_b16 v[226:227], v239 offset:53248\n"
"v_smfmac_f32_16x16x64_bf16 v[196:199], a[240:243], v[96:103], v241\n"
"buffer_load_dwordx4 v240, s[20:23], s56 offen nt lds\n"
"v_mfma_f32_16x16x32_bf16 v[8:11], v[96:99], a[244:247], v[8:11]\n"
"s_waitcnt lgkmcnt(4)\n"
"v_cvt_pk_bf16_f32 v109, v162, v163\n"
"v_cvt_pk_bf16_f32 v116, v164, v165\n"
"v_mfma_f32_16x16x32_bf16 v[8:11], v[100:103], a[248:251], v[8:11]\n"
"v_cvt_pk_bf16_f32 v110, v172, v173\n"
"v_cvt_pk_bf16_f32 v111, v174, v175\n"
"v_smfmac_f32_16x16x64_bf16 v[200:203], a[240:243], v[112:119], v241\n"
"ds_read_b128 v[220:223], v229 offset:1024\n"
"v_cvt_pk_bf16_f32 v108, v160, v161\n"
"v_mfma_f32_16x16x32_bf16 v[40:43], v[104:107], a[244:247], v[40:43]\n"
"v_add_f32 v196, v196, v197\n"
"v_and_b32 v245, 0x3f0, v242\n"
"v_mfma_f32_16x16x32_bf16 v[40:43], v[108:111], a[248:251], v[40:43]\n"
"ds_read_b128 v[212:215], v229 offset:0\n"
"v_lshrrev_b32 v245, 1, v245\n"
"v_smfmac_f32_16x16x64_bf16 v[198:201], a[240:243], v[104:111], v241\n"
"v_lshlrev_b32_e64 v244, 24, s43\n"
"v_sub_u32 v245, v242, v245\n"
"v_mfma_f32_16x16x32_bf16 v[72:75], v[112:115], a[244:247], v[72:75]\n"
"ds_read_b64_tr_b16 a[244:245], v234 offset:50176\n"
"ds_read_b64_tr_b16 a[246:247], v234 offset:54272\n"
"v_mfma_f32_16x16x32_bf16 v[72:75], v[116:119], a[248:251], v[72:75]\n"
"ds_read_b128 v[208:211], v228 offset:0\n"
"v_mul_f32 v244, 0x45800000, v244\n"
"v_mfma_f32_16x16x32_bf16 v[202:205], a[240:243], v[120:123], v[202:205]\n"
"s_waitcnt lgkmcnt(5)\n"
"ds_read_b64_tr_b16 a[250:251], v238 offset:54272\n"
"v_add_f32 v198, v198, v199\n"
"v_mfma_f32_16x16x32_bf16 v[76:79], v[116:119], v[224:227], v[76:79]\n"
"v_add_f32 v200, v200, v201\n"
"v_add_f32 v254, v196, v198\n"
"v_mfma_f32_16x16x32_bf16 v[76:79], v[112:115], a[252:255], v[76:79]\n"
"ds_read_b64_tr_b16 a[248:249], v238 offset:50176\n"
"v_add_f32 v254, v254, v200\n"
"v_mfma_f32_16x16x32_bf16 v[44:47], v[108:111], v[224:227], v[44:47]\n"
"v_add_f32 v244, v244, v202\n"
"v_min3_f32 v244, v244, v196, v198\n"
"v_mfma_f32_16x16x32_bf16 v[44:47], v[104:107], a[252:255], v[44:47]\n"
"v_min_f32 v244, v244, v200\n"
"v_rcp_f32 v244, v244\n"
"v_mfma_f32_16x16x32_bf16 v[12:15], v[100:103], v[224:227], v[12:15]\n"
"s_add_u32 s40, s56, s52\n"
"v_add_f32 v254, v254, v202\n"
"v_fmac_f32 v254, 0x68800000, v244\n"
"v_mfma_f32_16x16x32_bf16 v[12:15], v[96:99], a[252:255], v[12:15]\n"
"buffer_load_dwordx4 v240, s[20:23], s40 offen offset:1024 nt lds\n"
"v_mfma_f32_16x16x32_bf16 a[220:223], v[120:123], a[252:255], a[220:223]\n"
"s_waitcnt lgkmcnt(2)\n"
"v_cmp_nge_f32 vcc, s45, v254\n"
"s_cbranch_vccnz LSLOW_%=\nLBACK2_%=:\ns_mov_b64 vcc, s[64:65]\n"
"v_mfma_f32_16x16x32_bf16 a[232:235], v[120:123], a[244:247], a[232:235]\n"
"v_rcp_f32 v246, v196\n"
"v_rcp_f32 v206, v200\n"
"v_mov_b32 v252, v192\n"
"v_accvgpr_mov_b32 a9, a153\n"
"v_accvgpr_mov_b32 a10, a154\n"
"v_accvgpr_mov_b32 a28, a172\n"
"v_accvgpr_mov_b32 a31, a175\n"
"v_accvgpr_mov_b32 a33, a177\n"
"v_accvgpr_mov_b32 a40, a184\n"
"v_bfe_u32 v149, v242, 8, 2\n"
"v_lshl_add_u32 v149, v149, 4, s43\n"
"v_xor_b32 v150, 64, v149\n"
"v_accvgpr_mov_b32 a153, a57\n"
"v_mfma_f32_16x16x32_bf16 v[24:27], v[96:99], a[244:247], v[24:27]\n"
"s_waitcnt lgkmcnt(0)\n"
"ds_read_b128 v[128:131], v150 offset:0\n"
"v_accvgpr_mov_b32 a12, a156\n"
"v_mfma_f32_16x16x32_bf16 v[24:27], v[100:103], a[248:251], v[24:27]\n"
"ds_read_b128 v[124:127], v149 offset:0\n"
"v_accvgpr_mov_b32 a0, a144\n"
"v_accvgpr_mov_b32 a3, a147\n"
"v_accvgpr_mov_b32 a4, a148\n"
"v_accvgpr_mov_b32 a18, a162\n"
"v_accvgpr_mov_b32 a21, a165\n"
"v_accvgpr_mov_b32 a35, a179\n"
"v_accvgpr_mov_b32 a38, a182\n"
"v_accvgpr_mov_b32 a47, a191\n"
"v_accvgpr_mov_b32 a147, a51\n"
"v_mfma_f32_16x16x32_bf16 v[56:59], v[104:107], a[244:247], v[56:59]\n"
"ds_read_b128 v[132:135], v149 offset:1024\n"
"v_rcp_f32 v247, v198\n"
"v_accvgpr_mov_b32 a1, a145\n"
"v_accvgpr_mov_b32 a2, a146\n"
"v_accvgpr_mov_b32 a11, a155\n"
"v_accvgpr_mov_b32 a13, a157\n"
"v_accvgpr_mov_b32 a14, a158\n"
"v_accvgpr_mov_b32 a16, a160\n"
"v_accvgpr_mov_b32 a20, a164\n"
"v_accvgpr_mov_b32 a22, a166\n"
"v_accvgpr_mov_b32 a25, a169\n"
"v_accvgpr_mov_b32 a36, a180\n"
"v_xor_b32 v148, 0x400, v243\n"
"v_mfma_f32_16x16x32_bf16 v[56:59], v[108:111], a[248:251], v[56:59]\n"
"ds_read_b128 v[136:139], v150 offset:1024\n"
"v_mov_b32_dpp v246, v246 row_ror:12 row_mask:0x2 bank_mask:0xf\n"
"v_accvgpr_mov_b32 a6, a150\n"
"v_accvgpr_mov_b32 a7, a151\n"
"v_accvgpr_mov_b32 a8, a152\n"
"v_accvgpr_mov_b32 a24, a168\n"
"v_accvgpr_mov_b32 a29, a173\n"
"v_accvgpr_mov_b32 a34, a178\n"
"v_accvgpr_mov_b32 a39, a183\n"
"v_accvgpr_mov_b32 a43, a187\n"
"v_accvgpr_mov_b32 a45, a189\n"
"v_accvgpr_mov_b32 a151, a55\n"
"v_mfma_f32_16x16x32_bf16 v[88:91], v[112:115], a[244:247], v[88:91]\n"
"v_accvgpr_mov_b32 a19, a163\n"
"v_accvgpr_mov_b32 a177, a81\n"
"v_mfma_f32_16x16x32_bf16 v[88:91], v[116:119], a[248:251], v[88:91]\n"
"s_waitcnt lgkmcnt(2)\n"
"ds_read_b128 a[192:195], v243 offset:4096\n"
"v_accvgpr_mov_b32 a5, a149\n"
"v_accvgpr_mov_b32 a17, a161\n"
"v_accvgpr_mov_b32 a23, a167\n"
"v_accvgpr_mov_b32 a26, a170\n"
"v_accvgpr_mov_b32 a27, a171\n"
"v_accvgpr_mov_b32 a37, a181\n"
"v_accvgpr_mov_b32 a46, a190\n"
"v_accvgpr_mov_b32 a149, a53\n"
"v_accvgpr_mov_b32 a172, a76\n"
"v_accvgpr_mov_b32 a169, a73\n"
"v_mfma_f32_16x16x32_bf16 v[180:183], v[124:127], a[0:3], 0\n"
"ds_read_b128 a[196:199], v148 offset:4096\n"
"v_accvgpr_mov_b32 a15, a159\n"
"v_mfma_f32_16x16x32_bf16 v[180:183], v[128:131], a[4:7], v[180:183]\n"
"s_waitcnt lgkmcnt(3)\n"
"v_accvgpr_mov_b32 a165, a69\n"
"v_accvgpr_mov_b32 a167, a71\n"
"v_mfma_f32_16x16x32_bf16 v[180:183], v[132:135], a[8:11], v[180:183]\n"
"s_waitcnt lgkmcnt(2)\n"
"v_accvgpr_mov_b32 a41, a185\n"
"v_accvgpr_mov_b32 a171, a75\n"
"v_mfma_f32_16x16x32_bf16 v[180:183], v[136:139], a[12:15], v[180:183]\n"
"ds_read_b128 a[204:207], v148 offset:6144\n"
"v_mov_b32_dpp v246, v246 row_ror:8 row_mask:0x4 bank_mask:0xf\n"
"v_mfma_f32_16x16x32_bf16 v[184:187], v[124:127], a[16:19], 0\n"
"v_accvgpr_mov_b32 a32, a176\n"
"v_accvgpr_mov_b32 a160, a64\n"
"v_mfma_f32_16x16x32_bf16 v[184:187], v[128:131], a[20:23], v[184:187]\n"
"ds_read_b128 a[200:203], v243 offset:6144\n"
"v_accvgpr_mov_b32 a30, a174\n"
"v_mfma_f32_16x16x32_bf16 v[184:187], v[132:135], a[24:27], v[184:187]\n"
"s_add_u32 s40, s56, s53\n"
"v_accvgpr_mov_b32 a152, a56\n"
"v_sub_f32 v181, s74, v181\n"
"v_mfma_f32_16x16x32_bf16 v[184:187], v[136:139], a[28:31], v[184:187]\n"
"buffer_load_dwordx4 v240, s[20:23], s40 offen offset:2048 nt lds\n"
"v_mov_b32_dpp v247, v247 row_ror:12 row_mask:0x2 bank_mask:0xf\n"
"v_mov_b32_dpp v246, v246 row_ror:4 row_mask:0x8 bank_mask:0xf\n"
"v_accvgpr_mov_b32 a42, a186\n"
"v_accvgpr_mov_b32 a181, a85\n"
"v_accvgpr_mov_b32 a159, a63\n"
"v_accvgpr_mov_b32 a191, a95\n"
"v_accvgpr_mov_b32 a178, a82\n"
"v_sub_f32 v180, s74, v180\n"
"v_sub_f32 v183, s74, v183\n"
"v_mfma_f32_16x16x32_bf16 v[188:191], v[124:127], a[32:35], 0\n"
"v_accvgpr_mov_b32 a168, a72\n"
"v_accvgpr_mov_b32 a187, a91\n"
"v_mfma_f32_16x16x32_bf16 v[188:191], v[128:131], a[36:39], v[188:191]\n"
"v_accvgpr_mov_b32 a44, a188\n"
"v_accvgpr_mov_b32 a180, a84\n"
"v_mfma_f32_16x16x32_bf16 v[188:191], v[132:135], a[40:43], v[188:191]\n"
"v_accvgpr_mov_b32 a156, a60\n"
"v_sub_f32 v187, s74, v187\n"
"v_mfma_f32_16x16x32_bf16 v[188:191], v[136:139], a[44:47], v[188:191]\n"
"s_waitcnt lgkmcnt(1)\n"
"ds_read_b64_tr_b16 a[250:251], v236 offset:53248\n"
"v_sub_f32 v186, s74, v186\n"
"v_mfma_f32_16x16x32_bf16 v[192:195], v[124:127], a[192:195], 0\n"
"v_accvgpr_mov_b32 a163, a67\n"
"v_accvgpr_mov_b32 a170, a74\n"
"v_mfma_f32_16x16x32_bf16 v[192:195], v[128:131], a[196:199], v[192:195]\n"
"s_waitcnt lgkmcnt(1)\n"
"v_accvgpr_mov_b32 a150, a54\n"
"v_mul_f32_dpp v14, v246, v14 row_newbcast:2 row_mask:0xf bank_mask:0xf\n"
"v_mfma_f32_16x16x32_bf16 v[192:195], v[132:135], a[200:203], v[192:195]\n"
"ds_read_b64_tr_b16 a[252:253], v235 offset:50176\n"
"v_sub_f32 v182, s74, v182\n"
"v_mfma_f32_16x16x32_bf16 v[192:195], v[136:139], a[204:207], v[192:195]\n"
"ds_read_b64_tr_b16 v[226:227], v239 offset:54272\n"
"ds_read_b64_tr_b16 a[248:249], v236 offset:49152\n"
"v_xor_b32 v244, 0x11000, v242\n"
"v_accvgpr_mov_b32 a148, a52\n"
"v_accvgpr_mov_b32 a190, a94\n"
"v_mul_f32_dpp v12, v246, v12 row_newbcast:0 row_mask:0xf bank_mask:0xf\n"
"v_sub_f32 v185, s74, v185\n"
"v_sub_f32 v188, s74, v188\n"
"v_sub_f32 v189, s74, v189\n"
"v_sub_f32 v190, s74, v190\n"
"v_mul_f32_dpp v27, v246, v27 row_newbcast:3 row_mask:0xf bank_mask:0xf\n"
"v_mfma_f32_16x16x32_bf16 v[152:155], v[208:211], a[0:3], v[180:183]\n"
"v_accvgpr_mov_b32 a183, a87\n"
"v_mul_f32_dpp v10, v246, v10 row_newbcast:2 row_mask:0xf bank_mask:0xf\n"
"v_mfma_f32_16x16x32_bf16 v[152:155], v[212:215], a[4:7], v[152:155]\n"
"v_accvgpr_mov_b32 a174, a78\n"
"v_sub_f32 v184, s74, v184\n"
"v_mfma_f32_16x16x32_bf16 v[152:155], v[216:219], a[8:11], v[152:155]\n"
"s_add_u32 s40, s56, s54\n"
"v_accvgpr_mov_b32 a158, a62\n"
"v_sub_f32 v194, s74, v194\n"
"v_mfma_f32_16x16x32_bf16 v[152:155], v[220:223], a[12:15], v[152:155]\n"
"buffer_load_dwordx4 v240, s[20:23], s40 offen offset:3072 nt lds\n"
"v_mov_b32_dpp v206, v206 row_ror:12 row_mask:0x2 bank_mask:0xf\n"
"v_accvgpr_mov_b32 a182, a86\n"
"v_accvgpr_mov_b32 a161, a65\n"
"v_accvgpr_mov_b32 a176, a80\n"
"v_accvgpr_mov_b32 a155, a59\n"
"v_sub_f32 v191, s74, v191\n"
"v_sub_f32 v192, s74, v192\n"
"v_sub_f32 v193, s74, v193\n"
"v_sub_f32 v195, s74, v195\n"
"v_mfma_f32_16x16x32_bf16 v[156:159], v[216:219], a[24:27], v[184:187]\n"
"s_add_u32 s55, s55, s59\n"
"v_mov_b32_dpp v206, v206 row_ror:8 row_mask:0x4 bank_mask:0xf\n"
"v_accvgpr_mov_b32 a186, a90\n"
"v_mfma_f32_16x16x32_bf16 v[156:159], v[220:223], a[28:31], v[156:159]\n"
"s_add_u32 s41, s55, s57\n"
"v_accvgpr_mov_b32 a188, a92\n"
"v_accvgpr_mov_b32 a145, a49\n"
"v_mfma_f32_16x16x32_bf16 v[156:159], v[208:211], a[16:19], v[156:159]\n"
"s_and_b32 s41, s41, s58\n"
"ds_read_b64_tr_b16 v[224:225], v239 offset:50176\n"
"v_mov_b32_dpp v247, v247 row_ror:8 row_mask:0x4 bank_mask:0xf\n"
"v_mfma_f32_16x16x32_bf16 v[156:159], v[212:215], a[20:23], v[156:159]\n"
"s_andn2_b32 s56, s55, s58\n"
"v_accvgpr_mov_b32 a144, a48\n"
"v_accvgpr_mov_b32 a146, a50\n"
"v_mfma_f32_16x16x32_bf16 v[160:163], v[208:211], a[32:35], v[188:191]\n"
"s_or_b32 s56, s56, s41\n"
"ds_read_b64_tr_b16 a[254:255], v235 offset:54272\n"
"v_accvgpr_mov_b32 a162, a66\n"
"v_mfma_f32_16x16x32_bf16 v[160:163], v[212:215], a[36:39], v[160:163]\n"
"v_xor_b32 v250, 0x1000, v245\n"
"v_accvgpr_mov_b32 a189, a93\n"
"v_mfma_f32_16x16x32_bf16 v[160:163], v[216:219], a[40:43], v[160:163]\n"
"ds_read_b64_tr_b16 a[244:245], v232 offset:49152\n"
"v_accvgpr_mov_b32 a166, a70\n"
"v_mfma_f32_16x16x32_bf16 v[160:163], v[220:223], a[44:47], v[160:163]\n"
"v_accvgpr_mov_b32 a175, a79\n"
"v_mul_f32_dpp v25, v246, v25 row_newbcast:1 row_mask:0xf bank_mask:0xf\n"
"v_mfma_f32_16x16x32_bf16 v[164:167], v[216:219], a[200:203], v[192:195]\n"
"ds_read_b128 v[216:219], v228 offset:5120\n"
"v_accvgpr_mov_b32 a164, a68\n"
"v_mfma_f32_16x16x32_bf16 v[164:167], v[220:223], a[204:207], v[164:167]\n"
"v_accvgpr_mov_b32 a173, a77\n"
"v_accvgpr_mov_b32 a154, a58\n"
"v_mfma_f32_16x16x32_bf16 v[164:167], v[208:211], a[192:195], v[164:167]\n"
"ds_read_b128 v[220:223], v229 offset:5120\n"
"v_accvgpr_mov_b32 a184, a88\n"
"v_mfma_f32_16x16x32_bf16 v[164:167], v[212:215], a[196:199], v[164:167]\n"
"s_waitcnt lgkmcnt(4)\n"
"ds_read_b128 v[208:211], v228 offset:4096\n"
"v_mov_b32_dpp v206, v206 row_ror:4 row_mask:0x8 bank_mask:0xf\n"
"v_accvgpr_mov_b32 a157, a61\n"
"v_accvgpr_mov_b32 a179, a83\n"
"v_accvgpr_mov_b32 a185, a89\n"
"v_exp_f32 v161, v161\n"
"v_exp_f32 v162, v162\n"
"LTC_%=:\n"
"s_waitcnt lgkmcnt(5)\n"
"v_mfma_f32_16x16x32_bf16 v[92:95], v[116:119], v[224:227], v[92:95]\n"
"s_waitcnt lgkmcnt(4)\n"
"ds_read_b128 v[212:215], v229 offset:4096\n"
"v_mul_f32_dpp v11, v246, v11 row_newbcast:3 row_mask:0xf bank_mask:0xf\n"
"v_mfma_f32_16x16x32_bf16 v[92:95], v[112:115], a[252:255], v[92:95]\n"
"v_mov_b32_dpp v247, v247 row_ror:4 row_mask:0x8 bank_mask:0xf\n"
"v_mov_b32 v253, v202\n"
"v_mfma_f32_16x16x32_bf16 v[60:63], v[108:111], v[224:227], v[60:63]\n"
"s_add_u32 m0, s44, 32768\n"
"ds_read_b64_tr_b16 a[246:247], v232 offset:53248\n"
"v_mul_f32_dpp v46, v247, v46 row_newbcast:2 row_mask:0xf bank_mask:0xf\n"
"v_mfma_f32_16x16x32_bf16 v[60:63], v[104:107], a[252:255], v[60:63]\n"
"buffer_load_dwordx4 v240, s[16:19], s56 offen nt lds\n"
"v_mfma_f32_16x16x32_bf16 v[28:31], v[100:103], v[224:227], v[28:31]\n"
"v_exp_f32 v154, v154\n"
"v_mfma_f32_16x16x32_bf16 v[28:31], v[96:99], a[252:255], v[28:31]\n"
"v_mul_f32_dpp v13, v246, v13 row_newbcast:1 row_mask:0xf bank_mask:0xf\n"
"v_mul_f32_dpp v26, v246, v26 row_newbcast:2 row_mask:0xf bank_mask:0xf\n"
"v_mfma_f32_16x16x32_bf16 a[236:239], v[120:123], a[252:255], a[236:239]\n"
"s_waitcnt lgkmcnt(3)\n"
"ds_write_b128 v244, a[216:219] offset:32768\n"
"v_mul_f32_dpp v15, v246, v15 row_newbcast:3 row_mask:0xf bank_mask:0xf\n"
"v_exp_f32 v155, v155\n"
"v_exp_f32 v156, v156\n"
"v_exp_f32 v158, v158\n"
"v_exp_f32 v160, v160\n"
"v_exp_f32 v163, v163\n"
"v_exp_f32 v166, v166\n"
"v_mul_f32_dpp v24, v246, v24 row_newbcast:0 row_mask:0xf bank_mask:0xf\n"
"v_mfma_f32_16x16x32_bf16 v[168:171], v[216:219], a[8:11], v[180:183]\n"
"v_exp_f32 v159, v159\n"
"v_mfma_f32_16x16x32_bf16 v[168:171], v[220:223], a[12:15], v[168:171]\n"
"s_waitcnt lgkmcnt(2)\n"
"ds_read_b64_tr_b16 v[226:227], v237 offset:53248\n"
"v_mul_f32_dpp v74, v206, v74 row_newbcast:2 row_mask:0xf bank_mask:0xf\n"
"v_mfma_f32_16x16x32_bf16 v[168:171], v[208:211], a[0:3], v[168:171]\n"
"v_exp_f32 v153, v153\n"
"v_mfma_f32_16x16x32_bf16 v[168:171], v[212:215], a[4:7], v[168:171]\n"
"v_mul_f32_dpp v41, v247, v41 row_newbcast:1 row_mask:0xf bank_mask:0xf\n"
"v_cvt_pk_bf16_f32 v144, v160, v161\n"
"v_mfma_f32_16x16x32_bf16 v[172:175], v[208:211], a[16:19], v[184:187]\n"
"v_exp_f32 v157, v157\n"
"v_mfma_f32_16x16x32_bf16 v[172:175], v[212:215], a[20:23], v[172:175]\n"
"v_exp_f32 v152, v152\n"
"v_mfma_f32_16x16x32_bf16 v[172:175], v[216:219], a[24:27], v[172:175]\n"
"v_exp_f32 v164, v164\n"
"v_mfma_f32_16x16x32_bf16 v[172:175], v[220:223], a[28:31], v[172:175]\n"
"ds_read_b64_tr_b16 a[254:255], v233 offset:53248\n"
"v_mul_f32_dpp v9, v246, v9 row_newbcast:1 row_mask:0xf bank_mask:0xf\n"
"v_mfma_f32_16x16x32_bf16 v[176:179], v[216:219], a[40:43], v[188:191]\n"
"ds_read_b64_tr_b16 v[224:225], v237 offset:49152\n"
"v_mul_f32_dpp v73, v206, v73 row_newbcast:1 row_mask:0xf bank_mask:0xf\n"
"v_mfma_f32_16x16x32_bf16 v[176:179], v[220:223], a[44:47], v[176:179]\n"
"v_cvt_pk_bf16_f32 v128, v152, v153\n"
"v_cvt_pk_bf16_f32 v129, v154, v155\n"
"v_mfma_f32_16x16x32_bf16 v[176:179], v[208:211], a[32:35], v[176:179]\n"
"s_add_u32 s40, s56, s52\n"
"v_mul_f32_dpp v43, v247, v43 row_newbcast:3 row_mask:0xf bank_mask:0xf\n"
"v_cvt_pk_bf16_f32 v24, v24, v25\n"
"v_mfma_f32_16x16x32_bf16 v[176:179], v[212:215], a[36:39], v[176:179]\n"
"buffer_load_dwordx4 v240, s[16:19], s40 offen offset:1024 nt lds\n"
"v_mfma_f32_16x16x32_bf16 v[152:155], v[208:211], a[192:195], v[192:195]\n"
"v_cvt_pk_bf16_f32 v137, v158, v159\n"
"v_mul_f32_dpp v62, v247, v62 row_newbcast:2 row_mask:0xf bank_mask:0xf\n"
"v_mfma_f32_16x16x32_bf16 v[152:155], v[212:215], a[196:199], v[152:155]\n"
"v_cvt_pk_bf16_f32 v145, v162, v163\n"
"v_mul_f32_dpp v90, v206, v90 row_newbcast:2 row_mask:0xf bank_mask:0xf\n"
"v_mfma_f32_16x16x32_bf16 v[152:155], v[216:219], a[200:203], v[152:155]\n"
"ds_read_b128 v[212:215], v231 offset:0\n"
"v_mul_f32_dpp v40, v247, v40 row_newbcast:0 row_mask:0xf bank_mask:0xf\n"
"v_mfma_f32_16x16x32_bf16 v[152:155], v[220:223], a[204:207], v[152:155]\n"
"s_waitcnt lgkmcnt(2)\n"
"ds_read_b128 v[208:211], v230 offset:0\n"
"ds_read_b128 v[220:223], v231 offset:1024\n"
"v_mfma_f32_16x16x32_bf16 a[208:211], v[120:123], a[244:247], a[208:211]\n"
"v_exp_f32 v173, v173\n"
"v_mfma_f32_16x16x32_bf16 v[0:3], v[96:99], a[244:247], v[0:3]\n"
"ds_read_b128 v[216:219], v230 offset:1024\n"
"v_mul_f32_dpp v72, v206, v72 row_newbcast:0 row_mask:0xf bank_mask:0xf\n"
"v_mfma_f32_16x16x32_bf16 v[0:3], v[100:103], a[248:251], v[0:3]\n"
"v_exp_f32 v178, v178\n"
"v_mfma_f32_16x16x32_bf16 v[32:35], v[104:107], a[244:247], v[32:35]\n"
"v_exp_f32 v171, v171\n"
"v_mfma_f32_16x16x32_bf16 v[32:35], v[108:111], a[248:251], v[32:35]\n"
"ds_write_b128 v244, a[220:223] offset:33792\n"
"v_exp_f32 v165, v165\n"
"v_exp_f32 v167, v167\n"
"v_mul_f32_dpp v59, v247, v59 row_newbcast:3 row_mask:0xf bank_mask:0xf\n"
"v_exp_f32 v168, v168\n"
"v_mfma_f32_16x16x32_bf16 v[64:67], v[112:115], a[244:247], v[64:67]\n"
"v_exp_f32 v172, v172\n"
"v_mfma_f32_16x16x32_bf16 v[64:67], v[116:119], a[248:251], v[64:67]\n"
"s_waitcnt lgkmcnt(3)\n"
"ds_write_b64 v250, v[252:253] offset:52224\n"
"v_mul_f32_dpp v42, v247, v42 row_newbcast:2 row_mask:0xf bank_mask:0xf\n"
"v_mul_f32_dpp v75, v206, v75 row_newbcast:3 row_mask:0xf bank_mask:0xf\n"
"v_cvt_pk_bf16_f32 v136, v156, v157\n"
"v_cvt_pk_bf16_f32 v125, v166, v167\n"
"v_mul_f32_dpp v29, v246, v29 row_newbcast:1 row_mask:0xf bank_mask:0xf\n"
"v_mul_f32_dpp v57, v247, v57 row_newbcast:1 row_mask:0xf bank_mask:0xf\n"
"v_exp_f32 v155, v155\n"
"v_mul_f32_dpp v0, v246, v0 row_newbcast:0 row_mask:0xf bank_mask:0xf\n"
"v_mul_f32_dpp v1, v246, v1 row_newbcast:1 row_mask:0xf bank_mask:0xf\n"
"v_mfma_f32_16x16x32_bf16 v[156:159], v[208:211], a[0:3], v[180:183]\n"
"v_mul_f32_dpp v45, v247, v45 row_newbcast:1 row_mask:0xf bank_mask:0xf\n"
"v_mul_f32_dpp v89, v206, v89 row_newbcast:1 row_mask:0xf bank_mask:0xf\n"
"v_mfma_f32_16x16x32_bf16 v[156:159], v[212:215], a[4:7], v[156:159]\n"
"s_waitcnt lgkmcnt(2)\n"
"v_mul_f32_dpp v8, v246, v8 row_newbcast:0 row_mask:0xf bank_mask:0xf\n"
"v_mul_f32_dpp v30, v246, v30 row_newbcast:2 row_mask:0xf bank_mask:0xf\n"
"v_mfma_f32_16x16x32_bf16 v[156:159], v[216:219], a[8:11], v[156:159]\n"
"s_add_u32 s40, s56, s53\n"
"ds_read_b64_tr_b16 a[252:253], v233 offset:49152\n"
"v_mul_f32_dpp v56, v247, v56 row_newbcast:0 row_mask:0xf bank_mask:0xf\n"
"v_mfma_f32_16x16x32_bf16 v[156:159], v[220:223], a[12:15], v[156:159]\n"
"buffer_load_dwordx4 v240, s[16:19], s40 offen offset:2048 nt lds\n"
"v_mul_f32_dpp v44, v247, v44 row_newbcast:0 row_mask:0xf bank_mask:0xf\n"
"v_mul_f32_dpp v76, v206, v76 row_newbcast:0 row_mask:0xf bank_mask:0xf\n"
"v_mul_f32_dpp v88, v206, v88 row_newbcast:0 row_mask:0xf bank_mask:0xf\n"
"v_exp_f32 v170, v170\n"
"v_exp_f32 v174, v174\n"
"v_exp_f32 v175, v175\n"
"v_exp_f32 v176, v176\n"
"v_exp_f32 v152, v152\n"
"v_exp_f32 v154, v154\n"
"v_mul_f32_dpp v66, v206, v66 row_newbcast:2 row_mask:0xf bank_mask:0xf\n"
"v_mfma_f32_16x16x32_bf16 v[160:163], v[216:219], a[24:27], v[184:187]\n"
"v_mul_f32_dpp v92, v206, v92 row_newbcast:0 row_mask:0xf bank_mask:0xf\n"
"v_cvt_pk_bf16_f32 v0, v0, v1\n"
"v_mfma_f32_16x16x32_bf16 v[160:163], v[220:223], a[28:31], v[160:163]\n"
"v_mul_f32_dpp v95, v206, v95 row_newbcast:3 row_mask:0xf bank_mask:0xf\n"
"v_cvt_pk_bf16_f32 v131, v170, v171\n"
"v_mfma_f32_16x16x32_bf16 v[160:163], v[208:211], a[16:19], v[160:163]\n"
"v_mul_f32_dpp v58, v247, v58 row_newbcast:2 row_mask:0xf bank_mask:0xf\n"
"v_cvt_pk_bf16_f32 v139, v174, v175\n"
"v_mfma_f32_16x16x32_bf16 v[160:163], v[212:215], a[20:23], v[160:163]\n"
"ds_write_b128 v244, a[236:239] offset:35840\n"
"v_cvt_pk_bf16_f32 v124, v164, v165\n"
"v_mul_f32_dpp v94, v206, v94 row_newbcast:2 row_mask:0xf bank_mask:0xf\n"
"v_exp_f32 v169, v169\n"
"v_exp_f32 v156, v156\n"
"v_mfma_f32_16x16x32_bf16 v[164:167], v[208:211], a[32:35], v[188:191]\n"
"v_exp_f32 v157, v157\n"
"v_mfma_f32_16x16x32_bf16 v[164:167], v[212:215], a[36:39], v[164:167]\n"
"ds_read_b128 v[208:211], v230 offset:4096\n"
"v_cvt_pk_bf16_f32 v8, v8, v9\n"
"v_mfma_f32_16x16x32_bf16 v[164:167], v[216:219], a[40:43], v[164:167]\n"
"v_cvt_pk_bf16_f32 v40, v40, v41\n"
"v_mul_f32_dpp v60, v247, v60 row_newbcast:0 row_mask:0xf bank_mask:0xf\n"
"v_mfma_f32_16x16x32_bf16 v[164:167], v[220:223], a[44:47], v[164:167]\n"
"ds_read_b64_tr_b16 a[244:245], v232 offset:50176\n"
"ds_read_b64_tr_b16 a[248:249], v236 offset:50176\n"
"v_mul_f32_dpp v79, v206, v79 row_newbcast:3 row_mask:0xf bank_mask:0xf\n"
"v_cvt_pk_bf16_f32 v25, v26, v27\n"
"v_mul_f32_dpp v61, v247, v61 row_newbcast:1 row_mask:0xf bank_mask:0xf\n"
"v_exp_f32 v177, v177\n"
"v_exp_f32 v179, v179\n"
"v_exp_f32 v160, v160\n"
"v_exp_f32 v161, v161\n"
"v_mul_f32_dpp v33, v247, v33 row_newbcast:1 row_mask:0xf bank_mask:0xf\n"
"v_mfma_f32_16x16x32_bf16 v[68:71], v[116:119], v[224:227], v[68:71]\n"
"s_waitcnt lgkmcnt(4)\n"
"ds_read_b128 v[220:223], v231 offset:5120\n"
"v_mul_f32_dpp v77, v206, v77 row_newbcast:1 row_mask:0xf bank_mask:0xf\n"
"v_mfma_f32_16x16x32_bf16 v[68:71], v[112:115], a[252:255], v[68:71]\n"
"ds_read_b128 v[212:215], v231 offset:4096\n"
"v_mul_f32_dpp v32, v247, v32 row_newbcast:0 row_mask:0xf bank_mask:0xf\n"
"v_mfma_f32_16x16x32_bf16 v[36:39], v[108:111], v[224:227], v[36:39]\n"
"s_add_u32 s40, s56, s54\n"
"ds_read_b128 v[216:219], v230 offset:5120\n"
"v_mul_f32_dpp v2, v246, v2 row_newbcast:2 row_mask:0xf bank_mask:0xf\n"
"v_mfma_f32_16x16x32_bf16 v[36:39], v[104:107], a[252:255], v[36:39]\n"
"buffer_load_dwordx4 v240, s[16:19], s40 offen offset:3072 nt lds\n"
"v_mul_f32_dpp v47, v247, v47 row_newbcast:3 row_mask:0xf bank_mask:0xf\n"
"v_cvt_pk_bf16_f32 v41, v42, v43\n"
"v_mul_f32_dpp v31, v246, v31 row_newbcast:3 row_mask:0xf bank_mask:0xf\n"
"v_cvt_pk_bf16_f32 v147, v178, v179\n"
"v_exp_f32 v153, v153\n"
"v_exp_f32 v159, v159\n"
"v_exp_f32 v165, v165\n"
"v_mfma_f32_16x16x32_bf16 v[4:7], v[100:103], v[224:227], v[4:7]\n"
"v_cvt_pk_bf16_f32 v56, v56, v57\n"
"v_cvt_pk_bf16_f32 v138, v172, v173\n"
"v_mfma_f32_16x16x32_bf16 v[4:7], v[96:99], a[252:255], v[4:7]\n"
"ds_write_b128 v244, a[232:235] offset:34816\n"
"v_cvt_pk_bf16_f32 v9, v10, v11\n"
"v_mul_f32_dpp v63, v247, v63 row_newbcast:3 row_mask:0xf bank_mask:0xf\n"
"v_cvt_pk_bf16_f32 v130, v168, v169\n"
"v_cvt_pk_bf16_f32 v126, v152, v153\n"
"v_mul_f32_dpp v64, v206, v64 row_newbcast:0 row_mask:0xf bank_mask:0xf\n"
"v_mfma_f32_16x16x32_bf16 a[212:215], v[120:123], a[252:255], a[212:215]\n"
"s_waitcnt lgkmcnt(6)\n"
"v_cvt_pk_bf16_f32 v88, v88, v89\n"
"v_cvt_pk_bf16_f32 v146, v176, v177\n"
"v_mfma_f32_16x16x32_bf16 v[168:171], v[208:211], a[0:3], v[180:183]\n"
"s_waitcnt lgkmcnt(2)\n"
"ds_read_b64_tr_b16 a[246:247], v232 offset:54272\n"
"v_cvt_pk_bf16_f32 v43, v46, v47\n"
"v_mfma_f32_16x16x32_bf16 v[168:171], v[212:215], a[4:7], v[168:171]\n"
"s_waitcnt lgkmcnt(2)\n"
"ds_read_b64_tr_b16 v[224:225], v237 offset:50176\n"
"v_cvt_pk_bf16_f32 v57, v58, v59\n"
"v_mfma_f32_16x16x32_bf16 v[168:171], v[216:219], a[8:11], v[168:171]\n"
"v_exp_f32 v158, v158\n"
"v_mfma_f32_16x16x32_bf16 v[168:171], v[220:223], a[12:15], v[168:171]\n"
"ds_read_b64_tr_b16 a[250:251], v236 offset:54272\n"
"ds_read_b64_tr_b16 a[252:253], v233 offset:50176\n"
"v_cvt_pk_bf16_f32 v42, v44, v45\n"
"v_cvt_pk_bf16_f32 v72, v72, v73\n"
"v_cvt_pk_bf16_f32 v73, v74, v75\n"
"v_mul_f32_dpp v28, v246, v28 row_newbcast:0 row_mask:0xf bank_mask:0xf\n"
"v_mul_f32_dpp v91, v206, v91 row_newbcast:3 row_mask:0xf bank_mask:0xf\n"
"v_exp_f32 v167, v167\n"
"v_mul_f32_dpp v34, v247, v34 row_newbcast:2 row_mask:0xf bank_mask:0xf\n"
"v_mul_f32_dpp v35, v247, v35 row_newbcast:3 row_mask:0xf bank_mask:0xf\n"
"v_mul_f32_dpp v65, v206, v65 row_newbcast:1 row_mask:0xf bank_mask:0xf\n"
"v_mul_f32_dpp v68, v206, v68 row_newbcast:0 row_mask:0xf bank_mask:0xf\n"
"v_mfma_f32_16x16x32_bf16 v[172:175], v[216:219], a[24:27], v[184:187]\n"
"v_cvt_pk_bf16_f32 v10, v12, v13\n"
"v_xor_b32 v244, 0x1000, v244\n"
"v_mfma_f32_16x16x32_bf16 v[172:175], v[220:223], a[28:31], v[172:175]\n"
"ds_read_b64_tr_b16 v[226:227], v237 offset:54272\n"
"v_cvt_pk_bf16_f32 v127, v154, v155\n"
"v_mfma_f32_16x16x32_bf16 v[172:175], v[208:211], a[16:19], v[172:175]\n"
"v_cvt_pk_bf16_f32 v59, v62, v63\n"
"v_mul_f32_dpp v67, v206, v67 row_newbcast:3 row_mask:0xf bank_mask:0xf\n"
"v_mfma_f32_16x16x32_bf16 v[172:175], v[212:215], a[20:23], v[172:175]\n"
"s_cbranch_vccnz LPCT0_%=\nLPKT0_%=:\n"
"ds_write_b128 v248, v[40:43] offset:50176\n"
"v_cvt_pk_bf16_f32 v11, v14, v15\n"
"v_mul_f32_dpp v78, v206, v78 row_newbcast:2 row_mask:0xf bank_mask:0xf\n"
"v_mul_f32_dpp v93, v206, v93 row_newbcast:1 row_mask:0xf bank_mask:0xf\n"
"v_exp_f32 v164, v164\n"
"v_mul_f32_dpp v3, v246, v3 row_newbcast:3 row_mask:0xf bank_mask:0xf\n"
"v_mul_f32_dpp v36, v247, v36 row_newbcast:0 row_mask:0xf bank_mask:0xf\n"
"v_mul_f32_dpp v39, v247, v39 row_newbcast:3 row_mask:0xf bank_mask:0xf\n"
"v_mul_f32_dpp v70, v206, v70 row_newbcast:2 row_mask:0xf bank_mask:0xf\n"
"v_mov_b64 v[198:199], 0\n"
"v_mfma_f32_16x16x32_bf16 v[176:179], v[208:211], a[32:35], v[188:191]\n"
"ds_read_b64_tr_b16 a[68:69], v249 offset:50176\n"
"ds_read_b64_tr_b16 a[254:255], v233 offset:54272\n"
"v_mfma_f32_16x16x32_bf16 v[176:179], v[212:215], a[36:39], v[176:179]\n"
"ds_read_b64_tr_b16 a[70:71], v249 offset:50184\n"
"v_cvt_pk_bf16_f32 v74, v76, v77\n"
"v_mfma_f32_16x16x32_bf16 v[176:179], v[216:219], a[40:43], v[176:179]\n"
"v_mul_f32_dpp v69, v206, v69 row_newbcast:1 row_mask:0xf bank_mask:0xf\n"
"v_mul_f32_dpp v71, v206, v71 row_newbcast:3 row_mask:0xf bank_mask:0xf\n"
"v_mfma_f32_16x16x32_bf16 v[176:179], v[220:223], a[44:47], v[176:179]\n"
"s_waitcnt lgkmcnt(5)\n"
"ds_write_b128 v248, v[8:11] offset:49152\n"
"v_cvt_pk_bf16_f32 v75, v78, v79\n"
"v_cvt_pk_bf16_f32 v27, v30, v31\n"
"v_mul_f32_dpp v38, v247, v38 row_newbcast:2 row_mask:0xf bank_mask:0xf\n"
"v_cvt_pk_bf16_f32 v32, v32, v33\n"
"v_accvgpr_read_b32 v255, a209\n"
"v_mfma_f32_16x16x32_bf16 a[224:227], v[120:123], a[244:247], a[224:227]\n"
"ds_write_b128 v248, v[72:75] offset:51200\n"
"v_cvt_pk_bf16_f32 v89, v90, v91\n"
"v_mul_f32_dpp v5, v246, v5 row_newbcast:1 row_mask:0xf bank_mask:0xf\n"
"v_exp_f32 v170, v170\n"
"v_exp_f32 v175, v175\n"
"v_mfma_f32_16x16x32_bf16 v[16:19], v[96:99], a[244:247], v[16:19]\n"
"s_waitcnt vmcnt(8) lgkmcnt(3)\n"
"ds_read_b64_tr_b16 a[84:85], v249 offset:51200\n"
"v_mul_f32_dpp v37, v247, v37 row_newbcast:1 row_mask:0xf bank_mask:0xf\n"
"v_mfma_f32_16x16x32_bf16 v[16:19], v[100:103], a[248:251], v[16:19]\n"
"s_barrier\n"
"ds_read_b128 v[212:215], v229 offset:16384\n"
"ds_read_b64 v[250:251], v245 offset:52224\n"
"v_cvt_pk_bf16_f32 v26, v28, v29\n"
"v_cvt_pk_bf16_f32 v58, v60, v61\n"
"v_mul_f32_dpp v6, v246, v6 row_newbcast:2 row_mask:0xf bank_mask:0xf\n"
"v_mul_f32_dpp v7, v246, v7 row_newbcast:3 row_mask:0xf bank_mask:0xf\n"
"v_exp_f32 v172, v172\n"
"v_exp_f32 v177, v177\n"
"v_mfma_f32_16x16x32_bf16 v[48:51], v[104:107], a[244:247], v[48:51]\n"
"ds_read_b64_tr_b16 a[86:87], v249 offset:51208\n"
"ds_read_b128 v[208:211], v228 offset:16384\n"
"v_mfma_f32_16x16x32_bf16 v[48:51], v[108:111], a[248:251], v[48:51]\n"
"ds_read_b128 v[216:219], v228 offset:17408\n"
"v_cvt_pk_bf16_f32 v91, v94, v95\n"
"v_mfma_f32_16x16x32_bf16 v[80:83], v[112:115], a[244:247], v[80:83]\n"
"ds_read_b64_tr_b16 a[54:55], v249 offset:49160\n"
"v_cvt_pk_bf16_f32 v90, v92, v93\n"
"v_mfma_f32_16x16x32_bf16 v[80:83], v[116:119], a[248:251], v[80:83]\n"
"ds_read_b64_tr_b16 a[52:53], v249 offset:49152\n"
"ds_read_b128 v[28:31], v244 offset:33792\n"
"v_mfma_f32_16x16x32_bf16 v[84:87], v[116:119], v[224:227], v[84:87]\n"
"ds_read_b128 v[220:223], v229 offset:17408\n"
"v_cvt_pk_bf16_f32 v33, v34, v35\n"
"v_mfma_f32_16x16x32_bf16 v[84:87], v[112:115], a[252:255], v[84:87]\n"
"ds_write_b128 v248, v[56:59] offset:50176\n"
"v_cvt_pk_bf16_f32 v1, v2, v3\n"
"v_cvt_pk_bf16_f32 v64, v64, v65\n"
"v_exp_f32 v171, v171\n"
"v_exp_f32 v176, v176\n"
"v_mov_b64 v[202:203], 0\n"
"v_mfma_f32_16x16x32_bf16 v[52:55], v[108:111], v[224:227], v[52:55]\n"
"s_waitcnt lgkmcnt(8)\n"
"ds_write_b128 v248, v[24:27] offset:49152\n"
"v_cvt_pk_bf16_f32 v65, v66, v67\n"
"v_cvt_pk_bf16_f32 v66, v68, v69\n"
"v_cvt_pk_bf16_f32 v67, v70, v71\n"
"v_exp_f32 v169, v169\n"
"v_min_f32 v254, v252, v250\n"
"v_mfma_f32_16x16x32_bf16 v[52:55], v[104:107], a[252:255], v[52:55]\n"
"ds_write_b128 v248, v[88:91] offset:51200\n"
"v_cvt_pk_bf16_f32 v132, v156, v157\n"
"v_exp_f32 v162, v162\n"
"v_exp_f32 v166, v166\n"
"v_cvt_pk_bf16_f32 v3, v6, v7\n"
"v_exp_f32 v174, v174\n"
"v_mfma_f32_16x16x32_bf16 v[20:23], v[100:103], v[224:227], v[20:23]\n"
"ds_read_b64_tr_b16 a[76:77], v249 offset:50176\n"
"ds_read_b64_tr_b16 a[250:251], v238 offset:4096\n"
"v_mfma_f32_16x16x32_bf16 v[20:23], v[96:99], a[252:255], v[20:23]\n"
"ds_read_b128 v[24:27], v244 offset:32768\n"
"v_sub_f32 v252, v254, v252\n"
"v_mfma_f32_16x16x32_bf16 a[228:231], v[120:123], a[252:255], a[228:231]\n"
"s_waitcnt lgkmcnt(9)\n"
"ds_read_b64_tr_b16 a[62:63], v249 offset:49160\n"
"ds_read_b64_tr_b16 a[92:93], v249 offset:51200\n"
"ds_read_b64_tr_b16 a[94:95], v249 offset:51208\n"
"v_exp_f32 v163, v163\n"
"v_mul_f32_dpp v4, v246, v4 row_newbcast:0 row_mask:0xf bank_mask:0xf\n"
"v_cvt_pk_bf16_f32 v34, v36, v37\n"
"v_exp_f32 v178, v178\n"
"v_sub_f32 v250, v254, v250\n"
"v_exp_f32 v252, v252\n"
"v_mfma_f32_16x16x32_bf16 v[152:155], v[208:211], a[0:3], v[180:183]\n"
"ds_read_b64_tr_b16 a[78:79], v249 offset:50184\n"
"ds_read_b128 v[60:63], v244 offset:35840\n"
"v_mfma_f32_16x16x32_bf16 v[152:155], v[212:215], a[4:7], v[152:155]\n"
"ds_write_b128 v248, v[64:67] offset:51200\n"
"v_cvt_pk_bf16_f32 v133, v158, v159\n"
"v_cvt_pk_bf16_f32 v2, v4, v5\n"
"v_cvt_pk_bf16_f32 v35, v38, v39\n"
"v_exp_f32 v250, v250\n"
"v_mul_f32 v253, v253, v252\n"
"v_mfma_f32_16x16x32_bf16 v[152:155], v[216:219], a[8:11], v[152:155]\n"
"s_waitcnt lgkmcnt(11)\n"
"ds_read_b64_tr_b16 a[60:61], v249 offset:49152\n"
"ds_read_b64_tr_b16 a[82:83], v249 offset:51208\n"
"v_mfma_f32_16x16x32_bf16 v[152:155], v[220:223], a[12:15], v[152:155]\n"
"ds_write_b128 v248, v[0:3] offset:49152\n"
"ds_write_b128 v248, v[32:35] offset:50176\n"
"v_mul_f32_dpp v50, v247, v50 row_newbcast:2 row_mask:0xf bank_mask:0xf\n"
"v_mfma_f32_16x16x32_bf16 v[156:159], v[216:219], a[24:27], v[184:187]\n"
"ds_read_b64_tr_b16 a[66:67], v249 offset:50184\n"
"ds_read_b64_tr_b16 a[80:81], v249 offset:51200\n"
"v_mfma_f32_16x16x32_bf16 v[156:159], v[220:223], a[28:31], v[156:159]\n"
"ds_read_b64_tr_b16 a[48:49], v249 offset:49152\n"
"ds_read_b128 v[56:59], v244 offset:34816\n"
"v_mfma_f32_16x16x32_bf16 v[156:159], v[208:211], a[16:19], v[156:159]\n"
"ds_read_b64_tr_b16 v[224:225], v239 offset:0\n"
"v_cvt_pk_bf16_f32 v143, v174, v175\n"
"v_mfma_f32_16x16x32_bf16 v[156:159], v[212:215], a[20:23], v[156:159]\n"
"ds_read_b64_tr_b16 a[50:51], v249 offset:49160\n"
"ds_read_b64_tr_b16 a[64:65], v249 offset:50176\n"
"ds_read_b64_tr_b16 a[244:245], v234 offset:0\n"
"ds_read_b64_tr_b16 a[246:247], v234 offset:4096\n"
"v_cvt_pk_bf16_f32 v140, v160, v161\n"
"v_cvt_pk_bf16_f32 v141, v162, v163\n"
"v_cvt_pk_bf16_f32 v135, v170, v171\n"
"v_cvt_pk_bf16_f32 v150, v176, v177\n"
"v_fmac_f32 v253, v251, v250\n"
"v_xor_b32 v248, s75, v248\n"
"v_mfma_f32_16x16x32_bf16 v[160:163], v[208:211], a[32:35], v[188:191]\n"
"ds_read_b64_tr_b16 a[248:249], v238 offset:0\n"
"v_mul_f32_dpp v51, v247, v51 row_newbcast:3 row_mask:0xf bank_mask:0xf\n"
"v_mfma_f32_16x16x32_bf16 v[160:163], v[212:215], a[36:39], v[160:163]\n"
"v_cvt_pk_bf16_f32 v148, v164, v165\n"
"v_cvt_pk_bf16_f32 v149, v166, v167\n"
"v_mfma_f32_16x16x32_bf16 v[160:163], v[216:219], a[40:43], v[160:163]\n"
"v_rcp_f32 v253, v253\n"
"v_mfma_f32_16x16x32_bf16 v[160:163], v[220:223], a[44:47], v[160:163]\n"
"ds_read_b64_tr_b16 a[252:253], v235 offset:0\n"
"v_mul_f32 v250, v250, v253\n"
"v_mfma_f32_16x16x32_bf16 v[164:167], v[216:219], a[200:203], v[192:195]\n"
"ds_read_b64_tr_b16 v[226:227], v239 offset:4096\n"
"v_mul_f32 v252, v252, v253\n"
"v_mfma_f32_16x16x32_bf16 v[164:167], v[220:223], a[204:207], v[164:167]\n"
"v_exp_f32 v168, v168\n"
"v_mfma_f32_16x16x32_bf16 v[164:167], v[208:211], a[192:195], v[164:167]\n"
"ds_read_b128 v[216:219], v228 offset:21504\n"
"v_mov_b32_dpp v250, v250 row_ror:12 row_mask:0x2 bank_mask:0xf\n"
"v_mfma_f32_16x16x32_bf16 v[164:167], v[212:215], a[196:199], v[164:167]\n"
"s_waitcnt lgkmcnt(4)\n"
"ds_read_b64_tr_b16 a[254:255], v235 offset:4096\n"
"v_exp_f32 v173, v173\n"
"v_mov_b32_dpp v252, v252 row_ror:12 row_mask:0x2 bank_mask:0xf\n"
"v_accvgpr_read_b32 v254, a208\n"
"v_mul_f32_dpp v22, v246, v22 row_newbcast:2 row_mask:0xf bank_mask:0xf\n"
"v_mul_f32_dpp v80, v206, v80 row_newbcast:0 row_mask:0xf bank_mask:0xf\n"
"v_exp_f32 v156, v156\n"
"v_mfma_f32_16x16x32_bf16 a[216:219], v[124:127], a[244:247], 0\n"
"ds_read_b128 v[220:223], v229 offset:21504\n"
"v_cvt_pk_bf16_f32 v134, v168, v169\n"
"v_cvt_pk_bf16_f32 v142, v172, v173\n"
"v_mov_b32_dpp v250, v250 row_ror:8 row_mask:0x4 bank_mask:0xf\n"
"v_accvgpr_read_b32 v251, a211\n"
"v_mul_f32_dpp v84, v206, v84 row_newbcast:0 row_mask:0xf bank_mask:0xf\n"
"v_mov_b64 v[196:197], 0\n"
"v_exp_f32 v154, v154\n"
"v_exp_f32 v157, v157\n"
"v_mfma_f32_16x16x32_bf16 v[8:11], v[128:131], a[244:247], 0\n"
"s_waitcnt lgkmcnt(5)\n"
"s_add_u32 m0, s44, 98304\n"
"v_exp_f32 v179, v179\n"
"v_mfma_f32_16x16x32_bf16 v[8:11], v[132:135], a[248:251], v[8:11]\n"
"buffer_load_dwordx4 v240, s[20:23], s56 offen nt lds\n"
"v_mfma_f32_16x16x32_bf16 v[40:43], v[136:139], a[244:247], 0\n"
"v_exp_f32 v159, v159\n"
"v_mfma_f32_16x16x32_bf16 v[40:43], v[140:143], a[248:251], v[40:43]\n"
"v_cvt_pk_bf16_f32 v151, v178, v179\n"
"v_mov_b32_dpp v252, v252 row_ror:8 row_mask:0x4 bank_mask:0xf\n"
"v_mfma_f32_16x16x32_bf16 v[72:75], v[144:147], a[244:247], 0\n"
"v_mov_b32_dpp v250, v250 row_ror:4 row_mask:0x8 bank_mask:0xf\n"
"v_mul_f32_dpp v23, v246, v23 row_newbcast:3 row_mask:0xf bank_mask:0xf\n"
"v_mfma_f32_16x16x32_bf16 v[72:75], v[148:151], a[248:251], v[72:75]\n"
"s_waitcnt lgkmcnt(2)\n"
"ds_read_b128 v[212:215], v229 offset:20480\n"
"v_accvgpr_read_b32 v253, a210\n"
"v_mul_f32_dpp v27, v250, v27 row_newbcast:3 row_mask:0xf bank_mask:0xf\n"
"v_mul_f32_dpp v21, v246, v21 row_newbcast:1 row_mask:0xf bank_mask:0xf\n"
"v_mul_f32_dpp v86, v206, v86 row_newbcast:2 row_mask:0xf bank_mask:0xf\n"
"v_mul_f32_dpp v57, v250, v57 row_newbcast:1 row_mask:0xf bank_mask:0xf\n"
"v_exp_f32 v158, v158\n"
"v_exp_f32 v161, v161\n"
"v_mfma_f32_16x16x32_bf16 v[76:79], v[148:151], v[224:227], 0\n"
"s_waitcnt lgkmcnt(2)\n"
"ds_read_b128 v[208:211], v228 offset:20480\n"
"v_mov_b32_dpp v252, v252 row_ror:4 row_mask:0x8 bank_mask:0xf\n"
"v_mfma_f32_16x16x32_bf16 v[76:79], v[144:147], a[252:255], v[76:79]\n"
"ds_read_b64_tr_b16 a[248:249], v238 offset:1024\n"
"v_mul_f32_dpp v24, v250, v24 row_newbcast:0 row_mask:0xf bank_mask:0xf\n"
"v_mfma_f32_16x16x32_bf16 v[44:47], v[140:143], v[224:227], 0\n"
"v_mul_f32_dpp v26, v250, v26 row_newbcast:2 row_mask:0xf bank_mask:0xf\n"
"v_mul_f32_dpp v30, v250, v30 row_newbcast:2 row_mask:0xf bank_mask:0xf\n"
"v_mfma_f32_16x16x32_bf16 v[44:47], v[136:139], a[252:255], v[44:47]\n"
"ds_read_b64_tr_b16 a[244:245], v234 offset:1024\n"
"v_fmac_f32_dpp v26, v252, v253 row_newbcast:2 row_mask:0xf bank_mask:0xf\n"
"v_mfma_f32_16x16x32_bf16 v[12:15], v[132:135], v[224:227], 0\n"
"s_add_u32 s40, s56, s52\n"
"ds_read_b64_tr_b16 v[224:225], v239 offset:1024\n"
"v_mul_f32_dpp v25, v250, v25 row_newbcast:1 row_mask:0xf bank_mask:0xf\n"
"v_mfma_f32_16x16x32_bf16 v[12:15], v[128:131], a[252:255], v[12:15]\n"
"buffer_load_dwordx4 v240, s[20:23], s40 offen offset:1024 nt lds\n"
"v_fmac_f32_dpp v24, v252, v254 row_newbcast:0 row_mask:0xf bank_mask:0xf\n"
"v_fmac_f32_dpp v25, v252, v255 row_newbcast:1 row_mask:0xf bank_mask:0xf\n"
"v_accvgpr_read_b32 v254, a212\n"
"v_mul_f32_dpp v31, v250, v31 row_newbcast:3 row_mask:0xf bank_mask:0xf\n"
"v_mul_f32_dpp v62, v250, v62 row_newbcast:2 row_mask:0xf bank_mask:0xf\n"
"v_mul_f32_dpp v63, v250, v63 row_newbcast:3 row_mask:0xf bank_mask:0xf\n"
"v_exp_f32 v163, v163\n"
"v_mfma_f32_16x16x32_bf16 a[220:223], v[124:127], a[252:255], 0\n"
"v_mul_f32_dpp v28, v250, v28 row_newbcast:0 row_mask:0xf bank_mask:0xf\n"
"v_mul_f32_dpp v60, v250, v60 row_newbcast:0 row_mask:0xf bank_mask:0xf\n"
"v_smfmac_f32_16x16x64_bf16 v[196:199], a[240:243], v[128:135], v241\n"
"v_accvgpr_read_b32 v253, a214\n"
"v_mul_f32_dpp v53, v247, v53 row_newbcast:1 row_mask:0xf bank_mask:0xf\n"
"v_mfma_f32_16x16x32_bf16 v[202:205], a[240:243], v[124:127], v[202:205]\n"
"ds_read_b64_tr_b16 a[250:251], v238 offset:5120\n"
"v_fmac_f32_dpp v28, v252, v254 row_newbcast:0 row_mask:0xf bank_mask:0xf\n"
"v_mfma_f32_16x16x32_bf16 v[168:171], v[216:219], a[8:11], v[180:183]\n"
"s_waitcnt lgkmcnt(3)\n"
"v_exp_f32 v155, v155\n"
"v_mfma_f32_16x16x32_bf16 v[168:171], v[220:223], a[12:15], v[168:171]\n"
"ds_read_b64_tr_b16 a[246:247], v234 offset:5120\n"
"v_mul_f32_dpp v85, v206, v85 row_newbcast:1 row_mask:0xf bank_mask:0xf\n"
"v_mfma_f32_16x16x32_bf16 v[168:171], v[208:211], a[0:3], v[168:171]\n"
"v_mul_f32_dpp v29, v250, v29 row_newbcast:1 row_mask:0xf bank_mask:0xf\n"
"v_mul_f32_dpp v58, v250, v58 row_newbcast:2 row_mask:0xf bank_mask:0xf\n"
"v_mfma_f32_16x16x32_bf16 v[168:171], v[212:215], a[4:7], v[168:171]\n"
"v_fmac_f32_dpp v27, v252, v251 row_newbcast:3 row_mask:0xf bank_mask:0xf\n"
"v_accvgpr_read_b32 v255, a213\n"
"v_accvgpr_read_b32 v251, a215\n"
"v_fmac_f32_dpp v30, v252, v253 row_newbcast:2 row_mask:0xf bank_mask:0xf\n"
"v_cvt_pk_bf16_f32 v24, v24, v25\n"
"v_cvt_pk_bf16_f32 v25, v26, v27\n"
"v_accvgpr_read_b32 v254, a224\n"
"v_mul_f32_dpp v61, v250, v61 row_newbcast:1 row_mask:0xf bank_mask:0xf\n"
"v_exp_f32 v165, v165\n"
"v_mfma_f32_16x16x32_bf16 v[172:175], v[208:211], a[16:19], v[184:187]\n"
"ds_read_b64_tr_b16 v[226:227], v239 offset:5120\n"
"v_accvgpr_read_b32 v253, a226\n"
"v_mfma_f32_16x16x32_bf16 v[172:175], v[212:215], a[20:23], v[172:175]\n"
"v_fmac_f32_dpp v29, v252, v255 row_newbcast:1 row_mask:0xf bank_mask:0xf\n"
"v_mul_f32_dpp v56, v250, v56 row_newbcast:0 row_mask:0xf bank_mask:0xf\n"
"v_mfma_f32_16x16x32_bf16 v[172:175], v[216:219], a[24:27], v[172:175]\n"
"s_add_u32 s40, s56, s53\n"
"ds_read_b64_tr_b16 a[254:255], v235 offset:5120\n"
"v_mul_f32_dpp v59, v250, v59 row_newbcast:3 row_mask:0xf bank_mask:0xf\n"
"v_mfma_f32_16x16x32_bf16 v[172:175], v[220:223], a[28:31], v[172:175]\n"
"buffer_load_dwordx4 v240, s[20:23], s40 offen offset:2048 nt lds\n"
"v_fmac_f32_dpp v31, v252, v251 row_newbcast:3 row_mask:0xf bank_mask:0xf\n"
"v_mul_f32_dpp v16, v246, v16 row_newbcast:0 row_mask:0xf bank_mask:0xf\n"
"v_mul_f32_dpp v48, v247, v48 row_newbcast:0 row_mask:0xf bank_mask:0xf\n"
"v_mul_f32_dpp v49, v247, v49 row_newbcast:1 row_mask:0xf bank_mask:0xf\n"
"v_accvgpr_read_b32 v255, a225\n"
"v_accvgpr_read_b32 v251, a227\n"
"v_fmac_f32_dpp v56, v252, v254 row_newbcast:0 row_mask:0xf bank_mask:0xf\n"
"v_exp_f32 v167, v167\n"
"v_exp_f32 v171, v171\n"
"v_mfma_f32_16x16x32_bf16 v[176:179], v[216:219], a[40:43], v[188:191]\n"
"v_cvt_pk_bf16_f32 v26, v28, v29\n"
"v_mul_f32_dpp v81, v206, v81 row_newbcast:1 row_mask:0xf bank_mask:0xf\n"
"v_fmac_f32_dpp v57, v252, v255 row_newbcast:1 row_mask:0xf bank_mask:0xf\n"
"v_fmac_f32_dpp v58, v252, v253 row_newbcast:2 row_mask:0xf bank_mask:0xf\n"
"v_fmac_f32_dpp v59, v252, v251 row_newbcast:3 row_mask:0xf bank_mask:0xf\n"
"v_accvgpr_read_b32 v254, a228\n"
"v_accvgpr_read_b32 v255, a229\n"
"v_accvgpr_read_b32 v253, a230\n"
"v_accvgpr_read_b32 v251, a231\n"
"v_cvt_pk_bf16_f32 v56, v56, v57\n"
"v_exp_f32 v152, v152\n"
"v_exp_f32 v153, v153\n"
"v_cvt_pk_bf16_f32 v97, v154, v155\n"
"v_mfma_f32_16x16x32_bf16 v[176:179], v[220:223], a[44:47], v[176:179]\n"
"v_mul_f32_dpp v54, v247, v54 row_newbcast:2 row_mask:0xf bank_mask:0xf\n"
"v_fmac_f32_dpp v60, v252, v254 row_newbcast:0 row_mask:0xf bank_mask:0xf\n"
"v_mfma_f32_16x16x32_bf16 v[176:179], v[208:211], a[32:35], v[176:179]\n"
"v_cvt_pk_bf16_f32 v27, v30, v31\n"
"v_mul_f32_dpp v20, v246, v20 row_newbcast:0 row_mask:0xf bank_mask:0xf\n"
"v_mfma_f32_16x16x32_bf16 v[176:179], v[212:215], a[36:39], v[176:179]\n"
"ds_write_b128 v243, v[24:27] offset:0\n"
"v_mul_f32_dpp v87, v206, v87 row_newbcast:3 row_mask:0xf bank_mask:0xf\n"
"v_fmac_f32_dpp v61, v252, v255 row_newbcast:1 row_mask:0xf bank_mask:0xf\n"
"v_fmac_f32_dpp v62, v252, v253 row_newbcast:2 row_mask:0xf bank_mask:0xf\n"
"v_fmac_f32_dpp v63, v252, v251 row_newbcast:3 row_mask:0xf bank_mask:0xf\n"
"v_cvt_pk_bf16_f32 v57, v58, v59\n"
"v_cvt_pk_bf16_f32 v58, v60, v61\n"
"v_cvt_pk_bf16_f32 v59, v62, v63\n"
"v_cvt_pk_bf16_f32 v96, v152, v153\n"
"v_mfma_f32_16x16x32_bf16 v[152:155], v[208:211], a[192:195], v[192:195]\n"
"ds_write_b128 v243, v[56:59] offset:2048\n"
"v_cvt_pk_bf16_f32 v105, v158, v159\n"
"v_exp_f32 v162, v162\n"
"v_exp_f32 v172, v172\n"
"v_mfma_f32_16x16x32_bf16 v[152:155], v[212:215], a[196:199], v[152:155]\n"
"ds_read_b64_tr_b16 a[252:253], v235 offset:1024\n"
"v_mul_f32_dpp v18, v246, v18 row_newbcast:2 row_mask:0xf bank_mask:0xf\n"
"v_mfma_f32_16x16x32_bf16 v[152:155], v[216:219], a[200:203], v[152:155]\n"
"v_exp_f32 v174, v174\n"
"v_mfma_f32_16x16x32_bf16 v[152:155], v[220:223], a[204:207], v[152:155]\n"
"s_waitcnt lgkmcnt(3)\n"
"ds_read_b128 v[212:215], v231 offset:16384\n"
"v_mul_f32_dpp v55, v247, v55 row_newbcast:3 row_mask:0xf bank_mask:0xf\n"
"v_mfma_f32_16x16x32_bf16 a[232:235], v[124:127], a[244:247], 0\n"
"v_exp_f32 v169, v169\n"
"v_mfma_f32_16x16x32_bf16 v[24:27], v[128:131], a[244:247], 0\n"
"s_add_u32 s40, s56, s54\n"
"ds_read_b128 v[208:211], v230 offset:16384\n"
"v_mul_f32_dpp v17, v246, v17 row_newbcast:1 row_mask:0xf bank_mask:0xf\n"
"v_mfma_f32_16x16x32_bf16 v[24:27], v[132:135], a[248:251], v[24:27]\n"
"buffer_load_dwordx4 v240, s[20:23], s40 offen offset:3072 nt lds\n"
"v_mfma_f32_16x16x32_bf16 v[56:59], v[136:139], a[244:247], 0\n"
"s_add_u32 s55, s55, s59\n"
"v_exp_f32 v173, v173\n"
"v_mfma_f32_16x16x32_bf16 v[56:59], v[140:143], a[248:251], v[56:59]\n"
"s_add_u32 s41, s55, s57\n"
"v_exp_f32 v160, v160\n"
"v_mfma_f32_16x16x32_bf16 v[88:91], v[144:147], a[244:247], 0\n"
"s_and_b32 s41, s41, s58\n"
"v_exp_f32 v166, v166\n"
"v_mfma_f32_16x16x32_bf16 v[88:91], v[148:151], a[248:251], v[88:91]\n"
"s_andn2_b32 s56, s55, s58\n"
"ds_read_b64_tr_b16 a[248:249], v236 offset:0\n"
"v_mov_b64 v[200:201], 0\n"
"v_mfma_f32_16x16x32_bf16 v[92:95], v[148:151], v[224:227], 0\n"
"s_waitcnt lgkmcnt(2)\n"
"s_or_b32 s56, s56, s41\n"
"ds_read_b128 v[220:223], v231 offset:17408\n"
"v_cvt_pk_bf16_f32 v16, v16, v17\n"
"v_mfma_f32_16x16x32_bf16 v[92:95], v[144:147], a[252:255], v[92:95]\n"
"ds_read_b128 v[216:219], v230 offset:17408\n"
"v_exp_f32 v164, v164\n"
"v_exp_f32 v178, v178\n"
"v_exp_f32 v179, v179\n"
"v_mfma_f32_16x16x32_bf16 v[60:63], v[140:143], v[224:227], 0\n"
"s_add_u32 m0, s44, 49152\n"
"ds_read_b64_tr_b16 a[244:245], v232 offset:0\n"
"v_mul_f32_dpp v19, v246, v19 row_newbcast:3 row_mask:0xf bank_mask:0xf\n"
"v_mfma_f32_16x16x32_bf16 v[60:63], v[136:139], a[252:255], v[60:63]\n"
"buffer_load_dwordx4 v240, s[16:19], s56 offen nt lds\n"
"v_mfma_f32_16x16x32_bf16 v[28:31], v[132:135], v[224:227], 0\n"
"v_mul_f32_dpp v82, v206, v82 row_newbcast:2 row_mask:0xf bank_mask:0xf\n"
"v_cvt_pk_bf16_f32 v112, v160, v161\n"
"v_mfma_f32_16x16x32_bf16 v[28:31], v[128:131], a[252:255], v[28:31]\n"
"v_exp_f32 v154, v154\n"
"v_mfma_f32_16x16x32_bf16 a[236:239], v[124:127], a[252:255], 0\n"
"v_cvt_pk_bf16_f32 v17, v18, v19\n"
"v_cvt_pk_bf16_f32 v104, v156, v157\n"
"v_smfmac_f32_16x16x64_bf16 v[198:201], a[240:243], v[136:143], v241\n"
"s_waitcnt lgkmcnt(3)\n"
"ds_read_b64_tr_b16 v[226:227], v237 offset:4096\n"
"v_cvt_pk_bf16_f32 v48, v48, v49\n"
"v_mfma_f32_16x16x32_bf16 v[156:159], v[208:211], a[0:3], v[180:183]\n"
"v_mul_f32_dpp v52, v247, v52 row_newbcast:0 row_mask:0xf bank_mask:0xf\n"
"v_cvt_pk_bf16_f32 v120, v164, v165\n"
"v_mfma_f32_16x16x32_bf16 v[156:159], v[212:215], a[4:7], v[156:159]\n"
"s_waitcnt lgkmcnt(2)\n"
"ds_read_b64_tr_b16 v[224:225], v237 offset:0\n"
"v_cvt_pk_bf16_f32 v113, v162, v163\n"
"v_mfma_f32_16x16x32_bf16 v[156:159], v[216:219], a[8:11], v[156:159]\n"
"v_exp_f32 v176, v176\n"
"v_mfma_f32_16x16x32_bf16 v[156:159], v[220:223], a[12:15], v[156:159]\n"
"ds_read_b64_tr_b16 a[246:247], v232 offset:4096\n"
"ds_read_b64_tr_b16 a[254:255], v233 offset:4096\n"
"v_mfma_f32_16x16x32_bf16 v[160:163], v[216:219], a[24:27], v[184:187]\n"
"ds_read_b64_tr_b16 a[250:251], v236 offset:4096\n"
"v_cvt_pk_bf16_f32 v49, v50, v51\n"
"v_mfma_f32_16x16x32_bf16 v[160:163], v[220:223], a[28:31], v[160:163]\n"
"v_exp_f32 v168, v168\n"
"v_mfma_f32_16x16x32_bf16 v[160:163], v[208:211], a[16:19], v[160:163]\n"
"s_add_u32 s40, s56, s52\n"
"ds_read_b64_tr_b16 a[252:253], v233 offset:0\n"
"v_cvt_pk_bf16_f32 v121, v166, v167\n"
"v_mfma_f32_16x16x32_bf16 v[160:163], v[212:215], a[20:23], v[160:163]\n"
"buffer_load_dwordx4 v240, s[16:19], s40 offen offset:1024 nt lds\n"
"v_mfma_f32_16x16x32_bf16 v[164:167], v[208:211], a[32:35], v[188:191]\n"
"v_cvt_pk_bf16_f32 v51, v54, v55\n"
"v_add_u32 v243, 0x1000, v243\n"
"v_mfma_f32_16x16x32_bf16 v[164:167], v[212:215], a[36:39], v[164:167]\n"
"v_exp_f32 v177, v177\n"
"v_mfma_f32_16x16x32_bf16 v[164:167], v[216:219], a[40:43], v[164:167]\n"
"ds_read_b128 v[208:211], v230 offset:20480\n"
"v_cvt_pk_bf16_f32 v115, v178, v179\n"
"v_mfma_f32_16x16x32_bf16 v[164:167], v[220:223], a[44:47], v[164:167]\n"
"s_waitcnt lgkmcnt(3)\n"
"v_cvt_pk_bf16_f32 v50, v52, v53\n"
"v_mul_f32_dpp v83, v206, v83 row_newbcast:3 row_mask:0xf bank_mask:0xf\n"
"v_mfma_f32_16x16x32_bf16 a[208:211], v[124:127], a[244:247], 0\n"
"v_cvt_pk_bf16_f32 v19, v22, v23\n"
"v_cvt_pk_bf16_f32 v114, v176, v177\n"
"v_mfma_f32_16x16x32_bf16 v[0:3], v[128:131], a[244:247], 0\n"
"s_waitcnt lgkmcnt(2)\n"
"ds_read_b128 v[216:219], v230 offset:21504\n"
"v_xor_b32 v249, s75, v249\n"
"v_mfma_f32_16x16x32_bf16 v[0:3], v[132:135], a[248:251], v[0:3]\n"
"v_exp_f32 v175, v175\n"
"v_mfma_f32_16x16x32_bf16 v[32:35], v[136:139], a[244:247], 0\n"
"s_add_u32 s40, s56, s53\n"
"ds_read_b128 v[212:215], v231 offset:20480\n"
"v_cvt_pk_bf16_f32 v18, v20, v21\n"
"v_mfma_f32_16x16x32_bf16 v[32:35], v[140:143], a[248:251], v[32:35]\n"
"buffer_load_dwordx4 v240, s[16:19], s40 offen offset:2048 nt lds\n"
"v_mfma_f32_16x16x32_bf16 v[64:67], v[144:147], a[244:247], 0\n"
"v_cvt_pk_bf16_f32 v98, v168, v169\n"
"v_cvt_pk_bf16_f32 v107, v174, v175\n"
"v_mfma_f32_16x16x32_bf16 v[64:67], v[148:151], a[248:251], v[64:67]\n"
"v_exp_f32 v153, v153\n"
"v_mfma_f32_16x16x32_bf16 v[68:71], v[148:151], v[224:227], 0\n"
"s_waitcnt lgkmcnt(2)\n"
"ds_read_b128 v[220:223], v231 offset:21504\n"
"v_cvt_pk_bf16_f32 v106, v172, v173\n"
"v_mfma_f32_16x16x32_bf16 v[68:71], v[144:147], a[252:255], v[68:71]\n"
"ds_write_b128 v248, v[48:51] offset:50176\n"
"v_cvt_pk_bf16_f32 v80, v80, v81\n"
"v_cvt_pk_bf16_f32 v81, v82, v83\n"
"v_exp_f32 v170, v170\n"
"v_exp_f32 v159, v159\n"
"v_mfma_f32_16x16x32_bf16 v[36:39], v[140:143], v[224:227], 0\n"
"v_exp_f32 v167, v167\n"
"v_mfma_f32_16x16x32_bf16 v[36:39], v[136:139], a[252:255], v[36:39]\n"
"ds_read_b64_tr_b16 a[74:75], v249 offset:50184\n"
"ds_read_b64_tr_b16 a[244:245], v232 offset:1024\n"
"v_mfma_f32_16x16x32_bf16 v[4:7], v[132:135], v[224:227], 0\n"
"ds_read_b64_tr_b16 a[72:73], v249 offset:50176\n"
"v_cvt_pk_bf16_f32 v83, v86, v87\n"
"v_mfma_f32_16x16x32_bf16 v[4:7], v[128:131], a[252:255], v[4:7]\n"
"v_exp_f32 v166, v166\n"
"v_mfma_f32_16x16x32_bf16 a[212:215], v[124:127], a[252:255], 0\n"
"s_add_u32 s40, s56, s54\n"
"ds_write_b128 v248, v[16:19] offset:49152\n"
"v_cvt_pk_bf16_f32 v99, v170, v171\n"
"v_exp_f32 v161, v161\n"
"v_exp_f32 v164, v164\n"
"v_smfmac_f32_16x16x64_bf16 v[200:203], a[240:243], v[144:151], v241\n"
"buffer_load_dwordx4 v240, s[16:19], s40 offen offset:3072 nt lds\n"
"v_mfma_f32_16x16x32_bf16 v[168:171], v[208:211], a[0:3], v[180:183]\n"
"s_waitcnt lgkmcnt(5)\n"
"ds_read_b64_tr_b16 a[58:59], v249 offset:49160\n"
"ds_read_b64_tr_b16 a[246:247], v232 offset:5120\n"
"v_mfma_f32_16x16x32_bf16 v[168:171], v[212:215], a[4:7], v[168:171]\n"
"ds_read_b64_tr_b16 a[56:57], v249 offset:49152\n"
"v_cvt_pk_bf16_f32 v82, v84, v85\n"
"v_mfma_f32_16x16x32_bf16 v[168:171], v[216:219], a[8:11], v[168:171]\n"
"ds_write_b128 v248, v[80:83] offset:51200\n"
"v_exp_f32 v156, v156\n"
"v_exp_f32 v158, v158\n"
"v_exp_f32 v165, v165\n"
"v_mfma_f32_16x16x32_bf16 v[168:171], v[220:223], a[12:15], v[168:171]\n"
"ds_read_b64_tr_b16 a[88:89], v249 offset:51200\n"
"ds_read_b64_tr_b16 a[90:91], v249 offset:51208\n"
"v_mfma_f32_16x16x32_bf16 v[172:175], v[216:219], a[24:27], v[184:187]\n"
"ds_read_b64_tr_b16 a[248:249], v236 offset:1024\n"
"ds_read_b64_tr_b16 a[250:251], v236 offset:5120\n"
"v_mfma_f32_16x16x32_bf16 v[172:175], v[220:223], a[28:31], v[172:175]\n"
"v_xor_b32 v248, s75, v248\n"
"v_cvt_pk_bf16_f32 v117, v166, v167\n"
"v_mfma_f32_16x16x32_bf16 v[172:175], v[208:211], a[16:19], v[172:175]\n"
"ds_read_b64_tr_b16 v[226:227], v237 offset:5120\n"
"v_mfma_f32_16x16x32_bf16 v[172:175], v[212:215], a[20:23], v[172:175]\n"
"s_cbranch_vccnz LPCT1_%=\nLPKT1_%=:\n"
"ds_read_b64_tr_b16 a[254:255], v233 offset:5120\n"
"v_mfma_f32_16x16x32_bf16 v[176:179], v[208:211], a[32:35], v[188:191]\n"
"v_exp_f32 v163, v163\n"
"v_mfma_f32_16x16x32_bf16 v[176:179], v[212:215], a[36:39], v[176:179]\n"
"ds_read_b64_tr_b16 v[224:225], v237 offset:1024\n"
"v_cvt_pk_bf16_f32 v101, v158, v159\n"
"v_mfma_f32_16x16x32_bf16 v[176:179], v[216:219], a[40:43], v[176:179]\n"
"v_exp_f32 v175, v175\n"
"v_mfma_f32_16x16x32_bf16 v[176:179], v[220:223], a[44:47], v[176:179]\n"
"s_waitcnt lgkmcnt(5)\n"
"ds_read_b64_tr_b16 a[252:253], v233 offset:1024\n"
"v_mfma_f32_16x16x32_bf16 a[224:227], v[124:127], a[244:247], 0\n"
"v_exp_f32 v160, v160\n"
"v_mfma_f32_16x16x32_bf16 v[16:19], v[128:131], a[244:247], 0\n"
"s_waitcnt vmcnt(8) lgkmcnt(4)\n"
"v_exp_f32 v173, v173\n"
"v_mfma_f32_16x16x32_bf16 v[16:19], v[132:135], a[248:251], v[16:19]\n"
"s_barrier\n"
"ds_read_b128 v[216:219], v228 offset:33792\n"
"v_exp_f32 v155, v155\n"
"v_exp_f32 v157, v157\n"
"v_mfma_f32_16x16x32_bf16 v[48:51], v[136:139], a[244:247], 0\n"
"ds_read_b128 v[220:223], v229 offset:33792\n"
"v_cvt_pk_bf16_f32 v100, v156, v157\n"
"v_mfma_f32_16x16x32_bf16 v[48:51], v[140:143], a[248:251], v[48:51]\n"
"ds_read_b128 v[208:211], v228 offset:32768\n"
"ds_read_b128 v[212:215], v229 offset:32768\n"
"v_mfma_f32_16x16x32_bf16 v[80:83], v[144:147], a[244:247], 0\n"
"ds_read_b64_tr_b16 a[244:245], v234 offset:16384\n"
"ds_read_b64_tr_b16 a[246:247], v234 offset:20480\n"
"v_mfma_f32_16x16x32_bf16 v[80:83], v[148:151], a[248:251], v[80:83]\n"
"s_waitcnt lgkmcnt(7)\n"
"ds_read_b64_tr_b16 a[248:249], v238 offset:16384\n"
"ds_read_b64_tr_b16 a[250:251], v238 offset:20480\n"
"v_mfma_f32_16x16x32_bf16 v[84:87], v[148:151], v[224:227], 0\n"
"s_waitcnt lgkmcnt(8)\n"
"v_exp_f32 v162, v162\n"
"v_mfma_f32_16x16x32_bf16 v[84:87], v[144:147], a[252:255], v[84:87]\n"
"v_exp_f32 v152, v152\n"
"v_mfma_f32_16x16x32_bf16 v[52:55], v[140:143], v[224:227], 0\n"
"v_exp_f32 v178, v178\n"
"v_mfma_f32_16x16x32_bf16 v[52:55], v[136:139], a[252:255], v[52:55]\n"
"v_cvt_pk_bf16_f32 v122, v152, v153\n"
"v_cvt_pk_bf16_f32 v109, v162, v163\n"
"v_mfma_f32_16x16x32_bf16 v[20:23], v[132:135], v[224:227], 0\n"
"ds_read_b64_tr_b16 v[226:227], v239 offset:20480\n"
"v_cvt_pk_bf16_f32 v123, v154, v155\n"
"v_mfma_f32_16x16x32_bf16 v[20:23], v[128:131], a[252:255], v[20:23]\n"
"v_exp_f32 v169, v169\n"
"v_mfma_f32_16x16x32_bf16 a[228:231], v[124:127], a[252:255], 0\n"
"s_waitcnt lgkmcnt(3)\n"
"v_xor_b32 v249, s75, v249\n"
"s_branch LAY2_%=\n"
"LAX_%=:\n"
"v_mfma_f32_16x16x32_bf16 a[224:227], v[124:127], a[244:247], a[224:227]\n"
"v_exp_f32 v168, v168\n"
"v_mfma_f32_16x16x32_bf16 v[16:19], v[128:131], a[244:247], v[16:19]\n"
"s_waitcnt vmcnt(8) lgkmcnt(0)\n"
"v_exp_f32 v169, v169\n"
"v_mfma_f32_16x16x32_bf16 v[16:19], v[132:135], a[248:251], v[16:19]\n"
"s_barrier\n"
"v_mfma_f32_16x16x32_bf16 v[48:51], v[136:139], a[244:247], v[48:51]\n"
"v_exp_f32 v175, v175\n"
"v_mfma_f32_16x16x32_bf16 v[48:51], v[140:143], a[248:251], v[48:51]\n"
"v_exp_f32 v178, v178\n"
"v_mfma_f32_16x16x32_bf16 v[80:83], v[144:147], a[244:247], v[80:83]\n"
"ds_read_b64_tr_b16 a[246:247], v234 offset:53248\n"
"v_cvt_pk_bf16_f32 v109, v162, v163\n"
"v_mfma_f32_16x16x32_bf16 v[80:83], v[148:151], a[248:251], v[80:83]\n"
"ds_read_b64_tr_b16 a[244:245], v234 offset:49152\n"
"v_cvt_pk_bf16_f32 v123, v154, v155\n"
"v_cvt_pk_bf16_f32 v116, v164, v165\n"
"v_exp_f32 v174, v174\n"
"v_exp_f32 v176, v176\n"
"v_exp_f32 v177, v177\n"
"v_exp_f32 v179, v179\n"
"v_accvgpr_read_b32 v253, a96\n"
"v_mfma_f32_16x16x32_bf16 v[84:87], v[148:151], v[224:227], v[84:87]\n"
"ds_read_b64_tr_b16 a[248:249], v238 offset:49152\n"
"v_cvt_pk_bf16_f32 v111, v174, v175\n"
"v_mfma_f32_16x16x32_bf16 v[84:87], v[144:147], a[252:255], v[84:87]\n"
"ds_read_b64_tr_b16 a[250:251], v238 offset:53248\n"
"v_exp_f32 v156, v156\n"
"v_cvt_pk_bf16_f32 v101, v158, v159\n"
"v_cvt_pk_bf16_f32 v108, v160, v161\n"
"v_exp_f32 v170, v170\n"
"v_exp_f32 v171, v171\n"
"v_cvt_pk_bf16_f32 v118, v176, v177\n"
"v_and_b32 v245, 0x3f0, v242\n"
"v_mfma_f32_16x16x32_bf16 v[52:55], v[140:143], v[224:227], v[52:55]\n"
"v_exp_f32 v172, v172\n"
"v_mfma_f32_16x16x32_bf16 v[52:55], v[136:139], a[252:255], v[52:55]\n"
"v_cvt_pk_bf16_f32 v119, v178, v179\n"
"v_and_b32 v253, 0x7fff0000, v253\n"
"v_mfma_f32_16x16x32_bf16 v[20:23], v[132:135], v[224:227], v[20:23]\n"
"v_cvt_pk_bf16_f32 v102, v168, v169\n"
"v_cvt_pk_bf16_f32 v103, v170, v171\n"
"v_mfma_f32_16x16x32_bf16 v[20:23], v[128:131], a[252:255], v[20:23]\n"
"v_cvt_pk_bf16_f32 v100, v156, v157\n"
"v_lshlrev_b32_e64 v244, 24, s43\n"
"v_mfma_f32_16x16x32_bf16 a[228:231], v[124:127], a[252:255], a[228:231]\n"
"s_waitcnt lgkmcnt(2)\n"
"ds_read_b64_tr_b16 v[224:225], v239 offset:49152\n"
"v_cvt_pk_bf16_f32 v117, v166, v167\n"
"v_mfma_f32_16x16x32_bf16 a[216:219], v[120:123], a[244:247], a[216:219]\n"
"s_mov_b64 s[28:29], s[8:9]\n"
"v_exp_f32 v173, v173\n"
"v_smfmac_f32_16x16x64_bf16 v[196:199], a[240:243], v[96:103], v241\n"
"s_mov_b32 s30, 655360\n"
"ds_read_b64_tr_b16 a[254:255], v235 offset:53248\n"
"ds_read_b64_tr_b16 v[226:227], v239 offset:53248\n"
"v_mfma_f32_16x16x32_bf16 v[8:11], v[96:99], a[244:247], v[8:11]\n"
"s_waitcnt lgkmcnt(3)\n"
"s_mov_b32 s71, s44\n"
"ds_read_b64_tr_b16 a[252:253], v235 offset:49152\n"
"v_max_u32_dpp v253, v253, v253 quad_perm:[1,0,3,2] row_mask:0xf bank_mask:0xf\n"
"v_mfma_f32_16x16x32_bf16 v[8:11], v[100:103], a[248:251], v[8:11]\n"
"s_mov_b32 s70, s95\n"
"v_and_b32 v252, 0x7fff0000, v252\n"
"v_max_u32_dpp v253, v253, v253 quad_perm:[2,3,0,1] row_mask:0xf bank_mask:0xf\n"
"v_smfmac_f32_16x16x64_bf16 v[200:203], a[240:243], v[112:119], v241\n"
"v_cvt_pk_bf16_f32 v110, v172, v173\n"
"v_max_u32_dpp v252, v252, v252 quad_perm:[1,0,3,2] row_mask:0xf bank_mask:0xf\n"
"v_mfma_f32_16x16x32_bf16 v[40:43], v[104:107], a[244:247], v[40:43]\n"
"s_nop 0\n"
"v_max_u32_dpp v252, v252, v252 quad_perm:[2,3,0,1] row_mask:0xf bank_mask:0xf\n"
"v_max_u32_dpp v253, v253, v253 row_half_mirror row_mask:0xf bank_mask:0xf\n"
"v_mfma_f32_16x16x32_bf16 v[40:43], v[108:111], a[248:251], v[40:43]\n"
"s_add_u32 s41, s70, 327680\n"
"s_add_u32 m0, s71, 32768\n"
"v_add_f32 v196, v196, v197\n"
"v_smfmac_f32_16x16x64_bf16 v[198:201], a[240:243], v[104:111], v241\n"
"buffer_load_dwordx4 v240, s[28:31], s41 offen lds\n"
"v_mfma_f32_16x16x32_bf16 v[72:75], v[112:115], a[244:247], v[72:75]\n"
"v_min_u32_dpp v253, v253, v253 row_mirror row_mask:0xf bank_mask:0xf\n"
"v_lshrrev_b32 v245, 1, v245\n"
"v_mfma_f32_16x16x32_bf16 v[72:75], v[116:119], a[248:251], v[72:75]\n"
"v_max_u32_dpp v252, v252, v252 row_half_mirror row_mask:0xf bank_mask:0xf\n"
"v_min_u32_dpp v253, v253, v253 row_bcast:15 row_mask:0xa bank_mask:0xf\n"
"v_mfma_f32_16x16x32_bf16 v[202:205], a[240:243], v[120:123], v[202:205]\n"
"s_waitcnt lgkmcnt(0)\n"
"ds_read_b64_tr_b16 a[244:245], v234 offset:50176\n"
"ds_read_b64_tr_b16 a[248:249], v238 offset:50176\n"
"v_mfma_f32_16x16x32_bf16 v[76:79], v[116:119], v[224:227], v[76:79]\n"
"s_add_u32 s40, s41, s52\n"
"ds_read_b64_tr_b16 a[246:247], v234 offset:54272\n"
"ds_read_b64_tr_b16 a[250:251], v238 offset:54272\n"
"v_mfma_f32_16x16x32_bf16 v[76:79], v[112:115], a[252:255], v[76:79]\n"
"buffer_load_dwordx4 v240, s[28:31], s40 offen offset:1024 lds\n"
"v_mfma_f32_16x16x32_bf16 v[44:47], v[108:111], v[224:227], v[44:47]\n"
"v_min_u32_dpp v252, v252, v252 row_mirror row_mask:0xf bank_mask:0xf\n"
"v_mul_f32 v244, 0x45800000, v244\n"
"v_mfma_f32_16x16x32_bf16 v[44:47], v[104:107], a[252:255], v[44:47]\n"
"v_add_f32 v198, v198, v199\n"
"v_add_f32 v244, v244, v202\n"
"v_mfma_f32_16x16x32_bf16 v[12:15], v[100:103], v[224:227], v[12:15]\n"
"s_add_u32 s40, s41, s53\n"
"ds_read_b64_tr_b16 v[224:225], v239 offset:50176\n"
"ds_read_b64_tr_b16 v[226:227], v239 offset:54272\n"
"v_mfma_f32_16x16x32_bf16 v[12:15], v[96:99], a[252:255], v[12:15]\n"
"buffer_load_dwordx4 v240, s[28:31], s40 offen offset:2048 lds\n"
"v_min_u32_dpp v252, v252, v252 row_bcast:15 row_mask:0xa bank_mask:0xf\n"
"v_add_f32 v200, v200, v201\n"
"v_min3_f32 v244, v244, v196, v198\n"
"v_min_f32 v244, v244, v200\n"
"v_rcp_f32 v244, v244\n"
"v_sub_u32 v245, v242, v245\n"
"v_mfma_f32_16x16x32_bf16 a[220:223], v[120:123], a[252:255], a[220:223]\n"
"s_waitcnt lgkmcnt(3)\n"
"v_min_u32_dpp v253, v253, v253 row_bcast:31 row_mask:0xc bank_mask:0xf\n"
"v_add_f32 v254, v196, v198\n"
"v_add_f32 v254, v254, v200\n"
"v_add_f32 v254, v254, v202\n"
"v_fmac_f32 v254, 0x68800000, v244\n"
"v_cmp_nge_f32 vcc, s45, v254\n"
"s_cbranch_vccnz LSLOW_%=\nLBACK3_%=:\ns_mov_b64 vcc, s[64:65]\n"
"v_mfma_f32_16x16x32_bf16 a[232:235], v[120:123], a[244:247], a[232:235]\n"
"ds_read_b64_tr_b16 a[252:253], v235 offset:50176\n"
"ds_read_b64_tr_b16 a[254:255], v235 offset:54272\n"
"v_min_u32_dpp v252, v252, v252 row_bcast:31 row_mask:0xc bank_mask:0xf\n"
"v_rcp_f32 v246, v196\n"
"v_xor_b32 v244, 0x11000, v242\n"
"v_bfe_u32 v254, v242, 9, 1\n"
"v_mfma_f32_16x16x32_bf16 v[24:27], v[96:99], a[244:247], v[24:27]\n"
"s_waitcnt lgkmcnt(4)\n"
"v_rcp_f32 v247, v198\n"
"v_mfma_f32_16x16x32_bf16 v[24:27], v[100:103], a[248:251], v[24:27]\n"
"ds_write_b128 v244, a[216:219] offset:32768\n"
"v_rcp_f32 v206, v200\n"
"v_mov_b32_dpp v246, v246 row_ror:12 row_mask:0x2 bank_mask:0xf\n"
"v_mov_b32_dpp v247, v247 row_ror:12 row_mask:0x2 bank_mask:0xf\n"
"v_mfma_f32_16x16x32_bf16 v[56:59], v[104:107], a[244:247], v[56:59]\n"
"s_add_u32 s40, s41, s54\n"
"v_mov_b32_dpp v246, v246 row_ror:8 row_mask:0x4 bank_mask:0xf\n"
"v_lshl_add_u32 v254, v254, 4, s43\n"
"v_mfma_f32_16x16x32_bf16 v[56:59], v[108:111], a[248:251], v[56:59]\n"
"buffer_load_dwordx4 v240, s[28:31], s40 offen offset:3072 lds\n"
"v_mfma_f32_16x16x32_bf16 v[88:91], v[112:115], a[244:247], v[88:91]\n"
"s_add_u32 s71, s44, s75\n"
"v_mul_f32 v252, 0x43340000, v252\n"
"v_mov_b32_dpp v246, v246 row_ror:4 row_mask:0x8 bank_mask:0xf\n"
"v_mfma_f32_16x16x32_bf16 v[88:91], v[116:119], a[248:251], v[88:91]\n"
"s_waitcnt lgkmcnt(3)\n"
"s_mov_b32 s70, s100\n"
"ds_read_b64_tr_b16 a[244:245], v232 offset:49152\n"
"ds_read_b64_tr_b16 a[246:247], v232 offset:53248\n"
"ds_read_b64_tr_b16 a[248:249], v236 offset:49152\n"
"ds_read_b64_tr_b16 a[250:251], v236 offset:53248\n"
"v_mov_b32_dpp v247, v247 row_ror:8 row_mask:0x4 bank_mask:0xf\n"
"v_lshl_add_u32 v254, v254, 6, s75\n"
"v_mul_f32_dpp v9, v246, v9 row_newbcast:1 row_mask:0xf bank_mask:0xf\n"
"v_mul_f32_dpp v10, v246, v10 row_newbcast:2 row_mask:0xf bank_mask:0xf\n"
"v_mul_f32_dpp v11, v246, v11 row_newbcast:3 row_mask:0xf bank_mask:0xf\n"
"v_mul_f32_dpp v24, v246, v24 row_newbcast:0 row_mask:0xf bank_mask:0xf\n"
"v_mfma_f32_16x16x32_bf16 v[92:95], v[116:119], v[224:227], v[92:95]\n"
"s_waitcnt lgkmcnt(5)\n"
"s_add_u32 s41, s70, 163840\n"
"s_add_u32 m0, s71, 16384\n"
"v_mul_f32_dpp v14, v246, v14 row_newbcast:2 row_mask:0xf bank_mask:0xf\n"
"v_mfma_f32_16x16x32_bf16 v[92:95], v[112:115], a[252:255], v[92:95]\n"
"buffer_load_dwordx4 v240, s[28:31], s41 offen lds\n"
"v_mfma_f32_16x16x32_bf16 v[60:63], v[108:111], v[224:227], v[60:63]\n"
"v_mul_f32 v252, v252, v253\n"
"v_mov_b32_dpp v206, v206 row_ror:12 row_mask:0x2 bank_mask:0xf\n"
"v_mfma_f32_16x16x32_bf16 v[60:63], v[104:107], a[252:255], v[60:63]\n"
"ds_write_b128 v244, a[220:223] offset:33792\n"
"v_mov_b32_dpp v247, v247 row_ror:4 row_mask:0x8 bank_mask:0xf\n"
"v_sub_u32 v228, v232, v254\n"
"v_mul_f32_dpp v8, v246, v8 row_newbcast:0 row_mask:0xf bank_mask:0xf\n"
"v_mul_f32_dpp v25, v246, v25 row_newbcast:1 row_mask:0xf bank_mask:0xf\n"
"v_mul_f32_dpp v26, v246, v26 row_newbcast:2 row_mask:0xf bank_mask:0xf\n"
"v_mfma_f32_16x16x32_bf16 v[28:31], v[100:103], v[224:227], v[28:31]\n"
"s_add_u32 s40, s41, s52\n"
"v_min_f32 v252, 0x71800000, v252\n"
"v_mul_f32_dpp v12, v246, v12 row_newbcast:0 row_mask:0xf bank_mask:0xf\n"
"v_mfma_f32_16x16x32_bf16 v[28:31], v[96:99], a[252:255], v[28:31]\n"
"buffer_load_dwordx4 v240, s[28:31], s40 offen offset:1024 lds\n"
"v_mfma_f32_16x16x32_bf16 a[236:239], v[120:123], a[252:255], a[236:239]\n"
"s_waitcnt lgkmcnt(3)\n"
"ds_read_b64_tr_b16 a[252:253], v233 offset:49152\n"
"ds_read_b64_tr_b16 a[254:255], v233 offset:53248\n"
"ds_read_b64_tr_b16 v[224:225], v237 offset:49152\n"
"ds_read_b64_tr_b16 v[226:227], v237 offset:53248\n"
"v_readlane_b32 s23, v252, 63\n"
"v_mov_b32_dpp v206, v206 row_ror:8 row_mask:0x4 bank_mask:0xf\n"
"v_cvt_pk_bf16_f32 v8, v8, v9\n"
"v_mul_f32_dpp v47, v247, v47 row_newbcast:3 row_mask:0xf bank_mask:0xf\n"
"v_mul_f32_dpp v58, v247, v58 row_newbcast:2 row_mask:0xf bank_mask:0xf\n"
"v_mul_f32_dpp v60, v247, v60 row_newbcast:0 row_mask:0xf bank_mask:0xf\n"
"v_mfma_f32_16x16x32_bf16 a[208:211], v[120:123], a[244:247], a[208:211]\n"
"ds_write_b128 v244, a[232:235] offset:34816\n"
"v_readlane_b32 s22, v252, 63\n"
"v_mov_b32_dpp v206, v206 row_ror:4 row_mask:0x8 bank_mask:0xf\n"
"v_mul_f32_dpp v15, v246, v15 row_newbcast:3 row_mask:0xf bank_mask:0xf\n"
"v_cvt_pk_bf16_f32 v9, v10, v11\n"
"v_mul_f32_dpp v41, v247, v41 row_newbcast:1 row_mask:0xf bank_mask:0xf\n"
"v_mfma_f32_16x16x32_bf16 v[0:3], v[96:99], a[244:247], v[0:3]\n"
"s_waitcnt lgkmcnt(6)\n"
"v_sub_u32 v231, v235, v254\n"
"v_mov_b64 v[196:197], s[22:23]\n"
"v_mfma_f32_16x16x32_bf16 v[0:3], v[100:103], a[248:251], v[0:3]\n"
"ds_write_b128 v244, a[236:239] offset:35840\n"
"v_mul_f32_dpp v42, v247, v42 row_newbcast:2 row_mask:0xf bank_mask:0xf\n"
"v_mul_f32_dpp v75, v206, v75 row_newbcast:3 row_mask:0xf bank_mask:0xf\n"
"v_mul_f32_dpp v77, v206, v77 row_newbcast:1 row_mask:0xf bank_mask:0xf\n"
"v_cvt_pk_bf16_f32 v24, v24, v25\n"
"v_mul_f32_dpp v63, v247, v63 row_newbcast:3 row_mask:0xf bank_mask:0xf\n"
"v_mfma_f32_16x16x32_bf16 v[32:35], v[104:107], a[244:247], v[32:35]\n"
"s_add_u32 s40, s41, s53\n"
"v_xor_b32 v250, 0x1000, v245\n"
"v_mul_f32_dpp v94, v206, v94 row_newbcast:2 row_mask:0xf bank_mask:0xf\n"
"v_mfma_f32_16x16x32_bf16 v[32:35], v[108:111], a[248:251], v[32:35]\n"
"buffer_load_dwordx4 v240, s[28:31], s40 offen offset:2048 lds\n"
"v_mfma_f32_16x16x32_bf16 v[64:67], v[112:115], a[244:247], v[64:67]\n"
"v_mul_f32_dpp v13, v246, v13 row_newbcast:1 row_mask:0xf bank_mask:0xf\n"
"v_mul_f32_dpp v30, v246, v30 row_newbcast:2 row_mask:0xf bank_mask:0xf\n"
"v_mfma_f32_16x16x32_bf16 v[64:67], v[116:119], a[248:251], v[64:67]\n"
"s_waitcnt lgkmcnt(2)\n"
"ds_read_b64_tr_b16 a[246:247], v232 offset:54272\n"
"ds_read_b64_tr_b16 a[248:249], v236 offset:50176\n"
"v_mov_b32 v252, v192\n"
"v_cvt_pk_bf16_f32 v11, v14, v15\n"
"v_mul_f32_dpp v40, v247, v40 row_newbcast:0 row_mask:0xf bank_mask:0xf\n"
"v_mul_f32_dpp v43, v247, v43 row_newbcast:3 row_mask:0xf bank_mask:0xf\n"
"v_cvt_pk_bf16_f32 v40, v40, v41\n"
"v_mul_f32_dpp v78, v206, v78 row_newbcast:2 row_mask:0xf bank_mask:0xf\n"
"v_mul_f32_dpp v31, v246, v31 row_newbcast:3 row_mask:0xf bank_mask:0xf\n"
"v_mul_f32_dpp v92, v206, v92 row_newbcast:0 row_mask:0xf bank_mask:0xf\n"
"v_mfma_f32_16x16x32_bf16 v[68:71], v[116:119], v[224:227], v[68:71]\n"
"s_add_u32 s40, s41, s54\n"
"ds_read_b64_tr_b16 a[244:245], v232 offset:50176\n"
"ds_read_b64_tr_b16 a[250:251], v236 offset:54272\n"
"v_mfma_f32_16x16x32_bf16 v[68:71], v[112:115], a[252:255], v[68:71]\n"
"buffer_load_dwordx4 v240, s[28:31], s40 offen offset:3072 lds\n"
"v_mov_b32 v253, v202\n"
"v_mul_f32_dpp v44, v247, v44 row_newbcast:0 row_mask:0xf bank_mask:0xf\n"
"v_mul_f32_dpp v45, v247, v45 row_newbcast:1 row_mask:0xf bank_mask:0xf\n"
"v_mul_f32_dpp v46, v247, v46 row_newbcast:2 row_mask:0xf bank_mask:0xf\n"
"v_cvt_pk_bf16_f32 v41, v42, v43\n"
"v_mul_f32_dpp v72, v206, v72 row_newbcast:0 row_mask:0xf bank_mask:0xf\n"
"v_mul_f32_dpp v73, v206, v73 row_newbcast:1 row_mask:0xf bank_mask:0xf\n"
"v_mul_f32_dpp v2, v246, v2 row_newbcast:2 row_mask:0xf bank_mask:0xf\n"
"v_mfma_f32_16x16x32_bf16 v[36:39], v[108:111], v[224:227], v[36:39]\n"
"v_mul_f32_dpp v74, v206, v74 row_newbcast:2 row_mask:0xf bank_mask:0xf\n"
"v_add_u32 v236, s76, v249\n"
"v_mfma_f32_16x16x32_bf16 v[36:39], v[104:107], a[252:255], v[36:39]\n"
"ds_write_b64 v250, v[252:253] offset:52224\n"
"v_cvt_pk_bf16_f32 v10, v12, v13\n"
"v_mul_f32_dpp v76, v206, v76 row_newbcast:0 row_mask:0xf bank_mask:0xf\n"
"v_mul_f32_dpp v79, v206, v79 row_newbcast:3 row_mask:0xf bank_mask:0xf\n"
"v_mul_f32_dpp v93, v206, v93 row_newbcast:1 row_mask:0xf bank_mask:0xf\n"
"v_mul_f32_dpp v3, v246, v3 row_newbcast:3 row_mask:0xf bank_mask:0xf\n"
"v_mul_f32_dpp v33, v247, v33 row_newbcast:1 row_mask:0xf bank_mask:0xf\n"
"v_xor_b32 v232, 0x1000, v228\n"
"v_mfma_f32_16x16x32_bf16 v[4:7], v[100:103], v[224:227], v[4:7]\n"
"ds_read_b64_tr_b16 v[224:225], v237 offset:50176\n"
"ds_read_b64_tr_b16 v[226:227], v237 offset:54272\n"
"v_mfma_f32_16x16x32_bf16 v[4:7], v[96:99], a[252:255], v[4:7]\n"
"ds_write_b128 v248, v[8:11] offset:49152\n"
"v_cvt_pk_bf16_f32 v42, v44, v45\n"
"v_mul_f32_dpp v56, v247, v56 row_newbcast:0 row_mask:0xf bank_mask:0xf\n"
"v_mul_f32_dpp v61, v247, v61 row_newbcast:1 row_mask:0xf bank_mask:0xf\n"
"v_mul_f32_dpp v32, v247, v32 row_newbcast:0 row_mask:0xf bank_mask:0xf\n"
"v_mul_f32_dpp v35, v247, v35 row_newbcast:3 row_mask:0xf bank_mask:0xf\n"
"v_mfma_f32_16x16x32_bf16 a[212:215], v[120:123], a[252:255], a[212:215]\n"
"s_waitcnt lgkmcnt(5)\n"
"ds_read_b64_tr_b16 a[252:253], v233 offset:50176\n"
"ds_read_b64_tr_b16 a[254:255], v233 offset:54272\n"
"v_mfma_f32_16x16x32_bf16 a[224:227], v[120:123], a[244:247], a[224:227]\n"
"ds_read_b64_tr_b16 a[4:5], v249 offset:49152\n"
"v_mul_f32_dpp v62, v247, v62 row_newbcast:2 row_mask:0xf bank_mask:0xf\n"
"v_mfma_f32_16x16x32_bf16 v[16:19], v[96:99], a[244:247], v[16:19]\n"
"s_waitcnt vmcnt(8) lgkmcnt(1)\n"
"ds_read_b64_tr_b16 a[6:7], v249 offset:49160\n"
"v_cvt_pk_bf16_f32 v43, v46, v47\n"
"v_mfma_f32_16x16x32_bf16 v[16:19], v[100:103], a[248:251], v[16:19]\n"
"s_barrier\n"
"ds_read_b64_tr_b16 v[182:183], v228 offset:2048\n"
"ds_read_b64_tr_b16 v[186:187], v232 offset:2048\n"
"ds_read_b64_tr_b16 v[188:189], v228 offset:8192\n"
"ds_read_b64_tr_b16 v[208:209], v228 offset:16384\n"
"v_mul_f32_dpp v28, v246, v28 row_newbcast:0 row_mask:0xf bank_mask:0xf\n"
"v_mul_f32_dpp v57, v247, v57 row_newbcast:1 row_mask:0xf bank_mask:0xf\n"
"v_mul_f32_dpp v36, v247, v36 row_newbcast:0 row_mask:0xf bank_mask:0xf\n"
"v_mul_f32_dpp v38, v247, v38 row_newbcast:2 row_mask:0xf bank_mask:0xf\n"
"v_mfma_f32_16x16x32_bf16 v[48:51], v[104:107], a[244:247], v[48:51]\n"
"ds_read_b64_tr_b16 v[210:211], v228 offset:18432\n"
"v_mul_f32_dpp v90, v206, v90 row_newbcast:2 row_mask:0xf bank_mask:0xf\n"
"v_mfma_f32_16x16x32_bf16 v[48:51], v[108:111], a[248:251], v[48:51]\n"
"ds_read_b64_tr_b16 v[212:213], v232 offset:16384\n"
"ds_read_b64_tr_b16 v[218:219], v228 offset:26624\n"
"ds_read_b64_tr_b16 v[222:223], v232 offset:26624\n"
"ds_read_b64_tr_b16 a[202:203], v236 offset:2056\n"
"v_cvt_pk_bf16_f32 v72, v72, v73\n"
"v_cvt_pk_bf16_f32 v73, v74, v75\n"
"v_cvt_pk_bf16_f32 v56, v56, v57\n"
"v_mul_f32_dpp v0, v246, v0 row_newbcast:0 row_mask:0xf bank_mask:0xf\n"
"v_mul_f32_dpp v6, v246, v6 row_newbcast:2 row_mask:0xf bank_mask:0xf\n"
"v_mul_f32_dpp v64, v206, v64 row_newbcast:0 row_mask:0xf bank_mask:0xf\n"
"v_mfma_f32_16x16x32_bf16 v[80:83], v[112:115], a[244:247], v[80:83]\n"
"ds_write_b128 v248, v[40:43] offset:50176\n"
"v_cvt_pk_bf16_f32 v74, v76, v77\n"
"v_mul_f32_dpp v27, v246, v27 row_newbcast:3 row_mask:0xf bank_mask:0xf\n"
"v_mul_f32_dpp v59, v247, v59 row_newbcast:3 row_mask:0xf bank_mask:0xf\n"
"v_mul_f32_dpp v88, v206, v88 row_newbcast:0 row_mask:0xf bank_mask:0xf\n"
"v_mul_f32_dpp v66, v206, v66 row_newbcast:2 row_mask:0xf bank_mask:0xf\n"
"v_mfma_f32_16x16x32_bf16 v[80:83], v[116:119], a[248:251], v[80:83]\n"
"ds_read_b64_tr_b16 a[22:23], v249 offset:50184\n"
"ds_read_b64_tr_b16 v[180:181], v228 offset:0\n"
"ds_read_b64_tr_b16 v[184:185], v232 offset:0\n"
"ds_read_b64_tr_b16 v[192:193], v232 offset:8192\n"
"v_cvt_pk_bf16_f32 v75, v78, v79\n"
"v_mul_f32_dpp v29, v246, v29 row_newbcast:1 row_mask:0xf bank_mask:0xf\n"
"v_mul_f32_dpp v89, v206, v89 row_newbcast:1 row_mask:0xf bank_mask:0xf\n"
"v_mul_f32_dpp v34, v247, v34 row_newbcast:2 row_mask:0xf bank_mask:0xf\n"
"v_mul_f32_dpp v39, v247, v39 row_newbcast:3 row_mask:0xf bank_mask:0xf\n"
"v_mul_f32_dpp v68, v206, v68 row_newbcast:0 row_mask:0xf bank_mask:0xf\n"
"v_mfma_f32_16x16x32_bf16 v[84:87], v[116:119], v[224:227], v[84:87]\n"
"ds_write_b128 v248, v[72:75] offset:51200\n"
"v_cvt_pk_bf16_f32 v25, v26, v27\n"
"v_cvt_pk_bf16_f32 v26, v28, v29\n"
"v_cvt_pk_bf16_f32 v88, v88, v89\n"
"v_mul_f32_dpp v1, v246, v1 row_newbcast:1 row_mask:0xf bank_mask:0xf\n"
"v_mul_f32_dpp v65, v206, v65 row_newbcast:1 row_mask:0xf bank_mask:0xf\n"
"v_mfma_f32_16x16x32_bf16 v[84:87], v[112:115], a[252:255], v[84:87]\n"
"ds_read_b64_tr_b16 v[194:195], v232 offset:10240\n"
"ds_read_b64_tr_b16 v[214:215], v232 offset:18432\n"
"ds_read_b64_tr_b16 a[250:251], v236 offset:6152\n"
"v_cvt_pk_bf16_f32 v57, v58, v59\n"
"v_mul_f32_dpp v91, v206, v91 row_newbcast:3 row_mask:0xf bank_mask:0xf\n"
"v_mul_f32_dpp v95, v206, v95 row_newbcast:3 row_mask:0xf bank_mask:0xf\n"
"v_mul_f32_dpp v4, v246, v4 row_newbcast:0 row_mask:0xf bank_mask:0xf\n"
"v_mul_f32_dpp v5, v246, v5 row_newbcast:1 row_mask:0xf bank_mask:0xf\n"
"v_cvt_pk_bf16_f32 v0, v0, v1\n"
"v_cvt_pk_bf16_f32 v32, v32, v33\n"
"v_cvt_pk_bf16_f32 v64, v64, v65\n"
"v_mfma_f32_16x16x32_bf16 v[52:55], v[108:111], v[224:227], v[52:55]\n"
"ds_read_b64_tr_b16 v[190:191], v228 offset:10240\n"
"ds_read_b64_tr_b16 v[220:221], v232 offset:24576\n"
"v_mfma_f32_16x16x32_bf16 v[52:55], v[104:107], a[252:255], v[52:55]\n"
"ds_read_b64_tr_b16 a[20:21], v249 offset:50176\n"
"ds_read_b64_tr_b16 a[36:37], v249 offset:51200\n"
"ds_read_b64_tr_b16 a[38:39], v249 offset:51208\n"
"ds_read_b64_tr_b16 v[216:217], v228 offset:24576\n"
"v_cvt_pk_bf16_f32 v27, v30, v31\n"
"v_cvt_pk_bf16_f32 v58, v60, v61\n"
"v_mul_f32_dpp v7, v246, v7 row_newbcast:3 row_mask:0xf bank_mask:0xf\n"
"v_cvt_pk_bf16_f32 v1, v2, v3\n"
"v_mul_f32_dpp v69, v206, v69 row_newbcast:1 row_mask:0xf bank_mask:0xf\n"
"v_xor_b32 v244, 0x1000, v244\n"
"v_mfma_f32_16x16x32_bf16 v[20:23], v[100:103], v[224:227], v[20:23]\n"
"ds_write_b128 v248, v[24:27] offset:49152\n"
"v_sub_u32 v229, v233, v254\n"
"v_cvt_pk_bf16_f32 v59, v62, v63\n"
"v_cvt_pk_bf16_f32 v89, v90, v91\n"
"v_cvt_pk_bf16_f32 v91, v94, v95\n"
"v_cvt_pk_bf16_f32 v2, v4, v5\n"
"v_mfma_f32_16x16x32_bf16 v[20:23], v[96:99], a[252:255], v[20:23]\n"
"ds_write_b128 v248, v[56:59] offset:50176\n"
"v_cvt_pk_bf16_f32 v90, v92, v93\n"
"v_cvt_pk_bf16_f32 v33, v34, v35\n"
"v_mul_f32_dpp v67, v206, v67 row_newbcast:3 row_mask:0xf bank_mask:0xf\n"
"v_mul_f32_dpp v71, v206, v71 row_newbcast:3 row_mask:0xf bank_mask:0xf\n"
"v_mov_b64 v[198:199], s[22:23]\n"
"v_mfma_f32_16x16x32_bf16 a[228:231], v[120:123], a[252:255], a[228:231]\n"
"s_waitcnt lgkmcnt(14)\n"
"ds_write_b128 v248, v[88:91] offset:51200\n"
"v_cvt_pk_bf16_f32 v3, v6, v7\n"
"v_mul_f32_dpp v37, v247, v37 row_newbcast:1 row_mask:0xf bank_mask:0xf\n"
"v_cvt_pk_bf16_f32 v34, v36, v37\n"
"v_mul_f32_dpp v70, v206, v70 row_newbcast:2 row_mask:0xf bank_mask:0xf\n"
"v_xor_b32 v237, 0x400, v236\n"
"v_mfma_f32_16x16x32_bf16 v[124:127], v[180:183], a[96:99], v[196:199]\n"
"s_waitcnt lgkmcnt(13)\n"
"ds_read_b64_tr_b16 a[30:31], v249 offset:50184\n"
"ds_read_b64_tr_b16 a[44:45], v249 offset:51200\n"
"v_mfma_f32_16x16x32_bf16 v[124:127], v[184:187], a[100:103], v[124:127]\n"
"s_waitcnt lgkmcnt(10)\n"
"ds_read_b64_tr_b16 a[28:29], v249 offset:50176\n"
"ds_read_b64_tr_b16 a[244:245], v237 offset:4096\n"
"v_mfma_f32_16x16x32_bf16 v[124:127], v[188:191], a[104:107], v[124:127]\n"
"ds_read_b64_tr_b16 a[12:13], v249 offset:49152\n"
"ds_read_b64_tr_b16 a[14:15], v249 offset:49160\n"
"v_mfma_f32_16x16x32_bf16 v[124:127], v[192:195], a[108:111], v[124:127]\n"
"ds_write_b128 v248, v[0:3] offset:49152\n"
"v_cvt_pk_bf16_f32 v35, v38, v39\n"
"v_cvt_pk_bf16_f32 v65, v66, v67\n"
"v_cvt_pk_bf16_f32 v66, v68, v69\n"
"v_cvt_pk_bf16_f32 v67, v70, v71\n"
"v_mfma_f32_16x16x32_bf16 v[124:127], v[208:211], a[144:147], v[124:127]\n"
"ds_read_b64_tr_b16 a[46:47], v249 offset:51208\n"
"ds_read_b128 v[60:63], v244 offset:35840\n"
"v_mfma_f32_16x16x32_bf16 v[124:127], v[212:215], a[148:151], v[124:127]\n"
"s_waitcnt lgkmcnt(11)\n"
"ds_write_b128 v248, v[32:35] offset:50176\n"
"ds_write_b128 v248, v[64:67] offset:51200\n"
"v_mfma_f32_16x16x32_bf16 v[124:127], v[216:219], a[152:155], v[124:127]\n"
"ds_read_b64_tr_b16 a[2:3], v249 offset:49160\n"
"ds_read_b128 v[28:31], v244 offset:33792\n"
"v_mfma_f32_16x16x32_bf16 v[124:127], v[220:223], a[156:159], v[124:127]\n"
"ds_read_b64_tr_b16 a[16:17], v249 offset:50176\n"
"ds_read_b64_tr_b16 a[34:35], v249 offset:51208\n"
"ds_read_b64 v[250:251], v245 offset:52224\n"
"ds_read_b128 v[56:59], v244 offset:34816\n"
"ds_read_b64_tr_b16 a[204:205], v237 offset:2048\n"
"v_sub_u32 v230, v234, v254\n"
"v_xor_b32 v233, 0x1000, v229\n"
"v_mul_f32_dpp v22, v246, v22 row_newbcast:2 row_mask:0xf bank_mask:0xf\n"
"v_mfma_f32_16x16x32_bf16 v[140:143], v[220:223], a[172:175], v[196:199]\n"
"ds_read_b64_tr_b16 a[0:1], v249 offset:49152\n"
"ds_read_b64_tr_b16 a[246:247], v237 offset:4104\n"
"v_mfma_f32_16x16x32_bf16 v[140:143], v[216:219], a[168:171], v[140:143]\n"
"ds_read_b64_tr_b16 a[32:33], v249 offset:51200\n"
"ds_read_b128 v[24:27], v244 offset:32768\n"
"v_mfma_f32_16x16x32_bf16 v[140:143], v[212:215], a[164:167], v[140:143]\n"
"ds_read_b64_tr_b16 a[18:19], v249 offset:50184\n"
"ds_read_b64_tr_b16 a[248:249], v236 offset:6144\n"
"v_mfma_f32_16x16x32_bf16 v[140:143], v[208:211], a[160:163], v[140:143]\n"
"ds_read_b64_tr_b16 v[110:111], v233 offset:10240\n"
"ds_read_b64_tr_b16 a[254:255], v237 offset:6152\n"
"v_mfma_f32_16x16x32_bf16 v[140:143], v[192:195], a[124:127], v[140:143]\n"
"ds_read_b64_tr_b16 v[108:109], v233 offset:8192\n"
"ds_read_b64_tr_b16 a[252:253], v237 offset:6144\n"
"v_mfma_f32_16x16x32_bf16 v[140:143], v[188:191], a[120:123], v[140:143]\n"
"ds_read_b64_tr_b16 v[226:227], v233 offset:26624\n"
"ds_read_b64_tr_b16 a[242:243], v236 offset:4104\n"
"v_mfma_f32_16x16x32_bf16 v[140:143], v[184:187], a[116:119], v[140:143]\n"
"ds_read_b64_tr_b16 a[206:207], v237 offset:2056\n"
"ds_read_b64_tr_b16 v[98:99], v229 offset:2048\n"
"v_mfma_f32_16x16x32_bf16 v[140:143], v[180:183], a[112:115], v[140:143]\n"
"ds_read_b64_tr_b16 a[194:195], v236 offset:8\n"
"ds_read_b64_tr_b16 a[196:197], v237 offset:0\n"
"v_mfma_f32_16x16x32_bf16 v[156:159], v[180:183], a[128:131], v[196:199]\n"
"ds_read_b64_tr_b16 v[102:103], v233 offset:2048\n"
"ds_read_b64_tr_b16 v[106:107], v229 offset:10240\n"
"v_mfma_f32_16x16x32_bf16 v[156:159], v[184:187], a[132:135], v[156:159]\n"
"ds_read_b64_tr_b16 a[200:201], v236 offset:2048\n"
"ds_read_b64_tr_b16 v[224:225], v233 offset:24576\n"
"v_mfma_f32_16x16x32_bf16 v[156:159], v[188:191], a[136:139], v[156:159]\n"
"ds_read_b64_tr_b16 v[120:121], v229 offset:24576\n"
"ds_read_b64_tr_b16 a[240:241], v236 offset:4096\n"
"v_mfma_f32_16x16x32_bf16 v[156:159], v[192:195], a[140:143], v[156:159]\n"
"s_waitcnt lgkmcnt(14)\n"
"ds_read_b64_tr_b16 v[96:97], v229 offset:0\n"
"v_xor_b32 v234, 0x1000, v230\n"
"v_mfma_f32_16x16x32_bf16 v[156:159], v[208:211], a[176:179], v[156:159]\n"
"ds_read_b64_tr_b16 a[198:199], v237 offset:8\n"
"ds_read_b64_tr_b16 v[114:115], v229 offset:18432\n"
"v_mfma_f32_16x16x32_bf16 v[156:159], v[212:215], a[180:183], v[156:159]\n"
"s_add_u32 s41, s95, 491520\n"
"ds_read_b64_tr_b16 a[192:193], v236 offset:0\n"
"v_mul_f32_dpp v54, v247, v54 row_newbcast:2 row_mask:0xf bank_mask:0xf\n"
"v_mfma_f32_16x16x32_bf16 v[156:159], v[216:219], a[184:187], v[156:159]\n"
"s_add_u32 m0, s44, 49152\n"
"ds_read_b64_tr_b16 v[100:101], v233 offset:0\n"
"ds_read_b64_tr_b16 v[118:119], v233 offset:18432\n"
"v_mfma_f32_16x16x32_bf16 v[156:159], v[220:223], a[188:191], v[156:159]\n"
"s_waitcnt lgkmcnt(12)\n"
"buffer_load_dwordx4 v240, s[28:31], s41 offen lds\n"
"v_mfma_f32_16x16x32_bf16 v[172:175], v[220:223], a[252:255], v[196:199]\n"
"s_add_u32 s40, s41, s52\n"
"ds_read_b64_tr_b16 v[104:105], v229 offset:8192\n"
"ds_read_b64_tr_b16 v[116:117], v233 offset:16384\n"
"v_mfma_f32_16x16x32_bf16 v[172:175], v[216:219], a[248:251], v[172:175]\n"
"buffer_load_dwordx4 v240, s[28:31], s40 offen offset:1024 lds\n"
"v_mfma_f32_16x16x32_bf16 v[172:175], v[212:215], a[244:247], v[172:175]\n"
"s_waitcnt lgkmcnt(8)\n"
"s_add_u32 s40, s41, s53\n"
"ds_read_b64_tr_b16 v[112:113], v229 offset:16384\n"
"ds_read_b64_tr_b16 v[122:123], v229 offset:26624\n"
"v_mfma_f32_16x16x32_bf16 v[172:175], v[208:211], a[240:243], v[172:175]\n"
"buffer_load_dwordx4 v240, s[28:31], s40 offen offset:2048 lds\n"
"v_mfma_f32_16x16x32_bf16 v[172:175], v[192:195], a[204:207], v[172:175]\n"
"v_min_f32 v254, v252, v250\n"
"v_sub_f32 v250, v254, v250\n"
"v_mfma_f32_16x16x32_bf16 v[172:175], v[188:191], a[200:203], v[172:175]\n"
"s_waitcnt lgkmcnt(6)\n"
"v_sub_f32 v252, v254, v252\n"
"v_mul_f32_dpp v86, v206, v86 row_newbcast:2 row_mask:0xf bank_mask:0xf\n"
"v_mfma_f32_16x16x32_bf16 v[172:175], v[184:187], a[196:199], v[172:175]\n"
"s_add_u32 s40, s41, s54\n"
"ds_read_b64_tr_b16 v[192:193], v234 offset:8192\n"
"v_mul_f32_dpp v49, v247, v49 row_newbcast:1 row_mask:0xf bank_mask:0xf\n"
"v_mfma_f32_16x16x32_bf16 v[172:175], v[180:183], a[192:195], v[172:175]\n"
"buffer_load_dwordx4 v240, s[28:31], s40 offen offset:3072 lds\n"
"v_mfma_f32_16x16x32_bf16 v[128:131], v[96:99], a[96:99], v[196:199]\n"
"s_waitcnt lgkmcnt(3)\n"
"v_xor_b32 v248, s75, v248\n"
"v_mul_f32_dpp v23, v246, v23 row_newbcast:3 row_mask:0xf bank_mask:0xf\n"
"v_mfma_f32_16x16x32_bf16 v[128:131], v[100:103], a[100:103], v[128:131]\n"
"v_exp_f32 v252, v252\n"
"v_mfma_f32_16x16x32_bf16 v[128:131], v[104:107], a[104:107], v[128:131]\n"
"ds_read_b64_tr_b16 v[208:209], v230 offset:16384\n"
"v_mul_f32 v253, v253, v252\n"
"v_mfma_f32_16x16x32_bf16 v[128:131], v[108:111], a[108:111], v[128:131]\n"
"s_waitcnt lgkmcnt(2)\n"
"ds_read_b64_tr_b16 a[238:239], v237 offset:10248\n"
"ds_read_b64_tr_b16 v[210:211], v230 offset:18432\n"
"v_mfma_f32_16x16x32_bf16 v[128:131], v[112:115], a[144:147], v[128:131]\n"
"s_add_u32 s21, s60, 32\n"
"v_exp_f32 v250, v250\n"
"v_mfma_f32_16x16x32_bf16 v[128:131], v[116:119], a[148:151], v[128:131]\n"
"s_mov_b32 s69, 1\n"
"ds_read_b64_tr_b16 v[180:181], v230 offset:0\n"
"v_mul_f32_dpp v16, v246, v16 row_newbcast:0 row_mask:0xf bank_mask:0xf\n"
"v_mfma_f32_16x16x32_bf16 v[128:131], v[120:123], a[152:155], v[128:131]\n"
"v_bfe_u32 v201, v242, 2, 7\n"
"v_add_u32 v201, 0x100, v201\n"
"ds_read_b64_tr_b16 v[220:221], v234 offset:24576\n"
"v_mfma_f32_16x16x32_bf16 v[128:131], v[224:227], a[156:159], v[128:131]\n"
"global_load_dword v200, v201, s[6:7] sc1\n"
"v_mfma_f32_16x16x32_bf16 v[144:147], v[224:227], a[172:175], v[196:199]\n"
"v_accvgpr_read_b32 v254, a208\n"
"v_accvgpr_read_b32 v255, a209\n"
"v_mfma_f32_16x16x32_bf16 v[144:147], v[120:123], a[168:171], v[144:147]\n"
"v_mul_f32_dpp v48, v247, v48 row_newbcast:0 row_mask:0xf bank_mask:0xf\n"
"v_mul_f32_dpp v80, v206, v80 row_newbcast:0 row_mask:0xf bank_mask:0xf\n"
"v_mfma_f32_16x16x32_bf16 v[144:147], v[116:119], a[164:167], v[144:147]\n"
"ds_read_b64_tr_b16 v[188:189], v230 offset:8192\n"
"v_fmac_f32 v253, v251, v250\n"
"v_mfma_f32_16x16x32_bf16 v[144:147], v[112:115], a[160:163], v[144:147]\n"
"v_mul_f32_dpp v19, v246, v19 row_newbcast:3 row_mask:0xf bank_mask:0xf\n"
"v_mul_f32_dpp v21, v246, v21 row_newbcast:1 row_mask:0xf bank_mask:0xf\n"
"v_mfma_f32_16x16x32_bf16 v[144:147], v[108:111], a[124:127], v[144:147]\n"
"v_rcp_f32 v253, v253\n"
"v_mfma_f32_16x16x32_bf16 v[144:147], v[104:107], a[120:123], v[144:147]\n"
"ds_read_b64_tr_b16 v[218:219], v230 offset:26624\n"
"v_mul_f32 v250, v250, v253\n"
"v_mfma_f32_16x16x32_bf16 v[144:147], v[100:103], a[116:119], v[144:147]\n"
"s_nop 0\n"
"v_mov_b32_dpp v250, v250 row_ror:12 row_mask:0x2 bank_mask:0xf\n"
"v_mul_f32_dpp v52, v247, v52 row_newbcast:0 row_mask:0xf bank_mask:0xf\n"
"v_mfma_f32_16x16x32_bf16 v[144:147], v[96:99], a[112:115], v[144:147]\n"
"ds_read_b64_tr_b16 a[216:217], v236 offset:8192\n"
"ds_read_b64_tr_b16 v[182:183], v230 offset:2048\n"
"v_mul_f32 v252, v252, v253\n"
"v_mov_b32_dpp v250, v250 row_ror:8 row_mask:0x4 bank_mask:0xf\n"
"v_accvgpr_read_b32 v251, a211\n"
"v_mul_f32_dpp v17, v246, v17 row_newbcast:1 row_mask:0xf bank_mask:0xf\n"
"v_mul_f32_dpp v55, v247, v55 row_newbcast:3 row_mask:0xf bank_mask:0xf\n"
"v_mfma_f32_16x16x32_bf16 v[160:163], v[96:99], a[128:131], v[196:199]\n"
"v_mov_b32_dpp v250, v250 row_ror:4 row_mask:0x8 bank_mask:0xf\n"
"v_mul_f32_dpp v81, v206, v81 row_newbcast:1 row_mask:0xf bank_mask:0xf\n"
"v_mfma_f32_16x16x32_bf16 v[160:163], v[100:103], a[132:135], v[160:163]\n"
"v_xor_b32 v235, 0x1000, v231\n"
"v_mul_f32_dpp v57, v250, v57 row_newbcast:1 row_mask:0xf bank_mask:0xf\n"
"v_mfma_f32_16x16x32_bf16 v[160:163], v[104:107], a[136:139], v[160:163]\n"
"v_mul_f32_dpp v31, v250, v31 row_newbcast:3 row_mask:0xf bank_mask:0xf\n"
"v_mul_f32_dpp v58, v250, v58 row_newbcast:2 row_mask:0xf bank_mask:0xf\n"
"v_mfma_f32_16x16x32_bf16 v[160:163], v[108:111], a[140:143], v[160:163]\n"
"ds_read_b64_tr_b16 v[194:195], v234 offset:10240\n"
"v_mov_b32_dpp v252, v252 row_ror:12 row_mask:0x2 bank_mask:0xf\n"
"v_mfma_f32_16x16x32_bf16 v[160:163], v[112:115], a[176:179], v[160:163]\n"
"s_nop 0\n"
"v_mov_b32_dpp v252, v252 row_ror:8 row_mask:0x4 bank_mask:0xf\n"
"v_mul_f32_dpp v18, v246, v18 row_newbcast:2 row_mask:0xf bank_mask:0xf\n"
"v_mfma_f32_16x16x32_bf16 v[160:163], v[116:119], a[180:183], v[160:163]\n"
"ds_read_b64_tr_b16 a[218:219], v236 offset:8200\n"
"ds_read_b64_tr_b16 v[184:185], v234 offset:0\n"
"v_mfma_f32_16x16x32_bf16 v[160:163], v[120:123], a[184:187], v[160:163]\n"
"v_mul_f32_dpp v27, v250, v27 row_newbcast:3 row_mask:0xf bank_mask:0xf\n"
"v_cvt_pk_bf16_f32 v16, v16, v17\n"
"v_mfma_f32_16x16x32_bf16 v[160:163], v[224:227], a[188:191], v[160:163]\n"
"ds_read_b64_tr_b16 v[186:187], v234 offset:2048\n"
"ds_read_b64_tr_b16 v[190:191], v230 offset:10240\n"
"v_mov_b32_dpp v252, v252 row_ror:4 row_mask:0x8 bank_mask:0xf\n"
"v_mul_f32_dpp v26, v250, v26 row_newbcast:2 row_mask:0xf bank_mask:0xf\n"
"v_mul_f32_dpp v28, v250, v28 row_newbcast:0 row_mask:0xf bank_mask:0xf\n"
"v_mul_f32_dpp v59, v250, v59 row_newbcast:3 row_mask:0xf bank_mask:0xf\n"
"v_mul_f32_dpp v63, v250, v63 row_newbcast:3 row_mask:0xf bank_mask:0xf\n"
"v_mfma_f32_16x16x32_bf16 v[176:179], v[224:227], a[252:255], v[196:199]\n"
"ds_read_b64_tr_b16 v[212:213], v234 offset:16384\n"
"v_mul_f32_dpp v30, v250, v30 row_newbcast:2 row_mask:0xf bank_mask:0xf\n"
"v_mfma_f32_16x16x32_bf16 v[176:179], v[120:123], a[248:251], v[176:179]\n"
"v_mul_f32_dpp v24, v250, v24 row_newbcast:0 row_mask:0xf bank_mask:0xf\n"
"v_fmac_f32_dpp v27, v252, v251 row_newbcast:3 row_mask:0xf bank_mask:0xf\n"
"v_mfma_f32_16x16x32_bf16 v[176:179], v[116:119], a[244:247], v[176:179]\n"
"ds_read_b64_tr_b16 v[216:217], v230 offset:24576\n"
"v_accvgpr_read_b32 v253, a210\n"
"v_mfma_f32_16x16x32_bf16 v[176:179], v[112:115], a[240:243], v[176:179]\n"
"ds_read_b64_tr_b16 v[222:223], v234 offset:26624\n"
"v_mul_f32_dpp v25, v250, v25 row_newbcast:1 row_mask:0xf bank_mask:0xf\n"
"v_mfma_f32_16x16x32_bf16 v[176:179], v[108:111], a[204:207], v[176:179]\n"
"ds_read_b64_tr_b16 a[236:237], v237 offset:10240\n"
"v_mul_f32_dpp v29, v250, v29 row_newbcast:1 row_mask:0xf bank_mask:0xf\n"
"v_mfma_f32_16x16x32_bf16 v[176:179], v[104:107], a[200:203], v[176:179]\n"
"ds_read_b64_tr_b16 v[214:215], v234 offset:18432\n"
"v_fmac_f32_dpp v25, v252, v255 row_newbcast:1 row_mask:0xf bank_mask:0xf\n"
"v_mfma_f32_16x16x32_bf16 v[176:179], v[100:103], a[196:199], v[176:179]\n"
"v_accvgpr_read_b32 v255, a213\n"
"v_accvgpr_read_b32 v251, a215\n"
"v_mfma_f32_16x16x32_bf16 v[176:179], v[96:99], a[192:195], v[176:179]\n"
"s_waitcnt lgkmcnt(5)\n"
"ds_read_b64_tr_b16 v[112:113], v231 offset:16384\n"
"ds_read_b64_tr_b16 v[122:123], v231 offset:26624\n"
"v_fmac_f32_dpp v24, v252, v254 row_newbcast:0 row_mask:0xf bank_mask:0xf\n"
"v_fmac_f32_dpp v26, v252, v253 row_newbcast:2 row_mask:0xf bank_mask:0xf\n"
"v_accvgpr_read_b32 v254, a212\n"
"v_accvgpr_read_b32 v253, a214\n"
"v_fmac_f32_dpp v31, v252, v251 row_newbcast:3 row_mask:0xf bank_mask:0xf\n"
"v_accvgpr_read_b32 v251, a227\n"
"v_mfma_f32_16x16x32_bf16 v[132:135], v[180:183], a[96:99], v[196:199]\n"
"ds_read_b64_tr_b16 a[222:223], v237 offset:8200\n"
"v_cvt_pk_bf16_f32 v24, v24, v25\n"
"v_mfma_f32_16x16x32_bf16 v[132:135], v[184:187], a[100:103], v[132:135]\n"
"ds_read_b64_tr_b16 v[120:121], v231 offset:24576\n"
"v_mul_f32_dpp v56, v250, v56 row_newbcast:0 row_mask:0xf bank_mask:0xf\n"
"v_mfma_f32_16x16x32_bf16 v[132:135], v[188:191], a[104:107], v[132:135]\n"
"v_fmac_f32_dpp v28, v252, v254 row_newbcast:0 row_mask:0xf bank_mask:0xf\n"
"v_mul_f32_dpp v60, v250, v60 row_newbcast:0 row_mask:0xf bank_mask:0xf\n"
"v_mfma_f32_16x16x32_bf16 v[132:135], v[192:195], a[108:111], v[132:135]\n"
"v_fmac_f32_dpp v29, v252, v255 row_newbcast:1 row_mask:0xf bank_mask:0xf\n"
"v_accvgpr_read_b32 v255, a225\n"
"v_mfma_f32_16x16x32_bf16 v[132:135], v[208:211], a[144:147], v[132:135]\n"
"s_waitcnt lgkmcnt(4)\n"
"s_waitcnt vmcnt(0)\n"
"v_fmac_f32_dpp v30, v252, v253 row_newbcast:2 row_mask:0xf bank_mask:0xf\n"
"v_accvgpr_read_b32 v254, a224\n"
"v_mfma_f32_16x16x32_bf16 v[132:135], v[212:215], a[148:151], v[132:135]\n"
"v_subrev_u32 v200, s21, v200\n"
"ds_read_b64_tr_b16 v[226:227], v235 offset:26624\n"
"v_mfma_f32_16x16x32_bf16 v[132:135], v[216:219], a[152:155], v[132:135]\n"
"v_cmp_lt_u32 vcc, s69, v200\n"
"v_fmac_f32_dpp v59, v252, v251 row_newbcast:3 row_mask:0xf bank_mask:0xf\n"
"v_mfma_f32_16x16x32_bf16 v[132:135], v[220:223], a[156:159], v[132:135]\n"
"ds_read_b64_tr_b16 v[102:103], v235 offset:2048\n"
"ds_read_b64_tr_b16 v[110:111], v235 offset:10240\n"
"v_cvt_pk_bf16_f32 v17, v18, v19\n"
"v_accvgpr_read_b32 v253, a226\n"
"v_fmac_f32_dpp v56, v252, v254 row_newbcast:0 row_mask:0xf bank_mask:0xf\n"
"v_fmac_f32_dpp v57, v252, v255 row_newbcast:1 row_mask:0xf bank_mask:0xf\n"
"v_accvgpr_read_b32 v254, a228\n"
"v_accvgpr_read_b32 v255, a229\n"
"v_mul_f32_dpp v61, v250, v61 row_newbcast:1 row_mask:0xf bank_mask:0xf\n"
"s_cbranch_vccnz LGV_%=\nLGVK_%=:\n"
"v_mfma_f32_16x16x32_bf16 v[148:151], v[220:223], a[172:175], v[196:199]\n"
"ds_read_b64_tr_b16 v[100:101], v235 offset:0\n"
"v_cvt_pk_bf16_f32 v25, v26, v27\n"
"v_mfma_f32_16x16x32_bf16 v[148:151], v[216:219], a[168:171], v[148:151]\n"
"v_add_u32_dpp v200, v200, v200 quad_perm:[1,0,3,2] row_mask:0xf bank_mask:0xf\n"
"ds_read_b64_tr_b16 v[104:105], v231 offset:8192\n"
"v_mfma_f32_16x16x32_bf16 v[148:151], v[212:215], a[164:167], v[148:151]\n"
"ds_read_b64_tr_b16 v[98:99], v231 offset:2048\n"
"v_fmac_f32_dpp v58, v252, v253 row_newbcast:2 row_mask:0xf bank_mask:0xf\n"
"v_mfma_f32_16x16x32_bf16 v[148:151], v[208:211], a[160:163], v[148:151]\n"
"v_add_u32_dpp v200, v200, v200 quad_perm:[2,3,0,1] row_mask:0xf bank_mask:0xf\n"
"v_accvgpr_read_b32 v251, a231\n"
"v_mfma_f32_16x16x32_bf16 v[148:151], v[192:195], a[124:127], v[148:151]\n"
"ds_read_b64_tr_b16 v[96:97], v231 offset:0\n"
"ds_read_b64_tr_b16 v[116:117], v235 offset:16384\n"
"v_cvt_pk_bf16_f32 v26, v28, v29\n"
"v_cvt_pk_bf16_f32 v27, v30, v31\n"
"v_accvgpr_read_b32 v253, a230\n"
"v_mul_f32_dpp v62, v250, v62 row_newbcast:2 row_mask:0xf bank_mask:0xf\n"
"v_fmac_f32_dpp v60, v252, v254 row_newbcast:0 row_mask:0xf bank_mask:0xf\n"
"v_cvt_pk_bf16_f32 v56, v56, v57\n"
"v_mfma_f32_16x16x32_bf16 v[148:151], v[188:191], a[120:123], v[148:151]\n"
"v_cmp_lt_u32 vcc, 1, v200\n"
"ds_write_b128 v243, v[24:27] offset:0\n"
"v_fmac_f32_dpp v61, v252, v255 row_newbcast:1 row_mask:0xf bank_mask:0xf\n"
"v_fmac_f32_dpp v62, v252, v253 row_newbcast:2 row_mask:0xf bank_mask:0xf\n"
"v_cvt_pk_bf16_f32 v57, v58, v59\n"
"v_cvt_pk_bf16_f32 v58, v60, v61\n"
"v_mfma_f32_16x16x32_bf16 v[148:151], v[184:187], a[116:119], v[148:151]\n"
"v_fmac_f32_dpp v63, v252, v251 row_newbcast:3 row_mask:0xf bank_mask:0xf\n"
"v_cvt_pk_bf16_f32 v59, v62, v63\n"
"v_mfma_f32_16x16x32_bf16 v[148:151], v[180:183], a[112:115], v[148:151]\n"
"ds_write_b128 v243, v[56:59] offset:2048\n"
"v_mul_f32_dpp v50, v247, v50 row_newbcast:2 row_mask:0xf bank_mask:0xf\n"
"v_cvt_pk_bf16_f32 v48, v48, v49\n"
"v_mfma_f32_16x16x32_bf16 v[164:167], v[180:183], a[128:131], v[196:199]\n"
"s_mov_b32 s47, 0\n"
"v_xor_b32 v249, s75, v249\n"
"v_mul_f32_dpp v20, v246, v20 row_newbcast:0 row_mask:0xf bank_mask:0xf\n"
"v_mfma_f32_16x16x32_bf16 v[164:167], v[184:187], a[132:135], v[164:167]\n"
"s_mov_b32 s89, vcc_lo\n"
"ds_read_b64_tr_b16 v[106:107], v231 offset:10240\n"
"ds_read_b64_tr_b16 v[224:225], v235 offset:24576\n"
"v_mfma_f32_16x16x32_bf16 v[164:167], v[188:191], a[136:139], v[164:167]\n"
"s_lshr_b32 s90, s89, s93\n"
"ds_read_b64_tr_b16 v[114:115], v231 offset:18432\n"
"v_cvt_pk_bf16_f32 v19, v22, v23\n"
"v_mfma_f32_16x16x32_bf16 v[164:167], v[192:195], a[140:143], v[164:167]\n"
"s_and_b32 s90, s90, 1\n"
"ds_read_b64_tr_b16 v[108:109], v235 offset:8192\n"
"v_mul_f32_dpp v87, v206, v87 row_newbcast:3 row_mask:0xf bank_mask:0xf\n"
"v_mfma_f32_16x16x32_bf16 v[164:167], v[208:211], a[176:179], v[164:167]\n"
"s_lshl_b32 s16, s61, 7\n"
"v_cvt_pk_bf16_f32 v18, v20, v21\n"
"v_mul_f32_dpp v51, v247, v51 row_newbcast:3 row_mask:0xf bank_mask:0xf\n"
"v_mfma_f32_16x16x32_bf16 v[164:167], v[212:215], a[180:183], v[164:167]\n"
"s_add_u32 s90, s90, s16\n"
"ds_read_b64_tr_b16 v[118:119], v235 offset:18432\n"
"v_mul_f32_dpp v85, v206, v85 row_newbcast:1 row_mask:0xf bank_mask:0xf\n"
"v_mfma_f32_16x16x32_bf16 v[164:167], v[216:219], a[184:187], v[164:167]\n"
"s_waitcnt vmcnt(63) lgkmcnt(5)\n"
"s_lshl_b32 s90, s90, 13\n"
"v_cvt_pk_bf16_f32 v80, v80, v81\n"
"v_mfma_f32_16x16x32_bf16 v[164:167], v[220:223], a[188:191], v[164:167]\n"
"s_barrier\n"
"s_lshl_b32 s16, s95, 9\n"
"ds_write_b128 v248, v[16:19] offset:49152\n"
"v_mfma_f32_16x16x32_bf16 v[136:139], v[96:99], a[96:99], v[196:199]\n"
"s_add_u32 s16, s16, s90\n"
"v_mul_f32_dpp v53, v247, v53 row_newbcast:1 row_mask:0xf bank_mask:0xf\n"
"v_cvt_pk_bf16_f32 v49, v50, v51\n"
"v_mfma_f32_16x16x32_bf16 v[136:139], v[100:103], a[100:103], v[136:139]\n"
"s_waitcnt lgkmcnt(4)\n"
"s_add_u32 s17, s16, 4096\n"
"ds_read_b64_tr_b16 a[228:229], v237 offset:14336\n"
"v_cvt_pk_bf16_f32 v50, v52, v53\n"
"v_mfma_f32_16x16x32_bf16 v[136:139], v[104:107], a[104:107], v[136:139]\n"
"s_waitcnt lgkmcnt(3)\n"
"s_mov_b32 s72, s61\n"
"ds_read_b64_tr_b16 a[10:11], v249 offset:49160\n"
"v_mov_b64 v[90:91], 0\n"
"v_mfma_f32_16x16x32_bf16 v[136:139], v[108:111], a[108:111], v[136:139]\n"
"s_mov_b32 s21, s60\n"
"ds_read_b64_tr_b16 v[220:221], v232 offset:57344\n"
"v_mul_f32_dpp v84, v206, v84 row_newbcast:0 row_mask:0xf bank_mask:0xf\n"
"v_mfma_f32_16x16x32_bf16 v[136:139], v[112:115], a[144:147], v[136:139]\n"
"s_waitcnt lgkmcnt(4)\n"
"s_mov_b32 s18, s77\n"
"ds_read_b64_tr_b16 a[8:9], v249 offset:49152\n"
"v_mul_f32_dpp v83, v206, v83 row_newbcast:3 row_mask:0xf bank_mask:0xf\n"
"v_mfma_f32_16x16x32_bf16 v[136:139], v[116:119], a[148:151], v[136:139]\n"
"s_mov_b32 s19, s77\n"
"ds_read_b64_tr_b16 v[180:181], v228 offset:32768\n"
"v_cvt_pk_bf16_f32 v51, v54, v55\n"
"v_mfma_f32_16x16x32_bf16 v[136:139], v[120:123], a[152:155], v[136:139]\n"
"s_mov_b32 s69, -1\n"
"ds_read_b64_tr_b16 v[182:183], v228 offset:34816\n"
"v_mul_f32_dpp v82, v206, v82 row_newbcast:2 row_mask:0xf bank_mask:0xf\n"
"v_mfma_f32_16x16x32_bf16 v[136:139], v[224:227], a[156:159], v[136:139]\n"
"s_mul_i32 s88, s61, 0x10011111\n"
"ds_read_b64_tr_b16 v[212:213], v232 offset:49152\n"
"v_cvt_pk_bf16_f32 v81, v82, v83\n"
"v_mfma_f32_16x16x32_bf16 v[152:155], v[224:227], a[172:175], v[196:199]\n"
"s_add_u32 s88, s88, 484675\n"
"ds_read_b64_tr_b16 v[186:187], v232 offset:34816\n"
"v_cvt_pk_bf16_f32 v83, v86, v87\n"
"v_mfma_f32_16x16x32_bf16 v[152:155], v[120:123], a[168:171], v[152:155]\n"
"s_and_b32 s88, s88, 0x70077777\n"
"ds_read_b64_tr_b16 v[216:217], v228 offset:57344\n"
"v_cvt_pk_bf16_f32 v82, v84, v85\n"
"v_mfma_f32_16x16x32_bf16 v[152:155], v[116:119], a[164:167], v[152:155]\n"
"s_or_b32 s88, s88, 0x9800000\n"
"ds_write_b128 v248, v[48:51] offset:50176\n"
"ds_write_b128 v248, v[80:83] offset:51200\n"
"v_mfma_f32_16x16x32_bf16 v[152:155], v[112:115], a[160:163], v[152:155]\n"
"s_mov_b32 s84, s6\n"
"ds_read_b64_tr_b16 a[24:25], v249 offset:50176\n"
"ds_read_b64_tr_b16 a[26:27], v249 offset:50184\n"
"ds_read_b64_tr_b16 a[40:41], v249 offset:51200\n"
"ds_read_b64_tr_b16 a[42:43], v249 offset:51208\n"
"v_mfma_f32_16x16x32_bf16 v[152:155], v[108:111], a[124:127], v[152:155]\n"
"s_and_b32 s85, s7, 0xffff\n"
"ds_read_b64_tr_b16 v[188:189], v228 offset:40960\n"
"v_bfe_u32 v254, v242, 4, 4\n"
"v_mfma_f32_16x16x32_bf16 v[152:155], v[104:107], a[120:123], v[152:155]\n"
"s_mov_b32 s86, 128\n"
"ds_read_b64_tr_b16 v[184:185], v232 offset:32768\n"
"ds_read_b64_tr_b16 v[222:223], v232 offset:59392\n"
"v_mfma_f32_16x16x32_bf16 v[152:155], v[100:103], a[116:119], v[152:155]\n"
"s_mov_b32 s87, 131072\n"
"ds_read_b64_tr_b16 a[224:225], v236 offset:14336\n"
"v_mfma_f32_16x16x32_bf16 v[152:155], v[96:99], a[112:115], v[152:155]\n"
"ds_read_b64_tr_b16 v[192:193], v232 offset:40960\n"
"ds_read_b64_tr_b16 v[214:215], v232 offset:51200\n"
"v_mfma_f32_16x16x32_bf16 v[168:171], v[96:99], a[128:131], v[196:199]\n"
"ds_read_b64_tr_b16 v[190:191], v228 offset:43008\n"
"ds_read_b64_tr_b16 v[218:219], v228 offset:59392\n"
"v_mfma_f32_16x16x32_bf16 v[168:171], v[100:103], a[132:135], v[168:171]\n"
"ds_read_b64_tr_b16 v[194:195], v232 offset:43008\n"
"ds_read_b64_tr_b16 v[210:211], v228 offset:51200\n"
"v_mfma_f32_16x16x32_bf16 v[168:171], v[104:107], a[136:139], v[168:171]\n"
"ds_read_b64_tr_b16 a[234:235], v236 offset:10248\n"
"ds_read_b64_tr_b16 v[208:209], v228 offset:49152\n"
"v_mfma_f32_16x16x32_bf16 v[168:171], v[108:111], a[140:143], v[168:171]\n"
"s_waitcnt lgkmcnt(12)\n"
"ds_read_b64_tr_b16 a[212:213], v237 offset:12288\n"
"ds_read_b64_tr_b16 a[230:231], v237 offset:14344\n"
"v_mfma_f32_16x16x32_bf16 v[168:171], v[112:115], a[176:179], v[168:171]\n"
"ds_read_b64_tr_b16 a[232:233], v236 offset:10240\n"
"ds_read_b64_tr_b16 a[214:215], v237 offset:12296\n"
"v_mfma_f32_16x16x32_bf16 v[168:171], v[116:119], a[180:183], v[168:171]\n"
"ds_read_b64_tr_b16 v[96:97], v229 offset:32768\n"
"ds_read_b64_tr_b16 v[100:101], v233 offset:32768\n"
"v_mfma_f32_16x16x32_bf16 v[168:171], v[120:123], a[184:187], v[168:171]\n"
"s_add_u32 s41, s70, 327680\n"
"s_add_u32 m0, s71, 32768\n"
"ds_read_b64_tr_b16 v[104:105], v229 offset:40960\n"
"v_mfma_f32_16x16x32_bf16 v[168:171], v[224:227], a[188:191], v[168:171]\n"
"buffer_load_dwordx4 v240, s[28:31], s41 offen lds\n"
"v_mfma_f32_16x16x32_bf16 v[124:127], v[180:183], a[48:51], v[124:127]\n"
"s_waitcnt lgkmcnt(11)\n"
"v_and_b32 v243, 0x3f0, v242\n"
"v_mov_b64 v[246:247], 0\n"
"v_mfma_f32_16x16x32_bf16 v[124:127], v[184:187], a[52:55], v[124:127]\n"
"v_bfe_u32 v255, v242, 8, 1\n"
"v_mfma_f32_16x16x32_bf16 v[124:127], v[188:191], a[56:59], v[124:127]\n"
"s_waitcnt lgkmcnt(7)\n"
"s_add_u32 s40, s41, s52\n"
"ds_read_b64_tr_b16 a[210:211], v236 offset:12296\n"
"ds_read_b64_tr_b16 v[98:99], v229 offset:34816\n"
"v_mfma_f32_16x16x32_bf16 v[124:127], v[192:195], a[60:63], v[124:127]\n"
"buffer_load_dwordx4 v240, s[28:31], s40 offen offset:1024 lds\n"
"v_mfma_f32_16x16x32_bf16 v[124:127], v[208:211], a[0:3], v[124:127]\n"
"v_xor_b32 v248, s75, v248\n"
"v_mov_b64 v[52:53], s[22:23]\n"
"v_mfma_f32_16x16x32_bf16 v[124:127], v[212:215], a[4:7], v[124:127]\n"
"v_mov_b64 v[6:7], s[22:23]\n"
"v_mfma_f32_16x16x32_bf16 v[124:127], v[216:219], a[8:11], v[124:127]\n"
"s_add_u32 s40, s41, s53\n"
"ds_read_b64_tr_b16 v[102:103], v233 offset:34816\n"
"ds_read_b64_tr_b16 v[110:111], v233 offset:43008\n"
"v_mfma_f32_16x16x32_bf16 v[124:127], v[220:223], a[12:15], v[124:127]\n"
"buffer_load_dwordx4 v240, s[28:31], s40 offen offset:2048 lds\n"
"v_mfma_f32_16x16x32_bf16 v[140:143], v[220:223], a[28:31], v[140:143]\n"
"v_mov_b64 v[2:3], s[22:23]\n"
"v_mov_b64 v[10:11], s[22:23]\n"
"v_mfma_f32_16x16x32_bf16 v[140:143], v[216:219], a[24:27], v[140:143]\n"
"v_mfma_f32_16x16x32_bf16 v[140:143], v[212:215], a[20:23], v[140:143]\n"
"ds_read_b64_tr_b16 a[220:221], v237 offset:8192\n"
"ds_read_b64_tr_b16 v[116:117], v233 offset:49152\n"
"v_mfma_f32_16x16x32_bf16 v[140:143], v[208:211], a[16:19], v[140:143]\n"
"ds_read_b64_tr_b16 a[208:209], v236 offset:12288\n"
"v_mul_u32_u24 v254, 2560, v254\n"
"v_mfma_f32_16x16x32_bf16 v[140:143], v[192:195], a[76:79], v[140:143]\n"
"ds_read_b64_tr_b16 v[122:123], v229 offset:59392\n"
"v_subrev_f32 v124, s22, v124\n"
"v_mfma_f32_16x16x32_bf16 v[140:143], v[188:191], a[72:75], v[140:143]\n"
"ds_read_b64_tr_b16 a[226:227], v236 offset:14344\n"
"v_mov_b64 v[18:19], s[22:23]\n"
"v_mfma_f32_16x16x32_bf16 v[140:143], v[184:187], a[68:71], v[140:143]\n"
"s_add_u32 s40, s41, s54\n"
"ds_read_b64_tr_b16 v[112:113], v229 offset:49152\n"
"ds_read_b64_tr_b16 v[120:121], v229 offset:57344\n"
"v_mfma_f32_16x16x32_bf16 v[140:143], v[180:183], a[64:67], v[140:143]\n"
"buffer_load_dwordx4 v240, s[28:31], s40 offen offset:3072 lds\n"
"v_mfma_f32_16x16x32_bf16 v[156:159], v[180:183], a[80:83], v[156:159]\n"
"s_add_u32 s41, s70, 491520\n"
"s_add_u32 m0, s71, 49152\n"
"v_lshl_add_u32 v254, v255, 5, v254\n"
"v_mfma_f32_16x16x32_bf16 v[156:159], v[184:187], a[84:87], v[156:159]\n"
"buffer_load_dwordx4 v240, s[28:31], s41 offen lds\n"
"v_mfma_f32_16x16x32_bf16 v[156:159], v[188:191], a[88:91], v[156:159]\n"
"v_subrev_f32 v125, s22, v125\n"
"v_cvt_pk_bf16_f32 v124, v124, v125\n"
"v_mfma_f32_16x16x32_bf16 v[156:159], v[192:195], a[92:95], v[156:159]\n"
"v_bfe_u32 v255, v242, 9, 1\n"
"v_mfma_f32_16x16x32_bf16 v[156:159], v[208:211], a[32:35], v[156:159]\n"
"ds_read_b64_tr_b16 v[106:107], v229 offset:43008\n"
"v_lshl_add_u32 v254, v255, 4, v254\n"
"v_mfma_f32_16x16x32_bf16 v[156:159], v[212:215], a[36:39], v[156:159]\n"
"ds_read_b64_tr_b16 v[114:115], v229 offset:51200\n"
"v_subrev_f32 v142, s22, v142\n"
"v_mfma_f32_16x16x32_bf16 v[156:159], v[216:219], a[40:43], v[156:159]\n"
"s_add_u32 s40, s41, s52\n"
"ds_read_b64_tr_b16 v[108:109], v233 offset:40960\n"
"ds_read_b64_tr_b16 v[118:119], v233 offset:51200\n"
"v_mfma_f32_16x16x32_bf16 v[156:159], v[220:223], a[44:47], v[156:159]\n"
"s_waitcnt lgkmcnt(4)\n"
"buffer_load_dwordx4 v240, s[28:31], s40 offen offset:1024 lds\n"
"v_mfma_f32_16x16x32_bf16 v[172:175], v[220:223], a[228:231], v[172:175]\n"
"s_add_u32 s40, s41, s53\n"
"v_mov_b64 v[32:33], s[22:23]\n"
"v_mov_b64 v[34:35], s[22:23]\n"
"v_mfma_f32_16x16x32_bf16 v[172:175], v[216:219], a[224:227], v[172:175]\n"
"buffer_load_dwordx4 v240, s[28:31], s40 offen offset:2048 lds\n"
"v_mfma_f32_16x16x32_bf16 v[172:175], v[212:215], a[212:215], v[172:175]\n"
"v_mov_b64 v[88:89], 0\n"
"v_subrev_f32 v141, s22, v141\n"
"v_mfma_f32_16x16x32_bf16 v[172:175], v[208:211], a[208:211], v[172:175]\n"
"v_add_u32 v236, s94, v243\n"
"v_mov_b32 v241, s60\n"
"v_mfma_f32_16x16x32_bf16 v[172:175], v[192:195], a[236:239], v[172:175]\n"
"s_add_u32 s40, s41, s54\n"
"ds_read_b64_tr_b16 v[224:225], v233 offset:57344\n"
"ds_read_b64_tr_b16 v[226:227], v233 offset:59392\n"
"v_mfma_f32_16x16x32_bf16 v[172:175], v[188:191], a[232:235], v[172:175]\n"
"buffer_load_dwordx4 v240, s[28:31], s40 offen offset:3072 lds\n"
"v_mfma_f32_16x16x32_bf16 v[172:175], v[184:187], a[220:223], v[172:175]\n"
"s_lshl_b32 s62, s70, 9\n"
"v_xor_b32 v228, s75, v228\n"
"v_mfma_f32_16x16x32_bf16 v[172:175], v[180:183], a[216:219], v[172:175]\n"
"s_and_b32 s70, s88, 15\n"
"v_mov_b64 v[26:27], s[22:23]\n"
"v_mfma_f32_16x16x32_bf16 v[128:131], v[96:99], a[48:51], v[128:131]\n"
"s_lshr_b32 s88, s88, 4\n"
"ds_read_b64_tr_b16 v[186:187], v234 offset:34816\n"
"v_mfma_f32_16x16x32_bf16 v[128:131], v[100:103], a[52:55], v[128:131]\n"
"s_waitcnt lgkmcnt(3)\n"
"s_lshl_b32 s70, s70, 7\n"
"ds_read_b64_tr_b16 v[180:181], v230 offset:32768\n"
"v_subrev_f32 v143, s22, v143\n"
"v_mfma_f32_16x16x32_bf16 v[128:131], v[104:107], a[56:59], v[128:131]\n"
"s_xor_b32 s71, s71, s75\n"
"v_mov_b64 v[86:87], 0\n"
"v_subrev_f32 v127, s22, v127\n"
"v_mfma_f32_16x16x32_bf16 v[128:131], v[108:111], a[60:63], v[128:131]\n"
"v_lshlrev_b32_e64 v201, 2, s72\n"
"ds_read_b64_tr_b16 v[214:215], v234 offset:51200\n"
"v_subrev_f32 v173, s22, v173\n"
"v_mfma_f32_16x16x32_bf16 v[128:131], v[112:115], a[0:3], v[128:131]\n"
"global_load_dword v200, v201, s[6:7] sc1\n"
"v_mfma_f32_16x16x32_bf16 v[128:131], v[116:119], a[4:7], v[128:131]\n"
"v_mov_b64 v[28:29], s[22:23]\n"
"v_subrev_f32 v140, s22, v140\n"
"v_mfma_f32_16x16x32_bf16 v[128:131], v[120:123], a[8:11], v[128:131]\n"
"s_waitcnt lgkmcnt(3)\n"
"v_mov_b64 v[38:39], s[22:23]\n"
"v_add_u32 v241, 1, v241\n"
"v_mfma_f32_16x16x32_bf16 v[128:131], v[224:227], a[12:15], v[128:131]\n"
"ds_read_b64_tr_b16 v[208:209], v230 offset:49152\n"
"v_subrev_f32 v126, s22, v126\n"
"v_mfma_f32_16x16x32_bf16 v[144:147], v[224:227], a[28:31], v[144:147]\n"
"v_mov_b64 v[94:95], 0\n"
"v_cvt_pk_bf16_f32 v140, v140, v141\n"
"v_mfma_f32_16x16x32_bf16 v[144:147], v[120:123], a[24:27], v[144:147]\n"
"ds_read_b64_tr_b16 v[192:193], v234 offset:40960\n"
"v_subrev_f32 v175, s22, v175\n"
"v_mfma_f32_16x16x32_bf16 v[144:147], v[116:119], a[20:23], v[144:147]\n"
"ds_read_b64_tr_b16 v[222:223], v234 offset:59392\n"
"v_subrev_f32 v129, s22, v129\n"
"v_mfma_f32_16x16x32_bf16 v[144:147], v[112:115], a[16:19], v[144:147]\n"
"ds_read_b64_tr_b16 v[188:189], v230 offset:40960\n"
"v_lshl_add_u32 v248, v242, 8, s93\n"
"v_mfma_f32_16x16x32_bf16 v[144:147], v[108:111], a[76:79], v[144:147]\n"
"ds_read_b64_tr_b16 v[182:183], v230 offset:34816\n"
"v_cvt_pk_bf16_f32 v125, v126, v127\n"
"v_mfma_f32_16x16x32_bf16 v[144:147], v[104:107], a[72:75], v[144:147]\n"
"v_mov_b64 v[8:9], s[22:23]\n"
"v_subrev_f32 v130, s22, v130\n"
"v_mfma_f32_16x16x32_bf16 v[144:147], v[100:103], a[68:71], v[144:147]\n"
"ds_read_b64_tr_b16 v[212:213], v234 offset:49152\n"
"v_subrev_f32 v172, s22, v172\n"
"v_mfma_f32_16x16x32_bf16 v[144:147], v[96:99], a[64:67], v[144:147]\n"
"ds_read_b64_tr_b16 v[220:221], v234 offset:57344\n"
"v_mfma_f32_16x16x32_bf16 v[160:163], v[96:99], a[80:83], v[160:163]\n"
"ds_read_b64_tr_b16 v[216:217], v230 offset:57344\n"
"v_add_u32 v238, s97, v254\n"
"v_mfma_f32_16x16x32_bf16 v[160:163], v[100:103], a[84:87], v[160:163]\n"
"v_lshl_add_u32 v243, s44, 2, v243\n"
"v_bfe_u32 v237, v242, 4, 3\n"
"v_mfma_f32_16x16x32_bf16 v[160:163], v[104:107], a[88:91], v[160:163]\n"
"ds_read_b64_tr_b16 v[190:191], v230 offset:43008\n"
"v_subrev_f32 v128, s22, v128\n"
"v_mfma_f32_16x16x32_bf16 v[160:163], v[108:111], a[92:95], v[160:163]\n"
"v_max_u32 v237, 1, v237\n"
"v_subrev_f32 v156, s22, v156\n"
"v_mfma_f32_16x16x32_bf16 v[160:163], v[112:115], a[32:35], v[160:163]\n"
"ds_read_b64_tr_b16 v[184:185], v234 offset:32768\n"
"v_subrev_f32 v159, s22, v159\n"
"v_mfma_f32_16x16x32_bf16 v[160:163], v[116:119], a[36:39], v[160:163]\n"
"v_subrev_f32 v131, s22, v131\n"
"v_cvt_pk_bf16_f32 v126, v128, v129\n"
"v_mfma_f32_16x16x32_bf16 v[160:163], v[120:123], a[40:43], v[160:163]\n"
"ds_read_b64_tr_b16 v[194:195], v234 offset:43008\n"
"v_cvt_pk_bf16_f32 v127, v130, v131\n"
"v_mfma_f32_16x16x32_bf16 v[160:163], v[224:227], a[44:47], v[160:163]\n"
"buffer_store_dwordx4 v[124:127], v243, s[32:35], s16 offen sc0 sc1\n"
"v_subrev_f32 v147, s22, v147\n"
"v_mfma_f32_16x16x32_bf16 v[176:179], v[224:227], a[228:231], v[176:179]\n"
"ds_read_b64_tr_b16 v[218:219], v230 offset:59392\n"
"v_mfma_f32_16x16x32_bf16 v[176:179], v[120:123], a[224:227], v[176:179]\n"
"v_subrev_f32 v157, s22, v157\n"
"v_subrev_f32 v146, s22, v146\n"
"v_mfma_f32_16x16x32_bf16 v[176:179], v[116:119], a[212:215], v[176:179]\n"
"ds_read_b64_tr_b16 v[210:211], v230 offset:51200\n"
"v_mfma_f32_16x16x32_bf16 v[176:179], v[112:115], a[208:211], v[176:179]\n"
"v_cvt_pk_bf16_f32 v141, v142, v143\n"
"v_subrev_f32 v145, s22, v145\n"
"v_mfma_f32_16x16x32_bf16 v[176:179], v[108:111], a[236:239], v[176:179]\n"
"ds_read_b64_tr_b16 v[226:227], v235 offset:59392\n"
"v_subrev_f32 v158, s22, v158\n"
"v_mfma_f32_16x16x32_bf16 v[176:179], v[104:107], a[232:235], v[176:179]\n"
"v_subrev_f32 v174, s22, v174\n"
"v_mfma_f32_16x16x32_bf16 v[176:179], v[100:103], a[220:223], v[176:179]\n"
"ds_read_b64_tr_b16 v[100:101], v235 offset:32768\n"
"v_mov_b64 v[36:37], s[22:23]\n"
"v_mfma_f32_16x16x32_bf16 v[176:179], v[96:99], a[216:219], v[176:179]\n"
"s_waitcnt lgkmcnt(4)\n"
"v_subrev_f32 v144, s22, v144\n"
"v_subrev_f32 v162, s22, v162\n"
"v_mfma_f32_16x16x32_bf16 v[132:135], v[180:183], a[48:51], v[132:135]\n"
"ds_read_b64_tr_b16 v[106:107], v231 offset:43008\n"
"v_subrev_f32 v160, s22, v160\n"
"v_mfma_f32_16x16x32_bf16 v[132:135], v[184:187], a[52:55], v[132:135]\n"
"ds_read_b64_tr_b16 v[96:97], v231 offset:32768\n"
"v_mfma_f32_16x16x32_bf16 v[132:135], v[188:191], a[56:59], v[132:135]\n"
"v_cvt_pk_bf16_f32 v142, v144, v145\n"
"v_cvt_pk_bf16_f32 v143, v146, v147\n"
"v_mfma_f32_16x16x32_bf16 v[132:135], v[192:195], a[60:63], v[132:135]\n"
"s_waitcnt lgkmcnt(4)\n"
"ds_read_b64_tr_b16 v[112:113], v231 offset:49152\n"
"v_mov_b64 v[50:51], s[22:23]\n"
"v_mfma_f32_16x16x32_bf16 v[132:135], v[208:211], a[0:3], v[132:135]\n"
"v_xor_b32 v237, s61, v237\n"
"v_cvt_pk_bf16_f32 v172, v172, v173\n"
"v_mfma_f32_16x16x32_bf16 v[132:135], v[212:215], a[4:7], v[132:135]\n"
"ds_read_b64_tr_b16 v[116:117], v235 offset:49152\n"
"v_xor_b32 v233, s75, v233\n"
"v_mfma_f32_16x16x32_bf16 v[132:135], v[216:219], a[8:11], v[132:135]\n"
"v_mov_b64 v[30:31], s[22:23]\n"
"v_subrev_f32 v179, s22, v179\n"
"v_mfma_f32_16x16x32_bf16 v[132:135], v[220:223], a[12:15], v[132:135]\n"
"buffer_store_dwordx4 v[140:143], v243, s[32:35], s16 offen offset:2048 sc0 sc1\n"
"ds_read_b64_tr_b16 v[120:121], v231 offset:57344\n"
"v_mfma_f32_16x16x32_bf16 v[148:151], v[220:223], a[28:31], v[148:151]\n"
"v_cvt_pk_bf16_f32 v156, v156, v157\n"
"v_subrev_f32 v161, s22, v161\n"
"v_mfma_f32_16x16x32_bf16 v[148:151], v[216:219], a[24:27], v[148:151]\n"
"ds_read_b64_tr_b16 v[110:111], v235 offset:43008\n"
"v_subrev_f32 v178, s22, v178\n"
"v_mfma_f32_16x16x32_bf16 v[148:151], v[212:215], a[20:23], v[148:151]\n"
"ds_read_b64_tr_b16 v[102:103], v235 offset:34816\n"
"v_mov_b64 v[48:49], s[22:23]\n"
"v_mfma_f32_16x16x32_bf16 v[148:151], v[208:211], a[16:19], v[148:151]\n"
"v_add_u32 v253, s99, v254\n"
"v_subrev_f32 v132, s22, v132\n"
"v_mfma_f32_16x16x32_bf16 v[148:151], v[192:195], a[76:79], v[148:151]\n"
"ds_read_b64_tr_b16 v[98:99], v231 offset:34816\n"
"v_cvt_pk_bf16_f32 v157, v158, v159\n"
"v_mfma_f32_16x16x32_bf16 v[148:151], v[188:191], a[72:75], v[148:151]\n"
"ds_read_b64_tr_b16 v[108:109], v235 offset:40960\n"
"v_subrev_f32 v135, s22, v135\n"
"v_mfma_f32_16x16x32_bf16 v[148:151], v[184:187], a[68:71], v[148:151]\n"
"v_mfma_f32_16x16x32_bf16 v[148:151], v[180:183], a[64:67], v[148:151]\n"
"ds_read_b64_tr_b16 v[118:119], v235 offset:51200\n"
"v_mfma_f32_16x16x32_bf16 v[164:167], v[180:183], a[80:83], v[164:167]\n"
"ds_read_b64_tr_b16 v[104:105], v231 offset:40960\n"
"v_subrev_f32 v163, s22, v163\n"
"v_mfma_f32_16x16x32_bf16 v[164:167], v[184:187], a[84:87], v[164:167]\n"
"ds_read_b64_tr_b16 v[114:115], v231 offset:51200\n"
"v_mov_b64 v[84:85], 0\n"
"v_mfma_f32_16x16x32_bf16 v[164:167], v[188:191], a[88:91], v[164:167]\n"
"v_cvt_pk_bf16_f32 v158, v160, v161\n"
"v_subrev_f32 v133, s22, v133\n"
"v_mfma_f32_16x16x32_bf16 v[164:167], v[192:195], a[92:95], v[164:167]\n"
"ds_read_b64_tr_b16 v[122:123], v231 offset:59392\n"
"v_mov_b64 v[14:15], s[22:23]\n"
"v_mfma_f32_16x16x32_bf16 v[164:167], v[208:211], a[32:35], v[164:167]\n"
"v_mov_b64 v[24:25], s[22:23]\n"
"v_xor_b32 v232, s75, v232\n"
"v_mfma_f32_16x16x32_bf16 v[164:167], v[212:215], a[36:39], v[164:167]\n"
"ds_read_b64_tr_b16 v[224:225], v235 offset:57344\n"
"v_cvt_pk_bf16_f32 v159, v162, v163\n"
"v_mfma_f32_16x16x32_bf16 v[164:167], v[216:219], a[40:43], v[164:167]\n"
"v_cvt_pk_bf16_f32 v173, v174, v175\n"
"v_mfma_f32_16x16x32_bf16 v[164:167], v[220:223], a[44:47], v[164:167]\n"
"s_barrier\n"
"s_waitcnt lgkmcnt(4)\n"
"v_mfma_f32_16x16x32_bf16 v[136:139], v[96:99], a[48:51], v[136:139]\n"
"ds_read_b64_tr_b16 v[214:215], v232 offset:18432\n"
"v_cvt_pk_bf16_f32 v175, v178, v179\n"
"v_mfma_f32_16x16x32_bf16 v[136:139], v[100:103], a[52:55], v[136:139]\n"
"s_waitcnt lgkmcnt(3)\n"
"ds_read_b64_tr_b16 v[218:219], v228 offset:26624\n"
"v_mov_b64 v[0:1], s[22:23]\n"
"v_mfma_f32_16x16x32_bf16 v[136:139], v[104:107], a[56:59], v[136:139]\n"
"ds_read_b64_tr_b16 v[222:223], v232 offset:26624\n"
"v_mov_b64 v[46:47], s[22:23]\n"
"v_mfma_f32_16x16x32_bf16 v[136:139], v[108:111], a[60:63], v[136:139]\n"
"ds_read_b64_tr_b16 v[216:217], v228 offset:24576\n"
"v_mov_b64 v[44:45], s[22:23]\n"
"v_mfma_f32_16x16x32_bf16 v[136:139], v[112:115], a[0:3], v[136:139]\n"
"ds_read_b64_tr_b16 v[212:213], v232 offset:16384\n"
"v_lshlrev_b32 v237, 2, v237\n"
"v_mfma_f32_16x16x32_bf16 v[136:139], v[116:119], a[4:7], v[136:139]\n"
"s_waitcnt lgkmcnt(5)\n"
"ds_read_b64_tr_b16 v[180:181], v228 offset:0\n"
"v_cvt_pk_bf16_f32 v132, v132, v133\n"
"v_mfma_f32_16x16x32_bf16 v[136:139], v[120:123], a[8:11], v[136:139]\n"
"ds_read_b64_tr_b16 v[190:191], v228 offset:10240\n"
"v_xor_b32 v229, s75, v229\n"
"v_mfma_f32_16x16x32_bf16 v[136:139], v[224:227], a[12:15], v[136:139]\n"
"buffer_store_dwordx4 v[156:159], v243, s[32:35], s17 offen sc0 sc1\n"
"ds_read_b64_tr_b16 v[186:187], v232 offset:2048\n"
"v_mfma_f32_16x16x32_bf16 v[152:155], v[224:227], a[28:31], v[152:155]\n"
"ds_read_b64_tr_b16 v[220:221], v232 offset:24576\n"
"v_xor_b32 v239, 64, v238\n"
"v_mfma_f32_16x16x32_bf16 v[152:155], v[120:123], a[24:27], v[152:155]\n"
"ds_read_b64_tr_b16 v[210:211], v228 offset:18432\n"
"v_mfma_f32_16x16x32_bf16 v[152:155], v[116:119], a[20:23], v[152:155]\n"
"ds_read_b64_tr_b16 v[208:209], v228 offset:16384\n"
"v_subrev_f32 v134, s22, v134\n"
"v_mfma_f32_16x16x32_bf16 v[152:155], v[112:115], a[16:19], v[152:155]\n"
"ds_read_b64_tr_b16 v[194:195], v232 offset:10240\n"
"v_mov_b64 v[42:43], s[22:23]\n"
"v_mfma_f32_16x16x32_bf16 v[152:155], v[108:111], a[76:79], v[152:155]\n"
"ds_read_b64_tr_b16 v[182:183], v228 offset:2048\n"
"v_add_u32 v252, s98, v254\n"
"v_mfma_f32_16x16x32_bf16 v[152:155], v[104:107], a[72:75], v[152:155]\n"
"ds_read_b64_tr_b16 v[192:193], v232 offset:8192\n"
"v_xor_b32 v249, s75, v249\n"
"v_mfma_f32_16x16x32_bf16 v[152:155], v[100:103], a[68:71], v[152:155]\n"
"ds_read_b64_tr_b16 v[184:185], v232 offset:0\n"
"ds_read_b64_tr_b16 v[188:189], v228 offset:8192\n"
"v_mfma_f32_16x16x32_bf16 v[152:155], v[96:99], a[64:67], v[152:155]\n"
"v_mov_b64 v[4:5], s[22:23]\n"
"v_subrev_f32 v176, s22, v176\n"
"v_mfma_f32_16x16x32_bf16 v[168:171], v[96:99], a[80:83], v[168:171]\n"
"ds_read_b64_tr_b16 v[96:97], v229 offset:0\n"
"ds_read_b64_tr_b16 v[98:99], v229 offset:2048\n"
"v_mfma_f32_16x16x32_bf16 v[168:171], v[100:103], a[84:87], v[168:171]\n"
"ds_read_b64_tr_b16 v[100:101], v233 offset:0\n"
"ds_read_b64_tr_b16 v[102:103], v233 offset:2048\n"
"v_mfma_f32_16x16x32_bf16 v[168:171], v[104:107], a[88:91], v[168:171]\n"
"ds_read_b64_tr_b16 v[104:105], v229 offset:8192\n"
"v_subrev_f32 v177, s22, v177\n"
"v_mfma_f32_16x16x32_bf16 v[168:171], v[108:111], a[92:95], v[168:171]\n"
"v_mov_b64 v[12:13], s[22:23]\n"
"v_mov_b64 v[22:23], s[22:23]\n"
"v_mfma_f32_16x16x32_bf16 v[168:171], v[112:115], a[32:35], v[168:171]\n"
"v_mov_b64 v[40:41], s[22:23]\n"
"v_cvt_pk_bf16_f32 v174, v176, v177\n"
"v_mfma_f32_16x16x32_bf16 v[168:171], v[116:119], a[36:39], v[168:171]\n"
"v_mov_b64 v[16:17], s[22:23]\n"
"v_mfma_f32_16x16x32_bf16 v[168:171], v[120:123], a[40:43], v[168:171]\n"
"s_add_u32 s47, s47, 1\n"
"v_mov_b64 v[20:21], s[22:23]\n"
"v_mov_b64 v[92:93], 0\n"
"v_mfma_f32_16x16x32_bf16 v[168:171], v[224:227], a[44:47], v[168:171]\n"
"s_waitcnt lgkmcnt(5)\n"
"v_mov_b64 v[54:55], s[22:23]\n"
"v_mov_b64 v[244:245], 0\n"
"LGL_%=:\n"
"v_mfma_f32_16x16x32_bf16 v[124:127], v[180:183], a[96:99], v[196:199]\n"
"ds_read_b64_tr_b16 v[106:107], v229 offset:10240\n"
"v_cvt_pk_bf16_f32 v133, v134, v135\n"
"v_mfma_f32_16x16x32_bf16 v[124:127], v[184:187], a[100:103], v[124:127]\n"
"ds_read_b64_tr_b16 v[108:109], v233 offset:8192\n"
"v_subrev_f32 v136, s22, v136\n"
"v_mfma_f32_16x16x32_bf16 v[124:127], v[188:191], a[104:107], v[124:127]\n"
"ds_read_b64_tr_b16 v[110:111], v233 offset:10240\n"
"v_subrev_f32 v137, s22, v137\n"
"v_mfma_f32_16x16x32_bf16 v[124:127], v[192:195], a[108:111], v[124:127]\n"
"ds_read_b64_tr_b16 v[112:113], v229 offset:16384\n"
"v_subrev_f32 v138, s22, v138\n"
"v_mfma_f32_16x16x32_bf16 v[124:127], v[208:211], a[144:147], v[124:127]\n"
"ds_read_b64_tr_b16 v[114:115], v229 offset:18432\n"
"v_subrev_f32 v139, s22, v139\n"
"v_mfma_f32_16x16x32_bf16 v[124:127], v[212:215], a[148:151], v[124:127]\n"
"s_waitcnt lgkmcnt(7)\n"
"ds_read_b64_tr_b16 v[116:117], v233 offset:16384\n"
"v_cvt_pk_bf16_f32 v134, v136, v137\n"
"v_mfma_f32_16x16x32_bf16 v[124:127], v[216:219], a[152:155], v[124:127]\n"
"ds_read_b64_tr_b16 v[118:119], v233 offset:18432\n"
"v_cvt_pk_bf16_f32 v135, v138, v139\n"
"v_mfma_f32_16x16x32_bf16 v[124:127], v[220:223], a[156:159], v[124:127]\n"
"buffer_store_dwordx4 v[172:175], v243, s[32:35], s17 offen offset:2048 sc0 sc1\n"
"v_subrev_f32 v148, s22, v148\n"
"v_mfma_f32_16x16x32_bf16 v[140:143], v[220:223], a[172:175], v[196:199]\n"
"ds_read_b64_tr_b16 v[120:121], v229 offset:24576\n"
"v_subrev_f32 v149, s22, v149\n"
"v_mfma_f32_16x16x32_bf16 v[140:143], v[216:219], a[168:171], v[140:143]\n"
"ds_read_b64_tr_b16 v[122:123], v229 offset:26624\n"
"v_subrev_f32 v150, s22, v150\n"
"v_mfma_f32_16x16x32_bf16 v[140:143], v[212:215], a[164:167], v[140:143]\n"
"ds_read_b64_tr_b16 v[224:225], v233 offset:24576\n"
"v_subrev_f32 v151, s22, v151\n"
"v_mfma_f32_16x16x32_bf16 v[140:143], v[208:211], a[160:163], v[140:143]\n"
"ds_read_b64_tr_b16 v[226:227], v233 offset:26624\n"
"v_cvt_pk_bf16_f32 v148, v148, v149\n"
"v_mfma_f32_16x16x32_bf16 v[140:143], v[192:195], a[124:127], v[140:143]\n"
"v_cvt_pk_bf16_f32 v149, v150, v151\n"
"v_subrev_f32 v152, s22, v152\n"
"v_mfma_f32_16x16x32_bf16 v[140:143], v[188:191], a[120:123], v[140:143]\n"
"s_add_u32 s41, s70, 0\n"
"v_subrev_f32 v153, s22, v153\n"
"v_subrev_f32 v154, s22, v154\n"
"v_mfma_f32_16x16x32_bf16 v[140:143], v[184:187], a[116:119], v[140:143]\n"
"s_mov_b32 m0, s71\n"
"v_subrev_f32 v155, s22, v155\n"
"v_cvt_pk_bf16_f32 v150, v152, v153\n"
"v_mfma_f32_16x16x32_bf16 v[140:143], v[180:183], a[112:115], v[140:143]\n"
"s_waitcnt vmcnt(14)\n"
"buffer_load_dwordx4 v240, s[28:31], s41 offen lds\n"
"v_mfma_f32_16x16x32_bf16 v[156:159], v[180:183], a[128:131], v[196:199]\n"
"v_xor_b32 v230, s75, v230\n"
"v_cvt_pk_bf16_f32 v151, v154, v155\n"
"v_mfma_f32_16x16x32_bf16 v[156:159], v[184:187], a[132:135], v[156:159]\n"
"v_xor_b32 v234, s75, v234\n"
"v_subrev_f32 v164, s22, v164\n"
"v_mfma_f32_16x16x32_bf16 v[156:159], v[188:191], a[136:139], v[156:159]\n"
"v_subrev_f32 v165, s22, v165\n"
"v_subrev_f32 v166, s22, v166\n"
"v_mfma_f32_16x16x32_bf16 v[156:159], v[192:195], a[140:143], v[156:159]\n"
"v_subrev_f32 v167, s22, v167\n"
"v_cvt_pk_bf16_f32 v164, v164, v165\n"
"v_mfma_f32_16x16x32_bf16 v[156:159], v[208:211], a[176:179], v[156:159]\n"
"v_cvt_pk_bf16_f32 v165, v166, v167\n"
"v_subrev_f32 v168, s22, v168\n"
"v_mfma_f32_16x16x32_bf16 v[156:159], v[212:215], a[180:183], v[156:159]\n"
"v_subrev_f32 v169, s22, v169\n"
"v_subrev_f32 v170, s22, v170\n"
"v_mfma_f32_16x16x32_bf16 v[156:159], v[216:219], a[184:187], v[156:159]\n"
"v_subrev_f32 v171, s22, v171\n"
"v_cvt_pk_bf16_f32 v166, v168, v169\n"
"v_mfma_f32_16x16x32_bf16 v[156:159], v[220:223], a[188:191], v[156:159]\n"
"buffer_store_dwordx4 v[132:135], v243, s[32:35], s16 offen offset:1024 sc0 sc1\n"
"v_cvt_pk_bf16_f32 v167, v170, v171\n"
"v_mfma_f32_16x16x32_bf16 v[172:175], v[220:223], a[252:255], v[196:199]\n"
"ds_read_b64_tr_b16 v[220:221], v234 offset:24576\n"
"v_xor_b32 v231, s75, v231\n"
"v_mfma_f32_16x16x32_bf16 v[172:175], v[216:219], a[248:251], v[172:175]\n"
"ds_read_b64_tr_b16 v[216:217], v230 offset:24576\n"
"v_xor_b32 v235, s75, v235\n"
"v_mfma_f32_16x16x32_bf16 v[172:175], v[212:215], a[244:247], v[172:175]\n"
"ds_read_b64_tr_b16 v[212:213], v234 offset:16384\n"
"v_lshlrev_b32 v202, 16, v84\n"
"v_mfma_f32_16x16x32_bf16 v[172:175], v[208:211], a[240:243], v[172:175]\n"
"ds_read_b64_tr_b16 v[208:209], v230 offset:16384\n"
"v_and_b32 v84, s73, v84\n"
"v_mfma_f32_16x16x32_bf16 v[172:175], v[192:195], a[204:207], v[172:175]\n"
"ds_read_b64_tr_b16 v[192:193], v234 offset:8192\n"
"v_add_f32 v25, v25, v84\n"
"v_mfma_f32_16x16x32_bf16 v[172:175], v[188:191], a[200:203], v[172:175]\n"
"ds_read_b64_tr_b16 v[188:189], v230 offset:8192\n"
"v_lshlrev_b32 v203, 16, v85\n"
"v_mfma_f32_16x16x32_bf16 v[172:175], v[184:187], a[196:199], v[172:175]\n"
"s_add_u32 s40, s41, s52\n"
"ds_read_b64_tr_b16 v[184:185], v234 offset:0\n"
"v_and_b32 v85, s73, v85\n"
"v_mfma_f32_16x16x32_bf16 v[172:175], v[180:183], a[192:195], v[172:175]\n"
"s_waitcnt vmcnt(13) lgkmcnt(7)\n"
"buffer_load_dwordx4 v240, s[28:31], s40 offen offset:1024 lds\n"
"v_mfma_f32_16x16x32_bf16 v[128:131], v[96:99], a[96:99], v[196:199]\n"
"s_waitcnt lgkmcnt(6)\n"
"ds_read_b64_tr_b16 v[180:181], v230 offset:0\n"
"v_add_f32 v27, v27, v85\n"
"v_mfma_f32_16x16x32_bf16 v[128:131], v[100:103], a[100:103], v[128:131]\n"
"ds_read_b64_tr_b16 v[182:183], v230 offset:2048\n"
"v_lshlrev_b32 v204, 16, v86\n"
"v_mfma_f32_16x16x32_bf16 v[128:131], v[104:107], a[104:107], v[128:131]\n"
"ds_read_b64_tr_b16 v[186:187], v234 offset:2048\n"
"v_and_b32 v86, s73, v86\n"
"v_mfma_f32_16x16x32_bf16 v[128:131], v[108:111], a[108:111], v[128:131]\n"
"ds_read_b64_tr_b16 v[190:191], v230 offset:10240\n"
"v_add_f32 v29, v29, v86\n"
"v_mfma_f32_16x16x32_bf16 v[128:131], v[112:115], a[144:147], v[128:131]\n"
"s_waitcnt vmcnt(6)\n"
"ds_read_b64_tr_b16 v[194:195], v234 offset:10240\n"
"v_lshlrev_b32 v205, 16, v87\n"
"v_mfma_f32_16x16x32_bf16 v[128:131], v[116:119], a[148:151], v[128:131]\n"
"v_subrev_u32 v200, s21, v200\n"
"ds_read_b64_tr_b16 v[210:211], v230 offset:18432\n"
"v_mfma_f32_16x16x32_bf16 v[128:131], v[120:123], a[152:155], v[128:131]\n"
"v_cmp_lt_u32 vcc, s69, v200\n"
"ds_read_b64_tr_b16 v[214:215], v234 offset:18432\n"
"v_mfma_f32_16x16x32_bf16 v[128:131], v[224:227], a[156:159], v[128:131]\n"
"ds_read_b64_tr_b16 v[218:219], v230 offset:26624\n"
"s_cbranch_vccnz LGP_%=\nLGPK_%=:\n"
"v_mfma_f32_16x16x32_bf16 v[144:147], v[224:227], a[172:175], v[196:199]\n"
"s_sub_u32 s72, s72, 1\n"
"ds_read_b64_tr_b16 v[222:223], v234 offset:26624\n"
"v_and_b32 v87, s73, v87\n"
"v_mfma_f32_16x16x32_bf16 v[144:147], v[120:123], a[168:171], v[144:147]\n"
"s_and_b32 s72, s72, 7\n"
"v_add_f32 v24, v24, v202\n"
"v_add_f32 v31, v31, v87\n"
"v_mfma_f32_16x16x32_bf16 v[144:147], v[116:119], a[164:167], v[144:147]\n"
"v_add_f32 v26, v26, v203\n"
"v_lshlrev_b32 v206, 16, v88\n"
"v_mfma_f32_16x16x32_bf16 v[144:147], v[112:115], a[160:163], v[144:147]\n"
"buffer_load_dwordx4 v[72:75], v243, s[32:35], s18 offen sc1\n"
"v_mfma_f32_16x16x32_bf16 v[144:147], v[108:111], a[124:127], v[144:147]\n"
"v_and_b32 v88, s73, v88\n"
"v_add_f32 v33, v33, v88\n"
"v_mfma_f32_16x16x32_bf16 v[144:147], v[104:107], a[120:123], v[144:147]\n"
"v_lshlrev_b32 v207, 16, v89\n"
"v_and_b32 v89, s73, v89\n"
"v_mfma_f32_16x16x32_bf16 v[144:147], v[100:103], a[116:119], v[144:147]\n"
"v_add_f32 v35, v35, v89\n"
"v_lshlrev_b32 v202, 16, v90\n"
"v_mfma_f32_16x16x32_bf16 v[144:147], v[96:99], a[112:115], v[144:147]\n"
"buffer_store_dwordx4 v[148:151], v243, s[32:35], s16 offen offset:3072 sc0 sc1\n"
"v_and_b32 v90, s73, v90\n"
"v_mfma_f32_16x16x32_bf16 v[160:163], v[96:99], a[128:131], v[196:199]\n"
"v_add_f32 v37, v37, v90\n"
"v_lshlrev_b32 v203, 16, v91\n"
"v_mfma_f32_16x16x32_bf16 v[160:163], v[100:103], a[132:135], v[160:163]\n"
"v_and_b32 v91, s73, v91\n"
"v_add_f32 v39, v39, v91\n"
"v_mfma_f32_16x16x32_bf16 v[160:163], v[104:107], a[136:139], v[160:163]\n"
"v_add_f32 v28, v28, v204\n"
"v_add_f32 v30, v30, v205\n"
"v_mfma_f32_16x16x32_bf16 v[160:163], v[108:111], a[140:143], v[160:163]\n"
"v_add_f32 v32, v32, v206\n"
"v_add_f32 v34, v34, v207\n"
"v_mfma_f32_16x16x32_bf16 v[160:163], v[112:115], a[176:179], v[160:163]\n"
"v_lshlrev_b32 v204, 16, v92\n"
"v_and_b32 v92, s73, v92\n"
"v_mfma_f32_16x16x32_bf16 v[160:163], v[116:119], a[180:183], v[160:163]\n"
"v_add_f32 v41, v41, v92\n"
"v_lshlrev_b32 v205, 16, v93\n"
"v_mfma_f32_16x16x32_bf16 v[160:163], v[120:123], a[184:187], v[160:163]\n"
"s_add_u32 s40, s41, s53\n"
"v_and_b32 v93, s73, v93\n"
"v_add_f32 v43, v43, v93\n"
"v_mfma_f32_16x16x32_bf16 v[160:163], v[224:227], a[188:191], v[160:163]\n"
"buffer_load_dwordx4 v240, s[28:31], s40 offen offset:2048 lds\n"
"v_mfma_f32_16x16x32_bf16 v[176:179], v[224:227], a[252:255], v[196:199]\n"
"ds_read_b64_tr_b16 v[224:225], v235 offset:24576\n"
"v_lshlrev_b32 v206, 16, v94\n"
"v_mfma_f32_16x16x32_bf16 v[176:179], v[120:123], a[248:251], v[176:179]\n"
"ds_read_b64_tr_b16 v[120:121], v231 offset:24576\n"
"v_and_b32 v94, s73, v94\n"
"v_mfma_f32_16x16x32_bf16 v[176:179], v[116:119], a[244:247], v[176:179]\n"
"ds_read_b64_tr_b16 v[116:117], v235 offset:16384\n"
"v_add_f32 v45, v45, v94\n"
"v_mfma_f32_16x16x32_bf16 v[176:179], v[112:115], a[240:243], v[176:179]\n"
"buffer_load_dwordx4 v[76:79], v243, s[32:35], s18 offen offset:1024 sc1\n"
"v_mfma_f32_16x16x32_bf16 v[176:179], v[108:111], a[204:207], v[176:179]\n"
"ds_read_b64_tr_b16 v[108:109], v235 offset:8192\n"
"v_lshlrev_b32 v207, 16, v95\n"
"v_mfma_f32_16x16x32_bf16 v[176:179], v[104:107], a[200:203], v[176:179]\n"
"ds_read_b64_tr_b16 v[104:105], v231 offset:8192\n"
"v_and_b32 v95, s73, v95\n"
"v_mfma_f32_16x16x32_bf16 v[176:179], v[100:103], a[196:199], v[176:179]\n"
"s_add_u32 s40, s41, s54\n"
"ds_read_b64_tr_b16 v[100:101], v235 offset:0\n"
"v_add_f32 v47, v47, v95\n"
"v_mfma_f32_16x16x32_bf16 v[176:179], v[96:99], a[192:195], v[176:179]\n"
"s_waitcnt lgkmcnt(6)\n"
"buffer_load_dwordx4 v240, s[28:31], s40 offen offset:3072 lds\n"
"v_mfma_f32_16x16x32_bf16 v[132:135], v[180:183], a[96:99], v[196:199]\n"
"ds_read_b64_tr_b16 v[96:97], v231 offset:0\n"
"v_add_f32 v36, v36, v202\n"
"v_mfma_f32_16x16x32_bf16 v[132:135], v[184:187], a[100:103], v[132:135]\n"
"ds_read_b64_tr_b16 v[98:99], v231 offset:2048\n"
"v_add_f32 v38, v38, v203\n"
"v_mfma_f32_16x16x32_bf16 v[132:135], v[188:191], a[104:107], v[132:135]\n"
"ds_read_b64_tr_b16 v[102:103], v235 offset:2048\n"
"v_add_f32 v44, v44, v206\n"
"v_mfma_f32_16x16x32_bf16 v[132:135], v[192:195], a[108:111], v[132:135]\n"
"ds_read_b64_tr_b16 v[106:107], v231 offset:10240\n"
"v_add_f32 v46, v46, v207\n"
"v_mfma_f32_16x16x32_bf16 v[132:135], v[208:211], a[144:147], v[132:135]\n"
"ds_read_b64_tr_b16 v[110:111], v235 offset:10240\n"
"v_add_f32 v40, v40, v204\n"
"v_mfma_f32_16x16x32_bf16 v[132:135], v[212:215], a[148:151], v[132:135]\n"
"ds_read_b64_tr_b16 v[112:113], v231 offset:16384\n"
"v_add_f32 v42, v42, v205\n"
"v_mfma_f32_16x16x32_bf16 v[132:135], v[216:219], a[152:155], v[132:135]\n"
"ds_read_b64_tr_b16 v[114:115], v231 offset:18432\n"
"v_mfma_f32_16x16x32_bf16 v[132:135], v[220:223], a[156:159], v[132:135]\n"
"buffer_store_dwordx4 v[164:167], v243, s[32:35], s17 offen offset:1024 sc0 sc1\n"
"v_lshlrev_b32 v206, 16, v244\n"
"v_mfma_f32_16x16x32_bf16 v[148:151], v[220:223], a[172:175], v[196:199]\n"
"ds_read_b64_tr_b16 v[118:119], v235 offset:18432\n"
"v_and_b32 v244, s73, v244\n"
"v_mfma_f32_16x16x32_bf16 v[148:151], v[216:219], a[168:171], v[148:151]\n"
"ds_read_b64_tr_b16 v[122:123], v231 offset:26624\n"
"v_add_f32 v49, v49, v244\n"
"v_mfma_f32_16x16x32_bf16 v[148:151], v[212:215], a[164:167], v[148:151]\n"
"ds_read_b64_tr_b16 v[226:227], v235 offset:26624\n"
"v_lshlrev_b32 v207, 16, v245\n"
"v_mfma_f32_16x16x32_bf16 v[148:151], v[208:211], a[160:163], v[148:151]\n"
"buffer_load_dwordx4 v[80:83], v243, s[32:35], s18 offen offset:2048 sc1\n"
"v_mfma_f32_16x16x32_bf16 v[148:151], v[192:195], a[124:127], v[148:151]\n"
"v_and_b32 v245, s73, v245\n"
"v_add_f32 v51, v51, v245\n"
"v_mfma_f32_16x16x32_bf16 v[148:151], v[188:191], a[120:123], v[148:151]\n"
"s_add_u32 s41, s70, 163840\n"
"v_lshlrev_b32 v202, 16, v246\n"
"v_and_b32 v246, s73, v246\n"
"v_mfma_f32_16x16x32_bf16 v[148:151], v[184:187], a[116:119], v[148:151]\n"
"s_add_u32 m0, s71, 16384\n"
"v_add_f32 v53, v53, v246\n"
"v_lshlrev_b32 v203, 16, v247\n"
"v_mfma_f32_16x16x32_bf16 v[148:151], v[180:183], a[112:115], v[148:151]\n"
"buffer_load_dwordx4 v240, s[28:31], s41 offen lds\n"
"v_mfma_f32_16x16x32_bf16 v[164:167], v[180:183], a[128:131], v[196:199]\n"
"v_and_b32 v247, s73, v247\n"
"v_add_f32 v55, v55, v247\n"
"v_mfma_f32_16x16x32_bf16 v[164:167], v[184:187], a[132:135], v[164:167]\n"
"v_add_f32 v48, v48, v206\n"
"v_add_f32 v50, v50, v207\n"
"v_mfma_f32_16x16x32_bf16 v[164:167], v[188:191], a[136:139], v[164:167]\n"
"v_add_f32 v52, v52, v202\n"
"v_add_f32 v54, v54, v203\n"
"v_mfma_f32_16x16x32_bf16 v[164:167], v[192:195], a[140:143], v[164:167]\n"
"v_mfma_f32_16x16x32_bf16 v[164:167], v[208:211], a[176:179], v[164:167]\n"
"v_mfma_f32_16x16x32_bf16 v[164:167], v[212:215], a[180:183], v[164:167]\n"
"v_mfma_f32_16x16x32_bf16 v[164:167], v[216:219], a[184:187], v[164:167]\n"
"s_waitcnt vmcnt(12)\n"
"v_mfma_f32_16x16x32_bf16 v[164:167], v[220:223], a[188:191], v[164:167]\n"
"s_barrier\n"
"s_waitcnt lgkmcnt(0)\n"
"v_mfma_f32_16x16x32_bf16 v[136:139], v[96:99], a[96:99], v[196:199]\n"
"ds_read_b64_tr_b16 v[180:181], v228 offset:32768\n"
"v_mfma_f32_16x16x32_bf16 v[136:139], v[100:103], a[100:103], v[136:139]\n"
"ds_read_b64_tr_b16 v[182:183], v228 offset:34816\n"
"v_mfma_f32_16x16x32_bf16 v[136:139], v[104:107], a[104:107], v[136:139]\n"
"ds_read_b64_tr_b16 v[184:185], v232 offset:32768\n"
"v_mfma_f32_16x16x32_bf16 v[136:139], v[108:111], a[108:111], v[136:139]\n"
"ds_read_b64_tr_b16 v[186:187], v232 offset:34816\n"
"v_mfma_f32_16x16x32_bf16 v[136:139], v[112:115], a[144:147], v[136:139]\n"
"ds_read_b64_tr_b16 v[188:189], v228 offset:40960\n"
"v_mfma_f32_16x16x32_bf16 v[136:139], v[116:119], a[148:151], v[136:139]\n"
"ds_read_b64_tr_b16 v[190:191], v228 offset:43008\n"
"v_mfma_f32_16x16x32_bf16 v[136:139], v[120:123], a[152:155], v[136:139]\n"
"s_add_u32 s40, s41, s52\n"
"ds_read_b64_tr_b16 v[192:193], v232 offset:40960\n"
"v_mfma_f32_16x16x32_bf16 v[136:139], v[224:227], a[156:159], v[136:139]\n"
"buffer_load_dwordx4 v240, s[28:31], s40 offen offset:1024 lds\n"
"v_mfma_f32_16x16x32_bf16 v[152:155], v[224:227], a[172:175], v[196:199]\n"
"ds_read_b64_tr_b16 v[194:195], v232 offset:43008\n"
"v_mfma_f32_16x16x32_bf16 v[152:155], v[120:123], a[168:171], v[152:155]\n"
"ds_read_b64_tr_b16 v[208:209], v228 offset:49152\n"
"v_mfma_f32_16x16x32_bf16 v[152:155], v[116:119], a[164:167], v[152:155]\n"
"ds_read_b64_tr_b16 v[210:211], v228 offset:51200\n"
"v_mfma_f32_16x16x32_bf16 v[152:155], v[112:115], a[160:163], v[152:155]\n"
"ds_read_b64_tr_b16 v[212:213], v232 offset:49152\n"
"v_mfma_f32_16x16x32_bf16 v[152:155], v[108:111], a[124:127], v[152:155]\n"
"ds_read_b64_tr_b16 v[214:215], v232 offset:51200\n"
"v_mfma_f32_16x16x32_bf16 v[152:155], v[104:107], a[120:123], v[152:155]\n"
"ds_read_b64_tr_b16 v[216:217], v228 offset:57344\n"
"v_mfma_f32_16x16x32_bf16 v[152:155], v[100:103], a[116:119], v[152:155]\n"
"s_add_u32 s40, s41, s53\n"
"ds_read_b64_tr_b16 v[218:219], v228 offset:59392\n"
"v_xor_b32 v228, s75, v228\n"
"v_mfma_f32_16x16x32_bf16 v[152:155], v[96:99], a[112:115], v[152:155]\n"
"buffer_load_dwordx4 v240, s[28:31], s40 offen offset:2048 lds\n"
"v_mfma_f32_16x16x32_bf16 v[168:171], v[96:99], a[128:131], v[196:199]\n"
"ds_read_b64_tr_b16 v[220:221], v232 offset:57344\n"
"v_mfma_f32_16x16x32_bf16 v[168:171], v[100:103], a[132:135], v[168:171]\n"
"ds_read_b64_tr_b16 v[222:223], v232 offset:59392\n"
"v_xor_b32 v232, s75, v232\n"
"v_mfma_f32_16x16x32_bf16 v[168:171], v[104:107], a[136:139], v[168:171]\n"
"ds_read_b64_tr_b16 v[96:97], v229 offset:32768\n"
"v_mfma_f32_16x16x32_bf16 v[168:171], v[108:111], a[140:143], v[168:171]\n"
"ds_read_b64_tr_b16 v[98:99], v229 offset:34816\n"
"v_mfma_f32_16x16x32_bf16 v[168:171], v[112:115], a[176:179], v[168:171]\n"
"ds_read_b64_tr_b16 v[100:101], v233 offset:32768\n"
"v_mfma_f32_16x16x32_bf16 v[168:171], v[116:119], a[180:183], v[168:171]\n"
"ds_read_b64_tr_b16 v[102:103], v233 offset:34816\n"
"v_mfma_f32_16x16x32_bf16 v[168:171], v[120:123], a[184:187], v[168:171]\n"
"s_add_u32 s40, s41, s54\n"
"ds_read_b64_tr_b16 v[104:105], v229 offset:40960\n"
"v_mfma_f32_16x16x32_bf16 v[168:171], v[224:227], a[188:191], v[168:171]\n"
"s_waitcnt lgkmcnt(7)\n"
"buffer_load_dwordx4 v240, s[28:31], s40 offen offset:3072 lds\n"
"v_mfma_f32_16x16x32_bf16 v[124:127], v[180:183], a[48:51], v[124:127]\n"
"ds_read_b64_tr_b16 v[106:107], v229 offset:43008\n"
"v_mfma_f32_16x16x32_bf16 v[124:127], v[184:187], a[52:55], v[124:127]\n"
"ds_read_b64_tr_b16 v[108:109], v233 offset:40960\n"
"v_mfma_f32_16x16x32_bf16 v[124:127], v[188:191], a[56:59], v[124:127]\n"
"ds_read_b64_tr_b16 v[110:111], v233 offset:43008\n"
"v_mfma_f32_16x16x32_bf16 v[124:127], v[192:195], a[60:63], v[124:127]\n"
"ds_read_b64_tr_b16 v[112:113], v229 offset:49152\n"
"v_mfma_f32_16x16x32_bf16 v[124:127], v[208:211], a[0:3], v[124:127]\n"
"ds_read_b64_tr_b16 v[114:115], v229 offset:51200\n"
"v_mfma_f32_16x16x32_bf16 v[124:127], v[212:215], a[4:7], v[124:127]\n"
"s_add_u32 s41, s70, 327680\n"
"ds_read_b64_tr_b16 v[116:117], v233 offset:49152\n"
"v_mfma_f32_16x16x32_bf16 v[124:127], v[216:219], a[8:11], v[124:127]\n"
"s_waitcnt lgkmcnt(6)\n"
"s_add_u32 m0, s71, 32768\n"
"ds_read_b64_tr_b16 v[118:119], v233 offset:51200\n"
"v_mfma_f32_16x16x32_bf16 v[124:127], v[220:223], a[12:15], v[124:127]\n"
"buffer_load_dwordx4 v240, s[28:31], s41 offen lds\n"
"v_mfma_f32_16x16x32_bf16 v[140:143], v[220:223], a[28:31], v[140:143]\n"
"ds_read_b64_tr_b16 v[120:121], v229 offset:57344\n"
"v_mfma_f32_16x16x32_bf16 v[140:143], v[216:219], a[24:27], v[140:143]\n"
"ds_read_b64_tr_b16 v[122:123], v229 offset:59392\n"
"v_xor_b32 v229, s75, v229\n"
"v_mfma_f32_16x16x32_bf16 v[140:143], v[212:215], a[20:23], v[140:143]\n"
"ds_read_b64_tr_b16 v[224:225], v233 offset:57344\n"
"v_mfma_f32_16x16x32_bf16 v[140:143], v[208:211], a[16:19], v[140:143]\n"
"ds_read_b64_tr_b16 v[226:227], v233 offset:59392\n"
"v_xor_b32 v233, s75, v233\n"
"v_mfma_f32_16x16x32_bf16 v[140:143], v[192:195], a[76:79], v[140:143]\n"
"v_mfma_f32_16x16x32_bf16 v[140:143], v[188:191], a[72:75], v[140:143]\n"
"v_subrev_f32 v124, s22, v124\n"
"v_subrev_f32 v125, s22, v125\n"
"v_mfma_f32_16x16x32_bf16 v[140:143], v[184:187], a[68:71], v[140:143]\n"
"s_add_u32 s40, s41, s52\n"
"v_subrev_f32 v126, s22, v126\n"
"v_subrev_f32 v127, s22, v127\n"
"v_mfma_f32_16x16x32_bf16 v[140:143], v[180:183], a[64:67], v[140:143]\n"
"buffer_load_dwordx4 v240, s[28:31], s40 offen offset:1024 lds\n"
"v_mfma_f32_16x16x32_bf16 v[156:159], v[180:183], a[80:83], v[156:159]\n"
"v_cvt_pk_bf16_f32 v124, v124, v125\n"
"v_cvt_pk_bf16_f32 v125, v126, v127\n"
"v_mfma_f32_16x16x32_bf16 v[156:159], v[184:187], a[84:87], v[156:159]\n"
"v_mfma_f32_16x16x32_bf16 v[156:159], v[188:191], a[88:91], v[156:159]\n"
"v_mfma_f32_16x16x32_bf16 v[156:159], v[192:195], a[92:95], v[156:159]\n"
"v_mfma_f32_16x16x32_bf16 v[156:159], v[208:211], a[32:35], v[156:159]\n"
"v_mfma_f32_16x16x32_bf16 v[156:159], v[212:215], a[36:39], v[156:159]\n"
"v_subrev_f32 v140, s22, v140\n"
"v_subrev_f32 v141, s22, v141\n"
"v_mfma_f32_16x16x32_bf16 v[156:159], v[216:219], a[40:43], v[156:159]\n"
"s_waitcnt vmcnt(7)\n"
"v_subrev_f32 v142, s22, v142\n"
"v_subrev_f32 v143, s22, v143\n"
"v_mfma_f32_16x16x32_bf16 v[156:159], v[220:223], a[44:47], v[156:159]\n"
"s_barrier\n"
"buffer_store_dword v241, v248, s[84:87], 0 offen sc1\n"
"v_mfma_f32_16x16x32_bf16 v[172:175], v[220:223], a[228:231], v[172:175]\n"
"ds_read_b64_tr_b16 v[220:221], v234 offset:57344\n"
"v_cvt_pk_bf16_f32 v140, v140, v141\n"
"v_mfma_f32_16x16x32_bf16 v[172:175], v[216:219], a[224:227], v[172:175]\n"
"ds_read_b64_tr_b16 v[216:217], v230 offset:57344\n"
"v_cvt_pk_bf16_f32 v141, v142, v143\n"
"v_mfma_f32_16x16x32_bf16 v[172:175], v[212:215], a[212:215], v[172:175]\n"
"ds_read_b64_tr_b16 v[212:213], v234 offset:49152\n"
"v_add_u32 v241, 1, v241\n"
"v_mfma_f32_16x16x32_bf16 v[172:175], v[208:211], a[208:211], v[172:175]\n"
"s_add_u32 s16, s90, s62\n"
"buffer_load_dwordx4 v[84:87], v243, s[32:35], s18 offen offset:3072 sc1\n"
"v_mfma_f32_16x16x32_bf16 v[172:175], v[192:195], a[236:239], v[172:175]\n"
"s_add_u32 s17, s16, 4096\n"
"ds_read_b64_tr_b16 v[192:193], v234 offset:40960\n"
"v_mfma_f32_16x16x32_bf16 v[172:175], v[188:191], a[232:235], v[172:175]\n"
"s_lshl_b32 s62, s70, 9\n"
"ds_read_b64_tr_b16 v[188:189], v230 offset:40960\n"
"v_subrev_f32 v156, s22, v156\n"
"v_mfma_f32_16x16x32_bf16 v[172:175], v[184:187], a[220:223], v[172:175]\n"
"s_add_u32 s40, s41, s53\n"
"ds_read_b64_tr_b16 v[184:185], v234 offset:32768\n"
"v_subrev_f32 v157, s22, v157\n"
"v_mfma_f32_16x16x32_bf16 v[172:175], v[180:183], a[216:219], v[172:175]\n"
"buffer_load_dwordx4 v240, s[28:31], s40 offen offset:2048 lds\n"
"v_mfma_f32_16x16x32_bf16 v[128:131], v[96:99], a[48:51], v[128:131]\n"
"ds_read_b64_tr_b16 v[180:181], v230 offset:32768\n"
"v_subrev_f32 v158, s22, v158\n"
"v_mfma_f32_16x16x32_bf16 v[128:131], v[100:103], a[52:55], v[128:131]\n"
"s_waitcnt lgkmcnt(5)\n"
"ds_read_b64_tr_b16 v[182:183], v230 offset:34816\n"
"v_subrev_f32 v159, s22, v159\n"
"v_mfma_f32_16x16x32_bf16 v[128:131], v[104:107], a[56:59], v[128:131]\n"
"ds_read_b64_tr_b16 v[186:187], v234 offset:34816\n"
"v_cvt_pk_bf16_f32 v156, v156, v157\n"
"v_mfma_f32_16x16x32_bf16 v[128:131], v[108:111], a[60:63], v[128:131]\n"
"ds_read_b64_tr_b16 v[190:191], v230 offset:43008\n"
"v_cvt_pk_bf16_f32 v157, v158, v159\n"
"v_mfma_f32_16x16x32_bf16 v[128:131], v[112:115], a[0:3], v[128:131]\n"
"ds_read_b64_tr_b16 v[194:195], v234 offset:43008\n"
"v_lshlrev_b32 v202, 16, v72\n"
"v_mfma_f32_16x16x32_bf16 v[128:131], v[116:119], a[4:7], v[128:131]\n"
"ds_read_b64_tr_b16 v[208:209], v230 offset:49152\n"
"v_and_b32 v72, s73, v72\n"
"v_mfma_f32_16x16x32_bf16 v[128:131], v[120:123], a[8:11], v[128:131]\n"
"s_add_u32 s40, s41, s54\n"
"ds_read_b64_tr_b16 v[210:211], v230 offset:51200\n"
"v_add_f32 v0, v0, v202\n"
"v_mfma_f32_16x16x32_bf16 v[128:131], v[224:227], a[12:15], v[128:131]\n"
"buffer_load_dwordx4 v240, s[28:31], s40 offen offset:3072 lds\n"
"v_mfma_f32_16x16x32_bf16 v[144:147], v[224:227], a[28:31], v[144:147]\n"
"ds_read_b64_tr_b16 v[214:215], v234 offset:51200\n"
"v_add_f32 v1, v1, v72\n"
"v_mfma_f32_16x16x32_bf16 v[144:147], v[120:123], a[24:27], v[144:147]\n"
"ds_read_b64_tr_b16 v[218:219], v230 offset:59392\n"
"v_lshlrev_b32 v203, 16, v73\n"
"v_mfma_f32_16x16x32_bf16 v[144:147], v[116:119], a[20:23], v[144:147]\n"
"ds_read_b64_tr_b16 v[222:223], v234 offset:59392\n"
"v_and_b32 v73, s73, v73\n"
"v_mfma_f32_16x16x32_bf16 v[144:147], v[112:115], a[16:19], v[144:147]\n"
"buffer_load_dwordx4 v[88:91], v243, s[32:35], s19 offen sc1\n"
"v_mfma_f32_16x16x32_bf16 v[144:147], v[108:111], a[76:79], v[144:147]\n"
"v_add_f32 v2, v2, v203\n"
"v_add_f32 v3, v3, v73\n"
"v_mfma_f32_16x16x32_bf16 v[144:147], v[104:107], a[72:75], v[144:147]\n"
"s_add_u32 s41, s70, 491520\n"
"v_subrev_f32 v128, s22, v128\n"
"v_subrev_f32 v129, s22, v129\n"
"v_mfma_f32_16x16x32_bf16 v[144:147], v[100:103], a[68:71], v[144:147]\n"
"s_add_u32 m0, s71, 49152\n"
"v_subrev_f32 v130, s22, v130\n"
"v_subrev_f32 v131, s22, v131\n"
"v_mfma_f32_16x16x32_bf16 v[144:147], v[96:99], a[64:67], v[144:147]\n"
"buffer_load_dwordx4 v240, s[28:31], s41 offen lds\n"
"v_mfma_f32_16x16x32_bf16 v[160:163], v[96:99], a[80:83], v[160:163]\n"
"s_and_b32 s70, s88, 15\n"
"v_cvt_pk_bf16_f32 v126, v128, v129\n"
"v_cvt_pk_bf16_f32 v127, v130, v131\n"
"v_mfma_f32_16x16x32_bf16 v[160:163], v[100:103], a[84:87], v[160:163]\n"
"s_lshr_b32 s88, s88, 4\n"
"v_lshlrev_b32 v204, 16, v74\n"
"v_and_b32 v74, s73, v74\n"
"v_mfma_f32_16x16x32_bf16 v[160:163], v[104:107], a[88:91], v[160:163]\n"
"s_lshl_b32 s70, s70, 7\n"
"v_add_f32 v4, v4, v204\n"
"v_add_f32 v5, v5, v74\n"
"v_mfma_f32_16x16x32_bf16 v[160:163], v[108:111], a[92:95], v[160:163]\n"
"s_xor_b32 s71, s71, s75\n"
"v_lshlrev_b32 v205, 16, v75\n"
"v_and_b32 v75, s73, v75\n"
"v_mfma_f32_16x16x32_bf16 v[160:163], v[112:115], a[32:35], v[160:163]\n"
"v_add_f32 v6, v6, v205\n"
"v_add_f32 v7, v7, v75\n"
"v_mfma_f32_16x16x32_bf16 v[160:163], v[116:119], a[36:39], v[160:163]\n"
"v_subrev_f32 v144, s22, v144\n"
"v_subrev_f32 v145, s22, v145\n"
"v_mfma_f32_16x16x32_bf16 v[160:163], v[120:123], a[40:43], v[160:163]\n"
"v_subrev_f32 v146, s22, v146\n"
"v_subrev_f32 v147, s22, v147\n"
"v_mfma_f32_16x16x32_bf16 v[160:163], v[224:227], a[44:47], v[160:163]\n"
"buffer_store_dwordx4 v[124:127], v243, s[32:35], s16 offen sc0 sc1\n"
"v_cvt_pk_bf16_f32 v142, v144, v145\n"
"v_mfma_f32_16x16x32_bf16 v[176:179], v[224:227], a[228:231], v[176:179]\n"
"ds_read_b64_tr_b16 v[224:225], v235 offset:57344\n"
"v_cvt_pk_bf16_f32 v143, v146, v147\n"
"v_mfma_f32_16x16x32_bf16 v[176:179], v[120:123], a[224:227], v[176:179]\n"
"ds_read_b64_tr_b16 v[120:121], v231 offset:57344\n"
"v_lshlrev_b32 v206, 16, v76\n"
"v_mfma_f32_16x16x32_bf16 v[176:179], v[116:119], a[212:215], v[176:179]\n"
"ds_read_b64_tr_b16 v[116:117], v235 offset:49152\n"
"v_and_b32 v76, s73, v76\n"
"v_mfma_f32_16x16x32_bf16 v[176:179], v[112:115], a[208:211], v[176:179]\n"
"buffer_load_dwordx4 v[92:95], v243, s[32:35], s19 offen offset:1024 sc1\n"
"v_mfma_f32_16x16x32_bf16 v[176:179], v[108:111], a[236:239], v[176:179]\n"
"ds_read_b64_tr_b16 v[108:109], v235 offset:40960\n"
"v_add_f32 v8, v8, v206\n"
"v_mfma_f32_16x16x32_bf16 v[176:179], v[104:107], a[232:235], v[176:179]\n"
"ds_read_b64_tr_b16 v[104:105], v231 offset:40960\n"
"v_subrev_f32 v160, s22, v160\n"
"v_mfma_f32_16x16x32_bf16 v[176:179], v[100:103], a[220:223], v[176:179]\n"
"s_add_u32 s40, s41, s52\n"
"ds_read_b64_tr_b16 v[100:101], v235 offset:32768\n"
"v_subrev_f32 v161, s22, v161\n"
"v_mfma_f32_16x16x32_bf16 v[176:179], v[96:99], a[216:219], v[176:179]\n"
"s_waitcnt vmcnt(14) lgkmcnt(6)\n"
"buffer_load_dwordx4 v240, s[28:31], s40 offen offset:1024 lds\n"
"v_mfma_f32_16x16x32_bf16 v[132:135], v[180:183], a[48:51], v[132:135]\n"
"ds_read_b64_tr_b16 v[96:97], v231 offset:32768\n"
"v_subrev_f32 v162, s22, v162\n"
"v_mfma_f32_16x16x32_bf16 v[132:135], v[184:187], a[52:55], v[132:135]\n"
"ds_read_b64_tr_b16 v[98:99], v231 offset:34816\n"
"v_subrev_f32 v163, s22, v163\n"
"v_mfma_f32_16x16x32_bf16 v[132:135], v[188:191], a[56:59], v[132:135]\n"
"ds_read_b64_tr_b16 v[102:103], v235 offset:34816\n"
"v_cvt_pk_bf16_f32 v158, v160, v161\n"
"v_mfma_f32_16x16x32_bf16 v[132:135], v[192:195], a[60:63], v[132:135]\n"
"ds_read_b64_tr_b16 v[106:107], v231 offset:43008\n"
"v_cvt_pk_bf16_f32 v159, v162, v163\n"
"v_mfma_f32_16x16x32_bf16 v[132:135], v[208:211], a[0:3], v[132:135]\n"
"ds_read_b64_tr_b16 v[110:111], v235 offset:43008\n"
"v_add_f32 v9, v9, v76\n"
"v_mfma_f32_16x16x32_bf16 v[132:135], v[212:215], a[4:7], v[132:135]\n"
"ds_read_b64_tr_b16 v[112:113], v231 offset:49152\n"
"v_lshlrev_b32 v207, 16, v77\n"
"v_mfma_f32_16x16x32_bf16 v[132:135], v[216:219], a[8:11], v[132:135]\n"
"ds_read_b64_tr_b16 v[114:115], v231 offset:51200\n"
"v_and_b32 v77, s73, v77\n"
"v_mfma_f32_16x16x32_bf16 v[132:135], v[220:223], a[12:15], v[132:135]\n"
"buffer_store_dwordx4 v[140:143], v243, s[32:35], s16 offen offset:2048 sc0 sc1\n"
"v_add_f32 v10, v10, v207\n"
"v_mfma_f32_16x16x32_bf16 v[148:151], v[220:223], a[28:31], v[148:151]\n"
"ds_read_b64_tr_b16 v[118:119], v235 offset:51200\n"
"v_add_f32 v11, v11, v77\n"
"v_mfma_f32_16x16x32_bf16 v[148:151], v[216:219], a[24:27], v[148:151]\n"
"ds_read_b64_tr_b16 v[122:123], v231 offset:59392\n"
"v_lshlrev_b32 v202, 16, v78\n"
"v_mfma_f32_16x16x32_bf16 v[148:151], v[212:215], a[20:23], v[148:151]\n"
"ds_read_b64_tr_b16 v[226:227], v235 offset:59392\n"
"v_and_b32 v78, s73, v78\n"
"v_mfma_f32_16x16x32_bf16 v[148:151], v[208:211], a[16:19], v[148:151]\n"
"v_add_f32 v12, v12, v202\n"
"v_add_f32 v13, v13, v78\n"
"v_mfma_f32_16x16x32_bf16 v[148:151], v[192:195], a[76:79], v[148:151]\n"
"v_lshlrev_b32 v203, 16, v79\n"
"v_and_b32 v79, s73, v79\n"
"v_mfma_f32_16x16x32_bf16 v[148:151], v[188:191], a[72:75], v[148:151]\n"
"v_add_f32 v14, v14, v203\n"
"v_add_f32 v15, v15, v79\n"
"v_mfma_f32_16x16x32_bf16 v[148:151], v[184:187], a[68:71], v[148:151]\n"
"s_add_u32 s40, s41, s53\n"
"v_lshlrev_b32 v204, 16, v80\n"
"v_and_b32 v80, s73, v80\n"
"v_mfma_f32_16x16x32_bf16 v[148:151], v[180:183], a[64:67], v[148:151]\n"
"buffer_load_dwordx4 v240, s[28:31], s40 offen offset:2048 lds\n"
"v_mfma_f32_16x16x32_bf16 v[164:167], v[180:183], a[80:83], v[164:167]\n"
"v_add_f32 v16, v16, v204\n"
"v_add_f32 v17, v17, v80\n"
"v_mfma_f32_16x16x32_bf16 v[164:167], v[184:187], a[84:87], v[164:167]\n"
"v_lshlrev_b32 v205, 16, v81\n"
"v_and_b32 v81, s73, v81\n"
"v_mfma_f32_16x16x32_bf16 v[164:167], v[188:191], a[88:91], v[164:167]\n"
"v_add_f32 v18, v18, v205\n"
"v_add_f32 v19, v19, v81\n"
"v_mfma_f32_16x16x32_bf16 v[164:167], v[192:195], a[92:95], v[164:167]\n"
"buffer_load_dwordx4 v[244:247], v243, s[32:35], s19 offen offset:2048 sc1\n"
"v_mfma_f32_16x16x32_bf16 v[164:167], v[208:211], a[32:35], v[164:167]\n"
"s_lshl4_add_u32 s18, s72, s61\n"
"v_lshlrev_b32 v206, 16, v82\n"
"v_and_b32 v82, s73, v82\n"
"v_mfma_f32_16x16x32_bf16 v[164:167], v[212:215], a[36:39], v[164:167]\n"
"s_lshl_b32 s19, s72, 2\n"
"v_add_f32 v20, v20, v206\n"
"v_add_f32 v21, v21, v82\n"
"v_mfma_f32_16x16x32_bf16 v[164:167], v[216:219], a[40:43], v[164:167]\n"
"s_waitcnt vmcnt(14)\n"
"s_lshr_b32 s19, s89, s19\n"
"v_lshlrev_b32 v207, 16, v83\n"
"v_mfma_f32_16x16x32_bf16 v[164:167], v[220:223], a[44:47], v[164:167]\n"
"s_barrier\n"
"s_and_b32 s19, s19, 1\n"
"s_waitcnt lgkmcnt(0)\n"
"v_mfma_f32_16x16x32_bf16 v[136:139], v[96:99], a[48:51], v[136:139]\n"
"s_lshl3_add_u32 s18, s18, s19\n"
"ds_read_b64_tr_b16 v[180:181], v228 offset:0\n"
"v_and_b32 v83, s73, v83\n"
"v_mfma_f32_16x16x32_bf16 v[136:139], v[100:103], a[52:55], v[136:139]\n"
"s_lshl_b32 s18, s18, 13\n"
"ds_read_b64_tr_b16 v[182:183], v228 offset:2048\n"
"v_add_f32 v22, v22, v207\n"
"v_mfma_f32_16x16x32_bf16 v[136:139], v[104:107], a[56:59], v[136:139]\n"
"s_add_u32 s19, s18, 4096\n"
"v_lshlrev_b32_e64 v201, 2, s72\n"
"ds_read_b64_tr_b16 v[184:185], v232 offset:0\n"
"v_mfma_f32_16x16x32_bf16 v[136:139], v[108:111], a[60:63], v[136:139]\n"
"s_mov_b32 s69, 15\n"
"global_load_dword v200, v201, s[6:7] sc1\n"
"v_mfma_f32_16x16x32_bf16 v[136:139], v[112:115], a[0:3], v[136:139]\n"
"s_add_u32 s21, s21, 1\n"
"ds_read_b64_tr_b16 v[186:187], v232 offset:2048\n"
"v_add_f32 v23, v23, v83\n"
"v_mfma_f32_16x16x32_bf16 v[136:139], v[116:119], a[4:7], v[136:139]\n"
"ds_read_b64_tr_b16 v[188:189], v228 offset:8192\n"
"v_subrev_f32 v172, s22, v172\n"
"v_mfma_f32_16x16x32_bf16 v[136:139], v[120:123], a[8:11], v[136:139]\n"
"ds_read_b64_tr_b16 v[190:191], v228 offset:10240\n"
"v_subrev_f32 v173, s22, v173\n"
"v_mfma_f32_16x16x32_bf16 v[136:139], v[224:227], a[12:15], v[136:139]\n"
"buffer_store_dwordx4 v[156:159], v243, s[32:35], s17 offen sc0 sc1\n"
"v_subrev_f32 v174, s22, v174\n"
"v_mfma_f32_16x16x32_bf16 v[152:155], v[224:227], a[28:31], v[152:155]\n"
"ds_read_b64_tr_b16 v[192:193], v232 offset:8192\n"
"v_subrev_f32 v175, s22, v175\n"
"v_mfma_f32_16x16x32_bf16 v[152:155], v[120:123], a[24:27], v[152:155]\n"
"ds_read_b64_tr_b16 v[194:195], v232 offset:10240\n"
"v_cvt_pk_bf16_f32 v172, v172, v173\n"
"v_mfma_f32_16x16x32_bf16 v[152:155], v[116:119], a[20:23], v[152:155]\n"
"ds_read_b64_tr_b16 v[208:209], v228 offset:16384\n"
"v_cvt_pk_bf16_f32 v173, v174, v175\n"
"v_mfma_f32_16x16x32_bf16 v[152:155], v[112:115], a[16:19], v[152:155]\n"
"ds_read_b64_tr_b16 v[210:211], v228 offset:18432\n"
"v_subrev_f32 v176, s22, v176\n"
"v_mfma_f32_16x16x32_bf16 v[152:155], v[108:111], a[76:79], v[152:155]\n"
"ds_read_b64_tr_b16 v[212:213], v232 offset:16384\n"
"v_subrev_f32 v177, s22, v177\n"
"v_mfma_f32_16x16x32_bf16 v[152:155], v[104:107], a[72:75], v[152:155]\n"
"ds_read_b64_tr_b16 v[214:215], v232 offset:18432\n"
"v_subrev_f32 v178, s22, v178\n"
"v_mfma_f32_16x16x32_bf16 v[152:155], v[100:103], a[68:71], v[152:155]\n"
"s_add_u32 s40, s41, s54\n"
"ds_read_b64_tr_b16 v[216:217], v228 offset:24576\n"
"v_subrev_f32 v179, s22, v179\n"
"v_mfma_f32_16x16x32_bf16 v[152:155], v[96:99], a[64:67], v[152:155]\n"
"buffer_load_dwordx4 v240, s[28:31], s40 offen offset:3072 lds\n"
"v_mfma_f32_16x16x32_bf16 v[168:171], v[96:99], a[80:83], v[168:171]\n"
"ds_read_b64_tr_b16 v[218:219], v228 offset:26624\n"
"v_cvt_pk_bf16_f32 v174, v176, v177\n"
"v_mfma_f32_16x16x32_bf16 v[168:171], v[100:103], a[84:87], v[168:171]\n"
"ds_read_b64_tr_b16 v[220:221], v232 offset:24576\n"
"v_cvt_pk_bf16_f32 v175, v178, v179\n"
"v_mfma_f32_16x16x32_bf16 v[168:171], v[104:107], a[88:91], v[168:171]\n"
"ds_read_b64_tr_b16 v[222:223], v232 offset:26624\n"
"v_subrev_f32 v132, s22, v132\n"
"v_mfma_f32_16x16x32_bf16 v[168:171], v[108:111], a[92:95], v[168:171]\n"
"ds_read_b64_tr_b16 v[96:97], v229 offset:0\n"
"v_subrev_f32 v133, s22, v133\n"
"v_mfma_f32_16x16x32_bf16 v[168:171], v[112:115], a[32:35], v[168:171]\n"
"ds_read_b64_tr_b16 v[98:99], v229 offset:2048\n"
"v_subrev_f32 v134, s22, v134\n"
"v_mfma_f32_16x16x32_bf16 v[168:171], v[116:119], a[36:39], v[168:171]\n"
"ds_read_b64_tr_b16 v[100:101], v233 offset:0\n"
"v_subrev_f32 v135, s22, v135\n"
"v_mfma_f32_16x16x32_bf16 v[168:171], v[120:123], a[40:43], v[168:171]\n"
"s_add_u32 s47, s47, 1\n"
"ds_read_b64_tr_b16 v[102:103], v233 offset:2048\n"
"v_cvt_pk_bf16_f32 v132, v132, v133\n"
"v_mfma_f32_16x16x32_bf16 v[168:171], v[224:227], a[44:47], v[168:171]\n"
"s_waitcnt lgkmcnt(7)\n"
"s_cmp_lg_u32 s47, 9\n"
"ds_read_b64_tr_b16 v[104:105], v229 offset:8192\n"
"s_cbranch_scc1 LGL_%=\n"
"v_mfma_f32_16x16x32_bf16 v[0:3], v[180:183], a[96:99], v[0:3]\n"
"v_xor_b32 v235, s75, v235\n"
"v_subrev_f32 v136, s22, v136\n"
"v_mfma_f32_16x16x32_bf16 v[0:3], v[184:187], a[100:103], v[0:3]\n"
"ds_read_b64_tr_b16 v[114:115], v229 offset:18432\n"
"v_cvt_pk_bf16_f32 v133, v134, v135\n"
"v_mfma_f32_16x16x32_bf16 v[0:3], v[188:191], a[104:107], v[0:3]\n"
"v_mov_b32 v201, v237\n"
"v_subrev_f32 v138, s22, v138\n"
"v_subrev_f32 v139, s22, v139\n"
"v_mfma_f32_16x16x32_bf16 v[0:3], v[192:195], a[108:111], v[0:3]\n"
"global_load_dword v200, v201, s[6:7] sc1\n"
"v_mfma_f32_16x16x32_bf16 v[0:3], v[208:211], a[144:147], v[0:3]\n"
"v_subrev_f32 v167, s22, v167\n"
"v_cvt_pk_bf16_f32 v135, v138, v139\n"
"v_mfma_f32_16x16x32_bf16 v[0:3], v[212:215], a[148:151], v[0:3]\n"
"s_waitcnt lgkmcnt(3)\n"
"v_subrev_f32 v148, s22, v148\n"
"v_subrev_f32 v137, s22, v137\n"
"v_mfma_f32_16x16x32_bf16 v[0:3], v[216:219], a[152:155], v[0:3]\n"
"v_subrev_f32 v165, s22, v165\n"
"v_subrev_f32 v168, s22, v168\n"
"v_mfma_f32_16x16x32_bf16 v[0:3], v[220:223], a[156:159], v[0:3]\n"
"buffer_store_dwordx4 v[172:175], v243, s[32:35], s17 offen offset:2048 sc0 sc1\n"
"v_subrev_f32 v151, s22, v151\n"
"v_cvt_pk_bf16_f32 v134, v136, v137\n"
"v_mfma_f32_16x16x32_bf16 v[16:19], v[220:223], a[172:175], v[16:19]\n"
"buffer_store_dwordx4 v[132:135], v243, s[32:35], s16 offen offset:1024 sc0 sc1\n"
"v_subrev_f32 v149, s22, v149\n"
"v_subrev_f32 v150, s22, v150\n"
"v_subrev_f32 v164, s22, v164\n"
"v_cvt_pk_bf16_f32 v164, v164, v165\n"
"v_subrev_f32 v153, s22, v153\n"
"v_subrev_f32 v155, s22, v155\n"
"v_mfma_f32_16x16x32_bf16 v[16:19], v[216:219], a[168:171], v[16:19]\n"
"v_cvt_pk_bf16_f32 v148, v148, v149\n"
"v_subrev_f32 v154, s22, v154\n"
"v_mfma_f32_16x16x32_bf16 v[16:19], v[212:215], a[164:167], v[16:19]\n"
"v_cvt_pk_bf16_f32 v149, v150, v151\n"
"v_subrev_f32 v170, s22, v170\n"
"v_mfma_f32_16x16x32_bf16 v[16:19], v[208:211], a[160:163], v[16:19]\n"
"v_subrev_f32 v166, s22, v166\n"
"v_subrev_f32 v171, s22, v171\n"
"v_mfma_f32_16x16x32_bf16 v[16:19], v[192:195], a[124:127], v[16:19]\n"
"v_cvt_pk_bf16_f32 v165, v166, v167\n"
"v_cvt_pk_bf16_f32 v167, v170, v171\n"
"v_mfma_f32_16x16x32_bf16 v[16:19], v[188:191], a[120:123], v[16:19]\n"
"v_subrev_f32 v152, s22, v152\n"
"v_cvt_pk_bf16_f32 v151, v154, v155\n"
"v_mfma_f32_16x16x32_bf16 v[16:19], v[184:187], a[116:119], v[16:19]\n"
"v_cvt_pk_bf16_f32 v150, v152, v153\n"
"v_subrev_f32 v169, s22, v169\n"
"v_mfma_f32_16x16x32_bf16 v[16:19], v[180:183], a[112:115], v[16:19]\n"
"s_waitcnt vmcnt(16)\n"
"buffer_store_dwordx4 v[148:151], v243, s[32:35], s16 offen offset:3072 sc0 sc1\n"
"v_cvt_pk_bf16_f32 v166, v168, v169\n"
"v_mfma_f32_16x16x32_bf16 v[32:35], v[180:183], a[128:131], v[32:35]\n"
"buffer_store_dwordx4 v[164:167], v243, s[32:35], s17 offen offset:1024 sc0 sc1\n"
"ds_read_b64_tr_b16 v[120:121], v229 offset:24576\n"
"v_mfma_f32_16x16x32_bf16 v[32:35], v[184:187], a[132:135], v[32:35]\n"
"ds_read_b64_tr_b16 v[224:225], v233 offset:24576\n"
"v_xor_b32 v234, s75, v234\n"
"v_mfma_f32_16x16x32_bf16 v[32:35], v[188:191], a[136:139], v[32:35]\n"
"ds_read_b64_tr_b16 v[122:123], v229 offset:26624\n"
"v_lshlrev_b32 v203, 16, v85\n"
"v_mfma_f32_16x16x32_bf16 v[32:35], v[192:195], a[140:143], v[32:35]\n"
"ds_read_b64_tr_b16 v[108:109], v233 offset:8192\n"
"v_lshlrev_b32 v202, 16, v84\n"
"v_mfma_f32_16x16x32_bf16 v[32:35], v[208:211], a[176:179], v[32:35]\n"
"ds_read_b64_tr_b16 v[226:227], v233 offset:26624\n"
"v_and_b32 v84, s73, v84\n"
"v_mfma_f32_16x16x32_bf16 v[32:35], v[212:215], a[180:183], v[32:35]\n"
"ds_read_b64_tr_b16 v[110:111], v233 offset:10240\n"
"v_bfe_u32 v161, v242, 8, 2\n"
"v_mfma_f32_16x16x32_bf16 v[32:35], v[216:219], a[184:187], v[32:35]\n"
"ds_read_b64_tr_b16 v[106:107], v229 offset:10240\n"
"v_lshlrev_b32 v205, 16, v87\n"
"v_mfma_f32_16x16x32_bf16 v[32:35], v[220:223], a[188:191], v[32:35]\n"
"v_xor_b32 v230, s75, v230\n"
"v_lshlrev_b32 v204, 16, v86\n"
"v_mfma_f32_16x16x32_bf16 v[48:51], v[220:223], a[252:255], v[48:51]\n"
"ds_read_b64_tr_b16 v[220:221], v234 offset:24576\n"
"v_add_f32 v26, v26, v203\n"
"v_mfma_f32_16x16x32_bf16 v[48:51], v[216:219], a[248:251], v[48:51]\n"
"ds_read_b64_tr_b16 v[112:113], v229 offset:16384\n"
"v_bfe_u32 v160, v242, 6, 2\n"
"v_mfma_f32_16x16x32_bf16 v[48:51], v[212:215], a[244:247], v[48:51]\n"
"ds_read_b64_tr_b16 v[116:117], v233 offset:16384\n"
"v_add_f32 v24, v24, v202\n"
"v_mfma_f32_16x16x32_bf16 v[48:51], v[208:211], a[240:243], v[48:51]\n"
"ds_read_b64_tr_b16 v[118:119], v233 offset:18432\n"
"v_add_f32 v25, v25, v84\n"
"v_mfma_f32_16x16x32_bf16 v[48:51], v[192:195], a[204:207], v[48:51]\n"
"ds_read_b64_tr_b16 v[210:211], v230 offset:18432\n"
"v_add_f32 v28, v28, v204\n"
"v_mfma_f32_16x16x32_bf16 v[48:51], v[188:191], a[200:203], v[48:51]\n"
"ds_read_b64_tr_b16 v[188:189], v230 offset:8192\n"
"v_and_b32 v86, s73, v86\n"
"v_mfma_f32_16x16x32_bf16 v[48:51], v[184:187], a[196:199], v[48:51]\n"
"ds_read_b64_tr_b16 v[194:195], v234 offset:10240\n"
"v_mov_b32 v164, 0\n"
"v_mfma_f32_16x16x32_bf16 v[48:51], v[180:183], a[192:195], v[48:51]\n"
"s_waitcnt vmcnt(15)\n"
"v_xor_b32 v160, v160, v161\n"
"v_lshlrev_b32 v202, 16, v90\n"
"v_mfma_f32_16x16x32_bf16 v[4:7], v[96:99], a[96:99], v[4:7]\n"
"s_waitcnt lgkmcnt(6)\n"
"ds_read_b64_tr_b16 v[222:223], v234 offset:26624\n"
"v_lshlrev_b32 v203, 16, v91\n"
"v_mfma_f32_16x16x32_bf16 v[4:7], v[100:103], a[100:103], v[4:7]\n"
"ds_read_b64_tr_b16 v[184:185], v234 offset:0\n"
"v_lshlrev_b32 v206, 16, v88\n"
"v_mfma_f32_16x16x32_bf16 v[4:7], v[104:107], a[104:107], v[4:7]\n"
"ds_read_b64_tr_b16 v[216:217], v230 offset:24576\n"
"v_mfma_f32_16x16x32_bf16 v[4:7], v[108:111], a[108:111], v[4:7]\n"
"s_waitcnt lgkmcnt(6)\n"
"ds_read_b64_tr_b16 v[218:219], v230 offset:26624\n"
"v_and_b32 v91, s73, v91\n"
"v_mfma_f32_16x16x32_bf16 v[4:7], v[112:115], a[144:147], v[4:7]\n"
"s_waitcnt vmcnt(4)\n"
"ds_read_b64_tr_b16 v[190:191], v230 offset:10240\n"
"v_lshlrev_b32 v207, 16, v89\n"
"v_mfma_f32_16x16x32_bf16 v[4:7], v[116:119], a[148:151], v[4:7]\n"
"v_subrev_u32 v200, s21, v200\n"
"ds_read_b64_tr_b16 v[208:209], v230 offset:16384\n"
"v_mfma_f32_16x16x32_bf16 v[4:7], v[120:123], a[152:155], v[4:7]\n"
"v_cmp_lt_u32 vcc, s69, v200\n"
"ds_read_b64_tr_b16 v[182:183], v230 offset:2048\n"
"v_mfma_f32_16x16x32_bf16 v[4:7], v[224:227], a[156:159], v[4:7]\n"
"ds_read_b64_tr_b16 v[180:181], v230 offset:0\n"
"s_cbranch_vccnz LGQ_%=\nLGQK_%=:\n"
"v_mfma_f32_16x16x32_bf16 v[20:23], v[224:227], a[172:175], v[20:23]\n"
"s_bfe_u32 s20, s89, 65536\ns_lshl_b32 s20, s20, 13\ns_add_u32 s20, s20, 0\n"
"ds_read_b64_tr_b16 v[214:215], v234 offset:18432\n"
"v_and_b32 v85, s73, v85\n"
"v_mfma_f32_16x16x32_bf16 v[20:23], v[120:123], a[168:171], v[20:23]\n"
"buffer_load_dwordx4 v[124:127], v236, s[32:35], s20 offen sc1\n"
"v_mfma_f32_16x16x32_bf16 v[20:23], v[116:119], a[164:167], v[20:23]\n"
"v_and_b32 v90, s73, v90\n"
"v_add_f32 v37, v37, v90\n"
"v_mfma_f32_16x16x32_bf16 v[20:23], v[112:115], a[160:163], v[20:23]\n"
"s_bfe_u32 s20, s89, 65540\ns_lshl_b32 s20, s20, 13\ns_add_u32 s20, s20, 1048576\n"
"v_add_f32 v36, v36, v202\n"
"v_add_f32 v38, v38, v203\n"
"v_mfma_f32_16x16x32_bf16 v[20:23], v[108:111], a[124:127], v[20:23]\n"
"buffer_load_dwordx4 v[128:131], v236, s[32:35], s20 offen sc1\n"
"v_mfma_f32_16x16x32_bf16 v[20:23], v[104:107], a[120:123], v[20:23]\n"
"v_add_u32 v160, -1, v160\n"
"v_add_f32 v39, v39, v91\n"
"v_mfma_f32_16x16x32_bf16 v[20:23], v[100:103], a[116:119], v[20:23]\n"
"s_bfe_u32 s20, s89, 65544\ns_lshl_b32 s20, s20, 13\ns_add_u32 s20, s20, 2097152\n"
"v_add_f32 v32, v32, v206\n"
"v_mfma_f32_16x16x32_bf16 v[20:23], v[96:99], a[112:115], v[20:23]\n"
"buffer_load_dwordx4 v[132:135], v236, s[32:35], s20 offen sc1\n"
"v_mfma_f32_16x16x32_bf16 v[36:39], v[96:99], a[128:131], v[36:39]\n"
"v_lshlrev_b32 v204, 16, v92\n"
"v_add_f32 v40, v40, v204\n"
"v_mfma_f32_16x16x32_bf16 v[36:39], v[100:103], a[132:135], v[36:39]\n"
"s_bfe_u32 s20, s89, 65548\ns_lshl_b32 s20, s20, 13\ns_add_u32 s20, s20, 3145728\n"
"ds_read_b64_tr_b16 v[212:213], v234 offset:16384\n"
"v_add_f32 v30, v30, v205\n"
"v_mfma_f32_16x16x32_bf16 v[36:39], v[104:107], a[136:139], v[36:39]\n"
"buffer_load_dwordx4 v[136:139], v236, s[32:35], s20 offen sc1\n"
"v_mfma_f32_16x16x32_bf16 v[36:39], v[108:111], a[140:143], v[36:39]\n"
"v_and_b32 v92, s73, v92\n"
"v_lshlrev_b32 v206, 16, v94\n"
"v_mfma_f32_16x16x32_bf16 v[36:39], v[112:115], a[176:179], v[36:39]\n"
"s_bfe_u32 s20, s89, 65552\ns_lshl_b32 s20, s20, 13\ns_add_u32 s20, s20, 4194304\n"
"ds_read_b64_tr_b16 v[186:187], v234 offset:2048\n"
"ds_read_b64_tr_b16 v[192:193], v234 offset:8192\n"
"v_mfma_f32_16x16x32_bf16 v[36:39], v[116:119], a[180:183], v[36:39]\n"
"buffer_load_dwordx4 v[140:143], v236, s[32:35], s20 offen sc1\n"
"v_mfma_f32_16x16x32_bf16 v[36:39], v[120:123], a[184:187], v[36:39]\n"
"v_and_b32 v94, s73, v94\n"
"v_mfma_f32_16x16x32_bf16 v[36:39], v[224:227], a[188:191], v[36:39]\n"
"s_bfe_u32 s20, s89, 65556\ns_lshl_b32 s20, s20, 13\ns_add_u32 s20, s20, 5242880\n"
"v_add_f32 v34, v34, v207\n"
"v_lshlrev_b32 v205, 16, v93\n"
"v_mfma_f32_16x16x32_bf16 v[52:55], v[224:227], a[252:255], v[52:55]\n"
"buffer_load_dwordx4 v[144:147], v236, s[32:35], s20 offen sc1\n"
"v_mfma_f32_16x16x32_bf16 v[52:55], v[120:123], a[248:251], v[52:55]\n"
"v_ashrrev_i32 v160, 31, v160\n"
"v_add_f32 v44, v44, v206\n"
"v_mfma_f32_16x16x32_bf16 v[52:55], v[116:119], a[244:247], v[52:55]\n"
"s_bfe_u32 s20, s89, 65560\ns_lshl_b32 s20, s20, 13\ns_add_u32 s20, s20, 6291456\n"
"v_xor_b32 v231, s75, v231\n"
"v_lshlrev_b32 v207, 16, v95\n"
"v_mfma_f32_16x16x32_bf16 v[52:55], v[112:115], a[240:243], v[52:55]\n"
"buffer_load_dwordx4 v[148:151], v236, s[32:35], s20 offen sc1\n"
"v_mfma_f32_16x16x32_bf16 v[52:55], v[108:111], a[204:207], v[52:55]\n"
"s_bfe_u32 s20, s89, 65564\ns_lshl_b32 s20, s20, 13\ns_add_u32 s20, s20, 7340032\n"
"v_and_b32 v87, s73, v87\n"
"v_and_b32 v95, s73, v95\n"
"v_mfma_f32_16x16x32_bf16 v[52:55], v[104:107], a[200:203], v[52:55]\n"
"buffer_load_dwordx4 v[152:155], v236, s[32:35], s20 offen sc1\n"
"v_mfma_f32_16x16x32_bf16 v[52:55], v[100:103], a[196:199], v[52:55]\n"
"s_waitcnt vmcnt(8)\n"
"s_add_u32 s21, s21, 1\n"
"v_and_b32 v88, s73, v88\n"
"v_mfma_f32_16x16x32_bf16 v[52:55], v[96:99], a[192:195], v[52:55]\n"
"s_barrier\n"
"s_mov_b32 s52, 40960\n"
"buffer_store_dword v241, v248, s[84:87], 0 offen sc1\n"
"s_waitcnt lgkmcnt(0)\n"
"v_mfma_f32_16x16x32_bf16 v[8:11], v[180:183], a[96:99], v[8:11]\n"
"s_mov_b32 s53, 81920\n"
"ds_read_b64_tr_b16 v[98:99], v231 offset:2048\n"
"v_add_f32 v42, v42, v205\n"
"v_mfma_f32_16x16x32_bf16 v[8:11], v[184:187], a[100:103], v[8:11]\n"
"ds_read_b64_tr_b16 v[112:113], v231 offset:16384\n"
"v_add_f32 v31, v31, v87\n"
"v_mfma_f32_16x16x32_bf16 v[8:11], v[188:191], a[104:107], v[8:11]\n"
"v_add_f32 v29, v29, v86\n"
"v_and_b32 v161, 16, v242\n"
"v_mfma_f32_16x16x32_bf16 v[8:11], v[192:195], a[108:111], v[8:11]\n"
"ds_read_b64_tr_b16 v[118:119], v235 offset:18432\n"
"v_and_b32 v160, 0x3f80, v160\n"
"v_mfma_f32_16x16x32_bf16 v[8:11], v[208:211], a[144:147], v[8:11]\n"
"ds_read_b64_tr_b16 v[102:103], v235 offset:2048\n"
"v_add_f32 v27, v27, v85\n"
"v_mfma_f32_16x16x32_bf16 v[8:11], v[212:215], a[148:151], v[8:11]\n"
"ds_read_b64_tr_b16 v[106:107], v231 offset:10240\n"
"v_lshlrev_b32 v160, v161, v160\n"
"v_mfma_f32_16x16x32_bf16 v[8:11], v[216:219], a[152:155], v[8:11]\n"
"ds_read_b64_tr_b16 v[96:97], v231 offset:0\n"
"v_and_b32 v93, s73, v93\n"
"v_mfma_f32_16x16x32_bf16 v[8:11], v[220:223], a[156:159], v[8:11]\n"
"ds_read_b64_tr_b16 v[108:109], v235 offset:8192\n"
"v_add_f32 v41, v41, v92\n"
"v_mfma_f32_16x16x32_bf16 v[24:27], v[220:223], a[172:175], v[24:27]\n"
"v_bfe_i32 v161, v242, 5, 1\n"
"v_lshlrev_b32 v206, 16, v244\n"
"v_mfma_f32_16x16x32_bf16 v[24:27], v[216:219], a[168:171], v[24:27]\n"
"ds_read_b64_tr_b16 v[114:115], v231 offset:18432\n"
"v_mfma_f32_16x16x32_bf16 v[24:27], v[212:215], a[164:167], v[24:27]\n"
"ds_read_b64_tr_b16 v[104:105], v231 offset:8192\n"
"v_lshlrev_b32 v203, 16, v247\n"
"v_mfma_f32_16x16x32_bf16 v[24:27], v[208:211], a[160:163], v[24:27]\n"
"ds_read_b64_tr_b16 v[116:117], v235 offset:16384\n"
"v_add_f32 v48, v48, v206\n"
"v_mfma_f32_16x16x32_bf16 v[24:27], v[192:195], a[124:127], v[24:27]\n"
"ds_read_b64_tr_b16 v[110:111], v235 offset:10240\n"
"v_and_b32 v89, s73, v89\n"
"v_mfma_f32_16x16x32_bf16 v[24:27], v[188:191], a[120:123], v[24:27]\n"
"v_add_f32 v45, v45, v94\n"
"v_add_f32 v47, v47, v95\n"
"v_mfma_f32_16x16x32_bf16 v[24:27], v[184:187], a[116:119], v[24:27]\n"
"ds_read_b64_tr_b16 v[100:101], v235 offset:0\n"
"v_add_f32 v43, v43, v93\n"
"v_mfma_f32_16x16x32_bf16 v[24:27], v[180:183], a[112:115], v[24:27]\n"
"ds_read_b64_tr_b16 v[122:123], v231 offset:26624\n"
"v_add_f32 v46, v46, v207\n"
"v_mfma_f32_16x16x32_bf16 v[40:43], v[180:183], a[128:131], v[40:43]\n"
"v_add_f32 v33, v33, v88\n"
"v_and_b32 v244, s73, v244\n"
"v_mfma_f32_16x16x32_bf16 v[40:43], v[184:187], a[132:135], v[40:43]\n"
"ds_read_b64_tr_b16 v[120:121], v231 offset:24576\n"
"v_lshlrev_b32 v207, 16, v245\n"
"v_mfma_f32_16x16x32_bf16 v[40:43], v[188:191], a[136:139], v[40:43]\n"
"v_and_b32 v163, v160, v161\n"
"v_lshlrev_b32 v202, 16, v246\n"
"v_mfma_f32_16x16x32_bf16 v[40:43], v[192:195], a[140:143], v[40:43]\n"
"ds_read_b64_tr_b16 v[224:225], v235 offset:24576\n"
"v_and_b32 v246, s73, v246\n"
"v_mfma_f32_16x16x32_bf16 v[40:43], v[208:211], a[176:179], v[40:43]\n"
"v_add_f32 v52, v52, v202\n"
"v_and_b32 v247, s73, v247\n"
"v_mfma_f32_16x16x32_bf16 v[40:43], v[212:215], a[180:183], v[40:43]\n"
"ds_read_b64_tr_b16 v[226:227], v235 offset:26624\n"
"v_and_b32 v245, s73, v245\n"
"v_mfma_f32_16x16x32_bf16 v[40:43], v[216:219], a[184:187], v[40:43]\n"
"v_mov_b32 v161, 0\n"
"v_add_f32 v54, v54, v203\n"
"v_mfma_f32_16x16x32_bf16 v[40:43], v[220:223], a[188:191], v[40:43]\n"
"s_barrier\n"
"s_waitcnt lgkmcnt(3)\n"
"v_mfma_f32_16x16x32_bf16 v[12:15], v[96:99], a[96:99], v[12:15]\n"
"ds_read_b64_tr_b16 v[184:185], v232 offset:32768\n"
"v_add_f32 v49, v49, v244\n"
"v_mfma_f32_16x16x32_bf16 v[12:15], v[100:103], a[100:103], v[12:15]\n"
"ds_read_b64_tr_b16 v[216:217], v228 offset:57344\n"
"v_mfma_f32_16x16x32_bf16 v[12:15], v[104:107], a[104:107], v[12:15]\n"
"ds_read_b64_tr_b16 v[188:189], v228 offset:40960\n"
"v_add_f32 v35, v35, v89\n"
"v_mfma_f32_16x16x32_bf16 v[12:15], v[108:111], a[108:111], v[12:15]\n"
"ds_read_b64_tr_b16 v[190:191], v228 offset:43008\n"
"v_mfma_f32_16x16x32_bf16 v[12:15], v[112:115], a[144:147], v[12:15]\n"
"ds_read_b64_tr_b16 v[192:193], v232 offset:40960\n"
"v_add_f32 v53, v53, v246\n"
"v_mfma_f32_16x16x32_bf16 v[12:15], v[116:119], a[148:151], v[12:15]\n"
"s_waitcnt lgkmcnt(5)\n"
"ds_read_b64_tr_b16 v[212:213], v232 offset:49152\n"
"v_add_f32 v51, v51, v245\n"
"v_mfma_f32_16x16x32_bf16 v[12:15], v[120:123], a[152:155], v[12:15]\n"
"ds_read_b64_tr_b16 v[182:183], v228 offset:34816\n"
"v_xor_b32 v162, v160, v163\n"
"v_mfma_f32_16x16x32_bf16 v[12:15], v[224:227], a[156:159], v[12:15]\n"
"global_load_dword v200, v201, s[6:7] sc1\n"
"v_mfma_f32_16x16x32_bf16 v[28:31], v[224:227], a[172:175], v[28:31]\n"
"v_add_f32 v50, v50, v207\n"
"v_add_f32 v55, v55, v247\n"
"v_mfma_f32_16x16x32_bf16 v[28:31], v[120:123], a[168:171], v[28:31]\n"
"s_waitcnt vmcnt(9)\n"
"v_lshlrev_b32 v58, 16, v125\n"
"v_lshlrev_b32 v62, 16, v127\n"
"v_mfma_f32_16x16x32_bf16 v[28:31], v[116:119], a[164:167], v[28:31]\n"
"ds_read_b64_tr_b16 v[186:187], v232 offset:34816\n"
"ds_read_b64_tr_b16 v[214:215], v232 offset:51200\n"
"v_mfma_f32_16x16x32_bf16 v[28:31], v[112:115], a[160:163], v[28:31]\n"
"ds_read_b64_tr_b16 v[194:195], v232 offset:43008\n"
"v_and_b32 v61, s73, v126\n"
"v_mfma_f32_16x16x32_bf16 v[28:31], v[108:111], a[124:127], v[28:31]\n"
"s_waitcnt vmcnt(8)\n"
"ds_read_b64_tr_b16 v[210:211], v228 offset:51200\n"
"v_and_b32 v57, s73, v124\n"
"v_mfma_f32_16x16x32_bf16 v[28:31], v[104:107], a[120:123], v[28:31]\n"
"ds_read_b64_tr_b16 v[222:223], v232 offset:59392\n"
"v_lshlrev_b32 v56, 16, v124\n"
"v_mfma_f32_16x16x32_bf16 v[28:31], v[100:103], a[116:119], v[28:31]\n"
"ds_read_b64_tr_b16 v[180:181], v228 offset:32768\n"
"v_lshlrev_b32 v205, 16, v129\n"
"v_mfma_f32_16x16x32_bf16 v[28:31], v[96:99], a[112:115], v[28:31]\n"
"s_waitcnt vmcnt(7)\n"
"v_and_b32 v59, s73, v125\n"
"v_lshlrev_b32 v206, 16, v130\n"
"v_mfma_f32_16x16x32_bf16 v[44:47], v[96:99], a[128:131], v[44:47]\n"
"ds_read_b64_tr_b16 v[220:221], v232 offset:57344\n"
"v_lshlrev_b32 v204, 16, v128\n"
"v_mfma_f32_16x16x32_bf16 v[44:47], v[100:103], a[132:135], v[44:47]\n"
"ds_read_b64_tr_b16 v[208:209], v228 offset:49152\n"
"v_and_b32 v130, s73, v130\n"
"v_mfma_f32_16x16x32_bf16 v[44:47], v[104:107], a[136:139], v[44:47]\n"
"s_waitcnt vmcnt(6)\n"
"ds_read_b64_tr_b16 v[102:103], v233 offset:34816\n"
"v_add_f32 v58, v58, v205\n"
"v_mfma_f32_16x16x32_bf16 v[44:47], v[108:111], a[140:143], v[44:47]\n"
"v_lshlrev_b32 v207, 16, v131\n"
"v_add_f32 v62, v62, v207\n"
"v_mfma_f32_16x16x32_bf16 v[44:47], v[112:115], a[176:179], v[44:47]\n"
"ds_read_b64_tr_b16 v[218:219], v228 offset:59392\n"
"v_and_b32 v131, s73, v131\n"
"v_mfma_f32_16x16x32_bf16 v[44:47], v[116:119], a[180:183], v[44:47]\n"
"s_waitcnt vmcnt(5)\n"
"ds_read_b64_tr_b16 v[96:97], v229 offset:32768\n"
"v_lshlrev_b32 v205, 16, v135\n"
"v_mfma_f32_16x16x32_bf16 v[44:47], v[120:123], a[184:187], v[44:47]\n"
"v_lshlrev_b32 v202, 16, v132\n"
"v_lshlrev_b32 v207, 16, v137\n"
"v_mfma_f32_16x16x32_bf16 v[44:47], v[224:227], a[188:191], v[44:47]\n"
"s_waitcnt lgkmcnt(5)\n"
"ds_read_b64_tr_b16 v[120:121], v229 offset:57344\n"
"v_add_f32 v56, v56, v204\n"
"v_mfma_f32_16x16x32_bf16 v[0:3], v[180:183], a[48:51], v[0:3]\n"
"s_waitcnt vmcnt(4)\n"
"ds_read_b64_tr_b16 v[116:117], v233 offset:49152\n"
"v_add_f32 v61, v61, v130\n"
"v_mfma_f32_16x16x32_bf16 v[0:3], v[184:187], a[52:55], v[0:3]\n"
"v_lshlrev_b32 v60, 16, v126\n"
"v_lshlrev_b32 v204, 16, v134\n"
"v_mfma_f32_16x16x32_bf16 v[0:3], v[188:191], a[56:59], v[0:3]\n"
"ds_read_b64_tr_b16 v[110:111], v233 offset:43008\n"
"v_add_f32 v56, v56, v202\n"
"v_mfma_f32_16x16x32_bf16 v[0:3], v[192:195], a[60:63], v[0:3]\n"
"s_waitcnt lgkmcnt(5)\n"
"s_waitcnt vmcnt(3)\n"
"ds_read_b64_tr_b16 v[112:113], v229 offset:49152\n"
"v_and_b32 v63, s73, v127\n"
"v_mfma_f32_16x16x32_bf16 v[0:3], v[208:211], a[0:3], v[0:3]\n"
"ds_read_b64_tr_b16 v[224:225], v233 offset:57344\n"
"v_and_b32 v134, s73, v134\n"
"v_mfma_f32_16x16x32_bf16 v[0:3], v[212:215], a[4:7], v[0:3]\n"
"s_waitcnt lgkmcnt(5)\n"
"s_waitcnt vmcnt(2)\n"
"ds_read_b64_tr_b16 v[108:109], v233 offset:40960\n"
"v_and_b32 v129, s73, v129\n"
"v_mfma_f32_16x16x32_bf16 v[0:3], v[216:219], a[8:11], v[0:3]\n"
"v_add_f32 v60, v60, v206\n"
"v_add_f32 v60, v60, v204\n"
"v_mfma_f32_16x16x32_bf16 v[0:3], v[220:223], a[12:15], v[0:3]\n"
"ds_read_b64_tr_b16 v[98:99], v229 offset:34816\n"
"ds_read_b64_tr_b16 v[122:123], v229 offset:59392\n"
"v_mfma_f32_16x16x32_bf16 v[16:19], v[220:223], a[28:31], v[16:19]\n"
"v_mov_b32 v165, 0\n"
"v_add_f32 v59, v59, v129\n"
"v_mfma_f32_16x16x32_bf16 v[16:19], v[216:219], a[24:27], v[16:19]\n"
"v_add_f32 v62, v62, v205\n"
"v_lshlrev_b32 v206, 16, v136\n"
"v_mfma_f32_16x16x32_bf16 v[16:19], v[212:215], a[20:23], v[16:19]\n"
"ds_read_b64_tr_b16 v[104:105], v229 offset:40960\n"
"ds_read_b64_tr_b16 v[226:227], v233 offset:59392\n"
"v_mfma_f32_16x16x32_bf16 v[16:19], v[208:211], a[16:19], v[16:19]\n"
"v_add_f32 v56, v56, v206\n"
"v_lshlrev_b32 v202, 16, v138\n"
"v_mfma_f32_16x16x32_bf16 v[16:19], v[192:195], a[76:79], v[16:19]\n"
"s_waitcnt vmcnt(0)\n"
"ds_read_b64_tr_b16 v[118:119], v233 offset:51200\n"
"v_subrev_f32 v2, s22, v2\n"
"v_mfma_f32_16x16x32_bf16 v[16:19], v[188:191], a[72:75], v[16:19]\n"
"v_subrev_u32 v200, s21, v200\n"
"ds_read_b64_tr_b16 v[114:115], v229 offset:51200\n"
"v_mfma_f32_16x16x32_bf16 v[16:19], v[184:187], a[68:71], v[16:19]\n"
"v_cmp_lt_u32 vcc, s69, v200\n"
"ds_read_b64_tr_b16 v[100:101], v233 offset:32768\n"
"v_mfma_f32_16x16x32_bf16 v[16:19], v[180:183], a[64:67], v[16:19]\n"
"v_lshlrev_b32 v206, 16, v142\n"
"s_cbranch_vccnz LGR_%=\nLGRK_%=:\n"
"v_mfma_f32_16x16x32_bf16 v[32:35], v[180:183], a[80:83], v[32:35]\n"
"v_subrev_f32 v3, s22, v3\n"
"s_cmp_eq_u32 s93, 0\ns_cbranch_scc1 LEP_%=\nLEPK_%=:\n"
"v_mfma_f32_16x16x32_bf16 v[32:35], v[184:187], a[84:87], v[32:35]\n"
"s_bfe_u32 s20, s89, 65536\ns_lshl_b32 s20, s20, 13\ns_add_u32 s20, s20, 65536\n"
"ds_read_b64_tr_b16 v[106:107], v229 offset:43008\n"
"v_and_b32 v128, s73, v128\n"
"v_mfma_f32_16x16x32_bf16 v[32:35], v[188:191], a[88:91], v[32:35]\n"
"buffer_load_dwordx4 v[72:75], v236, s[32:35], s20 offen sc1\n"
"v_mfma_f32_16x16x32_bf16 v[32:35], v[192:195], a[92:95], v[32:35]\n"
"v_lshlrev_b32 v203, 16, v133\n"
"v_lshlrev_b32 v205, 16, v141\n"
"v_mfma_f32_16x16x32_bf16 v[32:35], v[208:211], a[32:35], v[32:35]\n"
"s_bfe_u32 s20, s89, 65540\ns_lshl_b32 s20, s20, 13\ns_add_u32 s20, s20, 1114112\n"
"v_lshlrev_b32 v204, 16, v140\n"
"v_add_f32 v56, v56, v204\n"
"v_mfma_f32_16x16x32_bf16 v[32:35], v[212:215], a[36:39], v[32:35]\n"
"buffer_load_dwordx4 v[76:79], v236, s[32:35], s20 offen sc1\n"
"v_mfma_f32_16x16x32_bf16 v[32:35], v[216:219], a[40:43], v[32:35]\n"
"v_and_b32 v136, s73, v136\n"
"v_subrev_f32 v17, s22, v17\n"
"v_mfma_f32_16x16x32_bf16 v[32:35], v[220:223], a[44:47], v[32:35]\n"
"s_bfe_u32 s20, s89, 65544\ns_lshl_b32 s20, s20, 13\ns_add_u32 s20, s20, 2162688\n"
"v_add_f32 v61, v61, v134\n"
"v_and_b32 v141, s73, v141\n"
"v_mfma_f32_16x16x32_bf16 v[48:51], v[220:223], a[228:231], v[48:51]\n"
"buffer_load_dwordx4 v[80:83], v236, s[32:35], s20 offen sc1\n"
"v_mfma_f32_16x16x32_bf16 v[48:51], v[216:219], a[224:227], v[48:51]\n"
"v_add_f32 v58, v58, v203\n"
"v_subrev_f32 v19, s22, v19\n"
"v_mfma_f32_16x16x32_bf16 v[48:51], v[212:215], a[212:215], v[48:51]\n"
"s_bfe_u32 s20, s89, 65548\ns_lshl_b32 s20, s20, 13\ns_add_u32 s20, s20, 3211264\n"
"v_and_b32 v140, s73, v140\n"
"v_lshlrev_b32 v204, 16, v146\n"
"v_mfma_f32_16x16x32_bf16 v[48:51], v[208:211], a[208:211], v[48:51]\n"
"buffer_load_dwordx4 v[84:87], v236, s[32:35], s20 offen sc1\n"
"v_mfma_f32_16x16x32_bf16 v[48:51], v[192:195], a[236:239], v[48:51]\n"
"v_add_f32 v57, v57, v128\n"
"v_subrev_f32 v34, s22, v34\n"
"v_mfma_f32_16x16x32_bf16 v[48:51], v[188:191], a[232:235], v[48:51]\n"
"s_bfe_u32 s20, s89, 65552\ns_lshl_b32 s20, s20, 13\ns_add_u32 s20, s20, 4259840\n"
"ds_read_b64_tr_b16 v[214:215], v234 offset:51200\n"
"v_and_b32 v132, s73, v132\n"
"v_mfma_f32_16x16x32_bf16 v[48:51], v[184:187], a[220:223], v[48:51]\n"
"buffer_load_dwordx4 v[88:91], v236, s[32:35], s20 offen sc1\n"
"v_mfma_f32_16x16x32_bf16 v[48:51], v[180:183], a[216:219], v[48:51]\n"
"s_waitcnt lgkmcnt(1)\n"
"v_add_f32 v60, v60, v202\n"
"v_add_f32 v60, v60, v206\n"
"v_mfma_f32_16x16x32_bf16 v[4:7], v[96:99], a[48:51], v[4:7]\n"
"s_bfe_u32 s20, s89, 65556\ns_lshl_b32 s20, s20, 13\ns_add_u32 s20, s20, 5308416\n"
"v_lshlrev_b32 v206, 16, v148\n"
"v_subrev_f32 v35, s22, v35\n"
"v_mfma_f32_16x16x32_bf16 v[4:7], v[100:103], a[52:55], v[4:7]\n"
"buffer_load_dwordx4 v[92:95], v236, s[32:35], s20 offen sc1\n"
"v_mfma_f32_16x16x32_bf16 v[4:7], v[104:107], a[56:59], v[4:7]\n"
"v_add_f32 v60, v60, v204\n"
"v_subrev_f32 v0, s22, v0\n"
"v_mfma_f32_16x16x32_bf16 v[4:7], v[108:111], a[60:63], v[4:7]\n"
"s_bfe_u32 s20, s89, 65560\ns_lshl_b32 s20, s20, 13\ns_add_u32 s20, s20, 6356992\n"
"v_add_f32 v57, v57, v132\n"
"v_subrev_f32 v33, s22, v33\n"
"v_mfma_f32_16x16x32_bf16 v[4:7], v[112:115], a[0:3], v[4:7]\n"
"buffer_load_dwordx4 v[244:247], v236, s[32:35], s20 offen sc1\n"
"v_mfma_f32_16x16x32_bf16 v[4:7], v[116:119], a[4:7], v[4:7]\n"
"v_and_b32 v138, s73, v138\n"
"v_subrev_f32 v18, s22, v18\n"
"v_mfma_f32_16x16x32_bf16 v[4:7], v[120:123], a[8:11], v[4:7]\n"
"s_bfe_u32 s20, s89, 65564\ns_lshl_b32 s20, s20, 13\ns_add_u32 s20, s20, 7405568\n"
"v_and_b32 v135, s73, v135\n"
"v_subrev_f32 v49, s22, v49\n"
"v_mfma_f32_16x16x32_bf16 v[4:7], v[224:227], a[12:15], v[4:7]\n"
"buffer_load_dwordx4 v[156:159], v236, s[32:35], s20 offen sc1\n"
"v_add_f32 v58, v58, v207\n"
"v_lshlrev_b32 v203, 16, v139\n"
"v_add_f32 v58, v58, v205\n"
"v_and_b32 v142, s73, v142\n"
"v_lshlrev_b32 v202, 16, v144\n"
"v_add_f32 v56, v56, v202\n"
"v_add_f32 v56, v56, v206\n"
"v_subrev_f32 v1, s22, v1\n"
"v_mfma_f32_16x16x32_bf16 v[20:23], v[224:227], a[28:31], v[20:23]\n"
"v_add_f32 v63, v63, v131\n"
"v_lshlrev_b32 v204, 16, v152\n"
"v_mfma_f32_16x16x32_bf16 v[20:23], v[120:123], a[24:27], v[20:23]\n"
"v_and_b32 v133, s73, v133\n"
"v_add_f32 v59, v59, v133\n"
"v_mfma_f32_16x16x32_bf16 v[20:23], v[116:119], a[20:23], v[20:23]\n"
"ds_read_b64_tr_b16 v[218:219], v230 offset:59392\n"
"v_and_b32 v137, s73, v137\n"
"v_mfma_f32_16x16x32_bf16 v[20:23], v[112:115], a[16:19], v[20:23]\n"
"v_and_b32 v148, s73, v148\n"
"v_subrev_f32 v16, s22, v16\n"
"v_mfma_f32_16x16x32_bf16 v[20:23], v[108:111], a[76:79], v[20:23]\n"
"v_add_f32 v62, v62, v203\n"
"v_subrev_f32 v6, s22, v6\n"
"v_mfma_f32_16x16x32_bf16 v[20:23], v[104:107], a[72:75], v[20:23]\n"
"v_add_f32 v57, v57, v136\n"
"v_lshlrev_b32 v205, 16, v147\n"
"v_mfma_f32_16x16x32_bf16 v[20:23], v[100:103], a[68:71], v[20:23]\n"
"ds_read_b64_tr_b16 v[194:195], v234 offset:43008\n"
"v_subrev_f32 v32, s22, v32\n"
"v_mfma_f32_16x16x32_bf16 v[20:23], v[96:99], a[64:67], v[20:23]\n"
"ds_read_b64_tr_b16 v[184:185], v234 offset:32768\n"
"v_add_f32 v59, v59, v137\n"
"v_and_b32 v139, s73, v139\n"
"v_add_f32 v59, v59, v141\n"
"v_lshlrev_b32 v207, 16, v143\n"
"v_lshlrev_b32 v203, 16, v145\n"
"v_and_b32 v145, s73, v145\n"
"v_add_f32 v58, v58, v203\n"
"v_add_f32 v59, v59, v145\n"
"v_and_b32 v147, s73, v147\n"
"v_lshlrev_b32 v203, 16, v151\n"
"v_subrev_f32 v4, s22, v4\n"
"v_subrev_f32 v5, s22, v5\n"
"v_subrev_f32 v7, s22, v7\n"
"v_cvt_pk_bf16_f32 v0, v0, v1\n"
"v_cvt_pk_bf16_f32 v1, v2, v3\n"
"v_cvt_pk_bf16_f32 v3, v6, v7\n"
"v_mfma_f32_16x16x32_bf16 v[36:39], v[96:99], a[80:83], v[36:39]\n"
"v_add_f32 v56, v56, v204\n"
"v_cvt_pk_bf16_f32 v2, v4, v5\n"
"v_mfma_f32_16x16x32_bf16 v[36:39], v[100:103], a[84:87], v[36:39]\n"
"ds_read_b64_tr_b16 v[192:193], v234 offset:40960\n"
"v_add_f32 v61, v61, v138\n"
"v_mfma_f32_16x16x32_bf16 v[36:39], v[104:107], a[88:91], v[36:39]\n"
"v_add_f32 v57, v57, v140\n"
"v_subrev_f32 v23, s22, v23\n"
"v_mfma_f32_16x16x32_bf16 v[36:39], v[108:111], a[92:95], v[36:39]\n"
"ds_read_b64_tr_b16 v[180:181], v230 offset:32768\n"
"v_and_b32 v144, s73, v144\n"
"v_mfma_f32_16x16x32_bf16 v[36:39], v[112:115], a[32:35], v[36:39]\n"
"ds_read_b64_tr_b16 v[210:211], v230 offset:51200\n"
"v_add_f32 v62, v62, v207\n"
"v_add_f32 v62, v62, v205\n"
"v_lshlrev_b32 v207, 16, v149\n"
"v_add_f32 v58, v58, v207\n"
"v_lshlrev_b32 v202, 16, v150\n"
"v_permlane16_swap_b32 v0, v2\n"
"v_permlane16_swap_b32 v1, v3\n"
"v_subrev_f32 v20, s22, v20\n"
"v_cvt_pk_bf16_f32 v16, v16, v17\n"
"v_mfma_f32_16x16x32_bf16 v[36:39], v[116:119], a[36:39], v[36:39]\n"
"ds_read_b64_tr_b16 v[186:187], v234 offset:34816\n"
"v_add_f32 v63, v63, v135\n"
"v_mfma_f32_16x16x32_bf16 v[36:39], v[120:123], a[40:43], v[36:39]\n"
"ds_read_b64_tr_b16 v[188:189], v230 offset:40960\n"
"buffer_store_dwordx4 v[0:3], v238, s[36:39], 0 offen sc0 sc1\n"
"v_and_b32 v143, s73, v143\n"
"v_and_b32 v146, s73, v146\n"
"v_and_b32 v149, s73, v149\n"
"v_lshlrev_b32 v205, 16, v153\n"
"v_and_b32 v153, s73, v153\n"
"v_lshlrev_b32 v206, 16, v154\n"
"v_lshlrev_b32 v207, 16, v155\n"
"v_subrev_f32 v21, s22, v21\n"
"v_subrev_f32 v22, s22, v22\n"
"v_cvt_pk_bf16_f32 v17, v18, v19\n"
"v_cvt_pk_bf16_f32 v18, v20, v21\n"
"v_cvt_pk_bf16_f32 v32, v32, v33\n"
"v_mfma_f32_16x16x32_bf16 v[36:39], v[224:227], a[44:47], v[36:39]\n"
"ds_read_b64_tr_b16 v[182:183], v230 offset:34816\n"
"v_add_f32 v63, v63, v139\n"
"v_add_f32 v61, v61, v142\n"
"v_add_f32 v57, v57, v144\n"
"v_add_f32 v57, v57, v148\n"
"v_add_f32 v59, v59, v149\n"
"v_cvt_pk_bf16_f32 v19, v22, v23\n"
"v_permlane16_swap_b32 v16, v18\n"
"v_cvt_pk_bf16_f32 v33, v34, v35\n"
"v_mfma_f32_16x16x32_bf16 v[52:55], v[224:227], a[228:231], v[52:55]\n"
"ds_read_b64_tr_b16 v[190:191], v230 offset:43008\n"
"v_add_f32 v59, v59, v153\n"
"v_mfma_f32_16x16x32_bf16 v[52:55], v[120:123], a[224:227], v[52:55]\n"
"v_permlane16_swap_b32 v17, v19\n"
"v_mfma_f32_16x16x32_bf16 v[52:55], v[116:119], a[212:215], v[52:55]\n"
"ds_read_b64_tr_b16 v[208:209], v230 offset:49152\n"
"v_subrev_f32 v36, s22, v36\n"
"v_mfma_f32_16x16x32_bf16 v[52:55], v[112:115], a[208:211], v[52:55]\n"
"ds_read_b64_tr_b16 v[222:223], v234 offset:59392\n"
"buffer_store_dwordx4 v[16:19], v238, s[36:39], s52 offen sc0 sc1\n"
"v_add_f32 v63, v63, v143\n"
"v_add_f32 v60, v60, v202\n"
"v_add_f32 v62, v62, v203\n"
"v_and_b32 v152, s73, v152\n"
"v_subrev_f32 v38, s22, v38\n"
"v_subrev_f32 v39, s22, v39\n"
"v_mfma_f32_16x16x32_bf16 v[52:55], v[108:111], a[236:239], v[52:55]\n"
"v_and_b32 v155, s73, v155\n"
"v_cvt_pk_bf16_f32 v35, v38, v39\n"
"v_mfma_f32_16x16x32_bf16 v[52:55], v[104:107], a[232:235], v[52:55]\n"
"ds_read_b64_tr_b16 v[216:217], v230 offset:57344\n"
"v_subrev_f32 v37, s22, v37\n"
"v_mfma_f32_16x16x32_bf16 v[52:55], v[100:103], a[220:223], v[52:55]\n"
"v_and_b32 v150, s73, v150\n"
"v_and_b32 v154, s73, v154\n"
"v_mfma_f32_16x16x32_bf16 v[52:55], v[96:99], a[216:219], v[52:55]\n"
"s_waitcnt lgkmcnt(4)\n"
"ds_read_b64_tr_b16 v[212:213], v234 offset:49152\n"
"ds_read_b64_tr_b16 v[220:221], v234 offset:57344\n"
"v_add_f32 v63, v63, v147\n"
"v_and_b32 v151, s73, v151\n"
"v_add_f32 v63, v63, v151\n"
"v_add_f32 v57, v57, v152\n"
"v_add_f32 v58, v58, v205\n"
"v_add_f32 v60, v60, v206\n"
"v_add_f32 v62, v62, v207\n"
"v_cvt_pk_bf16_f32 v34, v36, v37\n"
"v_permlane16_swap_b32 v33, v35\n"
"v_mfma_f32_16x16x32_bf16 v[8:11], v[180:183], a[48:51], v[8:11]\n"
"v_cvt_pk_bf16_f32 v56, v56, v57\n"
"v_cvt_pk_bf16_f32 v57, v58, v59\n"
"v_mfma_f32_16x16x32_bf16 v[8:11], v[184:187], a[52:55], v[8:11]\n"
"s_waitcnt lgkmcnt(5)\n"
"v_permlane16_swap_b32 v32, v34\n"
"v_mfma_f32_16x16x32_bf16 v[8:11], v[188:191], a[56:59], v[8:11]\n"
"buffer_store_dwordx4 v[32:35], v238, s[36:39], s53 offen sc0 sc1\n"
"ds_read_b64_tr_b16 v[120:121], v231 offset:57344\n"
"v_add_f32 v61, v61, v146\n"
"v_add_f32 v61, v61, v150\n"
"v_add_f32 v61, v61, v154\n"
"v_add_f32 v63, v63, v155\n"
"v_cvt_pk_bf16_f32 v58, v60, v61\n"
"v_cvt_pk_bf16_f32 v59, v62, v63\n"
"v_mfma_f32_16x16x32_bf16 v[8:11], v[192:195], a[60:63], v[8:11]\n"
"s_waitcnt lgkmcnt(4)\n"
"v_permlane16_swap_b32 v57, v59\n"
"v_mfma_f32_16x16x32_bf16 v[8:11], v[208:211], a[0:3], v[8:11]\n"
"s_waitcnt lgkmcnt(2)\n"
"v_permlane16_swap_b32 v56, v58\n"
"v_mfma_f32_16x16x32_bf16 v[8:11], v[212:215], a[4:7], v[8:11]\n"
"buffer_store_dwordx4 v[56:59], v253, s[36:39], 0 offen sc0 sc1\n"
"v_subrev_f32 v48, s22, v48\n"
"v_subrev_f32 v50, s22, v50\n"
"v_subrev_f32 v51, s22, v51\n"
"v_mfma_f32_16x16x32_bf16 v[8:11], v[216:219], a[8:11], v[8:11]\n"
"s_waitcnt lgkmcnt(1)\n"
"ds_read_b64_tr_b16 v[112:113], v231 offset:49152\n"
"v_subrev_f32 v54, s22, v54\n"
"v_mfma_f32_16x16x32_bf16 v[8:11], v[220:223], a[12:15], v[8:11]\n"
"ds_read_b64_tr_b16 v[100:101], v235 offset:32768\n"
"v_subrev_f32 v52, s22, v52\n"
"v_mfma_f32_16x16x32_bf16 v[24:27], v[220:223], a[28:31], v[24:27]\n"
"ds_read_b64_tr_b16 v[102:103], v235 offset:34816\n"
"v_subrev_f32 v53, s22, v53\n"
"v_mfma_f32_16x16x32_bf16 v[24:27], v[216:219], a[24:27], v[24:27]\n"
"v_cvt_pk_bf16_f32 v48, v48, v49\n"
"v_cvt_pk_bf16_f32 v49, v50, v51\n"
"v_mfma_f32_16x16x32_bf16 v[24:27], v[212:215], a[20:23], v[24:27]\n"
"ds_read_b64_tr_b16 v[96:97], v231 offset:32768\n"
"v_cvt_pk_bf16_f32 v50, v52, v53\n"
"v_mfma_f32_16x16x32_bf16 v[24:27], v[208:211], a[16:19], v[24:27]\n"
"s_nop 0\n"
"v_permlane16_swap_b32 v48, v50\n"
"v_mfma_f32_16x16x32_bf16 v[24:27], v[192:195], a[76:79], v[24:27]\n"
"ds_read_b64_tr_b16 v[116:117], v235 offset:49152\n"
"v_subrev_f32 v55, s22, v55\n"
"v_mfma_f32_16x16x32_bf16 v[24:27], v[188:191], a[72:75], v[24:27]\n"
"ds_read_b64_tr_b16 v[98:99], v231 offset:34816\n"
"v_cvt_pk_bf16_f32 v51, v54, v55\n"
"v_mfma_f32_16x16x32_bf16 v[24:27], v[184:187], a[68:71], v[24:27]\n"
"s_nop 0\n"
"v_permlane16_swap_b32 v49, v51\n"
"v_mfma_f32_16x16x32_bf16 v[24:27], v[180:183], a[64:67], v[24:27]\n"
"buffer_store_dwordx4 v[48:51], v252, s[36:39], 0 offen sc0 sc1\n"
"ds_read_b64_tr_b16 v[104:105], v231 offset:40960\n"
"v_mfma_f32_16x16x32_bf16 v[40:43], v[180:183], a[80:83], v[40:43]\n"
"ds_read_b64_tr_b16 v[106:107], v231 offset:43008\n"
"ds_read_b64_tr_b16 v[108:109], v235 offset:40960\n"
"v_mfma_f32_16x16x32_bf16 v[40:43], v[184:187], a[84:87], v[40:43]\n"
"ds_read_b64_tr_b16 v[110:111], v235 offset:43008\n"
"v_mfma_f32_16x16x32_bf16 v[40:43], v[188:191], a[88:91], v[40:43]\n"
"ds_read_b64_tr_b16 v[114:115], v231 offset:51200\n"
"v_subrev_f32 v10, s22, v10\n"
"v_mfma_f32_16x16x32_bf16 v[40:43], v[192:195], a[92:95], v[40:43]\n"
"ds_read_b64_tr_b16 v[118:119], v235 offset:51200\n"
"v_mfma_f32_16x16x32_bf16 v[40:43], v[208:211], a[32:35], v[40:43]\n"
"ds_read_b64_tr_b16 v[122:123], v231 offset:59392\n"
"v_subrev_f32 v24, s22, v24\n"
"v_mfma_f32_16x16x32_bf16 v[40:43], v[212:215], a[36:39], v[40:43]\n"
"ds_read_b64_tr_b16 v[224:225], v235 offset:57344\n"
"ds_read_b64_tr_b16 v[226:227], v235 offset:59392\n"
"v_mfma_f32_16x16x32_bf16 v[40:43], v[216:219], a[40:43], v[40:43]\n"
"v_mfma_f32_16x16x32_bf16 v[40:43], v[220:223], a[44:47], v[40:43]\n"
"s_waitcnt lgkmcnt(8)\n"
"v_mov_b32 v160, 0\n"
"v_subrev_f32 v11, s22, v11\n"
"v_mfma_f32_16x16x32_bf16 v[12:15], v[96:99], a[48:51], v[12:15]\n"
"v_subrev_f32 v8, s22, v8\n"
"v_subrev_f32 v9, s22, v9\n"
"v_mfma_f32_16x16x32_bf16 v[12:15], v[100:103], a[52:55], v[12:15]\n"
"s_waitcnt lgkmcnt(5)\n"
"v_cvt_pk_bf16_f32 v8, v8, v9\n"
"v_mfma_f32_16x16x32_bf16 v[12:15], v[104:107], a[56:59], v[12:15]\n"
"v_subrev_f32 v27, s22, v27\n"
"v_mfma_f32_16x16x32_bf16 v[12:15], v[108:111], a[60:63], v[12:15]\n"
"s_waitcnt lgkmcnt(3)\n"
"v_mfma_f32_16x16x32_bf16 v[12:15], v[112:115], a[0:3], v[12:15]\n"
"v_mfma_f32_16x16x32_bf16 v[12:15], v[116:119], a[4:7], v[12:15]\n"
"s_waitcnt lgkmcnt(0)\n"
"v_mfma_f32_16x16x32_bf16 v[12:15], v[120:123], a[8:11], v[12:15]\n"
"v_subrev_f32 v40, s22, v40\n"
"v_mfma_f32_16x16x32_bf16 v[12:15], v[224:227], a[12:15], v[12:15]\n"
"s_waitcnt vmcnt(5)\n"
"v_mfma_f32_16x16x32_bf16 v[64:67], v[162:165], v[72:75], 0\n"
"v_mfma_f32_16x16x32_bf16 v[64:67], v[162:165], v[76:79], v[64:67]\n"
"v_cvt_pk_bf16_f32 v9, v10, v11\n"
"v_mfma_f32_16x16x32_bf16 v[64:67], v[162:165], v[80:83], v[64:67]\n"
"s_nop 2\n"
"v_subrev_f32 v12, s22, v12\n"
"v_mfma_f32_16x16x32_bf16 v[64:67], v[162:165], v[84:87], v[64:67]\n"
"v_subrev_f32 v25, s22, v25\n"
"v_mfma_f32_16x16x32_bf16 v[64:67], v[162:165], v[88:91], v[64:67]\n"
"v_subrev_f32 v13, s22, v13\n"
"v_mfma_f32_16x16x32_bf16 v[64:67], v[162:165], v[92:95], v[64:67]\n"
"v_subrev_f32 v15, s22, v15\n"
"v_mfma_f32_16x16x32_bf16 v[64:67], v[162:165], v[244:247], v[64:67]\n"
"v_subrev_f32 v14, s22, v14\n"
"v_mfma_f32_16x16x32_bf16 v[64:67], v[162:165], v[156:159], v[64:67]\n"
"v_subrev_f32 v42, s22, v42\n"
"v_mfma_f32_16x16x32_bf16 v[68:71], v[160:163], v[72:75], 0\n"
"v_cvt_pk_bf16_f32 v11, v14, v15\n"
"v_subrev_f32 v41, s22, v41\n"
"v_mfma_f32_16x16x32_bf16 v[68:71], v[160:163], v[76:79], v[68:71]\n"
"v_cvt_pk_bf16_f32 v10, v12, v13\n"
"v_cvt_pk_bf16_f32 v24, v24, v25\n"
"v_mfma_f32_16x16x32_bf16 v[68:71], v[160:163], v[80:83], v[68:71]\n"
"v_mfma_f32_16x16x32_bf16 v[68:71], v[160:163], v[84:87], v[68:71]\n"
"v_permlane16_swap_b32 v9, v11\n"
"v_mfma_f32_16x16x32_bf16 v[68:71], v[160:163], v[88:91], v[68:71]\n"
"v_mfma_f32_16x16x32_bf16 v[68:71], v[160:163], v[92:95], v[68:71]\n"
"v_cvt_pk_bf16_f32 v64, v64, v65\n"
"v_cvt_pk_bf16_f32 v40, v40, v41\n"
"v_mfma_f32_16x16x32_bf16 v[68:71], v[160:163], v[244:247], v[68:71]\n"
"v_permlane16_swap_b32 v8, v10\n"
"v_mfma_f32_16x16x32_bf16 v[68:71], v[160:163], v[156:159], v[68:71]\n"
"buffer_store_dwordx4 v[8:11], v239, s[36:39], 0 offen sc0 sc1\n"
"v_mfma_f32_16x16x32_bf16 v[28:31], v[224:227], a[28:31], v[28:31]\n"
"v_mfma_f32_16x16x32_bf16 v[28:31], v[120:123], a[24:27], v[28:31]\n"
"v_cvt_pk_bf16_f32 v65, v66, v67\n"
"v_subrev_f32 v43, s22, v43\n"
"v_mfma_f32_16x16x32_bf16 v[28:31], v[116:119], a[20:23], v[28:31]\n"
"v_mfma_f32_16x16x32_bf16 v[28:31], v[112:115], a[16:19], v[28:31]\n"
"v_mfma_f32_16x16x32_bf16 v[28:31], v[108:111], a[76:79], v[28:31]\n"
"v_cvt_pk_bf16_f32 v66, v68, v69\n"
"v_cvt_pk_bf16_f32 v67, v70, v71\n"
"v_mfma_f32_16x16x32_bf16 v[28:31], v[104:107], a[72:75], v[28:31]\n"
"v_permlane16_swap_b32 v64, v66\n"
"v_mfma_f32_16x16x32_bf16 v[28:31], v[100:103], a[68:71], v[28:31]\n"
"v_permlane16_swap_b32 v65, v67\n"
"v_mfma_f32_16x16x32_bf16 v[28:31], v[96:99], a[64:67], v[28:31]\n"
"buffer_store_dwordx4 v[64:67], v253, s[36:39], 0 offen offset:128 sc0 sc1\n"
"v_cvt_pk_bf16_f32 v41, v42, v43\n"
"v_mfma_f32_16x16x32_bf16 v[44:47], v[96:99], a[80:83], v[44:47]\n"
"v_subrev_f32 v26, s22, v26\n"
"v_cvt_pk_bf16_f32 v25, v26, v27\n"
"v_mfma_f32_16x16x32_bf16 v[44:47], v[100:103], a[84:87], v[44:47]\n"
"v_mfma_f32_16x16x32_bf16 v[44:47], v[104:107], a[88:91], v[44:47]\n"
"s_nop 0\n"
"v_subrev_f32 v30, s22, v30\n"
"v_subrev_f32 v31, s22, v31\n"
"v_mfma_f32_16x16x32_bf16 v[44:47], v[108:111], a[92:95], v[44:47]\n"
"v_subrev_f32 v28, s22, v28\n"
"v_subrev_f32 v29, s22, v29\n"
"v_mfma_f32_16x16x32_bf16 v[44:47], v[112:115], a[32:35], v[44:47]\n"
"v_cvt_pk_bf16_f32 v26, v28, v29\n"
"v_cvt_pk_bf16_f32 v27, v30, v31\n"
"v_mfma_f32_16x16x32_bf16 v[44:47], v[116:119], a[36:39], v[44:47]\n"
"s_nop 0\n"
"v_permlane16_swap_b32 v25, v27\n"
"v_mfma_f32_16x16x32_bf16 v[44:47], v[120:123], a[40:43], v[44:47]\n"
"v_permlane16_swap_b32 v24, v26\n"
"v_mfma_f32_16x16x32_bf16 v[44:47], v[224:227], a[44:47], v[44:47]\n"
"buffer_store_dwordx4 v[24:27], v239, s[36:39], s52 offen sc0 sc1\n"
"s_nop 6\n"
"v_subrev_f32 v44, s22, v44\n"
"v_subrev_f32 v45, s22, v45\n"
"v_subrev_f32 v46, s22, v46\n"
"v_subrev_f32 v47, s22, v47\n"
"v_cvt_pk_bf16_f32 v42, v44, v45\n"
"v_cvt_pk_bf16_f32 v43, v46, v47\n"
"s_nop 0\n"
"v_permlane16_swap_b32 v40, v42\n"
"v_permlane16_swap_b32 v41, v43\n"
"buffer_store_dwordx4 v[40:43], v239, s[36:39], s53 offen sc0 sc1\n"
"s_branch LFIN_%=\n"
"LSW_%=:\n"
"s_add_u32 s16, s8, s95\n"
"s_addc_u32 s17, s9, 0\n"
"s_sub_u32 s18, 655360, s95\n"
"s_add_u32 s20, s8, s100\n"
"s_addc_u32 s21, s9, 0\n"
"s_sub_u32 s22, 655360, s100\n"
"s_mov_b32 s56, 0\n"
"s_mov_b32 s55, 0\n"
"s_mov_b32 s58, 0\n"
"s_mov_b32 s59, 163840\n"
"s_mov_b32 s52, 9216\n"
"s_mov_b32 s53, 18432\n"
"s_mov_b32 s54, 27648\n"
"v_lshrrev_b32 v206, 7, v242\n"
"v_mul_u32_u24 v206, 1280, v206\n"
"v_and_b32 v240, 0x7f, v240\n"
"v_add_u32 v240, v206, v240\n"
"buffer_load_dword v252, v240, s[16:19], 0 offen\n"
"s_branch LSWK_%=\n"
"LGP_%=:\n"
"s_mov_b32 s80, 0x40000\n"
"LGPa_%=:\n"
"s_sleep 1\n"
"global_load_dword v200, v201, s[6:7] sc1\n"
"s_sub_u32 s80, s80, 1\n"
"s_cmp_eq_u32 s80, 0\n"
"s_waitcnt vmcnt(0)\n"
"s_cbranch_scc1 LGPK_%=\n"
"v_subrev_u32 v200, s21, v200\n"
"v_cmp_lt_u32 vcc, s69, v200\n"
"s_nop 4\n"
"s_cbranch_vccnz LGPa_%=\n"
"s_branch LGPK_%=\n"
"LGQ_%=:\n"
"s_mov_b32 s80, 0x40000\n"
"LGQa_%=:\n"
"s_sleep 1\n"
"global_load_dword v200, v201, s[6:7] sc1\n"
"s_sub_u32 s80, s80, 1\n"
"s_cmp_eq_u32 s80, 0\n"
"s_waitcnt vmcnt(0)\n"
"s_cbranch_scc1 LGQK_%=\n"
"v_subrev_u32 v200, s21, v200\n"
"v_cmp_lt_u32 vcc, s69, v200\n"
"s_nop 4\n"
"s_cbranch_vccnz LGQa_%=\n"
"s_branch LGQK_%=\n"
"LGR_%=:\n"
"s_mov_b32 s80, 0x40000\n"
"LGRa_%=:\n"
"s_sleep 1\n"
"global_load_dword v200, v201, s[6:7] sc1\n"
"s_sub_u32 s80, s80, 1\n"
"s_cmp_eq_u32 s80, 0\n"
"s_waitcnt vmcnt(0)\n"
"s_cbranch_scc1 LGRK_%=\n"
"v_subrev_u32 v200, s21, v200\n"
"v_cmp_lt_u32 vcc, s69, v200\n"
"s_nop 4\n"
"s_cbranch_vccnz LGRa_%=\n"
"s_branch LGRK_%=\n"
"LGV_%=:\n"
"s_mov_b32 s80, 0x40000\n"
"LGVa_%=:\n"
"s_sleep 1\n"
"global_load_dword v200, v201, s[6:7] sc1\n"
"s_sub_u32 s80, s80, 1\n"
"s_cmp_eq_u32 s80, 0\n"
"s_waitcnt vmcnt(0)\n"
"s_cbranch_scc1 LGVK_%=\n"
"v_subrev_u32 v200, s21, v200\n"
"v_cmp_lt_u32 vcc, s69, v200\n"
"s_nop 4\n"
"s_cbranch_vccnz LGVa_%=\n"
"s_branch LGVK_%=\n"
"LEP_%=:\n"
"s_add_u32 s92, s92, 1\n"
"v_mov_b32 v200, s92\n"
"v_mov_b32 v255, 0\n"
"s_mov_b64 exec, 1\n"
"global_store_dword v255, v200, s[6:7] offset:128 sc1\n"
"s_mov_b64 exec, -1\n"
"s_branch LEPK_%=\n"
"LPCE0_%=:\n"
"s_memrealtime s[80:81]\n"
"s_sub_u32 s82, s67, s66\n"
"s_cmp_gt_i32 s82, 0\n"
"s_cselect_b32 s83, s67, s66\n"
"s_waitcnt lgkmcnt(0)\n"
"s_mov_b32 s63, 0\n"
"s_mov_b32 s62, 122\n"
"s_sub_u32 s82, s83, s80\n"
"s_cmp_gt_i32 s82, 3\n"
"s_cbranch_scc0 LPEE0_%=\n"
"LPDE0_%=:\n"
"s_sleep 1\n"
"s_memrealtime s[80:81]\n"
"s_waitcnt lgkmcnt(0)\n"
"s_sub_u32 s82, s83, s80\n"
"s_cmp_gt_i32 s82, 3\n"
"s_cbranch_scc1 LPDE0_%=\n"
"LPEE0_%=:\n"
"s_add_u32 s66, s66, s72\n"
"s_add_u32 s66, s66, 4\n"
"s_add_u32 s67, s80, s72\n"
"s_sub_u32 s72, s72, 3\n"
"s_max_u32 s72, s72, s62\n"
"s_branch LPKE0_%=\n"
"LPCE1_%=:\n"
"s_memrealtime s[80:81]\n"
"s_sub_u32 s82, s67, s66\n"
"s_cmp_gt_i32 s82, 0\n"
"s_cselect_b32 s83, s67, s66\n"
"s_waitcnt lgkmcnt(0)\n"
"s_add_u32 s72, s62, 16\n"
"s_sub_u32 s82, s83, s80\n"
"s_cmp_gt_i32 s82, 3\n"
"s_cbranch_scc0 LPEE1_%=\n"
"LPDE1_%=:\n"
"s_sleep 1\n"
"s_memrealtime s[80:81]\n"
"s_waitcnt lgkmcnt(0)\n"
"s_sub_u32 s82, s83, s80\n"
"s_cmp_gt_i32 s82, 3\n"
"s_cbranch_scc1 LPDE1_%=\n"
"LPEE1_%=:\n"
"s_add_u32 s66, s66, s72\n"
"s_add_u32 s66, s66, 4\n"
"s_add_u32 s67, s80, s72\n"
"s_sub_u32 s72, s72, 3\n"
"s_max_u32 s72, s72, s62\n"
"s_branch LPKE1_%=\n"
"LPCT0_%=:\n"
"s_memrealtime s[80:81]\n"
"s_sub_u32 s82, s67, s66\n"
"s_cmp_gt_i32 s82, 0\n"
"s_cselect_b32 s83, s67, s66\n"
"s_waitcnt lgkmcnt(0)\n"
"s_sub_u32 s82, s83, s80\n"
"s_cmp_gt_i32 s82, 3\n"
"s_cbranch_scc0 LPET0_%=\n"
"LPDT0_%=:\n"
"s_sleep 1\n"
"s_memrealtime s[80:81]\n"
"s_waitcnt lgkmcnt(0)\n"
"s_sub_u32 s82, s83, s80\n"
"s_cmp_gt_i32 s82, 3\n"
"s_cbranch_scc1 LPDT0_%=\n"
"LPET0_%=:\n"
"s_add_u32 s66, s66, s72\n"
"s_add_u32 s66, s66, 4\n"
"s_add_u32 s67, s80, s72\n"
"s_sub_u32 s72, s72, 3\n"
"s_max_u32 s72, s72, s62\n"
"s_branch LPKT0_%=\n"
"LPCT1_%=:\n"
"s_memrealtime s[80:81]\n"
"s_sub_u32 s82, s67, s66\n"
"s_cmp_gt_i32 s82, 0\n"
"s_cselect_b32 s83, s67, s66\n"
"s_waitcnt lgkmcnt(0)\n"
"s_add_u32 s72, s62, 16\n"
"s_sub_u32 s82, s83, s80\n"
"s_cmp_gt_i32 s82, 3\n"
"s_cbranch_scc0 LPET1_%=\n"
"LPDT1_%=:\n"
"s_sleep 1\n"
"s_memrealtime s[80:81]\n"
"s_waitcnt lgkmcnt(0)\n"
"s_sub_u32 s82, s83, s80\n"
"s_cmp_gt_i32 s82, 3\n"
"s_cbranch_scc1 LPDT1_%=\n"
"LPET1_%=:\n"
"s_add_u32 s66, s66, s72\n"
"s_add_u32 s66, s66, 4\n"
"s_add_u32 s67, s80, s72\n"
"s_sub_u32 s72, s72, 3\n"
"s_max_u32 s72, s72, s62\n"
"s_branch LPKT1_%=\n"
"LPCL0_%=:\n"
"s_memrealtime s[80:81]\n"
"s_sub_u32 s82, s67, s66\n"
"s_cmp_gt_i32 s82, 0\n"
"s_cselect_b32 s83, s67, s66\n"
"s_waitcnt lgkmcnt(0)\n"
"s_sub_u32 s82, s80, s67\n"
"s_add_u32 s63, s63, 1\n"
"s_min_i32 s81, s63, 0\n"
"s_cmp_gt_i32 s82, 20\n"
"s_cselect_b32 s63, s63, s81\n"
"s_cmp_ge_i32 s63, 2\n"
"s_cbranch_scc0 LPRL0_%=\n"
"s_mov_b32 s63, -9\n"
"s_add_u32 s62, s62, 14\n"
"s_min_u32 s62, s62, 146\n"
"s_max_u32 s72, s72, s62\n"
"s_add_u32 s83, s80, 200\n"
"s_and_b32 s83, s83, -1\n"
"LPRL0_%=:\n"
"s_sub_u32 s82, s83, s80\n"
"s_cmp_gt_i32 s82, 3\n"
"s_cbranch_scc0 LPEL0_%=\n"
"LPDL0_%=:\n"
"s_sleep 1\n"
"s_memrealtime s[80:81]\n"
"s_waitcnt lgkmcnt(0)\n"
"s_sub_u32 s82, s83, s80\n"
"s_cmp_gt_i32 s82, 3\n"
"s_cbranch_scc1 LPDL0_%=\n"
"LPEL0_%=:\n"
"s_add_u32 s66, s66, s72\n"
"s_add_u32 s66, s66, 4\n"
"s_add_u32 s67, s80, s72\n"
"s_sub_u32 s72, s72, 3\n"
"s_max_u32 s72, s72, s62\n"
"s_branch LPKL0_%=\n"
"LPCL1_%=:\n"
"s_memrealtime s[80:81]\n"
"s_sub_u32 s82, s67, s66\n"
"s_cmp_gt_i32 s82, 0\n"
"s_cselect_b32 s83, s67, s66\n"
"s_waitcnt lgkmcnt(0)\n"
"s_sub_u32 s82, s80, s67\n"
"s_add_u32 s63, s63, 1\n"
"s_min_i32 s81, s63, 0\n"
"s_cmp_gt_i32 s82, 20\n"
"s_cselect_b32 s63, s63, s81\n"
"s_cmp_ge_i32 s63, 2\n"
"s_cbranch_scc0 LPRL1_%=\n"
"s_mov_b32 s63, -9\n"
"s_add_u32 s62, s62, 14\n"
"s_min_u32 s62, s62, 146\n"
"s_max_u32 s72, s72, s62\n"
"s_add_u32 s83, s80, 200\n"
"s_and_b32 s83, s83, -1\n"
"LPRL1_%=:\n"
"s_sub_u32 s82, s83, s80\n"
"s_cmp_gt_i32 s82, 3\n"
"s_cbranch_scc0 LPEL1_%=\n"
"LPDL1_%=:\n"
"s_sleep 1\n"
"s_memrealtime s[80:81]\n"
"s_waitcnt lgkmcnt(0)\n"
"s_sub_u32 s82, s83, s80\n"
"s_cmp_gt_i32 s82, 3\n"
"s_cbranch_scc1 LPDL1_%=\n"
"LPEL1_%=:\n"
"s_add_u32 s66, s66, s72\n"
"s_add_u32 s66, s66, 4\n"
"s_add_u32 s67, s80, s72\n"
"s_sub_u32 s72, s72, 3\n"
"s_max_u32 s72, s72, s62\n"
"s_branch LPKL1_%=\n"
"LPCL2_%=:\n"
"s_memrealtime s[80:81]\n"
"s_sub_u32 s82, s67, s66\n"
"s_cmp_gt_i32 s82, 0\n"
"s_cselect_b32 s83, s67, s66\n"
"s_waitcnt lgkmcnt(0)\n"
"s_sub_u32 s82, s80, s67\n"
"s_add_u32 s63, s63, 1\n"
"s_min_i32 s81, s63, 0\n"
"s_cmp_gt_i32 s82, 20\n"
"s_cselect_b32 s63, s63, s81\n"
"s_cmp_ge_i32 s63, 2\n"
"s_cbranch_scc0 LPRL2_%=\n"
"s_mov_b32 s63, -9\n"
"s_add_u32 s62, s62, 14\n"
"s_min_u32 s62, s62, 146\n"
"s_max_u32 s72, s72, s62\n"
"s_add_u32 s83, s80, 200\n"
"s_and_b32 s83, s83, -1\n"
"LPRL2_%=:\n"
"s_sub_u32 s82, s83, s80\n"
"s_cmp_gt_i32 s82, 3\n"
"s_cbranch_scc0 LPEL2_%=\n"
"LPDL2_%=:\n"
"s_sleep 1\n"
"s_memrealtime s[80:81]\n"
"s_waitcnt lgkmcnt(0)\n"
"s_sub_u32 s82, s83, s80\n"
"s_cmp_gt_i32 s82, 3\n"
"s_cbranch_scc1 LPDL2_%=\n"
"LPEL2_%=:\n"
"s_add_u32 s66, s66, s72\n"
"s_add_u32 s66, s66, 4\n"
"s_add_u32 s67, s80, s72\n"
"s_sub_u32 s72, s72, 3\n"
"s_max_u32 s72, s72, s62\n"
"s_branch LPKL2_%=\n"
"LPCL3_%=:\n"
"s_memrealtime s[80:81]\n"
"s_sub_u32 s82, s67, s66\n"
"s_cmp_gt_i32 s82, 0\n"
"s_cselect_b32 s83, s67, s66\n"
"s_waitcnt lgkmcnt(0)\n"
"s_sub_u32 s82, s80, s67\n"
"s_add_u32 s63, s63, 1\n"
"s_min_i32 s81, s63, 0\n"
"s_cmp_gt_i32 s82, 20\n"
"s_cselect_b32 s63, s63, s81\n"
"s_cmp_ge_i32 s63, 2\n"
"s_cbranch_scc0 LPRL3_%=\n"
"s_mov_b32 s63, -9\n"
"s_add_u32 s62, s62, 14\n"
"s_min_u32 s62, s62, 146\n"
"s_max_u32 s72, s72, s62\n"
"s_add_u32 s83, s80, 200\n"
"s_and_b32 s83, s83, -1\n"
"LPRL3_%=:\n"
"s_sub_u32 s82, s83, s80\n"
"s_cmp_gt_i32 s82, 3\n"
"s_cbranch_scc0 LPEL3_%=\n"
"LPDL3_%=:\n"
"s_sleep 1\n"
"s_memrealtime s[80:81]\n"
"s_waitcnt lgkmcnt(0)\n"
"s_sub_u32 s82, s83, s80\n"
"s_cmp_gt_i32 s82, 3\n"
"s_cbranch_scc1 LPDL3_%=\n"
"LPEL3_%=:\n"
"s_add_u32 s66, s66, s72\n"
"s_add_u32 s66, s66, 4\n"
"s_add_u32 s67, s80, s72\n"
"s_sub_u32 s72, s72, 3\n"
"s_max_u32 s72, s72, s62\n"
"s_branch LPKL3_%=\n"
"LPN_%=:\n"
"v_mfma_f32_16x16x32_bf16 v[112:115], a[192:195], a[136:139], v[112:115]\n"
"v_mfma_f32_16x16x32_bf16 v[112:115], a[196:199], a[140:143], v[112:115]\n"
"v_mfma_f32_16x16x32_bf16 v[116:119], a[200:203], a[136:139], v[116:119]\n"
"s_waitcnt vmcnt(11)\n"
"v_mfma_f32_16x16x32_bf16 v[116:119], a[204:207], a[140:143], v[116:119]\n"
"s_barrier\n"
"v_mfma_f32_16x16x32_bf16 v[120:123], a[208:211], a[136:139], v[120:123]\n"
"s_add_u32 s40, s48, 256\n"
"s_xor_b32 m0, s50, 0xc000\n"
"ds_read_b64_tr_b16 a[224:225], v244 offset:16384\n"
"v_mfma_f32_16x16x32_bf16 v[120:123], a[212:215], a[140:143], v[120:123]\n"
"buffer_load_dwordx4 v224, s[28:31], s40 offen nt lds\n"
"v_mfma_f32_16x16x32_bf16 v[124:127], a[216:219], a[136:139], v[124:127]\n"
"ds_read_b64_tr_b16 a[226:227], v244 offset:16896\n"
"v_mfma_f32_16x16x32_bf16 v[124:127], a[220:223], a[140:143], v[124:127]\n"
"ds_read_b64_tr_b16 a[228:229], v244 offset:24576\n"
"v_mfma_f32_16x16x32_bf16 v[140:143], a[216:219], a[144:147], v[140:143]\n"
"ds_read_b64_tr_b16 a[230:231], v244 offset:25088\n"
"v_mfma_f32_16x16x32_bf16 v[140:143], a[220:223], a[148:151], v[140:143]\n"
"ds_read_b64_tr_b16 a[232:233], v245 offset:16384\n"
"v_mfma_f32_16x16x32_bf16 v[136:139], a[208:211], a[144:147], v[136:139]\n"
"ds_read_b64_tr_b16 a[234:235], v245 offset:16896\n"
"v_mfma_f32_16x16x32_bf16 v[136:139], a[212:215], a[148:151], v[136:139]\n"
"buffer_load_dwordx4 v225, s[28:31], s40 offen offset:1024 nt lds\n"
"v_mfma_f32_16x16x32_bf16 v[132:135], a[200:203], a[144:147], v[132:135]\n"
"ds_read_b64_tr_b16 a[236:237], v245 offset:24576\n"
"v_mfma_f32_16x16x32_bf16 v[132:135], a[204:207], a[148:151], v[132:135]\n"
"ds_read_b64_tr_b16 a[238:239], v245 offset:25088\n"
"v_mfma_f32_16x16x32_bf16 v[128:131], a[192:195], a[144:147], v[128:131]\n"
"ds_read_b64_tr_b16 a[240:241], v244 offset:17408\n"
"v_mfma_f32_16x16x32_bf16 v[128:131], a[196:199], a[148:151], v[128:131]\n"
"ds_read_b64_tr_b16 a[242:243], v244 offset:17920\n"
"v_mfma_f32_16x16x32_bf16 v[144:147], a[192:195], a[152:155], v[144:147]\n"
"ds_read_b64_tr_b16 a[244:245], v244 offset:25600\n"
"v_mfma_f32_16x16x32_bf16 v[144:147], a[196:199], a[156:159], v[144:147]\n"
"buffer_load_dwordx4 v226, s[28:31], s40 offen offset:2048 nt lds\n"
"v_mfma_f32_16x16x32_bf16 v[148:151], a[200:203], a[152:155], v[148:151]\n"
"ds_read_b64_tr_b16 a[246:247], v244 offset:26112\n"
"v_mfma_f32_16x16x32_bf16 v[148:151], a[204:207], a[156:159], v[148:151]\n"
"ds_read_b64_tr_b16 a[248:249], v245 offset:17408\n"
"v_mfma_f32_16x16x32_bf16 v[152:155], a[208:211], a[152:155], v[152:155]\n"
"ds_read_b64_tr_b16 a[250:251], v245 offset:17920\n"
"v_mfma_f32_16x16x32_bf16 v[152:155], a[212:215], a[156:159], v[152:155]\n"
"ds_read_b64_tr_b16 a[252:253], v245 offset:25600\n"
"v_mfma_f32_16x16x32_bf16 v[156:159], a[216:219], a[152:155], v[156:159]\n"
"ds_read_b64_tr_b16 a[254:255], v245 offset:26112\n"
"v_mfma_f32_16x16x32_bf16 v[156:159], a[220:223], a[156:159], v[156:159]\n"
"buffer_load_dwordx4 v227, s[28:31], s40 offen offset:3072 nt lds\n"
"v_mfma_f32_16x16x32_bf16 v[172:175], a[216:219], a[160:163], v[172:175]\n"
"s_cmp_eq_u32 s47, 8\n"
"v_mfma_f32_16x16x32_bf16 v[172:175], a[220:223], a[164:167], v[172:175]\n"
"s_cselect_b32 s41, s52, s58\n"
"v_mfma_f32_16x16x32_bf16 v[168:171], a[208:211], a[160:163], v[168:171]\n"
"s_add_u32 s48, s48, s41\n"
"v_mfma_f32_16x16x32_bf16 v[168:171], a[212:215], a[164:167], v[168:171]\n"
"s_sub_u32 s41, s48, 5242880\n"
"v_mfma_f32_16x16x32_bf16 v[164:167], a[200:203], a[160:163], v[164:167]\n"
"s_min_u32 s48, s48, s41\n"
"v_mfma_f32_16x16x32_bf16 v[164:167], a[204:207], a[164:167], v[164:167]\n"
"v_xor_b32 v244, s74, v244\n"
"v_xor_b32 v246, s74, v246\n"
"v_mfma_f32_16x16x32_bf16 v[160:163], a[192:195], a[160:163], v[160:163]\n"
"s_mov_b32 m0, s50\n"
"v_xor_b32 v245, s74, v245\n"
"v_xor_b32 v247, s74, v247\n"
"v_mfma_f32_16x16x32_bf16 v[160:163], a[196:199], a[164:167], v[160:163]\n"
"buffer_load_dwordx4 v224, s[28:31], s48 offen nt lds\n"
"v_mfma_f32_16x16x32_bf16 v[176:179], a[192:195], a[168:171], v[176:179]\n"
"v_xor_b32 v252, s74, v252\n"
"v_xor_b32 v253, s74, v253\n"
"v_mfma_f32_16x16x32_bf16 v[176:179], a[196:199], a[172:175], v[176:179]\n"
"v_xor_b32 v250, s74, v250\n"
"v_xor_b32 v251, s74, v251\n"
"v_mfma_f32_16x16x32_bf16 v[180:183], a[200:203], a[168:171], v[180:183]\n"
"v_mfma_f32_16x16x32_bf16 v[180:183], a[204:207], a[172:175], v[180:183]\n"
"buffer_load_dwordx4 v225, s[28:31], s48 offen offset:1024 nt lds\n"
"v_mfma_f32_16x16x32_bf16 v[184:187], a[208:211], a[168:171], v[184:187]\n"
"v_mfma_f32_16x16x32_bf16 v[184:187], a[212:215], a[172:175], v[184:187]\n"
"v_mfma_f32_16x16x32_bf16 v[188:191], a[216:219], a[168:171], v[188:191]\n"
"v_mfma_f32_16x16x32_bf16 v[188:191], a[220:223], a[172:175], v[188:191]\n"
"v_mfma_f32_16x16x32_bf16 v[204:207], a[216:219], a[176:179], v[204:207]\n"
"v_mfma_f32_16x16x32_bf16 v[204:207], a[220:223], a[180:183], v[204:207]\n"
"buffer_load_dwordx4 v226, s[28:31], s48 offen offset:2048 nt lds\n"
"v_mfma_f32_16x16x32_bf16 v[200:203], a[208:211], a[176:179], v[200:203]\n"
"v_mfma_f32_16x16x32_bf16 v[200:203], a[212:215], a[180:183], v[200:203]\n"
"v_mfma_f32_16x16x32_bf16 v[196:199], a[200:203], a[176:179], v[196:199]\n"
"v_mfma_f32_16x16x32_bf16 v[196:199], a[204:207], a[180:183], v[196:199]\n"
"v_mfma_f32_16x16x32_bf16 v[192:195], a[192:195], a[176:179], v[192:195]\n"
"v_mfma_f32_16x16x32_bf16 v[192:195], a[196:199], a[180:183], v[192:195]\n"
"s_waitcnt lgkmcnt(0)\n"
"buffer_load_dwordx4 v227, s[28:31], s48 offen offset:3072 nt lds\n"
"v_mfma_f32_16x16x32_bf16 v[208:211], a[192:195], a[184:187], v[208:211]\n"
"s_xor_b32 s50, s50, s74\n"
"v_mfma_f32_16x16x32_bf16 v[208:211], a[196:199], a[188:191], v[208:211]\n"
"v_mfma_f32_16x16x32_bf16 v[212:215], a[200:203], a[184:187], v[212:215]\n"
"v_mfma_f32_16x16x32_bf16 v[212:215], a[204:207], a[188:191], v[212:215]\n"
"v_mfma_f32_16x16x32_bf16 v[216:219], a[208:211], a[184:187], v[216:219]\n"
"v_mfma_f32_16x16x32_bf16 v[216:219], a[212:215], a[188:191], v[216:219]\n"
"v_mfma_f32_16x16x32_bf16 v[220:223], a[216:219], a[184:187], v[220:223]\n"
"v_mfma_f32_16x16x32_bf16 v[220:223], a[220:223], a[188:191], v[220:223]\n"
"v_mfma_f32_16x16x32_bf16 v[0:3], a[224:227], a[136:139], v[0:3]\n"
"v_mfma_f32_16x16x32_bf16 v[0:3], a[228:231], a[140:143], v[0:3]\n"
"v_mfma_f32_16x16x32_bf16 v[4:7], a[232:235], a[136:139], v[4:7]\n"
"s_waitcnt vmcnt(8)\n"
"v_mfma_f32_16x16x32_bf16 v[4:7], a[236:239], a[140:143], v[4:7]\n"
"s_barrier\n"
"v_mfma_f32_16x16x32_bf16 v[8:11], a[240:243], a[136:139], v[8:11]\n"
"s_mov_b32 m0, s51\n"
"ds_read_b64_tr_b16 a[192:193], v244 offset:0\n"
"v_mfma_f32_16x16x32_bf16 v[8:11], a[244:247], a[140:143], v[8:11]\n"
"buffer_load_dword v242, s[80:83], 0 offen lds\n"
"s_add_u32 s80, s80, 680\n"
"s_addc_u32 s81, s81, 0\n"
"buffer_load_dwordx4 v248, s[24:27], s49 offen nt lds\n"
"v_mfma_f32_16x16x32_bf16 v[12:15], a[248:251], a[136:139], v[12:15]\n"
"ds_read_b64_tr_b16 a[194:195], v244 offset:512\n"
"v_mfma_f32_16x16x32_bf16 v[12:15], a[252:255], a[140:143], v[12:15]\n"
"ds_read_b128 a[136:139], v246 offset:0\n"
"v_mfma_f32_16x16x32_bf16 v[28:31], a[248:251], a[144:147], v[28:31]\n"
"ds_read_b64_tr_b16 a[196:197], v244 offset:8192\n"
"v_mfma_f32_16x16x32_bf16 v[28:31], a[252:255], a[148:151], v[28:31]\n"
"ds_read_b64_tr_b16 a[198:199], v244 offset:8704\n"
"v_mfma_f32_16x16x32_bf16 v[24:27], a[240:243], a[144:147], v[24:27]\n"
"ds_read_b128 a[140:143], v247 offset:0\n"
"v_mfma_f32_16x16x32_bf16 v[24:27], a[244:247], a[148:151], v[24:27]\n"
"buffer_load_dwordx4 v249, s[24:27], s49 offen offset:1024 nt lds\n"
"v_mfma_f32_16x16x32_bf16 v[20:23], a[232:235], a[144:147], v[20:23]\n"
"ds_read_b64_tr_b16 a[200:201], v245 offset:0\n"
"v_mfma_f32_16x16x32_bf16 v[20:23], a[236:239], a[148:151], v[20:23]\n"
"ds_read_b64_tr_b16 a[202:203], v245 offset:512\n"
"v_mfma_f32_16x16x32_bf16 v[16:19], a[224:227], a[144:147], v[16:19]\n"
"ds_read_b64_tr_b16 a[204:205], v245 offset:8192\n"
"v_mfma_f32_16x16x32_bf16 v[16:19], a[228:231], a[148:151], v[16:19]\n"
"ds_read_b64_tr_b16 a[206:207], v245 offset:8704\n"
"v_mfma_f32_16x16x32_bf16 v[32:35], a[224:227], a[152:155], v[32:35]\n"
"ds_read_b64_tr_b16 a[208:209], v244 offset:1024\n"
"v_mfma_f32_16x16x32_bf16 v[32:35], a[228:231], a[156:159], v[32:35]\n"
"buffer_load_dwordx4 v254, s[24:27], s49 offen offset:2048 nt lds\n"
"v_mfma_f32_16x16x32_bf16 v[36:39], a[232:235], a[152:155], v[36:39]\n"
"ds_read_b64_tr_b16 a[210:211], v244 offset:1536\n"
"v_mfma_f32_16x16x32_bf16 v[36:39], a[236:239], a[156:159], v[36:39]\n"
"ds_read_b64_tr_b16 a[212:213], v244 offset:9216\n"
"v_mfma_f32_16x16x32_bf16 v[40:43], a[240:243], a[152:155], v[40:43]\n"
"ds_read_b64_tr_b16 a[214:215], v244 offset:9728\n"
"v_mfma_f32_16x16x32_bf16 v[40:43], a[244:247], a[156:159], v[40:43]\n"
"ds_read_b64_tr_b16 a[216:217], v245 offset:1024\n"
"v_mfma_f32_16x16x32_bf16 v[44:47], a[248:251], a[152:155], v[44:47]\n"
"ds_read_b64_tr_b16 a[218:219], v245 offset:1536\n"
"v_mfma_f32_16x16x32_bf16 v[44:47], a[252:255], a[156:159], v[44:47]\n"
"buffer_load_dwordx4 v255, s[24:27], s49 offen offset:3072 nt lds\n"
"v_mfma_f32_16x16x32_bf16 v[60:63], a[248:251], a[160:163], v[60:63]\n"
"ds_read_b64_tr_b16 a[220:221], v245 offset:9216\n"
"v_mfma_f32_16x16x32_bf16 v[60:63], a[252:255], a[164:167], v[60:63]\n"
"ds_read_b64_tr_b16 a[222:223], v245 offset:9728\n"
"v_mfma_f32_16x16x32_bf16 v[56:59], a[240:243], a[160:163], v[56:59]\n"
"ds_read_b128 a[144:147], v246 offset:2048\n"
"v_mfma_f32_16x16x32_bf16 v[56:59], a[244:247], a[164:167], v[56:59]\n"
"s_add_u32 s40, s49, 40960\n"
"ds_read_b128 a[148:151], v247 offset:2048\n"
"v_mfma_f32_16x16x32_bf16 v[52:55], a[232:235], a[160:163], v[52:55]\n"
"s_add_u32 m0, s51, 4096\n"
"ds_read_b128 a[152:155], v246 offset:4096\n"
"v_mfma_f32_16x16x32_bf16 v[52:55], a[236:239], a[164:167], v[52:55]\n"
"buffer_load_dwordx4 v248, s[24:27], s40 offen nt lds\n"
"v_mfma_f32_16x16x32_bf16 v[48:51], a[224:227], a[160:163], v[48:51]\n"
"ds_read_b128 a[156:159], v247 offset:4096\n"
"v_mfma_f32_16x16x32_bf16 v[48:51], a[228:231], a[164:167], v[48:51]\n"
"ds_read_b128 a[160:163], v252 offset:0\n"
"v_mfma_f32_16x16x32_bf16 v[64:67], a[224:227], a[168:171], v[64:67]\n"
"ds_read_b128 a[164:167], v253 offset:0\n"
"v_mfma_f32_16x16x32_bf16 v[64:67], a[228:231], a[172:175], v[64:67]\n"
"v_mfma_f32_16x16x32_bf16 v[68:71], a[232:235], a[168:171], v[68:71]\n"
"v_mfma_f32_16x16x32_bf16 v[68:71], a[236:239], a[172:175], v[68:71]\n"
"buffer_load_dwordx4 v249, s[24:27], s40 offen offset:1024 nt lds\n"
"v_mfma_f32_16x16x32_bf16 v[72:75], a[240:243], a[168:171], v[72:75]\n"
"v_mfma_f32_16x16x32_bf16 v[72:75], a[244:247], a[172:175], v[72:75]\n"
"v_mfma_f32_16x16x32_bf16 v[76:79], a[248:251], a[168:171], v[76:79]\n"
"ds_read_b128 a[168:171], v250 offset:0\n"
"v_mfma_f32_16x16x32_bf16 v[76:79], a[252:255], a[172:175], v[76:79]\n"
"ds_read_b128 a[172:175], v251 offset:0\n"
"v_mfma_f32_16x16x32_bf16 v[92:95], a[248:251], a[176:179], v[92:95]\n"
"v_mfma_f32_16x16x32_bf16 v[92:95], a[252:255], a[180:183], v[92:95]\n"
"buffer_load_dwordx4 v254, s[24:27], s40 offen offset:2048 nt lds\n"
"v_mfma_f32_16x16x32_bf16 v[88:91], a[240:243], a[176:179], v[88:91]\n"
"s_add_u32 s49, s49, 128\n"
"v_mfma_f32_16x16x32_bf16 v[88:91], a[244:247], a[180:183], v[88:91]\n"
"s_sub_u32 s41, s49, 1280\n"
"v_mfma_f32_16x16x32_bf16 v[84:87], a[232:235], a[176:179], v[84:87]\n"
"s_min_u32 s49, s49, s41\n"
"v_mfma_f32_16x16x32_bf16 v[84:87], a[236:239], a[180:183], v[84:87]\n"
"s_xor_b32 s51, s51, s74\n"
"v_mfma_f32_16x16x32_bf16 v[80:83], a[224:227], a[176:179], v[80:83]\n"
"ds_read_b128 a[176:179], v250 offset:2048\n"
"v_mfma_f32_16x16x32_bf16 v[80:83], a[228:231], a[180:183], v[80:83]\n"
"ds_read_b128 a[180:183], v251 offset:2048\n"
"v_mfma_f32_16x16x32_bf16 v[96:99], a[224:227], a[184:187], v[96:99]\n"
"v_mfma_f32_16x16x32_bf16 v[96:99], a[228:231], a[188:191], v[96:99]\n"
"v_mfma_f32_16x16x32_bf16 v[100:103], a[232:235], a[184:187], v[100:103]\n"
"v_mfma_f32_16x16x32_bf16 v[100:103], a[236:239], a[188:191], v[100:103]\n"
"v_mfma_f32_16x16x32_bf16 v[104:107], a[240:243], a[184:187], v[104:107]\n"
"v_mfma_f32_16x16x32_bf16 v[104:107], a[244:247], a[188:191], v[104:107]\n"
"v_mfma_f32_16x16x32_bf16 v[108:111], a[248:251], a[184:187], v[108:111]\n"
"s_add_u32 s47, s47, 1\n"
"ds_read_b128 a[184:187], v250 offset:4096\n"
"v_mfma_f32_16x16x32_bf16 v[108:111], a[252:255], a[188:191], v[108:111]\n"
"s_waitcnt lgkmcnt(1)\n"
"s_cmp_lg_u32 s47, s62\n"
"ds_read_b128 a[188:191], v251 offset:4096\n"
"s_cbranch_scc1 LPN_%=\n"
"s_branch LPX_%=\n"
"LEND_%=:\n"
"LSLOW_%=:\n"
"s_waitcnt vmcnt(0) lgkmcnt(0)\n"
"s_nop 9\n"
"v_bfe_u32 v148, v242, 4, 4\n"
"v_bfe_u32 v149, v242, 8, 2\n"
"v_lshlrev_b32 v144, 8, v148\n"
"v_lshl_add_u32 v144, v149, 4, v144\n"
"v_add_u32 v144, s43, v144\n"
"v_xor_b32 v145, 64, v144\n"
"v_lshlrev_b32 v146, 1, v148\n"
"v_lshl_add_u32 v146, v149, 10, v146\n"
"v_add_u32 v146, s43, v146\n"
"v_xor_b32 v147, 64, v146\n"
"v_mov_b32 v152, 0xff7fffff\n"
"v_mov_b32 v155, 0\n"
"v_mov_b32 v0, 0\n"
"v_mov_b32 v1, 0\n"
"v_mov_b32 v2, 0\n"
"v_mov_b32 v3, 0\n"
"v_mov_b32 v4, 0\n"
"v_mov_b32 v5, 0\n"
"v_mov_b32 v6, 0\n"
"v_mov_b32 v7, 0\n"
"v_mov_b32 v8, 0\n"
"v_mov_b32 v9, 0\n"
"v_mov_b32 v10, 0\n"
"v_mov_b32 v11, 0\n"
"v_mov_b32 v12, 0\n"
"v_mov_b32 v13, 0\n"
"v_mov_b32 v14, 0\n"
"v_mov_b32 v15, 0\n"
"v_mov_b32 v16, 0\n"
"v_mov_b32 v17, 0\n"
"v_mov_b32 v18, 0\n"
"v_mov_b32 v19, 0\n"
"v_mov_b32 v20, 0\n"
"v_mov_b32 v21, 0\n"
"v_mov_b32 v22, 0\n"
"v_mov_b32 v23, 0\n"
"v_mov_b32 v24, 0\n"
"v_mov_b32 v25, 0\n"
"v_mov_b32 v26, 0\n"
"v_mov_b32 v27, 0\n"
"v_mov_b32 v28, 0\n"
"v_mov_b32 v29, 0\n"
"v_mov_b32 v30, 0\n"
"v_mov_b32 v31, 0\n"
"v_mov_b32 v156, 0xff7fffff\n"
"v_mov_b32 v159, 0\n"
"v_mov_b32 v32, 0\n"
"v_mov_b32 v33, 0\n"
"v_mov_b32 v34, 0\n"
"v_mov_b32 v35, 0\n"
"v_mov_b32 v36, 0\n"
"v_mov_b32 v37, 0\n"
"v_mov_b32 v38, 0\n"
"v_mov_b32 v39, 0\n"
"v_mov_b32 v40, 0\n"
"v_mov_b32 v41, 0\n"
"v_mov_b32 v42, 0\n"
"v_mov_b32 v43, 0\n"
"v_mov_b32 v44, 0\n"
"v_mov_b32 v45, 0\n"
"v_mov_b32 v46, 0\n"
"v_mov_b32 v47, 0\n"
"v_mov_b32 v48, 0\n"
"v_mov_b32 v49, 0\n"
"v_mov_b32 v50, 0\n"
"v_mov_b32 v51, 0\n"
"v_mov_b32 v52, 0\n"
"v_mov_b32 v53, 0\n"
"v_mov_b32 v54, 0\n"
"v_mov_b32 v55, 0\n"
"v_mov_b32 v56, 0\n"
"v_mov_b32 v57, 0\n"
"v_mov_b32 v58, 0\n"
"v_mov_b32 v59, 0\n"
"v_mov_b32 v60, 0\n"
"v_mov_b32 v61, 0\n"
"v_mov_b32 v62, 0\n"
"v_mov_b32 v63, 0\n"
"v_mov_b32 v160, 0xff7fffff\n"
"v_mov_b32 v163, 0\n"
"v_mov_b32 v64, 0\n"
"v_mov_b32 v65, 0\n"
"v_mov_b32 v66, 0\n"
"v_mov_b32 v67, 0\n"
"v_mov_b32 v68, 0\n"
"v_mov_b32 v69, 0\n"
"v_mov_b32 v70, 0\n"
"v_mov_b32 v71, 0\n"
"v_mov_b32 v72, 0\n"
"v_mov_b32 v73, 0\n"
"v_mov_b32 v74, 0\n"
"v_mov_b32 v75, 0\n"
"v_mov_b32 v76, 0\n"
"v_mov_b32 v77, 0\n"
"v_mov_b32 v78, 0\n"
"v_mov_b32 v79, 0\n"
"v_mov_b32 v80, 0\n"
"v_mov_b32 v81, 0\n"
"v_mov_b32 v82, 0\n"
"v_mov_b32 v83, 0\n"
"v_mov_b32 v84, 0\n"
"v_mov_b32 v85, 0\n"
"v_mov_b32 v86, 0\n"
"v_mov_b32 v87, 0\n"
"v_mov_b32 v88, 0\n"
"v_mov_b32 v89, 0\n"
"v_mov_b32 v90, 0\n"
"v_mov_b32 v91, 0\n"
"v_mov_b32 v92, 0\n"
"v_mov_b32 v93, 0\n"
"v_mov_b32 v94, 0\n"
"v_mov_b32 v95, 0\n"
"v_mov_b32 v164, 0xff7fffff\n"
"v_mov_b32 v167, 0\n"
"v_accvgpr_write_b32 a208, 0\n"
"v_accvgpr_write_b32 a209, 0\n"
"v_accvgpr_write_b32 a210, 0\n"
"v_accvgpr_write_b32 a211, 0\n"
"v_accvgpr_write_b32 a212, 0\n"
"v_accvgpr_write_b32 a213, 0\n"
"v_accvgpr_write_b32 a214, 0\n"
"v_accvgpr_write_b32 a215, 0\n"
"v_accvgpr_write_b32 a216, 0\n"
"v_accvgpr_write_b32 a217, 0\n"
"v_accvgpr_write_b32 a218, 0\n"
"v_accvgpr_write_b32 a219, 0\n"
"v_accvgpr_write_b32 a220, 0\n"
"v_accvgpr_write_b32 a221, 0\n"
"v_accvgpr_write_b32 a222, 0\n"
"v_accvgpr_write_b32 a223, 0\n"
"v_accvgpr_write_b32 a224, 0\n"
"v_accvgpr_write_b32 a225, 0\n"
"v_accvgpr_write_b32 a226, 0\n"
"v_accvgpr_write_b32 a227, 0\n"
"v_accvgpr_write_b32 a228, 0\n"
"v_accvgpr_write_b32 a229, 0\n"
"v_accvgpr_write_b32 a230, 0\n"
"v_accvgpr_write_b32 a231, 0\n"
"v_accvgpr_write_b32 a232, 0\n"
"v_accvgpr_write_b32 a233, 0\n"
"v_accvgpr_write_b32 a234, 0\n"
"v_accvgpr_write_b32 a235, 0\n"
"v_accvgpr_write_b32 a236, 0\n"
"v_accvgpr_write_b32 a237, 0\n"
"v_accvgpr_write_b32 a238, 0\n"
"v_accvgpr_write_b32 a239, 0\n"
"s_lshl_b32 s80, s47, 16\n"
"s_sub_u32 s80, s80, 261248\n"
"s_add_u32 s81, s80, 4096\n"
"s_mov_b32 s82, 0\n"
"LSLOOP_%=:\n"
"s_lshl_b32 s83, s82, 6\n"
"s_and_b32 s83, s83, 64\n"
"buffer_load_dwordx4 v[128:131], v144, s[84:87], s80 offen offset:0\n"
"buffer_load_dwordx4 v[132:135], v145, s[84:87], s80 offen offset:0\n"
"buffer_load_dwordx4 v[136:139], v144, s[84:87], s80 offen offset:128\n"
"buffer_load_dwordx4 v[140:143], v145, s[84:87], s80 offen offset:128\n"
"s_waitcnt vmcnt(0)\n"
"v_mfma_f32_16x16x32_bf16 v[96:99], v[128:131], a[0:3], 0\n"
"v_mfma_f32_16x16x32_bf16 v[96:99], v[132:135], a[4:7], v[96:99]\n"
"v_mfma_f32_16x16x32_bf16 v[96:99], v[136:139], a[8:11], v[96:99]\n"
"v_mfma_f32_16x16x32_bf16 v[96:99], v[140:143], a[12:15], v[96:99]\n"
"v_mfma_f32_16x16x32_bf16 v[104:107], v[128:131], a[16:19], 0\n"
"v_mfma_f32_16x16x32_bf16 v[104:107], v[132:135], a[20:23], v[104:107]\n"
"v_mfma_f32_16x16x32_bf16 v[104:107], v[136:139], a[24:27], v[104:107]\n"
"v_mfma_f32_16x16x32_bf16 v[104:107], v[140:143], a[28:31], v[104:107]\n"
"v_mfma_f32_16x16x32_bf16 v[112:115], v[128:131], a[32:35], 0\n"
"v_mfma_f32_16x16x32_bf16 v[112:115], v[132:135], a[36:39], v[112:115]\n"
"v_mfma_f32_16x16x32_bf16 v[112:115], v[136:139], a[40:43], v[112:115]\n"
"v_mfma_f32_16x16x32_bf16 v[112:115], v[140:143], a[44:47], v[112:115]\n"
"v_mfma_f32_16x16x32_bf16 v[120:123], v[128:131], a[192:195], 0\n"
"v_mfma_f32_16x16x32_bf16 v[120:123], v[132:135], a[196:199], v[120:123]\n"
"v_mfma_f32_16x16x32_bf16 v[120:123], v[136:139], a[200:203], v[120:123]\n"
"v_mfma_f32_16x16x32_bf16 v[120:123], v[140:143], a[204:207], v[120:123]\n"
"s_nop 7\n"
"buffer_load_dwordx4 v[128:131], v144, s[84:87], s81 offen offset:0\n"
"buffer_load_dwordx4 v[132:135], v145, s[84:87], s81 offen offset:0\n"
"buffer_load_dwordx4 v[136:139], v144, s[84:87], s81 offen offset:128\n"
"buffer_load_dwordx4 v[140:143], v145, s[84:87], s81 offen offset:128\n"
"s_waitcnt vmcnt(0)\n"
"v_mfma_f32_16x16x32_bf16 v[100:103], v[128:131], a[0:3], 0\n"
"v_mfma_f32_16x16x32_bf16 v[100:103], v[132:135], a[4:7], v[100:103]\n"
"v_mfma_f32_16x16x32_bf16 v[100:103], v[136:139], a[8:11], v[100:103]\n"
"v_mfma_f32_16x16x32_bf16 v[100:103], v[140:143], a[12:15], v[100:103]\n"
"v_mfma_f32_16x16x32_bf16 v[108:111], v[128:131], a[16:19], 0\n"
"v_mfma_f32_16x16x32_bf16 v[108:111], v[132:135], a[20:23], v[108:111]\n"
"v_mfma_f32_16x16x32_bf16 v[108:111], v[136:139], a[24:27], v[108:111]\n"
"v_mfma_f32_16x16x32_bf16 v[108:111], v[140:143], a[28:31], v[108:111]\n"
"v_mfma_f32_16x16x32_bf16 v[116:119], v[128:131], a[32:35], 0\n"
"v_mfma_f32_16x16x32_bf16 v[116:119], v[132:135], a[36:39], v[116:119]\n"
"v_mfma_f32_16x16x32_bf16 v[116:119], v[136:139], a[40:43], v[116:119]\n"
"v_mfma_f32_16x16x32_bf16 v[116:119], v[140:143], a[44:47], v[116:119]\n"
"v_mfma_f32_16x16x32_bf16 v[124:127], v[128:131], a[192:195], 0\n"
"v_mfma_f32_16x16x32_bf16 v[124:127], v[132:135], a[196:199], v[124:127]\n"
"v_mfma_f32_16x16x32_bf16 v[124:127], v[136:139], a[200:203], v[124:127]\n"
"v_mfma_f32_16x16x32_bf16 v[124:127], v[140:143], a[204:207], v[124:127]\n"
"s_nop 7\n"
"v_max3_f32 v153, v96, v97, v98\n"
"v_max3_f32 v153, v153, v99, v100\n"
"v_max3_f32 v153, v153, v101, v102\n"
"v_max3_f32 v153, v153, v103, v152\n"
"v_mov_b32 v154, v153\n"
"s_nop 1\n"
"v_permlane32_swap_b32 v153, v154\n"
"v_max_f32 v153, v153, v154\n"
"v_mov_b32 v154, v153\n"
"s_nop 1\n"
"v_permlane16_swap_b32 v153, v154\n"
"v_max_f32 v153, v153, v154\n"
"v_sub_f32 v154, v152, v153\n"
"v_exp_f32 v154, v154\n"
"v_mov_b32 v152, v153\n"
"v_sub_f32 v96, v96, v152\n"
"v_sub_f32 v97, v97, v152\n"
"v_sub_f32 v98, v98, v152\n"
"v_sub_f32 v99, v99, v152\n"
"v_sub_f32 v100, v100, v152\n"
"v_sub_f32 v101, v101, v152\n"
"v_sub_f32 v102, v102, v152\n"
"v_sub_f32 v103, v103, v152\n"
"v_exp_f32 v96, v96\n"
"v_exp_f32 v97, v97\n"
"v_exp_f32 v98, v98\n"
"v_exp_f32 v99, v99\n"
"v_exp_f32 v100, v100\n"
"v_exp_f32 v101, v101\n"
"v_exp_f32 v102, v102\n"
"v_exp_f32 v103, v103\n"
"v_mul_f32 v155, v155, v154\n"
"v_add_f32 v155, v155, v96\n"
"v_add_f32 v155, v155, v97\n"
"v_add_f32 v155, v155, v98\n"
"v_add_f32 v155, v155, v99\n"
"v_add_f32 v155, v155, v100\n"
"v_add_f32 v155, v155, v101\n"
"v_add_f32 v155, v155, v102\n"
"v_add_f32 v155, v155, v103\n"
"v_cvt_pk_bf16_f32 v96, v96, v97\n"
"v_cvt_pk_bf16_f32 v97, v98, v99\n"
"v_cvt_pk_bf16_f32 v98, v100, v101\n"
"v_cvt_pk_bf16_f32 v99, v102, v103\n"
"v_mov_b32_dpp v154, v154 row_ror:12 row_mask:0x2 bank_mask:0xf\n"
"s_nop 1\n"
"v_mov_b32_dpp v154, v154 row_ror:8 row_mask:0x4 bank_mask:0xf\n"
"s_nop 1\n"
"v_mov_b32_dpp v154, v154 row_ror:4 row_mask:0x8 bank_mask:0xf\n"
"s_nop 1\n"
"v_mul_f32_dpp v0, v154, v0 row_newbcast:0 row_mask:0xf bank_mask:0xf\n"
"v_mul_f32_dpp v1, v154, v1 row_newbcast:1 row_mask:0xf bank_mask:0xf\n"
"v_mul_f32_dpp v2, v154, v2 row_newbcast:2 row_mask:0xf bank_mask:0xf\n"
"v_mul_f32_dpp v3, v154, v3 row_newbcast:3 row_mask:0xf bank_mask:0xf\n"
"v_mul_f32_dpp v4, v154, v4 row_newbcast:0 row_mask:0xf bank_mask:0xf\n"
"v_mul_f32_dpp v5, v154, v5 row_newbcast:1 row_mask:0xf bank_mask:0xf\n"
"v_mul_f32_dpp v6, v154, v6 row_newbcast:2 row_mask:0xf bank_mask:0xf\n"
"v_mul_f32_dpp v7, v154, v7 row_newbcast:3 row_mask:0xf bank_mask:0xf\n"
"v_mul_f32_dpp v8, v154, v8 row_newbcast:0 row_mask:0xf bank_mask:0xf\n"
"v_mul_f32_dpp v9, v154, v9 row_newbcast:1 row_mask:0xf bank_mask:0xf\n"
"v_mul_f32_dpp v10, v154, v10 row_newbcast:2 row_mask:0xf bank_mask:0xf\n"
"v_mul_f32_dpp v11, v154, v11 row_newbcast:3 row_mask:0xf bank_mask:0xf\n"
"v_mul_f32_dpp v12, v154, v12 row_newbcast:0 row_mask:0xf bank_mask:0xf\n"
"v_mul_f32_dpp v13, v154, v13 row_newbcast:1 row_mask:0xf bank_mask:0xf\n"
"v_mul_f32_dpp v14, v154, v14 row_newbcast:2 row_mask:0xf bank_mask:0xf\n"
"v_mul_f32_dpp v15, v154, v15 row_newbcast:3 row_mask:0xf bank_mask:0xf\n"
"v_mul_f32_dpp v16, v154, v16 row_newbcast:0 row_mask:0xf bank_mask:0xf\n"
"v_mul_f32_dpp v17, v154, v17 row_newbcast:1 row_mask:0xf bank_mask:0xf\n"
"v_mul_f32_dpp v18, v154, v18 row_newbcast:2 row_mask:0xf bank_mask:0xf\n"
"v_mul_f32_dpp v19, v154, v19 row_newbcast:3 row_mask:0xf bank_mask:0xf\n"
"v_mul_f32_dpp v20, v154, v20 row_newbcast:0 row_mask:0xf bank_mask:0xf\n"
"v_mul_f32_dpp v21, v154, v21 row_newbcast:1 row_mask:0xf bank_mask:0xf\n"
"v_mul_f32_dpp v22, v154, v22 row_newbcast:2 row_mask:0xf bank_mask:0xf\n"
"v_mul_f32_dpp v23, v154, v23 row_newbcast:3 row_mask:0xf bank_mask:0xf\n"
"v_mul_f32_dpp v24, v154, v24 row_newbcast:0 row_mask:0xf bank_mask:0xf\n"
"v_mul_f32_dpp v25, v154, v25 row_newbcast:1 row_mask:0xf bank_mask:0xf\n"
"v_mul_f32_dpp v26, v154, v26 row_newbcast:2 row_mask:0xf bank_mask:0xf\n"
"v_mul_f32_dpp v27, v154, v27 row_newbcast:3 row_mask:0xf bank_mask:0xf\n"
"v_mul_f32_dpp v28, v154, v28 row_newbcast:0 row_mask:0xf bank_mask:0xf\n"
"v_mul_f32_dpp v29, v154, v29 row_newbcast:1 row_mask:0xf bank_mask:0xf\n"
"v_mul_f32_dpp v30, v154, v30 row_newbcast:2 row_mask:0xf bank_mask:0xf\n"
"v_mul_f32_dpp v31, v154, v31 row_newbcast:3 row_mask:0xf bank_mask:0xf\n"
"v_max3_f32 v157, v104, v105, v106\n"
"v_max3_f32 v157, v157, v107, v108\n"
"v_max3_f32 v157, v157, v109, v110\n"
"v_max3_f32 v157, v157, v111, v156\n"
"v_mov_b32 v158, v157\n"
"s_nop 1\n"
"v_permlane32_swap_b32 v157, v158\n"
"v_max_f32 v157, v157, v158\n"
"v_mov_b32 v158, v157\n"
"s_nop 1\n"
"v_permlane16_swap_b32 v157, v158\n"
"v_max_f32 v157, v157, v158\n"
"v_sub_f32 v158, v156, v157\n"
"v_exp_f32 v158, v158\n"
"v_mov_b32 v156, v157\n"
"v_sub_f32 v104, v104, v156\n"
"v_sub_f32 v105, v105, v156\n"
"v_sub_f32 v106, v106, v156\n"
"v_sub_f32 v107, v107, v156\n"
"v_sub_f32 v108, v108, v156\n"
"v_sub_f32 v109, v109, v156\n"
"v_sub_f32 v110, v110, v156\n"
"v_sub_f32 v111, v111, v156\n"
"v_exp_f32 v104, v104\n"
"v_exp_f32 v105, v105\n"
"v_exp_f32 v106, v106\n"
"v_exp_f32 v107, v107\n"
"v_exp_f32 v108, v108\n"
"v_exp_f32 v109, v109\n"
"v_exp_f32 v110, v110\n"
"v_exp_f32 v111, v111\n"
"v_mul_f32 v159, v159, v158\n"
"v_add_f32 v159, v159, v104\n"
"v_add_f32 v159, v159, v105\n"
"v_add_f32 v159, v159, v106\n"
"v_add_f32 v159, v159, v107\n"
"v_add_f32 v159, v159, v108\n"
"v_add_f32 v159, v159, v109\n"
"v_add_f32 v159, v159, v110\n"
"v_add_f32 v159, v159, v111\n"
"v_cvt_pk_bf16_f32 v104, v104, v105\n"
"v_cvt_pk_bf16_f32 v105, v106, v107\n"
"v_cvt_pk_bf16_f32 v106, v108, v109\n"
"v_cvt_pk_bf16_f32 v107, v110, v111\n"
"v_mov_b32_dpp v158, v158 row_ror:12 row_mask:0x2 bank_mask:0xf\n"
"s_nop 1\n"
"v_mov_b32_dpp v158, v158 row_ror:8 row_mask:0x4 bank_mask:0xf\n"
"s_nop 1\n"
"v_mov_b32_dpp v158, v158 row_ror:4 row_mask:0x8 bank_mask:0xf\n"
"s_nop 1\n"
"v_mul_f32_dpp v32, v158, v32 row_newbcast:0 row_mask:0xf bank_mask:0xf\n"
"v_mul_f32_dpp v33, v158, v33 row_newbcast:1 row_mask:0xf bank_mask:0xf\n"
"v_mul_f32_dpp v34, v158, v34 row_newbcast:2 row_mask:0xf bank_mask:0xf\n"
"v_mul_f32_dpp v35, v158, v35 row_newbcast:3 row_mask:0xf bank_mask:0xf\n"
"v_mul_f32_dpp v36, v158, v36 row_newbcast:0 row_mask:0xf bank_mask:0xf\n"
"v_mul_f32_dpp v37, v158, v37 row_newbcast:1 row_mask:0xf bank_mask:0xf\n"
"v_mul_f32_dpp v38, v158, v38 row_newbcast:2 row_mask:0xf bank_mask:0xf\n"
"v_mul_f32_dpp v39, v158, v39 row_newbcast:3 row_mask:0xf bank_mask:0xf\n"
"v_mul_f32_dpp v40, v158, v40 row_newbcast:0 row_mask:0xf bank_mask:0xf\n"
"v_mul_f32_dpp v41, v158, v41 row_newbcast:1 row_mask:0xf bank_mask:0xf\n"
"v_mul_f32_dpp v42, v158, v42 row_newbcast:2 row_mask:0xf bank_mask:0xf\n"
"v_mul_f32_dpp v43, v158, v43 row_newbcast:3 row_mask:0xf bank_mask:0xf\n"
"v_mul_f32_dpp v44, v158, v44 row_newbcast:0 row_mask:0xf bank_mask:0xf\n"
"v_mul_f32_dpp v45, v158, v45 row_newbcast:1 row_mask:0xf bank_mask:0xf\n"
"v_mul_f32_dpp v46, v158, v46 row_newbcast:2 row_mask:0xf bank_mask:0xf\n"
"v_mul_f32_dpp v47, v158, v47 row_newbcast:3 row_mask:0xf bank_mask:0xf\n"
"v_mul_f32_dpp v48, v158, v48 row_newbcast:0 row_mask:0xf bank_mask:0xf\n"
"v_mul_f32_dpp v49, v158, v49 row_newbcast:1 row_mask:0xf bank_mask:0xf\n"
"v_mul_f32_dpp v50, v158, v50 row_newbcast:2 row_mask:0xf bank_mask:0xf\n"
"v_mul_f32_dpp v51, v158, v51 row_newbcast:3 row_mask:0xf bank_mask:0xf\n"
"v_mul_f32_dpp v52, v158, v52 row_newbcast:0 row_mask:0xf bank_mask:0xf\n"
"v_mul_f32_dpp v53, v158, v53 row_newbcast:1 row_mask:0xf bank_mask:0xf\n"
"v_mul_f32_dpp v54, v158, v54 row_newbcast:2 row_mask:0xf bank_mask:0xf\n"
"v_mul_f32_dpp v55, v158, v55 row_newbcast:3 row_mask:0xf bank_mask:0xf\n"
"v_mul_f32_dpp v56, v158, v56 row_newbcast:0 row_mask:0xf bank_mask:0xf\n"
"v_mul_f32_dpp v57, v158, v57 row_newbcast:1 row_mask:0xf bank_mask:0xf\n"
"v_mul_f32_dpp v58, v158, v58 row_newbcast:2 row_mask:0xf bank_mask:0xf\n"
"v_mul_f32_dpp v59, v158, v59 row_newbcast:3 row_mask:0xf bank_mask:0xf\n"
"v_mul_f32_dpp v60, v158, v60 row_newbcast:0 row_mask:0xf bank_mask:0xf\n"
"v_mul_f32_dpp v61, v158, v61 row_newbcast:1 row_mask:0xf bank_mask:0xf\n"
"v_mul_f32_dpp v62, v158, v62 row_newbcast:2 row_mask:0xf bank_mask:0xf\n"
"v_mul_f32_dpp v63, v158, v63 row_newbcast:3 row_mask:0xf bank_mask:0xf\n"
"v_max3_f32 v161, v112, v113, v114\n"
"v_max3_f32 v161, v161, v115, v116\n"
"v_max3_f32 v161, v161, v117, v118\n"
"v_max3_f32 v161, v161, v119, v160\n"
"v_mov_b32 v162, v161\n"
"s_nop 1\n"
"v_permlane32_swap_b32 v161, v162\n"
"v_max_f32 v161, v161, v162\n"
"v_mov_b32 v162, v161\n"
"s_nop 1\n"
"v_permlane16_swap_b32 v161, v162\n"
"v_max_f32 v161, v161, v162\n"
"v_sub_f32 v162, v160, v161\n"
"v_exp_f32 v162, v162\n"
"v_mov_b32 v160, v161\n"
"v_sub_f32 v112, v112, v160\n"
"v_sub_f32 v113, v113, v160\n"
"v_sub_f32 v114, v114, v160\n"
"v_sub_f32 v115, v115, v160\n"
"v_sub_f32 v116, v116, v160\n"
"v_sub_f32 v117, v117, v160\n"
"v_sub_f32 v118, v118, v160\n"
"v_sub_f32 v119, v119, v160\n"
"v_exp_f32 v112, v112\n"
"v_exp_f32 v113, v113\n"
"v_exp_f32 v114, v114\n"
"v_exp_f32 v115, v115\n"
"v_exp_f32 v116, v116\n"
"v_exp_f32 v117, v117\n"
"v_exp_f32 v118, v118\n"
"v_exp_f32 v119, v119\n"
"v_mul_f32 v163, v163, v162\n"
"v_add_f32 v163, v163, v112\n"
"v_add_f32 v163, v163, v113\n"
"v_add_f32 v163, v163, v114\n"
"v_add_f32 v163, v163, v115\n"
"v_add_f32 v163, v163, v116\n"
"v_add_f32 v163, v163, v117\n"
"v_add_f32 v163, v163, v118\n"
"v_add_f32 v163, v163, v119\n"
"v_cvt_pk_bf16_f32 v112, v112, v113\n"
"v_cvt_pk_bf16_f32 v113, v114, v115\n"
"v_cvt_pk_bf16_f32 v114, v116, v117\n"
"v_cvt_pk_bf16_f32 v115, v118, v119\n"
"v_mov_b32_dpp v162, v162 row_ror:12 row_mask:0x2 bank_mask:0xf\n"
"s_nop 1\n"
"v_mov_b32_dpp v162, v162 row_ror:8 row_mask:0x4 bank_mask:0xf\n"
"s_nop 1\n"
"v_mov_b32_dpp v162, v162 row_ror:4 row_mask:0x8 bank_mask:0xf\n"
"s_nop 1\n"
"v_mul_f32_dpp v64, v162, v64 row_newbcast:0 row_mask:0xf bank_mask:0xf\n"
"v_mul_f32_dpp v65, v162, v65 row_newbcast:1 row_mask:0xf bank_mask:0xf\n"
"v_mul_f32_dpp v66, v162, v66 row_newbcast:2 row_mask:0xf bank_mask:0xf\n"
"v_mul_f32_dpp v67, v162, v67 row_newbcast:3 row_mask:0xf bank_mask:0xf\n"
"v_mul_f32_dpp v68, v162, v68 row_newbcast:0 row_mask:0xf bank_mask:0xf\n"
"v_mul_f32_dpp v69, v162, v69 row_newbcast:1 row_mask:0xf bank_mask:0xf\n"
"v_mul_f32_dpp v70, v162, v70 row_newbcast:2 row_mask:0xf bank_mask:0xf\n"
"v_mul_f32_dpp v71, v162, v71 row_newbcast:3 row_mask:0xf bank_mask:0xf\n"
"v_mul_f32_dpp v72, v162, v72 row_newbcast:0 row_mask:0xf bank_mask:0xf\n"
"v_mul_f32_dpp v73, v162, v73 row_newbcast:1 row_mask:0xf bank_mask:0xf\n"
"v_mul_f32_dpp v74, v162, v74 row_newbcast:2 row_mask:0xf bank_mask:0xf\n"
"v_mul_f32_dpp v75, v162, v75 row_newbcast:3 row_mask:0xf bank_mask:0xf\n"
"v_mul_f32_dpp v76, v162, v76 row_newbcast:0 row_mask:0xf bank_mask:0xf\n"
"v_mul_f32_dpp v77, v162, v77 row_newbcast:1 row_mask:0xf bank_mask:0xf\n"
"v_mul_f32_dpp v78, v162, v78 row_newbcast:2 row_mask:0xf bank_mask:0xf\n"
"v_mul_f32_dpp v79, v162, v79 row_newbcast:3 row_mask:0xf bank_mask:0xf\n"
"v_mul_f32_dpp v80, v162, v80 row_newbcast:0 row_mask:0xf bank_mask:0xf\n"
"v_mul_f32_dpp v81, v162, v81 row_newbcast:1 row_mask:0xf bank_mask:0xf\n"
"v_mul_f32_dpp v82, v162, v82 row_newbcast:2 row_mask:0xf bank_mask:0xf\n"
"v_mul_f32_dpp v83, v162, v83 row_newbcast:3 row_mask:0xf bank_mask:0xf\n"
"v_mul_f32_dpp v84, v162, v84 row_newbcast:0 row_mask:0xf bank_mask:0xf\n"
"v_mul_f32_dpp v85, v162, v85 row_newbcast:1 row_mask:0xf bank_mask:0xf\n"
"v_mul_f32_dpp v86, v162, v86 row_newbcast:2 row_mask:0xf bank_mask:0xf\n"
"v_mul_f32_dpp v87, v162, v87 row_newbcast:3 row_mask:0xf bank_mask:0xf\n"
"v_mul_f32_dpp v88, v162, v88 row_newbcast:0 row_mask:0xf bank_mask:0xf\n"
"v_mul_f32_dpp v89, v162, v89 row_newbcast:1 row_mask:0xf bank_mask:0xf\n"
"v_mul_f32_dpp v90, v162, v90 row_newbcast:2 row_mask:0xf bank_mask:0xf\n"
"v_mul_f32_dpp v91, v162, v91 row_newbcast:3 row_mask:0xf bank_mask:0xf\n"
"v_mul_f32_dpp v92, v162, v92 row_newbcast:0 row_mask:0xf bank_mask:0xf\n"
"v_mul_f32_dpp v93, v162, v93 row_newbcast:1 row_mask:0xf bank_mask:0xf\n"
"v_mul_f32_dpp v94, v162, v94 row_newbcast:2 row_mask:0xf bank_mask:0xf\n"
"v_mul_f32_dpp v95, v162, v95 row_newbcast:3 row_mask:0xf bank_mask:0xf\n"
"s_cmp_lg_u32 s83, s43\n"
"s_cbranch_scc1 LSK3A_%=\n"
"v_max3_f32 v165, v120, v121, v122\n"
"v_max3_f32 v165, v165, v123, v124\n"
"v_max3_f32 v165, v165, v125, v126\n"
"v_max3_f32 v165, v165, v127, v164\n"
"v_mov_b32 v166, v165\n"
"s_nop 1\n"
"v_permlane32_swap_b32 v165, v166\n"
"v_max_f32 v165, v165, v166\n"
"v_mov_b32 v166, v165\n"
"s_nop 1\n"
"v_permlane16_swap_b32 v165, v166\n"
"v_max_f32 v165, v165, v166\n"
"v_sub_f32 v166, v164, v165\n"
"v_exp_f32 v166, v166\n"
"v_mov_b32 v164, v165\n"
"v_sub_f32 v120, v120, v164\n"
"v_sub_f32 v121, v121, v164\n"
"v_sub_f32 v122, v122, v164\n"
"v_sub_f32 v123, v123, v164\n"
"v_sub_f32 v124, v124, v164\n"
"v_sub_f32 v125, v125, v164\n"
"v_sub_f32 v126, v126, v164\n"
"v_sub_f32 v127, v127, v164\n"
"v_exp_f32 v120, v120\n"
"v_exp_f32 v121, v121\n"
"v_exp_f32 v122, v122\n"
"v_exp_f32 v123, v123\n"
"v_exp_f32 v124, v124\n"
"v_exp_f32 v125, v125\n"
"v_exp_f32 v126, v126\n"
"v_exp_f32 v127, v127\n"
"v_mul_f32 v167, v167, v166\n"
"v_add_f32 v167, v167, v120\n"
"v_add_f32 v167, v167, v121\n"
"v_add_f32 v167, v167, v122\n"
"v_add_f32 v167, v167, v123\n"
"v_add_f32 v167, v167, v124\n"
"v_add_f32 v167, v167, v125\n"
"v_add_f32 v167, v167, v126\n"
"v_add_f32 v167, v167, v127\n"
"v_cvt_pk_bf16_f32 v120, v120, v121\n"
"v_cvt_pk_bf16_f32 v121, v122, v123\n"
"v_cvt_pk_bf16_f32 v122, v124, v125\n"
"v_cvt_pk_bf16_f32 v123, v126, v127\n"
"v_mov_b32_dpp v166, v166 row_ror:12 row_mask:0x2 bank_mask:0xf\n"
"s_nop 1\n"
"v_mov_b32_dpp v166, v166 row_ror:8 row_mask:0x4 bank_mask:0xf\n"
"s_nop 1\n"
"v_mov_b32_dpp v166, v166 row_ror:4 row_mask:0x8 bank_mask:0xf\n"
"v_accvgpr_read_b32 v124, a208\n"
"s_nop 1\n"
"v_mul_f32_dpp v124, v166, v124 row_newbcast:0 row_mask:0xf bank_mask:0xf\n"
"v_accvgpr_write_b32 a208, v124\n"
"v_accvgpr_read_b32 v125, a209\n"
"s_nop 1\n"
"v_mul_f32_dpp v125, v166, v125 row_newbcast:1 row_mask:0xf bank_mask:0xf\n"
"v_accvgpr_write_b32 a209, v125\n"
"v_accvgpr_read_b32 v126, a210\n"
"s_nop 1\n"
"v_mul_f32_dpp v126, v166, v126 row_newbcast:2 row_mask:0xf bank_mask:0xf\n"
"v_accvgpr_write_b32 a210, v126\n"
"v_accvgpr_read_b32 v127, a211\n"
"s_nop 1\n"
"v_mul_f32_dpp v127, v166, v127 row_newbcast:3 row_mask:0xf bank_mask:0xf\n"
"v_accvgpr_write_b32 a211, v127\n"
"v_accvgpr_read_b32 v124, a212\n"
"s_nop 1\n"
"v_mul_f32_dpp v124, v166, v124 row_newbcast:0 row_mask:0xf bank_mask:0xf\n"
"v_accvgpr_write_b32 a212, v124\n"
"v_accvgpr_read_b32 v125, a213\n"
"s_nop 1\n"
"v_mul_f32_dpp v125, v166, v125 row_newbcast:1 row_mask:0xf bank_mask:0xf\n"
"v_accvgpr_write_b32 a213, v125\n"
"v_accvgpr_read_b32 v126, a214\n"
"s_nop 1\n"
"v_mul_f32_dpp v126, v166, v126 row_newbcast:2 row_mask:0xf bank_mask:0xf\n"
"v_accvgpr_write_b32 a214, v126\n"
"v_accvgpr_read_b32 v127, a215\n"
"s_nop 1\n"
"v_mul_f32_dpp v127, v166, v127 row_newbcast:3 row_mask:0xf bank_mask:0xf\n"
"v_accvgpr_write_b32 a215, v127\n"
"v_accvgpr_read_b32 v124, a216\n"
"s_nop 1\n"
"v_mul_f32_dpp v124, v166, v124 row_newbcast:0 row_mask:0xf bank_mask:0xf\n"
"v_accvgpr_write_b32 a216, v124\n"
"v_accvgpr_read_b32 v125, a217\n"
"s_nop 1\n"
"v_mul_f32_dpp v125, v166, v125 row_newbcast:1 row_mask:0xf bank_mask:0xf\n"
"v_accvgpr_write_b32 a217, v125\n"
"v_accvgpr_read_b32 v126, a218\n"
"s_nop 1\n"
"v_mul_f32_dpp v126, v166, v126 row_newbcast:2 row_mask:0xf bank_mask:0xf\n"
"v_accvgpr_write_b32 a218, v126\n"
"v_accvgpr_read_b32 v127, a219\n"
"s_nop 1\n"
"v_mul_f32_dpp v127, v166, v127 row_newbcast:3 row_mask:0xf bank_mask:0xf\n"
"v_accvgpr_write_b32 a219, v127\n"
"v_accvgpr_read_b32 v124, a220\n"
"s_nop 1\n"
"v_mul_f32_dpp v124, v166, v124 row_newbcast:0 row_mask:0xf bank_mask:0xf\n"
"v_accvgpr_write_b32 a220, v124\n"
"v_accvgpr_read_b32 v125, a221\n"
"s_nop 1\n"
"v_mul_f32_dpp v125, v166, v125 row_newbcast:1 row_mask:0xf bank_mask:0xf\n"
"v_accvgpr_write_b32 a221, v125\n"
"v_accvgpr_read_b32 v126, a222\n"
"s_nop 1\n"
"v_mul_f32_dpp v126, v166, v126 row_newbcast:2 row_mask:0xf bank_mask:0xf\n"
"v_accvgpr_write_b32 a222, v126\n"
"v_accvgpr_read_b32 v127, a223\n"
"s_nop 1\n"
"v_mul_f32_dpp v127, v166, v127 row_newbcast:3 row_mask:0xf bank_mask:0xf\n"
"v_accvgpr_write_b32 a223, v127\n"
"v_accvgpr_read_b32 v124, a224\n"
"s_nop 1\n"
"v_mul_f32_dpp v124, v166, v124 row_newbcast:0 row_mask:0xf bank_mask:0xf\n"
"v_accvgpr_write_b32 a224, v124\n"
"v_accvgpr_read_b32 v125, a225\n"
"s_nop 1\n"
"v_mul_f32_dpp v125, v166, v125 row_newbcast:1 row_mask:0xf bank_mask:0xf\n"
"v_accvgpr_write_b32 a225, v125\n"
"v_accvgpr_read_b32 v126, a226\n"
"s_nop 1\n"
"v_mul_f32_dpp v126, v166, v126 row_newbcast:2 row_mask:0xf bank_mask:0xf\n"
"v_accvgpr_write_b32 a226, v126\n"
"v_accvgpr_read_b32 v127, a227\n"
"s_nop 1\n"
"v_mul_f32_dpp v127, v166, v127 row_newbcast:3 row_mask:0xf bank_mask:0xf\n"
"v_accvgpr_write_b32 a227, v127\n"
"v_accvgpr_read_b32 v124, a228\n"
"s_nop 1\n"
"v_mul_f32_dpp v124, v166, v124 row_newbcast:0 row_mask:0xf bank_mask:0xf\n"
"v_accvgpr_write_b32 a228, v124\n"
"v_accvgpr_read_b32 v125, a229\n"
"s_nop 1\n"
"v_mul_f32_dpp v125, v166, v125 row_newbcast:1 row_mask:0xf bank_mask:0xf\n"
"v_accvgpr_write_b32 a229, v125\n"
"v_accvgpr_read_b32 v126, a230\n"
"s_nop 1\n"
"v_mul_f32_dpp v126, v166, v126 row_newbcast:2 row_mask:0xf bank_mask:0xf\n"
"v_accvgpr_write_b32 a230, v126\n"
"v_accvgpr_read_b32 v127, a231\n"
"s_nop 1\n"
"v_mul_f32_dpp v127, v166, v127 row_newbcast:3 row_mask:0xf bank_mask:0xf\n"
"v_accvgpr_write_b32 a231, v127\n"
"v_accvgpr_read_b32 v124, a232\n"
"s_nop 1\n"
"v_mul_f32_dpp v124, v166, v124 row_newbcast:0 row_mask:0xf bank_mask:0xf\n"
"v_accvgpr_write_b32 a232, v124\n"
"v_accvgpr_read_b32 v125, a233\n"
"s_nop 1\n"
"v_mul_f32_dpp v125, v166, v125 row_newbcast:1 row_mask:0xf bank_mask:0xf\n"
"v_accvgpr_write_b32 a233, v125\n"
"v_accvgpr_read_b32 v126, a234\n"
"s_nop 1\n"
"v_mul_f32_dpp v126, v166, v126 row_newbcast:2 row_mask:0xf bank_mask:0xf\n"
"v_accvgpr_write_b32 a234, v126\n"
"v_accvgpr_read_b32 v127, a235\n"
"s_nop 1\n"
"v_mul_f32_dpp v127, v166, v127 row_newbcast:3 row_mask:0xf bank_mask:0xf\n"
"v_accvgpr_write_b32 a235, v127\n"
"v_accvgpr_read_b32 v124, a236\n"
"s_nop 1\n"
"v_mul_f32_dpp v124, v166, v124 row_newbcast:0 row_mask:0xf bank_mask:0xf\n"
"v_accvgpr_write_b32 a236, v124\n"
"v_accvgpr_read_b32 v125, a237\n"
"s_nop 1\n"
"v_mul_f32_dpp v125, v166, v125 row_newbcast:1 row_mask:0xf bank_mask:0xf\n"
"v_accvgpr_write_b32 a237, v125\n"
"v_accvgpr_read_b32 v126, a238\n"
"s_nop 1\n"
"v_mul_f32_dpp v126, v166, v126 row_newbcast:2 row_mask:0xf bank_mask:0xf\n"
"v_accvgpr_write_b32 a238, v126\n"
"v_accvgpr_read_b32 v127, a239\n"
"s_nop 1\n"
"v_mul_f32_dpp v127, v166, v127 row_newbcast:3 row_mask:0xf bank_mask:0xf\n"
"v_accvgpr_write_b32 a239, v127\n"
"LSK3A_%=:\n"
"s_nop 3\n"
"buffer_load_ushort v128, v146, s[88:91], s80 offen offset:0\n"
"buffer_load_ushort v129, v146, s[88:91], s80 offen offset:256\n"
"buffer_load_ushort v130, v146, s[88:91], s80 offen offset:512\n"
"buffer_load_ushort v131, v146, s[88:91], s80 offen offset:768\n"
"buffer_load_ushort v132, v146, s[88:91], s81 offen offset:0\n"
"buffer_load_ushort v133, v146, s[88:91], s81 offen offset:256\n"
"buffer_load_ushort v134, v146, s[88:91], s81 offen offset:512\n"
"buffer_load_ushort v135, v146, s[88:91], s81 offen offset:768\n"
"buffer_load_ushort v136, v146, s[88:91], s80 offen offset:32\n"
"buffer_load_ushort v137, v146, s[88:91], s80 offen offset:288\n"
"buffer_load_ushort v138, v146, s[88:91], s80 offen offset:544\n"
"buffer_load_ushort v139, v146, s[88:91], s80 offen offset:800\n"
"buffer_load_ushort v140, v146, s[88:91], s81 offen offset:32\n"
"buffer_load_ushort v141, v146, s[88:91], s81 offen offset:288\n"
"buffer_load_ushort v142, v146, s[88:91], s81 offen offset:544\n"
"buffer_load_ushort v143, v146, s[88:91], s81 offen offset:800\n"
"s_waitcnt vmcnt(0)\n"
"v_lshl_or_b32 v128, v129, 16, v128\n"
"v_lshl_or_b32 v129, v131, 16, v130\n"
"v_lshl_or_b32 v130, v133, 16, v132\n"
"v_lshl_or_b32 v131, v135, 16, v134\n"
"v_lshl_or_b32 v136, v137, 16, v136\n"
"v_lshl_or_b32 v137, v139, 16, v138\n"
"v_lshl_or_b32 v138, v141, 16, v140\n"
"v_lshl_or_b32 v139, v143, 16, v142\n"
"v_mfma_f32_16x16x32_bf16 v[0:3], v[96:99], v[128:131], v[0:3]\n"
"s_nop 0\n"
"v_mfma_f32_16x16x32_bf16 v[4:7], v[96:99], v[136:139], v[4:7]\n"
"v_mfma_f32_16x16x32_bf16 v[32:35], v[104:107], v[128:131], v[32:35]\n"
"v_mfma_f32_16x16x32_bf16 v[36:39], v[104:107], v[136:139], v[36:39]\n"
"v_mfma_f32_16x16x32_bf16 v[64:67], v[112:115], v[128:131], v[64:67]\n"
"v_mfma_f32_16x16x32_bf16 v[68:71], v[112:115], v[136:139], v[68:71]\n"
"s_cmp_lg_u32 s83, s43\n"
"s_cbranch_scc1 LSK3B0_%=\n"
"v_mfma_f32_16x16x32_bf16 a[208:211], v[120:123], v[128:131], a[208:211]\n"
"v_mfma_f32_16x16x32_bf16 a[212:215], v[120:123], v[136:139], a[212:215]\n"
"LSK3B0_%=:\n"
"s_nop 3\n"
"buffer_load_ushort v128, v147, s[88:91], s80 offen offset:0\n"
"buffer_load_ushort v129, v147, s[88:91], s80 offen offset:256\n"
"buffer_load_ushort v130, v147, s[88:91], s80 offen offset:512\n"
"buffer_load_ushort v131, v147, s[88:91], s80 offen offset:768\n"
"buffer_load_ushort v132, v147, s[88:91], s81 offen offset:0\n"
"buffer_load_ushort v133, v147, s[88:91], s81 offen offset:256\n"
"buffer_load_ushort v134, v147, s[88:91], s81 offen offset:512\n"
"buffer_load_ushort v135, v147, s[88:91], s81 offen offset:768\n"
"buffer_load_ushort v136, v147, s[88:91], s80 offen offset:32\n"
"buffer_load_ushort v137, v147, s[88:91], s80 offen offset:288\n"
"buffer_load_ushort v138, v147, s[88:91], s80 offen offset:544\n"
"buffer_load_ushort v139, v147, s[88:91], s80 offen offset:800\n"
"buffer_load_ushort v140, v147, s[88:91], s81 offen offset:32\n"
"buffer_load_ushort v141, v147, s[88:91], s81 offen offset:288\n"
"buffer_load_ushort v142, v147, s[88:91], s81 offen offset:544\n"
"buffer_load_ushort v143, v147, s[88:91], s81 offen offset:800\n"
"s_waitcnt vmcnt(0)\n"
"v_lshl_or_b32 v128, v129, 16, v128\n"
"v_lshl_or_b32 v129, v131, 16, v130\n"
"v_lshl_or_b32 v130, v133, 16, v132\n"
"v_lshl_or_b32 v131, v135, 16, v134\n"
"v_lshl_or_b32 v136, v137, 16, v136\n"
"v_lshl_or_b32 v137, v139, 16, v138\n"
"v_lshl_or_b32 v138, v141, 16, v140\n"
"v_lshl_or_b32 v139, v143, 16, v142\n"
"v_mfma_f32_16x16x32_bf16 v[8:11], v[96:99], v[128:131], v[8:11]\n"
"s_nop 0\n"
"v_mfma_f32_16x16x32_bf16 v[12:15], v[96:99], v[136:139], v[12:15]\n"
"v_mfma_f32_16x16x32_bf16 v[40:43], v[104:107], v[128:131], v[40:43]\n"
"v_mfma_f32_16x16x32_bf16 v[44:47], v[104:107], v[136:139], v[44:47]\n"
"v_mfma_f32_16x16x32_bf16 v[72:75], v[112:115], v[128:131], v[72:75]\n"
"v_mfma_f32_16x16x32_bf16 v[76:79], v[112:115], v[136:139], v[76:79]\n"
"s_cmp_lg_u32 s83, s43\n"
"s_cbranch_scc1 LSK3B1_%=\n"
"v_mfma_f32_16x16x32_bf16 a[216:219], v[120:123], v[128:131], a[216:219]\n"
"v_mfma_f32_16x16x32_bf16 a[220:223], v[120:123], v[136:139], a[220:223]\n"
"LSK3B1_%=:\n"
"s_nop 3\n"
"buffer_load_ushort v128, v146, s[88:91], s80 offen offset:128\n"
"buffer_load_ushort v129, v146, s[88:91], s80 offen offset:384\n"
"buffer_load_ushort v130, v146, s[88:91], s80 offen offset:640\n"
"buffer_load_ushort v131, v146, s[88:91], s80 offen offset:896\n"
"buffer_load_ushort v132, v146, s[88:91], s81 offen offset:128\n"
"buffer_load_ushort v133, v146, s[88:91], s81 offen offset:384\n"
"buffer_load_ushort v134, v146, s[88:91], s81 offen offset:640\n"
"buffer_load_ushort v135, v146, s[88:91], s81 offen offset:896\n"
"buffer_load_ushort v136, v146, s[88:91], s80 offen offset:160\n"
"buffer_load_ushort v137, v146, s[88:91], s80 offen offset:416\n"
"buffer_load_ushort v138, v146, s[88:91], s80 offen offset:672\n"
"buffer_load_ushort v139, v146, s[88:91], s80 offen offset:928\n"
"buffer_load_ushort v140, v146, s[88:91], s81 offen offset:160\n"
"buffer_load_ushort v141, v146, s[88:91], s81 offen offset:416\n"
"buffer_load_ushort v142, v146, s[88:91], s81 offen offset:672\n"
"buffer_load_ushort v143, v146, s[88:91], s81 offen offset:928\n"
"s_waitcnt vmcnt(0)\n"
"v_lshl_or_b32 v128, v129, 16, v128\n"
"v_lshl_or_b32 v129, v131, 16, v130\n"
"v_lshl_or_b32 v130, v133, 16, v132\n"
"v_lshl_or_b32 v131, v135, 16, v134\n"
"v_lshl_or_b32 v136, v137, 16, v136\n"
"v_lshl_or_b32 v137, v139, 16, v138\n"
"v_lshl_or_b32 v138, v141, 16, v140\n"
"v_lshl_or_b32 v139, v143, 16, v142\n"
"v_mfma_f32_16x16x32_bf16 v[16:19], v[96:99], v[128:131], v[16:19]\n"
"s_nop 0\n"
"v_mfma_f32_16x16x32_bf16 v[20:23], v[96:99], v[136:139], v[20:23]\n"
"v_mfma_f32_16x16x32_bf16 v[48:51], v[104:107], v[128:131], v[48:51]\n"
"v_mfma_f32_16x16x32_bf16 v[52:55], v[104:107], v[136:139], v[52:55]\n"
"v_mfma_f32_16x16x32_bf16 v[80:83], v[112:115], v[128:131], v[80:83]\n"
"v_mfma_f32_16x16x32_bf16 v[84:87], v[112:115], v[136:139], v[84:87]\n"
"s_cmp_lg_u32 s83, s43\n"
"s_cbranch_scc1 LSK3B2_%=\n"
"v_mfma_f32_16x16x32_bf16 a[224:227], v[120:123], v[128:131], a[224:227]\n"
"v_mfma_f32_16x16x32_bf16 a[228:231], v[120:123], v[136:139], a[228:231]\n"
"LSK3B2_%=:\n"
"s_nop 3\n"
"buffer_load_ushort v128, v147, s[88:91], s80 offen offset:128\n"
"buffer_load_ushort v129, v147, s[88:91], s80 offen offset:384\n"
"buffer_load_ushort v130, v147, s[88:91], s80 offen offset:640\n"
"buffer_load_ushort v131, v147, s[88:91], s80 offen offset:896\n"
"buffer_load_ushort v132, v147, s[88:91], s81 offen offset:128\n"
"buffer_load_ushort v133, v147, s[88:91], s81 offen offset:384\n"
"buffer_load_ushort v134, v147, s[88:91], s81 offen offset:640\n"
"buffer_load_ushort v135, v147, s[88:91], s81 offen offset:896\n"
"buffer_load_ushort v136, v147, s[88:91], s80 offen offset:160\n"
"buffer_load_ushort v137, v147, s[88:91], s80 offen offset:416\n"
"buffer_load_ushort v138, v147, s[88:91], s80 offen offset:672\n"
"buffer_load_ushort v139, v147, s[88:91], s80 offen offset:928\n"
"buffer_load_ushort v140, v147, s[88:91], s81 offen offset:160\n"
"buffer_load_ushort v141, v147, s[88:91], s81 offen offset:416\n"
"buffer_load_ushort v142, v147, s[88:91], s81 offen offset:672\n"
"buffer_load_ushort v143, v147, s[88:91], s81 offen offset:928\n"
"s_waitcnt vmcnt(0)\n"
"v_lshl_or_b32 v128, v129, 16, v128\n"
"v_lshl_or_b32 v129, v131, 16, v130\n"
"v_lshl_or_b32 v130, v133, 16, v132\n"
"v_lshl_or_b32 v131, v135, 16, v134\n"
"v_lshl_or_b32 v136, v137, 16, v136\n"
"v_lshl_or_b32 v137, v139, 16, v138\n"
"v_lshl_or_b32 v138, v141, 16, v140\n"
"v_lshl_or_b32 v139, v143, 16, v142\n"
"v_mfma_f32_16x16x32_bf16 v[24:27], v[96:99], v[128:131], v[24:27]\n"
"s_nop 0\n"
"v_mfma_f32_16x16x32_bf16 v[28:31], v[96:99], v[136:139], v[28:31]\n"
"v_mfma_f32_16x16x32_bf16 v[56:59], v[104:107], v[128:131], v[56:59]\n"
"v_mfma_f32_16x16x32_bf16 v[60:63], v[104:107], v[136:139], v[60:63]\n"
"v_mfma_f32_16x16x32_bf16 v[88:91], v[112:115], v[128:131], v[88:91]\n"
"v_mfma_f32_16x16x32_bf16 v[92:95], v[112:115], v[136:139], v[92:95]\n"
"s_cmp_lg_u32 s83, s43\n"
"s_cbranch_scc1 LSK3B3_%=\n"
"v_mfma_f32_16x16x32_bf16 a[232:235], v[120:123], v[128:131], a[232:235]\n"
"v_mfma_f32_16x16x32_bf16 a[236:239], v[120:123], v[136:139], a[236:239]\n"
"LSK3B3_%=:\n"
"s_nop 3\n"
"s_nop 9\n"
"s_add_u32 s80, s80, 8192\n"
"s_add_u32 s81, s81, 8192\n"
"s_add_u32 s82, s82, 1\n"
"s_cmp_lt_u32 s82, 32\n"
"s_cbranch_scc1 LSLOOP_%=\n"
"v_mov_b32 v154, v155\n"
"s_nop 1\n"
"v_permlane32_swap_b32 v155, v154\n"
"v_add_f32 v155, v155, v154\n"
"v_mov_b32 v154, v155\n"
"s_nop 1\n"
"v_permlane16_swap_b32 v155, v154\n"
"v_add_f32 v155, v155, v154\n"
"v_mov_b32 v196, v155\n"
"v_mov_b32 v158, v159\n"
"s_nop 1\n"
"v_permlane32_swap_b32 v159, v158\n"
"v_add_f32 v159, v159, v158\n"
"v_mov_b32 v158, v159\n"
"s_nop 1\n"
"v_permlane16_swap_b32 v159, v158\n"
"v_add_f32 v159, v159, v158\n"
"v_mov_b32 v198, v159\n"
"v_mov_b32 v162, v163\n"
"s_nop 1\n"
"v_permlane32_swap_b32 v163, v162\n"
"v_add_f32 v163, v163, v162\n"
"v_mov_b32 v162, v163\n"
"s_nop 1\n"
"v_permlane16_swap_b32 v163, v162\n"
"v_add_f32 v163, v163, v162\n"
"v_mov_b32 v200, v163\n"
"v_mov_b32 v166, v167\n"
"s_nop 1\n"
"v_permlane32_swap_b32 v167, v166\n"
"v_add_f32 v167, v167, v166\n"
"v_mov_b32 v166, v167\n"
"s_nop 1\n"
"v_permlane16_swap_b32 v167, v166\n"
"v_add_f32 v167, v167, v166\n"
"v_mov_b32 v202, v167\n"
"v_sub_f32 v192, 0, v164\n"
"v_mov_b32 v96, 0\n"
"v_mov_b32 v97, 0\n"
"v_mov_b32 v98, 0\n"
"v_mov_b32 v99, 0\n"
"v_mov_b32 v100, 0\n"
"v_mov_b32 v101, 0\n"
"v_mov_b32 v102, 0\n"
"v_mov_b32 v103, 0\n"
"v_mov_b32 v104, 0\n"
"v_mov_b32 v105, 0\n"
"v_mov_b32 v106, 0\n"
"v_mov_b32 v107, 0\n"
"v_mov_b32 v108, 0\n"
"v_mov_b32 v109, 0\n"
"v_mov_b32 v110, 0\n"
"v_mov_b32 v111, 0\n"
"v_mov_b32 v112, 0\n"
"v_mov_b32 v113, 0\n"
"v_mov_b32 v114, 0\n"
"v_mov_b32 v115, 0\n"
"v_mov_b32 v116, 0\n"
"v_mov_b32 v117, 0\n"
"v_mov_b32 v118, 0\n"
"v_mov_b32 v119, 0\n"
"v_mov_b32 v120, 0\n"
"v_mov_b32 v121, 0\n"
"v_mov_b32 v122, 0\n"
"v_mov_b32 v123, 0\n"
"v_mov_b32 v204, 0\n"
"v_mov_b32 v205, 0\n"
"s_nop 3\n"
"s_mov_b32 s68, 0\n"
"s_cmp_eq_u32 s47, 4\n"
"s_cbranch_scc1 LBACK0_%=\n"
"s_cmp_eq_u32 s47, 8\n"
"s_cbranch_scc1 LBACK1_%=\n"
"s_cmp_eq_u32 s47, 12\n"
"s_cbranch_scc1 LBACK2_%=\n"
"s_branch LBACK3_%=\n"
"LFIN_%=:\n"
    :: [kalo]"s"((unsigned)ka), [kahi]"s"((unsigned)(ka >> 32)), [wg]"s"(blockIdx.x), [wid]"s"(wid)
    : "v0","v1","v2","v3","v4","v5","v6","v7","v8","v9","v10","v11","v12","v13","v14","v15","v16","v17","v18","v19","v20","v21","v22","v23","v24","v25","v26","v27","v28","v29","v30","v31","v32","v33","v34","v35","v36","v37","v38","v39","v40","v41","v42","v43","v44","v45","v46","v47","v48","v49","v50","v51","v52","v53","v54","v55","v56","v57","v58","v59","v60","v61","v62","v63","v64","v65","v66","v67","v68","v69","v70","v71","v72","v73","v74","v75","v76","v77","v78","v79","v80","v81","v82","v83","v84","v85","v86","v87","v88","v89","v90","v91","v92","v93","v94","v95","v96","v97","v98","v99","v100","v101","v102","v103","v104","v105","v106","v107","v108","v109","v110","v111","v112","v113","v114","v115","v116","v117","v118","v119","v120","v121","v122","v123","v124","v125","v126","v127","v128","v129","v130","v131","v132","v133","v134","v135","v136","v137","v138","v139","v140","v141","v142","v143","v144","v145","v146","v147","v148","v149","v150","v151","v152","v153","v154","v155","v156","v157","v158","v159","v160","v161","v162","v163","v164","v165","v166","v167","v168","v169","v170","v171","v172","v173","v174","v175","v176","v177","v178","v179","v180","v181","v182","v183","v184","v185","v186","v187","v188","v189","v190","v191","v192","v193","v194","v195","v196","v197","v198","v199","v200","v201","v202","v203","v204","v205","v206","v207","v208","v209","v210","v211","v212","v213","v214","v215","v216","v217","v218","v219","v220","v221","v222","v223","v224","v225","v226","v227","v228","v229","v230","v231","v232","v233","v234","v235","v236","v237","v238","v239","v240","v241","v242","v243","v244","v245","v246","v247","v248","v249","v250","v251","v252","v253","v254","v255","a0","a1","a2","a3","a4","a5","a6","a7","a8","a9","a10","a11","a12","a13","a14","a15","a16","a17","a18","a19","a20","a21","a22","a23","a24","a25","a26","a27","a28","a29","a30","a31","a32","a33","a34","a35","a36","a37","a38","a39","a40","a41","a42","a43","a44","a45","a46","a47","a48","a49","a50","a51","a52","a53","a54","a55","a56","a57","a58","a59","a60","a61","a62","a63","a64","a65","a66","a67","a68","a69","a70","a71","a72","a73","a74","a75","a76","a77","a78","a79","a80","a81","a82","a83","a84","a85","a86","a87","a88","a89","a90","a91","a92","a93","a94","a95","a96","a97","a98","a99","a100","a101","a102","a103","a104","a105","a106","a107","a108","a109","a110","a111","a112","a113","a114","a115","a116","a117","a118","a119","a120","a121","a122","a123","a124","a125","a126","a127","a128","a129","a130","a131","a132","a133","a134","a135","a136","a137","a138","a139","a140","a141","a142","a143","a144","a145","a146","a147","a148","a149","a150","a151","a152","a153","a154","a155","a156","a157","a158","a159","a160","a161","a162","a163","a164","a165","a166","a167","a168","a169","a170","a171","a172","a173","a174","a175","a176","a177","a178","a179","a180","a181","a182","a183","a184","a185","a186","a187","a188","a189","a190","a191","a192","a193","a194","a195","a196","a197","a198","a199","a200","a201","a202","a203","a204","a205","a206","a207","a208","a209","a210","a211","a212","a213","a214","a215","a216","a217","a218","a219","a220","a221","a222","a223","a224","a225","a226","a227","a228","a229","a230","a231","a232","a233","a234","a235","a236","a237","a238","a239","a240","a241","a242","a243","a244","a245","a246","a247","a248","a249","a250","a251","a252","a253","a254","a255","s6","s7","s8","s9","s10","s11","s12","s13","s14","s15","s16","s17","s18","s19","s20","s21","s22","s23","s24","s25","s26","s27","s28","s29","s30","s31","s32","s33","s34","s35","s36","s37","s38","s39","s40","s41","s42","s43","s44","s45","s46","s47","s48","s49","s50","s51","s52","s53","s54","s55","s56","s57","s58","s59","s60","s61","s62","s63","s64","s65","s66","s67","s68","s69","s70","s71","s72","s73","s74","s75","s76","s77","s78","s79","s80","s81","s82","s83","s84","s85","s86","s87","s88","s89","s90","s91","s92","s93","s94","s95","s96","s97","s98","s99","s100","vcc","scc","m0","memory");
}
extern "C" void kernel_launch(void* const* d_in, const int* in_sizes, int n_in, void* d_out, int out_size, void* d_ws, size_t ws_size, hipStream_t stream) {
  char* xch = (char*)d_ws + ((ws_size - (size_t)270532608) & ~(size_t)0xfffff);
  (void)hipFuncSetAttribute((const void*)fused_attn, hipFuncAttributeMaxDynamicSharedMemorySize, 163840);
  hipLaunchKernelGGL(fused_attn, dim3(256), dim3(256), 163840, stream,
                     (const char*)d_in[0], (const char*)d_in[1], (const char*)d_in[2], (const char*)d_in[3], (const char*)d_in[4],
                     (const char*)d_in[5], (const char*)d_in[6], (const char*)d_in[7], (const char*)d_in[8], (const char*)d_in[9], xch, (char*)d_out, (char*)d_ws);
}
```

```cpp
#include <hip/hip_runtime.h>
#include <hip/hip_bf16.h>
#include <stdint.h>

__global__ __launch_bounds__(256, 1)
void fused_attn(const char* xA, const char* kA, const char* vA, const char* wA, const char* uA,
                const char* xB, const char* kB, const char* vB, const char* wB, const char* uB, char* xch, char* out, char* flags) {
  extern __shared__ __attribute__((aligned(16))) char lds[];
  const unsigned wid = __builtin_amdgcn_readfirstlane(threadIdx.x >> 6);
  const unsigned long long ka = (unsigned long long)__builtin_amdgcn_kernarg_segment_ptr();
  asm volatile(
"s_memtime s[64:65]\n"
"s_memrealtime s[66:67]\n"
"s_bfe_u32 s10, %[wg], 0x10002\n"
"s_mul_i32 s40, s10, 40\n"
"s_add_u32 s40, s40, %[kalo]\n"
"s_addc_u32 s41, %[kahi], 0\n"
"s_mov_b32 s78, %[kalo]\n"
"s_mov_b32 s79, %[kahi]\n"
"s_load_dwordx2 s[24:25], s[40:41], 0\n"
"s_load_dwordx2 s[28:29], s[40:41], 24\n"
"s_load_dwordx2 s[6:7], s[78:79], 0x60\n"
"s_load_dwordx2 s[16:17], s[40:41], 8\n"
"s_load_dwordx2 s[20:21], s[40:41], 16\n"
"s_load_dwordx2 s[8:9], s[40:41], 32\n"
"s_load_dwordx4 s[12:15], s[78:79], 0x50\n"
"s_mov_b32 s42, %[wid]\n"
"s_and_b32 s11, %[wg], 1\n"
"s_lshl_b32 s11, s11, 3\n"
"s_lshr_b32 s60, %[wg], 5\n"
"s_add_u32 s11, s11, s60\n"
"s_bfe_u32 s61, %[wg], 0x10001\n"
"s_bfe_u32 s60, %[wg], 0x20003\n"
"s_lshl2_add_u32 s61, s61, s60\n"
"s_getpc_b64 s[80:81]\n"
"LCP_%=:\n"
"s_mov_b32 s82, LEND_%=-LCP_%=\n"
"s_mov_b32 s83, 0x20000\n"
"v_mbcnt_lo_u32_b32 v2, -1, 0\n"
"v_lshlrev_b32 v2, 7, v2\n"
"s_lshr_b32 s60, %[wg], 3\n"
"s_lshl2_add_u32 s60, s60, s42\n"
"s_mul_i32 s60, s60, 640\n"
"s_mov_b64 exec, 31\n"
"buffer_load_dword v3, v2, s[80:83], s60 offen\n"
"s_mov_b64 exec, -1\n"
"s_mov_b32 s19, 0x20000\n"
"s_mov_b32 s23, 0x20000\n"
"s_mov_b32 s27, 0x20000\n"
"s_mov_b32 s31, 0x20000\n"
"s_mov_b32 s35, 0x20000\n"
"s_mov_b32 s39, 0x20000\n"
"s_mov_b32 s26, 286720\n"
"s_lshl_b32 s44, s42, 12\n"
"s_mov_b32 s50, s44\n"
"s_mul_i32 s51, s42, 7168\n"
"s_add_u32 s51, s51, 0x10000\n"
"s_mov_b32 s74, 0x8000\n"
"s_mov_b32 s58, 524288\n"
"s_mov_b32 s47, 0\n"
"s_add_u32 s60, s11, s61\n"
"s_and_b32 s60, s60, 0\n"
"s_mul_i32 s60, s60, 3\n"
"s_lshl_b32 s49, s60, 7\n"
"s_lshl_b32 s48, s60, 19\n"
"v_mbcnt_lo_u32_b32 v4, -1, 0\n"
"v_mbcnt_hi_u32_b32 v4, -1, v4\n"
"v_and_b32 v5, 15, v4\n"
"v_lshrrev_b32 v6, 4, v4\n"
"v_lshrrev_b32 v7, 3, v4\n"
"v_and_b32 v8, 7, v4\n"
"v_lshlrev_b32 v9, 1, v6\n"
"v_xor_b32 v9, v9, v8\n"
"v_lshlrev_b32 v9, 4, v9\n"
"s_mul_i32 s60, s42, 71680\n"
"v_mul_u32_u24 v10, 1280, v7\n"
"v_add3_u32 v248, v10, v9, s60\n"
"v_add_u32 v249, 9216, v248\n"
"v_add_u32 v254, 18432, v248\n"
"v_add_u32 v255, 27648, v248\n"
"v_lshl_add_u32 v240, v7, 8, v9\n"
"v_add_u32 v240, s44, v240\n"
"v_and_b32 v10, 1, v6\n"
"v_xor_b32 v10, v10, v8\n"
"v_lshlrev_b32 v10, 4, v10\n"
"v_lshl_add_u32 v10, v7, 13, v10\n"
"s_lshl_b32 s60, s42, 17\n"
"v_add_u32 v10, s60, v10\n"
"v_add_u32 v224, 1024, v10\n"
"v_add_u32 v225, 128, v10\n"
"v_xor_b32 v10, 64, v10\n"
"v_add_u32 v226, 64512, v10\n"
"v_add_u32 v227, 63616, v10\n"
"s_mul_i32 s40, s11, 286720\n"
"s_lshl_b32 s41, s61, 10\n"
"s_sub_u32 s30, 5243904, s41\n"
"s_sub_u32 s41, s41, 1024\n"
"s_ashr_i32 s60, s41, 31\n"
"s_lshl4_add_u32 s92, s10, s11\n"
"s_lshl_b32 s93, s92, 9\n"
"v_mov_b32 v0, 0\n"
"s_waitcnt lgkmcnt(0)\n"
"s_add_u32 s24, s24, s40\n"
"s_addc_u32 s25, s25, 0\n"
"s_and_b32 s25, s25, 0xffff\n"
"s_add_u32 s28, s28, s41\n"
"s_addc_u32 s29, s29, s60\n"
"s_and_b32 s29, s29, 0xffff\n"
"s_add_u32 s6, s6, s93\n"
"s_addc_u32 s7, s7, 0\n"
"buffer_load_dword v12, v248, s[24:27], s49 offen\n"
"buffer_load_dword v13, v224, s[28:31], s48 offen\n"
"global_load_dword v1, v0, s[6:7] offset:128 sc1\n"
"s_mov_b32 m0, s51\n"
"s_nop 0\n"
"buffer_load_dwordx4 v248, s[24:27], s49 offen lds\n"
"buffer_load_dwordx4 v249, s[24:27], s49 offen offset:1024 lds\n"
"buffer_load_dwordx4 v254, s[24:27], s49 offen offset:2048 lds\n"
"buffer_load_dwordx4 v255, s[24:27], s49 offen offset:3072 lds\n"
"s_add_u32 m0, s51, 4096\n"
"s_add_u32 s40, s49, 40960\n"
"buffer_load_dwordx4 v248, s[24:27], s40 offen lds\n"
"buffer_load_dwordx4 v249, s[24:27], s40 offen offset:1024 lds\n"
"buffer_load_dwordx4 v254, s[24:27], s40 offen offset:2048 lds\n"
"s_add_u32 s49, s49, 128\n"
"s_sub_u32 s41, s49, 1280\n"
"s_min_u32 s49, s49, s41\n"
"s_xor_b32 s51, s51, s74\n"
"s_mov_b32 m0, s50\n"
"s_nop 0\n"
"buffer_load_dwordx4 v224, s[28:31], s48 offen lds\n"
"buffer_load_dwordx4 v225, s[28:31], s48 offen offset:1024 lds\n"
"buffer_load_dwordx4 v226, s[28:31], s48 offen offset:2048 lds\n"
"buffer_load_dwordx4 v227, s[28:31], s48 offen offset:3072 lds\n"
"s_xor_b32 s50, s50, s74\n"
"s_lshl2_add_u32 s63, s11, s42\n"
"s_mul_i32 s63, s63, 10240\n"
"s_and_b32 s78, s42, 1\n"
"s_lshl_b32 s43, s78, 6\n"
"s_mov_b32 s77, 0x80000000\n"
"s_mov_b32 s75, 0x10000\n"
"s_mov_b32 s73, 0xffff0000\n"
"s_mov_b32 s46, 0x3e0293ee\n"
"s_mov_b32 s45, 0x63800000\n"
"s_mov_b32 s62, 9\n"
"v_lshl_add_u32 v242, v4, 4, s44\n"
"s_lshr_b32 s79, s42, 1\n"
"s_lshl4_add_u32 s76, s79, s78\n"
"s_lshl_b32 s76, s76, 10\n"
"s_add_u32 s76, s76, 0x20000\n"
"s_sub_u32 s76, s76, s44\n"
"v_lshlrev_b32 v9, 6, v6\n"
"v_xor_b32 v9, v9, v242\n"
"v_add_u32 v243, s76, v9\n"
"v_and_b32 v9, 6, v5\n"
"s_xor_b32 m0, s50, 0xc000\n"
"s_add_u32 s40, s48, 256\n"
"buffer_load_dwordx4 v224, s[28:31], s40 offen lds\n"
"v_xor_b32 v9, v9, v6\n"
"v_lshlrev_b32 v9, 4, v9\n"
"v_lshl_add_u32 v10, v5, 7, v9\n"
"s_mul_i32 s79, s42, 6144\n"
"s_add_u32 s79, s79, 0x10000\n"
"v_add_u32 v246, s79, v10\n"
"v_xor_b32 v247, 64, v246\n"
"s_lshr_b32 s79, s42, 1\n"
"s_lshl_b32 s79, s79, 11\n"
"buffer_load_dwordx4 v225, s[28:31], s40 offen offset:1024 lds\n"
"s_add_u32 s79, s79, 90112\n"
"v_add_u32 v252, s79, v10\n"
"v_xor_b32 v253, 64, v252\n"
"s_xor_b32 s79, s42, 1\n"
"s_mul_i32 s79, s79, 6144\n"
"s_add_u32 s79, s79, 0x10000\n"
"v_add_u32 v250, s79, v10\n"
"v_xor_b32 v251, 64, v250\n"
"s_mul_i32 s79, s78, 8256\n"
"buffer_load_dwordx4 v226, s[28:31], s40 offen offset:2048 lds\n"
"v_lshrrev_b32 v9, 5, v4\n"
"v_lshlrev_b32 v9, 11, v9\n"
"v_bfe_u32 v10, v4, 2, 3\n"
"v_lshl_add_u32 v9, v10, 7, v9\n"
"v_and_b32 v10, 3, v4\n"
"v_lshl_add_u32 v9, v10, 3, v9\n"
"v_bfe_u32 v10, v4, 3, 2\n"
"v_lshl_add_u32 v9, v10, 5, v9\n"
"s_add_u32 s79, s79, 0x10000\n"
"buffer_load_dwordx4 v227, s[28:31], s40 offen offset:3072 lds\n"
"s_cmp_eq_u32 s47, 8\n"
"s_cselect_b32 s41, s52, s58\n"
"s_add_u32 s48, s48, s41\n"
"s_sub_u32 s41, s48, 5242880\n"
"s_min_u32 s48, s48, s41\n"
"v_xor_b32 v232, s79, v9\n"
"v_xor_b32 v233, 32, v232\n"
"v_xor_b32 v234, 64, v232\n"
"v_xor_b32 v235, 96, v232\n"
"v_xor_b32 v236, 0x2000, v232\n"
"v_xor_b32 v237, 0x2000, v233\n"
"v_xor_b32 v238, 0x2000, v234\n"
"v_xor_b32 v239, 0x2000, v235\n"
"v_lshlrev_b32 v9, 11, v6\n"
"s_mov_b32 m0, s50\n"
"s_nop 0\n"
"buffer_load_dwordx4 v224, s[28:31], s48 offen lds\n"
"v_bfe_u32 v10, v4, 2, 2\n"
"v_lshl_add_u32 v9, v10, 7, v9\n"
"v_and_b32 v10, 1, v4\n"
"v_lshl_add_u32 v9, v10, 3, v9\n"
"v_bfe_u32 v10, v4, 1, 1\n"
"v_lshl_add_u32 v9, v10, 5, v9\n"
"v_bfe_u32 v10, v4, 3, 1\n"
"v_lshl_add_u32 v9, v10, 4, v9\n"
"v_bfe_u32 v10, v4, 4, 1\n"
"buffer_load_dwordx4 v225, s[28:31], s48 offen offset:1024 lds\n"
"v_lshl_add_u32 v9, v10, 6, v9\n"
"v_xor_b32 v244, s43, v9\n"
"v_xor_b32 v245, 16, v244\n"
"s_lshl3_add_u32 s79, s11, s61\n"
"s_lshl_b32 s79, s79, 20\n"
"s_sub_u32 s79, s79, 896\n"
"s_ashr_i32 s60, s79, 31\n"
"s_add_u32 s16, s16, s79\n"
"s_addc_u32 s17, s17, s60\n"
"buffer_load_dwordx4 v226, s[28:31], s48 offen offset:2048 lds\n"
"s_and_b32 s17, s17, 0xffff\n"
"s_mov_b32 s18, 1049472\n"
"s_mov_b64 s[84:85], s[16:17]\n"
"s_mov_b64 s[86:87], s[18:19]\n"
"s_add_u32 s20, s20, s79\n"
"s_addc_u32 s21, s21, s60\n"
"s_and_b32 s21, s21, 0xffff\n"
"s_mov_b32 s22, 1049472\n"
"s_mov_b64 s[88:89], s[20:21]\n"
"buffer_load_dwordx4 v227, s[28:31], s48 offen offset:3072 lds\n"
"s_xor_b32 s50, s50, s74\n"
"s_mov_b64 s[90:91], s[22:23]\n"
"s_lshr_b32 s57, %[wg], 3\n"
"s_mul_i32 s57, s57, 5\n"
"s_and_b32 s57, s57, 15\n"
"s_lshl_b32 s57, s57, 14\n"
"s_mov_b32 s55, 896\n"
"s_add_u32 s56, s57, 896\n"
"s_mov_b32 s59, 16384\n"
"s_mov_b32 s54, -896\n"
"s_mov_b32 m0, s51\n"
"s_nop 0\n"
"buffer_load_dwordx4 v248, s[24:27], s49 offen lds\n"
"s_add_u32 s52, s11, s61\n"
"s_and_b32 s52, s52, 0\n"
"s_mul_i32 s52, s52, 3\n"
"s_add_u32 s52, s52, 1\n"
"s_lshl_b32 s53, s52, 7\n"
"s_lshl_b32 s52, s52, 19\n"
"s_add_u32 s52, s52, 512\n"
"s_mul_i32 s79, s61, 655360\n"
"s_add_u32 s8, s8, s79\n"
"s_addc_u32 s9, s9, 0\n"
"buffer_load_dwordx4 v249, s[24:27], s49 offen offset:1024 lds\n"
"s_and_b32 s9, s9, 0xffff\n"
"s_add_u32 s12, s12, 0x1fffff\n"
"s_addc_u32 s13, s13, 0\n"
"s_and_b32 s12, s12, 0xffe00000\n"
"s_mul_i32 s96, s42, 3\n"
"s_and_b32 s96, s96, 5\n"
"s_cmp_eq_u32 s42, 3\n"
"s_cselect_b32 s96, 5, s96\n"
"buffer_load_dwordx4 v254, s[24:27], s49 offen offset:2048 lds\n"
"s_lshl_b32 s79, s61, 8\n"
"s_add_u32 s96, s96, s79\n"
"s_add_u32 s96, s96, 160\n"
"s_lshl_b32 s96, s96, 12\n"
"s_mul_i32 s79, s92, 8388608\n"
"s_add_u32 s32, s12, s79\n"
"s_addc_u32 s33, s13, 0\n"
"s_and_b32 s33, s33, 0xffff\n"
"s_mov_b32 s34, 8388608\n"
"buffer_load_dwordx4 v255, s[24:27], s49 offen offset:3072 lds\n"
"s_mul_i32 s79, s11, 573440\n"
"s_mul_i32 s60, s10, 1280\n"
"s_sub_u32 s38, 573440, s60\n"
"s_add_u32 s79, s79, s60\n"
"s_add_u32 s36, s14, s79\n"
"s_addc_u32 s37, s15, 0\n"
"s_and_b32 s37, s37, 0xffff\n"
"s_lshl2_add_u32 s93, s61, s42\n"
"s_add_u32 s95, s61, 1\n"
"s_add_u32 m0, s51, 4096\n"
"s_add_u32 s40, s49, 40960\n"
"buffer_load_dwordx4 v248, s[24:27], s40 offen lds\n"
"s_and_b32 s95, s95, 7\n"
"s_lshl_b32 s95, s95, 7\n"
"s_add_u32 s100, s61, 2\n"
"s_and_b32 s100, s100, 7\n"
"s_lshl_b32 s100, s100, 7\n"
"s_lshl_b32 s60, s61, 7\n"
"s_add_u32 s60, s60, s43\n"
"s_mul_i32 s97, s42, 122880\n"
"s_add_u32 s97, s97, s60\n"
"buffer_load_dwordx4 v249, s[24:27], s40 offen offset:1024 lds\n"
"s_lshr_b32 s98, s42, 1\n"
"s_add_u32 s98, s98, 12\n"
"s_mul_i32 s98, s98, 40960\n"
"s_add_u32 s98, s98, s60\n"
"s_mul_i32 s60, s93, 37\n"
"s_lshr_b32 s60, s60, 8\n"
"s_mul_i32 s79, s60, 7\n"
"s_sub_u32 s79, s93, s79\n"
"s_lshl4_add_u32 s94, s60, s79\n"
"buffer_load_dwordx4 v254, s[24:27], s40 offen offset:2048 lds\n"
"s_add_u32 s49, s49, 128\n"
"s_sub_u32 s41, s49, 1280\n"
"s_min_u32 s49, s49, s41\n"
"s_xor_b32 s51, s51, s74\n"
"s_lshl_b32 s94, s94, 10\n"
"s_add_u32 s94, s94, 524288\n"
"s_mul_i32 s99, s60, 3\n"
"s_lshr_b32 s78, s79, 1\n"
"s_add_u32 s99, s99, s78\n"
"s_lshr_b32 s78, s60, 1\n"
"s_add_u32 s78, s78, 12\n"
"s_cmp_eq_u32 s79, 6\n"
"s_cselect_b32 s99, s78, s99\n"
"s_mul_i32 s99, s99, 40960\n"
"s_xor_b32 s79, s79, s60\n"
"s_and_b32 s79, s79, 1\n"
"s_lshl_b32 s79, s79, 6\n"
"s_add_u32 s99, s99, s79\n"
"s_add_u32 s99, s99, 1024\n"
"s_cmp_eq_u32 s61, 7\n"
"s_cselect_b32 s99, 0x40000000, s99\n"
"s_cselect_b32 s94, 0x40000000, s94\n"
"s_waitcnt vmcnt(27)\n"
"v_and_b32 v12, 0x7fff0000, v12\n"
"v_and_b32 v13, 0x7fff0000, v13\n"
"s_nop 0\n"
"v_max_u32_dpp v12, v12, v12 quad_perm:[1,0,3,2] row_mask:0xf bank_mask:0xf\n"
"v_max_u32_dpp v13, v13, v13 quad_perm:[1,0,3,2] row_mask:0xf bank_mask:0xf\n"
"s_nop 0\n"
"v_max_u32_dpp v12, v12, v12 quad_perm:[2,3,0,1] row_mask:0xf bank_mask:0xf\n"
"v_max_u32_dpp v13, v13, v13 quad_perm:[2,3,0,1] row_mask:0xf bank_mask:0xf\n"
"s_memtime s[80:81]\n"
"s_memrealtime s[82:83]\n"
"s_nop 0\n"
"v_max_u32_dpp v12, v12, v12 row_half_mirror row_mask:0xf bank_mask:0xf\n"
"v_max_u32_dpp v13, v13, v13 row_half_mirror row_mask:0xf bank_mask:0xf\n"
"s_nop 0\n"
"v_min_u32_dpp v12, v12, v12 row_mirror row_mask:0xf bank_mask:0xf\n"
"v_min_u32_dpp v13, v13, v13 row_mirror row_mask:0xf bank_mask:0xf\n"
"s_nop 0\n"
"v_min_u32_dpp v12, v12, v12 row_bcast:15 row_mask:0xa bank_mask:0xf\n"
"v_min_u32_dpp v13, v13, v13 row_bcast:15 row_mask:0xa bank_mask:0xf\n"
"s_nop 0\n"
"v_min_u32_dpp v12, v12, v12 row_bcast:31 row_mask:0xc bank_mask:0xf\n"
"v_min_u32_dpp v13, v13, v13 row_bcast:31 row_mask:0xc bank_mask:0xf\n"
"s_nop 0\n"
"v_mul_f32 v12, v12, v13\n"
"v_mul_f32 v12, 0x43c80000, v12\n"
"v_min_f32 v12, 0x71800000, v12\n"
"v_mul_f32 v13, s46, v12\n"
"s_nop 0\n"
"v_readlane_b32 s70, v12, 63\n"
"v_readlane_b32 s71, v12, 63\n"
"v_readlane_b32 s69, v13, 63\n"
"s_nop 1\n"
"v_mov_b64 v[228:229], s[70:71]\n"
"v_mov_b64 v[230:231], s[70:71]\n"
"s_waitcnt vmcnt(26)\n"
"v_readfirstlane_b32 s92, v1\n"
"v_mov_b64 v[0:1], s[70:71]\n"
"v_mov_b64 v[2:3], s[70:71]\n"
"s_mov_b32 s70, s46\n"
"s_xor_b32 s71, s69, s77\n"
"s_waitcnt lgkmcnt(0)\n"
"s_sub_u32 s80, s80, s64\n"
"s_sub_u32 s82, s82, s66\n"
"s_mul_i32 s80, s80, 10\n"
"s_mul_i32 s82, s82, 211\n"
"s_cmp_gt_u32 s80, s82\n"
"s_cselect_b32 s64, 1, 0\n"
"s_cselect_b32 s68, 655360, 0\n"
"s_cmp_eq_u32 s42, 0\n"
"s_cselect_b32 s64, s64, 0\n"
"s_mov_b32 s65, 0\n"
"s_add_u32 s66, s66, 2800\n"
"s_mov_b32 s67, s66\n"
"s_mov_b32 s72, 122\n"
"s_mov_b64 vcc, s[64:65]\n"
"s_sub_u32 s63, s63, s44\n"
"s_add_u32 s80, s8, s63\n"
"s_addc_u32 s81, s9, 0\n"
"s_add_u32 s82, s44, 704\n"
"s_cmp_lg_u32 s68, 0\n"
"s_cselect_b32 s82, s82, 0\n"
"s_mov_b32 s83, 0x20000\n"
"s_nop 2\n"
"s_mul_i32 s60, s92, 0x9e3779b1\n"
"s_add_u32 s60, s60, 0x7f4a7c15\n"
"s_waitcnt vmcnt(15)\n"
"s_barrier\n"
"ds_read_b128 a[136:139], v246 offset:0\n"
"ds_read_b128 a[140:143], v247 offset:0\n"
"ds_read_b64_tr_b16 a[192:193], v244 offset:0\n"
"ds_read_b64_tr_b16 a[194:195], v244 offset:512\n"
"ds_read_b64_tr_b16 a[196:197], v244 offset:8192\n"
"ds_read_b64_tr_b16 a[198:199], v244 offset:8704\n"
"ds_read_b64_tr_b16 a[200:201], v245 offset:0\n"
"ds_read_b64_tr_b16 a[202:203], v245 offset:512\n"
"ds_read_b64_tr_b16 a[204:205], v245 offset:8192\n"
"ds_read_b64_tr_b16 a[206:207], v245 offset:8704\n"
"ds_read_b64_tr_b16 a[208:209], v244 offset:1024\n"
"ds_read_b64_tr_b16 a[210:211], v244 offset:1536\n"
"ds_read_b64_tr_b16 a[212:213], v244 offset:9216\n"
"ds_read_b64_tr_b16 a[214:215], v244 offset:9728\n"
"s_waitcnt lgkmcnt(10)\n"
"v_mfma_f32_16x16x32_bf16 v[112:115], a[192:195], a[136:139], v[228:231]\n"
"ds_read_b64_tr_b16 a[216:217], v245 offset:1024\n"
"ds_read_b64_tr_b16 a[218:219], v245 offset:1536\n"
"s_waitcnt lgkmcnt(10)\n"
"v_mfma_f32_16x16x32_bf16 v[112:115], a[196:199], a[140:143], v[112:115]\n"
"ds_read_b64_tr_b16 a[220:221], v245 offset:9216\n"
"ds_read_b64_tr_b16 a[222:223], v245 offset:9728\n"
"s_waitcnt lgkmcnt(10)\n"
"v_mfma_f32_16x16x32_bf16 v[116:119], a[200:203], a[136:139], v[228:231]\n"
"s_waitcnt lgkmcnt(8)\n"
"ds_read_b128 a[144:147], v246 offset:2048\n"
"v_mfma_f32_16x16x32_bf16 v[116:119], a[204:207], a[140:143], v[116:119]\n"
"s_waitcnt lgkmcnt(7)\n"
"ds_read_b128 a[148:151], v247 offset:2048\n"
"v_mfma_f32_16x16x32_bf16 v[120:123], a[208:211], a[136:139], v[228:231]\n"
"s_waitcnt lgkmcnt(6)\n"
"ds_read_b128 a[152:155], v246 offset:4096\n"
"v_mfma_f32_16x16x32_bf16 v[120:123], a[212:215], a[140:143], v[120:123]\n"
"s_waitcnt lgkmcnt(5)\n"
"ds_read_b128 a[156:159], v247 offset:4096\n"
"v_mfma_f32_16x16x32_bf16 v[124:127], a[216:219], a[136:139], v[228:231]\n"
"s_waitcnt lgkmcnt(4)\n"
"ds_read_b128 a[160:163], v252 offset:0\n"
"v_mfma_f32_16x16x32_bf16 v[124:127], a[220:223], a[140:143], v[124:127]\n"
"s_waitcnt lgkmcnt(4)\n"
"ds_read_b128 a[164:167], v253 offset:0\n"
"v_mfma_f32_16x16x32_bf16 v[140:143], a[216:219], a[144:147], v[228:231]\n"
"s_waitcnt lgkmcnt(4)\n"
"ds_read_b128 a[168:171], v250 offset:0\n"
"v_mfma_f32_16x16x32_bf16 v[140:143], a[220:223], a[148:151], v[140:143]\n"
"ds_read_b128 a[172:175], v251 offset:0\n"
"v_mfma_f32_16x16x32_bf16 v[136:139], a[208:211], a[144:147], v[228:231]\n"
"s_waitcnt vmcnt(11)\n"
"ds_read_b128 a[176:179], v250 offset:2048\n"
"v_mfma_f32_16x16x32_bf16 v[136:139], a[212:215], a[148:151], v[136:139]\n"
"s_barrier\n"
"v_mfma_f32_16x16x32_bf16 v[132:135], a[200:203], a[144:147], v[228:231]\n"
"s_add_u32 s40, s48, 256\n"
"s_xor_b32 m0, s50, 0xc000\n"
"ds_read_b128 a[180:183], v251 offset:2048\n"
"v_mfma_f32_16x16x32_bf16 v[132:135], a[204:207], a[148:151], v[132:135]\n"
"buffer_load_dwordx4 v224, s[28:31], s40 offen lds\n"
"v_mfma_f32_16x16x32_bf16 v[128:131], a[192:195], a[144:147], v[228:231]\n"
"ds_read_b128 a[184:187], v250 offset:4096\n"
"v_mfma_f32_16x16x32_bf16 v[128:131], a[196:199], a[148:151], v[128:131]\n"
"s_waitcnt lgkmcnt(5)\n"
"ds_read_b128 a[188:191], v251 offset:4096\n"
"v_mfma_f32_16x16x32_bf16 v[144:147], a[192:195], a[152:155], v[228:231]\n"
"ds_read_b64_tr_b16 a[224:225], v244 offset:16384\n"
"v_mfma_f32_16x16x32_bf16 v[144:147], a[196:199], a[156:159], v[144:147]\n"
"ds_read_b64_tr_b16 a[226:227], v244 offset:16896\n"
"v_mfma_f32_16x16x32_bf16 v[148:151], a[200:203], a[152:155], v[228:231]\n"
"ds_read_b64_tr_b16 a[228:229], v244 offset:24576\n"
"v_mfma_f32_16x16x32_bf16 v[148:151], a[204:207], a[156:159], v[148:151]\n"
"buffer_load_dwordx4 v225, s[28:31], s40 offen offset:1024 lds\n"
"v_mfma_f32_16x16x32_bf16 v[152:155], a[208:211], a[152:155], v[228:231]\n"
"ds_read_b64_tr_b16 a[230:231], v244 offset:25088\n"
"v_mfma_f32_16x16x32_bf16 v[152:155], a[212:215], a[156:159], v[152:155]\n"
"ds_read_b64_tr_b16 a[232:233], v245 offset:16384\n"
"v_mfma_f32_16x16x32_bf16 v[156:159], a[216:219], a[152:155], v[228:231]\n"
"ds_read_b64_tr_b16 a[234:235], v245 offset:16896\n"
"v_mfma_f32_16x16x32_bf16 v[156:159], a[220:223], a[156:159], v[156:159]\n"
"ds_read_b64_tr_b16 a[236:237], v245 offset:24576\n"
"v_mfma_f32_16x16x32_bf16 v[172:175], a[216:219], a[160:163], v[228:231]\n"
"ds_read_b64_tr_b16 a[238:239], v245 offset:25088\n"
"v_mfma_f32_16x16x32_bf16 v[172:175], a[220:223], a[164:167], v[172:175]\n"
"buffer_load_dwordx4 v226, s[28:31], s40 offen offset:2048 lds\n"
"v_mfma_f32_16x16x32_bf16 v[168:171], a[208:211], a[160:163], v[228:231]\n"
"ds_read_b64_tr_b16 a[240:241], v244 offset:17408\n"
"v_mfma_f32_16x16x32_bf16 v[168:171], a[212:215], a[164:167], v[168:171]\n"
"ds_read_b64_tr_b16 a[242:243], v244 offset:17920\n"
"v_mfma_f32_16x16x32_bf16 v[164:167], a[200:203], a[160:163], v[228:231]\n"
"ds_read_b64_tr_b16 a[244:245], v244 offset:25600\n"
"v_mfma_f32_16x16x32_bf16 v[164:167], a[204:207], a[164:167], v[164:167]\n"
"buffer_load_dwordx4 v227, s[28:31], s40 offen offset:3072 lds\n"
"v_mfma_f32_16x16x32_bf16 v[160:163], a[192:195], a[160:163], v[228:231]\n"
"s_cmp_eq_u32 s47, 8\n"
"ds_read_b64_tr_b16 a[246:247], v244 offset:26112\n"
"v_xor_b32 v244, s74, v244\n"
"v_mfma_f32_16x16x32_bf16 v[160:163], a[196:199], a[164:167], v[160:163]\n"
"s_waitcnt lgkmcnt(5)\n"
"s_cselect_b32 s41, s52, s58\n"
"ds_read_b64_tr_b16 a[248:249], v245 offset:17408\n"
"v_xor_b32 v246, s74, v246\n"
"v_mfma_f32_16x16x32_bf16 v[176:179], a[192:195], a[168:171], v[228:231]\n"
"s_add_u32 s48, s48, s41\n"
"ds_read_b64_tr_b16 a[250:251], v245 offset:17920\n"
"v_xor_b32 v247, s74, v247\n"
"v_mfma_f32_16x16x32_bf16 v[176:179], a[196:199], a[172:175], v[176:179]\n"
"s_sub_u32 s41, s48, 5242880\n"
"ds_read_b64_tr_b16 a[252:253], v245 offset:25600\n"
"v_xor_b32 v252, s74, v252\n"
"v_mfma_f32_16x16x32_bf16 v[180:183], a[200:203], a[168:171], v[228:231]\n"
"s_min_u32 s48, s48, s41\n"
"ds_read_b64_tr_b16 a[254:255], v245 offset:26112\n"
"v_xor_b32 v245, s74, v245\n"
"v_mfma_f32_16x16x32_bf16 v[180:183], a[204:207], a[172:175], v[180:183]\n"
"v_xor_b32 v253, s74, v253\n"
"v_xor_b32 v250, s74, v250\n"
"v_mfma_f32_16x16x32_bf16 v[184:187], a[208:211], a[168:171], v[228:231]\n"
"s_mov_b32 m0, s50\n"
"v_xor_b32 v251, s74, v251\n"
"v_mfma_f32_16x16x32_bf16 v[184:187], a[212:215], a[172:175], v[184:187]\n"
"buffer_load_dwordx4 v224, s[28:31], s48 offen lds\n"
"v_mfma_f32_16x16x32_bf16 v[188:191], a[216:219], a[168:171], v[228:231]\n"
"v_mfma_f32_16x16x32_bf16 v[188:191], a[220:223], a[172:175], v[188:191]\n"
"v_mfma_f32_16x16x32_bf16 v[204:207], a[216:219], a[176:179], v[228:231]\n"
"v_mfma_f32_16x16x32_bf16 v[204:207], a[220:223], a[180:183], v[204:207]\n"
"buffer_load_dwordx4 v225, s[28:31], s48 offen offset:1024 lds\n"
"v_mfma_f32_16x16x32_bf16 v[200:203], a[208:211], a[176:179], v[228:231]\n"
"v_mfma_f32_16x16x32_bf16 v[200:203], a[212:215], a[180:183], v[200:203]\n"
"v_mfma_f32_16x16x32_bf16 v[196:199], a[200:203], a[176:179], v[228:231]\n"
"v_mfma_f32_16x16x32_bf16 v[196:199], a[204:207], a[180:183], v[196:199]\n"
"v_mfma_f32_16x16x32_bf16 v[192:195], a[192:195], a[176:179], v[228:231]\n"
"v_mfma_f32_16x16x32_bf16 v[192:195], a[196:199], a[180:183], v[192:195]\n"
"buffer_load_dwordx4 v226, s[28:31], s48 offen offset:2048 lds\n"
"v_mfma_f32_16x16x32_bf16 v[208:211], a[192:195], a[184:187], v[228:231]\n"
"v_mfma_f32_16x16x32_bf16 v[208:211], a[196:199], a[188:191], v[208:211]\n"
"v_mfma_f32_16x16x32_bf16 v[212:215], a[200:203], a[184:187], v[228:231]\n"
"v_mfma_f32_16x16x32_bf16 v[212:215], a[204:207], a[188:191], v[212:215]\n"
"v_mfma_f32_16x16x32_bf16 v[216:219], a[208:211], a[184:187], v[228:231]\n"
"v_mfma_f32_16x16x32_bf16 v[216:219], a[212:215], a[188:191], v[216:219]\n"
"buffer_load_dwordx4 v227, s[28:31], s48 offen offset:3072 lds\n"
"v_mfma_f32_16x16x32_bf16 v[220:223], a[216:219], a[184:187], v[228:231]\n"
"s_xor_b32 s50, s50, s74\n"
"v_mfma_f32_16x16x32_bf16 v[220:223], a[220:223], a[188:191], v[220:223]\n"
"v_mfma_f32_16x16x32_bf16 v[0:3], a[224:227], a[136:139], v[228:231]\n"
"v_mfma_f32_16x16x32_bf16 v[0:3], a[228:231], a[140:143], v[0:3]\n"
"v_mfma_f32_16x16x32_bf16 v[4:7], a[232:235], a[136:139], v[228:231]\n"
"s_waitcnt vmcnt(8) lgkmcnt(0)\n"
"v_mfma_f32_16x16x32_bf16 v[4:7], a[236:239], a[140:143], v[4:7]\n"
"s_barrier\n"
"v_mfma_f32_16x16x32_bf16 v[8:11], a[240:243], a[136:139], v[228:231]\n"
"s_mov_b32 m0, s51\n"
"ds_read_b64_tr_b16 a[192:193], v244 offset:0\n"
"v_mfma_f32_16x16x32_bf16 v[8:11], a[244:247], a[140:143], v[8:11]\n"
"buffer_load_dwordx4 v248, s[24:27], s49 offen lds\n"
"v_mfma_f32_16x16x32_bf16 v[12:15], a[248:251], a[136:139], v[228:231]\n"
"ds_read_b64_tr_b16 a[194:195], v244 offset:512\n"
"v_mfma_f32_16x16x32_bf16 v[12:15], a[252:255], a[140:143], v[12:15]\n"
"ds_read_b128 a[136:139], v246 offset:0\n"
"v_mfma_f32_16x16x32_bf16 v[28:31], a[248:251], a[144:147], v[228:231]\n"
"ds_read_b64_tr_b16 a[196:197], v244 offset:8192\n"
"v_mfma_f32_16x16x32_bf16 v[28:31], a[252:255], a[148:151], v[28:31]\n"
"ds_read_b64_tr_b16 a[198:199], v244 offset:8704\n"
"v_mfma_f32_16x16x32_bf16 v[24:27], a[240:243], a[144:147], v[228:231]\n"
"ds_read_b128 a[140:143], v247 offset:0\n"
"v_mfma_f32_16x16x32_bf16 v[24:27], a[244:247], a[148:151], v[24:27]\n"
"buffer_load_dwordx4 v249, s[24:27], s49 offen offset:1024 lds\n"
"v_mfma_f32_16x16x32_bf16 v[20:23], a[232:235], a[144:147], v[228:231]\n"
"ds_read_b64_tr_b16 a[200:201], v245 offset:0\n"
"v_mfma_f32_16x16x32_bf16 v[20:23], a[236:239], a[148:151], v[20:23]\n"
"ds_read_b64_tr_b16 a[202:203], v245 offset:512\n"
"v_mfma_f32_16x16x32_bf16 v[16:19], a[224:227], a[144:147], v[228:231]\n"
"ds_read_b64_tr_b16 a[204:205], v245 offset:8192\n"
"v_mfma_f32_16x16x32_bf16 v[16:19], a[228:231], a[148:151], v[16:19]\n"
"ds_read_b64_tr_b16 a[206:207], v245 offset:8704\n"
"v_mfma_f32_16x16x32_bf16 v[32:35], a[224:227], a[152:155], v[228:231]\n"
"ds_read_b64_tr_b16 a[208:209], v244 offset:1024\n"
"v_mfma_f32_16x16x32_bf16 v[32:35], a[228:231], a[156:159], v[32:35]\n"
"buffer_load_dwordx4 v254, s[24:27], s49 offen offset:2048 lds\n"
"v_mfma_f32_16x16x32_bf16 v[36:39], a[232:235], a[152:155], v[228:231]\n"
"ds_read_b64_tr_b16 a[210:211], v244 offset:1536\n"
"v_mfma_f32_16x16x32_bf16 v[36:39], a[236:239], a[156:159], v[36:39]\n"
"ds_read_b64_tr_b16 a[212:213], v244 offset:9216\n"
"v_mfma_f32_16x16x32_bf16 v[40:43], a[240:243], a[152:155], v[228:231]\n"
"ds_read_b64_tr_b16 a[214:215], v244 offset:9728\n"
"v_mfma_f32_16x16x32_bf16 v[40:43], a[244:247], a[156:159], v[40:43]\n"
"ds_read_b64_tr_b16 a[216:217], v245 offset:1024\n"
"v_mfma_f32_16x16x32_bf16 v[44:47], a[248:251], a[152:155], v[228:231]\n"
"ds_read_b64_tr_b16 a[218:219], v245 offset:1536\n"
"v_mfma_f32_16x16x32_bf16 v[44:47], a[252:255], a[156:159], v[44:47]\n"
"buffer_load_dwordx4 v255, s[24:27], s49 offen offset:3072 lds\n"
"v_mfma_f32_16x16x32_bf16 v[60:63], a[248:251], a[160:163], v[228:231]\n"
"ds_read_b64_tr_b16 a[220:221], v245 offset:9216\n"
"v_mfma_f32_16x16x32_bf16 v[60:63], a[252:255], a[164:167], v[60:63]\n"
"ds_read_b64_tr_b16 a[222:223], v245 offset:9728\n"
"v_mfma_f32_16x16x32_bf16 v[56:59], a[240:243], a[160:163], v[228:231]\n"
"ds_read_b128 a[144:147], v246 offset:2048\n"
"v_mfma_f32_16x16x32_bf16 v[56:59], a[244:247], a[164:167], v[56:59]\n"
"s_add_u32 s40, s49, 40960\n"
"ds_read_b128 a[148:151], v247 offset:2048\n"
"v_mfma_f32_16x16x32_bf16 v[52:55], a[232:235], a[160:163], v[228:231]\n"
"s_add_u32 m0, s51, 4096\n"
"ds_read_b128 a[152:155], v246 offset:4096\n"
"v_mfma_f32_16x16x32_bf16 v[52:55], a[236:239], a[164:167], v[52:55]\n"
"buffer_load_dwordx4 v248, s[24:27], s40 offen lds\n"
"v_mfma_f32_16x16x32_bf16 v[48:51], a[224:227], a[160:163], v[228:231]\n"
"ds_read_b128 a[156:159], v247 offset:4096\n"
"v_mfma_f32_16x16x32_bf16 v[48:51], a[228:231], a[164:167], v[48:51]\n"
"ds_read_b128 a[160:163], v252 offset:0\n"
"v_mfma_f32_16x16x32_bf16 v[64:67], a[224:227], a[168:171], v[228:231]\n"
"ds_read_b128 a[164:167], v253 offset:0\n"
"v_mfma_f32_16x16x32_bf16 v[64:67], a[228:231], a[172:175], v[64:67]\n"
"v_mfma_f32_16x16x32_bf16 v[68:71], a[232:235], a[168:171], v[228:231]\n"
"v_mfma_f32_16x16x32_bf16 v[68:71], a[236:239], a[172:175], v[68:71]\n"
"buffer_load_dwordx4 v249, s[24:27], s40 offen offset:1024 lds\n"
"v_mfma_f32_16x16x32_bf16 v[72:75], a[240:243], a[168:171], v[228:231]\n"
"v_mfma_f32_16x16x32_bf16 v[72:75], a[244:247], a[172:175], v[72:75]\n"
"v_mfma_f32_16x16x32_bf16 v[76:79], a[248:251], a[168:171], v[228:231]\n"
"ds_read_b128 a[168:171], v250 offset:0\n"
"v_mfma_f32_16x16x32_bf16 v[76:79], a[252:255], a[172:175], v[76:79]\n"
"ds_read_b128 a[172:175], v251 offset:0\n"
"v_mfma_f32_16x16x32_bf16 v[92:95], a[248:251], a[176:179], v[228:231]\n"
"v_mfma_f32_16x16x32_bf16 v[92:95], a[252:255], a[180:183], v[92:95]\n"
"buffer_load_dwordx4 v254, s[24:27], s40 offen offset:2048 lds\n"
"v_mfma_f32_16x16x32_bf16 v[88:91], a[240:243], a[176:179], v[228:231]\n"
"s_add_u32 s49, s49, 128\n"
"v_mfma_f32_16x16x32_bf16 v[88:91], a[244:247], a[180:183], v[88:91]\n"
"s_sub_u32 s41, s49, 1280\n"
"v_mfma_f32_16x16x32_bf16 v[84:87], a[232:235], a[176:179], v[228:231]\n"
"s_min_u32 s49, s49, s41\n"
"v_mfma_f32_16x16x32_bf16 v[84:87], a[236:239], a[180:183], v[84:87]\n"
"s_xor_b32 s51, s51, s74\n"
"v_mfma_f32_16x16x32_bf16 v[80:83], a[224:227], a[176:179], v[228:231]\n"
"ds_read_b128 a[176:179], v250 offset:2048\n"
"v_mfma_f32_16x16x32_bf16 v[80:83], a[228:231], a[180:183], v[80:83]\n"
"ds_read_b128 a[180:183], v251 offset:2048\n"
"v_mfma_f32_16x16x32_bf16 v[96:99], a[224:227], a[184:187], v[228:231]\n"
"v_mfma_f32_16x16x32_bf16 v[96:99], a[228:231], a[188:191], v[96:99]\n"
"v_mfma_f32_16x16x32_bf16 v[100:103], a[232:235], a[184:187], v[228:231]\n"
"v_mfma_f32_16x16x32_bf16 v[100:103], a[236:239], a[188:191], v[100:103]\n"
"v_mfma_f32_16x16x32_bf16 v[104:107], a[240:243], a[184:187], v[228:231]\n"
"v_mfma_f32_16x16x32_bf16 v[104:107], a[244:247], a[188:191], v[104:107]\n"
"v_mfma_f32_16x16x32_bf16 v[108:111], a[248:251], a[184:187], v[228:231]\n"
"s_add_u32 s47, s47, 1\n"
"ds_read_b128 a[184:187], v250 offset:4096\n"
"v_mfma_f32_16x16x32_bf16 v[108:111], a[252:255], a[188:191], v[108:111]\n"
"s_waitcnt lgkmcnt(1)\n"
"ds_read_b128 a[188:191], v251 offset:4096\n"
"LPE_%=:\n"
"s_cmp_lg_u32 s68, 0\n"
"s_cbranch_scc1 LPN_%=\n"
"LPL_%=:\n"
"v_mfma_f32_16x16x32_bf16 v[112:115], a[192:195], a[136:139], v[112:115]\n"
"v_mfma_f32_16x16x32_bf16 v[112:115], a[196:199], a[140:143], v[112:115]\n"
"v_mfma_f32_16x16x32_bf16 v[116:119], a[200:203], a[136:139], v[116:119]\n"
"s_waitcnt vmcnt(11)\n"
"v_mfma_f32_16x16x32_bf16 v[116:119], a[204:207], a[140:143], v[116:119]\n"
"s_barrier\n"
"v_mfma_f32_16x16x32_bf16 v[120:123], a[208:211], a[136:139], v[120:123]\n"
"s_add_u32 s40, s48, 256\n"
"s_xor_b32 m0, s50, 0xc000\n"
"ds_read_b64_tr_b16 a[224:225], v244 offset:16384\n"
"v_mfma_f32_16x16x32_bf16 v[120:123], a[212:215], a[140:143], v[120:123]\n"
"buffer_load_dwordx4 v224, s[28:31], s40 offen lds\n"
"v_mfma_f32_16x16x32_bf16 v[124:127], a[216:219], a[136:139], v[124:127]\n"
"ds_read_b64_tr_b16 a[226:227], v244 offset:16896\n"
"v_mfma_f32_16x16x32_bf16 v[124:127], a[220:223], a[140:143], v[124:127]\n"
"ds_read_b64_tr_b16 a[228:229], v244 offset:24576\n"
"v_mfma_f32_16x16x32_bf16 v[140:143], a[216:219], a[144:147], v[140:143]\n"
"ds_read_b64_tr_b16 a[230:231], v244 offset:25088\n"
"v_mfma_f32_16x16x32_bf16 v[140:143], a[220:223], a[148:151], v[140:143]\n"
"ds_read_b64_tr_b16 a[232:233], v245 offset:16384\n"
"v_mfma_f32_16x16x32_bf16 v[136:139], a[208:211], a[144:147], v[136:139]\n"
"ds_read_b64_tr_b16 a[234:235], v245 offset:16896\n"
"v_mfma_f32_16x16x32_bf16 v[136:139], a[212:215], a[148:151], v[136:139]\n"
"buffer_load_dwordx4 v225, s[28:31], s40 offen offset:1024 lds\n"
"v_mfma_f32_16x16x32_bf16 v[132:135], a[200:203], a[144:147], v[132:135]\n"
"ds_read_b64_tr_b16 a[236:237], v245 offset:24576\n"
"v_mfma_f32_16x16x32_bf16 v[132:135], a[204:207], a[148:151], v[132:135]\n"
"ds_read_b64_tr_b16 a[238:239], v245 offset:25088\n"
"v_mfma_f32_16x16x32_bf16 v[128:131], a[192:195], a[144:147], v[128:131]\n"
"ds_read_b64_tr_b16 a[240:241], v244 offset:17408\n"
"v_mfma_f32_16x16x32_bf16 v[128:131], a[196:199], a[148:151], v[128:131]\n"
"ds_read_b64_tr_b16 a[242:243], v244 offset:17920\n"
"v_mfma_f32_16x16x32_bf16 v[144:147], a[192:195], a[152:155], v[144:147]\n"
"ds_read_b64_tr_b16 a[244:245], v244 offset:25600\n"
"v_mfma_f32_16x16x32_bf16 v[144:147], a[196:199], a[156:159], v[144:147]\n"
"buffer_load_dwordx4 v226, s[28:31], s40 offen offset:2048 lds\n"
"v_mfma_f32_16x16x32_bf16 v[148:151], a[200:203], a[152:155], v[148:151]\n"
"ds_read_b64_tr_b16 a[246:247], v244 offset:26112\n"
"v_mfma_f32_16x16x32_bf16 v[148:151], a[204:207], a[156:159], v[148:151]\n"
"ds_read_b64_tr_b16 a[248:249], v245 offset:17408\n"
"v_mfma_f32_16x16x32_bf16 v[152:155], a[208:211], a[152:155], v[152:155]\n"
"ds_read_b64_tr_b16 a[250:251], v245 offset:17920\n"
"v_mfma_f32_16x16x32_bf16 v[152:155], a[212:215], a[156:159], v[152:155]\n"
"ds_read_b64_tr_b16 a[252:253], v245 offset:25600\n"
"v_mfma_f32_16x16x32_bf16 v[156:159], a[216:219], a[152:155], v[156:159]\n"
"ds_read_b64_tr_b16 a[254:255], v245 offset:26112\n"
"v_mfma_f32_16x16x32_bf16 v[156:159], a[220:223], a[156:159], v[156:159]\n"
"buffer_load_dwordx4 v227, s[28:31], s40 offen offset:3072 lds\n"
"v_mfma_f32_16x16x32_bf16 v[172:175], a[216:219], a[160:163], v[172:175]\n"
"s_cmp_eq_u32 s47, 8\n"
"v_mfma_f32_16x16x32_bf16 v[172:175], a[220:223], a[164:167], v[172:175]\n"
"s_cselect_b32 s41, s52, s58\n"
"v_mfma_f32_16x16x32_bf16 v[168:171], a[208:211], a[160:163], v[168:171]\n"
"s_add_u32 s48, s48, s41\n"
"v_mfma_f32_16x16x32_bf16 v[168:171], a[212:215], a[164:167], v[168:171]\n"
"s_sub_u32 s41, s48, 5242880\n"
"v_mfma_f32_16x16x32_bf16 v[164:167], a[200:203], a[160:163], v[164:167]\n"
"s_min_u32 s48, s48, s41\n"
"v_mfma_f32_16x16x32_bf16 v[164:167], a[204:207], a[164:167], v[164:167]\n"
"v_xor_b32 v244, s74, v244\n"
"v_xor_b32 v246, s74, v246\n"
"v_mfma_f32_16x16x32_bf16 v[160:163], a[192:195], a[160:163], v[160:163]\n"
"s_mov_b32 m0, s50\n"
"v_xor_b32 v245, s74, v245\n"
"v_xor_b32 v247, s74, v247\n"
"v_mfma_f32_16x16x32_bf16 v[160:163], a[196:199], a[164:167], v[160:163]\n"
"buffer_load_dwordx4 v224, s[28:31], s48 offen lds\n"
"v_mfma_f32_16x16x32_bf16 v[176:179], a[192:195], a[168:171], v[176:179]\n"
"v_xor_b32 v252, s74, v252\n"
"v_xor_b32 v253, s74, v253\n"
"v_mfma_f32_16x16x32_bf16 v[176:179], a[196:199], a[172:175], v[176:179]\n"
"v_xor_b32 v250, s74, v250\n"
"v_xor_b32 v251, s74, v251\n"
"v_mfma_f32_16x16x32_bf16 v[180:183], a[200:203], a[168:171], v[180:183]\n"
"v_mfma_f32_16x16x32_bf16 v[180:183], a[204:207], a[172:175], v[180:183]\n"
"buffer_load_dwordx4 v225, s[28:31], s48 offen offset:1024 lds\n"
"v_mfma_f32_16x16x32_bf16 v[184:187], a[208:211], a[168:171], v[184:187]\n"
"v_mfma_f32_16x16x32_bf16 v[184:187], a[212:215], a[172:175], v[184:187]\n"
"v_mfma_f32_16x16x32_bf16 v[188:191], a[216:219], a[168:171], v[188:191]\n"
"v_mfma_f32_16x16x32_bf16 v[188:191], a[220:223], a[172:175], v[188:191]\n"
"v_mfma_f32_16x16x32_bf16 v[204:207], a[216:219], a[176:179], v[204:207]\n"
"v_mfma_f32_16x16x32_bf16 v[204:207], a[220:223], a[180:183], v[204:207]\n"
"buffer_load_dwordx4 v226, s[28:31], s48 offen offset:2048 lds\n"
"v_mfma_f32_16x16x32_bf16 v[200:203], a[208:211], a[176:179], v[200:203]\n"
"v_mfma_f32_16x16x32_bf16 v[200:203], a[212:215], a[180:183], v[200:203]\n"
"v_mfma_f32_16x16x32_bf16 v[196:199], a[200:203], a[176:179], v[196:199]\n"
"v_mfma_f32_16x16x32_bf16 v[196:199], a[204:207], a[180:183], v[196:199]\n"
"v_mfma_f32_16x16x32_bf16 v[192:195], a[192:195], a[176:179], v[192:195]\n"
"v_mfma_f32_16x16x32_bf16 v[192:195], a[196:199], a[180:183], v[192:195]\n"
"s_waitcnt lgkmcnt(0)\n"
"buffer_load_dwordx4 v227, s[28:31], s48 offen offset:3072 lds\n"
"v_mfma_f32_16x16x32_bf16 v[208:211], a[192:195], a[184:187], v[208:211]\n"
"s_xor_b32 s50, s50, s74\n"
"v_mfma_f32_16x16x32_bf16 v[208:211], a[196:199], a[188:191], v[208:211]\n"
"v_mfma_f32_16x16x32_bf16 v[212:215], a[200:203], a[184:187], v[212:215]\n"
"v_mfma_f32_16x16x32_bf16 v[212:215], a[204:207], a[188:191], v[212:215]\n"
"v_mfma_f32_16x16x32_bf16 v[216:219], a[208:211], a[184:187], v[216:219]\n"
"v_mfma_f32_16x16x32_bf16 v[216:219], a[212:215], a[188:191], v[216:219]\n"
"v_mfma_f32_16x16x32_bf16 v[220:223], a[216:219], a[184:187], v[220:223]\n"
"v_mfma_f32_16x16x32_bf16 v[220:223], a[220:223], a[188:191], v[220:223]\n"
"v_mfma_f32_16x16x32_bf16 v[0:3], a[224:227], a[136:139], v[0:3]\n"
"v_mfma_f32_16x16x32_bf16 v[0:3], a[228:231], a[140:143], v[0:3]\n"
"v_mfma_f32_16x16x32_bf16 v[4:7], a[232:235], a[136:139], v[4:7]\n"
"s_waitcnt vmcnt(8)\n"
"v_mfma_f32_16x16x32_bf16 v[4:7], a[236:239], a[140:143], v[4:7]\n"
"s_barrier\n"
"v_mfma_f32_16x16x32_bf16 v[8:11], a[240:243], a[136:139], v[8:11]\n"
"s_mov_b32 m0, s51\n"
"ds_read_b64_tr_b16 a[192:193], v244 offset:0\n"
"v_mfma_f32_16x16x32_bf16 v[8:11], a[244:247], a[140:143], v[8:11]\n"
"buffer_load_dwordx4 v248, s[24:27], s49 offen lds\n"
"v_mfma_f32_16x16x32_bf16 v[12:15], a[248:251], a[136:139], v[12:15]\n"
"ds_read_b64_tr_b16 a[194:195], v244 offset:512\n"
"v_mfma_f32_16x16x32_bf16 v[12:15], a[252:255], a[140:143], v[12:15]\n"
"ds_read_b128 a[136:139], v246 offset:0\n"
"v_mfma_f32_16x16x32_bf16 v[28:31], a[248:251], a[144:147], v[28:31]\n"
"ds_read_b64_tr_b16 a[196:197], v244 offset:8192\n"
"v_mfma_f32_16x16x32_bf16 v[28:31], a[252:255], a[148:151], v[28:31]\n"
"ds_read_b64_tr_b16 a[198:199], v244 offset:8704\n"
"v_mfma_f32_16x16x32_bf16 v[24:27], a[240:243], a[144:147], v[24:27]\n"
"ds_read_b128 a[140:143], v247 offset:0\n"
"v_mfma_f32_16x16x32_bf16 v[24:27], a[244:247], a[148:151], v[24:27]\n"
"buffer_load_dwordx4 v249, s[24:27], s49 offen offset:1024 lds\n"
"v_mfma_f32_16x16x32_bf16 v[20:23], a[232:235], a[144:147], v[20:23]\n"
"ds_read_b64_tr_b16 a[200:201], v245 offset:0\n"
"v_mfma_f32_16x16x32_bf16 v[20:23], a[236:239], a[148:151], v[20:23]\n"
"ds_read_b64_tr_b16 a[202:203], v245 offset:512\n"
"v_mfma_f32_16x16x32_bf16 v[16:19], a[224:227], a[144:147], v[16:19]\n"
"ds_read_b64_tr_b16 a[204:205], v245 offset:8192\n"
"v_mfma_f32_16x16x32_bf16 v[16:19], a[228:231], a[148:151], v[16:19]\n"
"ds_read_b64_tr_b16 a[206:207], v245 offset:8704\n"
"v_mfma_f32_16x16x32_bf16 v[32:35], a[224:227], a[152:155], v[32:35]\n"
"ds_read_b64_tr_b16 a[208:209], v244 offset:1024\n"
"v_mfma_f32_16x16x32_bf16 v[32:35], a[228:231], a[156:159], v[32:35]\n"
"buffer_load_dwordx4 v254, s[24:27], s49 offen offset:2048 lds\n"
"v_mfma_f32_16x16x32_bf16 v[36:39], a[232:235], a[152:155], v[36:39]\n"
"ds_read_b64_tr_b16 a[210:211], v244 offset:1536\n"
"v_mfma_f32_16x16x32_bf16 v[36:39], a[236:239], a[156:159], v[36:39]\n"
"ds_read_b64_tr_b16 a[212:213], v244 offset:9216\n"
"v_mfma_f32_16x16x32_bf16 v[40:43], a[240:243], a[152:155], v[40:43]\n"
"ds_read_b64_tr_b16 a[214:215], v244 offset:9728\n"
"v_mfma_f32_16x16x32_bf16 v[40:43], a[244:247], a[156:159], v[40:43]\n"
"ds_read_b64_tr_b16 a[216:217], v245 offset:1024\n"
"v_mfma_f32_16x16x32_bf16 v[44:47], a[248:251], a[152:155], v[44:47]\n"
"ds_read_b64_tr_b16 a[218:219], v245 offset:1536\n"
"v_mfma_f32_16x16x32_bf16 v[44:47], a[252:255], a[156:159], v[44:47]\n"
"buffer_load_dwordx4 v255, s[24:27], s49 offen offset:3072 lds\n"
"v_mfma_f32_16x16x32_bf16 v[60:63], a[248:251], a[160:163], v[60:63]\n"
"ds_read_b64_tr_b16 a[220:221], v245 offset:9216\n"
"v_mfma_f32_16x16x32_bf16 v[60:63], a[252:255], a[164:167], v[60:63]\n"
"ds_read_b64_tr_b16 a[222:223], v245 offset:9728\n"
"v_mfma_f32_16x16x32_bf16 v[56:59], a[240:243], a[160:163], v[56:59]\n"
"ds_read_b128 a[144:147], v246 offset:2048\n"
"v_mfma_f32_16x16x32_bf16 v[56:59], a[244:247], a[164:167], v[56:59]\n"
"s_add_u32 s40, s49, 40960\n"
"ds_read_b128 a[148:151], v247 offset:2048\n"
"v_mfma_f32_16x16x32_bf16 v[52:55], a[232:235], a[160:163], v[52:55]\n"
"s_add_u32 m0, s51, 4096\n"
"ds_read_b128 a[152:155], v246 offset:4096\n"
"v_mfma_f32_16x16x32_bf16 v[52:55], a[236:239], a[164:167], v[52:55]\n"
"buffer_load_dwordx4 v248, s[24:27], s40 offen lds\n"
"v_mfma_f32_16x16x32_bf16 v[48:51], a[224:227], a[160:163], v[48:51]\n"
"ds_read_b128 a[156:159], v247 offset:4096\n"
"v_mfma_f32_16x16x32_bf16 v[48:51], a[228:231], a[164:167], v[48:51]\n"
"ds_read_b128 a[160:163], v252 offset:0\n"
"v_mfma_f32_16x16x32_bf16 v[64:67], a[224:227], a[168:171], v[64:67]\n"
"ds_read_b128 a[164:167], v253 offset:0\n"
"v_mfma_f32_16x16x32_bf16 v[64:67], a[228:231], a[172:175], v[64:67]\n"
"v_mfma_f32_16x16x32_bf16 v[68:71], a[232:235], a[168:171], v[68:71]\n"
"v_mfma_f32_16x16x32_bf16 v[68:71], a[236:239], a[172:175], v[68:71]\n"
"buffer_load_dwordx4 v249, s[24:27], s40 offen offset:1024 lds\n"
"v_mfma_f32_16x16x32_bf16 v[72:75], a[240:243], a[168:171], v[72:75]\n"
"v_mfma_f32_16x16x32_bf16 v[72:75], a[244:247], a[172:175], v[72:75]\n"
"v_mfma_f32_16x16x32_bf16 v[76:79], a[248:251], a[168:171], v[76:79]\n"
"ds_read_b128 a[168:171], v250 offset:0\n"
"v_mfma_f32_16x16x32_bf16 v[76:79], a[252:255], a[172:175], v[76:79]\n"
"ds_read_b128 a[172:175], v251 offset:0\n"
"v_mfma_f32_16x16x32_bf16 v[92:95], a[248:251], a[176:179], v[92:95]\n"
"v_mfma_f32_16x16x32_bf16 v[92:95], a[252:255], a[180:183], v[92:95]\n"
"buffer_load_dwordx4 v254, s[24:27], s40 offen offset:2048 lds\n"
"v_mfma_f32_16x16x32_bf16 v[88:91], a[240:243], a[176:179], v[88:91]\n"
"s_add_u32 s49, s49, 128\n"
"v_mfma_f32_16x16x32_bf16 v[88:91], a[244:247], a[180:183], v[88:91]\n"
"s_sub_u32 s41, s49, 1280\n"
"v_mfma_f32_16x16x32_bf16 v[84:87], a[232:235], a[176:179], v[84:87]\n"
"s_min_u32 s49, s49, s41\n"
"v_mfma_f32_16x16x32_bf16 v[84:87], a[236:239], a[180:183], v[84:87]\n"
"s_xor_b32 s51, s51, s74\n"
"v_mfma_f32_16x16x32_bf16 v[80:83], a[224:227], a[176:179], v[80:83]\n"
"ds_read_b128 a[176:179], v250 offset:2048\n"
"v_mfma_f32_16x16x32_bf16 v[80:83], a[228:231], a[180:183], v[80:83]\n"
"ds_read_b128 a[180:183], v251 offset:2048\n"
"v_mfma_f32_16x16x32_bf16 v[96:99], a[224:227], a[184:187], v[96:99]\n"
"v_mfma_f32_16x16x32_bf16 v[96:99], a[228:231], a[188:191], v[96:99]\n"
"v_mfma_f32_16x16x32_bf16 v[100:103], a[232:235], a[184:187], v[100:103]\n"
"v_mfma_f32_16x16x32_bf16 v[100:103], a[236:239], a[188:191], v[100:103]\n"
"v_mfma_f32_16x16x32_bf16 v[104:107], a[240:243], a[184:187], v[104:107]\n"
"v_mfma_f32_16x16x32_bf16 v[104:107], a[244:247], a[188:191], v[104:107]\n"
"v_mfma_f32_16x16x32_bf16 v[108:111], a[248:251], a[184:187], v[108:111]\n"
"s_add_u32 s47, s47, 1\n"
"ds_read_b128 a[184:187], v250 offset:4096\n"
"v_mfma_f32_16x16x32_bf16 v[108:111], a[252:255], a[188:191], v[108:111]\n"
"s_waitcnt lgkmcnt(1)\n"
"s_cmp_lg_u32 s47, s62\n"
"ds_read_b128 a[188:191], v251 offset:4096\n"
"s_cbranch_scc1 LPL_%=\n"
"LPX_%=:\n"
"s_cmp_eq_u32 s47, 18\n"
"s_cbranch_scc1 LPT2_%=\n"
"v_mfma_f32_16x16x32_bf16 v[112:115], a[192:195], a[136:139], v[112:115]\n"
"s_add_u32 s78, s96, 8192\n"
"v_mov_b32 v241, 1\n"
"v_mfma_f32_16x16x32_bf16 v[112:115], a[196:199], a[140:143], v[112:115]\n"
"s_mov_b64 exec, 1\n"
"buffer_store_dword v241, off, s[32:35], s96 sc0 sc1\n"
"buffer_store_dword v241, off, s[32:35], s78 sc0 sc1\n"
"s_mov_b64 exec, -1\n"
"v_mfma_f32_16x16x32_bf16 v[116:119], a[200:203], a[136:139], v[116:119]\n"
"s_waitcnt vmcnt(13)\n"
"v_mfma_f32_16x16x32_bf16 v[116:119], a[204:207], a[140:143], v[116:119]\n"
"s_barrier\n"
"v_mfma_f32_16x16x32_bf16 v[120:123], a[208:211], a[136:139], v[120:123]\n"
"s_add_u32 s40, s48, 256\n"
"s_xor_b32 m0, s50, 0xc000\n"
"ds_read_b64_tr_b16 a[238:239], v245 offset:25088\n"
"v_mfma_f32_16x16x32_bf16 v[120:123], a[212:215], a[140:143], v[120:123]\n"
"buffer_load_dwordx4 v224, s[28:31], s40 offen lds\n"
"v_mfma_f32_16x16x32_bf16 v[124:127], a[216:219], a[136:139], v[124:127]\n"
"v_mfma_f32_16x16x32_bf16 v[124:127], a[220:223], a[140:143], v[124:127]\n"
"v_mfma_f32_16x16x32_bf16 v[188:191], a[216:219], a[168:171], v[188:191]\n"
"ds_read_b64_tr_b16 a[254:255], v245 offset:26112\n"
"v_mfma_f32_16x16x32_bf16 v[188:191], a[220:223], a[172:175], v[188:191]\n"
"ds_read_b64_tr_b16 a[252:253], v245 offset:25600\n"
"v_mfma_f32_16x16x32_bf16 v[184:187], a[208:211], a[168:171], v[184:187]\n"
"ds_read_b64_tr_b16 a[224:225], v244 offset:16384\n"
"ds_read_b64_tr_b16 a[228:229], v244 offset:24576\n"
"v_mfma_f32_16x16x32_bf16 v[184:187], a[212:215], a[172:175], v[184:187]\n"
"buffer_load_dwordx4 v225, s[28:31], s40 offen offset:1024 lds\n"
"v_mfma_f32_16x16x32_bf16 v[180:183], a[200:203], a[168:171], v[180:183]\n"
"v_mfma_f32_16x16x32_bf16 v[180:183], a[204:207], a[172:175], v[180:183]\n"
"v_mfma_f32_16x16x32_bf16 v[176:179], a[192:195], a[168:171], v[176:179]\n"
"ds_read_b64_tr_b16 a[230:231], v244 offset:25088\n"
"v_mfma_f32_16x16x32_bf16 v[176:179], a[196:199], a[172:175], v[176:179]\n"
"ds_read_b64_tr_b16 a[244:245], v244 offset:25600\n"
"v_mfma_f32_16x16x32_bf16 v[192:195], a[192:195], a[176:179], v[192:195]\n"
"ds_read_b64_tr_b16 a[226:227], v244 offset:16896\n"
"ds_read_b64_tr_b16 a[250:251], v245 offset:17920\n"
"v_mfma_f32_16x16x32_bf16 v[192:195], a[196:199], a[180:183], v[192:195]\n"
"buffer_load_dwordx4 v226, s[28:31], s40 offen offset:2048 lds\n"
"v_mfma_f32_16x16x32_bf16 v[196:199], a[200:203], a[176:179], v[196:199]\n"
"v_mfma_f32_16x16x32_bf16 v[196:199], a[204:207], a[180:183], v[196:199]\n"
"v_mfma_f32_16x16x32_bf16 v[200:203], a[208:211], a[176:179], v[200:203]\n"
"ds_read_b64_tr_b16 a[236:237], v245 offset:24576\n"
"ds_read_b64_tr_b16 a[246:247], v244 offset:26112\n"
"v_mfma_f32_16x16x32_bf16 v[200:203], a[212:215], a[180:183], v[200:203]\n"
"ds_read_b64_tr_b16 a[240:241], v244 offset:17408\n"
"v_mfma_f32_16x16x32_bf16 v[204:207], a[216:219], a[176:179], v[204:207]\n"
"ds_read_b64_tr_b16 a[242:243], v244 offset:17920\n"
"ds_read_b64_tr_b16 a[248:249], v245 offset:17408\n"
"v_mfma_f32_16x16x32_bf16 v[204:207], a[220:223], a[180:183], v[204:207]\n"
"s_waitcnt lgkmcnt(7)\n"
"buffer_load_dwordx4 v227, s[28:31], s40 offen offset:3072 lds\n"
"v_mfma_f32_16x16x32_bf16 v[220:223], a[216:219], a[184:187], v[220:223]\n"
"s_cmp_eq_u32 s47, 8\n"
"v_mfma_f32_16x16x32_bf16 v[220:223], a[220:223], a[188:191], v[220:223]\n"
"s_cselect_b32 s41, s52, s58\n"
"s_nop 1\n"
"v_pk_fma_f32 v[176:177], v[176:177], s[70:71], s[70:71] op_sel:[0,0,1] op_sel_hi:[1,0,1]\nv_pk_fma_f32 v[178:179], v[178:179], s[70:71], s[70:71] op_sel:[0,0,1] op_sel_hi:[1,0,1]\nv_pk_fma_f32 v[180:181], v[180:181], s[70:71], s[70:71] op_sel:[0,0,1] op_sel_hi:[1,0,1]\nv_pk_fma_f32 v[182:183], v[182:183], s[70:71], s[70:71] op_sel:[0,0,1] op_sel_hi:[1,0,1]\nv_pk_fma_f32 v[184:185], v[184:185], s[70:71], s[70:71] op_sel:[0,0,1] op_sel_hi:[1,0,1]\nv_pk_fma_f32 v[186:187], v[186:187], s[70:71], s[70:71] op_sel:[0,0,1] op_sel_hi:[1,0,1]\nv_pk_fma_f32 v[188:189], v[188:189], s[70:71], s[70:71] op_sel:[0,0,1] op_sel_hi:[1,0,1]\nv_pk_fma_f32 v[190:191], v[190:191], s[70:71], s[70:71] op_sel:[0,0,1] op_sel_hi:[1,0,1]\nv_pk_fma_f32 v[192:193], v[192:193], s[70:71], s[70:71] op_sel:[0,0,1] op_sel_hi:[1,0,1]\nv_pk_fma_f32 v[194:195], v[194:195], s[70:71], s[70:71] op_sel:[0,0,1] op_sel_hi:[1,0,1]\nv_pk_fma_f32 v[196:197], v[196:197], s[70:71], s[70:71] op_sel:[0,0,1] op_sel_hi:[1,0,1]\nv_pk_fma_f32 v[198:199], v[198:199], s[70:71], s[70:71] op_sel:[0,0,1] op_sel_hi:[1,0,1]\nv_pk_fma_f32 v[200:201], v[200:201], s[70:71], s[70:71] op_sel:[0,0,1] op_sel_hi:[1,0,1]\nv_pk_fma_f32 v[202:203], v[202:203], s[70:71], s[70:71] op_sel:[0,0,1] op_sel_hi:[1,0,1]\nv_pk_fma_f32 v[204:205], v[204:205], s[70:71], s[70:71] op_sel:[0,0,1] op_sel_hi:[1,0,1]\nv_pk_fma_f32 v[206:207], v[206:207], s[70:71], s[70:71] op_sel:[0,0,1] op_sel_hi:[1,0,1]\n"
"v_mfma_f32_16x16x32_bf16 v[216:219], a[208:211], a[184:187], v[216:219]\n"
"s_add_u32 s48, s48, s41\n"
"v_cvt_pk_bf16_f32 v192, v192, v193\n"
"v_cvt_pk_bf16_f32 v193, v194, v195\n"
"v_mfma_f32_16x16x32_bf16 v[216:219], a[212:215], a[188:191], v[216:219]\n"
"s_sub_u32 s41, s48, 5242880\n"
"v_cvt_pk_bf16_f32 v176, v176, v177\n"
"v_cvt_pk_bf16_f32 v177, v178, v179\n"
"v_mfma_f32_16x16x32_bf16 v[212:215], a[200:203], a[184:187], v[212:215]\n"
"s_min_u32 s48, s48, s41\n"
"v_cvt_pk_bf16_f32 v178, v180, v181\n"
"v_cvt_pk_bf16_f32 v179, v182, v183\n"
"v_mfma_f32_16x16x32_bf16 v[212:215], a[204:207], a[188:191], v[212:215]\n"
"ds_read_b64_tr_b16 a[234:235], v245 offset:16896\n"
"v_xor_b32 v246, s74, v246\n"
"v_mfma_f32_16x16x32_bf16 v[208:211], a[192:195], a[184:187], v[208:211]\n"
"s_mov_b32 m0, s50\n"
"v_and_b32 v182, 0xf0, v242\n"
"v_cvt_pk_bf16_f32 v200, v200, v201\n"
"v_mfma_f32_16x16x32_bf16 v[208:211], a[196:199], a[188:191], v[208:211]\n"
"buffer_load_dwordx4 v224, s[28:31], s48 offen lds\n"
"v_mfma_f32_16x16x32_bf16 v[160:163], a[192:195], a[160:163], v[160:163]\n"
"v_bfe_u32 v180, v242, 9, 1\n"
"v_lshl_add_u32 v180, v180, 8, v182\n"
"v_mfma_f32_16x16x32_bf16 v[160:163], a[196:199], a[164:167], v[160:163]\n"
"v_bfe_u32 v182, v242, 8, 1\n"
"v_cvt_pk_bf16_f32 v184, v184, v185\n"
"v_mfma_f32_16x16x32_bf16 v[164:167], a[200:203], a[160:163], v[164:167]\n"
"ds_read_b64_tr_b16 a[232:233], v245 offset:16384\n"
"v_xor_b32 v247, s74, v247\n"
"v_mfma_f32_16x16x32_bf16 v[164:167], a[204:207], a[164:167], v[164:167]\n"
"buffer_load_dwordx4 v225, s[28:31], s48 offen offset:1024 lds\n"
"v_mfma_f32_16x16x32_bf16 v[168:171], a[208:211], a[160:163], v[168:171]\n"
"v_cvt_pk_bf16_f32 v201, v202, v203\n"
"v_xor_b32 v253, s74, v253\n"
"v_mfma_f32_16x16x32_bf16 v[168:171], a[212:215], a[164:167], v[168:171]\n"
"v_cvt_pk_bf16_f32 v194, v196, v197\n"
"v_xor_b32 v244, s74, v244\n"
"v_mfma_f32_16x16x32_bf16 v[172:175], a[216:219], a[160:163], v[172:175]\n"
"v_lshl_add_u32 v180, v182, 3, v180\n"
"v_cvt_pk_bf16_f32 v185, v186, v187\n"
"v_mfma_f32_16x16x32_bf16 v[172:175], a[220:223], a[164:167], v[172:175]\n"
"v_cvt_pk_bf16_f32 v186, v188, v189\n"
"v_cvt_pk_bf16_f32 v187, v190, v191\n"
"v_mfma_f32_16x16x32_bf16 v[140:143], a[216:219], a[144:147], v[140:143]\n"
"v_cvt_pk_bf16_f32 v203, v206, v207\n"
"v_bfe_u32 v181, v242, 9, 1\n"
"v_mfma_f32_16x16x32_bf16 v[140:143], a[220:223], a[148:151], v[140:143]\n"
"buffer_load_dwordx4 v226, s[28:31], s48 offen offset:2048 lds\n"
"v_mfma_f32_16x16x32_bf16 v[136:139], a[208:211], a[144:147], v[136:139]\n"
"v_xor_b32_e64 v182, s42, 1\n"
"v_lshl_add_u32 v180, v182, 10, v180\n"
"v_mfma_f32_16x16x32_bf16 v[136:139], a[212:215], a[148:151], v[136:139]\n"
"v_cvt_pk_bf16_f32 v202, v204, v205\n"
"v_xor_b32 v252, s74, v252\n"
"v_mfma_f32_16x16x32_bf16 v[132:135], a[200:203], a[144:147], v[132:135]\n"
"v_add_u32 v180, 94208, v180\n"
"v_and_b32 v182, 0xf0, v242\n"
"v_mfma_f32_16x16x32_bf16 v[132:135], a[204:207], a[148:151], v[132:135]\n"
"ds_write2st64_b64 v180, v[184:185], v[186:187] offset0:64 offset1:65\n"
"v_pk_fma_f32 v[208:209], v[208:209], s[70:71], s[70:71] op_sel:[0,0,1] op_sel_hi:[1,0,1]\nv_pk_fma_f32 v[210:211], v[210:211], s[70:71], s[70:71] op_sel:[0,0,1] op_sel_hi:[1,0,1]\nv_pk_fma_f32 v[212:213], v[212:213], s[70:71], s[70:71] op_sel:[0,0,1] op_sel_hi:[1,0,1]\nv_pk_fma_f32 v[214:215], v[214:215], s[70:71], s[70:71] op_sel:[0,0,1] op_sel_hi:[1,0,1]\nv_pk_fma_f32 v[216:217], v[216:217], s[70:71], s[70:71] op_sel:[0,0,1] op_sel_hi:[1,0,1]\nv_pk_fma_f32 v[218:219], v[218:219], s[70:71], s[70:71] op_sel:[0,0,1] op_sel_hi:[1,0,1]\nv_pk_fma_f32 v[220:221], v[220:221], s[70:71], s[70:71] op_sel:[0,0,1] op_sel_hi:[1,0,1]\nv_pk_fma_f32 v[222:223], v[222:223], s[70:71], s[70:71] op_sel:[0,0,1] op_sel_hi:[1,0,1]\nv_pk_fma_f32 v[160:161], v[160:161], s[70:71], s[70:71] op_sel:[0,0,1] op_sel_hi:[1,0,1]\nv_pk_fma_f32 v[162:163], v[162:163], s[70:71], s[70:71] op_sel:[0,0,1] op_sel_hi:[1,0,1]\nv_pk_fma_f32 v[164:165], v[164:165], s[70:71], s[70:71] op_sel:[0,0,1] op_sel_hi:[1,0,1]\nv_pk_fma_f32 v[166:167], v[166:167], s[70:71], s[70:71] op_sel:[0,0,1] op_sel_hi:[1,0,1]\nv_pk_fma_f32 v[168:169], v[168:169], s[70:71], s[70:71] op_sel:[0,0,1] op_sel_hi:[1,0,1]\nv_pk_fma_f32 v[170:171], v[170:171], s[70:71], s[70:71] op_sel:[0,0,1] op_sel_hi:[1,0,1]\nv_pk_fma_f32 v[172:173], v[172:173], s[70:71], s[70:71] op_sel:[0,0,1] op_sel_hi:[1,0,1]\nv_pk_fma_f32 v[174:175], v[174:175], s[70:71], s[70:71] op_sel:[0,0,1] op_sel_hi:[1,0,1]\n"
"v_cvt_pk_bf16_f32 v216, v216, v217\n"
"v_mfma_f32_16x16x32_bf16 v[128:131], a[192:195], a[144:147], v[128:131]\n"
"v_cvt_pk_bf16_f32 v168, v168, v169\n"
"v_xor_b32 v250, s74, v250\n"
"v_mfma_f32_16x16x32_bf16 v[128:131], a[196:199], a[148:151], v[128:131]\n"
"buffer_load_dwordx4 v227, s[28:31], s48 offen offset:3072 lds\n"
"v_mfma_f32_16x16x32_bf16 v[144:147], a[192:195], a[152:155], v[144:147]\n"
"s_xor_b32 s50, s50, s74\n"
"v_cvt_pk_bf16_f32 v208, v208, v209\n"
"v_xor_b32 v251, s74, v251\n"
"v_mfma_f32_16x16x32_bf16 v[144:147], a[196:199], a[156:159], v[144:147]\n"
"v_cvt_pk_bf16_f32 v217, v218, v219\n"
"v_cvt_pk_bf16_f32 v169, v170, v171\n"
"v_mfma_f32_16x16x32_bf16 v[148:151], a[200:203], a[152:155], v[148:151]\n"
"v_cvt_pk_bf16_f32 v195, v198, v199\n"
"v_cvt_pk_bf16_f32 v209, v210, v211\n"
"v_mfma_f32_16x16x32_bf16 v[148:151], a[204:207], a[156:159], v[148:151]\n"
"ds_write2st64_b64 v180, v[192:193], v[194:195] offset0:88 offset1:89\n"
"v_pk_fma_f32 v[112:113], v[112:113], s[70:71], s[70:71] op_sel:[0,0,1] op_sel_hi:[1,0,1]\nv_pk_fma_f32 v[114:115], v[114:115], s[70:71], s[70:71] op_sel:[0,0,1] op_sel_hi:[1,0,1]\nv_pk_fma_f32 v[116:117], v[116:117], s[70:71], s[70:71] op_sel:[0,0,1] op_sel_hi:[1,0,1]\nv_pk_fma_f32 v[118:119], v[118:119], s[70:71], s[70:71] op_sel:[0,0,1] op_sel_hi:[1,0,1]\nv_pk_fma_f32 v[120:121], v[120:121], s[70:71], s[70:71] op_sel:[0,0,1] op_sel_hi:[1,0,1]\nv_pk_fma_f32 v[122:123], v[122:123], s[70:71], s[70:71] op_sel:[0,0,1] op_sel_hi:[1,0,1]\nv_pk_fma_f32 v[124:125], v[124:125], s[70:71], s[70:71] op_sel:[0,0,1] op_sel_hi:[1,0,1]\nv_pk_fma_f32 v[126:127], v[126:127], s[70:71], s[70:71] op_sel:[0,0,1] op_sel_hi:[1,0,1]\nv_pk_fma_f32 v[128:129], v[128:129], s[70:71], s[70:71] op_sel:[0,0,1] op_sel_hi:[1,0,1]\nv_pk_fma_f32 v[130:131], v[130:131], s[70:71], s[70:71] op_sel:[0,0,1] op_sel_hi:[1,0,1]\nv_pk_fma_f32 v[132:133], v[132:133], s[70:71], s[70:71] op_sel:[0,0,1] op_sel_hi:[1,0,1]\nv_pk_fma_f32 v[134:135], v[134:135], s[70:71], s[70:71] op_sel:[0,0,1] op_sel_hi:[1,0,1]\nv_pk_fma_f32 v[136:137], v[136:137], s[70:71], s[70:71] op_sel:[0,0,1] op_sel_hi:[1,0,1]\nv_pk_fma_f32 v[138:139], v[138:139], s[70:71], s[70:71] op_sel:[0,0,1] op_sel_hi:[1,0,1]\nv_pk_fma_f32 v[140:141], v[140:141], s[70:71], s[70:71] op_sel:[0,0,1] op_sel_hi:[1,0,1]\nv_pk_fma_f32 v[142:143], v[142:143], s[70:71], s[70:71] op_sel:[0,0,1] op_sel_hi:[1,0,1]\n"
"v_mfma_f32_16x16x32_bf16 v[152:155], a[208:211], a[152:155], v[152:155]\n"
"v_cvt_pk_bf16_f32 v218, v220, v221\n"
"v_cvt_pk_bf16_f32 v136, v136, v137\n"
"v_mfma_f32_16x16x32_bf16 v[152:155], a[212:215], a[156:159], v[152:155]\n"
"v_cvt_pk_bf16_f32 v137, v138, v139\n"
"v_cvt_pk_bf16_f32 v139, v142, v143\n"
"v_mfma_f32_16x16x32_bf16 v[156:159], a[216:219], a[152:155], v[156:159]\n"
"v_cvt_pk_bf16_f32 v128, v128, v129\n"
"v_cvt_pk_bf16_f32 v138, v140, v141\n"
"v_mfma_f32_16x16x32_bf16 v[156:159], a[220:223], a[156:159], v[156:159]\n"
"s_waitcnt lgkmcnt(1)\n"
"v_cvt_pk_bf16_f32 v112, v112, v113\n"
"v_xor_b32 v245, s74, v245\n"
"v_mfma_f32_16x16x32_bf16 v[96:99], a[224:227], a[184:187], v[96:99]\n"
"v_cvt_pk_bf16_f32 v120, v120, v121\n"
"v_cvt_pk_bf16_f32 v129, v130, v131\n"
"v_mfma_f32_16x16x32_bf16 v[96:99], a[228:231], a[188:191], v[96:99]\n"
"ds_write2st64_b64 v180, v[200:201], v[202:203] offset0:96 offset1:97\n"
"v_cvt_pk_bf16_f32 v219, v222, v223\n"
"v_cvt_pk_bf16_f32 v170, v172, v173\n"
"v_cvt_pk_bf16_f32 v131, v134, v135\n"
"v_lshl_add_u32 v181, v181, 8, v182\n"
"v_bfe_u32 v182, v242, 8, 1\n"
"v_lshl_add_u32 v181, v182, 9, v181\n"
"v_mfma_f32_16x16x32_bf16 v[100:103], a[232:235], a[184:187], v[100:103]\n"
"s_waitcnt vmcnt(10)\n"
"v_permlane16_swap_b32 v136, v138\n"
"v_mfma_f32_16x16x32_bf16 v[100:103], a[236:239], a[188:191], v[100:103]\n"
"s_barrier\n"
"ds_write2st64_b64 v180, v[216:217], v[218:219] offset0:128 offset1:129\n"
"v_pk_fma_f32 v[144:145], v[144:145], s[70:71], s[70:71] op_sel:[0,0,1] op_sel_hi:[1,0,1]\nv_pk_fma_f32 v[146:147], v[146:147], s[70:71], s[70:71] op_sel:[0,0,1] op_sel_hi:[1,0,1]\nv_pk_fma_f32 v[148:149], v[148:149], s[70:71], s[70:71] op_sel:[0,0,1] op_sel_hi:[1,0,1]\nv_pk_fma_f32 v[150:151], v[150:151], s[70:71], s[70:71] op_sel:[0,0,1] op_sel_hi:[1,0,1]\nv_pk_fma_f32 v[152:153], v[152:153], s[70:71], s[70:71] op_sel:[0,0,1] op_sel_hi:[1,0,1]\nv_pk_fma_f32 v[154:155], v[154:155], s[70:71], s[70:71] op_sel:[0,0,1] op_sel_hi:[1,0,1]\nv_pk_fma_f32 v[156:157], v[156:157], s[70:71], s[70:71] op_sel:[0,0,1] op_sel_hi:[1,0,1]\nv_pk_fma_f32 v[158:159], v[158:159], s[70:71], s[70:71] op_sel:[0,0,1] op_sel_hi:[1,0,1]\n"
"v_cvt_pk_bf16_f32 v144, v144, v145\n"
"v_mfma_f32_16x16x32_bf16 v[104:107], a[240:243], a[184:187], v[104:107]\n"
"s_mov_b32 m0, s51\n"
"v_cvt_pk_bf16_f32 v171, v174, v175\n"
"v_cvt_pk_bf16_f32 v121, v122, v123\n"
"v_mfma_f32_16x16x32_bf16 v[104:107], a[244:247], a[188:191], v[104:107]\n"
"buffer_load_dwordx4 v248, s[24:27], s49 offen lds\n"
"v_mfma_f32_16x16x32_bf16 v[108:111], a[248:251], a[184:187], v[108:111]\n"
"v_xor_b32_e64 v182, s42, 0\n"
"v_lshl_add_u32 v181, v182, 10, v181\n"
"v_mfma_f32_16x16x32_bf16 v[108:111], a[252:255], a[188:191], v[108:111]\n"
"ds_read_b64_tr_b16 a[218:219], v245 offset:1536\n"
"v_cvt_pk_bf16_f32 v211, v214, v215\n"
"v_mfma_f32_16x16x32_bf16 v[92:95], a[248:251], a[176:179], v[92:95]\n"
"v_cvt_pk_bf16_f32 v160, v160, v161\n"
"v_cvt_pk_bf16_f32 v145, v146, v147\n"
"v_mfma_f32_16x16x32_bf16 v[92:95], a[252:255], a[180:183], v[92:95]\n"
"ds_write2st64_b64 v180, v[176:177], v[178:179] offset0:0 offset1:1\n"
"v_cvt_pk_bf16_f32 v122, v124, v125\n"
"v_cvt_pk_bf16_f32 v123, v126, v127\n"
"v_cvt_pk_bf16_f32 v146, v148, v149\n"
"v_cvt_pk_bf16_f32 v152, v152, v153\n"
"v_cvt_pk_bf16_f32 v153, v154, v155\n"
"v_cvt_pk_bf16_f32 v155, v158, v159\n"
"v_mfma_f32_16x16x32_bf16 v[88:91], a[240:243], a[176:179], v[88:91]\n"
"v_cvt_pk_bf16_f32 v210, v212, v213\n"
"v_cvt_pk_bf16_f32 v113, v114, v115\n"
"v_mfma_f32_16x16x32_bf16 v[88:91], a[244:247], a[180:183], v[88:91]\n"
"buffer_load_dwordx4 v249, s[24:27], s49 offen offset:1024 lds\n"
"v_mfma_f32_16x16x32_bf16 v[84:87], a[232:235], a[176:179], v[84:87]\n"
"v_cvt_pk_bf16_f32 v161, v162, v163\n"
"v_cvt_pk_bf16_f32 v154, v156, v157\n"
"v_mfma_f32_16x16x32_bf16 v[84:87], a[236:239], a[180:183], v[84:87]\n"
"s_nop 0\n"
"v_permlane16_swap_b32 v152, v154\n"
"v_mfma_f32_16x16x32_bf16 v[80:83], a[224:227], a[176:179], v[80:83]\n"
"ds_read_b64_tr_b16 a[196:197], v244 offset:8192\n"
"ds_read_b64_tr_b16 a[214:215], v244 offset:9728\n"
"v_mfma_f32_16x16x32_bf16 v[80:83], a[228:231], a[180:183], v[80:83]\n"
"ds_write2st64_b64 v180, v[208:209], v[210:211] offset0:120 offset1:121\n"
"v_cvt_pk_bf16_f32 v114, v116, v117\n"
"v_permlane16_swap_b32 v137, v139\n"
"v_permlane16_swap_b32 v144, v146\n"
"v_mfma_f32_16x16x32_bf16 v[64:67], a[224:227], a[168:171], v[64:67]\n"
"v_permlane16_swap_b32 v121, v123\n"
"v_mfma_f32_16x16x32_bf16 v[64:67], a[228:231], a[172:175], v[64:67]\n"
"buffer_load_dwordx4 v254, s[24:27], s49 offen offset:2048 lds\n"
"v_mfma_f32_16x16x32_bf16 v[68:71], a[232:235], a[168:171], v[68:71]\n"
"v_permlane16_swap_b32 v120, v122\n"
"v_mfma_f32_16x16x32_bf16 v[68:71], a[236:239], a[172:175], v[68:71]\n"
"v_cvt_pk_bf16_f32 v115, v118, v119\n"
"v_accvgpr_write_b32 a24, v136\n"
"v_mfma_f32_16x16x32_bf16 v[72:75], a[240:243], a[168:171], v[72:75]\n"
"ds_read_b64_tr_b16 a[206:207], v245 offset:8704\n"
"v_add_u32 v181, 94208, v181\n"
"v_mfma_f32_16x16x32_bf16 v[72:75], a[244:247], a[172:175], v[72:75]\n"
"ds_read_b64_tr_b16 a[216:217], v245 offset:1024\n"
"v_permlane16_swap_b32 v112, v114\n"
"v_permlane16_swap_b32 v153, v155\n"
"v_pk_fma_f32 v[96:97], v[96:97], s[70:71], s[70:71] op_sel:[0,0,1] op_sel_hi:[1,0,1]\nv_pk_fma_f32 v[98:99], v[98:99], s[70:71], s[70:71] op_sel:[0,0,1] op_sel_hi:[1,0,1]\nv_pk_fma_f32 v[100:101], v[100:101], s[70:71], s[70:71] op_sel:[0,0,1] op_sel_hi:[1,0,1]\nv_pk_fma_f32 v[102:103], v[102:103], s[70:71], s[70:71] op_sel:[0,0,1] op_sel_hi:[1,0,1]\nv_pk_fma_f32 v[104:105], v[104:105], s[70:71], s[70:71] op_sel:[0,0,1] op_sel_hi:[1,0,1]\nv_pk_fma_f32 v[106:107], v[106:107], s[70:71], s[70:71] op_sel:[0,0,1] op_sel_hi:[1,0,1]\nv_pk_fma_f32 v[108:109], v[108:109], s[70:71], s[70:71] op_sel:[0,0,1] op_sel_hi:[1,0,1]\nv_pk_fma_f32 v[110:111], v[110:111], s[70:71], s[70:71] op_sel:[0,0,1] op_sel_hi:[1,0,1]\nv_pk_fma_f32 v[80:81], v[80:81], s[70:71], s[70:71] op_sel:[0,0,1] op_sel_hi:[1,0,1]\nv_pk_fma_f32 v[82:83], v[82:83], s[70:71], s[70:71] op_sel:[0,0,1] op_sel_hi:[1,0,1]\nv_pk_fma_f32 v[84:85], v[84:85], s[70:71], s[70:71] op_sel:[0,0,1] op_sel_hi:[1,0,1]\nv_pk_fma_f32 v[86:87], v[86:87], s[70:71], s[70:71] op_sel:[0,0,1] op_sel_hi:[1,0,1]\nv_pk_fma_f32 v[88:89], v[88:89], s[70:71], s[70:71] op_sel:[0,0,1] op_sel_hi:[1,0,1]\nv_pk_fma_f32 v[90:91], v[90:91], s[70:71], s[70:71] op_sel:[0,0,1] op_sel_hi:[1,0,1]\nv_pk_fma_f32 v[92:93], v[92:93], s[70:71], s[70:71] op_sel:[0,0,1] op_sel_hi:[1,0,1]\nv_pk_fma_f32 v[94:95], v[94:95], s[70:71], s[70:71] op_sel:[0,0,1] op_sel_hi:[1,0,1]\n"
"v_mfma_f32_16x16x32_bf16 v[76:79], a[248:251], a[168:171], v[76:79]\n"
"ds_read_b64_tr_b16 a[208:209], v244 offset:1024\n"
"v_cvt_pk_bf16_f32 v80, v80, v81\n"
"v_mfma_f32_16x16x32_bf16 v[76:79], a[252:255], a[172:175], v[76:79]\n"
"buffer_load_dwordx4 v255, s[24:27], s49 offen offset:3072 lds\n"
"v_mfma_f32_16x16x32_bf16 v[60:63], a[248:251], a[160:163], v[60:63]\n"
"ds_read_b64_tr_b16 a[192:193], v244 offset:0\n"
"ds_read_b64_tr_b16 a[222:223], v245 offset:9728\n"
"v_mfma_f32_16x16x32_bf16 v[60:63], a[252:255], a[164:167], v[60:63]\n"
"v_permlane16_swap_b32 v168, v170\n"
"v_mfma_f32_16x16x32_bf16 v[56:59], a[240:243], a[160:163], v[56:59]\n"
"v_permlane16_swap_b32 v113, v115\n"
"v_mfma_f32_16x16x32_bf16 v[56:59], a[244:247], a[164:167], v[56:59]\n"
"s_add_u32 s40, s49, 40960\n"
"ds_read_b64_tr_b16 a[210:211], v244 offset:1536\n"
"v_add_u32 v182, s75, v181\n"
"v_mfma_f32_16x16x32_bf16 v[52:55], a[232:235], a[160:163], v[52:55]\n"
"s_add_u32 m0, s51, 4096\n"
"v_cvt_pk_bf16_f32 v130, v132, v133\n"
"v_accvgpr_write_b32 a25, v137\n"
"v_mfma_f32_16x16x32_bf16 v[52:55], a[236:239], a[164:167], v[52:55]\n"
"buffer_load_dwordx4 v248, s[24:27], s40 offen lds\n"
"v_mfma_f32_16x16x32_bf16 v[48:51], a[224:227], a[160:163], v[48:51]\n"
"s_waitcnt vmcnt(63) lgkmcnt(3)\n"
"v_permlane16_swap_b32 v129, v131\n"
"v_mfma_f32_16x16x32_bf16 v[48:51], a[228:231], a[164:167], v[48:51]\n"
"s_barrier\n"
"v_mfma_f32_16x16x32_bf16 v[0:3], a[224:227], a[136:139], v[0:3]\n"
"ds_read_b128 a[36:39], v181 offset:61440\n"
"v_accvgpr_write_b32 a43, v155\n"
"v_mfma_f32_16x16x32_bf16 v[0:3], a[228:231], a[140:143], v[0:3]\n"
"ds_read_b128 a[28:31], v181 offset:49152\n"
"v_accvgpr_write_b32 a10, v122\n"
"v_mfma_f32_16x16x32_bf16 v[4:7], a[232:235], a[136:139], v[4:7]\n"
"v_cvt_pk_bf16_f32 v96, v96, v97\n"
"v_cvt_pk_bf16_f32 v104, v104, v105\n"
"v_mfma_f32_16x16x32_bf16 v[4:7], a[236:239], a[140:143], v[4:7]\n"
"buffer_load_dwordx4 v249, s[24:27], s40 offen offset:1024 lds\n"
"v_mfma_f32_16x16x32_bf16 v[8:11], a[240:243], a[136:139], v[8:11]\n"
"v_cvt_pk_bf16_f32 v97, v98, v99\n"
"v_cvt_pk_bf16_f32 v99, v102, v103\n"
"v_mfma_f32_16x16x32_bf16 v[8:11], a[244:247], a[140:143], v[8:11]\n"
"ds_read_b64_tr_b16 a[220:221], v245 offset:9216\n"
"ds_read_b128 a[12:15], v181 offset:32768\n"
"v_mfma_f32_16x16x32_bf16 v[12:15], a[248:251], a[136:139], v[12:15]\n"
"ds_read_b128 a[164:167], v253 offset:0\n"
"v_accvgpr_write_b32 a32, v144\n"
"v_mfma_f32_16x16x32_bf16 v[12:15], a[252:255], a[140:143], v[12:15]\n"
"ds_read_b128 a[140:143], v247 offset:0\n"
"v_accvgpr_write_b32 a3, v115\n"
"v_mfma_f32_16x16x32_bf16 v[28:31], a[248:251], a[144:147], v[28:31]\n"
"ds_read_b64_tr_b16 a[194:195], v244 offset:512\n"
"v_cvt_pk_bf16_f32 v162, v164, v165\n"
"v_mfma_f32_16x16x32_bf16 v[28:31], a[252:255], a[148:151], v[28:31]\n"
"buffer_load_dwordx4 v254, s[24:27], s40 offen offset:2048 lds\n"
"v_mfma_f32_16x16x32_bf16 v[24:27], a[240:243], a[144:147], v[24:27]\n"
"s_add_u32 s49, s49, 128\n"
"v_cvt_pk_bf16_f32 v147, v150, v151\n"
"v_accvgpr_write_b32 a41, v153\n"
"v_mfma_f32_16x16x32_bf16 v[24:27], a[244:247], a[148:151], v[24:27]\n"
"s_sub_u32 s41, s49, 1280\n"
"v_cvt_pk_bf16_f32 v98, v100, v101\n"
"v_cvt_pk_bf16_f32 v88, v88, v89\n"
"v_mfma_f32_16x16x32_bf16 v[20:23], a[232:235], a[144:147], v[20:23]\n"
"s_min_u32 s49, s49, s41\n"
"ds_read_b128 a[136:139], v246 offset:0\n"
"v_accvgpr_write_b32 a19, v131\n"
"v_mfma_f32_16x16x32_bf16 v[20:23], a[236:239], a[148:151], v[20:23]\n"
"s_xor_b32 s51, s51, s74\n"
"ds_read_b64_tr_b16 a[198:199], v244 offset:8704\n"
"ds_read_b128 a[44:47], v182 offset:0\n"
"v_mfma_f32_16x16x32_bf16 v[16:19], a[224:227], a[144:147], v[16:19]\n"
"ds_read_b64_tr_b16 a[212:213], v244 offset:9216\n"
"v_cvt_pk_bf16_f32 v163, v166, v167\n"
"v_mfma_f32_16x16x32_bf16 v[16:19], a[228:231], a[148:151], v[16:19]\n"
"ds_read_b128 a[148:151], v247 offset:2048\n"
"v_accvgpr_write_b32 a11, v123\n"
"v_mfma_f32_16x16x32_bf16 v[32:35], a[224:227], a[152:155], v[32:35]\n"
"ds_read_b128 a[4:7], v181 offset:0\n"
"v_and_b32 v102, 0xf0, v242\n"
"v_mfma_f32_16x16x32_bf16 v[32:35], a[228:231], a[156:159], v[32:35]\n"
"ds_read_b64_tr_b16 a[202:203], v245 offset:512\n"
"ds_read_b64_tr_b16 a[204:205], v245 offset:8192\n"
"v_mfma_f32_16x16x32_bf16 v[36:39], a[232:235], a[152:155], v[36:39]\n"
"ds_read_b64_tr_b16 a[200:201], v245 offset:0\n"
"v_cvt_pk_bf16_f32 v89, v90, v91\n"
"v_mfma_f32_16x16x32_bf16 v[36:39], a[236:239], a[156:159], v[36:39]\n"
"ds_read_b128 a[20:23], v181 offset:45056\n"
"v_accvgpr_write_b32 a27, v139\n"
"v_mfma_f32_16x16x32_bf16 v[40:43], a[240:243], a[152:155], v[40:43]\n"
"v_accvgpr_write_b32 a0, v112\n"
"v_accvgpr_write_b32 a1, v113\n"
"v_mfma_f32_16x16x32_bf16 v[40:43], a[244:247], a[156:159], v[40:43]\n"
"ds_read_b128 a[180:183], v251 offset:2048\n"
"v_accvgpr_write_b32 a2, v114\n"
"v_mfma_f32_16x16x32_bf16 v[44:47], a[248:251], a[152:155], v[44:47]\n"
"s_add_u32 s47, s47, 1\n"
"ds_read_b128 a[144:147], v246 offset:2048\n"
"v_accvgpr_write_b32 a17, v129\n"
"v_mfma_f32_16x16x32_bf16 v[44:47], a[252:255], a[156:159], v[44:47]\n"
"s_waitcnt lgkmcnt(7)\n"
"ds_read_b128 a[160:163], v252 offset:0\n"
"v_permlane16_swap_b32 v161, v163\n"
"v_permlane16_swap_b32 v169, v171\n"
"v_accvgpr_write_b32 a9, v121\n"
"v_permlane16_swap_b32 v128, v130\n"
"v_permlane16_swap_b32 v145, v147\n"
"v_accvgpr_write_b32 a42, v154\n"
"v_bfe_u32 v100, v242, 9, 1\n"
"v_cvt_pk_bf16_f32 v91, v94, v95\n"
"v_pk_fma_f32 v[0:1], v[0:1], s[70:71], s[70:71] op_sel:[0,0,1] op_sel_hi:[1,0,1]\nv_pk_fma_f32 v[2:3], v[2:3], s[70:71], s[70:71] op_sel:[0,0,1] op_sel_hi:[1,0,1]\nv_pk_fma_f32 v[4:5], v[4:5], s[70:71], s[70:71] op_sel:[0,0,1] op_sel_hi:[1,0,1]\nv_pk_fma_f32 v[6:7], v[6:7], s[70:71], s[70:71] op_sel:[0,0,1] op_sel_hi:[1,0,1]\nv_pk_fma_f32 v[8:9], v[8:9], s[70:71], s[70:71] op_sel:[0,0,1] op_sel_hi:[1,0,1]\nv_pk_fma_f32 v[10:11], v[10:11], s[70:71], s[70:71] op_sel:[0,0,1] op_sel_hi:[1,0,1]\nv_pk_fma_f32 v[12:13], v[12:13], s[70:71], s[70:71] op_sel:[0,0,1] op_sel_hi:[1,0,1]\nv_pk_fma_f32 v[14:15], v[14:15], s[70:71], s[70:71] op_sel:[0,0,1] op_sel_hi:[1,0,1]\nv_pk_fma_f32 v[16:17], v[16:17], s[70:71], s[70:71] op_sel:[0,0,1] op_sel_hi:[1,0,1]\nv_pk_fma_f32 v[18:19], v[18:19], s[70:71], s[70:71] op_sel:[0,0,1] op_sel_hi:[1,0,1]\nv_pk_fma_f32 v[20:21], v[20:21], s[70:71], s[70:71] op_sel:[0,0,1] op_sel_hi:[1,0,1]\nv_pk_fma_f32 v[22:23], v[22:23], s[70:71], s[70:71] op_sel:[0,0,1] op_sel_hi:[1,0,1]\nv_pk_fma_f32 v[24:25], v[24:25], s[70:71], s[70:71] op_sel:[0,0,1] op_sel_hi:[1,0,1]\nv_pk_fma_f32 v[26:27], v[26:27], s[70:71], s[70:71] op_sel:[0,0,1] op_sel_hi:[1,0,1]\nv_pk_fma_f32 v[28:29], v[28:29], s[70:71], s[70:71] op_sel:[0,0,1] op_sel_hi:[1,0,1]\nv_pk_fma_f32 v[30:31], v[30:31], s[70:71], s[70:71] op_sel:[0,0,1] op_sel_hi:[1,0,1]\n"
"v_cvt_pk_bf16_f32 v16, v16, v17\n"
"v_cvt_pk_bf16_f32 v24, v24, v25\n"
"v_cvt_pk_bf16_f32 v25, v26, v27\n"
"v_mfma_f32_16x16x32_bf16 v[112:115], a[192:195], a[136:139], v[228:231]\n"
"v_cvt_pk_bf16_f32 v0, v0, v1\n"
"v_cvt_pk_bf16_f32 v8, v8, v9\n"
"v_mfma_f32_16x16x32_bf16 v[112:115], a[196:199], a[140:143], v[112:115]\n"
"s_waitcnt lgkmcnt(4)\n"
"ds_write_b128 v243, v[168:171] offset:2048\n"
"v_permlane16_swap_b32 v160, v162\n"
"v_accvgpr_write_b32 a8, v120\n"
"v_cvt_pk_bf16_f32 v81, v82, v83\n"
"v_pk_fma_f32 v[64:65], v[64:65], s[70:71], s[70:71] op_sel:[0,0,1] op_sel_hi:[1,0,1]\nv_pk_fma_f32 v[66:67], v[66:67], s[70:71], s[70:71] op_sel:[0,0,1] op_sel_hi:[1,0,1]\nv_pk_fma_f32 v[68:69], v[68:69], s[70:71], s[70:71] op_sel:[0,0,1] op_sel_hi:[1,0,1]\nv_pk_fma_f32 v[70:71], v[70:71], s[70:71], s[70:71] op_sel:[0,0,1] op_sel_hi:[1,0,1]\nv_pk_fma_f32 v[72:73], v[72:73], s[70:71], s[70:71] op_sel:[0,0,1] op_sel_hi:[1,0,1]\nv_pk_fma_f32 v[74:75], v[74:75], s[70:71], s[70:71] op_sel:[0,0,1] op_sel_hi:[1,0,1]\nv_pk_fma_f32 v[76:77], v[76:77], s[70:71], s[70:71] op_sel:[0,0,1] op_sel_hi:[1,0,1]\nv_pk_fma_f32 v[78:79], v[78:79], s[70:71], s[70:71] op_sel:[0,0,1] op_sel_hi:[1,0,1]\nv_pk_fma_f32 v[48:49], v[48:49], s[70:71], s[70:71] op_sel:[0,0,1] op_sel_hi:[1,0,1]\nv_pk_fma_f32 v[50:51], v[50:51], s[70:71], s[70:71] op_sel:[0,0,1] op_sel_hi:[1,0,1]\nv_pk_fma_f32 v[52:53], v[52:53], s[70:71], s[70:71] op_sel:[0,0,1] op_sel_hi:[1,0,1]\nv_pk_fma_f32 v[54:55], v[54:55], s[70:71], s[70:71] op_sel:[0,0,1] op_sel_hi:[1,0,1]\nv_pk_fma_f32 v[56:57], v[56:57], s[70:71], s[70:71] op_sel:[0,0,1] op_sel_hi:[1,0,1]\nv_pk_fma_f32 v[58:59], v[58:59], s[70:71], s[70:71] op_sel:[0,0,1] op_sel_hi:[1,0,1]\nv_pk_fma_f32 v[60:61], v[60:61], s[70:71], s[70:71] op_sel:[0,0,1] op_sel_hi:[1,0,1]\nv_pk_fma_f32 v[62:63], v[62:63], s[70:71], s[70:71] op_sel:[0,0,1] op_sel_hi:[1,0,1]\n"
"v_cvt_pk_bf16_f32 v64, v64, v65\n"
"v_mfma_f32_16x16x32_bf16 v[116:119], a[200:203], a[136:139], v[228:231]\n"
"s_waitcnt vmcnt(11) lgkmcnt(4)\n"
"v_accvgpr_write_b32 a18, v130\n"
"v_accvgpr_write_b32 a26, v138\n"
"v_mfma_f32_16x16x32_bf16 v[116:119], a[204:207], a[140:143], v[116:119]\n"
"s_barrier\n"
"ds_write_b128 v243, v[160:163] offset:0\n"
"v_accvgpr_write_b32 a16, v128\n"
"v_accvgpr_write_b32 a33, v145\n"
"v_cvt_pk_bf16_f32 v1, v2, v3\n"
"v_pk_fma_f32 v[32:33], v[32:33], s[70:71], s[70:71] op_sel:[0,0,1] op_sel_hi:[1,0,1]\nv_pk_fma_f32 v[34:35], v[34:35], s[70:71], s[70:71] op_sel:[0,0,1] op_sel_hi:[1,0,1]\nv_pk_fma_f32 v[36:37], v[36:37], s[70:71], s[70:71] op_sel:[0,0,1] op_sel_hi:[1,0,1]\nv_pk_fma_f32 v[38:39], v[38:39], s[70:71], s[70:71] op_sel:[0,0,1] op_sel_hi:[1,0,1]\nv_pk_fma_f32 v[40:41], v[40:41], s[70:71], s[70:71] op_sel:[0,0,1] op_sel_hi:[1,0,1]\nv_pk_fma_f32 v[42:43], v[42:43], s[70:71], s[70:71] op_sel:[0,0,1] op_sel_hi:[1,0,1]\nv_pk_fma_f32 v[44:45], v[44:45], s[70:71], s[70:71] op_sel:[0,0,1] op_sel_hi:[1,0,1]\nv_pk_fma_f32 v[46:47], v[46:47], s[70:71], s[70:71] op_sel:[0,0,1] op_sel_hi:[1,0,1]\n"
"v_cvt_pk_bf16_f32 v32, v32, v33\n"
"v_mfma_f32_16x16x32_bf16 v[120:123], a[208:211], a[136:139], v[228:231]\n"
"s_add_u32 s40, s48, 256\n"
"s_xor_b32 m0, s50, 0xc000\n"
"ds_read_b128 a[152:155], v246 offset:4096\n"
"v_mfma_f32_16x16x32_bf16 v[120:123], a[212:215], a[140:143], v[120:123]\n"
"buffer_load_dwordx4 v224, s[28:31], s40 offen lds\n"
"v_mfma_f32_16x16x32_bf16 v[124:127], a[216:219], a[136:139], v[228:231]\n"
"v_accvgpr_write_b32 a40, v152\n"
"v_cvt_pk_bf16_f32 v40, v40, v41\n"
"v_mfma_f32_16x16x32_bf16 v[124:127], a[220:223], a[140:143], v[124:127]\n"
"s_waitcnt lgkmcnt(3)\n"
"ds_read_b128 a[168:171], v250 offset:0\n"
"v_cvt_pk_bf16_f32 v105, v106, v107\n"
"v_mfma_f32_16x16x32_bf16 v[140:143], a[216:219], a[144:147], v[228:231]\n"
"ds_read_b128 a[156:159], v247 offset:4096\n"
"v_accvgpr_write_b32 a34, v146\n"
"v_mfma_f32_16x16x32_bf16 v[140:143], a[220:223], a[148:151], v[140:143]\n"
"ds_read_b128 a[172:175], v251 offset:0\n"
"v_cvt_pk_bf16_f32 v2, v4, v5\n"
"v_mfma_f32_16x16x32_bf16 v[136:139], a[208:211], a[144:147], v[228:231]\n"
"ds_read_b64_tr_b16 a[224:225], v244 offset:16384\n"
"v_lshl_add_u32 v100, v100, 8, v102\n"
"v_mfma_f32_16x16x32_bf16 v[136:139], a[212:215], a[148:151], v[136:139]\n"
"buffer_load_dwordx4 v225, s[28:31], s40 offen offset:1024 lds\n"
"v_mfma_f32_16x16x32_bf16 v[132:135], a[200:203], a[144:147], v[228:231]\n"
"v_bfe_u32 v102, v242, 8, 1\n"
"v_cvt_pk_bf16_f32 v41, v42, v43\n"
"v_mfma_f32_16x16x32_bf16 v[132:135], a[204:207], a[148:151], v[132:135]\n"
"v_accvgpr_write_b32 a35, v147\n"
"v_cvt_pk_bf16_f32 v65, v66, v67\n"
"v_mfma_f32_16x16x32_bf16 v[128:131], a[192:195], a[144:147], v[228:231]\n"
"ds_read_b64_tr_b16 a[234:235], v245 offset:16896\n"
"v_cvt_pk_bf16_f32 v72, v72, v73\n"
"v_mfma_f32_16x16x32_bf16 v[128:131], a[196:199], a[148:151], v[128:131]\n"
"s_waitcnt lgkmcnt(4)\n"
"ds_read_b64_tr_b16 a[236:237], v245 offset:24576\n"
"v_bfe_u32 v101, v242, 9, 1\n"
"v_mfma_f32_16x16x32_bf16 v[144:147], a[192:195], a[152:155], v[228:231]\n"
"s_waitcnt lgkmcnt(4)\n"
"ds_read_b64_tr_b16 a[226:227], v244 offset:16896\n"
"v_cvt_pk_bf16_f32 v9, v10, v11\n"
"v_mfma_f32_16x16x32_bf16 v[144:147], a[196:199], a[156:159], v[144:147]\n"
"buffer_load_dwordx4 v226, s[28:31], s40 offen offset:2048 lds\n"
"v_mfma_f32_16x16x32_bf16 v[148:151], a[200:203], a[152:155], v[228:231]\n"
"v_cvt_pk_bf16_f32 v3, v6, v7\n"
"v_cvt_pk_bf16_f32 v17, v18, v19\n"
"v_mfma_f32_16x16x32_bf16 v[148:151], a[204:207], a[156:159], v[148:151]\n"
"v_cvt_pk_bf16_f32 v66, v68, v69\n"
"v_cvt_pk_bf16_f32 v73, v74, v75\n"
"v_mfma_f32_16x16x32_bf16 v[152:155], a[208:211], a[152:155], v[228:231]\n"
"ds_read_b64_tr_b16 a[252:253], v245 offset:25600\n"
"v_lshl_add_u32 v100, v102, 3, v100\n"
"v_mfma_f32_16x16x32_bf16 v[152:155], a[212:215], a[156:159], v[152:155]\n"
"ds_read_b64_tr_b16 a[244:245], v244 offset:25600\n"
"ds_read_b64_tr_b16 a[250:251], v245 offset:17920\n"
"v_mfma_f32_16x16x32_bf16 v[156:159], a[216:219], a[152:155], v[228:231]\n"
"v_cvt_pk_bf16_f32 v48, v48, v49\n"
"v_cvt_pk_bf16_f32 v43, v46, v47\n"
"v_mfma_f32_16x16x32_bf16 v[156:159], a[220:223], a[156:159], v[156:159]\n"
"buffer_load_dwordx4 v227, s[28:31], s40 offen offset:3072 lds\n"
"v_mfma_f32_16x16x32_bf16 v[172:175], a[216:219], a[160:163], v[228:231]\n"
"s_cmp_eq_u32 s47, 8\n"
"v_cvt_pk_bf16_f32 v75, v78, v79\n"
"v_cvt_pk_bf16_f32 v27, v30, v31\n"
"v_mfma_f32_16x16x32_bf16 v[172:175], a[220:223], a[164:167], v[172:175]\n"
"s_cselect_b32 s41, s52, s58\n"
"v_cvt_pk_bf16_f32 v56, v56, v57\n"
"v_permlane16_swap_b32 v1, v3\n"
"v_cvt_pk_bf16_f32 v11, v14, v15\n"
"v_cvt_pk_bf16_f32 v26, v28, v29\n"
"v_cvt_pk_bf16_f32 v33, v34, v35\n"
"v_mfma_f32_16x16x32_bf16 v[168:171], a[208:211], a[160:163], v[228:231]\n"
"s_add_u32 s48, s48, s41\n"
"ds_read_b128 a[176:179], v250 offset:2048\n"
"v_cvt_pk_bf16_f32 v83, v86, v87\n"
"v_mfma_f32_16x16x32_bf16 v[168:171], a[212:215], a[164:167], v[168:171]\n"
"s_sub_u32 s41, s48, 5242880\n"
"ds_read_b64_tr_b16 a[230:231], v244 offset:25088\n"
"ds_read_b64_tr_b16 a[248:249], v245 offset:17408\n"
"v_mfma_f32_16x16x32_bf16 v[164:167], a[200:203], a[160:163], v[228:231]\n"
"s_min_u32 s48, s48, s41\n"
"ds_read_b64_tr_b16 a[232:233], v245 offset:16384\n"
"v_xor_b32_e64 v102, s42, 1\n"
"v_mfma_f32_16x16x32_bf16 v[164:167], a[204:207], a[164:167], v[164:167]\n"
"ds_read_b128 a[184:187], v250 offset:4096\n"
"v_lshl_add_u32 v100, v102, 10, v100\n"
"v_mfma_f32_16x16x32_bf16 v[160:163], a[192:195], a[160:163], v[228:231]\n"
"s_mov_b32 m0, s50\n"
"v_permlane16_swap_b32 v9, v11\n"
"v_mfma_f32_16x16x32_bf16 v[160:163], a[196:199], a[164:167], v[160:163]\n"
"buffer_load_dwordx4 v224, s[28:31], s48 offen lds\n"
"v_mfma_f32_16x16x32_bf16 v[176:179], a[192:195], a[168:171], v[228:231]\n"
"s_waitcnt lgkmcnt(5)\n"
"v_cvt_pk_bf16_f32 v49, v50, v51\n"
"v_cvt_pk_bf16_f32 v34, v36, v37\n"
"v_mfma_f32_16x16x32_bf16 v[176:179], a[196:199], a[172:175], v[176:179]\n"
"v_cvt_pk_bf16_f32 v10, v12, v13\n"
"v_and_b32 v102, 0xf0, v242\n"
"v_mfma_f32_16x16x32_bf16 v[180:183], a[200:203], a[168:171], v[228:231]\n"
"ds_read_b64_tr_b16 a[240:241], v244 offset:17408\n"
"ds_read_b64_tr_b16 a[254:255], v245 offset:26112\n"
"v_mfma_f32_16x16x32_bf16 v[180:183], a[204:207], a[172:175], v[180:183]\n"
"buffer_load_dwordx4 v225, s[28:31], s48 offen offset:1024 lds\n"
"v_mfma_f32_16x16x32_bf16 v[184:187], a[208:211], a[168:171], v[228:231]\n"
"v_cvt_pk_bf16_f32 v90, v92, v93\n"
"v_cvt_pk_bf16_f32 v42, v44, v45\n"
"v_mfma_f32_16x16x32_bf16 v[184:187], a[212:215], a[172:175], v[184:187]\n"
"v_permlane16_swap_b32 v0, v2\n"
"v_mfma_f32_16x16x32_bf16 v[188:191], a[216:219], a[168:171], v[228:231]\n"
"ds_read_b128 a[188:191], v251 offset:4096\n"
"v_accvgpr_write_b32 a51, v3\n"
"v_mfma_f32_16x16x32_bf16 v[188:191], a[220:223], a[172:175], v[188:191]\n"
"s_waitcnt lgkmcnt(3)\n"
"ds_read_b64_tr_b16 a[238:239], v245 offset:25088\n"
"v_cvt_pk_bf16_f32 v67, v70, v71\n"
"v_mfma_f32_16x16x32_bf16 v[204:207], a[216:219], a[176:179], v[228:231]\n"
"ds_read_b64_tr_b16 a[246:247], v244 offset:26112\n"
"v_add_u32 v100, 94208, v100\n"
"v_mfma_f32_16x16x32_bf16 v[204:207], a[220:223], a[180:183], v[204:207]\n"
"buffer_load_dwordx4 v226, s[28:31], s48 offen offset:2048 lds\n"
"v_mfma_f32_16x16x32_bf16 v[200:203], a[208:211], a[176:179], v[228:231]\n"
"v_accvgpr_write_b32 a50, v2\n"
"v_lshl_add_u32 v101, v101, 8, v102\n"
"v_mfma_f32_16x16x32_bf16 v[200:203], a[212:215], a[180:183], v[200:203]\n"
"v_permlane16_swap_b32 v24, v26\n"
"v_mfma_f32_16x16x32_bf16 v[196:199], a[200:203], a[176:179], v[228:231]\n"
"ds_read_b64_tr_b16 a[228:229], v244 offset:24576\n"
"v_cvt_pk_bf16_f32 v106, v108, v109\n"
"v_mfma_f32_16x16x32_bf16 v[196:199], a[204:207], a[180:183], v[196:199]\n"
"ds_write2st64_b64 v100, v[96:97], v[98:99] offset0:120 offset1:121\n"
"v_accvgpr_write_b32 a49, v1\n"
"v_cvt_pk_bf16_f32 v19, v22, v23\n"
"v_permlane16_swap_b32 v25, v27\n"
"v_cvt_pk_bf16_f32 v35, v38, v39\n"
"v_mfma_f32_16x16x32_bf16 v[192:195], a[192:195], a[176:179], v[228:231]\n"
"v_cvt_pk_bf16_f32 v74, v76, v77\n"
"v_cvt_pk_bf16_f32 v18, v20, v21\n"
"v_mfma_f32_16x16x32_bf16 v[192:195], a[196:199], a[180:183], v[192:195]\n"
"buffer_load_dwordx4 v227, s[28:31], s48 offen offset:3072 lds\n"
"v_mfma_f32_16x16x32_bf16 v[208:211], a[192:195], a[184:187], v[228:231]\n"
"s_waitcnt lgkmcnt(2)\n"
"s_xor_b32 s50, s50, s74\n"
"v_permlane16_swap_b32 v33, v35\n"
"v_mfma_f32_16x16x32_bf16 v[208:211], a[196:199], a[188:191], v[208:211]\n"
"v_permlane16_swap_b32 v40, v42\n"
"v_mfma_f32_16x16x32_bf16 v[212:215], a[200:203], a[184:187], v[228:231]\n"
"v_permlane16_swap_b32 v17, v19\n"
"v_mfma_f32_16x16x32_bf16 v[212:215], a[204:207], a[188:191], v[212:215]\n"
"ds_read_b64_tr_b16 a[242:243], v244 offset:17920\n"
"v_cvt_pk_bf16_f32 v107, v110, v111\n"
"v_mfma_f32_16x16x32_bf16 v[216:219], a[208:211], a[184:187], v[228:231]\n"
"v_permlane16_swap_b32 v16, v18\n"
"v_mfma_f32_16x16x32_bf16 v[216:219], a[212:215], a[188:191], v[216:219]\n"
"ds_write2st64_b64 v100, v[104:105], v[106:107] offset0:128 offset1:129\n"
"v_cvt_pk_bf16_f32 v82, v84, v85\n"
"v_accvgpr_write_b32 a75, v27\n"
"v_permlane16_swap_b32 v41, v43\n"
"v_mfma_f32_16x16x32_bf16 v[220:223], a[216:219], a[184:187], v[228:231]\n"
"v_accvgpr_write_b32 a48, v0\n"
"v_accvgpr_write_b32 a64, v16\n"
"v_mfma_f32_16x16x32_bf16 v[220:223], a[220:223], a[188:191], v[220:223]\n"
"ds_write2st64_b64 v100, v[80:81], v[82:83] offset0:88 offset1:89\n"
"v_cvt_pk_bf16_f32 v57, v58, v59\n"
"v_cvt_pk_bf16_f32 v58, v60, v61\n"
"v_permlane16_swap_b32 v8, v10\n"
"v_accvgpr_write_b32 a57, v9\n"
"v_mfma_f32_16x16x32_bf16 v[0:3], a[224:227], a[136:139], v[228:231]\n"
"s_waitcnt lgkmcnt(3)\n"
"v_cvt_pk_bf16_f32 v50, v52, v53\n"
"v_xor_b32 v245, s74, v245\n"
"v_mfma_f32_16x16x32_bf16 v[0:3], a[228:231], a[140:143], v[0:3]\n"
"ds_write2st64_b64 v100, v[72:73], v[74:75] offset0:64 offset1:65\n"
"v_cvt_pk_bf16_f32 v59, v62, v63\n"
"v_accvgpr_write_b32 a58, v10\n"
"v_accvgpr_write_b32 a66, v18\n"
"v_accvgpr_write_b32 a81, v33\n"
"v_mfma_f32_16x16x32_bf16 v[4:7], a[232:235], a[136:139], v[228:231]\n"
"s_waitcnt vmcnt(8) lgkmcnt(3)\n"
"v_permlane16_swap_b32 v57, v59\n"
"v_mfma_f32_16x16x32_bf16 v[4:7], a[236:239], a[140:143], v[4:7]\n"
"s_barrier\n"
"ds_write2st64_b64 v100, v[88:89], v[90:91] offset0:96 offset1:97\n"
"v_accvgpr_write_b32 a56, v8\n"
"v_accvgpr_write_b32 a59, v11\n"
"v_accvgpr_write_b32 a72, v24\n"
"v_mfma_f32_16x16x32_bf16 v[8:11], a[240:243], a[136:139], v[228:231]\n"
"s_mov_b32 m0, s51\n"
"ds_read_b64_tr_b16 a[202:203], v245 offset:512\n"
"v_accvgpr_write_b32 a73, v25\n"
"v_mfma_f32_16x16x32_bf16 v[8:11], a[244:247], a[140:143], v[8:11]\n"
"buffer_load_dwordx4 v248, s[24:27], s49 offen lds\n"
"v_mfma_f32_16x16x32_bf16 v[12:15], a[248:251], a[136:139], v[228:231]\n"
"v_accvgpr_write_b32 a65, v17\n"
"v_accvgpr_write_b32 a67, v19\n"
"v_mfma_f32_16x16x32_bf16 v[12:15], a[252:255], a[140:143], v[12:15]\n"
"v_permlane16_swap_b32 v56, v58\n"
"v_mfma_f32_16x16x32_bf16 v[28:31], a[248:251], a[144:147], v[228:231]\n"
"v_accvgpr_write_b32 a74, v26\n"
"v_accvgpr_write_b32 a83, v35\n"
"v_mfma_f32_16x16x32_bf16 v[28:31], a[252:255], a[148:151], v[28:31]\n"
"ds_write2st64_b64 v100, v[64:65], v[66:67] offset0:0 offset1:1\n"
"v_permlane16_swap_b32 v32, v34\n"
"v_accvgpr_write_b32 a88, v40\n"
"v_mfma_f32_16x16x32_bf16 v[24:27], a[240:243], a[144:147], v[228:231]\n"
"v_accvgpr_write_b32 a89, v41\n"
"v_accvgpr_write_b32 a90, v42\n"
"v_mfma_f32_16x16x32_bf16 v[24:27], a[244:247], a[148:151], v[24:27]\n"
"buffer_load_dwordx4 v249, s[24:27], s49 offen offset:1024 lds\n"
"v_mfma_f32_16x16x32_bf16 v[20:23], a[232:235], a[144:147], v[228:231]\n"
"v_accvgpr_write_b32 a82, v34\n"
"v_accvgpr_write_b32 a91, v43\n"
"v_mfma_f32_16x16x32_bf16 v[20:23], a[236:239], a[148:151], v[20:23]\n"
"v_cvt_pk_bf16_f32 v51, v54, v55\n"
"v_accvgpr_write_b32 a80, v32\n"
"v_mfma_f32_16x16x32_bf16 v[16:19], a[224:227], a[144:147], v[228:231]\n"
"ds_read_b64_tr_b16 a[216:217], v245 offset:1024\n"
"v_xor_b32 v247, s74, v247\n"
"v_mfma_f32_16x16x32_bf16 v[16:19], a[228:231], a[148:151], v[16:19]\n"
"ds_write_b128 v243, v[56:59] offset:6144\n"
"v_mfma_f32_16x16x32_bf16 v[32:35], a[224:227], a[152:155], v[228:231]\n"
"v_permlane16_swap_b32 v48, v50\n"
"v_mfma_f32_16x16x32_bf16 v[32:35], a[228:231], a[156:159], v[32:35]\n"
"buffer_load_dwordx4 v254, s[24:27], s49 offen offset:2048 lds\n"
"v_mfma_f32_16x16x32_bf16 v[36:39], a[232:235], a[152:155], v[228:231]\n"
"s_waitcnt vmcnt(63) lgkmcnt(2)\n"
"v_permlane16_swap_b32 v49, v51\n"
"v_mfma_f32_16x16x32_bf16 v[36:39], a[236:239], a[156:159], v[36:39]\n"
"s_barrier\n"
"v_mfma_f32_16x16x32_bf16 v[40:43], a[240:243], a[152:155], v[228:231]\n"
"ds_read_b64_tr_b16 a[204:205], v245 offset:8192\n"
"ds_read_b128 a[140:143], v247 offset:0\n"
"v_mfma_f32_16x16x32_bf16 v[40:43], a[244:247], a[156:159], v[40:43]\n"
"ds_write_b128 v243, v[48:51] offset:4096\n"
"v_mfma_f32_16x16x32_bf16 v[44:47], a[248:251], a[152:155], v[228:231]\n"
"v_bfe_u32 v102, v242, 8, 1\n"
"v_lshl_add_u32 v101, v102, 9, v101\n"
"v_mfma_f32_16x16x32_bf16 v[44:47], a[252:255], a[156:159], v[44:47]\n"
"buffer_load_dwordx4 v255, s[24:27], s49 offen offset:3072 lds\n"
"v_mfma_f32_16x16x32_bf16 v[60:63], a[248:251], a[160:163], v[228:231]\n"
"v_xor_b32 v244, s74, v244\n"
"v_xor_b32_e64 v102, s42, 0\n"
"v_mfma_f32_16x16x32_bf16 v[60:63], a[252:255], a[164:167], v[60:63]\n"
"v_lshl_add_u32 v101, v102, 10, v101\n"
"v_add_u32 v101, 94208, v101\n"
"v_mfma_f32_16x16x32_bf16 v[56:59], a[240:243], a[160:163], v[228:231]\n"
"ds_read_b64_tr_b16 a[198:199], v244 offset:8704\n"
"ds_read_b64_tr_b16 a[220:221], v245 offset:9216\n"
"v_mfma_f32_16x16x32_bf16 v[56:59], a[244:247], a[164:167], v[56:59]\n"
"s_add_u32 s40, s49, 40960\n"
"ds_read_b128 a[52:55], v101 offset:0\n"
"v_xor_b32 v253, s74, v253\n"
"v_mfma_f32_16x16x32_bf16 v[52:55], a[232:235], a[160:163], v[228:231]\n"
"s_add_u32 m0, s51, 4096\n"
"ds_read_b64_tr_b16 a[206:207], v245 offset:8704\n"
"ds_read_b128 a[76:79], v101 offset:49152\n"
"v_mfma_f32_16x16x32_bf16 v[52:55], a[236:239], a[164:167], v[52:55]\n"
"buffer_load_dwordx4 v248, s[24:27], s40 offen lds\n"
"v_mfma_f32_16x16x32_bf16 v[48:51], a[224:227], a[160:163], v[228:231]\n"
"v_xor_b32 v246, s74, v246\n"
"v_add_u32 v102, s75, v101\n"
"v_mfma_f32_16x16x32_bf16 v[48:51], a[228:231], a[164:167], v[48:51]\n"
"ds_read_b64_tr_b16 a[192:193], v244 offset:0\n"
"ds_read_b128 a[68:71], v101 offset:45056\n"
"v_mfma_f32_16x16x32_bf16 v[64:67], a[224:227], a[168:171], v[228:231]\n"
"ds_read_b64_tr_b16 a[212:213], v244 offset:9216\n"
"ds_read_b128 a[164:167], v253 offset:0\n"
"v_mfma_f32_16x16x32_bf16 v[64:67], a[228:231], a[172:175], v[64:67]\n"
"ds_read_b64_tr_b16 a[208:209], v244 offset:1024\n"
"ds_read_b64_tr_b16 a[218:219], v245 offset:1536\n"
"v_mfma_f32_16x16x32_bf16 v[68:71], a[232:235], a[168:171], v[228:231]\n"
"ds_read_b64_tr_b16 a[200:201], v245 offset:0\n"
"ds_read_b128 a[84:87], v101 offset:61440\n"
"v_mfma_f32_16x16x32_bf16 v[68:71], a[236:239], a[172:175], v[68:71]\n"
"buffer_load_dwordx4 v249, s[24:27], s40 offen offset:1024 lds\n"
"v_mfma_f32_16x16x32_bf16 v[72:75], a[240:243], a[168:171], v[228:231]\n"
"ds_read_b64_tr_b16 a[214:215], v244 offset:9728\n"
"ds_read_b128 a[60:63], v101 offset:32768\n"
"v_mfma_f32_16x16x32_bf16 v[72:75], a[244:247], a[172:175], v[72:75]\n"
"v_xor_b32 v250, s74, v250\n"
"v_xor_b32 v251, s74, v251\n"
"v_mfma_f32_16x16x32_bf16 v[76:79], a[248:251], a[168:171], v[228:231]\n"
"ds_read_b128 a[92:95], v102 offset:0\n"
"ds_read_b128 a[168:171], v250 offset:0\n"
"v_mfma_f32_16x16x32_bf16 v[76:79], a[252:255], a[172:175], v[76:79]\n"
"ds_read_b128 a[152:155], v246 offset:4096\n"
"v_xor_b32 v252, s74, v252\n"
"v_mfma_f32_16x16x32_bf16 v[92:95], a[248:251], a[176:179], v[228:231]\n"
"ds_read_b64_tr_b16 a[222:223], v245 offset:9728\n"
"ds_read_b128 a[160:163], v252 offset:0\n"
"v_mfma_f32_16x16x32_bf16 v[92:95], a[252:255], a[180:183], v[92:95]\n"
"buffer_load_dwordx4 v254, s[24:27], s40 offen offset:2048 lds\n"
"v_mfma_f32_16x16x32_bf16 v[88:91], a[240:243], a[176:179], v[228:231]\n"
"s_add_u32 s49, s49, 128\n"
"ds_read_b128 a[144:147], v246 offset:2048\n"
"ds_read_b128 a[148:151], v247 offset:2048\n"
"v_mfma_f32_16x16x32_bf16 v[88:91], a[244:247], a[180:183], v[88:91]\n"
"s_sub_u32 s41, s49, 1280\n"
"ds_read_b128 a[136:139], v246 offset:0\n"
"v_mfma_f32_16x16x32_bf16 v[84:87], a[232:235], a[176:179], v[228:231]\n"
"s_min_u32 s49, s49, s41\n"
"ds_read_b64_tr_b16 a[196:197], v244 offset:8192\n"
"ds_read_b64_tr_b16 a[210:211], v244 offset:1536\n"
"v_mfma_f32_16x16x32_bf16 v[84:87], a[236:239], a[180:183], v[84:87]\n"
"s_xor_b32 s51, s51, s74\n"
"ds_read_b64_tr_b16 a[194:195], v244 offset:512\n"
"ds_read_b128 a[156:159], v247 offset:4096\n"
"v_mfma_f32_16x16x32_bf16 v[80:83], a[224:227], a[176:179], v[228:231]\n"
"ds_read_b128 a[176:179], v250 offset:2048\n"
"v_mfma_f32_16x16x32_bf16 v[80:83], a[228:231], a[180:183], v[80:83]\n"
"ds_read_b128 a[172:175], v251 offset:0\n"
"ds_read_b128 a[180:183], v251 offset:2048\n"
"v_mfma_f32_16x16x32_bf16 v[96:99], a[224:227], a[184:187], v[228:231]\n"
"v_mfma_f32_16x16x32_bf16 v[96:99], a[228:231], a[188:191], v[96:99]\n"
"v_mfma_f32_16x16x32_bf16 v[100:103], a[232:235], a[184:187], v[228:231]\n"
"v_mfma_f32_16x16x32_bf16 v[100:103], a[236:239], a[188:191], v[100:103]\n"
"v_mfma_f32_16x16x32_bf16 v[104:107], a[240:243], a[184:187], v[228:231]\n"
"v_mfma_f32_16x16x32_bf16 v[104:107], a[244:247], a[188:191], v[104:107]\n"
"v_mfma_f32_16x16x32_bf16 v[108:111], a[248:251], a[184:187], v[228:231]\n"
"s_add_u32 s47, s47, 1\n"
"ds_read_b128 a[184:187], v250 offset:4096\n"
"v_mfma_f32_16x16x32_bf16 v[108:111], a[252:255], a[188:191], v[108:111]\n"
"s_waitcnt lgkmcnt(1)\n"
"ds_read_b128 a[188:191], v251 offset:4096\n"
"s_mov_b32 s62, 18\n"
"s_branch LPE_%=\n"
"LPT2_%=:\n"
"v_mfma_f32_16x16x32_bf16 v[112:115], a[192:195], a[136:139], v[112:115]\n"
"s_add_u32 s78, s32, s96\ns_addc_u32 s79, s33, 0\n"
"s_mov_b64 s[80:81], 0\n"
"s_mov_b32 s82, 0\n"
"v_mfma_f32_16x16x32_bf16 v[112:115], a[196:199], a[140:143], v[112:115]\n"
"v_mfma_f32_16x16x32_bf16 v[116:119], a[200:203], a[136:139], v[116:119]\n"
"s_waitcnt vmcnt(11)\n"
"s_load_dword s80, s[78:79], 0\n"
"v_mfma_f32_16x16x32_bf16 v[116:119], a[204:207], a[140:143], v[116:119]\n"
"s_barrier\n"
"v_mfma_f32_16x16x32_bf16 v[120:123], a[208:211], a[136:139], v[120:123]\n"
"s_add_u32 s40, s48, 256\n"
"s_xor_b32 m0, s50, 0xc000\n"
"ds_read_b64_tr_b16 a[254:255], v245 offset:26112\n"
"v_mfma_f32_16x16x32_bf16 v[120:123], a[212:215], a[140:143], v[120:123]\n"
"buffer_load_dwordx4 v224, s[28:31], s40 offen lds\n"
"v_mfma_f32_16x16x32_bf16 v[124:127], a[216:219], a[136:139], v[124:127]\n"
"v_mfma_f32_16x16x32_bf16 v[124:127], a[220:223], a[140:143], v[124:127]\n"
"v_mfma_f32_16x16x32_bf16 v[140:143], a[216:219], a[144:147], v[140:143]\n"
"ds_read_b64_tr_b16 a[246:247], v244 offset:26112\n"
"v_mfma_f32_16x16x32_bf16 v[140:143], a[220:223], a[148:151], v[140:143]\n"
"v_mfma_f32_16x16x32_bf16 v[136:139], a[208:211], a[144:147], v[136:139]\n"
"ds_read_b64_tr_b16 a[226:227], v244 offset:16896\n"
"v_mfma_f32_16x16x32_bf16 v[136:139], a[212:215], a[148:151], v[136:139]\n"
"buffer_load_dwordx4 v225, s[28:31], s40 offen offset:1024 lds\n"
"v_mfma_f32_16x16x32_bf16 v[132:135], a[200:203], a[144:147], v[132:135]\n"
"s_add_u32 s82, s82, s80\n"
"v_mfma_f32_16x16x32_bf16 v[132:135], a[204:207], a[148:151], v[132:135]\n"
"s_add_u32 s82, s82, s80\n"
"v_mfma_f32_16x16x32_bf16 v[128:131], a[192:195], a[144:147], v[128:131]\n"
"s_add_u32 s82, s82, s80\n"
"ds_read_b64_tr_b16 a[244:245], v244 offset:25600\n"
"v_mfma_f32_16x16x32_bf16 v[128:131], a[196:199], a[148:151], v[128:131]\n"
"s_add_u32 s82, s82, s80\n"
"v_mfma_f32_16x16x32_bf16 v[144:147], a[192:195], a[152:155], v[144:147]\n"
"s_add_u32 s82, s82, s80\n"
"ds_read_b64_tr_b16 a[236:237], v245 offset:24576\n"
"v_mfma_f32_16x16x32_bf16 v[144:147], a[196:199], a[156:159], v[144:147]\n"
"s_add_u32 s82, s82, s80\n"
"v_mfma_f32_16x16x32_bf16 v[148:151], a[200:203], a[152:155], v[148:151]\n"
"s_add_u32 s82, s82, s80\n"
"ds_read_b64_tr_b16 a[224:225], v244 offset:16384\n"
"v_mfma_f32_16x16x32_bf16 v[148:151], a[204:207], a[156:159], v[148:151]\n"
"s_add_u32 s82, s82, s80\n"
"buffer_load_dwordx4 v226, s[28:31], s40 offen offset:2048 lds\n"
"v_mfma_f32_16x16x32_bf16 v[152:155], a[208:211], a[152:155], v[152:155]\n"
"s_add_u32 s82, s82, s80\n"
"v_mfma_f32_16x16x32_bf16 v[152:155], a[212:215], a[156:159], v[152:155]\n"
"s_add_u32 s82, s82, s80\n"
"v_mfma_f32_16x16x32_bf16 v[156:159], a[216:219], a[152:155], v[156:159]\n"
"s_add_u32 s82, s82, s80\n"
"ds_read_b64_tr_b16 a[234:235], v245 offset:16896\n"
"v_mfma_f32_16x16x32_bf16 v[156:159], a[220:223], a[156:159], v[156:159]\n"
"s_add_u32 s82, s82, s80\n"
"v_mfma_f32_16x16x32_bf16 v[172:175], a[216:219], a[160:163], v[172:175]\n"
"s_add_u32 s82, s82, s80\n"
"ds_read_b64_tr_b16 a[250:251], v245 offset:17920\n"
"v_mfma_f32_16x16x32_bf16 v[172:175], a[220:223], a[164:167], v[172:175]\n"
"s_add_u32 s82, s82, s80\n"
"buffer_load_dwordx4 v227, s[28:31], s40 offen offset:3072 lds\n"
"v_mfma_f32_16x16x32_bf16 v[168:171], a[208:211], a[160:163], v[168:171]\n"
"s_add_u32 s82, s82, s80\n"
"s_mov_b32 s58, 245760\n"
"v_mfma_f32_16x16x32_bf16 v[168:171], a[212:215], a[164:167], v[168:171]\n"
"s_add_u32 s82, s82, s80\n"
"s_mov_b32 s47, 0\n"
"v_mfma_f32_16x16x32_bf16 v[164:167], a[200:203], a[160:163], v[164:167]\n"
"s_add_u32 s82, s82, s80\n"
"s_mov_b32 s52, -896\n"
"ds_read_b64_tr_b16 a[252:253], v245 offset:25600\n"
"v_mfma_f32_16x16x32_bf16 v[164:167], a[204:207], a[164:167], v[164:167]\n"
"s_load_dword s81, s[78:79], 0x2000\n"
"s_mov_b32 s53, 0\n"
"v_mfma_f32_16x16x32_bf16 v[160:163], a[192:195], a[160:163], v[160:163]\n"
"s_mov_b32 m0, s44\n"
"ds_read_b64_tr_b16 a[248:249], v245 offset:17408\n"
"v_mfma_f32_16x16x32_bf16 v[160:163], a[196:199], a[164:167], v[160:163]\n"
"buffer_load_dwordx4 v240, s[16:19], s56 offen nt lds\n"
"v_mfma_f32_16x16x32_bf16 v[176:179], a[192:195], a[168:171], v[176:179]\n"
"v_mfma_f32_16x16x32_bf16 v[176:179], a[196:199], a[172:175], v[176:179]\n"
"v_mfma_f32_16x16x32_bf16 v[180:183], a[200:203], a[168:171], v[180:183]\n"
"ds_read_b64_tr_b16 a[230:231], v244 offset:25088\n"
"v_mfma_f32_16x16x32_bf16 v[180:183], a[204:207], a[172:175], v[180:183]\n"
"v_mfma_f32_16x16x32_bf16 v[184:187], a[208:211], a[168:171], v[184:187]\n"
"ds_read_b64_tr_b16 a[238:239], v245 offset:25088\n"
"v_mfma_f32_16x16x32_bf16 v[184:187], a[212:215], a[172:175], v[184:187]\n"
"v_mfma_f32_16x16x32_bf16 v[188:191], a[216:219], a[168:171], v[188:191]\n"
"s_add_u32 s40, s56, s52\n"
"ds_read_b64_tr_b16 a[228:229], v244 offset:24576\n"
"v_mfma_f32_16x16x32_bf16 v[188:191], a[220:223], a[172:175], v[188:191]\n"
"s_sub_u32 s82, s82, s81\n"
"buffer_load_dwordx4 v240, s[16:19], s40 offen offset:1024 nt lds\n"
"v_mfma_f32_16x16x32_bf16 v[204:207], a[216:219], a[176:179], v[204:207]\n"
"s_sub_u32 s82, s82, s81\n"
"v_xor_b32 v250, s74, v250\n"
"v_mfma_f32_16x16x32_bf16 v[204:207], a[220:223], a[180:183], v[204:207]\n"
"s_sub_u32 s82, s82, s81\n"
"v_mfma_f32_16x16x32_bf16 v[200:203], a[208:211], a[176:179], v[200:203]\n"
"s_sub_u32 s82, s82, s81\n"
"ds_read_b64_tr_b16 a[232:233], v245 offset:16384\n"
"v_mfma_f32_16x16x32_bf16 v[200:203], a[212:215], a[180:183], v[200:203]\n"
"s_sub_u32 s82, s82, s81\n"
"v_mfma_f32_16x16x32_bf16 v[196:199], a[200:203], a[176:179], v[196:199]\n"
"s_sub_u32 s82, s82, s81\n"
"s_add_u32 s40, s56, s53\n"
"ds_read_b64_tr_b16 a[240:241], v244 offset:17408\n"
"v_mfma_f32_16x16x32_bf16 v[196:199], a[204:207], a[180:183], v[196:199]\n"
"s_sub_u32 s82, s82, s81\n"
"buffer_load_dwordx4 v240, s[16:19], s40 offen offset:2048 nt lds\n"
"v_mfma_f32_16x16x32_bf16 v[192:195], a[192:195], a[176:179], v[192:195]\n"
"s_sub_u32 s82, s82, s81\n"
"v_and_b32 v229, 0x60, v242\n"
"v_mfma_f32_16x16x32_bf16 v[192:195], a[196:199], a[180:183], v[192:195]\n"
"s_waitcnt lgkmcnt(2)\n"
"s_sub_u32 s82, s82, s81\n"
"v_mfma_f32_16x16x32_bf16 v[208:211], a[192:195], a[184:187], v[208:211]\n"
"s_sub_u32 s82, s82, s81\n"
"ds_read_b64_tr_b16 a[242:243], v244 offset:17920\n"
"v_mfma_f32_16x16x32_bf16 v[208:211], a[196:199], a[188:191], v[208:211]\n"
"s_sub_u32 s82, s82, s81\n"
"v_xor_b32 v246, s74, v246\n"
"v_mfma_f32_16x16x32_bf16 v[212:215], a[200:203], a[184:187], v[212:215]\n"
"s_sub_u32 s82, s82, s81\n"
"s_add_u32 s40, s56, s54\n"
"v_mfma_f32_16x16x32_bf16 v[212:215], a[204:207], a[188:191], v[212:215]\n"
"s_sub_u32 s82, s82, s81\n"
"buffer_load_dwordx4 v240, s[16:19], s40 offen offset:3072 nt lds\n"
"v_mfma_f32_16x16x32_bf16 v[216:219], a[208:211], a[184:187], v[216:219]\n"
"s_sub_u32 s82, s82, s81\n"
"v_bfe_u32 v228, v242, 8, 2\n"
"v_mfma_f32_16x16x32_bf16 v[216:219], a[212:215], a[188:191], v[216:219]\n"
"s_sub_u32 s82, s82, s81\n"
"v_mfma_f32_16x16x32_bf16 v[220:223], a[216:219], a[184:187], v[220:223]\n"
"s_sub_u32 s82, s82, s81\n"
"v_mfma_f32_16x16x32_bf16 v[220:223], a[220:223], a[188:191], v[220:223]\n"
"s_sub_u32 s82, s82, s81\n"
"v_mfma_f32_16x16x32_bf16 v[0:3], a[224:227], a[136:139], v[0:3]\n"
"v_mfma_f32_16x16x32_bf16 v[0:3], a[228:231], a[140:143], v[0:3]\n"
"s_waitcnt lgkmcnt(0)\n"
"v_mfma_f32_16x16x32_bf16 v[4:7], a[232:235], a[136:139], v[4:7]\n"
"s_waitcnt vmcnt(8)\n"
"v_xor_b32 v245, s74, v245\n"
"v_mfma_f32_16x16x32_bf16 v[4:7], a[236:239], a[140:143], v[4:7]\n"
"s_barrier\n"
"v_mfma_f32_16x16x32_bf16 v[8:11], a[240:243], a[136:139], v[8:11]\n"
"s_add_u32 m0, s44, 65536\n"
"ds_read_b64_tr_b16 a[204:205], v245 offset:8192\n"
"v_mfma_f32_16x16x32_bf16 v[8:11], a[244:247], a[140:143], v[8:11]\n"
"buffer_load_dwordx4 v240, s[20:23], s56 offen nt lds\n"
"v_mfma_f32_16x16x32_bf16 v[12:15], a[248:251], a[136:139], v[12:15]\n"
"s_lshr_b32 s82, s82, 31\n"
"v_xor_b32 v244, s74, v244\n"
"v_mfma_f32_16x16x32_bf16 v[12:15], a[252:255], a[140:143], v[12:15]\n"
"s_add_u32 s82, s82, s60\n"
"v_xor_b32 v247, s74, v247\n"
"v_mfma_f32_16x16x32_bf16 v[28:31], a[248:251], a[144:147], v[28:31]\n"
"s_add_u32 s82, s82, 32\n"
"ds_read_b64_tr_b16 a[208:209], v244 offset:1024\n"
"v_mfma_f32_16x16x32_bf16 v[28:31], a[252:255], a[148:151], v[28:31]\n"
"v_mov_b32 v254, s82\n"
"ds_read_b64_tr_b16 a[196:197], v244 offset:8192\n"
"v_mfma_f32_16x16x32_bf16 v[24:27], a[240:243], a[144:147], v[24:27]\n"
"v_lshlrev_b32_e64 v255, 2, s93\n"
"ds_read_b64_tr_b16 a[210:211], v244 offset:1536\n"
"v_mfma_f32_16x16x32_bf16 v[24:27], a[244:247], a[148:151], v[24:27]\n"
"ds_read_b64_tr_b16 a[222:223], v245 offset:9728\n"
"v_xor_b32 v251, s74, v251\n"
"v_mfma_f32_16x16x32_bf16 v[20:23], a[232:235], a[144:147], v[20:23]\n"
"s_add_u32 s40, s56, s52\n"
"ds_read_b64_tr_b16 a[202:203], v245 offset:512\n"
"v_mfma_f32_16x16x32_bf16 v[20:23], a[236:239], a[148:151], v[20:23]\n"
"buffer_load_dwordx4 v240, s[20:23], s40 offen offset:1024 nt lds\n"
"v_mfma_f32_16x16x32_bf16 v[16:19], a[224:227], a[144:147], v[16:19]\n"
"v_mfma_f32_16x16x32_bf16 v[16:19], a[228:231], a[148:151], v[16:19]\n"
"s_mov_b64 exec, 1\n"
"global_store_dword v255, v254, s[6:7] offset:256 sc1\n"
"s_mov_b64 exec, -1\n"
"v_mfma_f32_16x16x32_bf16 v[32:35], a[224:227], a[152:155], v[32:35]\n"
"ds_read_b64_tr_b16 a[192:193], v244 offset:0\n"
"v_bfe_i32 v241, v242, 4, 1\n"
"v_mfma_f32_16x16x32_bf16 v[32:35], a[228:231], a[156:159], v[32:35]\n"
"v_mfma_f32_16x16x32_bf16 v[36:39], a[232:235], a[152:155], v[36:39]\n"
"s_add_u32 s40, s56, s53\n"
"ds_read_b128 a[136:139], v246 offset:0\n"
"v_mfma_f32_16x16x32_bf16 v[36:39], a[236:239], a[156:159], v[36:39]\n"
"buffer_load_dwordx4 v240, s[20:23], s40 offen offset:2048 nt lds\n"
"v_mfma_f32_16x16x32_bf16 v[40:43], a[240:243], a[152:155], v[40:43]\n"
"v_mfma_f32_16x16x32_bf16 v[40:43], a[244:247], a[156:159], v[40:43]\n"
"v_mfma_f32_16x16x32_bf16 v[44:47], a[248:251], a[152:155], v[44:47]\n"
"ds_read_b128 a[140:143], v247 offset:0\n"
"v_mfma_f32_16x16x32_bf16 v[44:47], a[252:255], a[156:159], v[44:47]\n"
"v_mfma_f32_16x16x32_bf16 v[60:63], a[248:251], a[160:163], v[60:63]\n"
"s_add_u32 s40, s56, s54\n"
"ds_read_b64_tr_b16 a[194:195], v244 offset:512\n"
"v_mfma_f32_16x16x32_bf16 v[60:63], a[252:255], a[164:167], v[60:63]\n"
"buffer_load_dwordx4 v240, s[20:23], s40 offen offset:3072 nt lds\n"
"v_mfma_f32_16x16x32_bf16 v[56:59], a[240:243], a[160:163], v[56:59]\n"
"s_add_u32 s55, s55, s59\n"
"v_mfma_f32_16x16x32_bf16 v[56:59], a[244:247], a[164:167], v[56:59]\n"
"s_add_u32 s41, s55, s57\n"
"v_mfma_f32_16x16x32_bf16 v[52:55], a[232:235], a[160:163], v[52:55]\n"
"s_and_b32 s41, s41, s58\n"
"ds_read_b64_tr_b16 a[218:219], v245 offset:1536\n"
"v_mfma_f32_16x16x32_bf16 v[52:55], a[236:239], a[164:167], v[52:55]\n"
"s_andn2_b32 s56, s55, s58\n"
"ds_read_b64_tr_b16 a[200:201], v245 offset:0\n"
"v_lshlrev_b32 v228, 4, v228\n"
"v_mfma_f32_16x16x32_bf16 v[48:51], a[224:227], a[160:163], v[48:51]\n"
"s_or_b32 s56, s56, s41\n"
"ds_read_b64_tr_b16 a[212:213], v244 offset:9216\n"
"v_mfma_f32_16x16x32_bf16 v[48:51], a[228:231], a[164:167], v[48:51]\n"
"ds_read_b64_tr_b16 a[198:199], v244 offset:8704\n"
"v_mfma_f32_16x16x32_bf16 v[64:67], a[224:227], a[168:171], v[64:67]\n"
"s_add_u32 m0, s44, 16384\n"
"ds_read_b64_tr_b16 a[214:215], v244 offset:9728\n"
"v_mfma_f32_16x16x32_bf16 v[64:67], a[228:231], a[172:175], v[64:67]\n"
"buffer_load_dwordx4 v240, s[16:19], s56 offen nt lds\n"
"v_mfma_f32_16x16x32_bf16 v[68:71], a[232:235], a[168:171], v[68:71]\n"
"v_mfma_f32_16x16x32_bf16 v[68:71], a[236:239], a[172:175], v[68:71]\n"
"v_mfma_f32_16x16x32_bf16 v[72:75], a[240:243], a[168:171], v[72:75]\n"
"ds_read_b64_tr_b16 a[216:217], v245 offset:1024\n"
"v_and_b32 v254, 0x20, v242\n"
"v_mfma_f32_16x16x32_bf16 v[72:75], a[244:247], a[172:175], v[72:75]\n"
"ds_read_b64_tr_b16 a[220:221], v245 offset:9216\n"
"v_mfma_f32_16x16x32_bf16 v[76:79], a[248:251], a[168:171], v[76:79]\n"
"s_add_u32 s40, s56, s52\n"
"ds_read_b64_tr_b16 a[206:207], v245 offset:8704\n"
"v_mfma_f32_16x16x32_bf16 v[76:79], a[252:255], a[172:175], v[76:79]\n"
"buffer_load_dwordx4 v240, s[16:19], s40 offen offset:1024 nt lds\n"
"v_mfma_f32_16x16x32_bf16 v[92:95], a[248:251], a[176:179], v[92:95]\n"
"v_xor_b32 v228, v228, v229\n"
"v_mfma_f32_16x16x32_bf16 v[92:95], a[252:255], a[180:183], v[92:95]\n"
"v_and_b32 v229, 0x70, v242\n"
"v_mfma_f32_16x16x32_bf16 v[88:91], a[240:243], a[176:179], v[88:91]\n"
"ds_read_b128 a[172:175], v251 offset:0\n"
"v_mfma_f32_16x16x32_bf16 v[88:91], a[244:247], a[180:183], v[88:91]\n"
"v_lshl_add_u32 v228, v229, 3, v228\n"
"v_mfma_f32_16x16x32_bf16 v[84:87], a[232:235], a[176:179], v[84:87]\n"
"s_add_u32 s40, s56, s53\n"
"ds_read_b128 a[168:171], v250 offset:0\n"
"v_add_u32 v254, -1, v254\n"
"v_mfma_f32_16x16x32_bf16 v[84:87], a[236:239], a[180:183], v[84:87]\n"
"buffer_load_dwordx4 v240, s[16:19], s40 offen offset:2048 nt lds\n"
"v_mfma_f32_16x16x32_bf16 v[80:83], a[224:227], a[176:179], v[80:83]\n"
"v_mfma_f32_16x16x32_bf16 v[80:83], a[228:231], a[180:183], v[80:83]\n"
"v_mfma_f32_16x16x32_bf16 v[96:99], a[224:227], a[184:187], v[96:99]\n"
"ds_read_b128 a[152:155], v246 offset:4096\n"
"v_mfma_f32_16x16x32_bf16 v[96:99], a[228:231], a[188:191], v[96:99]\n"
"v_mfma_f32_16x16x32_bf16 v[100:103], a[232:235], a[184:187], v[100:103]\n"
"s_add_u32 s40, s56, s54\n"
"ds_read_b128 a[144:147], v246 offset:2048\n"
"v_and_b32 v241, 0xaaaaaaaa, v241\n"
"v_mfma_f32_16x16x32_bf16 v[100:103], a[236:239], a[188:191], v[100:103]\n"
"buffer_load_dwordx4 v240, s[16:19], s40 offen offset:3072 nt lds\n"
"v_mfma_f32_16x16x32_bf16 v[104:107], a[240:243], a[184:187], v[104:107]\n"
"v_mfma_f32_16x16x32_bf16 v[104:107], a[244:247], a[188:191], v[104:107]\n"
"v_mfma_f32_16x16x32_bf16 v[108:111], a[248:251], a[184:187], v[108:111]\n"
"ds_read_b128 a[180:183], v251 offset:2048\n"
"v_mfma_f32_16x16x32_bf16 v[108:111], a[252:255], a[188:191], v[108:111]\n"
"s_waitcnt lgkmcnt(3)\n"
"v_and_b32 v229, 0x80, v242\n"
"v_mfma_f32_16x16x32_bf16 v[112:115], a[192:195], a[136:139], v[112:115]\n"
"ds_read_b128 a[176:179], v250 offset:2048\n"
"v_and_b32 v249, 0xf0, v242\n"
"v_mfma_f32_16x16x32_bf16 v[112:115], a[196:199], a[140:143], v[112:115]\n"
"v_xor_b32 v253, s74, v253\n"
"v_bfe_u32 v255, v242, 9, 1\n"
"v_mfma_f32_16x16x32_bf16 v[116:119], a[200:203], a[136:139], v[116:119]\n"
"s_waitcnt vmcnt(13)\n"
"ds_read_b128 a[184:187], v250 offset:4096\n"
"v_lshl_add_u32 v255, v255, 8, v249\n"
"v_mfma_f32_16x16x32_bf16 v[116:119], a[204:207], a[140:143], v[116:119]\n"
"s_barrier\n"
"v_mfma_f32_16x16x32_bf16 v[120:123], a[208:211], a[136:139], v[120:123]\n"
"s_add_u32 m0, s44, 81920\n"
"ds_read_b64_tr_b16 a[238:239], v245 offset:25088\n"
"v_mfma_f32_16x16x32_bf16 v[120:123], a[212:215], a[140:143], v[120:123]\n"
"buffer_load_dwordx4 v240, s[20:23], s56 offen nt lds\n"
"v_mfma_f32_16x16x32_bf16 v[124:127], a[216:219], a[136:139], v[124:127]\n"
"v_ashrrev_i32 v254, 31, v254\n"
"v_bfe_u32 v248, v242, 9, 1\n"
"v_mfma_f32_16x16x32_bf16 v[124:127], a[220:223], a[140:143], v[124:127]\n"
"v_lshl_add_u32 v228, v229, 4, v228\n"
"v_bfe_u32 v249, v242, 8, 1\n"
"v_mfma_f32_16x16x32_bf16 v[188:191], a[216:219], a[168:171], v[188:191]\n"
"ds_read_b64_tr_b16 a[248:249], v245 offset:17408\n"
"v_or_b32 v241, 0x44444444, v241\n"
"v_mfma_f32_16x16x32_bf16 v[188:191], a[220:223], a[172:175], v[188:191]\n"
"ds_read_b64_tr_b16 a[242:243], v244 offset:17920\n"
"v_mfma_f32_16x16x32_bf16 v[184:187], a[208:211], a[168:171], v[184:187]\n"
"v_lshl_add_u32 v255, v249, 3, v255\n"
"v_xor_b32_e64 v249, s42, 1\n"
"v_mfma_f32_16x16x32_bf16 v[184:187], a[212:215], a[172:175], v[184:187]\n"
"ds_read_b128 a[188:191], v251 offset:4096\n"
"v_mov_b32 v229, s43\n"
"v_mfma_f32_16x16x32_bf16 v[180:183], a[200:203], a[168:171], v[180:183]\n"
"v_mfma_f32_16x16x32_bf16 v[180:183], a[204:207], a[172:175], v[180:183]\n"
"ds_read_b128 a[148:151], v247 offset:2048\n"
"v_mfma_f32_16x16x32_bf16 v[176:179], a[192:195], a[168:171], v[176:179]\n"
"v_lshl_add_u32 v255, v249, 12, v255\n"
"v_mfma_f32_16x16x32_bf16 v[176:179], a[196:199], a[172:175], v[176:179]\n"
"s_waitcnt lgkmcnt(4)\n"
"ds_read_b64_tr_b16 a[224:225], v244 offset:16384\n"
"v_xor_b32 v252, s74, v252\n"
"v_mfma_f32_16x16x32_bf16 v[192:195], a[192:195], a[176:179], v[192:195]\n"
"ds_read_b64_tr_b16 a[250:251], v245 offset:17920\n"
"v_mul_u32_u24 v229, 0x81, v229\n"
"v_mfma_f32_16x16x32_bf16 v[192:195], a[196:199], a[180:183], v[192:195]\n"
"ds_read_b64_tr_b16 a[236:237], v245 offset:24576\n"
"v_mfma_f32_16x16x32_bf16 v[196:199], a[200:203], a[176:179], v[196:199]\n"
"s_add_u32 s40, s56, s52\n"
"ds_read_b64_tr_b16 a[226:227], v244 offset:16896\n"
"v_mfma_f32_16x16x32_bf16 v[196:199], a[204:207], a[180:183], v[196:199]\n"
"buffer_load_dwordx4 v240, s[20:23], s40 offen offset:1024 nt lds\n"
"v_mfma_f32_16x16x32_bf16 v[200:203], a[208:211], a[176:179], v[200:203]\n"
"v_and_b32 v249, 0xf0, v242\n"
"v_mfma_f32_16x16x32_bf16 v[200:203], a[212:215], a[180:183], v[200:203]\n"
"v_mfma_f32_16x16x32_bf16 v[204:207], a[216:219], a[176:179], v[204:207]\n"
"ds_read_b128 a[164:167], v253 offset:0\n"
"v_mfma_f32_16x16x32_bf16 v[204:207], a[220:223], a[180:183], v[204:207]\n"
"v_mfma_f32_16x16x32_bf16 v[220:223], a[216:219], a[184:187], v[220:223]\n"
"s_waitcnt lgkmcnt(3)\n"
"ds_read_b64_tr_b16 a[234:235], v245 offset:16896\n"
"v_lshl_add_u32 v248, v248, 8, v249\n"
"v_mfma_f32_16x16x32_bf16 v[220:223], a[220:223], a[188:191], v[220:223]\n"
"v_mfma_f32_16x16x32_bf16 v[216:219], a[208:211], a[184:187], v[216:219]\n"
"ds_read_b128 a[160:163], v252 offset:0\n"
"v_xor_b32 v228, v228, v229\n"
"v_mfma_f32_16x16x32_bf16 v[216:219], a[212:215], a[188:191], v[216:219]\n"
"v_pk_fma_f32 v[176:177], v[176:177], s[70:71], s[70:71] op_sel:[0,0,1] op_sel_hi:[1,0,1]\nv_pk_fma_f32 v[178:179], v[178:179], s[70:71], s[70:71] op_sel:[0,0,1] op_sel_hi:[1,0,1]\nv_pk_fma_f32 v[180:181], v[180:181], s[70:71], s[70:71] op_sel:[0,0,1] op_sel_hi:[1,0,1]\nv_pk_fma_f32 v[182:183], v[182:183], s[70:71], s[70:71] op_sel:[0,0,1] op_sel_hi:[1,0,1]\nv_pk_fma_f32 v[184:185], v[184:185], s[70:71], s[70:71] op_sel:[0,0,1] op_sel_hi:[1,0,1]\nv_pk_fma_f32 v[186:187], v[186:187], s[70:71], s[70:71] op_sel:[0,0,1] op_sel_hi:[1,0,1]\nv_pk_fma_f32 v[188:189], v[188:189], s[70:71], s[70:71] op_sel:[0,0,1] op_sel_hi:[1,0,1]\nv_pk_fma_f32 v[190:191], v[190:191], s[70:71], s[70:71] op_sel:[0,0,1] op_sel_hi:[1,0,1]\nv_pk_fma_f32 v[192:193], v[192:193], s[70:71], s[70:71] op_sel:[0,0,1] op_sel_hi:[1,0,1]\nv_pk_fma_f32 v[194:195], v[194:195], s[70:71], s[70:71] op_sel:[0,0,1] op_sel_hi:[1,0,1]\nv_pk_fma_f32 v[196:197], v[196:197], s[70:71], s[70:71] op_sel:[0,0,1] op_sel_hi:[1,0,1]\nv_pk_fma_f32 v[198:199], v[198:199], s[70:71], s[70:71] op_sel:[0,0,1] op_sel_hi:[1,0,1]\nv_pk_fma_f32 v[200:201], v[200:201], s[70:71], s[70:71] op_sel:[0,0,1] op_sel_hi:[1,0,1]\nv_pk_fma_f32 v[202:203], v[202:203], s[70:71], s[70:71] op_sel:[0,0,1] op_sel_hi:[1,0,1]\nv_pk_fma_f32 v[204:205], v[204:205], s[70:71], s[70:71] op_sel:[0,0,1] op_sel_hi:[1,0,1]\nv_pk_fma_f32 v[206:207], v[206:207], s[70:71], s[70:71] op_sel:[0,0,1] op_sel_hi:[1,0,1]\n"
"v_mfma_f32_16x16x32_bf16 v[212:215], a[200:203], a[184:187], v[212:215]\n"
"ds_read_b64_tr_b16 a[252:253], v245 offset:25600\n"
"v_cvt_pk_bf16_f32 v176, v176, v177\n"
"v_mfma_f32_16x16x32_bf16 v[212:215], a[204:207], a[188:191], v[212:215]\n"
"v_cvt_pk_bf16_f32 v177, v178, v179\n"
"v_cvt_pk_bf16_f32 v192, v192, v193\n"
"v_mfma_f32_16x16x32_bf16 v[208:211], a[192:195], a[184:187], v[208:211]\n"
"s_add_u32 s40, s56, s53\n"
"ds_read_b128 a[156:159], v247 offset:4096\n"
"v_cvt_pk_bf16_f32 v184, v184, v185\n"
"v_mfma_f32_16x16x32_bf16 v[208:211], a[196:199], a[188:191], v[208:211]\n"
"s_waitcnt lgkmcnt(2)\n"
"buffer_load_dwordx4 v240, s[20:23], s40 offen offset:2048 nt lds\n"
"v_mfma_f32_16x16x32_bf16 v[160:163], a[192:195], a[160:163], v[160:163]\n"
"v_mfma_f32_16x16x32_bf16 v[160:163], a[196:199], a[164:167], v[160:163]\n"
"v_xor_b32 v230, 0x2000, v228\n"
"v_mfma_f32_16x16x32_bf16 v[164:167], a[200:203], a[160:163], v[164:167]\n"
"ds_read_b64_tr_b16 a[244:245], v244 offset:25600\n"
"v_cvt_pk_bf16_f32 v185, v186, v187\n"
"v_mfma_f32_16x16x32_bf16 v[164:167], a[204:207], a[164:167], v[164:167]\n"
"v_cvt_pk_bf16_f32 v193, v194, v195\n"
"v_cvt_pk_bf16_f32 v200, v200, v201\n"
"v_mfma_f32_16x16x32_bf16 v[168:171], a[208:211], a[160:163], v[168:171]\n"
"ds_read_b64_tr_b16 a[240:241], v244 offset:17408\n"
"v_cvt_pk_bf16_f32 v195, v198, v199\n"
"v_mfma_f32_16x16x32_bf16 v[168:171], a[212:215], a[164:167], v[168:171]\n"
"v_cvt_pk_bf16_f32 v194, v196, v197\n"
"v_xor_b32 v198, 0x400, v243\n"
"v_mfma_f32_16x16x32_bf16 v[172:175], a[216:219], a[160:163], v[172:175]\n"
"ds_read_b64_tr_b16 a[254:255], v245 offset:26112\n"
"v_mfma_f32_16x16x32_bf16 v[172:175], a[220:223], a[164:167], v[172:175]\n"
"v_cvt_pk_bf16_f32 v201, v202, v203\n"
"v_and_b32 v254, 0x3f803f80, v254\n"
"v_mfma_f32_16x16x32_bf16 v[140:143], a[216:219], a[144:147], v[140:143]\n"
"ds_read_b64_tr_b16 a[246:247], v244 offset:26112\n"
"v_bfe_u32 v196, v242, 8, 2\n"
"v_mfma_f32_16x16x32_bf16 v[140:143], a[220:223], a[148:151], v[140:143]\n"
"v_xor_b32 v229, 64, v228\n"
"v_cvt_pk_bf16_f32 v186, v188, v189\n"
"v_mfma_f32_16x16x32_bf16 v[136:139], a[208:211], a[144:147], v[136:139]\n"
"ds_read_b64_tr_b16 a[228:229], v244 offset:24576\n"
"v_bfe_u32 v249, v242, 8, 1\n"
"v_mfma_f32_16x16x32_bf16 v[136:139], a[212:215], a[148:151], v[136:139]\n"
"v_pk_fma_f32 v[208:209], v[208:209], s[70:71], s[70:71] op_sel:[0,0,1] op_sel_hi:[1,0,1]\nv_pk_fma_f32 v[210:211], v[210:211], s[70:71], s[70:71] op_sel:[0,0,1] op_sel_hi:[1,0,1]\nv_pk_fma_f32 v[212:213], v[212:213], s[70:71], s[70:71] op_sel:[0,0,1] op_sel_hi:[1,0,1]\nv_pk_fma_f32 v[214:215], v[214:215], s[70:71], s[70:71] op_sel:[0,0,1] op_sel_hi:[1,0,1]\nv_pk_fma_f32 v[216:217], v[216:217], s[70:71], s[70:71] op_sel:[0,0,1] op_sel_hi:[1,0,1]\nv_pk_fma_f32 v[218:219], v[218:219], s[70:71], s[70:71] op_sel:[0,0,1] op_sel_hi:[1,0,1]\nv_pk_fma_f32 v[220:221], v[220:221], s[70:71], s[70:71] op_sel:[0,0,1] op_sel_hi:[1,0,1]\nv_pk_fma_f32 v[222:223], v[222:223], s[70:71], s[70:71] op_sel:[0,0,1] op_sel_hi:[1,0,1]\nv_pk_fma_f32 v[160:161], v[160:161], s[70:71], s[70:71] op_sel:[0,0,1] op_sel_hi:[1,0,1]\nv_pk_fma_f32 v[162:163], v[162:163], s[70:71], s[70:71] op_sel:[0,0,1] op_sel_hi:[1,0,1]\nv_pk_fma_f32 v[164:165], v[164:165], s[70:71], s[70:71] op_sel:[0,0,1] op_sel_hi:[1,0,1]\nv_pk_fma_f32 v[166:167], v[166:167], s[70:71], s[70:71] op_sel:[0,0,1] op_sel_hi:[1,0,1]\nv_pk_fma_f32 v[168:169], v[168:169], s[70:71], s[70:71] op_sel:[0,0,1] op_sel_hi:[1,0,1]\nv_pk_fma_f32 v[170:171], v[170:171], s[70:71], s[70:71] op_sel:[0,0,1] op_sel_hi:[1,0,1]\nv_pk_fma_f32 v[172:173], v[172:173], s[70:71], s[70:71] op_sel:[0,0,1] op_sel_hi:[1,0,1]\nv_pk_fma_f32 v[174:175], v[174:175], s[70:71], s[70:71] op_sel:[0,0,1] op_sel_hi:[1,0,1]\n"
"v_mfma_f32_16x16x32_bf16 v[132:135], a[200:203], a[144:147], v[132:135]\n"
"s_add_u32 s40, s56, s54\n"
"v_cvt_pk_bf16_f32 v168, v168, v169\n"
"v_cvt_pk_bf16_f32 v169, v170, v171\n"
"v_mfma_f32_16x16x32_bf16 v[132:135], a[204:207], a[148:151], v[132:135]\n"
"buffer_load_dwordx4 v240, s[20:23], s40 offen offset:3072 nt lds\n"
"v_mfma_f32_16x16x32_bf16 v[128:131], a[192:195], a[144:147], v[128:131]\n"
"s_add_u32 s55, s55, s59\n"
"v_cvt_pk_bf16_f32 v216, v216, v217\n"
"v_cvt_pk_bf16_f32 v171, v174, v175\n"
"v_mfma_f32_16x16x32_bf16 v[128:131], a[196:199], a[148:151], v[128:131]\n"
"s_add_u32 s41, s55, s57\n"
"v_cvt_pk_bf16_f32 v179, v182, v183\n"
"v_cvt_pk_bf16_f32 v160, v160, v161\n"
"v_mfma_f32_16x16x32_bf16 v[144:147], a[192:195], a[152:155], v[144:147]\n"
"s_waitcnt lgkmcnt(1)\n"
"s_and_b32 s41, s41, s58\n"
"ds_read_b64_tr_b16 a[230:231], v244 offset:25088\n"
"v_cvt_pk_bf16_f32 v161, v162, v163\n"
"v_mfma_f32_16x16x32_bf16 v[144:147], a[196:199], a[156:159], v[144:147]\n"
"s_andn2_b32 s56, s55, s58\n"
"v_cvt_pk_bf16_f32 v202, v204, v205\n"
"v_cvt_pk_bf16_f32 v217, v218, v219\n"
"v_mfma_f32_16x16x32_bf16 v[148:151], a[200:203], a[152:155], v[148:151]\n"
"s_or_b32 s56, s56, s41\n"
"ds_read_b64_tr_b16 a[232:233], v245 offset:16384\n"
"v_cvt_pk_bf16_f32 v219, v222, v223\n"
"v_mfma_f32_16x16x32_bf16 v[148:151], a[204:207], a[156:159], v[148:151]\n"
"v_pk_fma_f32 v[112:113], v[112:113], s[70:71], s[70:71] op_sel:[0,0,1] op_sel_hi:[1,0,1]\nv_pk_fma_f32 v[114:115], v[114:115], s[70:71], s[70:71] op_sel:[0,0,1] op_sel_hi:[1,0,1]\nv_pk_fma_f32 v[116:117], v[116:117], s[70:71], s[70:71] op_sel:[0,0,1] op_sel_hi:[1,0,1]\nv_pk_fma_f32 v[118:119], v[118:119], s[70:71], s[70:71] op_sel:[0,0,1] op_sel_hi:[1,0,1]\nv_pk_fma_f32 v[120:121], v[120:121], s[70:71], s[70:71] op_sel:[0,0,1] op_sel_hi:[1,0,1]\nv_pk_fma_f32 v[122:123], v[122:123], s[70:71], s[70:71] op_sel:[0,0,1] op_sel_hi:[1,0,1]\nv_pk_fma_f32 v[124:125], v[124:125], s[70:71], s[70:71] op_sel:[0,0,1] op_sel_hi:[1,0,1]\nv_pk_fma_f32 v[126:127], v[126:127], s[70:71], s[70:71] op_sel:[0,0,1] op_sel_hi:[1,0,1]\nv_pk_fma_f32 v[128:129], v[128:129], s[70:71], s[70:71] op_sel:[0,0,1] op_sel_hi:[1,0,1]\nv_pk_fma_f32 v[130:131], v[130:131], s[70:71], s[70:71] op_sel:[0,0,1] op_sel_hi:[1,0,1]\nv_pk_fma_f32 v[132:133], v[132:133], s[70:71], s[70:71] op_sel:[0,0,1] op_sel_hi:[1,0,1]\nv_pk_fma_f32 v[134:135], v[134:135], s[70:71], s[70:71] op_sel:[0,0,1] op_sel_hi:[1,0,1]\nv_pk_fma_f32 v[136:137], v[136:137], s[70:71], s[70:71] op_sel:[0,0,1] op_sel_hi:[1,0,1]\nv_pk_fma_f32 v[138:139], v[138:139], s[70:71], s[70:71] op_sel:[0,0,1] op_sel_hi:[1,0,1]\nv_pk_fma_f32 v[140:141], v[140:141], s[70:71], s[70:71] op_sel:[0,0,1] op_sel_hi:[1,0,1]\nv_pk_fma_f32 v[142:143], v[142:143], s[70:71], s[70:71] op_sel:[0,0,1] op_sel_hi:[1,0,1]\n"
"v_mfma_f32_16x16x32_bf16 v[152:155], a[208:211], a[152:155], v[152:155]\n"
"v_cvt_pk_bf16_f32 v163, v166, v167\n"
"v_cvt_pk_bf16_f32 v120, v120, v121\n"
"v_mfma_f32_16x16x32_bf16 v[152:155], a[212:215], a[156:159], v[152:155]\n"
"v_cvt_pk_bf16_f32 v208, v208, v209\n"
"v_cvt_pk_bf16_f32 v218, v220, v221\n"
"v_mfma_f32_16x16x32_bf16 v[156:159], a[216:219], a[152:155], v[156:159]\n"
"v_cvt_pk_bf16_f32 v162, v164, v165\n"
"v_cvt_pk_bf16_f32 v170, v172, v173\n"
"v_mfma_f32_16x16x32_bf16 v[156:159], a[220:223], a[156:159], v[156:159]\n"
"v_permlane16_swap_b32 v169, v171\n"
"v_mfma_f32_16x16x32_bf16 v[96:99], a[224:227], a[184:187], v[96:99]\n"
"s_waitcnt lgkmcnt(1)\n"
"s_add_u32 m0, s44, 32768\n"
"v_cvt_pk_bf16_f32 v209, v210, v211\n"
"v_cvt_pk_bf16_f32 v128, v128, v129\n"
"v_mfma_f32_16x16x32_bf16 v[96:99], a[228:231], a[188:191], v[96:99]\n"
"s_waitcnt lgkmcnt(0)\n"
"buffer_load_dwordx4 v240, s[16:19], s56 offen nt lds\n"
"v_mfma_f32_16x16x32_bf16 v[100:103], a[232:235], a[184:187], v[100:103]\n"
"s_waitcnt vmcnt(14)\n"
"v_mov_b32 v205, 0\n"
"v_mfma_f32_16x16x32_bf16 v[100:103], a[236:239], a[188:191], v[100:103]\n"
"s_barrier\n"
"v_mfma_f32_16x16x32_bf16 v[104:107], a[240:243], a[184:187], v[104:107]\n"
"v_permlane16_swap_b32 v160, v162\n"
"v_mfma_f32_16x16x32_bf16 v[104:107], a[244:247], a[188:191], v[104:107]\n"
"s_mov_b32 s74, 0x41400000\n"
"ds_read_b128 a[204:207], v198 offset:2048\n"
"v_xor_b32 v231, 0x2000, v229\n"
"v_mfma_f32_16x16x32_bf16 v[108:111], a[248:251], a[184:187], v[108:111]\n"
"v_permlane16_swap_b32 v168, v170\n"
"v_mfma_f32_16x16x32_bf16 v[108:111], a[252:255], a[188:191], v[108:111]\n"
"ds_write2st64_b64 v255, v[192:193], v[194:195] offset0:100 offset1:101\n"
"v_mfma_f32_16x16x32_bf16 v[92:95], a[248:251], a[176:179], v[92:95]\n"
"v_cvt_pk_bf16_f32 v129, v130, v131\n"
"v_cvt_pk_bf16_f32 v136, v136, v137\n"
"v_mfma_f32_16x16x32_bf16 v[92:95], a[252:255], a[180:183], v[92:95]\n"
"v_pk_fma_f32 v[144:145], v[144:145], s[70:71], s[70:71] op_sel:[0,0,1] op_sel_hi:[1,0,1]\nv_pk_fma_f32 v[146:147], v[146:147], s[70:71], s[70:71] op_sel:[0,0,1] op_sel_hi:[1,0,1]\nv_pk_fma_f32 v[148:149], v[148:149], s[70:71], s[70:71] op_sel:[0,0,1] op_sel_hi:[1,0,1]\nv_pk_fma_f32 v[150:151], v[150:151], s[70:71], s[70:71] op_sel:[0,0,1] op_sel_hi:[1,0,1]\nv_pk_fma_f32 v[152:153], v[152:153], s[70:71], s[70:71] op_sel:[0,0,1] op_sel_hi:[1,0,1]\nv_pk_fma_f32 v[154:155], v[154:155], s[70:71], s[70:71] op_sel:[0,0,1] op_sel_hi:[1,0,1]\nv_pk_fma_f32 v[156:157], v[156:157], s[70:71], s[70:71] op_sel:[0,0,1] op_sel_hi:[1,0,1]\nv_pk_fma_f32 v[158:159], v[158:159], s[70:71], s[70:71] op_sel:[0,0,1] op_sel_hi:[1,0,1]\n"
"v_mfma_f32_16x16x32_bf16 v[88:91], a[240:243], a[176:179], v[88:91]\n"
"ds_read_b128 a[192:195], v243 offset:0\n"
"v_cvt_pk_bf16_f32 v187, v190, v191\n"
"v_mfma_f32_16x16x32_bf16 v[88:91], a[244:247], a[180:183], v[88:91]\n"
"ds_write2st64_b64 v255, v[184:185], v[186:187] offset0:98 offset1:99\n"
"v_cvt_pk_bf16_f32 v178, v180, v181\n"
"v_mfma_f32_16x16x32_bf16 v[84:87], a[232:235], a[176:179], v[84:87]\n"
"v_cvt_pk_bf16_f32 v210, v212, v213\n"
"v_lshl_add_u32 v196, v196, 4, s43\n"
"v_mfma_f32_16x16x32_bf16 v[84:87], a[236:239], a[180:183], v[84:87]\n"
"ds_write2st64_b64 v255, v[216:217], v[218:219] offset0:194 offset1:195\n"
"v_mfma_f32_16x16x32_bf16 v[80:83], a[224:227], a[176:179], v[80:83]\n"
"s_add_u32 s40, s56, s52\n"
"v_permlane16_swap_b32 v161, v163\n"
"v_mfma_f32_16x16x32_bf16 v[80:83], a[228:231], a[180:183], v[80:83]\n"
"buffer_load_dwordx4 v240, s[16:19], s40 offen offset:1024 nt lds\n"
"v_mfma_f32_16x16x32_bf16 v[64:67], a[224:227], a[168:171], v[64:67]\n"
"v_cvt_pk_bf16_f32 v211, v214, v215\n"
"v_cvt_pk_bf16_f32 v137, v138, v139\n"
"v_mfma_f32_16x16x32_bf16 v[64:67], a[228:231], a[172:175], v[64:67]\n"
"v_cvt_pk_bf16_f32 v131, v134, v135\n"
"v_cvt_pk_bf16_f32 v144, v144, v145\n"
"v_mfma_f32_16x16x32_bf16 v[68:71], a[232:235], a[168:171], v[68:71]\n"
"v_xor_b32 v197, 64, v196\n"
"v_cvt_pk_bf16_f32 v130, v132, v133\n"
"v_mfma_f32_16x16x32_bf16 v[68:71], a[236:239], a[172:175], v[68:71]\n"
"v_pk_fma_f32 v[96:97], v[96:97], s[70:71], s[70:71] op_sel:[0,0,1] op_sel_hi:[1,0,1]\nv_pk_fma_f32 v[98:99], v[98:99], s[70:71], s[70:71] op_sel:[0,0,1] op_sel_hi:[1,0,1]\nv_pk_fma_f32 v[100:101], v[100:101], s[70:71], s[70:71] op_sel:[0,0,1] op_sel_hi:[1,0,1]\nv_pk_fma_f32 v[102:103], v[102:103], s[70:71], s[70:71] op_sel:[0,0,1] op_sel_hi:[1,0,1]\nv_pk_fma_f32 v[104:105], v[104:105], s[70:71], s[70:71] op_sel:[0,0,1] op_sel_hi:[1,0,1]\nv_pk_fma_f32 v[106:107], v[106:107], s[70:71], s[70:71] op_sel:[0,0,1] op_sel_hi:[1,0,1]\nv_pk_fma_f32 v[108:109], v[108:109], s[70:71], s[70:71] op_sel:[0,0,1] op_sel_hi:[1,0,1]\nv_pk_fma_f32 v[110:111], v[110:111], s[70:71], s[70:71] op_sel:[0,0,1] op_sel_hi:[1,0,1]\nv_pk_fma_f32 v[80:81], v[80:81], s[70:71], s[70:71] op_sel:[0,0,1] op_sel_hi:[1,0,1]\nv_pk_fma_f32 v[82:83], v[82:83], s[70:71], s[70:71] op_sel:[0,0,1] op_sel_hi:[1,0,1]\nv_pk_fma_f32 v[84:85], v[84:85], s[70:71], s[70:71] op_sel:[0,0,1] op_sel_hi:[1,0,1]\nv_pk_fma_f32 v[86:87], v[86:87], s[70:71], s[70:71] op_sel:[0,0,1] op_sel_hi:[1,0,1]\nv_pk_fma_f32 v[88:89], v[88:89], s[70:71], s[70:71] op_sel:[0,0,1] op_sel_hi:[1,0,1]\nv_pk_fma_f32 v[90:91], v[90:91], s[70:71], s[70:71] op_sel:[0,0,1] op_sel_hi:[1,0,1]\nv_pk_fma_f32 v[92:93], v[92:93], s[70:71], s[70:71] op_sel:[0,0,1] op_sel_hi:[1,0,1]\nv_pk_fma_f32 v[94:95], v[94:95], s[70:71], s[70:71] op_sel:[0,0,1] op_sel_hi:[1,0,1]\n"
"v_mfma_f32_16x16x32_bf16 v[72:75], a[240:243], a[168:171], v[72:75]\n"
"v_cvt_pk_bf16_f32 v152, v152, v153\n"
"v_cvt_pk_bf16_f32 v80, v80, v81\n"
"v_mfma_f32_16x16x32_bf16 v[72:75], a[244:247], a[172:175], v[72:75]\n"
"v_cvt_pk_bf16_f32 v153, v154, v155\n"
"v_cvt_pk_bf16_f32 v81, v82, v83\n"
"v_mfma_f32_16x16x32_bf16 v[76:79], a[248:251], a[168:171], v[76:79]\n"
"v_cvt_pk_bf16_f32 v139, v142, v143\n"
"v_cvt_pk_bf16_f32 v104, v104, v105\n"
"v_mfma_f32_16x16x32_bf16 v[76:79], a[252:255], a[172:175], v[76:79]\n"
"ds_read_b128 v[212:215], v197 offset:0\n"
"v_cvt_pk_bf16_f32 v96, v96, v97\n"
"v_mfma_f32_16x16x32_bf16 v[60:63], a[248:251], a[160:163], v[60:63]\n"
"v_cvt_pk_bf16_f32 v138, v140, v141\n"
"v_cvt_pk_bf16_f32 v145, v146, v147\n"
"v_mfma_f32_16x16x32_bf16 v[60:63], a[252:255], a[164:167], v[60:63]\n"
"ds_write_b128 v243, v[168:171] offset:10240\n"
"v_mfma_f32_16x16x32_bf16 v[56:59], a[240:243], a[160:163], v[56:59]\n"
"s_add_u32 s40, s56, s53\n"
"v_cvt_pk_bf16_f32 v121, v122, v123\n"
"v_cvt_pk_bf16_f32 v155, v158, v159\n"
"v_mfma_f32_16x16x32_bf16 v[56:59], a[244:247], a[164:167], v[56:59]\n"
"buffer_load_dwordx4 v240, s[16:19], s40 offen offset:2048 nt lds\n"
"v_mfma_f32_16x16x32_bf16 v[52:55], a[232:235], a[160:163], v[52:55]\n"
"v_permlane16_swap_b32 v137, v139\n"
"v_mfma_f32_16x16x32_bf16 v[52:55], a[236:239], a[164:167], v[52:55]\n"
"v_permlane16_swap_b32 v128, v130\n"
"v_mfma_f32_16x16x32_bf16 v[48:51], a[224:227], a[160:163], v[48:51]\n"
"v_cvt_pk_bf16_f32 v122, v124, v125\n"
"v_cvt_pk_bf16_f32 v147, v150, v151\n"
"v_mfma_f32_16x16x32_bf16 v[48:51], a[228:231], a[164:167], v[48:51]\n"
"v_cvt_pk_bf16_f32 v83, v86, v87\n"
"v_lshl_add_u32 v248, v249, 9, v248\n"
"v_mfma_f32_16x16x32_bf16 v[0:3], a[224:227], a[136:139], v[0:3]\n"
"ds_read_b128 v[216:219], v196 offset:1024\n"
"v_cvt_pk_bf16_f32 v154, v156, v157\n"
"v_mfma_f32_16x16x32_bf16 v[0:3], a[228:231], a[140:143], v[0:3]\n"
"ds_write_b128 v243, v[160:163] offset:8192\n"
"v_mfma_f32_16x16x32_bf16 v[4:7], a[232:235], a[136:139], v[4:7]\n"
"v_permlane16_swap_b32 v136, v138\n"
"v_mfma_f32_16x16x32_bf16 v[4:7], a[236:239], a[140:143], v[4:7]\n"
"v_permlane16_swap_b32 v129, v131\n"
"v_mfma_f32_16x16x32_bf16 v[8:11], a[240:243], a[136:139], v[8:11]\n"
"ds_read_b128 a[200:203], v243 offset:2048\n"
"v_cvt_pk_bf16_f32 v105, v106, v107\n"
"v_mfma_f32_16x16x32_bf16 v[8:11], a[244:247], a[140:143], v[8:11]\n"
"v_accvgpr_write_b32 a120, v136\n"
"v_cvt_pk_bf16_f32 v146, v148, v149\n"
"v_mfma_f32_16x16x32_bf16 v[12:15], a[248:251], a[136:139], v[12:15]\n"
"v_permlane16_swap_b32 v152, v154\n"
"v_mfma_f32_16x16x32_bf16 v[12:15], a[252:255], a[140:143], v[12:15]\n"
"ds_write2st64_b64 v255, v[208:209], v[210:211] offset0:192 offset1:193\n"
"v_pk_fma_f32 v[64:65], v[64:65], s[70:71], s[70:71] op_sel:[0,0,1] op_sel_hi:[1,0,1]\nv_pk_fma_f32 v[66:67], v[66:67], s[70:71], s[70:71] op_sel:[0,0,1] op_sel_hi:[1,0,1]\nv_pk_fma_f32 v[68:69], v[68:69], s[70:71], s[70:71] op_sel:[0,0,1] op_sel_hi:[1,0,1]\nv_pk_fma_f32 v[70:71], v[70:71], s[70:71], s[70:71] op_sel:[0,0,1] op_sel_hi:[1,0,1]\nv_pk_fma_f32 v[72:73], v[72:73], s[70:71], s[70:71] op_sel:[0,0,1] op_sel_hi:[1,0,1]\nv_pk_fma_f32 v[74:75], v[74:75], s[70:71], s[70:71] op_sel:[0,0,1] op_sel_hi:[1,0,1]\nv_pk_fma_f32 v[76:77], v[76:77], s[70:71], s[70:71] op_sel:[0,0,1] op_sel_hi:[1,0,1]\nv_pk_fma_f32 v[78:79], v[78:79], s[70:71], s[70:71] op_sel:[0,0,1] op_sel_hi:[1,0,1]\nv_pk_fma_f32 v[48:49], v[48:49], s[70:71], s[70:71] op_sel:[0,0,1] op_sel_hi:[1,0,1]\nv_pk_fma_f32 v[50:51], v[50:51], s[70:71], s[70:71] op_sel:[0,0,1] op_sel_hi:[1,0,1]\nv_pk_fma_f32 v[52:53], v[52:53], s[70:71], s[70:71] op_sel:[0,0,1] op_sel_hi:[1,0,1]\nv_pk_fma_f32 v[54:55], v[54:55], s[70:71], s[70:71] op_sel:[0,0,1] op_sel_hi:[1,0,1]\nv_pk_fma_f32 v[56:57], v[56:57], s[70:71], s[70:71] op_sel:[0,0,1] op_sel_hi:[1,0,1]\nv_pk_fma_f32 v[58:59], v[58:59], s[70:71], s[70:71] op_sel:[0,0,1] op_sel_hi:[1,0,1]\nv_pk_fma_f32 v[60:61], v[60:61], s[70:71], s[70:71] op_sel:[0,0,1] op_sel_hi:[1,0,1]\nv_pk_fma_f32 v[62:63], v[62:63], s[70:71], s[70:71] op_sel:[0,0,1] op_sel_hi:[1,0,1]\n"
"v_mfma_f32_16x16x32_bf16 v[28:31], a[248:251], a[144:147], v[28:31]\n"
"s_add_u32 s40, s56, s54\n"
"v_permlane16_swap_b32 v145, v147\n"
"v_mfma_f32_16x16x32_bf16 v[28:31], a[252:255], a[148:151], v[28:31]\n"
"buffer_load_dwordx4 v240, s[16:19], s40 offen offset:3072 nt lds\n"
"v_mfma_f32_16x16x32_bf16 v[24:27], a[240:243], a[144:147], v[24:27]\n"
"v_cvt_pk_bf16_f32 v203, v206, v207\n"
"v_cvt_pk_bf16_f32 v72, v72, v73\n"
"v_mfma_f32_16x16x32_bf16 v[24:27], a[244:247], a[148:151], v[24:27]\n"
"v_cvt_pk_bf16_f32 v97, v98, v99\n"
"v_cvt_pk_bf16_f32 v64, v64, v65\n"
"v_mfma_f32_16x16x32_bf16 v[20:23], a[232:235], a[144:147], v[20:23]\n"
"v_cvt_pk_bf16_f32 v106, v108, v109\n"
"v_cvt_pk_bf16_f32 v56, v56, v57\n"
"v_mfma_f32_16x16x32_bf16 v[20:23], a[236:239], a[148:151], v[20:23]\n"
"v_accvgpr_write_b32 a112, v128\n"
"v_accvgpr_write_b32 a131, v147\n"
"v_mfma_f32_16x16x32_bf16 v[16:19], a[224:227], a[144:147], v[16:19]\n"
"ds_read_b128 v[208:211], v196 offset:0\n"
"v_cvt_pk_bf16_f32 v88, v88, v89\n"
"v_mfma_f32_16x16x32_bf16 v[16:19], a[228:231], a[148:151], v[16:19]\n"
"v_accvgpr_write_b32 a113, v129\n"
"v_cvt_pk_bf16_f32 v82, v84, v85\n"
"v_mfma_f32_16x16x32_bf16 v[32:35], a[224:227], a[152:155], v[32:35]\n"
"v_accvgpr_write_b32 a114, v130\n"
"v_cvt_pk_bf16_f32 v57, v58, v59\n"
"v_mfma_f32_16x16x32_bf16 v[32:35], a[228:231], a[156:159], v[32:35]\n"
"v_cvt_pk_bf16_f32 v73, v74, v75\n"
"v_xor_b32_e64 v249, s42, 0\n"
"v_mfma_f32_16x16x32_bf16 v[36:39], a[232:235], a[152:155], v[36:39]\n"
"ds_read_b128 v[220:223], v197 offset:1024\n"
"v_lshl_add_u32 v248, v249, 12, v248\n"
"v_mfma_f32_16x16x32_bf16 v[36:39], a[236:239], a[156:159], v[36:39]\n"
"v_cvt_pk_bf16_f32 v89, v90, v91\n"
"v_cvt_pk_bf16_f32 v65, v66, v67\n"
"v_mfma_f32_16x16x32_bf16 v[40:43], a[240:243], a[152:155], v[40:43]\n"
"v_permlane16_swap_b32 v120, v122\n"
"v_mfma_f32_16x16x32_bf16 v[40:43], a[244:247], a[156:159], v[40:43]\n"
"ds_write2st64_b64 v255, v[200:201], v[202:203] offset0:102 offset1:103\n"
"v_pk_fma_f32 v[0:1], v[0:1], s[70:71], s[70:71] op_sel:[0,0,1] op_sel_hi:[1,0,1]\nv_pk_fma_f32 v[2:3], v[2:3], s[70:71], s[70:71] op_sel:[0,0,1] op_sel_hi:[1,0,1]\nv_pk_fma_f32 v[4:5], v[4:5], s[70:71], s[70:71] op_sel:[0,0,1] op_sel_hi:[1,0,1]\nv_pk_fma_f32 v[6:7], v[6:7], s[70:71], s[70:71] op_sel:[0,0,1] op_sel_hi:[1,0,1]\nv_pk_fma_f32 v[8:9], v[8:9], s[70:71], s[70:71] op_sel:[0,0,1] op_sel_hi:[1,0,1]\nv_pk_fma_f32 v[10:11], v[10:11], s[70:71], s[70:71] op_sel:[0,0,1] op_sel_hi:[1,0,1]\nv_pk_fma_f32 v[12:13], v[12:13], s[70:71], s[70:71] op_sel:[0,0,1] op_sel_hi:[1,0,1]\nv_pk_fma_f32 v[14:15], v[14:15], s[70:71], s[70:71] op_sel:[0,0,1] op_sel_hi:[1,0,1]\nv_pk_fma_f32 v[16:17], v[16:17], s[70:71], s[70:71] op_sel:[0,0,1] op_sel_hi:[1,0,1]\nv_pk_fma_f32 v[18:19], v[18:19], s[70:71], s[70:71] op_sel:[0,0,1] op_sel_hi:[1,0,1]\nv_pk_fma_f32 v[20:21], v[20:21], s[70:71], s[70:71] op_sel:[0,0,1] op_sel_hi:[1,0,1]\nv_pk_fma_f32 v[22:23], v[22:23], s[70:71], s[70:71] op_sel:[0,0,1] op_sel_hi:[1,0,1]\nv_pk_fma_f32 v[24:25], v[24:25], s[70:71], s[70:71] op_sel:[0,0,1] op_sel_hi:[1,0,1]\nv_pk_fma_f32 v[26:27], v[26:27], s[70:71], s[70:71] op_sel:[0,0,1] op_sel_hi:[1,0,1]\nv_pk_fma_f32 v[28:29], v[28:29], s[70:71], s[70:71] op_sel:[0,0,1] op_sel_hi:[1,0,1]\nv_pk_fma_f32 v[30:31], v[30:31], s[70:71], s[70:71] op_sel:[0,0,1] op_sel_hi:[1,0,1]\n"
"v_mfma_f32_16x16x32_bf16 v[44:47], a[248:251], a[152:155], v[44:47]\n"
"v_accvgpr_write_b32 a138, v154\n"
"v_cvt_pk_bf16_f32 v91, v94, v95\n"
"v_mfma_f32_16x16x32_bf16 v[44:47], a[252:255], a[156:159], v[44:47]\n"
"s_waitcnt lgkmcnt(2)\n"
"v_accvgpr_write_b32 a122, v138\n"
"v_accvgpr_write_b32 a242, v254\n"
"v_mfma_f32_16x16x32_bf16 v[180:183], v[208:211], a[0:3], 0\n"
"v_permlane16_swap_b32 v144, v146\n"
"v_mfma_f32_16x16x32_bf16 v[180:183], v[212:215], a[4:7], v[180:183]\n"
"v_permlane16_swap_b32 v153, v155\n"
"v_mfma_f32_16x16x32_bf16 v[180:183], v[216:219], a[8:11], v[180:183]\n"
"s_waitcnt lgkmcnt(1)\n"
"ds_read_b128 a[196:199], v198 offset:0\n"
"v_cvt_pk_bf16_f32 v90, v92, v93\n"
"v_mfma_f32_16x16x32_bf16 v[180:183], v[220:223], a[12:15], v[180:183]\n"
"ds_write2st64_b64 v255, v[80:81], v[82:83] offset0:224 offset1:225\n"
"v_pk_fma_f32 v[32:33], v[32:33], s[70:71], s[70:71] op_sel:[0,0,1] op_sel_hi:[1,0,1]\nv_pk_fma_f32 v[34:35], v[34:35], s[70:71], s[70:71] op_sel:[0,0,1] op_sel_hi:[1,0,1]\nv_pk_fma_f32 v[36:37], v[36:37], s[70:71], s[70:71] op_sel:[0,0,1] op_sel_hi:[1,0,1]\nv_pk_fma_f32 v[38:39], v[38:39], s[70:71], s[70:71] op_sel:[0,0,1] op_sel_hi:[1,0,1]\nv_pk_fma_f32 v[40:41], v[40:41], s[70:71], s[70:71] op_sel:[0,0,1] op_sel_hi:[1,0,1]\nv_pk_fma_f32 v[42:43], v[42:43], s[70:71], s[70:71] op_sel:[0,0,1] op_sel_hi:[1,0,1]\nv_pk_fma_f32 v[44:45], v[44:45], s[70:71], s[70:71] op_sel:[0,0,1] op_sel_hi:[1,0,1]\nv_pk_fma_f32 v[46:47], v[46:47], s[70:71], s[70:71] op_sel:[0,0,1] op_sel_hi:[1,0,1]\n"
"v_mfma_f32_16x16x32_bf16 v[184:187], v[208:211], a[16:19], 0\n"
"v_accvgpr_write_b32 a139, v155\n"
"v_cvt_pk_bf16_f32 v48, v48, v49\n"
"v_mfma_f32_16x16x32_bf16 v[184:187], v[212:215], a[20:23], v[184:187]\n"
"v_accvgpr_write_b32 a123, v139\n"
"v_cvt_pk_bf16_f32 v49, v50, v51\n"
"v_mfma_f32_16x16x32_bf16 v[184:187], v[216:219], a[24:27], v[184:187]\n"
"v_accvgpr_write_b32 a136, v152\n"
"v_cvt_pk_bf16_f32 v40, v40, v41\n"
"v_mfma_f32_16x16x32_bf16 v[184:187], v[220:223], a[28:31], v[184:187]\n"
"ds_write2st64_b64 v255, v[176:177], v[178:179] offset0:96 offset1:97\n"
"v_sub_f32 v180, s74, v180\n"
"v_sub_f32 v181, s74, v181\n"
"v_mfma_f32_16x16x32_bf16 v[188:191], v[208:211], a[32:35], 0\n"
"v_sub_f32 v182, s74, v182\n"
"v_sub_f32 v183, s74, v183\n"
"v_mfma_f32_16x16x32_bf16 v[188:191], v[212:215], a[36:39], v[188:191]\n"
"v_accvgpr_write_b32 a128, v144\n"
"v_cvt_pk_bf16_f32 v74, v76, v77\n"
"v_mfma_f32_16x16x32_bf16 v[188:191], v[216:219], a[40:43], v[188:191]\n"
"v_cvt_pk_bf16_f32 v107, v110, v111\n"
"v_cvt_pk_bf16_f32 v8, v8, v9\n"
"v_mfma_f32_16x16x32_bf16 v[188:191], v[220:223], a[44:47], v[188:191]\n"
"v_cvt_pk_bf16_f32 v98, v100, v101\n"
"v_mov_b32 v199, 0\n"
"v_mfma_f32_16x16x32_bf16 v[192:195], v[208:211], a[192:195], 0\n"
"s_waitcnt lgkmcnt(1)\n"
"ds_read_b128 v[208:211], v228 offset:0\n"
"v_accvgpr_write_b32 a137, v153\n"
"v_mfma_f32_16x16x32_bf16 v[192:195], v[212:215], a[196:199], v[192:195]\n"
"ds_read_b128 v[212:215], v229 offset:0\n"
"v_sub_f32 v185, s74, v185\n"
"v_mfma_f32_16x16x32_bf16 v[192:195], v[216:219], a[200:203], v[192:195]\n"
"ds_read_b128 v[216:219], v228 offset:1024\n"
"v_sub_f32 v190, s74, v190\n"
"v_mfma_f32_16x16x32_bf16 v[192:195], v[220:223], a[204:207], v[192:195]\n"
"s_waitcnt lgkmcnt(2)\n"
"ds_read_b128 v[220:223], v229 offset:1024\n"
"v_mov_b32 v204, 0\n"
"v_mfma_f32_16x16x32_bf16 v[152:155], v[208:211], a[0:3], v[180:183]\n"
"s_waitcnt lgkmcnt(2)\n"
"v_sub_f32 v184, s74, v184\n"
"v_sub_f32 v188, s74, v188\n"
"v_mfma_f32_16x16x32_bf16 v[152:155], v[212:215], a[4:7], v[152:155]\n"
"s_waitcnt lgkmcnt(1)\n"
"v_sub_f32 v186, s74, v186\n"
"v_sub_f32 v187, s74, v187\n"
"v_mfma_f32_16x16x32_bf16 v[152:155], v[216:219], a[8:11], v[152:155]\n"
"s_waitcnt lgkmcnt(0)\n"
"v_cvt_pk_bf16_f32 v99, v102, v103\n"
"v_cvt_pk_bf16_f32 v9, v10, v11\n"
"v_mfma_f32_16x16x32_bf16 v[152:155], v[220:223], a[12:15], v[152:155]\n"
"ds_write2st64_b64 v255, v[88:89], v[90:91] offset0:226 offset1:227\n"
"v_accvgpr_write_b32 a121, v137\n"
"v_cvt_pk_bf16_f32 v58, v60, v61\n"
"v_sub_f32 v191, s74, v191\n"
"v_mfma_f32_16x16x32_bf16 v[156:159], v[216:219], a[24:27], v[184:187]\n"
"v_cvt_pk_bf16_f32 v24, v24, v25\n"
"v_sub_f32 v193, s74, v193\n"
"v_mfma_f32_16x16x32_bf16 v[156:159], v[220:223], a[28:31], v[156:159]\n"
"v_sub_f32 v189, s74, v189\n"
"v_sub_f32 v194, s74, v194\n"
"v_mfma_f32_16x16x32_bf16 v[156:159], v[208:211], a[16:19], v[156:159]\n"
"v_cvt_pk_bf16_f32 v50, v52, v53\n"
"v_sub_f32 v192, s74, v192\n"
"v_mfma_f32_16x16x32_bf16 v[156:159], v[212:215], a[20:23], v[156:159]\n"
"v_exp_f32 v154, v154\n"
"v_mfma_f32_16x16x32_bf16 v[160:163], v[208:211], a[32:35], v[188:191]\n"
"v_cvt_pk_bf16_f32 v11, v14, v15\n"
"v_sub_f32 v195, s74, v195\n"
"v_mfma_f32_16x16x32_bf16 v[160:163], v[212:215], a[36:39], v[160:163]\n"
"v_exp_f32 v152, v152\n"
"v_mfma_f32_16x16x32_bf16 v[160:163], v[216:219], a[40:43], v[160:163]\n"
"s_nop 0\n"
"v_exp_f32 v158, v158\n"
"v_mfma_f32_16x16x32_bf16 v[160:163], v[220:223], a[44:47], v[160:163]\n"
"v_accvgpr_write_b32 a129, v145\n"
"v_cvt_pk_bf16_f32 v41, v42, v43\n"
"v_mfma_f32_16x16x32_bf16 v[164:167], v[216:219], a[200:203], v[192:195]\n"
"ds_read_b128 v[216:219], v228 offset:5120\n"
"v_cvt_pk_bf16_f32 v25, v26, v27\n"
"v_mfma_f32_16x16x32_bf16 v[164:167], v[220:223], a[204:207], v[164:167]\n"
"ds_read_b128 v[220:223], v229 offset:5120\n"
"v_cvt_pk_bf16_f32 v66, v68, v69\n"
"v_mfma_f32_16x16x32_bf16 v[164:167], v[208:211], a[192:195], v[164:167]\n"
"ds_read_b128 v[208:211], v228 offset:4096\n"
"v_cvt_pk_bf16_f32 v123, v126, v127\n"
"v_mfma_f32_16x16x32_bf16 v[164:167], v[212:215], a[196:199], v[164:167]\n"
"s_waitcnt lgkmcnt(2)\n"
"ds_read_b128 v[212:215], v229 offset:4096\n"
"v_cvt_pk_bf16_f32 v59, v62, v63\n"
"v_mfma_f32_16x16x32_bf16 v[168:171], v[216:219], a[8:11], v[180:183]\n"
"s_waitcnt lgkmcnt(2)\n"
"v_exp_f32 v155, v155\n"
"v_mfma_f32_16x16x32_bf16 v[168:171], v[220:223], a[12:15], v[168:171]\n"
"s_waitcnt lgkmcnt(1)\n"
"v_exp_f32 v157, v157\n"
"v_mfma_f32_16x16x32_bf16 v[168:171], v[208:211], a[0:3], v[168:171]\n"
"s_waitcnt lgkmcnt(0)\n"
"v_permlane16_swap_b32 v57, v59\n"
"v_mfma_f32_16x16x32_bf16 v[168:171], v[212:215], a[4:7], v[168:171]\n"
"ds_write2st64_b64 v255, v[96:97], v[98:99] offset0:228 offset1:229\n"
"v_cvt_pk_bf16_f32 v51, v54, v55\n"
"v_exp_f32 v160, v160\n"
"v_exp_f32 v161, v161\n"
"v_mfma_f32_16x16x32_bf16 v[172:175], v[208:211], a[16:19], v[184:187]\n"
"v_exp_f32 v159, v159\n"
"v_mfma_f32_16x16x32_bf16 v[172:175], v[212:215], a[20:23], v[172:175]\n"
"v_exp_f32 v156, v156\n"
"v_mfma_f32_16x16x32_bf16 v[172:175], v[216:219], a[24:27], v[172:175]\n"
"v_exp_f32 v153, v153\n"
"v_mfma_f32_16x16x32_bf16 v[172:175], v[220:223], a[28:31], v[172:175]\n"
"ds_write2st64_b64 v255, v[104:105], v[106:107] offset0:230 offset1:231\n"
"v_cvt_pk_bf16_f32 v75, v78, v79\n"
"v_accvgpr_write_b32 a240, v254\n"
"v_accvgpr_write_b32 a243, v254\n"
"v_cvt_pk_bf16_f32 v136, v156, v157\n"
"v_cvt_pk_bf16_f32 v144, v160, v161\n"
"v_exp_f32 v162, v162\n"
"v_exp_f32 v164, v164\n"
"v_exp_f32 v167, v167\n"
"v_mfma_f32_16x16x32_bf16 v[176:179], v[216:219], a[40:43], v[188:191]\n"
"v_exp_f32 v169, v169\n"
"v_mfma_f32_16x16x32_bf16 v[176:179], v[220:223], a[44:47], v[176:179]\n"
"v_permlane16_swap_b32 v48, v50\n"
"v_mfma_f32_16x16x32_bf16 v[176:179], v[208:211], a[32:35], v[176:179]\n"
"v_cvt_pk_bf16_f32 v128, v152, v153\n"
"v_cvt_pk_bf16_f32 v129, v154, v155\n"
"v_mfma_f32_16x16x32_bf16 v[176:179], v[212:215], a[36:39], v[176:179]\n"
"v_accvgpr_write_b32 a115, v131\n"
"v_exp_f32 v163, v163\n"
"v_exp_f32 v165, v165\n"
"v_exp_f32 v166, v166\n"
"v_exp_f32 v168, v168\n"
"v_exp_f32 v170, v170\n"
"v_exp_f32 v173, v173\n"
"v_add_u32 v249, s75, v248\n"
"v_mfma_f32_16x16x32_bf16 v[152:155], v[208:211], a[192:195], v[192:195]\n"
"ds_read_b128 v[208:211], v230 offset:0\n"
"v_cvt_pk_bf16_f32 v67, v70, v71\n"
"v_mfma_f32_16x16x32_bf16 v[152:155], v[212:215], a[196:199], v[152:155]\n"
"ds_read_b128 v[212:215], v231 offset:0\n"
"v_cvt_pk_bf16_f32 v145, v162, v163\n"
"v_mfma_f32_16x16x32_bf16 v[152:155], v[216:219], a[200:203], v[152:155]\n"
"ds_read_b128 v[216:219], v230 offset:1024\n"
"v_mov_b32 v196, 0\n"
"v_mfma_f32_16x16x32_bf16 v[152:155], v[220:223], a[204:207], v[152:155]\n"
"s_waitcnt lgkmcnt(2)\n"
"ds_read_b128 v[220:223], v231 offset:1024\n"
"v_cvt_pk_bf16_f32 v137, v158, v159\n"
"v_mfma_f32_16x16x32_bf16 v[156:159], v[208:211], a[0:3], v[180:183]\n"
"s_waitcnt lgkmcnt(2)\n"
"v_permlane16_swap_b32 v49, v51\n"
"v_mfma_f32_16x16x32_bf16 v[156:159], v[212:215], a[4:7], v[156:159]\n"
"s_waitcnt lgkmcnt(1)\n"
"v_exp_f32 v178, v178\n"
"v_mfma_f32_16x16x32_bf16 v[156:159], v[216:219], a[8:11], v[156:159]\n"
"s_waitcnt lgkmcnt(0)\n"
"v_exp_f32 v174, v174\n"
"v_mfma_f32_16x16x32_bf16 v[156:159], v[220:223], a[12:15], v[156:159]\n"
"ds_write2st64_b64 v255, v[72:73], v[74:75] offset0:198 offset1:199\n"
"v_cvt_pk_bf16_f32 v112, v112, v113\n"
"v_permlane16_swap_b32 v56, v58\n"
"v_permlane16_swap_b32 v9, v11\n"
"v_cvt_pk_bf16_f32 v27, v30, v31\n"
"v_cvt_pk_bf16_f32 v125, v166, v167\n"
"v_cvt_pk_bf16_f32 v130, v168, v169\n"
"v_exp_f32 v171, v171\n"
"v_exp_f32 v172, v172\n"
"v_mfma_f32_16x16x32_bf16 v[160:163], v[216:219], a[24:27], v[184:187]\n"
"v_exp_f32 v179, v179\n"
"v_mfma_f32_16x16x32_bf16 v[160:163], v[220:223], a[28:31], v[160:163]\n"
"v_exp_f32 v154, v154\n"
"v_mfma_f32_16x16x32_bf16 v[160:163], v[208:211], a[16:19], v[160:163]\n"
"v_cvt_pk_bf16_f32 v124, v164, v165\n"
"v_cvt_pk_bf16_f32 v138, v172, v173\n"
"v_mfma_f32_16x16x32_bf16 v[160:163], v[212:215], a[20:23], v[160:163]\n"
"ds_write2st64_b64 v255, v[64:65], v[66:67] offset0:196 offset1:197\n"
"v_accvgpr_write_b32 a241, v254\n"
"v_cvt_pk_bf16_f32 v131, v170, v171\n"
"v_exp_f32 v176, v176\n"
"v_exp_f32 v157, v157\n"
"v_mfma_f32_16x16x32_bf16 v[164:167], v[208:211], a[32:35], v[188:191]\n"
"ds_read_b128 v[208:211], v230 offset:4096\n"
"v_cvt_pk_bf16_f32 v43, v46, v47\n"
"v_mfma_f32_16x16x32_bf16 v[164:167], v[212:215], a[36:39], v[164:167]\n"
"ds_read_b128 v[212:215], v231 offset:4096\n"
"v_cvt_pk_bf16_f32 v113, v114, v115\n"
"v_mfma_f32_16x16x32_bf16 v[164:167], v[216:219], a[40:43], v[164:167]\n"
"ds_read_b128 v[216:219], v230 offset:5120\n"
"v_cvt_pk_bf16_f32 v32, v32, v33\n"
"v_mfma_f32_16x16x32_bf16 v[164:167], v[220:223], a[44:47], v[164:167]\n"
"s_waitcnt lgkmcnt(2)\n"
"ds_read_b128 v[220:223], v231 offset:5120\n"
"v_accvgpr_write_b32 a130, v146\n"
"v_permlane16_swap_b32 v41, v43\n"
"v_exp_f32 v175, v175\n"
"v_exp_f32 v177, v177\n"
"v_exp_f32 v162, v162\n"
"v_mfma_f32_16x16x32_bf16 v[168:171], v[208:211], a[0:3], v[180:183]\n"
"s_waitcnt lgkmcnt(2)\n"
"v_cvt_pk_bf16_f32 v10, v12, v13\n"
"v_cvt_pk_bf16_f32 v139, v174, v175\n"
"v_mfma_f32_16x16x32_bf16 v[168:171], v[212:215], a[4:7], v[168:171]\n"
"s_waitcnt lgkmcnt(1)\n"
"v_exp_f32 v158, v158\n"
"v_mfma_f32_16x16x32_bf16 v[168:171], v[216:219], a[8:11], v[168:171]\n"
"s_waitcnt lgkmcnt(0)\n"
"v_cvt_pk_bf16_f32 v26, v28, v29\n"
"v_cvt_pk_bf16_f32 v147, v178, v179\n"
"v_mfma_f32_16x16x32_bf16 v[168:171], v[220:223], a[12:15], v[168:171]\n"
"ds_write_b128 v243, v[48:51] offset:12288\n"
"v_accvgpr_write_b32 a155, v11\n"
"v_accvgpr_write_b32 a187, v43\n"
"v_exp_f32 v153, v153\n"
"v_exp_f32 v156, v156\n"
"v_mfma_f32_16x16x32_bf16 v[172:175], v[216:219], a[24:27], v[184:187]\n"
"v_exp_f32 v155, v155\n"
"v_mfma_f32_16x16x32_bf16 v[172:175], v[220:223], a[28:31], v[172:175]\n"
"v_exp_f32 v160, v160\n"
"v_mfma_f32_16x16x32_bf16 v[172:175], v[208:211], a[16:19], v[172:175]\n"
"v_cvt_pk_bf16_f32 v42, v44, v45\n"
"v_cvt_pk_bf16_f32 v146, v176, v177\n"
"v_mfma_f32_16x16x32_bf16 v[172:175], v[212:215], a[20:23], v[172:175]\n"
"s_cbranch_vccnz LPCE0_%=\nLPKE0_%=:\n"
"ds_write_b128 v243, v[56:59] offset:14336\n"
"v_permlane16_swap_b32 v24, v26\n"
"v_cvt_pk_bf16_f32 v127, v154, v155\n"
"v_exp_f32 v163, v163\n"
"v_exp_f32 v164, v164\n"
"v_exp_f32 v165, v165\n"
"v_exp_f32 v166, v166\n"
"v_exp_f32 v167, v167\n"
"v_mfma_f32_16x16x32_bf16 v[176:179], v[208:211], a[32:35], v[188:191]\n"
"v_exp_f32 v152, v152\n"
"v_mfma_f32_16x16x32_bf16 v[176:179], v[212:215], a[36:39], v[176:179]\n"
"v_cvt_pk_bf16_f32 v0, v0, v1\n"
"v_cvt_pk_bf16_f32 v132, v156, v157\n"
"v_mfma_f32_16x16x32_bf16 v[176:179], v[216:219], a[40:43], v[176:179]\n"
"v_accvgpr_write_b32 a153, v9\n"
"v_accvgpr_write_b32 a170, v26\n"
"v_mfma_f32_16x16x32_bf16 v[176:179], v[220:223], a[44:47], v[176:179]\n"
"v_cvt_pk_bf16_f32 v1, v2, v3\n"
"v_cvt_pk_bf16_f32 v16, v16, v17\n"
"v_cvt_pk_bf16_f32 v126, v152, v153\n"
"v_exp_f32 v159, v159\n"
"v_exp_f32 v168, v168\n"
"v_exp_f32 v169, v169\n"
"v_exp_f32 v170, v170\n"
"v_exp_f32 v173, v173\n"
"s_waitcnt vmcnt(8) lgkmcnt(0)\n"
"s_barrier\n"
"ds_read_b128 v[208:211], v228 offset:16384\n"
"ds_read_b128 v[212:215], v229 offset:16384\n"
"ds_read_b128 v[216:219], v228 offset:17408\n"
"ds_read_b128 v[220:223], v229 offset:17408\n"
"s_waitcnt lgkmcnt(3)\n"
"v_mfma_f32_16x16x32_bf16 v[152:155], v[208:211], a[0:3], v[180:183]\n"
"v_exp_f32 v177, v177\n"
"s_waitcnt lgkmcnt(2)\n"
"v_mfma_f32_16x16x32_bf16 v[152:155], v[212:215], a[4:7], v[152:155]\n"
"ds_read_b128 a[140:143], v249 offset:33792\n"
"v_cvt_pk_bf16_f32 v141, v162, v163\n"
"s_waitcnt lgkmcnt(2)\n"
"v_mfma_f32_16x16x32_bf16 v[152:155], v[216:219], a[8:11], v[152:155]\n"
"s_waitcnt lgkmcnt(1)\n"
"v_exp_f32 v175, v175\n"
"v_mfma_f32_16x16x32_bf16 v[152:155], v[220:223], a[12:15], v[152:155]\n"
"ds_read_b64_tr_b16 a[246:247], v234 offset:4096\n"
"ds_read_b64_tr_b16 a[250:251], v238 offset:4096\n"
"v_permlane16_swap_b32 v8, v10\n"
"v_cvt_pk_bf16_f32 v33, v34, v35\n"
"v_cvt_pk_bf16_f32 v34, v36, v37\n"
"v_cvt_pk_bf16_f32 v133, v158, v159\n"
"v_exp_f32 v161, v161\n"
"v_exp_f32 v176, v176\n"
"v_mfma_f32_16x16x32_bf16 v[156:159], v[216:219], a[24:27], v[184:187]\n"
"ds_read_b128 a[148:151], v249 offset:34816\n"
"v_mov_b32 v203, 0\n"
"v_mfma_f32_16x16x32_bf16 v[156:159], v[220:223], a[28:31], v[156:159]\n"
"ds_read_b64_tr_b16 a[244:245], v234 offset:0\n"
"v_accvgpr_write_b32 a154, v10\n"
"v_mfma_f32_16x16x32_bf16 v[156:159], v[208:211], a[16:19], v[156:159]\n"
"ds_read_b64_tr_b16 a[248:249], v238 offset:0\n"
"ds_read_b128 a[172:175], v249 offset:50176\n"
"v_mfma_f32_16x16x32_bf16 v[156:159], v[212:215], a[20:23], v[156:159]\n"
"ds_read_b64_tr_b16 v[226:227], v239 offset:4096\n"
"v_cvt_pk_bf16_f32 v115, v118, v119\n"
"v_permlane16_swap_b32 v40, v42\n"
"v_cvt_pk_bf16_f32 v140, v160, v161\n"
"v_cvt_pk_bf16_f32 v148, v164, v165\n"
"v_cvt_pk_bf16_f32 v149, v166, v167\n"
"v_exp_f32 v171, v171\n"
"v_exp_f32 v172, v172\n"
"v_exp_f32 v174, v174\n"
"v_exp_f32 v178, v178\n"
"v_exp_f32 v152, v152\n"
"v_mfma_f32_16x16x32_bf16 v[160:163], v[208:211], a[32:35], v[188:191]\n"
"ds_read_b128 a[132:135], v249 offset:32768\n"
"v_cvt_pk_bf16_f32 v114, v116, v117\n"
"v_mfma_f32_16x16x32_bf16 v[160:163], v[212:215], a[36:39], v[160:163]\n"
"ds_read_b128 a[100:103], v248 offset:49152\n"
"v_cvt_pk_bf16_f32 v143, v174, v175\n"
"v_mfma_f32_16x16x32_bf16 v[160:163], v[216:219], a[40:43], v[160:163]\n"
"ds_read_b128 a[116:119], v248 offset:51200\n"
"v_cvt_pk_bf16_f32 v150, v176, v177\n"
"v_mfma_f32_16x16x32_bf16 v[160:163], v[220:223], a[44:47], v[160:163]\n"
"v_mov_b32 v201, 0\n"
"v_cvt_pk_bf16_f32 v134, v168, v169\n"
"v_mfma_f32_16x16x32_bf16 v[164:167], v[216:219], a[200:203], v[192:195]\n"
"ds_read_b64_tr_b16 a[254:255], v235 offset:4096\n"
"v_accvgpr_write_b32 a185, v41\n"
"v_mfma_f32_16x16x32_bf16 v[164:167], v[220:223], a[204:207], v[164:167]\n"
"ds_read_b64_tr_b16 a[252:253], v235 offset:0\n"
"ds_read_b128 a[164:167], v249 offset:49152\n"
"v_mfma_f32_16x16x32_bf16 v[164:167], v[208:211], a[192:195], v[164:167]\n"
"ds_read_b64_tr_b16 v[224:225], v239 offset:0\n"
"v_accvgpr_write_b32 a186, v42\n"
"v_mfma_f32_16x16x32_bf16 v[164:167], v[212:215], a[196:199], v[164:167]\n"
"s_waitcnt lgkmcnt(7)\n"
"ds_read_b128 v[220:223], v229 offset:21504\n"
"v_cvt_pk_bf16_f32 v3, v6, v7\n"
"v_mov_b32 v197, 0\n"
"v_cvt_pk_bf16_f32 v135, v170, v171\n"
"v_exp_f32 v179, v179\n"
"v_exp_f32 v154, v154\n"
"v_exp_f32 v156, v156\n"
"v_exp_f32 v160, v160\n"
"v_mfma_f32_16x16x32_bf16 a[216:219], v[124:127], a[244:247], 0\n"
"ds_read_b128 a[156:159], v249 offset:35840\n"
"v_accvgpr_write_b32 a152, v8\n"
"v_mfma_f32_16x16x32_bf16 v[8:11], v[128:131], a[244:247], 0\n"
"ds_read_b128 a[108:111], v248 offset:50176\n"
"v_accvgpr_write_b32 a184, v40\n"
"v_mfma_f32_16x16x32_bf16 v[8:11], v[132:135], a[248:251], v[8:11]\n"
"ds_read_b128 a[180:183], v249 offset:51200\n"
"v_cvt_pk_bf16_f32 v142, v172, v173\n"
"v_mfma_f32_16x16x32_bf16 v[40:43], v[136:139], a[244:247], 0\n"
"ds_read_b128 a[124:127], v248 offset:52224\n"
"v_bfe_u32 v248, v242, 8, 2\n"
"v_mfma_f32_16x16x32_bf16 v[40:43], v[140:143], a[248:251], v[40:43]\n"
"ds_read_b128 a[188:191], v249 offset:52224\n"
"v_cvt_pk_bf16_f32 v151, v178, v179\n"
"v_mfma_f32_16x16x32_bf16 v[72:75], v[144:147], a[244:247], 0\n"
"v_permlane16_swap_b32 v113, v115\n"
"v_mfma_f32_16x16x32_bf16 v[72:75], v[148:151], a[248:251], v[72:75]\n"
"s_waitcnt lgkmcnt(6)\n"
"ds_read_b128 v[208:211], v228 offset:20480\n"
"ds_read_b64_tr_b16 a[250:251], v238 offset:5120\n"
"v_accvgpr_write_b32 a97, v113\n"
"v_mov_b32 v202, 0\n"
"v_exp_f32 v153, v153\n"
"v_exp_f32 v155, v155\n"
"v_exp_f32 v162, v162\n"
"v_exp_f32 v166, v166\n"
"v_mfma_f32_16x16x32_bf16 v[76:79], v[148:151], v[224:227], 0\n"
"v_cvt_pk_bf16_f32 v17, v18, v19\n"
"v_mov_b32 v198, 0\n"
"v_mfma_f32_16x16x32_bf16 v[76:79], v[144:147], a[252:255], v[76:79]\n"
"ds_read_b128 v[216:219], v228 offset:21504\n"
"v_accvgpr_write_b32 a104, v120\n"
"v_mfma_f32_16x16x32_bf16 v[44:47], v[140:143], v[224:227], 0\n"
"v_cvt_pk_bf16_f32 v2, v4, v5\n"
"v_cvt_pk_bf16_f32 v35, v38, v39\n"
"v_mfma_f32_16x16x32_bf16 v[44:47], v[136:139], a[252:255], v[44:47]\n"
"ds_read_b128 v[212:215], v229 offset:20480\n"
"v_permlane16_swap_b32 v112, v114\n"
"v_permlane16_swap_b32 v1, v3\n"
"v_permlane16_swap_b32 v33, v35\n"
"v_exp_f32 v157, v157\n"
"v_exp_f32 v158, v158\n"
"v_mfma_f32_16x16x32_bf16 v[12:15], v[132:135], v[224:227], 0\n"
"ds_read_b64_tr_b16 a[244:245], v234 offset:1024\n"
"v_cvt_pk_bf16_f32 v96, v152, v153\n"
"v_mfma_f32_16x16x32_bf16 v[12:15], v[128:131], a[252:255], v[12:15]\n"
"v_exp_f32 v159, v159\n"
"v_mfma_f32_16x16x32_bf16 a[220:223], v[124:127], a[252:255], 0\n"
"s_waitcnt lgkmcnt(5)\n"
"v_exp_f32 v165, v165\n"
"v_smfmac_f32_16x16x64_bf16 v[196:199], a[240:243], v[128:135], v241\n"
"s_add_u32 m0, s44, 98304\n"
"ds_read_b64_tr_b16 a[246:247], v234 offset:5120\n"
"ds_read_b64_tr_b16 a[248:249], v238 offset:1024\n"
"v_mfma_f32_16x16x32_bf16 v[202:205], a[240:243], v[124:127], v[202:205]\n"
"s_waitcnt lgkmcnt(4)\n"
"buffer_load_dwordx4 v240, s[20:23], s56 offen nt lds\n"
"v_mfma_f32_16x16x32_bf16 v[168:171], v[216:219], a[8:11], v[180:183]\n"
"v_permlane16_swap_b32 v25, v27\n"
"v_mfma_f32_16x16x32_bf16 v[168:171], v[220:223], a[12:15], v[168:171]\n"
"v_accvgpr_write_b32 a99, v115\n"
"v_cvt_pk_bf16_f32 v97, v154, v155\n"
"v_mfma_f32_16x16x32_bf16 v[168:171], v[208:211], a[0:3], v[168:171]\n"
"s_waitcnt lgkmcnt(3)\n"
"v_exp_f32 v163, v163\n"
"v_mfma_f32_16x16x32_bf16 v[168:171], v[212:215], a[4:7], v[168:171]\n"
"v_cvt_pk_bf16_f32 v18, v20, v21\n"
"v_accvgpr_write_b32 a171, v27\n"
"v_mfma_f32_16x16x32_bf16 v[172:175], v[208:211], a[16:19], v[184:187]\n"
"ds_read_b64_tr_b16 a[254:255], v235 offset:5120\n"
"v_cvt_pk_bf16_f32 v113, v162, v163\n"
"v_mfma_f32_16x16x32_bf16 v[172:175], v[212:215], a[20:23], v[172:175]\n"
"ds_read_b64_tr_b16 v[224:225], v239 offset:1024\n"
"v_cvt_pk_bf16_f32 v19, v22, v23\n"
"v_mfma_f32_16x16x32_bf16 v[172:175], v[216:219], a[24:27], v[172:175]\n"
"s_add_u32 s40, s56, s52\n"
"v_exp_f32 v167, v167\n"
"v_mfma_f32_16x16x32_bf16 v[172:175], v[220:223], a[28:31], v[172:175]\n"
"buffer_load_dwordx4 v240, s[20:23], s40 offen offset:1024 nt lds\n"
"v_mfma_f32_16x16x32_bf16 v[176:179], v[216:219], a[40:43], v[188:191]\n"
"v_accvgpr_write_b32 a145, v1\n"
"v_accvgpr_write_b32 a168, v24\n"
"v_mfma_f32_16x16x32_bf16 v[176:179], v[220:223], a[44:47], v[176:179]\n"
"v_exp_f32 v171, v171\n"
"v_mfma_f32_16x16x32_bf16 v[176:179], v[208:211], a[32:35], v[176:179]\n"
"v_exp_f32 v169, v169\n"
"v_mfma_f32_16x16x32_bf16 v[176:179], v[212:215], a[36:39], v[176:179]\n"
"v_exp_f32 v170, v170\n"
"v_mfma_f32_16x16x32_bf16 v[152:155], v[208:211], a[192:195], v[192:195]\n"
"ds_read_b64_tr_b16 a[252:253], v235 offset:1024\n"
"ds_read_b64_tr_b16 v[226:227], v239 offset:5120\n"
"v_mfma_f32_16x16x32_bf16 v[152:155], v[212:215], a[196:199], v[152:155]\n"
"v_mov_b32 v200, 0\n"
"v_cvt_pk_bf16_f32 v104, v156, v157\n"
"v_mfma_f32_16x16x32_bf16 v[152:155], v[216:219], a[200:203], v[152:155]\n"
"ds_read_b128 v[208:211], v230 offset:16384\n"
"v_cvt_pk_bf16_f32 v105, v158, v159\n"
"v_mfma_f32_16x16x32_bf16 v[152:155], v[220:223], a[204:207], v[152:155]\n"
"s_waitcnt lgkmcnt(3)\n"
"v_permlane16_swap_b32 v0, v2\n"
"v_mfma_f32_16x16x32_bf16 a[232:235], v[124:127], a[244:247], 0\n"
"ds_read_b128 v[212:215], v231 offset:16384\n"
"v_accvgpr_write_b32 a169, v25\n"
"v_mfma_f32_16x16x32_bf16 v[24:27], v[128:131], a[244:247], 0\n"
"s_add_u32 s40, s56, s53\n"
"ds_read_b128 v[220:223], v231 offset:17408\n"
"v_accvgpr_write_b32 a146, v2\n"
"v_mfma_f32_16x16x32_bf16 v[24:27], v[132:135], a[248:251], v[24:27]\n"
"buffer_load_dwordx4 v240, s[20:23], s40 offen offset:2048 nt lds\n"
"v_mfma_f32_16x16x32_bf16 v[56:59], v[136:139], a[244:247], 0\n"
"v_exp_f32 v164, v164\n"
"v_mfma_f32_16x16x32_bf16 v[56:59], v[140:143], a[248:251], v[56:59]\n"
"v_accvgpr_write_b32 a96, v112\n"
"v_accvgpr_write_b32 a179, v35\n"
"v_mfma_f32_16x16x32_bf16 v[88:91], v[144:147], a[244:247], 0\n"
"v_exp_f32 v179, v179\n"
"v_mfma_f32_16x16x32_bf16 v[88:91], v[148:151], a[248:251], v[88:91]\n"
"s_waitcnt lgkmcnt(2)\n"
"v_permlane16_swap_b32 v16, v18\n"
"v_mfma_f32_16x16x32_bf16 v[92:95], v[148:151], v[224:227], 0\n"
"s_add_u32 s40, s56, s54\n"
"ds_read_b128 v[216:219], v230 offset:17408\n"
"v_cvt_pk_bf16_f32 v99, v170, v171\n"
"v_mfma_f32_16x16x32_bf16 v[92:95], v[144:147], a[252:255], v[92:95]\n"
"buffer_load_dwordx4 v240, s[20:23], s40 offen offset:3072 nt lds\n"
"v_mfma_f32_16x16x32_bf16 v[60:63], v[140:143], v[224:227], 0\n"
"s_add_u32 s55, s55, s59\n"
"v_exp_f32 v161, v161\n"
"v_mfma_f32_16x16x32_bf16 v[60:63], v[136:139], a[252:255], v[60:63]\n"
"s_add_u32 s41, s55, s57\n"
"v_permlane16_swap_b32 v121, v123\n"
"v_mfma_f32_16x16x32_bf16 v[28:31], v[132:135], v[224:227], 0\n"
"s_and_b32 s41, s41, s58\n"
"v_permlane16_swap_b32 v32, v34\n"
"v_mfma_f32_16x16x32_bf16 v[28:31], v[128:131], a[252:255], v[28:31]\n"
"s_andn2_b32 s56, s55, s58\n"
"v_exp_f32 v176, v176\n"
"v_mfma_f32_16x16x32_bf16 a[236:239], v[124:127], a[252:255], 0\n"
"s_or_b32 s56, s56, s41\n"
"ds_read_b64_tr_b16 a[250:251], v236 offset:4096\n"
"ds_read_b64_tr_b16 a[252:253], v233 offset:0\n"
"v_smfmac_f32_16x16x64_bf16 v[198:201], a[240:243], v[136:143], v241\n"
"ds_read_b64_tr_b16 a[254:255], v233 offset:4096\n"
"v_accvgpr_write_b32 a176, v32\n"
"v_mfma_f32_16x16x32_bf16 v[156:159], v[208:211], a[0:3], v[180:183]\n"
"s_waitcnt lgkmcnt(3)\n"
"ds_read_b64_tr_b16 a[244:245], v232 offset:0\n"
"ds_read_b64_tr_b16 a[248:249], v236 offset:0\n"
"v_mfma_f32_16x16x32_bf16 v[156:159], v[212:215], a[4:7], v[156:159]\n"
"v_cvt_pk_bf16_f32 v112, v160, v161\n"
"v_cvt_pk_bf16_f32 v120, v164, v165\n"
"v_mfma_f32_16x16x32_bf16 v[156:159], v[216:219], a[8:11], v[156:159]\n"
"s_add_u32 m0, s44, 49152\n"
"ds_read_b64_tr_b16 a[246:247], v232 offset:4096\n"
"ds_read_b64_tr_b16 v[224:225], v237 offset:0\n"
"v_mfma_f32_16x16x32_bf16 v[156:159], v[220:223], a[12:15], v[156:159]\n"
"buffer_load_dwordx4 v240, s[16:19], s56 offen nt lds\n"
"v_mfma_f32_16x16x32_bf16 v[160:163], v[216:219], a[24:27], v[184:187]\n"
"v_accvgpr_write_b32 a105, v121\n"
"v_accvgpr_write_b32 a144, v0\n"
"v_mfma_f32_16x16x32_bf16 v[160:163], v[220:223], a[28:31], v[160:163]\n"
"v_accvgpr_write_b32 a178, v34\n"
"v_cvt_pk_bf16_f32 v121, v166, v167\n"
"v_mfma_f32_16x16x32_bf16 v[160:163], v[208:211], a[16:19], v[160:163]\n"
"ds_read_b64_tr_b16 v[226:227], v237 offset:4096\n"
"v_accvgpr_write_b32 a98, v114\n"
"v_mfma_f32_16x16x32_bf16 v[160:163], v[212:215], a[20:23], v[160:163]\n"
"v_exp_f32 v172, v172\n"
"v_mfma_f32_16x16x32_bf16 v[164:167], v[208:211], a[32:35], v[188:191]\n"
"ds_read_b128 v[208:211], v230 offset:20480\n"
"v_accvgpr_write_b32 a147, v3\n"
"v_mfma_f32_16x16x32_bf16 v[164:167], v[212:215], a[36:39], v[164:167]\n"
"v_exp_f32 v168, v168\n"
"v_mfma_f32_16x16x32_bf16 v[164:167], v[216:219], a[40:43], v[164:167]\n"
"v_exp_f32 v174, v174\n"
"v_mfma_f32_16x16x32_bf16 v[164:167], v[220:223], a[44:47], v[164:167]\n"
"s_waitcnt lgkmcnt(2)\n"
"ds_read_b128 v[216:219], v230 offset:21504\n"
"v_accvgpr_write_b32 a177, v33\n"
"v_mfma_f32_16x16x32_bf16 a[208:211], v[124:127], a[244:247], 0\n"
"v_exp_f32 v177, v177\n"
"v_mfma_f32_16x16x32_bf16 v[0:3], v[128:131], a[244:247], 0\n"
"s_add_u32 s40, s56, s52\n"
"v_exp_f32 v154, v154\n"
"v_mfma_f32_16x16x32_bf16 v[0:3], v[132:135], a[248:251], v[0:3]\n"
"buffer_load_dwordx4 v240, s[16:19], s40 offen offset:1024 nt lds\n"
"v_mfma_f32_16x16x32_bf16 v[32:35], v[136:139], a[244:247], 0\n"
"v_exp_f32 v178, v178\n"
"v_mfma_f32_16x16x32_bf16 v[32:35], v[140:143], a[248:251], v[32:35]\n"
"v_permlane16_swap_b32 v17, v19\n"
"v_mfma_f32_16x16x32_bf16 v[64:67], v[144:147], a[244:247], 0\n"
"ds_read_b128 v[212:215], v231 offset:20480\n"
"v_cvt_pk_bf16_f32 v98, v168, v169\n"
"v_mfma_f32_16x16x32_bf16 v[64:67], v[148:151], a[248:251], v[64:67]\n"
"s_waitcnt lgkmcnt(2)\n"
"v_exp_f32 v162, v162\n"
"v_mfma_f32_16x16x32_bf16 v[68:71], v[148:151], v[224:227], 0\n"
"s_add_u32 s40, s56, s53\n"
"ds_read_b128 v[220:223], v231 offset:21504\n"
"v_bfe_u32 v254, v242, 6, 4\n"
"v_mfma_f32_16x16x32_bf16 v[68:71], v[144:147], a[252:255], v[68:71]\n"
"buffer_load_dwordx4 v240, s[16:19], s40 offen offset:2048 nt lds\n"
"v_mfma_f32_16x16x32_bf16 v[36:39], v[140:143], v[224:227], 0\n"
"v_exp_f32 v156, v156\n"
"v_mfma_f32_16x16x32_bf16 v[36:39], v[136:139], a[252:255], v[36:39]\n"
"v_exp_f32 v163, v163\n"
"v_mfma_f32_16x16x32_bf16 v[4:7], v[132:135], v[224:227], 0\n"
"v_exp_f32 v173, v173\n"
"v_mfma_f32_16x16x32_bf16 v[4:7], v[128:131], a[252:255], v[4:7]\n"
"ds_read_b64_tr_b16 a[250:251], v236 offset:5120\n"
"v_lshlrev_b32 v248, 6, v248\n"
"v_exp_f32 v175, v175\n"
"v_cvt_pk_bf16_f32 v114, v176, v177\n"
"v_cvt_pk_bf16_f32 v115, v178, v179\n"
"v_exp_f32 v152, v152\n"
"v_exp_f32 v155, v155\n"
"v_exp_f32 v157, v157\n"
"v_exp_f32 v161, v161\n"
"v_mfma_f32_16x16x32_bf16 a[212:215], v[124:127], a[252:255], 0\n"
"v_exp_f32 v160, v160\n"
"v_smfmac_f32_16x16x64_bf16 v[200:203], a[240:243], v[144:151], v241\n"
"ds_read_b64_tr_b16 a[246:247], v232 offset:5120\n"
"v_cvt_pk_bf16_f32 v107, v174, v175\n"
"v_mfma_f32_16x16x32_bf16 v[168:171], v[208:211], a[0:3], v[180:183]\n"
"s_waitcnt lgkmcnt(2)\n"
"ds_read_b64_tr_b16 a[244:245], v232 offset:1024\n"
"v_cvt_pk_bf16_f32 v106, v172, v173\n"
"v_mfma_f32_16x16x32_bf16 v[168:171], v[212:215], a[4:7], v[168:171]\n"
"v_accvgpr_write_b32 a163, v19\n"
"v_bfe_u32 v249, v242, 4, 2\n"
"v_mfma_f32_16x16x32_bf16 v[168:171], v[216:219], a[8:11], v[168:171]\n"
"s_add_u32 s40, s56, s54\n"
"ds_read_b64_tr_b16 a[248:249], v236 offset:1024\n"
"v_accvgpr_write_b32 a162, v18\n"
"v_mfma_f32_16x16x32_bf16 v[168:171], v[220:223], a[12:15], v[168:171]\n"
"buffer_load_dwordx4 v240, s[16:19], s40 offen offset:3072 nt lds\n"
"v_mfma_f32_16x16x32_bf16 v[172:175], v[216:219], a[24:27], v[184:187]\n"
"v_exp_f32 v158, v158\n"
"v_mfma_f32_16x16x32_bf16 v[172:175], v[220:223], a[28:31], v[172:175]\n"
"v_exp_f32 v164, v164\n"
"v_mfma_f32_16x16x32_bf16 v[172:175], v[208:211], a[16:19], v[172:175]\n"
"ds_read_b64_tr_b16 a[254:255], v233 offset:5120\n"
"v_accvgpr_write_b32 a161, v17\n"
"v_mfma_f32_16x16x32_bf16 v[172:175], v[212:215], a[20:23], v[172:175]\n"
"s_cbranch_vccnz LPCE1_%=\nLPKE1_%=:\n"
"v_cvt_pk_bf16_f32 v100, v156, v157\n"
"v_mfma_f32_16x16x32_bf16 v[176:179], v[208:211], a[32:35], v[188:191]\n"
"ds_read_b64_tr_b16 a[252:253], v233 offset:1024\n"
"v_accvgpr_write_b32 a160, v16\n"
"v_mfma_f32_16x16x32_bf16 v[176:179], v[212:215], a[36:39], v[176:179]\n"
"ds_read_b64_tr_b16 v[224:225], v237 offset:1024\n"
"ds_read_b64_tr_b16 v[226:227], v237 offset:5120\n"
"v_mfma_f32_16x16x32_bf16 v[176:179], v[216:219], a[40:43], v[176:179]\n"
"v_exp_f32 v159, v159\n"
"v_mfma_f32_16x16x32_bf16 v[176:179], v[220:223], a[44:47], v[176:179]\n"
"s_waitcnt lgkmcnt(4)\n"
"v_accvgpr_write_b32 a107, v123\n"
"v_mul_u32_u24 v249, 20, v249\n"
"v_mfma_f32_16x16x32_bf16 a[224:227], v[124:127], a[244:247], 0\n"
"v_exp_f32 v166, v166\n"
"v_mfma_f32_16x16x32_bf16 v[16:19], v[128:131], a[244:247], 0\n"
"s_waitcnt vmcnt(8)\n"
"v_xor_b32 v249, v249, v254\n"
"v_cvt_pk_bf16_f32 v109, v162, v163\n"
"v_mfma_f32_16x16x32_bf16 v[16:19], v[132:135], a[248:251], v[16:19]\n"
"s_barrier\n"
"ds_read_b128 v[216:219], v228 offset:33792\n"
"ds_read_b128 v[220:223], v229 offset:33792\n"
"v_cvt_pk_bf16_f32 v101, v158, v159\n"
"v_exp_f32 v167, v167\n"
"v_exp_f32 v169, v169\n"
"v_exp_f32 v178, v178\n"
"v_mfma_f32_16x16x32_bf16 v[48:51], v[136:139], a[244:247], 0\n"
"v_exp_f32 v165, v165\n"
"v_mfma_f32_16x16x32_bf16 v[48:51], v[140:143], a[248:251], v[48:51]\n"
"ds_read_b128 v[208:211], v228 offset:32768\n"
"ds_read_b128 v[212:215], v229 offset:32768\n"
"v_mfma_f32_16x16x32_bf16 v[80:83], v[144:147], a[244:247], 0\n"
"ds_read_b64_tr_b16 a[244:245], v234 offset:16384\n"
"ds_read_b64_tr_b16 a[246:247], v234 offset:20480\n"
"v_mfma_f32_16x16x32_bf16 v[80:83], v[148:151], a[248:251], v[80:83]\n"
"s_waitcnt lgkmcnt(6)\n"
"ds_read_b64_tr_b16 a[248:249], v238 offset:16384\n"
"ds_read_b64_tr_b16 a[250:251], v238 offset:20480\n"
"v_mfma_f32_16x16x32_bf16 v[84:87], v[148:151], v[224:227], 0\n"
"v_exp_f32 v173, v173\n"
"v_mfma_f32_16x16x32_bf16 v[84:87], v[144:147], a[252:255], v[84:87]\n"
"v_exp_f32 v175, v175\n"
"v_mfma_f32_16x16x32_bf16 v[52:55], v[140:143], v[224:227], 0\n"
"v_xor_b32 v248, v248, v242\n"
"v_lshl_add_u32 v249, v249, 4, s44\n"
"v_mfma_f32_16x16x32_bf16 v[52:55], v[136:139], a[252:255], v[52:55]\n"
"v_exp_f32 v153, v153\n"
"v_mfma_f32_16x16x32_bf16 v[20:23], v[132:135], v[224:227], 0\n"
"ds_read_b64_tr_b16 v[226:227], v239 offset:20480\n"
"v_cvt_pk_bf16_f32 v117, v166, v167\n"
"v_mfma_f32_16x16x32_bf16 v[20:23], v[128:131], a[252:255], v[20:23]\n"
"v_accvgpr_write_b32 a106, v122\n"
"v_cvt_pk_bf16_f32 v122, v152, v153\n"
"v_mfma_f32_16x16x32_bf16 a[228:231], v[124:127], a[252:255], 0\n"
"s_waitcnt lgkmcnt(3)\n"
"v_cvt_pk_bf16_f32 v123, v154, v155\n"
"s_branch LAY2_%=\n"
"LAL_%=:\n"
"v_mfma_f32_16x16x32_bf16 a[224:227], v[124:127], a[244:247], a[224:227]\n"
"v_exp_f32 v169, v169\n"
"v_mfma_f32_16x16x32_bf16 v[16:19], v[128:131], a[244:247], v[16:19]\n"
"s_waitcnt vmcnt(8)\n"
"v_exp_f32 v175, v175\n"
"v_mfma_f32_16x16x32_bf16 v[16:19], v[132:135], a[248:251], v[16:19]\n"
"s_barrier\n"
"ds_read_b128 v[220:223], v229 offset:1024\n"
"v_mfma_f32_16x16x32_bf16 v[48:51], v[136:139], a[244:247], v[48:51]\n"
"ds_read_b128 v[216:219], v228 offset:1024\n"
"v_cvt_pk_bf16_f32 v109, v162, v163\n"
"v_mfma_f32_16x16x32_bf16 v[48:51], v[140:143], a[248:251], v[48:51]\n"
"ds_read_b128 v[212:215], v229 offset:0\n"
"v_mfma_f32_16x16x32_bf16 v[80:83], v[144:147], a[244:247], v[80:83]\n"
"ds_read_b128 v[208:211], v228 offset:0\n"
"v_cvt_pk_bf16_f32 v101, v158, v159\n"
"v_mfma_f32_16x16x32_bf16 v[80:83], v[148:151], a[248:251], v[80:83]\n"
"s_waitcnt lgkmcnt(4)\n"
"v_exp_f32 v178, v178\n"
"v_mfma_f32_16x16x32_bf16 v[84:87], v[148:151], v[224:227], v[84:87]\n"
"ds_read_b64_tr_b16 a[244:245], v234 offset:49152\n"
"v_cvt_pk_bf16_f32 v117, v166, v167\n"
"v_mfma_f32_16x16x32_bf16 v[84:87], v[144:147], a[252:255], v[84:87]\n"
"ds_read_b64_tr_b16 a[250:251], v238 offset:53248\n"
"v_cvt_pk_bf16_f32 v123, v154, v155\n"
"v_mfma_f32_16x16x32_bf16 v[52:55], v[140:143], v[224:227], v[52:55]\n"
"v_exp_f32 v173, v173\n"
"v_mfma_f32_16x16x32_bf16 v[52:55], v[136:139], a[252:255], v[52:55]\n"
"ds_read_b64_tr_b16 a[248:249], v238 offset:49152\n"
"v_mfma_f32_16x16x32_bf16 v[20:23], v[132:135], v[224:227], v[20:23]\n"
"v_exp_f32 v156, v156\n"
"v_mfma_f32_16x16x32_bf16 v[20:23], v[128:131], a[252:255], v[20:23]\n"
"ds_read_b64_tr_b16 v[226:227], v239 offset:53248\n"
"v_cvt_pk_bf16_f32 v100, v156, v157\n"
"v_mfma_f32_16x16x32_bf16 a[228:231], v[124:127], a[252:255], a[228:231]\n"
"s_waitcnt lgkmcnt(4)\n"
"ds_read_b64_tr_b16 a[246:247], v234 offset:53248\n"
"v_mfma_f32_16x16x32_bf16 v[152:155], v[208:211], a[0:3], v[180:183]\n"
"ds_read_b64_tr_b16 v[224:225], v239 offset:49152\n"
"v_cvt_pk_bf16_f32 v116, v164, v165\n"
"v_mfma_f32_16x16x32_bf16 v[152:155], v[212:215], a[4:7], v[152:155]\n"
"ds_read_b64_tr_b16 a[252:253], v235 offset:49152\n"
"v_cvt_pk_bf16_f32 v108, v160, v161\n"
"v_mfma_f32_16x16x32_bf16 v[152:155], v[216:219], a[8:11], v[152:155]\n"
"s_add_u32 m0, s44, 81920\n"
"ds_read_b64_tr_b16 a[254:255], v235 offset:53248\n"
"v_mfma_f32_16x16x32_bf16 v[152:155], v[220:223], a[12:15], v[152:155]\n"
"buffer_load_dwordx4 v240, s[20:23], s56 offen nt lds\n"
"v_mfma_f32_16x16x32_bf16 v[156:159], v[216:219], a[24:27], v[184:187]\n"
"v_exp_f32 v177, v177\n"
"v_mfma_f32_16x16x32_bf16 v[156:159], v[220:223], a[28:31], v[156:159]\n"
"v_exp_f32 v168, v168\n"
"v_mfma_f32_16x16x32_bf16 v[156:159], v[208:211], a[16:19], v[156:159]\n"
"v_exp_f32 v179, v179\n"
"v_mfma_f32_16x16x32_bf16 v[156:159], v[212:215], a[20:23], v[156:159]\n"
"v_exp_f32 v174, v174\n"
"v_mfma_f32_16x16x32_bf16 v[160:163], v[208:211], a[32:35], v[188:191]\n"
"v_exp_f32 v172, v172\n"
"v_mfma_f32_16x16x32_bf16 v[160:163], v[212:215], a[36:39], v[160:163]\n"
"v_exp_f32 v171, v171\n"
"v_mfma_f32_16x16x32_bf16 v[160:163], v[216:219], a[40:43], v[160:163]\n"
"v_exp_f32 v154, v154\n"
"v_mfma_f32_16x16x32_bf16 v[160:163], v[220:223], a[44:47], v[160:163]\n"
"v_exp_f32 v170, v170\n"
"v_mfma_f32_16x16x32_bf16 v[164:167], v[216:219], a[200:203], v[192:195]\n"
"ds_read_b128 v[216:219], v228 offset:5120\n"
"v_cvt_pk_bf16_f32 v103, v170, v171\n"
"v_mfma_f32_16x16x32_bf16 v[164:167], v[220:223], a[204:207], v[164:167]\n"
"v_cvt_pk_bf16_f32 v102, v168, v169\n"
"v_cvt_pk_bf16_f32 v119, v178, v179\n"
"v_mfma_f32_16x16x32_bf16 v[164:167], v[208:211], a[192:195], v[164:167]\n"
"s_add_u32 s40, s56, s52\n"
"ds_read_b128 v[208:211], v228 offset:4096\n"
"v_mfma_f32_16x16x32_bf16 v[164:167], v[212:215], a[196:199], v[164:167]\n"
"s_waitcnt lgkmcnt(2)\n"
"buffer_load_dwordx4 v240, s[20:23], s40 offen offset:1024 nt lds\n"
"v_mfma_f32_16x16x32_bf16 a[216:219], v[120:123], a[244:247], a[216:219]\n"
"v_cvt_pk_bf16_f32 v110, v172, v173\n"
"v_cvt_pk_bf16_f32 v111, v174, v175\n"
"v_mfma_f32_16x16x32_bf16 v[8:11], v[96:99], a[244:247], v[8:11]\n"
"v_exp_f32 v176, v176\n"
"v_mfma_f32_16x16x32_bf16 v[8:11], v[100:103], a[248:251], v[8:11]\n"
"v_exp_f32 v155, v155\n"
"v_mfma_f32_16x16x32_bf16 v[40:43], v[104:107], a[244:247], v[40:43]\n"
"v_exp_f32 v152, v152\n"
"v_mfma_f32_16x16x32_bf16 v[40:43], v[108:111], a[248:251], v[40:43]\n"
"ds_read_b128 v[212:215], v229 offset:4096\n"
"v_cvt_pk_bf16_f32 v118, v176, v177\n"
"v_mfma_f32_16x16x32_bf16 v[72:75], v[112:115], a[244:247], v[72:75]\n"
"v_exp_f32 v158, v158\n"
"v_mfma_f32_16x16x32_bf16 v[72:75], v[116:119], a[248:251], v[72:75]\n"
"v_exp_f32 v163, v163\n"
"v_mfma_f32_16x16x32_bf16 v[76:79], v[116:119], v[224:227], v[76:79]\n"
"ds_read_b128 v[220:223], v229 offset:5120\n"
"v_mfma_f32_16x16x32_bf16 v[76:79], v[112:115], a[252:255], v[76:79]\n"
"v_exp_f32 v159, v159\n"
"v_mfma_f32_16x16x32_bf16 v[44:47], v[108:111], v[224:227], v[44:47]\n"
"s_add_u32 s40, s56, s53\n"
"ds_read_b64_tr_b16 a[244:245], v234 offset:50176\n"
"v_mfma_f32_16x16x32_bf16 v[44:47], v[104:107], a[252:255], v[44:47]\n"
"buffer_load_dwordx4 v240, s[20:23], s40 offen offset:2048 nt lds\n"
"v_mfma_f32_16x16x32_bf16 v[12:15], v[100:103], v[224:227], v[12:15]\n"
"v_exp_f32 v153, v153\n"
"v_mfma_f32_16x16x32_bf16 v[12:15], v[96:99], a[252:255], v[12:15]\n"
"v_exp_f32 v165, v165\n"
"v_mfma_f32_16x16x32_bf16 a[220:223], v[120:123], a[252:255], a[220:223]\n"
"v_exp_f32 v164, v164\n"
"v_smfmac_f32_16x16x64_bf16 v[196:199], a[240:243], v[96:103], v241\n"
"v_cvt_pk_bf16_f32 v129, v154, v155\n"
"v_mfma_f32_16x16x32_bf16 v[202:205], a[240:243], v[120:123], v[202:205]\n"
"s_waitcnt lgkmcnt(1)\n"
"ds_read_b64_tr_b16 a[254:255], v235 offset:54272\n"
"v_mfma_f32_16x16x32_bf16 v[168:171], v[216:219], a[8:11], v[180:183]\n"
"ds_read_b64_tr_b16 a[248:249], v238 offset:50176\n"
"v_mfma_f32_16x16x32_bf16 v[168:171], v[220:223], a[12:15], v[168:171]\n"
"ds_read_b64_tr_b16 a[246:247], v234 offset:54272\n"
"v_mfma_f32_16x16x32_bf16 v[168:171], v[208:211], a[0:3], v[168:171]\n"
"ds_read_b64_tr_b16 v[224:225], v239 offset:50176\n"
"v_cvt_pk_bf16_f32 v124, v164, v165\n"
"v_mfma_f32_16x16x32_bf16 v[168:171], v[212:215], a[4:7], v[168:171]\n"
"v_exp_f32 v166, v166\n"
"v_mfma_f32_16x16x32_bf16 v[172:175], v[208:211], a[16:19], v[184:187]\n"
"ds_read_b64_tr_b16 a[252:253], v235 offset:50176\n"
"v_cvt_pk_bf16_f32 v128, v152, v153\n"
"v_mfma_f32_16x16x32_bf16 v[172:175], v[212:215], a[20:23], v[172:175]\n"
"ds_read_b64_tr_b16 v[226:227], v239 offset:54272\n"
"v_cvt_pk_bf16_f32 v137, v158, v159\n"
"v_mfma_f32_16x16x32_bf16 v[172:175], v[216:219], a[24:27], v[172:175]\n"
"s_add_u32 s40, s56, s54\n"
"ds_read_b64_tr_b16 a[250:251], v238 offset:54272\n"
"v_mfma_f32_16x16x32_bf16 v[172:175], v[220:223], a[28:31], v[172:175]\n"
"buffer_load_dwordx4 v240, s[20:23], s40 offen offset:3072 nt lds\n"
"v_mfma_f32_16x16x32_bf16 v[176:179], v[216:219], a[40:43], v[188:191]\n"
"s_add_u32 s55, s55, s59\n"
"v_exp_f32 v167, v167\n"
"v_mfma_f32_16x16x32_bf16 v[176:179], v[220:223], a[44:47], v[176:179]\n"
"s_add_u32 s41, s55, s57\n"
"v_exp_f32 v161, v161\n"
"v_mfma_f32_16x16x32_bf16 v[176:179], v[208:211], a[32:35], v[176:179]\n"
"s_and_b32 s41, s41, s58\n"
"v_exp_f32 v157, v157\n"
"v_mfma_f32_16x16x32_bf16 v[176:179], v[212:215], a[36:39], v[176:179]\n"
"s_andn2_b32 s56, s55, s58\n"
"v_exp_f32 v160, v160\n"
"v_mfma_f32_16x16x32_bf16 v[152:155], v[208:211], a[192:195], v[192:195]\n"
"s_or_b32 s56, s56, s41\n"
"ds_read_b128 v[208:211], v230 offset:0\n"
"v_mfma_f32_16x16x32_bf16 v[152:155], v[212:215], a[196:199], v[152:155]\n"
"v_exp_f32 v172, v172\n"
"v_mfma_f32_16x16x32_bf16 v[152:155], v[216:219], a[200:203], v[152:155]\n"
"ds_read_b128 v[216:219], v230 offset:1024\n"
"v_mfma_f32_16x16x32_bf16 v[152:155], v[220:223], a[204:207], v[152:155]\n"
"s_waitcnt lgkmcnt(2)\n"
"v_exp_f32 v162, v162\n"
"v_mfma_f32_16x16x32_bf16 a[232:235], v[120:123], a[244:247], a[232:235]\n"
"ds_read_b128 v[212:215], v231 offset:0\n"
"v_mfma_f32_16x16x32_bf16 v[24:27], v[96:99], a[244:247], v[24:27]\n"
"s_add_u32 m0, s44, 32768\n"
"ds_read_b128 v[220:223], v231 offset:1024\n"
"v_mfma_f32_16x16x32_bf16 v[24:27], v[100:103], a[248:251], v[24:27]\n"
"buffer_load_dwordx4 v240, s[16:19], s56 offen nt lds\n"
"v_mfma_f32_16x16x32_bf16 v[56:59], v[104:107], a[244:247], v[56:59]\n"
"v_exp_f32 v171, v171\n"
"v_mfma_f32_16x16x32_bf16 v[56:59], v[108:111], a[248:251], v[56:59]\n"
"v_exp_f32 v169, v169\n"
"v_mfma_f32_16x16x32_bf16 v[88:91], v[112:115], a[244:247], v[88:91]\n"
"v_exp_f32 v154, v154\n"
"v_mfma_f32_16x16x32_bf16 v[88:91], v[116:119], a[248:251], v[88:91]\n"
"v_exp_f32 v156, v156\n"
"v_mfma_f32_16x16x32_bf16 v[92:95], v[116:119], v[224:227], v[92:95]\n"
"ds_read_b64_tr_b16 a[244:245], v232 offset:49152\n"
"v_cvt_pk_bf16_f32 v145, v162, v163\n"
"v_mfma_f32_16x16x32_bf16 v[92:95], v[112:115], a[252:255], v[92:95]\n"
"ds_read_b64_tr_b16 a[250:251], v236 offset:53248\n"
"v_cvt_pk_bf16_f32 v136, v156, v157\n"
"v_mfma_f32_16x16x32_bf16 v[60:63], v[108:111], v[224:227], v[60:63]\n"
"ds_read_b64_tr_b16 a[246:247], v232 offset:53248\n"
"v_cvt_pk_bf16_f32 v144, v160, v161\n"
"v_mfma_f32_16x16x32_bf16 v[60:63], v[104:107], a[252:255], v[60:63]\n"
"v_exp_f32 v152, v152\n"
"v_mfma_f32_16x16x32_bf16 v[28:31], v[100:103], v[224:227], v[28:31]\n"
"ds_read_b64_tr_b16 v[224:225], v237 offset:49152\n"
"ds_read_b64_tr_b16 v[226:227], v237 offset:53248\n"
"v_mfma_f32_16x16x32_bf16 v[28:31], v[96:99], a[252:255], v[28:31]\n"
"ds_read_b64_tr_b16 a[248:249], v236 offset:49152\n"
"v_cvt_pk_bf16_f32 v125, v166, v167\n"
"v_mfma_f32_16x16x32_bf16 a[236:239], v[120:123], a[252:255], a[236:239]\n"
"s_add_u32 s40, s56, s52\n"
"ds_read_b64_tr_b16 a[252:253], v233 offset:49152\n"
"v_smfmac_f32_16x16x64_bf16 v[198:201], a[240:243], v[104:111], v241\n"
"s_waitcnt lgkmcnt(7)\n"
"buffer_load_dwordx4 v240, s[16:19], s40 offen offset:1024 nt lds\n"
"v_mfma_f32_16x16x32_bf16 v[156:159], v[208:211], a[0:3], v[180:183]\n"
"v_exp_f32 v175, v175\n"
"v_mfma_f32_16x16x32_bf16 v[156:159], v[212:215], a[4:7], v[156:159]\n"
"v_exp_f32 v173, v173\n"
"v_mfma_f32_16x16x32_bf16 v[156:159], v[216:219], a[8:11], v[156:159]\n"
"v_exp_f32 v168, v168\n"
"v_mfma_f32_16x16x32_bf16 v[156:159], v[220:223], a[12:15], v[156:159]\n"
"ds_read_b64_tr_b16 a[254:255], v233 offset:53248\n"
"v_mfma_f32_16x16x32_bf16 v[160:163], v[216:219], a[24:27], v[184:187]\n"
"v_exp_f32 v170, v170\n"
"v_mfma_f32_16x16x32_bf16 v[160:163], v[220:223], a[28:31], v[160:163]\n"
"v_exp_f32 v178, v178\n"
"v_mfma_f32_16x16x32_bf16 v[160:163], v[208:211], a[16:19], v[160:163]\n"
"v_exp_f32 v177, v177\n"
"v_mfma_f32_16x16x32_bf16 v[160:163], v[212:215], a[20:23], v[160:163]\n"
"v_exp_f32 v176, v176\n"
"v_mfma_f32_16x16x32_bf16 v[164:167], v[208:211], a[32:35], v[188:191]\n"
"ds_read_b128 v[208:211], v230 offset:4096\n"
"v_cvt_pk_bf16_f32 v131, v170, v171\n"
"v_mfma_f32_16x16x32_bf16 v[164:167], v[212:215], a[36:39], v[164:167]\n"
"ds_read_b128 v[212:215], v231 offset:4096\n"
"v_mfma_f32_16x16x32_bf16 v[164:167], v[216:219], a[40:43], v[164:167]\n"
"v_exp_f32 v153, v153\n"
"v_mfma_f32_16x16x32_bf16 v[164:167], v[220:223], a[44:47], v[164:167]\n"
"s_waitcnt lgkmcnt(2)\n"
"ds_read_b128 v[220:223], v231 offset:5120\n"
"v_cvt_pk_bf16_f32 v138, v172, v173\n"
"v_mfma_f32_16x16x32_bf16 a[208:211], v[120:123], a[244:247], a[208:211]\n"
"v_exp_f32 v174, v174\n"
"v_mfma_f32_16x16x32_bf16 v[0:3], v[96:99], a[244:247], v[0:3]\n"
"s_add_u32 s40, s56, s53\n"
"ds_read_b128 v[216:219], v230 offset:5120\n"
"v_mfma_f32_16x16x32_bf16 v[0:3], v[100:103], a[248:251], v[0:3]\n"
"buffer_load_dwordx4 v240, s[16:19], s40 offen offset:2048 nt lds\n"
"v_mfma_f32_16x16x32_bf16 v[32:35], v[104:107], a[244:247], v[32:35]\n"
"v_exp_f32 v163, v163\n"
"v_mfma_f32_16x16x32_bf16 v[32:35], v[108:111], a[248:251], v[32:35]\n"
"v_exp_f32 v161, v161\n"
"v_mfma_f32_16x16x32_bf16 v[64:67], v[112:115], a[244:247], v[64:67]\n"
"v_exp_f32 v155, v155\n"
"v_mfma_f32_16x16x32_bf16 v[64:67], v[116:119], a[248:251], v[64:67]\n"
"v_exp_f32 v166, v166\n"
"v_mfma_f32_16x16x32_bf16 v[68:71], v[116:119], v[224:227], v[68:71]\n"
"ds_read_b64_tr_b16 a[250:251], v236 offset:54272\n"
"v_cvt_pk_bf16_f32 v139, v174, v175\n"
"v_mfma_f32_16x16x32_bf16 v[68:71], v[112:115], a[252:255], v[68:71]\n"
"ds_read_b64_tr_b16 a[246:247], v232 offset:54272\n"
"v_cvt_pk_bf16_f32 v130, v168, v169\n"
"v_mfma_f32_16x16x32_bf16 v[36:39], v[108:111], v[224:227], v[36:39]\n"
"v_exp_f32 v179, v179\n"
"v_mfma_f32_16x16x32_bf16 v[36:39], v[104:107], a[252:255], v[36:39]\n"
"ds_read_b64_tr_b16 a[244:245], v232 offset:50176\n"
"v_cvt_pk_bf16_f32 v126, v152, v153\n"
"v_mfma_f32_16x16x32_bf16 v[4:7], v[100:103], v[224:227], v[4:7]\n"
"s_add_u32 s40, s56, s54\n"
"ds_read_b64_tr_b16 a[248:249], v236 offset:50176\n"
"v_mfma_f32_16x16x32_bf16 v[4:7], v[96:99], a[252:255], v[4:7]\n"
"buffer_load_dwordx4 v240, s[16:19], s40 offen offset:3072 nt lds\n"
"v_mfma_f32_16x16x32_bf16 a[212:215], v[120:123], a[252:255], a[212:215]\n"
"v_exp_f32 v162, v162\n"
"v_smfmac_f32_16x16x64_bf16 v[200:203], a[240:243], v[112:119], v241\n"
"s_waitcnt lgkmcnt(4)\n"
"v_cvt_pk_bf16_f32 v146, v176, v177\n"
"v_mfma_f32_16x16x32_bf16 v[168:171], v[208:211], a[0:3], v[180:183]\n"
"v_exp_f32 v157, v157\n"
"v_mfma_f32_16x16x32_bf16 v[168:171], v[212:215], a[4:7], v[168:171]\n"
"v_exp_f32 v165, v165\n"
"v_mfma_f32_16x16x32_bf16 v[168:171], v[216:219], a[8:11], v[168:171]\n"
"ds_read_b64_tr_b16 a[254:255], v233 offset:54272\n"
"ds_read_b64_tr_b16 v[226:227], v237 offset:54272\n"
"v_mfma_f32_16x16x32_bf16 v[168:171], v[220:223], a[12:15], v[168:171]\n"
"ds_read_b64_tr_b16 a[252:253], v233 offset:50176\n"
"v_cvt_pk_bf16_f32 v147, v178, v179\n"
"v_mfma_f32_16x16x32_bf16 v[172:175], v[216:219], a[24:27], v[184:187]\n"
"ds_read_b64_tr_b16 v[224:225], v237 offset:50176\n"
"v_mfma_f32_16x16x32_bf16 v[172:175], v[220:223], a[28:31], v[172:175]\n"
"v_exp_f32 v160, v160\n"
"v_mfma_f32_16x16x32_bf16 v[172:175], v[208:211], a[16:19], v[172:175]\n"
"v_exp_f32 v158, v158\n"
"v_mfma_f32_16x16x32_bf16 v[172:175], v[212:215], a[20:23], v[172:175]\n"
"s_cbranch_vccnz LPCL0_%=\nLPKL0_%=:\n"
"v_mfma_f32_16x16x32_bf16 v[176:179], v[208:211], a[32:35], v[188:191]\n"
"v_exp_f32 v159, v159\n"
"v_mfma_f32_16x16x32_bf16 v[176:179], v[212:215], a[36:39], v[176:179]\n"
"v_exp_f32 v167, v167\n"
"v_mfma_f32_16x16x32_bf16 v[176:179], v[216:219], a[40:43], v[176:179]\n"
"v_exp_f32 v164, v164\n"
"v_mfma_f32_16x16x32_bf16 v[176:179], v[220:223], a[44:47], v[176:179]\n"
"s_waitcnt lgkmcnt(1)\n"
"v_mfma_f32_16x16x32_bf16 a[224:227], v[120:123], a[244:247], a[224:227]\n"
"v_exp_f32 v169, v169\n"
"v_mfma_f32_16x16x32_bf16 v[16:19], v[96:99], a[244:247], v[16:19]\n"
"s_waitcnt vmcnt(8)\n"
"v_exp_f32 v175, v175\n"
"v_mfma_f32_16x16x32_bf16 v[16:19], v[100:103], a[248:251], v[16:19]\n"
"s_barrier\n"
"ds_read_b128 v[220:223], v229 offset:17408\n"
"v_mfma_f32_16x16x32_bf16 v[48:51], v[104:107], a[244:247], v[48:51]\n"
"ds_read_b128 v[216:219], v228 offset:17408\n"
"v_cvt_pk_bf16_f32 v141, v162, v163\n"
"v_mfma_f32_16x16x32_bf16 v[48:51], v[108:111], a[248:251], v[48:51]\n"
"ds_read_b128 v[212:215], v229 offset:16384\n"
"v_mfma_f32_16x16x32_bf16 v[80:83], v[112:115], a[244:247], v[80:83]\n"
"ds_read_b128 v[208:211], v228 offset:16384\n"
"v_cvt_pk_bf16_f32 v133, v158, v159\n"
"v_mfma_f32_16x16x32_bf16 v[80:83], v[116:119], a[248:251], v[80:83]\n"
"s_waitcnt lgkmcnt(4)\n"
"v_exp_f32 v178, v178\n"
"v_mfma_f32_16x16x32_bf16 v[84:87], v[116:119], v[224:227], v[84:87]\n"
"ds_read_b64_tr_b16 a[244:245], v234 offset:0\n"
"v_cvt_pk_bf16_f32 v149, v166, v167\n"
"v_mfma_f32_16x16x32_bf16 v[84:87], v[112:115], a[252:255], v[84:87]\n"
"ds_read_b64_tr_b16 a[250:251], v238 offset:4096\n"
"v_cvt_pk_bf16_f32 v127, v154, v155\n"
"v_mfma_f32_16x16x32_bf16 v[52:55], v[108:111], v[224:227], v[52:55]\n"
"v_exp_f32 v173, v173\n"
"v_mfma_f32_16x16x32_bf16 v[52:55], v[104:107], a[252:255], v[52:55]\n"
"ds_read_b64_tr_b16 a[248:249], v238 offset:0\n"
"v_mfma_f32_16x16x32_bf16 v[20:23], v[100:103], v[224:227], v[20:23]\n"
"v_exp_f32 v156, v156\n"
"v_mfma_f32_16x16x32_bf16 v[20:23], v[96:99], a[252:255], v[20:23]\n"
"ds_read_b64_tr_b16 v[226:227], v239 offset:4096\n"
"v_cvt_pk_bf16_f32 v132, v156, v157\n"
"v_mfma_f32_16x16x32_bf16 a[228:231], v[120:123], a[252:255], a[228:231]\n"
"s_waitcnt lgkmcnt(4)\n"
"ds_read_b64_tr_b16 a[246:247], v234 offset:4096\n"
"v_mfma_f32_16x16x32_bf16 v[152:155], v[208:211], a[0:3], v[180:183]\n"
"ds_read_b64_tr_b16 v[224:225], v239 offset:0\n"
"v_cvt_pk_bf16_f32 v148, v164, v165\n"
"v_mfma_f32_16x16x32_bf16 v[152:155], v[212:215], a[4:7], v[152:155]\n"
"ds_read_b64_tr_b16 a[252:253], v235 offset:0\n"
"v_cvt_pk_bf16_f32 v140, v160, v161\n"
"v_mfma_f32_16x16x32_bf16 v[152:155], v[216:219], a[8:11], v[152:155]\n"
"s_add_u32 m0, s44, 98304\n"
"ds_read_b64_tr_b16 a[254:255], v235 offset:4096\n"
"v_mfma_f32_16x16x32_bf16 v[152:155], v[220:223], a[12:15], v[152:155]\n"
"buffer_load_dwordx4 v240, s[20:23], s56 offen nt lds\n"
"v_mfma_f32_16x16x32_bf16 v[156:159], v[216:219], a[24:27], v[184:187]\n"
"v_exp_f32 v177, v177\n"
"v_mfma_f32_16x16x32_bf16 v[156:159], v[220:223], a[28:31], v[156:159]\n"
"v_exp_f32 v168, v168\n"
"v_mfma_f32_16x16x32_bf16 v[156:159], v[208:211], a[16:19], v[156:159]\n"
"v_exp_f32 v179, v179\n"
"v_mfma_f32_16x16x32_bf16 v[156:159], v[212:215], a[20:23], v[156:159]\n"
"v_exp_f32 v174, v174\n"
"v_mfma_f32_16x16x32_bf16 v[160:163], v[208:211], a[32:35], v[188:191]\n"
"v_exp_f32 v172, v172\n"
"v_mfma_f32_16x16x32_bf16 v[160:163], v[212:215], a[36:39], v[160:163]\n"
"v_exp_f32 v171, v171\n"
"v_mfma_f32_16x16x32_bf16 v[160:163], v[216:219], a[40:43], v[160:163]\n"
"v_exp_f32 v154, v154\n"
"v_mfma_f32_16x16x32_bf16 v[160:163], v[220:223], a[44:47], v[160:163]\n"
"v_exp_f32 v170, v170\n"
"v_mfma_f32_16x16x32_bf16 v[164:167], v[216:219], a[200:203], v[192:195]\n"
"ds_read_b128 v[216:219], v228 offset:21504\n"
"v_cvt_pk_bf16_f32 v135, v170, v171\n"
"v_mfma_f32_16x16x32_bf16 v[164:167], v[220:223], a[204:207], v[164:167]\n"
"v_cvt_pk_bf16_f32 v134, v168, v169\n"
"v_cvt_pk_bf16_f32 v151, v178, v179\n"
"v_mfma_f32_16x16x32_bf16 v[164:167], v[208:211], a[192:195], v[164:167]\n"
"s_add_u32 s40, s56, s52\n"
"ds_read_b128 v[208:211], v228 offset:20480\n"
"v_mfma_f32_16x16x32_bf16 v[164:167], v[212:215], a[196:199], v[164:167]\n"
"s_waitcnt lgkmcnt(2)\n"
"buffer_load_dwordx4 v240, s[20:23], s40 offen offset:1024 nt lds\n"
"v_mfma_f32_16x16x32_bf16 a[216:219], v[124:127], a[244:247], a[216:219]\n"
"v_cvt_pk_bf16_f32 v142, v172, v173\n"
"v_cvt_pk_bf16_f32 v143, v174, v175\n"
"v_mfma_f32_16x16x32_bf16 v[8:11], v[128:131], a[244:247], v[8:11]\n"
"v_exp_f32 v176, v176\n"
"v_mfma_f32_16x16x32_bf16 v[8:11], v[132:135], a[248:251], v[8:11]\n"
"v_exp_f32 v155, v155\n"
"v_mfma_f32_16x16x32_bf16 v[40:43], v[136:139], a[244:247], v[40:43]\n"
"v_exp_f32 v152, v152\n"
"v_mfma_f32_16x16x32_bf16 v[40:43], v[140:143], a[248:251], v[40:43]\n"
"ds_read_b128 v[212:215], v229 offset:20480\n"
"v_cvt_pk_bf16_f32 v150, v176, v177\n"
"v_mfma_f32_16x16x32_bf16 v[72:75], v[144:147], a[244:247], v[72:75]\n"
"v_exp_f32 v158, v158\n"
"v_mfma_f32_16x16x32_bf16 v[72:75], v[148:151], a[248:251], v[72:75]\n"
"v_exp_f32 v163, v163\n"
"v_mfma_f32_16x16x32_bf16 v[76:79], v[148:151], v[224:227], v[76:79]\n"
"ds_read_b128 v[220:223], v229 offset:21504\n"
"v_mfma_f32_16x16x32_bf16 v[76:79], v[144:147], a[252:255], v[76:79]\n"
"v_exp_f32 v159, v159\n"
"v_mfma_f32_16x16x32_bf16 v[44:47], v[140:143], v[224:227], v[44:47]\n"
"s_add_u32 s40, s56, s53\n"
"ds_read_b64_tr_b16 a[244:245], v234 offset:1024\n"
"v_mfma_f32_16x16x32_bf16 v[44:47], v[136:139], a[252:255], v[44:47]\n"
"buffer_load_dwordx4 v240, s[20:23], s40 offen offset:2048 nt lds\n"
"v_mfma_f32_16x16x32_bf16 v[12:15], v[132:135], v[224:227], v[12:15]\n"
"v_exp_f32 v153, v153\n"
"v_mfma_f32_16x16x32_bf16 v[12:15], v[128:131], a[252:255], v[12:15]\n"
"v_exp_f32 v165, v165\n"
"v_mfma_f32_16x16x32_bf16 a[220:223], v[124:127], a[252:255], a[220:223]\n"
"v_exp_f32 v164, v164\n"
"v_smfmac_f32_16x16x64_bf16 v[196:199], a[240:243], v[128:135], v241\n"
"v_cvt_pk_bf16_f32 v97, v154, v155\n"
"v_mfma_f32_16x16x32_bf16 v[202:205], a[240:243], v[124:127], v[202:205]\n"
"s_waitcnt lgkmcnt(1)\n"
"ds_read_b64_tr_b16 a[254:255], v235 offset:5120\n"
"v_mfma_f32_16x16x32_bf16 v[168:171], v[216:219], a[8:11], v[180:183]\n"
"ds_read_b64_tr_b16 a[248:249], v238 offset:1024\n"
"v_mfma_f32_16x16x32_bf16 v[168:171], v[220:223], a[12:15], v[168:171]\n"
"ds_read_b64_tr_b16 a[246:247], v234 offset:5120\n"
"v_mfma_f32_16x16x32_bf16 v[168:171], v[208:211], a[0:3], v[168:171]\n"
"ds_read_b64_tr_b16 v[224:225], v239 offset:1024\n"
"v_cvt_pk_bf16_f32 v120, v164, v165\n"
"v_mfma_f32_16x16x32_bf16 v[168:171], v[212:215], a[4:7], v[168:171]\n"
"v_exp_f32 v166, v166\n"
"v_mfma_f32_16x16x32_bf16 v[172:175], v[208:211], a[16:19], v[184:187]\n"
"ds_read_b64_tr_b16 a[252:253], v235 offset:1024\n"
"v_cvt_pk_bf16_f32 v96, v152, v153\n"
"v_mfma_f32_16x16x32_bf16 v[172:175], v[212:215], a[20:23], v[172:175]\n"
"ds_read_b64_tr_b16 v[226:227], v239 offset:5120\n"
"v_cvt_pk_bf16_f32 v105, v158, v159\n"
"v_mfma_f32_16x16x32_bf16 v[172:175], v[216:219], a[24:27], v[172:175]\n"
"s_add_u32 s40, s56, s54\n"
"ds_read_b64_tr_b16 a[250:251], v238 offset:5120\n"
"v_mfma_f32_16x16x32_bf16 v[172:175], v[220:223], a[28:31], v[172:175]\n"
"buffer_load_dwordx4 v240, s[20:23], s40 offen offset:3072 nt lds\n"
"v_mfma_f32_16x16x32_bf16 v[176:179], v[216:219], a[40:43], v[188:191]\n"
"s_add_u32 s55, s55, s59\n"
"v_exp_f32 v167, v167\n"
"v_mfma_f32_16x16x32_bf16 v[176:179], v[220:223], a[44:47], v[176:179]\n"
"s_add_u32 s41, s55, s57\n"
"v_exp_f32 v161, v161\n"
"v_mfma_f32_16x16x32_bf16 v[176:179], v[208:211], a[32:35], v[176:179]\n"
"s_and_b32 s41, s41, s58\n"
"v_exp_f32 v157, v157\n"
"v_mfma_f32_16x16x32_bf16 v[176:179], v[212:215], a[36:39], v[176:179]\n"
"s_andn2_b32 s56, s55, s58\n"
"v_exp_f32 v160, v160\n"
"v_mfma_f32_16x16x32_bf16 v[152:155], v[208:211], a[192:195], v[192:195]\n"
"s_or_b32 s56, s56, s41\n"
"ds_read_b128 v[208:211], v230 offset:16384\n"
"v_mfma_f32_16x16x32_bf16 v[152:155], v[212:215], a[196:199], v[152:155]\n"
"v_exp_f32 v172, v172\n"
"v_mfma_f32_16x16x32_bf16 v[152:155], v[216:219], a[200:203], v[152:155]\n"
"ds_read_b128 v[216:219], v230 offset:17408\n"
"v_mfma_f32_16x16x32_bf16 v[152:155], v[220:223], a[204:207], v[152:155]\n"
"s_waitcnt lgkmcnt(2)\n"
"v_exp_f32 v162, v162\n"
"v_mfma_f32_16x16x32_bf16 a[232:235], v[124:127], a[244:247], a[232:235]\n"
"ds_read_b128 v[212:215], v231 offset:16384\n"
"v_mfma_f32_16x16x32_bf16 v[24:27], v[128:131], a[244:247], v[24:27]\n"
"s_add_u32 m0, s44, 49152\n"
"ds_read_b128 v[220:223], v231 offset:17408\n"
"v_mfma_f32_16x16x32_bf16 v[24:27], v[132:135], a[248:251], v[24:27]\n"
"buffer_load_dwordx4 v240, s[16:19], s56 offen nt lds\n"
"v_mfma_f32_16x16x32_bf16 v[56:59], v[136:139], a[244:247], v[56:59]\n"
"v_exp_f32 v171, v171\n"
"v_mfma_f32_16x16x32_bf16 v[56:59], v[140:143], a[248:251], v[56:59]\n"
"v_exp_f32 v169, v169\n"
"v_mfma_f32_16x16x32_bf16 v[88:91], v[144:147], a[244:247], v[88:91]\n"
"v_exp_f32 v154, v154\n"
"v_mfma_f32_16x16x32_bf16 v[88:91], v[148:151], a[248:251], v[88:91]\n"
"v_exp_f32 v156, v156\n"
"v_mfma_f32_16x16x32_bf16 v[92:95], v[148:151], v[224:227], v[92:95]\n"
"ds_read_b64_tr_b16 a[244:245], v232 offset:0\n"
"v_cvt_pk_bf16_f32 v113, v162, v163\n"
"v_mfma_f32_16x16x32_bf16 v[92:95], v[144:147], a[252:255], v[92:95]\n"
"ds_read_b64_tr_b16 a[250:251], v236 offset:4096\n"
"v_cvt_pk_bf16_f32 v104, v156, v157\n"
"v_mfma_f32_16x16x32_bf16 v[60:63], v[140:143], v[224:227], v[60:63]\n"
"ds_read_b64_tr_b16 a[246:247], v232 offset:4096\n"
"v_cvt_pk_bf16_f32 v112, v160, v161\n"
"v_mfma_f32_16x16x32_bf16 v[60:63], v[136:139], a[252:255], v[60:63]\n"
"v_exp_f32 v152, v152\n"
"v_mfma_f32_16x16x32_bf16 v[28:31], v[132:135], v[224:227], v[28:31]\n"
"ds_read_b64_tr_b16 v[224:225], v237 offset:0\n"
"ds_read_b64_tr_b16 v[226:227], v237 offset:4096\n"
"v_mfma_f32_16x16x32_bf16 v[28:31], v[128:131], a[252:255], v[28:31]\n"
"ds_read_b64_tr_b16 a[248:249], v236 offset:0\n"
"v_cvt_pk_bf16_f32 v121, v166, v167\n"
"v_mfma_f32_16x16x32_bf16 a[236:239], v[124:127], a[252:255], a[236:239]\n"
"s_add_u32 s40, s56, s52\n"
"ds_read_b64_tr_b16 a[252:253], v233 offset:0\n"
"v_smfmac_f32_16x16x64_bf16 v[198:201], a[240:243], v[136:143], v241\n"
"s_waitcnt lgkmcnt(7)\n"
"buffer_load_dwordx4 v240, s[16:19], s40 offen offset:1024 nt lds\n"
"v_mfma_f32_16x16x32_bf16 v[156:159], v[208:211], a[0:3], v[180:183]\n"
"v_exp_f32 v175, v175\n"
"v_mfma_f32_16x16x32_bf16 v[156:159], v[212:215], a[4:7], v[156:159]\n"
"v_exp_f32 v173, v173\n"
"v_mfma_f32_16x16x32_bf16 v[156:159], v[216:219], a[8:11], v[156:159]\n"
"v_exp_f32 v168, v168\n"
"v_mfma_f32_16x16x32_bf16 v[156:159], v[220:223], a[12:15], v[156:159]\n"
"ds_read_b64_tr_b16 a[254:255], v233 offset:4096\n"
"v_mfma_f32_16x16x32_bf16 v[160:163], v[216:219], a[24:27], v[184:187]\n"
"v_exp_f32 v170, v170\n"
"v_mfma_f32_16x16x32_bf16 v[160:163], v[220:223], a[28:31], v[160:163]\n"
"v_exp_f32 v178, v178\n"
"v_mfma_f32_16x16x32_bf16 v[160:163], v[208:211], a[16:19], v[160:163]\n"
"v_exp_f32 v177, v177\n"
"v_mfma_f32_16x16x32_bf16 v[160:163], v[212:215], a[20:23], v[160:163]\n"
"v_exp_f32 v176, v176\n"
"v_mfma_f32_16x16x32_bf16 v[164:167], v[208:211], a[32:35], v[188:191]\n"
"ds_read_b128 v[208:211], v230 offset:20480\n"
"v_cvt_pk_bf16_f32 v99, v170, v171\n"
"v_mfma_f32_16x16x32_bf16 v[164:167], v[212:215], a[36:39], v[164:167]\n"
"ds_read_b128 v[212:215], v231 offset:20480\n"
"v_mfma_f32_16x16x32_bf16 v[164:167], v[216:219], a[40:43], v[164:167]\n"
"v_exp_f32 v153, v153\n"
"v_mfma_f32_16x16x32_bf16 v[164:167], v[220:223], a[44:47], v[164:167]\n"
"s_waitcnt lgkmcnt(2)\n"
"ds_read_b128 v[220:223], v231 offset:21504\n"
"v_cvt_pk_bf16_f32 v106, v172, v173\n"
"v_mfma_f32_16x16x32_bf16 a[208:211], v[124:127], a[244:247], a[208:211]\n"
"v_exp_f32 v174, v174\n"
"v_mfma_f32_16x16x32_bf16 v[0:3], v[128:131], a[244:247], v[0:3]\n"
"s_add_u32 s40, s56, s53\n"
"ds_read_b128 v[216:219], v230 offset:21504\n"
"v_mfma_f32_16x16x32_bf16 v[0:3], v[132:135], a[248:251], v[0:3]\n"
"buffer_load_dwordx4 v240, s[16:19], s40 offen offset:2048 nt lds\n"
"v_mfma_f32_16x16x32_bf16 v[32:35], v[136:139], a[244:247], v[32:35]\n"
"v_exp_f32 v163, v163\n"
"v_mfma_f32_16x16x32_bf16 v[32:35], v[140:143], a[248:251], v[32:35]\n"
"v_exp_f32 v161, v161\n"
"v_mfma_f32_16x16x32_bf16 v[64:67], v[144:147], a[244:247], v[64:67]\n"
"v_exp_f32 v155, v155\n"
"v_mfma_f32_16x16x32_bf16 v[64:67], v[148:151], a[248:251], v[64:67]\n"
"v_exp_f32 v166, v166\n"
"v_mfma_f32_16x16x32_bf16 v[68:71], v[148:151], v[224:227], v[68:71]\n"
"ds_read_b64_tr_b16 a[250:251], v236 offset:5120\n"
"v_cvt_pk_bf16_f32 v107, v174, v175\n"
"v_mfma_f32_16x16x32_bf16 v[68:71], v[144:147], a[252:255], v[68:71]\n"
"ds_read_b64_tr_b16 a[246:247], v232 offset:5120\n"
"v_cvt_pk_bf16_f32 v98, v168, v169\n"
"v_mfma_f32_16x16x32_bf16 v[36:39], v[140:143], v[224:227], v[36:39]\n"
"v_exp_f32 v179, v179\n"
"v_mfma_f32_16x16x32_bf16 v[36:39], v[136:139], a[252:255], v[36:39]\n"
"ds_read_b64_tr_b16 a[244:245], v232 offset:1024\n"
"v_cvt_pk_bf16_f32 v122, v152, v153\n"
"v_mfma_f32_16x16x32_bf16 v[4:7], v[132:135], v[224:227], v[4:7]\n"
"s_add_u32 s40, s56, s54\n"
"ds_read_b64_tr_b16 a[248:249], v236 offset:1024\n"
"v_mfma_f32_16x16x32_bf16 v[4:7], v[128:131], a[252:255], v[4:7]\n"
"buffer_load_dwordx4 v240, s[16:19], s40 offen offset:3072 nt lds\n"
"v_mfma_f32_16x16x32_bf16 a[212:215], v[124:127], a[252:255], a[212:215]\n"
"v_exp_f32 v162, v162\n"
"v_smfmac_f32_16x16x64_bf16 v[200:203], a[240:243], v[144:151], v241\n"
"s_waitcnt lgkmcnt(4)\n"
"v_cvt_pk_bf16_f32 v114, v176, v177\n"
"v_mfma_f32_16x16x32_bf16 v[168:171], v[208:211], a[0:3], v[180:183]\n"
"v_exp_f32 v157, v157\n"
"v_mfma_f32_16x16x32_bf16 v[168:171], v[212:215], a[4:7], v[168:171]\n"
"v_exp_f32 v165, v165\n"
"v_mfma_f32_16x16x32_bf16 v[168:171], v[216:219], a[8:11], v[168:171]\n"
"ds_read_b64_tr_b16 a[254:255], v233 offset:5120\n"
"ds_read_b64_tr_b16 v[226:227], v237 offset:5120\n"
"v_mfma_f32_16x16x32_bf16 v[168:171], v[220:223], a[12:15], v[168:171]\n"
"ds_read_b64_tr_b16 a[252:253], v233 offset:1024\n"
"v_cvt_pk_bf16_f32 v115, v178, v179\n"
"v_mfma_f32_16x16x32_bf16 v[172:175], v[216:219], a[24:27], v[184:187]\n"
"ds_read_b64_tr_b16 v[224:225], v237 offset:1024\n"
"v_mfma_f32_16x16x32_bf16 v[172:175], v[220:223], a[28:31], v[172:175]\n"
"v_exp_f32 v160, v160\n"
"v_mfma_f32_16x16x32_bf16 v[172:175], v[208:211], a[16:19], v[172:175]\n"
"v_exp_f32 v158, v158\n"
"v_mfma_f32_16x16x32_bf16 v[172:175], v[212:215], a[20:23], v[172:175]\n"
"s_cbranch_vccnz LPCL1_%=\nLPKL1_%=:\n"
"v_mfma_f32_16x16x32_bf16 v[176:179], v[208:211], a[32:35], v[188:191]\n"
"v_exp_f32 v159, v159\n"
"v_mfma_f32_16x16x32_bf16 v[176:179], v[212:215], a[36:39], v[176:179]\n"
"v_exp_f32 v167, v167\n"
"v_mfma_f32_16x16x32_bf16 v[176:179], v[216:219], a[40:43], v[176:179]\n"
"v_exp_f32 v164, v164\n"
"v_mfma_f32_16x16x32_bf16 v[176:179], v[220:223], a[44:47], v[176:179]\n"
"s_waitcnt lgkmcnt(1)\n"
"v_mfma_f32_16x16x32_bf16 a[224:227], v[124:127], a[244:247], a[224:227]\n"
"v_exp_f32 v169, v169\n"
"v_mfma_f32_16x16x32_bf16 v[16:19], v[128:131], a[244:247], v[16:19]\n"
"s_waitcnt vmcnt(8)\n"
"v_exp_f32 v175, v175\n"
"v_mfma_f32_16x16x32_bf16 v[16:19], v[132:135], a[248:251], v[16:19]\n"
"s_barrier\n"
"ds_read_b128 v[220:223], v229 offset:33792\n"
"v_mfma_f32_16x16x32_bf16 v[48:51], v[136:139], a[244:247], v[48:51]\n"
"ds_read_b128 v[216:219], v228 offset:33792\n"
"v_cvt_pk_bf16_f32 v109, v162, v163\n"
"v_mfma_f32_16x16x32_bf16 v[48:51], v[140:143], a[248:251], v[48:51]\n"
"ds_read_b128 v[212:215], v229 offset:32768\n"
"v_mfma_f32_16x16x32_bf16 v[80:83], v[144:147], a[244:247], v[80:83]\n"
"ds_read_b128 v[208:211], v228 offset:32768\n"
"v_cvt_pk_bf16_f32 v101, v158, v159\n"
"v_mfma_f32_16x16x32_bf16 v[80:83], v[148:151], a[248:251], v[80:83]\n"
"s_waitcnt lgkmcnt(4)\n"
"v_exp_f32 v178, v178\n"
"v_mfma_f32_16x16x32_bf16 v[84:87], v[148:151], v[224:227], v[84:87]\n"
"ds_read_b64_tr_b16 a[244:245], v234 offset:16384\n"
"v_cvt_pk_bf16_f32 v117, v166, v167\n"
"v_mfma_f32_16x16x32_bf16 v[84:87], v[144:147], a[252:255], v[84:87]\n"
"ds_read_b64_tr_b16 a[250:251], v238 offset:20480\n"
"v_cvt_pk_bf16_f32 v123, v154, v155\n"
"v_mfma_f32_16x16x32_bf16 v[52:55], v[140:143], v[224:227], v[52:55]\n"
"v_exp_f32 v173, v173\n"
"v_mfma_f32_16x16x32_bf16 v[52:55], v[136:139], a[252:255], v[52:55]\n"
"ds_read_b64_tr_b16 a[248:249], v238 offset:16384\n"
"v_mfma_f32_16x16x32_bf16 v[20:23], v[132:135], v[224:227], v[20:23]\n"
"v_exp_f32 v156, v156\n"
"v_mfma_f32_16x16x32_bf16 v[20:23], v[128:131], a[252:255], v[20:23]\n"
"ds_read_b64_tr_b16 v[226:227], v239 offset:20480\n"
"v_cvt_pk_bf16_f32 v100, v156, v157\n"
"v_mfma_f32_16x16x32_bf16 a[228:231], v[124:127], a[252:255], a[228:231]\n"
"s_waitcnt lgkmcnt(4)\n"
"ds_read_b64_tr_b16 a[246:247], v234 offset:20480\n"
"LAY2_%=:\n"
"v_mfma_f32_16x16x32_bf16 v[152:155], v[208:211], a[0:3], v[180:183]\n"
"ds_read_b64_tr_b16 v[224:225], v239 offset:16384\n"
"v_cvt_pk_bf16_f32 v116, v164, v165\n"
"v_mfma_f32_16x16x32_bf16 v[152:155], v[212:215], a[4:7], v[152:155]\n"
"ds_read_b64_tr_b16 a[252:253], v235 offset:16384\n"
"v_cvt_pk_bf16_f32 v108, v160, v161\n"
"v_mfma_f32_16x16x32_bf16 v[152:155], v[216:219], a[8:11], v[152:155]\n"
"s_add_u32 m0, s44, 114688\n"
"ds_read_b64_tr_b16 a[254:255], v235 offset:20480\n"
"v_mfma_f32_16x16x32_bf16 v[152:155], v[220:223], a[12:15], v[152:155]\n"
"buffer_load_dwordx4 v240, s[20:23], s56 offen nt lds\n"
"v_mfma_f32_16x16x32_bf16 v[156:159], v[216:219], a[24:27], v[184:187]\n"
"v_exp_f32 v177, v177\n"
"v_mfma_f32_16x16x32_bf16 v[156:159], v[220:223], a[28:31], v[156:159]\n"
"v_exp_f32 v168, v168\n"
"v_mfma_f32_16x16x32_bf16 v[156:159], v[208:211], a[16:19], v[156:159]\n"
"v_exp_f32 v179, v179\n"
"v_mfma_f32_16x16x32_bf16 v[156:159], v[212:215], a[20:23], v[156:159]\n"
"v_exp_f32 v174, v174\n"
"v_mfma_f32_16x16x32_bf16 v[160:163], v[208:211], a[32:35], v[188:191]\n"
"v_exp_f32 v172, v172\n"
"v_mfma_f32_16x16x32_bf16 v[160:163], v[212:215], a[36:39], v[160:163]\n"
"v_exp_f32 v171, v171\n"
"v_mfma_f32_16x16x32_bf16 v[160:163], v[216:219], a[40:43], v[160:163]\n"
"v_exp_f32 v154, v154\n"
"v_mfma_f32_16x16x32_bf16 v[160:163], v[220:223], a[44:47], v[160:163]\n"
"v_exp_f32 v170, v170\n"
"v_mfma_f32_16x16x32_bf16 v[164:167], v[216:219], a[200:203], v[192:195]\n"
"ds_read_b128 v[216:219], v228 offset:37888\n"
"v_cvt_pk_bf16_f32 v103, v170, v171\n"
"v_mfma_f32_16x16x32_bf16 v[164:167], v[220:223], a[204:207], v[164:167]\n"
"v_cvt_pk_bf16_f32 v102, v168, v169\n"
"v_cvt_pk_bf16_f32 v119, v178, v179\n"
"v_mfma_f32_16x16x32_bf16 v[164:167], v[208:211], a[192:195], v[164:167]\n"
"s_add_u32 s40, s56, s52\n"
"ds_read_b128 v[208:211], v228 offset:36864\n"
"v_mfma_f32_16x16x32_bf16 v[164:167], v[212:215], a[196:199], v[164:167]\n"
"s_waitcnt lgkmcnt(2)\n"
"buffer_load_dwordx4 v240, s[20:23], s40 offen offset:1024 nt lds\n"
"v_mfma_f32_16x16x32_bf16 a[216:219], v[120:123], a[244:247], a[216:219]\n"
"v_cvt_pk_bf16_f32 v110, v172, v173\n"
"v_cvt_pk_bf16_f32 v111, v174, v175\n"
"v_mfma_f32_16x16x32_bf16 v[8:11], v[96:99], a[244:247], v[8:11]\n"
"s_waitcnt lgkmcnt(2)\n"
"v_exp_f32 v176, v176\n"
"v_mfma_f32_16x16x32_bf16 v[8:11], v[100:103], a[248:251], v[8:11]\n"
"v_exp_f32 v155, v155\n"
"v_mfma_f32_16x16x32_bf16 v[40:43], v[104:107], a[244:247], v[40:43]\n"
"v_exp_f32 v152, v152\n"
"v_mfma_f32_16x16x32_bf16 v[40:43], v[108:111], a[248:251], v[40:43]\n"
"ds_read_b128 v[212:215], v229 offset:36864\n"
"v_cvt_pk_bf16_f32 v118, v176, v177\n"
"v_mfma_f32_16x16x32_bf16 v[72:75], v[112:115], a[244:247], v[72:75]\n"
"v_exp_f32 v158, v158\n"
"v_mfma_f32_16x16x32_bf16 v[72:75], v[116:119], a[248:251], v[72:75]\n"
"v_exp_f32 v163, v163\n"
"v_mfma_f32_16x16x32_bf16 v[76:79], v[116:119], v[224:227], v[76:79]\n"
"ds_read_b128 v[220:223], v229 offset:37888\n"
"v_mfma_f32_16x16x32_bf16 v[76:79], v[112:115], a[252:255], v[76:79]\n"
"v_exp_f32 v159, v159\n"
"v_mfma_f32_16x16x32_bf16 v[44:47], v[108:111], v[224:227], v[44:47]\n"
"s_add_u32 s40, s56, s53\n"
"ds_read_b64_tr_b16 a[244:245], v234 offset:17408\n"
"v_mfma_f32_16x16x32_bf16 v[44:47], v[104:107], a[252:255], v[44:47]\n"
"buffer_load_dwordx4 v240, s[20:23], s40 offen offset:2048 nt lds\n"
"v_mfma_f32_16x16x32_bf16 v[12:15], v[100:103], v[224:227], v[12:15]\n"
"v_exp_f32 v153, v153\n"
"v_mfma_f32_16x16x32_bf16 v[12:15], v[96:99], a[252:255], v[12:15]\n"
"v_exp_f32 v165, v165\n"
"v_mfma_f32_16x16x32_bf16 a[220:223], v[120:123], a[252:255], a[220:223]\n"
"v_exp_f32 v164, v164\n"
"v_smfmac_f32_16x16x64_bf16 v[196:199], a[240:243], v[96:103], v241\n"
"v_cvt_pk_bf16_f32 v129, v154, v155\n"
"v_mfma_f32_16x16x32_bf16 v[202:205], a[240:243], v[120:123], v[202:205]\n"
"s_waitcnt lgkmcnt(1)\n"
"ds_read_b64_tr_b16 a[254:255], v235 offset:21504\n"
"v_mfma_f32_16x16x32_bf16 v[168:171], v[216:219], a[8:11], v[180:183]\n"
"ds_read_b64_tr_b16 a[248:249], v238 offset:17408\n"
"v_mfma_f32_16x16x32_bf16 v[168:171], v[220:223], a[12:15], v[168:171]\n"
"ds_read_b64_tr_b16 a[246:247], v234 offset:21504\n"
"v_mfma_f32_16x16x32_bf16 v[168:171], v[208:211], a[0:3], v[168:171]\n"
"ds_read_b64_tr_b16 v[224:225], v239 offset:17408\n"
"v_cvt_pk_bf16_f32 v124, v164, v165\n"
"v_mfma_f32_16x16x32_bf16 v[168:171], v[212:215], a[4:7], v[168:171]\n"
"v_exp_f32 v166, v166\n"
"v_mfma_f32_16x16x32_bf16 v[172:175], v[208:211], a[16:19], v[184:187]\n"
"ds_read_b64_tr_b16 a[252:253], v235 offset:17408\n"
"v_cvt_pk_bf16_f32 v128, v152, v153\n"
"v_mfma_f32_16x16x32_bf16 v[172:175], v[212:215], a[20:23], v[172:175]\n"
"ds_read_b64_tr_b16 v[226:227], v239 offset:21504\n"
"v_cvt_pk_bf16_f32 v137, v158, v159\n"
"v_mfma_f32_16x16x32_bf16 v[172:175], v[216:219], a[24:27], v[172:175]\n"
"s_add_u32 s40, s56, s54\n"
"ds_read_b64_tr_b16 a[250:251], v238 offset:21504\n"
"v_mfma_f32_16x16x32_bf16 v[172:175], v[220:223], a[28:31], v[172:175]\n"
"buffer_load_dwordx4 v240, s[20:23], s40 offen offset:3072 nt lds\n"
"v_mfma_f32_16x16x32_bf16 v[176:179], v[216:219], a[40:43], v[188:191]\n"
"s_add_u32 s55, s55, s59\n"
"v_exp_f32 v167, v167\n"
"v_mfma_f32_16x16x32_bf16 v[176:179], v[220:223], a[44:47], v[176:179]\n"
"s_add_u32 s41, s55, s57\n"
"v_exp_f32 v161, v161\n"
"v_mfma_f32_16x16x32_bf16 v[176:179], v[208:211], a[32:35], v[176:179]\n"
"s_and_b32 s41, s41, s58\n"
"v_exp_f32 v157, v157\n"
"v_mfma_f32_16x16x32_bf16 v[176:179], v[212:215], a[36:39], v[176:179]\n"
"s_andn2_b32 s56, s55, s58\n"
"v_exp_f32 v160, v160\n"
"v_mfma_f32_16x16x32_bf16 v[152:155], v[208:211], a[192:195], v[192:195]\n"
"s_or_b32 s56, s56, s41\n"
"ds_read_b128 v[208:211], v230 offset:32768\n"
"v_mfma_f32_16x16x32_bf16 v[152:155], v[212:215], a[196:199], v[152:155]\n"
"s_cmp_ge_u32 s47, 15\n"
"v_exp_f32 v172, v172\n"
"v_mfma_f32_16x16x32_bf16 v[152:155], v[216:219], a[200:203], v[152:155]\n"
"s_cbranch_scc1 LSW_%=\nLSWK_%=:\n"
"ds_read_b128 v[216:219], v230 offset:33792\n"
"v_mfma_f32_16x16x32_bf16 v[152:155], v[220:223], a[204:207], v[152:155]\n"
"s_waitcnt lgkmcnt(2)\n"
"v_exp_f32 v162, v162\n"
"v_mfma_f32_16x16x32_bf16 a[232:235], v[120:123], a[244:247], a[232:235]\n"
"ds_read_b128 v[212:215], v231 offset:32768\n"
"v_mfma_f32_16x16x32_bf16 v[24:27], v[96:99], a[244:247], v[24:27]\n"
"s_mov_b32 m0, s44\n"
"ds_read_b128 v[220:223], v231 offset:33792\n"
"v_mfma_f32_16x16x32_bf16 v[24:27], v[100:103], a[248:251], v[24:27]\n"
"buffer_load_dwordx4 v240, s[16:19], s56 offen nt lds\n"
"v_mfma_f32_16x16x32_bf16 v[56:59], v[104:107], a[244:247], v[56:59]\n"
"v_exp_f32 v171, v171\n"
"v_mfma_f32_16x16x32_bf16 v[56:59], v[108:111], a[248:251], v[56:59]\n"
"v_exp_f32 v169, v169\n"
"v_mfma_f32_16x16x32_bf16 v[88:91], v[112:115], a[244:247], v[88:91]\n"
"v_exp_f32 v154, v154\n"
"v_mfma_f32_16x16x32_bf16 v[88:91], v[116:119], a[248:251], v[88:91]\n"
"v_exp_f32 v156, v156\n"
"v_mfma_f32_16x16x32_bf16 v[92:95], v[116:119], v[224:227], v[92:95]\n"
"ds_read_b64_tr_b16 a[244:245], v232 offset:16384\n"
"v_cvt_pk_bf16_f32 v145, v162, v163\n"
"v_mfma_f32_16x16x32_bf16 v[92:95], v[112:115], a[252:255], v[92:95]\n"
"ds_read_b64_tr_b16 a[250:251], v236 offset:20480\n"
"v_cvt_pk_bf16_f32 v136, v156, v157\n"
"v_mfma_f32_16x16x32_bf16 v[60:63], v[108:111], v[224:227], v[60:63]\n"
"ds_read_b64_tr_b16 a[246:247], v232 offset:20480\n"
"v_cvt_pk_bf16_f32 v144, v160, v161\n"
"v_mfma_f32_16x16x32_bf16 v[60:63], v[104:107], a[252:255], v[60:63]\n"
"v_exp_f32 v152, v152\n"
"v_mfma_f32_16x16x32_bf16 v[28:31], v[100:103], v[224:227], v[28:31]\n"
"ds_read_b64_tr_b16 v[224:225], v237 offset:16384\n"
"ds_read_b64_tr_b16 v[226:227], v237 offset:20480\n"
"v_mfma_f32_16x16x32_bf16 v[28:31], v[96:99], a[252:255], v[28:31]\n"
"ds_read_b64_tr_b16 a[248:249], v236 offset:16384\n"
"v_cvt_pk_bf16_f32 v125, v166, v167\n"
"v_mfma_f32_16x16x32_bf16 a[236:239], v[120:123], a[252:255], a[236:239]\n"
"s_add_u32 s40, s56, s52\n"
"ds_read_b64_tr_b16 a[252:253], v233 offset:16384\n"
"v_smfmac_f32_16x16x64_bf16 v[198:201], a[240:243], v[104:111], v241\n"
"s_waitcnt lgkmcnt(7)\n"
"buffer_load_dwordx4 v240, s[16:19], s40 offen offset:1024 nt lds\n"
"v_mfma_f32_16x16x32_bf16 v[156:159], v[208:211], a[0:3], v[180:183]\n"
"v_exp_f32 v175, v175\n"
"v_mfma_f32_16x16x32_bf16 v[156:159], v[212:215], a[4:7], v[156:159]\n"
"v_exp_f32 v173, v173\n"
"v_mfma_f32_16x16x32_bf16 v[156:159], v[216:219], a[8:11], v[156:159]\n"
"v_exp_f32 v168, v168\n"
"v_mfma_f32_16x16x32_bf16 v[156:159], v[220:223], a[12:15], v[156:159]\n"
"ds_read_b64_tr_b16 a[254:255], v233 offset:20480\n"
"v_mfma_f32_16x16x32_bf16 v[160:163], v[216:219], a[24:27], v[184:187]\n"
"v_exp_f32 v170, v170\n"
"v_mfma_f32_16x16x32_bf16 v[160:163], v[220:223], a[28:31], v[160:163]\n"
"v_exp_f32 v178, v178\n"
"v_mfma_f32_16x16x32_bf16 v[160:163], v[208:211], a[16:19], v[160:163]\n"
"v_exp_f32 v177, v177\n"
"v_mfma_f32_16x16x32_bf16 v[160:163], v[212:215], a[20:23], v[160:163]\n"
"v_exp_f32 v176, v176\n"
"v_mfma_f32_16x16x32_bf16 v[164:167], v[208:211], a[32:35], v[188:191]\n"
"ds_read_b128 v[208:211], v230 offset:36864\n"
"v_cvt_pk_bf16_f32 v131, v170, v171\n"
"v_mfma_f32_16x16x32_bf16 v[164:167], v[212:215], a[36:39], v[164:167]\n"
"ds_read_b128 v[212:215], v231 offset:36864\n"
"v_mfma_f32_16x16x32_bf16 v[164:167], v[216:219], a[40:43], v[164:167]\n"
"v_exp_f32 v153, v153\n"
"v_mfma_f32_16x16x32_bf16 v[164:167], v[220:223], a[44:47], v[164:167]\n"
"s_waitcnt lgkmcnt(2)\n"
"ds_read_b128 v[220:223], v231 offset:37888\n"
"v_cvt_pk_bf16_f32 v138, v172, v173\n"
"v_mfma_f32_16x16x32_bf16 a[208:211], v[120:123], a[244:247], a[208:211]\n"
"v_exp_f32 v174, v174\n"
"v_mfma_f32_16x16x32_bf16 v[0:3], v[96:99], a[244:247], v[0:3]\n"
"s_add_u32 s40, s56, s53\n"
"ds_read_b128 v[216:219], v230 offset:37888\n"
"v_mfma_f32_16x16x32_bf16 v[0:3], v[100:103], a[248:251], v[0:3]\n"
"buffer_load_dwordx4 v240, s[16:19], s40 offen offset:2048 nt lds\n"
"v_mfma_f32_16x16x32_bf16 v[32:35], v[104:107], a[244:247], v[32:35]\n"
"v_exp_f32 v163, v163\n"
"v_mfma_f32_16x16x32_bf16 v[32:35], v[108:111], a[248:251], v[32:35]\n"
"v_exp_f32 v161, v161\n"
"v_mfma_f32_16x16x32_bf16 v[64:67], v[112:115], a[244:247], v[64:67]\n"
"v_exp_f32 v155, v155\n"
"v_mfma_f32_16x16x32_bf16 v[64:67], v[116:119], a[248:251], v[64:67]\n"
"v_exp_f32 v166, v166\n"
"v_mfma_f32_16x16x32_bf16 v[68:71], v[116:119], v[224:227], v[68:71]\n"
"ds_read_b64_tr_b16 a[250:251], v236 offset:21504\n"
"v_cvt_pk_bf16_f32 v139, v174, v175\n"
"v_mfma_f32_16x16x32_bf16 v[68:71], v[112:115], a[252:255], v[68:71]\n"
"ds_read_b64_tr_b16 a[246:247], v232 offset:21504\n"
"v_cvt_pk_bf16_f32 v130, v168, v169\n"
"v_mfma_f32_16x16x32_bf16 v[36:39], v[108:111], v[224:227], v[36:39]\n"
"v_exp_f32 v179, v179\n"
"v_mfma_f32_16x16x32_bf16 v[36:39], v[104:107], a[252:255], v[36:39]\n"
"ds_read_b64_tr_b16 a[244:245], v232 offset:17408\n"
"v_cvt_pk_bf16_f32 v126, v152, v153\n"
"v_mfma_f32_16x16x32_bf16 v[4:7], v[100:103], v[224:227], v[4:7]\n"
"s_add_u32 s40, s56, s54\n"
"ds_read_b64_tr_b16 a[248:249], v236 offset:17408\n"
"v_mfma_f32_16x16x32_bf16 v[4:7], v[96:99], a[252:255], v[4:7]\n"
"buffer_load_dwordx4 v240, s[16:19], s40 offen offset:3072 nt lds\n"
"v_mfma_f32_16x16x32_bf16 a[212:215], v[120:123], a[252:255], a[212:215]\n"
"v_exp_f32 v162, v162\n"
"v_smfmac_f32_16x16x64_bf16 v[200:203], a[240:243], v[112:119], v241\n"
"s_waitcnt lgkmcnt(4)\n"
"v_cvt_pk_bf16_f32 v146, v176, v177\n"
"v_mfma_f32_16x16x32_bf16 v[168:171], v[208:211], a[0:3], v[180:183]\n"
"v_exp_f32 v157, v157\n"
"v_mfma_f32_16x16x32_bf16 v[168:171], v[212:215], a[4:7], v[168:171]\n"
"v_exp_f32 v165, v165\n"
"v_mfma_f32_16x16x32_bf16 v[168:171], v[216:219], a[8:11], v[168:171]\n"
"ds_read_b64_tr_b16 a[254:255], v233 offset:21504\n"
"ds_read_b64_tr_b16 v[226:227], v237 offset:21504\n"
"v_mfma_f32_16x16x32_bf16 v[168:171], v[220:223], a[12:15], v[168:171]\n"
"ds_read_b64_tr_b16 a[252:253], v233 offset:17408\n"
"v_cvt_pk_bf16_f32 v147, v178, v179\n"
"v_mfma_f32_16x16x32_bf16 v[172:175], v[216:219], a[24:27], v[184:187]\n"
"ds_read_b64_tr_b16 v[224:225], v237 offset:17408\n"
"v_mfma_f32_16x16x32_bf16 v[172:175], v[220:223], a[28:31], v[172:175]\n"
"v_exp_f32 v160, v160\n"
"v_mfma_f32_16x16x32_bf16 v[172:175], v[208:211], a[16:19], v[172:175]\n"
"v_exp_f32 v158, v158\n"
"v_mfma_f32_16x16x32_bf16 v[172:175], v[212:215], a[20:23], v[172:175]\n"
"s_cbranch_vccnz LPCL2_%=\nLPKL2_%=:\n"
"v_mfma_f32_16x16x32_bf16 v[176:179], v[208:211], a[32:35], v[188:191]\n"
"v_exp_f32 v159, v159\n"
"v_mfma_f32_16x16x32_bf16 v[176:179], v[212:215], a[36:39], v[176:179]\n"
"v_exp_f32 v167, v167\n"
"v_mfma_f32_16x16x32_bf16 v[176:179], v[216:219], a[40:43], v[176:179]\n"
"v_exp_f32 v164, v164\n"
"v_mfma_f32_16x16x32_bf16 v[176:179], v[220:223], a[44:47], v[176:179]\n"
"s_waitcnt lgkmcnt(1)\n"
"v_mfma_f32_16x16x32_bf16 a[224:227], v[120:123], a[244:247], a[224:227]\n"
"v_exp_f32 v169, v169\n"
"v_mfma_f32_16x16x32_bf16 v[16:19], v[96:99], a[244:247], v[16:19]\n"
"s_waitcnt vmcnt(8)\n"
"v_exp_f32 v175, v175\n"
"v_mfma_f32_16x16x32_bf16 v[16:19], v[100:103], a[248:251], v[16:19]\n"
"s_barrier\n"
"ds_read_b128 v[220:223], v229 offset:50176\n"
"v_mfma_f32_16x16x32_bf16 v[48:51], v[104:107], a[244:247], v[48:51]\n"
"ds_read_b128 v[216:219], v228 offset:50176\n"
"v_cvt_pk_bf16_f32 v141, v162, v163\n"
"v_mfma_f32_16x16x32_bf16 v[48:51], v[108:111], a[248:251], v[48:51]\n"
"ds_read_b128 v[212:215], v229 offset:49152\n"
"v_mfma_f32_16x16x32_bf16 v[80:83], v[112:115], a[244:247], v[80:83]\n"
"ds_read_b128 v[208:211], v228 offset:49152\n"
"v_cvt_pk_bf16_f32 v133, v158, v159\n"
"v_mfma_f32_16x16x32_bf16 v[80:83], v[116:119], a[248:251], v[80:83]\n"
"s_waitcnt lgkmcnt(4)\n"
"v_exp_f32 v178, v178\n"
"v_mfma_f32_16x16x32_bf16 v[84:87], v[116:119], v[224:227], v[84:87]\n"
"ds_read_b64_tr_b16 a[244:245], v234 offset:32768\n"
"v_cvt_pk_bf16_f32 v149, v166, v167\n"
"v_mfma_f32_16x16x32_bf16 v[84:87], v[112:115], a[252:255], v[84:87]\n"
"ds_read_b64_tr_b16 a[250:251], v238 offset:36864\n"
"v_cvt_pk_bf16_f32 v127, v154, v155\n"
"v_mfma_f32_16x16x32_bf16 v[52:55], v[108:111], v[224:227], v[52:55]\n"
"v_exp_f32 v173, v173\n"
"v_mfma_f32_16x16x32_bf16 v[52:55], v[104:107], a[252:255], v[52:55]\n"
"ds_read_b64_tr_b16 a[248:249], v238 offset:32768\n"
"v_mfma_f32_16x16x32_bf16 v[20:23], v[100:103], v[224:227], v[20:23]\n"
"v_exp_f32 v156, v156\n"
"v_mfma_f32_16x16x32_bf16 v[20:23], v[96:99], a[252:255], v[20:23]\n"
"ds_read_b64_tr_b16 v[226:227], v239 offset:36864\n"
"v_cvt_pk_bf16_f32 v132, v156, v157\n"
"v_mfma_f32_16x16x32_bf16 a[228:231], v[120:123], a[252:255], a[228:231]\n"
"s_waitcnt lgkmcnt(4)\n"
"ds_read_b64_tr_b16 a[246:247], v234 offset:36864\n"
"v_mfma_f32_16x16x32_bf16 v[152:155], v[208:211], a[0:3], v[180:183]\n"
"ds_read_b64_tr_b16 v[224:225], v239 offset:32768\n"
"v_cvt_pk_bf16_f32 v148, v164, v165\n"
"v_mfma_f32_16x16x32_bf16 v[152:155], v[212:215], a[4:7], v[152:155]\n"
"ds_read_b64_tr_b16 a[252:253], v235 offset:32768\n"
"v_cvt_pk_bf16_f32 v140, v160, v161\n"
"v_mfma_f32_16x16x32_bf16 v[152:155], v[216:219], a[8:11], v[152:155]\n"
"s_add_u32 m0, s44, 65536\n"
"ds_read_b64_tr_b16 a[254:255], v235 offset:36864\n"
"v_mfma_f32_16x16x32_bf16 v[152:155], v[220:223], a[12:15], v[152:155]\n"
"buffer_load_dwordx4 v240, s[20:23], s56 offen nt lds\n"
"v_mfma_f32_16x16x32_bf16 v[156:159], v[216:219], a[24:27], v[184:187]\n"
"v_exp_f32 v177, v177\n"
"v_mfma_f32_16x16x32_bf16 v[156:159], v[220:223], a[28:31], v[156:159]\n"
"v_exp_f32 v168, v168\n"
"v_mfma_f32_16x16x32_bf16 v[156:159], v[208:211], a[16:19], v[156:159]\n"
"v_exp_f32 v179, v179\n"
"v_mfma_f32_16x16x32_bf16 v[156:159], v[212:215], a[20:23], v[156:159]\n"
"v_exp_f32 v174, v174\n"
"v_mfma_f32_16x16x32_bf16 v[160:163], v[208:211], a[32:35], v[188:191]\n"
"v_exp_f32 v172, v172\n"
"v_mfma_f32_16x16x32_bf16 v[160:163], v[212:215], a[36:39], v[160:163]\n"
"v_exp_f32 v171, v171\n"
"v_mfma_f32_16x16x32_bf16 v[160:163], v[216:219], a[40:43], v[160:163]\n"
"v_exp_f32 v154, v154\n"
"v_mfma_f32_16x16x32_bf16 v[160:163], v[220:223], a[44:47], v[160:163]\n"
"v_exp_f32 v170, v170\n"
"v_mfma_f32_16x16x32_bf16 v[164:167], v[216:219], a[200:203], v[192:195]\n"
"ds_read_b128 v[216:219], v228 offset:54272\n"
"v_cvt_pk_bf16_f32 v135, v170, v171\n"
"v_mfma_f32_16x16x32_bf16 v[164:167], v[220:223], a[204:207], v[164:167]\n"
"v_cvt_pk_bf16_f32 v134, v168, v169\n"
"v_cvt_pk_bf16_f32 v151, v178, v179\n"
"v_mfma_f32_16x16x32_bf16 v[164:167], v[208:211], a[192:195], v[164:167]\n"
"s_add_u32 s40, s56, s52\n"
"ds_read_b128 v[208:211], v228 offset:53248\n"
"v_mfma_f32_16x16x32_bf16 v[164:167], v[212:215], a[196:199], v[164:167]\n"
"s_waitcnt lgkmcnt(2)\n"
"buffer_load_dwordx4 v240, s[20:23], s40 offen offset:1024 nt lds\n"
"v_mfma_f32_16x16x32_bf16 a[216:219], v[124:127], a[244:247], a[216:219]\n"
"v_cvt_pk_bf16_f32 v142, v172, v173\n"
"v_cvt_pk_bf16_f32 v143, v174, v175\n"
"v_mfma_f32_16x16x32_bf16 v[8:11], v[128:131], a[244:247], v[8:11]\n"
"v_exp_f32 v176, v176\n"
"v_mfma_f32_16x16x32_bf16 v[8:11], v[132:135], a[248:251], v[8:11]\n"
"v_exp_f32 v155, v155\n"
"v_mfma_f32_16x16x32_bf16 v[40:43], v[136:139], a[244:247], v[40:43]\n"
"v_exp_f32 v152, v152\n"
"v_mfma_f32_16x16x32_bf16 v[40:43], v[140:143], a[248:251], v[40:43]\n"
"ds_read_b128 v[212:215], v229 offset:53248\n"
"v_cvt_pk_bf16_f32 v150, v176, v177\n"
"v_mfma_f32_16x16x32_bf16 v[72:75], v[144:147], a[244:247], v[72:75]\n"
"v_exp_f32 v158, v158\n"
"v_mfma_f32_16x16x32_bf16 v[72:75], v[148:151], a[248:251], v[72:75]\n"
"v_exp_f32 v163, v163\n"
"v_mfma_f32_16x16x32_bf16 v[76:79], v[148:151], v[224:227], v[76:79]\n"
"ds_read_b128 v[220:223], v229 offset:54272\n"
"v_mfma_f32_16x16x32_bf16 v[76:79], v[144:147], a[252:255], v[76:79]\n"
"v_exp_f32 v159, v159\n"
"v_mfma_f32_16x16x32_bf16 v[44:47], v[140:143], v[224:227], v[44:47]\n"
"s_add_u32 s40, s56, s53\n"
"ds_read_b64_tr_b16 a[244:245], v234 offset:33792\n"
"v_mfma_f32_16x16x32_bf16 v[44:47], v[136:139], a[252:255], v[44:47]\n"
"buffer_load_dwordx4 v240, s[20:23], s40 offen offset:2048 nt lds\n"
"v_mfma_f32_16x16x32_bf16 v[12:15], v[132:135], v[224:227], v[12:15]\n"
"v_exp_f32 v153, v153\n"
"v_mfma_f32_16x16x32_bf16 v[12:15], v[128:131], a[252:255], v[12:15]\n"
"v_exp_f32 v165, v165\n"
"v_mfma_f32_16x16x32_bf16 a[220:223], v[124:127], a[252:255], a[220:223]\n"
"v_exp_f32 v164, v164\n"
"v_smfmac_f32_16x16x64_bf16 v[196:199], a[240:243], v[128:135], v241\n"
"v_cvt_pk_bf16_f32 v97, v154, v155\n"
"v_mfma_f32_16x16x32_bf16 v[202:205], a[240:243], v[124:127], v[202:205]\n"
"s_waitcnt lgkmcnt(1)\n"
"ds_read_b64_tr_b16 a[254:255], v235 offset:37888\n"
"v_mfma_f32_16x16x32_bf16 v[168:171], v[216:219], a[8:11], v[180:183]\n"
"ds_read_b64_tr_b16 a[248:249], v238 offset:33792\n"
"v_mfma_f32_16x16x32_bf16 v[168:171], v[220:223], a[12:15], v[168:171]\n"
"ds_read_b64_tr_b16 a[246:247], v234 offset:37888\n"
"v_mfma_f32_16x16x32_bf16 v[168:171], v[208:211], a[0:3], v[168:171]\n"
"ds_read_b64_tr_b16 v[224:225], v239 offset:33792\n"
"v_cvt_pk_bf16_f32 v120, v164, v165\n"
"v_mfma_f32_16x16x32_bf16 v[168:171], v[212:215], a[4:7], v[168:171]\n"
"v_exp_f32 v166, v166\n"
"v_mfma_f32_16x16x32_bf16 v[172:175], v[208:211], a[16:19], v[184:187]\n"
"ds_read_b64_tr_b16 a[252:253], v235 offset:33792\n"
"v_cvt_pk_bf16_f32 v96, v152, v153\n"
"v_mfma_f32_16x16x32_bf16 v[172:175], v[212:215], a[20:23], v[172:175]\n"
"ds_read_b64_tr_b16 v[226:227], v239 offset:37888\n"
"v_cvt_pk_bf16_f32 v105, v158, v159\n"
"v_mfma_f32_16x16x32_bf16 v[172:175], v[216:219], a[24:27], v[172:175]\n"
"s_add_u32 s40, s56, s54\n"
"ds_read_b64_tr_b16 a[250:251], v238 offset:37888\n"
"v_mfma_f32_16x16x32_bf16 v[172:175], v[220:223], a[28:31], v[172:175]\n"
"buffer_load_dwordx4 v240, s[20:23], s40 offen offset:3072 nt lds\n"
"v_mfma_f32_16x16x32_bf16 v[176:179], v[216:219], a[40:43], v[188:191]\n"
"s_add_u32 s55, s55, s59\n"
"v_exp_f32 v167, v167\n"
"v_mfma_f32_16x16x32_bf16 v[176:179], v[220:223], a[44:47], v[176:179]\n"
"s_add_u32 s41, s55, s57\n"
"v_exp_f32 v161, v161\n"
"v_mfma_f32_16x16x32_bf16 v[176:179], v[208:211], a[32:35], v[176:179]\n"
"s_and_b32 s41, s41, s58\n"
"v_exp_f32 v157, v157\n"
"v_mfma_f32_16x16x32_bf16 v[176:179], v[212:215], a[36:39], v[176:179]\n"
"s_andn2_b32 s56, s55, s58\n"
"v_exp_f32 v160, v160\n"
"v_mfma_f32_16x16x32_bf16 v[152:155], v[208:211], a[192:195], v[192:195]\n"
"s_or_b32 s56, s56, s41\n"
"ds_read_b128 v[208:211], v230 offset:49152\n"
"v_mfma_f32_16x16x32_bf16 v[152:155], v[212:215], a[196:199], v[152:155]\n"
"v_exp_f32 v172, v172\n"
"v_mfma_f32_16x16x32_bf16 v[152:155], v[216:219], a[200:203], v[152:155]\n"
"ds_read_b128 v[216:219], v230 offset:50176\n"
"v_mfma_f32_16x16x32_bf16 v[152:155], v[220:223], a[204:207], v[152:155]\n"
"s_waitcnt lgkmcnt(2)\n"
"v_exp_f32 v162, v162\n"
"v_mfma_f32_16x16x32_bf16 a[232:235], v[124:127], a[244:247], a[232:235]\n"
"ds_read_b128 v[212:215], v231 offset:49152\n"
"v_mfma_f32_16x16x32_bf16 v[24:27], v[128:131], a[244:247], v[24:27]\n"
"s_add_u32 m0, s44, 16384\n"
"ds_read_b128 v[220:223], v231 offset:50176\n"
"v_mfma_f32_16x16x32_bf16 v[24:27], v[132:135], a[248:251], v[24:27]\n"
"buffer_load_dwordx4 v240, s[16:19], s56 offen nt lds\n"
"v_mfma_f32_16x16x32_bf16 v[56:59], v[136:139], a[244:247], v[56:59]\n"
"v_exp_f32 v171, v171\n"
"v_mfma_f32_16x16x32_bf16 v[56:59], v[140:143], a[248:251], v[56:59]\n"
"v_exp_f32 v169, v169\n"
"v_mfma_f32_16x16x32_bf16 v[88:91], v[144:147], a[244:247], v[88:91]\n"
"v_exp_f32 v154, v154\n"
"v_mfma_f32_16x16x32_bf16 v[88:91], v[148:151], a[248:251], v[88:91]\n"
"v_exp_f32 v156, v156\n"
"v_mfma_f32_16x16x32_bf16 v[92:95], v[148:151], v[224:227], v[92:95]\n"
"ds_read_b64_tr_b16 a[244:245], v232 offset:32768\n"
"v_cvt_pk_bf16_f32 v113, v162, v163\n"
"v_mfma_f32_16x16x32_bf16 v[92:95], v[144:147], a[252:255], v[92:95]\n"
"ds_read_b64_tr_b16 a[250:251], v236 offset:36864\n"
"v_cvt_pk_bf16_f32 v104, v156, v157\n"
"v_mfma_f32_16x16x32_bf16 v[60:63], v[140:143], v[224:227], v[60:63]\n"
"ds_read_b64_tr_b16 a[246:247], v232 offset:36864\n"
"v_cvt_pk_bf16_f32 v112, v160, v161\n"
"v_mfma_f32_16x16x32_bf16 v[60:63], v[136:139], a[252:255], v[60:63]\n"
"v_exp_f32 v152, v152\n"
"v_mfma_f32_16x16x32_bf16 v[28:31], v[132:135], v[224:227], v[28:31]\n"
"ds_read_b64_tr_b16 v[224:225], v237 offset:32768\n"
"ds_read_b64_tr_b16 v[226:227], v237 offset:36864\n"
"v_mfma_f32_16x16x32_bf16 v[28:31], v[128:131], a[252:255], v[28:31]\n"
"ds_read_b64_tr_b16 a[248:249], v236 offset:32768\n"
"v_cvt_pk_bf16_f32 v121, v166, v167\n"
"v_mfma_f32_16x16x32_bf16 a[236:239], v[124:127], a[252:255], a[236:239]\n"
"s_add_u32 s40, s56, s52\n"
"ds_read_b64_tr_b16 a[252:253], v233 offset:32768\n"
"v_smfmac_f32_16x16x64_bf16 v[198:201], a[240:243], v[136:143], v241\n"
"s_waitcnt lgkmcnt(7)\n"
"buffer_load_dwordx4 v240, s[16:19], s40 offen offset:1024 nt lds\n"
"v_mfma_f32_16x16x32_bf16 v[156:159], v[208:211], a[0:3], v[180:183]\n"
"v_exp_f32 v175, v175\n"
"v_mfma_f32_16x16x32_bf16 v[156:159], v[212:215], a[4:7], v[156:159]\n"
"v_exp_f32 v173, v173\n"
"v_mfma_f32_16x16x32_bf16 v[156:159], v[216:219], a[8:11], v[156:159]\n"
"v_exp_f32 v168, v168\n"
"v_mfma_f32_16x16x32_bf16 v[156:159], v[220:223], a[12:15], v[156:159]\n"
"ds_read_b64_tr_b16 a[254:255], v233 offset:36864\n"
"v_mfma_f32_16x16x32_bf16 v[160:163], v[216:219], a[24:27], v[184:187]\n"
"v_exp_f32 v170, v170\n"
"v_mfma_f32_16x16x32_bf16 v[160:163], v[220:223], a[28:31], v[160:163]\n"
"v_exp_f32 v178, v178\n"
"v_mfma_f32_16x16x32_bf16 v[160:163], v[208:211], a[16:19], v[160:163]\n"
"v_exp_f32 v177, v177\n"
"v_mfma_f32_16x16x32_bf16 v[160:163], v[212:215], a[20:23], v[160:163]\n"
"v_exp_f32 v176, v176\n"
"v_mfma_f32_16x16x32_bf16 v[164:167], v[208:211], a[32:35], v[188:191]\n"
"ds_read_b128 v[208:211], v230 offset:53248\n"
"v_cvt_pk_bf16_f32 v99, v170, v171\n"
"v_mfma_f32_16x16x32_bf16 v[164:167], v[212:215], a[36:39], v[164:167]\n"
"ds_read_b128 v[212:215], v231 offset:53248\n"
"v_mfma_f32_16x16x32_bf16 v[164:167], v[216:219], a[40:43], v[164:167]\n"
"v_exp_f32 v153, v153\n"
"v_mfma_f32_16x16x32_bf16 v[164:167], v[220:223], a[44:47], v[164:167]\n"
"s_waitcnt lgkmcnt(2)\n"
"ds_read_b128 v[220:223], v231 offset:54272\n"
"v_cvt_pk_bf16_f32 v106, v172, v173\n"
"v_mfma_f32_16x16x32_bf16 a[208:211], v[124:127], a[244:247], a[208:211]\n"
"v_exp_f32 v174, v174\n"
"v_mfma_f32_16x16x32_bf16 v[0:3], v[128:131], a[244:247], v[0:3]\n"
"s_add_u32 s40, s56, s53\n"
"ds_read_b128 v[216:219], v230 offset:54272\n"
"v_mfma_f32_16x16x32_bf16 v[0:3], v[132:135], a[248:251], v[0:3]\n"
"buffer_load_dwordx4 v240, s[16:19], s40 offen offset:2048 nt lds\n"
"v_mfma_f32_16x16x32_bf16 v[32:35], v[136:139], a[244:247], v[32:35]\n"
"v_exp_f32 v163, v163\n"
"v_mfma_f32_16x16x32_bf16 v[32:35], v[140:143], a[248:251], v[32:35]\n"
"v_exp_f32 v161, v161\n"
"v_mfma_f32_16x16x32_bf16 v[64:67], v[144:147], a[244:247], v[64:67]\n"
"v_exp_f32 v155, v155\n"
"v_mfma_f32_16x16x32_bf16 v[64:67], v[148:151], a[248:251], v[64:67]\n"
"v_exp_f32 v166, v166\n"
"v_mfma_f32_16x16x32_bf16 v[68:71], v[148:151], v[224:227], v[68:71]\n"
"ds_read_b64_tr_b16 a[250:251], v236 offset:37888\n"
"v_cvt_pk_bf16_f32 v107, v174, v175\n"
"v_mfma_f32_16x16x32_bf16 v[68:71], v[144:147], a[252:255], v[68:71]\n"
"ds_read_b64_tr_b16 a[246:247], v232 offset:37888\n"
"v_cvt_pk_bf16_f32 v98, v168, v169\n"
"v_mfma_f32_16x16x32_bf16 v[36:39], v[140:143], v[224:227], v[36:39]\n"
"v_exp_f32 v179, v179\n"
"v_mfma_f32_16x16x32_bf16 v[36:39], v[136:139], a[252:255], v[36:39]\n"
"ds_read_b64_tr_b16 a[244:245], v232 offset:33792\n"
"v_cvt_pk_bf16_f32 v122, v152, v153\n"
"v_mfma_f32_16x16x32_bf16 v[4:7], v[132:135], v[224:227], v[4:7]\n"
"s_add_u32 s40, s56, s54\n"
"ds_read_b64_tr_b16 a[248:249], v236 offset:33792\n"
"v_mfma_f32_16x16x32_bf16 v[4:7], v[128:131], a[252:255], v[4:7]\n"
"buffer_load_dwordx4 v240, s[16:19], s40 offen offset:3072 nt lds\n"
"v_mfma_f32_16x16x32_bf16 a[212:215], v[124:127], a[252:255], a[212:215]\n"
"v_exp_f32 v162, v162\n"
"v_smfmac_f32_16x16x64_bf16 v[200:203], a[240:243], v[144:151], v241\n"
"s_waitcnt lgkmcnt(4)\n"
"v_cvt_pk_bf16_f32 v114, v176, v177\n"
"v_mfma_f32_16x16x32_bf16 v[168:171], v[208:211], a[0:3], v[180:183]\n"
"v_exp_f32 v157, v157\n"
"v_mfma_f32_16x16x32_bf16 v[168:171], v[212:215], a[4:7], v[168:171]\n"
"v_exp_f32 v165, v165\n"
"v_mfma_f32_16x16x32_bf16 v[168:171], v[216:219], a[8:11], v[168:171]\n"
"ds_read_b64_tr_b16 a[254:255], v233 offset:37888\n"
"ds_read_b64_tr_b16 v[226:227], v237 offset:37888\n"
"v_mfma_f32_16x16x32_bf16 v[168:171], v[220:223], a[12:15], v[168:171]\n"
"ds_read_b64_tr_b16 a[252:253], v233 offset:33792\n"
"v_cvt_pk_bf16_f32 v115, v178, v179\n"
"v_mfma_f32_16x16x32_bf16 v[172:175], v[216:219], a[24:27], v[184:187]\n"
"ds_read_b64_tr_b16 v[224:225], v237 offset:33792\n"
"v_mfma_f32_16x16x32_bf16 v[172:175], v[220:223], a[28:31], v[172:175]\n"
"v_exp_f32 v160, v160\n"
"v_mfma_f32_16x16x32_bf16 v[172:175], v[208:211], a[16:19], v[172:175]\n"
"v_exp_f32 v158, v158\n"
"v_mfma_f32_16x16x32_bf16 v[172:175], v[212:215], a[20:23], v[172:175]\n"
"s_cbranch_vccnz LPCL3_%=\nLPKL3_%=:\n"
"v_mfma_f32_16x16x32_bf16 v[176:179], v[208:211], a[32:35], v[188:191]\n"
"s_add_u32 s47, s47, 1\n"
"v_exp_f32 v159, v159\n"
"v_mfma_f32_16x16x32_bf16 v[176:179], v[212:215], a[36:39], v[176:179]\n"
"v_exp_f32 v167, v167\n"
"v_mfma_f32_16x16x32_bf16 v[176:179], v[216:219], a[40:43], v[176:179]\n"
"s_and_b32 s40, s47, 3\n"
"v_exp_f32 v164, v164\n"
"v_mfma_f32_16x16x32_bf16 v[176:179], v[220:223], a[44:47], v[176:179]\n"
"s_waitcnt lgkmcnt(1)\n"
"s_cbranch_scc1 LAL_%=\n"
"s_cmp_eq_u32 s47, 8\n"
"s_cbranch_scc1 LTH1_%=\n"
"s_cmp_eq_u32 s47, 12\n"
"s_cbranch_scc1 LTH2_%=\n"
"s_cmp_eq_u32 s47, 16\n"
"s_cbranch_scc1 LAX_%=\n"
"LTH0_%=:\n"
"v_mfma_f32_16x16x32_bf16 a[224:227], v[124:127], a[244:247], a[224:227]\n"
"v_exp_f32 v168, v168\n"
"v_mfma_f32_16x16x32_bf16 v[16:19], v[128:131], a[244:247], v[16:19]\n"
"s_waitcnt vmcnt(8) lgkmcnt(0)\n"
"v_exp_f32 v169, v169\n"
"v_mfma_f32_16x16x32_bf16 v[16:19], v[132:135], a[248:251], v[16:19]\n"
"s_barrier\n"
"v_mfma_f32_16x16x32_bf16 v[48:51], v[136:139], a[244:247], v[48:51]\n"
"v_exp_f32 v172, v172\n"
"v_mfma_f32_16x16x32_bf16 v[48:51], v[140:143], a[248:251], v[48:51]\n"
"ds_read_b128 v[216:219], v228 offset:1024\n"
"v_cvt_pk_bf16_f32 v102, v168, v169\n"
"v_mfma_f32_16x16x32_bf16 v[80:83], v[144:147], a[244:247], v[80:83]\n"
"v_exp_f32 v176, v176\n"
"v_mfma_f32_16x16x32_bf16 v[80:83], v[148:151], a[248:251], v[80:83]\n"
"ds_read_b64_tr_b16 a[244:245], v234 offset:49152\n"
"ds_read_b64_tr_b16 a[246:247], v234 offset:53248\n"
"v_cvt_pk_bf16_f32 v123, v154, v155\n"
"v_cvt_pk_bf16_f32 v117, v166, v167\n"
"v_exp_f32 v170, v170\n"
"v_exp_f32 v175, v175\n"
"v_exp_f32 v177, v177\n"
"v_mfma_f32_16x16x32_bf16 v[84:87], v[148:151], v[224:227], v[84:87]\n"
"v_exp_f32 v156, v156\n"
"v_mfma_f32_16x16x32_bf16 v[84:87], v[144:147], a[252:255], v[84:87]\n"
"ds_read_b64_tr_b16 a[248:249], v238 offset:49152\n"
"ds_read_b64_tr_b16 a[250:251], v238 offset:53248\n"
"v_mfma_f32_16x16x32_bf16 v[52:55], v[140:143], v[224:227], v[52:55]\n"
"v_cvt_pk_bf16_f32 v101, v158, v159\n"
"v_cvt_pk_bf16_f32 v118, v176, v177\n"
"v_mfma_f32_16x16x32_bf16 v[52:55], v[136:139], a[252:255], v[52:55]\n"
"v_cvt_pk_bf16_f32 v100, v156, v157\n"
"v_exp_f32 v173, v173\n"
"v_exp_f32 v174, v174\n"
"v_exp_f32 v178, v178\n"
"v_exp_f32 v179, v179\n"
"v_mfma_f32_16x16x32_bf16 v[20:23], v[132:135], v[224:227], v[20:23]\n"
"v_exp_f32 v171, v171\n"
"v_mfma_f32_16x16x32_bf16 v[20:23], v[128:131], a[252:255], v[20:23]\n"
"ds_read_b64_tr_b16 v[224:225], v239 offset:49152\n"
"v_cvt_pk_bf16_f32 v103, v170, v171\n"
"v_mfma_f32_16x16x32_bf16 a[228:231], v[124:127], a[252:255], a[228:231]\n"
"s_waitcnt lgkmcnt(3)\n"
"ds_read_b64_tr_b16 a[252:253], v235 offset:49152\n"
"v_cvt_pk_bf16_f32 v119, v178, v179\n"
"v_mfma_f32_16x16x32_bf16 a[216:219], v[120:123], a[244:247], a[216:219]\n"
"s_add_u32 m0, s44, 81920\n"
"ds_read_b64_tr_b16 a[254:255], v235 offset:53248\n"
"ds_read_b64_tr_b16 v[226:227], v239 offset:53248\n"
"v_smfmac_f32_16x16x64_bf16 v[196:199], a[240:243], v[96:103], v241\n"
"buffer_load_dwordx4 v240, s[20:23], s56 offen nt lds\n"
"v_mfma_f32_16x16x32_bf16 v[8:11], v[96:99], a[244:247], v[8:11]\n"
"s_waitcnt lgkmcnt(4)\n"
"v_cvt_pk_bf16_f32 v109, v162, v163\n"
"v_cvt_pk_bf16_f32 v116, v164, v165\n"
"v_mfma_f32_16x16x32_bf16 v[8:11], v[100:103], a[248:251], v[8:11]\n"
"v_cvt_pk_bf16_f32 v110, v172, v173\n"
"v_cvt_pk_bf16_f32 v111, v174, v175\n"
"v_smfmac_f32_16x16x64_bf16 v[200:203], a[240:243], v[112:119], v241\n"
"ds_read_b128 v[220:223], v229 offset:1024\n"
"v_cvt_pk_bf16_f32 v108, v160, v161\n"
"v_mfma_f32_16x16x32_bf16 v[40:43], v[104:107], a[244:247], v[40:43]\n"
"v_add_f32 v196, v196, v197\n"
"v_and_b32 v245, 0x3f0, v242\n"
"v_mfma_f32_16x16x32_bf16 v[40:43], v[108:111], a[248:251], v[40:43]\n"
"ds_read_b128 v[212:215], v229 offset:0\n"
"v_lshrrev_b32 v245, 1, v245\n"
"v_smfmac_f32_16x16x64_bf16 v[198:201], a[240:243], v[104:111], v241\n"
"v_lshlrev_b32_e64 v244, 24, s43\n"
"v_sub_u32 v245, v242, v245\n"
"v_mfma_f32_16x16x32_bf16 v[72:75], v[112:115], a[244:247], v[72:75]\n"
"ds_read_b64_tr_b16 a[244:245], v234 offset:50176\n"
"ds_read_b64_tr_b16 a[246:247], v234 offset:54272\n"
"v_mfma_f32_16x16x32_bf16 v[72:75], v[116:119], a[248:251], v[72:75]\n"
"ds_read_b128 v[208:211], v228 offset:0\n"
"v_mul_f32 v244, 0x45800000, v244\n"
"v_mfma_f32_16x16x32_bf16 v[202:205], a[240:243], v[120:123], v[202:205]\n"
"s_waitcnt lgkmcnt(5)\n"
"ds_read_b64_tr_b16 a[250:251], v238 offset:54272\n"
"v_add_f32 v198, v198, v199\n"
"v_mfma_f32_16x16x32_bf16 v[76:79], v[116:119], v[224:227], v[76:79]\n"
"v_add_f32 v200, v200, v201\n"
"v_add_f32 v254, v196, v198\n"
"v_mfma_f32_16x16x32_bf16 v[76:79], v[112:115], a[252:255], v[76:79]\n"
"ds_read_b64_tr_b16 a[248:249], v238 offset:50176\n"
"v_add_f32 v254, v254, v200\n"
"v_mfma_f32_16x16x32_bf16 v[44:47], v[108:111], v[224:227], v[44:47]\n"
"v_add_f32 v244, v244, v202\n"
"v_min3_f32 v244, v244, v196, v198\n"
"v_mfma_f32_16x16x32_bf16 v[44:47], v[104:107], a[252:255], v[44:47]\n"
"v_min_f32 v244, v244, v200\n"
"v_rcp_f32 v244, v244\n"
"v_mfma_f32_16x16x32_bf16 v[12:15], v[100:103], v[224:227], v[12:15]\n"
"s_add_u32 s40, s56, s52\n"
"v_add_f32 v254, v254, v202\n"
"v_fmac_f32 v254, 0x68800000, v244\n"
"v_mfma_f32_16x16x32_bf16 v[12:15], v[96:99], a[252:255], v[12:15]\n"
"buffer_load_dwordx4 v240, s[20:23], s40 offen offset:1024 nt lds\n"
"v_mfma_f32_16x16x32_bf16 a[220:223], v[120:123], a[252:255], a[220:223]\n"
"s_waitcnt lgkmcnt(2)\n"
"v_cmp_nge_f32 vcc, s45, v254\n"
"s_cbranch_vccnz LSLOW_%=\nLBACK0_%=:\ns_mov_b64 vcc, s[64:65]\n"
"v_mfma_f32_16x16x32_bf16 a[232:235], v[120:123], a[244:247], a[232:235]\n"
"v_rcp_f32 v246, v196\n"
"v_rcp_f32 v206, v200\n"
"v_mov_b32 v252, v192\n"
"v_accvgpr_mov_b32 a9, a57\n"
"v_accvgpr_mov_b32 a10, a58\n"
"v_accvgpr_mov_b32 a28, a76\n"
"v_accvgpr_mov_b32 a31, a79\n"
"v_accvgpr_mov_b32 a33, a81\n"
"v_accvgpr_mov_b32 a40, a88\n"
"v_bfe_u32 v149, v242, 8, 2\n"
"v_lshl_add_u32 v149, v149, 4, s43\n"
"v_xor_b32 v150, 64, v149\n"
"v_mfma_f32_16x16x32_bf16 v[24:27], v[96:99], a[244:247], v[24:27]\n"
"s_waitcnt lgkmcnt(0)\n"
"ds_read_b128 v[128:131], v150 offset:0\n"
"v_accvgpr_mov_b32 a12, a60\n"
"v_mfma_f32_16x16x32_bf16 v[24:27], v[100:103], a[248:251], v[24:27]\n"
"ds_read_b128 v[124:127], v149 offset:0\n"
"v_accvgpr_mov_b32 a0, a48\n"
"v_accvgpr_mov_b32 a3, a51\n"
"v_accvgpr_mov_b32 a4, a52\n"
"v_accvgpr_mov_b32 a18, a66\n"
"v_accvgpr_mov_b32 a21, a69\n"
"v_accvgpr_mov_b32 a35, a83\n"
"v_accvgpr_mov_b32 a38, a86\n"
"v_accvgpr_mov_b32 a47, a95\n"
"v_mfma_f32_16x16x32_bf16 v[56:59], v[104:107], a[244:247], v[56:59]\n"
"ds_read_b128 v[132:135], v149 offset:1024\n"
"v_rcp_f32 v247, v198\n"
"v_accvgpr_mov_b32 a1, a49\n"
"v_accvgpr_mov_b32 a2, a50\n"
"v_accvgpr_mov_b32 a11, a59\n"
"v_accvgpr_mov_b32 a13, a61\n"
"v_accvgpr_mov_b32 a14, a62\n"
"v_accvgpr_mov_b32 a16, a64\n"
"v_accvgpr_mov_b32 a20, a68\n"
"v_accvgpr_mov_b32 a22, a70\n"
"v_accvgpr_mov_b32 a25, a73\n"
"v_accvgpr_mov_b32 a36, a84\n"
"v_xor_b32 v148, 0x400, v243\n"
"v_mfma_f32_16x16x32_bf16 v[56:59], v[108:111], a[248:251], v[56:59]\n"
"ds_read_b128 v[136:139], v150 offset:1024\n"
"v_mov_b32_dpp v246, v246 row_ror:12 row_mask:0x2 bank_mask:0xf\n"
"v_accvgpr_mov_b32 a6, a54\n"
"v_accvgpr_mov_b32 a7, a55\n"
"v_accvgpr_mov_b32 a8, a56\n"
"v_accvgpr_mov_b32 a24, a72\n"
"v_accvgpr_mov_b32 a29, a77\n"
"v_accvgpr_mov_b32 a34, a82\n"
"v_accvgpr_mov_b32 a39, a87\n"
"v_accvgpr_mov_b32 a43, a91\n"
"v_accvgpr_mov_b32 a45, a93\n"
"v_mfma_f32_16x16x32_bf16 v[88:91], v[112:115], a[244:247], v[88:91]\n"
"v_accvgpr_mov_b32 a19, a67\n"
"v_mfma_f32_16x16x32_bf16 v[88:91], v[116:119], a[248:251], v[88:91]\n"
"ds_read_b128 a[192:195], v243 offset:4096\n"
"v_accvgpr_mov_b32 a5, a53\n"
"v_accvgpr_mov_b32 a17, a65\n"
"v_accvgpr_mov_b32 a23, a71\n"
"v_accvgpr_mov_b32 a26, a74\n"
"v_accvgpr_mov_b32 a27, a75\n"
"v_accvgpr_mov_b32 a37, a85\n"
"v_accvgpr_mov_b32 a46, a94\n"
"s_waitcnt lgkmcnt(3)\n"
"v_mfma_f32_16x16x32_bf16 v[180:183], v[124:127], a[0:3], 0\n"
"ds_read_b128 a[196:199], v148 offset:4096\n"
"v_accvgpr_mov_b32 a15, a63\n"
"v_mfma_f32_16x16x32_bf16 v[180:183], v[128:131], a[4:7], v[180:183]\n"
"s_waitcnt lgkmcnt(3)\n"
"v_mfma_f32_16x16x32_bf16 v[180:183], v[132:135], a[8:11], v[180:183]\n"
"v_accvgpr_mov_b32 a41, a89\n"
"s_waitcnt lgkmcnt(2)\n"
"v_mfma_f32_16x16x32_bf16 v[180:183], v[136:139], a[12:15], v[180:183]\n"
"ds_read_b128 a[204:207], v148 offset:6144\n"
"v_mov_b32_dpp v246, v246 row_ror:8 row_mask:0x4 bank_mask:0xf\n"
"v_mfma_f32_16x16x32_bf16 v[184:187], v[124:127], a[16:19], 0\n"
"v_accvgpr_mov_b32 a32, a80\n"
"v_mfma_f32_16x16x32_bf16 v[184:187], v[128:131], a[20:23], v[184:187]\n"
"ds_read_b128 a[200:203], v243 offset:6144\n"
"v_accvgpr_mov_b32 a30, a78\n"
"v_mfma_f32_16x16x32_bf16 v[184:187], v[132:135], a[24:27], v[184:187]\n"
"s_add_u32 s40, s56, s53\n"
"v_sub_f32 v181, s74, v181\n"
"v_mfma_f32_16x16x32_bf16 v[184:187], v[136:139], a[28:31], v[184:187]\n"
"buffer_load_dwordx4 v240, s[20:23], s40 offen offset:2048 nt lds\n"
"v_mov_b32_dpp v247, v247 row_ror:12 row_mask:0x2 bank_mask:0xf\n"
"v_mov_b32_dpp v246, v246 row_ror:4 row_mask:0x8 bank_mask:0xf\n"
"v_accvgpr_mov_b32 a42, a90\n"
"v_sub_f32 v180, s74, v180\n"
"v_sub_f32 v183, s74, v183\n"
"v_mfma_f32_16x16x32_bf16 v[188:191], v[124:127], a[32:35], 0\n"
"v_mfma_f32_16x16x32_bf16 v[188:191], v[128:131], a[36:39], v[188:191]\n"
"v_accvgpr_mov_b32 a44, a92\n"
"v_mfma_f32_16x16x32_bf16 v[188:191], v[132:135], a[40:43], v[188:191]\n"
"v_sub_f32 v187, s74, v187\n"
"v_mfma_f32_16x16x32_bf16 v[188:191], v[136:139], a[44:47], v[188:191]\n"
"s_waitcnt lgkmcnt(1)\n"
"ds_read_b64_tr_b16 a[250:251], v236 offset:53248\n"
"v_sub_f32 v186, s74, v186\n"
"v_mfma_f32_16x16x32_bf16 v[192:195], v[124:127], a[192:195], 0\n"
"v_mfma_f32_16x16x32_bf16 v[192:195], v[128:131], a[196:199], v[192:195]\n"
"v_mul_f32_dpp v14, v246, v14 row_newbcast:2 row_mask:0xf bank_mask:0xf\n"
"s_waitcnt lgkmcnt(1)\n"
"v_mfma_f32_16x16x32_bf16 v[192:195], v[132:135], a[200:203], v[192:195]\n"
"ds_read_b64_tr_b16 a[252:253], v235 offset:50176\n"
"v_sub_f32 v182, s74, v182\n"
"v_mfma_f32_16x16x32_bf16 v[192:195], v[136:139], a[204:207], v[192:195]\n"
"ds_read_b64_tr_b16 v[226:227], v239 offset:54272\n"
"ds_read_b64_tr_b16 a[248:249], v236 offset:49152\n"
"v_xor_b32 v244, 0x11000, v242\n"
"v_mul_f32_dpp v12, v246, v12 row_newbcast:0 row_mask:0xf bank_mask:0xf\n"
"v_sub_f32 v185, s74, v185\n"
"v_sub_f32 v188, s74, v188\n"
"v_sub_f32 v189, s74, v189\n"
"v_sub_f32 v190, s74, v190\n"
"v_mul_f32_dpp v27, v246, v27 row_newbcast:3 row_mask:0xf bank_mask:0xf\n"
"v_mfma_f32_16x16x32_bf16 v[152:155], v[208:211], a[0:3], v[180:183]\n"
"v_mul_f32_dpp v10, v246, v10 row_newbcast:2 row_mask:0xf bank_mask:0xf\n"
"v_mfma_f32_16x16x32_bf16 v[152:155], v[212:215], a[4:7], v[152:155]\n"
"v_sub_f32 v184, s74, v184\n"
"v_mfma_f32_16x16x32_bf16 v[152:155], v[216:219], a[8:11], v[152:155]\n"
"s_add_u32 s40, s56, s54\n"
"v_sub_f32 v194, s74, v194\n"
"v_mfma_f32_16x16x32_bf16 v[152:155], v[220:223], a[12:15], v[152:155]\n"
"buffer_load_dwordx4 v240, s[20:23], s40 offen offset:3072 nt lds\n"
"v_mov_b32_dpp v206, v206 row_ror:12 row_mask:0x2 bank_mask:0xf\n"
"v_sub_f32 v191, s74, v191\n"
"v_sub_f32 v192, s74, v192\n"
"v_sub_f32 v193, s74, v193\n"
"v_sub_f32 v195, s74, v195\n"
"v_mfma_f32_16x16x32_bf16 v[156:159], v[216:219], a[24:27], v[184:187]\n"
"s_add_u32 s55, s55, s59\n"
"v_mov_b32_dpp v206, v206 row_ror:8 row_mask:0x4 bank_mask:0xf\n"
"v_mfma_f32_16x16x32_bf16 v[156:159], v[220:223], a[28:31], v[156:159]\n"
"s_add_u32 s41, s55, s57\n"
"v_mfma_f32_16x16x32_bf16 v[156:159], v[208:211], a[16:19], v[156:159]\n"
"s_and_b32 s41, s41, s58\n"
"ds_read_b64_tr_b16 v[224:225], v239 offset:50176\n"
"v_mov_b32_dpp v247, v247 row_ror:8 row_mask:0x4 bank_mask:0xf\n"
"v_mfma_f32_16x16x32_bf16 v[156:159], v[212:215], a[20:23], v[156:159]\n"
"s_andn2_b32 s56, s55, s58\n"
"v_mfma_f32_16x16x32_bf16 v[160:163], v[208:211], a[32:35], v[188:191]\n"
"s_or_b32 s56, s56, s41\n"
"ds_read_b64_tr_b16 a[254:255], v235 offset:54272\n"
"v_mfma_f32_16x16x32_bf16 v[160:163], v[212:215], a[36:39], v[160:163]\n"
"v_xor_b32 v250, 0x1000, v245\n"
"v_mfma_f32_16x16x32_bf16 v[160:163], v[216:219], a[40:43], v[160:163]\n"
"ds_read_b64_tr_b16 a[244:245], v232 offset:49152\n"
"v_mfma_f32_16x16x32_bf16 v[160:163], v[220:223], a[44:47], v[160:163]\n"
"v_mul_f32_dpp v25, v246, v25 row_newbcast:1 row_mask:0xf bank_mask:0xf\n"
"v_mfma_f32_16x16x32_bf16 v[164:167], v[216:219], a[200:203], v[192:195]\n"
"ds_read_b128 v[216:219], v228 offset:5120\n"
"v_mfma_f32_16x16x32_bf16 v[164:167], v[220:223], a[204:207], v[164:167]\n"
"v_mfma_f32_16x16x32_bf16 v[164:167], v[208:211], a[192:195], v[164:167]\n"
"ds_read_b128 v[220:223], v229 offset:5120\n"
"v_mfma_f32_16x16x32_bf16 v[164:167], v[212:215], a[196:199], v[164:167]\n"
"ds_read_b128 v[208:211], v228 offset:4096\n"
"v_mov_b32_dpp v206, v206 row_ror:4 row_mask:0x8 bank_mask:0xf\n"
"v_exp_f32 v161, v161\n"
"v_exp_f32 v162, v162\n"
"s_branch LTC_%=\n"
"LTH1_%=:\n"
"v_mfma_f32_16x16x32_bf16 a[224:227], v[124:127], a[244:247], a[224:227]\n"
"v_exp_f32 v168, v168\n"
"v_mfma_f32_16x16x32_bf16 v[16:19], v[128:131], a[244:247], v[16:19]\n"
"s_waitcnt vmcnt(8) lgkmcnt(0)\n"
"v_exp_f32 v169, v169\n"
"v_mfma_f32_16x16x32_bf16 v[16:19], v[132:135], a[248:251], v[16:19]\n"
"s_barrier\n"
"v_mfma_f32_16x16x32_bf16 v[48:51], v[136:139], a[244:247], v[48:51]\n"
"v_exp_f32 v172, v172\n"
"v_mfma_f32_16x16x32_bf16 v[48:51], v[140:143], a[248:251], v[48:51]\n"
"ds_read_b128 v[216:219], v228 offset:1024\n"
"v_cvt_pk_bf16_f32 v102, v168, v169\n"
"v_mfma_f32_16x16x32_bf16 v[80:83], v[144:147], a[244:247], v[80:83]\n"
"v_exp_f32 v176, v176\n"
"v_mfma_f32_16x16x32_bf16 v[80:83], v[148:151], a[248:251], v[80:83]\n"
"ds_read_b64_tr_b16 a[244:245], v234 offset:49152\n"
"ds_read_b64_tr_b16 a[246:247], v234 offset:53248\n"
"v_cvt_pk_bf16_f32 v123, v154, v155\n"
"v_cvt_pk_bf16_f32 v117, v166, v167\n"
"v_exp_f32 v170, v170\n"
"v_exp_f32 v175, v175\n"
"v_exp_f32 v177, v177\n"
"v_mfma_f32_16x16x32_bf16 v[84:87], v[148:151], v[224:227], v[84:87]\n"
"v_exp_f32 v156, v156\n"
"v_mfma_f32_16x16x32_bf16 v[84:87], v[144:147], a[252:255], v[84:87]\n"
"ds_read_b64_tr_b16 a[248:249], v238 offset:49152\n"
"ds_read_b64_tr_b16 a[250:251], v238 offset:53248\n"
"v_mfma_f32_16x16x32_bf16 v[52:55], v[140:143], v[224:227], v[52:55]\n"
"v_cvt_pk_bf16_f32 v101, v158, v159\n"
"v_cvt_pk_bf16_f32 v118, v176, v177\n"
"v_mfma_f32_16x16x32_bf16 v[52:55], v[136:139], a[252:255], v[52:55]\n"
"v_cvt_pk_bf16_f32 v100, v156, v157\n"
"v_exp_f32 v173, v173\n"
"v_exp_f32 v174, v174\n"
"v_exp_f32 v178, v178\n"
"v_exp_f32 v179, v179\n"
"v_mfma_f32_16x16x32_bf16 v[20:23], v[132:135], v[224:227], v[20:23]\n"
"v_exp_f32 v171, v171\n"
"v_mfma_f32_16x16x32_bf16 v[20:23], v[128:131], a[252:255], v[20:23]\n"
"ds_read_b64_tr_b16 v[224:225], v239 offset:49152\n"
"v_cvt_pk_bf16_f32 v103, v170, v171\n"
"v_mfma_f32_16x16x32_bf16 a[228:231], v[124:127], a[252:255], a[228:231]\n"
"s_waitcnt lgkmcnt(3)\n"
"ds_read_b64_tr_b16 a[252:253], v235 offset:49152\n"
"v_cvt_pk_bf16_f32 v119, v178, v179\n"
"v_mfma_f32_16x16x32_bf16 a[216:219], v[120:123], a[244:247], a[216:219]\n"
"s_add_u32 m0, s44, 81920\n"
"ds_read_b64_tr_b16 a[254:255], v235 offset:53248\n"
"ds_read_b64_tr_b16 v[226:227], v239 offset:53248\n"
"v_smfmac_f32_16x16x64_bf16 v[196:199], a[240:243], v[96:103], v241\n"
"buffer_load_dwordx4 v240, s[20:23], s56 offen nt lds\n"
"v_mfma_f32_16x16x32_bf16 v[8:11], v[96:99], a[244:247], v[8:11]\n"
"s_waitcnt lgkmcnt(4)\n"
"v_cvt_pk_bf16_f32 v109, v162, v163\n"
"v_cvt_pk_bf16_f32 v116, v164, v165\n"
"v_mfma_f32_16x16x32_bf16 v[8:11], v[100:103], a[248:251], v[8:11]\n"
"v_cvt_pk_bf16_f32 v110, v172, v173\n"
"v_cvt_pk_bf16_f32 v111, v174, v175\n"
"v_smfmac_f32_16x16x64_bf16 v[200:203], a[240:243], v[112:119], v241\n"
"ds_read_b128 v[220:223], v229 offset:1024\n"
"v_cvt_pk_bf16_f32 v108, v160, v161\n"
"v_mfma_f32_16x16x32_bf16 v[40:43], v[104:107], a[244:247], v[40:43]\n"
"v_add_f32 v196, v196, v197\n"
"v_and_b32 v245, 0x3f0, v242\n"
"v_mfma_f32_16x16x32_bf16 v[40:43], v[108:111], a[248:251], v[40:43]\n"
"ds_read_b128 v[212:215], v229 offset:0\n"
"v_lshrrev_b32 v245, 1, v245\n"
"v_smfmac_f32_16x16x64_bf16 v[198:201], a[240:243], v[104:111], v241\n"
"v_lshlrev_b32_e64 v244, 24, s43\n"
"v_sub_u32 v245, v242, v245\n"
"v_mfma_f32_16x16x32_bf16 v[72:75], v[112:115], a[244:247], v[72:75]\n"
"ds_read_b64_tr_b16 a[244:245], v234 offset:50176\n"
"ds_read_b64_tr_b16 a[246:247], v234 offset:54272\n"
"v_mfma_f32_16x16x32_bf16 v[72:75], v[116:119], a[248:251], v[72:75]\n"
"ds_read_b128 v[208:211], v228 offset:0\n"
"v_mul_f32 v244, 0x45800000, v244\n"
"v_mfma_f32_16x16x32_bf16 v[202:205], a[240:243], v[120:123], v[202:205]\n"
"s_waitcnt lgkmcnt(5)\n"
"ds_read_b64_tr_b16 a[250:251], v238 offset:54272\n"
"v_add_f32 v198, v198, v199\n"
"v_mfma_f32_16x16x32_bf16 v[76:79], v[116:119], v[224:227], v[76:79]\n"
"v_add_f32 v200, v200, v201\n"
"v_add_f32 v254, v196, v198\n"
"v_mfma_f32_16x16x32_bf16 v[76:79], v[112:115], a[252:255], v[76:79]\n"
"ds_read_b64_tr_b16 a[248:249], v238 offset:50176\n"
"v_add_f32 v254, v254, v200\n"
"v_mfma_f32_16x16x32_bf16 v[44:47], v[108:111], v[224:227], v[44:47]\n"
"v_add_f32 v244, v244, v202\n"
"v_min3_f32 v244, v244, v196, v198\n"
"v_mfma_f32_16x16x32_bf16 v[44:47], v[104:107], a[252:255], v[44:47]\n"
"v_min_f32 v244, v244, v200\n"
"v_rcp_f32 v244, v244\n"
"v_mfma_f32_16x16x32_bf16 v[12:15], v[100:103], v[224:227], v[12:15]\n"
"s_add_u32 s40, s56, s52\n"
"v_add_f32 v254, v254, v202\n"
"v_fmac_f32 v254, 0x68800000, v244\n"
"v_mfma_f32_16x16x32_bf16 v[12:15], v[96:99], a[252:255], v[12:15]\n"
"buffer_load_dwordx4 v240, s[20:23], s40 offen offset:1024 nt lds\n"
"v_mfma_f32_16x16x32_bf16 a[220:223], v[120:123], a[252:255], a[220:223]\n"
"s_waitcnt lgkmcnt(2)\n"
"v_cmp_nge_f32 vcc, s45, v254\n"
"s_cbranch_vccnz LSLOW_%=\nLBACK1_%=:\ns_mov_b64 vcc, s[64:65]\n"
"v_mfma_f32_16x16x32_bf16 a[232:235], v[120:123], a[244:247], a[232:235]\n"
"v_rcp_f32 v246, v196\n"
"v_rcp_f32 v206, v200\n"
"v_mov_b32 v252, v192\n"
"v_accvgpr_mov_b32 a9, a105\n"
"v_accvgpr_mov_b32 a10, a106\n"
"v_accvgpr_mov_b32 a28, a124\n"
"v_accvgpr_mov_b32 a31, a127\n"
"v_accvgpr_mov_b32 a33, a129\n"
"v_accvgpr_mov_b32 a40, a136\n"
"v_bfe_u32 v149, v242, 8, 2\n"
"v_lshl_add_u32 v149, v149, 4, s43\n"
"v_xor_b32 v150, 64, v149\n"
"v_accvgpr_mov_b32 a105, a57\n"
"v_mfma_f32_16x16x32_bf16 v[24:27], v[96:99], a[244:247], v[24:27]\n"
"s_waitcnt lgkmcnt(0)\n"
"ds_read_b128 v[128:131], v150 offset:0\n"
"v_accvgpr_mov_b32 a12, a108\n"
"v_mfma_f32_16x16x32_bf16 v[24:27], v[100:103], a[248:251], v[24:27]\n"
"ds_read_b128 v[124:127], v149 offset:0\n"
"v_accvgpr_mov_b32 a0, a96\n"
"v_accvgpr_mov_b32 a3, a99\n"
"v_accvgpr_mov_b32 a4, a100\n"
"v_accvgpr_mov_b32 a18, a114\n"
"v_accvgpr_mov_b32 a21, a117\n"
"v_accvgpr_mov_b32 a35, a131\n"
"v_accvgpr_mov_b32 a38, a134\n"
"v_accvgpr_mov_b32 a47, a143\n"
"v_accvgpr_mov_b32 a99, a51\n"
"v_mfma_f32_16x16x32_bf16 v[56:59], v[104:107], a[244:247], v[56:59]\n"
"ds_read_b128 v[132:135], v149 offset:1024\n"
"v_rcp_f32 v247, v198\n"
"v_accvgpr_mov_b32 a1, a97\n"
"v_accvgpr_mov_b32 a2, a98\n"
"v_accvgpr_mov_b32 a11, a107\n"
"v_accvgpr_mov_b32 a13, a109\n"
"v_accvgpr_mov_b32 a14, a110\n"
"v_accvgpr_mov_b32 a16, a112\n"
"v_accvgpr_mov_b32 a20, a116\n"
"v_accvgpr_mov_b32 a22, a118\n"
"v_accvgpr_mov_b32 a25, a121\n"
"v_accvgpr_mov_b32 a36, a132\n"
"v_xor_b32 v148, 0x400, v243\n"
"v_mfma_f32_16x16x32_bf16 v[56:59], v[108:111], a[248:251], v[56:59]\n"
"ds_read_b128 v[136:139], v150 offset:1024\n"
"v_mov_b32_dpp v246, v246 row_ror:12 row_mask:0x2 bank_mask:0xf\n"
"v_accvgpr_mov_b32 a6, a102\n"
"v_accvgpr_mov_b32 a7, a103\n"
"v_accvgpr_mov_b32 a8, a104\n"
"v_accvgpr_mov_b32 a24, a120\n"
"v_accvgpr_mov_b32 a29, a125\n"
"v_accvgpr_mov_b32 a34, a130\n"
"v_accvgpr_mov_b32 a39, a135\n"
"v_accvgpr_mov_b32 a43, a139\n"
"v_accvgpr_mov_b32 a45, a141\n"
"v_accvgpr_mov_b32 a103, a55\n"
"v_mfma_f32_16x16x32_bf16 v[88:91], v[112:115], a[244:247], v[88:91]\n"
"v_accvgpr_mov_b32 a19, a115\n"
"v_accvgpr_mov_b32 a129, a81\n"
"v_mfma_f32_16x16x32_bf16 v[88:91], v[116:119], a[248:251], v[88:91]\n"
"s_waitcnt lgkmcnt(2)\n"
"ds_read_b128 a[192:195], v243 offset:4096\n"
"v_accvgpr_mov_b32 a5, a101\n"
"v_accvgpr_mov_b32 a17, a113\n"
"v_accvgpr_mov_b32 a23, a119\n"
"v_accvgpr_mov_b32 a26, a122\n"
"v_accvgpr_mov_b32 a27, a123\n"
"v_accvgpr_mov_b32 a37, a133\n"
"v_accvgpr_mov_b32 a46, a142\n"
"v_accvgpr_mov_b32 a101, a53\n"
"v_accvgpr_mov_b32 a124, a76\n"
"v_accvgpr_mov_b32 a121, a73\n"
"v_mfma_f32_16x16x32_bf16 v[180:183], v[124:127], a[0:3], 0\n"
"ds_read_b128 a[196:199], v148 offset:4096\n"
"v_accvgpr_mov_b32 a15, a111\n"
"v_mfma_f32_16x16x32_bf16 v[180:183], v[128:131], a[4:7], v[180:183]\n"
"s_waitcnt lgkmcnt(3)\n"
"v_accvgpr_mov_b32 a117, a69\n"
"v_accvgpr_mov_b32 a119, a71\n"
"v_mfma_f32_16x16x32_bf16 v[180:183], v[132:135], a[8:11], v[180:183]\n"
"s_waitcnt lgkmcnt(2)\n"
"v_accvgpr_mov_b32 a41, a137\n"
"v_accvgpr_mov_b32 a123, a75\n"
"v_mfma_f32_16x16x32_bf16 v[180:183], v[136:139], a[12:15], v[180:183]\n"
"ds_read_b128 a[204:207], v148 offset:6144\n"
"v_mov_b32_dpp v246, v246 row_ror:8 row_mask:0x4 bank_mask:0xf\n"
"v_mfma_f32_16x16x32_bf16 v[184:187], v[124:127], a[16:19], 0\n"
"v_accvgpr_mov_b32 a32, a128\n"
"v_accvgpr_mov_b32 a112, a64\n"
"v_mfma_f32_16x16x32_bf16 v[184:187], v[128:131], a[20:23], v[184:187]\n"
"ds_read_b128 a[200:203], v243 offset:6144\n"
"v_accvgpr_mov_b32 a30, a126\n"
"v_mfma_f32_16x16x32_bf16 v[184:187], v[132:135], a[24:27], v[184:187]\n"
"s_add_u32 s40, s56, s53\n"
"v_accvgpr_mov_b32 a104, a56\n"
"v_sub_f32 v181, s74, v181\n"
"v_mfma_f32_16x16x32_bf16 v[184:187], v[136:139], a[28:31], v[184:187]\n"
"buffer_load_dwordx4 v240, s[20:23], s40 offen offset:2048 nt lds\n"
"v_mov_b32_dpp v247, v247 row_ror:12 row_mask:0x2 bank_mask:0xf\n"
"v_mov_b32_dpp v246, v246 row_ror:4 row_mask:0x8 bank_mask:0xf\n"
"v_accvgpr_mov_b32 a42, a138\n"
"v_accvgpr_mov_b32 a133, a85\n"
"v_accvgpr_mov_b32 a111, a63\n"
"v_accvgpr_mov_b32 a143, a95\n"
"v_accvgpr_mov_b32 a130, a82\n"
"v_sub_f32 v180, s74, v180\n"
"v_sub_f32 v183, s74, v183\n"
"v_mfma_f32_16x16x32_bf16 v[188:191], v[124:127], a[32:35], 0\n"
"v_accvgpr_mov_b32 a120, a72\n"
"v_accvgpr_mov_b32 a139, a91\n"
"v_mfma_f32_16x16x32_bf16 v[188:191], v[128:131], a[36:39], v[188:191]\n"
"v_accvgpr_mov_b32 a44, a140\n"
"v_accvgpr_mov_b32 a132, a84\n"
"v_mfma_f32_16x16x32_bf16 v[188:191], v[132:135], a[40:43], v[188:191]\n"
"v_accvgpr_mov_b32 a108, a60\n"
"v_sub_f32 v187, s74, v187\n"
"v_mfma_f32_16x16x32_bf16 v[188:191], v[136:139], a[44:47], v[188:191]\n"
"s_waitcnt lgkmcnt(1)\n"
"ds_read_b64_tr_b16 a[250:251], v236 offset:53248\n"
"v_sub_f32 v186, s74, v186\n"
"v_mfma_f32_16x16x32_bf16 v[192:195], v[124:127], a[192:195], 0\n"
"v_accvgpr_mov_b32 a115, a67\n"
"v_accvgpr_mov_b32 a122, a74\n"
"v_mfma_f32_16x16x32_bf16 v[192:195], v[128:131], a[196:199], v[192:195]\n"
"s_waitcnt lgkmcnt(1)\n"
"v_accvgpr_mov_b32 a102, a54\n"
"v_mul_f32_dpp v14, v246, v14 row_newbcast:2 row_mask:0xf bank_mask:0xf\n"
"v_mfma_f32_16x16x32_bf16 v[192:195], v[132:135], a[200:203], v[192:195]\n"
"ds_read_b64_tr_b16 a[252:253], v235 offset:50176\n"
"v_sub_f32 v182, s74, v182\n"
"v_mfma_f32_16x16x32_bf16 v[192:195], v[136:139], a[204:207], v[192:195]\n"
"ds_read_b64_tr_b16 v[226:227], v239 offset:54272\n"
"ds_read_b64_tr_b16 a[248:249], v236 offset:49152\n"
"v_xor_b32 v244, 0x11000, v242\n"
"v_accvgpr_mov_b32 a100, a52\n"
"v_accvgpr_mov_b32 a142, a94\n"
"v_mul_f32_dpp v12, v246, v12 row_newbcast:0 row_mask:0xf bank_mask:0xf\n"
"v_sub_f32 v185, s74, v185\n"
"v_sub_f32 v188, s74, v188\n"
"v_sub_f32 v189, s74, v189\n"
"v_sub_f32 v190, s74, v190\n"
"v_mul_f32_dpp v27, v246, v27 row_newbcast:3 row_mask:0xf bank_mask:0xf\n"
"v_mfma_f32_16x16x32_bf16 v[152:155], v[208:211], a[0:3], v[180:183]\n"
"v_accvgpr_mov_b32 a135, a87\n"
"v_mul_f32_dpp v10, v246, v10 row_newbcast:2 row_mask:0xf bank_mask:0xf\n"
"v_mfma_f32_16x16x32_bf16 v[152:155], v[212:215], a[4:7], v[152:155]\n"
"v_accvgpr_mov_b32 a126, a78\n"
"v_sub_f32 v184, s74, v184\n"
"v_mfma_f32_16x16x32_bf16 v[152:155], v[216:219], a[8:11], v[152:155]\n"
"s_add_u32 s40, s56, s54\n"
"v_accvgpr_mov_b32 a110, a62\n"
"v_sub_f32 v194, s74, v194\n"
"v_mfma_f32_16x16x32_bf16 v[152:155], v[220:223], a[12:15], v[152:155]\n"
"buffer_load_dwordx4 v240, s[20:23], s40 offen offset:3072 nt lds\n"
"v_mov_b32_dpp v206, v206 row_ror:12 row_mask:0x2 bank_mask:0xf\n"
"v_accvgpr_mov_b32 a134, a86\n"
"v_accvgpr_mov_b32 a113, a65\n"
"v_accvgpr_mov_b32 a128, a80\n"
"v_accvgpr_mov_b32 a107, a59\n"
"v_sub_f32 v191, s74, v191\n"
"v_sub_f32 v192, s74, v192\n"
"v_sub_f32 v193, s74, v193\n"
"v_sub_f32 v195, s74, v195\n"
"v_mfma_f32_16x16x32_bf16 v[156:159], v[216:219], a[24:27], v[184:187]\n"
"s_add_u32 s55, s55, s59\n"
"v_mov_b32_dpp v206, v206 row_ror:8 row_mask:0x4 bank_mask:0xf\n"
"v_accvgpr_mov_b32 a138, a90\n"
"v_mfma_f32_16x16x32_bf16 v[156:159], v[220:223], a[28:31], v[156:159]\n"
"s_add_u32 s41, s55, s57\n"
"v_accvgpr_mov_b32 a140, a92\n"
"v_accvgpr_mov_b32 a97, a49\n"
"v_mfma_f32_16x16x32_bf16 v[156:159], v[208:211], a[16:19], v[156:159]\n"
"s_and_b32 s41, s41, s58\n"
"ds_read_b64_tr_b16 v[224:225], v239 offset:50176\n"
"v_mov_b32_dpp v247, v247 row_ror:8 row_mask:0x4 bank_mask:0xf\n"
"v_mfma_f32_16x16x32_bf16 v[156:159], v[212:215], a[20:23], v[156:159]\n"
"s_andn2_b32 s56, s55, s58\n"
"v_accvgpr_mov_b32 a96, a48\n"
"v_accvgpr_mov_b32 a98, a50\n"
"v_mfma_f32_16x16x32_bf16 v[160:163], v[208:211], a[32:35], v[188:191]\n"
"s_or_b32 s56, s56, s41\n"
"ds_read_b64_tr_b16 a[254:255], v235 offset:54272\n"
"v_accvgpr_mov_b32 a114, a66\n"
"v_mfma_f32_16x16x32_bf16 v[160:163], v[212:215], a[36:39], v[160:163]\n"
"v_xor_b32 v250, 0x1000, v245\n"
"v_accvgpr_mov_b32 a141, a93\n"
"v_mfma_f32_16x16x32_bf16 v[160:163], v[216:219], a[40:43], v[160:163]\n"
"ds_read_b64_tr_b16 a[244:245], v232 offset:49152\n"
"v_accvgpr_mov_b32 a118, a70\n"
"v_mfma_f32_16x16x32_bf16 v[160:163], v[220:223], a[44:47], v[160:163]\n"
"v_accvgpr_mov_b32 a127, a79\n"
"v_mul_f32_dpp v25, v246, v25 row_newbcast:1 row_mask:0xf bank_mask:0xf\n"
"v_mfma_f32_16x16x32_bf16 v[164:167], v[216:219], a[200:203], v[192:195]\n"
"ds_read_b128 v[216:219], v228 offset:5120\n"
"v_accvgpr_mov_b32 a116, a68\n"
"v_mfma_f32_16x16x32_bf16 v[164:167], v[220:223], a[204:207], v[164:167]\n"
"v_accvgpr_mov_b32 a125, a77\n"
"v_accvgpr_mov_b32 a106, a58\n"
"v_mfma_f32_16x16x32_bf16 v[164:167], v[208:211], a[192:195], v[164:167]\n"
"ds_read_b128 v[220:223], v229 offset:5120\n"
"v_accvgpr_mov_b32 a136, a88\n"
"v_mfma_f32_16x16x32_bf16 v[164:167], v[212:215], a[196:199], v[164:167]\n"
"s_waitcnt lgkmcnt(4)\n"
"ds_read_b128 v[208:211], v228 offset:4096\n"
"v_mov_b32_dpp v206, v206 row_ror:4 row_mask:0x8 bank_mask:0xf\n"
"v_accvgpr_mov_b32 a109, a61\n"
"v_accvgpr_mov_b32 a131, a83\n"
"v_accvgpr_mov_b32 a137, a89\n"
"v_exp_f32 v161, v161\n"
"v_exp_f32 v162, v162\n"
"s_branch LTC_%=\n"
"LTH2_%=:\n"
"v_mfma_f32_16x16x32_bf16 a[224:227], v[124:127], a[244:247], a[224:227]\n"
"v_exp_f32 v168, v168\n"
"v_mfma_f32_16x16x32_bf16 v[16:19], v[128:131], a[244:247], v[16:19]\n"
"s_waitcnt vmcnt(8) lgkmcnt(0)\n"
"v_exp_f32 v169, v169\n"
"v_mfma_f32_16x16x32_bf16 v[16:19], v[132:135], a[248:251], v[16:19]\n"
"s_barrier\n"
"v_mfma_f32_16x16x32_bf16 v[48:51], v[136:139], a[244:247], v[48:51]\n"
"v_exp_f32 v172, v172\n"
"v_mfma_f32_16x16x32_bf16 v[48:51], v[140:143], a[248:251], v[48:51]\n"
"ds_read_b128 v[216:219], v228 offset:1024\n"
"v_cvt_pk_bf16_f32 v102, v168, v169\n"
"v_mfma_f32_16x16x32_bf16 v[80:83], v[144:147], a[244:247], v[80:83]\n"
"v_exp_f32 v176, v176\n"
"v_mfma_f32_16x16x32_bf16 v[80:83], v[148:151], a[248:251], v[80:83]\n"
"ds_read_b64_tr_b16 a[244:245], v234 offset:49152\n"
"ds_read_b64_tr_b16 a[246:247], v234 offset:53248\n"
"v_cvt_pk_bf16_f32 v123, v154, v155\n"
"v_cvt_pk_bf16_f32 v117, v166, v167\n"
"v_exp_f32 v170, v170\n"
"v_exp_f32 v175, v175\n"
"v_exp_f32 v177, v177\n"
"v_mfma_f32_16x16x32_bf16 v[84:87], v[148:151], v[224:227], v[84:87]\n"
"v_exp_f32 v156, v156\n"
"v_mfma_f32_16x16x32_bf16 v[84:87], v[144:147], a[252:255], v[84:87]\n"
"ds_read_b64_tr_b16 a[248:249], v238 offset:49152\n"
"ds_read_b64_tr_b16 a[250:251], v238 offset:53248\n"
"v_mfma_f32_16x16x32_bf16 v[52:55], v[140:143], v[224:227], v[52:55]\n"
"v_cvt_pk_bf16_f32 v101, v158, v159\n"
"v_cvt_pk_bf16_f32 v118, v176, v177\n"
"v_mfma_f32_16x16x32_bf16 v[52:55], v[136:139], a[252:255], v[52:55]\n"
"v_cvt_pk_bf16_f32 v100, v156, v157\n"
"v_exp_f32 v173, v173\n"
"v_exp_f32 v174, v174\n"
"v_exp_f32 v178, v178\n"
"v_exp_f32 v179, v179\n"
"v_mfma_f32_16x16x32_bf16 v[20:23], v[132:135], v[224:227], v[20:23]\n"
"v_exp_f32 v171, v171\n"
"v_mfma_f32_16x16x32_bf16 v[20:23], v[128:131], a[252:255], v[20:23]\n"
"ds_read_b64_tr_b16 v[224:225], v239 offset:49152\n"
"v_cvt_pk_bf16_f32 v103, v170, v171\n"
"v_mfma_f32_16x16x32_bf16 a[228:231], v[124:127], a[252:255], a[228:231]\n"
"s_waitcnt lgkmcnt(3)\n"
"ds_read_b64_tr_b16 a[252:253], v235 offset:49152\n"
"v_cvt_pk_bf16_f32 v119, v178, v179\n"
"v_mfma_f32_16x16x32_bf16 a[216:219], v[120:123], a[244:247], a[216:219]\n"
"s_add_u32 m0, s44, 81920\n"
"ds_read_b64_tr_b16 a[254:255], v235 offset:53248\n"
"ds_read_b64_tr_b16 v[226:227], v239 offset:53248\n"
"v_smfmac_f32_16x16x64_bf16 v[196:199], a[240:243], v[96:103], v241\n"
"buffer_load_dwordx4 v240, s[20:23], s56 offen nt lds\n"
"v_mfma_f32_16x16x32_bf16 v[8:11], v[96:99], a[244:247], v[8:11]\n"
"s_waitcnt lgkmcnt(4)\n"
"v_cvt_pk_bf16_f32 v109, v162, v163\n"
"v_cvt_pk_bf16_f32 v116, v164, v165\n"
"v_mfma_f32_16x16x32_bf16 v[8:11], v[100:103], a[248:251], v[8:11]\n"
"v_cvt_pk_bf16_f32 v110, v172, v173\n"
"v_cvt_pk_bf16_f32 v111, v174, v175\n"
"v_smfmac_f32_16x16x64_bf16 v[200:203], a[240:243], v[112:119], v241\n"
"ds_read_b128 v[220:223], v229 offset:1024\n"
"v_cvt_pk_bf16_f32 v108, v160, v161\n"
"v_mfma_f32_16x16x32_bf16 v[40:43], v[104:107], a[244:247], v[40:43]\n"
"v_add_f32 v196, v196, v197\n"
"v_and_b32 v245, 0x3f0, v242\n"
"v_mfma_f32_16x16x32_bf16 v[40:43], v[108:111], a[248:251], v[40:43]\n"
"ds_read_b128 v[212:215], v229 offset:0\n"
"v_lshrrev_b32 v245, 1, v245\n"
"v_smfmac_f32_16x16x64_bf16 v[198:201], a[240:243], v[104:111], v241\n"
"v_lshlrev_b32_e64 v244, 24, s43\n"
"v_sub_u32 v245, v242, v245\n"
"v_mfma_f32_16x16x32_bf16 v[72:75], v[112:115], a[244:247], v[72:75]\n"
"ds_read_b64_tr_b16 a[244:245], v234 offset:50176\n"
"ds_read_b64_tr_b16 a[246:247], v234 offset:54272\n"
"v_mfma_f32_16x16x32_bf16 v[72:75], v[116:119], a[248:251], v[72:75]\n"
"ds_read_b128 v[208:211], v228 offset:0\n"
"v_mul_f32 v244, 0x45800000, v244\n"
"v_mfma_f32_16x16x32_bf16 v[202:205], a[240:243], v[120:123], v[202:205]\n"
"s_waitcnt lgkmcnt(5)\n"
"ds_read_b64_tr_b16 a[250:251], v238 offset:54272\n"
"v_add_f32 v198, v198, v199\n"
"v_mfma_f32_16x16x32_bf16 v[76:79], v[116:119], v[224:227], v[76:79]\n"
"v_add_f32 v200, v200, v201\n"
"v_add_f32 v254, v196, v198\n"
"v_mfma_f32_16x16x32_bf16 v[76:79], v[112:115], a[252:255], v[76:79]\n"
"ds_read_b64_tr_b16 a[248:249], v238 offset:50176\n"
"v_add_f32 v254, v254, v200\n"
"v_mfma_f32_16x16x32_bf16 v[44:47], v[108:111], v[224:227], v[44:47]\n"
"v_add_f32 v244, v244, v202\n"
"v_min3_f32 v244, v244, v196, v198\n"
"v_mfma_f32_16x16x32_bf16 v[44:47], v[104:107], a[252:255], v[44:47]\n"
"v_min_f32 v244, v244, v200\n"
"v_rcp_f32 v244, v244\n"
"v_mfma_f32_16x16x32_bf16 v[12:15], v[100:103], v[224:227], v[12:15]\n"
"s_add_u32 s40, s56, s52\n"
"v_add_f32 v254, v254, v202\n"
"v_fmac_f32 v254, 0x68800000, v244\n"
"v_mfma_f32_16x16x32_bf16 v[12:15], v[96:99], a[252:255], v[12:15]\n"
"buffer_load_dwordx4 v240, s[20:23], s40 offen offset:1024 nt lds\n"
"v_mfma_f32_16x16x32_bf16 a[220:223], v[120:123], a[252:255], a[220:223]\n"
"s_waitcnt lgkmcnt(2)\n"
"v_cmp_nge_f32 vcc, s45, v254\n"
"s_cbranch_vccnz LSLOW_%=\nLBACK2_%=:\ns_mov_b64 vcc, s[64:65]\n"
"v_mfma_f32_16x16x32_bf16 a[232:235], v[120:123], a[244:247], a[232:235]\n"
"v_rcp_f32 v246, v196\n"
"v_rcp_f32 v206, v200\n"
"v_mov_b32 v252, v192\n"
"v_accvgpr_mov_b32 a9, a153\n"
"v_accvgpr_mov_b32 a10, a154\n"
"v_accvgpr_mov_b32 a28, a172\n"
"v_accvgpr_mov_b32 a31, a175\n"
"v_accvgpr_mov_b32 a33, a177\n"
"v_accvgpr_mov_b32 a40, a184\n"
"v_bfe_u32 v149, v242, 8, 2\n"
"v_lshl_add_u32 v149, v149, 4, s43\n"
"v_xor_b32 v150, 64, v149\n"
"v_accvgpr_mov_b32 a153, a57\n"
"v_mfma_f32_16x16x32_bf16 v[24:27], v[96:99], a[244:247], v[24:27]\n"
"s_waitcnt lgkmcnt(0)\n"
"ds_read_b128 v[128:131], v150 offset:0\n"
"v_accvgpr_mov_b32 a12, a156\n"
"v_mfma_f32_16x16x32_bf16 v[24:27], v[100:103], a[248:251], v[24:27]\n"
"ds_read_b128 v[124:127], v149 offset:0\n"
"v_accvgpr_mov_b32 a0, a144\n"
"v_accvgpr_mov_b32 a3, a147\n"
"v_accvgpr_mov_b32 a4, a148\n"
"v_accvgpr_mov_b32 a18, a162\n"
"v_accvgpr_mov_b32 a21, a165\n"
"v_accvgpr_mov_b32 a35, a179\n"
"v_accvgpr_mov_b32 a38, a182\n"
"v_accvgpr_mov_b32 a47, a191\n"
"v_accvgpr_mov_b32 a147, a51\n"
"v_mfma_f32_16x16x32_bf16 v[56:59], v[104:107], a[244:247], v[56:59]\n"
"ds_read_b128 v[132:135], v149 offset:1024\n"
"v_rcp_f32 v247, v198\n"
"v_accvgpr_mov_b32 a1, a145\n"
"v_accvgpr_mov_b32 a2, a146\n"
"v_accvgpr_mov_b32 a11, a155\n"
"v_accvgpr_mov_b32 a13, a157\n"
"v_accvgpr_mov_b32 a14, a158\n"
"v_accvgpr_mov_b32 a16, a160\n"
"v_accvgpr_mov_b32 a20, a164\n"
"v_accvgpr_mov_b32 a22, a166\n"
"v_accvgpr_mov_b32 a25, a169\n"
"v_accvgpr_mov_b32 a36, a180\n"
"v_xor_b32 v148, 0x400, v243\n"
"v_mfma_f32_16x16x32_bf16 v[56:59], v[108:111], a[248:251], v[56:59]\n"
"ds_read_b128 v[136:139], v150 offset:1024\n"
"v_mov_b32_dpp v246, v246 row_ror:12 row_mask:0x2 bank_mask:0xf\n"
"v_accvgpr_mov_b32 a6, a150\n"
"v_accvgpr_mov_b32 a7, a151\n"
"v_accvgpr_mov_b32 a8, a152\n"
"v_accvgpr_mov_b32 a24, a168\n"
"v_accvgpr_mov_b32 a29, a173\n"
"v_accvgpr_mov_b32 a34, a178\n"
"v_accvgpr_mov_b32 a39, a183\n"
"v_accvgpr_mov_b32 a43, a187\n"
"v_accvgpr_mov_b32 a45, a189\n"
"v_accvgpr_mov_b32 a151, a55\n"
"v_mfma_f32_16x16x32_bf16 v[88:91], v[112:115], a[244:247], v[88:91]\n"
"v_accvgpr_mov_b32 a19, a163\n"
"v_accvgpr_mov_b32 a177, a81\n"
"v_mfma_f32_16x16x32_bf16 v[88:91], v[116:119], a[248:251], v[88:91]\n"
"s_waitcnt lgkmcnt(2)\n"
"ds_read_b128 a[192:195], v243 offset:4096\n"
"v_accvgpr_mov_b32 a5, a149\n"
"v_accvgpr_mov_b32 a17, a161\n"
"v_accvgpr_mov_b32 a23, a167\n"
"v_accvgpr_mov_b32 a26, a170\n"
"v_accvgpr_mov_b32 a27, a171\n"
"v_accvgpr_mov_b32 a37, a181\n"
"v_accvgpr_mov_b32 a46, a190\n"
"v_accvgpr_mov_b32 a149, a53\n"
"v_accvgpr_mov_b32 a172, a76\n"
"v_accvgpr_mov_b32 a169, a73\n"
"v_mfma_f32_16x16x32_bf16 v[180:183], v[124:127], a[0:3], 0\n"
"ds_read_b128 a[196:199], v148 offset:4096\n"
"v_accvgpr_mov_b32 a15, a159\n"
"v_mfma_f32_16x16x32_bf16 v[180:183], v[128:131], a[4:7], v[180:183]\n"
"s_waitcnt lgkmcnt(3)\n"
"v_accvgpr_mov_b32 a165, a69\n"
"v_accvgpr_mov_b32 a167, a71\n"
"v_mfma_f32_16x16x32_bf16 v[180:183], v[132:135], a[8:11], v[180:183]\n"
"s_waitcnt lgkmcnt(2)\n"
"v_accvgpr_mov_b32 a41, a185\n"
"v_accvgpr_mov_b32 a171, a75\n"
"v_mfma_f32_16x16x32_bf16 v[180:183], v[136:139], a[12:15], v[180:183]\n"
"ds_read_b128 a[204:207], v148 offset:6144\n"
"v_mov_b32_dpp v246, v246 row_ror:8 row_mask:0x4 bank_mask:0xf\n"
"v_mfma_f32_16x16x32_bf16 v[184:187], v[124:127], a[16:19], 0\n"
"v_accvgpr_mov_b32 a32, a176\n"
"v_accvgpr_mov_b32 a160, a64\n"
"v_mfma_f32_16x16x32_bf16 v[184:187], v[128:131], a[20:23], v[184:187]\n"
"ds_read_b128 a[200:203], v243 offset:6144\n"
"v_accvgpr_mov_b32 a30, a174\n"
"v_mfma_f32_16x16x32_bf16 v[184:187], v[132:135], a[24:27], v[184:187]\n"
"s_add_u32 s40, s56, s53\n"
"v_accvgpr_mov_b32 a152, a56\n"
"v_sub_f32 v181, s74, v181\n"
"v_mfma_f32_16x16x32_bf16 v[184:187], v[136:139], a[28:31], v[184:187]\n"
"buffer_load_dwordx4 v240, s[20:23], s40 offen offset:2048 nt lds\n"
"v_mov_b32_dpp v247, v247 row_ror:12 row_mask:0x2 bank_mask:0xf\n"
"v_mov_b32_dpp v246, v246 row_ror:4 row_mask:0x8 bank_mask:0xf\n"
"v_accvgpr_mov_b32 a42, a186\n"
"v_accvgpr_mov_b32 a181, a85\n"
"v_accvgpr_mov_b32 a159, a63\n"
"v_accvgpr_mov_b32 a191, a95\n"
"v_accvgpr_mov_b32 a178, a82\n"
"v_sub_f32 v180, s74, v180\n"
"v_sub_f32 v183, s74, v183\n"
"v_mfma_f32_16x16x32_bf16 v[188:191], v[124:127], a[32:35], 0\n"
"v_accvgpr_mov_b32 a168, a72\n"
"v_accvgpr_mov_b32 a187, a91\n"
"v_mfma_f32_16x16x32_bf16 v[188:191], v[128:131], a[36:39], v[188:191]\n"
"v_accvgpr_mov_b32 a44, a188\n"
"v_accvgpr_mov_b32 a180, a84\n"
"v_mfma_f32_16x16x32_bf16 v[188:191], v[132:135], a[40:43], v[188:191]\n"
"v_accvgpr_mov_b32 a156, a60\n"
"v_sub_f32 v187, s74, v187\n"
"v_mfma_f32_16x16x32_bf16 v[188:191], v[136:139], a[44:47], v[188:191]\n"
"s_waitcnt lgkmcnt(1)\n"
"ds_read_b64_tr_b16 a[250:251], v236 offset:53248\n"
"v_sub_f32 v186, s74, v186\n"
"v_mfma_f32_16x16x32_bf16 v[192:195], v[124:127], a[192:195], 0\n"
"v_accvgpr_mov_b32 a163, a67\n"
"v_accvgpr_mov_b32 a170, a74\n"
"v_mfma_f32_16x16x32_bf16 v[192:195], v[128:131], a[196:199], v[192:195]\n"
"s_waitcnt lgkmcnt(1)\n"
"v_accvgpr_mov_b32 a150, a54\n"
"v_mul_f32_dpp v14, v246, v14 row_newbcast:2 row_mask:0xf bank_mask:0xf\n"
"v_mfma_f32_16x16x32_bf16 v[192:195], v[132:135], a[200:203], v[192:195]\n"
"ds_read_b64_tr_b16 a[252:253], v235 offset:50176\n"
"v_sub_f32 v182, s74, v182\n"
"v_mfma_f32_16x16x32_bf16 v[192:195], v[136:139], a[204:207], v[192:195]\n"
"ds_read_b64_tr_b16 v[226:227], v239 offset:54272\n"
"ds_read_b64_tr_b16 a[248:249], v236 offset:49152\n"
"v_xor_b32 v244, 0x11000, v242\n"
"v_accvgpr_mov_b32 a148, a52\n"
"v_accvgpr_mov_b32 a190, a94\n"
"v_mul_f32_dpp v12, v246, v12 row_newbcast:0 row_mask:0xf bank_mask:0xf\n"
"v_sub_f32 v185, s74, v185\n"
"v_sub_f32 v188, s74, v188\n"
"v_sub_f32 v189, s74, v189\n"
"v_sub_f32 v190, s74, v190\n"
"v_mul_f32_dpp v27, v246, v27 row_newbcast:3 row_mask:0xf bank_mask:0xf\n"
"v_mfma_f32_16x16x32_bf16 v[152:155], v[208:211], a[0:3], v[180:183]\n"
"v_accvgpr_mov_b32 a183, a87\n"
"v_mul_f32_dpp v10, v246, v10 row_newbcast:2 row_mask:0xf bank_mask:0xf\n"
"v_mfma_f32_16x16x32_bf16 v[152:155], v[212:215], a[4:7], v[152:155]\n"
"v_accvgpr_mov_b32 a174, a78\n"
"v_sub_f32 v184, s74, v184\n"
"v_mfma_f32_16x16x32_bf16 v[152:155], v[216:219], a[8:11], v[152:155]\n"
"s_add_u32 s40, s56, s54\n"
"v_accvgpr_mov_b32 a158, a62\n"
"v_sub_f32 v194, s74, v194\n"
"v_mfma_f32_16x16x32_bf16 v[152:155], v[220:223], a[12:15], v[152:155]\n"
"buffer_load_dwordx4 v240, s[20:23], s40 offen offset:3072 nt lds\n"
"v_mov_b32_dpp v206, v206 row_ror:12 row_mask:0x2 bank_mask:0xf\n"
"v_accvgpr_mov_b32 a182, a86\n"
"v_accvgpr_mov_b32 a161, a65\n"
"v_accvgpr_mov_b32 a176, a80\n"
"v_accvgpr_mov_b32 a155, a59\n"
"v_sub_f32 v191, s74, v191\n"
"v_sub_f32 v192, s74, v192\n"
"v_sub_f32 v193, s74, v193\n"
"v_sub_f32 v195, s74, v195\n"
"v_mfma_f32_16x16x32_bf16 v[156:159], v[216:219], a[24:27], v[184:187]\n"
"s_add_u32 s55, s55, s59\n"
"v_mov_b32_dpp v206, v206 row_ror:8 row_mask:0x4 bank_mask:0xf\n"
"v_accvgpr_mov_b32 a186, a90\n"
"v_mfma_f32_16x16x32_bf16 v[156:159], v[220:223], a[28:31], v[156:159]\n"
"s_add_u32 s41, s55, s57\n"
"v_accvgpr_mov_b32 a188, a92\n"
"v_accvgpr_mov_b32 a145, a49\n"
"v_mfma_f32_16x16x32_bf16 v[156:159], v[208:211], a[16:19], v[156:159]\n"
"s_and_b32 s41, s41, s58\n"
"ds_read_b64_tr_b16 v[224:225], v239 offset:50176\n"
"v_mov_b32_dpp v247, v247 row_ror:8 row_mask:0x4 bank_mask:0xf\n"
"v_mfma_f32_16x16x32_bf16 v[156:159], v[212:215], a[20:23], v[156:159]\n"
"s_andn2_b32 s56, s55, s58\n"
"v_accvgpr_mov_b32 a144, a48\n"
"v_accvgpr_mov_b32 a146, a50\n"
"v_mfma_f32_16x16x32_bf16 v[160:163], v[208:211], a[32:35], v[188:191]\n"
"s_or_b32 s56, s56, s41\n"
"ds_read_b64_tr_b16 a[254:255], v235 offset:54272\n"
"v_accvgpr_mov_b32 a162, a66\n"
"v_mfma_f32_16x16x32_bf16 v[160:163], v[212:215], a[36:39], v[160:163]\n"
"v_xor_b32 v250, 0x1000, v245\n"
"v_accvgpr_mov_b32 a189, a93\n"
"v_mfma_f32_16x16x32_bf16 v[160:163], v[216:219], a[40:43], v[160:163]\n"
"ds_read_b64_tr_b16 a[244:245], v232 offset:49152\n"
"v_accvgpr_mov_b32 a166, a70\n"
"v_mfma_f32_16x16x32_bf16 v[160:163], v[220:223], a[44:47], v[160:163]\n"
"v_accvgpr_mov_b32 a175, a79\n"
"v_mul_f32_dpp v25, v246, v25 row_newbcast:1 row_mask:0xf bank_mask:0xf\n"
"v_mfma_f32_16x16x32_bf16 v[164:167], v[216:219], a[200:203], v[192:195]\n"
"ds_read_b128 v[216:219], v228 offset:5120\n"
"v_accvgpr_mov_b32 a164, a68\n"
"v_mfma_f32_16x16x32_bf16 v[164:167], v[220:223], a[204:207], v[164:167]\n"
"v_accvgpr_mov_b32 a173, a77\n"
"v_accvgpr_mov_b32 a154, a58\n"
"v_mfma_f32_16x16x32_bf16 v[164:167], v[208:211], a[192:195], v[164:167]\n"
"ds_read_b128 v[220:223], v229 offset:5120\n"
"v_accvgpr_mov_b32 a184, a88\n"
"v_mfma_f32_16x16x32_bf16 v[164:167], v[212:215], a[196:199], v[164:167]\n"
"s_waitcnt lgkmcnt(4)\n"
"ds_read_b128 v[208:211], v228 offset:4096\n"
"v_mov_b32_dpp v206, v206 row_ror:4 row_mask:0x8 bank_mask:0xf\n"
"v_accvgpr_mov_b32 a157, a61\n"
"v_accvgpr_mov_b32 a179, a83\n"
"v_accvgpr_mov_b32 a185, a89\n"
"v_exp_f32 v161, v161\n"
"v_exp_f32 v162, v162\n"
"LTC_%=:\n"
"s_waitcnt lgkmcnt(5)\n"
"v_mfma_f32_16x16x32_bf16 v[92:95], v[116:119], v[224:227], v[92:95]\n"
"s_waitcnt lgkmcnt(4)\n"
"ds_read_b128 v[212:215], v229 offset:4096\n"
"v_mul_f32_dpp v11, v246, v11 row_newbcast:3 row_mask:0xf bank_mask:0xf\n"
"v_mfma_f32_16x16x32_bf16 v[92:95], v[112:115], a[252:255], v[92:95]\n"
"v_mov_b32_dpp v247, v247 row_ror:4 row_mask:0x8 bank_mask:0xf\n"
"v_mov_b32 v253, v202\n"
"v_mfma_f32_16x16x32_bf16 v[60:63], v[108:111], v[224:227], v[60:63]\n"
"s_add_u32 m0, s44, 32768\n"
"ds_read_b64_tr_b16 a[246:247], v232 offset:53248\n"
"v_mul_f32_dpp v46, v247, v46 row_newbcast:2 row_mask:0xf bank_mask:0xf\n"
"v_mfma_f32_16x16x32_bf16 v[60:63], v[104:107], a[252:255], v[60:63]\n"
"buffer_load_dwordx4 v240, s[16:19], s56 offen nt lds\n"
"v_mfma_f32_16x16x32_bf16 v[28:31], v[100:103], v[224:227], v[28:31]\n"
"v_exp_f32 v154, v154\n"
"v_mfma_f32_16x16x32_bf16 v[28:31], v[96:99], a[252:255], v[28:31]\n"
"v_mul_f32_dpp v13, v246, v13 row_newbcast:1 row_mask:0xf bank_mask:0xf\n"
"v_mul_f32_dpp v26, v246, v26 row_newbcast:2 row_mask:0xf bank_mask:0xf\n"
"v_mfma_f32_16x16x32_bf16 a[236:239], v[120:123], a[252:255], a[236:239]\n"
"s_waitcnt lgkmcnt(3)\n"
"ds_write_b128 v244, a[216:219] offset:32768\n"
"v_mul_f32_dpp v15, v246, v15 row_newbcast:3 row_mask:0xf bank_mask:0xf\n"
"v_exp_f32 v155, v155\n"
"v_exp_f32 v156, v156\n"
"v_exp_f32 v158, v158\n"
"v_exp_f32 v160, v160\n"
"v_exp_f32 v163, v163\n"
"v_exp_f32 v166, v166\n"
"v_mul_f32_dpp v24, v246, v24 row_newbcast:0 row_mask:0xf bank_mask:0xf\n"
"v_mfma_f32_16x16x32_bf16 v[168:171], v[216:219], a[8:11], v[180:183]\n"
"v_exp_f32 v159, v159\n"
"v_mfma_f32_16x16x32_bf16 v[168:171], v[220:223], a[12:15], v[168:171]\n"
"s_waitcnt lgkmcnt(2)\n"
"ds_read_b64_tr_b16 v[226:227], v237 offset:53248\n"
"v_mul_f32_dpp v74, v206, v74 row_newbcast:2 row_mask:0xf bank_mask:0xf\n"
"v_mfma_f32_16x16x32_bf16 v[168:171], v[208:211], a[0:3], v[168:171]\n"
"v_exp_f32 v153, v153\n"
"v_mfma_f32_16x16x32_bf16 v[168:171], v[212:215], a[4:7], v[168:171]\n"
"v_mul_f32_dpp v41, v247, v41 row_newbcast:1 row_mask:0xf bank_mask:0xf\n"
"v_cvt_pk_bf16_f32 v144, v160, v161\n"
"v_mfma_f32_16x16x32_bf16 v[172:175], v[208:211], a[16:19], v[184:187]\n"
"v_exp_f32 v157, v157\n"
"v_mfma_f32_16x16x32_bf16 v[172:175], v[212:215], a[20:23], v[172:175]\n"
"v_exp_f32 v152, v152\n"
"v_mfma_f32_16x16x32_bf16 v[172:175], v[216:219], a[24:27], v[172:175]\n"
"v_exp_f32 v164, v164\n"
"v_mfma_f32_16x16x32_bf16 v[172:175], v[220:223], a[28:31], v[172:175]\n"
"ds_read_b64_tr_b16 a[254:255], v233 offset:53248\n"
"v_mul_f32_dpp v9, v246, v9 row_newbcast:1 row_mask:0xf bank_mask:0xf\n"
"v_mfma_f32_16x16x32_bf16 v[176:179], v[216:219], a[40:43], v[188:191]\n"
"ds_read_b64_tr_b16 v[224:225], v237 offset:49152\n"
"v_mul_f32_dpp v73, v206, v73 row_newbcast:1 row_mask:0xf bank_mask:0xf\n"
"v_mfma_f32_16x16x32_bf16 v[176:179], v[220:223], a[44:47], v[176:179]\n"
"v_cvt_pk_bf16_f32 v128, v152, v153\n"
"v_cvt_pk_bf16_f32 v129, v154, v155\n"
"v_mfma_f32_16x16x32_bf16 v[176:179], v[208:211], a[32:35], v[176:179]\n"
"s_add_u32 s40, s56, s52\n"
"v_mul_f32_dpp v43, v247, v43 row_newbcast:3 row_mask:0xf bank_mask:0xf\n"
"v_cvt_pk_bf16_f32 v24, v24, v25\n"
"v_mfma_f32_16x16x32_bf16 v[176:179], v[212:215], a[36:39], v[176:179]\n"
"buffer_load_dwordx4 v240, s[16:19], s40 offen offset:1024 nt lds\n"
"v_mfma_f32_16x16x32_bf16 v[152:155], v[208:211], a[192:195], v[192:195]\n"
"v_cvt_pk_bf16_f32 v137, v158, v159\n"
"v_mul_f32_dpp v62, v247, v62 row_newbcast:2 row_mask:0xf bank_mask:0xf\n"
"v_mfma_f32_16x16x32_bf16 v[152:155], v[212:215], a[196:199], v[152:155]\n"
"v_cvt_pk_bf16_f32 v145, v162, v163\n"
"v_mul_f32_dpp v90, v206, v90 row_newbcast:2 row_mask:0xf bank_mask:0xf\n"
"v_mfma_f32_16x16x32_bf16 v[152:155], v[216:219], a[200:203], v[152:155]\n"
"ds_read_b128 v[212:215], v231 offset:0\n"
"v_mul_f32_dpp v40, v247, v40 row_newbcast:0 row_mask:0xf bank_mask:0xf\n"
"v_mfma_f32_16x16x32_bf16 v[152:155], v[220:223], a[204:207], v[152:155]\n"
"s_waitcnt lgkmcnt(2)\n"
"ds_read_b128 v[208:211], v230 offset:0\n"
"ds_read_b128 v[220:223], v231 offset:1024\n"
"v_mfma_f32_16x16x32_bf16 a[208:211], v[120:123], a[244:247], a[208:211]\n"
"v_exp_f32 v173, v173\n"
"v_mfma_f32_16x16x32_bf16 v[0:3], v[96:99], a[244:247], v[0:3]\n"
"ds_read_b128 v[216:219], v230 offset:1024\n"
"v_mul_f32_dpp v72, v206, v72 row_newbcast:0 row_mask:0xf bank_mask:0xf\n"
"v_mfma_f32_16x16x32_bf16 v[0:3], v[100:103], a[248:251], v[0:3]\n"
"v_exp_f32 v178, v178\n"
"v_mfma_f32_16x16x32_bf16 v[32:35], v[104:107], a[244:247], v[32:35]\n"
"v_exp_f32 v171, v171\n"
"v_mfma_f32_16x16x32_bf16 v[32:35], v[108:111], a[248:251], v[32:35]\n"
"ds_write_b128 v244, a[220:223] offset:33792\n"
"v_exp_f32 v165, v165\n"
"v_exp_f32 v167, v167\n"
"v_mul_f32_dpp v59, v247, v59 row_newbcast:3 row_mask:0xf bank_mask:0xf\n"
"v_exp_f32 v168, v168\n"
"v_mfma_f32_16x16x32_bf16 v[64:67], v[112:115], a[244:247], v[64:67]\n"
"v_exp_f32 v172, v172\n"
"v_mfma_f32_16x16x32_bf16 v[64:67], v[116:119], a[248:251], v[64:67]\n"
"s_waitcnt lgkmcnt(3)\n"
"ds_write_b64 v250, v[252:253] offset:52224\n"
"v_mul_f32_dpp v42, v247, v42 row_newbcast:2 row_mask:0xf bank_mask:0xf\n"
"v_mul_f32_dpp v75, v206, v75 row_newbcast:3 row_mask:0xf bank_mask:0xf\n"
"v_cvt_pk_bf16_f32 v136, v156, v157\n"
"v_cvt_pk_bf16_f32 v125, v166, v167\n"
"v_mul_f32_dpp v29, v246, v29 row_newbcast:1 row_mask:0xf bank_mask:0xf\n"
"v_mul_f32_dpp v57, v247, v57 row_newbcast:1 row_mask:0xf bank_mask:0xf\n"
"v_exp_f32 v155, v155\n"
"v_mul_f32_dpp v0, v246, v0 row_newbcast:0 row_mask:0xf bank_mask:0xf\n"
"v_mul_f32_dpp v1, v246, v1 row_newbcast:1 row_mask:0xf bank_mask:0xf\n"
"v_mfma_f32_16x16x32_bf16 v[156:159], v[208:211], a[0:3], v[180:183]\n"
"v_mul_f32_dpp v45, v247, v45 row_newbcast:1 row_mask:0xf bank_mask:0xf\n"
"v_mul_f32_dpp v89, v206, v89 row_newbcast:1 row_mask:0xf bank_mask:0xf\n"
"v_mfma_f32_16x16x32_bf16 v[156:159], v[212:215], a[4:7], v[156:159]\n"
"s_waitcnt lgkmcnt(2)\n"
"v_mul_f32_dpp v8, v246, v8 row_newbcast:0 row_mask:0xf bank_mask:0xf\n"
"v_mul_f32_dpp v30, v246, v30 row_newbcast:2 row_mask:0xf bank_mask:0xf\n"
"v_mfma_f32_16x16x32_bf16 v[156:159], v[216:219], a[8:11], v[156:159]\n"
"s_add_u32 s40, s56, s53\n"
"ds_read_b64_tr_b16 a[252:253], v233 offset:49152\n"
"v_mul_f32_dpp v56, v247, v56 row_newbcast:0 row_mask:0xf bank_mask:0xf\n"
"v_mfma_f32_16x16x32_bf16 v[156:159], v[220:223], a[12:15], v[156:159]\n"
"buffer_load_dwordx4 v240, s[16:19], s40 offen offset:2048 nt lds\n"
"v_mul_f32_dpp v44, v247, v44 row_newbcast:0 row_mask:0xf bank_mask:0xf\n"
"v_mul_f32_dpp v76, v206, v76 row_newbcast:0 row_mask:0xf bank_mask:0xf\n"
"v_mul_f32_dpp v88, v206, v88 row_newbcast:0 row_mask:0xf bank_mask:0xf\n"
"v_exp_f32 v170, v170\n"
"v_exp_f32 v174, v174\n"
"v_exp_f32 v175, v175\n"
"v_exp_f32 v176, v176\n"
"v_exp_f32 v152, v152\n"
"v_exp_f32 v154, v154\n"
"v_mul_f32_dpp v66, v206, v66 row_newbcast:2 row_mask:0xf bank_mask:0xf\n"
"v_mfma_f32_16x16x32_bf16 v[160:163], v[216:219], a[24:27], v[184:187]\n"
"v_mul_f32_dpp v92, v206, v92 row_newbcast:0 row_mask:0xf bank_mask:0xf\n"
"v_cvt_pk_bf16_f32 v0, v0, v1\n"
"v_mfma_f32_16x16x32_bf16 v[160:163], v[220:223], a[28:31], v[160:163]\n"
"v_mul_f32_dpp v95, v206, v95 row_newbcast:3 row_mask:0xf bank_mask:0xf\n"
"v_cvt_pk_bf16_f32 v131, v170, v171\n"
"v_mfma_f32_16x16x32_bf16 v[160:163], v[208:211], a[16:19], v[160:163]\n"
"v_mul_f32_dpp v58, v247, v58 row_newbcast:2 row_mask:0xf bank_mask:0xf\n"
"v_cvt_pk_bf16_f32 v139, v174, v175\n"
"v_mfma_f32_16x16x32_bf16 v[160:163], v[212:215], a[20:23], v[160:163]\n"
"ds_write_b128 v244, a[236:239] offset:35840\n"
"v_cvt_pk_bf16_f32 v124, v164, v165\n"
"v_mul_f32_dpp v94, v206, v94 row_newbcast:2 row_mask:0xf bank_mask:0xf\n"
"v_exp_f32 v169, v169\n"
"v_exp_f32 v156, v156\n"
"v_mfma_f32_16x16x32_bf16 v[164:167], v[208:211], a[32:35], v[188:191]\n"
"v_exp_f32 v157, v157\n"
"v_mfma_f32_16x16x32_bf16 v[164:167], v[212:215], a[36:39], v[164:167]\n"
"ds_read_b128 v[208:211], v230 offset:4096\n"
"v_cvt_pk_bf16_f32 v8, v8, v9\n"
"v_mfma_f32_16x16x32_bf16 v[164:167], v[216:219], a[40:43], v[164:167]\n"
"v_cvt_pk_bf16_f32 v40, v40, v41\n"
"v_mul_f32_dpp v60, v247, v60 row_newbcast:0 row_mask:0xf bank_mask:0xf\n"
"v_mfma_f32_16x16x32_bf16 v[164:167], v[220:223], a[44:47], v[164:167]\n"
"ds_read_b64_tr_b16 a[244:245], v232 offset:50176\n"
"ds_read_b64_tr_b16 a[248:249], v236 offset:50176\n"
"v_mul_f32_dpp v79, v206, v79 row_newbcast:3 row_mask:0xf bank_mask:0xf\n"
"v_cvt_pk_bf16_f32 v25, v26, v27\n"
"v_mul_f32_dpp v61, v247, v61 row_newbcast:1 row_mask:0xf bank_mask:0xf\n"
"v_exp_f32 v177, v177\n"
"v_exp_f32 v179, v179\n"
"v_exp_f32 v160, v160\n"
"v_exp_f32 v161, v161\n"
"v_mul_f32_dpp v33, v247, v33 row_newbcast:1 row_mask:0xf bank_mask:0xf\n"
"v_mfma_f32_16x16x32_bf16 v[68:71], v[116:119], v[224:227], v[68:71]\n"
"s_waitcnt lgkmcnt(4)\n"
"ds_read_b128 v[220:223], v231 offset:5120\n"
"v_mul_f32_dpp v77, v206, v77 row_newbcast:1 row_mask:0xf bank_mask:0xf\n"
"v_mfma_f32_16x16x32_bf16 v[68:71], v[112:115], a[252:255], v[68:71]\n"
"ds_read_b128 v[212:215], v231 offset:4096\n"
"v_mul_f32_dpp v32, v247, v32 row_newbcast:0 row_mask:0xf bank_mask:0xf\n"
"v_mfma_f32_16x16x32_bf16 v[36:39], v[108:111], v[224:227], v[36:39]\n"
"s_add_u32 s40, s56, s54\n"
"ds_read_b128 v[216:219], v230 offset:5120\n"
"v_mul_f32_dpp v2, v246, v2 row_newbcast:2 row_mask:0xf bank_mask:0xf\n"
"v_mfma_f32_16x16x32_bf16 v[36:39], v[104:107], a[252:255], v[36:39]\n"
"buffer_load_dwordx4 v240, s[16:19], s40 offen offset:3072 nt lds\n"
"v_mul_f32_dpp v47, v247, v47 row_newbcast:3 row_mask:0xf bank_mask:0xf\n"
"v_cvt_pk_bf16_f32 v41, v42, v43\n"
"v_mul_f32_dpp v31, v246, v31 row_newbcast:3 row_mask:0xf bank_mask:0xf\n"
"v_cvt_pk_bf16_f32 v147, v178, v179\n"
"v_exp_f32 v153, v153\n"
"v_exp_f32 v159, v159\n"
"v_exp_f32 v165, v165\n"
"v_mfma_f32_16x16x32_bf16 v[4:7], v[100:103], v[224:227], v[4:7]\n"
"v_cvt_pk_bf16_f32 v56, v56, v57\n"
"v_cvt_pk_bf16_f32 v138, v172, v173\n"
"v_mfma_f32_16x16x32_bf16 v[4:7], v[96:99], a[252:255], v[4:7]\n"
"ds_write_b128 v244, a[232:235] offset:34816\n"
"v_cvt_pk_bf16_f32 v9, v10, v11\n"
"v_mul_f32_dpp v63, v247, v63 row_newbcast:3 row_mask:0xf bank_mask:0xf\n"
"v_cvt_pk_bf16_f32 v130, v168, v169\n"
"v_cvt_pk_bf16_f32 v126, v152, v153\n"
"v_mul_f32_dpp v64, v206, v64 row_newbcast:0 row_mask:0xf bank_mask:0xf\n"
"v_mfma_f32_16x16x32_bf16 a[212:215], v[120:123], a[252:255], a[212:215]\n"
"s_waitcnt lgkmcnt(6)\n"
"v_cvt_pk_bf16_f32 v88, v88, v89\n"
"v_cvt_pk_bf16_f32 v146, v176, v177\n"
"v_mfma_f32_16x16x32_bf16 v[168:171], v[208:211], a[0:3], v[180:183]\n"
"s_waitcnt lgkmcnt(2)\n"
"ds_read_b64_tr_b16 a[246:247], v232 offset:54272\n"
"v_cvt_pk_bf16_f32 v43, v46, v47\n"
"v_mfma_f32_16x16x32_bf16 v[168:171], v[212:215], a[4:7], v[168:171]\n"
"s_waitcnt lgkmcnt(2)\n"
"ds_read_b64_tr_b16 v[224:225], v237 offset:50176\n"
"v_cvt_pk_bf16_f32 v57, v58, v59\n"
"v_mfma_f32_16x16x32_bf16 v[168:171], v[216:219], a[8:11], v[168:171]\n"
"v_exp_f32 v158, v158\n"
"v_mfma_f32_16x16x32_bf16 v[168:171], v[220:223], a[12:15], v[168:171]\n"
"ds_read_b64_tr_b16 a[250:251], v236 offset:54272\n"
"ds_read_b64_tr_b16 a[252:253], v233 offset:50176\n"
"v_cvt_pk_bf16_f32 v42, v44, v45\n"
"v_cvt_pk_bf16_f32 v72, v72, v73\n"
"v_cvt_pk_bf16_f32 v73, v74, v75\n"
"v_mul_f32_dpp v28, v246, v28 row_newbcast:0 row_mask:0xf bank_mask:0xf\n"
"v_mul_f32_dpp v91, v206, v91 row_newbcast:3 row_mask:0xf bank_mask:0xf\n"
"v_exp_f32 v167, v167\n"
"v_mul_f32_dpp v34, v247, v34 row_newbcast:2 row_mask:0xf bank_mask:0xf\n"
"v_mul_f32_dpp v35, v247, v35 row_newbcast:3 row_mask:0xf bank_mask:0xf\n"
"v_mul_f32_dpp v65, v206, v65 row_newbcast:1 row_mask:0xf bank_mask:0xf\n"
"v_mul_f32_dpp v68, v206, v68 row_newbcast:0 row_mask:0xf bank_mask:0xf\n"
"v_mfma_f32_16x16x32_bf16 v[172:175], v[216:219], a[24:27], v[184:187]\n"
"v_cvt_pk_bf16_f32 v10, v12, v13\n"
"v_xor_b32 v244, 0x1000, v244\n"
"v_mfma_f32_16x16x32_bf16 v[172:175], v[220:223], a[28:31], v[172:175]\n"
"ds_read_b64_tr_b16 v[226:227], v237 offset:54272\n"
"v_cvt_pk_bf16_f32 v127, v154, v155\n"
"v_mfma_f32_16x16x32_bf16 v[172:175], v[208:211], a[16:19], v[172:175]\n"
"v_cvt_pk_bf16_f32 v59, v62, v63\n"
"v_mul_f32_dpp v67, v206, v67 row_newbcast:3 row_mask:0xf bank_mask:0xf\n"
"v_mfma_f32_16x16x32_bf16 v[172:175], v[212:215], a[20:23], v[172:175]\n"
"s_cbranch_vccnz LPCT0_%=\nLPKT0_%=:\n"
"ds_write_b128 v248, v[40:43] offset:50176\n"
"v_cvt_pk_bf16_f32 v11, v14, v15\n"
"v_mul_f32_dpp v78, v206, v78 row_newbcast:2 row_mask:0xf bank_mask:0xf\n"
"v_mul_f32_dpp v93, v206, v93 row_newbcast:1 row_mask:0xf bank_mask:0xf\n"
"v_exp_f32 v164, v164\n"
"v_mul_f32_dpp v3, v246, v3 row_newbcast:3 row_mask:0xf bank_mask:0xf\n"
"v_mul_f32_dpp v36, v247, v36 row_newbcast:0 row_mask:0xf bank_mask:0xf\n"
"v_mul_f32_dpp v39, v247, v39 row_newbcast:3 row_mask:0xf bank_mask:0xf\n"
"v_mul_f32_dpp v70, v206, v70 row_newbcast:2 row_mask:0xf bank_mask:0xf\n"
"v_mov_b64 v[198:199], 0\n"
"v_mfma_f32_16x16x32_bf16 v[176:179], v[208:211], a[32:35], v[188:191]\n"
"ds_read_b64_tr_b16 a[68:69], v249 offset:50176\n"
"ds_read_b64_tr_b16 a[254:255], v233 offset:54272\n"
"v_mfma_f32_16x16x32_bf16 v[176:179], v[212:215], a[36:39], v[176:179]\n"
"ds_read_b64_tr_b16 a[70:71], v249 offset:50184\n"
"v_cvt_pk_bf16_f32 v74, v76, v77\n"
"v_mfma_f32_16x16x32_bf16 v[176:179], v[216:219], a[40:43], v[176:179]\n"
"v_mul_f32_dpp v69, v206, v69 row_newbcast:1 row_mask:0xf bank_mask:0xf\n"
"v_mul_f32_dpp v71, v206, v71 row_newbcast:3 row_mask:0xf bank_mask:0xf\n"
"v_mfma_f32_16x16x32_bf16 v[176:179], v[220:223], a[44:47], v[176:179]\n"
"s_waitcnt lgkmcnt(5)\n"
"ds_write_b128 v248, v[8:11] offset:49152\n"
"v_cvt_pk_bf16_f32 v75, v78, v79\n"
"v_cvt_pk_bf16_f32 v27, v30, v31\n"
"v_mul_f32_dpp v38, v247, v38 row_newbcast:2 row_mask:0xf bank_mask:0xf\n"
"v_cvt_pk_bf16_f32 v32, v32, v33\n"
"v_accvgpr_read_b32 v255, a209\n"
"v_mfma_f32_16x16x32_bf16 a[224:227], v[120:123], a[244:247], a[224:227]\n"
"ds_write_b128 v248, v[72:75] offset:51200\n"
"v_cvt_pk_bf16_f32 v89, v90, v91\n"
"v_mul_f32_dpp v5, v246, v5 row_newbcast:1 row_mask:0xf bank_mask:0xf\n"
"v_exp_f32 v170, v170\n"
"v_exp_f32 v175, v175\n"
"v_mfma_f32_16x16x32_bf16 v[16:19], v[96:99], a[244:247], v[16:19]\n"
"s_waitcnt vmcnt(8) lgkmcnt(3)\n"
"ds_read_b64_tr_b16 a[84:85], v249 offset:51200\n"
"v_mul_f32_dpp v37, v247, v37 row_newbcast:1 row_mask:0xf bank_mask:0xf\n"
"v_mfma_f32_16x16x32_bf16 v[16:19], v[100:103], a[248:251], v[16:19]\n"
"s_barrier\n"
"ds_read_b128 v[212:215], v229 offset:16384\n"
"ds_read_b64 v[250:251], v245 offset:52224\n"
"v_cvt_pk_bf16_f32 v26, v28, v29\n"
"v_cvt_pk_bf16_f32 v58, v60, v61\n"
"v_mul_f32_dpp v6, v246, v6 row_newbcast:2 row_mask:0xf bank_mask:0xf\n"
"v_mul_f32_dpp v7, v246, v7 row_newbcast:3 row_mask:0xf bank_mask:0xf\n"
"v_exp_f32 v172, v172\n"
"v_exp_f32 v177, v177\n"
"v_mfma_f32_16x16x32_bf16 v[48:51], v[104:107], a[244:247], v[48:51]\n"
"ds_read_b64_tr_b16 a[86:87], v249 offset:51208\n"
"ds_read_b128 v[208:211], v228 offset:16384\n"
"v_mfma_f32_16x16x32_bf16 v[48:51], v[108:111], a[248:251], v[48:51]\n"
"ds_read_b128 v[216:219], v228 offset:17408\n"
"v_cvt_pk_bf16_f32 v91, v94, v95\n"
"v_mfma_f32_16x16x32_bf16 v[80:83], v[112:115], a[244:247], v[80:83]\n"
"ds_read_b64_tr_b16 a[54:55], v249 offset:49160\n"
"v_cvt_pk_bf16_f32 v90, v92, v93\n"
"v_mfma_f32_16x16x32_bf16 v[80:83], v[116:119], a[248:251], v[80:83]\n"
"ds_read_b64_tr_b16 a[52:53], v249 offset:49152\n"
"ds_read_b128 v[28:31], v244 offset:33792\n"
"v_mfma_f32_16x16x32_bf16 v[84:87], v[116:119], v[224:227], v[84:87]\n"
"ds_read_b128 v[220:223], v229 offset:17408\n"
"v_cvt_pk_bf16_f32 v33, v34, v35\n"
"v_mfma_f32_16x16x32_bf16 v[84:87], v[112:115], a[252:255], v[84:87]\n"
"ds_write_b128 v248, v[56:59] offset:50176\n"
"v_cvt_pk_bf16_f32 v1, v2, v3\n"
"v_cvt_pk_bf16_f32 v64, v64, v65\n"
"v_exp_f32 v171, v171\n"
"v_exp_f32 v176, v176\n"
"v_mov_b64 v[202:203], 0\n"
"v_mfma_f32_16x16x32_bf16 v[52:55], v[108:111], v[224:227], v[52:55]\n"
"s_waitcnt lgkmcnt(8)\n"
"ds_write_b128 v248, v[24:27] offset:49152\n"
"v_cvt_pk_bf16_f32 v65, v66, v67\n"
"v_cvt_pk_bf16_f32 v66, v68, v69\n"
"v_cvt_pk_bf16_f32 v67, v70, v71\n"
"v_exp_f32 v169, v169\n"
"v_min_f32 v254, v252, v250\n"
"v_mfma_f32_16x16x32_bf16 v[52:55], v[104:107], a[252:255], v[52:55]\n"
"ds_write_b128 v248, v[88:91] offset:51200\n"
"v_cvt_pk_bf16_f32 v132, v156, v157\n"
"v_exp_f32 v162, v162\n"
"v_exp_f32 v166, v166\n"
"v_cvt_pk_bf16_f32 v3, v6, v7\n"
"v_exp_f32 v174, v174\n"
"v_mfma_f32_16x16x32_bf16 v[20:23], v[100:103], v[224:227], v[20:23]\n"
"ds_read_b64_tr_b16 a[76:77], v249 offset:50176\n"
"ds_read_b64_tr_b16 a[250:251], v238 offset:4096\n"
"v_mfma_f32_16x16x32_bf16 v[20:23], v[96:99], a[252:255], v[20:23]\n"
"ds_read_b128 v[24:27], v244 offset:32768\n"
"v_sub_f32 v252, v254, v252\n"
"v_mfma_f32_16x16x32_bf16 a[228:231], v[120:123], a[252:255], a[228:231]\n"
"s_waitcnt lgkmcnt(9)\n"
"ds_read_b64_tr_b16 a[62:63], v249 offset:49160\n"
"ds_read_b64_tr_b16 a[92:93], v249 offset:51200\n"
"ds_read_b64_tr_b16 a[94:95], v249 offset:51208\n"
"v_exp_f32 v163, v163\n"
"v_mul_f32_dpp v4, v246, v4 row_newbcast:0 row_mask:0xf bank_mask:0xf\n"
"v_cvt_pk_bf16_f32 v34, v36, v37\n"
"v_exp_f32 v178, v178\n"
"v_sub_f32 v250, v254, v250\n"
"v_exp_f32 v252, v252\n"
"v_mfma_f32_16x16x32_bf16 v[152:155], v[208:211], a[0:3], v[180:183]\n"
"ds_read_b64_tr_b16 a[78:79], v249 offset:50184\n"
"ds_read_b128 v[60:63], v244 offset:35840\n"
"v_mfma_f32_16x16x32_bf16 v[152:155], v[212:215], a[4:7], v[152:155]\n"
"ds_write_b128 v248, v[64:67] offset:51200\n"
"v_cvt_pk_bf16_f32 v133, v158, v159\n"
"v_cvt_pk_bf16_f32 v2, v4, v5\n"
"v_cvt_pk_bf16_f32 v35, v38, v39\n"
"v_exp_f32 v250, v250\n"
"v_mul_f32 v253, v253, v252\n"
"v_mfma_f32_16x16x32_bf16 v[152:155], v[216:219], a[8:11], v[152:155]\n"
"s_waitcnt lgkmcnt(11)\n"
"ds_read_b64_tr_b16 a[60:61], v249 offset:49152\n"
"ds_read_b64_tr_b16 a[82:83], v249 offset:51208\n"
"v_mfma_f32_16x16x32_bf16 v[152:155], v[220:223], a[12:15], v[152:155]\n"
"ds_write_b128 v248, v[0:3] offset:49152\n"
"ds_write_b128 v248, v[32:35] offset:50176\n"
"v_mul_f32_dpp v50, v247, v50 row_newbcast:2 row_mask:0xf bank_mask:0xf\n"
"v_mfma_f32_16x16x32_bf16 v[156:159], v[216:219], a[24:27], v[184:187]\n"
"ds_read_b64_tr_b16 a[66:67], v249 offset:50184\n"
"ds_read_b64_tr_b16 a[80:81], v249 offset:51200\n"
"v_mfma_f32_16x16x32_bf16 v[156:159], v[220:223], a[28:31], v[156:159]\n"
"ds_read_b64_tr_b16 a[48:49], v249 offset:49152\n"
"ds_read_b128 v[56:59], v244 offset:34816\n"
"v_mfma_f32_16x16x32_bf16 v[156:159], v[208:211], a[16:19], v[156:159]\n"
"ds_read_b64_tr_b16 v[224:225], v239 offset:0\n"
"v_cvt_pk_bf16_f32 v143, v174, v175\n"
"v_mfma_f32_16x16x32_bf16 v[156:159], v[212:215], a[20:23], v[156:159]\n"
"ds_read_b64_tr_b16 a[50:51], v249 offset:49160\n"
"ds_read_b64_tr_b16 a[64:65], v249 offset:50176\n"
"ds_read_b64_tr_b16 a[244:245], v234 offset:0\n"
"ds_read_b64_tr_b16 a[246:247], v234 offset:4096\n"
"v_cvt_pk_bf16_f32 v140, v160, v161\n"
"v_cvt_pk_bf16_f32 v141, v162, v163\n"
"v_cvt_pk_bf16_f32 v135, v170, v171\n"
"v_cvt_pk_bf16_f32 v150, v176, v177\n"
"v_fmac_f32 v253, v251, v250\n"
"v_xor_b32 v248, s75, v248\n"
"v_mfma_f32_16x16x32_bf16 v[160:163], v[208:211], a[32:35], v[188:191]\n"
"ds_read_b64_tr_b16 a[248:249], v238 offset:0\n"
"v_mul_f32_dpp v51, v247, v51 row_newbcast:3 row_mask:0xf bank_mask:0xf\n"
"v_mfma_f32_16x16x32_bf16 v[160:163], v[212:215], a[36:39], v[160:163]\n"
"v_cvt_pk_bf16_f32 v148, v164, v165\n"
"v_cvt_pk_bf16_f32 v149, v166, v167\n"
"v_mfma_f32_16x16x32_bf16 v[160:163], v[216:219], a[40:43], v[160:163]\n"
"v_rcp_f32 v253, v253\n"
"v_mfma_f32_16x16x32_bf16 v[160:163], v[220:223], a[44:47], v[160:163]\n"
"ds_read_b64_tr_b16 a[252:253], v235 offset:0\n"
"v_mul_f32 v250, v250, v253\n"
"v_mfma_f32_16x16x32_bf16 v[164:167], v[216:219], a[200:203], v[192:195]\n"
"ds_read_b64_tr_b16 v[226:227], v239 offset:4096\n"
"v_mul_f32 v252, v252, v253\n"
"v_mfma_f32_16x16x32_bf16 v[164:167], v[220:223], a[204:207], v[164:167]\n"
"v_exp_f32 v168, v168\n"
"v_mfma_f32_16x16x32_bf16 v[164:167], v[208:211], a[192:195], v[164:167]\n"
"ds_read_b128 v[216:219], v228 offset:21504\n"
"v_mov_b32_dpp v250, v250 row_ror:12 row_mask:0x2 bank_mask:0xf\n"
"v_mfma_f32_16x16x32_bf16 v[164:167], v[212:215], a[196:199], v[164:167]\n"
"s_waitcnt lgkmcnt(4)\n"
"ds_read_b64_tr_b16 a[254:255], v235 offset:4096\n"
"v_exp_f32 v173, v173\n"
"v_mov_b32_dpp v252, v252 row_ror:12 row_mask:0x2 bank_mask:0xf\n"
"v_accvgpr_read_b32 v254, a208\n"
"v_mul_f32_dpp v22, v246, v22 row_newbcast:2 row_mask:0xf bank_mask:0xf\n"
"v_mul_f32_dpp v80, v206, v80 row_newbcast:0 row_mask:0xf bank_mask:0xf\n"
"v_exp_f32 v156, v156\n"
"v_mfma_f32_16x16x32_bf16 a[216:219], v[124:127], a[244:247], 0\n"
"ds_read_b128 v[220:223], v229 offset:21504\n"
"v_cvt_pk_bf16_f32 v134, v168, v169\n"
"v_cvt_pk_bf16_f32 v142, v172, v173\n"
"v_mov_b32_dpp v250, v250 row_ror:8 row_mask:0x4 bank_mask:0xf\n"
"v_accvgpr_read_b32 v251, a211\n"
"v_mul_f32_dpp v84, v206, v84 row_newbcast:0 row_mask:0xf bank_mask:0xf\n"
"v_mov_b64 v[196:197], 0\n"
"v_exp_f32 v154, v154\n"
"v_exp_f32 v157, v157\n"
"v_mfma_f32_16x16x32_bf16 v[8:11], v[128:131], a[244:247], 0\n"
"s_waitcnt lgkmcnt(5)\n"
"s_add_u32 m0, s44, 98304\n"
"v_exp_f32 v179, v179\n"
"v_mfma_f32_16x16x32_bf16 v[8:11], v[132:135], a[248:251], v[8:11]\n"
"buffer_load_dwordx4 v240, s[20:23], s56 offen nt lds\n"
"v_mfma_f32_16x16x32_bf16 v[40:43], v[136:139], a[244:247], 0\n"
"v_exp_f32 v159, v159\n"
"v_mfma_f32_16x16x32_bf16 v[40:43], v[140:143], a[248:251], v[40:43]\n"
"v_cvt_pk_bf16_f32 v151, v178, v179\n"
"v_mov_b32_dpp v252, v252 row_ror:8 row_mask:0x4 bank_mask:0xf\n"
"v_mfma_f32_16x16x32_bf16 v[72:75], v[144:147], a[244:247], 0\n"
"v_mov_b32_dpp v250, v250 row_ror:4 row_mask:0x8 bank_mask:0xf\n"
"v_mul_f32_dpp v23, v246, v23 row_newbcast:3 row_mask:0xf bank_mask:0xf\n"
"v_mfma_f32_16x16x32_bf16 v[72:75], v[148:151], a[248:251], v[72:75]\n"
"s_waitcnt lgkmcnt(2)\n"
"ds_read_b128 v[212:215], v229 offset:20480\n"
"v_accvgpr_read_b32 v253, a210\n"
"v_mul_f32_dpp v27, v250, v27 row_newbcast:3 row_mask:0xf bank_mask:0xf\n"
"v_mul_f32_dpp v21, v246, v21 row_newbcast:1 row_mask:0xf bank_mask:0xf\n"
"v_mul_f32_dpp v86, v206, v86 row_newbcast:2 row_mask:0xf bank_mask:0xf\n"
"v_mul_f32_dpp v57, v250, v57 row_newbcast:1 row_mask:0xf bank_mask:0xf\n"
"v_exp_f32 v158, v158\n"
"v_exp_f32 v161, v161\n"
"v_mfma_f32_16x16x32_bf16 v[76:79], v[148:151], v[224:227], 0\n"
"s_waitcnt lgkmcnt(2)\n"
"ds_read_b128 v[208:211], v228 offset:20480\n"
"v_mov_b32_dpp v252, v252 row_ror:4 row_mask:0x8 bank_mask:0xf\n"
"v_mfma_f32_16x16x32_bf16 v[76:79], v[144:147], a[252:255], v[76:79]\n"
"ds_read_b64_tr_b16 a[248:249], v238 offset:1024\n"
"v_mul_f32_dpp v24, v250, v24 row_newbcast:0 row_mask:0xf bank_mask:0xf\n"
"v_mfma_f32_16x16x32_bf16 v[44:47], v[140:143], v[224:227], 0\n"
"v_mul_f32_dpp v26, v250, v26 row_newbcast:2 row_mask:0xf bank_mask:0xf\n"
"v_mul_f32_dpp v30, v250, v30 row_newbcast:2 row_mask:0xf bank_mask:0xf\n"
"v_mfma_f32_16x16x32_bf16 v[44:47], v[136:139], a[252:255], v[44:47]\n"
"ds_read_b64_tr_b16 a[244:245], v234 offset:1024\n"
"v_fmac_f32_dpp v26, v252, v253 row_newbcast:2 row_mask:0xf bank_mask:0xf\n"
"v_mfma_f32_16x16x32_bf16 v[12:15], v[132:135], v[224:227], 0\n"
"s_add_u32 s40, s56, s52\n"
"ds_read_b64_tr_b16 v[224:225], v239 offset:1024\n"
"v_mul_f32_dpp v25, v250, v25 row_newbcast:1 row_mask:0xf bank_mask:0xf\n"
"v_mfma_f32_16x16x32_bf16 v[12:15], v[128:131], a[252:255], v[12:15]\n"
"buffer_load_dwordx4 v240, s[20:23], s40 offen offset:1024 nt lds\n"
"v_fmac_f32_dpp v24, v252, v254 row_newbcast:0 row_mask:0xf bank_mask:0xf\n"
"v_fmac_f32_dpp v25, v252, v255 row_newbcast:1 row_mask:0xf bank_mask:0xf\n"
"v_accvgpr_read_b32 v254, a212\n"
"v_mul_f32_dpp v31, v250, v31 row_newbcast:3 row_mask:0xf bank_mask:0xf\n"
"v_mul_f32_dpp v62, v250, v62 row_newbcast:2 row_mask:0xf bank_mask:0xf\n"
"v_mul_f32_dpp v63, v250, v63 row_newbcast:3 row_mask:0xf bank_mask:0xf\n"
"v_exp_f32 v163, v163\n"
"v_mfma_f32_16x16x32_bf16 a[220:223], v[124:127], a[252:255], 0\n"
"v_mul_f32_dpp v28, v250, v28 row_newbcast:0 row_mask:0xf bank_mask:0xf\n"
"v_mul_f32_dpp v60, v250, v60 row_newbcast:0 row_mask:0xf bank_mask:0xf\n"
"v_smfmac_f32_16x16x64_bf16 v[196:199], a[240:243], v[128:135], v241\n"
"v_accvgpr_read_b32 v253, a214\n"
"v_mul_f32_dpp v53, v247, v53 row_newbcast:1 row_mask:0xf bank_mask:0xf\n"
"v_mfma_f32_16x16x32_bf16 v[202:205], a[240:243], v[124:127], v[202:205]\n"
"ds_read_b64_tr_b16 a[250:251], v238 offset:5120\n"
"v_fmac_f32_dpp v28, v252, v254 row_newbcast:0 row_mask:0xf bank_mask:0xf\n"
"v_mfma_f32_16x16x32_bf16 v[168:171], v[216:219], a[8:11], v[180:183]\n"
"s_waitcnt lgkmcnt(3)\n"
"v_exp_f32 v155, v155\n"
"v_mfma_f32_16x16x32_bf16 v[168:171], v[220:223], a[12:15], v[168:171]\n"
"ds_read_b64_tr_b16 a[246:247], v234 offset:5120\n"
"v_mul_f32_dpp v85, v206, v85 row_newbcast:1 row_mask:0xf bank_mask:0xf\n"
"v_mfma_f32_16x16x32_bf16 v[168:171], v[208:211], a[0:3], v[168:171]\n"
"v_mul_f32_dpp v29, v250, v29 row_newbcast:1 row_mask:0xf bank_mask:0xf\n"
"v_mul_f32_dpp v58, v250, v58 row_newbcast:2 row_mask:0xf bank_mask:0xf\n"
"v_mfma_f32_16x16x32_bf16 v[168:171], v[212:215], a[4:7], v[168:171]\n"
"v_fmac_f32_dpp v27, v252, v251 row_newbcast:3 row_mask:0xf bank_mask:0xf\n"
"v_accvgpr_read_b32 v255, a213\n"
"v_accvgpr_read_b32 v251, a215\n"
"v_fmac_f32_dpp v30, v252, v253 row_newbcast:2 row_mask:0xf bank_mask:0xf\n"
"v_cvt_pk_bf16_f32 v24, v24, v25\n"
"v_cvt_pk_bf16_f32 v25, v26, v27\n"
"v_accvgpr_read_b32 v254, a224\n"
"v_mul_f32_dpp v61, v250, v61 row_newbcast:1 row_mask:0xf bank_mask:0xf\n"
"v_exp_f32 v165, v165\n"
"v_mfma_f32_16x16x32_bf16 v[172:175], v[208:211], a[16:19], v[184:187]\n"
"ds_read_b64_tr_b16 v[226:227], v239 offset:5120\n"
"v_accvgpr_read_b32 v253, a226\n"
"v_mfma_f32_16x16x32_bf16 v[172:175], v[212:215], a[20:23], v[172:175]\n"
"v_fmac_f32_dpp v29, v252, v255 row_newbcast:1 row_mask:0xf bank_mask:0xf\n"
"v_mul_f32_dpp v56, v250, v56 row_newbcast:0 row_mask:0xf bank_mask:0xf\n"
"v_mfma_f32_16x16x32_bf16 v[172:175], v[216:219], a[24:27], v[172:175]\n"
"s_add_u32 s40, s56, s53\n"
"ds_read_b64_tr_b16 a[254:255], v235 offset:5120\n"
"v_mul_f32_dpp v59, v250, v59 row_newbcast:3 row_mask:0xf bank_mask:0xf\n"
"v_mfma_f32_16x16x32_bf16 v[172:175], v[220:223], a[28:31], v[172:175]\n"
"buffer_load_dwordx4 v240, s[20:23], s40 offen offset:2048 nt lds\n"
"v_fmac_f32_dpp v31, v252, v251 row_newbcast:3 row_mask:0xf bank_mask:0xf\n"
"v_mul_f32_dpp v16, v246, v16 row_newbcast:0 row_mask:0xf bank_mask:0xf\n"
"v_mul_f32_dpp v48, v247, v48 row_newbcast:0 row_mask:0xf bank_mask:0xf\n"
"v_mul_f32_dpp v49, v247, v49 row_newbcast:1 row_mask:0xf bank_mask:0xf\n"
"v_accvgpr_read_b32 v255, a225\n"
"v_accvgpr_read_b32 v251, a227\n"
"v_fmac_f32_dpp v56, v252, v254 row_newbcast:0 row_mask:0xf bank_mask:0xf\n"
"v_exp_f32 v167, v167\n"
"v_exp_f32 v171, v171\n"
"v_mfma_f32_16x16x32_bf16 v[176:179], v[216:219], a[40:43], v[188:191]\n"
"v_cvt_pk_bf16_f32 v26, v28, v29\n"
"v_mul_f32_dpp v81, v206, v81 row_newbcast:1 row_mask:0xf bank_mask:0xf\n"
"v_fmac_f32_dpp v57, v252, v255 row_newbcast:1 row_mask:0xf bank_mask:0xf\n"
"v_fmac_f32_dpp v58, v252, v253 row_newbcast:2 row_mask:0xf bank_mask:0xf\n"
"v_fmac_f32_dpp v59, v252, v251 row_newbcast:3 row_mask:0xf bank_mask:0xf\n"
"v_accvgpr_read_b32 v254, a228\n"
"v_accvgpr_read_b32 v255, a229\n"
"v_accvgpr_read_b32 v253, a230\n"
"v_accvgpr_read_b32 v251, a231\n"
"v_cvt_pk_bf16_f32 v56, v56, v57\n"
"v_exp_f32 v152, v152\n"
"v_exp_f32 v153, v153\n"
"v_cvt_pk_bf16_f32 v97, v154, v155\n"
"v_mfma_f32_16x16x32_bf16 v[176:179], v[220:223], a[44:47], v[176:179]\n"
"v_mul_f32_dpp v54, v247, v54 row_newbcast:2 row_mask:0xf bank_mask:0xf\n"
"v_fmac_f32_dpp v60, v252, v254 row_newbcast:0 row_mask:0xf bank_mask:0xf\n"
"v_mfma_f32_16x16x32_bf16 v[176:179], v[208:211], a[32:35], v[176:179]\n"
"v_cvt_pk_bf16_f32 v27, v30, v31\n"
"v_mul_f32_dpp v20, v246, v20 row_newbcast:0 row_mask:0xf bank_mask:0xf\n"
"v_mfma_f32_16x16x32_bf16 v[176:179], v[212:215], a[36:39], v[176:179]\n"
"ds_write_b128 v243, v[24:27] offset:0\n"
"v_mul_f32_dpp v87, v206, v87 row_newbcast:3 row_mask:0xf bank_mask:0xf\n"
"v_fmac_f32_dpp v61, v252, v255 row_newbcast:1 row_mask:0xf bank_mask:0xf\n"
"v_fmac_f32_dpp v62, v252, v253 row_newbcast:2 row_mask:0xf bank_mask:0xf\n"
"v_fmac_f32_dpp v63, v252, v251 row_newbcast:3 row_mask:0xf bank_mask:0xf\n"
"v_cvt_pk_bf16_f32 v57, v58, v59\n"
"v_cvt_pk_bf16_f32 v58, v60, v61\n"
"v_cvt_pk_bf16_f32 v59, v62, v63\n"
"v_cvt_pk_bf16_f32 v96, v152, v153\n"
"v_mfma_f32_16x16x32_bf16 v[152:155], v[208:211], a[192:195], v[192:195]\n"
"ds_write_b128 v243, v[56:59] offset:2048\n"
"v_cvt_pk_bf16_f32 v105, v158, v159\n"
"v_exp_f32 v162, v162\n"
"v_exp_f32 v172, v172\n"
"v_mfma_f32_16x16x32_bf16 v[152:155], v[212:215], a[196:199], v[152:155]\n"
"ds_read_b64_tr_b16 a[252:253], v235 offset:1024\n"
"v_mul_f32_dpp v18, v246, v18 row_newbcast:2 row_mask:0xf bank_mask:0xf\n"
"v_mfma_f32_16x16x32_bf16 v[152:155], v[216:219], a[200:203], v[152:155]\n"
"v_exp_f32 v174, v174\n"
"v_mfma_f32_16x16x32_bf16 v[152:155], v[220:223], a[204:207], v[152:155]\n"
"s_waitcnt lgkmcnt(3)\n"
"ds_read_b128 v[212:215], v231 offset:16384\n"
"v_mul_f32_dpp v55, v247, v55 row_newbcast:3 row_mask:0xf bank_mask:0xf\n"
"v_mfma_f32_16x16x32_bf16 a[232:235], v[124:127], a[244:247], 0\n"
"v_exp_f32 v169, v169\n"
"v_mfma_f32_16x16x32_bf16 v[24:27], v[128:131], a[244:247], 0\n"
"s_add_u32 s40, s56, s54\n"
"ds_read_b128 v[208:211], v230 offset:16384\n"
"v_mul_f32_dpp v17, v246, v17 row_newbcast:1 row_mask:0xf bank_mask:0xf\n"
"v_mfma_f32_16x16x32_bf16 v[24:27], v[132:135], a[248:251], v[24:27]\n"
"buffer_load_dwordx4 v240, s[20:23], s40 offen offset:3072 nt lds\n"
"v_mfma_f32_16x16x32_bf16 v[56:59], v[136:139], a[244:247], 0\n"
"s_add_u32 s55, s55, s59\n"
"v_exp_f32 v173, v173\n"
"v_mfma_f32_16x16x32_bf16 v[56:59], v[140:143], a[248:251], v[56:59]\n"
"s_add_u32 s41, s55, s57\n"
"v_exp_f32 v160, v160\n"
"v_mfma_f32_16x16x32_bf16 v[88:91], v[144:147], a[244:247], 0\n"
"s_and_b32 s41, s41, s58\n"
"v_exp_f32 v166, v166\n"
"v_mfma_f32_16x16x32_bf16 v[88:91], v[148:151], a[248:251], v[88:91]\n"
"s_andn2_b32 s56, s55, s58\n"
"ds_read_b64_tr_b16 a[248:249], v236 offset:0\n"
"v_mov_b64 v[200:201], 0\n"
"v_mfma_f32_16x16x32_bf16 v[92:95], v[148:151], v[224:227], 0\n"
"s_waitcnt lgkmcnt(2)\n"
"s_or_b32 s56, s56, s41\n"
"ds_read_b128 v[220:223], v231 offset:17408\n"
"v_cvt_pk_bf16_f32 v16, v16, v17\n"
"v_mfma_f32_16x16x32_bf16 v[92:95], v[144:147], a[252:255], v[92:95]\n"
"ds_read_b128 v[216:219], v230 offset:17408\n"
"v_exp_f32 v164, v164\n"
"v_exp_f32 v178, v178\n"
"v_exp_f32 v179, v179\n"
"v_mfma_f32_16x16x32_bf16 v[60:63], v[140:143], v[224:227], 0\n"
"s_add_u32 m0, s44, 49152\n"
"ds_read_b64_tr_b16 a[244:245], v232 offset:0\n"
"v_mul_f32_dpp v19, v246, v19 row_newbcast:3 row_mask:0xf bank_mask:0xf\n"
"v_mfma_f32_16x16x32_bf16 v[60:63], v[136:139], a[252:255], v[60:63]\n"
"buffer_load_dwordx4 v240, s[16:19], s56 offen nt lds\n"
"v_mfma_f32_16x16x32_bf16 v[28:31], v[132:135], v[224:227], 0\n"
"v_mul_f32_dpp v82, v206, v82 row_newbcast:2 row_mask:0xf bank_mask:0xf\n"
"v_cvt_pk_bf16_f32 v112, v160, v161\n"
"v_mfma_f32_16x16x32_bf16 v[28:31], v[128:131], a[252:255], v[28:31]\n"
"v_exp_f32 v154, v154\n"
"v_mfma_f32_16x16x32_bf16 a[236:239], v[124:127], a[252:255], 0\n"
"v_cvt_pk_bf16_f32 v17, v18, v19\n"
"v_cvt_pk_bf16_f32 v104, v156, v157\n"
"v_smfmac_f32_16x16x64_bf16 v[198:201], a[240:243], v[136:143], v241\n"
"s_waitcnt lgkmcnt(3)\n"
"ds_read_b64_tr_b16 v[226:227], v237 offset:4096\n"
"v_cvt_pk_bf16_f32 v48, v48, v49\n"
"v_mfma_f32_16x16x32_bf16 v[156:159], v[208:211], a[0:3], v[180:183]\n"
"v_mul_f32_dpp v52, v247, v52 row_newbcast:0 row_mask:0xf bank_mask:0xf\n"
"v_cvt_pk_bf16_f32 v120, v164, v165\n"
"v_mfma_f32_16x16x32_bf16 v[156:159], v[212:215], a[4:7], v[156:159]\n"
"s_waitcnt lgkmcnt(2)\n"
"ds_read_b64_tr_b16 v[224:225], v237 offset:0\n"
"v_cvt_pk_bf16_f32 v113, v162, v163\n"
"v_mfma_f32_16x16x32_bf16 v[156:159], v[216:219], a[8:11], v[156:159]\n"
"v_exp_f32 v176, v176\n"
"v_mfma_f32_16x16x32_bf16 v[156:159], v[220:223], a[12:15], v[156:159]\n"
"ds_read_b64_tr_b16 a[246:247], v232 offset:4096\n"
"ds_read_b64_tr_b16 a[254:255], v233 offset:4096\n"
"v_mfma_f32_16x16x32_bf16 v[160:163], v[216:219], a[24:27], v[184:187]\n"
"ds_read_b64_tr_b16 a[250:251], v236 offset:4096\n"
"v_cvt_pk_bf16_f32 v49, v50, v51\n"
"v_mfma_f32_16x16x32_bf16 v[160:163], v[220:223], a[28:31], v[160:163]\n"
"v_exp_f32 v168, v168\n"
"v_mfma_f32_16x16x32_bf16 v[160:163], v[208:211], a[16:19], v[160:163]\n"
"s_add_u32 s40, s56, s52\n"
"ds_read_b64_tr_b16 a[252:253], v233 offset:0\n"
"v_cvt_pk_bf16_f32 v121, v166, v167\n"
"v_mfma_f32_16x16x32_bf16 v[160:163], v[212:215], a[20:23], v[160:163]\n"
"buffer_load_dwordx4 v240, s[16:19], s40 offen offset:1024 nt lds\n"
"v_mfma_f32_16x16x32_bf16 v[164:167], v[208:211], a[32:35], v[188:191]\n"
"v_cvt_pk_bf16_f32 v51, v54, v55\n"
"v_add_u32 v243, 0x1000, v243\n"
"v_mfma_f32_16x16x32_bf16 v[164:167], v[212:215], a[36:39], v[164:167]\n"
"v_exp_f32 v177, v177\n"
"v_mfma_f32_16x16x32_bf16 v[164:167], v[216:219], a[40:43], v[164:167]\n"
"ds_read_b128 v[208:211], v230 offset:20480\n"
"v_cvt_pk_bf16_f32 v115, v178, v179\n"
"v_mfma_f32_16x16x32_bf16 v[164:167], v[220:223], a[44:47], v[164:167]\n"
"s_waitcnt lgkmcnt(3)\n"
"v_cvt_pk_bf16_f32 v50, v52, v53\n"
"v_mul_f32_dpp v83, v206, v83 row_newbcast:3 row_mask:0xf bank_mask:0xf\n"
"v_mfma_f32_16x16x32_bf16 a[208:211], v[124:127], a[244:247], 0\n"
"v_cvt_pk_bf16_f32 v19, v22, v23\n"
"v_cvt_pk_bf16_f32 v114, v176, v177\n"
"v_mfma_f32_16x16x32_bf16 v[0:3], v[128:131], a[244:247], 0\n"
"s_waitcnt lgkmcnt(2)\n"
"ds_read_b128 v[216:219], v230 offset:21504\n"
"v_xor_b32 v249, s75, v249\n"
"v_mfma_f32_16x16x32_bf16 v[0:3], v[132:135], a[248:251], v[0:3]\n"
"v_exp_f32 v175, v175\n"
"v_mfma_f32_16x16x32_bf16 v[32:35], v[136:139], a[244:247], 0\n"
"s_add_u32 s40, s56, s53\n"
"ds_read_b128 v[212:215], v231 offset:20480\n"
"v_cvt_pk_bf16_f32 v18, v20, v21\n"
"v_mfma_f32_16x16x32_bf16 v[32:35], v[140:143], a[248:251], v[32:35]\n"
"buffer_load_dwordx4 v240, s[16:19], s40 offen offset:2048 nt lds\n"
"v_mfma_f32_16x16x32_bf16 v[64:67], v[144:147], a[244:247], 0\n"
"v_cvt_pk_bf16_f32 v98, v168, v169\n"
"v_cvt_pk_bf16_f32 v107, v174, v175\n"
"v_mfma_f32_16x16x32_bf16 v[64:67], v[148:151], a[248:251], v[64:67]\n"
"v_exp_f32 v153, v153\n"
"v_mfma_f32_16x16x32_bf16 v[68:71], v[148:151], v[224:227], 0\n"
"s_waitcnt lgkmcnt(2)\n"
"ds_read_b128 v[220:223], v231 offset:21504\n"
"v_cvt_pk_bf16_f32 v106, v172, v173\n"
"v_mfma_f32_16x16x32_bf16 v[68:71], v[144:147], a[252:255], v[68:71]\n"
"ds_write_b128 v248, v[48:51] offset:50176\n"
"v_cvt_pk_bf16_f32 v80, v80, v81\n"
"v_cvt_pk_bf16_f32 v81, v82, v83\n"
"v_exp_f32 v170, v170\n"
"v_exp_f32 v159, v159\n"
"v_mfma_f32_16x16x32_bf16 v[36:39], v[140:143], v[224:227], 0\n"
"v_exp_f32 v167, v167\n"
"v_mfma_f32_16x16x32_bf16 v[36:39], v[136:139], a[252:255], v[36:39]\n"
"ds_read_b64_tr_b16 a[74:75], v249 offset:50184\n"
"ds_read_b64_tr_b16 a[244:245], v232 offset:1024\n"
"v_mfma_f32_16x16x32_bf16 v[4:7], v[132:135], v[224:227], 0\n"
"ds_read_b64_tr_b16 a[72:73], v249 offset:50176\n"
"v_cvt_pk_bf16_f32 v83, v86, v87\n"
"v_mfma_f32_16x16x32_bf16 v[4:7], v[128:131], a[252:255], v[4:7]\n"
"v_exp_f32 v166, v166\n"
"v_mfma_f32_16x16x32_bf16 a[212:215], v[124:127], a[252:255], 0\n"
"s_add_u32 s40, s56, s54\n"
"ds_write_b128 v248, v[16:19] offset:49152\n"
"v_cvt_pk_bf16_f32 v99, v170, v171\n"
"v_exp_f32 v161, v161\n"
"v_exp_f32 v164, v164\n"
"v_smfmac_f32_16x16x64_bf16 v[200:203], a[240:243], v[144:151], v241\n"
"buffer_load_dwordx4 v240, s[16:19], s40 offen offset:3072 nt lds\n"
"v_mfma_f32_16x16x32_bf16 v[168:171], v[208:211], a[0:3], v[180:183]\n"
"s_waitcnt lgkmcnt(5)\n"
"ds_read_b64_tr_b16 a[58:59], v249 offset:49160\n"
"ds_read_b64_tr_b16 a[246:247], v232 offset:5120\n"
"v_mfma_f32_16x16x32_bf16 v[168:171], v[212:215], a[4:7], v[168:171]\n"
"ds_read_b64_tr_b16 a[56:57], v249 offset:49152\n"
"v_cvt_pk_bf16_f32 v82, v84, v85\n"
"v_mfma_f32_16x16x32_bf16 v[168:171], v[216:219], a[8:11], v[168:171]\n"
"ds_write_b128 v248, v[80:83] offset:51200\n"
"v_exp_f32 v156, v156\n"
"v_exp_f32 v158, v158\n"
"v_exp_f32 v165, v165\n"
"v_mfma_f32_16x16x32_bf16 v[168:171], v[220:223], a[12:15], v[168:171]\n"
"ds_read_b64_tr_b16 a[88:89], v249 offset:51200\n"
"ds_read_b64_tr_b16 a[90:91], v249 offset:51208\n"
"v_mfma_f32_16x16x32_bf16 v[172:175], v[216:219], a[24:27], v[184:187]\n"
"ds_read_b64_tr_b16 a[248:249], v236 offset:1024\n"
"ds_read_b64_tr_b16 a[250:251], v236 offset:5120\n"
"v_mfma_f32_16x16x32_bf16 v[172:175], v[220:223], a[28:31], v[172:175]\n"
"v_xor_b32 v248, s75, v248\n"
"v_cvt_pk_bf16_f32 v117, v166, v167\n"
"v_mfma_f32_16x16x32_bf16 v[172:175], v[208:211], a[16:19], v[172:175]\n"
"ds_read_b64_tr_b16 v[226:227], v237 offset:5120\n"
"v_mfma_f32_16x16x32_bf16 v[172:175], v[212:215], a[20:23], v[172:175]\n"
"s_cbranch_vccnz LPCT1_%=\nLPKT1_%=:\n"
"ds_read_b64_tr_b16 a[254:255], v233 offset:5120\n"
"v_mfma_f32_16x16x32_bf16 v[176:179], v[208:211], a[32:35], v[188:191]\n"
"v_exp_f32 v163, v163\n"
"v_mfma_f32_16x16x32_bf16 v[176:179], v[212:215], a[36:39], v[176:179]\n"
"ds_read_b64_tr_b16 v[224:225], v237 offset:1024\n"
"v_cvt_pk_bf16_f32 v101, v158, v159\n"
"v_mfma_f32_16x16x32_bf16 v[176:179], v[216:219], a[40:43], v[176:179]\n"
"v_exp_f32 v175, v175\n"
"v_mfma_f32_16x16x32_bf16 v[176:179], v[220:223], a[44:47], v[176:179]\n"
"s_waitcnt lgkmcnt(5)\n"
"ds_read_b64_tr_b16 a[252:253], v233 offset:1024\n"
"v_mfma_f32_16x16x32_bf16 a[224:227], v[124:127], a[244:247], 0\n"
"v_exp_f32 v160, v160\n"
"v_mfma_f32_16x16x32_bf16 v[16:19], v[128:131], a[244:247], 0\n"
"s_waitcnt vmcnt(8) lgkmcnt(4)\n"
"v_exp_f32 v173, v173\n"
"v_mfma_f32_16x16x32_bf16 v[16:19], v[132:135], a[248:251], v[16:19]\n"
"s_barrier\n"
"ds_read_b128 v[216:219], v228 offset:33792\n"
"v_exp_f32 v155, v155\n"
"v_exp_f32 v157, v157\n"
"v_mfma_f32_16x16x32_bf16 v[48:51], v[136:139], a[244:247], 0\n"
"ds_read_b128 v[220:223], v229 offset:33792\n"
"v_cvt_pk_bf16_f32 v100, v156, v157\n"
"v_mfma_f32_16x16x32_bf16 v[48:51], v[140:143], a[248:251], v[48:51]\n"
"ds_read_b128 v[208:211], v228 offset:32768\n"
"ds_read_b128 v[212:215], v229 offset:32768\n"
"v_mfma_f32_16x16x32_bf16 v[80:83], v[144:147], a[244:247], 0\n"
"ds_read_b64_tr_b16 a[244:245], v234 offset:16384\n"
"ds_read_b64_tr_b16 a[246:247], v234 offset:20480\n"
"v_mfma_f32_16x16x32_bf16 v[80:83], v[148:151], a[248:251], v[80:83]\n"
"s_waitcnt lgkmcnt(7)\n"
"ds_read_b64_tr_b16 a[248:249], v238 offset:16384\n"
"ds_read_b64_tr_b16 a[250:251], v238 offset:20480\n"
"v_mfma_f32_16x16x32_bf16 v[84:87], v[148:151], v[224:227], 0\n"
"s_waitcnt lgkmcnt(8)\n"
"v_exp_f32 v162, v162\n"
"v_mfma_f32_16x16x32_bf16 v[84:87], v[144:147], a[252:255], v[84:87]\n"
"v_exp_f32 v152, v152\n"
"v_mfma_f32_16x16x32_bf16 v[52:55], v[140:143], v[224:227], 0\n"
"v_exp_f32 v178, v178\n"
"v_mfma_f32_16x16x32_bf16 v[52:55], v[136:139], a[252:255], v[52:55]\n"
"v_cvt_pk_bf16_f32 v122, v152, v153\n"
"v_cvt_pk_bf16_f32 v109, v162, v163\n"
"v_mfma_f32_16x16x32_bf16 v[20:23], v[132:135], v[224:227], 0\n"
"ds_read_b64_tr_b16 v[226:227], v239 offset:20480\n"
"v_cvt_pk_bf16_f32 v123, v154, v155\n"
"v_mfma_f32_16x16x32_bf16 v[20:23], v[128:131], a[252:255], v[20:23]\n"
"v_exp_f32 v169, v169\n"
"v_mfma_f32_16x16x32_bf16 a[228:231], v[124:127], a[252:255], 0\n"
"s_waitcnt lgkmcnt(3)\n"
"v_xor_b32 v249, s75, v249\n"
"s_branch LAY2_%=\n"
"LAX_%=:\n"
"v_mfma_f32_16x16x32_bf16 a[224:227], v[124:127], a[244:247], a[224:227]\n"
"v_exp_f32 v168, v168\n"
"v_mfma_f32_16x16x32_bf16 v[16:19], v[128:131], a[244:247], v[16:19]\n"
"s_waitcnt vmcnt(8) lgkmcnt(0)\n"
"v_exp_f32 v169, v169\n"
"v_mfma_f32_16x16x32_bf16 v[16:19], v[132:135], a[248:251], v[16:19]\n"
"s_barrier\n"
"v_mfma_f32_16x16x32_bf16 v[48:51], v[136:139], a[244:247], v[48:51]\n"
"v_exp_f32 v175, v175\n"
"v_mfma_f32_16x16x32_bf16 v[48:51], v[140:143], a[248:251], v[48:51]\n"
"v_exp_f32 v178, v178\n"
"v_mfma_f32_16x16x32_bf16 v[80:83], v[144:147], a[244:247], v[80:83]\n"
"ds_read_b64_tr_b16 a[246:247], v234 offset:53248\n"
"v_cvt_pk_bf16_f32 v109, v162, v163\n"
"v_mfma_f32_16x16x32_bf16 v[80:83], v[148:151], a[248:251], v[80:83]\n"
"ds_read_b64_tr_b16 a[244:245], v234 offset:49152\n"
"v_cvt_pk_bf16_f32 v123, v154, v155\n"
"v_cvt_pk_bf16_f32 v116, v164, v165\n"
"v_exp_f32 v174, v174\n"
"v_exp_f32 v176, v176\n"
"v_exp_f32 v177, v177\n"
"v_exp_f32 v179, v179\n"
"v_accvgpr_read_b32 v253, a96\n"
"v_mfma_f32_16x16x32_bf16 v[84:87], v[148:151], v[224:227], v[84:87]\n"
"ds_read_b64_tr_b16 a[248:249], v238 offset:49152\n"
"v_cvt_pk_bf16_f32 v111, v174, v175\n"
"v_mfma_f32_16x16x32_bf16 v[84:87], v[144:147], a[252:255], v[84:87]\n"
"ds_read_b64_tr_b16 a[250:251], v238 offset:53248\n"
"v_exp_f32 v156, v156\n"
"v_cvt_pk_bf16_f32 v101, v158, v159\n"
"v_cvt_pk_bf16_f32 v108, v160, v161\n"
"v_exp_f32 v170, v170\n"
"v_exp_f32 v171, v171\n"
"v_cvt_pk_bf16_f32 v118, v176, v177\n"
"v_and_b32 v245, 0x3f0, v242\n"
"v_mfma_f32_16x16x32_bf16 v[52:55], v[140:143], v[224:227], v[52:55]\n"
"v_exp_f32 v172, v172\n"
"v_mfma_f32_16x16x32_bf16 v[52:55], v[136:139], a[252:255], v[52:55]\n"
"v_cvt_pk_bf16_f32 v119, v178, v179\n"
"v_and_b32 v253, 0x7fff0000, v253\n"
"v_mfma_f32_16x16x32_bf16 v[20:23], v[132:135], v[224:227], v[20:23]\n"
"v_cvt_pk_bf16_f32 v102, v168, v169\n"
"v_cvt_pk_bf16_f32 v103, v170, v171\n"
"v_mfma_f32_16x16x32_bf16 v[20:23], v[128:131], a[252:255], v[20:23]\n"
"v_cvt_pk_bf16_f32 v100, v156, v157\n"
"v_lshlrev_b32_e64 v244, 24, s43\n"
"v_mfma_f32_16x16x32_bf16 a[228:231], v[124:127], a[252:255], a[228:231]\n"
"s_waitcnt lgkmcnt(2)\n"
"ds_read_b64_tr_b16 v[224:225], v239 offset:49152\n"
"v_cvt_pk_bf16_f32 v117, v166, v167\n"
"v_mfma_f32_16x16x32_bf16 a[216:219], v[120:123], a[244:247], a[216:219]\n"
"s_mov_b64 s[28:29], s[8:9]\n"
"v_exp_f32 v173, v173\n"
"v_smfmac_f32_16x16x64_bf16 v[196:199], a[240:243], v[96:103], v241\n"
"s_mov_b32 s30, 655360\n"
"ds_read_b64_tr_b16 a[254:255], v235 offset:53248\n"
"ds_read_b64_tr_b16 v[226:227], v239 offset:53248\n"
"v_mfma_f32_16x16x32_bf16 v[8:11], v[96:99], a[244:247], v[8:11]\n"
"s_waitcnt lgkmcnt(3)\n"
"s_mov_b32 s71, s44\n"
"ds_read_b64_tr_b16 a[252:253], v235 offset:49152\n"
"v_max_u32_dpp v253, v253, v253 quad_perm:[1,0,3,2] row_mask:0xf bank_mask:0xf\n"
"v_mfma_f32_16x16x32_bf16 v[8:11], v[100:103], a[248:251], v[8:11]\n"
"s_mov_b32 s70, s95\n"
"v_and_b32 v252, 0x7fff0000, v252\n"
"v_max_u32_dpp v253, v253, v253 quad_perm:[2,3,0,1] row_mask:0xf bank_mask:0xf\n"
"v_smfmac_f32_16x16x64_bf16 v[200:203], a[240:243], v[112:119], v241\n"
"v_cvt_pk_bf16_f32 v110, v172, v173\n"
"v_max_u32_dpp v252, v252, v252 quad_perm:[1,0,3,2] row_mask:0xf bank_mask:0xf\n"
"v_mfma_f32_16x16x32_bf16 v[40:43], v[104:107], a[244:247], v[40:43]\n"
"s_nop 0\n"
"v_max_u32_dpp v252, v252, v252 quad_perm:[2,3,0,1] row_mask:0xf bank_mask:0xf\n"
"v_max_u32_dpp v253, v253, v253 row_half_mirror row_mask:0xf bank_mask:0xf\n"
"v_mfma_f32_16x16x32_bf16 v[40:43], v[108:111], a[248:251], v[40:43]\n"
"s_add_u32 s41, s70, 327680\n"
"s_add_u32 m0, s71, 32768\n"
"v_add_f32 v196, v196, v197\n"
"v_smfmac_f32_16x16x64_bf16 v[198:201], a[240:243], v[104:111], v241\n"
"buffer_load_dwordx4 v240, s[28:31], s41 offen lds\n"
"v_mfma_f32_16x16x32_bf16 v[72:75], v[112:115], a[244:247], v[72:75]\n"
"v_min_u32_dpp v253, v253, v253 row_mirror row_mask:0xf bank_mask:0xf\n"
"v_lshrrev_b32 v245, 1, v245\n"
"v_mfma_f32_16x16x32_bf16 v[72:75], v[116:119], a[248:251], v[72:75]\n"
"v_max_u32_dpp v252, v252, v252 row_half_mirror row_mask:0xf bank_mask:0xf\n"
"v_min_u32_dpp v253, v253, v253 row_bcast:15 row_mask:0xa bank_mask:0xf\n"
"v_mfma_f32_16x16x32_bf16 v[202:205], a[240:243], v[120:123], v[202:205]\n"
"s_waitcnt lgkmcnt(0)\n"
"ds_read_b64_tr_b16 a[244:245], v234 offset:50176\n"
"ds_read_b64_tr_b16 a[248:249], v238 offset:50176\n"
"v_mfma_f32_16x16x32_bf16 v[76:79], v[116:119], v[224:227], v[76:79]\n"
"s_add_u32 s40, s41, s52\n"
"ds_read_b64_tr_b16 a[246:247], v234 offset:54272\n"
"ds_read_b64_tr_b16 a[250:251], v238 offset:54272\n"
"v_mfma_f32_16x16x32_bf16 v[76:79], v[112:115], a[252:255], v[76:79]\n"
"buffer_load_dwordx4 v240, s[28:31], s40 offen offset:1024 lds\n"
"v_mfma_f32_16x16x32_bf16 v[44:47], v[108:111], v[224:227], v[44:47]\n"
"v_min_u32_dpp v252, v252, v252 row_mirror row_mask:0xf bank_mask:0xf\n"
"v_mul_f32 v244, 0x45800000, v244\n"
"v_mfma_f32_16x16x32_bf16 v[44:47], v[104:107], a[252:255], v[44:47]\n"
"v_add_f32 v198, v198, v199\n"
"v_add_f32 v244, v244, v202\n"
"v_mfma_f32_16x16x32_bf16 v[12:15], v[100:103], v[224:227], v[12:15]\n"
"s_add_u32 s40, s41, s53\n"
"ds_read_b64_tr_b16 v[224:225], v239 offset:50176\n"
"ds_read_b64_tr_b16 v[226:227], v239 offset:54272\n"
"v_mfma_f32_16x16x32_bf16 v[12:15], v[96:99], a[252:255], v[12:15]\n"
"buffer_load_dwordx4 v240, s[28:31], s40 offen offset:2048 lds\n"
"v_min_u32_dpp v252, v252, v252 row_bcast:15 row_mask:0xa bank_mask:0xf\n"
"v_add_f32 v200, v200, v201\n"
"v_min3_f32 v244, v244, v196, v198\n"
"v_min_f32 v244, v244, v200\n"
"v_rcp_f32 v244, v244\n"
"v_sub_u32 v245, v242, v245\n"
"v_mfma_f32_16x16x32_bf16 a[220:223], v[120:123], a[252:255], a[220:223]\n"
"s_waitcnt lgkmcnt(3)\n"
"v_min_u32_dpp v253, v253, v253 row_bcast:31 row_mask:0xc bank_mask:0xf\n"
"v_add_f32 v254, v196, v198\n"
"v_add_f32 v254, v254, v200\n"
"v_add_f32 v254, v254, v202\n"
"v_fmac_f32 v254, 0x68800000, v244\n"
"v_cmp_nge_f32 vcc, s45, v254\n"
"s_cbranch_vccnz LSLOW_%=\nLBACK3_%=:\ns_mov_b64 vcc, s[64:65]\n"
"v_mfma_f32_16x16x32_bf16 a[232:235], v[120:123], a[244:247], a[232:235]\n"
"ds_read_b64_tr_b16 a[252:253], v235 offset:50176\n"
"ds_read_b64_tr_b16 a[254:255], v235 offset:54272\n"
"v_min_u32_dpp v252, v252, v252 row_bcast:31 row_mask:0xc bank_mask:0xf\n"
"v_rcp_f32 v246, v196\n"
"v_xor_b32 v244, 0x11000, v242\n"
"v_bfe_u32 v254, v242, 9, 1\n"
"v_mfma_f32_16x16x32_bf16 v[24:27], v[96:99], a[244:247], v[24:27]\n"
"s_waitcnt lgkmcnt(4)\n"
"v_rcp_f32 v247, v198\n"
"v_mfma_f32_16x16x32_bf16 v[24:27], v[100:103], a[248:251], v[24:27]\n"
"ds_write_b128 v244, a[216:219] offset:32768\n"
"v_rcp_f32 v206, v200\n"
"v_mov_b32_dpp v246, v246 row_ror:12 row_mask:0x2 bank_mask:0xf\n"
"v_mov_b32_dpp v247, v247 row_ror:12 row_mask:0x2 bank_mask:0xf\n"
"v_mfma_f32_16x16x32_bf16 v[56:59], v[104:107], a[244:247], v[56:59]\n"
"s_add_u32 s40, s41, s54\n"
"v_mov_b32_dpp v246, v246 row_ror:8 row_mask:0x4 bank_mask:0xf\n"
"v_lshl_add_u32 v254, v254, 4, s43\n"
"v_mfma_f32_16x16x32_bf16 v[56:59], v[108:111], a[248:251], v[56:59]\n"
"buffer_load_dwordx4 v240, s[28:31], s40 offen offset:3072 lds\n"
"v_mfma_f32_16x16x32_bf16 v[88:91], v[112:115], a[244:247], v[88:91]\n"
"s_add_u32 s71, s44, s75\n"
"v_mul_f32 v252, 0x43340000, v252\n"
"v_mov_b32_dpp v246, v246 row_ror:4 row_mask:0x8 bank_mask:0xf\n"
"v_mfma_f32_16x16x32_bf16 v[88:91], v[116:119], a[248:251], v[88:91]\n"
"s_waitcnt lgkmcnt(3)\n"
"s_mov_b32 s70, s100\n"
"ds_read_b64_tr_b16 a[244:245], v232 offset:49152\n"
"ds_read_b64_tr_b16 a[246:247], v232 offset:53248\n"
"ds_read_b64_tr_b16 a[248:249], v236 offset:49152\n"
"ds_read_b64_tr_b16 a[250:251], v236 offset:53248\n"
"v_mov_b32_dpp v247, v247 row_ror:8 row_mask:0x4 bank_mask:0xf\n"
"v_lshl_add_u32 v254, v254, 6, s75\n"
"v_mul_f32_dpp v9, v246, v9 row_newbcast:1 row_mask:0xf bank_mask:0xf\n"
"v_mul_f32_dpp v10, v246, v10 row_newbcast:2 row_mask:0xf bank_mask:0xf\n"
"v_mul_f32_dpp v11, v246, v11 row_newbcast:3 row_mask:0xf bank_mask:0xf\n"
"v_mul_f32_dpp v24, v246, v24 row_newbcast:0 row_mask:0xf bank_mask:0xf\n"
"v_mfma_f32_16x16x32_bf16 v[92:95], v[116:119], v[224:227], v[92:95]\n"
"s_waitcnt lgkmcnt(5)\n"
"s_add_u32 s41, s70, 163840\n"
"s_add_u32 m0, s71, 16384\n"
"v_mul_f32_dpp v14, v246, v14 row_newbcast:2 row_mask:0xf bank_mask:0xf\n"
"v_mfma_f32_16x16x32_bf16 v[92:95], v[112:115], a[252:255], v[92:95]\n"
"buffer_load_dwordx4 v240, s[28:31], s41 offen lds\n"
"v_mfma_f32_16x16x32_bf16 v[60:63], v[108:111], v[224:227], v[60:63]\n"
"v_mul_f32 v252, v252, v253\n"
"v_mov_b32_dpp v206, v206 row_ror:12 row_mask:0x2 bank_mask:0xf\n"
"v_mfma_f32_16x16x32_bf16 v[60:63], v[104:107], a[252:255], v[60:63]\n"
"ds_write_b128 v244, a[220:223] offset:33792\n"
"v_mov_b32_dpp v247, v247 row_ror:4 row_mask:0x8 bank_mask:0xf\n"
"v_sub_u32 v228, v232, v254\n"
"v_mul_f32_dpp v8, v246, v8 row_newbcast:0 row_mask:0xf bank_mask:0xf\n"
"v_mul_f32_dpp v25, v246, v25 row_newbcast:1 row_mask:0xf bank_mask:0xf\n"
"v_mul_f32_dpp v26, v246, v26 row_newbcast:2 row_mask:0xf bank_mask:0xf\n"
"v_mfma_f32_16x16x32_bf16 v[28:31], v[100:103], v[224:227], v[28:31]\n"
"s_add_u32 s40, s41, s52\n"
"v_min_f32 v252, 0x71800000, v252\n"
"v_mul_f32_dpp v12, v246, v12 row_newbcast:0 row_mask:0xf bank_mask:0xf\n"
"v_mfma_f32_16x16x32_bf16 v[28:31], v[96:99], a[252:255], v[28:31]\n"
"buffer_load_dwordx4 v240, s[28:31], s40 offen offset:1024 lds\n"
"v_mfma_f32_16x16x32_bf16 a[236:239], v[120:123], a[252:255], a[236:239]\n"
"s_waitcnt lgkmcnt(3)\n"
"ds_read_b64_tr_b16 a[252:253], v233 offset:49152\n"
"ds_read_b64_tr_b16 a[254:255], v233 offset:53248\n"
"ds_read_b64_tr_b16 v[224:225], v237 offset:49152\n"
"ds_read_b64_tr_b16 v[226:227], v237 offset:53248\n"
"v_readlane_b32 s23, v252, 63\n"
"v_mov_b32_dpp v206, v206 row_ror:8 row_mask:0x4 bank_mask:0xf\n"
"v_cvt_pk_bf16_f32 v8, v8, v9\n"
"v_mul_f32_dpp v47, v247, v47 row_newbcast:3 row_mask:0xf bank_mask:0xf\n"
"v_mul_f32_dpp v58, v247, v58 row_newbcast:2 row_mask:0xf bank_mask:0xf\n"
"v_mul_f32_dpp v60, v247, v60 row_newbcast:0 row_mask:0xf bank_mask:0xf\n"
"v_mfma_f32_16x16x32_bf16 a[208:211], v[120:123], a[244:247], a[208:211]\n"
"ds_write_b128 v244, a[232:235] offset:34816\n"
"v_readlane_b32 s22, v252, 63\n"
"v_mov_b32_dpp v206, v206 row_ror:4 row_mask:0x8 bank_mask:0xf\n"
"v_mul_f32_dpp v15, v246, v15 row_newbcast:3 row_mask:0xf bank_mask:0xf\n"
"v_cvt_pk_bf16_f32 v9, v10, v11\n"
"v_mul_f32_dpp v41, v247, v41 row_newbcast:1 row_mask:0xf bank_mask:0xf\n"
"v_mfma_f32_16x16x32_bf16 v[0:3], v[96:99], a[244:247], v[0:3]\n"
"s_waitcnt lgkmcnt(6)\n"
"v_sub_u32 v231, v235, v254\n"
"v_mov_b64 v[196:197], s[22:23]\n"
"v_mfma_f32_16x16x32_bf16 v[0:3], v[100:103], a[248:251], v[0:3]\n"
"ds_write_b128 v244, a[236:239] offset:35840\n"
"v_mul_f32_dpp v42, v247, v42 row_newbcast:2 row_mask:0xf bank_mask:0xf\n"
"v_mul_f32_dpp v75, v206, v75 row_newbcast:3 row_mask:0xf bank_mask:0xf\n"
"v_mul_f32_dpp v77, v206, v77 row_newbcast:1 row_mask:0xf bank_mask:0xf\n"
"v_cvt_pk_bf16_f32 v24, v24, v25\n"
"v_mul_f32_dpp v63, v247, v63 row_newbcast:3 row_mask:0xf bank_mask:0xf\n"
"v_mfma_f32_16x16x32_bf16 v[32:35], v[104:107], a[244:247], v[32:35]\n"
"s_add_u32 s40, s41, s53\n"
"v_xor_b32 v250, 0x1000, v245\n"
"v_mul_f32_dpp v94, v206, v94 row_newbcast:2 row_mask:0xf bank_mask:0xf\n"
"v_mfma_f32_16x16x32_bf16 v[32:35], v[108:111], a[248:251], v[32:35]\n"
"buffer_load_dwordx4 v240, s[28:31], s40 offen offset:2048 lds\n"
"v_mfma_f32_16x16x32_bf16 v[64:67], v[112:115], a[244:247], v[64:67]\n"
"v_mul_f32_dpp v13, v246, v13 row_newbcast:1 row_mask:0xf bank_mask:0xf\n"
"v_mul_f32_dpp v30, v246, v30 row_newbcast:2 row_mask:0xf bank_mask:0xf\n"
"v_mfma_f32_16x16x32_bf16 v[64:67], v[116:119], a[248:251], v[64:67]\n"
"s_waitcnt lgkmcnt(2)\n"
"ds_read_b64_tr_b16 a[246:247], v232 offset:54272\n"
"ds_read_b64_tr_b16 a[248:249], v236 offset:50176\n"
"v_mov_b32 v252, v192\n"
"v_cvt_pk_bf16_f32 v11, v14, v15\n"
"v_mul_f32_dpp v40, v247, v40 row_newbcast:0 row_mask:0xf bank_mask:0xf\n"
"v_mul_f32_dpp v43, v247, v43 row_newbcast:3 row_mask:0xf bank_mask:0xf\n"
"v_cvt_pk_bf16_f32 v40, v40, v41\n"
"v_mul_f32_dpp v78, v206, v78 row_newbcast:2 row_mask:0xf bank_mask:0xf\n"
"v_mul_f32_dpp v31, v246, v31 row_newbcast:3 row_mask:0xf bank_mask:0xf\n"
"v_mul_f32_dpp v92, v206, v92 row_newbcast:0 row_mask:0xf bank_mask:0xf\n"
"v_mfma_f32_16x16x32_bf16 v[68:71], v[116:119], v[224:227], v[68:71]\n"
"s_add_u32 s40, s41, s54\n"
"ds_read_b64_tr_b16 a[244:245], v232 offset:50176\n"
"ds_read_b64_tr_b16 a[250:251], v236 offset:54272\n"
"v_mfma_f32_16x16x32_bf16 v[68:71], v[112:115], a[252:255], v[68:71]\n"
"buffer_load_dwordx4 v240, s[28:31], s40 offen offset:3072 lds\n"
"v_mov_b32 v253, v202\n"
"v_mul_f32_dpp v44, v247, v44 row_newbcast:0 row_mask:0xf bank_mask:0xf\n"
"v_mul_f32_dpp v45, v247, v45 row_newbcast:1 row_mask:0xf bank_mask:0xf\n"
"v_mul_f32_dpp v46, v247, v46 row_newbcast:2 row_mask:0xf bank_mask:0xf\n"
"v_cvt_pk_bf16_f32 v41, v42, v43\n"
"v_mul_f32_dpp v72, v206, v72 row_newbcast:0 row_mask:0xf bank_mask:0xf\n"
"v_mul_f32_dpp v73, v206, v73 row_newbcast:1 row_mask:0xf bank_mask:0xf\n"
"v_mul_f32_dpp v2, v246, v2 row_newbcast:2 row_mask:0xf bank_mask:0xf\n"
"v_mfma_f32_16x16x32_bf16 v[36:39], v[108:111], v[224:227], v[36:39]\n"
"v_mul_f32_dpp v74, v206, v74 row_newbcast:2 row_mask:0xf bank_mask:0xf\n"
"v_add_u32 v236, s76, v249\n"
"v_mfma_f32_16x16x32_bf16 v[36:39], v[104:107], a[252:255], v[36:39]\n"
"ds_write_b64 v250, v[252:253] offset:52224\n"
"v_cvt_pk_bf16_f32 v10, v12, v13\n"
"v_mul_f32_dpp v76, v206, v76 row_newbcast:0 row_mask:0xf bank_mask:0xf\n"
"v_mul_f32_dpp v79, v206, v79 row_newbcast:3 row_mask:0xf bank_mask:0xf\n"
"v_mul_f32_dpp v93, v206, v93 row_newbcast:1 row_mask:0xf bank_mask:0xf\n"
"v_mul_f32_dpp v3, v246, v3 row_newbcast:3 row_mask:0xf bank_mask:0xf\n"
"v_mul_f32_dpp v33, v247, v33 row_newbcast:1 row_mask:0xf bank_mask:0xf\n"
"v_xor_b32 v232, 0x1000, v228\n"
"v_mfma_f32_16x16x32_bf16 v[4:7], v[100:103], v[224:227], v[4:7]\n"
"ds_read_b64_tr_b16 v[224:225], v237 offset:50176\n"
"ds_read_b64_tr_b16 v[226:227], v237 offset:54272\n"
"v_mfma_f32_16x16x32_bf16 v[4:7], v[96:99], a[252:255], v[4:7]\n"
"ds_write_b128 v248, v[8:11] offset:49152\n"
"v_cvt_pk_bf16_f32 v42, v44, v45\n"
"v_mul_f32_dpp v56, v247, v56 row_newbcast:0 row_mask:0xf bank_mask:0xf\n"
"v_mul_f32_dpp v61, v247, v61 row_newbcast:1 row_mask:0xf bank_mask:0xf\n"
"v_mul_f32_dpp v32, v247, v32 row_newbcast:0 row_mask:0xf bank_mask:0xf\n"
"v_mul_f32_dpp v35, v247, v35 row_newbcast:3 row_mask:0xf bank_mask:0xf\n"
"v_mfma_f32_16x16x32_bf16 a[212:215], v[120:123], a[252:255], a[212:215]\n"
"s_waitcnt lgkmcnt(5)\n"
"ds_read_b64_tr_b16 a[252:253], v233 offset:50176\n"
"ds_read_b64_tr_b16 a[254:255], v233 offset:54272\n"
"v_mfma_f32_16x16x32_bf16 a[224:227], v[120:123], a[244:247], a[224:227]\n"
"ds_read_b64_tr_b16 a[4:5], v249 offset:49152\n"
"v_mul_f32_dpp v62, v247, v62 row_newbcast:2 row_mask:0xf bank_mask:0xf\n"
"v_mfma_f32_16x16x32_bf16 v[16:19], v[96:99], a[244:247], v[16:19]\n"
"s_waitcnt vmcnt(8) lgkmcnt(1)\n"
"ds_read_b64_tr_b16 a[6:7], v249 offset:49160\n"
"v_cvt_pk_bf16_f32 v43, v46, v47\n"
"v_mfma_f32_16x16x32_bf16 v[16:19], v[100:103], a[248:251], v[16:19]\n"
"s_barrier\n"
"ds_read_b64_tr_b16 v[182:183], v228 offset:2048\n"
"ds_read_b64_tr_b16 v[186:187], v232 offset:2048\n"
"ds_read_b64_tr_b16 v[188:189], v228 offset:8192\n"
"ds_read_b64_tr_b16 v[208:209], v228 offset:16384\n"
"v_mul_f32_dpp v28, v246, v28 row_newbcast:0 row_mask:0xf bank_mask:0xf\n"
"v_mul_f32_dpp v57, v247, v57 row_newbcast:1 row_mask:0xf bank_mask:0xf\n"
"v_mul_f32_dpp v36, v247, v36 row_newbcast:0 row_mask:0xf bank_mask:0xf\n"
"v_mul_f32_dpp v38, v247, v38 row_newbcast:2 row_mask:0xf bank_mask:0xf\n"
"v_mfma_f32_16x16x32_bf16 v[48:51], v[104:107], a[244:247], v[48:51]\n"
"ds_read_b64_tr_b16 v[210:211], v228 offset:18432\n"
"v_mul_f32_dpp v90, v206, v90 row_newbcast:2 row_mask:0xf bank_mask:0xf\n"
"v_mfma_f32_16x16x32_bf16 v[48:51], v[108:111], a[248:251], v[48:51]\n"
"ds_read_b64_tr_b16 v[212:213], v232 offset:16384\n"
"ds_read_b64_tr_b16 v[218:219], v228 offset:26624\n"
"ds_read_b64_tr_b16 v[222:223], v232 offset:26624\n"
"ds_read_b64_tr_b16 a[202:203], v236 offset:2056\n"
"v_cvt_pk_bf16_f32 v72, v72, v73\n"
"v_cvt_pk_bf16_f32 v73, v74, v75\n"
"v_cvt_pk_bf16_f32 v56, v56, v57\n"
"v_mul_f32_dpp v0, v246, v0 row_newbcast:0 row_mask:0xf bank_mask:0xf\n"
"v_mul_f32_dpp v6, v246, v6 row_newbcast:2 row_mask:0xf bank_mask:0xf\n"
"v_mul_f32_dpp v64, v206, v64 row_newbcast:0 row_mask:0xf bank_mask:0xf\n"
"v_mfma_f32_16x16x32_bf16 v[80:83], v[112:115], a[244:247], v[80:83]\n"
"ds_write_b128 v248, v[40:43] offset:50176\n"
"v_cvt_pk_bf16_f32 v74, v76, v77\n"
"v_mul_f32_dpp v27, v246, v27 row_newbcast:3 row_mask:0xf bank_mask:0xf\n"
"v_mul_f32_dpp v59, v247, v59 row_newbcast:3 row_mask:0xf bank_mask:0xf\n"
"v_mul_f32_dpp v88, v206, v88 row_newbcast:0 row_mask:0xf bank_mask:0xf\n"
"v_mul_f32_dpp v66, v206, v66 row_newbcast:2 row_mask:0xf bank_mask:0xf\n"
"v_mfma_f32_16x16x32_bf16 v[80:83], v[116:119], a[248:251], v[80:83]\n"
"ds_read_b64_tr_b16 a[22:23], v249 offset:50184\n"
"ds_read_b64_tr_b16 v[180:181], v228 offset:0\n"
"ds_read_b64_tr_b16 v[184:185], v232 offset:0\n"
"ds_read_b64_tr_b16 v[192:193], v232 offset:8192\n"
"v_cvt_pk_bf16_f32 v75, v78, v79\n"
"v_mul_f32_dpp v29, v246, v29 row_newbcast:1 row_mask:0xf bank_mask:0xf\n"
"v_mul_f32_dpp v89, v206, v89 row_newbcast:1 row_mask:0xf bank_mask:0xf\n"
"v_mul_f32_dpp v34, v247, v34 row_newbcast:2 row_mask:0xf bank_mask:0xf\n"
"v_mul_f32_dpp v39, v247, v39 row_newbcast:3 row_mask:0xf bank_mask:0xf\n"
"v_mul_f32_dpp v68, v206, v68 row_newbcast:0 row_mask:0xf bank_mask:0xf\n"
"v_mfma_f32_16x16x32_bf16 v[84:87], v[116:119], v[224:227], v[84:87]\n"
"ds_write_b128 v248, v[72:75] offset:51200\n"
"v_cvt_pk_bf16_f32 v25, v26, v27\n"
"v_cvt_pk_bf16_f32 v26, v28, v29\n"
"v_cvt_pk_bf16_f32 v88, v88, v89\n"
"v_mul_f32_dpp v1, v246, v1 row_newbcast:1 row_mask:0xf bank_mask:0xf\n"
"v_mul_f32_dpp v65, v206, v65 row_newbcast:1 row_mask:0xf bank_mask:0xf\n"
"v_mfma_f32_16x16x32_bf16 v[84:87], v[112:115], a[252:255], v[84:87]\n"
"ds_read_b64_tr_b16 v[194:195], v232 offset:10240\n"
"ds_read_b64_tr_b16 v[214:215], v232 offset:18432\n"
"ds_read_b64_tr_b16 a[250:251], v236 offset:6152\n"
"v_cvt_pk_bf16_f32 v57, v58, v59\n"
"v_mul_f32_dpp v91, v206, v91 row_newbcast:3 row_mask:0xf bank_mask:0xf\n"
"v_mul_f32_dpp v95, v206, v95 row_newbcast:3 row_mask:0xf bank_mask:0xf\n"
"v_mul_f32_dpp v4, v246, v4 row_newbcast:0 row_mask:0xf bank_mask:0xf\n"
"v_mul_f32_dpp v5, v246, v5 row_newbcast:1 row_mask:0xf bank_mask:0xf\n"
"v_cvt_pk_bf16_f32 v0, v0, v1\n"
"v_cvt_pk_bf16_f32 v32, v32, v33\n"
"v_cvt_pk_bf16_f32 v64, v64, v65\n"
"v_mfma_f32_16x16x32_bf16 v[52:55], v[108:111], v[224:227], v[52:55]\n"
"ds_read_b64_tr_b16 v[190:191], v228 offset:10240\n"
"ds_read_b64_tr_b16 v[220:221], v232 offset:24576\n"
"v_mfma_f32_16x16x32_bf16 v[52:55], v[104:107], a[252:255], v[52:55]\n"
"ds_read_b64_tr_b16 a[20:21], v249 offset:50176\n"
"ds_read_b64_tr_b16 a[36:37], v249 offset:51200\n"
"ds_read_b64_tr_b16 a[38:39], v249 offset:51208\n"
"ds_read_b64_tr_b16 v[216:217], v228 offset:24576\n"
"v_cvt_pk_bf16_f32 v27, v30, v31\n"
"v_cvt_pk_bf16_f32 v58, v60, v61\n"
"v_mul_f32_dpp v7, v246, v7 row_newbcast:3 row_mask:0xf bank_mask:0xf\n"
"v_cvt_pk_bf16_f32 v1, v2, v3\n"
"v_mul_f32_dpp v69, v206, v69 row_newbcast:1 row_mask:0xf bank_mask:0xf\n"
"v_xor_b32 v244, 0x1000, v244\n"
"v_mfma_f32_16x16x32_bf16 v[20:23], v[100:103], v[224:227], v[20:23]\n"
"ds_write_b128 v248, v[24:27] offset:49152\n"
"v_sub_u32 v229, v233, v254\n"
"v_cvt_pk_bf16_f32 v59, v62, v63\n"
"v_cvt_pk_bf16_f32 v89, v90, v91\n"
"v_cvt_pk_bf16_f32 v91, v94, v95\n"
"v_cvt_pk_bf16_f32 v2, v4, v5\n"
"v_mfma_f32_16x16x32_bf16 v[20:23], v[96:99], a[252:255], v[20:23]\n"
"ds_write_b128 v248, v[56:59] offset:50176\n"
"v_cvt_pk_bf16_f32 v90, v92, v93\n"
"v_cvt_pk_bf16_f32 v33, v34, v35\n"
"v_mul_f32_dpp v67, v206, v67 row_newbcast:3 row_mask:0xf bank_mask:0xf\n"
"v_mul_f32_dpp v71, v206, v71 row_newbcast:3 row_mask:0xf bank_mask:0xf\n"
"v_mov_b64 v[198:199], s[22:23]\n"
"v_mfma_f32_16x16x32_bf16 a[228:231], v[120:123], a[252:255], a[228:231]\n"
"s_waitcnt lgkmcnt(14)\n"
"ds_write_b128 v248, v[88:91] offset:51200\n"
"v_cvt_pk_bf16_f32 v3, v6, v7\n"
"v_mul_f32_dpp v37, v247, v37 row_newbcast:1 row_mask:0xf bank_mask:0xf\n"
"v_cvt_pk_bf16_f32 v34, v36, v37\n"
"v_mul_f32_dpp v70, v206, v70 row_newbcast:2 row_mask:0xf bank_mask:0xf\n"
"v_xor_b32 v237, 0x400, v236\n"
"v_mfma_f32_16x16x32_bf16 v[124:127], v[180:183], a[96:99], v[196:199]\n"
"s_waitcnt lgkmcnt(13)\n"
"ds_read_b64_tr_b16 a[30:31], v249 offset:50184\n"
"ds_read_b64_tr_b16 a[44:45], v249 offset:51200\n"
"v_mfma_f32_16x16x32_bf16 v[124:127], v[184:187], a[100:103], v[124:127]\n"
"s_waitcnt lgkmcnt(10)\n"
"ds_read_b64_tr_b16 a[28:29], v249 offset:50176\n"
"ds_read_b64_tr_b16 a[244:245], v237 offset:4096\n"
"v_mfma_f32_16x16x32_bf16 v[124:127], v[188:191], a[104:107], v[124:127]\n"
"ds_read_b64_tr_b16 a[12:13], v249 offset:49152\n"
"ds_read_b64_tr_b16 a[14:15], v249 offset:49160\n"
"v_mfma_f32_16x16x32_bf16 v[124:127], v[192:195], a[108:111], v[124:127]\n"
"ds_write_b128 v248, v[0:3] offset:49152\n"
"v_cvt_pk_bf16_f32 v35, v38, v39\n"
"v_cvt_pk_bf16_f32 v65, v66, v67\n"
"v_cvt_pk_bf16_f32 v66, v68, v69\n"
"v_cvt_pk_bf16_f32 v67, v70, v71\n"
"v_mfma_f32_16x16x32_bf16 v[124:127], v[208:211], a[144:147], v[124:127]\n"
"ds_read_b64_tr_b16 a[46:47], v249 offset:51208\n"
"ds_read_b128 v[60:63], v244 offset:35840\n"
"v_mfma_f32_16x16x32_bf16 v[124:127], v[212:215], a[148:151], v[124:127]\n"
"s_waitcnt lgkmcnt(11)\n"
"ds_write_b128 v248, v[32:35] offset:50176\n"
"ds_write_b128 v248, v[64:67] offset:51200\n"
"v_mfma_f32_16x16x32_bf16 v[124:127], v[216:219], a[152:155], v[124:127]\n"
"ds_read_b64_tr_b16 a[2:3], v249 offset:49160\n"
"ds_read_b128 v[28:31], v244 offset:33792\n"
"v_mfma_f32_16x16x32_bf16 v[124:127], v[220:223], a[156:159], v[124:127]\n"
"ds_read_b64_tr_b16 a[16:17], v249 offset:50176\n"
"ds_read_b64_tr_b16 a[34:35], v249 offset:51208\n"
"ds_read_b64 v[250:251], v245 offset:52224\n"
"ds_read_b128 v[56:59], v244 offset:34816\n"
"ds_read_b64_tr_b16 a[204:205], v237 offset:2048\n"
"v_sub_u32 v230, v234, v254\n"
"v_xor_b32 v233, 0x1000, v229\n"
"v_mul_f32_dpp v22, v246, v22 row_newbcast:2 row_mask:0xf bank_mask:0xf\n"
"v_mfma_f32_16x16x32_bf16 v[140:143], v[220:223], a[172:175], v[196:199]\n"
"ds_read_b64_tr_b16 a[0:1], v249 offset:49152\n"
"ds_read_b64_tr_b16 a[246:247], v237 offset:4104\n"
"v_mfma_f32_16x16x32_bf16 v[140:143], v[216:219], a[168:171], v[140:143]\n"
"ds_read_b64_tr_b16 a[32:33], v249 offset:51200\n"
"ds_read_b128 v[24:27], v244 offset:32768\n"
"v_mfma_f32_16x16x32_bf16 v[140:143], v[212:215], a[164:167], v[140:143]\n"
"ds_read_b64_tr_b16 a[18:19], v249 offset:50184\n"
"ds_read_b64_tr_b16 a[248:249], v236 offset:6144\n"
"v_mfma_f32_16x16x32_bf16 v[140:143], v[208:211], a[160:163], v[140:143]\n"
"ds_read_b64_tr_b16 v[110:111], v233 offset:10240\n"
"ds_read_b64_tr_b16 a[254:255], v237 offset:6152\n"
"v_mfma_f32_16x16x32_bf16 v[140:143], v[192:195], a[124:127], v[140:143]\n"
"ds_read_b64_tr_b16 v[108:109], v233 offset:8192\n"
"ds_read_b64_tr_b16 a[252:253], v237 offset:6144\n"
"v_mfma_f32_16x16x32_bf16 v[140:143], v[188:191], a[120:123], v[140:143]\n"
"ds_read_b64_tr_b16 v[226:227], v233 offset:26624\n"
"ds_read_b64_tr_b16 a[242:243], v236 offset:4104\n"
"v_mfma_f32_16x16x32_bf16 v[140:143], v[184:187], a[116:119], v[140:143]\n"
"ds_read_b64_tr_b16 a[206:207], v237 offset:2056\n"
"ds_read_b64_tr_b16 v[98:99], v229 offset:2048\n"
"v_mfma_f32_16x16x32_bf16 v[140:143], v[180:183], a[112:115], v[140:143]\n"
"ds_read_b64_tr_b16 a[194:195], v236 offset:8\n"
"ds_read_b64_tr_b16 a[196:197], v237 offset:0\n"
"v_mfma_f32_16x16x32_bf16 v[156:159], v[180:183], a[128:131], v[196:199]\n"
"ds_read_b64_tr_b16 v[102:103], v233 offset:2048\n"
"ds_read_b64_tr_b16 v[106:107], v229 offset:10240\n"
"v_mfma_f32_16x16x32_bf16 v[156:159], v[184:187], a[132:135], v[156:159]\n"
"ds_read_b64_tr_b16 a[200:201], v236 offset:2048\n"
"ds_read_b64_tr_b16 v[224:225], v233 offset:24576\n"
"v_mfma_f32_16x16x32_bf16 v[156:159], v[188:191], a[136:139], v[156:159]\n"
"ds_read_b64_tr_b16 v[120:121], v229 offset:24576\n"
"ds_read_b64_tr_b16 a[240:241], v236 offset:4096\n"
"v_mfma_f32_16x16x32_bf16 v[156:159], v[192:195], a[140:143], v[156:159]\n"
"s_waitcnt lgkmcnt(14)\n"
"ds_read_b64_tr_b16 v[96:97], v229 offset:0\n"
"v_xor_b32 v234, 0x1000, v230\n"
"v_mfma_f32_16x16x32_bf16 v[156:159], v[208:211], a[176:179], v[156:159]\n"
"ds_read_b64_tr_b16 a[198:199], v237 offset:8\n"
"ds_read_b64_tr_b16 v[114:115], v229 offset:18432\n"
"v_mfma_f32_16x16x32_bf16 v[156:159], v[212:215], a[180:183], v[156:159]\n"
"s_add_u32 s41, s95, 491520\n"
"ds_read_b64_tr_b16 a[192:193], v236 offset:0\n"
"v_mul_f32_dpp v54, v247, v54 row_newbcast:2 row_mask:0xf bank_mask:0xf\n"
"v_mfma_f32_16x16x32_bf16 v[156:159], v[216:219], a[184:187], v[156:159]\n"
"s_add_u32 m0, s44, 49152\n"
"ds_read_b64_tr_b16 v[100:101], v233 offset:0\n"
"ds_read_b64_tr_b16 v[118:119], v233 offset:18432\n"
"v_mfma_f32_16x16x32_bf16 v[156:159], v[220:223], a[188:191], v[156:159]\n"
"s_waitcnt lgkmcnt(12)\n"
"buffer_load_dwordx4 v240, s[28:31], s41 offen lds\n"
"v_mfma_f32_16x16x32_bf16 v[172:175], v[220:223], a[252:255], v[196:199]\n"
"s_add_u32 s40, s41, s52\n"
"ds_read_b64_tr_b16 v[104:105], v229 offset:8192\n"
"ds_read_b64_tr_b16 v[116:117], v233 offset:16384\n"
"v_mfma_f32_16x16x32_bf16 v[172:175], v[216:219], a[248:251], v[172:175]\n"
"buffer_load_dwordx4 v240, s[28:31], s40 offen offset:1024 lds\n"
"v_mfma_f32_16x16x32_bf16 v[172:175], v[212:215], a[244:247], v[172:175]\n"
"s_waitcnt lgkmcnt(8)\n"
"s_add_u32 s40, s41, s53\n"
"ds_read_b64_tr_b16 v[112:113], v229 offset:16384\n"
"ds_read_b64_tr_b16 v[122:123], v229 offset:26624\n"
"v_mfma_f32_16x16x32_bf16 v[172:175], v[208:211], a[240:243], v[172:175]\n"
"buffer_load_dwordx4 v240, s[28:31], s40 offen offset:2048 lds\n"
"v_mfma_f32_16x16x32_bf16 v[172:175], v[192:195], a[204:207], v[172:175]\n"
"v_min_f32 v254, v252, v250\n"
"v_sub_f32 v250, v254, v250\n"
"v_mfma_f32_16x16x32_bf16 v[172:175], v[188:191], a[200:203], v[172:175]\n"
"s_waitcnt lgkmcnt(6)\n"
"v_sub_f32 v252, v254, v252\n"
"v_mul_f32_dpp v86, v206, v86 row_newbcast:2 row_mask:0xf bank_mask:0xf\n"
"v_mfma_f32_16x16x32_bf16 v[172:175], v[184:187], a[196:199], v[172:175]\n"
"s_add_u32 s40, s41, s54\n"
"ds_read_b64_tr_b16 v[192:193], v234 offset:8192\n"
"v_mul_f32_dpp v49, v247, v49 row_newbcast:1 row_mask:0xf bank_mask:0xf\n"
"v_mfma_f32_16x16x32_bf16 v[172:175], v[180:183], a[192:195], v[172:175]\n"
"buffer_load_dwordx4 v240, s[28:31], s40 offen offset:3072 lds\n"
"v_mfma_f32_16x16x32_bf16 v[128:131], v[96:99], a[96:99], v[196:199]\n"
"s_waitcnt lgkmcnt(3)\n"
"v_xor_b32 v248, s75, v248\n"
"v_mul_f32_dpp v23, v246, v23 row_newbcast:3 row_mask:0xf bank_mask:0xf\n"
"v_mfma_f32_16x16x32_bf16 v[128:131], v[100:103], a[100:103], v[128:131]\n"
"v_exp_f32 v252, v252\n"
"v_mfma_f32_16x16x32_bf16 v[128:131], v[104:107], a[104:107], v[128:131]\n"
"ds_read_b64_tr_b16 v[208:209], v230 offset:16384\n"
"v_mul_f32 v253, v253, v252\n"
"v_mfma_f32_16x16x32_bf16 v[128:131], v[108:111], a[108:111], v[128:131]\n"
"s_waitcnt lgkmcnt(2)\n"
"ds_read_b64_tr_b16 a[238:239], v237 offset:10248\n"
"ds_read_b64_tr_b16 v[210:211], v230 offset:18432\n"
"v_mfma_f32_16x16x32_bf16 v[128:131], v[112:115], a[144:147], v[128:131]\n"
"s_add_u32 s21, s60, 32\n"
"v_exp_f32 v250, v250\n"
"v_mfma_f32_16x16x32_bf16 v[128:131], v[116:119], a[148:151], v[128:131]\n"
"s_mov_b32 s69, 1\n"
"ds_read_b64_tr_b16 v[180:181], v230 offset:0\n"
"v_mul_f32_dpp v16, v246, v16 row_newbcast:0 row_mask:0xf bank_mask:0xf\n"
"v_mfma_f32_16x16x32_bf16 v[128:131], v[120:123], a[152:155], v[128:131]\n"
"v_bfe_u32 v201, v242, 2, 7\n"
"v_add_u32 v201, 0x100, v201\n"
"ds_read_b64_tr_b16 v[220:221], v234 offset:24576\n"
"v_mfma_f32_16x16x32_bf16 v[128:131], v[224:227], a[156:159], v[128:131]\n"
"global_load_dword v200, v201, s[6:7] sc1\n"
"v_mfma_f32_16x16x32_bf16 v[144:147], v[224:227], a[172:175], v[196:199]\n"
"v_accvgpr_read_b32 v254, a208\n"
"v_accvgpr_read_b32 v255, a209\n"
"v_mfma_f32_16x16x32_bf16 v[144:147], v[120:123], a[168:171], v[144:147]\n"
"v_mul_f32_dpp v48, v247, v48 row_newbcast:0 row_mask:0xf bank_mask:0xf\n"
"v_mul_f32_dpp v80, v206, v80 row_newbcast:0 row_mask:0xf bank_mask:0xf\n"
"v_mfma_f32_16x16x32_bf16 v[144:147], v[116:119], a[164:167], v[144:147]\n"
"ds_read_b64_tr_b16 v[188:189], v230 offset:8192\n"
"v_fmac_f32 v253, v251, v250\n"
"v_mfma_f32_16x16x32_bf16 v[144:147], v[112:115], a[160:163], v[144:147]\n"
"v_mul_f32_dpp v19, v246, v19 row_newbcast:3 row_mask:0xf bank_mask:0xf\n"
"v_mul_f32_dpp v21, v246, v21 row_newbcast:1 row_mask:0xf bank_mask:0xf\n"
"v_mfma_f32_16x16x32_bf16 v[144:147], v[108:111], a[124:127], v[144:147]\n"
"v_rcp_f32 v253, v253\n"
"v_mfma_f32_16x16x32_bf16 v[144:147], v[104:107], a[120:123], v[144:147]\n"
"ds_read_b64_tr_b16 v[218:219], v230 offset:26624\n"
"v_mul_f32 v250, v250, v253\n"
"v_mfma_f32_16x16x32_bf16 v[144:147], v[100:103], a[116:119], v[144:147]\n"
"s_nop 0\n"
"v_mov_b32_dpp v250, v250 row_ror:12 row_mask:0x2 bank_mask:0xf\n"
"v_mul_f32_dpp v52, v247, v52 row_newbcast:0 row_mask:0xf bank_mask:0xf\n"
"v_mfma_f32_16x16x32_bf16 v[144:147], v[96:99], a[112:115], v[144:147]\n"
"ds_read_b64_tr_b16 a[216:217], v236 offset:8192\n"
"ds_read_b64_tr_b16 v[182:183], v230 offset:2048\n"
"v_mul_f32 v252, v252, v253\n"
"v_mov_b32_dpp v250, v250 row_ror:8 row_mask:0x4 bank_mask:0xf\n"
"v_accvgpr_read_b32 v251, a211\n"
"v_mul_f32_dpp v17, v246, v17 row_newbcast:1 row_mask:0xf bank_mask:0xf\n"
"v_mul_f32_dpp v55, v247, v55 row_newbcast:3 row_mask:0xf bank_mask:0xf\n"
"v_mfma_f32_16x16x32_bf16 v[160:163], v[96:99], a[128:131], v[196:199]\n"
"v_mov_b32_dpp v250, v250 row_ror:4 row_mask:0x8 bank_mask:0xf\n"
"v_mul_f32_dpp v81, v206, v81 row_newbcast:1 row_mask:0xf bank_mask:0xf\n"
"v_mfma_f32_16x16x32_bf16 v[160:163], v[100:103], a[132:135], v[160:163]\n"
"v_xor_b32 v235, 0x1000, v231\n"
"v_mul_f32_dpp v57, v250, v57 row_newbcast:1 row_mask:0xf bank_mask:0xf\n"
"v_mfma_f32_16x16x32_bf16 v[160:163], v[104:107], a[136:139], v[160:163]\n"
"v_mul_f32_dpp v31, v250, v31 row_newbcast:3 row_mask:0xf bank_mask:0xf\n"
"v_mul_f32_dpp v58, v250, v58 row_newbcast:2 row_mask:0xf bank_mask:0xf\n"
"v_mfma_f32_16x16x32_bf16 v[160:163], v[108:111], a[140:143], v[160:163]\n"
"ds_read_b64_tr_b16 v[194:195], v234 offset:10240\n"
"v_mov_b32_dpp v252, v252 row_ror:12 row_mask:0x2 bank_mask:0xf\n"
"v_mfma_f32_16x16x32_bf16 v[160:163], v[112:115], a[176:179], v[160:163]\n"
"s_nop 0\n"
"v_mov_b32_dpp v252, v252 row_ror:8 row_mask:0x4 bank_mask:0xf\n"
"v_mul_f32_dpp v18, v246, v18 row_newbcast:2 row_mask:0xf bank_mask:0xf\n"
"v_mfma_f32_16x16x32_bf16 v[160:163], v[116:119], a[180:183], v[160:163]\n"
"ds_read_b64_tr_b16 a[218:219], v236 offset:8200\n"
"ds_read_b64_tr_b16 v[184:185], v234 offset:0\n"
"v_mfma_f32_16x16x32_bf16 v[160:163], v[120:123], a[184:187], v[160:163]\n"
"v_mul_f32_dpp v27, v250, v27 row_newbcast:3 row_mask:0xf bank_mask:0xf\n"
"v_cvt_pk_bf16_f32 v16, v16, v17\n"
"v_mfma_f32_16x16x32_bf16 v[160:163], v[224:227], a[188:191], v[160:163]\n"
"ds_read_b64_tr_b16 v[186:187], v234 offset:2048\n"
"ds_read_b64_tr_b16 v[190:191], v230 offset:10240\n"
"v_mov_b32_dpp v252, v252 row_ror:4 row_mask:0x8 bank_mask:0xf\n"
"v_mul_f32_dpp v26, v250, v26 row_newbcast:2 row_mask:0xf bank_mask:0xf\n"
"v_mul_f32_dpp v28, v250, v28 row_newbcast:0 row_mask:0xf bank_mask:0xf\n"
"v_mul_f32_dpp v59, v250, v59 row_newbcast:3 row_mask:0xf bank_mask:0xf\n"
"v_mul_f32_dpp v63, v250, v63 row_newbcast:3 row_mask:0xf bank_mask:0xf\n"
"v_mfma_f32_16x16x32_bf16 v[176:179], v[224:227], a[252:255], v[196:199]\n"
"ds_read_b64_tr_b16 v[212:213], v234 offset:16384\n"
"v_mul_f32_dpp v30, v250, v30 row_newbcast:2 row_mask:0xf bank_mask:0xf\n"
"v_mfma_f32_16x16x32_bf16 v[176:179], v[120:123], a[248:251], v[176:179]\n"
"v_mul_f32_dpp v24, v250, v24 row_newbcast:0 row_mask:0xf bank_mask:0xf\n"
"v_fmac_f32_dpp v27, v252, v251 row_newbcast:3 row_mask:0xf bank_mask:0xf\n"
"v_mfma_f32_16x16x32_bf16 v[176:179], v[116:119], a[244:247], v[176:179]\n"
"ds_read_b64_tr_b16 v[216:217], v230 offset:24576\n"
"v_accvgpr_read_b32 v253, a210\n"
"v_mfma_f32_16x16x32_bf16 v[176:179], v[112:115], a[240:243], v[176:179]\n"
"ds_read_b64_tr_b16 v[222:223], v234 offset:26624\n"
"v_mul_f32_dpp v25, v250, v25 row_newbcast:1 row_mask:0xf bank_mask:0xf\n"
"v_mfma_f32_16x16x32_bf16 v[176:179], v[108:111], a[204:207], v[176:179]\n"
"ds_read_b64_tr_b16 a[236:237], v237 offset:10240\n"
"v_mul_f32_dpp v29, v250, v29 row_newbcast:1 row_mask:0xf bank_mask:0xf\n"
"v_mfma_f32_16x16x32_bf16 v[176:179], v[104:107], a[200:203], v[176:179]\n"
"ds_read_b64_tr_b16 v[214:215], v234 offset:18432\n"
"v_fmac_f32_dpp v25, v252, v255 row_newbcast:1 row_mask:0xf bank_mask:0xf\n"
"v_mfma_f32_16x16x32_bf16 v[176:179], v[100:103], a[196:199], v[176:179]\n"
"v_accvgpr_read_b32 v255, a213\n"
"v_accvgpr_read_b32 v251, a215\n"
"v_mfma_f32_16x16x32_bf16 v[176:179], v[96:99], a[192:195], v[176:179]\n"
"s_waitcnt lgkmcnt(5)\n"
"ds_read_b64_tr_b16 v[112:113], v231 offset:16384\n"
"ds_read_b64_tr_b16 v[122:123], v231 offset:26624\n"
"v_fmac_f32_dpp v24, v252, v254 row_newbcast:0 row_mask:0xf bank_mask:0xf\n"
"v_fmac_f32_dpp v26, v252, v253 row_newbcast:2 row_mask:0xf bank_mask:0xf\n"
"v_accvgpr_read_b32 v254, a212\n"
"v_accvgpr_read_b32 v253, a214\n"
"v_fmac_f32_dpp v31, v252, v251 row_newbcast:3 row_mask:0xf bank_mask:0xf\n"
"v_accvgpr_read_b32 v251, a227\n"
"v_mfma_f32_16x16x32_bf16 v[132:135], v[180:183], a[96:99], v[196:199]\n"
"ds_read_b64_tr_b16 a[222:223], v237 offset:8200\n"
"v_cvt_pk_bf16_f32 v24, v24, v25\n"
"v_mfma_f32_16x16x32_bf16 v[132:135], v[184:187], a[100:103], v[132:135]\n"
"ds_read_b64_tr_b16 v[120:121], v231 offset:24576\n"
"v_mul_f32_dpp v56, v250, v56 row_newbcast:0 row_mask:0xf bank_mask:0xf\n"
"v_mfma_f32_16x16x32_bf16 v[132:135], v[188:191], a[104:107], v[132:135]\n"
"v_fmac_f32_dpp v28, v252, v254 row_newbcast:0 row_mask:0xf bank_mask:0xf\n"
"v_mul_f32_dpp v60, v250, v60 row_newbcast:0 row_mask:0xf bank_mask:0xf\n"
"v_mfma_f32_16x16x32_bf16 v[132:135], v[192:195], a[108:111], v[132:135]\n"
"v_fmac_f32_dpp v29, v252, v255 row_newbcast:1 row_mask:0xf bank_mask:0xf\n"
"v_accvgpr_read_b32 v255, a225\n"
"v_mfma_f32_16x16x32_bf16 v[132:135], v[208:211], a[144:147], v[132:135]\n"
"s_waitcnt lgkmcnt(4)\n"
"s_waitcnt vmcnt(0)\n"
"v_fmac_f32_dpp v30, v252, v253 row_newbcast:2 row_mask:0xf bank_mask:0xf\n"
"v_accvgpr_read_b32 v254, a224\n"
"v_mfma_f32_16x16x32_bf16 v[132:135], v[212:215], a[148:151], v[132:135]\n"
"v_subrev_u32 v200, s21, v200\n"
"ds_read_b64_tr_b16 v[226:227], v235 offset:26624\n"
"v_mfma_f32_16x16x32_bf16 v[132:135], v[216:219], a[152:155], v[132:135]\n"
"v_cmp_lt_u32 vcc, s69, v200\n"
"v_fmac_f32_dpp v59, v252, v251 row_newbcast:3 row_mask:0xf bank_mask:0xf\n"
"v_mfma_f32_16x16x32_bf16 v[132:135], v[220:223], a[156:159], v[132:135]\n"
"ds_read_b64_tr_b16 v[102:103], v235 offset:2048\n"
"ds_read_b64_tr_b16 v[110:111], v235 offset:10240\n"
"v_cvt_pk_bf16_f32 v17, v18, v19\n"
"v_accvgpr_read_b32 v253, a226\n"
"v_fmac_f32_dpp v56, v252, v254 row_newbcast:0 row_mask:0xf bank_mask:0xf\n"
"v_fmac_f32_dpp v57, v252, v255 row_newbcast:1 row_mask:0xf bank_mask:0xf\n"
"v_accvgpr_read_b32 v254, a228\n"
"v_accvgpr_read_b32 v255, a229\n"
"v_mul_f32_dpp v61, v250, v61 row_newbcast:1 row_mask:0xf bank_mask:0xf\n"
"s_cbranch_vccnz LGV_%=\nLGVK_%=:\n"
"v_mfma_f32_16x16x32_bf16 v[148:151], v[220:223], a[172:175], v[196:199]\n"
"ds_read_b64_tr_b16 v[100:101], v235 offset:0\n"
"v_cvt_pk_bf16_f32 v25, v26, v27\n"
"v_mfma_f32_16x16x32_bf16 v[148:151], v[216:219], a[168:171], v[148:151]\n"
"v_add_u32_dpp v200, v200, v200 quad_perm:[1,0,3,2] row_mask:0xf bank_mask:0xf\n"
"ds_read_b64_tr_b16 v[104:105], v231 offset:8192\n"
"v_mfma_f32_16x16x32_bf16 v[148:151], v[212:215], a[164:167], v[148:151]\n"
"ds_read_b64_tr_b16 v[98:99], v231 offset:2048\n"
"v_fmac_f32_dpp v58, v252, v253 row_newbcast:2 row_mask:0xf bank_mask:0xf\n"
"v_mfma_f32_16x16x32_bf16 v[148:151], v[208:211], a[160:163], v[148:151]\n"
"v_add_u32_dpp v200, v200, v200 quad_perm:[2,3,0,1] row_mask:0xf bank_mask:0xf\n"
"v_accvgpr_read_b32 v251, a231\n"
"v_mfma_f32_16x16x32_bf16 v[148:151], v[192:195], a[124:127], v[148:151]\n"
"ds_read_b64_tr_b16 v[96:97], v231 offset:0\n"
"ds_read_b64_tr_b16 v[116:117], v235 offset:16384\n"
"v_cvt_pk_bf16_f32 v26, v28, v29\n"
"v_cvt_pk_bf16_f32 v27, v30, v31\n"
"v_accvgpr_read_b32 v253, a230\n"
"v_mul_f32_dpp v62, v250, v62 row_newbcast:2 row_mask:0xf bank_mask:0xf\n"
"v_fmac_f32_dpp v60, v252, v254 row_newbcast:0 row_mask:0xf bank_mask:0xf\n"
"v_cvt_pk_bf16_f32 v56, v56, v57\n"
"v_mfma_f32_16x16x32_bf16 v[148:151], v[188:191], a[120:123], v[148:151]\n"
"v_cmp_lt_u32 vcc, 1, v200\n"
"ds_write_b128 v243, v[24:27] offset:0\n"
"v_fmac_f32_dpp v61, v252, v255 row_newbcast:1 row_mask:0xf bank_mask:0xf\n"
"v_fmac_f32_dpp v62, v252, v253 row_newbcast:2 row_mask:0xf bank_mask:0xf\n"
"v_cvt_pk_bf16_f32 v57, v58, v59\n"
"v_cvt_pk_bf16_f32 v58, v60, v61\n"
"v_mfma_f32_16x16x32_bf16 v[148:151], v[184:187], a[116:119], v[148:151]\n"
"v_fmac_f32_dpp v63, v252, v251 row_newbcast:3 row_mask:0xf bank_mask:0xf\n"
"v_cvt_pk_bf16_f32 v59, v62, v63\n"
"v_mfma_f32_16x16x32_bf16 v[148:151], v[180:183], a[112:115], v[148:151]\n"
"ds_write_b128 v243, v[56:59] offset:2048\n"
"v_mul_f32_dpp v50, v247, v50 row_newbcast:2 row_mask:0xf bank_mask:0xf\n"
"v_cvt_pk_bf16_f32 v48, v48, v49\n"
"v_mfma_f32_16x16x32_bf16 v[164:167], v[180:183], a[128:131], v[196:199]\n"
"s_mov_b32 s47, 0\n"
"v_xor_b32 v249, s75, v249\n"
"v_mul_f32_dpp v20, v246, v20 row_newbcast:0 row_mask:0xf bank_mask:0xf\n"
"v_mfma_f32_16x16x32_bf16 v[164:167], v[184:187], a[132:135], v[164:167]\n"
"s_mov_b32 s89, vcc_lo\n"
"ds_read_b64_tr_b16 v[106:107], v231 offset:10240\n"
"ds_read_b64_tr_b16 v[224:225], v235 offset:24576\n"
"v_mfma_f32_16x16x32_bf16 v[164:167], v[188:191], a[136:139], v[164:167]\n"
"s_lshr_b32 s90, s89, s93\n"
"ds_read_b64_tr_b16 v[114:115], v231 offset:18432\n"
"v_cvt_pk_bf16_f32 v19, v22, v23\n"
"v_mfma_f32_16x16x32_bf16 v[164:167], v[192:195], a[140:143], v[164:167]\n"
"s_and_b32 s90, s90, 1\n"
"ds_read_b64_tr_b16 v[108:109], v235 offset:8192\n"
"v_mul_f32_dpp v87, v206, v87 row_newbcast:3 row_mask:0xf bank_mask:0xf\n"
"v_mfma_f32_16x16x32_bf16 v[164:167], v[208:211], a[176:179], v[164:167]\n"
"s_lshl_b32 s16, s61, 7\n"
"v_cvt_pk_bf16_f32 v18, v20, v21\n"
"v_mul_f32_dpp v51, v247, v51 row_newbcast:3 row_mask:0xf bank_mask:0xf\n"
"v_mfma_f32_16x16x32_bf16 v[164:167], v[212:215], a[180:183], v[164:167]\n"
"s_add_u32 s90, s90, s16\n"
"ds_read_b64_tr_b16 v[118:119], v235 offset:18432\n"
"v_mul_f32_dpp v85, v206, v85 row_newbcast:1 row_mask:0xf bank_mask:0xf\n"
"v_mfma_f32_16x16x32_bf16 v[164:167], v[216:219], a[184:187], v[164:167]\n"
"s_waitcnt vmcnt(63) lgkmcnt(5)\n"
"s_lshl_b32 s90, s90, 13\n"
"v_cvt_pk_bf16_f32 v80, v80, v81\n"
"v_mfma_f32_16x16x32_bf16 v[164:167], v[220:223], a[188:191], v[164:167]\n"
"s_barrier\n"
"s_lshl_b32 s16, s95, 9\n"
"ds_write_b128 v248, v[16:19] offset:49152\n"
"v_mfma_f32_16x16x32_bf16 v[136:139], v[96:99], a[96:99], v[196:199]\n"
"s_add_u32 s16, s16, s90\n"
"v_mul_f32_dpp v53, v247, v53 row_newbcast:1 row_mask:0xf bank_mask:0xf\n"
"v_cvt_pk_bf16_f32 v49, v50, v51\n"
"v_mfma_f32_16x16x32_bf16 v[136:139], v[100:103], a[100:103], v[136:139]\n"
"s_waitcnt lgkmcnt(4)\n"
"s_add_u32 s17, s16, 4096\n"
"ds_read_b64_tr_b16 a[228:229], v237 offset:14336\n"
"v_cvt_pk_bf16_f32 v50, v52, v53\n"
"v_mfma_f32_16x16x32_bf16 v[136:139], v[104:107], a[104:107], v[136:139]\n"
"s_waitcnt lgkmcnt(3)\n"
"s_mov_b32 s72, s61\n"
"ds_read_b64_tr_b16 a[10:11], v249 offset:49160\n"
"v_mov_b64 v[90:91], 0\n"
"v_mfma_f32_16x16x32_bf16 v[136:139], v[108:111], a[108:111], v[136:139]\n"
"s_mov_b32 s21, s60\n"
"ds_read_b64_tr_b16 v[220:221], v232 offset:57344\n"
"v_mul_f32_dpp v84, v206, v84 row_newbcast:0 row_mask:0xf bank_mask:0xf\n"
"v_mfma_f32_16x16x32_bf16 v[136:139], v[112:115], a[144:147], v[136:139]\n"
"s_waitcnt lgkmcnt(4)\n"
"s_mov_b32 s18, s77\n"
"ds_read_b64_tr_b16 a[8:9], v249 offset:49152\n"
"v_mul_f32_dpp v83, v206, v83 row_newbcast:3 row_mask:0xf bank_mask:0xf\n"
"v_mfma_f32_16x16x32_bf16 v[136:139], v[116:119], a[148:151], v[136:139]\n"
"s_mov_b32 s19, s77\n"
"ds_read_b64_tr_b16 v[180:181], v228 offset:32768\n"
"v_cvt_pk_bf16_f32 v51, v54, v55\n"
"v_mfma_f32_16x16x32_bf16 v[136:139], v[120:123], a[152:155], v[136:139]\n"
"s_mov_b32 s69, -1\n"
"ds_read_b64_tr_b16 v[182:183], v228 offset:34816\n"
"v_mul_f32_dpp v82, v206, v82 row_newbcast:2 row_mask:0xf bank_mask:0xf\n"
"v_mfma_f32_16x16x32_bf16 v[136:139], v[224:227], a[156:159], v[136:139]\n"
"s_mul_i32 s88, s61, 0x10011111\n"
"ds_read_b64_tr_b16 v[212:213], v232 offset:49152\n"
"v_cvt_pk_bf16_f32 v81, v82, v83\n"
"v_mfma_f32_16x16x32_bf16 v[152:155], v[224:227], a[172:175], v[196:199]\n"
"s_add_u32 s88, s88, 484675\n"
"ds_read_b64_tr_b16 v[186:187], v232 offset:34816\n"
"v_cvt_pk_bf16_f32 v83, v86, v87\n"
"v_mfma_f32_16x16x32_bf16 v[152:155], v[120:123], a[168:171], v[152:155]\n"
"s_and_b32 s88, s88, 0x70077777\n"
"ds_read_b64_tr_b16 v[216:217], v228 offset:57344\n"
"v_cvt_pk_bf16_f32 v82, v84, v85\n"
"v_mfma_f32_16x16x32_bf16 v[152:155], v[116:119], a[164:167], v[152:155]\n"
"s_or_b32 s88, s88, 0x9800000\n"
"ds_write_b128 v248, v[48:51] offset:50176\n"
"ds_write_b128 v248, v[80:83] offset:51200\n"
"v_mfma_f32_16x16x32_bf16 v[152:155], v[112:115], a[160:163], v[152:155]\n"
"s_mov_b32 s84, s6\n"
"ds_read_b64_tr_b16 a[24:25], v249 offset:50176\n"
"ds_read_b64_tr_b16 a[26:27], v249 offset:50184\n"
"ds_read_b64_tr_b16 a[40:41], v249 offset:51200\n"
"ds_read_b64_tr_b16 a[42:43], v249 offset:51208\n"
"v_mfma_f32_16x16x32_bf16 v[152:155], v[108:111], a[124:127], v[152:155]\n"
"s_and_b32 s85, s7, 0xffff\n"
"ds_read_b64_tr_b16 v[188:189], v228 offset:40960\n"
"v_bfe_u32 v254, v242, 4, 4\n"
"v_mfma_f32_16x16x32_bf16 v[152:155], v[104:107], a[120:123], v[152:155]\n"
"s_mov_b32 s86, 128\n"
"ds_read_b64_tr_b16 v[184:185], v232 offset:32768\n"
"ds_read_b64_tr_b16 v[222:223], v232 offset:59392\n"
"v_mfma_f32_16x16x32_bf16 v[152:155], v[100:103], a[116:119], v[152:155]\n"
"s_mov_b32 s87, 131072\n"
"ds_read_b64_tr_b16 a[224:225], v236 offset:14336\n"
"v_mfma_f32_16x16x32_bf16 v[152:155], v[96:99], a[112:115], v[152:155]\n"
"ds_read_b64_tr_b16 v[192:193], v232 offset:40960\n"
"ds_read_b64_tr_b16 v[214:215], v232 offset:51200\n"
"v_mfma_f32_16x16x32_bf16 v[168:171], v[96:99], a[128:131], v[196:199]\n"
"ds_read_b64_tr_b16 v[190:191], v228 offset:43008\n"
"ds_read_b64_tr_b16 v[218:219], v228 offset:59392\n"
"v_mfma_f32_16x16x32_bf16 v[168:171], v[100:103], a[132:135], v[168:171]\n"
"ds_read_b64_tr_b16 v[194:195], v232 offset:43008\n"
"ds_read_b64_tr_b16 v[210:211], v228 offset:51200\n"
"v_mfma_f32_16x16x32_bf16 v[168:171], v[104:107], a[136:139], v[168:171]\n"
"ds_read_b64_tr_b16 a[234:235], v236 offset:10248\n"
"ds_read_b64_tr_b16 v[208:209], v228 offset:49152\n"
"v_mfma_f32_16x16x32_bf16 v[168:171], v[108:111], a[140:143], v[168:171]\n"
"s_waitcnt lgkmcnt(12)\n"
"ds_read_b64_tr_b16 a[212:213], v237 offset:12288\n"
"ds_read_b64_tr_b16 a[230:231], v237 offset:14344\n"
"v_mfma_f32_16x16x32_bf16 v[168:171], v[112:115], a[176:179], v[168:171]\n"
"ds_read_b64_tr_b16 a[232:233], v236 offset:10240\n"
"ds_read_b64_tr_b16 a[214:215], v237 offset:12296\n"
"v_mfma_f32_16x16x32_bf16 v[168:171], v[116:119], a[180:183], v[168:171]\n"
"ds_read_b64_tr_b16 v[96:97], v229 offset:32768\n"
"ds_read_b64_tr_b16 v[100:101], v233 offset:32768\n"
"v_mfma_f32_16x16x32_bf16 v[168:171], v[120:123], a[184:187], v[168:171]\n"
"s_add_u32 s41, s70, 327680\n"
"s_add_u32 m0, s71, 32768\n"
"ds_read_b64_tr_b16 v[104:105], v229 offset:40960\n"
"v_mfma_f32_16x16x32_bf16 v[168:171], v[224:227], a[188:191], v[168:171]\n"
"buffer_load_dwordx4 v240, s[28:31], s41 offen lds\n"
"v_mfma_f32_16x16x32_bf16 v[124:127], v[180:183], a[48:51], v[124:127]\n"
"s_waitcnt lgkmcnt(11)\n"
"v_and_b32 v243, 0x3f0, v242\n"
"v_mov_b64 v[246:247], 0\n"
"v_mfma_f32_16x16x32_bf16 v[124:127], v[184:187], a[52:55], v[124:127]\n"
"v_bfe_u32 v255, v242, 8, 1\n"
"v_mfma_f32_16x16x32_bf16 v[124:127], v[188:191], a[56:59], v[124:127]\n"
"s_waitcnt lgkmcnt(7)\n"
"s_add_u32 s40, s41, s52\n"
"ds_read_b64_tr_b16 a[210:211], v236 offset:12296\n"
"ds_read_b64_tr_b16 v[98:99], v229 offset:34816\n"
"v_mfma_f32_16x16x32_bf16 v[124:127], v[192:195], a[60:63], v[124:127]\n"
"buffer_load_dwordx4 v240, s[28:31], s40 offen offset:1024 lds\n"
"v_mfma_f32_16x16x32_bf16 v[124:127], v[208:211], a[0:3], v[124:127]\n"
"v_xor_b32 v248, s75, v248\n"
"v_mov_b64 v[52:53], s[22:23]\n"
"v_mfma_f32_16x16x32_bf16 v[124:127], v[212:215], a[4:7], v[124:127]\n"
"v_mov_b64 v[6:7], s[22:23]\n"
"v_mfma_f32_16x16x32_bf16 v[124:127], v[216:219], a[8:11], v[124:127]\n"
"s_add_u32 s40, s41, s53\n"
"ds_read_b64_tr_b16 v[102:103], v233 offset:34816\n"
"ds_read_b64_tr_b16 v[110:111], v233 offset:43008\n"
"v_mfma_f32_16x16x32_bf16 v[124:127], v[220:223], a[12:15], v[124:127]\n"
"buffer_load_dwordx4 v240, s[28:31], s40 offen offset:2048 lds\n"
"v_mfma_f32_16x16x32_bf16 v[140:143], v[220:223], a[28:31], v[140:143]\n"
"v_mov_b64 v[2:3], s[22:23]\n"
"v_mov_b64 v[10:11], s[22:23]\n"
"v_mfma_f32_16x16x32_bf16 v[140:143], v[216:219], a[24:27], v[140:143]\n"
"v_mfma_f32_16x16x32_bf16 v[140:143], v[212:215], a[20:23], v[140:143]\n"
"ds_read_b64_tr_b16 a[220:221], v237 offset:8192\n"
"ds_read_b64_tr_b16 v[116:117], v233 offset:49152\n"
"v_mfma_f32_16x16x32_bf16 v[140:143], v[208:211], a[16:19], v[140:143]\n"
"ds_read_b64_tr_b16 a[208:209], v236 offset:12288\n"
"v_mul_u32_u24 v254, 2560, v254\n"
"v_mfma_f32_16x16x32_bf16 v[140:143], v[192:195], a[76:79], v[140:143]\n"
"ds_read_b64_tr_b16 v[122:123], v229 offset:59392\n"
"v_subrev_f32 v124, s22, v124\n"
"v_mfma_f32_16x16x32_bf16 v[140:143], v[188:191], a[72:75], v[140:143]\n"
"ds_read_b64_tr_b16 a[226:227], v236 offset:14344\n"
"v_mov_b64 v[18:19], s[22:23]\n"
"v_mfma_f32_16x16x32_bf16 v[140:143], v[184:187], a[68:71], v[140:143]\n"
"s_add_u32 s40, s41, s54\n"
"ds_read_b64_tr_b16 v[112:113], v229 offset:49152\n"
"ds_read_b64_tr_b16 v[120:121], v229 offset:57344\n"
"v_mfma_f32_16x16x32_bf16 v[140:143], v[180:183], a[64:67], v[140:143]\n"
"buffer_load_dwordx4 v240, s[28:31], s40 offen offset:3072 lds\n"
"v_mfma_f32_16x16x32_bf16 v[156:159], v[180:183], a[80:83], v[156:159]\n"
"s_add_u32 s41, s70, 491520\n"
"s_add_u32 m0, s71, 49152\n"
"v_lshl_add_u32 v254, v255, 5, v254\n"
"v_mfma_f32_16x16x32_bf16 v[156:159], v[184:187], a[84:87], v[156:159]\n"
"buffer_load_dwordx4 v240, s[28:31], s41 offen lds\n"
"v_mfma_f32_16x16x32_bf16 v[156:159], v[188:191], a[88:91], v[156:159]\n"
"v_subrev_f32 v125, s22, v125\n"
"v_cvt_pk_bf16_f32 v124, v124, v125\n"
"v_mfma_f32_16x16x32_bf16 v[156:159], v[192:195], a[92:95], v[156:159]\n"
"v_bfe_u32 v255, v242, 9, 1\n"
"v_mfma_f32_16x16x32_bf16 v[156:159], v[208:211], a[32:35], v[156:159]\n"
"ds_read_b64_tr_b16 v[106:107], v229 offset:43008\n"
"v_lshl_add_u32 v254, v255, 4, v254\n"
"v_mfma_f32_16x16x32_bf16 v[156:159], v[212:215], a[36:39], v[156:159]\n"
"ds_read_b64_tr_b16 v[114:115], v229 offset:51200\n"
"v_subrev_f32 v142, s22, v142\n"
"v_mfma_f32_16x16x32_bf16 v[156:159], v[216:219], a[40:43], v[156:159]\n"
"s_add_u32 s40, s41, s52\n"
"ds_read_b64_tr_b16 v[108:109], v233 offset:40960\n"
"ds_read_b64_tr_b16 v[118:119], v233 offset:51200\n"
"v_mfma_f32_16x16x32_bf16 v[156:159], v[220:223], a[44:47], v[156:159]\n"
"s_waitcnt lgkmcnt(4)\n"
"buffer_load_dwordx4 v240, s[28:31], s40 offen offset:1024 lds\n"
"v_mfma_f32_16x16x32_bf16 v[172:175], v[220:223], a[228:231], v[172:175]\n"
"s_add_u32 s40, s41, s53\n"
"v_mov_b64 v[32:33], s[22:23]\n"
"v_mov_b64 v[34:35], s[22:23]\n"
"v_mfma_f32_16x16x32_bf16 v[172:175], v[216:219], a[224:227], v[172:175]\n"
"buffer_load_dwordx4 v240, s[28:31], s40 offen offset:2048 lds\n"
"v_mfma_f32_16x16x32_bf16 v[172:175], v[212:215], a[212:215], v[172:175]\n"
"v_mov_b64 v[88:89], 0\n"
"v_subrev_f32 v141, s22, v141\n"
"v_mfma_f32_16x16x32_bf16 v[172:175], v[208:211], a[208:211], v[172:175]\n"
"v_add_u32 v236, s94, v243\n"
"v_mov_b32 v241, s60\n"
"v_mfma_f32_16x16x32_bf16 v[172:175], v[192:195], a[236:239], v[172:175]\n"
"s_add_u32 s40, s41, s54\n"
"ds_read_b64_tr_b16 v[224:225], v233 offset:57344\n"
"ds_read_b64_tr_b16 v[226:227], v233 offset:59392\n"
"v_mfma_f32_16x16x32_bf16 v[172:175], v[188:191], a[232:235], v[172:175]\n"
"buffer_load_dwordx4 v240, s[28:31], s40 offen offset:3072 lds\n"
"v_mfma_f32_16x16x32_bf16 v[172:175], v[184:187], a[220:223], v[172:175]\n"
"s_lshl_b32 s62, s70, 9\n"
"v_xor_b32 v228, s75, v228\n"
"v_mfma_f32_16x16x32_bf16 v[172:175], v[180:183], a[216:219], v[172:175]\n"
"s_and_b32 s70, s88, 15\n"
"v_mov_b64 v[26:27], s[22:23]\n"
"v_mfma_f32_16x16x32_bf16 v[128:131], v[96:99], a[48:51], v[128:131]\n"
"s_lshr_b32 s88, s88, 4\n"
"ds_read_b64_tr_b16 v[186:187], v234 offset:34816\n"
"v_mfma_f32_16x16x32_bf16 v[128:131], v[100:103], a[52:55], v[128:131]\n"
"s_waitcnt lgkmcnt(3)\n"
"s_lshl_b32 s70, s70, 7\n"
"ds_read_b64_tr_b16 v[180:181], v230 offset:32768\n"
"v_subrev_f32 v143, s22, v143\n"
"v_mfma_f32_16x16x32_bf16 v[128:131], v[104:107], a[56:59], v[128:131]\n"
"s_xor_b32 s71, s71, s75\n"
"v_mov_b64 v[86:87], 0\n"
"v_subrev_f32 v127, s22, v127\n"
"v_mfma_f32_16x16x32_bf16 v[128:131], v[108:111], a[60:63], v[128:131]\n"
"v_lshlrev_b32_e64 v201, 2, s72\n"
"ds_read_b64_tr_b16 v[214:215], v234 offset:51200\n"
"v_subrev_f32 v173, s22, v173\n"
"v_mfma_f32_16x16x32_bf16 v[128:131], v[112:115], a[0:3], v[128:131]\n"
"global_load_dword v200, v201, s[6:7] sc1\n"
"v_mfma_f32_16x16x32_bf16 v[128:131], v[116:119], a[4:7], v[128:131]\n"
"v_mov_b64 v[28:29], s[22:23]\n"
"v_subrev_f32 v140, s22, v140\n"
"v_mfma_f32_16x16x32_bf16 v[128:131], v[120:123], a[8:11], v[128:131]\n"
"s_waitcnt lgkmcnt(3)\n"
"v_mov_b64 v[38:39], s[22:23]\n"
"v_add_u32 v241, 1, v241\n"
"v_mfma_f32_16x16x32_bf16 v[128:131], v[224:227], a[12:15], v[128:131]\n"
"ds_read_b64_tr_b16 v[208:209], v230 offset:49152\n"
"v_subrev_f32 v126, s22, v126\n"
"v_mfma_f32_16x16x32_bf16 v[144:147], v[224:227], a[28:31], v[144:147]\n"
"v_mov_b64 v[94:95], 0\n"
"v_cvt_pk_bf16_f32 v140, v140, v141\n"
"v_mfma_f32_16x16x32_bf16 v[144:147], v[120:123], a[24:27], v[144:147]\n"
"ds_read_b64_tr_b16 v[192:193], v234 offset:40960\n"
"v_subrev_f32 v175, s22, v175\n"
"v_mfma_f32_16x16x32_bf16 v[144:147], v[116:119], a[20:23], v[144:147]\n"
"ds_read_b64_tr_b16 v[222:223], v234 offset:59392\n"
"v_subrev_f32 v129, s22, v129\n"
"v_mfma_f32_16x16x32_bf16 v[144:147], v[112:115], a[16:19], v[144:147]\n"
"ds_read_b64_tr_b16 v[188:189], v230 offset:40960\n"
"v_lshl_add_u32 v248, v242, 8, s93\n"
"v_mfma_f32_16x16x32_bf16 v[144:147], v[108:111], a[76:79], v[144:147]\n"
"ds_read_b64_tr_b16 v[182:183], v230 offset:34816\n"
"v_cvt_pk_bf16_f32 v125, v126, v127\n"
"v_mfma_f32_16x16x32_bf16 v[144:147], v[104:107], a[72:75], v[144:147]\n"
"v_mov_b64 v[8:9], s[22:23]\n"
"v_subrev_f32 v130, s22, v130\n"
"v_mfma_f32_16x16x32_bf16 v[144:147], v[100:103], a[68:71], v[144:147]\n"
"ds_read_b64_tr_b16 v[212:213], v234 offset:49152\n"
"v_subrev_f32 v172, s22, v172\n"
"v_mfma_f32_16x16x32_bf16 v[144:147], v[96:99], a[64:67], v[144:147]\n"
"ds_read_b64_tr_b16 v[220:221], v234 offset:57344\n"
"v_mfma_f32_16x16x32_bf16 v[160:163], v[96:99], a[80:83], v[160:163]\n"
"ds_read_b64_tr_b16 v[216:217], v230 offset:57344\n"
"v_add_u32 v238, s97, v254\n"
"v_mfma_f32_16x16x32_bf16 v[160:163], v[100:103], a[84:87], v[160:163]\n"
"v_lshl_add_u32 v243, s44, 2, v243\n"
"v_bfe_u32 v237, v242, 4, 3\n"
"v_mfma_f32_16x16x32_bf16 v[160:163], v[104:107], a[88:91], v[160:163]\n"
"ds_read_b64_tr_b16 v[190:191], v230 offset:43008\n"
"v_subrev_f32 v128, s22, v128\n"
"v_mfma_f32_16x16x32_bf16 v[160:163], v[108:111], a[92:95], v[160:163]\n"
"v_max_u32 v237, 1, v237\n"
"v_subrev_f32 v156, s22, v156\n"
"v_mfma_f32_16x16x32_bf16 v[160:163], v[112:115], a[32:35], v[160:163]\n"
"ds_read_b64_tr_b16 v[184:185], v234 offset:32768\n"
"v_subrev_f32 v159, s22, v159\n"
"v_mfma_f32_16x16x32_bf16 v[160:163], v[116:119], a[36:39], v[160:163]\n"
"v_subrev_f32 v131, s22, v131\n"
"v_cvt_pk_bf16_f32 v126, v128, v129\n"
"v_mfma_f32_16x16x32_bf16 v[160:163], v[120:123], a[40:43], v[160:163]\n"
"ds_read_b64_tr_b16 v[194:195], v234 offset:43008\n"
"v_cvt_pk_bf16_f32 v127, v130, v131\n"
"v_mfma_f32_16x16x32_bf16 v[160:163], v[224:227], a[44:47], v[160:163]\n"
"buffer_store_dwordx4 v[124:127], v243, s[32:35], s16 offen sc1\n"
"v_subrev_f32 v147, s22, v147\n"
"v_mfma_f32_16x16x32_bf16 v[176:179], v[224:227], a[228:231], v[176:179]\n"
"ds_read_b64_tr_b16 v[218:219], v230 offset:59392\n"
"v_mfma_f32_16x16x32_bf16 v[176:179], v[120:123], a[224:227], v[176:179]\n"
"v_subrev_f32 v157, s22, v157\n"
"v_subrev_f32 v146, s22, v146\n"
"v_mfma_f32_16x16x32_bf16 v[176:179], v[116:119], a[212:215], v[176:179]\n"
"ds_read_b64_tr_b16 v[210:211], v230 offset:51200\n"
"v_mfma_f32_16x16x32_bf16 v[176:179], v[112:115], a[208:211], v[176:179]\n"
"v_cvt_pk_bf16_f32 v141, v142, v143\n"
"v_subrev_f32 v145, s22, v145\n"
"v_mfma_f32_16x16x32_bf16 v[176:179], v[108:111], a[236:239], v[176:179]\n"
"ds_read_b64_tr_b16 v[226:227], v235 offset:59392\n"
"v_subrev_f32 v158, s22, v158\n"
"v_mfma_f32_16x16x32_bf16 v[176:179], v[104:107], a[232:235], v[176:179]\n"
"v_subrev_f32 v174, s22, v174\n"
"v_mfma_f32_16x16x32_bf16 v[176:179], v[100:103], a[220:223], v[176:179]\n"
"ds_read_b64_tr_b16 v[100:101], v235 offset:32768\n"
"v_mov_b64 v[36:37], s[22:23]\n"
"v_mfma_f32_16x16x32_bf16 v[176:179], v[96:99], a[216:219], v[176:179]\n"
"s_waitcnt lgkmcnt(4)\n"
"v_subrev_f32 v144, s22, v144\n"
"v_subrev_f32 v162, s22, v162\n"
"v_mfma_f32_16x16x32_bf16 v[132:135], v[180:183], a[48:51], v[132:135]\n"
"ds_read_b64_tr_b16 v[106:107], v231 offset:43008\n"
"v_subrev_f32 v160, s22, v160\n"
"v_mfma_f32_16x16x32_bf16 v[132:135], v[184:187], a[52:55], v[132:135]\n"
"ds_read_b64_tr_b16 v[96:97], v231 offset:32768\n"
"v_mfma_f32_16x16x32_bf16 v[132:135], v[188:191], a[56:59], v[132:135]\n"
"v_cvt_pk_bf16_f32 v142, v144, v145\n"
"v_cvt_pk_bf16_f32 v143, v146, v147\n"
"v_mfma_f32_16x16x32_bf16 v[132:135], v[192:195], a[60:63], v[132:135]\n"
"s_waitcnt lgkmcnt(4)\n"
"ds_read_b64_tr_b16 v[112:113], v231 offset:49152\n"
"v_mov_b64 v[50:51], s[22:23]\n"
"v_mfma_f32_16x16x32_bf16 v[132:135], v[208:211], a[0:3], v[132:135]\n"
"v_xor_b32 v237, s61, v237\n"
"v_cvt_pk_bf16_f32 v172, v172, v173\n"
"v_mfma_f32_16x16x32_bf16 v[132:135], v[212:215], a[4:7], v[132:135]\n"
"ds_read_b64_tr_b16 v[116:117], v235 offset:49152\n"
"v_xor_b32 v233, s75, v233\n"
"v_mfma_f32_16x16x32_bf16 v[132:135], v[216:219], a[8:11], v[132:135]\n"
"v_mov_b64 v[30:31], s[22:23]\n"
"v_subrev_f32 v179, s22, v179\n"
"v_mfma_f32_16x16x32_bf16 v[132:135], v[220:223], a[12:15], v[132:135]\n"
"buffer_store_dwordx4 v[140:143], v243, s[32:35], s16 offen offset:2048 sc1\n"
"ds_read_b64_tr_b16 v[120:121], v231 offset:57344\n"
"v_mfma_f32_16x16x32_bf16 v[148:151], v[220:223], a[28:31], v[148:151]\n"
"v_cvt_pk_bf16_f32 v156, v156, v157\n"
"v_subrev_f32 v161, s22, v161\n"
"v_mfma_f32_16x16x32_bf16 v[148:151], v[216:219], a[24:27], v[148:151]\n"
"ds_read_b64_tr_b16 v[110:111], v235 offset:43008\n"
"v_subrev_f32 v178, s22, v178\n"
"v_mfma_f32_16x16x32_bf16 v[148:151], v[212:215], a[20:23], v[148:151]\n"
"ds_read_b64_tr_b16 v[102:103], v235 offset:34816\n"
"v_mov_b64 v[48:49], s[22:23]\n"
"v_mfma_f32_16x16x32_bf16 v[148:151], v[208:211], a[16:19], v[148:151]\n"
"v_add_u32 v253, s99, v254\n"
"v_subrev_f32 v132, s22, v132\n"
"v_mfma_f32_16x16x32_bf16 v[148:151], v[192:195], a[76:79], v[148:151]\n"
"ds_read_b64_tr_b16 v[98:99], v231 offset:34816\n"
"v_cvt_pk_bf16_f32 v157, v158, v159\n"
"v_mfma_f32_16x16x32_bf16 v[148:151], v[188:191], a[72:75], v[148:151]\n"
"ds_read_b64_tr_b16 v[108:109], v235 offset:40960\n"
"v_subrev_f32 v135, s22, v135\n"
"v_mfma_f32_16x16x32_bf16 v[148:151], v[184:187], a[68:71], v[148:151]\n"
"v_mfma_f32_16x16x32_bf16 v[148:151], v[180:183], a[64:67], v[148:151]\n"
"ds_read_b64_tr_b16 v[118:119], v235 offset:51200\n"
"v_mfma_f32_16x16x32_bf16 v[164:167], v[180:183], a[80:83], v[164:167]\n"
"ds_read_b64_tr_b16 v[104:105], v231 offset:40960\n"
"v_subrev_f32 v163, s22, v163\n"
"v_mfma_f32_16x16x32_bf16 v[164:167], v[184:187], a[84:87], v[164:167]\n"
"ds_read_b64_tr_b16 v[114:115], v231 offset:51200\n"
"v_mov_b64 v[84:85], 0\n"
"v_mfma_f32_16x16x32_bf16 v[164:167], v[188:191], a[88:91], v[164:167]\n"
"v_cvt_pk_bf16_f32 v158, v160, v161\n"
"v_subrev_f32 v133, s22, v133\n"
"v_mfma_f32_16x16x32_bf16 v[164:167], v[192:195], a[92:95], v[164:167]\n"
"ds_read_b64_tr_b16 v[122:123], v231 offset:59392\n"
"v_mov_b64 v[14:15], s[22:23]\n"
"v_mfma_f32_16x16x32_bf16 v[164:167], v[208:211], a[32:35], v[164:167]\n"
"v_mov_b64 v[24:25], s[22:23]\n"
"v_xor_b32 v232, s75, v232\n"
"v_mfma_f32_16x16x32_bf16 v[164:167], v[212:215], a[36:39], v[164:167]\n"
"ds_read_b64_tr_b16 v[224:225], v235 offset:57344\n"
"v_cvt_pk_bf16_f32 v159, v162, v163\n"
"v_mfma_f32_16x16x32_bf16 v[164:167], v[216:219], a[40:43], v[164:167]\n"
"v_cvt_pk_bf16_f32 v173, v174, v175\n"
"v_mfma_f32_16x16x32_bf16 v[164:167], v[220:223], a[44:47], v[164:167]\n"
"s_barrier\n"
"s_waitcnt lgkmcnt(4)\n"
"v_mfma_f32_16x16x32_bf16 v[136:139], v[96:99], a[48:51], v[136:139]\n"
"ds_read_b64_tr_b16 v[214:215], v232 offset:18432\n"
"v_cvt_pk_bf16_f32 v175, v178, v179\n"
"v_mfma_f32_16x16x32_bf16 v[136:139], v[100:103], a[52:55], v[136:139]\n"
"s_waitcnt lgkmcnt(3)\n"
"ds_read_b64_tr_b16 v[218:219], v228 offset:26624\n"
"v_mov_b64 v[0:1], s[22:23]\n"
"v_mfma_f32_16x16x32_bf16 v[136:139], v[104:107], a[56:59], v[136:139]\n"
"ds_read_b64_tr_b16 v[222:223], v232 offset:26624\n"
"v_mov_b64 v[46:47], s[22:23]\n"
"v_mfma_f32_16x16x32_bf16 v[136:139], v[108:111], a[60:63], v[136:139]\n"
"ds_read_b64_tr_b16 v[216:217], v228 offset:24576\n"
"v_mov_b64 v[44:45], s[22:23]\n"
"v_mfma_f32_16x16x32_bf16 v[136:139], v[112:115], a[0:3], v[136:139]\n"
"ds_read_b64_tr_b16 v[212:213], v232 offset:16384\n"
"v_lshlrev_b32 v237, 2, v237\n"
"v_mfma_f32_16x16x32_bf16 v[136:139], v[116:119], a[4:7], v[136:139]\n"
"s_waitcnt lgkmcnt(5)\n"
"ds_read_b64_tr_b16 v[180:181], v228 offset:0\n"
"v_cvt_pk_bf16_f32 v132, v132, v133\n"
"v_mfma_f32_16x16x32_bf16 v[136:139], v[120:123], a[8:11], v[136:139]\n"
"ds_read_b64_tr_b16 v[190:191], v228 offset:10240\n"
"v_xor_b32 v229, s75, v229\n"
"v_mfma_f32_16x16x32_bf16 v[136:139], v[224:227], a[12:15], v[136:139]\n"
"buffer_store_dwordx4 v[156:159], v243, s[32:35], s17 offen sc1\n"
"ds_read_b64_tr_b16 v[186:187], v232 offset:2048\n"
"v_mfma_f32_16x16x32_bf16 v[152:155], v[224:227], a[28:31], v[152:155]\n"
"ds_read_b64_tr_b16 v[220:221], v232 offset:24576\n"
"v_xor_b32 v239, 64, v238\n"
"v_mfma_f32_16x16x32_bf16 v[152:155], v[120:123], a[24:27], v[152:155]\n"
"ds_read_b64_tr_b16 v[210:211], v228 offset:18432\n"
"v_mfma_f32_16x16x32_bf16 v[152:155], v[116:119], a[20:23], v[152:155]\n"
"ds_read_b64_tr_b16 v[208:209], v228 offset:16384\n"
"v_subrev_f32 v134, s22, v134\n"
"v_mfma_f32_16x16x32_bf16 v[152:155], v[112:115], a[16:19], v[152:155]\n"
"ds_read_b64_tr_b16 v[194:195], v232 offset:10240\n"
"v_mov_b64 v[42:43], s[22:23]\n"
"v_mfma_f32_16x16x32_bf16 v[152:155], v[108:111], a[76:79], v[152:155]\n"
"ds_read_b64_tr_b16 v[182:183], v228 offset:2048\n"
"v_add_u32 v252, s98, v254\n"
"v_mfma_f32_16x16x32_bf16 v[152:155], v[104:107], a[72:75], v[152:155]\n"
"ds_read_b64_tr_b16 v[192:193], v232 offset:8192\n"
"v_xor_b32 v249, s75, v249\n"
"v_mfma_f32_16x16x32_bf16 v[152:155], v[100:103], a[68:71], v[152:155]\n"
"ds_read_b64_tr_b16 v[184:185], v232 offset:0\n"
"ds_read_b64_tr_b16 v[188:189], v228 offset:8192\n"
"v_mfma_f32_16x16x32_bf16 v[152:155], v[96:99], a[64:67], v[152:155]\n"
"v_mov_b64 v[4:5], s[22:23]\n"
"v_subrev_f32 v176, s22, v176\n"
"v_mfma_f32_16x16x32_bf16 v[168:171], v[96:99], a[80:83], v[168:171]\n"
"ds_read_b64_tr_b16 v[96:97], v229 offset:0\n"
"ds_read_b64_tr_b16 v[98:99], v229 offset:2048\n"
"v_mfma_f32_16x16x32_bf16 v[168:171], v[100:103], a[84:87], v[168:171]\n"
"ds_read_b64_tr_b16 v[100:101], v233 offset:0\n"
"ds_read_b64_tr_b16 v[102:103], v233 offset:2048\n"
"v_mfma_f32_16x16x32_bf16 v[168:171], v[104:107], a[88:91], v[168:171]\n"
"ds_read_b64_tr_b16 v[104:105], v229 offset:8192\n"
"v_subrev_f32 v177, s22, v177\n"
"v_mfma_f32_16x16x32_bf16 v[168:171], v[108:111], a[92:95], v[168:171]\n"
"v_mov_b64 v[12:13], s[22:23]\n"
"v_mov_b64 v[22:23], s[22:23]\n"
"v_mfma_f32_16x16x32_bf16 v[168:171], v[112:115], a[32:35], v[168:171]\n"
"v_mov_b64 v[40:41], s[22:23]\n"
"v_cvt_pk_bf16_f32 v174, v176, v177\n"
"v_mfma_f32_16x16x32_bf16 v[168:171], v[116:119], a[36:39], v[168:171]\n"
"v_mov_b64 v[16:17], s[22:23]\n"
"v_mfma_f32_16x16x32_bf16 v[168:171], v[120:123], a[40:43], v[168:171]\n"
"s_add_u32 s47, s47, 1\n"
"v_mov_b64 v[20:21], s[22:23]\n"
"v_mov_b64 v[92:93], 0\n"
"v_mfma_f32_16x16x32_bf16 v[168:171], v[224:227], a[44:47], v[168:171]\n"
"s_waitcnt lgkmcnt(5)\n"
"v_mov_b64 v[54:55], s[22:23]\n"
"v_mov_b64 v[244:245], 0\n"
"LGL_%=:\n"
"v_mfma_f32_16x16x32_bf16 v[124:127], v[180:183], a[96:99], v[196:199]\n"
"ds_read_b64_tr_b16 v[106:107], v229 offset:10240\n"
"v_cvt_pk_bf16_f32 v133, v134, v135\n"
"v_mfma_f32_16x16x32_bf16 v[124:127], v[184:187], a[100:103], v[124:127]\n"
"ds_read_b64_tr_b16 v[108:109], v233 offset:8192\n"
"v_subrev_f32 v136, s22, v136\n"
"v_mfma_f32_16x16x32_bf16 v[124:127], v[188:191], a[104:107], v[124:127]\n"
"ds_read_b64_tr_b16 v[110:111], v233 offset:10240\n"
"v_subrev_f32 v137, s22, v137\n"
"v_mfma_f32_16x16x32_bf16 v[124:127], v[192:195], a[108:111], v[124:127]\n"
"ds_read_b64_tr_b16 v[112:113], v229 offset:16384\n"
"v_subrev_f32 v138, s22, v138\n"
"v_mfma_f32_16x16x32_bf16 v[124:127], v[208:211], a[144:147], v[124:127]\n"
"ds_read_b64_tr_b16 v[114:115], v229 offset:18432\n"
"v_subrev_f32 v139, s22, v139\n"
"v_mfma_f32_16x16x32_bf16 v[124:127], v[212:215], a[148:151], v[124:127]\n"
"s_waitcnt lgkmcnt(7)\n"
"ds_read_b64_tr_b16 v[116:117], v233 offset:16384\n"
"v_cvt_pk_bf16_f32 v134, v136, v137\n"
"v_mfma_f32_16x16x32_bf16 v[124:127], v[216:219], a[152:155], v[124:127]\n"
"ds_read_b64_tr_b16 v[118:119], v233 offset:18432\n"
"v_cvt_pk_bf16_f32 v135, v138, v139\n"
"v_mfma_f32_16x16x32_bf16 v[124:127], v[220:223], a[156:159], v[124:127]\n"
"buffer_store_dwordx4 v[172:175], v243, s[32:35], s17 offen offset:2048 sc1\n"
"v_subrev_f32 v148, s22, v148\n"
"v_mfma_f32_16x16x32_bf16 v[140:143], v[220:223], a[172:175], v[196:199]\n"
"ds_read_b64_tr_b16 v[120:121], v229 offset:24576\n"
"v_subrev_f32 v149, s22, v149\n"
"v_mfma_f32_16x16x32_bf16 v[140:143], v[216:219], a[168:171], v[140:143]\n"
"ds_read_b64_tr_b16 v[122:123], v229 offset:26624\n"
"v_subrev_f32 v150, s22, v150\n"
"v_mfma_f32_16x16x32_bf16 v[140:143], v[212:215], a[164:167], v[140:143]\n"
"ds_read_b64_tr_b16 v[224:225], v233 offset:24576\n"
"v_subrev_f32 v151, s22, v151\n"
"v_mfma_f32_16x16x32_bf16 v[140:143], v[208:211], a[160:163], v[140:143]\n"
"ds_read_b64_tr_b16 v[226:227], v233 offset:26624\n"
"v_cvt_pk_bf16_f32 v148, v148, v149\n"
"v_mfma_f32_16x16x32_bf16 v[140:143], v[192:195], a[124:127], v[140:143]\n"
"v_cvt_pk_bf16_f32 v149, v150, v151\n"
"v_subrev_f32 v152, s22, v152\n"
"v_mfma_f32_16x16x32_bf16 v[140:143], v[188:191], a[120:123], v[140:143]\n"
"s_add_u32 s41, s70, 0\n"
"v_subrev_f32 v153, s22, v153\n"
"v_subrev_f32 v154, s22, v154\n"
"v_mfma_f32_16x16x32_bf16 v[140:143], v[184:187], a[116:119], v[140:143]\n"
"s_mov_b32 m0, s71\n"
"v_subrev_f32 v155, s22, v155\n"
"v_cvt_pk_bf16_f32 v150, v152, v153\n"
"v_mfma_f32_16x16x32_bf16 v[140:143], v[180:183], a[112:115], v[140:143]\n"
"s_waitcnt vmcnt(14)\n"
"buffer_load_dwordx4 v240, s[28:31], s41 offen lds\n"
"v_mfma_f32_16x16x32_bf16 v[156:159], v[180:183], a[128:131], v[196:199]\n"
"v_xor_b32 v230, s75, v230\n"
"v_cvt_pk_bf16_f32 v151, v154, v155\n"
"v_mfma_f32_16x16x32_bf16 v[156:159], v[184:187], a[132:135], v[156:159]\n"
"v_xor_b32 v234, s75, v234\n"
"v_subrev_f32 v164, s22, v164\n"
"v_mfma_f32_16x16x32_bf16 v[156:159], v[188:191], a[136:139], v[156:159]\n"
"v_subrev_f32 v165, s22, v165\n"
"v_subrev_f32 v166, s22, v166\n"
"v_mfma_f32_16x16x32_bf16 v[156:159], v[192:195], a[140:143], v[156:159]\n"
"v_subrev_f32 v167, s22, v167\n"
"v_cvt_pk_bf16_f32 v164, v164, v165\n"
"v_mfma_f32_16x16x32_bf16 v[156:159], v[208:211], a[176:179], v[156:159]\n"
"v_cvt_pk_bf16_f32 v165, v166, v167\n"
"v_subrev_f32 v168, s22, v168\n"
"v_mfma_f32_16x16x32_bf16 v[156:159], v[212:215], a[180:183], v[156:159]\n"
"v_subrev_f32 v169, s22, v169\n"
"v_subrev_f32 v170, s22, v170\n"
"v_mfma_f32_16x16x32_bf16 v[156:159], v[216:219], a[184:187], v[156:159]\n"
"v_subrev_f32 v171, s22, v171\n"
"v_cvt_pk_bf16_f32 v166, v168, v169\n"
"v_mfma_f32_16x16x32_bf16 v[156:159], v[220:223], a[188:191], v[156:159]\n"
"buffer_store_dwordx4 v[132:135], v243, s[32:35], s16 offen offset:1024 sc1\n"
"v_cvt_pk_bf16_f32 v167, v170, v171\n"
"v_mfma_f32_16x16x32_bf16 v[172:175], v[220:223], a[252:255], v[196:199]\n"
"ds_read_b64_tr_b16 v[220:221], v234 offset:24576\n"
"v_xor_b32 v231, s75, v231\n"
"v_mfma_f32_16x16x32_bf16 v[172:175], v[216:219], a[248:251], v[172:175]\n"
"ds_read_b64_tr_b16 v[216:217], v230 offset:24576\n"
"v_xor_b32 v235, s75, v235\n"
"v_mfma_f32_16x16x32_bf16 v[172:175], v[212:215], a[244:247], v[172:175]\n"
"ds_read_b64_tr_b16 v[212:213], v234 offset:16384\n"
"v_lshlrev_b32 v202, 16, v84\n"
"v_mfma_f32_16x16x32_bf16 v[172:175], v[208:211], a[240:243], v[172:175]\n"
"ds_read_b64_tr_b16 v[208:209], v230 offset:16384\n"
"v_and_b32 v84, s73, v84\n"
"v_mfma_f32_16x16x32_bf16 v[172:175], v[192:195], a[204:207], v[172:175]\n"
"ds_read_b64_tr_b16 v[192:193], v234 offset:8192\n"
"v_add_f32 v25, v25, v84\n"
"v_mfma_f32_16x16x32_bf16 v[172:175], v[188:191], a[200:203], v[172:175]\n"
"ds_read_b64_tr_b16 v[188:189], v230 offset:8192\n"
"v_lshlrev_b32 v203, 16, v85\n"
"v_mfma_f32_16x16x32_bf16 v[172:175], v[184:187], a[196:199], v[172:175]\n"
"s_add_u32 s40, s41, s52\n"
"ds_read_b64_tr_b16 v[184:185], v234 offset:0\n"
"v_and_b32 v85, s73, v85\n"
"v_mfma_f32_16x16x32_bf16 v[172:175], v[180:183], a[192:195], v[172:175]\n"
"s_waitcnt vmcnt(13) lgkmcnt(7)\n"
"buffer_load_dwordx4 v240, s[28:31], s40 offen offset:1024 lds\n"
"v_mfma_f32_16x16x32_bf16 v[128:131], v[96:99], a[96:99], v[196:199]\n"
"s_waitcnt lgkmcnt(6)\n"
"ds_read_b64_tr_b16 v[180:181], v230 offset:0\n"
"v_add_f32 v27, v27, v85\n"
"v_mfma_f32_16x16x32_bf16 v[128:131], v[100:103], a[100:103], v[128:131]\n"
"ds_read_b64_tr_b16 v[182:183], v230 offset:2048\n"
"v_lshlrev_b32 v204, 16, v86\n"
"v_mfma_f32_16x16x32_bf16 v[128:131], v[104:107], a[104:107], v[128:131]\n"
"ds_read_b64_tr_b16 v[186:187], v234 offset:2048\n"
"v_and_b32 v86, s73, v86\n"
"v_mfma_f32_16x16x32_bf16 v[128:131], v[108:111], a[108:111], v[128:131]\n"
"ds_read_b64_tr_b16 v[190:191], v230 offset:10240\n"
"v_add_f32 v29, v29, v86\n"
"v_mfma_f32_16x16x32_bf16 v[128:131], v[112:115], a[144:147], v[128:131]\n"
"s_waitcnt vmcnt(6)\n"
"ds_read_b64_tr_b16 v[194:195], v234 offset:10240\n"
"v_lshlrev_b32 v205, 16, v87\n"
"v_mfma_f32_16x16x32_bf16 v[128:131], v[116:119], a[148:151], v[128:131]\n"
"v_subrev_u32 v200, s21, v200\n"
"ds_read_b64_tr_b16 v[210:211], v230 offset:18432\n"
"v_mfma_f32_16x16x32_bf16 v[128:131], v[120:123], a[152:155], v[128:131]\n"
"v_cmp_lt_u32 vcc, s69, v200\n"
"ds_read_b64_tr_b16 v[214:215], v234 offset:18432\n"
"v_mfma_f32_16x16x32_bf16 v[128:131], v[224:227], a[156:159], v[128:131]\n"
"ds_read_b64_tr_b16 v[218:219], v230 offset:26624\n"
"s_cbranch_vccnz LGP_%=\nLGPK_%=:\n"
"v_mfma_f32_16x16x32_bf16 v[144:147], v[224:227], a[172:175], v[196:199]\n"
"s_sub_u32 s72, s72, 1\n"
"ds_read_b64_tr_b16 v[222:223], v234 offset:26624\n"
"v_and_b32 v87, s73, v87\n"
"v_mfma_f32_16x16x32_bf16 v[144:147], v[120:123], a[168:171], v[144:147]\n"
"s_and_b32 s72, s72, 7\n"
"v_add_f32 v24, v24, v202\n"
"v_add_f32 v31, v31, v87\n"
"v_mfma_f32_16x16x32_bf16 v[144:147], v[116:119], a[164:167], v[144:147]\n"
"v_add_f32 v26, v26, v203\n"
"v_lshlrev_b32 v206, 16, v88\n"
"v_mfma_f32_16x16x32_bf16 v[144:147], v[112:115], a[160:163], v[144:147]\n"
"buffer_load_dwordx4 v[72:75], v243, s[32:35], s18 offen sc1\n"
"v_mfma_f32_16x16x32_bf16 v[144:147], v[108:111], a[124:127], v[144:147]\n"
"v_and_b32 v88, s73, v88\n"
"v_add_f32 v33, v33, v88\n"
"v_mfma_f32_16x16x32_bf16 v[144:147], v[104:107], a[120:123], v[144:147]\n"
"v_lshlrev_b32 v207, 16, v89\n"
"v_and_b32 v89, s73, v89\n"
"v_mfma_f32_16x16x32_bf16 v[144:147], v[100:103], a[116:119], v[144:147]\n"
"v_add_f32 v35, v35, v89\n"
"v_lshlrev_b32 v202, 16, v90\n"
"v_mfma_f32_16x16x32_bf16 v[144:147], v[96:99], a[112:115], v[144:147]\n"
"buffer_store_dwordx4 v[148:151], v243, s[32:35], s16 offen offset:3072 sc1\n"
"v_and_b32 v90, s73, v90\n"
"v_mfma_f32_16x16x32_bf16 v[160:163], v[96:99], a[128:131], v[196:199]\n"
"v_add_f32 v37, v37, v90\n"
"v_lshlrev_b32 v203, 16, v91\n"
"v_mfma_f32_16x16x32_bf16 v[160:163], v[100:103], a[132:135], v[160:163]\n"
"v_and_b32 v91, s73, v91\n"
"v_add_f32 v39, v39, v91\n"
"v_mfma_f32_16x16x32_bf16 v[160:163], v[104:107], a[136:139], v[160:163]\n"
"v_add_f32 v28, v28, v204\n"
"v_add_f32 v30, v30, v205\n"
"v_mfma_f32_16x16x32_bf16 v[160:163], v[108:111], a[140:143], v[160:163]\n"
"v_add_f32 v32, v32, v206\n"
"v_add_f32 v34, v34, v207\n"
"v_mfma_f32_16x16x32_bf16 v[160:163], v[112:115], a[176:179], v[160:163]\n"
"v_lshlrev_b32 v204, 16, v92\n"
"v_and_b32 v92, s73, v92\n"
"v_mfma_f32_16x16x32_bf16 v[160:163], v[116:119], a[180:183], v[160:163]\n"
"v_add_f32 v41, v41, v92\n"
"v_lshlrev_b32 v205, 16, v93\n"
"v_mfma_f32_16x16x32_bf16 v[160:163], v[120:123], a[184:187], v[160:163]\n"
"s_add_u32 s40, s41, s53\n"
"v_and_b32 v93, s73, v93\n"
"v_add_f32 v43, v43, v93\n"
"v_mfma_f32_16x16x32_bf16 v[160:163], v[224:227], a[188:191], v[160:163]\n"
"buffer_load_dwordx4 v240, s[28:31], s40 offen offset:2048 lds\n"
"v_mfma_f32_16x16x32_bf16 v[176:179], v[224:227], a[252:255], v[196:199]\n"
"ds_read_b64_tr_b16 v[224:225], v235 offset:24576\n"
"v_lshlrev_b32 v206, 16, v94\n"
"v_mfma_f32_16x16x32_bf16 v[176:179], v[120:123], a[248:251], v[176:179]\n"
"ds_read_b64_tr_b16 v[120:121], v231 offset:24576\n"
"v_and_b32 v94, s73, v94\n"
"v_mfma_f32_16x16x32_bf16 v[176:179], v[116:119], a[244:247], v[176:179]\n"
"ds_read_b64_tr_b16 v[116:117], v235 offset:16384\n"
"v_add_f32 v45, v45, v94\n"
"v_mfma_f32_16x16x32_bf16 v[176:179], v[112:115], a[240:243], v[176:179]\n"
"buffer_load_dwordx4 v[76:79], v243, s[32:35], s18 offen offset:1024 sc1\n"
"v_mfma_f32_16x16x32_bf16 v[176:179], v[108:111], a[204:207], v[176:179]\n"
"ds_read_b64_tr_b16 v[108:109], v235 offset:8192\n"
"v_lshlrev_b32 v207, 16, v95\n"
"v_mfma_f32_16x16x32_bf16 v[176:179], v[104:107], a[200:203], v[176:179]\n"
"ds_read_b64_tr_b16 v[104:105], v231 offset:8192\n"
"v_and_b32 v95, s73, v95\n"
"v_mfma_f32_16x16x32_bf16 v[176:179], v[100:103], a[196:199], v[176:179]\n"
"s_add_u32 s40, s41, s54\n"
"ds_read_b64_tr_b16 v[100:101], v235 offset:0\n"
"v_add_f32 v47, v47, v95\n"
"v_mfma_f32_16x16x32_bf16 v[176:179], v[96:99], a[192:195], v[176:179]\n"
"s_waitcnt lgkmcnt(6)\n"
"buffer_load_dwordx4 v240, s[28:31], s40 offen offset:3072 lds\n"
"v_mfma_f32_16x16x32_bf16 v[132:135], v[180:183], a[96:99], v[196:199]\n"
"ds_read_b64_tr_b16 v[96:97], v231 offset:0\n"
"v_add_f32 v36, v36, v202\n"
"v_mfma_f32_16x16x32_bf16 v[132:135], v[184:187], a[100:103], v[132:135]\n"
"ds_read_b64_tr_b16 v[98:99], v231 offset:2048\n"
"v_add_f32 v38, v38, v203\n"
"v_mfma_f32_16x16x32_bf16 v[132:135], v[188:191], a[104:107], v[132:135]\n"
"ds_read_b64_tr_b16 v[102:103], v235 offset:2048\n"
"v_add_f32 v44, v44, v206\n"
"v_mfma_f32_16x16x32_bf16 v[132:135], v[192:195], a[108:111], v[132:135]\n"
"ds_read_b64_tr_b16 v[106:107], v231 offset:10240\n"
"v_add_f32 v46, v46, v207\n"
"v_mfma_f32_16x16x32_bf16 v[132:135], v[208:211], a[144:147], v[132:135]\n"
"ds_read_b64_tr_b16 v[110:111], v235 offset:10240\n"
"v_add_f32 v40, v40, v204\n"
"v_mfma_f32_16x16x32_bf16 v[132:135], v[212:215], a[148:151], v[132:135]\n"
"ds_read_b64_tr_b16 v[112:113], v231 offset:16384\n"
"v_add_f32 v42, v42, v205\n"
"v_mfma_f32_16x16x32_bf16 v[132:135], v[216:219], a[152:155], v[132:135]\n"
"ds_read_b64_tr_b16 v[114:115], v231 offset:18432\n"
"v_mfma_f32_16x16x32_bf16 v[132:135], v[220:223], a[156:159], v[132:135]\n"
"buffer_store_dwordx4 v[164:167], v243, s[32:35], s17 offen offset:1024 sc1\n"
"v_lshlrev_b32 v206, 16, v244\n"
"v_mfma_f32_16x16x32_bf16 v[148:151], v[220:223], a[172:175], v[196:199]\n"
"ds_read_b64_tr_b16 v[118:119], v235 offset:18432\n"
"v_and_b32 v244, s73, v244\n"
"v_mfma_f32_16x16x32_bf16 v[148:151], v[216:219], a[168:171], v[148:151]\n"
"ds_read_b64_tr_b16 v[122:123], v231 offset:26624\n"
"v_add_f32 v49, v49, v244\n"
"v_mfma_f32_16x16x32_bf16 v[148:151], v[212:215], a[164:167], v[148:151]\n"
"ds_read_b64_tr_b16 v[226:227], v235 offset:26624\n"
"v_lshlrev_b32 v207, 16, v245\n"
"v_mfma_f32_16x16x32_bf16 v[148:151], v[208:211], a[160:163], v[148:151]\n"
"buffer_load_dwordx4 v[80:83], v243, s[32:35], s18 offen offset:2048 sc1\n"
"v_mfma_f32_16x16x32_bf16 v[148:151], v[192:195], a[124:127], v[148:151]\n"
"v_and_b32 v245, s73, v245\n"
"v_add_f32 v51, v51, v245\n"
"v_mfma_f32_16x16x32_bf16 v[148:151], v[188:191], a[120:123], v[148:151]\n"
"s_add_u32 s41, s70, 163840\n"
"v_lshlrev_b32 v202, 16, v246\n"
"v_and_b32 v246, s73, v246\n"
"v_mfma_f32_16x16x32_bf16 v[148:151], v[184:187], a[116:119], v[148:151]\n"
"s_add_u32 m0, s71, 16384\n"
"v_add_f32 v53, v53, v246\n"
"v_lshlrev_b32 v203, 16, v247\n"
"v_mfma_f32_16x16x32_bf16 v[148:151], v[180:183], a[112:115], v[148:151]\n"
"buffer_load_dwordx4 v240, s[28:31], s41 offen lds\n"
"v_mfma_f32_16x16x32_bf16 v[164:167], v[180:183], a[128:131], v[196:199]\n"
"v_and_b32 v247, s73, v247\n"
"v_add_f32 v55, v55, v247\n"
"v_mfma_f32_16x16x32_bf16 v[164:167], v[184:187], a[132:135], v[164:167]\n"
"v_add_f32 v48, v48, v206\n"
"v_add_f32 v50, v50, v207\n"
"v_mfma_f32_16x16x32_bf16 v[164:167], v[188:191], a[136:139], v[164:167]\n"
"v_add_f32 v52, v52, v202\n"
"v_add_f32 v54, v54, v203\n"
"v_mfma_f32_16x16x32_bf16 v[164:167], v[192:195], a[140:143], v[164:167]\n"
"v_mfma_f32_16x16x32_bf16 v[164:167], v[208:211], a[176:179], v[164:167]\n"
"v_mfma_f32_16x16x32_bf16 v[164:167], v[212:215], a[180:183], v[164:167]\n"
"v_mfma_f32_16x16x32_bf16 v[164:167], v[216:219], a[184:187], v[164:167]\n"
"s_waitcnt vmcnt(12)\n"
"v_mfma_f32_16x16x32_bf16 v[164:167], v[220:223], a[188:191], v[164:167]\n"
"s_barrier\n"
"s_waitcnt lgkmcnt(0)\n"
"v_mfma_f32_16x16x32_bf16 v[136:139], v[96:99], a[96:99], v[196:199]\n"
"ds_read_b64_tr_b16 v[180:181], v228 offset:32768\n"
"v_mfma_f32_16x16x32_bf16 v[136:139], v[100:103], a[100:103], v[136:139]\n"
"ds_read_b64_tr_b16 v[182:183], v228 offset:34816\n"
"v_mfma_f32_16x16x32_bf16 v[136:139], v[104:107], a[104:107], v[136:139]\n"
"ds_read_b64_tr_b16 v[184:185], v232 offset:32768\n"
"v_mfma_f32_16x16x32_bf16 v[136:139], v[108:111], a[108:111], v[136:139]\n"
"ds_read_b64_tr_b16 v[186:187], v232 offset:34816\n"
"v_mfma_f32_16x16x32_bf16 v[136:139], v[112:115], a[144:147], v[136:139]\n"
"ds_read_b64_tr_b16 v[188:189], v228 offset:40960\n"
"v_mfma_f32_16x16x32_bf16 v[136:139], v[116:119], a[148:151], v[136:139]\n"
"ds_read_b64_tr_b16 v[190:191], v228 offset:43008\n"
"v_mfma_f32_16x16x32_bf16 v[136:139], v[120:123], a[152:155], v[136:139]\n"
"s_add_u32 s40, s41, s52\n"
"ds_read_b64_tr_b16 v[192:193], v232 offset:40960\n"
"v_mfma_f32_16x16x32_bf16 v[136:139], v[224:227], a[156:159], v[136:139]\n"
"buffer_load_dwordx4 v240, s[28:31], s40 offen offset:1024 lds\n"
"v_mfma_f32_16x16x32_bf16 v[152:155], v[224:227], a[172:175], v[196:199]\n"
"ds_read_b64_tr_b16 v[194:195], v232 offset:43008\n"
"v_mfma_f32_16x16x32_bf16 v[152:155], v[120:123], a[168:171], v[152:155]\n"
"ds_read_b64_tr_b16 v[208:209], v228 offset:49152\n"
"v_mfma_f32_16x16x32_bf16 v[152:155], v[116:119], a[164:167], v[152:155]\n"
"ds_read_b64_tr_b16 v[210:211], v228 offset:51200\n"
"v_mfma_f32_16x16x32_bf16 v[152:155], v[112:115], a[160:163], v[152:155]\n"
"ds_read_b64_tr_b16 v[212:213], v232 offset:49152\n"
"v_mfma_f32_16x16x32_bf16 v[152:155], v[108:111], a[124:127], v[152:155]\n"
"ds_read_b64_tr_b16 v[214:215], v232 offset:51200\n"
"v_mfma_f32_16x16x32_bf16 v[152:155], v[104:107], a[120:123], v[152:155]\n"
"ds_read_b64_tr_b16 v[216:217], v228 offset:57344\n"
"v_mfma_f32_16x16x32_bf16 v[152:155], v[100:103], a[116:119], v[152:155]\n"
"s_add_u32 s40, s41, s53\n"
"ds_read_b64_tr_b16 v[218:219], v228 offset:59392\n"
"v_xor_b32 v228, s75, v228\n"
"v_mfma_f32_16x16x32_bf16 v[152:155], v[96:99], a[112:115], v[152:155]\n"
"buffer_load_dwordx4 v240, s[28:31], s40 offen offset:2048 lds\n"
"v_mfma_f32_16x16x32_bf16 v[168:171], v[96:99], a[128:131], v[196:199]\n"
"ds_read_b64_tr_b16 v[220:221], v232 offset:57344\n"
"v_mfma_f32_16x16x32_bf16 v[168:171], v[100:103], a[132:135], v[168:171]\n"
"ds_read_b64_tr_b16 v[222:223], v232 offset:59392\n"
"v_xor_b32 v232, s75, v232\n"
"v_mfma_f32_16x16x32_bf16 v[168:171], v[104:107], a[136:139], v[168:171]\n"
"ds_read_b64_tr_b16 v[96:97], v229 offset:32768\n"
"v_mfma_f32_16x16x32_bf16 v[168:171], v[108:111], a[140:143], v[168:171]\n"
"ds_read_b64_tr_b16 v[98:99], v229 offset:34816\n"
"v_mfma_f32_16x16x32_bf16 v[168:171], v[112:115], a[176:179], v[168:171]\n"
"ds_read_b64_tr_b16 v[100:101], v233 offset:32768\n"
"v_mfma_f32_16x16x32_bf16 v[168:171], v[116:119], a[180:183], v[168:171]\n"
"ds_read_b64_tr_b16 v[102:103], v233 offset:34816\n"
"v_mfma_f32_16x16x32_bf16 v[168:171], v[120:123], a[184:187], v[168:171]\n"
"s_add_u32 s40, s41, s54\n"
"ds_read_b64_tr_b16 v[104:105], v229 offset:40960\n"
"v_mfma_f32_16x16x32_bf16 v[168:171], v[224:227], a[188:191], v[168:171]\n"
"s_waitcnt lgkmcnt(7)\n"
"buffer_load_dwordx4 v240, s[28:31], s40 offen offset:3072 lds\n"
"v_mfma_f32_16x16x32_bf16 v[124:127], v[180:183], a[48:51], v[124:127]\n"
"ds_read_b64_tr_b16 v[106:107], v229 offset:43008\n"
"v_mfma_f32_16x16x32_bf16 v[124:127], v[184:187], a[52:55], v[124:127]\n"
"ds_read_b64_tr_b16 v[108:109], v233 offset:40960\n"
"v_mfma_f32_16x16x32_bf16 v[124:127], v[188:191], a[56:59], v[124:127]\n"
"ds_read_b64_tr_b16 v[110:111], v233 offset:43008\n"
"v_mfma_f32_16x16x32_bf16 v[124:127], v[192:195], a[60:63], v[124:127]\n"
"ds_read_b64_tr_b16 v[112:113], v229 offset:49152\n"
"v_mfma_f32_16x16x32_bf16 v[124:127], v[208:211], a[0:3], v[124:127]\n"
"ds_read_b64_tr_b16 v[114:115], v229 offset:51200\n"
"v_mfma_f32_16x16x32_bf16 v[124:127], v[212:215], a[4:7], v[124:127]\n"
"s_add_u32 s41, s70, 327680\n"
"ds_read_b64_tr_b16 v[116:117], v233 offset:49152\n"
"v_mfma_f32_16x16x32_bf16 v[124:127], v[216:219], a[8:11], v[124:127]\n"
"s_waitcnt lgkmcnt(6)\n"
"s_add_u32 m0, s71, 32768\n"
"ds_read_b64_tr_b16 v[118:119], v233 offset:51200\n"
"v_mfma_f32_16x16x32_bf16 v[124:127], v[220:223], a[12:15], v[124:127]\n"
"buffer_load_dwordx4 v240, s[28:31], s41 offen lds\n"
"v_mfma_f32_16x16x32_bf16 v[140:143], v[220:223], a[28:31], v[140:143]\n"
"ds_read_b64_tr_b16 v[120:121], v229 offset:57344\n"
"v_mfma_f32_16x16x32_bf16 v[140:143], v[216:219], a[24:27], v[140:143]\n"
"ds_read_b64_tr_b16 v[122:123], v229 offset:59392\n"
"v_xor_b32 v229, s75, v229\n"
"v_mfma_f32_16x16x32_bf16 v[140:143], v[212:215], a[20:23], v[140:143]\n"
"ds_read_b64_tr_b16 v[224:225], v233 offset:57344\n"
"v_mfma_f32_16x16x32_bf16 v[140:143], v[208:211], a[16:19], v[140:143]\n"
"ds_read_b64_tr_b16 v[226:227], v233 offset:59392\n"
"v_xor_b32 v233, s75, v233\n"
"v_mfma_f32_16x16x32_bf16 v[140:143], v[192:195], a[76:79], v[140:143]\n"
"v_mfma_f32_16x16x32_bf16 v[140:143], v[188:191], a[72:75], v[140:143]\n"
"v_subrev_f32 v124, s22, v124\n"
"v_subrev_f32 v125, s22, v125\n"
"v_mfma_f32_16x16x32_bf16 v[140:143], v[184:187], a[68:71], v[140:143]\n"
"s_add_u32 s40, s41, s52\n"
"v_subrev_f32 v126, s22, v126\n"
"v_subrev_f32 v127, s22, v127\n"
"v_mfma_f32_16x16x32_bf16 v[140:143], v[180:183], a[64:67], v[140:143]\n"
"buffer_load_dwordx4 v240, s[28:31], s40 offen offset:1024 lds\n"
"v_mfma_f32_16x16x32_bf16 v[156:159], v[180:183], a[80:83], v[156:159]\n"
"v_cvt_pk_bf16_f32 v124, v124, v125\n"
"v_cvt_pk_bf16_f32 v125, v126, v127\n"
"v_mfma_f32_16x16x32_bf16 v[156:159], v[184:187], a[84:87], v[156:159]\n"
"v_mfma_f32_16x16x32_bf16 v[156:159], v[188:191], a[88:91], v[156:159]\n"
"v_mfma_f32_16x16x32_bf16 v[156:159], v[192:195], a[92:95], v[156:159]\n"
"v_mfma_f32_16x16x32_bf16 v[156:159], v[208:211], a[32:35], v[156:159]\n"
"v_mfma_f32_16x16x32_bf16 v[156:159], v[212:215], a[36:39], v[156:159]\n"
"v_subrev_f32 v140, s22, v140\n"
"v_subrev_f32 v141, s22, v141\n"
"v_mfma_f32_16x16x32_bf16 v[156:159], v[216:219], a[40:43], v[156:159]\n"
"s_waitcnt vmcnt(7)\n"
"v_subrev_f32 v142, s22, v142\n"
"v_subrev_f32 v143, s22, v143\n"
"v_mfma_f32_16x16x32_bf16 v[156:159], v[220:223], a[44:47], v[156:159]\n"
"s_barrier\n"
"buffer_store_dword v241, v248, s[84:87], 0 offen sc1\n"
"v_mfma_f32_16x16x32_bf16 v[172:175], v[220:223], a[228:231], v[172:175]\n"
"ds_read_b64_tr_b16 v[220:221], v234 offset:57344\n"
"v_cvt_pk_bf16_f32 v140, v140, v141\n"
"v_mfma_f32_16x16x32_bf16 v[172:175], v[216:219], a[224:227], v[172:175]\n"
"ds_read_b64_tr_b16 v[216:217], v230 offset:57344\n"
"v_cvt_pk_bf16_f32 v141, v142, v143\n"
"v_mfma_f32_16x16x32_bf16 v[172:175], v[212:215], a[212:215], v[172:175]\n"
"ds_read_b64_tr_b16 v[212:213], v234 offset:49152\n"
"v_add_u32 v241, 1, v241\n"
"v_mfma_f32_16x16x32_bf16 v[172:175], v[208:211], a[208:211], v[172:175]\n"
"s_add_u32 s16, s90, s62\n"
"buffer_load_dwordx4 v[84:87], v243, s[32:35], s18 offen offset:3072 sc1\n"
"v_mfma_f32_16x16x32_bf16 v[172:175], v[192:195], a[236:239], v[172:175]\n"
"s_add_u32 s17, s16, 4096\n"
"ds_read_b64_tr_b16 v[192:193], v234 offset:40960\n"
"v_mfma_f32_16x16x32_bf16 v[172:175], v[188:191], a[232:235], v[172:175]\n"
"s_lshl_b32 s62, s70, 9\n"
"ds_read_b64_tr_b16 v[188:189], v230 offset:40960\n"
"v_subrev_f32 v156, s22, v156\n"
"v_mfma_f32_16x16x32_bf16 v[172:175], v[184:187], a[220:223], v[172:175]\n"
"s_add_u32 s40, s41, s53\n"
"ds_read_b64_tr_b16 v[184:185], v234 offset:32768\n"
"v_subrev_f32 v157, s22, v157\n"
"v_mfma_f32_16x16x32_bf16 v[172:175], v[180:183], a[216:219], v[172:175]\n"
"buffer_load_dwordx4 v240, s[28:31], s40 offen offset:2048 lds\n"
"v_mfma_f32_16x16x32_bf16 v[128:131], v[96:99], a[48:51], v[128:131]\n"
"ds_read_b64_tr_b16 v[180:181], v230 offset:32768\n"
"v_subrev_f32 v158, s22, v158\n"
"v_mfma_f32_16x16x32_bf16 v[128:131], v[100:103], a[52:55], v[128:131]\n"
"s_waitcnt lgkmcnt(5)\n"
"ds_read_b64_tr_b16 v[182:183], v230 offset:34816\n"
"v_subrev_f32 v159, s22, v159\n"
"v_mfma_f32_16x16x32_bf16 v[128:131], v[104:107], a[56:59], v[128:131]\n"
"ds_read_b64_tr_b16 v[186:187], v234 offset:34816\n"
"v_cvt_pk_bf16_f32 v156, v156, v157\n"
"v_mfma_f32_16x16x32_bf16 v[128:131], v[108:111], a[60:63], v[128:131]\n"
"ds_read_b64_tr_b16 v[190:191], v230 offset:43008\n"
"v_cvt_pk_bf16_f32 v157, v158, v159\n"
"v_mfma_f32_16x16x32_bf16 v[128:131], v[112:115], a[0:3], v[128:131]\n"
"ds_read_b64_tr_b16 v[194:195], v234 offset:43008\n"
"v_lshlrev_b32 v202, 16, v72\n"
"v_mfma_f32_16x16x32_bf16 v[128:131], v[116:119], a[4:7], v[128:131]\n"
"ds_read_b64_tr_b16 v[208:209], v230 offset:49152\n"
"v_and_b32 v72, s73, v72\n"
"v_mfma_f32_16x16x32_bf16 v[128:131], v[120:123], a[8:11], v[128:131]\n"
"s_add_u32 s40, s41, s54\n"
"ds_read_b64_tr_b16 v[210:211], v230 offset:51200\n"
"v_add_f32 v0, v0, v202\n"
"v_mfma_f32_16x16x32_bf16 v[128:131], v[224:227], a[12:15], v[128:131]\n"
"buffer_load_dwordx4 v240, s[28:31], s40 offen offset:3072 lds\n"
"v_mfma_f32_16x16x32_bf16 v[144:147], v[224:227], a[28:31], v[144:147]\n"
"ds_read_b64_tr_b16 v[214:215], v234 offset:51200\n"
"v_add_f32 v1, v1, v72\n"
"v_mfma_f32_16x16x32_bf16 v[144:147], v[120:123], a[24:27], v[144:147]\n"
"ds_read_b64_tr_b16 v[218:219], v230 offset:59392\n"
"v_lshlrev_b32 v203, 16, v73\n"
"v_mfma_f32_16x16x32_bf16 v[144:147], v[116:119], a[20:23], v[144:147]\n"
"ds_read_b64_tr_b16 v[222:223], v234 offset:59392\n"
"v_and_b32 v73, s73, v73\n"
"v_mfma_f32_16x16x32_bf16 v[144:147], v[112:115], a[16:19], v[144:147]\n"
"buffer_load_dwordx4 v[88:91], v243, s[32:35], s19 offen sc1\n"
"v_mfma_f32_16x16x32_bf16 v[144:147], v[108:111], a[76:79], v[144:147]\n"
"v_add_f32 v2, v2, v203\n"
"v_add_f32 v3, v3, v73\n"
"v_mfma_f32_16x16x32_bf16 v[144:147], v[104:107], a[72:75], v[144:147]\n"
"s_add_u32 s41, s70, 491520\n"
"v_subrev_f32 v128, s22, v128\n"
"v_subrev_f32 v129, s22, v129\n"
"v_mfma_f32_16x16x32_bf16 v[144:147], v[100:103], a[68:71], v[144:147]\n"
"s_add_u32 m0, s71, 49152\n"
"v_subrev_f32 v130, s22, v130\n"
"v_subrev_f32 v131, s22, v131\n"
"v_mfma_f32_16x16x32_bf16 v[144:147], v[96:99], a[64:67], v[144:147]\n"
"buffer_load_dwordx4 v240, s[28:31], s41 offen lds\n"
"v_mfma_f32_16x16x32_bf16 v[160:163], v[96:99], a[80:83], v[160:163]\n"
"s_and_b32 s70, s88, 15\n"
"v_cvt_pk_bf16_f32 v126, v128, v129\n"
"v_cvt_pk_bf16_f32 v127, v130, v131\n"
"v_mfma_f32_16x16x32_bf16 v[160:163], v[100:103], a[84:87], v[160:163]\n"
"s_lshr_b32 s88, s88, 4\n"
"v_lshlrev_b32 v204, 16, v74\n"
"v_and_b32 v74, s73, v74\n"
"v_mfma_f32_16x16x32_bf16 v[160:163], v[104:107], a[88:91], v[160:163]\n"
"s_lshl_b32 s70, s70, 7\n"
"v_add_f32 v4, v4, v204\n"
"v_add_f32 v5, v5, v74\n"
"v_mfma_f32_16x16x32_bf16 v[160:163], v[108:111], a[92:95], v[160:163]\n"
"s_xor_b32 s71, s71, s75\n"
"v_lshlrev_b32 v205, 16, v75\n"
"v_and_b32 v75, s73, v75\n"
"v_mfma_f32_16x16x32_bf16 v[160:163], v[112:115], a[32:35], v[160:163]\n"
"v_add_f32 v6, v6, v205\n"
"v_add_f32 v7, v7, v75\n"
"v_mfma_f32_16x16x32_bf16 v[160:163], v[116:119], a[36:39], v[160:163]\n"
"v_subrev_f32 v144, s22, v144\n"
"v_subrev_f32 v145, s22, v145\n"
"v_mfma_f32_16x16x32_bf16 v[160:163], v[120:123], a[40:43], v[160:163]\n"
"v_subrev_f32 v146, s22, v146\n"
"v_subrev_f32 v147, s22, v147\n"
"v_mfma_f32_16x16x32_bf16 v[160:163], v[224:227], a[44:47], v[160:163]\n"
"buffer_store_dwordx4 v[124:127], v243, s[32:35], s16 offen sc1\n"
"v_cvt_pk_bf16_f32 v142, v144, v145\n"
"v_mfma_f32_16x16x32_bf16 v[176:179], v[224:227], a[228:231], v[176:179]\n"
"ds_read_b64_tr_b16 v[224:225], v235 offset:57344\n"
"v_cvt_pk_bf16_f32 v143, v146, v147\n"
"v_mfma_f32_16x16x32_bf16 v[176:179], v[120:123], a[224:227], v[176:179]\n"
"ds_read_b64_tr_b16 v[120:121], v231 offset:57344\n"
"v_lshlrev_b32 v206, 16, v76\n"
"v_mfma_f32_16x16x32_bf16 v[176:179], v[116:119], a[212:215], v[176:179]\n"
"ds_read_b64_tr_b16 v[116:117], v235 offset:49152\n"
"v_and_b32 v76, s73, v76\n"
"v_mfma_f32_16x16x32_bf16 v[176:179], v[112:115], a[208:211], v[176:179]\n"
"buffer_load_dwordx4 v[92:95], v243, s[32:35], s19 offen offset:1024 sc1\n"
"v_mfma_f32_16x16x32_bf16 v[176:179], v[108:111], a[236:239], v[176:179]\n"
"ds_read_b64_tr_b16 v[108:109], v235 offset:40960\n"
"v_add_f32 v8, v8, v206\n"
"v_mfma_f32_16x16x32_bf16 v[176:179], v[104:107], a[232:235], v[176:179]\n"
"ds_read_b64_tr_b16 v[104:105], v231 offset:40960\n"
"v_subrev_f32 v160, s22, v160\n"
"v_mfma_f32_16x16x32_bf16 v[176:179], v[100:103], a[220:223], v[176:179]\n"
"s_add_u32 s40, s41, s52\n"
"ds_read_b64_tr_b16 v[100:101], v235 offset:32768\n"
"v_subrev_f32 v161, s22, v161\n"
"v_mfma_f32_16x16x32_bf16 v[176:179], v[96:99], a[216:219], v[176:179]\n"
"s_waitcnt vmcnt(14) lgkmcnt(6)\n"
"buffer_load_dwordx4 v240, s[28:31], s40 offen offset:1024 lds\n"
"v_mfma_f32_16x16x32_bf16 v[132:135], v[180:183], a[48:51], v[132:135]\n"
"ds_read_b64_tr_b16 v[96:97], v231 offset:32768\n"
"v_subrev_f32 v162, s22, v162\n"
"v_mfma_f32_16x16x32_bf16 v[132:135], v[184:187], a[52:55], v[132:135]\n"
"ds_read_b64_tr_b16 v[98:99], v231 offset:34816\n"
"v_subrev_f32 v163, s22, v163\n"
"v_mfma_f32_16x16x32_bf16 v[132:135], v[188:191], a[56:59], v[132:135]\n"
"ds_read_b64_tr_b16 v[102:103], v235 offset:34816\n"
"v_cvt_pk_bf16_f32 v158, v160, v161\n"
"v_mfma_f32_16x16x32_bf16 v[132:135], v[192:195], a[60:63], v[132:135]\n"
"ds_read_b64_tr_b16 v[106:107], v231 offset:43008\n"
"v_cvt_pk_bf16_f32 v159, v162, v163\n"
"v_mfma_f32_16x16x32_bf16 v[132:135], v[208:211], a[0:3], v[132:135]\n"
"ds_read_b64_tr_b16 v[110:111], v235 offset:43008\n"
"v_add_f32 v9, v9, v76\n"
"v_mfma_f32_16x16x32_bf16 v[132:135], v[212:215], a[4:7], v[132:135]\n"
"ds_read_b64_tr_b16 v[112:113], v231 offset:49152\n"
"v_lshlrev_b32 v207, 16, v77\n"
"v_mfma_f32_16x16x32_bf16 v[132:135], v[216:219], a[8:11], v[132:135]\n"
"ds_read_b64_tr_b16 v[114:115], v231 offset:51200\n"
"v_and_b32 v77, s73, v77\n"
"v_mfma_f32_16x16x32_bf16 v[132:135], v[220:223], a[12:15], v[132:135]\n"
"buffer_store_dwordx4 v[140:143], v243, s[32:35], s16 offen offset:2048 sc1\n"
"v_add_f32 v10, v10, v207\n"
"v_mfma_f32_16x16x32_bf16 v[148:151], v[220:223], a[28:31], v[148:151]\n"
"ds_read_b64_tr_b16 v[118:119], v235 offset:51200\n"
"v_add_f32 v11, v11, v77\n"
"v_mfma_f32_16x16x32_bf16 v[148:151], v[216:219], a[24:27], v[148:151]\n"
"ds_read_b64_tr_b16 v[122:123], v231 offset:59392\n"
"v_lshlrev_b32 v202, 16, v78\n"
"v_mfma_f32_16x16x32_bf16 v[148:151], v[212:215], a[20:23], v[148:151]\n"
"ds_read_b64_tr_b16 v[226:227], v235 offset:59392\n"
"v_and_b32 v78, s73, v78\n"
"v_mfma_f32_16x16x32_bf16 v[148:151], v[208:211], a[16:19], v[148:151]\n"
"v_add_f32 v12, v12, v202\n"
"v_add_f32 v13, v13, v78\n"
"v_mfma_f32_16x16x32_bf16 v[148:151], v[192:195], a[76:79], v[148:151]\n"
"v_lshlrev_b32 v203, 16, v79\n"
"v_and_b32 v79, s73, v79\n"
"v_mfma_f32_16x16x32_bf16 v[148:151], v[188:191], a[72:75], v[148:151]\n"
"v_add_f32 v14, v14, v203\n"
"v_add_f32 v15, v15, v79\n"
"v_mfma_f32_16x16x32_bf16 v[148:151], v[184:187], a[68:71], v[148:151]\n"
"s_add_u32 s40, s41, s53\n"
"v_lshlrev_b32 v204, 16, v80\n"
"v_and_b32 v80, s73, v80\n"
"v_mfma_f32_16x16x32_bf16 v[148:151], v[180:183], a[64:67], v[148:151]\n"
"buffer_load_dwordx4 v240, s[28:31], s40 offen offset:2048 lds\n"
"v_mfma_f32_16x16x32_bf16 v[164:167], v[180:183], a[80:83], v[164:167]\n"
"v_add_f32 v16, v16, v204\n"
"v_add_f32 v17, v17, v80\n"
"v_mfma_f32_16x16x32_bf16 v[164:167], v[184:187], a[84:87], v[164:167]\n"
"v_lshlrev_b32 v205, 16, v81\n"
"v_and_b32 v81, s73, v81\n"
"v_mfma_f32_16x16x32_bf16 v[164:167], v[188:191], a[88:91], v[164:167]\n"
"v_add_f32 v18, v18, v205\n"
"v_add_f32 v19, v19, v81\n"
"v_mfma_f32_16x16x32_bf16 v[164:167], v[192:195], a[92:95], v[164:167]\n"
"buffer_load_dwordx4 v[244:247], v243, s[32:35], s19 offen offset:2048 sc1\n"
"v_mfma_f32_16x16x32_bf16 v[164:167], v[208:211], a[32:35], v[164:167]\n"
"s_lshl4_add_u32 s18, s72, s61\n"
"v_lshlrev_b32 v206, 16, v82\n"
"v_and_b32 v82, s73, v82\n"
"v_mfma_f32_16x16x32_bf16 v[164:167], v[212:215], a[36:39], v[164:167]\n"
"s_lshl_b32 s19, s72, 2\n"
"v_add_f32 v20, v20, v206\n"
"v_add_f32 v21, v21, v82\n"
"v_mfma_f32_16x16x32_bf16 v[164:167], v[216:219], a[40:43], v[164:167]\n"
"s_waitcnt vmcnt(14)\n"
"s_lshr_b32 s19, s89, s19\n"
"v_lshlrev_b32 v207, 16, v83\n"
"v_mfma_f32_16x16x32_bf16 v[164:167], v[220:223], a[44:47], v[164:167]\n"
"s_barrier\n"
"s_and_b32 s19, s19, 1\n"
"s_waitcnt lgkmcnt(0)\n"
"v_mfma_f32_16x16x32_bf16 v[136:139], v[96:99], a[48:51], v[136:139]\n"
"s_lshl3_add_u32 s18, s18, s19\n"
"ds_read_b64_tr_b16 v[180:181], v228 offset:0\n"
"v_and_b32 v83, s73, v83\n"
"v_mfma_f32_16x16x32_bf16 v[136:139], v[100:103], a[52:55], v[136:139]\n"
"s_lshl_b32 s18, s18, 13\n"
"ds_read_b64_tr_b16 v[182:183], v228 offset:2048\n"
"v_add_f32 v22, v22, v207\n"
"v_mfma_f32_16x16x32_bf16 v[136:139], v[104:107], a[56:59], v[136:139]\n"
"s_add_u32 s19, s18, 4096\n"
"v_lshlrev_b32_e64 v201, 2, s72\n"
"ds_read_b64_tr_b16 v[184:185], v232 offset:0\n"
"v_mfma_f32_16x16x32_bf16 v[136:139], v[108:111], a[60:63], v[136:139]\n"
"s_mov_b32 s69, 15\n"
"global_load_dword v200, v201, s[6:7] sc1\n"
"v_mfma_f32_16x16x32_bf16 v[136:139], v[112:115], a[0:3], v[136:139]\n"
"s_add_u32 s21, s21, 1\n"
"ds_read_b64_tr_b16 v[186:187], v232 offset:2048\n"
"v_add_f32 v23, v23, v83\n"
"v_mfma_f32_16x16x32_bf16 v[136:139], v[116:119], a[4:7], v[136:139]\n"
"ds_read_b64_tr_b16 v[188:189], v228 offset:8192\n"
"v_subrev_f32 v172, s22, v172\n"
"v_mfma_f32_16x16x32_bf16 v[136:139], v[120:123], a[8:11], v[136:139]\n"
"ds_read_b64_tr_b16 v[190:191], v228 offset:10240\n"
"v_subrev_f32 v173, s22, v173\n"
"v_mfma_f32_16x16x32_bf16 v[136:139], v[224:227], a[12:15], v[136:139]\n"
"buffer_store_dwordx4 v[156:159], v243, s[32:35], s17 offen sc1\n"
"v_subrev_f32 v174, s22, v174\n"
"v_mfma_f32_16x16x32_bf16 v[152:155], v[224:227], a[28:31], v[152:155]\n"
"ds_read_b64_tr_b16 v[192:193], v232 offset:8192\n"
"v_subrev_f32 v175, s22, v175\n"
"v_mfma_f32_16x16x32_bf16 v[152:155], v[120:123], a[24:27], v[152:155]\n"
"ds_read_b64_tr_b16 v[194:195], v232 offset:10240\n"
"v_cvt_pk_bf16_f32 v172, v172, v173\n"
"v_mfma_f32_16x16x32_bf16 v[152:155], v[116:119], a[20:23], v[152:155]\n"
"ds_read_b64_tr_b16 v[208:209], v228 offset:16384\n"
"v_cvt_pk_bf16_f32 v173, v174, v175\n"
"v_mfma_f32_16x16x32_bf16 v[152:155], v[112:115], a[16:19], v[152:155]\n"
"ds_read_b64_tr_b16 v[210:211], v228 offset:18432\n"
"v_subrev_f32 v176, s22, v176\n"
"v_mfma_f32_16x16x32_bf16 v[152:155], v[108:111], a[76:79], v[152:155]\n"
"ds_read_b64_tr_b16 v[212:213], v232 offset:16384\n"
"v_subrev_f32 v177, s22, v177\n"
"v_mfma_f32_16x16x32_bf16 v[152:155], v[104:107], a[72:75], v[152:155]\n"
"ds_read_b64_tr_b16 v[214:215], v232 offset:18432\n"
"v_subrev_f32 v178, s22, v178\n"
"v_mfma_f32_16x16x32_bf16 v[152:155], v[100:103], a[68:71], v[152:155]\n"
"s_add_u32 s40, s41, s54\n"
"ds_read_b64_tr_b16 v[216:217], v228 offset:24576\n"
"v_subrev_f32 v179, s22, v179\n"
"v_mfma_f32_16x16x32_bf16 v[152:155], v[96:99], a[64:67], v[152:155]\n"
"buffer_load_dwordx4 v240, s[28:31], s40 offen offset:3072 lds\n"
"v_mfma_f32_16x16x32_bf16 v[168:171], v[96:99], a[80:83], v[168:171]\n"
"ds_read_b64_tr_b16 v[218:219], v228 offset:26624\n"
"v_cvt_pk_bf16_f32 v174, v176, v177\n"
"v_mfma_f32_16x16x32_bf16 v[168:171], v[100:103], a[84:87], v[168:171]\n"
"ds_read_b64_tr_b16 v[220:221], v232 offset:24576\n"
"v_cvt_pk_bf16_f32 v175, v178, v179\n"
"v_mfma_f32_16x16x32_bf16 v[168:171], v[104:107], a[88:91], v[168:171]\n"
"ds_read_b64_tr_b16 v[222:223], v232 offset:26624\n"
"v_subrev_f32 v132, s22, v132\n"
"v_mfma_f32_16x16x32_bf16 v[168:171], v[108:111], a[92:95], v[168:171]\n"
"ds_read_b64_tr_b16 v[96:97], v229 offset:0\n"
"v_subrev_f32 v133, s22, v133\n"
"v_mfma_f32_16x16x32_bf16 v[168:171], v[112:115], a[32:35], v[168:171]\n"
"ds_read_b64_tr_b16 v[98:99], v229 offset:2048\n"
"v_subrev_f32 v134, s22, v134\n"
"v_mfma_f32_16x16x32_bf16 v[168:171], v[116:119], a[36:39], v[168:171]\n"
"ds_read_b64_tr_b16 v[100:101], v233 offset:0\n"
"v_subrev_f32 v135, s22, v135\n"
"v_mfma_f32_16x16x32_bf16 v[168:171], v[120:123], a[40:43], v[168:171]\n"
"s_add_u32 s47, s47, 1\n"
"ds_read_b64_tr_b16 v[102:103], v233 offset:2048\n"
"v_cvt_pk_bf16_f32 v132, v132, v133\n"
"v_mfma_f32_16x16x32_bf16 v[168:171], v[224:227], a[44:47], v[168:171]\n"
"s_waitcnt lgkmcnt(7)\n"
"s_cmp_lg_u32 s47, 9\n"
"ds_read_b64_tr_b16 v[104:105], v229 offset:8192\n"
"s_cbranch_scc1 LGL_%=\n"
"v_mfma_f32_16x16x32_bf16 v[0:3], v[180:183], a[96:99], v[0:3]\n"
"v_xor_b32 v235, s75, v235\n"
"v_subrev_f32 v136, s22, v136\n"
"v_mfma_f32_16x16x32_bf16 v[0:3], v[184:187], a[100:103], v[0:3]\n"
"ds_read_b64_tr_b16 v[114:115], v229 offset:18432\n"
"v_cvt_pk_bf16_f32 v133, v134, v135\n"
"v_mfma_f32_16x16x32_bf16 v[0:3], v[188:191], a[104:107], v[0:3]\n"
"v_mov_b32 v201, v237\n"
"v_subrev_f32 v138, s22, v138\n"
"v_subrev_f32 v139, s22, v139\n"
"v_mfma_f32_16x16x32_bf16 v[0:3], v[192:195], a[108:111], v[0:3]\n"
"global_load_dword v200, v201, s[6:7] sc1\n"
"v_mfma_f32_16x16x32_bf16 v[0:3], v[208:211], a[144:147], v[0:3]\n"
"v_subrev_f32 v167, s22, v167\n"
"v_cvt_pk_bf16_f32 v135, v138, v139\n"
"v_mfma_f32_16x16x32_bf16 v[0:3], v[212:215], a[148:151], v[0:3]\n"
"s_waitcnt lgkmcnt(3)\n"
"v_subrev_f32 v148, s22, v148\n"
"v_subrev_f32 v137, s22, v137\n"
"v_mfma_f32_16x16x32_bf16 v[0:3], v[216:219], a[152:155], v[0:3]\n"
"v_subrev_f32 v165, s22, v165\n"
"v_subrev_f32 v168, s22, v168\n"
"v_mfma_f32_16x16x32_bf16 v[0:3], v[220:223], a[156:159], v[0:3]\n"
"buffer_store_dwordx4 v[172:175], v243, s[32:35], s17 offen offset:2048 sc1\n"
"v_subrev_f32 v151, s22, v151\n"
"v_cvt_pk_bf16_f32 v134, v136, v137\n"
"v_mfma_f32_16x16x32_bf16 v[16:19], v[220:223], a[172:175], v[16:19]\n"
"buffer_store_dwordx4 v[132:135], v243, s[32:35], s16 offen offset:1024 sc1\n"
"v_subrev_f32 v149, s22, v149\n"
"v_subrev_f32 v150, s22, v150\n"
"v_subrev_f32 v164, s22, v164\n"
"v_cvt_pk_bf16_f32 v164, v164, v165\n"
"v_subrev_f32 v153, s22, v153\n"
"v_subrev_f32 v155, s22, v155\n"
"v_mfma_f32_16x16x32_bf16 v[16:19], v[216:219], a[168:171], v[16:19]\n"
"v_cvt_pk_bf16_f32 v148, v148, v149\n"
"v_subrev_f32 v154, s22, v154\n"
"v_mfma_f32_16x16x32_bf16 v[16:19], v[212:215], a[164:167], v[16:19]\n"
"v_cvt_pk_bf16_f32 v149, v150, v151\n"
"v_subrev_f32 v170, s22, v170\n"
"v_mfma_f32_16x16x32_bf16 v[16:19], v[208:211], a[160:163], v[16:19]\n"
"v_subrev_f32 v166, s22, v166\n"
"v_subrev_f32 v171, s22, v171\n"
"v_mfma_f32_16x16x32_bf16 v[16:19], v[192:195], a[124:127], v[16:19]\n"
"v_cvt_pk_bf16_f32 v165, v166, v167\n"
"v_cvt_pk_bf16_f32 v167, v170, v171\n"
"v_mfma_f32_16x16x32_bf16 v[16:19], v[188:191], a[120:123], v[16:19]\n"
"v_subrev_f32 v152, s22, v152\n"
"v_cvt_pk_bf16_f32 v151, v154, v155\n"
"v_mfma_f32_16x16x32_bf16 v[16:19], v[184:187], a[116:119], v[16:19]\n"
"v_cvt_pk_bf16_f32 v150, v152, v153\n"
"v_subrev_f32 v169, s22, v169\n"
"v_mfma_f32_16x16x32_bf16 v[16:19], v[180:183], a[112:115], v[16:19]\n"
"s_waitcnt vmcnt(16)\n"
"buffer_store_dwordx4 v[148:151], v243, s[32:35], s16 offen offset:3072 sc1\n"
"v_cvt_pk_bf16_f32 v166, v168, v169\n"
"v_mfma_f32_16x16x32_bf16 v[32:35], v[180:183], a[128:131], v[32:35]\n"
"buffer_store_dwordx4 v[164:167], v243, s[32:35], s17 offen offset:1024 sc1\n"
"ds_read_b64_tr_b16 v[120:121], v229 offset:24576\n"
"v_mfma_f32_16x16x32_bf16 v[32:35], v[184:187], a[132:135], v[32:35]\n"
"ds_read_b64_tr_b16 v[224:225], v233 offset:24576\n"
"v_xor_b32 v234, s75, v234\n"
"v_mfma_f32_16x16x32_bf16 v[32:35], v[188:191], a[136:139], v[32:35]\n"
"ds_read_b64_tr_b16 v[122:123], v229 offset:26624\n"
"v_lshlrev_b32 v203, 16, v85\n"
"v_mfma_f32_16x16x32_bf16 v[32:35], v[192:195], a[140:143], v[32:35]\n"
"ds_read_b64_tr_b16 v[108:109], v233 offset:8192\n"
"v_lshlrev_b32 v202, 16, v84\n"
"v_mfma_f32_16x16x32_bf16 v[32:35], v[208:211], a[176:179], v[32:35]\n"
"ds_read_b64_tr_b16 v[226:227], v233 offset:26624\n"
"v_and_b32 v84, s73, v84\n"
"v_mfma_f32_16x16x32_bf16 v[32:35], v[212:215], a[180:183], v[32:35]\n"
"ds_read_b64_tr_b16 v[110:111], v233 offset:10240\n"
"v_bfe_u32 v161, v242, 8, 2\n"
"v_mfma_f32_16x16x32_bf16 v[32:35], v[216:219], a[184:187], v[32:35]\n"
"ds_read_b64_tr_b16 v[106:107], v229 offset:10240\n"
"v_lshlrev_b32 v205, 16, v87\n"
"v_mfma_f32_16x16x32_bf16 v[32:35], v[220:223], a[188:191], v[32:35]\n"
"v_xor_b32 v230, s75, v230\n"
"v_lshlrev_b32 v204, 16, v86\n"
"v_mfma_f32_16x16x32_bf16 v[48:51], v[220:223], a[252:255], v[48:51]\n"
"ds_read_b64_tr_b16 v[220:221], v234 offset:24576\n"
"v_add_f32 v26, v26, v203\n"
"v_mfma_f32_16x16x32_bf16 v[48:51], v[216:219], a[248:251], v[48:51]\n"
"ds_read_b64_tr_b16 v[112:113], v229 offset:16384\n"
"v_bfe_u32 v160, v242, 6, 2\n"
"v_mfma_f32_16x16x32_bf16 v[48:51], v[212:215], a[244:247], v[48:51]\n"
"ds_read_b64_tr_b16 v[116:117], v233 offset:16384\n"
"v_add_f32 v24, v24, v202\n"
"v_mfma_f32_16x16x32_bf16 v[48:51], v[208:211], a[240:243], v[48:51]\n"
"ds_read_b64_tr_b16 v[118:119], v233 offset:18432\n"
"v_add_f32 v25, v25, v84\n"
"v_mfma_f32_16x16x32_bf16 v[48:51], v[192:195], a[204:207], v[48:51]\n"
"ds_read_b64_tr_b16 v[210:211], v230 offset:18432\n"
"v_add_f32 v28, v28, v204\n"
"v_mfma_f32_16x16x32_bf16 v[48:51], v[188:191], a[200:203], v[48:51]\n"
"ds_read_b64_tr_b16 v[188:189], v230 offset:8192\n"
"v_and_b32 v86, s73, v86\n"
"v_mfma_f32_16x16x32_bf16 v[48:51], v[184:187], a[196:199], v[48:51]\n"
"ds_read_b64_tr_b16 v[194:195], v234 offset:10240\n"
"v_mov_b32 v164, 0\n"
"v_mfma_f32_16x16x32_bf16 v[48:51], v[180:183], a[192:195], v[48:51]\n"
"s_waitcnt vmcnt(15)\n"
"v_xor_b32 v160, v160, v161\n"
"v_lshlrev_b32 v202, 16, v90\n"
"v_mfma_f32_16x16x32_bf16 v[4:7], v[96:99], a[96:99], v[4:7]\n"
"s_waitcnt lgkmcnt(6)\n"
"ds_read_b64_tr_b16 v[222:223], v234 offset:26624\n"
"v_lshlrev_b32 v203, 16, v91\n"
"v_mfma_f32_16x16x32_bf16 v[4:7], v[100:103], a[100:103], v[4:7]\n"
"ds_read_b64_tr_b16 v[184:185], v234 offset:0\n"
"v_lshlrev_b32 v206, 16, v88\n"
"v_mfma_f32_16x16x32_bf16 v[4:7], v[104:107], a[104:107], v[4:7]\n"
"ds_read_b64_tr_b16 v[216:217], v230 offset:24576\n"
"v_mfma_f32_16x16x32_bf16 v[4:7], v[108:111], a[108:111], v[4:7]\n"
"s_waitcnt lgkmcnt(6)\n"
"ds_read_b64_tr_b16 v[218:219], v230 offset:26624\n"
"v_and_b32 v91, s73, v91\n"
"v_mfma_f32_16x16x32_bf16 v[4:7], v[112:115], a[144:147], v[4:7]\n"
"s_waitcnt vmcnt(4)\n"
"ds_read_b64_tr_b16 v[190:191], v230 offset:10240\n"
"v_lshlrev_b32 v207, 16, v89\n"
"v_mfma_f32_16x16x32_bf16 v[4:7], v[116:119], a[148:151], v[4:7]\n"
"v_subrev_u32 v200, s21, v200\n"
"ds_read_b64_tr_b16 v[208:209], v230 offset:16384\n"
"v_mfma_f32_16x16x32_bf16 v[4:7], v[120:123], a[152:155], v[4:7]\n"
"v_cmp_lt_u32 vcc, s69, v200\n"
"ds_read_b64_tr_b16 v[182:183], v230 offset:2048\n"
"v_mfma_f32_16x16x32_bf16 v[4:7], v[224:227], a[156:159], v[4:7]\n"
"ds_read_b64_tr_b16 v[180:181], v230 offset:0\n"
"s_cbranch_vccnz LGQ_%=\nLGQK_%=:\n"
"v_mfma_f32_16x16x32_bf16 v[20:23], v[224:227], a[172:175], v[20:23]\n"
"s_bfe_u32 s20, s89, 65536\ns_lshl_b32 s20, s20, 13\ns_add_u32 s20, s20, 0\n"
"ds_read_b64_tr_b16 v[214:215], v234 offset:18432\n"
"v_and_b32 v85, s73, v85\n"
"v_mfma_f32_16x16x32_bf16 v[20:23], v[120:123], a[168:171], v[20:23]\n"
"buffer_load_dwordx4 v[124:127], v236, s[32:35], s20 offen sc1\n"
"v_mfma_f32_16x16x32_bf16 v[20:23], v[116:119], a[164:167], v[20:23]\n"
"v_and_b32 v90, s73, v90\n"
"v_add_f32 v37, v37, v90\n"
"v_mfma_f32_16x16x32_bf16 v[20:23], v[112:115], a[160:163], v[20:23]\n"
"s_bfe_u32 s20, s89, 65540\ns_lshl_b32 s20, s20, 13\ns_add_u32 s20, s20, 1048576\n"
"v_add_f32 v36, v36, v202\n"
"v_add_f32 v38, v38, v203\n"
"v_mfma_f32_16x16x32_bf16 v[20:23], v[108:111], a[124:127], v[20:23]\n"
"buffer_load_dwordx4 v[128:131], v236, s[32:35], s20 offen sc1\n"
"v_mfma_f32_16x16x32_bf16 v[20:23], v[104:107], a[120:123], v[20:23]\n"
"v_add_u32 v160, -1, v160\n"
"v_add_f32 v39, v39, v91\n"
"v_mfma_f32_16x16x32_bf16 v[20:23], v[100:103], a[116:119], v[20:23]\n"
"s_bfe_u32 s20, s89, 65544\ns_lshl_b32 s20, s20, 13\ns_add_u32 s20, s20, 2097152\n"
"v_add_f32 v32, v32, v206\n"
"v_mfma_f32_16x16x32_bf16 v[20:23], v[96:99], a[112:115], v[20:23]\n"
"buffer_load_dwordx4 v[132:135], v236, s[32:35], s20 offen sc1\n"
"v_mfma_f32_16x16x32_bf16 v[36:39], v[96:99], a[128:131], v[36:39]\n"
"v_lshlrev_b32 v204, 16, v92\n"
"v_add_f32 v40, v40, v204\n"
"v_mfma_f32_16x16x32_bf16 v[36:39], v[100:103], a[132:135], v[36:39]\n"
"s_bfe_u32 s20, s89, 65548\ns_lshl_b32 s20, s20, 13\ns_add_u32 s20, s20, 3145728\n"
"ds_read_b64_tr_b16 v[212:213], v234 offset:16384\n"
"v_add_f32 v30, v30, v205\n"
"v_mfma_f32_16x16x32_bf16 v[36:39], v[104:107], a[136:139], v[36:39]\n"
"buffer_load_dwordx4 v[136:139], v236, s[32:35], s20 offen sc1\n"
"v_mfma_f32_16x16x32_bf16 v[36:39], v[108:111], a[140:143], v[36:39]\n"
"v_and_b32 v92, s73, v92\n"
"v_lshlrev_b32 v206, 16, v94\n"
"v_mfma_f32_16x16x32_bf16 v[36:39], v[112:115], a[176:179], v[36:39]\n"
"s_bfe_u32 s20, s89, 65552\ns_lshl_b32 s20, s20, 13\ns_add_u32 s20, s20, 4194304\n"
"ds_read_b64_tr_b16 v[186:187], v234 offset:2048\n"
"ds_read_b64_tr_b16 v[192:193], v234 offset:8192\n"
"v_mfma_f32_16x16x32_bf16 v[36:39], v[116:119], a[180:183], v[36:39]\n"
"buffer_load_dwordx4 v[140:143], v236, s[32:35], s20 offen sc1\n"
"v_mfma_f32_16x16x32_bf16 v[36:39], v[120:123], a[184:187], v[36:39]\n"
"v_and_b32 v94, s73, v94\n"
"v_mfma_f32_16x16x32_bf16 v[36:39], v[224:227], a[188:191], v[36:39]\n"
"s_bfe_u32 s20, s89, 65556\ns_lshl_b32 s20, s20, 13\ns_add_u32 s20, s20, 5242880\n"
"v_add_f32 v34, v34, v207\n"
"v_lshlrev_b32 v205, 16, v93\n"
"v_mfma_f32_16x16x32_bf16 v[52:55], v[224:227], a[252:255], v[52:55]\n"
"buffer_load_dwordx4 v[144:147], v236, s[32:35], s20 offen sc1\n"
"v_mfma_f32_16x16x32_bf16 v[52:55], v[120:123], a[248:251], v[52:55]\n"
"v_ashrrev_i32 v160, 31, v160\n"
"v_add_f32 v44, v44, v206\n"
"v_mfma_f32_16x16x32_bf16 v[52:55], v[116:119], a[244:247], v[52:55]\n"
"s_bfe_u32 s20, s89, 65560\ns_lshl_b32 s20, s20, 13\ns_add_u32 s20, s20, 6291456\n"
"v_xor_b32 v231, s75, v231\n"
"v_lshlrev_b32 v207, 16, v95\n"
"v_mfma_f32_16x16x32_bf16 v[52:55], v[112:115], a[240:243], v[52:55]\n"
"buffer_load_dwordx4 v[148:151], v236, s[32:35], s20 offen sc1\n"
"v_mfma_f32_16x16x32_bf16 v[52:55], v[108:111], a[204:207], v[52:55]\n"
"s_bfe_u32 s20, s89, 65564\ns_lshl_b32 s20, s20, 13\ns_add_u32 s20, s20, 7340032\n"
"v_and_b32 v87, s73, v87\n"
"v_and_b32 v95, s73, v95\n"
"v_mfma_f32_16x16x32_bf16 v[52:55], v[104:107], a[200:203], v[52:55]\n"
"buffer_load_dwordx4 v[152:155], v236, s[32:35], s20 offen sc1\n"
"v_mfma_f32_16x16x32_bf16 v[52:55], v[100:103], a[196:199], v[52:55]\n"
"s_waitcnt vmcnt(8)\n"
"s_add_u32 s21, s21, 1\n"
"v_and_b32 v88, s73, v88\n"
"v_mfma_f32_16x16x32_bf16 v[52:55], v[96:99], a[192:195], v[52:55]\n"
"s_barrier\n"
"s_mov_b32 s52, 40960\n"
"buffer_store_dword v241, v248, s[84:87], 0 offen sc1\n"
"s_waitcnt lgkmcnt(0)\n"
"v_mfma_f32_16x16x32_bf16 v[8:11], v[180:183], a[96:99], v[8:11]\n"
"s_mov_b32 s53, 81920\n"
"ds_read_b64_tr_b16 v[98:99], v231 offset:2048\n"
"v_add_f32 v42, v42, v205\n"
"v_mfma_f32_16x16x32_bf16 v[8:11], v[184:187], a[100:103], v[8:11]\n"
"ds_read_b64_tr_b16 v[112:113], v231 offset:16384\n"
"v_add_f32 v31, v31, v87\n"
"v_mfma_f32_16x16x32_bf16 v[8:11], v[188:191], a[104:107], v[8:11]\n"
"v_add_f32 v29, v29, v86\n"
"v_and_b32 v161, 16, v242\n"
"v_mfma_f32_16x16x32_bf16 v[8:11], v[192:195], a[108:111], v[8:11]\n"
"ds_read_b64_tr_b16 v[118:119], v235 offset:18432\n"
"v_and_b32 v160, 0x3f80, v160\n"
"v_mfma_f32_16x16x32_bf16 v[8:11], v[208:211], a[144:147], v[8:11]\n"
"ds_read_b64_tr_b16 v[102:103], v235 offset:2048\n"
"v_add_f32 v27, v27, v85\n"
"v_mfma_f32_16x16x32_bf16 v[8:11], v[212:215], a[148:151], v[8:11]\n"
"ds_read_b64_tr_b16 v[106:107], v231 offset:10240\n"
"v_lshlrev_b32 v160, v161, v160\n"
"v_mfma_f32_16x16x32_bf16 v[8:11], v[216:219], a[152:155], v[8:11]\n"
"ds_read_b64_tr_b16 v[96:97], v231 offset:0\n"
"v_and_b32 v93, s73, v93\n"
"v_mfma_f32_16x16x32_bf16 v[8:11], v[220:223], a[156:159], v[8:11]\n"
"ds_read_b64_tr_b16 v[108:109], v235 offset:8192\n"
"v_add_f32 v41, v41, v92\n"
"v_mfma_f32_16x16x32_bf16 v[24:27], v[220:223], a[172:175], v[24:27]\n"
"v_bfe_i32 v161, v242, 5, 1\n"
"v_lshlrev_b32 v206, 16, v244\n"
"v_mfma_f32_16x16x32_bf16 v[24:27], v[216:219], a[168:171], v[24:27]\n"
"ds_read_b64_tr_b16 v[114:115], v231 offset:18432\n"
"v_mfma_f32_16x16x32_bf16 v[24:27], v[212:215], a[164:167], v[24:27]\n"
"ds_read_b64_tr_b16 v[104:105], v231 offset:8192\n"
"v_lshlrev_b32 v203, 16, v247\n"
"v_mfma_f32_16x16x32_bf16 v[24:27], v[208:211], a[160:163], v[24:27]\n"
"ds_read_b64_tr_b16 v[116:117], v235 offset:16384\n"
"v_add_f32 v48, v48, v206\n"
"v_mfma_f32_16x16x32_bf16 v[24:27], v[192:195], a[124:127], v[24:27]\n"
"ds_read_b64_tr_b16 v[110:111], v235 offset:10240\n"
"v_and_b32 v89, s73, v89\n"
"v_mfma_f32_16x16x32_bf16 v[24:27], v[188:191], a[120:123], v[24:27]\n"
"v_add_f32 v45, v45, v94\n"
"v_add_f32 v47, v47, v95\n"
"v_mfma_f32_16x16x32_bf16 v[24:27], v[184:187], a[116:119], v[24:27]\n"
"ds_read_b64_tr_b16 v[100:101], v235 offset:0\n"
"v_add_f32 v43, v43, v93\n"
"v_mfma_f32_16x16x32_bf16 v[24:27], v[180:183], a[112:115], v[24:27]\n"
"ds_read_b64_tr_b16 v[122:123], v231 offset:26624\n"
"v_add_f32 v46, v46, v207\n"
"v_mfma_f32_16x16x32_bf16 v[40:43], v[180:183], a[128:131], v[40:43]\n"
"v_add_f32 v33, v33, v88\n"
"v_and_b32 v244, s73, v244\n"
"v_mfma_f32_16x16x32_bf16 v[40:43], v[184:187], a[132:135], v[40:43]\n"
"ds_read_b64_tr_b16 v[120:121], v231 offset:24576\n"
"v_lshlrev_b32 v207, 16, v245\n"
"v_mfma_f32_16x16x32_bf16 v[40:43], v[188:191], a[136:139], v[40:43]\n"
"v_and_b32 v163, v160, v161\n"
"v_lshlrev_b32 v202, 16, v246\n"
"v_mfma_f32_16x16x32_bf16 v[40:43], v[192:195], a[140:143], v[40:43]\n"
"ds_read_b64_tr_b16 v[224:225], v235 offset:24576\n"
"v_and_b32 v246, s73, v246\n"
"v_mfma_f32_16x16x32_bf16 v[40:43], v[208:211], a[176:179], v[40:43]\n"
"v_add_f32 v52, v52, v202\n"
"v_and_b32 v247, s73, v247\n"
"v_mfma_f32_16x16x32_bf16 v[40:43], v[212:215], a[180:183], v[40:43]\n"
"ds_read_b64_tr_b16 v[226:227], v235 offset:26624\n"
"v_and_b32 v245, s73, v245\n"
"v_mfma_f32_16x16x32_bf16 v[40:43], v[216:219], a[184:187], v[40:43]\n"
"v_mov_b32 v161, 0\n"
"v_add_f32 v54, v54, v203\n"
"v_mfma_f32_16x16x32_bf16 v[40:43], v[220:223], a[188:191], v[40:43]\n"
"s_barrier\n"
"s_waitcnt lgkmcnt(3)\n"
"v_mfma_f32_16x16x32_bf16 v[12:15], v[96:99], a[96:99], v[12:15]\n"
"ds_read_b64_tr_b16 v[184:185], v232 offset:32768\n"
"v_add_f32 v49, v49, v244\n"
"v_mfma_f32_16x16x32_bf16 v[12:15], v[100:103], a[100:103], v[12:15]\n"
"ds_read_b64_tr_b16 v[216:217], v228 offset:57344\n"
"v_mfma_f32_16x16x32_bf16 v[12:15], v[104:107], a[104:107], v[12:15]\n"
"ds_read_b64_tr_b16 v[188:189], v228 offset:40960\n"
"v_add_f32 v35, v35, v89\n"
"v_mfma_f32_16x16x32_bf16 v[12:15], v[108:111], a[108:111], v[12:15]\n"
"ds_read_b64_tr_b16 v[190:191], v228 offset:43008\n"
"v_mfma_f32_16x16x32_bf16 v[12:15], v[112:115], a[144:147], v[12:15]\n"
"ds_read_b64_tr_b16 v[192:193], v232 offset:40960\n"
"v_add_f32 v53, v53, v246\n"
"v_mfma_f32_16x16x32_bf16 v[12:15], v[116:119], a[148:151], v[12:15]\n"
"s_waitcnt lgkmcnt(5)\n"
"ds_read_b64_tr_b16 v[212:213], v232 offset:49152\n"
"v_add_f32 v51, v51, v245\n"
"v_mfma_f32_16x16x32_bf16 v[12:15], v[120:123], a[152:155], v[12:15]\n"
"ds_read_b64_tr_b16 v[182:183], v228 offset:34816\n"
"v_xor_b32 v162, v160, v163\n"
"v_mfma_f32_16x16x32_bf16 v[12:15], v[224:227], a[156:159], v[12:15]\n"
"global_load_dword v200, v201, s[6:7] sc1\n"
"v_mfma_f32_16x16x32_bf16 v[28:31], v[224:227], a[172:175], v[28:31]\n"
"v_add_f32 v50, v50, v207\n"
"v_add_f32 v55, v55, v247\n"
"v_mfma_f32_16x16x32_bf16 v[28:31], v[120:123], a[168:171], v[28:31]\n"
"s_waitcnt vmcnt(9)\n"
"v_lshlrev_b32 v58, 16, v125\n"
"v_lshlrev_b32 v62, 16, v127\n"
"v_mfma_f32_16x16x32_bf16 v[28:31], v[116:119], a[164:167], v[28:31]\n"
"ds_read_b64_tr_b16 v[186:187], v232 offset:34816\n"
"ds_read_b64_tr_b16 v[214:215], v232 offset:51200\n"
"v_mfma_f32_16x16x32_bf16 v[28:31], v[112:115], a[160:163], v[28:31]\n"
"ds_read_b64_tr_b16 v[194:195], v232 offset:43008\n"
"v_and_b32 v61, s73, v126\n"
"v_mfma_f32_16x16x32_bf16 v[28:31], v[108:111], a[124:127], v[28:31]\n"
"s_waitcnt vmcnt(8)\n"
"ds_read_b64_tr_b16 v[210:211], v228 offset:51200\n"
"v_and_b32 v57, s73, v124\n"
"v_mfma_f32_16x16x32_bf16 v[28:31], v[104:107], a[120:123], v[28:31]\n"
"ds_read_b64_tr_b16 v[222:223], v232 offset:59392\n"
"v_lshlrev_b32 v56, 16, v124\n"
"v_mfma_f32_16x16x32_bf16 v[28:31], v[100:103], a[116:119], v[28:31]\n"
"ds_read_b64_tr_b16 v[180:181], v228 offset:32768\n"
"v_lshlrev_b32 v205, 16, v129\n"
"v_mfma_f32_16x16x32_bf16 v[28:31], v[96:99], a[112:115], v[28:31]\n"
"s_waitcnt vmcnt(7)\n"
"v_and_b32 v59, s73, v125\n"
"v_lshlrev_b32 v206, 16, v130\n"
"v_mfma_f32_16x16x32_bf16 v[44:47], v[96:99], a[128:131], v[44:47]\n"
"ds_read_b64_tr_b16 v[220:221], v232 offset:57344\n"
"v_lshlrev_b32 v204, 16, v128\n"
"v_mfma_f32_16x16x32_bf16 v[44:47], v[100:103], a[132:135], v[44:47]\n"
"ds_read_b64_tr_b16 v[208:209], v228 offset:49152\n"
"v_and_b32 v130, s73, v130\n"
"v_mfma_f32_16x16x32_bf16 v[44:47], v[104:107], a[136:139], v[44:47]\n"
"s_waitcnt vmcnt(6)\n"
"ds_read_b64_tr_b16 v[102:103], v233 offset:34816\n"
"v_add_f32 v58, v58, v205\n"
"v_mfma_f32_16x16x32_bf16 v[44:47], v[108:111], a[140:143], v[44:47]\n"
"v_lshlrev_b32 v207, 16, v131\n"
"v_add_f32 v62, v62, v207\n"
"v_mfma_f32_16x16x32_bf16 v[44:47], v[112:115], a[176:179], v[44:47]\n"
"ds_read_b64_tr_b16 v[218:219], v228 offset:59392\n"
"v_and_b32 v131, s73, v131\n"
"v_mfma_f32_16x16x32_bf16 v[44:47], v[116:119], a[180:183], v[44:47]\n"
"s_waitcnt vmcnt(5)\n"
"ds_read_b64_tr_b16 v[96:97], v229 offset:32768\n"
"v_lshlrev_b32 v205, 16, v135\n"
"v_mfma_f32_16x16x32_bf16 v[44:47], v[120:123], a[184:187], v[44:47]\n"
"v_lshlrev_b32 v202, 16, v132\n"
"v_lshlrev_b32 v207, 16, v137\n"
"v_mfma_f32_16x16x32_bf16 v[44:47], v[224:227], a[188:191], v[44:47]\n"
"s_waitcnt lgkmcnt(5)\n"
"ds_read_b64_tr_b16 v[120:121], v229 offset:57344\n"
"v_add_f32 v56, v56, v204\n"
"v_mfma_f32_16x16x32_bf16 v[0:3], v[180:183], a[48:51], v[0:3]\n"
"s_waitcnt vmcnt(4)\n"
"ds_read_b64_tr_b16 v[116:117], v233 offset:49152\n"
"v_add_f32 v61, v61, v130\n"
"v_mfma_f32_16x16x32_bf16 v[0:3], v[184:187], a[52:55], v[0:3]\n"
"v_lshlrev_b32 v60, 16, v126\n"
"v_lshlrev_b32 v204, 16, v134\n"
"v_mfma_f32_16x16x32_bf16 v[0:3], v[188:191], a[56:59], v[0:3]\n"
"ds_read_b64_tr_b16 v[110:111], v233 offset:43008\n"
"v_add_f32 v56, v56, v202\n"
"v_mfma_f32_16x16x32_bf16 v[0:3], v[192:195], a[60:63], v[0:3]\n"
"s_waitcnt lgkmcnt(5)\n"
"s_waitcnt vmcnt(3)\n"
"ds_read_b64_tr_b16 v[112:113], v229 offset:49152\n"
"v_and_b32 v63, s73, v127\n"
"v_mfma_f32_16x16x32_bf16 v[0:3], v[208:211], a[0:3], v[0:3]\n"
"ds_read_b64_tr_b16 v[224:225], v233 offset:57344\n"
"v_and_b32 v134, s73, v134\n"
"v_mfma_f32_16x16x32_bf16 v[0:3], v[212:215], a[4:7], v[0:3]\n"
"s_waitcnt lgkmcnt(5)\n"
"s_waitcnt vmcnt(2)\n"
"ds_read_b64_tr_b16 v[108:109], v233 offset:40960\n"
"v_and_b32 v129, s73, v129\n"
"v_mfma_f32_16x16x32_bf16 v[0:3], v[216:219], a[8:11], v[0:3]\n"
"v_add_f32 v60, v60, v206\n"
"v_add_f32 v60, v60, v204\n"
"v_mfma_f32_16x16x32_bf16 v[0:3], v[220:223], a[12:15], v[0:3]\n"
"ds_read_b64_tr_b16 v[98:99], v229 offset:34816\n"
"ds_read_b64_tr_b16 v[122:123], v229 offset:59392\n"
"v_mfma_f32_16x16x32_bf16 v[16:19], v[220:223], a[28:31], v[16:19]\n"
"v_mov_b32 v165, 0\n"
"v_add_f32 v59, v59, v129\n"
"v_mfma_f32_16x16x32_bf16 v[16:19], v[216:219], a[24:27], v[16:19]\n"
"v_add_f32 v62, v62, v205\n"
"v_lshlrev_b32 v206, 16, v136\n"
"v_mfma_f32_16x16x32_bf16 v[16:19], v[212:215], a[20:23], v[16:19]\n"
"ds_read_b64_tr_b16 v[104:105], v229 offset:40960\n"
"ds_read_b64_tr_b16 v[226:227], v233 offset:59392\n"
"v_mfma_f32_16x16x32_bf16 v[16:19], v[208:211], a[16:19], v[16:19]\n"
"v_add_f32 v56, v56, v206\n"
"v_lshlrev_b32 v202, 16, v138\n"
"v_mfma_f32_16x16x32_bf16 v[16:19], v[192:195], a[76:79], v[16:19]\n"
"s_waitcnt vmcnt(0)\n"
"ds_read_b64_tr_b16 v[118:119], v233 offset:51200\n"
"v_subrev_f32 v2, s22, v2\n"
"v_mfma_f32_16x16x32_bf16 v[16:19], v[188:191], a[72:75], v[16:19]\n"
"v_subrev_u32 v200, s21, v200\n"
"ds_read_b64_tr_b16 v[114:115], v229 offset:51200\n"
"v_mfma_f32_16x16x32_bf16 v[16:19], v[184:187], a[68:71], v[16:19]\n"
"v_cmp_lt_u32 vcc, s69, v200\n"
"ds_read_b64_tr_b16 v[100:101], v233 offset:32768\n"
"v_mfma_f32_16x16x32_bf16 v[16:19], v[180:183], a[64:67], v[16:19]\n"
"v_lshlrev_b32 v206, 16, v142\n"
"s_cbranch_vccnz LGR_%=\nLGRK_%=:\n"
"v_mfma_f32_16x16x32_bf16 v[32:35], v[180:183], a[80:83], v[32:35]\n"
"v_subrev_f32 v3, s22, v3\n"
"s_cmp_eq_u32 s93, 0\ns_cbranch_scc1 LEP_%=\nLEPK_%=:\n"
"v_mfma_f32_16x16x32_bf16 v[32:35], v[184:187], a[84:87], v[32:35]\n"
"s_bfe_u32 s20, s89, 65536\ns_lshl_b32 s20, s20, 13\ns_add_u32 s20, s20, 65536\n"
"ds_read_b64_tr_b16 v[106:107], v229 offset:43008\n"
"v_and_b32 v128, s73, v128\n"
"v_mfma_f32_16x16x32_bf16 v[32:35], v[188:191], a[88:91], v[32:35]\n"
"buffer_load_dwordx4 v[72:75], v236, s[32:35], s20 offen sc1\n"
"v_mfma_f32_16x16x32_bf16 v[32:35], v[192:195], a[92:95], v[32:35]\n"
"v_lshlrev_b32 v203, 16, v133\n"
"v_lshlrev_b32 v205, 16, v141\n"
"v_mfma_f32_16x16x32_bf16 v[32:35], v[208:211], a[32:35], v[32:35]\n"
"s_bfe_u32 s20, s89, 65540\ns_lshl_b32 s20, s20, 13\ns_add_u32 s20, s20, 1114112\n"
"v_lshlrev_b32 v204, 16, v140\n"
"v_add_f32 v56, v56, v204\n"
"v_mfma_f32_16x16x32_bf16 v[32:35], v[212:215], a[36:39], v[32:35]\n"
"buffer_load_dwordx4 v[76:79], v236, s[32:35], s20 offen sc1\n"
"v_mfma_f32_16x16x32_bf16 v[32:35], v[216:219], a[40:43], v[32:35]\n"
"v_and_b32 v136, s73, v136\n"
"v_subrev_f32 v17, s22, v17\n"
"v_mfma_f32_16x16x32_bf16 v[32:35], v[220:223], a[44:47], v[32:35]\n"
"s_bfe_u32 s20, s89, 65544\ns_lshl_b32 s20, s20, 13\ns_add_u32 s20, s20, 2162688\n"
"v_add_f32 v61, v61, v134\n"
"v_and_b32 v141, s73, v141\n"
"v_mfma_f32_16x16x32_bf16 v[48:51], v[220:223], a[228:231], v[48:51]\n"
"buffer_load_dwordx4 v[80:83], v236, s[32:35], s20 offen sc1\n"
"v_mfma_f32_16x16x32_bf16 v[48:51], v[216:219], a[224:227], v[48:51]\n"
"v_add_f32 v58, v58, v203\n"
"v_subrev_f32 v19, s22, v19\n"
"v_mfma_f32_16x16x32_bf16 v[48:51], v[212:215], a[212:215], v[48:51]\n"
"s_bfe_u32 s20, s89, 65548\ns_lshl_b32 s20, s20, 13\ns_add_u32 s20, s20, 3211264\n"
"v_and_b32 v140, s73, v140\n"
"v_lshlrev_b32 v204, 16, v146\n"
"v_mfma_f32_16x16x32_bf16 v[48:51], v[208:211], a[208:211], v[48:51]\n"
"buffer_load_dwordx4 v[84:87], v236, s[32:35], s20 offen sc1\n"
"v_mfma_f32_16x16x32_bf16 v[48:51], v[192:195], a[236:239], v[48:51]\n"
"v_add_f32 v57, v57, v128\n"
"v_subrev_f32 v34, s22, v34\n"
"v_mfma_f32_16x16x32_bf16 v[48:51], v[188:191], a[232:235], v[48:51]\n"
"s_bfe_u32 s20, s89, 65552\ns_lshl_b32 s20, s20, 13\ns_add_u32 s20, s20, 4259840\n"
"ds_read_b64_tr_b16 v[214:215], v234 offset:51200\n"
"v_and_b32 v132, s73, v132\n"
"v_mfma_f32_16x16x32_bf16 v[48:51], v[184:187], a[220:223], v[48:51]\n"
"buffer_load_dwordx4 v[88:91], v236, s[32:35], s20 offen sc1\n"
"v_mfma_f32_16x16x32_bf16 v[48:51], v[180:183], a[216:219], v[48:51]\n"
"s_waitcnt lgkmcnt(1)\n"
"v_add_f32 v60, v60, v202\n"
"v_add_f32 v60, v60, v206\n"
"v_mfma_f32_16x16x32_bf16 v[4:7], v[96:99], a[48:51], v[4:7]\n"
"s_bfe_u32 s20, s89, 65556\ns_lshl_b32 s20, s20, 13\ns_add_u32 s20, s20, 5308416\n"
"v_lshlrev_b32 v206, 16, v148\n"
"v_subrev_f32 v35, s22, v35\n"
"v_mfma_f32_16x16x32_bf16 v[4:7], v[100:103], a[52:55], v[4:7]\n"
"buffer_load_dwordx4 v[92:95], v236, s[32:35], s20 offen sc1\n"
"v_mfma_f32_16x16x32_bf16 v[4:7], v[104:107], a[56:59], v[4:7]\n"
"v_add_f32 v60, v60, v204\n"
"v_subrev_f32 v0, s22, v0\n"
"v_mfma_f32_16x16x32_bf16 v[4:7], v[108:111], a[60:63], v[4:7]\n"
"s_bfe_u32 s20, s89, 65560\ns_lshl_b32 s20, s20, 13\ns_add_u32 s20, s20, 6356992\n"
"v_add_f32 v57, v57, v132\n"
"v_subrev_f32 v33, s22, v33\n"
"v_mfma_f32_16x16x32_bf16 v[4:7], v[112:115], a[0:3], v[4:7]\n"
"buffer_load_dwordx4 v[244:247], v236, s[32:35], s20 offen sc1\n"
"v_mfma_f32_16x16x32_bf16 v[4:7], v[116:119], a[4:7], v[4:7]\n"
"v_and_b32 v138, s73, v138\n"
"v_subrev_f32 v18, s22, v18\n"
"v_mfma_f32_16x16x32_bf16 v[4:7], v[120:123], a[8:11], v[4:7]\n"
"s_bfe_u32 s20, s89, 65564\ns_lshl_b32 s20, s20, 13\ns_add_u32 s20, s20, 7405568\n"
"v_and_b32 v135, s73, v135\n"
"v_subrev_f32 v49, s22, v49\n"
"v_mfma_f32_16x16x32_bf16 v[4:7], v[224:227], a[12:15], v[4:7]\n"
"buffer_load_dwordx4 v[156:159], v236, s[32:35], s20 offen sc1\n"
"v_add_f32 v58, v58, v207\n"
"v_lshlrev_b32 v203, 16, v139\n"
"v_add_f32 v58, v58, v205\n"
"v_and_b32 v142, s73, v142\n"
"v_lshlrev_b32 v202, 16, v144\n"
"v_add_f32 v56, v56, v202\n"
"v_add_f32 v56, v56, v206\n"
"v_subrev_f32 v1, s22, v1\n"
"v_mfma_f32_16x16x32_bf16 v[20:23], v[224:227], a[28:31], v[20:23]\n"
"v_add_f32 v63, v63, v131\n"
"v_lshlrev_b32 v204, 16, v152\n"
"v_mfma_f32_16x16x32_bf16 v[20:23], v[120:123], a[24:27], v[20:23]\n"
"v_and_b32 v133, s73, v133\n"
"v_add_f32 v59, v59, v133\n"
"v_mfma_f32_16x16x32_bf16 v[20:23], v[116:119], a[20:23], v[20:23]\n"
"ds_read_b64_tr_b16 v[218:219], v230 offset:59392\n"
"v_and_b32 v137, s73, v137\n"
"v_mfma_f32_16x16x32_bf16 v[20:23], v[112:115], a[16:19], v[20:23]\n"
"v_and_b32 v148, s73, v148\n"
"v_subrev_f32 v16, s22, v16\n"
"v_mfma_f32_16x16x32_bf16 v[20:23], v[108:111], a[76:79], v[20:23]\n"
"v_add_f32 v62, v62, v203\n"
"v_subrev_f32 v6, s22, v6\n"
"v_mfma_f32_16x16x32_bf16 v[20:23], v[104:107], a[72:75], v[20:23]\n"
"v_add_f32 v57, v57, v136\n"
"v_lshlrev_b32 v205, 16, v147\n"
"v_mfma_f32_16x16x32_bf16 v[20:23], v[100:103], a[68:71], v[20:23]\n"
"ds_read_b64_tr_b16 v[194:195], v234 offset:43008\n"
"v_subrev_f32 v32, s22, v32\n"
"v_mfma_f32_16x16x32_bf16 v[20:23], v[96:99], a[64:67], v[20:23]\n"
"ds_read_b64_tr_b16 v[184:185], v234 offset:32768\n"
"v_add_f32 v59, v59, v137\n"
"v_and_b32 v139, s73, v139\n"
"v_add_f32 v59, v59, v141\n"
"v_lshlrev_b32 v207, 16, v143\n"
"v_lshlrev_b32 v203, 16, v145\n"
"v_and_b32 v145, s73, v145\n"
"v_add_f32 v58, v58, v203\n"
"v_add_f32 v59, v59, v145\n"
"v_and_b32 v147, s73, v147\n"
"v_lshlrev_b32 v203, 16, v151\n"
"v_subrev_f32 v4, s22, v4\n"
"v_subrev_f32 v5, s22, v5\n"
"v_subrev_f32 v7, s22, v7\n"
"v_cvt_pk_bf16_f32 v0, v0, v1\n"
"v_cvt_pk_bf16_f32 v1, v2, v3\n"
"v_cvt_pk_bf16_f32 v3, v6, v7\n"
"v_mfma_f32_16x16x32_bf16 v[36:39], v[96:99], a[80:83], v[36:39]\n"
"v_add_f32 v56, v56, v204\n"
"v_cvt_pk_bf16_f32 v2, v4, v5\n"
"v_mfma_f32_16x16x32_bf16 v[36:39], v[100:103], a[84:87], v[36:39]\n"
"ds_read_b64_tr_b16 v[192:193], v234 offset:40960\n"
"v_add_f32 v61, v61, v138\n"
"v_mfma_f32_16x16x32_bf16 v[36:39], v[104:107], a[88:91], v[36:39]\n"
"v_add_f32 v57, v57, v140\n"
"v_subrev_f32 v23, s22, v23\n"
"v_mfma_f32_16x16x32_bf16 v[36:39], v[108:111], a[92:95], v[36:39]\n"
"ds_read_b64_tr_b16 v[180:181], v230 offset:32768\n"
"v_and_b32 v144, s73, v144\n"
"v_mfma_f32_16x16x32_bf16 v[36:39], v[112:115], a[32:35], v[36:39]\n"
"ds_read_b64_tr_b16 v[210:211], v230 offset:51200\n"
"v_add_f32 v62, v62, v207\n"
"v_add_f32 v62, v62, v205\n"
"v_lshlrev_b32 v207, 16, v149\n"
"v_add_f32 v58, v58, v207\n"
"v_lshlrev_b32 v202, 16, v150\n"
"v_permlane16_swap_b32 v0, v2\n"
"v_permlane16_swap_b32 v1, v3\n"
"v_subrev_f32 v20, s22, v20\n"
"v_cvt_pk_bf16_f32 v16, v16, v17\n"
"v_mfma_f32_16x16x32_bf16 v[36:39], v[116:119], a[36:39], v[36:39]\n"
"ds_read_b64_tr_b16 v[186:187], v234 offset:34816\n"
"v_add_f32 v63, v63, v135\n"
"v_mfma_f32_16x16x32_bf16 v[36:39], v[120:123], a[40:43], v[36:39]\n"
"ds_read_b64_tr_b16 v[188:189], v230 offset:40960\n"
"buffer_store_dwordx4 v[0:3], v238, s[36:39], 0 offen sc0 sc1\n"
"v_and_b32 v143, s73, v143\n"
"v_and_b32 v146, s73, v146\n"
"v_and_b32 v149, s73, v149\n"
"v_lshlrev_b32 v205, 16, v153\n"
"v_and_b32 v153, s73, v153\n"
"v_lshlrev_b32 v206, 16, v154\n"
"v_lshlrev_b32 v207, 16, v155\n"
"v_subrev_f32 v21, s22, v21\n"
"v_subrev_f32 v22, s22, v22\n"
"v_cvt_pk_bf16_f32 v17, v18, v19\n"
"v_cvt_pk_bf16_f32 v18, v20, v21\n"
"v_cvt_pk_bf16_f32 v32, v32, v33\n"
"v_mfma_f32_16x16x32_bf16 v[36:39], v[224:227], a[44:47], v[36:39]\n"
"ds_read_b64_tr_b16 v[182:183], v230 offset:34816\n"
"v_add_f32 v63, v63, v139\n"
"v_add_f32 v61, v61, v142\n"
"v_add_f32 v57, v57, v144\n"
"v_add_f32 v57, v57, v148\n"
"v_add_f32 v59, v59, v149\n"
"v_cvt_pk_bf16_f32 v19, v22, v23\n"
"v_permlane16_swap_b32 v16, v18\n"
"v_cvt_pk_bf16_f32 v33, v34, v35\n"
"v_mfma_f32_16x16x32_bf16 v[52:55], v[224:227], a[228:231], v[52:55]\n"
"ds_read_b64_tr_b16 v[190:191], v230 offset:43008\n"
"v_add_f32 v59, v59, v153\n"
"v_mfma_f32_16x16x32_bf16 v[52:55], v[120:123], a[224:227], v[52:55]\n"
"v_permlane16_swap_b32 v17, v19\n"
"v_mfma_f32_16x16x32_bf16 v[52:55], v[116:119], a[212:215], v[52:55]\n"
"ds_read_b64_tr_b16 v[208:209], v230 offset:49152\n"
"v_subrev_f32 v36, s22, v36\n"
"v_mfma_f32_16x16x32_bf16 v[52:55], v[112:115], a[208:211], v[52:55]\n"
"ds_read_b64_tr_b16 v[222:223], v234 offset:59392\n"
"buffer_store_dwordx4 v[16:19], v238, s[36:39], s52 offen sc0 sc1\n"
"v_add_f32 v63, v63, v143\n"
"v_add_f32 v60, v60, v202\n"
"v_add_f32 v62, v62, v203\n"
"v_and_b32 v152, s73, v152\n"
"v_subrev_f32 v38, s22, v38\n"
"v_subrev_f32 v39, s22, v39\n"
"v_mfma_f32_16x16x32_bf16 v[52:55], v[108:111], a[236:239], v[52:55]\n"
"v_and_b32 v155, s73, v155\n"
"v_cvt_pk_bf16_f32 v35, v38, v39\n"
"v_mfma_f32_16x16x32_bf16 v[52:55], v[104:107], a[232:235], v[52:55]\n"
"ds_read_b64_tr_b16 v[216:217], v230 offset:57344\n"
"v_subrev_f32 v37, s22, v37\n"
"v_mfma_f32_16x16x32_bf16 v[52:55], v[100:103], a[220:223], v[52:55]\n"
"v_and_b32 v150, s73, v150\n"
"v_and_b32 v154, s73, v154\n"
"v_mfma_f32_16x16x32_bf16 v[52:55], v[96:99], a[216:219], v[52:55]\n"
"s_waitcnt lgkmcnt(4)\n"
"ds_read_b64_tr_b16 v[212:213], v234 offset:49152\n"
"ds_read_b64_tr_b16 v[220:221], v234 offset:57344\n"
"v_add_f32 v63, v63, v147\n"
"v_and_b32 v151, s73, v151\n"
"v_add_f32 v63, v63, v151\n"
"v_add_f32 v57, v57, v152\n"
"v_add_f32 v58, v58, v205\n"
"v_add_f32 v60, v60, v206\n"
"v_add_f32 v62, v62, v207\n"
"v_cvt_pk_bf16_f32 v34, v36, v37\n"
"v_permlane16_swap_b32 v33, v35\n"
"v_mfma_f32_16x16x32_bf16 v[8:11], v[180:183], a[48:51], v[8:11]\n"
"v_cvt_pk_bf16_f32 v56, v56, v57\n"
"v_cvt_pk_bf16_f32 v57, v58, v59\n"
"v_mfma_f32_16x16x32_bf16 v[8:11], v[184:187], a[52:55], v[8:11]\n"
"s_waitcnt lgkmcnt(5)\n"
"v_permlane16_swap_b32 v32, v34\n"
"v_mfma_f32_16x16x32_bf16 v[8:11], v[188:191], a[56:59], v[8:11]\n"
"buffer_store_dwordx4 v[32:35], v238, s[36:39], s53 offen sc0 sc1\n"
"ds_read_b64_tr_b16 v[120:121], v231 offset:57344\n"
"v_add_f32 v61, v61, v146\n"
"v_add_f32 v61, v61, v150\n"
"v_add_f32 v61, v61, v154\n"
"v_add_f32 v63, v63, v155\n"
"v_cvt_pk_bf16_f32 v58, v60, v61\n"
"v_cvt_pk_bf16_f32 v59, v62, v63\n"
"v_mfma_f32_16x16x32_bf16 v[8:11], v[192:195], a[60:63], v[8:11]\n"
"s_waitcnt lgkmcnt(4)\n"
"v_permlane16_swap_b32 v57, v59\n"
"v_mfma_f32_16x16x32_bf16 v[8:11], v[208:211], a[0:3], v[8:11]\n"
"s_waitcnt lgkmcnt(2)\n"
"v_permlane16_swap_b32 v56, v58\n"
"v_mfma_f32_16x16x32_bf16 v[8:11], v[212:215], a[4:7], v[8:11]\n"
"buffer_store_dwordx4 v[56:59], v253, s[36:39], 0 offen sc0 sc1\n"
"v_subrev_f32 v48, s22, v48\n"
"v_subrev_f32 v50, s22, v50\n"
"v_subrev_f32 v51, s22, v51\n"
"v_mfma_f32_16x16x32_bf16 v[8:11], v[216:219], a[8:11], v[8:11]\n"
"s_waitcnt lgkmcnt(1)\n"
"ds_read_b64_tr_b16 v[112:113], v231 offset:49152\n"
"v_subrev_f32 v54, s22, v54\n"
"v_mfma_f32_16x16x32_bf16 v[8:11], v[220:223], a[12:15], v[8:11]\n"
"ds_read_b64_tr_b16 v[100:101], v235 offset:32768\n"
"v_subrev_f32 v52, s22, v52\n"
"v_mfma_f32_16x16x32_bf16 v[24:27], v[220:223], a[28:31], v[24:27]\n"
"ds_read_b64_tr_b16 v[102:103], v235 offset:34816\n"
"v_subrev_f32 v53, s22, v53\n"
"v_mfma_f32_16x16x32_bf16 v[24:27], v[216:219], a[24:27], v[24:27]\n"
"v_cvt_pk_bf16_f32 v48, v48, v49\n"
"v_cvt_pk_bf16_f32 v49, v50, v51\n"
"v_mfma_f32_16x16x32_bf16 v[24:27], v[212:215], a[20:23], v[24:27]\n"
"ds_read_b64_tr_b16 v[96:97], v231 offset:32768\n"
"v_cvt_pk_bf16_f32 v50, v52, v53\n"
"v_mfma_f32_16x16x32_bf16 v[24:27], v[208:211], a[16:19], v[24:27]\n"
"s_nop 0\n"
"v_permlane16_swap_b32 v48, v50\n"
"v_mfma_f32_16x16x32_bf16 v[24:27], v[192:195], a[76:79], v[24:27]\n"
"ds_read_b64_tr_b16 v[116:117], v235 offset:49152\n"
"v_subrev_f32 v55, s22, v55\n"
"v_mfma_f32_16x16x32_bf16 v[24:27], v[188:191], a[72:75], v[24:27]\n"
"ds_read_b64_tr_b16 v[98:99], v231 offset:34816\n"
"v_cvt_pk_bf16_f32 v51, v54, v55\n"
"v_mfma_f32_16x16x32_bf16 v[24:27], v[184:187], a[68:71], v[24:27]\n"
"s_nop 0\n"
"v_permlane16_swap_b32 v49, v51\n"
"v_mfma_f32_16x16x32_bf16 v[24:27], v[180:183], a[64:67], v[24:27]\n"
"buffer_store_dwordx4 v[48:51], v252, s[36:39], 0 offen sc0 sc1\n"
"ds_read_b64_tr_b16 v[104:105], v231 offset:40960\n"
"v_mfma_f32_16x16x32_bf16 v[40:43], v[180:183], a[80:83], v[40:43]\n"
"ds_read_b64_tr_b16 v[106:107], v231 offset:43008\n"
"ds_read_b64_tr_b16 v[108:109], v235 offset:40960\n"
"v_mfma_f32_16x16x32_bf16 v[40:43], v[184:187], a[84:87], v[40:43]\n"
"ds_read_b64_tr_b16 v[110:111], v235 offset:43008\n"
"v_mfma_f32_16x16x32_bf16 v[40:43], v[188:191], a[88:91], v[40:43]\n"
"ds_read_b64_tr_b16 v[114:115], v231 offset:51200\n"
"v_subrev_f32 v10, s22, v10\n"
"v_mfma_f32_16x16x32_bf16 v[40:43], v[192:195], a[92:95], v[40:43]\n"
"ds_read_b64_tr_b16 v[118:119], v235 offset:51200\n"
"v_mfma_f32_16x16x32_bf16 v[40:43], v[208:211], a[32:35], v[40:43]\n"
"ds_read_b64_tr_b16 v[122:123], v231 offset:59392\n"
"v_subrev_f32 v24, s22, v24\n"
"v_mfma_f32_16x16x32_bf16 v[40:43], v[212:215], a[36:39], v[40:43]\n"
"ds_read_b64_tr_b16 v[224:225], v235 offset:57344\n"
"ds_read_b64_tr_b16 v[226:227], v235 offset:59392\n"
"v_mfma_f32_16x16x32_bf16 v[40:43], v[216:219], a[40:43], v[40:43]\n"
"v_mfma_f32_16x16x32_bf16 v[40:43], v[220:223], a[44:47], v[40:43]\n"
"s_waitcnt lgkmcnt(8)\n"
"v_mov_b32 v160, 0\n"
"v_subrev_f32 v11, s22, v11\n"
"v_mfma_f32_16x16x32_bf16 v[12:15], v[96:99], a[48:51], v[12:15]\n"
"v_subrev_f32 v8, s22, v8\n"
"v_subrev_f32 v9, s22, v9\n"
"v_mfma_f32_16x16x32_bf16 v[12:15], v[100:103], a[52:55], v[12:15]\n"
"s_waitcnt lgkmcnt(5)\n"
"v_cvt_pk_bf16_f32 v8, v8, v9\n"
"v_mfma_f32_16x16x32_bf16 v[12:15], v[104:107], a[56:59], v[12:15]\n"
"v_subrev_f32 v27, s22, v27\n"
"v_mfma_f32_16x16x32_bf16 v[12:15], v[108:111], a[60:63], v[12:15]\n"
"s_waitcnt lgkmcnt(3)\n"
"v_mfma_f32_16x16x32_bf16 v[12:15], v[112:115], a[0:3], v[12:15]\n"
"v_mfma_f32_16x16x32_bf16 v[12:15], v[116:119], a[4:7], v[12:15]\n"
"s_waitcnt lgkmcnt(0)\n"
"v_mfma_f32_16x16x32_bf16 v[12:15], v[120:123], a[8:11], v[12:15]\n"
"v_subrev_f32 v40, s22, v40\n"
"v_mfma_f32_16x16x32_bf16 v[12:15], v[224:227], a[12:15], v[12:15]\n"
"s_waitcnt vmcnt(5)\n"
"v_mfma_f32_16x16x32_bf16 v[64:67], v[162:165], v[72:75], 0\n"
"v_mfma_f32_16x16x32_bf16 v[64:67], v[162:165], v[76:79], v[64:67]\n"
"v_cvt_pk_bf16_f32 v9, v10, v11\n"
"v_mfma_f32_16x16x32_bf16 v[64:67], v[162:165], v[80:83], v[64:67]\n"
"s_nop 2\n"
"v_subrev_f32 v12, s22, v12\n"
"v_mfma_f32_16x16x32_bf16 v[64:67], v[162:165], v[84:87], v[64:67]\n"
"v_subrev_f32 v25, s22, v25\n"
"v_mfma_f32_16x16x32_bf16 v[64:67], v[162:165], v[88:91], v[64:67]\n"
"v_subrev_f32 v13, s22, v13\n"
"v_mfma_f32_16x16x32_bf16 v[64:67], v[162:165], v[92:95], v[64:67]\n"
"v_subrev_f32 v15, s22, v15\n"
"v_mfma_f32_16x16x32_bf16 v[64:67], v[162:165], v[244:247], v[64:67]\n"
"v_subrev_f32 v14, s22, v14\n"
"v_mfma_f32_16x16x32_bf16 v[64:67], v[162:165], v[156:159], v[64:67]\n"
"v_subrev_f32 v42, s22, v42\n"
"v_mfma_f32_16x16x32_bf16 v[68:71], v[160:163], v[72:75], 0\n"
"v_cvt_pk_bf16_f32 v11, v14, v15\n"
"v_subrev_f32 v41, s22, v41\n"
"v_mfma_f32_16x16x32_bf16 v[68:71], v[160:163], v[76:79], v[68:71]\n"
"v_cvt_pk_bf16_f32 v10, v12, v13\n"
"v_cvt_pk_bf16_f32 v24, v24, v25\n"
"v_mfma_f32_16x16x32_bf16 v[68:71], v[160:163], v[80:83], v[68:71]\n"
"v_mfma_f32_16x16x32_bf16 v[68:71], v[160:163], v[84:87], v[68:71]\n"
"v_permlane16_swap_b32 v9, v11\n"
"v_mfma_f32_16x16x32_bf16 v[68:71], v[160:163], v[88:91], v[68:71]\n"
"v_mfma_f32_16x16x32_bf16 v[68:71], v[160:163], v[92:95], v[68:71]\n"
"v_cvt_pk_bf16_f32 v64, v64, v65\n"
"v_cvt_pk_bf16_f32 v40, v40, v41\n"
"v_mfma_f32_16x16x32_bf16 v[68:71], v[160:163], v[244:247], v[68:71]\n"
"v_permlane16_swap_b32 v8, v10\n"
"v_mfma_f32_16x16x32_bf16 v[68:71], v[160:163], v[156:159], v[68:71]\n"
"buffer_store_dwordx4 v[8:11], v239, s[36:39], 0 offen sc0 sc1\n"
"v_mfma_f32_16x16x32_bf16 v[28:31], v[224:227], a[28:31], v[28:31]\n"
"v_mfma_f32_16x16x32_bf16 v[28:31], v[120:123], a[24:27], v[28:31]\n"
"v_cvt_pk_bf16_f32 v65, v66, v67\n"
"v_subrev_f32 v43, s22, v43\n"
"v_mfma_f32_16x16x32_bf16 v[28:31], v[116:119], a[20:23], v[28:31]\n"
"v_mfma_f32_16x16x32_bf16 v[28:31], v[112:115], a[16:19], v[28:31]\n"
"v_mfma_f32_16x16x32_bf16 v[28:31], v[108:111], a[76:79], v[28:31]\n"
"v_cvt_pk_bf16_f32 v66, v68, v69\n"
"v_cvt_pk_bf16_f32 v67, v70, v71\n"
"v_mfma_f32_16x16x32_bf16 v[28:31], v[104:107], a[72:75], v[28:31]\n"
"v_permlane16_swap_b32 v64, v66\n"
"v_mfma_f32_16x16x32_bf16 v[28:31], v[100:103], a[68:71], v[28:31]\n"
"v_permlane16_swap_b32 v65, v67\n"
"v_mfma_f32_16x16x32_bf16 v[28:31], v[96:99], a[64:67], v[28:31]\n"
"buffer_store_dwordx4 v[64:67], v253, s[36:39], 0 offen offset:128 sc0 sc1\n"
"v_cvt_pk_bf16_f32 v41, v42, v43\n"
"v_mfma_f32_16x16x32_bf16 v[44:47], v[96:99], a[80:83], v[44:47]\n"
"v_subrev_f32 v26, s22, v26\n"
"v_cvt_pk_bf16_f32 v25, v26, v27\n"
"v_mfma_f32_16x16x32_bf16 v[44:47], v[100:103], a[84:87], v[44:47]\n"
"v_mfma_f32_16x16x32_bf16 v[44:47], v[104:107], a[88:91], v[44:47]\n"
"s_nop 0\n"
"v_subrev_f32 v30, s22, v30\n"
"v_subrev_f32 v31, s22, v31\n"
"v_mfma_f32_16x16x32_bf16 v[44:47], v[108:111], a[92:95], v[44:47]\n"
"v_subrev_f32 v28, s22, v28\n"
"v_subrev_f32 v29, s22, v29\n"
"v_mfma_f32_16x16x32_bf16 v[44:47], v[112:115], a[32:35], v[44:47]\n"
"v_cvt_pk_bf16_f32 v26, v28, v29\n"
"v_cvt_pk_bf16_f32 v27, v30, v31\n"
"v_mfma_f32_16x16x32_bf16 v[44:47], v[116:119], a[36:39], v[44:47]\n"
"s_nop 0\n"
"v_permlane16_swap_b32 v25, v27\n"
"v_mfma_f32_16x16x32_bf16 v[44:47], v[120:123], a[40:43], v[44:47]\n"
"v_permlane16_swap_b32 v24, v26\n"
"v_mfma_f32_16x16x32_bf16 v[44:47], v[224:227], a[44:47], v[44:47]\n"
"buffer_store_dwordx4 v[24:27], v239, s[36:39], s52 offen sc0 sc1\n"
"s_nop 6\n"
"v_subrev_f32 v44, s22, v44\n"
"v_subrev_f32 v45, s22, v45\n"
"v_subrev_f32 v46, s22, v46\n"
"v_subrev_f32 v47, s22, v47\n"
"v_cvt_pk_bf16_f32 v42, v44, v45\n"
"v_cvt_pk_bf16_f32 v43, v46, v47\n"
"s_nop 0\n"
"v_permlane16_swap_b32 v40, v42\n"
"v_permlane16_swap_b32 v41, v43\n"
"buffer_store_dwordx4 v[40:43], v239, s[36:39], s53 offen sc0 sc1\n"
"s_branch LFIN_%=\n"
"LSW_%=:\n"
"s_add_u32 s16, s8, s95\n"
"s_addc_u32 s17, s9, 0\n"
"s_sub_u32 s18, 655360, s95\n"
"s_add_u32 s20, s8, s100\n"
"s_addc_u32 s21, s9, 0\n"
"s_sub_u32 s22, 655360, s100\n"
"s_mov_b32 s56, 0\n"
"s_mov_b32 s55, 0\n"
"s_mov_b32 s58, 0\n"
"s_mov_b32 s59, 163840\n"
"s_mov_b32 s52, 9216\n"
"s_mov_b32 s53, 18432\n"
"s_mov_b32 s54, 27648\n"
"v_lshrrev_b32 v206, 7, v242\n"
"v_mul_u32_u24 v206, 1280, v206\n"
"v_and_b32 v240, 0x7f, v240\n"
"v_add_u32 v240, v206, v240\n"
"buffer_load_dword v252, v240, s[16:19], 0 offen\n"
"s_branch LSWK_%=\n"
"LGP_%=:\n"
"s_mov_b32 s80, 0x40000\n"
"LGPa_%=:\n"
"s_sleep 1\n"
"global_load_dword v200, v201, s[6:7] sc1\n"
"s_sub_u32 s80, s80, 1\n"
"s_cmp_eq_u32 s80, 0\n"
"s_waitcnt vmcnt(0)\n"
"s_cbranch_scc1 LGPK_%=\n"
"v_subrev_u32 v200, s21, v200\n"
"v_cmp_lt_u32 vcc, s69, v200\n"
"s_nop 4\n"
"s_cbranch_vccnz LGPa_%=\n"
"s_branch LGPK_%=\n"
"LGQ_%=:\n"
"s_mov_b32 s80, 0x40000\n"
"LGQa_%=:\n"
"s_sleep 1\n"
"global_load_dword v200, v201, s[6:7] sc1\n"
"s_sub_u32 s80, s80, 1\n"
"s_cmp_eq_u32 s80, 0\n"
"s_waitcnt vmcnt(0)\n"
"s_cbranch_scc1 LGQK_%=\n"
"v_subrev_u32 v200, s21, v200\n"
"v_cmp_lt_u32 vcc, s69, v200\n"
"s_nop 4\n"
"s_cbranch_vccnz LGQa_%=\n"
"s_branch LGQK_%=\n"
"LGR_%=:\n"
"s_mov_b32 s80, 0x40000\n"
"LGRa_%=:\n"
"s_sleep 1\n"
"global_load_dword v200, v201, s[6:7] sc1\n"
"s_sub_u32 s80, s80, 1\n"
"s_cmp_eq_u32 s80, 0\n"
"s_waitcnt vmcnt(0)\n"
"s_cbranch_scc1 LGRK_%=\n"
"v_subrev_u32 v200, s21, v200\n"
"v_cmp_lt_u32 vcc, s69, v200\n"
"s_nop 4\n"
"s_cbranch_vccnz LGRa_%=\n"
"s_branch LGRK_%=\n"
"LGV_%=:\n"
"s_mov_b32 s80, 0x40000\n"
"LGVa_%=:\n"
"s_sleep 1\n"
"global_load_dword v200, v201, s[6:7] sc1\n"
"s_sub_u32 s80, s80, 1\n"
"s_cmp_eq_u32 s80, 0\n"
"s_waitcnt vmcnt(0)\n"
"s_cbranch_scc1 LGVK_%=\n"
"v_subrev_u32 v200, s21, v200\n"
"v_cmp_lt_u32 vcc, s69, v200\n"
"s_nop 4\n"
"s_cbranch_vccnz LGVa_%=\n"
"s_branch LGVK_%=\n"
"LEP_%=:\n"
"s_add_u32 s92, s92, 1\n"
"v_mov_b32 v200, s92\n"
"v_mov_b32 v255, 0\n"
"s_mov_b64 exec, 1\n"
"global_store_dword v255, v200, s[6:7] offset:128 sc1\n"
"s_mov_b64 exec, -1\n"
"s_branch LEPK_%=\n"
"LPCE0_%=:\n"
"s_memrealtime s[80:81]\n"
"s_sub_u32 s82, s67, s66\n"
"s_cmp_gt_i32 s82, 0\n"
"s_cselect_b32 s83, s67, s66\n"
"s_waitcnt lgkmcnt(0)\n"
"s_mov_b32 s63, 0\n"
"s_mov_b32 s62, 122\n"
"s_sub_u32 s82, s83, s80\n"
"s_cmp_gt_i32 s82, 3\n"
"s_cbranch_scc0 LPEE0_%=\n"
"LPDE0_%=:\n"
"s_sleep 1\n"
"s_memrealtime s[80:81]\n"
"s_waitcnt lgkmcnt(0)\n"
"s_sub_u32 s82, s83, s80\n"
"s_cmp_gt_i32 s82, 3\n"
"s_cbranch_scc1 LPDE0_%=\n"
"LPEE0_%=:\n"
"s_add_u32 s66, s66, s72\n"
"s_add_u32 s66, s66, 4\n"
"s_add_u32 s67, s80, s72\n"
"s_sub_u32 s72, s72, 3\n"
"s_max_u32 s72, s72, s62\n"
"s_branch LPKE0_%=\n"
"LPCE1_%=:\n"
"s_memrealtime s[80:81]\n"
"s_sub_u32 s82, s67, s66\n"
"s_cmp_gt_i32 s82, 0\n"
"s_cselect_b32 s83, s67, s66\n"
"s_waitcnt lgkmcnt(0)\n"
"s_add_u32 s72, s62, 16\n"
"s_sub_u32 s82, s83, s80\n"
"s_cmp_gt_i32 s82, 3\n"
"s_cbranch_scc0 LPEE1_%=\n"
"LPDE1_%=:\n"
"s_sleep 1\n"
"s_memrealtime s[80:81]\n"
"s_waitcnt lgkmcnt(0)\n"
"s_sub_u32 s82, s83, s80\n"
"s_cmp_gt_i32 s82, 3\n"
"s_cbranch_scc1 LPDE1_%=\n"
"LPEE1_%=:\n"
"s_add_u32 s66, s66, s72\n"
"s_add_u32 s66, s66, 4\n"
"s_add_u32 s67, s80, s72\n"
"s_sub_u32 s72, s72, 3\n"
"s_max_u32 s72, s72, s62\n"
"s_branch LPKE1_%=\n"
"LPCT0_%=:\n"
"s_memrealtime s[80:81]\n"
"s_sub_u32 s82, s67, s66\n"
"s_cmp_gt_i32 s82, 0\n"
"s_cselect_b32 s83, s67, s66\n"
"s_waitcnt lgkmcnt(0)\n"
"s_sub_u32 s82, s83, s80\n"
"s_cmp_gt_i32 s82, 3\n"
"s_cbranch_scc0 LPET0_%=\n"
"LPDT0_%=:\n"
"s_sleep 1\n"
"s_memrealtime s[80:81]\n"
"s_waitcnt lgkmcnt(0)\n"
"s_sub_u32 s82, s83, s80\n"
"s_cmp_gt_i32 s82, 3\n"
"s_cbranch_scc1 LPDT0_%=\n"
"LPET0_%=:\n"
"s_add_u32 s66, s66, s72\n"
"s_add_u32 s66, s66, 4\n"
"s_add_u32 s67, s80, s72\n"
"s_sub_u32 s72, s72, 3\n"
"s_max_u32 s72, s72, s62\n"
"s_branch LPKT0_%=\n"
"LPCT1_%=:\n"
"s_memrealtime s[80:81]\n"
"s_sub_u32 s82, s67, s66\n"
"s_cmp_gt_i32 s82, 0\n"
"s_cselect_b32 s83, s67, s66\n"
"s_waitcnt lgkmcnt(0)\n"
"s_add_u32 s72, s62, 16\n"
"s_sub_u32 s82, s83, s80\n"
"s_cmp_gt_i32 s82, 3\n"
"s_cbranch_scc0 LPET1_%=\n"
"LPDT1_%=:\n"
"s_sleep 1\n"
"s_memrealtime s[80:81]\n"
"s_waitcnt lgkmcnt(0)\n"
"s_sub_u32 s82, s83, s80\n"
"s_cmp_gt_i32 s82, 3\n"
"s_cbranch_scc1 LPDT1_%=\n"
"LPET1_%=:\n"
"s_add_u32 s66, s66, s72\n"
"s_add_u32 s66, s66, 4\n"
"s_add_u32 s67, s80, s72\n"
"s_sub_u32 s72, s72, 3\n"
"s_max_u32 s72, s72, s62\n"
"s_branch LPKT1_%=\n"
"LPCL0_%=:\n"
"s_memrealtime s[80:81]\n"
"s_sub_u32 s82, s67, s66\n"
"s_cmp_gt_i32 s82, 0\n"
"s_cselect_b32 s83, s67, s66\n"
"s_waitcnt lgkmcnt(0)\n"
"s_sub_u32 s82, s80, s67\n"
"s_add_u32 s63, s63, 1\n"
"s_min_i32 s81, s63, 0\n"
"s_cmp_gt_i32 s82, 20\n"
"s_cselect_b32 s63, s63, s81\n"
"s_cmp_ge_i32 s63, 2\n"
"s_cbranch_scc0 LPRL0_%=\n"
"s_mov_b32 s63, -9\n"
"s_add_u32 s62, s62, 14\n"
"s_min_u32 s62, s62, 146\n"
"s_max_u32 s72, s72, s62\n"
"s_add_u32 s83, s80, 200\n"
"s_and_b32 s83, s83, -1\n"
"LPRL0_%=:\n"
"s_sub_u32 s82, s83, s80\n"
"s_cmp_gt_i32 s82, 3\n"
"s_cbranch_scc0 LPEL0_%=\n"
"LPDL0_%=:\n"
"s_sleep 1\n"
"s_memrealtime s[80:81]\n"
"s_waitcnt lgkmcnt(0)\n"
"s_sub_u32 s82, s83, s80\n"
"s_cmp_gt_i32 s82, 3\n"
"s_cbranch_scc1 LPDL0_%=\n"
"LPEL0_%=:\n"
"s_add_u32 s66, s66, s72\n"
"s_add_u32 s66, s66, 4\n"
"s_add_u32 s67, s80, s72\n"
"s_sub_u32 s72, s72, 3\n"
"s_max_u32 s72, s72, s62\n"
"s_branch LPKL0_%=\n"
"LPCL1_%=:\n"
"s_memrealtime s[80:81]\n"
"s_sub_u32 s82, s67, s66\n"
"s_cmp_gt_i32 s82, 0\n"
"s_cselect_b32 s83, s67, s66\n"
"s_waitcnt lgkmcnt(0)\n"
"s_sub_u32 s82, s80, s67\n"
"s_add_u32 s63, s63, 1\n"
"s_min_i32 s81, s63, 0\n"
"s_cmp_gt_i32 s82, 20\n"
"s_cselect_b32 s63, s63, s81\n"
"s_cmp_ge_i32 s63, 2\n"
"s_cbranch_scc0 LPRL1_%=\n"
"s_mov_b32 s63, -9\n"
"s_add_u32 s62, s62, 14\n"
"s_min_u32 s62, s62, 146\n"
"s_max_u32 s72, s72, s62\n"
"s_add_u32 s83, s80, 200\n"
"s_and_b32 s83, s83, -1\n"
"LPRL1_%=:\n"
"s_sub_u32 s82, s83, s80\n"
"s_cmp_gt_i32 s82, 3\n"
"s_cbranch_scc0 LPEL1_%=\n"
"LPDL1_%=:\n"
"s_sleep 1\n"
"s_memrealtime s[80:81]\n"
"s_waitcnt lgkmcnt(0)\n"
"s_sub_u32 s82, s83, s80\n"
"s_cmp_gt_i32 s82, 3\n"
"s_cbranch_scc1 LPDL1_%=\n"
"LPEL1_%=:\n"
"s_add_u32 s66, s66, s72\n"
"s_add_u32 s66, s66, 4\n"
"s_add_u32 s67, s80, s72\n"
"s_sub_u32 s72, s72, 3\n"
"s_max_u32 s72, s72, s62\n"
"s_branch LPKL1_%=\n"
"LPCL2_%=:\n"
"s_memrealtime s[80:81]\n"
"s_sub_u32 s82, s67, s66\n"
"s_cmp_gt_i32 s82, 0\n"
"s_cselect_b32 s83, s67, s66\n"
"s_waitcnt lgkmcnt(0)\n"
"s_sub_u32 s82, s80, s67\n"
"s_add_u32 s63, s63, 1\n"
"s_min_i32 s81, s63, 0\n"
"s_cmp_gt_i32 s82, 20\n"
"s_cselect_b32 s63, s63, s81\n"
"s_cmp_ge_i32 s63, 2\n"
"s_cbranch_scc0 LPRL2_%=\n"
"s_mov_b32 s63, -9\n"
"s_add_u32 s62, s62, 14\n"
"s_min_u32 s62, s62, 146\n"
"s_max_u32 s72, s72, s62\n"
"s_add_u32 s83, s80, 200\n"
"s_and_b32 s83, s83, -1\n"
"LPRL2_%=:\n"
"s_sub_u32 s82, s83, s80\n"
"s_cmp_gt_i32 s82, 3\n"
"s_cbranch_scc0 LPEL2_%=\n"
"LPDL2_%=:\n"
"s_sleep 1\n"
"s_memrealtime s[80:81]\n"
"s_waitcnt lgkmcnt(0)\n"
"s_sub_u32 s82, s83, s80\n"
"s_cmp_gt_i32 s82, 3\n"
"s_cbranch_scc1 LPDL2_%=\n"
"LPEL2_%=:\n"
"s_add_u32 s66, s66, s72\n"
"s_add_u32 s66, s66, 4\n"
"s_add_u32 s67, s80, s72\n"
"s_sub_u32 s72, s72, 3\n"
"s_max_u32 s72, s72, s62\n"
"s_branch LPKL2_%=\n"
"LPCL3_%=:\n"
"s_memrealtime s[80:81]\n"
"s_sub_u32 s82, s67, s66\n"
"s_cmp_gt_i32 s82, 0\n"
"s_cselect_b32 s83, s67, s66\n"
"s_waitcnt lgkmcnt(0)\n"
"s_sub_u32 s82, s80, s67\n"
"s_add_u32 s63, s63, 1\n"
"s_min_i32 s81, s63, 0\n"
"s_cmp_gt_i32 s82, 20\n"
"s_cselect_b32 s63, s63, s81\n"
"s_cmp_ge_i32 s63, 2\n"
"s_cbranch_scc0 LPRL3_%=\n"
"s_mov_b32 s63, -9\n"
"s_add_u32 s62, s62, 14\n"
"s_min_u32 s62, s62, 146\n"
"s_max_u32 s72, s72, s62\n"
"s_add_u32 s83, s80, 200\n"
"s_and_b32 s83, s83, -1\n"
"LPRL3_%=:\n"
"s_sub_u32 s82, s83, s80\n"
"s_cmp_gt_i32 s82, 3\n"
"s_cbranch_scc0 LPEL3_%=\n"
"LPDL3_%=:\n"
"s_sleep 1\n"
"s_memrealtime s[80:81]\n"
"s_waitcnt lgkmcnt(0)\n"
"s_sub_u32 s82, s83, s80\n"
"s_cmp_gt_i32 s82, 3\n"
"s_cbranch_scc1 LPDL3_%=\n"
"LPEL3_%=:\n"
"s_add_u32 s66, s66, s72\n"
"s_add_u32 s66, s66, 4\n"
"s_add_u32 s67, s80, s72\n"
"s_sub_u32 s72, s72, 3\n"
"s_max_u32 s72, s72, s62\n"
"s_branch LPKL3_%=\n"
"LPN_%=:\n"
"v_mfma_f32_16x16x32_bf16 v[112:115], a[192:195], a[136:139], v[112:115]\n"
"v_mfma_f32_16x16x32_bf16 v[112:115], a[196:199], a[140:143], v[112:115]\n"
"v_mfma_f32_16x16x32_bf16 v[116:119], a[200:203], a[136:139], v[116:119]\n"
"s_waitcnt vmcnt(11)\n"
"v_mfma_f32_16x16x32_bf16 v[116:119], a[204:207], a[140:143], v[116:119]\n"
"s_barrier\n"
"v_mfma_f32_16x16x32_bf16 v[120:123], a[208:211], a[136:139], v[120:123]\n"
"s_add_u32 s40, s48, 256\n"
"s_xor_b32 m0, s50, 0xc000\n"
"ds_read_b64_tr_b16 a[224:225], v244 offset:16384\n"
"v_mfma_f32_16x16x32_bf16 v[120:123], a[212:215], a[140:143], v[120:123]\n"
"buffer_load_dwordx4 v224, s[28:31], s40 offen nt lds\n"
"v_mfma_f32_16x16x32_bf16 v[124:127], a[216:219], a[136:139], v[124:127]\n"
"ds_read_b64_tr_b16 a[226:227], v244 offset:16896\n"
"v_mfma_f32_16x16x32_bf16 v[124:127], a[220:223], a[140:143], v[124:127]\n"
"ds_read_b64_tr_b16 a[228:229], v244 offset:24576\n"
"v_mfma_f32_16x16x32_bf16 v[140:143], a[216:219], a[144:147], v[140:143]\n"
"ds_read_b64_tr_b16 a[230:231], v244 offset:25088\n"
"v_mfma_f32_16x16x32_bf16 v[140:143], a[220:223], a[148:151], v[140:143]\n"
"ds_read_b64_tr_b16 a[232:233], v245 offset:16384\n"
"v_mfma_f32_16x16x32_bf16 v[136:139], a[208:211], a[144:147], v[136:139]\n"
"ds_read_b64_tr_b16 a[234:235], v245 offset:16896\n"
"v_mfma_f32_16x16x32_bf16 v[136:139], a[212:215], a[148:151], v[136:139]\n"
"buffer_load_dwordx4 v225, s[28:31], s40 offen offset:1024 nt lds\n"
"v_mfma_f32_16x16x32_bf16 v[132:135], a[200:203], a[144:147], v[132:135]\n"
"ds_read_b64_tr_b16 a[236:237], v245 offset:24576\n"
"v_mfma_f32_16x16x32_bf16 v[132:135], a[204:207], a[148:151], v[132:135]\n"
"ds_read_b64_tr_b16 a[238:239], v245 offset:25088\n"
"v_mfma_f32_16x16x32_bf16 v[128:131], a[192:195], a[144:147], v[128:131]\n"
"ds_read_b64_tr_b16 a[240:241], v244 offset:17408\n"
"v_mfma_f32_16x16x32_bf16 v[128:131], a[196:199], a[148:151], v[128:131]\n"
"ds_read_b64_tr_b16 a[242:243], v244 offset:17920\n"
"v_mfma_f32_16x16x32_bf16 v[144:147], a[192:195], a[152:155], v[144:147]\n"
"ds_read_b64_tr_b16 a[244:245], v244 offset:25600\n"
"v_mfma_f32_16x16x32_bf16 v[144:147], a[196:199], a[156:159], v[144:147]\n"
"buffer_load_dwordx4 v226, s[28:31], s40 offen offset:2048 nt lds\n"
"v_mfma_f32_16x16x32_bf16 v[148:151], a[200:203], a[152:155], v[148:151]\n"
"ds_read_b64_tr_b16 a[246:247], v244 offset:26112\n"
"v_mfma_f32_16x16x32_bf16 v[148:151], a[204:207], a[156:159], v[148:151]\n"
"ds_read_b64_tr_b16 a[248:249], v245 offset:17408\n"
"v_mfma_f32_16x16x32_bf16 v[152:155], a[208:211], a[152:155], v[152:155]\n"
"ds_read_b64_tr_b16 a[250:251], v245 offset:17920\n"
"v_mfma_f32_16x16x32_bf16 v[152:155], a[212:215], a[156:159], v[152:155]\n"
"ds_read_b64_tr_b16 a[252:253], v245 offset:25600\n"
"v_mfma_f32_16x16x32_bf16 v[156:159], a[216:219], a[152:155], v[156:159]\n"
"ds_read_b64_tr_b16 a[254:255], v245 offset:26112\n"
"v_mfma_f32_16x16x32_bf16 v[156:159], a[220:223], a[156:159], v[156:159]\n"
"buffer_load_dwordx4 v227, s[28:31], s40 offen offset:3072 nt lds\n"
"v_mfma_f32_16x16x32_bf16 v[172:175], a[216:219], a[160:163], v[172:175]\n"
"s_cmp_eq_u32 s47, 8\n"
"v_mfma_f32_16x16x32_bf16 v[172:175], a[220:223], a[164:167], v[172:175]\n"
"s_cselect_b32 s41, s52, s58\n"
"v_mfma_f32_16x16x32_bf16 v[168:171], a[208:211], a[160:163], v[168:171]\n"
"s_add_u32 s48, s48, s41\n"
"v_mfma_f32_16x16x32_bf16 v[168:171], a[212:215], a[164:167], v[168:171]\n"
"s_sub_u32 s41, s48, 5242880\n"
"v_mfma_f32_16x16x32_bf16 v[164:167], a[200:203], a[160:163], v[164:167]\n"
"s_min_u32 s48, s48, s41\n"
"v_mfma_f32_16x16x32_bf16 v[164:167], a[204:207], a[164:167], v[164:167]\n"
"v_xor_b32 v244, s74, v244\n"
"v_xor_b32 v246, s74, v246\n"
"v_mfma_f32_16x16x32_bf16 v[160:163], a[192:195], a[160:163], v[160:163]\n"
"s_mov_b32 m0, s50\n"
"v_xor_b32 v245, s74, v245\n"
"v_xor_b32 v247, s74, v247\n"
"v_mfma_f32_16x16x32_bf16 v[160:163], a[196:199], a[164:167], v[160:163]\n"
"buffer_load_dwordx4 v224, s[28:31], s48 offen nt lds\n"
"v_mfma_f32_16x16x32_bf16 v[176:179], a[192:195], a[168:171], v[176:179]\n"
"v_xor_b32 v252, s74, v252\n"
"v_xor_b32 v253, s74, v253\n"
"v_mfma_f32_16x16x32_bf16 v[176:179], a[196:199], a[172:175], v[176:179]\n"
"v_xor_b32 v250, s74, v250\n"
"v_xor_b32 v251, s74, v251\n"
"v_mfma_f32_16x16x32_bf16 v[180:183], a[200:203], a[168:171], v[180:183]\n"
"v_mfma_f32_16x16x32_bf16 v[180:183], a[204:207], a[172:175], v[180:183]\n"
"buffer_load_dwordx4 v225, s[28:31], s48 offen offset:1024 nt lds\n"
"v_mfma_f32_16x16x32_bf16 v[184:187], a[208:211], a[168:171], v[184:187]\n"
"v_mfma_f32_16x16x32_bf16 v[184:187], a[212:215], a[172:175], v[184:187]\n"
"v_mfma_f32_16x16x32_bf16 v[188:191], a[216:219], a[168:171], v[188:191]\n"
"v_mfma_f32_16x16x32_bf16 v[188:191], a[220:223], a[172:175], v[188:191]\n"
"v_mfma_f32_16x16x32_bf16 v[204:207], a[216:219], a[176:179], v[204:207]\n"
"v_mfma_f32_16x16x32_bf16 v[204:207], a[220:223], a[180:183], v[204:207]\n"
"buffer_load_dwordx4 v226, s[28:31], s48 offen offset:2048 nt lds\n"
"v_mfma_f32_16x16x32_bf16 v[200:203], a[208:211], a[176:179], v[200:203]\n"
"v_mfma_f32_16x16x32_bf16 v[200:203], a[212:215], a[180:183], v[200:203]\n"
"v_mfma_f32_16x16x32_bf16 v[196:199], a[200:203], a[176:179], v[196:199]\n"
"v_mfma_f32_16x16x32_bf16 v[196:199], a[204:207], a[180:183], v[196:199]\n"
"v_mfma_f32_16x16x32_bf16 v[192:195], a[192:195], a[176:179], v[192:195]\n"
"v_mfma_f32_16x16x32_bf16 v[192:195], a[196:199], a[180:183], v[192:195]\n"
"s_waitcnt lgkmcnt(0)\n"
"buffer_load_dwordx4 v227, s[28:31], s48 offen offset:3072 nt lds\n"
"v_mfma_f32_16x16x32_bf16 v[208:211], a[192:195], a[184:187], v[208:211]\n"
"s_xor_b32 s50, s50, s74\n"
"v_mfma_f32_16x16x32_bf16 v[208:211], a[196:199], a[188:191], v[208:211]\n"
"v_mfma_f32_16x16x32_bf16 v[212:215], a[200:203], a[184:187], v[212:215]\n"
"v_mfma_f32_16x16x32_bf16 v[212:215], a[204:207], a[188:191], v[212:215]\n"
"v_mfma_f32_16x16x32_bf16 v[216:219], a[208:211], a[184:187], v[216:219]\n"
"v_mfma_f32_16x16x32_bf16 v[216:219], a[212:215], a[188:191], v[216:219]\n"
"v_mfma_f32_16x16x32_bf16 v[220:223], a[216:219], a[184:187], v[220:223]\n"
"v_mfma_f32_16x16x32_bf16 v[220:223], a[220:223], a[188:191], v[220:223]\n"
"v_mfma_f32_16x16x32_bf16 v[0:3], a[224:227], a[136:139], v[0:3]\n"
"v_mfma_f32_16x16x32_bf16 v[0:3], a[228:231], a[140:143], v[0:3]\n"
"v_mfma_f32_16x16x32_bf16 v[4:7], a[232:235], a[136:139], v[4:7]\n"
"s_waitcnt vmcnt(8)\n"
"v_mfma_f32_16x16x32_bf16 v[4:7], a[236:239], a[140:143], v[4:7]\n"
"s_barrier\n"
"v_mfma_f32_16x16x32_bf16 v[8:11], a[240:243], a[136:139], v[8:11]\n"
"s_mov_b32 m0, s51\n"
"ds_read_b64_tr_b16 a[192:193], v244 offset:0\n"
"v_mfma_f32_16x16x32_bf16 v[8:11], a[244:247], a[140:143], v[8:11]\n"
"buffer_load_dword v242, s[80:83], 0 offen lds\n"
"s_add_u32 s80, s80, 680\n"
"s_addc_u32 s81, s81, 0\n"
"buffer_load_dwordx4 v248, s[24:27], s49 offen nt lds\n"
"v_mfma_f32_16x16x32_bf16 v[12:15], a[248:251], a[136:139], v[12:15]\n"
"ds_read_b64_tr_b16 a[194:195], v244 offset:512\n"
"v_mfma_f32_16x16x32_bf16 v[12:15], a[252:255], a[140:143], v[12:15]\n"
"ds_read_b128 a[136:139], v246 offset:0\n"
"v_mfma_f32_16x16x32_bf16 v[28:31], a[248:251], a[144:147], v[28:31]\n"
"ds_read_b64_tr_b16 a[196:197], v244 offset:8192\n"
"v_mfma_f32_16x16x32_bf16 v[28:31], a[252:255], a[148:151], v[28:31]\n"
"ds_read_b64_tr_b16 a[198:199], v244 offset:8704\n"
"v_mfma_f32_16x16x32_bf16 v[24:27], a[240:243], a[144:147], v[24:27]\n"
"ds_read_b128 a[140:143], v247 offset:0\n"
"v_mfma_f32_16x16x32_bf16 v[24:27], a[244:247], a[148:151], v[24:27]\n"
"buffer_load_dwordx4 v249, s[24:27], s49 offen offset:1024 nt lds\n"
"v_mfma_f32_16x16x32_bf16 v[20:23], a[232:235], a[144:147], v[20:23]\n"
"ds_read_b64_tr_b16 a[200:201], v245 offset:0\n"
"v_mfma_f32_16x16x32_bf16 v[20:23], a[236:239], a[148:151], v[20:23]\n"
"ds_read_b64_tr_b16 a[202:203], v245 offset:512\n"
"v_mfma_f32_16x16x32_bf16 v[16:19], a[224:227], a[144:147], v[16:19]\n"
"ds_read_b64_tr_b16 a[204:205], v245 offset:8192\n"
"v_mfma_f32_16x16x32_bf16 v[16:19], a[228:231], a[148:151], v[16:19]\n"
"ds_read_b64_tr_b16 a[206:207], v245 offset:8704\n"
"v_mfma_f32_16x16x32_bf16 v[32:35], a[224:227], a[152:155], v[32:35]\n"
"ds_read_b64_tr_b16 a[208:209], v244 offset:1024\n"
"v_mfma_f32_16x16x32_bf16 v[32:35], a[228:231], a[156:159], v[32:35]\n"
"buffer_load_dwordx4 v254, s[24:27], s49 offen offset:2048 nt lds\n"
"v_mfma_f32_16x16x32_bf16 v[36:39], a[232:235], a[152:155], v[36:39]\n"
"ds_read_b64_tr_b16 a[210:211], v244 offset:1536\n"
"v_mfma_f32_16x16x32_bf16 v[36:39], a[236:239], a[156:159], v[36:39]\n"
"ds_read_b64_tr_b16 a[212:213], v244 offset:9216\n"
"v_mfma_f32_16x16x32_bf16 v[40:43], a[240:243], a[152:155], v[40:43]\n"
"ds_read_b64_tr_b16 a[214:215], v244 offset:9728\n"
"v_mfma_f32_16x16x32_bf16 v[40:43], a[244:247], a[156:159], v[40:43]\n"
"ds_read_b64_tr_b16 a[216:217], v245 offset:1024\n"
"v_mfma_f32_16x16x32_bf16 v[44:47], a[248:251], a[152:155], v[44:47]\n"
"ds_read_b64_tr_b16 a[218:219], v245 offset:1536\n"
"v_mfma_f32_16x16x32_bf16 v[44:47], a[252:255], a[156:159], v[44:47]\n"
"buffer_load_dwordx4 v255, s[24:27], s49 offen offset:3072 nt lds\n"
"v_mfma_f32_16x16x32_bf16 v[60:63], a[248:251], a[160:163], v[60:63]\n"
"ds_read_b64_tr_b16 a[220:221], v245 offset:9216\n"
"v_mfma_f32_16x16x32_bf16 v[60:63], a[252:255], a[164:167], v[60:63]\n"
"ds_read_b64_tr_b16 a[222:223], v245 offset:9728\n"
"v_mfma_f32_16x16x32_bf16 v[56:59], a[240:243], a[160:163], v[56:59]\n"
"ds_read_b128 a[144:147], v246 offset:2048\n"
"v_mfma_f32_16x16x32_bf16 v[56:59], a[244:247], a[164:167], v[56:59]\n"
"s_add_u32 s40, s49, 40960\n"
"ds_read_b128 a[148:151], v247 offset:2048\n"
"v_mfma_f32_16x16x32_bf16 v[52:55], a[232:235], a[160:163], v[52:55]\n"
"s_add_u32 m0, s51, 4096\n"
"ds_read_b128 a[152:155], v246 offset:4096\n"
"v_mfma_f32_16x16x32_bf16 v[52:55], a[236:239], a[164:167], v[52:55]\n"
"buffer_load_dwordx4 v248, s[24:27], s40 offen nt lds\n"
"v_mfma_f32_16x16x32_bf16 v[48:51], a[224:227], a[160:163], v[48:51]\n"
"ds_read_b128 a[156:159], v247 offset:4096\n"
"v_mfma_f32_16x16x32_bf16 v[48:51], a[228:231], a[164:167], v[48:51]\n"
"ds_read_b128 a[160:163], v252 offset:0\n"
"v_mfma_f32_16x16x32_bf16 v[64:67], a[224:227], a[168:171], v[64:67]\n"
"ds_read_b128 a[164:167], v253 offset:0\n"
"v_mfma_f32_16x16x32_bf16 v[64:67], a[228:231], a[172:175], v[64:67]\n"
"v_mfma_f32_16x16x32_bf16 v[68:71], a[232:235], a[168:171], v[68:71]\n"
"v_mfma_f32_16x16x32_bf16 v[68:71], a[236:239], a[172:175], v[68:71]\n"
"buffer_load_dwordx4 v249, s[24:27], s40 offen offset:1024 nt lds\n"
"v_mfma_f32_16x16x32_bf16 v[72:75], a[240:243], a[168:171], v[72:75]\n"
"v_mfma_f32_16x16x32_bf16 v[72:75], a[244:247], a[172:175], v[72:75]\n"
"v_mfma_f32_16x16x32_bf16 v[76:79], a[248:251], a[168:171], v[76:79]\n"
"ds_read_b128 a[168:171], v250 offset:0\n"
"v_mfma_f32_16x16x32_bf16 v[76:79], a[252:255], a[172:175], v[76:79]\n"
"ds_read_b128 a[172:175], v251 offset:0\n"
"v_mfma_f32_16x16x32_bf16 v[92:95], a[248:251], a[176:179], v[92:95]\n"
"v_mfma_f32_16x16x32_bf16 v[92:95], a[252:255], a[180:183], v[92:95]\n"
"buffer_load_dwordx4 v254, s[24:27], s40 offen offset:2048 nt lds\n"
"v_mfma_f32_16x16x32_bf16 v[88:91], a[240:243], a[176:179], v[88:91]\n"
"s_add_u32 s49, s49, 128\n"
"v_mfma_f32_16x16x32_bf16 v[88:91], a[244:247], a[180:183], v[88:91]\n"
"s_sub_u32 s41, s49, 1280\n"
"v_mfma_f32_16x16x32_bf16 v[84:87], a[232:235], a[176:179], v[84:87]\n"
"s_min_u32 s49, s49, s41\n"
"v_mfma_f32_16x16x32_bf16 v[84:87], a[236:239], a[180:183], v[84:87]\n"
"s_xor_b32 s51, s51, s74\n"
"v_mfma_f32_16x16x32_bf16 v[80:83], a[224:227], a[176:179], v[80:83]\n"
"ds_read_b128 a[176:179], v250 offset:2048\n"
"v_mfma_f32_16x16x32_bf16 v[80:83], a[228:231], a[180:183], v[80:83]\n"
"ds_read_b128 a[180:183], v251 offset:2048\n"
"v_mfma_f32_16x16x32_bf16 v[96:99], a[224:227], a[184:187], v[96:99]\n"
"v_mfma_f32_16x16x32_bf16 v[96:99], a[228:231], a[188:191], v[96:99]\n"
"v_mfma_f32_16x16x32_bf16 v[100:103], a[232:235], a[184:187], v[100:103]\n"
"v_mfma_f32_16x16x32_bf16 v[100:103], a[236:239], a[188:191], v[100:103]\n"
"v_mfma_f32_16x16x32_bf16 v[104:107], a[240:243], a[184:187], v[104:107]\n"
"v_mfma_f32_16x16x32_bf16 v[104:107], a[244:247], a[188:191], v[104:107]\n"
"v_mfma_f32_16x16x32_bf16 v[108:111], a[248:251], a[184:187], v[108:111]\n"
"s_add_u32 s47, s47, 1\n"
"ds_read_b128 a[184:187], v250 offset:4096\n"
"v_mfma_f32_16x16x32_bf16 v[108:111], a[252:255], a[188:191], v[108:111]\n"
"s_waitcnt lgkmcnt(1)\n"
"s_cmp_lg_u32 s47, s62\n"
"ds_read_b128 a[188:191], v251 offset:4096\n"
"s_cbranch_scc1 LPN_%=\n"
"s_branch LPX_%=\n"
"LEND_%=:\n"
"LSLOW_%=:\n"
"s_waitcnt vmcnt(0) lgkmcnt(0)\n"
"s_nop 9\n"
"v_bfe_u32 v148, v242, 4, 4\n"
"v_bfe_u32 v149, v242, 8, 2\n"
"v_lshlrev_b32 v144, 8, v148\n"
"v_lshl_add_u32 v144, v149, 4, v144\n"
"v_add_u32 v144, s43, v144\n"
"v_xor_b32 v145, 64, v144\n"
"v_lshlrev_b32 v146, 1, v148\n"
"v_lshl_add_u32 v146, v149, 10, v146\n"
"v_add_u32 v146, s43, v146\n"
"v_xor_b32 v147, 64, v146\n"
"v_mov_b32 v152, 0xff7fffff\n"
"v_mov_b32 v155, 0\n"
"v_mov_b32 v0, 0\n"
"v_mov_b32 v1, 0\n"
"v_mov_b32 v2, 0\n"
"v_mov_b32 v3, 0\n"
"v_mov_b32 v4, 0\n"
"v_mov_b32 v5, 0\n"
"v_mov_b32 v6, 0\n"
"v_mov_b32 v7, 0\n"
"v_mov_b32 v8, 0\n"
"v_mov_b32 v9, 0\n"
"v_mov_b32 v10, 0\n"
"v_mov_b32 v11, 0\n"
"v_mov_b32 v12, 0\n"
"v_mov_b32 v13, 0\n"
"v_mov_b32 v14, 0\n"
"v_mov_b32 v15, 0\n"
"v_mov_b32 v16, 0\n"
"v_mov_b32 v17, 0\n"
"v_mov_b32 v18, 0\n"
"v_mov_b32 v19, 0\n"
"v_mov_b32 v20, 0\n"
"v_mov_b32 v21, 0\n"
"v_mov_b32 v22, 0\n"
"v_mov_b32 v23, 0\n"
"v_mov_b32 v24, 0\n"
"v_mov_b32 v25, 0\n"
"v_mov_b32 v26, 0\n"
"v_mov_b32 v27, 0\n"
"v_mov_b32 v28, 0\n"
"v_mov_b32 v29, 0\n"
"v_mov_b32 v30, 0\n"
"v_mov_b32 v31, 0\n"
"v_mov_b32 v156, 0xff7fffff\n"
"v_mov_b32 v159, 0\n"
"v_mov_b32 v32, 0\n"
"v_mov_b32 v33, 0\n"
"v_mov_b32 v34, 0\n"
"v_mov_b32 v35, 0\n"
"v_mov_b32 v36, 0\n"
"v_mov_b32 v37, 0\n"
"v_mov_b32 v38, 0\n"
"v_mov_b32 v39, 0\n"
"v_mov_b32 v40, 0\n"
"v_mov_b32 v41, 0\n"
"v_mov_b32 v42, 0\n"
"v_mov_b32 v43, 0\n"
"v_mov_b32 v44, 0\n"
"v_mov_b32 v45, 0\n"
"v_mov_b32 v46, 0\n"
"v_mov_b32 v47, 0\n"
"v_mov_b32 v48, 0\n"
"v_mov_b32 v49, 0\n"
"v_mov_b32 v50, 0\n"
"v_mov_b32 v51, 0\n"
"v_mov_b32 v52, 0\n"
"v_mov_b32 v53, 0\n"
"v_mov_b32 v54, 0\n"
"v_mov_b32 v55, 0\n"
"v_mov_b32 v56, 0\n"
"v_mov_b32 v57, 0\n"
"v_mov_b32 v58, 0\n"
"v_mov_b32 v59, 0\n"
"v_mov_b32 v60, 0\n"
"v_mov_b32 v61, 0\n"
"v_mov_b32 v62, 0\n"
"v_mov_b32 v63, 0\n"
"v_mov_b32 v160, 0xff7fffff\n"
"v_mov_b32 v163, 0\n"
"v_mov_b32 v64, 0\n"
"v_mov_b32 v65, 0\n"
"v_mov_b32 v66, 0\n"
"v_mov_b32 v67, 0\n"
"v_mov_b32 v68, 0\n"
"v_mov_b32 v69, 0\n"
"v_mov_b32 v70, 0\n"
"v_mov_b32 v71, 0\n"
"v_mov_b32 v72, 0\n"
"v_mov_b32 v73, 0\n"
"v_mov_b32 v74, 0\n"
"v_mov_b32 v75, 0\n"
"v_mov_b32 v76, 0\n"
"v_mov_b32 v77, 0\n"
"v_mov_b32 v78, 0\n"
"v_mov_b32 v79, 0\n"
"v_mov_b32 v80, 0\n"
"v_mov_b32 v81, 0\n"
"v_mov_b32 v82, 0\n"
"v_mov_b32 v83, 0\n"
"v_mov_b32 v84, 0\n"
"v_mov_b32 v85, 0\n"
"v_mov_b32 v86, 0\n"
"v_mov_b32 v87, 0\n"
"v_mov_b32 v88, 0\n"
"v_mov_b32 v89, 0\n"
"v_mov_b32 v90, 0\n"
"v_mov_b32 v91, 0\n"
"v_mov_b32 v92, 0\n"
"v_mov_b32 v93, 0\n"
"v_mov_b32 v94, 0\n"
"v_mov_b32 v95, 0\n"
"v_mov_b32 v164, 0xff7fffff\n"
"v_mov_b32 v167, 0\n"
"v_accvgpr_write_b32 a208, 0\n"
"v_accvgpr_write_b32 a209, 0\n"
"v_accvgpr_write_b32 a210, 0\n"
"v_accvgpr_write_b32 a211, 0\n"
"v_accvgpr_write_b32 a212, 0\n"
"v_accvgpr_write_b32 a213, 0\n"
"v_accvgpr_write_b32 a214, 0\n"
"v_accvgpr_write_b32 a215, 0\n"
"v_accvgpr_write_b32 a216, 0\n"
"v_accvgpr_write_b32 a217, 0\n"
"v_accvgpr_write_b32 a218, 0\n"
"v_accvgpr_write_b32 a219, 0\n"
"v_accvgpr_write_b32 a220, 0\n"
"v_accvgpr_write_b32 a221, 0\n"
"v_accvgpr_write_b32 a222, 0\n"
"v_accvgpr_write_b32 a223, 0\n"
"v_accvgpr_write_b32 a224, 0\n"
"v_accvgpr_write_b32 a225, 0\n"
"v_accvgpr_write_b32 a226, 0\n"
"v_accvgpr_write_b32 a227, 0\n"
"v_accvgpr_write_b32 a228, 0\n"
"v_accvgpr_write_b32 a229, 0\n"
"v_accvgpr_write_b32 a230, 0\n"
"v_accvgpr_write_b32 a231, 0\n"
"v_accvgpr_write_b32 a232, 0\n"
"v_accvgpr_write_b32 a233, 0\n"
"v_accvgpr_write_b32 a234, 0\n"
"v_accvgpr_write_b32 a235, 0\n"
"v_accvgpr_write_b32 a236, 0\n"
"v_accvgpr_write_b32 a237, 0\n"
"v_accvgpr_write_b32 a238, 0\n"
"v_accvgpr_write_b32 a239, 0\n"
"s_lshl_b32 s80, s47, 16\n"
"s_sub_u32 s80, s80, 261248\n"
"s_add_u32 s81, s80, 4096\n"
"s_mov_b32 s82, 0\n"
"LSLOOP_%=:\n"
"s_lshl_b32 s83, s82, 6\n"
"s_and_b32 s83, s83, 64\n"
"buffer_load_dwordx4 v[128:131], v144, s[84:87], s80 offen offset:0\n"
"buffer_load_dwordx4 v[132:135], v145, s[84:87], s80 offen offset:0\n"
"buffer_load_dwordx4 v[136:139], v144, s[84:87], s80 offen offset:128\n"
"buffer_load_dwordx4 v[140:143], v145, s[84:87], s80 offen offset:128\n"
"s_waitcnt vmcnt(0)\n"
"v_mfma_f32_16x16x32_bf16 v[96:99], v[128:131], a[0:3], 0\n"
"v_mfma_f32_16x16x32_bf16 v[96:99], v[132:135], a[4:7], v[96:99]\n"
"v_mfma_f32_16x16x32_bf16 v[96:99], v[136:139], a[8:11], v[96:99]\n"
"v_mfma_f32_16x16x32_bf16 v[96:99], v[140:143], a[12:15], v[96:99]\n"
"v_mfma_f32_16x16x32_bf16 v[104:107], v[128:131], a[16:19], 0\n"
"v_mfma_f32_16x16x32_bf16 v[104:107], v[132:135], a[20:23], v[104:107]\n"
"v_mfma_f32_16x16x32_bf16 v[104:107], v[136:139], a[24:27], v[104:107]\n"
"v_mfma_f32_16x16x32_bf16 v[104:107], v[140:143], a[28:31], v[104:107]\n"
"v_mfma_f32_16x16x32_bf16 v[112:115], v[128:131], a[32:35], 0\n"
"v_mfma_f32_16x16x32_bf16 v[112:115], v[132:135], a[36:39], v[112:115]\n"
"v_mfma_f32_16x16x32_bf16 v[112:115], v[136:139], a[40:43], v[112:115]\n"
"v_mfma_f32_16x16x32_bf16 v[112:115], v[140:143], a[44:47], v[112:115]\n"
"v_mfma_f32_16x16x32_bf16 v[120:123], v[128:131], a[192:195], 0\n"
"v_mfma_f32_16x16x32_bf16 v[120:123], v[132:135], a[196:199], v[120:123]\n"
"v_mfma_f32_16x16x32_bf16 v[120:123], v[136:139], a[200:203], v[120:123]\n"
"v_mfma_f32_16x16x32_bf16 v[120:123], v[140:143], a[204:207], v[120:123]\n"
"s_nop 7\n"
"buffer_load_dwordx4 v[128:131], v144, s[84:87], s81 offen offset:0\n"
"buffer_load_dwordx4 v[132:135], v145, s[84:87], s81 offen offset:0\n"
"buffer_load_dwordx4 v[136:139], v144, s[84:87], s81 offen offset:128\n"
"buffer_load_dwordx4 v[140:143], v145, s[84:87], s81 offen offset:128\n"
"s_waitcnt vmcnt(0)\n"
"v_mfma_f32_16x16x32_bf16 v[100:103], v[128:131], a[0:3], 0\n"
"v_mfma_f32_16x16x32_bf16 v[100:103], v[132:135], a[4:7], v[100:103]\n"
"v_mfma_f32_16x16x32_bf16 v[100:103], v[136:139], a[8:11], v[100:103]\n"
"v_mfma_f32_16x16x32_bf16 v[100:103], v[140:143], a[12:15], v[100:103]\n"
"v_mfma_f32_16x16x32_bf16 v[108:111], v[128:131], a[16:19], 0\n"
"v_mfma_f32_16x16x32_bf16 v[108:111], v[132:135], a[20:23], v[108:111]\n"
"v_mfma_f32_16x16x32_bf16 v[108:111], v[136:139], a[24:27], v[108:111]\n"
"v_mfma_f32_16x16x32_bf16 v[108:111], v[140:143], a[28:31], v[108:111]\n"
"v_mfma_f32_16x16x32_bf16 v[116:119], v[128:131], a[32:35], 0\n"
"v_mfma_f32_16x16x32_bf16 v[116:119], v[132:135], a[36:39], v[116:119]\n"
"v_mfma_f32_16x16x32_bf16 v[116:119], v[136:139], a[40:43], v[116:119]\n"
"v_mfma_f32_16x16x32_bf16 v[116:119], v[140:143], a[44:47], v[116:119]\n"
"v_mfma_f32_16x16x32_bf16 v[124:127], v[128:131], a[192:195], 0\n"
"v_mfma_f32_16x16x32_bf16 v[124:127], v[132:135], a[196:199], v[124:127]\n"
"v_mfma_f32_16x16x32_bf16 v[124:127], v[136:139], a[200:203], v[124:127]\n"
"v_mfma_f32_16x16x32_bf16 v[124:127], v[140:143], a[204:207], v[124:127]\n"
"s_nop 7\n"
"v_max3_f32 v153, v96, v97, v98\n"
"v_max3_f32 v153, v153, v99, v100\n"
"v_max3_f32 v153, v153, v101, v102\n"
"v_max3_f32 v153, v153, v103, v152\n"
"v_mov_b32 v154, v153\n"
"s_nop 1\n"
"v_permlane32_swap_b32 v153, v154\n"
"v_max_f32 v153, v153, v154\n"
"v_mov_b32 v154, v153\n"
"s_nop 1\n"
"v_permlane16_swap_b32 v153, v154\n"
"v_max_f32 v153, v153, v154\n"
"v_sub_f32 v154, v152, v153\n"
"v_exp_f32 v154, v154\n"
"v_mov_b32 v152, v153\n"
"v_sub_f32 v96, v96, v152\n"
"v_sub_f32 v97, v97, v152\n"
"v_sub_f32 v98, v98, v152\n"
"v_sub_f32 v99, v99, v152\n"
"v_sub_f32 v100, v100, v152\n"
"v_sub_f32 v101, v101, v152\n"
"v_sub_f32 v102, v102, v152\n"
"v_sub_f32 v103, v103, v152\n"
"v_exp_f32 v96, v96\n"
"v_exp_f32 v97, v97\n"
"v_exp_f32 v98, v98\n"
"v_exp_f32 v99, v99\n"
"v_exp_f32 v100, v100\n"
"v_exp_f32 v101, v101\n"
"v_exp_f32 v102, v102\n"
"v_exp_f32 v103, v103\n"
"v_mul_f32 v155, v155, v154\n"
"v_add_f32 v155, v155, v96\n"
"v_add_f32 v155, v155, v97\n"
"v_add_f32 v155, v155, v98\n"
"v_add_f32 v155, v155, v99\n"
"v_add_f32 v155, v155, v100\n"
"v_add_f32 v155, v155, v101\n"
"v_add_f32 v155, v155, v102\n"
"v_add_f32 v155, v155, v103\n"
"v_cvt_pk_bf16_f32 v96, v96, v97\n"
"v_cvt_pk_bf16_f32 v97, v98, v99\n"
"v_cvt_pk_bf16_f32 v98, v100, v101\n"
"v_cvt_pk_bf16_f32 v99, v102, v103\n"
"v_mov_b32_dpp v154, v154 row_ror:12 row_mask:0x2 bank_mask:0xf\n"
"s_nop 1\n"
"v_mov_b32_dpp v154, v154 row_ror:8 row_mask:0x4 bank_mask:0xf\n"
"s_nop 1\n"
"v_mov_b32_dpp v154, v154 row_ror:4 row_mask:0x8 bank_mask:0xf\n"
"s_nop 1\n"
"v_mul_f32_dpp v0, v154, v0 row_newbcast:0 row_mask:0xf bank_mask:0xf\n"
"v_mul_f32_dpp v1, v154, v1 row_newbcast:1 row_mask:0xf bank_mask:0xf\n"
"v_mul_f32_dpp v2, v154, v2 row_newbcast:2 row_mask:0xf bank_mask:0xf\n"
"v_mul_f32_dpp v3, v154, v3 row_newbcast:3 row_mask:0xf bank_mask:0xf\n"
"v_mul_f32_dpp v4, v154, v4 row_newbcast:0 row_mask:0xf bank_mask:0xf\n"
"v_mul_f32_dpp v5, v154, v5 row_newbcast:1 row_mask:0xf bank_mask:0xf\n"
"v_mul_f32_dpp v6, v154, v6 row_newbcast:2 row_mask:0xf bank_mask:0xf\n"
"v_mul_f32_dpp v7, v154, v7 row_newbcast:3 row_mask:0xf bank_mask:0xf\n"
"v_mul_f32_dpp v8, v154, v8 row_newbcast:0 row_mask:0xf bank_mask:0xf\n"
"v_mul_f32_dpp v9, v154, v9 row_newbcast:1 row_mask:0xf bank_mask:0xf\n"
"v_mul_f32_dpp v10, v154, v10 row_newbcast:2 row_mask:0xf bank_mask:0xf\n"
"v_mul_f32_dpp v11, v154, v11 row_newbcast:3 row_mask:0xf bank_mask:0xf\n"
"v_mul_f32_dpp v12, v154, v12 row_newbcast:0 row_mask:0xf bank_mask:0xf\n"
"v_mul_f32_dpp v13, v154, v13 row_newbcast:1 row_mask:0xf bank_mask:0xf\n"
"v_mul_f32_dpp v14, v154, v14 row_newbcast:2 row_mask:0xf bank_mask:0xf\n"
"v_mul_f32_dpp v15, v154, v15 row_newbcast:3 row_mask:0xf bank_mask:0xf\n"
"v_mul_f32_dpp v16, v154, v16 row_newbcast:0 row_mask:0xf bank_mask:0xf\n"
"v_mul_f32_dpp v17, v154, v17 row_newbcast:1 row_mask:0xf bank_mask:0xf\n"
"v_mul_f32_dpp v18, v154, v18 row_newbcast:2 row_mask:0xf bank_mask:0xf\n"
"v_mul_f32_dpp v19, v154, v19 row_newbcast:3 row_mask:0xf bank_mask:0xf\n"
"v_mul_f32_dpp v20, v154, v20 row_newbcast:0 row_mask:0xf bank_mask:0xf\n"
"v_mul_f32_dpp v21, v154, v21 row_newbcast:1 row_mask:0xf bank_mask:0xf\n"
"v_mul_f32_dpp v22, v154, v22 row_newbcast:2 row_mask:0xf bank_mask:0xf\n"
"v_mul_f32_dpp v23, v154, v23 row_newbcast:3 row_mask:0xf bank_mask:0xf\n"
"v_mul_f32_dpp v24, v154, v24 row_newbcast:0 row_mask:0xf bank_mask:0xf\n"
"v_mul_f32_dpp v25, v154, v25 row_newbcast:1 row_mask:0xf bank_mask:0xf\n"
"v_mul_f32_dpp v26, v154, v26 row_newbcast:2 row_mask:0xf bank_mask:0xf\n"
"v_mul_f32_dpp v27, v154, v27 row_newbcast:3 row_mask:0xf bank_mask:0xf\n"
"v_mul_f32_dpp v28, v154, v28 row_newbcast:0 row_mask:0xf bank_mask:0xf\n"
"v_mul_f32_dpp v29, v154, v29 row_newbcast:1 row_mask:0xf bank_mask:0xf\n"
"v_mul_f32_dpp v30, v154, v30 row_newbcast:2 row_mask:0xf bank_mask:0xf\n"
"v_mul_f32_dpp v31, v154, v31 row_newbcast:3 row_mask:0xf bank_mask:0xf\n"
"v_max3_f32 v157, v104, v105, v106\n"
"v_max3_f32 v157, v157, v107, v108\n"
"v_max3_f32 v157, v157, v109, v110\n"
"v_max3_f32 v157, v157, v111, v156\n"
"v_mov_b32 v158, v157\n"
"s_nop 1\n"
"v_permlane32_swap_b32 v157, v158\n"
"v_max_f32 v157, v157, v158\n"
"v_mov_b32 v158, v157\n"
"s_nop 1\n"
"v_permlane16_swap_b32 v157, v158\n"
"v_max_f32 v157, v157, v158\n"
"v_sub_f32 v158, v156, v157\n"
"v_exp_f32 v158, v158\n"
"v_mov_b32 v156, v157\n"
"v_sub_f32 v104, v104, v156\n"
"v_sub_f32 v105, v105, v156\n"
"v_sub_f32 v106, v106, v156\n"
"v_sub_f32 v107, v107, v156\n"
"v_sub_f32 v108, v108, v156\n"
"v_sub_f32 v109, v109, v156\n"
"v_sub_f32 v110, v110, v156\n"
"v_sub_f32 v111, v111, v156\n"
"v_exp_f32 v104, v104\n"
"v_exp_f32 v105, v105\n"
"v_exp_f32 v106, v106\n"
"v_exp_f32 v107, v107\n"
"v_exp_f32 v108, v108\n"
"v_exp_f32 v109, v109\n"
"v_exp_f32 v110, v110\n"
"v_exp_f32 v111, v111\n"
"v_mul_f32 v159, v159, v158\n"
"v_add_f32 v159, v159, v104\n"
"v_add_f32 v159, v159, v105\n"
"v_add_f32 v159, v159, v106\n"
"v_add_f32 v159, v159, v107\n"
"v_add_f32 v159, v159, v108\n"
"v_add_f32 v159, v159, v109\n"
"v_add_f32 v159, v159, v110\n"
"v_add_f32 v159, v159, v111\n"
"v_cvt_pk_bf16_f32 v104, v104, v105\n"
"v_cvt_pk_bf16_f32 v105, v106, v107\n"
"v_cvt_pk_bf16_f32 v106, v108, v109\n"
"v_cvt_pk_bf16_f32 v107, v110, v111\n"
"v_mov_b32_dpp v158, v158 row_ror:12 row_mask:0x2 bank_mask:0xf\n"
"s_nop 1\n"
"v_mov_b32_dpp v158, v158 row_ror:8 row_mask:0x4 bank_mask:0xf\n"
"s_nop 1\n"
"v_mov_b32_dpp v158, v158 row_ror:4 row_mask:0x8 bank_mask:0xf\n"
"s_nop 1\n"
"v_mul_f32_dpp v32, v158, v32 row_newbcast:0 row_mask:0xf bank_mask:0xf\n"
"v_mul_f32_dpp v33, v158, v33 row_newbcast:1 row_mask:0xf bank_mask:0xf\n"
"v_mul_f32_dpp v34, v158, v34 row_newbcast:2 row_mask:0xf bank_mask:0xf\n"
"v_mul_f32_dpp v35, v158, v35 row_newbcast:3 row_mask:0xf bank_mask:0xf\n"
"v_mul_f32_dpp v36, v158, v36 row_newbcast:0 row_mask:0xf bank_mask:0xf\n"
"v_mul_f32_dpp v37, v158, v37 row_newbcast:1 row_mask:0xf bank_mask:0xf\n"
"v_mul_f32_dpp v38, v158, v38 row_newbcast:2 row_mask:0xf bank_mask:0xf\n"
"v_mul_f32_dpp v39, v158, v39 row_newbcast:3 row_mask:0xf bank_mask:0xf\n"
"v_mul_f32_dpp v40, v158, v40 row_newbcast:0 row_mask:0xf bank_mask:0xf\n"
"v_mul_f32_dpp v41, v158, v41 row_newbcast:1 row_mask:0xf bank_mask:0xf\n"
"v_mul_f32_dpp v42, v158, v42 row_newbcast:2 row_mask:0xf bank_mask:0xf\n"
"v_mul_f32_dpp v43, v158, v43 row_newbcast:3 row_mask:0xf bank_mask:0xf\n"
"v_mul_f32_dpp v44, v158, v44 row_newbcast:0 row_mask:0xf bank_mask:0xf\n"
"v_mul_f32_dpp v45, v158, v45 row_newbcast:1 row_mask:0xf bank_mask:0xf\n"
"v_mul_f32_dpp v46, v158, v46 row_newbcast:2 row_mask:0xf bank_mask:0xf\n"
"v_mul_f32_dpp v47, v158, v47 row_newbcast:3 row_mask:0xf bank_mask:0xf\n"
"v_mul_f32_dpp v48, v158, v48 row_newbcast:0 row_mask:0xf bank_mask:0xf\n"
"v_mul_f32_dpp v49, v158, v49 row_newbcast:1 row_mask:0xf bank_mask:0xf\n"
"v_mul_f32_dpp v50, v158, v50 row_newbcast:2 row_mask:0xf bank_mask:0xf\n"
"v_mul_f32_dpp v51, v158, v51 row_newbcast:3 row_mask:0xf bank_mask:0xf\n"
"v_mul_f32_dpp v52, v158, v52 row_newbcast:0 row_mask:0xf bank_mask:0xf\n"
"v_mul_f32_dpp v53, v158, v53 row_newbcast:1 row_mask:0xf bank_mask:0xf\n"
"v_mul_f32_dpp v54, v158, v54 row_newbcast:2 row_mask:0xf bank_mask:0xf\n"
"v_mul_f32_dpp v55, v158, v55 row_newbcast:3 row_mask:0xf bank_mask:0xf\n"
"v_mul_f32_dpp v56, v158, v56 row_newbcast:0 row_mask:0xf bank_mask:0xf\n"
"v_mul_f32_dpp v57, v158, v57 row_newbcast:1 row_mask:0xf bank_mask:0xf\n"
"v_mul_f32_dpp v58, v158, v58 row_newbcast:2 row_mask:0xf bank_mask:0xf\n"
"v_mul_f32_dpp v59, v158, v59 row_newbcast:3 row_mask:0xf bank_mask:0xf\n"
"v_mul_f32_dpp v60, v158, v60 row_newbcast:0 row_mask:0xf bank_mask:0xf\n"
"v_mul_f32_dpp v61, v158, v61 row_newbcast:1 row_mask:0xf bank_mask:0xf\n"
"v_mul_f32_dpp v62, v158, v62 row_newbcast:2 row_mask:0xf bank_mask:0xf\n"
"v_mul_f32_dpp v63, v158, v63 row_newbcast:3 row_mask:0xf bank_mask:0xf\n"
"v_max3_f32 v161, v112, v113, v114\n"
"v_max3_f32 v161, v161, v115, v116\n"
"v_max3_f32 v161, v161, v117, v118\n"
"v_max3_f32 v161, v161, v119, v160\n"
"v_mov_b32 v162, v161\n"
"s_nop 1\n"
"v_permlane32_swap_b32 v161, v162\n"
"v_max_f32 v161, v161, v162\n"
"v_mov_b32 v162, v161\n"
"s_nop 1\n"
"v_permlane16_swap_b32 v161, v162\n"
"v_max_f32 v161, v161, v162\n"
"v_sub_f32 v162, v160, v161\n"
"v_exp_f32 v162, v162\n"
"v_mov_b32 v160, v161\n"
"v_sub_f32 v112, v112, v160\n"
"v_sub_f32 v113, v113, v160\n"
"v_sub_f32 v114, v114, v160\n"
"v_sub_f32 v115, v115, v160\n"
"v_sub_f32 v116, v116, v160\n"
"v_sub_f32 v117, v117, v160\n"
"v_sub_f32 v118, v118, v160\n"
"v_sub_f32 v119, v119, v160\n"
"v_exp_f32 v112, v112\n"
"v_exp_f32 v113, v113\n"
"v_exp_f32 v114, v114\n"
"v_exp_f32 v115, v115\n"
"v_exp_f32 v116, v116\n"
"v_exp_f32 v117, v117\n"
"v_exp_f32 v118, v118\n"
"v_exp_f32 v119, v119\n"
"v_mul_f32 v163, v163, v162\n"
"v_add_f32 v163, v163, v112\n"
"v_add_f32 v163, v163, v113\n"
"v_add_f32 v163, v163, v114\n"
"v_add_f32 v163, v163, v115\n"
"v_add_f32 v163, v163, v116\n"
"v_add_f32 v163, v163, v117\n"
"v_add_f32 v163, v163, v118\n"
"v_add_f32 v163, v163, v119\n"
"v_cvt_pk_bf16_f32 v112, v112, v113\n"
"v_cvt_pk_bf16_f32 v113, v114, v115\n"
"v_cvt_pk_bf16_f32 v114, v116, v117\n"
"v_cvt_pk_bf16_f32 v115, v118, v119\n"
"v_mov_b32_dpp v162, v162 row_ror:12 row_mask:0x2 bank_mask:0xf\n"
"s_nop 1\n"
"v_mov_b32_dpp v162, v162 row_ror:8 row_mask:0x4 bank_mask:0xf\n"
"s_nop 1\n"
"v_mov_b32_dpp v162, v162 row_ror:4 row_mask:0x8 bank_mask:0xf\n"
"s_nop 1\n"
"v_mul_f32_dpp v64, v162, v64 row_newbcast:0 row_mask:0xf bank_mask:0xf\n"
"v_mul_f32_dpp v65, v162, v65 row_newbcast:1 row_mask:0xf bank_mask:0xf\n"
"v_mul_f32_dpp v66, v162, v66 row_newbcast:2 row_mask:0xf bank_mask:0xf\n"
"v_mul_f32_dpp v67, v162, v67 row_newbcast:3 row_mask:0xf bank_mask:0xf\n"
"v_mul_f32_dpp v68, v162, v68 row_newbcast:0 row_mask:0xf bank_mask:0xf\n"
"v_mul_f32_dpp v69, v162, v69 row_newbcast:1 row_mask:0xf bank_mask:0xf\n"
"v_mul_f32_dpp v70, v162, v70 row_newbcast:2 row_mask:0xf bank_mask:0xf\n"
"v_mul_f32_dpp v71, v162, v71 row_newbcast:3 row_mask:0xf bank_mask:0xf\n"
"v_mul_f32_dpp v72, v162, v72 row_newbcast:0 row_mask:0xf bank_mask:0xf\n"
"v_mul_f32_dpp v73, v162, v73 row_newbcast:1 row_mask:0xf bank_mask:0xf\n"
"v_mul_f32_dpp v74, v162, v74 row_newbcast:2 row_mask:0xf bank_mask:0xf\n"
"v_mul_f32_dpp v75, v162, v75 row_newbcast:3 row_mask:0xf bank_mask:0xf\n"
"v_mul_f32_dpp v76, v162, v76 row_newbcast:0 row_mask:0xf bank_mask:0xf\n"
"v_mul_f32_dpp v77, v162, v77 row_newbcast:1 row_mask:0xf bank_mask:0xf\n"
"v_mul_f32_dpp v78, v162, v78 row_newbcast:2 row_mask:0xf bank_mask:0xf\n"
"v_mul_f32_dpp v79, v162, v79 row_newbcast:3 row_mask:0xf bank_mask:0xf\n"
"v_mul_f32_dpp v80, v162, v80 row_newbcast:0 row_mask:0xf bank_mask:0xf\n"
"v_mul_f32_dpp v81, v162, v81 row_newbcast:1 row_mask:0xf bank_mask:0xf\n"
"v_mul_f32_dpp v82, v162, v82 row_newbcast:2 row_mask:0xf bank_mask:0xf\n"
"v_mul_f32_dpp v83, v162, v83 row_newbcast:3 row_mask:0xf bank_mask:0xf\n"
"v_mul_f32_dpp v84, v162, v84 row_newbcast:0 row_mask:0xf bank_mask:0xf\n"
"v_mul_f32_dpp v85, v162, v85 row_newbcast:1 row_mask:0xf bank_mask:0xf\n"
"v_mul_f32_dpp v86, v162, v86 row_newbcast:2 row_mask:0xf bank_mask:0xf\n"
"v_mul_f32_dpp v87, v162, v87 row_newbcast:3 row_mask:0xf bank_mask:0xf\n"
"v_mul_f32_dpp v88, v162, v88 row_newbcast:0 row_mask:0xf bank_mask:0xf\n"
"v_mul_f32_dpp v89, v162, v89 row_newbcast:1 row_mask:0xf bank_mask:0xf\n"
"v_mul_f32_dpp v90, v162, v90 row_newbcast:2 row_mask:0xf bank_mask:0xf\n"
"v_mul_f32_dpp v91, v162, v91 row_newbcast:3 row_mask:0xf bank_mask:0xf\n"
"v_mul_f32_dpp v92, v162, v92 row_newbcast:0 row_mask:0xf bank_mask:0xf\n"
"v_mul_f32_dpp v93, v162, v93 row_newbcast:1 row_mask:0xf bank_mask:0xf\n"
"v_mul_f32_dpp v94, v162, v94 row_newbcast:2 row_mask:0xf bank_mask:0xf\n"
"v_mul_f32_dpp v95, v162, v95 row_newbcast:3 row_mask:0xf bank_mask:0xf\n"
"s_cmp_lg_u32 s83, s43\n"
"s_cbranch_scc1 LSK3A_%=\n"
"v_max3_f32 v165, v120, v121, v122\n"
"v_max3_f32 v165, v165, v123, v124\n"
"v_max3_f32 v165, v165, v125, v126\n"
"v_max3_f32 v165, v165, v127, v164\n"
"v_mov_b32 v166, v165\n"
"s_nop 1\n"
"v_permlane32_swap_b32 v165, v166\n"
"v_max_f32 v165, v165, v166\n"
"v_mov_b32 v166, v165\n"
"s_nop 1\n"
"v_permlane16_swap_b32 v165, v166\n"
"v_max_f32 v165, v165, v166\n"
"v_sub_f32 v166, v164, v165\n"
"v_exp_f32 v166, v166\n"
"v_mov_b32 v164, v165\n"
"v_sub_f32 v120, v120, v164\n"
"v_sub_f32 v121, v121, v164\n"
"v_sub_f32 v122, v122, v164\n"
"v_sub_f32 v123, v123, v164\n"
"v_sub_f32 v124, v124, v164\n"
"v_sub_f32 v125, v125, v164\n"
"v_sub_f32 v126, v126, v164\n"
"v_sub_f32 v127, v127, v164\n"
"v_exp_f32 v120, v120\n"
"v_exp_f32 v121, v121\n"
"v_exp_f32 v122, v122\n"
"v_exp_f32 v123, v123\n"
"v_exp_f32 v124, v124\n"
"v_exp_f32 v125, v125\n"
"v_exp_f32 v126, v126\n"
"v_exp_f32 v127, v127\n"
"v_mul_f32 v167, v167, v166\n"
"v_add_f32 v167, v167, v120\n"
"v_add_f32 v167, v167, v121\n"
"v_add_f32 v167, v167, v122\n"
"v_add_f32 v167, v167, v123\n"
"v_add_f32 v167, v167, v124\n"
"v_add_f32 v167, v167, v125\n"
"v_add_f32 v167, v167, v126\n"
"v_add_f32 v167, v167, v127\n"
"v_cvt_pk_bf16_f32 v120, v120, v121\n"
"v_cvt_pk_bf16_f32 v121, v122, v123\n"
"v_cvt_pk_bf16_f32 v122, v124, v125\n"
"v_cvt_pk_bf16_f32 v123, v126, v127\n"
"v_mov_b32_dpp v166, v166 row_ror:12 row_mask:0x2 bank_mask:0xf\n"
"s_nop 1\n"
"v_mov_b32_dpp v166, v166 row_ror:8 row_mask:0x4 bank_mask:0xf\n"
"s_nop 1\n"
"v_mov_b32_dpp v166, v166 row_ror:4 row_mask:0x8 bank_mask:0xf\n"
"v_accvgpr_read_b32 v124, a208\n"
"s_nop 1\n"
"v_mul_f32_dpp v124, v166, v124 row_newbcast:0 row_mask:0xf bank_mask:0xf\n"
"v_accvgpr_write_b32 a208, v124\n"
"v_accvgpr_read_b32 v125, a209\n"
"s_nop 1\n"
"v_mul_f32_dpp v125, v166, v125 row_newbcast:1 row_mask:0xf bank_mask:0xf\n"
"v_accvgpr_write_b32 a209, v125\n"
"v_accvgpr_read_b32 v126, a210\n"
"s_nop 1\n"
"v_mul_f32_dpp v126, v166, v126 row_newbcast:2 row_mask:0xf bank_mask:0xf\n"
"v_accvgpr_write_b32 a210, v126\n"
"v_accvgpr_read_b32 v127, a211\n"
"s_nop 1\n"
"v_mul_f32_dpp v127, v166, v127 row_newbcast:3 row_mask:0xf bank_mask:0xf\n"
"v_accvgpr_write_b32 a211, v127\n"
"v_accvgpr_read_b32 v124, a212\n"
"s_nop 1\n"
"v_mul_f32_dpp v124, v166, v124 row_newbcast:0 row_mask:0xf bank_mask:0xf\n"
"v_accvgpr_write_b32 a212, v124\n"
"v_accvgpr_read_b32 v125, a213\n"
"s_nop 1\n"
"v_mul_f32_dpp v125, v166, v125 row_newbcast:1 row_mask:0xf bank_mask:0xf\n"
"v_accvgpr_write_b32 a213, v125\n"
"v_accvgpr_read_b32 v126, a214\n"
"s_nop 1\n"
"v_mul_f32_dpp v126, v166, v126 row_newbcast:2 row_mask:0xf bank_mask:0xf\n"
"v_accvgpr_write_b32 a214, v126\n"
"v_accvgpr_read_b32 v127, a215\n"
"s_nop 1\n"
"v_mul_f32_dpp v127, v166, v127 row_newbcast:3 row_mask:0xf bank_mask:0xf\n"
"v_accvgpr_write_b32 a215, v127\n"
"v_accvgpr_read_b32 v124, a216\n"
"s_nop 1\n"
"v_mul_f32_dpp v124, v166, v124 row_newbcast:0 row_mask:0xf bank_mask:0xf\n"
"v_accvgpr_write_b32 a216, v124\n"
"v_accvgpr_read_b32 v125, a217\n"
"s_nop 1\n"
"v_mul_f32_dpp v125, v166, v125 row_newbcast:1 row_mask:0xf bank_mask:0xf\n"
"v_accvgpr_write_b32 a217, v125\n"
"v_accvgpr_read_b32 v126, a218\n"
"s_nop 1\n"
"v_mul_f32_dpp v126, v166, v126 row_newbcast:2 row_mask:0xf bank_mask:0xf\n"
"v_accvgpr_write_b32 a218, v126\n"
"v_accvgpr_read_b32 v127, a219\n"
"s_nop 1\n"
"v_mul_f32_dpp v127, v166, v127 row_newbcast:3 row_mask:0xf bank_mask:0xf\n"
"v_accvgpr_write_b32 a219, v127\n"
"v_accvgpr_read_b32 v124, a220\n"
"s_nop 1\n"
"v_mul_f32_dpp v124, v166, v124 row_newbcast:0 row_mask:0xf bank_mask:0xf\n"
"v_accvgpr_write_b32 a220, v124\n"
"v_accvgpr_read_b32 v125, a221\n"
"s_nop 1\n"
"v_mul_f32_dpp v125, v166, v125 row_newbcast:1 row_mask:0xf bank_mask:0xf\n"
"v_accvgpr_write_b32 a221, v125\n"
"v_accvgpr_read_b32 v126, a222\n"
"s_nop 1\n"
"v_mul_f32_dpp v126, v166, v126 row_newbcast:2 row_mask:0xf bank_mask:0xf\n"
"v_accvgpr_write_b32 a222, v126\n"
"v_accvgpr_read_b32 v127, a223\n"
"s_nop 1\n"
"v_mul_f32_dpp v127, v166, v127 row_newbcast:3 row_mask:0xf bank_mask:0xf\n"
"v_accvgpr_write_b32 a223, v127\n"
"v_accvgpr_read_b32 v124, a224\n"
"s_nop 1\n"
"v_mul_f32_dpp v124, v166, v124 row_newbcast:0 row_mask:0xf bank_mask:0xf\n"
"v_accvgpr_write_b32 a224, v124\n"
"v_accvgpr_read_b32 v125, a225\n"
"s_nop 1\n"
"v_mul_f32_dpp v125, v166, v125 row_newbcast:1 row_mask:0xf bank_mask:0xf\n"
"v_accvgpr_write_b32 a225, v125\n"
"v_accvgpr_read_b32 v126, a226\n"
"s_nop 1\n"
"v_mul_f32_dpp v126, v166, v126 row_newbcast:2 row_mask:0xf bank_mask:0xf\n"
"v_accvgpr_write_b32 a226, v126\n"
"v_accvgpr_read_b32 v127, a227\n"
"s_nop 1\n"
"v_mul_f32_dpp v127, v166, v127 row_newbcast:3 row_mask:0xf bank_mask:0xf\n"
"v_accvgpr_write_b32 a227, v127\n"
"v_accvgpr_read_b32 v124, a228\n"
"s_nop 1\n"
"v_mul_f32_dpp v124, v166, v124 row_newbcast:0 row_mask:0xf bank_mask:0xf\n"
"v_accvgpr_write_b32 a228, v124\n"
"v_accvgpr_read_b32 v125, a229\n"
"s_nop 1\n"
"v_mul_f32_dpp v125, v166, v125 row_newbcast:1 row_mask:0xf bank_mask:0xf\n"
"v_accvgpr_write_b32 a229, v125\n"
"v_accvgpr_read_b32 v126, a230\n"
"s_nop 1\n"
"v_mul_f32_dpp v126, v166, v126 row_newbcast:2 row_mask:0xf bank_mask:0xf\n"
"v_accvgpr_write_b32 a230, v126\n"
"v_accvgpr_read_b32 v127, a231\n"
"s_nop 1\n"
"v_mul_f32_dpp v127, v166, v127 row_newbcast:3 row_mask:0xf bank_mask:0xf\n"
"v_accvgpr_write_b32 a231, v127\n"
"v_accvgpr_read_b32 v124, a232\n"
"s_nop 1\n"
"v_mul_f32_dpp v124, v166, v124 row_newbcast:0 row_mask:0xf bank_mask:0xf\n"
"v_accvgpr_write_b32 a232, v124\n"
"v_accvgpr_read_b32 v125, a233\n"
"s_nop 1\n"
"v_mul_f32_dpp v125, v166, v125 row_newbcast:1 row_mask:0xf bank_mask:0xf\n"
"v_accvgpr_write_b32 a233, v125\n"
"v_accvgpr_read_b32 v126, a234\n"
"s_nop 1\n"
"v_mul_f32_dpp v126, v166, v126 row_newbcast:2 row_mask:0xf bank_mask:0xf\n"
"v_accvgpr_write_b32 a234, v126\n"
"v_accvgpr_read_b32 v127, a235\n"
"s_nop 1\n"
"v_mul_f32_dpp v127, v166, v127 row_newbcast:3 row_mask:0xf bank_mask:0xf\n"
"v_accvgpr_write_b32 a235, v127\n"
"v_accvgpr_read_b32 v124, a236\n"
"s_nop 1\n"
"v_mul_f32_dpp v124, v166, v124 row_newbcast:0 row_mask:0xf bank_mask:0xf\n"
"v_accvgpr_write_b32 a236, v124\n"
"v_accvgpr_read_b32 v125, a237\n"
"s_nop 1\n"
"v_mul_f32_dpp v125, v166, v125 row_newbcast:1 row_mask:0xf bank_mask:0xf\n"
"v_accvgpr_write_b32 a237, v125\n"
"v_accvgpr_read_b32 v126, a238\n"
"s_nop 1\n"
"v_mul_f32_dpp v126, v166, v126 row_newbcast:2 row_mask:0xf bank_mask:0xf\n"
"v_accvgpr_write_b32 a238, v126\n"
"v_accvgpr_read_b32 v127, a239\n"
"s_nop 1\n"
"v_mul_f32_dpp v127, v166, v127 row_newbcast:3 row_mask:0xf bank_mask:0xf\n"
"v_accvgpr_write_b32 a239, v127\n"
"LSK3A_%=:\n"
"s_nop 3\n"
"buffer_load_ushort v128, v146, s[88:91], s80 offen offset:0\n"
"buffer_load_ushort v129, v146, s[88:91], s80 offen offset:256\n"
"buffer_load_ushort v130, v146, s[88:91], s80 offen offset:512\n"
"buffer_load_ushort v131, v146, s[88:91], s80 offen offset:768\n"
"buffer_load_ushort v132, v146, s[88:91], s81 offen offset:0\n"
"buffer_load_ushort v133, v146, s[88:91], s81 offen offset:256\n"
"buffer_load_ushort v134, v146, s[88:91], s81 offen offset:512\n"
"buffer_load_ushort v135, v146, s[88:91], s81 offen offset:768\n"
"buffer_load_ushort v136, v146, s[88:91], s80 offen offset:32\n"
"buffer_load_ushort v137, v146, s[88:91], s80 offen offset:288\n"
"buffer_load_ushort v138, v146, s[88:91], s80 offen offset:544\n"
"buffer_load_ushort v139, v146, s[88:91], s80 offen offset:800\n"
"buffer_load_ushort v140, v146, s[88:91], s81 offen offset:32\n"
"buffer_load_ushort v141, v146, s[88:91], s81 offen offset:288\n"
"buffer_load_ushort v142, v146, s[88:91], s81 offen offset:544\n"
"buffer_load_ushort v143, v146, s[88:91], s81 offen offset:800\n"
"s_waitcnt vmcnt(0)\n"
"v_lshl_or_b32 v128, v129, 16, v128\n"
"v_lshl_or_b32 v129, v131, 16, v130\n"
"v_lshl_or_b32 v130, v133, 16, v132\n"
"v_lshl_or_b32 v131, v135, 16, v134\n"
"v_lshl_or_b32 v136, v137, 16, v136\n"
"v_lshl_or_b32 v137, v139, 16, v138\n"
"v_lshl_or_b32 v138, v141, 16, v140\n"
"v_lshl_or_b32 v139, v143, 16, v142\n"
"v_mfma_f32_16x16x32_bf16 v[0:3], v[96:99], v[128:131], v[0:3]\n"
"s_nop 0\n"
"v_mfma_f32_16x16x32_bf16 v[4:7], v[96:99], v[136:139], v[4:7]\n"
"v_mfma_f32_16x16x32_bf16 v[32:35], v[104:107], v[128:131], v[32:35]\n"
"v_mfma_f32_16x16x32_bf16 v[36:39], v[104:107], v[136:139], v[36:39]\n"
"v_mfma_f32_16x16x32_bf16 v[64:67], v[112:115], v[128:131], v[64:67]\n"
"v_mfma_f32_16x16x32_bf16 v[68:71], v[112:115], v[136:139], v[68:71]\n"
"s_cmp_lg_u32 s83, s43\n"
"s_cbranch_scc1 LSK3B0_%=\n"
"v_mfma_f32_16x16x32_bf16 a[208:211], v[120:123], v[128:131], a[208:211]\n"
"v_mfma_f32_16x16x32_bf16 a[212:215], v[120:123], v[136:139], a[212:215]\n"
"LSK3B0_%=:\n"
"s_nop 3\n"
"buffer_load_ushort v128, v147, s[88:91], s80 offen offset:0\n"
"buffer_load_ushort v129, v147, s[88:91], s80 offen offset:256\n"
"buffer_load_ushort v130, v147, s[88:91], s80 offen offset:512\n"
"buffer_load_ushort v131, v147, s[88:91], s80 offen offset:768\n"
"buffer_load_ushort v132, v147, s[88:91], s81 offen offset:0\n"
"buffer_load_ushort v133, v147, s[88:91], s81 offen offset:256\n"
"buffer_load_ushort v134, v147, s[88:91], s81 offen offset:512\n"
"buffer_load_ushort v135, v147, s[88:91], s81 offen offset:768\n"
"buffer_load_ushort v136, v147, s[88:91], s80 offen offset:32\n"
"buffer_load_ushort v137, v147, s[88:91], s80 offen offset:288\n"
"buffer_load_ushort v138, v147, s[88:91], s80 offen offset:544\n"
"buffer_load_ushort v139, v147, s[88:91], s80 offen offset:800\n"
"buffer_load_ushort v140, v147, s[88:91], s81 offen offset:32\n"
"buffer_load_ushort v141, v147, s[88:91], s81 offen offset:288\n"
"buffer_load_ushort v142, v147, s[88:91], s81 offen offset:544\n"
"buffer_load_ushort v143, v147, s[88:91], s81 offen offset:800\n"
"s_waitcnt vmcnt(0)\n"
"v_lshl_or_b32 v128, v129, 16, v128\n"
"v_lshl_or_b32 v129, v131, 16, v130\n"
"v_lshl_or_b32 v130, v133, 16, v132\n"
"v_lshl_or_b32 v131, v135, 16, v134\n"
"v_lshl_or_b32 v136, v137, 16, v136\n"
"v_lshl_or_b32 v137, v139, 16, v138\n"
"v_lshl_or_b32 v138, v141, 16, v140\n"
"v_lshl_or_b32 v139, v143, 16, v142\n"
"v_mfma_f32_16x16x32_bf16 v[8:11], v[96:99], v[128:131], v[8:11]\n"
"s_nop 0\n"
"v_mfma_f32_16x16x32_bf16 v[12:15], v[96:99], v[136:139], v[12:15]\n"
"v_mfma_f32_16x16x32_bf16 v[40:43], v[104:107], v[128:131], v[40:43]\n"
"v_mfma_f32_16x16x32_bf16 v[44:47], v[104:107], v[136:139], v[44:47]\n"
"v_mfma_f32_16x16x32_bf16 v[72:75], v[112:115], v[128:131], v[72:75]\n"
"v_mfma_f32_16x16x32_bf16 v[76:79], v[112:115], v[136:139], v[76:79]\n"
"s_cmp_lg_u32 s83, s43\n"
"s_cbranch_scc1 LSK3B1_%=\n"
"v_mfma_f32_16x16x32_bf16 a[216:219], v[120:123], v[128:131], a[216:219]\n"
"v_mfma_f32_16x16x32_bf16 a[220:223], v[120:123], v[136:139], a[220:223]\n"
"LSK3B1_%=:\n"
"s_nop 3\n"
"buffer_load_ushort v128, v146, s[88:91], s80 offen offset:128\n"
"buffer_load_ushort v129, v146, s[88:91], s80 offen offset:384\n"
"buffer_load_ushort v130, v146, s[88:91], s80 offen offset:640\n"
"buffer_load_ushort v131, v146, s[88:91], s80 offen offset:896\n"
"buffer_load_ushort v132, v146, s[88:91], s81 offen offset:128\n"
"buffer_load_ushort v133, v146, s[88:91], s81 offen offset:384\n"
"buffer_load_ushort v134, v146, s[88:91], s81 offen offset:640\n"
"buffer_load_ushort v135, v146, s[88:91], s81 offen offset:896\n"
"buffer_load_ushort v136, v146, s[88:91], s80 offen offset:160\n"
"buffer_load_ushort v137, v146, s[88:91], s80 offen offset:416\n"
"buffer_load_ushort v138, v146, s[88:91], s80 offen offset:672\n"
"buffer_load_ushort v139, v146, s[88:91], s80 offen offset:928\n"
"buffer_load_ushort v140, v146, s[88:91], s81 offen offset:160\n"
"buffer_load_ushort v141, v146, s[88:91], s81 offen offset:416\n"
"buffer_load_ushort v142, v146, s[88:91], s81 offen offset:672\n"
"buffer_load_ushort v143, v146, s[88:91], s81 offen offset:928\n"
"s_waitcnt vmcnt(0)\n"
"v_lshl_or_b32 v128, v129, 16, v128\n"
"v_lshl_or_b32 v129, v131, 16, v130\n"
"v_lshl_or_b32 v130, v133, 16, v132\n"
"v_lshl_or_b32 v131, v135, 16, v134\n"
"v_lshl_or_b32 v136, v137, 16, v136\n"
"v_lshl_or_b32 v137, v139, 16, v138\n"
"v_lshl_or_b32 v138, v141, 16, v140\n"
"v_lshl_or_b32 v139, v143, 16, v142\n"
"v_mfma_f32_16x16x32_bf16 v[16:19], v[96:99], v[128:131], v[16:19]\n"
"s_nop 0\n"
"v_mfma_f32_16x16x32_bf16 v[20:23], v[96:99], v[136:139], v[20:23]\n"
"v_mfma_f32_16x16x32_bf16 v[48:51], v[104:107], v[128:131], v[48:51]\n"
"v_mfma_f32_16x16x32_bf16 v[52:55], v[104:107], v[136:139], v[52:55]\n"
"v_mfma_f32_16x16x32_bf16 v[80:83], v[112:115], v[128:131], v[80:83]\n"
"v_mfma_f32_16x16x32_bf16 v[84:87], v[112:115], v[136:139], v[84:87]\n"
"s_cmp_lg_u32 s83, s43\n"
"s_cbranch_scc1 LSK3B2_%=\n"
"v_mfma_f32_16x16x32_bf16 a[224:227], v[120:123], v[128:131], a[224:227]\n"
"v_mfma_f32_16x16x32_bf16 a[228:231], v[120:123], v[136:139], a[228:231]\n"
"LSK3B2_%=:\n"
"s_nop 3\n"
"buffer_load_ushort v128, v147, s[88:91], s80 offen offset:128\n"
"buffer_load_ushort v129, v147, s[88:91], s80 offen offset:384\n"
"buffer_load_ushort v130, v147, s[88:91], s80 offen offset:640\n"
"buffer_load_ushort v131, v147, s[88:91], s80 offen offset:896\n"
"buffer_load_ushort v132, v147, s[88:91], s81 offen offset:128\n"
"buffer_load_ushort v133, v147, s[88:91], s81 offen offset:384\n"
"buffer_load_ushort v134, v147, s[88:91], s81 offen offset:640\n"
"buffer_load_ushort v135, v147, s[88:91], s81 offen offset:896\n"
"buffer_load_ushort v136, v147, s[88:91], s80 offen offset:160\n"
"buffer_load_ushort v137, v147, s[88:91], s80 offen offset:416\n"
"buffer_load_ushort v138, v147, s[88:91], s80 offen offset:672\n"
"buffer_load_ushort v139, v147, s[88:91], s80 offen offset:928\n"
"buffer_load_ushort v140, v147, s[88:91], s81 offen offset:160\n"
"buffer_load_ushort v141, v147, s[88:91], s81 offen offset:416\n"
"buffer_load_ushort v142, v147, s[88:91], s81 offen offset:672\n"
"buffer_load_ushort v143, v147, s[88:91], s81 offen offset:928\n"
"s_waitcnt vmcnt(0)\n"
"v_lshl_or_b32 v128, v129, 16, v128\n"
"v_lshl_or_b32 v129, v131, 16, v130\n"
"v_lshl_or_b32 v130, v133, 16, v132\n"
"v_lshl_or_b32 v131, v135, 16, v134\n"
"v_lshl_or_b32 v136, v137, 16, v136\n"
"v_lshl_or_b32 v137, v139, 16, v138\n"
"v_lshl_or_b32 v138, v141, 16, v140\n"
"v_lshl_or_b32 v139, v143, 16, v142\n"
"v_mfma_f32_16x16x32_bf16 v[24:27], v[96:99], v[128:131], v[24:27]\n"
"s_nop 0\n"
"v_mfma_f32_16x16x32_bf16 v[28:31], v[96:99], v[136:139], v[28:31]\n"
"v_mfma_f32_16x16x32_bf16 v[56:59], v[104:107], v[128:131], v[56:59]\n"
"v_mfma_f32_16x16x32_bf16 v[60:63], v[104:107], v[136:139], v[60:63]\n"
"v_mfma_f32_16x16x32_bf16 v[88:91], v[112:115], v[128:131], v[88:91]\n"
"v_mfma_f32_16x16x32_bf16 v[92:95], v[112:115], v[136:139], v[92:95]\n"
"s_cmp_lg_u32 s83, s43\n"
"s_cbranch_scc1 LSK3B3_%=\n"
"v_mfma_f32_16x16x32_bf16 a[232:235], v[120:123], v[128:131], a[232:235]\n"
"v_mfma_f32_16x16x32_bf16 a[236:239], v[120:123], v[136:139], a[236:239]\n"
"LSK3B3_%=:\n"
"s_nop 3\n"
"s_nop 9\n"
"s_add_u32 s80, s80, 8192\n"
"s_add_u32 s81, s81, 8192\n"
"s_add_u32 s82, s82, 1\n"
"s_cmp_lt_u32 s82, 32\n"
"s_cbranch_scc1 LSLOOP_%=\n"
"v_mov_b32 v154, v155\n"
"s_nop 1\n"
"v_permlane32_swap_b32 v155, v154\n"
"v_add_f32 v155, v155, v154\n"
"v_mov_b32 v154, v155\n"
"s_nop 1\n"
"v_permlane16_swap_b32 v155, v154\n"
"v_add_f32 v155, v155, v154\n"
"v_mov_b32 v196, v155\n"
"v_mov_b32 v158, v159\n"
"s_nop 1\n"
"v_permlane32_swap_b32 v159, v158\n"
"v_add_f32 v159, v159, v158\n"
"v_mov_b32 v158, v159\n"
"s_nop 1\n"
"v_permlane16_swap_b32 v159, v158\n"
"v_add_f32 v159, v159, v158\n"
"v_mov_b32 v198, v159\n"
"v_mov_b32 v162, v163\n"
"s_nop 1\n"
"v_permlane32_swap_b32 v163, v162\n"
"v_add_f32 v163, v163, v162\n"
"v_mov_b32 v162, v163\n"
"s_nop 1\n"
"v_permlane16_swap_b32 v163, v162\n"
"v_add_f32 v163, v163, v162\n"
"v_mov_b32 v200, v163\n"
"v_mov_b32 v166, v167\n"
"s_nop 1\n"
"v_permlane32_swap_b32 v167, v166\n"
"v_add_f32 v167, v167, v166\n"
"v_mov_b32 v166, v167\n"
"s_nop 1\n"
"v_permlane16_swap_b32 v167, v166\n"
"v_add_f32 v167, v167, v166\n"
"v_mov_b32 v202, v167\n"
"v_sub_f32 v192, 0, v164\n"
"v_mov_b32 v96, 0\n"
"v_mov_b32 v97, 0\n"
"v_mov_b32 v98, 0\n"
"v_mov_b32 v99, 0\n"
"v_mov_b32 v100, 0\n"
"v_mov_b32 v101, 0\n"
"v_mov_b32 v102, 0\n"
"v_mov_b32 v103, 0\n"
"v_mov_b32 v104, 0\n"
"v_mov_b32 v105, 0\n"
"v_mov_b32 v106, 0\n"
"v_mov_b32 v107, 0\n"
"v_mov_b32 v108, 0\n"
"v_mov_b32 v109, 0\n"
"v_mov_b32 v110, 0\n"
"v_mov_b32 v111, 0\n"
"v_mov_b32 v112, 0\n"
"v_mov_b32 v113, 0\n"
"v_mov_b32 v114, 0\n"
"v_mov_b32 v115, 0\n"
"v_mov_b32 v116, 0\n"
"v_mov_b32 v117, 0\n"
"v_mov_b32 v118, 0\n"
"v_mov_b32 v119, 0\n"
"v_mov_b32 v120, 0\n"
"v_mov_b32 v121, 0\n"
"v_mov_b32 v122, 0\n"
"v_mov_b32 v123, 0\n"
"v_mov_b32 v204, 0\n"
"v_mov_b32 v205, 0\n"
"s_nop 3\n"
"s_mov_b32 s68, 0\n"
"s_cmp_eq_u32 s47, 4\n"
"s_cbranch_scc1 LBACK0_%=\n"
"s_cmp_eq_u32 s47, 8\n"
"s_cbranch_scc1 LBACK1_%=\n"
"s_cmp_eq_u32 s47, 12\n"
"s_cbranch_scc1 LBACK2_%=\n"
"s_branch LBACK3_%=\n"
"LFIN_%=:\n"
    :: [kalo]"s"((unsigned)ka), [kahi]"s"((unsigned)(ka >> 32)), [wg]"s"(blockIdx.x), [wid]"s"(wid)
    : "v0","v1","v2","v3","v4","v5","v6","v7","v8","v9","v10","v11","v12","v13","v14","v15","v16","v17","v18","v19","v20","v21","v22","v23","v24","v25","v26","v27","v28","v29","v30","v31","v32","v33","v34","v35","v36","v37","v38","v39","v40","v41","v42","v43","v44","v45","v46","v47","v48","v49","v50","v51","v52","v53","v54","v55","v56","v57","v58","v59","v60","v61","v62","v63","v64","v65","v66","v67","v68","v69","v70","v71","v72","v73","v74","v75","v76","v77","v78","v79","v80","v81","v82","v83","v84","v85","v86","v87","v88","v89","v90","v91","v92","v93","v94","v95","v96","v97","v98","v99","v100","v101","v102","v103","v104","v105","v106","v107","v108","v109","v110","v111","v112","v113","v114","v115","v116","v117","v118","v119","v120","v121","v122","v123","v124","v125","v126","v127","v128","v129","v130","v131","v132","v133","v134","v135","v136","v137","v138","v139","v140","v141","v142","v143","v144","v145","v146","v147","v148","v149","v150","v151","v152","v153","v154","v155","v156","v157","v158","v159","v160","v161","v162","v163","v164","v165","v166","v167","v168","v169","v170","v171","v172","v173","v174","v175","v176","v177","v178","v179","v180","v181","v182","v183","v184","v185","v186","v187","v188","v189","v190","v191","v192","v193","v194","v195","v196","v197","v198","v199","v200","v201","v202","v203","v204","v205","v206","v207","v208","v209","v210","v211","v212","v213","v214","v215","v216","v217","v218","v219","v220","v221","v222","v223","v224","v225","v226","v227","v228","v229","v230","v231","v232","v233","v234","v235","v236","v237","v238","v239","v240","v241","v242","v243","v244","v245","v246","v247","v248","v249","v250","v251","v252","v253","v254","v255","a0","a1","a2","a3","a4","a5","a6","a7","a8","a9","a10","a11","a12","a13","a14","a15","a16","a17","a18","a19","a20","a21","a22","a23","a24","a25","a26","a27","a28","a29","a30","a31","a32","a33","a34","a35","a36","a37","a38","a39","a40","a41","a42","a43","a44","a45","a46","a47","a48","a49","a50","a51","a52","a53","a54","a55","a56","a57","a58","a59","a60","a61","a62","a63","a64","a65","a66","a67","a68","a69","a70","a71","a72","a73","a74","a75","a76","a77","a78","a79","a80","a81","a82","a83","a84","a85","a86","a87","a88","a89","a90","a91","a92","a93","a94","a95","a96","a97","a98","a99","a100","a101","a102","a103","a104","a105","a106","a107","a108","a109","a110","a111","a112","a113","a114","a115","a116","a117","a118","a119","a120","a121","a122","a123","a124","a125","a126","a127","a128","a129","a130","a131","a132","a133","a134","a135","a136","a137","a138","a139","a140","a141","a142","a143","a144","a145","a146","a147","a148","a149","a150","a151","a152","a153","a154","a155","a156","a157","a158","a159","a160","a161","a162","a163","a164","a165","a166","a167","a168","a169","a170","a171","a172","a173","a174","a175","a176","a177","a178","a179","a180","a181","a182","a183","a184","a185","a186","a187","a188","a189","a190","a191","a192","a193","a194","a195","a196","a197","a198","a199","a200","a201","a202","a203","a204","a205","a206","a207","a208","a209","a210","a211","a212","a213","a214","a215","a216","a217","a218","a219","a220","a221","a222","a223","a224","a225","a226","a227","a228","a229","a230","a231","a232","a233","a234","a235","a236","a237","a238","a239","a240","a241","a242","a243","a244","a245","a246","a247","a248","a249","a250","a251","a252","a253","a254","a255","s6","s7","s8","s9","s10","s11","s12","s13","s14","s15","s16","s17","s18","s19","s20","s21","s22","s23","s24","s25","s26","s27","s28","s29","s30","s31","s32","s33","s34","s35","s36","s37","s38","s39","s40","s41","s42","s43","s44","s45","s46","s47","s48","s49","s50","s51","s52","s53","s54","s55","s56","s57","s58","s59","s60","s61","s62","s63","s64","s65","s66","s67","s68","s69","s70","s71","s72","s73","s74","s75","s76","s77","s78","s79","s80","s81","s82","s83","s84","s85","s86","s87","s88","s89","s90","s91","s92","s93","s94","s95","s96","s97","s98","s99","s100","vcc","scc","m0","memory");
}
extern "C" void kernel_launch(void* const* d_in, const int* in_sizes, int n_in, void* d_out, int out_size, void* d_ws, size_t ws_size, hipStream_t stream) {
  char* xch = (char*)d_ws + ((ws_size - (size_t)270532608) & ~(size_t)0xfffff);
  (void)hipFuncSetAttribute((const void*)fused_attn, hipFuncAttributeMaxDynamicSharedMemorySize, 163840);
  hipLaunchKernelGGL(fused_attn, dim3(256), dim3(256), 163840, stream,
                     (const char*)d_in[0], (const char*)d_in[1], (const char*)d_in[2], (const char*)d_in[3], (const char*)d_in[4],
                     (const char*)d_in[5], (const char*)d_in[6], (const char*)d_in[7], (const char*)d_in[8], (const char*)d_in[9], xch, (char*)d_out, (char*)d_ws);
}
```

```cpp
#include <hip/hip_runtime.h>
#include <hip/hip_bf16.h>
#include <stdint.h>

__global__ __launch_bounds__(256, 1)
void fused_attn(const char* xA, const char* kA, const char* vA, const char* wA, const char* uA,
                const char* xB, const char* kB, const char* vB, const char* wB, const char* uB, char* xch, char* out, char* flags) {
  extern __shared__ __attribute__((aligned(16))) char lds[];
  const unsigned wid = __builtin_amdgcn_readfirstlane(threadIdx.x >> 6);
  const unsigned long long ka = (unsigned long long)__builtin_amdgcn_kernarg_segment_ptr();
  asm volatile(
"s_memtime s[64:65]\n"
"s_memrealtime s[66:67]\n"
"s_bfe_u32 s10, %[wg], 0x10002\n"
"s_mul_i32 s40, s10, 40\n"
"s_add_u32 s40, s40, %[kalo]\n"
"s_addc_u32 s41, %[kahi], 0\n"
"s_mov_b32 s78, %[kalo]\n"
"s_mov_b32 s79, %[kahi]\n"
"s_load_dwordx2 s[24:25], s[40:41], 0\n"
"s_load_dwordx2 s[28:29], s[40:41], 24\n"
"s_load_dwordx2 s[6:7], s[78:79], 0x60\n"
"s_load_dwordx2 s[16:17], s[40:41], 8\n"
"s_load_dwordx2 s[20:21], s[40:41], 16\n"
"s_load_dwordx2 s[8:9], s[40:41], 32\n"
"s_load_dwordx4 s[12:15], s[78:79], 0x50\n"
"s_mov_b32 s42, %[wid]\n"
"s_and_b32 s11, %[wg], 1\n"
"s_lshl_b32 s11, s11, 3\n"
"s_lshr_b32 s60, %[wg], 5\n"
"s_add_u32 s11, s11, s60\n"
"s_bfe_u32 s61, %[wg], 0x10001\n"
"s_bfe_u32 s60, %[wg], 0x20003\n"
"s_lshl2_add_u32 s61, s61, s60\n"
"s_getpc_b64 s[80:81]\n"
"LCP_%=:\n"
"s_mov_b32 s82, LEND_%=-LCP_%=\n"
"s_mov_b32 s83, 0x20000\n"
"v_mbcnt_lo_u32_b32 v2, -1, 0\n"
"v_lshlrev_b32 v2, 7, v2\n"
"s_lshr_b32 s60, %[wg], 3\n"
"s_lshl2_add_u32 s60, s60, s42\n"
"s_mul_i32 s60, s60, 640\n"
"s_mov_b64 exec, 31\n"
"buffer_load_dword v3, v2, s[80:83], s60 offen\n"
"s_mov_b64 exec, -1\n"
"s_mov_b32 s19, 0x20000\n"
"s_mov_b32 s23, 0x20000\n"
"s_mov_b32 s27, 0x20000\n"
"s_mov_b32 s31, 0x20000\n"
"s_mov_b32 s35, 0x20000\n"
"s_mov_b32 s39, 0x20000\n"
"s_mov_b32 s26, 286720\n"
"s_lshl_b32 s44, s42, 12\n"
"s_mov_b32 s50, s44\n"
"s_mul_i32 s51, s42, 7168\n"
"s_add_u32 s51, s51, 0x10000\n"
"s_mov_b32 s74, 0x8000\n"
"s_mov_b32 s58, 524288\n"
"s_mov_b32 s47, 0\n"
"s_add_u32 s60, s11, s61\n"
"s_and_b32 s60, s60, 0\n"
"s_mul_i32 s60, s60, 3\n"
"s_lshl_b32 s49, s60, 7\n"
"s_lshl_b32 s48, s60, 19\n"
"v_mbcnt_lo_u32_b32 v4, -1, 0\n"
"v_mbcnt_hi_u32_b32 v4, -1, v4\n"
"v_and_b32 v5, 15, v4\n"
"v_lshrrev_b32 v6, 4, v4\n"
"v_lshrrev_b32 v7, 3, v4\n"
"v_and_b32 v8, 7, v4\n"
"v_lshlrev_b32 v9, 1, v6\n"
"v_xor_b32 v9, v9, v8\n"
"v_lshlrev_b32 v9, 4, v9\n"
"s_mul_i32 s60, s42, 71680\n"
"v_mul_u32_u24 v10, 1280, v7\n"
"v_add3_u32 v248, v10, v9, s60\n"
"v_add_u32 v249, 9216, v248\n"
"v_add_u32 v254, 18432, v248\n"
"v_add_u32 v255, 27648, v248\n"
"v_lshl_add_u32 v240, v7, 8, v9\n"
"s_and_b32 s60, s42, 1\n"
"v_lshl_add_u32 v240, s60, 16, v240\n"
"s_lshr_b32 s60, s42, 1\n"
"v_lshl_add_u32 v240, s60, 13, v240\n"
"v_and_b32 v10, 1, v6\n"
"v_xor_b32 v10, v10, v8\n"
"v_lshlrev_b32 v10, 4, v10\n"
"v_lshl_add_u32 v10, v7, 13, v10\n"
"s_lshl_b32 s60, s42, 17\n"
"v_add_u32 v10, s60, v10\n"
"v_add_u32 v224, 1024, v10\n"
"v_add_u32 v225, 128, v10\n"
"v_xor_b32 v10, 64, v10\n"
"v_add_u32 v226, 64512, v10\n"
"v_add_u32 v227, 63616, v10\n"
"s_mul_i32 s40, s11, 286720\n"
"s_lshl_b32 s41, s61, 10\n"
"s_sub_u32 s30, 5243904, s41\n"
"s_sub_u32 s41, s41, 1024\n"
"s_ashr_i32 s60, s41, 31\n"
"s_lshl4_add_u32 s92, s10, s11\n"
"s_lshl_b32 s93, s92, 9\n"
"v_mov_b32 v0, 0\n"
"s_waitcnt lgkmcnt(0)\n"
"s_add_u32 s24, s24, s40\n"
"s_addc_u32 s25, s25, 0\n"
"s_and_b32 s25, s25, 0xffff\n"
"s_add_u32 s28, s28, s41\n"
"s_addc_u32 s29, s29, s60\n"
"s_and_b32 s29, s29, 0xffff\n"
"s_add_u32 s6, s6, s93\n"
"s_addc_u32 s7, s7, 0\n"
"buffer_load_dword v12, v248, s[24:27], s49 offen\n"
"buffer_load_dword v13, v224, s[28:31], s48 offen\n"
"global_load_dword v1, v0, s[6:7] offset:128 sc1\n"
"s_mov_b32 m0, s51\n"
"s_nop 0\n"
"buffer_load_dwordx4 v248, s[24:27], s49 offen lds\n"
"buffer_load_dwordx4 v249, s[24:27], s49 offen offset:1024 lds\n"
"buffer_load_dwordx4 v254, s[24:27], s49 offen offset:2048 lds\n"
"buffer_load_dwordx4 v255, s[24:27], s49 offen offset:3072 lds\n"
"s_add_u32 m0, s51, 4096\n"
"s_add_u32 s40, s49, 40960\n"
"buffer_load_dwordx4 v248, s[24:27], s40 offen lds\n"
"buffer_load_dwordx4 v249, s[24:27], s40 offen offset:1024 lds\n"
"buffer_load_dwordx4 v254, s[24:27], s40 offen offset:2048 lds\n"
"s_add_u32 s49, s49, 128\n"
"s_sub_u32 s41, s49, 1280\n"
"s_min_u32 s49, s49, s41\n"
"s_xor_b32 s51, s51, s74\n"
"s_mov_b32 m0, s50\n"
"s_nop 0\n"
"buffer_load_dwordx4 v224, s[28:31], s48 offen lds\n"
"buffer_load_dwordx4 v225, s[28:31], s48 offen offset:1024 lds\n"
"buffer_load_dwordx4 v226, s[28:31], s48 offen offset:2048 lds\n"
"buffer_load_dwordx4 v227, s[28:31], s48 offen offset:3072 lds\n"
"s_xor_b32 s50, s50, s74\n"
"s_lshl2_add_u32 s63, s11, s42\n"
"s_mul_i32 s63, s63, 10240\n"
"s_and_b32 s78, s42, 1\n"
"s_lshl_b32 s43, s78, 6\n"
"s_mov_b32 s77, 0x80000000\n"
"s_mov_b32 s75, 0x10000\n"
"s_mov_b32 s73, 0xffff0000\n"
"s_mov_b32 s46, 0x3e0293ee\n"
"s_mov_b32 s45, 0x63800000\n"
"s_mov_b32 s62, 9\n"
"v_lshl_add_u32 v242, v4, 4, s44\n"
"s_lshr_b32 s79, s42, 1\n"
"s_lshl4_add_u32 s76, s79, s78\n"
"s_lshl_b32 s76, s76, 10\n"
"s_add_u32 s76, s76, 0x20000\n"
"s_sub_u32 s76, s76, s44\n"
"v_lshlrev_b32 v9, 6, v6\n"
"v_xor_b32 v9, v9, v242\n"
"v_add_u32 v243, s76, v9\n"
"v_and_b32 v9, 6, v5\n"
"s_xor_b32 m0, s50, 0xc000\n"
"s_add_u32 s40, s48, 256\n"
"buffer_load_dwordx4 v224, s[28:31], s40 offen lds\n"
"v_xor_b32 v9, v9, v6\n"
"v_lshlrev_b32 v9, 4, v9\n"
"v_lshl_add_u32 v10, v5, 7, v9\n"
"s_mul_i32 s79, s42, 6144\n"
"s_add_u32 s79, s79, 0x10000\n"
"v_add_u32 v246, s79, v10\n"
"v_xor_b32 v247, 64, v246\n"
"s_lshr_b32 s79, s42, 1\n"
"s_lshl_b32 s79, s79, 11\n"
"buffer_load_dwordx4 v225, s[28:31], s40 offen offset:1024 lds\n"
"s_add_u32 s79, s79, 90112\n"
"v_add_u32 v252, s79, v10\n"
"v_xor_b32 v253, 64, v252\n"
"s_xor_b32 s79, s42, 1\n"
"s_mul_i32 s79, s79, 6144\n"
"s_add_u32 s79, s79, 0x10000\n"
"v_add_u32 v250, s79, v10\n"
"v_xor_b32 v251, 64, v250\n"
"s_mul_i32 s79, s78, 8256\n"
"buffer_load_dwordx4 v226, s[28:31], s40 offen offset:2048 lds\n"
"v_lshrrev_b32 v9, 5, v4\n"
"v_lshlrev_b32 v9, 11, v9\n"
"v_bfe_u32 v10, v4, 2, 3\n"
"v_lshl_add_u32 v9, v10, 7, v9\n"
"v_and_b32 v10, 3, v4\n"
"v_lshl_add_u32 v9, v10, 3, v9\n"
"v_bfe_u32 v10, v4, 3, 2\n"
"v_lshl_add_u32 v9, v10, 5, v9\n"
"s_add_u32 s79, s79, 0x10000\n"
"buffer_load_dwordx4 v227, s[28:31], s40 offen offset:3072 lds\n"
"s_cmp_eq_u32 s47, 8\n"
"s_cselect_b32 s41, s52, s58\n"
"s_add_u32 s48, s48, s41\n"
"s_sub_u32 s41, s48, 5242880\n"
"s_min_u32 s48, s48, s41\n"
"v_xor_b32 v232, s79, v9\n"
"v_xor_b32 v233, 32, v232\n"
"v_xor_b32 v234, 64, v232\n"
"v_xor_b32 v235, 96, v232\n"
"v_xor_b32 v236, 0x2000, v232\n"
"v_xor_b32 v237, 0x2000, v233\n"
"v_xor_b32 v238, 0x2000, v234\n"
"v_xor_b32 v239, 0x2000, v235\n"
"v_lshlrev_b32 v9, 11, v6\n"
"s_mov_b32 m0, s50\n"
"s_nop 0\n"
"buffer_load_dwordx4 v224, s[28:31], s48 offen lds\n"
"v_bfe_u32 v10, v4, 2, 2\n"
"v_lshl_add_u32 v9, v10, 7, v9\n"
"v_and_b32 v10, 1, v4\n"
"v_lshl_add_u32 v9, v10, 3, v9\n"
"v_bfe_u32 v10, v4, 1, 1\n"
"v_lshl_add_u32 v9, v10, 5, v9\n"
"v_bfe_u32 v10, v4, 3, 1\n"
"v_lshl_add_u32 v9, v10, 4, v9\n"
"v_bfe_u32 v10, v4, 4, 1\n"
"buffer_load_dwordx4 v225, s[28:31], s48 offen offset:1024 lds\n"
"v_lshl_add_u32 v9, v10, 6, v9\n"
"v_xor_b32 v244, s43, v9\n"
"v_xor_b32 v245, 16, v244\n"
"s_lshl3_add_u32 s79, s11, s61\n"
"s_lshl_b32 s79, s79, 20\n"
"s_sub_u32 s79, s79, 896\n"
"s_ashr_i32 s60, s79, 31\n"
"s_add_u32 s16, s16, s79\n"
"s_addc_u32 s17, s17, s60\n"
"buffer_load_dwordx4 v226, s[28:31], s48 offen offset:2048 lds\n"
"s_and_b32 s17, s17, 0xffff\n"
"s_mov_b32 s18, 1049472\n"
"s_mov_b64 s[84:85], s[16:17]\n"
"s_mov_b64 s[86:87], s[18:19]\n"
"s_add_u32 s20, s20, s79\n"
"s_addc_u32 s21, s21, s60\n"
"s_and_b32 s21, s21, 0xffff\n"
"s_mov_b32 s22, 1049472\n"
"s_mov_b64 s[88:89], s[20:21]\n"
"buffer_load_dwordx4 v227, s[28:31], s48 offen offset:3072 lds\n"
"s_xor_b32 s50, s50, s74\n"
"s_mov_b64 s[90:91], s[22:23]\n"
"s_mov_b32 s57, 106496\n"
"s_bfe_u32 s55, %[wg], 65539\n"
"s_mul_i32 s55, s55, 16384\n"
"s_xor_b32 s57, s57, s55\n"
"s_bfe_u32 s55, %[wg], 65541\n"
"s_mul_i32 s55, s55, 2048\n"
"s_xor_b32 s57, s57, s55\n"
"s_bfe_u32 s55, %[wg], 65542\n"
"s_mov_b32 m0, s51\n"
"s_nop 0\n"
"buffer_load_dwordx4 v248, s[24:27], s49 offen lds\n"
"s_mul_i32 s55, s55, 4096\n"
"s_xor_b32 s57, s57, s55\n"
"s_mov_b32 s55, 107392\n"
"s_xor_b32 s56, s55, s57\n"
"s_mov_b32 s59, 2048\n"
"s_mov_b32 s54, 29824\n"
"s_add_u32 s52, s11, s61\n"
"s_and_b32 s52, s52, 0\n"
"s_mul_i32 s52, s52, 3\n"
"buffer_load_dwordx4 v249, s[24:27], s49 offen offset:1024 lds\n"
"s_add_u32 s52, s52, 1\n"
"s_lshl_b32 s53, s52, 7\n"
"s_lshl_b32 s52, s52, 19\n"
"s_add_u32 s52, s52, 512\n"
"s_mul_i32 s79, s61, 655360\n"
"s_add_u32 s8, s8, s79\n"
"s_addc_u32 s9, s9, 0\n"
"s_and_b32 s9, s9, 0xffff\n"
"s_add_u32 s12, s12, 0x1fffff\n"
"s_addc_u32 s13, s13, 0\n"
"buffer_load_dwordx4 v254, s[24:27], s49 offen offset:2048 lds\n"
"s_and_b32 s12, s12, 0xffe00000\n"
"s_mul_i32 s96, s42, 3\n"
"s_and_b32 s96, s96, 5\n"
"s_cmp_eq_u32 s42, 3\n"
"s_cselect_b32 s96, 5, s96\n"
"s_lshl_b32 s79, s61, 8\n"
"s_add_u32 s96, s96, s79\n"
"s_add_u32 s96, s96, 160\n"
"buffer_load_dwordx4 v255, s[24:27], s49 offen offset:3072 lds\n"
"s_lshl_b32 s96, s96, 12\n"
"s_mul_i32 s79, s92, 8388608\n"
"s_add_u32 s32, s12, s79\n"
"s_addc_u32 s33, s13, 0\n"
"s_and_b32 s33, s33, 0xffff\n"
"s_mov_b32 s34, 8388608\n"
"s_mul_i32 s79, s11, 573440\n"
"s_mul_i32 s60, s10, 1280\n"
"s_sub_u32 s38, 573440, s60\n"
"s_add_u32 m0, s51, 4096\n"
"s_add_u32 s40, s49, 40960\n"
"buffer_load_dwordx4 v248, s[24:27], s40 offen lds\n"
"s_add_u32 s79, s79, s60\n"
"s_add_u32 s36, s14, s79\n"
"s_addc_u32 s37, s15, 0\n"
"s_and_b32 s37, s37, 0xffff\n"
"s_lshl2_add_u32 s93, s61, s42\n"
"s_add_u32 s95, s61, 1\n"
"s_and_b32 s95, s95, 7\n"
"s_lshl_b32 s95, s95, 7\n"
"s_add_u32 s100, s61, 2\n"
"buffer_load_dwordx4 v249, s[24:27], s40 offen offset:1024 lds\n"
"s_and_b32 s100, s100, 7\n"
"s_lshl_b32 s100, s100, 7\n"
"s_lshl_b32 s60, s61, 7\n"
"s_add_u32 s60, s60, s43\n"
"s_mul_i32 s97, s42, 122880\n"
"s_add_u32 s97, s97, s60\n"
"s_lshr_b32 s98, s42, 1\n"
"s_add_u32 s98, s98, 12\n"
"s_mul_i32 s98, s98, 40960\n"
"buffer_load_dwordx4 v254, s[24:27], s40 offen offset:2048 lds\n"
"s_add_u32 s49, s49, 128\n"
"s_sub_u32 s41, s49, 1280\n"
"s_min_u32 s49, s49, s41\n"
"s_xor_b32 s51, s51, s74\n"
"s_add_u32 s98, s98, s60\n"
"s_mul_i32 s60, s93, 37\n"
"s_lshr_b32 s60, s60, 8\n"
"s_mul_i32 s79, s60, 7\n"
"s_sub_u32 s79, s93, s79\n"
"s_lshl4_add_u32 s94, s60, s79\n"
"s_lshl_b32 s94, s94, 10\n"
"s_add_u32 s94, s94, 524288\n"
"s_mul_i32 s99, s60, 3\n"
"s_lshr_b32 s78, s79, 1\n"
"s_add_u32 s99, s99, s78\n"
"s_lshr_b32 s78, s60, 1\n"
"s_add_u32 s78, s78, 12\n"
"s_cmp_eq_u32 s79, 6\n"
"s_cselect_b32 s99, s78, s99\n"
"s_mul_i32 s99, s99, 40960\n"
"s_xor_b32 s79, s79, s60\n"
"s_and_b32 s79, s79, 1\n"
"s_lshl_b32 s79, s79, 6\n"
"s_add_u32 s99, s99, s79\n"
"s_add_u32 s99, s99, 1024\n"
"s_cmp_eq_u32 s61, 7\n"
"s_cselect_b32 s99, 0x40000000, s99\n"
"s_cselect_b32 s94, 0x40000000, s94\n"
"s_waitcnt vmcnt(27)\n"
"v_and_b32 v12, 0x7fff0000, v12\n"
"v_and_b32 v13, 0x7fff0000, v13\n"
"s_nop 0\n"
"v_max_u32_dpp v12, v12, v12 quad_perm:[1,0,3,2] row_mask:0xf bank_mask:0xf\n"
"v_max_u32_dpp v13, v13, v13 quad_perm:[1,0,3,2] row_mask:0xf bank_mask:0xf\n"
"s_nop 0\n"
"v_max_u32_dpp v12, v12, v12 quad_perm:[2,3,0,1] row_mask:0xf bank_mask:0xf\n"
"v_max_u32_dpp v13, v13, v13 quad_perm:[2,3,0,1] row_mask:0xf bank_mask:0xf\n"
"s_memtime s[80:81]\n"
"s_memrealtime s[82:83]\n"
"s_nop 0\n"
"v_max_u32_dpp v12, v12, v12 row_half_mirror row_mask:0xf bank_mask:0xf\n"
"v_max_u32_dpp v13, v13, v13 row_half_mirror row_mask:0xf bank_mask:0xf\n"
"s_nop 0\n"
"v_min_u32_dpp v12, v12, v12 row_mirror row_mask:0xf bank_mask:0xf\n"
"v_min_u32_dpp v13, v13, v13 row_mirror row_mask:0xf bank_mask:0xf\n"
"s_nop 0\n"
"v_min_u32_dpp v12, v12, v12 row_bcast:15 row_mask:0xa bank_mask:0xf\n"
"v_min_u32_dpp v13, v13, v13 row_bcast:15 row_mask:0xa bank_mask:0xf\n"
"s_nop 0\n"
"v_min_u32_dpp v12, v12, v12 row_bcast:31 row_mask:0xc bank_mask:0xf\n"
"v_min_u32_dpp v13, v13, v13 row_bcast:31 row_mask:0xc bank_mask:0xf\n"
"s_nop 0\n"
"v_mul_f32 v12, v12, v13\n"
"v_mul_f32 v12, 0x43c80000, v12\n"
"v_min_f32 v12, 0x71800000, v12\n"
"v_mul_f32 v13, s46, v12\n"
"s_nop 0\n"
"v_readlane_b32 s70, v12, 63\n"
"v_readlane_b32 s71, v12, 63\n"
"v_readlane_b32 s69, v13, 63\n"
"s_nop 1\n"
"v_mov_b64 v[228:229], s[70:71]\n"
"v_mov_b64 v[230:231], s[70:71]\n"
"s_waitcnt vmcnt(26)\n"
"v_readfirstlane_b32 s92, v1\n"
"v_mov_b64 v[0:1], s[70:71]\n"
"v_mov_b64 v[2:3], s[70:71]\n"
"s_mov_b32 s70, s46\n"
"s_xor_b32 s71, s69, s77\n"
"s_waitcnt lgkmcnt(0)\n"
"s_sub_u32 s80, s80, s64\n"
"s_sub_u32 s82, s82, s66\n"
"s_mul_i32 s80, s80, 10\n"
"s_mul_i32 s82, s82, 211\n"
"s_cmp_gt_u32 s80, s82\n"
"s_cselect_b32 s64, 1, 0\n"
"s_cselect_b32 s68, 655360, 0\n"
"s_cmp_eq_u32 s42, 0\n"
"s_cselect_b32 s64, s64, 0\n"
"s_mov_b32 s65, 0\n"
"s_add_u32 s66, s66, 2800\n"
"s_mov_b32 s67, s66\n"
"s_mov_b32 s72, 118\n"
"s_mov_b64 vcc, s[64:65]\n"
"s_sub_u32 s63, s63, s44\n"
"s_add_u32 s80, s8, s63\n"
"s_addc_u32 s81, s9, 0\n"
"s_add_u32 s82, s44, 704\n"
"s_cmp_lg_u32 s68, 0\n"
"s_cselect_b32 s82, s82, 0\n"
"s_mov_b32 s83, 0x20000\n"
"s_nop 2\n"
"s_mul_i32 s60, s92, 0x9e3779b1\n"
"s_add_u32 s60, s60, 0x7f4a7c15\n"
"s_waitcnt vmcnt(15)\n"
"s_barrier\n"
"ds_read_b128 a[136:139], v246 offset:0\n"
"ds_read_b128 a[140:143], v247 offset:0\n"
"ds_read_b64_tr_b16 a[192:193], v244 offset:0\n"
"ds_read_b64_tr_b16 a[194:195], v244 offset:512\n"
"ds_read_b64_tr_b16 a[196:197], v244 offset:8192\n"
"ds_read_b64_tr_b16 a[198:199], v244 offset:8704\n"
"ds_read_b64_tr_b16 a[200:201], v245 offset:0\n"
"ds_read_b64_tr_b16 a[202:203], v245 offset:512\n"
"ds_read_b64_tr_b16 a[204:205], v245 offset:8192\n"
"ds_read_b64_tr_b16 a[206:207], v245 offset:8704\n"
"ds_read_b64_tr_b16 a[208:209], v244 offset:1024\n"
"ds_read_b64_tr_b16 a[210:211], v244 offset:1536\n"
"ds_read_b64_tr_b16 a[212:213], v244 offset:9216\n"
"ds_read_b64_tr_b16 a[214:215], v244 offset:9728\n"
"s_waitcnt lgkmcnt(10)\n"
"v_mfma_f32_16x16x32_bf16 v[112:115], a[192:195], a[136:139], v[228:231]\n"
"ds_read_b64_tr_b16 a[216:217], v245 offset:1024\n"
"ds_read_b64_tr_b16 a[218:219], v245 offset:1536\n"
"s_waitcnt lgkmcnt(10)\n"
"v_mfma_f32_16x16x32_bf16 v[112:115], a[196:199], a[140:143], v[112:115]\n"
"ds_read_b64_tr_b16 a[220:221], v245 offset:9216\n"
"ds_read_b64_tr_b16 a[222:223], v245 offset:9728\n"
"s_waitcnt lgkmcnt(10)\n"
"v_mfma_f32_16x16x32_bf16 v[116:119], a[200:203], a[136:139], v[228:231]\n"
"s_waitcnt lgkmcnt(8)\n"
"ds_read_b128 a[144:147], v246 offset:2048\n"
"v_mfma_f32_16x16x32_bf16 v[116:119], a[204:207], a[140:143], v[116:119]\n"
"s_waitcnt lgkmcnt(7)\n"
"ds_read_b128 a[148:151], v247 offset:2048\n"
"v_mfma_f32_16x16x32_bf16 v[120:123], a[208:211], a[136:139], v[228:231]\n"
"s_waitcnt lgkmcnt(6)\n"
"ds_read_b128 a[152:155], v246 offset:4096\n"
"v_mfma_f32_16x16x32_bf16 v[120:123], a[212:215], a[140:143], v[120:123]\n"
"s_waitcnt lgkmcnt(5)\n"
"ds_read_b128 a[156:159], v247 offset:4096\n"
"v_mfma_f32_16x16x32_bf16 v[124:127], a[216:219], a[136:139], v[228:231]\n"
"s_waitcnt lgkmcnt(4)\n"
"ds_read_b128 a[160:163], v252 offset:0\n"
"v_mfma_f32_16x16x32_bf16 v[124:127], a[220:223], a[140:143], v[124:127]\n"
"s_waitcnt lgkmcnt(4)\n"
"ds_read_b128 a[164:167], v253 offset:0\n"
"v_mfma_f32_16x16x32_bf16 v[140:143], a[216:219], a[144:147], v[228:231]\n"
"s_waitcnt lgkmcnt(4)\n"
"ds_read_b128 a[168:171], v250 offset:0\n"
"v_mfma_f32_16x16x32_bf16 v[140:143], a[220:223], a[148:151], v[140:143]\n"
"ds_read_b128 a[172:175], v251 offset:0\n"
"v_mfma_f32_16x16x32_bf16 v[136:139], a[208:211], a[144:147], v[228:231]\n"
"s_waitcnt vmcnt(11)\n"
"ds_read_b128 a[176:179], v250 offset:2048\n"
"v_mfma_f32_16x16x32_bf16 v[136:139], a[212:215], a[148:151], v[136:139]\n"
"s_barrier\n"
"v_mfma_f32_16x16x32_bf16 v[132:135], a[200:203], a[144:147], v[228:231]\n"
"s_add_u32 s40, s48, 256\n"
"s_xor_b32 m0, s50, 0xc000\n"
"ds_read_b128 a[180:183], v251 offset:2048\n"
"v_mfma_f32_16x16x32_bf16 v[132:135], a[204:207], a[148:151], v[132:135]\n"
"buffer_load_dwordx4 v224, s[28:31], s40 offen lds\n"
"v_mfma_f32_16x16x32_bf16 v[128:131], a[192:195], a[144:147], v[228:231]\n"
"ds_read_b128 a[184:187], v250 offset:4096\n"
"v_mfma_f32_16x16x32_bf16 v[128:131], a[196:199], a[148:151], v[128:131]\n"
"s_waitcnt lgkmcnt(5)\n"
"ds_read_b128 a[188:191], v251 offset:4096\n"
"v_mfma_f32_16x16x32_bf16 v[144:147], a[192:195], a[152:155], v[228:231]\n"
"ds_read_b64_tr_b16 a[224:225], v244 offset:16384\n"
"v_mfma_f32_16x16x32_bf16 v[144:147], a[196:199], a[156:159], v[144:147]\n"
"ds_read_b64_tr_b16 a[226:227], v244 offset:16896\n"
"v_mfma_f32_16x16x32_bf16 v[148:151], a[200:203], a[152:155], v[228:231]\n"
"ds_read_b64_tr_b16 a[228:229], v244 offset:24576\n"
"v_mfma_f32_16x16x32_bf16 v[148:151], a[204:207], a[156:159], v[148:151]\n"
"buffer_load_dwordx4 v225, s[28:31], s40 offen offset:1024 lds\n"
"v_mfma_f32_16x16x32_bf16 v[152:155], a[208:211], a[152:155], v[228:231]\n"
"ds_read_b64_tr_b16 a[230:231], v244 offset:25088\n"
"v_mfma_f32_16x16x32_bf16 v[152:155], a[212:215], a[156:159], v[152:155]\n"
"ds_read_b64_tr_b16 a[232:233], v245 offset:16384\n"
"v_mfma_f32_16x16x32_bf16 v[156:159], a[216:219], a[152:155], v[228:231]\n"
"ds_read_b64_tr_b16 a[234:235], v245 offset:16896\n"
"v_mfma_f32_16x16x32_bf16 v[156:159], a[220:223], a[156:159], v[156:159]\n"
"ds_read_b64_tr_b16 a[236:237], v245 offset:24576\n"
"v_mfma_f32_16x16x32_bf16 v[172:175], a[216:219], a[160:163], v[228:231]\n"
"ds_read_b64_tr_b16 a[238:239], v245 offset:25088\n"
"v_mfma_f32_16x16x32_bf16 v[172:175], a[220:223], a[164:167], v[172:175]\n"
"buffer_load_dwordx4 v226, s[28:31], s40 offen offset:2048 lds\n"
"v_mfma_f32_16x16x32_bf16 v[168:171], a[208:211], a[160:163], v[228:231]\n"
"ds_read_b64_tr_b16 a[240:241], v244 offset:17408\n"
"v_mfma_f32_16x16x32_bf16 v[168:171], a[212:215], a[164:167], v[168:171]\n"
"ds_read_b64_tr_b16 a[242:243], v244 offset:17920\n"
"v_mfma_f32_16x16x32_bf16 v[164:167], a[200:203], a[160:163], v[228:231]\n"
"ds_read_b64_tr_b16 a[244:245], v244 offset:25600\n"
"v_mfma_f32_16x16x32_bf16 v[164:167], a[204:207], a[164:167], v[164:167]\n"
"buffer_load_dwordx4 v227, s[28:31], s40 offen offset:3072 lds\n"
"v_mfma_f32_16x16x32_bf16 v[160:163], a[192:195], a[160:163], v[228:231]\n"
"s_cmp_eq_u32 s47, 8\n"
"ds_read_b64_tr_b16 a[246:247], v244 offset:26112\n"
"v_xor_b32 v244, s74, v244\n"
"v_mfma_f32_16x16x32_bf16 v[160:163], a[196:199], a[164:167], v[160:163]\n"
"s_waitcnt lgkmcnt(5)\n"
"s_cselect_b32 s41, s52, s58\n"
"ds_read_b64_tr_b16 a[248:249], v245 offset:17408\n"
"v_xor_b32 v246, s74, v246\n"
"v_mfma_f32_16x16x32_bf16 v[176:179], a[192:195], a[168:171], v[228:231]\n"
"s_add_u32 s48, s48, s41\n"
"ds_read_b64_tr_b16 a[250:251], v245 offset:17920\n"
"v_xor_b32 v247, s74, v247\n"
"v_mfma_f32_16x16x32_bf16 v[176:179], a[196:199], a[172:175], v[176:179]\n"
"s_sub_u32 s41, s48, 5242880\n"
"ds_read_b64_tr_b16 a[252:253], v245 offset:25600\n"
"v_xor_b32 v252, s74, v252\n"
"v_mfma_f32_16x16x32_bf16 v[180:183], a[200:203], a[168:171], v[228:231]\n"
"s_min_u32 s48, s48, s41\n"
"ds_read_b64_tr_b16 a[254:255], v245 offset:26112\n"
"v_xor_b32 v245, s74, v245\n"
"v_mfma_f32_16x16x32_bf16 v[180:183], a[204:207], a[172:175], v[180:183]\n"
"v_xor_b32 v253, s74, v253\n"
"v_xor_b32 v250, s74, v250\n"
"v_mfma_f32_16x16x32_bf16 v[184:187], a[208:211], a[168:171], v[228:231]\n"
"s_mov_b32 m0, s50\n"
"v_xor_b32 v251, s74, v251\n"
"v_mfma_f32_16x16x32_bf16 v[184:187], a[212:215], a[172:175], v[184:187]\n"
"buffer_load_dwordx4 v224, s[28:31], s48 offen lds\n"
"v_mfma_f32_16x16x32_bf16 v[188:191], a[216:219], a[168:171], v[228:231]\n"
"v_mfma_f32_16x16x32_bf16 v[188:191], a[220:223], a[172:175], v[188:191]\n"
"v_mfma_f32_16x16x32_bf16 v[204:207], a[216:219], a[176:179], v[228:231]\n"
"v_mfma_f32_16x16x32_bf16 v[204:207], a[220:223], a[180:183], v[204:207]\n"
"buffer_load_dwordx4 v225, s[28:31], s48 offen offset:1024 lds\n"
"v_mfma_f32_16x16x32_bf16 v[200:203], a[208:211], a[176:179], v[228:231]\n"
"v_mfma_f32_16x16x32_bf16 v[200:203], a[212:215], a[180:183], v[200:203]\n"
"v_mfma_f32_16x16x32_bf16 v[196:199], a[200:203], a[176:179], v[228:231]\n"
"v_mfma_f32_16x16x32_bf16 v[196:199], a[204:207], a[180:183], v[196:199]\n"
"v_mfma_f32_16x16x32_bf16 v[192:195], a[192:195], a[176:179], v[228:231]\n"
"v_mfma_f32_16x16x32_bf16 v[192:195], a[196:199], a[180:183], v[192:195]\n"
"buffer_load_dwordx4 v226, s[28:31], s48 offen offset:2048 lds\n"
"v_mfma_f32_16x16x32_bf16 v[208:211], a[192:195], a[184:187], v[228:231]\n"
"v_mfma_f32_16x16x32_bf16 v[208:211], a[196:199], a[188:191], v[208:211]\n"
"v_mfma_f32_16x16x32_bf16 v[212:215], a[200:203], a[184:187], v[228:231]\n"
"v_mfma_f32_16x16x32_bf16 v[212:215], a[204:207], a[188:191], v[212:215]\n"
"v_mfma_f32_16x16x32_bf16 v[216:219], a[208:211], a[184:187], v[228:231]\n"
"v_mfma_f32_16x16x32_bf16 v[216:219], a[212:215], a[188:191], v[216:219]\n"
"buffer_load_dwordx4 v227, s[28:31], s48 offen offset:3072 lds\n"
"v_mfma_f32_16x16x32_bf16 v[220:223], a[216:219], a[184:187], v[228:231]\n"
"s_xor_b32 s50, s50, s74\n"
"v_mfma_f32_16x16x32_bf16 v[220:223], a[220:223], a[188:191], v[220:223]\n"
"v_mfma_f32_16x16x32_bf16 v[0:3], a[224:227], a[136:139], v[228:231]\n"
"v_mfma_f32_16x16x32_bf16 v[0:3], a[228:231], a[140:143], v[0:3]\n"
"v_mfma_f32_16x16x32_bf16 v[4:7], a[232:235], a[136:139], v[228:231]\n"
"s_waitcnt vmcnt(8) lgkmcnt(0)\n"
"v_mfma_f32_16x16x32_bf16 v[4:7], a[236:239], a[140:143], v[4:7]\n"
"s_barrier\n"
"v_mfma_f32_16x16x32_bf16 v[8:11], a[240:243], a[136:139], v[228:231]\n"
"s_mov_b32 m0, s51\n"
"ds_read_b64_tr_b16 a[192:193], v244 offset:0\n"
"v_mfma_f32_16x16x32_bf16 v[8:11], a[244:247], a[140:143], v[8:11]\n"
"buffer_load_dwordx4 v248, s[24:27], s49 offen lds\n"
"v_mfma_f32_16x16x32_bf16 v[12:15], a[248:251], a[136:139], v[228:231]\n"
"ds_read_b64_tr_b16 a[194:195], v244 offset:512\n"
"v_mfma_f32_16x16x32_bf16 v[12:15], a[252:255], a[140:143], v[12:15]\n"
"ds_read_b128 a[136:139], v246 offset:0\n"
"v_mfma_f32_16x16x32_bf16 v[28:31], a[248:251], a[144:147], v[228:231]\n"
"ds_read_b64_tr_b16 a[196:197], v244 offset:8192\n"
"v_mfma_f32_16x16x32_bf16 v[28:31], a[252:255], a[148:151], v[28:31]\n"
"ds_read_b64_tr_b16 a[198:199], v244 offset:8704\n"
"v_mfma_f32_16x16x32_bf16 v[24:27], a[240:243], a[144:147], v[228:231]\n"
"ds_read_b128 a[140:143], v247 offset:0\n"
"v_mfma_f32_16x16x32_bf16 v[24:27], a[244:247], a[148:151], v[24:27]\n"
"buffer_load_dwordx4 v249, s[24:27], s49 offen offset:1024 lds\n"
"v_mfma_f32_16x16x32_bf16 v[20:23], a[232:235], a[144:147], v[228:231]\n"
"ds_read_b64_tr_b16 a[200:201], v245 offset:0\n"
"v_mfma_f32_16x16x32_bf16 v[20:23], a[236:239], a[148:151], v[20:23]\n"
"ds_read_b64_tr_b16 a[202:203], v245 offset:512\n"
"v_mfma_f32_16x16x32_bf16 v[16:19], a[224:227], a[144:147], v[228:231]\n"
"ds_read_b64_tr_b16 a[204:205], v245 offset:8192\n"
"v_mfma_f32_16x16x32_bf16 v[16:19], a[228:231], a[148:151], v[16:19]\n"
"ds_read_b64_tr_b16 a[206:207], v245 offset:8704\n"
"v_mfma_f32_16x16x32_bf16 v[32:35], a[224:227], a[152:155], v[228:231]\n"
"ds_read_b64_tr_b16 a[208:209], v244 offset:1024\n"
"v_mfma_f32_16x16x32_bf16 v[32:35], a[228:231], a[156:159], v[32:35]\n"
"buffer_load_dwordx4 v254, s[24:27], s49 offen offset:2048 lds\n"
"v_mfma_f32_16x16x32_bf16 v[36:39], a[232:235], a[152:155], v[228:231]\n"
"ds_read_b64_tr_b16 a[210:211], v244 offset:1536\n"
"v_mfma_f32_16x16x32_bf16 v[36:39], a[236:239], a[156:159], v[36:39]\n"
"ds_read_b64_tr_b16 a[212:213], v244 offset:9216\n"
"v_mfma_f32_16x16x32_bf16 v[40:43], a[240:243], a[152:155], v[228:231]\n"
"ds_read_b64_tr_b16 a[214:215], v244 offset:9728\n"
"v_mfma_f32_16x16x32_bf16 v[40:43], a[244:247], a[156:159], v[40:43]\n"
"ds_read_b64_tr_b16 a[216:217], v245 offset:1024\n"
"v_mfma_f32_16x16x32_bf16 v[44:47], a[248:251], a[152:155], v[228:231]\n"
"ds_read_b64_tr_b16 a[218:219], v245 offset:1536\n"
"v_mfma_f32_16x16x32_bf16 v[44:47], a[252:255], a[156:159], v[44:47]\n"
"buffer_load_dwordx4 v255, s[24:27], s49 offen offset:3072 lds\n"
"v_mfma_f32_16x16x32_bf16 v[60:63], a[248:251], a[160:163], v[228:231]\n"
"ds_read_b64_tr_b16 a[220:221], v245 offset:9216\n"
"v_mfma_f32_16x16x32_bf16 v[60:63], a[252:255], a[164:167], v[60:63]\n"
"ds_read_b64_tr_b16 a[222:223], v245 offset:9728\n"
"v_mfma_f32_16x16x32_bf16 v[56:59], a[240:243], a[160:163], v[228:231]\n"
"ds_read_b128 a[144:147], v246 offset:2048\n"
"v_mfma_f32_16x16x32_bf16 v[56:59], a[244:247], a[164:167], v[56:59]\n"
"s_add_u32 s40, s49, 40960\n"
"ds_read_b128 a[148:151], v247 offset:2048\n"
"v_mfma_f32_16x16x32_bf16 v[52:55], a[232:235], a[160:163], v[228:231]\n"
"s_add_u32 m0, s51, 4096\n"
"ds_read_b128 a[152:155], v246 offset:4096\n"
"v_mfma_f32_16x16x32_bf16 v[52:55], a[236:239], a[164:167], v[52:55]\n"
"buffer_load_dwordx4 v248, s[24:27], s40 offen lds\n"
"v_mfma_f32_16x16x32_bf16 v[48:51], a[224:227], a[160:163], v[228:231]\n"
"ds_read_b128 a[156:159], v247 offset:4096\n"
"v_mfma_f32_16x16x32_bf16 v[48:51], a[228:231], a[164:167], v[48:51]\n"
"ds_read_b128 a[160:163], v252 offset:0\n"
"v_mfma_f32_16x16x32_bf16 v[64:67], a[224:227], a[168:171], v[228:231]\n"
"ds_read_b128 a[164:167], v253 offset:0\n"
"v_mfma_f32_16x16x32_bf16 v[64:67], a[228:231], a[172:175], v[64:67]\n"
"v_mfma_f32_16x16x32_bf16 v[68:71], a[232:235], a[168:171], v[228:231]\n"
"v_mfma_f32_16x16x32_bf16 v[68:71], a[236:239], a[172:175], v[68:71]\n"
"buffer_load_dwordx4 v249, s[24:27], s40 offen offset:1024 lds\n"
"v_mfma_f32_16x16x32_bf16 v[72:75], a[240:243], a[168:171], v[228:231]\n"
"v_mfma_f32_16x16x32_bf16 v[72:75], a[244:247], a[172:175], v[72:75]\n"
"v_mfma_f32_16x16x32_bf16 v[76:79], a[248:251], a[168:171], v[228:231]\n"
"ds_read_b128 a[168:171], v250 offset:0\n"
"v_mfma_f32_16x16x32_bf16 v[76:79], a[252:255], a[172:175], v[76:79]\n"
"ds_read_b128 a[172:175], v251 offset:0\n"
"v_mfma_f32_16x16x32_bf16 v[92:95], a[248:251], a[176:179], v[228:231]\n"
"v_mfma_f32_16x16x32_bf16 v[92:95], a[252:255], a[180:183], v[92:95]\n"
"buffer_load_dwordx4 v254, s[24:27], s40 offen offset:2048 lds\n"
"v_mfma_f32_16x16x32_bf16 v[88:91], a[240:243], a[176:179], v[228:231]\n"
"s_add_u32 s49, s49, 128\n"
"v_mfma_f32_16x16x32_bf16 v[88:91], a[244:247], a[180:183], v[88:91]\n"
"s_sub_u32 s41, s49, 1280\n"
"v_mfma_f32_16x16x32_bf16 v[84:87], a[232:235], a[176:179], v[228:231]\n"
"s_min_u32 s49, s49, s41\n"
"v_mfma_f32_16x16x32_bf16 v[84:87], a[236:239], a[180:183], v[84:87]\n"
"s_xor_b32 s51, s51, s74\n"
"v_mfma_f32_16x16x32_bf16 v[80:83], a[224:227], a[176:179], v[228:231]\n"
"ds_read_b128 a[176:179], v250 offset:2048\n"
"v_mfma_f32_16x16x32_bf16 v[80:83], a[228:231], a[180:183], v[80:83]\n"
"ds_read_b128 a[180:183], v251 offset:2048\n"
"v_mfma_f32_16x16x32_bf16 v[96:99], a[224:227], a[184:187], v[228:231]\n"
"v_mfma_f32_16x16x32_bf16 v[96:99], a[228:231], a[188:191], v[96:99]\n"
"v_mfma_f32_16x16x32_bf16 v[100:103], a[232:235], a[184:187], v[228:231]\n"
"v_mfma_f32_16x16x32_bf16 v[100:103], a[236:239], a[188:191], v[100:103]\n"
"v_mfma_f32_16x16x32_bf16 v[104:107], a[240:243], a[184:187], v[228:231]\n"
"v_mfma_f32_16x16x32_bf16 v[104:107], a[244:247], a[188:191], v[104:107]\n"
"v_mfma_f32_16x16x32_bf16 v[108:111], a[248:251], a[184:187], v[228:231]\n"
"s_add_u32 s47, s47, 1\n"
"ds_read_b128 a[184:187], v250 offset:4096\n"
"v_mfma_f32_16x16x32_bf16 v[108:111], a[252:255], a[188:191], v[108:111]\n"
"s_waitcnt lgkmcnt(1)\n"
"ds_read_b128 a[188:191], v251 offset:4096\n"
"LPE_%=:\n"
"s_cmp_lg_u32 s68, 0\n"
"s_cbranch_scc1 LPN_%=\n"
"LPL_%=:\n"
"v_mfma_f32_16x16x32_bf16 v[112:115], a[192:195], a[136:139], v[112:115]\n"
"v_mfma_f32_16x16x32_bf16 v[112:115], a[196:199], a[140:143], v[112:115]\n"
"v_mfma_f32_16x16x32_bf16 v[116:119], a[200:203], a[136:139], v[116:119]\n"
"s_waitcnt vmcnt(11)\n"
"v_mfma_f32_16x16x32_bf16 v[116:119], a[204:207], a[140:143], v[116:119]\n"
"s_barrier\n"
"v_mfma_f32_16x16x32_bf16 v[120:123], a[208:211], a[136:139], v[120:123]\n"
"s_add_u32 s40, s48, 256\n"
"s_xor_b32 m0, s50, 0xc000\n"
"ds_read_b64_tr_b16 a[224:225], v244 offset:16384\n"
"v_mfma_f32_16x16x32_bf16 v[120:123], a[212:215], a[140:143], v[120:123]\n"
"buffer_load_dwordx4 v224, s[28:31], s40 offen lds\n"
"v_mfma_f32_16x16x32_bf16 v[124:127], a[216:219], a[136:139], v[124:127]\n"
"ds_read_b64_tr_b16 a[226:227], v244 offset:16896\n"
"v_mfma_f32_16x16x32_bf16 v[124:127], a[220:223], a[140:143], v[124:127]\n"
"ds_read_b64_tr_b16 a[228:229], v244 offset:24576\n"
"v_mfma_f32_16x16x32_bf16 v[140:143], a[216:219], a[144:147], v[140:143]\n"
"ds_read_b64_tr_b16 a[230:231], v244 offset:25088\n"
"v_mfma_f32_16x16x32_bf16 v[140:143], a[220:223], a[148:151], v[140:143]\n"
"ds_read_b64_tr_b16 a[232:233], v245 offset:16384\n"
"v_mfma_f32_16x16x32_bf16 v[136:139], a[208:211], a[144:147], v[136:139]\n"
"ds_read_b64_tr_b16 a[234:235], v245 offset:16896\n"
"v_mfma_f32_16x16x32_bf16 v[136:139], a[212:215], a[148:151], v[136:139]\n"
"buffer_load_dwordx4 v225, s[28:31], s40 offen offset:1024 lds\n"
"v_mfma_f32_16x16x32_bf16 v[132:135], a[200:203], a[144:147], v[132:135]\n"
"ds_read_b64_tr_b16 a[236:237], v245 offset:24576\n"
"v_mfma_f32_16x16x32_bf16 v[132:135], a[204:207], a[148:151], v[132:135]\n"
"ds_read_b64_tr_b16 a[238:239], v245 offset:25088\n"
"v_mfma_f32_16x16x32_bf16 v[128:131], a[192:195], a[144:147], v[128:131]\n"
"ds_read_b64_tr_b16 a[240:241], v244 offset:17408\n"
"v_mfma_f32_16x16x32_bf16 v[128:131], a[196:199], a[148:151], v[128:131]\n"
"ds_read_b64_tr_b16 a[242:243], v244 offset:17920\n"
"v_mfma_f32_16x16x32_bf16 v[144:147], a[192:195], a[152:155], v[144:147]\n"
"ds_read_b64_tr_b16 a[244:245], v244 offset:25600\n"
"v_mfma_f32_16x16x32_bf16 v[144:147], a[196:199], a[156:159], v[144:147]\n"
"buffer_load_dwordx4 v226, s[28:31], s40 offen offset:2048 lds\n"
"v_mfma_f32_16x16x32_bf16 v[148:151], a[200:203], a[152:155], v[148:151]\n"
"ds_read_b64_tr_b16 a[246:247], v244 offset:26112\n"
"v_mfma_f32_16x16x32_bf16 v[148:151], a[204:207], a[156:159], v[148:151]\n"
"ds_read_b64_tr_b16 a[248:249], v245 offset:17408\n"
"v_mfma_f32_16x16x32_bf16 v[152:155], a[208:211], a[152:155], v[152:155]\n"
"ds_read_b64_tr_b16 a[250:251], v245 offset:17920\n"
"v_mfma_f32_16x16x32_bf16 v[152:155], a[212:215], a[156:159], v[152:155]\n"
"ds_read_b64_tr_b16 a[252:253], v245 offset:25600\n"
"v_mfma_f32_16x16x32_bf16 v[156:159], a[216:219], a[152:155], v[156:159]\n"
"ds_read_b64_tr_b16 a[254:255], v245 offset:26112\n"
"v_mfma_f32_16x16x32_bf16 v[156:159], a[220:223], a[156:159], v[156:159]\n"
"buffer_load_dwordx4 v227, s[28:31], s40 offen offset:3072 lds\n"
"v_mfma_f32_16x16x32_bf16 v[172:175], a[216:219], a[160:163], v[172:175]\n"
"s_cmp_eq_u32 s47, 8\n"
"v_mfma_f32_16x16x32_bf16 v[172:175], a[220:223], a[164:167], v[172:175]\n"
"s_cselect_b32 s41, s52, s58\n"
"v_mfma_f32_16x16x32_bf16 v[168:171], a[208:211], a[160:163], v[168:171]\n"
"s_add_u32 s48, s48, s41\n"
"v_mfma_f32_16x16x32_bf16 v[168:171], a[212:215], a[164:167], v[168:171]\n"
"s_sub_u32 s41, s48, 5242880\n"
"v_mfma_f32_16x16x32_bf16 v[164:167], a[200:203], a[160:163], v[164:167]\n"
"s_min_u32 s48, s48, s41\n"
"v_mfma_f32_16x16x32_bf16 v[164:167], a[204:207], a[164:167], v[164:167]\n"
"v_xor_b32 v244, s74, v244\n"
"v_xor_b32 v246, s74, v246\n"
"v_mfma_f32_16x16x32_bf16 v[160:163], a[192:195], a[160:163], v[160:163]\n"
"s_mov_b32 m0, s50\n"
"v_xor_b32 v245, s74, v245\n"
"v_xor_b32 v247, s74, v247\n"
"v_mfma_f32_16x16x32_bf16 v[160:163], a[196:199], a[164:167], v[160:163]\n"
"buffer_load_dwordx4 v224, s[28:31], s48 offen lds\n"
"v_mfma_f32_16x16x32_bf16 v[176:179], a[192:195], a[168:171], v[176:179]\n"
"v_xor_b32 v252, s74, v252\n"
"v_xor_b32 v253, s74, v253\n"
"v_mfma_f32_16x16x32_bf16 v[176:179], a[196:199], a[172:175], v[176:179]\n"
"v_xor_b32 v250, s74, v250\n"
"v_xor_b32 v251, s74, v251\n"
"v_mfma_f32_16x16x32_bf16 v[180:183], a[200:203], a[168:171], v[180:183]\n"
"v_mfma_f32_16x16x32_bf16 v[180:183], a[204:207], a[172:175], v[180:183]\n"
"buffer_load_dwordx4 v225, s[28:31], s48 offen offset:1024 lds\n"
"v_mfma_f32_16x16x32_bf16 v[184:187], a[208:211], a[168:171], v[184:187]\n"
"v_mfma_f32_16x16x32_bf16 v[184:187], a[212:215], a[172:175], v[184:187]\n"
"v_mfma_f32_16x16x32_bf16 v[188:191], a[216:219], a[168:171], v[188:191]\n"
"v_mfma_f32_16x16x32_bf16 v[188:191], a[220:223], a[172:175], v[188:191]\n"
"v_mfma_f32_16x16x32_bf16 v[204:207], a[216:219], a[176:179], v[204:207]\n"
"v_mfma_f32_16x16x32_bf16 v[204:207], a[220:223], a[180:183], v[204:207]\n"
"buffer_load_dwordx4 v226, s[28:31], s48 offen offset:2048 lds\n"
"v_mfma_f32_16x16x32_bf16 v[200:203], a[208:211], a[176:179], v[200:203]\n"
"v_mfma_f32_16x16x32_bf16 v[200:203], a[212:215], a[180:183], v[200:203]\n"
"v_mfma_f32_16x16x32_bf16 v[196:199], a[200:203], a[176:179], v[196:199]\n"
"v_mfma_f32_16x16x32_bf16 v[196:199], a[204:207], a[180:183], v[196:199]\n"
"v_mfma_f32_16x16x32_bf16 v[192:195], a[192:195], a[176:179], v[192:195]\n"
"v_mfma_f32_16x16x32_bf16 v[192:195], a[196:199], a[180:183], v[192:195]\n"
"s_waitcnt lgkmcnt(0)\n"
"buffer_load_dwordx4 v227, s[28:31], s48 offen offset:3072 lds\n"
"v_mfma_f32_16x16x32_bf16 v[208:211], a[192:195], a[184:187], v[208:211]\n"
"s_xor_b32 s50, s50, s74\n"
"v_mfma_f32_16x16x32_bf16 v[208:211], a[196:199], a[188:191], v[208:211]\n"
"v_mfma_f32_16x16x32_bf16 v[212:215], a[200:203], a[184:187], v[212:215]\n"
"v_mfma_f32_16x16x32_bf16 v[212:215], a[204:207], a[188:191], v[212:215]\n"
"v_mfma_f32_16x16x32_bf16 v[216:219], a[208:211], a[184:187], v[216:219]\n"
"v_mfma_f32_16x16x32_bf16 v[216:219], a[212:215], a[188:191], v[216:219]\n"
"v_mfma_f32_16x16x32_bf16 v[220:223], a[216:219], a[184:187], v[220:223]\n"
"v_mfma_f32_16x16x32_bf16 v[220:223], a[220:223], a[188:191], v[220:223]\n"
"v_mfma_f32_16x16x32_bf16 v[0:3], a[224:227], a[136:139], v[0:3]\n"
"v_mfma_f32_16x16x32_bf16 v[0:3], a[228:231], a[140:143], v[0:3]\n"
"v_mfma_f32_16x16x32_bf16 v[4:7], a[232:235], a[136:139], v[4:7]\n"
"s_waitcnt vmcnt(8)\n"
"v_mfma_f32_16x16x32_bf16 v[4:7], a[236:239], a[140:143], v[4:7]\n"
"s_barrier\n"
"v_mfma_f32_16x16x32_bf16 v[8:11], a[240:243], a[136:139], v[8:11]\n"
"s_mov_b32 m0, s51\n"
"ds_read_b64_tr_b16 a[192:193], v244 offset:0\n"
"v_mfma_f32_16x16x32_bf16 v[8:11], a[244:247], a[140:143], v[8:11]\n"
"buffer_load_dwordx4 v248, s[24:27], s49 offen lds\n"
"v_mfma_f32_16x16x32_bf16 v[12:15], a[248:251], a[136:139], v[12:15]\n"
"ds_read_b64_tr_b16 a[194:195], v244 offset:512\n"
"v_mfma_f32_16x16x32_bf16 v[12:15], a[252:255], a[140:143], v[12:15]\n"
"ds_read_b128 a[136:139], v246 offset:0\n"
"v_mfma_f32_16x16x32_bf16 v[28:31], a[248:251], a[144:147], v[28:31]\n"
"ds_read_b64_tr_b16 a[196:197], v244 offset:8192\n"
"v_mfma_f32_16x16x32_bf16 v[28:31], a[252:255], a[148:151], v[28:31]\n"
"ds_read_b64_tr_b16 a[198:199], v244 offset:8704\n"
"v_mfma_f32_16x16x32_bf16 v[24:27], a[240:243], a[144:147], v[24:27]\n"
"ds_read_b128 a[140:143], v247 offset:0\n"
"v_mfma_f32_16x16x32_bf16 v[24:27], a[244:247], a[148:151], v[24:27]\n"
"buffer_load_dwordx4 v249, s[24:27], s49 offen offset:1024 lds\n"
"v_mfma_f32_16x16x32_bf16 v[20:23], a[232:235], a[144:147], v[20:23]\n"
"ds_read_b64_tr_b16 a[200:201], v245 offset:0\n"
"v_mfma_f32_16x16x32_bf16 v[20:23], a[236:239], a[148:151], v[20:23]\n"
"ds_read_b64_tr_b16 a[202:203], v245 offset:512\n"
"v_mfma_f32_16x16x32_bf16 v[16:19], a[224:227], a[144:147], v[16:19]\n"
"ds_read_b64_tr_b16 a[204:205], v245 offset:8192\n"
"v_mfma_f32_16x16x32_bf16 v[16:19], a[228:231], a[148:151], v[16:19]\n"
"ds_read_b64_tr_b16 a[206:207], v245 offset:8704\n"
"v_mfma_f32_16x16x32_bf16 v[32:35], a[224:227], a[152:155], v[32:35]\n"
"ds_read_b64_tr_b16 a[208:209], v244 offset:1024\n"
"v_mfma_f32_16x16x32_bf16 v[32:35], a[228:231], a[156:159], v[32:35]\n"
"buffer_load_dwordx4 v254, s[24:27], s49 offen offset:2048 lds\n"
"v_mfma_f32_16x16x32_bf16 v[36:39], a[232:235], a[152:155], v[36:39]\n"
"ds_read_b64_tr_b16 a[210:211], v244 offset:1536\n"
"v_mfma_f32_16x16x32_bf16 v[36:39], a[236:239], a[156:159], v[36:39]\n"
"ds_read_b64_tr_b16 a[212:213], v244 offset:9216\n"
"v_mfma_f32_16x16x32_bf16 v[40:43], a[240:243], a[152:155], v[40:43]\n"
"ds_read_b64_tr_b16 a[214:215], v244 offset:9728\n"
"v_mfma_f32_16x16x32_bf16 v[40:43], a[244:247], a[156:159], v[40:43]\n"
"ds_read_b64_tr_b16 a[216:217], v245 offset:1024\n"
"v_mfma_f32_16x16x32_bf16 v[44:47], a[248:251], a[152:155], v[44:47]\n"
"ds_read_b64_tr_b16 a[218:219], v245 offset:1536\n"
"v_mfma_f32_16x16x32_bf16 v[44:47], a[252:255], a[156:159], v[44:47]\n"
"buffer_load_dwordx4 v255, s[24:27], s49 offen offset:3072 lds\n"
"v_mfma_f32_16x16x32_bf16 v[60:63], a[248:251], a[160:163], v[60:63]\n"
"ds_read_b64_tr_b16 a[220:221], v245 offset:9216\n"
"v_mfma_f32_16x16x32_bf16 v[60:63], a[252:255], a[164:167], v[60:63]\n"
"ds_read_b64_tr_b16 a[222:223], v245 offset:9728\n"
"v_mfma_f32_16x16x32_bf16 v[56:59], a[240:243], a[160:163], v[56:59]\n"
"ds_read_b128 a[144:147], v246 offset:2048\n"
"v_mfma_f32_16x16x32_bf16 v[56:59], a[244:247], a[164:167], v[56:59]\n"
"s_add_u32 s40, s49, 40960\n"
"ds_read_b128 a[148:151], v247 offset:2048\n"
"v_mfma_f32_16x16x32_bf16 v[52:55], a[232:235], a[160:163], v[52:55]\n"
"s_add_u32 m0, s51, 4096\n"
"ds_read_b128 a[152:155], v246 offset:4096\n"
"v_mfma_f32_16x16x32_bf16 v[52:55], a[236:239], a[164:167], v[52:55]\n"
"buffer_load_dwordx4 v248, s[24:27], s40 offen lds\n"
"v_mfma_f32_16x16x32_bf16 v[48:51], a[224:227], a[160:163], v[48:51]\n"
"ds_read_b128 a[156:159], v247 offset:4096\n"
"v_mfma_f32_16x16x32_bf16 v[48:51], a[228:231], a[164:167], v[48:51]\n"
"ds_read_b128 a[160:163], v252 offset:0\n"
"v_mfma_f32_16x16x32_bf16 v[64:67], a[224:227], a[168:171], v[64:67]\n"
"ds_read_b128 a[164:167], v253 offset:0\n"
"v_mfma_f32_16x16x32_bf16 v[64:67], a[228:231], a[172:175], v[64:67]\n"
"v_mfma_f32_16x16x32_bf16 v[68:71], a[232:235], a[168:171], v[68:71]\n"
"v_mfma_f32_16x16x32_bf16 v[68:71], a[236:239], a[172:175], v[68:71]\n"
"buffer_load_dwordx4 v249, s[24:27], s40 offen offset:1024 lds\n"
"v_mfma_f32_16x16x32_bf16 v[72:75], a[240:243], a[168:171], v[72:75]\n"
"v_mfma_f32_16x16x32_bf16 v[72:75], a[244:247], a[172:175], v[72:75]\n"
"v_mfma_f32_16x16x32_bf16 v[76:79], a[248:251], a[168:171], v[76:79]\n"
"ds_read_b128 a[168:171], v250 offset:0\n"
"v_mfma_f32_16x16x32_bf16 v[76:79], a[252:255], a[172:175], v[76:79]\n"
"ds_read_b128 a[172:175], v251 offset:0\n"
"v_mfma_f32_16x16x32_bf16 v[92:95], a[248:251], a[176:179], v[92:95]\n"
"v_mfma_f32_16x16x32_bf16 v[92:95], a[252:255], a[180:183], v[92:95]\n"
"buffer_load_dwordx4 v254, s[24:27], s40 offen offset:2048 lds\n"
"v_mfma_f32_16x16x32_bf16 v[88:91], a[240:243], a[176:179], v[88:91]\n"
"s_add_u32 s49, s49, 128\n"
"v_mfma_f32_16x16x32_bf16 v[88:91], a[244:247], a[180:183], v[88:91]\n"
"s_sub_u32 s41, s49, 1280\n"
"v_mfma_f32_16x16x32_bf16 v[84:87], a[232:235], a[176:179], v[84:87]\n"
"s_min_u32 s49, s49, s41\n"
"v_mfma_f32_16x16x32_bf16 v[84:87], a[236:239], a[180:183], v[84:87]\n"
"s_xor_b32 s51, s51, s74\n"
"v_mfma_f32_16x16x32_bf16 v[80:83], a[224:227], a[176:179], v[80:83]\n"
"ds_read_b128 a[176:179], v250 offset:2048\n"
"v_mfma_f32_16x16x32_bf16 v[80:83], a[228:231], a[180:183], v[80:83]\n"
"ds_read_b128 a[180:183], v251 offset:2048\n"
"v_mfma_f32_16x16x32_bf16 v[96:99], a[224:227], a[184:187], v[96:99]\n"
"v_mfma_f32_16x16x32_bf16 v[96:99], a[228:231], a[188:191], v[96:99]\n"
"v_mfma_f32_16x16x32_bf16 v[100:103], a[232:235], a[184:187], v[100:103]\n"
"v_mfma_f32_16x16x32_bf16 v[100:103], a[236:239], a[188:191], v[100:103]\n"
"v_mfma_f32_16x16x32_bf16 v[104:107], a[240:243], a[184:187], v[104:107]\n"
"v_mfma_f32_16x16x32_bf16 v[104:107], a[244:247], a[188:191], v[104:107]\n"
"v_mfma_f32_16x16x32_bf16 v[108:111], a[248:251], a[184:187], v[108:111]\n"
"s_add_u32 s47, s47, 1\n"
"ds_read_b128 a[184:187], v250 offset:4096\n"
"v_mfma_f32_16x16x32_bf16 v[108:111], a[252:255], a[188:191], v[108:111]\n"
"s_waitcnt lgkmcnt(1)\n"
"s_cmp_lg_u32 s47, s62\n"
"ds_read_b128 a[188:191], v251 offset:4096\n"
"s_cbranch_scc1 LPL_%=\n"
"LPX_%=:\n"
"s_cmp_eq_u32 s47, 18\n"
"s_cbranch_scc1 LPT2_%=\n"
"v_mfma_f32_16x16x32_bf16 v[112:115], a[192:195], a[136:139], v[112:115]\n"
"s_add_u32 s78, s96, 8192\n"
"v_mov_b32 v241, 1\n"
"v_mfma_f32_16x16x32_bf16 v[112:115], a[196:199], a[140:143], v[112:115]\n"
"s_mov_b64 exec, 1\n"
"buffer_store_dword v241, off, s[32:35], s96 sc0 sc1\n"
"buffer_store_dword v241, off, s[32:35], s78 sc0 sc1\n"
"s_mov_b64 exec, -1\n"
"v_mfma_f32_16x16x32_bf16 v[116:119], a[200:203], a[136:139], v[116:119]\n"
"s_waitcnt vmcnt(13)\n"
"v_mfma_f32_16x16x32_bf16 v[116:119], a[204:207], a[140:143], v[116:119]\n"
"s_barrier\n"
"v_mfma_f32_16x16x32_bf16 v[120:123], a[208:211], a[136:139], v[120:123]\n"
"s_add_u32 s40, s48, 256\n"
"s_xor_b32 m0, s50, 0xc000\n"
"ds_read_b64_tr_b16 a[238:239], v245 offset:25088\n"
"v_mfma_f32_16x16x32_bf16 v[120:123], a[212:215], a[140:143], v[120:123]\n"
"buffer_load_dwordx4 v224, s[28:31], s40 offen lds\n"
"v_mfma_f32_16x16x32_bf16 v[124:127], a[216:219], a[136:139], v[124:127]\n"
"v_mfma_f32_16x16x32_bf16 v[124:127], a[220:223], a[140:143], v[124:127]\n"
"v_mfma_f32_16x16x32_bf16 v[188:191], a[216:219], a[168:171], v[188:191]\n"
"ds_read_b64_tr_b16 a[254:255], v245 offset:26112\n"
"v_mfma_f32_16x16x32_bf16 v[188:191], a[220:223], a[172:175], v[188:191]\n"
"ds_read_b64_tr_b16 a[252:253], v245 offset:25600\n"
"v_mfma_f32_16x16x32_bf16 v[184:187], a[208:211], a[168:171], v[184:187]\n"
"ds_read_b64_tr_b16 a[224:225], v244 offset:16384\n"
"ds_read_b64_tr_b16 a[228:229], v244 offset:24576\n"
"v_mfma_f32_16x16x32_bf16 v[184:187], a[212:215], a[172:175], v[184:187]\n"
"buffer_load_dwordx4 v225, s[28:31], s40 offen offset:1024 lds\n"
"v_mfma_f32_16x16x32_bf16 v[180:183], a[200:203], a[168:171], v[180:183]\n"
"v_mfma_f32_16x16x32_bf16 v[180:183], a[204:207], a[172:175], v[180:183]\n"
"v_mfma_f32_16x16x32_bf16 v[176:179], a[192:195], a[168:171], v[176:179]\n"
"ds_read_b64_tr_b16 a[230:231], v244 offset:25088\n"
"v_mfma_f32_16x16x32_bf16 v[176:179], a[196:199], a[172:175], v[176:179]\n"
"ds_read_b64_tr_b16 a[244:245], v244 offset:25600\n"
"v_mfma_f32_16x16x32_bf16 v[192:195], a[192:195], a[176:179], v[192:195]\n"
"ds_read_b64_tr_b16 a[226:227], v244 offset:16896\n"
"ds_read_b64_tr_b16 a[250:251], v245 offset:17920\n"
"v_mfma_f32_16x16x32_bf16 v[192:195], a[196:199], a[180:183], v[192:195]\n"
"buffer_load_dwordx4 v226, s[28:31], s40 offen offset:2048 lds\n"
"v_mfma_f32_16x16x32_bf16 v[196:199], a[200:203], a[176:179], v[196:199]\n"
"v_mfma_f32_16x16x32_bf16 v[196:199], a[204:207], a[180:183], v[196:199]\n"
"v_mfma_f32_16x16x32_bf16 v[200:203], a[208:211], a[176:179], v[200:203]\n"
"ds_read_b64_tr_b16 a[236:237], v245 offset:24576\n"
"ds_read_b64_tr_b16 a[246:247], v244 offset:26112\n"
"v_mfma_f32_16x16x32_bf16 v[200:203], a[212:215], a[180:183], v[200:203]\n"
"ds_read_b64_tr_b16 a[240:241], v244 offset:17408\n"
"v_mfma_f32_16x16x32_bf16 v[204:207], a[216:219], a[176:179], v[204:207]\n"
"ds_read_b64_tr_b16 a[242:243], v244 offset:17920\n"
"ds_read_b64_tr_b16 a[248:249], v245 offset:17408\n"
"v_mfma_f32_16x16x32_bf16 v[204:207], a[220:223], a[180:183], v[204:207]\n"
"s_waitcnt lgkmcnt(7)\n"
"buffer_load_dwordx4 v227, s[28:31], s40 offen offset:3072 lds\n"
"v_mfma_f32_16x16x32_bf16 v[220:223], a[216:219], a[184:187], v[220:223]\n"
"s_cmp_eq_u32 s47, 8\n"
"v_mfma_f32_16x16x32_bf16 v[220:223], a[220:223], a[188:191], v[220:223]\n"
"s_cselect_b32 s41, s52, s58\n"
"s_nop 1\n"
"v_pk_fma_f32 v[176:177], v[176:177], s[70:71], s[70:71] op_sel:[0,0,1] op_sel_hi:[1,0,1]\nv_pk_fma_f32 v[178:179], v[178:179], s[70:71], s[70:71] op_sel:[0,0,1] op_sel_hi:[1,0,1]\nv_pk_fma_f32 v[180:181], v[180:181], s[70:71], s[70:71] op_sel:[0,0,1] op_sel_hi:[1,0,1]\nv_pk_fma_f32 v[182:183], v[182:183], s[70:71], s[70:71] op_sel:[0,0,1] op_sel_hi:[1,0,1]\nv_pk_fma_f32 v[184:185], v[184:185], s[70:71], s[70:71] op_sel:[0,0,1] op_sel_hi:[1,0,1]\nv_pk_fma_f32 v[186:187], v[186:187], s[70:71], s[70:71] op_sel:[0,0,1] op_sel_hi:[1,0,1]\nv_pk_fma_f32 v[188:189], v[188:189], s[70:71], s[70:71] op_sel:[0,0,1] op_sel_hi:[1,0,1]\nv_pk_fma_f32 v[190:191], v[190:191], s[70:71], s[70:71] op_sel:[0,0,1] op_sel_hi:[1,0,1]\nv_pk_fma_f32 v[192:193], v[192:193], s[70:71], s[70:71] op_sel:[0,0,1] op_sel_hi:[1,0,1]\nv_pk_fma_f32 v[194:195], v[194:195], s[70:71], s[70:71] op_sel:[0,0,1] op_sel_hi:[1,0,1]\nv_pk_fma_f32 v[196:197], v[196:197], s[70:71], s[70:71] op_sel:[0,0,1] op_sel_hi:[1,0,1]\nv_pk_fma_f32 v[198:199], v[198:199], s[70:71], s[70:71] op_sel:[0,0,1] op_sel_hi:[1,0,1]\nv_pk_fma_f32 v[200:201], v[200:201], s[70:71], s[70:71] op_sel:[0,0,1] op_sel_hi:[1,0,1]\nv_pk_fma_f32 v[202:203], v[202:203], s[70:71], s[70:71] op_sel:[0,0,1] op_sel_hi:[1,0,1]\nv_pk_fma_f32 v[204:205], v[204:205], s[70:71], s[70:71] op_sel:[0,0,1] op_sel_hi:[1,0,1]\nv_pk_fma_f32 v[206:207], v[206:207], s[70:71], s[70:71] op_sel:[0,0,1] op_sel_hi:[1,0,1]\n"
"v_mfma_f32_16x16x32_bf16 v[216:219], a[208:211], a[184:187], v[216:219]\n"
"s_add_u32 s48, s48, s41\n"
"v_cvt_pk_bf16_f32 v192, v192, v193\n"
"v_cvt_pk_bf16_f32 v193, v194, v195\n"
"v_mfma_f32_16x16x32_bf16 v[216:219], a[212:215], a[188:191], v[216:219]\n"
"s_sub_u32 s41, s48, 5242880\n"
"v_cvt_pk_bf16_f32 v176, v176, v177\n"
"v_cvt_pk_bf16_f32 v177, v178, v179\n"
"v_mfma_f32_16x16x32_bf16 v[212:215], a[200:203], a[184:187], v[212:215]\n"
"s_min_u32 s48, s48, s41\n"
"v_cvt_pk_bf16_f32 v178, v180, v181\n"
"v_cvt_pk_bf16_f32 v179, v182, v183\n"
"v_mfma_f32_16x16x32_bf16 v[212:215], a[204:207], a[188:191], v[212:215]\n"
"ds_read_b64_tr_b16 a[234:235], v245 offset:16896\n"
"v_xor_b32 v246, s74, v246\n"
"v_mfma_f32_16x16x32_bf16 v[208:211], a[192:195], a[184:187], v[208:211]\n"
"s_mov_b32 m0, s50\n"
"v_and_b32 v182, 0xf0, v242\n"
"v_cvt_pk_bf16_f32 v200, v200, v201\n"
"v_mfma_f32_16x16x32_bf16 v[208:211], a[196:199], a[188:191], v[208:211]\n"
"buffer_load_dwordx4 v224, s[28:31], s48 offen lds\n"
"v_mfma_f32_16x16x32_bf16 v[160:163], a[192:195], a[160:163], v[160:163]\n"
"v_bfe_u32 v180, v242, 9, 1\n"
"v_lshl_add_u32 v180, v180, 8, v182\n"
"v_mfma_f32_16x16x32_bf16 v[160:163], a[196:199], a[164:167], v[160:163]\n"
"v_bfe_u32 v182, v242, 8, 1\n"
"v_cvt_pk_bf16_f32 v184, v184, v185\n"
"v_mfma_f32_16x16x32_bf16 v[164:167], a[200:203], a[160:163], v[164:167]\n"
"ds_read_b64_tr_b16 a[232:233], v245 offset:16384\n"
"v_xor_b32 v247, s74, v247\n"
"v_mfma_f32_16x16x32_bf16 v[164:167], a[204:207], a[164:167], v[164:167]\n"
"buffer_load_dwordx4 v225, s[28:31], s48 offen offset:1024 lds\n"
"v_mfma_f32_16x16x32_bf16 v[168:171], a[208:211], a[160:163], v[168:171]\n"
"v_cvt_pk_bf16_f32 v201, v202, v203\n"
"v_xor_b32 v253, s74, v253\n"
"v_mfma_f32_16x16x32_bf16 v[168:171], a[212:215], a[164:167], v[168:171]\n"
"v_cvt_pk_bf16_f32 v194, v196, v197\n"
"v_xor_b32 v244, s74, v244\n"
"v_mfma_f32_16x16x32_bf16 v[172:175], a[216:219], a[160:163], v[172:175]\n"
"v_lshl_add_u32 v180, v182, 3, v180\n"
"v_cvt_pk_bf16_f32 v185, v186, v187\n"
"v_mfma_f32_16x16x32_bf16 v[172:175], a[220:223], a[164:167], v[172:175]\n"
"v_cvt_pk_bf16_f32 v186, v188, v189\n"
"v_cvt_pk_bf16_f32 v187, v190, v191\n"
"v_mfma_f32_16x16x32_bf16 v[140:143], a[216:219], a[144:147], v[140:143]\n"
"v_cvt_pk_bf16_f32 v203, v206, v207\n"
"v_bfe_u32 v181, v242, 9, 1\n"
"v_mfma_f32_16x16x32_bf16 v[140:143], a[220:223], a[148:151], v[140:143]\n"
"buffer_load_dwordx4 v226, s[28:31], s48 offen offset:2048 lds\n"
"v_mfma_f32_16x16x32_bf16 v[136:139], a[208:211], a[144:147], v[136:139]\n"
"v_xor_b32_e64 v182, s42, 1\n"
"v_lshl_add_u32 v180, v182, 10, v180\n"
"v_mfma_f32_16x16x32_bf16 v[136:139], a[212:215], a[148:151], v[136:139]\n"
"v_cvt_pk_bf16_f32 v202, v204, v205\n"
"v_xor_b32 v252, s74, v252\n"
"v_mfma_f32_16x16x32_bf16 v[132:135], a[200:203], a[144:147], v[132:135]\n"
"v_add_u32 v180, 94208, v180\n"
"v_and_b32 v182, 0xf0, v242\n"
"v_mfma_f32_16x16x32_bf16 v[132:135], a[204:207], a[148:151], v[132:135]\n"
"ds_write2st64_b64 v180, v[184:185], v[186:187] offset0:64 offset1:65\n"
"v_pk_fma_f32 v[208:209], v[208:209], s[70:71], s[70:71] op_sel:[0,0,1] op_sel_hi:[1,0,1]\nv_pk_fma_f32 v[210:211], v[210:211], s[70:71], s[70:71] op_sel:[0,0,1] op_sel_hi:[1,0,1]\nv_pk_fma_f32 v[212:213], v[212:213], s[70:71], s[70:71] op_sel:[0,0,1] op_sel_hi:[1,0,1]\nv_pk_fma_f32 v[214:215], v[214:215], s[70:71], s[70:71] op_sel:[0,0,1] op_sel_hi:[1,0,1]\nv_pk_fma_f32 v[216:217], v[216:217], s[70:71], s[70:71] op_sel:[0,0,1] op_sel_hi:[1,0,1]\nv_pk_fma_f32 v[218:219], v[218:219], s[70:71], s[70:71] op_sel:[0,0,1] op_sel_hi:[1,0,1]\nv_pk_fma_f32 v[220:221], v[220:221], s[70:71], s[70:71] op_sel:[0,0,1] op_sel_hi:[1,0,1]\nv_pk_fma_f32 v[222:223], v[222:223], s[70:71], s[70:71] op_sel:[0,0,1] op_sel_hi:[1,0,1]\nv_pk_fma_f32 v[160:161], v[160:161], s[70:71], s[70:71] op_sel:[0,0,1] op_sel_hi:[1,0,1]\nv_pk_fma_f32 v[162:163], v[162:163], s[70:71], s[70:71] op_sel:[0,0,1] op_sel_hi:[1,0,1]\nv_pk_fma_f32 v[164:165], v[164:165], s[70:71], s[70:71] op_sel:[0,0,1] op_sel_hi:[1,0,1]\nv_pk_fma_f32 v[166:167], v[166:167], s[70:71], s[70:71] op_sel:[0,0,1] op_sel_hi:[1,0,1]\nv_pk_fma_f32 v[168:169], v[168:169], s[70:71], s[70:71] op_sel:[0,0,1] op_sel_hi:[1,0,1]\nv_pk_fma_f32 v[170:171], v[170:171], s[70:71], s[70:71] op_sel:[0,0,1] op_sel_hi:[1,0,1]\nv_pk_fma_f32 v[172:173], v[172:173], s[70:71], s[70:71] op_sel:[0,0,1] op_sel_hi:[1,0,1]\nv_pk_fma_f32 v[174:175], v[174:175], s[70:71], s[70:71] op_sel:[0,0,1] op_sel_hi:[1,0,1]\n"
"v_cvt_pk_bf16_f32 v216, v216, v217\n"
"v_mfma_f32_16x16x32_bf16 v[128:131], a[192:195], a[144:147], v[128:131]\n"
"v_cvt_pk_bf16_f32 v168, v168, v169\n"
"v_xor_b32 v250, s74, v250\n"
"v_mfma_f32_16x16x32_bf16 v[128:131], a[196:199], a[148:151], v[128:131]\n"
"buffer_load_dwordx4 v227, s[28:31], s48 offen offset:3072 lds\n"
"v_mfma_f32_16x16x32_bf16 v[144:147], a[192:195], a[152:155], v[144:147]\n"
"s_xor_b32 s50, s50, s74\n"
"v_cvt_pk_bf16_f32 v208, v208, v209\n"
"v_xor_b32 v251, s74, v251\n"
"v_mfma_f32_16x16x32_bf16 v[144:147], a[196:199], a[156:159], v[144:147]\n"
"v_cvt_pk_bf16_f32 v217, v218, v219\n"
"v_cvt_pk_bf16_f32 v169, v170, v171\n"
"v_mfma_f32_16x16x32_bf16 v[148:151], a[200:203], a[152:155], v[148:151]\n"
"v_cvt_pk_bf16_f32 v195, v198, v199\n"
"v_cvt_pk_bf16_f32 v209, v210, v211\n"
"v_mfma_f32_16x16x32_bf16 v[148:151], a[204:207], a[156:159], v[148:151]\n"
"ds_write2st64_b64 v180, v[192:193], v[194:195] offset0:88 offset1:89\n"
"v_pk_fma_f32 v[112:113], v[112:113], s[70:71], s[70:71] op_sel:[0,0,1] op_sel_hi:[1,0,1]\nv_pk_fma_f32 v[114:115], v[114:115], s[70:71], s[70:71] op_sel:[0,0,1] op_sel_hi:[1,0,1]\nv_pk_fma_f32 v[116:117], v[116:117], s[70:71], s[70:71] op_sel:[0,0,1] op_sel_hi:[1,0,1]\nv_pk_fma_f32 v[118:119], v[118:119], s[70:71], s[70:71] op_sel:[0,0,1] op_sel_hi:[1,0,1]\nv_pk_fma_f32 v[120:121], v[120:121], s[70:71], s[70:71] op_sel:[0,0,1] op_sel_hi:[1,0,1]\nv_pk_fma_f32 v[122:123], v[122:123], s[70:71], s[70:71] op_sel:[0,0,1] op_sel_hi:[1,0,1]\nv_pk_fma_f32 v[124:125], v[124:125], s[70:71], s[70:71] op_sel:[0,0,1] op_sel_hi:[1,0,1]\nv_pk_fma_f32 v[126:127], v[126:127], s[70:71], s[70:71] op_sel:[0,0,1] op_sel_hi:[1,0,1]\nv_pk_fma_f32 v[128:129], v[128:129], s[70:71], s[70:71] op_sel:[0,0,1] op_sel_hi:[1,0,1]\nv_pk_fma_f32 v[130:131], v[130:131], s[70:71], s[70:71] op_sel:[0,0,1] op_sel_hi:[1,0,1]\nv_pk_fma_f32 v[132:133], v[132:133], s[70:71], s[70:71] op_sel:[0,0,1] op_sel_hi:[1,0,1]\nv_pk_fma_f32 v[134:135], v[134:135], s[70:71], s[70:71] op_sel:[0,0,1] op_sel_hi:[1,0,1]\nv_pk_fma_f32 v[136:137], v[136:137], s[70:71], s[70:71] op_sel:[0,0,1] op_sel_hi:[1,0,1]\nv_pk_fma_f32 v[138:139], v[138:139], s[70:71], s[70:71] op_sel:[0,0,1] op_sel_hi:[1,0,1]\nv_pk_fma_f32 v[140:141], v[140:141], s[70:71], s[70:71] op_sel:[0,0,1] op_sel_hi:[1,0,1]\nv_pk_fma_f32 v[142:143], v[142:143], s[70:71], s[70:71] op_sel:[0,0,1] op_sel_hi:[1,0,1]\n"
"v_mfma_f32_16x16x32_bf16 v[152:155], a[208:211], a[152:155], v[152:155]\n"
"v_cvt_pk_bf16_f32 v218, v220, v221\n"
"v_cvt_pk_bf16_f32 v136, v136, v137\n"
"v_mfma_f32_16x16x32_bf16 v[152:155], a[212:215], a[156:159], v[152:155]\n"
"v_cvt_pk_bf16_f32 v137, v138, v139\n"
"v_cvt_pk_bf16_f32 v139, v142, v143\n"
"v_mfma_f32_16x16x32_bf16 v[156:159], a[216:219], a[152:155], v[156:159]\n"
"v_cvt_pk_bf16_f32 v128, v128, v129\n"
"v_cvt_pk_bf16_f32 v138, v140, v141\n"
"v_mfma_f32_16x16x32_bf16 v[156:159], a[220:223], a[156:159], v[156:159]\n"
"s_waitcnt lgkmcnt(1)\n"
"v_cvt_pk_bf16_f32 v112, v112, v113\n"
"v_xor_b32 v245, s74, v245\n"
"v_mfma_f32_16x16x32_bf16 v[96:99], a[224:227], a[184:187], v[96:99]\n"
"v_cvt_pk_bf16_f32 v120, v120, v121\n"
"v_cvt_pk_bf16_f32 v129, v130, v131\n"
"v_mfma_f32_16x16x32_bf16 v[96:99], a[228:231], a[188:191], v[96:99]\n"
"ds_write2st64_b64 v180, v[200:201], v[202:203] offset0:96 offset1:97\n"
"v_cvt_pk_bf16_f32 v219, v222, v223\n"
"v_cvt_pk_bf16_f32 v170, v172, v173\n"
"v_cvt_pk_bf16_f32 v131, v134, v135\n"
"v_lshl_add_u32 v181, v181, 8, v182\n"
"v_bfe_u32 v182, v242, 8, 1\n"
"v_lshl_add_u32 v181, v182, 9, v181\n"
"v_mfma_f32_16x16x32_bf16 v[100:103], a[232:235], a[184:187], v[100:103]\n"
"s_waitcnt vmcnt(10)\n"
"v_permlane16_swap_b32 v136, v138\n"
"v_mfma_f32_16x16x32_bf16 v[100:103], a[236:239], a[188:191], v[100:103]\n"
"s_barrier\n"
"ds_write2st64_b64 v180, v[216:217], v[218:219] offset0:128 offset1:129\n"
"v_pk_fma_f32 v[144:145], v[144:145], s[70:71], s[70:71] op_sel:[0,0,1] op_sel_hi:[1,0,1]\nv_pk_fma_f32 v[146:147], v[146:147], s[70:71], s[70:71] op_sel:[0,0,1] op_sel_hi:[1,0,1]\nv_pk_fma_f32 v[148:149], v[148:149], s[70:71], s[70:71] op_sel:[0,0,1] op_sel_hi:[1,0,1]\nv_pk_fma_f32 v[150:151], v[150:151], s[70:71], s[70:71] op_sel:[0,0,1] op_sel_hi:[1,0,1]\nv_pk_fma_f32 v[152:153], v[152:153], s[70:71], s[70:71] op_sel:[0,0,1] op_sel_hi:[1,0,1]\nv_pk_fma_f32 v[154:155], v[154:155], s[70:71], s[70:71] op_sel:[0,0,1] op_sel_hi:[1,0,1]\nv_pk_fma_f32 v[156:157], v[156:157], s[70:71], s[70:71] op_sel:[0,0,1] op_sel_hi:[1,0,1]\nv_pk_fma_f32 v[158:159], v[158:159], s[70:71], s[70:71] op_sel:[0,0,1] op_sel_hi:[1,0,1]\n"
"v_cvt_pk_bf16_f32 v144, v144, v145\n"
"v_mfma_f32_16x16x32_bf16 v[104:107], a[240:243], a[184:187], v[104:107]\n"
"s_mov_b32 m0, s51\n"
"v_cvt_pk_bf16_f32 v171, v174, v175\n"
"v_cvt_pk_bf16_f32 v121, v122, v123\n"
"v_mfma_f32_16x16x32_bf16 v[104:107], a[244:247], a[188:191], v[104:107]\n"
"buffer_load_dwordx4 v248, s[24:27], s49 offen lds\n"
"v_mfma_f32_16x16x32_bf16 v[108:111], a[248:251], a[184:187], v[108:111]\n"
"v_xor_b32_e64 v182, s42, 0\n"
"v_lshl_add_u32 v181, v182, 10, v181\n"
"v_mfma_f32_16x16x32_bf16 v[108:111], a[252:255], a[188:191], v[108:111]\n"
"ds_read_b64_tr_b16 a[218:219], v245 offset:1536\n"
"v_cvt_pk_bf16_f32 v211, v214, v215\n"
"v_mfma_f32_16x16x32_bf16 v[92:95], a[248:251], a[176:179], v[92:95]\n"
"v_cvt_pk_bf16_f32 v160, v160, v161\n"
"v_cvt_pk_bf16_f32 v145, v146, v147\n"
"v_mfma_f32_16x16x32_bf16 v[92:95], a[252:255], a[180:183], v[92:95]\n"
"ds_write2st64_b64 v180, v[176:177], v[178:179] offset0:0 offset1:1\n"
"v_cvt_pk_bf16_f32 v122, v124, v125\n"
"v_cvt_pk_bf16_f32 v123, v126, v127\n"
"v_cvt_pk_bf16_f32 v146, v148, v149\n"
"v_cvt_pk_bf16_f32 v152, v152, v153\n"
"v_cvt_pk_bf16_f32 v153, v154, v155\n"
"v_cvt_pk_bf16_f32 v155, v158, v159\n"
"v_mfma_f32_16x16x32_bf16 v[88:91], a[240:243], a[176:179], v[88:91]\n"
"v_cvt_pk_bf16_f32 v210, v212, v213\n"
"v_cvt_pk_bf16_f32 v113, v114, v115\n"
"v_mfma_f32_16x16x32_bf16 v[88:91], a[244:247], a[180:183], v[88:91]\n"
"buffer_load_dwordx4 v249, s[24:27], s49 offen offset:1024 lds\n"
"v_mfma_f32_16x16x32_bf16 v[84:87], a[232:235], a[176:179], v[84:87]\n"
"v_cvt_pk_bf16_f32 v161, v162, v163\n"
"v_cvt_pk_bf16_f32 v154, v156, v157\n"
"v_mfma_f32_16x16x32_bf16 v[84:87], a[236:239], a[180:183], v[84:87]\n"
"s_nop 0\n"
"v_permlane16_swap_b32 v152, v154\n"
"v_mfma_f32_16x16x32_bf16 v[80:83], a[224:227], a[176:179], v[80:83]\n"
"ds_read_b64_tr_b16 a[196:197], v244 offset:8192\n"
"ds_read_b64_tr_b16 a[214:215], v244 offset:9728\n"
"v_mfma_f32_16x16x32_bf16 v[80:83], a[228:231], a[180:183], v[80:83]\n"
"ds_write2st64_b64 v180, v[208:209], v[210:211] offset0:120 offset1:121\n"
"v_cvt_pk_bf16_f32 v114, v116, v117\n"
"v_permlane16_swap_b32 v137, v139\n"
"v_permlane16_swap_b32 v144, v146\n"
"v_mfma_f32_16x16x32_bf16 v[64:67], a[224:227], a[168:171], v[64:67]\n"
"v_permlane16_swap_b32 v121, v123\n"
"v_mfma_f32_16x16x32_bf16 v[64:67], a[228:231], a[172:175], v[64:67]\n"
"buffer_load_dwordx4 v254, s[24:27], s49 offen offset:2048 lds\n"
"v_mfma_f32_16x16x32_bf16 v[68:71], a[232:235], a[168:171], v[68:71]\n"
"v_permlane16_swap_b32 v120, v122\n"
"v_mfma_f32_16x16x32_bf16 v[68:71], a[236:239], a[172:175], v[68:71]\n"
"v_cvt_pk_bf16_f32 v115, v118, v119\n"
"v_accvgpr_write_b32 a24, v136\n"
"v_mfma_f32_16x16x32_bf16 v[72:75], a[240:243], a[168:171], v[72:75]\n"
"ds_read_b64_tr_b16 a[206:207], v245 offset:8704\n"
"v_add_u32 v181, 94208, v181\n"
"v_mfma_f32_16x16x32_bf16 v[72:75], a[244:247], a[172:175], v[72:75]\n"
"ds_read_b64_tr_b16 a[216:217], v245 offset:1024\n"
"v_permlane16_swap_b32 v112, v114\n"
"v_permlane16_swap_b32 v153, v155\n"
"v_pk_fma_f32 v[96:97], v[96:97], s[70:71], s[70:71] op_sel:[0,0,1] op_sel_hi:[1,0,1]\nv_pk_fma_f32 v[98:99], v[98:99], s[70:71], s[70:71] op_sel:[0,0,1] op_sel_hi:[1,0,1]\nv_pk_fma_f32 v[100:101], v[100:101], s[70:71], s[70:71] op_sel:[0,0,1] op_sel_hi:[1,0,1]\nv_pk_fma_f32 v[102:103], v[102:103], s[70:71], s[70:71] op_sel:[0,0,1] op_sel_hi:[1,0,1]\nv_pk_fma_f32 v[104:105], v[104:105], s[70:71], s[70:71] op_sel:[0,0,1] op_sel_hi:[1,0,1]\nv_pk_fma_f32 v[106:107], v[106:107], s[70:71], s[70:71] op_sel:[0,0,1] op_sel_hi:[1,0,1]\nv_pk_fma_f32 v[108:109], v[108:109], s[70:71], s[70:71] op_sel:[0,0,1] op_sel_hi:[1,0,1]\nv_pk_fma_f32 v[110:111], v[110:111], s[70:71], s[70:71] op_sel:[0,0,1] op_sel_hi:[1,0,1]\nv_pk_fma_f32 v[80:81], v[80:81], s[70:71], s[70:71] op_sel:[0,0,1] op_sel_hi:[1,0,1]\nv_pk_fma_f32 v[82:83], v[82:83], s[70:71], s[70:71] op_sel:[0,0,1] op_sel_hi:[1,0,1]\nv_pk_fma_f32 v[84:85], v[84:85], s[70:71], s[70:71] op_sel:[0,0,1] op_sel_hi:[1,0,1]\nv_pk_fma_f32 v[86:87], v[86:87], s[70:71], s[70:71] op_sel:[0,0,1] op_sel_hi:[1,0,1]\nv_pk_fma_f32 v[88:89], v[88:89], s[70:71], s[70:71] op_sel:[0,0,1] op_sel_hi:[1,0,1]\nv_pk_fma_f32 v[90:91], v[90:91], s[70:71], s[70:71] op_sel:[0,0,1] op_sel_hi:[1,0,1]\nv_pk_fma_f32 v[92:93], v[92:93], s[70:71], s[70:71] op_sel:[0,0,1] op_sel_hi:[1,0,1]\nv_pk_fma_f32 v[94:95], v[94:95], s[70:71], s[70:71] op_sel:[0,0,1] op_sel_hi:[1,0,1]\n"
"v_mfma_f32_16x16x32_bf16 v[76:79], a[248:251], a[168:171], v[76:79]\n"
"ds_read_b64_tr_b16 a[208:209], v244 offset:1024\n"
"v_cvt_pk_bf16_f32 v80, v80, v81\n"
"v_mfma_f32_16x16x32_bf16 v[76:79], a[252:255], a[172:175], v[76:79]\n"
"buffer_load_dwordx4 v255, s[24:27], s49 offen offset:3072 lds\n"
"v_mfma_f32_16x16x32_bf16 v[60:63], a[248:251], a[160:163], v[60:63]\n"
"ds_read_b64_tr_b16 a[192:193], v244 offset:0\n"
"ds_read_b64_tr_b16 a[222:223], v245 offset:9728\n"
"v_mfma_f32_16x16x32_bf16 v[60:63], a[252:255], a[164:167], v[60:63]\n"
"v_permlane16_swap_b32 v168, v170\n"
"v_mfma_f32_16x16x32_bf16 v[56:59], a[240:243], a[160:163], v[56:59]\n"
"v_permlane16_swap_b32 v113, v115\n"
"v_mfma_f32_16x16x32_bf16 v[56:59], a[244:247], a[164:167], v[56:59]\n"
"s_add_u32 s40, s49, 40960\n"
"ds_read_b64_tr_b16 a[210:211], v244 offset:1536\n"
"v_add_u32 v182, s75, v181\n"
"v_mfma_f32_16x16x32_bf16 v[52:55], a[232:235], a[160:163], v[52:55]\n"
"s_add_u32 m0, s51, 4096\n"
"v_cvt_pk_bf16_f32 v130, v132, v133\n"
"v_accvgpr_write_b32 a25, v137\n"
"v_mfma_f32_16x16x32_bf16 v[52:55], a[236:239], a[164:167], v[52:55]\n"
"buffer_load_dwordx4 v248, s[24:27], s40 offen lds\n"
"v_mfma_f32_16x16x32_bf16 v[48:51], a[224:227], a[160:163], v[48:51]\n"
"s_waitcnt vmcnt(63) lgkmcnt(3)\n"
"v_permlane16_swap_b32 v129, v131\n"
"v_mfma_f32_16x16x32_bf16 v[48:51], a[228:231], a[164:167], v[48:51]\n"
"s_barrier\n"
"v_mfma_f32_16x16x32_bf16 v[0:3], a[224:227], a[136:139], v[0:3]\n"
"ds_read_b128 a[36:39], v181 offset:61440\n"
"v_accvgpr_write_b32 a43, v155\n"
"v_mfma_f32_16x16x32_bf16 v[0:3], a[228:231], a[140:143], v[0:3]\n"
"ds_read_b128 a[28:31], v181 offset:49152\n"
"v_accvgpr_write_b32 a10, v122\n"
"v_mfma_f32_16x16x32_bf16 v[4:7], a[232:235], a[136:139], v[4:7]\n"
"v_cvt_pk_bf16_f32 v96, v96, v97\n"
"v_cvt_pk_bf16_f32 v104, v104, v105\n"
"v_mfma_f32_16x16x32_bf16 v[4:7], a[236:239], a[140:143], v[4:7]\n"
"buffer_load_dwordx4 v249, s[24:27], s40 offen offset:1024 lds\n"
"v_mfma_f32_16x16x32_bf16 v[8:11], a[240:243], a[136:139], v[8:11]\n"
"v_cvt_pk_bf16_f32 v97, v98, v99\n"
"v_cvt_pk_bf16_f32 v99, v102, v103\n"
"v_mfma_f32_16x16x32_bf16 v[8:11], a[244:247], a[140:143], v[8:11]\n"
"ds_read_b64_tr_b16 a[220:221], v245 offset:9216\n"
"ds_read_b128 a[12:15], v181 offset:32768\n"
"v_mfma_f32_16x16x32_bf16 v[12:15], a[248:251], a[136:139], v[12:15]\n"
"ds_read_b128 a[164:167], v253 offset:0\n"
"v_accvgpr_write_b32 a32, v144\n"
"v_mfma_f32_16x16x32_bf16 v[12:15], a[252:255], a[140:143], v[12:15]\n"
"ds_read_b128 a[140:143], v247 offset:0\n"
"v_accvgpr_write_b32 a3, v115\n"
"v_mfma_f32_16x16x32_bf16 v[28:31], a[248:251], a[144:147], v[28:31]\n"
"ds_read_b64_tr_b16 a[194:195], v244 offset:512\n"
"v_cvt_pk_bf16_f32 v162, v164, v165\n"
"v_mfma_f32_16x16x32_bf16 v[28:31], a[252:255], a[148:151], v[28:31]\n"
"buffer_load_dwordx4 v254, s[24:27], s40 offen offset:2048 lds\n"
"v_mfma_f32_16x16x32_bf16 v[24:27], a[240:243], a[144:147], v[24:27]\n"
"s_add_u32 s49, s49, 128\n"
"v_cvt_pk_bf16_f32 v147, v150, v151\n"
"v_accvgpr_write_b32 a41, v153\n"
"v_mfma_f32_16x16x32_bf16 v[24:27], a[244:247], a[148:151], v[24:27]\n"
"s_sub_u32 s41, s49, 1280\n"
"v_cvt_pk_bf16_f32 v98, v100, v101\n"
"v_cvt_pk_bf16_f32 v88, v88, v89\n"
"v_mfma_f32_16x16x32_bf16 v[20:23], a[232:235], a[144:147], v[20:23]\n"
"s_min_u32 s49, s49, s41\n"
"ds_read_b128 a[136:139], v246 offset:0\n"
"v_accvgpr_write_b32 a19, v131\n"
"v_mfma_f32_16x16x32_bf16 v[20:23], a[236:239], a[148:151], v[20:23]\n"
"s_xor_b32 s51, s51, s74\n"
"ds_read_b64_tr_b16 a[198:199], v244 offset:8704\n"
"ds_read_b128 a[44:47], v182 offset:0\n"
"v_mfma_f32_16x16x32_bf16 v[16:19], a[224:227], a[144:147], v[16:19]\n"
"ds_read_b64_tr_b16 a[212:213], v244 offset:9216\n"
"v_cvt_pk_bf16_f32 v163, v166, v167\n"
"v_mfma_f32_16x16x32_bf16 v[16:19], a[228:231], a[148:151], v[16:19]\n"
"ds_read_b128 a[148:151], v247 offset:2048\n"
"v_accvgpr_write_b32 a11, v123\n"
"v_mfma_f32_16x16x32_bf16 v[32:35], a[224:227], a[152:155], v[32:35]\n"
"ds_read_b128 a[4:7], v181 offset:0\n"
"v_and_b32 v102, 0xf0, v242\n"
"v_mfma_f32_16x16x32_bf16 v[32:35], a[228:231], a[156:159], v[32:35]\n"
"ds_read_b64_tr_b16 a[202:203], v245 offset:512\n"
"ds_read_b64_tr_b16 a[204:205], v245 offset:8192\n"
"v_mfma_f32_16x16x32_bf16 v[36:39], a[232:235], a[152:155], v[36:39]\n"
"ds_read_b64_tr_b16 a[200:201], v245 offset:0\n"
"v_cvt_pk_bf16_f32 v89, v90, v91\n"
"v_mfma_f32_16x16x32_bf16 v[36:39], a[236:239], a[156:159], v[36:39]\n"
"ds_read_b128 a[20:23], v181 offset:45056\n"
"v_accvgpr_write_b32 a27, v139\n"
"v_mfma_f32_16x16x32_bf16 v[40:43], a[240:243], a[152:155], v[40:43]\n"
"v_accvgpr_write_b32 a0, v112\n"
"v_accvgpr_write_b32 a1, v113\n"
"v_mfma_f32_16x16x32_bf16 v[40:43], a[244:247], a[156:159], v[40:43]\n"
"ds_read_b128 a[180:183], v251 offset:2048\n"
"v_accvgpr_write_b32 a2, v114\n"
"v_mfma_f32_16x16x32_bf16 v[44:47], a[248:251], a[152:155], v[44:47]\n"
"s_add_u32 s47, s47, 1\n"
"ds_read_b128 a[144:147], v246 offset:2048\n"
"v_accvgpr_write_b32 a17, v129\n"
"v_mfma_f32_16x16x32_bf16 v[44:47], a[252:255], a[156:159], v[44:47]\n"
"s_waitcnt lgkmcnt(7)\n"
"ds_read_b128 a[160:163], v252 offset:0\n"
"v_permlane16_swap_b32 v161, v163\n"
"v_permlane16_swap_b32 v169, v171\n"
"v_accvgpr_write_b32 a9, v121\n"
"v_permlane16_swap_b32 v128, v130\n"
"v_permlane16_swap_b32 v145, v147\n"
"v_accvgpr_write_b32 a42, v154\n"
"v_bfe_u32 v100, v242, 9, 1\n"
"v_cvt_pk_bf16_f32 v91, v94, v95\n"
"v_pk_fma_f32 v[0:1], v[0:1], s[70:71], s[70:71] op_sel:[0,0,1] op_sel_hi:[1,0,1]\nv_pk_fma_f32 v[2:3], v[2:3], s[70:71], s[70:71] op_sel:[0,0,1] op_sel_hi:[1,0,1]\nv_pk_fma_f32 v[4:5], v[4:5], s[70:71], s[70:71] op_sel:[0,0,1] op_sel_hi:[1,0,1]\nv_pk_fma_f32 v[6:7], v[6:7], s[70:71], s[70:71] op_sel:[0,0,1] op_sel_hi:[1,0,1]\nv_pk_fma_f32 v[8:9], v[8:9], s[70:71], s[70:71] op_sel:[0,0,1] op_sel_hi:[1,0,1]\nv_pk_fma_f32 v[10:11], v[10:11], s[70:71], s[70:71] op_sel:[0,0,1] op_sel_hi:[1,0,1]\nv_pk_fma_f32 v[12:13], v[12:13], s[70:71], s[70:71] op_sel:[0,0,1] op_sel_hi:[1,0,1]\nv_pk_fma_f32 v[14:15], v[14:15], s[70:71], s[70:71] op_sel:[0,0,1] op_sel_hi:[1,0,1]\nv_pk_fma_f32 v[16:17], v[16:17], s[70:71], s[70:71] op_sel:[0,0,1] op_sel_hi:[1,0,1]\nv_pk_fma_f32 v[18:19], v[18:19], s[70:71], s[70:71] op_sel:[0,0,1] op_sel_hi:[1,0,1]\nv_pk_fma_f32 v[20:21], v[20:21], s[70:71], s[70:71] op_sel:[0,0,1] op_sel_hi:[1,0,1]\nv_pk_fma_f32 v[22:23], v[22:23], s[70:71], s[70:71] op_sel:[0,0,1] op_sel_hi:[1,0,1]\nv_pk_fma_f32 v[24:25], v[24:25], s[70:71], s[70:71] op_sel:[0,0,1] op_sel_hi:[1,0,1]\nv_pk_fma_f32 v[26:27], v[26:27], s[70:71], s[70:71] op_sel:[0,0,1] op_sel_hi:[1,0,1]\nv_pk_fma_f32 v[28:29], v[28:29], s[70:71], s[70:71] op_sel:[0,0,1] op_sel_hi:[1,0,1]\nv_pk_fma_f32 v[30:31], v[30:31], s[70:71], s[70:71] op_sel:[0,0,1] op_sel_hi:[1,0,1]\n"
"v_cvt_pk_bf16_f32 v16, v16, v17\n"
"v_cvt_pk_bf16_f32 v24, v24, v25\n"
"v_cvt_pk_bf16_f32 v25, v26, v27\n"
"v_mfma_f32_16x16x32_bf16 v[112:115], a[192:195], a[136:139], v[228:231]\n"
"v_cvt_pk_bf16_f32 v0, v0, v1\n"
"v_cvt_pk_bf16_f32 v8, v8, v9\n"
"v_mfma_f32_16x16x32_bf16 v[112:115], a[196:199], a[140:143], v[112:115]\n"
"s_waitcnt lgkmcnt(4)\n"
"ds_write_b128 v243, v[168:171] offset:2048\n"
"v_permlane16_swap_b32 v160, v162\n"
"v_accvgpr_write_b32 a8, v120\n"
"v_cvt_pk_bf16_f32 v81, v82, v83\n"
"v_pk_fma_f32 v[64:65], v[64:65], s[70:71], s[70:71] op_sel:[0,0,1] op_sel_hi:[1,0,1]\nv_pk_fma_f32 v[66:67], v[66:67], s[70:71], s[70:71] op_sel:[0,0,1] op_sel_hi:[1,0,1]\nv_pk_fma_f32 v[68:69], v[68:69], s[70:71], s[70:71] op_sel:[0,0,1] op_sel_hi:[1,0,1]\nv_pk_fma_f32 v[70:71], v[70:71], s[70:71], s[70:71] op_sel:[0,0,1] op_sel_hi:[1,0,1]\nv_pk_fma_f32 v[72:73], v[72:73], s[70:71], s[70:71] op_sel:[0,0,1] op_sel_hi:[1,0,1]\nv_pk_fma_f32 v[74:75], v[74:75], s[70:71], s[70:71] op_sel:[0,0,1] op_sel_hi:[1,0,1]\nv_pk_fma_f32 v[76:77], v[76:77], s[70:71], s[70:71] op_sel:[0,0,1] op_sel_hi:[1,0,1]\nv_pk_fma_f32 v[78:79], v[78:79], s[70:71], s[70:71] op_sel:[0,0,1] op_sel_hi:[1,0,1]\nv_pk_fma_f32 v[48:49], v[48:49], s[70:71], s[70:71] op_sel:[0,0,1] op_sel_hi:[1,0,1]\nv_pk_fma_f32 v[50:51], v[50:51], s[70:71], s[70:71] op_sel:[0,0,1] op_sel_hi:[1,0,1]\nv_pk_fma_f32 v[52:53], v[52:53], s[70:71], s[70:71] op_sel:[0,0,1] op_sel_hi:[1,0,1]\nv_pk_fma_f32 v[54:55], v[54:55], s[70:71], s[70:71] op_sel:[0,0,1] op_sel_hi:[1,0,1]\nv_pk_fma_f32 v[56:57], v[56:57], s[70:71], s[70:71] op_sel:[0,0,1] op_sel_hi:[1,0,1]\nv_pk_fma_f32 v[58:59], v[58:59], s[70:71], s[70:71] op_sel:[0,0,1] op_sel_hi:[1,0,1]\nv_pk_fma_f32 v[60:61], v[60:61], s[70:71], s[70:71] op_sel:[0,0,1] op_sel_hi:[1,0,1]\nv_pk_fma_f32 v[62:63], v[62:63], s[70:71], s[70:71] op_sel:[0,0,1] op_sel_hi:[1,0,1]\n"
"v_cvt_pk_bf16_f32 v64, v64, v65\n"
"v_mfma_f32_16x16x32_bf16 v[116:119], a[200:203], a[136:139], v[228:231]\n"
"s_waitcnt vmcnt(11) lgkmcnt(4)\n"
"v_accvgpr_write_b32 a18, v130\n"
"v_accvgpr_write_b32 a26, v138\n"
"v_mfma_f32_16x16x32_bf16 v[116:119], a[204:207], a[140:143], v[116:119]\n"
"s_barrier\n"
"ds_write_b128 v243, v[160:163] offset:0\n"
"v_accvgpr_write_b32 a16, v128\n"
"v_accvgpr_write_b32 a33, v145\n"
"v_cvt_pk_bf16_f32 v1, v2, v3\n"
"v_pk_fma_f32 v[32:33], v[32:33], s[70:71], s[70:71] op_sel:[0,0,1] op_sel_hi:[1,0,1]\nv_pk_fma_f32 v[34:35], v[34:35], s[70:71], s[70:71] op_sel:[0,0,1] op_sel_hi:[1,0,1]\nv_pk_fma_f32 v[36:37], v[36:37], s[70:71], s[70:71] op_sel:[0,0,1] op_sel_hi:[1,0,1]\nv_pk_fma_f32 v[38:39], v[38:39], s[70:71], s[70:71] op_sel:[0,0,1] op_sel_hi:[1,0,1]\nv_pk_fma_f32 v[40:41], v[40:41], s[70:71], s[70:71] op_sel:[0,0,1] op_sel_hi:[1,0,1]\nv_pk_fma_f32 v[42:43], v[42:43], s[70:71], s[70:71] op_sel:[0,0,1] op_sel_hi:[1,0,1]\nv_pk_fma_f32 v[44:45], v[44:45], s[70:71], s[70:71] op_sel:[0,0,1] op_sel_hi:[1,0,1]\nv_pk_fma_f32 v[46:47], v[46:47], s[70:71], s[70:71] op_sel:[0,0,1] op_sel_hi:[1,0,1]\n"
"v_cvt_pk_bf16_f32 v32, v32, v33\n"
"v_mfma_f32_16x16x32_bf16 v[120:123], a[208:211], a[136:139], v[228:231]\n"
"s_add_u32 s40, s48, 256\n"
"s_xor_b32 m0, s50, 0xc000\n"
"ds_read_b128 a[152:155], v246 offset:4096\n"
"v_mfma_f32_16x16x32_bf16 v[120:123], a[212:215], a[140:143], v[120:123]\n"
"buffer_load_dwordx4 v224, s[28:31], s40 offen lds\n"
"v_mfma_f32_16x16x32_bf16 v[124:127], a[216:219], a[136:139], v[228:231]\n"
"v_accvgpr_write_b32 a40, v152\n"
"v_cvt_pk_bf16_f32 v40, v40, v41\n"
"v_mfma_f32_16x16x32_bf16 v[124:127], a[220:223], a[140:143], v[124:127]\n"
"s_waitcnt lgkmcnt(3)\n"
"ds_read_b128 a[168:171], v250 offset:0\n"
"v_cvt_pk_bf16_f32 v105, v106, v107\n"
"v_mfma_f32_16x16x32_bf16 v[140:143], a[216:219], a[144:147], v[228:231]\n"
"ds_read_b128 a[156:159], v247 offset:4096\n"
"v_accvgpr_write_b32 a34, v146\n"
"v_mfma_f32_16x16x32_bf16 v[140:143], a[220:223], a[148:151], v[140:143]\n"
"ds_read_b128 a[172:175], v251 offset:0\n"
"v_cvt_pk_bf16_f32 v2, v4, v5\n"
"v_mfma_f32_16x16x32_bf16 v[136:139], a[208:211], a[144:147], v[228:231]\n"
"ds_read_b64_tr_b16 a[224:225], v244 offset:16384\n"
"v_lshl_add_u32 v100, v100, 8, v102\n"
"v_mfma_f32_16x16x32_bf16 v[136:139], a[212:215], a[148:151], v[136:139]\n"
"buffer_load_dwordx4 v225, s[28:31], s40 offen offset:1024 lds\n"
"v_mfma_f32_16x16x32_bf16 v[132:135], a[200:203], a[144:147], v[228:231]\n"
"v_bfe_u32 v102, v242, 8, 1\n"
"v_cvt_pk_bf16_f32 v41, v42, v43\n"
"v_mfma_f32_16x16x32_bf16 v[132:135], a[204:207], a[148:151], v[132:135]\n"
"v_accvgpr_write_b32 a35, v147\n"
"v_cvt_pk_bf16_f32 v65, v66, v67\n"
"v_mfma_f32_16x16x32_bf16 v[128:131], a[192:195], a[144:147], v[228:231]\n"
"ds_read_b64_tr_b16 a[234:235], v245 offset:16896\n"
"v_cvt_pk_bf16_f32 v72, v72, v73\n"
"v_mfma_f32_16x16x32_bf16 v[128:131], a[196:199], a[148:151], v[128:131]\n"
"s_waitcnt lgkmcnt(4)\n"
"ds_read_b64_tr_b16 a[236:237], v245 offset:24576\n"
"v_bfe_u32 v101, v242, 9, 1\n"
"v_mfma_f32_16x16x32_bf16 v[144:147], a[192:195], a[152:155], v[228:231]\n"
"s_waitcnt lgkmcnt(4)\n"
"ds_read_b64_tr_b16 a[226:227], v244 offset:16896\n"
"v_cvt_pk_bf16_f32 v9, v10, v11\n"
"v_mfma_f32_16x16x32_bf16 v[144:147], a[196:199], a[156:159], v[144:147]\n"
"buffer_load_dwordx4 v226, s[28:31], s40 offen offset:2048 lds\n"
"v_mfma_f32_16x16x32_bf16 v[148:151], a[200:203], a[152:155], v[228:231]\n"
"v_cvt_pk_bf16_f32 v3, v6, v7\n"
"v_cvt_pk_bf16_f32 v17, v18, v19\n"
"v_mfma_f32_16x16x32_bf16 v[148:151], a[204:207], a[156:159], v[148:151]\n"
"v_cvt_pk_bf16_f32 v66, v68, v69\n"
"v_cvt_pk_bf16_f32 v73, v74, v75\n"
"v_mfma_f32_16x16x32_bf16 v[152:155], a[208:211], a[152:155], v[228:231]\n"
"ds_read_b64_tr_b16 a[252:253], v245 offset:25600\n"
"v_lshl_add_u32 v100, v102, 3, v100\n"
"v_mfma_f32_16x16x32_bf16 v[152:155], a[212:215], a[156:159], v[152:155]\n"
"ds_read_b64_tr_b16 a[244:245], v244 offset:25600\n"
"ds_read_b64_tr_b16 a[250:251], v245 offset:17920\n"
"v_mfma_f32_16x16x32_bf16 v[156:159], a[216:219], a[152:155], v[228:231]\n"
"v_cvt_pk_bf16_f32 v48, v48, v49\n"
"v_cvt_pk_bf16_f32 v43, v46, v47\n"
"v_mfma_f32_16x16x32_bf16 v[156:159], a[220:223], a[156:159], v[156:159]\n"
"buffer_load_dwordx4 v227, s[28:31], s40 offen offset:3072 lds\n"
"v_mfma_f32_16x16x32_bf16 v[172:175], a[216:219], a[160:163], v[228:231]\n"
"s_cmp_eq_u32 s47, 8\n"
"v_cvt_pk_bf16_f32 v75, v78, v79\n"
"v_cvt_pk_bf16_f32 v27, v30, v31\n"
"v_mfma_f32_16x16x32_bf16 v[172:175], a[220:223], a[164:167], v[172:175]\n"
"s_cselect_b32 s41, s52, s58\n"
"v_cvt_pk_bf16_f32 v56, v56, v57\n"
"v_permlane16_swap_b32 v1, v3\n"
"v_cvt_pk_bf16_f32 v11, v14, v15\n"
"v_cvt_pk_bf16_f32 v26, v28, v29\n"
"v_cvt_pk_bf16_f32 v33, v34, v35\n"
"v_mfma_f32_16x16x32_bf16 v[168:171], a[208:211], a[160:163], v[228:231]\n"
"s_add_u32 s48, s48, s41\n"
"ds_read_b128 a[176:179], v250 offset:2048\n"
"v_cvt_pk_bf16_f32 v83, v86, v87\n"
"v_mfma_f32_16x16x32_bf16 v[168:171], a[212:215], a[164:167], v[168:171]\n"
"s_sub_u32 s41, s48, 5242880\n"
"ds_read_b64_tr_b16 a[230:231], v244 offset:25088\n"
"ds_read_b64_tr_b16 a[248:249], v245 offset:17408\n"
"v_mfma_f32_16x16x32_bf16 v[164:167], a[200:203], a[160:163], v[228:231]\n"
"s_min_u32 s48, s48, s41\n"
"ds_read_b64_tr_b16 a[232:233], v245 offset:16384\n"
"v_xor_b32_e64 v102, s42, 1\n"
"v_mfma_f32_16x16x32_bf16 v[164:167], a[204:207], a[164:167], v[164:167]\n"
"ds_read_b128 a[184:187], v250 offset:4096\n"
"v_lshl_add_u32 v100, v102, 10, v100\n"
"v_mfma_f32_16x16x32_bf16 v[160:163], a[192:195], a[160:163], v[228:231]\n"
"s_mov_b32 m0, s50\n"
"v_permlane16_swap_b32 v9, v11\n"
"v_mfma_f32_16x16x32_bf16 v[160:163], a[196:199], a[164:167], v[160:163]\n"
"buffer_load_dwordx4 v224, s[28:31], s48 offen lds\n"
"v_mfma_f32_16x16x32_bf16 v[176:179], a[192:195], a[168:171], v[228:231]\n"
"s_waitcnt lgkmcnt(5)\n"
"v_cvt_pk_bf16_f32 v49, v50, v51\n"
"v_cvt_pk_bf16_f32 v34, v36, v37\n"
"v_mfma_f32_16x16x32_bf16 v[176:179], a[196:199], a[172:175], v[176:179]\n"
"v_cvt_pk_bf16_f32 v10, v12, v13\n"
"v_and_b32 v102, 0xf0, v242\n"
"v_mfma_f32_16x16x32_bf16 v[180:183], a[200:203], a[168:171], v[228:231]\n"
"ds_read_b64_tr_b16 a[240:241], v244 offset:17408\n"
"ds_read_b64_tr_b16 a[254:255], v245 offset:26112\n"
"v_mfma_f32_16x16x32_bf16 v[180:183], a[204:207], a[172:175], v[180:183]\n"
"buffer_load_dwordx4 v225, s[28:31], s48 offen offset:1024 lds\n"
"v_mfma_f32_16x16x32_bf16 v[184:187], a[208:211], a[168:171], v[228:231]\n"
"v_cvt_pk_bf16_f32 v90, v92, v93\n"
"v_cvt_pk_bf16_f32 v42, v44, v45\n"
"v_mfma_f32_16x16x32_bf16 v[184:187], a[212:215], a[172:175], v[184:187]\n"
"v_permlane16_swap_b32 v0, v2\n"
"v_mfma_f32_16x16x32_bf16 v[188:191], a[216:219], a[168:171], v[228:231]\n"
"ds_read_b128 a[188:191], v251 offset:4096\n"
"v_accvgpr_write_b32 a51, v3\n"
"v_mfma_f32_16x16x32_bf16 v[188:191], a[220:223], a[172:175], v[188:191]\n"
"s_waitcnt lgkmcnt(3)\n"
"ds_read_b64_tr_b16 a[238:239], v245 offset:25088\n"
"v_cvt_pk_bf16_f32 v67, v70, v71\n"
"v_mfma_f32_16x16x32_bf16 v[204:207], a[216:219], a[176:179], v[228:231]\n"
"ds_read_b64_tr_b16 a[246:247], v244 offset:26112\n"
"v_add_u32 v100, 94208, v100\n"
"v_mfma_f32_16x16x32_bf16 v[204:207], a[220:223], a[180:183], v[204:207]\n"
"buffer_load_dwordx4 v226, s[28:31], s48 offen offset:2048 lds\n"
"v_mfma_f32_16x16x32_bf16 v[200:203], a[208:211], a[176:179], v[228:231]\n"
"v_accvgpr_write_b32 a50, v2\n"
"v_lshl_add_u32 v101, v101, 8, v102\n"
"v_mfma_f32_16x16x32_bf16 v[200:203], a[212:215], a[180:183], v[200:203]\n"
"v_permlane16_swap_b32 v24, v26\n"
"v_mfma_f32_16x16x32_bf16 v[196:199], a[200:203], a[176:179], v[228:231]\n"
"ds_read_b64_tr_b16 a[228:229], v244 offset:24576\n"
"v_cvt_pk_bf16_f32 v106, v108, v109\n"
"v_mfma_f32_16x16x32_bf16 v[196:199], a[204:207], a[180:183], v[196:199]\n"
"ds_write2st64_b64 v100, v[96:97], v[98:99] offset0:120 offset1:121\n"
"v_accvgpr_write_b32 a49, v1\n"
"v_cvt_pk_bf16_f32 v19, v22, v23\n"
"v_permlane16_swap_b32 v25, v27\n"
"v_cvt_pk_bf16_f32 v35, v38, v39\n"
"v_mfma_f32_16x16x32_bf16 v[192:195], a[192:195], a[176:179], v[228:231]\n"
"v_cvt_pk_bf16_f32 v74, v76, v77\n"
"v_cvt_pk_bf16_f32 v18, v20, v21\n"
"v_mfma_f32_16x16x32_bf16 v[192:195], a[196:199], a[180:183], v[192:195]\n"
"buffer_load_dwordx4 v227, s[28:31], s48 offen offset:3072 lds\n"
"v_mfma_f32_16x16x32_bf16 v[208:211], a[192:195], a[184:187], v[228:231]\n"
"s_waitcnt lgkmcnt(2)\n"
"s_xor_b32 s50, s50, s74\n"
"v_permlane16_swap_b32 v33, v35\n"
"v_mfma_f32_16x16x32_bf16 v[208:211], a[196:199], a[188:191], v[208:211]\n"
"v_permlane16_swap_b32 v40, v42\n"
"v_mfma_f32_16x16x32_bf16 v[212:215], a[200:203], a[184:187], v[228:231]\n"
"v_permlane16_swap_b32 v17, v19\n"
"v_mfma_f32_16x16x32_bf16 v[212:215], a[204:207], a[188:191], v[212:215]\n"
"ds_read_b64_tr_b16 a[242:243], v244 offset:17920\n"
"v_cvt_pk_bf16_f32 v107, v110, v111\n"
"v_mfma_f32_16x16x32_bf16 v[216:219], a[208:211], a[184:187], v[228:231]\n"
"v_permlane16_swap_b32 v16, v18\n"
"v_mfma_f32_16x16x32_bf16 v[216:219], a[212:215], a[188:191], v[216:219]\n"
"ds_write2st64_b64 v100, v[104:105], v[106:107] offset0:128 offset1:129\n"
"v_cvt_pk_bf16_f32 v82, v84, v85\n"
"v_accvgpr_write_b32 a75, v27\n"
"v_permlane16_swap_b32 v41, v43\n"
"v_mfma_f32_16x16x32_bf16 v[220:223], a[216:219], a[184:187], v[228:231]\n"
"v_accvgpr_write_b32 a48, v0\n"
"v_accvgpr_write_b32 a64, v16\n"
"v_mfma_f32_16x16x32_bf16 v[220:223], a[220:223], a[188:191], v[220:223]\n"
"ds_write2st64_b64 v100, v[80:81], v[82:83] offset0:88 offset1:89\n"
"v_cvt_pk_bf16_f32 v57, v58, v59\n"
"v_cvt_pk_bf16_f32 v58, v60, v61\n"
"v_permlane16_swap_b32 v8, v10\n"
"v_accvgpr_write_b32 a57, v9\n"
"v_mfma_f32_16x16x32_bf16 v[0:3], a[224:227], a[136:139], v[228:231]\n"
"s_waitcnt lgkmcnt(3)\n"
"v_cvt_pk_bf16_f32 v50, v52, v53\n"
"v_xor_b32 v245, s74, v245\n"
"v_mfma_f32_16x16x32_bf16 v[0:3], a[228:231], a[140:143], v[0:3]\n"
"ds_write2st64_b64 v100, v[72:73], v[74:75] offset0:64 offset1:65\n"
"v_cvt_pk_bf16_f32 v59, v62, v63\n"
"v_accvgpr_write_b32 a58, v10\n"
"v_accvgpr_write_b32 a66, v18\n"
"v_accvgpr_write_b32 a81, v33\n"
"v_mfma_f32_16x16x32_bf16 v[4:7], a[232:235], a[136:139], v[228:231]\n"
"s_waitcnt vmcnt(8) lgkmcnt(3)\n"
"v_permlane16_swap_b32 v57, v59\n"
"v_mfma_f32_16x16x32_bf16 v[4:7], a[236:239], a[140:143], v[4:7]\n"
"s_barrier\n"
"ds_write2st64_b64 v100, v[88:89], v[90:91] offset0:96 offset1:97\n"
"v_accvgpr_write_b32 a56, v8\n"
"v_accvgpr_write_b32 a59, v11\n"
"v_accvgpr_write_b32 a72, v24\n"
"v_mfma_f32_16x16x32_bf16 v[8:11], a[240:243], a[136:139], v[228:231]\n"
"s_mov_b32 m0, s51\n"
"ds_read_b64_tr_b16 a[202:203], v245 offset:512\n"
"v_accvgpr_write_b32 a73, v25\n"
"v_mfma_f32_16x16x32_bf16 v[8:11], a[244:247], a[140:143], v[8:11]\n"
"buffer_load_dwordx4 v248, s[24:27], s49 offen lds\n"
"v_mfma_f32_16x16x32_bf16 v[12:15], a[248:251], a[136:139], v[228:231]\n"
"v_accvgpr_write_b32 a65, v17\n"
"v_accvgpr_write_b32 a67, v19\n"
"v_mfma_f32_16x16x32_bf16 v[12:15], a[252:255], a[140:143], v[12:15]\n"
"v_permlane16_swap_b32 v56, v58\n"
"v_mfma_f32_16x16x32_bf16 v[28:31], a[248:251], a[144:147], v[228:231]\n"
"v_accvgpr_write_b32 a74, v26\n"
"v_accvgpr_write_b32 a83, v35\n"
"v_mfma_f32_16x16x32_bf16 v[28:31], a[252:255], a[148:151], v[28:31]\n"
"ds_write2st64_b64 v100, v[64:65], v[66:67] offset0:0 offset1:1\n"
"v_permlane16_swap_b32 v32, v34\n"
"v_accvgpr_write_b32 a88, v40\n"
"v_mfma_f32_16x16x32_bf16 v[24:27], a[240:243], a[144:147], v[228:231]\n"
"v_accvgpr_write_b32 a89, v41\n"
"v_accvgpr_write_b32 a90, v42\n"
"v_mfma_f32_16x16x32_bf16 v[24:27], a[244:247], a[148:151], v[24:27]\n"
"buffer_load_dwordx4 v249, s[24:27], s49 offen offset:1024 lds\n"
"v_mfma_f32_16x16x32_bf16 v[20:23], a[232:235], a[144:147], v[228:231]\n"
"v_accvgpr_write_b32 a82, v34\n"
"v_accvgpr_write_b32 a91, v43\n"
"v_mfma_f32_16x16x32_bf16 v[20:23], a[236:239], a[148:151], v[20:23]\n"
"v_cvt_pk_bf16_f32 v51, v54, v55\n"
"v_accvgpr_write_b32 a80, v32\n"
"v_mfma_f32_16x16x32_bf16 v[16:19], a[224:227], a[144:147], v[228:231]\n"
"ds_read_b64_tr_b16 a[216:217], v245 offset:1024\n"
"v_xor_b32 v247, s74, v247\n"
"v_mfma_f32_16x16x32_bf16 v[16:19], a[228:231], a[148:151], v[16:19]\n"
"ds_write_b128 v243, v[56:59] offset:6144\n"
"v_mfma_f32_16x16x32_bf16 v[32:35], a[224:227], a[152:155], v[228:231]\n"
"v_permlane16_swap_b32 v48, v50\n"
"v_mfma_f32_16x16x32_bf16 v[32:35], a[228:231], a[156:159], v[32:35]\n"
"buffer_load_dwordx4 v254, s[24:27], s49 offen offset:2048 lds\n"
"v_mfma_f32_16x16x32_bf16 v[36:39], a[232:235], a[152:155], v[228:231]\n"
"s_waitcnt vmcnt(63) lgkmcnt(2)\n"
"v_permlane16_swap_b32 v49, v51\n"
"v_mfma_f32_16x16x32_bf16 v[36:39], a[236:239], a[156:159], v[36:39]\n"
"s_barrier\n"
"v_mfma_f32_16x16x32_bf16 v[40:43], a[240:243], a[152:155], v[228:231]\n"
"ds_read_b64_tr_b16 a[204:205], v245 offset:8192\n"
"ds_read_b128 a[140:143], v247 offset:0\n"
"v_mfma_f32_16x16x32_bf16 v[40:43], a[244:247], a[156:159], v[40:43]\n"
"ds_write_b128 v243, v[48:51] offset:4096\n"
"v_mfma_f32_16x16x32_bf16 v[44:47], a[248:251], a[152:155], v[228:231]\n"
"v_bfe_u32 v102, v242, 8, 1\n"
"v_lshl_add_u32 v101, v102, 9, v101\n"
"v_mfma_f32_16x16x32_bf16 v[44:47], a[252:255], a[156:159], v[44:47]\n"
"buffer_load_dwordx4 v255, s[24:27], s49 offen offset:3072 lds\n"
"v_mfma_f32_16x16x32_bf16 v[60:63], a[248:251], a[160:163], v[228:231]\n"
"v_xor_b32 v244, s74, v244\n"
"v_xor_b32_e64 v102, s42, 0\n"
"v_mfma_f32_16x16x32_bf16 v[60:63], a[252:255], a[164:167], v[60:63]\n"
"v_lshl_add_u32 v101, v102, 10, v101\n"
"v_add_u32 v101, 94208, v101\n"
"v_mfma_f32_16x16x32_bf16 v[56:59], a[240:243], a[160:163], v[228:231]\n"
"ds_read_b64_tr_b16 a[198:199], v244 offset:8704\n"
"ds_read_b64_tr_b16 a[220:221], v245 offset:9216\n"
"v_mfma_f32_16x16x32_bf16 v[56:59], a[244:247], a[164:167], v[56:59]\n"
"s_add_u32 s40, s49, 40960\n"
"ds_read_b128 a[52:55], v101 offset:0\n"
"v_xor_b32 v253, s74, v253\n"
"v_mfma_f32_16x16x32_bf16 v[52:55], a[232:235], a[160:163], v[228:231]\n"
"s_add_u32 m0, s51, 4096\n"
"ds_read_b64_tr_b16 a[206:207], v245 offset:8704\n"
"ds_read_b128 a[76:79], v101 offset:49152\n"
"v_mfma_f32_16x16x32_bf16 v[52:55], a[236:239], a[164:167], v[52:55]\n"
"buffer_load_dwordx4 v248, s[24:27], s40 offen lds\n"
"v_mfma_f32_16x16x32_bf16 v[48:51], a[224:227], a[160:163], v[228:231]\n"
"v_xor_b32 v246, s74, v246\n"
"v_add_u32 v102, s75, v101\n"
"v_mfma_f32_16x16x32_bf16 v[48:51], a[228:231], a[164:167], v[48:51]\n"
"ds_read_b64_tr_b16 a[192:193], v244 offset:0\n"
"ds_read_b128 a[68:71], v101 offset:45056\n"
"v_mfma_f32_16x16x32_bf16 v[64:67], a[224:227], a[168:171], v[228:231]\n"
"ds_read_b64_tr_b16 a[212:213], v244 offset:9216\n"
"ds_read_b128 a[164:167], v253 offset:0\n"
"v_mfma_f32_16x16x32_bf16 v[64:67], a[228:231], a[172:175], v[64:67]\n"
"ds_read_b64_tr_b16 a[208:209], v244 offset:1024\n"
"ds_read_b64_tr_b16 a[218:219], v245 offset:1536\n"
"v_mfma_f32_16x16x32_bf16 v[68:71], a[232:235], a[168:171], v[228:231]\n"
"ds_read_b64_tr_b16 a[200:201], v245 offset:0\n"
"ds_read_b128 a[84:87], v101 offset:61440\n"
"v_mfma_f32_16x16x32_bf16 v[68:71], a[236:239], a[172:175], v[68:71]\n"
"buffer_load_dwordx4 v249, s[24:27], s40 offen offset:1024 lds\n"
"v_mfma_f32_16x16x32_bf16 v[72:75], a[240:243], a[168:171], v[228:231]\n"
"ds_read_b64_tr_b16 a[214:215], v244 offset:9728\n"
"ds_read_b128 a[60:63], v101 offset:32768\n"
"v_mfma_f32_16x16x32_bf16 v[72:75], a[244:247], a[172:175], v[72:75]\n"
"v_xor_b32 v250, s74, v250\n"
"v_xor_b32 v251, s74, v251\n"
"v_mfma_f32_16x16x32_bf16 v[76:79], a[248:251], a[168:171], v[228:231]\n"
"ds_read_b128 a[92:95], v102 offset:0\n"
"ds_read_b128 a[168:171], v250 offset:0\n"
"v_mfma_f32_16x16x32_bf16 v[76:79], a[252:255], a[172:175], v[76:79]\n"
"ds_read_b128 a[152:155], v246 offset:4096\n"
"v_xor_b32 v252, s74, v252\n"
"v_mfma_f32_16x16x32_bf16 v[92:95], a[248:251], a[176:179], v[228:231]\n"
"ds_read_b64_tr_b16 a[222:223], v245 offset:9728\n"
"ds_read_b128 a[160:163], v252 offset:0\n"
"v_mfma_f32_16x16x32_bf16 v[92:95], a[252:255], a[180:183], v[92:95]\n"
"buffer_load_dwordx4 v254, s[24:27], s40 offen offset:2048 lds\n"
"v_mfma_f32_16x16x32_bf16 v[88:91], a[240:243], a[176:179], v[228:231]\n"
"s_add_u32 s49, s49, 128\n"
"ds_read_b128 a[144:147], v246 offset:2048\n"
"ds_read_b128 a[148:151], v247 offset:2048\n"
"v_mfma_f32_16x16x32_bf16 v[88:91], a[244:247], a[180:183], v[88:91]\n"
"s_sub_u32 s41, s49, 1280\n"
"ds_read_b128 a[136:139], v246 offset:0\n"
"v_mfma_f32_16x16x32_bf16 v[84:87], a[232:235], a[176:179], v[228:231]\n"
"s_min_u32 s49, s49, s41\n"
"ds_read_b64_tr_b16 a[196:197], v244 offset:8192\n"
"ds_read_b64_tr_b16 a[210:211], v244 offset:1536\n"
"v_mfma_f32_16x16x32_bf16 v[84:87], a[236:239], a[180:183], v[84:87]\n"
"s_xor_b32 s51, s51, s74\n"
"ds_read_b64_tr_b16 a[194:195], v244 offset:512\n"
"ds_read_b128 a[156:159], v247 offset:4096\n"
"v_mfma_f32_16x16x32_bf16 v[80:83], a[224:227], a[176:179], v[228:231]\n"
"ds_read_b128 a[176:179], v250 offset:2048\n"
"v_mfma_f32_16x16x32_bf16 v[80:83], a[228:231], a[180:183], v[80:83]\n"
"ds_read_b128 a[172:175], v251 offset:0\n"
"ds_read_b128 a[180:183], v251 offset:2048\n"
"v_mfma_f32_16x16x32_bf16 v[96:99], a[224:227], a[184:187], v[228:231]\n"
"v_mfma_f32_16x16x32_bf16 v[96:99], a[228:231], a[188:191], v[96:99]\n"
"v_mfma_f32_16x16x32_bf16 v[100:103], a[232:235], a[184:187], v[228:231]\n"
"v_mfma_f32_16x16x32_bf16 v[100:103], a[236:239], a[188:191], v[100:103]\n"
"v_mfma_f32_16x16x32_bf16 v[104:107], a[240:243], a[184:187], v[228:231]\n"
"v_mfma_f32_16x16x32_bf16 v[104:107], a[244:247], a[188:191], v[104:107]\n"
"v_mfma_f32_16x16x32_bf16 v[108:111], a[248:251], a[184:187], v[228:231]\n"
"s_add_u32 s47, s47, 1\n"
"ds_read_b128 a[184:187], v250 offset:4096\n"
"v_mfma_f32_16x16x32_bf16 v[108:111], a[252:255], a[188:191], v[108:111]\n"
"s_waitcnt lgkmcnt(1)\n"
"ds_read_b128 a[188:191], v251 offset:4096\n"
"s_mov_b32 s62, 18\n"
"s_branch LPE_%=\n"
"LPT2_%=:\n"
"v_mfma_f32_16x16x32_bf16 v[112:115], a[192:195], a[136:139], v[112:115]\n"
"s_add_u32 s78, s32, s96\ns_addc_u32 s79, s33, 0\n"
"s_mov_b64 s[80:81], 0\n"
"s_mov_b32 s82, 0\n"
"v_mfma_f32_16x16x32_bf16 v[112:115], a[196:199], a[140:143], v[112:115]\n"
"v_bfe_i32 v241, v242, 4, 1\n"
"v_mfma_f32_16x16x32_bf16 v[116:119], a[200:203], a[136:139], v[116:119]\n"
"s_waitcnt vmcnt(11)\n"
"s_load_dword s80, s[78:79], 0\n"
"v_mfma_f32_16x16x32_bf16 v[116:119], a[204:207], a[140:143], v[116:119]\n"
"s_barrier\n"
"v_mfma_f32_16x16x32_bf16 v[120:123], a[208:211], a[136:139], v[120:123]\n"
"s_add_u32 s40, s48, 256\n"
"s_xor_b32 m0, s50, 0xc000\n"
"ds_read_b64_tr_b16 a[238:239], v245 offset:25088\n"
"v_mfma_f32_16x16x32_bf16 v[120:123], a[212:215], a[140:143], v[120:123]\n"
"buffer_load_dwordx4 v224, s[28:31], s40 offen lds\n"
"v_mfma_f32_16x16x32_bf16 v[124:127], a[216:219], a[136:139], v[124:127]\n"
"v_mfma_f32_16x16x32_bf16 v[124:127], a[220:223], a[140:143], v[124:127]\n"
"v_mfma_f32_16x16x32_bf16 v[140:143], a[216:219], a[144:147], v[140:143]\n"
"ds_read_b64_tr_b16 a[230:231], v244 offset:25088\n"
"v_bfe_u32 v228, v242, 8, 2\n"
"v_mfma_f32_16x16x32_bf16 v[140:143], a[220:223], a[148:151], v[140:143]\n"
"v_mfma_f32_16x16x32_bf16 v[136:139], a[208:211], a[144:147], v[136:139]\n"
"ds_read_b64_tr_b16 a[252:253], v245 offset:25600\n"
"v_mfma_f32_16x16x32_bf16 v[136:139], a[212:215], a[148:151], v[136:139]\n"
"buffer_load_dwordx4 v225, s[28:31], s40 offen offset:1024 lds\n"
"v_mfma_f32_16x16x32_bf16 v[132:135], a[200:203], a[144:147], v[132:135]\n"
"s_add_u32 s82, s82, s80\n"
"v_mfma_f32_16x16x32_bf16 v[132:135], a[204:207], a[148:151], v[132:135]\n"
"s_add_u32 s82, s82, s80\n"
"v_mfma_f32_16x16x32_bf16 v[128:131], a[192:195], a[144:147], v[128:131]\n"
"s_add_u32 s82, s82, s80\n"
"ds_read_b64_tr_b16 a[242:243], v244 offset:17920\n"
"v_mfma_f32_16x16x32_bf16 v[128:131], a[196:199], a[148:151], v[128:131]\n"
"s_add_u32 s82, s82, s80\n"
"v_mfma_f32_16x16x32_bf16 v[144:147], a[192:195], a[152:155], v[144:147]\n"
"s_add_u32 s82, s82, s80\n"
"ds_read_b64_tr_b16 a[248:249], v245 offset:17408\n"
"v_mfma_f32_16x16x32_bf16 v[144:147], a[196:199], a[156:159], v[144:147]\n"
"s_add_u32 s82, s82, s80\n"
"v_mfma_f32_16x16x32_bf16 v[148:151], a[200:203], a[152:155], v[148:151]\n"
"s_add_u32 s82, s82, s80\n"
"ds_read_b64_tr_b16 a[236:237], v245 offset:24576\n"
"v_mfma_f32_16x16x32_bf16 v[148:151], a[204:207], a[156:159], v[148:151]\n"
"s_add_u32 s82, s82, s80\n"
"buffer_load_dwordx4 v226, s[28:31], s40 offen offset:2048 lds\n"
"v_mfma_f32_16x16x32_bf16 v[152:155], a[208:211], a[152:155], v[152:155]\n"
"s_add_u32 s82, s82, s80\n"
"v_mfma_f32_16x16x32_bf16 v[152:155], a[212:215], a[156:159], v[152:155]\n"
"s_add_u32 s82, s82, s80\n"
"v_mfma_f32_16x16x32_bf16 v[156:159], a[216:219], a[152:155], v[156:159]\n"
"s_add_u32 s82, s82, s80\n"
"ds_read_b64_tr_b16 a[246:247], v244 offset:26112\n"
"v_mfma_f32_16x16x32_bf16 v[156:159], a[220:223], a[156:159], v[156:159]\n"
"s_add_u32 s82, s82, s80\n"
"v_mfma_f32_16x16x32_bf16 v[172:175], a[216:219], a[160:163], v[172:175]\n"
"s_add_u32 s82, s82, s80\n"
"ds_read_b64_tr_b16 a[228:229], v244 offset:24576\n"
"v_mfma_f32_16x16x32_bf16 v[172:175], a[220:223], a[164:167], v[172:175]\n"
"s_add_u32 s82, s82, s80\n"
"buffer_load_dwordx4 v227, s[28:31], s40 offen offset:3072 lds\n"
"v_mfma_f32_16x16x32_bf16 v[168:171], a[208:211], a[160:163], v[168:171]\n"
"s_add_u32 s82, s82, s80\n"
"s_mov_b32 s58, 106496\n"
"v_mfma_f32_16x16x32_bf16 v[168:171], a[212:215], a[164:167], v[168:171]\n"
"s_add_u32 s82, s82, s80\n"
"s_mov_b32 s47, 0\n"
"v_mfma_f32_16x16x32_bf16 v[164:167], a[200:203], a[160:163], v[164:167]\n"
"s_add_u32 s82, s82, s80\n"
"s_mov_b32 s52, -896\n"
"ds_read_b64_tr_b16 a[244:245], v244 offset:25600\n"
"v_mfma_f32_16x16x32_bf16 v[164:167], a[204:207], a[164:167], v[164:167]\n"
"s_load_dword s81, s[78:79], 0x2000\n"
"s_mov_b32 s53, 30720\n"
"v_mfma_f32_16x16x32_bf16 v[160:163], a[192:195], a[160:163], v[160:163]\n"
"s_mov_b32 m0, s44\n"
"ds_read_b64_tr_b16 a[250:251], v245 offset:17920\n"
"v_mfma_f32_16x16x32_bf16 v[160:163], a[196:199], a[164:167], v[160:163]\n"
"buffer_load_dwordx4 v240, s[16:19], s56 offen nt lds\n"
"v_mfma_f32_16x16x32_bf16 v[176:179], a[192:195], a[168:171], v[176:179]\n"
"v_mfma_f32_16x16x32_bf16 v[176:179], a[196:199], a[172:175], v[176:179]\n"
"v_mfma_f32_16x16x32_bf16 v[180:183], a[200:203], a[168:171], v[180:183]\n"
"ds_read_b64_tr_b16 a[232:233], v245 offset:16384\n"
"v_mfma_f32_16x16x32_bf16 v[180:183], a[204:207], a[172:175], v[180:183]\n"
"v_mfma_f32_16x16x32_bf16 v[184:187], a[208:211], a[168:171], v[184:187]\n"
"ds_read_b64_tr_b16 a[234:235], v245 offset:16896\n"
"v_mfma_f32_16x16x32_bf16 v[184:187], a[212:215], a[172:175], v[184:187]\n"
"v_mfma_f32_16x16x32_bf16 v[188:191], a[216:219], a[168:171], v[188:191]\n"
"s_add_u32 s40, s56, s52\n"
"ds_read_b64_tr_b16 a[226:227], v244 offset:16896\n"
"v_mfma_f32_16x16x32_bf16 v[188:191], a[220:223], a[172:175], v[188:191]\n"
"s_sub_u32 s82, s82, s81\n"
"buffer_load_dwordx4 v240, s[16:19], s40 offen offset:1024 nt lds\n"
"v_mfma_f32_16x16x32_bf16 v[204:207], a[216:219], a[176:179], v[204:207]\n"
"s_sub_u32 s82, s82, s81\n"
"v_and_b32 v229, 0x60, v242\n"
"v_mfma_f32_16x16x32_bf16 v[204:207], a[220:223], a[180:183], v[204:207]\n"
"s_sub_u32 s82, s82, s81\n"
"v_mfma_f32_16x16x32_bf16 v[200:203], a[208:211], a[176:179], v[200:203]\n"
"s_sub_u32 s82, s82, s81\n"
"ds_read_b64_tr_b16 a[224:225], v244 offset:16384\n"
"v_mfma_f32_16x16x32_bf16 v[200:203], a[212:215], a[180:183], v[200:203]\n"
"s_sub_u32 s82, s82, s81\n"
"v_lshlrev_b32 v228, 4, v228\n"
"v_mfma_f32_16x16x32_bf16 v[196:199], a[200:203], a[176:179], v[196:199]\n"
"s_sub_u32 s82, s82, s81\n"
"s_add_u32 s40, s56, s53\n"
"ds_read_b64_tr_b16 a[254:255], v245 offset:26112\n"
"v_mfma_f32_16x16x32_bf16 v[196:199], a[204:207], a[180:183], v[196:199]\n"
"s_sub_u32 s82, s82, s81\n"
"buffer_load_dwordx4 v240, s[16:19], s40 offen offset:2048 nt lds\n"
"v_mfma_f32_16x16x32_bf16 v[192:195], a[192:195], a[176:179], v[192:195]\n"
"s_sub_u32 s82, s82, s81\n"
"v_mfma_f32_16x16x32_bf16 v[192:195], a[196:199], a[180:183], v[192:195]\n"
"s_waitcnt lgkmcnt(2)\n"
"s_sub_u32 s82, s82, s81\n"
"v_mfma_f32_16x16x32_bf16 v[208:211], a[192:195], a[184:187], v[208:211]\n"
"s_sub_u32 s82, s82, s81\n"
"v_mfma_f32_16x16x32_bf16 v[208:211], a[196:199], a[188:191], v[208:211]\n"
"s_sub_u32 s82, s82, s81\n"
"ds_read_b64_tr_b16 a[240:241], v244 offset:17408\n"
"v_xor_b32 v250, s74, v250\n"
"v_mfma_f32_16x16x32_bf16 v[212:215], a[200:203], a[184:187], v[212:215]\n"
"s_sub_u32 s82, s82, s81\n"
"s_add_u32 s40, s56, s54\n"
"v_mfma_f32_16x16x32_bf16 v[212:215], a[204:207], a[188:191], v[212:215]\n"
"s_sub_u32 s82, s82, s81\n"
"buffer_load_dwordx4 v240, s[16:19], s40 offen offset:3072 nt lds\n"
"v_mfma_f32_16x16x32_bf16 v[216:219], a[208:211], a[184:187], v[216:219]\n"
"s_sub_u32 s82, s82, s81\n"
"v_mfma_f32_16x16x32_bf16 v[216:219], a[212:215], a[188:191], v[216:219]\n"
"s_sub_u32 s82, s82, s81\n"
"v_mfma_f32_16x16x32_bf16 v[220:223], a[216:219], a[184:187], v[220:223]\n"
"s_sub_u32 s82, s82, s81\n"
"v_xor_b32 v228, v228, v229\n"
"v_and_b32 v229, 0x70, v242\n"
"v_mfma_f32_16x16x32_bf16 v[220:223], a[220:223], a[188:191], v[220:223]\n"
"s_waitcnt lgkmcnt(1)\n"
"s_sub_u32 s82, s82, s81\n"
"v_mfma_f32_16x16x32_bf16 v[0:3], a[224:227], a[136:139], v[0:3]\n"
"v_xor_b32 v244, s74, v244\n"
"v_xor_b32 v247, s74, v247\n"
"v_mfma_f32_16x16x32_bf16 v[0:3], a[228:231], a[140:143], v[0:3]\n"
"v_xor_b32 v245, s74, v245\n"
"v_mfma_f32_16x16x32_bf16 v[4:7], a[232:235], a[136:139], v[4:7]\n"
"s_waitcnt vmcnt(8) lgkmcnt(0)\n"
"v_mfma_f32_16x16x32_bf16 v[4:7], a[236:239], a[140:143], v[4:7]\n"
"s_barrier\n"
"v_mfma_f32_16x16x32_bf16 v[8:11], a[240:243], a[136:139], v[8:11]\n"
"s_add_u32 m0, s44, 65536\n"
"ds_read_b64_tr_b16 a[198:199], v244 offset:8704\n"
"v_mfma_f32_16x16x32_bf16 v[8:11], a[244:247], a[140:143], v[8:11]\n"
"buffer_load_dwordx4 v240, s[20:23], s56 offen nt lds\n"
"v_mfma_f32_16x16x32_bf16 v[12:15], a[248:251], a[136:139], v[12:15]\n"
"s_lshr_b32 s82, s82, 31\n"
"v_xor_b32 v253, s74, v253\n"
"v_mfma_f32_16x16x32_bf16 v[12:15], a[252:255], a[140:143], v[12:15]\n"
"s_add_u32 s82, s82, s60\n"
"v_mfma_f32_16x16x32_bf16 v[28:31], a[248:251], a[144:147], v[28:31]\n"
"s_add_u32 s82, s82, 32\n"
"ds_read_b128 a[140:143], v247 offset:0\n"
"v_mfma_f32_16x16x32_bf16 v[28:31], a[252:255], a[148:151], v[28:31]\n"
"v_mov_b32 v254, s82\n"
"v_lshl_add_u32 v228, v229, 3, v228\n"
"v_mfma_f32_16x16x32_bf16 v[24:27], a[240:243], a[144:147], v[24:27]\n"
"v_lshlrev_b32_e64 v255, 2, s93\n"
"ds_read_b64_tr_b16 a[192:193], v244 offset:0\n"
"v_mfma_f32_16x16x32_bf16 v[24:27], a[244:247], a[148:151], v[24:27]\n"
"ds_read_b64_tr_b16 a[204:205], v245 offset:8192\n"
"v_mfma_f32_16x16x32_bf16 v[20:23], a[232:235], a[144:147], v[20:23]\n"
"s_add_u32 s40, s56, s52\n"
"ds_read_b64_tr_b16 a[222:223], v245 offset:9728\n"
"v_mfma_f32_16x16x32_bf16 v[20:23], a[236:239], a[148:151], v[20:23]\n"
"buffer_load_dwordx4 v240, s[20:23], s40 offen offset:1024 nt lds\n"
"v_mfma_f32_16x16x32_bf16 v[16:19], a[224:227], a[144:147], v[16:19]\n"
"v_and_b32 v229, 0x80, v242\n"
"v_mfma_f32_16x16x32_bf16 v[16:19], a[228:231], a[148:151], v[16:19]\n"
"s_mov_b64 exec, 1\n"
"global_store_dword v255, v254, s[6:7] offset:256 sc1\n"
"s_mov_b64 exec, -1\n"
"v_mfma_f32_16x16x32_bf16 v[32:35], a[224:227], a[152:155], v[32:35]\n"
"ds_read_b64_tr_b16 a[202:203], v245 offset:512\n"
"v_mfma_f32_16x16x32_bf16 v[32:35], a[228:231], a[156:159], v[32:35]\n"
"ds_read_b64_tr_b16 a[194:195], v244 offset:512\n"
"v_lshl_add_u32 v228, v229, 4, v228\n"
"v_mfma_f32_16x16x32_bf16 v[36:39], a[232:235], a[152:155], v[36:39]\n"
"s_add_u32 s40, s56, s53\n"
"ds_read_b64_tr_b16 a[210:211], v244 offset:1536\n"
"v_xor_b32 v252, s74, v252\n"
"v_mfma_f32_16x16x32_bf16 v[36:39], a[236:239], a[156:159], v[36:39]\n"
"buffer_load_dwordx4 v240, s[20:23], s40 offen offset:2048 nt lds\n"
"v_mfma_f32_16x16x32_bf16 v[40:43], a[240:243], a[152:155], v[40:43]\n"
"v_mfma_f32_16x16x32_bf16 v[40:43], a[244:247], a[156:159], v[40:43]\n"
"v_mfma_f32_16x16x32_bf16 v[44:47], a[248:251], a[152:155], v[44:47]\n"
"ds_read_b64_tr_b16 a[208:209], v244 offset:1024\n"
"v_mfma_f32_16x16x32_bf16 v[44:47], a[252:255], a[156:159], v[44:47]\n"
"ds_read_b64_tr_b16 a[218:219], v245 offset:1536\n"
"v_mfma_f32_16x16x32_bf16 v[60:63], a[248:251], a[160:163], v[60:63]\n"
"s_add_u32 s40, s56, s54\n"
"ds_read_b64_tr_b16 a[200:201], v245 offset:0\n"
"v_mfma_f32_16x16x32_bf16 v[60:63], a[252:255], a[164:167], v[60:63]\n"
"buffer_load_dwordx4 v240, s[20:23], s40 offen offset:3072 nt lds\n"
"v_mfma_f32_16x16x32_bf16 v[56:59], a[240:243], a[160:163], v[56:59]\n"
"s_add_u32 s55, s55, s59\n"
"v_mfma_f32_16x16x32_bf16 v[56:59], a[244:247], a[164:167], v[56:59]\n"
"s_or_b32 s55, s55, s58\n"
"v_mfma_f32_16x16x32_bf16 v[52:55], a[232:235], a[160:163], v[52:55]\n"
"s_xor_b32 s56, s55, s57\n"
"ds_read_b64_tr_b16 a[206:207], v245 offset:8704\n"
"v_mov_b32 v229, s43\n"
"v_mfma_f32_16x16x32_bf16 v[52:55], a[236:239], a[164:167], v[52:55]\n"
"v_mul_u32_u24 v229, 0x81, v229\n"
"v_xor_b32 v246, s74, v246\n"
"v_mfma_f32_16x16x32_bf16 v[48:51], a[224:227], a[160:163], v[48:51]\n"
"s_add_u32 m0, s44, 16384\n"
"ds_read_b128 a[136:139], v246 offset:0\n"
"v_mfma_f32_16x16x32_bf16 v[48:51], a[228:231], a[164:167], v[48:51]\n"
"buffer_load_dwordx4 v240, s[16:19], s56 offen nt lds\n"
"v_mfma_f32_16x16x32_bf16 v[64:67], a[224:227], a[168:171], v[64:67]\n"
"v_mfma_f32_16x16x32_bf16 v[64:67], a[228:231], a[172:175], v[64:67]\n"
"v_and_b32 v254, 0x20, v242\n"
"v_mfma_f32_16x16x32_bf16 v[68:71], a[232:235], a[168:171], v[68:71]\n"
"ds_read_b64_tr_b16 a[216:217], v245 offset:1024\n"
"v_xor_b32 v251, s74, v251\n"
"v_mfma_f32_16x16x32_bf16 v[68:71], a[236:239], a[172:175], v[68:71]\n"
"v_mfma_f32_16x16x32_bf16 v[72:75], a[240:243], a[168:171], v[72:75]\n"
"ds_read_b128 a[148:151], v247 offset:2048\n"
"v_mfma_f32_16x16x32_bf16 v[72:75], a[244:247], a[172:175], v[72:75]\n"
"v_add_u32 v254, -1, v254\n"
"v_mfma_f32_16x16x32_bf16 v[76:79], a[248:251], a[168:171], v[76:79]\n"
"s_add_u32 s40, s56, s52\n"
"ds_read_b64_tr_b16 a[220:221], v245 offset:9216\n"
"v_mfma_f32_16x16x32_bf16 v[76:79], a[252:255], a[172:175], v[76:79]\n"
"buffer_load_dwordx4 v240, s[16:19], s40 offen offset:1024 nt lds\n"
"v_mfma_f32_16x16x32_bf16 v[92:95], a[248:251], a[176:179], v[92:95]\n"
"v_mfma_f32_16x16x32_bf16 v[92:95], a[252:255], a[180:183], v[92:95]\n"
"v_mfma_f32_16x16x32_bf16 v[88:91], a[240:243], a[176:179], v[88:91]\n"
"ds_read_b64_tr_b16 a[212:213], v244 offset:9216\n"
"v_mfma_f32_16x16x32_bf16 v[88:91], a[244:247], a[180:183], v[88:91]\n"
"ds_read_b64_tr_b16 a[214:215], v244 offset:9728\n"
"v_mfma_f32_16x16x32_bf16 v[84:87], a[232:235], a[176:179], v[84:87]\n"
"s_add_u32 s40, s56, s53\n"
"ds_read_b64_tr_b16 a[196:197], v244 offset:8192\n"
"v_mfma_f32_16x16x32_bf16 v[84:87], a[236:239], a[180:183], v[84:87]\n"
"buffer_load_dwordx4 v240, s[16:19], s40 offen offset:2048 nt lds\n"
"v_mfma_f32_16x16x32_bf16 v[80:83], a[224:227], a[176:179], v[80:83]\n"
"v_mfma_f32_16x16x32_bf16 v[80:83], a[228:231], a[180:183], v[80:83]\n"
"v_mfma_f32_16x16x32_bf16 v[96:99], a[224:227], a[184:187], v[96:99]\n"
"ds_read_b128 a[172:175], v251 offset:0\n"
"v_mfma_f32_16x16x32_bf16 v[96:99], a[228:231], a[188:191], v[96:99]\n"
"v_xor_b32 v228, v228, v229\n"
"v_mfma_f32_16x16x32_bf16 v[100:103], a[232:235], a[184:187], v[100:103]\n"
"s_add_u32 s40, s56, s54\n"
"ds_read_b128 a[168:171], v250 offset:0\n"
"v_mfma_f32_16x16x32_bf16 v[100:103], a[236:239], a[188:191], v[100:103]\n"
"buffer_load_dwordx4 v240, s[16:19], s40 offen offset:3072 nt lds\n"
"v_mfma_f32_16x16x32_bf16 v[104:107], a[240:243], a[184:187], v[104:107]\n"
"v_mfma_f32_16x16x32_bf16 v[104:107], a[244:247], a[188:191], v[104:107]\n"
"v_mfma_f32_16x16x32_bf16 v[108:111], a[248:251], a[184:187], v[108:111]\n"
"ds_read_b128 a[180:183], v251 offset:2048\n"
"v_mfma_f32_16x16x32_bf16 v[108:111], a[252:255], a[188:191], v[108:111]\n"
"s_waitcnt lgkmcnt(3)\n"
"v_mfma_f32_16x16x32_bf16 v[112:115], a[192:195], a[136:139], v[112:115]\n"
"ds_read_b128 a[176:179], v250 offset:2048\n"
"v_and_b32 v249, 0xf0, v242\n"
"v_mfma_f32_16x16x32_bf16 v[112:115], a[196:199], a[140:143], v[112:115]\n"
"v_bfe_u32 v255, v242, 9, 1\n"
"v_and_b32 v241, 0xaaaaaaaa, v241\n"
"v_mfma_f32_16x16x32_bf16 v[116:119], a[200:203], a[136:139], v[116:119]\n"
"s_waitcnt vmcnt(13)\n"
"ds_read_b128 a[144:147], v246 offset:2048\n"
"v_lshl_add_u32 v255, v255, 8, v249\n"
"v_mfma_f32_16x16x32_bf16 v[116:119], a[204:207], a[140:143], v[116:119]\n"
"s_barrier\n"
"v_mfma_f32_16x16x32_bf16 v[120:123], a[208:211], a[136:139], v[120:123]\n"
"s_add_u32 m0, s44, 81920\n"
"ds_read_b128 a[184:187], v250 offset:4096\n"
"v_mfma_f32_16x16x32_bf16 v[120:123], a[212:215], a[140:143], v[120:123]\n"
"buffer_load_dwordx4 v240, s[20:23], s56 offen nt lds\n"
"v_mfma_f32_16x16x32_bf16 v[124:127], a[216:219], a[136:139], v[124:127]\n"
"v_or_b32 v241, 0x44444444, v241\n"
"v_mfma_f32_16x16x32_bf16 v[124:127], a[220:223], a[140:143], v[124:127]\n"
"s_waitcnt lgkmcnt(3)\n"
"v_mfma_f32_16x16x32_bf16 v[188:191], a[216:219], a[168:171], v[188:191]\n"
"ds_read_b128 a[188:191], v251 offset:4096\n"
"v_mfma_f32_16x16x32_bf16 v[188:191], a[220:223], a[172:175], v[188:191]\n"
"v_mfma_f32_16x16x32_bf16 v[184:187], a[208:211], a[168:171], v[184:187]\n"
"ds_read_b128 a[160:163], v252 offset:0\n"
"v_ashrrev_i32 v254, 31, v254\n"
"v_mfma_f32_16x16x32_bf16 v[184:187], a[212:215], a[172:175], v[184:187]\n"
"v_xor_b32 v229, 64, v228\n"
"v_bfe_u32 v249, v242, 8, 1\n"
"v_mfma_f32_16x16x32_bf16 v[180:183], a[200:203], a[168:171], v[180:183]\n"
"ds_read_b128 a[164:167], v253 offset:0\n"
"v_mfma_f32_16x16x32_bf16 v[180:183], a[204:207], a[172:175], v[180:183]\n"
"v_lshl_add_u32 v255, v249, 3, v255\n"
"v_xor_b32_e64 v249, s42, 1\n"
"v_mfma_f32_16x16x32_bf16 v[176:179], a[192:195], a[168:171], v[176:179]\n"
"ds_read_b64_tr_b16 a[238:239], v245 offset:25088\n"
"v_bfe_u32 v248, v242, 9, 1\n"
"v_mfma_f32_16x16x32_bf16 v[176:179], a[196:199], a[172:175], v[176:179]\n"
"s_waitcnt lgkmcnt(4)\n"
"v_mfma_f32_16x16x32_bf16 v[192:195], a[192:195], a[176:179], v[192:195]\n"
"ds_read_b64_tr_b16 a[228:229], v244 offset:24576\n"
"v_lshl_add_u32 v255, v249, 12, v255\n"
"v_mfma_f32_16x16x32_bf16 v[192:195], a[196:199], a[180:183], v[192:195]\n"
"ds_read_b64_tr_b16 a[226:227], v244 offset:16896\n"
"v_mfma_f32_16x16x32_bf16 v[196:199], a[200:203], a[176:179], v[196:199]\n"
"s_add_u32 s40, s56, s52\n"
"ds_read_b64_tr_b16 a[248:249], v245 offset:17408\n"
"v_and_b32 v249, 0xf0, v242\n"
"v_mfma_f32_16x16x32_bf16 v[196:199], a[204:207], a[180:183], v[196:199]\n"
"buffer_load_dwordx4 v240, s[20:23], s40 offen offset:1024 nt lds\n"
"v_mfma_f32_16x16x32_bf16 v[200:203], a[208:211], a[176:179], v[200:203]\n"
"v_lshl_add_u32 v248, v248, 8, v249\n"
"v_mfma_f32_16x16x32_bf16 v[200:203], a[212:215], a[180:183], v[200:203]\n"
"v_mfma_f32_16x16x32_bf16 v[204:207], a[216:219], a[176:179], v[204:207]\n"
"ds_read_b64_tr_b16 a[230:231], v244 offset:25088\n"
"v_and_b32 v254, 0x3f803f80, v254\n"
"v_mfma_f32_16x16x32_bf16 v[204:207], a[220:223], a[180:183], v[204:207]\n"
"ds_read_b64_tr_b16 a[240:241], v244 offset:17408\n"
"v_bfe_u32 v249, v242, 8, 1\n"
"v_mfma_f32_16x16x32_bf16 v[220:223], a[216:219], a[184:187], v[220:223]\n"
"s_waitcnt lgkmcnt(4)\n"
"v_mfma_f32_16x16x32_bf16 v[220:223], a[220:223], a[188:191], v[220:223]\n"
"ds_read_b64_tr_b16 a[252:253], v245 offset:25600\n"
"v_xor_b32 v230, 0x2000, v228\n"
"v_mfma_f32_16x16x32_bf16 v[216:219], a[208:211], a[184:187], v[216:219]\n"
"ds_read_b64_tr_b16 a[224:225], v244 offset:16384\n"
"v_xor_b32 v231, 0x2000, v229\n"
"v_mfma_f32_16x16x32_bf16 v[216:219], a[212:215], a[188:191], v[216:219]\n"
"v_pk_fma_f32 v[176:177], v[176:177], s[70:71], s[70:71] op_sel:[0,0,1] op_sel_hi:[1,0,1]\nv_pk_fma_f32 v[178:179], v[178:179], s[70:71], s[70:71] op_sel:[0,0,1] op_sel_hi:[1,0,1]\nv_pk_fma_f32 v[180:181], v[180:181], s[70:71], s[70:71] op_sel:[0,0,1] op_sel_hi:[1,0,1]\nv_pk_fma_f32 v[182:183], v[182:183], s[70:71], s[70:71] op_sel:[0,0,1] op_sel_hi:[1,0,1]\nv_pk_fma_f32 v[184:185], v[184:185], s[70:71], s[70:71] op_sel:[0,0,1] op_sel_hi:[1,0,1]\nv_pk_fma_f32 v[186:187], v[186:187], s[70:71], s[70:71] op_sel:[0,0,1] op_sel_hi:[1,0,1]\nv_pk_fma_f32 v[188:189], v[188:189], s[70:71], s[70:71] op_sel:[0,0,1] op_sel_hi:[1,0,1]\nv_pk_fma_f32 v[190:191], v[190:191], s[70:71], s[70:71] op_sel:[0,0,1] op_sel_hi:[1,0,1]\nv_pk_fma_f32 v[192:193], v[192:193], s[70:71], s[70:71] op_sel:[0,0,1] op_sel_hi:[1,0,1]\nv_pk_fma_f32 v[194:195], v[194:195], s[70:71], s[70:71] op_sel:[0,0,1] op_sel_hi:[1,0,1]\nv_pk_fma_f32 v[196:197], v[196:197], s[70:71], s[70:71] op_sel:[0,0,1] op_sel_hi:[1,0,1]\nv_pk_fma_f32 v[198:199], v[198:199], s[70:71], s[70:71] op_sel:[0,0,1] op_sel_hi:[1,0,1]\nv_pk_fma_f32 v[200:201], v[200:201], s[70:71], s[70:71] op_sel:[0,0,1] op_sel_hi:[1,0,1]\nv_pk_fma_f32 v[202:203], v[202:203], s[70:71], s[70:71] op_sel:[0,0,1] op_sel_hi:[1,0,1]\nv_pk_fma_f32 v[204:205], v[204:205], s[70:71], s[70:71] op_sel:[0,0,1] op_sel_hi:[1,0,1]\nv_pk_fma_f32 v[206:207], v[206:207], s[70:71], s[70:71] op_sel:[0,0,1] op_sel_hi:[1,0,1]\n"
"v_mfma_f32_16x16x32_bf16 v[212:215], a[200:203], a[184:187], v[212:215]\n"
"ds_read_b64_tr_b16 a[246:247], v244 offset:26112\n"
"v_cvt_pk_bf16_f32 v200, v200, v201\n"
"v_mfma_f32_16x16x32_bf16 v[212:215], a[204:207], a[188:191], v[212:215]\n"
"v_cvt_pk_bf16_f32 v176, v176, v177\n"
"v_cvt_pk_bf16_f32 v184, v184, v185\n"
"v_mfma_f32_16x16x32_bf16 v[208:211], a[192:195], a[184:187], v[208:211]\n"
"s_add_u32 s40, s56, s53\n"
"ds_read_b64_tr_b16 a[232:233], v245 offset:16384\n"
"v_cvt_pk_bf16_f32 v201, v202, v203\n"
"v_mfma_f32_16x16x32_bf16 v[208:211], a[196:199], a[188:191], v[208:211]\n"
"buffer_load_dwordx4 v240, s[20:23], s40 offen offset:2048 nt lds\n"
"v_mfma_f32_16x16x32_bf16 v[160:163], a[192:195], a[160:163], v[160:163]\n"
"v_cvt_pk_bf16_f32 v192, v192, v193\n"
"v_cvt_pk_bf16_f32 v193, v194, v195\n"
"v_mfma_f32_16x16x32_bf16 v[160:163], a[196:199], a[164:167], v[160:163]\n"
"v_cvt_pk_bf16_f32 v177, v178, v179\n"
"v_cvt_pk_bf16_f32 v185, v186, v187\n"
"v_mfma_f32_16x16x32_bf16 v[164:167], a[200:203], a[160:163], v[164:167]\n"
"ds_read_b64_tr_b16 a[234:235], v245 offset:16896\n"
"v_cvt_pk_bf16_f32 v187, v190, v191\n"
"v_mfma_f32_16x16x32_bf16 v[164:167], a[204:207], a[164:167], v[164:167]\n"
"v_mfma_f32_16x16x32_bf16 v[168:171], a[208:211], a[160:163], v[168:171]\n"
"ds_read_b64_tr_b16 a[254:255], v245 offset:26112\n"
"v_mfma_f32_16x16x32_bf16 v[168:171], a[212:215], a[164:167], v[168:171]\n"
"v_cvt_pk_bf16_f32 v195, v198, v199\n"
"v_lshl_add_u32 v248, v249, 9, v248\n"
"v_mfma_f32_16x16x32_bf16 v[172:175], a[216:219], a[160:163], v[172:175]\n"
"ds_read_b64_tr_b16 a[250:251], v245 offset:17920\n"
"v_cvt_pk_bf16_f32 v186, v188, v189\n"
"v_mfma_f32_16x16x32_bf16 v[172:175], a[220:223], a[164:167], v[172:175]\n"
"v_cvt_pk_bf16_f32 v194, v196, v197\n"
"v_cvt_pk_bf16_f32 v202, v204, v205\n"
"v_mfma_f32_16x16x32_bf16 v[140:143], a[216:219], a[144:147], v[140:143]\n"
"ds_read_b128 a[156:159], v247 offset:4096\n"
"v_cvt_pk_bf16_f32 v179, v182, v183\n"
"v_mfma_f32_16x16x32_bf16 v[140:143], a[220:223], a[148:151], v[140:143]\n"
"v_cvt_pk_bf16_f32 v178, v180, v181\n"
"v_bfe_u32 v196, v242, 8, 2\n"
"v_mfma_f32_16x16x32_bf16 v[136:139], a[208:211], a[144:147], v[136:139]\n"
"ds_read_b128 a[152:155], v246 offset:4096\n"
"v_mfma_f32_16x16x32_bf16 v[136:139], a[212:215], a[148:151], v[136:139]\n"
"v_pk_fma_f32 v[208:209], v[208:209], s[70:71], s[70:71] op_sel:[0,0,1] op_sel_hi:[1,0,1]\nv_pk_fma_f32 v[210:211], v[210:211], s[70:71], s[70:71] op_sel:[0,0,1] op_sel_hi:[1,0,1]\nv_pk_fma_f32 v[212:213], v[212:213], s[70:71], s[70:71] op_sel:[0,0,1] op_sel_hi:[1,0,1]\nv_pk_fma_f32 v[214:215], v[214:215], s[70:71], s[70:71] op_sel:[0,0,1] op_sel_hi:[1,0,1]\nv_pk_fma_f32 v[216:217], v[216:217], s[70:71], s[70:71] op_sel:[0,0,1] op_sel_hi:[1,0,1]\nv_pk_fma_f32 v[218:219], v[218:219], s[70:71], s[70:71] op_sel:[0,0,1] op_sel_hi:[1,0,1]\nv_pk_fma_f32 v[220:221], v[220:221], s[70:71], s[70:71] op_sel:[0,0,1] op_sel_hi:[1,0,1]\nv_pk_fma_f32 v[222:223], v[222:223], s[70:71], s[70:71] op_sel:[0,0,1] op_sel_hi:[1,0,1]\nv_pk_fma_f32 v[160:161], v[160:161], s[70:71], s[70:71] op_sel:[0,0,1] op_sel_hi:[1,0,1]\nv_pk_fma_f32 v[162:163], v[162:163], s[70:71], s[70:71] op_sel:[0,0,1] op_sel_hi:[1,0,1]\nv_pk_fma_f32 v[164:165], v[164:165], s[70:71], s[70:71] op_sel:[0,0,1] op_sel_hi:[1,0,1]\nv_pk_fma_f32 v[166:167], v[166:167], s[70:71], s[70:71] op_sel:[0,0,1] op_sel_hi:[1,0,1]\nv_pk_fma_f32 v[168:169], v[168:169], s[70:71], s[70:71] op_sel:[0,0,1] op_sel_hi:[1,0,1]\nv_pk_fma_f32 v[170:171], v[170:171], s[70:71], s[70:71] op_sel:[0,0,1] op_sel_hi:[1,0,1]\nv_pk_fma_f32 v[172:173], v[172:173], s[70:71], s[70:71] op_sel:[0,0,1] op_sel_hi:[1,0,1]\nv_pk_fma_f32 v[174:175], v[174:175], s[70:71], s[70:71] op_sel:[0,0,1] op_sel_hi:[1,0,1]\n"
"v_mfma_f32_16x16x32_bf16 v[132:135], a[200:203], a[144:147], v[132:135]\n"
"s_add_u32 s40, s56, s54\n"
"ds_read_b64_tr_b16 a[244:245], v244 offset:25600\n"
"v_cvt_pk_bf16_f32 v208, v208, v209\n"
"v_mfma_f32_16x16x32_bf16 v[132:135], a[204:207], a[148:151], v[132:135]\n"
"buffer_load_dwordx4 v240, s[20:23], s40 offen offset:3072 nt lds\n"
"v_mfma_f32_16x16x32_bf16 v[128:131], a[192:195], a[144:147], v[128:131]\n"
"s_add_u32 s55, s55, s59\n"
"v_cvt_pk_bf16_f32 v216, v216, v217\n"
"v_mov_b32 v199, 0\n"
"v_mfma_f32_16x16x32_bf16 v[128:131], a[196:199], a[148:151], v[128:131]\n"
"s_waitcnt lgkmcnt(1)\n"
"s_or_b32 s55, s55, s58\n"
"v_cvt_pk_bf16_f32 v209, v210, v211\n"
"v_cvt_pk_bf16_f32 v168, v168, v169\n"
"v_mfma_f32_16x16x32_bf16 v[144:147], a[192:195], a[152:155], v[144:147]\n"
"s_xor_b32 s56, s55, s57\n"
"v_cvt_pk_bf16_f32 v160, v160, v161\n"
"v_lshl_add_u32 v196, v196, 4, s43\n"
"v_mfma_f32_16x16x32_bf16 v[144:147], a[196:199], a[156:159], v[144:147]\n"
"ds_read_b64_tr_b16 a[242:243], v244 offset:17920\n"
"v_mov_b32 v205, 0\n"
"v_mfma_f32_16x16x32_bf16 v[148:151], a[200:203], a[152:155], v[148:151]\n"
"v_cvt_pk_bf16_f32 v210, v212, v213\n"
"v_cvt_pk_bf16_f32 v217, v218, v219\n"
"v_mfma_f32_16x16x32_bf16 v[148:151], a[204:207], a[156:159], v[148:151]\n"
"ds_read_b64_tr_b16 a[236:237], v245 offset:24576\n"
"v_cvt_pk_bf16_f32 v169, v170, v171\n"
"v_mfma_f32_16x16x32_bf16 v[152:155], a[208:211], a[152:155], v[152:155]\n"
"v_cvt_pk_bf16_f32 v219, v222, v223\n"
"v_cvt_pk_bf16_f32 v161, v162, v163\n"
"v_mfma_f32_16x16x32_bf16 v[152:155], a[212:215], a[156:159], v[152:155]\n"
"v_pk_fma_f32 v[112:113], v[112:113], s[70:71], s[70:71] op_sel:[0,0,1] op_sel_hi:[1,0,1]\nv_pk_fma_f32 v[114:115], v[114:115], s[70:71], s[70:71] op_sel:[0,0,1] op_sel_hi:[1,0,1]\nv_pk_fma_f32 v[116:117], v[116:117], s[70:71], s[70:71] op_sel:[0,0,1] op_sel_hi:[1,0,1]\nv_pk_fma_f32 v[118:119], v[118:119], s[70:71], s[70:71] op_sel:[0,0,1] op_sel_hi:[1,0,1]\nv_pk_fma_f32 v[120:121], v[120:121], s[70:71], s[70:71] op_sel:[0,0,1] op_sel_hi:[1,0,1]\nv_pk_fma_f32 v[122:123], v[122:123], s[70:71], s[70:71] op_sel:[0,0,1] op_sel_hi:[1,0,1]\nv_pk_fma_f32 v[124:125], v[124:125], s[70:71], s[70:71] op_sel:[0,0,1] op_sel_hi:[1,0,1]\nv_pk_fma_f32 v[126:127], v[126:127], s[70:71], s[70:71] op_sel:[0,0,1] op_sel_hi:[1,0,1]\nv_pk_fma_f32 v[128:129], v[128:129], s[70:71], s[70:71] op_sel:[0,0,1] op_sel_hi:[1,0,1]\nv_pk_fma_f32 v[130:131], v[130:131], s[70:71], s[70:71] op_sel:[0,0,1] op_sel_hi:[1,0,1]\nv_pk_fma_f32 v[132:133], v[132:133], s[70:71], s[70:71] op_sel:[0,0,1] op_sel_hi:[1,0,1]\nv_pk_fma_f32 v[134:135], v[134:135], s[70:71], s[70:71] op_sel:[0,0,1] op_sel_hi:[1,0,1]\nv_pk_fma_f32 v[136:137], v[136:137], s[70:71], s[70:71] op_sel:[0,0,1] op_sel_hi:[1,0,1]\nv_pk_fma_f32 v[138:139], v[138:139], s[70:71], s[70:71] op_sel:[0,0,1] op_sel_hi:[1,0,1]\nv_pk_fma_f32 v[140:141], v[140:141], s[70:71], s[70:71] op_sel:[0,0,1] op_sel_hi:[1,0,1]\nv_pk_fma_f32 v[142:143], v[142:143], s[70:71], s[70:71] op_sel:[0,0,1] op_sel_hi:[1,0,1]\n"
"v_mfma_f32_16x16x32_bf16 v[156:159], a[216:219], a[152:155], v[156:159]\n"
"v_cvt_pk_bf16_f32 v211, v214, v215\n"
"v_cvt_pk_bf16_f32 v128, v128, v129\n"
"v_mfma_f32_16x16x32_bf16 v[156:159], a[220:223], a[156:159], v[156:159]\n"
"v_cvt_pk_bf16_f32 v162, v164, v165\n"
"v_cvt_pk_bf16_f32 v163, v166, v167\n"
"v_mfma_f32_16x16x32_bf16 v[96:99], a[224:227], a[184:187], v[96:99]\n"
"s_add_u32 m0, s44, 32768\n"
"v_cvt_pk_bf16_f32 v218, v220, v221\n"
"v_cvt_pk_bf16_f32 v171, v174, v175\n"
"v_mfma_f32_16x16x32_bf16 v[96:99], a[228:231], a[188:191], v[96:99]\n"
"buffer_load_dwordx4 v240, s[16:19], s56 offen nt lds\n"
"v_mfma_f32_16x16x32_bf16 v[100:103], a[232:235], a[184:187], v[100:103]\n"
"s_waitcnt vmcnt(14) lgkmcnt(0)\n"
"v_cvt_pk_bf16_f32 v129, v130, v131\n"
"v_cvt_pk_bf16_f32 v131, v134, v135\n"
"v_mfma_f32_16x16x32_bf16 v[100:103], a[236:239], a[188:191], v[100:103]\n"
"s_barrier\n"
"v_mfma_f32_16x16x32_bf16 v[104:107], a[240:243], a[184:187], v[104:107]\n"
"v_cvt_pk_bf16_f32 v170, v172, v173\n"
"v_xor_b32_e64 v249, s42, 0\n"
"v_mfma_f32_16x16x32_bf16 v[104:107], a[244:247], a[188:191], v[104:107]\n"
"s_mov_b32 s74, 0x41400000\n"
"v_pk_fma_f32 v[144:145], v[144:145], s[70:71], s[70:71] op_sel:[0,0,1] op_sel_hi:[1,0,1]\nv_pk_fma_f32 v[146:147], v[146:147], s[70:71], s[70:71] op_sel:[0,0,1] op_sel_hi:[1,0,1]\nv_pk_fma_f32 v[148:149], v[148:149], s[70:71], s[70:71] op_sel:[0,0,1] op_sel_hi:[1,0,1]\nv_pk_fma_f32 v[150:151], v[150:151], s[70:71], s[70:71] op_sel:[0,0,1] op_sel_hi:[1,0,1]\nv_pk_fma_f32 v[152:153], v[152:153], s[70:71], s[70:71] op_sel:[0,0,1] op_sel_hi:[1,0,1]\nv_pk_fma_f32 v[154:155], v[154:155], s[70:71], s[70:71] op_sel:[0,0,1] op_sel_hi:[1,0,1]\nv_pk_fma_f32 v[156:157], v[156:157], s[70:71], s[70:71] op_sel:[0,0,1] op_sel_hi:[1,0,1]\nv_pk_fma_f32 v[158:159], v[158:159], s[70:71], s[70:71] op_sel:[0,0,1] op_sel_hi:[1,0,1]\n"
"v_mfma_f32_16x16x32_bf16 v[108:111], a[248:251], a[184:187], v[108:111]\n"
"v_permlane16_swap_b32 v160, v162\n"
"v_mfma_f32_16x16x32_bf16 v[108:111], a[252:255], a[188:191], v[108:111]\n"
"ds_write2st64_b64 v255, v[208:209], v[210:211] offset0:192 offset1:193\n"
"v_mfma_f32_16x16x32_bf16 v[92:95], a[248:251], a[176:179], v[92:95]\n"
"v_cvt_pk_bf16_f32 v136, v136, v137\n"
"v_cvt_pk_bf16_f32 v152, v152, v153\n"
"v_mfma_f32_16x16x32_bf16 v[92:95], a[252:255], a[180:183], v[92:95]\n"
"v_xor_b32 v197, 64, v196\n"
"v_cvt_pk_bf16_f32 v112, v112, v113\n"
"v_mfma_f32_16x16x32_bf16 v[88:91], a[240:243], a[176:179], v[88:91]\n"
"v_cvt_pk_bf16_f32 v130, v132, v133\n"
"v_cvt_pk_bf16_f32 v137, v138, v139\n"
"v_mfma_f32_16x16x32_bf16 v[88:91], a[244:247], a[180:183], v[88:91]\n"
"ds_write2st64_b64 v255, v[184:185], v[186:187] offset0:98 offset1:99\n"
"v_mfma_f32_16x16x32_bf16 v[84:87], a[232:235], a[176:179], v[84:87]\n"
"v_cvt_pk_bf16_f32 v153, v154, v155\n"
"v_cvt_pk_bf16_f32 v155, v158, v159\n"
"v_mfma_f32_16x16x32_bf16 v[84:87], a[236:239], a[180:183], v[84:87]\n"
"ds_write2st64_b64 v255, v[192:193], v[194:195] offset0:100 offset1:101\n"
"v_mfma_f32_16x16x32_bf16 v[80:83], a[224:227], a[176:179], v[80:83]\n"
"s_add_u32 s40, s56, s52\n"
"v_permlane16_swap_b32 v161, v163\n"
"v_mfma_f32_16x16x32_bf16 v[80:83], a[228:231], a[180:183], v[80:83]\n"
"buffer_load_dwordx4 v240, s[16:19], s40 offen offset:1024 nt lds\n"
"v_mfma_f32_16x16x32_bf16 v[64:67], a[224:227], a[168:171], v[64:67]\n"
"v_permlane16_swap_b32 v153, v155\n"
"v_mfma_f32_16x16x32_bf16 v[64:67], a[228:231], a[172:175], v[64:67]\n"
"v_cvt_pk_bf16_f32 v139, v142, v143\n"
"v_cvt_pk_bf16_f32 v144, v144, v145\n"
"v_mfma_f32_16x16x32_bf16 v[68:71], a[232:235], a[168:171], v[68:71]\n"
"v_cvt_pk_bf16_f32 v154, v156, v157\n"
"v_lshl_add_u32 v248, v249, 12, v248\n"
"v_mfma_f32_16x16x32_bf16 v[68:71], a[236:239], a[172:175], v[68:71]\n"
"ds_write_b128 v243, v[160:163] offset:8192\n"
"v_pk_fma_f32 v[96:97], v[96:97], s[70:71], s[70:71] op_sel:[0,0,1] op_sel_hi:[1,0,1]\nv_pk_fma_f32 v[98:99], v[98:99], s[70:71], s[70:71] op_sel:[0,0,1] op_sel_hi:[1,0,1]\nv_pk_fma_f32 v[100:101], v[100:101], s[70:71], s[70:71] op_sel:[0,0,1] op_sel_hi:[1,0,1]\nv_pk_fma_f32 v[102:103], v[102:103], s[70:71], s[70:71] op_sel:[0,0,1] op_sel_hi:[1,0,1]\nv_pk_fma_f32 v[104:105], v[104:105], s[70:71], s[70:71] op_sel:[0,0,1] op_sel_hi:[1,0,1]\nv_pk_fma_f32 v[106:107], v[106:107], s[70:71], s[70:71] op_sel:[0,0,1] op_sel_hi:[1,0,1]\nv_pk_fma_f32 v[108:109], v[108:109], s[70:71], s[70:71] op_sel:[0,0,1] op_sel_hi:[1,0,1]\nv_pk_fma_f32 v[110:111], v[110:111], s[70:71], s[70:71] op_sel:[0,0,1] op_sel_hi:[1,0,1]\nv_pk_fma_f32 v[80:81], v[80:81], s[70:71], s[70:71] op_sel:[0,0,1] op_sel_hi:[1,0,1]\nv_pk_fma_f32 v[82:83], v[82:83], s[70:71], s[70:71] op_sel:[0,0,1] op_sel_hi:[1,0,1]\nv_pk_fma_f32 v[84:85], v[84:85], s[70:71], s[70:71] op_sel:[0,0,1] op_sel_hi:[1,0,1]\nv_pk_fma_f32 v[86:87], v[86:87], s[70:71], s[70:71] op_sel:[0,0,1] op_sel_hi:[1,0,1]\nv_pk_fma_f32 v[88:89], v[88:89], s[70:71], s[70:71] op_sel:[0,0,1] op_sel_hi:[1,0,1]\nv_pk_fma_f32 v[90:91], v[90:91], s[70:71], s[70:71] op_sel:[0,0,1] op_sel_hi:[1,0,1]\nv_pk_fma_f32 v[92:93], v[92:93], s[70:71], s[70:71] op_sel:[0,0,1] op_sel_hi:[1,0,1]\nv_pk_fma_f32 v[94:95], v[94:95], s[70:71], s[70:71] op_sel:[0,0,1] op_sel_hi:[1,0,1]\n"
"v_mfma_f32_16x16x32_bf16 v[72:75], a[240:243], a[168:171], v[72:75]\n"
"v_cvt_pk_bf16_f32 v203, v206, v207\n"
"v_cvt_pk_bf16_f32 v88, v88, v89\n"
"v_mfma_f32_16x16x32_bf16 v[72:75], a[244:247], a[172:175], v[72:75]\n"
"ds_write2st64_b64 v255, v[176:177], v[178:179] offset0:96 offset1:97\n"
"v_mfma_f32_16x16x32_bf16 v[76:79], a[248:251], a[168:171], v[76:79]\n"
"v_cvt_pk_bf16_f32 v104, v104, v105\n"
"v_cvt_pk_bf16_f32 v80, v80, v81\n"
"v_mfma_f32_16x16x32_bf16 v[76:79], a[252:255], a[172:175], v[76:79]\n"
"v_cvt_pk_bf16_f32 v120, v120, v121\n"
"v_cvt_pk_bf16_f32 v138, v140, v141\n"
"v_mfma_f32_16x16x32_bf16 v[60:63], a[248:251], a[160:163], v[60:63]\n"
"ds_read_b128 a[192:195], v243 offset:0\n"
"v_mov_b32 v204, 0\n"
"v_mfma_f32_16x16x32_bf16 v[60:63], a[252:255], a[164:167], v[60:63]\n"
"v_cvt_pk_bf16_f32 v81, v82, v83\n"
"v_cvt_pk_bf16_f32 v82, v84, v85\n"
"v_mfma_f32_16x16x32_bf16 v[56:59], a[240:243], a[160:163], v[56:59]\n"
"s_add_u32 s40, s56, s53\n"
"ds_read_b128 v[212:215], v197 offset:0\n"
"v_add_u32 v249, s75, v248\n"
"v_mfma_f32_16x16x32_bf16 v[56:59], a[244:247], a[164:167], v[56:59]\n"
"buffer_load_dwordx4 v240, s[16:19], s40 offen offset:2048 nt lds\n"
"v_mfma_f32_16x16x32_bf16 v[52:55], a[232:235], a[160:163], v[52:55]\n"
"v_cvt_pk_bf16_f32 v145, v146, v147\n"
"v_xor_b32 v198, 0x400, v243\n"
"v_mfma_f32_16x16x32_bf16 v[52:55], a[236:239], a[164:167], v[52:55]\n"
"v_permlane16_swap_b32 v128, v130\n"
"v_mfma_f32_16x16x32_bf16 v[48:51], a[224:227], a[160:163], v[48:51]\n"
"v_permlane16_swap_b32 v137, v139\n"
"v_mfma_f32_16x16x32_bf16 v[48:51], a[228:231], a[164:167], v[48:51]\n"
"ds_read_b128 v[208:211], v196 offset:0\n"
"v_accvgpr_write_b32 a123, v139\n"
"v_mfma_f32_16x16x32_bf16 v[0:3], a[224:227], a[136:139], v[0:3]\n"
"v_permlane16_swap_b32 v168, v170\n"
"v_mfma_f32_16x16x32_bf16 v[0:3], a[228:231], a[140:143], v[0:3]\n"
"ds_read_b128 a[204:207], v198 offset:2048\n"
"v_cvt_pk_bf16_f32 v105, v106, v107\n"
"v_mfma_f32_16x16x32_bf16 v[4:7], a[232:235], a[136:139], v[4:7]\n"
"v_permlane16_swap_b32 v129, v131\n"
"v_mfma_f32_16x16x32_bf16 v[4:7], a[236:239], a[140:143], v[4:7]\n"
"v_cvt_pk_bf16_f32 v146, v148, v149\n"
"v_cvt_pk_bf16_f32 v89, v90, v91\n"
"v_mfma_f32_16x16x32_bf16 v[8:11], a[240:243], a[136:139], v[8:11]\n"
"v_permlane16_swap_b32 v152, v154\n"
"v_mfma_f32_16x16x32_bf16 v[8:11], a[244:247], a[140:143], v[8:11]\n"
"ds_read_b128 a[200:203], v243 offset:2048\n"
"v_cvt_pk_bf16_f32 v96, v96, v97\n"
"v_mfma_f32_16x16x32_bf16 v[12:15], a[248:251], a[136:139], v[12:15]\n"
"v_accvgpr_write_b32 a113, v129\n"
"v_cvt_pk_bf16_f32 v97, v98, v99\n"
"v_mfma_f32_16x16x32_bf16 v[12:15], a[252:255], a[140:143], v[12:15]\n"
"ds_write2st64_b64 v255, v[200:201], v[202:203] offset0:102 offset1:103\n"
"v_pk_fma_f32 v[64:65], v[64:65], s[70:71], s[70:71] op_sel:[0,0,1] op_sel_hi:[1,0,1]\nv_pk_fma_f32 v[66:67], v[66:67], s[70:71], s[70:71] op_sel:[0,0,1] op_sel_hi:[1,0,1]\nv_pk_fma_f32 v[68:69], v[68:69], s[70:71], s[70:71] op_sel:[0,0,1] op_sel_hi:[1,0,1]\nv_pk_fma_f32 v[70:71], v[70:71], s[70:71], s[70:71] op_sel:[0,0,1] op_sel_hi:[1,0,1]\nv_pk_fma_f32 v[72:73], v[72:73], s[70:71], s[70:71] op_sel:[0,0,1] op_sel_hi:[1,0,1]\nv_pk_fma_f32 v[74:75], v[74:75], s[70:71], s[70:71] op_sel:[0,0,1] op_sel_hi:[1,0,1]\nv_pk_fma_f32 v[76:77], v[76:77], s[70:71], s[70:71] op_sel:[0,0,1] op_sel_hi:[1,0,1]\nv_pk_fma_f32 v[78:79], v[78:79], s[70:71], s[70:71] op_sel:[0,0,1] op_sel_hi:[1,0,1]\nv_pk_fma_f32 v[48:49], v[48:49], s[70:71], s[70:71] op_sel:[0,0,1] op_sel_hi:[1,0,1]\nv_pk_fma_f32 v[50:51], v[50:51], s[70:71], s[70:71] op_sel:[0,0,1] op_sel_hi:[1,0,1]\nv_pk_fma_f32 v[52:53], v[52:53], s[70:71], s[70:71] op_sel:[0,0,1] op_sel_hi:[1,0,1]\nv_pk_fma_f32 v[54:55], v[54:55], s[70:71], s[70:71] op_sel:[0,0,1] op_sel_hi:[1,0,1]\nv_pk_fma_f32 v[56:57], v[56:57], s[70:71], s[70:71] op_sel:[0,0,1] op_sel_hi:[1,0,1]\nv_pk_fma_f32 v[58:59], v[58:59], s[70:71], s[70:71] op_sel:[0,0,1] op_sel_hi:[1,0,1]\nv_pk_fma_f32 v[60:61], v[60:61], s[70:71], s[70:71] op_sel:[0,0,1] op_sel_hi:[1,0,1]\nv_pk_fma_f32 v[62:63], v[62:63], s[70:71], s[70:71] op_sel:[0,0,1] op_sel_hi:[1,0,1]\n"
"v_mfma_f32_16x16x32_bf16 v[28:31], a[248:251], a[144:147], v[28:31]\n"
"s_add_u32 s40, s56, s54\n"
"ds_read_b128 v[220:223], v197 offset:1024\n"
"v_cvt_pk_bf16_f32 v90, v92, v93\n"
"v_mfma_f32_16x16x32_bf16 v[28:31], a[252:255], a[148:151], v[28:31]\n"
"buffer_load_dwordx4 v240, s[16:19], s40 offen offset:3072 nt lds\n"
"v_mfma_f32_16x16x32_bf16 v[24:27], a[240:243], a[144:147], v[24:27]\n"
"v_cvt_pk_bf16_f32 v99, v102, v103\n"
"v_mov_b32 v197, 0\n"
"v_mfma_f32_16x16x32_bf16 v[24:27], a[244:247], a[148:151], v[24:27]\n"
"v_cvt_pk_bf16_f32 v147, v150, v151\n"
"v_cvt_pk_bf16_f32 v48, v48, v49\n"
"v_mfma_f32_16x16x32_bf16 v[20:23], a[232:235], a[144:147], v[20:23]\n"
"v_cvt_pk_bf16_f32 v121, v122, v123\n"
"v_accvgpr_write_b32 a138, v154\n"
"v_mfma_f32_16x16x32_bf16 v[20:23], a[236:239], a[148:151], v[20:23]\n"
"ds_write2st64_b64 v255, v[216:217], v[218:219] offset0:194 offset1:195\n"
"v_mfma_f32_16x16x32_bf16 v[16:19], a[224:227], a[144:147], v[16:19]\n"
"v_permlane16_swap_b32 v136, v138\n"
"v_mfma_f32_16x16x32_bf16 v[16:19], a[228:231], a[148:151], v[16:19]\n"
"v_cvt_pk_bf16_f32 v122, v124, v125\n"
"v_accvgpr_write_b32 a114, v130\n"
"v_mfma_f32_16x16x32_bf16 v[32:35], a[224:227], a[152:155], v[32:35]\n"
"v_cvt_pk_bf16_f32 v106, v108, v109\n"
"v_cvt_pk_bf16_f32 v64, v64, v65\n"
"v_mfma_f32_16x16x32_bf16 v[32:35], a[228:231], a[156:159], v[32:35]\n"
"ds_read_b128 v[216:219], v196 offset:1024\n"
"v_accvgpr_write_b32 a115, v131\n"
"v_mfma_f32_16x16x32_bf16 v[36:39], a[232:235], a[152:155], v[36:39]\n"
"v_cvt_pk_bf16_f32 v123, v126, v127\n"
"v_cvt_pk_bf16_f32 v107, v110, v111\n"
"v_mfma_f32_16x16x32_bf16 v[36:39], a[236:239], a[156:159], v[36:39]\n"
"v_permlane16_swap_b32 v169, v171\n"
"v_mfma_f32_16x16x32_bf16 v[40:43], a[240:243], a[152:155], v[40:43]\n"
"v_permlane16_swap_b32 v145, v147\n"
"v_mfma_f32_16x16x32_bf16 v[40:43], a[244:247], a[156:159], v[40:43]\n"
"ds_write_b128 v243, v[168:171] offset:10240\n"
"v_cvt_pk_bf16_f32 v72, v72, v73\n"
"v_cvt_pk_bf16_f32 v56, v56, v57\n"
"v_mfma_f32_16x16x32_bf16 v[44:47], a[248:251], a[152:155], v[44:47]\n"
"v_accvgpr_write_b32 a137, v153\n"
"v_cvt_pk_bf16_f32 v49, v50, v51\n"
"v_mfma_f32_16x16x32_bf16 v[44:47], a[252:255], a[156:159], v[44:47]\n"
"s_waitcnt lgkmcnt(2)\n"
"v_cvt_pk_bf16_f32 v91, v94, v95\n"
"v_cvt_pk_bf16_f32 v65, v66, v67\n"
"v_mfma_f32_16x16x32_bf16 v[180:183], v[208:211], a[0:3], 0\n"
"v_permlane16_swap_b32 v144, v146\n"
"v_mfma_f32_16x16x32_bf16 v[180:183], v[212:215], a[4:7], v[180:183]\n"
"s_waitcnt lgkmcnt(1)\n"
"v_accvgpr_write_b32 a129, v145\n"
"v_cvt_pk_bf16_f32 v57, v58, v59\n"
"v_mfma_f32_16x16x32_bf16 v[180:183], v[216:219], a[8:11], v[180:183]\n"
"ds_read_b128 a[196:199], v198 offset:0\n"
"v_accvgpr_write_b32 a139, v155\n"
"v_mfma_f32_16x16x32_bf16 v[180:183], v[220:223], a[12:15], v[180:183]\n"
"ds_write2st64_b64 v255, v[104:105], v[106:107] offset0:230 offset1:231\n"
"v_pk_fma_f32 v[0:1], v[0:1], s[70:71], s[70:71] op_sel:[0,0,1] op_sel_hi:[1,0,1]\nv_pk_fma_f32 v[2:3], v[2:3], s[70:71], s[70:71] op_sel:[0,0,1] op_sel_hi:[1,0,1]\nv_pk_fma_f32 v[4:5], v[4:5], s[70:71], s[70:71] op_sel:[0,0,1] op_sel_hi:[1,0,1]\nv_pk_fma_f32 v[6:7], v[6:7], s[70:71], s[70:71] op_sel:[0,0,1] op_sel_hi:[1,0,1]\nv_pk_fma_f32 v[8:9], v[8:9], s[70:71], s[70:71] op_sel:[0,0,1] op_sel_hi:[1,0,1]\nv_pk_fma_f32 v[10:11], v[10:11], s[70:71], s[70:71] op_sel:[0,0,1] op_sel_hi:[1,0,1]\nv_pk_fma_f32 v[12:13], v[12:13], s[70:71], s[70:71] op_sel:[0,0,1] op_sel_hi:[1,0,1]\nv_pk_fma_f32 v[14:15], v[14:15], s[70:71], s[70:71] op_sel:[0,0,1] op_sel_hi:[1,0,1]\nv_pk_fma_f32 v[16:17], v[16:17], s[70:71], s[70:71] op_sel:[0,0,1] op_sel_hi:[1,0,1]\nv_pk_fma_f32 v[18:19], v[18:19], s[70:71], s[70:71] op_sel:[0,0,1] op_sel_hi:[1,0,1]\nv_pk_fma_f32 v[20:21], v[20:21], s[70:71], s[70:71] op_sel:[0,0,1] op_sel_hi:[1,0,1]\nv_pk_fma_f32 v[22:23], v[22:23], s[70:71], s[70:71] op_sel:[0,0,1] op_sel_hi:[1,0,1]\nv_pk_fma_f32 v[24:25], v[24:25], s[70:71], s[70:71] op_sel:[0,0,1] op_sel_hi:[1,0,1]\nv_pk_fma_f32 v[26:27], v[26:27], s[70:71], s[70:71] op_sel:[0,0,1] op_sel_hi:[1,0,1]\nv_pk_fma_f32 v[28:29], v[28:29], s[70:71], s[70:71] op_sel:[0,0,1] op_sel_hi:[1,0,1]\nv_pk_fma_f32 v[30:31], v[30:31], s[70:71], s[70:71] op_sel:[0,0,1] op_sel_hi:[1,0,1]\n"
"v_mfma_f32_16x16x32_bf16 v[184:187], v[208:211], a[16:19], 0\n"
"v_accvgpr_write_b32 a122, v138\n"
"v_cvt_pk_bf16_f32 v83, v86, v87\n"
"v_mfma_f32_16x16x32_bf16 v[184:187], v[212:215], a[20:23], v[184:187]\n"
"v_accvgpr_write_b32 a136, v152\n"
"v_cvt_pk_bf16_f32 v8, v8, v9\n"
"v_mfma_f32_16x16x32_bf16 v[184:187], v[216:219], a[24:27], v[184:187]\n"
"v_accvgpr_write_b32 a121, v137\n"
"v_cvt_pk_bf16_f32 v66, v68, v69\n"
"v_mfma_f32_16x16x32_bf16 v[184:187], v[220:223], a[28:31], v[184:187]\n"
"v_accvgpr_write_b32 a112, v128\n"
"v_sub_f32 v182, s74, v182\n"
"v_mfma_f32_16x16x32_bf16 v[188:191], v[208:211], a[32:35], 0\n"
"v_permlane16_swap_b32 v120, v122\n"
"v_mfma_f32_16x16x32_bf16 v[188:191], v[212:215], a[36:39], v[188:191]\n"
"v_sub_f32 v181, s74, v181\n"
"v_sub_f32 v183, s74, v183\n"
"v_mfma_f32_16x16x32_bf16 v[188:191], v[216:219], a[40:43], v[188:191]\n"
"v_cvt_pk_bf16_f32 v51, v54, v55\n"
"v_sub_f32 v187, s74, v187\n"
"v_mfma_f32_16x16x32_bf16 v[188:191], v[220:223], a[44:47], v[188:191]\n"
"ds_write2st64_b64 v255, v[80:81], v[82:83] offset0:224 offset1:225\n"
"v_pk_fma_f32 v[32:33], v[32:33], s[70:71], s[70:71] op_sel:[0,0,1] op_sel_hi:[1,0,1]\nv_pk_fma_f32 v[34:35], v[34:35], s[70:71], s[70:71] op_sel:[0,0,1] op_sel_hi:[1,0,1]\nv_pk_fma_f32 v[36:37], v[36:37], s[70:71], s[70:71] op_sel:[0,0,1] op_sel_hi:[1,0,1]\nv_pk_fma_f32 v[38:39], v[38:39], s[70:71], s[70:71] op_sel:[0,0,1] op_sel_hi:[1,0,1]\nv_pk_fma_f32 v[40:41], v[40:41], s[70:71], s[70:71] op_sel:[0,0,1] op_sel_hi:[1,0,1]\nv_pk_fma_f32 v[42:43], v[42:43], s[70:71], s[70:71] op_sel:[0,0,1] op_sel_hi:[1,0,1]\nv_pk_fma_f32 v[44:45], v[44:45], s[70:71], s[70:71] op_sel:[0,0,1] op_sel_hi:[1,0,1]\nv_pk_fma_f32 v[46:47], v[46:47], s[70:71], s[70:71] op_sel:[0,0,1] op_sel_hi:[1,0,1]\n"
"v_sub_f32 v184, s74, v184\n"
"v_mfma_f32_16x16x32_bf16 v[192:195], v[208:211], a[192:195], 0\n"
"s_waitcnt lgkmcnt(1)\n"
"ds_read_b128 v[208:211], v228 offset:0\n"
"v_sub_f32 v186, s74, v186\n"
"v_mfma_f32_16x16x32_bf16 v[192:195], v[212:215], a[196:199], v[192:195]\n"
"ds_read_b128 v[212:215], v229 offset:0\n"
"v_cvt_pk_bf16_f32 v98, v100, v101\n"
"v_mfma_f32_16x16x32_bf16 v[192:195], v[216:219], a[200:203], v[192:195]\n"
"ds_read_b128 v[216:219], v228 offset:1024\n"
"v_sub_f32 v180, s74, v180\n"
"v_mfma_f32_16x16x32_bf16 v[192:195], v[220:223], a[204:207], v[192:195]\n"
"s_waitcnt lgkmcnt(2)\n"
"ds_read_b128 v[220:223], v229 offset:1024\n"
"v_sub_f32 v191, s74, v191\n"
"v_mfma_f32_16x16x32_bf16 v[152:155], v[208:211], a[0:3], v[180:183]\n"
"s_waitcnt lgkmcnt(2)\n"
"v_sub_f32 v185, s74, v185\n"
"v_sub_f32 v188, s74, v188\n"
"v_mfma_f32_16x16x32_bf16 v[152:155], v[212:215], a[4:7], v[152:155]\n"
"s_waitcnt lgkmcnt(1)\n"
"v_cvt_pk_bf16_f32 v67, v70, v71\n"
"v_sub_f32 v190, s74, v190\n"
"v_mfma_f32_16x16x32_bf16 v[152:155], v[216:219], a[8:11], v[152:155]\n"
"s_waitcnt lgkmcnt(0)\n"
"v_accvgpr_write_b32 a128, v144\n"
"v_cvt_pk_bf16_f32 v24, v24, v25\n"
"v_mfma_f32_16x16x32_bf16 v[152:155], v[220:223], a[12:15], v[152:155]\n"
"ds_write2st64_b64 v255, v[96:97], v[98:99] offset0:228 offset1:229\n"
"v_cvt_pk_bf16_f32 v73, v74, v75\n"
"v_cvt_pk_bf16_f32 v58, v60, v61\n"
"v_mfma_f32_16x16x32_bf16 v[156:159], v[216:219], a[24:27], v[184:187]\n"
"v_sub_f32 v194, s74, v194\n"
"v_sub_f32 v195, s74, v195\n"
"v_mfma_f32_16x16x32_bf16 v[156:159], v[220:223], a[28:31], v[156:159]\n"
"v_sub_f32 v189, s74, v189\n"
"v_sub_f32 v193, s74, v193\n"
"v_mfma_f32_16x16x32_bf16 v[156:159], v[208:211], a[16:19], v[156:159]\n"
"v_accvgpr_write_b32 a241, v254\n"
"v_sub_f32 v192, s74, v192\n"
"v_mfma_f32_16x16x32_bf16 v[156:159], v[212:215], a[20:23], v[156:159]\n"
"v_exp_f32 v154, v154\n"
"v_mfma_f32_16x16x32_bf16 v[160:163], v[208:211], a[32:35], v[188:191]\n"
"v_exp_f32 v152, v152\n"
"v_mfma_f32_16x16x32_bf16 v[160:163], v[212:215], a[36:39], v[160:163]\n"
"v_exp_f32 v155, v155\n"
"v_mfma_f32_16x16x32_bf16 v[160:163], v[216:219], a[40:43], v[160:163]\n"
"v_accvgpr_write_b32 a130, v146\n"
"v_cvt_pk_bf16_f32 v59, v62, v63\n"
"v_mfma_f32_16x16x32_bf16 v[160:163], v[220:223], a[44:47], v[160:163]\n"
"v_permlane16_swap_b32 v49, v51\n"
"v_mfma_f32_16x16x32_bf16 v[164:167], v[216:219], a[200:203], v[192:195]\n"
"ds_read_b128 v[216:219], v228 offset:5120\n"
"v_accvgpr_write_b32 a120, v136\n"
"v_mfma_f32_16x16x32_bf16 v[164:167], v[220:223], a[204:207], v[164:167]\n"
"ds_read_b128 v[220:223], v229 offset:5120\n"
"v_cvt_pk_bf16_f32 v9, v10, v11\n"
"v_mfma_f32_16x16x32_bf16 v[164:167], v[208:211], a[192:195], v[164:167]\n"
"ds_read_b128 v[208:211], v228 offset:4096\n"
"v_cvt_pk_bf16_f32 v50, v52, v53\n"
"v_mfma_f32_16x16x32_bf16 v[164:167], v[212:215], a[196:199], v[164:167]\n"
"s_waitcnt lgkmcnt(2)\n"
"ds_read_b128 v[212:215], v229 offset:4096\n"
"v_cvt_pk_bf16_f32 v40, v40, v41\n"
"v_mfma_f32_16x16x32_bf16 v[168:171], v[216:219], a[8:11], v[180:183]\n"
"s_waitcnt lgkmcnt(2)\n"
"v_exp_f32 v156, v156\n"
"v_mfma_f32_16x16x32_bf16 v[168:171], v[220:223], a[12:15], v[168:171]\n"
"s_waitcnt lgkmcnt(1)\n"
"v_exp_f32 v162, v162\n"
"v_mfma_f32_16x16x32_bf16 v[168:171], v[208:211], a[0:3], v[168:171]\n"
"s_waitcnt lgkmcnt(0)\n"
"v_exp_f32 v153, v153\n"
"v_mfma_f32_16x16x32_bf16 v[168:171], v[212:215], a[4:7], v[168:171]\n"
"ds_write2st64_b64 v255, v[88:89], v[90:91] offset0:226 offset1:227\n"
"v_cvt_pk_bf16_f32 v75, v78, v79\n"
"v_permlane16_swap_b32 v57, v59\n"
"v_exp_f32 v157, v157\n"
"v_exp_f32 v161, v161\n"
"v_exp_f32 v163, v163\n"
"v_mfma_f32_16x16x32_bf16 v[172:175], v[208:211], a[16:19], v[184:187]\n"
"v_exp_f32 v160, v160\n"
"v_mfma_f32_16x16x32_bf16 v[172:175], v[212:215], a[20:23], v[172:175]\n"
"v_exp_f32 v159, v159\n"
"v_mfma_f32_16x16x32_bf16 v[172:175], v[216:219], a[24:27], v[172:175]\n"
"v_accvgpr_write_b32 a240, v254\n"
"v_cvt_pk_bf16_f32 v128, v152, v153\n"
"v_mfma_f32_16x16x32_bf16 v[172:175], v[220:223], a[28:31], v[172:175]\n"
"v_permlane16_swap_b32 v48, v50\n"
"v_cvt_pk_bf16_f32 v25, v26, v27\n"
"v_cvt_pk_bf16_f32 v136, v156, v157\n"
"v_exp_f32 v158, v158\n"
"v_cvt_pk_bf16_f32 v144, v160, v161\n"
"v_exp_f32 v164, v164\n"
"v_exp_f32 v165, v165\n"
"v_exp_f32 v169, v169\n"
"v_mfma_f32_16x16x32_bf16 v[176:179], v[216:219], a[40:43], v[188:191]\n"
"v_exp_f32 v166, v166\n"
"v_mfma_f32_16x16x32_bf16 v[176:179], v[220:223], a[44:47], v[176:179]\n"
"v_exp_f32 v171, v171\n"
"v_mfma_f32_16x16x32_bf16 v[176:179], v[208:211], a[32:35], v[176:179]\n"
"v_cvt_pk_bf16_f32 v113, v114, v115\n"
"v_cvt_pk_bf16_f32 v129, v154, v155\n"
"v_mfma_f32_16x16x32_bf16 v[176:179], v[212:215], a[36:39], v[176:179]\n"
"v_cvt_pk_bf16_f32 v41, v42, v43\n"
"v_cvt_pk_bf16_f32 v137, v158, v159\n"
"v_mfma_f32_16x16x32_bf16 v[152:155], v[208:211], a[192:195], v[192:195]\n"
"ds_read_b128 v[208:211], v230 offset:0\n"
"v_mov_b32 v202, 0\n"
"v_mfma_f32_16x16x32_bf16 v[152:155], v[212:215], a[196:199], v[152:155]\n"
"ds_read_b128 v[212:215], v231 offset:0\n"
"v_cvt_pk_bf16_f32 v10, v12, v13\n"
"v_mfma_f32_16x16x32_bf16 v[152:155], v[216:219], a[200:203], v[152:155]\n"
"ds_read_b128 v[216:219], v230 offset:1024\n"
"v_cvt_pk_bf16_f32 v145, v162, v163\n"
"v_mfma_f32_16x16x32_bf16 v[152:155], v[220:223], a[204:207], v[152:155]\n"
"s_waitcnt lgkmcnt(2)\n"
"ds_read_b128 v[220:223], v231 offset:1024\n"
"v_permlane16_swap_b32 v8, v10\n"
"v_exp_f32 v167, v167\n"
"v_exp_f32 v168, v168\n"
"v_exp_f32 v170, v170\n"
"v_exp_f32 v172, v172\n"
"v_exp_f32 v173, v173\n"
"v_mfma_f32_16x16x32_bf16 v[156:159], v[208:211], a[0:3], v[180:183]\n"
"s_waitcnt lgkmcnt(2)\n"
"v_permlane16_swap_b32 v56, v58\n"
"v_mfma_f32_16x16x32_bf16 v[156:159], v[212:215], a[4:7], v[156:159]\n"
"s_waitcnt lgkmcnt(1)\n"
"v_cvt_pk_bf16_f32 v11, v14, v15\n"
"v_cvt_pk_bf16_f32 v131, v170, v171\n"
"v_mfma_f32_16x16x32_bf16 v[156:159], v[216:219], a[8:11], v[156:159]\n"
"s_waitcnt lgkmcnt(0)\n"
"v_exp_f32 v153, v153\n"
"v_mfma_f32_16x16x32_bf16 v[156:159], v[220:223], a[12:15], v[156:159]\n"
"ds_write_b128 v243, v[48:51] offset:12288\n"
"v_permlane16_swap_b32 v121, v123\n"
"v_cvt_pk_bf16_f32 v124, v164, v165\n"
"v_cvt_pk_bf16_f32 v130, v168, v169\n"
"v_exp_f32 v178, v178\n"
"v_exp_f32 v179, v179\n"
"v_exp_f32 v152, v152\n"
"v_exp_f32 v155, v155\n"
"v_mfma_f32_16x16x32_bf16 v[160:163], v[216:219], a[24:27], v[184:187]\n"
"v_cvt_pk_bf16_f32 v0, v0, v1\n"
"v_cvt_pk_bf16_f32 v42, v44, v45\n"
"v_mfma_f32_16x16x32_bf16 v[160:163], v[220:223], a[28:31], v[160:163]\n"
"v_exp_f32 v176, v176\n"
"v_mfma_f32_16x16x32_bf16 v[160:163], v[208:211], a[16:19], v[160:163]\n"
"v_cvt_pk_bf16_f32 v43, v46, v47\n"
"v_cvt_pk_bf16_f32 v125, v166, v167\n"
"v_mfma_f32_16x16x32_bf16 v[160:163], v[212:215], a[20:23], v[160:163]\n"
"ds_write2st64_b64 v255, v[64:65], v[66:67] offset0:196 offset1:197\n"
"v_cvt_pk_bf16_f32 v74, v76, v77\n"
"v_accvgpr_write_b32 a152, v8\n"
"v_exp_f32 v174, v174\n"
"v_exp_f32 v175, v175\n"
"v_exp_f32 v177, v177\n"
"v_exp_f32 v156, v156\n"
"v_mfma_f32_16x16x32_bf16 v[164:167], v[208:211], a[32:35], v[188:191]\n"
"ds_read_b128 v[208:211], v230 offset:4096\n"
"v_cvt_pk_bf16_f32 v26, v28, v29\n"
"v_mfma_f32_16x16x32_bf16 v[164:167], v[212:215], a[36:39], v[164:167]\n"
"ds_read_b128 v[212:215], v231 offset:4096\n"
"v_mov_b32 v203, 0\n"
"v_mfma_f32_16x16x32_bf16 v[164:167], v[216:219], a[40:43], v[164:167]\n"
"ds_read_b128 v[216:219], v230 offset:5120\n"
"v_cvt_pk_bf16_f32 v32, v32, v33\n"
"v_mfma_f32_16x16x32_bf16 v[164:167], v[220:223], a[44:47], v[164:167]\n"
"s_waitcnt lgkmcnt(2)\n"
"ds_read_b128 v[220:223], v231 offset:5120\n"
"v_cvt_pk_bf16_f32 v139, v174, v175\n"
"v_mfma_f32_16x16x32_bf16 v[168:171], v[208:211], a[0:3], v[180:183]\n"
"s_waitcnt lgkmcnt(2)\n"
"v_exp_f32 v154, v154\n"
"v_mfma_f32_16x16x32_bf16 v[168:171], v[212:215], a[4:7], v[168:171]\n"
"s_waitcnt lgkmcnt(1)\n"
"v_accvgpr_write_b32 a106, v122\n"
"v_cvt_pk_bf16_f32 v138, v172, v173\n"
"v_mfma_f32_16x16x32_bf16 v[168:171], v[216:219], a[8:11], v[168:171]\n"
"s_waitcnt lgkmcnt(0)\n"
"v_mov_b32 v198, 0\n"
"v_cvt_pk_bf16_f32 v146, v176, v177\n"
"v_mfma_f32_16x16x32_bf16 v[168:171], v[220:223], a[12:15], v[168:171]\n"
"ds_write_b128 v243, v[56:59] offset:14336\n"
"v_cvt_pk_bf16_f32 v115, v118, v119\n"
"v_accvgpr_write_b32 a131, v147\n"
"v_mov_b32 v196, 0\n"
"v_exp_f32 v159, v159\n"
"v_exp_f32 v164, v164\n"
"v_exp_f32 v166, v166\n"
"v_mfma_f32_16x16x32_bf16 v[172:175], v[216:219], a[24:27], v[184:187]\n"
"v_exp_f32 v157, v157\n"
"v_mfma_f32_16x16x32_bf16 v[172:175], v[220:223], a[28:31], v[172:175]\n"
"v_exp_f32 v165, v165\n"
"v_mfma_f32_16x16x32_bf16 v[172:175], v[208:211], a[16:19], v[172:175]\n"
"v_exp_f32 v160, v160\n"
"v_mfma_f32_16x16x32_bf16 v[172:175], v[212:215], a[20:23], v[172:175]\n"
"s_cbranch_vccnz LPCE0_%=\nLPKE0_%=:\n"
"ds_write2st64_b64 v255, v[72:73], v[74:75] offset0:198 offset1:199\n"
"v_cvt_pk_bf16_f32 v147, v178, v179\n"
"v_cvt_pk_bf16_f32 v126, v152, v153\n"
"v_cvt_pk_bf16_f32 v127, v154, v155\n"
"v_exp_f32 v161, v161\n"
"v_exp_f32 v162, v162\n"
"v_exp_f32 v163, v163\n"
"v_cvt_pk_bf16_f32 v148, v164, v165\n"
"v_mfma_f32_16x16x32_bf16 v[176:179], v[208:211], a[32:35], v[188:191]\n"
"v_exp_f32 v158, v158\n"
"v_mfma_f32_16x16x32_bf16 v[176:179], v[212:215], a[36:39], v[176:179]\n"
"v_permlane16_swap_b32 v40, v42\n"
"v_mfma_f32_16x16x32_bf16 v[176:179], v[216:219], a[40:43], v[176:179]\n"
"v_exp_f32 v175, v175\n"
"v_mfma_f32_16x16x32_bf16 v[176:179], v[220:223], a[44:47], v[176:179]\n"
"v_permlane16_swap_b32 v9, v11\n"
"v_accvgpr_write_b32 a155, v11\n"
"v_permlane16_swap_b32 v24, v26\n"
"v_cvt_pk_bf16_f32 v132, v156, v157\n"
"v_exp_f32 v168, v168\n"
"v_exp_f32 v172, v172\n"
"v_exp_f32 v173, v173\n"
"s_waitcnt vmcnt(8) lgkmcnt(0)\n"
"s_barrier\n"
"ds_read_b128 v[208:211], v228 offset:16384\n"
"ds_read_b128 v[212:215], v229 offset:16384\n"
"ds_read_b128 v[216:219], v228 offset:17408\n"
"ds_read_b128 v[220:223], v229 offset:17408\n"
"s_waitcnt lgkmcnt(3)\n"
"v_mfma_f32_16x16x32_bf16 v[152:155], v[208:211], a[0:3], v[180:183]\n"
"ds_read_b64_tr_b16 v[226:227], v239 offset:4096\n"
"v_cvt_pk_bf16_f32 v133, v158, v159\n"
"s_waitcnt lgkmcnt(3)\n"
"v_mfma_f32_16x16x32_bf16 v[152:155], v[212:215], a[4:7], v[152:155]\n"
"ds_read_b64_tr_b16 a[250:251], v238 offset:4096\n"
"ds_read_b64_tr_b16 a[252:253], v235 offset:0\n"
"s_waitcnt lgkmcnt(4)\n"
"v_mfma_f32_16x16x32_bf16 v[152:155], v[216:219], a[8:11], v[152:155]\n"
"s_waitcnt lgkmcnt(3)\n"
"v_exp_f32 v177, v177\n"
"v_mfma_f32_16x16x32_bf16 v[152:155], v[220:223], a[12:15], v[152:155]\n"
"ds_read_b64_tr_b16 a[254:255], v235 offset:4096\n"
"ds_read_b128 a[132:135], v249 offset:32768\n"
"v_cvt_pk_bf16_f32 v140, v160, v161\n"
"v_exp_f32 v167, v167\n"
"v_exp_f32 v169, v169\n"
"v_exp_f32 v171, v171\n"
"v_exp_f32 v174, v174\n"
"v_exp_f32 v176, v176\n"
"v_exp_f32 v178, v178\n"
"v_mfma_f32_16x16x32_bf16 v[156:159], v[216:219], a[24:27], v[184:187]\n"
"v_exp_f32 v170, v170\n"
"v_mfma_f32_16x16x32_bf16 v[156:159], v[220:223], a[28:31], v[156:159]\n"
"ds_read_b128 a[156:159], v249 offset:35840\n"
"v_cvt_pk_bf16_f32 v141, v162, v163\n"
"v_mfma_f32_16x16x32_bf16 v[156:159], v[208:211], a[16:19], v[156:159]\n"
"ds_read_b64_tr_b16 a[244:245], v234 offset:0\n"
"v_accvgpr_write_b32 a184, v40\n"
"v_mfma_f32_16x16x32_bf16 v[156:159], v[212:215], a[20:23], v[156:159]\n"
"ds_read_b64_tr_b16 a[246:247], v234 offset:4096\n"
"v_accvgpr_write_b32 a154, v10\n"
"v_mfma_f32_16x16x32_bf16 v[160:163], v[208:211], a[32:35], v[188:191]\n"
"ds_read_b64_tr_b16 a[248:249], v238 offset:0\n"
"v_cvt_pk_bf16_f32 v134, v168, v169\n"
"v_mfma_f32_16x16x32_bf16 v[160:163], v[212:215], a[36:39], v[160:163]\n"
"ds_read_b128 a[188:191], v249 offset:52224\n"
"v_cvt_pk_bf16_f32 v1, v2, v3\n"
"v_mfma_f32_16x16x32_bf16 v[160:163], v[216:219], a[40:43], v[160:163]\n"
"ds_read_b128 a[124:127], v248 offset:52224\n"
"v_cvt_pk_bf16_f32 v149, v166, v167\n"
"v_mfma_f32_16x16x32_bf16 v[160:163], v[220:223], a[44:47], v[160:163]\n"
"ds_read_b128 a[180:183], v249 offset:51200\n"
"v_permlane16_swap_b32 v41, v43\n"
"v_accvgpr_write_b32 a242, v254\n"
"v_cvt_pk_bf16_f32 v142, v172, v173\n"
"v_cvt_pk_bf16_f32 v143, v174, v175\n"
"v_exp_f32 v179, v179\n"
"v_exp_f32 v152, v152\n"
"v_mfma_f32_16x16x32_bf16 v[164:167], v[216:219], a[200:203], v[192:195]\n"
"ds_read_b64_tr_b16 v[224:225], v239 offset:0\n"
"v_cvt_pk_bf16_f32 v150, v176, v177\n"
"v_mfma_f32_16x16x32_bf16 v[164:167], v[220:223], a[204:207], v[164:167]\n"
"ds_read_b128 a[164:167], v249 offset:49152\n"
"v_accvgpr_write_b32 a153, v9\n"
"v_mfma_f32_16x16x32_bf16 v[164:167], v[208:211], a[192:195], v[164:167]\n"
"ds_read_b128 a[116:119], v248 offset:51200\n"
"v_cvt_pk_bf16_f32 v135, v170, v171\n"
"v_mfma_f32_16x16x32_bf16 v[164:167], v[212:215], a[196:199], v[164:167]\n"
"s_waitcnt lgkmcnt(7)\n"
"ds_read_b128 a[100:103], v248 offset:49152\n"
"v_cvt_pk_bf16_f32 v114, v116, v117\n"
"v_mfma_f32_16x16x32_bf16 a[216:219], v[124:127], a[244:247], 0\n"
"ds_read_b128 a[140:143], v249 offset:33792\n"
"v_cvt_pk_bf16_f32 v27, v30, v31\n"
"v_mfma_f32_16x16x32_bf16 v[8:11], v[128:131], a[244:247], 0\n"
"s_waitcnt lgkmcnt(7)\n"
"ds_read_b128 v[216:219], v228 offset:21504\n"
"v_mov_b32 v200, 0\n"
"v_mfma_f32_16x16x32_bf16 v[8:11], v[132:135], a[248:251], v[8:11]\n"
"ds_read_b128 a[148:151], v249 offset:34816\n"
"v_permlane16_swap_b32 v113, v115\n"
"v_cvt_pk_bf16_f32 v3, v6, v7\n"
"v_accvgpr_write_b32 a185, v41\n"
"v_accvgpr_write_b32 a186, v42\n"
"v_accvgpr_write_b32 a187, v43\n"
"v_exp_f32 v153, v153\n"
"v_exp_f32 v163, v163\n"
"v_exp_f32 v167, v167\n"
"v_mfma_f32_16x16x32_bf16 v[40:43], v[136:139], a[244:247], 0\n"
"ds_read_b128 a[108:111], v248 offset:50176\n"
"v_accvgpr_write_b32 a170, v26\n"
"v_mfma_f32_16x16x32_bf16 v[40:43], v[140:143], a[248:251], v[40:43]\n"
"ds_read_b128 a[172:175], v249 offset:50176\n"
"v_permlane16_swap_b32 v25, v27\n"
"v_accvgpr_write_b32 a169, v25\n"
"v_cvt_pk_bf16_f32 v33, v34, v35\n"
"v_accvgpr_write_b32 a243, v254\n"
"v_cvt_pk_bf16_f32 v151, v178, v179\n"
"v_exp_f32 v154, v154\n"
"v_exp_f32 v165, v165\n"
"v_mfma_f32_16x16x32_bf16 v[72:75], v[144:147], a[244:247], 0\n"
"ds_read_b128 v[220:223], v229 offset:21504\n"
"v_cvt_pk_bf16_f32 v96, v152, v153\n"
"v_mfma_f32_16x16x32_bf16 v[72:75], v[148:151], a[248:251], v[72:75]\n"
"s_waitcnt lgkmcnt(7)\n"
"v_exp_f32 v164, v164\n"
"v_mfma_f32_16x16x32_bf16 v[76:79], v[148:151], v[224:227], 0\n"
"ds_read_b128 v[208:211], v228 offset:20480\n"
"v_accvgpr_write_b32 a105, v121\n"
"v_mfma_f32_16x16x32_bf16 v[76:79], v[144:147], a[252:255], v[76:79]\n"
"ds_read_b128 v[212:215], v229 offset:20480\n"
"v_cvt_pk_bf16_f32 v2, v4, v5\n"
"v_mfma_f32_16x16x32_bf16 v[44:47], v[140:143], v[224:227], 0\n"
"v_exp_f32 v158, v158\n"
"v_mfma_f32_16x16x32_bf16 v[44:47], v[136:139], a[252:255], v[44:47]\n"
"v_exp_f32 v161, v161\n"
"v_mfma_f32_16x16x32_bf16 v[12:15], v[132:135], v[224:227], 0\n"
"v_exp_f32 v160, v160\n"
"v_mfma_f32_16x16x32_bf16 v[12:15], v[128:131], a[252:255], v[12:15]\n"
"ds_read_b64_tr_b16 a[244:245], v234 offset:1024\n"
"v_accvgpr_write_b32 a168, v24\n"
"v_mfma_f32_16x16x32_bf16 a[220:223], v[124:127], a[252:255], 0\n"
"s_waitcnt lgkmcnt(3)\n"
"v_permlane16_swap_b32 v112, v114\n"
"v_smfmac_f32_16x16x64_bf16 v[196:199], a[240:243], v[128:135], v241\n"
"s_add_u32 m0, s44, 98304\n"
"ds_read_b64_tr_b16 v[226:227], v239 offset:5120\n"
"v_bfe_u32 v248, v242, 8, 2\n"
"v_mfma_f32_16x16x32_bf16 v[202:205], a[240:243], v[124:127], v[202:205]\n"
"buffer_load_dwordx4 v240, s[20:23], s56 offen nt lds\n"
"v_mfma_f32_16x16x32_bf16 v[168:171], v[216:219], a[8:11], v[180:183]\n"
"v_exp_f32 v156, v156\n"
"v_mfma_f32_16x16x32_bf16 v[168:171], v[220:223], a[12:15], v[168:171]\n"
"s_waitcnt lgkmcnt(2)\n"
"v_exp_f32 v155, v155\n"
"v_mfma_f32_16x16x32_bf16 v[168:171], v[208:211], a[0:3], v[168:171]\n"
"ds_read_b64_tr_b16 a[252:253], v235 offset:1024\n"
"v_accvgpr_write_b32 a171, v27\n"
"v_mfma_f32_16x16x32_bf16 v[168:171], v[212:215], a[4:7], v[168:171]\n"
"ds_read_b64_tr_b16 a[250:251], v238 offset:5120\n"
"v_accvgpr_write_b32 a97, v113\n"
"v_mfma_f32_16x16x32_bf16 v[172:175], v[208:211], a[16:19], v[184:187]\n"
"ds_read_b64_tr_b16 a[248:249], v238 offset:1024\n"
"v_cvt_pk_bf16_f32 v35, v38, v39\n"
"v_mfma_f32_16x16x32_bf16 v[172:175], v[212:215], a[20:23], v[172:175]\n"
"ds_read_b64_tr_b16 a[246:247], v234 offset:5120\n"
"v_cvt_pk_bf16_f32 v97, v154, v155\n"
"v_mfma_f32_16x16x32_bf16 v[172:175], v[216:219], a[24:27], v[172:175]\n"
"s_add_u32 s40, s56, s52\n"
"ds_read_b64_tr_b16 a[254:255], v235 offset:5120\n"
"v_cvt_pk_bf16_f32 v16, v16, v17\n"
"v_mfma_f32_16x16x32_bf16 v[172:175], v[220:223], a[28:31], v[172:175]\n"
"buffer_load_dwordx4 v240, s[20:23], s40 offen offset:1024 nt lds\n"
"v_mfma_f32_16x16x32_bf16 v[176:179], v[216:219], a[40:43], v[188:191]\n"
"v_exp_f32 v170, v170\n"
"v_mfma_f32_16x16x32_bf16 v[176:179], v[220:223], a[44:47], v[176:179]\n"
"v_exp_f32 v157, v157\n"
"v_mfma_f32_16x16x32_bf16 v[176:179], v[208:211], a[32:35], v[176:179]\n"
"ds_read_b64_tr_b16 v[224:225], v239 offset:1024\n"
"v_lshlrev_b32 v248, 6, v248\n"
"v_mfma_f32_16x16x32_bf16 v[176:179], v[212:215], a[36:39], v[176:179]\n"
"v_exp_f32 v159, v159\n"
"v_mfma_f32_16x16x32_bf16 v[152:155], v[208:211], a[192:195], v[192:195]\n"
"v_permlane16_swap_b32 v1, v3\n"
"v_mfma_f32_16x16x32_bf16 v[152:155], v[212:215], a[196:199], v[152:155]\n"
"ds_read_b128 v[212:215], v231 offset:16384\n"
"v_accvgpr_write_b32 a145, v1\n"
"v_mfma_f32_16x16x32_bf16 v[152:155], v[216:219], a[200:203], v[152:155]\n"
"v_exp_f32 v172, v172\n"
"v_mfma_f32_16x16x32_bf16 v[152:155], v[220:223], a[204:207], v[152:155]\n"
"s_waitcnt lgkmcnt(2)\n"
"ds_read_b128 v[216:219], v230 offset:17408\n"
"v_accvgpr_write_b32 a98, v114\n"
"v_mfma_f32_16x16x32_bf16 a[232:235], v[124:127], a[244:247], 0\n"
"v_permlane16_swap_b32 v33, v35\n"
"v_mfma_f32_16x16x32_bf16 v[24:27], v[128:131], a[244:247], 0\n"
"s_add_u32 s40, s56, s53\n"
"v_accvgpr_write_b32 a96, v112\n"
"v_cvt_pk_bf16_f32 v105, v158, v159\n"
"v_mfma_f32_16x16x32_bf16 v[24:27], v[132:135], a[248:251], v[24:27]\n"
"buffer_load_dwordx4 v240, s[20:23], s40 offen offset:2048 nt lds\n"
"v_mfma_f32_16x16x32_bf16 v[56:59], v[136:139], a[244:247], 0\n"
"v_exp_f32 v174, v174\n"
"v_mfma_f32_16x16x32_bf16 v[56:59], v[140:143], a[248:251], v[56:59]\n"
"v_accvgpr_write_b32 a104, v120\n"
"v_accvgpr_write_b32 a147, v3\n"
"v_mfma_f32_16x16x32_bf16 v[88:91], v[144:147], a[244:247], 0\n"
"ds_read_b128 v[220:223], v231 offset:17408\n"
"v_cvt_pk_bf16_f32 v104, v156, v157\n"
"v_mfma_f32_16x16x32_bf16 v[88:91], v[148:151], a[248:251], v[88:91]\n"
"s_waitcnt lgkmcnt(2)\n"
"ds_read_b128 v[208:211], v230 offset:16384\n"
"v_mov_b32 v201, 0\n"
"v_mfma_f32_16x16x32_bf16 v[92:95], v[148:151], v[224:227], 0\n"
"s_add_u32 s40, s56, s54\n"
"v_exp_f32 v171, v171\n"
"v_mfma_f32_16x16x32_bf16 v[92:95], v[144:147], a[252:255], v[92:95]\n"
"buffer_load_dwordx4 v240, s[20:23], s40 offen offset:3072 nt lds\n"
"v_permlane16_swap_b32 v0, v2\n"
"v_accvgpr_write_b32 a144, v0\n"
"v_cvt_pk_bf16_f32 v17, v18, v19\n"
"v_xor_b32 v248, v248, v242\n"
"v_bfe_u32 v249, v242, 4, 2\n"
"v_exp_f32 v169, v169\n"
"v_mfma_f32_16x16x32_bf16 v[60:63], v[140:143], v[224:227], 0\n"
"s_add_u32 s55, s55, s59\n"
"v_exp_f32 v162, v162\n"
"v_mfma_f32_16x16x32_bf16 v[60:63], v[136:139], a[252:255], v[60:63]\n"
"s_or_b32 s55, s55, s58\n"
"v_cvt_pk_bf16_f32 v112, v160, v161\n"
"v_cvt_pk_bf16_f32 v113, v162, v163\n"
"v_mfma_f32_16x16x32_bf16 v[28:31], v[132:135], v[224:227], 0\n"
"s_xor_b32 s56, s55, s57\n"
"ds_read_b64_tr_b16 a[244:245], v232 offset:0\n"
"v_accvgpr_write_b32 a99, v115\n"
"v_mfma_f32_16x16x32_bf16 v[28:31], v[128:131], a[252:255], v[28:31]\n"
"ds_read_b64_tr_b16 a[246:247], v232 offset:4096\n"
"ds_read_b64_tr_b16 v[226:227], v237 offset:4096\n"
"v_mfma_f32_16x16x32_bf16 a[236:239], v[124:127], a[252:255], 0\n"
"v_exp_f32 v166, v166\n"
"v_exp_f32 v168, v168\n"
"v_exp_f32 v175, v175\n"
"v_exp_f32 v177, v177\n"
"v_exp_f32 v179, v179\n"
"v_smfmac_f32_16x16x64_bf16 v[198:201], a[240:243], v[136:143], v241\n"
"s_waitcnt lgkmcnt(3)\n"
"ds_read_b64_tr_b16 a[248:249], v236 offset:0\n"
"v_cvt_pk_bf16_f32 v121, v166, v167\n"
"v_mfma_f32_16x16x32_bf16 v[156:159], v[208:211], a[0:3], v[180:183]\n"
"ds_read_b64_tr_b16 v[224:225], v237 offset:0\n"
"v_cvt_pk_bf16_f32 v34, v36, v37\n"
"v_mfma_f32_16x16x32_bf16 v[156:159], v[212:215], a[4:7], v[156:159]\n"
"ds_read_b64_tr_b16 a[250:251], v236 offset:4096\n"
"v_accvgpr_write_b32 a146, v2\n"
"v_mfma_f32_16x16x32_bf16 v[156:159], v[216:219], a[8:11], v[156:159]\n"
"s_add_u32 m0, s44, 49152\n"
"v_exp_f32 v154, v154\n"
"v_mfma_f32_16x16x32_bf16 v[156:159], v[220:223], a[12:15], v[156:159]\n"
"buffer_load_dwordx4 v240, s[16:19], s56 offen nt lds\n"
"v_mfma_f32_16x16x32_bf16 v[160:163], v[216:219], a[24:27], v[184:187]\n"
"v_exp_f32 v152, v152\n"
"v_mfma_f32_16x16x32_bf16 v[160:163], v[220:223], a[28:31], v[160:163]\n"
"v_cvt_pk_bf16_f32 v18, v20, v21\n"
"v_cvt_pk_bf16_f32 v120, v164, v165\n"
"v_mfma_f32_16x16x32_bf16 v[160:163], v[208:211], a[16:19], v[160:163]\n"
"v_permlane16_swap_b32 v32, v34\n"
"v_mfma_f32_16x16x32_bf16 v[160:163], v[212:215], a[20:23], v[160:163]\n"
"v_exp_f32 v178, v178\n"
"v_mfma_f32_16x16x32_bf16 v[164:167], v[208:211], a[32:35], v[188:191]\n"
"ds_read_b64_tr_b16 a[252:253], v233 offset:0\n"
"ds_read_b128 v[208:211], v230 offset:20480\n"
"v_mfma_f32_16x16x32_bf16 v[164:167], v[212:215], a[36:39], v[164:167]\n"
"v_accvgpr_write_b32 a177, v33\n"
"v_accvgpr_write_b32 a179, v35\n"
"v_mfma_f32_16x16x32_bf16 v[164:167], v[216:219], a[40:43], v[164:167]\n"
"ds_read_b64_tr_b16 a[254:255], v233 offset:4096\n"
"v_cvt_pk_bf16_f32 v99, v170, v171\n"
"v_mfma_f32_16x16x32_bf16 v[164:167], v[220:223], a[44:47], v[164:167]\n"
"s_waitcnt lgkmcnt(3)\n"
"ds_read_b128 v[212:215], v231 offset:20480\n"
"v_accvgpr_write_b32 a176, v32\n"
"v_mfma_f32_16x16x32_bf16 a[208:211], v[124:127], a[244:247], 0\n"
"v_accvgpr_write_b32 a107, v123\n"
"v_cvt_pk_bf16_f32 v115, v178, v179\n"
"v_mfma_f32_16x16x32_bf16 v[0:3], v[128:131], a[244:247], 0\n"
"s_add_u32 s40, s56, s52\n"
"ds_read_b128 v[220:223], v231 offset:21504\n"
"v_accvgpr_write_b32 a178, v34\n"
"v_mfma_f32_16x16x32_bf16 v[0:3], v[132:135], a[248:251], v[0:3]\n"
"buffer_load_dwordx4 v240, s[16:19], s40 offen offset:1024 nt lds\n"
"v_mfma_f32_16x16x32_bf16 v[32:35], v[136:139], a[244:247], 0\n"
"v_exp_f32 v176, v176\n"
"v_mfma_f32_16x16x32_bf16 v[32:35], v[140:143], a[248:251], v[32:35]\n"
"v_exp_f32 v156, v156\n"
"v_mfma_f32_16x16x32_bf16 v[64:67], v[144:147], a[244:247], 0\n"
"v_exp_f32 v167, v167\n"
"v_mfma_f32_16x16x32_bf16 v[64:67], v[148:151], a[248:251], v[64:67]\n"
"ds_read_b64_tr_b16 a[248:249], v236 offset:1024\n"
"v_cvt_pk_bf16_f32 v19, v22, v23\n"
"v_permlane16_swap_b32 v16, v18\n"
"v_mul_u32_u24 v249, 20, v249\n"
"v_exp_f32 v173, v173\n"
"v_exp_f32 v153, v153\n"
"v_exp_f32 v158, v158\n"
"v_exp_f32 v163, v163\n"
"v_exp_f32 v165, v165\n"
"v_mfma_f32_16x16x32_bf16 v[68:71], v[148:151], v[224:227], 0\n"
"s_waitcnt lgkmcnt(2)\n"
"s_add_u32 s40, s56, s53\n"
"ds_read_b128 v[216:219], v230 offset:21504\n"
"v_cvt_pk_bf16_f32 v98, v168, v169\n"
"v_mfma_f32_16x16x32_bf16 v[68:71], v[144:147], a[252:255], v[68:71]\n"
"buffer_load_dwordx4 v240, s[16:19], s40 offen offset:2048 nt lds\n"
"v_mfma_f32_16x16x32_bf16 v[36:39], v[140:143], v[224:227], 0\n"
"v_exp_f32 v162, v162\n"
"v_mfma_f32_16x16x32_bf16 v[36:39], v[136:139], a[252:255], v[36:39]\n"
"v_exp_f32 v160, v160\n"
"v_mfma_f32_16x16x32_bf16 v[4:7], v[132:135], v[224:227], 0\n"
"ds_read_b64_tr_b16 a[250:251], v236 offset:5120\n"
"v_accvgpr_write_b32 a160, v16\n"
"v_mfma_f32_16x16x32_bf16 v[4:7], v[128:131], a[252:255], v[4:7]\n"
"v_exp_f32 v166, v166\n"
"v_mfma_f32_16x16x32_bf16 a[212:215], v[124:127], a[252:255], 0\n"
"ds_read_b64_tr_b16 v[224:225], v237 offset:1024\n"
"v_bfe_u32 v254, v242, 6, 4\n"
"v_smfmac_f32_16x16x64_bf16 v[200:203], a[240:243], v[144:151], v241\n"
"ds_read_b64_tr_b16 a[244:245], v232 offset:1024\n"
"v_cvt_pk_bf16_f32 v122, v152, v153\n"
"v_mfma_f32_16x16x32_bf16 v[168:171], v[208:211], a[0:3], v[180:183]\n"
"v_cvt_pk_bf16_f32 v107, v174, v175\n"
"v_cvt_pk_bf16_f32 v114, v176, v177\n"
"v_mfma_f32_16x16x32_bf16 v[168:171], v[212:215], a[4:7], v[168:171]\n"
"s_waitcnt lgkmcnt(3)\n"
"ds_read_b64_tr_b16 a[246:247], v232 offset:5120\n"
"v_cvt_pk_bf16_f32 v106, v172, v173\n"
"v_mfma_f32_16x16x32_bf16 v[168:171], v[216:219], a[8:11], v[168:171]\n"
"s_add_u32 s40, s56, s54\n"
"v_permlane16_swap_b32 v17, v19\n"
"v_mfma_f32_16x16x32_bf16 v[168:171], v[220:223], a[12:15], v[168:171]\n"
"buffer_load_dwordx4 v240, s[16:19], s40 offen offset:3072 nt lds\n"
"v_mfma_f32_16x16x32_bf16 v[172:175], v[216:219], a[24:27], v[184:187]\n"
"v_exp_f32 v159, v159\n"
"v_mfma_f32_16x16x32_bf16 v[172:175], v[220:223], a[28:31], v[172:175]\n"
"v_exp_f32 v161, v161\n"
"v_mfma_f32_16x16x32_bf16 v[172:175], v[208:211], a[16:19], v[172:175]\n"
"v_exp_f32 v155, v155\n"
"v_mfma_f32_16x16x32_bf16 v[172:175], v[212:215], a[20:23], v[172:175]\n"
"s_cbranch_vccnz LPCE1_%=\nLPKE1_%=:\n"
"ds_read_b64_tr_b16 a[252:253], v233 offset:1024\n"
"v_mfma_f32_16x16x32_bf16 v[176:179], v[208:211], a[32:35], v[188:191]\n"
"v_accvgpr_write_b32 a162, v18\n"
"v_accvgpr_write_b32 a163, v19\n"
"v_mfma_f32_16x16x32_bf16 v[176:179], v[212:215], a[36:39], v[176:179]\n"
"ds_read_b64_tr_b16 v[226:227], v237 offset:5120\n"
"v_accvgpr_write_b32 a161, v17\n"
"v_mfma_f32_16x16x32_bf16 v[176:179], v[216:219], a[40:43], v[176:179]\n"
"v_exp_f32 v164, v164\n"
"v_mfma_f32_16x16x32_bf16 v[176:179], v[220:223], a[44:47], v[176:179]\n"
"s_waitcnt lgkmcnt(2)\n"
"ds_read_b64_tr_b16 a[254:255], v233 offset:5120\n"
"v_cvt_pk_bf16_f32 v109, v162, v163\n"
"v_mfma_f32_16x16x32_bf16 a[224:227], v[124:127], a[244:247], 0\n"
"v_exp_f32 v157, v157\n"
"v_mfma_f32_16x16x32_bf16 v[16:19], v[128:131], a[244:247], 0\n"
"s_waitcnt vmcnt(8)\n"
"v_cvt_pk_bf16_f32 v100, v156, v157\n"
"v_cvt_pk_bf16_f32 v101, v158, v159\n"
"v_mfma_f32_16x16x32_bf16 v[16:19], v[132:135], a[248:251], v[16:19]\n"
"s_barrier\n"
"ds_read_b128 v[212:215], v229 offset:32768\n"
"v_xor_b32 v249, v249, v254\n"
"v_mfma_f32_16x16x32_bf16 v[48:51], v[136:139], a[244:247], 0\n"
"ds_read_b128 v[216:219], v228 offset:33792\n"
"ds_read_b128 v[220:223], v229 offset:33792\n"
"v_mfma_f32_16x16x32_bf16 v[48:51], v[140:143], a[248:251], v[48:51]\n"
"ds_read_b128 v[208:211], v228 offset:32768\n"
"v_cvt_pk_bf16_f32 v117, v166, v167\n"
"v_mfma_f32_16x16x32_bf16 v[80:83], v[144:147], a[244:247], 0\n"
"ds_read_b64_tr_b16 a[246:247], v234 offset:20480\n"
"v_cvt_pk_bf16_f32 v123, v154, v155\n"
"v_mfma_f32_16x16x32_bf16 v[80:83], v[148:151], a[248:251], v[80:83]\n"
"s_waitcnt lgkmcnt(6)\n"
"ds_read_b64_tr_b16 a[250:251], v238 offset:20480\n"
"v_lshl_add_u32 v249, v249, 4, s44\n"
"v_mfma_f32_16x16x32_bf16 v[84:87], v[148:151], v[224:227], 0\n"
"s_waitcnt lgkmcnt(6)\n"
"v_exp_f32 v177, v177\n"
"v_mfma_f32_16x16x32_bf16 v[84:87], v[144:147], a[252:255], v[84:87]\n"
"v_exp_f32 v168, v168\n"
"v_mfma_f32_16x16x32_bf16 v[52:55], v[140:143], v[224:227], 0\n"
"v_exp_f32 v176, v176\n"
"v_mfma_f32_16x16x32_bf16 v[52:55], v[136:139], a[252:255], v[52:55]\n"
"v_exp_f32 v174, v174\n"
"v_mfma_f32_16x16x32_bf16 v[20:23], v[132:135], v[224:227], 0\n"
"v_exp_f32 v169, v169\n"
"v_mfma_f32_16x16x32_bf16 v[20:23], v[128:131], a[252:255], v[20:23]\n"
"v_cvt_pk_bf16_f32 v116, v164, v165\n"
"v_cvt_pk_bf16_f32 v118, v176, v177\n"
"v_mfma_f32_16x16x32_bf16 a[228:231], v[124:127], a[252:255], 0\n"
"s_waitcnt lgkmcnt(1)\n"
"ds_read_b64_tr_b16 a[254:255], v235 offset:20480\n"
"s_branch LAY2_%=\n"
"LAL_%=:\n"
"v_mfma_f32_16x16x32_bf16 a[224:227], v[124:127], a[244:247], a[224:227]\n"
"v_exp_f32 v162, v162\n"
"v_mfma_f32_16x16x32_bf16 v[16:19], v[128:131], a[244:247], v[16:19]\n"
"s_waitcnt vmcnt(8)\n"
"v_exp_f32 v168, v168\n"
"v_mfma_f32_16x16x32_bf16 v[16:19], v[132:135], a[248:251], v[16:19]\n"
"s_barrier\n"
"v_mfma_f32_16x16x32_bf16 v[48:51], v[136:139], a[244:247], v[48:51]\n"
"ds_read_b128 v[208:211], v228 offset:0\n"
"v_cvt_pk_bf16_f32 v101, v158, v159\n"
"v_mfma_f32_16x16x32_bf16 v[48:51], v[140:143], a[248:251], v[48:51]\n"
"ds_read_b128 v[216:219], v228 offset:1024\n"
"v_cvt_pk_bf16_f32 v109, v162, v163\n"
"v_mfma_f32_16x16x32_bf16 v[80:83], v[144:147], a[244:247], v[80:83]\n"
"ds_read_b128 v[220:223], v229 offset:1024\n"
"v_cvt_pk_bf16_f32 v123, v154, v155\n"
"v_mfma_f32_16x16x32_bf16 v[80:83], v[148:151], a[248:251], v[80:83]\n"
"ds_read_b128 v[212:215], v229 offset:0\n"
"v_cvt_pk_bf16_f32 v117, v166, v167\n"
"v_mfma_f32_16x16x32_bf16 v[84:87], v[148:151], v[224:227], v[84:87]\n"
"v_exp_f32 v176, v176\n"
"v_mfma_f32_16x16x32_bf16 v[84:87], v[144:147], a[252:255], v[84:87]\n"
"v_exp_f32 v169, v169\n"
"v_mfma_f32_16x16x32_bf16 v[52:55], v[140:143], v[224:227], v[52:55]\n"
"ds_read_b64_tr_b16 a[246:247], v234 offset:53248\n"
"v_cvt_pk_bf16_f32 v116, v164, v165\n"
"v_mfma_f32_16x16x32_bf16 v[52:55], v[136:139], a[252:255], v[52:55]\n"
"v_exp_f32 v177, v177\n"
"v_mfma_f32_16x16x32_bf16 v[20:23], v[132:135], v[224:227], v[20:23]\n"
"ds_read_b64_tr_b16 a[250:251], v238 offset:53248\n"
"v_cvt_pk_bf16_f32 v118, v176, v177\n"
"v_mfma_f32_16x16x32_bf16 v[20:23], v[128:131], a[252:255], v[20:23]\n"
"v_exp_f32 v174, v174\n"
"v_mfma_f32_16x16x32_bf16 a[228:231], v[124:127], a[252:255], a[228:231]\n"
"s_waitcnt lgkmcnt(3)\n"
"ds_read_b64_tr_b16 a[254:255], v235 offset:53248\n"
"v_cvt_pk_bf16_f32 v100, v156, v157\n"
"v_mfma_f32_16x16x32_bf16 v[152:155], v[208:211], a[0:3], v[180:183]\n"
"s_waitcnt lgkmcnt(3)\n"
"ds_read_b64_tr_b16 a[248:249], v238 offset:49152\n"
"v_cvt_pk_bf16_f32 v102, v168, v169\n"
"v_mfma_f32_16x16x32_bf16 v[152:155], v[212:215], a[4:7], v[152:155]\n"
"v_exp_f32 v172, v172\n"
"v_mfma_f32_16x16x32_bf16 v[152:155], v[216:219], a[8:11], v[152:155]\n"
"ds_read_b64_tr_b16 a[244:245], v234 offset:49152\n"
"v_cvt_pk_bf16_f32 v108, v160, v161\n"
"v_mfma_f32_16x16x32_bf16 v[152:155], v[220:223], a[12:15], v[152:155]\n"
"v_exp_f32 v175, v175\n"
"v_mfma_f32_16x16x32_bf16 v[156:159], v[216:219], a[24:27], v[184:187]\n"
"ds_read_b64_tr_b16 a[252:253], v235 offset:49152\n"
"v_cvt_pk_bf16_f32 v111, v174, v175\n"
"v_mfma_f32_16x16x32_bf16 v[156:159], v[220:223], a[28:31], v[156:159]\n"
"ds_read_b64_tr_b16 v[224:225], v239 offset:49152\n"
"v_mfma_f32_16x16x32_bf16 v[156:159], v[208:211], a[16:19], v[156:159]\n"
"s_add_u32 m0, s44, 81920\n"
"ds_read_b64_tr_b16 v[226:227], v239 offset:53248\n"
"v_mfma_f32_16x16x32_bf16 v[156:159], v[212:215], a[20:23], v[156:159]\n"
"buffer_load_dwordx4 v240, s[20:23], s56 offen nt lds\n"
"v_mfma_f32_16x16x32_bf16 v[160:163], v[208:211], a[32:35], v[188:191]\n"
"v_exp_f32 v178, v178\n"
"v_mfma_f32_16x16x32_bf16 v[160:163], v[212:215], a[36:39], v[160:163]\n"
"v_exp_f32 v171, v171\n"
"v_mfma_f32_16x16x32_bf16 v[160:163], v[216:219], a[40:43], v[160:163]\n"
"s_add_u32 s40, s56, s52\n"
"v_mfma_f32_16x16x32_bf16 v[160:163], v[220:223], a[44:47], v[160:163]\n"
"buffer_load_dwordx4 v240, s[20:23], s40 offen offset:1024 nt lds\n"
"v_mfma_f32_16x16x32_bf16 v[164:167], v[216:219], a[200:203], v[192:195]\n"
"v_exp_f32 v170, v170\n"
"v_mfma_f32_16x16x32_bf16 v[164:167], v[220:223], a[204:207], v[164:167]\n"
"v_exp_f32 v153, v153\n"
"v_mfma_f32_16x16x32_bf16 v[164:167], v[208:211], a[192:195], v[164:167]\n"
"v_exp_f32 v179, v179\n"
"v_mfma_f32_16x16x32_bf16 v[164:167], v[212:215], a[196:199], v[164:167]\n"
"s_waitcnt lgkmcnt(0)\n"
"v_exp_f32 v158, v158\n"
"v_mfma_f32_16x16x32_bf16 a[216:219], v[120:123], a[244:247], a[216:219]\n"
"ds_read_b128 v[216:219], v228 offset:5120\n"
"v_cvt_pk_bf16_f32 v103, v170, v171\n"
"v_mfma_f32_16x16x32_bf16 v[8:11], v[96:99], a[244:247], v[8:11]\n"
"v_exp_f32 v173, v173\n"
"v_mfma_f32_16x16x32_bf16 v[8:11], v[100:103], a[248:251], v[8:11]\n"
"ds_read_b128 v[208:211], v228 offset:4096\n"
"v_cvt_pk_bf16_f32 v110, v172, v173\n"
"v_mfma_f32_16x16x32_bf16 v[40:43], v[104:107], a[244:247], v[40:43]\n"
"v_exp_f32 v162, v162\n"
"v_mfma_f32_16x16x32_bf16 v[40:43], v[108:111], a[248:251], v[40:43]\n"
"ds_read_b128 v[220:223], v229 offset:5120\n"
"v_cvt_pk_bf16_f32 v119, v178, v179\n"
"v_mfma_f32_16x16x32_bf16 v[72:75], v[112:115], a[244:247], v[72:75]\n"
"s_add_u32 s40, s56, s53\n"
"ds_read_b128 v[212:215], v229 offset:4096\n"
"v_mfma_f32_16x16x32_bf16 v[72:75], v[116:119], a[248:251], v[72:75]\n"
"buffer_load_dwordx4 v240, s[20:23], s40 offen offset:2048 nt lds\n"
"v_mfma_f32_16x16x32_bf16 v[76:79], v[116:119], v[224:227], v[76:79]\n"
"v_exp_f32 v163, v163\n"
"v_mfma_f32_16x16x32_bf16 v[76:79], v[112:115], a[252:255], v[76:79]\n"
"v_exp_f32 v156, v156\n"
"v_mfma_f32_16x16x32_bf16 v[44:47], v[108:111], v[224:227], v[44:47]\n"
"v_exp_f32 v160, v160\n"
"v_mfma_f32_16x16x32_bf16 v[44:47], v[104:107], a[252:255], v[44:47]\n"
"v_exp_f32 v155, v155\n"
"v_mfma_f32_16x16x32_bf16 v[12:15], v[100:103], v[224:227], v[12:15]\n"
"ds_read_b64_tr_b16 v[226:227], v239 offset:54272\n"
"v_mfma_f32_16x16x32_bf16 v[12:15], v[96:99], a[252:255], v[12:15]\n"
"ds_read_b64_tr_b16 a[248:249], v238 offset:50176\n"
"v_mfma_f32_16x16x32_bf16 a[220:223], v[120:123], a[252:255], a[220:223]\n"
"v_exp_f32 v154, v154\n"
"v_smfmac_f32_16x16x64_bf16 v[196:199], a[240:243], v[96:103], v241\n"
"ds_read_b64_tr_b16 a[250:251], v238 offset:54272\n"
"v_mfma_f32_16x16x32_bf16 v[202:205], a[240:243], v[120:123], v[202:205]\n"
"s_waitcnt lgkmcnt(3)\n"
"v_exp_f32 v152, v152\n"
"v_mfma_f32_16x16x32_bf16 v[168:171], v[216:219], a[8:11], v[180:183]\n"
"ds_read_b64_tr_b16 a[244:245], v234 offset:50176\n"
"v_cvt_pk_bf16_f32 v128, v152, v153\n"
"v_mfma_f32_16x16x32_bf16 v[168:171], v[220:223], a[12:15], v[168:171]\n"
"v_exp_f32 v165, v165\n"
"v_mfma_f32_16x16x32_bf16 v[168:171], v[208:211], a[0:3], v[168:171]\n"
"ds_read_b64_tr_b16 a[246:247], v234 offset:54272\n"
"v_cvt_pk_bf16_f32 v145, v162, v163\n"
"v_mfma_f32_16x16x32_bf16 v[168:171], v[212:215], a[4:7], v[168:171]\n"
"ds_read_b64_tr_b16 a[254:255], v235 offset:54272\n"
"v_mfma_f32_16x16x32_bf16 v[172:175], v[208:211], a[16:19], v[184:187]\n"
"ds_read_b64_tr_b16 v[224:225], v239 offset:50176\n"
"v_cvt_pk_bf16_f32 v129, v154, v155\n"
"v_mfma_f32_16x16x32_bf16 v[172:175], v[212:215], a[20:23], v[172:175]\n"
"v_mfma_f32_16x16x32_bf16 v[172:175], v[216:219], a[24:27], v[172:175]\n"
"s_add_u32 s40, s56, s54\n"
"ds_read_b64_tr_b16 a[252:253], v235 offset:50176\n"
"v_mfma_f32_16x16x32_bf16 v[172:175], v[220:223], a[28:31], v[172:175]\n"
"buffer_load_dwordx4 v240, s[20:23], s40 offen offset:3072 nt lds\n"
"v_mfma_f32_16x16x32_bf16 v[176:179], v[216:219], a[40:43], v[188:191]\n"
"s_add_u32 s55, s55, s59\n"
"v_exp_f32 v157, v157\n"
"v_mfma_f32_16x16x32_bf16 v[176:179], v[220:223], a[44:47], v[176:179]\n"
"s_or_b32 s55, s55, s58\n"
"v_exp_f32 v171, v171\n"
"v_mfma_f32_16x16x32_bf16 v[176:179], v[208:211], a[32:35], v[176:179]\n"
"s_xor_b32 s56, s55, s57\n"
"v_exp_f32 v159, v159\n"
"v_mfma_f32_16x16x32_bf16 v[176:179], v[212:215], a[36:39], v[176:179]\n"
"v_exp_f32 v167, v167\n"
"v_mfma_f32_16x16x32_bf16 v[152:155], v[208:211], a[192:195], v[192:195]\n"
"v_cvt_pk_bf16_f32 v137, v158, v159\n"
"v_mfma_f32_16x16x32_bf16 v[152:155], v[212:215], a[196:199], v[152:155]\n"
"ds_read_b128 v[208:211], v230 offset:0\n"
"v_cvt_pk_bf16_f32 v136, v156, v157\n"
"v_mfma_f32_16x16x32_bf16 v[152:155], v[216:219], a[200:203], v[152:155]\n"
"v_exp_f32 v166, v166\n"
"v_mfma_f32_16x16x32_bf16 v[152:155], v[220:223], a[204:207], v[152:155]\n"
"s_waitcnt lgkmcnt(1)\n"
"ds_read_b128 v[216:219], v230 offset:1024\n"
"v_mfma_f32_16x16x32_bf16 a[232:235], v[120:123], a[244:247], a[232:235]\n"
"v_exp_f32 v169, v169\n"
"v_mfma_f32_16x16x32_bf16 v[24:27], v[96:99], a[244:247], v[24:27]\n"
"ds_read_b128 v[220:223], v231 offset:1024\n"
"v_mfma_f32_16x16x32_bf16 v[24:27], v[100:103], a[248:251], v[24:27]\n"
"v_exp_f32 v164, v164\n"
"v_mfma_f32_16x16x32_bf16 v[56:59], v[104:107], a[244:247], v[56:59]\n"
"ds_read_b128 v[212:215], v231 offset:0\n"
"v_mfma_f32_16x16x32_bf16 v[56:59], v[108:111], a[248:251], v[56:59]\n"
"v_exp_f32 v178, v178\n"
"v_mfma_f32_16x16x32_bf16 v[88:91], v[112:115], a[244:247], v[88:91]\n"
"s_add_u32 m0, s44, 32768\n"
"ds_read_b64_tr_b16 a[246:247], v232 offset:53248\n"
"v_mfma_f32_16x16x32_bf16 v[88:91], v[116:119], a[248:251], v[88:91]\n"
"buffer_load_dwordx4 v240, s[16:19], s56 offen nt lds\n"
"v_mfma_f32_16x16x32_bf16 v[92:95], v[116:119], v[224:227], v[92:95]\n"
"v_exp_f32 v175, v175\n"
"v_mfma_f32_16x16x32_bf16 v[92:95], v[112:115], a[252:255], v[92:95]\n"
"v_exp_f32 v168, v168\n"
"v_mfma_f32_16x16x32_bf16 v[60:63], v[108:111], v[224:227], v[60:63]\n"
"v_exp_f32 v161, v161\n"
"v_mfma_f32_16x16x32_bf16 v[60:63], v[104:107], a[252:255], v[60:63]\n"
"v_exp_f32 v177, v177\n"
"v_mfma_f32_16x16x32_bf16 v[28:31], v[100:103], v[224:227], v[28:31]\n"
"ds_read_b64_tr_b16 v[226:227], v237 offset:53248\n"
"v_mfma_f32_16x16x32_bf16 v[28:31], v[96:99], a[252:255], v[28:31]\n"
"v_exp_f32 v172, v172\n"
"v_mfma_f32_16x16x32_bf16 a[236:239], v[120:123], a[252:255], a[236:239]\n"
"ds_read_b64_tr_b16 a[250:251], v236 offset:53248\n"
"v_cvt_pk_bf16_f32 v124, v164, v165\n"
"v_smfmac_f32_16x16x64_bf16 v[198:201], a[240:243], v[104:111], v241\n"
"s_waitcnt lgkmcnt(2)\n"
"ds_read_b64_tr_b16 a[252:253], v233 offset:49152\n"
"v_mfma_f32_16x16x32_bf16 v[156:159], v[208:211], a[0:3], v[180:183]\n"
"ds_read_b64_tr_b16 a[248:249], v236 offset:49152\n"
"v_cvt_pk_bf16_f32 v125, v166, v167\n"
"v_mfma_f32_16x16x32_bf16 v[156:159], v[212:215], a[4:7], v[156:159]\n"
"ds_read_b64_tr_b16 v[224:225], v237 offset:49152\n"
"v_cvt_pk_bf16_f32 v130, v168, v169\n"
"v_mfma_f32_16x16x32_bf16 v[156:159], v[216:219], a[8:11], v[156:159]\n"
"ds_read_b64_tr_b16 a[254:255], v233 offset:53248\n"
"v_cvt_pk_bf16_f32 v144, v160, v161\n"
"v_mfma_f32_16x16x32_bf16 v[156:159], v[220:223], a[12:15], v[156:159]\n"
"ds_read_b64_tr_b16 a[244:245], v232 offset:49152\n"
"v_mfma_f32_16x16x32_bf16 v[160:163], v[216:219], a[24:27], v[184:187]\n"
"v_exp_f32 v176, v176\n"
"v_mfma_f32_16x16x32_bf16 v[160:163], v[220:223], a[28:31], v[160:163]\n"
"v_exp_f32 v152, v152\n"
"v_mfma_f32_16x16x32_bf16 v[160:163], v[208:211], a[16:19], v[160:163]\n"
"s_add_u32 s40, s56, s52\n"
"v_cvt_pk_bf16_f32 v146, v176, v177\n"
"v_mfma_f32_16x16x32_bf16 v[160:163], v[212:215], a[20:23], v[160:163]\n"
"buffer_load_dwordx4 v240, s[16:19], s40 offen offset:1024 nt lds\n"
"v_mfma_f32_16x16x32_bf16 v[164:167], v[208:211], a[32:35], v[188:191]\n"
"v_exp_f32 v170, v170\n"
"v_mfma_f32_16x16x32_bf16 v[164:167], v[212:215], a[36:39], v[164:167]\n"
"v_exp_f32 v173, v173\n"
"v_mfma_f32_16x16x32_bf16 v[164:167], v[216:219], a[40:43], v[164:167]\n"
"v_exp_f32 v153, v153\n"
"v_mfma_f32_16x16x32_bf16 v[164:167], v[220:223], a[44:47], v[164:167]\n"
"s_waitcnt lgkmcnt(0)\n"
"v_exp_f32 v174, v174\n"
"v_mfma_f32_16x16x32_bf16 a[208:211], v[120:123], a[244:247], a[208:211]\n"
"ds_read_b128 v[208:211], v230 offset:4096\n"
"v_cvt_pk_bf16_f32 v139, v174, v175\n"
"v_mfma_f32_16x16x32_bf16 v[0:3], v[96:99], a[244:247], v[0:3]\n"
"v_exp_f32 v156, v156\n"
"v_mfma_f32_16x16x32_bf16 v[0:3], v[100:103], a[248:251], v[0:3]\n"
"ds_read_b128 v[220:223], v231 offset:5120\n"
"v_cvt_pk_bf16_f32 v138, v172, v173\n"
"v_mfma_f32_16x16x32_bf16 v[32:35], v[104:107], a[244:247], v[32:35]\n"
"v_exp_f32 v161, v161\n"
"v_mfma_f32_16x16x32_bf16 v[32:35], v[108:111], a[248:251], v[32:35]\n"
"ds_read_b128 v[216:219], v230 offset:5120\n"
"v_cvt_pk_bf16_f32 v126, v152, v153\n"
"v_mfma_f32_16x16x32_bf16 v[64:67], v[112:115], a[244:247], v[64:67]\n"
"s_add_u32 s40, s56, s53\n"
"ds_read_b128 v[212:215], v231 offset:4096\n"
"v_mfma_f32_16x16x32_bf16 v[64:67], v[116:119], a[248:251], v[64:67]\n"
"buffer_load_dwordx4 v240, s[16:19], s40 offen offset:2048 nt lds\n"
"v_mfma_f32_16x16x32_bf16 v[68:71], v[116:119], v[224:227], v[68:71]\n"
"v_exp_f32 v159, v159\n"
"v_mfma_f32_16x16x32_bf16 v[68:71], v[112:115], a[252:255], v[68:71]\n"
"v_exp_f32 v155, v155\n"
"v_mfma_f32_16x16x32_bf16 v[36:39], v[108:111], v[224:227], v[36:39]\n"
"v_exp_f32 v160, v160\n"
"v_mfma_f32_16x16x32_bf16 v[36:39], v[104:107], a[252:255], v[36:39]\n"
"v_exp_f32 v179, v179\n"
"v_mfma_f32_16x16x32_bf16 v[4:7], v[100:103], v[224:227], v[4:7]\n"
"ds_read_b64_tr_b16 a[244:245], v232 offset:50176\n"
"ds_read_b64_tr_b16 v[224:225], v237 offset:50176\n"
"v_mfma_f32_16x16x32_bf16 v[4:7], v[96:99], a[252:255], v[4:7]\n"
"v_exp_f32 v158, v158\n"
"v_mfma_f32_16x16x32_bf16 a[212:215], v[120:123], a[252:255], a[212:215]\n"
"ds_read_b64_tr_b16 a[246:247], v232 offset:54272\n"
"ds_read_b64_tr_b16 a[250:251], v236 offset:54272\n"
"v_smfmac_f32_16x16x64_bf16 v[200:203], a[240:243], v[112:119], v241\n"
"s_waitcnt lgkmcnt(4)\n"
"ds_read_b64_tr_b16 v[226:227], v237 offset:54272\n"
"v_cvt_pk_bf16_f32 v131, v170, v171\n"
"v_mfma_f32_16x16x32_bf16 v[168:171], v[208:211], a[0:3], v[180:183]\n"
"v_exp_f32 v164, v164\n"
"v_mfma_f32_16x16x32_bf16 v[168:171], v[212:215], a[4:7], v[168:171]\n"
"ds_read_b64_tr_b16 a[248:249], v236 offset:50176\n"
"ds_read_b64_tr_b16 a[254:255], v233 offset:54272\n"
"v_mfma_f32_16x16x32_bf16 v[168:171], v[216:219], a[8:11], v[168:171]\n"
"s_add_u32 s40, s56, s54\n"
"ds_read_b64_tr_b16 a[252:253], v233 offset:50176\n"
"v_mfma_f32_16x16x32_bf16 v[168:171], v[220:223], a[12:15], v[168:171]\n"
"buffer_load_dwordx4 v240, s[16:19], s40 offen offset:3072 nt lds\n"
"v_mfma_f32_16x16x32_bf16 v[172:175], v[216:219], a[24:27], v[184:187]\n"
"v_exp_f32 v154, v154\n"
"v_mfma_f32_16x16x32_bf16 v[172:175], v[220:223], a[28:31], v[172:175]\n"
"v_exp_f32 v167, v167\n"
"v_mfma_f32_16x16x32_bf16 v[172:175], v[208:211], a[16:19], v[172:175]\n"
"v_exp_f32 v163, v163\n"
"v_mfma_f32_16x16x32_bf16 v[172:175], v[212:215], a[20:23], v[172:175]\n"
"s_cbranch_vccnz LPCL0_%=\nLPKL0_%=:\n"
"v_cvt_pk_bf16_f32 v147, v178, v179\n"
"v_mfma_f32_16x16x32_bf16 v[176:179], v[208:211], a[32:35], v[188:191]\n"
"v_exp_f32 v165, v165\n"
"v_mfma_f32_16x16x32_bf16 v[176:179], v[212:215], a[36:39], v[176:179]\n"
"v_exp_f32 v157, v157\n"
"v_mfma_f32_16x16x32_bf16 v[176:179], v[216:219], a[40:43], v[176:179]\n"
"v_exp_f32 v166, v166\n"
"v_mfma_f32_16x16x32_bf16 v[176:179], v[220:223], a[44:47], v[176:179]\n"
"s_waitcnt lgkmcnt(0)\n"
"v_mfma_f32_16x16x32_bf16 a[224:227], v[120:123], a[244:247], a[224:227]\n"
"v_exp_f32 v162, v162\n"
"v_mfma_f32_16x16x32_bf16 v[16:19], v[96:99], a[244:247], v[16:19]\n"
"s_waitcnt vmcnt(8)\n"
"v_exp_f32 v168, v168\n"
"v_mfma_f32_16x16x32_bf16 v[16:19], v[100:103], a[248:251], v[16:19]\n"
"s_barrier\n"
"v_mfma_f32_16x16x32_bf16 v[48:51], v[104:107], a[244:247], v[48:51]\n"
"ds_read_b128 v[208:211], v228 offset:16384\n"
"v_cvt_pk_bf16_f32 v133, v158, v159\n"
"v_mfma_f32_16x16x32_bf16 v[48:51], v[108:111], a[248:251], v[48:51]\n"
"ds_read_b128 v[216:219], v228 offset:17408\n"
"v_cvt_pk_bf16_f32 v141, v162, v163\n"
"v_mfma_f32_16x16x32_bf16 v[80:83], v[112:115], a[244:247], v[80:83]\n"
"ds_read_b128 v[220:223], v229 offset:17408\n"
"v_cvt_pk_bf16_f32 v127, v154, v155\n"
"v_mfma_f32_16x16x32_bf16 v[80:83], v[116:119], a[248:251], v[80:83]\n"
"ds_read_b128 v[212:215], v229 offset:16384\n"
"v_cvt_pk_bf16_f32 v149, v166, v167\n"
"v_mfma_f32_16x16x32_bf16 v[84:87], v[116:119], v[224:227], v[84:87]\n"
"v_exp_f32 v176, v176\n"
"v_mfma_f32_16x16x32_bf16 v[84:87], v[112:115], a[252:255], v[84:87]\n"
"v_exp_f32 v169, v169\n"
"v_mfma_f32_16x16x32_bf16 v[52:55], v[108:111], v[224:227], v[52:55]\n"
"ds_read_b64_tr_b16 a[246:247], v234 offset:4096\n"
"v_cvt_pk_bf16_f32 v148, v164, v165\n"
"v_mfma_f32_16x16x32_bf16 v[52:55], v[104:107], a[252:255], v[52:55]\n"
"v_exp_f32 v177, v177\n"
"v_mfma_f32_16x16x32_bf16 v[20:23], v[100:103], v[224:227], v[20:23]\n"
"ds_read_b64_tr_b16 a[250:251], v238 offset:4096\n"
"v_cvt_pk_bf16_f32 v150, v176, v177\n"
"v_mfma_f32_16x16x32_bf16 v[20:23], v[96:99], a[252:255], v[20:23]\n"
"v_exp_f32 v174, v174\n"
"v_mfma_f32_16x16x32_bf16 a[228:231], v[120:123], a[252:255], a[228:231]\n"
"s_waitcnt lgkmcnt(3)\n"
"ds_read_b64_tr_b16 a[254:255], v235 offset:4096\n"
"v_cvt_pk_bf16_f32 v132, v156, v157\n"
"v_mfma_f32_16x16x32_bf16 v[152:155], v[208:211], a[0:3], v[180:183]\n"
"s_waitcnt lgkmcnt(3)\n"
"ds_read_b64_tr_b16 a[248:249], v238 offset:0\n"
"v_cvt_pk_bf16_f32 v134, v168, v169\n"
"v_mfma_f32_16x16x32_bf16 v[152:155], v[212:215], a[4:7], v[152:155]\n"
"v_exp_f32 v172, v172\n"
"v_mfma_f32_16x16x32_bf16 v[152:155], v[216:219], a[8:11], v[152:155]\n"
"ds_read_b64_tr_b16 a[244:245], v234 offset:0\n"
"v_cvt_pk_bf16_f32 v140, v160, v161\n"
"v_mfma_f32_16x16x32_bf16 v[152:155], v[220:223], a[12:15], v[152:155]\n"
"v_exp_f32 v175, v175\n"
"v_mfma_f32_16x16x32_bf16 v[156:159], v[216:219], a[24:27], v[184:187]\n"
"ds_read_b64_tr_b16 a[252:253], v235 offset:0\n"
"v_cvt_pk_bf16_f32 v143, v174, v175\n"
"v_mfma_f32_16x16x32_bf16 v[156:159], v[220:223], a[28:31], v[156:159]\n"
"ds_read_b64_tr_b16 v[224:225], v239 offset:0\n"
"v_mfma_f32_16x16x32_bf16 v[156:159], v[208:211], a[16:19], v[156:159]\n"
"s_add_u32 m0, s44, 98304\n"
"ds_read_b64_tr_b16 v[226:227], v239 offset:4096\n"
"v_mfma_f32_16x16x32_bf16 v[156:159], v[212:215], a[20:23], v[156:159]\n"
"buffer_load_dwordx4 v240, s[20:23], s56 offen nt lds\n"
"v_mfma_f32_16x16x32_bf16 v[160:163], v[208:211], a[32:35], v[188:191]\n"
"v_exp_f32 v178, v178\n"
"v_mfma_f32_16x16x32_bf16 v[160:163], v[212:215], a[36:39], v[160:163]\n"
"v_exp_f32 v171, v171\n"
"v_mfma_f32_16x16x32_bf16 v[160:163], v[216:219], a[40:43], v[160:163]\n"
"s_add_u32 s40, s56, s52\n"
"v_mfma_f32_16x16x32_bf16 v[160:163], v[220:223], a[44:47], v[160:163]\n"
"buffer_load_dwordx4 v240, s[20:23], s40 offen offset:1024 nt lds\n"
"v_mfma_f32_16x16x32_bf16 v[164:167], v[216:219], a[200:203], v[192:195]\n"
"v_exp_f32 v170, v170\n"
"v_mfma_f32_16x16x32_bf16 v[164:167], v[220:223], a[204:207], v[164:167]\n"
"v_exp_f32 v153, v153\n"
"v_mfma_f32_16x16x32_bf16 v[164:167], v[208:211], a[192:195], v[164:167]\n"
"v_exp_f32 v179, v179\n"
"v_mfma_f32_16x16x32_bf16 v[164:167], v[212:215], a[196:199], v[164:167]\n"
"s_waitcnt lgkmcnt(0)\n"
"v_exp_f32 v158, v158\n"
"v_mfma_f32_16x16x32_bf16 a[216:219], v[124:127], a[244:247], a[216:219]\n"
"ds_read_b128 v[216:219], v228 offset:21504\n"
"v_cvt_pk_bf16_f32 v135, v170, v171\n"
"v_mfma_f32_16x16x32_bf16 v[8:11], v[128:131], a[244:247], v[8:11]\n"
"v_exp_f32 v173, v173\n"
"v_mfma_f32_16x16x32_bf16 v[8:11], v[132:135], a[248:251], v[8:11]\n"
"ds_read_b128 v[208:211], v228 offset:20480\n"
"v_cvt_pk_bf16_f32 v142, v172, v173\n"
"v_mfma_f32_16x16x32_bf16 v[40:43], v[136:139], a[244:247], v[40:43]\n"
"v_exp_f32 v162, v162\n"
"v_mfma_f32_16x16x32_bf16 v[40:43], v[140:143], a[248:251], v[40:43]\n"
"ds_read_b128 v[220:223], v229 offset:21504\n"
"v_cvt_pk_bf16_f32 v151, v178, v179\n"
"v_mfma_f32_16x16x32_bf16 v[72:75], v[144:147], a[244:247], v[72:75]\n"
"s_add_u32 s40, s56, s53\n"
"ds_read_b128 v[212:215], v229 offset:20480\n"
"v_mfma_f32_16x16x32_bf16 v[72:75], v[148:151], a[248:251], v[72:75]\n"
"buffer_load_dwordx4 v240, s[20:23], s40 offen offset:2048 nt lds\n"
"v_mfma_f32_16x16x32_bf16 v[76:79], v[148:151], v[224:227], v[76:79]\n"
"v_exp_f32 v163, v163\n"
"v_mfma_f32_16x16x32_bf16 v[76:79], v[144:147], a[252:255], v[76:79]\n"
"v_exp_f32 v156, v156\n"
"v_mfma_f32_16x16x32_bf16 v[44:47], v[140:143], v[224:227], v[44:47]\n"
"v_exp_f32 v160, v160\n"
"v_mfma_f32_16x16x32_bf16 v[44:47], v[136:139], a[252:255], v[44:47]\n"
"v_exp_f32 v155, v155\n"
"v_mfma_f32_16x16x32_bf16 v[12:15], v[132:135], v[224:227], v[12:15]\n"
"ds_read_b64_tr_b16 v[226:227], v239 offset:5120\n"
"v_mfma_f32_16x16x32_bf16 v[12:15], v[128:131], a[252:255], v[12:15]\n"
"ds_read_b64_tr_b16 a[248:249], v238 offset:1024\n"
"v_mfma_f32_16x16x32_bf16 a[220:223], v[124:127], a[252:255], a[220:223]\n"
"v_exp_f32 v154, v154\n"
"v_smfmac_f32_16x16x64_bf16 v[196:199], a[240:243], v[128:135], v241\n"
"ds_read_b64_tr_b16 a[250:251], v238 offset:5120\n"
"v_mfma_f32_16x16x32_bf16 v[202:205], a[240:243], v[124:127], v[202:205]\n"
"s_waitcnt lgkmcnt(3)\n"
"v_exp_f32 v152, v152\n"
"v_mfma_f32_16x16x32_bf16 v[168:171], v[216:219], a[8:11], v[180:183]\n"
"ds_read_b64_tr_b16 a[244:245], v234 offset:1024\n"
"v_cvt_pk_bf16_f32 v96, v152, v153\n"
"v_mfma_f32_16x16x32_bf16 v[168:171], v[220:223], a[12:15], v[168:171]\n"
"v_exp_f32 v165, v165\n"
"v_mfma_f32_16x16x32_bf16 v[168:171], v[208:211], a[0:3], v[168:171]\n"
"ds_read_b64_tr_b16 a[246:247], v234 offset:5120\n"
"v_cvt_pk_bf16_f32 v113, v162, v163\n"
"v_mfma_f32_16x16x32_bf16 v[168:171], v[212:215], a[4:7], v[168:171]\n"
"ds_read_b64_tr_b16 a[254:255], v235 offset:5120\n"
"v_mfma_f32_16x16x32_bf16 v[172:175], v[208:211], a[16:19], v[184:187]\n"
"ds_read_b64_tr_b16 v[224:225], v239 offset:1024\n"
"v_cvt_pk_bf16_f32 v97, v154, v155\n"
"v_mfma_f32_16x16x32_bf16 v[172:175], v[212:215], a[20:23], v[172:175]\n"
"v_mfma_f32_16x16x32_bf16 v[172:175], v[216:219], a[24:27], v[172:175]\n"
"s_add_u32 s40, s56, s54\n"
"ds_read_b64_tr_b16 a[252:253], v235 offset:1024\n"
"v_mfma_f32_16x16x32_bf16 v[172:175], v[220:223], a[28:31], v[172:175]\n"
"buffer_load_dwordx4 v240, s[20:23], s40 offen offset:3072 nt lds\n"
"v_mfma_f32_16x16x32_bf16 v[176:179], v[216:219], a[40:43], v[188:191]\n"
"s_add_u32 s55, s55, s59\n"
"v_exp_f32 v157, v157\n"
"v_mfma_f32_16x16x32_bf16 v[176:179], v[220:223], a[44:47], v[176:179]\n"
"s_or_b32 s55, s55, s58\n"
"v_exp_f32 v171, v171\n"
"v_mfma_f32_16x16x32_bf16 v[176:179], v[208:211], a[32:35], v[176:179]\n"
"s_xor_b32 s56, s55, s57\n"
"v_exp_f32 v159, v159\n"
"v_mfma_f32_16x16x32_bf16 v[176:179], v[212:215], a[36:39], v[176:179]\n"
"v_exp_f32 v167, v167\n"
"v_mfma_f32_16x16x32_bf16 v[152:155], v[208:211], a[192:195], v[192:195]\n"
"v_cvt_pk_bf16_f32 v105, v158, v159\n"
"v_mfma_f32_16x16x32_bf16 v[152:155], v[212:215], a[196:199], v[152:155]\n"
"ds_read_b128 v[208:211], v230 offset:16384\n"
"v_cvt_pk_bf16_f32 v104, v156, v157\n"
"v_mfma_f32_16x16x32_bf16 v[152:155], v[216:219], a[200:203], v[152:155]\n"
"v_exp_f32 v166, v166\n"
"v_mfma_f32_16x16x32_bf16 v[152:155], v[220:223], a[204:207], v[152:155]\n"
"s_waitcnt lgkmcnt(1)\n"
"ds_read_b128 v[216:219], v230 offset:17408\n"
"v_mfma_f32_16x16x32_bf16 a[232:235], v[124:127], a[244:247], a[232:235]\n"
"v_exp_f32 v169, v169\n"
"v_mfma_f32_16x16x32_bf16 v[24:27], v[128:131], a[244:247], v[24:27]\n"
"ds_read_b128 v[220:223], v231 offset:17408\n"
"v_mfma_f32_16x16x32_bf16 v[24:27], v[132:135], a[248:251], v[24:27]\n"
"v_exp_f32 v164, v164\n"
"v_mfma_f32_16x16x32_bf16 v[56:59], v[136:139], a[244:247], v[56:59]\n"
"ds_read_b128 v[212:215], v231 offset:16384\n"
"v_mfma_f32_16x16x32_bf16 v[56:59], v[140:143], a[248:251], v[56:59]\n"
"v_exp_f32 v178, v178\n"
"v_mfma_f32_16x16x32_bf16 v[88:91], v[144:147], a[244:247], v[88:91]\n"
"s_add_u32 m0, s44, 49152\n"
"ds_read_b64_tr_b16 a[246:247], v232 offset:4096\n"
"v_mfma_f32_16x16x32_bf16 v[88:91], v[148:151], a[248:251], v[88:91]\n"
"buffer_load_dwordx4 v240, s[16:19], s56 offen nt lds\n"
"v_mfma_f32_16x16x32_bf16 v[92:95], v[148:151], v[224:227], v[92:95]\n"
"v_exp_f32 v175, v175\n"
"v_mfma_f32_16x16x32_bf16 v[92:95], v[144:147], a[252:255], v[92:95]\n"
"v_exp_f32 v168, v168\n"
"v_mfma_f32_16x16x32_bf16 v[60:63], v[140:143], v[224:227], v[60:63]\n"
"v_exp_f32 v161, v161\n"
"v_mfma_f32_16x16x32_bf16 v[60:63], v[136:139], a[252:255], v[60:63]\n"
"v_exp_f32 v177, v177\n"
"v_mfma_f32_16x16x32_bf16 v[28:31], v[132:135], v[224:227], v[28:31]\n"
"ds_read_b64_tr_b16 v[226:227], v237 offset:4096\n"
"v_mfma_f32_16x16x32_bf16 v[28:31], v[128:131], a[252:255], v[28:31]\n"
"v_exp_f32 v172, v172\n"
"v_mfma_f32_16x16x32_bf16 a[236:239], v[124:127], a[252:255], a[236:239]\n"
"ds_read_b64_tr_b16 a[250:251], v236 offset:4096\n"
"v_cvt_pk_bf16_f32 v120, v164, v165\n"
"v_smfmac_f32_16x16x64_bf16 v[198:201], a[240:243], v[136:143], v241\n"
"s_waitcnt lgkmcnt(2)\n"
"ds_read_b64_tr_b16 a[252:253], v233 offset:0\n"
"v_mfma_f32_16x16x32_bf16 v[156:159], v[208:211], a[0:3], v[180:183]\n"
"ds_read_b64_tr_b16 a[248:249], v236 offset:0\n"
"v_cvt_pk_bf16_f32 v121, v166, v167\n"
"v_mfma_f32_16x16x32_bf16 v[156:159], v[212:215], a[4:7], v[156:159]\n"
"ds_read_b64_tr_b16 v[224:225], v237 offset:0\n"
"v_cvt_pk_bf16_f32 v98, v168, v169\n"
"v_mfma_f32_16x16x32_bf16 v[156:159], v[216:219], a[8:11], v[156:159]\n"
"ds_read_b64_tr_b16 a[254:255], v233 offset:4096\n"
"v_cvt_pk_bf16_f32 v112, v160, v161\n"
"v_mfma_f32_16x16x32_bf16 v[156:159], v[220:223], a[12:15], v[156:159]\n"
"ds_read_b64_tr_b16 a[244:245], v232 offset:0\n"
"v_mfma_f32_16x16x32_bf16 v[160:163], v[216:219], a[24:27], v[184:187]\n"
"v_exp_f32 v176, v176\n"
"v_mfma_f32_16x16x32_bf16 v[160:163], v[220:223], a[28:31], v[160:163]\n"
"v_exp_f32 v152, v152\n"
"v_mfma_f32_16x16x32_bf16 v[160:163], v[208:211], a[16:19], v[160:163]\n"
"s_add_u32 s40, s56, s52\n"
"v_cvt_pk_bf16_f32 v114, v176, v177\n"
"v_mfma_f32_16x16x32_bf16 v[160:163], v[212:215], a[20:23], v[160:163]\n"
"buffer_load_dwordx4 v240, s[16:19], s40 offen offset:1024 nt lds\n"
"v_mfma_f32_16x16x32_bf16 v[164:167], v[208:211], a[32:35], v[188:191]\n"
"v_exp_f32 v170, v170\n"
"v_mfma_f32_16x16x32_bf16 v[164:167], v[212:215], a[36:39], v[164:167]\n"
"v_exp_f32 v173, v173\n"
"v_mfma_f32_16x16x32_bf16 v[164:167], v[216:219], a[40:43], v[164:167]\n"
"v_exp_f32 v153, v153\n"
"v_mfma_f32_16x16x32_bf16 v[164:167], v[220:223], a[44:47], v[164:167]\n"
"s_waitcnt lgkmcnt(0)\n"
"v_exp_f32 v174, v174\n"
"v_mfma_f32_16x16x32_bf16 a[208:211], v[124:127], a[244:247], a[208:211]\n"
"ds_read_b128 v[208:211], v230 offset:20480\n"
"v_cvt_pk_bf16_f32 v107, v174, v175\n"
"v_mfma_f32_16x16x32_bf16 v[0:3], v[128:131], a[244:247], v[0:3]\n"
"v_exp_f32 v156, v156\n"
"v_mfma_f32_16x16x32_bf16 v[0:3], v[132:135], a[248:251], v[0:3]\n"
"ds_read_b128 v[220:223], v231 offset:21504\n"
"v_cvt_pk_bf16_f32 v106, v172, v173\n"
"v_mfma_f32_16x16x32_bf16 v[32:35], v[136:139], a[244:247], v[32:35]\n"
"v_exp_f32 v161, v161\n"
"v_mfma_f32_16x16x32_bf16 v[32:35], v[140:143], a[248:251], v[32:35]\n"
"ds_read_b128 v[216:219], v230 offset:21504\n"
"v_cvt_pk_bf16_f32 v122, v152, v153\n"
"v_mfma_f32_16x16x32_bf16 v[64:67], v[144:147], a[244:247], v[64:67]\n"
"s_add_u32 s40, s56, s53\n"
"ds_read_b128 v[212:215], v231 offset:20480\n"
"v_mfma_f32_16x16x32_bf16 v[64:67], v[148:151], a[248:251], v[64:67]\n"
"buffer_load_dwordx4 v240, s[16:19], s40 offen offset:2048 nt lds\n"
"v_mfma_f32_16x16x32_bf16 v[68:71], v[148:151], v[224:227], v[68:71]\n"
"v_exp_f32 v159, v159\n"
"v_mfma_f32_16x16x32_bf16 v[68:71], v[144:147], a[252:255], v[68:71]\n"
"v_exp_f32 v155, v155\n"
"v_mfma_f32_16x16x32_bf16 v[36:39], v[140:143], v[224:227], v[36:39]\n"
"v_exp_f32 v160, v160\n"
"v_mfma_f32_16x16x32_bf16 v[36:39], v[136:139], a[252:255], v[36:39]\n"
"v_exp_f32 v179, v179\n"
"v_mfma_f32_16x16x32_bf16 v[4:7], v[132:135], v[224:227], v[4:7]\n"
"ds_read_b64_tr_b16 a[244:245], v232 offset:1024\n"
"ds_read_b64_tr_b16 v[224:225], v237 offset:1024\n"
"v_mfma_f32_16x16x32_bf16 v[4:7], v[128:131], a[252:255], v[4:7]\n"
"v_exp_f32 v158, v158\n"
"v_mfma_f32_16x16x32_bf16 a[212:215], v[124:127], a[252:255], a[212:215]\n"
"ds_read_b64_tr_b16 a[246:247], v232 offset:5120\n"
"ds_read_b64_tr_b16 a[250:251], v236 offset:5120\n"
"v_smfmac_f32_16x16x64_bf16 v[200:203], a[240:243], v[144:151], v241\n"
"s_waitcnt lgkmcnt(4)\n"
"ds_read_b64_tr_b16 v[226:227], v237 offset:5120\n"
"v_cvt_pk_bf16_f32 v99, v170, v171\n"
"v_mfma_f32_16x16x32_bf16 v[168:171], v[208:211], a[0:3], v[180:183]\n"
"v_exp_f32 v164, v164\n"
"v_mfma_f32_16x16x32_bf16 v[168:171], v[212:215], a[4:7], v[168:171]\n"
"ds_read_b64_tr_b16 a[248:249], v236 offset:1024\n"
"ds_read_b64_tr_b16 a[254:255], v233 offset:5120\n"
"v_mfma_f32_16x16x32_bf16 v[168:171], v[216:219], a[8:11], v[168:171]\n"
"s_add_u32 s40, s56, s54\n"
"ds_read_b64_tr_b16 a[252:253], v233 offset:1024\n"
"v_mfma_f32_16x16x32_bf16 v[168:171], v[220:223], a[12:15], v[168:171]\n"
"buffer_load_dwordx4 v240, s[16:19], s40 offen offset:3072 nt lds\n"
"v_mfma_f32_16x16x32_bf16 v[172:175], v[216:219], a[24:27], v[184:187]\n"
"v_exp_f32 v154, v154\n"
"v_mfma_f32_16x16x32_bf16 v[172:175], v[220:223], a[28:31], v[172:175]\n"
"v_exp_f32 v167, v167\n"
"v_mfma_f32_16x16x32_bf16 v[172:175], v[208:211], a[16:19], v[172:175]\n"
"v_exp_f32 v163, v163\n"
"v_mfma_f32_16x16x32_bf16 v[172:175], v[212:215], a[20:23], v[172:175]\n"
"s_cbranch_vccnz LPCL1_%=\nLPKL1_%=:\n"
"v_cvt_pk_bf16_f32 v115, v178, v179\n"
"v_mfma_f32_16x16x32_bf16 v[176:179], v[208:211], a[32:35], v[188:191]\n"
"v_exp_f32 v165, v165\n"
"v_mfma_f32_16x16x32_bf16 v[176:179], v[212:215], a[36:39], v[176:179]\n"
"v_exp_f32 v157, v157\n"
"v_mfma_f32_16x16x32_bf16 v[176:179], v[216:219], a[40:43], v[176:179]\n"
"v_exp_f32 v166, v166\n"
"v_mfma_f32_16x16x32_bf16 v[176:179], v[220:223], a[44:47], v[176:179]\n"
"s_waitcnt lgkmcnt(0)\n"
"v_mfma_f32_16x16x32_bf16 a[224:227], v[124:127], a[244:247], a[224:227]\n"
"v_exp_f32 v162, v162\n"
"v_mfma_f32_16x16x32_bf16 v[16:19], v[128:131], a[244:247], v[16:19]\n"
"s_waitcnt vmcnt(8)\n"
"v_exp_f32 v168, v168\n"
"v_mfma_f32_16x16x32_bf16 v[16:19], v[132:135], a[248:251], v[16:19]\n"
"s_barrier\n"
"v_mfma_f32_16x16x32_bf16 v[48:51], v[136:139], a[244:247], v[48:51]\n"
"ds_read_b128 v[208:211], v228 offset:32768\n"
"v_cvt_pk_bf16_f32 v101, v158, v159\n"
"v_mfma_f32_16x16x32_bf16 v[48:51], v[140:143], a[248:251], v[48:51]\n"
"ds_read_b128 v[216:219], v228 offset:33792\n"
"v_cvt_pk_bf16_f32 v109, v162, v163\n"
"v_mfma_f32_16x16x32_bf16 v[80:83], v[144:147], a[244:247], v[80:83]\n"
"ds_read_b128 v[220:223], v229 offset:33792\n"
"v_cvt_pk_bf16_f32 v123, v154, v155\n"
"v_mfma_f32_16x16x32_bf16 v[80:83], v[148:151], a[248:251], v[80:83]\n"
"ds_read_b128 v[212:215], v229 offset:32768\n"
"v_cvt_pk_bf16_f32 v117, v166, v167\n"
"v_mfma_f32_16x16x32_bf16 v[84:87], v[148:151], v[224:227], v[84:87]\n"
"v_exp_f32 v176, v176\n"
"v_mfma_f32_16x16x32_bf16 v[84:87], v[144:147], a[252:255], v[84:87]\n"
"v_exp_f32 v169, v169\n"
"v_mfma_f32_16x16x32_bf16 v[52:55], v[140:143], v[224:227], v[52:55]\n"
"ds_read_b64_tr_b16 a[246:247], v234 offset:20480\n"
"v_cvt_pk_bf16_f32 v116, v164, v165\n"
"v_mfma_f32_16x16x32_bf16 v[52:55], v[136:139], a[252:255], v[52:55]\n"
"v_exp_f32 v177, v177\n"
"v_mfma_f32_16x16x32_bf16 v[20:23], v[132:135], v[224:227], v[20:23]\n"
"ds_read_b64_tr_b16 a[250:251], v238 offset:20480\n"
"v_cvt_pk_bf16_f32 v118, v176, v177\n"
"v_mfma_f32_16x16x32_bf16 v[20:23], v[128:131], a[252:255], v[20:23]\n"
"v_exp_f32 v174, v174\n"
"v_mfma_f32_16x16x32_bf16 a[228:231], v[124:127], a[252:255], a[228:231]\n"
"s_waitcnt lgkmcnt(3)\n"
"ds_read_b64_tr_b16 a[254:255], v235 offset:20480\n"
"v_cvt_pk_bf16_f32 v100, v156, v157\n"
"LAY2_%=:\n"
"v_mfma_f32_16x16x32_bf16 v[152:155], v[208:211], a[0:3], v[180:183]\n"
"s_waitcnt lgkmcnt(3)\n"
"ds_read_b64_tr_b16 a[248:249], v238 offset:16384\n"
"v_cvt_pk_bf16_f32 v102, v168, v169\n"
"v_mfma_f32_16x16x32_bf16 v[152:155], v[212:215], a[4:7], v[152:155]\n"
"v_exp_f32 v172, v172\n"
"v_mfma_f32_16x16x32_bf16 v[152:155], v[216:219], a[8:11], v[152:155]\n"
"ds_read_b64_tr_b16 a[244:245], v234 offset:16384\n"
"v_cvt_pk_bf16_f32 v108, v160, v161\n"
"v_mfma_f32_16x16x32_bf16 v[152:155], v[220:223], a[12:15], v[152:155]\n"
"v_exp_f32 v175, v175\n"
"v_mfma_f32_16x16x32_bf16 v[156:159], v[216:219], a[24:27], v[184:187]\n"
"ds_read_b64_tr_b16 a[252:253], v235 offset:16384\n"
"v_cvt_pk_bf16_f32 v111, v174, v175\n"
"v_mfma_f32_16x16x32_bf16 v[156:159], v[220:223], a[28:31], v[156:159]\n"
"ds_read_b64_tr_b16 v[224:225], v239 offset:16384\n"
"v_mfma_f32_16x16x32_bf16 v[156:159], v[208:211], a[16:19], v[156:159]\n"
"s_add_u32 m0, s44, 114688\n"
"ds_read_b64_tr_b16 v[226:227], v239 offset:20480\n"
"v_mfma_f32_16x16x32_bf16 v[156:159], v[212:215], a[20:23], v[156:159]\n"
"buffer_load_dwordx4 v240, s[20:23], s56 offen nt lds\n"
"v_mfma_f32_16x16x32_bf16 v[160:163], v[208:211], a[32:35], v[188:191]\n"
"v_exp_f32 v178, v178\n"
"v_mfma_f32_16x16x32_bf16 v[160:163], v[212:215], a[36:39], v[160:163]\n"
"v_exp_f32 v171, v171\n"
"v_mfma_f32_16x16x32_bf16 v[160:163], v[216:219], a[40:43], v[160:163]\n"
"s_add_u32 s40, s56, s52\n"
"v_mfma_f32_16x16x32_bf16 v[160:163], v[220:223], a[44:47], v[160:163]\n"
"buffer_load_dwordx4 v240, s[20:23], s40 offen offset:1024 nt lds\n"
"v_mfma_f32_16x16x32_bf16 v[164:167], v[216:219], a[200:203], v[192:195]\n"
"v_exp_f32 v170, v170\n"
"v_mfma_f32_16x16x32_bf16 v[164:167], v[220:223], a[204:207], v[164:167]\n"
"v_exp_f32 v153, v153\n"
"v_mfma_f32_16x16x32_bf16 v[164:167], v[208:211], a[192:195], v[164:167]\n"
"v_exp_f32 v179, v179\n"
"v_mfma_f32_16x16x32_bf16 v[164:167], v[212:215], a[196:199], v[164:167]\n"
"s_waitcnt lgkmcnt(0)\n"
"v_exp_f32 v158, v158\n"
"v_mfma_f32_16x16x32_bf16 a[216:219], v[120:123], a[244:247], a[216:219]\n"
"ds_read_b128 v[216:219], v228 offset:37888\n"
"v_cvt_pk_bf16_f32 v103, v170, v171\n"
"v_mfma_f32_16x16x32_bf16 v[8:11], v[96:99], a[244:247], v[8:11]\n"
"v_exp_f32 v173, v173\n"
"v_mfma_f32_16x16x32_bf16 v[8:11], v[100:103], a[248:251], v[8:11]\n"
"ds_read_b128 v[208:211], v228 offset:36864\n"
"v_cvt_pk_bf16_f32 v110, v172, v173\n"
"v_mfma_f32_16x16x32_bf16 v[40:43], v[104:107], a[244:247], v[40:43]\n"
"v_exp_f32 v162, v162\n"
"v_mfma_f32_16x16x32_bf16 v[40:43], v[108:111], a[248:251], v[40:43]\n"
"ds_read_b128 v[220:223], v229 offset:37888\n"
"v_cvt_pk_bf16_f32 v119, v178, v179\n"
"v_mfma_f32_16x16x32_bf16 v[72:75], v[112:115], a[244:247], v[72:75]\n"
"s_add_u32 s40, s56, s53\n"
"ds_read_b128 v[212:215], v229 offset:36864\n"
"v_mfma_f32_16x16x32_bf16 v[72:75], v[116:119], a[248:251], v[72:75]\n"
"buffer_load_dwordx4 v240, s[20:23], s40 offen offset:2048 nt lds\n"
"v_mfma_f32_16x16x32_bf16 v[76:79], v[116:119], v[224:227], v[76:79]\n"
"v_exp_f32 v163, v163\n"
"v_mfma_f32_16x16x32_bf16 v[76:79], v[112:115], a[252:255], v[76:79]\n"
"v_exp_f32 v156, v156\n"
"v_mfma_f32_16x16x32_bf16 v[44:47], v[108:111], v[224:227], v[44:47]\n"
"v_exp_f32 v160, v160\n"
"v_mfma_f32_16x16x32_bf16 v[44:47], v[104:107], a[252:255], v[44:47]\n"
"v_exp_f32 v155, v155\n"
"v_mfma_f32_16x16x32_bf16 v[12:15], v[100:103], v[224:227], v[12:15]\n"
"ds_read_b64_tr_b16 v[226:227], v239 offset:21504\n"
"v_mfma_f32_16x16x32_bf16 v[12:15], v[96:99], a[252:255], v[12:15]\n"
"ds_read_b64_tr_b16 a[248:249], v238 offset:17408\n"
"v_mfma_f32_16x16x32_bf16 a[220:223], v[120:123], a[252:255], a[220:223]\n"
"v_exp_f32 v154, v154\n"
"v_smfmac_f32_16x16x64_bf16 v[196:199], a[240:243], v[96:103], v241\n"
"ds_read_b64_tr_b16 a[250:251], v238 offset:21504\n"
"v_mfma_f32_16x16x32_bf16 v[202:205], a[240:243], v[120:123], v[202:205]\n"
"s_waitcnt lgkmcnt(3)\n"
"v_exp_f32 v152, v152\n"
"v_mfma_f32_16x16x32_bf16 v[168:171], v[216:219], a[8:11], v[180:183]\n"
"ds_read_b64_tr_b16 a[244:245], v234 offset:17408\n"
"v_cvt_pk_bf16_f32 v128, v152, v153\n"
"v_mfma_f32_16x16x32_bf16 v[168:171], v[220:223], a[12:15], v[168:171]\n"
"v_exp_f32 v165, v165\n"
"v_mfma_f32_16x16x32_bf16 v[168:171], v[208:211], a[0:3], v[168:171]\n"
"ds_read_b64_tr_b16 a[246:247], v234 offset:21504\n"
"v_cvt_pk_bf16_f32 v145, v162, v163\n"
"v_mfma_f32_16x16x32_bf16 v[168:171], v[212:215], a[4:7], v[168:171]\n"
"ds_read_b64_tr_b16 a[254:255], v235 offset:21504\n"
"v_mfma_f32_16x16x32_bf16 v[172:175], v[208:211], a[16:19], v[184:187]\n"
"ds_read_b64_tr_b16 v[224:225], v239 offset:17408\n"
"v_cvt_pk_bf16_f32 v129, v154, v155\n"
"v_mfma_f32_16x16x32_bf16 v[172:175], v[212:215], a[20:23], v[172:175]\n"
"v_mfma_f32_16x16x32_bf16 v[172:175], v[216:219], a[24:27], v[172:175]\n"
"s_add_u32 s40, s56, s54\n"
"ds_read_b64_tr_b16 a[252:253], v235 offset:17408\n"
"v_mfma_f32_16x16x32_bf16 v[172:175], v[220:223], a[28:31], v[172:175]\n"
"buffer_load_dwordx4 v240, s[20:23], s40 offen offset:3072 nt lds\n"
"v_mfma_f32_16x16x32_bf16 v[176:179], v[216:219], a[40:43], v[188:191]\n"
"s_add_u32 s55, s55, s59\n"
"v_exp_f32 v157, v157\n"
"v_mfma_f32_16x16x32_bf16 v[176:179], v[220:223], a[44:47], v[176:179]\n"
"s_or_b32 s55, s55, s58\n"
"v_exp_f32 v171, v171\n"
"v_mfma_f32_16x16x32_bf16 v[176:179], v[208:211], a[32:35], v[176:179]\n"
"s_xor_b32 s56, s55, s57\n"
"v_exp_f32 v159, v159\n"
"v_mfma_f32_16x16x32_bf16 v[176:179], v[212:215], a[36:39], v[176:179]\n"
"s_cmp_ge_u32 s47, 15\n"
"v_exp_f32 v167, v167\n"
"v_mfma_f32_16x16x32_bf16 v[152:155], v[208:211], a[192:195], v[192:195]\n"
"s_cbranch_scc1 LSW_%=\nLSWK_%=:\n"
"v_cvt_pk_bf16_f32 v137, v158, v159\n"
"v_mfma_f32_16x16x32_bf16 v[152:155], v[212:215], a[196:199], v[152:155]\n"
"ds_read_b128 v[208:211], v230 offset:32768\n"
"v_cvt_pk_bf16_f32 v136, v156, v157\n"
"v_mfma_f32_16x16x32_bf16 v[152:155], v[216:219], a[200:203], v[152:155]\n"
"v_exp_f32 v166, v166\n"
"v_mfma_f32_16x16x32_bf16 v[152:155], v[220:223], a[204:207], v[152:155]\n"
"s_waitcnt lgkmcnt(1)\n"
"ds_read_b128 v[216:219], v230 offset:33792\n"
"v_mfma_f32_16x16x32_bf16 a[232:235], v[120:123], a[244:247], a[232:235]\n"
"v_exp_f32 v169, v169\n"
"v_mfma_f32_16x16x32_bf16 v[24:27], v[96:99], a[244:247], v[24:27]\n"
"ds_read_b128 v[220:223], v231 offset:33792\n"
"v_mfma_f32_16x16x32_bf16 v[24:27], v[100:103], a[248:251], v[24:27]\n"
"v_exp_f32 v164, v164\n"
"v_mfma_f32_16x16x32_bf16 v[56:59], v[104:107], a[244:247], v[56:59]\n"
"ds_read_b128 v[212:215], v231 offset:32768\n"
"v_mfma_f32_16x16x32_bf16 v[56:59], v[108:111], a[248:251], v[56:59]\n"
"v_exp_f32 v178, v178\n"
"v_mfma_f32_16x16x32_bf16 v[88:91], v[112:115], a[244:247], v[88:91]\n"
"s_mov_b32 m0, s44\n"
"ds_read_b64_tr_b16 a[246:247], v232 offset:20480\n"
"v_mfma_f32_16x16x32_bf16 v[88:91], v[116:119], a[248:251], v[88:91]\n"
"buffer_load_dwordx4 v240, s[16:19], s56 offen nt lds\n"
"v_mfma_f32_16x16x32_bf16 v[92:95], v[116:119], v[224:227], v[92:95]\n"
"v_exp_f32 v175, v175\n"
"v_mfma_f32_16x16x32_bf16 v[92:95], v[112:115], a[252:255], v[92:95]\n"
"v_exp_f32 v168, v168\n"
"v_mfma_f32_16x16x32_bf16 v[60:63], v[108:111], v[224:227], v[60:63]\n"
"v_exp_f32 v161, v161\n"
"v_mfma_f32_16x16x32_bf16 v[60:63], v[104:107], a[252:255], v[60:63]\n"
"v_exp_f32 v177, v177\n"
"v_mfma_f32_16x16x32_bf16 v[28:31], v[100:103], v[224:227], v[28:31]\n"
"ds_read_b64_tr_b16 v[226:227], v237 offset:20480\n"
"v_mfma_f32_16x16x32_bf16 v[28:31], v[96:99], a[252:255], v[28:31]\n"
"v_exp_f32 v172, v172\n"
"v_mfma_f32_16x16x32_bf16 a[236:239], v[120:123], a[252:255], a[236:239]\n"
"ds_read_b64_tr_b16 a[250:251], v236 offset:20480\n"
"v_cvt_pk_bf16_f32 v124, v164, v165\n"
"v_smfmac_f32_16x16x64_bf16 v[198:201], a[240:243], v[104:111], v241\n"
"s_waitcnt lgkmcnt(2)\n"
"ds_read_b64_tr_b16 a[252:253], v233 offset:16384\n"
"v_mfma_f32_16x16x32_bf16 v[156:159], v[208:211], a[0:3], v[180:183]\n"
"ds_read_b64_tr_b16 a[248:249], v236 offset:16384\n"
"v_cvt_pk_bf16_f32 v125, v166, v167\n"
"v_mfma_f32_16x16x32_bf16 v[156:159], v[212:215], a[4:7], v[156:159]\n"
"ds_read_b64_tr_b16 v[224:225], v237 offset:16384\n"
"v_cvt_pk_bf16_f32 v130, v168, v169\n"
"v_mfma_f32_16x16x32_bf16 v[156:159], v[216:219], a[8:11], v[156:159]\n"
"ds_read_b64_tr_b16 a[254:255], v233 offset:20480\n"
"v_cvt_pk_bf16_f32 v144, v160, v161\n"
"v_mfma_f32_16x16x32_bf16 v[156:159], v[220:223], a[12:15], v[156:159]\n"
"ds_read_b64_tr_b16 a[244:245], v232 offset:16384\n"
"v_mfma_f32_16x16x32_bf16 v[160:163], v[216:219], a[24:27], v[184:187]\n"
"v_exp_f32 v176, v176\n"
"v_mfma_f32_16x16x32_bf16 v[160:163], v[220:223], a[28:31], v[160:163]\n"
"v_exp_f32 v152, v152\n"
"v_mfma_f32_16x16x32_bf16 v[160:163], v[208:211], a[16:19], v[160:163]\n"
"s_add_u32 s40, s56, s52\n"
"v_cvt_pk_bf16_f32 v146, v176, v177\n"
"v_mfma_f32_16x16x32_bf16 v[160:163], v[212:215], a[20:23], v[160:163]\n"
"buffer_load_dwordx4 v240, s[16:19], s40 offen offset:1024 nt lds\n"
"v_mfma_f32_16x16x32_bf16 v[164:167], v[208:211], a[32:35], v[188:191]\n"
"v_exp_f32 v170, v170\n"
"v_mfma_f32_16x16x32_bf16 v[164:167], v[212:215], a[36:39], v[164:167]\n"
"v_exp_f32 v173, v173\n"
"v_mfma_f32_16x16x32_bf16 v[164:167], v[216:219], a[40:43], v[164:167]\n"
"v_exp_f32 v153, v153\n"
"v_mfma_f32_16x16x32_bf16 v[164:167], v[220:223], a[44:47], v[164:167]\n"
"s_waitcnt lgkmcnt(0)\n"
"v_exp_f32 v174, v174\n"
"v_mfma_f32_16x16x32_bf16 a[208:211], v[120:123], a[244:247], a[208:211]\n"
"ds_read_b128 v[208:211], v230 offset:36864\n"
"v_cvt_pk_bf16_f32 v139, v174, v175\n"
"v_mfma_f32_16x16x32_bf16 v[0:3], v[96:99], a[244:247], v[0:3]\n"
"v_exp_f32 v156, v156\n"
"v_mfma_f32_16x16x32_bf16 v[0:3], v[100:103], a[248:251], v[0:3]\n"
"ds_read_b128 v[220:223], v231 offset:37888\n"
"v_cvt_pk_bf16_f32 v138, v172, v173\n"
"v_mfma_f32_16x16x32_bf16 v[32:35], v[104:107], a[244:247], v[32:35]\n"
"v_exp_f32 v161, v161\n"
"v_mfma_f32_16x16x32_bf16 v[32:35], v[108:111], a[248:251], v[32:35]\n"
"ds_read_b128 v[216:219], v230 offset:37888\n"
"v_cvt_pk_bf16_f32 v126, v152, v153\n"
"v_mfma_f32_16x16x32_bf16 v[64:67], v[112:115], a[244:247], v[64:67]\n"
"s_add_u32 s40, s56, s53\n"
"ds_read_b128 v[212:215], v231 offset:36864\n"
"v_mfma_f32_16x16x32_bf16 v[64:67], v[116:119], a[248:251], v[64:67]\n"
"buffer_load_dwordx4 v240, s[16:19], s40 offen offset:2048 nt lds\n"
"v_mfma_f32_16x16x32_bf16 v[68:71], v[116:119], v[224:227], v[68:71]\n"
"v_exp_f32 v159, v159\n"
"v_mfma_f32_16x16x32_bf16 v[68:71], v[112:115], a[252:255], v[68:71]\n"
"v_exp_f32 v155, v155\n"
"v_mfma_f32_16x16x32_bf16 v[36:39], v[108:111], v[224:227], v[36:39]\n"
"v_exp_f32 v160, v160\n"
"v_mfma_f32_16x16x32_bf16 v[36:39], v[104:107], a[252:255], v[36:39]\n"
"v_exp_f32 v179, v179\n"
"v_mfma_f32_16x16x32_bf16 v[4:7], v[100:103], v[224:227], v[4:7]\n"
"ds_read_b64_tr_b16 a[244:245], v232 offset:17408\n"
"ds_read_b64_tr_b16 v[224:225], v237 offset:17408\n"
"v_mfma_f32_16x16x32_bf16 v[4:7], v[96:99], a[252:255], v[4:7]\n"
"v_exp_f32 v158, v158\n"
"v_mfma_f32_16x16x32_bf16 a[212:215], v[120:123], a[252:255], a[212:215]\n"
"ds_read_b64_tr_b16 a[246:247], v232 offset:21504\n"
"ds_read_b64_tr_b16 a[250:251], v236 offset:21504\n"
"v_smfmac_f32_16x16x64_bf16 v[200:203], a[240:243], v[112:119], v241\n"
"s_waitcnt lgkmcnt(4)\n"
"ds_read_b64_tr_b16 v[226:227], v237 offset:21504\n"
"v_cvt_pk_bf16_f32 v131, v170, v171\n"
"v_mfma_f32_16x16x32_bf16 v[168:171], v[208:211], a[0:3], v[180:183]\n"
"v_exp_f32 v164, v164\n"
"v_mfma_f32_16x16x32_bf16 v[168:171], v[212:215], a[4:7], v[168:171]\n"
"ds_read_b64_tr_b16 a[248:249], v236 offset:17408\n"
"ds_read_b64_tr_b16 a[254:255], v233 offset:21504\n"
"v_mfma_f32_16x16x32_bf16 v[168:171], v[216:219], a[8:11], v[168:171]\n"
"s_add_u32 s40, s56, s54\n"
"ds_read_b64_tr_b16 a[252:253], v233 offset:17408\n"
"v_mfma_f32_16x16x32_bf16 v[168:171], v[220:223], a[12:15], v[168:171]\n"
"buffer_load_dwordx4 v240, s[16:19], s40 offen offset:3072 nt lds\n"
"v_mfma_f32_16x16x32_bf16 v[172:175], v[216:219], a[24:27], v[184:187]\n"
"v_exp_f32 v154, v154\n"
"v_mfma_f32_16x16x32_bf16 v[172:175], v[220:223], a[28:31], v[172:175]\n"
"v_exp_f32 v167, v167\n"
"v_mfma_f32_16x16x32_bf16 v[172:175], v[208:211], a[16:19], v[172:175]\n"
"v_exp_f32 v163, v163\n"
"v_mfma_f32_16x16x32_bf16 v[172:175], v[212:215], a[20:23], v[172:175]\n"
"s_cbranch_vccnz LPCL2_%=\nLPKL2_%=:\n"
"v_cvt_pk_bf16_f32 v147, v178, v179\n"
"v_mfma_f32_16x16x32_bf16 v[176:179], v[208:211], a[32:35], v[188:191]\n"
"v_exp_f32 v165, v165\n"
"v_mfma_f32_16x16x32_bf16 v[176:179], v[212:215], a[36:39], v[176:179]\n"
"v_exp_f32 v157, v157\n"
"v_mfma_f32_16x16x32_bf16 v[176:179], v[216:219], a[40:43], v[176:179]\n"
"v_exp_f32 v166, v166\n"
"v_mfma_f32_16x16x32_bf16 v[176:179], v[220:223], a[44:47], v[176:179]\n"
"s_waitcnt lgkmcnt(0)\n"
"v_mfma_f32_16x16x32_bf16 a[224:227], v[120:123], a[244:247], a[224:227]\n"
"v_exp_f32 v162, v162\n"
"v_mfma_f32_16x16x32_bf16 v[16:19], v[96:99], a[244:247], v[16:19]\n"
"s_waitcnt vmcnt(8)\n"
"v_exp_f32 v168, v168\n"
"v_mfma_f32_16x16x32_bf16 v[16:19], v[100:103], a[248:251], v[16:19]\n"
"s_barrier\n"
"v_mfma_f32_16x16x32_bf16 v[48:51], v[104:107], a[244:247], v[48:51]\n"
"ds_read_b128 v[208:211], v228 offset:49152\n"
"v_cvt_pk_bf16_f32 v133, v158, v159\n"
"v_mfma_f32_16x16x32_bf16 v[48:51], v[108:111], a[248:251], v[48:51]\n"
"ds_read_b128 v[216:219], v228 offset:50176\n"
"v_cvt_pk_bf16_f32 v141, v162, v163\n"
"v_mfma_f32_16x16x32_bf16 v[80:83], v[112:115], a[244:247], v[80:83]\n"
"ds_read_b128 v[220:223], v229 offset:50176\n"
"v_cvt_pk_bf16_f32 v127, v154, v155\n"
"v_mfma_f32_16x16x32_bf16 v[80:83], v[116:119], a[248:251], v[80:83]\n"
"ds_read_b128 v[212:215], v229 offset:49152\n"
"v_cvt_pk_bf16_f32 v149, v166, v167\n"
"v_mfma_f32_16x16x32_bf16 v[84:87], v[116:119], v[224:227], v[84:87]\n"
"v_exp_f32 v176, v176\n"
"v_mfma_f32_16x16x32_bf16 v[84:87], v[112:115], a[252:255], v[84:87]\n"
"v_exp_f32 v169, v169\n"
"v_mfma_f32_16x16x32_bf16 v[52:55], v[108:111], v[224:227], v[52:55]\n"
"ds_read_b64_tr_b16 a[246:247], v234 offset:36864\n"
"v_cvt_pk_bf16_f32 v148, v164, v165\n"
"v_mfma_f32_16x16x32_bf16 v[52:55], v[104:107], a[252:255], v[52:55]\n"
"v_exp_f32 v177, v177\n"
"v_mfma_f32_16x16x32_bf16 v[20:23], v[100:103], v[224:227], v[20:23]\n"
"ds_read_b64_tr_b16 a[250:251], v238 offset:36864\n"
"v_cvt_pk_bf16_f32 v150, v176, v177\n"
"v_mfma_f32_16x16x32_bf16 v[20:23], v[96:99], a[252:255], v[20:23]\n"
"v_exp_f32 v174, v174\n"
"v_mfma_f32_16x16x32_bf16 a[228:231], v[120:123], a[252:255], a[228:231]\n"
"s_waitcnt lgkmcnt(3)\n"
"ds_read_b64_tr_b16 a[254:255], v235 offset:36864\n"
"v_cvt_pk_bf16_f32 v132, v156, v157\n"
"v_mfma_f32_16x16x32_bf16 v[152:155], v[208:211], a[0:3], v[180:183]\n"
"s_waitcnt lgkmcnt(3)\n"
"ds_read_b64_tr_b16 a[248:249], v238 offset:32768\n"
"v_cvt_pk_bf16_f32 v134, v168, v169\n"
"v_mfma_f32_16x16x32_bf16 v[152:155], v[212:215], a[4:7], v[152:155]\n"
"v_exp_f32 v172, v172\n"
"v_mfma_f32_16x16x32_bf16 v[152:155], v[216:219], a[8:11], v[152:155]\n"
"ds_read_b64_tr_b16 a[244:245], v234 offset:32768\n"
"v_cvt_pk_bf16_f32 v140, v160, v161\n"
"v_mfma_f32_16x16x32_bf16 v[152:155], v[220:223], a[12:15], v[152:155]\n"
"v_exp_f32 v175, v175\n"
"v_mfma_f32_16x16x32_bf16 v[156:159], v[216:219], a[24:27], v[184:187]\n"
"ds_read_b64_tr_b16 a[252:253], v235 offset:32768\n"
"v_cvt_pk_bf16_f32 v143, v174, v175\n"
"v_mfma_f32_16x16x32_bf16 v[156:159], v[220:223], a[28:31], v[156:159]\n"
"ds_read_b64_tr_b16 v[224:225], v239 offset:32768\n"
"v_mfma_f32_16x16x32_bf16 v[156:159], v[208:211], a[16:19], v[156:159]\n"
"s_add_u32 m0, s44, 65536\n"
"ds_read_b64_tr_b16 v[226:227], v239 offset:36864\n"
"v_mfma_f32_16x16x32_bf16 v[156:159], v[212:215], a[20:23], v[156:159]\n"
"buffer_load_dwordx4 v240, s[20:23], s56 offen nt lds\n"
"v_mfma_f32_16x16x32_bf16 v[160:163], v[208:211], a[32:35], v[188:191]\n"
"v_exp_f32 v178, v178\n"
"v_mfma_f32_16x16x32_bf16 v[160:163], v[212:215], a[36:39], v[160:163]\n"
"v_exp_f32 v171, v171\n"
"v_mfma_f32_16x16x32_bf16 v[160:163], v[216:219], a[40:43], v[160:163]\n"
"s_add_u32 s40, s56, s52\n"
"v_mfma_f32_16x16x32_bf16 v[160:163], v[220:223], a[44:47], v[160:163]\n"
"buffer_load_dwordx4 v240, s[20:23], s40 offen offset:1024 nt lds\n"
"v_mfma_f32_16x16x32_bf16 v[164:167], v[216:219], a[200:203], v[192:195]\n"
"v_exp_f32 v170, v170\n"
"v_mfma_f32_16x16x32_bf16 v[164:167], v[220:223], a[204:207], v[164:167]\n"
"v_exp_f32 v153, v153\n"
"v_mfma_f32_16x16x32_bf16 v[164:167], v[208:211], a[192:195], v[164:167]\n"
"v_exp_f32 v179, v179\n"
"v_mfma_f32_16x16x32_bf16 v[164:167], v[212:215], a[196:199], v[164:167]\n"
"s_waitcnt lgkmcnt(0)\n"
"v_exp_f32 v158, v158\n"
"v_mfma_f32_16x16x32_bf16 a[216:219], v[124:127], a[244:247], a[216:219]\n"
"ds_read_b128 v[216:219], v228 offset:54272\n"
"v_cvt_pk_bf16_f32 v135, v170, v171\n"
"v_mfma_f32_16x16x32_bf16 v[8:11], v[128:131], a[244:247], v[8:11]\n"
"v_exp_f32 v173, v173\n"
"v_mfma_f32_16x16x32_bf16 v[8:11], v[132:135], a[248:251], v[8:11]\n"
"ds_read_b128 v[208:211], v228 offset:53248\n"
"v_cvt_pk_bf16_f32 v142, v172, v173\n"
"v_mfma_f32_16x16x32_bf16 v[40:43], v[136:139], a[244:247], v[40:43]\n"
"v_exp_f32 v162, v162\n"
"v_mfma_f32_16x16x32_bf16 v[40:43], v[140:143], a[248:251], v[40:43]\n"
"ds_read_b128 v[220:223], v229 offset:54272\n"
"v_cvt_pk_bf16_f32 v151, v178, v179\n"
"v_mfma_f32_16x16x32_bf16 v[72:75], v[144:147], a[244:247], v[72:75]\n"
"s_add_u32 s40, s56, s53\n"
"ds_read_b128 v[212:215], v229 offset:53248\n"
"v_mfma_f32_16x16x32_bf16 v[72:75], v[148:151], a[248:251], v[72:75]\n"
"buffer_load_dwordx4 v240, s[20:23], s40 offen offset:2048 nt lds\n"
"v_mfma_f32_16x16x32_bf16 v[76:79], v[148:151], v[224:227], v[76:79]\n"
"v_exp_f32 v163, v163\n"
"v_mfma_f32_16x16x32_bf16 v[76:79], v[144:147], a[252:255], v[76:79]\n"
"v_exp_f32 v156, v156\n"
"v_mfma_f32_16x16x32_bf16 v[44:47], v[140:143], v[224:227], v[44:47]\n"
"v_exp_f32 v160, v160\n"
"v_mfma_f32_16x16x32_bf16 v[44:47], v[136:139], a[252:255], v[44:47]\n"
"v_exp_f32 v155, v155\n"
"v_mfma_f32_16x16x32_bf16 v[12:15], v[132:135], v[224:227], v[12:15]\n"
"ds_read_b64_tr_b16 v[226:227], v239 offset:37888\n"
"v_mfma_f32_16x16x32_bf16 v[12:15], v[128:131], a[252:255], v[12:15]\n"
"ds_read_b64_tr_b16 a[248:249], v238 offset:33792\n"
"v_mfma_f32_16x16x32_bf16 a[220:223], v[124:127], a[252:255], a[220:223]\n"
"v_exp_f32 v154, v154\n"
"v_smfmac_f32_16x16x64_bf16 v[196:199], a[240:243], v[128:135], v241\n"
"ds_read_b64_tr_b16 a[250:251], v238 offset:37888\n"
"v_mfma_f32_16x16x32_bf16 v[202:205], a[240:243], v[124:127], v[202:205]\n"
"s_waitcnt lgkmcnt(3)\n"
"v_exp_f32 v152, v152\n"
"v_mfma_f32_16x16x32_bf16 v[168:171], v[216:219], a[8:11], v[180:183]\n"
"ds_read_b64_tr_b16 a[244:245], v234 offset:33792\n"
"v_cvt_pk_bf16_f32 v96, v152, v153\n"
"v_mfma_f32_16x16x32_bf16 v[168:171], v[220:223], a[12:15], v[168:171]\n"
"v_exp_f32 v165, v165\n"
"v_mfma_f32_16x16x32_bf16 v[168:171], v[208:211], a[0:3], v[168:171]\n"
"ds_read_b64_tr_b16 a[246:247], v234 offset:37888\n"
"v_cvt_pk_bf16_f32 v113, v162, v163\n"
"v_mfma_f32_16x16x32_bf16 v[168:171], v[212:215], a[4:7], v[168:171]\n"
"ds_read_b64_tr_b16 a[254:255], v235 offset:37888\n"
"v_mfma_f32_16x16x32_bf16 v[172:175], v[208:211], a[16:19], v[184:187]\n"
"ds_read_b64_tr_b16 v[224:225], v239 offset:33792\n"
"v_cvt_pk_bf16_f32 v97, v154, v155\n"
"v_mfma_f32_16x16x32_bf16 v[172:175], v[212:215], a[20:23], v[172:175]\n"
"v_mfma_f32_16x16x32_bf16 v[172:175], v[216:219], a[24:27], v[172:175]\n"
"s_add_u32 s40, s56, s54\n"
"ds_read_b64_tr_b16 a[252:253], v235 offset:33792\n"
"v_mfma_f32_16x16x32_bf16 v[172:175], v[220:223], a[28:31], v[172:175]\n"
"buffer_load_dwordx4 v240, s[20:23], s40 offen offset:3072 nt lds\n"
"v_mfma_f32_16x16x32_bf16 v[176:179], v[216:219], a[40:43], v[188:191]\n"
"s_add_u32 s55, s55, s59\n"
"v_exp_f32 v157, v157\n"
"v_mfma_f32_16x16x32_bf16 v[176:179], v[220:223], a[44:47], v[176:179]\n"
"s_or_b32 s55, s55, s58\n"
"v_exp_f32 v171, v171\n"
"v_mfma_f32_16x16x32_bf16 v[176:179], v[208:211], a[32:35], v[176:179]\n"
"s_xor_b32 s56, s55, s57\n"
"v_exp_f32 v159, v159\n"
"v_mfma_f32_16x16x32_bf16 v[176:179], v[212:215], a[36:39], v[176:179]\n"
"v_exp_f32 v167, v167\n"
"v_mfma_f32_16x16x32_bf16 v[152:155], v[208:211], a[192:195], v[192:195]\n"
"v_cvt_pk_bf16_f32 v105, v158, v159\n"
"v_mfma_f32_16x16x32_bf16 v[152:155], v[212:215], a[196:199], v[152:155]\n"
"ds_read_b128 v[208:211], v230 offset:49152\n"
"v_cvt_pk_bf16_f32 v104, v156, v157\n"
"v_mfma_f32_16x16x32_bf16 v[152:155], v[216:219], a[200:203], v[152:155]\n"
"v_exp_f32 v166, v166\n"
"v_mfma_f32_16x16x32_bf16 v[152:155], v[220:223], a[204:207], v[152:155]\n"
"s_waitcnt lgkmcnt(1)\n"
"ds_read_b128 v[216:219], v230 offset:50176\n"
"v_mfma_f32_16x16x32_bf16 a[232:235], v[124:127], a[244:247], a[232:235]\n"
"v_exp_f32 v169, v169\n"
"v_mfma_f32_16x16x32_bf16 v[24:27], v[128:131], a[244:247], v[24:27]\n"
"ds_read_b128 v[220:223], v231 offset:50176\n"
"v_mfma_f32_16x16x32_bf16 v[24:27], v[132:135], a[248:251], v[24:27]\n"
"v_exp_f32 v164, v164\n"
"v_mfma_f32_16x16x32_bf16 v[56:59], v[136:139], a[244:247], v[56:59]\n"
"ds_read_b128 v[212:215], v231 offset:49152\n"
"v_mfma_f32_16x16x32_bf16 v[56:59], v[140:143], a[248:251], v[56:59]\n"
"v_exp_f32 v178, v178\n"
"v_mfma_f32_16x16x32_bf16 v[88:91], v[144:147], a[244:247], v[88:91]\n"
"s_add_u32 m0, s44, 16384\n"
"ds_read_b64_tr_b16 a[246:247], v232 offset:36864\n"
"v_mfma_f32_16x16x32_bf16 v[88:91], v[148:151], a[248:251], v[88:91]\n"
"buffer_load_dwordx4 v240, s[16:19], s56 offen nt lds\n"
"v_mfma_f32_16x16x32_bf16 v[92:95], v[148:151], v[224:227], v[92:95]\n"
"v_exp_f32 v175, v175\n"
"v_mfma_f32_16x16x32_bf16 v[92:95], v[144:147], a[252:255], v[92:95]\n"
"v_exp_f32 v168, v168\n"
"v_mfma_f32_16x16x32_bf16 v[60:63], v[140:143], v[224:227], v[60:63]\n"
"v_exp_f32 v161, v161\n"
"v_mfma_f32_16x16x32_bf16 v[60:63], v[136:139], a[252:255], v[60:63]\n"
"v_exp_f32 v177, v177\n"
"v_mfma_f32_16x16x32_bf16 v[28:31], v[132:135], v[224:227], v[28:31]\n"
"ds_read_b64_tr_b16 v[226:227], v237 offset:36864\n"
"v_mfma_f32_16x16x32_bf16 v[28:31], v[128:131], a[252:255], v[28:31]\n"
"v_exp_f32 v172, v172\n"
"v_mfma_f32_16x16x32_bf16 a[236:239], v[124:127], a[252:255], a[236:239]\n"
"ds_read_b64_tr_b16 a[250:251], v236 offset:36864\n"
"v_cvt_pk_bf16_f32 v120, v164, v165\n"
"v_smfmac_f32_16x16x64_bf16 v[198:201], a[240:243], v[136:143], v241\n"
"s_waitcnt lgkmcnt(2)\n"
"ds_read_b64_tr_b16 a[252:253], v233 offset:32768\n"
"v_mfma_f32_16x16x32_bf16 v[156:159], v[208:211], a[0:3], v[180:183]\n"
"ds_read_b64_tr_b16 a[248:249], v236 offset:32768\n"
"v_cvt_pk_bf16_f32 v121, v166, v167\n"
"v_mfma_f32_16x16x32_bf16 v[156:159], v[212:215], a[4:7], v[156:159]\n"
"ds_read_b64_tr_b16 v[224:225], v237 offset:32768\n"
"v_cvt_pk_bf16_f32 v98, v168, v169\n"
"v_mfma_f32_16x16x32_bf16 v[156:159], v[216:219], a[8:11], v[156:159]\n"
"ds_read_b64_tr_b16 a[254:255], v233 offset:36864\n"
"v_cvt_pk_bf16_f32 v112, v160, v161\n"
"v_mfma_f32_16x16x32_bf16 v[156:159], v[220:223], a[12:15], v[156:159]\n"
"ds_read_b64_tr_b16 a[244:245], v232 offset:32768\n"
"v_mfma_f32_16x16x32_bf16 v[160:163], v[216:219], a[24:27], v[184:187]\n"
"v_exp_f32 v176, v176\n"
"v_mfma_f32_16x16x32_bf16 v[160:163], v[220:223], a[28:31], v[160:163]\n"
"v_exp_f32 v152, v152\n"
"v_mfma_f32_16x16x32_bf16 v[160:163], v[208:211], a[16:19], v[160:163]\n"
"s_add_u32 s40, s56, s52\n"
"v_cvt_pk_bf16_f32 v114, v176, v177\n"
"v_mfma_f32_16x16x32_bf16 v[160:163], v[212:215], a[20:23], v[160:163]\n"
"buffer_load_dwordx4 v240, s[16:19], s40 offen offset:1024 nt lds\n"
"v_mfma_f32_16x16x32_bf16 v[164:167], v[208:211], a[32:35], v[188:191]\n"
"v_exp_f32 v170, v170\n"
"v_mfma_f32_16x16x32_bf16 v[164:167], v[212:215], a[36:39], v[164:167]\n"
"v_exp_f32 v173, v173\n"
"v_mfma_f32_16x16x32_bf16 v[164:167], v[216:219], a[40:43], v[164:167]\n"
"v_exp_f32 v153, v153\n"
"v_mfma_f32_16x16x32_bf16 v[164:167], v[220:223], a[44:47], v[164:167]\n"
"s_waitcnt lgkmcnt(0)\n"
"v_exp_f32 v174, v174\n"
"v_mfma_f32_16x16x32_bf16 a[208:211], v[124:127], a[244:247], a[208:211]\n"
"ds_read_b128 v[208:211], v230 offset:53248\n"
"v_cvt_pk_bf16_f32 v107, v174, v175\n"
"v_mfma_f32_16x16x32_bf16 v[0:3], v[128:131], a[244:247], v[0:3]\n"
"v_exp_f32 v156, v156\n"
"v_mfma_f32_16x16x32_bf16 v[0:3], v[132:135], a[248:251], v[0:3]\n"
"ds_read_b128 v[220:223], v231 offset:54272\n"
"v_cvt_pk_bf16_f32 v106, v172, v173\n"
"v_mfma_f32_16x16x32_bf16 v[32:35], v[136:139], a[244:247], v[32:35]\n"
"v_exp_f32 v161, v161\n"
"v_mfma_f32_16x16x32_bf16 v[32:35], v[140:143], a[248:251], v[32:35]\n"
"ds_read_b128 v[216:219], v230 offset:54272\n"
"v_cvt_pk_bf16_f32 v122, v152, v153\n"
"v_mfma_f32_16x16x32_bf16 v[64:67], v[144:147], a[244:247], v[64:67]\n"
"s_add_u32 s40, s56, s53\n"
"ds_read_b128 v[212:215], v231 offset:53248\n"
"v_mfma_f32_16x16x32_bf16 v[64:67], v[148:151], a[248:251], v[64:67]\n"
"buffer_load_dwordx4 v240, s[16:19], s40 offen offset:2048 nt lds\n"
"v_mfma_f32_16x16x32_bf16 v[68:71], v[148:151], v[224:227], v[68:71]\n"
"v_exp_f32 v159, v159\n"
"v_mfma_f32_16x16x32_bf16 v[68:71], v[144:147], a[252:255], v[68:71]\n"
"v_exp_f32 v155, v155\n"
"v_mfma_f32_16x16x32_bf16 v[36:39], v[140:143], v[224:227], v[36:39]\n"
"v_exp_f32 v160, v160\n"
"v_mfma_f32_16x16x32_bf16 v[36:39], v[136:139], a[252:255], v[36:39]\n"
"v_exp_f32 v179, v179\n"
"v_mfma_f32_16x16x32_bf16 v[4:7], v[132:135], v[224:227], v[4:7]\n"
"ds_read_b64_tr_b16 a[244:245], v232 offset:33792\n"
"ds_read_b64_tr_b16 v[224:225], v237 offset:33792\n"
"v_mfma_f32_16x16x32_bf16 v[4:7], v[128:131], a[252:255], v[4:7]\n"
"v_exp_f32 v158, v158\n"
"v_mfma_f32_16x16x32_bf16 a[212:215], v[124:127], a[252:255], a[212:215]\n"
"ds_read_b64_tr_b16 a[246:247], v232 offset:37888\n"
"ds_read_b64_tr_b16 a[250:251], v236 offset:37888\n"
"v_smfmac_f32_16x16x64_bf16 v[200:203], a[240:243], v[144:151], v241\n"
"s_waitcnt lgkmcnt(4)\n"
"ds_read_b64_tr_b16 v[226:227], v237 offset:37888\n"
"v_cvt_pk_bf16_f32 v99, v170, v171\n"
"v_mfma_f32_16x16x32_bf16 v[168:171], v[208:211], a[0:3], v[180:183]\n"
"v_exp_f32 v164, v164\n"
"v_mfma_f32_16x16x32_bf16 v[168:171], v[212:215], a[4:7], v[168:171]\n"
"ds_read_b64_tr_b16 a[248:249], v236 offset:33792\n"
"ds_read_b64_tr_b16 a[254:255], v233 offset:37888\n"
"v_mfma_f32_16x16x32_bf16 v[168:171], v[216:219], a[8:11], v[168:171]\n"
"s_add_u32 s40, s56, s54\n"
"ds_read_b64_tr_b16 a[252:253], v233 offset:33792\n"
"v_mfma_f32_16x16x32_bf16 v[168:171], v[220:223], a[12:15], v[168:171]\n"
"buffer_load_dwordx4 v240, s[16:19], s40 offen offset:3072 nt lds\n"
"v_mfma_f32_16x16x32_bf16 v[172:175], v[216:219], a[24:27], v[184:187]\n"
"v_exp_f32 v154, v154\n"
"v_mfma_f32_16x16x32_bf16 v[172:175], v[220:223], a[28:31], v[172:175]\n"
"v_exp_f32 v167, v167\n"
"v_mfma_f32_16x16x32_bf16 v[172:175], v[208:211], a[16:19], v[172:175]\n"
"v_exp_f32 v163, v163\n"
"v_mfma_f32_16x16x32_bf16 v[172:175], v[212:215], a[20:23], v[172:175]\n"
"s_cbranch_vccnz LPCL3_%=\nLPKL3_%=:\n"
"v_cvt_pk_bf16_f32 v115, v178, v179\n"
"v_mfma_f32_16x16x32_bf16 v[176:179], v[208:211], a[32:35], v[188:191]\n"
"s_add_u32 s47, s47, 1\n"
"v_exp_f32 v165, v165\n"
"v_mfma_f32_16x16x32_bf16 v[176:179], v[212:215], a[36:39], v[176:179]\n"
"v_exp_f32 v157, v157\n"
"v_mfma_f32_16x16x32_bf16 v[176:179], v[216:219], a[40:43], v[176:179]\n"
"s_and_b32 s40, s47, 3\n"
"v_exp_f32 v166, v166\n"
"v_mfma_f32_16x16x32_bf16 v[176:179], v[220:223], a[44:47], v[176:179]\n"
"s_waitcnt lgkmcnt(0)\n"
"s_cbranch_scc1 LAL_%=\n"
"s_cmp_eq_u32 s47, 8\n"
"s_cbranch_scc1 LTH1_%=\n"
"s_cmp_eq_u32 s47, 12\n"
"s_cbranch_scc1 LTH2_%=\n"
"s_cmp_eq_u32 s47, 16\n"
"s_cbranch_scc1 LAX_%=\n"
"LTH0_%=:\n"
"v_mfma_f32_16x16x32_bf16 a[224:227], v[124:127], a[244:247], a[224:227]\n"
"v_exp_f32 v171, v171\n"
"v_mfma_f32_16x16x32_bf16 v[16:19], v[128:131], a[244:247], v[16:19]\n"
"s_waitcnt vmcnt(8)\n"
"v_exp_f32 v172, v172\n"
"v_mfma_f32_16x16x32_bf16 v[16:19], v[132:135], a[248:251], v[16:19]\n"
"s_barrier\n"
"v_mfma_f32_16x16x32_bf16 v[48:51], v[136:139], a[244:247], v[48:51]\n"
"v_exp_f32 v169, v169\n"
"v_mfma_f32_16x16x32_bf16 v[48:51], v[140:143], a[248:251], v[48:51]\n"
"ds_read_b128 v[220:223], v229 offset:1024\n"
"v_cvt_pk_bf16_f32 v117, v166, v167\n"
"v_exp_f32 v176, v176\n"
"v_exp_f32 v177, v177\n"
"v_exp_f32 v178, v178\n"
"v_mfma_f32_16x16x32_bf16 v[80:83], v[144:147], a[244:247], v[80:83]\n"
"ds_read_b64_tr_b16 a[244:245], v234 offset:49152\n"
"v_cvt_pk_bf16_f32 v108, v160, v161\n"
"v_mfma_f32_16x16x32_bf16 v[80:83], v[148:151], a[248:251], v[80:83]\n"
"ds_read_b64_tr_b16 a[246:247], v234 offset:53248\n"
"ds_read_b64_tr_b16 a[248:249], v238 offset:49152\n"
"v_exp_f32 v162, v162\n"
"v_exp_f32 v168, v168\n"
"v_exp_f32 v173, v173\n"
"v_exp_f32 v175, v175\n"
"v_mfma_f32_16x16x32_bf16 v[84:87], v[148:151], v[224:227], v[84:87]\n"
"ds_read_b64_tr_b16 a[250:251], v238 offset:53248\n"
"v_cvt_pk_bf16_f32 v116, v164, v165\n"
"v_mfma_f32_16x16x32_bf16 v[84:87], v[144:147], a[252:255], v[84:87]\n"
"v_exp_f32 v170, v170\n"
"v_mfma_f32_16x16x32_bf16 v[52:55], v[140:143], v[224:227], v[52:55]\n"
"v_cvt_pk_bf16_f32 v103, v170, v171\n"
"v_cvt_pk_bf16_f32 v110, v172, v173\n"
"v_mfma_f32_16x16x32_bf16 v[52:55], v[136:139], a[252:255], v[52:55]\n"
"v_cvt_pk_bf16_f32 v123, v154, v155\n"
"v_cvt_pk_bf16_f32 v101, v158, v159\n"
"v_mfma_f32_16x16x32_bf16 v[20:23], v[132:135], v[224:227], v[20:23]\n"
"v_cvt_pk_bf16_f32 v100, v156, v157\n"
"v_cvt_pk_bf16_f32 v102, v168, v169\n"
"v_mfma_f32_16x16x32_bf16 v[20:23], v[128:131], a[252:255], v[20:23]\n"
"v_exp_f32 v179, v179\n"
"v_mfma_f32_16x16x32_bf16 a[228:231], v[124:127], a[252:255], a[228:231]\n"
"s_waitcnt lgkmcnt(2)\n"
"ds_read_b64_tr_b16 a[252:253], v235 offset:49152\n"
"ds_read_b64_tr_b16 v[226:227], v239 offset:53248\n"
"v_mfma_f32_16x16x32_bf16 a[216:219], v[120:123], a[244:247], a[216:219]\n"
"s_add_u32 m0, s44, 81920\n"
"ds_read_b64_tr_b16 v[224:225], v239 offset:49152\n"
"v_cvt_pk_bf16_f32 v119, v178, v179\n"
"v_smfmac_f32_16x16x64_bf16 v[196:199], a[240:243], v[96:103], v241\n"
"buffer_load_dwordx4 v240, s[20:23], s56 offen nt lds\n"
"v_mfma_f32_16x16x32_bf16 v[8:11], v[96:99], a[244:247], v[8:11]\n"
"s_waitcnt lgkmcnt(3)\n"
"ds_read_b64_tr_b16 a[254:255], v235 offset:53248\n"
"v_cvt_pk_bf16_f32 v118, v176, v177\n"
"v_mfma_f32_16x16x32_bf16 v[8:11], v[100:103], a[248:251], v[8:11]\n"
"v_exp_f32 v174, v174\n"
"v_smfmac_f32_16x16x64_bf16 v[200:203], a[240:243], v[112:119], v241\n"
"v_cvt_pk_bf16_f32 v109, v162, v163\n"
"v_cvt_pk_bf16_f32 v111, v174, v175\n"
"v_mfma_f32_16x16x32_bf16 v[40:43], v[104:107], a[244:247], v[40:43]\n"
"ds_read_b128 v[212:215], v229 offset:0\n"
"v_and_b32 v245, 0x3f0, v242\n"
"v_mfma_f32_16x16x32_bf16 v[40:43], v[108:111], a[248:251], v[40:43]\n"
"v_lshlrev_b32_e64 v244, 24, s43\n"
"v_mul_f32 v244, 0x45800000, v244\n"
"v_smfmac_f32_16x16x64_bf16 v[198:201], a[240:243], v[104:111], v241\n"
"ds_read_b128 v[208:211], v228 offset:0\n"
"v_add_f32 v196, v196, v197\n"
"v_mfma_f32_16x16x32_bf16 v[72:75], v[112:115], a[244:247], v[72:75]\n"
"ds_read_b64_tr_b16 a[244:245], v234 offset:50176\n"
"v_lshrrev_b32 v245, 1, v245\n"
"v_mfma_f32_16x16x32_bf16 v[72:75], v[116:119], a[248:251], v[72:75]\n"
"ds_read_b64_tr_b16 a[246:247], v234 offset:54272\n"
"v_sub_u32 v245, v242, v245\n"
"v_mfma_f32_16x16x32_bf16 v[202:205], a[240:243], v[120:123], v[202:205]\n"
"s_waitcnt lgkmcnt(4)\n"
"ds_read_b64_tr_b16 a[248:249], v238 offset:50176\n"
"v_add_f32 v200, v200, v201\n"
"v_mfma_f32_16x16x32_bf16 v[76:79], v[116:119], v[224:227], v[76:79]\n"
"v_add_f32 v198, v198, v199\n"
"v_add_f32 v254, v196, v198\n"
"v_mfma_f32_16x16x32_bf16 v[76:79], v[112:115], a[252:255], v[76:79]\n"
"ds_read_b64_tr_b16 a[250:251], v238 offset:54272\n"
"v_xor_b32 v250, 0x1000, v245\n"
"v_mfma_f32_16x16x32_bf16 v[44:47], v[108:111], v[224:227], v[44:47]\n"
"ds_read_b128 v[216:219], v228 offset:1024\n"
"v_add_f32 v254, v254, v200\n"
"v_mfma_f32_16x16x32_bf16 v[44:47], v[104:107], a[252:255], v[44:47]\n"
"v_add_f32 v244, v244, v202\n"
"v_add_f32 v254, v254, v202\n"
"v_mfma_f32_16x16x32_bf16 v[12:15], v[100:103], v[224:227], v[12:15]\n"
"s_add_u32 s40, s56, s52\n"
"ds_read_b64_tr_b16 v[226:227], v239 offset:54272\n"
"v_min3_f32 v244, v244, v196, v198\n"
"v_mfma_f32_16x16x32_bf16 v[12:15], v[96:99], a[252:255], v[12:15]\n"
"buffer_load_dwordx4 v240, s[20:23], s40 offen offset:1024 nt lds\n"
"v_min_f32 v244, v244, v200\n"
"v_rcp_f32 v244, v244\n"
"v_mfma_f32_16x16x32_bf16 a[220:223], v[120:123], a[252:255], a[220:223]\n"
"s_waitcnt lgkmcnt(4)\n"
"v_fmac_f32 v254, 0x68800000, v244\n"
"v_cmp_nge_f32 vcc, s45, v254\n"
"s_cbranch_vccnz LSLOW_%=\nLBACK0_%=:\ns_mov_b64 vcc, s[64:65]\n"
"v_mfma_f32_16x16x32_bf16 a[232:235], v[120:123], a[244:247], a[232:235]\n"
"v_rcp_f32 v246, v196\n"
"v_accvgpr_mov_b32 a2, a50\n"
"v_accvgpr_mov_b32 a7, a55\n"
"v_accvgpr_mov_b32 a10, a58\n"
"v_accvgpr_mov_b32 a15, a63\n"
"v_accvgpr_mov_b32 a22, a70\n"
"v_accvgpr_mov_b32 a30, a78\n"
"v_bfe_u32 v149, v242, 8, 2\n"
"v_lshl_add_u32 v149, v149, 4, s43\n"
"v_mfma_f32_16x16x32_bf16 v[24:27], v[96:99], a[244:247], v[24:27]\n"
"s_waitcnt lgkmcnt(2)\n"
"ds_read_b128 v[124:127], v149 offset:0\n"
"v_accvgpr_mov_b32 a28, a76\n"
"v_mfma_f32_16x16x32_bf16 v[24:27], v[100:103], a[248:251], v[24:27]\n"
"ds_read_b128 v[132:135], v149 offset:1024\n"
"v_mov_b32_dpp v246, v246 row_ror:12 row_mask:0x2 bank_mask:0xf\n"
"v_accvgpr_mov_b32 a3, a51\n"
"v_accvgpr_mov_b32 a12, a60\n"
"v_accvgpr_mov_b32 a18, a66\n"
"v_accvgpr_mov_b32 a19, a67\n"
"v_accvgpr_mov_b32 a26, a74\n"
"v_accvgpr_mov_b32 a36, a84\n"
"v_xor_b32 v148, 0x400, v243\n"
"v_xor_b32 v150, 64, v149\n"
"v_mfma_f32_16x16x32_bf16 v[56:59], v[104:107], a[244:247], v[56:59]\n"
"ds_read_b128 v[128:131], v150 offset:0\n"
"v_accvgpr_mov_b32 a39, a87\n"
"v_mfma_f32_16x16x32_bf16 v[56:59], v[108:111], a[248:251], v[56:59]\n"
"ds_read_b128 v[136:139], v150 offset:1024\n"
"v_rcp_f32 v247, v198\n"
"v_mov_b32_dpp v246, v246 row_ror:8 row_mask:0x4 bank_mask:0xf\n"
"v_accvgpr_mov_b32 a0, a48\n"
"v_accvgpr_mov_b32 a1, a49\n"
"v_accvgpr_mov_b32 a4, a52\n"
"v_accvgpr_mov_b32 a5, a53\n"
"v_accvgpr_mov_b32 a8, a56\n"
"v_accvgpr_mov_b32 a16, a64\n"
"v_accvgpr_mov_b32 a25, a73\n"
"v_mfma_f32_16x16x32_bf16 v[88:91], v[112:115], a[244:247], v[88:91]\n"
"v_accvgpr_mov_b32 a20, a68\n"
"v_accvgpr_mov_b32 a29, a77\n"
"v_mfma_f32_16x16x32_bf16 v[88:91], v[116:119], a[248:251], v[88:91]\n"
"ds_read_b128 a[192:195], v243 offset:4096\n"
"ds_read_b128 a[200:203], v243 offset:6144\n"
"v_accvgpr_mov_b32 a6, a54\n"
"v_accvgpr_mov_b32 a9, a57\n"
"v_accvgpr_mov_b32 a11, a59\n"
"v_accvgpr_mov_b32 a13, a61\n"
"v_accvgpr_mov_b32 a14, a62\n"
"v_accvgpr_mov_b32 a17, a65\n"
"v_accvgpr_mov_b32 a23, a71\n"
"v_accvgpr_mov_b32 a31, a79\n"
"v_accvgpr_mov_b32 a33, a81\n"
"v_accvgpr_mov_b32 a35, a83\n"
"v_accvgpr_mov_b32 a44, a92\n"
"v_accvgpr_mov_b32 a47, a95\n"
"s_waitcnt lgkmcnt(5)\n"
"v_mfma_f32_16x16x32_bf16 v[180:183], v[124:127], a[0:3], 0\n"
"v_xor_b32 v244, 0x11000, v242\n"
"s_waitcnt lgkmcnt(3)\n"
"v_mfma_f32_16x16x32_bf16 v[180:183], v[128:131], a[4:7], v[180:183]\n"
"v_accvgpr_mov_b32 a34, a82\n"
"v_accvgpr_mov_b32 a43, a91\n"
"v_mfma_f32_16x16x32_bf16 v[180:183], v[132:135], a[8:11], v[180:183]\n"
"s_waitcnt lgkmcnt(2)\n"
"ds_read_b128 a[204:207], v148 offset:6144\n"
"v_accvgpr_mov_b32 a37, a85\n"
"v_mfma_f32_16x16x32_bf16 v[180:183], v[136:139], a[12:15], v[180:183]\n"
"ds_read_b128 a[196:199], v148 offset:4096\n"
"v_accvgpr_mov_b32 a21, a69\n"
"v_accvgpr_mov_b32 a24, a72\n"
"v_accvgpr_mov_b32 a32, a80\n"
"v_accvgpr_mov_b32 a38, a86\n"
"v_accvgpr_mov_b32 a40, a88\n"
"v_accvgpr_mov_b32 a42, a90\n"
"v_accvgpr_mov_b32 a46, a94\n"
"v_mfma_f32_16x16x32_bf16 v[184:187], v[124:127], a[16:19], 0\n"
"v_mov_b32_dpp v246, v246 row_ror:4 row_mask:0x8 bank_mask:0xf\n"
"v_accvgpr_mov_b32 a27, a75\n"
"v_mfma_f32_16x16x32_bf16 v[184:187], v[128:131], a[20:23], v[184:187]\n"
"v_mov_b32_dpp v247, v247 row_ror:12 row_mask:0x2 bank_mask:0xf\n"
"v_mfma_f32_16x16x32_bf16 v[184:187], v[132:135], a[24:27], v[184:187]\n"
"s_add_u32 s40, s56, s53\n"
"ds_read_b64_tr_b16 v[224:225], v239 offset:50176\n"
"v_mfma_f32_16x16x32_bf16 v[184:187], v[136:139], a[28:31], v[184:187]\n"
"buffer_load_dwordx4 v240, s[20:23], s40 offen offset:2048 nt lds\n"
"v_accvgpr_mov_b32 a41, a89\n"
"v_sub_f32 v180, s74, v180\n"
"v_sub_f32 v181, s74, v181\n"
"v_sub_f32 v183, s74, v183\n"
"v_mul_f32_dpp v26, v246, v26 row_newbcast:2 row_mask:0xf bank_mask:0xf\n"
"v_mfma_f32_16x16x32_bf16 v[188:191], v[124:127], a[32:35], 0\n"
"v_mov_b32_dpp v247, v247 row_ror:8 row_mask:0x4 bank_mask:0xf\n"
"v_accvgpr_mov_b32 a45, a93\n"
"v_mfma_f32_16x16x32_bf16 v[188:191], v[128:131], a[36:39], v[188:191]\n"
"v_mov_b32 v252, v192\n"
"v_mfma_f32_16x16x32_bf16 v[188:191], v[132:135], a[40:43], v[188:191]\n"
"v_mfma_f32_16x16x32_bf16 v[188:191], v[136:139], a[44:47], v[188:191]\n"
"v_mul_f32_dpp v11, v246, v11 row_newbcast:3 row_mask:0xf bank_mask:0xf\n"
"s_waitcnt lgkmcnt(1)\n"
"v_mfma_f32_16x16x32_bf16 v[192:195], v[124:127], a[192:195], 0\n"
"ds_read_b64_tr_b16 a[252:253], v235 offset:50176\n"
"v_sub_f32 v185, s74, v185\n"
"v_mfma_f32_16x16x32_bf16 v[192:195], v[128:131], a[196:199], v[192:195]\n"
"ds_read_b64_tr_b16 a[248:249], v236 offset:49152\n"
"v_sub_f32 v182, s74, v182\n"
"v_mfma_f32_16x16x32_bf16 v[192:195], v[132:135], a[200:203], v[192:195]\n"
"v_sub_f32 v184, s74, v184\n"
"v_mfma_f32_16x16x32_bf16 v[192:195], v[136:139], a[204:207], v[192:195]\n"
"ds_read_b64_tr_b16 a[254:255], v235 offset:54272\n"
"ds_read_b64_tr_b16 a[246:247], v232 offset:53248\n"
"v_mov_b32 v253, v202\n"
"v_sub_f32 v186, s74, v186\n"
"v_mfma_f32_16x16x32_bf16 v[152:155], v[208:211], a[0:3], v[180:183]\n"
"v_mfma_f32_16x16x32_bf16 v[152:155], v[212:215], a[4:7], v[152:155]\n"
"v_sub_f32 v187, s74, v187\n"
"v_mfma_f32_16x16x32_bf16 v[152:155], v[216:219], a[8:11], v[152:155]\n"
"s_add_u32 s40, s56, s54\n"
"v_mfma_f32_16x16x32_bf16 v[152:155], v[220:223], a[12:15], v[152:155]\n"
"buffer_load_dwordx4 v240, s[20:23], s40 offen offset:3072 nt lds\n"
"v_mov_b32_dpp v247, v247 row_ror:4 row_mask:0x8 bank_mask:0xf\n"
"v_sub_f32 v189, s74, v189\n"
"v_sub_f32 v190, s74, v190\n"
"v_sub_f32 v191, s74, v191\n"
"v_sub_f32 v192, s74, v192\n"
"v_mfma_f32_16x16x32_bf16 v[156:159], v[216:219], a[24:27], v[184:187]\n"
"s_add_u32 s55, s55, s59\n"
"v_mfma_f32_16x16x32_bf16 v[156:159], v[220:223], a[28:31], v[156:159]\n"
"s_or_b32 s55, s55, s58\n"
"v_sub_f32 v188, s74, v188\n"
"v_mfma_f32_16x16x32_bf16 v[156:159], v[208:211], a[16:19], v[156:159]\n"
"s_xor_b32 s56, s55, s57\n"
"v_exp_f32 v153, v153\n"
"v_mfma_f32_16x16x32_bf16 v[156:159], v[212:215], a[20:23], v[156:159]\n"
"ds_write_b64 v250, v[252:253] offset:52224\n"
"v_sub_f32 v193, s74, v193\n"
"v_sub_f32 v194, s74, v194\n"
"v_exp_f32 v152, v152\n"
"v_mfma_f32_16x16x32_bf16 v[160:163], v[208:211], a[32:35], v[188:191]\n"
"v_mfma_f32_16x16x32_bf16 v[160:163], v[212:215], a[36:39], v[160:163]\n"
"v_rcp_f32 v206, v200\n"
"v_mfma_f32_16x16x32_bf16 v[160:163], v[216:219], a[40:43], v[160:163]\n"
"v_sub_f32 v195, s74, v195\n"
"v_mfma_f32_16x16x32_bf16 v[160:163], v[220:223], a[44:47], v[160:163]\n"
"ds_write_b128 v244, a[216:219] offset:32768\n"
"v_mul_f32_dpp v25, v246, v25 row_newbcast:1 row_mask:0xf bank_mask:0xf\n"
"v_mfma_f32_16x16x32_bf16 v[164:167], v[216:219], a[200:203], v[192:195]\n"
"v_mov_b32_dpp v206, v206 row_ror:12 row_mask:0x2 bank_mask:0xf\n"
"v_mfma_f32_16x16x32_bf16 v[164:167], v[220:223], a[204:207], v[164:167]\n"
"v_mfma_f32_16x16x32_bf16 v[164:167], v[208:211], a[192:195], v[164:167]\n"
"ds_read_b128 v[208:211], v228 offset:4096\n"
"v_mfma_f32_16x16x32_bf16 v[164:167], v[212:215], a[196:199], v[164:167]\n"
"ds_read_b128 v[216:219], v228 offset:5120\n"
"ds_read_b128 v[220:223], v229 offset:5120\n"
"v_mov_b32_dpp v206, v206 row_ror:8 row_mask:0x4 bank_mask:0xf\n"
"v_exp_f32 v155, v155\n"
"v_exp_f32 v160, v160\n"
"s_branch LTC_%=\n"
"LTH1_%=:\n"
"v_mfma_f32_16x16x32_bf16 a[224:227], v[124:127], a[244:247], a[224:227]\n"
"v_exp_f32 v171, v171\n"
"v_mfma_f32_16x16x32_bf16 v[16:19], v[128:131], a[244:247], v[16:19]\n"
"s_waitcnt vmcnt(8)\n"
"v_exp_f32 v172, v172\n"
"v_mfma_f32_16x16x32_bf16 v[16:19], v[132:135], a[248:251], v[16:19]\n"
"s_barrier\n"
"v_mfma_f32_16x16x32_bf16 v[48:51], v[136:139], a[244:247], v[48:51]\n"
"v_exp_f32 v169, v169\n"
"v_mfma_f32_16x16x32_bf16 v[48:51], v[140:143], a[248:251], v[48:51]\n"
"ds_read_b128 v[220:223], v229 offset:1024\n"
"v_cvt_pk_bf16_f32 v117, v166, v167\n"
"v_exp_f32 v176, v176\n"
"v_exp_f32 v177, v177\n"
"v_exp_f32 v178, v178\n"
"v_mfma_f32_16x16x32_bf16 v[80:83], v[144:147], a[244:247], v[80:83]\n"
"ds_read_b64_tr_b16 a[244:245], v234 offset:49152\n"
"v_cvt_pk_bf16_f32 v108, v160, v161\n"
"v_mfma_f32_16x16x32_bf16 v[80:83], v[148:151], a[248:251], v[80:83]\n"
"ds_read_b64_tr_b16 a[246:247], v234 offset:53248\n"
"ds_read_b64_tr_b16 a[248:249], v238 offset:49152\n"
"v_exp_f32 v162, v162\n"
"v_exp_f32 v168, v168\n"
"v_exp_f32 v173, v173\n"
"v_exp_f32 v175, v175\n"
"v_mfma_f32_16x16x32_bf16 v[84:87], v[148:151], v[224:227], v[84:87]\n"
"ds_read_b64_tr_b16 a[250:251], v238 offset:53248\n"
"v_cvt_pk_bf16_f32 v116, v164, v165\n"
"v_mfma_f32_16x16x32_bf16 v[84:87], v[144:147], a[252:255], v[84:87]\n"
"v_exp_f32 v170, v170\n"
"v_mfma_f32_16x16x32_bf16 v[52:55], v[140:143], v[224:227], v[52:55]\n"
"v_cvt_pk_bf16_f32 v103, v170, v171\n"
"v_cvt_pk_bf16_f32 v110, v172, v173\n"
"v_mfma_f32_16x16x32_bf16 v[52:55], v[136:139], a[252:255], v[52:55]\n"
"v_cvt_pk_bf16_f32 v123, v154, v155\n"
"v_cvt_pk_bf16_f32 v101, v158, v159\n"
"v_mfma_f32_16x16x32_bf16 v[20:23], v[132:135], v[224:227], v[20:23]\n"
"v_cvt_pk_bf16_f32 v100, v156, v157\n"
"v_cvt_pk_bf16_f32 v102, v168, v169\n"
"v_mfma_f32_16x16x32_bf16 v[20:23], v[128:131], a[252:255], v[20:23]\n"
"v_exp_f32 v179, v179\n"
"v_mfma_f32_16x16x32_bf16 a[228:231], v[124:127], a[252:255], a[228:231]\n"
"s_waitcnt lgkmcnt(2)\n"
"ds_read_b64_tr_b16 a[252:253], v235 offset:49152\n"
"ds_read_b64_tr_b16 v[226:227], v239 offset:53248\n"
"v_mfma_f32_16x16x32_bf16 a[216:219], v[120:123], a[244:247], a[216:219]\n"
"s_add_u32 m0, s44, 81920\n"
"ds_read_b64_tr_b16 v[224:225], v239 offset:49152\n"
"v_cvt_pk_bf16_f32 v119, v178, v179\n"
"v_smfmac_f32_16x16x64_bf16 v[196:199], a[240:243], v[96:103], v241\n"
"buffer_load_dwordx4 v240, s[20:23], s56 offen nt lds\n"
"v_mfma_f32_16x16x32_bf16 v[8:11], v[96:99], a[244:247], v[8:11]\n"
"s_waitcnt lgkmcnt(3)\n"
"ds_read_b64_tr_b16 a[254:255], v235 offset:53248\n"
"v_cvt_pk_bf16_f32 v118, v176, v177\n"
"v_mfma_f32_16x16x32_bf16 v[8:11], v[100:103], a[248:251], v[8:11]\n"
"v_exp_f32 v174, v174\n"
"v_smfmac_f32_16x16x64_bf16 v[200:203], a[240:243], v[112:119], v241\n"
"v_cvt_pk_bf16_f32 v109, v162, v163\n"
"v_cvt_pk_bf16_f32 v111, v174, v175\n"
"v_mfma_f32_16x16x32_bf16 v[40:43], v[104:107], a[244:247], v[40:43]\n"
"ds_read_b128 v[212:215], v229 offset:0\n"
"v_and_b32 v245, 0x3f0, v242\n"
"v_mfma_f32_16x16x32_bf16 v[40:43], v[108:111], a[248:251], v[40:43]\n"
"v_lshlrev_b32_e64 v244, 24, s43\n"
"v_mul_f32 v244, 0x45800000, v244\n"
"v_smfmac_f32_16x16x64_bf16 v[198:201], a[240:243], v[104:111], v241\n"
"ds_read_b128 v[208:211], v228 offset:0\n"
"v_add_f32 v196, v196, v197\n"
"v_mfma_f32_16x16x32_bf16 v[72:75], v[112:115], a[244:247], v[72:75]\n"
"ds_read_b64_tr_b16 a[244:245], v234 offset:50176\n"
"v_lshrrev_b32 v245, 1, v245\n"
"v_mfma_f32_16x16x32_bf16 v[72:75], v[116:119], a[248:251], v[72:75]\n"
"ds_read_b64_tr_b16 a[246:247], v234 offset:54272\n"
"v_sub_u32 v245, v242, v245\n"
"v_mfma_f32_16x16x32_bf16 v[202:205], a[240:243], v[120:123], v[202:205]\n"
"s_waitcnt lgkmcnt(4)\n"
"ds_read_b64_tr_b16 a[248:249], v238 offset:50176\n"
"v_add_f32 v200, v200, v201\n"
"v_mfma_f32_16x16x32_bf16 v[76:79], v[116:119], v[224:227], v[76:79]\n"
"v_add_f32 v198, v198, v199\n"
"v_add_f32 v254, v196, v198\n"
"v_mfma_f32_16x16x32_bf16 v[76:79], v[112:115], a[252:255], v[76:79]\n"
"ds_read_b64_tr_b16 a[250:251], v238 offset:54272\n"
"v_xor_b32 v250, 0x1000, v245\n"
"v_mfma_f32_16x16x32_bf16 v[44:47], v[108:111], v[224:227], v[44:47]\n"
"ds_read_b128 v[216:219], v228 offset:1024\n"
"v_add_f32 v254, v254, v200\n"
"v_mfma_f32_16x16x32_bf16 v[44:47], v[104:107], a[252:255], v[44:47]\n"
"v_add_f32 v244, v244, v202\n"
"v_add_f32 v254, v254, v202\n"
"v_mfma_f32_16x16x32_bf16 v[12:15], v[100:103], v[224:227], v[12:15]\n"
"s_add_u32 s40, s56, s52\n"
"ds_read_b64_tr_b16 v[226:227], v239 offset:54272\n"
"v_min3_f32 v244, v244, v196, v198\n"
"v_mfma_f32_16x16x32_bf16 v[12:15], v[96:99], a[252:255], v[12:15]\n"
"buffer_load_dwordx4 v240, s[20:23], s40 offen offset:1024 nt lds\n"
"v_min_f32 v244, v244, v200\n"
"v_rcp_f32 v244, v244\n"
"v_mfma_f32_16x16x32_bf16 a[220:223], v[120:123], a[252:255], a[220:223]\n"
"s_waitcnt lgkmcnt(4)\n"
"v_fmac_f32 v254, 0x68800000, v244\n"
"v_cmp_nge_f32 vcc, s45, v254\n"
"s_cbranch_vccnz LSLOW_%=\nLBACK1_%=:\ns_mov_b64 vcc, s[64:65]\n"
"v_mfma_f32_16x16x32_bf16 a[232:235], v[120:123], a[244:247], a[232:235]\n"
"v_rcp_f32 v246, v196\n"
"v_accvgpr_mov_b32 a2, a98\n"
"v_accvgpr_mov_b32 a7, a103\n"
"v_accvgpr_mov_b32 a10, a106\n"
"v_accvgpr_mov_b32 a15, a111\n"
"v_accvgpr_mov_b32 a22, a118\n"
"v_accvgpr_mov_b32 a30, a126\n"
"v_bfe_u32 v149, v242, 8, 2\n"
"v_lshl_add_u32 v149, v149, 4, s43\n"
"v_mfma_f32_16x16x32_bf16 v[24:27], v[96:99], a[244:247], v[24:27]\n"
"s_waitcnt lgkmcnt(2)\n"
"ds_read_b128 v[124:127], v149 offset:0\n"
"v_accvgpr_mov_b32 a28, a124\n"
"v_mfma_f32_16x16x32_bf16 v[24:27], v[100:103], a[248:251], v[24:27]\n"
"ds_read_b128 v[132:135], v149 offset:1024\n"
"v_mov_b32_dpp v246, v246 row_ror:12 row_mask:0x2 bank_mask:0xf\n"
"v_accvgpr_mov_b32 a3, a99\n"
"v_accvgpr_mov_b32 a12, a108\n"
"v_accvgpr_mov_b32 a18, a114\n"
"v_accvgpr_mov_b32 a19, a115\n"
"v_accvgpr_mov_b32 a26, a122\n"
"v_accvgpr_mov_b32 a36, a132\n"
"v_xor_b32 v148, 0x400, v243\n"
"v_xor_b32 v150, 64, v149\n"
"v_accvgpr_mov_b32 a124, a76\n"
"v_mfma_f32_16x16x32_bf16 v[56:59], v[104:107], a[244:247], v[56:59]\n"
"ds_read_b128 v[128:131], v150 offset:0\n"
"v_accvgpr_mov_b32 a39, a135\n"
"v_mfma_f32_16x16x32_bf16 v[56:59], v[108:111], a[248:251], v[56:59]\n"
"ds_read_b128 v[136:139], v150 offset:1024\n"
"v_rcp_f32 v247, v198\n"
"v_mov_b32_dpp v246, v246 row_ror:8 row_mask:0x4 bank_mask:0xf\n"
"v_accvgpr_mov_b32 a0, a96\n"
"v_accvgpr_mov_b32 a1, a97\n"
"v_accvgpr_mov_b32 a4, a100\n"
"v_accvgpr_mov_b32 a5, a101\n"
"v_accvgpr_mov_b32 a8, a104\n"
"v_accvgpr_mov_b32 a16, a112\n"
"v_accvgpr_mov_b32 a25, a121\n"
"v_mfma_f32_16x16x32_bf16 v[88:91], v[112:115], a[244:247], v[88:91]\n"
"v_accvgpr_mov_b32 a20, a116\n"
"v_accvgpr_mov_b32 a29, a125\n"
"v_mfma_f32_16x16x32_bf16 v[88:91], v[116:119], a[248:251], v[88:91]\n"
"s_waitcnt lgkmcnt(3)\n"
"ds_read_b128 a[192:195], v243 offset:4096\n"
"ds_read_b128 a[200:203], v243 offset:6144\n"
"v_accvgpr_mov_b32 a6, a102\n"
"v_accvgpr_mov_b32 a9, a105\n"
"v_accvgpr_mov_b32 a11, a107\n"
"v_accvgpr_mov_b32 a13, a109\n"
"v_accvgpr_mov_b32 a14, a110\n"
"v_accvgpr_mov_b32 a17, a113\n"
"v_accvgpr_mov_b32 a23, a119\n"
"v_accvgpr_mov_b32 a31, a127\n"
"v_accvgpr_mov_b32 a33, a129\n"
"v_accvgpr_mov_b32 a35, a131\n"
"v_accvgpr_mov_b32 a44, a140\n"
"v_accvgpr_mov_b32 a47, a143\n"
"v_accvgpr_mov_b32 a107, a59\n"
"v_mfma_f32_16x16x32_bf16 v[180:183], v[124:127], a[0:3], 0\n"
"s_waitcnt lgkmcnt(3)\n"
"v_xor_b32 v244, 0x11000, v242\n"
"v_accvgpr_mov_b32 a132, a84\n"
"v_mfma_f32_16x16x32_bf16 v[180:183], v[128:131], a[4:7], v[180:183]\n"
"v_accvgpr_mov_b32 a34, a130\n"
"v_accvgpr_mov_b32 a43, a139\n"
"v_mfma_f32_16x16x32_bf16 v[180:183], v[132:135], a[8:11], v[180:183]\n"
"s_waitcnt lgkmcnt(2)\n"
"ds_read_b128 a[204:207], v148 offset:6144\n"
"v_accvgpr_mov_b32 a37, a133\n"
"v_mfma_f32_16x16x32_bf16 v[180:183], v[136:139], a[12:15], v[180:183]\n"
"ds_read_b128 a[196:199], v148 offset:4096\n"
"v_accvgpr_mov_b32 a21, a117\n"
"v_accvgpr_mov_b32 a24, a120\n"
"v_accvgpr_mov_b32 a32, a128\n"
"v_accvgpr_mov_b32 a38, a134\n"
"v_accvgpr_mov_b32 a40, a136\n"
"v_accvgpr_mov_b32 a42, a138\n"
"v_accvgpr_mov_b32 a46, a142\n"
"v_accvgpr_mov_b32 a117, a69\n"
"v_accvgpr_mov_b32 a111, a63\n"
"v_mfma_f32_16x16x32_bf16 v[184:187], v[124:127], a[16:19], 0\n"
"v_mov_b32_dpp v246, v246 row_ror:4 row_mask:0x8 bank_mask:0xf\n"
"v_accvgpr_mov_b32 a27, a123\n"
"v_mfma_f32_16x16x32_bf16 v[184:187], v[128:131], a[20:23], v[184:187]\n"
"v_mov_b32_dpp v247, v247 row_ror:12 row_mask:0x2 bank_mask:0xf\n"
"v_accvgpr_mov_b32 a97, a49\n"
"v_mfma_f32_16x16x32_bf16 v[184:187], v[132:135], a[24:27], v[184:187]\n"
"s_add_u32 s40, s56, s53\n"
"ds_read_b64_tr_b16 v[224:225], v239 offset:50176\n"
"v_accvgpr_mov_b32 a112, a64\n"
"v_mfma_f32_16x16x32_bf16 v[184:187], v[136:139], a[28:31], v[184:187]\n"
"buffer_load_dwordx4 v240, s[20:23], s40 offen offset:2048 nt lds\n"
"v_accvgpr_mov_b32 a41, a137\n"
"v_accvgpr_mov_b32 a126, a78\n"
"v_accvgpr_mov_b32 a114, a66\n"
"v_accvgpr_mov_b32 a137, a89\n"
"v_sub_f32 v180, s74, v180\n"
"v_sub_f32 v181, s74, v181\n"
"v_sub_f32 v183, s74, v183\n"
"v_mul_f32_dpp v26, v246, v26 row_newbcast:2 row_mask:0xf bank_mask:0xf\n"
"v_mfma_f32_16x16x32_bf16 v[188:191], v[124:127], a[32:35], 0\n"
"v_mov_b32_dpp v247, v247 row_ror:8 row_mask:0x4 bank_mask:0xf\n"
"v_accvgpr_mov_b32 a45, a141\n"
"v_mfma_f32_16x16x32_bf16 v[188:191], v[128:131], a[36:39], v[188:191]\n"
"v_mov_b32 v252, v192\n"
"v_accvgpr_mov_b32 a133, a85\n"
"v_mfma_f32_16x16x32_bf16 v[188:191], v[132:135], a[40:43], v[188:191]\n"
"v_accvgpr_mov_b32 a118, a70\n"
"v_accvgpr_mov_b32 a105, a57\n"
"v_mfma_f32_16x16x32_bf16 v[188:191], v[136:139], a[44:47], v[188:191]\n"
"s_waitcnt lgkmcnt(1)\n"
"v_accvgpr_mov_b32 a109, a61\n"
"v_mul_f32_dpp v11, v246, v11 row_newbcast:3 row_mask:0xf bank_mask:0xf\n"
"v_mfma_f32_16x16x32_bf16 v[192:195], v[124:127], a[192:195], 0\n"
"ds_read_b64_tr_b16 a[252:253], v235 offset:50176\n"
"v_sub_f32 v185, s74, v185\n"
"v_mfma_f32_16x16x32_bf16 v[192:195], v[128:131], a[196:199], v[192:195]\n"
"ds_read_b64_tr_b16 a[248:249], v236 offset:49152\n"
"v_sub_f32 v182, s74, v182\n"
"v_mfma_f32_16x16x32_bf16 v[192:195], v[132:135], a[200:203], v[192:195]\n"
"v_accvgpr_mov_b32 a138, a90\n"
"v_sub_f32 v184, s74, v184\n"
"v_mfma_f32_16x16x32_bf16 v[192:195], v[136:139], a[204:207], v[192:195]\n"
"ds_read_b64_tr_b16 a[254:255], v235 offset:54272\n"
"ds_read_b64_tr_b16 a[246:247], v232 offset:53248\n"
"v_mov_b32 v253, v202\n"
"v_accvgpr_mov_b32 a116, a68\n"
"v_accvgpr_mov_b32 a125, a77\n"
"v_accvgpr_mov_b32 a113, a65\n"
"v_accvgpr_mov_b32 a128, a80\n"
"v_accvgpr_mov_b32 a130, a82\n"
"v_accvgpr_mov_b32 a136, a88\n"
"v_sub_f32 v186, s74, v186\n"
"v_mfma_f32_16x16x32_bf16 v[152:155], v[208:211], a[0:3], v[180:183]\n"
"v_accvgpr_mov_b32 a103, a55\n"
"v_accvgpr_mov_b32 a120, a72\n"
"v_mfma_f32_16x16x32_bf16 v[152:155], v[212:215], a[4:7], v[152:155]\n"
"v_accvgpr_mov_b32 a141, a93\n"
"v_sub_f32 v187, s74, v187\n"
"v_mfma_f32_16x16x32_bf16 v[152:155], v[216:219], a[8:11], v[152:155]\n"
"s_add_u32 s40, s56, s54\n"
"v_accvgpr_mov_b32 a127, a79\n"
"v_accvgpr_mov_b32 a140, a92\n"
"v_mfma_f32_16x16x32_bf16 v[152:155], v[220:223], a[12:15], v[152:155]\n"
"buffer_load_dwordx4 v240, s[20:23], s40 offen offset:3072 nt lds\n"
"v_mov_b32_dpp v247, v247 row_ror:4 row_mask:0x8 bank_mask:0xf\n"
"v_accvgpr_mov_b32 a121, a73\n"
"v_accvgpr_mov_b32 a122, a74\n"
"v_accvgpr_mov_b32 a123, a75\n"
"v_sub_f32 v189, s74, v189\n"
"v_sub_f32 v190, s74, v190\n"
"v_sub_f32 v191, s74, v191\n"
"v_sub_f32 v192, s74, v192\n"
"v_mfma_f32_16x16x32_bf16 v[156:159], v[216:219], a[24:27], v[184:187]\n"
"s_add_u32 s55, s55, s59\n"
"v_accvgpr_mov_b32 a143, a95\n"
"v_accvgpr_mov_b32 a115, a67\n"
"v_mfma_f32_16x16x32_bf16 v[156:159], v[220:223], a[28:31], v[156:159]\n"
"s_or_b32 s55, s55, s58\n"
"v_accvgpr_mov_b32 a101, a53\n"
"v_sub_f32 v188, s74, v188\n"
"v_mfma_f32_16x16x32_bf16 v[156:159], v[208:211], a[16:19], v[156:159]\n"
"s_xor_b32 s56, s55, s57\n"
"v_exp_f32 v153, v153\n"
"v_mfma_f32_16x16x32_bf16 v[156:159], v[212:215], a[20:23], v[156:159]\n"
"ds_write_b64 v250, v[252:253] offset:52224\n"
"v_accvgpr_mov_b32 a100, a52\n"
"v_accvgpr_mov_b32 a135, a87\n"
"v_accvgpr_mov_b32 a98, a50\n"
"v_sub_f32 v193, s74, v193\n"
"v_sub_f32 v194, s74, v194\n"
"v_exp_f32 v152, v152\n"
"v_mfma_f32_16x16x32_bf16 v[160:163], v[208:211], a[32:35], v[188:191]\n"
"v_accvgpr_mov_b32 a110, a62\n"
"v_accvgpr_mov_b32 a96, a48\n"
"v_mfma_f32_16x16x32_bf16 v[160:163], v[212:215], a[36:39], v[160:163]\n"
"v_rcp_f32 v206, v200\n"
"v_mfma_f32_16x16x32_bf16 v[160:163], v[216:219], a[40:43], v[160:163]\n"
"v_accvgpr_mov_b32 a142, a94\n"
"v_sub_f32 v195, s74, v195\n"
"v_mfma_f32_16x16x32_bf16 v[160:163], v[220:223], a[44:47], v[160:163]\n"
"ds_write_b128 v244, a[216:219] offset:32768\n"
"v_accvgpr_mov_b32 a102, a54\n"
"v_accvgpr_mov_b32 a108, a60\n"
"v_accvgpr_mov_b32 a99, a51\n"
"v_accvgpr_mov_b32 a129, a81\n"
"v_mul_f32_dpp v25, v246, v25 row_newbcast:1 row_mask:0xf bank_mask:0xf\n"
"v_mfma_f32_16x16x32_bf16 v[164:167], v[216:219], a[200:203], v[192:195]\n"
"v_mov_b32_dpp v206, v206 row_ror:12 row_mask:0x2 bank_mask:0xf\n"
"v_accvgpr_mov_b32 a119, a71\n"
"v_mfma_f32_16x16x32_bf16 v[164:167], v[220:223], a[204:207], v[164:167]\n"
"v_accvgpr_mov_b32 a131, a83\n"
"v_accvgpr_mov_b32 a139, a91\n"
"v_mfma_f32_16x16x32_bf16 v[164:167], v[208:211], a[192:195], v[164:167]\n"
"ds_read_b128 v[208:211], v228 offset:4096\n"
"v_accvgpr_mov_b32 a106, a58\n"
"v_mfma_f32_16x16x32_bf16 v[164:167], v[212:215], a[196:199], v[164:167]\n"
"s_waitcnt lgkmcnt(2)\n"
"ds_read_b128 v[216:219], v228 offset:5120\n"
"ds_read_b128 v[220:223], v229 offset:5120\n"
"v_mov_b32_dpp v206, v206 row_ror:8 row_mask:0x4 bank_mask:0xf\n"
"v_accvgpr_mov_b32 a134, a86\n"
"v_accvgpr_mov_b32 a104, a56\n"
"v_exp_f32 v155, v155\n"
"v_exp_f32 v160, v160\n"
"s_branch LTC_%=\n"
"LTH2_%=:\n"
"v_mfma_f32_16x16x32_bf16 a[224:227], v[124:127], a[244:247], a[224:227]\n"
"v_exp_f32 v171, v171\n"
"v_mfma_f32_16x16x32_bf16 v[16:19], v[128:131], a[244:247], v[16:19]\n"
"s_waitcnt vmcnt(8)\n"
"v_exp_f32 v172, v172\n"
"v_mfma_f32_16x16x32_bf16 v[16:19], v[132:135], a[248:251], v[16:19]\n"
"s_barrier\n"
"v_mfma_f32_16x16x32_bf16 v[48:51], v[136:139], a[244:247], v[48:51]\n"
"v_exp_f32 v169, v169\n"
"v_mfma_f32_16x16x32_bf16 v[48:51], v[140:143], a[248:251], v[48:51]\n"
"ds_read_b128 v[220:223], v229 offset:1024\n"
"v_cvt_pk_bf16_f32 v117, v166, v167\n"
"v_exp_f32 v176, v176\n"
"v_exp_f32 v177, v177\n"
"v_exp_f32 v178, v178\n"
"v_mfma_f32_16x16x32_bf16 v[80:83], v[144:147], a[244:247], v[80:83]\n"
"ds_read_b64_tr_b16 a[244:245], v234 offset:49152\n"
"v_cvt_pk_bf16_f32 v108, v160, v161\n"
"v_mfma_f32_16x16x32_bf16 v[80:83], v[148:151], a[248:251], v[80:83]\n"
"ds_read_b64_tr_b16 a[246:247], v234 offset:53248\n"
"ds_read_b64_tr_b16 a[248:249], v238 offset:49152\n"
"v_exp_f32 v162, v162\n"
"v_exp_f32 v168, v168\n"
"v_exp_f32 v173, v173\n"
"v_exp_f32 v175, v175\n"
"v_mfma_f32_16x16x32_bf16 v[84:87], v[148:151], v[224:227], v[84:87]\n"
"ds_read_b64_tr_b16 a[250:251], v238 offset:53248\n"
"v_cvt_pk_bf16_f32 v116, v164, v165\n"
"v_mfma_f32_16x16x32_bf16 v[84:87], v[144:147], a[252:255], v[84:87]\n"
"v_exp_f32 v170, v170\n"
"v_mfma_f32_16x16x32_bf16 v[52:55], v[140:143], v[224:227], v[52:55]\n"
"v_cvt_pk_bf16_f32 v103, v170, v171\n"
"v_cvt_pk_bf16_f32 v110, v172, v173\n"
"v_mfma_f32_16x16x32_bf16 v[52:55], v[136:139], a[252:255], v[52:55]\n"
"v_cvt_pk_bf16_f32 v123, v154, v155\n"
"v_cvt_pk_bf16_f32 v101, v158, v159\n"
"v_mfma_f32_16x16x32_bf16 v[20:23], v[132:135], v[224:227], v[20:23]\n"
"v_cvt_pk_bf16_f32 v100, v156, v157\n"
"v_cvt_pk_bf16_f32 v102, v168, v169\n"
"v_mfma_f32_16x16x32_bf16 v[20:23], v[128:131], a[252:255], v[20:23]\n"
"v_exp_f32 v179, v179\n"
"v_mfma_f32_16x16x32_bf16 a[228:231], v[124:127], a[252:255], a[228:231]\n"
"s_waitcnt lgkmcnt(2)\n"
"ds_read_b64_tr_b16 a[252:253], v235 offset:49152\n"
"ds_read_b64_tr_b16 v[226:227], v239 offset:53248\n"
"v_mfma_f32_16x16x32_bf16 a[216:219], v[120:123], a[244:247], a[216:219]\n"
"s_add_u32 m0, s44, 81920\n"
"ds_read_b64_tr_b16 v[224:225], v239 offset:49152\n"
"v_cvt_pk_bf16_f32 v119, v178, v179\n"
"v_smfmac_f32_16x16x64_bf16 v[196:199], a[240:243], v[96:103], v241\n"
"buffer_load_dwordx4 v240, s[20:23], s56 offen nt lds\n"
"v_mfma_f32_16x16x32_bf16 v[8:11], v[96:99], a[244:247], v[8:11]\n"
"s_waitcnt lgkmcnt(3)\n"
"ds_read_b64_tr_b16 a[254:255], v235 offset:53248\n"
"v_cvt_pk_bf16_f32 v118, v176, v177\n"
"v_mfma_f32_16x16x32_bf16 v[8:11], v[100:103], a[248:251], v[8:11]\n"
"v_exp_f32 v174, v174\n"
"v_smfmac_f32_16x16x64_bf16 v[200:203], a[240:243], v[112:119], v241\n"
"v_cvt_pk_bf16_f32 v109, v162, v163\n"
"v_cvt_pk_bf16_f32 v111, v174, v175\n"
"v_mfma_f32_16x16x32_bf16 v[40:43], v[104:107], a[244:247], v[40:43]\n"
"ds_read_b128 v[212:215], v229 offset:0\n"
"v_and_b32 v245, 0x3f0, v242\n"
"v_mfma_f32_16x16x32_bf16 v[40:43], v[108:111], a[248:251], v[40:43]\n"
"v_lshlrev_b32_e64 v244, 24, s43\n"
"v_mul_f32 v244, 0x45800000, v244\n"
"v_smfmac_f32_16x16x64_bf16 v[198:201], a[240:243], v[104:111], v241\n"
"ds_read_b128 v[208:211], v228 offset:0\n"
"v_add_f32 v196, v196, v197\n"
"v_mfma_f32_16x16x32_bf16 v[72:75], v[112:115], a[244:247], v[72:75]\n"
"ds_read_b64_tr_b16 a[244:245], v234 offset:50176\n"
"v_lshrrev_b32 v245, 1, v245\n"
"v_mfma_f32_16x16x32_bf16 v[72:75], v[116:119], a[248:251], v[72:75]\n"
"ds_read_b64_tr_b16 a[246:247], v234 offset:54272\n"
"v_sub_u32 v245, v242, v245\n"
"v_mfma_f32_16x16x32_bf16 v[202:205], a[240:243], v[120:123], v[202:205]\n"
"s_waitcnt lgkmcnt(4)\n"
"ds_read_b64_tr_b16 a[248:249], v238 offset:50176\n"
"v_add_f32 v200, v200, v201\n"
"v_mfma_f32_16x16x32_bf16 v[76:79], v[116:119], v[224:227], v[76:79]\n"
"v_add_f32 v198, v198, v199\n"
"v_add_f32 v254, v196, v198\n"
"v_mfma_f32_16x16x32_bf16 v[76:79], v[112:115], a[252:255], v[76:79]\n"
"ds_read_b64_tr_b16 a[250:251], v238 offset:54272\n"
"v_xor_b32 v250, 0x1000, v245\n"
"v_mfma_f32_16x16x32_bf16 v[44:47], v[108:111], v[224:227], v[44:47]\n"
"ds_read_b128 v[216:219], v228 offset:1024\n"
"v_add_f32 v254, v254, v200\n"
"v_mfma_f32_16x16x32_bf16 v[44:47], v[104:107], a[252:255], v[44:47]\n"
"v_add_f32 v244, v244, v202\n"
"v_add_f32 v254, v254, v202\n"
"v_mfma_f32_16x16x32_bf16 v[12:15], v[100:103], v[224:227], v[12:15]\n"
"s_add_u32 s40, s56, s52\n"
"ds_read_b64_tr_b16 v[226:227], v239 offset:54272\n"
"v_min3_f32 v244, v244, v196, v198\n"
"v_mfma_f32_16x16x32_bf16 v[12:15], v[96:99], a[252:255], v[12:15]\n"
"buffer_load_dwordx4 v240, s[20:23], s40 offen offset:1024 nt lds\n"
"v_min_f32 v244, v244, v200\n"
"v_rcp_f32 v244, v244\n"
"v_mfma_f32_16x16x32_bf16 a[220:223], v[120:123], a[252:255], a[220:223]\n"
"s_waitcnt lgkmcnt(4)\n"
"v_fmac_f32 v254, 0x68800000, v244\n"
"v_cmp_nge_f32 vcc, s45, v254\n"
"s_cbranch_vccnz LSLOW_%=\nLBACK2_%=:\ns_mov_b64 vcc, s[64:65]\n"
"v_mfma_f32_16x16x32_bf16 a[232:235], v[120:123], a[244:247], a[232:235]\n"
"v_rcp_f32 v246, v196\n"
"v_accvgpr_mov_b32 a2, a146\n"
"v_accvgpr_mov_b32 a7, a151\n"
"v_accvgpr_mov_b32 a10, a154\n"
"v_accvgpr_mov_b32 a15, a159\n"
"v_accvgpr_mov_b32 a22, a166\n"
"v_accvgpr_mov_b32 a30, a174\n"
"v_bfe_u32 v149, v242, 8, 2\n"
"v_lshl_add_u32 v149, v149, 4, s43\n"
"v_mfma_f32_16x16x32_bf16 v[24:27], v[96:99], a[244:247], v[24:27]\n"
"s_waitcnt lgkmcnt(2)\n"
"ds_read_b128 v[124:127], v149 offset:0\n"
"v_accvgpr_mov_b32 a28, a172\n"
"v_mfma_f32_16x16x32_bf16 v[24:27], v[100:103], a[248:251], v[24:27]\n"
"ds_read_b128 v[132:135], v149 offset:1024\n"
"v_mov_b32_dpp v246, v246 row_ror:12 row_mask:0x2 bank_mask:0xf\n"
"v_accvgpr_mov_b32 a3, a147\n"
"v_accvgpr_mov_b32 a12, a156\n"
"v_accvgpr_mov_b32 a18, a162\n"
"v_accvgpr_mov_b32 a19, a163\n"
"v_accvgpr_mov_b32 a26, a170\n"
"v_accvgpr_mov_b32 a36, a180\n"
"v_xor_b32 v148, 0x400, v243\n"
"v_xor_b32 v150, 64, v149\n"
"v_accvgpr_mov_b32 a172, a76\n"
"v_mfma_f32_16x16x32_bf16 v[56:59], v[104:107], a[244:247], v[56:59]\n"
"ds_read_b128 v[128:131], v150 offset:0\n"
"v_accvgpr_mov_b32 a39, a183\n"
"v_mfma_f32_16x16x32_bf16 v[56:59], v[108:111], a[248:251], v[56:59]\n"
"ds_read_b128 v[136:139], v150 offset:1024\n"
"v_rcp_f32 v247, v198\n"
"v_mov_b32_dpp v246, v246 row_ror:8 row_mask:0x4 bank_mask:0xf\n"
"v_accvgpr_mov_b32 a0, a144\n"
"v_accvgpr_mov_b32 a1, a145\n"
"v_accvgpr_mov_b32 a4, a148\n"
"v_accvgpr_mov_b32 a5, a149\n"
"v_accvgpr_mov_b32 a8, a152\n"
"v_accvgpr_mov_b32 a16, a160\n"
"v_accvgpr_mov_b32 a25, a169\n"
"v_mfma_f32_16x16x32_bf16 v[88:91], v[112:115], a[244:247], v[88:91]\n"
"v_accvgpr_mov_b32 a20, a164\n"
"v_accvgpr_mov_b32 a29, a173\n"
"v_mfma_f32_16x16x32_bf16 v[88:91], v[116:119], a[248:251], v[88:91]\n"
"s_waitcnt lgkmcnt(3)\n"
"ds_read_b128 a[192:195], v243 offset:4096\n"
"ds_read_b128 a[200:203], v243 offset:6144\n"
"v_accvgpr_mov_b32 a6, a150\n"
"v_accvgpr_mov_b32 a9, a153\n"
"v_accvgpr_mov_b32 a11, a155\n"
"v_accvgpr_mov_b32 a13, a157\n"
"v_accvgpr_mov_b32 a14, a158\n"
"v_accvgpr_mov_b32 a17, a161\n"
"v_accvgpr_mov_b32 a23, a167\n"
"v_accvgpr_mov_b32 a31, a175\n"
"v_accvgpr_mov_b32 a33, a177\n"
"v_accvgpr_mov_b32 a35, a179\n"
"v_accvgpr_mov_b32 a44, a188\n"
"v_accvgpr_mov_b32 a47, a191\n"
"v_accvgpr_mov_b32 a155, a59\n"
"v_mfma_f32_16x16x32_bf16 v[180:183], v[124:127], a[0:3], 0\n"
"s_waitcnt lgkmcnt(3)\n"
"v_xor_b32 v244, 0x11000, v242\n"
"v_accvgpr_mov_b32 a180, a84\n"
"v_mfma_f32_16x16x32_bf16 v[180:183], v[128:131], a[4:7], v[180:183]\n"
"v_accvgpr_mov_b32 a34, a178\n"
"v_accvgpr_mov_b32 a43, a187\n"
"v_mfma_f32_16x16x32_bf16 v[180:183], v[132:135], a[8:11], v[180:183]\n"
"s_waitcnt lgkmcnt(2)\n"
"ds_read_b128 a[204:207], v148 offset:6144\n"
"v_accvgpr_mov_b32 a37, a181\n"
"v_mfma_f32_16x16x32_bf16 v[180:183], v[136:139], a[12:15], v[180:183]\n"
"ds_read_b128 a[196:199], v148 offset:4096\n"
"v_accvgpr_mov_b32 a21, a165\n"
"v_accvgpr_mov_b32 a24, a168\n"
"v_accvgpr_mov_b32 a32, a176\n"
"v_accvgpr_mov_b32 a38, a182\n"
"v_accvgpr_mov_b32 a40, a184\n"
"v_accvgpr_mov_b32 a42, a186\n"
"v_accvgpr_mov_b32 a46, a190\n"
"v_accvgpr_mov_b32 a165, a69\n"
"v_accvgpr_mov_b32 a159, a63\n"
"v_mfma_f32_16x16x32_bf16 v[184:187], v[124:127], a[16:19], 0\n"
"v_mov_b32_dpp v246, v246 row_ror:4 row_mask:0x8 bank_mask:0xf\n"
"v_accvgpr_mov_b32 a27, a171\n"
"v_mfma_f32_16x16x32_bf16 v[184:187], v[128:131], a[20:23], v[184:187]\n"
"v_mov_b32_dpp v247, v247 row_ror:12 row_mask:0x2 bank_mask:0xf\n"
"v_accvgpr_mov_b32 a145, a49\n"
"v_mfma_f32_16x16x32_bf16 v[184:187], v[132:135], a[24:27], v[184:187]\n"
"s_add_u32 s40, s56, s53\n"
"ds_read_b64_tr_b16 v[224:225], v239 offset:50176\n"
"v_accvgpr_mov_b32 a160, a64\n"
"v_mfma_f32_16x16x32_bf16 v[184:187], v[136:139], a[28:31], v[184:187]\n"
"buffer_load_dwordx4 v240, s[20:23], s40 offen offset:2048 nt lds\n"
"v_accvgpr_mov_b32 a41, a185\n"
"v_accvgpr_mov_b32 a174, a78\n"
"v_accvgpr_mov_b32 a162, a66\n"
"v_accvgpr_mov_b32 a185, a89\n"
"v_sub_f32 v180, s74, v180\n"
"v_sub_f32 v181, s74, v181\n"
"v_sub_f32 v183, s74, v183\n"
"v_mul_f32_dpp v26, v246, v26 row_newbcast:2 row_mask:0xf bank_mask:0xf\n"
"v_mfma_f32_16x16x32_bf16 v[188:191], v[124:127], a[32:35], 0\n"
"v_mov_b32_dpp v247, v247 row_ror:8 row_mask:0x4 bank_mask:0xf\n"
"v_accvgpr_mov_b32 a45, a189\n"
"v_mfma_f32_16x16x32_bf16 v[188:191], v[128:131], a[36:39], v[188:191]\n"
"v_mov_b32 v252, v192\n"
"v_accvgpr_mov_b32 a181, a85\n"
"v_mfma_f32_16x16x32_bf16 v[188:191], v[132:135], a[40:43], v[188:191]\n"
"v_accvgpr_mov_b32 a166, a70\n"
"v_accvgpr_mov_b32 a153, a57\n"
"v_mfma_f32_16x16x32_bf16 v[188:191], v[136:139], a[44:47], v[188:191]\n"
"s_waitcnt lgkmcnt(1)\n"
"v_accvgpr_mov_b32 a157, a61\n"
"v_mul_f32_dpp v11, v246, v11 row_newbcast:3 row_mask:0xf bank_mask:0xf\n"
"v_mfma_f32_16x16x32_bf16 v[192:195], v[124:127], a[192:195], 0\n"
"ds_read_b64_tr_b16 a[252:253], v235 offset:50176\n"
"v_sub_f32 v185, s74, v185\n"
"v_mfma_f32_16x16x32_bf16 v[192:195], v[128:131], a[196:199], v[192:195]\n"
"ds_read_b64_tr_b16 a[248:249], v236 offset:49152\n"
"v_sub_f32 v182, s74, v182\n"
"v_mfma_f32_16x16x32_bf16 v[192:195], v[132:135], a[200:203], v[192:195]\n"
"v_accvgpr_mov_b32 a186, a90\n"
"v_sub_f32 v184, s74, v184\n"
"v_mfma_f32_16x16x32_bf16 v[192:195], v[136:139], a[204:207], v[192:195]\n"
"ds_read_b64_tr_b16 a[254:255], v235 offset:54272\n"
"ds_read_b64_tr_b16 a[246:247], v232 offset:53248\n"
"v_mov_b32 v253, v202\n"
"v_accvgpr_mov_b32 a164, a68\n"
"v_accvgpr_mov_b32 a173, a77\n"
"v_accvgpr_mov_b32 a161, a65\n"
"v_accvgpr_mov_b32 a176, a80\n"
"v_accvgpr_mov_b32 a178, a82\n"
"v_accvgpr_mov_b32 a184, a88\n"
"v_sub_f32 v186, s74, v186\n"
"v_mfma_f32_16x16x32_bf16 v[152:155], v[208:211], a[0:3], v[180:183]\n"
"v_accvgpr_mov_b32 a151, a55\n"
"v_accvgpr_mov_b32 a168, a72\n"
"v_mfma_f32_16x16x32_bf16 v[152:155], v[212:215], a[4:7], v[152:155]\n"
"v_accvgpr_mov_b32 a189, a93\n"
"v_sub_f32 v187, s74, v187\n"
"v_mfma_f32_16x16x32_bf16 v[152:155], v[216:219], a[8:11], v[152:155]\n"
"s_add_u32 s40, s56, s54\n"
"v_accvgpr_mov_b32 a175, a79\n"
"v_accvgpr_mov_b32 a188, a92\n"
"v_mfma_f32_16x16x32_bf16 v[152:155], v[220:223], a[12:15], v[152:155]\n"
"buffer_load_dwordx4 v240, s[20:23], s40 offen offset:3072 nt lds\n"
"v_mov_b32_dpp v247, v247 row_ror:4 row_mask:0x8 bank_mask:0xf\n"
"v_accvgpr_mov_b32 a169, a73\n"
"v_accvgpr_mov_b32 a170, a74\n"
"v_accvgpr_mov_b32 a171, a75\n"
"v_sub_f32 v189, s74, v189\n"
"v_sub_f32 v190, s74, v190\n"
"v_sub_f32 v191, s74, v191\n"
"v_sub_f32 v192, s74, v192\n"
"v_mfma_f32_16x16x32_bf16 v[156:159], v[216:219], a[24:27], v[184:187]\n"
"s_add_u32 s55, s55, s59\n"
"v_accvgpr_mov_b32 a191, a95\n"
"v_accvgpr_mov_b32 a163, a67\n"
"v_mfma_f32_16x16x32_bf16 v[156:159], v[220:223], a[28:31], v[156:159]\n"
"s_or_b32 s55, s55, s58\n"
"v_accvgpr_mov_b32 a149, a53\n"
"v_sub_f32 v188, s74, v188\n"
"v_mfma_f32_16x16x32_bf16 v[156:159], v[208:211], a[16:19], v[156:159]\n"
"s_xor_b32 s56, s55, s57\n"
"v_exp_f32 v153, v153\n"
"v_mfma_f32_16x16x32_bf16 v[156:159], v[212:215], a[20:23], v[156:159]\n"
"ds_write_b64 v250, v[252:253] offset:52224\n"
"v_accvgpr_mov_b32 a148, a52\n"
"v_accvgpr_mov_b32 a183, a87\n"
"v_accvgpr_mov_b32 a146, a50\n"
"v_sub_f32 v193, s74, v193\n"
"v_sub_f32 v194, s74, v194\n"
"v_exp_f32 v152, v152\n"
"v_mfma_f32_16x16x32_bf16 v[160:163], v[208:211], a[32:35], v[188:191]\n"
"v_accvgpr_mov_b32 a158, a62\n"
"v_accvgpr_mov_b32 a144, a48\n"
"v_mfma_f32_16x16x32_bf16 v[160:163], v[212:215], a[36:39], v[160:163]\n"
"v_rcp_f32 v206, v200\n"
"v_mfma_f32_16x16x32_bf16 v[160:163], v[216:219], a[40:43], v[160:163]\n"
"v_accvgpr_mov_b32 a190, a94\n"
"v_sub_f32 v195, s74, v195\n"
"v_mfma_f32_16x16x32_bf16 v[160:163], v[220:223], a[44:47], v[160:163]\n"
"ds_write_b128 v244, a[216:219] offset:32768\n"
"v_accvgpr_mov_b32 a150, a54\n"
"v_accvgpr_mov_b32 a156, a60\n"
"v_accvgpr_mov_b32 a147, a51\n"
"v_accvgpr_mov_b32 a177, a81\n"
"v_mul_f32_dpp v25, v246, v25 row_newbcast:1 row_mask:0xf bank_mask:0xf\n"
"v_mfma_f32_16x16x32_bf16 v[164:167], v[216:219], a[200:203], v[192:195]\n"
"v_mov_b32_dpp v206, v206 row_ror:12 row_mask:0x2 bank_mask:0xf\n"
"v_accvgpr_mov_b32 a167, a71\n"
"v_mfma_f32_16x16x32_bf16 v[164:167], v[220:223], a[204:207], v[164:167]\n"
"v_accvgpr_mov_b32 a179, a83\n"
"v_accvgpr_mov_b32 a187, a91\n"
"v_mfma_f32_16x16x32_bf16 v[164:167], v[208:211], a[192:195], v[164:167]\n"
"ds_read_b128 v[208:211], v228 offset:4096\n"
"v_accvgpr_mov_b32 a154, a58\n"
"v_mfma_f32_16x16x32_bf16 v[164:167], v[212:215], a[196:199], v[164:167]\n"
"s_waitcnt lgkmcnt(2)\n"
"ds_read_b128 v[216:219], v228 offset:5120\n"
"ds_read_b128 v[220:223], v229 offset:5120\n"
"v_mov_b32_dpp v206, v206 row_ror:8 row_mask:0x4 bank_mask:0xf\n"
"v_accvgpr_mov_b32 a182, a86\n"
"v_accvgpr_mov_b32 a152, a56\n"
"v_exp_f32 v155, v155\n"
"v_exp_f32 v160, v160\n"
"LTC_%=:\n"
"s_waitcnt lgkmcnt(4)\n"
"v_mfma_f32_16x16x32_bf16 v[92:95], v[116:119], v[224:227], v[92:95]\n"
"v_exp_f32 v154, v154\n"
"v_mfma_f32_16x16x32_bf16 v[92:95], v[112:115], a[252:255], v[92:95]\n"
"ds_read_b128 v[212:215], v229 offset:4096\n"
"v_mul_f32_dpp v24, v246, v24 row_newbcast:0 row_mask:0xf bank_mask:0xf\n"
"v_mfma_f32_16x16x32_bf16 v[60:63], v[108:111], v[224:227], v[60:63]\n"
"s_add_u32 m0, s44, 32768\n"
"v_exp_f32 v163, v163\n"
"v_mfma_f32_16x16x32_bf16 v[60:63], v[104:107], a[252:255], v[60:63]\n"
"buffer_load_dwordx4 v240, s[16:19], s56 offen nt lds\n"
"v_mul_f32_dpp v8, v246, v8 row_newbcast:0 row_mask:0xf bank_mask:0xf\n"
"v_exp_f32 v156, v156\n"
"v_exp_f32 v158, v158\n"
"v_exp_f32 v161, v161\n"
"v_exp_f32 v165, v165\n"
"v_mfma_f32_16x16x32_bf16 v[28:31], v[100:103], v[224:227], v[28:31]\n"
"v_cvt_pk_bf16_f32 v128, v152, v153\n"
"v_cvt_pk_bf16_f32 v129, v154, v155\n"
"v_mfma_f32_16x16x32_bf16 v[28:31], v[96:99], a[252:255], v[28:31]\n"
"v_mov_b32_dpp v206, v206 row_ror:4 row_mask:0x8 bank_mask:0xf\n"
"v_mul_f32_dpp v9, v246, v9 row_newbcast:1 row_mask:0xf bank_mask:0xf\n"
"v_mfma_f32_16x16x32_bf16 a[236:239], v[120:123], a[252:255], a[236:239]\n"
"s_waitcnt lgkmcnt(2)\n"
"v_exp_f32 v162, v162\n"
"v_mfma_f32_16x16x32_bf16 v[168:171], v[216:219], a[8:11], v[180:183]\n"
"s_waitcnt lgkmcnt(1)\n"
"v_exp_f32 v167, v167\n"
"v_mfma_f32_16x16x32_bf16 v[168:171], v[220:223], a[12:15], v[168:171]\n"
"v_mul_f32_dpp v12, v246, v12 row_newbcast:0 row_mask:0xf bank_mask:0xf\n"
"v_mul_f32_dpp v46, v247, v46 row_newbcast:2 row_mask:0xf bank_mask:0xf\n"
"v_mfma_f32_16x16x32_bf16 v[168:171], v[208:211], a[0:3], v[168:171]\n"
"s_waitcnt lgkmcnt(0)\n"
"ds_read_b64_tr_b16 a[244:245], v232 offset:49152\n"
"v_cvt_pk_bf16_f32 v145, v162, v163\n"
"v_mfma_f32_16x16x32_bf16 v[168:171], v[212:215], a[4:7], v[168:171]\n"
"v_exp_f32 v159, v159\n"
"v_mfma_f32_16x16x32_bf16 v[172:175], v[208:211], a[16:19], v[184:187]\n"
"ds_read_b64_tr_b16 a[252:253], v233 offset:49152\n"
"v_mul_f32_dpp v72, v206, v72 row_newbcast:0 row_mask:0xf bank_mask:0xf\n"
"v_mfma_f32_16x16x32_bf16 v[172:175], v[212:215], a[20:23], v[172:175]\n"
"v_exp_f32 v164, v164\n"
"v_mfma_f32_16x16x32_bf16 v[172:175], v[216:219], a[24:27], v[172:175]\n"
"ds_read_b64_tr_b16 a[250:251], v236 offset:53248\n"
"v_mul_f32_dpp v14, v246, v14 row_newbcast:2 row_mask:0xf bank_mask:0xf\n"
"v_mfma_f32_16x16x32_bf16 v[172:175], v[220:223], a[28:31], v[172:175]\n"
"ds_write_b128 v244, a[220:223] offset:33792\n"
"v_mul_f32_dpp v78, v206, v78 row_newbcast:2 row_mask:0xf bank_mask:0xf\n"
"v_exp_f32 v157, v157\n"
"v_mul_f32_dpp v89, v206, v89 row_newbcast:1 row_mask:0xf bank_mask:0xf\n"
"v_exp_f32 v168, v168\n"
"v_mfma_f32_16x16x32_bf16 v[176:179], v[216:219], a[40:43], v[188:191]\n"
"v_mul_f32_dpp v10, v246, v10 row_newbcast:2 row_mask:0xf bank_mask:0xf\n"
"v_mul_f32_dpp v40, v247, v40 row_newbcast:0 row_mask:0xf bank_mask:0xf\n"
"v_mfma_f32_16x16x32_bf16 v[176:179], v[220:223], a[44:47], v[176:179]\n"
"v_cvt_pk_bf16_f32 v137, v158, v159\n"
"v_mul_f32_dpp v28, v246, v28 row_newbcast:0 row_mask:0xf bank_mask:0xf\n"
"v_mfma_f32_16x16x32_bf16 v[176:179], v[208:211], a[32:35], v[176:179]\n"
"s_add_u32 s40, s56, s52\n"
"v_mul_f32_dpp v73, v206, v73 row_newbcast:1 row_mask:0xf bank_mask:0xf\n"
"v_cvt_pk_bf16_f32 v136, v156, v157\n"
"v_mfma_f32_16x16x32_bf16 v[176:179], v[212:215], a[36:39], v[176:179]\n"
"buffer_load_dwordx4 v240, s[16:19], s40 offen offset:1024 nt lds\n"
"v_mfma_f32_16x16x32_bf16 v[152:155], v[208:211], a[192:195], v[192:195]\n"
"v_mul_f32_dpp v76, v206, v76 row_newbcast:0 row_mask:0xf bank_mask:0xf\n"
"v_mul_f32_dpp v88, v206, v88 row_newbcast:0 row_mask:0xf bank_mask:0xf\n"
"v_mfma_f32_16x16x32_bf16 v[152:155], v[212:215], a[196:199], v[152:155]\n"
"ds_read_b128 v[208:211], v230 offset:0\n"
"v_cvt_pk_bf16_f32 v144, v160, v161\n"
"v_mfma_f32_16x16x32_bf16 v[152:155], v[216:219], a[200:203], v[152:155]\n"
"v_mul_f32_dpp v93, v206, v93 row_newbcast:1 row_mask:0xf bank_mask:0xf\n"
"v_cvt_pk_bf16_f32 v88, v88, v89\n"
"v_mfma_f32_16x16x32_bf16 v[152:155], v[220:223], a[204:207], v[152:155]\n"
"s_waitcnt lgkmcnt(1)\n"
"ds_read_b128 v[212:215], v231 offset:0\n"
"v_mul_f32_dpp v13, v246, v13 row_newbcast:1 row_mask:0xf bank_mask:0xf\n"
"v_mfma_f32_16x16x32_bf16 a[208:211], v[120:123], a[244:247], a[208:211]\n"
"v_exp_f32 v170, v170\n"
"v_mfma_f32_16x16x32_bf16 v[0:3], v[96:99], a[244:247], v[0:3]\n"
"ds_read_b128 v[216:219], v230 offset:1024\n"
"v_cvt_pk_bf16_f32 v124, v164, v165\n"
"v_mfma_f32_16x16x32_bf16 v[0:3], v[100:103], a[248:251], v[0:3]\n"
"ds_read_b64_tr_b16 v[226:227], v237 offset:53248\n"
"v_mul_f32_dpp v15, v246, v15 row_newbcast:3 row_mask:0xf bank_mask:0xf\n"
"v_mul_f32_dpp v41, v247, v41 row_newbcast:1 row_mask:0xf bank_mask:0xf\n"
"v_mul_f32_dpp v42, v247, v42 row_newbcast:2 row_mask:0xf bank_mask:0xf\n"
"v_exp_f32 v166, v166\n"
"v_mul_f32_dpp v57, v247, v57 row_newbcast:1 row_mask:0xf bank_mask:0xf\n"
"v_mul_f32_dpp v91, v206, v91 row_newbcast:3 row_mask:0xf bank_mask:0xf\n"
"v_exp_f32 v172, v172\n"
"v_exp_f32 v177, v177\n"
"v_mfma_f32_16x16x32_bf16 v[32:35], v[104:107], a[244:247], v[32:35]\n"
"ds_read_b64_tr_b16 v[224:225], v237 offset:49152\n"
"ds_read_b128 v[220:223], v231 offset:1024\n"
"v_mfma_f32_16x16x32_bf16 v[32:35], v[108:111], a[248:251], v[32:35]\n"
"ds_write_b128 v244, a[232:235] offset:34816\n"
"v_cvt_pk_bf16_f32 v8, v8, v9\n"
"v_mul_f32_dpp v75, v206, v75 row_newbcast:3 row_mask:0xf bank_mask:0xf\n"
"v_mul_f32_dpp v30, v246, v30 row_newbcast:2 row_mask:0xf bank_mask:0xf\n"
"v_mul_f32_dpp v94, v206, v94 row_newbcast:2 row_mask:0xf bank_mask:0xf\n"
"v_exp_f32 v176, v176\n"
"v_mfma_f32_16x16x32_bf16 v[64:67], v[112:115], a[244:247], v[64:67]\n"
"v_exp_f32 v173, v173\n"
"v_mfma_f32_16x16x32_bf16 v[64:67], v[116:119], a[248:251], v[64:67]\n"
"s_waitcnt lgkmcnt(6)\n"
"ds_write_b128 v244, a[236:239] offset:35840\n"
"v_mul_f32_dpp v77, v206, v77 row_newbcast:1 row_mask:0xf bank_mask:0xf\n"
"v_mul_f32_dpp v62, v247, v62 row_newbcast:2 row_mask:0xf bank_mask:0xf\n"
"v_exp_f32 v169, v169\n"
"v_cvt_pk_bf16_f32 v138, v172, v173\n"
"v_exp_f32 v153, v153\n"
"v_mfma_f32_16x16x32_bf16 v[156:159], v[208:211], a[0:3], v[180:183]\n"
"s_waitcnt lgkmcnt(6)\n"
"ds_read_b64_tr_b16 a[254:255], v233 offset:53248\n"
"v_mul_f32_dpp v63, v247, v63 row_newbcast:3 row_mask:0xf bank_mask:0xf\n"
"v_mfma_f32_16x16x32_bf16 v[156:159], v[212:215], a[4:7], v[156:159]\n"
"s_waitcnt lgkmcnt(6)\n"
"v_exp_f32 v174, v174\n"
"v_mfma_f32_16x16x32_bf16 v[156:159], v[216:219], a[8:11], v[156:159]\n"
"s_waitcnt lgkmcnt(3)\n"
"s_add_u32 s40, s56, s53\n"
"v_cvt_pk_bf16_f32 v40, v40, v41\n"
"v_mul_f32_dpp v0, v246, v0 row_newbcast:0 row_mask:0xf bank_mask:0xf\n"
"v_mfma_f32_16x16x32_bf16 v[156:159], v[220:223], a[12:15], v[156:159]\n"
"buffer_load_dwordx4 v240, s[16:19], s40 offen offset:2048 nt lds\n"
"v_cvt_pk_bf16_f32 v9, v10, v11\n"
"v_mul_f32_dpp v44, v247, v44 row_newbcast:0 row_mask:0xf bank_mask:0xf\n"
"v_cvt_pk_bf16_f32 v125, v166, v167\n"
"v_mul_f32_dpp v31, v246, v31 row_newbcast:3 row_mask:0xf bank_mask:0xf\n"
"v_exp_f32 v152, v152\n"
"v_mul_f32_dpp v34, v247, v34 row_newbcast:2 row_mask:0xf bank_mask:0xf\n"
"v_mov_b64 v[200:201], 0\n"
"v_mfma_f32_16x16x32_bf16 v[160:163], v[216:219], a[24:27], v[184:187]\n"
"v_mul_f32_dpp v60, v247, v60 row_newbcast:0 row_mask:0xf bank_mask:0xf\n"
"v_xor_b32 v244, 0x1000, v244\n"
"v_mfma_f32_16x16x32_bf16 v[160:163], v[220:223], a[28:31], v[160:163]\n"
"v_exp_f32 v178, v178\n"
"v_mfma_f32_16x16x32_bf16 v[160:163], v[208:211], a[16:19], v[160:163]\n"
"ds_read_b64_tr_b16 a[250:251], v236 offset:54272\n"
"v_mul_f32_dpp v33, v247, v33 row_newbcast:1 row_mask:0xf bank_mask:0xf\n"
"v_mfma_f32_16x16x32_bf16 v[160:163], v[212:215], a[20:23], v[160:163]\n"
"ds_read_b64_tr_b16 a[244:245], v232 offset:50176\n"
"v_mul_f32_dpp v43, v247, v43 row_newbcast:3 row_mask:0xf bank_mask:0xf\n"
"v_mfma_f32_16x16x32_bf16 v[164:167], v[208:211], a[32:35], v[188:191]\n"
"v_mul_f32_dpp v45, v247, v45 row_newbcast:1 row_mask:0xf bank_mask:0xf\n"
"v_cvt_pk_bf16_f32 v72, v72, v73\n"
"v_mfma_f32_16x16x32_bf16 v[164:167], v[212:215], a[36:39], v[164:167]\n"
"ds_read_b128 v[208:211], v230 offset:4096\n"
"v_mul_f32_dpp v74, v206, v74 row_newbcast:2 row_mask:0xf bank_mask:0xf\n"
"v_mfma_f32_16x16x32_bf16 v[164:167], v[216:219], a[40:43], v[164:167]\n"
"v_mul_f32_dpp v56, v247, v56 row_newbcast:0 row_mask:0xf bank_mask:0xf\n"
"v_mul_f32_dpp v58, v247, v58 row_newbcast:2 row_mask:0xf bank_mask:0xf\n"
"v_mfma_f32_16x16x32_bf16 v[164:167], v[220:223], a[44:47], v[164:167]\n"
"ds_read_b128 v[212:215], v231 offset:4096\n"
"ds_read_b128 v[220:223], v231 offset:5120\n"
"v_mul_f32_dpp v47, v247, v47 row_newbcast:3 row_mask:0xf bank_mask:0xf\n"
"v_cvt_pk_bf16_f32 v73, v74, v75\n"
"v_mul_f32_dpp v29, v246, v29 row_newbcast:1 row_mask:0xf bank_mask:0xf\n"
"v_exp_f32 v175, v175\n"
"v_exp_f32 v179, v179\n"
"v_mul_f32_dpp v1, v246, v1 row_newbcast:1 row_mask:0xf bank_mask:0xf\n"
"v_mul_f32_dpp v2, v246, v2 row_newbcast:2 row_mask:0xf bank_mask:0xf\n"
"v_mfma_f32_16x16x32_bf16 v[68:71], v[116:119], v[224:227], v[68:71]\n"
"s_waitcnt lgkmcnt(5)\n"
"v_exp_f32 v171, v171\n"
"v_mfma_f32_16x16x32_bf16 v[68:71], v[112:115], a[252:255], v[68:71]\n"
"ds_read_b128 v[216:219], v230 offset:5120\n"
"v_mul_f32_dpp v27, v246, v27 row_newbcast:3 row_mask:0xf bank_mask:0xf\n"
"v_mfma_f32_16x16x32_bf16 v[36:39], v[108:111], v[224:227], v[36:39]\n"
"s_add_u32 s40, s56, s54\n"
"v_cvt_pk_bf16_f32 v24, v24, v25\n"
"v_mul_f32_dpp v59, v247, v59 row_newbcast:3 row_mask:0xf bank_mask:0xf\n"
"v_mfma_f32_16x16x32_bf16 v[36:39], v[104:107], a[252:255], v[36:39]\n"
"buffer_load_dwordx4 v240, s[16:19], s40 offen offset:3072 nt lds\n"
"v_cvt_pk_bf16_f32 v74, v76, v77\n"
"v_cvt_pk_bf16_f32 v25, v26, v27\n"
"v_cvt_pk_bf16_f32 v130, v168, v169\n"
"v_exp_f32 v154, v154\n"
"v_exp_f32 v162, v162\n"
"v_mul_f32_dpp v3, v246, v3 row_newbcast:3 row_mask:0xf bank_mask:0xf\n"
"v_mfma_f32_16x16x32_bf16 v[4:7], v[100:103], v[224:227], v[4:7]\n"
"v_cvt_pk_bf16_f32 v10, v12, v13\n"
"v_cvt_pk_bf16_f32 v0, v0, v1\n"
"v_mfma_f32_16x16x32_bf16 v[4:7], v[96:99], a[252:255], v[4:7]\n"
"v_mul_f32_dpp v90, v206, v90 row_newbcast:2 row_mask:0xf bank_mask:0xf\n"
"v_mul_f32_dpp v92, v206, v92 row_newbcast:0 row_mask:0xf bank_mask:0xf\n"
"v_mfma_f32_16x16x32_bf16 a[212:215], v[120:123], a[252:255], a[212:215]\n"
"s_waitcnt lgkmcnt(3)\n"
"ds_read_b64_tr_b16 a[246:247], v232 offset:54272\n"
"ds_read_b64_tr_b16 v[224:225], v237 offset:50176\n"
"v_cvt_pk_bf16_f32 v11, v14, v15\n"
"v_cvt_pk_bf16_f32 v41, v42, v43\n"
"v_cvt_pk_bf16_f32 v43, v46, v47\n"
"v_cvt_pk_bf16_f32 v56, v56, v57\n"
"v_cvt_pk_bf16_f32 v131, v170, v171\n"
"v_cvt_pk_bf16_f32 v146, v176, v177\n"
"v_exp_f32 v167, v167\n"
"v_mfma_f32_16x16x32_bf16 v[168:171], v[208:211], a[0:3], v[180:183]\n"
"s_waitcnt lgkmcnt(3)\n"
"v_exp_f32 v159, v159\n"
"v_mfma_f32_16x16x32_bf16 v[168:171], v[212:215], a[4:7], v[168:171]\n"
"s_waitcnt lgkmcnt(2)\n"
"ds_read_b64_tr_b16 a[248:249], v236 offset:50176\n"
"ds_read_b64_tr_b16 a[254:255], v233 offset:54272\n"
"v_mfma_f32_16x16x32_bf16 v[168:171], v[216:219], a[8:11], v[168:171]\n"
"v_cvt_pk_bf16_f32 v89, v90, v91\n"
"v_cvt_pk_bf16_f32 v139, v174, v175\n"
"v_mfma_f32_16x16x32_bf16 v[168:171], v[220:223], a[12:15], v[168:171]\n"
"ds_write_b128 v248, v[8:11] offset:49152\n"
"v_mul_f32_dpp v79, v206, v79 row_newbcast:3 row_mask:0xf bank_mask:0xf\n"
"v_cvt_pk_bf16_f32 v147, v178, v179\n"
"v_exp_f32 v165, v165\n"
"v_mul_f32_dpp v6, v246, v6 row_newbcast:2 row_mask:0xf bank_mask:0xf\n"
"v_cvt_pk_bf16_f32 v1, v2, v3\n"
"v_mfma_f32_16x16x32_bf16 v[172:175], v[216:219], a[24:27], v[184:187]\n"
"ds_read_b64_tr_b16 v[226:227], v237 offset:54272\n"
"v_mul_f32_dpp v32, v247, v32 row_newbcast:0 row_mask:0xf bank_mask:0xf\n"
"v_mfma_f32_16x16x32_bf16 v[172:175], v[220:223], a[28:31], v[172:175]\n"
"v_mul_f32_dpp v39, v247, v39 row_newbcast:3 row_mask:0xf bank_mask:0xf\n"
"v_mul_f32_dpp v68, v206, v68 row_newbcast:0 row_mask:0xf bank_mask:0xf\n"
"v_mfma_f32_16x16x32_bf16 v[172:175], v[208:211], a[16:19], v[172:175]\n"
"ds_read_b64_tr_b16 a[252:253], v233 offset:50176\n"
"v_cvt_pk_bf16_f32 v75, v78, v79\n"
"v_mfma_f32_16x16x32_bf16 v[172:175], v[212:215], a[20:23], v[172:175]\n"
"s_cbranch_vccnz LPCT0_%=\nLPKT0_%=:\n"
"ds_read_b64_tr_b16 a[54:55], v249 offset:49160\n"
"v_mfma_f32_16x16x32_bf16 v[176:179], v[208:211], a[32:35], v[188:191]\n"
"v_cvt_pk_bf16_f32 v42, v44, v45\n"
"v_mul_f32_dpp v61, v247, v61 row_newbcast:1 row_mask:0xf bank_mask:0xf\n"
"v_mfma_f32_16x16x32_bf16 v[176:179], v[212:215], a[36:39], v[176:179]\n"
"ds_read_b64_tr_b16 a[52:53], v249 offset:49152\n"
"v_cvt_pk_bf16_f32 v57, v58, v59\n"
"v_mfma_f32_16x16x32_bf16 v[176:179], v[216:219], a[40:43], v[176:179]\n"
"v_exp_f32 v155, v155\n"
"v_mfma_f32_16x16x32_bf16 v[176:179], v[220:223], a[44:47], v[176:179]\n"
"s_waitcnt lgkmcnt(4)\n"
"ds_write_b128 v248, v[40:43] offset:50176\n"
"v_cvt_pk_bf16_f32 v27, v30, v31\n"
"v_cvt_pk_bf16_f32 v90, v92, v93\n"
"v_cvt_pk_bf16_f32 v126, v152, v153\n"
"v_mul_f32_dpp v7, v246, v7 row_newbcast:3 row_mask:0xf bank_mask:0xf\n"
"v_mul_f32_dpp v65, v206, v65 row_newbcast:1 row_mask:0xf bank_mask:0xf\n"
"v_mul_f32_dpp v67, v206, v67 row_newbcast:3 row_mask:0xf bank_mask:0xf\n"
"v_mul_f32_dpp v70, v206, v70 row_newbcast:2 row_mask:0xf bank_mask:0xf\n"
"v_mul_f32_dpp v71, v206, v71 row_newbcast:3 row_mask:0xf bank_mask:0xf\n"
"v_mfma_f32_16x16x32_bf16 a[224:227], v[120:123], a[244:247], a[224:227]\n"
"ds_write_b128 v248, v[72:75] offset:51200\n"
"v_cvt_pk_bf16_f32 v26, v28, v29\n"
"v_exp_f32 v166, v166\n"
"v_cvt_pk_bf16_f32 v3, v6, v7\n"
"v_mul_f32_dpp v66, v206, v66 row_newbcast:2 row_mask:0xf bank_mask:0xf\n"
"v_mfma_f32_16x16x32_bf16 v[16:19], v[96:99], a[244:247], v[16:19]\n"
"s_waitcnt vmcnt(8) lgkmcnt(4)\n"
"ds_read_b64_tr_b16 a[68:69], v249 offset:50176\n"
"v_mul_f32_dpp v69, v206, v69 row_newbcast:1 row_mask:0xf bank_mask:0xf\n"
"v_mfma_f32_16x16x32_bf16 v[16:19], v[100:103], a[248:251], v[16:19]\n"
"s_barrier\n"
"ds_read_b64_tr_b16 a[70:71], v249 offset:50184\n"
"ds_read_b128 v[212:215], v229 offset:16384\n"
"ds_read_b64 v[250:251], v245 offset:52224\n"
"v_mul_f32_dpp v5, v246, v5 row_newbcast:1 row_mask:0xf bank_mask:0xf\n"
"v_mul_f32_dpp v35, v247, v35 row_newbcast:3 row_mask:0xf bank_mask:0xf\n"
"v_exp_f32 v173, v173\n"
"v_exp_f32 v175, v175\n"
"v_mfma_f32_16x16x32_bf16 v[48:51], v[104:107], a[244:247], v[48:51]\n"
"v_cvt_pk_bf16_f32 v59, v62, v63\n"
"v_mul_f32_dpp v38, v247, v38 row_newbcast:2 row_mask:0xf bank_mask:0xf\n"
"v_mfma_f32_16x16x32_bf16 v[48:51], v[108:111], a[248:251], v[48:51]\n"
"ds_read_b128 v[208:211], v228 offset:16384\n"
"ds_read_b128 v[220:223], v229 offset:17408\n"
"v_mul_f32_dpp v95, v206, v95 row_newbcast:3 row_mask:0xf bank_mask:0xf\n"
"v_exp_f32 v161, v161\n"
"v_cvt_pk_bf16_f32 v149, v166, v167\n"
"v_cvt_pk_bf16_f32 v32, v32, v33\n"
"v_exp_f32 v170, v170\n"
"v_exp_f32 v172, v172\n"
"v_mfma_f32_16x16x32_bf16 v[80:83], v[112:115], a[244:247], v[80:83]\n"
"ds_read_b64_tr_b16 a[84:85], v249 offset:51200\n"
"ds_read_b64_tr_b16 a[86:87], v249 offset:51208\n"
"v_mfma_f32_16x16x32_bf16 v[80:83], v[116:119], a[248:251], v[80:83]\n"
"ds_read_b128 v[28:31], v244 offset:33792\n"
"v_mov_b64 v[198:199], 0\n"
"v_mfma_f32_16x16x32_bf16 v[84:87], v[116:119], v[224:227], v[84:87]\n"
"ds_write_b128 v248, v[24:27] offset:49152\n"
"v_cvt_pk_bf16_f32 v91, v94, v95\n"
"v_exp_f32 v156, v156\n"
"v_exp_f32 v157, v157\n"
"v_mul_f32_dpp v37, v247, v37 row_newbcast:1 row_mask:0xf bank_mask:0xf\n"
"v_mfma_f32_16x16x32_bf16 v[84:87], v[112:115], a[252:255], v[84:87]\n"
"ds_read_b64_tr_b16 a[60:61], v249 offset:49152\n"
"ds_read_b128 v[216:219], v228 offset:17408\n"
"v_cvt_pk_bf16_f32 v127, v154, v155\n"
"v_exp_f32 v160, v160\n"
"v_exp_f32 v164, v164\n"
"v_mul_f32_dpp v36, v247, v36 row_newbcast:0 row_mask:0xf bank_mask:0xf\n"
"v_exp_f32 v176, v176\n"
"v_mfma_f32_16x16x32_bf16 v[52:55], v[108:111], v[224:227], v[52:55]\n"
"ds_write_b128 v248, v[88:91] offset:51200\n"
"v_cvt_pk_bf16_f32 v58, v60, v61\n"
"v_exp_f32 v163, v163\n"
"v_cvt_pk_bf16_f32 v33, v34, v35\n"
"v_exp_f32 v169, v169\n"
"v_mfma_f32_16x16x32_bf16 v[52:55], v[104:107], a[252:255], v[52:55]\n"
"s_waitcnt lgkmcnt(9)\n"
"ds_write_b128 v248, v[56:59] offset:50176\n"
"v_mul_f32_dpp v4, v246, v4 row_newbcast:0 row_mask:0xf bank_mask:0xf\n"
"v_exp_f32 v168, v168\n"
"v_min_f32 v254, v252, v250\n"
"v_sub_f32 v252, v254, v252\n"
"v_mfma_f32_16x16x32_bf16 v[20:23], v[100:103], v[224:227], v[20:23]\n"
"ds_read_b64_tr_b16 a[92:93], v249 offset:51200\n"
"ds_read_b64_tr_b16 a[244:245], v234 offset:0\n"
"v_mfma_f32_16x16x32_bf16 v[20:23], v[96:99], a[252:255], v[20:23]\n"
"ds_read_b64_tr_b16 a[62:63], v249 offset:49160\n"
"ds_read_b64_tr_b16 a[78:79], v249 offset:50184\n"
"ds_read_b64_tr_b16 a[94:95], v249 offset:51208\n"
"v_cvt_pk_bf16_f32 v2, v4, v5\n"
"v_mul_f32_dpp v64, v206, v64 row_newbcast:0 row_mask:0xf bank_mask:0xf\n"
"v_cvt_pk_bf16_f32 v64, v64, v65\n"
"v_cvt_pk_bf16_f32 v65, v66, v67\n"
"v_cvt_pk_bf16_f32 v66, v68, v69\n"
"v_cvt_pk_bf16_f32 v67, v70, v71\n"
"v_exp_f32 v177, v177\n"
"v_mfma_f32_16x16x32_bf16 a[228:231], v[120:123], a[252:255], a[228:231]\n"
"s_waitcnt lgkmcnt(11)\n"
"ds_write_b128 v248, v[64:67] offset:51200\n"
"v_cvt_pk_bf16_f32 v132, v156, v157\n"
"v_cvt_pk_bf16_f32 v34, v36, v37\n"
"v_cvt_pk_bf16_f32 v35, v38, v39\n"
"v_exp_f32 v171, v171\n"
"v_exp_f32 v179, v179\n"
"v_sub_f32 v250, v254, v250\n"
"v_mul_f32_dpp v83, v206, v83 row_newbcast:3 row_mask:0xf bank_mask:0xf\n"
"v_mfma_f32_16x16x32_bf16 v[152:155], v[208:211], a[0:3], v[180:183]\n"
"ds_read_b64_tr_b16 a[76:77], v249 offset:50176\n"
"ds_read_b64_tr_b16 a[80:81], v249 offset:51200\n"
"v_mfma_f32_16x16x32_bf16 v[152:155], v[212:215], a[4:7], v[152:155]\n"
"s_waitcnt lgkmcnt(10)\n"
"ds_write_b128 v248, v[0:3] offset:49152\n"
"v_exp_f32 v158, v158\n"
"v_cvt_pk_bf16_f32 v140, v160, v161\n"
"v_cvt_pk_bf16_f32 v142, v172, v173\n"
"v_mul_f32_dpp v18, v246, v18 row_newbcast:2 row_mask:0xf bank_mask:0xf\n"
"v_mfma_f32_16x16x32_bf16 v[152:155], v[216:219], a[8:11], v[152:155]\n"
"ds_write_b128 v248, v[32:35] offset:50176\n"
"v_cvt_pk_bf16_f32 v141, v162, v163\n"
"v_exp_f32 v178, v178\n"
"v_exp_f32 v250, v250\n"
"v_mfma_f32_16x16x32_bf16 v[152:155], v[220:223], a[12:15], v[152:155]\n"
"ds_read_b64_tr_b16 a[50:51], v249 offset:49160\n"
"ds_read_b64_tr_b16 a[64:65], v249 offset:50176\n"
"ds_read_b128 v[24:27], v244 offset:32768\n"
"v_cvt_pk_bf16_f32 v133, v158, v159\n"
"v_cvt_pk_bf16_f32 v148, v164, v165\n"
"v_cvt_pk_bf16_f32 v150, v176, v177\n"
"v_exp_f32 v252, v252\n"
"v_mul_f32_dpp v19, v246, v19 row_newbcast:3 row_mask:0xf bank_mask:0xf\n"
"v_mul_f32_dpp v22, v246, v22 row_newbcast:2 row_mask:0xf bank_mask:0xf\n"
"v_mfma_f32_16x16x32_bf16 v[156:159], v[216:219], a[24:27], v[184:187]\n"
"ds_read_b64_tr_b16 a[66:67], v249 offset:50184\n"
"ds_read_b64_tr_b16 a[248:249], v238 offset:0\n"
"v_mfma_f32_16x16x32_bf16 v[156:159], v[220:223], a[28:31], v[156:159]\n"
"ds_read_b64_tr_b16 a[48:49], v249 offset:49152\n"
"ds_read_b128 v[60:63], v244 offset:35840\n"
"v_mfma_f32_16x16x32_bf16 v[156:159], v[208:211], a[16:19], v[156:159]\n"
"ds_read_b64_tr_b16 a[82:83], v249 offset:51208\n"
"ds_read_b128 v[56:59], v244 offset:34816\n"
"v_mfma_f32_16x16x32_bf16 v[156:159], v[212:215], a[20:23], v[156:159]\n"
"ds_read_b64_tr_b16 a[246:247], v234 offset:4096\n"
"ds_read_b64_tr_b16 v[226:227], v239 offset:4096\n"
"v_mfma_f32_16x16x32_bf16 v[160:163], v[208:211], a[32:35], v[188:191]\n"
"v_exp_f32 v174, v174\n"
"v_mfma_f32_16x16x32_bf16 v[160:163], v[212:215], a[36:39], v[160:163]\n"
"ds_read_b64_tr_b16 a[250:251], v238 offset:4096\n"
"v_mul_f32_dpp v49, v247, v49 row_newbcast:1 row_mask:0xf bank_mask:0xf\n"
"v_mfma_f32_16x16x32_bf16 v[160:163], v[216:219], a[40:43], v[160:163]\n"
"ds_read_b64_tr_b16 v[224:225], v239 offset:0\n"
"v_mov_b64 v[202:203], 0\n"
"v_mfma_f32_16x16x32_bf16 v[160:163], v[220:223], a[44:47], v[160:163]\n"
"v_exp_f32 v154, v154\n"
"v_mfma_f32_16x16x32_bf16 v[164:167], v[216:219], a[200:203], v[192:195]\n"
"v_cvt_pk_bf16_f32 v151, v178, v179\n"
"v_mul_f32 v253, v253, v252\n"
"v_mfma_f32_16x16x32_bf16 v[164:167], v[220:223], a[204:207], v[164:167]\n"
"v_cvt_pk_bf16_f32 v134, v168, v169\n"
"v_fmac_f32 v253, v251, v250\n"
"v_mfma_f32_16x16x32_bf16 v[164:167], v[208:211], a[192:195], v[164:167]\n"
"ds_read_b64_tr_b16 a[252:253], v235 offset:0\n"
"ds_read_b64_tr_b16 a[254:255], v235 offset:4096\n"
"v_mfma_f32_16x16x32_bf16 v[164:167], v[212:215], a[196:199], v[164:167]\n"
"s_waitcnt lgkmcnt(4)\n"
"v_cvt_pk_bf16_f32 v135, v170, v171\n"
"v_mfma_f32_16x16x32_bf16 a[216:219], v[124:127], a[244:247], 0\n"
"ds_read_b128 v[216:219], v228 offset:21504\n"
"v_cvt_pk_bf16_f32 v143, v174, v175\n"
"v_mfma_f32_16x16x32_bf16 v[8:11], v[128:131], a[244:247], 0\n"
"s_waitcnt lgkmcnt(4)\n"
"s_add_u32 m0, s44, 98304\n"
"v_rcp_f32 v253, v253\n"
"v_mfma_f32_16x16x32_bf16 v[8:11], v[132:135], a[248:251], v[8:11]\n"
"buffer_load_dwordx4 v240, s[20:23], s56 offen nt lds\n"
"v_exp_f32 v152, v152\n"
"v_exp_f32 v155, v155\n"
"v_exp_f32 v158, v158\n"
"v_exp_f32 v167, v167\n"
"v_mfma_f32_16x16x32_bf16 v[40:43], v[136:139], a[244:247], 0\n"
"v_accvgpr_read_b32 v254, a208\n"
"v_mul_f32_dpp v85, v206, v85 row_newbcast:1 row_mask:0xf bank_mask:0xf\n"
"v_mfma_f32_16x16x32_bf16 v[40:43], v[140:143], a[248:251], v[40:43]\n"
"v_mul_f32 v250, v250, v253\n"
"v_accvgpr_read_b32 v251, a211\n"
"v_mfma_f32_16x16x32_bf16 v[72:75], v[144:147], a[244:247], 0\n"
"ds_read_b128 v[220:223], v229 offset:21504\n"
"v_mul_f32_dpp v17, v246, v17 row_newbcast:1 row_mask:0xf bank_mask:0xf\n"
"v_mfma_f32_16x16x32_bf16 v[72:75], v[148:151], a[248:251], v[72:75]\n"
"s_waitcnt lgkmcnt(2)\n"
"v_exp_f32 v164, v164\n"
"v_mfma_f32_16x16x32_bf16 v[76:79], v[148:151], v[224:227], 0\n"
"v_exp_f32 v153, v153\n"
"v_mfma_f32_16x16x32_bf16 v[76:79], v[144:147], a[252:255], v[76:79]\n"
"v_mul_f32 v252, v252, v253\n"
"v_mov_b32_dpp v250, v250 row_ror:12 row_mask:0x2 bank_mask:0xf\n"
"v_mfma_f32_16x16x32_bf16 v[44:47], v[140:143], v[224:227], 0\n"
"ds_read_b64_tr_b16 a[244:245], v234 offset:1024\n"
"v_mov_b32_dpp v252, v252 row_ror:12 row_mask:0x2 bank_mask:0xf\n"
"v_mfma_f32_16x16x32_bf16 v[44:47], v[136:139], a[252:255], v[44:47]\n"
"ds_read_b128 v[212:215], v229 offset:20480\n"
"v_mov_b32_dpp v250, v250 row_ror:8 row_mask:0x4 bank_mask:0xf\n"
"v_mfma_f32_16x16x32_bf16 v[12:15], v[132:135], v[224:227], 0\n"
"s_add_u32 s40, s56, s52\n"
"ds_read_b128 v[208:211], v228 offset:20480\n"
"v_cvt_pk_bf16_f32 v96, v152, v153\n"
"v_mfma_f32_16x16x32_bf16 v[12:15], v[128:131], a[252:255], v[12:15]\n"
"buffer_load_dwordx4 v240, s[20:23], s40 offen offset:1024 nt lds\n"
"v_mov_b32_dpp v252, v252 row_ror:8 row_mask:0x4 bank_mask:0xf\n"
"v_mov_b32_dpp v250, v250 row_ror:4 row_mask:0x8 bank_mask:0xf\n"
"v_accvgpr_read_b32 v255, a209\n"
"v_accvgpr_read_b32 v253, a210\n"
"v_mov_b64 v[196:197], 0\n"
"v_cvt_pk_bf16_f32 v97, v154, v155\n"
"v_exp_f32 v157, v157\n"
"v_mfma_f32_16x16x32_bf16 a[220:223], v[124:127], a[252:255], 0\n"
"v_mov_b32_dpp v252, v252 row_ror:4 row_mask:0x8 bank_mask:0xf\n"
"v_mul_f32_dpp v24, v250, v24 row_newbcast:0 row_mask:0xf bank_mask:0xf\n"
"v_mul_f32_dpp v25, v250, v25 row_newbcast:1 row_mask:0xf bank_mask:0xf\n"
"v_mul_f32_dpp v26, v250, v26 row_newbcast:2 row_mask:0xf bank_mask:0xf\n"
"v_mul_f32_dpp v29, v250, v29 row_newbcast:1 row_mask:0xf bank_mask:0xf\n"
"v_mul_f32_dpp v54, v247, v54 row_newbcast:2 row_mask:0xf bank_mask:0xf\n"
"v_exp_f32 v162, v162\n"
"v_exp_f32 v163, v163\n"
"v_smfmac_f32_16x16x64_bf16 v[196:199], a[240:243], v[128:135], v241\n"
"v_mul_f32_dpp v27, v250, v27 row_newbcast:3 row_mask:0xf bank_mask:0xf\n"
"v_fmac_f32_dpp v24, v252, v254 row_newbcast:0 row_mask:0xf bank_mask:0xf\n"
"v_mfma_f32_16x16x32_bf16 v[202:205], a[240:243], v[124:127], v[202:205]\n"
"s_waitcnt lgkmcnt(3)\n"
"ds_read_b64_tr_b16 a[252:253], v235 offset:1024\n"
"ds_read_b64_tr_b16 a[254:255], v235 offset:5120\n"
"ds_read_b64_tr_b16 v[224:225], v239 offset:1024\n"
"v_fmac_f32_dpp v25, v252, v255 row_newbcast:1 row_mask:0xf bank_mask:0xf\n"
"v_fmac_f32_dpp v26, v252, v253 row_newbcast:2 row_mask:0xf bank_mask:0xf\n"
"v_mul_f32_dpp v28, v250, v28 row_newbcast:0 row_mask:0xf bank_mask:0xf\n"
"v_mul_f32_dpp v30, v250, v30 row_newbcast:2 row_mask:0xf bank_mask:0xf\n"
"v_mul_f32_dpp v50, v247, v50 row_newbcast:2 row_mask:0xf bank_mask:0xf\n"
"v_mul_f32_dpp v58, v250, v58 row_newbcast:2 row_mask:0xf bank_mask:0xf\n"
"v_exp_f32 v156, v156\n"
"v_mfma_f32_16x16x32_bf16 v[168:171], v[216:219], a[8:11], v[180:183]\n"
"v_accvgpr_read_b32 v254, a212\n"
"v_accvgpr_read_b32 v253, a214\n"
"v_mfma_f32_16x16x32_bf16 v[168:171], v[220:223], a[12:15], v[168:171]\n"
"s_waitcnt lgkmcnt(3)\n"
"v_mul_f32_dpp v48, v247, v48 row_newbcast:0 row_mask:0xf bank_mask:0xf\n"
"v_mul_f32_dpp v57, v250, v57 row_newbcast:1 row_mask:0xf bank_mask:0xf\n"
"v_mfma_f32_16x16x32_bf16 v[168:171], v[208:211], a[0:3], v[168:171]\n"
"ds_read_b64_tr_b16 a[250:251], v238 offset:5120\n"
"v_mul_f32_dpp v56, v250, v56 row_newbcast:0 row_mask:0xf bank_mask:0xf\n"
"v_mfma_f32_16x16x32_bf16 v[168:171], v[212:215], a[4:7], v[168:171]\n"
"ds_read_b64_tr_b16 a[248:249], v238 offset:1024\n"
"ds_read_b64_tr_b16 v[226:227], v239 offset:5120\n"
"v_fmac_f32_dpp v27, v252, v251 row_newbcast:3 row_mask:0xf bank_mask:0xf\n"
"v_mul_f32_dpp v31, v250, v31 row_newbcast:3 row_mask:0xf bank_mask:0xf\n"
"v_fmac_f32_dpp v28, v252, v254 row_newbcast:0 row_mask:0xf bank_mask:0xf\n"
"v_fmac_f32_dpp v30, v252, v253 row_newbcast:2 row_mask:0xf bank_mask:0xf\n"
"v_cvt_pk_bf16_f32 v24, v24, v25\n"
"v_cvt_pk_bf16_f32 v25, v26, v27\n"
"v_mul_f32_dpp v52, v247, v52 row_newbcast:0 row_mask:0xf bank_mask:0xf\n"
"v_accvgpr_read_b32 v254, a224\n"
"v_mul_f32_dpp v59, v250, v59 row_newbcast:3 row_mask:0xf bank_mask:0xf\n"
"v_mul_f32_dpp v60, v250, v60 row_newbcast:0 row_mask:0xf bank_mask:0xf\n"
"v_mul_f32_dpp v62, v250, v62 row_newbcast:2 row_mask:0xf bank_mask:0xf\n"
"v_mul_f32_dpp v63, v250, v63 row_newbcast:3 row_mask:0xf bank_mask:0xf\n"
"v_exp_f32 v159, v159\n"
"v_exp_f32 v161, v161\n"
"v_mfma_f32_16x16x32_bf16 v[172:175], v[208:211], a[16:19], v[184:187]\n"
"v_accvgpr_read_b32 v255, a213\n"
"v_accvgpr_read_b32 v251, a215\n"
"v_mfma_f32_16x16x32_bf16 v[172:175], v[212:215], a[20:23], v[172:175]\n"
"ds_read_b64_tr_b16 a[246:247], v234 offset:5120\n"
"v_fmac_f32_dpp v29, v252, v255 row_newbcast:1 row_mask:0xf bank_mask:0xf\n"
"v_mfma_f32_16x16x32_bf16 v[172:175], v[216:219], a[24:27], v[172:175]\n"
"s_add_u32 s40, s56, s53\n"
"v_xor_b32 v249, s75, v249\n"
"v_accvgpr_read_b32 v255, a225\n"
"v_mfma_f32_16x16x32_bf16 v[172:175], v[220:223], a[28:31], v[172:175]\n"
"buffer_load_dwordx4 v240, s[20:23], s40 offen offset:2048 nt lds\n"
"v_fmac_f32_dpp v31, v252, v251 row_newbcast:3 row_mask:0xf bank_mask:0xf\n"
"v_cvt_pk_bf16_f32 v27, v30, v31\n"
"v_mul_f32_dpp v16, v246, v16 row_newbcast:0 row_mask:0xf bank_mask:0xf\n"
"v_mul_f32_dpp v51, v247, v51 row_newbcast:3 row_mask:0xf bank_mask:0xf\n"
"v_accvgpr_read_b32 v253, a226\n"
"v_accvgpr_read_b32 v251, a227\n"
"v_fmac_f32_dpp v56, v252, v254 row_newbcast:0 row_mask:0xf bank_mask:0xf\n"
"v_accvgpr_read_b32 v254, a228\n"
"v_mfma_f32_16x16x32_bf16 v[176:179], v[216:219], a[40:43], v[188:191]\n"
"v_cvt_pk_bf16_f32 v26, v28, v29\n"
"v_fmac_f32_dpp v57, v252, v255 row_newbcast:1 row_mask:0xf bank_mask:0xf\n"
"v_mfma_f32_16x16x32_bf16 v[176:179], v[220:223], a[44:47], v[176:179]\n"
"v_fmac_f32_dpp v58, v252, v253 row_newbcast:2 row_mask:0xf bank_mask:0xf\n"
"v_accvgpr_read_b32 v255, a229\n"
"v_mfma_f32_16x16x32_bf16 v[176:179], v[208:211], a[32:35], v[176:179]\n"
"v_cvt_pk_bf16_f32 v16, v16, v17\n"
"v_mul_f32_dpp v61, v250, v61 row_newbcast:1 row_mask:0xf bank_mask:0xf\n"
"v_mfma_f32_16x16x32_bf16 v[176:179], v[212:215], a[36:39], v[176:179]\n"
"ds_write_b128 v243, v[24:27] offset:0\n"
"v_cvt_pk_bf16_f32 v48, v48, v49\n"
"v_fmac_f32_dpp v59, v252, v251 row_newbcast:3 row_mask:0xf bank_mask:0xf\n"
"v_accvgpr_read_b32 v253, a230\n"
"v_accvgpr_read_b32 v251, a231\n"
"v_fmac_f32_dpp v60, v252, v254 row_newbcast:0 row_mask:0xf bank_mask:0xf\n"
"v_fmac_f32_dpp v61, v252, v255 row_newbcast:1 row_mask:0xf bank_mask:0xf\n"
"v_cvt_pk_bf16_f32 v56, v56, v57\n"
"v_cvt_pk_bf16_f32 v57, v58, v59\n"
"v_cvt_pk_bf16_f32 v58, v60, v61\n"
"v_mfma_f32_16x16x32_bf16 v[152:155], v[208:211], a[192:195], v[192:195]\n"
"v_mul_f32_dpp v81, v206, v81 row_newbcast:1 row_mask:0xf bank_mask:0xf\n"
"v_mul_f32_dpp v82, v206, v82 row_newbcast:2 row_mask:0xf bank_mask:0xf\n"
"v_mul_f32_dpp v87, v206, v87 row_newbcast:3 row_mask:0xf bank_mask:0xf\n"
"v_fmac_f32_dpp v62, v252, v253 row_newbcast:2 row_mask:0xf bank_mask:0xf\n"
"v_fmac_f32_dpp v63, v252, v251 row_newbcast:3 row_mask:0xf bank_mask:0xf\n"
"v_cvt_pk_bf16_f32 v59, v62, v63\n"
"ds_write_b128 v243, v[56:59] offset:2048\n"
"v_mfma_f32_16x16x32_bf16 v[152:155], v[212:215], a[196:199], v[152:155]\n"
"v_exp_f32 v174, v174\n"
"v_mfma_f32_16x16x32_bf16 v[152:155], v[216:219], a[200:203], v[152:155]\n"
"v_exp_f32 v166, v166\n"
"v_mfma_f32_16x16x32_bf16 v[152:155], v[220:223], a[204:207], v[152:155]\n"
"s_waitcnt lgkmcnt(2)\n"
"ds_read_b128 v[212:215], v231 offset:16384\n"
"v_cvt_pk_bf16_f32 v105, v158, v159\n"
"v_mfma_f32_16x16x32_bf16 a[232:235], v[124:127], a[244:247], 0\n"
"v_mul_f32_dpp v20, v246, v20 row_newbcast:0 row_mask:0xf bank_mask:0xf\n"
"v_mul_f32_dpp v23, v246, v23 row_newbcast:3 row_mask:0xf bank_mask:0xf\n"
"v_mfma_f32_16x16x32_bf16 v[24:27], v[128:131], a[244:247], 0\n"
"s_add_u32 s40, s56, s54\n"
"ds_read_b128 v[220:223], v231 offset:17408\n"
"v_mul_f32_dpp v21, v246, v21 row_newbcast:1 row_mask:0xf bank_mask:0xf\n"
"v_mfma_f32_16x16x32_bf16 v[24:27], v[132:135], a[248:251], v[24:27]\n"
"buffer_load_dwordx4 v240, s[20:23], s40 offen offset:3072 nt lds\n"
"v_mfma_f32_16x16x32_bf16 v[56:59], v[136:139], a[244:247], 0\n"
"s_add_u32 s55, s55, s59\n"
"v_cvt_pk_bf16_f32 v17, v18, v19\n"
"v_cvt_pk_bf16_f32 v104, v156, v157\n"
"v_mfma_f32_16x16x32_bf16 v[56:59], v[140:143], a[248:251], v[56:59]\n"
"s_or_b32 s55, s55, s58\n"
"v_exp_f32 v178, v178\n"
"v_mfma_f32_16x16x32_bf16 v[88:91], v[144:147], a[244:247], 0\n"
"s_xor_b32 s56, s55, s57\n"
"ds_read_b128 v[208:211], v230 offset:16384\n"
"v_cvt_pk_bf16_f32 v49, v50, v51\n"
"v_mfma_f32_16x16x32_bf16 v[88:91], v[148:151], a[248:251], v[88:91]\n"
"ds_read_b128 v[216:219], v230 offset:17408\n"
"v_cvt_pk_bf16_f32 v18, v20, v21\n"
"v_mfma_f32_16x16x32_bf16 v[92:95], v[148:151], v[224:227], 0\n"
"v_exp_f32 v160, v160\n"
"v_mfma_f32_16x16x32_bf16 v[92:95], v[144:147], a[252:255], v[92:95]\n"
"v_exp_f32 v165, v165\n"
"v_mfma_f32_16x16x32_bf16 v[60:63], v[140:143], v[224:227], 0\n"
"s_add_u32 m0, s44, 49152\n"
"ds_read_b64_tr_b16 a[248:249], v236 offset:0\n"
"v_mul_f32_dpp v80, v206, v80 row_newbcast:0 row_mask:0xf bank_mask:0xf\n"
"v_mfma_f32_16x16x32_bf16 v[60:63], v[136:139], a[252:255], v[60:63]\n"
"buffer_load_dwordx4 v240, s[16:19], s56 offen nt lds\n"
"v_mfma_f32_16x16x32_bf16 v[28:31], v[132:135], v[224:227], 0\n"
"v_exp_f32 v177, v177\n"
"v_mfma_f32_16x16x32_bf16 v[28:31], v[128:131], a[252:255], v[28:31]\n"
"v_exp_f32 v169, v169\n"
"v_mfma_f32_16x16x32_bf16 a[236:239], v[124:127], a[252:255], 0\n"
"ds_read_b64_tr_b16 a[250:251], v236 offset:4096\n"
"ds_read_b64_tr_b16 v[224:225], v237 offset:0\n"
"v_smfmac_f32_16x16x64_bf16 v[198:201], a[240:243], v[136:143], v241\n"
"s_waitcnt lgkmcnt(3)\n"
"ds_read_b64_tr_b16 a[246:247], v232 offset:4096\n"
"v_cvt_pk_bf16_f32 v112, v160, v161\n"
"v_mfma_f32_16x16x32_bf16 v[156:159], v[208:211], a[0:3], v[180:183]\n"
"v_cvt_pk_bf16_f32 v80, v80, v81\n"
"v_cvt_pk_bf16_f32 v113, v162, v163\n"
"v_mfma_f32_16x16x32_bf16 v[156:159], v[212:215], a[4:7], v[156:159]\n"
"ds_read_b64_tr_b16 a[254:255], v233 offset:4096\n"
"v_xor_b32 v248, s75, v248\n"
"v_mfma_f32_16x16x32_bf16 v[156:159], v[216:219], a[8:11], v[156:159]\n"
"ds_read_b64_tr_b16 a[244:245], v232 offset:0\n"
"v_cvt_pk_bf16_f32 v120, v164, v165\n"
"v_mfma_f32_16x16x32_bf16 v[156:159], v[220:223], a[12:15], v[156:159]\n"
"v_exp_f32 v170, v170\n"
"v_mfma_f32_16x16x32_bf16 v[160:163], v[216:219], a[24:27], v[184:187]\n"
"v_exp_f32 v179, v179\n"
"v_mfma_f32_16x16x32_bf16 v[160:163], v[220:223], a[28:31], v[160:163]\n"
"ds_read_b64_tr_b16 v[226:227], v237 offset:4096\n"
"v_cvt_pk_bf16_f32 v121, v166, v167\n"
"v_mfma_f32_16x16x32_bf16 v[160:163], v[208:211], a[16:19], v[160:163]\n"
"s_add_u32 s40, s56, s52\n"
"v_exp_f32 v168, v168\n"
"v_mfma_f32_16x16x32_bf16 v[160:163], v[212:215], a[20:23], v[160:163]\n"
"buffer_load_dwordx4 v240, s[16:19], s40 offen offset:1024 nt lds\n"
"v_mfma_f32_16x16x32_bf16 v[164:167], v[208:211], a[32:35], v[188:191]\n"
"v_exp_f32 v176, v176\n"
"v_mfma_f32_16x16x32_bf16 v[164:167], v[212:215], a[36:39], v[164:167]\n"
"v_cvt_pk_bf16_f32 v19, v22, v23\n"
"v_mul_f32_dpp v53, v247, v53 row_newbcast:1 row_mask:0xf bank_mask:0xf\n"
"v_mfma_f32_16x16x32_bf16 v[164:167], v[216:219], a[40:43], v[164:167]\n"
"ds_read_b64_tr_b16 a[252:253], v233 offset:0\n"
"ds_read_b128 v[208:211], v230 offset:20480\n"
"v_mfma_f32_16x16x32_bf16 v[164:167], v[220:223], a[44:47], v[164:167]\n"
"s_waitcnt lgkmcnt(3)\n"
"v_exp_f32 v158, v158\n"
"v_mfma_f32_16x16x32_bf16 a[208:211], v[124:127], a[244:247], 0\n"
"ds_read_b128 v[220:223], v231 offset:21504\n"
"v_cvt_pk_bf16_f32 v81, v82, v83\n"
"v_mfma_f32_16x16x32_bf16 v[0:3], v[128:131], a[244:247], 0\n"
"ds_read_b128 v[212:215], v231 offset:20480\n"
"v_mul_f32_dpp v55, v247, v55 row_newbcast:3 row_mask:0xf bank_mask:0xf\n"
"v_mfma_f32_16x16x32_bf16 v[0:3], v[132:135], a[248:251], v[0:3]\n"
"v_exp_f32 v175, v175\n"
"v_mfma_f32_16x16x32_bf16 v[32:35], v[136:139], a[244:247], 0\n"
"s_add_u32 s40, s56, s53\n"
"ds_read_b128 v[216:219], v230 offset:21504\n"
"v_mul_f32_dpp v84, v206, v84 row_newbcast:0 row_mask:0xf bank_mask:0xf\n"
"v_mfma_f32_16x16x32_bf16 v[32:35], v[140:143], a[248:251], v[32:35]\n"
"buffer_load_dwordx4 v240, s[16:19], s40 offen offset:2048 nt lds\n"
"v_mfma_f32_16x16x32_bf16 v[64:67], v[144:147], a[244:247], 0\n"
"v_cvt_pk_bf16_f32 v50, v52, v53\n"
"v_mul_f32_dpp v86, v206, v86 row_newbcast:2 row_mask:0xf bank_mask:0xf\n"
"v_mfma_f32_16x16x32_bf16 v[64:67], v[148:151], a[248:251], v[64:67]\n"
"s_waitcnt lgkmcnt(3)\n"
"ds_write_b128 v248, v[16:19] offset:49152\n"
"v_cvt_pk_bf16_f32 v82, v84, v85\n"
"v_exp_f32 v172, v172\n"
"v_exp_f32 v152, v152\n"
"v_exp_f32 v159, v159\n"
"v_exp_f32 v161, v161\n"
"v_exp_f32 v164, v164\n"
"v_exp_f32 v167, v167\n"
"v_mfma_f32_16x16x32_bf16 v[68:71], v[148:151], v[224:227], 0\n"
"v_cvt_pk_bf16_f32 v51, v54, v55\n"
"v_cvt_pk_bf16_f32 v98, v168, v169\n"
"v_mfma_f32_16x16x32_bf16 v[68:71], v[144:147], a[252:255], v[68:71]\n"
"ds_write_b128 v248, v[48:51] offset:50176\n"
"v_exp_f32 v171, v171\n"
"v_exp_f32 v173, v173\n"
"v_exp_f32 v156, v156\n"
"v_exp_f32 v163, v163\n"
"v_mfma_f32_16x16x32_bf16 v[36:39], v[140:143], v[224:227], 0\n"
"ds_read_b64_tr_b16 a[246:247], v232 offset:5120\n"
"ds_read_b64_tr_b16 a[250:251], v236 offset:5120\n"
"v_mfma_f32_16x16x32_bf16 v[36:39], v[136:139], a[252:255], v[36:39]\n"
"ds_read_b64_tr_b16 a[74:75], v249 offset:50184\n"
"v_cvt_pk_bf16_f32 v107, v174, v175\n"
"v_mfma_f32_16x16x32_bf16 v[4:7], v[132:135], v[224:227], 0\n"
"ds_read_b64_tr_b16 a[56:57], v249 offset:49152\n"
"v_cvt_pk_bf16_f32 v83, v86, v87\n"
"v_mfma_f32_16x16x32_bf16 v[4:7], v[128:131], a[252:255], v[4:7]\n"
"ds_write_b128 v248, v[80:83] offset:51200\n"
"v_cvt_pk_bf16_f32 v99, v170, v171\n"
"v_exp_f32 v155, v155\n"
"v_exp_f32 v157, v157\n"
"v_mfma_f32_16x16x32_bf16 a[212:215], v[124:127], a[252:255], 0\n"
"s_add_u32 s40, s56, s54\n"
"ds_read_b64_tr_b16 a[58:59], v249 offset:49160\n"
"ds_read_b64_tr_b16 v[224:225], v237 offset:1024\n"
"v_smfmac_f32_16x16x64_bf16 v[200:203], a[240:243], v[144:151], v241\n"
"buffer_load_dwordx4 v240, s[16:19], s40 offen offset:3072 nt lds\n"
"v_mfma_f32_16x16x32_bf16 v[168:171], v[208:211], a[0:3], v[180:183]\n"
"s_waitcnt lgkmcnt(8)\n"
"v_exp_f32 v153, v153\n"
"v_mfma_f32_16x16x32_bf16 v[168:171], v[212:215], a[4:7], v[168:171]\n"
"ds_read_b64_tr_b16 a[72:73], v249 offset:50176\n"
"ds_read_b64_tr_b16 a[90:91], v249 offset:51208\n"
"v_mfma_f32_16x16x32_bf16 v[168:171], v[216:219], a[8:11], v[168:171]\n"
"ds_read_b64_tr_b16 a[88:89], v249 offset:51200\n"
"v_cvt_pk_bf16_f32 v115, v178, v179\n"
"v_mfma_f32_16x16x32_bf16 v[168:171], v[220:223], a[12:15], v[168:171]\n"
"ds_read_b64_tr_b16 a[244:245], v232 offset:1024\n"
"v_cvt_pk_bf16_f32 v106, v172, v173\n"
"v_mfma_f32_16x16x32_bf16 v[172:175], v[216:219], a[24:27], v[184:187]\n"
"ds_read_b64_tr_b16 a[248:249], v236 offset:1024\n"
"v_cvt_pk_bf16_f32 v114, v176, v177\n"
"v_mfma_f32_16x16x32_bf16 v[172:175], v[220:223], a[28:31], v[172:175]\n"
"v_exp_f32 v162, v162\n"
"v_mfma_f32_16x16x32_bf16 v[172:175], v[208:211], a[16:19], v[172:175]\n"
"ds_read_b64_tr_b16 v[226:227], v237 offset:5120\n"
"v_cvt_pk_bf16_f32 v122, v152, v153\n"
"v_mfma_f32_16x16x32_bf16 v[172:175], v[212:215], a[20:23], v[172:175]\n"
"s_cbranch_vccnz LPCT1_%=\nLPKT1_%=:\n"
"ds_read_b64_tr_b16 a[252:253], v233 offset:1024\n"
"v_mfma_f32_16x16x32_bf16 v[176:179], v[208:211], a[32:35], v[188:191]\n"
"v_exp_f32 v166, v166\n"
"v_mfma_f32_16x16x32_bf16 v[176:179], v[212:215], a[36:39], v[176:179]\n"
"ds_read_b64_tr_b16 a[254:255], v233 offset:5120\n"
"v_cvt_pk_bf16_f32 v109, v162, v163\n"
"v_mfma_f32_16x16x32_bf16 v[176:179], v[216:219], a[40:43], v[176:179]\n"
"v_exp_f32 v169, v169\n"
"v_mfma_f32_16x16x32_bf16 v[176:179], v[220:223], a[44:47], v[176:179]\n"
"s_waitcnt lgkmcnt(4)\n"
"v_cvt_pk_bf16_f32 v101, v158, v159\n"
"v_mfma_f32_16x16x32_bf16 a[224:227], v[124:127], a[244:247], 0\n"
"v_exp_f32 v154, v154\n"
"v_mfma_f32_16x16x32_bf16 v[16:19], v[128:131], a[244:247], 0\n"
"s_waitcnt vmcnt(8) lgkmcnt(3)\n"
"v_exp_f32 v168, v168\n"
"v_mfma_f32_16x16x32_bf16 v[16:19], v[132:135], a[248:251], v[16:19]\n"
"s_barrier\n"
"ds_read_b128 v[212:215], v229 offset:32768\n"
"ds_read_b128 v[216:219], v228 offset:33792\n"
"v_exp_f32 v165, v165\n"
"v_mfma_f32_16x16x32_bf16 v[48:51], v[136:139], a[244:247], 0\n"
"ds_read_b128 v[220:223], v229 offset:33792\n"
"v_xor_b32 v249, s75, v249\n"
"v_mfma_f32_16x16x32_bf16 v[48:51], v[140:143], a[248:251], v[48:51]\n"
"ds_read_b128 v[208:211], v228 offset:32768\n"
"v_add_u32 v243, 0x1000, v243\n"
"v_mfma_f32_16x16x32_bf16 v[80:83], v[144:147], a[244:247], 0\n"
"ds_read_b64_tr_b16 a[246:247], v234 offset:20480\n"
"v_cvt_pk_bf16_f32 v117, v166, v167\n"
"v_mfma_f32_16x16x32_bf16 v[80:83], v[148:151], a[248:251], v[80:83]\n"
"s_waitcnt lgkmcnt(5)\n"
"ds_read_b64_tr_b16 a[250:251], v238 offset:20480\n"
"v_xor_b32 v248, s75, v248\n"
"v_mfma_f32_16x16x32_bf16 v[84:87], v[148:151], v[224:227], 0\n"
"v_cvt_pk_bf16_f32 v100, v156, v157\n"
"v_cvt_pk_bf16_f32 v116, v164, v165\n"
"v_mfma_f32_16x16x32_bf16 v[84:87], v[144:147], a[252:255], v[84:87]\n"
"v_exp_f32 v160, v160\n"
"v_mfma_f32_16x16x32_bf16 v[52:55], v[140:143], v[224:227], 0\n"
"v_exp_f32 v176, v176\n"
"v_mfma_f32_16x16x32_bf16 v[52:55], v[136:139], a[252:255], v[52:55]\n"
"v_exp_f32 v177, v177\n"
"v_mfma_f32_16x16x32_bf16 v[20:23], v[132:135], v[224:227], 0\n"
"v_cvt_pk_bf16_f32 v123, v154, v155\n"
"v_cvt_pk_bf16_f32 v118, v176, v177\n"
"v_mfma_f32_16x16x32_bf16 v[20:23], v[128:131], a[252:255], v[20:23]\n"
"v_exp_f32 v174, v174\n"
"v_mfma_f32_16x16x32_bf16 a[228:231], v[124:127], a[252:255], 0\n"
"s_waitcnt lgkmcnt(1)\n"
"ds_read_b64_tr_b16 a[254:255], v235 offset:20480\n"
"s_branch LAY2_%=\n"
"LAX_%=:\n"
"v_mfma_f32_16x16x32_bf16 a[224:227], v[124:127], a[244:247], a[224:227]\n"
"v_exp_f32 v169, v169\n"
"v_mfma_f32_16x16x32_bf16 v[16:19], v[128:131], a[244:247], v[16:19]\n"
"s_waitcnt vmcnt(8)\n"
"v_exp_f32 v168, v168\n"
"v_mfma_f32_16x16x32_bf16 v[16:19], v[132:135], a[248:251], v[16:19]\n"
"s_barrier\n"
"v_mfma_f32_16x16x32_bf16 v[48:51], v[136:139], a[244:247], v[48:51]\n"
"v_lshlrev_b32_e64 v244, 24, s43\n"
"v_and_b32 v245, 0x3f0, v242\n"
"v_mfma_f32_16x16x32_bf16 v[48:51], v[140:143], a[248:251], v[48:51]\n"
"v_accvgpr_read_b32 v253, a96\n"
"v_and_b32 v253, 0x7fff0000, v253\n"
"v_mfma_f32_16x16x32_bf16 v[80:83], v[144:147], a[244:247], v[80:83]\n"
"v_exp_f32 v178, v178\n"
"v_mfma_f32_16x16x32_bf16 v[80:83], v[148:151], a[248:251], v[80:83]\n"
"ds_read_b64_tr_b16 a[244:245], v234 offset:49152\n"
"ds_read_b64_tr_b16 a[246:247], v234 offset:53248\n"
"v_cvt_pk_bf16_f32 v123, v154, v155\n"
"v_cvt_pk_bf16_f32 v102, v168, v169\n"
"v_exp_f32 v173, v173\n"
"v_exp_f32 v177, v177\n"
"v_exp_f32 v179, v179\n"
"v_mfma_f32_16x16x32_bf16 v[84:87], v[148:151], v[224:227], v[84:87]\n"
"ds_read_b64_tr_b16 a[248:249], v238 offset:49152\n"
"v_cvt_pk_bf16_f32 v116, v164, v165\n"
"v_mfma_f32_16x16x32_bf16 v[84:87], v[144:147], a[252:255], v[84:87]\n"
"ds_read_b64_tr_b16 a[250:251], v238 offset:53248\n"
"v_cvt_pk_bf16_f32 v100, v156, v157\n"
"v_cvt_pk_bf16_f32 v108, v160, v161\n"
"v_exp_f32 v162, v162\n"
"v_exp_f32 v171, v171\n"
"v_exp_f32 v174, v174\n"
"v_exp_f32 v176, v176\n"
"v_max_u32_dpp v253, v253, v253 quad_perm:[1,0,3,2] row_mask:0xf bank_mask:0xf\n"
"v_mfma_f32_16x16x32_bf16 v[52:55], v[140:143], v[224:227], v[52:55]\n"
"v_exp_f32 v170, v170\n"
"v_mfma_f32_16x16x32_bf16 v[52:55], v[136:139], a[252:255], v[52:55]\n"
"v_cvt_pk_bf16_f32 v101, v158, v159\n"
"v_max_u32_dpp v253, v253, v253 quad_perm:[2,3,0,1] row_mask:0xf bank_mask:0xf\n"
"v_mfma_f32_16x16x32_bf16 v[20:23], v[132:135], v[224:227], v[20:23]\n"
"v_exp_f32 v172, v172\n"
"v_mfma_f32_16x16x32_bf16 v[20:23], v[128:131], a[252:255], v[20:23]\n"
"v_cvt_pk_bf16_f32 v103, v170, v171\n"
"v_cvt_pk_bf16_f32 v118, v176, v177\n"
"v_mfma_f32_16x16x32_bf16 a[228:231], v[124:127], a[252:255], a[228:231]\n"
"s_waitcnt lgkmcnt(2)\n"
"ds_read_b64_tr_b16 a[252:253], v235 offset:49152\n"
"v_cvt_pk_bf16_f32 v110, v172, v173\n"
"v_mfma_f32_16x16x32_bf16 a[216:219], v[120:123], a[244:247], a[216:219]\n"
"s_mov_b64 s[28:29], s[8:9]\n"
"ds_read_b64_tr_b16 v[224:225], v239 offset:49152\n"
"v_cvt_pk_bf16_f32 v117, v166, v167\n"
"v_smfmac_f32_16x16x64_bf16 v[196:199], a[240:243], v[96:103], v241\n"
"s_mov_b32 s30, 655360\n"
"ds_read_b64_tr_b16 v[226:227], v239 offset:53248\n"
"v_and_b32 v252, 0x7fff0000, v252\n"
"v_mfma_f32_16x16x32_bf16 v[8:11], v[96:99], a[244:247], v[8:11]\n"
"s_waitcnt lgkmcnt(3)\n"
"s_mov_b32 s71, s44\n"
"ds_read_b64_tr_b16 a[254:255], v235 offset:53248\n"
"v_cvt_pk_bf16_f32 v119, v178, v179\n"
"v_mfma_f32_16x16x32_bf16 v[8:11], v[100:103], a[248:251], v[8:11]\n"
"s_mov_b32 s70, s95\n"
"v_exp_f32 v175, v175\n"
"v_smfmac_f32_16x16x64_bf16 v[200:203], a[240:243], v[112:119], v241\n"
"v_cvt_pk_bf16_f32 v109, v162, v163\n"
"v_cvt_pk_bf16_f32 v111, v174, v175\n"
"v_mfma_f32_16x16x32_bf16 v[40:43], v[104:107], a[244:247], v[40:43]\n"
"v_max_u32_dpp v253, v253, v253 row_half_mirror row_mask:0xf bank_mask:0xf\n"
"v_lshrrev_b32 v245, 1, v245\n"
"v_mfma_f32_16x16x32_bf16 v[40:43], v[108:111], a[248:251], v[40:43]\n"
"s_add_u32 s41, s70, 327680\n"
"s_add_u32 m0, s71, 32768\n"
"v_mul_f32 v244, 0x45800000, v244\n"
"v_smfmac_f32_16x16x64_bf16 v[198:201], a[240:243], v[104:111], v241\n"
"buffer_load_dwordx4 v240, s[28:31], s41 offen lds\n"
"v_mfma_f32_16x16x32_bf16 v[72:75], v[112:115], a[244:247], v[72:75]\n"
"v_max_u32_dpp v252, v252, v252 quad_perm:[1,0,3,2] row_mask:0xf bank_mask:0xf\n"
"v_add_f32 v196, v196, v197\n"
"v_mfma_f32_16x16x32_bf16 v[72:75], v[116:119], a[248:251], v[72:75]\n"
"ds_read_b64_tr_b16 a[248:249], v238 offset:50176\n"
"ds_read_b64_tr_b16 a[250:251], v238 offset:54272\n"
"v_mfma_f32_16x16x32_bf16 v[202:205], a[240:243], v[120:123], v[202:205]\n"
"s_waitcnt lgkmcnt(2)\n"
"ds_read_b64_tr_b16 a[244:245], v234 offset:50176\n"
"v_add_f32 v200, v200, v201\n"
"v_mfma_f32_16x16x32_bf16 v[76:79], v[116:119], v[224:227], v[76:79]\n"
"s_add_u32 s40, s41, s52\n"
"ds_read_b64_tr_b16 a[246:247], v234 offset:54272\n"
"v_min_u32_dpp v253, v253, v253 row_mirror row_mask:0xf bank_mask:0xf\n"
"v_mfma_f32_16x16x32_bf16 v[76:79], v[112:115], a[252:255], v[76:79]\n"
"buffer_load_dwordx4 v240, s[28:31], s40 offen offset:1024 lds\n"
"v_mfma_f32_16x16x32_bf16 v[44:47], v[108:111], v[224:227], v[44:47]\n"
"v_max_u32_dpp v252, v252, v252 quad_perm:[2,3,0,1] row_mask:0xf bank_mask:0xf\n"
"v_add_f32 v198, v198, v199\n"
"v_mfma_f32_16x16x32_bf16 v[44:47], v[104:107], a[252:255], v[44:47]\n"
"v_max_u32_dpp v252, v252, v252 row_half_mirror row_mask:0xf bank_mask:0xf\n"
"v_min_u32_dpp v253, v253, v253 row_bcast:15 row_mask:0xa bank_mask:0xf\n"
"v_add_f32 v244, v244, v202\n"
"v_min3_f32 v244, v244, v196, v198\n"
"v_min_f32 v244, v244, v200\n"
"v_rcp_f32 v244, v244\n"
"v_mfma_f32_16x16x32_bf16 v[12:15], v[100:103], v[224:227], v[12:15]\n"
"s_add_u32 s40, s41, s53\n"
"ds_read_b64_tr_b16 v[224:225], v239 offset:50176\n"
"ds_read_b64_tr_b16 v[226:227], v239 offset:54272\n"
"v_mfma_f32_16x16x32_bf16 v[12:15], v[96:99], a[252:255], v[12:15]\n"
"buffer_load_dwordx4 v240, s[28:31], s40 offen offset:2048 lds\n"
"v_mfma_f32_16x16x32_bf16 a[220:223], v[120:123], a[252:255], a[220:223]\n"
"s_waitcnt lgkmcnt(2)\n"
"ds_read_b64_tr_b16 a[252:253], v235 offset:50176\n"
"ds_read_b64_tr_b16 a[254:255], v235 offset:54272\n"
"v_add_f32 v254, v196, v198\n"
"v_add_f32 v254, v254, v200\n"
"v_add_f32 v254, v254, v202\n"
"v_fmac_f32 v254, 0x68800000, v244\n"
"v_cmp_nge_f32 vcc, s45, v254\n"
"v_sub_u32 v245, v242, v245\n"
"v_bfe_u32 v254, v242, 9, 1\n"
"s_cbranch_vccnz LSLOW_%=\nLBACK3_%=:\ns_mov_b64 vcc, s[64:65]\n"
"v_mfma_f32_16x16x32_bf16 a[232:235], v[120:123], a[244:247], a[232:235]\n"
"v_rcp_f32 v206, v200\n"
"v_mfma_f32_16x16x32_bf16 v[24:27], v[96:99], a[244:247], v[24:27]\n"
"v_min_u32_dpp v252, v252, v252 row_mirror row_mask:0xf bank_mask:0xf\n"
"v_mov_b32_dpp v206, v206 row_ror:12 row_mask:0x2 bank_mask:0xf\n"
"v_mfma_f32_16x16x32_bf16 v[24:27], v[100:103], a[248:251], v[24:27]\n"
"v_rcp_f32 v246, v196\n"
"v_mfma_f32_16x16x32_bf16 v[56:59], v[104:107], a[244:247], v[56:59]\n"
"s_add_u32 s40, s41, s54\n"
"v_min_u32_dpp v253, v253, v253 row_bcast:31 row_mask:0xc bank_mask:0xf\n"
"v_mov_b32_dpp v246, v246 row_ror:12 row_mask:0x2 bank_mask:0xf\n"
"v_mfma_f32_16x16x32_bf16 v[56:59], v[108:111], a[248:251], v[56:59]\n"
"buffer_load_dwordx4 v240, s[28:31], s40 offen offset:3072 lds\n"
"v_min_u32_dpp v252, v252, v252 row_bcast:15 row_mask:0xa bank_mask:0xf\n"
"v_rcp_f32 v247, v198\n"
"v_mov_b32_dpp v246, v246 row_ror:8 row_mask:0x4 bank_mask:0xf\n"
"v_mov_b32_dpp v206, v206 row_ror:8 row_mask:0x4 bank_mask:0xf\n"
"v_lshl_add_u32 v254, v254, 4, s43\n"
"v_mfma_f32_16x16x32_bf16 v[88:91], v[112:115], a[244:247], v[88:91]\n"
"s_add_u32 s71, s44, s75\n"
"v_mov_b32_dpp v247, v247 row_ror:12 row_mask:0x2 bank_mask:0xf\n"
"v_xor_b32 v244, 0x11000, v242\n"
"v_mfma_f32_16x16x32_bf16 v[88:91], v[116:119], a[248:251], v[88:91]\n"
"s_waitcnt lgkmcnt(2)\n"
"s_mov_b32 s70, s100\n"
"ds_read_b64_tr_b16 a[244:245], v232 offset:49152\n"
"ds_read_b64_tr_b16 a[248:249], v236 offset:49152\n"
"ds_read_b64_tr_b16 a[250:251], v236 offset:53248\n"
"v_min_u32_dpp v252, v252, v252 row_bcast:31 row_mask:0xc bank_mask:0xf\n"
"v_mul_f32 v252, 0x43340000, v252\n"
"v_mul_f32 v252, v252, v253\n"
"v_mov_b32_dpp v247, v247 row_ror:8 row_mask:0x4 bank_mask:0xf\n"
"v_mov_b32_dpp v246, v246 row_ror:4 row_mask:0x8 bank_mask:0xf\n"
"v_mov_b32_dpp v206, v206 row_ror:4 row_mask:0x8 bank_mask:0xf\n"
"v_xor_b32 v250, 0x1000, v245\n"
"v_mfma_f32_16x16x32_bf16 v[92:95], v[116:119], v[224:227], v[92:95]\n"
"s_waitcnt lgkmcnt(3)\n"
"s_add_u32 s41, s70, 163840\n"
"s_add_u32 m0, s71, 16384\n"
"ds_read_b64_tr_b16 a[246:247], v232 offset:53248\n"
"v_mfma_f32_16x16x32_bf16 v[92:95], v[112:115], a[252:255], v[92:95]\n"
"buffer_load_dwordx4 v240, s[28:31], s41 offen lds\n"
"v_mov_b32_dpp v247, v247 row_ror:4 row_mask:0x8 bank_mask:0xf\n"
"v_lshl_add_u32 v254, v254, 6, s75\n"
"v_mul_f32_dpp v9, v246, v9 row_newbcast:1 row_mask:0xf bank_mask:0xf\n"
"v_mul_f32_dpp v72, v206, v72 row_newbcast:0 row_mask:0xf bank_mask:0xf\n"
"v_mul_f32_dpp v76, v206, v76 row_newbcast:0 row_mask:0xf bank_mask:0xf\n"
"v_mul_f32_dpp v78, v206, v78 row_newbcast:2 row_mask:0xf bank_mask:0xf\n"
"v_mul_f32_dpp v25, v246, v25 row_newbcast:1 row_mask:0xf bank_mask:0xf\n"
"v_mul_f32_dpp v26, v246, v26 row_newbcast:2 row_mask:0xf bank_mask:0xf\n"
"v_mfma_f32_16x16x32_bf16 v[60:63], v[108:111], v[224:227], v[60:63]\n"
"v_mul_f32_dpp v77, v206, v77 row_newbcast:1 row_mask:0xf bank_mask:0xf\n"
"v_mul_f32_dpp v58, v247, v58 row_newbcast:2 row_mask:0xf bank_mask:0xf\n"
"v_mfma_f32_16x16x32_bf16 v[60:63], v[104:107], a[252:255], v[60:63]\n"
"ds_write_b128 v244, a[220:223] offset:33792\n"
"v_mul_f32_dpp v11, v246, v11 row_newbcast:3 row_mask:0xf bank_mask:0xf\n"
"v_mul_f32_dpp v12, v246, v12 row_newbcast:0 row_mask:0xf bank_mask:0xf\n"
"v_mul_f32_dpp v74, v206, v74 row_newbcast:2 row_mask:0xf bank_mask:0xf\n"
"v_mul_f32_dpp v75, v206, v75 row_newbcast:3 row_mask:0xf bank_mask:0xf\n"
"v_mul_f32_dpp v79, v206, v79 row_newbcast:3 row_mask:0xf bank_mask:0xf\n"
"v_mfma_f32_16x16x32_bf16 v[28:31], v[100:103], v[224:227], v[28:31]\n"
"s_add_u32 s40, s41, s52\n"
"ds_read_b64_tr_b16 v[226:227], v237 offset:53248\n"
"v_mul_f32_dpp v73, v206, v73 row_newbcast:1 row_mask:0xf bank_mask:0xf\n"
"v_mfma_f32_16x16x32_bf16 v[28:31], v[96:99], a[252:255], v[28:31]\n"
"buffer_load_dwordx4 v240, s[28:31], s40 offen offset:1024 lds\n"
"v_mfma_f32_16x16x32_bf16 a[236:239], v[120:123], a[252:255], a[236:239]\n"
"s_waitcnt lgkmcnt(2)\n"
"v_min_f32 v252, 0x71800000, v252\n"
"v_mul_f32_dpp v95, v206, v95 row_newbcast:3 row_mask:0xf bank_mask:0xf\n"
"v_mfma_f32_16x16x32_bf16 a[208:211], v[120:123], a[244:247], a[208:211]\n"
"ds_read_b64_tr_b16 a[254:255], v233 offset:53248\n"
"ds_read_b64_tr_b16 v[224:225], v237 offset:49152\n"
"v_mfma_f32_16x16x32_bf16 v[0:3], v[96:99], a[244:247], v[0:3]\n"
"ds_read_b64_tr_b16 a[252:253], v233 offset:49152\n"
"v_mul_f32_dpp v89, v206, v89 row_newbcast:1 row_mask:0xf bank_mask:0xf\n"
"v_mfma_f32_16x16x32_bf16 v[0:3], v[100:103], a[248:251], v[0:3]\n"
"ds_write_b128 v244, a[216:219] offset:32768\n"
"v_readlane_b32 s23, v252, 63\n"
"v_cvt_pk_bf16_f32 v72, v72, v73\n"
"v_cvt_pk_bf16_f32 v73, v74, v75\n"
"v_mul_f32_dpp v56, v247, v56 row_newbcast:0 row_mask:0xf bank_mask:0xf\n"
"v_mul_f32_dpp v62, v247, v62 row_newbcast:2 row_mask:0xf bank_mask:0xf\n"
"v_mfma_f32_16x16x32_bf16 v[32:35], v[104:107], a[244:247], v[32:35]\n"
"s_add_u32 s40, s41, s53\n"
"v_mul_f32_dpp v46, v247, v46 row_newbcast:2 row_mask:0xf bank_mask:0xf\n"
"v_mul_f32_dpp v24, v246, v24 row_newbcast:0 row_mask:0xf bank_mask:0xf\n"
"v_mfma_f32_16x16x32_bf16 v[32:35], v[108:111], a[248:251], v[32:35]\n"
"buffer_load_dwordx4 v240, s[28:31], s40 offen offset:2048 lds\n"
"v_readlane_b32 s22, v252, 63\n"
"v_mul_f32_dpp v41, v247, v41 row_newbcast:1 row_mask:0xf bank_mask:0xf\n"
"v_mul_f32_dpp v42, v247, v42 row_newbcast:2 row_mask:0xf bank_mask:0xf\n"
"v_mul_f32_dpp v44, v247, v44 row_newbcast:0 row_mask:0xf bank_mask:0xf\n"
"v_mul_f32_dpp v45, v247, v45 row_newbcast:1 row_mask:0xf bank_mask:0xf\n"
"v_cvt_pk_bf16_f32 v74, v76, v77\n"
"v_mul_f32_dpp v31, v246, v31 row_newbcast:3 row_mask:0xf bank_mask:0xf\n"
"v_mul_f32_dpp v88, v206, v88 row_newbcast:0 row_mask:0xf bank_mask:0xf\n"
"v_mfma_f32_16x16x32_bf16 v[64:67], v[112:115], a[244:247], v[64:67]\n"
"ds_read_b64_tr_b16 a[244:245], v232 offset:50176\n"
"ds_read_b64_tr_b16 a[246:247], v232 offset:54272\n"
"v_mfma_f32_16x16x32_bf16 v[64:67], v[116:119], a[248:251], v[64:67]\n"
"s_waitcnt lgkmcnt(4)\n"
"ds_write_b128 v244, a[232:235] offset:34816\n"
"v_mul_f32_dpp v13, v246, v13 row_newbcast:1 row_mask:0xf bank_mask:0xf\n"
"v_cvt_pk_bf16_f32 v75, v78, v79\n"
"v_mul_f32_dpp v57, v247, v57 row_newbcast:1 row_mask:0xf bank_mask:0xf\n"
"v_mul_f32_dpp v92, v206, v92 row_newbcast:0 row_mask:0xf bank_mask:0xf\n"
"v_mul_f32_dpp v1, v246, v1 row_newbcast:1 row_mask:0xf bank_mask:0xf\n"
"v_mfma_f32_16x16x32_bf16 v[68:71], v[116:119], v[224:227], v[68:71]\n"
"s_waitcnt lgkmcnt(4)\n"
"s_add_u32 s40, s41, s54\n"
"ds_read_b64_tr_b16 a[248:249], v236 offset:50176\n"
"ds_read_b64_tr_b16 a[250:251], v236 offset:54272\n"
"v_mfma_f32_16x16x32_bf16 v[68:71], v[112:115], a[252:255], v[68:71]\n"
"buffer_load_dwordx4 v240, s[28:31], s40 offen offset:3072 lds\n"
"v_mov_b32 v253, v202\n"
"v_sub_u32 v228, v232, v254\n"
"v_mul_f32_dpp v15, v246, v15 row_newbcast:3 row_mask:0xf bank_mask:0xf\n"
"v_mul_f32_dpp v43, v247, v43 row_newbcast:3 row_mask:0xf bank_mask:0xf\n"
"v_cvt_pk_bf16_f32 v24, v24, v25\n"
"v_mul_f32_dpp v59, v247, v59 row_newbcast:3 row_mask:0xf bank_mask:0xf\n"
"v_mul_f32_dpp v0, v246, v0 row_newbcast:0 row_mask:0xf bank_mask:0xf\n"
"v_cvt_pk_bf16_f32 v0, v0, v1\n"
"v_mfma_f32_16x16x32_bf16 v[36:39], v[108:111], v[224:227], v[36:39]\n"
"v_cvt_pk_bf16_f32 v56, v56, v57\n"
"v_mul_f32_dpp v33, v247, v33 row_newbcast:1 row_mask:0xf bank_mask:0xf\n"
"v_mfma_f32_16x16x32_bf16 v[36:39], v[104:107], a[252:255], v[36:39]\n"
"ds_write_b128 v244, a[236:239] offset:35840\n"
"v_mov_b32 v252, v192\n"
"v_mul_f32_dpp v8, v246, v8 row_newbcast:0 row_mask:0xf bank_mask:0xf\n"
"v_mul_f32_dpp v60, v247, v60 row_newbcast:0 row_mask:0xf bank_mask:0xf\n"
"v_mul_f32_dpp v34, v247, v34 row_newbcast:2 row_mask:0xf bank_mask:0xf\n"
"v_xor_b32 v232, 0x1000, v228\n"
"v_mfma_f32_16x16x32_bf16 v[4:7], v[100:103], v[224:227], v[4:7]\n"
"ds_read_b64_tr_b16 v[224:225], v237 offset:50176\n"
"ds_read_b64_tr_b16 v[226:227], v237 offset:54272\n"
"v_mfma_f32_16x16x32_bf16 v[4:7], v[96:99], a[252:255], v[4:7]\n"
"ds_write_b64 v250, v[252:253] offset:52224\n"
"v_mul_f32_dpp v40, v247, v40 row_newbcast:0 row_mask:0xf bank_mask:0xf\n"
"v_cvt_pk_bf16_f32 v40, v40, v41\n"
"v_mul_f32_dpp v27, v246, v27 row_newbcast:3 row_mask:0xf bank_mask:0xf\n"
"v_mul_f32_dpp v28, v246, v28 row_newbcast:0 row_mask:0xf bank_mask:0xf\n"
"v_mul_f32_dpp v93, v206, v93 row_newbcast:1 row_mask:0xf bank_mask:0xf\n"
"v_cvt_pk_bf16_f32 v88, v88, v89\n"
"v_mul_f32_dpp v2, v246, v2 row_newbcast:2 row_mask:0xf bank_mask:0xf\n"
"v_mfma_f32_16x16x32_bf16 a[212:215], v[120:123], a[252:255], a[212:215]\n"
"s_waitcnt lgkmcnt(7)\n"
"ds_read_b64_tr_b16 a[252:253], v233 offset:50176\n"
"ds_read_b64_tr_b16 a[254:255], v233 offset:54272\n"
"v_mul_f32_dpp v10, v246, v10 row_newbcast:2 row_mask:0xf bank_mask:0xf\n"
"v_mul_f32_dpp v47, v247, v47 row_newbcast:3 row_mask:0xf bank_mask:0xf\n"
"v_cvt_pk_bf16_f32 v41, v42, v43\n"
"v_cvt_pk_bf16_f32 v43, v46, v47\n"
"v_mul_f32_dpp v30, v246, v30 row_newbcast:2 row_mask:0xf bank_mask:0xf\n"
"v_mul_f32_dpp v61, v247, v61 row_newbcast:1 row_mask:0xf bank_mask:0xf\n"
"v_mul_f32_dpp v36, v247, v36 row_newbcast:0 row_mask:0xf bank_mask:0xf\n"
"v_mul_f32_dpp v38, v247, v38 row_newbcast:2 row_mask:0xf bank_mask:0xf\n"
"v_mfma_f32_16x16x32_bf16 a[224:227], v[120:123], a[244:247], a[224:227]\n"
"ds_write_b128 v248, v[72:75] offset:51200\n"
"v_mul_f32_dpp v14, v246, v14 row_newbcast:2 row_mask:0xf bank_mask:0xf\n"
"v_cvt_pk_bf16_f32 v42, v44, v45\n"
"v_mul_f32_dpp v35, v247, v35 row_newbcast:3 row_mask:0xf bank_mask:0xf\n"
"v_mul_f32_dpp v64, v206, v64 row_newbcast:0 row_mask:0xf bank_mask:0xf\n"
"v_mul_f32_dpp v68, v206, v68 row_newbcast:0 row_mask:0xf bank_mask:0xf\n"
"v_mfma_f32_16x16x32_bf16 v[16:19], v[96:99], a[244:247], v[16:19]\n"
"s_waitcnt vmcnt(8) lgkmcnt(1)\n"
"ds_write_b128 v248, v[40:43] offset:50176\n"
"v_mul_f32_dpp v90, v206, v90 row_newbcast:2 row_mask:0xf bank_mask:0xf\n"
"v_mul_f32_dpp v91, v206, v91 row_newbcast:3 row_mask:0xf bank_mask:0xf\n"
"v_mul_f32_dpp v5, v246, v5 row_newbcast:1 row_mask:0xf bank_mask:0xf\n"
"v_mul_f32_dpp v6, v246, v6 row_newbcast:2 row_mask:0xf bank_mask:0xf\n"
"v_mul_f32_dpp v32, v247, v32 row_newbcast:0 row_mask:0xf bank_mask:0xf\n"
"v_mfma_f32_16x16x32_bf16 v[16:19], v[100:103], a[248:251], v[16:19]\n"
"s_barrier\n"
"ds_read_b64_tr_b16 a[20:21], v249 offset:50176\n"
"ds_read_b64_tr_b16 a[22:23], v249 offset:50184\n"
"ds_read_b64_tr_b16 v[210:211], v228 offset:18432\n"
"ds_read_b64_tr_b16 v[216:217], v228 offset:24576\n"
"v_cvt_pk_bf16_f32 v8, v8, v9\n"
"v_cvt_pk_bf16_f32 v89, v90, v91\n"
"v_cvt_pk_bf16_f32 v90, v92, v93\n"
"v_add_u32 v236, s76, v249\n"
"v_mfma_f32_16x16x32_bf16 v[48:51], v[104:107], a[244:247], v[48:51]\n"
"ds_read_b64_tr_b16 v[184:185], v232 offset:0\n"
"v_mul_f32_dpp v65, v206, v65 row_newbcast:1 row_mask:0xf bank_mask:0xf\n"
"v_mfma_f32_16x16x32_bf16 v[48:51], v[108:111], a[248:251], v[48:51]\n"
"ds_read_b64_tr_b16 v[182:183], v228 offset:2048\n"
"ds_read_b64_tr_b16 v[192:193], v232 offset:8192\n"
"ds_read_b64_tr_b16 v[212:213], v232 offset:16384\n"
"ds_read_b64_tr_b16 v[222:223], v232 offset:26624\n"
"v_cvt_pk_bf16_f32 v9, v10, v11\n"
"v_cvt_pk_bf16_f32 v10, v12, v13\n"
"v_cvt_pk_bf16_f32 v11, v14, v15\n"
"v_mul_f32_dpp v29, v246, v29 row_newbcast:1 row_mask:0xf bank_mask:0xf\n"
"v_cvt_pk_bf16_f32 v25, v26, v27\n"
"v_mul_f32_dpp v66, v206, v66 row_newbcast:2 row_mask:0xf bank_mask:0xf\n"
"v_mfma_f32_16x16x32_bf16 v[80:83], v[112:115], a[244:247], v[80:83]\n"
"ds_read_b64 v[250:251], v245 offset:52224\n"
"ds_read_b64_tr_b16 v[186:187], v232 offset:2048\n"
"v_mfma_f32_16x16x32_bf16 v[80:83], v[116:119], a[248:251], v[80:83]\n"
"ds_read_b64_tr_b16 a[36:37], v249 offset:51200\n"
"ds_read_b64_tr_b16 a[38:39], v249 offset:51208\n"
"ds_read_b64_tr_b16 v[190:191], v228 offset:10240\n"
"ds_read_b64_tr_b16 v[194:195], v232 offset:10240\n"
"v_sub_u32 v229, v233, v254\n"
"v_mul_f32_dpp v63, v247, v63 row_newbcast:3 row_mask:0xf bank_mask:0xf\n"
"v_cvt_pk_bf16_f32 v57, v58, v59\n"
"v_cvt_pk_bf16_f32 v59, v62, v63\n"
"v_mul_f32_dpp v67, v206, v67 row_newbcast:3 row_mask:0xf bank_mask:0xf\n"
"v_mul_f32_dpp v71, v206, v71 row_newbcast:3 row_mask:0xf bank_mask:0xf\n"
"v_mfma_f32_16x16x32_bf16 v[84:87], v[116:119], v[224:227], v[84:87]\n"
"ds_read_b64_tr_b16 v[208:209], v228 offset:16384\n"
"ds_read_b64_tr_b16 v[218:219], v228 offset:26624\n"
"v_mfma_f32_16x16x32_bf16 v[84:87], v[112:115], a[252:255], v[84:87]\n"
"ds_write_b128 v248, v[8:11] offset:49152\n"
"v_cvt_pk_bf16_f32 v26, v28, v29\n"
"v_cvt_pk_bf16_f32 v27, v30, v31\n"
"v_mul_f32_dpp v94, v206, v94 row_newbcast:2 row_mask:0xf bank_mask:0xf\n"
"v_mul_f32_dpp v3, v246, v3 row_newbcast:3 row_mask:0xf bank_mask:0xf\n"
"v_xor_b32 v244, 0x1000, v244\n"
"v_mfma_f32_16x16x32_bf16 v[52:55], v[108:111], v[224:227], v[52:55]\n"
"ds_read_b64_tr_b16 v[180:181], v228 offset:0\n"
"ds_read_b64_tr_b16 v[188:189], v228 offset:8192\n"
"v_mfma_f32_16x16x32_bf16 v[52:55], v[104:107], a[252:255], v[52:55]\n"
"ds_read_b64_tr_b16 a[4:5], v249 offset:49152\n"
"ds_read_b64_tr_b16 a[6:7], v249 offset:49160\n"
"ds_read_b64_tr_b16 v[214:215], v232 offset:18432\n"
"ds_read_b64_tr_b16 v[220:221], v232 offset:24576\n"
"v_cvt_pk_bf16_f32 v58, v60, v61\n"
"v_mul_f32_dpp v4, v246, v4 row_newbcast:0 row_mask:0xf bank_mask:0xf\n"
"v_mul_f32_dpp v7, v246, v7 row_newbcast:3 row_mask:0xf bank_mask:0xf\n"
"v_mul_f32_dpp v39, v247, v39 row_newbcast:3 row_mask:0xf bank_mask:0xf\n"
"v_mov_b64 v[196:197], s[22:23]\n"
"v_mfma_f32_16x16x32_bf16 v[20:23], v[100:103], v[224:227], v[20:23]\n"
"ds_write_b128 v248, v[56:59] offset:50176\n"
"v_cvt_pk_bf16_f32 v91, v94, v95\n"
"v_cvt_pk_bf16_f32 v32, v32, v33\n"
"v_cvt_pk_bf16_f32 v33, v34, v35\n"
"v_mul_f32_dpp v69, v206, v69 row_newbcast:1 row_mask:0xf bank_mask:0xf\n"
"v_xor_b32 v237, 0x400, v236\n"
"v_mfma_f32_16x16x32_bf16 v[20:23], v[96:99], a[252:255], v[20:23]\n"
"ds_write_b128 v248, v[24:27] offset:49152\n"
"v_mul_f32_dpp v70, v206, v70 row_newbcast:2 row_mask:0xf bank_mask:0xf\n"
"v_cvt_pk_bf16_f32 v64, v64, v65\n"
"v_cvt_pk_bf16_f32 v65, v66, v67\n"
"v_mov_b64 v[198:199], s[22:23]\n"
"v_xor_b32 v233, 0x1000, v229\n"
"v_mfma_f32_16x16x32_bf16 a[228:231], v[120:123], a[252:255], a[228:231]\n"
"s_waitcnt lgkmcnt(7)\n"
"ds_write_b128 v248, v[88:91] offset:51200\n"
"v_sub_u32 v231, v235, v254\n"
"v_cvt_pk_bf16_f32 v1, v2, v3\n"
"v_cvt_pk_bf16_f32 v2, v4, v5\n"
"v_cvt_pk_bf16_f32 v3, v6, v7\n"
"v_mul_f32_dpp v37, v247, v37 row_newbcast:1 row_mask:0xf bank_mask:0xf\n"
"v_mfma_f32_16x16x32_bf16 v[124:127], v[180:183], a[96:99], v[196:199]\n"
"ds_read_b64_tr_b16 a[12:13], v249 offset:49152\n"
"ds_read_b64_tr_b16 a[28:29], v249 offset:50176\n"
"v_mfma_f32_16x16x32_bf16 v[124:127], v[184:187], a[100:103], v[124:127]\n"
"s_waitcnt lgkmcnt(9)\n"
"ds_read_b64_tr_b16 a[14:15], v249 offset:49160\n"
"ds_read_b64_tr_b16 a[30:31], v249 offset:50184\n"
"v_mfma_f32_16x16x32_bf16 v[124:127], v[188:191], a[104:107], v[124:127]\n"
"ds_read_b128 v[56:59], v244 offset:34816\n"
"ds_read_b128 v[60:63], v244 offset:35840\n"
"v_mfma_f32_16x16x32_bf16 v[124:127], v[192:195], a[108:111], v[124:127]\n"
"ds_write_b128 v248, v[0:3] offset:49152\n"
"v_sub_u32 v230, v234, v254\n"
"v_cvt_pk_bf16_f32 v34, v36, v37\n"
"v_cvt_pk_bf16_f32 v35, v38, v39\n"
"v_cvt_pk_bf16_f32 v66, v68, v69\n"
"v_cvt_pk_bf16_f32 v67, v70, v71\n"
"v_mfma_f32_16x16x32_bf16 v[124:127], v[208:211], a[144:147], v[124:127]\n"
"s_waitcnt lgkmcnt(10)\n"
"ds_read_b64_tr_b16 a[44:45], v249 offset:51200\n"
"ds_read_b64_tr_b16 a[46:47], v249 offset:51208\n"
"v_mfma_f32_16x16x32_bf16 v[124:127], v[212:215], a[148:151], v[124:127]\n"
"ds_write_b128 v248, v[32:35] offset:50176\n"
"ds_write_b128 v248, v[64:67] offset:51200\n"
"v_mfma_f32_16x16x32_bf16 v[124:127], v[216:219], a[152:155], v[124:127]\n"
"ds_read_b128 v[24:27], v244 offset:32768\n"
"ds_read_b128 v[28:31], v244 offset:33792\n"
"v_mfma_f32_16x16x32_bf16 v[124:127], v[220:223], a[156:159], v[124:127]\n"
"ds_read_b64_tr_b16 a[0:1], v249 offset:49152\n"
"ds_read_b64_tr_b16 a[16:17], v249 offset:50176\n"
"v_mfma_f32_16x16x32_bf16 v[140:143], v[220:223], a[172:175], v[196:199]\n"
"ds_read_b64_tr_b16 a[2:3], v249 offset:49160\n"
"ds_read_b64_tr_b16 a[254:255], v237 offset:6152\n"
"v_mfma_f32_16x16x32_bf16 v[140:143], v[216:219], a[168:171], v[140:143]\n"
"ds_read_b64_tr_b16 a[32:33], v249 offset:51200\n"
"ds_read_b64_tr_b16 v[118:119], v233 offset:18432\n"
"v_mfma_f32_16x16x32_bf16 v[140:143], v[212:215], a[164:167], v[140:143]\n"
"ds_read_b64_tr_b16 a[18:19], v249 offset:50184\n"
"ds_read_b64_tr_b16 a[34:35], v249 offset:51208\n"
"v_mfma_f32_16x16x32_bf16 v[140:143], v[208:211], a[160:163], v[140:143]\n"
"ds_read_b64_tr_b16 a[244:245], v237 offset:4096\n"
"ds_read_b64_tr_b16 a[248:249], v236 offset:6144\n"
"v_mfma_f32_16x16x32_bf16 v[140:143], v[192:195], a[124:127], v[140:143]\n"
"ds_read_b64_tr_b16 a[202:203], v236 offset:2056\n"
"ds_read_b64_tr_b16 a[252:253], v237 offset:6144\n"
"v_mfma_f32_16x16x32_bf16 v[140:143], v[188:191], a[120:123], v[140:143]\n"
"ds_read_b64_tr_b16 a[196:197], v237 offset:0\n"
"ds_read_b64_tr_b16 a[250:251], v236 offset:6152\n"
"v_mfma_f32_16x16x32_bf16 v[140:143], v[184:187], a[116:119], v[140:143]\n"
"ds_read_b64_tr_b16 v[112:113], v229 offset:16384\n"
"ds_read_b64_tr_b16 a[242:243], v236 offset:4104\n"
"v_mfma_f32_16x16x32_bf16 v[140:143], v[180:183], a[112:115], v[140:143]\n"
"ds_read_b64_tr_b16 a[204:205], v237 offset:2048\n"
"ds_read_b64_tr_b16 a[206:207], v237 offset:2056\n"
"ds_read_b64_tr_b16 v[100:101], v233 offset:0\n"
"ds_read_b64_tr_b16 v[104:105], v229 offset:8192\n"
"ds_read_b64_tr_b16 a[246:247], v237 offset:4104\n"
"v_mul_f32_dpp v23, v246, v23 row_newbcast:3 row_mask:0xf bank_mask:0xf\n"
"v_mfma_f32_16x16x32_bf16 v[156:159], v[180:183], a[128:131], v[196:199]\n"
"ds_read_b64_tr_b16 v[96:97], v229 offset:0\n"
"ds_read_b64_tr_b16 v[108:109], v233 offset:8192\n"
"v_mfma_f32_16x16x32_bf16 v[156:159], v[184:187], a[132:135], v[156:159]\n"
"ds_read_b64_tr_b16 a[198:199], v237 offset:8\n"
"ds_read_b64_tr_b16 a[240:241], v236 offset:4096\n"
"v_mfma_f32_16x16x32_bf16 v[156:159], v[188:191], a[136:139], v[156:159]\n"
"ds_read_b64_tr_b16 v[106:107], v229 offset:10240\n"
"ds_read_b64_tr_b16 a[234:235], v236 offset:10248\n"
"v_mfma_f32_16x16x32_bf16 v[156:159], v[192:195], a[140:143], v[156:159]\n"
"s_waitcnt lgkmcnt(15)\n"
"ds_read_b64_tr_b16 a[194:195], v236 offset:8\n"
"ds_read_b64_tr_b16 v[110:111], v233 offset:10240\n"
"v_mfma_f32_16x16x32_bf16 v[156:159], v[208:211], a[176:179], v[156:159]\n"
"ds_read_b64_tr_b16 a[192:193], v236 offset:0\n"
"ds_read_b64_tr_b16 a[200:201], v236 offset:2048\n"
"v_mfma_f32_16x16x32_bf16 v[156:159], v[212:215], a[180:183], v[156:159]\n"
"s_add_u32 s41, s95, 491520\n"
"ds_read_b64_tr_b16 v[102:103], v233 offset:2048\n"
"ds_read_b64_tr_b16 v[226:227], v233 offset:26624\n"
"v_mfma_f32_16x16x32_bf16 v[156:159], v[216:219], a[184:187], v[156:159]\n"
"s_add_u32 m0, s44, 49152\n"
"ds_read_b64_tr_b16 v[98:99], v229 offset:2048\n"
"v_xor_b32 v234, 0x1000, v230\n"
"v_mfma_f32_16x16x32_bf16 v[156:159], v[220:223], a[188:191], v[156:159]\n"
"buffer_load_dwordx4 v240, s[28:31], s41 offen lds\n"
"v_mfma_f32_16x16x32_bf16 v[172:175], v[220:223], a[252:255], v[196:199]\n"
"s_waitcnt lgkmcnt(11)\n"
"s_add_u32 s40, s41, s52\n"
"ds_read_b64_tr_b16 v[114:115], v229 offset:18432\n"
"ds_read_b64_tr_b16 v[116:117], v233 offset:16384\n"
"v_mfma_f32_16x16x32_bf16 v[172:175], v[216:219], a[248:251], v[172:175]\n"
"buffer_load_dwordx4 v240, s[28:31], s40 offen offset:1024 lds\n"
"v_mfma_f32_16x16x32_bf16 v[172:175], v[212:215], a[244:247], v[172:175]\n"
"s_waitcnt lgkmcnt(9)\n"
"s_add_u32 s40, s41, s53\n"
"v_min_f32 v254, v252, v250\n"
"v_sub_f32 v252, v254, v252\n"
"v_mfma_f32_16x16x32_bf16 v[172:175], v[208:211], a[240:243], v[172:175]\n"
"buffer_load_dwordx4 v240, s[28:31], s40 offen offset:2048 lds\n"
"v_mfma_f32_16x16x32_bf16 v[172:175], v[192:195], a[204:207], v[172:175]\n"
"s_waitcnt lgkmcnt(5)\n"
"ds_read_b64_tr_b16 v[120:121], v229 offset:24576\n"
"ds_read_b64_tr_b16 v[122:123], v229 offset:26624\n"
"v_mfma_f32_16x16x32_bf16 v[172:175], v[188:191], a[200:203], v[172:175]\n"
"v_exp_f32 v252, v252\n"
"v_mfma_f32_16x16x32_bf16 v[172:175], v[184:187], a[196:199], v[172:175]\n"
"s_add_u32 s40, s41, s54\n"
"ds_read_b64_tr_b16 v[224:225], v233 offset:24576\n"
"ds_read_b64_tr_b16 v[218:219], v230 offset:26624\n"
"v_mfma_f32_16x16x32_bf16 v[172:175], v[180:183], a[192:195], v[172:175]\n"
"s_waitcnt lgkmcnt(6)\n"
"buffer_load_dwordx4 v240, s[28:31], s40 offen offset:3072 lds\n"
"v_mfma_f32_16x16x32_bf16 v[128:131], v[96:99], a[96:99], v[196:199]\n"
"v_mul_f32 v253, v253, v252\n"
"v_mul_f32_dpp v84, v206, v84 row_newbcast:0 row_mask:0xf bank_mask:0xf\n"
"v_mfma_f32_16x16x32_bf16 v[128:131], v[100:103], a[100:103], v[128:131]\n"
"v_mul_f32_dpp v16, v246, v16 row_newbcast:0 row_mask:0xf bank_mask:0xf\n"
"v_mul_f32_dpp v20, v246, v20 row_newbcast:0 row_mask:0xf bank_mask:0xf\n"
"v_mfma_f32_16x16x32_bf16 v[128:131], v[104:107], a[104:107], v[128:131]\n"
"ds_read_b64_tr_b16 v[190:191], v230 offset:10240\n"
"v_sub_f32 v250, v254, v250\n"
"v_mfma_f32_16x16x32_bf16 v[128:131], v[108:111], a[108:111], v[128:131]\n"
"s_waitcnt lgkmcnt(5)\n"
"ds_read_b64_tr_b16 v[180:181], v230 offset:0\n"
"ds_read_b64_tr_b16 v[186:187], v234 offset:2048\n"
"v_mfma_f32_16x16x32_bf16 v[128:131], v[112:115], a[144:147], v[128:131]\n"
"s_add_u32 s21, s60, 32\n"
"ds_read_b64_tr_b16 a[238:239], v237 offset:10248\n"
"v_mul_f32_dpp v17, v246, v17 row_newbcast:1 row_mask:0xf bank_mask:0xf\n"
"v_mfma_f32_16x16x32_bf16 v[128:131], v[116:119], a[148:151], v[128:131]\n"
"s_waitcnt lgkmcnt(6)\n"
"s_mov_b32 s69, 1\n"
"v_exp_f32 v250, v250\n"
"v_mfma_f32_16x16x32_bf16 v[128:131], v[120:123], a[152:155], v[128:131]\n"
"s_waitcnt lgkmcnt(4)\n"
"v_bfe_u32 v201, v242, 2, 7\n"
"v_add_u32 v201, 0x100, v201\n"
"v_fmac_f32 v253, v251, v250\n"
"v_mfma_f32_16x16x32_bf16 v[128:131], v[224:227], a[156:159], v[128:131]\n"
"global_load_dword v200, v201, s[6:7] sc1\n"
"v_mfma_f32_16x16x32_bf16 v[144:147], v[224:227], a[172:175], v[196:199]\n"
"v_accvgpr_read_b32 v251, a211\n"
"v_mul_f32_dpp v80, v206, v80 row_newbcast:0 row_mask:0xf bank_mask:0xf\n"
"v_mfma_f32_16x16x32_bf16 v[144:147], v[120:123], a[168:171], v[144:147]\n"
"v_rcp_f32 v253, v253\n"
"v_mfma_f32_16x16x32_bf16 v[144:147], v[116:119], a[164:167], v[144:147]\n"
"ds_read_b64_tr_b16 a[216:217], v236 offset:8192\n"
"v_mul_f32 v252, v252, v253\n"
"v_mfma_f32_16x16x32_bf16 v[144:147], v[112:115], a[160:163], v[144:147]\n"
"s_nop 0\n"
"v_mov_b32_dpp v252, v252 row_ror:12 row_mask:0x2 bank_mask:0xf\n"
"v_mul_f32_dpp v55, v247, v55 row_newbcast:3 row_mask:0xf bank_mask:0xf\n"
"v_mfma_f32_16x16x32_bf16 v[144:147], v[108:111], a[124:127], v[144:147]\n"
"ds_read_b64_tr_b16 a[218:219], v236 offset:8200\n"
"v_mul_f32_dpp v86, v206, v86 row_newbcast:2 row_mask:0xf bank_mask:0xf\n"
"v_mfma_f32_16x16x32_bf16 v[144:147], v[104:107], a[120:123], v[144:147]\n"
"v_accvgpr_read_b32 v254, a208\n"
"v_mul_f32_dpp v81, v206, v81 row_newbcast:1 row_mask:0xf bank_mask:0xf\n"
"v_mfma_f32_16x16x32_bf16 v[144:147], v[100:103], a[116:119], v[144:147]\n"
"ds_read_b64_tr_b16 v[184:185], v234 offset:0\n"
"v_mul_f32_dpp v52, v247, v52 row_newbcast:0 row_mask:0xf bank_mask:0xf\n"
"v_mfma_f32_16x16x32_bf16 v[144:147], v[96:99], a[112:115], v[144:147]\n"
"ds_read_b64_tr_b16 v[222:223], v234 offset:26624\n"
"v_mul_f32 v250, v250, v253\n"
"v_mfma_f32_16x16x32_bf16 v[160:163], v[96:99], a[128:131], v[196:199]\n"
"s_nop 0\n"
"v_mov_b32_dpp v250, v250 row_ror:12 row_mask:0x2 bank_mask:0xf\n"
"v_mul_f32_dpp v82, v206, v82 row_newbcast:2 row_mask:0xf bank_mask:0xf\n"
"v_mfma_f32_16x16x32_bf16 v[160:163], v[100:103], a[132:135], v[160:163]\n"
"ds_read_b64_tr_b16 v[182:183], v230 offset:2048\n"
"v_mov_b32_dpp v250, v250 row_ror:8 row_mask:0x4 bank_mask:0xf\n"
"v_mfma_f32_16x16x32_bf16 v[160:163], v[104:107], a[136:139], v[160:163]\n"
"ds_read_b64_tr_b16 v[214:215], v234 offset:18432\n"
"v_mul_f32_dpp v49, v247, v49 row_newbcast:1 row_mask:0xf bank_mask:0xf\n"
"v_mfma_f32_16x16x32_bf16 v[160:163], v[108:111], a[140:143], v[160:163]\n"
"ds_read_b64_tr_b16 v[210:211], v230 offset:18432\n"
"ds_read_b64_tr_b16 v[212:213], v234 offset:16384\n"
"v_mfma_f32_16x16x32_bf16 v[160:163], v[112:115], a[176:179], v[160:163]\n"
"ds_read_b64_tr_b16 v[220:221], v234 offset:24576\n"
"v_mov_b32_dpp v250, v250 row_ror:4 row_mask:0x8 bank_mask:0xf\n"
"v_mfma_f32_16x16x32_bf16 v[160:163], v[116:119], a[180:183], v[160:163]\n"
"ds_read_b64_tr_b16 v[208:209], v230 offset:16384\n"
"v_mov_b32_dpp v252, v252 row_ror:8 row_mask:0x4 bank_mask:0xf\n"
"v_mfma_f32_16x16x32_bf16 v[160:163], v[120:123], a[184:187], v[160:163]\n"
"s_nop 0\n"
"v_mov_b32_dpp v252, v252 row_ror:4 row_mask:0x8 bank_mask:0xf\n"
"v_mul_f32_dpp v27, v250, v27 row_newbcast:3 row_mask:0xf bank_mask:0xf\n"
"v_mfma_f32_16x16x32_bf16 v[160:163], v[224:227], a[188:191], v[160:163]\n"
"ds_read_b64_tr_b16 v[188:189], v230 offset:8192\n"
"ds_read_b64_tr_b16 v[192:193], v234 offset:8192\n"
"ds_read_b64_tr_b16 v[194:195], v234 offset:10240\n"
"v_accvgpr_read_b32 v255, a209\n"
"v_mul_f32_dpp v25, v250, v25 row_newbcast:1 row_mask:0xf bank_mask:0xf\n"
"v_mul_f32_dpp v26, v250, v26 row_newbcast:2 row_mask:0xf bank_mask:0xf\n"
"v_xor_b32 v235, 0x1000, v231\n"
"v_mul_f32_dpp v56, v250, v56 row_newbcast:0 row_mask:0xf bank_mask:0xf\n"
"v_mul_f32_dpp v59, v250, v59 row_newbcast:3 row_mask:0xf bank_mask:0xf\n"
"v_mfma_f32_16x16x32_bf16 v[176:179], v[224:227], a[252:255], v[196:199]\n"
"v_mul_f32_dpp v24, v250, v24 row_newbcast:0 row_mask:0xf bank_mask:0xf\n"
"v_accvgpr_read_b32 v253, a210\n"
"v_mfma_f32_16x16x32_bf16 v[176:179], v[120:123], a[248:251], v[176:179]\n"
"ds_read_b64_tr_b16 v[216:217], v230 offset:24576\n"
"v_mul_f32_dpp v60, v250, v60 row_newbcast:0 row_mask:0xf bank_mask:0xf\n"
"v_mfma_f32_16x16x32_bf16 v[176:179], v[116:119], a[244:247], v[176:179]\n"
"v_fmac_f32_dpp v27, v252, v251 row_newbcast:3 row_mask:0xf bank_mask:0xf\n"
"v_accvgpr_read_b32 v251, a215\n"
"v_mfma_f32_16x16x32_bf16 v[176:179], v[112:115], a[240:243], v[176:179]\n"
"ds_read_b64_tr_b16 v[114:115], v231 offset:18432\n"
"v_mul_f32_dpp v31, v250, v31 row_newbcast:3 row_mask:0xf bank_mask:0xf\n"
"v_mfma_f32_16x16x32_bf16 v[176:179], v[108:111], a[204:207], v[176:179]\n"
"ds_read_b64_tr_b16 v[122:123], v231 offset:26624\n"
"v_mul_f32_dpp v87, v206, v87 row_newbcast:3 row_mask:0xf bank_mask:0xf\n"
"v_mfma_f32_16x16x32_bf16 v[176:179], v[104:107], a[200:203], v[176:179]\n"
"v_fmac_f32_dpp v24, v252, v254 row_newbcast:0 row_mask:0xf bank_mask:0xf\n"
"v_mul_f32_dpp v57, v250, v57 row_newbcast:1 row_mask:0xf bank_mask:0xf\n"
"v_mfma_f32_16x16x32_bf16 v[176:179], v[100:103], a[196:199], v[176:179]\n"
"v_fmac_f32_dpp v25, v252, v255 row_newbcast:1 row_mask:0xf bank_mask:0xf\n"
"v_accvgpr_read_b32 v254, a212\n"
"v_mfma_f32_16x16x32_bf16 v[176:179], v[96:99], a[192:195], v[176:179]\n"
"s_waitcnt lgkmcnt(3)\n"
"ds_read_b64_tr_b16 v[106:107], v231 offset:10240\n"
"ds_read_b64_tr_b16 v[120:121], v231 offset:24576\n"
"v_accvgpr_read_b32 v255, a213\n"
"v_mul_f32_dpp v29, v250, v29 row_newbcast:1 row_mask:0xf bank_mask:0xf\n"
"v_fmac_f32_dpp v31, v252, v251 row_newbcast:3 row_mask:0xf bank_mask:0xf\n"
"v_mul_f32_dpp v50, v247, v50 row_newbcast:2 row_mask:0xf bank_mask:0xf\n"
"v_accvgpr_read_b32 v251, a227\n"
"v_mul_f32_dpp v61, v250, v61 row_newbcast:1 row_mask:0xf bank_mask:0xf\n"
"v_mfma_f32_16x16x32_bf16 v[132:135], v[180:183], a[96:99], v[196:199]\n"
"v_mul_f32_dpp v48, v247, v48 row_newbcast:0 row_mask:0xf bank_mask:0xf\n"
"v_mul_f32_dpp v62, v250, v62 row_newbcast:2 row_mask:0xf bank_mask:0xf\n"
"v_mfma_f32_16x16x32_bf16 v[132:135], v[184:187], a[100:103], v[132:135]\n"
"ds_read_b64_tr_b16 a[220:221], v237 offset:8192\n"
"v_fmac_f32_dpp v26, v252, v253 row_newbcast:2 row_mask:0xf bank_mask:0xf\n"
"v_mfma_f32_16x16x32_bf16 v[132:135], v[188:191], a[104:107], v[132:135]\n"
"ds_read_b64_tr_b16 v[96:97], v231 offset:0\n"
"v_mul_f32_dpp v30, v250, v30 row_newbcast:2 row_mask:0xf bank_mask:0xf\n"
"v_mfma_f32_16x16x32_bf16 v[132:135], v[192:195], a[108:111], v[132:135]\n"
"v_mul_f32_dpp v51, v247, v51 row_newbcast:3 row_mask:0xf bank_mask:0xf\n"
"v_fmac_f32_dpp v59, v252, v251 row_newbcast:3 row_mask:0xf bank_mask:0xf\n"
"v_mfma_f32_16x16x32_bf16 v[132:135], v[208:211], a[144:147], v[132:135]\n"
"s_waitcnt vmcnt(0)\n"
"ds_read_b64_tr_b16 v[108:109], v235 offset:8192\n"
"v_mul_f32_dpp v28, v250, v28 row_newbcast:0 row_mask:0xf bank_mask:0xf\n"
"v_mfma_f32_16x16x32_bf16 v[132:135], v[212:215], a[148:151], v[132:135]\n"
"s_waitcnt lgkmcnt(5)\n"
"v_subrev_u32 v200, s21, v200\n"
"ds_read_b64_tr_b16 a[232:233], v236 offset:10240\n"
"v_accvgpr_read_b32 v253, a214\n"
"v_fmac_f32_dpp v28, v252, v254 row_newbcast:0 row_mask:0xf bank_mask:0xf\n"
"v_fmac_f32_dpp v29, v252, v255 row_newbcast:1 row_mask:0xf bank_mask:0xf\n"
"v_accvgpr_read_b32 v254, a224\n"
"v_accvgpr_read_b32 v255, a225\n"
"v_accvgpr_read_b32 v251, a231\n"
"v_mfma_f32_16x16x32_bf16 v[132:135], v[216:219], a[152:155], v[132:135]\n"
"v_cmp_lt_u32 vcc, s69, v200\n"
"ds_read_b64_tr_b16 v[226:227], v235 offset:26624\n"
"v_mfma_f32_16x16x32_bf16 v[132:135], v[220:223], a[156:159], v[132:135]\n"
"ds_read_b64_tr_b16 v[102:103], v235 offset:2048\n"
"ds_read_b64_tr_b16 v[110:111], v235 offset:10240\n"
"v_fmac_f32_dpp v30, v252, v253 row_newbcast:2 row_mask:0xf bank_mask:0xf\n"
"v_cvt_pk_bf16_f32 v24, v24, v25\n"
"v_cvt_pk_bf16_f32 v25, v26, v27\n"
"v_accvgpr_read_b32 v253, a226\n"
"v_mul_f32_dpp v58, v250, v58 row_newbcast:2 row_mask:0xf bank_mask:0xf\n"
"v_fmac_f32_dpp v57, v252, v255 row_newbcast:1 row_mask:0xf bank_mask:0xf\n"
"v_accvgpr_read_b32 v255, a229\n"
"s_cbranch_vccnz LGV_%=\nLGVK_%=:\n"
"v_mfma_f32_16x16x32_bf16 v[148:151], v[220:223], a[172:175], v[196:199]\n"
"v_fmac_f32_dpp v56, v252, v254 row_newbcast:0 row_mask:0xf bank_mask:0xf\n"
"v_mul_f32_dpp v63, v250, v63 row_newbcast:3 row_mask:0xf bank_mask:0xf\n"
"v_mfma_f32_16x16x32_bf16 v[148:151], v[216:219], a[168:171], v[148:151]\n"
"v_add_u32_dpp v200, v200, v200 quad_perm:[1,0,3,2] row_mask:0xf bank_mask:0xf\n"
"ds_read_b64_tr_b16 v[104:105], v231 offset:8192\n"
"ds_read_b64_tr_b16 v[112:113], v231 offset:16384\n"
"v_cvt_pk_bf16_f32 v26, v28, v29\n"
"v_cvt_pk_bf16_f32 v27, v30, v31\n"
"v_fmac_f32_dpp v58, v252, v253 row_newbcast:2 row_mask:0xf bank_mask:0xf\n"
"v_accvgpr_read_b32 v253, a230\n"
"v_fmac_f32_dpp v61, v252, v255 row_newbcast:1 row_mask:0xf bank_mask:0xf\n"
"v_cvt_pk_bf16_f32 v56, v56, v57\n"
"v_cvt_pk_bf16_f32 v57, v58, v59\n"
"v_mfma_f32_16x16x32_bf16 v[148:151], v[212:215], a[164:167], v[148:151]\n"
"ds_read_b64_tr_b16 a[236:237], v237 offset:10240\n"
"v_cvt_pk_bf16_f32 v16, v16, v17\n"
"v_mfma_f32_16x16x32_bf16 v[148:151], v[208:211], a[160:163], v[148:151]\n"
"v_add_u32_dpp v200, v200, v200 quad_perm:[2,3,0,1] row_mask:0xf bank_mask:0xf\n"
"v_accvgpr_read_b32 v254, a228\n"
"v_mfma_f32_16x16x32_bf16 v[148:151], v[192:195], a[124:127], v[148:151]\n"
"ds_read_b64_tr_b16 v[98:99], v231 offset:2048\n"
"v_fmac_f32_dpp v62, v252, v253 row_newbcast:2 row_mask:0xf bank_mask:0xf\n"
"v_mfma_f32_16x16x32_bf16 v[148:151], v[188:191], a[120:123], v[148:151]\n"
"v_cmp_lt_u32 vcc, 1, v200\n"
"ds_write_b128 v243, v[24:27] offset:0\n"
"v_cvt_pk_bf16_f32 v48, v48, v49\n"
"v_fmac_f32_dpp v60, v252, v254 row_newbcast:0 row_mask:0xf bank_mask:0xf\n"
"v_fmac_f32_dpp v63, v252, v251 row_newbcast:3 row_mask:0xf bank_mask:0xf\n"
"v_cvt_pk_bf16_f32 v59, v62, v63\n"
"v_mfma_f32_16x16x32_bf16 v[148:151], v[184:187], a[116:119], v[148:151]\n"
"ds_read_b64_tr_b16 v[100:101], v235 offset:0\n"
"v_cvt_pk_bf16_f32 v58, v60, v61\n"
"v_mfma_f32_16x16x32_bf16 v[148:151], v[180:183], a[112:115], v[148:151]\n"
"ds_write_b128 v243, v[56:59] offset:2048\n"
"v_mul_f32_dpp v19, v246, v19 row_newbcast:3 row_mask:0xf bank_mask:0xf\n"
"v_mul_f32_dpp v85, v206, v85 row_newbcast:1 row_mask:0xf bank_mask:0xf\n"
"v_mfma_f32_16x16x32_bf16 v[164:167], v[180:183], a[128:131], v[196:199]\n"
"s_mov_b32 s47, 0\n"
"v_xor_b32 v249, s75, v249\n"
"v_cvt_pk_bf16_f32 v80, v80, v81\n"
"v_mfma_f32_16x16x32_bf16 v[164:167], v[184:187], a[132:135], v[164:167]\n"
"s_mov_b32 s89, vcc_lo\n"
"ds_read_b64_tr_b16 v[116:117], v235 offset:16384\n"
"ds_read_b64_tr_b16 v[118:119], v235 offset:18432\n"
"v_mfma_f32_16x16x32_bf16 v[164:167], v[188:191], a[136:139], v[164:167]\n"
"s_lshr_b32 s90, s89, s93\n"
"v_cvt_pk_bf16_f32 v49, v50, v51\n"
"v_mul_f32_dpp v83, v206, v83 row_newbcast:3 row_mask:0xf bank_mask:0xf\n"
"v_mfma_f32_16x16x32_bf16 v[164:167], v[192:195], a[140:143], v[164:167]\n"
"s_and_b32 s90, s90, 1\n"
"v_mul_f32_dpp v22, v246, v22 row_newbcast:2 row_mask:0xf bank_mask:0xf\n"
"v_cvt_pk_bf16_f32 v81, v82, v83\n"
"v_mfma_f32_16x16x32_bf16 v[164:167], v[208:211], a[176:179], v[164:167]\n"
"s_lshl_b32 s16, s61, 7\n"
"v_xor_b32 v248, s75, v248\n"
"v_cvt_pk_bf16_f32 v83, v86, v87\n"
"v_mfma_f32_16x16x32_bf16 v[164:167], v[212:215], a[180:183], v[164:167]\n"
"s_add_u32 s90, s90, s16\n"
"ds_read_b64_tr_b16 v[224:225], v235 offset:24576\n"
"v_mul_f32_dpp v53, v247, v53 row_newbcast:1 row_mask:0xf bank_mask:0xf\n"
"v_mfma_f32_16x16x32_bf16 v[164:167], v[216:219], a[184:187], v[164:167]\n"
"s_waitcnt vmcnt(63) lgkmcnt(3)\n"
"s_lshl_b32 s90, s90, 13\n"
"v_cvt_pk_bf16_f32 v82, v84, v85\n"
"v_mfma_f32_16x16x32_bf16 v[164:167], v[220:223], a[188:191], v[164:167]\n"
"s_barrier\n"
"s_lshl_b32 s16, s95, 9\n"
"ds_write_b128 v248, v[80:83] offset:51200\n"
"v_mfma_f32_16x16x32_bf16 v[136:139], v[96:99], a[96:99], v[196:199]\n"
"s_add_u32 s16, s16, s90\n"
"v_mul_f32_dpp v54, v247, v54 row_newbcast:2 row_mask:0xf bank_mask:0xf\n"
"v_cvt_pk_bf16_f32 v51, v54, v55\n"
"v_mfma_f32_16x16x32_bf16 v[136:139], v[100:103], a[100:103], v[136:139]\n"
"s_add_u32 s17, s16, 4096\n"
"ds_read_b64_tr_b16 v[212:213], v232 offset:49152\n"
"v_mul_f32_dpp v21, v246, v21 row_newbcast:1 row_mask:0xf bank_mask:0xf\n"
"v_mfma_f32_16x16x32_bf16 v[136:139], v[104:107], a[104:107], v[136:139]\n"
"s_mov_b32 s72, s61\n"
"ds_read_b64_tr_b16 v[180:181], v228 offset:32768\n"
"v_cvt_pk_bf16_f32 v50, v52, v53\n"
"v_mfma_f32_16x16x32_bf16 v[136:139], v[108:111], a[108:111], v[136:139]\n"
"s_mov_b32 s21, s60\n"
"ds_read_b64_tr_b16 a[42:43], v249 offset:51208\n"
"v_mul_f32_dpp v18, v246, v18 row_newbcast:2 row_mask:0xf bank_mask:0xf\n"
"v_mfma_f32_16x16x32_bf16 v[136:139], v[112:115], a[144:147], v[136:139]\n"
"s_waitcnt lgkmcnt(5)\n"
"s_mov_b32 s18, s77\n"
"ds_read_b64_tr_b16 v[182:183], v228 offset:34816\n"
"v_cvt_pk_bf16_f32 v17, v18, v19\n"
"v_mfma_f32_16x16x32_bf16 v[136:139], v[116:119], a[148:151], v[136:139]\n"
"s_mov_b32 s19, s77\n"
"ds_read_b64_tr_b16 a[214:215], v237 offset:12296\n"
"v_cvt_pk_bf16_f32 v19, v22, v23\n"
"v_mfma_f32_16x16x32_bf16 v[136:139], v[120:123], a[152:155], v[136:139]\n"
"s_waitcnt lgkmcnt(6)\n"
"s_mov_b32 s69, -1\n"
"ds_read_b64_tr_b16 a[40:41], v249 offset:51200\n"
"v_cvt_pk_bf16_f32 v18, v20, v21\n"
"v_mfma_f32_16x16x32_bf16 v[136:139], v[224:227], a[156:159], v[136:139]\n"
"s_mul_i32 s88, s61, 0x10011111\n"
"ds_write_b128 v248, v[16:19] offset:49152\n"
"ds_write_b128 v248, v[48:51] offset:50176\n"
"v_mfma_f32_16x16x32_bf16 v[152:155], v[224:227], a[172:175], v[196:199]\n"
"s_add_u32 s88, s88, 484675\n"
"ds_read_b64_tr_b16 a[8:9], v249 offset:49152\n"
"ds_read_b64_tr_b16 a[10:11], v249 offset:49160\n"
"v_mfma_f32_16x16x32_bf16 v[152:155], v[120:123], a[168:171], v[152:155]\n"
"s_and_b32 s88, s88, 0x70077777\n"
"ds_read_b64_tr_b16 a[24:25], v249 offset:50176\n"
"ds_read_b64_tr_b16 a[26:27], v249 offset:50184\n"
"v_mfma_f32_16x16x32_bf16 v[152:155], v[116:119], a[164:167], v[152:155]\n"
"s_or_b32 s88, s88, 0x9800000\n"
"ds_read_b64_tr_b16 v[220:221], v232 offset:57344\n"
"v_mov_b64 v[90:91], 0\n"
"v_mfma_f32_16x16x32_bf16 v[152:155], v[112:115], a[160:163], v[152:155]\n"
"s_mov_b32 s84, s6\n"
"ds_read_b64_tr_b16 v[184:185], v232 offset:32768\n"
"ds_read_b64_tr_b16 v[192:193], v232 offset:40960\n"
"v_mfma_f32_16x16x32_bf16 v[152:155], v[108:111], a[124:127], v[152:155]\n"
"s_and_b32 s85, s7, 0xffff\n"
"ds_read_b64_tr_b16 v[208:209], v228 offset:49152\n"
"v_mov_b64 v[34:35], s[22:23]\n"
"v_mfma_f32_16x16x32_bf16 v[152:155], v[104:107], a[120:123], v[152:155]\n"
"s_mov_b32 s86, 128\n"
"ds_read_b64_tr_b16 v[186:187], v232 offset:34816\n"
"ds_read_b64_tr_b16 v[216:217], v228 offset:57344\n"
"v_mfma_f32_16x16x32_bf16 v[152:155], v[100:103], a[116:119], v[152:155]\n"
"s_mov_b32 s87, 131072\n"
"ds_read_b64_tr_b16 v[188:189], v228 offset:40960\n"
"v_mov_b64 v[30:31], s[22:23]\n"
"v_mfma_f32_16x16x32_bf16 v[152:155], v[96:99], a[112:115], v[152:155]\n"
"ds_read_b64_tr_b16 a[224:225], v236 offset:14336\n"
"v_mov_b64 v[6:7], s[22:23]\n"
"v_mfma_f32_16x16x32_bf16 v[168:171], v[96:99], a[128:131], v[196:199]\n"
"ds_read_b64_tr_b16 v[190:191], v228 offset:43008\n"
"ds_read_b64_tr_b16 v[194:195], v232 offset:43008\n"
"v_mfma_f32_16x16x32_bf16 v[168:171], v[100:103], a[132:135], v[168:171]\n"
"ds_read_b64_tr_b16 v[210:211], v228 offset:51200\n"
"v_bfe_u32 v254, v242, 4, 4\n"
"v_mfma_f32_16x16x32_bf16 v[168:171], v[104:107], a[136:139], v[168:171]\n"
"ds_read_b64_tr_b16 v[218:219], v228 offset:59392\n"
"ds_read_b64_tr_b16 v[222:223], v232 offset:59392\n"
"v_mfma_f32_16x16x32_bf16 v[168:171], v[108:111], a[140:143], v[168:171]\n"
"s_waitcnt lgkmcnt(10)\n"
"ds_read_b64_tr_b16 a[208:209], v236 offset:12288\n"
"ds_read_b64_tr_b16 v[214:215], v232 offset:51200\n"
"v_mfma_f32_16x16x32_bf16 v[168:171], v[112:115], a[176:179], v[168:171]\n"
"ds_read_b64_tr_b16 v[104:105], v229 offset:40960\n"
"v_mov_b64 v[244:245], 0\n"
"v_mfma_f32_16x16x32_bf16 v[168:171], v[116:119], a[180:183], v[168:171]\n"
"ds_read_b64_tr_b16 v[100:101], v233 offset:32768\n"
"ds_read_b64_tr_b16 v[112:113], v229 offset:49152\n"
"v_mfma_f32_16x16x32_bf16 v[168:171], v[120:123], a[184:187], v[168:171]\n"
"s_add_u32 s41, s70, 327680\n"
"s_add_u32 m0, s71, 32768\n"
"ds_read_b64_tr_b16 a[210:211], v236 offset:12296\n"
"v_mfma_f32_16x16x32_bf16 v[168:171], v[224:227], a[188:191], v[168:171]\n"
"buffer_load_dwordx4 v240, s[28:31], s41 offen lds\n"
"v_mfma_f32_16x16x32_bf16 v[124:127], v[180:183], a[48:51], v[124:127]\n"
"s_waitcnt lgkmcnt(9)\n"
"v_mul_u32_u24 v254, 2560, v254\n"
"v_mfma_f32_16x16x32_bf16 v[124:127], v[184:187], a[52:55], v[124:127]\n"
"v_bfe_u32 v255, v242, 8, 1\n"
"v_mov_b64 v[44:45], s[22:23]\n"
"v_mfma_f32_16x16x32_bf16 v[124:127], v[188:191], a[56:59], v[124:127]\n"
"s_add_u32 s40, s41, s52\n"
"ds_read_b64_tr_b16 a[212:213], v237 offset:12288\n"
"ds_read_b64_tr_b16 a[226:227], v236 offset:14344\n"
"v_mfma_f32_16x16x32_bf16 v[124:127], v[192:195], a[60:63], v[124:127]\n"
"s_waitcnt lgkmcnt(6)\n"
"buffer_load_dwordx4 v240, s[28:31], s40 offen offset:1024 lds\n"
"v_mfma_f32_16x16x32_bf16 v[124:127], v[208:211], a[0:3], v[124:127]\n"
"v_mfma_f32_16x16x32_bf16 v[124:127], v[212:215], a[4:7], v[124:127]\n"
"v_lshl_add_u32 v254, v255, 5, v254\n"
"v_mov_b64 v[28:29], s[22:23]\n"
"v_mfma_f32_16x16x32_bf16 v[124:127], v[216:219], a[8:11], v[124:127]\n"
"s_add_u32 s40, s41, s53\n"
"ds_read_b64_tr_b16 v[96:97], v229 offset:32768\n"
"ds_read_b64_tr_b16 v[114:115], v229 offset:51200\n"
"v_mfma_f32_16x16x32_bf16 v[124:127], v[220:223], a[12:15], v[124:127]\n"
"buffer_load_dwordx4 v240, s[28:31], s40 offen offset:2048 lds\n"
"v_mfma_f32_16x16x32_bf16 v[140:143], v[220:223], a[28:31], v[140:143]\n"
"v_and_b32 v243, 0x3f0, v242\n"
"v_mfma_f32_16x16x32_bf16 v[140:143], v[216:219], a[24:27], v[140:143]\n"
"v_mov_b64 v[10:11], s[22:23]\n"
"v_mov_b64 v[18:19], s[22:23]\n"
"v_mfma_f32_16x16x32_bf16 v[140:143], v[212:215], a[20:23], v[140:143]\n"
"ds_read_b64_tr_b16 a[228:229], v237 offset:14336\n"
"ds_read_b64_tr_b16 v[118:119], v233 offset:51200\n"
"v_mfma_f32_16x16x32_bf16 v[140:143], v[208:211], a[16:19], v[140:143]\n"
"ds_read_b64_tr_b16 v[226:227], v233 offset:59392\n"
"v_mfma_f32_16x16x32_bf16 v[140:143], v[192:195], a[76:79], v[140:143]\n"
"ds_read_b64_tr_b16 v[102:103], v233 offset:34816\n"
"v_mov_b64 v[246:247], 0\n"
"v_mfma_f32_16x16x32_bf16 v[140:143], v[188:191], a[72:75], v[140:143]\n"
"ds_read_b64_tr_b16 a[230:231], v237 offset:14344\n"
"v_mfma_f32_16x16x32_bf16 v[140:143], v[184:187], a[68:71], v[140:143]\n"
"s_add_u32 s40, s41, s54\n"
"ds_read_b64_tr_b16 a[222:223], v237 offset:8200\n"
"ds_read_b64_tr_b16 v[116:117], v233 offset:49152\n"
"v_mfma_f32_16x16x32_bf16 v[140:143], v[180:183], a[64:67], v[140:143]\n"
"buffer_load_dwordx4 v240, s[28:31], s40 offen offset:3072 lds\n"
"v_mfma_f32_16x16x32_bf16 v[156:159], v[180:183], a[80:83], v[156:159]\n"
"s_add_u32 s41, s70, 491520\n"
"s_add_u32 m0, s71, 49152\n"
"v_xor_b32 v249, s75, v249\n"
"v_mfma_f32_16x16x32_bf16 v[156:159], v[184:187], a[84:87], v[156:159]\n"
"buffer_load_dwordx4 v240, s[28:31], s41 offen lds\n"
"v_mfma_f32_16x16x32_bf16 v[156:159], v[188:191], a[88:91], v[156:159]\n"
"v_bfe_u32 v237, v242, 4, 3\n"
"v_xor_b32 v232, s75, v232\n"
"v_mfma_f32_16x16x32_bf16 v[156:159], v[192:195], a[92:95], v[156:159]\n"
"v_mov_b64 v[38:39], s[22:23]\n"
"v_mfma_f32_16x16x32_bf16 v[156:159], v[208:211], a[32:35], v[156:159]\n"
"ds_read_b64_tr_b16 v[106:107], v229 offset:43008\n"
"ds_read_b64_tr_b16 v[108:109], v233 offset:40960\n"
"v_mfma_f32_16x16x32_bf16 v[156:159], v[212:215], a[36:39], v[156:159]\n"
"ds_read_b64_tr_b16 v[98:99], v229 offset:34816\n"
"v_subrev_f32 v126, s22, v126\n"
"v_mfma_f32_16x16x32_bf16 v[156:159], v[216:219], a[40:43], v[156:159]\n"
"s_add_u32 s40, s41, s52\n"
"ds_read_b64_tr_b16 v[110:111], v233 offset:43008\n"
"ds_read_b64_tr_b16 v[224:225], v233 offset:57344\n"
"v_mfma_f32_16x16x32_bf16 v[156:159], v[220:223], a[44:47], v[156:159]\n"
"s_waitcnt lgkmcnt(5)\n"
"buffer_load_dwordx4 v240, s[28:31], s40 offen offset:1024 lds\n"
"v_mfma_f32_16x16x32_bf16 v[172:175], v[220:223], a[228:231], v[172:175]\n"
"s_add_u32 s40, s41, s53\n"
"v_mov_b64 v[24:25], s[22:23]\n"
"v_subrev_f32 v124, s22, v124\n"
"v_mfma_f32_16x16x32_bf16 v[172:175], v[216:219], a[224:227], v[172:175]\n"
"buffer_load_dwordx4 v240, s[28:31], s40 offen offset:2048 lds\n"
"v_mfma_f32_16x16x32_bf16 v[172:175], v[212:215], a[212:215], v[172:175]\n"
"v_bfe_u32 v255, v242, 9, 1\n"
"v_subrev_f32 v143, s22, v143\n"
"v_mfma_f32_16x16x32_bf16 v[172:175], v[208:211], a[208:211], v[172:175]\n"
"v_lshl_add_u32 v254, v255, 4, v254\n"
"v_subrev_f32 v127, s22, v127\n"
"v_mfma_f32_16x16x32_bf16 v[172:175], v[192:195], a[236:239], v[172:175]\n"
"s_add_u32 s40, s41, s54\n"
"ds_read_b64_tr_b16 v[120:121], v229 offset:57344\n"
"ds_read_b64_tr_b16 v[122:123], v229 offset:59392\n"
"v_mfma_f32_16x16x32_bf16 v[172:175], v[188:191], a[232:235], v[172:175]\n"
"buffer_load_dwordx4 v240, s[28:31], s40 offen offset:3072 lds\n"
"v_mfma_f32_16x16x32_bf16 v[172:175], v[184:187], a[220:223], v[172:175]\n"
"s_lshl_b32 s62, s70, 9\n"
"v_mov_b64 v[48:49], s[22:23]\n"
"v_subrev_f32 v141, s22, v141\n"
"v_mfma_f32_16x16x32_bf16 v[172:175], v[180:183], a[216:219], v[172:175]\n"
"s_waitcnt lgkmcnt(2)\n"
"s_and_b32 s70, s88, 15\n"
"v_mov_b64 v[92:93], 0\n"
"v_subrev_f32 v140, s22, v140\n"
"v_mfma_f32_16x16x32_bf16 v[128:131], v[96:99], a[48:51], v[128:131]\n"
"s_lshr_b32 s88, s88, 4\n"
"ds_read_b64_tr_b16 v[192:193], v234 offset:40960\n"
"v_mfma_f32_16x16x32_bf16 v[128:131], v[100:103], a[52:55], v[128:131]\n"
"s_lshl_b32 s70, s70, 7\n"
"v_mov_b64 v[42:43], s[22:23]\n"
"v_mfma_f32_16x16x32_bf16 v[128:131], v[104:107], a[56:59], v[128:131]\n"
"s_xor_b32 s71, s71, s75\n"
"ds_read_b64_tr_b16 v[182:183], v230 offset:34816\n"
"v_subrev_f32 v172, s22, v172\n"
"v_mfma_f32_16x16x32_bf16 v[128:131], v[108:111], a[60:63], v[128:131]\n"
"v_lshlrev_b32_e64 v201, 2, s72\n"
"ds_read_b64_tr_b16 v[220:221], v234 offset:57344\n"
"v_subrev_f32 v125, s22, v125\n"
"v_mfma_f32_16x16x32_bf16 v[128:131], v[112:115], a[0:3], v[128:131]\n"
"global_load_dword v200, v201, s[6:7] sc1\n"
"v_mfma_f32_16x16x32_bf16 v[128:131], v[116:119], a[4:7], v[128:131]\n"
"s_waitcnt lgkmcnt(3)\n"
"v_cvt_pk_bf16_f32 v124, v124, v125\n"
"v_cvt_pk_bf16_f32 v140, v140, v141\n"
"v_mfma_f32_16x16x32_bf16 v[128:131], v[120:123], a[8:11], v[128:131]\n"
"v_xor_b32 v228, s75, v228\n"
"v_mfma_f32_16x16x32_bf16 v[128:131], v[224:227], a[12:15], v[128:131]\n"
"ds_read_b64_tr_b16 v[180:181], v230 offset:32768\n"
"v_mfma_f32_16x16x32_bf16 v[144:147], v[224:227], a[28:31], v[144:147]\n"
"v_add_u32 v253, s99, v254\n"
"v_cvt_pk_bf16_f32 v125, v126, v127\n"
"v_mfma_f32_16x16x32_bf16 v[144:147], v[120:123], a[24:27], v[144:147]\n"
"ds_read_b64_tr_b16 v[184:185], v234 offset:32768\n"
"v_mov_b32 v241, s60\n"
"v_mfma_f32_16x16x32_bf16 v[144:147], v[116:119], a[20:23], v[144:147]\n"
"v_mov_b64 v[22:23], s[22:23]\n"
"v_subrev_f32 v131, s22, v131\n"
"v_mfma_f32_16x16x32_bf16 v[144:147], v[112:115], a[16:19], v[144:147]\n"
"ds_read_b64_tr_b16 v[188:189], v230 offset:40960\n"
"v_subrev_f32 v159, s22, v159\n"
"v_mfma_f32_16x16x32_bf16 v[144:147], v[108:111], a[76:79], v[144:147]\n"
"ds_read_b64_tr_b16 v[190:191], v230 offset:43008\n"
"v_subrev_f32 v128, s22, v128\n"
"v_mfma_f32_16x16x32_bf16 v[144:147], v[104:107], a[72:75], v[144:147]\n"
"v_add_u32 v236, s94, v243\n"
"v_mfma_f32_16x16x32_bf16 v[144:147], v[100:103], a[68:71], v[144:147]\n"
"ds_read_b64_tr_b16 v[194:195], v234 offset:43008\n"
"v_max_u32 v237, 1, v237\n"
"v_mfma_f32_16x16x32_bf16 v[144:147], v[96:99], a[64:67], v[144:147]\n"
"v_mov_b64 v[40:41], s[22:23]\n"
"v_subrev_f32 v175, s22, v175\n"
"v_mfma_f32_16x16x32_bf16 v[160:163], v[96:99], a[80:83], v[160:163]\n"
"ds_read_b64_tr_b16 v[208:209], v230 offset:49152\n"
"v_mov_b64 v[0:1], s[22:23]\n"
"v_mfma_f32_16x16x32_bf16 v[160:163], v[100:103], a[84:87], v[160:163]\n"
"ds_read_b64_tr_b16 v[222:223], v234 offset:59392\n"
"v_subrev_f32 v130, s22, v130\n"
"v_mfma_f32_16x16x32_bf16 v[160:163], v[104:107], a[88:91], v[160:163]\n"
"v_subrev_f32 v129, s22, v129\n"
"v_cvt_pk_bf16_f32 v127, v130, v131\n"
"v_mfma_f32_16x16x32_bf16 v[160:163], v[108:111], a[92:95], v[160:163]\n"
"ds_read_b64_tr_b16 v[214:215], v234 offset:51200\n"
"v_subrev_f32 v142, s22, v142\n"
"v_mfma_f32_16x16x32_bf16 v[160:163], v[112:115], a[32:35], v[160:163]\n"
"ds_read_b64_tr_b16 v[210:211], v230 offset:51200\n"
"v_cvt_pk_bf16_f32 v126, v128, v129\n"
"v_mfma_f32_16x16x32_bf16 v[160:163], v[116:119], a[36:39], v[160:163]\n"
"v_mov_b64 v[8:9], s[22:23]\n"
"v_subrev_f32 v174, s22, v174\n"
"v_mfma_f32_16x16x32_bf16 v[160:163], v[120:123], a[40:43], v[160:163]\n"
"ds_read_b64_tr_b16 v[186:187], v234 offset:34816\n"
"v_lshl_add_u32 v243, s44, 2, v243\n"
"v_mfma_f32_16x16x32_bf16 v[160:163], v[224:227], a[44:47], v[160:163]\n"
"buffer_store_dwordx4 v[124:127], v243, s[32:35], s16 offen sc1\n"
"v_subrev_f32 v157, s22, v157\n"
"v_mfma_f32_16x16x32_bf16 v[176:179], v[224:227], a[228:231], v[176:179]\n"
"ds_read_b64_tr_b16 v[212:213], v234 offset:49152\n"
"v_subrev_f32 v144, s22, v144\n"
"v_mfma_f32_16x16x32_bf16 v[176:179], v[120:123], a[224:227], v[176:179]\n"
"v_mov_b64 v[88:89], 0\n"
"v_subrev_f32 v173, s22, v173\n"
"v_mfma_f32_16x16x32_bf16 v[176:179], v[116:119], a[212:215], v[176:179]\n"
"ds_read_b64_tr_b16 v[216:217], v230 offset:57344\n"
"v_mov_b64 v[4:5], s[22:23]\n"
"v_mfma_f32_16x16x32_bf16 v[176:179], v[112:115], a[208:211], v[176:179]\n"
"ds_read_b64_tr_b16 v[218:219], v230 offset:59392\n"
"v_cvt_pk_bf16_f32 v172, v172, v173\n"
"v_mfma_f32_16x16x32_bf16 v[176:179], v[108:111], a[236:239], v[176:179]\n"
"ds_read_b64_tr_b16 v[118:119], v235 offset:51200\n"
"v_mfma_f32_16x16x32_bf16 v[176:179], v[104:107], a[232:235], v[176:179]\n"
"v_subrev_f32 v158, s22, v158\n"
"v_subrev_f32 v145, s22, v145\n"
"v_mfma_f32_16x16x32_bf16 v[176:179], v[100:103], a[220:223], v[176:179]\n"
"ds_read_b64_tr_b16 v[120:121], v231 offset:57344\n"
"v_cvt_pk_bf16_f32 v141, v142, v143\n"
"v_mfma_f32_16x16x32_bf16 v[176:179], v[96:99], a[216:219], v[176:179]\n"
"s_waitcnt lgkmcnt(5)\n"
"v_mov_b64 v[54:55], s[22:23]\n"
"v_cvt_pk_bf16_f32 v142, v144, v145\n"
"v_mfma_f32_16x16x32_bf16 v[132:135], v[180:183], a[48:51], v[132:135]\n"
"ds_read_b64_tr_b16 v[104:105], v231 offset:40960\n"
"v_subrev_f32 v147, s22, v147\n"
"v_mfma_f32_16x16x32_bf16 v[132:135], v[184:187], a[52:55], v[132:135]\n"
"v_subrev_f32 v146, s22, v146\n"
"v_mfma_f32_16x16x32_bf16 v[132:135], v[188:191], a[56:59], v[132:135]\n"
"ds_read_b64_tr_b16 v[102:103], v235 offset:34816\n"
"v_cvt_pk_bf16_f32 v143, v146, v147\n"
"v_mfma_f32_16x16x32_bf16 v[132:135], v[192:195], a[60:63], v[132:135]\n"
"ds_read_b64_tr_b16 v[114:115], v231 offset:51200\n"
"v_subrev_f32 v163, s22, v163\n"
"v_mfma_f32_16x16x32_bf16 v[132:135], v[208:211], a[0:3], v[132:135]\n"
"s_waitcnt lgkmcnt(4)\n"
"v_mov_b64 v[26:27], s[22:23]\n"
"v_mov_b64 v[50:51], s[22:23]\n"
"v_mfma_f32_16x16x32_bf16 v[132:135], v[212:215], a[4:7], v[132:135]\n"
"ds_read_b64_tr_b16 v[108:109], v235 offset:40960\n"
"v_add_u32 v238, s97, v254\n"
"v_mfma_f32_16x16x32_bf16 v[132:135], v[216:219], a[8:11], v[132:135]\n"
"v_xor_b32 v248, s75, v248\n"
"v_xor_b32 v237, s61, v237\n"
"v_mfma_f32_16x16x32_bf16 v[132:135], v[220:223], a[12:15], v[132:135]\n"
"buffer_store_dwordx4 v[140:143], v243, s[32:35], s16 offen offset:2048 sc1\n"
"ds_read_b64_tr_b16 v[96:97], v231 offset:32768\n"
"v_mfma_f32_16x16x32_bf16 v[148:151], v[220:223], a[28:31], v[148:151]\n"
"v_subrev_f32 v156, s22, v156\n"
"v_cvt_pk_bf16_f32 v156, v156, v157\n"
"v_mfma_f32_16x16x32_bf16 v[148:151], v[216:219], a[24:27], v[148:151]\n"
"ds_read_b64_tr_b16 v[116:117], v235 offset:49152\n"
"v_cvt_pk_bf16_f32 v157, v158, v159\n"
"v_mfma_f32_16x16x32_bf16 v[148:151], v[212:215], a[20:23], v[148:151]\n"
"ds_read_b64_tr_b16 v[106:107], v231 offset:43008\n"
"v_subrev_f32 v133, s22, v133\n"
"v_mfma_f32_16x16x32_bf16 v[148:151], v[208:211], a[16:19], v[148:151]\n"
"v_subrev_f32 v161, s22, v161\n"
"v_xor_b32 v229, s75, v229\n"
"v_mfma_f32_16x16x32_bf16 v[148:151], v[192:195], a[76:79], v[148:151]\n"
"ds_read_b64_tr_b16 v[100:101], v235 offset:32768\n"
"v_mfma_f32_16x16x32_bf16 v[148:151], v[188:191], a[72:75], v[148:151]\n"
"ds_read_b64_tr_b16 v[98:99], v231 offset:34816\n"
"v_mov_b64 v[32:33], s[22:23]\n"
"v_mfma_f32_16x16x32_bf16 v[148:151], v[184:187], a[68:71], v[148:151]\n"
"ds_read_b64_tr_b16 v[122:123], v231 offset:59392\n"
"v_subrev_f32 v160, s22, v160\n"
"v_mfma_f32_16x16x32_bf16 v[148:151], v[180:183], a[64:67], v[148:151]\n"
"ds_read_b64_tr_b16 v[110:111], v235 offset:43008\n"
"v_subrev_f32 v132, s22, v132\n"
"v_mfma_f32_16x16x32_bf16 v[164:167], v[180:183], a[80:83], v[164:167]\n"
"v_mov_b64 v[46:47], s[22:23]\n"
"v_subrev_f32 v162, s22, v162\n"
"v_mfma_f32_16x16x32_bf16 v[164:167], v[184:187], a[84:87], v[164:167]\n"
"ds_read_b64_tr_b16 v[112:113], v231 offset:49152\n"
"v_cvt_pk_bf16_f32 v132, v132, v133\n"
"v_mfma_f32_16x16x32_bf16 v[164:167], v[188:191], a[88:91], v[164:167]\n"
"v_mov_b64 v[94:95], 0\n"
"v_cvt_pk_bf16_f32 v159, v162, v163\n"
"v_mfma_f32_16x16x32_bf16 v[164:167], v[192:195], a[92:95], v[164:167]\n"
"ds_read_b64_tr_b16 v[226:227], v235 offset:59392\n"
"v_mfma_f32_16x16x32_bf16 v[164:167], v[208:211], a[32:35], v[164:167]\n"
"v_subrev_f32 v176, s22, v176\n"
"v_mfma_f32_16x16x32_bf16 v[164:167], v[212:215], a[36:39], v[164:167]\n"
"ds_read_b64_tr_b16 v[224:225], v235 offset:57344\n"
"v_mov_b64 v[16:17], s[22:23]\n"
"v_mfma_f32_16x16x32_bf16 v[164:167], v[216:219], a[40:43], v[164:167]\n"
"v_cvt_pk_bf16_f32 v173, v174, v175\n"
"v_subrev_f32 v177, s22, v177\n"
"v_mfma_f32_16x16x32_bf16 v[164:167], v[220:223], a[44:47], v[164:167]\n"
"s_barrier\n"
"s_waitcnt lgkmcnt(3)\n"
"v_mfma_f32_16x16x32_bf16 v[136:139], v[96:99], a[48:51], v[136:139]\n"
"ds_read_b64_tr_b16 v[218:219], v228 offset:26624\n"
"v_subrev_f32 v135, s22, v135\n"
"v_mfma_f32_16x16x32_bf16 v[136:139], v[100:103], a[52:55], v[136:139]\n"
"ds_read_b64_tr_b16 v[210:211], v228 offset:18432\n"
"v_cvt_pk_bf16_f32 v158, v160, v161\n"
"v_mfma_f32_16x16x32_bf16 v[136:139], v[104:107], a[56:59], v[136:139]\n"
"ds_read_b64_tr_b16 v[182:183], v228 offset:2048\n"
"v_subrev_f32 v134, s22, v134\n"
"v_mfma_f32_16x16x32_bf16 v[136:139], v[108:111], a[60:63], v[136:139]\n"
"s_waitcnt lgkmcnt(4)\n"
"ds_read_b64_tr_b16 v[192:193], v232 offset:8192\n"
"v_mov_b64 v[36:37], s[22:23]\n"
"v_mfma_f32_16x16x32_bf16 v[136:139], v[112:115], a[0:3], v[136:139]\n"
"ds_read_b64_tr_b16 v[208:209], v228 offset:16384\n"
"v_mov_b64 v[84:85], 0\n"
"v_mfma_f32_16x16x32_bf16 v[136:139], v[116:119], a[4:7], v[136:139]\n"
"ds_read_b64_tr_b16 v[180:181], v228 offset:0\n"
"v_cvt_pk_bf16_f32 v174, v176, v177\n"
"v_mfma_f32_16x16x32_bf16 v[136:139], v[120:123], a[8:11], v[136:139]\n"
"s_waitcnt lgkmcnt(6)\n"
"ds_read_b64_tr_b16 v[216:217], v228 offset:24576\n"
"v_mov_b64 v[14:15], s[22:23]\n"
"v_mfma_f32_16x16x32_bf16 v[136:139], v[224:227], a[12:15], v[136:139]\n"
"buffer_store_dwordx4 v[156:159], v243, s[32:35], s17 offen sc1\n"
"ds_read_b64_tr_b16 v[194:195], v232 offset:10240\n"
"v_mfma_f32_16x16x32_bf16 v[152:155], v[224:227], a[28:31], v[152:155]\n"
"ds_read_b64_tr_b16 v[212:213], v232 offset:16384\n"
"v_lshlrev_b32 v237, 2, v237\n"
"v_mfma_f32_16x16x32_bf16 v[152:155], v[120:123], a[24:27], v[152:155]\n"
"ds_read_b64_tr_b16 v[190:191], v228 offset:10240\n"
"v_mov_b64 v[52:53], s[22:23]\n"
"v_mfma_f32_16x16x32_bf16 v[152:155], v[116:119], a[20:23], v[152:155]\n"
"ds_read_b64_tr_b16 v[184:185], v232 offset:0\n"
"v_xor_b32 v233, s75, v233\n"
"v_mfma_f32_16x16x32_bf16 v[152:155], v[112:115], a[16:19], v[152:155]\n"
"ds_read_b64_tr_b16 v[220:221], v232 offset:24576\n"
"v_subrev_f32 v179, s22, v179\n"
"v_mfma_f32_16x16x32_bf16 v[152:155], v[108:111], a[76:79], v[152:155]\n"
"ds_read_b64_tr_b16 v[188:189], v228 offset:8192\n"
"v_xor_b32 v239, 64, v238\n"
"v_mfma_f32_16x16x32_bf16 v[152:155], v[104:107], a[72:75], v[152:155]\n"
"ds_read_b64_tr_b16 v[214:215], v232 offset:18432\n"
"v_add_u32 v252, s98, v254\n"
"v_mfma_f32_16x16x32_bf16 v[152:155], v[100:103], a[68:71], v[152:155]\n"
"ds_read_b64_tr_b16 v[186:187], v232 offset:2048\n"
"ds_read_b64_tr_b16 v[222:223], v232 offset:26624\n"
"v_mfma_f32_16x16x32_bf16 v[152:155], v[96:99], a[64:67], v[152:155]\n"
"v_mov_b64 v[12:13], s[22:23]\n"
"v_mov_b64 v[86:87], 0\n"
"v_mfma_f32_16x16x32_bf16 v[168:171], v[96:99], a[80:83], v[168:171]\n"
"ds_read_b64_tr_b16 v[96:97], v229 offset:0\n"
"ds_read_b64_tr_b16 v[98:99], v229 offset:2048\n"
"v_mfma_f32_16x16x32_bf16 v[168:171], v[100:103], a[84:87], v[168:171]\n"
"ds_read_b64_tr_b16 v[100:101], v233 offset:0\n"
"ds_read_b64_tr_b16 v[102:103], v233 offset:2048\n"
"v_mfma_f32_16x16x32_bf16 v[168:171], v[104:107], a[88:91], v[168:171]\n"
"ds_read_b64_tr_b16 v[104:105], v229 offset:8192\n"
"v_add_u32 v241, 1, v241\n"
"v_mfma_f32_16x16x32_bf16 v[168:171], v[108:111], a[92:95], v[168:171]\n"
"v_subrev_f32 v178, s22, v178\n"
"v_mfma_f32_16x16x32_bf16 v[168:171], v[112:115], a[32:35], v[168:171]\n"
"v_mov_b64 v[20:21], s[22:23]\n"
"v_cvt_pk_bf16_f32 v175, v178, v179\n"
"v_mfma_f32_16x16x32_bf16 v[168:171], v[116:119], a[36:39], v[168:171]\n"
"v_mov_b64 v[2:3], s[22:23]\n"
"v_mfma_f32_16x16x32_bf16 v[168:171], v[120:123], a[40:43], v[168:171]\n"
"s_add_u32 s47, s47, 1\n"
"v_mfma_f32_16x16x32_bf16 v[168:171], v[224:227], a[44:47], v[168:171]\n"
"s_waitcnt lgkmcnt(5)\n"
"v_lshl_add_u32 v248, v242, 8, s93\n"
"LGL_%=:\n"
"v_mfma_f32_16x16x32_bf16 v[124:127], v[180:183], a[96:99], v[196:199]\n"
"ds_read_b64_tr_b16 v[106:107], v229 offset:10240\n"
"v_cvt_pk_bf16_f32 v133, v134, v135\n"
"v_mfma_f32_16x16x32_bf16 v[124:127], v[184:187], a[100:103], v[124:127]\n"
"ds_read_b64_tr_b16 v[108:109], v233 offset:8192\n"
"v_subrev_f32 v136, s22, v136\n"
"v_mfma_f32_16x16x32_bf16 v[124:127], v[188:191], a[104:107], v[124:127]\n"
"ds_read_b64_tr_b16 v[110:111], v233 offset:10240\n"
"v_subrev_f32 v137, s22, v137\n"
"v_mfma_f32_16x16x32_bf16 v[124:127], v[192:195], a[108:111], v[124:127]\n"
"ds_read_b64_tr_b16 v[112:113], v229 offset:16384\n"
"v_subrev_f32 v138, s22, v138\n"
"v_mfma_f32_16x16x32_bf16 v[124:127], v[208:211], a[144:147], v[124:127]\n"
"ds_read_b64_tr_b16 v[114:115], v229 offset:18432\n"
"v_subrev_f32 v139, s22, v139\n"
"v_mfma_f32_16x16x32_bf16 v[124:127], v[212:215], a[148:151], v[124:127]\n"
"s_waitcnt lgkmcnt(7)\n"
"ds_read_b64_tr_b16 v[116:117], v233 offset:16384\n"
"v_cvt_pk_bf16_f32 v134, v136, v137\n"
"v_mfma_f32_16x16x32_bf16 v[124:127], v[216:219], a[152:155], v[124:127]\n"
"ds_read_b64_tr_b16 v[118:119], v233 offset:18432\n"
"v_cvt_pk_bf16_f32 v135, v138, v139\n"
"v_mfma_f32_16x16x32_bf16 v[124:127], v[220:223], a[156:159], v[124:127]\n"
"buffer_store_dwordx4 v[172:175], v243, s[32:35], s17 offen offset:2048 sc1\n"
"v_subrev_f32 v148, s22, v148\n"
"v_mfma_f32_16x16x32_bf16 v[140:143], v[220:223], a[172:175], v[196:199]\n"
"ds_read_b64_tr_b16 v[120:121], v229 offset:24576\n"
"v_subrev_f32 v149, s22, v149\n"
"v_mfma_f32_16x16x32_bf16 v[140:143], v[216:219], a[168:171], v[140:143]\n"
"ds_read_b64_tr_b16 v[122:123], v229 offset:26624\n"
"v_subrev_f32 v150, s22, v150\n"
"v_mfma_f32_16x16x32_bf16 v[140:143], v[212:215], a[164:167], v[140:143]\n"
"ds_read_b64_tr_b16 v[224:225], v233 offset:24576\n"
"v_subrev_f32 v151, s22, v151\n"
"v_mfma_f32_16x16x32_bf16 v[140:143], v[208:211], a[160:163], v[140:143]\n"
"ds_read_b64_tr_b16 v[226:227], v233 offset:26624\n"
"v_cvt_pk_bf16_f32 v148, v148, v149\n"
"v_mfma_f32_16x16x32_bf16 v[140:143], v[192:195], a[124:127], v[140:143]\n"
"v_cvt_pk_bf16_f32 v149, v150, v151\n"
"v_subrev_f32 v152, s22, v152\n"
"v_mfma_f32_16x16x32_bf16 v[140:143], v[188:191], a[120:123], v[140:143]\n"
"s_add_u32 s41, s70, 0\n"
"v_subrev_f32 v153, s22, v153\n"
"v_subrev_f32 v154, s22, v154\n"
"v_mfma_f32_16x16x32_bf16 v[140:143], v[184:187], a[116:119], v[140:143]\n"
"s_mov_b32 m0, s71\n"
"v_subrev_f32 v155, s22, v155\n"
"v_cvt_pk_bf16_f32 v150, v152, v153\n"
"v_mfma_f32_16x16x32_bf16 v[140:143], v[180:183], a[112:115], v[140:143]\n"
"s_waitcnt vmcnt(14)\n"
"buffer_load_dwordx4 v240, s[28:31], s41 offen lds\n"
"v_mfma_f32_16x16x32_bf16 v[156:159], v[180:183], a[128:131], v[196:199]\n"
"v_xor_b32 v230, s75, v230\n"
"v_cvt_pk_bf16_f32 v151, v154, v155\n"
"v_mfma_f32_16x16x32_bf16 v[156:159], v[184:187], a[132:135], v[156:159]\n"
"v_xor_b32 v234, s75, v234\n"
"v_subrev_f32 v164, s22, v164\n"
"v_mfma_f32_16x16x32_bf16 v[156:159], v[188:191], a[136:139], v[156:159]\n"
"v_subrev_f32 v165, s22, v165\n"
"v_subrev_f32 v166, s22, v166\n"
"v_mfma_f32_16x16x32_bf16 v[156:159], v[192:195], a[140:143], v[156:159]\n"
"v_subrev_f32 v167, s22, v167\n"
"v_cvt_pk_bf16_f32 v164, v164, v165\n"
"v_mfma_f32_16x16x32_bf16 v[156:159], v[208:211], a[176:179], v[156:159]\n"
"v_cvt_pk_bf16_f32 v165, v166, v167\n"
"v_subrev_f32 v168, s22, v168\n"
"v_mfma_f32_16x16x32_bf16 v[156:159], v[212:215], a[180:183], v[156:159]\n"
"v_subrev_f32 v169, s22, v169\n"
"v_subrev_f32 v170, s22, v170\n"
"v_mfma_f32_16x16x32_bf16 v[156:159], v[216:219], a[184:187], v[156:159]\n"
"v_subrev_f32 v171, s22, v171\n"
"v_cvt_pk_bf16_f32 v166, v168, v169\n"
"v_mfma_f32_16x16x32_bf16 v[156:159], v[220:223], a[188:191], v[156:159]\n"
"buffer_store_dwordx4 v[132:135], v243, s[32:35], s16 offen offset:1024 sc1\n"
"v_cvt_pk_bf16_f32 v167, v170, v171\n"
"v_mfma_f32_16x16x32_bf16 v[172:175], v[220:223], a[252:255], v[196:199]\n"
"ds_read_b64_tr_b16 v[220:221], v234 offset:24576\n"
"v_xor_b32 v231, s75, v231\n"
"v_mfma_f32_16x16x32_bf16 v[172:175], v[216:219], a[248:251], v[172:175]\n"
"ds_read_b64_tr_b16 v[216:217], v230 offset:24576\n"
"v_xor_b32 v235, s75, v235\n"
"v_mfma_f32_16x16x32_bf16 v[172:175], v[212:215], a[244:247], v[172:175]\n"
"ds_read_b64_tr_b16 v[212:213], v234 offset:16384\n"
"v_lshlrev_b32 v202, 16, v84\n"
"v_mfma_f32_16x16x32_bf16 v[172:175], v[208:211], a[240:243], v[172:175]\n"
"ds_read_b64_tr_b16 v[208:209], v230 offset:16384\n"
"v_and_b32 v84, s73, v84\n"
"v_mfma_f32_16x16x32_bf16 v[172:175], v[192:195], a[204:207], v[172:175]\n"
"ds_read_b64_tr_b16 v[192:193], v234 offset:8192\n"
"v_add_f32 v25, v25, v84\n"
"v_mfma_f32_16x16x32_bf16 v[172:175], v[188:191], a[200:203], v[172:175]\n"
"ds_read_b64_tr_b16 v[188:189], v230 offset:8192\n"
"v_lshlrev_b32 v203, 16, v85\n"
"v_mfma_f32_16x16x32_bf16 v[172:175], v[184:187], a[196:199], v[172:175]\n"
"s_add_u32 s40, s41, s52\n"
"ds_read_b64_tr_b16 v[184:185], v234 offset:0\n"
"v_and_b32 v85, s73, v85\n"
"v_mfma_f32_16x16x32_bf16 v[172:175], v[180:183], a[192:195], v[172:175]\n"
"s_waitcnt vmcnt(13) lgkmcnt(7)\n"
"buffer_load_dwordx4 v240, s[28:31], s40 offen offset:1024 lds\n"
"v_mfma_f32_16x16x32_bf16 v[128:131], v[96:99], a[96:99], v[196:199]\n"
"s_waitcnt lgkmcnt(6)\n"
"ds_read_b64_tr_b16 v[180:181], v230 offset:0\n"
"v_add_f32 v27, v27, v85\n"
"v_mfma_f32_16x16x32_bf16 v[128:131], v[100:103], a[100:103], v[128:131]\n"
"ds_read_b64_tr_b16 v[182:183], v230 offset:2048\n"
"v_lshlrev_b32 v204, 16, v86\n"
"v_mfma_f32_16x16x32_bf16 v[128:131], v[104:107], a[104:107], v[128:131]\n"
"ds_read_b64_tr_b16 v[186:187], v234 offset:2048\n"
"v_and_b32 v86, s73, v86\n"
"v_mfma_f32_16x16x32_bf16 v[128:131], v[108:111], a[108:111], v[128:131]\n"
"ds_read_b64_tr_b16 v[190:191], v230 offset:10240\n"
"v_add_f32 v29, v29, v86\n"
"v_mfma_f32_16x16x32_bf16 v[128:131], v[112:115], a[144:147], v[128:131]\n"
"s_waitcnt vmcnt(6)\n"
"ds_read_b64_tr_b16 v[194:195], v234 offset:10240\n"
"v_lshlrev_b32 v205, 16, v87\n"
"v_mfma_f32_16x16x32_bf16 v[128:131], v[116:119], a[148:151], v[128:131]\n"
"v_subrev_u32 v200, s21, v200\n"
"ds_read_b64_tr_b16 v[210:211], v230 offset:18432\n"
"v_mfma_f32_16x16x32_bf16 v[128:131], v[120:123], a[152:155], v[128:131]\n"
"v_cmp_lt_u32 vcc, s69, v200\n"
"ds_read_b64_tr_b16 v[214:215], v234 offset:18432\n"
"v_mfma_f32_16x16x32_bf16 v[128:131], v[224:227], a[156:159], v[128:131]\n"
"ds_read_b64_tr_b16 v[218:219], v230 offset:26624\n"
"s_cbranch_vccnz LGP_%=\nLGPK_%=:\n"
"v_mfma_f32_16x16x32_bf16 v[144:147], v[224:227], a[172:175], v[196:199]\n"
"s_sub_u32 s72, s72, 1\n"
"ds_read_b64_tr_b16 v[222:223], v234 offset:26624\n"
"v_and_b32 v87, s73, v87\n"
"v_mfma_f32_16x16x32_bf16 v[144:147], v[120:123], a[168:171], v[144:147]\n"
"s_and_b32 s72, s72, 7\n"
"v_add_f32 v24, v24, v202\n"
"v_add_f32 v31, v31, v87\n"
"v_mfma_f32_16x16x32_bf16 v[144:147], v[116:119], a[164:167], v[144:147]\n"
"v_add_f32 v26, v26, v203\n"
"v_lshlrev_b32 v206, 16, v88\n"
"v_mfma_f32_16x16x32_bf16 v[144:147], v[112:115], a[160:163], v[144:147]\n"
"buffer_load_dwordx4 v[72:75], v243, s[32:35], s18 offen sc1\n"
"v_mfma_f32_16x16x32_bf16 v[144:147], v[108:111], a[124:127], v[144:147]\n"
"v_and_b32 v88, s73, v88\n"
"v_add_f32 v33, v33, v88\n"
"v_mfma_f32_16x16x32_bf16 v[144:147], v[104:107], a[120:123], v[144:147]\n"
"v_lshlrev_b32 v207, 16, v89\n"
"v_and_b32 v89, s73, v89\n"
"v_mfma_f32_16x16x32_bf16 v[144:147], v[100:103], a[116:119], v[144:147]\n"
"v_add_f32 v35, v35, v89\n"
"v_lshlrev_b32 v202, 16, v90\n"
"v_mfma_f32_16x16x32_bf16 v[144:147], v[96:99], a[112:115], v[144:147]\n"
"buffer_store_dwordx4 v[148:151], v243, s[32:35], s16 offen offset:3072 sc1\n"
"v_and_b32 v90, s73, v90\n"
"v_mfma_f32_16x16x32_bf16 v[160:163], v[96:99], a[128:131], v[196:199]\n"
"v_add_f32 v37, v37, v90\n"
"v_lshlrev_b32 v203, 16, v91\n"
"v_mfma_f32_16x16x32_bf16 v[160:163], v[100:103], a[132:135], v[160:163]\n"
"v_and_b32 v91, s73, v91\n"
"v_add_f32 v39, v39, v91\n"
"v_mfma_f32_16x16x32_bf16 v[160:163], v[104:107], a[136:139], v[160:163]\n"
"v_add_f32 v28, v28, v204\n"
"v_add_f32 v30, v30, v205\n"
"v_mfma_f32_16x16x32_bf16 v[160:163], v[108:111], a[140:143], v[160:163]\n"
"v_add_f32 v32, v32, v206\n"
"v_add_f32 v34, v34, v207\n"
"v_mfma_f32_16x16x32_bf16 v[160:163], v[112:115], a[176:179], v[160:163]\n"
"v_lshlrev_b32 v204, 16, v92\n"
"v_and_b32 v92, s73, v92\n"
"v_mfma_f32_16x16x32_bf16 v[160:163], v[116:119], a[180:183], v[160:163]\n"
"v_add_f32 v41, v41, v92\n"
"v_lshlrev_b32 v205, 16, v93\n"
"v_mfma_f32_16x16x32_bf16 v[160:163], v[120:123], a[184:187], v[160:163]\n"
"s_add_u32 s40, s41, s53\n"
"v_and_b32 v93, s73, v93\n"
"v_add_f32 v43, v43, v93\n"
"v_mfma_f32_16x16x32_bf16 v[160:163], v[224:227], a[188:191], v[160:163]\n"
"buffer_load_dwordx4 v240, s[28:31], s40 offen offset:2048 lds\n"
"v_mfma_f32_16x16x32_bf16 v[176:179], v[224:227], a[252:255], v[196:199]\n"
"ds_read_b64_tr_b16 v[224:225], v235 offset:24576\n"
"v_lshlrev_b32 v206, 16, v94\n"
"v_mfma_f32_16x16x32_bf16 v[176:179], v[120:123], a[248:251], v[176:179]\n"
"ds_read_b64_tr_b16 v[120:121], v231 offset:24576\n"
"v_and_b32 v94, s73, v94\n"
"v_mfma_f32_16x16x32_bf16 v[176:179], v[116:119], a[244:247], v[176:179]\n"
"ds_read_b64_tr_b16 v[116:117], v235 offset:16384\n"
"v_add_f32 v45, v45, v94\n"
"v_mfma_f32_16x16x32_bf16 v[176:179], v[112:115], a[240:243], v[176:179]\n"
"buffer_load_dwordx4 v[76:79], v243, s[32:35], s18 offen offset:1024 sc1\n"
"v_mfma_f32_16x16x32_bf16 v[176:179], v[108:111], a[204:207], v[176:179]\n"
"ds_read_b64_tr_b16 v[108:109], v235 offset:8192\n"
"v_lshlrev_b32 v207, 16, v95\n"
"v_mfma_f32_16x16x32_bf16 v[176:179], v[104:107], a[200:203], v[176:179]\n"
"ds_read_b64_tr_b16 v[104:105], v231 offset:8192\n"
"v_and_b32 v95, s73, v95\n"
"v_mfma_f32_16x16x32_bf16 v[176:179], v[100:103], a[196:199], v[176:179]\n"
"s_add_u32 s40, s41, s54\n"
"ds_read_b64_tr_b16 v[100:101], v235 offset:0\n"
"v_add_f32 v47, v47, v95\n"
"v_mfma_f32_16x16x32_bf16 v[176:179], v[96:99], a[192:195], v[176:179]\n"
"s_waitcnt lgkmcnt(6)\n"
"buffer_load_dwordx4 v240, s[28:31], s40 offen offset:3072 lds\n"
"v_mfma_f32_16x16x32_bf16 v[132:135], v[180:183], a[96:99], v[196:199]\n"
"ds_read_b64_tr_b16 v[96:97], v231 offset:0\n"
"v_add_f32 v36, v36, v202\n"
"v_mfma_f32_16x16x32_bf16 v[132:135], v[184:187], a[100:103], v[132:135]\n"
"ds_read_b64_tr_b16 v[98:99], v231 offset:2048\n"
"v_add_f32 v38, v38, v203\n"
"v_mfma_f32_16x16x32_bf16 v[132:135], v[188:191], a[104:107], v[132:135]\n"
"ds_read_b64_tr_b16 v[102:103], v235 offset:2048\n"
"v_add_f32 v44, v44, v206\n"
"v_mfma_f32_16x16x32_bf16 v[132:135], v[192:195], a[108:111], v[132:135]\n"
"ds_read_b64_tr_b16 v[106:107], v231 offset:10240\n"
"v_add_f32 v46, v46, v207\n"
"v_mfma_f32_16x16x32_bf16 v[132:135], v[208:211], a[144:147], v[132:135]\n"
"ds_read_b64_tr_b16 v[110:111], v235 offset:10240\n"
"v_add_f32 v40, v40, v204\n"
"v_mfma_f32_16x16x32_bf16 v[132:135], v[212:215], a[148:151], v[132:135]\n"
"ds_read_b64_tr_b16 v[112:113], v231 offset:16384\n"
"v_add_f32 v42, v42, v205\n"
"v_mfma_f32_16x16x32_bf16 v[132:135], v[216:219], a[152:155], v[132:135]\n"
"ds_read_b64_tr_b16 v[114:115], v231 offset:18432\n"
"v_mfma_f32_16x16x32_bf16 v[132:135], v[220:223], a[156:159], v[132:135]\n"
"buffer_store_dwordx4 v[164:167], v243, s[32:35], s17 offen offset:1024 sc1\n"
"v_lshlrev_b32 v206, 16, v244\n"
"v_mfma_f32_16x16x32_bf16 v[148:151], v[220:223], a[172:175], v[196:199]\n"
"ds_read_b64_tr_b16 v[118:119], v235 offset:18432\n"
"v_and_b32 v244, s73, v244\n"
"v_mfma_f32_16x16x32_bf16 v[148:151], v[216:219], a[168:171], v[148:151]\n"
"ds_read_b64_tr_b16 v[122:123], v231 offset:26624\n"
"v_add_f32 v49, v49, v244\n"
"v_mfma_f32_16x16x32_bf16 v[148:151], v[212:215], a[164:167], v[148:151]\n"
"ds_read_b64_tr_b16 v[226:227], v235 offset:26624\n"
"v_lshlrev_b32 v207, 16, v245\n"
"v_mfma_f32_16x16x32_bf16 v[148:151], v[208:211], a[160:163], v[148:151]\n"
"buffer_load_dwordx4 v[80:83], v243, s[32:35], s18 offen offset:2048 sc1\n"
"v_mfma_f32_16x16x32_bf16 v[148:151], v[192:195], a[124:127], v[148:151]\n"
"v_and_b32 v245, s73, v245\n"
"v_add_f32 v51, v51, v245\n"
"v_mfma_f32_16x16x32_bf16 v[148:151], v[188:191], a[120:123], v[148:151]\n"
"s_add_u32 s41, s70, 163840\n"
"v_lshlrev_b32 v202, 16, v246\n"
"v_and_b32 v246, s73, v246\n"
"v_mfma_f32_16x16x32_bf16 v[148:151], v[184:187], a[116:119], v[148:151]\n"
"s_add_u32 m0, s71, 16384\n"
"v_add_f32 v53, v53, v246\n"
"v_lshlrev_b32 v203, 16, v247\n"
"v_mfma_f32_16x16x32_bf16 v[148:151], v[180:183], a[112:115], v[148:151]\n"
"buffer_load_dwordx4 v240, s[28:31], s41 offen lds\n"
"v_mfma_f32_16x16x32_bf16 v[164:167], v[180:183], a[128:131], v[196:199]\n"
"v_and_b32 v247, s73, v247\n"
"v_add_f32 v55, v55, v247\n"
"v_mfma_f32_16x16x32_bf16 v[164:167], v[184:187], a[132:135], v[164:167]\n"
"v_add_f32 v48, v48, v206\n"
"v_add_f32 v50, v50, v207\n"
"v_mfma_f32_16x16x32_bf16 v[164:167], v[188:191], a[136:139], v[164:167]\n"
"v_add_f32 v52, v52, v202\n"
"v_add_f32 v54, v54, v203\n"
"v_mfma_f32_16x16x32_bf16 v[164:167], v[192:195], a[140:143], v[164:167]\n"
"v_mfma_f32_16x16x32_bf16 v[164:167], v[208:211], a[176:179], v[164:167]\n"
"v_mfma_f32_16x16x32_bf16 v[164:167], v[212:215], a[180:183], v[164:167]\n"
"v_mfma_f32_16x16x32_bf16 v[164:167], v[216:219], a[184:187], v[164:167]\n"
"s_waitcnt vmcnt(12)\n"
"v_mfma_f32_16x16x32_bf16 v[164:167], v[220:223], a[188:191], v[164:167]\n"
"s_barrier\n"
"s_waitcnt lgkmcnt(0)\n"
"v_mfma_f32_16x16x32_bf16 v[136:139], v[96:99], a[96:99], v[196:199]\n"
"ds_read_b64_tr_b16 v[180:181], v228 offset:32768\n"
"v_mfma_f32_16x16x32_bf16 v[136:139], v[100:103], a[100:103], v[136:139]\n"
"ds_read_b64_tr_b16 v[182:183], v228 offset:34816\n"
"v_mfma_f32_16x16x32_bf16 v[136:139], v[104:107], a[104:107], v[136:139]\n"
"ds_read_b64_tr_b16 v[184:185], v232 offset:32768\n"
"v_mfma_f32_16x16x32_bf16 v[136:139], v[108:111], a[108:111], v[136:139]\n"
"ds_read_b64_tr_b16 v[186:187], v232 offset:34816\n"
"v_mfma_f32_16x16x32_bf16 v[136:139], v[112:115], a[144:147], v[136:139]\n"
"ds_read_b64_tr_b16 v[188:189], v228 offset:40960\n"
"v_mfma_f32_16x16x32_bf16 v[136:139], v[116:119], a[148:151], v[136:139]\n"
"ds_read_b64_tr_b16 v[190:191], v228 offset:43008\n"
"v_mfma_f32_16x16x32_bf16 v[136:139], v[120:123], a[152:155], v[136:139]\n"
"s_add_u32 s40, s41, s52\n"
"ds_read_b64_tr_b16 v[192:193], v232 offset:40960\n"
"v_mfma_f32_16x16x32_bf16 v[136:139], v[224:227], a[156:159], v[136:139]\n"
"buffer_load_dwordx4 v240, s[28:31], s40 offen offset:1024 lds\n"
"v_mfma_f32_16x16x32_bf16 v[152:155], v[224:227], a[172:175], v[196:199]\n"
"ds_read_b64_tr_b16 v[194:195], v232 offset:43008\n"
"v_mfma_f32_16x16x32_bf16 v[152:155], v[120:123], a[168:171], v[152:155]\n"
"ds_read_b64_tr_b16 v[208:209], v228 offset:49152\n"
"v_mfma_f32_16x16x32_bf16 v[152:155], v[116:119], a[164:167], v[152:155]\n"
"ds_read_b64_tr_b16 v[210:211], v228 offset:51200\n"
"v_mfma_f32_16x16x32_bf16 v[152:155], v[112:115], a[160:163], v[152:155]\n"
"ds_read_b64_tr_b16 v[212:213], v232 offset:49152\n"
"v_mfma_f32_16x16x32_bf16 v[152:155], v[108:111], a[124:127], v[152:155]\n"
"ds_read_b64_tr_b16 v[214:215], v232 offset:51200\n"
"v_mfma_f32_16x16x32_bf16 v[152:155], v[104:107], a[120:123], v[152:155]\n"
"ds_read_b64_tr_b16 v[216:217], v228 offset:57344\n"
"v_mfma_f32_16x16x32_bf16 v[152:155], v[100:103], a[116:119], v[152:155]\n"
"s_add_u32 s40, s41, s53\n"
"ds_read_b64_tr_b16 v[218:219], v228 offset:59392\n"
"v_xor_b32 v228, s75, v228\n"
"v_mfma_f32_16x16x32_bf16 v[152:155], v[96:99], a[112:115], v[152:155]\n"
"buffer_load_dwordx4 v240, s[28:31], s40 offen offset:2048 lds\n"
"v_mfma_f32_16x16x32_bf16 v[168:171], v[96:99], a[128:131], v[196:199]\n"
"ds_read_b64_tr_b16 v[220:221], v232 offset:57344\n"
"v_mfma_f32_16x16x32_bf16 v[168:171], v[100:103], a[132:135], v[168:171]\n"
"ds_read_b64_tr_b16 v[222:223], v232 offset:59392\n"
"v_xor_b32 v232, s75, v232\n"
"v_mfma_f32_16x16x32_bf16 v[168:171], v[104:107], a[136:139], v[168:171]\n"
"ds_read_b64_tr_b16 v[96:97], v229 offset:32768\n"
"v_mfma_f32_16x16x32_bf16 v[168:171], v[108:111], a[140:143], v[168:171]\n"
"ds_read_b64_tr_b16 v[98:99], v229 offset:34816\n"
"v_mfma_f32_16x16x32_bf16 v[168:171], v[112:115], a[176:179], v[168:171]\n"
"ds_read_b64_tr_b16 v[100:101], v233 offset:32768\n"
"v_mfma_f32_16x16x32_bf16 v[168:171], v[116:119], a[180:183], v[168:171]\n"
"ds_read_b64_tr_b16 v[102:103], v233 offset:34816\n"
"v_mfma_f32_16x16x32_bf16 v[168:171], v[120:123], a[184:187], v[168:171]\n"
"s_add_u32 s40, s41, s54\n"
"ds_read_b64_tr_b16 v[104:105], v229 offset:40960\n"
"v_mfma_f32_16x16x32_bf16 v[168:171], v[224:227], a[188:191], v[168:171]\n"
"s_waitcnt lgkmcnt(7)\n"
"buffer_load_dwordx4 v240, s[28:31], s40 offen offset:3072 lds\n"
"v_mfma_f32_16x16x32_bf16 v[124:127], v[180:183], a[48:51], v[124:127]\n"
"ds_read_b64_tr_b16 v[106:107], v229 offset:43008\n"
"v_mfma_f32_16x16x32_bf16 v[124:127], v[184:187], a[52:55], v[124:127]\n"
"ds_read_b64_tr_b16 v[108:109], v233 offset:40960\n"
"v_mfma_f32_16x16x32_bf16 v[124:127], v[188:191], a[56:59], v[124:127]\n"
"ds_read_b64_tr_b16 v[110:111], v233 offset:43008\n"
"v_mfma_f32_16x16x32_bf16 v[124:127], v[192:195], a[60:63], v[124:127]\n"
"ds_read_b64_tr_b16 v[112:113], v229 offset:49152\n"
"v_mfma_f32_16x16x32_bf16 v[124:127], v[208:211], a[0:3], v[124:127]\n"
"ds_read_b64_tr_b16 v[114:115], v229 offset:51200\n"
"v_mfma_f32_16x16x32_bf16 v[124:127], v[212:215], a[4:7], v[124:127]\n"
"s_add_u32 s41, s70, 327680\n"
"ds_read_b64_tr_b16 v[116:117], v233 offset:49152\n"
"v_mfma_f32_16x16x32_bf16 v[124:127], v[216:219], a[8:11], v[124:127]\n"
"s_waitcnt lgkmcnt(6)\n"
"s_add_u32 m0, s71, 32768\n"
"ds_read_b64_tr_b16 v[118:119], v233 offset:51200\n"
"v_mfma_f32_16x16x32_bf16 v[124:127], v[220:223], a[12:15], v[124:127]\n"
"buffer_load_dwordx4 v240, s[28:31], s41 offen lds\n"
"v_mfma_f32_16x16x32_bf16 v[140:143], v[220:223], a[28:31], v[140:143]\n"
"ds_read_b64_tr_b16 v[120:121], v229 offset:57344\n"
"v_mfma_f32_16x16x32_bf16 v[140:143], v[216:219], a[24:27], v[140:143]\n"
"ds_read_b64_tr_b16 v[122:123], v229 offset:59392\n"
"v_xor_b32 v229, s75, v229\n"
"v_mfma_f32_16x16x32_bf16 v[140:143], v[212:215], a[20:23], v[140:143]\n"
"ds_read_b64_tr_b16 v[224:225], v233 offset:57344\n"
"v_mfma_f32_16x16x32_bf16 v[140:143], v[208:211], a[16:19], v[140:143]\n"
"ds_read_b64_tr_b16 v[226:227], v233 offset:59392\n"
"v_xor_b32 v233, s75, v233\n"
"v_mfma_f32_16x16x32_bf16 v[140:143], v[192:195], a[76:79], v[140:143]\n"
"v_mfma_f32_16x16x32_bf16 v[140:143], v[188:191], a[72:75], v[140:143]\n"
"v_subrev_f32 v124, s22, v124\n"
"v_subrev_f32 v125, s22, v125\n"
"v_mfma_f32_16x16x32_bf16 v[140:143], v[184:187], a[68:71], v[140:143]\n"
"s_add_u32 s40, s41, s52\n"
"v_subrev_f32 v126, s22, v126\n"
"v_subrev_f32 v127, s22, v127\n"
"v_mfma_f32_16x16x32_bf16 v[140:143], v[180:183], a[64:67], v[140:143]\n"
"buffer_load_dwordx4 v240, s[28:31], s40 offen offset:1024 lds\n"
"v_mfma_f32_16x16x32_bf16 v[156:159], v[180:183], a[80:83], v[156:159]\n"
"v_cvt_pk_bf16_f32 v124, v124, v125\n"
"v_cvt_pk_bf16_f32 v125, v126, v127\n"
"v_mfma_f32_16x16x32_bf16 v[156:159], v[184:187], a[84:87], v[156:159]\n"
"v_mfma_f32_16x16x32_bf16 v[156:159], v[188:191], a[88:91], v[156:159]\n"
"v_mfma_f32_16x16x32_bf16 v[156:159], v[192:195], a[92:95], v[156:159]\n"
"v_mfma_f32_16x16x32_bf16 v[156:159], v[208:211], a[32:35], v[156:159]\n"
"v_mfma_f32_16x16x32_bf16 v[156:159], v[212:215], a[36:39], v[156:159]\n"
"v_subrev_f32 v140, s22, v140\n"
"v_subrev_f32 v141, s22, v141\n"
"v_mfma_f32_16x16x32_bf16 v[156:159], v[216:219], a[40:43], v[156:159]\n"
"s_waitcnt vmcnt(7)\n"
"v_subrev_f32 v142, s22, v142\n"
"v_subrev_f32 v143, s22, v143\n"
"v_mfma_f32_16x16x32_bf16 v[156:159], v[220:223], a[44:47], v[156:159]\n"
"s_barrier\n"
"buffer_store_dword v241, v248, s[84:87], 0 offen sc1\n"
"v_mfma_f32_16x16x32_bf16 v[172:175], v[220:223], a[228:231], v[172:175]\n"
"ds_read_b64_tr_b16 v[220:221], v234 offset:57344\n"
"v_cvt_pk_bf16_f32 v140, v140, v141\n"
"v_mfma_f32_16x16x32_bf16 v[172:175], v[216:219], a[224:227], v[172:175]\n"
"ds_read_b64_tr_b16 v[216:217], v230 offset:57344\n"
"v_cvt_pk_bf16_f32 v141, v142, v143\n"
"v_mfma_f32_16x16x32_bf16 v[172:175], v[212:215], a[212:215], v[172:175]\n"
"ds_read_b64_tr_b16 v[212:213], v234 offset:49152\n"
"v_add_u32 v241, 1, v241\n"
"v_mfma_f32_16x16x32_bf16 v[172:175], v[208:211], a[208:211], v[172:175]\n"
"s_add_u32 s16, s90, s62\n"
"buffer_load_dwordx4 v[84:87], v243, s[32:35], s18 offen offset:3072 sc1\n"
"v_mfma_f32_16x16x32_bf16 v[172:175], v[192:195], a[236:239], v[172:175]\n"
"s_add_u32 s17, s16, 4096\n"
"ds_read_b64_tr_b16 v[192:193], v234 offset:40960\n"
"v_mfma_f32_16x16x32_bf16 v[172:175], v[188:191], a[232:235], v[172:175]\n"
"s_lshl_b32 s62, s70, 9\n"
"ds_read_b64_tr_b16 v[188:189], v230 offset:40960\n"
"v_subrev_f32 v156, s22, v156\n"
"v_mfma_f32_16x16x32_bf16 v[172:175], v[184:187], a[220:223], v[172:175]\n"
"s_add_u32 s40, s41, s53\n"
"ds_read_b64_tr_b16 v[184:185], v234 offset:32768\n"
"v_subrev_f32 v157, s22, v157\n"
"v_mfma_f32_16x16x32_bf16 v[172:175], v[180:183], a[216:219], v[172:175]\n"
"buffer_load_dwordx4 v240, s[28:31], s40 offen offset:2048 lds\n"
"v_mfma_f32_16x16x32_bf16 v[128:131], v[96:99], a[48:51], v[128:131]\n"
"ds_read_b64_tr_b16 v[180:181], v230 offset:32768\n"
"v_subrev_f32 v158, s22, v158\n"
"v_mfma_f32_16x16x32_bf16 v[128:131], v[100:103], a[52:55], v[128:131]\n"
"s_waitcnt lgkmcnt(5)\n"
"ds_read_b64_tr_b16 v[182:183], v230 offset:34816\n"
"v_subrev_f32 v159, s22, v159\n"
"v_mfma_f32_16x16x32_bf16 v[128:131], v[104:107], a[56:59], v[128:131]\n"
"ds_read_b64_tr_b16 v[186:187], v234 offset:34816\n"
"v_cvt_pk_bf16_f32 v156, v156, v157\n"
"v_mfma_f32_16x16x32_bf16 v[128:131], v[108:111], a[60:63], v[128:131]\n"
"ds_read_b64_tr_b16 v[190:191], v230 offset:43008\n"
"v_cvt_pk_bf16_f32 v157, v158, v159\n"
"v_mfma_f32_16x16x32_bf16 v[128:131], v[112:115], a[0:3], v[128:131]\n"
"ds_read_b64_tr_b16 v[194:195], v234 offset:43008\n"
"v_lshlrev_b32 v202, 16, v72\n"
"v_mfma_f32_16x16x32_bf16 v[128:131], v[116:119], a[4:7], v[128:131]\n"
"ds_read_b64_tr_b16 v[208:209], v230 offset:49152\n"
"v_and_b32 v72, s73, v72\n"
"v_mfma_f32_16x16x32_bf16 v[128:131], v[120:123], a[8:11], v[128:131]\n"
"s_add_u32 s40, s41, s54\n"
"ds_read_b64_tr_b16 v[210:211], v230 offset:51200\n"
"v_add_f32 v0, v0, v202\n"
"v_mfma_f32_16x16x32_bf16 v[128:131], v[224:227], a[12:15], v[128:131]\n"
"buffer_load_dwordx4 v240, s[28:31], s40 offen offset:3072 lds\n"
"v_mfma_f32_16x16x32_bf16 v[144:147], v[224:227], a[28:31], v[144:147]\n"
"ds_read_b64_tr_b16 v[214:215], v234 offset:51200\n"
"v_add_f32 v1, v1, v72\n"
"v_mfma_f32_16x16x32_bf16 v[144:147], v[120:123], a[24:27], v[144:147]\n"
"ds_read_b64_tr_b16 v[218:219], v230 offset:59392\n"
"v_lshlrev_b32 v203, 16, v73\n"
"v_mfma_f32_16x16x32_bf16 v[144:147], v[116:119], a[20:23], v[144:147]\n"
"ds_read_b64_tr_b16 v[222:223], v234 offset:59392\n"
"v_and_b32 v73, s73, v73\n"
"v_mfma_f32_16x16x32_bf16 v[144:147], v[112:115], a[16:19], v[144:147]\n"
"buffer_load_dwordx4 v[88:91], v243, s[32:35], s19 offen sc1\n"
"v_mfma_f32_16x16x32_bf16 v[144:147], v[108:111], a[76:79], v[144:147]\n"
"v_add_f32 v2, v2, v203\n"
"v_add_f32 v3, v3, v73\n"
"v_mfma_f32_16x16x32_bf16 v[144:147], v[104:107], a[72:75], v[144:147]\n"
"s_add_u32 s41, s70, 491520\n"
"v_subrev_f32 v128, s22, v128\n"
"v_subrev_f32 v129, s22, v129\n"
"v_mfma_f32_16x16x32_bf16 v[144:147], v[100:103], a[68:71], v[144:147]\n"
"s_add_u32 m0, s71, 49152\n"
"v_subrev_f32 v130, s22, v130\n"
"v_subrev_f32 v131, s22, v131\n"
"v_mfma_f32_16x16x32_bf16 v[144:147], v[96:99], a[64:67], v[144:147]\n"
"buffer_load_dwordx4 v240, s[28:31], s41 offen lds\n"
"v_mfma_f32_16x16x32_bf16 v[160:163], v[96:99], a[80:83], v[160:163]\n"
"s_and_b32 s70, s88, 15\n"
"v_cvt_pk_bf16_f32 v126, v128, v129\n"
"v_cvt_pk_bf16_f32 v127, v130, v131\n"
"v_mfma_f32_16x16x32_bf16 v[160:163], v[100:103], a[84:87], v[160:163]\n"
"s_lshr_b32 s88, s88, 4\n"
"v_lshlrev_b32 v204, 16, v74\n"
"v_and_b32 v74, s73, v74\n"
"v_mfma_f32_16x16x32_bf16 v[160:163], v[104:107], a[88:91], v[160:163]\n"
"s_lshl_b32 s70, s70, 7\n"
"v_add_f32 v4, v4, v204\n"
"v_add_f32 v5, v5, v74\n"
"v_mfma_f32_16x16x32_bf16 v[160:163], v[108:111], a[92:95], v[160:163]\n"
"s_xor_b32 s71, s71, s75\n"
"v_lshlrev_b32 v205, 16, v75\n"
"v_and_b32 v75, s73, v75\n"
"v_mfma_f32_16x16x32_bf16 v[160:163], v[112:115], a[32:35], v[160:163]\n"
"v_add_f32 v6, v6, v205\n"
"v_add_f32 v7, v7, v75\n"
"v_mfma_f32_16x16x32_bf16 v[160:163], v[116:119], a[36:39], v[160:163]\n"
"v_subrev_f32 v144, s22, v144\n"
"v_subrev_f32 v145, s22, v145\n"
"v_mfma_f32_16x16x32_bf16 v[160:163], v[120:123], a[40:43], v[160:163]\n"
"v_subrev_f32 v146, s22, v146\n"
"v_subrev_f32 v147, s22, v147\n"
"v_mfma_f32_16x16x32_bf16 v[160:163], v[224:227], a[44:47], v[160:163]\n"
"buffer_store_dwordx4 v[124:127], v243, s[32:35], s16 offen sc1\n"
"v_cvt_pk_bf16_f32 v142, v144, v145\n"
"v_mfma_f32_16x16x32_bf16 v[176:179], v[224:227], a[228:231], v[176:179]\n"
"ds_read_b64_tr_b16 v[224:225], v235 offset:57344\n"
"v_cvt_pk_bf16_f32 v143, v146, v147\n"
"v_mfma_f32_16x16x32_bf16 v[176:179], v[120:123], a[224:227], v[176:179]\n"
"ds_read_b64_tr_b16 v[120:121], v231 offset:57344\n"
"v_lshlrev_b32 v206, 16, v76\n"
"v_mfma_f32_16x16x32_bf16 v[176:179], v[116:119], a[212:215], v[176:179]\n"
"ds_read_b64_tr_b16 v[116:117], v235 offset:49152\n"
"v_and_b32 v76, s73, v76\n"
"v_mfma_f32_16x16x32_bf16 v[176:179], v[112:115], a[208:211], v[176:179]\n"
"buffer_load_dwordx4 v[92:95], v243, s[32:35], s19 offen offset:1024 sc1\n"
"v_mfma_f32_16x16x32_bf16 v[176:179], v[108:111], a[236:239], v[176:179]\n"
"ds_read_b64_tr_b16 v[108:109], v235 offset:40960\n"
"v_add_f32 v8, v8, v206\n"
"v_mfma_f32_16x16x32_bf16 v[176:179], v[104:107], a[232:235], v[176:179]\n"
"ds_read_b64_tr_b16 v[104:105], v231 offset:40960\n"
"v_subrev_f32 v160, s22, v160\n"
"v_mfma_f32_16x16x32_bf16 v[176:179], v[100:103], a[220:223], v[176:179]\n"
"s_add_u32 s40, s41, s52\n"
"ds_read_b64_tr_b16 v[100:101], v235 offset:32768\n"
"v_subrev_f32 v161, s22, v161\n"
"v_mfma_f32_16x16x32_bf16 v[176:179], v[96:99], a[216:219], v[176:179]\n"
"s_waitcnt vmcnt(14) lgkmcnt(6)\n"
"buffer_load_dwordx4 v240, s[28:31], s40 offen offset:1024 lds\n"
"v_mfma_f32_16x16x32_bf16 v[132:135], v[180:183], a[48:51], v[132:135]\n"
"ds_read_b64_tr_b16 v[96:97], v231 offset:32768\n"
"v_subrev_f32 v162, s22, v162\n"
"v_mfma_f32_16x16x32_bf16 v[132:135], v[184:187], a[52:55], v[132:135]\n"
"ds_read_b64_tr_b16 v[98:99], v231 offset:34816\n"
"v_subrev_f32 v163, s22, v163\n"
"v_mfma_f32_16x16x32_bf16 v[132:135], v[188:191], a[56:59], v[132:135]\n"
"ds_read_b64_tr_b16 v[102:103], v235 offset:34816\n"
"v_cvt_pk_bf16_f32 v158, v160, v161\n"
"v_mfma_f32_16x16x32_bf16 v[132:135], v[192:195], a[60:63], v[132:135]\n"
"ds_read_b64_tr_b16 v[106:107], v231 offset:43008\n"
"v_cvt_pk_bf16_f32 v159, v162, v163\n"
"v_mfma_f32_16x16x32_bf16 v[132:135], v[208:211], a[0:3], v[132:135]\n"
"ds_read_b64_tr_b16 v[110:111], v235 offset:43008\n"
"v_add_f32 v9, v9, v76\n"
"v_mfma_f32_16x16x32_bf16 v[132:135], v[212:215], a[4:7], v[132:135]\n"
"ds_read_b64_tr_b16 v[112:113], v231 offset:49152\n"
"v_lshlrev_b32 v207, 16, v77\n"
"v_mfma_f32_16x16x32_bf16 v[132:135], v[216:219], a[8:11], v[132:135]\n"
"ds_read_b64_tr_b16 v[114:115], v231 offset:51200\n"
"v_and_b32 v77, s73, v77\n"
"v_mfma_f32_16x16x32_bf16 v[132:135], v[220:223], a[12:15], v[132:135]\n"
"buffer_store_dwordx4 v[140:143], v243, s[32:35], s16 offen offset:2048 sc1\n"
"v_add_f32 v10, v10, v207\n"
"v_mfma_f32_16x16x32_bf16 v[148:151], v[220:223], a[28:31], v[148:151]\n"
"ds_read_b64_tr_b16 v[118:119], v235 offset:51200\n"
"v_add_f32 v11, v11, v77\n"
"v_mfma_f32_16x16x32_bf16 v[148:151], v[216:219], a[24:27], v[148:151]\n"
"ds_read_b64_tr_b16 v[122:123], v231 offset:59392\n"
"v_lshlrev_b32 v202, 16, v78\n"
"v_mfma_f32_16x16x32_bf16 v[148:151], v[212:215], a[20:23], v[148:151]\n"
"ds_read_b64_tr_b16 v[226:227], v235 offset:59392\n"
"v_and_b32 v78, s73, v78\n"
"v_mfma_f32_16x16x32_bf16 v[148:151], v[208:211], a[16:19], v[148:151]\n"
"v_add_f32 v12, v12, v202\n"
"v_add_f32 v13, v13, v78\n"
"v_mfma_f32_16x16x32_bf16 v[148:151], v[192:195], a[76:79], v[148:151]\n"
"v_lshlrev_b32 v203, 16, v79\n"
"v_and_b32 v79, s73, v79\n"
"v_mfma_f32_16x16x32_bf16 v[148:151], v[188:191], a[72:75], v[148:151]\n"
"v_add_f32 v14, v14, v203\n"
"v_add_f32 v15, v15, v79\n"
"v_mfma_f32_16x16x32_bf16 v[148:151], v[184:187], a[68:71], v[148:151]\n"
"s_add_u32 s40, s41, s53\n"
"v_lshlrev_b32 v204, 16, v80\n"
"v_and_b32 v80, s73, v80\n"
"v_mfma_f32_16x16x32_bf16 v[148:151], v[180:183], a[64:67], v[148:151]\n"
"buffer_load_dwordx4 v240, s[28:31], s40 offen offset:2048 lds\n"
"v_mfma_f32_16x16x32_bf16 v[164:167], v[180:183], a[80:83], v[164:167]\n"
"v_add_f32 v16, v16, v204\n"
"v_add_f32 v17, v17, v80\n"
"v_mfma_f32_16x16x32_bf16 v[164:167], v[184:187], a[84:87], v[164:167]\n"
"v_lshlrev_b32 v205, 16, v81\n"
"v_and_b32 v81, s73, v81\n"
"v_mfma_f32_16x16x32_bf16 v[164:167], v[188:191], a[88:91], v[164:167]\n"
"v_add_f32 v18, v18, v205\n"
"v_add_f32 v19, v19, v81\n"
"v_mfma_f32_16x16x32_bf16 v[164:167], v[192:195], a[92:95], v[164:167]\n"
"buffer_load_dwordx4 v[244:247], v243, s[32:35], s19 offen offset:2048 sc1\n"
"v_mfma_f32_16x16x32_bf16 v[164:167], v[208:211], a[32:35], v[164:167]\n"
"s_lshl4_add_u32 s18, s72, s61\n"
"v_lshlrev_b32 v206, 16, v82\n"
"v_and_b32 v82, s73, v82\n"
"v_mfma_f32_16x16x32_bf16 v[164:167], v[212:215], a[36:39], v[164:167]\n"
"s_lshl_b32 s19, s72, 2\n"
"v_add_f32 v20, v20, v206\n"
"v_add_f32 v21, v21, v82\n"
"v_mfma_f32_16x16x32_bf16 v[164:167], v[216:219], a[40:43], v[164:167]\n"
"s_waitcnt vmcnt(14)\n"
"s_lshr_b32 s19, s89, s19\n"
"v_lshlrev_b32 v207, 16, v83\n"
"v_mfma_f32_16x16x32_bf16 v[164:167], v[220:223], a[44:47], v[164:167]\n"
"s_barrier\n"
"s_and_b32 s19, s19, 1\n"
"s_waitcnt lgkmcnt(0)\n"
"v_mfma_f32_16x16x32_bf16 v[136:139], v[96:99], a[48:51], v[136:139]\n"
"s_lshl3_add_u32 s18, s18, s19\n"
"ds_read_b64_tr_b16 v[180:181], v228 offset:0\n"
"v_and_b32 v83, s73, v83\n"
"v_mfma_f32_16x16x32_bf16 v[136:139], v[100:103], a[52:55], v[136:139]\n"
"s_lshl_b32 s18, s18, 13\n"
"ds_read_b64_tr_b16 v[182:183], v228 offset:2048\n"
"v_add_f32 v22, v22, v207\n"
"v_mfma_f32_16x16x32_bf16 v[136:139], v[104:107], a[56:59], v[136:139]\n"
"s_add_u32 s19, s18, 4096\n"
"v_lshlrev_b32_e64 v201, 2, s72\n"
"ds_read_b64_tr_b16 v[184:185], v232 offset:0\n"
"v_mfma_f32_16x16x32_bf16 v[136:139], v[108:111], a[60:63], v[136:139]\n"
"s_mov_b32 s69, 15\n"
"global_load_dword v200, v201, s[6:7] sc1\n"
"v_mfma_f32_16x16x32_bf16 v[136:139], v[112:115], a[0:3], v[136:139]\n"
"s_add_u32 s21, s21, 1\n"
"ds_read_b64_tr_b16 v[186:187], v232 offset:2048\n"
"v_add_f32 v23, v23, v83\n"
"v_mfma_f32_16x16x32_bf16 v[136:139], v[116:119], a[4:7], v[136:139]\n"
"ds_read_b64_tr_b16 v[188:189], v228 offset:8192\n"
"v_subrev_f32 v172, s22, v172\n"
"v_mfma_f32_16x16x32_bf16 v[136:139], v[120:123], a[8:11], v[136:139]\n"
"ds_read_b64_tr_b16 v[190:191], v228 offset:10240\n"
"v_subrev_f32 v173, s22, v173\n"
"v_mfma_f32_16x16x32_bf16 v[136:139], v[224:227], a[12:15], v[136:139]\n"
"buffer_store_dwordx4 v[156:159], v243, s[32:35], s17 offen sc1\n"
"v_subrev_f32 v174, s22, v174\n"
"v_mfma_f32_16x16x32_bf16 v[152:155], v[224:227], a[28:31], v[152:155]\n"
"ds_read_b64_tr_b16 v[192:193], v232 offset:8192\n"
"v_subrev_f32 v175, s22, v175\n"
"v_mfma_f32_16x16x32_bf16 v[152:155], v[120:123], a[24:27], v[152:155]\n"
"ds_read_b64_tr_b16 v[194:195], v232 offset:10240\n"
"v_cvt_pk_bf16_f32 v172, v172, v173\n"
"v_mfma_f32_16x16x32_bf16 v[152:155], v[116:119], a[20:23], v[152:155]\n"
"ds_read_b64_tr_b16 v[208:209], v228 offset:16384\n"
"v_cvt_pk_bf16_f32 v173, v174, v175\n"
"v_mfma_f32_16x16x32_bf16 v[152:155], v[112:115], a[16:19], v[152:155]\n"
"ds_read_b64_tr_b16 v[210:211], v228 offset:18432\n"
"v_subrev_f32 v176, s22, v176\n"
"v_mfma_f32_16x16x32_bf16 v[152:155], v[108:111], a[76:79], v[152:155]\n"
"ds_read_b64_tr_b16 v[212:213], v232 offset:16384\n"
"v_subrev_f32 v177, s22, v177\n"
"v_mfma_f32_16x16x32_bf16 v[152:155], v[104:107], a[72:75], v[152:155]\n"
"ds_read_b64_tr_b16 v[214:215], v232 offset:18432\n"
"v_subrev_f32 v178, s22, v178\n"
"v_mfma_f32_16x16x32_bf16 v[152:155], v[100:103], a[68:71], v[152:155]\n"
"s_add_u32 s40, s41, s54\n"
"ds_read_b64_tr_b16 v[216:217], v228 offset:24576\n"
"v_subrev_f32 v179, s22, v179\n"
"v_mfma_f32_16x16x32_bf16 v[152:155], v[96:99], a[64:67], v[152:155]\n"
"buffer_load_dwordx4 v240, s[28:31], s40 offen offset:3072 lds\n"
"v_mfma_f32_16x16x32_bf16 v[168:171], v[96:99], a[80:83], v[168:171]\n"
"ds_read_b64_tr_b16 v[218:219], v228 offset:26624\n"
"v_cvt_pk_bf16_f32 v174, v176, v177\n"
"v_mfma_f32_16x16x32_bf16 v[168:171], v[100:103], a[84:87], v[168:171]\n"
"ds_read_b64_tr_b16 v[220:221], v232 offset:24576\n"
"v_cvt_pk_bf16_f32 v175, v178, v179\n"
"v_mfma_f32_16x16x32_bf16 v[168:171], v[104:107], a[88:91], v[168:171]\n"
"ds_read_b64_tr_b16 v[222:223], v232 offset:26624\n"
"v_subrev_f32 v132, s22, v132\n"
"v_mfma_f32_16x16x32_bf16 v[168:171], v[108:111], a[92:95], v[168:171]\n"
"ds_read_b64_tr_b16 v[96:97], v229 offset:0\n"
"v_subrev_f32 v133, s22, v133\n"
"v_mfma_f32_16x16x32_bf16 v[168:171], v[112:115], a[32:35], v[168:171]\n"
"ds_read_b64_tr_b16 v[98:99], v229 offset:2048\n"
"v_subrev_f32 v134, s22, v134\n"
"v_mfma_f32_16x16x32_bf16 v[168:171], v[116:119], a[36:39], v[168:171]\n"
"ds_read_b64_tr_b16 v[100:101], v233 offset:0\n"
"v_subrev_f32 v135, s22, v135\n"
"v_mfma_f32_16x16x32_bf16 v[168:171], v[120:123], a[40:43], v[168:171]\n"
"s_add_u32 s47, s47, 1\n"
"ds_read_b64_tr_b16 v[102:103], v233 offset:2048\n"
"v_cvt_pk_bf16_f32 v132, v132, v133\n"
"v_mfma_f32_16x16x32_bf16 v[168:171], v[224:227], a[44:47], v[168:171]\n"
"s_waitcnt lgkmcnt(7)\n"
"s_cmp_lg_u32 s47, 9\n"
"ds_read_b64_tr_b16 v[104:105], v229 offset:8192\n"
"s_cbranch_scc1 LGL_%=\n"
"v_mfma_f32_16x16x32_bf16 v[0:3], v[180:183], a[96:99], v[0:3]\n"
"v_xor_b32 v235, s75, v235\n"
"v_subrev_f32 v136, s22, v136\n"
"v_mfma_f32_16x16x32_bf16 v[0:3], v[184:187], a[100:103], v[0:3]\n"
"ds_read_b64_tr_b16 v[114:115], v229 offset:18432\n"
"v_cvt_pk_bf16_f32 v133, v134, v135\n"
"v_mfma_f32_16x16x32_bf16 v[0:3], v[188:191], a[104:107], v[0:3]\n"
"v_mov_b32 v201, v237\n"
"v_subrev_f32 v138, s22, v138\n"
"v_subrev_f32 v139, s22, v139\n"
"v_mfma_f32_16x16x32_bf16 v[0:3], v[192:195], a[108:111], v[0:3]\n"
"global_load_dword v200, v201, s[6:7] sc1\n"
"v_mfma_f32_16x16x32_bf16 v[0:3], v[208:211], a[144:147], v[0:3]\n"
"v_subrev_f32 v167, s22, v167\n"
"v_cvt_pk_bf16_f32 v135, v138, v139\n"
"v_mfma_f32_16x16x32_bf16 v[0:3], v[212:215], a[148:151], v[0:3]\n"
"s_waitcnt lgkmcnt(3)\n"
"v_subrev_f32 v148, s22, v148\n"
"v_subrev_f32 v137, s22, v137\n"
"v_mfma_f32_16x16x32_bf16 v[0:3], v[216:219], a[152:155], v[0:3]\n"
"v_subrev_f32 v165, s22, v165\n"
"v_subrev_f32 v168, s22, v168\n"
"v_mfma_f32_16x16x32_bf16 v[0:3], v[220:223], a[156:159], v[0:3]\n"
"buffer_store_dwordx4 v[172:175], v243, s[32:35], s17 offen offset:2048 sc1\n"
"v_subrev_f32 v151, s22, v151\n"
"v_cvt_pk_bf16_f32 v134, v136, v137\n"
"v_mfma_f32_16x16x32_bf16 v[16:19], v[220:223], a[172:175], v[16:19]\n"
"buffer_store_dwordx4 v[132:135], v243, s[32:35], s16 offen offset:1024 sc1\n"
"v_subrev_f32 v149, s22, v149\n"
"v_subrev_f32 v150, s22, v150\n"
"v_subrev_f32 v164, s22, v164\n"
"v_cvt_pk_bf16_f32 v164, v164, v165\n"
"v_subrev_f32 v153, s22, v153\n"
"v_subrev_f32 v155, s22, v155\n"
"v_mfma_f32_16x16x32_bf16 v[16:19], v[216:219], a[168:171], v[16:19]\n"
"v_cvt_pk_bf16_f32 v148, v148, v149\n"
"v_subrev_f32 v154, s22, v154\n"
"v_mfma_f32_16x16x32_bf16 v[16:19], v[212:215], a[164:167], v[16:19]\n"
"v_cvt_pk_bf16_f32 v149, v150, v151\n"
"v_subrev_f32 v170, s22, v170\n"
"v_mfma_f32_16x16x32_bf16 v[16:19], v[208:211], a[160:163], v[16:19]\n"
"v_subrev_f32 v166, s22, v166\n"
"v_subrev_f32 v171, s22, v171\n"
"v_mfma_f32_16x16x32_bf16 v[16:19], v[192:195], a[124:127], v[16:19]\n"
"v_cvt_pk_bf16_f32 v165, v166, v167\n"
"v_cvt_pk_bf16_f32 v167, v170, v171\n"
"v_mfma_f32_16x16x32_bf16 v[16:19], v[188:191], a[120:123], v[16:19]\n"
"v_subrev_f32 v152, s22, v152\n"
"v_cvt_pk_bf16_f32 v151, v154, v155\n"
"v_mfma_f32_16x16x32_bf16 v[16:19], v[184:187], a[116:119], v[16:19]\n"
"v_cvt_pk_bf16_f32 v150, v152, v153\n"
"v_subrev_f32 v169, s22, v169\n"
"v_mfma_f32_16x16x32_bf16 v[16:19], v[180:183], a[112:115], v[16:19]\n"
"s_waitcnt vmcnt(16)\n"
"buffer_store_dwordx4 v[148:151], v243, s[32:35], s16 offen offset:3072 sc1\n"
"v_cvt_pk_bf16_f32 v166, v168, v169\n"
"v_mfma_f32_16x16x32_bf16 v[32:35], v[180:183], a[128:131], v[32:35]\n"
"buffer_store_dwordx4 v[164:167], v243, s[32:35], s17 offen offset:1024 sc1\n"
"ds_read_b64_tr_b16 v[120:121], v229 offset:24576\n"
"v_mfma_f32_16x16x32_bf16 v[32:35], v[184:187], a[132:135], v[32:35]\n"
"ds_read_b64_tr_b16 v[224:225], v233 offset:24576\n"
"v_xor_b32 v234, s75, v234\n"
"v_mfma_f32_16x16x32_bf16 v[32:35], v[188:191], a[136:139], v[32:35]\n"
"ds_read_b64_tr_b16 v[122:123], v229 offset:26624\n"
"v_lshlrev_b32 v203, 16, v85\n"
"v_mfma_f32_16x16x32_bf16 v[32:35], v[192:195], a[140:143], v[32:35]\n"
"ds_read_b64_tr_b16 v[108:109], v233 offset:8192\n"
"v_lshlrev_b32 v202, 16, v84\n"
"v_mfma_f32_16x16x32_bf16 v[32:35], v[208:211], a[176:179], v[32:35]\n"
"ds_read_b64_tr_b16 v[226:227], v233 offset:26624\n"
"v_and_b32 v84, s73, v84\n"
"v_mfma_f32_16x16x32_bf16 v[32:35], v[212:215], a[180:183], v[32:35]\n"
"ds_read_b64_tr_b16 v[110:111], v233 offset:10240\n"
"v_bfe_u32 v161, v242, 8, 2\n"
"v_mfma_f32_16x16x32_bf16 v[32:35], v[216:219], a[184:187], v[32:35]\n"
"ds_read_b64_tr_b16 v[106:107], v229 offset:10240\n"
"v_lshlrev_b32 v205, 16, v87\n"
"v_mfma_f32_16x16x32_bf16 v[32:35], v[220:223], a[188:191], v[32:35]\n"
"v_xor_b32 v230, s75, v230\n"
"v_lshlrev_b32 v204, 16, v86\n"
"v_mfma_f32_16x16x32_bf16 v[48:51], v[220:223], a[252:255], v[48:51]\n"
"ds_read_b64_tr_b16 v[220:221], v234 offset:24576\n"
"v_add_f32 v26, v26, v203\n"
"v_mfma_f32_16x16x32_bf16 v[48:51], v[216:219], a[248:251], v[48:51]\n"
"ds_read_b64_tr_b16 v[112:113], v229 offset:16384\n"
"v_bfe_u32 v160, v242, 6, 2\n"
"v_mfma_f32_16x16x32_bf16 v[48:51], v[212:215], a[244:247], v[48:51]\n"
"ds_read_b64_tr_b16 v[116:117], v233 offset:16384\n"
"v_add_f32 v24, v24, v202\n"
"v_mfma_f32_16x16x32_bf16 v[48:51], v[208:211], a[240:243], v[48:51]\n"
"ds_read_b64_tr_b16 v[118:119], v233 offset:18432\n"
"v_add_f32 v25, v25, v84\n"
"v_mfma_f32_16x16x32_bf16 v[48:51], v[192:195], a[204:207], v[48:51]\n"
"ds_read_b64_tr_b16 v[210:211], v230 offset:18432\n"
"v_add_f32 v28, v28, v204\n"
"v_mfma_f32_16x16x32_bf16 v[48:51], v[188:191], a[200:203], v[48:51]\n"
"ds_read_b64_tr_b16 v[188:189], v230 offset:8192\n"
"v_and_b32 v86, s73, v86\n"
"v_mfma_f32_16x16x32_bf16 v[48:51], v[184:187], a[196:199], v[48:51]\n"
"ds_read_b64_tr_b16 v[194:195], v234 offset:10240\n"
"v_mov_b32 v164, 0\n"
"v_mfma_f32_16x16x32_bf16 v[48:51], v[180:183], a[192:195], v[48:51]\n"
"s_waitcnt vmcnt(15)\n"
"v_xor_b32 v160, v160, v161\n"
"v_lshlrev_b32 v202, 16, v90\n"
"v_mfma_f32_16x16x32_bf16 v[4:7], v[96:99], a[96:99], v[4:7]\n"
"s_waitcnt lgkmcnt(6)\n"
"ds_read_b64_tr_b16 v[222:223], v234 offset:26624\n"
"v_lshlrev_b32 v203, 16, v91\n"
"v_mfma_f32_16x16x32_bf16 v[4:7], v[100:103], a[100:103], v[4:7]\n"
"ds_read_b64_tr_b16 v[184:185], v234 offset:0\n"
"v_lshlrev_b32 v206, 16, v88\n"
"v_mfma_f32_16x16x32_bf16 v[4:7], v[104:107], a[104:107], v[4:7]\n"
"ds_read_b64_tr_b16 v[216:217], v230 offset:24576\n"
"v_mfma_f32_16x16x32_bf16 v[4:7], v[108:111], a[108:111], v[4:7]\n"
"s_waitcnt lgkmcnt(6)\n"
"ds_read_b64_tr_b16 v[218:219], v230 offset:26624\n"
"v_and_b32 v91, s73, v91\n"
"v_mfma_f32_16x16x32_bf16 v[4:7], v[112:115], a[144:147], v[4:7]\n"
"s_waitcnt vmcnt(4)\n"
"ds_read_b64_tr_b16 v[190:191], v230 offset:10240\n"
"v_lshlrev_b32 v207, 16, v89\n"
"v_mfma_f32_16x16x32_bf16 v[4:7], v[116:119], a[148:151], v[4:7]\n"
"v_subrev_u32 v200, s21, v200\n"
"ds_read_b64_tr_b16 v[208:209], v230 offset:16384\n"
"v_mfma_f32_16x16x32_bf16 v[4:7], v[120:123], a[152:155], v[4:7]\n"
"v_cmp_lt_u32 vcc, s69, v200\n"
"ds_read_b64_tr_b16 v[182:183], v230 offset:2048\n"
"v_mfma_f32_16x16x32_bf16 v[4:7], v[224:227], a[156:159], v[4:7]\n"
"ds_read_b64_tr_b16 v[180:181], v230 offset:0\n"
"s_cbranch_vccnz LGQ_%=\nLGQK_%=:\n"
"v_mfma_f32_16x16x32_bf16 v[20:23], v[224:227], a[172:175], v[20:23]\n"
"s_bfe_u32 s20, s89, 65536\ns_lshl_b32 s20, s20, 13\ns_add_u32 s20, s20, 0\n"
"ds_read_b64_tr_b16 v[214:215], v234 offset:18432\n"
"v_and_b32 v85, s73, v85\n"
"v_mfma_f32_16x16x32_bf16 v[20:23], v[120:123], a[168:171], v[20:23]\n"
"buffer_load_dwordx4 v[124:127], v236, s[32:35], s20 offen sc1\n"
"v_mfma_f32_16x16x32_bf16 v[20:23], v[116:119], a[164:167], v[20:23]\n"
"v_and_b32 v90, s73, v90\n"
"v_add_f32 v37, v37, v90\n"
"v_mfma_f32_16x16x32_bf16 v[20:23], v[112:115], a[160:163], v[20:23]\n"
"s_bfe_u32 s20, s89, 65540\ns_lshl_b32 s20, s20, 13\ns_add_u32 s20, s20, 1048576\n"
"v_add_f32 v36, v36, v202\n"
"v_add_f32 v38, v38, v203\n"
"v_mfma_f32_16x16x32_bf16 v[20:23], v[108:111], a[124:127], v[20:23]\n"
"buffer_load_dwordx4 v[128:131], v236, s[32:35], s20 offen sc1\n"
"v_mfma_f32_16x16x32_bf16 v[20:23], v[104:107], a[120:123], v[20:23]\n"
"v_add_u32 v160, -1, v160\n"
"v_add_f32 v39, v39, v91\n"
"v_mfma_f32_16x16x32_bf16 v[20:23], v[100:103], a[116:119], v[20:23]\n"
"s_bfe_u32 s20, s89, 65544\ns_lshl_b32 s20, s20, 13\ns_add_u32 s20, s20, 2097152\n"
"v_add_f32 v32, v32, v206\n"
"v_mfma_f32_16x16x32_bf16 v[20:23], v[96:99], a[112:115], v[20:23]\n"
"buffer_load_dwordx4 v[132:135], v236, s[32:35], s20 offen sc1\n"
"v_mfma_f32_16x16x32_bf16 v[36:39], v[96:99], a[128:131], v[36:39]\n"
"v_lshlrev_b32 v204, 16, v92\n"
"v_add_f32 v40, v40, v204\n"
"v_mfma_f32_16x16x32_bf16 v[36:39], v[100:103], a[132:135], v[36:39]\n"
"s_bfe_u32 s20, s89, 65548\ns_lshl_b32 s20, s20, 13\ns_add_u32 s20, s20, 3145728\n"
"ds_read_b64_tr_b16 v[212:213], v234 offset:16384\n"
"v_add_f32 v30, v30, v205\n"
"v_mfma_f32_16x16x32_bf16 v[36:39], v[104:107], a[136:139], v[36:39]\n"
"buffer_load_dwordx4 v[136:139], v236, s[32:35], s20 offen sc1\n"
"v_mfma_f32_16x16x32_bf16 v[36:39], v[108:111], a[140:143], v[36:39]\n"
"v_and_b32 v92, s73, v92\n"
"v_lshlrev_b32 v206, 16, v94\n"
"v_mfma_f32_16x16x32_bf16 v[36:39], v[112:115], a[176:179], v[36:39]\n"
"s_bfe_u32 s20, s89, 65552\ns_lshl_b32 s20, s20, 13\ns_add_u32 s20, s20, 4194304\n"
"ds_read_b64_tr_b16 v[186:187], v234 offset:2048\n"
"ds_read_b64_tr_b16 v[192:193], v234 offset:8192\n"
"v_mfma_f32_16x16x32_bf16 v[36:39], v[116:119], a[180:183], v[36:39]\n"
"buffer_load_dwordx4 v[140:143], v236, s[32:35], s20 offen sc1\n"
"v_mfma_f32_16x16x32_bf16 v[36:39], v[120:123], a[184:187], v[36:39]\n"
"v_and_b32 v94, s73, v94\n"
"v_mfma_f32_16x16x32_bf16 v[36:39], v[224:227], a[188:191], v[36:39]\n"
"s_bfe_u32 s20, s89, 65556\ns_lshl_b32 s20, s20, 13\ns_add_u32 s20, s20, 5242880\n"
"v_add_f32 v34, v34, v207\n"
"v_lshlrev_b32 v205, 16, v93\n"
"v_mfma_f32_16x16x32_bf16 v[52:55], v[224:227], a[252:255], v[52:55]\n"
"buffer_load_dwordx4 v[144:147], v236, s[32:35], s20 offen sc1\n"
"v_mfma_f32_16x16x32_bf16 v[52:55], v[120:123], a[248:251], v[52:55]\n"
"v_ashrrev_i32 v160, 31, v160\n"
"v_add_f32 v44, v44, v206\n"
"v_mfma_f32_16x16x32_bf16 v[52:55], v[116:119], a[244:247], v[52:55]\n"
"s_bfe_u32 s20, s89, 65560\ns_lshl_b32 s20, s20, 13\ns_add_u32 s20, s20, 6291456\n"
"v_xor_b32 v231, s75, v231\n"
"v_lshlrev_b32 v207, 16, v95\n"
"v_mfma_f32_16x16x32_bf16 v[52:55], v[112:115], a[240:243], v[52:55]\n"
"buffer_load_dwordx4 v[148:151], v236, s[32:35], s20 offen sc1\n"
"v_mfma_f32_16x16x32_bf16 v[52:55], v[108:111], a[204:207], v[52:55]\n"
"s_bfe_u32 s20, s89, 65564\ns_lshl_b32 s20, s20, 13\ns_add_u32 s20, s20, 7340032\n"
"v_and_b32 v87, s73, v87\n"
"v_and_b32 v95, s73, v95\n"
"v_mfma_f32_16x16x32_bf16 v[52:55], v[104:107], a[200:203], v[52:55]\n"
"buffer_load_dwordx4 v[152:155], v236, s[32:35], s20 offen sc1\n"
"v_mfma_f32_16x16x32_bf16 v[52:55], v[100:103], a[196:199], v[52:55]\n"
"s_waitcnt vmcnt(8)\n"
"s_add_u32 s21, s21, 1\n"
"v_and_b32 v88, s73, v88\n"
"v_mfma_f32_16x16x32_bf16 v[52:55], v[96:99], a[192:195], v[52:55]\n"
"s_barrier\n"
"s_mov_b32 s52, 40960\n"
"buffer_store_dword v241, v248, s[84:87], 0 offen sc1\n"
"s_waitcnt lgkmcnt(0)\n"
"v_mfma_f32_16x16x32_bf16 v[8:11], v[180:183], a[96:99], v[8:11]\n"
"s_mov_b32 s53, 81920\n"
"ds_read_b64_tr_b16 v[98:99], v231 offset:2048\n"
"v_add_f32 v42, v42, v205\n"
"v_mfma_f32_16x16x32_bf16 v[8:11], v[184:187], a[100:103], v[8:11]\n"
"ds_read_b64_tr_b16 v[112:113], v231 offset:16384\n"
"v_add_f32 v31, v31, v87\n"
"v_mfma_f32_16x16x32_bf16 v[8:11], v[188:191], a[104:107], v[8:11]\n"
"v_add_f32 v29, v29, v86\n"
"v_and_b32 v161, 16, v242\n"
"v_mfma_f32_16x16x32_bf16 v[8:11], v[192:195], a[108:111], v[8:11]\n"
"ds_read_b64_tr_b16 v[118:119], v235 offset:18432\n"
"v_and_b32 v160, 0x3f80, v160\n"
"v_mfma_f32_16x16x32_bf16 v[8:11], v[208:211], a[144:147], v[8:11]\n"
"ds_read_b64_tr_b16 v[102:103], v235 offset:2048\n"
"v_add_f32 v27, v27, v85\n"
"v_mfma_f32_16x16x32_bf16 v[8:11], v[212:215], a[148:151], v[8:11]\n"
"ds_read_b64_tr_b16 v[106:107], v231 offset:10240\n"
"v_lshlrev_b32 v160, v161, v160\n"
"v_mfma_f32_16x16x32_bf16 v[8:11], v[216:219], a[152:155], v[8:11]\n"
"ds_read_b64_tr_b16 v[96:97], v231 offset:0\n"
"v_and_b32 v93, s73, v93\n"
"v_mfma_f32_16x16x32_bf16 v[8:11], v[220:223], a[156:159], v[8:11]\n"
"ds_read_b64_tr_b16 v[108:109], v235 offset:8192\n"
"v_add_f32 v41, v41, v92\n"
"v_mfma_f32_16x16x32_bf16 v[24:27], v[220:223], a[172:175], v[24:27]\n"
"v_bfe_i32 v161, v242, 5, 1\n"
"v_lshlrev_b32 v206, 16, v244\n"
"v_mfma_f32_16x16x32_bf16 v[24:27], v[216:219], a[168:171], v[24:27]\n"
"ds_read_b64_tr_b16 v[114:115], v231 offset:18432\n"
"v_mfma_f32_16x16x32_bf16 v[24:27], v[212:215], a[164:167], v[24:27]\n"
"ds_read_b64_tr_b16 v[104:105], v231 offset:8192\n"
"v_lshlrev_b32 v203, 16, v247\n"
"v_mfma_f32_16x16x32_bf16 v[24:27], v[208:211], a[160:163], v[24:27]\n"
"ds_read_b64_tr_b16 v[116:117], v235 offset:16384\n"
"v_add_f32 v48, v48, v206\n"
"v_mfma_f32_16x16x32_bf16 v[24:27], v[192:195], a[124:127], v[24:27]\n"
"ds_read_b64_tr_b16 v[110:111], v235 offset:10240\n"
"v_and_b32 v89, s73, v89\n"
"v_mfma_f32_16x16x32_bf16 v[24:27], v[188:191], a[120:123], v[24:27]\n"
"v_add_f32 v45, v45, v94\n"
"v_add_f32 v47, v47, v95\n"
"v_mfma_f32_16x16x32_bf16 v[24:27], v[184:187], a[116:119], v[24:27]\n"
"ds_read_b64_tr_b16 v[100:101], v235 offset:0\n"
"v_add_f32 v43, v43, v93\n"
"v_mfma_f32_16x16x32_bf16 v[24:27], v[180:183], a[112:115], v[24:27]\n"
"ds_read_b64_tr_b16 v[122:123], v231 offset:26624\n"
"v_add_f32 v46, v46, v207\n"
"v_mfma_f32_16x16x32_bf16 v[40:43], v[180:183], a[128:131], v[40:43]\n"
"v_add_f32 v33, v33, v88\n"
"v_and_b32 v244, s73, v244\n"
"v_mfma_f32_16x16x32_bf16 v[40:43], v[184:187], a[132:135], v[40:43]\n"
"ds_read_b64_tr_b16 v[120:121], v231 offset:24576\n"
"v_lshlrev_b32 v207, 16, v245\n"
"v_mfma_f32_16x16x32_bf16 v[40:43], v[188:191], a[136:139], v[40:43]\n"
"v_and_b32 v163, v160, v161\n"
"v_lshlrev_b32 v202, 16, v246\n"
"v_mfma_f32_16x16x32_bf16 v[40:43], v[192:195], a[140:143], v[40:43]\n"
"ds_read_b64_tr_b16 v[224:225], v235 offset:24576\n"
"v_and_b32 v246, s73, v246\n"
"v_mfma_f32_16x16x32_bf16 v[40:43], v[208:211], a[176:179], v[40:43]\n"
"v_add_f32 v52, v52, v202\n"
"v_and_b32 v247, s73, v247\n"
"v_mfma_f32_16x16x32_bf16 v[40:43], v[212:215], a[180:183], v[40:43]\n"
"ds_read_b64_tr_b16 v[226:227], v235 offset:26624\n"
"v_and_b32 v245, s73, v245\n"
"v_mfma_f32_16x16x32_bf16 v[40:43], v[216:219], a[184:187], v[40:43]\n"
"v_mov_b32 v161, 0\n"
"v_add_f32 v54, v54, v203\n"
"v_mfma_f32_16x16x32_bf16 v[40:43], v[220:223], a[188:191], v[40:43]\n"
"s_barrier\n"
"s_waitcnt lgkmcnt(3)\n"
"v_mfma_f32_16x16x32_bf16 v[12:15], v[96:99], a[96:99], v[12:15]\n"
"ds_read_b64_tr_b16 v[184:185], v232 offset:32768\n"
"v_add_f32 v49, v49, v244\n"
"v_mfma_f32_16x16x32_bf16 v[12:15], v[100:103], a[100:103], v[12:15]\n"
"ds_read_b64_tr_b16 v[216:217], v228 offset:57344\n"
"v_mfma_f32_16x16x32_bf16 v[12:15], v[104:107], a[104:107], v[12:15]\n"
"ds_read_b64_tr_b16 v[188:189], v228 offset:40960\n"
"v_add_f32 v35, v35, v89\n"
"v_mfma_f32_16x16x32_bf16 v[12:15], v[108:111], a[108:111], v[12:15]\n"
"ds_read_b64_tr_b16 v[190:191], v228 offset:43008\n"
"v_mfma_f32_16x16x32_bf16 v[12:15], v[112:115], a[144:147], v[12:15]\n"
"ds_read_b64_tr_b16 v[192:193], v232 offset:40960\n"
"v_add_f32 v53, v53, v246\n"
"v_mfma_f32_16x16x32_bf16 v[12:15], v[116:119], a[148:151], v[12:15]\n"
"s_waitcnt lgkmcnt(5)\n"
"ds_read_b64_tr_b16 v[212:213], v232 offset:49152\n"
"v_add_f32 v51, v51, v245\n"
"v_mfma_f32_16x16x32_bf16 v[12:15], v[120:123], a[152:155], v[12:15]\n"
"ds_read_b64_tr_b16 v[182:183], v228 offset:34816\n"
"v_xor_b32 v162, v160, v163\n"
"v_mfma_f32_16x16x32_bf16 v[12:15], v[224:227], a[156:159], v[12:15]\n"
"global_load_dword v200, v201, s[6:7] sc1\n"
"v_mfma_f32_16x16x32_bf16 v[28:31], v[224:227], a[172:175], v[28:31]\n"
"v_add_f32 v50, v50, v207\n"
"v_add_f32 v55, v55, v247\n"
"v_mfma_f32_16x16x32_bf16 v[28:31], v[120:123], a[168:171], v[28:31]\n"
"s_waitcnt vmcnt(9)\n"
"v_lshlrev_b32 v58, 16, v125\n"
"v_lshlrev_b32 v62, 16, v127\n"
"v_mfma_f32_16x16x32_bf16 v[28:31], v[116:119], a[164:167], v[28:31]\n"
"ds_read_b64_tr_b16 v[186:187], v232 offset:34816\n"
"ds_read_b64_tr_b16 v[214:215], v232 offset:51200\n"
"v_mfma_f32_16x16x32_bf16 v[28:31], v[112:115], a[160:163], v[28:31]\n"
"ds_read_b64_tr_b16 v[194:195], v232 offset:43008\n"
"v_and_b32 v61, s73, v126\n"
"v_mfma_f32_16x16x32_bf16 v[28:31], v[108:111], a[124:127], v[28:31]\n"
"s_waitcnt vmcnt(8)\n"
"ds_read_b64_tr_b16 v[210:211], v228 offset:51200\n"
"v_and_b32 v57, s73, v124\n"
"v_mfma_f32_16x16x32_bf16 v[28:31], v[104:107], a[120:123], v[28:31]\n"
"ds_read_b64_tr_b16 v[222:223], v232 offset:59392\n"
"v_lshlrev_b32 v56, 16, v124\n"
"v_mfma_f32_16x16x32_bf16 v[28:31], v[100:103], a[116:119], v[28:31]\n"
"ds_read_b64_tr_b16 v[180:181], v228 offset:32768\n"
"v_lshlrev_b32 v205, 16, v129\n"
"v_mfma_f32_16x16x32_bf16 v[28:31], v[96:99], a[112:115], v[28:31]\n"
"s_waitcnt vmcnt(7)\n"
"v_and_b32 v59, s73, v125\n"
"v_lshlrev_b32 v206, 16, v130\n"
"v_mfma_f32_16x16x32_bf16 v[44:47], v[96:99], a[128:131], v[44:47]\n"
"ds_read_b64_tr_b16 v[220:221], v232 offset:57344\n"
"v_lshlrev_b32 v204, 16, v128\n"
"v_mfma_f32_16x16x32_bf16 v[44:47], v[100:103], a[132:135], v[44:47]\n"
"ds_read_b64_tr_b16 v[208:209], v228 offset:49152\n"
"v_and_b32 v130, s73, v130\n"
"v_mfma_f32_16x16x32_bf16 v[44:47], v[104:107], a[136:139], v[44:47]\n"
"s_waitcnt vmcnt(6)\n"
"ds_read_b64_tr_b16 v[102:103], v233 offset:34816\n"
"v_add_f32 v58, v58, v205\n"
"v_mfma_f32_16x16x32_bf16 v[44:47], v[108:111], a[140:143], v[44:47]\n"
"v_lshlrev_b32 v207, 16, v131\n"
"v_add_f32 v62, v62, v207\n"
"v_mfma_f32_16x16x32_bf16 v[44:47], v[112:115], a[176:179], v[44:47]\n"
"ds_read_b64_tr_b16 v[218:219], v228 offset:59392\n"
"v_and_b32 v131, s73, v131\n"
"v_mfma_f32_16x16x32_bf16 v[44:47], v[116:119], a[180:183], v[44:47]\n"
"s_waitcnt vmcnt(5)\n"
"ds_read_b64_tr_b16 v[96:97], v229 offset:32768\n"
"v_lshlrev_b32 v205, 16, v135\n"
"v_mfma_f32_16x16x32_bf16 v[44:47], v[120:123], a[184:187], v[44:47]\n"
"v_lshlrev_b32 v202, 16, v132\n"
"v_lshlrev_b32 v207, 16, v137\n"
"v_mfma_f32_16x16x32_bf16 v[44:47], v[224:227], a[188:191], v[44:47]\n"
"s_waitcnt lgkmcnt(5)\n"
"ds_read_b64_tr_b16 v[120:121], v229 offset:57344\n"
"v_add_f32 v56, v56, v204\n"
"v_mfma_f32_16x16x32_bf16 v[0:3], v[180:183], a[48:51], v[0:3]\n"
"s_waitcnt vmcnt(4)\n"
"ds_read_b64_tr_b16 v[116:117], v233 offset:49152\n"
"v_add_f32 v61, v61, v130\n"
"v_mfma_f32_16x16x32_bf16 v[0:3], v[184:187], a[52:55], v[0:3]\n"
"v_lshlrev_b32 v60, 16, v126\n"
"v_lshlrev_b32 v204, 16, v134\n"
"v_mfma_f32_16x16x32_bf16 v[0:3], v[188:191], a[56:59], v[0:3]\n"
"ds_read_b64_tr_b16 v[110:111], v233 offset:43008\n"
"v_add_f32 v56, v56, v202\n"
"v_mfma_f32_16x16x32_bf16 v[0:3], v[192:195], a[60:63], v[0:3]\n"
"s_waitcnt lgkmcnt(5)\n"
"s_waitcnt vmcnt(3)\n"
"ds_read_b64_tr_b16 v[112:113], v229 offset:49152\n"
"v_and_b32 v63, s73, v127\n"
"v_mfma_f32_16x16x32_bf16 v[0:3], v[208:211], a[0:3], v[0:3]\n"
"ds_read_b64_tr_b16 v[224:225], v233 offset:57344\n"
"v_and_b32 v134, s73, v134\n"
"v_mfma_f32_16x16x32_bf16 v[0:3], v[212:215], a[4:7], v[0:3]\n"
"s_waitcnt lgkmcnt(5)\n"
"s_waitcnt vmcnt(2)\n"
"ds_read_b64_tr_b16 v[108:109], v233 offset:40960\n"
"v_and_b32 v129, s73, v129\n"
"v_mfma_f32_16x16x32_bf16 v[0:3], v[216:219], a[8:11], v[0:3]\n"
"v_add_f32 v60, v60, v206\n"
"v_add_f32 v60, v60, v204\n"
"v_mfma_f32_16x16x32_bf16 v[0:3], v[220:223], a[12:15], v[0:3]\n"
"ds_read_b64_tr_b16 v[98:99], v229 offset:34816\n"
"ds_read_b64_tr_b16 v[122:123], v229 offset:59392\n"
"v_mfma_f32_16x16x32_bf16 v[16:19], v[220:223], a[28:31], v[16:19]\n"
"v_mov_b32 v165, 0\n"
"v_add_f32 v59, v59, v129\n"
"v_mfma_f32_16x16x32_bf16 v[16:19], v[216:219], a[24:27], v[16:19]\n"
"v_add_f32 v62, v62, v205\n"
"v_lshlrev_b32 v206, 16, v136\n"
"v_mfma_f32_16x16x32_bf16 v[16:19], v[212:215], a[20:23], v[16:19]\n"
"ds_read_b64_tr_b16 v[104:105], v229 offset:40960\n"
"ds_read_b64_tr_b16 v[226:227], v233 offset:59392\n"
"v_mfma_f32_16x16x32_bf16 v[16:19], v[208:211], a[16:19], v[16:19]\n"
"v_add_f32 v56, v56, v206\n"
"v_lshlrev_b32 v202, 16, v138\n"
"v_mfma_f32_16x16x32_bf16 v[16:19], v[192:195], a[76:79], v[16:19]\n"
"s_waitcnt vmcnt(0)\n"
"ds_read_b64_tr_b16 v[118:119], v233 offset:51200\n"
"v_subrev_f32 v2, s22, v2\n"
"v_mfma_f32_16x16x32_bf16 v[16:19], v[188:191], a[72:75], v[16:19]\n"
"v_subrev_u32 v200, s21, v200\n"
"ds_read_b64_tr_b16 v[114:115], v229 offset:51200\n"
"v_mfma_f32_16x16x32_bf16 v[16:19], v[184:187], a[68:71], v[16:19]\n"
"v_cmp_lt_u32 vcc, s69, v200\n"
"ds_read_b64_tr_b16 v[100:101], v233 offset:32768\n"
"v_mfma_f32_16x16x32_bf16 v[16:19], v[180:183], a[64:67], v[16:19]\n"
"v_lshlrev_b32 v206, 16, v142\n"
"s_cbranch_vccnz LGR_%=\nLGRK_%=:\n"
"v_mfma_f32_16x16x32_bf16 v[32:35], v[180:183], a[80:83], v[32:35]\n"
"v_subrev_f32 v3, s22, v3\n"
"s_cmp_eq_u32 s93, 0\ns_cbranch_scc1 LEP_%=\nLEPK_%=:\n"
"v_mfma_f32_16x16x32_bf16 v[32:35], v[184:187], a[84:87], v[32:35]\n"
"s_bfe_u32 s20, s89, 65536\ns_lshl_b32 s20, s20, 13\ns_add_u32 s20, s20, 65536\n"
"ds_read_b64_tr_b16 v[106:107], v229 offset:43008\n"
"v_and_b32 v128, s73, v128\n"
"v_mfma_f32_16x16x32_bf16 v[32:35], v[188:191], a[88:91], v[32:35]\n"
"buffer_load_dwordx4 v[72:75], v236, s[32:35], s20 offen sc1\n"
"v_mfma_f32_16x16x32_bf16 v[32:35], v[192:195], a[92:95], v[32:35]\n"
"v_lshlrev_b32 v203, 16, v133\n"
"v_lshlrev_b32 v205, 16, v141\n"
"v_mfma_f32_16x16x32_bf16 v[32:35], v[208:211], a[32:35], v[32:35]\n"
"s_bfe_u32 s20, s89, 65540\ns_lshl_b32 s20, s20, 13\ns_add_u32 s20, s20, 1114112\n"
"v_lshlrev_b32 v204, 16, v140\n"
"v_add_f32 v56, v56, v204\n"
"v_mfma_f32_16x16x32_bf16 v[32:35], v[212:215], a[36:39], v[32:35]\n"
"buffer_load_dwordx4 v[76:79], v236, s[32:35], s20 offen sc1\n"
"v_mfma_f32_16x16x32_bf16 v[32:35], v[216:219], a[40:43], v[32:35]\n"
"v_and_b32 v136, s73, v136\n"
"v_subrev_f32 v17, s22, v17\n"
"v_mfma_f32_16x16x32_bf16 v[32:35], v[220:223], a[44:47], v[32:35]\n"
"s_bfe_u32 s20, s89, 65544\ns_lshl_b32 s20, s20, 13\ns_add_u32 s20, s20, 2162688\n"
"v_add_f32 v61, v61, v134\n"
"v_and_b32 v141, s73, v141\n"
"v_mfma_f32_16x16x32_bf16 v[48:51], v[220:223], a[228:231], v[48:51]\n"
"buffer_load_dwordx4 v[80:83], v236, s[32:35], s20 offen sc1\n"
"v_mfma_f32_16x16x32_bf16 v[48:51], v[216:219], a[224:227], v[48:51]\n"
"v_add_f32 v58, v58, v203\n"
"v_subrev_f32 v19, s22, v19\n"
"v_mfma_f32_16x16x32_bf16 v[48:51], v[212:215], a[212:215], v[48:51]\n"
"s_bfe_u32 s20, s89, 65548\ns_lshl_b32 s20, s20, 13\ns_add_u32 s20, s20, 3211264\n"
"v_and_b32 v140, s73, v140\n"
"v_lshlrev_b32 v204, 16, v146\n"
"v_mfma_f32_16x16x32_bf16 v[48:51], v[208:211], a[208:211], v[48:51]\n"
"buffer_load_dwordx4 v[84:87], v236, s[32:35], s20 offen sc1\n"
"v_mfma_f32_16x16x32_bf16 v[48:51], v[192:195], a[236:239], v[48:51]\n"
"v_add_f32 v57, v57, v128\n"
"v_subrev_f32 v34, s22, v34\n"
"v_mfma_f32_16x16x32_bf16 v[48:51], v[188:191], a[232:235], v[48:51]\n"
"s_bfe_u32 s20, s89, 65552\ns_lshl_b32 s20, s20, 13\ns_add_u32 s20, s20, 4259840\n"
"ds_read_b64_tr_b16 v[214:215], v234 offset:51200\n"
"v_and_b32 v132, s73, v132\n"
"v_mfma_f32_16x16x32_bf16 v[48:51], v[184:187], a[220:223], v[48:51]\n"
"buffer_load_dwordx4 v[88:91], v236, s[32:35], s20 offen sc1\n"
"v_mfma_f32_16x16x32_bf16 v[48:51], v[180:183], a[216:219], v[48:51]\n"
"s_waitcnt lgkmcnt(1)\n"
"v_add_f32 v60, v60, v202\n"
"v_add_f32 v60, v60, v206\n"
"v_mfma_f32_16x16x32_bf16 v[4:7], v[96:99], a[48:51], v[4:7]\n"
"s_bfe_u32 s20, s89, 65556\ns_lshl_b32 s20, s20, 13\ns_add_u32 s20, s20, 5308416\n"
"v_lshlrev_b32 v206, 16, v148\n"
"v_subrev_f32 v35, s22, v35\n"
"v_mfma_f32_16x16x32_bf16 v[4:7], v[100:103], a[52:55], v[4:7]\n"
"buffer_load_dwordx4 v[92:95], v236, s[32:35], s20 offen sc1\n"
"v_mfma_f32_16x16x32_bf16 v[4:7], v[104:107], a[56:59], v[4:7]\n"
"v_add_f32 v60, v60, v204\n"
"v_subrev_f32 v0, s22, v0\n"
"v_mfma_f32_16x16x32_bf16 v[4:7], v[108:111], a[60:63], v[4:7]\n"
"s_bfe_u32 s20, s89, 65560\ns_lshl_b32 s20, s20, 13\ns_add_u32 s20, s20, 6356992\n"
"v_add_f32 v57, v57, v132\n"
"v_subrev_f32 v33, s22, v33\n"
"v_mfma_f32_16x16x32_bf16 v[4:7], v[112:115], a[0:3], v[4:7]\n"
"buffer_load_dwordx4 v[244:247], v236, s[32:35], s20 offen sc1\n"
"v_mfma_f32_16x16x32_bf16 v[4:7], v[116:119], a[4:7], v[4:7]\n"
"v_and_b32 v138, s73, v138\n"
"v_subrev_f32 v18, s22, v18\n"
"v_mfma_f32_16x16x32_bf16 v[4:7], v[120:123], a[8:11], v[4:7]\n"
"s_bfe_u32 s20, s89, 65564\ns_lshl_b32 s20, s20, 13\ns_add_u32 s20, s20, 7405568\n"
"v_and_b32 v135, s73, v135\n"
"v_subrev_f32 v49, s22, v49\n"
"v_mfma_f32_16x16x32_bf16 v[4:7], v[224:227], a[12:15], v[4:7]\n"
"buffer_load_dwordx4 v[156:159], v236, s[32:35], s20 offen sc1\n"
"v_add_f32 v58, v58, v207\n"
"v_lshlrev_b32 v203, 16, v139\n"
"v_add_f32 v58, v58, v205\n"
"v_and_b32 v142, s73, v142\n"
"v_lshlrev_b32 v202, 16, v144\n"
"v_add_f32 v56, v56, v202\n"
"v_add_f32 v56, v56, v206\n"
"v_subrev_f32 v1, s22, v1\n"
"v_mfma_f32_16x16x32_bf16 v[20:23], v[224:227], a[28:31], v[20:23]\n"
"v_add_f32 v63, v63, v131\n"
"v_lshlrev_b32 v204, 16, v152\n"
"v_mfma_f32_16x16x32_bf16 v[20:23], v[120:123], a[24:27], v[20:23]\n"
"v_and_b32 v133, s73, v133\n"
"v_add_f32 v59, v59, v133\n"
"v_mfma_f32_16x16x32_bf16 v[20:23], v[116:119], a[20:23], v[20:23]\n"
"ds_read_b64_tr_b16 v[218:219], v230 offset:59392\n"
"v_and_b32 v137, s73, v137\n"
"v_mfma_f32_16x16x32_bf16 v[20:23], v[112:115], a[16:19], v[20:23]\n"
"v_and_b32 v148, s73, v148\n"
"v_subrev_f32 v16, s22, v16\n"
"v_mfma_f32_16x16x32_bf16 v[20:23], v[108:111], a[76:79], v[20:23]\n"
"v_add_f32 v62, v62, v203\n"
"v_subrev_f32 v6, s22, v6\n"
"v_mfma_f32_16x16x32_bf16 v[20:23], v[104:107], a[72:75], v[20:23]\n"
"v_add_f32 v57, v57, v136\n"
"v_lshlrev_b32 v205, 16, v147\n"
"v_mfma_f32_16x16x32_bf16 v[20:23], v[100:103], a[68:71], v[20:23]\n"
"ds_read_b64_tr_b16 v[194:195], v234 offset:43008\n"
"v_subrev_f32 v32, s22, v32\n"
"v_mfma_f32_16x16x32_bf16 v[20:23], v[96:99], a[64:67], v[20:23]\n"
"ds_read_b64_tr_b16 v[184:185], v234 offset:32768\n"
"v_add_f32 v59, v59, v137\n"
"v_and_b32 v139, s73, v139\n"
"v_add_f32 v59, v59, v141\n"
"v_lshlrev_b32 v207, 16, v143\n"
"v_lshlrev_b32 v203, 16, v145\n"
"v_and_b32 v145, s73, v145\n"
"v_add_f32 v58, v58, v203\n"
"v_add_f32 v59, v59, v145\n"
"v_and_b32 v147, s73, v147\n"
"v_lshlrev_b32 v203, 16, v151\n"
"v_subrev_f32 v4, s22, v4\n"
"v_subrev_f32 v5, s22, v5\n"
"v_subrev_f32 v7, s22, v7\n"
"v_cvt_pk_bf16_f32 v0, v0, v1\n"
"v_cvt_pk_bf16_f32 v1, v2, v3\n"
"v_cvt_pk_bf16_f32 v3, v6, v7\n"
"v_mfma_f32_16x16x32_bf16 v[36:39], v[96:99], a[80:83], v[36:39]\n"
"v_add_f32 v56, v56, v204\n"
"v_cvt_pk_bf16_f32 v2, v4, v5\n"
"v_mfma_f32_16x16x32_bf16 v[36:39], v[100:103], a[84:87], v[36:39]\n"
"ds_read_b64_tr_b16 v[192:193], v234 offset:40960\n"
"v_add_f32 v61, v61, v138\n"
"v_mfma_f32_16x16x32_bf16 v[36:39], v[104:107], a[88:91], v[36:39]\n"
"v_add_f32 v57, v57, v140\n"
"v_subrev_f32 v23, s22, v23\n"
"v_mfma_f32_16x16x32_bf16 v[36:39], v[108:111], a[92:95], v[36:39]\n"
"ds_read_b64_tr_b16 v[180:181], v230 offset:32768\n"
"v_and_b32 v144, s73, v144\n"
"v_mfma_f32_16x16x32_bf16 v[36:39], v[112:115], a[32:35], v[36:39]\n"
"ds_read_b64_tr_b16 v[210:211], v230 offset:51200\n"
"v_add_f32 v62, v62, v207\n"
"v_add_f32 v62, v62, v205\n"
"v_lshlrev_b32 v207, 16, v149\n"
"v_add_f32 v58, v58, v207\n"
"v_lshlrev_b32 v202, 16, v150\n"
"v_permlane16_swap_b32 v0, v2\n"
"v_permlane16_swap_b32 v1, v3\n"
"v_subrev_f32 v20, s22, v20\n"
"v_cvt_pk_bf16_f32 v16, v16, v17\n"
"v_mfma_f32_16x16x32_bf16 v[36:39], v[116:119], a[36:39], v[36:39]\n"
"ds_read_b64_tr_b16 v[186:187], v234 offset:34816\n"
"v_add_f32 v63, v63, v135\n"
"v_mfma_f32_16x16x32_bf16 v[36:39], v[120:123], a[40:43], v[36:39]\n"
"ds_read_b64_tr_b16 v[188:189], v230 offset:40960\n"
"buffer_store_dwordx4 v[0:3], v238, s[36:39], 0 offen sc0 sc1\n"
"v_and_b32 v143, s73, v143\n"
"v_and_b32 v146, s73, v146\n"
"v_and_b32 v149, s73, v149\n"
"v_lshlrev_b32 v205, 16, v153\n"
"v_and_b32 v153, s73, v153\n"
"v_lshlrev_b32 v206, 16, v154\n"
"v_lshlrev_b32 v207, 16, v155\n"
"v_subrev_f32 v21, s22, v21\n"
"v_subrev_f32 v22, s22, v22\n"
"v_cvt_pk_bf16_f32 v17, v18, v19\n"
"v_cvt_pk_bf16_f32 v18, v20, v21\n"
"v_cvt_pk_bf16_f32 v32, v32, v33\n"
"v_mfma_f32_16x16x32_bf16 v[36:39], v[224:227], a[44:47], v[36:39]\n"
"ds_read_b64_tr_b16 v[182:183], v230 offset:34816\n"
"v_add_f32 v63, v63, v139\n"
"v_add_f32 v61, v61, v142\n"
"v_add_f32 v57, v57, v144\n"
"v_add_f32 v57, v57, v148\n"
"v_add_f32 v59, v59, v149\n"
"v_cvt_pk_bf16_f32 v19, v22, v23\n"
"v_permlane16_swap_b32 v16, v18\n"
"v_cvt_pk_bf16_f32 v33, v34, v35\n"
"v_mfma_f32_16x16x32_bf16 v[52:55], v[224:227], a[228:231], v[52:55]\n"
"ds_read_b64_tr_b16 v[190:191], v230 offset:43008\n"
"v_add_f32 v59, v59, v153\n"
"v_mfma_f32_16x16x32_bf16 v[52:55], v[120:123], a[224:227], v[52:55]\n"
"v_permlane16_swap_b32 v17, v19\n"
"v_mfma_f32_16x16x32_bf16 v[52:55], v[116:119], a[212:215], v[52:55]\n"
"ds_read_b64_tr_b16 v[208:209], v230 offset:49152\n"
"v_subrev_f32 v36, s22, v36\n"
"v_mfma_f32_16x16x32_bf16 v[52:55], v[112:115], a[208:211], v[52:55]\n"
"ds_read_b64_tr_b16 v[222:223], v234 offset:59392\n"
"buffer_store_dwordx4 v[16:19], v238, s[36:39], s52 offen sc0 sc1\n"
"v_add_f32 v63, v63, v143\n"
"v_add_f32 v60, v60, v202\n"
"v_add_f32 v62, v62, v203\n"
"v_and_b32 v152, s73, v152\n"
"v_subrev_f32 v38, s22, v38\n"
"v_subrev_f32 v39, s22, v39\n"
"v_mfma_f32_16x16x32_bf16 v[52:55], v[108:111], a[236:239], v[52:55]\n"
"v_and_b32 v155, s73, v155\n"
"v_cvt_pk_bf16_f32 v35, v38, v39\n"
"v_mfma_f32_16x16x32_bf16 v[52:55], v[104:107], a[232:235], v[52:55]\n"
"ds_read_b64_tr_b16 v[216:217], v230 offset:57344\n"
"v_subrev_f32 v37, s22, v37\n"
"v_mfma_f32_16x16x32_bf16 v[52:55], v[100:103], a[220:223], v[52:55]\n"
"v_and_b32 v150, s73, v150\n"
"v_and_b32 v154, s73, v154\n"
"v_mfma_f32_16x16x32_bf16 v[52:55], v[96:99], a[216:219], v[52:55]\n"
"s_waitcnt lgkmcnt(4)\n"
"ds_read_b64_tr_b16 v[212:213], v234 offset:49152\n"
"ds_read_b64_tr_b16 v[220:221], v234 offset:57344\n"
"v_add_f32 v63, v63, v147\n"
"v_and_b32 v151, s73, v151\n"
"v_add_f32 v63, v63, v151\n"
"v_add_f32 v57, v57, v152\n"
"v_add_f32 v58, v58, v205\n"
"v_add_f32 v60, v60, v206\n"
"v_add_f32 v62, v62, v207\n"
"v_cvt_pk_bf16_f32 v34, v36, v37\n"
"v_permlane16_swap_b32 v33, v35\n"
"v_mfma_f32_16x16x32_bf16 v[8:11], v[180:183], a[48:51], v[8:11]\n"
"v_cvt_pk_bf16_f32 v56, v56, v57\n"
"v_cvt_pk_bf16_f32 v57, v58, v59\n"
"v_mfma_f32_16x16x32_bf16 v[8:11], v[184:187], a[52:55], v[8:11]\n"
"s_waitcnt lgkmcnt(5)\n"
"v_permlane16_swap_b32 v32, v34\n"
"v_mfma_f32_16x16x32_bf16 v[8:11], v[188:191], a[56:59], v[8:11]\n"
"buffer_store_dwordx4 v[32:35], v238, s[36:39], s53 offen sc0 sc1\n"
"ds_read_b64_tr_b16 v[120:121], v231 offset:57344\n"
"v_add_f32 v61, v61, v146\n"
"v_add_f32 v61, v61, v150\n"
"v_add_f32 v61, v61, v154\n"
"v_add_f32 v63, v63, v155\n"
"v_cvt_pk_bf16_f32 v58, v60, v61\n"
"v_cvt_pk_bf16_f32 v59, v62, v63\n"
"v_mfma_f32_16x16x32_bf16 v[8:11], v[192:195], a[60:63], v[8:11]\n"
"s_waitcnt lgkmcnt(4)\n"
"v_permlane16_swap_b32 v57, v59\n"
"v_mfma_f32_16x16x32_bf16 v[8:11], v[208:211], a[0:3], v[8:11]\n"
"s_waitcnt lgkmcnt(2)\n"
"v_permlane16_swap_b32 v56, v58\n"
"v_mfma_f32_16x16x32_bf16 v[8:11], v[212:215], a[4:7], v[8:11]\n"
"buffer_store_dwordx4 v[56:59], v253, s[36:39], 0 offen sc0 sc1\n"
"v_subrev_f32 v48, s22, v48\n"
"v_subrev_f32 v50, s22, v50\n"
"v_subrev_f32 v51, s22, v51\n"
"v_mfma_f32_16x16x32_bf16 v[8:11], v[216:219], a[8:11], v[8:11]\n"
"s_waitcnt lgkmcnt(1)\n"
"ds_read_b64_tr_b16 v[112:113], v231 offset:49152\n"
"v_subrev_f32 v54, s22, v54\n"
"v_mfma_f32_16x16x32_bf16 v[8:11], v[220:223], a[12:15], v[8:11]\n"
"ds_read_b64_tr_b16 v[100:101], v235 offset:32768\n"
"v_subrev_f32 v52, s22, v52\n"
"v_mfma_f32_16x16x32_bf16 v[24:27], v[220:223], a[28:31], v[24:27]\n"
"ds_read_b64_tr_b16 v[102:103], v235 offset:34816\n"
"v_subrev_f32 v53, s22, v53\n"
"v_mfma_f32_16x16x32_bf16 v[24:27], v[216:219], a[24:27], v[24:27]\n"
"v_cvt_pk_bf16_f32 v48, v48, v49\n"
"v_cvt_pk_bf16_f32 v49, v50, v51\n"
"v_mfma_f32_16x16x32_bf16 v[24:27], v[212:215], a[20:23], v[24:27]\n"
"ds_read_b64_tr_b16 v[96:97], v231 offset:32768\n"
"v_cvt_pk_bf16_f32 v50, v52, v53\n"
"v_mfma_f32_16x16x32_bf16 v[24:27], v[208:211], a[16:19], v[24:27]\n"
"s_nop 0\n"
"v_permlane16_swap_b32 v48, v50\n"
"v_mfma_f32_16x16x32_bf16 v[24:27], v[192:195], a[76:79], v[24:27]\n"
"ds_read_b64_tr_b16 v[116:117], v235 offset:49152\n"
"v_subrev_f32 v55, s22, v55\n"
"v_mfma_f32_16x16x32_bf16 v[24:27], v[188:191], a[72:75], v[24:27]\n"
"ds_read_b64_tr_b16 v[98:99], v231 offset:34816\n"
"v_cvt_pk_bf16_f32 v51, v54, v55\n"
"v_mfma_f32_16x16x32_bf16 v[24:27], v[184:187], a[68:71], v[24:27]\n"
"s_nop 0\n"
"v_permlane16_swap_b32 v49, v51\n"
"v_mfma_f32_16x16x32_bf16 v[24:27], v[180:183], a[64:67], v[24:27]\n"
"buffer_store_dwordx4 v[48:51], v252, s[36:39], 0 offen sc0 sc1\n"
"ds_read_b64_tr_b16 v[104:105], v231 offset:40960\n"
"v_mfma_f32_16x16x32_bf16 v[40:43], v[180:183], a[80:83], v[40:43]\n"
"ds_read_b64_tr_b16 v[106:107], v231 offset:43008\n"
"ds_read_b64_tr_b16 v[108:109], v235 offset:40960\n"
"v_mfma_f32_16x16x32_bf16 v[40:43], v[184:187], a[84:87], v[40:43]\n"
"ds_read_b64_tr_b16 v[110:111], v235 offset:43008\n"
"v_mfma_f32_16x16x32_bf16 v[40:43], v[188:191], a[88:91], v[40:43]\n"
"ds_read_b64_tr_b16 v[114:115], v231 offset:51200\n"
"v_subrev_f32 v10, s22, v10\n"
"v_mfma_f32_16x16x32_bf16 v[40:43], v[192:195], a[92:95], v[40:43]\n"
"ds_read_b64_tr_b16 v[118:119], v235 offset:51200\n"
"v_mfma_f32_16x16x32_bf16 v[40:43], v[208:211], a[32:35], v[40:43]\n"
"ds_read_b64_tr_b16 v[122:123], v231 offset:59392\n"
"v_subrev_f32 v24, s22, v24\n"
"v_mfma_f32_16x16x32_bf16 v[40:43], v[212:215], a[36:39], v[40:43]\n"
"ds_read_b64_tr_b16 v[224:225], v235 offset:57344\n"
"ds_read_b64_tr_b16 v[226:227], v235 offset:59392\n"
"v_mfma_f32_16x16x32_bf16 v[40:43], v[216:219], a[40:43], v[40:43]\n"
"v_mfma_f32_16x16x32_bf16 v[40:43], v[220:223], a[44:47], v[40:43]\n"
"s_waitcnt lgkmcnt(8)\n"
"v_mov_b32 v160, 0\n"
"v_subrev_f32 v11, s22, v11\n"
"v_mfma_f32_16x16x32_bf16 v[12:15], v[96:99], a[48:51], v[12:15]\n"
"v_subrev_f32 v8, s22, v8\n"
"v_subrev_f32 v9, s22, v9\n"
"v_mfma_f32_16x16x32_bf16 v[12:15], v[100:103], a[52:55], v[12:15]\n"
"s_waitcnt lgkmcnt(5)\n"
"v_cvt_pk_bf16_f32 v8, v8, v9\n"
"v_mfma_f32_16x16x32_bf16 v[12:15], v[104:107], a[56:59], v[12:15]\n"
"v_subrev_f32 v27, s22, v27\n"
"v_mfma_f32_16x16x32_bf16 v[12:15], v[108:111], a[60:63], v[12:15]\n"
"s_waitcnt lgkmcnt(3)\n"
"v_mfma_f32_16x16x32_bf16 v[12:15], v[112:115], a[0:3], v[12:15]\n"
"v_mfma_f32_16x16x32_bf16 v[12:15], v[116:119], a[4:7], v[12:15]\n"
"s_waitcnt lgkmcnt(0)\n"
"v_mfma_f32_16x16x32_bf16 v[12:15], v[120:123], a[8:11], v[12:15]\n"
"v_subrev_f32 v40, s22, v40\n"
"v_mfma_f32_16x16x32_bf16 v[12:15], v[224:227], a[12:15], v[12:15]\n"
"s_waitcnt vmcnt(5)\n"
"v_mfma_f32_16x16x32_bf16 v[64:67], v[162:165], v[72:75], 0\n"
"v_mfma_f32_16x16x32_bf16 v[64:67], v[162:165], v[76:79], v[64:67]\n"
"v_cvt_pk_bf16_f32 v9, v10, v11\n"
"v_mfma_f32_16x16x32_bf16 v[64:67], v[162:165], v[80:83], v[64:67]\n"
"s_nop 2\n"
"v_subrev_f32 v12, s22, v12\n"
"v_mfma_f32_16x16x32_bf16 v[64:67], v[162:165], v[84:87], v[64:67]\n"
"v_subrev_f32 v25, s22, v25\n"
"v_mfma_f32_16x16x32_bf16 v[64:67], v[162:165], v[88:91], v[64:67]\n"
"v_subrev_f32 v13, s22, v13\n"
"v_mfma_f32_16x16x32_bf16 v[64:67], v[162:165], v[92:95], v[64:67]\n"
"v_subrev_f32 v15, s22, v15\n"
"v_mfma_f32_16x16x32_bf16 v[64:67], v[162:165], v[244:247], v[64:67]\n"
"v_subrev_f32 v14, s22, v14\n"
"v_mfma_f32_16x16x32_bf16 v[64:67], v[162:165], v[156:159], v[64:67]\n"
"v_subrev_f32 v42, s22, v42\n"
"v_mfma_f32_16x16x32_bf16 v[68:71], v[160:163], v[72:75], 0\n"
"v_cvt_pk_bf16_f32 v11, v14, v15\n"
"v_subrev_f32 v41, s22, v41\n"
"v_mfma_f32_16x16x32_bf16 v[68:71], v[160:163], v[76:79], v[68:71]\n"
"v_cvt_pk_bf16_f32 v10, v12, v13\n"
"v_cvt_pk_bf16_f32 v24, v24, v25\n"
"v_mfma_f32_16x16x32_bf16 v[68:71], v[160:163], v[80:83], v[68:71]\n"
"v_mfma_f32_16x16x32_bf16 v[68:71], v[160:163], v[84:87], v[68:71]\n"
"v_permlane16_swap_b32 v9, v11\n"
"v_mfma_f32_16x16x32_bf16 v[68:71], v[160:163], v[88:91], v[68:71]\n"
"v_mfma_f32_16x16x32_bf16 v[68:71], v[160:163], v[92:95], v[68:71]\n"
"v_cvt_pk_bf16_f32 v64, v64, v65\n"
"v_cvt_pk_bf16_f32 v40, v40, v41\n"
"v_mfma_f32_16x16x32_bf16 v[68:71], v[160:163], v[244:247], v[68:71]\n"
"v_permlane16_swap_b32 v8, v10\n"
"v_mfma_f32_16x16x32_bf16 v[68:71], v[160:163], v[156:159], v[68:71]\n"
"buffer_store_dwordx4 v[8:11], v239, s[36:39], 0 offen sc0 sc1\n"
"v_mfma_f32_16x16x32_bf16 v[28:31], v[224:227], a[28:31], v[28:31]\n"
"v_mfma_f32_16x16x32_bf16 v[28:31], v[120:123], a[24:27], v[28:31]\n"
"v_cvt_pk_bf16_f32 v65, v66, v67\n"
"v_subrev_f32 v43, s22, v43\n"
"v_mfma_f32_16x16x32_bf16 v[28:31], v[116:119], a[20:23], v[28:31]\n"
"v_mfma_f32_16x16x32_bf16 v[28:31], v[112:115], a[16:19], v[28:31]\n"
"v_mfma_f32_16x16x32_bf16 v[28:31], v[108:111], a[76:79], v[28:31]\n"
"v_cvt_pk_bf16_f32 v66, v68, v69\n"
"v_cvt_pk_bf16_f32 v67, v70, v71\n"
"v_mfma_f32_16x16x32_bf16 v[28:31], v[104:107], a[72:75], v[28:31]\n"
"v_permlane16_swap_b32 v64, v66\n"
"v_mfma_f32_16x16x32_bf16 v[28:31], v[100:103], a[68:71], v[28:31]\n"
"v_permlane16_swap_b32 v65, v67\n"
"v_mfma_f32_16x16x32_bf16 v[28:31], v[96:99], a[64:67], v[28:31]\n"
"buffer_store_dwordx4 v[64:67], v253, s[36:39], 0 offen offset:128 sc0 sc1\n"
"v_cvt_pk_bf16_f32 v41, v42, v43\n"
"v_mfma_f32_16x16x32_bf16 v[44:47], v[96:99], a[80:83], v[44:47]\n"
"v_subrev_f32 v26, s22, v26\n"
"v_cvt_pk_bf16_f32 v25, v26, v27\n"
"v_mfma_f32_16x16x32_bf16 v[44:47], v[100:103], a[84:87], v[44:47]\n"
"v_mfma_f32_16x16x32_bf16 v[44:47], v[104:107], a[88:91], v[44:47]\n"
"s_nop 0\n"
"v_subrev_f32 v30, s22, v30\n"
"v_subrev_f32 v31, s22, v31\n"
"v_mfma_f32_16x16x32_bf16 v[44:47], v[108:111], a[92:95], v[44:47]\n"
"v_subrev_f32 v28, s22, v28\n"
"v_subrev_f32 v29, s22, v29\n"
"v_mfma_f32_16x16x32_bf16 v[44:47], v[112:115], a[32:35], v[44:47]\n"
"v_cvt_pk_bf16_f32 v26, v28, v29\n"
"v_cvt_pk_bf16_f32 v27, v30, v31\n"
"v_mfma_f32_16x16x32_bf16 v[44:47], v[116:119], a[36:39], v[44:47]\n"
"s_nop 0\n"
"v_permlane16_swap_b32 v25, v27\n"
"v_mfma_f32_16x16x32_bf16 v[44:47], v[120:123], a[40:43], v[44:47]\n"
"v_permlane16_swap_b32 v24, v26\n"
"v_mfma_f32_16x16x32_bf16 v[44:47], v[224:227], a[44:47], v[44:47]\n"
"buffer_store_dwordx4 v[24:27], v239, s[36:39], s52 offen sc0 sc1\n"
"s_nop 6\n"
"v_subrev_f32 v44, s22, v44\n"
"v_subrev_f32 v45, s22, v45\n"
"v_subrev_f32 v46, s22, v46\n"
"v_subrev_f32 v47, s22, v47\n"
"v_cvt_pk_bf16_f32 v42, v44, v45\n"
"v_cvt_pk_bf16_f32 v43, v46, v47\n"
"s_nop 0\n"
"v_permlane16_swap_b32 v40, v42\n"
"v_permlane16_swap_b32 v41, v43\n"
"buffer_store_dwordx4 v[40:43], v239, s[36:39], s53 offen sc0 sc1\n"
"s_branch LFIN_%=\n"
"LSW_%=:\n"
"s_add_u32 s16, s8, s95\n"
"s_addc_u32 s17, s9, 0\n"
"s_sub_u32 s18, 655360, s95\n"
"s_add_u32 s20, s8, s100\n"
"s_addc_u32 s21, s9, 0\n"
"s_sub_u32 s22, 655360, s100\n"
"s_mov_b32 s56, 0\n"
"s_mov_b32 s55, 0\n"
"s_mov_b32 s58, 0\n"
"s_mov_b32 s57, 0\n"
"s_mov_b32 s59, 163840\n"
"s_mov_b32 s52, 9216\n"
"s_mov_b32 s53, 18432\n"
"s_mov_b32 s54, 27648\n"
"v_lshrrev_b32 v206, 7, v242\n"
"v_mul_u32_u24 v206, 1280, v206\n"
"v_and_b32 v240, 0x7f, v240\n"
"v_add_u32 v240, v206, v240\n"
"buffer_load_dword v252, v240, s[16:19], 0 offen\n"
"s_branch LSWK_%=\n"
"LGP_%=:\n"
"s_mov_b32 s80, 0x40000\n"
"LGPa_%=:\n"
"s_sleep 1\n"
"global_load_dword v200, v201, s[6:7] sc1\n"
"s_sub_u32 s80, s80, 1\n"
"s_cmp_eq_u32 s80, 0\n"
"s_waitcnt vmcnt(0)\n"
"s_cbranch_scc1 LGPK_%=\n"
"v_subrev_u32 v200, s21, v200\n"
"v_cmp_lt_u32 vcc, s69, v200\n"
"s_nop 4\n"
"s_cbranch_vccnz LGPa_%=\n"
"s_branch LGPK_%=\n"
"LGQ_%=:\n"
"s_mov_b32 s80, 0x40000\n"
"LGQa_%=:\n"
"s_sleep 1\n"
"global_load_dword v200, v201, s[6:7] sc1\n"
"s_sub_u32 s80, s80, 1\n"
"s_cmp_eq_u32 s80, 0\n"
"s_waitcnt vmcnt(0)\n"
"s_cbranch_scc1 LGQK_%=\n"
"v_subrev_u32 v200, s21, v200\n"
"v_cmp_lt_u32 vcc, s69, v200\n"
"s_nop 4\n"
"s_cbranch_vccnz LGQa_%=\n"
"s_branch LGQK_%=\n"
"LGR_%=:\n"
"s_mov_b32 s80, 0x40000\n"
"LGRa_%=:\n"
"s_sleep 1\n"
"global_load_dword v200, v201, s[6:7] sc1\n"
"s_sub_u32 s80, s80, 1\n"
"s_cmp_eq_u32 s80, 0\n"
"s_waitcnt vmcnt(0)\n"
"s_cbranch_scc1 LGRK_%=\n"
"v_subrev_u32 v200, s21, v200\n"
"v_cmp_lt_u32 vcc, s69, v200\n"
"s_nop 4\n"
"s_cbranch_vccnz LGRa_%=\n"
"s_branch LGRK_%=\n"
"LGV_%=:\n"
"s_mov_b32 s80, 0x40000\n"
"LGVa_%=:\n"
"s_sleep 1\n"
"global_load_dword v200, v201, s[6:7] sc1\n"
"s_sub_u32 s80, s80, 1\n"
"s_cmp_eq_u32 s80, 0\n"
"s_waitcnt vmcnt(0)\n"
"s_cbranch_scc1 LGVK_%=\n"
"v_subrev_u32 v200, s21, v200\n"
"v_cmp_lt_u32 vcc, s69, v200\n"
"s_nop 4\n"
"s_cbranch_vccnz LGVa_%=\n"
"s_branch LGVK_%=\n"
"LEP_%=:\n"
"s_add_u32 s92, s92, 1\n"
"v_mov_b32 v200, s92\n"
"v_mov_b32 v255, 0\n"
"s_mov_b64 exec, 1\n"
"global_store_dword v255, v200, s[6:7] offset:128 sc1\n"
"s_mov_b64 exec, -1\n"
"s_branch LEPK_%=\n"
"LPCE0_%=:\n"
"s_memrealtime s[80:81]\n"
"s_sub_u32 s82, s67, s66\n"
"s_cmp_gt_i32 s82, 0\n"
"s_cselect_b32 s83, s67, s66\n"
"s_waitcnt lgkmcnt(0)\n"
"s_mov_b32 s63, 0\n"
"s_mov_b32 s62, 118\n"
"s_sub_u32 s82, s83, s80\n"
"s_cmp_gt_i32 s82, 3\n"
"s_cbranch_scc0 LPEE0_%=\n"
"LPDE0_%=:\n"
"s_sleep 1\n"
"s_memrealtime s[80:81]\n"
"s_waitcnt lgkmcnt(0)\n"
"s_sub_u32 s82, s83, s80\n"
"s_cmp_gt_i32 s82, 3\n"
"s_cbranch_scc1 LPDE0_%=\n"
"LPEE0_%=:\n"
"s_add_u32 s66, s66, s72\n"
"s_add_u32 s66, s66, 4\n"
"s_add_u32 s67, s80, s72\n"
"s_sub_u32 s72, s72, 3\n"
"s_max_u32 s72, s72, s62\n"
"s_branch LPKE0_%=\n"
"LPCE1_%=:\n"
"s_memrealtime s[80:81]\n"
"s_sub_u32 s82, s67, s66\n"
"s_cmp_gt_i32 s82, 0\n"
"s_cselect_b32 s83, s67, s66\n"
"s_waitcnt lgkmcnt(0)\n"
"s_add_u32 s72, s62, 16\n"
"s_sub_u32 s82, s83, s80\n"
"s_cmp_gt_i32 s82, 3\n"
"s_cbranch_scc0 LPEE1_%=\n"
"LPDE1_%=:\n"
"s_sleep 1\n"
"s_memrealtime s[80:81]\n"
"s_waitcnt lgkmcnt(0)\n"
"s_sub_u32 s82, s83, s80\n"
"s_cmp_gt_i32 s82, 3\n"
"s_cbranch_scc1 LPDE1_%=\n"
"LPEE1_%=:\n"
"s_add_u32 s66, s66, s72\n"
"s_add_u32 s66, s66, 4\n"
"s_add_u32 s67, s80, s72\n"
"s_sub_u32 s72, s72, 3\n"
"s_max_u32 s72, s72, s62\n"
"s_branch LPKE1_%=\n"
"LPCT0_%=:\n"
"s_memrealtime s[80:81]\n"
"s_sub_u32 s82, s67, s66\n"
"s_cmp_gt_i32 s82, 0\n"
"s_cselect_b32 s83, s67, s66\n"
"s_waitcnt lgkmcnt(0)\n"
"s_sub_u32 s82, s83, s80\n"
"s_cmp_gt_i32 s82, 3\n"
"s_cbranch_scc0 LPET0_%=\n"
"LPDT0_%=:\n"
"s_sleep 1\n"
"s_memrealtime s[80:81]\n"
"s_waitcnt lgkmcnt(0)\n"
"s_sub_u32 s82, s83, s80\n"
"s_cmp_gt_i32 s82, 3\n"
"s_cbranch_scc1 LPDT0_%=\n"
"LPET0_%=:\n"
"s_add_u32 s66, s66, s72\n"
"s_add_u32 s66, s66, 4\n"
"s_add_u32 s67, s80, s72\n"
"s_sub_u32 s72, s72, 3\n"
"s_max_u32 s72, s72, s62\n"
"s_branch LPKT0_%=\n"
"LPCT1_%=:\n"
"s_memrealtime s[80:81]\n"
"s_sub_u32 s82, s67, s66\n"
"s_cmp_gt_i32 s82, 0\n"
"s_cselect_b32 s83, s67, s66\n"
"s_waitcnt lgkmcnt(0)\n"
"s_add_u32 s72, s62, 16\n"
"s_sub_u32 s82, s83, s80\n"
"s_cmp_gt_i32 s82, 3\n"
"s_cbranch_scc0 LPET1_%=\n"
"LPDT1_%=:\n"
"s_sleep 1\n"
"s_memrealtime s[80:81]\n"
"s_waitcnt lgkmcnt(0)\n"
"s_sub_u32 s82, s83, s80\n"
"s_cmp_gt_i32 s82, 3\n"
"s_cbranch_scc1 LPDT1_%=\n"
"LPET1_%=:\n"
"s_add_u32 s66, s66, s72\n"
"s_add_u32 s66, s66, 4\n"
"s_add_u32 s67, s80, s72\n"
"s_sub_u32 s72, s72, 3\n"
"s_max_u32 s72, s72, s62\n"
"s_branch LPKT1_%=\n"
"LPCL0_%=:\n"
"s_memrealtime s[80:81]\n"
"s_sub_u32 s82, s67, s66\n"
"s_cmp_gt_i32 s82, 0\n"
"s_cselect_b32 s83, s67, s66\n"
"s_waitcnt lgkmcnt(0)\n"
"s_sub_u32 s82, s80, s67\n"
"s_add_u32 s63, s63, 1\n"
"s_min_i32 s81, s63, 0\n"
"s_cmp_gt_i32 s82, 20\n"
"s_cselect_b32 s63, s63, s81\n"
"s_cmp_ge_i32 s63, 2\n"
"s_cbranch_scc0 LPRL0_%=\n"
"s_mov_b32 s63, -9\n"
"s_add_u32 s62, s62, 18\n"
"s_min_u32 s62, s62, 146\n"
"s_max_u32 s72, s72, s62\n"
"s_add_u32 s83, s80, 200\n"
"s_and_b32 s83, s83, -1\n"
"LPRL0_%=:\n"
"s_sub_u32 s82, s83, s80\n"
"s_cmp_gt_i32 s82, 3\n"
"s_cbranch_scc0 LPEL0_%=\n"
"LPDL0_%=:\n"
"s_sleep 1\n"
"s_memrealtime s[80:81]\n"
"s_waitcnt lgkmcnt(0)\n"
"s_sub_u32 s82, s83, s80\n"
"s_cmp_gt_i32 s82, 3\n"
"s_cbranch_scc1 LPDL0_%=\n"
"LPEL0_%=:\n"
"s_add_u32 s66, s66, s72\n"
"s_add_u32 s66, s66, 4\n"
"s_add_u32 s67, s80, s72\n"
"s_sub_u32 s72, s72, 3\n"
"s_max_u32 s72, s72, s62\n"
"s_branch LPKL0_%=\n"
"LPCL1_%=:\n"
"s_memrealtime s[80:81]\n"
"s_sub_u32 s82, s67, s66\n"
"s_cmp_gt_i32 s82, 0\n"
"s_cselect_b32 s83, s67, s66\n"
"s_waitcnt lgkmcnt(0)\n"
"s_sub_u32 s82, s80, s67\n"
"s_add_u32 s63, s63, 1\n"
"s_min_i32 s81, s63, 0\n"
"s_cmp_gt_i32 s82, 20\n"
"s_cselect_b32 s63, s63, s81\n"
"s_cmp_ge_i32 s63, 2\n"
"s_cbranch_scc0 LPRL1_%=\n"
"s_mov_b32 s63, -9\n"
"s_add_u32 s62, s62, 18\n"
"s_min_u32 s62, s62, 146\n"
"s_max_u32 s72, s72, s62\n"
"s_add_u32 s83, s80, 200\n"
"s_and_b32 s83, s83, -1\n"
"LPRL1_%=:\n"
"s_sub_u32 s82, s83, s80\n"
"s_cmp_gt_i32 s82, 3\n"
"s_cbranch_scc0 LPEL1_%=\n"
"LPDL1_%=:\n"
"s_sleep 1\n"
"s_memrealtime s[80:81]\n"
"s_waitcnt lgkmcnt(0)\n"
"s_sub_u32 s82, s83, s80\n"
"s_cmp_gt_i32 s82, 3\n"
"s_cbranch_scc1 LPDL1_%=\n"
"LPEL1_%=:\n"
"s_add_u32 s66, s66, s72\n"
"s_add_u32 s66, s66, 4\n"
"s_add_u32 s67, s80, s72\n"
"s_sub_u32 s72, s72, 3\n"
"s_max_u32 s72, s72, s62\n"
"s_branch LPKL1_%=\n"
"LPCL2_%=:\n"
"s_memrealtime s[80:81]\n"
"s_sub_u32 s82, s67, s66\n"
"s_cmp_gt_i32 s82, 0\n"
"s_cselect_b32 s83, s67, s66\n"
"s_waitcnt lgkmcnt(0)\n"
"s_sub_u32 s82, s80, s67\n"
"s_add_u32 s63, s63, 1\n"
"s_min_i32 s81, s63, 0\n"
"s_cmp_gt_i32 s82, 20\n"
"s_cselect_b32 s63, s63, s81\n"
"s_cmp_ge_i32 s63, 2\n"
"s_cbranch_scc0 LPRL2_%=\n"
"s_mov_b32 s63, -9\n"
"s_add_u32 s62, s62, 18\n"
"s_min_u32 s62, s62, 146\n"
"s_max_u32 s72, s72, s62\n"
"s_add_u32 s83, s80, 200\n"
"s_and_b32 s83, s83, -1\n"
"LPRL2_%=:\n"
"s_sub_u32 s82, s83, s80\n"
"s_cmp_gt_i32 s82, 3\n"
"s_cbranch_scc0 LPEL2_%=\n"
"LPDL2_%=:\n"
"s_sleep 1\n"
"s_memrealtime s[80:81]\n"
"s_waitcnt lgkmcnt(0)\n"
"s_sub_u32 s82, s83, s80\n"
"s_cmp_gt_i32 s82, 3\n"
"s_cbranch_scc1 LPDL2_%=\n"
"LPEL2_%=:\n"
"s_add_u32 s66, s66, s72\n"
"s_add_u32 s66, s66, 4\n"
"s_add_u32 s67, s80, s72\n"
"s_sub_u32 s72, s72, 3\n"
"s_max_u32 s72, s72, s62\n"
"s_branch LPKL2_%=\n"
"LPCL3_%=:\n"
"s_memrealtime s[80:81]\n"
"s_sub_u32 s82, s67, s66\n"
"s_cmp_gt_i32 s82, 0\n"
"s_cselect_b32 s83, s67, s66\n"
"s_waitcnt lgkmcnt(0)\n"
"s_sub_u32 s82, s80, s67\n"
"s_add_u32 s63, s63, 1\n"
"s_min_i32 s81, s63, 0\n"
"s_cmp_gt_i32 s82, 20\n"
"s_cselect_b32 s63, s63, s81\n"
"s_cmp_ge_i32 s63, 2\n"
"s_cbranch_scc0 LPRL3_%=\n"
"s_mov_b32 s63, -9\n"
"s_add_u32 s62, s62, 18\n"
"s_min_u32 s62, s62, 146\n"
"s_max_u32 s72, s72, s62\n"
"s_add_u32 s83, s80, 200\n"
"s_and_b32 s83, s83, -1\n"
"LPRL3_%=:\n"
"s_sub_u32 s82, s83, s80\n"
"s_cmp_gt_i32 s82, 3\n"
"s_cbranch_scc0 LPEL3_%=\n"
"LPDL3_%=:\n"
"s_sleep 1\n"
"s_memrealtime s[80:81]\n"
"s_waitcnt lgkmcnt(0)\n"
"s_sub_u32 s82, s83, s80\n"
"s_cmp_gt_i32 s82, 3\n"
"s_cbranch_scc1 LPDL3_%=\n"
"LPEL3_%=:\n"
"s_add_u32 s66, s66, s72\n"
"s_add_u32 s66, s66, 4\n"
"s_add_u32 s67, s80, s72\n"
"s_sub_u32 s72, s72, 3\n"
"s_max_u32 s72, s72, s62\n"
"s_branch LPKL3_%=\n"
"LPN_%=:\n"
"v_mfma_f32_16x16x32_bf16 v[112:115], a[192:195], a[136:139], v[112:115]\n"
"v_mfma_f32_16x16x32_bf16 v[112:115], a[196:199], a[140:143], v[112:115]\n"
"v_mfma_f32_16x16x32_bf16 v[116:119], a[200:203], a[136:139], v[116:119]\n"
"s_waitcnt vmcnt(11)\n"
"v_mfma_f32_16x16x32_bf16 v[116:119], a[204:207], a[140:143], v[116:119]\n"
"s_barrier\n"
"v_mfma_f32_16x16x32_bf16 v[120:123], a[208:211], a[136:139], v[120:123]\n"
"s_add_u32 s40, s48, 256\n"
"s_xor_b32 m0, s50, 0xc000\n"
"ds_read_b64_tr_b16 a[224:225], v244 offset:16384\n"
"v_mfma_f32_16x16x32_bf16 v[120:123], a[212:215], a[140:143], v[120:123]\n"
"buffer_load_dwordx4 v224, s[28:31], s40 offen nt lds\n"
"v_mfma_f32_16x16x32_bf16 v[124:127], a[216:219], a[136:139], v[124:127]\n"
"ds_read_b64_tr_b16 a[226:227], v244 offset:16896\n"
"v_mfma_f32_16x16x32_bf16 v[124:127], a[220:223], a[140:143], v[124:127]\n"
"ds_read_b64_tr_b16 a[228:229], v244 offset:24576\n"
"v_mfma_f32_16x16x32_bf16 v[140:143], a[216:219], a[144:147], v[140:143]\n"
"ds_read_b64_tr_b16 a[230:231], v244 offset:25088\n"
"v_mfma_f32_16x16x32_bf16 v[140:143], a[220:223], a[148:151], v[140:143]\n"
"ds_read_b64_tr_b16 a[232:233], v245 offset:16384\n"
"v_mfma_f32_16x16x32_bf16 v[136:139], a[208:211], a[144:147], v[136:139]\n"
"ds_read_b64_tr_b16 a[234:235], v245 offset:16896\n"
"v_mfma_f32_16x16x32_bf16 v[136:139], a[212:215], a[148:151], v[136:139]\n"
"buffer_load_dwordx4 v225, s[28:31], s40 offen offset:1024 nt lds\n"
"v_mfma_f32_16x16x32_bf16 v[132:135], a[200:203], a[144:147], v[132:135]\n"
"ds_read_b64_tr_b16 a[236:237], v245 offset:24576\n"
"v_mfma_f32_16x16x32_bf16 v[132:135], a[204:207], a[148:151], v[132:135]\n"
"ds_read_b64_tr_b16 a[238:239], v245 offset:25088\n"
"v_mfma_f32_16x16x32_bf16 v[128:131], a[192:195], a[144:147], v[128:131]\n"
"ds_read_b64_tr_b16 a[240:241], v244 offset:17408\n"
"v_mfma_f32_16x16x32_bf16 v[128:131], a[196:199], a[148:151], v[128:131]\n"
"ds_read_b64_tr_b16 a[242:243], v244 offset:17920\n"
"v_mfma_f32_16x16x32_bf16 v[144:147], a[192:195], a[152:155], v[144:147]\n"
"ds_read_b64_tr_b16 a[244:245], v244 offset:25600\n"
"v_mfma_f32_16x16x32_bf16 v[144:147], a[196:199], a[156:159], v[144:147]\n"
"buffer_load_dwordx4 v226, s[28:31], s40 offen offset:2048 nt lds\n"
"v_mfma_f32_16x16x32_bf16 v[148:151], a[200:203], a[152:155], v[148:151]\n"
"ds_read_b64_tr_b16 a[246:247], v244 offset:26112\n"
"v_mfma_f32_16x16x32_bf16 v[148:151], a[204:207], a[156:159], v[148:151]\n"
"ds_read_b64_tr_b16 a[248:249], v245 offset:17408\n"
"v_mfma_f32_16x16x32_bf16 v[152:155], a[208:211], a[152:155], v[152:155]\n"
"ds_read_b64_tr_b16 a[250:251], v245 offset:17920\n"
"v_mfma_f32_16x16x32_bf16 v[152:155], a[212:215], a[156:159], v[152:155]\n"
"ds_read_b64_tr_b16 a[252:253], v245 offset:25600\n"
"v_mfma_f32_16x16x32_bf16 v[156:159], a[216:219], a[152:155], v[156:159]\n"
"ds_read_b64_tr_b16 a[254:255], v245 offset:26112\n"
"v_mfma_f32_16x16x32_bf16 v[156:159], a[220:223], a[156:159], v[156:159]\n"
"buffer_load_dwordx4 v227, s[28:31], s40 offen offset:3072 nt lds\n"
"v_mfma_f32_16x16x32_bf16 v[172:175], a[216:219], a[160:163], v[172:175]\n"
"s_cmp_eq_u32 s47, 8\n"
"v_mfma_f32_16x16x32_bf16 v[172:175], a[220:223], a[164:167], v[172:175]\n"
"s_cselect_b32 s41, s52, s58\n"
"v_mfma_f32_16x16x32_bf16 v[168:171], a[208:211], a[160:163], v[168:171]\n"
"s_add_u32 s48, s48, s41\n"
"v_mfma_f32_16x16x32_bf16 v[168:171], a[212:215], a[164:167], v[168:171]\n"
"s_sub_u32 s41, s48, 5242880\n"
"v_mfma_f32_16x16x32_bf16 v[164:167], a[200:203], a[160:163], v[164:167]\n"
"s_min_u32 s48, s48, s41\n"
"v_mfma_f32_16x16x32_bf16 v[164:167], a[204:207], a[164:167], v[164:167]\n"
"v_xor_b32 v244, s74, v244\n"
"v_xor_b32 v246, s74, v246\n"
"v_mfma_f32_16x16x32_bf16 v[160:163], a[192:195], a[160:163], v[160:163]\n"
"s_mov_b32 m0, s50\n"
"v_xor_b32 v245, s74, v245\n"
"v_xor_b32 v247, s74, v247\n"
"v_mfma_f32_16x16x32_bf16 v[160:163], a[196:199], a[164:167], v[160:163]\n"
"buffer_load_dwordx4 v224, s[28:31], s48 offen nt lds\n"
"v_mfma_f32_16x16x32_bf16 v[176:179], a[192:195], a[168:171], v[176:179]\n"
"v_xor_b32 v252, s74, v252\n"
"v_xor_b32 v253, s74, v253\n"
"v_mfma_f32_16x16x32_bf16 v[176:179], a[196:199], a[172:175], v[176:179]\n"
"v_xor_b32 v250, s74, v250\n"
"v_xor_b32 v251, s74, v251\n"
"v_mfma_f32_16x16x32_bf16 v[180:183], a[200:203], a[168:171], v[180:183]\n"
"v_mfma_f32_16x16x32_bf16 v[180:183], a[204:207], a[172:175], v[180:183]\n"
"buffer_load_dwordx4 v225, s[28:31], s48 offen offset:1024 nt lds\n"
"v_mfma_f32_16x16x32_bf16 v[184:187], a[208:211], a[168:171], v[184:187]\n"
"v_mfma_f32_16x16x32_bf16 v[184:187], a[212:215], a[172:175], v[184:187]\n"
"v_mfma_f32_16x16x32_bf16 v[188:191], a[216:219], a[168:171], v[188:191]\n"
"v_mfma_f32_16x16x32_bf16 v[188:191], a[220:223], a[172:175], v[188:191]\n"
"v_mfma_f32_16x16x32_bf16 v[204:207], a[216:219], a[176:179], v[204:207]\n"
"v_mfma_f32_16x16x32_bf16 v[204:207], a[220:223], a[180:183], v[204:207]\n"
"buffer_load_dwordx4 v226, s[28:31], s48 offen offset:2048 nt lds\n"
"v_mfma_f32_16x16x32_bf16 v[200:203], a[208:211], a[176:179], v[200:203]\n"
"v_mfma_f32_16x16x32_bf16 v[200:203], a[212:215], a[180:183], v[200:203]\n"
"v_mfma_f32_16x16x32_bf16 v[196:199], a[200:203], a[176:179], v[196:199]\n"
"v_mfma_f32_16x16x32_bf16 v[196:199], a[204:207], a[180:183], v[196:199]\n"
"v_mfma_f32_16x16x32_bf16 v[192:195], a[192:195], a[176:179], v[192:195]\n"
"v_mfma_f32_16x16x32_bf16 v[192:195], a[196:199], a[180:183], v[192:195]\n"
"s_waitcnt lgkmcnt(0)\n"
"buffer_load_dwordx4 v227, s[28:31], s48 offen offset:3072 nt lds\n"
"v_mfma_f32_16x16x32_bf16 v[208:211], a[192:195], a[184:187], v[208:211]\n"
"s_xor_b32 s50, s50, s74\n"
"v_mfma_f32_16x16x32_bf16 v[208:211], a[196:199], a[188:191], v[208:211]\n"
"v_mfma_f32_16x16x32_bf16 v[212:215], a[200:203], a[184:187], v[212:215]\n"
"v_mfma_f32_16x16x32_bf16 v[212:215], a[204:207], a[188:191], v[212:215]\n"
"v_mfma_f32_16x16x32_bf16 v[216:219], a[208:211], a[184:187], v[216:219]\n"
"v_mfma_f32_16x16x32_bf16 v[216:219], a[212:215], a[188:191], v[216:219]\n"
"v_mfma_f32_16x16x32_bf16 v[220:223], a[216:219], a[184:187], v[220:223]\n"
"v_mfma_f32_16x16x32_bf16 v[220:223], a[220:223], a[188:191], v[220:223]\n"
"v_mfma_f32_16x16x32_bf16 v[0:3], a[224:227], a[136:139], v[0:3]\n"
"v_mfma_f32_16x16x32_bf16 v[0:3], a[228:231], a[140:143], v[0:3]\n"
"v_mfma_f32_16x16x32_bf16 v[4:7], a[232:235], a[136:139], v[4:7]\n"
"s_waitcnt vmcnt(8)\n"
"v_mfma_f32_16x16x32_bf16 v[4:7], a[236:239], a[140:143], v[4:7]\n"
"s_barrier\n"
"v_mfma_f32_16x16x32_bf16 v[8:11], a[240:243], a[136:139], v[8:11]\n"
"s_mov_b32 m0, s51\n"
"ds_read_b64_tr_b16 a[192:193], v244 offset:0\n"
"v_mfma_f32_16x16x32_bf16 v[8:11], a[244:247], a[140:143], v[8:11]\n"
"buffer_load_dword v242, s[80:83], 0 offen lds\n"
"s_add_u32 s80, s80, 680\n"
"s_addc_u32 s81, s81, 0\n"
"buffer_load_dwordx4 v248, s[24:27], s49 offen nt lds\n"
"v_mfma_f32_16x16x32_bf16 v[12:15], a[248:251], a[136:139], v[12:15]\n"
"ds_read_b64_tr_b16 a[194:195], v244 offset:512\n"
"v_mfma_f32_16x16x32_bf16 v[12:15], a[252:255], a[140:143], v[12:15]\n"
"ds_read_b128 a[136:139], v246 offset:0\n"
"v_mfma_f32_16x16x32_bf16 v[28:31], a[248:251], a[144:147], v[28:31]\n"
"ds_read_b64_tr_b16 a[196:197], v244 offset:8192\n"
"v_mfma_f32_16x16x32_bf16 v[28:31], a[252:255], a[148:151], v[28:31]\n"
"ds_read_b64_tr_b16 a[198:199], v244 offset:8704\n"
"v_mfma_f32_16x16x32_bf16 v[24:27], a[240:243], a[144:147], v[24:27]\n"
"ds_read_b128 a[140:143], v247 offset:0\n"
"v_mfma_f32_16x16x32_bf16 v[24:27], a[244:247], a[148:151], v[24:27]\n"
"buffer_load_dwordx4 v249, s[24:27], s49 offen offset:1024 nt lds\n"
"v_mfma_f32_16x16x32_bf16 v[20:23], a[232:235], a[144:147], v[20:23]\n"
"ds_read_b64_tr_b16 a[200:201], v245 offset:0\n"
"v_mfma_f32_16x16x32_bf16 v[20:23], a[236:239], a[148:151], v[20:23]\n"
"ds_read_b64_tr_b16 a[202:203], v245 offset:512\n"
"v_mfma_f32_16x16x32_bf16 v[16:19], a[224:227], a[144:147], v[16:19]\n"
"ds_read_b64_tr_b16 a[204:205], v245 offset:8192\n"
"v_mfma_f32_16x16x32_bf16 v[16:19], a[228:231], a[148:151], v[16:19]\n"
"ds_read_b64_tr_b16 a[206:207], v245 offset:8704\n"
"v_mfma_f32_16x16x32_bf16 v[32:35], a[224:227], a[152:155], v[32:35]\n"
"ds_read_b64_tr_b16 a[208:209], v244 offset:1024\n"
"v_mfma_f32_16x16x32_bf16 v[32:35], a[228:231], a[156:159], v[32:35]\n"
"buffer_load_dwordx4 v254, s[24:27], s49 offen offset:2048 nt lds\n"
"v_mfma_f32_16x16x32_bf16 v[36:39], a[232:235], a[152:155], v[36:39]\n"
"ds_read_b64_tr_b16 a[210:211], v244 offset:1536\n"
"v_mfma_f32_16x16x32_bf16 v[36:39], a[236:239], a[156:159], v[36:39]\n"
"ds_read_b64_tr_b16 a[212:213], v244 offset:9216\n"
"v_mfma_f32_16x16x32_bf16 v[40:43], a[240:243], a[152:155], v[40:43]\n"
"ds_read_b64_tr_b16 a[214:215], v244 offset:9728\n"
"v_mfma_f32_16x16x32_bf16 v[40:43], a[244:247], a[156:159], v[40:43]\n"
"ds_read_b64_tr_b16 a[216:217], v245 offset:1024\n"
"v_mfma_f32_16x16x32_bf16 v[44:47], a[248:251], a[152:155], v[44:47]\n"
"ds_read_b64_tr_b16 a[218:219], v245 offset:1536\n"
"v_mfma_f32_16x16x32_bf16 v[44:47], a[252:255], a[156:159], v[44:47]\n"
"buffer_load_dwordx4 v255, s[24:27], s49 offen offset:3072 nt lds\n"
"v_mfma_f32_16x16x32_bf16 v[60:63], a[248:251], a[160:163], v[60:63]\n"
"ds_read_b64_tr_b16 a[220:221], v245 offset:9216\n"
"v_mfma_f32_16x16x32_bf16 v[60:63], a[252:255], a[164:167], v[60:63]\n"
"ds_read_b64_tr_b16 a[222:223], v245 offset:9728\n"
"v_mfma_f32_16x16x32_bf16 v[56:59], a[240:243], a[160:163], v[56:59]\n"
"ds_read_b128 a[144:147], v246 offset:2048\n"
"v_mfma_f32_16x16x32_bf16 v[56:59], a[244:247], a[164:167], v[56:59]\n"
"s_add_u32 s40, s49, 40960\n"
"ds_read_b128 a[148:151], v247 offset:2048\n"
"v_mfma_f32_16x16x32_bf16 v[52:55], a[232:235], a[160:163], v[52:55]\n"
"s_add_u32 m0, s51, 4096\n"
"ds_read_b128 a[152:155], v246 offset:4096\n"
"v_mfma_f32_16x16x32_bf16 v[52:55], a[236:239], a[164:167], v[52:55]\n"
"buffer_load_dwordx4 v248, s[24:27], s40 offen nt lds\n"
"v_mfma_f32_16x16x32_bf16 v[48:51], a[224:227], a[160:163], v[48:51]\n"
"ds_read_b128 a[156:159], v247 offset:4096\n"
"v_mfma_f32_16x16x32_bf16 v[48:51], a[228:231], a[164:167], v[48:51]\n"
"ds_read_b128 a[160:163], v252 offset:0\n"
"v_mfma_f32_16x16x32_bf16 v[64:67], a[224:227], a[168:171], v[64:67]\n"
"ds_read_b128 a[164:167], v253 offset:0\n"
"v_mfma_f32_16x16x32_bf16 v[64:67], a[228:231], a[172:175], v[64:67]\n"
"v_mfma_f32_16x16x32_bf16 v[68:71], a[232:235], a[168:171], v[68:71]\n"
"v_mfma_f32_16x16x32_bf16 v[68:71], a[236:239], a[172:175], v[68:71]\n"
"buffer_load_dwordx4 v249, s[24:27], s40 offen offset:1024 nt lds\n"
"v_mfma_f32_16x16x32_bf16 v[72:75], a[240:243], a[168:171], v[72:75]\n"
"v_mfma_f32_16x16x32_bf16 v[72:75], a[244:247], a[172:175], v[72:75]\n"
"v_mfma_f32_16x16x32_bf16 v[76:79], a[248:251], a[168:171], v[76:79]\n"
"ds_read_b128 a[168:171], v250 offset:0\n"
"v_mfma_f32_16x16x32_bf16 v[76:79], a[252:255], a[172:175], v[76:79]\n"
"ds_read_b128 a[172:175], v251 offset:0\n"
"v_mfma_f32_16x16x32_bf16 v[92:95], a[248:251], a[176:179], v[92:95]\n"
"v_mfma_f32_16x16x32_bf16 v[92:95], a[252:255], a[180:183], v[92:95]\n"
"buffer_load_dwordx4 v254, s[24:27], s40 offen offset:2048 nt lds\n"
"v_mfma_f32_16x16x32_bf16 v[88:91], a[240:243], a[176:179], v[88:91]\n"
"s_add_u32 s49, s49, 128\n"
"v_mfma_f32_16x16x32_bf16 v[88:91], a[244:247], a[180:183], v[88:91]\n"
"s_sub_u32 s41, s49, 1280\n"
"v_mfma_f32_16x16x32_bf16 v[84:87], a[232:235], a[176:179], v[84:87]\n"
"s_min_u32 s49, s49, s41\n"
"v_mfma_f32_16x16x32_bf16 v[84:87], a[236:239], a[180:183], v[84:87]\n"
"s_xor_b32 s51, s51, s74\n"
"v_mfma_f32_16x16x32_bf16 v[80:83], a[224:227], a[176:179], v[80:83]\n"
"ds_read_b128 a[176:179], v250 offset:2048\n"
"v_mfma_f32_16x16x32_bf16 v[80:83], a[228:231], a[180:183], v[80:83]\n"
"ds_read_b128 a[180:183], v251 offset:2048\n"
"v_mfma_f32_16x16x32_bf16 v[96:99], a[224:227], a[184:187], v[96:99]\n"
"v_mfma_f32_16x16x32_bf16 v[96:99], a[228:231], a[188:191], v[96:99]\n"
"v_mfma_f32_16x16x32_bf16 v[100:103], a[232:235], a[184:187], v[100:103]\n"
"v_mfma_f32_16x16x32_bf16 v[100:103], a[236:239], a[188:191], v[100:103]\n"
"v_mfma_f32_16x16x32_bf16 v[104:107], a[240:243], a[184:187], v[104:107]\n"
"v_mfma_f32_16x16x32_bf16 v[104:107], a[244:247], a[188:191], v[104:107]\n"
"v_mfma_f32_16x16x32_bf16 v[108:111], a[248:251], a[184:187], v[108:111]\n"
"s_add_u32 s47, s47, 1\n"
"ds_read_b128 a[184:187], v250 offset:4096\n"
"v_mfma_f32_16x16x32_bf16 v[108:111], a[252:255], a[188:191], v[108:111]\n"
"s_waitcnt lgkmcnt(1)\n"
"s_cmp_lg_u32 s47, s62\n"
"ds_read_b128 a[188:191], v251 offset:4096\n"
"s_cbranch_scc1 LPN_%=\n"
"s_branch LPX_%=\n"
"LEND_%=:\n"
"LSLOW_%=:\n"
"s_waitcnt vmcnt(0) lgkmcnt(0)\n"
"s_nop 9\n"
"v_bfe_u32 v148, v242, 4, 4\n"
"v_bfe_u32 v149, v242, 8, 2\n"
"v_lshlrev_b32 v144, 8, v148\n"
"v_lshl_add_u32 v144, v149, 4, v144\n"
"v_add_u32 v144, s43, v144\n"
"v_xor_b32 v145, 64, v144\n"
"v_lshlrev_b32 v146, 1, v148\n"
"v_lshl_add_u32 v146, v149, 10, v146\n"
"v_add_u32 v146, s43, v146\n"
"v_xor_b32 v147, 64, v146\n"
"v_mov_b32 v152, 0xff7fffff\n"
"v_mov_b32 v155, 0\n"
"v_mov_b32 v0, 0\n"
"v_mov_b32 v1, 0\n"
"v_mov_b32 v2, 0\n"
"v_mov_b32 v3, 0\n"
"v_mov_b32 v4, 0\n"
"v_mov_b32 v5, 0\n"
"v_mov_b32 v6, 0\n"
"v_mov_b32 v7, 0\n"
"v_mov_b32 v8, 0\n"
"v_mov_b32 v9, 0\n"
"v_mov_b32 v10, 0\n"
"v_mov_b32 v11, 0\n"
"v_mov_b32 v12, 0\n"
"v_mov_b32 v13, 0\n"
"v_mov_b32 v14, 0\n"
"v_mov_b32 v15, 0\n"
"v_mov_b32 v16, 0\n"
"v_mov_b32 v17, 0\n"
"v_mov_b32 v18, 0\n"
"v_mov_b32 v19, 0\n"
"v_mov_b32 v20, 0\n"
"v_mov_b32 v21, 0\n"
"v_mov_b32 v22, 0\n"
"v_mov_b32 v23, 0\n"
"v_mov_b32 v24, 0\n"
"v_mov_b32 v25, 0\n"
"v_mov_b32 v26, 0\n"
"v_mov_b32 v27, 0\n"
"v_mov_b32 v28, 0\n"
"v_mov_b32 v29, 0\n"
"v_mov_b32 v30, 0\n"
"v_mov_b32 v31, 0\n"
"v_mov_b32 v156, 0xff7fffff\n"
"v_mov_b32 v159, 0\n"
"v_mov_b32 v32, 0\n"
"v_mov_b32 v33, 0\n"
"v_mov_b32 v34, 0\n"
"v_mov_b32 v35, 0\n"
"v_mov_b32 v36, 0\n"
"v_mov_b32 v37, 0\n"
"v_mov_b32 v38, 0\n"
"v_mov_b32 v39, 0\n"
"v_mov_b32 v40, 0\n"
"v_mov_b32 v41, 0\n"
"v_mov_b32 v42, 0\n"
"v_mov_b32 v43, 0\n"
"v_mov_b32 v44, 0\n"
"v_mov_b32 v45, 0\n"
"v_mov_b32 v46, 0\n"
"v_mov_b32 v47, 0\n"
"v_mov_b32 v48, 0\n"
"v_mov_b32 v49, 0\n"
"v_mov_b32 v50, 0\n"
"v_mov_b32 v51, 0\n"
"v_mov_b32 v52, 0\n"
"v_mov_b32 v53, 0\n"
"v_mov_b32 v54, 0\n"
"v_mov_b32 v55, 0\n"
"v_mov_b32 v56, 0\n"
"v_mov_b32 v57, 0\n"
"v_mov_b32 v58, 0\n"
"v_mov_b32 v59, 0\n"
"v_mov_b32 v60, 0\n"
"v_mov_b32 v61, 0\n"
"v_mov_b32 v62, 0\n"
"v_mov_b32 v63, 0\n"
"v_mov_b32 v160, 0xff7fffff\n"
"v_mov_b32 v163, 0\n"
"v_mov_b32 v64, 0\n"
"v_mov_b32 v65, 0\n"
"v_mov_b32 v66, 0\n"
"v_mov_b32 v67, 0\n"
"v_mov_b32 v68, 0\n"
"v_mov_b32 v69, 0\n"
"v_mov_b32 v70, 0\n"
"v_mov_b32 v71, 0\n"
"v_mov_b32 v72, 0\n"
"v_mov_b32 v73, 0\n"
"v_mov_b32 v74, 0\n"
"v_mov_b32 v75, 0\n"
"v_mov_b32 v76, 0\n"
"v_mov_b32 v77, 0\n"
"v_mov_b32 v78, 0\n"
"v_mov_b32 v79, 0\n"
"v_mov_b32 v80, 0\n"
"v_mov_b32 v81, 0\n"
"v_mov_b32 v82, 0\n"
"v_mov_b32 v83, 0\n"
"v_mov_b32 v84, 0\n"
"v_mov_b32 v85, 0\n"
"v_mov_b32 v86, 0\n"
"v_mov_b32 v87, 0\n"
"v_mov_b32 v88, 0\n"
"v_mov_b32 v89, 0\n"
"v_mov_b32 v90, 0\n"
"v_mov_b32 v91, 0\n"
"v_mov_b32 v92, 0\n"
"v_mov_b32 v93, 0\n"
"v_mov_b32 v94, 0\n"
"v_mov_b32 v95, 0\n"
"v_mov_b32 v164, 0xff7fffff\n"
"v_mov_b32 v167, 0\n"
"v_accvgpr_write_b32 a208, 0\n"
"v_accvgpr_write_b32 a209, 0\n"
"v_accvgpr_write_b32 a210, 0\n"
"v_accvgpr_write_b32 a211, 0\n"
"v_accvgpr_write_b32 a212, 0\n"
"v_accvgpr_write_b32 a213, 0\n"
"v_accvgpr_write_b32 a214, 0\n"
"v_accvgpr_write_b32 a215, 0\n"
"v_accvgpr_write_b32 a216, 0\n"
"v_accvgpr_write_b32 a217, 0\n"
"v_accvgpr_write_b32 a218, 0\n"
"v_accvgpr_write_b32 a219, 0\n"
"v_accvgpr_write_b32 a220, 0\n"
"v_accvgpr_write_b32 a221, 0\n"
"v_accvgpr_write_b32 a222, 0\n"
"v_accvgpr_write_b32 a223, 0\n"
"v_accvgpr_write_b32 a224, 0\n"
"v_accvgpr_write_b32 a225, 0\n"
"v_accvgpr_write_b32 a226, 0\n"
"v_accvgpr_write_b32 a227, 0\n"
"v_accvgpr_write_b32 a228, 0\n"
"v_accvgpr_write_b32 a229, 0\n"
"v_accvgpr_write_b32 a230, 0\n"
"v_accvgpr_write_b32 a231, 0\n"
"v_accvgpr_write_b32 a232, 0\n"
"v_accvgpr_write_b32 a233, 0\n"
"v_accvgpr_write_b32 a234, 0\n"
"v_accvgpr_write_b32 a235, 0\n"
"v_accvgpr_write_b32 a236, 0\n"
"v_accvgpr_write_b32 a237, 0\n"
"v_accvgpr_write_b32 a238, 0\n"
"v_accvgpr_write_b32 a239, 0\n"
"s_lshl_b32 s80, s47, 16\n"
"s_sub_u32 s80, s80, 261248\n"
"s_add_u32 s81, s80, 4096\n"
"s_mov_b32 s82, 0\n"
"LSLOOP_%=:\n"
"s_lshl_b32 s83, s82, 6\n"
"s_and_b32 s83, s83, 64\n"
"buffer_load_dwordx4 v[128:131], v144, s[84:87], s80 offen offset:0\n"
"buffer_load_dwordx4 v[132:135], v145, s[84:87], s80 offen offset:0\n"
"buffer_load_dwordx4 v[136:139], v144, s[84:87], s80 offen offset:128\n"
"buffer_load_dwordx4 v[140:143], v145, s[84:87], s80 offen offset:128\n"
"s_waitcnt vmcnt(0)\n"
"v_mfma_f32_16x16x32_bf16 v[96:99], v[128:131], a[0:3], 0\n"
"v_mfma_f32_16x16x32_bf16 v[96:99], v[132:135], a[4:7], v[96:99]\n"
"v_mfma_f32_16x16x32_bf16 v[96:99], v[136:139], a[8:11], v[96:99]\n"
"v_mfma_f32_16x16x32_bf16 v[96:99], v[140:143], a[12:15], v[96:99]\n"
"v_mfma_f32_16x16x32_bf16 v[104:107], v[128:131], a[16:19], 0\n"
"v_mfma_f32_16x16x32_bf16 v[104:107], v[132:135], a[20:23], v[104:107]\n"
"v_mfma_f32_16x16x32_bf16 v[104:107], v[136:139], a[24:27], v[104:107]\n"
"v_mfma_f32_16x16x32_bf16 v[104:107], v[140:143], a[28:31], v[104:107]\n"
"v_mfma_f32_16x16x32_bf16 v[112:115], v[128:131], a[32:35], 0\n"
"v_mfma_f32_16x16x32_bf16 v[112:115], v[132:135], a[36:39], v[112:115]\n"
"v_mfma_f32_16x16x32_bf16 v[112:115], v[136:139], a[40:43], v[112:115]\n"
"v_mfma_f32_16x16x32_bf16 v[112:115], v[140:143], a[44:47], v[112:115]\n"
"v_mfma_f32_16x16x32_bf16 v[120:123], v[128:131], a[192:195], 0\n"
"v_mfma_f32_16x16x32_bf16 v[120:123], v[132:135], a[196:199], v[120:123]\n"
"v_mfma_f32_16x16x32_bf16 v[120:123], v[136:139], a[200:203], v[120:123]\n"
"v_mfma_f32_16x16x32_bf16 v[120:123], v[140:143], a[204:207], v[120:123]\n"
"s_nop 7\n"
"buffer_load_dwordx4 v[128:131], v144, s[84:87], s81 offen offset:0\n"
"buffer_load_dwordx4 v[132:135], v145, s[84:87], s81 offen offset:0\n"
"buffer_load_dwordx4 v[136:139], v144, s[84:87], s81 offen offset:128\n"
"buffer_load_dwordx4 v[140:143], v145, s[84:87], s81 offen offset:128\n"
"s_waitcnt vmcnt(0)\n"
"v_mfma_f32_16x16x32_bf16 v[100:103], v[128:131], a[0:3], 0\n"
"v_mfma_f32_16x16x32_bf16 v[100:103], v[132:135], a[4:7], v[100:103]\n"
"v_mfma_f32_16x16x32_bf16 v[100:103], v[136:139], a[8:11], v[100:103]\n"
"v_mfma_f32_16x16x32_bf16 v[100:103], v[140:143], a[12:15], v[100:103]\n"
"v_mfma_f32_16x16x32_bf16 v[108:111], v[128:131], a[16:19], 0\n"
"v_mfma_f32_16x16x32_bf16 v[108:111], v[132:135], a[20:23], v[108:111]\n"
"v_mfma_f32_16x16x32_bf16 v[108:111], v[136:139], a[24:27], v[108:111]\n"
"v_mfma_f32_16x16x32_bf16 v[108:111], v[140:143], a[28:31], v[108:111]\n"
"v_mfma_f32_16x16x32_bf16 v[116:119], v[128:131], a[32:35], 0\n"
"v_mfma_f32_16x16x32_bf16 v[116:119], v[132:135], a[36:39], v[116:119]\n"
"v_mfma_f32_16x16x32_bf16 v[116:119], v[136:139], a[40:43], v[116:119]\n"
"v_mfma_f32_16x16x32_bf16 v[116:119], v[140:143], a[44:47], v[116:119]\n"
"v_mfma_f32_16x16x32_bf16 v[124:127], v[128:131], a[192:195], 0\n"
"v_mfma_f32_16x16x32_bf16 v[124:127], v[132:135], a[196:199], v[124:127]\n"
"v_mfma_f32_16x16x32_bf16 v[124:127], v[136:139], a[200:203], v[124:127]\n"
"v_mfma_f32_16x16x32_bf16 v[124:127], v[140:143], a[204:207], v[124:127]\n"
"s_nop 7\n"
"v_max3_f32 v153, v96, v97, v98\n"
"v_max3_f32 v153, v153, v99, v100\n"
"v_max3_f32 v153, v153, v101, v102\n"
"v_max3_f32 v153, v153, v103, v152\n"
"v_mov_b32 v154, v153\n"
"s_nop 1\n"
"v_permlane32_swap_b32 v153, v154\n"
"v_max_f32 v153, v153, v154\n"
"v_mov_b32 v154, v153\n"
"s_nop 1\n"
"v_permlane16_swap_b32 v153, v154\n"
"v_max_f32 v153, v153, v154\n"
"v_sub_f32 v154, v152, v153\n"
"v_exp_f32 v154, v154\n"
"v_mov_b32 v152, v153\n"
"v_sub_f32 v96, v96, v152\n"
"v_sub_f32 v97, v97, v152\n"
"v_sub_f32 v98, v98, v152\n"
"v_sub_f32 v99, v99, v152\n"
"v_sub_f32 v100, v100, v152\n"
"v_sub_f32 v101, v101, v152\n"
"v_sub_f32 v102, v102, v152\n"
"v_sub_f32 v103, v103, v152\n"
"v_exp_f32 v96, v96\n"
"v_exp_f32 v97, v97\n"
"v_exp_f32 v98, v98\n"
"v_exp_f32 v99, v99\n"
"v_exp_f32 v100, v100\n"
"v_exp_f32 v101, v101\n"
"v_exp_f32 v102, v102\n"
"v_exp_f32 v103, v103\n"
"v_mul_f32 v155, v155, v154\n"
"v_add_f32 v155, v155, v96\n"
"v_add_f32 v155, v155, v97\n"
"v_add_f32 v155, v155, v98\n"
"v_add_f32 v155, v155, v99\n"
"v_add_f32 v155, v155, v100\n"
"v_add_f32 v155, v155, v101\n"
"v_add_f32 v155, v155, v102\n"
"v_add_f32 v155, v155, v103\n"
"v_cvt_pk_bf16_f32 v96, v96, v97\n"
"v_cvt_pk_bf16_f32 v97, v98, v99\n"
"v_cvt_pk_bf16_f32 v98, v100, v101\n"
"v_cvt_pk_bf16_f32 v99, v102, v103\n"
"v_mov_b32_dpp v154, v154 row_ror:12 row_mask:0x2 bank_mask:0xf\n"
"s_nop 1\n"
"v_mov_b32_dpp v154, v154 row_ror:8 row_mask:0x4 bank_mask:0xf\n"
"s_nop 1\n"
"v_mov_b32_dpp v154, v154 row_ror:4 row_mask:0x8 bank_mask:0xf\n"
"s_nop 1\n"
"v_mul_f32_dpp v0, v154, v0 row_newbcast:0 row_mask:0xf bank_mask:0xf\n"
"v_mul_f32_dpp v1, v154, v1 row_newbcast:1 row_mask:0xf bank_mask:0xf\n"
"v_mul_f32_dpp v2, v154, v2 row_newbcast:2 row_mask:0xf bank_mask:0xf\n"
"v_mul_f32_dpp v3, v154, v3 row_newbcast:3 row_mask:0xf bank_mask:0xf\n"
"v_mul_f32_dpp v4, v154, v4 row_newbcast:0 row_mask:0xf bank_mask:0xf\n"
"v_mul_f32_dpp v5, v154, v5 row_newbcast:1 row_mask:0xf bank_mask:0xf\n"
"v_mul_f32_dpp v6, v154, v6 row_newbcast:2 row_mask:0xf bank_mask:0xf\n"
"v_mul_f32_dpp v7, v154, v7 row_newbcast:3 row_mask:0xf bank_mask:0xf\n"
"v_mul_f32_dpp v8, v154, v8 row_newbcast:0 row_mask:0xf bank_mask:0xf\n"
"v_mul_f32_dpp v9, v154, v9 row_newbcast:1 row_mask:0xf bank_mask:0xf\n"
"v_mul_f32_dpp v10, v154, v10 row_newbcast:2 row_mask:0xf bank_mask:0xf\n"
"v_mul_f32_dpp v11, v154, v11 row_newbcast:3 row_mask:0xf bank_mask:0xf\n"
"v_mul_f32_dpp v12, v154, v12 row_newbcast:0 row_mask:0xf bank_mask:0xf\n"
"v_mul_f32_dpp v13, v154, v13 row_newbcast:1 row_mask:0xf bank_mask:0xf\n"
"v_mul_f32_dpp v14, v154, v14 row_newbcast:2 row_mask:0xf bank_mask:0xf\n"
"v_mul_f32_dpp v15, v154, v15 row_newbcast:3 row_mask:0xf bank_mask:0xf\n"
"v_mul_f32_dpp v16, v154, v16 row_newbcast:0 row_mask:0xf bank_mask:0xf\n"
"v_mul_f32_dpp v17, v154, v17 row_newbcast:1 row_mask:0xf bank_mask:0xf\n"
"v_mul_f32_dpp v18, v154, v18 row_newbcast:2 row_mask:0xf bank_mask:0xf\n"
"v_mul_f32_dpp v19, v154, v19 row_newbcast:3 row_mask:0xf bank_mask:0xf\n"
"v_mul_f32_dpp v20, v154, v20 row_newbcast:0 row_mask:0xf bank_mask:0xf\n"
"v_mul_f32_dpp v21, v154, v21 row_newbcast:1 row_mask:0xf bank_mask:0xf\n"
"v_mul_f32_dpp v22, v154, v22 row_newbcast:2 row_mask:0xf bank_mask:0xf\n"
"v_mul_f32_dpp v23, v154, v23 row_newbcast:3 row_mask:0xf bank_mask:0xf\n"
"v_mul_f32_dpp v24, v154, v24 row_newbcast:0 row_mask:0xf bank_mask:0xf\n"
"v_mul_f32_dpp v25, v154, v25 row_newbcast:1 row_mask:0xf bank_mask:0xf\n"
"v_mul_f32_dpp v26, v154, v26 row_newbcast:2 row_mask:0xf bank_mask:0xf\n"
"v_mul_f32_dpp v27, v154, v27 row_newbcast:3 row_mask:0xf bank_mask:0xf\n"
"v_mul_f32_dpp v28, v154, v28 row_newbcast:0 row_mask:0xf bank_mask:0xf\n"
"v_mul_f32_dpp v29, v154, v29 row_newbcast:1 row_mask:0xf bank_mask:0xf\n"
"v_mul_f32_dpp v30, v154, v30 row_newbcast:2 row_mask:0xf bank_mask:0xf\n"
"v_mul_f32_dpp v31, v154, v31 row_newbcast:3 row_mask:0xf bank_mask:0xf\n"
"v_max3_f32 v157, v104, v105, v106\n"
"v_max3_f32 v157, v157, v107, v108\n"
"v_max3_f32 v157, v157, v109, v110\n"
"v_max3_f32 v157, v157, v111, v156\n"
"v_mov_b32 v158, v157\n"
"s_nop 1\n"
"v_permlane32_swap_b32 v157, v158\n"
"v_max_f32 v157, v157, v158\n"
"v_mov_b32 v158, v157\n"
"s_nop 1\n"
"v_permlane16_swap_b32 v157, v158\n"
"v_max_f32 v157, v157, v158\n"
"v_sub_f32 v158, v156, v157\n"
"v_exp_f32 v158, v158\n"
"v_mov_b32 v156, v157\n"
"v_sub_f32 v104, v104, v156\n"
"v_sub_f32 v105, v105, v156\n"
"v_sub_f32 v106, v106, v156\n"
"v_sub_f32 v107, v107, v156\n"
"v_sub_f32 v108, v108, v156\n"
"v_sub_f32 v109, v109, v156\n"
"v_sub_f32 v110, v110, v156\n"
"v_sub_f32 v111, v111, v156\n"
"v_exp_f32 v104, v104\n"
"v_exp_f32 v105, v105\n"
"v_exp_f32 v106, v106\n"
"v_exp_f32 v107, v107\n"
"v_exp_f32 v108, v108\n"
"v_exp_f32 v109, v109\n"
"v_exp_f32 v110, v110\n"
"v_exp_f32 v111, v111\n"
"v_mul_f32 v159, v159, v158\n"
"v_add_f32 v159, v159, v104\n"
"v_add_f32 v159, v159, v105\n"
"v_add_f32 v159, v159, v106\n"
"v_add_f32 v159, v159, v107\n"
"v_add_f32 v159, v159, v108\n"
"v_add_f32 v159, v159, v109\n"
"v_add_f32 v159, v159, v110\n"
"v_add_f32 v159, v159, v111\n"
"v_cvt_pk_bf16_f32 v104, v104, v105\n"
"v_cvt_pk_bf16_f32 v105, v106, v107\n"
"v_cvt_pk_bf16_f32 v106, v108, v109\n"
"v_cvt_pk_bf16_f32 v107, v110, v111\n"
"v_mov_b32_dpp v158, v158 row_ror:12 row_mask:0x2 bank_mask:0xf\n"
"s_nop 1\n"
"v_mov_b32_dpp v158, v158 row_ror:8 row_mask:0x4 bank_mask:0xf\n"
"s_nop 1\n"
"v_mov_b32_dpp v158, v158 row_ror:4 row_mask:0x8 bank_mask:0xf\n"
"s_nop 1\n"
"v_mul_f32_dpp v32, v158, v32 row_newbcast:0 row_mask:0xf bank_mask:0xf\n"
"v_mul_f32_dpp v33, v158, v33 row_newbcast:1 row_mask:0xf bank_mask:0xf\n"
"v_mul_f32_dpp v34, v158, v34 row_newbcast:2 row_mask:0xf bank_mask:0xf\n"
"v_mul_f32_dpp v35, v158, v35 row_newbcast:3 row_mask:0xf bank_mask:0xf\n"
"v_mul_f32_dpp v36, v158, v36 row_newbcast:0 row_mask:0xf bank_mask:0xf\n"
"v_mul_f32_dpp v37, v158, v37 row_newbcast:1 row_mask:0xf bank_mask:0xf\n"
"v_mul_f32_dpp v38, v158, v38 row_newbcast:2 row_mask:0xf bank_mask:0xf\n"
"v_mul_f32_dpp v39, v158, v39 row_newbcast:3 row_mask:0xf bank_mask:0xf\n"
"v_mul_f32_dpp v40, v158, v40 row_newbcast:0 row_mask:0xf bank_mask:0xf\n"
"v_mul_f32_dpp v41, v158, v41 row_newbcast:1 row_mask:0xf bank_mask:0xf\n"
"v_mul_f32_dpp v42, v158, v42 row_newbcast:2 row_mask:0xf bank_mask:0xf\n"
"v_mul_f32_dpp v43, v158, v43 row_newbcast:3 row_mask:0xf bank_mask:0xf\n"
"v_mul_f32_dpp v44, v158, v44 row_newbcast:0 row_mask:0xf bank_mask:0xf\n"
"v_mul_f32_dpp v45, v158, v45 row_newbcast:1 row_mask:0xf bank_mask:0xf\n"
"v_mul_f32_dpp v46, v158, v46 row_newbcast:2 row_mask:0xf bank_mask:0xf\n"
"v_mul_f32_dpp v47, v158, v47 row_newbcast:3 row_mask:0xf bank_mask:0xf\n"
"v_mul_f32_dpp v48, v158, v48 row_newbcast:0 row_mask:0xf bank_mask:0xf\n"
"v_mul_f32_dpp v49, v158, v49 row_newbcast:1 row_mask:0xf bank_mask:0xf\n"
"v_mul_f32_dpp v50, v158, v50 row_newbcast:2 row_mask:0xf bank_mask:0xf\n"
"v_mul_f32_dpp v51, v158, v51 row_newbcast:3 row_mask:0xf bank_mask:0xf\n"
"v_mul_f32_dpp v52, v158, v52 row_newbcast:0 row_mask:0xf bank_mask:0xf\n"
"v_mul_f32_dpp v53, v158, v53 row_newbcast:1 row_mask:0xf bank_mask:0xf\n"
"v_mul_f32_dpp v54, v158, v54 row_newbcast:2 row_mask:0xf bank_mask:0xf\n"
"v_mul_f32_dpp v55, v158, v55 row_newbcast:3 row_mask:0xf bank_mask:0xf\n"
"v_mul_f32_dpp v56, v158, v56 row_newbcast:0 row_mask:0xf bank_mask:0xf\n"
"v_mul_f32_dpp v57, v158, v57 row_newbcast:1 row_mask:0xf bank_mask:0xf\n"
"v_mul_f32_dpp v58, v158, v58 row_newbcast:2 row_mask:0xf bank_mask:0xf\n"
"v_mul_f32_dpp v59, v158, v59 row_newbcast:3 row_mask:0xf bank_mask:0xf\n"
"v_mul_f32_dpp v60, v158, v60 row_newbcast:0 row_mask:0xf bank_mask:0xf\n"
"v_mul_f32_dpp v61, v158, v61 row_newbcast:1 row_mask:0xf bank_mask:0xf\n"
"v_mul_f32_dpp v62, v158, v62 row_newbcast:2 row_mask:0xf bank_mask:0xf\n"
"v_mul_f32_dpp v63, v158, v63 row_newbcast:3 row_mask:0xf bank_mask:0xf\n"
"v_max3_f32 v161, v112, v113, v114\n"
"v_max3_f32 v161, v161, v115, v116\n"
"v_max3_f32 v161, v161, v117, v118\n"
"v_max3_f32 v161, v161, v119, v160\n"
"v_mov_b32 v162, v161\n"
"s_nop 1\n"
"v_permlane32_swap_b32 v161, v162\n"
"v_max_f32 v161, v161, v162\n"
"v_mov_b32 v162, v161\n"
"s_nop 1\n"
"v_permlane16_swap_b32 v161, v162\n"
"v_max_f32 v161, v161, v162\n"
"v_sub_f32 v162, v160, v161\n"
"v_exp_f32 v162, v162\n"
"v_mov_b32 v160, v161\n"
"v_sub_f32 v112, v112, v160\n"
"v_sub_f32 v113, v113, v160\n"
"v_sub_f32 v114, v114, v160\n"
"v_sub_f32 v115, v115, v160\n"
"v_sub_f32 v116, v116, v160\n"
"v_sub_f32 v117, v117, v160\n"
"v_sub_f32 v118, v118, v160\n"
"v_sub_f32 v119, v119, v160\n"
"v_exp_f32 v112, v112\n"
"v_exp_f32 v113, v113\n"
"v_exp_f32 v114, v114\n"
"v_exp_f32 v115, v115\n"
"v_exp_f32 v116, v116\n"
"v_exp_f32 v117, v117\n"
"v_exp_f32 v118, v118\n"
"v_exp_f32 v119, v119\n"
"v_mul_f32 v163, v163, v162\n"
"v_add_f32 v163, v163, v112\n"
"v_add_f32 v163, v163, v113\n"
"v_add_f32 v163, v163, v114\n"
"v_add_f32 v163, v163, v115\n"
"v_add_f32 v163, v163, v116\n"
"v_add_f32 v163, v163, v117\n"
"v_add_f32 v163, v163, v118\n"
"v_add_f32 v163, v163, v119\n"
"v_cvt_pk_bf16_f32 v112, v112, v113\n"
"v_cvt_pk_bf16_f32 v113, v114, v115\n"
"v_cvt_pk_bf16_f32 v114, v116, v117\n"
"v_cvt_pk_bf16_f32 v115, v118, v119\n"
"v_mov_b32_dpp v162, v162 row_ror:12 row_mask:0x2 bank_mask:0xf\n"
"s_nop 1\n"
"v_mov_b32_dpp v162, v162 row_ror:8 row_mask:0x4 bank_mask:0xf\n"
"s_nop 1\n"
"v_mov_b32_dpp v162, v162 row_ror:4 row_mask:0x8 bank_mask:0xf\n"
"s_nop 1\n"
"v_mul_f32_dpp v64, v162, v64 row_newbcast:0 row_mask:0xf bank_mask:0xf\n"
"v_mul_f32_dpp v65, v162, v65 row_newbcast:1 row_mask:0xf bank_mask:0xf\n"
"v_mul_f32_dpp v66, v162, v66 row_newbcast:2 row_mask:0xf bank_mask:0xf\n"
"v_mul_f32_dpp v67, v162, v67 row_newbcast:3 row_mask:0xf bank_mask:0xf\n"
"v_mul_f32_dpp v68, v162, v68 row_newbcast:0 row_mask:0xf bank_mask:0xf\n"
"v_mul_f32_dpp v69, v162, v69 row_newbcast:1 row_mask:0xf bank_mask:0xf\n"
"v_mul_f32_dpp v70, v162, v70 row_newbcast:2 row_mask:0xf bank_mask:0xf\n"
"v_mul_f32_dpp v71, v162, v71 row_newbcast:3 row_mask:0xf bank_mask:0xf\n"
"v_mul_f32_dpp v72, v162, v72 row_newbcast:0 row_mask:0xf bank_mask:0xf\n"
"v_mul_f32_dpp v73, v162, v73 row_newbcast:1 row_mask:0xf bank_mask:0xf\n"
"v_mul_f32_dpp v74, v162, v74 row_newbcast:2 row_mask:0xf bank_mask:0xf\n"
"v_mul_f32_dpp v75, v162, v75 row_newbcast:3 row_mask:0xf bank_mask:0xf\n"
"v_mul_f32_dpp v76, v162, v76 row_newbcast:0 row_mask:0xf bank_mask:0xf\n"
"v_mul_f32_dpp v77, v162, v77 row_newbcast:1 row_mask:0xf bank_mask:0xf\n"
"v_mul_f32_dpp v78, v162, v78 row_newbcast:2 row_mask:0xf bank_mask:0xf\n"
"v_mul_f32_dpp v79, v162, v79 row_newbcast:3 row_mask:0xf bank_mask:0xf\n"
"v_mul_f32_dpp v80, v162, v80 row_newbcast:0 row_mask:0xf bank_mask:0xf\n"
"v_mul_f32_dpp v81, v162, v81 row_newbcast:1 row_mask:0xf bank_mask:0xf\n"
"v_mul_f32_dpp v82, v162, v82 row_newbcast:2 row_mask:0xf bank_mask:0xf\n"
"v_mul_f32_dpp v83, v162, v83 row_newbcast:3 row_mask:0xf bank_mask:0xf\n"
"v_mul_f32_dpp v84, v162, v84 row_newbcast:0 row_mask:0xf bank_mask:0xf\n"
"v_mul_f32_dpp v85, v162, v85 row_newbcast:1 row_mask:0xf bank_mask:0xf\n"
"v_mul_f32_dpp v86, v162, v86 row_newbcast:2 row_mask:0xf bank_mask:0xf\n"
"v_mul_f32_dpp v87, v162, v87 row_newbcast:3 row_mask:0xf bank_mask:0xf\n"
"v_mul_f32_dpp v88, v162, v88 row_newbcast:0 row_mask:0xf bank_mask:0xf\n"
"v_mul_f32_dpp v89, v162, v89 row_newbcast:1 row_mask:0xf bank_mask:0xf\n"
"v_mul_f32_dpp v90, v162, v90 row_newbcast:2 row_mask:0xf bank_mask:0xf\n"
"v_mul_f32_dpp v91, v162, v91 row_newbcast:3 row_mask:0xf bank_mask:0xf\n"
"v_mul_f32_dpp v92, v162, v92 row_newbcast:0 row_mask:0xf bank_mask:0xf\n"
"v_mul_f32_dpp v93, v162, v93 row_newbcast:1 row_mask:0xf bank_mask:0xf\n"
"v_mul_f32_dpp v94, v162, v94 row_newbcast:2 row_mask:0xf bank_mask:0xf\n"
"v_mul_f32_dpp v95, v162, v95 row_newbcast:3 row_mask:0xf bank_mask:0xf\n"
"s_cmp_lg_u32 s83, s43\n"
"s_cbranch_scc1 LSK3A_%=\n"
"v_max3_f32 v165, v120, v121, v122\n"
"v_max3_f32 v165, v165, v123, v124\n"
"v_max3_f32 v165, v165, v125, v126\n"
"v_max3_f32 v165, v165, v127, v164\n"
"v_mov_b32 v166, v165\n"
"s_nop 1\n"
"v_permlane32_swap_b32 v165, v166\n"
"v_max_f32 v165, v165, v166\n"
"v_mov_b32 v166, v165\n"
"s_nop 1\n"
"v_permlane16_swap_b32 v165, v166\n"
"v_max_f32 v165, v165, v166\n"
"v_sub_f32 v166, v164, v165\n"
"v_exp_f32 v166, v166\n"
"v_mov_b32 v164, v165\n"
"v_sub_f32 v120, v120, v164\n"
"v_sub_f32 v121, v121, v164\n"
"v_sub_f32 v122, v122, v164\n"
"v_sub_f32 v123, v123, v164\n"
"v_sub_f32 v124, v124, v164\n"
"v_sub_f32 v125, v125, v164\n"
"v_sub_f32 v126, v126, v164\n"
"v_sub_f32 v127, v127, v164\n"
"v_exp_f32 v120, v120\n"
"v_exp_f32 v121, v121\n"
"v_exp_f32 v122, v122\n"
"v_exp_f32 v123, v123\n"
"v_exp_f32 v124, v124\n"
"v_exp_f32 v125, v125\n"
"v_exp_f32 v126, v126\n"
"v_exp_f32 v127, v127\n"
"v_mul_f32 v167, v167, v166\n"
"v_add_f32 v167, v167, v120\n"
"v_add_f32 v167, v167, v121\n"
"v_add_f32 v167, v167, v122\n"
"v_add_f32 v167, v167, v123\n"
"v_add_f32 v167, v167, v124\n"
"v_add_f32 v167, v167, v125\n"
"v_add_f32 v167, v167, v126\n"
"v_add_f32 v167, v167, v127\n"
"v_cvt_pk_bf16_f32 v120, v120, v121\n"
"v_cvt_pk_bf16_f32 v121, v122, v123\n"
"v_cvt_pk_bf16_f32 v122, v124, v125\n"
"v_cvt_pk_bf16_f32 v123, v126, v127\n"
"v_mov_b32_dpp v166, v166 row_ror:12 row_mask:0x2 bank_mask:0xf\n"
"s_nop 1\n"
"v_mov_b32_dpp v166, v166 row_ror:8 row_mask:0x4 bank_mask:0xf\n"
"s_nop 1\n"
"v_mov_b32_dpp v166, v166 row_ror:4 row_mask:0x8 bank_mask:0xf\n"
"v_accvgpr_read_b32 v124, a208\n"
"s_nop 1\n"
"v_mul_f32_dpp v124, v166, v124 row_newbcast:0 row_mask:0xf bank_mask:0xf\n"
"v_accvgpr_write_b32 a208, v124\n"
"v_accvgpr_read_b32 v125, a209\n"
"s_nop 1\n"
"v_mul_f32_dpp v125, v166, v125 row_newbcast:1 row_mask:0xf bank_mask:0xf\n"
"v_accvgpr_write_b32 a209, v125\n"
"v_accvgpr_read_b32 v126, a210\n"
"s_nop 1\n"
"v_mul_f32_dpp v126, v166, v126 row_newbcast:2 row_mask:0xf bank_mask:0xf\n"
"v_accvgpr_write_b32 a210, v126\n"
"v_accvgpr_read_b32 v127, a211\n"
"s_nop 1\n"
"v_mul_f32_dpp v127, v166, v127 row_newbcast:3 row_mask:0xf bank_mask:0xf\n"
"v_accvgpr_write_b32 a211, v127\n"
"v_accvgpr_read_b32 v124, a212\n"
"s_nop 1\n"
"v_mul_f32_dpp v124, v166, v124 row_newbcast:0 row_mask:0xf bank_mask:0xf\n"
"v_accvgpr_write_b32 a212, v124\n"
"v_accvgpr_read_b32 v125, a213\n"
"s_nop 1\n"
"v_mul_f32_dpp v125, v166, v125 row_newbcast:1 row_mask:0xf bank_mask:0xf\n"
"v_accvgpr_write_b32 a213, v125\n"
"v_accvgpr_read_b32 v126, a214\n"
"s_nop 1\n"
"v_mul_f32_dpp v126, v166, v126 row_newbcast:2 row_mask:0xf bank_mask:0xf\n"
"v_accvgpr_write_b32 a214, v126\n"
"v_accvgpr_read_b32 v127, a215\n"
"s_nop 1\n"
"v_mul_f32_dpp v127, v166, v127 row_newbcast:3 row_mask:0xf bank_mask:0xf\n"
"v_accvgpr_write_b32 a215, v127\n"
"v_accvgpr_read_b32 v124, a216\n"
"s_nop 1\n"
"v_mul_f32_dpp v124, v166, v124 row_newbcast:0 row_mask:0xf bank_mask:0xf\n"
"v_accvgpr_write_b32 a216, v124\n"
"v_accvgpr_read_b32 v125, a217\n"
"s_nop 1\n"
"v_mul_f32_dpp v125, v166, v125 row_newbcast:1 row_mask:0xf bank_mask:0xf\n"
"v_accvgpr_write_b32 a217, v125\n"
"v_accvgpr_read_b32 v126, a218\n"
"s_nop 1\n"
"v_mul_f32_dpp v126, v166, v126 row_newbcast:2 row_mask:0xf bank_mask:0xf\n"
"v_accvgpr_write_b32 a218, v126\n"
"v_accvgpr_read_b32 v127, a219\n"
"s_nop 1\n"
"v_mul_f32_dpp v127, v166, v127 row_newbcast:3 row_mask:0xf bank_mask:0xf\n"
"v_accvgpr_write_b32 a219, v127\n"
"v_accvgpr_read_b32 v124, a220\n"
"s_nop 1\n"
"v_mul_f32_dpp v124, v166, v124 row_newbcast:0 row_mask:0xf bank_mask:0xf\n"
"v_accvgpr_write_b32 a220, v124\n"
"v_accvgpr_read_b32 v125, a221\n"
"s_nop 1\n"
"v_mul_f32_dpp v125, v166, v125 row_newbcast:1 row_mask:0xf bank_mask:0xf\n"
"v_accvgpr_write_b32 a221, v125\n"
"v_accvgpr_read_b32 v126, a222\n"
"s_nop 1\n"
"v_mul_f32_dpp v126, v166, v126 row_newbcast:2 row_mask:0xf bank_mask:0xf\n"
"v_accvgpr_write_b32 a222, v126\n"
"v_accvgpr_read_b32 v127, a223\n"
"s_nop 1\n"
"v_mul_f32_dpp v127, v166, v127 row_newbcast:3 row_mask:0xf bank_mask:0xf\n"
"v_accvgpr_write_b32 a223, v127\n"
"v_accvgpr_read_b32 v124, a224\n"
"s_nop 1\n"
"v_mul_f32_dpp v124, v166, v124 row_newbcast:0 row_mask:0xf bank_mask:0xf\n"
"v_accvgpr_write_b32 a224, v124\n"
"v_accvgpr_read_b32 v125, a225\n"
"s_nop 1\n"
"v_mul_f32_dpp v125, v166, v125 row_newbcast:1 row_mask:0xf bank_mask:0xf\n"
"v_accvgpr_write_b32 a225, v125\n"
"v_accvgpr_read_b32 v126, a226\n"
"s_nop 1\n"
"v_mul_f32_dpp v126, v166, v126 row_newbcast:2 row_mask:0xf bank_mask:0xf\n"
"v_accvgpr_write_b32 a226, v126\n"
"v_accvgpr_read_b32 v127, a227\n"
"s_nop 1\n"
"v_mul_f32_dpp v127, v166, v127 row_newbcast:3 row_mask:0xf bank_mask:0xf\n"
"v_accvgpr_write_b32 a227, v127\n"
"v_accvgpr_read_b32 v124, a228\n"
"s_nop 1\n"
"v_mul_f32_dpp v124, v166, v124 row_newbcast:0 row_mask:0xf bank_mask:0xf\n"
"v_accvgpr_write_b32 a228, v124\n"
"v_accvgpr_read_b32 v125, a229\n"
"s_nop 1\n"
"v_mul_f32_dpp v125, v166, v125 row_newbcast:1 row_mask:0xf bank_mask:0xf\n"
"v_accvgpr_write_b32 a229, v125\n"
"v_accvgpr_read_b32 v126, a230\n"
"s_nop 1\n"
"v_mul_f32_dpp v126, v166, v126 row_newbcast:2 row_mask:0xf bank_mask:0xf\n"
"v_accvgpr_write_b32 a230, v126\n"
"v_accvgpr_read_b32 v127, a231\n"
"s_nop 1\n"
"v_mul_f32_dpp v127, v166, v127 row_newbcast:3 row_mask:0xf bank_mask:0xf\n"
"v_accvgpr_write_b32 a231, v127\n"
"v_accvgpr_read_b32 v124, a232\n"
"s_nop 1\n"
"v_mul_f32_dpp v124, v166, v124 row_newbcast:0 row_mask:0xf bank_mask:0xf\n"
"v_accvgpr_write_b32 a232, v124\n"
"v_accvgpr_read_b32 v125, a233\n"
"s_nop 1\n"
"v_mul_f32_dpp v125, v166, v125 row_newbcast:1 row_mask:0xf bank_mask:0xf\n"
"v_accvgpr_write_b32 a233, v125\n"
"v_accvgpr_read_b32 v126, a234\n"
"s_nop 1\n"
"v_mul_f32_dpp v126, v166, v126 row_newbcast:2 row_mask:0xf bank_mask:0xf\n"
"v_accvgpr_write_b32 a234, v126\n"
"v_accvgpr_read_b32 v127, a235\n"
"s_nop 1\n"
"v_mul_f32_dpp v127, v166, v127 row_newbcast:3 row_mask:0xf bank_mask:0xf\n"
"v_accvgpr_write_b32 a235, v127\n"
"v_accvgpr_read_b32 v124, a236\n"
"s_nop 1\n"
"v_mul_f32_dpp v124, v166, v124 row_newbcast:0 row_mask:0xf bank_mask:0xf\n"
"v_accvgpr_write_b32 a236, v124\n"
"v_accvgpr_read_b32 v125, a237\n"
"s_nop 1\n"
"v_mul_f32_dpp v125, v166, v125 row_newbcast:1 row_mask:0xf bank_mask:0xf\n"
"v_accvgpr_write_b32 a237, v125\n"
"v_accvgpr_read_b32 v126, a238\n"
"s_nop 1\n"
"v_mul_f32_dpp v126, v166, v126 row_newbcast:2 row_mask:0xf bank_mask:0xf\n"
"v_accvgpr_write_b32 a238, v126\n"
"v_accvgpr_read_b32 v127, a239\n"
"s_nop 1\n"
"v_mul_f32_dpp v127, v166, v127 row_newbcast:3 row_mask:0xf bank_mask:0xf\n"
"v_accvgpr_write_b32 a239, v127\n"
"LSK3A_%=:\n"
"s_nop 3\n"
"buffer_load_ushort v128, v146, s[88:91], s80 offen offset:0\n"
"buffer_load_ushort v129, v146, s[88:91], s80 offen offset:256\n"
"buffer_load_ushort v130, v146, s[88:91], s80 offen offset:512\n"
"buffer_load_ushort v131, v146, s[88:91], s80 offen offset:768\n"
"buffer_load_ushort v132, v146, s[88:91], s81 offen offset:0\n"
"buffer_load_ushort v133, v146, s[88:91], s81 offen offset:256\n"
"buffer_load_ushort v134, v146, s[88:91], s81 offen offset:512\n"
"buffer_load_ushort v135, v146, s[88:91], s81 offen offset:768\n"
"buffer_load_ushort v136, v146, s[88:91], s80 offen offset:32\n"
"buffer_load_ushort v137, v146, s[88:91], s80 offen offset:288\n"
"buffer_load_ushort v138, v146, s[88:91], s80 offen offset:544\n"
"buffer_load_ushort v139, v146, s[88:91], s80 offen offset:800\n"
"buffer_load_ushort v140, v146, s[88:91], s81 offen offset:32\n"
"buffer_load_ushort v141, v146, s[88:91], s81 offen offset:288\n"
"buffer_load_ushort v142, v146, s[88:91], s81 offen offset:544\n"
"buffer_load_ushort v143, v146, s[88:91], s81 offen offset:800\n"
"s_waitcnt vmcnt(0)\n"
"v_lshl_or_b32 v128, v129, 16, v128\n"
"v_lshl_or_b32 v129, v131, 16, v130\n"
"v_lshl_or_b32 v130, v133, 16, v132\n"
"v_lshl_or_b32 v131, v135, 16, v134\n"
"v_lshl_or_b32 v136, v137, 16, v136\n"
"v_lshl_or_b32 v137, v139, 16, v138\n"
"v_lshl_or_b32 v138, v141, 16, v140\n"
"v_lshl_or_b32 v139, v143, 16, v142\n"
"v_mfma_f32_16x16x32_bf16 v[0:3], v[96:99], v[128:131], v[0:3]\n"
"s_nop 0\n"
"v_mfma_f32_16x16x32_bf16 v[4:7], v[96:99], v[136:139], v[4:7]\n"
"v_mfma_f32_16x16x32_bf16 v[32:35], v[104:107], v[128:131], v[32:35]\n"
"v_mfma_f32_16x16x32_bf16 v[36:39], v[104:107], v[136:139], v[36:39]\n"
"v_mfma_f32_16x16x32_bf16 v[64:67], v[112:115], v[128:131], v[64:67]\n"
"v_mfma_f32_16x16x32_bf16 v[68:71], v[112:115], v[136:139], v[68:71]\n"
"s_cmp_lg_u32 s83, s43\n"
"s_cbranch_scc1 LSK3B0_%=\n"
"v_mfma_f32_16x16x32_bf16 a[208:211], v[120:123], v[128:131], a[208:211]\n"
"v_mfma_f32_16x16x32_bf16 a[212:215], v[120:123], v[136:139], a[212:215]\n"
"LSK3B0_%=:\n"
"s_nop 3\n"
"buffer_load_ushort v128, v147, s[88:91], s80 offen offset:0\n"
"buffer_load_ushort v129, v147, s[88:91], s80 offen offset:256\n"
"buffer_load_ushort v130, v147, s[88:91], s80 offen offset:512\n"
"buffer_load_ushort v131, v147, s[88:91], s80 offen offset:768\n"
"buffer_load_ushort v132, v147, s[88:91], s81 offen offset:0\n"
"buffer_load_ushort v133, v147, s[88:91], s81 offen offset:256\n"
"buffer_load_ushort v134, v147, s[88:91], s81 offen offset:512\n"
"buffer_load_ushort v135, v147, s[88:91], s81 offen offset:768\n"
"buffer_load_ushort v136, v147, s[88:91], s80 offen offset:32\n"
"buffer_load_ushort v137, v147, s[88:91], s80 offen offset:288\n"
"buffer_load_ushort v138, v147, s[88:91], s80 offen offset:544\n"
"buffer_load_ushort v139, v147, s[88:91], s80 offen offset:800\n"
"buffer_load_ushort v140, v147, s[88:91], s81 offen offset:32\n"
"buffer_load_ushort v141, v147, s[88:91], s81 offen offset:288\n"
"buffer_load_ushort v142, v147, s[88:91], s81 offen offset:544\n"
"buffer_load_ushort v143, v147, s[88:91], s81 offen offset:800\n"
"s_waitcnt vmcnt(0)\n"
"v_lshl_or_b32 v128, v129, 16, v128\n"
"v_lshl_or_b32 v129, v131, 16, v130\n"
"v_lshl_or_b32 v130, v133, 16, v132\n"
"v_lshl_or_b32 v131, v135, 16, v134\n"
"v_lshl_or_b32 v136, v137, 16, v136\n"
"v_lshl_or_b32 v137, v139, 16, v138\n"
"v_lshl_or_b32 v138, v141, 16, v140\n"
"v_lshl_or_b32 v139, v143, 16, v142\n"
"v_mfma_f32_16x16x32_bf16 v[8:11], v[96:99], v[128:131], v[8:11]\n"
"s_nop 0\n"
"v_mfma_f32_16x16x32_bf16 v[12:15], v[96:99], v[136:139], v[12:15]\n"
"v_mfma_f32_16x16x32_bf16 v[40:43], v[104:107], v[128:131], v[40:43]\n"
"v_mfma_f32_16x16x32_bf16 v[44:47], v[104:107], v[136:139], v[44:47]\n"
"v_mfma_f32_16x16x32_bf16 v[72:75], v[112:115], v[128:131], v[72:75]\n"
"v_mfma_f32_16x16x32_bf16 v[76:79], v[112:115], v[136:139], v[76:79]\n"
"s_cmp_lg_u32 s83, s43\n"
"s_cbranch_scc1 LSK3B1_%=\n"
"v_mfma_f32_16x16x32_bf16 a[216:219], v[120:123], v[128:131], a[216:219]\n"
"v_mfma_f32_16x16x32_bf16 a[220:223], v[120:123], v[136:139], a[220:223]\n"
"LSK3B1_%=:\n"
"s_nop 3\n"
"buffer_load_ushort v128, v146, s[88:91], s80 offen offset:128\n"
"buffer_load_ushort v129, v146, s[88:91], s80 offen offset:384\n"
"buffer_load_ushort v130, v146, s[88:91], s80 offen offset:640\n"
"buffer_load_ushort v131, v146, s[88:91], s80 offen offset:896\n"
"buffer_load_ushort v132, v146, s[88:91], s81 offen offset:128\n"
"buffer_load_ushort v133, v146, s[88:91], s81 offen offset:384\n"
"buffer_load_ushort v134, v146, s[88:91], s81 offen offset:640\n"
"buffer_load_ushort v135, v146, s[88:91], s81 offen offset:896\n"
"buffer_load_ushort v136, v146, s[88:91], s80 offen offset:160\n"
"buffer_load_ushort v137, v146, s[88:91], s80 offen offset:416\n"
"buffer_load_ushort v138, v146, s[88:91], s80 offen offset:672\n"
"buffer_load_ushort v139, v146, s[88:91], s80 offen offset:928\n"
"buffer_load_ushort v140, v146, s[88:91], s81 offen offset:160\n"
"buffer_load_ushort v141, v146, s[88:91], s81 offen offset:416\n"
"buffer_load_ushort v142, v146, s[88:91], s81 offen offset:672\n"
"buffer_load_ushort v143, v146, s[88:91], s81 offen offset:928\n"
"s_waitcnt vmcnt(0)\n"
"v_lshl_or_b32 v128, v129, 16, v128\n"
"v_lshl_or_b32 v129, v131, 16, v130\n"
"v_lshl_or_b32 v130, v133, 16, v132\n"
"v_lshl_or_b32 v131, v135, 16, v134\n"
"v_lshl_or_b32 v136, v137, 16, v136\n"
"v_lshl_or_b32 v137, v139, 16, v138\n"
"v_lshl_or_b32 v138, v141, 16, v140\n"
"v_lshl_or_b32 v139, v143, 16, v142\n"
"v_mfma_f32_16x16x32_bf16 v[16:19], v[96:99], v[128:131], v[16:19]\n"
"s_nop 0\n"
"v_mfma_f32_16x16x32_bf16 v[20:23], v[96:99], v[136:139], v[20:23]\n"
"v_mfma_f32_16x16x32_bf16 v[48:51], v[104:107], v[128:131], v[48:51]\n"
"v_mfma_f32_16x16x32_bf16 v[52:55], v[104:107], v[136:139], v[52:55]\n"
"v_mfma_f32_16x16x32_bf16 v[80:83], v[112:115], v[128:131], v[80:83]\n"
"v_mfma_f32_16x16x32_bf16 v[84:87], v[112:115], v[136:139], v[84:87]\n"
"s_cmp_lg_u32 s83, s43\n"
"s_cbranch_scc1 LSK3B2_%=\n"
"v_mfma_f32_16x16x32_bf16 a[224:227], v[120:123], v[128:131], a[224:227]\n"
"v_mfma_f32_16x16x32_bf16 a[228:231], v[120:123], v[136:139], a[228:231]\n"
"LSK3B2_%=:\n"
"s_nop 3\n"
"buffer_load_ushort v128, v147, s[88:91], s80 offen offset:128\n"
"buffer_load_ushort v129, v147, s[88:91], s80 offen offset:384\n"
"buffer_load_ushort v130, v147, s[88:91], s80 offen offset:640\n"
"buffer_load_ushort v131, v147, s[88:91], s80 offen offset:896\n"
"buffer_load_ushort v132, v147, s[88:91], s81 offen offset:128\n"
"buffer_load_ushort v133, v147, s[88:91], s81 offen offset:384\n"
"buffer_load_ushort v134, v147, s[88:91], s81 offen offset:640\n"
"buffer_load_ushort v135, v147, s[88:91], s81 offen offset:896\n"
"buffer_load_ushort v136, v147, s[88:91], s80 offen offset:160\n"
"buffer_load_ushort v137, v147, s[88:91], s80 offen offset:416\n"
"buffer_load_ushort v138, v147, s[88:91], s80 offen offset:672\n"
"buffer_load_ushort v139, v147, s[88:91], s80 offen offset:928\n"
"buffer_load_ushort v140, v147, s[88:91], s81 offen offset:160\n"
"buffer_load_ushort v141, v147, s[88:91], s81 offen offset:416\n"
"buffer_load_ushort v142, v147, s[88:91], s81 offen offset:672\n"
"buffer_load_ushort v143, v147, s[88:91], s81 offen offset:928\n"
"s_waitcnt vmcnt(0)\n"
"v_lshl_or_b32 v128, v129, 16, v128\n"
"v_lshl_or_b32 v129, v131, 16, v130\n"
"v_lshl_or_b32 v130, v133, 16, v132\n"
"v_lshl_or_b32 v131, v135, 16, v134\n"
"v_lshl_or_b32 v136, v137, 16, v136\n"
"v_lshl_or_b32 v137, v139, 16, v138\n"
"v_lshl_or_b32 v138, v141, 16, v140\n"
"v_lshl_or_b32 v139, v143, 16, v142\n"
"v_mfma_f32_16x16x32_bf16 v[24:27], v[96:99], v[128:131], v[24:27]\n"
"s_nop 0\n"
"v_mfma_f32_16x16x32_bf16 v[28:31], v[96:99], v[136:139], v[28:31]\n"
"v_mfma_f32_16x16x32_bf16 v[56:59], v[104:107], v[128:131], v[56:59]\n"
"v_mfma_f32_16x16x32_bf16 v[60:63], v[104:107], v[136:139], v[60:63]\n"
"v_mfma_f32_16x16x32_bf16 v[88:91], v[112:115], v[128:131], v[88:91]\n"
"v_mfma_f32_16x16x32_bf16 v[92:95], v[112:115], v[136:139], v[92:95]\n"
"s_cmp_lg_u32 s83, s43\n"
"s_cbranch_scc1 LSK3B3_%=\n"
"v_mfma_f32_16x16x32_bf16 a[232:235], v[120:123], v[128:131], a[232:235]\n"
"v_mfma_f32_16x16x32_bf16 a[236:239], v[120:123], v[136:139], a[236:239]\n"
"LSK3B3_%=:\n"
"s_nop 3\n"
"s_nop 9\n"
"s_add_u32 s80, s80, 8192\n"
"s_add_u32 s81, s81, 8192\n"
"s_add_u32 s82, s82, 1\n"
"s_cmp_lt_u32 s82, 32\n"
"s_cbranch_scc1 LSLOOP_%=\n"
"v_mov_b32 v154, v155\n"
"s_nop 1\n"
"v_permlane32_swap_b32 v155, v154\n"
"v_add_f32 v155, v155, v154\n"
"v_mov_b32 v154, v155\n"
"s_nop 1\n"
"v_permlane16_swap_b32 v155, v154\n"
"v_add_f32 v155, v155, v154\n"
"v_mov_b32 v196, v155\n"
"v_mov_b32 v158, v159\n"
"s_nop 1\n"
"v_permlane32_swap_b32 v159, v158\n"
"v_add_f32 v159, v159, v158\n"
"v_mov_b32 v158, v159\n"
"s_nop 1\n"
"v_permlane16_swap_b32 v159, v158\n"
"v_add_f32 v159, v159, v158\n"
"v_mov_b32 v198, v159\n"
"v_mov_b32 v162, v163\n"
"s_nop 1\n"
"v_permlane32_swap_b32 v163, v162\n"
"v_add_f32 v163, v163, v162\n"
"v_mov_b32 v162, v163\n"
"s_nop 1\n"
"v_permlane16_swap_b32 v163, v162\n"
"v_add_f32 v163, v163, v162\n"
"v_mov_b32 v200, v163\n"
"v_mov_b32 v166, v167\n"
"s_nop 1\n"
"v_permlane32_swap_b32 v167, v166\n"
"v_add_f32 v167, v167, v166\n"
"v_mov_b32 v166, v167\n"
"s_nop 1\n"
"v_permlane16_swap_b32 v167, v166\n"
"v_add_f32 v167, v167, v166\n"
"v_mov_b32 v202, v167\n"
"v_sub_f32 v192, 0, v164\n"
"v_mov_b32 v96, 0\n"
"v_mov_b32 v97, 0\n"
"v_mov_b32 v98, 0\n"
"v_mov_b32 v99, 0\n"
"v_mov_b32 v100, 0\n"
"v_mov_b32 v101, 0\n"
"v_mov_b32 v102, 0\n"
"v_mov_b32 v103, 0\n"
"v_mov_b32 v104, 0\n"
"v_mov_b32 v105, 0\n"
"v_mov_b32 v106, 0\n"
"v_mov_b32 v107, 0\n"
"v_mov_b32 v108, 0\n"
"v_mov_b32 v109, 0\n"
"v_mov_b32 v110, 0\n"
"v_mov_b32 v111, 0\n"
"v_mov_b32 v112, 0\n"
"v_mov_b32 v113, 0\n"
"v_mov_b32 v114, 0\n"
"v_mov_b32 v115, 0\n"
"v_mov_b32 v116, 0\n"
"v_mov_b32 v117, 0\n"
"v_mov_b32 v118, 0\n"
"v_mov_b32 v119, 0\n"
"v_mov_b32 v120, 0\n"
"v_mov_b32 v121, 0\n"
"v_mov_b32 v122, 0\n"
"v_mov_b32 v123, 0\n"
"v_mov_b32 v204, 0\n"
"v_mov_b32 v205, 0\n"
"s_nop 3\n"
"s_mov_b32 s68, 0\n"
"s_cmp_eq_u32 s47, 4\n"
"s_cbranch_scc1 LBACK0_%=\n"
"s_cmp_eq_u32 s47, 8\n"
"s_cbranch_scc1 LBACK1_%=\n"
"s_cmp_eq_u32 s47, 12\n"
"s_cbranch_scc1 LBACK2_%=\n"
"s_branch LBACK3_%=\n"
"LFIN_%=:\n"
    :: [kalo]"s"((unsigned)ka), [kahi]"s"((unsigned)(ka >> 32)), [wg]"s"(blockIdx.x), [wid]"s"(wid)
    : "v0","v1","v2","v3","v4","v5","v6","v7","v8","v9","v10","v11","v12","v13","v14","v15","v16","v17","v18","v19","v20","v21","v22","v23","v24","v25","v26","v27","v28","v29","v30","v31","v32","v33","v34","v35","v36","v37","v38","v39","v40","v41","v42","v43","v44","v45","v46","v47","v48","v49","v50","v51","v52","v53","v54","v55","v56","v57","v58","v59","v60","v61","v62","v63","v64","v65","v66","v67","v68","v69","v70","v71","v72","v73","v74","v75","v76","v77","v78","v79","v80","v81","v82","v83","v84","v85","v86","v87","v88","v89","v90","v91","v92","v93","v94","v95","v96","v97","v98","v99","v100","v101","v102","v103","v104","v105","v106","v107","v108","v109","v110","v111","v112","v113","v114","v115","v116","v117","v118","v119","v120","v121","v122","v123","v124","v125","v126","v127","v128","v129","v130","v131","v132","v133","v134","v135","v136","v137","v138","v139","v140","v141","v142","v143","v144","v145","v146","v147","v148","v149","v150","v151","v152","v153","v154","v155","v156","v157","v158","v159","v160","v161","v162","v163","v164","v165","v166","v167","v168","v169","v170","v171","v172","v173","v174","v175","v176","v177","v178","v179","v180","v181","v182","v183","v184","v185","v186","v187","v188","v189","v190","v191","v192","v193","v194","v195","v196","v197","v198","v199","v200","v201","v202","v203","v204","v205","v206","v207","v208","v209","v210","v211","v212","v213","v214","v215","v216","v217","v218","v219","v220","v221","v222","v223","v224","v225","v226","v227","v228","v229","v230","v231","v232","v233","v234","v235","v236","v237","v238","v239","v240","v241","v242","v243","v244","v245","v246","v247","v248","v249","v250","v251","v252","v253","v254","v255","a0","a1","a2","a3","a4","a5","a6","a7","a8","a9","a10","a11","a12","a13","a14","a15","a16","a17","a18","a19","a20","a21","a22","a23","a24","a25","a26","a27","a28","a29","a30","a31","a32","a33","a34","a35","a36","a37","a38","a39","a40","a41","a42","a43","a44","a45","a46","a47","a48","a49","a50","a51","a52","a53","a54","a55","a56","a57","a58","a59","a60","a61","a62","a63","a64","a65","a66","a67","a68","a69","a70","a71","a72","a73","a74","a75","a76","a77","a78","a79","a80","a81","a82","a83","a84","a85","a86","a87","a88","a89","a90","a91","a92","a93","a94","a95","a96","a97","a98","a99","a100","a101","a102","a103","a104","a105","a106","a107","a108","a109","a110","a111","a112","a113","a114","a115","a116","a117","a118","a119","a120","a121","a122","a123","a124","a125","a126","a127","a128","a129","a130","a131","a132","a133","a134","a135","a136","a137","a138","a139","a140","a141","a142","a143","a144","a145","a146","a147","a148","a149","a150","a151","a152","a153","a154","a155","a156","a157","a158","a159","a160","a161","a162","a163","a164","a165","a166","a167","a168","a169","a170","a171","a172","a173","a174","a175","a176","a177","a178","a179","a180","a181","a182","a183","a184","a185","a186","a187","a188","a189","a190","a191","a192","a193","a194","a195","a196","a197","a198","a199","a200","a201","a202","a203","a204","a205","a206","a207","a208","a209","a210","a211","a212","a213","a214","a215","a216","a217","a218","a219","a220","a221","a222","a223","a224","a225","a226","a227","a228","a229","a230","a231","a232","a233","a234","a235","a236","a237","a238","a239","a240","a241","a242","a243","a244","a245","a246","a247","a248","a249","a250","a251","a252","a253","a254","a255","s6","s7","s8","s9","s10","s11","s12","s13","s14","s15","s16","s17","s18","s19","s20","s21","s22","s23","s24","s25","s26","s27","s28","s29","s30","s31","s32","s33","s34","s35","s36","s37","s38","s39","s40","s41","s42","s43","s44","s45","s46","s47","s48","s49","s50","s51","s52","s53","s54","s55","s56","s57","s58","s59","s60","s61","s62","s63","s64","s65","s66","s67","s68","s69","s70","s71","s72","s73","s74","s75","s76","s77","s78","s79","s80","s81","s82","s83","s84","s85","s86","s87","s88","s89","s90","s91","s92","s93","s94","s95","s96","s97","s98","s99","s100","vcc","scc","m0","memory");
}
extern "C" void kernel_launch(void* const* d_in, const int* in_sizes, int n_in, void* d_out, int out_size, void* d_ws, size_t ws_size, hipStream_t stream) {
  char* xch = (char*)d_ws + ((ws_size - (size_t)270532608) & ~(size_t)0xfffff);
  (void)hipFuncSetAttribute((const void*)fused_attn, hipFuncAttributeMaxDynamicSharedMemorySize, 163840);
  hipLaunchKernelGGL(fused_attn, dim3(256), dim3(256), 163840, stream,
                     (const char*)d_in[0], (const char*)d_in[1], (const char*)d_in[2], (const char*)d_in[3], (const char*)d_in[4],
                     (const char*)d_in[5], (const char*)d_in[6], (const char*)d_in[7], (const char*)d_in[8], (const char*)d_in[9], xch, (char*)d_out, (char*)d_ws);
}
```

```cpp
#include <hip/hip_runtime.h>
#include <hip/hip_bf16.h>
#include <stdint.h>

__global__ __launch_bounds__(256, 1)
void fused_attn(const char* xA, const char* kA, const char* vA, const char* wA, const char* uA,
                const char* xB, const char* kB, const char* vB, const char* wB, const char* uB, char* xch, char* out, char* flags) {
  extern __shared__ __attribute__((aligned(16))) char lds[];
  const unsigned wid = __builtin_amdgcn_readfirstlane(threadIdx.x >> 6);
  const unsigned long long ka = (unsigned long long)__builtin_amdgcn_kernarg_segment_ptr();
  asm volatile(
"s_memtime s[64:65]\n"
"s_memrealtime s[66:67]\n"
"s_bfe_u32 s10, %[wg], 0x10002\n"
"s_mul_i32 s40, s10, 40\n"
"s_add_u32 s40, s40, %[kalo]\n"
"s_addc_u32 s41, %[kahi], 0\n"
"s_mov_b32 s78, %[kalo]\n"
"s_mov_b32 s79, %[kahi]\n"
"s_load_dwordx2 s[24:25], s[40:41], 0\n"
"s_load_dwordx2 s[28:29], s[40:41], 24\n"
"s_load_dwordx2 s[6:7], s[78:79], 0x60\n"
"s_load_dwordx2 s[16:17], s[40:41], 8\n"
"s_load_dwordx2 s[20:21], s[40:41], 16\n"
"s_load_dwordx2 s[8:9], s[40:41], 32\n"
"s_load_dwordx4 s[12:15], s[78:79], 0x50\n"
"s_mov_b32 s42, %[wid]\n"
"s_and_b32 s11, %[wg], 1\n"
"s_lshl_b32 s11, s11, 3\n"
"s_lshr_b32 s60, %[wg], 5\n"
"s_add_u32 s11, s11, s60\n"
"s_bfe_u32 s61, %[wg], 0x10001\n"
"s_bfe_u32 s60, %[wg], 0x20003\n"
"s_lshl2_add_u32 s61, s61, s60\n"
"s_getpc_b64 s[80:81]\n"
"LCP_%=:\n"
"s_mov_b32 s82, LEND_%=-LCP_%=\n"
"s_mov_b32 s83, 0x20000\n"
"v_mbcnt_lo_u32_b32 v2, -1, 0\n"
"v_lshlrev_b32 v2, 7, v2\n"
"s_lshr_b32 s60, %[wg], 3\n"
"s_lshl2_add_u32 s60, s60, s42\n"
"s_mul_i32 s60, s60, 640\n"
"s_mov_b64 exec, 31\n"
"buffer_load_dword v3, v2, s[80:83], s60 offen\n"
"s_mov_b64 exec, -1\n"
"s_mov_b32 s19, 0x20000\n"
"s_mov_b32 s23, 0x20000\n"
"s_mov_b32 s27, 0x20000\n"
"s_mov_b32 s31, 0x20000\n"
"s_mov_b32 s35, 0x20000\n"
"s_mov_b32 s39, 0x20000\n"
"s_mov_b32 s26, 286720\n"
"s_lshl_b32 s44, s42, 12\n"
"s_mov_b32 s50, s44\n"
"s_mul_i32 s51, s42, 7168\n"
"s_add_u32 s51, s51, 0x10000\n"
"s_mov_b32 s74, 0x8000\n"
"s_mov_b32 s58, 524288\n"
"s_mov_b32 s47, 0\n"
"s_add_u32 s60, s11, s61\n"
"s_and_b32 s60, s60, 0\n"
"s_mul_i32 s60, s60, 3\n"
"s_lshl_b32 s49, s60, 7\n"
"s_lshl_b32 s48, s60, 19\n"
"v_mbcnt_lo_u32_b32 v4, -1, 0\n"
"v_mbcnt_hi_u32_b32 v4, -1, v4\n"
"v_and_b32 v5, 15, v4\n"
"v_lshrrev_b32 v6, 4, v4\n"
"v_lshrrev_b32 v7, 3, v4\n"
"v_and_b32 v8, 7, v4\n"
"v_lshlrev_b32 v9, 1, v6\n"
"v_xor_b32 v9, v9, v8\n"
"v_lshlrev_b32 v9, 4, v9\n"
"s_mul_i32 s60, s42, 71680\n"
"v_mul_u32_u24 v10, 1280, v7\n"
"v_add3_u32 v248, v10, v9, s60\n"
"v_add_u32 v249, 9216, v248\n"
"v_add_u32 v254, 18432, v248\n"
"v_add_u32 v255, 27648, v248\n"
"v_and_b32 v10, 2, v7\n"
"v_xor_b32 v240, v10, v8\n"
"v_lshlrev_b32 v240, 4, v240\n"
"v_bfe_u32 v10, v7, 0, 1\n"
"v_lshl_add_u32 v240, v10, 8, v240\n"
"v_bfe_u32 v10, v7, 1, 1\n"
"v_lshl_add_u32 v240, v10, 9, v240\n"
"v_bfe_u32 v10, v7, 2, 1\n"
"v_lshl_add_u32 v240, v10, 7, v240\n"
"s_and_b32 s60, s42, 1\n"
"v_lshl_add_u32 v240, s60, 15, v240\n"
"s_lshr_b32 s60, s42, 1\n"
"v_lshl_add_u32 v240, s60, 16, v240\n"
"v_and_b32 v10, 1, v6\n"
"v_xor_b32 v10, v10, v8\n"
"v_lshlrev_b32 v10, 4, v10\n"
"v_lshl_add_u32 v10, v7, 13, v10\n"
"s_lshl_b32 s60, s42, 17\n"
"v_add_u32 v10, s60, v10\n"
"v_add_u32 v224, 1024, v10\n"
"v_add_u32 v225, 128, v10\n"
"v_xor_b32 v10, 64, v10\n"
"v_add_u32 v226, 64512, v10\n"
"v_add_u32 v227, 63616, v10\n"
"s_mul_i32 s40, s11, 286720\n"
"s_lshl_b32 s41, s61, 10\n"
"s_sub_u32 s30, 5243904, s41\n"
"s_sub_u32 s41, s41, 1024\n"
"s_ashr_i32 s60, s41, 31\n"
"s_lshl4_add_u32 s92, s10, s11\n"
"s_lshl_b32 s93, s92, 9\n"
"v_mov_b32 v0, 0\n"
"s_waitcnt lgkmcnt(0)\n"
"s_add_u32 s24, s24, s40\n"
"s_addc_u32 s25, s25, 0\n"
"s_and_b32 s25, s25, 0xffff\n"
"s_add_u32 s28, s28, s41\n"
"s_addc_u32 s29, s29, s60\n"
"s_and_b32 s29, s29, 0xffff\n"
"s_add_u32 s6, s6, s93\n"
"s_addc_u32 s7, s7, 0\n"
"buffer_load_dword v12, v248, s[24:27], s49 offen\n"
"buffer_load_dword v13, v224, s[28:31], s48 offen\n"
"global_load_dword v1, v0, s[6:7] offset:128 sc1\n"
"s_mov_b32 m0, s51\n"
"s_nop 0\n"
"buffer_load_dwordx4 v248, s[24:27], s49 offen lds\n"
"buffer_load_dwordx4 v249, s[24:27], s49 offen offset:1024 lds\n"
"buffer_load_dwordx4 v254, s[24:27], s49 offen offset:2048 lds\n"
"buffer_load_dwordx4 v255, s[24:27], s49 offen offset:3072 lds\n"
"s_add_u32 m0, s51, 4096\n"
"s_add_u32 s40, s49, 40960\n"
"buffer_load_dwordx4 v248, s[24:27], s40 offen lds\n"
"buffer_load_dwordx4 v249, s[24:27], s40 offen offset:1024 lds\n"
"buffer_load_dwordx4 v254, s[24:27], s40 offen offset:2048 lds\n"
"s_add_u32 s49, s49, 128\n"
"s_sub_u32 s41, s49, 1280\n"
"s_min_u32 s49, s49, s41\n"
"s_xor_b32 s51, s51, s74\n"
"s_mov_b32 m0, s50\n"
"s_nop 0\n"
"buffer_load_dwordx4 v224, s[28:31], s48 offen lds\n"
"buffer_load_dwordx4 v225, s[28:31], s48 offen offset:1024 lds\n"
"buffer_load_dwordx4 v226, s[28:31], s48 offen offset:2048 lds\n"
"buffer_load_dwordx4 v227, s[28:31], s48 offen offset:3072 lds\n"
"s_xor_b32 s50, s50, s74\n"
"s_lshl2_add_u32 s63, s11, s42\n"
"s_mul_i32 s63, s63, 10240\n"
"s_and_b32 s78, s42, 1\n"
"s_lshl_b32 s43, s78, 6\n"
"s_mov_b32 s77, 0x80000000\n"
"s_mov_b32 s75, 0x10000\n"
"s_mov_b32 s73, 0xffff0000\n"
"s_mov_b32 s46, 0x3e0293ee\n"
"s_mov_b32 s45, 0x63800000\n"
"s_mov_b32 s62, 9\n"
"v_lshl_add_u32 v242, v4, 4, s44\n"
"s_lshr_b32 s79, s42, 1\n"
"s_lshl4_add_u32 s76, s79, s78\n"
"s_lshl_b32 s76, s76, 10\n"
"s_add_u32 s76, s76, 0x20000\n"
"s_sub_u32 s76, s76, s44\n"
"v_lshlrev_b32 v9, 6, v6\n"
"v_xor_b32 v9, v9, v242\n"
"v_add_u32 v243, s76, v9\n"
"v_and_b32 v9, 6, v5\n"
"s_xor_b32 m0, s50, 0xc000\n"
"s_add_u32 s40, s48, 256\n"
"buffer_load_dwordx4 v224, s[28:31], s40 offen lds\n"
"v_xor_b32 v9, v9, v6\n"
"v_lshlrev_b32 v9, 4, v9\n"
"v_lshl_add_u32 v10, v5, 7, v9\n"
"s_mul_i32 s79, s42, 6144\n"
"s_add_u32 s79, s79, 0x10000\n"
"v_add_u32 v246, s79, v10\n"
"v_xor_b32 v247, 64, v246\n"
"s_lshr_b32 s79, s42, 1\n"
"s_lshl_b32 s79, s79, 11\n"
"buffer_load_dwordx4 v225, s[28:31], s40 offen offset:1024 lds\n"
"s_add_u32 s79, s79, 90112\n"
"v_add_u32 v252, s79, v10\n"
"v_xor_b32 v253, 64, v252\n"
"s_xor_b32 s79, s42, 1\n"
"s_mul_i32 s79, s79, 6144\n"
"s_add_u32 s79, s79, 0x10000\n"
"v_add_u32 v250, s79, v10\n"
"v_xor_b32 v251, 64, v250\n"
"s_mul_i32 s79, s78, 8256\n"
"buffer_load_dwordx4 v226, s[28:31], s40 offen offset:2048 lds\n"
"v_lshlrev_b32 v9, 10, v6\n"
"v_bfe_u32 v10, v4, 2, 2\n"
"v_lshl_add_u32 v9, v10, 7, v9\n"
"v_and_b32 v10, 3, v4\n"
"v_lshl_add_u32 v9, v10, 3, v9\n"
"v_bfe_u32 v10, v4, 3, 2\n"
"v_lshl_add_u32 v9, v10, 5, v9\n"
"s_add_u32 s79, s79, 0x10000\n"
"v_xor_b32 v232, s79, v9\n"
"buffer_load_dwordx4 v227, s[28:31], s40 offen offset:3072 lds\n"
"s_cmp_eq_u32 s47, 8\n"
"s_cselect_b32 s41, s52, s58\n"
"s_add_u32 s48, s48, s41\n"
"s_sub_u32 s41, s48, 5242880\n"
"s_min_u32 s48, s48, s41\n"
"v_xor_b32 v233, 32, v232\n"
"v_xor_b32 v234, 64, v232\n"
"v_xor_b32 v235, 96, v232\n"
"v_xor_b32 v236, 0x2000, v232\n"
"v_xor_b32 v237, 0x2000, v233\n"
"v_xor_b32 v238, 0x2000, v234\n"
"v_xor_b32 v239, 0x2000, v235\n"
"v_lshlrev_b32 v9, 11, v6\n"
"v_bfe_u32 v10, v4, 2, 2\n"
"s_mov_b32 m0, s50\n"
"s_nop 0\n"
"buffer_load_dwordx4 v224, s[28:31], s48 offen lds\n"
"v_lshl_add_u32 v9, v10, 7, v9\n"
"v_and_b32 v10, 1, v4\n"
"v_lshl_add_u32 v9, v10, 3, v9\n"
"v_bfe_u32 v10, v4, 1, 1\n"
"v_lshl_add_u32 v9, v10, 5, v9\n"
"v_bfe_u32 v10, v4, 3, 1\n"
"v_lshl_add_u32 v9, v10, 4, v9\n"
"v_bfe_u32 v10, v4, 4, 1\n"
"v_lshl_add_u32 v9, v10, 6, v9\n"
"buffer_load_dwordx4 v225, s[28:31], s48 offen offset:1024 lds\n"
"v_xor_b32 v244, s43, v9\n"
"v_xor_b32 v245, 16, v244\n"
"s_lshl3_add_u32 s79, s11, s61\n"
"s_lshl_b32 s79, s79, 20\n"
"s_sub_u32 s79, s79, 262144\n"
"s_ashr_i32 s60, s79, 31\n"
"s_add_u32 s16, s16, s79\n"
"s_addc_u32 s17, s17, s60\n"
"s_and_b32 s17, s17, 0xffff\n"
"buffer_load_dwordx4 v226, s[28:31], s48 offen offset:2048 lds\n"
"s_mov_b32 s18, 1310720\n"
"s_mov_b64 s[84:85], s[16:17]\n"
"s_mov_b64 s[86:87], s[18:19]\n"
"s_add_u32 s20, s20, s79\n"
"s_addc_u32 s21, s21, s60\n"
"s_and_b32 s21, s21, 0xffff\n"
"s_mov_b32 s22, 1310720\n"
"s_mov_b64 s[88:89], s[20:21]\n"
"s_mov_b64 s[90:91], s[22:23]\n"
"buffer_load_dwordx4 v227, s[28:31], s48 offen offset:3072 lds\n"
"s_xor_b32 s50, s50, s74\n"
"s_mov_b32 s57, 106496\n"
"s_bfe_u32 s55, %[wg], 65539\n"
"s_mul_i32 s55, s55, 16384\n"
"s_xor_b32 s57, s57, s55\n"
"s_bfe_u32 s55, %[wg], 65541\n"
"s_mul_i32 s55, s55, 2048\n"
"s_xor_b32 s57, s57, s55\n"
"s_bfe_u32 s55, %[wg], 65542\n"
"s_mul_i32 s55, s55, 4096\n"
"s_mov_b32 m0, s51\n"
"s_nop 0\n"
"buffer_load_dwordx4 v248, s[24:27], s49 offen lds\n"
"s_xor_b32 s57, s57, s55\n"
"s_mov_b32 s55, 368640\n"
"s_xor_b32 s56, s55, s57\n"
"s_mov_b32 s59, 2048\n"
"s_mov_b32 s54, 6144\n"
"s_add_u32 s52, s11, s61\n"
"s_and_b32 s52, s52, 0\n"
"s_mul_i32 s52, s52, 3\n"
"s_add_u32 s52, s52, 1\n"
"buffer_load_dwordx4 v249, s[24:27], s49 offen offset:1024 lds\n"
"s_lshl_b32 s53, s52, 7\n"
"s_lshl_b32 s52, s52, 19\n"
"s_add_u32 s52, s52, 512\n"
"s_mul_i32 s79, s61, 655360\n"
"s_add_u32 s8, s8, s79\n"
"s_addc_u32 s9, s9, 0\n"
"s_and_b32 s9, s9, 0xffff\n"
"s_add_u32 s12, s12, 0x1fffff\n"
"s_addc_u32 s13, s13, 0\n"
"buffer_load_dwordx4 v254, s[24:27], s49 offen offset:2048 lds\n"
"s_and_b32 s12, s12, 0xffe00000\n"
"s_mul_i32 s96, s42, 3\n"
"s_and_b32 s96, s96, 5\n"
"s_cmp_eq_u32 s42, 3\n"
"s_cselect_b32 s96, 5, s96\n"
"s_lshl_b32 s79, s61, 8\n"
"s_add_u32 s96, s96, s79\n"
"s_add_u32 s96, s96, 160\n"
"s_lshl_b32 s96, s96, 12\n"
"buffer_load_dwordx4 v255, s[24:27], s49 offen offset:3072 lds\n"
"s_mul_i32 s79, s92, 8388608\n"
"s_add_u32 s32, s12, s79\n"
"s_addc_u32 s33, s13, 0\n"
"s_and_b32 s33, s33, 0xffff\n"
"s_mov_b32 s34, 8388608\n"
"s_mul_i32 s79, s11, 573440\n"
"s_mul_i32 s60, s10, 1280\n"
"s_sub_u32 s38, 573440, s60\n"
"s_add_u32 s79, s79, s60\n"
"s_add_u32 m0, s51, 4096\n"
"s_add_u32 s40, s49, 40960\n"
"buffer_load_dwordx4 v248, s[24:27], s40 offen lds\n"
"s_add_u32 s36, s14, s79\n"
"s_addc_u32 s37, s15, 0\n"
"s_and_b32 s37, s37, 0xffff\n"
"s_lshl2_add_u32 s93, s61, s42\n"
"s_add_u32 s95, s61, 1\n"
"s_and_b32 s95, s95, 7\n"
"s_lshl_b32 s95, s95, 7\n"
"s_add_u32 s100, s61, 2\n"
"s_and_b32 s100, s100, 7\n"
"buffer_load_dwordx4 v249, s[24:27], s40 offen offset:1024 lds\n"
"s_lshl_b32 s100, s100, 7\n"
"s_lshl_b32 s60, s61, 7\n"
"s_add_u32 s60, s60, s43\n"
"s_mul_i32 s97, s42, 122880\n"
"s_add_u32 s97, s97, s60\n"
"s_lshr_b32 s98, s42, 1\n"
"s_add_u32 s98, s98, 12\n"
"s_mul_i32 s98, s98, 40960\n"
"s_add_u32 s98, s98, s60\n"
"buffer_load_dwordx4 v254, s[24:27], s40 offen offset:2048 lds\n"
"s_add_u32 s49, s49, 128\n"
"s_sub_u32 s41, s49, 1280\n"
"s_min_u32 s49, s49, s41\n"
"s_xor_b32 s51, s51, s74\n"
"s_mul_i32 s60, s93, 37\n"
"s_lshr_b32 s60, s60, 8\n"
"s_mul_i32 s79, s60, 7\n"
"s_sub_u32 s79, s93, s79\n"
"s_lshl4_add_u32 s94, s60, s79\n"
"s_lshl_b32 s94, s94, 10\n"
"s_add_u32 s94, s94, 524288\n"
"s_mul_i32 s99, s60, 3\n"
"s_lshr_b32 s78, s79, 1\n"
"s_add_u32 s99, s99, s78\n"
"s_lshr_b32 s78, s60, 1\n"
"s_add_u32 s78, s78, 12\n"
"s_cmp_eq_u32 s79, 6\n"
"s_cselect_b32 s99, s78, s99\n"
"s_mul_i32 s99, s99, 40960\n"
"s_xor_b32 s79, s79, s60\n"
"s_and_b32 s79, s79, 1\n"
"s_lshl_b32 s79, s79, 6\n"
"s_add_u32 s99, s99, s79\n"
"s_add_u32 s99, s99, 1024\n"
"s_cmp_eq_u32 s61, 7\n"
"s_cselect_b32 s99, 0x40000000, s99\n"
"s_cselect_b32 s94, 0x40000000, s94\n"
"s_waitcnt vmcnt(27)\n"
"v_and_b32 v12, 0x7fff0000, v12\n"
"v_and_b32 v13, 0x7fff0000, v13\n"
"s_nop 0\n"
"v_max_u32_dpp v12, v12, v12 quad_perm:[1,0,3,2] row_mask:0xf bank_mask:0xf\n"
"v_max_u32_dpp v13, v13, v13 quad_perm:[1,0,3,2] row_mask:0xf bank_mask:0xf\n"
"s_nop 0\n"
"v_max_u32_dpp v12, v12, v12 quad_perm:[2,3,0,1] row_mask:0xf bank_mask:0xf\n"
"v_max_u32_dpp v13, v13, v13 quad_perm:[2,3,0,1] row_mask:0xf bank_mask:0xf\n"
"s_memtime s[80:81]\n"
"s_memrealtime s[82:83]\n"
"s_nop 0\n"
"v_max_u32_dpp v12, v12, v12 row_half_mirror row_mask:0xf bank_mask:0xf\n"
"v_max_u32_dpp v13, v13, v13 row_half_mirror row_mask:0xf bank_mask:0xf\n"
"s_nop 0\n"
"v_min_u32_dpp v12, v12, v12 row_mirror row_mask:0xf bank_mask:0xf\n"
"v_min_u32_dpp v13, v13, v13 row_mirror row_mask:0xf bank_mask:0xf\n"
"s_nop 0\n"
"v_min_u32_dpp v12, v12, v12 row_bcast:15 row_mask:0xa bank_mask:0xf\n"
"v_min_u32_dpp v13, v13, v13 row_bcast:15 row_mask:0xa bank_mask:0xf\n"
"s_nop 0\n"
"v_min_u32_dpp v12, v12, v12 row_bcast:31 row_mask:0xc bank_mask:0xf\n"
"v_min_u32_dpp v13, v13, v13 row_bcast:31 row_mask:0xc bank_mask:0xf\n"
"s_nop 0\n"
"v_mul_f32 v12, v12, v13\n"
"v_mul_f32 v12, 0x43c80000, v12\n"
"v_min_f32 v12, 0x71800000, v12\n"
"v_mul_f32 v13, s46, v12\n"
"s_nop 0\n"
"v_readlane_b32 s70, v12, 63\n"
"v_readlane_b32 s71, v12, 63\n"
"v_readlane_b32 s69, v13, 63\n"
"s_nop 1\n"
"v_mov_b64 v[228:229], s[70:71]\n"
"v_mov_b64 v[230:231], s[70:71]\n"
"s_waitcnt vmcnt(26)\n"
"v_readfirstlane_b32 s92, v1\n"
"v_mov_b64 v[0:1], s[70:71]\n"
"v_mov_b64 v[2:3], s[70:71]\n"
"s_mov_b32 s70, s46\n"
"s_xor_b32 s71, s69, s77\n"
"s_waitcnt lgkmcnt(0)\n"
"s_sub_u32 s80, s80, s64\n"
"s_sub_u32 s82, s82, s66\n"
"s_mul_i32 s80, s80, 10\n"
"s_mul_i32 s82, s82, 211\n"
"s_cmp_gt_u32 s80, s82\n"
"s_cselect_b32 s64, 1, 0\n"
"s_cselect_b32 s68, 655360, 0\n"
"s_cmp_eq_u32 s42, 0\n"
"s_cselect_b32 s64, s64, 0\n"
"s_mov_b32 s65, 0\n"
"s_add_u32 s66, s66, 2800\n"
"s_mov_b32 s67, s66\n"
"s_mov_b32 s72, 118\n"
"s_mov_b64 vcc, s[64:65]\n"
"s_sub_u32 s63, s63, s44\n"
"s_add_u32 s80, s8, s63\n"
"s_addc_u32 s81, s9, 0\n"
"s_add_u32 s82, s44, 704\n"
"s_cmp_lg_u32 s68, 0\n"
"s_cselect_b32 s82, s82, 0\n"
"s_mov_b32 s83, 0x20000\n"
"s_nop 2\n"
"s_mul_i32 s60, s92, 0x9e3779b1\n"
"s_add_u32 s60, s60, 0x7f4a7c15\n"
"s_waitcnt vmcnt(15)\n"
"s_barrier\n"
"ds_read_b128 a[136:139], v246 offset:0\n"
"ds_read_b128 a[140:143], v247 offset:0\n"
"ds_read_b64_tr_b16 a[192:193], v244 offset:0\n"
"ds_read_b64_tr_b16 a[194:195], v244 offset:512\n"
"ds_read_b64_tr_b16 a[196:197], v244 offset:8192\n"
"ds_read_b64_tr_b16 a[198:199], v244 offset:8704\n"
"ds_read_b64_tr_b16 a[200:201], v245 offset:0\n"
"ds_read_b64_tr_b16 a[202:203], v245 offset:512\n"
"ds_read_b64_tr_b16 a[204:205], v245 offset:8192\n"
"ds_read_b64_tr_b16 a[206:207], v245 offset:8704\n"
"ds_read_b64_tr_b16 a[208:209], v244 offset:1024\n"
"ds_read_b64_tr_b16 a[210:211], v244 offset:1536\n"
"ds_read_b64_tr_b16 a[212:213], v244 offset:9216\n"
"ds_read_b64_tr_b16 a[214:215], v244 offset:9728\n"
"s_waitcnt lgkmcnt(10)\n"
"v_mfma_f32_16x16x32_bf16 v[112:115], a[192:195], a[136:139], v[228:231]\n"
"ds_read_b64_tr_b16 a[216:217], v245 offset:1024\n"
"ds_read_b64_tr_b16 a[218:219], v245 offset:1536\n"
"s_waitcnt lgkmcnt(10)\n"
"v_mfma_f32_16x16x32_bf16 v[112:115], a[196:199], a[140:143], v[112:115]\n"
"ds_read_b64_tr_b16 a[220:221], v245 offset:9216\n"
"ds_read_b64_tr_b16 a[222:223], v245 offset:9728\n"
"s_waitcnt lgkmcnt(10)\n"
"v_mfma_f32_16x16x32_bf16 v[116:119], a[200:203], a[136:139], v[228:231]\n"
"s_waitcnt lgkmcnt(8)\n"
"ds_read_b128 a[144:147], v246 offset:2048\n"
"v_mfma_f32_16x16x32_bf16 v[116:119], a[204:207], a[140:143], v[116:119]\n"
"s_waitcnt lgkmcnt(7)\n"
"ds_read_b128 a[148:151], v247 offset:2048\n"
"v_mfma_f32_16x16x32_bf16 v[120:123], a[208:211], a[136:139], v[228:231]\n"
"s_waitcnt lgkmcnt(6)\n"
"ds_read_b128 a[152:155], v246 offset:4096\n"
"v_mfma_f32_16x16x32_bf16 v[120:123], a[212:215], a[140:143], v[120:123]\n"
"s_waitcnt lgkmcnt(5)\n"
"ds_read_b128 a[156:159], v247 offset:4096\n"
"v_mfma_f32_16x16x32_bf16 v[124:127], a[216:219], a[136:139], v[228:231]\n"
"s_waitcnt lgkmcnt(4)\n"
"ds_read_b128 a[160:163], v252 offset:0\n"
"v_mfma_f32_16x16x32_bf16 v[124:127], a[220:223], a[140:143], v[124:127]\n"
"s_waitcnt lgkmcnt(4)\n"
"ds_read_b128 a[164:167], v253 offset:0\n"
"v_mfma_f32_16x16x32_bf16 v[140:143], a[216:219], a[144:147], v[228:231]\n"
"s_waitcnt lgkmcnt(4)\n"
"ds_read_b128 a[168:171], v250 offset:0\n"
"v_mfma_f32_16x16x32_bf16 v[140:143], a[220:223], a[148:151], v[140:143]\n"
"ds_read_b128 a[172:175], v251 offset:0\n"
"v_mfma_f32_16x16x32_bf16 v[136:139], a[208:211], a[144:147], v[228:231]\n"
"s_waitcnt vmcnt(11)\n"
"ds_read_b128 a[176:179], v250 offset:2048\n"
"v_mfma_f32_16x16x32_bf16 v[136:139], a[212:215], a[148:151], v[136:139]\n"
"s_barrier\n"
"v_mfma_f32_16x16x32_bf16 v[132:135], a[200:203], a[144:147], v[228:231]\n"
"s_add_u32 s40, s48, 256\n"
"s_xor_b32 m0, s50, 0xc000\n"
"ds_read_b128 a[180:183], v251 offset:2048\n"
"v_mfma_f32_16x16x32_bf16 v[132:135], a[204:207], a[148:151], v[132:135]\n"
"buffer_load_dwordx4 v224, s[28:31], s40 offen lds\n"
"v_mfma_f32_16x16x32_bf16 v[128:131], a[192:195], a[144:147], v[228:231]\n"
"ds_read_b128 a[184:187], v250 offset:4096\n"
"v_mfma_f32_16x16x32_bf16 v[128:131], a[196:199], a[148:151], v[128:131]\n"
"s_waitcnt lgkmcnt(5)\n"
"ds_read_b128 a[188:191], v251 offset:4096\n"
"v_mfma_f32_16x16x32_bf16 v[144:147], a[192:195], a[152:155], v[228:231]\n"
"ds_read_b64_tr_b16 a[224:225], v244 offset:16384\n"
"v_mfma_f32_16x16x32_bf16 v[144:147], a[196:199], a[156:159], v[144:147]\n"
"ds_read_b64_tr_b16 a[226:227], v244 offset:16896\n"
"v_mfma_f32_16x16x32_bf16 v[148:151], a[200:203], a[152:155], v[228:231]\n"
"ds_read_b64_tr_b16 a[228:229], v244 offset:24576\n"
"v_mfma_f32_16x16x32_bf16 v[148:151], a[204:207], a[156:159], v[148:151]\n"
"buffer_load_dwordx4 v225, s[28:31], s40 offen offset:1024 lds\n"
"v_mfma_f32_16x16x32_bf16 v[152:155], a[208:211], a[152:155], v[228:231]\n"
"ds_read_b64_tr_b16 a[230:231], v244 offset:25088\n"
"v_mfma_f32_16x16x32_bf16 v[152:155], a[212:215], a[156:159], v[152:155]\n"
"ds_read_b64_tr_b16 a[232:233], v245 offset:16384\n"
"v_mfma_f32_16x16x32_bf16 v[156:159], a[216:219], a[152:155], v[228:231]\n"
"ds_read_b64_tr_b16 a[234:235], v245 offset:16896\n"
"v_mfma_f32_16x16x32_bf16 v[156:159], a[220:223], a[156:159], v[156:159]\n"
"ds_read_b64_tr_b16 a[236:237], v245 offset:24576\n"
"v_mfma_f32_16x16x32_bf16 v[172:175], a[216:219], a[160:163], v[228:231]\n"
"ds_read_b64_tr_b16 a[238:239], v245 offset:25088\n"
"v_mfma_f32_16x16x32_bf16 v[172:175], a[220:223], a[164:167], v[172:175]\n"
"buffer_load_dwordx4 v226, s[28:31], s40 offen offset:2048 lds\n"
"v_mfma_f32_16x16x32_bf16 v[168:171], a[208:211], a[160:163], v[228:231]\n"
"ds_read_b64_tr_b16 a[240:241], v244 offset:17408\n"
"v_mfma_f32_16x16x32_bf16 v[168:171], a[212:215], a[164:167], v[168:171]\n"
"ds_read_b64_tr_b16 a[242:243], v244 offset:17920\n"
"v_mfma_f32_16x16x32_bf16 v[164:167], a[200:203], a[160:163], v[228:231]\n"
"ds_read_b64_tr_b16 a[244:245], v244 offset:25600\n"
"v_mfma_f32_16x16x32_bf16 v[164:167], a[204:207], a[164:167], v[164:167]\n"
"buffer_load_dwordx4 v227, s[28:31], s40 offen offset:3072 lds\n"
"v_mfma_f32_16x16x32_bf16 v[160:163], a[192:195], a[160:163], v[228:231]\n"
"s_cmp_eq_u32 s47, 8\n"
"ds_read_b64_tr_b16 a[246:247], v244 offset:26112\n"
"v_xor_b32 v244, s74, v244\n"
"v_mfma_f32_16x16x32_bf16 v[160:163], a[196:199], a[164:167], v[160:163]\n"
"s_waitcnt lgkmcnt(5)\n"
"s_cselect_b32 s41, s52, s58\n"
"ds_read_b64_tr_b16 a[248:249], v245 offset:17408\n"
"v_xor_b32 v246, s74, v246\n"
"v_mfma_f32_16x16x32_bf16 v[176:179], a[192:195], a[168:171], v[228:231]\n"
"s_add_u32 s48, s48, s41\n"
"ds_read_b64_tr_b16 a[250:251], v245 offset:17920\n"
"v_xor_b32 v247, s74, v247\n"
"v_mfma_f32_16x16x32_bf16 v[176:179], a[196:199], a[172:175], v[176:179]\n"
"s_sub_u32 s41, s48, 5242880\n"
"ds_read_b64_tr_b16 a[252:253], v245 offset:25600\n"
"v_xor_b32 v252, s74, v252\n"
"v_mfma_f32_16x16x32_bf16 v[180:183], a[200:203], a[168:171], v[228:231]\n"
"s_min_u32 s48, s48, s41\n"
"ds_read_b64_tr_b16 a[254:255], v245 offset:26112\n"
"v_xor_b32 v245, s74, v245\n"
"v_mfma_f32_16x16x32_bf16 v[180:183], a[204:207], a[172:175], v[180:183]\n"
"v_xor_b32 v253, s74, v253\n"
"v_xor_b32 v250, s74, v250\n"
"v_mfma_f32_16x16x32_bf16 v[184:187], a[208:211], a[168:171], v[228:231]\n"
"s_mov_b32 m0, s50\n"
"v_xor_b32 v251, s74, v251\n"
"v_mfma_f32_16x16x32_bf16 v[184:187], a[212:215], a[172:175], v[184:187]\n"
"buffer_load_dwordx4 v224, s[28:31], s48 offen lds\n"
"v_mfma_f32_16x16x32_bf16 v[188:191], a[216:219], a[168:171], v[228:231]\n"
"v_mfma_f32_16x16x32_bf16 v[188:191], a[220:223], a[172:175], v[188:191]\n"
"v_mfma_f32_16x16x32_bf16 v[204:207], a[216:219], a[176:179], v[228:231]\n"
"v_mfma_f32_16x16x32_bf16 v[204:207], a[220:223], a[180:183], v[204:207]\n"
"buffer_load_dwordx4 v225, s[28:31], s48 offen offset:1024 lds\n"
"v_mfma_f32_16x16x32_bf16 v[200:203], a[208:211], a[176:179], v[228:231]\n"
"v_mfma_f32_16x16x32_bf16 v[200:203], a[212:215], a[180:183], v[200:203]\n"
"v_mfma_f32_16x16x32_bf16 v[196:199], a[200:203], a[176:179], v[228:231]\n"
"v_mfma_f32_16x16x32_bf16 v[196:199], a[204:207], a[180:183], v[196:199]\n"
"v_mfma_f32_16x16x32_bf16 v[192:195], a[192:195], a[176:179], v[228:231]\n"
"v_mfma_f32_16x16x32_bf16 v[192:195], a[196:199], a[180:183], v[192:195]\n"
"buffer_load_dwordx4 v226, s[28:31], s48 offen offset:2048 lds\n"
"v_mfma_f32_16x16x32_bf16 v[208:211], a[192:195], a[184:187], v[228:231]\n"
"v_mfma_f32_16x16x32_bf16 v[208:211], a[196:199], a[188:191], v[208:211]\n"
"v_mfma_f32_16x16x32_bf16 v[212:215], a[200:203], a[184:187], v[228:231]\n"
"v_mfma_f32_16x16x32_bf16 v[212:215], a[204:207], a[188:191], v[212:215]\n"
"v_mfma_f32_16x16x32_bf16 v[216:219], a[208:211], a[184:187], v[228:231]\n"
"v_mfma_f32_16x16x32_bf16 v[216:219], a[212:215], a[188:191], v[216:219]\n"
"buffer_load_dwordx4 v227, s[28:31], s48 offen offset:3072 lds\n"
"v_mfma_f32_16x16x32_bf16 v[220:223], a[216:219], a[184:187], v[228:231]\n"
"s_xor_b32 s50, s50, s74\n"
"v_mfma_f32_16x16x32_bf16 v[220:223], a[220:223], a[188:191], v[220:223]\n"
"v_mfma_f32_16x16x32_bf16 v[0:3], a[224:227], a[136:139], v[228:231]\n"
"v_mfma_f32_16x16x32_bf16 v[0:3], a[228:231], a[140:143], v[0:3]\n"
"v_mfma_f32_16x16x32_bf16 v[4:7], a[232:235], a[136:139], v[228:231]\n"
"s_waitcnt vmcnt(8) lgkmcnt(0)\n"
"v_mfma_f32_16x16x32_bf16 v[4:7], a[236:239], a[140:143], v[4:7]\n"
"s_barrier\n"
"v_mfma_f32_16x16x32_bf16 v[8:11], a[240:243], a[136:139], v[228:231]\n"
"s_mov_b32 m0, s51\n"
"ds_read_b64_tr_b16 a[192:193], v244 offset:0\n"
"v_mfma_f32_16x16x32_bf16 v[8:11], a[244:247], a[140:143], v[8:11]\n"
"buffer_load_dwordx4 v248, s[24:27], s49 offen lds\n"
"v_mfma_f32_16x16x32_bf16 v[12:15], a[248:251], a[136:139], v[228:231]\n"
"ds_read_b64_tr_b16 a[194:195], v244 offset:512\n"
"v_mfma_f32_16x16x32_bf16 v[12:15], a[252:255], a[140:143], v[12:15]\n"
"ds_read_b128 a[136:139], v246 offset:0\n"
"v_mfma_f32_16x16x32_bf16 v[28:31], a[248:251], a[144:147], v[228:231]\n"
"ds_read_b64_tr_b16 a[196:197], v244 offset:8192\n"
"v_mfma_f32_16x16x32_bf16 v[28:31], a[252:255], a[148:151], v[28:31]\n"
"ds_read_b64_tr_b16 a[198:199], v244 offset:8704\n"
"v_mfma_f32_16x16x32_bf16 v[24:27], a[240:243], a[144:147], v[228:231]\n"
"ds_read_b128 a[140:143], v247 offset:0\n"
"v_mfma_f32_16x16x32_bf16 v[24:27], a[244:247], a[148:151], v[24:27]\n"
"buffer_load_dwordx4 v249, s[24:27], s49 offen offset:1024 lds\n"
"v_mfma_f32_16x16x32_bf16 v[20:23], a[232:235], a[144:147], v[228:231]\n"
"ds_read_b64_tr_b16 a[200:201], v245 offset:0\n"
"v_mfma_f32_16x16x32_bf16 v[20:23], a[236:239], a[148:151], v[20:23]\n"
"ds_read_b64_tr_b16 a[202:203], v245 offset:512\n"
"v_mfma_f32_16x16x32_bf16 v[16:19], a[224:227], a[144:147], v[228:231]\n"
"ds_read_b64_tr_b16 a[204:205], v245 offset:8192\n"
"v_mfma_f32_16x16x32_bf16 v[16:19], a[228:231], a[148:151], v[16:19]\n"
"ds_read_b64_tr_b16 a[206:207], v245 offset:8704\n"
"v_mfma_f32_16x16x32_bf16 v[32:35], a[224:227], a[152:155], v[228:231]\n"
"ds_read_b64_tr_b16 a[208:209], v244 offset:1024\n"
"v_mfma_f32_16x16x32_bf16 v[32:35], a[228:231], a[156:159], v[32:35]\n"
"buffer_load_dwordx4 v254, s[24:27], s49 offen offset:2048 lds\n"
"v_mfma_f32_16x16x32_bf16 v[36:39], a[232:235], a[152:155], v[228:231]\n"
"ds_read_b64_tr_b16 a[210:211], v244 offset:1536\n"
"v_mfma_f32_16x16x32_bf16 v[36:39], a[236:239], a[156:159], v[36:39]\n"
"ds_read_b64_tr_b16 a[212:213], v244 offset:9216\n"
"v_mfma_f32_16x16x32_bf16 v[40:43], a[240:243], a[152:155], v[228:231]\n"
"ds_read_b64_tr_b16 a[214:215], v244 offset:9728\n"
"v_mfma_f32_16x16x32_bf16 v[40:43], a[244:247], a[156:159], v[40:43]\n"
"ds_read_b64_tr_b16 a[216:217], v245 offset:1024\n"
"v_mfma_f32_16x16x32_bf16 v[44:47], a[248:251], a[152:155], v[228:231]\n"
"ds_read_b64_tr_b16 a[218:219], v245 offset:1536\n"
"v_mfma_f32_16x16x32_bf16 v[44:47], a[252:255], a[156:159], v[44:47]\n"
"buffer_load_dwordx4 v255, s[24:27], s49 offen offset:3072 lds\n"
"v_mfma_f32_16x16x32_bf16 v[60:63], a[248:251], a[160:163], v[228:231]\n"
"ds_read_b64_tr_b16 a[220:221], v245 offset:9216\n"
"v_mfma_f32_16x16x32_bf16 v[60:63], a[252:255], a[164:167], v[60:63]\n"
"ds_read_b64_tr_b16 a[222:223], v245 offset:9728\n"
"v_mfma_f32_16x16x32_bf16 v[56:59], a[240:243], a[160:163], v[228:231]\n"
"ds_read_b128 a[144:147], v246 offset:2048\n"
"v_mfma_f32_16x16x32_bf16 v[56:59], a[244:247], a[164:167], v[56:59]\n"
"s_add_u32 s40, s49, 40960\n"
"ds_read_b128 a[148:151], v247 offset:2048\n"
"v_mfma_f32_16x16x32_bf16 v[52:55], a[232:235], a[160:163], v[228:231]\n"
"s_add_u32 m0, s51, 4096\n"
"ds_read_b128 a[152:155], v246 offset:4096\n"
"v_mfma_f32_16x16x32_bf16 v[52:55], a[236:239], a[164:167], v[52:55]\n"
"buffer_load_dwordx4 v248, s[24:27], s40 offen lds\n"
"v_mfma_f32_16x16x32_bf16 v[48:51], a[224:227], a[160:163], v[228:231]\n"
"ds_read_b128 a[156:159], v247 offset:4096\n"
"v_mfma_f32_16x16x32_bf16 v[48:51], a[228:231], a[164:167], v[48:51]\n"
"ds_read_b128 a[160:163], v252 offset:0\n"
"v_mfma_f32_16x16x32_bf16 v[64:67], a[224:227], a[168:171], v[228:231]\n"
"ds_read_b128 a[164:167], v253 offset:0\n"
"v_mfma_f32_16x16x32_bf16 v[64:67], a[228:231], a[172:175], v[64:67]\n"
"v_mfma_f32_16x16x32_bf16 v[68:71], a[232:235], a[168:171], v[228:231]\n"
"v_mfma_f32_16x16x32_bf16 v[68:71], a[236:239], a[172:175], v[68:71]\n"
"buffer_load_dwordx4 v249, s[24:27], s40 offen offset:1024 lds\n"
"v_mfma_f32_16x16x32_bf16 v[72:75], a[240:243], a[168:171], v[228:231]\n"
"v_mfma_f32_16x16x32_bf16 v[72:75], a[244:247], a[172:175], v[72:75]\n"
"v_mfma_f32_16x16x32_bf16 v[76:79], a[248:251], a[168:171], v[228:231]\n"
"ds_read_b128 a[168:171], v250 offset:0\n"
"v_mfma_f32_16x16x32_bf16 v[76:79], a[252:255], a[172:175], v[76:79]\n"
"ds_read_b128 a[172:175], v251 offset:0\n"
"v_mfma_f32_16x16x32_bf16 v[92:95], a[248:251], a[176:179], v[228:231]\n"
"v_mfma_f32_16x16x32_bf16 v[92:95], a[252:255], a[180:183], v[92:95]\n"
"buffer_load_dwordx4 v254, s[24:27], s40 offen offset:2048 lds\n"
"v_mfma_f32_16x16x32_bf16 v[88:91], a[240:243], a[176:179], v[228:231]\n"
"s_add_u32 s49, s49, 128\n"
"v_mfma_f32_16x16x32_bf16 v[88:91], a[244:247], a[180:183], v[88:91]\n"
"s_sub_u32 s41, s49, 1280\n"
"v_mfma_f32_16x16x32_bf16 v[84:87], a[232:235], a[176:179], v[228:231]\n"
"s_min_u32 s49, s49, s41\n"
"v_mfma_f32_16x16x32_bf16 v[84:87], a[236:239], a[180:183], v[84:87]\n"
"s_xor_b32 s51, s51, s74\n"
"v_mfma_f32_16x16x32_bf16 v[80:83], a[224:227], a[176:179], v[228:231]\n"
"ds_read_b128 a[176:179], v250 offset:2048\n"
"v_mfma_f32_16x16x32_bf16 v[80:83], a[228:231], a[180:183], v[80:83]\n"
"ds_read_b128 a[180:183], v251 offset:2048\n"
"v_mfma_f32_16x16x32_bf16 v[96:99], a[224:227], a[184:187], v[228:231]\n"
"v_mfma_f32_16x16x32_bf16 v[96:99], a[228:231], a[188:191], v[96:99]\n"
"v_mfma_f32_16x16x32_bf16 v[100:103], a[232:235], a[184:187], v[228:231]\n"
"v_mfma_f32_16x16x32_bf16 v[100:103], a[236:239], a[188:191], v[100:103]\n"
"v_mfma_f32_16x16x32_bf16 v[104:107], a[240:243], a[184:187], v[228:231]\n"
"v_mfma_f32_16x16x32_bf16 v[104:107], a[244:247], a[188:191], v[104:107]\n"
"v_mfma_f32_16x16x32_bf16 v[108:111], a[248:251], a[184:187], v[228:231]\n"
"s_add_u32 s47, s47, 1\n"
"ds_read_b128 a[184:187], v250 offset:4096\n"
"v_mfma_f32_16x16x32_bf16 v[108:111], a[252:255], a[188:191], v[108:111]\n"
"s_waitcnt lgkmcnt(1)\n"
"ds_read_b128 a[188:191], v251 offset:4096\n"
"LPE_%=:\n"
"s_cmp_lg_u32 s68, 0\n"
"s_cbranch_scc1 LPN_%=\n"
"LPL_%=:\n"
"v_mfma_f32_16x16x32_bf16 v[112:115], a[192:195], a[136:139], v[112:115]\n"
"v_mfma_f32_16x16x32_bf16 v[112:115], a[196:199], a[140:143], v[112:115]\n"
"v_mfma_f32_16x16x32_bf16 v[116:119], a[200:203], a[136:139], v[116:119]\n"
"s_waitcnt vmcnt(11)\n"
"v_mfma_f32_16x16x32_bf16 v[116:119], a[204:207], a[140:143], v[116:119]\n"
"s_barrier\n"
"v_mfma_f32_16x16x32_bf16 v[120:123], a[208:211], a[136:139], v[120:123]\n"
"s_add_u32 s40, s48, 256\n"
"s_xor_b32 m0, s50, 0xc000\n"
"ds_read_b64_tr_b16 a[224:225], v244 offset:16384\n"
"v_mfma_f32_16x16x32_bf16 v[120:123], a[212:215], a[140:143], v[120:123]\n"
"buffer_load_dwordx4 v224, s[28:31], s40 offen lds\n"
"v_mfma_f32_16x16x32_bf16 v[124:127], a[216:219], a[136:139], v[124:127]\n"
"ds_read_b64_tr_b16 a[226:227], v244 offset:16896\n"
"v_mfma_f32_16x16x32_bf16 v[124:127], a[220:223], a[140:143], v[124:127]\n"
"ds_read_b64_tr_b16 a[228:229], v244 offset:24576\n"
"v_mfma_f32_16x16x32_bf16 v[140:143], a[216:219], a[144:147], v[140:143]\n"
"ds_read_b64_tr_b16 a[230:231], v244 offset:25088\n"
"v_mfma_f32_16x16x32_bf16 v[140:143], a[220:223], a[148:151], v[140:143]\n"
"ds_read_b64_tr_b16 a[232:233], v245 offset:16384\n"
"v_mfma_f32_16x16x32_bf16 v[136:139], a[208:211], a[144:147], v[136:139]\n"
"ds_read_b64_tr_b16 a[234:235], v245 offset:16896\n"
"v_mfma_f32_16x16x32_bf16 v[136:139], a[212:215], a[148:151], v[136:139]\n"
"buffer_load_dwordx4 v225, s[28:31], s40 offen offset:1024 lds\n"
"v_mfma_f32_16x16x32_bf16 v[132:135], a[200:203], a[144:147], v[132:135]\n"
"ds_read_b64_tr_b16 a[236:237], v245 offset:24576\n"
"v_mfma_f32_16x16x32_bf16 v[132:135], a[204:207], a[148:151], v[132:135]\n"
"ds_read_b64_tr_b16 a[238:239], v245 offset:25088\n"
"v_mfma_f32_16x16x32_bf16 v[128:131], a[192:195], a[144:147], v[128:131]\n"
"ds_read_b64_tr_b16 a[240:241], v244 offset:17408\n"
"v_mfma_f32_16x16x32_bf16 v[128:131], a[196:199], a[148:151], v[128:131]\n"
"ds_read_b64_tr_b16 a[242:243], v244 offset:17920\n"
"v_mfma_f32_16x16x32_bf16 v[144:147], a[192:195], a[152:155], v[144:147]\n"
"ds_read_b64_tr_b16 a[244:245], v244 offset:25600\n"
"v_mfma_f32_16x16x32_bf16 v[144:147], a[196:199], a[156:159], v[144:147]\n"
"buffer_load_dwordx4 v226, s[28:31], s40 offen offset:2048 lds\n"
"v_mfma_f32_16x16x32_bf16 v[148:151], a[200:203], a[152:155], v[148:151]\n"
"ds_read_b64_tr_b16 a[246:247], v244 offset:26112\n"
"v_mfma_f32_16x16x32_bf16 v[148:151], a[204:207], a[156:159], v[148:151]\n"
"ds_read_b64_tr_b16 a[248:249], v245 offset:17408\n"
"v_mfma_f32_16x16x32_bf16 v[152:155], a[208:211], a[152:155], v[152:155]\n"
"ds_read_b64_tr_b16 a[250:251], v245 offset:17920\n"
"v_mfma_f32_16x16x32_bf16 v[152:155], a[212:215], a[156:159], v[152:155]\n"
"ds_read_b64_tr_b16 a[252:253], v245 offset:25600\n"
"v_mfma_f32_16x16x32_bf16 v[156:159], a[216:219], a[152:155], v[156:159]\n"
"ds_read_b64_tr_b16 a[254:255], v245 offset:26112\n"
"v_mfma_f32_16x16x32_bf16 v[156:159], a[220:223], a[156:159], v[156:159]\n"
"buffer_load_dwordx4 v227, s[28:31], s40 offen offset:3072 lds\n"
"v_mfma_f32_16x16x32_bf16 v[172:175], a[216:219], a[160:163], v[172:175]\n"
"s_cmp_eq_u32 s47, 8\n"
"v_mfma_f32_16x16x32_bf16 v[172:175], a[220:223], a[164:167], v[172:175]\n"
"s_cselect_b32 s41, s52, s58\n"
"v_mfma_f32_16x16x32_bf16 v[168:171], a[208:211], a[160:163], v[168:171]\n"
"s_add_u32 s48, s48, s41\n"
"v_mfma_f32_16x16x32_bf16 v[168:171], a[212:215], a[164:167], v[168:171]\n"
"s_sub_u32 s41, s48, 5242880\n"
"v_mfma_f32_16x16x32_bf16 v[164:167], a[200:203], a[160:163], v[164:167]\n"
"s_min_u32 s48, s48, s41\n"
"v_mfma_f32_16x16x32_bf16 v[164:167], a[204:207], a[164:167], v[164:167]\n"
"v_xor_b32 v244, s74, v244\n"
"v_xor_b32 v246, s74, v246\n"
"v_mfma_f32_16x16x32_bf16 v[160:163], a[192:195], a[160:163], v[160:163]\n"
"s_mov_b32 m0, s50\n"
"v_xor_b32 v245, s74, v245\n"
"v_xor_b32 v247, s74, v247\n"
"v_mfma_f32_16x16x32_bf16 v[160:163], a[196:199], a[164:167], v[160:163]\n"
"buffer_load_dwordx4 v224, s[28:31], s48 offen lds\n"
"v_mfma_f32_16x16x32_bf16 v[176:179], a[192:195], a[168:171], v[176:179]\n"
"v_xor_b32 v252, s74, v252\n"
"v_xor_b32 v253, s74, v253\n"
"v_mfma_f32_16x16x32_bf16 v[176:179], a[196:199], a[172:175], v[176:179]\n"
"v_xor_b32 v250, s74, v250\n"
"v_xor_b32 v251, s74, v251\n"
"v_mfma_f32_16x16x32_bf16 v[180:183], a[200:203], a[168:171], v[180:183]\n"
"v_mfma_f32_16x16x32_bf16 v[180:183], a[204:207], a[172:175], v[180:183]\n"
"buffer_load_dwordx4 v225, s[28:31], s48 offen offset:1024 lds\n"
"v_mfma_f32_16x16x32_bf16 v[184:187], a[208:211], a[168:171], v[184:187]\n"
"v_mfma_f32_16x16x32_bf16 v[184:187], a[212:215], a[172:175], v[184:187]\n"
"v_mfma_f32_16x16x32_bf16 v[188:191], a[216:219], a[168:171], v[188:191]\n"
"v_mfma_f32_16x16x32_bf16 v[188:191], a[220:223], a[172:175], v[188:191]\n"
"v_mfma_f32_16x16x32_bf16 v[204:207], a[216:219], a[176:179], v[204:207]\n"
"v_mfma_f32_16x16x32_bf16 v[204:207], a[220:223], a[180:183], v[204:207]\n"
"buffer_load_dwordx4 v226, s[28:31], s48 offen offset:2048 lds\n"
"v_mfma_f32_16x16x32_bf16 v[200:203], a[208:211], a[176:179], v[200:203]\n"
"v_mfma_f32_16x16x32_bf16 v[200:203], a[212:215], a[180:183], v[200:203]\n"
"v_mfma_f32_16x16x32_bf16 v[196:199], a[200:203], a[176:179], v[196:199]\n"
"v_mfma_f32_16x16x32_bf16 v[196:199], a[204:207], a[180:183], v[196:199]\n"
"v_mfma_f32_16x16x32_bf16 v[192:195], a[192:195], a[176:179], v[192:195]\n"
"v_mfma_f32_16x16x32_bf16 v[192:195], a[196:199], a[180:183], v[192:195]\n"
"s_waitcnt lgkmcnt(0)\n"
"buffer_load_dwordx4 v227, s[28:31], s48 offen offset:3072 lds\n"
"v_mfma_f32_16x16x32_bf16 v[208:211], a[192:195], a[184:187], v[208:211]\n"
"s_xor_b32 s50, s50, s74\n"
"v_mfma_f32_16x16x32_bf16 v[208:211], a[196:199], a[188:191], v[208:211]\n"
"v_mfma_f32_16x16x32_bf16 v[212:215], a[200:203], a[184:187], v[212:215]\n"
"v_mfma_f32_16x16x32_bf16 v[212:215], a[204:207], a[188:191], v[212:215]\n"
"v_mfma_f32_16x16x32_bf16 v[216:219], a[208:211], a[184:187], v[216:219]\n"
"v_mfma_f32_16x16x32_bf16 v[216:219], a[212:215], a[188:191], v[216:219]\n"
"v_mfma_f32_16x16x32_bf16 v[220:223], a[216:219], a[184:187], v[220:223]\n"
"v_mfma_f32_16x16x32_bf16 v[220:223], a[220:223], a[188:191], v[220:223]\n"
"v_mfma_f32_16x16x32_bf16 v[0:3], a[224:227], a[136:139], v[0:3]\n"
"v_mfma_f32_16x16x32_bf16 v[0:3], a[228:231], a[140:143], v[0:3]\n"
"v_mfma_f32_16x16x32_bf16 v[4:7], a[232:235], a[136:139], v[4:7]\n"
"s_waitcnt vmcnt(8)\n"
"v_mfma_f32_16x16x32_bf16 v[4:7], a[236:239], a[140:143], v[4:7]\n"
"s_barrier\n"
"v_mfma_f32_16x16x32_bf16 v[8:11], a[240:243], a[136:139], v[8:11]\n"
"s_mov_b32 m0, s51\n"
"ds_read_b64_tr_b16 a[192:193], v244 offset:0\n"
"v_mfma_f32_16x16x32_bf16 v[8:11], a[244:247], a[140:143], v[8:11]\n"
"buffer_load_dwordx4 v248, s[24:27], s49 offen lds\n"
"v_mfma_f32_16x16x32_bf16 v[12:15], a[248:251], a[136:139], v[12:15]\n"
"ds_read_b64_tr_b16 a[194:195], v244 offset:512\n"
"v_mfma_f32_16x16x32_bf16 v[12:15], a[252:255], a[140:143], v[12:15]\n"
"ds_read_b128 a[136:139], v246 offset:0\n"
"v_mfma_f32_16x16x32_bf16 v[28:31], a[248:251], a[144:147], v[28:31]\n"
"ds_read_b64_tr_b16 a[196:197], v244 offset:8192\n"
"v_mfma_f32_16x16x32_bf16 v[28:31], a[252:255], a[148:151], v[28:31]\n"
"ds_read_b64_tr_b16 a[198:199], v244 offset:8704\n"
"v_mfma_f32_16x16x32_bf16 v[24:27], a[240:243], a[144:147], v[24:27]\n"
"ds_read_b128 a[140:143], v247 offset:0\n"
"v_mfma_f32_16x16x32_bf16 v[24:27], a[244:247], a[148:151], v[24:27]\n"
"buffer_load_dwordx4 v249, s[24:27], s49 offen offset:1024 lds\n"
"v_mfma_f32_16x16x32_bf16 v[20:23], a[232:235], a[144:147], v[20:23]\n"
"ds_read_b64_tr_b16 a[200:201], v245 offset:0\n"
"v_mfma_f32_16x16x32_bf16 v[20:23], a[236:239], a[148:151], v[20:23]\n"
"ds_read_b64_tr_b16 a[202:203], v245 offset:512\n"
"v_mfma_f32_16x16x32_bf16 v[16:19], a[224:227], a[144:147], v[16:19]\n"
"ds_read_b64_tr_b16 a[204:205], v245 offset:8192\n"
"v_mfma_f32_16x16x32_bf16 v[16:19], a[228:231], a[148:151], v[16:19]\n"
"ds_read_b64_tr_b16 a[206:207], v245 offset:8704\n"
"v_mfma_f32_16x16x32_bf16 v[32:35], a[224:227], a[152:155], v[32:35]\n"
"ds_read_b64_tr_b16 a[208:209], v244 offset:1024\n"
"v_mfma_f32_16x16x32_bf16 v[32:35], a[228:231], a[156:159], v[32:35]\n"
"buffer_load_dwordx4 v254, s[24:27], s49 offen offset:2048 lds\n"
"v_mfma_f32_16x16x32_bf16 v[36:39], a[232:235], a[152:155], v[36:39]\n"
"ds_read_b64_tr_b16 a[210:211], v244 offset:1536\n"
"v_mfma_f32_16x16x32_bf16 v[36:39], a[236:239], a[156:159], v[36:39]\n"
"ds_read_b64_tr_b16 a[212:213], v244 offset:9216\n"
"v_mfma_f32_16x16x32_bf16 v[40:43], a[240:243], a[152:155], v[40:43]\n"
"ds_read_b64_tr_b16 a[214:215], v244 offset:9728\n"
"v_mfma_f32_16x16x32_bf16 v[40:43], a[244:247], a[156:159], v[40:43]\n"
"ds_read_b64_tr_b16 a[216:217], v245 offset:1024\n"
"v_mfma_f32_16x16x32_bf16 v[44:47], a[248:251], a[152:155], v[44:47]\n"
"ds_read_b64_tr_b16 a[218:219], v245 offset:1536\n"
"v_mfma_f32_16x16x32_bf16 v[44:47], a[252:255], a[156:159], v[44:47]\n"
"buffer_load_dwordx4 v255, s[24:27], s49 offen offset:3072 lds\n"
"v_mfma_f32_16x16x32_bf16 v[60:63], a[248:251], a[160:163], v[60:63]\n"
"ds_read_b64_tr_b16 a[220:221], v245 offset:9216\n"
"v_mfma_f32_16x16x32_bf16 v[60:63], a[252:255], a[164:167], v[60:63]\n"
"ds_read_b64_tr_b16 a[222:223], v245 offset:9728\n"
"v_mfma_f32_16x16x32_bf16 v[56:59], a[240:243], a[160:163], v[56:59]\n"
"ds_read_b128 a[144:147], v246 offset:2048\n"
"v_mfma_f32_16x16x32_bf16 v[56:59], a[244:247], a[164:167], v[56:59]\n"
"s_add_u32 s40, s49, 40960\n"
"ds_read_b128 a[148:151], v247 offset:2048\n"
"v_mfma_f32_16x16x32_bf16 v[52:55], a[232:235], a[160:163], v[52:55]\n"
"s_add_u32 m0, s51, 4096\n"
"ds_read_b128 a[152:155], v246 offset:4096\n"
"v_mfma_f32_16x16x32_bf16 v[52:55], a[236:239], a[164:167], v[52:55]\n"
"buffer_load_dwordx4 v248, s[24:27], s40 offen lds\n"
"v_mfma_f32_16x16x32_bf16 v[48:51], a[224:227], a[160:163], v[48:51]\n"
"ds_read_b128 a[156:159], v247 offset:4096\n"
"v_mfma_f32_16x16x32_bf16 v[48:51], a[228:231], a[164:167], v[48:51]\n"
"ds_read_b128 a[160:163], v252 offset:0\n"
"v_mfma_f32_16x16x32_bf16 v[64:67], a[224:227], a[168:171], v[64:67]\n"
"ds_read_b128 a[164:167], v253 offset:0\n"
"v_mfma_f32_16x16x32_bf16 v[64:67], a[228:231], a[172:175], v[64:67]\n"
"v_mfma_f32_16x16x32_bf16 v[68:71], a[232:235], a[168:171], v[68:71]\n"
"v_mfma_f32_16x16x32_bf16 v[68:71], a[236:239], a[172:175], v[68:71]\n"
"buffer_load_dwordx4 v249, s[24:27], s40 offen offset:1024 lds\n"
"v_mfma_f32_16x16x32_bf16 v[72:75], a[240:243], a[168:171], v[72:75]\n"
"v_mfma_f32_16x16x32_bf16 v[72:75], a[244:247], a[172:175], v[72:75]\n"
"v_mfma_f32_16x16x32_bf16 v[76:79], a[248:251], a[168:171], v[76:79]\n"
"ds_read_b128 a[168:171], v250 offset:0\n"
"v_mfma_f32_16x16x32_bf16 v[76:79], a[252:255], a[172:175], v[76:79]\n"
"ds_read_b128 a[172:175], v251 offset:0\n"
"v_mfma_f32_16x16x32_bf16 v[92:95], a[248:251], a[176:179], v[92:95]\n"
"v_mfma_f32_16x16x32_bf16 v[92:95], a[252:255], a[180:183], v[92:95]\n"
"buffer_load_dwordx4 v254, s[24:27], s40 offen offset:2048 lds\n"
"v_mfma_f32_16x16x32_bf16 v[88:91], a[240:243], a[176:179], v[88:91]\n"
"s_add_u32 s49, s49, 128\n"
"v_mfma_f32_16x16x32_bf16 v[88:91], a[244:247], a[180:183], v[88:91]\n"
"s_sub_u32 s41, s49, 1280\n"
"v_mfma_f32_16x16x32_bf16 v[84:87], a[232:235], a[176:179], v[84:87]\n"
"s_min_u32 s49, s49, s41\n"
"v_mfma_f32_16x16x32_bf16 v[84:87], a[236:239], a[180:183], v[84:87]\n"
"s_xor_b32 s51, s51, s74\n"
"v_mfma_f32_16x16x32_bf16 v[80:83], a[224:227], a[176:179], v[80:83]\n"
"ds_read_b128 a[176:179], v250 offset:2048\n"
"v_mfma_f32_16x16x32_bf16 v[80:83], a[228:231], a[180:183], v[80:83]\n"
"ds_read_b128 a[180:183], v251 offset:2048\n"
"v_mfma_f32_16x16x32_bf16 v[96:99], a[224:227], a[184:187], v[96:99]\n"
"v_mfma_f32_16x16x32_bf16 v[96:99], a[228:231], a[188:191], v[96:99]\n"
"v_mfma_f32_16x16x32_bf16 v[100:103], a[232:235], a[184:187], v[100:103]\n"
"v_mfma_f32_16x16x32_bf16 v[100:103], a[236:239], a[188:191], v[100:103]\n"
"v_mfma_f32_16x16x32_bf16 v[104:107], a[240:243], a[184:187], v[104:107]\n"
"v_mfma_f32_16x16x32_bf16 v[104:107], a[244:247], a[188:191], v[104:107]\n"
"v_mfma_f32_16x16x32_bf16 v[108:111], a[248:251], a[184:187], v[108:111]\n"
"s_add_u32 s47, s47, 1\n"
"ds_read_b128 a[184:187], v250 offset:4096\n"
"v_mfma_f32_16x16x32_bf16 v[108:111], a[252:255], a[188:191], v[108:111]\n"
"s_waitcnt lgkmcnt(1)\n"
"s_cmp_lg_u32 s47, s62\n"
"ds_read_b128 a[188:191], v251 offset:4096\n"
"s_cbranch_scc1 LPL_%=\n"
"LPX_%=:\n"
"s_cmp_eq_u32 s47, 18\n"
"s_cbranch_scc1 LPT2_%=\n"
"v_mfma_f32_16x16x32_bf16 v[112:115], a[192:195], a[136:139], v[112:115]\n"
"s_add_u32 s78, s96, 8192\n"
"v_mov_b32 v241, 1\n"
"v_mfma_f32_16x16x32_bf16 v[112:115], a[196:199], a[140:143], v[112:115]\n"
"s_mov_b64 exec, 1\n"
"buffer_store_dword v241, off, s[32:35], s96 sc0 sc1\n"
"buffer_store_dword v241, off, s[32:35], s78 sc0 sc1\n"
"s_mov_b64 exec, -1\n"
"v_mfma_f32_16x16x32_bf16 v[116:119], a[200:203], a[136:139], v[116:119]\n"
"s_waitcnt vmcnt(13)\n"
"v_mfma_f32_16x16x32_bf16 v[116:119], a[204:207], a[140:143], v[116:119]\n"
"s_barrier\n"
"v_mfma_f32_16x16x32_bf16 v[120:123], a[208:211], a[136:139], v[120:123]\n"
"s_add_u32 s40, s48, 256\n"
"s_xor_b32 m0, s50, 0xc000\n"
"ds_read_b64_tr_b16 a[238:239], v245 offset:25088\n"
"v_mfma_f32_16x16x32_bf16 v[120:123], a[212:215], a[140:143], v[120:123]\n"
"buffer_load_dwordx4 v224, s[28:31], s40 offen lds\n"
"v_mfma_f32_16x16x32_bf16 v[124:127], a[216:219], a[136:139], v[124:127]\n"
"v_mfma_f32_16x16x32_bf16 v[124:127], a[220:223], a[140:143], v[124:127]\n"
"v_mfma_f32_16x16x32_bf16 v[188:191], a[216:219], a[168:171], v[188:191]\n"
"ds_read_b64_tr_b16 a[254:255], v245 offset:26112\n"
"v_mfma_f32_16x16x32_bf16 v[188:191], a[220:223], a[172:175], v[188:191]\n"
"ds_read_b64_tr_b16 a[252:253], v245 offset:25600\n"
"v_mfma_f32_16x16x32_bf16 v[184:187], a[208:211], a[168:171], v[184:187]\n"
"ds_read_b64_tr_b16 a[224:225], v244 offset:16384\n"
"ds_read_b64_tr_b16 a[228:229], v244 offset:24576\n"
"v_mfma_f32_16x16x32_bf16 v[184:187], a[212:215], a[172:175], v[184:187]\n"
"buffer_load_dwordx4 v225, s[28:31], s40 offen offset:1024 lds\n"
"v_mfma_f32_16x16x32_bf16 v[180:183], a[200:203], a[168:171], v[180:183]\n"
"v_mfma_f32_16x16x32_bf16 v[180:183], a[204:207], a[172:175], v[180:183]\n"
"v_mfma_f32_16x16x32_bf16 v[176:179], a[192:195], a[168:171], v[176:179]\n"
"ds_read_b64_tr_b16 a[230:231], v244 offset:25088\n"
"v_mfma_f32_16x16x32_bf16 v[176:179], a[196:199], a[172:175], v[176:179]\n"
"ds_read_b64_tr_b16 a[244:245], v244 offset:25600\n"
"v_mfma_f32_16x16x32_bf16 v[192:195], a[192:195], a[176:179], v[192:195]\n"
"ds_read_b64_tr_b16 a[226:227], v244 offset:16896\n"
"ds_read_b64_tr_b16 a[250:251], v245 offset:17920\n"
"v_mfma_f32_16x16x32_bf16 v[192:195], a[196:199], a[180:183], v[192:195]\n"
"buffer_load_dwordx4 v226, s[28:31], s40 offen offset:2048 lds\n"
"v_mfma_f32_16x16x32_bf16 v[196:199], a[200:203], a[176:179], v[196:199]\n"
"v_mfma_f32_16x16x32_bf16 v[196:199], a[204:207], a[180:183], v[196:199]\n"
"v_mfma_f32_16x16x32_bf16 v[200:203], a[208:211], a[176:179], v[200:203]\n"
"ds_read_b64_tr_b16 a[236:237], v245 offset:24576\n"
"ds_read_b64_tr_b16 a[246:247], v244 offset:26112\n"
"v_mfma_f32_16x16x32_bf16 v[200:203], a[212:215], a[180:183], v[200:203]\n"
"ds_read_b64_tr_b16 a[240:241], v244 offset:17408\n"
"v_mfma_f32_16x16x32_bf16 v[204:207], a[216:219], a[176:179], v[204:207]\n"
"ds_read_b64_tr_b16 a[242:243], v244 offset:17920\n"
"ds_read_b64_tr_b16 a[248:249], v245 offset:17408\n"
"v_mfma_f32_16x16x32_bf16 v[204:207], a[220:223], a[180:183], v[204:207]\n"
"s_waitcnt lgkmcnt(7)\n"
"buffer_load_dwordx4 v227, s[28:31], s40 offen offset:3072 lds\n"
"v_mfma_f32_16x16x32_bf16 v[220:223], a[216:219], a[184:187], v[220:223]\n"
"s_cmp_eq_u32 s47, 8\n"
"v_mfma_f32_16x16x32_bf16 v[220:223], a[220:223], a[188:191], v[220:223]\n"
"s_cselect_b32 s41, s52, s58\n"
"s_nop 1\n"
"v_pk_fma_f32 v[176:177], v[176:177], s[70:71], s[70:71] op_sel:[0,0,1] op_sel_hi:[1,0,1]\nv_pk_fma_f32 v[178:179], v[178:179], s[70:71], s[70:71] op_sel:[0,0,1] op_sel_hi:[1,0,1]\nv_pk_fma_f32 v[180:181], v[180:181], s[70:71], s[70:71] op_sel:[0,0,1] op_sel_hi:[1,0,1]\nv_pk_fma_f32 v[182:183], v[182:183], s[70:71], s[70:71] op_sel:[0,0,1] op_sel_hi:[1,0,1]\nv_pk_fma_f32 v[184:185], v[184:185], s[70:71], s[70:71] op_sel:[0,0,1] op_sel_hi:[1,0,1]\nv_pk_fma_f32 v[186:187], v[186:187], s[70:71], s[70:71] op_sel:[0,0,1] op_sel_hi:[1,0,1]\nv_pk_fma_f32 v[188:189], v[188:189], s[70:71], s[70:71] op_sel:[0,0,1] op_sel_hi:[1,0,1]\nv_pk_fma_f32 v[190:191], v[190:191], s[70:71], s[70:71] op_sel:[0,0,1] op_sel_hi:[1,0,1]\nv_pk_fma_f32 v[192:193], v[192:193], s[70:71], s[70:71] op_sel:[0,0,1] op_sel_hi:[1,0,1]\nv_pk_fma_f32 v[194:195], v[194:195], s[70:71], s[70:71] op_sel:[0,0,1] op_sel_hi:[1,0,1]\nv_pk_fma_f32 v[196:197], v[196:197], s[70:71], s[70:71] op_sel:[0,0,1] op_sel_hi:[1,0,1]\nv_pk_fma_f32 v[198:199], v[198:199], s[70:71], s[70:71] op_sel:[0,0,1] op_sel_hi:[1,0,1]\nv_pk_fma_f32 v[200:201], v[200:201], s[70:71], s[70:71] op_sel:[0,0,1] op_sel_hi:[1,0,1]\nv_pk_fma_f32 v[202:203], v[202:203], s[70:71], s[70:71] op_sel:[0,0,1] op_sel_hi:[1,0,1]\nv_pk_fma_f32 v[204:205], v[204:205], s[70:71], s[70:71] op_sel:[0,0,1] op_sel_hi:[1,0,1]\nv_pk_fma_f32 v[206:207], v[206:207], s[70:71], s[70:71] op_sel:[0,0,1] op_sel_hi:[1,0,1]\n"
"v_mfma_f32_16x16x32_bf16 v[216:219], a[208:211], a[184:187], v[216:219]\n"
"s_add_u32 s48, s48, s41\n"
"v_cvt_pk_bf16_f32 v192, v192, v193\n"
"v_cvt_pk_bf16_f32 v193, v194, v195\n"
"v_mfma_f32_16x16x32_bf16 v[216:219], a[212:215], a[188:191], v[216:219]\n"
"s_sub_u32 s41, s48, 5242880\n"
"v_cvt_pk_bf16_f32 v176, v176, v177\n"
"v_cvt_pk_bf16_f32 v177, v178, v179\n"
"v_mfma_f32_16x16x32_bf16 v[212:215], a[200:203], a[184:187], v[212:215]\n"
"s_min_u32 s48, s48, s41\n"
"v_cvt_pk_bf16_f32 v178, v180, v181\n"
"v_cvt_pk_bf16_f32 v179, v182, v183\n"
"v_mfma_f32_16x16x32_bf16 v[212:215], a[204:207], a[188:191], v[212:215]\n"
"ds_read_b64_tr_b16 a[234:235], v245 offset:16896\n"
"v_xor_b32 v246, s74, v246\n"
"v_mfma_f32_16x16x32_bf16 v[208:211], a[192:195], a[184:187], v[208:211]\n"
"s_mov_b32 m0, s50\n"
"v_and_b32 v182, 0xf0, v242\n"
"v_cvt_pk_bf16_f32 v200, v200, v201\n"
"v_mfma_f32_16x16x32_bf16 v[208:211], a[196:199], a[188:191], v[208:211]\n"
"buffer_load_dwordx4 v224, s[28:31], s48 offen lds\n"
"v_mfma_f32_16x16x32_bf16 v[160:163], a[192:195], a[160:163], v[160:163]\n"
"v_bfe_u32 v180, v242, 9, 1\n"
"v_lshl_add_u32 v180, v180, 8, v182\n"
"v_mfma_f32_16x16x32_bf16 v[160:163], a[196:199], a[164:167], v[160:163]\n"
"v_bfe_u32 v182, v242, 8, 1\n"
"v_cvt_pk_bf16_f32 v184, v184, v185\n"
"v_mfma_f32_16x16x32_bf16 v[164:167], a[200:203], a[160:163], v[164:167]\n"
"ds_read_b64_tr_b16 a[232:233], v245 offset:16384\n"
"v_xor_b32 v247, s74, v247\n"
"v_mfma_f32_16x16x32_bf16 v[164:167], a[204:207], a[164:167], v[164:167]\n"
"buffer_load_dwordx4 v225, s[28:31], s48 offen offset:1024 lds\n"
"v_mfma_f32_16x16x32_bf16 v[168:171], a[208:211], a[160:163], v[168:171]\n"
"v_cvt_pk_bf16_f32 v201, v202, v203\n"
"v_xor_b32 v253, s74, v253\n"
"v_mfma_f32_16x16x32_bf16 v[168:171], a[212:215], a[164:167], v[168:171]\n"
"v_cvt_pk_bf16_f32 v194, v196, v197\n"
"v_xor_b32 v244, s74, v244\n"
"v_mfma_f32_16x16x32_bf16 v[172:175], a[216:219], a[160:163], v[172:175]\n"
"v_lshl_add_u32 v180, v182, 3, v180\n"
"v_cvt_pk_bf16_f32 v185, v186, v187\n"
"v_mfma_f32_16x16x32_bf16 v[172:175], a[220:223], a[164:167], v[172:175]\n"
"v_cvt_pk_bf16_f32 v186, v188, v189\n"
"v_cvt_pk_bf16_f32 v187, v190, v191\n"
"v_mfma_f32_16x16x32_bf16 v[140:143], a[216:219], a[144:147], v[140:143]\n"
"v_cvt_pk_bf16_f32 v203, v206, v207\n"
"v_bfe_u32 v181, v242, 9, 1\n"
"v_mfma_f32_16x16x32_bf16 v[140:143], a[220:223], a[148:151], v[140:143]\n"
"buffer_load_dwordx4 v226, s[28:31], s48 offen offset:2048 lds\n"
"v_mfma_f32_16x16x32_bf16 v[136:139], a[208:211], a[144:147], v[136:139]\n"
"v_xor_b32_e64 v182, s42, 1\n"
"v_lshl_add_u32 v180, v182, 10, v180\n"
"v_mfma_f32_16x16x32_bf16 v[136:139], a[212:215], a[148:151], v[136:139]\n"
"v_cvt_pk_bf16_f32 v202, v204, v205\n"
"v_xor_b32 v252, s74, v252\n"
"v_mfma_f32_16x16x32_bf16 v[132:135], a[200:203], a[144:147], v[132:135]\n"
"v_add_u32 v180, 94208, v180\n"
"v_and_b32 v182, 0xf0, v242\n"
"v_mfma_f32_16x16x32_bf16 v[132:135], a[204:207], a[148:151], v[132:135]\n"
"ds_write2st64_b64 v180, v[184:185], v[186:187] offset0:64 offset1:65\n"
"v_pk_fma_f32 v[208:209], v[208:209], s[70:71], s[70:71] op_sel:[0,0,1] op_sel_hi:[1,0,1]\nv_pk_fma_f32 v[210:211], v[210:211], s[70:71], s[70:71] op_sel:[0,0,1] op_sel_hi:[1,0,1]\nv_pk_fma_f32 v[212:213], v[212:213], s[70:71], s[70:71] op_sel:[0,0,1] op_sel_hi:[1,0,1]\nv_pk_fma_f32 v[214:215], v[214:215], s[70:71], s[70:71] op_sel:[0,0,1] op_sel_hi:[1,0,1]\nv_pk_fma_f32 v[216:217], v[216:217], s[70:71], s[70:71] op_sel:[0,0,1] op_sel_hi:[1,0,1]\nv_pk_fma_f32 v[218:219], v[218:219], s[70:71], s[70:71] op_sel:[0,0,1] op_sel_hi:[1,0,1]\nv_pk_fma_f32 v[220:221], v[220:221], s[70:71], s[70:71] op_sel:[0,0,1] op_sel_hi:[1,0,1]\nv_pk_fma_f32 v[222:223], v[222:223], s[70:71], s[70:71] op_sel:[0,0,1] op_sel_hi:[1,0,1]\nv_pk_fma_f32 v[160:161], v[160:161], s[70:71], s[70:71] op_sel:[0,0,1] op_sel_hi:[1,0,1]\nv_pk_fma_f32 v[162:163], v[162:163], s[70:71], s[70:71] op_sel:[0,0,1] op_sel_hi:[1,0,1]\nv_pk_fma_f32 v[164:165], v[164:165], s[70:71], s[70:71] op_sel:[0,0,1] op_sel_hi:[1,0,1]\nv_pk_fma_f32 v[166:167], v[166:167], s[70:71], s[70:71] op_sel:[0,0,1] op_sel_hi:[1,0,1]\nv_pk_fma_f32 v[168:169], v[168:169], s[70:71], s[70:71] op_sel:[0,0,1] op_sel_hi:[1,0,1]\nv_pk_fma_f32 v[170:171], v[170:171], s[70:71], s[70:71] op_sel:[0,0,1] op_sel_hi:[1,0,1]\nv_pk_fma_f32 v[172:173], v[172:173], s[70:71], s[70:71] op_sel:[0,0,1] op_sel_hi:[1,0,1]\nv_pk_fma_f32 v[174:175], v[174:175], s[70:71], s[70:71] op_sel:[0,0,1] op_sel_hi:[1,0,1]\n"
"v_cvt_pk_bf16_f32 v216, v216, v217\n"
"v_mfma_f32_16x16x32_bf16 v[128:131], a[192:195], a[144:147], v[128:131]\n"
"v_cvt_pk_bf16_f32 v168, v168, v169\n"
"v_xor_b32 v250, s74, v250\n"
"v_mfma_f32_16x16x32_bf16 v[128:131], a[196:199], a[148:151], v[128:131]\n"
"buffer_load_dwordx4 v227, s[28:31], s48 offen offset:3072 lds\n"
"v_mfma_f32_16x16x32_bf16 v[144:147], a[192:195], a[152:155], v[144:147]\n"
"s_xor_b32 s50, s50, s74\n"
"v_cvt_pk_bf16_f32 v208, v208, v209\n"
"v_xor_b32 v251, s74, v251\n"
"v_mfma_f32_16x16x32_bf16 v[144:147], a[196:199], a[156:159], v[144:147]\n"
"v_cvt_pk_bf16_f32 v217, v218, v219\n"
"v_cvt_pk_bf16_f32 v169, v170, v171\n"
"v_mfma_f32_16x16x32_bf16 v[148:151], a[200:203], a[152:155], v[148:151]\n"
"v_cvt_pk_bf16_f32 v195, v198, v199\n"
"v_cvt_pk_bf16_f32 v209, v210, v211\n"
"v_mfma_f32_16x16x32_bf16 v[148:151], a[204:207], a[156:159], v[148:151]\n"
"ds_write2st64_b64 v180, v[192:193], v[194:195] offset0:88 offset1:89\n"
"v_pk_fma_f32 v[112:113], v[112:113], s[70:71], s[70:71] op_sel:[0,0,1] op_sel_hi:[1,0,1]\nv_pk_fma_f32 v[114:115], v[114:115], s[70:71], s[70:71] op_sel:[0,0,1] op_sel_hi:[1,0,1]\nv_pk_fma_f32 v[116:117], v[116:117], s[70:71], s[70:71] op_sel:[0,0,1] op_sel_hi:[1,0,1]\nv_pk_fma_f32 v[118:119], v[118:119], s[70:71], s[70:71] op_sel:[0,0,1] op_sel_hi:[1,0,1]\nv_pk_fma_f32 v[120:121], v[120:121], s[70:71], s[70:71] op_sel:[0,0,1] op_sel_hi:[1,0,1]\nv_pk_fma_f32 v[122:123], v[122:123], s[70:71], s[70:71] op_sel:[0,0,1] op_sel_hi:[1,0,1]\nv_pk_fma_f32 v[124:125], v[124:125], s[70:71], s[70:71] op_sel:[0,0,1] op_sel_hi:[1,0,1]\nv_pk_fma_f32 v[126:127], v[126:127], s[70:71], s[70:71] op_sel:[0,0,1] op_sel_hi:[1,0,1]\nv_pk_fma_f32 v[128:129], v[128:129], s[70:71], s[70:71] op_sel:[0,0,1] op_sel_hi:[1,0,1]\nv_pk_fma_f32 v[130:131], v[130:131], s[70:71], s[70:71] op_sel:[0,0,1] op_sel_hi:[1,0,1]\nv_pk_fma_f32 v[132:133], v[132:133], s[70:71], s[70:71] op_sel:[0,0,1] op_sel_hi:[1,0,1]\nv_pk_fma_f32 v[134:135], v[134:135], s[70:71], s[70:71] op_sel:[0,0,1] op_sel_hi:[1,0,1]\nv_pk_fma_f32 v[136:137], v[136:137], s[70:71], s[70:71] op_sel:[0,0,1] op_sel_hi:[1,0,1]\nv_pk_fma_f32 v[138:139], v[138:139], s[70:71], s[70:71] op_sel:[0,0,1] op_sel_hi:[1,0,1]\nv_pk_fma_f32 v[140:141], v[140:141], s[70:71], s[70:71] op_sel:[0,0,1] op_sel_hi:[1,0,1]\nv_pk_fma_f32 v[142:143], v[142:143], s[70:71], s[70:71] op_sel:[0,0,1] op_sel_hi:[1,0,1]\n"
"v_mfma_f32_16x16x32_bf16 v[152:155], a[208:211], a[152:155], v[152:155]\n"
"v_cvt_pk_bf16_f32 v218, v220, v221\n"
"v_cvt_pk_bf16_f32 v136, v136, v137\n"
"v_mfma_f32_16x16x32_bf16 v[152:155], a[212:215], a[156:159], v[152:155]\n"
"v_cvt_pk_bf16_f32 v137, v138, v139\n"
"v_cvt_pk_bf16_f32 v139, v142, v143\n"
"v_mfma_f32_16x16x32_bf16 v[156:159], a[216:219], a[152:155], v[156:159]\n"
"v_cvt_pk_bf16_f32 v128, v128, v129\n"
"v_cvt_pk_bf16_f32 v138, v140, v141\n"
"v_mfma_f32_16x16x32_bf16 v[156:159], a[220:223], a[156:159], v[156:159]\n"
"s_waitcnt lgkmcnt(1)\n"
"v_cvt_pk_bf16_f32 v112, v112, v113\n"
"v_xor_b32 v245, s74, v245\n"
"v_mfma_f32_16x16x32_bf16 v[96:99], a[224:227], a[184:187], v[96:99]\n"
"v_cvt_pk_bf16_f32 v120, v120, v121\n"
"v_cvt_pk_bf16_f32 v129, v130, v131\n"
"v_mfma_f32_16x16x32_bf16 v[96:99], a[228:231], a[188:191], v[96:99]\n"
"ds_write2st64_b64 v180, v[200:201], v[202:203] offset0:96 offset1:97\n"
"v_cvt_pk_bf16_f32 v219, v222, v223\n"
"v_cvt_pk_bf16_f32 v170, v172, v173\n"
"v_cvt_pk_bf16_f32 v131, v134, v135\n"
"v_lshl_add_u32 v181, v181, 8, v182\n"
"v_bfe_u32 v182, v242, 8, 1\n"
"v_lshl_add_u32 v181, v182, 9, v181\n"
"v_mfma_f32_16x16x32_bf16 v[100:103], a[232:235], a[184:187], v[100:103]\n"
"s_waitcnt vmcnt(10)\n"
"v_permlane16_swap_b32 v136, v138\n"
"v_mfma_f32_16x16x32_bf16 v[100:103], a[236:239], a[188:191], v[100:103]\n"
"s_barrier\n"
"ds_write2st64_b64 v180, v[216:217], v[218:219] offset0:128 offset1:129\n"
"v_pk_fma_f32 v[144:145], v[144:145], s[70:71], s[70:71] op_sel:[0,0,1] op_sel_hi:[1,0,1]\nv_pk_fma_f32 v[146:147], v[146:147], s[70:71], s[70:71] op_sel:[0,0,1] op_sel_hi:[1,0,1]\nv_pk_fma_f32 v[148:149], v[148:149], s[70:71], s[70:71] op_sel:[0,0,1] op_sel_hi:[1,0,1]\nv_pk_fma_f32 v[150:151], v[150:151], s[70:71], s[70:71] op_sel:[0,0,1] op_sel_hi:[1,0,1]\nv_pk_fma_f32 v[152:153], v[152:153], s[70:71], s[70:71] op_sel:[0,0,1] op_sel_hi:[1,0,1]\nv_pk_fma_f32 v[154:155], v[154:155], s[70:71], s[70:71] op_sel:[0,0,1] op_sel_hi:[1,0,1]\nv_pk_fma_f32 v[156:157], v[156:157], s[70:71], s[70:71] op_sel:[0,0,1] op_sel_hi:[1,0,1]\nv_pk_fma_f32 v[158:159], v[158:159], s[70:71], s[70:71] op_sel:[0,0,1] op_sel_hi:[1,0,1]\n"
"v_cvt_pk_bf16_f32 v144, v144, v145\n"
"v_mfma_f32_16x16x32_bf16 v[104:107], a[240:243], a[184:187], v[104:107]\n"
"s_mov_b32 m0, s51\n"
"v_cvt_pk_bf16_f32 v171, v174, v175\n"
"v_cvt_pk_bf16_f32 v121, v122, v123\n"
"v_mfma_f32_16x16x32_bf16 v[104:107], a[244:247], a[188:191], v[104:107]\n"
"buffer_load_dwordx4 v248, s[24:27], s49 offen lds\n"
"v_mfma_f32_16x16x32_bf16 v[108:111], a[248:251], a[184:187], v[108:111]\n"
"v_xor_b32_e64 v182, s42, 0\n"
"v_lshl_add_u32 v181, v182, 10, v181\n"
"v_mfma_f32_16x16x32_bf16 v[108:111], a[252:255], a[188:191], v[108:111]\n"
"ds_read_b64_tr_b16 a[218:219], v245 offset:1536\n"
"v_cvt_pk_bf16_f32 v211, v214, v215\n"
"v_mfma_f32_16x16x32_bf16 v[92:95], a[248:251], a[176:179], v[92:95]\n"
"v_cvt_pk_bf16_f32 v160, v160, v161\n"
"v_cvt_pk_bf16_f32 v145, v146, v147\n"
"v_mfma_f32_16x16x32_bf16 v[92:95], a[252:255], a[180:183], v[92:95]\n"
"ds_write2st64_b64 v180, v[176:177], v[178:179] offset0:0 offset1:1\n"
"v_cvt_pk_bf16_f32 v122, v124, v125\n"
"v_cvt_pk_bf16_f32 v123, v126, v127\n"
"v_cvt_pk_bf16_f32 v146, v148, v149\n"
"v_cvt_pk_bf16_f32 v152, v152, v153\n"
"v_cvt_pk_bf16_f32 v153, v154, v155\n"
"v_cvt_pk_bf16_f32 v155, v158, v159\n"
"v_mfma_f32_16x16x32_bf16 v[88:91], a[240:243], a[176:179], v[88:91]\n"
"v_cvt_pk_bf16_f32 v210, v212, v213\n"
"v_cvt_pk_bf16_f32 v113, v114, v115\n"
"v_mfma_f32_16x16x32_bf16 v[88:91], a[244:247], a[180:183], v[88:91]\n"
"buffer_load_dwordx4 v249, s[24:27], s49 offen offset:1024 lds\n"
"v_mfma_f32_16x16x32_bf16 v[84:87], a[232:235], a[176:179], v[84:87]\n"
"v_cvt_pk_bf16_f32 v161, v162, v163\n"
"v_cvt_pk_bf16_f32 v154, v156, v157\n"
"v_mfma_f32_16x16x32_bf16 v[84:87], a[236:239], a[180:183], v[84:87]\n"
"s_nop 0\n"
"v_permlane16_swap_b32 v152, v154\n"
"v_mfma_f32_16x16x32_bf16 v[80:83], a[224:227], a[176:179], v[80:83]\n"
"ds_read_b64_tr_b16 a[196:197], v244 offset:8192\n"
"ds_read_b64_tr_b16 a[214:215], v244 offset:9728\n"
"v_mfma_f32_16x16x32_bf16 v[80:83], a[228:231], a[180:183], v[80:83]\n"
"ds_write2st64_b64 v180, v[208:209], v[210:211] offset0:120 offset1:121\n"
"v_cvt_pk_bf16_f32 v114, v116, v117\n"
"v_permlane16_swap_b32 v137, v139\n"
"v_permlane16_swap_b32 v144, v146\n"
"v_mfma_f32_16x16x32_bf16 v[64:67], a[224:227], a[168:171], v[64:67]\n"
"v_permlane16_swap_b32 v121, v123\n"
"v_mfma_f32_16x16x32_bf16 v[64:67], a[228:231], a[172:175], v[64:67]\n"
"buffer_load_dwordx4 v254, s[24:27], s49 offen offset:2048 lds\n"
"v_mfma_f32_16x16x32_bf16 v[68:71], a[232:235], a[168:171], v[68:71]\n"
"v_permlane16_swap_b32 v120, v122\n"
"v_mfma_f32_16x16x32_bf16 v[68:71], a[236:239], a[172:175], v[68:71]\n"
"v_cvt_pk_bf16_f32 v115, v118, v119\n"
"v_accvgpr_write_b32 a24, v136\n"
"v_mfma_f32_16x16x32_bf16 v[72:75], a[240:243], a[168:171], v[72:75]\n"
"ds_read_b64_tr_b16 a[206:207], v245 offset:8704\n"
"v_add_u32 v181, 94208, v181\n"
"v_mfma_f32_16x16x32_bf16 v[72:75], a[244:247], a[172:175], v[72:75]\n"
"ds_read_b64_tr_b16 a[216:217], v245 offset:1024\n"
"v_permlane16_swap_b32 v112, v114\n"
"v_permlane16_swap_b32 v153, v155\n"
"v_pk_fma_f32 v[96:97], v[96:97], s[70:71], s[70:71] op_sel:[0,0,1] op_sel_hi:[1,0,1]\nv_pk_fma_f32 v[98:99], v[98:99], s[70:71], s[70:71] op_sel:[0,0,1] op_sel_hi:[1,0,1]\nv_pk_fma_f32 v[100:101], v[100:101], s[70:71], s[70:71] op_sel:[0,0,1] op_sel_hi:[1,0,1]\nv_pk_fma_f32 v[102:103], v[102:103], s[70:71], s[70:71] op_sel:[0,0,1] op_sel_hi:[1,0,1]\nv_pk_fma_f32 v[104:105], v[104:105], s[70:71], s[70:71] op_sel:[0,0,1] op_sel_hi:[1,0,1]\nv_pk_fma_f32 v[106:107], v[106:107], s[70:71], s[70:71] op_sel:[0,0,1] op_sel_hi:[1,0,1]\nv_pk_fma_f32 v[108:109], v[108:109], s[70:71], s[70:71] op_sel:[0,0,1] op_sel_hi:[1,0,1]\nv_pk_fma_f32 v[110:111], v[110:111], s[70:71], s[70:71] op_sel:[0,0,1] op_sel_hi:[1,0,1]\nv_pk_fma_f32 v[80:81], v[80:81], s[70:71], s[70:71] op_sel:[0,0,1] op_sel_hi:[1,0,1]\nv_pk_fma_f32 v[82:83], v[82:83], s[70:71], s[70:71] op_sel:[0,0,1] op_sel_hi:[1,0,1]\nv_pk_fma_f32 v[84:85], v[84:85], s[70:71], s[70:71] op_sel:[0,0,1] op_sel_hi:[1,0,1]\nv_pk_fma_f32 v[86:87], v[86:87], s[70:71], s[70:71] op_sel:[0,0,1] op_sel_hi:[1,0,1]\nv_pk_fma_f32 v[88:89], v[88:89], s[70:71], s[70:71] op_sel:[0,0,1] op_sel_hi:[1,0,1]\nv_pk_fma_f32 v[90:91], v[90:91], s[70:71], s[70:71] op_sel:[0,0,1] op_sel_hi:[1,0,1]\nv_pk_fma_f32 v[92:93], v[92:93], s[70:71], s[70:71] op_sel:[0,0,1] op_sel_hi:[1,0,1]\nv_pk_fma_f32 v[94:95], v[94:95], s[70:71], s[70:71] op_sel:[0,0,1] op_sel_hi:[1,0,1]\n"
"v_mfma_f32_16x16x32_bf16 v[76:79], a[248:251], a[168:171], v[76:79]\n"
"ds_read_b64_tr_b16 a[208:209], v244 offset:1024\n"
"v_cvt_pk_bf16_f32 v80, v80, v81\n"
"v_mfma_f32_16x16x32_bf16 v[76:79], a[252:255], a[172:175], v[76:79]\n"
"buffer_load_dwordx4 v255, s[24:27], s49 offen offset:3072 lds\n"
"v_mfma_f32_16x16x32_bf16 v[60:63], a[248:251], a[160:163], v[60:63]\n"
"ds_read_b64_tr_b16 a[192:193], v244 offset:0\n"
"ds_read_b64_tr_b16 a[222:223], v245 offset:9728\n"
"v_mfma_f32_16x16x32_bf16 v[60:63], a[252:255], a[164:167], v[60:63]\n"
"v_permlane16_swap_b32 v168, v170\n"
"v_mfma_f32_16x16x32_bf16 v[56:59], a[240:243], a[160:163], v[56:59]\n"
"v_permlane16_swap_b32 v113, v115\n"
"v_mfma_f32_16x16x32_bf16 v[56:59], a[244:247], a[164:167], v[56:59]\n"
"s_add_u32 s40, s49, 40960\n"
"ds_read_b64_tr_b16 a[210:211], v244 offset:1536\n"
"v_add_u32 v182, s75, v181\n"
"v_mfma_f32_16x16x32_bf16 v[52:55], a[232:235], a[160:163], v[52:55]\n"
"s_add_u32 m0, s51, 4096\n"
"v_cvt_pk_bf16_f32 v130, v132, v133\n"
"v_accvgpr_write_b32 a25, v137\n"
"v_mfma_f32_16x16x32_bf16 v[52:55], a[236:239], a[164:167], v[52:55]\n"
"buffer_load_dwordx4 v248, s[24:27], s40 offen lds\n"
"v_mfma_f32_16x16x32_bf16 v[48:51], a[224:227], a[160:163], v[48:51]\n"
"s_waitcnt vmcnt(63) lgkmcnt(3)\n"
"v_permlane16_swap_b32 v129, v131\n"
"v_mfma_f32_16x16x32_bf16 v[48:51], a[228:231], a[164:167], v[48:51]\n"
"s_barrier\n"
"v_mfma_f32_16x16x32_bf16 v[0:3], a[224:227], a[136:139], v[0:3]\n"
"ds_read_b128 a[36:39], v181 offset:61440\n"
"v_accvgpr_write_b32 a43, v155\n"
"v_mfma_f32_16x16x32_bf16 v[0:3], a[228:231], a[140:143], v[0:3]\n"
"ds_read_b128 a[28:31], v181 offset:49152\n"
"v_accvgpr_write_b32 a10, v122\n"
"v_mfma_f32_16x16x32_bf16 v[4:7], a[232:235], a[136:139], v[4:7]\n"
"v_cvt_pk_bf16_f32 v96, v96, v97\n"
"v_cvt_pk_bf16_f32 v104, v104, v105\n"
"v_mfma_f32_16x16x32_bf16 v[4:7], a[236:239], a[140:143], v[4:7]\n"
"buffer_load_dwordx4 v249, s[24:27], s40 offen offset:1024 lds\n"
"v_mfma_f32_16x16x32_bf16 v[8:11], a[240:243], a[136:139], v[8:11]\n"
"v_cvt_pk_bf16_f32 v97, v98, v99\n"
"v_cvt_pk_bf16_f32 v99, v102, v103\n"
"v_mfma_f32_16x16x32_bf16 v[8:11], a[244:247], a[140:143], v[8:11]\n"
"ds_read_b64_tr_b16 a[220:221], v245 offset:9216\n"
"ds_read_b128 a[12:15], v181 offset:32768\n"
"v_mfma_f32_16x16x32_bf16 v[12:15], a[248:251], a[136:139], v[12:15]\n"
"ds_read_b128 a[164:167], v253 offset:0\n"
"v_accvgpr_write_b32 a32, v144\n"
"v_mfma_f32_16x16x32_bf16 v[12:15], a[252:255], a[140:143], v[12:15]\n"
"ds_read_b128 a[140:143], v247 offset:0\n"
"v_accvgpr_write_b32 a3, v115\n"
"v_mfma_f32_16x16x32_bf16 v[28:31], a[248:251], a[144:147], v[28:31]\n"
"ds_read_b64_tr_b16 a[194:195], v244 offset:512\n"
"v_cvt_pk_bf16_f32 v162, v164, v165\n"
"v_mfma_f32_16x16x32_bf16 v[28:31], a[252:255], a[148:151], v[28:31]\n"
"buffer_load_dwordx4 v254, s[24:27], s40 offen offset:2048 lds\n"
"v_mfma_f32_16x16x32_bf16 v[24:27], a[240:243], a[144:147], v[24:27]\n"
"s_add_u32 s49, s49, 128\n"
"v_cvt_pk_bf16_f32 v147, v150, v151\n"
"v_accvgpr_write_b32 a41, v153\n"
"v_mfma_f32_16x16x32_bf16 v[24:27], a[244:247], a[148:151], v[24:27]\n"
"s_sub_u32 s41, s49, 1280\n"
"v_cvt_pk_bf16_f32 v98, v100, v101\n"
"v_cvt_pk_bf16_f32 v88, v88, v89\n"
"v_mfma_f32_16x16x32_bf16 v[20:23], a[232:235], a[144:147], v[20:23]\n"
"s_min_u32 s49, s49, s41\n"
"ds_read_b128 a[136:139], v246 offset:0\n"
"v_accvgpr_write_b32 a19, v131\n"
"v_mfma_f32_16x16x32_bf16 v[20:23], a[236:239], a[148:151], v[20:23]\n"
"s_xor_b32 s51, s51, s74\n"
"ds_read_b64_tr_b16 a[198:199], v244 offset:8704\n"
"ds_read_b128 a[44:47], v182 offset:0\n"
"v_mfma_f32_16x16x32_bf16 v[16:19], a[224:227], a[144:147], v[16:19]\n"
"ds_read_b64_tr_b16 a[212:213], v244 offset:9216\n"
"v_cvt_pk_bf16_f32 v163, v166, v167\n"
"v_mfma_f32_16x16x32_bf16 v[16:19], a[228:231], a[148:151], v[16:19]\n"
"ds_read_b128 a[148:151], v247 offset:2048\n"
"v_accvgpr_write_b32 a11, v123\n"
"v_mfma_f32_16x16x32_bf16 v[32:35], a[224:227], a[152:155], v[32:35]\n"
"ds_read_b128 a[4:7], v181 offset:0\n"
"v_and_b32 v102, 0xf0, v242\n"
"v_mfma_f32_16x16x32_bf16 v[32:35], a[228:231], a[156:159], v[32:35]\n"
"ds_read_b64_tr_b16 a[202:203], v245 offset:512\n"
"ds_read_b64_tr_b16 a[204:205], v245 offset:8192\n"
"v_mfma_f32_16x16x32_bf16 v[36:39], a[232:235], a[152:155], v[36:39]\n"
"ds_read_b64_tr_b16 a[200:201], v245 offset:0\n"
"v_cvt_pk_bf16_f32 v89, v90, v91\n"
"v_mfma_f32_16x16x32_bf16 v[36:39], a[236:239], a[156:159], v[36:39]\n"
"ds_read_b128 a[20:23], v181 offset:45056\n"
"v_accvgpr_write_b32 a27, v139\n"
"v_mfma_f32_16x16x32_bf16 v[40:43], a[240:243], a[152:155], v[40:43]\n"
"v_accvgpr_write_b32 a0, v112\n"
"v_accvgpr_write_b32 a1, v113\n"
"v_mfma_f32_16x16x32_bf16 v[40:43], a[244:247], a[156:159], v[40:43]\n"
"ds_read_b128 a[180:183], v251 offset:2048\n"
"v_accvgpr_write_b32 a2, v114\n"
"v_mfma_f32_16x16x32_bf16 v[44:47], a[248:251], a[152:155], v[44:47]\n"
"s_add_u32 s47, s47, 1\n"
"ds_read_b128 a[144:147], v246 offset:2048\n"
"v_accvgpr_write_b32 a17, v129\n"
"v_mfma_f32_16x16x32_bf16 v[44:47], a[252:255], a[156:159], v[44:47]\n"
"s_waitcnt lgkmcnt(7)\n"
"ds_read_b128 a[160:163], v252 offset:0\n"
"v_permlane16_swap_b32 v161, v163\n"
"v_permlane16_swap_b32 v169, v171\n"
"v_accvgpr_write_b32 a9, v121\n"
"v_permlane16_swap_b32 v128, v130\n"
"v_permlane16_swap_b32 v145, v147\n"
"v_accvgpr_write_b32 a42, v154\n"
"v_bfe_u32 v100, v242, 9, 1\n"
"v_cvt_pk_bf16_f32 v91, v94, v95\n"
"v_pk_fma_f32 v[0:1], v[0:1], s[70:71], s[70:71] op_sel:[0,0,1] op_sel_hi:[1,0,1]\nv_pk_fma_f32 v[2:3], v[2:3], s[70:71], s[70:71] op_sel:[0,0,1] op_sel_hi:[1,0,1]\nv_pk_fma_f32 v[4:5], v[4:5], s[70:71], s[70:71] op_sel:[0,0,1] op_sel_hi:[1,0,1]\nv_pk_fma_f32 v[6:7], v[6:7], s[70:71], s[70:71] op_sel:[0,0,1] op_sel_hi:[1,0,1]\nv_pk_fma_f32 v[8:9], v[8:9], s[70:71], s[70:71] op_sel:[0,0,1] op_sel_hi:[1,0,1]\nv_pk_fma_f32 v[10:11], v[10:11], s[70:71], s[70:71] op_sel:[0,0,1] op_sel_hi:[1,0,1]\nv_pk_fma_f32 v[12:13], v[12:13], s[70:71], s[70:71] op_sel:[0,0,1] op_sel_hi:[1,0,1]\nv_pk_fma_f32 v[14:15], v[14:15], s[70:71], s[70:71] op_sel:[0,0,1] op_sel_hi:[1,0,1]\nv_pk_fma_f32 v[16:17], v[16:17], s[70:71], s[70:71] op_sel:[0,0,1] op_sel_hi:[1,0,1]\nv_pk_fma_f32 v[18:19], v[18:19], s[70:71], s[70:71] op_sel:[0,0,1] op_sel_hi:[1,0,1]\nv_pk_fma_f32 v[20:21], v[20:21], s[70:71], s[70:71] op_sel:[0,0,1] op_sel_hi:[1,0,1]\nv_pk_fma_f32 v[22:23], v[22:23], s[70:71], s[70:71] op_sel:[0,0,1] op_sel_hi:[1,0,1]\nv_pk_fma_f32 v[24:25], v[24:25], s[70:71], s[70:71] op_sel:[0,0,1] op_sel_hi:[1,0,1]\nv_pk_fma_f32 v[26:27], v[26:27], s[70:71], s[70:71] op_sel:[0,0,1] op_sel_hi:[1,0,1]\nv_pk_fma_f32 v[28:29], v[28:29], s[70:71], s[70:71] op_sel:[0,0,1] op_sel_hi:[1,0,1]\nv_pk_fma_f32 v[30:31], v[30:31], s[70:71], s[70:71] op_sel:[0,0,1] op_sel_hi:[1,0,1]\n"
"v_cvt_pk_bf16_f32 v16, v16, v17\n"
"v_cvt_pk_bf16_f32 v24, v24, v25\n"
"v_cvt_pk_bf16_f32 v25, v26, v27\n"
"v_mfma_f32_16x16x32_bf16 v[112:115], a[192:195], a[136:139], v[228:231]\n"
"v_cvt_pk_bf16_f32 v0, v0, v1\n"
"v_cvt_pk_bf16_f32 v8, v8, v9\n"
"v_mfma_f32_16x16x32_bf16 v[112:115], a[196:199], a[140:143], v[112:115]\n"
"s_waitcnt lgkmcnt(4)\n"
"ds_write_b128 v243, v[168:171] offset:2048\n"
"v_permlane16_swap_b32 v160, v162\n"
"v_accvgpr_write_b32 a8, v120\n"
"v_cvt_pk_bf16_f32 v81, v82, v83\n"
"v_pk_fma_f32 v[64:65], v[64:65], s[70:71], s[70:71] op_sel:[0,0,1] op_sel_hi:[1,0,1]\nv_pk_fma_f32 v[66:67], v[66:67], s[70:71], s[70:71] op_sel:[0,0,1] op_sel_hi:[1,0,1]\nv_pk_fma_f32 v[68:69], v[68:69], s[70:71], s[70:71] op_sel:[0,0,1] op_sel_hi:[1,0,1]\nv_pk_fma_f32 v[70:71], v[70:71], s[70:71], s[70:71] op_sel:[0,0,1] op_sel_hi:[1,0,1]\nv_pk_fma_f32 v[72:73], v[72:73], s[70:71], s[70:71] op_sel:[0,0,1] op_sel_hi:[1,0,1]\nv_pk_fma_f32 v[74:75], v[74:75], s[70:71], s[70:71] op_sel:[0,0,1] op_sel_hi:[1,0,1]\nv_pk_fma_f32 v[76:77], v[76:77], s[70:71], s[70:71] op_sel:[0,0,1] op_sel_hi:[1,0,1]\nv_pk_fma_f32 v[78:79], v[78:79], s[70:71], s[70:71] op_sel:[0,0,1] op_sel_hi:[1,0,1]\nv_pk_fma_f32 v[48:49], v[48:49], s[70:71], s[70:71] op_sel:[0,0,1] op_sel_hi:[1,0,1]\nv_pk_fma_f32 v[50:51], v[50:51], s[70:71], s[70:71] op_sel:[0,0,1] op_sel_hi:[1,0,1]\nv_pk_fma_f32 v[52:53], v[52:53], s[70:71], s[70:71] op_sel:[0,0,1] op_sel_hi:[1,0,1]\nv_pk_fma_f32 v[54:55], v[54:55], s[70:71], s[70:71] op_sel:[0,0,1] op_sel_hi:[1,0,1]\nv_pk_fma_f32 v[56:57], v[56:57], s[70:71], s[70:71] op_sel:[0,0,1] op_sel_hi:[1,0,1]\nv_pk_fma_f32 v[58:59], v[58:59], s[70:71], s[70:71] op_sel:[0,0,1] op_sel_hi:[1,0,1]\nv_pk_fma_f32 v[60:61], v[60:61], s[70:71], s[70:71] op_sel:[0,0,1] op_sel_hi:[1,0,1]\nv_pk_fma_f32 v[62:63], v[62:63], s[70:71], s[70:71] op_sel:[0,0,1] op_sel_hi:[1,0,1]\n"
"v_cvt_pk_bf16_f32 v64, v64, v65\n"
"v_mfma_f32_16x16x32_bf16 v[116:119], a[200:203], a[136:139], v[228:231]\n"
"s_waitcnt vmcnt(11) lgkmcnt(4)\n"
"v_accvgpr_write_b32 a18, v130\n"
"v_accvgpr_write_b32 a26, v138\n"
"v_mfma_f32_16x16x32_bf16 v[116:119], a[204:207], a[140:143], v[116:119]\n"
"s_barrier\n"
"ds_write_b128 v243, v[160:163] offset:0\n"
"v_accvgpr_write_b32 a16, v128\n"
"v_accvgpr_write_b32 a33, v145\n"
"v_cvt_pk_bf16_f32 v1, v2, v3\n"
"v_pk_fma_f32 v[32:33], v[32:33], s[70:71], s[70:71] op_sel:[0,0,1] op_sel_hi:[1,0,1]\nv_pk_fma_f32 v[34:35], v[34:35], s[70:71], s[70:71] op_sel:[0,0,1] op_sel_hi:[1,0,1]\nv_pk_fma_f32 v[36:37], v[36:37], s[70:71], s[70:71] op_sel:[0,0,1] op_sel_hi:[1,0,1]\nv_pk_fma_f32 v[38:39], v[38:39], s[70:71], s[70:71] op_sel:[0,0,1] op_sel_hi:[1,0,1]\nv_pk_fma_f32 v[40:41], v[40:41], s[70:71], s[70:71] op_sel:[0,0,1] op_sel_hi:[1,0,1]\nv_pk_fma_f32 v[42:43], v[42:43], s[70:71], s[70:71] op_sel:[0,0,1] op_sel_hi:[1,0,1]\nv_pk_fma_f32 v[44:45], v[44:45], s[70:71], s[70:71] op_sel:[0,0,1] op_sel_hi:[1,0,1]\nv_pk_fma_f32 v[46:47], v[46:47], s[70:71], s[70:71] op_sel:[0,0,1] op_sel_hi:[1,0,1]\n"
"v_cvt_pk_bf16_f32 v32, v32, v33\n"
"v_mfma_f32_16x16x32_bf16 v[120:123], a[208:211], a[136:139], v[228:231]\n"
"s_add_u32 s40, s48, 256\n"
"s_xor_b32 m0, s50, 0xc000\n"
"ds_read_b128 a[152:155], v246 offset:4096\n"
"v_mfma_f32_16x16x32_bf16 v[120:123], a[212:215], a[140:143], v[120:123]\n"
"buffer_load_dwordx4 v224, s[28:31], s40 offen lds\n"
"v_mfma_f32_16x16x32_bf16 v[124:127], a[216:219], a[136:139], v[228:231]\n"
"v_accvgpr_write_b32 a40, v152\n"
"v_cvt_pk_bf16_f32 v40, v40, v41\n"
"v_mfma_f32_16x16x32_bf16 v[124:127], a[220:223], a[140:143], v[124:127]\n"
"s_waitcnt lgkmcnt(3)\n"
"ds_read_b128 a[168:171], v250 offset:0\n"
"v_cvt_pk_bf16_f32 v105, v106, v107\n"
"v_mfma_f32_16x16x32_bf16 v[140:143], a[216:219], a[144:147], v[228:231]\n"
"ds_read_b128 a[156:159], v247 offset:4096\n"
"v_accvgpr_write_b32 a34, v146\n"
"v_mfma_f32_16x16x32_bf16 v[140:143], a[220:223], a[148:151], v[140:143]\n"
"ds_read_b128 a[172:175], v251 offset:0\n"
"v_cvt_pk_bf16_f32 v2, v4, v5\n"
"v_mfma_f32_16x16x32_bf16 v[136:139], a[208:211], a[144:147], v[228:231]\n"
"ds_read_b64_tr_b16 a[224:225], v244 offset:16384\n"
"v_lshl_add_u32 v100, v100, 8, v102\n"
"v_mfma_f32_16x16x32_bf16 v[136:139], a[212:215], a[148:151], v[136:139]\n"
"buffer_load_dwordx4 v225, s[28:31], s40 offen offset:1024 lds\n"
"v_mfma_f32_16x16x32_bf16 v[132:135], a[200:203], a[144:147], v[228:231]\n"
"v_bfe_u32 v102, v242, 8, 1\n"
"v_cvt_pk_bf16_f32 v41, v42, v43\n"
"v_mfma_f32_16x16x32_bf16 v[132:135], a[204:207], a[148:151], v[132:135]\n"
"v_accvgpr_write_b32 a35, v147\n"
"v_cvt_pk_bf16_f32 v65, v66, v67\n"
"v_mfma_f32_16x16x32_bf16 v[128:131], a[192:195], a[144:147], v[228:231]\n"
"ds_read_b64_tr_b16 a[234:235], v245 offset:16896\n"
"v_cvt_pk_bf16_f32 v72, v72, v73\n"
"v_mfma_f32_16x16x32_bf16 v[128:131], a[196:199], a[148:151], v[128:131]\n"
"s_waitcnt lgkmcnt(4)\n"
"ds_read_b64_tr_b16 a[236:237], v245 offset:24576\n"
"v_bfe_u32 v101, v242, 9, 1\n"
"v_mfma_f32_16x16x32_bf16 v[144:147], a[192:195], a[152:155], v[228:231]\n"
"s_waitcnt lgkmcnt(4)\n"
"ds_read_b64_tr_b16 a[226:227], v244 offset:16896\n"
"v_cvt_pk_bf16_f32 v9, v10, v11\n"
"v_mfma_f32_16x16x32_bf16 v[144:147], a[196:199], a[156:159], v[144:147]\n"
"buffer_load_dwordx4 v226, s[28:31], s40 offen offset:2048 lds\n"
"v_mfma_f32_16x16x32_bf16 v[148:151], a[200:203], a[152:155], v[228:231]\n"
"v_cvt_pk_bf16_f32 v3, v6, v7\n"
"v_cvt_pk_bf16_f32 v17, v18, v19\n"
"v_mfma_f32_16x16x32_bf16 v[148:151], a[204:207], a[156:159], v[148:151]\n"
"v_cvt_pk_bf16_f32 v66, v68, v69\n"
"v_cvt_pk_bf16_f32 v73, v74, v75\n"
"v_mfma_f32_16x16x32_bf16 v[152:155], a[208:211], a[152:155], v[228:231]\n"
"ds_read_b64_tr_b16 a[252:253], v245 offset:25600\n"
"v_lshl_add_u32 v100, v102, 3, v100\n"
"v_mfma_f32_16x16x32_bf16 v[152:155], a[212:215], a[156:159], v[152:155]\n"
"ds_read_b64_tr_b16 a[244:245], v244 offset:25600\n"
"ds_read_b64_tr_b16 a[250:251], v245 offset:17920\n"
"v_mfma_f32_16x16x32_bf16 v[156:159], a[216:219], a[152:155], v[228:231]\n"
"v_cvt_pk_bf16_f32 v48, v48, v49\n"
"v_cvt_pk_bf16_f32 v43, v46, v47\n"
"v_mfma_f32_16x16x32_bf16 v[156:159], a[220:223], a[156:159], v[156:159]\n"
"buffer_load_dwordx4 v227, s[28:31], s40 offen offset:3072 lds\n"
"v_mfma_f32_16x16x32_bf16 v[172:175], a[216:219], a[160:163], v[228:231]\n"
"s_cmp_eq_u32 s47, 8\n"
"v_cvt_pk_bf16_f32 v75, v78, v79\n"
"v_cvt_pk_bf16_f32 v27, v30, v31\n"
"v_mfma_f32_16x16x32_bf16 v[172:175], a[220:223], a[164:167], v[172:175]\n"
"s_cselect_b32 s41, s52, s58\n"
"v_cvt_pk_bf16_f32 v56, v56, v57\n"
"v_permlane16_swap_b32 v1, v3\n"
"v_cvt_pk_bf16_f32 v11, v14, v15\n"
"v_cvt_pk_bf16_f32 v26, v28, v29\n"
"v_cvt_pk_bf16_f32 v33, v34, v35\n"
"v_mfma_f32_16x16x32_bf16 v[168:171], a[208:211], a[160:163], v[228:231]\n"
"s_add_u32 s48, s48, s41\n"
"ds_read_b128 a[176:179], v250 offset:2048\n"
"v_cvt_pk_bf16_f32 v83, v86, v87\n"
"v_mfma_f32_16x16x32_bf16 v[168:171], a[212:215], a[164:167], v[168:171]\n"
"s_sub_u32 s41, s48, 5242880\n"
"ds_read_b64_tr_b16 a[230:231], v244 offset:25088\n"
"ds_read_b64_tr_b16 a[248:249], v245 offset:17408\n"
"v_mfma_f32_16x16x32_bf16 v[164:167], a[200:203], a[160:163], v[228:231]\n"
"s_min_u32 s48, s48, s41\n"
"ds_read_b64_tr_b16 a[232:233], v245 offset:16384\n"
"v_xor_b32_e64 v102, s42, 1\n"
"v_mfma_f32_16x16x32_bf16 v[164:167], a[204:207], a[164:167], v[164:167]\n"
"ds_read_b128 a[184:187], v250 offset:4096\n"
"v_lshl_add_u32 v100, v102, 10, v100\n"
"v_mfma_f32_16x16x32_bf16 v[160:163], a[192:195], a[160:163], v[228:231]\n"
"s_mov_b32 m0, s50\n"
"v_permlane16_swap_b32 v9, v11\n"
"v_mfma_f32_16x16x32_bf16 v[160:163], a[196:199], a[164:167], v[160:163]\n"
"buffer_load_dwordx4 v224, s[28:31], s48 offen lds\n"
"v_mfma_f32_16x16x32_bf16 v[176:179], a[192:195], a[168:171], v[228:231]\n"
"s_waitcnt lgkmcnt(5)\n"
"v_cvt_pk_bf16_f32 v49, v50, v51\n"
"v_cvt_pk_bf16_f32 v34, v36, v37\n"
"v_mfma_f32_16x16x32_bf16 v[176:179], a[196:199], a[172:175], v[176:179]\n"
"v_cvt_pk_bf16_f32 v10, v12, v13\n"
"v_and_b32 v102, 0xf0, v242\n"
"v_mfma_f32_16x16x32_bf16 v[180:183], a[200:203], a[168:171], v[228:231]\n"
"ds_read_b64_tr_b16 a[240:241], v244 offset:17408\n"
"ds_read_b64_tr_b16 a[254:255], v245 offset:26112\n"
"v_mfma_f32_16x16x32_bf16 v[180:183], a[204:207], a[172:175], v[180:183]\n"
"buffer_load_dwordx4 v225, s[28:31], s48 offen offset:1024 lds\n"
"v_mfma_f32_16x16x32_bf16 v[184:187], a[208:211], a[168:171], v[228:231]\n"
"v_cvt_pk_bf16_f32 v90, v92, v93\n"
"v_cvt_pk_bf16_f32 v42, v44, v45\n"
"v_mfma_f32_16x16x32_bf16 v[184:187], a[212:215], a[172:175], v[184:187]\n"
"v_permlane16_swap_b32 v0, v2\n"
"v_mfma_f32_16x16x32_bf16 v[188:191], a[216:219], a[168:171], v[228:231]\n"
"ds_read_b128 a[188:191], v251 offset:4096\n"
"v_accvgpr_write_b32 a51, v3\n"
"v_mfma_f32_16x16x32_bf16 v[188:191], a[220:223], a[172:175], v[188:191]\n"
"s_waitcnt lgkmcnt(3)\n"
"ds_read_b64_tr_b16 a[238:239], v245 offset:25088\n"
"v_cvt_pk_bf16_f32 v67, v70, v71\n"
"v_mfma_f32_16x16x32_bf16 v[204:207], a[216:219], a[176:179], v[228:231]\n"
"ds_read_b64_tr_b16 a[246:247], v244 offset:26112\n"
"v_add_u32 v100, 94208, v100\n"
"v_mfma_f32_16x16x32_bf16 v[204:207], a[220:223], a[180:183], v[204:207]\n"
"buffer_load_dwordx4 v226, s[28:31], s48 offen offset:2048 lds\n"
"v_mfma_f32_16x16x32_bf16 v[200:203], a[208:211], a[176:179], v[228:231]\n"
"v_accvgpr_write_b32 a50, v2\n"
"v_lshl_add_u32 v101, v101, 8, v102\n"
"v_mfma_f32_16x16x32_bf16 v[200:203], a[212:215], a[180:183], v[200:203]\n"
"v_permlane16_swap_b32 v24, v26\n"
"v_mfma_f32_16x16x32_bf16 v[196:199], a[200:203], a[176:179], v[228:231]\n"
"ds_read_b64_tr_b16 a[228:229], v244 offset:24576\n"
"v_cvt_pk_bf16_f32 v106, v108, v109\n"
"v_mfma_f32_16x16x32_bf16 v[196:199], a[204:207], a[180:183], v[196:199]\n"
"ds_write2st64_b64 v100, v[96:97], v[98:99] offset0:120 offset1:121\n"
"v_accvgpr_write_b32 a49, v1\n"
"v_cvt_pk_bf16_f32 v19, v22, v23\n"
"v_permlane16_swap_b32 v25, v27\n"
"v_cvt_pk_bf16_f32 v35, v38, v39\n"
"v_mfma_f32_16x16x32_bf16 v[192:195], a[192:195], a[176:179], v[228:231]\n"
"v_cvt_pk_bf16_f32 v74, v76, v77\n"
"v_cvt_pk_bf16_f32 v18, v20, v21\n"
"v_mfma_f32_16x16x32_bf16 v[192:195], a[196:199], a[180:183], v[192:195]\n"
"buffer_load_dwordx4 v227, s[28:31], s48 offen offset:3072 lds\n"
"v_mfma_f32_16x16x32_bf16 v[208:211], a[192:195], a[184:187], v[228:231]\n"
"s_waitcnt lgkmcnt(2)\n"
"s_xor_b32 s50, s50, s74\n"
"v_permlane16_swap_b32 v33, v35\n"
"v_mfma_f32_16x16x32_bf16 v[208:211], a[196:199], a[188:191], v[208:211]\n"
"v_permlane16_swap_b32 v40, v42\n"
"v_mfma_f32_16x16x32_bf16 v[212:215], a[200:203], a[184:187], v[228:231]\n"
"v_permlane16_swap_b32 v17, v19\n"
"v_mfma_f32_16x16x32_bf16 v[212:215], a[204:207], a[188:191], v[212:215]\n"
"ds_read_b64_tr_b16 a[242:243], v244 offset:17920\n"
"v_cvt_pk_bf16_f32 v107, v110, v111\n"
"v_mfma_f32_16x16x32_bf16 v[216:219], a[208:211], a[184:187], v[228:231]\n"
"v_permlane16_swap_b32 v16, v18\n"
"v_mfma_f32_16x16x32_bf16 v[216:219], a[212:215], a[188:191], v[216:219]\n"
"ds_write2st64_b64 v100, v[104:105], v[106:107] offset0:128 offset1:129\n"
"v_cvt_pk_bf16_f32 v82, v84, v85\n"
"v_accvgpr_write_b32 a75, v27\n"
"v_permlane16_swap_b32 v41, v43\n"
"v_mfma_f32_16x16x32_bf16 v[220:223], a[216:219], a[184:187], v[228:231]\n"
"v_accvgpr_write_b32 a48, v0\n"
"v_accvgpr_write_b32 a64, v16\n"
"v_mfma_f32_16x16x32_bf16 v[220:223], a[220:223], a[188:191], v[220:223]\n"
"ds_write2st64_b64 v100, v[80:81], v[82:83] offset0:88 offset1:89\n"
"v_cvt_pk_bf16_f32 v57, v58, v59\n"
"v_cvt_pk_bf16_f32 v58, v60, v61\n"
"v_permlane16_swap_b32 v8, v10\n"
"v_accvgpr_write_b32 a57, v9\n"
"v_mfma_f32_16x16x32_bf16 v[0:3], a[224:227], a[136:139], v[228:231]\n"
"s_waitcnt lgkmcnt(3)\n"
"v_cvt_pk_bf16_f32 v50, v52, v53\n"
"v_xor_b32 v245, s74, v245\n"
"v_mfma_f32_16x16x32_bf16 v[0:3], a[228:231], a[140:143], v[0:3]\n"
"ds_write2st64_b64 v100, v[72:73], v[74:75] offset0:64 offset1:65\n"
"v_cvt_pk_bf16_f32 v59, v62, v63\n"
"v_accvgpr_write_b32 a58, v10\n"
"v_accvgpr_write_b32 a66, v18\n"
"v_accvgpr_write_b32 a81, v33\n"
"v_mfma_f32_16x16x32_bf16 v[4:7], a[232:235], a[136:139], v[228:231]\n"
"s_waitcnt vmcnt(8) lgkmcnt(3)\n"
"v_permlane16_swap_b32 v57, v59\n"
"v_mfma_f32_16x16x32_bf16 v[4:7], a[236:239], a[140:143], v[4:7]\n"
"s_barrier\n"
"ds_write2st64_b64 v100, v[88:89], v[90:91] offset0:96 offset1:97\n"
"v_accvgpr_write_b32 a56, v8\n"
"v_accvgpr_write_b32 a59, v11\n"
"v_accvgpr_write_b32 a72, v24\n"
"v_mfma_f32_16x16x32_bf16 v[8:11], a[240:243], a[136:139], v[228:231]\n"
"s_mov_b32 m0, s51\n"
"ds_read_b64_tr_b16 a[202:203], v245 offset:512\n"
"v_accvgpr_write_b32 a73, v25\n"
"v_mfma_f32_16x16x32_bf16 v[8:11], a[244:247], a[140:143], v[8:11]\n"
"buffer_load_dwordx4 v248, s[24:27], s49 offen lds\n"
"v_mfma_f32_16x16x32_bf16 v[12:15], a[248:251], a[136:139], v[228:231]\n"
"v_accvgpr_write_b32 a65, v17\n"
"v_accvgpr_write_b32 a67, v19\n"
"v_mfma_f32_16x16x32_bf16 v[12:15], a[252:255], a[140:143], v[12:15]\n"
"v_permlane16_swap_b32 v56, v58\n"
"v_mfma_f32_16x16x32_bf16 v[28:31], a[248:251], a[144:147], v[228:231]\n"
"v_accvgpr_write_b32 a74, v26\n"
"v_accvgpr_write_b32 a83, v35\n"
"v_mfma_f32_16x16x32_bf16 v[28:31], a[252:255], a[148:151], v[28:31]\n"
"ds_write2st64_b64 v100, v[64:65], v[66:67] offset0:0 offset1:1\n"
"v_permlane16_swap_b32 v32, v34\n"
"v_accvgpr_write_b32 a88, v40\n"
"v_mfma_f32_16x16x32_bf16 v[24:27], a[240:243], a[144:147], v[228:231]\n"
"v_accvgpr_write_b32 a89, v41\n"
"v_accvgpr_write_b32 a90, v42\n"
"v_mfma_f32_16x16x32_bf16 v[24:27], a[244:247], a[148:151], v[24:27]\n"
"buffer_load_dwordx4 v249, s[24:27], s49 offen offset:1024 lds\n"
"v_mfma_f32_16x16x32_bf16 v[20:23], a[232:235], a[144:147], v[228:231]\n"
"v_accvgpr_write_b32 a82, v34\n"
"v_accvgpr_write_b32 a91, v43\n"
"v_mfma_f32_16x16x32_bf16 v[20:23], a[236:239], a[148:151], v[20:23]\n"
"v_cvt_pk_bf16_f32 v51, v54, v55\n"
"v_accvgpr_write_b32 a80, v32\n"
"v_mfma_f32_16x16x32_bf16 v[16:19], a[224:227], a[144:147], v[228:231]\n"
"ds_read_b64_tr_b16 a[216:217], v245 offset:1024\n"
"v_xor_b32 v247, s74, v247\n"
"v_mfma_f32_16x16x32_bf16 v[16:19], a[228:231], a[148:151], v[16:19]\n"
"ds_write_b128 v243, v[56:59] offset:6144\n"
"v_mfma_f32_16x16x32_bf16 v[32:35], a[224:227], a[152:155], v[228:231]\n"
"v_permlane16_swap_b32 v48, v50\n"
"v_mfma_f32_16x16x32_bf16 v[32:35], a[228:231], a[156:159], v[32:35]\n"
"buffer_load_dwordx4 v254, s[24:27], s49 offen offset:2048 lds\n"
"v_mfma_f32_16x16x32_bf16 v[36:39], a[232:235], a[152:155], v[228:231]\n"
"s_waitcnt vmcnt(63) lgkmcnt(2)\n"
"v_permlane16_swap_b32 v49, v51\n"
"v_mfma_f32_16x16x32_bf16 v[36:39], a[236:239], a[156:159], v[36:39]\n"
"s_barrier\n"
"v_mfma_f32_16x16x32_bf16 v[40:43], a[240:243], a[152:155], v[228:231]\n"
"ds_read_b64_tr_b16 a[204:205], v245 offset:8192\n"
"ds_read_b128 a[140:143], v247 offset:0\n"
"v_mfma_f32_16x16x32_bf16 v[40:43], a[244:247], a[156:159], v[40:43]\n"
"ds_write_b128 v243, v[48:51] offset:4096\n"
"v_mfma_f32_16x16x32_bf16 v[44:47], a[248:251], a[152:155], v[228:231]\n"
"v_bfe_u32 v102, v242, 8, 1\n"
"v_lshl_add_u32 v101, v102, 9, v101\n"
"v_mfma_f32_16x16x32_bf16 v[44:47], a[252:255], a[156:159], v[44:47]\n"
"buffer_load_dwordx4 v255, s[24:27], s49 offen offset:3072 lds\n"
"v_mfma_f32_16x16x32_bf16 v[60:63], a[248:251], a[160:163], v[228:231]\n"
"v_xor_b32 v244, s74, v244\n"
"v_xor_b32_e64 v102, s42, 0\n"
"v_mfma_f32_16x16x32_bf16 v[60:63], a[252:255], a[164:167], v[60:63]\n"
"v_lshl_add_u32 v101, v102, 10, v101\n"
"v_add_u32 v101, 94208, v101\n"
"v_mfma_f32_16x16x32_bf16 v[56:59], a[240:243], a[160:163], v[228:231]\n"
"ds_read_b64_tr_b16 a[198:199], v244 offset:8704\n"
"ds_read_b64_tr_b16 a[220:221], v245 offset:9216\n"
"v_mfma_f32_16x16x32_bf16 v[56:59], a[244:247], a[164:167], v[56:59]\n"
"s_add_u32 s40, s49, 40960\n"
"ds_read_b128 a[52:55], v101 offset:0\n"
"v_xor_b32 v253, s74, v253\n"
"v_mfma_f32_16x16x32_bf16 v[52:55], a[232:235], a[160:163], v[228:231]\n"
"s_add_u32 m0, s51, 4096\n"
"ds_read_b64_tr_b16 a[206:207], v245 offset:8704\n"
"ds_read_b128 a[76:79], v101 offset:49152\n"
"v_mfma_f32_16x16x32_bf16 v[52:55], a[236:239], a[164:167], v[52:55]\n"
"buffer_load_dwordx4 v248, s[24:27], s40 offen lds\n"
"v_mfma_f32_16x16x32_bf16 v[48:51], a[224:227], a[160:163], v[228:231]\n"
"v_xor_b32 v246, s74, v246\n"
"v_add_u32 v102, s75, v101\n"
"v_mfma_f32_16x16x32_bf16 v[48:51], a[228:231], a[164:167], v[48:51]\n"
"ds_read_b64_tr_b16 a[192:193], v244 offset:0\n"
"ds_read_b128 a[68:71], v101 offset:45056\n"
"v_mfma_f32_16x16x32_bf16 v[64:67], a[224:227], a[168:171], v[228:231]\n"
"ds_read_b64_tr_b16 a[212:213], v244 offset:9216\n"
"ds_read_b128 a[164:167], v253 offset:0\n"
"v_mfma_f32_16x16x32_bf16 v[64:67], a[228:231], a[172:175], v[64:67]\n"
"ds_read_b64_tr_b16 a[208:209], v244 offset:1024\n"
"ds_read_b64_tr_b16 a[218:219], v245 offset:1536\n"
"v_mfma_f32_16x16x32_bf16 v[68:71], a[232:235], a[168:171], v[228:231]\n"
"ds_read_b64_tr_b16 a[200:201], v245 offset:0\n"
"ds_read_b128 a[84:87], v101 offset:61440\n"
"v_mfma_f32_16x16x32_bf16 v[68:71], a[236:239], a[172:175], v[68:71]\n"
"buffer_load_dwordx4 v249, s[24:27], s40 offen offset:1024 lds\n"
"v_mfma_f32_16x16x32_bf16 v[72:75], a[240:243], a[168:171], v[228:231]\n"
"ds_read_b64_tr_b16 a[214:215], v244 offset:9728\n"
"ds_read_b128 a[60:63], v101 offset:32768\n"
"v_mfma_f32_16x16x32_bf16 v[72:75], a[244:247], a[172:175], v[72:75]\n"
"v_xor_b32 v250, s74, v250\n"
"v_xor_b32 v251, s74, v251\n"
"v_mfma_f32_16x16x32_bf16 v[76:79], a[248:251], a[168:171], v[228:231]\n"
"ds_read_b128 a[92:95], v102 offset:0\n"
"ds_read_b128 a[168:171], v250 offset:0\n"
"v_mfma_f32_16x16x32_bf16 v[76:79], a[252:255], a[172:175], v[76:79]\n"
"ds_read_b128 a[152:155], v246 offset:4096\n"
"v_xor_b32 v252, s74, v252\n"
"v_mfma_f32_16x16x32_bf16 v[92:95], a[248:251], a[176:179], v[228:231]\n"
"ds_read_b64_tr_b16 a[222:223], v245 offset:9728\n"
"ds_read_b128 a[160:163], v252 offset:0\n"
"v_mfma_f32_16x16x32_bf16 v[92:95], a[252:255], a[180:183], v[92:95]\n"
"buffer_load_dwordx4 v254, s[24:27], s40 offen offset:2048 lds\n"
"v_mfma_f32_16x16x32_bf16 v[88:91], a[240:243], a[176:179], v[228:231]\n"
"s_add_u32 s49, s49, 128\n"
"ds_read_b128 a[144:147], v246 offset:2048\n"
"ds_read_b128 a[148:151], v247 offset:2048\n"
"v_mfma_f32_16x16x32_bf16 v[88:91], a[244:247], a[180:183], v[88:91]\n"
"s_sub_u32 s41, s49, 1280\n"
"ds_read_b128 a[136:139], v246 offset:0\n"
"v_mfma_f32_16x16x32_bf16 v[84:87], a[232:235], a[176:179], v[228:231]\n"
"s_min_u32 s49, s49, s41\n"
"ds_read_b64_tr_b16 a[196:197], v244 offset:8192\n"
"ds_read_b64_tr_b16 a[210:211], v244 offset:1536\n"
"v_mfma_f32_16x16x32_bf16 v[84:87], a[236:239], a[180:183], v[84:87]\n"
"s_xor_b32 s51, s51, s74\n"
"ds_read_b64_tr_b16 a[194:195], v244 offset:512\n"
"ds_read_b128 a[156:159], v247 offset:4096\n"
"v_mfma_f32_16x16x32_bf16 v[80:83], a[224:227], a[176:179], v[228:231]\n"
"ds_read_b128 a[176:179], v250 offset:2048\n"
"v_mfma_f32_16x16x32_bf16 v[80:83], a[228:231], a[180:183], v[80:83]\n"
"ds_read_b128 a[172:175], v251 offset:0\n"
"ds_read_b128 a[180:183], v251 offset:2048\n"
"v_mfma_f32_16x16x32_bf16 v[96:99], a[224:227], a[184:187], v[228:231]\n"
"v_mfma_f32_16x16x32_bf16 v[96:99], a[228:231], a[188:191], v[96:99]\n"
"v_mfma_f32_16x16x32_bf16 v[100:103], a[232:235], a[184:187], v[228:231]\n"
"v_mfma_f32_16x16x32_bf16 v[100:103], a[236:239], a[188:191], v[100:103]\n"
"v_mfma_f32_16x16x32_bf16 v[104:107], a[240:243], a[184:187], v[228:231]\n"
"v_mfma_f32_16x16x32_bf16 v[104:107], a[244:247], a[188:191], v[104:107]\n"
"v_mfma_f32_16x16x32_bf16 v[108:111], a[248:251], a[184:187], v[228:231]\n"
"s_add_u32 s47, s47, 1\n"
"ds_read_b128 a[184:187], v250 offset:4096\n"
"v_mfma_f32_16x16x32_bf16 v[108:111], a[252:255], a[188:191], v[108:111]\n"
"s_waitcnt lgkmcnt(1)\n"
"ds_read_b128 a[188:191], v251 offset:4096\n"
"s_mov_b32 s62, 18\n"
"s_branch LPE_%=\n"
"LPT2_%=:\n"
"v_mfma_f32_16x16x32_bf16 v[112:115], a[192:195], a[136:139], v[112:115]\n"
"s_add_u32 s78, s32, s96\ns_addc_u32 s79, s33, 0\n"
"s_mov_b64 s[80:81], 0\n"
"s_mov_b32 s82, 0\n"
"v_mfma_f32_16x16x32_bf16 v[112:115], a[196:199], a[140:143], v[112:115]\n"
"v_xor_b32 v248, 64, v240\n"
"v_mfma_f32_16x16x32_bf16 v[116:119], a[200:203], a[136:139], v[116:119]\n"
"s_waitcnt vmcnt(11)\n"
"s_load_dword s80, s[78:79], 0\n"
"v_mfma_f32_16x16x32_bf16 v[116:119], a[204:207], a[140:143], v[116:119]\n"
"s_barrier\n"
"v_mfma_f32_16x16x32_bf16 v[120:123], a[208:211], a[136:139], v[120:123]\n"
"s_add_u32 s40, s48, 256\n"
"s_xor_b32 m0, s50, 0xc000\n"
"ds_read_b64_tr_b16 a[238:239], v245 offset:25088\n"
"v_mfma_f32_16x16x32_bf16 v[120:123], a[212:215], a[140:143], v[120:123]\n"
"buffer_load_dwordx4 v224, s[28:31], s40 offen lds\n"
"v_mfma_f32_16x16x32_bf16 v[124:127], a[216:219], a[136:139], v[124:127]\n"
"v_mfma_f32_16x16x32_bf16 v[124:127], a[220:223], a[140:143], v[124:127]\n"
"v_mfma_f32_16x16x32_bf16 v[140:143], a[216:219], a[144:147], v[140:143]\n"
"ds_read_b64_tr_b16 a[244:245], v244 offset:25600\n"
"v_mfma_f32_16x16x32_bf16 v[140:143], a[220:223], a[148:151], v[140:143]\n"
"v_mfma_f32_16x16x32_bf16 v[136:139], a[208:211], a[144:147], v[136:139]\n"
"ds_read_b64_tr_b16 a[250:251], v245 offset:17920\n"
"v_bfe_i32 v241, v242, 4, 1\n"
"v_mfma_f32_16x16x32_bf16 v[136:139], a[212:215], a[148:151], v[136:139]\n"
"buffer_load_dwordx4 v225, s[28:31], s40 offen offset:1024 lds\n"
"v_mfma_f32_16x16x32_bf16 v[132:135], a[200:203], a[144:147], v[132:135]\n"
"s_add_u32 s82, s82, s80\n"
"v_mfma_f32_16x16x32_bf16 v[132:135], a[204:207], a[148:151], v[132:135]\n"
"s_add_u32 s82, s82, s80\n"
"v_mfma_f32_16x16x32_bf16 v[128:131], a[192:195], a[144:147], v[128:131]\n"
"s_add_u32 s82, s82, s80\n"
"ds_read_b64_tr_b16 a[230:231], v244 offset:25088\n"
"v_mfma_f32_16x16x32_bf16 v[128:131], a[196:199], a[148:151], v[128:131]\n"
"s_add_u32 s82, s82, s80\n"
"v_bfe_u32 v228, v242, 8, 2\n"
"v_mfma_f32_16x16x32_bf16 v[144:147], a[192:195], a[152:155], v[144:147]\n"
"s_add_u32 s82, s82, s80\n"
"ds_read_b64_tr_b16 a[242:243], v244 offset:17920\n"
"v_mfma_f32_16x16x32_bf16 v[144:147], a[196:199], a[156:159], v[144:147]\n"
"s_add_u32 s82, s82, s80\n"
"v_mfma_f32_16x16x32_bf16 v[148:151], a[200:203], a[152:155], v[148:151]\n"
"s_add_u32 s82, s82, s80\n"
"ds_read_b64_tr_b16 a[228:229], v244 offset:24576\n"
"v_mfma_f32_16x16x32_bf16 v[148:151], a[204:207], a[156:159], v[148:151]\n"
"s_add_u32 s82, s82, s80\n"
"buffer_load_dwordx4 v226, s[28:31], s40 offen offset:2048 lds\n"
"v_mfma_f32_16x16x32_bf16 v[152:155], a[208:211], a[152:155], v[152:155]\n"
"s_add_u32 s82, s82, s80\n"
"v_mfma_f32_16x16x32_bf16 v[152:155], a[212:215], a[156:159], v[152:155]\n"
"s_add_u32 s82, s82, s80\n"
"v_and_b32 v229, 0x60, v242\n"
"v_mfma_f32_16x16x32_bf16 v[156:159], a[216:219], a[152:155], v[156:159]\n"
"s_add_u32 s82, s82, s80\n"
"ds_read_b64_tr_b16 a[232:233], v245 offset:16384\n"
"v_mfma_f32_16x16x32_bf16 v[156:159], a[220:223], a[156:159], v[156:159]\n"
"s_add_u32 s82, s82, s80\n"
"v_mfma_f32_16x16x32_bf16 v[172:175], a[216:219], a[160:163], v[172:175]\n"
"s_add_u32 s82, s82, s80\n"
"ds_read_b64_tr_b16 a[234:235], v245 offset:16896\n"
"v_lshlrev_b32 v228, 4, v228\n"
"v_mfma_f32_16x16x32_bf16 v[172:175], a[220:223], a[164:167], v[172:175]\n"
"s_add_u32 s82, s82, s80\n"
"buffer_load_dwordx4 v227, s[28:31], s40 offen offset:3072 lds\n"
"v_mfma_f32_16x16x32_bf16 v[168:171], a[208:211], a[160:163], v[168:171]\n"
"s_add_u32 s82, s82, s80\n"
"s_mov_b32 s58, 106496\n"
"v_mfma_f32_16x16x32_bf16 v[168:171], a[212:215], a[164:167], v[168:171]\n"
"s_add_u32 s82, s82, s80\n"
"s_mov_b32 s47, 0\n"
"v_mfma_f32_16x16x32_bf16 v[164:167], a[200:203], a[160:163], v[164:167]\n"
"s_add_u32 s82, s82, s80\n"
"s_mov_b32 s52, 0\n"
"ds_read_b64_tr_b16 a[246:247], v244 offset:26112\n"
"v_mfma_f32_16x16x32_bf16 v[164:167], a[204:207], a[164:167], v[164:167]\n"
"s_load_dword s81, s[78:79], 0x2000\n"
"s_mov_b32 s53, 6144\n"
"v_mfma_f32_16x16x32_bf16 v[160:163], a[192:195], a[160:163], v[160:163]\n"
"s_mov_b32 m0, s44\n"
"ds_read_b64_tr_b16 a[240:241], v244 offset:17408\n"
"v_mfma_f32_16x16x32_bf16 v[160:163], a[196:199], a[164:167], v[160:163]\n"
"buffer_load_dwordx4 v240, s[16:19], s56 offen nt lds\n"
"v_mfma_f32_16x16x32_bf16 v[176:179], a[192:195], a[168:171], v[176:179]\n"
"v_mfma_f32_16x16x32_bf16 v[176:179], a[196:199], a[172:175], v[176:179]\n"
"v_mfma_f32_16x16x32_bf16 v[180:183], a[200:203], a[168:171], v[180:183]\n"
"ds_read_b64_tr_b16 a[252:253], v245 offset:25600\n"
"v_mfma_f32_16x16x32_bf16 v[180:183], a[204:207], a[172:175], v[180:183]\n"
"v_mfma_f32_16x16x32_bf16 v[184:187], a[208:211], a[168:171], v[184:187]\n"
"ds_read_b64_tr_b16 a[236:237], v245 offset:24576\n"
"v_xor_b32 v228, v228, v229\n"
"v_mfma_f32_16x16x32_bf16 v[184:187], a[212:215], a[172:175], v[184:187]\n"
"v_mfma_f32_16x16x32_bf16 v[188:191], a[216:219], a[168:171], v[188:191]\n"
"s_add_u32 s40, s56, s52\n"
"ds_read_b64_tr_b16 a[226:227], v244 offset:16896\n"
"v_mfma_f32_16x16x32_bf16 v[188:191], a[220:223], a[172:175], v[188:191]\n"
"s_sub_u32 s82, s82, s81\n"
"buffer_load_dwordx4 v248, s[16:19], s40 offen offset:1024 nt lds\n"
"v_mfma_f32_16x16x32_bf16 v[204:207], a[216:219], a[176:179], v[204:207]\n"
"s_sub_u32 s82, s82, s81\n"
"v_mfma_f32_16x16x32_bf16 v[204:207], a[220:223], a[180:183], v[204:207]\n"
"s_sub_u32 s82, s82, s81\n"
"v_mfma_f32_16x16x32_bf16 v[200:203], a[208:211], a[176:179], v[200:203]\n"
"s_sub_u32 s82, s82, s81\n"
"ds_read_b64_tr_b16 a[248:249], v245 offset:17408\n"
"v_and_b32 v229, 0x30, v242\n"
"v_mfma_f32_16x16x32_bf16 v[200:203], a[212:215], a[180:183], v[200:203]\n"
"s_sub_u32 s82, s82, s81\n"
"v_mfma_f32_16x16x32_bf16 v[196:199], a[200:203], a[176:179], v[196:199]\n"
"s_sub_u32 s82, s82, s81\n"
"s_add_u32 s40, s56, s53\n"
"ds_read_b64_tr_b16 a[224:225], v244 offset:16384\n"
"v_mfma_f32_16x16x32_bf16 v[196:199], a[204:207], a[180:183], v[196:199]\n"
"s_sub_u32 s82, s82, s81\n"
"buffer_load_dwordx4 v240, s[16:19], s40 offen offset:2048 nt lds\n"
"v_mfma_f32_16x16x32_bf16 v[192:195], a[192:195], a[176:179], v[192:195]\n"
"s_sub_u32 s82, s82, s81\n"
"v_lshl_add_u32 v228, v229, 3, v228\n"
"v_mfma_f32_16x16x32_bf16 v[192:195], a[196:199], a[180:183], v[192:195]\n"
"s_waitcnt lgkmcnt(2)\n"
"s_sub_u32 s82, s82, s81\n"
"v_xor_b32 v244, s74, v244\n"
"v_mfma_f32_16x16x32_bf16 v[208:211], a[192:195], a[184:187], v[208:211]\n"
"s_sub_u32 s82, s82, s81\n"
"v_mfma_f32_16x16x32_bf16 v[208:211], a[196:199], a[188:191], v[208:211]\n"
"s_sub_u32 s82, s82, s81\n"
"ds_read_b64_tr_b16 a[254:255], v245 offset:26112\n"
"v_mfma_f32_16x16x32_bf16 v[212:215], a[200:203], a[184:187], v[212:215]\n"
"s_sub_u32 s82, s82, s81\n"
"s_add_u32 s40, s56, s54\n"
"v_mfma_f32_16x16x32_bf16 v[212:215], a[204:207], a[188:191], v[212:215]\n"
"s_sub_u32 s82, s82, s81\n"
"buffer_load_dwordx4 v248, s[16:19], s40 offen offset:3072 nt lds\n"
"v_mfma_f32_16x16x32_bf16 v[216:219], a[208:211], a[184:187], v[216:219]\n"
"s_sub_u32 s82, s82, s81\n"
"v_and_b32 v229, 0xc0, v242\n"
"v_xor_b32 v251, s74, v251\n"
"v_mfma_f32_16x16x32_bf16 v[216:219], a[212:215], a[188:191], v[216:219]\n"
"s_sub_u32 s82, s82, s81\n"
"v_mfma_f32_16x16x32_bf16 v[220:223], a[216:219], a[184:187], v[220:223]\n"
"s_sub_u32 s82, s82, s81\n"
"v_mfma_f32_16x16x32_bf16 v[220:223], a[220:223], a[188:191], v[220:223]\n"
"s_waitcnt lgkmcnt(1)\n"
"s_sub_u32 s82, s82, s81\n"
"v_mfma_f32_16x16x32_bf16 v[0:3], a[224:227], a[136:139], v[0:3]\n"
"v_mfma_f32_16x16x32_bf16 v[0:3], a[228:231], a[140:143], v[0:3]\n"
"v_lshl_add_u32 v228, v229, 4, v228\n"
"v_xor_b32 v245, s74, v245\n"
"v_mfma_f32_16x16x32_bf16 v[4:7], a[232:235], a[136:139], v[4:7]\n"
"s_waitcnt vmcnt(8) lgkmcnt(0)\n"
"v_mfma_f32_16x16x32_bf16 v[4:7], a[236:239], a[140:143], v[4:7]\n"
"s_barrier\n"
"v_mfma_f32_16x16x32_bf16 v[8:11], a[240:243], a[136:139], v[8:11]\n"
"s_add_u32 m0, s44, 65536\n"
"ds_read_b64_tr_b16 a[222:223], v245 offset:9728\n"
"v_mfma_f32_16x16x32_bf16 v[8:11], a[244:247], a[140:143], v[8:11]\n"
"buffer_load_dwordx4 v240, s[20:23], s56 offen nt lds\n"
"v_mfma_f32_16x16x32_bf16 v[12:15], a[248:251], a[136:139], v[12:15]\n"
"s_lshr_b32 s82, s82, 31\n"
"v_mfma_f32_16x16x32_bf16 v[12:15], a[252:255], a[140:143], v[12:15]\n"
"s_add_u32 s82, s82, s60\n"
"v_mfma_f32_16x16x32_bf16 v[28:31], a[248:251], a[144:147], v[28:31]\n"
"s_add_u32 s82, s82, 32\n"
"ds_read_b64_tr_b16 a[216:217], v245 offset:1024\n"
"v_xor_b32 v247, s74, v247\n"
"v_mfma_f32_16x16x32_bf16 v[28:31], a[252:255], a[148:151], v[28:31]\n"
"v_mov_b32 v254, s82\n"
"ds_read_b64_tr_b16 a[196:197], v244 offset:8192\n"
"v_mfma_f32_16x16x32_bf16 v[24:27], a[240:243], a[144:147], v[24:27]\n"
"v_lshlrev_b32_e64 v255, 2, s93\n"
"ds_read_b64_tr_b16 a[198:199], v244 offset:8704\n"
"v_xor_b32 v246, s74, v246\n"
"v_mfma_f32_16x16x32_bf16 v[24:27], a[244:247], a[148:151], v[24:27]\n"
"ds_read_b64_tr_b16 a[214:215], v244 offset:9728\n"
"v_mov_b32 v229, s43\n"
"v_mfma_f32_16x16x32_bf16 v[20:23], a[232:235], a[144:147], v[20:23]\n"
"s_add_u32 s40, s56, s52\n"
"ds_read_b64_tr_b16 a[204:205], v245 offset:8192\n"
"v_mfma_f32_16x16x32_bf16 v[20:23], a[236:239], a[148:151], v[20:23]\n"
"buffer_load_dwordx4 v248, s[20:23], s40 offen offset:1024 nt lds\n"
"v_mfma_f32_16x16x32_bf16 v[16:19], a[224:227], a[144:147], v[16:19]\n"
"v_mfma_f32_16x16x32_bf16 v[16:19], a[228:231], a[148:151], v[16:19]\n"
"s_mov_b64 exec, 1\n"
"global_store_dword v255, v254, s[6:7] offset:256 sc1\n"
"s_mov_b64 exec, -1\n"
"v_mfma_f32_16x16x32_bf16 v[32:35], a[224:227], a[152:155], v[32:35]\n"
"ds_read_b64_tr_b16 a[202:203], v245 offset:512\n"
"v_mfma_f32_16x16x32_bf16 v[32:35], a[228:231], a[156:159], v[32:35]\n"
"v_mfma_f32_16x16x32_bf16 v[36:39], a[232:235], a[152:155], v[36:39]\n"
"s_add_u32 s40, s56, s53\n"
"ds_read_b128 a[140:143], v247 offset:0\n"
"v_mfma_f32_16x16x32_bf16 v[36:39], a[236:239], a[156:159], v[36:39]\n"
"buffer_load_dwordx4 v240, s[20:23], s40 offen offset:2048 nt lds\n"
"v_mfma_f32_16x16x32_bf16 v[40:43], a[240:243], a[152:155], v[40:43]\n"
"v_xor_b32 v250, s74, v250\n"
"v_mfma_f32_16x16x32_bf16 v[40:43], a[244:247], a[156:159], v[40:43]\n"
"v_mfma_f32_16x16x32_bf16 v[44:47], a[248:251], a[152:155], v[44:47]\n"
"ds_read_b64_tr_b16 a[200:201], v245 offset:0\n"
"v_mul_u32_u24 v229, 0x81, v229\n"
"v_mfma_f32_16x16x32_bf16 v[44:47], a[252:255], a[156:159], v[44:47]\n"
"ds_read_b64_tr_b16 a[220:221], v245 offset:9216\n"
"v_mfma_f32_16x16x32_bf16 v[60:63], a[248:251], a[160:163], v[60:63]\n"
"s_add_u32 s40, s56, s54\n"
"ds_read_b64_tr_b16 a[210:211], v244 offset:1536\n"
"v_xor_b32 v253, s74, v253\n"
"v_mfma_f32_16x16x32_bf16 v[60:63], a[252:255], a[164:167], v[60:63]\n"
"buffer_load_dwordx4 v248, s[20:23], s40 offen offset:3072 nt lds\n"
"v_mfma_f32_16x16x32_bf16 v[56:59], a[240:243], a[160:163], v[56:59]\n"
"s_add_u32 s55, s55, s59\n"
"v_mfma_f32_16x16x32_bf16 v[56:59], a[244:247], a[164:167], v[56:59]\n"
"s_or_b32 s55, s55, s58\n"
"v_mfma_f32_16x16x32_bf16 v[52:55], a[232:235], a[160:163], v[52:55]\n"
"s_xor_b32 s56, s55, s57\n"
"ds_read_b128 a[136:139], v246 offset:0\n"
"v_mfma_f32_16x16x32_bf16 v[52:55], a[236:239], a[164:167], v[52:55]\n"
"v_mfma_f32_16x16x32_bf16 v[48:51], a[224:227], a[160:163], v[48:51]\n"
"s_add_u32 m0, s44, 16384\n"
"ds_read_b128 a[156:159], v247 offset:4096\n"
"v_mfma_f32_16x16x32_bf16 v[48:51], a[228:231], a[164:167], v[48:51]\n"
"buffer_load_dwordx4 v240, s[16:19], s56 offen nt lds\n"
"v_mfma_f32_16x16x32_bf16 v[64:67], a[224:227], a[168:171], v[64:67]\n"
"v_mfma_f32_16x16x32_bf16 v[64:67], a[228:231], a[172:175], v[64:67]\n"
"v_mfma_f32_16x16x32_bf16 v[68:71], a[232:235], a[168:171], v[68:71]\n"
"ds_read_b128 a[164:167], v253 offset:0\n"
"v_mfma_f32_16x16x32_bf16 v[68:71], a[236:239], a[172:175], v[68:71]\n"
"v_mfma_f32_16x16x32_bf16 v[72:75], a[240:243], a[168:171], v[72:75]\n"
"ds_read_b64_tr_b16 a[218:219], v245 offset:1536\n"
"v_mfma_f32_16x16x32_bf16 v[72:75], a[244:247], a[172:175], v[72:75]\n"
"ds_read_b64_tr_b16 a[194:195], v244 offset:512\n"
"v_mfma_f32_16x16x32_bf16 v[76:79], a[248:251], a[168:171], v[76:79]\n"
"s_add_u32 s40, s56, s52\n"
"ds_read_b64_tr_b16 a[206:207], v245 offset:8704\n"
"v_mfma_f32_16x16x32_bf16 v[76:79], a[252:255], a[172:175], v[76:79]\n"
"buffer_load_dwordx4 v248, s[16:19], s40 offen offset:1024 nt lds\n"
"v_mfma_f32_16x16x32_bf16 v[92:95], a[248:251], a[176:179], v[92:95]\n"
"v_mfma_f32_16x16x32_bf16 v[92:95], a[252:255], a[180:183], v[92:95]\n"
"v_mfma_f32_16x16x32_bf16 v[88:91], a[240:243], a[176:179], v[88:91]\n"
"ds_read_b64_tr_b16 a[208:209], v244 offset:1024\n"
"v_mfma_f32_16x16x32_bf16 v[88:91], a[244:247], a[180:183], v[88:91]\n"
"ds_read_b64_tr_b16 a[212:213], v244 offset:9216\n"
"v_mfma_f32_16x16x32_bf16 v[84:87], a[232:235], a[176:179], v[84:87]\n"
"s_add_u32 s40, s56, s53\n"
"ds_read_b64_tr_b16 a[192:193], v244 offset:0\n"
"v_mfma_f32_16x16x32_bf16 v[84:87], a[236:239], a[180:183], v[84:87]\n"
"buffer_load_dwordx4 v240, s[16:19], s40 offen offset:2048 nt lds\n"
"v_mfma_f32_16x16x32_bf16 v[80:83], a[224:227], a[176:179], v[80:83]\n"
"v_mfma_f32_16x16x32_bf16 v[80:83], a[228:231], a[180:183], v[80:83]\n"
"v_mfma_f32_16x16x32_bf16 v[96:99], a[224:227], a[184:187], v[96:99]\n"
"ds_read_b128 a[172:175], v251 offset:0\n"
"v_mfma_f32_16x16x32_bf16 v[96:99], a[228:231], a[188:191], v[96:99]\n"
"v_mfma_f32_16x16x32_bf16 v[100:103], a[232:235], a[184:187], v[100:103]\n"
"s_add_u32 s40, s56, s54\n"
"ds_read_b128 a[148:151], v247 offset:2048\n"
"v_mfma_f32_16x16x32_bf16 v[100:103], a[236:239], a[188:191], v[100:103]\n"
"buffer_load_dwordx4 v248, s[16:19], s40 offen offset:3072 nt lds\n"
"v_mfma_f32_16x16x32_bf16 v[104:107], a[240:243], a[184:187], v[104:107]\n"
"v_mfma_f32_16x16x32_bf16 v[104:107], a[244:247], a[188:191], v[104:107]\n"
"v_and_b32 v254, 0x20, v242\n"
"v_and_b32 v241, 0xaaaaaaaa, v241\n"
"v_mfma_f32_16x16x32_bf16 v[108:111], a[248:251], a[184:187], v[108:111]\n"
"ds_read_b128 a[168:171], v250 offset:0\n"
"v_mfma_f32_16x16x32_bf16 v[108:111], a[252:255], a[188:191], v[108:111]\n"
"s_waitcnt lgkmcnt(3)\n"
"v_mfma_f32_16x16x32_bf16 v[112:115], a[192:195], a[136:139], v[112:115]\n"
"ds_read_b128 a[184:187], v250 offset:4096\n"
"v_mfma_f32_16x16x32_bf16 v[112:115], a[196:199], a[140:143], v[112:115]\n"
"v_mfma_f32_16x16x32_bf16 v[116:119], a[200:203], a[136:139], v[116:119]\n"
"s_waitcnt vmcnt(13)\n"
"ds_read_b128 a[180:183], v251 offset:2048\n"
"v_and_b32 v249, 0xf0, v242\n"
"v_mfma_f32_16x16x32_bf16 v[116:119], a[204:207], a[140:143], v[116:119]\n"
"s_barrier\n"
"v_mfma_f32_16x16x32_bf16 v[120:123], a[208:211], a[136:139], v[120:123]\n"
"s_add_u32 m0, s44, 81920\n"
"ds_read_b128 a[176:179], v250 offset:2048\n"
"v_mfma_f32_16x16x32_bf16 v[120:123], a[212:215], a[140:143], v[120:123]\n"
"buffer_load_dwordx4 v240, s[20:23], s56 offen nt lds\n"
"v_mfma_f32_16x16x32_bf16 v[124:127], a[216:219], a[136:139], v[124:127]\n"
"v_bfe_u32 v255, v242, 9, 1\n"
"v_lshl_add_u32 v255, v255, 8, v249\n"
"v_mfma_f32_16x16x32_bf16 v[124:127], a[220:223], a[140:143], v[124:127]\n"
"s_waitcnt lgkmcnt(3)\n"
"v_xor_b32 v252, s74, v252\n"
"v_mfma_f32_16x16x32_bf16 v[188:191], a[216:219], a[168:171], v[188:191]\n"
"ds_read_b128 a[188:191], v251 offset:4096\n"
"v_xor_b32 v228, v228, v229\n"
"v_mfma_f32_16x16x32_bf16 v[188:191], a[220:223], a[172:175], v[188:191]\n"
"v_mfma_f32_16x16x32_bf16 v[184:187], a[208:211], a[168:171], v[184:187]\n"
"ds_read_b128 a[144:147], v246 offset:2048\n"
"v_add_u32 v254, -1, v254\n"
"v_mfma_f32_16x16x32_bf16 v[184:187], a[212:215], a[172:175], v[184:187]\n"
"v_mfma_f32_16x16x32_bf16 v[180:183], a[200:203], a[168:171], v[180:183]\n"
"ds_read_b64_tr_b16 a[234:235], v245 offset:16896\n"
"v_mfma_f32_16x16x32_bf16 v[180:183], a[204:207], a[172:175], v[180:183]\n"
"v_mfma_f32_16x16x32_bf16 v[176:179], a[192:195], a[168:171], v[176:179]\n"
"ds_read_b128 a[160:163], v252 offset:0\n"
"v_mfma_f32_16x16x32_bf16 v[176:179], a[196:199], a[172:175], v[176:179]\n"
"s_waitcnt lgkmcnt(4)\n"
"v_mfma_f32_16x16x32_bf16 v[192:195], a[192:195], a[176:179], v[192:195]\n"
"ds_read_b64_tr_b16 a[230:231], v244 offset:25088\n"
"v_or_b32 v241, 0x44444444, v241\n"
"v_mfma_f32_16x16x32_bf16 v[192:195], a[196:199], a[180:183], v[192:195]\n"
"v_bfe_u32 v249, v242, 8, 1\n"
"v_mfma_f32_16x16x32_bf16 v[196:199], a[200:203], a[176:179], v[196:199]\n"
"s_add_u32 s40, s56, s52\n"
"ds_read_b64_tr_b16 a[246:247], v244 offset:26112\n"
"v_mfma_f32_16x16x32_bf16 v[196:199], a[204:207], a[180:183], v[196:199]\n"
"buffer_load_dwordx4 v248, s[20:23], s40 offen offset:1024 nt lds\n"
"v_mfma_f32_16x16x32_bf16 v[200:203], a[208:211], a[176:179], v[200:203]\n"
"v_xor_b32 v229, 64, v228\n"
"v_mfma_f32_16x16x32_bf16 v[200:203], a[212:215], a[180:183], v[200:203]\n"
"v_mfma_f32_16x16x32_bf16 v[204:207], a[216:219], a[176:179], v[204:207]\n"
"ds_read_b64_tr_b16 a[240:241], v244 offset:17408\n"
"v_lshl_add_u32 v255, v249, 3, v255\n"
"v_mfma_f32_16x16x32_bf16 v[204:207], a[220:223], a[180:183], v[204:207]\n"
"v_xor_b32 v231, 0x2000, v229\n"
"v_xor_b32_e64 v249, s42, 1\n"
"v_mfma_f32_16x16x32_bf16 v[220:223], a[216:219], a[184:187], v[220:223]\n"
"s_waitcnt lgkmcnt(2)\n"
"ds_read_b64_tr_b16 a[232:233], v245 offset:16384\n"
"v_mfma_f32_16x16x32_bf16 v[220:223], a[220:223], a[188:191], v[220:223]\n"
"ds_read_b64_tr_b16 a[226:227], v244 offset:16896\n"
"v_mfma_f32_16x16x32_bf16 v[216:219], a[208:211], a[184:187], v[216:219]\n"
"ds_read_b64_tr_b16 a[224:225], v244 offset:16384\n"
"v_xor_b32 v230, 0x2000, v228\n"
"v_mfma_f32_16x16x32_bf16 v[216:219], a[212:215], a[188:191], v[216:219]\n"
"v_pk_fma_f32 v[176:177], v[176:177], s[70:71], s[70:71] op_sel:[0,0,1] op_sel_hi:[1,0,1]\nv_pk_fma_f32 v[178:179], v[178:179], s[70:71], s[70:71] op_sel:[0,0,1] op_sel_hi:[1,0,1]\nv_pk_fma_f32 v[180:181], v[180:181], s[70:71], s[70:71] op_sel:[0,0,1] op_sel_hi:[1,0,1]\nv_pk_fma_f32 v[182:183], v[182:183], s[70:71], s[70:71] op_sel:[0,0,1] op_sel_hi:[1,0,1]\nv_pk_fma_f32 v[184:185], v[184:185], s[70:71], s[70:71] op_sel:[0,0,1] op_sel_hi:[1,0,1]\nv_pk_fma_f32 v[186:187], v[186:187], s[70:71], s[70:71] op_sel:[0,0,1] op_sel_hi:[1,0,1]\nv_pk_fma_f32 v[188:189], v[188:189], s[70:71], s[70:71] op_sel:[0,0,1] op_sel_hi:[1,0,1]\nv_pk_fma_f32 v[190:191], v[190:191], s[70:71], s[70:71] op_sel:[0,0,1] op_sel_hi:[1,0,1]\nv_pk_fma_f32 v[192:193], v[192:193], s[70:71], s[70:71] op_sel:[0,0,1] op_sel_hi:[1,0,1]\nv_pk_fma_f32 v[194:195], v[194:195], s[70:71], s[70:71] op_sel:[0,0,1] op_sel_hi:[1,0,1]\nv_pk_fma_f32 v[196:197], v[196:197], s[70:71], s[70:71] op_sel:[0,0,1] op_sel_hi:[1,0,1]\nv_pk_fma_f32 v[198:199], v[198:199], s[70:71], s[70:71] op_sel:[0,0,1] op_sel_hi:[1,0,1]\nv_pk_fma_f32 v[200:201], v[200:201], s[70:71], s[70:71] op_sel:[0,0,1] op_sel_hi:[1,0,1]\nv_pk_fma_f32 v[202:203], v[202:203], s[70:71], s[70:71] op_sel:[0,0,1] op_sel_hi:[1,0,1]\nv_pk_fma_f32 v[204:205], v[204:205], s[70:71], s[70:71] op_sel:[0,0,1] op_sel_hi:[1,0,1]\nv_pk_fma_f32 v[206:207], v[206:207], s[70:71], s[70:71] op_sel:[0,0,1] op_sel_hi:[1,0,1]\n"
"v_mfma_f32_16x16x32_bf16 v[212:215], a[200:203], a[184:187], v[212:215]\n"
"ds_read_b64_tr_b16 a[252:253], v245 offset:25600\n"
"v_cvt_pk_bf16_f32 v192, v192, v193\n"
"v_mfma_f32_16x16x32_bf16 v[212:215], a[204:207], a[188:191], v[212:215]\n"
"v_cvt_pk_bf16_f32 v176, v176, v177\n"
"v_cvt_pk_bf16_f32 v193, v194, v195\n"
"v_mfma_f32_16x16x32_bf16 v[208:211], a[192:195], a[184:187], v[208:211]\n"
"s_add_u32 s40, s56, s53\n"
"ds_read_b64_tr_b16 a[248:249], v245 offset:17408\n"
"v_mfma_f32_16x16x32_bf16 v[208:211], a[196:199], a[188:191], v[208:211]\n"
"buffer_load_dwordx4 v240, s[20:23], s40 offen offset:2048 nt lds\n"
"v_mfma_f32_16x16x32_bf16 v[160:163], a[192:195], a[160:163], v[160:163]\n"
"v_cvt_pk_bf16_f32 v177, v178, v179\n"
"v_cvt_pk_bf16_f32 v184, v184, v185\n"
"v_mfma_f32_16x16x32_bf16 v[160:163], a[196:199], a[164:167], v[160:163]\n"
"v_cvt_pk_bf16_f32 v179, v182, v183\n"
"v_cvt_pk_bf16_f32 v185, v186, v187\n"
"v_mfma_f32_16x16x32_bf16 v[164:167], a[200:203], a[160:163], v[164:167]\n"
"ds_read_b64_tr_b16 a[244:245], v244 offset:25600\n"
"v_cvt_pk_bf16_f32 v200, v200, v201\n"
"v_mfma_f32_16x16x32_bf16 v[164:167], a[204:207], a[164:167], v[164:167]\n"
"v_cvt_pk_bf16_f32 v201, v202, v203\n"
"v_ashrrev_i32 v254, 31, v254\n"
"v_mfma_f32_16x16x32_bf16 v[168:171], a[208:211], a[160:163], v[168:171]\n"
"ds_read_b64_tr_b16 a[254:255], v245 offset:26112\n"
"v_cvt_pk_bf16_f32 v194, v196, v197\n"
"v_mfma_f32_16x16x32_bf16 v[168:171], a[212:215], a[164:167], v[168:171]\n"
"v_cvt_pk_bf16_f32 v202, v204, v205\n"
"v_mfma_f32_16x16x32_bf16 v[172:175], a[216:219], a[160:163], v[172:175]\n"
"ds_read_b64_tr_b16 a[242:243], v244 offset:17920\n"
"v_and_b32 v254, 0x3f803f80, v254\n"
"v_mfma_f32_16x16x32_bf16 v[172:175], a[220:223], a[164:167], v[172:175]\n"
"v_bfe_u32 v196, v242, 8, 2\n"
"v_lshl_add_u32 v196, v196, 4, s43\n"
"v_mfma_f32_16x16x32_bf16 v[140:143], a[216:219], a[144:147], v[140:143]\n"
"ds_read_b64_tr_b16 a[236:237], v245 offset:24576\n"
"v_lshl_add_u32 v255, v249, 12, v255\n"
"v_mfma_f32_16x16x32_bf16 v[140:143], a[220:223], a[148:151], v[140:143]\n"
"v_cvt_pk_bf16_f32 v203, v206, v207\n"
"v_mfma_f32_16x16x32_bf16 v[136:139], a[208:211], a[144:147], v[136:139]\n"
"ds_read_b128 a[152:155], v246 offset:4096\n"
"v_cvt_pk_bf16_f32 v178, v180, v181\n"
"v_mfma_f32_16x16x32_bf16 v[136:139], a[212:215], a[148:151], v[136:139]\n"
"v_pk_fma_f32 v[208:209], v[208:209], s[70:71], s[70:71] op_sel:[0,0,1] op_sel_hi:[1,0,1]\nv_pk_fma_f32 v[210:211], v[210:211], s[70:71], s[70:71] op_sel:[0,0,1] op_sel_hi:[1,0,1]\nv_pk_fma_f32 v[212:213], v[212:213], s[70:71], s[70:71] op_sel:[0,0,1] op_sel_hi:[1,0,1]\nv_pk_fma_f32 v[214:215], v[214:215], s[70:71], s[70:71] op_sel:[0,0,1] op_sel_hi:[1,0,1]\nv_pk_fma_f32 v[216:217], v[216:217], s[70:71], s[70:71] op_sel:[0,0,1] op_sel_hi:[1,0,1]\nv_pk_fma_f32 v[218:219], v[218:219], s[70:71], s[70:71] op_sel:[0,0,1] op_sel_hi:[1,0,1]\nv_pk_fma_f32 v[220:221], v[220:221], s[70:71], s[70:71] op_sel:[0,0,1] op_sel_hi:[1,0,1]\nv_pk_fma_f32 v[222:223], v[222:223], s[70:71], s[70:71] op_sel:[0,0,1] op_sel_hi:[1,0,1]\nv_pk_fma_f32 v[160:161], v[160:161], s[70:71], s[70:71] op_sel:[0,0,1] op_sel_hi:[1,0,1]\nv_pk_fma_f32 v[162:163], v[162:163], s[70:71], s[70:71] op_sel:[0,0,1] op_sel_hi:[1,0,1]\nv_pk_fma_f32 v[164:165], v[164:165], s[70:71], s[70:71] op_sel:[0,0,1] op_sel_hi:[1,0,1]\nv_pk_fma_f32 v[166:167], v[166:167], s[70:71], s[70:71] op_sel:[0,0,1] op_sel_hi:[1,0,1]\nv_pk_fma_f32 v[168:169], v[168:169], s[70:71], s[70:71] op_sel:[0,0,1] op_sel_hi:[1,0,1]\nv_pk_fma_f32 v[170:171], v[170:171], s[70:71], s[70:71] op_sel:[0,0,1] op_sel_hi:[1,0,1]\nv_pk_fma_f32 v[172:173], v[172:173], s[70:71], s[70:71] op_sel:[0,0,1] op_sel_hi:[1,0,1]\nv_pk_fma_f32 v[174:175], v[174:175], s[70:71], s[70:71] op_sel:[0,0,1] op_sel_hi:[1,0,1]\n"
"v_mfma_f32_16x16x32_bf16 v[132:135], a[200:203], a[144:147], v[132:135]\n"
"s_add_u32 s40, s56, s54\n"
"ds_read_b64_tr_b16 a[250:251], v245 offset:17920\n"
"v_cvt_pk_bf16_f32 v160, v160, v161\n"
"v_mfma_f32_16x16x32_bf16 v[132:135], a[204:207], a[148:151], v[132:135]\n"
"buffer_load_dwordx4 v248, s[20:23], s40 offen offset:3072 nt lds\n"
"v_mfma_f32_16x16x32_bf16 v[128:131], a[192:195], a[144:147], v[128:131]\n"
"s_add_u32 s55, s55, s59\n"
"v_cvt_pk_bf16_f32 v186, v188, v189\n"
"v_cvt_pk_bf16_f32 v216, v216, v217\n"
"v_mfma_f32_16x16x32_bf16 v[128:131], a[196:199], a[148:151], v[128:131]\n"
"s_waitcnt lgkmcnt(1)\n"
"s_or_b32 s55, s55, s58\n"
"v_cvt_pk_bf16_f32 v208, v208, v209\n"
"v_cvt_pk_bf16_f32 v161, v162, v163\n"
"v_mfma_f32_16x16x32_bf16 v[144:147], a[192:195], a[152:155], v[144:147]\n"
"s_xor_b32 s56, s55, s57\n"
"ds_read_b64_tr_b16 a[238:239], v245 offset:25088\n"
"v_cvt_pk_bf16_f32 v187, v190, v191\n"
"v_mfma_f32_16x16x32_bf16 v[144:147], a[196:199], a[156:159], v[144:147]\n"
"v_cvt_pk_bf16_f32 v217, v218, v219\n"
"v_cvt_pk_bf16_f32 v168, v168, v169\n"
"v_mfma_f32_16x16x32_bf16 v[148:151], a[200:203], a[152:155], v[148:151]\n"
"v_cvt_pk_bf16_f32 v195, v198, v199\n"
"v_xor_b32 v198, 0x400, v243\n"
"v_mfma_f32_16x16x32_bf16 v[148:151], a[204:207], a[156:159], v[148:151]\n"
"ds_read_b64_tr_b16 a[228:229], v244 offset:24576\n"
"v_cvt_pk_bf16_f32 v169, v170, v171\n"
"v_mfma_f32_16x16x32_bf16 v[152:155], a[208:211], a[152:155], v[152:155]\n"
"v_cvt_pk_bf16_f32 v163, v166, v167\n"
"v_cvt_pk_bf16_f32 v171, v174, v175\n"
"v_mfma_f32_16x16x32_bf16 v[152:155], a[212:215], a[156:159], v[152:155]\n"
"v_pk_fma_f32 v[112:113], v[112:113], s[70:71], s[70:71] op_sel:[0,0,1] op_sel_hi:[1,0,1]\nv_pk_fma_f32 v[114:115], v[114:115], s[70:71], s[70:71] op_sel:[0,0,1] op_sel_hi:[1,0,1]\nv_pk_fma_f32 v[116:117], v[116:117], s[70:71], s[70:71] op_sel:[0,0,1] op_sel_hi:[1,0,1]\nv_pk_fma_f32 v[118:119], v[118:119], s[70:71], s[70:71] op_sel:[0,0,1] op_sel_hi:[1,0,1]\nv_pk_fma_f32 v[120:121], v[120:121], s[70:71], s[70:71] op_sel:[0,0,1] op_sel_hi:[1,0,1]\nv_pk_fma_f32 v[122:123], v[122:123], s[70:71], s[70:71] op_sel:[0,0,1] op_sel_hi:[1,0,1]\nv_pk_fma_f32 v[124:125], v[124:125], s[70:71], s[70:71] op_sel:[0,0,1] op_sel_hi:[1,0,1]\nv_pk_fma_f32 v[126:127], v[126:127], s[70:71], s[70:71] op_sel:[0,0,1] op_sel_hi:[1,0,1]\nv_pk_fma_f32 v[128:129], v[128:129], s[70:71], s[70:71] op_sel:[0,0,1] op_sel_hi:[1,0,1]\nv_pk_fma_f32 v[130:131], v[130:131], s[70:71], s[70:71] op_sel:[0,0,1] op_sel_hi:[1,0,1]\nv_pk_fma_f32 v[132:133], v[132:133], s[70:71], s[70:71] op_sel:[0,0,1] op_sel_hi:[1,0,1]\nv_pk_fma_f32 v[134:135], v[134:135], s[70:71], s[70:71] op_sel:[0,0,1] op_sel_hi:[1,0,1]\nv_pk_fma_f32 v[136:137], v[136:137], s[70:71], s[70:71] op_sel:[0,0,1] op_sel_hi:[1,0,1]\nv_pk_fma_f32 v[138:139], v[138:139], s[70:71], s[70:71] op_sel:[0,0,1] op_sel_hi:[1,0,1]\nv_pk_fma_f32 v[140:141], v[140:141], s[70:71], s[70:71] op_sel:[0,0,1] op_sel_hi:[1,0,1]\nv_pk_fma_f32 v[142:143], v[142:143], s[70:71], s[70:71] op_sel:[0,0,1] op_sel_hi:[1,0,1]\n"
"v_mfma_f32_16x16x32_bf16 v[156:159], a[216:219], a[152:155], v[156:159]\n"
"v_cvt_pk_bf16_f32 v136, v136, v137\n"
"v_cvt_pk_bf16_f32 v137, v138, v139\n"
"v_mfma_f32_16x16x32_bf16 v[156:159], a[220:223], a[156:159], v[156:159]\n"
"v_xor_b32 v197, 64, v196\n"
"v_cvt_pk_bf16_f32 v120, v120, v121\n"
"v_mfma_f32_16x16x32_bf16 v[96:99], a[224:227], a[184:187], v[96:99]\n"
"s_waitcnt lgkmcnt(0)\n"
"s_add_u32 m0, s44, 32768\n"
"v_cvt_pk_bf16_f32 v209, v210, v211\n"
"v_cvt_pk_bf16_f32 v210, v212, v213\n"
"v_mfma_f32_16x16x32_bf16 v[96:99], a[228:231], a[188:191], v[96:99]\n"
"buffer_load_dwordx4 v240, s[16:19], s56 offen nt lds\n"
"v_mfma_f32_16x16x32_bf16 v[100:103], a[232:235], a[184:187], v[100:103]\n"
"s_waitcnt vmcnt(14)\n"
"v_cvt_pk_bf16_f32 v128, v128, v129\n"
"v_cvt_pk_bf16_f32 v129, v130, v131\n"
"v_mfma_f32_16x16x32_bf16 v[100:103], a[236:239], a[188:191], v[100:103]\n"
"s_barrier\n"
"v_mfma_f32_16x16x32_bf16 v[104:107], a[240:243], a[184:187], v[104:107]\n"
"ds_read_b128 a[196:199], v198 offset:0\n"
"v_cvt_pk_bf16_f32 v211, v214, v215\n"
"v_mfma_f32_16x16x32_bf16 v[104:107], a[244:247], a[188:191], v[104:107]\n"
"s_mov_b32 s74, 0x41400000\n"
"v_cvt_pk_bf16_f32 v138, v140, v141\n"
"v_cvt_pk_bf16_f32 v139, v142, v143\n"
"v_mfma_f32_16x16x32_bf16 v[108:111], a[248:251], a[184:187], v[108:111]\n"
"ds_read_b128 a[200:203], v243 offset:2048\n"
"v_cvt_pk_bf16_f32 v162, v164, v165\n"
"v_mfma_f32_16x16x32_bf16 v[108:111], a[252:255], a[188:191], v[108:111]\n"
"v_pk_fma_f32 v[144:145], v[144:145], s[70:71], s[70:71] op_sel:[0,0,1] op_sel_hi:[1,0,1]\nv_pk_fma_f32 v[146:147], v[146:147], s[70:71], s[70:71] op_sel:[0,0,1] op_sel_hi:[1,0,1]\nv_pk_fma_f32 v[148:149], v[148:149], s[70:71], s[70:71] op_sel:[0,0,1] op_sel_hi:[1,0,1]\nv_pk_fma_f32 v[150:151], v[150:151], s[70:71], s[70:71] op_sel:[0,0,1] op_sel_hi:[1,0,1]\nv_pk_fma_f32 v[152:153], v[152:153], s[70:71], s[70:71] op_sel:[0,0,1] op_sel_hi:[1,0,1]\nv_pk_fma_f32 v[154:155], v[154:155], s[70:71], s[70:71] op_sel:[0,0,1] op_sel_hi:[1,0,1]\nv_pk_fma_f32 v[156:157], v[156:157], s[70:71], s[70:71] op_sel:[0,0,1] op_sel_hi:[1,0,1]\nv_pk_fma_f32 v[158:159], v[158:159], s[70:71], s[70:71] op_sel:[0,0,1] op_sel_hi:[1,0,1]\n"
"v_mfma_f32_16x16x32_bf16 v[92:95], a[248:251], a[176:179], v[92:95]\n"
"v_permlane16_swap_b32 v161, v163\n"
"v_mfma_f32_16x16x32_bf16 v[92:95], a[252:255], a[180:183], v[92:95]\n"
"ds_write2st64_b64 v255, v[200:201], v[202:203] offset0:102 offset1:103\n"
"v_mfma_f32_16x16x32_bf16 v[88:91], a[240:243], a[176:179], v[88:91]\n"
"v_cvt_pk_bf16_f32 v144, v144, v145\n"
"v_cvt_pk_bf16_f32 v152, v152, v153\n"
"v_mfma_f32_16x16x32_bf16 v[88:91], a[244:247], a[180:183], v[88:91]\n"
"v_cvt_pk_bf16_f32 v145, v146, v147\n"
"v_cvt_pk_bf16_f32 v153, v154, v155\n"
"v_mfma_f32_16x16x32_bf16 v[84:87], a[232:235], a[176:179], v[84:87]\n"
"v_cvt_pk_bf16_f32 v130, v132, v133\n"
"v_cvt_pk_bf16_f32 v131, v134, v135\n"
"v_mfma_f32_16x16x32_bf16 v[84:87], a[236:239], a[180:183], v[84:87]\n"
"ds_write2st64_b64 v255, v[176:177], v[178:179] offset0:96 offset1:97\n"
"v_mfma_f32_16x16x32_bf16 v[80:83], a[224:227], a[176:179], v[80:83]\n"
"s_add_u32 s40, s56, s52\n"
"v_permlane16_swap_b32 v160, v162\n"
"v_mfma_f32_16x16x32_bf16 v[80:83], a[228:231], a[180:183], v[80:83]\n"
"buffer_load_dwordx4 v248, s[16:19], s40 offen offset:1024 nt lds\n"
"v_mfma_f32_16x16x32_bf16 v[64:67], a[224:227], a[168:171], v[64:67]\n"
"v_cvt_pk_bf16_f32 v218, v220, v221\n"
"v_cvt_pk_bf16_f32 v154, v156, v157\n"
"v_mfma_f32_16x16x32_bf16 v[64:67], a[228:231], a[172:175], v[64:67]\n"
"v_permlane16_swap_b32 v128, v130\n"
"v_mfma_f32_16x16x32_bf16 v[68:71], a[232:235], a[168:171], v[68:71]\n"
"ds_read_b128 v[212:215], v197 offset:0\n"
"v_cvt_pk_bf16_f32 v155, v158, v159\n"
"v_mfma_f32_16x16x32_bf16 v[68:71], a[236:239], a[172:175], v[68:71]\n"
"v_pk_fma_f32 v[96:97], v[96:97], s[70:71], s[70:71] op_sel:[0,0,1] op_sel_hi:[1,0,1]\nv_pk_fma_f32 v[98:99], v[98:99], s[70:71], s[70:71] op_sel:[0,0,1] op_sel_hi:[1,0,1]\nv_pk_fma_f32 v[100:101], v[100:101], s[70:71], s[70:71] op_sel:[0,0,1] op_sel_hi:[1,0,1]\nv_pk_fma_f32 v[102:103], v[102:103], s[70:71], s[70:71] op_sel:[0,0,1] op_sel_hi:[1,0,1]\nv_pk_fma_f32 v[104:105], v[104:105], s[70:71], s[70:71] op_sel:[0,0,1] op_sel_hi:[1,0,1]\nv_pk_fma_f32 v[106:107], v[106:107], s[70:71], s[70:71] op_sel:[0,0,1] op_sel_hi:[1,0,1]\nv_pk_fma_f32 v[108:109], v[108:109], s[70:71], s[70:71] op_sel:[0,0,1] op_sel_hi:[1,0,1]\nv_pk_fma_f32 v[110:111], v[110:111], s[70:71], s[70:71] op_sel:[0,0,1] op_sel_hi:[1,0,1]\nv_pk_fma_f32 v[80:81], v[80:81], s[70:71], s[70:71] op_sel:[0,0,1] op_sel_hi:[1,0,1]\nv_pk_fma_f32 v[82:83], v[82:83], s[70:71], s[70:71] op_sel:[0,0,1] op_sel_hi:[1,0,1]\nv_pk_fma_f32 v[84:85], v[84:85], s[70:71], s[70:71] op_sel:[0,0,1] op_sel_hi:[1,0,1]\nv_pk_fma_f32 v[86:87], v[86:87], s[70:71], s[70:71] op_sel:[0,0,1] op_sel_hi:[1,0,1]\nv_pk_fma_f32 v[88:89], v[88:89], s[70:71], s[70:71] op_sel:[0,0,1] op_sel_hi:[1,0,1]\nv_pk_fma_f32 v[90:91], v[90:91], s[70:71], s[70:71] op_sel:[0,0,1] op_sel_hi:[1,0,1]\nv_pk_fma_f32 v[92:93], v[92:93], s[70:71], s[70:71] op_sel:[0,0,1] op_sel_hi:[1,0,1]\nv_pk_fma_f32 v[94:95], v[94:95], s[70:71], s[70:71] op_sel:[0,0,1] op_sel_hi:[1,0,1]\n"
"v_mfma_f32_16x16x32_bf16 v[72:75], a[240:243], a[168:171], v[72:75]\n"
"v_permlane16_swap_b32 v137, v139\n"
"v_mfma_f32_16x16x32_bf16 v[72:75], a[244:247], a[172:175], v[72:75]\n"
"ds_write2st64_b64 v255, v[184:185], v[186:187] offset0:98 offset1:99\n"
"v_mfma_f32_16x16x32_bf16 v[76:79], a[248:251], a[168:171], v[76:79]\n"
"v_cvt_pk_bf16_f32 v121, v122, v123\n"
"v_cvt_pk_bf16_f32 v96, v96, v97\n"
"v_mfma_f32_16x16x32_bf16 v[76:79], a[252:255], a[172:175], v[76:79]\n"
"ds_read_b128 a[192:195], v243 offset:0\n"
"v_mov_b32 v199, 0\n"
"v_mfma_f32_16x16x32_bf16 v[60:63], a[248:251], a[160:163], v[60:63]\n"
"v_cvt_pk_bf16_f32 v104, v104, v105\n"
"v_cvt_pk_bf16_f32 v105, v106, v107\n"
"v_mfma_f32_16x16x32_bf16 v[60:63], a[252:255], a[164:167], v[60:63]\n"
"ds_write2st64_b64 v255, v[208:209], v[210:211] offset0:192 offset1:193\n"
"v_mfma_f32_16x16x32_bf16 v[56:59], a[240:243], a[160:163], v[56:59]\n"
"s_add_u32 s40, s56, s53\n"
"v_accvgpr_write_b32 a121, v137\n"
"v_cvt_pk_bf16_f32 v146, v148, v149\n"
"v_mfma_f32_16x16x32_bf16 v[56:59], a[244:247], a[164:167], v[56:59]\n"
"buffer_load_dwordx4 v240, s[16:19], s40 offen offset:2048 nt lds\n"
"v_mfma_f32_16x16x32_bf16 v[52:55], a[232:235], a[160:163], v[52:55]\n"
"v_cvt_pk_bf16_f32 v97, v98, v99\n"
"v_cvt_pk_bf16_f32 v88, v88, v89\n"
"v_mfma_f32_16x16x32_bf16 v[52:55], a[236:239], a[164:167], v[52:55]\n"
"v_cvt_pk_bf16_f32 v106, v108, v109\n"
"v_mov_b32 v204, 0\n"
"v_mfma_f32_16x16x32_bf16 v[48:51], a[224:227], a[160:163], v[48:51]\n"
"v_cvt_pk_bf16_f32 v219, v222, v223\n"
"v_mov_b32 v205, 0\n"
"v_mfma_f32_16x16x32_bf16 v[48:51], a[228:231], a[164:167], v[48:51]\n"
"ds_write_b128 v243, v[160:163] offset:8192\n"
"v_cvt_pk_bf16_f32 v170, v172, v173\n"
"v_mfma_f32_16x16x32_bf16 v[0:3], a[224:227], a[136:139], v[0:3]\n"
"v_permlane16_swap_b32 v153, v155\n"
"v_mfma_f32_16x16x32_bf16 v[0:3], a[228:231], a[140:143], v[0:3]\n"
"ds_write2st64_b64 v255, v[216:217], v[218:219] offset0:194 offset1:195\n"
"v_mfma_f32_16x16x32_bf16 v[4:7], a[232:235], a[136:139], v[4:7]\n"
"v_permlane16_swap_b32 v169, v171\n"
"v_mfma_f32_16x16x32_bf16 v[4:7], a[236:239], a[140:143], v[4:7]\n"
"v_pk_fma_f32 v[64:65], v[64:65], s[70:71], s[70:71] op_sel:[0,0,1] op_sel_hi:[1,0,1]\nv_pk_fma_f32 v[66:67], v[66:67], s[70:71], s[70:71] op_sel:[0,0,1] op_sel_hi:[1,0,1]\nv_pk_fma_f32 v[68:69], v[68:69], s[70:71], s[70:71] op_sel:[0,0,1] op_sel_hi:[1,0,1]\nv_pk_fma_f32 v[70:71], v[70:71], s[70:71], s[70:71] op_sel:[0,0,1] op_sel_hi:[1,0,1]\nv_pk_fma_f32 v[72:73], v[72:73], s[70:71], s[70:71] op_sel:[0,0,1] op_sel_hi:[1,0,1]\nv_pk_fma_f32 v[74:75], v[74:75], s[70:71], s[70:71] op_sel:[0,0,1] op_sel_hi:[1,0,1]\nv_pk_fma_f32 v[76:77], v[76:77], s[70:71], s[70:71] op_sel:[0,0,1] op_sel_hi:[1,0,1]\nv_pk_fma_f32 v[78:79], v[78:79], s[70:71], s[70:71] op_sel:[0,0,1] op_sel_hi:[1,0,1]\nv_pk_fma_f32 v[48:49], v[48:49], s[70:71], s[70:71] op_sel:[0,0,1] op_sel_hi:[1,0,1]\nv_pk_fma_f32 v[50:51], v[50:51], s[70:71], s[70:71] op_sel:[0,0,1] op_sel_hi:[1,0,1]\nv_pk_fma_f32 v[52:53], v[52:53], s[70:71], s[70:71] op_sel:[0,0,1] op_sel_hi:[1,0,1]\nv_pk_fma_f32 v[54:55], v[54:55], s[70:71], s[70:71] op_sel:[0,0,1] op_sel_hi:[1,0,1]\nv_pk_fma_f32 v[56:57], v[56:57], s[70:71], s[70:71] op_sel:[0,0,1] op_sel_hi:[1,0,1]\nv_pk_fma_f32 v[58:59], v[58:59], s[70:71], s[70:71] op_sel:[0,0,1] op_sel_hi:[1,0,1]\nv_pk_fma_f32 v[60:61], v[60:61], s[70:71], s[70:71] op_sel:[0,0,1] op_sel_hi:[1,0,1]\nv_pk_fma_f32 v[62:63], v[62:63], s[70:71], s[70:71] op_sel:[0,0,1] op_sel_hi:[1,0,1]\n"
"v_mfma_f32_16x16x32_bf16 v[8:11], a[240:243], a[136:139], v[8:11]\n"
"ds_read_b128 a[204:207], v198 offset:2048\n"
"v_cvt_pk_bf16_f32 v56, v56, v57\n"
"v_mfma_f32_16x16x32_bf16 v[8:11], a[244:247], a[140:143], v[8:11]\n"
"v_permlane16_swap_b32 v152, v154\n"
"v_mfma_f32_16x16x32_bf16 v[12:15], a[248:251], a[136:139], v[12:15]\n"
"v_permlane16_swap_b32 v144, v146\n"
"v_mfma_f32_16x16x32_bf16 v[12:15], a[252:255], a[140:143], v[12:15]\n"
"v_accvgpr_write_b32 a137, v153\n"
"v_cvt_pk_bf16_f32 v80, v80, v81\n"
"v_mfma_f32_16x16x32_bf16 v[28:31], a[248:251], a[144:147], v[28:31]\n"
"s_add_u32 s40, s56, s54\n"
"ds_read_b128 v[208:211], v196 offset:0\n"
"v_cvt_pk_bf16_f32 v147, v150, v151\n"
"v_mfma_f32_16x16x32_bf16 v[28:31], a[252:255], a[148:151], v[28:31]\n"
"buffer_load_dwordx4 v248, s[16:19], s40 offen offset:3072 nt lds\n"
"v_mfma_f32_16x16x32_bf16 v[24:27], a[240:243], a[144:147], v[24:27]\n"
"v_cvt_pk_bf16_f32 v107, v110, v111\n"
"v_cvt_pk_bf16_f32 v48, v48, v49\n"
"v_mfma_f32_16x16x32_bf16 v[24:27], a[244:247], a[148:151], v[24:27]\n"
"v_permlane16_swap_b32 v129, v131\n"
"v_mfma_f32_16x16x32_bf16 v[20:23], a[232:235], a[144:147], v[20:23]\n"
"v_permlane16_swap_b32 v136, v138\n"
"v_mfma_f32_16x16x32_bf16 v[20:23], a[236:239], a[148:151], v[20:23]\n"
"ds_read_b128 v[216:219], v196 offset:512\n"
"v_cvt_pk_bf16_f32 v64, v64, v65\n"
"v_mfma_f32_16x16x32_bf16 v[16:19], a[224:227], a[144:147], v[16:19]\n"
"v_cvt_pk_bf16_f32 v122, v124, v125\n"
"v_cvt_pk_bf16_f32 v72, v72, v73\n"
"v_mfma_f32_16x16x32_bf16 v[16:19], a[228:231], a[148:151], v[16:19]\n"
"v_accvgpr_write_b32 a113, v129\n"
"v_accvgpr_write_b32 a122, v138\n"
"v_mfma_f32_16x16x32_bf16 v[32:35], a[224:227], a[152:155], v[32:35]\n"
"v_cvt_pk_bf16_f32 v98, v100, v101\n"
"v_cvt_pk_bf16_f32 v65, v66, v67\n"
"v_mfma_f32_16x16x32_bf16 v[32:35], a[228:231], a[156:159], v[32:35]\n"
"ds_read_b128 v[220:223], v197 offset:512\n"
"v_accvgpr_write_b32 a138, v154\n"
"v_mfma_f32_16x16x32_bf16 v[36:39], a[232:235], a[152:155], v[36:39]\n"
"v_accvgpr_write_b32 a136, v152\n"
"v_cvt_pk_bf16_f32 v89, v90, v91\n"
"v_mfma_f32_16x16x32_bf16 v[36:39], a[236:239], a[156:159], v[36:39]\n"
"v_permlane16_swap_b32 v145, v147\n"
"v_mfma_f32_16x16x32_bf16 v[40:43], a[240:243], a[152:155], v[40:43]\n"
"v_accvgpr_write_b32 a128, v144\n"
"v_cvt_pk_bf16_f32 v99, v102, v103\n"
"v_mfma_f32_16x16x32_bf16 v[40:43], a[244:247], a[156:159], v[40:43]\n"
"ds_write2st64_b64 v255, v[192:193], v[194:195] offset0:100 offset1:101\n"
"v_pk_fma_f32 v[0:1], v[0:1], s[70:71], s[70:71] op_sel:[0,0,1] op_sel_hi:[1,0,1]\nv_pk_fma_f32 v[2:3], v[2:3], s[70:71], s[70:71] op_sel:[0,0,1] op_sel_hi:[1,0,1]\nv_pk_fma_f32 v[4:5], v[4:5], s[70:71], s[70:71] op_sel:[0,0,1] op_sel_hi:[1,0,1]\nv_pk_fma_f32 v[6:7], v[6:7], s[70:71], s[70:71] op_sel:[0,0,1] op_sel_hi:[1,0,1]\nv_pk_fma_f32 v[8:9], v[8:9], s[70:71], s[70:71] op_sel:[0,0,1] op_sel_hi:[1,0,1]\nv_pk_fma_f32 v[10:11], v[10:11], s[70:71], s[70:71] op_sel:[0,0,1] op_sel_hi:[1,0,1]\nv_pk_fma_f32 v[12:13], v[12:13], s[70:71], s[70:71] op_sel:[0,0,1] op_sel_hi:[1,0,1]\nv_pk_fma_f32 v[14:15], v[14:15], s[70:71], s[70:71] op_sel:[0,0,1] op_sel_hi:[1,0,1]\nv_pk_fma_f32 v[16:17], v[16:17], s[70:71], s[70:71] op_sel:[0,0,1] op_sel_hi:[1,0,1]\nv_pk_fma_f32 v[18:19], v[18:19], s[70:71], s[70:71] op_sel:[0,0,1] op_sel_hi:[1,0,1]\nv_pk_fma_f32 v[20:21], v[20:21], s[70:71], s[70:71] op_sel:[0,0,1] op_sel_hi:[1,0,1]\nv_pk_fma_f32 v[22:23], v[22:23], s[70:71], s[70:71] op_sel:[0,0,1] op_sel_hi:[1,0,1]\nv_pk_fma_f32 v[24:25], v[24:25], s[70:71], s[70:71] op_sel:[0,0,1] op_sel_hi:[1,0,1]\nv_pk_fma_f32 v[26:27], v[26:27], s[70:71], s[70:71] op_sel:[0,0,1] op_sel_hi:[1,0,1]\nv_pk_fma_f32 v[28:29], v[28:29], s[70:71], s[70:71] op_sel:[0,0,1] op_sel_hi:[1,0,1]\nv_pk_fma_f32 v[30:31], v[30:31], s[70:71], s[70:71] op_sel:[0,0,1] op_sel_hi:[1,0,1]\n"
"v_mfma_f32_16x16x32_bf16 v[44:47], a[248:251], a[152:155], v[44:47]\n"
"v_accvgpr_write_b32 a115, v131\n"
"v_accvgpr_write_b32 a120, v136\n"
"v_mfma_f32_16x16x32_bf16 v[44:47], a[252:255], a[156:159], v[44:47]\n"
"s_waitcnt lgkmcnt(2)\n"
"ds_write2st64_b64 v255, v[96:97], v[98:99] offset0:228 offset1:229\n"
"v_cvt_pk_bf16_f32 v81, v82, v83\n"
"v_mfma_f32_16x16x32_bf16 v[180:183], v[208:211], a[0:3], 0\n"
"v_accvgpr_write_b32 a112, v128\n"
"v_cvt_pk_bf16_f32 v91, v94, v95\n"
"v_mfma_f32_16x16x32_bf16 v[180:183], v[212:215], a[4:7], v[180:183]\n"
"v_permlane16_swap_b32 v168, v170\n"
"v_mfma_f32_16x16x32_bf16 v[180:183], v[216:219], a[8:11], v[180:183]\n"
"s_waitcnt lgkmcnt(2)\n"
"v_cvt_pk_bf16_f32 v73, v74, v75\n"
"v_cvt_pk_bf16_f32 v8, v8, v9\n"
"v_mfma_f32_16x16x32_bf16 v[180:183], v[220:223], a[12:15], v[180:183]\n"
"ds_write_b128 v243, v[168:171] offset:10240\n"
"v_pk_fma_f32 v[32:33], v[32:33], s[70:71], s[70:71] op_sel:[0,0,1] op_sel_hi:[1,0,1]\nv_pk_fma_f32 v[34:35], v[34:35], s[70:71], s[70:71] op_sel:[0,0,1] op_sel_hi:[1,0,1]\nv_pk_fma_f32 v[36:37], v[36:37], s[70:71], s[70:71] op_sel:[0,0,1] op_sel_hi:[1,0,1]\nv_pk_fma_f32 v[38:39], v[38:39], s[70:71], s[70:71] op_sel:[0,0,1] op_sel_hi:[1,0,1]\nv_pk_fma_f32 v[40:41], v[40:41], s[70:71], s[70:71] op_sel:[0,0,1] op_sel_hi:[1,0,1]\nv_pk_fma_f32 v[42:43], v[42:43], s[70:71], s[70:71] op_sel:[0,0,1] op_sel_hi:[1,0,1]\nv_pk_fma_f32 v[44:45], v[44:45], s[70:71], s[70:71] op_sel:[0,0,1] op_sel_hi:[1,0,1]\nv_pk_fma_f32 v[46:47], v[46:47], s[70:71], s[70:71] op_sel:[0,0,1] op_sel_hi:[1,0,1]\n"
"v_mfma_f32_16x16x32_bf16 v[184:187], v[208:211], a[16:19], 0\n"
"v_cvt_pk_bf16_f32 v49, v50, v51\n"
"v_cvt_pk_bf16_f32 v51, v54, v55\n"
"v_mfma_f32_16x16x32_bf16 v[184:187], v[212:215], a[20:23], v[184:187]\n"
"v_permlane16_swap_b32 v120, v122\n"
"v_mfma_f32_16x16x32_bf16 v[184:187], v[216:219], a[24:27], v[184:187]\n"
"v_cvt_pk_bf16_f32 v90, v92, v93\n"
"v_sub_f32 v183, s74, v183\n"
"v_mfma_f32_16x16x32_bf16 v[184:187], v[220:223], a[28:31], v[184:187]\n"
"ds_write2st64_b64 v255, v[104:105], v[106:107] offset0:230 offset1:231\n"
"v_cvt_pk_bf16_f32 v67, v70, v71\n"
"v_sub_f32 v182, s74, v182\n"
"v_mfma_f32_16x16x32_bf16 v[188:191], v[208:211], a[32:35], 0\n"
"v_cvt_pk_bf16_f32 v40, v40, v41\n"
"v_sub_f32 v180, s74, v180\n"
"v_mfma_f32_16x16x32_bf16 v[188:191], v[212:215], a[36:39], v[188:191]\n"
"v_accvgpr_write_b32 a139, v155\n"
"v_sub_f32 v181, s74, v181\n"
"v_mfma_f32_16x16x32_bf16 v[188:191], v[216:219], a[40:43], v[188:191]\n"
"v_cvt_pk_bf16_f32 v9, v10, v11\n"
"v_sub_f32 v186, s74, v186\n"
"v_mfma_f32_16x16x32_bf16 v[188:191], v[220:223], a[44:47], v[188:191]\n"
"v_cvt_pk_bf16_f32 v83, v86, v87\n"
"v_cvt_pk_bf16_f32 v57, v58, v59\n"
"v_mfma_f32_16x16x32_bf16 v[192:195], v[208:211], a[192:195], 0\n"
"ds_read_b128 v[208:211], v228 offset:0\n"
"v_sub_f32 v185, s74, v185\n"
"v_mfma_f32_16x16x32_bf16 v[192:195], v[212:215], a[196:199], v[192:195]\n"
"ds_read_b128 v[212:215], v229 offset:0\n"
"v_cvt_pk_bf16_f32 v41, v42, v43\n"
"v_mfma_f32_16x16x32_bf16 v[192:195], v[216:219], a[200:203], v[192:195]\n"
"ds_read_b128 v[216:219], v228 offset:512\n"
"v_sub_f32 v189, s74, v189\n"
"v_mfma_f32_16x16x32_bf16 v[192:195], v[220:223], a[204:207], v[192:195]\n"
"s_waitcnt lgkmcnt(2)\n"
"ds_read_b128 v[220:223], v229 offset:512\n"
"v_sub_f32 v184, s74, v184\n"
"v_mfma_f32_16x16x32_bf16 v[152:155], v[208:211], a[0:3], v[180:183]\n"
"s_waitcnt lgkmcnt(2)\n"
"v_cvt_pk_bf16_f32 v42, v44, v45\n"
"v_sub_f32 v187, s74, v187\n"
"v_mfma_f32_16x16x32_bf16 v[152:155], v[212:215], a[4:7], v[152:155]\n"
"s_waitcnt lgkmcnt(1)\n"
"v_accvgpr_write_b32 a129, v145\n"
"v_cvt_pk_bf16_f32 v82, v84, v85\n"
"v_mfma_f32_16x16x32_bf16 v[152:155], v[216:219], a[8:11], v[152:155]\n"
"s_waitcnt lgkmcnt(0)\n"
"v_cvt_pk_bf16_f32 v58, v60, v61\n"
"v_sub_f32 v188, s74, v188\n"
"v_mfma_f32_16x16x32_bf16 v[152:155], v[220:223], a[12:15], v[152:155]\n"
"v_cvt_pk_bf16_f32 v59, v62, v63\n"
"v_sub_f32 v190, s74, v190\n"
"v_mfma_f32_16x16x32_bf16 v[156:159], v[216:219], a[24:27], v[184:187]\n"
"v_cvt_pk_bf16_f32 v11, v14, v15\n"
"v_cvt_pk_bf16_f32 v43, v46, v47\n"
"v_mfma_f32_16x16x32_bf16 v[156:159], v[220:223], a[28:31], v[156:159]\n"
"v_sub_f32 v191, s74, v191\n"
"v_sub_f32 v192, s74, v192\n"
"v_mfma_f32_16x16x32_bf16 v[156:159], v[208:211], a[16:19], v[156:159]\n"
"v_cvt_pk_bf16_f32 v123, v126, v127\n"
"v_cvt_pk_bf16_f32 v10, v12, v13\n"
"v_mfma_f32_16x16x32_bf16 v[156:159], v[212:215], a[20:23], v[156:159]\n"
"ds_write2st64_b64 v255, v[88:89], v[90:91] offset0:226 offset1:227\n"
"v_permlane16_swap_b32 v57, v59\n"
"v_cvt_pk_bf16_f32 v16, v16, v17\n"
"v_accvgpr_write_b32 a241, v254\n"
"v_mov_b32 v196, 0\n"
"v_sub_f32 v193, s74, v193\n"
"v_exp_f32 v153, v153\n"
"v_mfma_f32_16x16x32_bf16 v[160:163], v[208:211], a[32:35], v[188:191]\n"
"v_sub_f32 v194, s74, v194\n"
"v_sub_f32 v195, s74, v195\n"
"v_mfma_f32_16x16x32_bf16 v[160:163], v[212:215], a[36:39], v[160:163]\n"
"v_exp_f32 v152, v152\n"
"v_mfma_f32_16x16x32_bf16 v[160:163], v[216:219], a[40:43], v[160:163]\n"
"v_exp_f32 v158, v158\n"
"v_mfma_f32_16x16x32_bf16 v[160:163], v[220:223], a[44:47], v[160:163]\n"
"v_exp_f32 v156, v156\n"
"v_mfma_f32_16x16x32_bf16 v[164:167], v[216:219], a[200:203], v[192:195]\n"
"ds_read_b128 v[216:219], v228 offset:4608\n"
"v_cvt_pk_bf16_f32 v74, v76, v77\n"
"v_mfma_f32_16x16x32_bf16 v[164:167], v[220:223], a[204:207], v[164:167]\n"
"ds_read_b128 v[220:223], v229 offset:4608\n"
"v_cvt_pk_bf16_f32 v66, v68, v69\n"
"v_mfma_f32_16x16x32_bf16 v[164:167], v[208:211], a[192:195], v[164:167]\n"
"ds_read_b128 v[208:211], v228 offset:4096\n"
"v_cvt_pk_bf16_f32 v24, v24, v25\n"
"v_mfma_f32_16x16x32_bf16 v[164:167], v[212:215], a[196:199], v[164:167]\n"
"s_waitcnt lgkmcnt(2)\n"
"ds_read_b128 v[212:215], v229 offset:4096\n"
"v_accvgpr_write_b32 a114, v130\n"
"v_mfma_f32_16x16x32_bf16 v[168:171], v[216:219], a[8:11], v[180:183]\n"
"s_waitcnt lgkmcnt(2)\n"
"v_exp_f32 v163, v163\n"
"v_mfma_f32_16x16x32_bf16 v[168:171], v[220:223], a[12:15], v[168:171]\n"
"s_waitcnt lgkmcnt(1)\n"
"v_exp_f32 v162, v162\n"
"v_mfma_f32_16x16x32_bf16 v[168:171], v[208:211], a[0:3], v[168:171]\n"
"s_waitcnt lgkmcnt(0)\n"
"v_exp_f32 v160, v160\n"
"v_mfma_f32_16x16x32_bf16 v[168:171], v[212:215], a[4:7], v[168:171]\n"
"ds_write2st64_b64 v255, v[80:81], v[82:83] offset0:224 offset1:225\n"
"v_permlane16_swap_b32 v121, v123\n"
"v_accvgpr_write_b32 a131, v147\n"
"v_exp_f32 v154, v154\n"
"v_exp_f32 v157, v157\n"
"v_exp_f32 v159, v159\n"
"v_exp_f32 v161, v161\n"
"v_exp_f32 v167, v167\n"
"v_mfma_f32_16x16x32_bf16 v[172:175], v[208:211], a[16:19], v[184:187]\n"
"v_exp_f32 v165, v165\n"
"v_mfma_f32_16x16x32_bf16 v[172:175], v[212:215], a[20:23], v[172:175]\n"
"v_exp_f32 v166, v166\n"
"v_mfma_f32_16x16x32_bf16 v[172:175], v[216:219], a[24:27], v[172:175]\n"
"v_exp_f32 v169, v169\n"
"v_mfma_f32_16x16x32_bf16 v[172:175], v[220:223], a[28:31], v[172:175]\n"
"v_exp_f32 v155, v155\n"
"v_mfma_f32_16x16x32_bf16 v[176:179], v[216:219], a[40:43], v[188:191]\n"
"v_cvt_pk_bf16_f32 v128, v152, v153\n"
"v_bfe_u32 v248, v242, 9, 1\n"
"v_mfma_f32_16x16x32_bf16 v[176:179], v[220:223], a[44:47], v[176:179]\n"
"v_exp_f32 v171, v171\n"
"v_mfma_f32_16x16x32_bf16 v[176:179], v[208:211], a[32:35], v[176:179]\n"
"v_cvt_pk_bf16_f32 v129, v154, v155\n"
"v_and_b32 v249, 0xf0, v242\n"
"v_mfma_f32_16x16x32_bf16 v[176:179], v[212:215], a[36:39], v[176:179]\n"
"ds_write2st64_b64 v255, v[64:65], v[66:67] offset0:196 offset1:197\n"
"v_cvt_pk_bf16_f32 v136, v156, v157\n"
"v_cvt_pk_bf16_f32 v137, v158, v159\n"
"v_cvt_pk_bf16_f32 v145, v162, v163\n"
"v_exp_f32 v170, v170\n"
"v_mfma_f32_16x16x32_bf16 v[152:155], v[208:211], a[192:195], v[192:195]\n"
"ds_read_b128 v[208:211], v230 offset:0\n"
"v_cvt_pk_bf16_f32 v125, v166, v167\n"
"v_mfma_f32_16x16x32_bf16 v[152:155], v[212:215], a[196:199], v[152:155]\n"
"ds_read_b128 v[212:215], v231 offset:0\n"
"v_accvgpr_write_b32 a123, v139\n"
"v_mfma_f32_16x16x32_bf16 v[152:155], v[216:219], a[200:203], v[152:155]\n"
"ds_read_b128 v[216:219], v230 offset:512\n"
"v_cvt_pk_bf16_f32 v50, v52, v53\n"
"v_mfma_f32_16x16x32_bf16 v[152:155], v[220:223], a[204:207], v[152:155]\n"
"s_waitcnt lgkmcnt(2)\n"
"ds_read_b128 v[220:223], v231 offset:512\n"
"v_permlane16_swap_b32 v40, v42\n"
"v_cvt_pk_bf16_f32 v144, v160, v161\n"
"v_exp_f32 v164, v164\n"
"v_exp_f32 v168, v168\n"
"v_cvt_pk_bf16_f32 v131, v170, v171\n"
"v_exp_f32 v172, v172\n"
"v_exp_f32 v173, v173\n"
"v_exp_f32 v174, v174\n"
"v_exp_f32 v176, v176\n"
"v_exp_f32 v179, v179\n"
"v_lshl_add_u32 v248, v248, 8, v249\n"
"v_mfma_f32_16x16x32_bf16 v[156:159], v[208:211], a[0:3], v[180:183]\n"
"s_waitcnt lgkmcnt(2)\n"
"v_cvt_pk_bf16_f32 v75, v78, v79\n"
"v_cvt_pk_bf16_f32 v130, v168, v169\n"
"v_mfma_f32_16x16x32_bf16 v[156:159], v[212:215], a[4:7], v[156:159]\n"
"s_waitcnt lgkmcnt(1)\n"
"v_bfe_u32 v249, v242, 8, 1\n"
"v_lshl_add_u32 v248, v249, 9, v248\n"
"v_mfma_f32_16x16x32_bf16 v[156:159], v[216:219], a[8:11], v[156:159]\n"
"s_waitcnt lgkmcnt(0)\n"
"v_exp_f32 v177, v177\n"
"v_mfma_f32_16x16x32_bf16 v[156:159], v[220:223], a[12:15], v[156:159]\n"
"v_mov_b32 v202, 0\n"
"v_cvt_pk_bf16_f32 v124, v164, v165\n"
"v_mfma_f32_16x16x32_bf16 v[160:163], v[216:219], a[24:27], v[184:187]\n"
"v_exp_f32 v155, v155\n"
"v_mfma_f32_16x16x32_bf16 v[160:163], v[220:223], a[28:31], v[160:163]\n"
"v_permlane16_swap_b32 v49, v51\n"
"v_mfma_f32_16x16x32_bf16 v[160:163], v[208:211], a[16:19], v[160:163]\n"
"v_mov_b32 v201, 0\n"
"v_xor_b32_e64 v249, s42, 0\n"
"v_mfma_f32_16x16x32_bf16 v[160:163], v[212:215], a[20:23], v[160:163]\n"
"ds_write2st64_b64 v255, v[72:73], v[74:75] offset0:198 offset1:199\n"
"v_permlane16_swap_b32 v48, v50\n"
"v_permlane16_swap_b32 v56, v58\n"
"v_cvt_pk_bf16_f32 v25, v26, v27\n"
"v_cvt_pk_bf16_f32 v138, v172, v173\n"
"v_exp_f32 v153, v153\n"
"v_exp_f32 v157, v157\n"
"v_mfma_f32_16x16x32_bf16 v[164:167], v[208:211], a[32:35], v[188:191]\n"
"ds_read_b128 v[208:211], v230 offset:4096\n"
"v_mov_b32 v197, 0\n"
"v_mfma_f32_16x16x32_bf16 v[164:167], v[212:215], a[36:39], v[164:167]\n"
"ds_read_b128 v[212:215], v231 offset:4096\n"
"v_accvgpr_write_b32 a243, v254\n"
"v_mfma_f32_16x16x32_bf16 v[164:167], v[216:219], a[40:43], v[164:167]\n"
"ds_read_b128 v[216:219], v230 offset:4608\n"
"v_lshl_add_u32 v248, v249, 12, v248\n"
"v_mfma_f32_16x16x32_bf16 v[164:167], v[220:223], a[44:47], v[164:167]\n"
"s_waitcnt lgkmcnt(2)\n"
"ds_read_b128 v[220:223], v231 offset:4608\n"
"v_accvgpr_write_b32 a104, v120\n"
"v_permlane16_swap_b32 v8, v10\n"
"v_permlane16_swap_b32 v9, v11\n"
"v_accvgpr_write_b32 a240, v254\n"
"v_exp_f32 v175, v175\n"
"v_exp_f32 v178, v178\n"
"v_exp_f32 v159, v159\n"
"v_exp_f32 v160, v160\n"
"v_exp_f32 v163, v163\n"
"v_mfma_f32_16x16x32_bf16 v[168:171], v[208:211], a[0:3], v[180:183]\n"
"s_waitcnt lgkmcnt(2)\n"
"v_exp_f32 v154, v154\n"
"v_mfma_f32_16x16x32_bf16 v[168:171], v[212:215], a[4:7], v[168:171]\n"
"s_waitcnt lgkmcnt(1)\n"
"v_accvgpr_write_b32 a186, v42\n"
"v_add_u32 v249, s75, v248\n"
"v_mfma_f32_16x16x32_bf16 v[168:171], v[216:219], a[8:11], v[168:171]\n"
"s_waitcnt lgkmcnt(0)\n"
"v_exp_f32 v152, v152\n"
"v_mfma_f32_16x16x32_bf16 v[168:171], v[220:223], a[12:15], v[168:171]\n"
"ds_write_b128 v243, v[48:51] offset:12288\n"
"v_accvgpr_write_b32 a130, v146\n"
"v_permlane16_swap_b32 v41, v43\n"
"v_cvt_pk_bf16_f32 v139, v174, v175\n"
"v_exp_f32 v164, v164\n"
"v_mfma_f32_16x16x32_bf16 v[172:175], v[216:219], a[24:27], v[184:187]\n"
"v_exp_f32 v156, v156\n"
"v_mfma_f32_16x16x32_bf16 v[172:175], v[220:223], a[28:31], v[172:175]\n"
"v_exp_f32 v158, v158\n"
"v_mfma_f32_16x16x32_bf16 v[172:175], v[208:211], a[16:19], v[172:175]\n"
"v_exp_f32 v168, v168\n"
"v_mfma_f32_16x16x32_bf16 v[172:175], v[212:215], a[20:23], v[172:175]\n"
"s_cbranch_vccnz LPCE0_%=\nLPKE0_%=:\n"
"ds_write_b128 v243, v[56:59] offset:14336\n"
"v_accvgpr_write_b32 a185, v41\n"
"v_cvt_pk_bf16_f32 v146, v176, v177\n"
"v_cvt_pk_bf16_f32 v147, v178, v179\n"
"v_cvt_pk_bf16_f32 v126, v152, v153\n"
"v_cvt_pk_bf16_f32 v133, v158, v159\n"
"v_exp_f32 v165, v165\n"
"v_mfma_f32_16x16x32_bf16 v[176:179], v[208:211], a[32:35], v[188:191]\n"
"v_cvt_pk_bf16_f32 v127, v154, v155\n"
"v_cvt_pk_bf16_f32 v132, v156, v157\n"
"v_mfma_f32_16x16x32_bf16 v[176:179], v[212:215], a[36:39], v[176:179]\n"
"v_exp_f32 v161, v161\n"
"v_mfma_f32_16x16x32_bf16 v[176:179], v[216:219], a[40:43], v[176:179]\n"
"v_cvt_pk_bf16_f32 v17, v18, v19\n"
"v_accvgpr_write_b32 a187, v43\n"
"v_mfma_f32_16x16x32_bf16 v[176:179], v[220:223], a[44:47], v[176:179]\n"
"v_cvt_pk_bf16_f32 v140, v160, v161\n"
"v_exp_f32 v162, v162\n"
"v_exp_f32 v167, v167\n"
"v_exp_f32 v169, v169\n"
"v_exp_f32 v172, v172\n"
"v_exp_f32 v173, v173\n"
"v_exp_f32 v174, v174\n"
"s_waitcnt vmcnt(8) lgkmcnt(0)\n"
"s_barrier\n"
"ds_read_b128 v[208:211], v228 offset:16384\n"
"ds_read_b128 v[212:215], v229 offset:16384\n"
"ds_read_b128 v[216:219], v228 offset:16896\n"
"ds_read_b128 v[220:223], v229 offset:16896\n"
"s_waitcnt lgkmcnt(3)\n"
"v_mfma_f32_16x16x32_bf16 v[152:155], v[208:211], a[0:3], v[180:183]\n"
"v_accvgpr_write_b32 a153, v9\n"
"v_cvt_pk_bf16_f32 v32, v32, v33\n"
"s_waitcnt lgkmcnt(2)\n"
"v_mfma_f32_16x16x32_bf16 v[152:155], v[212:215], a[4:7], v[152:155]\n"
"ds_read_b64_tr_b16 v[224:225], v239 offset:0\n"
"v_cvt_pk_bf16_f32 v141, v162, v163\n"
"s_waitcnt lgkmcnt(2)\n"
"v_mfma_f32_16x16x32_bf16 v[152:155], v[216:219], a[8:11], v[152:155]\n"
"s_waitcnt lgkmcnt(1)\n"
"ds_read_b128 a[188:191], v249 offset:52224\n"
"v_mov_b32 v203, 0\n"
"v_mfma_f32_16x16x32_bf16 v[152:155], v[220:223], a[12:15], v[152:155]\n"
"ds_read_b128 a[116:119], v248 offset:51200\n"
"v_cvt_pk_bf16_f32 v27, v30, v31\n"
"v_mfma_f32_16x16x32_bf16 v[156:159], v[216:219], a[24:27], v[184:187]\n"
"ds_read_b64_tr_b16 v[226:227], v239 offset:4096\n"
"ds_read_b128 a[180:183], v249 offset:51200\n"
"v_mfma_f32_16x16x32_bf16 v[156:159], v[220:223], a[28:31], v[156:159]\n"
"v_accvgpr_write_b32 a154, v10\n"
"v_accvgpr_write_b32 a184, v40\n"
"v_mfma_f32_16x16x32_bf16 v[156:159], v[208:211], a[16:19], v[156:159]\n"
"ds_read_b64_tr_b16 a[244:245], v234 offset:0\n"
"ds_read_b64_tr_b16 a[246:247], v234 offset:4096\n"
"v_mfma_f32_16x16x32_bf16 v[156:159], v[212:215], a[20:23], v[156:159]\n"
"ds_read_b128 a[100:103], v248 offset:49152\n"
"v_exp_f32 v166, v166\n"
"v_exp_f32 v175, v175\n"
"v_exp_f32 v176, v176\n"
"v_exp_f32 v177, v177\n"
"v_exp_f32 v154, v154\n"
"v_mfma_f32_16x16x32_bf16 v[160:163], v[208:211], a[32:35], v[188:191]\n"
"v_mov_b32 v198, 0\n"
"v_cvt_pk_bf16_f32 v148, v164, v165\n"
"v_mfma_f32_16x16x32_bf16 v[160:163], v[212:215], a[36:39], v[160:163]\n"
"ds_read_b64_tr_b16 a[250:251], v238 offset:4096\n"
"ds_read_b64_tr_b16 a[254:255], v235 offset:4096\n"
"v_mfma_f32_16x16x32_bf16 v[160:163], v[216:219], a[40:43], v[160:163]\n"
"ds_read_b64_tr_b16 a[248:249], v238 offset:0\n"
"ds_read_b128 a[156:159], v249 offset:35840\n"
"v_mfma_f32_16x16x32_bf16 v[160:163], v[220:223], a[44:47], v[160:163]\n"
"ds_read_b128 a[172:175], v249 offset:50176\n"
"v_accvgpr_write_b32 a155, v11\n"
"v_cvt_pk_bf16_f32 v149, v166, v167\n"
"v_cvt_pk_bf16_f32 v134, v168, v169\n"
"v_exp_f32 v170, v170\n"
"v_exp_f32 v171, v171\n"
"v_cvt_pk_bf16_f32 v143, v174, v175\n"
"v_exp_f32 v179, v179\n"
"v_mfma_f32_16x16x32_bf16 v[164:167], v[216:219], a[200:203], v[192:195]\n"
"ds_read_b128 a[164:167], v249 offset:49152\n"
"v_cvt_pk_bf16_f32 v142, v172, v173\n"
"v_mfma_f32_16x16x32_bf16 v[164:167], v[220:223], a[204:207], v[164:167]\n"
"ds_read_b64_tr_b16 a[252:253], v235 offset:0\n"
"v_cvt_pk_bf16_f32 v112, v112, v113\n"
"v_mfma_f32_16x16x32_bf16 v[164:167], v[208:211], a[192:195], v[164:167]\n"
"ds_read_b128 a[140:143], v249 offset:33792\n"
"v_cvt_pk_bf16_f32 v0, v0, v1\n"
"v_mfma_f32_16x16x32_bf16 v[164:167], v[212:215], a[196:199], v[164:167]\n"
"s_waitcnt lgkmcnt(8)\n"
"ds_read_b128 a[148:151], v249 offset:34816\n"
"v_accvgpr_write_b32 a152, v8\n"
"v_cvt_pk_bf16_f32 v33, v34, v35\n"
"v_cvt_pk_bf16_f32 v135, v170, v171\n"
"v_exp_f32 v178, v178\n"
"v_xor_b32 v207, 64, v240\n"
"v_exp_f32 v153, v153\n"
"v_exp_f32 v162, v162\n"
"v_mfma_f32_16x16x32_bf16 a[216:219], v[124:127], a[244:247], 0\n"
"ds_read_b128 a[108:111], v248 offset:50176\n"
"v_cvt_pk_bf16_f32 v1, v2, v3\n"
"v_mfma_f32_16x16x32_bf16 v[8:11], v[128:131], a[244:247], 0\n"
"s_waitcnt lgkmcnt(7)\n"
"ds_read_b128 a[124:127], v248 offset:52224\n"
"v_cvt_pk_bf16_f32 v34, v36, v37\n"
"v_mfma_f32_16x16x32_bf16 v[8:11], v[132:135], a[248:251], v[8:11]\n"
"ds_read_b128 v[216:219], v228 offset:20992\n"
"v_cvt_pk_bf16_f32 v26, v28, v29\n"
"v_mfma_f32_16x16x32_bf16 v[40:43], v[136:139], a[244:247], 0\n"
"ds_read_b128 a[132:135], v249 offset:32768\n"
"v_cvt_pk_bf16_f32 v150, v176, v177\n"
"v_mfma_f32_16x16x32_bf16 v[40:43], v[140:143], a[248:251], v[40:43]\n"
"v_cvt_pk_bf16_f32 v3, v6, v7\n"
"v_cvt_pk_bf16_f32 v151, v178, v179\n"
"v_mfma_f32_16x16x32_bf16 v[72:75], v[144:147], a[244:247], 0\n"
"ds_read_b128 v[208:211], v228 offset:20480\n"
"v_accvgpr_write_b32 a242, v254\n"
"v_mfma_f32_16x16x32_bf16 v[72:75], v[148:151], a[248:251], v[72:75]\n"
"ds_read_b128 v[220:223], v229 offset:20992\n"
"v_cvt_pk_bf16_f32 v113, v114, v115\n"
"v_mfma_f32_16x16x32_bf16 v[76:79], v[148:151], v[224:227], 0\n"
"s_waitcnt lgkmcnt(6)\n"
"v_exp_f32 v159, v159\n"
"v_mfma_f32_16x16x32_bf16 v[76:79], v[144:147], a[252:255], v[76:79]\n"
"v_permlane16_swap_b32 v24, v26\n"
"v_mfma_f32_16x16x32_bf16 v[44:47], v[140:143], v[224:227], 0\n"
"v_permlane16_swap_b32 v32, v34\n"
"v_mfma_f32_16x16x32_bf16 v[44:47], v[136:139], a[252:255], v[44:47]\n"
"ds_read_b128 v[212:215], v229 offset:20480\n"
"v_permlane16_swap_b32 v25, v27\n"
"v_cvt_pk_bf16_f32 v35, v38, v39\n"
"v_exp_f32 v155, v155\n"
"v_exp_f32 v160, v160\n"
"v_exp_f32 v167, v167\n"
"v_mfma_f32_16x16x32_bf16 v[12:15], v[132:135], v[224:227], 0\n"
"v_exp_f32 v164, v164\n"
"v_mfma_f32_16x16x32_bf16 v[12:15], v[128:131], a[252:255], v[12:15]\n"
"v_exp_f32 v166, v166\n"
"v_mfma_f32_16x16x32_bf16 a[220:223], v[124:127], a[252:255], 0\n"
"s_waitcnt lgkmcnt(2)\n"
"ds_read_b64_tr_b16 a[254:255], v235 offset:4608\n"
"v_accvgpr_write_b32 a169, v25\n"
"v_smfmac_f32_16x16x64_bf16 v[196:199], a[240:243], v[128:135], v241\n"
"s_add_u32 m0, s44, 98304\n"
"ds_read_b64_tr_b16 v[224:225], v239 offset:512\n"
"v_cvt_pk_bf16_f32 v97, v154, v155\n"
"v_mfma_f32_16x16x32_bf16 v[202:205], a[240:243], v[124:127], v[202:205]\n"
"buffer_load_dwordx4 v240, s[20:23], s56 offen nt lds\n"
"v_mfma_f32_16x16x32_bf16 v[168:171], v[216:219], a[8:11], v[180:183]\n"
"s_waitcnt lgkmcnt(3)\n"
"v_exp_f32 v152, v152\n"
"v_mfma_f32_16x16x32_bf16 v[168:171], v[220:223], a[12:15], v[168:171]\n"
"ds_read_b64_tr_b16 a[252:253], v235 offset:512\n"
"v_accvgpr_write_b32 a168, v24\n"
"v_mfma_f32_16x16x32_bf16 v[168:171], v[208:211], a[0:3], v[168:171]\n"
"s_waitcnt lgkmcnt(3)\n"
"ds_read_b64_tr_b16 a[244:245], v234 offset:512\n"
"v_cvt_pk_bf16_f32 v96, v152, v153\n"
"v_mfma_f32_16x16x32_bf16 v[168:171], v[212:215], a[4:7], v[168:171]\n"
"ds_read_b64_tr_b16 a[248:249], v238 offset:512\n"
"ds_read_b64_tr_b16 a[250:251], v238 offset:4608\n"
"v_mfma_f32_16x16x32_bf16 v[172:175], v[208:211], a[16:19], v[184:187]\n"
"ds_read_b64_tr_b16 a[246:247], v234 offset:4608\n"
"v_cvt_pk_bf16_f32 v115, v118, v119\n"
"v_mfma_f32_16x16x32_bf16 v[172:175], v[212:215], a[20:23], v[172:175]\n"
"v_exp_f32 v165, v165\n"
"v_mfma_f32_16x16x32_bf16 v[172:175], v[216:219], a[24:27], v[172:175]\n"
"s_add_u32 s40, s56, s52\n"
"v_exp_f32 v158, v158\n"
"v_mfma_f32_16x16x32_bf16 v[172:175], v[220:223], a[28:31], v[172:175]\n"
"buffer_load_dwordx4 v207, s[20:23], s40 offen offset:1024 nt lds\n"
"v_mfma_f32_16x16x32_bf16 v[176:179], v[216:219], a[40:43], v[188:191]\n"
"v_exp_f32 v157, v157\n"
"v_mfma_f32_16x16x32_bf16 v[176:179], v[220:223], a[44:47], v[176:179]\n"
"v_accvgpr_write_b32 a105, v121\n"
"v_accvgpr_write_b32 a170, v26\n"
"v_mfma_f32_16x16x32_bf16 v[176:179], v[208:211], a[32:35], v[176:179]\n"
"v_exp_f32 v156, v156\n"
"v_mfma_f32_16x16x32_bf16 v[176:179], v[212:215], a[36:39], v[176:179]\n"
"v_permlane16_swap_b32 v113, v115\n"
"v_cvt_pk_bf16_f32 v2, v4, v5\n"
"v_exp_f32 v161, v161\n"
"v_exp_f32 v163, v163\n"
"v_exp_f32 v172, v172\n"
"v_mfma_f32_16x16x32_bf16 v[152:155], v[208:211], a[192:195], v[192:195]\n"
"v_accvgpr_write_b32 a171, v27\n"
"v_mov_b32 v200, 0\n"
"v_mfma_f32_16x16x32_bf16 v[152:155], v[212:215], a[196:199], v[152:155]\n"
"ds_read_b64_tr_b16 v[226:227], v239 offset:4608\n"
"v_accvgpr_write_b32 a176, v32\n"
"v_mfma_f32_16x16x32_bf16 v[152:155], v[216:219], a[200:203], v[152:155]\n"
"ds_read_b128 v[212:215], v231 offset:16384\n"
"v_cvt_pk_bf16_f32 v105, v158, v159\n"
"v_mfma_f32_16x16x32_bf16 v[152:155], v[220:223], a[204:207], v[152:155]\n"
"s_waitcnt lgkmcnt(2)\n"
"v_permlane16_swap_b32 v0, v2\n"
"v_mfma_f32_16x16x32_bf16 a[232:235], v[124:127], a[244:247], 0\n"
"ds_read_b128 v[216:219], v230 offset:16896\n"
"v_cvt_pk_bf16_f32 v18, v20, v21\n"
"v_mfma_f32_16x16x32_bf16 v[24:27], v[128:131], a[244:247], 0\n"
"s_add_u32 s40, s56, s53\n"
"ds_read_b128 v[208:211], v230 offset:16384\n"
"v_cvt_pk_bf16_f32 v104, v156, v157\n"
"v_mfma_f32_16x16x32_bf16 v[24:27], v[132:135], a[248:251], v[24:27]\n"
"buffer_load_dwordx4 v240, s[20:23], s40 offen offset:2048 nt lds\n"
"v_mfma_f32_16x16x32_bf16 v[56:59], v[136:139], a[244:247], 0\n"
"v_exp_f32 v174, v174\n"
"v_mfma_f32_16x16x32_bf16 v[56:59], v[140:143], a[248:251], v[56:59]\n"
"v_accvgpr_write_b32 a97, v113\n"
"v_accvgpr_write_b32 a144, v0\n"
"v_mfma_f32_16x16x32_bf16 v[88:91], v[144:147], a[244:247], 0\n"
"v_exp_f32 v171, v171\n"
"v_mfma_f32_16x16x32_bf16 v[88:91], v[148:151], a[248:251], v[88:91]\n"
"s_waitcnt lgkmcnt(2)\n"
"ds_read_b128 v[220:223], v231 offset:16896\n"
"v_cvt_pk_bf16_f32 v114, v116, v117\n"
"v_mfma_f32_16x16x32_bf16 v[92:95], v[148:151], v[224:227], 0\n"
"s_add_u32 s40, s56, s54\n"
"v_permlane16_swap_b32 v33, v35\n"
"v_mfma_f32_16x16x32_bf16 v[92:95], v[144:147], a[252:255], v[92:95]\n"
"buffer_load_dwordx4 v207, s[20:23], s40 offen offset:3072 nt lds\n"
"v_permlane16_swap_b32 v112, v114\n"
"v_accvgpr_write_b32 a99, v115\n"
"v_cvt_pk_bf16_f32 v120, v164, v165\n"
"v_cvt_pk_bf16_f32 v121, v166, v167\n"
"v_exp_f32 v175, v175\n"
"v_exp_f32 v178, v178\n"
"v_mfma_f32_16x16x32_bf16 v[60:63], v[140:143], v[224:227], 0\n"
"s_add_u32 s55, s55, s59\n"
"v_permlane16_swap_b32 v1, v3\n"
"v_mfma_f32_16x16x32_bf16 v[60:63], v[136:139], a[252:255], v[60:63]\n"
"s_or_b32 s55, s55, s58\n"
"v_accvgpr_write_b32 a96, v112\n"
"v_accvgpr_write_b32 a178, v34\n"
"v_mfma_f32_16x16x32_bf16 v[28:31], v[132:135], v[224:227], 0\n"
"s_xor_b32 s56, s55, s57\n"
"ds_read_b64_tr_b16 v[224:225], v237 offset:0\n"
"v_accvgpr_write_b32 a179, v35\n"
"v_mfma_f32_16x16x32_bf16 v[28:31], v[128:131], a[252:255], v[28:31]\n"
"ds_read_b64_tr_b16 a[244:245], v232 offset:0\n"
"v_accvgpr_write_b32 a145, v1\n"
"v_mfma_f32_16x16x32_bf16 a[236:239], v[124:127], a[252:255], 0\n"
"v_exp_f32 v179, v179\n"
"v_smfmac_f32_16x16x64_bf16 v[198:201], a[240:243], v[136:143], v241\n"
"s_waitcnt lgkmcnt(2)\n"
"ds_read_b64_tr_b16 a[252:253], v233 offset:0\n"
"v_accvgpr_write_b32 a177, v33\n"
"v_mfma_f32_16x16x32_bf16 v[156:159], v[208:211], a[0:3], v[180:183]\n"
"ds_read_b64_tr_b16 a[246:247], v232 offset:4096\n"
"v_cvt_pk_bf16_f32 v113, v162, v163\n"
"v_mfma_f32_16x16x32_bf16 v[156:159], v[212:215], a[4:7], v[156:159]\n"
"ds_read_b64_tr_b16 a[250:251], v236 offset:4096\n"
"v_cvt_pk_bf16_f32 v112, v160, v161\n"
"v_mfma_f32_16x16x32_bf16 v[156:159], v[216:219], a[8:11], v[156:159]\n"
"s_add_u32 m0, s44, 49152\n"
"ds_read_b64_tr_b16 a[248:249], v236 offset:0\n"
"ds_read_b64_tr_b16 v[226:227], v237 offset:4096\n"
"v_mfma_f32_16x16x32_bf16 v[156:159], v[220:223], a[12:15], v[156:159]\n"
"buffer_load_dwordx4 v240, s[16:19], s56 offen nt lds\n"
"v_mfma_f32_16x16x32_bf16 v[160:163], v[216:219], a[24:27], v[184:187]\n"
"v_exp_f32 v169, v169\n"
"v_mfma_f32_16x16x32_bf16 v[160:163], v[220:223], a[28:31], v[160:163]\n"
"v_accvgpr_write_b32 a98, v114\n"
"v_accvgpr_write_b32 a147, v3\n"
"v_mfma_f32_16x16x32_bf16 v[160:163], v[208:211], a[16:19], v[160:163]\n"
"v_accvgpr_write_b32 a146, v2\n"
"v_cvt_pk_bf16_f32 v107, v174, v175\n"
"v_mfma_f32_16x16x32_bf16 v[160:163], v[212:215], a[20:23], v[160:163]\n"
"ds_read_b64_tr_b16 a[254:255], v233 offset:4096\n"
"v_cvt_pk_bf16_f32 v115, v178, v179\n"
"v_mfma_f32_16x16x32_bf16 v[164:167], v[208:211], a[32:35], v[188:191]\n"
"v_exp_f32 v158, v158\n"
"v_mfma_f32_16x16x32_bf16 v[164:167], v[212:215], a[36:39], v[164:167]\n"
"v_exp_f32 v168, v168\n"
"v_mfma_f32_16x16x32_bf16 v[164:167], v[216:219], a[40:43], v[164:167]\n"
"v_exp_f32 v170, v170\n"
"v_mfma_f32_16x16x32_bf16 v[164:167], v[220:223], a[44:47], v[164:167]\n"
"s_waitcnt lgkmcnt(1)\n"
"v_exp_f32 v159, v159\n"
"v_mfma_f32_16x16x32_bf16 a[208:211], v[124:127], a[244:247], 0\n"
"ds_read_b128 v[208:211], v230 offset:20480\n"
"v_cvt_pk_bf16_f32 v19, v22, v23\n"
"v_mfma_f32_16x16x32_bf16 v[0:3], v[128:131], a[244:247], 0\n"
"s_add_u32 s40, s56, s52\n"
"ds_read_b128 v[216:219], v230 offset:20992\n"
"v_cvt_pk_bf16_f32 v98, v168, v169\n"
"v_mfma_f32_16x16x32_bf16 v[0:3], v[132:135], a[248:251], v[0:3]\n"
"buffer_load_dwordx4 v207, s[16:19], s40 offen offset:1024 nt lds\n"
"v_mfma_f32_16x16x32_bf16 v[32:35], v[136:139], a[244:247], 0\n"
"v_exp_f32 v165, v165\n"
"v_mfma_f32_16x16x32_bf16 v[32:35], v[140:143], a[248:251], v[32:35]\n"
"v_exp_f32 v176, v176\n"
"v_mfma_f32_16x16x32_bf16 v[64:67], v[144:147], a[244:247], 0\n"
"ds_read_b128 v[212:215], v231 offset:20480\n"
"v_cvt_pk_bf16_f32 v99, v170, v171\n"
"v_mfma_f32_16x16x32_bf16 v[64:67], v[148:151], a[248:251], v[64:67]\n"
"ds_read_b128 v[220:223], v231 offset:20992\n"
"v_accvgpr_write_b32 a107, v123\n"
"v_permlane16_swap_b32 v16, v18\n"
"v_exp_f32 v173, v173\n"
"v_exp_f32 v154, v154\n"
"v_exp_f32 v155, v155\n"
"v_mfma_f32_16x16x32_bf16 v[68:71], v[148:151], v[224:227], 0\n"
"s_waitcnt lgkmcnt(4)\n"
"s_add_u32 s40, s56, s53\n"
"v_exp_f32 v164, v164\n"
"v_mfma_f32_16x16x32_bf16 v[68:71], v[144:147], a[252:255], v[68:71]\n"
"buffer_load_dwordx4 v240, s[16:19], s40 offen offset:2048 nt lds\n"
"v_mfma_f32_16x16x32_bf16 v[36:39], v[140:143], v[224:227], 0\n"
"v_exp_f32 v153, v153\n"
"v_mfma_f32_16x16x32_bf16 v[36:39], v[136:139], a[252:255], v[36:39]\n"
"v_exp_f32 v166, v166\n"
"v_mfma_f32_16x16x32_bf16 v[4:7], v[132:135], v[224:227], 0\n"
"v_permlane16_swap_b32 v17, v19\n"
"v_mfma_f32_16x16x32_bf16 v[4:7], v[128:131], a[252:255], v[4:7]\n"
"ds_read_b64_tr_b16 a[250:251], v236 offset:4608\n"
"v_cvt_pk_bf16_f32 v106, v172, v173\n"
"v_mfma_f32_16x16x32_bf16 a[212:215], v[124:127], a[252:255], 0\n"
"v_exp_f32 v162, v162\n"
"v_smfmac_f32_16x16x64_bf16 v[200:203], a[240:243], v[144:151], v241\n"
"s_waitcnt lgkmcnt(1)\n"
"ds_read_b64_tr_b16 a[246:247], v232 offset:4608\n"
"ds_read_b64_tr_b16 a[248:249], v236 offset:512\n"
"v_mfma_f32_16x16x32_bf16 v[168:171], v[208:211], a[0:3], v[180:183]\n"
"v_exp_f32 v177, v177\n"
"v_mfma_f32_16x16x32_bf16 v[168:171], v[212:215], a[4:7], v[168:171]\n"
"ds_read_b64_tr_b16 a[244:245], v232 offset:512\n"
"v_cvt_pk_bf16_f32 v114, v176, v177\n"
"v_mfma_f32_16x16x32_bf16 v[168:171], v[216:219], a[8:11], v[168:171]\n"
"s_add_u32 s40, s56, s54\n"
"v_exp_f32 v157, v157\n"
"v_mfma_f32_16x16x32_bf16 v[168:171], v[220:223], a[12:15], v[168:171]\n"
"buffer_load_dwordx4 v207, s[16:19], s40 offen offset:3072 nt lds\n"
"v_mfma_f32_16x16x32_bf16 v[172:175], v[216:219], a[24:27], v[184:187]\n"
"v_exp_f32 v167, v167\n"
"v_mfma_f32_16x16x32_bf16 v[172:175], v[220:223], a[28:31], v[172:175]\n"
"v_exp_f32 v160, v160\n"
"v_mfma_f32_16x16x32_bf16 v[172:175], v[208:211], a[16:19], v[172:175]\n"
"ds_read_b64_tr_b16 a[254:255], v233 offset:4608\n"
"v_accvgpr_write_b32 a162, v18\n"
"v_mfma_f32_16x16x32_bf16 v[172:175], v[212:215], a[20:23], v[172:175]\n"
"s_cbranch_vccnz LPCE1_%=\nLPKE1_%=:\n"
"v_bfe_u32 v249, v242, 4, 2\n"
"v_mfma_f32_16x16x32_bf16 v[176:179], v[208:211], a[32:35], v[188:191]\n"
"v_accvgpr_write_b32 a106, v122\n"
"v_bfe_u32 v248, v242, 8, 2\n"
"v_mfma_f32_16x16x32_bf16 v[176:179], v[212:215], a[36:39], v[176:179]\n"
"ds_read_b64_tr_b16 a[252:253], v233 offset:512\n"
"v_accvgpr_write_b32 a161, v17\n"
"v_mfma_f32_16x16x32_bf16 v[176:179], v[216:219], a[40:43], v[176:179]\n"
"ds_read_b64_tr_b16 v[224:225], v237 offset:512\n"
"v_accvgpr_write_b32 a160, v16\n"
"v_mfma_f32_16x16x32_bf16 v[176:179], v[220:223], a[44:47], v[176:179]\n"
"s_waitcnt lgkmcnt(3)\n"
"ds_read_b64_tr_b16 v[226:227], v237 offset:4608\n"
"v_accvgpr_write_b32 a163, v19\n"
"v_lshlrev_b32 v248, 6, v248\n"
"v_exp_f32 v152, v152\n"
"v_exp_f32 v163, v163\n"
"v_exp_f32 v169, v169\n"
"v_mfma_f32_16x16x32_bf16 a[224:227], v[124:127], a[244:247], 0\n"
"v_exp_f32 v156, v156\n"
"v_mfma_f32_16x16x32_bf16 v[16:19], v[128:131], a[244:247], 0\n"
"s_waitcnt vmcnt(8)\n"
"v_xor_b32 v248, v248, v242\n"
"v_bfe_u32 v254, v242, 6, 4\n"
"v_mfma_f32_16x16x32_bf16 v[16:19], v[132:135], a[248:251], v[16:19]\n"
"s_barrier\n"
"ds_read_b128 v[216:219], v228 offset:33280\n"
"v_mul_u32_u24 v249, 20, v249\n"
"v_cvt_pk_bf16_f32 v101, v158, v159\n"
"v_exp_f32 v176, v176\n"
"v_exp_f32 v177, v177\n"
"v_mfma_f32_16x16x32_bf16 v[48:51], v[136:139], a[244:247], 0\n"
"ds_read_b128 v[220:223], v229 offset:33280\n"
"v_cvt_pk_bf16_f32 v109, v162, v163\n"
"v_mfma_f32_16x16x32_bf16 v[48:51], v[140:143], a[248:251], v[48:51]\n"
"ds_read_b128 v[208:211], v228 offset:32768\n"
"ds_read_b128 v[212:215], v229 offset:32768\n"
"v_mfma_f32_16x16x32_bf16 v[80:83], v[144:147], a[244:247], 0\n"
"ds_read_b64_tr_b16 a[246:247], v234 offset:20480\n"
"v_cvt_pk_bf16_f32 v122, v152, v153\n"
"v_mfma_f32_16x16x32_bf16 v[80:83], v[148:151], a[248:251], v[80:83]\n"
"s_waitcnt lgkmcnt(5)\n"
"ds_read_b64_tr_b16 a[250:251], v238 offset:20480\n"
"v_cvt_pk_bf16_f32 v123, v154, v155\n"
"v_mfma_f32_16x16x32_bf16 v[84:87], v[148:151], v[224:227], 0\n"
"v_cvt_pk_bf16_f32 v116, v164, v165\n"
"v_cvt_pk_bf16_f32 v117, v166, v167\n"
"v_mfma_f32_16x16x32_bf16 v[84:87], v[144:147], a[252:255], v[84:87]\n"
"v_xor_b32 v249, v249, v254\n"
"v_cvt_pk_bf16_f32 v118, v176, v177\n"
"v_mfma_f32_16x16x32_bf16 v[52:55], v[140:143], v[224:227], 0\n"
"v_lshl_add_u32 v249, v249, 4, s44\n"
"v_cvt_pk_bf16_f32 v100, v156, v157\n"
"v_mfma_f32_16x16x32_bf16 v[52:55], v[136:139], a[252:255], v[52:55]\n"
"v_exp_f32 v161, v161\n"
"v_mfma_f32_16x16x32_bf16 v[20:23], v[132:135], v[224:227], 0\n"
"v_exp_f32 v174, v174\n"
"v_mfma_f32_16x16x32_bf16 v[20:23], v[128:131], a[252:255], v[20:23]\n"
"v_exp_f32 v168, v168\n"
"v_mfma_f32_16x16x32_bf16 a[228:231], v[124:127], a[252:255], 0\n"
"s_waitcnt lgkmcnt(1)\n"
"ds_read_b64_tr_b16 a[254:255], v235 offset:20480\n"
"s_branch LAY2_%=\n"
"LAL_%=:\n"
"v_mfma_f32_16x16x32_bf16 a[224:227], v[124:127], a[244:247], a[224:227]\n"
"v_exp_f32 v162, v162\n"
"v_mfma_f32_16x16x32_bf16 v[16:19], v[128:131], a[244:247], v[16:19]\n"
"s_waitcnt vmcnt(8)\n"
"v_exp_f32 v168, v168\n"
"v_mfma_f32_16x16x32_bf16 v[16:19], v[132:135], a[248:251], v[16:19]\n"
"s_barrier\n"
"v_mfma_f32_16x16x32_bf16 v[48:51], v[136:139], a[244:247], v[48:51]\n"
"ds_read_b128 v[208:211], v228 offset:0\n"
"v_cvt_pk_bf16_f32 v101, v158, v159\n"
"v_mfma_f32_16x16x32_bf16 v[48:51], v[140:143], a[248:251], v[48:51]\n"
"ds_read_b128 v[216:219], v228 offset:512\n"
"v_cvt_pk_bf16_f32 v109, v162, v163\n"
"v_mfma_f32_16x16x32_bf16 v[80:83], v[144:147], a[244:247], v[80:83]\n"
"ds_read_b128 v[220:223], v229 offset:512\n"
"v_cvt_pk_bf16_f32 v123, v154, v155\n"
"v_mfma_f32_16x16x32_bf16 v[80:83], v[148:151], a[248:251], v[80:83]\n"
"ds_read_b128 v[212:215], v229 offset:0\n"
"v_cvt_pk_bf16_f32 v117, v166, v167\n"
"v_mfma_f32_16x16x32_bf16 v[84:87], v[148:151], v[224:227], v[84:87]\n"
"v_exp_f32 v176, v176\n"
"v_mfma_f32_16x16x32_bf16 v[84:87], v[144:147], a[252:255], v[84:87]\n"
"v_exp_f32 v169, v169\n"
"v_mfma_f32_16x16x32_bf16 v[52:55], v[140:143], v[224:227], v[52:55]\n"
"ds_read_b64_tr_b16 a[246:247], v234 offset:53248\n"
"v_cvt_pk_bf16_f32 v116, v164, v165\n"
"v_mfma_f32_16x16x32_bf16 v[52:55], v[136:139], a[252:255], v[52:55]\n"
"v_exp_f32 v177, v177\n"
"v_mfma_f32_16x16x32_bf16 v[20:23], v[132:135], v[224:227], v[20:23]\n"
"ds_read_b64_tr_b16 a[250:251], v238 offset:53248\n"
"v_cvt_pk_bf16_f32 v118, v176, v177\n"
"v_mfma_f32_16x16x32_bf16 v[20:23], v[128:131], a[252:255], v[20:23]\n"
"v_exp_f32 v174, v174\n"
"v_mfma_f32_16x16x32_bf16 a[228:231], v[124:127], a[252:255], a[228:231]\n"
"s_waitcnt lgkmcnt(3)\n"
"ds_read_b64_tr_b16 a[254:255], v235 offset:53248\n"
"v_cvt_pk_bf16_f32 v100, v156, v157\n"
"v_mfma_f32_16x16x32_bf16 v[152:155], v[208:211], a[0:3], v[180:183]\n"
"s_waitcnt lgkmcnt(3)\n"
"ds_read_b64_tr_b16 a[248:249], v238 offset:49152\n"
"v_cvt_pk_bf16_f32 v102, v168, v169\n"
"v_mfma_f32_16x16x32_bf16 v[152:155], v[212:215], a[4:7], v[152:155]\n"
"v_exp_f32 v172, v172\n"
"v_mfma_f32_16x16x32_bf16 v[152:155], v[216:219], a[8:11], v[152:155]\n"
"ds_read_b64_tr_b16 a[244:245], v234 offset:49152\n"
"v_cvt_pk_bf16_f32 v108, v160, v161\n"
"v_mfma_f32_16x16x32_bf16 v[152:155], v[220:223], a[12:15], v[152:155]\n"
"v_exp_f32 v175, v175\n"
"v_mfma_f32_16x16x32_bf16 v[156:159], v[216:219], a[24:27], v[184:187]\n"
"ds_read_b64_tr_b16 a[252:253], v235 offset:49152\n"
"v_cvt_pk_bf16_f32 v111, v174, v175\n"
"v_mfma_f32_16x16x32_bf16 v[156:159], v[220:223], a[28:31], v[156:159]\n"
"ds_read_b64_tr_b16 v[224:225], v239 offset:49152\n"
"v_mfma_f32_16x16x32_bf16 v[156:159], v[208:211], a[16:19], v[156:159]\n"
"s_add_u32 m0, s44, 81920\n"
"ds_read_b64_tr_b16 v[226:227], v239 offset:53248\n"
"v_mfma_f32_16x16x32_bf16 v[156:159], v[212:215], a[20:23], v[156:159]\n"
"buffer_load_dwordx4 v240, s[20:23], s56 offen nt lds\n"
"v_mfma_f32_16x16x32_bf16 v[160:163], v[208:211], a[32:35], v[188:191]\n"
"v_exp_f32 v178, v178\n"
"v_mfma_f32_16x16x32_bf16 v[160:163], v[212:215], a[36:39], v[160:163]\n"
"v_exp_f32 v171, v171\n"
"v_mfma_f32_16x16x32_bf16 v[160:163], v[216:219], a[40:43], v[160:163]\n"
"s_add_u32 s40, s56, s52\n"
"v_mfma_f32_16x16x32_bf16 v[160:163], v[220:223], a[44:47], v[160:163]\n"
"buffer_load_dwordx4 v207, s[20:23], s40 offen offset:1024 nt lds\n"
"v_mfma_f32_16x16x32_bf16 v[164:167], v[216:219], a[200:203], v[192:195]\n"
"v_exp_f32 v170, v170\n"
"v_mfma_f32_16x16x32_bf16 v[164:167], v[220:223], a[204:207], v[164:167]\n"
"v_exp_f32 v153, v153\n"
"v_mfma_f32_16x16x32_bf16 v[164:167], v[208:211], a[192:195], v[164:167]\n"
"v_exp_f32 v179, v179\n"
"v_mfma_f32_16x16x32_bf16 v[164:167], v[212:215], a[196:199], v[164:167]\n"
"s_waitcnt lgkmcnt(0)\n"
"v_exp_f32 v158, v158\n"
"v_mfma_f32_16x16x32_bf16 a[216:219], v[120:123], a[244:247], a[216:219]\n"
"ds_read_b128 v[216:219], v228 offset:4608\n"
"v_cvt_pk_bf16_f32 v103, v170, v171\n"
"v_mfma_f32_16x16x32_bf16 v[8:11], v[96:99], a[244:247], v[8:11]\n"
"v_exp_f32 v173, v173\n"
"v_mfma_f32_16x16x32_bf16 v[8:11], v[100:103], a[248:251], v[8:11]\n"
"ds_read_b128 v[208:211], v228 offset:4096\n"
"v_cvt_pk_bf16_f32 v110, v172, v173\n"
"v_mfma_f32_16x16x32_bf16 v[40:43], v[104:107], a[244:247], v[40:43]\n"
"v_exp_f32 v162, v162\n"
"v_mfma_f32_16x16x32_bf16 v[40:43], v[108:111], a[248:251], v[40:43]\n"
"ds_read_b128 v[220:223], v229 offset:4608\n"
"v_cvt_pk_bf16_f32 v119, v178, v179\n"
"v_mfma_f32_16x16x32_bf16 v[72:75], v[112:115], a[244:247], v[72:75]\n"
"s_add_u32 s40, s56, s53\n"
"ds_read_b128 v[212:215], v229 offset:4096\n"
"v_mfma_f32_16x16x32_bf16 v[72:75], v[116:119], a[248:251], v[72:75]\n"
"buffer_load_dwordx4 v240, s[20:23], s40 offen offset:2048 nt lds\n"
"v_mfma_f32_16x16x32_bf16 v[76:79], v[116:119], v[224:227], v[76:79]\n"
"v_exp_f32 v163, v163\n"
"v_mfma_f32_16x16x32_bf16 v[76:79], v[112:115], a[252:255], v[76:79]\n"
"v_exp_f32 v156, v156\n"
"v_mfma_f32_16x16x32_bf16 v[44:47], v[108:111], v[224:227], v[44:47]\n"
"v_exp_f32 v160, v160\n"
"v_mfma_f32_16x16x32_bf16 v[44:47], v[104:107], a[252:255], v[44:47]\n"
"v_exp_f32 v155, v155\n"
"v_mfma_f32_16x16x32_bf16 v[12:15], v[100:103], v[224:227], v[12:15]\n"
"ds_read_b64_tr_b16 v[226:227], v239 offset:53760\n"
"v_mfma_f32_16x16x32_bf16 v[12:15], v[96:99], a[252:255], v[12:15]\n"
"ds_read_b64_tr_b16 a[248:249], v238 offset:49664\n"
"v_mfma_f32_16x16x32_bf16 a[220:223], v[120:123], a[252:255], a[220:223]\n"
"v_exp_f32 v154, v154\n"
"v_smfmac_f32_16x16x64_bf16 v[196:199], a[240:243], v[96:103], v241\n"
"ds_read_b64_tr_b16 a[250:251], v238 offset:53760\n"
"v_mfma_f32_16x16x32_bf16 v[202:205], a[240:243], v[120:123], v[202:205]\n"
"s_waitcnt lgkmcnt(3)\n"
"v_exp_f32 v152, v152\n"
"v_mfma_f32_16x16x32_bf16 v[168:171], v[216:219], a[8:11], v[180:183]\n"
"ds_read_b64_tr_b16 a[244:245], v234 offset:49664\n"
"v_cvt_pk_bf16_f32 v128, v152, v153\n"
"v_mfma_f32_16x16x32_bf16 v[168:171], v[220:223], a[12:15], v[168:171]\n"
"v_exp_f32 v165, v165\n"
"v_mfma_f32_16x16x32_bf16 v[168:171], v[208:211], a[0:3], v[168:171]\n"
"ds_read_b64_tr_b16 a[246:247], v234 offset:53760\n"
"v_cvt_pk_bf16_f32 v145, v162, v163\n"
"v_mfma_f32_16x16x32_bf16 v[168:171], v[212:215], a[4:7], v[168:171]\n"
"ds_read_b64_tr_b16 a[254:255], v235 offset:53760\n"
"v_mfma_f32_16x16x32_bf16 v[172:175], v[208:211], a[16:19], v[184:187]\n"
"ds_read_b64_tr_b16 v[224:225], v239 offset:49664\n"
"v_cvt_pk_bf16_f32 v129, v154, v155\n"
"v_mfma_f32_16x16x32_bf16 v[172:175], v[212:215], a[20:23], v[172:175]\n"
"v_mfma_f32_16x16x32_bf16 v[172:175], v[216:219], a[24:27], v[172:175]\n"
"s_add_u32 s40, s56, s54\n"
"ds_read_b64_tr_b16 a[252:253], v235 offset:49664\n"
"v_mfma_f32_16x16x32_bf16 v[172:175], v[220:223], a[28:31], v[172:175]\n"
"buffer_load_dwordx4 v207, s[20:23], s40 offen offset:3072 nt lds\n"
"v_mfma_f32_16x16x32_bf16 v[176:179], v[216:219], a[40:43], v[188:191]\n"
"s_add_u32 s55, s55, s59\n"
"v_exp_f32 v157, v157\n"
"v_mfma_f32_16x16x32_bf16 v[176:179], v[220:223], a[44:47], v[176:179]\n"
"s_or_b32 s55, s55, s58\n"
"v_exp_f32 v171, v171\n"
"v_mfma_f32_16x16x32_bf16 v[176:179], v[208:211], a[32:35], v[176:179]\n"
"s_xor_b32 s56, s55, s57\n"
"v_exp_f32 v159, v159\n"
"v_mfma_f32_16x16x32_bf16 v[176:179], v[212:215], a[36:39], v[176:179]\n"
"v_exp_f32 v167, v167\n"
"v_mfma_f32_16x16x32_bf16 v[152:155], v[208:211], a[192:195], v[192:195]\n"
"v_cvt_pk_bf16_f32 v137, v158, v159\n"
"v_mfma_f32_16x16x32_bf16 v[152:155], v[212:215], a[196:199], v[152:155]\n"
"ds_read_b128 v[208:211], v230 offset:0\n"
"v_cvt_pk_bf16_f32 v136, v156, v157\n"
"v_mfma_f32_16x16x32_bf16 v[152:155], v[216:219], a[200:203], v[152:155]\n"
"v_exp_f32 v166, v166\n"
"v_mfma_f32_16x16x32_bf16 v[152:155], v[220:223], a[204:207], v[152:155]\n"
"s_waitcnt lgkmcnt(1)\n"
"ds_read_b128 v[216:219], v230 offset:512\n"
"v_mfma_f32_16x16x32_bf16 a[232:235], v[120:123], a[244:247], a[232:235]\n"
"v_exp_f32 v169, v169\n"
"v_mfma_f32_16x16x32_bf16 v[24:27], v[96:99], a[244:247], v[24:27]\n"
"ds_read_b128 v[220:223], v231 offset:512\n"
"v_mfma_f32_16x16x32_bf16 v[24:27], v[100:103], a[248:251], v[24:27]\n"
"v_exp_f32 v164, v164\n"
"v_mfma_f32_16x16x32_bf16 v[56:59], v[104:107], a[244:247], v[56:59]\n"
"ds_read_b128 v[212:215], v231 offset:0\n"
"v_mfma_f32_16x16x32_bf16 v[56:59], v[108:111], a[248:251], v[56:59]\n"
"v_exp_f32 v178, v178\n"
"v_mfma_f32_16x16x32_bf16 v[88:91], v[112:115], a[244:247], v[88:91]\n"
"s_add_u32 m0, s44, 32768\n"
"ds_read_b64_tr_b16 a[246:247], v232 offset:53248\n"
"v_mfma_f32_16x16x32_bf16 v[88:91], v[116:119], a[248:251], v[88:91]\n"
"buffer_load_dwordx4 v240, s[16:19], s56 offen nt lds\n"
"v_mfma_f32_16x16x32_bf16 v[92:95], v[116:119], v[224:227], v[92:95]\n"
"v_exp_f32 v175, v175\n"
"v_mfma_f32_16x16x32_bf16 v[92:95], v[112:115], a[252:255], v[92:95]\n"
"v_exp_f32 v168, v168\n"
"v_mfma_f32_16x16x32_bf16 v[60:63], v[108:111], v[224:227], v[60:63]\n"
"v_exp_f32 v161, v161\n"
"v_mfma_f32_16x16x32_bf16 v[60:63], v[104:107], a[252:255], v[60:63]\n"
"v_exp_f32 v177, v177\n"
"v_mfma_f32_16x16x32_bf16 v[28:31], v[100:103], v[224:227], v[28:31]\n"
"ds_read_b64_tr_b16 v[226:227], v237 offset:53248\n"
"v_mfma_f32_16x16x32_bf16 v[28:31], v[96:99], a[252:255], v[28:31]\n"
"v_exp_f32 v172, v172\n"
"v_mfma_f32_16x16x32_bf16 a[236:239], v[120:123], a[252:255], a[236:239]\n"
"ds_read_b64_tr_b16 a[250:251], v236 offset:53248\n"
"v_cvt_pk_bf16_f32 v124, v164, v165\n"
"v_smfmac_f32_16x16x64_bf16 v[198:201], a[240:243], v[104:111], v241\n"
"s_waitcnt lgkmcnt(2)\n"
"ds_read_b64_tr_b16 a[252:253], v233 offset:49152\n"
"v_mfma_f32_16x16x32_bf16 v[156:159], v[208:211], a[0:3], v[180:183]\n"
"ds_read_b64_tr_b16 a[248:249], v236 offset:49152\n"
"v_cvt_pk_bf16_f32 v125, v166, v167\n"
"v_mfma_f32_16x16x32_bf16 v[156:159], v[212:215], a[4:7], v[156:159]\n"
"ds_read_b64_tr_b16 v[224:225], v237 offset:49152\n"
"v_cvt_pk_bf16_f32 v130, v168, v169\n"
"v_mfma_f32_16x16x32_bf16 v[156:159], v[216:219], a[8:11], v[156:159]\n"
"ds_read_b64_tr_b16 a[254:255], v233 offset:53248\n"
"v_cvt_pk_bf16_f32 v144, v160, v161\n"
"v_mfma_f32_16x16x32_bf16 v[156:159], v[220:223], a[12:15], v[156:159]\n"
"ds_read_b64_tr_b16 a[244:245], v232 offset:49152\n"
"v_mfma_f32_16x16x32_bf16 v[160:163], v[216:219], a[24:27], v[184:187]\n"
"v_exp_f32 v176, v176\n"
"v_mfma_f32_16x16x32_bf16 v[160:163], v[220:223], a[28:31], v[160:163]\n"
"v_exp_f32 v152, v152\n"
"v_mfma_f32_16x16x32_bf16 v[160:163], v[208:211], a[16:19], v[160:163]\n"
"s_add_u32 s40, s56, s52\n"
"v_cvt_pk_bf16_f32 v146, v176, v177\n"
"v_mfma_f32_16x16x32_bf16 v[160:163], v[212:215], a[20:23], v[160:163]\n"
"buffer_load_dwordx4 v207, s[16:19], s40 offen offset:1024 nt lds\n"
"v_mfma_f32_16x16x32_bf16 v[164:167], v[208:211], a[32:35], v[188:191]\n"
"v_exp_f32 v170, v170\n"
"v_mfma_f32_16x16x32_bf16 v[164:167], v[212:215], a[36:39], v[164:167]\n"
"v_exp_f32 v173, v173\n"
"v_mfma_f32_16x16x32_bf16 v[164:167], v[216:219], a[40:43], v[164:167]\n"
"v_exp_f32 v153, v153\n"
"v_mfma_f32_16x16x32_bf16 v[164:167], v[220:223], a[44:47], v[164:167]\n"
"s_waitcnt lgkmcnt(0)\n"
"v_exp_f32 v174, v174\n"
"v_mfma_f32_16x16x32_bf16 a[208:211], v[120:123], a[244:247], a[208:211]\n"
"ds_read_b128 v[208:211], v230 offset:4096\n"
"v_cvt_pk_bf16_f32 v139, v174, v175\n"
"v_mfma_f32_16x16x32_bf16 v[0:3], v[96:99], a[244:247], v[0:3]\n"
"v_exp_f32 v156, v156\n"
"v_mfma_f32_16x16x32_bf16 v[0:3], v[100:103], a[248:251], v[0:3]\n"
"ds_read_b128 v[220:223], v231 offset:4608\n"
"v_cvt_pk_bf16_f32 v138, v172, v173\n"
"v_mfma_f32_16x16x32_bf16 v[32:35], v[104:107], a[244:247], v[32:35]\n"
"v_exp_f32 v161, v161\n"
"v_mfma_f32_16x16x32_bf16 v[32:35], v[108:111], a[248:251], v[32:35]\n"
"ds_read_b128 v[216:219], v230 offset:4608\n"
"v_cvt_pk_bf16_f32 v126, v152, v153\n"
"v_mfma_f32_16x16x32_bf16 v[64:67], v[112:115], a[244:247], v[64:67]\n"
"s_add_u32 s40, s56, s53\n"
"ds_read_b128 v[212:215], v231 offset:4096\n"
"v_mfma_f32_16x16x32_bf16 v[64:67], v[116:119], a[248:251], v[64:67]\n"
"buffer_load_dwordx4 v240, s[16:19], s40 offen offset:2048 nt lds\n"
"v_mfma_f32_16x16x32_bf16 v[68:71], v[116:119], v[224:227], v[68:71]\n"
"v_exp_f32 v159, v159\n"
"v_mfma_f32_16x16x32_bf16 v[68:71], v[112:115], a[252:255], v[68:71]\n"
"v_exp_f32 v155, v155\n"
"v_mfma_f32_16x16x32_bf16 v[36:39], v[108:111], v[224:227], v[36:39]\n"
"v_exp_f32 v160, v160\n"
"v_mfma_f32_16x16x32_bf16 v[36:39], v[104:107], a[252:255], v[36:39]\n"
"v_exp_f32 v179, v179\n"
"v_mfma_f32_16x16x32_bf16 v[4:7], v[100:103], v[224:227], v[4:7]\n"
"ds_read_b64_tr_b16 a[244:245], v232 offset:49664\n"
"ds_read_b64_tr_b16 v[224:225], v237 offset:49664\n"
"v_mfma_f32_16x16x32_bf16 v[4:7], v[96:99], a[252:255], v[4:7]\n"
"v_exp_f32 v158, v158\n"
"v_mfma_f32_16x16x32_bf16 a[212:215], v[120:123], a[252:255], a[212:215]\n"
"ds_read_b64_tr_b16 a[246:247], v232 offset:53760\n"
"ds_read_b64_tr_b16 a[250:251], v236 offset:53760\n"
"v_smfmac_f32_16x16x64_bf16 v[200:203], a[240:243], v[112:119], v241\n"
"s_waitcnt lgkmcnt(4)\n"
"ds_read_b64_tr_b16 v[226:227], v237 offset:53760\n"
"v_cvt_pk_bf16_f32 v131, v170, v171\n"
"v_mfma_f32_16x16x32_bf16 v[168:171], v[208:211], a[0:3], v[180:183]\n"
"v_exp_f32 v164, v164\n"
"v_mfma_f32_16x16x32_bf16 v[168:171], v[212:215], a[4:7], v[168:171]\n"
"ds_read_b64_tr_b16 a[248:249], v236 offset:49664\n"
"ds_read_b64_tr_b16 a[254:255], v233 offset:53760\n"
"v_mfma_f32_16x16x32_bf16 v[168:171], v[216:219], a[8:11], v[168:171]\n"
"s_add_u32 s40, s56, s54\n"
"ds_read_b64_tr_b16 a[252:253], v233 offset:49664\n"
"v_mfma_f32_16x16x32_bf16 v[168:171], v[220:223], a[12:15], v[168:171]\n"
"buffer_load_dwordx4 v207, s[16:19], s40 offen offset:3072 nt lds\n"
"v_mfma_f32_16x16x32_bf16 v[172:175], v[216:219], a[24:27], v[184:187]\n"
"v_exp_f32 v154, v154\n"
"v_mfma_f32_16x16x32_bf16 v[172:175], v[220:223], a[28:31], v[172:175]\n"
"v_exp_f32 v167, v167\n"
"v_mfma_f32_16x16x32_bf16 v[172:175], v[208:211], a[16:19], v[172:175]\n"
"v_exp_f32 v163, v163\n"
"v_mfma_f32_16x16x32_bf16 v[172:175], v[212:215], a[20:23], v[172:175]\n"
"s_cbranch_vccnz LPCL0_%=\nLPKL0_%=:\n"
"v_cvt_pk_bf16_f32 v147, v178, v179\n"
"v_mfma_f32_16x16x32_bf16 v[176:179], v[208:211], a[32:35], v[188:191]\n"
"v_exp_f32 v165, v165\n"
"v_mfma_f32_16x16x32_bf16 v[176:179], v[212:215], a[36:39], v[176:179]\n"
"v_exp_f32 v157, v157\n"
"v_mfma_f32_16x16x32_bf16 v[176:179], v[216:219], a[40:43], v[176:179]\n"
"v_exp_f32 v166, v166\n"
"v_mfma_f32_16x16x32_bf16 v[176:179], v[220:223], a[44:47], v[176:179]\n"
"s_waitcnt lgkmcnt(0)\n"
"v_mfma_f32_16x16x32_bf16 a[224:227], v[120:123], a[244:247], a[224:227]\n"
"v_exp_f32 v162, v162\n"
"v_mfma_f32_16x16x32_bf16 v[16:19], v[96:99], a[244:247], v[16:19]\n"
"s_waitcnt vmcnt(8)\n"
"v_exp_f32 v168, v168\n"
"v_mfma_f32_16x16x32_bf16 v[16:19], v[100:103], a[248:251], v[16:19]\n"
"s_barrier\n"
"v_mfma_f32_16x16x32_bf16 v[48:51], v[104:107], a[244:247], v[48:51]\n"
"ds_read_b128 v[208:211], v228 offset:16384\n"
"v_cvt_pk_bf16_f32 v133, v158, v159\n"
"v_mfma_f32_16x16x32_bf16 v[48:51], v[108:111], a[248:251], v[48:51]\n"
"ds_read_b128 v[216:219], v228 offset:16896\n"
"v_cvt_pk_bf16_f32 v141, v162, v163\n"
"v_mfma_f32_16x16x32_bf16 v[80:83], v[112:115], a[244:247], v[80:83]\n"
"ds_read_b128 v[220:223], v229 offset:16896\n"
"v_cvt_pk_bf16_f32 v127, v154, v155\n"
"v_mfma_f32_16x16x32_bf16 v[80:83], v[116:119], a[248:251], v[80:83]\n"
"ds_read_b128 v[212:215], v229 offset:16384\n"
"v_cvt_pk_bf16_f32 v149, v166, v167\n"
"v_mfma_f32_16x16x32_bf16 v[84:87], v[116:119], v[224:227], v[84:87]\n"
"v_exp_f32 v176, v176\n"
"v_mfma_f32_16x16x32_bf16 v[84:87], v[112:115], a[252:255], v[84:87]\n"
"v_exp_f32 v169, v169\n"
"v_mfma_f32_16x16x32_bf16 v[52:55], v[108:111], v[224:227], v[52:55]\n"
"ds_read_b64_tr_b16 a[246:247], v234 offset:4096\n"
"v_cvt_pk_bf16_f32 v148, v164, v165\n"
"v_mfma_f32_16x16x32_bf16 v[52:55], v[104:107], a[252:255], v[52:55]\n"
"v_exp_f32 v177, v177\n"
"v_mfma_f32_16x16x32_bf16 v[20:23], v[100:103], v[224:227], v[20:23]\n"
"ds_read_b64_tr_b16 a[250:251], v238 offset:4096\n"
"v_cvt_pk_bf16_f32 v150, v176, v177\n"
"v_mfma_f32_16x16x32_bf16 v[20:23], v[96:99], a[252:255], v[20:23]\n"
"v_exp_f32 v174, v174\n"
"v_mfma_f32_16x16x32_bf16 a[228:231], v[120:123], a[252:255], a[228:231]\n"
"s_waitcnt lgkmcnt(3)\n"
"ds_read_b64_tr_b16 a[254:255], v235 offset:4096\n"
"v_cvt_pk_bf16_f32 v132, v156, v157\n"
"v_mfma_f32_16x16x32_bf16 v[152:155], v[208:211], a[0:3], v[180:183]\n"
"s_waitcnt lgkmcnt(3)\n"
"ds_read_b64_tr_b16 a[248:249], v238 offset:0\n"
"v_cvt_pk_bf16_f32 v134, v168, v169\n"
"v_mfma_f32_16x16x32_bf16 v[152:155], v[212:215], a[4:7], v[152:155]\n"
"v_exp_f32 v172, v172\n"
"v_mfma_f32_16x16x32_bf16 v[152:155], v[216:219], a[8:11], v[152:155]\n"
"ds_read_b64_tr_b16 a[244:245], v234 offset:0\n"
"v_cvt_pk_bf16_f32 v140, v160, v161\n"
"v_mfma_f32_16x16x32_bf16 v[152:155], v[220:223], a[12:15], v[152:155]\n"
"v_exp_f32 v175, v175\n"
"v_mfma_f32_16x16x32_bf16 v[156:159], v[216:219], a[24:27], v[184:187]\n"
"ds_read_b64_tr_b16 a[252:253], v235 offset:0\n"
"v_cvt_pk_bf16_f32 v143, v174, v175\n"
"v_mfma_f32_16x16x32_bf16 v[156:159], v[220:223], a[28:31], v[156:159]\n"
"ds_read_b64_tr_b16 v[224:225], v239 offset:0\n"
"v_mfma_f32_16x16x32_bf16 v[156:159], v[208:211], a[16:19], v[156:159]\n"
"s_add_u32 m0, s44, 98304\n"
"ds_read_b64_tr_b16 v[226:227], v239 offset:4096\n"
"v_mfma_f32_16x16x32_bf16 v[156:159], v[212:215], a[20:23], v[156:159]\n"
"buffer_load_dwordx4 v240, s[20:23], s56 offen nt lds\n"
"v_mfma_f32_16x16x32_bf16 v[160:163], v[208:211], a[32:35], v[188:191]\n"
"v_exp_f32 v178, v178\n"
"v_mfma_f32_16x16x32_bf16 v[160:163], v[212:215], a[36:39], v[160:163]\n"
"v_exp_f32 v171, v171\n"
"v_mfma_f32_16x16x32_bf16 v[160:163], v[216:219], a[40:43], v[160:163]\n"
"s_add_u32 s40, s56, s52\n"
"v_mfma_f32_16x16x32_bf16 v[160:163], v[220:223], a[44:47], v[160:163]\n"
"buffer_load_dwordx4 v207, s[20:23], s40 offen offset:1024 nt lds\n"
"v_mfma_f32_16x16x32_bf16 v[164:167], v[216:219], a[200:203], v[192:195]\n"
"v_exp_f32 v170, v170\n"
"v_mfma_f32_16x16x32_bf16 v[164:167], v[220:223], a[204:207], v[164:167]\n"
"v_exp_f32 v153, v153\n"
"v_mfma_f32_16x16x32_bf16 v[164:167], v[208:211], a[192:195], v[164:167]\n"
"v_exp_f32 v179, v179\n"
"v_mfma_f32_16x16x32_bf16 v[164:167], v[212:215], a[196:199], v[164:167]\n"
"s_waitcnt lgkmcnt(0)\n"
"v_exp_f32 v158, v158\n"
"v_mfma_f32_16x16x32_bf16 a[216:219], v[124:127], a[244:247], a[216:219]\n"
"ds_read_b128 v[216:219], v228 offset:20992\n"
"v_cvt_pk_bf16_f32 v135, v170, v171\n"
"v_mfma_f32_16x16x32_bf16 v[8:11], v[128:131], a[244:247], v[8:11]\n"
"v_exp_f32 v173, v173\n"
"v_mfma_f32_16x16x32_bf16 v[8:11], v[132:135], a[248:251], v[8:11]\n"
"ds_read_b128 v[208:211], v228 offset:20480\n"
"v_cvt_pk_bf16_f32 v142, v172, v173\n"
"v_mfma_f32_16x16x32_bf16 v[40:43], v[136:139], a[244:247], v[40:43]\n"
"v_exp_f32 v162, v162\n"
"v_mfma_f32_16x16x32_bf16 v[40:43], v[140:143], a[248:251], v[40:43]\n"
"ds_read_b128 v[220:223], v229 offset:20992\n"
"v_cvt_pk_bf16_f32 v151, v178, v179\n"
"v_mfma_f32_16x16x32_bf16 v[72:75], v[144:147], a[244:247], v[72:75]\n"
"s_add_u32 s40, s56, s53\n"
"ds_read_b128 v[212:215], v229 offset:20480\n"
"v_mfma_f32_16x16x32_bf16 v[72:75], v[148:151], a[248:251], v[72:75]\n"
"buffer_load_dwordx4 v240, s[20:23], s40 offen offset:2048 nt lds\n"
"v_mfma_f32_16x16x32_bf16 v[76:79], v[148:151], v[224:227], v[76:79]\n"
"v_exp_f32 v163, v163\n"
"v_mfma_f32_16x16x32_bf16 v[76:79], v[144:147], a[252:255], v[76:79]\n"
"v_exp_f32 v156, v156\n"
"v_mfma_f32_16x16x32_bf16 v[44:47], v[140:143], v[224:227], v[44:47]\n"
"v_exp_f32 v160, v160\n"
"v_mfma_f32_16x16x32_bf16 v[44:47], v[136:139], a[252:255], v[44:47]\n"
"v_exp_f32 v155, v155\n"
"v_mfma_f32_16x16x32_bf16 v[12:15], v[132:135], v[224:227], v[12:15]\n"
"ds_read_b64_tr_b16 v[226:227], v239 offset:4608\n"
"v_mfma_f32_16x16x32_bf16 v[12:15], v[128:131], a[252:255], v[12:15]\n"
"ds_read_b64_tr_b16 a[248:249], v238 offset:512\n"
"v_mfma_f32_16x16x32_bf16 a[220:223], v[124:127], a[252:255], a[220:223]\n"
"v_exp_f32 v154, v154\n"
"v_smfmac_f32_16x16x64_bf16 v[196:199], a[240:243], v[128:135], v241\n"
"ds_read_b64_tr_b16 a[250:251], v238 offset:4608\n"
"v_mfma_f32_16x16x32_bf16 v[202:205], a[240:243], v[124:127], v[202:205]\n"
"s_waitcnt lgkmcnt(3)\n"
"v_exp_f32 v152, v152\n"
"v_mfma_f32_16x16x32_bf16 v[168:171], v[216:219], a[8:11], v[180:183]\n"
"ds_read_b64_tr_b16 a[244:245], v234 offset:512\n"
"v_cvt_pk_bf16_f32 v96, v152, v153\n"
"v_mfma_f32_16x16x32_bf16 v[168:171], v[220:223], a[12:15], v[168:171]\n"
"v_exp_f32 v165, v165\n"
"v_mfma_f32_16x16x32_bf16 v[168:171], v[208:211], a[0:3], v[168:171]\n"
"ds_read_b64_tr_b16 a[246:247], v234 offset:4608\n"
"v_cvt_pk_bf16_f32 v113, v162, v163\n"
"v_mfma_f32_16x16x32_bf16 v[168:171], v[212:215], a[4:7], v[168:171]\n"
"ds_read_b64_tr_b16 a[254:255], v235 offset:4608\n"
"v_mfma_f32_16x16x32_bf16 v[172:175], v[208:211], a[16:19], v[184:187]\n"
"ds_read_b64_tr_b16 v[224:225], v239 offset:512\n"
"v_cvt_pk_bf16_f32 v97, v154, v155\n"
"v_mfma_f32_16x16x32_bf16 v[172:175], v[212:215], a[20:23], v[172:175]\n"
"v_mfma_f32_16x16x32_bf16 v[172:175], v[216:219], a[24:27], v[172:175]\n"
"s_add_u32 s40, s56, s54\n"
"ds_read_b64_tr_b16 a[252:253], v235 offset:512\n"
"v_mfma_f32_16x16x32_bf16 v[172:175], v[220:223], a[28:31], v[172:175]\n"
"buffer_load_dwordx4 v207, s[20:23], s40 offen offset:3072 nt lds\n"
"v_mfma_f32_16x16x32_bf16 v[176:179], v[216:219], a[40:43], v[188:191]\n"
"s_add_u32 s55, s55, s59\n"
"v_exp_f32 v157, v157\n"
"v_mfma_f32_16x16x32_bf16 v[176:179], v[220:223], a[44:47], v[176:179]\n"
"s_or_b32 s55, s55, s58\n"
"v_exp_f32 v171, v171\n"
"v_mfma_f32_16x16x32_bf16 v[176:179], v[208:211], a[32:35], v[176:179]\n"
"s_xor_b32 s56, s55, s57\n"
"v_exp_f32 v159, v159\n"
"v_mfma_f32_16x16x32_bf16 v[176:179], v[212:215], a[36:39], v[176:179]\n"
"v_exp_f32 v167, v167\n"
"v_mfma_f32_16x16x32_bf16 v[152:155], v[208:211], a[192:195], v[192:195]\n"
"v_cvt_pk_bf16_f32 v105, v158, v159\n"
"v_mfma_f32_16x16x32_bf16 v[152:155], v[212:215], a[196:199], v[152:155]\n"
"ds_read_b128 v[208:211], v230 offset:16384\n"
"v_cvt_pk_bf16_f32 v104, v156, v157\n"
"v_mfma_f32_16x16x32_bf16 v[152:155], v[216:219], a[200:203], v[152:155]\n"
"v_exp_f32 v166, v166\n"
"v_mfma_f32_16x16x32_bf16 v[152:155], v[220:223], a[204:207], v[152:155]\n"
"s_waitcnt lgkmcnt(1)\n"
"ds_read_b128 v[216:219], v230 offset:16896\n"
"v_mfma_f32_16x16x32_bf16 a[232:235], v[124:127], a[244:247], a[232:235]\n"
"v_exp_f32 v169, v169\n"
"v_mfma_f32_16x16x32_bf16 v[24:27], v[128:131], a[244:247], v[24:27]\n"
"ds_read_b128 v[220:223], v231 offset:16896\n"
"v_mfma_f32_16x16x32_bf16 v[24:27], v[132:135], a[248:251], v[24:27]\n"
"v_exp_f32 v164, v164\n"
"v_mfma_f32_16x16x32_bf16 v[56:59], v[136:139], a[244:247], v[56:59]\n"
"ds_read_b128 v[212:215], v231 offset:16384\n"
"v_mfma_f32_16x16x32_bf16 v[56:59], v[140:143], a[248:251], v[56:59]\n"
"v_exp_f32 v178, v178\n"
"v_mfma_f32_16x16x32_bf16 v[88:91], v[144:147], a[244:247], v[88:91]\n"
"s_add_u32 m0, s44, 49152\n"
"ds_read_b64_tr_b16 a[246:247], v232 offset:4096\n"
"v_mfma_f32_16x16x32_bf16 v[88:91], v[148:151], a[248:251], v[88:91]\n"
"buffer_load_dwordx4 v240, s[16:19], s56 offen nt lds\n"
"v_mfma_f32_16x16x32_bf16 v[92:95], v[148:151], v[224:227], v[92:95]\n"
"v_exp_f32 v175, v175\n"
"v_mfma_f32_16x16x32_bf16 v[92:95], v[144:147], a[252:255], v[92:95]\n"
"v_exp_f32 v168, v168\n"
"v_mfma_f32_16x16x32_bf16 v[60:63], v[140:143], v[224:227], v[60:63]\n"
"v_exp_f32 v161, v161\n"
"v_mfma_f32_16x16x32_bf16 v[60:63], v[136:139], a[252:255], v[60:63]\n"
"v_exp_f32 v177, v177\n"
"v_mfma_f32_16x16x32_bf16 v[28:31], v[132:135], v[224:227], v[28:31]\n"
"ds_read_b64_tr_b16 v[226:227], v237 offset:4096\n"
"v_mfma_f32_16x16x32_bf16 v[28:31], v[128:131], a[252:255], v[28:31]\n"
"v_exp_f32 v172, v172\n"
"v_mfma_f32_16x16x32_bf16 a[236:239], v[124:127], a[252:255], a[236:239]\n"
"ds_read_b64_tr_b16 a[250:251], v236 offset:4096\n"
"v_cvt_pk_bf16_f32 v120, v164, v165\n"
"v_smfmac_f32_16x16x64_bf16 v[198:201], a[240:243], v[136:143], v241\n"
"s_waitcnt lgkmcnt(2)\n"
"ds_read_b64_tr_b16 a[252:253], v233 offset:0\n"
"v_mfma_f32_16x16x32_bf16 v[156:159], v[208:211], a[0:3], v[180:183]\n"
"ds_read_b64_tr_b16 a[248:249], v236 offset:0\n"
"v_cvt_pk_bf16_f32 v121, v166, v167\n"
"v_mfma_f32_16x16x32_bf16 v[156:159], v[212:215], a[4:7], v[156:159]\n"
"ds_read_b64_tr_b16 v[224:225], v237 offset:0\n"
"v_cvt_pk_bf16_f32 v98, v168, v169\n"
"v_mfma_f32_16x16x32_bf16 v[156:159], v[216:219], a[8:11], v[156:159]\n"
"ds_read_b64_tr_b16 a[254:255], v233 offset:4096\n"
"v_cvt_pk_bf16_f32 v112, v160, v161\n"
"v_mfma_f32_16x16x32_bf16 v[156:159], v[220:223], a[12:15], v[156:159]\n"
"ds_read_b64_tr_b16 a[244:245], v232 offset:0\n"
"v_mfma_f32_16x16x32_bf16 v[160:163], v[216:219], a[24:27], v[184:187]\n"
"v_exp_f32 v176, v176\n"
"v_mfma_f32_16x16x32_bf16 v[160:163], v[220:223], a[28:31], v[160:163]\n"
"v_exp_f32 v152, v152\n"
"v_mfma_f32_16x16x32_bf16 v[160:163], v[208:211], a[16:19], v[160:163]\n"
"s_add_u32 s40, s56, s52\n"
"v_cvt_pk_bf16_f32 v114, v176, v177\n"
"v_mfma_f32_16x16x32_bf16 v[160:163], v[212:215], a[20:23], v[160:163]\n"
"buffer_load_dwordx4 v207, s[16:19], s40 offen offset:1024 nt lds\n"
"v_mfma_f32_16x16x32_bf16 v[164:167], v[208:211], a[32:35], v[188:191]\n"
"v_exp_f32 v170, v170\n"
"v_mfma_f32_16x16x32_bf16 v[164:167], v[212:215], a[36:39], v[164:167]\n"
"v_exp_f32 v173, v173\n"
"v_mfma_f32_16x16x32_bf16 v[164:167], v[216:219], a[40:43], v[164:167]\n"
"v_exp_f32 v153, v153\n"
"v_mfma_f32_16x16x32_bf16 v[164:167], v[220:223], a[44:47], v[164:167]\n"
"s_waitcnt lgkmcnt(0)\n"
"v_exp_f32 v174, v174\n"
"v_mfma_f32_16x16x32_bf16 a[208:211], v[124:127], a[244:247], a[208:211]\n"
"ds_read_b128 v[208:211], v230 offset:20480\n"
"v_cvt_pk_bf16_f32 v107, v174, v175\n"
"v_mfma_f32_16x16x32_bf16 v[0:3], v[128:131], a[244:247], v[0:3]\n"
"v_exp_f32 v156, v156\n"
"v_mfma_f32_16x16x32_bf16 v[0:3], v[132:135], a[248:251], v[0:3]\n"
"ds_read_b128 v[220:223], v231 offset:20992\n"
"v_cvt_pk_bf16_f32 v106, v172, v173\n"
"v_mfma_f32_16x16x32_bf16 v[32:35], v[136:139], a[244:247], v[32:35]\n"
"v_exp_f32 v161, v161\n"
"v_mfma_f32_16x16x32_bf16 v[32:35], v[140:143], a[248:251], v[32:35]\n"
"ds_read_b128 v[216:219], v230 offset:20992\n"
"v_cvt_pk_bf16_f32 v122, v152, v153\n"
"v_mfma_f32_16x16x32_bf16 v[64:67], v[144:147], a[244:247], v[64:67]\n"
"s_add_u32 s40, s56, s53\n"
"ds_read_b128 v[212:215], v231 offset:20480\n"
"v_mfma_f32_16x16x32_bf16 v[64:67], v[148:151], a[248:251], v[64:67]\n"
"buffer_load_dwordx4 v240, s[16:19], s40 offen offset:2048 nt lds\n"
"v_mfma_f32_16x16x32_bf16 v[68:71], v[148:151], v[224:227], v[68:71]\n"
"v_exp_f32 v159, v159\n"
"v_mfma_f32_16x16x32_bf16 v[68:71], v[144:147], a[252:255], v[68:71]\n"
"v_exp_f32 v155, v155\n"
"v_mfma_f32_16x16x32_bf16 v[36:39], v[140:143], v[224:227], v[36:39]\n"
"v_exp_f32 v160, v160\n"
"v_mfma_f32_16x16x32_bf16 v[36:39], v[136:139], a[252:255], v[36:39]\n"
"v_exp_f32 v179, v179\n"
"v_mfma_f32_16x16x32_bf16 v[4:7], v[132:135], v[224:227], v[4:7]\n"
"ds_read_b64_tr_b16 a[244:245], v232 offset:512\n"
"ds_read_b64_tr_b16 v[224:225], v237 offset:512\n"
"v_mfma_f32_16x16x32_bf16 v[4:7], v[128:131], a[252:255], v[4:7]\n"
"v_exp_f32 v158, v158\n"
"v_mfma_f32_16x16x32_bf16 a[212:215], v[124:127], a[252:255], a[212:215]\n"
"ds_read_b64_tr_b16 a[246:247], v232 offset:4608\n"
"ds_read_b64_tr_b16 a[250:251], v236 offset:4608\n"
"v_smfmac_f32_16x16x64_bf16 v[200:203], a[240:243], v[144:151], v241\n"
"s_waitcnt lgkmcnt(4)\n"
"ds_read_b64_tr_b16 v[226:227], v237 offset:4608\n"
"v_cvt_pk_bf16_f32 v99, v170, v171\n"
"v_mfma_f32_16x16x32_bf16 v[168:171], v[208:211], a[0:3], v[180:183]\n"
"v_exp_f32 v164, v164\n"
"v_mfma_f32_16x16x32_bf16 v[168:171], v[212:215], a[4:7], v[168:171]\n"
"ds_read_b64_tr_b16 a[248:249], v236 offset:512\n"
"ds_read_b64_tr_b16 a[254:255], v233 offset:4608\n"
"v_mfma_f32_16x16x32_bf16 v[168:171], v[216:219], a[8:11], v[168:171]\n"
"s_add_u32 s40, s56, s54\n"
"ds_read_b64_tr_b16 a[252:253], v233 offset:512\n"
"v_mfma_f32_16x16x32_bf16 v[168:171], v[220:223], a[12:15], v[168:171]\n"
"buffer_load_dwordx4 v207, s[16:19], s40 offen offset:3072 nt lds\n"
"v_mfma_f32_16x16x32_bf16 v[172:175], v[216:219], a[24:27], v[184:187]\n"
"v_exp_f32 v154, v154\n"
"v_mfma_f32_16x16x32_bf16 v[172:175], v[220:223], a[28:31], v[172:175]\n"
"v_exp_f32 v167, v167\n"
"v_mfma_f32_16x16x32_bf16 v[172:175], v[208:211], a[16:19], v[172:175]\n"
"v_exp_f32 v163, v163\n"
"v_mfma_f32_16x16x32_bf16 v[172:175], v[212:215], a[20:23], v[172:175]\n"
"s_cbranch_vccnz LPCL1_%=\nLPKL1_%=:\n"
"v_cvt_pk_bf16_f32 v115, v178, v179\n"
"v_mfma_f32_16x16x32_bf16 v[176:179], v[208:211], a[32:35], v[188:191]\n"
"v_exp_f32 v165, v165\n"
"v_mfma_f32_16x16x32_bf16 v[176:179], v[212:215], a[36:39], v[176:179]\n"
"v_exp_f32 v157, v157\n"
"v_mfma_f32_16x16x32_bf16 v[176:179], v[216:219], a[40:43], v[176:179]\n"
"v_exp_f32 v166, v166\n"
"v_mfma_f32_16x16x32_bf16 v[176:179], v[220:223], a[44:47], v[176:179]\n"
"s_waitcnt lgkmcnt(0)\n"
"v_mfma_f32_16x16x32_bf16 a[224:227], v[124:127], a[244:247], a[224:227]\n"
"v_exp_f32 v162, v162\n"
"v_mfma_f32_16x16x32_bf16 v[16:19], v[128:131], a[244:247], v[16:19]\n"
"s_waitcnt vmcnt(8)\n"
"v_exp_f32 v168, v168\n"
"v_mfma_f32_16x16x32_bf16 v[16:19], v[132:135], a[248:251], v[16:19]\n"
"s_barrier\n"
"v_mfma_f32_16x16x32_bf16 v[48:51], v[136:139], a[244:247], v[48:51]\n"
"ds_read_b128 v[208:211], v228 offset:32768\n"
"v_cvt_pk_bf16_f32 v101, v158, v159\n"
"v_mfma_f32_16x16x32_bf16 v[48:51], v[140:143], a[248:251], v[48:51]\n"
"ds_read_b128 v[216:219], v228 offset:33280\n"
"v_cvt_pk_bf16_f32 v109, v162, v163\n"
"v_mfma_f32_16x16x32_bf16 v[80:83], v[144:147], a[244:247], v[80:83]\n"
"ds_read_b128 v[220:223], v229 offset:33280\n"
"v_cvt_pk_bf16_f32 v123, v154, v155\n"
"v_mfma_f32_16x16x32_bf16 v[80:83], v[148:151], a[248:251], v[80:83]\n"
"ds_read_b128 v[212:215], v229 offset:32768\n"
"v_cvt_pk_bf16_f32 v117, v166, v167\n"
"v_mfma_f32_16x16x32_bf16 v[84:87], v[148:151], v[224:227], v[84:87]\n"
"v_exp_f32 v176, v176\n"
"v_mfma_f32_16x16x32_bf16 v[84:87], v[144:147], a[252:255], v[84:87]\n"
"v_exp_f32 v169, v169\n"
"v_mfma_f32_16x16x32_bf16 v[52:55], v[140:143], v[224:227], v[52:55]\n"
"ds_read_b64_tr_b16 a[246:247], v234 offset:20480\n"
"v_cvt_pk_bf16_f32 v116, v164, v165\n"
"v_mfma_f32_16x16x32_bf16 v[52:55], v[136:139], a[252:255], v[52:55]\n"
"v_exp_f32 v177, v177\n"
"v_mfma_f32_16x16x32_bf16 v[20:23], v[132:135], v[224:227], v[20:23]\n"
"ds_read_b64_tr_b16 a[250:251], v238 offset:20480\n"
"v_cvt_pk_bf16_f32 v118, v176, v177\n"
"v_mfma_f32_16x16x32_bf16 v[20:23], v[128:131], a[252:255], v[20:23]\n"
"v_exp_f32 v174, v174\n"
"v_mfma_f32_16x16x32_bf16 a[228:231], v[124:127], a[252:255], a[228:231]\n"
"s_waitcnt lgkmcnt(3)\n"
"ds_read_b64_tr_b16 a[254:255], v235 offset:20480\n"
"v_cvt_pk_bf16_f32 v100, v156, v157\n"
"LAY2_%=:\n"
"v_mfma_f32_16x16x32_bf16 v[152:155], v[208:211], a[0:3], v[180:183]\n"
"s_waitcnt lgkmcnt(3)\n"
"ds_read_b64_tr_b16 a[248:249], v238 offset:16384\n"
"v_cvt_pk_bf16_f32 v102, v168, v169\n"
"v_mfma_f32_16x16x32_bf16 v[152:155], v[212:215], a[4:7], v[152:155]\n"
"v_exp_f32 v172, v172\n"
"v_mfma_f32_16x16x32_bf16 v[152:155], v[216:219], a[8:11], v[152:155]\n"
"ds_read_b64_tr_b16 a[244:245], v234 offset:16384\n"
"v_cvt_pk_bf16_f32 v108, v160, v161\n"
"v_mfma_f32_16x16x32_bf16 v[152:155], v[220:223], a[12:15], v[152:155]\n"
"v_exp_f32 v175, v175\n"
"v_mfma_f32_16x16x32_bf16 v[156:159], v[216:219], a[24:27], v[184:187]\n"
"ds_read_b64_tr_b16 a[252:253], v235 offset:16384\n"
"v_cvt_pk_bf16_f32 v111, v174, v175\n"
"v_mfma_f32_16x16x32_bf16 v[156:159], v[220:223], a[28:31], v[156:159]\n"
"ds_read_b64_tr_b16 v[224:225], v239 offset:16384\n"
"v_mfma_f32_16x16x32_bf16 v[156:159], v[208:211], a[16:19], v[156:159]\n"
"s_add_u32 m0, s44, 114688\n"
"ds_read_b64_tr_b16 v[226:227], v239 offset:20480\n"
"v_mfma_f32_16x16x32_bf16 v[156:159], v[212:215], a[20:23], v[156:159]\n"
"buffer_load_dwordx4 v240, s[20:23], s56 offen nt lds\n"
"v_mfma_f32_16x16x32_bf16 v[160:163], v[208:211], a[32:35], v[188:191]\n"
"v_exp_f32 v178, v178\n"
"v_mfma_f32_16x16x32_bf16 v[160:163], v[212:215], a[36:39], v[160:163]\n"
"v_exp_f32 v171, v171\n"
"v_mfma_f32_16x16x32_bf16 v[160:163], v[216:219], a[40:43], v[160:163]\n"
"s_add_u32 s40, s56, s52\n"
"v_mfma_f32_16x16x32_bf16 v[160:163], v[220:223], a[44:47], v[160:163]\n"
"buffer_load_dwordx4 v207, s[20:23], s40 offen offset:1024 nt lds\n"
"v_mfma_f32_16x16x32_bf16 v[164:167], v[216:219], a[200:203], v[192:195]\n"
"v_exp_f32 v170, v170\n"
"v_mfma_f32_16x16x32_bf16 v[164:167], v[220:223], a[204:207], v[164:167]\n"
"v_exp_f32 v153, v153\n"
"v_mfma_f32_16x16x32_bf16 v[164:167], v[208:211], a[192:195], v[164:167]\n"
"v_exp_f32 v179, v179\n"
"v_mfma_f32_16x16x32_bf16 v[164:167], v[212:215], a[196:199], v[164:167]\n"
"s_waitcnt lgkmcnt(0)\n"
"v_exp_f32 v158, v158\n"
"v_mfma_f32_16x16x32_bf16 a[216:219], v[120:123], a[244:247], a[216:219]\n"
"ds_read_b128 v[216:219], v228 offset:37376\n"
"v_cvt_pk_bf16_f32 v103, v170, v171\n"
"v_mfma_f32_16x16x32_bf16 v[8:11], v[96:99], a[244:247], v[8:11]\n"
"v_exp_f32 v173, v173\n"
"v_mfma_f32_16x16x32_bf16 v[8:11], v[100:103], a[248:251], v[8:11]\n"
"ds_read_b128 v[208:211], v228 offset:36864\n"
"v_cvt_pk_bf16_f32 v110, v172, v173\n"
"v_mfma_f32_16x16x32_bf16 v[40:43], v[104:107], a[244:247], v[40:43]\n"
"v_exp_f32 v162, v162\n"
"v_mfma_f32_16x16x32_bf16 v[40:43], v[108:111], a[248:251], v[40:43]\n"
"ds_read_b128 v[220:223], v229 offset:37376\n"
"v_cvt_pk_bf16_f32 v119, v178, v179\n"
"v_mfma_f32_16x16x32_bf16 v[72:75], v[112:115], a[244:247], v[72:75]\n"
"s_add_u32 s40, s56, s53\n"
"ds_read_b128 v[212:215], v229 offset:36864\n"
"v_mfma_f32_16x16x32_bf16 v[72:75], v[116:119], a[248:251], v[72:75]\n"
"buffer_load_dwordx4 v240, s[20:23], s40 offen offset:2048 nt lds\n"
"v_mfma_f32_16x16x32_bf16 v[76:79], v[116:119], v[224:227], v[76:79]\n"
"v_exp_f32 v163, v163\n"
"v_mfma_f32_16x16x32_bf16 v[76:79], v[112:115], a[252:255], v[76:79]\n"
"v_exp_f32 v156, v156\n"
"v_mfma_f32_16x16x32_bf16 v[44:47], v[108:111], v[224:227], v[44:47]\n"
"v_exp_f32 v160, v160\n"
"v_mfma_f32_16x16x32_bf16 v[44:47], v[104:107], a[252:255], v[44:47]\n"
"v_exp_f32 v155, v155\n"
"v_mfma_f32_16x16x32_bf16 v[12:15], v[100:103], v[224:227], v[12:15]\n"
"ds_read_b64_tr_b16 v[226:227], v239 offset:20992\n"
"v_mfma_f32_16x16x32_bf16 v[12:15], v[96:99], a[252:255], v[12:15]\n"
"ds_read_b64_tr_b16 a[248:249], v238 offset:16896\n"
"v_mfma_f32_16x16x32_bf16 a[220:223], v[120:123], a[252:255], a[220:223]\n"
"v_exp_f32 v154, v154\n"
"v_smfmac_f32_16x16x64_bf16 v[196:199], a[240:243], v[96:103], v241\n"
"ds_read_b64_tr_b16 a[250:251], v238 offset:20992\n"
"v_mfma_f32_16x16x32_bf16 v[202:205], a[240:243], v[120:123], v[202:205]\n"
"s_waitcnt lgkmcnt(3)\n"
"v_exp_f32 v152, v152\n"
"v_mfma_f32_16x16x32_bf16 v[168:171], v[216:219], a[8:11], v[180:183]\n"
"ds_read_b64_tr_b16 a[244:245], v234 offset:16896\n"
"v_cvt_pk_bf16_f32 v128, v152, v153\n"
"v_mfma_f32_16x16x32_bf16 v[168:171], v[220:223], a[12:15], v[168:171]\n"
"v_exp_f32 v165, v165\n"
"v_mfma_f32_16x16x32_bf16 v[168:171], v[208:211], a[0:3], v[168:171]\n"
"ds_read_b64_tr_b16 a[246:247], v234 offset:20992\n"
"v_cvt_pk_bf16_f32 v145, v162, v163\n"
"v_mfma_f32_16x16x32_bf16 v[168:171], v[212:215], a[4:7], v[168:171]\n"
"ds_read_b64_tr_b16 a[254:255], v235 offset:20992\n"
"v_mfma_f32_16x16x32_bf16 v[172:175], v[208:211], a[16:19], v[184:187]\n"
"ds_read_b64_tr_b16 v[224:225], v239 offset:16896\n"
"v_cvt_pk_bf16_f32 v129, v154, v155\n"
"v_mfma_f32_16x16x32_bf16 v[172:175], v[212:215], a[20:23], v[172:175]\n"
"v_mfma_f32_16x16x32_bf16 v[172:175], v[216:219], a[24:27], v[172:175]\n"
"s_add_u32 s40, s56, s54\n"
"ds_read_b64_tr_b16 a[252:253], v235 offset:16896\n"
"v_mfma_f32_16x16x32_bf16 v[172:175], v[220:223], a[28:31], v[172:175]\n"
"buffer_load_dwordx4 v207, s[20:23], s40 offen offset:3072 nt lds\n"
"v_mfma_f32_16x16x32_bf16 v[176:179], v[216:219], a[40:43], v[188:191]\n"
"s_add_u32 s55, s55, s59\n"
"v_exp_f32 v157, v157\n"
"v_mfma_f32_16x16x32_bf16 v[176:179], v[220:223], a[44:47], v[176:179]\n"
"s_or_b32 s55, s55, s58\n"
"v_exp_f32 v171, v171\n"
"v_mfma_f32_16x16x32_bf16 v[176:179], v[208:211], a[32:35], v[176:179]\n"
"s_xor_b32 s56, s55, s57\n"
"v_exp_f32 v159, v159\n"
"v_mfma_f32_16x16x32_bf16 v[176:179], v[212:215], a[36:39], v[176:179]\n"
"s_cmp_ge_u32 s47, 15\n"
"v_exp_f32 v167, v167\n"
"v_mfma_f32_16x16x32_bf16 v[152:155], v[208:211], a[192:195], v[192:195]\n"
"s_cbranch_scc1 LSW_%=\nLSWK_%=:\n"
"v_cvt_pk_bf16_f32 v137, v158, v159\n"
"v_mfma_f32_16x16x32_bf16 v[152:155], v[212:215], a[196:199], v[152:155]\n"
"ds_read_b128 v[208:211], v230 offset:32768\n"
"v_cvt_pk_bf16_f32 v136, v156, v157\n"
"v_mfma_f32_16x16x32_bf16 v[152:155], v[216:219], a[200:203], v[152:155]\n"
"v_exp_f32 v166, v166\n"
"v_mfma_f32_16x16x32_bf16 v[152:155], v[220:223], a[204:207], v[152:155]\n"
"s_waitcnt lgkmcnt(1)\n"
"ds_read_b128 v[216:219], v230 offset:33280\n"
"v_mfma_f32_16x16x32_bf16 a[232:235], v[120:123], a[244:247], a[232:235]\n"
"v_exp_f32 v169, v169\n"
"v_mfma_f32_16x16x32_bf16 v[24:27], v[96:99], a[244:247], v[24:27]\n"
"ds_read_b128 v[220:223], v231 offset:33280\n"
"v_mfma_f32_16x16x32_bf16 v[24:27], v[100:103], a[248:251], v[24:27]\n"
"v_exp_f32 v164, v164\n"
"v_mfma_f32_16x16x32_bf16 v[56:59], v[104:107], a[244:247], v[56:59]\n"
"ds_read_b128 v[212:215], v231 offset:32768\n"
"v_mfma_f32_16x16x32_bf16 v[56:59], v[108:111], a[248:251], v[56:59]\n"
"v_exp_f32 v178, v178\n"
"v_mfma_f32_16x16x32_bf16 v[88:91], v[112:115], a[244:247], v[88:91]\n"
"s_mov_b32 m0, s44\n"
"ds_read_b64_tr_b16 a[246:247], v232 offset:20480\n"
"v_mfma_f32_16x16x32_bf16 v[88:91], v[116:119], a[248:251], v[88:91]\n"
"buffer_load_dwordx4 v240, s[16:19], s56 offen nt lds\n"
"v_mfma_f32_16x16x32_bf16 v[92:95], v[116:119], v[224:227], v[92:95]\n"
"v_exp_f32 v175, v175\n"
"v_mfma_f32_16x16x32_bf16 v[92:95], v[112:115], a[252:255], v[92:95]\n"
"v_exp_f32 v168, v168\n"
"v_mfma_f32_16x16x32_bf16 v[60:63], v[108:111], v[224:227], v[60:63]\n"
"v_exp_f32 v161, v161\n"
"v_mfma_f32_16x16x32_bf16 v[60:63], v[104:107], a[252:255], v[60:63]\n"
"v_exp_f32 v177, v177\n"
"v_mfma_f32_16x16x32_bf16 v[28:31], v[100:103], v[224:227], v[28:31]\n"
"ds_read_b64_tr_b16 v[226:227], v237 offset:20480\n"
"v_mfma_f32_16x16x32_bf16 v[28:31], v[96:99], a[252:255], v[28:31]\n"
"v_exp_f32 v172, v172\n"
"v_mfma_f32_16x16x32_bf16 a[236:239], v[120:123], a[252:255], a[236:239]\n"
"ds_read_b64_tr_b16 a[250:251], v236 offset:20480\n"
"v_cvt_pk_bf16_f32 v124, v164, v165\n"
"v_smfmac_f32_16x16x64_bf16 v[198:201], a[240:243], v[104:111], v241\n"
"s_waitcnt lgkmcnt(2)\n"
"ds_read_b64_tr_b16 a[252:253], v233 offset:16384\n"
"v_mfma_f32_16x16x32_bf16 v[156:159], v[208:211], a[0:3], v[180:183]\n"
"ds_read_b64_tr_b16 a[248:249], v236 offset:16384\n"
"v_cvt_pk_bf16_f32 v125, v166, v167\n"
"v_mfma_f32_16x16x32_bf16 v[156:159], v[212:215], a[4:7], v[156:159]\n"
"ds_read_b64_tr_b16 v[224:225], v237 offset:16384\n"
"v_cvt_pk_bf16_f32 v130, v168, v169\n"
"v_mfma_f32_16x16x32_bf16 v[156:159], v[216:219], a[8:11], v[156:159]\n"
"ds_read_b64_tr_b16 a[254:255], v233 offset:20480\n"
"v_cvt_pk_bf16_f32 v144, v160, v161\n"
"v_mfma_f32_16x16x32_bf16 v[156:159], v[220:223], a[12:15], v[156:159]\n"
"ds_read_b64_tr_b16 a[244:245], v232 offset:16384\n"
"v_mfma_f32_16x16x32_bf16 v[160:163], v[216:219], a[24:27], v[184:187]\n"
"v_exp_f32 v176, v176\n"
"v_mfma_f32_16x16x32_bf16 v[160:163], v[220:223], a[28:31], v[160:163]\n"
"v_exp_f32 v152, v152\n"
"v_mfma_f32_16x16x32_bf16 v[160:163], v[208:211], a[16:19], v[160:163]\n"
"s_add_u32 s40, s56, s52\n"
"v_cvt_pk_bf16_f32 v146, v176, v177\n"
"v_mfma_f32_16x16x32_bf16 v[160:163], v[212:215], a[20:23], v[160:163]\n"
"buffer_load_dwordx4 v207, s[16:19], s40 offen offset:1024 nt lds\n"
"v_mfma_f32_16x16x32_bf16 v[164:167], v[208:211], a[32:35], v[188:191]\n"
"v_exp_f32 v170, v170\n"
"v_mfma_f32_16x16x32_bf16 v[164:167], v[212:215], a[36:39], v[164:167]\n"
"v_exp_f32 v173, v173\n"
"v_mfma_f32_16x16x32_bf16 v[164:167], v[216:219], a[40:43], v[164:167]\n"
"v_exp_f32 v153, v153\n"
"v_mfma_f32_16x16x32_bf16 v[164:167], v[220:223], a[44:47], v[164:167]\n"
"s_waitcnt lgkmcnt(0)\n"
"v_exp_f32 v174, v174\n"
"v_mfma_f32_16x16x32_bf16 a[208:211], v[120:123], a[244:247], a[208:211]\n"
"ds_read_b128 v[208:211], v230 offset:36864\n"
"v_cvt_pk_bf16_f32 v139, v174, v175\n"
"v_mfma_f32_16x16x32_bf16 v[0:3], v[96:99], a[244:247], v[0:3]\n"
"v_exp_f32 v156, v156\n"
"v_mfma_f32_16x16x32_bf16 v[0:3], v[100:103], a[248:251], v[0:3]\n"
"ds_read_b128 v[220:223], v231 offset:37376\n"
"v_cvt_pk_bf16_f32 v138, v172, v173\n"
"v_mfma_f32_16x16x32_bf16 v[32:35], v[104:107], a[244:247], v[32:35]\n"
"v_exp_f32 v161, v161\n"
"v_mfma_f32_16x16x32_bf16 v[32:35], v[108:111], a[248:251], v[32:35]\n"
"ds_read_b128 v[216:219], v230 offset:37376\n"
"v_cvt_pk_bf16_f32 v126, v152, v153\n"
"v_mfma_f32_16x16x32_bf16 v[64:67], v[112:115], a[244:247], v[64:67]\n"
"s_add_u32 s40, s56, s53\n"
"ds_read_b128 v[212:215], v231 offset:36864\n"
"v_mfma_f32_16x16x32_bf16 v[64:67], v[116:119], a[248:251], v[64:67]\n"
"buffer_load_dwordx4 v240, s[16:19], s40 offen offset:2048 nt lds\n"
"v_mfma_f32_16x16x32_bf16 v[68:71], v[116:119], v[224:227], v[68:71]\n"
"v_exp_f32 v159, v159\n"
"v_mfma_f32_16x16x32_bf16 v[68:71], v[112:115], a[252:255], v[68:71]\n"
"v_exp_f32 v155, v155\n"
"v_mfma_f32_16x16x32_bf16 v[36:39], v[108:111], v[224:227], v[36:39]\n"
"v_exp_f32 v160, v160\n"
"v_mfma_f32_16x16x32_bf16 v[36:39], v[104:107], a[252:255], v[36:39]\n"
"v_exp_f32 v179, v179\n"
"v_mfma_f32_16x16x32_bf16 v[4:7], v[100:103], v[224:227], v[4:7]\n"
"ds_read_b64_tr_b16 a[244:245], v232 offset:16896\n"
"ds_read_b64_tr_b16 v[224:225], v237 offset:16896\n"
"v_mfma_f32_16x16x32_bf16 v[4:7], v[96:99], a[252:255], v[4:7]\n"
"v_exp_f32 v158, v158\n"
"v_mfma_f32_16x16x32_bf16 a[212:215], v[120:123], a[252:255], a[212:215]\n"
"ds_read_b64_tr_b16 a[246:247], v232 offset:20992\n"
"ds_read_b64_tr_b16 a[250:251], v236 offset:20992\n"
"v_smfmac_f32_16x16x64_bf16 v[200:203], a[240:243], v[112:119], v241\n"
"s_waitcnt lgkmcnt(4)\n"
"ds_read_b64_tr_b16 v[226:227], v237 offset:20992\n"
"v_cvt_pk_bf16_f32 v131, v170, v171\n"
"v_mfma_f32_16x16x32_bf16 v[168:171], v[208:211], a[0:3], v[180:183]\n"
"v_exp_f32 v164, v164\n"
"v_mfma_f32_16x16x32_bf16 v[168:171], v[212:215], a[4:7], v[168:171]\n"
"ds_read_b64_tr_b16 a[248:249], v236 offset:16896\n"
"ds_read_b64_tr_b16 a[254:255], v233 offset:20992\n"
"v_mfma_f32_16x16x32_bf16 v[168:171], v[216:219], a[8:11], v[168:171]\n"
"s_add_u32 s40, s56, s54\n"
"ds_read_b64_tr_b16 a[252:253], v233 offset:16896\n"
"v_mfma_f32_16x16x32_bf16 v[168:171], v[220:223], a[12:15], v[168:171]\n"
"buffer_load_dwordx4 v207, s[16:19], s40 offen offset:3072 nt lds\n"
"v_mfma_f32_16x16x32_bf16 v[172:175], v[216:219], a[24:27], v[184:187]\n"
"v_exp_f32 v154, v154\n"
"v_mfma_f32_16x16x32_bf16 v[172:175], v[220:223], a[28:31], v[172:175]\n"
"v_exp_f32 v167, v167\n"
"v_mfma_f32_16x16x32_bf16 v[172:175], v[208:211], a[16:19], v[172:175]\n"
"v_exp_f32 v163, v163\n"
"v_mfma_f32_16x16x32_bf16 v[172:175], v[212:215], a[20:23], v[172:175]\n"
"s_cbranch_vccnz LPCL2_%=\nLPKL2_%=:\n"
"v_cvt_pk_bf16_f32 v147, v178, v179\n"
"v_mfma_f32_16x16x32_bf16 v[176:179], v[208:211], a[32:35], v[188:191]\n"
"v_exp_f32 v165, v165\n"
"v_mfma_f32_16x16x32_bf16 v[176:179], v[212:215], a[36:39], v[176:179]\n"
"v_exp_f32 v157, v157\n"
"v_mfma_f32_16x16x32_bf16 v[176:179], v[216:219], a[40:43], v[176:179]\n"
"v_exp_f32 v166, v166\n"
"v_mfma_f32_16x16x32_bf16 v[176:179], v[220:223], a[44:47], v[176:179]\n"
"s_waitcnt lgkmcnt(0)\n"
"v_mfma_f32_16x16x32_bf16 a[224:227], v[120:123], a[244:247], a[224:227]\n"
"v_exp_f32 v162, v162\n"
"v_mfma_f32_16x16x32_bf16 v[16:19], v[96:99], a[244:247], v[16:19]\n"
"s_waitcnt vmcnt(8)\n"
"v_exp_f32 v168, v168\n"
"v_mfma_f32_16x16x32_bf16 v[16:19], v[100:103], a[248:251], v[16:19]\n"
"s_barrier\n"
"v_mfma_f32_16x16x32_bf16 v[48:51], v[104:107], a[244:247], v[48:51]\n"
"ds_read_b128 v[208:211], v228 offset:49152\n"
"v_cvt_pk_bf16_f32 v133, v158, v159\n"
"v_mfma_f32_16x16x32_bf16 v[48:51], v[108:111], a[248:251], v[48:51]\n"
"ds_read_b128 v[216:219], v228 offset:49664\n"
"v_cvt_pk_bf16_f32 v141, v162, v163\n"
"v_mfma_f32_16x16x32_bf16 v[80:83], v[112:115], a[244:247], v[80:83]\n"
"ds_read_b128 v[220:223], v229 offset:49664\n"
"v_cvt_pk_bf16_f32 v127, v154, v155\n"
"v_mfma_f32_16x16x32_bf16 v[80:83], v[116:119], a[248:251], v[80:83]\n"
"ds_read_b128 v[212:215], v229 offset:49152\n"
"v_cvt_pk_bf16_f32 v149, v166, v167\n"
"v_mfma_f32_16x16x32_bf16 v[84:87], v[116:119], v[224:227], v[84:87]\n"
"v_exp_f32 v176, v176\n"
"v_mfma_f32_16x16x32_bf16 v[84:87], v[112:115], a[252:255], v[84:87]\n"
"v_exp_f32 v169, v169\n"
"v_mfma_f32_16x16x32_bf16 v[52:55], v[108:111], v[224:227], v[52:55]\n"
"ds_read_b64_tr_b16 a[246:247], v234 offset:36864\n"
"v_cvt_pk_bf16_f32 v148, v164, v165\n"
"v_mfma_f32_16x16x32_bf16 v[52:55], v[104:107], a[252:255], v[52:55]\n"
"v_exp_f32 v177, v177\n"
"v_mfma_f32_16x16x32_bf16 v[20:23], v[100:103], v[224:227], v[20:23]\n"
"ds_read_b64_tr_b16 a[250:251], v238 offset:36864\n"
"v_cvt_pk_bf16_f32 v150, v176, v177\n"
"v_mfma_f32_16x16x32_bf16 v[20:23], v[96:99], a[252:255], v[20:23]\n"
"v_exp_f32 v174, v174\n"
"v_mfma_f32_16x16x32_bf16 a[228:231], v[120:123], a[252:255], a[228:231]\n"
"s_waitcnt lgkmcnt(3)\n"
"ds_read_b64_tr_b16 a[254:255], v235 offset:36864\n"
"v_cvt_pk_bf16_f32 v132, v156, v157\n"
"v_mfma_f32_16x16x32_bf16 v[152:155], v[208:211], a[0:3], v[180:183]\n"
"s_waitcnt lgkmcnt(3)\n"
"ds_read_b64_tr_b16 a[248:249], v238 offset:32768\n"
"v_cvt_pk_bf16_f32 v134, v168, v169\n"
"v_mfma_f32_16x16x32_bf16 v[152:155], v[212:215], a[4:7], v[152:155]\n"
"v_exp_f32 v172, v172\n"
"v_mfma_f32_16x16x32_bf16 v[152:155], v[216:219], a[8:11], v[152:155]\n"
"ds_read_b64_tr_b16 a[244:245], v234 offset:32768\n"
"v_cvt_pk_bf16_f32 v140, v160, v161\n"
"v_mfma_f32_16x16x32_bf16 v[152:155], v[220:223], a[12:15], v[152:155]\n"
"v_exp_f32 v175, v175\n"
"v_mfma_f32_16x16x32_bf16 v[156:159], v[216:219], a[24:27], v[184:187]\n"
"ds_read_b64_tr_b16 a[252:253], v235 offset:32768\n"
"v_cvt_pk_bf16_f32 v143, v174, v175\n"
"v_mfma_f32_16x16x32_bf16 v[156:159], v[220:223], a[28:31], v[156:159]\n"
"ds_read_b64_tr_b16 v[224:225], v239 offset:32768\n"
"v_mfma_f32_16x16x32_bf16 v[156:159], v[208:211], a[16:19], v[156:159]\n"
"s_add_u32 m0, s44, 65536\n"
"ds_read_b64_tr_b16 v[226:227], v239 offset:36864\n"
"v_mfma_f32_16x16x32_bf16 v[156:159], v[212:215], a[20:23], v[156:159]\n"
"buffer_load_dwordx4 v240, s[20:23], s56 offen nt lds\n"
"v_mfma_f32_16x16x32_bf16 v[160:163], v[208:211], a[32:35], v[188:191]\n"
"v_exp_f32 v178, v178\n"
"v_mfma_f32_16x16x32_bf16 v[160:163], v[212:215], a[36:39], v[160:163]\n"
"v_exp_f32 v171, v171\n"
"v_mfma_f32_16x16x32_bf16 v[160:163], v[216:219], a[40:43], v[160:163]\n"
"s_add_u32 s40, s56, s52\n"
"v_mfma_f32_16x16x32_bf16 v[160:163], v[220:223], a[44:47], v[160:163]\n"
"buffer_load_dwordx4 v207, s[20:23], s40 offen offset:1024 nt lds\n"
"v_mfma_f32_16x16x32_bf16 v[164:167], v[216:219], a[200:203], v[192:195]\n"
"v_exp_f32 v170, v170\n"
"v_mfma_f32_16x16x32_bf16 v[164:167], v[220:223], a[204:207], v[164:167]\n"
"v_exp_f32 v153, v153\n"
"v_mfma_f32_16x16x32_bf16 v[164:167], v[208:211], a[192:195], v[164:167]\n"
"v_exp_f32 v179, v179\n"
"v_mfma_f32_16x16x32_bf16 v[164:167], v[212:215], a[196:199], v[164:167]\n"
"s_waitcnt lgkmcnt(0)\n"
"v_exp_f32 v158, v158\n"
"v_mfma_f32_16x16x32_bf16 a[216:219], v[124:127], a[244:247], a[216:219]\n"
"ds_read_b128 v[216:219], v228 offset:53760\n"
"v_cvt_pk_bf16_f32 v135, v170, v171\n"
"v_mfma_f32_16x16x32_bf16 v[8:11], v[128:131], a[244:247], v[8:11]\n"
"v_exp_f32 v173, v173\n"
"v_mfma_f32_16x16x32_bf16 v[8:11], v[132:135], a[248:251], v[8:11]\n"
"ds_read_b128 v[208:211], v228 offset:53248\n"
"v_cvt_pk_bf16_f32 v142, v172, v173\n"
"v_mfma_f32_16x16x32_bf16 v[40:43], v[136:139], a[244:247], v[40:43]\n"
"v_exp_f32 v162, v162\n"
"v_mfma_f32_16x16x32_bf16 v[40:43], v[140:143], a[248:251], v[40:43]\n"
"ds_read_b128 v[220:223], v229 offset:53760\n"
"v_cvt_pk_bf16_f32 v151, v178, v179\n"
"v_mfma_f32_16x16x32_bf16 v[72:75], v[144:147], a[244:247], v[72:75]\n"
"s_add_u32 s40, s56, s53\n"
"ds_read_b128 v[212:215], v229 offset:53248\n"
"v_mfma_f32_16x16x32_bf16 v[72:75], v[148:151], a[248:251], v[72:75]\n"
"buffer_load_dwordx4 v240, s[20:23], s40 offen offset:2048 nt lds\n"
"v_mfma_f32_16x16x32_bf16 v[76:79], v[148:151], v[224:227], v[76:79]\n"
"v_exp_f32 v163, v163\n"
"v_mfma_f32_16x16x32_bf16 v[76:79], v[144:147], a[252:255], v[76:79]\n"
"v_exp_f32 v156, v156\n"
"v_mfma_f32_16x16x32_bf16 v[44:47], v[140:143], v[224:227], v[44:47]\n"
"v_exp_f32 v160, v160\n"
"v_mfma_f32_16x16x32_bf16 v[44:47], v[136:139], a[252:255], v[44:47]\n"
"v_exp_f32 v155, v155\n"
"v_mfma_f32_16x16x32_bf16 v[12:15], v[132:135], v[224:227], v[12:15]\n"
"ds_read_b64_tr_b16 v[226:227], v239 offset:37376\n"
"v_mfma_f32_16x16x32_bf16 v[12:15], v[128:131], a[252:255], v[12:15]\n"
"ds_read_b64_tr_b16 a[248:249], v238 offset:33280\n"
"v_mfma_f32_16x16x32_bf16 a[220:223], v[124:127], a[252:255], a[220:223]\n"
"v_exp_f32 v154, v154\n"
"v_smfmac_f32_16x16x64_bf16 v[196:199], a[240:243], v[128:135], v241\n"
"ds_read_b64_tr_b16 a[250:251], v238 offset:37376\n"
"v_mfma_f32_16x16x32_bf16 v[202:205], a[240:243], v[124:127], v[202:205]\n"
"s_waitcnt lgkmcnt(3)\n"
"v_exp_f32 v152, v152\n"
"v_mfma_f32_16x16x32_bf16 v[168:171], v[216:219], a[8:11], v[180:183]\n"
"ds_read_b64_tr_b16 a[244:245], v234 offset:33280\n"
"v_cvt_pk_bf16_f32 v96, v152, v153\n"
"v_mfma_f32_16x16x32_bf16 v[168:171], v[220:223], a[12:15], v[168:171]\n"
"v_exp_f32 v165, v165\n"
"v_mfma_f32_16x16x32_bf16 v[168:171], v[208:211], a[0:3], v[168:171]\n"
"ds_read_b64_tr_b16 a[246:247], v234 offset:37376\n"
"v_cvt_pk_bf16_f32 v113, v162, v163\n"
"v_mfma_f32_16x16x32_bf16 v[168:171], v[212:215], a[4:7], v[168:171]\n"
"ds_read_b64_tr_b16 a[254:255], v235 offset:37376\n"
"v_mfma_f32_16x16x32_bf16 v[172:175], v[208:211], a[16:19], v[184:187]\n"
"ds_read_b64_tr_b16 v[224:225], v239 offset:33280\n"
"v_cvt_pk_bf16_f32 v97, v154, v155\n"
"v_mfma_f32_16x16x32_bf16 v[172:175], v[212:215], a[20:23], v[172:175]\n"
"v_mfma_f32_16x16x32_bf16 v[172:175], v[216:219], a[24:27], v[172:175]\n"
"s_add_u32 s40, s56, s54\n"
"ds_read_b64_tr_b16 a[252:253], v235 offset:33280\n"
"v_mfma_f32_16x16x32_bf16 v[172:175], v[220:223], a[28:31], v[172:175]\n"
"buffer_load_dwordx4 v207, s[20:23], s40 offen offset:3072 nt lds\n"
"v_mfma_f32_16x16x32_bf16 v[176:179], v[216:219], a[40:43], v[188:191]\n"
"s_add_u32 s55, s55, s59\n"
"v_exp_f32 v157, v157\n"
"v_mfma_f32_16x16x32_bf16 v[176:179], v[220:223], a[44:47], v[176:179]\n"
"s_or_b32 s55, s55, s58\n"
"v_exp_f32 v171, v171\n"
"v_mfma_f32_16x16x32_bf16 v[176:179], v[208:211], a[32:35], v[176:179]\n"
"s_xor_b32 s56, s55, s57\n"
"v_exp_f32 v159, v159\n"
"v_mfma_f32_16x16x32_bf16 v[176:179], v[212:215], a[36:39], v[176:179]\n"
"v_exp_f32 v167, v167\n"
"v_mfma_f32_16x16x32_bf16 v[152:155], v[208:211], a[192:195], v[192:195]\n"
"v_cvt_pk_bf16_f32 v105, v158, v159\n"
"v_mfma_f32_16x16x32_bf16 v[152:155], v[212:215], a[196:199], v[152:155]\n"
"ds_read_b128 v[208:211], v230 offset:49152\n"
"v_cvt_pk_bf16_f32 v104, v156, v157\n"
"v_mfma_f32_16x16x32_bf16 v[152:155], v[216:219], a[200:203], v[152:155]\n"
"v_exp_f32 v166, v166\n"
"v_mfma_f32_16x16x32_bf16 v[152:155], v[220:223], a[204:207], v[152:155]\n"
"s_waitcnt lgkmcnt(1)\n"
"ds_read_b128 v[216:219], v230 offset:49664\n"
"v_mfma_f32_16x16x32_bf16 a[232:235], v[124:127], a[244:247], a[232:235]\n"
"v_exp_f32 v169, v169\n"
"v_mfma_f32_16x16x32_bf16 v[24:27], v[128:131], a[244:247], v[24:27]\n"
"ds_read_b128 v[220:223], v231 offset:49664\n"
"v_mfma_f32_16x16x32_bf16 v[24:27], v[132:135], a[248:251], v[24:27]\n"
"v_exp_f32 v164, v164\n"
"v_mfma_f32_16x16x32_bf16 v[56:59], v[136:139], a[244:247], v[56:59]\n"
"ds_read_b128 v[212:215], v231 offset:49152\n"
"v_mfma_f32_16x16x32_bf16 v[56:59], v[140:143], a[248:251], v[56:59]\n"
"v_exp_f32 v178, v178\n"
"v_mfma_f32_16x16x32_bf16 v[88:91], v[144:147], a[244:247], v[88:91]\n"
"s_add_u32 m0, s44, 16384\n"
"ds_read_b64_tr_b16 a[246:247], v232 offset:36864\n"
"v_mfma_f32_16x16x32_bf16 v[88:91], v[148:151], a[248:251], v[88:91]\n"
"buffer_load_dwordx4 v240, s[16:19], s56 offen nt lds\n"
"v_mfma_f32_16x16x32_bf16 v[92:95], v[148:151], v[224:227], v[92:95]\n"
"v_exp_f32 v175, v175\n"
"v_mfma_f32_16x16x32_bf16 v[92:95], v[144:147], a[252:255], v[92:95]\n"
"v_exp_f32 v168, v168\n"
"v_mfma_f32_16x16x32_bf16 v[60:63], v[140:143], v[224:227], v[60:63]\n"
"v_exp_f32 v161, v161\n"
"v_mfma_f32_16x16x32_bf16 v[60:63], v[136:139], a[252:255], v[60:63]\n"
"v_exp_f32 v177, v177\n"
"v_mfma_f32_16x16x32_bf16 v[28:31], v[132:135], v[224:227], v[28:31]\n"
"ds_read_b64_tr_b16 v[226:227], v237 offset:36864\n"
"v_mfma_f32_16x16x32_bf16 v[28:31], v[128:131], a[252:255], v[28:31]\n"
"v_exp_f32 v172, v172\n"
"v_mfma_f32_16x16x32_bf16 a[236:239], v[124:127], a[252:255], a[236:239]\n"
"ds_read_b64_tr_b16 a[250:251], v236 offset:36864\n"
"v_cvt_pk_bf16_f32 v120, v164, v165\n"
"v_smfmac_f32_16x16x64_bf16 v[198:201], a[240:243], v[136:143], v241\n"
"s_waitcnt lgkmcnt(2)\n"
"ds_read_b64_tr_b16 a[252:253], v233 offset:32768\n"
"v_mfma_f32_16x16x32_bf16 v[156:159], v[208:211], a[0:3], v[180:183]\n"
"ds_read_b64_tr_b16 a[248:249], v236 offset:32768\n"
"v_cvt_pk_bf16_f32 v121, v166, v167\n"
"v_mfma_f32_16x16x32_bf16 v[156:159], v[212:215], a[4:7], v[156:159]\n"
"ds_read_b64_tr_b16 v[224:225], v237 offset:32768\n"
"v_cvt_pk_bf16_f32 v98, v168, v169\n"
"v_mfma_f32_16x16x32_bf16 v[156:159], v[216:219], a[8:11], v[156:159]\n"
"ds_read_b64_tr_b16 a[254:255], v233 offset:36864\n"
"v_cvt_pk_bf16_f32 v112, v160, v161\n"
"v_mfma_f32_16x16x32_bf16 v[156:159], v[220:223], a[12:15], v[156:159]\n"
"ds_read_b64_tr_b16 a[244:245], v232 offset:32768\n"
"v_mfma_f32_16x16x32_bf16 v[160:163], v[216:219], a[24:27], v[184:187]\n"
"v_exp_f32 v176, v176\n"
"v_mfma_f32_16x16x32_bf16 v[160:163], v[220:223], a[28:31], v[160:163]\n"
"v_exp_f32 v152, v152\n"
"v_mfma_f32_16x16x32_bf16 v[160:163], v[208:211], a[16:19], v[160:163]\n"
"s_add_u32 s40, s56, s52\n"
"v_cvt_pk_bf16_f32 v114, v176, v177\n"
"v_mfma_f32_16x16x32_bf16 v[160:163], v[212:215], a[20:23], v[160:163]\n"
"buffer_load_dwordx4 v207, s[16:19], s40 offen offset:1024 nt lds\n"
"v_mfma_f32_16x16x32_bf16 v[164:167], v[208:211], a[32:35], v[188:191]\n"
"v_exp_f32 v170, v170\n"
"v_mfma_f32_16x16x32_bf16 v[164:167], v[212:215], a[36:39], v[164:167]\n"
"v_exp_f32 v173, v173\n"
"v_mfma_f32_16x16x32_bf16 v[164:167], v[216:219], a[40:43], v[164:167]\n"
"v_exp_f32 v153, v153\n"
"v_mfma_f32_16x16x32_bf16 v[164:167], v[220:223], a[44:47], v[164:167]\n"
"s_waitcnt lgkmcnt(0)\n"
"v_exp_f32 v174, v174\n"
"v_mfma_f32_16x16x32_bf16 a[208:211], v[124:127], a[244:247], a[208:211]\n"
"ds_read_b128 v[208:211], v230 offset:53248\n"
"v_cvt_pk_bf16_f32 v107, v174, v175\n"
"v_mfma_f32_16x16x32_bf16 v[0:3], v[128:131], a[244:247], v[0:3]\n"
"v_exp_f32 v156, v156\n"
"v_mfma_f32_16x16x32_bf16 v[0:3], v[132:135], a[248:251], v[0:3]\n"
"ds_read_b128 v[220:223], v231 offset:53760\n"
"v_cvt_pk_bf16_f32 v106, v172, v173\n"
"v_mfma_f32_16x16x32_bf16 v[32:35], v[136:139], a[244:247], v[32:35]\n"
"v_exp_f32 v161, v161\n"
"v_mfma_f32_16x16x32_bf16 v[32:35], v[140:143], a[248:251], v[32:35]\n"
"ds_read_b128 v[216:219], v230 offset:53760\n"
"v_cvt_pk_bf16_f32 v122, v152, v153\n"
"v_mfma_f32_16x16x32_bf16 v[64:67], v[144:147], a[244:247], v[64:67]\n"
"s_add_u32 s40, s56, s53\n"
"ds_read_b128 v[212:215], v231 offset:53248\n"
"v_mfma_f32_16x16x32_bf16 v[64:67], v[148:151], a[248:251], v[64:67]\n"
"buffer_load_dwordx4 v240, s[16:19], s40 offen offset:2048 nt lds\n"
"v_mfma_f32_16x16x32_bf16 v[68:71], v[148:151], v[224:227], v[68:71]\n"
"v_exp_f32 v159, v159\n"
"v_mfma_f32_16x16x32_bf16 v[68:71], v[144:147], a[252:255], v[68:71]\n"
"v_exp_f32 v155, v155\n"
"v_mfma_f32_16x16x32_bf16 v[36:39], v[140:143], v[224:227], v[36:39]\n"
"v_exp_f32 v160, v160\n"
"v_mfma_f32_16x16x32_bf16 v[36:39], v[136:139], a[252:255], v[36:39]\n"
"v_exp_f32 v179, v179\n"
"v_mfma_f32_16x16x32_bf16 v[4:7], v[132:135], v[224:227], v[4:7]\n"
"ds_read_b64_tr_b16 a[244:245], v232 offset:33280\n"
"ds_read_b64_tr_b16 v[224:225], v237 offset:33280\n"
"v_mfma_f32_16x16x32_bf16 v[4:7], v[128:131], a[252:255], v[4:7]\n"
"v_exp_f32 v158, v158\n"
"v_mfma_f32_16x16x32_bf16 a[212:215], v[124:127], a[252:255], a[212:215]\n"
"ds_read_b64_tr_b16 a[246:247], v232 offset:37376\n"
"ds_read_b64_tr_b16 a[250:251], v236 offset:37376\n"
"v_smfmac_f32_16x16x64_bf16 v[200:203], a[240:243], v[144:151], v241\n"
"s_waitcnt lgkmcnt(4)\n"
"ds_read_b64_tr_b16 v[226:227], v237 offset:37376\n"
"v_cvt_pk_bf16_f32 v99, v170, v171\n"
"v_mfma_f32_16x16x32_bf16 v[168:171], v[208:211], a[0:3], v[180:183]\n"
"v_exp_f32 v164, v164\n"
"v_mfma_f32_16x16x32_bf16 v[168:171], v[212:215], a[4:7], v[168:171]\n"
"ds_read_b64_tr_b16 a[248:249], v236 offset:33280\n"
"ds_read_b64_tr_b16 a[254:255], v233 offset:37376\n"
"v_mfma_f32_16x16x32_bf16 v[168:171], v[216:219], a[8:11], v[168:171]\n"
"s_add_u32 s40, s56, s54\n"
"ds_read_b64_tr_b16 a[252:253], v233 offset:33280\n"
"v_mfma_f32_16x16x32_bf16 v[168:171], v[220:223], a[12:15], v[168:171]\n"
"buffer_load_dwordx4 v207, s[16:19], s40 offen offset:3072 nt lds\n"
"v_mfma_f32_16x16x32_bf16 v[172:175], v[216:219], a[24:27], v[184:187]\n"
"v_exp_f32 v154, v154\n"
"v_mfma_f32_16x16x32_bf16 v[172:175], v[220:223], a[28:31], v[172:175]\n"
"v_exp_f32 v167, v167\n"
"v_mfma_f32_16x16x32_bf16 v[172:175], v[208:211], a[16:19], v[172:175]\n"
"v_exp_f32 v163, v163\n"
"v_mfma_f32_16x16x32_bf16 v[172:175], v[212:215], a[20:23], v[172:175]\n"
"s_cbranch_vccnz LPCL3_%=\nLPKL3_%=:\n"
"v_cvt_pk_bf16_f32 v115, v178, v179\n"
"v_mfma_f32_16x16x32_bf16 v[176:179], v[208:211], a[32:35], v[188:191]\n"
"s_add_u32 s47, s47, 1\n"
"v_exp_f32 v165, v165\n"
"v_mfma_f32_16x16x32_bf16 v[176:179], v[212:215], a[36:39], v[176:179]\n"
"v_exp_f32 v157, v157\n"
"v_mfma_f32_16x16x32_bf16 v[176:179], v[216:219], a[40:43], v[176:179]\n"
"s_and_b32 s40, s47, 3\n"
"v_exp_f32 v166, v166\n"
"v_mfma_f32_16x16x32_bf16 v[176:179], v[220:223], a[44:47], v[176:179]\n"
"s_waitcnt lgkmcnt(0)\n"
"s_cbranch_scc1 LAL_%=\n"
"s_cmp_eq_u32 s47, 8\n"
"s_cbranch_scc1 LTH1_%=\n"
"s_cmp_eq_u32 s47, 12\n"
"s_cbranch_scc1 LTH2_%=\n"
"s_cmp_eq_u32 s47, 16\n"
"s_cbranch_scc1 LAX_%=\n"
"LTH0_%=:\n"
"v_mfma_f32_16x16x32_bf16 a[224:227], v[124:127], a[244:247], a[224:227]\n"
"v_exp_f32 v171, v171\n"
"v_mfma_f32_16x16x32_bf16 v[16:19], v[128:131], a[244:247], v[16:19]\n"
"s_waitcnt vmcnt(8)\n"
"v_exp_f32 v172, v172\n"
"v_mfma_f32_16x16x32_bf16 v[16:19], v[132:135], a[248:251], v[16:19]\n"
"s_barrier\n"
"v_mfma_f32_16x16x32_bf16 v[48:51], v[136:139], a[244:247], v[48:51]\n"
"v_exp_f32 v169, v169\n"
"v_mfma_f32_16x16x32_bf16 v[48:51], v[140:143], a[248:251], v[48:51]\n"
"ds_read_b128 v[220:223], v229 offset:512\n"
"v_cvt_pk_bf16_f32 v117, v166, v167\n"
"v_exp_f32 v176, v176\n"
"v_exp_f32 v177, v177\n"
"v_exp_f32 v178, v178\n"
"v_mfma_f32_16x16x32_bf16 v[80:83], v[144:147], a[244:247], v[80:83]\n"
"ds_read_b64_tr_b16 a[244:245], v234 offset:49152\n"
"v_cvt_pk_bf16_f32 v108, v160, v161\n"
"v_mfma_f32_16x16x32_bf16 v[80:83], v[148:151], a[248:251], v[80:83]\n"
"ds_read_b64_tr_b16 a[246:247], v234 offset:53248\n"
"ds_read_b64_tr_b16 a[248:249], v238 offset:49152\n"
"v_exp_f32 v162, v162\n"
"v_exp_f32 v168, v168\n"
"v_exp_f32 v173, v173\n"
"v_exp_f32 v175, v175\n"
"v_mfma_f32_16x16x32_bf16 v[84:87], v[148:151], v[224:227], v[84:87]\n"
"ds_read_b64_tr_b16 a[250:251], v238 offset:53248\n"
"v_cvt_pk_bf16_f32 v116, v164, v165\n"
"v_mfma_f32_16x16x32_bf16 v[84:87], v[144:147], a[252:255], v[84:87]\n"
"v_exp_f32 v170, v170\n"
"v_mfma_f32_16x16x32_bf16 v[52:55], v[140:143], v[224:227], v[52:55]\n"
"v_cvt_pk_bf16_f32 v103, v170, v171\n"
"v_cvt_pk_bf16_f32 v110, v172, v173\n"
"v_mfma_f32_16x16x32_bf16 v[52:55], v[136:139], a[252:255], v[52:55]\n"
"v_cvt_pk_bf16_f32 v123, v154, v155\n"
"v_cvt_pk_bf16_f32 v101, v158, v159\n"
"v_mfma_f32_16x16x32_bf16 v[20:23], v[132:135], v[224:227], v[20:23]\n"
"v_cvt_pk_bf16_f32 v100, v156, v157\n"
"v_cvt_pk_bf16_f32 v102, v168, v169\n"
"v_mfma_f32_16x16x32_bf16 v[20:23], v[128:131], a[252:255], v[20:23]\n"
"v_exp_f32 v179, v179\n"
"v_mfma_f32_16x16x32_bf16 a[228:231], v[124:127], a[252:255], a[228:231]\n"
"s_waitcnt lgkmcnt(2)\n"
"ds_read_b64_tr_b16 a[252:253], v235 offset:49152\n"
"ds_read_b64_tr_b16 v[226:227], v239 offset:53248\n"
"v_mfma_f32_16x16x32_bf16 a[216:219], v[120:123], a[244:247], a[216:219]\n"
"s_add_u32 m0, s44, 81920\n"
"ds_read_b64_tr_b16 v[224:225], v239 offset:49152\n"
"v_cvt_pk_bf16_f32 v119, v178, v179\n"
"v_smfmac_f32_16x16x64_bf16 v[196:199], a[240:243], v[96:103], v241\n"
"buffer_load_dwordx4 v240, s[20:23], s56 offen nt lds\n"
"v_mfma_f32_16x16x32_bf16 v[8:11], v[96:99], a[244:247], v[8:11]\n"
"s_waitcnt lgkmcnt(3)\n"
"ds_read_b64_tr_b16 a[254:255], v235 offset:53248\n"
"v_cvt_pk_bf16_f32 v118, v176, v177\n"
"v_mfma_f32_16x16x32_bf16 v[8:11], v[100:103], a[248:251], v[8:11]\n"
"v_exp_f32 v174, v174\n"
"v_smfmac_f32_16x16x64_bf16 v[200:203], a[240:243], v[112:119], v241\n"
"v_cvt_pk_bf16_f32 v109, v162, v163\n"
"v_cvt_pk_bf16_f32 v111, v174, v175\n"
"v_mfma_f32_16x16x32_bf16 v[40:43], v[104:107], a[244:247], v[40:43]\n"
"ds_read_b128 v[212:215], v229 offset:0\n"
"v_and_b32 v245, 0x3f0, v242\n"
"v_mfma_f32_16x16x32_bf16 v[40:43], v[108:111], a[248:251], v[40:43]\n"
"v_lshlrev_b32_e64 v244, 24, s43\n"
"v_mul_f32 v244, 0x45800000, v244\n"
"v_smfmac_f32_16x16x64_bf16 v[198:201], a[240:243], v[104:111], v241\n"
"ds_read_b128 v[208:211], v228 offset:0\n"
"v_add_f32 v196, v196, v197\n"
"v_mfma_f32_16x16x32_bf16 v[72:75], v[112:115], a[244:247], v[72:75]\n"
"ds_read_b64_tr_b16 a[244:245], v234 offset:49664\n"
"v_lshrrev_b32 v245, 1, v245\n"
"v_mfma_f32_16x16x32_bf16 v[72:75], v[116:119], a[248:251], v[72:75]\n"
"ds_read_b64_tr_b16 a[246:247], v234 offset:53760\n"
"v_sub_u32 v245, v242, v245\n"
"v_mfma_f32_16x16x32_bf16 v[202:205], a[240:243], v[120:123], v[202:205]\n"
"s_waitcnt lgkmcnt(4)\n"
"ds_read_b64_tr_b16 a[248:249], v238 offset:49664\n"
"v_add_f32 v200, v200, v201\n"
"v_mfma_f32_16x16x32_bf16 v[76:79], v[116:119], v[224:227], v[76:79]\n"
"v_add_f32 v198, v198, v199\n"
"v_add_f32 v254, v196, v198\n"
"v_mfma_f32_16x16x32_bf16 v[76:79], v[112:115], a[252:255], v[76:79]\n"
"ds_read_b64_tr_b16 a[250:251], v238 offset:53760\n"
"v_xor_b32 v250, 0x1000, v245\n"
"v_mfma_f32_16x16x32_bf16 v[44:47], v[108:111], v[224:227], v[44:47]\n"
"ds_read_b128 v[216:219], v228 offset:512\n"
"v_add_f32 v254, v254, v200\n"
"v_mfma_f32_16x16x32_bf16 v[44:47], v[104:107], a[252:255], v[44:47]\n"
"v_add_f32 v244, v244, v202\n"
"v_add_f32 v254, v254, v202\n"
"v_mfma_f32_16x16x32_bf16 v[12:15], v[100:103], v[224:227], v[12:15]\n"
"s_add_u32 s40, s56, s52\n"
"ds_read_b64_tr_b16 v[226:227], v239 offset:53760\n"
"v_min3_f32 v244, v244, v196, v198\n"
"v_mfma_f32_16x16x32_bf16 v[12:15], v[96:99], a[252:255], v[12:15]\n"
"buffer_load_dwordx4 v207, s[20:23], s40 offen offset:1024 nt lds\n"
"v_min_f32 v244, v244, v200\n"
"v_rcp_f32 v244, v244\n"
"v_mfma_f32_16x16x32_bf16 a[220:223], v[120:123], a[252:255], a[220:223]\n"
"s_waitcnt lgkmcnt(4)\n"
"v_fmac_f32 v254, 0x68800000, v244\n"
"v_cmp_nge_f32 vcc, s45, v254\n"
"s_cbranch_vccnz LSLOW_%=\nLBACK0_%=:\ns_mov_b64 vcc, s[64:65]\n"
"v_mfma_f32_16x16x32_bf16 a[232:235], v[120:123], a[244:247], a[232:235]\n"
"v_rcp_f32 v246, v196\n"
"v_accvgpr_mov_b32 a2, a50\n"
"v_accvgpr_mov_b32 a7, a55\n"
"v_accvgpr_mov_b32 a10, a58\n"
"v_accvgpr_mov_b32 a15, a63\n"
"v_accvgpr_mov_b32 a22, a70\n"
"v_accvgpr_mov_b32 a30, a78\n"
"v_bfe_u32 v149, v242, 8, 2\n"
"v_lshl_add_u32 v149, v149, 4, s43\n"
"v_mfma_f32_16x16x32_bf16 v[24:27], v[96:99], a[244:247], v[24:27]\n"
"s_waitcnt lgkmcnt(2)\n"
"ds_read_b128 v[124:127], v149 offset:0\n"
"v_accvgpr_mov_b32 a28, a76\n"
"v_mfma_f32_16x16x32_bf16 v[24:27], v[100:103], a[248:251], v[24:27]\n"
"ds_read_b128 v[132:135], v149 offset:512\n"
"v_mov_b32_dpp v246, v246 row_ror:12 row_mask:0x2 bank_mask:0xf\n"
"v_accvgpr_mov_b32 a3, a51\n"
"v_accvgpr_mov_b32 a12, a60\n"
"v_accvgpr_mov_b32 a18, a66\n"
"v_accvgpr_mov_b32 a19, a67\n"
"v_accvgpr_mov_b32 a26, a74\n"
"v_accvgpr_mov_b32 a36, a84\n"
"v_xor_b32 v148, 0x400, v243\n"
"v_xor_b32 v150, 64, v149\n"
"v_mfma_f32_16x16x32_bf16 v[56:59], v[104:107], a[244:247], v[56:59]\n"
"ds_read_b128 v[128:131], v150 offset:0\n"
"v_accvgpr_mov_b32 a39, a87\n"
"v_mfma_f32_16x16x32_bf16 v[56:59], v[108:111], a[248:251], v[56:59]\n"
"ds_read_b128 v[136:139], v150 offset:512\n"
"v_rcp_f32 v247, v198\n"
"v_mov_b32_dpp v246, v246 row_ror:8 row_mask:0x4 bank_mask:0xf\n"
"v_accvgpr_mov_b32 a0, a48\n"
"v_accvgpr_mov_b32 a1, a49\n"
"v_accvgpr_mov_b32 a4, a52\n"
"v_accvgpr_mov_b32 a5, a53\n"
"v_accvgpr_mov_b32 a8, a56\n"
"v_accvgpr_mov_b32 a16, a64\n"
"v_accvgpr_mov_b32 a25, a73\n"
"v_mfma_f32_16x16x32_bf16 v[88:91], v[112:115], a[244:247], v[88:91]\n"
"v_accvgpr_mov_b32 a20, a68\n"
"v_accvgpr_mov_b32 a29, a77\n"
"v_mfma_f32_16x16x32_bf16 v[88:91], v[116:119], a[248:251], v[88:91]\n"
"ds_read_b128 a[192:195], v243 offset:4096\n"
"ds_read_b128 a[200:203], v243 offset:6144\n"
"v_accvgpr_mov_b32 a6, a54\n"
"v_accvgpr_mov_b32 a9, a57\n"
"v_accvgpr_mov_b32 a11, a59\n"
"v_accvgpr_mov_b32 a13, a61\n"
"v_accvgpr_mov_b32 a14, a62\n"
"v_accvgpr_mov_b32 a17, a65\n"
"v_accvgpr_mov_b32 a23, a71\n"
"v_accvgpr_mov_b32 a31, a79\n"
"v_accvgpr_mov_b32 a33, a81\n"
"v_accvgpr_mov_b32 a35, a83\n"
"v_accvgpr_mov_b32 a44, a92\n"
"v_accvgpr_mov_b32 a47, a95\n"
"s_waitcnt lgkmcnt(5)\n"
"v_mfma_f32_16x16x32_bf16 v[180:183], v[124:127], a[0:3], 0\n"
"v_xor_b32 v244, 0x11000, v242\n"
"s_waitcnt lgkmcnt(3)\n"
"v_mfma_f32_16x16x32_bf16 v[180:183], v[128:131], a[4:7], v[180:183]\n"
"v_accvgpr_mov_b32 a34, a82\n"
"v_accvgpr_mov_b32 a43, a91\n"
"v_mfma_f32_16x16x32_bf16 v[180:183], v[132:135], a[8:11], v[180:183]\n"
"s_waitcnt lgkmcnt(2)\n"
"ds_read_b128 a[204:207], v148 offset:6144\n"
"v_accvgpr_mov_b32 a37, a85\n"
"v_mfma_f32_16x16x32_bf16 v[180:183], v[136:139], a[12:15], v[180:183]\n"
"ds_read_b128 a[196:199], v148 offset:4096\n"
"v_accvgpr_mov_b32 a21, a69\n"
"v_accvgpr_mov_b32 a24, a72\n"
"v_accvgpr_mov_b32 a32, a80\n"
"v_accvgpr_mov_b32 a38, a86\n"
"v_accvgpr_mov_b32 a40, a88\n"
"v_accvgpr_mov_b32 a42, a90\n"
"v_accvgpr_mov_b32 a46, a94\n"
"v_mfma_f32_16x16x32_bf16 v[184:187], v[124:127], a[16:19], 0\n"
"v_mov_b32_dpp v246, v246 row_ror:4 row_mask:0x8 bank_mask:0xf\n"
"v_accvgpr_mov_b32 a27, a75\n"
"v_mfma_f32_16x16x32_bf16 v[184:187], v[128:131], a[20:23], v[184:187]\n"
"v_mov_b32_dpp v247, v247 row_ror:12 row_mask:0x2 bank_mask:0xf\n"
"v_mfma_f32_16x16x32_bf16 v[184:187], v[132:135], a[24:27], v[184:187]\n"
"s_add_u32 s40, s56, s53\n"
"ds_read_b64_tr_b16 v[224:225], v239 offset:49664\n"
"v_mfma_f32_16x16x32_bf16 v[184:187], v[136:139], a[28:31], v[184:187]\n"
"buffer_load_dwordx4 v240, s[20:23], s40 offen offset:2048 nt lds\n"
"v_accvgpr_mov_b32 a41, a89\n"
"v_sub_f32 v180, s74, v180\n"
"v_sub_f32 v181, s74, v181\n"
"v_sub_f32 v183, s74, v183\n"
"v_mul_f32_dpp v26, v246, v26 row_newbcast:2 row_mask:0xf bank_mask:0xf\n"
"v_mfma_f32_16x16x32_bf16 v[188:191], v[124:127], a[32:35], 0\n"
"v_mov_b32_dpp v247, v247 row_ror:8 row_mask:0x4 bank_mask:0xf\n"
"v_accvgpr_mov_b32 a45, a93\n"
"v_mfma_f32_16x16x32_bf16 v[188:191], v[128:131], a[36:39], v[188:191]\n"
"v_mov_b32 v252, v192\n"
"v_mfma_f32_16x16x32_bf16 v[188:191], v[132:135], a[40:43], v[188:191]\n"
"v_mfma_f32_16x16x32_bf16 v[188:191], v[136:139], a[44:47], v[188:191]\n"
"v_mul_f32_dpp v11, v246, v11 row_newbcast:3 row_mask:0xf bank_mask:0xf\n"
"s_waitcnt lgkmcnt(1)\n"
"v_mfma_f32_16x16x32_bf16 v[192:195], v[124:127], a[192:195], 0\n"
"ds_read_b64_tr_b16 a[252:253], v235 offset:49664\n"
"v_sub_f32 v185, s74, v185\n"
"v_mfma_f32_16x16x32_bf16 v[192:195], v[128:131], a[196:199], v[192:195]\n"
"ds_read_b64_tr_b16 a[248:249], v236 offset:49152\n"
"v_sub_f32 v182, s74, v182\n"
"v_mfma_f32_16x16x32_bf16 v[192:195], v[132:135], a[200:203], v[192:195]\n"
"v_sub_f32 v184, s74, v184\n"
"v_mfma_f32_16x16x32_bf16 v[192:195], v[136:139], a[204:207], v[192:195]\n"
"ds_read_b64_tr_b16 a[254:255], v235 offset:53760\n"
"ds_read_b64_tr_b16 a[246:247], v232 offset:53248\n"
"v_mov_b32 v253, v202\n"
"v_sub_f32 v186, s74, v186\n"
"v_mfma_f32_16x16x32_bf16 v[152:155], v[208:211], a[0:3], v[180:183]\n"
"v_mfma_f32_16x16x32_bf16 v[152:155], v[212:215], a[4:7], v[152:155]\n"
"v_sub_f32 v187, s74, v187\n"
"v_mfma_f32_16x16x32_bf16 v[152:155], v[216:219], a[8:11], v[152:155]\n"
"s_add_u32 s40, s56, s54\n"
"v_mfma_f32_16x16x32_bf16 v[152:155], v[220:223], a[12:15], v[152:155]\n"
"buffer_load_dwordx4 v207, s[20:23], s40 offen offset:3072 nt lds\n"
"v_mov_b32_dpp v247, v247 row_ror:4 row_mask:0x8 bank_mask:0xf\n"
"v_sub_f32 v189, s74, v189\n"
"v_sub_f32 v190, s74, v190\n"
"v_sub_f32 v191, s74, v191\n"
"v_sub_f32 v192, s74, v192\n"
"v_mfma_f32_16x16x32_bf16 v[156:159], v[216:219], a[24:27], v[184:187]\n"
"s_add_u32 s55, s55, s59\n"
"v_mfma_f32_16x16x32_bf16 v[156:159], v[220:223], a[28:31], v[156:159]\n"
"s_or_b32 s55, s55, s58\n"
"v_sub_f32 v188, s74, v188\n"
"v_mfma_f32_16x16x32_bf16 v[156:159], v[208:211], a[16:19], v[156:159]\n"
"s_xor_b32 s56, s55, s57\n"
"v_exp_f32 v153, v153\n"
"v_mfma_f32_16x16x32_bf16 v[156:159], v[212:215], a[20:23], v[156:159]\n"
"ds_write_b64 v250, v[252:253] offset:52224\n"
"v_sub_f32 v193, s74, v193\n"
"v_sub_f32 v194, s74, v194\n"
"v_exp_f32 v152, v152\n"
"v_mfma_f32_16x16x32_bf16 v[160:163], v[208:211], a[32:35], v[188:191]\n"
"v_mfma_f32_16x16x32_bf16 v[160:163], v[212:215], a[36:39], v[160:163]\n"
"v_rcp_f32 v206, v200\n"
"v_mfma_f32_16x16x32_bf16 v[160:163], v[216:219], a[40:43], v[160:163]\n"
"v_sub_f32 v195, s74, v195\n"
"v_mfma_f32_16x16x32_bf16 v[160:163], v[220:223], a[44:47], v[160:163]\n"
"ds_write_b128 v244, a[216:219] offset:32768\n"
"v_mul_f32_dpp v25, v246, v25 row_newbcast:1 row_mask:0xf bank_mask:0xf\n"
"v_mfma_f32_16x16x32_bf16 v[164:167], v[216:219], a[200:203], v[192:195]\n"
"v_mov_b32_dpp v206, v206 row_ror:12 row_mask:0x2 bank_mask:0xf\n"
"v_mfma_f32_16x16x32_bf16 v[164:167], v[220:223], a[204:207], v[164:167]\n"
"v_mfma_f32_16x16x32_bf16 v[164:167], v[208:211], a[192:195], v[164:167]\n"
"ds_read_b128 v[208:211], v228 offset:4096\n"
"v_mfma_f32_16x16x32_bf16 v[164:167], v[212:215], a[196:199], v[164:167]\n"
"ds_read_b128 v[216:219], v228 offset:4608\n"
"ds_read_b128 v[220:223], v229 offset:4608\n"
"v_mov_b32_dpp v206, v206 row_ror:8 row_mask:0x4 bank_mask:0xf\n"
"v_exp_f32 v155, v155\n"
"v_exp_f32 v160, v160\n"
"s_branch LTC_%=\n"
"LTH1_%=:\n"
"v_mfma_f32_16x16x32_bf16 a[224:227], v[124:127], a[244:247], a[224:227]\n"
"v_exp_f32 v171, v171\n"
"v_mfma_f32_16x16x32_bf16 v[16:19], v[128:131], a[244:247], v[16:19]\n"
"s_waitcnt vmcnt(8)\n"
"v_exp_f32 v172, v172\n"
"v_mfma_f32_16x16x32_bf16 v[16:19], v[132:135], a[248:251], v[16:19]\n"
"s_barrier\n"
"v_mfma_f32_16x16x32_bf16 v[48:51], v[136:139], a[244:247], v[48:51]\n"
"v_exp_f32 v169, v169\n"
"v_mfma_f32_16x16x32_bf16 v[48:51], v[140:143], a[248:251], v[48:51]\n"
"ds_read_b128 v[220:223], v229 offset:512\n"
"v_cvt_pk_bf16_f32 v117, v166, v167\n"
"v_exp_f32 v176, v176\n"
"v_exp_f32 v177, v177\n"
"v_exp_f32 v178, v178\n"
"v_mfma_f32_16x16x32_bf16 v[80:83], v[144:147], a[244:247], v[80:83]\n"
"ds_read_b64_tr_b16 a[244:245], v234 offset:49152\n"
"v_cvt_pk_bf16_f32 v108, v160, v161\n"
"v_mfma_f32_16x16x32_bf16 v[80:83], v[148:151], a[248:251], v[80:83]\n"
"ds_read_b64_tr_b16 a[246:247], v234 offset:53248\n"
"ds_read_b64_tr_b16 a[248:249], v238 offset:49152\n"
"v_exp_f32 v162, v162\n"
"v_exp_f32 v168, v168\n"
"v_exp_f32 v173, v173\n"
"v_exp_f32 v175, v175\n"
"v_mfma_f32_16x16x32_bf16 v[84:87], v[148:151], v[224:227], v[84:87]\n"
"ds_read_b64_tr_b16 a[250:251], v238 offset:53248\n"
"v_cvt_pk_bf16_f32 v116, v164, v165\n"
"v_mfma_f32_16x16x32_bf16 v[84:87], v[144:147], a[252:255], v[84:87]\n"
"v_exp_f32 v170, v170\n"
"v_mfma_f32_16x16x32_bf16 v[52:55], v[140:143], v[224:227], v[52:55]\n"
"v_cvt_pk_bf16_f32 v103, v170, v171\n"
"v_cvt_pk_bf16_f32 v110, v172, v173\n"
"v_mfma_f32_16x16x32_bf16 v[52:55], v[136:139], a[252:255], v[52:55]\n"
"v_cvt_pk_bf16_f32 v123, v154, v155\n"
"v_cvt_pk_bf16_f32 v101, v158, v159\n"
"v_mfma_f32_16x16x32_bf16 v[20:23], v[132:135], v[224:227], v[20:23]\n"
"v_cvt_pk_bf16_f32 v100, v156, v157\n"
"v_cvt_pk_bf16_f32 v102, v168, v169\n"
"v_mfma_f32_16x16x32_bf16 v[20:23], v[128:131], a[252:255], v[20:23]\n"
"v_exp_f32 v179, v179\n"
"v_mfma_f32_16x16x32_bf16 a[228:231], v[124:127], a[252:255], a[228:231]\n"
"s_waitcnt lgkmcnt(2)\n"
"ds_read_b64_tr_b16 a[252:253], v235 offset:49152\n"
"ds_read_b64_tr_b16 v[226:227], v239 offset:53248\n"
"v_mfma_f32_16x16x32_bf16 a[216:219], v[120:123], a[244:247], a[216:219]\n"
"s_add_u32 m0, s44, 81920\n"
"ds_read_b64_tr_b16 v[224:225], v239 offset:49152\n"
"v_cvt_pk_bf16_f32 v119, v178, v179\n"
"v_smfmac_f32_16x16x64_bf16 v[196:199], a[240:243], v[96:103], v241\n"
"buffer_load_dwordx4 v240, s[20:23], s56 offen nt lds\n"
"v_mfma_f32_16x16x32_bf16 v[8:11], v[96:99], a[244:247], v[8:11]\n"
"s_waitcnt lgkmcnt(3)\n"
"ds_read_b64_tr_b16 a[254:255], v235 offset:53248\n"
"v_cvt_pk_bf16_f32 v118, v176, v177\n"
"v_mfma_f32_16x16x32_bf16 v[8:11], v[100:103], a[248:251], v[8:11]\n"
"v_exp_f32 v174, v174\n"
"v_smfmac_f32_16x16x64_bf16 v[200:203], a[240:243], v[112:119], v241\n"
"v_cvt_pk_bf16_f32 v109, v162, v163\n"
"v_cvt_pk_bf16_f32 v111, v174, v175\n"
"v_mfma_f32_16x16x32_bf16 v[40:43], v[104:107], a[244:247], v[40:43]\n"
"ds_read_b128 v[212:215], v229 offset:0\n"
"v_and_b32 v245, 0x3f0, v242\n"
"v_mfma_f32_16x16x32_bf16 v[40:43], v[108:111], a[248:251], v[40:43]\n"
"v_lshlrev_b32_e64 v244, 24, s43\n"
"v_mul_f32 v244, 0x45800000, v244\n"
"v_smfmac_f32_16x16x64_bf16 v[198:201], a[240:243], v[104:111], v241\n"
"ds_read_b128 v[208:211], v228 offset:0\n"
"v_add_f32 v196, v196, v197\n"
"v_mfma_f32_16x16x32_bf16 v[72:75], v[112:115], a[244:247], v[72:75]\n"
"ds_read_b64_tr_b16 a[244:245], v234 offset:49664\n"
"v_lshrrev_b32 v245, 1, v245\n"
"v_mfma_f32_16x16x32_bf16 v[72:75], v[116:119], a[248:251], v[72:75]\n"
"ds_read_b64_tr_b16 a[246:247], v234 offset:53760\n"
"v_sub_u32 v245, v242, v245\n"
"v_mfma_f32_16x16x32_bf16 v[202:205], a[240:243], v[120:123], v[202:205]\n"
"s_waitcnt lgkmcnt(4)\n"
"ds_read_b64_tr_b16 a[248:249], v238 offset:49664\n"
"v_add_f32 v200, v200, v201\n"
"v_mfma_f32_16x16x32_bf16 v[76:79], v[116:119], v[224:227], v[76:79]\n"
"v_add_f32 v198, v198, v199\n"
"v_add_f32 v254, v196, v198\n"
"v_mfma_f32_16x16x32_bf16 v[76:79], v[112:115], a[252:255], v[76:79]\n"
"ds_read_b64_tr_b16 a[250:251], v238 offset:53760\n"
"v_xor_b32 v250, 0x1000, v245\n"
"v_mfma_f32_16x16x32_bf16 v[44:47], v[108:111], v[224:227], v[44:47]\n"
"ds_read_b128 v[216:219], v228 offset:512\n"
"v_add_f32 v254, v254, v200\n"
"v_mfma_f32_16x16x32_bf16 v[44:47], v[104:107], a[252:255], v[44:47]\n"
"v_add_f32 v244, v244, v202\n"
"v_add_f32 v254, v254, v202\n"
"v_mfma_f32_16x16x32_bf16 v[12:15], v[100:103], v[224:227], v[12:15]\n"
"s_add_u32 s40, s56, s52\n"
"ds_read_b64_tr_b16 v[226:227], v239 offset:53760\n"
"v_min3_f32 v244, v244, v196, v198\n"
"v_mfma_f32_16x16x32_bf16 v[12:15], v[96:99], a[252:255], v[12:15]\n"
"buffer_load_dwordx4 v207, s[20:23], s40 offen offset:1024 nt lds\n"
"v_min_f32 v244, v244, v200\n"
"v_rcp_f32 v244, v244\n"
"v_mfma_f32_16x16x32_bf16 a[220:223], v[120:123], a[252:255], a[220:223]\n"
"s_waitcnt lgkmcnt(4)\n"
"v_fmac_f32 v254, 0x68800000, v244\n"
"v_cmp_nge_f32 vcc, s45, v254\n"
"s_cbranch_vccnz LSLOW_%=\nLBACK1_%=:\ns_mov_b64 vcc, s[64:65]\n"
"v_mfma_f32_16x16x32_bf16 a[232:235], v[120:123], a[244:247], a[232:235]\n"
"v_rcp_f32 v246, v196\n"
"v_accvgpr_mov_b32 a2, a98\n"
"v_accvgpr_mov_b32 a7, a103\n"
"v_accvgpr_mov_b32 a10, a106\n"
"v_accvgpr_mov_b32 a15, a111\n"
"v_accvgpr_mov_b32 a22, a118\n"
"v_accvgpr_mov_b32 a30, a126\n"
"v_bfe_u32 v149, v242, 8, 2\n"
"v_lshl_add_u32 v149, v149, 4, s43\n"
"v_mfma_f32_16x16x32_bf16 v[24:27], v[96:99], a[244:247], v[24:27]\n"
"s_waitcnt lgkmcnt(2)\n"
"ds_read_b128 v[124:127], v149 offset:0\n"
"v_accvgpr_mov_b32 a28, a124\n"
"v_mfma_f32_16x16x32_bf16 v[24:27], v[100:103], a[248:251], v[24:27]\n"
"ds_read_b128 v[132:135], v149 offset:512\n"
"v_mov_b32_dpp v246, v246 row_ror:12 row_mask:0x2 bank_mask:0xf\n"
"v_accvgpr_mov_b32 a3, a99\n"
"v_accvgpr_mov_b32 a12, a108\n"
"v_accvgpr_mov_b32 a18, a114\n"
"v_accvgpr_mov_b32 a19, a115\n"
"v_accvgpr_mov_b32 a26, a122\n"
"v_accvgpr_mov_b32 a36, a132\n"
"v_xor_b32 v148, 0x400, v243\n"
"v_xor_b32 v150, 64, v149\n"
"v_accvgpr_mov_b32 a124, a76\n"
"v_mfma_f32_16x16x32_bf16 v[56:59], v[104:107], a[244:247], v[56:59]\n"
"ds_read_b128 v[128:131], v150 offset:0\n"
"v_accvgpr_mov_b32 a39, a135\n"
"v_mfma_f32_16x16x32_bf16 v[56:59], v[108:111], a[248:251], v[56:59]\n"
"ds_read_b128 v[136:139], v150 offset:512\n"
"v_rcp_f32 v247, v198\n"
"v_mov_b32_dpp v246, v246 row_ror:8 row_mask:0x4 bank_mask:0xf\n"
"v_accvgpr_mov_b32 a0, a96\n"
"v_accvgpr_mov_b32 a1, a97\n"
"v_accvgpr_mov_b32 a4, a100\n"
"v_accvgpr_mov_b32 a5, a101\n"
"v_accvgpr_mov_b32 a8, a104\n"
"v_accvgpr_mov_b32 a16, a112\n"
"v_accvgpr_mov_b32 a25, a121\n"
"v_mfma_f32_16x16x32_bf16 v[88:91], v[112:115], a[244:247], v[88:91]\n"
"v_accvgpr_mov_b32 a20, a116\n"
"v_accvgpr_mov_b32 a29, a125\n"
"v_mfma_f32_16x16x32_bf16 v[88:91], v[116:119], a[248:251], v[88:91]\n"
"s_waitcnt lgkmcnt(3)\n"
"ds_read_b128 a[192:195], v243 offset:4096\n"
"ds_read_b128 a[200:203], v243 offset:6144\n"
"v_accvgpr_mov_b32 a6, a102\n"
"v_accvgpr_mov_b32 a9, a105\n"
"v_accvgpr_mov_b32 a11, a107\n"
"v_accvgpr_mov_b32 a13, a109\n"
"v_accvgpr_mov_b32 a14, a110\n"
"v_accvgpr_mov_b32 a17, a113\n"
"v_accvgpr_mov_b32 a23, a119\n"
"v_accvgpr_mov_b32 a31, a127\n"
"v_accvgpr_mov_b32 a33, a129\n"
"v_accvgpr_mov_b32 a35, a131\n"
"v_accvgpr_mov_b32 a44, a140\n"
"v_accvgpr_mov_b32 a47, a143\n"
"v_accvgpr_mov_b32 a107, a59\n"
"v_mfma_f32_16x16x32_bf16 v[180:183], v[124:127], a[0:3], 0\n"
"s_waitcnt lgkmcnt(3)\n"
"v_xor_b32 v244, 0x11000, v242\n"
"v_accvgpr_mov_b32 a132, a84\n"
"v_mfma_f32_16x16x32_bf16 v[180:183], v[128:131], a[4:7], v[180:183]\n"
"v_accvgpr_mov_b32 a34, a130\n"
"v_accvgpr_mov_b32 a43, a139\n"
"v_mfma_f32_16x16x32_bf16 v[180:183], v[132:135], a[8:11], v[180:183]\n"
"s_waitcnt lgkmcnt(2)\n"
"ds_read_b128 a[204:207], v148 offset:6144\n"
"v_accvgpr_mov_b32 a37, a133\n"
"v_mfma_f32_16x16x32_bf16 v[180:183], v[136:139], a[12:15], v[180:183]\n"
"ds_read_b128 a[196:199], v148 offset:4096\n"
"v_accvgpr_mov_b32 a21, a117\n"
"v_accvgpr_mov_b32 a24, a120\n"
"v_accvgpr_mov_b32 a32, a128\n"
"v_accvgpr_mov_b32 a38, a134\n"
"v_accvgpr_mov_b32 a40, a136\n"
"v_accvgpr_mov_b32 a42, a138\n"
"v_accvgpr_mov_b32 a46, a142\n"
"v_accvgpr_mov_b32 a117, a69\n"
"v_accvgpr_mov_b32 a111, a63\n"
"v_mfma_f32_16x16x32_bf16 v[184:187], v[124:127], a[16:19], 0\n"
"v_mov_b32_dpp v246, v246 row_ror:4 row_mask:0x8 bank_mask:0xf\n"
"v_accvgpr_mov_b32 a27, a123\n"
"v_mfma_f32_16x16x32_bf16 v[184:187], v[128:131], a[20:23], v[184:187]\n"
"v_mov_b32_dpp v247, v247 row_ror:12 row_mask:0x2 bank_mask:0xf\n"
"v_accvgpr_mov_b32 a97, a49\n"
"v_mfma_f32_16x16x32_bf16 v[184:187], v[132:135], a[24:27], v[184:187]\n"
"s_add_u32 s40, s56, s53\n"
"ds_read_b64_tr_b16 v[224:225], v239 offset:49664\n"
"v_accvgpr_mov_b32 a112, a64\n"
"v_mfma_f32_16x16x32_bf16 v[184:187], v[136:139], a[28:31], v[184:187]\n"
"buffer_load_dwordx4 v240, s[20:23], s40 offen offset:2048 nt lds\n"
"v_accvgpr_mov_b32 a41, a137\n"
"v_accvgpr_mov_b32 a126, a78\n"
"v_accvgpr_mov_b32 a114, a66\n"
"v_accvgpr_mov_b32 a137, a89\n"
"v_sub_f32 v180, s74, v180\n"
"v_sub_f32 v181, s74, v181\n"
"v_sub_f32 v183, s74, v183\n"
"v_mul_f32_dpp v26, v246, v26 row_newbcast:2 row_mask:0xf bank_mask:0xf\n"
"v_mfma_f32_16x16x32_bf16 v[188:191], v[124:127], a[32:35], 0\n"
"v_mov_b32_dpp v247, v247 row_ror:8 row_mask:0x4 bank_mask:0xf\n"
"v_accvgpr_mov_b32 a45, a141\n"
"v_mfma_f32_16x16x32_bf16 v[188:191], v[128:131], a[36:39], v[188:191]\n"
"v_mov_b32 v252, v192\n"
"v_accvgpr_mov_b32 a133, a85\n"
"v_mfma_f32_16x16x32_bf16 v[188:191], v[132:135], a[40:43], v[188:191]\n"
"v_accvgpr_mov_b32 a118, a70\n"
"v_accvgpr_mov_b32 a105, a57\n"
"v_mfma_f32_16x16x32_bf16 v[188:191], v[136:139], a[44:47], v[188:191]\n"
"s_waitcnt lgkmcnt(1)\n"
"v_accvgpr_mov_b32 a109, a61\n"
"v_mul_f32_dpp v11, v246, v11 row_newbcast:3 row_mask:0xf bank_mask:0xf\n"
"v_mfma_f32_16x16x32_bf16 v[192:195], v[124:127], a[192:195], 0\n"
"ds_read_b64_tr_b16 a[252:253], v235 offset:49664\n"
"v_sub_f32 v185, s74, v185\n"
"v_mfma_f32_16x16x32_bf16 v[192:195], v[128:131], a[196:199], v[192:195]\n"
"ds_read_b64_tr_b16 a[248:249], v236 offset:49152\n"
"v_sub_f32 v182, s74, v182\n"
"v_mfma_f32_16x16x32_bf16 v[192:195], v[132:135], a[200:203], v[192:195]\n"
"v_accvgpr_mov_b32 a138, a90\n"
"v_sub_f32 v184, s74, v184\n"
"v_mfma_f32_16x16x32_bf16 v[192:195], v[136:139], a[204:207], v[192:195]\n"
"ds_read_b64_tr_b16 a[254:255], v235 offset:53760\n"
"ds_read_b64_tr_b16 a[246:247], v232 offset:53248\n"
"v_mov_b32 v253, v202\n"
"v_accvgpr_mov_b32 a116, a68\n"
"v_accvgpr_mov_b32 a125, a77\n"
"v_accvgpr_mov_b32 a113, a65\n"
"v_accvgpr_mov_b32 a128, a80\n"
"v_accvgpr_mov_b32 a130, a82\n"
"v_accvgpr_mov_b32 a136, a88\n"
"v_sub_f32 v186, s74, v186\n"
"v_mfma_f32_16x16x32_bf16 v[152:155], v[208:211], a[0:3], v[180:183]\n"
"v_accvgpr_mov_b32 a103, a55\n"
"v_accvgpr_mov_b32 a120, a72\n"
"v_mfma_f32_16x16x32_bf16 v[152:155], v[212:215], a[4:7], v[152:155]\n"
"v_accvgpr_mov_b32 a141, a93\n"
"v_sub_f32 v187, s74, v187\n"
"v_mfma_f32_16x16x32_bf16 v[152:155], v[216:219], a[8:11], v[152:155]\n"
"s_add_u32 s40, s56, s54\n"
"v_accvgpr_mov_b32 a127, a79\n"
"v_accvgpr_mov_b32 a140, a92\n"
"v_mfma_f32_16x16x32_bf16 v[152:155], v[220:223], a[12:15], v[152:155]\n"
"buffer_load_dwordx4 v207, s[20:23], s40 offen offset:3072 nt lds\n"
"v_mov_b32_dpp v247, v247 row_ror:4 row_mask:0x8 bank_mask:0xf\n"
"v_accvgpr_mov_b32 a121, a73\n"
"v_accvgpr_mov_b32 a122, a74\n"
"v_accvgpr_mov_b32 a123, a75\n"
"v_sub_f32 v189, s74, v189\n"
"v_sub_f32 v190, s74, v190\n"
"v_sub_f32 v191, s74, v191\n"
"v_sub_f32 v192, s74, v192\n"
"v_mfma_f32_16x16x32_bf16 v[156:159], v[216:219], a[24:27], v[184:187]\n"
"s_add_u32 s55, s55, s59\n"
"v_accvgpr_mov_b32 a143, a95\n"
"v_accvgpr_mov_b32 a115, a67\n"
"v_mfma_f32_16x16x32_bf16 v[156:159], v[220:223], a[28:31], v[156:159]\n"
"s_or_b32 s55, s55, s58\n"
"v_accvgpr_mov_b32 a101, a53\n"
"v_sub_f32 v188, s74, v188\n"
"v_mfma_f32_16x16x32_bf16 v[156:159], v[208:211], a[16:19], v[156:159]\n"
"s_xor_b32 s56, s55, s57\n"
"v_exp_f32 v153, v153\n"
"v_mfma_f32_16x16x32_bf16 v[156:159], v[212:215], a[20:23], v[156:159]\n"
"ds_write_b64 v250, v[252:253] offset:52224\n"
"v_accvgpr_mov_b32 a100, a52\n"
"v_accvgpr_mov_b32 a135, a87\n"
"v_accvgpr_mov_b32 a98, a50\n"
"v_sub_f32 v193, s74, v193\n"
"v_sub_f32 v194, s74, v194\n"
"v_exp_f32 v152, v152\n"
"v_mfma_f32_16x16x32_bf16 v[160:163], v[208:211], a[32:35], v[188:191]\n"
"v_accvgpr_mov_b32 a110, a62\n"
"v_accvgpr_mov_b32 a96, a48\n"
"v_mfma_f32_16x16x32_bf16 v[160:163], v[212:215], a[36:39], v[160:163]\n"
"v_rcp_f32 v206, v200\n"
"v_mfma_f32_16x16x32_bf16 v[160:163], v[216:219], a[40:43], v[160:163]\n"
"v_accvgpr_mov_b32 a142, a94\n"
"v_sub_f32 v195, s74, v195\n"
"v_mfma_f32_16x16x32_bf16 v[160:163], v[220:223], a[44:47], v[160:163]\n"
"ds_write_b128 v244, a[216:219] offset:32768\n"
"v_accvgpr_mov_b32 a102, a54\n"
"v_accvgpr_mov_b32 a108, a60\n"
"v_accvgpr_mov_b32 a99, a51\n"
"v_accvgpr_mov_b32 a129, a81\n"
"v_mul_f32_dpp v25, v246, v25 row_newbcast:1 row_mask:0xf bank_mask:0xf\n"
"v_mfma_f32_16x16x32_bf16 v[164:167], v[216:219], a[200:203], v[192:195]\n"
"v_mov_b32_dpp v206, v206 row_ror:12 row_mask:0x2 bank_mask:0xf\n"
"v_accvgpr_mov_b32 a119, a71\n"
"v_mfma_f32_16x16x32_bf16 v[164:167], v[220:223], a[204:207], v[164:167]\n"
"v_accvgpr_mov_b32 a131, a83\n"
"v_accvgpr_mov_b32 a139, a91\n"
"v_mfma_f32_16x16x32_bf16 v[164:167], v[208:211], a[192:195], v[164:167]\n"
"ds_read_b128 v[208:211], v228 offset:4096\n"
"v_accvgpr_mov_b32 a106, a58\n"
"v_mfma_f32_16x16x32_bf16 v[164:167], v[212:215], a[196:199], v[164:167]\n"
"s_waitcnt lgkmcnt(2)\n"
"ds_read_b128 v[216:219], v228 offset:4608\n"
"ds_read_b128 v[220:223], v229 offset:4608\n"
"v_mov_b32_dpp v206, v206 row_ror:8 row_mask:0x4 bank_mask:0xf\n"
"v_accvgpr_mov_b32 a134, a86\n"
"v_accvgpr_mov_b32 a104, a56\n"
"v_exp_f32 v155, v155\n"
"v_exp_f32 v160, v160\n"
"s_branch LTC_%=\n"
"LTH2_%=:\n"
"v_mfma_f32_16x16x32_bf16 a[224:227], v[124:127], a[244:247], a[224:227]\n"
"v_exp_f32 v171, v171\n"
"v_mfma_f32_16x16x32_bf16 v[16:19], v[128:131], a[244:247], v[16:19]\n"
"s_waitcnt vmcnt(8)\n"
"v_exp_f32 v172, v172\n"
"v_mfma_f32_16x16x32_bf16 v[16:19], v[132:135], a[248:251], v[16:19]\n"
"s_barrier\n"
"v_mfma_f32_16x16x32_bf16 v[48:51], v[136:139], a[244:247], v[48:51]\n"
"v_exp_f32 v169, v169\n"
"v_mfma_f32_16x16x32_bf16 v[48:51], v[140:143], a[248:251], v[48:51]\n"
"ds_read_b128 v[220:223], v229 offset:512\n"
"v_cvt_pk_bf16_f32 v117, v166, v167\n"
"v_exp_f32 v176, v176\n"
"v_exp_f32 v177, v177\n"
"v_exp_f32 v178, v178\n"
"v_mfma_f32_16x16x32_bf16 v[80:83], v[144:147], a[244:247], v[80:83]\n"
"ds_read_b64_tr_b16 a[244:245], v234 offset:49152\n"
"v_cvt_pk_bf16_f32 v108, v160, v161\n"
"v_mfma_f32_16x16x32_bf16 v[80:83], v[148:151], a[248:251], v[80:83]\n"
"ds_read_b64_tr_b16 a[246:247], v234 offset:53248\n"
"ds_read_b64_tr_b16 a[248:249], v238 offset:49152\n"
"v_exp_f32 v162, v162\n"
"v_exp_f32 v168, v168\n"
"v_exp_f32 v173, v173\n"
"v_exp_f32 v175, v175\n"
"v_mfma_f32_16x16x32_bf16 v[84:87], v[148:151], v[224:227], v[84:87]\n"
"ds_read_b64_tr_b16 a[250:251], v238 offset:53248\n"
"v_cvt_pk_bf16_f32 v116, v164, v165\n"
"v_mfma_f32_16x16x32_bf16 v[84:87], v[144:147], a[252:255], v[84:87]\n"
"v_exp_f32 v170, v170\n"
"v_mfma_f32_16x16x32_bf16 v[52:55], v[140:143], v[224:227], v[52:55]\n"
"v_cvt_pk_bf16_f32 v103, v170, v171\n"
"v_cvt_pk_bf16_f32 v110, v172, v173\n"
"v_mfma_f32_16x16x32_bf16 v[52:55], v[136:139], a[252:255], v[52:55]\n"
"v_cvt_pk_bf16_f32 v123, v154, v155\n"
"v_cvt_pk_bf16_f32 v101, v158, v159\n"
"v_mfma_f32_16x16x32_bf16 v[20:23], v[132:135], v[224:227], v[20:23]\n"
"v_cvt_pk_bf16_f32 v100, v156, v157\n"
"v_cvt_pk_bf16_f32 v102, v168, v169\n"
"v_mfma_f32_16x16x32_bf16 v[20:23], v[128:131], a[252:255], v[20:23]\n"
"v_exp_f32 v179, v179\n"
"v_mfma_f32_16x16x32_bf16 a[228:231], v[124:127], a[252:255], a[228:231]\n"
"s_waitcnt lgkmcnt(2)\n"
"ds_read_b64_tr_b16 a[252:253], v235 offset:49152\n"
"ds_read_b64_tr_b16 v[226:227], v239 offset:53248\n"
"v_mfma_f32_16x16x32_bf16 a[216:219], v[120:123], a[244:247], a[216:219]\n"
"s_add_u32 m0, s44, 81920\n"
"ds_read_b64_tr_b16 v[224:225], v239 offset:49152\n"
"v_cvt_pk_bf16_f32 v119, v178, v179\n"
"v_smfmac_f32_16x16x64_bf16 v[196:199], a[240:243], v[96:103], v241\n"
"buffer_load_dwordx4 v240, s[20:23], s56 offen nt lds\n"
"v_mfma_f32_16x16x32_bf16 v[8:11], v[96:99], a[244:247], v[8:11]\n"
"s_waitcnt lgkmcnt(3)\n"
"ds_read_b64_tr_b16 a[254:255], v235 offset:53248\n"
"v_cvt_pk_bf16_f32 v118, v176, v177\n"
"v_mfma_f32_16x16x32_bf16 v[8:11], v[100:103], a[248:251], v[8:11]\n"
"v_exp_f32 v174, v174\n"
"v_smfmac_f32_16x16x64_bf16 v[200:203], a[240:243], v[112:119], v241\n"
"v_cvt_pk_bf16_f32 v109, v162, v163\n"
"v_cvt_pk_bf16_f32 v111, v174, v175\n"
"v_mfma_f32_16x16x32_bf16 v[40:43], v[104:107], a[244:247], v[40:43]\n"
"ds_read_b128 v[212:215], v229 offset:0\n"
"v_and_b32 v245, 0x3f0, v242\n"
"v_mfma_f32_16x16x32_bf16 v[40:43], v[108:111], a[248:251], v[40:43]\n"
"v_lshlrev_b32_e64 v244, 24, s43\n"
"v_mul_f32 v244, 0x45800000, v244\n"
"v_smfmac_f32_16x16x64_bf16 v[198:201], a[240:243], v[104:111], v241\n"
"ds_read_b128 v[208:211], v228 offset:0\n"
"v_add_f32 v196, v196, v197\n"
"v_mfma_f32_16x16x32_bf16 v[72:75], v[112:115], a[244:247], v[72:75]\n"
"ds_read_b64_tr_b16 a[244:245], v234 offset:49664\n"
"v_lshrrev_b32 v245, 1, v245\n"
"v_mfma_f32_16x16x32_bf16 v[72:75], v[116:119], a[248:251], v[72:75]\n"
"ds_read_b64_tr_b16 a[246:247], v234 offset:53760\n"
"v_sub_u32 v245, v242, v245\n"
"v_mfma_f32_16x16x32_bf16 v[202:205], a[240:243], v[120:123], v[202:205]\n"
"s_waitcnt lgkmcnt(4)\n"
"ds_read_b64_tr_b16 a[248:249], v238 offset:49664\n"
"v_add_f32 v200, v200, v201\n"
"v_mfma_f32_16x16x32_bf16 v[76:79], v[116:119], v[224:227], v[76:79]\n"
"v_add_f32 v198, v198, v199\n"
"v_add_f32 v254, v196, v198\n"
"v_mfma_f32_16x16x32_bf16 v[76:79], v[112:115], a[252:255], v[76:79]\n"
"ds_read_b64_tr_b16 a[250:251], v238 offset:53760\n"
"v_xor_b32 v250, 0x1000, v245\n"
"v_mfma_f32_16x16x32_bf16 v[44:47], v[108:111], v[224:227], v[44:47]\n"
"ds_read_b128 v[216:219], v228 offset:512\n"
"v_add_f32 v254, v254, v200\n"
"v_mfma_f32_16x16x32_bf16 v[44:47], v[104:107], a[252:255], v[44:47]\n"
"v_add_f32 v244, v244, v202\n"
"v_add_f32 v254, v254, v202\n"
"v_mfma_f32_16x16x32_bf16 v[12:15], v[100:103], v[224:227], v[12:15]\n"
"s_add_u32 s40, s56, s52\n"
"ds_read_b64_tr_b16 v[226:227], v239 offset:53760\n"
"v_min3_f32 v244, v244, v196, v198\n"
"v_mfma_f32_16x16x32_bf16 v[12:15], v[96:99], a[252:255], v[12:15]\n"
"buffer_load_dwordx4 v207, s[20:23], s40 offen offset:1024 nt lds\n"
"v_min_f32 v244, v244, v200\n"
"v_rcp_f32 v244, v244\n"
"v_mfma_f32_16x16x32_bf16 a[220:223], v[120:123], a[252:255], a[220:223]\n"
"s_waitcnt lgkmcnt(4)\n"
"v_fmac_f32 v254, 0x68800000, v244\n"
"v_cmp_nge_f32 vcc, s45, v254\n"
"s_cbranch_vccnz LSLOW_%=\nLBACK2_%=:\ns_mov_b64 vcc, s[64:65]\n"
"v_mfma_f32_16x16x32_bf16 a[232:235], v[120:123], a[244:247], a[232:235]\n"
"v_rcp_f32 v246, v196\n"
"v_accvgpr_mov_b32 a2, a146\n"
"v_accvgpr_mov_b32 a7, a151\n"
"v_accvgpr_mov_b32 a10, a154\n"
"v_accvgpr_mov_b32 a15, a159\n"
"v_accvgpr_mov_b32 a22, a166\n"
"v_accvgpr_mov_b32 a30, a174\n"
"v_bfe_u32 v149, v242, 8, 2\n"
"v_lshl_add_u32 v149, v149, 4, s43\n"
"v_mfma_f32_16x16x32_bf16 v[24:27], v[96:99], a[244:247], v[24:27]\n"
"s_waitcnt lgkmcnt(2)\n"
"ds_read_b128 v[124:127], v149 offset:0\n"
"v_accvgpr_mov_b32 a28, a172\n"
"v_mfma_f32_16x16x32_bf16 v[24:27], v[100:103], a[248:251], v[24:27]\n"
"ds_read_b128 v[132:135], v149 offset:512\n"
"v_mov_b32_dpp v246, v246 row_ror:12 row_mask:0x2 bank_mask:0xf\n"
"v_accvgpr_mov_b32 a3, a147\n"
"v_accvgpr_mov_b32 a12, a156\n"
"v_accvgpr_mov_b32 a18, a162\n"
"v_accvgpr_mov_b32 a19, a163\n"
"v_accvgpr_mov_b32 a26, a170\n"
"v_accvgpr_mov_b32 a36, a180\n"
"v_xor_b32 v148, 0x400, v243\n"
"v_xor_b32 v150, 64, v149\n"
"v_accvgpr_mov_b32 a172, a76\n"
"v_mfma_f32_16x16x32_bf16 v[56:59], v[104:107], a[244:247], v[56:59]\n"
"ds_read_b128 v[128:131], v150 offset:0\n"
"v_accvgpr_mov_b32 a39, a183\n"
"v_mfma_f32_16x16x32_bf16 v[56:59], v[108:111], a[248:251], v[56:59]\n"
"ds_read_b128 v[136:139], v150 offset:512\n"
"v_rcp_f32 v247, v198\n"
"v_mov_b32_dpp v246, v246 row_ror:8 row_mask:0x4 bank_mask:0xf\n"
"v_accvgpr_mov_b32 a0, a144\n"
"v_accvgpr_mov_b32 a1, a145\n"
"v_accvgpr_mov_b32 a4, a148\n"
"v_accvgpr_mov_b32 a5, a149\n"
"v_accvgpr_mov_b32 a8, a152\n"
"v_accvgpr_mov_b32 a16, a160\n"
"v_accvgpr_mov_b32 a25, a169\n"
"v_mfma_f32_16x16x32_bf16 v[88:91], v[112:115], a[244:247], v[88:91]\n"
"v_accvgpr_mov_b32 a20, a164\n"
"v_accvgpr_mov_b32 a29, a173\n"
"v_mfma_f32_16x16x32_bf16 v[88:91], v[116:119], a[248:251], v[88:91]\n"
"s_waitcnt lgkmcnt(3)\n"
"ds_read_b128 a[192:195], v243 offset:4096\n"
"ds_read_b128 a[200:203], v243 offset:6144\n"
"v_accvgpr_mov_b32 a6, a150\n"
"v_accvgpr_mov_b32 a9, a153\n"
"v_accvgpr_mov_b32 a11, a155\n"
"v_accvgpr_mov_b32 a13, a157\n"
"v_accvgpr_mov_b32 a14, a158\n"
"v_accvgpr_mov_b32 a17, a161\n"
"v_accvgpr_mov_b32 a23, a167\n"
"v_accvgpr_mov_b32 a31, a175\n"
"v_accvgpr_mov_b32 a33, a177\n"
"v_accvgpr_mov_b32 a35, a179\n"
"v_accvgpr_mov_b32 a44, a188\n"
"v_accvgpr_mov_b32 a47, a191\n"
"v_accvgpr_mov_b32 a155, a59\n"
"v_mfma_f32_16x16x32_bf16 v[180:183], v[124:127], a[0:3], 0\n"
"s_waitcnt lgkmcnt(3)\n"
"v_xor_b32 v244, 0x11000, v242\n"
"v_accvgpr_mov_b32 a180, a84\n"
"v_mfma_f32_16x16x32_bf16 v[180:183], v[128:131], a[4:7], v[180:183]\n"
"v_accvgpr_mov_b32 a34, a178\n"
"v_accvgpr_mov_b32 a43, a187\n"
"v_mfma_f32_16x16x32_bf16 v[180:183], v[132:135], a[8:11], v[180:183]\n"
"s_waitcnt lgkmcnt(2)\n"
"ds_read_b128 a[204:207], v148 offset:6144\n"
"v_accvgpr_mov_b32 a37, a181\n"
"v_mfma_f32_16x16x32_bf16 v[180:183], v[136:139], a[12:15], v[180:183]\n"
"ds_read_b128 a[196:199], v148 offset:4096\n"
"v_accvgpr_mov_b32 a21, a165\n"
"v_accvgpr_mov_b32 a24, a168\n"
"v_accvgpr_mov_b32 a32, a176\n"
"v_accvgpr_mov_b32 a38, a182\n"
"v_accvgpr_mov_b32 a40, a184\n"
"v_accvgpr_mov_b32 a42, a186\n"
"v_accvgpr_mov_b32 a46, a190\n"
"v_accvgpr_mov_b32 a165, a69\n"
"v_accvgpr_mov_b32 a159, a63\n"
"v_mfma_f32_16x16x32_bf16 v[184:187], v[124:127], a[16:19], 0\n"
"v_mov_b32_dpp v246, v246 row_ror:4 row_mask:0x8 bank_mask:0xf\n"
"v_accvgpr_mov_b32 a27, a171\n"
"v_mfma_f32_16x16x32_bf16 v[184:187], v[128:131], a[20:23], v[184:187]\n"
"v_mov_b32_dpp v247, v247 row_ror:12 row_mask:0x2 bank_mask:0xf\n"
"v_accvgpr_mov_b32 a145, a49\n"
"v_mfma_f32_16x16x32_bf16 v[184:187], v[132:135], a[24:27], v[184:187]\n"
"s_add_u32 s40, s56, s53\n"
"ds_read_b64_tr_b16 v[224:225], v239 offset:49664\n"
"v_accvgpr_mov_b32 a160, a64\n"
"v_mfma_f32_16x16x32_bf16 v[184:187], v[136:139], a[28:31], v[184:187]\n"
"buffer_load_dwordx4 v240, s[20:23], s40 offen offset:2048 nt lds\n"
"v_accvgpr_mov_b32 a41, a185\n"
"v_accvgpr_mov_b32 a174, a78\n"
"v_accvgpr_mov_b32 a162, a66\n"
"v_accvgpr_mov_b32 a185, a89\n"
"v_sub_f32 v180, s74, v180\n"
"v_sub_f32 v181, s74, v181\n"
"v_sub_f32 v183, s74, v183\n"
"v_mul_f32_dpp v26, v246, v26 row_newbcast:2 row_mask:0xf bank_mask:0xf\n"
"v_mfma_f32_16x16x32_bf16 v[188:191], v[124:127], a[32:35], 0\n"
"v_mov_b32_dpp v247, v247 row_ror:8 row_mask:0x4 bank_mask:0xf\n"
"v_accvgpr_mov_b32 a45, a189\n"
"v_mfma_f32_16x16x32_bf16 v[188:191], v[128:131], a[36:39], v[188:191]\n"
"v_mov_b32 v252, v192\n"
"v_accvgpr_mov_b32 a181, a85\n"
"v_mfma_f32_16x16x32_bf16 v[188:191], v[132:135], a[40:43], v[188:191]\n"
"v_accvgpr_mov_b32 a166, a70\n"
"v_accvgpr_mov_b32 a153, a57\n"
"v_mfma_f32_16x16x32_bf16 v[188:191], v[136:139], a[44:47], v[188:191]\n"
"s_waitcnt lgkmcnt(1)\n"
"v_accvgpr_mov_b32 a157, a61\n"
"v_mul_f32_dpp v11, v246, v11 row_newbcast:3 row_mask:0xf bank_mask:0xf\n"
"v_mfma_f32_16x16x32_bf16 v[192:195], v[124:127], a[192:195], 0\n"
"ds_read_b64_tr_b16 a[252:253], v235 offset:49664\n"
"v_sub_f32 v185, s74, v185\n"
"v_mfma_f32_16x16x32_bf16 v[192:195], v[128:131], a[196:199], v[192:195]\n"
"ds_read_b64_tr_b16 a[248:249], v236 offset:49152\n"
"v_sub_f32 v182, s74, v182\n"
"v_mfma_f32_16x16x32_bf16 v[192:195], v[132:135], a[200:203], v[192:195]\n"
"v_accvgpr_mov_b32 a186, a90\n"
"v_sub_f32 v184, s74, v184\n"
"v_mfma_f32_16x16x32_bf16 v[192:195], v[136:139], a[204:207], v[192:195]\n"
"ds_read_b64_tr_b16 a[254:255], v235 offset:53760\n"
"ds_read_b64_tr_b16 a[246:247], v232 offset:53248\n"
"v_mov_b32 v253, v202\n"
"v_accvgpr_mov_b32 a164, a68\n"
"v_accvgpr_mov_b32 a173, a77\n"
"v_accvgpr_mov_b32 a161, a65\n"
"v_accvgpr_mov_b32 a176, a80\n"
"v_accvgpr_mov_b32 a178, a82\n"
"v_accvgpr_mov_b32 a184, a88\n"
"v_sub_f32 v186, s74, v186\n"
"v_mfma_f32_16x16x32_bf16 v[152:155], v[208:211], a[0:3], v[180:183]\n"
"v_accvgpr_mov_b32 a151, a55\n"
"v_accvgpr_mov_b32 a168, a72\n"
"v_mfma_f32_16x16x32_bf16 v[152:155], v[212:215], a[4:7], v[152:155]\n"
"v_accvgpr_mov_b32 a189, a93\n"
"v_sub_f32 v187, s74, v187\n"
"v_mfma_f32_16x16x32_bf16 v[152:155], v[216:219], a[8:11], v[152:155]\n"
"s_add_u32 s40, s56, s54\n"
"v_accvgpr_mov_b32 a175, a79\n"
"v_accvgpr_mov_b32 a188, a92\n"
"v_mfma_f32_16x16x32_bf16 v[152:155], v[220:223], a[12:15], v[152:155]\n"
"buffer_load_dwordx4 v207, s[20:23], s40 offen offset:3072 nt lds\n"
"v_mov_b32_dpp v247, v247 row_ror:4 row_mask:0x8 bank_mask:0xf\n"
"v_accvgpr_mov_b32 a169, a73\n"
"v_accvgpr_mov_b32 a170, a74\n"
"v_accvgpr_mov_b32 a171, a75\n"
"v_sub_f32 v189, s74, v189\n"
"v_sub_f32 v190, s74, v190\n"
"v_sub_f32 v191, s74, v191\n"
"v_sub_f32 v192, s74, v192\n"
"v_mfma_f32_16x16x32_bf16 v[156:159], v[216:219], a[24:27], v[184:187]\n"
"s_add_u32 s55, s55, s59\n"
"v_accvgpr_mov_b32 a191, a95\n"
"v_accvgpr_mov_b32 a163, a67\n"
"v_mfma_f32_16x16x32_bf16 v[156:159], v[220:223], a[28:31], v[156:159]\n"
"s_or_b32 s55, s55, s58\n"
"v_accvgpr_mov_b32 a149, a53\n"
"v_sub_f32 v188, s74, v188\n"
"v_mfma_f32_16x16x32_bf16 v[156:159], v[208:211], a[16:19], v[156:159]\n"
"s_xor_b32 s56, s55, s57\n"
"v_exp_f32 v153, v153\n"
"v_mfma_f32_16x16x32_bf16 v[156:159], v[212:215], a[20:23], v[156:159]\n"
"ds_write_b64 v250, v[252:253] offset:52224\n"
"v_accvgpr_mov_b32 a148, a52\n"
"v_accvgpr_mov_b32 a183, a87\n"
"v_accvgpr_mov_b32 a146, a50\n"
"v_sub_f32 v193, s74, v193\n"
"v_sub_f32 v194, s74, v194\n"
"v_exp_f32 v152, v152\n"
"v_mfma_f32_16x16x32_bf16 v[160:163], v[208:211], a[32:35], v[188:191]\n"
"v_accvgpr_mov_b32 a158, a62\n"
"v_accvgpr_mov_b32 a144, a48\n"
"v_mfma_f32_16x16x32_bf16 v[160:163], v[212:215], a[36:39], v[160:163]\n"
"v_rcp_f32 v206, v200\n"
"v_mfma_f32_16x16x32_bf16 v[160:163], v[216:219], a[40:43], v[160:163]\n"
"v_accvgpr_mov_b32 a190, a94\n"
"v_sub_f32 v195, s74, v195\n"
"v_mfma_f32_16x16x32_bf16 v[160:163], v[220:223], a[44:47], v[160:163]\n"
"ds_write_b128 v244, a[216:219] offset:32768\n"
"v_accvgpr_mov_b32 a150, a54\n"
"v_accvgpr_mov_b32 a156, a60\n"
"v_accvgpr_mov_b32 a147, a51\n"
"v_accvgpr_mov_b32 a177, a81\n"
"v_mul_f32_dpp v25, v246, v25 row_newbcast:1 row_mask:0xf bank_mask:0xf\n"
"v_mfma_f32_16x16x32_bf16 v[164:167], v[216:219], a[200:203], v[192:195]\n"
"v_mov_b32_dpp v206, v206 row_ror:12 row_mask:0x2 bank_mask:0xf\n"
"v_accvgpr_mov_b32 a167, a71\n"
"v_mfma_f32_16x16x32_bf16 v[164:167], v[220:223], a[204:207], v[164:167]\n"
"v_accvgpr_mov_b32 a179, a83\n"
"v_accvgpr_mov_b32 a187, a91\n"
"v_mfma_f32_16x16x32_bf16 v[164:167], v[208:211], a[192:195], v[164:167]\n"
"ds_read_b128 v[208:211], v228 offset:4096\n"
"v_accvgpr_mov_b32 a154, a58\n"
"v_mfma_f32_16x16x32_bf16 v[164:167], v[212:215], a[196:199], v[164:167]\n"
"s_waitcnt lgkmcnt(2)\n"
"ds_read_b128 v[216:219], v228 offset:4608\n"
"ds_read_b128 v[220:223], v229 offset:4608\n"
"v_mov_b32_dpp v206, v206 row_ror:8 row_mask:0x4 bank_mask:0xf\n"
"v_accvgpr_mov_b32 a182, a86\n"
"v_accvgpr_mov_b32 a152, a56\n"
"v_exp_f32 v155, v155\n"
"v_exp_f32 v160, v160\n"
"LTC_%=:\n"
"s_waitcnt lgkmcnt(4)\n"
"v_mfma_f32_16x16x32_bf16 v[92:95], v[116:119], v[224:227], v[92:95]\n"
"v_exp_f32 v154, v154\n"
"v_mfma_f32_16x16x32_bf16 v[92:95], v[112:115], a[252:255], v[92:95]\n"
"ds_read_b128 v[212:215], v229 offset:4096\n"
"v_mul_f32_dpp v24, v246, v24 row_newbcast:0 row_mask:0xf bank_mask:0xf\n"
"v_mfma_f32_16x16x32_bf16 v[60:63], v[108:111], v[224:227], v[60:63]\n"
"s_add_u32 m0, s44, 32768\n"
"v_exp_f32 v163, v163\n"
"v_mfma_f32_16x16x32_bf16 v[60:63], v[104:107], a[252:255], v[60:63]\n"
"buffer_load_dwordx4 v240, s[16:19], s56 offen nt lds\n"
"v_mul_f32_dpp v8, v246, v8 row_newbcast:0 row_mask:0xf bank_mask:0xf\n"
"v_exp_f32 v156, v156\n"
"v_exp_f32 v158, v158\n"
"v_exp_f32 v161, v161\n"
"v_exp_f32 v165, v165\n"
"v_mfma_f32_16x16x32_bf16 v[28:31], v[100:103], v[224:227], v[28:31]\n"
"v_cvt_pk_bf16_f32 v128, v152, v153\n"
"v_cvt_pk_bf16_f32 v129, v154, v155\n"
"v_mfma_f32_16x16x32_bf16 v[28:31], v[96:99], a[252:255], v[28:31]\n"
"v_mov_b32_dpp v206, v206 row_ror:4 row_mask:0x8 bank_mask:0xf\n"
"v_mul_f32_dpp v9, v246, v9 row_newbcast:1 row_mask:0xf bank_mask:0xf\n"
"v_mfma_f32_16x16x32_bf16 a[236:239], v[120:123], a[252:255], a[236:239]\n"
"s_waitcnt lgkmcnt(2)\n"
"v_exp_f32 v162, v162\n"
"v_mfma_f32_16x16x32_bf16 v[168:171], v[216:219], a[8:11], v[180:183]\n"
"s_waitcnt lgkmcnt(1)\n"
"v_exp_f32 v167, v167\n"
"v_mfma_f32_16x16x32_bf16 v[168:171], v[220:223], a[12:15], v[168:171]\n"
"v_mul_f32_dpp v12, v246, v12 row_newbcast:0 row_mask:0xf bank_mask:0xf\n"
"v_mul_f32_dpp v46, v247, v46 row_newbcast:2 row_mask:0xf bank_mask:0xf\n"
"v_mfma_f32_16x16x32_bf16 v[168:171], v[208:211], a[0:3], v[168:171]\n"
"s_waitcnt lgkmcnt(0)\n"
"ds_read_b64_tr_b16 a[244:245], v232 offset:49152\n"
"v_cvt_pk_bf16_f32 v145, v162, v163\n"
"v_mfma_f32_16x16x32_bf16 v[168:171], v[212:215], a[4:7], v[168:171]\n"
"v_exp_f32 v159, v159\n"
"v_mfma_f32_16x16x32_bf16 v[172:175], v[208:211], a[16:19], v[184:187]\n"
"ds_read_b64_tr_b16 a[252:253], v233 offset:49152\n"
"v_mul_f32_dpp v72, v206, v72 row_newbcast:0 row_mask:0xf bank_mask:0xf\n"
"v_mfma_f32_16x16x32_bf16 v[172:175], v[212:215], a[20:23], v[172:175]\n"
"v_exp_f32 v164, v164\n"
"v_mfma_f32_16x16x32_bf16 v[172:175], v[216:219], a[24:27], v[172:175]\n"
"ds_read_b64_tr_b16 a[250:251], v236 offset:53248\n"
"v_mul_f32_dpp v14, v246, v14 row_newbcast:2 row_mask:0xf bank_mask:0xf\n"
"v_mfma_f32_16x16x32_bf16 v[172:175], v[220:223], a[28:31], v[172:175]\n"
"ds_write_b128 v244, a[220:223] offset:33792\n"
"v_mul_f32_dpp v78, v206, v78 row_newbcast:2 row_mask:0xf bank_mask:0xf\n"
"v_exp_f32 v157, v157\n"
"v_mul_f32_dpp v89, v206, v89 row_newbcast:1 row_mask:0xf bank_mask:0xf\n"
"v_exp_f32 v168, v168\n"
"v_mfma_f32_16x16x32_bf16 v[176:179], v[216:219], a[40:43], v[188:191]\n"
"v_mul_f32_dpp v10, v246, v10 row_newbcast:2 row_mask:0xf bank_mask:0xf\n"
"v_mul_f32_dpp v40, v247, v40 row_newbcast:0 row_mask:0xf bank_mask:0xf\n"
"v_mfma_f32_16x16x32_bf16 v[176:179], v[220:223], a[44:47], v[176:179]\n"
"v_cvt_pk_bf16_f32 v137, v158, v159\n"
"v_mul_f32_dpp v28, v246, v28 row_newbcast:0 row_mask:0xf bank_mask:0xf\n"
"v_mfma_f32_16x16x32_bf16 v[176:179], v[208:211], a[32:35], v[176:179]\n"
"s_add_u32 s40, s56, s52\n"
"v_mul_f32_dpp v73, v206, v73 row_newbcast:1 row_mask:0xf bank_mask:0xf\n"
"v_cvt_pk_bf16_f32 v136, v156, v157\n"
"v_mfma_f32_16x16x32_bf16 v[176:179], v[212:215], a[36:39], v[176:179]\n"
"buffer_load_dwordx4 v207, s[16:19], s40 offen offset:1024 nt lds\n"
"v_mfma_f32_16x16x32_bf16 v[152:155], v[208:211], a[192:195], v[192:195]\n"
"v_mul_f32_dpp v76, v206, v76 row_newbcast:0 row_mask:0xf bank_mask:0xf\n"
"v_mul_f32_dpp v88, v206, v88 row_newbcast:0 row_mask:0xf bank_mask:0xf\n"
"v_mfma_f32_16x16x32_bf16 v[152:155], v[212:215], a[196:199], v[152:155]\n"
"ds_read_b128 v[208:211], v230 offset:0\n"
"v_cvt_pk_bf16_f32 v144, v160, v161\n"
"v_mfma_f32_16x16x32_bf16 v[152:155], v[216:219], a[200:203], v[152:155]\n"
"v_mul_f32_dpp v93, v206, v93 row_newbcast:1 row_mask:0xf bank_mask:0xf\n"
"v_cvt_pk_bf16_f32 v88, v88, v89\n"
"v_mfma_f32_16x16x32_bf16 v[152:155], v[220:223], a[204:207], v[152:155]\n"
"s_waitcnt lgkmcnt(1)\n"
"ds_read_b128 v[212:215], v231 offset:0\n"
"v_mul_f32_dpp v13, v246, v13 row_newbcast:1 row_mask:0xf bank_mask:0xf\n"
"v_mfma_f32_16x16x32_bf16 a[208:211], v[120:123], a[244:247], a[208:211]\n"
"v_exp_f32 v170, v170\n"
"v_mfma_f32_16x16x32_bf16 v[0:3], v[96:99], a[244:247], v[0:3]\n"
"ds_read_b128 v[216:219], v230 offset:512\n"
"v_cvt_pk_bf16_f32 v124, v164, v165\n"
"v_mfma_f32_16x16x32_bf16 v[0:3], v[100:103], a[248:251], v[0:3]\n"
"ds_read_b64_tr_b16 v[226:227], v237 offset:53248\n"
"v_mul_f32_dpp v15, v246, v15 row_newbcast:3 row_mask:0xf bank_mask:0xf\n"
"v_mul_f32_dpp v41, v247, v41 row_newbcast:1 row_mask:0xf bank_mask:0xf\n"
"v_mul_f32_dpp v42, v247, v42 row_newbcast:2 row_mask:0xf bank_mask:0xf\n"
"v_exp_f32 v166, v166\n"
"v_mul_f32_dpp v57, v247, v57 row_newbcast:1 row_mask:0xf bank_mask:0xf\n"
"v_mul_f32_dpp v91, v206, v91 row_newbcast:3 row_mask:0xf bank_mask:0xf\n"
"v_exp_f32 v172, v172\n"
"v_exp_f32 v177, v177\n"
"v_mfma_f32_16x16x32_bf16 v[32:35], v[104:107], a[244:247], v[32:35]\n"
"ds_read_b64_tr_b16 v[224:225], v237 offset:49152\n"
"ds_read_b128 v[220:223], v231 offset:512\n"
"v_mfma_f32_16x16x32_bf16 v[32:35], v[108:111], a[248:251], v[32:35]\n"
"ds_write_b128 v244, a[232:235] offset:34816\n"
"v_cvt_pk_bf16_f32 v8, v8, v9\n"
"v_mul_f32_dpp v75, v206, v75 row_newbcast:3 row_mask:0xf bank_mask:0xf\n"
"v_mul_f32_dpp v30, v246, v30 row_newbcast:2 row_mask:0xf bank_mask:0xf\n"
"v_mul_f32_dpp v94, v206, v94 row_newbcast:2 row_mask:0xf bank_mask:0xf\n"
"v_exp_f32 v176, v176\n"
"v_mfma_f32_16x16x32_bf16 v[64:67], v[112:115], a[244:247], v[64:67]\n"
"v_exp_f32 v173, v173\n"
"v_mfma_f32_16x16x32_bf16 v[64:67], v[116:119], a[248:251], v[64:67]\n"
"s_waitcnt lgkmcnt(6)\n"
"ds_write_b128 v244, a[236:239] offset:35840\n"
"v_mul_f32_dpp v77, v206, v77 row_newbcast:1 row_mask:0xf bank_mask:0xf\n"
"v_mul_f32_dpp v62, v247, v62 row_newbcast:2 row_mask:0xf bank_mask:0xf\n"
"v_exp_f32 v169, v169\n"
"v_cvt_pk_bf16_f32 v138, v172, v173\n"
"v_exp_f32 v153, v153\n"
"v_mfma_f32_16x16x32_bf16 v[156:159], v[208:211], a[0:3], v[180:183]\n"
"s_waitcnt lgkmcnt(6)\n"
"ds_read_b64_tr_b16 a[254:255], v233 offset:53248\n"
"v_mul_f32_dpp v63, v247, v63 row_newbcast:3 row_mask:0xf bank_mask:0xf\n"
"v_mfma_f32_16x16x32_bf16 v[156:159], v[212:215], a[4:7], v[156:159]\n"
"s_waitcnt lgkmcnt(6)\n"
"v_exp_f32 v174, v174\n"
"v_mfma_f32_16x16x32_bf16 v[156:159], v[216:219], a[8:11], v[156:159]\n"
"s_waitcnt lgkmcnt(3)\n"
"s_add_u32 s40, s56, s53\n"
"v_cvt_pk_bf16_f32 v40, v40, v41\n"
"v_mul_f32_dpp v0, v246, v0 row_newbcast:0 row_mask:0xf bank_mask:0xf\n"
"v_mfma_f32_16x16x32_bf16 v[156:159], v[220:223], a[12:15], v[156:159]\n"
"buffer_load_dwordx4 v240, s[16:19], s40 offen offset:2048 nt lds\n"
"v_cvt_pk_bf16_f32 v9, v10, v11\n"
"v_mul_f32_dpp v44, v247, v44 row_newbcast:0 row_mask:0xf bank_mask:0xf\n"
"v_cvt_pk_bf16_f32 v125, v166, v167\n"
"v_mul_f32_dpp v31, v246, v31 row_newbcast:3 row_mask:0xf bank_mask:0xf\n"
"v_exp_f32 v152, v152\n"
"v_mul_f32_dpp v34, v247, v34 row_newbcast:2 row_mask:0xf bank_mask:0xf\n"
"v_mov_b64 v[200:201], 0\n"
"v_mfma_f32_16x16x32_bf16 v[160:163], v[216:219], a[24:27], v[184:187]\n"
"v_mul_f32_dpp v60, v247, v60 row_newbcast:0 row_mask:0xf bank_mask:0xf\n"
"v_xor_b32 v244, 0x1000, v244\n"
"v_mfma_f32_16x16x32_bf16 v[160:163], v[220:223], a[28:31], v[160:163]\n"
"v_exp_f32 v178, v178\n"
"v_mfma_f32_16x16x32_bf16 v[160:163], v[208:211], a[16:19], v[160:163]\n"
"ds_read_b64_tr_b16 a[250:251], v236 offset:53760\n"
"v_mul_f32_dpp v33, v247, v33 row_newbcast:1 row_mask:0xf bank_mask:0xf\n"
"v_mfma_f32_16x16x32_bf16 v[160:163], v[212:215], a[20:23], v[160:163]\n"
"ds_read_b64_tr_b16 a[244:245], v232 offset:49664\n"
"v_mul_f32_dpp v43, v247, v43 row_newbcast:3 row_mask:0xf bank_mask:0xf\n"
"v_mfma_f32_16x16x32_bf16 v[164:167], v[208:211], a[32:35], v[188:191]\n"
"v_mul_f32_dpp v45, v247, v45 row_newbcast:1 row_mask:0xf bank_mask:0xf\n"
"v_cvt_pk_bf16_f32 v72, v72, v73\n"
"v_mfma_f32_16x16x32_bf16 v[164:167], v[212:215], a[36:39], v[164:167]\n"
"ds_read_b128 v[208:211], v230 offset:4096\n"
"v_mul_f32_dpp v74, v206, v74 row_newbcast:2 row_mask:0xf bank_mask:0xf\n"
"v_mfma_f32_16x16x32_bf16 v[164:167], v[216:219], a[40:43], v[164:167]\n"
"v_mul_f32_dpp v56, v247, v56 row_newbcast:0 row_mask:0xf bank_mask:0xf\n"
"v_mul_f32_dpp v58, v247, v58 row_newbcast:2 row_mask:0xf bank_mask:0xf\n"
"v_mfma_f32_16x16x32_bf16 v[164:167], v[220:223], a[44:47], v[164:167]\n"
"ds_read_b128 v[212:215], v231 offset:4096\n"
"ds_read_b128 v[220:223], v231 offset:4608\n"
"v_mul_f32_dpp v47, v247, v47 row_newbcast:3 row_mask:0xf bank_mask:0xf\n"
"v_cvt_pk_bf16_f32 v73, v74, v75\n"
"v_mul_f32_dpp v29, v246, v29 row_newbcast:1 row_mask:0xf bank_mask:0xf\n"
"v_exp_f32 v175, v175\n"
"v_exp_f32 v179, v179\n"
"v_mul_f32_dpp v1, v246, v1 row_newbcast:1 row_mask:0xf bank_mask:0xf\n"
"v_mul_f32_dpp v2, v246, v2 row_newbcast:2 row_mask:0xf bank_mask:0xf\n"
"v_mfma_f32_16x16x32_bf16 v[68:71], v[116:119], v[224:227], v[68:71]\n"
"s_waitcnt lgkmcnt(5)\n"
"v_exp_f32 v171, v171\n"
"v_mfma_f32_16x16x32_bf16 v[68:71], v[112:115], a[252:255], v[68:71]\n"
"ds_read_b128 v[216:219], v230 offset:4608\n"
"v_mul_f32_dpp v27, v246, v27 row_newbcast:3 row_mask:0xf bank_mask:0xf\n"
"v_mfma_f32_16x16x32_bf16 v[36:39], v[108:111], v[224:227], v[36:39]\n"
"s_add_u32 s40, s56, s54\n"
"v_cvt_pk_bf16_f32 v24, v24, v25\n"
"v_mul_f32_dpp v59, v247, v59 row_newbcast:3 row_mask:0xf bank_mask:0xf\n"
"v_mfma_f32_16x16x32_bf16 v[36:39], v[104:107], a[252:255], v[36:39]\n"
"buffer_load_dwordx4 v207, s[16:19], s40 offen offset:3072 nt lds\n"
"v_cvt_pk_bf16_f32 v74, v76, v77\n"
"v_cvt_pk_bf16_f32 v25, v26, v27\n"
"v_cvt_pk_bf16_f32 v130, v168, v169\n"
"v_exp_f32 v154, v154\n"
"v_exp_f32 v162, v162\n"
"v_mul_f32_dpp v3, v246, v3 row_newbcast:3 row_mask:0xf bank_mask:0xf\n"
"v_mfma_f32_16x16x32_bf16 v[4:7], v[100:103], v[224:227], v[4:7]\n"
"v_cvt_pk_bf16_f32 v10, v12, v13\n"
"v_cvt_pk_bf16_f32 v0, v0, v1\n"
"v_mfma_f32_16x16x32_bf16 v[4:7], v[96:99], a[252:255], v[4:7]\n"
"v_mul_f32_dpp v90, v206, v90 row_newbcast:2 row_mask:0xf bank_mask:0xf\n"
"v_mul_f32_dpp v92, v206, v92 row_newbcast:0 row_mask:0xf bank_mask:0xf\n"
"v_mfma_f32_16x16x32_bf16 a[212:215], v[120:123], a[252:255], a[212:215]\n"
"s_waitcnt lgkmcnt(3)\n"
"ds_read_b64_tr_b16 a[246:247], v232 offset:53760\n"
"ds_read_b64_tr_b16 v[224:225], v237 offset:49664\n"
"v_cvt_pk_bf16_f32 v11, v14, v15\n"
"v_cvt_pk_bf16_f32 v41, v42, v43\n"
"v_cvt_pk_bf16_f32 v43, v46, v47\n"
"v_cvt_pk_bf16_f32 v56, v56, v57\n"
"v_cvt_pk_bf16_f32 v131, v170, v171\n"
"v_cvt_pk_bf16_f32 v146, v176, v177\n"
"v_exp_f32 v167, v167\n"
"v_mfma_f32_16x16x32_bf16 v[168:171], v[208:211], a[0:3], v[180:183]\n"
"s_waitcnt lgkmcnt(3)\n"
"v_exp_f32 v159, v159\n"
"v_mfma_f32_16x16x32_bf16 v[168:171], v[212:215], a[4:7], v[168:171]\n"
"s_waitcnt lgkmcnt(2)\n"
"ds_read_b64_tr_b16 a[248:249], v236 offset:49664\n"
"ds_read_b64_tr_b16 a[254:255], v233 offset:53760\n"
"v_mfma_f32_16x16x32_bf16 v[168:171], v[216:219], a[8:11], v[168:171]\n"
"v_cvt_pk_bf16_f32 v89, v90, v91\n"
"v_cvt_pk_bf16_f32 v139, v174, v175\n"
"v_mfma_f32_16x16x32_bf16 v[168:171], v[220:223], a[12:15], v[168:171]\n"
"ds_write_b128 v248, v[8:11] offset:49152\n"
"v_mul_f32_dpp v79, v206, v79 row_newbcast:3 row_mask:0xf bank_mask:0xf\n"
"v_cvt_pk_bf16_f32 v147, v178, v179\n"
"v_exp_f32 v165, v165\n"
"v_mul_f32_dpp v6, v246, v6 row_newbcast:2 row_mask:0xf bank_mask:0xf\n"
"v_cvt_pk_bf16_f32 v1, v2, v3\n"
"v_mfma_f32_16x16x32_bf16 v[172:175], v[216:219], a[24:27], v[184:187]\n"
"ds_read_b64_tr_b16 v[226:227], v237 offset:53760\n"
"v_mul_f32_dpp v32, v247, v32 row_newbcast:0 row_mask:0xf bank_mask:0xf\n"
"v_mfma_f32_16x16x32_bf16 v[172:175], v[220:223], a[28:31], v[172:175]\n"
"v_mul_f32_dpp v39, v247, v39 row_newbcast:3 row_mask:0xf bank_mask:0xf\n"
"v_mul_f32_dpp v68, v206, v68 row_newbcast:0 row_mask:0xf bank_mask:0xf\n"
"v_mfma_f32_16x16x32_bf16 v[172:175], v[208:211], a[16:19], v[172:175]\n"
"ds_read_b64_tr_b16 a[252:253], v233 offset:49664\n"
"v_cvt_pk_bf16_f32 v75, v78, v79\n"
"v_mfma_f32_16x16x32_bf16 v[172:175], v[212:215], a[20:23], v[172:175]\n"
"s_cbranch_vccnz LPCT0_%=\nLPKT0_%=:\n"
"ds_read_b64_tr_b16 a[54:55], v249 offset:49160\n"
"v_mfma_f32_16x16x32_bf16 v[176:179], v[208:211], a[32:35], v[188:191]\n"
"v_cvt_pk_bf16_f32 v42, v44, v45\n"
"v_mul_f32_dpp v61, v247, v61 row_newbcast:1 row_mask:0xf bank_mask:0xf\n"
"v_mfma_f32_16x16x32_bf16 v[176:179], v[212:215], a[36:39], v[176:179]\n"
"ds_read_b64_tr_b16 a[52:53], v249 offset:49152\n"
"v_cvt_pk_bf16_f32 v57, v58, v59\n"
"v_mfma_f32_16x16x32_bf16 v[176:179], v[216:219], a[40:43], v[176:179]\n"
"v_exp_f32 v155, v155\n"
"v_mfma_f32_16x16x32_bf16 v[176:179], v[220:223], a[44:47], v[176:179]\n"
"s_waitcnt lgkmcnt(4)\n"
"ds_write_b128 v248, v[40:43] offset:50176\n"
"v_cvt_pk_bf16_f32 v27, v30, v31\n"
"v_cvt_pk_bf16_f32 v90, v92, v93\n"
"v_cvt_pk_bf16_f32 v126, v152, v153\n"
"v_mul_f32_dpp v7, v246, v7 row_newbcast:3 row_mask:0xf bank_mask:0xf\n"
"v_mul_f32_dpp v65, v206, v65 row_newbcast:1 row_mask:0xf bank_mask:0xf\n"
"v_mul_f32_dpp v67, v206, v67 row_newbcast:3 row_mask:0xf bank_mask:0xf\n"
"v_mul_f32_dpp v70, v206, v70 row_newbcast:2 row_mask:0xf bank_mask:0xf\n"
"v_mul_f32_dpp v71, v206, v71 row_newbcast:3 row_mask:0xf bank_mask:0xf\n"
"v_mfma_f32_16x16x32_bf16 a[224:227], v[120:123], a[244:247], a[224:227]\n"
"ds_write_b128 v248, v[72:75] offset:51200\n"
"v_cvt_pk_bf16_f32 v26, v28, v29\n"
"v_exp_f32 v166, v166\n"
"v_cvt_pk_bf16_f32 v3, v6, v7\n"
"v_mul_f32_dpp v66, v206, v66 row_newbcast:2 row_mask:0xf bank_mask:0xf\n"
"v_mfma_f32_16x16x32_bf16 v[16:19], v[96:99], a[244:247], v[16:19]\n"
"s_waitcnt vmcnt(8) lgkmcnt(4)\n"
"ds_read_b64_tr_b16 a[68:69], v249 offset:50176\n"
"v_mul_f32_dpp v69, v206, v69 row_newbcast:1 row_mask:0xf bank_mask:0xf\n"
"v_mfma_f32_16x16x32_bf16 v[16:19], v[100:103], a[248:251], v[16:19]\n"
"s_barrier\n"
"ds_read_b64_tr_b16 a[70:71], v249 offset:50184\n"
"ds_read_b128 v[212:215], v229 offset:16384\n"
"ds_read_b64 v[250:251], v245 offset:52224\n"
"v_mul_f32_dpp v5, v246, v5 row_newbcast:1 row_mask:0xf bank_mask:0xf\n"
"v_mul_f32_dpp v35, v247, v35 row_newbcast:3 row_mask:0xf bank_mask:0xf\n"
"v_exp_f32 v173, v173\n"
"v_exp_f32 v175, v175\n"
"v_mfma_f32_16x16x32_bf16 v[48:51], v[104:107], a[244:247], v[48:51]\n"
"v_cvt_pk_bf16_f32 v59, v62, v63\n"
"v_mul_f32_dpp v38, v247, v38 row_newbcast:2 row_mask:0xf bank_mask:0xf\n"
"v_mfma_f32_16x16x32_bf16 v[48:51], v[108:111], a[248:251], v[48:51]\n"
"ds_read_b128 v[208:211], v228 offset:16384\n"
"ds_read_b128 v[220:223], v229 offset:16896\n"
"v_mul_f32_dpp v95, v206, v95 row_newbcast:3 row_mask:0xf bank_mask:0xf\n"
"v_exp_f32 v161, v161\n"
"v_cvt_pk_bf16_f32 v149, v166, v167\n"
"v_cvt_pk_bf16_f32 v32, v32, v33\n"
"v_exp_f32 v170, v170\n"
"v_exp_f32 v172, v172\n"
"v_mfma_f32_16x16x32_bf16 v[80:83], v[112:115], a[244:247], v[80:83]\n"
"ds_read_b64_tr_b16 a[84:85], v249 offset:51200\n"
"ds_read_b64_tr_b16 a[86:87], v249 offset:51208\n"
"v_mfma_f32_16x16x32_bf16 v[80:83], v[116:119], a[248:251], v[80:83]\n"
"ds_read_b128 v[28:31], v244 offset:33792\n"
"v_mov_b64 v[198:199], 0\n"
"v_mfma_f32_16x16x32_bf16 v[84:87], v[116:119], v[224:227], v[84:87]\n"
"ds_write_b128 v248, v[24:27] offset:49152\n"
"v_cvt_pk_bf16_f32 v91, v94, v95\n"
"v_exp_f32 v156, v156\n"
"v_exp_f32 v157, v157\n"
"v_mul_f32_dpp v37, v247, v37 row_newbcast:1 row_mask:0xf bank_mask:0xf\n"
"v_mfma_f32_16x16x32_bf16 v[84:87], v[112:115], a[252:255], v[84:87]\n"
"ds_read_b64_tr_b16 a[60:61], v249 offset:49152\n"
"ds_read_b128 v[216:219], v228 offset:16896\n"
"v_cvt_pk_bf16_f32 v127, v154, v155\n"
"v_exp_f32 v160, v160\n"
"v_exp_f32 v164, v164\n"
"v_mul_f32_dpp v36, v247, v36 row_newbcast:0 row_mask:0xf bank_mask:0xf\n"
"v_exp_f32 v176, v176\n"
"v_mfma_f32_16x16x32_bf16 v[52:55], v[108:111], v[224:227], v[52:55]\n"
"ds_write_b128 v248, v[88:91] offset:51200\n"
"v_cvt_pk_bf16_f32 v58, v60, v61\n"
"v_exp_f32 v163, v163\n"
"v_cvt_pk_bf16_f32 v33, v34, v35\n"
"v_exp_f32 v169, v169\n"
"v_mfma_f32_16x16x32_bf16 v[52:55], v[104:107], a[252:255], v[52:55]\n"
"s_waitcnt lgkmcnt(9)\n"
"ds_write_b128 v248, v[56:59] offset:50176\n"
"v_mul_f32_dpp v4, v246, v4 row_newbcast:0 row_mask:0xf bank_mask:0xf\n"
"v_exp_f32 v168, v168\n"
"v_min_f32 v254, v252, v250\n"
"v_sub_f32 v252, v254, v252\n"
"v_mfma_f32_16x16x32_bf16 v[20:23], v[100:103], v[224:227], v[20:23]\n"
"ds_read_b64_tr_b16 a[92:93], v249 offset:51200\n"
"ds_read_b64_tr_b16 a[244:245], v234 offset:0\n"
"v_mfma_f32_16x16x32_bf16 v[20:23], v[96:99], a[252:255], v[20:23]\n"
"ds_read_b64_tr_b16 a[62:63], v249 offset:49160\n"
"ds_read_b64_tr_b16 a[78:79], v249 offset:50184\n"
"ds_read_b64_tr_b16 a[94:95], v249 offset:51208\n"
"v_cvt_pk_bf16_f32 v2, v4, v5\n"
"v_mul_f32_dpp v64, v206, v64 row_newbcast:0 row_mask:0xf bank_mask:0xf\n"
"v_cvt_pk_bf16_f32 v64, v64, v65\n"
"v_cvt_pk_bf16_f32 v65, v66, v67\n"
"v_cvt_pk_bf16_f32 v66, v68, v69\n"
"v_cvt_pk_bf16_f32 v67, v70, v71\n"
"v_exp_f32 v177, v177\n"
"v_mfma_f32_16x16x32_bf16 a[228:231], v[120:123], a[252:255], a[228:231]\n"
"s_waitcnt lgkmcnt(11)\n"
"ds_write_b128 v248, v[64:67] offset:51200\n"
"v_cvt_pk_bf16_f32 v132, v156, v157\n"
"v_cvt_pk_bf16_f32 v34, v36, v37\n"
"v_cvt_pk_bf16_f32 v35, v38, v39\n"
"v_exp_f32 v171, v171\n"
"v_exp_f32 v179, v179\n"
"v_sub_f32 v250, v254, v250\n"
"v_mul_f32_dpp v83, v206, v83 row_newbcast:3 row_mask:0xf bank_mask:0xf\n"
"v_mfma_f32_16x16x32_bf16 v[152:155], v[208:211], a[0:3], v[180:183]\n"
"ds_read_b64_tr_b16 a[76:77], v249 offset:50176\n"
"ds_read_b64_tr_b16 a[80:81], v249 offset:51200\n"
"v_mfma_f32_16x16x32_bf16 v[152:155], v[212:215], a[4:7], v[152:155]\n"
"s_waitcnt lgkmcnt(10)\n"
"ds_write_b128 v248, v[0:3] offset:49152\n"
"v_exp_f32 v158, v158\n"
"v_cvt_pk_bf16_f32 v140, v160, v161\n"
"v_cvt_pk_bf16_f32 v142, v172, v173\n"
"v_mul_f32_dpp v18, v246, v18 row_newbcast:2 row_mask:0xf bank_mask:0xf\n"
"v_mfma_f32_16x16x32_bf16 v[152:155], v[216:219], a[8:11], v[152:155]\n"
"ds_write_b128 v248, v[32:35] offset:50176\n"
"v_cvt_pk_bf16_f32 v141, v162, v163\n"
"v_exp_f32 v178, v178\n"
"v_exp_f32 v250, v250\n"
"v_mfma_f32_16x16x32_bf16 v[152:155], v[220:223], a[12:15], v[152:155]\n"
"ds_read_b64_tr_b16 a[50:51], v249 offset:49160\n"
"ds_read_b64_tr_b16 a[64:65], v249 offset:50176\n"
"ds_read_b128 v[24:27], v244 offset:32768\n"
"v_cvt_pk_bf16_f32 v133, v158, v159\n"
"v_cvt_pk_bf16_f32 v148, v164, v165\n"
"v_cvt_pk_bf16_f32 v150, v176, v177\n"
"v_exp_f32 v252, v252\n"
"v_mul_f32_dpp v19, v246, v19 row_newbcast:3 row_mask:0xf bank_mask:0xf\n"
"v_mul_f32_dpp v22, v246, v22 row_newbcast:2 row_mask:0xf bank_mask:0xf\n"
"v_mfma_f32_16x16x32_bf16 v[156:159], v[216:219], a[24:27], v[184:187]\n"
"ds_read_b64_tr_b16 a[66:67], v249 offset:50184\n"
"ds_read_b64_tr_b16 a[248:249], v238 offset:0\n"
"v_mfma_f32_16x16x32_bf16 v[156:159], v[220:223], a[28:31], v[156:159]\n"
"ds_read_b64_tr_b16 a[48:49], v249 offset:49152\n"
"ds_read_b128 v[60:63], v244 offset:35840\n"
"v_mfma_f32_16x16x32_bf16 v[156:159], v[208:211], a[16:19], v[156:159]\n"
"ds_read_b64_tr_b16 a[82:83], v249 offset:51208\n"
"ds_read_b128 v[56:59], v244 offset:34816\n"
"v_mfma_f32_16x16x32_bf16 v[156:159], v[212:215], a[20:23], v[156:159]\n"
"ds_read_b64_tr_b16 a[246:247], v234 offset:4096\n"
"ds_read_b64_tr_b16 v[226:227], v239 offset:4096\n"
"v_mfma_f32_16x16x32_bf16 v[160:163], v[208:211], a[32:35], v[188:191]\n"
"v_exp_f32 v174, v174\n"
"v_mfma_f32_16x16x32_bf16 v[160:163], v[212:215], a[36:39], v[160:163]\n"
"ds_read_b64_tr_b16 a[250:251], v238 offset:4096\n"
"v_mul_f32_dpp v49, v247, v49 row_newbcast:1 row_mask:0xf bank_mask:0xf\n"
"v_mfma_f32_16x16x32_bf16 v[160:163], v[216:219], a[40:43], v[160:163]\n"
"ds_read_b64_tr_b16 v[224:225], v239 offset:0\n"
"v_mov_b64 v[202:203], 0\n"
"v_mfma_f32_16x16x32_bf16 v[160:163], v[220:223], a[44:47], v[160:163]\n"
"v_exp_f32 v154, v154\n"
"v_mfma_f32_16x16x32_bf16 v[164:167], v[216:219], a[200:203], v[192:195]\n"
"v_cvt_pk_bf16_f32 v151, v178, v179\n"
"v_mul_f32 v253, v253, v252\n"
"v_mfma_f32_16x16x32_bf16 v[164:167], v[220:223], a[204:207], v[164:167]\n"
"v_cvt_pk_bf16_f32 v134, v168, v169\n"
"v_fmac_f32 v253, v251, v250\n"
"v_mfma_f32_16x16x32_bf16 v[164:167], v[208:211], a[192:195], v[164:167]\n"
"ds_read_b64_tr_b16 a[252:253], v235 offset:0\n"
"ds_read_b64_tr_b16 a[254:255], v235 offset:4096\n"
"v_mfma_f32_16x16x32_bf16 v[164:167], v[212:215], a[196:199], v[164:167]\n"
"s_waitcnt lgkmcnt(4)\n"
"v_cvt_pk_bf16_f32 v135, v170, v171\n"
"v_mfma_f32_16x16x32_bf16 a[216:219], v[124:127], a[244:247], 0\n"
"ds_read_b128 v[216:219], v228 offset:20992\n"
"v_cvt_pk_bf16_f32 v143, v174, v175\n"
"v_mfma_f32_16x16x32_bf16 v[8:11], v[128:131], a[244:247], 0\n"
"s_waitcnt lgkmcnt(4)\n"
"s_add_u32 m0, s44, 98304\n"
"v_rcp_f32 v253, v253\n"
"v_mfma_f32_16x16x32_bf16 v[8:11], v[132:135], a[248:251], v[8:11]\n"
"buffer_load_dwordx4 v240, s[20:23], s56 offen nt lds\n"
"v_exp_f32 v152, v152\n"
"v_exp_f32 v155, v155\n"
"v_exp_f32 v158, v158\n"
"v_exp_f32 v167, v167\n"
"v_mfma_f32_16x16x32_bf16 v[40:43], v[136:139], a[244:247], 0\n"
"v_accvgpr_read_b32 v254, a208\n"
"v_mul_f32_dpp v85, v206, v85 row_newbcast:1 row_mask:0xf bank_mask:0xf\n"
"v_mfma_f32_16x16x32_bf16 v[40:43], v[140:143], a[248:251], v[40:43]\n"
"v_mul_f32 v250, v250, v253\n"
"v_accvgpr_read_b32 v251, a211\n"
"v_mfma_f32_16x16x32_bf16 v[72:75], v[144:147], a[244:247], 0\n"
"ds_read_b128 v[220:223], v229 offset:20992\n"
"v_mul_f32_dpp v17, v246, v17 row_newbcast:1 row_mask:0xf bank_mask:0xf\n"
"v_mfma_f32_16x16x32_bf16 v[72:75], v[148:151], a[248:251], v[72:75]\n"
"s_waitcnt lgkmcnt(2)\n"
"v_exp_f32 v164, v164\n"
"v_mfma_f32_16x16x32_bf16 v[76:79], v[148:151], v[224:227], 0\n"
"v_exp_f32 v153, v153\n"
"v_mfma_f32_16x16x32_bf16 v[76:79], v[144:147], a[252:255], v[76:79]\n"
"v_mul_f32 v252, v252, v253\n"
"v_mov_b32_dpp v250, v250 row_ror:12 row_mask:0x2 bank_mask:0xf\n"
"v_mfma_f32_16x16x32_bf16 v[44:47], v[140:143], v[224:227], 0\n"
"ds_read_b64_tr_b16 a[244:245], v234 offset:512\n"
"v_mov_b32_dpp v252, v252 row_ror:12 row_mask:0x2 bank_mask:0xf\n"
"v_mfma_f32_16x16x32_bf16 v[44:47], v[136:139], a[252:255], v[44:47]\n"
"ds_read_b128 v[212:215], v229 offset:20480\n"
"v_mov_b32_dpp v250, v250 row_ror:8 row_mask:0x4 bank_mask:0xf\n"
"v_mfma_f32_16x16x32_bf16 v[12:15], v[132:135], v[224:227], 0\n"
"s_add_u32 s40, s56, s52\n"
"ds_read_b128 v[208:211], v228 offset:20480\n"
"v_cvt_pk_bf16_f32 v96, v152, v153\n"
"v_mfma_f32_16x16x32_bf16 v[12:15], v[128:131], a[252:255], v[12:15]\n"
"buffer_load_dwordx4 v207, s[20:23], s40 offen offset:1024 nt lds\n"
"v_mov_b32_dpp v252, v252 row_ror:8 row_mask:0x4 bank_mask:0xf\n"
"v_mov_b32_dpp v250, v250 row_ror:4 row_mask:0x8 bank_mask:0xf\n"
"v_accvgpr_read_b32 v255, a209\n"
"v_accvgpr_read_b32 v253, a210\n"
"v_mov_b64 v[196:197], 0\n"
"v_cvt_pk_bf16_f32 v97, v154, v155\n"
"v_exp_f32 v157, v157\n"
"v_mfma_f32_16x16x32_bf16 a[220:223], v[124:127], a[252:255], 0\n"
"v_mov_b32_dpp v252, v252 row_ror:4 row_mask:0x8 bank_mask:0xf\n"
"v_mul_f32_dpp v24, v250, v24 row_newbcast:0 row_mask:0xf bank_mask:0xf\n"
"v_mul_f32_dpp v25, v250, v25 row_newbcast:1 row_mask:0xf bank_mask:0xf\n"
"v_mul_f32_dpp v26, v250, v26 row_newbcast:2 row_mask:0xf bank_mask:0xf\n"
"v_mul_f32_dpp v29, v250, v29 row_newbcast:1 row_mask:0xf bank_mask:0xf\n"
"v_mul_f32_dpp v54, v247, v54 row_newbcast:2 row_mask:0xf bank_mask:0xf\n"
"v_exp_f32 v162, v162\n"
"v_exp_f32 v163, v163\n"
"v_smfmac_f32_16x16x64_bf16 v[196:199], a[240:243], v[128:135], v241\n"
"v_mul_f32_dpp v27, v250, v27 row_newbcast:3 row_mask:0xf bank_mask:0xf\n"
"v_fmac_f32_dpp v24, v252, v254 row_newbcast:0 row_mask:0xf bank_mask:0xf\n"
"v_mfma_f32_16x16x32_bf16 v[202:205], a[240:243], v[124:127], v[202:205]\n"
"s_waitcnt lgkmcnt(3)\n"
"ds_read_b64_tr_b16 a[252:253], v235 offset:512\n"
"ds_read_b64_tr_b16 a[254:255], v235 offset:4608\n"
"ds_read_b64_tr_b16 v[224:225], v239 offset:512\n"
"v_fmac_f32_dpp v25, v252, v255 row_newbcast:1 row_mask:0xf bank_mask:0xf\n"
"v_fmac_f32_dpp v26, v252, v253 row_newbcast:2 row_mask:0xf bank_mask:0xf\n"
"v_mul_f32_dpp v28, v250, v28 row_newbcast:0 row_mask:0xf bank_mask:0xf\n"
"v_mul_f32_dpp v30, v250, v30 row_newbcast:2 row_mask:0xf bank_mask:0xf\n"
"v_mul_f32_dpp v50, v247, v50 row_newbcast:2 row_mask:0xf bank_mask:0xf\n"
"v_mul_f32_dpp v58, v250, v58 row_newbcast:2 row_mask:0xf bank_mask:0xf\n"
"v_exp_f32 v156, v156\n"
"v_mfma_f32_16x16x32_bf16 v[168:171], v[216:219], a[8:11], v[180:183]\n"
"v_accvgpr_read_b32 v254, a212\n"
"v_accvgpr_read_b32 v253, a214\n"
"v_mfma_f32_16x16x32_bf16 v[168:171], v[220:223], a[12:15], v[168:171]\n"
"s_waitcnt lgkmcnt(3)\n"
"v_mul_f32_dpp v48, v247, v48 row_newbcast:0 row_mask:0xf bank_mask:0xf\n"
"v_mul_f32_dpp v57, v250, v57 row_newbcast:1 row_mask:0xf bank_mask:0xf\n"
"v_mfma_f32_16x16x32_bf16 v[168:171], v[208:211], a[0:3], v[168:171]\n"
"ds_read_b64_tr_b16 a[250:251], v238 offset:4608\n"
"v_mul_f32_dpp v56, v250, v56 row_newbcast:0 row_mask:0xf bank_mask:0xf\n"
"v_mfma_f32_16x16x32_bf16 v[168:171], v[212:215], a[4:7], v[168:171]\n"
"ds_read_b64_tr_b16 a[248:249], v238 offset:512\n"
"ds_read_b64_tr_b16 v[226:227], v239 offset:4608\n"
"v_fmac_f32_dpp v27, v252, v251 row_newbcast:3 row_mask:0xf bank_mask:0xf\n"
"v_mul_f32_dpp v31, v250, v31 row_newbcast:3 row_mask:0xf bank_mask:0xf\n"
"v_fmac_f32_dpp v28, v252, v254 row_newbcast:0 row_mask:0xf bank_mask:0xf\n"
"v_fmac_f32_dpp v30, v252, v253 row_newbcast:2 row_mask:0xf bank_mask:0xf\n"
"v_cvt_pk_bf16_f32 v24, v24, v25\n"
"v_cvt_pk_bf16_f32 v25, v26, v27\n"
"v_mul_f32_dpp v52, v247, v52 row_newbcast:0 row_mask:0xf bank_mask:0xf\n"
"v_accvgpr_read_b32 v254, a224\n"
"v_mul_f32_dpp v59, v250, v59 row_newbcast:3 row_mask:0xf bank_mask:0xf\n"
"v_mul_f32_dpp v60, v250, v60 row_newbcast:0 row_mask:0xf bank_mask:0xf\n"
"v_mul_f32_dpp v62, v250, v62 row_newbcast:2 row_mask:0xf bank_mask:0xf\n"
"v_mul_f32_dpp v63, v250, v63 row_newbcast:3 row_mask:0xf bank_mask:0xf\n"
"v_exp_f32 v159, v159\n"
"v_exp_f32 v161, v161\n"
"v_mfma_f32_16x16x32_bf16 v[172:175], v[208:211], a[16:19], v[184:187]\n"
"v_accvgpr_read_b32 v255, a213\n"
"v_accvgpr_read_b32 v251, a215\n"
"v_mfma_f32_16x16x32_bf16 v[172:175], v[212:215], a[20:23], v[172:175]\n"
"ds_read_b64_tr_b16 a[246:247], v234 offset:4608\n"
"v_fmac_f32_dpp v29, v252, v255 row_newbcast:1 row_mask:0xf bank_mask:0xf\n"
"v_mfma_f32_16x16x32_bf16 v[172:175], v[216:219], a[24:27], v[172:175]\n"
"s_add_u32 s40, s56, s53\n"
"v_xor_b32 v249, s75, v249\n"
"v_accvgpr_read_b32 v255, a225\n"
"v_mfma_f32_16x16x32_bf16 v[172:175], v[220:223], a[28:31], v[172:175]\n"
"buffer_load_dwordx4 v240, s[20:23], s40 offen offset:2048 nt lds\n"
"v_fmac_f32_dpp v31, v252, v251 row_newbcast:3 row_mask:0xf bank_mask:0xf\n"
"v_cvt_pk_bf16_f32 v27, v30, v31\n"
"v_mul_f32_dpp v16, v246, v16 row_newbcast:0 row_mask:0xf bank_mask:0xf\n"
"v_mul_f32_dpp v51, v247, v51 row_newbcast:3 row_mask:0xf bank_mask:0xf\n"
"v_accvgpr_read_b32 v253, a226\n"
"v_accvgpr_read_b32 v251, a227\n"
"v_fmac_f32_dpp v56, v252, v254 row_newbcast:0 row_mask:0xf bank_mask:0xf\n"
"v_accvgpr_read_b32 v254, a228\n"
"v_mfma_f32_16x16x32_bf16 v[176:179], v[216:219], a[40:43], v[188:191]\n"
"v_cvt_pk_bf16_f32 v26, v28, v29\n"
"v_fmac_f32_dpp v57, v252, v255 row_newbcast:1 row_mask:0xf bank_mask:0xf\n"
"v_mfma_f32_16x16x32_bf16 v[176:179], v[220:223], a[44:47], v[176:179]\n"
"v_fmac_f32_dpp v58, v252, v253 row_newbcast:2 row_mask:0xf bank_mask:0xf\n"
"v_accvgpr_read_b32 v255, a229\n"
"v_mfma_f32_16x16x32_bf16 v[176:179], v[208:211], a[32:35], v[176:179]\n"
"v_cvt_pk_bf16_f32 v16, v16, v17\n"
"v_mul_f32_dpp v61, v250, v61 row_newbcast:1 row_mask:0xf bank_mask:0xf\n"
"v_mfma_f32_16x16x32_bf16 v[176:179], v[212:215], a[36:39], v[176:179]\n"
"ds_write_b128 v243, v[24:27] offset:0\n"
"v_cvt_pk_bf16_f32 v48, v48, v49\n"
"v_fmac_f32_dpp v59, v252, v251 row_newbcast:3 row_mask:0xf bank_mask:0xf\n"
"v_accvgpr_read_b32 v253, a230\n"
"v_accvgpr_read_b32 v251, a231\n"
"v_fmac_f32_dpp v60, v252, v254 row_newbcast:0 row_mask:0xf bank_mask:0xf\n"
"v_fmac_f32_dpp v61, v252, v255 row_newbcast:1 row_mask:0xf bank_mask:0xf\n"
"v_cvt_pk_bf16_f32 v56, v56, v57\n"
"v_cvt_pk_bf16_f32 v57, v58, v59\n"
"v_cvt_pk_bf16_f32 v58, v60, v61\n"
"v_mfma_f32_16x16x32_bf16 v[152:155], v[208:211], a[192:195], v[192:195]\n"
"v_mul_f32_dpp v81, v206, v81 row_newbcast:1 row_mask:0xf bank_mask:0xf\n"
"v_mul_f32_dpp v82, v206, v82 row_newbcast:2 row_mask:0xf bank_mask:0xf\n"
"v_mul_f32_dpp v87, v206, v87 row_newbcast:3 row_mask:0xf bank_mask:0xf\n"
"v_fmac_f32_dpp v62, v252, v253 row_newbcast:2 row_mask:0xf bank_mask:0xf\n"
"v_fmac_f32_dpp v63, v252, v251 row_newbcast:3 row_mask:0xf bank_mask:0xf\n"
"v_cvt_pk_bf16_f32 v59, v62, v63\n"
"ds_write_b128 v243, v[56:59] offset:2048\n"
"v_mfma_f32_16x16x32_bf16 v[152:155], v[212:215], a[196:199], v[152:155]\n"
"v_exp_f32 v174, v174\n"
"v_mfma_f32_16x16x32_bf16 v[152:155], v[216:219], a[200:203], v[152:155]\n"
"v_exp_f32 v166, v166\n"
"v_mfma_f32_16x16x32_bf16 v[152:155], v[220:223], a[204:207], v[152:155]\n"
"s_waitcnt lgkmcnt(2)\n"
"ds_read_b128 v[212:215], v231 offset:16384\n"
"v_cvt_pk_bf16_f32 v105, v158, v159\n"
"v_mfma_f32_16x16x32_bf16 a[232:235], v[124:127], a[244:247], 0\n"
"v_mul_f32_dpp v20, v246, v20 row_newbcast:0 row_mask:0xf bank_mask:0xf\n"
"v_mul_f32_dpp v23, v246, v23 row_newbcast:3 row_mask:0xf bank_mask:0xf\n"
"v_mfma_f32_16x16x32_bf16 v[24:27], v[128:131], a[244:247], 0\n"
"s_add_u32 s40, s56, s54\n"
"ds_read_b128 v[220:223], v231 offset:16896\n"
"v_mul_f32_dpp v21, v246, v21 row_newbcast:1 row_mask:0xf bank_mask:0xf\n"
"v_mfma_f32_16x16x32_bf16 v[24:27], v[132:135], a[248:251], v[24:27]\n"
"buffer_load_dwordx4 v207, s[20:23], s40 offen offset:3072 nt lds\n"
"v_mfma_f32_16x16x32_bf16 v[56:59], v[136:139], a[244:247], 0\n"
"s_add_u32 s55, s55, s59\n"
"v_cvt_pk_bf16_f32 v17, v18, v19\n"
"v_cvt_pk_bf16_f32 v104, v156, v157\n"
"v_mfma_f32_16x16x32_bf16 v[56:59], v[140:143], a[248:251], v[56:59]\n"
"s_or_b32 s55, s55, s58\n"
"v_exp_f32 v178, v178\n"
"v_mfma_f32_16x16x32_bf16 v[88:91], v[144:147], a[244:247], 0\n"
"s_xor_b32 s56, s55, s57\n"
"ds_read_b128 v[208:211], v230 offset:16384\n"
"v_cvt_pk_bf16_f32 v49, v50, v51\n"
"v_mfma_f32_16x16x32_bf16 v[88:91], v[148:151], a[248:251], v[88:91]\n"
"ds_read_b128 v[216:219], v230 offset:16896\n"
"v_cvt_pk_bf16_f32 v18, v20, v21\n"
"v_mfma_f32_16x16x32_bf16 v[92:95], v[148:151], v[224:227], 0\n"
"v_exp_f32 v160, v160\n"
"v_mfma_f32_16x16x32_bf16 v[92:95], v[144:147], a[252:255], v[92:95]\n"
"v_exp_f32 v165, v165\n"
"v_mfma_f32_16x16x32_bf16 v[60:63], v[140:143], v[224:227], 0\n"
"s_add_u32 m0, s44, 49152\n"
"ds_read_b64_tr_b16 a[248:249], v236 offset:0\n"
"v_mul_f32_dpp v80, v206, v80 row_newbcast:0 row_mask:0xf bank_mask:0xf\n"
"v_mfma_f32_16x16x32_bf16 v[60:63], v[136:139], a[252:255], v[60:63]\n"
"buffer_load_dwordx4 v240, s[16:19], s56 offen nt lds\n"
"v_mfma_f32_16x16x32_bf16 v[28:31], v[132:135], v[224:227], 0\n"
"v_exp_f32 v177, v177\n"
"v_mfma_f32_16x16x32_bf16 v[28:31], v[128:131], a[252:255], v[28:31]\n"
"v_exp_f32 v169, v169\n"
"v_mfma_f32_16x16x32_bf16 a[236:239], v[124:127], a[252:255], 0\n"
"ds_read_b64_tr_b16 a[250:251], v236 offset:4096\n"
"ds_read_b64_tr_b16 v[224:225], v237 offset:0\n"
"v_smfmac_f32_16x16x64_bf16 v[198:201], a[240:243], v[136:143], v241\n"
"s_waitcnt lgkmcnt(3)\n"
"ds_read_b64_tr_b16 a[246:247], v232 offset:4096\n"
"v_cvt_pk_bf16_f32 v112, v160, v161\n"
"v_mfma_f32_16x16x32_bf16 v[156:159], v[208:211], a[0:3], v[180:183]\n"
"v_cvt_pk_bf16_f32 v80, v80, v81\n"
"v_cvt_pk_bf16_f32 v113, v162, v163\n"
"v_mfma_f32_16x16x32_bf16 v[156:159], v[212:215], a[4:7], v[156:159]\n"
"ds_read_b64_tr_b16 a[254:255], v233 offset:4096\n"
"v_xor_b32 v248, s75, v248\n"
"v_mfma_f32_16x16x32_bf16 v[156:159], v[216:219], a[8:11], v[156:159]\n"
"ds_read_b64_tr_b16 a[244:245], v232 offset:0\n"
"v_cvt_pk_bf16_f32 v120, v164, v165\n"
"v_mfma_f32_16x16x32_bf16 v[156:159], v[220:223], a[12:15], v[156:159]\n"
"v_exp_f32 v170, v170\n"
"v_mfma_f32_16x16x32_bf16 v[160:163], v[216:219], a[24:27], v[184:187]\n"
"v_exp_f32 v179, v179\n"
"v_mfma_f32_16x16x32_bf16 v[160:163], v[220:223], a[28:31], v[160:163]\n"
"ds_read_b64_tr_b16 v[226:227], v237 offset:4096\n"
"v_cvt_pk_bf16_f32 v121, v166, v167\n"
"v_mfma_f32_16x16x32_bf16 v[160:163], v[208:211], a[16:19], v[160:163]\n"
"s_add_u32 s40, s56, s52\n"
"v_exp_f32 v168, v168\n"
"v_mfma_f32_16x16x32_bf16 v[160:163], v[212:215], a[20:23], v[160:163]\n"
"buffer_load_dwordx4 v207, s[16:19], s40 offen offset:1024 nt lds\n"
"v_mfma_f32_16x16x32_bf16 v[164:167], v[208:211], a[32:35], v[188:191]\n"
"v_exp_f32 v176, v176\n"
"v_mfma_f32_16x16x32_bf16 v[164:167], v[212:215], a[36:39], v[164:167]\n"
"v_cvt_pk_bf16_f32 v19, v22, v23\n"
"v_mul_f32_dpp v53, v247, v53 row_newbcast:1 row_mask:0xf bank_mask:0xf\n"
"v_mfma_f32_16x16x32_bf16 v[164:167], v[216:219], a[40:43], v[164:167]\n"
"ds_read_b64_tr_b16 a[252:253], v233 offset:0\n"
"ds_read_b128 v[208:211], v230 offset:20480\n"
"v_mfma_f32_16x16x32_bf16 v[164:167], v[220:223], a[44:47], v[164:167]\n"
"s_waitcnt lgkmcnt(3)\n"
"v_exp_f32 v158, v158\n"
"v_mfma_f32_16x16x32_bf16 a[208:211], v[124:127], a[244:247], 0\n"
"ds_read_b128 v[220:223], v231 offset:20992\n"
"v_cvt_pk_bf16_f32 v81, v82, v83\n"
"v_mfma_f32_16x16x32_bf16 v[0:3], v[128:131], a[244:247], 0\n"
"ds_read_b128 v[212:215], v231 offset:20480\n"
"v_mul_f32_dpp v55, v247, v55 row_newbcast:3 row_mask:0xf bank_mask:0xf\n"
"v_mfma_f32_16x16x32_bf16 v[0:3], v[132:135], a[248:251], v[0:3]\n"
"v_exp_f32 v175, v175\n"
"v_mfma_f32_16x16x32_bf16 v[32:35], v[136:139], a[244:247], 0\n"
"s_add_u32 s40, s56, s53\n"
"ds_read_b128 v[216:219], v230 offset:20992\n"
"v_mul_f32_dpp v84, v206, v84 row_newbcast:0 row_mask:0xf bank_mask:0xf\n"
"v_mfma_f32_16x16x32_bf16 v[32:35], v[140:143], a[248:251], v[32:35]\n"
"buffer_load_dwordx4 v240, s[16:19], s40 offen offset:2048 nt lds\n"
"v_mfma_f32_16x16x32_bf16 v[64:67], v[144:147], a[244:247], 0\n"
"v_cvt_pk_bf16_f32 v50, v52, v53\n"
"v_mul_f32_dpp v86, v206, v86 row_newbcast:2 row_mask:0xf bank_mask:0xf\n"
"v_mfma_f32_16x16x32_bf16 v[64:67], v[148:151], a[248:251], v[64:67]\n"
"s_waitcnt lgkmcnt(3)\n"
"ds_write_b128 v248, v[16:19] offset:49152\n"
"v_cvt_pk_bf16_f32 v82, v84, v85\n"
"v_exp_f32 v172, v172\n"
"v_exp_f32 v152, v152\n"
"v_exp_f32 v159, v159\n"
"v_exp_f32 v161, v161\n"
"v_exp_f32 v164, v164\n"
"v_exp_f32 v167, v167\n"
"v_mfma_f32_16x16x32_bf16 v[68:71], v[148:151], v[224:227], 0\n"
"v_cvt_pk_bf16_f32 v51, v54, v55\n"
"v_cvt_pk_bf16_f32 v98, v168, v169\n"
"v_mfma_f32_16x16x32_bf16 v[68:71], v[144:147], a[252:255], v[68:71]\n"
"ds_write_b128 v248, v[48:51] offset:50176\n"
"v_exp_f32 v171, v171\n"
"v_exp_f32 v173, v173\n"
"v_exp_f32 v156, v156\n"
"v_exp_f32 v163, v163\n"
"v_mfma_f32_16x16x32_bf16 v[36:39], v[140:143], v[224:227], 0\n"
"ds_read_b64_tr_b16 a[246:247], v232 offset:4608\n"
"ds_read_b64_tr_b16 a[250:251], v236 offset:4608\n"
"v_mfma_f32_16x16x32_bf16 v[36:39], v[136:139], a[252:255], v[36:39]\n"
"ds_read_b64_tr_b16 a[74:75], v249 offset:50184\n"
"v_cvt_pk_bf16_f32 v107, v174, v175\n"
"v_mfma_f32_16x16x32_bf16 v[4:7], v[132:135], v[224:227], 0\n"
"ds_read_b64_tr_b16 a[56:57], v249 offset:49152\n"
"v_cvt_pk_bf16_f32 v83, v86, v87\n"
"v_mfma_f32_16x16x32_bf16 v[4:7], v[128:131], a[252:255], v[4:7]\n"
"ds_write_b128 v248, v[80:83] offset:51200\n"
"v_cvt_pk_bf16_f32 v99, v170, v171\n"
"v_exp_f32 v155, v155\n"
"v_exp_f32 v157, v157\n"
"v_mfma_f32_16x16x32_bf16 a[212:215], v[124:127], a[252:255], 0\n"
"s_add_u32 s40, s56, s54\n"
"ds_read_b64_tr_b16 a[58:59], v249 offset:49160\n"
"ds_read_b64_tr_b16 v[224:225], v237 offset:512\n"
"v_smfmac_f32_16x16x64_bf16 v[200:203], a[240:243], v[144:151], v241\n"
"buffer_load_dwordx4 v207, s[16:19], s40 offen offset:3072 nt lds\n"
"v_mfma_f32_16x16x32_bf16 v[168:171], v[208:211], a[0:3], v[180:183]\n"
"s_waitcnt lgkmcnt(8)\n"
"v_exp_f32 v153, v153\n"
"v_mfma_f32_16x16x32_bf16 v[168:171], v[212:215], a[4:7], v[168:171]\n"
"ds_read_b64_tr_b16 a[72:73], v249 offset:50176\n"
"ds_read_b64_tr_b16 a[90:91], v249 offset:51208\n"
"v_mfma_f32_16x16x32_bf16 v[168:171], v[216:219], a[8:11], v[168:171]\n"
"ds_read_b64_tr_b16 a[88:89], v249 offset:51200\n"
"v_cvt_pk_bf16_f32 v115, v178, v179\n"
"v_mfma_f32_16x16x32_bf16 v[168:171], v[220:223], a[12:15], v[168:171]\n"
"ds_read_b64_tr_b16 a[244:245], v232 offset:512\n"
"v_cvt_pk_bf16_f32 v106, v172, v173\n"
"v_mfma_f32_16x16x32_bf16 v[172:175], v[216:219], a[24:27], v[184:187]\n"
"ds_read_b64_tr_b16 a[248:249], v236 offset:512\n"
"v_cvt_pk_bf16_f32 v114, v176, v177\n"
"v_mfma_f32_16x16x32_bf16 v[172:175], v[220:223], a[28:31], v[172:175]\n"
"v_exp_f32 v162, v162\n"
"v_mfma_f32_16x16x32_bf16 v[172:175], v[208:211], a[16:19], v[172:175]\n"
"ds_read_b64_tr_b16 v[226:227], v237 offset:4608\n"
"v_cvt_pk_bf16_f32 v122, v152, v153\n"
"v_mfma_f32_16x16x32_bf16 v[172:175], v[212:215], a[20:23], v[172:175]\n"
"s_cbranch_vccnz LPCT1_%=\nLPKT1_%=:\n"
"ds_read_b64_tr_b16 a[252:253], v233 offset:512\n"
"v_mfma_f32_16x16x32_bf16 v[176:179], v[208:211], a[32:35], v[188:191]\n"
"v_exp_f32 v166, v166\n"
"v_mfma_f32_16x16x32_bf16 v[176:179], v[212:215], a[36:39], v[176:179]\n"
"ds_read_b64_tr_b16 a[254:255], v233 offset:4608\n"
"v_cvt_pk_bf16_f32 v109, v162, v163\n"
"v_mfma_f32_16x16x32_bf16 v[176:179], v[216:219], a[40:43], v[176:179]\n"
"v_exp_f32 v169, v169\n"
"v_mfma_f32_16x16x32_bf16 v[176:179], v[220:223], a[44:47], v[176:179]\n"
"s_waitcnt lgkmcnt(4)\n"
"v_cvt_pk_bf16_f32 v101, v158, v159\n"
"v_mfma_f32_16x16x32_bf16 a[224:227], v[124:127], a[244:247], 0\n"
"v_exp_f32 v154, v154\n"
"v_mfma_f32_16x16x32_bf16 v[16:19], v[128:131], a[244:247], 0\n"
"s_waitcnt vmcnt(8) lgkmcnt(3)\n"
"v_exp_f32 v168, v168\n"
"v_mfma_f32_16x16x32_bf16 v[16:19], v[132:135], a[248:251], v[16:19]\n"
"s_barrier\n"
"ds_read_b128 v[212:215], v229 offset:32768\n"
"ds_read_b128 v[216:219], v228 offset:33280\n"
"v_exp_f32 v165, v165\n"
"v_mfma_f32_16x16x32_bf16 v[48:51], v[136:139], a[244:247], 0\n"
"ds_read_b128 v[220:223], v229 offset:33280\n"
"v_xor_b32 v249, s75, v249\n"
"v_mfma_f32_16x16x32_bf16 v[48:51], v[140:143], a[248:251], v[48:51]\n"
"ds_read_b128 v[208:211], v228 offset:32768\n"
"v_add_u32 v243, 0x1000, v243\n"
"v_mfma_f32_16x16x32_bf16 v[80:83], v[144:147], a[244:247], 0\n"
"ds_read_b64_tr_b16 a[246:247], v234 offset:20480\n"
"v_cvt_pk_bf16_f32 v117, v166, v167\n"
"v_mfma_f32_16x16x32_bf16 v[80:83], v[148:151], a[248:251], v[80:83]\n"
"s_waitcnt lgkmcnt(5)\n"
"ds_read_b64_tr_b16 a[250:251], v238 offset:20480\n"
"v_xor_b32 v248, s75, v248\n"
"v_mfma_f32_16x16x32_bf16 v[84:87], v[148:151], v[224:227], 0\n"
"v_cvt_pk_bf16_f32 v100, v156, v157\n"
"v_cvt_pk_bf16_f32 v116, v164, v165\n"
"v_mfma_f32_16x16x32_bf16 v[84:87], v[144:147], a[252:255], v[84:87]\n"
"v_exp_f32 v160, v160\n"
"v_mfma_f32_16x16x32_bf16 v[52:55], v[140:143], v[224:227], 0\n"
"v_exp_f32 v176, v176\n"
"v_mfma_f32_16x16x32_bf16 v[52:55], v[136:139], a[252:255], v[52:55]\n"
"v_exp_f32 v177, v177\n"
"v_mfma_f32_16x16x32_bf16 v[20:23], v[132:135], v[224:227], 0\n"
"v_cvt_pk_bf16_f32 v123, v154, v155\n"
"v_cvt_pk_bf16_f32 v118, v176, v177\n"
"v_mfma_f32_16x16x32_bf16 v[20:23], v[128:131], a[252:255], v[20:23]\n"
"v_exp_f32 v174, v174\n"
"v_mfma_f32_16x16x32_bf16 a[228:231], v[124:127], a[252:255], 0\n"
"s_waitcnt lgkmcnt(1)\n"
"ds_read_b64_tr_b16 a[254:255], v235 offset:20480\n"
"s_branch LAY2_%=\n"
"LAX_%=:\n"
"v_mfma_f32_16x16x32_bf16 a[224:227], v[124:127], a[244:247], a[224:227]\n"
"v_exp_f32 v169, v169\n"
"v_mfma_f32_16x16x32_bf16 v[16:19], v[128:131], a[244:247], v[16:19]\n"
"s_waitcnt vmcnt(8)\n"
"v_exp_f32 v168, v168\n"
"v_mfma_f32_16x16x32_bf16 v[16:19], v[132:135], a[248:251], v[16:19]\n"
"s_barrier\n"
"v_mfma_f32_16x16x32_bf16 v[48:51], v[136:139], a[244:247], v[48:51]\n"
"v_lshlrev_b32_e64 v244, 24, s43\n"
"v_and_b32 v245, 0x3f0, v242\n"
"v_mfma_f32_16x16x32_bf16 v[48:51], v[140:143], a[248:251], v[48:51]\n"
"v_accvgpr_read_b32 v253, a96\n"
"v_and_b32 v253, 0x7fff0000, v253\n"
"v_mfma_f32_16x16x32_bf16 v[80:83], v[144:147], a[244:247], v[80:83]\n"
"v_exp_f32 v178, v178\n"
"v_mfma_f32_16x16x32_bf16 v[80:83], v[148:151], a[248:251], v[80:83]\n"
"ds_read_b64_tr_b16 a[244:245], v234 offset:49152\n"
"ds_read_b64_tr_b16 a[246:247], v234 offset:53248\n"
"v_cvt_pk_bf16_f32 v123, v154, v155\n"
"v_cvt_pk_bf16_f32 v102, v168, v169\n"
"v_exp_f32 v173, v173\n"
"v_exp_f32 v177, v177\n"
"v_exp_f32 v179, v179\n"
"v_mfma_f32_16x16x32_bf16 v[84:87], v[148:151], v[224:227], v[84:87]\n"
"ds_read_b64_tr_b16 a[248:249], v238 offset:49152\n"
"v_cvt_pk_bf16_f32 v116, v164, v165\n"
"v_mfma_f32_16x16x32_bf16 v[84:87], v[144:147], a[252:255], v[84:87]\n"
"ds_read_b64_tr_b16 a[250:251], v238 offset:53248\n"
"v_cvt_pk_bf16_f32 v100, v156, v157\n"
"v_cvt_pk_bf16_f32 v108, v160, v161\n"
"v_exp_f32 v162, v162\n"
"v_exp_f32 v171, v171\n"
"v_exp_f32 v174, v174\n"
"v_exp_f32 v176, v176\n"
"v_max_u32_dpp v253, v253, v253 quad_perm:[1,0,3,2] row_mask:0xf bank_mask:0xf\n"
"v_mfma_f32_16x16x32_bf16 v[52:55], v[140:143], v[224:227], v[52:55]\n"
"v_exp_f32 v170, v170\n"
"v_mfma_f32_16x16x32_bf16 v[52:55], v[136:139], a[252:255], v[52:55]\n"
"v_cvt_pk_bf16_f32 v101, v158, v159\n"
"v_max_u32_dpp v253, v253, v253 quad_perm:[2,3,0,1] row_mask:0xf bank_mask:0xf\n"
"v_mfma_f32_16x16x32_bf16 v[20:23], v[132:135], v[224:227], v[20:23]\n"
"v_exp_f32 v172, v172\n"
"v_mfma_f32_16x16x32_bf16 v[20:23], v[128:131], a[252:255], v[20:23]\n"
"v_cvt_pk_bf16_f32 v103, v170, v171\n"
"v_cvt_pk_bf16_f32 v118, v176, v177\n"
"v_mfma_f32_16x16x32_bf16 a[228:231], v[124:127], a[252:255], a[228:231]\n"
"s_waitcnt lgkmcnt(2)\n"
"ds_read_b64_tr_b16 a[252:253], v235 offset:49152\n"
"v_cvt_pk_bf16_f32 v110, v172, v173\n"
"v_mfma_f32_16x16x32_bf16 a[216:219], v[120:123], a[244:247], a[216:219]\n"
"s_mov_b64 s[28:29], s[8:9]\n"
"ds_read_b64_tr_b16 v[224:225], v239 offset:49152\n"
"v_cvt_pk_bf16_f32 v117, v166, v167\n"
"v_smfmac_f32_16x16x64_bf16 v[196:199], a[240:243], v[96:103], v241\n"
"s_mov_b32 s30, 655360\n"
"ds_read_b64_tr_b16 v[226:227], v239 offset:53248\n"
"v_and_b32 v252, 0x7fff0000, v252\n"
"v_mfma_f32_16x16x32_bf16 v[8:11], v[96:99], a[244:247], v[8:11]\n"
"s_waitcnt lgkmcnt(3)\n"
"s_mov_b32 s71, s44\n"
"ds_read_b64_tr_b16 a[254:255], v235 offset:53248\n"
"v_cvt_pk_bf16_f32 v119, v178, v179\n"
"v_mfma_f32_16x16x32_bf16 v[8:11], v[100:103], a[248:251], v[8:11]\n"
"s_mov_b32 s70, s95\n"
"v_exp_f32 v175, v175\n"
"v_smfmac_f32_16x16x64_bf16 v[200:203], a[240:243], v[112:119], v241\n"
"v_cvt_pk_bf16_f32 v109, v162, v163\n"
"v_cvt_pk_bf16_f32 v111, v174, v175\n"
"v_mfma_f32_16x16x32_bf16 v[40:43], v[104:107], a[244:247], v[40:43]\n"
"v_max_u32_dpp v253, v253, v253 row_half_mirror row_mask:0xf bank_mask:0xf\n"
"v_lshrrev_b32 v245, 1, v245\n"
"v_mfma_f32_16x16x32_bf16 v[40:43], v[108:111], a[248:251], v[40:43]\n"
"s_add_u32 s41, s70, 327680\n"
"s_add_u32 m0, s71, 32768\n"
"v_mul_f32 v244, 0x45800000, v244\n"
"v_smfmac_f32_16x16x64_bf16 v[198:201], a[240:243], v[104:111], v241\n"
"buffer_load_dwordx4 v240, s[28:31], s41 offen lds\n"
"v_mfma_f32_16x16x32_bf16 v[72:75], v[112:115], a[244:247], v[72:75]\n"
"v_max_u32_dpp v252, v252, v252 quad_perm:[1,0,3,2] row_mask:0xf bank_mask:0xf\n"
"v_add_f32 v196, v196, v197\n"
"v_mfma_f32_16x16x32_bf16 v[72:75], v[116:119], a[248:251], v[72:75]\n"
"ds_read_b64_tr_b16 a[248:249], v238 offset:49664\n"
"ds_read_b64_tr_b16 a[250:251], v238 offset:53760\n"
"v_mfma_f32_16x16x32_bf16 v[202:205], a[240:243], v[120:123], v[202:205]\n"
"s_waitcnt lgkmcnt(2)\n"
"ds_read_b64_tr_b16 a[244:245], v234 offset:49664\n"
"v_add_f32 v200, v200, v201\n"
"v_mfma_f32_16x16x32_bf16 v[76:79], v[116:119], v[224:227], v[76:79]\n"
"s_add_u32 s40, s41, s52\n"
"ds_read_b64_tr_b16 a[246:247], v234 offset:53760\n"
"v_min_u32_dpp v253, v253, v253 row_mirror row_mask:0xf bank_mask:0xf\n"
"v_mfma_f32_16x16x32_bf16 v[76:79], v[112:115], a[252:255], v[76:79]\n"
"buffer_load_dwordx4 v240, s[28:31], s40 offen offset:1024 lds\n"
"v_mfma_f32_16x16x32_bf16 v[44:47], v[108:111], v[224:227], v[44:47]\n"
"v_max_u32_dpp v252, v252, v252 quad_perm:[2,3,0,1] row_mask:0xf bank_mask:0xf\n"
"v_add_f32 v198, v198, v199\n"
"v_mfma_f32_16x16x32_bf16 v[44:47], v[104:107], a[252:255], v[44:47]\n"
"v_max_u32_dpp v252, v252, v252 row_half_mirror row_mask:0xf bank_mask:0xf\n"
"v_min_u32_dpp v253, v253, v253 row_bcast:15 row_mask:0xa bank_mask:0xf\n"
"v_add_f32 v244, v244, v202\n"
"v_min3_f32 v244, v244, v196, v198\n"
"v_min_f32 v244, v244, v200\n"
"v_rcp_f32 v244, v244\n"
"v_mfma_f32_16x16x32_bf16 v[12:15], v[100:103], v[224:227], v[12:15]\n"
"s_add_u32 s40, s41, s53\n"
"ds_read_b64_tr_b16 v[224:225], v239 offset:49664\n"
"ds_read_b64_tr_b16 v[226:227], v239 offset:53760\n"
"v_mfma_f32_16x16x32_bf16 v[12:15], v[96:99], a[252:255], v[12:15]\n"
"buffer_load_dwordx4 v240, s[28:31], s40 offen offset:2048 lds\n"
"v_mfma_f32_16x16x32_bf16 a[220:223], v[120:123], a[252:255], a[220:223]\n"
"s_waitcnt lgkmcnt(2)\n"
"ds_read_b64_tr_b16 a[252:253], v235 offset:49664\n"
"ds_read_b64_tr_b16 a[254:255], v235 offset:53760\n"
"v_add_f32 v254, v196, v198\n"
"v_add_f32 v254, v254, v200\n"
"v_add_f32 v254, v254, v202\n"
"v_fmac_f32 v254, 0x68800000, v244\n"
"v_cmp_nge_f32 vcc, s45, v254\n"
"v_sub_u32 v245, v242, v245\n"
"v_bfe_u32 v254, v242, 8, 2\n"
"s_cbranch_vccnz LSLOW_%=\nLBACK3_%=:\ns_mov_b64 vcc, s[64:65]\n"
"v_mfma_f32_16x16x32_bf16 a[232:235], v[120:123], a[244:247], a[232:235]\n"
"v_rcp_f32 v206, v200\n"
"v_mfma_f32_16x16x32_bf16 v[24:27], v[96:99], a[244:247], v[24:27]\n"
"v_min_u32_dpp v252, v252, v252 row_mirror row_mask:0xf bank_mask:0xf\n"
"v_mov_b32_dpp v206, v206 row_ror:12 row_mask:0x2 bank_mask:0xf\n"
"v_mfma_f32_16x16x32_bf16 v[24:27], v[100:103], a[248:251], v[24:27]\n"
"v_rcp_f32 v246, v196\n"
"v_mfma_f32_16x16x32_bf16 v[56:59], v[104:107], a[244:247], v[56:59]\n"
"s_add_u32 s40, s41, s54\n"
"v_min_u32_dpp v253, v253, v253 row_bcast:31 row_mask:0xc bank_mask:0xf\n"
"v_mov_b32_dpp v246, v246 row_ror:12 row_mask:0x2 bank_mask:0xf\n"
"v_mfma_f32_16x16x32_bf16 v[56:59], v[108:111], a[248:251], v[56:59]\n"
"buffer_load_dwordx4 v240, s[28:31], s40 offen offset:3072 lds\n"
"v_min_u32_dpp v252, v252, v252 row_bcast:15 row_mask:0xa bank_mask:0xf\n"
"v_rcp_f32 v247, v198\n"
"v_mov_b32_dpp v246, v246 row_ror:8 row_mask:0x4 bank_mask:0xf\n"
"v_mov_b32_dpp v206, v206 row_ror:8 row_mask:0x4 bank_mask:0xf\n"
"v_lshl_add_u32 v254, v254, 3, s43\n"
"v_mfma_f32_16x16x32_bf16 v[88:91], v[112:115], a[244:247], v[88:91]\n"
"s_add_u32 s71, s44, s75\n"
"v_mov_b32_dpp v247, v247 row_ror:12 row_mask:0x2 bank_mask:0xf\n"
"v_xor_b32 v244, 0x11000, v242\n"
"v_mfma_f32_16x16x32_bf16 v[88:91], v[116:119], a[248:251], v[88:91]\n"
"s_waitcnt lgkmcnt(2)\n"
"s_mov_b32 s70, s100\n"
"ds_read_b64_tr_b16 a[244:245], v232 offset:49152\n"
"ds_read_b64_tr_b16 a[248:249], v236 offset:49152\n"
"ds_read_b64_tr_b16 a[250:251], v236 offset:53248\n"
"v_min_u32_dpp v252, v252, v252 row_bcast:31 row_mask:0xc bank_mask:0xf\n"
"v_mul_f32 v252, 0x43340000, v252\n"
"v_mul_f32 v252, v252, v253\n"
"v_mov_b32_dpp v247, v247 row_ror:8 row_mask:0x4 bank_mask:0xf\n"
"v_mov_b32_dpp v246, v246 row_ror:4 row_mask:0x8 bank_mask:0xf\n"
"v_mov_b32_dpp v206, v206 row_ror:4 row_mask:0x8 bank_mask:0xf\n"
"v_xor_b32 v250, 0x1000, v245\n"
"v_mfma_f32_16x16x32_bf16 v[92:95], v[116:119], v[224:227], v[92:95]\n"
"s_waitcnt lgkmcnt(3)\n"
"s_add_u32 s41, s70, 163840\n"
"s_add_u32 m0, s71, 16384\n"
"ds_read_b64_tr_b16 a[246:247], v232 offset:53248\n"
"v_mfma_f32_16x16x32_bf16 v[92:95], v[112:115], a[252:255], v[92:95]\n"
"buffer_load_dwordx4 v240, s[28:31], s41 offen lds\n"
"v_mov_b32_dpp v247, v247 row_ror:4 row_mask:0x8 bank_mask:0xf\n"
"v_lshl_add_u32 v254, v254, 6, s75\n"
"v_mul_f32_dpp v9, v246, v9 row_newbcast:1 row_mask:0xf bank_mask:0xf\n"
"v_mul_f32_dpp v72, v206, v72 row_newbcast:0 row_mask:0xf bank_mask:0xf\n"
"v_mul_f32_dpp v76, v206, v76 row_newbcast:0 row_mask:0xf bank_mask:0xf\n"
"v_mul_f32_dpp v78, v206, v78 row_newbcast:2 row_mask:0xf bank_mask:0xf\n"
"v_mul_f32_dpp v25, v246, v25 row_newbcast:1 row_mask:0xf bank_mask:0xf\n"
"v_mul_f32_dpp v26, v246, v26 row_newbcast:2 row_mask:0xf bank_mask:0xf\n"
"v_mfma_f32_16x16x32_bf16 v[60:63], v[108:111], v[224:227], v[60:63]\n"
"v_mul_f32_dpp v77, v206, v77 row_newbcast:1 row_mask:0xf bank_mask:0xf\n"
"v_mul_f32_dpp v58, v247, v58 row_newbcast:2 row_mask:0xf bank_mask:0xf\n"
"v_mfma_f32_16x16x32_bf16 v[60:63], v[104:107], a[252:255], v[60:63]\n"
"ds_write_b128 v244, a[220:223] offset:33792\n"
"v_mul_f32_dpp v11, v246, v11 row_newbcast:3 row_mask:0xf bank_mask:0xf\n"
"v_mul_f32_dpp v12, v246, v12 row_newbcast:0 row_mask:0xf bank_mask:0xf\n"
"v_mul_f32_dpp v74, v206, v74 row_newbcast:2 row_mask:0xf bank_mask:0xf\n"
"v_mul_f32_dpp v75, v206, v75 row_newbcast:3 row_mask:0xf bank_mask:0xf\n"
"v_mul_f32_dpp v79, v206, v79 row_newbcast:3 row_mask:0xf bank_mask:0xf\n"
"v_mfma_f32_16x16x32_bf16 v[28:31], v[100:103], v[224:227], v[28:31]\n"
"s_add_u32 s40, s41, s52\n"
"ds_read_b64_tr_b16 v[226:227], v237 offset:53248\n"
"v_mul_f32_dpp v73, v206, v73 row_newbcast:1 row_mask:0xf bank_mask:0xf\n"
"v_mfma_f32_16x16x32_bf16 v[28:31], v[96:99], a[252:255], v[28:31]\n"
"buffer_load_dwordx4 v240, s[28:31], s40 offen offset:1024 lds\n"
"v_mfma_f32_16x16x32_bf16 a[236:239], v[120:123], a[252:255], a[236:239]\n"
"s_waitcnt lgkmcnt(2)\n"
"v_min_f32 v252, 0x71800000, v252\n"
"v_mul_f32_dpp v95, v206, v95 row_newbcast:3 row_mask:0xf bank_mask:0xf\n"
"v_mfma_f32_16x16x32_bf16 a[208:211], v[120:123], a[244:247], a[208:211]\n"
"ds_read_b64_tr_b16 a[254:255], v233 offset:53248\n"
"ds_read_b64_tr_b16 v[224:225], v237 offset:49152\n"
"v_mfma_f32_16x16x32_bf16 v[0:3], v[96:99], a[244:247], v[0:3]\n"
"ds_read_b64_tr_b16 a[252:253], v233 offset:49152\n"
"v_mul_f32_dpp v89, v206, v89 row_newbcast:1 row_mask:0xf bank_mask:0xf\n"
"v_mfma_f32_16x16x32_bf16 v[0:3], v[100:103], a[248:251], v[0:3]\n"
"ds_write_b128 v244, a[216:219] offset:32768\n"
"v_readlane_b32 s23, v252, 63\n"
"v_cvt_pk_bf16_f32 v72, v72, v73\n"
"v_cvt_pk_bf16_f32 v73, v74, v75\n"
"v_mul_f32_dpp v56, v247, v56 row_newbcast:0 row_mask:0xf bank_mask:0xf\n"
"v_mul_f32_dpp v62, v247, v62 row_newbcast:2 row_mask:0xf bank_mask:0xf\n"
"v_mfma_f32_16x16x32_bf16 v[32:35], v[104:107], a[244:247], v[32:35]\n"
"s_add_u32 s40, s41, s53\n"
"v_mul_f32_dpp v46, v247, v46 row_newbcast:2 row_mask:0xf bank_mask:0xf\n"
"v_mul_f32_dpp v24, v246, v24 row_newbcast:0 row_mask:0xf bank_mask:0xf\n"
"v_mfma_f32_16x16x32_bf16 v[32:35], v[108:111], a[248:251], v[32:35]\n"
"buffer_load_dwordx4 v240, s[28:31], s40 offen offset:2048 lds\n"
"v_readlane_b32 s22, v252, 63\n"
"v_mul_f32_dpp v41, v247, v41 row_newbcast:1 row_mask:0xf bank_mask:0xf\n"
"v_mul_f32_dpp v42, v247, v42 row_newbcast:2 row_mask:0xf bank_mask:0xf\n"
"v_mul_f32_dpp v44, v247, v44 row_newbcast:0 row_mask:0xf bank_mask:0xf\n"
"v_mul_f32_dpp v45, v247, v45 row_newbcast:1 row_mask:0xf bank_mask:0xf\n"
"v_cvt_pk_bf16_f32 v74, v76, v77\n"
"v_mul_f32_dpp v31, v246, v31 row_newbcast:3 row_mask:0xf bank_mask:0xf\n"
"v_mul_f32_dpp v88, v206, v88 row_newbcast:0 row_mask:0xf bank_mask:0xf\n"
"v_mfma_f32_16x16x32_bf16 v[64:67], v[112:115], a[244:247], v[64:67]\n"
"ds_read_b64_tr_b16 a[244:245], v232 offset:49664\n"
"ds_read_b64_tr_b16 a[246:247], v232 offset:53760\n"
"v_mfma_f32_16x16x32_bf16 v[64:67], v[116:119], a[248:251], v[64:67]\n"
"s_waitcnt lgkmcnt(4)\n"
"ds_write_b128 v244, a[232:235] offset:34816\n"
"v_mul_f32_dpp v13, v246, v13 row_newbcast:1 row_mask:0xf bank_mask:0xf\n"
"v_cvt_pk_bf16_f32 v75, v78, v79\n"
"v_mul_f32_dpp v57, v247, v57 row_newbcast:1 row_mask:0xf bank_mask:0xf\n"
"v_mul_f32_dpp v92, v206, v92 row_newbcast:0 row_mask:0xf bank_mask:0xf\n"
"v_mul_f32_dpp v1, v246, v1 row_newbcast:1 row_mask:0xf bank_mask:0xf\n"
"v_mfma_f32_16x16x32_bf16 v[68:71], v[116:119], v[224:227], v[68:71]\n"
"s_waitcnt lgkmcnt(4)\n"
"s_add_u32 s40, s41, s54\n"
"ds_read_b64_tr_b16 a[248:249], v236 offset:49664\n"
"ds_read_b64_tr_b16 a[250:251], v236 offset:53760\n"
"v_mfma_f32_16x16x32_bf16 v[68:71], v[112:115], a[252:255], v[68:71]\n"
"buffer_load_dwordx4 v240, s[28:31], s40 offen offset:3072 lds\n"
"v_mov_b32 v253, v202\n"
"v_sub_u32 v228, v232, v254\n"
"v_mul_f32_dpp v15, v246, v15 row_newbcast:3 row_mask:0xf bank_mask:0xf\n"
"v_mul_f32_dpp v43, v247, v43 row_newbcast:3 row_mask:0xf bank_mask:0xf\n"
"v_cvt_pk_bf16_f32 v24, v24, v25\n"
"v_mul_f32_dpp v59, v247, v59 row_newbcast:3 row_mask:0xf bank_mask:0xf\n"
"v_mul_f32_dpp v0, v246, v0 row_newbcast:0 row_mask:0xf bank_mask:0xf\n"
"v_cvt_pk_bf16_f32 v0, v0, v1\n"
"v_mfma_f32_16x16x32_bf16 v[36:39], v[108:111], v[224:227], v[36:39]\n"
"v_cvt_pk_bf16_f32 v56, v56, v57\n"
"v_mul_f32_dpp v33, v247, v33 row_newbcast:1 row_mask:0xf bank_mask:0xf\n"
"v_mfma_f32_16x16x32_bf16 v[36:39], v[104:107], a[252:255], v[36:39]\n"
"ds_write_b128 v244, a[236:239] offset:35840\n"
"v_mov_b32 v252, v192\n"
"v_mul_f32_dpp v8, v246, v8 row_newbcast:0 row_mask:0xf bank_mask:0xf\n"
"v_mul_f32_dpp v60, v247, v60 row_newbcast:0 row_mask:0xf bank_mask:0xf\n"
"v_mul_f32_dpp v34, v247, v34 row_newbcast:2 row_mask:0xf bank_mask:0xf\n"
"v_xor_b32 v232, 0x1000, v228\n"
"v_mfma_f32_16x16x32_bf16 v[4:7], v[100:103], v[224:227], v[4:7]\n"
"ds_read_b64_tr_b16 v[224:225], v237 offset:49664\n"
"ds_read_b64_tr_b16 v[226:227], v237 offset:53760\n"
"v_mfma_f32_16x16x32_bf16 v[4:7], v[96:99], a[252:255], v[4:7]\n"
"ds_write_b64 v250, v[252:253] offset:52224\n"
"v_mul_f32_dpp v40, v247, v40 row_newbcast:0 row_mask:0xf bank_mask:0xf\n"
"v_cvt_pk_bf16_f32 v40, v40, v41\n"
"v_mul_f32_dpp v27, v246, v27 row_newbcast:3 row_mask:0xf bank_mask:0xf\n"
"v_mul_f32_dpp v28, v246, v28 row_newbcast:0 row_mask:0xf bank_mask:0xf\n"
"v_mul_f32_dpp v93, v206, v93 row_newbcast:1 row_mask:0xf bank_mask:0xf\n"
"v_cvt_pk_bf16_f32 v88, v88, v89\n"
"v_mul_f32_dpp v2, v246, v2 row_newbcast:2 row_mask:0xf bank_mask:0xf\n"
"v_mfma_f32_16x16x32_bf16 a[212:215], v[120:123], a[252:255], a[212:215]\n"
"s_waitcnt lgkmcnt(7)\n"
"ds_read_b64_tr_b16 a[252:253], v233 offset:49664\n"
"ds_read_b64_tr_b16 a[254:255], v233 offset:53760\n"
"v_mul_f32_dpp v10, v246, v10 row_newbcast:2 row_mask:0xf bank_mask:0xf\n"
"v_mul_f32_dpp v47, v247, v47 row_newbcast:3 row_mask:0xf bank_mask:0xf\n"
"v_cvt_pk_bf16_f32 v41, v42, v43\n"
"v_cvt_pk_bf16_f32 v43, v46, v47\n"
"v_mul_f32_dpp v30, v246, v30 row_newbcast:2 row_mask:0xf bank_mask:0xf\n"
"v_mul_f32_dpp v61, v247, v61 row_newbcast:1 row_mask:0xf bank_mask:0xf\n"
"v_mul_f32_dpp v36, v247, v36 row_newbcast:0 row_mask:0xf bank_mask:0xf\n"
"v_mul_f32_dpp v38, v247, v38 row_newbcast:2 row_mask:0xf bank_mask:0xf\n"
"v_mfma_f32_16x16x32_bf16 a[224:227], v[120:123], a[244:247], a[224:227]\n"
"ds_write_b128 v248, v[72:75] offset:51200\n"
"v_mul_f32_dpp v14, v246, v14 row_newbcast:2 row_mask:0xf bank_mask:0xf\n"
"v_cvt_pk_bf16_f32 v42, v44, v45\n"
"v_mul_f32_dpp v35, v247, v35 row_newbcast:3 row_mask:0xf bank_mask:0xf\n"
"v_mul_f32_dpp v64, v206, v64 row_newbcast:0 row_mask:0xf bank_mask:0xf\n"
"v_mul_f32_dpp v68, v206, v68 row_newbcast:0 row_mask:0xf bank_mask:0xf\n"
"v_mfma_f32_16x16x32_bf16 v[16:19], v[96:99], a[244:247], v[16:19]\n"
"s_waitcnt vmcnt(8) lgkmcnt(1)\n"
"ds_write_b128 v248, v[40:43] offset:50176\n"
"v_mul_f32_dpp v90, v206, v90 row_newbcast:2 row_mask:0xf bank_mask:0xf\n"
"v_mul_f32_dpp v91, v206, v91 row_newbcast:3 row_mask:0xf bank_mask:0xf\n"
"v_mul_f32_dpp v5, v246, v5 row_newbcast:1 row_mask:0xf bank_mask:0xf\n"
"v_mul_f32_dpp v6, v246, v6 row_newbcast:2 row_mask:0xf bank_mask:0xf\n"
"v_mul_f32_dpp v32, v247, v32 row_newbcast:0 row_mask:0xf bank_mask:0xf\n"
"v_mfma_f32_16x16x32_bf16 v[16:19], v[100:103], a[248:251], v[16:19]\n"
"s_barrier\n"
"ds_read_b64_tr_b16 a[20:21], v249 offset:50176\n"
"ds_read_b64_tr_b16 a[22:23], v249 offset:50184\n"
"ds_read_b64_tr_b16 v[210:211], v228 offset:18432\n"
"ds_read_b64_tr_b16 v[216:217], v228 offset:24576\n"
"v_cvt_pk_bf16_f32 v8, v8, v9\n"
"v_cvt_pk_bf16_f32 v89, v90, v91\n"
"v_cvt_pk_bf16_f32 v90, v92, v93\n"
"v_add_u32 v236, s76, v249\n"
"v_mfma_f32_16x16x32_bf16 v[48:51], v[104:107], a[244:247], v[48:51]\n"
"ds_read_b64_tr_b16 v[184:185], v232 offset:0\n"
"v_mul_f32_dpp v65, v206, v65 row_newbcast:1 row_mask:0xf bank_mask:0xf\n"
"v_mfma_f32_16x16x32_bf16 v[48:51], v[108:111], a[248:251], v[48:51]\n"
"ds_read_b64_tr_b16 v[182:183], v228 offset:2048\n"
"ds_read_b64_tr_b16 v[192:193], v232 offset:8192\n"
"ds_read_b64_tr_b16 v[212:213], v232 offset:16384\n"
"ds_read_b64_tr_b16 v[222:223], v232 offset:26624\n"
"v_cvt_pk_bf16_f32 v9, v10, v11\n"
"v_cvt_pk_bf16_f32 v10, v12, v13\n"
"v_cvt_pk_bf16_f32 v11, v14, v15\n"
"v_mul_f32_dpp v29, v246, v29 row_newbcast:1 row_mask:0xf bank_mask:0xf\n"
"v_cvt_pk_bf16_f32 v25, v26, v27\n"
"v_mul_f32_dpp v66, v206, v66 row_newbcast:2 row_mask:0xf bank_mask:0xf\n"
"v_mfma_f32_16x16x32_bf16 v[80:83], v[112:115], a[244:247], v[80:83]\n"
"ds_read_b64 v[250:251], v245 offset:52224\n"
"ds_read_b64_tr_b16 v[186:187], v232 offset:2048\n"
"v_mfma_f32_16x16x32_bf16 v[80:83], v[116:119], a[248:251], v[80:83]\n"
"ds_read_b64_tr_b16 a[36:37], v249 offset:51200\n"
"ds_read_b64_tr_b16 a[38:39], v249 offset:51208\n"
"ds_read_b64_tr_b16 v[190:191], v228 offset:10240\n"
"ds_read_b64_tr_b16 v[194:195], v232 offset:10240\n"
"v_sub_u32 v229, v233, v254\n"
"v_mul_f32_dpp v63, v247, v63 row_newbcast:3 row_mask:0xf bank_mask:0xf\n"
"v_cvt_pk_bf16_f32 v57, v58, v59\n"
"v_cvt_pk_bf16_f32 v59, v62, v63\n"
"v_mul_f32_dpp v67, v206, v67 row_newbcast:3 row_mask:0xf bank_mask:0xf\n"
"v_mul_f32_dpp v71, v206, v71 row_newbcast:3 row_mask:0xf bank_mask:0xf\n"
"v_mfma_f32_16x16x32_bf16 v[84:87], v[116:119], v[224:227], v[84:87]\n"
"ds_read_b64_tr_b16 v[208:209], v228 offset:16384\n"
"ds_read_b64_tr_b16 v[218:219], v228 offset:26624\n"
"v_mfma_f32_16x16x32_bf16 v[84:87], v[112:115], a[252:255], v[84:87]\n"
"ds_write_b128 v248, v[8:11] offset:49152\n"
"v_cvt_pk_bf16_f32 v26, v28, v29\n"
"v_cvt_pk_bf16_f32 v27, v30, v31\n"
"v_mul_f32_dpp v94, v206, v94 row_newbcast:2 row_mask:0xf bank_mask:0xf\n"
"v_mul_f32_dpp v3, v246, v3 row_newbcast:3 row_mask:0xf bank_mask:0xf\n"
"v_xor_b32 v244, 0x1000, v244\n"
"v_mfma_f32_16x16x32_bf16 v[52:55], v[108:111], v[224:227], v[52:55]\n"
"ds_read_b64_tr_b16 v[180:181], v228 offset:0\n"
"ds_read_b64_tr_b16 v[188:189], v228 offset:8192\n"
"v_mfma_f32_16x16x32_bf16 v[52:55], v[104:107], a[252:255], v[52:55]\n"
"ds_read_b64_tr_b16 a[4:5], v249 offset:49152\n"
"ds_read_b64_tr_b16 a[6:7], v249 offset:49160\n"
"ds_read_b64_tr_b16 v[214:215], v232 offset:18432\n"
"ds_read_b64_tr_b16 v[220:221], v232 offset:24576\n"
"v_cvt_pk_bf16_f32 v58, v60, v61\n"
"v_mul_f32_dpp v4, v246, v4 row_newbcast:0 row_mask:0xf bank_mask:0xf\n"
"v_mul_f32_dpp v7, v246, v7 row_newbcast:3 row_mask:0xf bank_mask:0xf\n"
"v_mul_f32_dpp v39, v247, v39 row_newbcast:3 row_mask:0xf bank_mask:0xf\n"
"v_mov_b64 v[196:197], s[22:23]\n"
"v_mfma_f32_16x16x32_bf16 v[20:23], v[100:103], v[224:227], v[20:23]\n"
"ds_write_b128 v248, v[56:59] offset:50176\n"
"v_cvt_pk_bf16_f32 v91, v94, v95\n"
"v_cvt_pk_bf16_f32 v32, v32, v33\n"
"v_cvt_pk_bf16_f32 v33, v34, v35\n"
"v_mul_f32_dpp v69, v206, v69 row_newbcast:1 row_mask:0xf bank_mask:0xf\n"
"v_xor_b32 v237, 0x400, v236\n"
"v_mfma_f32_16x16x32_bf16 v[20:23], v[96:99], a[252:255], v[20:23]\n"
"ds_write_b128 v248, v[24:27] offset:49152\n"
"v_mul_f32_dpp v70, v206, v70 row_newbcast:2 row_mask:0xf bank_mask:0xf\n"
"v_cvt_pk_bf16_f32 v64, v64, v65\n"
"v_cvt_pk_bf16_f32 v65, v66, v67\n"
"v_mov_b64 v[198:199], s[22:23]\n"
"v_xor_b32 v233, 0x1000, v229\n"
"v_mfma_f32_16x16x32_bf16 a[228:231], v[120:123], a[252:255], a[228:231]\n"
"s_waitcnt lgkmcnt(7)\n"
"ds_write_b128 v248, v[88:91] offset:51200\n"
"v_sub_u32 v231, v235, v254\n"
"v_cvt_pk_bf16_f32 v1, v2, v3\n"
"v_cvt_pk_bf16_f32 v2, v4, v5\n"
"v_cvt_pk_bf16_f32 v3, v6, v7\n"
"v_mul_f32_dpp v37, v247, v37 row_newbcast:1 row_mask:0xf bank_mask:0xf\n"
"v_mfma_f32_16x16x32_bf16 v[124:127], v[180:183], a[96:99], v[196:199]\n"
"ds_read_b64_tr_b16 a[12:13], v249 offset:49152\n"
"ds_read_b64_tr_b16 a[28:29], v249 offset:50176\n"
"v_mfma_f32_16x16x32_bf16 v[124:127], v[184:187], a[100:103], v[124:127]\n"
"s_waitcnt lgkmcnt(9)\n"
"ds_read_b64_tr_b16 a[14:15], v249 offset:49160\n"
"ds_read_b64_tr_b16 a[30:31], v249 offset:50184\n"
"v_mfma_f32_16x16x32_bf16 v[124:127], v[188:191], a[104:107], v[124:127]\n"
"ds_read_b128 v[56:59], v244 offset:34816\n"
"ds_read_b128 v[60:63], v244 offset:35840\n"
"v_mfma_f32_16x16x32_bf16 v[124:127], v[192:195], a[108:111], v[124:127]\n"
"ds_write_b128 v248, v[0:3] offset:49152\n"
"v_sub_u32 v230, v234, v254\n"
"v_cvt_pk_bf16_f32 v34, v36, v37\n"
"v_cvt_pk_bf16_f32 v35, v38, v39\n"
"v_cvt_pk_bf16_f32 v66, v68, v69\n"
"v_cvt_pk_bf16_f32 v67, v70, v71\n"
"v_mfma_f32_16x16x32_bf16 v[124:127], v[208:211], a[144:147], v[124:127]\n"
"s_waitcnt lgkmcnt(10)\n"
"ds_read_b64_tr_b16 a[44:45], v249 offset:51200\n"
"ds_read_b64_tr_b16 a[46:47], v249 offset:51208\n"
"v_mfma_f32_16x16x32_bf16 v[124:127], v[212:215], a[148:151], v[124:127]\n"
"ds_write_b128 v248, v[32:35] offset:50176\n"
"ds_write_b128 v248, v[64:67] offset:51200\n"
"v_mfma_f32_16x16x32_bf16 v[124:127], v[216:219], a[152:155], v[124:127]\n"
"ds_read_b128 v[24:27], v244 offset:32768\n"
"ds_read_b128 v[28:31], v244 offset:33792\n"
"v_mfma_f32_16x16x32_bf16 v[124:127], v[220:223], a[156:159], v[124:127]\n"
"ds_read_b64_tr_b16 a[0:1], v249 offset:49152\n"
"ds_read_b64_tr_b16 a[16:17], v249 offset:50176\n"
"v_mfma_f32_16x16x32_bf16 v[140:143], v[220:223], a[172:175], v[196:199]\n"
"ds_read_b64_tr_b16 a[2:3], v249 offset:49160\n"
"ds_read_b64_tr_b16 a[254:255], v237 offset:6152\n"
"v_mfma_f32_16x16x32_bf16 v[140:143], v[216:219], a[168:171], v[140:143]\n"
"ds_read_b64_tr_b16 a[32:33], v249 offset:51200\n"
"ds_read_b64_tr_b16 v[118:119], v233 offset:18432\n"
"v_mfma_f32_16x16x32_bf16 v[140:143], v[212:215], a[164:167], v[140:143]\n"
"ds_read_b64_tr_b16 a[18:19], v249 offset:50184\n"
"ds_read_b64_tr_b16 a[34:35], v249 offset:51208\n"
"v_mfma_f32_16x16x32_bf16 v[140:143], v[208:211], a[160:163], v[140:143]\n"
"ds_read_b64_tr_b16 a[244:245], v237 offset:4096\n"
"ds_read_b64_tr_b16 a[248:249], v236 offset:6144\n"
"v_mfma_f32_16x16x32_bf16 v[140:143], v[192:195], a[124:127], v[140:143]\n"
"ds_read_b64_tr_b16 a[202:203], v236 offset:2056\n"
"ds_read_b64_tr_b16 a[252:253], v237 offset:6144\n"
"v_mfma_f32_16x16x32_bf16 v[140:143], v[188:191], a[120:123], v[140:143]\n"
"ds_read_b64_tr_b16 a[196:197], v237 offset:0\n"
"ds_read_b64_tr_b16 a[250:251], v236 offset:6152\n"
"v_mfma_f32_16x16x32_bf16 v[140:143], v[184:187], a[116:119], v[140:143]\n"
"ds_read_b64_tr_b16 v[112:113], v229 offset:16384\n"
"ds_read_b64_tr_b16 a[242:243], v236 offset:4104\n"
"v_mfma_f32_16x16x32_bf16 v[140:143], v[180:183], a[112:115], v[140:143]\n"
"ds_read_b64_tr_b16 a[204:205], v237 offset:2048\n"
"ds_read_b64_tr_b16 a[206:207], v237 offset:2056\n"
"ds_read_b64_tr_b16 v[100:101], v233 offset:0\n"
"ds_read_b64_tr_b16 v[104:105], v229 offset:8192\n"
"ds_read_b64_tr_b16 a[246:247], v237 offset:4104\n"
"v_mul_f32_dpp v23, v246, v23 row_newbcast:3 row_mask:0xf bank_mask:0xf\n"
"v_mfma_f32_16x16x32_bf16 v[156:159], v[180:183], a[128:131], v[196:199]\n"
"ds_read_b64_tr_b16 v[96:97], v229 offset:0\n"
"ds_read_b64_tr_b16 v[108:109], v233 offset:8192\n"
"v_mfma_f32_16x16x32_bf16 v[156:159], v[184:187], a[132:135], v[156:159]\n"
"ds_read_b64_tr_b16 a[198:199], v237 offset:8\n"
"ds_read_b64_tr_b16 a[240:241], v236 offset:4096\n"
"v_mfma_f32_16x16x32_bf16 v[156:159], v[188:191], a[136:139], v[156:159]\n"
"ds_read_b64_tr_b16 v[106:107], v229 offset:10240\n"
"ds_read_b64_tr_b16 a[234:235], v236 offset:10248\n"
"v_mfma_f32_16x16x32_bf16 v[156:159], v[192:195], a[140:143], v[156:159]\n"
"s_waitcnt lgkmcnt(15)\n"
"ds_read_b64_tr_b16 a[194:195], v236 offset:8\n"
"ds_read_b64_tr_b16 v[110:111], v233 offset:10240\n"
"v_mfma_f32_16x16x32_bf16 v[156:159], v[208:211], a[176:179], v[156:159]\n"
"ds_read_b64_tr_b16 a[192:193], v236 offset:0\n"
"ds_read_b64_tr_b16 a[200:201], v236 offset:2048\n"
"v_mfma_f32_16x16x32_bf16 v[156:159], v[212:215], a[180:183], v[156:159]\n"
"s_add_u32 s41, s95, 491520\n"
"ds_read_b64_tr_b16 v[102:103], v233 offset:2048\n"
"ds_read_b64_tr_b16 v[226:227], v233 offset:26624\n"
"v_mfma_f32_16x16x32_bf16 v[156:159], v[216:219], a[184:187], v[156:159]\n"
"s_add_u32 m0, s44, 49152\n"
"ds_read_b64_tr_b16 v[98:99], v229 offset:2048\n"
"v_xor_b32 v234, 0x1000, v230\n"
"v_mfma_f32_16x16x32_bf16 v[156:159], v[220:223], a[188:191], v[156:159]\n"
"buffer_load_dwordx4 v240, s[28:31], s41 offen lds\n"
"v_mfma_f32_16x16x32_bf16 v[172:175], v[220:223], a[252:255], v[196:199]\n"
"s_waitcnt lgkmcnt(11)\n"
"s_add_u32 s40, s41, s52\n"
"ds_read_b64_tr_b16 v[114:115], v229 offset:18432\n"
"ds_read_b64_tr_b16 v[116:117], v233 offset:16384\n"
"v_mfma_f32_16x16x32_bf16 v[172:175], v[216:219], a[248:251], v[172:175]\n"
"buffer_load_dwordx4 v240, s[28:31], s40 offen offset:1024 lds\n"
"v_mfma_f32_16x16x32_bf16 v[172:175], v[212:215], a[244:247], v[172:175]\n"
"s_waitcnt lgkmcnt(9)\n"
"s_add_u32 s40, s41, s53\n"
"v_min_f32 v254, v252, v250\n"
"v_sub_f32 v252, v254, v252\n"
"v_mfma_f32_16x16x32_bf16 v[172:175], v[208:211], a[240:243], v[172:175]\n"
"buffer_load_dwordx4 v240, s[28:31], s40 offen offset:2048 lds\n"
"v_mfma_f32_16x16x32_bf16 v[172:175], v[192:195], a[204:207], v[172:175]\n"
"s_waitcnt lgkmcnt(5)\n"
"ds_read_b64_tr_b16 v[120:121], v229 offset:24576\n"
"ds_read_b64_tr_b16 v[122:123], v229 offset:26624\n"
"v_mfma_f32_16x16x32_bf16 v[172:175], v[188:191], a[200:203], v[172:175]\n"
"v_exp_f32 v252, v252\n"
"v_mfma_f32_16x16x32_bf16 v[172:175], v[184:187], a[196:199], v[172:175]\n"
"s_add_u32 s40, s41, s54\n"
"ds_read_b64_tr_b16 v[224:225], v233 offset:24576\n"
"ds_read_b64_tr_b16 v[218:219], v230 offset:26624\n"
"v_mfma_f32_16x16x32_bf16 v[172:175], v[180:183], a[192:195], v[172:175]\n"
"s_waitcnt lgkmcnt(6)\n"
"buffer_load_dwordx4 v240, s[28:31], s40 offen offset:3072 lds\n"
"v_mfma_f32_16x16x32_bf16 v[128:131], v[96:99], a[96:99], v[196:199]\n"
"v_mul_f32 v253, v253, v252\n"
"v_mul_f32_dpp v84, v206, v84 row_newbcast:0 row_mask:0xf bank_mask:0xf\n"
"v_mfma_f32_16x16x32_bf16 v[128:131], v[100:103], a[100:103], v[128:131]\n"
"v_mul_f32_dpp v16, v246, v16 row_newbcast:0 row_mask:0xf bank_mask:0xf\n"
"v_mul_f32_dpp v20, v246, v20 row_newbcast:0 row_mask:0xf bank_mask:0xf\n"
"v_mfma_f32_16x16x32_bf16 v[128:131], v[104:107], a[104:107], v[128:131]\n"
"ds_read_b64_tr_b16 v[190:191], v230 offset:10240\n"
"v_sub_f32 v250, v254, v250\n"
"v_mfma_f32_16x16x32_bf16 v[128:131], v[108:111], a[108:111], v[128:131]\n"
"s_waitcnt lgkmcnt(5)\n"
"ds_read_b64_tr_b16 v[180:181], v230 offset:0\n"
"ds_read_b64_tr_b16 v[186:187], v234 offset:2048\n"
"v_mfma_f32_16x16x32_bf16 v[128:131], v[112:115], a[144:147], v[128:131]\n"
"s_add_u32 s21, s60, 32\n"
"ds_read_b64_tr_b16 a[238:239], v237 offset:10248\n"
"v_mul_f32_dpp v17, v246, v17 row_newbcast:1 row_mask:0xf bank_mask:0xf\n"
"v_mfma_f32_16x16x32_bf16 v[128:131], v[116:119], a[148:151], v[128:131]\n"
"s_waitcnt lgkmcnt(6)\n"
"s_mov_b32 s69, 1\n"
"v_exp_f32 v250, v250\n"
"v_mfma_f32_16x16x32_bf16 v[128:131], v[120:123], a[152:155], v[128:131]\n"
"s_waitcnt lgkmcnt(4)\n"
"v_bfe_u32 v201, v242, 2, 7\n"
"v_add_u32 v201, 0x100, v201\n"
"v_fmac_f32 v253, v251, v250\n"
"v_mfma_f32_16x16x32_bf16 v[128:131], v[224:227], a[156:159], v[128:131]\n"
"global_load_dword v200, v201, s[6:7] sc1\n"
"v_mfma_f32_16x16x32_bf16 v[144:147], v[224:227], a[172:175], v[196:199]\n"
"v_accvgpr_read_b32 v251, a211\n"
"v_mul_f32_dpp v80, v206, v80 row_newbcast:0 row_mask:0xf bank_mask:0xf\n"
"v_mfma_f32_16x16x32_bf16 v[144:147], v[120:123], a[168:171], v[144:147]\n"
"v_rcp_f32 v253, v253\n"
"v_mfma_f32_16x16x32_bf16 v[144:147], v[116:119], a[164:167], v[144:147]\n"
"ds_read_b64_tr_b16 a[216:217], v236 offset:8192\n"
"v_mul_f32 v252, v252, v253\n"
"v_mfma_f32_16x16x32_bf16 v[144:147], v[112:115], a[160:163], v[144:147]\n"
"s_nop 0\n"
"v_mov_b32_dpp v252, v252 row_ror:12 row_mask:0x2 bank_mask:0xf\n"
"v_mul_f32_dpp v55, v247, v55 row_newbcast:3 row_mask:0xf bank_mask:0xf\n"
"v_mfma_f32_16x16x32_bf16 v[144:147], v[108:111], a[124:127], v[144:147]\n"
"ds_read_b64_tr_b16 a[218:219], v236 offset:8200\n"
"v_mul_f32_dpp v86, v206, v86 row_newbcast:2 row_mask:0xf bank_mask:0xf\n"
"v_mfma_f32_16x16x32_bf16 v[144:147], v[104:107], a[120:123], v[144:147]\n"
"v_accvgpr_read_b32 v254, a208\n"
"v_mul_f32_dpp v81, v206, v81 row_newbcast:1 row_mask:0xf bank_mask:0xf\n"
"v_mfma_f32_16x16x32_bf16 v[144:147], v[100:103], a[116:119], v[144:147]\n"
"ds_read_b64_tr_b16 v[184:185], v234 offset:0\n"
"v_mul_f32_dpp v52, v247, v52 row_newbcast:0 row_mask:0xf bank_mask:0xf\n"
"v_mfma_f32_16x16x32_bf16 v[144:147], v[96:99], a[112:115], v[144:147]\n"
"ds_read_b64_tr_b16 v[222:223], v234 offset:26624\n"
"v_mul_f32 v250, v250, v253\n"
"v_mfma_f32_16x16x32_bf16 v[160:163], v[96:99], a[128:131], v[196:199]\n"
"s_nop 0\n"
"v_mov_b32_dpp v250, v250 row_ror:12 row_mask:0x2 bank_mask:0xf\n"
"v_mul_f32_dpp v82, v206, v82 row_newbcast:2 row_mask:0xf bank_mask:0xf\n"
"v_mfma_f32_16x16x32_bf16 v[160:163], v[100:103], a[132:135], v[160:163]\n"
"ds_read_b64_tr_b16 v[182:183], v230 offset:2048\n"
"v_mov_b32_dpp v250, v250 row_ror:8 row_mask:0x4 bank_mask:0xf\n"
"v_mfma_f32_16x16x32_bf16 v[160:163], v[104:107], a[136:139], v[160:163]\n"
"ds_read_b64_tr_b16 v[214:215], v234 offset:18432\n"
"v_mul_f32_dpp v49, v247, v49 row_newbcast:1 row_mask:0xf bank_mask:0xf\n"
"v_mfma_f32_16x16x32_bf16 v[160:163], v[108:111], a[140:143], v[160:163]\n"
"ds_read_b64_tr_b16 v[210:211], v230 offset:18432\n"
"ds_read_b64_tr_b16 v[212:213], v234 offset:16384\n"
"v_mfma_f32_16x16x32_bf16 v[160:163], v[112:115], a[176:179], v[160:163]\n"
"ds_read_b64_tr_b16 v[220:221], v234 offset:24576\n"
"v_mov_b32_dpp v250, v250 row_ror:4 row_mask:0x8 bank_mask:0xf\n"
"v_mfma_f32_16x16x32_bf16 v[160:163], v[116:119], a[180:183], v[160:163]\n"
"ds_read_b64_tr_b16 v[208:209], v230 offset:16384\n"
"v_mov_b32_dpp v252, v252 row_ror:8 row_mask:0x4 bank_mask:0xf\n"
"v_mfma_f32_16x16x32_bf16 v[160:163], v[120:123], a[184:187], v[160:163]\n"
"s_nop 0\n"
"v_mov_b32_dpp v252, v252 row_ror:4 row_mask:0x8 bank_mask:0xf\n"
"v_mul_f32_dpp v27, v250, v27 row_newbcast:3 row_mask:0xf bank_mask:0xf\n"
"v_mfma_f32_16x16x32_bf16 v[160:163], v[224:227], a[188:191], v[160:163]\n"
"ds_read_b64_tr_b16 v[188:189], v230 offset:8192\n"
"ds_read_b64_tr_b16 v[192:193], v234 offset:8192\n"
"ds_read_b64_tr_b16 v[194:195], v234 offset:10240\n"
"v_accvgpr_read_b32 v255, a209\n"
"v_mul_f32_dpp v25, v250, v25 row_newbcast:1 row_mask:0xf bank_mask:0xf\n"
"v_mul_f32_dpp v26, v250, v26 row_newbcast:2 row_mask:0xf bank_mask:0xf\n"
"v_xor_b32 v235, 0x1000, v231\n"
"v_mul_f32_dpp v56, v250, v56 row_newbcast:0 row_mask:0xf bank_mask:0xf\n"
"v_mul_f32_dpp v59, v250, v59 row_newbcast:3 row_mask:0xf bank_mask:0xf\n"
"v_mfma_f32_16x16x32_bf16 v[176:179], v[224:227], a[252:255], v[196:199]\n"
"v_mul_f32_dpp v24, v250, v24 row_newbcast:0 row_mask:0xf bank_mask:0xf\n"
"v_accvgpr_read_b32 v253, a210\n"
"v_mfma_f32_16x16x32_bf16 v[176:179], v[120:123], a[248:251], v[176:179]\n"
"ds_read_b64_tr_b16 v[216:217], v230 offset:24576\n"
"v_mul_f32_dpp v60, v250, v60 row_newbcast:0 row_mask:0xf bank_mask:0xf\n"
"v_mfma_f32_16x16x32_bf16 v[176:179], v[116:119], a[244:247], v[176:179]\n"
"v_fmac_f32_dpp v27, v252, v251 row_newbcast:3 row_mask:0xf bank_mask:0xf\n"
"v_accvgpr_read_b32 v251, a215\n"
"v_mfma_f32_16x16x32_bf16 v[176:179], v[112:115], a[240:243], v[176:179]\n"
"ds_read_b64_tr_b16 v[114:115], v231 offset:18432\n"
"v_mul_f32_dpp v31, v250, v31 row_newbcast:3 row_mask:0xf bank_mask:0xf\n"
"v_mfma_f32_16x16x32_bf16 v[176:179], v[108:111], a[204:207], v[176:179]\n"
"ds_read_b64_tr_b16 v[122:123], v231 offset:26624\n"
"v_mul_f32_dpp v87, v206, v87 row_newbcast:3 row_mask:0xf bank_mask:0xf\n"
"v_mfma_f32_16x16x32_bf16 v[176:179], v[104:107], a[200:203], v[176:179]\n"
"v_fmac_f32_dpp v24, v252, v254 row_newbcast:0 row_mask:0xf bank_mask:0xf\n"
"v_mul_f32_dpp v57, v250, v57 row_newbcast:1 row_mask:0xf bank_mask:0xf\n"
"v_mfma_f32_16x16x32_bf16 v[176:179], v[100:103], a[196:199], v[176:179]\n"
"v_fmac_f32_dpp v25, v252, v255 row_newbcast:1 row_mask:0xf bank_mask:0xf\n"
"v_accvgpr_read_b32 v254, a212\n"
"v_mfma_f32_16x16x32_bf16 v[176:179], v[96:99], a[192:195], v[176:179]\n"
"s_waitcnt lgkmcnt(3)\n"
"ds_read_b64_tr_b16 v[106:107], v231 offset:10240\n"
"ds_read_b64_tr_b16 v[120:121], v231 offset:24576\n"
"v_accvgpr_read_b32 v255, a213\n"
"v_mul_f32_dpp v29, v250, v29 row_newbcast:1 row_mask:0xf bank_mask:0xf\n"
"v_fmac_f32_dpp v31, v252, v251 row_newbcast:3 row_mask:0xf bank_mask:0xf\n"
"v_mul_f32_dpp v50, v247, v50 row_newbcast:2 row_mask:0xf bank_mask:0xf\n"
"v_accvgpr_read_b32 v251, a227\n"
"v_mul_f32_dpp v61, v250, v61 row_newbcast:1 row_mask:0xf bank_mask:0xf\n"
"v_mfma_f32_16x16x32_bf16 v[132:135], v[180:183], a[96:99], v[196:199]\n"
"v_mul_f32_dpp v48, v247, v48 row_newbcast:0 row_mask:0xf bank_mask:0xf\n"
"v_mul_f32_dpp v62, v250, v62 row_newbcast:2 row_mask:0xf bank_mask:0xf\n"
"v_mfma_f32_16x16x32_bf16 v[132:135], v[184:187], a[100:103], v[132:135]\n"
"ds_read_b64_tr_b16 a[220:221], v237 offset:8192\n"
"v_fmac_f32_dpp v26, v252, v253 row_newbcast:2 row_mask:0xf bank_mask:0xf\n"
"v_mfma_f32_16x16x32_bf16 v[132:135], v[188:191], a[104:107], v[132:135]\n"
"ds_read_b64_tr_b16 v[96:97], v231 offset:0\n"
"v_mul_f32_dpp v30, v250, v30 row_newbcast:2 row_mask:0xf bank_mask:0xf\n"
"v_mfma_f32_16x16x32_bf16 v[132:135], v[192:195], a[108:111], v[132:135]\n"
"v_mul_f32_dpp v51, v247, v51 row_newbcast:3 row_mask:0xf bank_mask:0xf\n"
"v_fmac_f32_dpp v59, v252, v251 row_newbcast:3 row_mask:0xf bank_mask:0xf\n"
"v_mfma_f32_16x16x32_bf16 v[132:135], v[208:211], a[144:147], v[132:135]\n"
"s_waitcnt vmcnt(0)\n"
"ds_read_b64_tr_b16 v[108:109], v235 offset:8192\n"
"v_mul_f32_dpp v28, v250, v28 row_newbcast:0 row_mask:0xf bank_mask:0xf\n"
"v_mfma_f32_16x16x32_bf16 v[132:135], v[212:215], a[148:151], v[132:135]\n"
"s_waitcnt lgkmcnt(5)\n"
"v_subrev_u32 v200, s21, v200\n"
"ds_read_b64_tr_b16 a[232:233], v236 offset:10240\n"
"v_accvgpr_read_b32 v253, a214\n"
"v_fmac_f32_dpp v28, v252, v254 row_newbcast:0 row_mask:0xf bank_mask:0xf\n"
"v_fmac_f32_dpp v29, v252, v255 row_newbcast:1 row_mask:0xf bank_mask:0xf\n"
"v_accvgpr_read_b32 v254, a224\n"
"v_accvgpr_read_b32 v255, a225\n"
"v_accvgpr_read_b32 v251, a231\n"
"v_mfma_f32_16x16x32_bf16 v[132:135], v[216:219], a[152:155], v[132:135]\n"
"v_cmp_lt_u32 vcc, s69, v200\n"
"ds_read_b64_tr_b16 v[226:227], v235 offset:26624\n"
"v_mfma_f32_16x16x32_bf16 v[132:135], v[220:223], a[156:159], v[132:135]\n"
"ds_read_b64_tr_b16 v[102:103], v235 offset:2048\n"
"ds_read_b64_tr_b16 v[110:111], v235 offset:10240\n"
"v_fmac_f32_dpp v30, v252, v253 row_newbcast:2 row_mask:0xf bank_mask:0xf\n"
"v_cvt_pk_bf16_f32 v24, v24, v25\n"
"v_cvt_pk_bf16_f32 v25, v26, v27\n"
"v_accvgpr_read_b32 v253, a226\n"
"v_mul_f32_dpp v58, v250, v58 row_newbcast:2 row_mask:0xf bank_mask:0xf\n"
"v_fmac_f32_dpp v57, v252, v255 row_newbcast:1 row_mask:0xf bank_mask:0xf\n"
"v_accvgpr_read_b32 v255, a229\n"
"s_cbranch_vccnz LGV_%=\nLGVK_%=:\n"
"v_mfma_f32_16x16x32_bf16 v[148:151], v[220:223], a[172:175], v[196:199]\n"
"v_fmac_f32_dpp v56, v252, v254 row_newbcast:0 row_mask:0xf bank_mask:0xf\n"
"v_mul_f32_dpp v63, v250, v63 row_newbcast:3 row_mask:0xf bank_mask:0xf\n"
"v_mfma_f32_16x16x32_bf16 v[148:151], v[216:219], a[168:171], v[148:151]\n"
"v_add_u32_dpp v200, v200, v200 quad_perm:[1,0,3,2] row_mask:0xf bank_mask:0xf\n"
"ds_read_b64_tr_b16 v[104:105], v231 offset:8192\n"
"ds_read_b64_tr_b16 v[112:113], v231 offset:16384\n"
"v_cvt_pk_bf16_f32 v26, v28, v29\n"
"v_cvt_pk_bf16_f32 v27, v30, v31\n"
"v_fmac_f32_dpp v58, v252, v253 row_newbcast:2 row_mask:0xf bank_mask:0xf\n"
"v_accvgpr_read_b32 v253, a230\n"
"v_fmac_f32_dpp v61, v252, v255 row_newbcast:1 row_mask:0xf bank_mask:0xf\n"
"v_cvt_pk_bf16_f32 v56, v56, v57\n"
"v_cvt_pk_bf16_f32 v57, v58, v59\n"
"v_mfma_f32_16x16x32_bf16 v[148:151], v[212:215], a[164:167], v[148:151]\n"
"ds_read_b64_tr_b16 a[236:237], v237 offset:10240\n"
"v_cvt_pk_bf16_f32 v16, v16, v17\n"
"v_mfma_f32_16x16x32_bf16 v[148:151], v[208:211], a[160:163], v[148:151]\n"
"v_add_u32_dpp v200, v200, v200 quad_perm:[2,3,0,1] row_mask:0xf bank_mask:0xf\n"
"v_accvgpr_read_b32 v254, a228\n"
"v_mfma_f32_16x16x32_bf16 v[148:151], v[192:195], a[124:127], v[148:151]\n"
"ds_read_b64_tr_b16 v[98:99], v231 offset:2048\n"
"v_fmac_f32_dpp v62, v252, v253 row_newbcast:2 row_mask:0xf bank_mask:0xf\n"
"v_mfma_f32_16x16x32_bf16 v[148:151], v[188:191], a[120:123], v[148:151]\n"
"v_cmp_lt_u32 vcc, 1, v200\n"
"ds_write_b128 v243, v[24:27] offset:0\n"
"v_cvt_pk_bf16_f32 v48, v48, v49\n"
"v_fmac_f32_dpp v60, v252, v254 row_newbcast:0 row_mask:0xf bank_mask:0xf\n"
"v_fmac_f32_dpp v63, v252, v251 row_newbcast:3 row_mask:0xf bank_mask:0xf\n"
"v_cvt_pk_bf16_f32 v59, v62, v63\n"
"v_mfma_f32_16x16x32_bf16 v[148:151], v[184:187], a[116:119], v[148:151]\n"
"ds_read_b64_tr_b16 v[100:101], v235 offset:0\n"
"v_cvt_pk_bf16_f32 v58, v60, v61\n"
"v_mfma_f32_16x16x32_bf16 v[148:151], v[180:183], a[112:115], v[148:151]\n"
"ds_write_b128 v243, v[56:59] offset:2048\n"
"v_mul_f32_dpp v19, v246, v19 row_newbcast:3 row_mask:0xf bank_mask:0xf\n"
"v_mul_f32_dpp v85, v206, v85 row_newbcast:1 row_mask:0xf bank_mask:0xf\n"
"v_mfma_f32_16x16x32_bf16 v[164:167], v[180:183], a[128:131], v[196:199]\n"
"s_mov_b32 s47, 0\n"
"v_xor_b32 v249, s75, v249\n"
"v_cvt_pk_bf16_f32 v80, v80, v81\n"
"v_mfma_f32_16x16x32_bf16 v[164:167], v[184:187], a[132:135], v[164:167]\n"
"s_mov_b32 s89, vcc_lo\n"
"ds_read_b64_tr_b16 v[116:117], v235 offset:16384\n"
"ds_read_b64_tr_b16 v[118:119], v235 offset:18432\n"
"v_mfma_f32_16x16x32_bf16 v[164:167], v[188:191], a[136:139], v[164:167]\n"
"s_lshr_b32 s90, s89, s93\n"
"v_cvt_pk_bf16_f32 v49, v50, v51\n"
"v_mul_f32_dpp v83, v206, v83 row_newbcast:3 row_mask:0xf bank_mask:0xf\n"
"v_mfma_f32_16x16x32_bf16 v[164:167], v[192:195], a[140:143], v[164:167]\n"
"s_and_b32 s90, s90, 1\n"
"v_mul_f32_dpp v22, v246, v22 row_newbcast:2 row_mask:0xf bank_mask:0xf\n"
"v_cvt_pk_bf16_f32 v81, v82, v83\n"
"v_mfma_f32_16x16x32_bf16 v[164:167], v[208:211], a[176:179], v[164:167]\n"
"s_lshl_b32 s16, s61, 7\n"
"v_xor_b32 v248, s75, v248\n"
"v_cvt_pk_bf16_f32 v83, v86, v87\n"
"v_mfma_f32_16x16x32_bf16 v[164:167], v[212:215], a[180:183], v[164:167]\n"
"s_add_u32 s90, s90, s16\n"
"ds_read_b64_tr_b16 v[224:225], v235 offset:24576\n"
"v_mul_f32_dpp v53, v247, v53 row_newbcast:1 row_mask:0xf bank_mask:0xf\n"
"v_mfma_f32_16x16x32_bf16 v[164:167], v[216:219], a[184:187], v[164:167]\n"
"s_waitcnt vmcnt(63) lgkmcnt(3)\n"
"s_lshl_b32 s90, s90, 13\n"
"v_cvt_pk_bf16_f32 v82, v84, v85\n"
"v_mfma_f32_16x16x32_bf16 v[164:167], v[220:223], a[188:191], v[164:167]\n"
"s_barrier\n"
"s_lshl_b32 s16, s95, 9\n"
"ds_write_b128 v248, v[80:83] offset:51200\n"
"v_mfma_f32_16x16x32_bf16 v[136:139], v[96:99], a[96:99], v[196:199]\n"
"s_add_u32 s16, s16, s90\n"
"v_mul_f32_dpp v54, v247, v54 row_newbcast:2 row_mask:0xf bank_mask:0xf\n"
"v_cvt_pk_bf16_f32 v51, v54, v55\n"
"v_mfma_f32_16x16x32_bf16 v[136:139], v[100:103], a[100:103], v[136:139]\n"
"s_add_u32 s17, s16, 4096\n"
"ds_read_b64_tr_b16 v[212:213], v232 offset:49152\n"
"v_mul_f32_dpp v21, v246, v21 row_newbcast:1 row_mask:0xf bank_mask:0xf\n"
"v_mfma_f32_16x16x32_bf16 v[136:139], v[104:107], a[104:107], v[136:139]\n"
"s_mov_b32 s72, s61\n"
"ds_read_b64_tr_b16 v[180:181], v228 offset:32768\n"
"v_cvt_pk_bf16_f32 v50, v52, v53\n"
"v_mfma_f32_16x16x32_bf16 v[136:139], v[108:111], a[108:111], v[136:139]\n"
"s_mov_b32 s21, s60\n"
"ds_read_b64_tr_b16 a[42:43], v249 offset:51208\n"
"v_mul_f32_dpp v18, v246, v18 row_newbcast:2 row_mask:0xf bank_mask:0xf\n"
"v_mfma_f32_16x16x32_bf16 v[136:139], v[112:115], a[144:147], v[136:139]\n"
"s_waitcnt lgkmcnt(5)\n"
"s_mov_b32 s18, s77\n"
"ds_read_b64_tr_b16 v[182:183], v228 offset:34816\n"
"v_cvt_pk_bf16_f32 v17, v18, v19\n"
"v_mfma_f32_16x16x32_bf16 v[136:139], v[116:119], a[148:151], v[136:139]\n"
"s_mov_b32 s19, s77\n"
"ds_read_b64_tr_b16 a[214:215], v237 offset:12296\n"
"v_cvt_pk_bf16_f32 v19, v22, v23\n"
"v_mfma_f32_16x16x32_bf16 v[136:139], v[120:123], a[152:155], v[136:139]\n"
"s_waitcnt lgkmcnt(6)\n"
"s_mov_b32 s69, -1\n"
"ds_read_b64_tr_b16 a[40:41], v249 offset:51200\n"
"v_cvt_pk_bf16_f32 v18, v20, v21\n"
"v_mfma_f32_16x16x32_bf16 v[136:139], v[224:227], a[156:159], v[136:139]\n"
"s_mul_i32 s88, s61, 0x10011111\n"
"ds_write_b128 v248, v[16:19] offset:49152\n"
"ds_write_b128 v248, v[48:51] offset:50176\n"
"v_mfma_f32_16x16x32_bf16 v[152:155], v[224:227], a[172:175], v[196:199]\n"
"s_add_u32 s88, s88, 484675\n"
"ds_read_b64_tr_b16 a[8:9], v249 offset:49152\n"
"ds_read_b64_tr_b16 a[10:11], v249 offset:49160\n"
"v_mfma_f32_16x16x32_bf16 v[152:155], v[120:123], a[168:171], v[152:155]\n"
"s_and_b32 s88, s88, 0x70077777\n"
"ds_read_b64_tr_b16 a[24:25], v249 offset:50176\n"
"ds_read_b64_tr_b16 a[26:27], v249 offset:50184\n"
"v_mfma_f32_16x16x32_bf16 v[152:155], v[116:119], a[164:167], v[152:155]\n"
"s_or_b32 s88, s88, 0x9800000\n"
"ds_read_b64_tr_b16 v[220:221], v232 offset:57344\n"
"v_mov_b64 v[90:91], 0\n"
"v_mfma_f32_16x16x32_bf16 v[152:155], v[112:115], a[160:163], v[152:155]\n"
"s_mov_b32 s84, s6\n"
"ds_read_b64_tr_b16 v[184:185], v232 offset:32768\n"
"ds_read_b64_tr_b16 v[192:193], v232 offset:40960\n"
"v_mfma_f32_16x16x32_bf16 v[152:155], v[108:111], a[124:127], v[152:155]\n"
"s_and_b32 s85, s7, 0xffff\n"
"ds_read_b64_tr_b16 v[208:209], v228 offset:49152\n"
"v_mov_b64 v[34:35], s[22:23]\n"
"v_mfma_f32_16x16x32_bf16 v[152:155], v[104:107], a[120:123], v[152:155]\n"
"s_mov_b32 s86, 128\n"
"ds_read_b64_tr_b16 v[186:187], v232 offset:34816\n"
"ds_read_b64_tr_b16 v[216:217], v228 offset:57344\n"
"v_mfma_f32_16x16x32_bf16 v[152:155], v[100:103], a[116:119], v[152:155]\n"
"s_mov_b32 s87, 131072\n"
"ds_read_b64_tr_b16 v[188:189], v228 offset:40960\n"
"v_mov_b64 v[30:31], s[22:23]\n"
"v_mfma_f32_16x16x32_bf16 v[152:155], v[96:99], a[112:115], v[152:155]\n"
"ds_read_b64_tr_b16 a[224:225], v236 offset:14336\n"
"v_mov_b64 v[6:7], s[22:23]\n"
"v_mfma_f32_16x16x32_bf16 v[168:171], v[96:99], a[128:131], v[196:199]\n"
"ds_read_b64_tr_b16 v[190:191], v228 offset:43008\n"
"ds_read_b64_tr_b16 v[194:195], v232 offset:43008\n"
"v_mfma_f32_16x16x32_bf16 v[168:171], v[100:103], a[132:135], v[168:171]\n"
"ds_read_b64_tr_b16 v[210:211], v228 offset:51200\n"
"v_bfe_u32 v254, v242, 4, 4\n"
"v_mfma_f32_16x16x32_bf16 v[168:171], v[104:107], a[136:139], v[168:171]\n"
"ds_read_b64_tr_b16 v[218:219], v228 offset:59392\n"
"ds_read_b64_tr_b16 v[222:223], v232 offset:59392\n"
"v_mfma_f32_16x16x32_bf16 v[168:171], v[108:111], a[140:143], v[168:171]\n"
"s_waitcnt lgkmcnt(10)\n"
"ds_read_b64_tr_b16 a[208:209], v236 offset:12288\n"
"ds_read_b64_tr_b16 v[214:215], v232 offset:51200\n"
"v_mfma_f32_16x16x32_bf16 v[168:171], v[112:115], a[176:179], v[168:171]\n"
"ds_read_b64_tr_b16 v[104:105], v229 offset:40960\n"
"v_mov_b64 v[244:245], 0\n"
"v_mfma_f32_16x16x32_bf16 v[168:171], v[116:119], a[180:183], v[168:171]\n"
"ds_read_b64_tr_b16 v[100:101], v233 offset:32768\n"
"ds_read_b64_tr_b16 v[112:113], v229 offset:49152\n"
"v_mfma_f32_16x16x32_bf16 v[168:171], v[120:123], a[184:187], v[168:171]\n"
"s_add_u32 s41, s70, 327680\n"
"s_add_u32 m0, s71, 32768\n"
"ds_read_b64_tr_b16 a[210:211], v236 offset:12296\n"
"v_mfma_f32_16x16x32_bf16 v[168:171], v[224:227], a[188:191], v[168:171]\n"
"buffer_load_dwordx4 v240, s[28:31], s41 offen lds\n"
"v_mfma_f32_16x16x32_bf16 v[124:127], v[180:183], a[48:51], v[124:127]\n"
"s_waitcnt lgkmcnt(9)\n"
"v_mul_u32_u24 v254, 2560, v254\n"
"v_mfma_f32_16x16x32_bf16 v[124:127], v[184:187], a[52:55], v[124:127]\n"
"v_bfe_u32 v255, v242, 8, 1\n"
"v_mov_b64 v[44:45], s[22:23]\n"
"v_mfma_f32_16x16x32_bf16 v[124:127], v[188:191], a[56:59], v[124:127]\n"
"s_add_u32 s40, s41, s52\n"
"ds_read_b64_tr_b16 a[212:213], v237 offset:12288\n"
"ds_read_b64_tr_b16 a[226:227], v236 offset:14344\n"
"v_mfma_f32_16x16x32_bf16 v[124:127], v[192:195], a[60:63], v[124:127]\n"
"s_waitcnt lgkmcnt(6)\n"
"buffer_load_dwordx4 v240, s[28:31], s40 offen offset:1024 lds\n"
"v_mfma_f32_16x16x32_bf16 v[124:127], v[208:211], a[0:3], v[124:127]\n"
"v_mfma_f32_16x16x32_bf16 v[124:127], v[212:215], a[4:7], v[124:127]\n"
"v_lshl_add_u32 v254, v255, 5, v254\n"
"v_mov_b64 v[28:29], s[22:23]\n"
"v_mfma_f32_16x16x32_bf16 v[124:127], v[216:219], a[8:11], v[124:127]\n"
"s_add_u32 s40, s41, s53\n"
"ds_read_b64_tr_b16 v[96:97], v229 offset:32768\n"
"ds_read_b64_tr_b16 v[114:115], v229 offset:51200\n"
"v_mfma_f32_16x16x32_bf16 v[124:127], v[220:223], a[12:15], v[124:127]\n"
"buffer_load_dwordx4 v240, s[28:31], s40 offen offset:2048 lds\n"
"v_mfma_f32_16x16x32_bf16 v[140:143], v[220:223], a[28:31], v[140:143]\n"
"v_and_b32 v243, 0x3f0, v242\n"
"v_mfma_f32_16x16x32_bf16 v[140:143], v[216:219], a[24:27], v[140:143]\n"
"v_mov_b64 v[10:11], s[22:23]\n"
"v_mov_b64 v[18:19], s[22:23]\n"
"v_mfma_f32_16x16x32_bf16 v[140:143], v[212:215], a[20:23], v[140:143]\n"
"ds_read_b64_tr_b16 a[228:229], v237 offset:14336\n"
"ds_read_b64_tr_b16 v[118:119], v233 offset:51200\n"
"v_mfma_f32_16x16x32_bf16 v[140:143], v[208:211], a[16:19], v[140:143]\n"
"ds_read_b64_tr_b16 v[226:227], v233 offset:59392\n"
"v_mfma_f32_16x16x32_bf16 v[140:143], v[192:195], a[76:79], v[140:143]\n"
"ds_read_b64_tr_b16 v[102:103], v233 offset:34816\n"
"v_mov_b64 v[246:247], 0\n"
"v_mfma_f32_16x16x32_bf16 v[140:143], v[188:191], a[72:75], v[140:143]\n"
"ds_read_b64_tr_b16 a[230:231], v237 offset:14344\n"
"v_mfma_f32_16x16x32_bf16 v[140:143], v[184:187], a[68:71], v[140:143]\n"
"s_add_u32 s40, s41, s54\n"
"ds_read_b64_tr_b16 a[222:223], v237 offset:8200\n"
"ds_read_b64_tr_b16 v[116:117], v233 offset:49152\n"
"v_mfma_f32_16x16x32_bf16 v[140:143], v[180:183], a[64:67], v[140:143]\n"
"buffer_load_dwordx4 v240, s[28:31], s40 offen offset:3072 lds\n"
"v_mfma_f32_16x16x32_bf16 v[156:159], v[180:183], a[80:83], v[156:159]\n"
"s_add_u32 s41, s70, 491520\n"
"s_add_u32 m0, s71, 49152\n"
"v_xor_b32 v249, s75, v249\n"
"v_mfma_f32_16x16x32_bf16 v[156:159], v[184:187], a[84:87], v[156:159]\n"
"buffer_load_dwordx4 v240, s[28:31], s41 offen lds\n"
"v_mfma_f32_16x16x32_bf16 v[156:159], v[188:191], a[88:91], v[156:159]\n"
"v_bfe_u32 v237, v242, 4, 3\n"
"v_xor_b32 v232, s75, v232\n"
"v_mfma_f32_16x16x32_bf16 v[156:159], v[192:195], a[92:95], v[156:159]\n"
"v_mov_b64 v[38:39], s[22:23]\n"
"v_mfma_f32_16x16x32_bf16 v[156:159], v[208:211], a[32:35], v[156:159]\n"
"ds_read_b64_tr_b16 v[106:107], v229 offset:43008\n"
"ds_read_b64_tr_b16 v[108:109], v233 offset:40960\n"
"v_mfma_f32_16x16x32_bf16 v[156:159], v[212:215], a[36:39], v[156:159]\n"
"ds_read_b64_tr_b16 v[98:99], v229 offset:34816\n"
"v_subrev_f32 v126, s22, v126\n"
"v_mfma_f32_16x16x32_bf16 v[156:159], v[216:219], a[40:43], v[156:159]\n"
"s_add_u32 s40, s41, s52\n"
"ds_read_b64_tr_b16 v[110:111], v233 offset:43008\n"
"ds_read_b64_tr_b16 v[224:225], v233 offset:57344\n"
"v_mfma_f32_16x16x32_bf16 v[156:159], v[220:223], a[44:47], v[156:159]\n"
"s_waitcnt lgkmcnt(5)\n"
"buffer_load_dwordx4 v240, s[28:31], s40 offen offset:1024 lds\n"
"v_mfma_f32_16x16x32_bf16 v[172:175], v[220:223], a[228:231], v[172:175]\n"
"s_add_u32 s40, s41, s53\n"
"v_mov_b64 v[24:25], s[22:23]\n"
"v_subrev_f32 v124, s22, v124\n"
"v_mfma_f32_16x16x32_bf16 v[172:175], v[216:219], a[224:227], v[172:175]\n"
"buffer_load_dwordx4 v240, s[28:31], s40 offen offset:2048 lds\n"
"v_mfma_f32_16x16x32_bf16 v[172:175], v[212:215], a[212:215], v[172:175]\n"
"v_bfe_u32 v255, v242, 9, 1\n"
"v_subrev_f32 v143, s22, v143\n"
"v_mfma_f32_16x16x32_bf16 v[172:175], v[208:211], a[208:211], v[172:175]\n"
"v_lshl_add_u32 v254, v255, 4, v254\n"
"v_subrev_f32 v127, s22, v127\n"
"v_mfma_f32_16x16x32_bf16 v[172:175], v[192:195], a[236:239], v[172:175]\n"
"s_add_u32 s40, s41, s54\n"
"ds_read_b64_tr_b16 v[120:121], v229 offset:57344\n"
"ds_read_b64_tr_b16 v[122:123], v229 offset:59392\n"
"v_mfma_f32_16x16x32_bf16 v[172:175], v[188:191], a[232:235], v[172:175]\n"
"buffer_load_dwordx4 v240, s[28:31], s40 offen offset:3072 lds\n"
"v_mfma_f32_16x16x32_bf16 v[172:175], v[184:187], a[220:223], v[172:175]\n"
"s_lshl_b32 s62, s70, 9\n"
"v_mov_b64 v[48:49], s[22:23]\n"
"v_subrev_f32 v141, s22, v141\n"
"v_mfma_f32_16x16x32_bf16 v[172:175], v[180:183], a[216:219], v[172:175]\n"
"s_waitcnt lgkmcnt(2)\n"
"s_and_b32 s70, s88, 15\n"
"v_mov_b64 v[92:93], 0\n"
"v_subrev_f32 v140, s22, v140\n"
"v_mfma_f32_16x16x32_bf16 v[128:131], v[96:99], a[48:51], v[128:131]\n"
"s_lshr_b32 s88, s88, 4\n"
"ds_read_b64_tr_b16 v[192:193], v234 offset:40960\n"
"v_mfma_f32_16x16x32_bf16 v[128:131], v[100:103], a[52:55], v[128:131]\n"
"s_lshl_b32 s70, s70, 7\n"
"v_mov_b64 v[42:43], s[22:23]\n"
"v_mfma_f32_16x16x32_bf16 v[128:131], v[104:107], a[56:59], v[128:131]\n"
"s_xor_b32 s71, s71, s75\n"
"ds_read_b64_tr_b16 v[182:183], v230 offset:34816\n"
"v_subrev_f32 v172, s22, v172\n"
"v_mfma_f32_16x16x32_bf16 v[128:131], v[108:111], a[60:63], v[128:131]\n"
"v_lshlrev_b32_e64 v201, 2, s72\n"
"ds_read_b64_tr_b16 v[220:221], v234 offset:57344\n"
"v_subrev_f32 v125, s22, v125\n"
"v_mfma_f32_16x16x32_bf16 v[128:131], v[112:115], a[0:3], v[128:131]\n"
"global_load_dword v200, v201, s[6:7] sc1\n"
"v_mfma_f32_16x16x32_bf16 v[128:131], v[116:119], a[4:7], v[128:131]\n"
"s_waitcnt lgkmcnt(3)\n"
"v_cvt_pk_bf16_f32 v124, v124, v125\n"
"v_cvt_pk_bf16_f32 v140, v140, v141\n"
"v_mfma_f32_16x16x32_bf16 v[128:131], v[120:123], a[8:11], v[128:131]\n"
"v_xor_b32 v228, s75, v228\n"
"v_mfma_f32_16x16x32_bf16 v[128:131], v[224:227], a[12:15], v[128:131]\n"
"ds_read_b64_tr_b16 v[180:181], v230 offset:32768\n"
"v_mfma_f32_16x16x32_bf16 v[144:147], v[224:227], a[28:31], v[144:147]\n"
"v_add_u32 v253, s99, v254\n"
"v_cvt_pk_bf16_f32 v125, v126, v127\n"
"v_mfma_f32_16x16x32_bf16 v[144:147], v[120:123], a[24:27], v[144:147]\n"
"ds_read_b64_tr_b16 v[184:185], v234 offset:32768\n"
"v_mov_b32 v241, s60\n"
"v_mfma_f32_16x16x32_bf16 v[144:147], v[116:119], a[20:23], v[144:147]\n"
"v_mov_b64 v[22:23], s[22:23]\n"
"v_subrev_f32 v131, s22, v131\n"
"v_mfma_f32_16x16x32_bf16 v[144:147], v[112:115], a[16:19], v[144:147]\n"
"ds_read_b64_tr_b16 v[188:189], v230 offset:40960\n"
"v_subrev_f32 v159, s22, v159\n"
"v_mfma_f32_16x16x32_bf16 v[144:147], v[108:111], a[76:79], v[144:147]\n"
"ds_read_b64_tr_b16 v[190:191], v230 offset:43008\n"
"v_subrev_f32 v128, s22, v128\n"
"v_mfma_f32_16x16x32_bf16 v[144:147], v[104:107], a[72:75], v[144:147]\n"
"v_add_u32 v236, s94, v243\n"
"v_mfma_f32_16x16x32_bf16 v[144:147], v[100:103], a[68:71], v[144:147]\n"
"ds_read_b64_tr_b16 v[194:195], v234 offset:43008\n"
"v_max_u32 v237, 1, v237\n"
"v_mfma_f32_16x16x32_bf16 v[144:147], v[96:99], a[64:67], v[144:147]\n"
"v_mov_b64 v[40:41], s[22:23]\n"
"v_subrev_f32 v175, s22, v175\n"
"v_mfma_f32_16x16x32_bf16 v[160:163], v[96:99], a[80:83], v[160:163]\n"
"ds_read_b64_tr_b16 v[208:209], v230 offset:49152\n"
"v_mov_b64 v[0:1], s[22:23]\n"
"v_mfma_f32_16x16x32_bf16 v[160:163], v[100:103], a[84:87], v[160:163]\n"
"ds_read_b64_tr_b16 v[222:223], v234 offset:59392\n"
"v_subrev_f32 v130, s22, v130\n"
"v_mfma_f32_16x16x32_bf16 v[160:163], v[104:107], a[88:91], v[160:163]\n"
"v_subrev_f32 v129, s22, v129\n"
"v_cvt_pk_bf16_f32 v127, v130, v131\n"
"v_mfma_f32_16x16x32_bf16 v[160:163], v[108:111], a[92:95], v[160:163]\n"
"ds_read_b64_tr_b16 v[214:215], v234 offset:51200\n"
"v_subrev_f32 v142, s22, v142\n"
"v_mfma_f32_16x16x32_bf16 v[160:163], v[112:115], a[32:35], v[160:163]\n"
"ds_read_b64_tr_b16 v[210:211], v230 offset:51200\n"
"v_cvt_pk_bf16_f32 v126, v128, v129\n"
"v_mfma_f32_16x16x32_bf16 v[160:163], v[116:119], a[36:39], v[160:163]\n"
"v_mov_b64 v[8:9], s[22:23]\n"
"v_subrev_f32 v174, s22, v174\n"
"v_mfma_f32_16x16x32_bf16 v[160:163], v[120:123], a[40:43], v[160:163]\n"
"ds_read_b64_tr_b16 v[186:187], v234 offset:34816\n"
"v_lshl_add_u32 v243, s44, 2, v243\n"
"v_mfma_f32_16x16x32_bf16 v[160:163], v[224:227], a[44:47], v[160:163]\n"
"buffer_store_dwordx4 v[124:127], v243, s[32:35], s16 offen sc1\n"
"v_subrev_f32 v157, s22, v157\n"
"v_mfma_f32_16x16x32_bf16 v[176:179], v[224:227], a[228:231], v[176:179]\n"
"ds_read_b64_tr_b16 v[212:213], v234 offset:49152\n"
"v_subrev_f32 v144, s22, v144\n"
"v_mfma_f32_16x16x32_bf16 v[176:179], v[120:123], a[224:227], v[176:179]\n"
"v_mov_b64 v[88:89], 0\n"
"v_subrev_f32 v173, s22, v173\n"
"v_mfma_f32_16x16x32_bf16 v[176:179], v[116:119], a[212:215], v[176:179]\n"
"ds_read_b64_tr_b16 v[216:217], v230 offset:57344\n"
"v_mov_b64 v[4:5], s[22:23]\n"
"v_mfma_f32_16x16x32_bf16 v[176:179], v[112:115], a[208:211], v[176:179]\n"
"ds_read_b64_tr_b16 v[218:219], v230 offset:59392\n"
"v_cvt_pk_bf16_f32 v172, v172, v173\n"
"v_mfma_f32_16x16x32_bf16 v[176:179], v[108:111], a[236:239], v[176:179]\n"
"ds_read_b64_tr_b16 v[118:119], v235 offset:51200\n"
"v_mfma_f32_16x16x32_bf16 v[176:179], v[104:107], a[232:235], v[176:179]\n"
"v_subrev_f32 v158, s22, v158\n"
"v_subrev_f32 v145, s22, v145\n"
"v_mfma_f32_16x16x32_bf16 v[176:179], v[100:103], a[220:223], v[176:179]\n"
"ds_read_b64_tr_b16 v[120:121], v231 offset:57344\n"
"v_cvt_pk_bf16_f32 v141, v142, v143\n"
"v_mfma_f32_16x16x32_bf16 v[176:179], v[96:99], a[216:219], v[176:179]\n"
"s_waitcnt lgkmcnt(5)\n"
"v_mov_b64 v[54:55], s[22:23]\n"
"v_cvt_pk_bf16_f32 v142, v144, v145\n"
"v_mfma_f32_16x16x32_bf16 v[132:135], v[180:183], a[48:51], v[132:135]\n"
"ds_read_b64_tr_b16 v[104:105], v231 offset:40960\n"
"v_subrev_f32 v147, s22, v147\n"
"v_mfma_f32_16x16x32_bf16 v[132:135], v[184:187], a[52:55], v[132:135]\n"
"v_subrev_f32 v146, s22, v146\n"
"v_mfma_f32_16x16x32_bf16 v[132:135], v[188:191], a[56:59], v[132:135]\n"
"ds_read_b64_tr_b16 v[102:103], v235 offset:34816\n"
"v_cvt_pk_bf16_f32 v143, v146, v147\n"
"v_mfma_f32_16x16x32_bf16 v[132:135], v[192:195], a[60:63], v[132:135]\n"
"ds_read_b64_tr_b16 v[114:115], v231 offset:51200\n"
"v_subrev_f32 v163, s22, v163\n"
"v_mfma_f32_16x16x32_bf16 v[132:135], v[208:211], a[0:3], v[132:135]\n"
"s_waitcnt lgkmcnt(4)\n"
"v_mov_b64 v[26:27], s[22:23]\n"
"v_mov_b64 v[50:51], s[22:23]\n"
"v_mfma_f32_16x16x32_bf16 v[132:135], v[212:215], a[4:7], v[132:135]\n"
"ds_read_b64_tr_b16 v[108:109], v235 offset:40960\n"
"v_add_u32 v238, s97, v254\n"
"v_mfma_f32_16x16x32_bf16 v[132:135], v[216:219], a[8:11], v[132:135]\n"
"v_xor_b32 v248, s75, v248\n"
"v_xor_b32 v237, s61, v237\n"
"v_mfma_f32_16x16x32_bf16 v[132:135], v[220:223], a[12:15], v[132:135]\n"
"buffer_store_dwordx4 v[140:143], v243, s[32:35], s16 offen offset:2048 sc1\n"
"ds_read_b64_tr_b16 v[96:97], v231 offset:32768\n"
"v_mfma_f32_16x16x32_bf16 v[148:151], v[220:223], a[28:31], v[148:151]\n"
"v_subrev_f32 v156, s22, v156\n"
"v_cvt_pk_bf16_f32 v156, v156, v157\n"
"v_mfma_f32_16x16x32_bf16 v[148:151], v[216:219], a[24:27], v[148:151]\n"
"ds_read_b64_tr_b16 v[116:117], v235 offset:49152\n"
"v_cvt_pk_bf16_f32 v157, v158, v159\n"
"v_mfma_f32_16x16x32_bf16 v[148:151], v[212:215], a[20:23], v[148:151]\n"
"ds_read_b64_tr_b16 v[106:107], v231 offset:43008\n"
"v_subrev_f32 v133, s22, v133\n"
"v_mfma_f32_16x16x32_bf16 v[148:151], v[208:211], a[16:19], v[148:151]\n"
"v_subrev_f32 v161, s22, v161\n"
"v_xor_b32 v229, s75, v229\n"
"v_mfma_f32_16x16x32_bf16 v[148:151], v[192:195], a[76:79], v[148:151]\n"
"ds_read_b64_tr_b16 v[100:101], v235 offset:32768\n"
"v_mfma_f32_16x16x32_bf16 v[148:151], v[188:191], a[72:75], v[148:151]\n"
"ds_read_b64_tr_b16 v[98:99], v231 offset:34816\n"
"v_mov_b64 v[32:33], s[22:23]\n"
"v_mfma_f32_16x16x32_bf16 v[148:151], v[184:187], a[68:71], v[148:151]\n"
"ds_read_b64_tr_b16 v[122:123], v231 offset:59392\n"
"v_subrev_f32 v160, s22, v160\n"
"v_mfma_f32_16x16x32_bf16 v[148:151], v[180:183], a[64:67], v[148:151]\n"
"ds_read_b64_tr_b16 v[110:111], v235 offset:43008\n"
"v_subrev_f32 v132, s22, v132\n"
"v_mfma_f32_16x16x32_bf16 v[164:167], v[180:183], a[80:83], v[164:167]\n"
"v_mov_b64 v[46:47], s[22:23]\n"
"v_subrev_f32 v162, s22, v162\n"
"v_mfma_f32_16x16x32_bf16 v[164:167], v[184:187], a[84:87], v[164:167]\n"
"ds_read_b64_tr_b16 v[112:113], v231 offset:49152\n"
"v_cvt_pk_bf16_f32 v132, v132, v133\n"
"v_mfma_f32_16x16x32_bf16 v[164:167], v[188:191], a[88:91], v[164:167]\n"
"v_mov_b64 v[94:95], 0\n"
"v_cvt_pk_bf16_f32 v159, v162, v163\n"
"v_mfma_f32_16x16x32_bf16 v[164:167], v[192:195], a[92:95], v[164:167]\n"
"ds_read_b64_tr_b16 v[226:227], v235 offset:59392\n"
"v_mfma_f32_16x16x32_bf16 v[164:167], v[208:211], a[32:35], v[164:167]\n"
"v_subrev_f32 v176, s22, v176\n"
"v_mfma_f32_16x16x32_bf16 v[164:167], v[212:215], a[36:39], v[164:167]\n"
"ds_read_b64_tr_b16 v[224:225], v235 offset:57344\n"
"v_mov_b64 v[16:17], s[22:23]\n"
"v_mfma_f32_16x16x32_bf16 v[164:167], v[216:219], a[40:43], v[164:167]\n"
"v_cvt_pk_bf16_f32 v173, v174, v175\n"
"v_subrev_f32 v177, s22, v177\n"
"v_mfma_f32_16x16x32_bf16 v[164:167], v[220:223], a[44:47], v[164:167]\n"
"s_barrier\n"
"s_waitcnt lgkmcnt(3)\n"
"v_mfma_f32_16x16x32_bf16 v[136:139], v[96:99], a[48:51], v[136:139]\n"
"ds_read_b64_tr_b16 v[218:219], v228 offset:26624\n"
"v_subrev_f32 v135, s22, v135\n"
"v_mfma_f32_16x16x32_bf16 v[136:139], v[100:103], a[52:55], v[136:139]\n"
"ds_read_b64_tr_b16 v[210:211], v228 offset:18432\n"
"v_cvt_pk_bf16_f32 v158, v160, v161\n"
"v_mfma_f32_16x16x32_bf16 v[136:139], v[104:107], a[56:59], v[136:139]\n"
"ds_read_b64_tr_b16 v[182:183], v228 offset:2048\n"
"v_subrev_f32 v134, s22, v134\n"
"v_mfma_f32_16x16x32_bf16 v[136:139], v[108:111], a[60:63], v[136:139]\n"
"s_waitcnt lgkmcnt(4)\n"
"ds_read_b64_tr_b16 v[192:193], v232 offset:8192\n"
"v_mov_b64 v[36:37], s[22:23]\n"
"v_mfma_f32_16x16x32_bf16 v[136:139], v[112:115], a[0:3], v[136:139]\n"
"ds_read_b64_tr_b16 v[208:209], v228 offset:16384\n"
"v_mov_b64 v[84:85], 0\n"
"v_mfma_f32_16x16x32_bf16 v[136:139], v[116:119], a[4:7], v[136:139]\n"
"ds_read_b64_tr_b16 v[180:181], v228 offset:0\n"
"v_cvt_pk_bf16_f32 v174, v176, v177\n"
"v_mfma_f32_16x16x32_bf16 v[136:139], v[120:123], a[8:11], v[136:139]\n"
"s_waitcnt lgkmcnt(6)\n"
"ds_read_b64_tr_b16 v[216:217], v228 offset:24576\n"
"v_mov_b64 v[14:15], s[22:23]\n"
"v_mfma_f32_16x16x32_bf16 v[136:139], v[224:227], a[12:15], v[136:139]\n"
"buffer_store_dwordx4 v[156:159], v243, s[32:35], s17 offen sc1\n"
"ds_read_b64_tr_b16 v[194:195], v232 offset:10240\n"
"v_mfma_f32_16x16x32_bf16 v[152:155], v[224:227], a[28:31], v[152:155]\n"
"ds_read_b64_tr_b16 v[212:213], v232 offset:16384\n"
"v_lshlrev_b32 v237, 2, v237\n"
"v_mfma_f32_16x16x32_bf16 v[152:155], v[120:123], a[24:27], v[152:155]\n"
"ds_read_b64_tr_b16 v[190:191], v228 offset:10240\n"
"v_mov_b64 v[52:53], s[22:23]\n"
"v_mfma_f32_16x16x32_bf16 v[152:155], v[116:119], a[20:23], v[152:155]\n"
"ds_read_b64_tr_b16 v[184:185], v232 offset:0\n"
"v_xor_b32 v233, s75, v233\n"
"v_mfma_f32_16x16x32_bf16 v[152:155], v[112:115], a[16:19], v[152:155]\n"
"ds_read_b64_tr_b16 v[220:221], v232 offset:24576\n"
"v_subrev_f32 v179, s22, v179\n"
"v_mfma_f32_16x16x32_bf16 v[152:155], v[108:111], a[76:79], v[152:155]\n"
"ds_read_b64_tr_b16 v[188:189], v228 offset:8192\n"
"v_xor_b32 v239, 64, v238\n"
"v_mfma_f32_16x16x32_bf16 v[152:155], v[104:107], a[72:75], v[152:155]\n"
"ds_read_b64_tr_b16 v[214:215], v232 offset:18432\n"
"v_add_u32 v252, s98, v254\n"
"v_mfma_f32_16x16x32_bf16 v[152:155], v[100:103], a[68:71], v[152:155]\n"
"ds_read_b64_tr_b16 v[186:187], v232 offset:2048\n"
"ds_read_b64_tr_b16 v[222:223], v232 offset:26624\n"
"v_mfma_f32_16x16x32_bf16 v[152:155], v[96:99], a[64:67], v[152:155]\n"
"v_mov_b64 v[12:13], s[22:23]\n"
"v_mov_b64 v[86:87], 0\n"
"v_mfma_f32_16x16x32_bf16 v[168:171], v[96:99], a[80:83], v[168:171]\n"
"ds_read_b64_tr_b16 v[96:97], v229 offset:0\n"
"ds_read_b64_tr_b16 v[98:99], v229 offset:2048\n"
"v_mfma_f32_16x16x32_bf16 v[168:171], v[100:103], a[84:87], v[168:171]\n"
"ds_read_b64_tr_b16 v[100:101], v233 offset:0\n"
"ds_read_b64_tr_b16 v[102:103], v233 offset:2048\n"
"v_mfma_f32_16x16x32_bf16 v[168:171], v[104:107], a[88:91], v[168:171]\n"
"ds_read_b64_tr_b16 v[104:105], v229 offset:8192\n"
"v_add_u32 v241, 1, v241\n"
"v_mfma_f32_16x16x32_bf16 v[168:171], v[108:111], a[92:95], v[168:171]\n"
"v_subrev_f32 v178, s22, v178\n"
"v_mfma_f32_16x16x32_bf16 v[168:171], v[112:115], a[32:35], v[168:171]\n"
"v_mov_b64 v[20:21], s[22:23]\n"
"v_cvt_pk_bf16_f32 v175, v178, v179\n"
"v_mfma_f32_16x16x32_bf16 v[168:171], v[116:119], a[36:39], v[168:171]\n"
"v_mov_b64 v[2:3], s[22:23]\n"
"v_mfma_f32_16x16x32_bf16 v[168:171], v[120:123], a[40:43], v[168:171]\n"
"s_add_u32 s47, s47, 1\n"
"v_mfma_f32_16x16x32_bf16 v[168:171], v[224:227], a[44:47], v[168:171]\n"
"s_waitcnt lgkmcnt(5)\n"
"v_lshl_add_u32 v248, v242, 8, s93\n"
"LGL_%=:\n"
"v_mfma_f32_16x16x32_bf16 v[124:127], v[180:183], a[96:99], v[196:199]\n"
"ds_read_b64_tr_b16 v[106:107], v229 offset:10240\n"
"v_cvt_pk_bf16_f32 v133, v134, v135\n"
"v_mfma_f32_16x16x32_bf16 v[124:127], v[184:187], a[100:103], v[124:127]\n"
"ds_read_b64_tr_b16 v[108:109], v233 offset:8192\n"
"v_subrev_f32 v136, s22, v136\n"
"v_mfma_f32_16x16x32_bf16 v[124:127], v[188:191], a[104:107], v[124:127]\n"
"ds_read_b64_tr_b16 v[110:111], v233 offset:10240\n"
"v_subrev_f32 v137, s22, v137\n"
"v_mfma_f32_16x16x32_bf16 v[124:127], v[192:195], a[108:111], v[124:127]\n"
"ds_read_b64_tr_b16 v[112:113], v229 offset:16384\n"
"v_subrev_f32 v138, s22, v138\n"
"v_mfma_f32_16x16x32_bf16 v[124:127], v[208:211], a[144:147], v[124:127]\n"
"ds_read_b64_tr_b16 v[114:115], v229 offset:18432\n"
"v_subrev_f32 v139, s22, v139\n"
"v_mfma_f32_16x16x32_bf16 v[124:127], v[212:215], a[148:151], v[124:127]\n"
"s_waitcnt lgkmcnt(7)\n"
"ds_read_b64_tr_b16 v[116:117], v233 offset:16384\n"
"v_cvt_pk_bf16_f32 v134, v136, v137\n"
"v_mfma_f32_16x16x32_bf16 v[124:127], v[216:219], a[152:155], v[124:127]\n"
"ds_read_b64_tr_b16 v[118:119], v233 offset:18432\n"
"v_cvt_pk_bf16_f32 v135, v138, v139\n"
"v_mfma_f32_16x16x32_bf16 v[124:127], v[220:223], a[156:159], v[124:127]\n"
"buffer_store_dwordx4 v[172:175], v243, s[32:35], s17 offen offset:2048 sc1\n"
"v_subrev_f32 v148, s22, v148\n"
"v_mfma_f32_16x16x32_bf16 v[140:143], v[220:223], a[172:175], v[196:199]\n"
"ds_read_b64_tr_b16 v[120:121], v229 offset:24576\n"
"v_subrev_f32 v149, s22, v149\n"
"v_mfma_f32_16x16x32_bf16 v[140:143], v[216:219], a[168:171], v[140:143]\n"
"ds_read_b64_tr_b16 v[122:123], v229 offset:26624\n"
"v_subrev_f32 v150, s22, v150\n"
"v_mfma_f32_16x16x32_bf16 v[140:143], v[212:215], a[164:167], v[140:143]\n"
"ds_read_b64_tr_b16 v[224:225], v233 offset:24576\n"
"v_subrev_f32 v151, s22, v151\n"
"v_mfma_f32_16x16x32_bf16 v[140:143], v[208:211], a[160:163], v[140:143]\n"
"ds_read_b64_tr_b16 v[226:227], v233 offset:26624\n"
"v_cvt_pk_bf16_f32 v148, v148, v149\n"
"v_mfma_f32_16x16x32_bf16 v[140:143], v[192:195], a[124:127], v[140:143]\n"
"v_cvt_pk_bf16_f32 v149, v150, v151\n"
"v_subrev_f32 v152, s22, v152\n"
"v_mfma_f32_16x16x32_bf16 v[140:143], v[188:191], a[120:123], v[140:143]\n"
"s_add_u32 s41, s70, 0\n"
"v_subrev_f32 v153, s22, v153\n"
"v_subrev_f32 v154, s22, v154\n"
"v_mfma_f32_16x16x32_bf16 v[140:143], v[184:187], a[116:119], v[140:143]\n"
"s_mov_b32 m0, s71\n"
"v_subrev_f32 v155, s22, v155\n"
"v_cvt_pk_bf16_f32 v150, v152, v153\n"
"v_mfma_f32_16x16x32_bf16 v[140:143], v[180:183], a[112:115], v[140:143]\n"
"s_waitcnt vmcnt(14)\n"
"buffer_load_dwordx4 v240, s[28:31], s41 offen lds\n"
"v_mfma_f32_16x16x32_bf16 v[156:159], v[180:183], a[128:131], v[196:199]\n"
"v_xor_b32 v230, s75, v230\n"
"v_cvt_pk_bf16_f32 v151, v154, v155\n"
"v_mfma_f32_16x16x32_bf16 v[156:159], v[184:187], a[132:135], v[156:159]\n"
"v_xor_b32 v234, s75, v234\n"
"v_subrev_f32 v164, s22, v164\n"
"v_mfma_f32_16x16x32_bf16 v[156:159], v[188:191], a[136:139], v[156:159]\n"
"v_subrev_f32 v165, s22, v165\n"
"v_subrev_f32 v166, s22, v166\n"
"v_mfma_f32_16x16x32_bf16 v[156:159], v[192:195], a[140:143], v[156:159]\n"
"v_subrev_f32 v167, s22, v167\n"
"v_cvt_pk_bf16_f32 v164, v164, v165\n"
"v_mfma_f32_16x16x32_bf16 v[156:159], v[208:211], a[176:179], v[156:159]\n"
"v_cvt_pk_bf16_f32 v165, v166, v167\n"
"v_subrev_f32 v168, s22, v168\n"
"v_mfma_f32_16x16x32_bf16 v[156:159], v[212:215], a[180:183], v[156:159]\n"
"v_subrev_f32 v169, s22, v169\n"
"v_subrev_f32 v170, s22, v170\n"
"v_mfma_f32_16x16x32_bf16 v[156:159], v[216:219], a[184:187], v[156:159]\n"
"v_subrev_f32 v171, s22, v171\n"
"v_cvt_pk_bf16_f32 v166, v168, v169\n"
"v_mfma_f32_16x16x32_bf16 v[156:159], v[220:223], a[188:191], v[156:159]\n"
"buffer_store_dwordx4 v[132:135], v243, s[32:35], s16 offen offset:1024 sc1\n"
"v_cvt_pk_bf16_f32 v167, v170, v171\n"
"v_mfma_f32_16x16x32_bf16 v[172:175], v[220:223], a[252:255], v[196:199]\n"
"ds_read_b64_tr_b16 v[220:221], v234 offset:24576\n"
"v_xor_b32 v231, s75, v231\n"
"v_mfma_f32_16x16x32_bf16 v[172:175], v[216:219], a[248:251], v[172:175]\n"
"ds_read_b64_tr_b16 v[216:217], v230 offset:24576\n"
"v_xor_b32 v235, s75, v235\n"
"v_mfma_f32_16x16x32_bf16 v[172:175], v[212:215], a[244:247], v[172:175]\n"
"ds_read_b64_tr_b16 v[212:213], v234 offset:16384\n"
"v_lshlrev_b32 v202, 16, v84\n"
"v_mfma_f32_16x16x32_bf16 v[172:175], v[208:211], a[240:243], v[172:175]\n"
"ds_read_b64_tr_b16 v[208:209], v230 offset:16384\n"
"v_and_b32 v84, s73, v84\n"
"v_mfma_f32_16x16x32_bf16 v[172:175], v[192:195], a[204:207], v[172:175]\n"
"ds_read_b64_tr_b16 v[192:193], v234 offset:8192\n"
"v_add_f32 v25, v25, v84\n"
"v_mfma_f32_16x16x32_bf16 v[172:175], v[188:191], a[200:203], v[172:175]\n"
"ds_read_b64_tr_b16 v[188:189], v230 offset:8192\n"
"v_lshlrev_b32 v203, 16, v85\n"
"v_mfma_f32_16x16x32_bf16 v[172:175], v[184:187], a[196:199], v[172:175]\n"
"s_add_u32 s40, s41, s52\n"
"ds_read_b64_tr_b16 v[184:185], v234 offset:0\n"
"v_and_b32 v85, s73, v85\n"
"v_mfma_f32_16x16x32_bf16 v[172:175], v[180:183], a[192:195], v[172:175]\n"
"s_waitcnt vmcnt(13) lgkmcnt(7)\n"
"buffer_load_dwordx4 v240, s[28:31], s40 offen offset:1024 lds\n"
"v_mfma_f32_16x16x32_bf16 v[128:131], v[96:99], a[96:99], v[196:199]\n"
"s_waitcnt lgkmcnt(6)\n"
"ds_read_b64_tr_b16 v[180:181], v230 offset:0\n"
"v_add_f32 v27, v27, v85\n"
"v_mfma_f32_16x16x32_bf16 v[128:131], v[100:103], a[100:103], v[128:131]\n"
"ds_read_b64_tr_b16 v[182:183], v230 offset:2048\n"
"v_lshlrev_b32 v204, 16, v86\n"
"v_mfma_f32_16x16x32_bf16 v[128:131], v[104:107], a[104:107], v[128:131]\n"
"ds_read_b64_tr_b16 v[186:187], v234 offset:2048\n"
"v_and_b32 v86, s73, v86\n"
"v_mfma_f32_16x16x32_bf16 v[128:131], v[108:111], a[108:111], v[128:131]\n"
"ds_read_b64_tr_b16 v[190:191], v230 offset:10240\n"
"v_add_f32 v29, v29, v86\n"
"v_mfma_f32_16x16x32_bf16 v[128:131], v[112:115], a[144:147], v[128:131]\n"
"s_waitcnt vmcnt(6)\n"
"ds_read_b64_tr_b16 v[194:195], v234 offset:10240\n"
"v_lshlrev_b32 v205, 16, v87\n"
"v_mfma_f32_16x16x32_bf16 v[128:131], v[116:119], a[148:151], v[128:131]\n"
"v_subrev_u32 v200, s21, v200\n"
"ds_read_b64_tr_b16 v[210:211], v230 offset:18432\n"
"v_mfma_f32_16x16x32_bf16 v[128:131], v[120:123], a[152:155], v[128:131]\n"
"v_cmp_lt_u32 vcc, s69, v200\n"
"ds_read_b64_tr_b16 v[214:215], v234 offset:18432\n"
"v_mfma_f32_16x16x32_bf16 v[128:131], v[224:227], a[156:159], v[128:131]\n"
"ds_read_b64_tr_b16 v[218:219], v230 offset:26624\n"
"s_cbranch_vccnz LGP_%=\nLGPK_%=:\n"
"v_mfma_f32_16x16x32_bf16 v[144:147], v[224:227], a[172:175], v[196:199]\n"
"s_sub_u32 s72, s72, 1\n"
"ds_read_b64_tr_b16 v[222:223], v234 offset:26624\n"
"v_and_b32 v87, s73, v87\n"
"v_mfma_f32_16x16x32_bf16 v[144:147], v[120:123], a[168:171], v[144:147]\n"
"s_and_b32 s72, s72, 7\n"
"v_add_f32 v24, v24, v202\n"
"v_add_f32 v31, v31, v87\n"
"v_mfma_f32_16x16x32_bf16 v[144:147], v[116:119], a[164:167], v[144:147]\n"
"v_add_f32 v26, v26, v203\n"
"v_lshlrev_b32 v206, 16, v88\n"
"v_mfma_f32_16x16x32_bf16 v[144:147], v[112:115], a[160:163], v[144:147]\n"
"buffer_load_dwordx4 v[72:75], v243, s[32:35], s18 offen sc1\n"
"v_mfma_f32_16x16x32_bf16 v[144:147], v[108:111], a[124:127], v[144:147]\n"
"v_and_b32 v88, s73, v88\n"
"v_add_f32 v33, v33, v88\n"
"v_mfma_f32_16x16x32_bf16 v[144:147], v[104:107], a[120:123], v[144:147]\n"
"v_lshlrev_b32 v207, 16, v89\n"
"v_and_b32 v89, s73, v89\n"
"v_mfma_f32_16x16x32_bf16 v[144:147], v[100:103], a[116:119], v[144:147]\n"
"v_add_f32 v35, v35, v89\n"
"v_lshlrev_b32 v202, 16, v90\n"
"v_mfma_f32_16x16x32_bf16 v[144:147], v[96:99], a[112:115], v[144:147]\n"
"buffer_store_dwordx4 v[148:151], v243, s[32:35], s16 offen offset:3072 sc1\n"
"v_and_b32 v90, s73, v90\n"
"v_mfma_f32_16x16x32_bf16 v[160:163], v[96:99], a[128:131], v[196:199]\n"
"v_add_f32 v37, v37, v90\n"
"v_lshlrev_b32 v203, 16, v91\n"
"v_mfma_f32_16x16x32_bf16 v[160:163], v[100:103], a[132:135], v[160:163]\n"
"v_and_b32 v91, s73, v91\n"
"v_add_f32 v39, v39, v91\n"
"v_mfma_f32_16x16x32_bf16 v[160:163], v[104:107], a[136:139], v[160:163]\n"
"v_add_f32 v28, v28, v204\n"
"v_add_f32 v30, v30, v205\n"
"v_mfma_f32_16x16x32_bf16 v[160:163], v[108:111], a[140:143], v[160:163]\n"
"v_add_f32 v32, v32, v206\n"
"v_add_f32 v34, v34, v207\n"
"v_mfma_f32_16x16x32_bf16 v[160:163], v[112:115], a[176:179], v[160:163]\n"
"v_lshlrev_b32 v204, 16, v92\n"
"v_and_b32 v92, s73, v92\n"
"v_mfma_f32_16x16x32_bf16 v[160:163], v[116:119], a[180:183], v[160:163]\n"
"v_add_f32 v41, v41, v92\n"
"v_lshlrev_b32 v205, 16, v93\n"
"v_mfma_f32_16x16x32_bf16 v[160:163], v[120:123], a[184:187], v[160:163]\n"
"s_add_u32 s40, s41, s53\n"
"v_and_b32 v93, s73, v93\n"
"v_add_f32 v43, v43, v93\n"
"v_mfma_f32_16x16x32_bf16 v[160:163], v[224:227], a[188:191], v[160:163]\n"
"buffer_load_dwordx4 v240, s[28:31], s40 offen offset:2048 lds\n"
"v_mfma_f32_16x16x32_bf16 v[176:179], v[224:227], a[252:255], v[196:199]\n"
"ds_read_b64_tr_b16 v[224:225], v235 offset:24576\n"
"v_lshlrev_b32 v206, 16, v94\n"
"v_mfma_f32_16x16x32_bf16 v[176:179], v[120:123], a[248:251], v[176:179]\n"
"ds_read_b64_tr_b16 v[120:121], v231 offset:24576\n"
"v_and_b32 v94, s73, v94\n"
"v_mfma_f32_16x16x32_bf16 v[176:179], v[116:119], a[244:247], v[176:179]\n"
"ds_read_b64_tr_b16 v[116:117], v235 offset:16384\n"
"v_add_f32 v45, v45, v94\n"
"v_mfma_f32_16x16x32_bf16 v[176:179], v[112:115], a[240:243], v[176:179]\n"
"buffer_load_dwordx4 v[76:79], v243, s[32:35], s18 offen offset:1024 sc1\n"
"v_mfma_f32_16x16x32_bf16 v[176:179], v[108:111], a[204:207], v[176:179]\n"
"ds_read_b64_tr_b16 v[108:109], v235 offset:8192\n"
"v_lshlrev_b32 v207, 16, v95\n"
"v_mfma_f32_16x16x32_bf16 v[176:179], v[104:107], a[200:203], v[176:179]\n"
"ds_read_b64_tr_b16 v[104:105], v231 offset:8192\n"
"v_and_b32 v95, s73, v95\n"
"v_mfma_f32_16x16x32_bf16 v[176:179], v[100:103], a[196:199], v[176:179]\n"
"s_add_u32 s40, s41, s54\n"
"ds_read_b64_tr_b16 v[100:101], v235 offset:0\n"
"v_add_f32 v47, v47, v95\n"
"v_mfma_f32_16x16x32_bf16 v[176:179], v[96:99], a[192:195], v[176:179]\n"
"s_waitcnt lgkmcnt(6)\n"
"buffer_load_dwordx4 v240, s[28:31], s40 offen offset:3072 lds\n"
"v_mfma_f32_16x16x32_bf16 v[132:135], v[180:183], a[96:99], v[196:199]\n"
"ds_read_b64_tr_b16 v[96:97], v231 offset:0\n"
"v_add_f32 v36, v36, v202\n"
"v_mfma_f32_16x16x32_bf16 v[132:135], v[184:187], a[100:103], v[132:135]\n"
"ds_read_b64_tr_b16 v[98:99], v231 offset:2048\n"
"v_add_f32 v38, v38, v203\n"
"v_mfma_f32_16x16x32_bf16 v[132:135], v[188:191], a[104:107], v[132:135]\n"
"ds_read_b64_tr_b16 v[102:103], v235 offset:2048\n"
"v_add_f32 v44, v44, v206\n"
"v_mfma_f32_16x16x32_bf16 v[132:135], v[192:195], a[108:111], v[132:135]\n"
"ds_read_b64_tr_b16 v[106:107], v231 offset:10240\n"
"v_add_f32 v46, v46, v207\n"
"v_mfma_f32_16x16x32_bf16 v[132:135], v[208:211], a[144:147], v[132:135]\n"
"ds_read_b64_tr_b16 v[110:111], v235 offset:10240\n"
"v_add_f32 v40, v40, v204\n"
"v_mfma_f32_16x16x32_bf16 v[132:135], v[212:215], a[148:151], v[132:135]\n"
"ds_read_b64_tr_b16 v[112:113], v231 offset:16384\n"
"v_add_f32 v42, v42, v205\n"
"v_mfma_f32_16x16x32_bf16 v[132:135], v[216:219], a[152:155], v[132:135]\n"
"ds_read_b64_tr_b16 v[114:115], v231 offset:18432\n"
"v_mfma_f32_16x16x32_bf16 v[132:135], v[220:223], a[156:159], v[132:135]\n"
"buffer_store_dwordx4 v[164:167], v243, s[32:35], s17 offen offset:1024 sc1\n"
"v_lshlrev_b32 v206, 16, v244\n"
"v_mfma_f32_16x16x32_bf16 v[148:151], v[220:223], a[172:175], v[196:199]\n"
"ds_read_b64_tr_b16 v[118:119], v235 offset:18432\n"
"v_and_b32 v244, s73, v244\n"
"v_mfma_f32_16x16x32_bf16 v[148:151], v[216:219], a[168:171], v[148:151]\n"
"ds_read_b64_tr_b16 v[122:123], v231 offset:26624\n"
"v_add_f32 v49, v49, v244\n"
"v_mfma_f32_16x16x32_bf16 v[148:151], v[212:215], a[164:167], v[148:151]\n"
"ds_read_b64_tr_b16 v[226:227], v235 offset:26624\n"
"v_lshlrev_b32 v207, 16, v245\n"
"v_mfma_f32_16x16x32_bf16 v[148:151], v[208:211], a[160:163], v[148:151]\n"
"buffer_load_dwordx4 v[80:83], v243, s[32:35], s18 offen offset:2048 sc1\n"
"v_mfma_f32_16x16x32_bf16 v[148:151], v[192:195], a[124:127], v[148:151]\n"
"v_and_b32 v245, s73, v245\n"
"v_add_f32 v51, v51, v245\n"
"v_mfma_f32_16x16x32_bf16 v[148:151], v[188:191], a[120:123], v[148:151]\n"
"s_add_u32 s41, s70, 163840\n"
"v_lshlrev_b32 v202, 16, v246\n"
"v_and_b32 v246, s73, v246\n"
"v_mfma_f32_16x16x32_bf16 v[148:151], v[184:187], a[116:119], v[148:151]\n"
"s_add_u32 m0, s71, 16384\n"
"v_add_f32 v53, v53, v246\n"
"v_lshlrev_b32 v203, 16, v247\n"
"v_mfma_f32_16x16x32_bf16 v[148:151], v[180:183], a[112:115], v[148:151]\n"
"buffer_load_dwordx4 v240, s[28:31], s41 offen lds\n"
"v_mfma_f32_16x16x32_bf16 v[164:167], v[180:183], a[128:131], v[196:199]\n"
"v_and_b32 v247, s73, v247\n"
"v_add_f32 v55, v55, v247\n"
"v_mfma_f32_16x16x32_bf16 v[164:167], v[184:187], a[132:135], v[164:167]\n"
"v_add_f32 v48, v48, v206\n"
"v_add_f32 v50, v50, v207\n"
"v_mfma_f32_16x16x32_bf16 v[164:167], v[188:191], a[136:139], v[164:167]\n"
"v_add_f32 v52, v52, v202\n"
"v_add_f32 v54, v54, v203\n"
"v_mfma_f32_16x16x32_bf16 v[164:167], v[192:195], a[140:143], v[164:167]\n"
"v_mfma_f32_16x16x32_bf16 v[164:167], v[208:211], a[176:179], v[164:167]\n"
"v_mfma_f32_16x16x32_bf16 v[164:167], v[212:215], a[180:183], v[164:167]\n"
"v_mfma_f32_16x16x32_bf16 v[164:167], v[216:219], a[184:187], v[164:167]\n"
"s_waitcnt vmcnt(12)\n"
"v_mfma_f32_16x16x32_bf16 v[164:167], v[220:223], a[188:191], v[164:167]\n"
"s_barrier\n"
"s_waitcnt lgkmcnt(0)\n"
"v_mfma_f32_16x16x32_bf16 v[136:139], v[96:99], a[96:99], v[196:199]\n"
"ds_read_b64_tr_b16 v[180:181], v228 offset:32768\n"
"v_mfma_f32_16x16x32_bf16 v[136:139], v[100:103], a[100:103], v[136:139]\n"
"ds_read_b64_tr_b16 v[182:183], v228 offset:34816\n"
"v_mfma_f32_16x16x32_bf16 v[136:139], v[104:107], a[104:107], v[136:139]\n"
"ds_read_b64_tr_b16 v[184:185], v232 offset:32768\n"
"v_mfma_f32_16x16x32_bf16 v[136:139], v[108:111], a[108:111], v[136:139]\n"
"ds_read_b64_tr_b16 v[186:187], v232 offset:34816\n"
"v_mfma_f32_16x16x32_bf16 v[136:139], v[112:115], a[144:147], v[136:139]\n"
"ds_read_b64_tr_b16 v[188:189], v228 offset:40960\n"
"v_mfma_f32_16x16x32_bf16 v[136:139], v[116:119], a[148:151], v[136:139]\n"
"ds_read_b64_tr_b16 v[190:191], v228 offset:43008\n"
"v_mfma_f32_16x16x32_bf16 v[136:139], v[120:123], a[152:155], v[136:139]\n"
"s_add_u32 s40, s41, s52\n"
"ds_read_b64_tr_b16 v[192:193], v232 offset:40960\n"
"v_mfma_f32_16x16x32_bf16 v[136:139], v[224:227], a[156:159], v[136:139]\n"
"buffer_load_dwordx4 v240, s[28:31], s40 offen offset:1024 lds\n"
"v_mfma_f32_16x16x32_bf16 v[152:155], v[224:227], a[172:175], v[196:199]\n"
"ds_read_b64_tr_b16 v[194:195], v232 offset:43008\n"
"v_mfma_f32_16x16x32_bf16 v[152:155], v[120:123], a[168:171], v[152:155]\n"
"ds_read_b64_tr_b16 v[208:209], v228 offset:49152\n"
"v_mfma_f32_16x16x32_bf16 v[152:155], v[116:119], a[164:167], v[152:155]\n"
"ds_read_b64_tr_b16 v[210:211], v228 offset:51200\n"
"v_mfma_f32_16x16x32_bf16 v[152:155], v[112:115], a[160:163], v[152:155]\n"
"ds_read_b64_tr_b16 v[212:213], v232 offset:49152\n"
"v_mfma_f32_16x16x32_bf16 v[152:155], v[108:111], a[124:127], v[152:155]\n"
"ds_read_b64_tr_b16 v[214:215], v232 offset:51200\n"
"v_mfma_f32_16x16x32_bf16 v[152:155], v[104:107], a[120:123], v[152:155]\n"
"ds_read_b64_tr_b16 v[216:217], v228 offset:57344\n"
"v_mfma_f32_16x16x32_bf16 v[152:155], v[100:103], a[116:119], v[152:155]\n"
"s_add_u32 s40, s41, s53\n"
"ds_read_b64_tr_b16 v[218:219], v228 offset:59392\n"
"v_xor_b32 v228, s75, v228\n"
"v_mfma_f32_16x16x32_bf16 v[152:155], v[96:99], a[112:115], v[152:155]\n"
"buffer_load_dwordx4 v240, s[28:31], s40 offen offset:2048 lds\n"
"v_mfma_f32_16x16x32_bf16 v[168:171], v[96:99], a[128:131], v[196:199]\n"
"ds_read_b64_tr_b16 v[220:221], v232 offset:57344\n"
"v_mfma_f32_16x16x32_bf16 v[168:171], v[100:103], a[132:135], v[168:171]\n"
"ds_read_b64_tr_b16 v[222:223], v232 offset:59392\n"
"v_xor_b32 v232, s75, v232\n"
"v_mfma_f32_16x16x32_bf16 v[168:171], v[104:107], a[136:139], v[168:171]\n"
"ds_read_b64_tr_b16 v[96:97], v229 offset:32768\n"
"v_mfma_f32_16x16x32_bf16 v[168:171], v[108:111], a[140:143], v[168:171]\n"
"ds_read_b64_tr_b16 v[98:99], v229 offset:34816\n"
"v_mfma_f32_16x16x32_bf16 v[168:171], v[112:115], a[176:179], v[168:171]\n"
"ds_read_b64_tr_b16 v[100:101], v233 offset:32768\n"
"v_mfma_f32_16x16x32_bf16 v[168:171], v[116:119], a[180:183], v[168:171]\n"
"ds_read_b64_tr_b16 v[102:103], v233 offset:34816\n"
"v_mfma_f32_16x16x32_bf16 v[168:171], v[120:123], a[184:187], v[168:171]\n"
"s_add_u32 s40, s41, s54\n"
"ds_read_b64_tr_b16 v[104:105], v229 offset:40960\n"
"v_mfma_f32_16x16x32_bf16 v[168:171], v[224:227], a[188:191], v[168:171]\n"
"s_waitcnt lgkmcnt(7)\n"
"buffer_load_dwordx4 v240, s[28:31], s40 offen offset:3072 lds\n"
"v_mfma_f32_16x16x32_bf16 v[124:127], v[180:183], a[48:51], v[124:127]\n"
"ds_read_b64_tr_b16 v[106:107], v229 offset:43008\n"
"v_mfma_f32_16x16x32_bf16 v[124:127], v[184:187], a[52:55], v[124:127]\n"
"ds_read_b64_tr_b16 v[108:109], v233 offset:40960\n"
"v_mfma_f32_16x16x32_bf16 v[124:127], v[188:191], a[56:59], v[124:127]\n"
"ds_read_b64_tr_b16 v[110:111], v233 offset:43008\n"
"v_mfma_f32_16x16x32_bf16 v[124:127], v[192:195], a[60:63], v[124:127]\n"
"ds_read_b64_tr_b16 v[112:113], v229 offset:49152\n"
"v_mfma_f32_16x16x32_bf16 v[124:127], v[208:211], a[0:3], v[124:127]\n"
"ds_read_b64_tr_b16 v[114:115], v229 offset:51200\n"
"v_mfma_f32_16x16x32_bf16 v[124:127], v[212:215], a[4:7], v[124:127]\n"
"s_add_u32 s41, s70, 327680\n"
"ds_read_b64_tr_b16 v[116:117], v233 offset:49152\n"
"v_mfma_f32_16x16x32_bf16 v[124:127], v[216:219], a[8:11], v[124:127]\n"
"s_waitcnt lgkmcnt(6)\n"
"s_add_u32 m0, s71, 32768\n"
"ds_read_b64_tr_b16 v[118:119], v233 offset:51200\n"
"v_mfma_f32_16x16x32_bf16 v[124:127], v[220:223], a[12:15], v[124:127]\n"
"buffer_load_dwordx4 v240, s[28:31], s41 offen lds\n"
"v_mfma_f32_16x16x32_bf16 v[140:143], v[220:223], a[28:31], v[140:143]\n"
"ds_read_b64_tr_b16 v[120:121], v229 offset:57344\n"
"v_mfma_f32_16x16x32_bf16 v[140:143], v[216:219], a[24:27], v[140:143]\n"
"ds_read_b64_tr_b16 v[122:123], v229 offset:59392\n"
"v_xor_b32 v229, s75, v229\n"
"v_mfma_f32_16x16x32_bf16 v[140:143], v[212:215], a[20:23], v[140:143]\n"
"ds_read_b64_tr_b16 v[224:225], v233 offset:57344\n"
"v_mfma_f32_16x16x32_bf16 v[140:143], v[208:211], a[16:19], v[140:143]\n"
"ds_read_b64_tr_b16 v[226:227], v233 offset:59392\n"
"v_xor_b32 v233, s75, v233\n"
"v_mfma_f32_16x16x32_bf16 v[140:143], v[192:195], a[76:79], v[140:143]\n"
"v_mfma_f32_16x16x32_bf16 v[140:143], v[188:191], a[72:75], v[140:143]\n"
"v_subrev_f32 v124, s22, v124\n"
"v_subrev_f32 v125, s22, v125\n"
"v_mfma_f32_16x16x32_bf16 v[140:143], v[184:187], a[68:71], v[140:143]\n"
"s_add_u32 s40, s41, s52\n"
"v_subrev_f32 v126, s22, v126\n"
"v_subrev_f32 v127, s22, v127\n"
"v_mfma_f32_16x16x32_bf16 v[140:143], v[180:183], a[64:67], v[140:143]\n"
"buffer_load_dwordx4 v240, s[28:31], s40 offen offset:1024 lds\n"
"v_mfma_f32_16x16x32_bf16 v[156:159], v[180:183], a[80:83], v[156:159]\n"
"v_cvt_pk_bf16_f32 v124, v124, v125\n"
"v_cvt_pk_bf16_f32 v125, v126, v127\n"
"v_mfma_f32_16x16x32_bf16 v[156:159], v[184:187], a[84:87], v[156:159]\n"
"v_mfma_f32_16x16x32_bf16 v[156:159], v[188:191], a[88:91], v[156:159]\n"
"v_mfma_f32_16x16x32_bf16 v[156:159], v[192:195], a[92:95], v[156:159]\n"
"v_mfma_f32_16x16x32_bf16 v[156:159], v[208:211], a[32:35], v[156:159]\n"
"v_mfma_f32_16x16x32_bf16 v[156:159], v[212:215], a[36:39], v[156:159]\n"
"v_subrev_f32 v140, s22, v140\n"
"v_subrev_f32 v141, s22, v141\n"
"v_mfma_f32_16x16x32_bf16 v[156:159], v[216:219], a[40:43], v[156:159]\n"
"s_waitcnt vmcnt(7)\n"
"v_subrev_f32 v142, s22, v142\n"
"v_subrev_f32 v143, s22, v143\n"
"v_mfma_f32_16x16x32_bf16 v[156:159], v[220:223], a[44:47], v[156:159]\n"
"s_barrier\n"
"buffer_store_dword v241, v248, s[84:87], 0 offen sc1\n"
"v_mfma_f32_16x16x32_bf16 v[172:175], v[220:223], a[228:231], v[172:175]\n"
"ds_read_b64_tr_b16 v[220:221], v234 offset:57344\n"
"v_cvt_pk_bf16_f32 v140, v140, v141\n"
"v_mfma_f32_16x16x32_bf16 v[172:175], v[216:219], a[224:227], v[172:175]\n"
"ds_read_b64_tr_b16 v[216:217], v230 offset:57344\n"
"v_cvt_pk_bf16_f32 v141, v142, v143\n"
"v_mfma_f32_16x16x32_bf16 v[172:175], v[212:215], a[212:215], v[172:175]\n"
"ds_read_b64_tr_b16 v[212:213], v234 offset:49152\n"
"v_add_u32 v241, 1, v241\n"
"v_mfma_f32_16x16x32_bf16 v[172:175], v[208:211], a[208:211], v[172:175]\n"
"s_add_u32 s16, s90, s62\n"
"buffer_load_dwordx4 v[84:87], v243, s[32:35], s18 offen offset:3072 sc1\n"
"v_mfma_f32_16x16x32_bf16 v[172:175], v[192:195], a[236:239], v[172:175]\n"
"s_add_u32 s17, s16, 4096\n"
"ds_read_b64_tr_b16 v[192:193], v234 offset:40960\n"
"v_mfma_f32_16x16x32_bf16 v[172:175], v[188:191], a[232:235], v[172:175]\n"
"s_lshl_b32 s62, s70, 9\n"
"ds_read_b64_tr_b16 v[188:189], v230 offset:40960\n"
"v_subrev_f32 v156, s22, v156\n"
"v_mfma_f32_16x16x32_bf16 v[172:175], v[184:187], a[220:223], v[172:175]\n"
"s_add_u32 s40, s41, s53\n"
"ds_read_b64_tr_b16 v[184:185], v234 offset:32768\n"
"v_subrev_f32 v157, s22, v157\n"
"v_mfma_f32_16x16x32_bf16 v[172:175], v[180:183], a[216:219], v[172:175]\n"
"buffer_load_dwordx4 v240, s[28:31], s40 offen offset:2048 lds\n"
"v_mfma_f32_16x16x32_bf16 v[128:131], v[96:99], a[48:51], v[128:131]\n"
"ds_read_b64_tr_b16 v[180:181], v230 offset:32768\n"
"v_subrev_f32 v158, s22, v158\n"
"v_mfma_f32_16x16x32_bf16 v[128:131], v[100:103], a[52:55], v[128:131]\n"
"s_waitcnt lgkmcnt(5)\n"
"ds_read_b64_tr_b16 v[182:183], v230 offset:34816\n"
"v_subrev_f32 v159, s22, v159\n"
"v_mfma_f32_16x16x32_bf16 v[128:131], v[104:107], a[56:59], v[128:131]\n"
"ds_read_b64_tr_b16 v[186:187], v234 offset:34816\n"
"v_cvt_pk_bf16_f32 v156, v156, v157\n"
"v_mfma_f32_16x16x32_bf16 v[128:131], v[108:111], a[60:63], v[128:131]\n"
"ds_read_b64_tr_b16 v[190:191], v230 offset:43008\n"
"v_cvt_pk_bf16_f32 v157, v158, v159\n"
"v_mfma_f32_16x16x32_bf16 v[128:131], v[112:115], a[0:3], v[128:131]\n"
"ds_read_b64_tr_b16 v[194:195], v234 offset:43008\n"
"v_lshlrev_b32 v202, 16, v72\n"
"v_mfma_f32_16x16x32_bf16 v[128:131], v[116:119], a[4:7], v[128:131]\n"
"ds_read_b64_tr_b16 v[208:209], v230 offset:49152\n"
"v_and_b32 v72, s73, v72\n"
"v_mfma_f32_16x16x32_bf16 v[128:131], v[120:123], a[8:11], v[128:131]\n"
"s_add_u32 s40, s41, s54\n"
"ds_read_b64_tr_b16 v[210:211], v230 offset:51200\n"
"v_add_f32 v0, v0, v202\n"
"v_mfma_f32_16x16x32_bf16 v[128:131], v[224:227], a[12:15], v[128:131]\n"
"buffer_load_dwordx4 v240, s[28:31], s40 offen offset:3072 lds\n"
"v_mfma_f32_16x16x32_bf16 v[144:147], v[224:227], a[28:31], v[144:147]\n"
"ds_read_b64_tr_b16 v[214:215], v234 offset:51200\n"
"v_add_f32 v1, v1, v72\n"
"v_mfma_f32_16x16x32_bf16 v[144:147], v[120:123], a[24:27], v[144:147]\n"
"ds_read_b64_tr_b16 v[218:219], v230 offset:59392\n"
"v_lshlrev_b32 v203, 16, v73\n"
"v_mfma_f32_16x16x32_bf16 v[144:147], v[116:119], a[20:23], v[144:147]\n"
"ds_read_b64_tr_b16 v[222:223], v234 offset:59392\n"
"v_and_b32 v73, s73, v73\n"
"v_mfma_f32_16x16x32_bf16 v[144:147], v[112:115], a[16:19], v[144:147]\n"
"buffer_load_dwordx4 v[88:91], v243, s[32:35], s19 offen sc1\n"
"v_mfma_f32_16x16x32_bf16 v[144:147], v[108:111], a[76:79], v[144:147]\n"
"v_add_f32 v2, v2, v203\n"
"v_add_f32 v3, v3, v73\n"
"v_mfma_f32_16x16x32_bf16 v[144:147], v[104:107], a[72:75], v[144:147]\n"
"s_add_u32 s41, s70, 491520\n"
"v_subrev_f32 v128, s22, v128\n"
"v_subrev_f32 v129, s22, v129\n"
"v_mfma_f32_16x16x32_bf16 v[144:147], v[100:103], a[68:71], v[144:147]\n"
"s_add_u32 m0, s71, 49152\n"
"v_subrev_f32 v130, s22, v130\n"
"v_subrev_f32 v131, s22, v131\n"
"v_mfma_f32_16x16x32_bf16 v[144:147], v[96:99], a[64:67], v[144:147]\n"
"buffer_load_dwordx4 v240, s[28:31], s41 offen lds\n"
"v_mfma_f32_16x16x32_bf16 v[160:163], v[96:99], a[80:83], v[160:163]\n"
"s_and_b32 s70, s88, 15\n"
"v_cvt_pk_bf16_f32 v126, v128, v129\n"
"v_cvt_pk_bf16_f32 v127, v130, v131\n"
"v_mfma_f32_16x16x32_bf16 v[160:163], v[100:103], a[84:87], v[160:163]\n"
"s_lshr_b32 s88, s88, 4\n"
"v_lshlrev_b32 v204, 16, v74\n"
"v_and_b32 v74, s73, v74\n"
"v_mfma_f32_16x16x32_bf16 v[160:163], v[104:107], a[88:91], v[160:163]\n"
"s_lshl_b32 s70, s70, 7\n"
"v_add_f32 v4, v4, v204\n"
"v_add_f32 v5, v5, v74\n"
"v_mfma_f32_16x16x32_bf16 v[160:163], v[108:111], a[92:95], v[160:163]\n"
"s_xor_b32 s71, s71, s75\n"
"v_lshlrev_b32 v205, 16, v75\n"
"v_and_b32 v75, s73, v75\n"
"v_mfma_f32_16x16x32_bf16 v[160:163], v[112:115], a[32:35], v[160:163]\n"
"v_add_f32 v6, v6, v205\n"
"v_add_f32 v7, v7, v75\n"
"v_mfma_f32_16x16x32_bf16 v[160:163], v[116:119], a[36:39], v[160:163]\n"
"v_subrev_f32 v144, s22, v144\n"
"v_subrev_f32 v145, s22, v145\n"
"v_mfma_f32_16x16x32_bf16 v[160:163], v[120:123], a[40:43], v[160:163]\n"
"v_subrev_f32 v146, s22, v146\n"
"v_subrev_f32 v147, s22, v147\n"
"v_mfma_f32_16x16x32_bf16 v[160:163], v[224:227], a[44:47], v[160:163]\n"
"buffer_store_dwordx4 v[124:127], v243, s[32:35], s16 offen sc1\n"
"v_cvt_pk_bf16_f32 v142, v144, v145\n"
"v_mfma_f32_16x16x32_bf16 v[176:179], v[224:227], a[228:231], v[176:179]\n"
"ds_read_b64_tr_b16 v[224:225], v235 offset:57344\n"
"v_cvt_pk_bf16_f32 v143, v146, v147\n"
"v_mfma_f32_16x16x32_bf16 v[176:179], v[120:123], a[224:227], v[176:179]\n"
"ds_read_b64_tr_b16 v[120:121], v231 offset:57344\n"
"v_lshlrev_b32 v206, 16, v76\n"
"v_mfma_f32_16x16x32_bf16 v[176:179], v[116:119], a[212:215], v[176:179]\n"
"ds_read_b64_tr_b16 v[116:117], v235 offset:49152\n"
"v_and_b32 v76, s73, v76\n"
"v_mfma_f32_16x16x32_bf16 v[176:179], v[112:115], a[208:211], v[176:179]\n"
"buffer_load_dwordx4 v[92:95], v243, s[32:35], s19 offen offset:1024 sc1\n"
"v_mfma_f32_16x16x32_bf16 v[176:179], v[108:111], a[236:239], v[176:179]\n"
"ds_read_b64_tr_b16 v[108:109], v235 offset:40960\n"
"v_add_f32 v8, v8, v206\n"
"v_mfma_f32_16x16x32_bf16 v[176:179], v[104:107], a[232:235], v[176:179]\n"
"ds_read_b64_tr_b16 v[104:105], v231 offset:40960\n"
"v_subrev_f32 v160, s22, v160\n"
"v_mfma_f32_16x16x32_bf16 v[176:179], v[100:103], a[220:223], v[176:179]\n"
"s_add_u32 s40, s41, s52\n"
"ds_read_b64_tr_b16 v[100:101], v235 offset:32768\n"
"v_subrev_f32 v161, s22, v161\n"
"v_mfma_f32_16x16x32_bf16 v[176:179], v[96:99], a[216:219], v[176:179]\n"
"s_waitcnt vmcnt(14) lgkmcnt(6)\n"
"buffer_load_dwordx4 v240, s[28:31], s40 offen offset:1024 lds\n"
"v_mfma_f32_16x16x32_bf16 v[132:135], v[180:183], a[48:51], v[132:135]\n"
"ds_read_b64_tr_b16 v[96:97], v231 offset:32768\n"
"v_subrev_f32 v162, s22, v162\n"
"v_mfma_f32_16x16x32_bf16 v[132:135], v[184:187], a[52:55], v[132:135]\n"
"ds_read_b64_tr_b16 v[98:99], v231 offset:34816\n"
"v_subrev_f32 v163, s22, v163\n"
"v_mfma_f32_16x16x32_bf16 v[132:135], v[188:191], a[56:59], v[132:135]\n"
"ds_read_b64_tr_b16 v[102:103], v235 offset:34816\n"
"v_cvt_pk_bf16_f32 v158, v160, v161\n"
"v_mfma_f32_16x16x32_bf16 v[132:135], v[192:195], a[60:63], v[132:135]\n"
"ds_read_b64_tr_b16 v[106:107], v231 offset:43008\n"
"v_cvt_pk_bf16_f32 v159, v162, v163\n"
"v_mfma_f32_16x16x32_bf16 v[132:135], v[208:211], a[0:3], v[132:135]\n"
"ds_read_b64_tr_b16 v[110:111], v235 offset:43008\n"
"v_add_f32 v9, v9, v76\n"
"v_mfma_f32_16x16x32_bf16 v[132:135], v[212:215], a[4:7], v[132:135]\n"
"ds_read_b64_tr_b16 v[112:113], v231 offset:49152\n"
"v_lshlrev_b32 v207, 16, v77\n"
"v_mfma_f32_16x16x32_bf16 v[132:135], v[216:219], a[8:11], v[132:135]\n"
"ds_read_b64_tr_b16 v[114:115], v231 offset:51200\n"
"v_and_b32 v77, s73, v77\n"
"v_mfma_f32_16x16x32_bf16 v[132:135], v[220:223], a[12:15], v[132:135]\n"
"buffer_store_dwordx4 v[140:143], v243, s[32:35], s16 offen offset:2048 sc1\n"
"v_add_f32 v10, v10, v207\n"
"v_mfma_f32_16x16x32_bf16 v[148:151], v[220:223], a[28:31], v[148:151]\n"
"ds_read_b64_tr_b16 v[118:119], v235 offset:51200\n"
"v_add_f32 v11, v11, v77\n"
"v_mfma_f32_16x16x32_bf16 v[148:151], v[216:219], a[24:27], v[148:151]\n"
"ds_read_b64_tr_b16 v[122:123], v231 offset:59392\n"
"v_lshlrev_b32 v202, 16, v78\n"
"v_mfma_f32_16x16x32_bf16 v[148:151], v[212:215], a[20:23], v[148:151]\n"
"ds_read_b64_tr_b16 v[226:227], v235 offset:59392\n"
"v_and_b32 v78, s73, v78\n"
"v_mfma_f32_16x16x32_bf16 v[148:151], v[208:211], a[16:19], v[148:151]\n"
"v_add_f32 v12, v12, v202\n"
"v_add_f32 v13, v13, v78\n"
"v_mfma_f32_16x16x32_bf16 v[148:151], v[192:195], a[76:79], v[148:151]\n"
"v_lshlrev_b32 v203, 16, v79\n"
"v_and_b32 v79, s73, v79\n"
"v_mfma_f32_16x16x32_bf16 v[148:151], v[188:191], a[72:75], v[148:151]\n"
"v_add_f32 v14, v14, v203\n"
"v_add_f32 v15, v15, v79\n"
"v_mfma_f32_16x16x32_bf16 v[148:151], v[184:187], a[68:71], v[148:151]\n"
"s_add_u32 s40, s41, s53\n"
"v_lshlrev_b32 v204, 16, v80\n"
"v_and_b32 v80, s73, v80\n"
"v_mfma_f32_16x16x32_bf16 v[148:151], v[180:183], a[64:67], v[148:151]\n"
"buffer_load_dwordx4 v240, s[28:31], s40 offen offset:2048 lds\n"
"v_mfma_f32_16x16x32_bf16 v[164:167], v[180:183], a[80:83], v[164:167]\n"
"v_add_f32 v16, v16, v204\n"
"v_add_f32 v17, v17, v80\n"
"v_mfma_f32_16x16x32_bf16 v[164:167], v[184:187], a[84:87], v[164:167]\n"
"v_lshlrev_b32 v205, 16, v81\n"
"v_and_b32 v81, s73, v81\n"
"v_mfma_f32_16x16x32_bf16 v[164:167], v[188:191], a[88:91], v[164:167]\n"
"v_add_f32 v18, v18, v205\n"
"v_add_f32 v19, v19, v81\n"
"v_mfma_f32_16x16x32_bf16 v[164:167], v[192:195], a[92:95], v[164:167]\n"
"buffer_load_dwordx4 v[244:247], v243, s[32:35], s19 offen offset:2048 sc1\n"
"v_mfma_f32_16x16x32_bf16 v[164:167], v[208:211], a[32:35], v[164:167]\n"
"s_lshl4_add_u32 s18, s72, s61\n"
"v_lshlrev_b32 v206, 16, v82\n"
"v_and_b32 v82, s73, v82\n"
"v_mfma_f32_16x16x32_bf16 v[164:167], v[212:215], a[36:39], v[164:167]\n"
"s_lshl_b32 s19, s72, 2\n"
"v_add_f32 v20, v20, v206\n"
"v_add_f32 v21, v21, v82\n"
"v_mfma_f32_16x16x32_bf16 v[164:167], v[216:219], a[40:43], v[164:167]\n"
"s_waitcnt vmcnt(14)\n"
"s_lshr_b32 s19, s89, s19\n"
"v_lshlrev_b32 v207, 16, v83\n"
"v_mfma_f32_16x16x32_bf16 v[164:167], v[220:223], a[44:47], v[164:167]\n"
"s_barrier\n"
"s_and_b32 s19, s19, 1\n"
"s_waitcnt lgkmcnt(0)\n"
"v_mfma_f32_16x16x32_bf16 v[136:139], v[96:99], a[48:51], v[136:139]\n"
"s_lshl3_add_u32 s18, s18, s19\n"
"ds_read_b64_tr_b16 v[180:181], v228 offset:0\n"
"v_and_b32 v83, s73, v83\n"
"v_mfma_f32_16x16x32_bf16 v[136:139], v[100:103], a[52:55], v[136:139]\n"
"s_lshl_b32 s18, s18, 13\n"
"ds_read_b64_tr_b16 v[182:183], v228 offset:2048\n"
"v_add_f32 v22, v22, v207\n"
"v_mfma_f32_16x16x32_bf16 v[136:139], v[104:107], a[56:59], v[136:139]\n"
"s_add_u32 s19, s18, 4096\n"
"v_lshlrev_b32_e64 v201, 2, s72\n"
"ds_read_b64_tr_b16 v[184:185], v232 offset:0\n"
"v_mfma_f32_16x16x32_bf16 v[136:139], v[108:111], a[60:63], v[136:139]\n"
"s_mov_b32 s69, 15\n"
"global_load_dword v200, v201, s[6:7] sc1\n"
"v_mfma_f32_16x16x32_bf16 v[136:139], v[112:115], a[0:3], v[136:139]\n"
"s_add_u32 s21, s21, 1\n"
"ds_read_b64_tr_b16 v[186:187], v232 offset:2048\n"
"v_add_f32 v23, v23, v83\n"
"v_mfma_f32_16x16x32_bf16 v[136:139], v[116:119], a[4:7], v[136:139]\n"
"ds_read_b64_tr_b16 v[188:189], v228 offset:8192\n"
"v_subrev_f32 v172, s22, v172\n"
"v_mfma_f32_16x16x32_bf16 v[136:139], v[120:123], a[8:11], v[136:139]\n"
"ds_read_b64_tr_b16 v[190:191], v228 offset:10240\n"
"v_subrev_f32 v173, s22, v173\n"
"v_mfma_f32_16x16x32_bf16 v[136:139], v[224:227], a[12:15], v[136:139]\n"
"buffer_store_dwordx4 v[156:159], v243, s[32:35], s17 offen sc1\n"
"v_subrev_f32 v174, s22, v174\n"
"v_mfma_f32_16x16x32_bf16 v[152:155], v[224:227], a[28:31], v[152:155]\n"
"ds_read_b64_tr_b16 v[192:193], v232 offset:8192\n"
"v_subrev_f32 v175, s22, v175\n"
"v_mfma_f32_16x16x32_bf16 v[152:155], v[120:123], a[24:27], v[152:155]\n"
"ds_read_b64_tr_b16 v[194:195], v232 offset:10240\n"
"v_cvt_pk_bf16_f32 v172, v172, v173\n"
"v_mfma_f32_16x16x32_bf16 v[152:155], v[116:119], a[20:23], v[152:155]\n"
"ds_read_b64_tr_b16 v[208:209], v228 offset:16384\n"
"v_cvt_pk_bf16_f32 v173, v174, v175\n"
"v_mfma_f32_16x16x32_bf16 v[152:155], v[112:115], a[16:19], v[152:155]\n"
"ds_read_b64_tr_b16 v[210:211], v228 offset:18432\n"
"v_subrev_f32 v176, s22, v176\n"
"v_mfma_f32_16x16x32_bf16 v[152:155], v[108:111], a[76:79], v[152:155]\n"
"ds_read_b64_tr_b16 v[212:213], v232 offset:16384\n"
"v_subrev_f32 v177, s22, v177\n"
"v_mfma_f32_16x16x32_bf16 v[152:155], v[104:107], a[72:75], v[152:155]\n"
"ds_read_b64_tr_b16 v[214:215], v232 offset:18432\n"
"v_subrev_f32 v178, s22, v178\n"
"v_mfma_f32_16x16x32_bf16 v[152:155], v[100:103], a[68:71], v[152:155]\n"
"s_add_u32 s40, s41, s54\n"
"ds_read_b64_tr_b16 v[216:217], v228 offset:24576\n"
"v_subrev_f32 v179, s22, v179\n"
"v_mfma_f32_16x16x32_bf16 v[152:155], v[96:99], a[64:67], v[152:155]\n"
"buffer_load_dwordx4 v240, s[28:31], s40 offen offset:3072 lds\n"
"v_mfma_f32_16x16x32_bf16 v[168:171], v[96:99], a[80:83], v[168:171]\n"
"ds_read_b64_tr_b16 v[218:219], v228 offset:26624\n"
"v_cvt_pk_bf16_f32 v174, v176, v177\n"
"v_mfma_f32_16x16x32_bf16 v[168:171], v[100:103], a[84:87], v[168:171]\n"
"ds_read_b64_tr_b16 v[220:221], v232 offset:24576\n"
"v_cvt_pk_bf16_f32 v175, v178, v179\n"
"v_mfma_f32_16x16x32_bf16 v[168:171], v[104:107], a[88:91], v[168:171]\n"
"ds_read_b64_tr_b16 v[222:223], v232 offset:26624\n"
"v_subrev_f32 v132, s22, v132\n"
"v_mfma_f32_16x16x32_bf16 v[168:171], v[108:111], a[92:95], v[168:171]\n"
"ds_read_b64_tr_b16 v[96:97], v229 offset:0\n"
"v_subrev_f32 v133, s22, v133\n"
"v_mfma_f32_16x16x32_bf16 v[168:171], v[112:115], a[32:35], v[168:171]\n"
"ds_read_b64_tr_b16 v[98:99], v229 offset:2048\n"
"v_subrev_f32 v134, s22, v134\n"
"v_mfma_f32_16x16x32_bf16 v[168:171], v[116:119], a[36:39], v[168:171]\n"
"ds_read_b64_tr_b16 v[100:101], v233 offset:0\n"
"v_subrev_f32 v135, s22, v135\n"
"v_mfma_f32_16x16x32_bf16 v[168:171], v[120:123], a[40:43], v[168:171]\n"
"s_add_u32 s47, s47, 1\n"
"ds_read_b64_tr_b16 v[102:103], v233 offset:2048\n"
"v_cvt_pk_bf16_f32 v132, v132, v133\n"
"v_mfma_f32_16x16x32_bf16 v[168:171], v[224:227], a[44:47], v[168:171]\n"
"s_waitcnt lgkmcnt(7)\n"
"s_cmp_lg_u32 s47, 9\n"
"ds_read_b64_tr_b16 v[104:105], v229 offset:8192\n"
"s_cbranch_scc1 LGL_%=\n"
"v_mfma_f32_16x16x32_bf16 v[0:3], v[180:183], a[96:99], v[0:3]\n"
"v_xor_b32 v235, s75, v235\n"
"v_subrev_f32 v136, s22, v136\n"
"v_mfma_f32_16x16x32_bf16 v[0:3], v[184:187], a[100:103], v[0:3]\n"
"ds_read_b64_tr_b16 v[114:115], v229 offset:18432\n"
"v_cvt_pk_bf16_f32 v133, v134, v135\n"
"v_mfma_f32_16x16x32_bf16 v[0:3], v[188:191], a[104:107], v[0:3]\n"
"v_mov_b32 v201, v237\n"
"v_subrev_f32 v138, s22, v138\n"
"v_subrev_f32 v139, s22, v139\n"
"v_mfma_f32_16x16x32_bf16 v[0:3], v[192:195], a[108:111], v[0:3]\n"
"global_load_dword v200, v201, s[6:7] sc1\n"
"v_mfma_f32_16x16x32_bf16 v[0:3], v[208:211], a[144:147], v[0:3]\n"
"v_subrev_f32 v167, s22, v167\n"
"v_cvt_pk_bf16_f32 v135, v138, v139\n"
"v_mfma_f32_16x16x32_bf16 v[0:3], v[212:215], a[148:151], v[0:3]\n"
"s_waitcnt lgkmcnt(3)\n"
"v_subrev_f32 v148, s22, v148\n"
"v_subrev_f32 v137, s22, v137\n"
"v_mfma_f32_16x16x32_bf16 v[0:3], v[216:219], a[152:155], v[0:3]\n"
"v_subrev_f32 v165, s22, v165\n"
"v_subrev_f32 v168, s22, v168\n"
"v_mfma_f32_16x16x32_bf16 v[0:3], v[220:223], a[156:159], v[0:3]\n"
"buffer_store_dwordx4 v[172:175], v243, s[32:35], s17 offen offset:2048 sc1\n"
"v_subrev_f32 v151, s22, v151\n"
"v_cvt_pk_bf16_f32 v134, v136, v137\n"
"v_mfma_f32_16x16x32_bf16 v[16:19], v[220:223], a[172:175], v[16:19]\n"
"buffer_store_dwordx4 v[132:135], v243, s[32:35], s16 offen offset:1024 sc1\n"
"v_subrev_f32 v149, s22, v149\n"
"v_subrev_f32 v150, s22, v150\n"
"v_subrev_f32 v164, s22, v164\n"
"v_cvt_pk_bf16_f32 v164, v164, v165\n"
"v_subrev_f32 v153, s22, v153\n"
"v_subrev_f32 v155, s22, v155\n"
"v_mfma_f32_16x16x32_bf16 v[16:19], v[216:219], a[168:171], v[16:19]\n"
"v_cvt_pk_bf16_f32 v148, v148, v149\n"
"v_subrev_f32 v154, s22, v154\n"
"v_mfma_f32_16x16x32_bf16 v[16:19], v[212:215], a[164:167], v[16:19]\n"
"v_cvt_pk_bf16_f32 v149, v150, v151\n"
"v_subrev_f32 v170, s22, v170\n"
"v_mfma_f32_16x16x32_bf16 v[16:19], v[208:211], a[160:163], v[16:19]\n"
"v_subrev_f32 v166, s22, v166\n"
"v_subrev_f32 v171, s22, v171\n"
"v_mfma_f32_16x16x32_bf16 v[16:19], v[192:195], a[124:127], v[16:19]\n"
"v_cvt_pk_bf16_f32 v165, v166, v167\n"
"v_cvt_pk_bf16_f32 v167, v170, v171\n"
"v_mfma_f32_16x16x32_bf16 v[16:19], v[188:191], a[120:123], v[16:19]\n"
"v_subrev_f32 v152, s22, v152\n"
"v_cvt_pk_bf16_f32 v151, v154, v155\n"
"v_mfma_f32_16x16x32_bf16 v[16:19], v[184:187], a[116:119], v[16:19]\n"
"v_cvt_pk_bf16_f32 v150, v152, v153\n"
"v_subrev_f32 v169, s22, v169\n"
"v_mfma_f32_16x16x32_bf16 v[16:19], v[180:183], a[112:115], v[16:19]\n"
"s_waitcnt vmcnt(16)\n"
"buffer_store_dwordx4 v[148:151], v243, s[32:35], s16 offen offset:3072 sc1\n"
"v_cvt_pk_bf16_f32 v166, v168, v169\n"
"v_mfma_f32_16x16x32_bf16 v[32:35], v[180:183], a[128:131], v[32:35]\n"
"buffer_store_dwordx4 v[164:167], v243, s[32:35], s17 offen offset:1024 sc1\n"
"ds_read_b64_tr_b16 v[120:121], v229 offset:24576\n"
"v_mfma_f32_16x16x32_bf16 v[32:35], v[184:187], a[132:135], v[32:35]\n"
"ds_read_b64_tr_b16 v[224:225], v233 offset:24576\n"
"v_xor_b32 v234, s75, v234\n"
"v_mfma_f32_16x16x32_bf16 v[32:35], v[188:191], a[136:139], v[32:35]\n"
"ds_read_b64_tr_b16 v[122:123], v229 offset:26624\n"
"v_lshlrev_b32 v203, 16, v85\n"
"v_mfma_f32_16x16x32_bf16 v[32:35], v[192:195], a[140:143], v[32:35]\n"
"ds_read_b64_tr_b16 v[108:109], v233 offset:8192\n"
"v_lshlrev_b32 v202, 16, v84\n"
"v_mfma_f32_16x16x32_bf16 v[32:35], v[208:211], a[176:179], v[32:35]\n"
"ds_read_b64_tr_b16 v[226:227], v233 offset:26624\n"
"v_and_b32 v84, s73, v84\n"
"v_mfma_f32_16x16x32_bf16 v[32:35], v[212:215], a[180:183], v[32:35]\n"
"ds_read_b64_tr_b16 v[110:111], v233 offset:10240\n"
"v_bfe_u32 v161, v242, 8, 2\n"
"v_mfma_f32_16x16x32_bf16 v[32:35], v[216:219], a[184:187], v[32:35]\n"
"ds_read_b64_tr_b16 v[106:107], v229 offset:10240\n"
"v_lshlrev_b32 v205, 16, v87\n"
"v_mfma_f32_16x16x32_bf16 v[32:35], v[220:223], a[188:191], v[32:35]\n"
"v_xor_b32 v230, s75, v230\n"
"v_lshlrev_b32 v204, 16, v86\n"
"v_mfma_f32_16x16x32_bf16 v[48:51], v[220:223], a[252:255], v[48:51]\n"
"ds_read_b64_tr_b16 v[220:221], v234 offset:24576\n"
"v_add_f32 v26, v26, v203\n"
"v_mfma_f32_16x16x32_bf16 v[48:51], v[216:219], a[248:251], v[48:51]\n"
"ds_read_b64_tr_b16 v[112:113], v229 offset:16384\n"
"v_bfe_u32 v160, v242, 6, 2\n"
"v_mfma_f32_16x16x32_bf16 v[48:51], v[212:215], a[244:247], v[48:51]\n"
"ds_read_b64_tr_b16 v[116:117], v233 offset:16384\n"
"v_add_f32 v24, v24, v202\n"
"v_mfma_f32_16x16x32_bf16 v[48:51], v[208:211], a[240:243], v[48:51]\n"
"ds_read_b64_tr_b16 v[118:119], v233 offset:18432\n"
"v_add_f32 v25, v25, v84\n"
"v_mfma_f32_16x16x32_bf16 v[48:51], v[192:195], a[204:207], v[48:51]\n"
"ds_read_b64_tr_b16 v[210:211], v230 offset:18432\n"
"v_add_f32 v28, v28, v204\n"
"v_mfma_f32_16x16x32_bf16 v[48:51], v[188:191], a[200:203], v[48:51]\n"
"ds_read_b64_tr_b16 v[188:189], v230 offset:8192\n"
"v_and_b32 v86, s73, v86\n"
"v_mfma_f32_16x16x32_bf16 v[48:51], v[184:187], a[196:199], v[48:51]\n"
"ds_read_b64_tr_b16 v[194:195], v234 offset:10240\n"
"v_mov_b32 v164, 0\n"
"v_mfma_f32_16x16x32_bf16 v[48:51], v[180:183], a[192:195], v[48:51]\n"
"s_waitcnt vmcnt(15)\n"
"v_xor_b32 v160, v160, v161\n"
"v_lshlrev_b32 v202, 16, v90\n"
"v_mfma_f32_16x16x32_bf16 v[4:7], v[96:99], a[96:99], v[4:7]\n"
"s_waitcnt lgkmcnt(6)\n"
"ds_read_b64_tr_b16 v[222:223], v234 offset:26624\n"
"v_lshlrev_b32 v203, 16, v91\n"
"v_mfma_f32_16x16x32_bf16 v[4:7], v[100:103], a[100:103], v[4:7]\n"
"ds_read_b64_tr_b16 v[184:185], v234 offset:0\n"
"v_lshlrev_b32 v206, 16, v88\n"
"v_mfma_f32_16x16x32_bf16 v[4:7], v[104:107], a[104:107], v[4:7]\n"
"ds_read_b64_tr_b16 v[216:217], v230 offset:24576\n"
"v_mfma_f32_16x16x32_bf16 v[4:7], v[108:111], a[108:111], v[4:7]\n"
"s_waitcnt lgkmcnt(6)\n"
"ds_read_b64_tr_b16 v[218:219], v230 offset:26624\n"
"v_and_b32 v91, s73, v91\n"
"v_mfma_f32_16x16x32_bf16 v[4:7], v[112:115], a[144:147], v[4:7]\n"
"s_waitcnt vmcnt(4)\n"
"ds_read_b64_tr_b16 v[190:191], v230 offset:10240\n"
"v_lshlrev_b32 v207, 16, v89\n"
"v_mfma_f32_16x16x32_bf16 v[4:7], v[116:119], a[148:151], v[4:7]\n"
"v_subrev_u32 v200, s21, v200\n"
"ds_read_b64_tr_b16 v[208:209], v230 offset:16384\n"
"v_mfma_f32_16x16x32_bf16 v[4:7], v[120:123], a[152:155], v[4:7]\n"
"v_cmp_lt_u32 vcc, s69, v200\n"
"ds_read_b64_tr_b16 v[182:183], v230 offset:2048\n"
"v_mfma_f32_16x16x32_bf16 v[4:7], v[224:227], a[156:159], v[4:7]\n"
"ds_read_b64_tr_b16 v[180:181], v230 offset:0\n"
"s_cbranch_vccnz LGQ_%=\nLGQK_%=:\n"
"v_mfma_f32_16x16x32_bf16 v[20:23], v[224:227], a[172:175], v[20:23]\n"
"s_bfe_u32 s20, s89, 65536\ns_lshl_b32 s20, s20, 13\ns_add_u32 s20, s20, 0\n"
"ds_read_b64_tr_b16 v[214:215], v234 offset:18432\n"
"v_and_b32 v85, s73, v85\n"
"v_mfma_f32_16x16x32_bf16 v[20:23], v[120:123], a[168:171], v[20:23]\n"
"buffer_load_dwordx4 v[124:127], v236, s[32:35], s20 offen sc1\n"
"v_mfma_f32_16x16x32_bf16 v[20:23], v[116:119], a[164:167], v[20:23]\n"
"v_and_b32 v90, s73, v90\n"
"v_add_f32 v37, v37, v90\n"
"v_mfma_f32_16x16x32_bf16 v[20:23], v[112:115], a[160:163], v[20:23]\n"
"s_bfe_u32 s20, s89, 65540\ns_lshl_b32 s20, s20, 13\ns_add_u32 s20, s20, 1048576\n"
"v_add_f32 v36, v36, v202\n"
"v_add_f32 v38, v38, v203\n"
"v_mfma_f32_16x16x32_bf16 v[20:23], v[108:111], a[124:127], v[20:23]\n"
"buffer_load_dwordx4 v[128:131], v236, s[32:35], s20 offen sc1\n"
"v_mfma_f32_16x16x32_bf16 v[20:23], v[104:107], a[120:123], v[20:23]\n"
"v_add_u32 v160, -1, v160\n"
"v_add_f32 v39, v39, v91\n"
"v_mfma_f32_16x16x32_bf16 v[20:23], v[100:103], a[116:119], v[20:23]\n"
"s_bfe_u32 s20, s89, 65544\ns_lshl_b32 s20, s20, 13\ns_add_u32 s20, s20, 2097152\n"
"v_add_f32 v32, v32, v206\n"
"v_mfma_f32_16x16x32_bf16 v[20:23], v[96:99], a[112:115], v[20:23]\n"
"buffer_load_dwordx4 v[132:135], v236, s[32:35], s20 offen sc1\n"
"v_mfma_f32_16x16x32_bf16 v[36:39], v[96:99], a[128:131], v[36:39]\n"
"v_lshlrev_b32 v204, 16, v92\n"
"v_add_f32 v40, v40, v204\n"
"v_mfma_f32_16x16x32_bf16 v[36:39], v[100:103], a[132:135], v[36:39]\n"
"s_bfe_u32 s20, s89, 65548\ns_lshl_b32 s20, s20, 13\ns_add_u32 s20, s20, 3145728\n"
"ds_read_b64_tr_b16 v[212:213], v234 offset:16384\n"
"v_add_f32 v30, v30, v205\n"
"v_mfma_f32_16x16x32_bf16 v[36:39], v[104:107], a[136:139], v[36:39]\n"
"buffer_load_dwordx4 v[136:139], v236, s[32:35], s20 offen sc1\n"
"v_mfma_f32_16x16x32_bf16 v[36:39], v[108:111], a[140:143], v[36:39]\n"
"v_and_b32 v92, s73, v92\n"
"v_lshlrev_b32 v206, 16, v94\n"
"v_mfma_f32_16x16x32_bf16 v[36:39], v[112:115], a[176:179], v[36:39]\n"
"s_bfe_u32 s20, s89, 65552\ns_lshl_b32 s20, s20, 13\ns_add_u32 s20, s20, 4194304\n"
"ds_read_b64_tr_b16 v[186:187], v234 offset:2048\n"
"ds_read_b64_tr_b16 v[192:193], v234 offset:8192\n"
"v_mfma_f32_16x16x32_bf16 v[36:39], v[116:119], a[180:183], v[36:39]\n"
"buffer_load_dwordx4 v[140:143], v236, s[32:35], s20 offen sc1\n"
"v_mfma_f32_16x16x32_bf16 v[36:39], v[120:123], a[184:187], v[36:39]\n"
"v_and_b32 v94, s73, v94\n"
"v_mfma_f32_16x16x32_bf16 v[36:39], v[224:227], a[188:191], v[36:39]\n"
"s_bfe_u32 s20, s89, 65556\ns_lshl_b32 s20, s20, 13\ns_add_u32 s20, s20, 5242880\n"
"v_add_f32 v34, v34, v207\n"
"v_lshlrev_b32 v205, 16, v93\n"
"v_mfma_f32_16x16x32_bf16 v[52:55], v[224:227], a[252:255], v[52:55]\n"
"buffer_load_dwordx4 v[144:147], v236, s[32:35], s20 offen sc1\n"
"v_mfma_f32_16x16x32_bf16 v[52:55], v[120:123], a[248:251], v[52:55]\n"
"v_ashrrev_i32 v160, 31, v160\n"
"v_add_f32 v44, v44, v206\n"
"v_mfma_f32_16x16x32_bf16 v[52:55], v[116:119], a[244:247], v[52:55]\n"
"s_bfe_u32 s20, s89, 65560\ns_lshl_b32 s20, s20, 13\ns_add_u32 s20, s20, 6291456\n"
"v_xor_b32 v231, s75, v231\n"
"v_lshlrev_b32 v207, 16, v95\n"
"v_mfma_f32_16x16x32_bf16 v[52:55], v[112:115], a[240:243], v[52:55]\n"
"buffer_load_dwordx4 v[148:151], v236, s[32:35], s20 offen sc1\n"
"v_mfma_f32_16x16x32_bf16 v[52:55], v[108:111], a[204:207], v[52:55]\n"
"s_bfe_u32 s20, s89, 65564\ns_lshl_b32 s20, s20, 13\ns_add_u32 s20, s20, 7340032\n"
"v_and_b32 v87, s73, v87\n"
"v_and_b32 v95, s73, v95\n"
"v_mfma_f32_16x16x32_bf16 v[52:55], v[104:107], a[200:203], v[52:55]\n"
"buffer_load_dwordx4 v[152:155], v236, s[32:35], s20 offen sc1\n"
"v_mfma_f32_16x16x32_bf16 v[52:55], v[100:103], a[196:199], v[52:55]\n"
"s_waitcnt vmcnt(8)\n"
"s_add_u32 s21, s21, 1\n"
"v_and_b32 v88, s73, v88\n"
"v_mfma_f32_16x16x32_bf16 v[52:55], v[96:99], a[192:195], v[52:55]\n"
"s_barrier\n"
"s_mov_b32 s52, 40960\n"
"buffer_store_dword v241, v248, s[84:87], 0 offen sc1\n"
"s_waitcnt lgkmcnt(0)\n"
"v_mfma_f32_16x16x32_bf16 v[8:11], v[180:183], a[96:99], v[8:11]\n"
"s_mov_b32 s53, 81920\n"
"ds_read_b64_tr_b16 v[98:99], v231 offset:2048\n"
"v_add_f32 v42, v42, v205\n"
"v_mfma_f32_16x16x32_bf16 v[8:11], v[184:187], a[100:103], v[8:11]\n"
"ds_read_b64_tr_b16 v[112:113], v231 offset:16384\n"
"v_add_f32 v31, v31, v87\n"
"v_mfma_f32_16x16x32_bf16 v[8:11], v[188:191], a[104:107], v[8:11]\n"
"v_add_f32 v29, v29, v86\n"
"v_and_b32 v161, 16, v242\n"
"v_mfma_f32_16x16x32_bf16 v[8:11], v[192:195], a[108:111], v[8:11]\n"
"ds_read_b64_tr_b16 v[118:119], v235 offset:18432\n"
"v_and_b32 v160, 0x3f80, v160\n"
"v_mfma_f32_16x16x32_bf16 v[8:11], v[208:211], a[144:147], v[8:11]\n"
"ds_read_b64_tr_b16 v[102:103], v235 offset:2048\n"
"v_add_f32 v27, v27, v85\n"
"v_mfma_f32_16x16x32_bf16 v[8:11], v[212:215], a[148:151], v[8:11]\n"
"ds_read_b64_tr_b16 v[106:107], v231 offset:10240\n"
"v_lshlrev_b32 v160, v161, v160\n"
"v_mfma_f32_16x16x32_bf16 v[8:11], v[216:219], a[152:155], v[8:11]\n"
"ds_read_b64_tr_b16 v[96:97], v231 offset:0\n"
"v_and_b32 v93, s73, v93\n"
"v_mfma_f32_16x16x32_bf16 v[8:11], v[220:223], a[156:159], v[8:11]\n"
"ds_read_b64_tr_b16 v[108:109], v235 offset:8192\n"
"v_add_f32 v41, v41, v92\n"
"v_mfma_f32_16x16x32_bf16 v[24:27], v[220:223], a[172:175], v[24:27]\n"
"v_bfe_i32 v161, v242, 5, 1\n"
"v_lshlrev_b32 v206, 16, v244\n"
"v_mfma_f32_16x16x32_bf16 v[24:27], v[216:219], a[168:171], v[24:27]\n"
"ds_read_b64_tr_b16 v[114:115], v231 offset:18432\n"
"v_mfma_f32_16x16x32_bf16 v[24:27], v[212:215], a[164:167], v[24:27]\n"
"ds_read_b64_tr_b16 v[104:105], v231 offset:8192\n"
"v_lshlrev_b32 v203, 16, v247\n"
"v_mfma_f32_16x16x32_bf16 v[24:27], v[208:211], a[160:163], v[24:27]\n"
"ds_read_b64_tr_b16 v[116:117], v235 offset:16384\n"
"v_add_f32 v48, v48, v206\n"
"v_mfma_f32_16x16x32_bf16 v[24:27], v[192:195], a[124:127], v[24:27]\n"
"ds_read_b64_tr_b16 v[110:111], v235 offset:10240\n"
"v_and_b32 v89, s73, v89\n"
"v_mfma_f32_16x16x32_bf16 v[24:27], v[188:191], a[120:123], v[24:27]\n"
"v_add_f32 v45, v45, v94\n"
"v_add_f32 v47, v47, v95\n"
"v_mfma_f32_16x16x32_bf16 v[24:27], v[184:187], a[116:119], v[24:27]\n"
"ds_read_b64_tr_b16 v[100:101], v235 offset:0\n"
"v_add_f32 v43, v43, v93\n"
"v_mfma_f32_16x16x32_bf16 v[24:27], v[180:183], a[112:115], v[24:27]\n"
"ds_read_b64_tr_b16 v[122:123], v231 offset:26624\n"
"v_add_f32 v46, v46, v207\n"
"v_mfma_f32_16x16x32_bf16 v[40:43], v[180:183], a[128:131], v[40:43]\n"
"v_add_f32 v33, v33, v88\n"
"v_and_b32 v244, s73, v244\n"
"v_mfma_f32_16x16x32_bf16 v[40:43], v[184:187], a[132:135], v[40:43]\n"
"ds_read_b64_tr_b16 v[120:121], v231 offset:24576\n"
"v_lshlrev_b32 v207, 16, v245\n"
"v_mfma_f32_16x16x32_bf16 v[40:43], v[188:191], a[136:139], v[40:43]\n"
"v_and_b32 v163, v160, v161\n"
"v_lshlrev_b32 v202, 16, v246\n"
"v_mfma_f32_16x16x32_bf16 v[40:43], v[192:195], a[140:143], v[40:43]\n"
"ds_read_b64_tr_b16 v[224:225], v235 offset:24576\n"
"v_and_b32 v246, s73, v246\n"
"v_mfma_f32_16x16x32_bf16 v[40:43], v[208:211], a[176:179], v[40:43]\n"
"v_add_f32 v52, v52, v202\n"
"v_and_b32 v247, s73, v247\n"
"v_mfma_f32_16x16x32_bf16 v[40:43], v[212:215], a[180:183], v[40:43]\n"
"ds_read_b64_tr_b16 v[226:227], v235 offset:26624\n"
"v_and_b32 v245, s73, v245\n"
"v_mfma_f32_16x16x32_bf16 v[40:43], v[216:219], a[184:187], v[40:43]\n"
"v_mov_b32 v161, 0\n"
"v_add_f32 v54, v54, v203\n"
"v_mfma_f32_16x16x32_bf16 v[40:43], v[220:223], a[188:191], v[40:43]\n"
"s_barrier\n"
"s_waitcnt lgkmcnt(3)\n"
"v_mfma_f32_16x16x32_bf16 v[12:15], v[96:99], a[96:99], v[12:15]\n"
"ds_read_b64_tr_b16 v[184:185], v232 offset:32768\n"
"v_add_f32 v49, v49, v244\n"
"v_mfma_f32_16x16x32_bf16 v[12:15], v[100:103], a[100:103], v[12:15]\n"
"ds_read_b64_tr_b16 v[216:217], v228 offset:57344\n"
"v_mfma_f32_16x16x32_bf16 v[12:15], v[104:107], a[104:107], v[12:15]\n"
"ds_read_b64_tr_b16 v[188:189], v228 offset:40960\n"
"v_add_f32 v35, v35, v89\n"
"v_mfma_f32_16x16x32_bf16 v[12:15], v[108:111], a[108:111], v[12:15]\n"
"ds_read_b64_tr_b16 v[190:191], v228 offset:43008\n"
"v_mfma_f32_16x16x32_bf16 v[12:15], v[112:115], a[144:147], v[12:15]\n"
"ds_read_b64_tr_b16 v[192:193], v232 offset:40960\n"
"v_add_f32 v53, v53, v246\n"
"v_mfma_f32_16x16x32_bf16 v[12:15], v[116:119], a[148:151], v[12:15]\n"
"s_waitcnt lgkmcnt(5)\n"
"ds_read_b64_tr_b16 v[212:213], v232 offset:49152\n"
"v_add_f32 v51, v51, v245\n"
"v_mfma_f32_16x16x32_bf16 v[12:15], v[120:123], a[152:155], v[12:15]\n"
"ds_read_b64_tr_b16 v[182:183], v228 offset:34816\n"
"v_xor_b32 v162, v160, v163\n"
"v_mfma_f32_16x16x32_bf16 v[12:15], v[224:227], a[156:159], v[12:15]\n"
"global_load_dword v200, v201, s[6:7] sc1\n"
"v_mfma_f32_16x16x32_bf16 v[28:31], v[224:227], a[172:175], v[28:31]\n"
"v_add_f32 v50, v50, v207\n"
"v_add_f32 v55, v55, v247\n"
"v_mfma_f32_16x16x32_bf16 v[28:31], v[120:123], a[168:171], v[28:31]\n"
"s_waitcnt vmcnt(9)\n"
"v_lshlrev_b32 v58, 16, v125\n"
"v_lshlrev_b32 v62, 16, v127\n"
"v_mfma_f32_16x16x32_bf16 v[28:31], v[116:119], a[164:167], v[28:31]\n"
"ds_read_b64_tr_b16 v[186:187], v232 offset:34816\n"
"ds_read_b64_tr_b16 v[214:215], v232 offset:51200\n"
"v_mfma_f32_16x16x32_bf16 v[28:31], v[112:115], a[160:163], v[28:31]\n"
"ds_read_b64_tr_b16 v[194:195], v232 offset:43008\n"
"v_and_b32 v61, s73, v126\n"
"v_mfma_f32_16x16x32_bf16 v[28:31], v[108:111], a[124:127], v[28:31]\n"
"s_waitcnt vmcnt(8)\n"
"ds_read_b64_tr_b16 v[210:211], v228 offset:51200\n"
"v_and_b32 v57, s73, v124\n"
"v_mfma_f32_16x16x32_bf16 v[28:31], v[104:107], a[120:123], v[28:31]\n"
"ds_read_b64_tr_b16 v[222:223], v232 offset:59392\n"
"v_lshlrev_b32 v56, 16, v124\n"
"v_mfma_f32_16x16x32_bf16 v[28:31], v[100:103], a[116:119], v[28:31]\n"
"ds_read_b64_tr_b16 v[180:181], v228 offset:32768\n"
"v_lshlrev_b32 v205, 16, v129\n"
"v_mfma_f32_16x16x32_bf16 v[28:31], v[96:99], a[112:115], v[28:31]\n"
"s_waitcnt vmcnt(7)\n"
"v_and_b32 v59, s73, v125\n"
"v_lshlrev_b32 v206, 16, v130\n"
"v_mfma_f32_16x16x32_bf16 v[44:47], v[96:99], a[128:131], v[44:47]\n"
"ds_read_b64_tr_b16 v[220:221], v232 offset:57344\n"
"v_lshlrev_b32 v204, 16, v128\n"
"v_mfma_f32_16x16x32_bf16 v[44:47], v[100:103], a[132:135], v[44:47]\n"
"ds_read_b64_tr_b16 v[208:209], v228 offset:49152\n"
"v_and_b32 v130, s73, v130\n"
"v_mfma_f32_16x16x32_bf16 v[44:47], v[104:107], a[136:139], v[44:47]\n"
"s_waitcnt vmcnt(6)\n"
"ds_read_b64_tr_b16 v[102:103], v233 offset:34816\n"
"v_add_f32 v58, v58, v205\n"
"v_mfma_f32_16x16x32_bf16 v[44:47], v[108:111], a[140:143], v[44:47]\n"
"v_lshlrev_b32 v207, 16, v131\n"
"v_add_f32 v62, v62, v207\n"
"v_mfma_f32_16x16x32_bf16 v[44:47], v[112:115], a[176:179], v[44:47]\n"
"ds_read_b64_tr_b16 v[218:219], v228 offset:59392\n"
"v_and_b32 v131, s73, v131\n"
"v_mfma_f32_16x16x32_bf16 v[44:47], v[116:119], a[180:183], v[44:47]\n"
"s_waitcnt vmcnt(5)\n"
"ds_read_b64_tr_b16 v[96:97], v229 offset:32768\n"
"v_lshlrev_b32 v205, 16, v135\n"
"v_mfma_f32_16x16x32_bf16 v[44:47], v[120:123], a[184:187], v[44:47]\n"
"v_lshlrev_b32 v202, 16, v132\n"
"v_lshlrev_b32 v207, 16, v137\n"
"v_mfma_f32_16x16x32_bf16 v[44:47], v[224:227], a[188:191], v[44:47]\n"
"s_waitcnt lgkmcnt(5)\n"
"ds_read_b64_tr_b16 v[120:121], v229 offset:57344\n"
"v_add_f32 v56, v56, v204\n"
"v_mfma_f32_16x16x32_bf16 v[0:3], v[180:183], a[48:51], v[0:3]\n"
"s_waitcnt vmcnt(4)\n"
"ds_read_b64_tr_b16 v[116:117], v233 offset:49152\n"
"v_add_f32 v61, v61, v130\n"
"v_mfma_f32_16x16x32_bf16 v[0:3], v[184:187], a[52:55], v[0:3]\n"
"v_lshlrev_b32 v60, 16, v126\n"
"v_lshlrev_b32 v204, 16, v134\n"
"v_mfma_f32_16x16x32_bf16 v[0:3], v[188:191], a[56:59], v[0:3]\n"
"ds_read_b64_tr_b16 v[110:111], v233 offset:43008\n"
"v_add_f32 v56, v56, v202\n"
"v_mfma_f32_16x16x32_bf16 v[0:3], v[192:195], a[60:63], v[0:3]\n"
"s_waitcnt lgkmcnt(5)\n"
"s_waitcnt vmcnt(3)\n"
"ds_read_b64_tr_b16 v[112:113], v229 offset:49152\n"
"v_and_b32 v63, s73, v127\n"
"v_mfma_f32_16x16x32_bf16 v[0:3], v[208:211], a[0:3], v[0:3]\n"
"ds_read_b64_tr_b16 v[224:225], v233 offset:57344\n"
"v_and_b32 v134, s73, v134\n"
"v_mfma_f32_16x16x32_bf16 v[0:3], v[212:215], a[4:7], v[0:3]\n"
"s_waitcnt lgkmcnt(5)\n"
"s_waitcnt vmcnt(2)\n"
"ds_read_b64_tr_b16 v[108:109], v233 offset:40960\n"
"v_and_b32 v129, s73, v129\n"
"v_mfma_f32_16x16x32_bf16 v[0:3], v[216:219], a[8:11], v[0:3]\n"
"v_add_f32 v60, v60, v206\n"
"v_add_f32 v60, v60, v204\n"
"v_mfma_f32_16x16x32_bf16 v[0:3], v[220:223], a[12:15], v[0:3]\n"
"ds_read_b64_tr_b16 v[98:99], v229 offset:34816\n"
"ds_read_b64_tr_b16 v[122:123], v229 offset:59392\n"
"v_mfma_f32_16x16x32_bf16 v[16:19], v[220:223], a[28:31], v[16:19]\n"
"v_mov_b32 v165, 0\n"
"v_add_f32 v59, v59, v129\n"
"v_mfma_f32_16x16x32_bf16 v[16:19], v[216:219], a[24:27], v[16:19]\n"
"v_add_f32 v62, v62, v205\n"
"v_lshlrev_b32 v206, 16, v136\n"
"v_mfma_f32_16x16x32_bf16 v[16:19], v[212:215], a[20:23], v[16:19]\n"
"ds_read_b64_tr_b16 v[104:105], v229 offset:40960\n"
"ds_read_b64_tr_b16 v[226:227], v233 offset:59392\n"
"v_mfma_f32_16x16x32_bf16 v[16:19], v[208:211], a[16:19], v[16:19]\n"
"v_add_f32 v56, v56, v206\n"
"v_lshlrev_b32 v202, 16, v138\n"
"v_mfma_f32_16x16x32_bf16 v[16:19], v[192:195], a[76:79], v[16:19]\n"
"s_waitcnt vmcnt(0)\n"
"ds_read_b64_tr_b16 v[118:119], v233 offset:51200\n"
"v_subrev_f32 v2, s22, v2\n"
"v_mfma_f32_16x16x32_bf16 v[16:19], v[188:191], a[72:75], v[16:19]\n"
"v_subrev_u32 v200, s21, v200\n"
"ds_read_b64_tr_b16 v[114:115], v229 offset:51200\n"
"v_mfma_f32_16x16x32_bf16 v[16:19], v[184:187], a[68:71], v[16:19]\n"
"v_cmp_lt_u32 vcc, s69, v200\n"
"ds_read_b64_tr_b16 v[100:101], v233 offset:32768\n"
"v_mfma_f32_16x16x32_bf16 v[16:19], v[180:183], a[64:67], v[16:19]\n"
"v_lshlrev_b32 v206, 16, v142\n"
"s_cbranch_vccnz LGR_%=\nLGRK_%=:\n"
"v_mfma_f32_16x16x32_bf16 v[32:35], v[180:183], a[80:83], v[32:35]\n"
"v_subrev_f32 v3, s22, v3\n"
"s_cmp_eq_u32 s93, 0\ns_cbranch_scc1 LEP_%=\nLEPK_%=:\n"
"v_mfma_f32_16x16x32_bf16 v[32:35], v[184:187], a[84:87], v[32:35]\n"
"s_bfe_u32 s20, s89, 65536\ns_lshl_b32 s20, s20, 13\ns_add_u32 s20, s20, 65536\n"
"ds_read_b64_tr_b16 v[106:107], v229 offset:43008\n"
"v_and_b32 v128, s73, v128\n"
"v_mfma_f32_16x16x32_bf16 v[32:35], v[188:191], a[88:91], v[32:35]\n"
"buffer_load_dwordx4 v[72:75], v236, s[32:35], s20 offen sc1\n"
"v_mfma_f32_16x16x32_bf16 v[32:35], v[192:195], a[92:95], v[32:35]\n"
"v_lshlrev_b32 v203, 16, v133\n"
"v_lshlrev_b32 v205, 16, v141\n"
"v_mfma_f32_16x16x32_bf16 v[32:35], v[208:211], a[32:35], v[32:35]\n"
"s_bfe_u32 s20, s89, 65540\ns_lshl_b32 s20, s20, 13\ns_add_u32 s20, s20, 1114112\n"
"v_lshlrev_b32 v204, 16, v140\n"
"v_add_f32 v56, v56, v204\n"
"v_mfma_f32_16x16x32_bf16 v[32:35], v[212:215], a[36:39], v[32:35]\n"
"buffer_load_dwordx4 v[76:79], v236, s[32:35], s20 offen sc1\n"
"v_mfma_f32_16x16x32_bf16 v[32:35], v[216:219], a[40:43], v[32:35]\n"
"v_and_b32 v136, s73, v136\n"
"v_subrev_f32 v17, s22, v17\n"
"v_mfma_f32_16x16x32_bf16 v[32:35], v[220:223], a[44:47], v[32:35]\n"
"s_bfe_u32 s20, s89, 65544\ns_lshl_b32 s20, s20, 13\ns_add_u32 s20, s20, 2162688\n"
"v_add_f32 v61, v61, v134\n"
"v_and_b32 v141, s73, v141\n"
"v_mfma_f32_16x16x32_bf16 v[48:51], v[220:223], a[228:231], v[48:51]\n"
"buffer_load_dwordx4 v[80:83], v236, s[32:35], s20 offen sc1\n"
"v_mfma_f32_16x16x32_bf16 v[48:51], v[216:219], a[224:227], v[48:51]\n"
"v_add_f32 v58, v58, v203\n"
"v_subrev_f32 v19, s22, v19\n"
"v_mfma_f32_16x16x32_bf16 v[48:51], v[212:215], a[212:215], v[48:51]\n"
"s_bfe_u32 s20, s89, 65548\ns_lshl_b32 s20, s20, 13\ns_add_u32 s20, s20, 3211264\n"
"v_and_b32 v140, s73, v140\n"
"v_lshlrev_b32 v204, 16, v146\n"
"v_mfma_f32_16x16x32_bf16 v[48:51], v[208:211], a[208:211], v[48:51]\n"
"buffer_load_dwordx4 v[84:87], v236, s[32:35], s20 offen sc1\n"
"v_mfma_f32_16x16x32_bf16 v[48:51], v[192:195], a[236:239], v[48:51]\n"
"v_add_f32 v57, v57, v128\n"
"v_subrev_f32 v34, s22, v34\n"
"v_mfma_f32_16x16x32_bf16 v[48:51], v[188:191], a[232:235], v[48:51]\n"
"s_bfe_u32 s20, s89, 65552\ns_lshl_b32 s20, s20, 13\ns_add_u32 s20, s20, 4259840\n"
"ds_read_b64_tr_b16 v[214:215], v234 offset:51200\n"
"v_and_b32 v132, s73, v132\n"
"v_mfma_f32_16x16x32_bf16 v[48:51], v[184:187], a[220:223], v[48:51]\n"
"buffer_load_dwordx4 v[88:91], v236, s[32:35], s20 offen sc1\n"
"v_mfma_f32_16x16x32_bf16 v[48:51], v[180:183], a[216:219], v[48:51]\n"
"s_waitcnt lgkmcnt(1)\n"
"v_add_f32 v60, v60, v202\n"
"v_add_f32 v60, v60, v206\n"
"v_mfma_f32_16x16x32_bf16 v[4:7], v[96:99], a[48:51], v[4:7]\n"
"s_bfe_u32 s20, s89, 65556\ns_lshl_b32 s20, s20, 13\ns_add_u32 s20, s20, 5308416\n"
"v_lshlrev_b32 v206, 16, v148\n"
"v_subrev_f32 v35, s22, v35\n"
"v_mfma_f32_16x16x32_bf16 v[4:7], v[100:103], a[52:55], v[4:7]\n"
"buffer_load_dwordx4 v[92:95], v236, s[32:35], s20 offen sc1\n"
"v_mfma_f32_16x16x32_bf16 v[4:7], v[104:107], a[56:59], v[4:7]\n"
"v_add_f32 v60, v60, v204\n"
"v_subrev_f32 v0, s22, v0\n"
"v_mfma_f32_16x16x32_bf16 v[4:7], v[108:111], a[60:63], v[4:7]\n"
"s_bfe_u32 s20, s89, 65560\ns_lshl_b32 s20, s20, 13\ns_add_u32 s20, s20, 6356992\n"
"v_add_f32 v57, v57, v132\n"
"v_subrev_f32 v33, s22, v33\n"
"v_mfma_f32_16x16x32_bf16 v[4:7], v[112:115], a[0:3], v[4:7]\n"
"buffer_load_dwordx4 v[244:247], v236, s[32:35], s20 offen sc1\n"
"v_mfma_f32_16x16x32_bf16 v[4:7], v[116:119], a[4:7], v[4:7]\n"
"v_and_b32 v138, s73, v138\n"
"v_subrev_f32 v18, s22, v18\n"
"v_mfma_f32_16x16x32_bf16 v[4:7], v[120:123], a[8:11], v[4:7]\n"
"s_bfe_u32 s20, s89, 65564\ns_lshl_b32 s20, s20, 13\ns_add_u32 s20, s20, 7405568\n"
"v_and_b32 v135, s73, v135\n"
"v_subrev_f32 v49, s22, v49\n"
"v_mfma_f32_16x16x32_bf16 v[4:7], v[224:227], a[12:15], v[4:7]\n"
"buffer_load_dwordx4 v[156:159], v236, s[32:35], s20 offen sc1\n"
"v_add_f32 v58, v58, v207\n"
"v_lshlrev_b32 v203, 16, v139\n"
"v_add_f32 v58, v58, v205\n"
"v_and_b32 v142, s73, v142\n"
"v_lshlrev_b32 v202, 16, v144\n"
"v_add_f32 v56, v56, v202\n"
"v_add_f32 v56, v56, v206\n"
"v_subrev_f32 v1, s22, v1\n"
"v_mfma_f32_16x16x32_bf16 v[20:23], v[224:227], a[28:31], v[20:23]\n"
"v_add_f32 v63, v63, v131\n"
"v_lshlrev_b32 v204, 16, v152\n"
"v_mfma_f32_16x16x32_bf16 v[20:23], v[120:123], a[24:27], v[20:23]\n"
"v_and_b32 v133, s73, v133\n"
"v_add_f32 v59, v59, v133\n"
"v_mfma_f32_16x16x32_bf16 v[20:23], v[116:119], a[20:23], v[20:23]\n"
"ds_read_b64_tr_b16 v[218:219], v230 offset:59392\n"
"v_and_b32 v137, s73, v137\n"
"v_mfma_f32_16x16x32_bf16 v[20:23], v[112:115], a[16:19], v[20:23]\n"
"v_and_b32 v148, s73, v148\n"
"v_subrev_f32 v16, s22, v16\n"
"v_mfma_f32_16x16x32_bf16 v[20:23], v[108:111], a[76:79], v[20:23]\n"
"v_add_f32 v62, v62, v203\n"
"v_subrev_f32 v6, s22, v6\n"
"v_mfma_f32_16x16x32_bf16 v[20:23], v[104:107], a[72:75], v[20:23]\n"
"v_add_f32 v57, v57, v136\n"
"v_lshlrev_b32 v205, 16, v147\n"
"v_mfma_f32_16x16x32_bf16 v[20:23], v[100:103], a[68:71], v[20:23]\n"
"ds_read_b64_tr_b16 v[194:195], v234 offset:43008\n"
"v_subrev_f32 v32, s22, v32\n"
"v_mfma_f32_16x16x32_bf16 v[20:23], v[96:99], a[64:67], v[20:23]\n"
"ds_read_b64_tr_b16 v[184:185], v234 offset:32768\n"
"v_add_f32 v59, v59, v137\n"
"v_and_b32 v139, s73, v139\n"
"v_add_f32 v59, v59, v141\n"
"v_lshlrev_b32 v207, 16, v143\n"
"v_lshlrev_b32 v203, 16, v145\n"
"v_and_b32 v145, s73, v145\n"
"v_add_f32 v58, v58, v203\n"
"v_add_f32 v59, v59, v145\n"
"v_and_b32 v147, s73, v147\n"
"v_lshlrev_b32 v203, 16, v151\n"
"v_subrev_f32 v4, s22, v4\n"
"v_subrev_f32 v5, s22, v5\n"
"v_subrev_f32 v7, s22, v7\n"
"v_cvt_pk_bf16_f32 v0, v0, v1\n"
"v_cvt_pk_bf16_f32 v1, v2, v3\n"
"v_cvt_pk_bf16_f32 v3, v6, v7\n"
"v_mfma_f32_16x16x32_bf16 v[36:39], v[96:99], a[80:83], v[36:39]\n"
"v_add_f32 v56, v56, v204\n"
"v_cvt_pk_bf16_f32 v2, v4, v5\n"
"v_mfma_f32_16x16x32_bf16 v[36:39], v[100:103], a[84:87], v[36:39]\n"
"ds_read_b64_tr_b16 v[192:193], v234 offset:40960\n"
"v_add_f32 v61, v61, v138\n"
"v_mfma_f32_16x16x32_bf16 v[36:39], v[104:107], a[88:91], v[36:39]\n"
"v_add_f32 v57, v57, v140\n"
"v_subrev_f32 v23, s22, v23\n"
"v_mfma_f32_16x16x32_bf16 v[36:39], v[108:111], a[92:95], v[36:39]\n"
"ds_read_b64_tr_b16 v[180:181], v230 offset:32768\n"
"v_and_b32 v144, s73, v144\n"
"v_mfma_f32_16x16x32_bf16 v[36:39], v[112:115], a[32:35], v[36:39]\n"
"ds_read_b64_tr_b16 v[210:211], v230 offset:51200\n"
"v_add_f32 v62, v62, v207\n"
"v_add_f32 v62, v62, v205\n"
"v_lshlrev_b32 v207, 16, v149\n"
"v_add_f32 v58, v58, v207\n"
"v_lshlrev_b32 v202, 16, v150\n"
"v_permlane16_swap_b32 v0, v2\n"
"v_permlane16_swap_b32 v1, v3\n"
"v_subrev_f32 v20, s22, v20\n"
"v_cvt_pk_bf16_f32 v16, v16, v17\n"
"v_mfma_f32_16x16x32_bf16 v[36:39], v[116:119], a[36:39], v[36:39]\n"
"ds_read_b64_tr_b16 v[186:187], v234 offset:34816\n"
"v_add_f32 v63, v63, v135\n"
"v_mfma_f32_16x16x32_bf16 v[36:39], v[120:123], a[40:43], v[36:39]\n"
"ds_read_b64_tr_b16 v[188:189], v230 offset:40960\n"
"buffer_store_dwordx4 v[0:3], v238, s[36:39], 0 offen sc0 sc1\n"
"v_and_b32 v143, s73, v143\n"
"v_and_b32 v146, s73, v146\n"
"v_and_b32 v149, s73, v149\n"
"v_lshlrev_b32 v205, 16, v153\n"
"v_and_b32 v153, s73, v153\n"
"v_lshlrev_b32 v206, 16, v154\n"
"v_lshlrev_b32 v207, 16, v155\n"
"v_subrev_f32 v21, s22, v21\n"
"v_subrev_f32 v22, s22, v22\n"
"v_cvt_pk_bf16_f32 v17, v18, v19\n"
"v_cvt_pk_bf16_f32 v18, v20, v21\n"
"v_cvt_pk_bf16_f32 v32, v32, v33\n"
"v_mfma_f32_16x16x32_bf16 v[36:39], v[224:227], a[44:47], v[36:39]\n"
"ds_read_b64_tr_b16 v[182:183], v230 offset:34816\n"
"v_add_f32 v63, v63, v139\n"
"v_add_f32 v61, v61, v142\n"
"v_add_f32 v57, v57, v144\n"
"v_add_f32 v57, v57, v148\n"
"v_add_f32 v59, v59, v149\n"
"v_cvt_pk_bf16_f32 v19, v22, v23\n"
"v_permlane16_swap_b32 v16, v18\n"
"v_cvt_pk_bf16_f32 v33, v34, v35\n"
"v_mfma_f32_16x16x32_bf16 v[52:55], v[224:227], a[228:231], v[52:55]\n"
"ds_read_b64_tr_b16 v[190:191], v230 offset:43008\n"
"v_add_f32 v59, v59, v153\n"
"v_mfma_f32_16x16x32_bf16 v[52:55], v[120:123], a[224:227], v[52:55]\n"
"v_permlane16_swap_b32 v17, v19\n"
"v_mfma_f32_16x16x32_bf16 v[52:55], v[116:119], a[212:215], v[52:55]\n"
"ds_read_b64_tr_b16 v[208:209], v230 offset:49152\n"
"v_subrev_f32 v36, s22, v36\n"
"v_mfma_f32_16x16x32_bf16 v[52:55], v[112:115], a[208:211], v[52:55]\n"
"ds_read_b64_tr_b16 v[222:223], v234 offset:59392\n"
"buffer_store_dwordx4 v[16:19], v238, s[36:39], s52 offen sc0 sc1\n"
"v_add_f32 v63, v63, v143\n"
"v_add_f32 v60, v60, v202\n"
"v_add_f32 v62, v62, v203\n"
"v_and_b32 v152, s73, v152\n"
"v_subrev_f32 v38, s22, v38\n"
"v_subrev_f32 v39, s22, v39\n"
"v_mfma_f32_16x16x32_bf16 v[52:55], v[108:111], a[236:239], v[52:55]\n"
"v_and_b32 v155, s73, v155\n"
"v_cvt_pk_bf16_f32 v35, v38, v39\n"
"v_mfma_f32_16x16x32_bf16 v[52:55], v[104:107], a[232:235], v[52:55]\n"
"ds_read_b64_tr_b16 v[216:217], v230 offset:57344\n"
"v_subrev_f32 v37, s22, v37\n"
"v_mfma_f32_16x16x32_bf16 v[52:55], v[100:103], a[220:223], v[52:55]\n"
"v_and_b32 v150, s73, v150\n"
"v_and_b32 v154, s73, v154\n"
"v_mfma_f32_16x16x32_bf16 v[52:55], v[96:99], a[216:219], v[52:55]\n"
"s_waitcnt lgkmcnt(4)\n"
"ds_read_b64_tr_b16 v[212:213], v234 offset:49152\n"
"ds_read_b64_tr_b16 v[220:221], v234 offset:57344\n"
"v_add_f32 v63, v63, v147\n"
"v_and_b32 v151, s73, v151\n"
"v_add_f32 v63, v63, v151\n"
"v_add_f32 v57, v57, v152\n"
"v_add_f32 v58, v58, v205\n"
"v_add_f32 v60, v60, v206\n"
"v_add_f32 v62, v62, v207\n"
"v_cvt_pk_bf16_f32 v34, v36, v37\n"
"v_permlane16_swap_b32 v33, v35\n"
"v_mfma_f32_16x16x32_bf16 v[8:11], v[180:183], a[48:51], v[8:11]\n"
"v_cvt_pk_bf16_f32 v56, v56, v57\n"
"v_cvt_pk_bf16_f32 v57, v58, v59\n"
"v_mfma_f32_16x16x32_bf16 v[8:11], v[184:187], a[52:55], v[8:11]\n"
"s_waitcnt lgkmcnt(5)\n"
"v_permlane16_swap_b32 v32, v34\n"
"v_mfma_f32_16x16x32_bf16 v[8:11], v[188:191], a[56:59], v[8:11]\n"
"buffer_store_dwordx4 v[32:35], v238, s[36:39], s53 offen sc0 sc1\n"
"ds_read_b64_tr_b16 v[120:121], v231 offset:57344\n"
"v_add_f32 v61, v61, v146\n"
"v_add_f32 v61, v61, v150\n"
"v_add_f32 v61, v61, v154\n"
"v_add_f32 v63, v63, v155\n"
"v_cvt_pk_bf16_f32 v58, v60, v61\n"
"v_cvt_pk_bf16_f32 v59, v62, v63\n"
"v_mfma_f32_16x16x32_bf16 v[8:11], v[192:195], a[60:63], v[8:11]\n"
"s_waitcnt lgkmcnt(4)\n"
"v_permlane16_swap_b32 v57, v59\n"
"v_mfma_f32_16x16x32_bf16 v[8:11], v[208:211], a[0:3], v[8:11]\n"
"s_waitcnt lgkmcnt(2)\n"
"v_permlane16_swap_b32 v56, v58\n"
"v_mfma_f32_16x16x32_bf16 v[8:11], v[212:215], a[4:7], v[8:11]\n"
"buffer_store_dwordx4 v[56:59], v253, s[36:39], 0 offen sc0 sc1\n"
"v_subrev_f32 v48, s22, v48\n"
"v_subrev_f32 v50, s22, v50\n"
"v_subrev_f32 v51, s22, v51\n"
"v_mfma_f32_16x16x32_bf16 v[8:11], v[216:219], a[8:11], v[8:11]\n"
"s_waitcnt lgkmcnt(1)\n"
"ds_read_b64_tr_b16 v[112:113], v231 offset:49152\n"
"v_subrev_f32 v54, s22, v54\n"
"v_mfma_f32_16x16x32_bf16 v[8:11], v[220:223], a[12:15], v[8:11]\n"
"ds_read_b64_tr_b16 v[100:101], v235 offset:32768\n"
"v_subrev_f32 v52, s22, v52\n"
"v_mfma_f32_16x16x32_bf16 v[24:27], v[220:223], a[28:31], v[24:27]\n"
"ds_read_b64_tr_b16 v[102:103], v235 offset:34816\n"
"v_subrev_f32 v53, s22, v53\n"
"v_mfma_f32_16x16x32_bf16 v[24:27], v[216:219], a[24:27], v[24:27]\n"
"v_cvt_pk_bf16_f32 v48, v48, v49\n"
"v_cvt_pk_bf16_f32 v49, v50, v51\n"
"v_mfma_f32_16x16x32_bf16 v[24:27], v[212:215], a[20:23], v[24:27]\n"
"ds_read_b64_tr_b16 v[96:97], v231 offset:32768\n"
"v_cvt_pk_bf16_f32 v50, v52, v53\n"
"v_mfma_f32_16x16x32_bf16 v[24:27], v[208:211], a[16:19], v[24:27]\n"
"s_nop 0\n"
"v_permlane16_swap_b32 v48, v50\n"
"v_mfma_f32_16x16x32_bf16 v[24:27], v[192:195], a[76:79], v[24:27]\n"
"ds_read_b64_tr_b16 v[116:117], v235 offset:49152\n"
"v_subrev_f32 v55, s22, v55\n"
"v_mfma_f32_16x16x32_bf16 v[24:27], v[188:191], a[72:75], v[24:27]\n"
"ds_read_b64_tr_b16 v[98:99], v231 offset:34816\n"
"v_cvt_pk_bf16_f32 v51, v54, v55\n"
"v_mfma_f32_16x16x32_bf16 v[24:27], v[184:187], a[68:71], v[24:27]\n"
"s_nop 0\n"
"v_permlane16_swap_b32 v49, v51\n"
"v_mfma_f32_16x16x32_bf16 v[24:27], v[180:183], a[64:67], v[24:27]\n"
"buffer_store_dwordx4 v[48:51], v252, s[36:39], 0 offen sc0 sc1\n"
"ds_read_b64_tr_b16 v[104:105], v231 offset:40960\n"
"v_mfma_f32_16x16x32_bf16 v[40:43], v[180:183], a[80:83], v[40:43]\n"
"ds_read_b64_tr_b16 v[106:107], v231 offset:43008\n"
"ds_read_b64_tr_b16 v[108:109], v235 offset:40960\n"
"v_mfma_f32_16x16x32_bf16 v[40:43], v[184:187], a[84:87], v[40:43]\n"
"ds_read_b64_tr_b16 v[110:111], v235 offset:43008\n"
"v_mfma_f32_16x16x32_bf16 v[40:43], v[188:191], a[88:91], v[40:43]\n"
"ds_read_b64_tr_b16 v[114:115], v231 offset:51200\n"
"v_subrev_f32 v10, s22, v10\n"
"v_mfma_f32_16x16x32_bf16 v[40:43], v[192:195], a[92:95], v[40:43]\n"
"ds_read_b64_tr_b16 v[118:119], v235 offset:51200\n"
"v_mfma_f32_16x16x32_bf16 v[40:43], v[208:211], a[32:35], v[40:43]\n"
"ds_read_b64_tr_b16 v[122:123], v231 offset:59392\n"
"v_subrev_f32 v24, s22, v24\n"
"v_mfma_f32_16x16x32_bf16 v[40:43], v[212:215], a[36:39], v[40:43]\n"
"ds_read_b64_tr_b16 v[224:225], v235 offset:57344\n"
"ds_read_b64_tr_b16 v[226:227], v235 offset:59392\n"
"v_mfma_f32_16x16x32_bf16 v[40:43], v[216:219], a[40:43], v[40:43]\n"
"v_mfma_f32_16x16x32_bf16 v[40:43], v[220:223], a[44:47], v[40:43]\n"
"s_waitcnt lgkmcnt(8)\n"
"v_mov_b32 v160, 0\n"
"v_subrev_f32 v11, s22, v11\n"
"v_mfma_f32_16x16x32_bf16 v[12:15], v[96:99], a[48:51], v[12:15]\n"
"v_subrev_f32 v8, s22, v8\n"
"v_subrev_f32 v9, s22, v9\n"
"v_mfma_f32_16x16x32_bf16 v[12:15], v[100:103], a[52:55], v[12:15]\n"
"s_waitcnt lgkmcnt(5)\n"
"v_cvt_pk_bf16_f32 v8, v8, v9\n"
"v_mfma_f32_16x16x32_bf16 v[12:15], v[104:107], a[56:59], v[12:15]\n"
"v_subrev_f32 v27, s22, v27\n"
"v_mfma_f32_16x16x32_bf16 v[12:15], v[108:111], a[60:63], v[12:15]\n"
"s_waitcnt lgkmcnt(3)\n"
"v_mfma_f32_16x16x32_bf16 v[12:15], v[112:115], a[0:3], v[12:15]\n"
"v_mfma_f32_16x16x32_bf16 v[12:15], v[116:119], a[4:7], v[12:15]\n"
"s_waitcnt lgkmcnt(0)\n"
"v_mfma_f32_16x16x32_bf16 v[12:15], v[120:123], a[8:11], v[12:15]\n"
"v_subrev_f32 v40, s22, v40\n"
"v_mfma_f32_16x16x32_bf16 v[12:15], v[224:227], a[12:15], v[12:15]\n"
"s_waitcnt vmcnt(5)\n"
"v_mfma_f32_16x16x32_bf16 v[64:67], v[162:165], v[72:75], 0\n"
"v_mfma_f32_16x16x32_bf16 v[64:67], v[162:165], v[76:79], v[64:67]\n"
"v_cvt_pk_bf16_f32 v9, v10, v11\n"
"v_mfma_f32_16x16x32_bf16 v[64:67], v[162:165], v[80:83], v[64:67]\n"
"s_nop 2\n"
"v_subrev_f32 v12, s22, v12\n"
"v_mfma_f32_16x16x32_bf16 v[64:67], v[162:165], v[84:87], v[64:67]\n"
"v_subrev_f32 v25, s22, v25\n"
"v_mfma_f32_16x16x32_bf16 v[64:67], v[162:165], v[88:91], v[64:67]\n"
"v_subrev_f32 v13, s22, v13\n"
"v_mfma_f32_16x16x32_bf16 v[64:67], v[162:165], v[92:95], v[64:67]\n"
"v_subrev_f32 v15, s22, v15\n"
"v_mfma_f32_16x16x32_bf16 v[64:67], v[162:165], v[244:247], v[64:67]\n"
"v_subrev_f32 v14, s22, v14\n"
"v_mfma_f32_16x16x32_bf16 v[64:67], v[162:165], v[156:159], v[64:67]\n"
"v_subrev_f32 v42, s22, v42\n"
"v_mfma_f32_16x16x32_bf16 v[68:71], v[160:163], v[72:75], 0\n"
"v_cvt_pk_bf16_f32 v11, v14, v15\n"
"v_subrev_f32 v41, s22, v41\n"
"v_mfma_f32_16x16x32_bf16 v[68:71], v[160:163], v[76:79], v[68:71]\n"
"v_cvt_pk_bf16_f32 v10, v12, v13\n"
"v_cvt_pk_bf16_f32 v24, v24, v25\n"
"v_mfma_f32_16x16x32_bf16 v[68:71], v[160:163], v[80:83], v[68:71]\n"
"v_mfma_f32_16x16x32_bf16 v[68:71], v[160:163], v[84:87], v[68:71]\n"
"v_permlane16_swap_b32 v9, v11\n"
"v_mfma_f32_16x16x32_bf16 v[68:71], v[160:163], v[88:91], v[68:71]\n"
"v_mfma_f32_16x16x32_bf16 v[68:71], v[160:163], v[92:95], v[68:71]\n"
"v_cvt_pk_bf16_f32 v64, v64, v65\n"
"v_cvt_pk_bf16_f32 v40, v40, v41\n"
"v_mfma_f32_16x16x32_bf16 v[68:71], v[160:163], v[244:247], v[68:71]\n"
"v_permlane16_swap_b32 v8, v10\n"
"v_mfma_f32_16x16x32_bf16 v[68:71], v[160:163], v[156:159], v[68:71]\n"
"buffer_store_dwordx4 v[8:11], v239, s[36:39], 0 offen sc0 sc1\n"
"v_mfma_f32_16x16x32_bf16 v[28:31], v[224:227], a[28:31], v[28:31]\n"
"v_mfma_f32_16x16x32_bf16 v[28:31], v[120:123], a[24:27], v[28:31]\n"
"v_cvt_pk_bf16_f32 v65, v66, v67\n"
"v_subrev_f32 v43, s22, v43\n"
"v_mfma_f32_16x16x32_bf16 v[28:31], v[116:119], a[20:23], v[28:31]\n"
"v_mfma_f32_16x16x32_bf16 v[28:31], v[112:115], a[16:19], v[28:31]\n"
"v_mfma_f32_16x16x32_bf16 v[28:31], v[108:111], a[76:79], v[28:31]\n"
"v_cvt_pk_bf16_f32 v66, v68, v69\n"
"v_cvt_pk_bf16_f32 v67, v70, v71\n"
"v_mfma_f32_16x16x32_bf16 v[28:31], v[104:107], a[72:75], v[28:31]\n"
"v_permlane16_swap_b32 v64, v66\n"
"v_mfma_f32_16x16x32_bf16 v[28:31], v[100:103], a[68:71], v[28:31]\n"
"v_permlane16_swap_b32 v65, v67\n"
"v_mfma_f32_16x16x32_bf16 v[28:31], v[96:99], a[64:67], v[28:31]\n"
"buffer_store_dwordx4 v[64:67], v253, s[36:39], 0 offen offset:128 sc0 sc1\n"
"v_cvt_pk_bf16_f32 v41, v42, v43\n"
"v_mfma_f32_16x16x32_bf16 v[44:47], v[96:99], a[80:83], v[44:47]\n"
"v_subrev_f32 v26, s22, v26\n"
"v_cvt_pk_bf16_f32 v25, v26, v27\n"
"v_mfma_f32_16x16x32_bf16 v[44:47], v[100:103], a[84:87], v[44:47]\n"
"v_mfma_f32_16x16x32_bf16 v[44:47], v[104:107], a[88:91], v[44:47]\n"
"s_nop 0\n"
"v_subrev_f32 v30, s22, v30\n"
"v_subrev_f32 v31, s22, v31\n"
"v_mfma_f32_16x16x32_bf16 v[44:47], v[108:111], a[92:95], v[44:47]\n"
"v_subrev_f32 v28, s22, v28\n"
"v_subrev_f32 v29, s22, v29\n"
"v_mfma_f32_16x16x32_bf16 v[44:47], v[112:115], a[32:35], v[44:47]\n"
"v_cvt_pk_bf16_f32 v26, v28, v29\n"
"v_cvt_pk_bf16_f32 v27, v30, v31\n"
"v_mfma_f32_16x16x32_bf16 v[44:47], v[116:119], a[36:39], v[44:47]\n"
"s_nop 0\n"
"v_permlane16_swap_b32 v25, v27\n"
"v_mfma_f32_16x16x32_bf16 v[44:47], v[120:123], a[40:43], v[44:47]\n"
"v_permlane16_swap_b32 v24, v26\n"
"v_mfma_f32_16x16x32_bf16 v[44:47], v[224:227], a[44:47], v[44:47]\n"
"buffer_store_dwordx4 v[24:27], v239, s[36:39], s52 offen sc0 sc1\n"
"s_nop 6\n"
"v_subrev_f32 v44, s22, v44\n"
"v_subrev_f32 v45, s22, v45\n"
"v_subrev_f32 v46, s22, v46\n"
"v_subrev_f32 v47, s22, v47\n"
"v_cvt_pk_bf16_f32 v42, v44, v45\n"
"v_cvt_pk_bf16_f32 v43, v46, v47\n"
"s_nop 0\n"
"v_permlane16_swap_b32 v40, v42\n"
"v_permlane16_swap_b32 v41, v43\n"
"buffer_store_dwordx4 v[40:43], v239, s[36:39], s53 offen sc0 sc1\n"
"s_branch LFIN_%=\n"
"LSW_%=:\n"
"s_add_u32 s16, s8, s95\n"
"s_addc_u32 s17, s9, 0\n"
"s_sub_u32 s18, 655360, s95\n"
"s_add_u32 s20, s8, s100\n"
"s_addc_u32 s21, s9, 0\n"
"s_sub_u32 s22, 655360, s100\n"
"s_mov_b32 s56, 0\n"
"s_mov_b32 s55, 0\n"
"s_mov_b32 s58, 0\n"
"s_mov_b32 s57, 0\n"
"s_mov_b32 s59, 163840\n"
"s_mov_b32 s52, 9216\n"
"s_mov_b32 s53, 18432\n"
"s_mov_b32 s54, 27648\n"
"v_lshrrev_b32 v206, 7, v242\n"
"v_mul_u32_u24 v206, 1280, v206\n"
"v_lshrrev_b32 v240, 3, v242\n"
"v_and_b32 v240, 0x60, v240\n"
"v_and_b32 v207, 0x70, v242\n"
"v_xor_b32 v240, v240, v207\n"
"v_add_u32 v240, v206, v240\n"
"v_mov_b32 v207, v240\n"
"buffer_load_dword v252, v240, s[16:19], 0 offen\n"
"s_branch LSWK_%=\n"
"LGP_%=:\n"
"s_mov_b32 s80, 0x40000\n"
"LGPa_%=:\n"
"s_sleep 1\n"
"global_load_dword v200, v201, s[6:7] sc1\n"
"s_sub_u32 s80, s80, 1\n"
"s_cmp_eq_u32 s80, 0\n"
"s_waitcnt vmcnt(0)\n"
"s_cbranch_scc1 LGPK_%=\n"
"v_subrev_u32 v200, s21, v200\n"
"v_cmp_lt_u32 vcc, s69, v200\n"
"s_nop 4\n"
"s_cbranch_vccnz LGPa_%=\n"
"s_branch LGPK_%=\n"
"LGQ_%=:\n"
"s_mov_b32 s80, 0x40000\n"
"LGQa_%=:\n"
"s_sleep 1\n"
"global_load_dword v200, v201, s[6:7] sc1\n"
"s_sub_u32 s80, s80, 1\n"
"s_cmp_eq_u32 s80, 0\n"
"s_waitcnt vmcnt(0)\n"
"s_cbranch_scc1 LGQK_%=\n"
"v_subrev_u32 v200, s21, v200\n"
"v_cmp_lt_u32 vcc, s69, v200\n"
"s_nop 4\n"
"s_cbranch_vccnz LGQa_%=\n"
"s_branch LGQK_%=\n"
"LGR_%=:\n"
"s_mov_b32 s80, 0x40000\n"
"LGRa_%=:\n"
"s_sleep 1\n"
"global_load_dword v200, v201, s[6:7] sc1\n"
"s_sub_u32 s80, s80, 1\n"
"s_cmp_eq_u32 s80, 0\n"
"s_waitcnt vmcnt(0)\n"
"s_cbranch_scc1 LGRK_%=\n"
"v_subrev_u32 v200, s21, v200\n"
"v_cmp_lt_u32 vcc, s69, v200\n"
"s_nop 4\n"
"s_cbranch_vccnz LGRa_%=\n"
"s_branch LGRK_%=\n"
"LGV_%=:\n"
"s_mov_b32 s80, 0x40000\n"
"LGVa_%=:\n"
"s_sleep 1\n"
"global_load_dword v200, v201, s[6:7] sc1\n"
"s_sub_u32 s80, s80, 1\n"
"s_cmp_eq_u32 s80, 0\n"
"s_waitcnt vmcnt(0)\n"
"s_cbranch_scc1 LGVK_%=\n"
"v_subrev_u32 v200, s21, v200\n"
"v_cmp_lt_u32 vcc, s69, v200\n"
"s_nop 4\n"
"s_cbranch_vccnz LGVa_%=\n"
"s_branch LGVK_%=\n"
"LEP_%=:\n"
"s_add_u32 s92, s92, 1\n"
"v_mov_b32 v200, s92\n"
"v_mov_b32 v255, 0\n"
"s_mov_b64 exec, 1\n"
"global_store_dword v255, v200, s[6:7] offset:128 sc1\n"
"s_mov_b64 exec, -1\n"
"s_branch LEPK_%=\n"
"LPCE0_%=:\n"
"s_memrealtime s[80:81]\n"
"s_sub_u32 s82, s67, s66\n"
"s_cmp_gt_i32 s82, 0\n"
"s_cselect_b32 s83, s67, s66\n"
"s_waitcnt lgkmcnt(0)\n"
"s_mov_b32 s63, 0\n"
"s_mov_b32 s62, 118\n"
"s_sub_u32 s82, s83, s80\n"
"s_cmp_gt_i32 s82, 3\n"
"s_cbranch_scc0 LPEE0_%=\n"
"LPDE0_%=:\n"
"s_sleep 1\n"
"s_memrealtime s[80:81]\n"
"s_waitcnt lgkmcnt(0)\n"
"s_sub_u32 s82, s83, s80\n"
"s_cmp_gt_i32 s82, 3\n"
"s_cbranch_scc1 LPDE0_%=\n"
"LPEE0_%=:\n"
"s_add_u32 s66, s66, s72\n"
"s_add_u32 s66, s66, 4\n"
"s_add_u32 s67, s80, s72\n"
"s_sub_u32 s72, s72, 3\n"
"s_max_u32 s72, s72, s62\n"
"s_branch LPKE0_%=\n"
"LPCE1_%=:\n"
"s_memrealtime s[80:81]\n"
"s_sub_u32 s82, s67, s66\n"
"s_cmp_gt_i32 s82, 0\n"
"s_cselect_b32 s83, s67, s66\n"
"s_waitcnt lgkmcnt(0)\n"
"s_add_u32 s72, s62, 16\n"
"s_sub_u32 s82, s83, s80\n"
"s_cmp_gt_i32 s82, 3\n"
"s_cbranch_scc0 LPEE1_%=\n"
"LPDE1_%=:\n"
"s_sleep 1\n"
"s_memrealtime s[80:81]\n"
"s_waitcnt lgkmcnt(0)\n"
"s_sub_u32 s82, s83, s80\n"
"s_cmp_gt_i32 s82, 3\n"
"s_cbranch_scc1 LPDE1_%=\n"
"LPEE1_%=:\n"
"s_add_u32 s66, s66, s72\n"
"s_add_u32 s66, s66, 4\n"
"s_add_u32 s67, s80, s72\n"
"s_sub_u32 s72, s72, 3\n"
"s_max_u32 s72, s72, s62\n"
"s_branch LPKE1_%=\n"
"LPCT0_%=:\n"
"s_memrealtime s[80:81]\n"
"s_sub_u32 s82, s67, s66\n"
"s_cmp_gt_i32 s82, 0\n"
"s_cselect_b32 s83, s67, s66\n"
"s_waitcnt lgkmcnt(0)\n"
"s_sub_u32 s82, s83, s80\n"
"s_cmp_gt_i32 s82, 3\n"
"s_cbranch_scc0 LPET0_%=\n"
"LPDT0_%=:\n"
"s_sleep 1\n"
"s_memrealtime s[80:81]\n"
"s_waitcnt lgkmcnt(0)\n"
"s_sub_u32 s82, s83, s80\n"
"s_cmp_gt_i32 s82, 3\n"
"s_cbranch_scc1 LPDT0_%=\n"
"LPET0_%=:\n"
"s_add_u32 s66, s66, s72\n"
"s_add_u32 s66, s66, 4\n"
"s_add_u32 s67, s80, s72\n"
"s_sub_u32 s72, s72, 3\n"
"s_max_u32 s72, s72, s62\n"
"s_branch LPKT0_%=\n"
"LPCT1_%=:\n"
"s_memrealtime s[80:81]\n"
"s_sub_u32 s82, s67, s66\n"
"s_cmp_gt_i32 s82, 0\n"
"s_cselect_b32 s83, s67, s66\n"
"s_waitcnt lgkmcnt(0)\n"
"s_add_u32 s72, s62, 16\n"
"s_sub_u32 s82, s83, s80\n"
"s_cmp_gt_i32 s82, 3\n"
"s_cbranch_scc0 LPET1_%=\n"
"LPDT1_%=:\n"
"s_sleep 1\n"
"s_memrealtime s[80:81]\n"
"s_waitcnt lgkmcnt(0)\n"
"s_sub_u32 s82, s83, s80\n"
"s_cmp_gt_i32 s82, 3\n"
"s_cbranch_scc1 LPDT1_%=\n"
"LPET1_%=:\n"
"s_add_u32 s66, s66, s72\n"
"s_add_u32 s66, s66, 4\n"
"s_add_u32 s67, s80, s72\n"
"s_sub_u32 s72, s72, 3\n"
"s_max_u32 s72, s72, s62\n"
"s_branch LPKT1_%=\n"
"LPCL0_%=:\n"
"s_memrealtime s[80:81]\n"
"s_sub_u32 s82, s67, s66\n"
"s_cmp_gt_i32 s82, 0\n"
"s_cselect_b32 s83, s67, s66\n"
"s_waitcnt lgkmcnt(0)\n"
"s_sub_u32 s82, s80, s67\n"
"s_add_u32 s63, s63, 1\n"
"s_min_i32 s81, s63, 0\n"
"s_cmp_gt_i32 s82, 20\n"
"s_cselect_b32 s63, s63, s81\n"
"s_cmp_ge_i32 s63, 2\n"
"s_cbranch_scc0 LPRL0_%=\n"
"s_mov_b32 s63, -9\n"
"s_add_u32 s62, s62, 18\n"
"s_min_u32 s62, s62, 146\n"
"s_max_u32 s72, s72, s62\n"
"s_add_u32 s83, s80, 200\n"
"s_and_b32 s83, s83, -1\n"
"LPRL0_%=:\n"
"s_sub_u32 s82, s83, s80\n"
"s_cmp_gt_i32 s82, 3\n"
"s_cbranch_scc0 LPEL0_%=\n"
"LPDL0_%=:\n"
"s_sleep 1\n"
"s_memrealtime s[80:81]\n"
"s_waitcnt lgkmcnt(0)\n"
"s_sub_u32 s82, s83, s80\n"
"s_cmp_gt_i32 s82, 3\n"
"s_cbranch_scc1 LPDL0_%=\n"
"LPEL0_%=:\n"
"s_add_u32 s66, s66, s72\n"
"s_add_u32 s66, s66, 4\n"
"s_add_u32 s67, s80, s72\n"
"s_sub_u32 s72, s72, 3\n"
"s_max_u32 s72, s72, s62\n"
"s_branch LPKL0_%=\n"
"LPCL1_%=:\n"
"s_memrealtime s[80:81]\n"
"s_sub_u32 s82, s67, s66\n"
"s_cmp_gt_i32 s82, 0\n"
"s_cselect_b32 s83, s67, s66\n"
"s_waitcnt lgkmcnt(0)\n"
"s_sub_u32 s82, s80, s67\n"
"s_add_u32 s63, s63, 1\n"
"s_min_i32 s81, s63, 0\n"
"s_cmp_gt_i32 s82, 20\n"
"s_cselect_b32 s63, s63, s81\n"
"s_cmp_ge_i32 s63, 2\n"
"s_cbranch_scc0 LPRL1_%=\n"
"s_mov_b32 s63, -9\n"
"s_add_u32 s62, s62, 18\n"
"s_min_u32 s62, s62, 146\n"
"s_max_u32 s72, s72, s62\n"
"s_add_u32 s83, s80, 200\n"
"s_and_b32 s83, s83, -1\n"
"LPRL1_%=:\n"
"s_sub_u32 s82, s83, s80\n"
"s_cmp_gt_i32 s82, 3\n"
"s_cbranch_scc0 LPEL1_%=\n"
"LPDL1_%=:\n"
"s_sleep 1\n"
"s_memrealtime s[80:81]\n"
"s_waitcnt lgkmcnt(0)\n"
"s_sub_u32 s82, s83, s80\n"
"s_cmp_gt_i32 s82, 3\n"
"s_cbranch_scc1 LPDL1_%=\n"
"LPEL1_%=:\n"
"s_add_u32 s66, s66, s72\n"
"s_add_u32 s66, s66, 4\n"
"s_add_u32 s67, s80, s72\n"
"s_sub_u32 s72, s72, 3\n"
"s_max_u32 s72, s72, s62\n"
"s_branch LPKL1_%=\n"
"LPCL2_%=:\n"
"s_memrealtime s[80:81]\n"
"s_sub_u32 s82, s67, s66\n"
"s_cmp_gt_i32 s82, 0\n"
"s_cselect_b32 s83, s67, s66\n"
"s_waitcnt lgkmcnt(0)\n"
"s_sub_u32 s82, s80, s67\n"
"s_add_u32 s63, s63, 1\n"
"s_min_i32 s81, s63, 0\n"
"s_cmp_gt_i32 s82, 20\n"
"s_cselect_b32 s63, s63, s81\n"
"s_cmp_ge_i32 s63, 2\n"
"s_cbranch_scc0 LPRL2_%=\n"
"s_mov_b32 s63, -9\n"
"s_add_u32 s62, s62, 18\n"
"s_min_u32 s62, s62, 146\n"
"s_max_u32 s72, s72, s62\n"
"s_add_u32 s83, s80, 200\n"
"s_and_b32 s83, s83, -1\n"
"LPRL2_%=:\n"
"s_sub_u32 s82, s83, s80\n"
"s_cmp_gt_i32 s82, 3\n"
"s_cbranch_scc0 LPEL2_%=\n"
"LPDL2_%=:\n"
"s_sleep 1\n"
"s_memrealtime s[80:81]\n"
"s_waitcnt lgkmcnt(0)\n"
"s_sub_u32 s82, s83, s80\n"
"s_cmp_gt_i32 s82, 3\n"
"s_cbranch_scc1 LPDL2_%=\n"
"LPEL2_%=:\n"
"s_add_u32 s66, s66, s72\n"
"s_add_u32 s66, s66, 4\n"
"s_add_u32 s67, s80, s72\n"
"s_sub_u32 s72, s72, 3\n"
"s_max_u32 s72, s72, s62\n"
"s_branch LPKL2_%=\n"
"LPCL3_%=:\n"
"s_memrealtime s[80:81]\n"
"s_sub_u32 s82, s67, s66\n"
"s_cmp_gt_i32 s82, 0\n"
"s_cselect_b32 s83, s67, s66\n"
"s_waitcnt lgkmcnt(0)\n"
"s_sub_u32 s82, s80, s67\n"
"s_add_u32 s63, s63, 1\n"
"s_min_i32 s81, s63, 0\n"
"s_cmp_gt_i32 s82, 20\n"
"s_cselect_b32 s63, s63, s81\n"
"s_cmp_ge_i32 s63, 2\n"
"s_cbranch_scc0 LPRL3_%=\n"
"s_mov_b32 s63, -9\n"
"s_add_u32 s62, s62, 18\n"
"s_min_u32 s62, s62, 146\n"
"s_max_u32 s72, s72, s62\n"
"s_add_u32 s83, s80, 200\n"
"s_and_b32 s83, s83, -1\n"
"LPRL3_%=:\n"
"s_sub_u32 s82, s83, s80\n"
"s_cmp_gt_i32 s82, 3\n"
"s_cbranch_scc0 LPEL3_%=\n"
"LPDL3_%=:\n"
"s_sleep 1\n"
"s_memrealtime s[80:81]\n"
"s_waitcnt lgkmcnt(0)\n"
"s_sub_u32 s82, s83, s80\n"
"s_cmp_gt_i32 s82, 3\n"
"s_cbranch_scc1 LPDL3_%=\n"
"LPEL3_%=:\n"
"s_add_u32 s66, s66, s72\n"
"s_add_u32 s66, s66, 4\n"
"s_add_u32 s67, s80, s72\n"
"s_sub_u32 s72, s72, 3\n"
"s_max_u32 s72, s72, s62\n"
"s_branch LPKL3_%=\n"
"LPN_%=:\n"
"v_mfma_f32_16x16x32_bf16 v[112:115], a[192:195], a[136:139], v[112:115]\n"
"v_mfma_f32_16x16x32_bf16 v[112:115], a[196:199], a[140:143], v[112:115]\n"
"v_mfma_f32_16x16x32_bf16 v[116:119], a[200:203], a[136:139], v[116:119]\n"
"s_waitcnt vmcnt(11)\n"
"v_mfma_f32_16x16x32_bf16 v[116:119], a[204:207], a[140:143], v[116:119]\n"
"s_barrier\n"
"v_mfma_f32_16x16x32_bf16 v[120:123], a[208:211], a[136:139], v[120:123]\n"
"s_add_u32 s40, s48, 256\n"
"s_xor_b32 m0, s50, 0xc000\n"
"ds_read_b64_tr_b16 a[224:225], v244 offset:16384\n"
"v_mfma_f32_16x16x32_bf16 v[120:123], a[212:215], a[140:143], v[120:123]\n"
"buffer_load_dwordx4 v224, s[28:31], s40 offen nt lds\n"
"v_mfma_f32_16x16x32_bf16 v[124:127], a[216:219], a[136:139], v[124:127]\n"
"ds_read_b64_tr_b16 a[226:227], v244 offset:16896\n"
"v_mfma_f32_16x16x32_bf16 v[124:127], a[220:223], a[140:143], v[124:127]\n"
"ds_read_b64_tr_b16 a[228:229], v244 offset:24576\n"
"v_mfma_f32_16x16x32_bf16 v[140:143], a[216:219], a[144:147], v[140:143]\n"
"ds_read_b64_tr_b16 a[230:231], v244 offset:25088\n"
"v_mfma_f32_16x16x32_bf16 v[140:143], a[220:223], a[148:151], v[140:143]\n"
"ds_read_b64_tr_b16 a[232:233], v245 offset:16384\n"
"v_mfma_f32_16x16x32_bf16 v[136:139], a[208:211], a[144:147], v[136:139]\n"
"ds_read_b64_tr_b16 a[234:235], v245 offset:16896\n"
"v_mfma_f32_16x16x32_bf16 v[136:139], a[212:215], a[148:151], v[136:139]\n"
"buffer_load_dwordx4 v225, s[28:31], s40 offen offset:1024 nt lds\n"
"v_mfma_f32_16x16x32_bf16 v[132:135], a[200:203], a[144:147], v[132:135]\n"
"ds_read_b64_tr_b16 a[236:237], v245 offset:24576\n"
"v_mfma_f32_16x16x32_bf16 v[132:135], a[204:207], a[148:151], v[132:135]\n"
"ds_read_b64_tr_b16 a[238:239], v245 offset:25088\n"
"v_mfma_f32_16x16x32_bf16 v[128:131], a[192:195], a[144:147], v[128:131]\n"
"ds_read_b64_tr_b16 a[240:241], v244 offset:17408\n"
"v_mfma_f32_16x16x32_bf16 v[128:131], a[196:199], a[148:151], v[128:131]\n"
"ds_read_b64_tr_b16 a[242:243], v244 offset:17920\n"
"v_mfma_f32_16x16x32_bf16 v[144:147], a[192:195], a[152:155], v[144:147]\n"
"ds_read_b64_tr_b16 a[244:245], v244 offset:25600\n"
"v_mfma_f32_16x16x32_bf16 v[144:147], a[196:199], a[156:159], v[144:147]\n"
"buffer_load_dwordx4 v226, s[28:31], s40 offen offset:2048 nt lds\n"
"v_mfma_f32_16x16x32_bf16 v[148:151], a[200:203], a[152:155], v[148:151]\n"
"ds_read_b64_tr_b16 a[246:247], v244 offset:26112\n"
"v_mfma_f32_16x16x32_bf16 v[148:151], a[204:207], a[156:159], v[148:151]\n"
"ds_read_b64_tr_b16 a[248:249], v245 offset:17408\n"
"v_mfma_f32_16x16x32_bf16 v[152:155], a[208:211], a[152:155], v[152:155]\n"
"ds_read_b64_tr_b16 a[250:251], v245 offset:17920\n"
"v_mfma_f32_16x16x32_bf16 v[152:155], a[212:215], a[156:159], v[152:155]\n"
"ds_read_b64_tr_b16 a[252:253], v245 offset:25600\n"
"v_mfma_f32_16x16x32_bf16 v[156:159], a[216:219], a[152:155], v[156:159]\n"
"ds_read_b64_tr_b16 a[254:255], v245 offset:26112\n"
"v_mfma_f32_16x16x32_bf16 v[156:159], a[220:223], a[156:159], v[156:159]\n"
"buffer_load_dwordx4 v227, s[28:31], s40 offen offset:3072 nt lds\n"
"v_mfma_f32_16x16x32_bf16 v[172:175], a[216:219], a[160:163], v[172:175]\n"
"s_cmp_eq_u32 s47, 8\n"
"v_mfma_f32_16x16x32_bf16 v[172:175], a[220:223], a[164:167], v[172:175]\n"
"s_cselect_b32 s41, s52, s58\n"
"v_mfma_f32_16x16x32_bf16 v[168:171], a[208:211], a[160:163], v[168:171]\n"
"s_add_u32 s48, s48, s41\n"
"v_mfma_f32_16x16x32_bf16 v[168:171], a[212:215], a[164:167], v[168:171]\n"
"s_sub_u32 s41, s48, 5242880\n"
"v_mfma_f32_16x16x32_bf16 v[164:167], a[200:203], a[160:163], v[164:167]\n"
"s_min_u32 s48, s48, s41\n"
"v_mfma_f32_16x16x32_bf16 v[164:167], a[204:207], a[164:167], v[164:167]\n"
"v_xor_b32 v244, s74, v244\n"
"v_xor_b32 v246, s74, v246\n"
"v_mfma_f32_16x16x32_bf16 v[160:163], a[192:195], a[160:163], v[160:163]\n"
"s_mov_b32 m0, s50\n"
"v_xor_b32 v245, s74, v245\n"
"v_xor_b32 v247, s74, v247\n"
"v_mfma_f32_16x16x32_bf16 v[160:163], a[196:199], a[164:167], v[160:163]\n"
"buffer_load_dwordx4 v224, s[28:31], s48 offen nt lds\n"
"v_mfma_f32_16x16x32_bf16 v[176:179], a[192:195], a[168:171], v[176:179]\n"
"v_xor_b32 v252, s74, v252\n"
"v_xor_b32 v253, s74, v253\n"
"v_mfma_f32_16x16x32_bf16 v[176:179], a[196:199], a[172:175], v[176:179]\n"
"v_xor_b32 v250, s74, v250\n"
"v_xor_b32 v251, s74, v251\n"
"v_mfma_f32_16x16x32_bf16 v[180:183], a[200:203], a[168:171], v[180:183]\n"
"v_mfma_f32_16x16x32_bf16 v[180:183], a[204:207], a[172:175], v[180:183]\n"
"buffer_load_dwordx4 v225, s[28:31], s48 offen offset:1024 nt lds\n"
"v_mfma_f32_16x16x32_bf16 v[184:187], a[208:211], a[168:171], v[184:187]\n"
"v_mfma_f32_16x16x32_bf16 v[184:187], a[212:215], a[172:175], v[184:187]\n"
"v_mfma_f32_16x16x32_bf16 v[188:191], a[216:219], a[168:171], v[188:191]\n"
"v_mfma_f32_16x16x32_bf16 v[188:191], a[220:223], a[172:175], v[188:191]\n"
"v_mfma_f32_16x16x32_bf16 v[204:207], a[216:219], a[176:179], v[204:207]\n"
"v_mfma_f32_16x16x32_bf16 v[204:207], a[220:223], a[180:183], v[204:207]\n"
"buffer_load_dwordx4 v226, s[28:31], s48 offen offset:2048 nt lds\n"
"v_mfma_f32_16x16x32_bf16 v[200:203], a[208:211], a[176:179], v[200:203]\n"
"v_mfma_f32_16x16x32_bf16 v[200:203], a[212:215], a[180:183], v[200:203]\n"
"v_mfma_f32_16x16x32_bf16 v[196:199], a[200:203], a[176:179], v[196:199]\n"
"v_mfma_f32_16x16x32_bf16 v[196:199], a[204:207], a[180:183], v[196:199]\n"
"v_mfma_f32_16x16x32_bf16 v[192:195], a[192:195], a[176:179], v[192:195]\n"
"v_mfma_f32_16x16x32_bf16 v[192:195], a[196:199], a[180:183], v[192:195]\n"
"s_waitcnt lgkmcnt(0)\n"
"buffer_load_dwordx4 v227, s[28:31], s48 offen offset:3072 nt lds\n"
"v_mfma_f32_16x16x32_bf16 v[208:211], a[192:195], a[184:187], v[208:211]\n"
"s_xor_b32 s50, s50, s74\n"
"v_mfma_f32_16x16x32_bf16 v[208:211], a[196:199], a[188:191], v[208:211]\n"
"v_mfma_f32_16x16x32_bf16 v[212:215], a[200:203], a[184:187], v[212:215]\n"
"v_mfma_f32_16x16x32_bf16 v[212:215], a[204:207], a[188:191], v[212:215]\n"
"v_mfma_f32_16x16x32_bf16 v[216:219], a[208:211], a[184:187], v[216:219]\n"
"v_mfma_f32_16x16x32_bf16 v[216:219], a[212:215], a[188:191], v[216:219]\n"
"v_mfma_f32_16x16x32_bf16 v[220:223], a[216:219], a[184:187], v[220:223]\n"
"v_mfma_f32_16x16x32_bf16 v[220:223], a[220:223], a[188:191], v[220:223]\n"
"v_mfma_f32_16x16x32_bf16 v[0:3], a[224:227], a[136:139], v[0:3]\n"
"v_mfma_f32_16x16x32_bf16 v[0:3], a[228:231], a[140:143], v[0:3]\n"
"v_mfma_f32_16x16x32_bf16 v[4:7], a[232:235], a[136:139], v[4:7]\n"
"s_waitcnt vmcnt(8)\n"
"v_mfma_f32_16x16x32_bf16 v[4:7], a[236:239], a[140:143], v[4:7]\n"
"s_barrier\n"
"v_mfma_f32_16x16x32_bf16 v[8:11], a[240:243], a[136:139], v[8:11]\n"
"s_mov_b32 m0, s51\n"
"ds_read_b64_tr_b16 a[192:193], v244 offset:0\n"
"v_mfma_f32_16x16x32_bf16 v[8:11], a[244:247], a[140:143], v[8:11]\n"
"buffer_load_dword v242, s[80:83], 0 offen lds\n"
"s_add_u32 s80, s80, 680\n"
"s_addc_u32 s81, s81, 0\n"
"buffer_load_dwordx4 v248, s[24:27], s49 offen nt lds\n"
"v_mfma_f32_16x16x32_bf16 v[12:15], a[248:251], a[136:139], v[12:15]\n"
"ds_read_b64_tr_b16 a[194:195], v244 offset:512\n"
"v_mfma_f32_16x16x32_bf16 v[12:15], a[252:255], a[140:143], v[12:15]\n"
"ds_read_b128 a[136:139], v246 offset:0\n"
"v_mfma_f32_16x16x32_bf16 v[28:31], a[248:251], a[144:147], v[28:31]\n"
"ds_read_b64_tr_b16 a[196:197], v244 offset:8192\n"
"v_mfma_f32_16x16x32_bf16 v[28:31], a[252:255], a[148:151], v[28:31]\n"
"ds_read_b64_tr_b16 a[198:199], v244 offset:8704\n"
"v_mfma_f32_16x16x32_bf16 v[24:27], a[240:243], a[144:147], v[24:27]\n"
"ds_read_b128 a[140:143], v247 offset:0\n"
"v_mfma_f32_16x16x32_bf16 v[24:27], a[244:247], a[148:151], v[24:27]\n"
"buffer_load_dwordx4 v249, s[24:27], s49 offen offset:1024 nt lds\n"
"v_mfma_f32_16x16x32_bf16 v[20:23], a[232:235], a[144:147], v[20:23]\n"
"ds_read_b64_tr_b16 a[200:201], v245 offset:0\n"
"v_mfma_f32_16x16x32_bf16 v[20:23], a[236:239], a[148:151], v[20:23]\n"
"ds_read_b64_tr_b16 a[202:203], v245 offset:512\n"
"v_mfma_f32_16x16x32_bf16 v[16:19], a[224:227], a[144:147], v[16:19]\n"
"ds_read_b64_tr_b16 a[204:205], v245 offset:8192\n"
"v_mfma_f32_16x16x32_bf16 v[16:19], a[228:231], a[148:151], v[16:19]\n"
"ds_read_b64_tr_b16 a[206:207], v245 offset:8704\n"
"v_mfma_f32_16x16x32_bf16 v[32:35], a[224:227], a[152:155], v[32:35]\n"
"ds_read_b64_tr_b16 a[208:209], v244 offset:1024\n"
"v_mfma_f32_16x16x32_bf16 v[32:35], a[228:231], a[156:159], v[32:35]\n"
"buffer_load_dwordx4 v254, s[24:27], s49 offen offset:2048 nt lds\n"
"v_mfma_f32_16x16x32_bf16 v[36:39], a[232:235], a[152:155], v[36:39]\n"
"ds_read_b64_tr_b16 a[210:211], v244 offset:1536\n"
"v_mfma_f32_16x16x32_bf16 v[36:39], a[236:239], a[156:159], v[36:39]\n"
"ds_read_b64_tr_b16 a[212:213], v244 offset:9216\n"
"v_mfma_f32_16x16x32_bf16 v[40:43], a[240:243], a[152:155], v[40:43]\n"
"ds_read_b64_tr_b16 a[214:215], v244 offset:9728\n"
"v_mfma_f32_16x16x32_bf16 v[40:43], a[244:247], a[156:159], v[40:43]\n"
"ds_read_b64_tr_b16 a[216:217], v245 offset:1024\n"
"v_mfma_f32_16x16x32_bf16 v[44:47], a[248:251], a[152:155], v[44:47]\n"
"ds_read_b64_tr_b16 a[218:219], v245 offset:1536\n"
"v_mfma_f32_16x16x32_bf16 v[44:47], a[252:255], a[156:159], v[44:47]\n"
"buffer_load_dwordx4 v255, s[24:27], s49 offen offset:3072 nt lds\n"
"v_mfma_f32_16x16x32_bf16 v[60:63], a[248:251], a[160:163], v[60:63]\n"
"ds_read_b64_tr_b16 a[220:221], v245 offset:9216\n"
"v_mfma_f32_16x16x32_bf16 v[60:63], a[252:255], a[164:167], v[60:63]\n"
"ds_read_b64_tr_b16 a[222:223], v245 offset:9728\n"
"v_mfma_f32_16x16x32_bf16 v[56:59], a[240:243], a[160:163], v[56:59]\n"
"ds_read_b128 a[144:147], v246 offset:2048\n"
"v_mfma_f32_16x16x32_bf16 v[56:59], a[244:247], a[164:167], v[56:59]\n"
"s_add_u32 s40, s49, 40960\n"
"ds_read_b128 a[148:151], v247 offset:2048\n"
"v_mfma_f32_16x16x32_bf16 v[52:55], a[232:235], a[160:163], v[52:55]\n"
"s_add_u32 m0, s51, 4096\n"
"ds_read_b128 a[152:155], v246 offset:4096\n"
"v_mfma_f32_16x16x32_bf16 v[52:55], a[236:239], a[164:167], v[52:55]\n"
"buffer_load_dwordx4 v248, s[24:27], s40 offen nt lds\n"
"v_mfma_f32_16x16x32_bf16 v[48:51], a[224:227], a[160:163], v[48:51]\n"
"ds_read_b128 a[156:159], v247 offset:4096\n"
"v_mfma_f32_16x16x32_bf16 v[48:51], a[228:231], a[164:167], v[48:51]\n"
"ds_read_b128 a[160:163], v252 offset:0\n"
"v_mfma_f32_16x16x32_bf16 v[64:67], a[224:227], a[168:171], v[64:67]\n"
"ds_read_b128 a[164:167], v253 offset:0\n"
"v_mfma_f32_16x16x32_bf16 v[64:67], a[228:231], a[172:175], v[64:67]\n"
"v_mfma_f32_16x16x32_bf16 v[68:71], a[232:235], a[168:171], v[68:71]\n"
"v_mfma_f32_16x16x32_bf16 v[68:71], a[236:239], a[172:175], v[68:71]\n"
"buffer_load_dwordx4 v249, s[24:27], s40 offen offset:1024 nt lds\n"
"v_mfma_f32_16x16x32_bf16 v[72:75], a[240:243], a[168:171], v[72:75]\n"
"v_mfma_f32_16x16x32_bf16 v[72:75], a[244:247], a[172:175], v[72:75]\n"
"v_mfma_f32_16x16x32_bf16 v[76:79], a[248:251], a[168:171], v[76:79]\n"
"ds_read_b128 a[168:171], v250 offset:0\n"
"v_mfma_f32_16x16x32_bf16 v[76:79], a[252:255], a[172:175], v[76:79]\n"
"ds_read_b128 a[172:175], v251 offset:0\n"
"v_mfma_f32_16x16x32_bf16 v[92:95], a[248:251], a[176:179], v[92:95]\n"
"v_mfma_f32_16x16x32_bf16 v[92:95], a[252:255], a[180:183], v[92:95]\n"
"buffer_load_dwordx4 v254, s[24:27], s40 offen offset:2048 nt lds\n"
"v_mfma_f32_16x16x32_bf16 v[88:91], a[240:243], a[176:179], v[88:91]\n"
"s_add_u32 s49, s49, 128\n"
"v_mfma_f32_16x16x32_bf16 v[88:91], a[244:247], a[180:183], v[88:91]\n"
"s_sub_u32 s41, s49, 1280\n"
"v_mfma_f32_16x16x32_bf16 v[84:87], a[232:235], a[176:179], v[84:87]\n"
"s_min_u32 s49, s49, s41\n"
"v_mfma_f32_16x16x32_bf16 v[84:87], a[236:239], a[180:183], v[84:87]\n"
"s_xor_b32 s51, s51, s74\n"
"v_mfma_f32_16x16x32_bf16 v[80:83], a[224:227], a[176:179], v[80:83]\n"
"ds_read_b128 a[176:179], v250 offset:2048\n"
"v_mfma_f32_16x16x32_bf16 v[80:83], a[228:231], a[180:183], v[80:83]\n"
"ds_read_b128 a[180:183], v251 offset:2048\n"
"v_mfma_f32_16x16x32_bf16 v[96:99], a[224:227], a[184:187], v[96:99]\n"
"v_mfma_f32_16x16x32_bf16 v[96:99], a[228:231], a[188:191], v[96:99]\n"
"v_mfma_f32_16x16x32_bf16 v[100:103], a[232:235], a[184:187], v[100:103]\n"
"v_mfma_f32_16x16x32_bf16 v[100:103], a[236:239], a[188:191], v[100:103]\n"
"v_mfma_f32_16x16x32_bf16 v[104:107], a[240:243], a[184:187], v[104:107]\n"
"v_mfma_f32_16x16x32_bf16 v[104:107], a[244:247], a[188:191], v[104:107]\n"
"v_mfma_f32_16x16x32_bf16 v[108:111], a[248:251], a[184:187], v[108:111]\n"
"s_add_u32 s47, s47, 1\n"
"ds_read_b128 a[184:187], v250 offset:4096\n"
"v_mfma_f32_16x16x32_bf16 v[108:111], a[252:255], a[188:191], v[108:111]\n"
"s_waitcnt lgkmcnt(1)\n"
"s_cmp_lg_u32 s47, s62\n"
"ds_read_b128 a[188:191], v251 offset:4096\n"
"s_cbranch_scc1 LPN_%=\n"
"s_branch LPX_%=\n"
"LEND_%=:\n"
"LSLOW_%=:\n"
"s_waitcnt vmcnt(0) lgkmcnt(0)\n"
"s_nop 9\n"
"v_bfe_u32 v148, v242, 4, 4\n"
"v_bfe_u32 v149, v242, 8, 2\n"
"v_lshlrev_b32 v144, 8, v148\n"
"v_lshl_add_u32 v144, v149, 4, v144\n"
"v_add_u32 v144, s43, v144\n"
"v_xor_b32 v145, 64, v144\n"
"v_lshlrev_b32 v146, 1, v148\n"
"v_lshl_add_u32 v146, v149, 10, v146\n"
"v_add_u32 v146, s43, v146\n"
"v_xor_b32 v147, 64, v146\n"
"v_mov_b32 v152, 0xff7fffff\n"
"v_mov_b32 v155, 0\n"
"v_mov_b32 v0, 0\n"
"v_mov_b32 v1, 0\n"
"v_mov_b32 v2, 0\n"
"v_mov_b32 v3, 0\n"
"v_mov_b32 v4, 0\n"
"v_mov_b32 v5, 0\n"
"v_mov_b32 v6, 0\n"
"v_mov_b32 v7, 0\n"
"v_mov_b32 v8, 0\n"
"v_mov_b32 v9, 0\n"
"v_mov_b32 v10, 0\n"
"v_mov_b32 v11, 0\n"
"v_mov_b32 v12, 0\n"
"v_mov_b32 v13, 0\n"
"v_mov_b32 v14, 0\n"
"v_mov_b32 v15, 0\n"
"v_mov_b32 v16, 0\n"
"v_mov_b32 v17, 0\n"
"v_mov_b32 v18, 0\n"
"v_mov_b32 v19, 0\n"
"v_mov_b32 v20, 0\n"
"v_mov_b32 v21, 0\n"
"v_mov_b32 v22, 0\n"
"v_mov_b32 v23, 0\n"
"v_mov_b32 v24, 0\n"
"v_mov_b32 v25, 0\n"
"v_mov_b32 v26, 0\n"
"v_mov_b32 v27, 0\n"
"v_mov_b32 v28, 0\n"
"v_mov_b32 v29, 0\n"
"v_mov_b32 v30, 0\n"
"v_mov_b32 v31, 0\n"
"v_mov_b32 v156, 0xff7fffff\n"
"v_mov_b32 v159, 0\n"
"v_mov_b32 v32, 0\n"
"v_mov_b32 v33, 0\n"
"v_mov_b32 v34, 0\n"
"v_mov_b32 v35, 0\n"
"v_mov_b32 v36, 0\n"
"v_mov_b32 v37, 0\n"
"v_mov_b32 v38, 0\n"
"v_mov_b32 v39, 0\n"
"v_mov_b32 v40, 0\n"
"v_mov_b32 v41, 0\n"
"v_mov_b32 v42, 0\n"
"v_mov_b32 v43, 0\n"
"v_mov_b32 v44, 0\n"
"v_mov_b32 v45, 0\n"
"v_mov_b32 v46, 0\n"
"v_mov_b32 v47, 0\n"
"v_mov_b32 v48, 0\n"
"v_mov_b32 v49, 0\n"
"v_mov_b32 v50, 0\n"
"v_mov_b32 v51, 0\n"
"v_mov_b32 v52, 0\n"
"v_mov_b32 v53, 0\n"
"v_mov_b32 v54, 0\n"
"v_mov_b32 v55, 0\n"
"v_mov_b32 v56, 0\n"
"v_mov_b32 v57, 0\n"
"v_mov_b32 v58, 0\n"
"v_mov_b32 v59, 0\n"
"v_mov_b32 v60, 0\n"
"v_mov_b32 v61, 0\n"
"v_mov_b32 v62, 0\n"
"v_mov_b32 v63, 0\n"
"v_mov_b32 v160, 0xff7fffff\n"
"v_mov_b32 v163, 0\n"
"v_mov_b32 v64, 0\n"
"v_mov_b32 v65, 0\n"
"v_mov_b32 v66, 0\n"
"v_mov_b32 v67, 0\n"
"v_mov_b32 v68, 0\n"
"v_mov_b32 v69, 0\n"
"v_mov_b32 v70, 0\n"
"v_mov_b32 v71, 0\n"
"v_mov_b32 v72, 0\n"
"v_mov_b32 v73, 0\n"
"v_mov_b32 v74, 0\n"
"v_mov_b32 v75, 0\n"
"v_mov_b32 v76, 0\n"
"v_mov_b32 v77, 0\n"
"v_mov_b32 v78, 0\n"
"v_mov_b32 v79, 0\n"
"v_mov_b32 v80, 0\n"
"v_mov_b32 v81, 0\n"
"v_mov_b32 v82, 0\n"
"v_mov_b32 v83, 0\n"
"v_mov_b32 v84, 0\n"
"v_mov_b32 v85, 0\n"
"v_mov_b32 v86, 0\n"
"v_mov_b32 v87, 0\n"
"v_mov_b32 v88, 0\n"
"v_mov_b32 v89, 0\n"
"v_mov_b32 v90, 0\n"
"v_mov_b32 v91, 0\n"
"v_mov_b32 v92, 0\n"
"v_mov_b32 v93, 0\n"
"v_mov_b32 v94, 0\n"
"v_mov_b32 v95, 0\n"
"v_mov_b32 v164, 0xff7fffff\n"
"v_mov_b32 v167, 0\n"
"v_accvgpr_write_b32 a208, 0\n"
"v_accvgpr_write_b32 a209, 0\n"
"v_accvgpr_write_b32 a210, 0\n"
"v_accvgpr_write_b32 a211, 0\n"
"v_accvgpr_write_b32 a212, 0\n"
"v_accvgpr_write_b32 a213, 0\n"
"v_accvgpr_write_b32 a214, 0\n"
"v_accvgpr_write_b32 a215, 0\n"
"v_accvgpr_write_b32 a216, 0\n"
"v_accvgpr_write_b32 a217, 0\n"
"v_accvgpr_write_b32 a218, 0\n"
"v_accvgpr_write_b32 a219, 0\n"
"v_accvgpr_write_b32 a220, 0\n"
"v_accvgpr_write_b32 a221, 0\n"
"v_accvgpr_write_b32 a222, 0\n"
"v_accvgpr_write_b32 a223, 0\n"
"v_accvgpr_write_b32 a224, 0\n"
"v_accvgpr_write_b32 a225, 0\n"
"v_accvgpr_write_b32 a226, 0\n"
"v_accvgpr_write_b32 a227, 0\n"
"v_accvgpr_write_b32 a228, 0\n"
"v_accvgpr_write_b32 a229, 0\n"
"v_accvgpr_write_b32 a230, 0\n"
"v_accvgpr_write_b32 a231, 0\n"
"v_accvgpr_write_b32 a232, 0\n"
"v_accvgpr_write_b32 a233, 0\n"
"v_accvgpr_write_b32 a234, 0\n"
"v_accvgpr_write_b32 a235, 0\n"
"v_accvgpr_write_b32 a236, 0\n"
"v_accvgpr_write_b32 a237, 0\n"
"v_accvgpr_write_b32 a238, 0\n"
"v_accvgpr_write_b32 a239, 0\n"
"s_lshl_b32 s80, s47, 16\n"
"s_add_u32 s81, s80, 4096\n"
"s_mov_b32 s82, 0\n"
"LSLOOP_%=:\n"
"s_lshl_b32 s83, s82, 3\n"
"s_and_b32 s83, s83, 64\n"
"buffer_load_dwordx4 v[128:131], v144, s[84:87], s80 offen offset:0\n"
"buffer_load_dwordx4 v[132:135], v145, s[84:87], s80 offen offset:0\n"
"buffer_load_dwordx4 v[136:139], v144, s[84:87], s80 offen offset:128\n"
"buffer_load_dwordx4 v[140:143], v145, s[84:87], s80 offen offset:128\n"
"s_waitcnt vmcnt(0)\n"
"v_mfma_f32_16x16x32_bf16 v[96:99], v[128:131], a[0:3], 0\n"
"v_mfma_f32_16x16x32_bf16 v[96:99], v[132:135], a[4:7], v[96:99]\n"
"v_mfma_f32_16x16x32_bf16 v[96:99], v[136:139], a[8:11], v[96:99]\n"
"v_mfma_f32_16x16x32_bf16 v[96:99], v[140:143], a[12:15], v[96:99]\n"
"v_mfma_f32_16x16x32_bf16 v[104:107], v[128:131], a[16:19], 0\n"
"v_mfma_f32_16x16x32_bf16 v[104:107], v[132:135], a[20:23], v[104:107]\n"
"v_mfma_f32_16x16x32_bf16 v[104:107], v[136:139], a[24:27], v[104:107]\n"
"v_mfma_f32_16x16x32_bf16 v[104:107], v[140:143], a[28:31], v[104:107]\n"
"v_mfma_f32_16x16x32_bf16 v[112:115], v[128:131], a[32:35], 0\n"
"v_mfma_f32_16x16x32_bf16 v[112:115], v[132:135], a[36:39], v[112:115]\n"
"v_mfma_f32_16x16x32_bf16 v[112:115], v[136:139], a[40:43], v[112:115]\n"
"v_mfma_f32_16x16x32_bf16 v[112:115], v[140:143], a[44:47], v[112:115]\n"
"v_mfma_f32_16x16x32_bf16 v[120:123], v[128:131], a[192:195], 0\n"
"v_mfma_f32_16x16x32_bf16 v[120:123], v[132:135], a[196:199], v[120:123]\n"
"v_mfma_f32_16x16x32_bf16 v[120:123], v[136:139], a[200:203], v[120:123]\n"
"v_mfma_f32_16x16x32_bf16 v[120:123], v[140:143], a[204:207], v[120:123]\n"
"s_nop 7\n"
"buffer_load_dwordx4 v[128:131], v144, s[84:87], s81 offen offset:0\n"
"buffer_load_dwordx4 v[132:135], v145, s[84:87], s81 offen offset:0\n"
"buffer_load_dwordx4 v[136:139], v144, s[84:87], s81 offen offset:128\n"
"buffer_load_dwordx4 v[140:143], v145, s[84:87], s81 offen offset:128\n"
"s_waitcnt vmcnt(0)\n"
"v_mfma_f32_16x16x32_bf16 v[100:103], v[128:131], a[0:3], 0\n"
"v_mfma_f32_16x16x32_bf16 v[100:103], v[132:135], a[4:7], v[100:103]\n"
"v_mfma_f32_16x16x32_bf16 v[100:103], v[136:139], a[8:11], v[100:103]\n"
"v_mfma_f32_16x16x32_bf16 v[100:103], v[140:143], a[12:15], v[100:103]\n"
"v_mfma_f32_16x16x32_bf16 v[108:111], v[128:131], a[16:19], 0\n"
"v_mfma_f32_16x16x32_bf16 v[108:111], v[132:135], a[20:23], v[108:111]\n"
"v_mfma_f32_16x16x32_bf16 v[108:111], v[136:139], a[24:27], v[108:111]\n"
"v_mfma_f32_16x16x32_bf16 v[108:111], v[140:143], a[28:31], v[108:111]\n"
"v_mfma_f32_16x16x32_bf16 v[116:119], v[128:131], a[32:35], 0\n"
"v_mfma_f32_16x16x32_bf16 v[116:119], v[132:135], a[36:39], v[116:119]\n"
"v_mfma_f32_16x16x32_bf16 v[116:119], v[136:139], a[40:43], v[116:119]\n"
"v_mfma_f32_16x16x32_bf16 v[116:119], v[140:143], a[44:47], v[116:119]\n"
"v_mfma_f32_16x16x32_bf16 v[124:127], v[128:131], a[192:195], 0\n"
"v_mfma_f32_16x16x32_bf16 v[124:127], v[132:135], a[196:199], v[124:127]\n"
"v_mfma_f32_16x16x32_bf16 v[124:127], v[136:139], a[200:203], v[124:127]\n"
"v_mfma_f32_16x16x32_bf16 v[124:127], v[140:143], a[204:207], v[124:127]\n"
"s_nop 7\n"
"v_max3_f32 v153, v96, v97, v98\n"
"v_max3_f32 v153, v153, v99, v100\n"
"v_max3_f32 v153, v153, v101, v102\n"
"v_max3_f32 v153, v153, v103, v152\n"
"v_mov_b32 v154, v153\n"
"s_nop 1\n"
"v_permlane32_swap_b32 v153, v154\n"
"v_max_f32 v153, v153, v154\n"
"v_mov_b32 v154, v153\n"
"s_nop 1\n"
"v_permlane16_swap_b32 v153, v154\n"
"v_max_f32 v153, v153, v154\n"
"v_sub_f32 v154, v152, v153\n"
"v_exp_f32 v154, v154\n"
"v_mov_b32 v152, v153\n"
"v_sub_f32 v96, v96, v152\n"
"v_sub_f32 v97, v97, v152\n"
"v_sub_f32 v98, v98, v152\n"
"v_sub_f32 v99, v99, v152\n"
"v_sub_f32 v100, v100, v152\n"
"v_sub_f32 v101, v101, v152\n"
"v_sub_f32 v102, v102, v152\n"
"v_sub_f32 v103, v103, v152\n"
"v_exp_f32 v96, v96\n"
"v_exp_f32 v97, v97\n"
"v_exp_f32 v98, v98\n"
"v_exp_f32 v99, v99\n"
"v_exp_f32 v100, v100\n"
"v_exp_f32 v101, v101\n"
"v_exp_f32 v102, v102\n"
"v_exp_f32 v103, v103\n"
"v_mul_f32 v155, v155, v154\n"
"v_add_f32 v155, v155, v96\n"
"v_add_f32 v155, v155, v97\n"
"v_add_f32 v155, v155, v98\n"
"v_add_f32 v155, v155, v99\n"
"v_add_f32 v155, v155, v100\n"
"v_add_f32 v155, v155, v101\n"
"v_add_f32 v155, v155, v102\n"
"v_add_f32 v155, v155, v103\n"
"v_cvt_pk_bf16_f32 v96, v96, v97\n"
"v_cvt_pk_bf16_f32 v97, v98, v99\n"
"v_cvt_pk_bf16_f32 v98, v100, v101\n"
"v_cvt_pk_bf16_f32 v99, v102, v103\n"
"v_mov_b32_dpp v154, v154 row_ror:12 row_mask:0x2 bank_mask:0xf\n"
"s_nop 1\n"
"v_mov_b32_dpp v154, v154 row_ror:8 row_mask:0x4 bank_mask:0xf\n"
"s_nop 1\n"
"v_mov_b32_dpp v154, v154 row_ror:4 row_mask:0x8 bank_mask:0xf\n"
"s_nop 1\n"
"v_mul_f32_dpp v0, v154, v0 row_newbcast:0 row_mask:0xf bank_mask:0xf\n"
"v_mul_f32_dpp v1, v154, v1 row_newbcast:1 row_mask:0xf bank_mask:0xf\n"
"v_mul_f32_dpp v2, v154, v2 row_newbcast:2 row_mask:0xf bank_mask:0xf\n"
"v_mul_f32_dpp v3, v154, v3 row_newbcast:3 row_mask:0xf bank_mask:0xf\n"
"v_mul_f32_dpp v4, v154, v4 row_newbcast:0 row_mask:0xf bank_mask:0xf\n"
"v_mul_f32_dpp v5, v154, v5 row_newbcast:1 row_mask:0xf bank_mask:0xf\n"
"v_mul_f32_dpp v6, v154, v6 row_newbcast:2 row_mask:0xf bank_mask:0xf\n"
"v_mul_f32_dpp v7, v154, v7 row_newbcast:3 row_mask:0xf bank_mask:0xf\n"
"v_mul_f32_dpp v8, v154, v8 row_newbcast:0 row_mask:0xf bank_mask:0xf\n"
"v_mul_f32_dpp v9, v154, v9 row_newbcast:1 row_mask:0xf bank_mask:0xf\n"
"v_mul_f32_dpp v10, v154, v10 row_newbcast:2 row_mask:0xf bank_mask:0xf\n"
"v_mul_f32_dpp v11, v154, v11 row_newbcast:3 row_mask:0xf bank_mask:0xf\n"
"v_mul_f32_dpp v12, v154, v12 row_newbcast:0 row_mask:0xf bank_mask:0xf\n"
"v_mul_f32_dpp v13, v154, v13 row_newbcast:1 row_mask:0xf bank_mask:0xf\n"
"v_mul_f32_dpp v14, v154, v14 row_newbcast:2 row_mask:0xf bank_mask:0xf\n"
"v_mul_f32_dpp v15, v154, v15 row_newbcast:3 row_mask:0xf bank_mask:0xf\n"
"v_mul_f32_dpp v16, v154, v16 row_newbcast:0 row_mask:0xf bank_mask:0xf\n"
"v_mul_f32_dpp v17, v154, v17 row_newbcast:1 row_mask:0xf bank_mask:0xf\n"
"v_mul_f32_dpp v18, v154, v18 row_newbcast:2 row_mask:0xf bank_mask:0xf\n"
"v_mul_f32_dpp v19, v154, v19 row_newbcast:3 row_mask:0xf bank_mask:0xf\n"
"v_mul_f32_dpp v20, v154, v20 row_newbcast:0 row_mask:0xf bank_mask:0xf\n"
"v_mul_f32_dpp v21, v154, v21 row_newbcast:1 row_mask:0xf bank_mask:0xf\n"
"v_mul_f32_dpp v22, v154, v22 row_newbcast:2 row_mask:0xf bank_mask:0xf\n"
"v_mul_f32_dpp v23, v154, v23 row_newbcast:3 row_mask:0xf bank_mask:0xf\n"
"v_mul_f32_dpp v24, v154, v24 row_newbcast:0 row_mask:0xf bank_mask:0xf\n"
"v_mul_f32_dpp v25, v154, v25 row_newbcast:1 row_mask:0xf bank_mask:0xf\n"
"v_mul_f32_dpp v26, v154, v26 row_newbcast:2 row_mask:0xf bank_mask:0xf\n"
"v_mul_f32_dpp v27, v154, v27 row_newbcast:3 row_mask:0xf bank_mask:0xf\n"
"v_mul_f32_dpp v28, v154, v28 row_newbcast:0 row_mask:0xf bank_mask:0xf\n"
"v_mul_f32_dpp v29, v154, v29 row_newbcast:1 row_mask:0xf bank_mask:0xf\n"
"v_mul_f32_dpp v30, v154, v30 row_newbcast:2 row_mask:0xf bank_mask:0xf\n"
"v_mul_f32_dpp v31, v154, v31 row_newbcast:3 row_mask:0xf bank_mask:0xf\n"
"v_max3_f32 v157, v104, v105, v106\n"
"v_max3_f32 v157, v157, v107, v108\n"
"v_max3_f32 v157, v157, v109, v110\n"
"v_max3_f32 v157, v157, v111, v156\n"
"v_mov_b32 v158, v157\n"
"s_nop 1\n"
"v_permlane32_swap_b32 v157, v158\n"
"v_max_f32 v157, v157, v158\n"
"v_mov_b32 v158, v157\n"
"s_nop 1\n"
"v_permlane16_swap_b32 v157, v158\n"
"v_max_f32 v157, v157, v158\n"
"v_sub_f32 v158, v156, v157\n"
"v_exp_f32 v158, v158\n"
"v_mov_b32 v156, v157\n"
"v_sub_f32 v104, v104, v156\n"
"v_sub_f32 v105, v105, v156\n"
"v_sub_f32 v106, v106, v156\n"
"v_sub_f32 v107, v107, v156\n"
"v_sub_f32 v108, v108, v156\n"
"v_sub_f32 v109, v109, v156\n"
"v_sub_f32 v110, v110, v156\n"
"v_sub_f32 v111, v111, v156\n"
"v_exp_f32 v104, v104\n"
"v_exp_f32 v105, v105\n"
"v_exp_f32 v106, v106\n"
"v_exp_f32 v107, v107\n"
"v_exp_f32 v108, v108\n"
"v_exp_f32 v109, v109\n"
"v_exp_f32 v110, v110\n"
"v_exp_f32 v111, v111\n"
"v_mul_f32 v159, v159, v158\n"
"v_add_f32 v159, v159, v104\n"
"v_add_f32 v159, v159, v105\n"
"v_add_f32 v159, v159, v106\n"
"v_add_f32 v159, v159, v107\n"
"v_add_f32 v159, v159, v108\n"
"v_add_f32 v159, v159, v109\n"
"v_add_f32 v159, v159, v110\n"
"v_add_f32 v159, v159, v111\n"
"v_cvt_pk_bf16_f32 v104, v104, v105\n"
"v_cvt_pk_bf16_f32 v105, v106, v107\n"
"v_cvt_pk_bf16_f32 v106, v108, v109\n"
"v_cvt_pk_bf16_f32 v107, v110, v111\n"
"v_mov_b32_dpp v158, v158 row_ror:12 row_mask:0x2 bank_mask:0xf\n"
"s_nop 1\n"
"v_mov_b32_dpp v158, v158 row_ror:8 row_mask:0x4 bank_mask:0xf\n"
"s_nop 1\n"
"v_mov_b32_dpp v158, v158 row_ror:4 row_mask:0x8 bank_mask:0xf\n"
"s_nop 1\n"
"v_mul_f32_dpp v32, v158, v32 row_newbcast:0 row_mask:0xf bank_mask:0xf\n"
"v_mul_f32_dpp v33, v158, v33 row_newbcast:1 row_mask:0xf bank_mask:0xf\n"
"v_mul_f32_dpp v34, v158, v34 row_newbcast:2 row_mask:0xf bank_mask:0xf\n"
"v_mul_f32_dpp v35, v158, v35 row_newbcast:3 row_mask:0xf bank_mask:0xf\n"
"v_mul_f32_dpp v36, v158, v36 row_newbcast:0 row_mask:0xf bank_mask:0xf\n"
"v_mul_f32_dpp v37, v158, v37 row_newbcast:1 row_mask:0xf bank_mask:0xf\n"
"v_mul_f32_dpp v38, v158, v38 row_newbcast:2 row_mask:0xf bank_mask:0xf\n"
"v_mul_f32_dpp v39, v158, v39 row_newbcast:3 row_mask:0xf bank_mask:0xf\n"
"v_mul_f32_dpp v40, v158, v40 row_newbcast:0 row_mask:0xf bank_mask:0xf\n"
"v_mul_f32_dpp v41, v158, v41 row_newbcast:1 row_mask:0xf bank_mask:0xf\n"
"v_mul_f32_dpp v42, v158, v42 row_newbcast:2 row_mask:0xf bank_mask:0xf\n"
"v_mul_f32_dpp v43, v158, v43 row_newbcast:3 row_mask:0xf bank_mask:0xf\n"
"v_mul_f32_dpp v44, v158, v44 row_newbcast:0 row_mask:0xf bank_mask:0xf\n"
"v_mul_f32_dpp v45, v158, v45 row_newbcast:1 row_mask:0xf bank_mask:0xf\n"
"v_mul_f32_dpp v46, v158, v46 row_newbcast:2 row_mask:0xf bank_mask:0xf\n"
"v_mul_f32_dpp v47, v158, v47 row_newbcast:3 row_mask:0xf bank_mask:0xf\n"
"v_mul_f32_dpp v48, v158, v48 row_newbcast:0 row_mask:0xf bank_mask:0xf\n"
"v_mul_f32_dpp v49, v158, v49 row_newbcast:1 row_mask:0xf bank_mask:0xf\n"
"v_mul_f32_dpp v50, v158, v50 row_newbcast:2 row_mask:0xf bank_mask:0xf\n"
"v_mul_f32_dpp v51, v158, v51 row_newbcast:3 row_mask:0xf bank_mask:0xf\n"
"v_mul_f32_dpp v52, v158, v52 row_newbcast:0 row_mask:0xf bank_mask:0xf\n"
"v_mul_f32_dpp v53, v158, v53 row_newbcast:1 row_mask:0xf bank_mask:0xf\n"
"v_mul_f32_dpp v54, v158, v54 row_newbcast:2 row_mask:0xf bank_mask:0xf\n"
"v_mul_f32_dpp v55, v158, v55 row_newbcast:3 row_mask:0xf bank_mask:0xf\n"
"v_mul_f32_dpp v56, v158, v56 row_newbcast:0 row_mask:0xf bank_mask:0xf\n"
"v_mul_f32_dpp v57, v158, v57 row_newbcast:1 row_mask:0xf bank_mask:0xf\n"
"v_mul_f32_dpp v58, v158, v58 row_newbcast:2 row_mask:0xf bank_mask:0xf\n"
"v_mul_f32_dpp v59, v158, v59 row_newbcast:3 row_mask:0xf bank_mask:0xf\n"
"v_mul_f32_dpp v60, v158, v60 row_newbcast:0 row_mask:0xf bank_mask:0xf\n"
"v_mul_f32_dpp v61, v158, v61 row_newbcast:1 row_mask:0xf bank_mask:0xf\n"
"v_mul_f32_dpp v62, v158, v62 row_newbcast:2 row_mask:0xf bank_mask:0xf\n"
"v_mul_f32_dpp v63, v158, v63 row_newbcast:3 row_mask:0xf bank_mask:0xf\n"
"v_max3_f32 v161, v112, v113, v114\n"
"v_max3_f32 v161, v161, v115, v116\n"
"v_max3_f32 v161, v161, v117, v118\n"
"v_max3_f32 v161, v161, v119, v160\n"
"v_mov_b32 v162, v161\n"
"s_nop 1\n"
"v_permlane32_swap_b32 v161, v162\n"
"v_max_f32 v161, v161, v162\n"
"v_mov_b32 v162, v161\n"
"s_nop 1\n"
"v_permlane16_swap_b32 v161, v162\n"
"v_max_f32 v161, v161, v162\n"
"v_sub_f32 v162, v160, v161\n"
"v_exp_f32 v162, v162\n"
"v_mov_b32 v160, v161\n"
"v_sub_f32 v112, v112, v160\n"
"v_sub_f32 v113, v113, v160\n"
"v_sub_f32 v114, v114, v160\n"
"v_sub_f32 v115, v115, v160\n"
"v_sub_f32 v116, v116, v160\n"
"v_sub_f32 v117, v117, v160\n"
"v_sub_f32 v118, v118, v160\n"
"v_sub_f32 v119, v119, v160\n"
"v_exp_f32 v112, v112\n"
"v_exp_f32 v113, v113\n"
"v_exp_f32 v114, v114\n"
"v_exp_f32 v115, v115\n"
"v_exp_f32 v116, v116\n"
"v_exp_f32 v117, v117\n"
"v_exp_f32 v118, v118\n"
"v_exp_f32 v119, v119\n"
"v_mul_f32 v163, v163, v162\n"
"v_add_f32 v163, v163, v112\n"
"v_add_f32 v163, v163, v113\n"
"v_add_f32 v163, v163, v114\n"
"v_add_f32 v163, v163, v115\n"
"v_add_f32 v163, v163, v116\n"
"v_add_f32 v163, v163, v117\n"
"v_add_f32 v163, v163, v118\n"
"v_add_f32 v163, v163, v119\n"
"v_cvt_pk_bf16_f32 v112, v112, v113\n"
"v_cvt_pk_bf16_f32 v113, v114, v115\n"
"v_cvt_pk_bf16_f32 v114, v116, v117\n"
"v_cvt_pk_bf16_f32 v115, v118, v119\n"
"v_mov_b32_dpp v162, v162 row_ror:12 row_mask:0x2 bank_mask:0xf\n"
"s_nop 1\n"
"v_mov_b32_dpp v162, v162 row_ror:8 row_mask:0x4 bank_mask:0xf\n"
"s_nop 1\n"
"v_mov_b32_dpp v162, v162 row_ror:4 row_mask:0x8 bank_mask:0xf\n"
"s_nop 1\n"
"v_mul_f32_dpp v64, v162, v64 row_newbcast:0 row_mask:0xf bank_mask:0xf\n"
"v_mul_f32_dpp v65, v162, v65 row_newbcast:1 row_mask:0xf bank_mask:0xf\n"
"v_mul_f32_dpp v66, v162, v66 row_newbcast:2 row_mask:0xf bank_mask:0xf\n"
"v_mul_f32_dpp v67, v162, v67 row_newbcast:3 row_mask:0xf bank_mask:0xf\n"
"v_mul_f32_dpp v68, v162, v68 row_newbcast:0 row_mask:0xf bank_mask:0xf\n"
"v_mul_f32_dpp v69, v162, v69 row_newbcast:1 row_mask:0xf bank_mask:0xf\n"
"v_mul_f32_dpp v70, v162, v70 row_newbcast:2 row_mask:0xf bank_mask:0xf\n"
"v_mul_f32_dpp v71, v162, v71 row_newbcast:3 row_mask:0xf bank_mask:0xf\n"
"v_mul_f32_dpp v72, v162, v72 row_newbcast:0 row_mask:0xf bank_mask:0xf\n"
"v_mul_f32_dpp v73, v162, v73 row_newbcast:1 row_mask:0xf bank_mask:0xf\n"
"v_mul_f32_dpp v74, v162, v74 row_newbcast:2 row_mask:0xf bank_mask:0xf\n"
"v_mul_f32_dpp v75, v162, v75 row_newbcast:3 row_mask:0xf bank_mask:0xf\n"
"v_mul_f32_dpp v76, v162, v76 row_newbcast:0 row_mask:0xf bank_mask:0xf\n"
"v_mul_f32_dpp v77, v162, v77 row_newbcast:1 row_mask:0xf bank_mask:0xf\n"
"v_mul_f32_dpp v78, v162, v78 row_newbcast:2 row_mask:0xf bank_mask:0xf\n"
"v_mul_f32_dpp v79, v162, v79 row_newbcast:3 row_mask:0xf bank_mask:0xf\n"
"v_mul_f32_dpp v80, v162, v80 row_newbcast:0 row_mask:0xf bank_mask:0xf\n"
"v_mul_f32_dpp v81, v162, v81 row_newbcast:1 row_mask:0xf bank_mask:0xf\n"
"v_mul_f32_dpp v82, v162, v82 row_newbcast:2 row_mask:0xf bank_mask:0xf\n"
"v_mul_f32_dpp v83, v162, v83 row_newbcast:3 row_mask:0xf bank_mask:0xf\n"
"v_mul_f32_dpp v84, v162, v84 row_newbcast:0 row_mask:0xf bank_mask:0xf\n"
"v_mul_f32_dpp v85, v162, v85 row_newbcast:1 row_mask:0xf bank_mask:0xf\n"
"v_mul_f32_dpp v86, v162, v86 row_newbcast:2 row_mask:0xf bank_mask:0xf\n"
"v_mul_f32_dpp v87, v162, v87 row_newbcast:3 row_mask:0xf bank_mask:0xf\n"
"v_mul_f32_dpp v88, v162, v88 row_newbcast:0 row_mask:0xf bank_mask:0xf\n"
"v_mul_f32_dpp v89, v162, v89 row_newbcast:1 row_mask:0xf bank_mask:0xf\n"
"v_mul_f32_dpp v90, v162, v90 row_newbcast:2 row_mask:0xf bank_mask:0xf\n"
"v_mul_f32_dpp v91, v162, v91 row_newbcast:3 row_mask:0xf bank_mask:0xf\n"
"v_mul_f32_dpp v92, v162, v92 row_newbcast:0 row_mask:0xf bank_mask:0xf\n"
"v_mul_f32_dpp v93, v162, v93 row_newbcast:1 row_mask:0xf bank_mask:0xf\n"
"v_mul_f32_dpp v94, v162, v94 row_newbcast:2 row_mask:0xf bank_mask:0xf\n"
"v_mul_f32_dpp v95, v162, v95 row_newbcast:3 row_mask:0xf bank_mask:0xf\n"
"s_cmp_lg_u32 s83, s43\n"
"s_cbranch_scc1 LSK3A_%=\n"
"v_max3_f32 v165, v120, v121, v122\n"
"v_max3_f32 v165, v165, v123, v124\n"
"v_max3_f32 v165, v165, v125, v126\n"
"v_max3_f32 v165, v165, v127, v164\n"
"v_mov_b32 v166, v165\n"
"s_nop 1\n"
"v_permlane32_swap_b32 v165, v166\n"
"v_max_f32 v165, v165, v166\n"
"v_mov_b32 v166, v165\n"
"s_nop 1\n"
"v_permlane16_swap_b32 v165, v166\n"
"v_max_f32 v165, v165, v166\n"
"v_sub_f32 v166, v164, v165\n"
"v_exp_f32 v166, v166\n"
"v_mov_b32 v164, v165\n"
"v_sub_f32 v120, v120, v164\n"
"v_sub_f32 v121, v121, v164\n"
"v_sub_f32 v122, v122, v164\n"
"v_sub_f32 v123, v123, v164\n"
"v_sub_f32 v124, v124, v164\n"
"v_sub_f32 v125, v125, v164\n"
"v_sub_f32 v126, v126, v164\n"
"v_sub_f32 v127, v127, v164\n"
"v_exp_f32 v120, v120\n"
"v_exp_f32 v121, v121\n"
"v_exp_f32 v122, v122\n"
"v_exp_f32 v123, v123\n"
"v_exp_f32 v124, v124\n"
"v_exp_f32 v125, v125\n"
"v_exp_f32 v126, v126\n"
"v_exp_f32 v127, v127\n"
"v_mul_f32 v167, v167, v166\n"
"v_add_f32 v167, v167, v120\n"
"v_add_f32 v167, v167, v121\n"
"v_add_f32 v167, v167, v122\n"
"v_add_f32 v167, v167, v123\n"
"v_add_f32 v167, v167, v124\n"
"v_add_f32 v167, v167, v125\n"
"v_add_f32 v167, v167, v126\n"
"v_add_f32 v167, v167, v127\n"
"v_cvt_pk_bf16_f32 v120, v120, v121\n"
"v_cvt_pk_bf16_f32 v121, v122, v123\n"
"v_cvt_pk_bf16_f32 v122, v124, v125\n"
"v_cvt_pk_bf16_f32 v123, v126, v127\n"
"v_mov_b32_dpp v166, v166 row_ror:12 row_mask:0x2 bank_mask:0xf\n"
"s_nop 1\n"
"v_mov_b32_dpp v166, v166 row_ror:8 row_mask:0x4 bank_mask:0xf\n"
"s_nop 1\n"
"v_mov_b32_dpp v166, v166 row_ror:4 row_mask:0x8 bank_mask:0xf\n"
"v_accvgpr_read_b32 v124, a208\n"
"s_nop 1\n"
"v_mul_f32_dpp v124, v166, v124 row_newbcast:0 row_mask:0xf bank_mask:0xf\n"
"v_accvgpr_write_b32 a208, v124\n"
"v_accvgpr_read_b32 v125, a209\n"
"s_nop 1\n"
"v_mul_f32_dpp v125, v166, v125 row_newbcast:1 row_mask:0xf bank_mask:0xf\n"
"v_accvgpr_write_b32 a209, v125\n"
"v_accvgpr_read_b32 v126, a210\n"
"s_nop 1\n"
"v_mul_f32_dpp v126, v166, v126 row_newbcast:2 row_mask:0xf bank_mask:0xf\n"
"v_accvgpr_write_b32 a210, v126\n"
"v_accvgpr_read_b32 v127, a211\n"
"s_nop 1\n"
"v_mul_f32_dpp v127, v166, v127 row_newbcast:3 row_mask:0xf bank_mask:0xf\n"
"v_accvgpr_write_b32 a211, v127\n"
"v_accvgpr_read_b32 v124, a212\n"
"s_nop 1\n"
"v_mul_f32_dpp v124, v166, v124 row_newbcast:0 row_mask:0xf bank_mask:0xf\n"
"v_accvgpr_write_b32 a212, v124\n"
"v_accvgpr_read_b32 v125, a213\n"
"s_nop 1\n"
"v_mul_f32_dpp v125, v166, v125 row_newbcast:1 row_mask:0xf bank_mask:0xf\n"
"v_accvgpr_write_b32 a213, v125\n"
"v_accvgpr_read_b32 v126, a214\n"
"s_nop 1\n"
"v_mul_f32_dpp v126, v166, v126 row_newbcast:2 row_mask:0xf bank_mask:0xf\n"
"v_accvgpr_write_b32 a214, v126\n"
"v_accvgpr_read_b32 v127, a215\n"
"s_nop 1\n"
"v_mul_f32_dpp v127, v166, v127 row_newbcast:3 row_mask:0xf bank_mask:0xf\n"
"v_accvgpr_write_b32 a215, v127\n"
"v_accvgpr_read_b32 v124, a216\n"
"s_nop 1\n"
"v_mul_f32_dpp v124, v166, v124 row_newbcast:0 row_mask:0xf bank_mask:0xf\n"
"v_accvgpr_write_b32 a216, v124\n"
"v_accvgpr_read_b32 v125, a217\n"
"s_nop 1\n"
"v_mul_f32_dpp v125, v166, v125 row_newbcast:1 row_mask:0xf bank_mask:0xf\n"
"v_accvgpr_write_b32 a217, v125\n"
"v_accvgpr_read_b32 v126, a218\n"
"s_nop 1\n"
"v_mul_f32_dpp v126, v166, v126 row_newbcast:2 row_mask:0xf bank_mask:0xf\n"
"v_accvgpr_write_b32 a218, v126\n"
"v_accvgpr_read_b32 v127, a219\n"
"s_nop 1\n"
"v_mul_f32_dpp v127, v166, v127 row_newbcast:3 row_mask:0xf bank_mask:0xf\n"
"v_accvgpr_write_b32 a219, v127\n"
"v_accvgpr_read_b32 v124, a220\n"
"s_nop 1\n"
"v_mul_f32_dpp v124, v166, v124 row_newbcast:0 row_mask:0xf bank_mask:0xf\n"
"v_accvgpr_write_b32 a220, v124\n"
"v_accvgpr_read_b32 v125, a221\n"
"s_nop 1\n"
"v_mul_f32_dpp v125, v166, v125 row_newbcast:1 row_mask:0xf bank_mask:0xf\n"
"v_accvgpr_write_b32 a221, v125\n"
"v_accvgpr_read_b32 v126, a222\n"
"s_nop 1\n"
"v_mul_f32_dpp v126, v166, v126 row_newbcast:2 row_mask:0xf bank_mask:0xf\n"
"v_accvgpr_write_b32 a222, v126\n"
"v_accvgpr_read_b32 v127, a223\n"
"s_nop 1\n"
"v_mul_f32_dpp v127, v166, v127 row_newbcast:3 row_mask:0xf bank_mask:0xf\n"
"v_accvgpr_write_b32 a223, v127\n"
"v_accvgpr_read_b32 v124, a224\n"
"s_nop 1\n"
"v_mul_f32_dpp v124, v166, v124 row_newbcast:0 row_mask:0xf bank_mask:0xf\n"
"v_accvgpr_write_b32 a224, v124\n"
"v_accvgpr_read_b32 v125, a225\n"
"s_nop 1\n"
"v_mul_f32_dpp v125, v166, v125 row_newbcast:1 row_mask:0xf bank_mask:0xf\n"
"v_accvgpr_write_b32 a225, v125\n"
"v_accvgpr_read_b32 v126, a226\n"
"s_nop 1\n"
"v_mul_f32_dpp v126, v166, v126 row_newbcast:2 row_mask:0xf bank_mask:0xf\n"
"v_accvgpr_write_b32 a226, v126\n"
"v_accvgpr_read_b32 v127, a227\n"
"s_nop 1\n"
"v_mul_f32_dpp v127, v166, v127 row_newbcast:3 row_mask:0xf bank_mask:0xf\n"
"v_accvgpr_write_b32 a227, v127\n"
"v_accvgpr_read_b32 v124, a228\n"
"s_nop 1\n"
"v_mul_f32_dpp v124, v166, v124 row_newbcast:0 row_mask:0xf bank_mask:0xf\n"
"v_accvgpr_write_b32 a228, v124\n"
"v_accvgpr_read_b32 v125, a229\n"
"s_nop 1\n"
"v_mul_f32_dpp v125, v166, v125 row_newbcast:1 row_mask:0xf bank_mask:0xf\n"
"v_accvgpr_write_b32 a229, v125\n"
"v_accvgpr_read_b32 v126, a230\n"
"s_nop 1\n"
"v_mul_f32_dpp v126, v166, v126 row_newbcast:2 row_mask:0xf bank_mask:0xf\n"
"v_accvgpr_write_b32 a230, v126\n"
"v_accvgpr_read_b32 v127, a231\n"
"s_nop 1\n"
"v_mul_f32_dpp v127, v166, v127 row_newbcast:3 row_mask:0xf bank_mask:0xf\n"
"v_accvgpr_write_b32 a231, v127\n"
"v_accvgpr_read_b32 v124, a232\n"
"s_nop 1\n"
"v_mul_f32_dpp v124, v166, v124 row_newbcast:0 row_mask:0xf bank_mask:0xf\n"
"v_accvgpr_write_b32 a232, v124\n"
"v_accvgpr_read_b32 v125, a233\n"
"s_nop 1\n"
"v_mul_f32_dpp v125, v166, v125 row_newbcast:1 row_mask:0xf bank_mask:0xf\n"
"v_accvgpr_write_b32 a233, v125\n"
"v_accvgpr_read_b32 v126, a234\n"
"s_nop 1\n"
"v_mul_f32_dpp v126, v166, v126 row_newbcast:2 row_mask:0xf bank_mask:0xf\n"
"v_accvgpr_write_b32 a234, v126\n"
"v_accvgpr_read_b32 v127, a235\n"
"s_nop 1\n"
"v_mul_f32_dpp v127, v166, v127 row_newbcast:3 row_mask:0xf bank_mask:0xf\n"
"v_accvgpr_write_b32 a235, v127\n"
"v_accvgpr_read_b32 v124, a236\n"
"s_nop 1\n"
"v_mul_f32_dpp v124, v166, v124 row_newbcast:0 row_mask:0xf bank_mask:0xf\n"
"v_accvgpr_write_b32 a236, v124\n"
"v_accvgpr_read_b32 v125, a237\n"
"s_nop 1\n"
"v_mul_f32_dpp v125, v166, v125 row_newbcast:1 row_mask:0xf bank_mask:0xf\n"
"v_accvgpr_write_b32 a237, v125\n"
"v_accvgpr_read_b32 v126, a238\n"
"s_nop 1\n"
"v_mul_f32_dpp v126, v166, v126 row_newbcast:2 row_mask:0xf bank_mask:0xf\n"
"v_accvgpr_write_b32 a238, v126\n"
"v_accvgpr_read_b32 v127, a239\n"
"s_nop 1\n"
"v_mul_f32_dpp v127, v166, v127 row_newbcast:3 row_mask:0xf bank_mask:0xf\n"
"v_accvgpr_write_b32 a239, v127\n"
"LSK3A_%=:\n"
"s_nop 3\n"
"buffer_load_ushort v128, v146, s[88:91], s80 offen offset:0\n"
"buffer_load_ushort v129, v146, s[88:91], s80 offen offset:256\n"
"buffer_load_ushort v130, v146, s[88:91], s80 offen offset:512\n"
"buffer_load_ushort v131, v146, s[88:91], s80 offen offset:768\n"
"buffer_load_ushort v132, v146, s[88:91], s81 offen offset:0\n"
"buffer_load_ushort v133, v146, s[88:91], s81 offen offset:256\n"
"buffer_load_ushort v134, v146, s[88:91], s81 offen offset:512\n"
"buffer_load_ushort v135, v146, s[88:91], s81 offen offset:768\n"
"buffer_load_ushort v136, v146, s[88:91], s80 offen offset:32\n"
"buffer_load_ushort v137, v146, s[88:91], s80 offen offset:288\n"
"buffer_load_ushort v138, v146, s[88:91], s80 offen offset:544\n"
"buffer_load_ushort v139, v146, s[88:91], s80 offen offset:800\n"
"buffer_load_ushort v140, v146, s[88:91], s81 offen offset:32\n"
"buffer_load_ushort v141, v146, s[88:91], s81 offen offset:288\n"
"buffer_load_ushort v142, v146, s[88:91], s81 offen offset:544\n"
"buffer_load_ushort v143, v146, s[88:91], s81 offen offset:800\n"
"s_waitcnt vmcnt(0)\n"
"v_lshl_or_b32 v128, v129, 16, v128\n"
"v_lshl_or_b32 v129, v131, 16, v130\n"
"v_lshl_or_b32 v130, v133, 16, v132\n"
"v_lshl_or_b32 v131, v135, 16, v134\n"
"v_lshl_or_b32 v136, v137, 16, v136\n"
"v_lshl_or_b32 v137, v139, 16, v138\n"
"v_lshl_or_b32 v138, v141, 16, v140\n"
"v_lshl_or_b32 v139, v143, 16, v142\n"
"v_mfma_f32_16x16x32_bf16 v[0:3], v[96:99], v[128:131], v[0:3]\n"
"s_nop 0\n"
"v_mfma_f32_16x16x32_bf16 v[4:7], v[96:99], v[136:139], v[4:7]\n"
"v_mfma_f32_16x16x32_bf16 v[32:35], v[104:107], v[128:131], v[32:35]\n"
"v_mfma_f32_16x16x32_bf16 v[36:39], v[104:107], v[136:139], v[36:39]\n"
"v_mfma_f32_16x16x32_bf16 v[64:67], v[112:115], v[128:131], v[64:67]\n"
"v_mfma_f32_16x16x32_bf16 v[68:71], v[112:115], v[136:139], v[68:71]\n"
"s_cmp_lg_u32 s83, s43\n"
"s_cbranch_scc1 LSK3B0_%=\n"
"v_mfma_f32_16x16x32_bf16 a[208:211], v[120:123], v[128:131], a[208:211]\n"
"v_mfma_f32_16x16x32_bf16 a[212:215], v[120:123], v[136:139], a[212:215]\n"
"LSK3B0_%=:\n"
"s_nop 3\n"
"buffer_load_ushort v128, v147, s[88:91], s80 offen offset:0\n"
"buffer_load_ushort v129, v147, s[88:91], s80 offen offset:256\n"
"buffer_load_ushort v130, v147, s[88:91], s80 offen offset:512\n"
"buffer_load_ushort v131, v147, s[88:91], s80 offen offset:768\n"
"buffer_load_ushort v132, v147, s[88:91], s81 offen offset:0\n"
"buffer_load_ushort v133, v147, s[88:91], s81 offen offset:256\n"
"buffer_load_ushort v134, v147, s[88:91], s81 offen offset:512\n"
"buffer_load_ushort v135, v147, s[88:91], s81 offen offset:768\n"
"buffer_load_ushort v136, v147, s[88:91], s80 offen offset:32\n"
"buffer_load_ushort v137, v147, s[88:91], s80 offen offset:288\n"
"buffer_load_ushort v138, v147, s[88:91], s80 offen offset:544\n"
"buffer_load_ushort v139, v147, s[88:91], s80 offen offset:800\n"
"buffer_load_ushort v140, v147, s[88:91], s81 offen offset:32\n"
"buffer_load_ushort v141, v147, s[88:91], s81 offen offset:288\n"
"buffer_load_ushort v142, v147, s[88:91], s81 offen offset:544\n"
"buffer_load_ushort v143, v147, s[88:91], s81 offen offset:800\n"
"s_waitcnt vmcnt(0)\n"
"v_lshl_or_b32 v128, v129, 16, v128\n"
"v_lshl_or_b32 v129, v131, 16, v130\n"
"v_lshl_or_b32 v130, v133, 16, v132\n"
"v_lshl_or_b32 v131, v135, 16, v134\n"
"v_lshl_or_b32 v136, v137, 16, v136\n"
"v_lshl_or_b32 v137, v139, 16, v138\n"
"v_lshl_or_b32 v138, v141, 16, v140\n"
"v_lshl_or_b32 v139, v143, 16, v142\n"
"v_mfma_f32_16x16x32_bf16 v[8:11], v[96:99], v[128:131], v[8:11]\n"
"s_nop 0\n"
"v_mfma_f32_16x16x32_bf16 v[12:15], v[96:99], v[136:139], v[12:15]\n"
"v_mfma_f32_16x16x32_bf16 v[40:43], v[104:107], v[128:131], v[40:43]\n"
"v_mfma_f32_16x16x32_bf16 v[44:47], v[104:107], v[136:139], v[44:47]\n"
"v_mfma_f32_16x16x32_bf16 v[72:75], v[112:115], v[128:131], v[72:75]\n"
"v_mfma_f32_16x16x32_bf16 v[76:79], v[112:115], v[136:139], v[76:79]\n"
"s_cmp_lg_u32 s83, s43\n"
"s_cbranch_scc1 LSK3B1_%=\n"
"v_mfma_f32_16x16x32_bf16 a[216:219], v[120:123], v[128:131], a[216:219]\n"
"v_mfma_f32_16x16x32_bf16 a[220:223], v[120:123], v[136:139], a[220:223]\n"
"LSK3B1_%=:\n"
"s_nop 3\n"
"buffer_load_ushort v128, v146, s[88:91], s80 offen offset:128\n"
"buffer_load_ushort v129, v146, s[88:91], s80 offen offset:384\n"
"buffer_load_ushort v130, v146, s[88:91], s80 offen offset:640\n"
"buffer_load_ushort v131, v146, s[88:91], s80 offen offset:896\n"
"buffer_load_ushort v132, v146, s[88:91], s81 offen offset:128\n"
"buffer_load_ushort v133, v146, s[88:91], s81 offen offset:384\n"
"buffer_load_ushort v134, v146, s[88:91], s81 offen offset:640\n"
"buffer_load_ushort v135, v146, s[88:91], s81 offen offset:896\n"
"buffer_load_ushort v136, v146, s[88:91], s80 offen offset:160\n"
"buffer_load_ushort v137, v146, s[88:91], s80 offen offset:416\n"
"buffer_load_ushort v138, v146, s[88:91], s80 offen offset:672\n"
"buffer_load_ushort v139, v146, s[88:91], s80 offen offset:928\n"
"buffer_load_ushort v140, v146, s[88:91], s81 offen offset:160\n"
"buffer_load_ushort v141, v146, s[88:91], s81 offen offset:416\n"
"buffer_load_ushort v142, v146, s[88:91], s81 offen offset:672\n"
"buffer_load_ushort v143, v146, s[88:91], s81 offen offset:928\n"
"s_waitcnt vmcnt(0)\n"
"v_lshl_or_b32 v128, v129, 16, v128\n"
"v_lshl_or_b32 v129, v131, 16, v130\n"
"v_lshl_or_b32 v130, v133, 16, v132\n"
"v_lshl_or_b32 v131, v135, 16, v134\n"
"v_lshl_or_b32 v136, v137, 16, v136\n"
"v_lshl_or_b32 v137, v139, 16, v138\n"
"v_lshl_or_b32 v138, v141, 16, v140\n"
"v_lshl_or_b32 v139, v143, 16, v142\n"
"v_mfma_f32_16x16x32_bf16 v[16:19], v[96:99], v[128:131], v[16:19]\n"
"s_nop 0\n"
"v_mfma_f32_16x16x32_bf16 v[20:23], v[96:99], v[136:139], v[20:23]\n"
"v_mfma_f32_16x16x32_bf16 v[48:51], v[104:107], v[128:131], v[48:51]\n"
"v_mfma_f32_16x16x32_bf16 v[52:55], v[104:107], v[136:139], v[52:55]\n"
"v_mfma_f32_16x16x32_bf16 v[80:83], v[112:115], v[128:131], v[80:83]\n"
"v_mfma_f32_16x16x32_bf16 v[84:87], v[112:115], v[136:139], v[84:87]\n"
"s_cmp_lg_u32 s83, s43\n"
"s_cbranch_scc1 LSK3B2_%=\n"
"v_mfma_f32_16x16x32_bf16 a[224:227], v[120:123], v[128:131], a[224:227]\n"
"v_mfma_f32_16x16x32_bf16 a[228:231], v[120:123], v[136:139], a[228:231]\n"
"LSK3B2_%=:\n"
"s_nop 3\n"
"buffer_load_ushort v128, v147, s[88:91], s80 offen offset:128\n"
"buffer_load_ushort v129, v147, s[88:91], s80 offen offset:384\n"
"buffer_load_ushort v130, v147, s[88:91], s80 offen offset:640\n"
"buffer_load_ushort v131, v147, s[88:91], s80 offen offset:896\n"
"buffer_load_ushort v132, v147, s[88:91], s81 offen offset:128\n"
"buffer_load_ushort v133, v147, s[88:91], s81 offen offset:384\n"
"buffer_load_ushort v134, v147, s[88:91], s81 offen offset:640\n"
"buffer_load_ushort v135, v147, s[88:91], s81 offen offset:896\n"
"buffer_load_ushort v136, v147, s[88:91], s80 offen offset:160\n"
"buffer_load_ushort v137, v147, s[88:91], s80 offen offset:416\n"
"buffer_load_ushort v138, v147, s[88:91], s80 offen offset:672\n"
"buffer_load_ushort v139, v147, s[88:91], s80 offen offset:928\n"
"buffer_load_ushort v140, v147, s[88:91], s81 offen offset:160\n"
"buffer_load_ushort v141, v147, s[88:91], s81 offen offset:416\n"
"buffer_load_ushort v142, v147, s[88:91], s81 offen offset:672\n"
"buffer_load_ushort v143, v147, s[88:91], s81 offen offset:928\n"
"s_waitcnt vmcnt(0)\n"
"v_lshl_or_b32 v128, v129, 16, v128\n"
"v_lshl_or_b32 v129, v131, 16, v130\n"
"v_lshl_or_b32 v130, v133, 16, v132\n"
"v_lshl_or_b32 v131, v135, 16, v134\n"
"v_lshl_or_b32 v136, v137, 16, v136\n"
"v_lshl_or_b32 v137, v139, 16, v138\n"
"v_lshl_or_b32 v138, v141, 16, v140\n"
"v_lshl_or_b32 v139, v143, 16, v142\n"
"v_mfma_f32_16x16x32_bf16 v[24:27], v[96:99], v[128:131], v[24:27]\n"
"s_nop 0\n"
"v_mfma_f32_16x16x32_bf16 v[28:31], v[96:99], v[136:139], v[28:31]\n"
"v_mfma_f32_16x16x32_bf16 v[56:59], v[104:107], v[128:131], v[56:59]\n"
"v_mfma_f32_16x16x32_bf16 v[60:63], v[104:107], v[136:139], v[60:63]\n"
"v_mfma_f32_16x16x32_bf16 v[88:91], v[112:115], v[128:131], v[88:91]\n"
"v_mfma_f32_16x16x32_bf16 v[92:95], v[112:115], v[136:139], v[92:95]\n"
"s_cmp_lg_u32 s83, s43\n"
"s_cbranch_scc1 LSK3B3_%=\n"
"v_mfma_f32_16x16x32_bf16 a[232:235], v[120:123], v[128:131], a[232:235]\n"
"v_mfma_f32_16x16x32_bf16 a[236:239], v[120:123], v[136:139], a[236:239]\n"
"LSK3B3_%=:\n"
"s_nop 3\n"
"s_nop 9\n"
"s_add_u32 s80, s80, 8192\n"
"s_add_u32 s81, s81, 8192\n"
"s_add_u32 s82, s82, 1\n"
"s_cmp_lt_u32 s82, 32\n"
"s_cbranch_scc1 LSLOOP_%=\n"
"v_mov_b32 v154, v155\n"
"s_nop 1\n"
"v_permlane32_swap_b32 v155, v154\n"
"v_add_f32 v155, v155, v154\n"
"v_mov_b32 v154, v155\n"
"s_nop 1\n"
"v_permlane16_swap_b32 v155, v154\n"
"v_add_f32 v155, v155, v154\n"
"v_mov_b32 v196, v155\n"
"v_mov_b32 v158, v159\n"
"s_nop 1\n"
"v_permlane32_swap_b32 v159, v158\n"
"v_add_f32 v159, v159, v158\n"
"v_mov_b32 v158, v159\n"
"s_nop 1\n"
"v_permlane16_swap_b32 v159, v158\n"
"v_add_f32 v159, v159, v158\n"
"v_mov_b32 v198, v159\n"
"v_mov_b32 v162, v163\n"
"s_nop 1\n"
"v_permlane32_swap_b32 v163, v162\n"
"v_add_f32 v163, v163, v162\n"
"v_mov_b32 v162, v163\n"
"s_nop 1\n"
"v_permlane16_swap_b32 v163, v162\n"
"v_add_f32 v163, v163, v162\n"
"v_mov_b32 v200, v163\n"
"v_mov_b32 v166, v167\n"
"s_nop 1\n"
"v_permlane32_swap_b32 v167, v166\n"
"v_add_f32 v167, v167, v166\n"
"v_mov_b32 v166, v167\n"
"s_nop 1\n"
"v_permlane16_swap_b32 v167, v166\n"
"v_add_f32 v167, v167, v166\n"
"v_mov_b32 v202, v167\n"
"v_sub_f32 v192, 0, v164\n"
"v_mov_b32 v96, 0\n"
"v_mov_b32 v97, 0\n"
"v_mov_b32 v98, 0\n"
"v_mov_b32 v99, 0\n"
"v_mov_b32 v100, 0\n"
"v_mov_b32 v101, 0\n"
"v_mov_b32 v102, 0\n"
"v_mov_b32 v103, 0\n"
"v_mov_b32 v104, 0\n"
"v_mov_b32 v105, 0\n"
"v_mov_b32 v106, 0\n"
"v_mov_b32 v107, 0\n"
"v_mov_b32 v108, 0\n"
"v_mov_b32 v109, 0\n"
"v_mov_b32 v110, 0\n"
"v_mov_b32 v111, 0\n"
"v_mov_b32 v112, 0\n"
"v_mov_b32 v113, 0\n"
"v_mov_b32 v114, 0\n"
"v_mov_b32 v115, 0\n"
"v_mov_b32 v116, 0\n"
"v_mov_b32 v117, 0\n"
"v_mov_b32 v118, 0\n"
"v_mov_b32 v119, 0\n"
"v_mov_b32 v120, 0\n"
"v_mov_b32 v121, 0\n"
"v_mov_b32 v122, 0\n"
"v_mov_b32 v123, 0\n"
"v_mov_b32 v204, 0\n"
"v_mov_b32 v205, 0\n"
"s_nop 3\n"
"s_mov_b32 s68, 0\n"
"s_cmp_eq_u32 s47, 4\n"
"s_cbranch_scc1 LBACK0_%=\n"
"s_cmp_eq_u32 s47, 8\n"
"s_cbranch_scc1 LBACK1_%=\n"
"s_cmp_eq_u32 s47, 12\n"
"s_cbranch_scc1 LBACK2_%=\n"
"s_branch LBACK3_%=\n"
"LFIN_%=:\n"
    :: [kalo]"s"((unsigned)ka), [kahi]"s"((unsigned)(ka >> 32)), [wg]"s"(blockIdx.x), [wid]"s"(wid)
    : "v0","v1","v2","v3","v4","v5","v6","v7","v8","v9","v10","v11","v12","v13","v14","v15","v16","v17","v18","v19","v20","v21","v22","v23","v24","v25","v26","v27","v28","v29","v30","v31","v32","v33","v34","v35","v36","v37","v38","v39","v40","v41","v42","v43","v44","v45","v46","v47","v48","v49","v50","v51","v52","v53","v54","v55","v56","v57","v58","v59","v60","v61","v62","v63","v64","v65","v66","v67","v68","v69","v70","v71","v72","v73","v74","v75","v76","v77","v78","v79","v80","v81","v82","v83","v84","v85","v86","v87","v88","v89","v90","v91","v92","v93","v94","v95","v96","v97","v98","v99","v100","v101","v102","v103","v104","v105","v106","v107","v108","v109","v110","v111","v112","v113","v114","v115","v116","v117","v118","v119","v120","v121","v122","v123","v124","v125","v126","v127","v128","v129","v130","v131","v132","v133","v134","v135","v136","v137","v138","v139","v140","v141","v142","v143","v144","v145","v146","v147","v148","v149","v150","v151","v152","v153","v154","v155","v156","v157","v158","v159","v160","v161","v162","v163","v164","v165","v166","v167","v168","v169","v170","v171","v172","v173","v174","v175","v176","v177","v178","v179","v180","v181","v182","v183","v184","v185","v186","v187","v188","v189","v190","v191","v192","v193","v194","v195","v196","v197","v198","v199","v200","v201","v202","v203","v204","v205","v206","v207","v208","v209","v210","v211","v212","v213","v214","v215","v216","v217","v218","v219","v220","v221","v222","v223","v224","v225","v226","v227","v228","v229","v230","v231","v232","v233","v234","v235","v236","v237","v238","v239","v240","v241","v242","v243","v244","v245","v246","v247","v248","v249","v250","v251","v252","v253","v254","v255","a0","a1","a2","a3","a4","a5","a6","a7","a8","a9","a10","a11","a12","a13","a14","a15","a16","a17","a18","a19","a20","a21","a22","a23","a24","a25","a26","a27","a28","a29","a30","a31","a32","a33","a34","a35","a36","a37","a38","a39","a40","a41","a42","a43","a44","a45","a46","a47","a48","a49","a50","a51","a52","a53","a54","a55","a56","a57","a58","a59","a60","a61","a62","a63","a64","a65","a66","a67","a68","a69","a70","a71","a72","a73","a74","a75","a76","a77","a78","a79","a80","a81","a82","a83","a84","a85","a86","a87","a88","a89","a90","a91","a92","a93","a94","a95","a96","a97","a98","a99","a100","a101","a102","a103","a104","a105","a106","a107","a108","a109","a110","a111","a112","a113","a114","a115","a116","a117","a118","a119","a120","a121","a122","a123","a124","a125","a126","a127","a128","a129","a130","a131","a132","a133","a134","a135","a136","a137","a138","a139","a140","a141","a142","a143","a144","a145","a146","a147","a148","a149","a150","a151","a152","a153","a154","a155","a156","a157","a158","a159","a160","a161","a162","a163","a164","a165","a166","a167","a168","a169","a170","a171","a172","a173","a174","a175","a176","a177","a178","a179","a180","a181","a182","a183","a184","a185","a186","a187","a188","a189","a190","a191","a192","a193","a194","a195","a196","a197","a198","a199","a200","a201","a202","a203","a204","a205","a206","a207","a208","a209","a210","a211","a212","a213","a214","a215","a216","a217","a218","a219","a220","a221","a222","a223","a224","a225","a226","a227","a228","a229","a230","a231","a232","a233","a234","a235","a236","a237","a238","a239","a240","a241","a242","a243","a244","a245","a246","a247","a248","a249","a250","a251","a252","a253","a254","a255","s6","s7","s8","s9","s10","s11","s12","s13","s14","s15","s16","s17","s18","s19","s20","s21","s22","s23","s24","s25","s26","s27","s28","s29","s30","s31","s32","s33","s34","s35","s36","s37","s38","s39","s40","s41","s42","s43","s44","s45","s46","s47","s48","s49","s50","s51","s52","s53","s54","s55","s56","s57","s58","s59","s60","s61","s62","s63","s64","s65","s66","s67","s68","s69","s70","s71","s72","s73","s74","s75","s76","s77","s78","s79","s80","s81","s82","s83","s84","s85","s86","s87","s88","s89","s90","s91","s92","s93","s94","s95","s96","s97","s98","s99","s100","vcc","scc","m0","memory");
}
extern "C" void kernel_launch(void* const* d_in, const int* in_sizes, int n_in, void* d_out, int out_size, void* d_ws, size_t ws_size, hipStream_t stream) {
  char* xch = (char*)d_ws + ((ws_size - (size_t)270532608) & ~(size_t)0xfffff);
  (void)hipFuncSetAttribute((const void*)fused_attn, hipFuncAttributeMaxDynamicSharedMemorySize, 163840);
  hipLaunchKernelGGL(fused_attn, dim3(256), dim3(256), 163840, stream,
                     (const char*)d_in[0], (const char*)d_in[1], (const char*)d_in[2], (const char*)d_in[3], (const char*)d_in[4],
                     (const char*)d_in[5], (const char*)d_in[6], (const char*)d_in[7], (const char*)d_in[8], (const char*)d_in[9], xch, (char*)d_out, (char*)d_ws);
}
```

```cpp
#include <hip/hip_runtime.h>
#include <hip/hip_bf16.h>
#include <stdint.h>

__global__ __launch_bounds__(256, 1)
void fused_attn(const char* xA, const char* kA, const char* vA, const char* wA, const char* uA,
                const char* xB, const char* kB, const char* vB, const char* wB, const char* uB, char* xch, char* out, char* flags) {
  extern __shared__ __attribute__((aligned(16))) char lds[];
  const unsigned wid = __builtin_amdgcn_readfirstlane(threadIdx.x >> 6);
  const unsigned long long ka = (unsigned long long)__builtin_amdgcn_kernarg_segment_ptr();
  asm volatile(
"s_memtime s[64:65]\n"
"s_memrealtime s[66:67]\n"
"s_bfe_u32 s10, %[wg], 0x10002\n"
"s_mul_i32 s40, s10, 40\n"
"s_add_u32 s40, s40, %[kalo]\n"
"s_addc_u32 s41, %[kahi], 0\n"
"s_mov_b32 s78, %[kalo]\n"
"s_mov_b32 s79, %[kahi]\n"
"s_load_dwordx2 s[24:25], s[40:41], 0\n"
"s_load_dwordx2 s[28:29], s[40:41], 24\n"
"s_load_dwordx2 s[6:7], s[78:79], 0x60\n"
"s_load_dwordx2 s[16:17], s[40:41], 8\n"
"s_load_dwordx2 s[20:21], s[40:41], 16\n"
"s_load_dwordx2 s[8:9], s[40:41], 32\n"
"s_load_dwordx4 s[12:15], s[78:79], 0x50\n"
"s_mov_b32 s42, %[wid]\n"
"s_and_b32 s11, %[wg], 1\n"
"s_lshl_b32 s11, s11, 3\n"
"s_lshr_b32 s60, %[wg], 5\n"
"s_add_u32 s11, s11, s60\n"
"s_bfe_u32 s61, %[wg], 0x10001\n"
"s_bfe_u32 s60, %[wg], 0x20003\n"
"s_lshl2_add_u32 s61, s61, s60\n"
"s_getpc_b64 s[80:81]\n"
"LCP_%=:\n"
"s_mov_b32 s82, LEND_%=-LCP_%=\n"
"s_mov_b32 s83, 0x20000\n"
"v_mbcnt_lo_u32_b32 v2, -1, 0\n"
"v_lshlrev_b32 v2, 7, v2\n"
"s_lshr_b32 s60, %[wg], 3\n"
"s_lshl2_add_u32 s60, s60, s42\n"
"s_mul_i32 s60, s60, 640\n"
"s_mov_b64 exec, 31\n"
"buffer_load_dword v3, v2, s[80:83], s60 offen\n"
"s_mov_b64 exec, -1\n"
"s_mov_b32 s19, 0x20000\n"
"s_mov_b32 s23, 0x20000\n"
"s_mov_b32 s27, 0x20000\n"
"s_mov_b32 s31, 0x20000\n"
"s_mov_b32 s35, 0x20000\n"
"s_mov_b32 s39, 0x20000\n"
"s_mov_b32 s26, 286720\n"
"s_lshl_b32 s44, s42, 12\n"
"s_mov_b32 s50, s44\n"
"s_mul_i32 s51, s42, 7168\n"
"s_add_u32 s51, s51, 0x10000\n"
"s_mov_b32 s74, 0x8000\n"
"s_mov_b32 s58, 524288\n"
"s_mov_b32 s47, 0\n"
"s_add_u32 s60, s11, s61\n"
"s_and_b32 s60, s60, 0\n"
"s_mul_i32 s60, s60, 3\n"
"s_lshl_b32 s49, s60, 7\n"
"s_lshl_b32 s48, s60, 19\n"
"v_mbcnt_lo_u32_b32 v4, -1, 0\n"
"v_mbcnt_hi_u32_b32 v4, -1, v4\n"
"v_and_b32 v5, 15, v4\n"
"v_lshrrev_b32 v6, 4, v4\n"
"v_lshrrev_b32 v7, 3, v4\n"
"v_and_b32 v8, 7, v4\n"
"v_lshlrev_b32 v9, 1, v6\n"
"v_xor_b32 v9, v9, v8\n"
"v_lshlrev_b32 v9, 4, v9\n"
"s_mul_i32 s60, s42, 71680\n"
"v_mul_u32_u24 v10, 1280, v7\n"
"v_add3_u32 v248, v10, v9, s60\n"
"v_add_u32 v249, 9216, v248\n"
"v_add_u32 v254, 18432, v248\n"
"v_add_u32 v255, 27648, v248\n"
"v_and_b32 v10, 2, v7\n"
"v_xor_b32 v240, v10, v8\n"
"v_lshlrev_b32 v240, 4, v240\n"
"v_bfe_u32 v10, v7, 0, 1\n"
"v_lshl_add_u32 v240, v10, 8, v240\n"
"v_bfe_u32 v10, v7, 1, 1\n"
"v_lshl_add_u32 v240, v10, 9, v240\n"
"v_bfe_u32 v10, v7, 2, 1\n"
"v_lshl_add_u32 v240, v10, 7, v240\n"
"s_and_b32 s60, s42, 1\n"
"v_lshl_add_u32 v240, s60, 15, v240\n"
"s_lshr_b32 s60, s42, 1\n"
"v_lshl_add_u32 v240, s60, 16, v240\n"
"v_and_b32 v10, 1, v6\n"
"v_xor_b32 v10, v10, v8\n"
"v_lshlrev_b32 v10, 4, v10\n"
"v_lshl_add_u32 v10, v7, 13, v10\n"
"s_lshl_b32 s60, s42, 17\n"
"v_add_u32 v10, s60, v10\n"
"v_add_u32 v224, 1024, v10\n"
"v_add_u32 v225, 128, v10\n"
"v_xor_b32 v10, 64, v10\n"
"v_add_u32 v226, 64512, v10\n"
"v_add_u32 v227, 63616, v10\n"
"s_mul_i32 s40, s11, 286720\n"
"s_lshl_b32 s41, s61, 10\n"
"s_sub_u32 s30, 5243904, s41\n"
"s_sub_u32 s41, s41, 1024\n"
"s_ashr_i32 s60, s41, 31\n"
"s_lshl4_add_u32 s92, s10, s11\n"
"s_lshl_b32 s93, s92, 9\n"
"v_mov_b32 v0, 0\n"
"s_waitcnt lgkmcnt(0)\n"
"s_add_u32 s24, s24, s40\n"
"s_addc_u32 s25, s25, 0\n"
"s_and_b32 s25, s25, 0xffff\n"
"s_add_u32 s28, s28, s41\n"
"s_addc_u32 s29, s29, s60\n"
"s_and_b32 s29, s29, 0xffff\n"
"s_add_u32 s6, s6, s93\n"
"s_addc_u32 s7, s7, 0\n"
"buffer_load_dword v12, v248, s[24:27], s49 offen\n"
"buffer_load_dword v13, v224, s[28:31], s48 offen\n"
"global_load_dword v1, v0, s[6:7] offset:128 sc1\n"
"s_mov_b32 m0, s51\n"
"s_nop 0\n"
"buffer_load_dwordx4 v248, s[24:27], s49 offen lds\n"
"buffer_load_dwordx4 v249, s[24:27], s49 offen offset:1024 lds\n"
"buffer_load_dwordx4 v254, s[24:27], s49 offen offset:2048 lds\n"
"buffer_load_dwordx4 v255, s[24:27], s49 offen offset:3072 lds\n"
"s_add_u32 m0, s51, 4096\n"
"s_add_u32 s40, s49, 40960\n"
"buffer_load_dwordx4 v248, s[24:27], s40 offen lds\n"
"buffer_load_dwordx4 v249, s[24:27], s40 offen offset:1024 lds\n"
"buffer_load_dwordx4 v254, s[24:27], s40 offen offset:2048 lds\n"
"s_add_u32 s49, s49, 128\n"
"s_sub_u32 s41, s49, 1280\n"
"s_min_u32 s49, s49, s41\n"
"s_xor_b32 s51, s51, s74\n"
"s_mov_b32 m0, s50\n"
"s_nop 0\n"
"buffer_load_dwordx4 v224, s[28:31], s48 offen lds\n"
"buffer_load_dwordx4 v225, s[28:31], s48 offen offset:1024 lds\n"
"buffer_load_dwordx4 v226, s[28:31], s48 offen offset:2048 lds\n"
"buffer_load_dwordx4 v227, s[28:31], s48 offen offset:3072 lds\n"
"s_xor_b32 s50, s50, s74\n"
"s_lshl2_add_u32 s63, s11, s42\n"
"s_mul_i32 s63, s63, 10240\n"
"s_and_b32 s78, s42, 1\n"
"s_lshl_b32 s43, s78, 6\n"
"s_mov_b32 s77, 0x80000000\n"
"s_mov_b32 s75, 0x10000\n"
"s_mov_b32 s73, 0xffff0000\n"
"s_mov_b32 s46, 0x3e0293ee\n"
"s_mov_b32 s45, 0x63800000\n"
"s_mov_b32 s62, 9\n"
"v_lshl_add_u32 v242, v4, 4, s44\n"
"s_lshr_b32 s79, s42, 1\n"
"s_lshl4_add_u32 s76, s79, s78\n"
"s_lshl_b32 s76, s76, 10\n"
"s_add_u32 s76, s76, 0x20000\n"
"s_sub_u32 s76, s76, s44\n"
"v_lshlrev_b32 v9, 6, v6\n"
"v_xor_b32 v9, v9, v242\n"
"v_add_u32 v243, s76, v9\n"
"v_and_b32 v9, 6, v5\n"
"s_xor_b32 m0, s50, 0xc000\n"
"s_add_u32 s40, s48, 256\n"
"buffer_load_dwordx4 v224, s[28:31], s40 offen lds\n"
"v_xor_b32 v9, v9, v6\n"
"v_lshlrev_b32 v9, 4, v9\n"
"v_lshl_add_u32 v10, v5, 7, v9\n"
"s_mul_i32 s79, s42, 6144\n"
"s_add_u32 s79, s79, 0x10000\n"
"v_add_u32 v246, s79, v10\n"
"v_xor_b32 v247, 64, v246\n"
"s_lshr_b32 s79, s42, 1\n"
"s_lshl_b32 s79, s79, 11\n"
"buffer_load_dwordx4 v225, s[28:31], s40 offen offset:1024 lds\n"
"s_add_u32 s79, s79, 90112\n"
"v_add_u32 v252, s79, v10\n"
"v_xor_b32 v253, 64, v252\n"
"s_xor_b32 s79, s42, 1\n"
"s_mul_i32 s79, s79, 6144\n"
"s_add_u32 s79, s79, 0x10000\n"
"v_add_u32 v250, s79, v10\n"
"v_xor_b32 v251, 64, v250\n"
"s_mul_i32 s79, s78, 8256\n"
"buffer_load_dwordx4 v226, s[28:31], s40 offen offset:2048 lds\n"
"v_lshlrev_b32 v9, 10, v6\n"
"v_bfe_u32 v10, v4, 2, 2\n"
"v_lshl_add_u32 v9, v10, 7, v9\n"
"v_and_b32 v10, 3, v4\n"
"v_lshl_add_u32 v9, v10, 3, v9\n"
"v_bfe_u32 v10, v4, 3, 2\n"
"v_lshl_add_u32 v9, v10, 5, v9\n"
"s_add_u32 s79, s79, 0x10000\n"
"v_xor_b32 v232, s79, v9\n"
"buffer_load_dwordx4 v227, s[28:31], s40 offen offset:3072 lds\n"
"s_cmp_eq_u32 s47, 8\n"
"s_cselect_b32 s41, s52, s58\n"
"s_add_u32 s48, s48, s41\n"
"s_sub_u32 s41, s48, 5242880\n"
"s_min_u32 s48, s48, s41\n"
"v_xor_b32 v233, 32, v232\n"
"v_xor_b32 v234, 64, v232\n"
"v_xor_b32 v235, 96, v232\n"
"v_xor_b32 v236, 0x2000, v232\n"
"v_xor_b32 v237, 0x2000, v233\n"
"v_xor_b32 v238, 0x2000, v234\n"
"v_xor_b32 v239, 0x2000, v235\n"
"v_lshlrev_b32 v9, 11, v6\n"
"v_bfe_u32 v10, v4, 2, 2\n"
"s_mov_b32 m0, s50\n"
"s_nop 0\n"
"buffer_load_dwordx4 v224, s[28:31], s48 offen lds\n"
"v_lshl_add_u32 v9, v10, 7, v9\n"
"v_and_b32 v10, 1, v4\n"
"v_lshl_add_u32 v9, v10, 3, v9\n"
"v_bfe_u32 v10, v4, 1, 1\n"
"v_lshl_add_u32 v9, v10, 5, v9\n"
"v_bfe_u32 v10, v4, 3, 1\n"
"v_lshl_add_u32 v9, v10, 4, v9\n"
"v_bfe_u32 v10, v4, 4, 1\n"
"v_lshl_add_u32 v9, v10, 6, v9\n"
"buffer_load_dwordx4 v225, s[28:31], s48 offen offset:1024 lds\n"
"v_xor_b32 v244, s43, v9\n"
"v_xor_b32 v245, 16, v244\n"
"s_lshl3_add_u32 s79, s11, s61\n"
"s_lshl_b32 s79, s79, 20\n"
"s_sub_u32 s79, s79, 262144\n"
"s_ashr_i32 s60, s79, 31\n"
"s_add_u32 s16, s16, s79\n"
"s_addc_u32 s17, s17, s60\n"
"s_and_b32 s17, s17, 0xffff\n"
"buffer_load_dwordx4 v226, s[28:31], s48 offen offset:2048 lds\n"
"s_mov_b32 s18, 1310720\n"
"s_mov_b64 s[84:85], s[16:17]\n"
"s_mov_b64 s[86:87], s[18:19]\n"
"s_add_u32 s20, s20, s79\n"
"s_addc_u32 s21, s21, s60\n"
"s_and_b32 s21, s21, 0xffff\n"
"s_mov_b32 s22, 1310720\n"
"s_mov_b64 s[88:89], s[20:21]\n"
"s_mov_b64 s[90:91], s[22:23]\n"
"buffer_load_dwordx4 v227, s[28:31], s48 offen offset:3072 lds\n"
"s_xor_b32 s50, s50, s74\n"
"s_mov_b32 s57, 122880\n"
"s_bfe_u32 s55, %[wg], 65541\n"
"s_mul_i32 s55, s55, 1024\n"
"s_xor_b32 s57, s57, s55\n"
"s_bfe_u32 s55, %[wg], 65542\n"
"s_mul_i32 s55, s55, 2048\n"
"s_xor_b32 s57, s57, s55\n"
"s_mov_b32 s55, 385024\n"
"s_xor_b32 s56, s55, s57\n"
"s_mov_b32 m0, s51\n"
"s_nop 0\n"
"buffer_load_dwordx4 v248, s[24:27], s49 offen lds\n"
"s_mov_b32 s59, 1024\n"
"s_mov_b32 s54, 21504\n"
"s_add_u32 s52, s11, s61\n"
"s_and_b32 s52, s52, 0\n"
"s_mul_i32 s52, s52, 3\n"
"s_add_u32 s52, s52, 1\n"
"s_lshl_b32 s53, s52, 7\n"
"s_lshl_b32 s52, s52, 19\n"
"s_add_u32 s52, s52, 512\n"
"buffer_load_dwordx4 v249, s[24:27], s49 offen offset:1024 lds\n"
"s_mul_i32 s79, s61, 655360\n"
"s_add_u32 s8, s8, s79\n"
"s_addc_u32 s9, s9, 0\n"
"s_and_b32 s9, s9, 0xffff\n"
"s_add_u32 s12, s12, 0x1fffff\n"
"s_addc_u32 s13, s13, 0\n"
"s_and_b32 s12, s12, 0xffe00000\n"
"s_mul_i32 s96, s42, 3\n"
"s_and_b32 s96, s96, 5\n"
"buffer_load_dwordx4 v254, s[24:27], s49 offen offset:2048 lds\n"
"s_cmp_eq_u32 s42, 3\n"
"s_cselect_b32 s96, 5, s96\n"
"s_lshl_b32 s79, s61, 8\n"
"s_add_u32 s96, s96, s79\n"
"s_add_u32 s96, s96, 160\n"
"s_lshl_b32 s96, s96, 12\n"
"s_mul_i32 s79, s92, 8388608\n"
"s_add_u32 s32, s12, s79\n"
"s_addc_u32 s33, s13, 0\n"
"buffer_load_dwordx4 v255, s[24:27], s49 offen offset:3072 lds\n"
"s_and_b32 s33, s33, 0xffff\n"
"s_mov_b32 s34, 8388608\n"
"s_mul_i32 s79, s11, 573440\n"
"s_mul_i32 s60, s10, 1280\n"
"s_sub_u32 s38, 573440, s60\n"
"s_add_u32 s79, s79, s60\n"
"s_add_u32 s36, s14, s79\n"
"s_addc_u32 s37, s15, 0\n"
"s_and_b32 s37, s37, 0xffff\n"
"s_add_u32 m0, s51, 4096\n"
"s_add_u32 s40, s49, 40960\n"
"buffer_load_dwordx4 v248, s[24:27], s40 offen lds\n"
"s_lshl2_add_u32 s93, s61, s42\n"
"s_add_u32 s95, s61, 1\n"
"s_and_b32 s95, s95, 7\n"
"s_lshl_b32 s95, s95, 7\n"
"s_add_u32 s100, s61, 2\n"
"s_and_b32 s100, s100, 7\n"
"s_lshl_b32 s100, s100, 7\n"
"s_lshl_b32 s60, s61, 7\n"
"s_add_u32 s60, s60, s43\n"
"buffer_load_dwordx4 v249, s[24:27], s40 offen offset:1024 lds\n"
"s_mul_i32 s97, s42, 122880\n"
"s_add_u32 s97, s97, s60\n"
"s_lshr_b32 s98, s42, 1\n"
"s_add_u32 s98, s98, 12\n"
"s_mul_i32 s98, s98, 40960\n"
"s_add_u32 s98, s98, s60\n"
"s_mul_i32 s60, s93, 37\n"
"s_lshr_b32 s60, s60, 8\n"
"s_mul_i32 s79, s60, 7\n"
"buffer_load_dwordx4 v254, s[24:27], s40 offen offset:2048 lds\n"
"s_add_u32 s49, s49, 128\n"
"s_sub_u32 s41, s49, 1280\n"
"s_min_u32 s49, s49, s41\n"
"s_xor_b32 s51, s51, s74\n"
"s_sub_u32 s79, s93, s79\n"
"s_lshl_b32 s94, s60, 4\n"
"s_add_u32 s94, s94, s79\n"
"s_lshl_b32 s94, s94, 10\n"
"s_add_u32 s94, s94, 524288\n"
"s_mul_i32 s99, s60, 3\n"
"s_lshr_b32 s78, s79, 1\n"
"s_add_u32 s99, s99, s78\n"
"s_lshr_b32 s78, s60, 1\n"
"s_add_u32 s78, s78, 12\n"
"s_cmp_eq_u32 s79, 6\n"
"s_cselect_b32 s99, s78, s99\n"
"s_mul_i32 s99, s99, 40960\n"
"s_xor_b32 s79, s79, s60\n"
"s_and_b32 s79, s79, 1\n"
"s_lshl_b32 s79, s79, 6\n"
"s_add_u32 s99, s99, s79\n"
"s_add_u32 s99, s99, 1024\n"
"s_cmp_eq_u32 s61, 7\n"
"s_cselect_b32 s99, 0x40000000, s99\n"
"s_cselect_b32 s94, 0x40000000, s94\n"
"s_waitcnt vmcnt(27)\n"
"v_and_b32 v12, 0x7fff0000, v12\n"
"v_and_b32 v13, 0x7fff0000, v13\n"
"s_nop 0\n"
"v_max_u32_dpp v12, v12, v12 quad_perm:[1,0,3,2] row_mask:0xf bank_mask:0xf\n"
"v_max_u32_dpp v13, v13, v13 quad_perm:[1,0,3,2] row_mask:0xf bank_mask:0xf\n"
"s_nop 0\n"
"v_max_u32_dpp v12, v12, v12 quad_perm:[2,3,0,1] row_mask:0xf bank_mask:0xf\n"
"v_max_u32_dpp v13, v13, v13 quad_perm:[2,3,0,1] row_mask:0xf bank_mask:0xf\n"
"s_memtime s[80:81]\n"
"s_memrealtime s[82:83]\n"
"s_nop 0\n"
"v_max_u32_dpp v12, v12, v12 row_half_mirror row_mask:0xf bank_mask:0xf\n"
"v_max_u32_dpp v13, v13, v13 row_half_mirror row_mask:0xf bank_mask:0xf\n"
"s_nop 0\n"
"v_min_u32_dpp v12, v12, v12 row_mirror row_mask:0xf bank_mask:0xf\n"
"v_min_u32_dpp v13, v13, v13 row_mirror row_mask:0xf bank_mask:0xf\n"
"s_nop 0\n"
"v_min_u32_dpp v12, v12, v12 row_bcast:15 row_mask:0xa bank_mask:0xf\n"
"v_min_u32_dpp v13, v13, v13 row_bcast:15 row_mask:0xa bank_mask:0xf\n"
"s_nop 0\n"
"v_min_u32_dpp v12, v12, v12 row_bcast:31 row_mask:0xc bank_mask:0xf\n"
"v_min_u32_dpp v13, v13, v13 row_bcast:31 row_mask:0xc bank_mask:0xf\n"
"s_nop 0\n"
"v_mul_f32 v12, v12, v13\n"
"v_mul_f32 v12, 0x43c80000, v12\n"
"v_min_f32 v12, 0x71800000, v12\n"
"v_mul_f32 v13, s46, v12\n"
"s_nop 0\n"
"v_readlane_b32 s70, v12, 63\n"
"v_readlane_b32 s71, v12, 63\n"
"v_readlane_b32 s69, v13, 63\n"
"s_nop 1\n"
"v_mov_b64 v[228:229], s[70:71]\n"
"v_mov_b64 v[230:231], s[70:71]\n"
"s_waitcnt vmcnt(26)\n"
"v_readfirstlane_b32 s92, v1\n"
"v_mov_b64 v[0:1], s[70:71]\n"
"v_mov_b64 v[2:3], s[70:71]\n"
"s_mov_b32 s70, s46\n"
"s_xor_b32 s71, s69, s77\n"
"s_waitcnt lgkmcnt(0)\n"
"s_sub_u32 s80, s80, s64\n"
"s_sub_u32 s82, s82, s66\n"
"s_mul_i32 s80, s80, 10\n"
"s_mul_i32 s82, s82, 211\n"
"s_cmp_gt_u32 s80, s82\n"
"s_cselect_b32 s64, 1, 0\n"
"s_cselect_b32 s68, 655360, 0\n"
"s_cmp_eq_u32 s42, 0\n"
"s_cselect_b32 s64, s64, 0\n"
"s_mov_b32 s65, 0\n"
"s_add_u32 s66, s66, 2800\n"
"s_mov_b32 s67, s66\n"
"s_mov_b32 s72, 118\n"
"s_mov_b64 vcc, s[64:65]\n"
"s_sub_u32 s63, s63, s44\n"
"s_add_u32 s80, s8, s63\n"
"s_addc_u32 s81, s9, 0\n"
"s_add_u32 s82, s44, 704\n"
"s_cmp_lg_u32 s68, 0\n"
"s_cselect_b32 s82, s82, 0\n"
"s_mov_b32 s83, 0x20000\n"
"s_nop 2\n"
"s_mul_i32 s60, s92, 0x9e3779b1\n"
"s_add_u32 s60, s60, 0x7f4a7c15\n"
"s_waitcnt vmcnt(15)\n"
"s_barrier\n"
"ds_read_b128 a[136:139], v246 offset:0\n"
"ds_read_b128 a[140:143], v247 offset:0\n"
"ds_read_b64_tr_b16 a[192:193], v244 offset:0\n"
"ds_read_b64_tr_b16 a[194:195], v244 offset:512\n"
"ds_read_b64_tr_b16 a[196:197], v244 offset:8192\n"
"ds_read_b64_tr_b16 a[198:199], v244 offset:8704\n"
"ds_read_b64_tr_b16 a[200:201], v245 offset:0\n"
"ds_read_b64_tr_b16 a[202:203], v245 offset:512\n"
"ds_read_b64_tr_b16 a[204:205], v245 offset:8192\n"
"ds_read_b64_tr_b16 a[206:207], v245 offset:8704\n"
"ds_read_b64_tr_b16 a[208:209], v244 offset:1024\n"
"ds_read_b64_tr_b16 a[210:211], v244 offset:1536\n"
"ds_read_b64_tr_b16 a[212:213], v244 offset:9216\n"
"ds_read_b64_tr_b16 a[214:215], v244 offset:9728\n"
"s_waitcnt lgkmcnt(10)\n"
"v_mfma_f32_16x16x32_bf16 v[112:115], a[192:195], a[136:139], v[228:231]\n"
"ds_read_b64_tr_b16 a[216:217], v245 offset:1024\n"
"ds_read_b64_tr_b16 a[218:219], v245 offset:1536\n"
"s_waitcnt lgkmcnt(10)\n"
"v_mfma_f32_16x16x32_bf16 v[112:115], a[196:199], a[140:143], v[112:115]\n"
"ds_read_b64_tr_b16 a[220:221], v245 offset:9216\n"
"ds_read_b64_tr_b16 a[222:223], v245 offset:9728\n"
"s_waitcnt lgkmcnt(10)\n"
"v_mfma_f32_16x16x32_bf16 v[116:119], a[200:203], a[136:139], v[228:231]\n"
"s_waitcnt lgkmcnt(8)\n"
"ds_read_b128 a[144:147], v246 offset:2048\n"
"v_mfma_f32_16x16x32_bf16 v[116:119], a[204:207], a[140:143], v[116:119]\n"
"s_waitcnt lgkmcnt(7)\n"
"ds_read_b128 a[148:151], v247 offset:2048\n"
"v_mfma_f32_16x16x32_bf16 v[120:123], a[208:211], a[136:139], v[228:231]\n"
"s_waitcnt lgkmcnt(6)\n"
"ds_read_b128 a[152:155], v246 offset:4096\n"
"v_mfma_f32_16x16x32_bf16 v[120:123], a[212:215], a[140:143], v[120:123]\n"
"s_waitcnt lgkmcnt(5)\n"
"ds_read_b128 a[156:159], v247 offset:4096\n"
"v_mfma_f32_16x16x32_bf16 v[124:127], a[216:219], a[136:139], v[228:231]\n"
"s_waitcnt lgkmcnt(4)\n"
"ds_read_b128 a[160:163], v252 offset:0\n"
"v_mfma_f32_16x16x32_bf16 v[124:127], a[220:223], a[140:143], v[124:127]\n"
"s_waitcnt lgkmcnt(4)\n"
"ds_read_b128 a[164:167], v253 offset:0\n"
"v_mfma_f32_16x16x32_bf16 v[140:143], a[216:219], a[144:147], v[228:231]\n"
"s_waitcnt lgkmcnt(4)\n"
"ds_read_b128 a[168:171], v250 offset:0\n"
"v_mfma_f32_16x16x32_bf16 v[140:143], a[220:223], a[148:151], v[140:143]\n"
"ds_read_b128 a[172:175], v251 offset:0\n"
"v_mfma_f32_16x16x32_bf16 v[136:139], a[208:211], a[144:147], v[228:231]\n"
"s_waitcnt vmcnt(11)\n"
"ds_read_b128 a[176:179], v250 offset:2048\n"
"v_mfma_f32_16x16x32_bf16 v[136:139], a[212:215], a[148:151], v[136:139]\n"
"s_barrier\n"
"v_mfma_f32_16x16x32_bf16 v[132:135], a[200:203], a[144:147], v[228:231]\n"
"s_add_u32 s40, s48, 256\n"
"s_xor_b32 m0, s50, 0xc000\n"
"ds_read_b128 a[180:183], v251 offset:2048\n"
"v_mfma_f32_16x16x32_bf16 v[132:135], a[204:207], a[148:151], v[132:135]\n"
"buffer_load_dwordx4 v224, s[28:31], s40 offen lds\n"
"v_mfma_f32_16x16x32_bf16 v[128:131], a[192:195], a[144:147], v[228:231]\n"
"ds_read_b128 a[184:187], v250 offset:4096\n"
"v_mfma_f32_16x16x32_bf16 v[128:131], a[196:199], a[148:151], v[128:131]\n"
"s_waitcnt lgkmcnt(5)\n"
"ds_read_b128 a[188:191], v251 offset:4096\n"
"v_mfma_f32_16x16x32_bf16 v[144:147], a[192:195], a[152:155], v[228:231]\n"
"ds_read_b64_tr_b16 a[224:225], v244 offset:16384\n"
"v_mfma_f32_16x16x32_bf16 v[144:147], a[196:199], a[156:159], v[144:147]\n"
"ds_read_b64_tr_b16 a[226:227], v244 offset:16896\n"
"v_mfma_f32_16x16x32_bf16 v[148:151], a[200:203], a[152:155], v[228:231]\n"
"ds_read_b64_tr_b16 a[228:229], v244 offset:24576\n"
"v_mfma_f32_16x16x32_bf16 v[148:151], a[204:207], a[156:159], v[148:151]\n"
"buffer_load_dwordx4 v225, s[28:31], s40 offen offset:1024 lds\n"
"v_mfma_f32_16x16x32_bf16 v[152:155], a[208:211], a[152:155], v[228:231]\n"
"ds_read_b64_tr_b16 a[230:231], v244 offset:25088\n"
"v_mfma_f32_16x16x32_bf16 v[152:155], a[212:215], a[156:159], v[152:155]\n"
"ds_read_b64_tr_b16 a[232:233], v245 offset:16384\n"
"v_mfma_f32_16x16x32_bf16 v[156:159], a[216:219], a[152:155], v[228:231]\n"
"ds_read_b64_tr_b16 a[234:235], v245 offset:16896\n"
"v_mfma_f32_16x16x32_bf16 v[156:159], a[220:223], a[156:159], v[156:159]\n"
"ds_read_b64_tr_b16 a[236:237], v245 offset:24576\n"
"v_mfma_f32_16x16x32_bf16 v[172:175], a[216:219], a[160:163], v[228:231]\n"
"ds_read_b64_tr_b16 a[238:239], v245 offset:25088\n"
"v_mfma_f32_16x16x32_bf16 v[172:175], a[220:223], a[164:167], v[172:175]\n"
"buffer_load_dwordx4 v226, s[28:31], s40 offen offset:2048 lds\n"
"v_mfma_f32_16x16x32_bf16 v[168:171], a[208:211], a[160:163], v[228:231]\n"
"ds_read_b64_tr_b16 a[240:241], v244 offset:17408\n"
"v_mfma_f32_16x16x32_bf16 v[168:171], a[212:215], a[164:167], v[168:171]\n"
"ds_read_b64_tr_b16 a[242:243], v244 offset:17920\n"
"v_mfma_f32_16x16x32_bf16 v[164:167], a[200:203], a[160:163], v[228:231]\n"
"ds_read_b64_tr_b16 a[244:245], v244 offset:25600\n"
"v_mfma_f32_16x16x32_bf16 v[164:167], a[204:207], a[164:167], v[164:167]\n"
"buffer_load_dwordx4 v227, s[28:31], s40 offen offset:3072 lds\n"
"v_mfma_f32_16x16x32_bf16 v[160:163], a[192:195], a[160:163], v[228:231]\n"
"s_cmp_eq_u32 s47, 8\n"
"ds_read_b64_tr_b16 a[246:247], v244 offset:26112\n"
"v_xor_b32 v244, s74, v244\n"
"v_mfma_f32_16x16x32_bf16 v[160:163], a[196:199], a[164:167], v[160:163]\n"
"s_waitcnt lgkmcnt(5)\n"
"s_cselect_b32 s41, s52, s58\n"
"ds_read_b64_tr_b16 a[248:249], v245 offset:17408\n"
"v_xor_b32 v246, s74, v246\n"
"v_mfma_f32_16x16x32_bf16 v[176:179], a[192:195], a[168:171], v[228:231]\n"
"s_add_u32 s48, s48, s41\n"
"ds_read_b64_tr_b16 a[250:251], v245 offset:17920\n"
"v_xor_b32 v247, s74, v247\n"
"v_mfma_f32_16x16x32_bf16 v[176:179], a[196:199], a[172:175], v[176:179]\n"
"s_sub_u32 s41, s48, 5242880\n"
"ds_read_b64_tr_b16 a[252:253], v245 offset:25600\n"
"v_xor_b32 v252, s74, v252\n"
"v_mfma_f32_16x16x32_bf16 v[180:183], a[200:203], a[168:171], v[228:231]\n"
"s_min_u32 s48, s48, s41\n"
"ds_read_b64_tr_b16 a[254:255], v245 offset:26112\n"
"v_xor_b32 v245, s74, v245\n"
"v_mfma_f32_16x16x32_bf16 v[180:183], a[204:207], a[172:175], v[180:183]\n"
"v_xor_b32 v253, s74, v253\n"
"v_xor_b32 v250, s74, v250\n"
"v_mfma_f32_16x16x32_bf16 v[184:187], a[208:211], a[168:171], v[228:231]\n"
"s_mov_b32 m0, s50\n"
"v_xor_b32 v251, s74, v251\n"
"v_mfma_f32_16x16x32_bf16 v[184:187], a[212:215], a[172:175], v[184:187]\n"
"buffer_load_dwordx4 v224, s[28:31], s48 offen lds\n"
"v_mfma_f32_16x16x32_bf16 v[188:191], a[216:219], a[168:171], v[228:231]\n"
"v_mfma_f32_16x16x32_bf16 v[188:191], a[220:223], a[172:175], v[188:191]\n"
"v_mfma_f32_16x16x32_bf16 v[204:207], a[216:219], a[176:179], v[228:231]\n"
"v_mfma_f32_16x16x32_bf16 v[204:207], a[220:223], a[180:183], v[204:207]\n"
"buffer_load_dwordx4 v225, s[28:31], s48 offen offset:1024 lds\n"
"v_mfma_f32_16x16x32_bf16 v[200:203], a[208:211], a[176:179], v[228:231]\n"
"v_mfma_f32_16x16x32_bf16 v[200:203], a[212:215], a[180:183], v[200:203]\n"
"v_mfma_f32_16x16x32_bf16 v[196:199], a[200:203], a[176:179], v[228:231]\n"
"v_mfma_f32_16x16x32_bf16 v[196:199], a[204:207], a[180:183], v[196:199]\n"
"v_mfma_f32_16x16x32_bf16 v[192:195], a[192:195], a[176:179], v[228:231]\n"
"v_mfma_f32_16x16x32_bf16 v[192:195], a[196:199], a[180:183], v[192:195]\n"
"buffer_load_dwordx4 v226, s[28:31], s48 offen offset:2048 lds\n"
"v_mfma_f32_16x16x32_bf16 v[208:211], a[192:195], a[184:187], v[228:231]\n"
"v_mfma_f32_16x16x32_bf16 v[208:211], a[196:199], a[188:191], v[208:211]\n"
"v_mfma_f32_16x16x32_bf16 v[212:215], a[200:203], a[184:187], v[228:231]\n"
"v_mfma_f32_16x16x32_bf16 v[212:215], a[204:207], a[188:191], v[212:215]\n"
"v_mfma_f32_16x16x32_bf16 v[216:219], a[208:211], a[184:187], v[228:231]\n"
"v_mfma_f32_16x16x32_bf16 v[216:219], a[212:215], a[188:191], v[216:219]\n"
"buffer_load_dwordx4 v227, s[28:31], s48 offen offset:3072 lds\n"
"v_mfma_f32_16x16x32_bf16 v[220:223], a[216:219], a[184:187], v[228:231]\n"
"s_xor_b32 s50, s50, s74\n"
"v_mfma_f32_16x16x32_bf16 v[220:223], a[220:223], a[188:191], v[220:223]\n"
"v_mfma_f32_16x16x32_bf16 v[0:3], a[224:227], a[136:139], v[228:231]\n"
"v_mfma_f32_16x16x32_bf16 v[0:3], a[228:231], a[140:143], v[0:3]\n"
"v_mfma_f32_16x16x32_bf16 v[4:7], a[232:235], a[136:139], v[228:231]\n"
"s_waitcnt vmcnt(8) lgkmcnt(0)\n"
"v_mfma_f32_16x16x32_bf16 v[4:7], a[236:239], a[140:143], v[4:7]\n"
"s_barrier\n"
"v_mfma_f32_16x16x32_bf16 v[8:11], a[240:243], a[136:139], v[228:231]\n"
"s_mov_b32 m0, s51\n"
"ds_read_b64_tr_b16 a[192:193], v244 offset:0\n"
"v_mfma_f32_16x16x32_bf16 v[8:11], a[244:247], a[140:143], v[8:11]\n"
"buffer_load_dwordx4 v248, s[24:27], s49 offen lds\n"
"v_mfma_f32_16x16x32_bf16 v[12:15], a[248:251], a[136:139], v[228:231]\n"
"ds_read_b64_tr_b16 a[194:195], v244 offset:512\n"
"v_mfma_f32_16x16x32_bf16 v[12:15], a[252:255], a[140:143], v[12:15]\n"
"ds_read_b128 a[136:139], v246 offset:0\n"
"v_mfma_f32_16x16x32_bf16 v[28:31], a[248:251], a[144:147], v[228:231]\n"
"ds_read_b64_tr_b16 a[196:197], v244 offset:8192\n"
"v_mfma_f32_16x16x32_bf16 v[28:31], a[252:255], a[148:151], v[28:31]\n"
"ds_read_b64_tr_b16 a[198:199], v244 offset:8704\n"
"v_mfma_f32_16x16x32_bf16 v[24:27], a[240:243], a[144:147], v[228:231]\n"
"ds_read_b128 a[140:143], v247 offset:0\n"
"v_mfma_f32_16x16x32_bf16 v[24:27], a[244:247], a[148:151], v[24:27]\n"
"buffer_load_dwordx4 v249, s[24:27], s49 offen offset:1024 lds\n"
"v_mfma_f32_16x16x32_bf16 v[20:23], a[232:235], a[144:147], v[228:231]\n"
"ds_read_b64_tr_b16 a[200:201], v245 offset:0\n"
"v_mfma_f32_16x16x32_bf16 v[20:23], a[236:239], a[148:151], v[20:23]\n"
"ds_read_b64_tr_b16 a[202:203], v245 offset:512\n"
"v_mfma_f32_16x16x32_bf16 v[16:19], a[224:227], a[144:147], v[228:231]\n"
"ds_read_b64_tr_b16 a[204:205], v245 offset:8192\n"
"v_mfma_f32_16x16x32_bf16 v[16:19], a[228:231], a[148:151], v[16:19]\n"
"ds_read_b64_tr_b16 a[206:207], v245 offset:8704\n"
"v_mfma_f32_16x16x32_bf16 v[32:35], a[224:227], a[152:155], v[228:231]\n"
"ds_read_b64_tr_b16 a[208:209], v244 offset:1024\n"
"v_mfma_f32_16x16x32_bf16 v[32:35], a[228:231], a[156:159], v[32:35]\n"
"buffer_load_dwordx4 v254, s[24:27], s49 offen offset:2048 lds\n"
"v_mfma_f32_16x16x32_bf16 v[36:39], a[232:235], a[152:155], v[228:231]\n"
"ds_read_b64_tr_b16 a[210:211], v244 offset:1536\n"
"v_mfma_f32_16x16x32_bf16 v[36:39], a[236:239], a[156:159], v[36:39]\n"
"ds_read_b64_tr_b16 a[212:213], v244 offset:9216\n"
"v_mfma_f32_16x16x32_bf16 v[40:43], a[240:243], a[152:155], v[228:231]\n"
"ds_read_b64_tr_b16 a[214:215], v244 offset:9728\n"
"v_mfma_f32_16x16x32_bf16 v[40:43], a[244:247], a[156:159], v[40:43]\n"
"ds_read_b64_tr_b16 a[216:217], v245 offset:1024\n"
"v_mfma_f32_16x16x32_bf16 v[44:47], a[248:251], a[152:155], v[228:231]\n"
"ds_read_b64_tr_b16 a[218:219], v245 offset:1536\n"
"v_mfma_f32_16x16x32_bf16 v[44:47], a[252:255], a[156:159], v[44:47]\n"
"buffer_load_dwordx4 v255, s[24:27], s49 offen offset:3072 lds\n"
"v_mfma_f32_16x16x32_bf16 v[60:63], a[248:251], a[160:163], v[228:231]\n"
"ds_read_b64_tr_b16 a[220:221], v245 offset:9216\n"
"v_mfma_f32_16x16x32_bf16 v[60:63], a[252:255], a[164:167], v[60:63]\n"
"ds_read_b64_tr_b16 a[222:223], v245 offset:9728\n"
"v_mfma_f32_16x16x32_bf16 v[56:59], a[240:243], a[160:163], v[228:231]\n"
"ds_read_b128 a[144:147], v246 offset:2048\n"
"v_mfma_f32_16x16x32_bf16 v[56:59], a[244:247], a[164:167], v[56:59]\n"
"s_add_u32 s40, s49, 40960\n"
"ds_read_b128 a[148:151], v247 offset:2048\n"
"v_mfma_f32_16x16x32_bf16 v[52:55], a[232:235], a[160:163], v[228:231]\n"
"s_add_u32 m0, s51, 4096\n"
"ds_read_b128 a[152:155], v246 offset:4096\n"
"v_mfma_f32_16x16x32_bf16 v[52:55], a[236:239], a[164:167], v[52:55]\n"
"buffer_load_dwordx4 v248, s[24:27], s40 offen lds\n"
"v_mfma_f32_16x16x32_bf16 v[48:51], a[224:227], a[160:163], v[228:231]\n"
"ds_read_b128 a[156:159], v247 offset:4096\n"
"v_mfma_f32_16x16x32_bf16 v[48:51], a[228:231], a[164:167], v[48:51]\n"
"ds_read_b128 a[160:163], v252 offset:0\n"
"v_mfma_f32_16x16x32_bf16 v[64:67], a[224:227], a[168:171], v[228:231]\n"
"ds_read_b128 a[164:167], v253 offset:0\n"
"v_mfma_f32_16x16x32_bf16 v[64:67], a[228:231], a[172:175], v[64:67]\n"
"v_mfma_f32_16x16x32_bf16 v[68:71], a[232:235], a[168:171], v[228:231]\n"
"v_mfma_f32_16x16x32_bf16 v[68:71], a[236:239], a[172:175], v[68:71]\n"
"buffer_load_dwordx4 v249, s[24:27], s40 offen offset:1024 lds\n"
"v_mfma_f32_16x16x32_bf16 v[72:75], a[240:243], a[168:171], v[228:231]\n"
"v_mfma_f32_16x16x32_bf16 v[72:75], a[244:247], a[172:175], v[72:75]\n"
"v_mfma_f32_16x16x32_bf16 v[76:79], a[248:251], a[168:171], v[228:231]\n"
"ds_read_b128 a[168:171], v250 offset:0\n"
"v_mfma_f32_16x16x32_bf16 v[76:79], a[252:255], a[172:175], v[76:79]\n"
"ds_read_b128 a[172:175], v251 offset:0\n"
"v_mfma_f32_16x16x32_bf16 v[92:95], a[248:251], a[176:179], v[228:231]\n"
"v_mfma_f32_16x16x32_bf16 v[92:95], a[252:255], a[180:183], v[92:95]\n"
"buffer_load_dwordx4 v254, s[24:27], s40 offen offset:2048 lds\n"
"v_mfma_f32_16x16x32_bf16 v[88:91], a[240:243], a[176:179], v[228:231]\n"
"s_add_u32 s49, s49, 128\n"
"v_mfma_f32_16x16x32_bf16 v[88:91], a[244:247], a[180:183], v[88:91]\n"
"s_sub_u32 s41, s49, 1280\n"
"v_mfma_f32_16x16x32_bf16 v[84:87], a[232:235], a[176:179], v[228:231]\n"
"s_min_u32 s49, s49, s41\n"
"v_mfma_f32_16x16x32_bf16 v[84:87], a[236:239], a[180:183], v[84:87]\n"
"s_xor_b32 s51, s51, s74\n"
"v_mfma_f32_16x16x32_bf16 v[80:83], a[224:227], a[176:179], v[228:231]\n"
"ds_read_b128 a[176:179], v250 offset:2048\n"
"v_mfma_f32_16x16x32_bf16 v[80:83], a[228:231], a[180:183], v[80:83]\n"
"ds_read_b128 a[180:183], v251 offset:2048\n"
"v_mfma_f32_16x16x32_bf16 v[96:99], a[224:227], a[184:187], v[228:231]\n"
"v_mfma_f32_16x16x32_bf16 v[96:99], a[228:231], a[188:191], v[96:99]\n"
"v_mfma_f32_16x16x32_bf16 v[100:103], a[232:235], a[184:187], v[228:231]\n"
"v_mfma_f32_16x16x32_bf16 v[100:103], a[236:239], a[188:191], v[100:103]\n"
"v_mfma_f32_16x16x32_bf16 v[104:107], a[240:243], a[184:187], v[228:231]\n"
"v_mfma_f32_16x16x32_bf16 v[104:107], a[244:247], a[188:191], v[104:107]\n"
"v_mfma_f32_16x16x32_bf16 v[108:111], a[248:251], a[184:187], v[228:231]\n"
"s_add_u32 s47, s47, 1\n"
"ds_read_b128 a[184:187], v250 offset:4096\n"
"v_mfma_f32_16x16x32_bf16 v[108:111], a[252:255], a[188:191], v[108:111]\n"
"s_waitcnt lgkmcnt(1)\n"
"ds_read_b128 a[188:191], v251 offset:4096\n"
"LPE_%=:\n"
"s_cmp_lg_u32 s68, 0\n"
"s_cbranch_scc1 LPN_%=\n"
"LPL_%=:\n"
"v_mfma_f32_16x16x32_bf16 v[112:115], a[192:195], a[136:139], v[112:115]\n"
"v_mfma_f32_16x16x32_bf16 v[112:115], a[196:199], a[140:143], v[112:115]\n"
"v_mfma_f32_16x16x32_bf16 v[116:119], a[200:203], a[136:139], v[116:119]\n"
"s_waitcnt vmcnt(11)\n"
"v_mfma_f32_16x16x32_bf16 v[116:119], a[204:207], a[140:143], v[116:119]\n"
"s_barrier\n"
"v_mfma_f32_16x16x32_bf16 v[120:123], a[208:211], a[136:139], v[120:123]\n"
"s_add_u32 s40, s48, 256\n"
"s_xor_b32 m0, s50, 0xc000\n"
"ds_read_b64_tr_b16 a[224:225], v244 offset:16384\n"
"v_mfma_f32_16x16x32_bf16 v[120:123], a[212:215], a[140:143], v[120:123]\n"
"buffer_load_dwordx4 v224, s[28:31], s40 offen lds\n"
"v_mfma_f32_16x16x32_bf16 v[124:127], a[216:219], a[136:139], v[124:127]\n"
"ds_read_b64_tr_b16 a[226:227], v244 offset:16896\n"
"v_mfma_f32_16x16x32_bf16 v[124:127], a[220:223], a[140:143], v[124:127]\n"
"ds_read_b64_tr_b16 a[228:229], v244 offset:24576\n"
"v_mfma_f32_16x16x32_bf16 v[140:143], a[216:219], a[144:147], v[140:143]\n"
"ds_read_b64_tr_b16 a[230:231], v244 offset:25088\n"
"v_mfma_f32_16x16x32_bf16 v[140:143], a[220:223], a[148:151], v[140:143]\n"
"ds_read_b64_tr_b16 a[232:233], v245 offset:16384\n"
"v_mfma_f32_16x16x32_bf16 v[136:139], a[208:211], a[144:147], v[136:139]\n"
"ds_read_b64_tr_b16 a[234:235], v245 offset:16896\n"
"v_mfma_f32_16x16x32_bf16 v[136:139], a[212:215], a[148:151], v[136:139]\n"
"buffer_load_dwordx4 v225, s[28:31], s40 offen offset:1024 lds\n"
"v_mfma_f32_16x16x32_bf16 v[132:135], a[200:203], a[144:147], v[132:135]\n"
"ds_read_b64_tr_b16 a[236:237], v245 offset:24576\n"
"v_mfma_f32_16x16x32_bf16 v[132:135], a[204:207], a[148:151], v[132:135]\n"
"ds_read_b64_tr_b16 a[238:239], v245 offset:25088\n"
"v_mfma_f32_16x16x32_bf16 v[128:131], a[192:195], a[144:147], v[128:131]\n"
"ds_read_b64_tr_b16 a[240:241], v244 offset:17408\n"
"v_mfma_f32_16x16x32_bf16 v[128:131], a[196:199], a[148:151], v[128:131]\n"
"ds_read_b64_tr_b16 a[242:243], v244 offset:17920\n"
"v_mfma_f32_16x16x32_bf16 v[144:147], a[192:195], a[152:155], v[144:147]\n"
"ds_read_b64_tr_b16 a[244:245], v244 offset:25600\n"
"v_mfma_f32_16x16x32_bf16 v[144:147], a[196:199], a[156:159], v[144:147]\n"
"buffer_load_dwordx4 v226, s[28:31], s40 offen offset:2048 lds\n"
"v_mfma_f32_16x16x32_bf16 v[148:151], a[200:203], a[152:155], v[148:151]\n"
"ds_read_b64_tr_b16 a[246:247], v244 offset:26112\n"
"v_mfma_f32_16x16x32_bf16 v[148:151], a[204:207], a[156:159], v[148:151]\n"
"ds_read_b64_tr_b16 a[248:249], v245 offset:17408\n"
"v_mfma_f32_16x16x32_bf16 v[152:155], a[208:211], a[152:155], v[152:155]\n"
"ds_read_b64_tr_b16 a[250:251], v245 offset:17920\n"
"v_mfma_f32_16x16x32_bf16 v[152:155], a[212:215], a[156:159], v[152:155]\n"
"ds_read_b64_tr_b16 a[252:253], v245 offset:25600\n"
"v_mfma_f32_16x16x32_bf16 v[156:159], a[216:219], a[152:155], v[156:159]\n"
"ds_read_b64_tr_b16 a[254:255], v245 offset:26112\n"
"v_mfma_f32_16x16x32_bf16 v[156:159], a[220:223], a[156:159], v[156:159]\n"
"buffer_load_dwordx4 v227, s[28:31], s40 offen offset:3072 lds\n"
"v_mfma_f32_16x16x32_bf16 v[172:175], a[216:219], a[160:163], v[172:175]\n"
"s_cmp_eq_u32 s47, 8\n"
"v_mfma_f32_16x16x32_bf16 v[172:175], a[220:223], a[164:167], v[172:175]\n"
"s_cselect_b32 s41, s52, s58\n"
"v_mfma_f32_16x16x32_bf16 v[168:171], a[208:211], a[160:163], v[168:171]\n"
"s_add_u32 s48, s48, s41\n"
"v_mfma_f32_16x16x32_bf16 v[168:171], a[212:215], a[164:167], v[168:171]\n"
"s_sub_u32 s41, s48, 5242880\n"
"v_mfma_f32_16x16x32_bf16 v[164:167], a[200:203], a[160:163], v[164:167]\n"
"s_min_u32 s48, s48, s41\n"
"v_mfma_f32_16x16x32_bf16 v[164:167], a[204:207], a[164:167], v[164:167]\n"
"v_xor_b32 v244, s74, v244\n"
"v_xor_b32 v246, s74, v246\n"
"v_mfma_f32_16x16x32_bf16 v[160:163], a[192:195], a[160:163], v[160:163]\n"
"s_mov_b32 m0, s50\n"
"v_xor_b32 v245, s74, v245\n"
"v_xor_b32 v247, s74, v247\n"
"v_mfma_f32_16x16x32_bf16 v[160:163], a[196:199], a[164:167], v[160:163]\n"
"buffer_load_dwordx4 v224, s[28:31], s48 offen lds\n"
"v_mfma_f32_16x16x32_bf16 v[176:179], a[192:195], a[168:171], v[176:179]\n"
"v_xor_b32 v252, s74, v252\n"
"v_xor_b32 v253, s74, v253\n"
"v_mfma_f32_16x16x32_bf16 v[176:179], a[196:199], a[172:175], v[176:179]\n"
"v_xor_b32 v250, s74, v250\n"
"v_xor_b32 v251, s74, v251\n"
"v_mfma_f32_16x16x32_bf16 v[180:183], a[200:203], a[168:171], v[180:183]\n"
"v_mfma_f32_16x16x32_bf16 v[180:183], a[204:207], a[172:175], v[180:183]\n"
"buffer_load_dwordx4 v225, s[28:31], s48 offen offset:1024 lds\n"
"v_mfma_f32_16x16x32_bf16 v[184:187], a[208:211], a[168:171], v[184:187]\n"
"v_mfma_f32_16x16x32_bf16 v[184:187], a[212:215], a[172:175], v[184:187]\n"
"v_mfma_f32_16x16x32_bf16 v[188:191], a[216:219], a[168:171], v[188:191]\n"
"v_mfma_f32_16x16x32_bf16 v[188:191], a[220:223], a[172:175], v[188:191]\n"
"v_mfma_f32_16x16x32_bf16 v[204:207], a[216:219], a[176:179], v[204:207]\n"
"v_mfma_f32_16x16x32_bf16 v[204:207], a[220:223], a[180:183], v[204:207]\n"
"buffer_load_dwordx4 v226, s[28:31], s48 offen offset:2048 lds\n"
"v_mfma_f32_16x16x32_bf16 v[200:203], a[208:211], a[176:179], v[200:203]\n"
"v_mfma_f32_16x16x32_bf16 v[200:203], a[212:215], a[180:183], v[200:203]\n"
"v_mfma_f32_16x16x32_bf16 v[196:199], a[200:203], a[176:179], v[196:199]\n"
"v_mfma_f32_16x16x32_bf16 v[196:199], a[204:207], a[180:183], v[196:199]\n"
"v_mfma_f32_16x16x32_bf16 v[192:195], a[192:195], a[176:179], v[192:195]\n"
"v_mfma_f32_16x16x32_bf16 v[192:195], a[196:199], a[180:183], v[192:195]\n"
"s_waitcnt lgkmcnt(0)\n"
"buffer_load_dwordx4 v227, s[28:31], s48 offen offset:3072 lds\n"
"v_mfma_f32_16x16x32_bf16 v[208:211], a[192:195], a[184:187], v[208:211]\n"
"s_xor_b32 s50, s50, s74\n"
"v_mfma_f32_16x16x32_bf16 v[208:211], a[196:199], a[188:191], v[208:211]\n"
"v_mfma_f32_16x16x32_bf16 v[212:215], a[200:203], a[184:187], v[212:215]\n"
"v_mfma_f32_16x16x32_bf16 v[212:215], a[204:207], a[188:191], v[212:215]\n"
"v_mfma_f32_16x16x32_bf16 v[216:219], a[208:211], a[184:187], v[216:219]\n"
"v_mfma_f32_16x16x32_bf16 v[216:219], a[212:215], a[188:191], v[216:219]\n"
"v_mfma_f32_16x16x32_bf16 v[220:223], a[216:219], a[184:187], v[220:223]\n"
"v_mfma_f32_16x16x32_bf16 v[220:223], a[220:223], a[188:191], v[220:223]\n"
"v_mfma_f32_16x16x32_bf16 v[0:3], a[224:227], a[136:139], v[0:3]\n"
"v_mfma_f32_16x16x32_bf16 v[0:3], a[228:231], a[140:143], v[0:3]\n"
"v_mfma_f32_16x16x32_bf16 v[4:7], a[232:235], a[136:139], v[4:7]\n"
"s_waitcnt vmcnt(8)\n"
"v_mfma_f32_16x16x32_bf16 v[4:7], a[236:239], a[140:143], v[4:7]\n"
"s_barrier\n"
"v_mfma_f32_16x16x32_bf16 v[8:11], a[240:243], a[136:139], v[8:11]\n"
"s_mov_b32 m0, s51\n"
"ds_read_b64_tr_b16 a[192:193], v244 offset:0\n"
"v_mfma_f32_16x16x32_bf16 v[8:11], a[244:247], a[140:143], v[8:11]\n"
"buffer_load_dwordx4 v248, s[24:27], s49 offen lds\n"
"v_mfma_f32_16x16x32_bf16 v[12:15], a[248:251], a[136:139], v[12:15]\n"
"ds_read_b64_tr_b16 a[194:195], v244 offset:512\n"
"v_mfma_f32_16x16x32_bf16 v[12:15], a[252:255], a[140:143], v[12:15]\n"
"ds_read_b128 a[136:139], v246 offset:0\n"
"v_mfma_f32_16x16x32_bf16 v[28:31], a[248:251], a[144:147], v[28:31]\n"
"ds_read_b64_tr_b16 a[196:197], v244 offset:8192\n"
"v_mfma_f32_16x16x32_bf16 v[28:31], a[252:255], a[148:151], v[28:31]\n"
"ds_read_b64_tr_b16 a[198:199], v244 offset:8704\n"
"v_mfma_f32_16x16x32_bf16 v[24:27], a[240:243], a[144:147], v[24:27]\n"
"ds_read_b128 a[140:143], v247 offset:0\n"
"v_mfma_f32_16x16x32_bf16 v[24:27], a[244:247], a[148:151], v[24:27]\n"
"buffer_load_dwordx4 v249, s[24:27], s49 offen offset:1024 lds\n"
"v_mfma_f32_16x16x32_bf16 v[20:23], a[232:235], a[144:147], v[20:23]\n"
"ds_read_b64_tr_b16 a[200:201], v245 offset:0\n"
"v_mfma_f32_16x16x32_bf16 v[20:23], a[236:239], a[148:151], v[20:23]\n"
"ds_read_b64_tr_b16 a[202:203], v245 offset:512\n"
"v_mfma_f32_16x16x32_bf16 v[16:19], a[224:227], a[144:147], v[16:19]\n"
"ds_read_b64_tr_b16 a[204:205], v245 offset:8192\n"
"v_mfma_f32_16x16x32_bf16 v[16:19], a[228:231], a[148:151], v[16:19]\n"
"ds_read_b64_tr_b16 a[206:207], v245 offset:8704\n"
"v_mfma_f32_16x16x32_bf16 v[32:35], a[224:227], a[152:155], v[32:35]\n"
"ds_read_b64_tr_b16 a[208:209], v244 offset:1024\n"
"v_mfma_f32_16x16x32_bf16 v[32:35], a[228:231], a[156:159], v[32:35]\n"
"buffer_load_dwordx4 v254, s[24:27], s49 offen offset:2048 lds\n"
"v_mfma_f32_16x16x32_bf16 v[36:39], a[232:235], a[152:155], v[36:39]\n"
"ds_read_b64_tr_b16 a[210:211], v244 offset:1536\n"
"v_mfma_f32_16x16x32_bf16 v[36:39], a[236:239], a[156:159], v[36:39]\n"
"ds_read_b64_tr_b16 a[212:213], v244 offset:9216\n"
"v_mfma_f32_16x16x32_bf16 v[40:43], a[240:243], a[152:155], v[40:43]\n"
"ds_read_b64_tr_b16 a[214:215], v244 offset:9728\n"
"v_mfma_f32_16x16x32_bf16 v[40:43], a[244:247], a[156:159], v[40:43]\n"
"ds_read_b64_tr_b16 a[216:217], v245 offset:1024\n"
"v_mfma_f32_16x16x32_bf16 v[44:47], a[248:251], a[152:155], v[44:47]\n"
"ds_read_b64_tr_b16 a[218:219], v245 offset:1536\n"
"v_mfma_f32_16x16x32_bf16 v[44:47], a[252:255], a[156:159], v[44:47]\n"
"buffer_load_dwordx4 v255, s[24:27], s49 offen offset:3072 lds\n"
"v_mfma_f32_16x16x32_bf16 v[60:63], a[248:251], a[160:163], v[60:63]\n"
"ds_read_b64_tr_b16 a[220:221], v245 offset:9216\n"
"v_mfma_f32_16x16x32_bf16 v[60:63], a[252:255], a[164:167], v[60:63]\n"
"ds_read_b64_tr_b16 a[222:223], v245 offset:9728\n"
"v_mfma_f32_16x16x32_bf16 v[56:59], a[240:243], a[160:163], v[56:59]\n"
"ds_read_b128 a[144:147], v246 offset:2048\n"
"v_mfma_f32_16x16x32_bf16 v[56:59], a[244:247], a[164:167], v[56:59]\n"
"s_add_u32 s40, s49, 40960\n"
"ds_read_b128 a[148:151], v247 offset:2048\n"
"v_mfma_f32_16x16x32_bf16 v[52:55], a[232:235], a[160:163], v[52:55]\n"
"s_add_u32 m0, s51, 4096\n"
"ds_read_b128 a[152:155], v246 offset:4096\n"
"v_mfma_f32_16x16x32_bf16 v[52:55], a[236:239], a[164:167], v[52:55]\n"
"buffer_load_dwordx4 v248, s[24:27], s40 offen lds\n"
"v_mfma_f32_16x16x32_bf16 v[48:51], a[224:227], a[160:163], v[48:51]\n"
"ds_read_b128 a[156:159], v247 offset:4096\n"
"v_mfma_f32_16x16x32_bf16 v[48:51], a[228:231], a[164:167], v[48:51]\n"
"ds_read_b128 a[160:163], v252 offset:0\n"
"v_mfma_f32_16x16x32_bf16 v[64:67], a[224:227], a[168:171], v[64:67]\n"
"ds_read_b128 a[164:167], v253 offset:0\n"
"v_mfma_f32_16x16x32_bf16 v[64:67], a[228:231], a[172:175], v[64:67]\n"
"v_mfma_f32_16x16x32_bf16 v[68:71], a[232:235], a[168:171], v[68:71]\n"
"v_mfma_f32_16x16x32_bf16 v[68:71], a[236:239], a[172:175], v[68:71]\n"
"buffer_load_dwordx4 v249, s[24:27], s40 offen offset:1024 lds\n"
"v_mfma_f32_16x16x32_bf16 v[72:75], a[240:243], a[168:171], v[72:75]\n"
"v_mfma_f32_16x16x32_bf16 v[72:75], a[244:247], a[172:175], v[72:75]\n"
"v_mfma_f32_16x16x32_bf16 v[76:79], a[248:251], a[168:171], v[76:79]\n"
"ds_read_b128 a[168:171], v250 offset:0\n"
"v_mfma_f32_16x16x32_bf16 v[76:79], a[252:255], a[172:175], v[76:79]\n"
"ds_read_b128 a[172:175], v251 offset:0\n"
"v_mfma_f32_16x16x32_bf16 v[92:95], a[248:251], a[176:179], v[92:95]\n"
"v_mfma_f32_16x16x32_bf16 v[92:95], a[252:255], a[180:183], v[92:95]\n"
"buffer_load_dwordx4 v254, s[24:27], s40 offen offset:2048 lds\n"
"v_mfma_f32_16x16x32_bf16 v[88:91], a[240:243], a[176:179], v[88:91]\n"
"s_add_u32 s49, s49, 128\n"
"v_mfma_f32_16x16x32_bf16 v[88:91], a[244:247], a[180:183], v[88:91]\n"
"s_sub_u32 s41, s49, 1280\n"
"v_mfma_f32_16x16x32_bf16 v[84:87], a[232:235], a[176:179], v[84:87]\n"
"s_min_u32 s49, s49, s41\n"
"v_mfma_f32_16x16x32_bf16 v[84:87], a[236:239], a[180:183], v[84:87]\n"
"s_xor_b32 s51, s51, s74\n"
"v_mfma_f32_16x16x32_bf16 v[80:83], a[224:227], a[176:179], v[80:83]\n"
"ds_read_b128 a[176:179], v250 offset:2048\n"
"v_mfma_f32_16x16x32_bf16 v[80:83], a[228:231], a[180:183], v[80:83]\n"
"ds_read_b128 a[180:183], v251 offset:2048\n"
"v_mfma_f32_16x16x32_bf16 v[96:99], a[224:227], a[184:187], v[96:99]\n"
"v_mfma_f32_16x16x32_bf16 v[96:99], a[228:231], a[188:191], v[96:99]\n"
"v_mfma_f32_16x16x32_bf16 v[100:103], a[232:235], a[184:187], v[100:103]\n"
"v_mfma_f32_16x16x32_bf16 v[100:103], a[236:239], a[188:191], v[100:103]\n"
"v_mfma_f32_16x16x32_bf16 v[104:107], a[240:243], a[184:187], v[104:107]\n"
"v_mfma_f32_16x16x32_bf16 v[104:107], a[244:247], a[188:191], v[104:107]\n"
"v_mfma_f32_16x16x32_bf16 v[108:111], a[248:251], a[184:187], v[108:111]\n"
"s_add_u32 s47, s47, 1\n"
"ds_read_b128 a[184:187], v250 offset:4096\n"
"v_mfma_f32_16x16x32_bf16 v[108:111], a[252:255], a[188:191], v[108:111]\n"
"s_waitcnt lgkmcnt(1)\n"
"s_cmp_lg_u32 s47, s62\n"
"ds_read_b128 a[188:191], v251 offset:4096\n"
"s_cbranch_scc1 LPL_%=\n"
"LPX_%=:\n"
"s_cmp_eq_u32 s47, 18\n"
"s_cbranch_scc1 LPT2_%=\n"
"v_mfma_f32_16x16x32_bf16 v[112:115], a[192:195], a[136:139], v[112:115]\n"
"s_add_u32 s78, s96, 8192\n"
"v_mov_b32 v241, 1\n"
"v_mfma_f32_16x16x32_bf16 v[112:115], a[196:199], a[140:143], v[112:115]\n"
"s_mov_b64 exec, 1\n"
"buffer_store_dword v241, off, s[32:35], s96 sc0 sc1\n"
"buffer_store_dword v241, off, s[32:35], s78 sc0 sc1\n"
"s_mov_b64 exec, -1\n"
"v_mfma_f32_16x16x32_bf16 v[116:119], a[200:203], a[136:139], v[116:119]\n"
"s_waitcnt vmcnt(13)\n"
"v_mfma_f32_16x16x32_bf16 v[116:119], a[204:207], a[140:143], v[116:119]\n"
"s_barrier\n"
"v_mfma_f32_16x16x32_bf16 v[120:123], a[208:211], a[136:139], v[120:123]\n"
"s_add_u32 s40, s48, 256\n"
"s_xor_b32 m0, s50, 0xc000\n"
"ds_read_b64_tr_b16 a[238:239], v245 offset:25088\n"
"v_mfma_f32_16x16x32_bf16 v[120:123], a[212:215], a[140:143], v[120:123]\n"
"buffer_load_dwordx4 v224, s[28:31], s40 offen lds\n"
"v_mfma_f32_16x16x32_bf16 v[124:127], a[216:219], a[136:139], v[124:127]\n"
"v_mfma_f32_16x16x32_bf16 v[124:127], a[220:223], a[140:143], v[124:127]\n"
"v_mfma_f32_16x16x32_bf16 v[188:191], a[216:219], a[168:171], v[188:191]\n"
"ds_read_b64_tr_b16 a[254:255], v245 offset:26112\n"
"v_mfma_f32_16x16x32_bf16 v[188:191], a[220:223], a[172:175], v[188:191]\n"
"ds_read_b64_tr_b16 a[252:253], v245 offset:25600\n"
"v_mfma_f32_16x16x32_bf16 v[184:187], a[208:211], a[168:171], v[184:187]\n"
"ds_read_b64_tr_b16 a[224:225], v244 offset:16384\n"
"ds_read_b64_tr_b16 a[228:229], v244 offset:24576\n"
"v_mfma_f32_16x16x32_bf16 v[184:187], a[212:215], a[172:175], v[184:187]\n"
"buffer_load_dwordx4 v225, s[28:31], s40 offen offset:1024 lds\n"
"v_mfma_f32_16x16x32_bf16 v[180:183], a[200:203], a[168:171], v[180:183]\n"
"v_mfma_f32_16x16x32_bf16 v[180:183], a[204:207], a[172:175], v[180:183]\n"
"v_mfma_f32_16x16x32_bf16 v[176:179], a[192:195], a[168:171], v[176:179]\n"
"ds_read_b64_tr_b16 a[230:231], v244 offset:25088\n"
"v_mfma_f32_16x16x32_bf16 v[176:179], a[196:199], a[172:175], v[176:179]\n"
"ds_read_b64_tr_b16 a[244:245], v244 offset:25600\n"
"v_mfma_f32_16x16x32_bf16 v[192:195], a[192:195], a[176:179], v[192:195]\n"
"ds_read_b64_tr_b16 a[226:227], v244 offset:16896\n"
"ds_read_b64_tr_b16 a[250:251], v245 offset:17920\n"
"v_mfma_f32_16x16x32_bf16 v[192:195], a[196:199], a[180:183], v[192:195]\n"
"buffer_load_dwordx4 v226, s[28:31], s40 offen offset:2048 lds\n"
"v_mfma_f32_16x16x32_bf16 v[196:199], a[200:203], a[176:179], v[196:199]\n"
"v_mfma_f32_16x16x32_bf16 v[196:199], a[204:207], a[180:183], v[196:199]\n"
"v_mfma_f32_16x16x32_bf16 v[200:203], a[208:211], a[176:179], v[200:203]\n"
"ds_read_b64_tr_b16 a[236:237], v245 offset:24576\n"
"ds_read_b64_tr_b16 a[246:247], v244 offset:26112\n"
"v_mfma_f32_16x16x32_bf16 v[200:203], a[212:215], a[180:183], v[200:203]\n"
"ds_read_b64_tr_b16 a[240:241], v244 offset:17408\n"
"v_mfma_f32_16x16x32_bf16 v[204:207], a[216:219], a[176:179], v[204:207]\n"
"ds_read_b64_tr_b16 a[242:243], v244 offset:17920\n"
"ds_read_b64_tr_b16 a[248:249], v245 offset:17408\n"
"v_mfma_f32_16x16x32_bf16 v[204:207], a[220:223], a[180:183], v[204:207]\n"
"s_waitcnt lgkmcnt(7)\n"
"buffer_load_dwordx4 v227, s[28:31], s40 offen offset:3072 lds\n"
"v_mfma_f32_16x16x32_bf16 v[220:223], a[216:219], a[184:187], v[220:223]\n"
"s_cmp_eq_u32 s47, 8\n"
"v_mfma_f32_16x16x32_bf16 v[220:223], a[220:223], a[188:191], v[220:223]\n"
"s_cselect_b32 s41, s52, s58\n"
"s_nop 1\n"
"v_pk_fma_f32 v[176:177], v[176:177], s[70:71], s[70:71] op_sel:[0,0,1] op_sel_hi:[1,0,1]\nv_pk_fma_f32 v[178:179], v[178:179], s[70:71], s[70:71] op_sel:[0,0,1] op_sel_hi:[1,0,1]\nv_pk_fma_f32 v[180:181], v[180:181], s[70:71], s[70:71] op_sel:[0,0,1] op_sel_hi:[1,0,1]\nv_pk_fma_f32 v[182:183], v[182:183], s[70:71], s[70:71] op_sel:[0,0,1] op_sel_hi:[1,0,1]\nv_pk_fma_f32 v[184:185], v[184:185], s[70:71], s[70:71] op_sel:[0,0,1] op_sel_hi:[1,0,1]\nv_pk_fma_f32 v[186:187], v[186:187], s[70:71], s[70:71] op_sel:[0,0,1] op_sel_hi:[1,0,1]\nv_pk_fma_f32 v[188:189], v[188:189], s[70:71], s[70:71] op_sel:[0,0,1] op_sel_hi:[1,0,1]\nv_pk_fma_f32 v[190:191], v[190:191], s[70:71], s[70:71] op_sel:[0,0,1] op_sel_hi:[1,0,1]\nv_pk_fma_f32 v[192:193], v[192:193], s[70:71], s[70:71] op_sel:[0,0,1] op_sel_hi:[1,0,1]\nv_pk_fma_f32 v[194:195], v[194:195], s[70:71], s[70:71] op_sel:[0,0,1] op_sel_hi:[1,0,1]\nv_pk_fma_f32 v[196:197], v[196:197], s[70:71], s[70:71] op_sel:[0,0,1] op_sel_hi:[1,0,1]\nv_pk_fma_f32 v[198:199], v[198:199], s[70:71], s[70:71] op_sel:[0,0,1] op_sel_hi:[1,0,1]\nv_pk_fma_f32 v[200:201], v[200:201], s[70:71], s[70:71] op_sel:[0,0,1] op_sel_hi:[1,0,1]\nv_pk_fma_f32 v[202:203], v[202:203], s[70:71], s[70:71] op_sel:[0,0,1] op_sel_hi:[1,0,1]\nv_pk_fma_f32 v[204:205], v[204:205], s[70:71], s[70:71] op_sel:[0,0,1] op_sel_hi:[1,0,1]\nv_pk_fma_f32 v[206:207], v[206:207], s[70:71], s[70:71] op_sel:[0,0,1] op_sel_hi:[1,0,1]\n"
"v_mfma_f32_16x16x32_bf16 v[216:219], a[208:211], a[184:187], v[216:219]\n"
"s_add_u32 s48, s48, s41\n"
"v_cvt_pk_bf16_f32 v192, v192, v193\n"
"v_cvt_pk_bf16_f32 v193, v194, v195\n"
"v_mfma_f32_16x16x32_bf16 v[216:219], a[212:215], a[188:191], v[216:219]\n"
"s_sub_u32 s41, s48, 5242880\n"
"v_cvt_pk_bf16_f32 v176, v176, v177\n"
"v_cvt_pk_bf16_f32 v177, v178, v179\n"
"v_mfma_f32_16x16x32_bf16 v[212:215], a[200:203], a[184:187], v[212:215]\n"
"s_min_u32 s48, s48, s41\n"
"v_cvt_pk_bf16_f32 v178, v180, v181\n"
"v_cvt_pk_bf16_f32 v179, v182, v183\n"
"v_mfma_f32_16x16x32_bf16 v[212:215], a[204:207], a[188:191], v[212:215]\n"
"ds_read_b64_tr_b16 a[234:235], v245 offset:16896\n"
"v_xor_b32 v246, s74, v246\n"
"v_mfma_f32_16x16x32_bf16 v[208:211], a[192:195], a[184:187], v[208:211]\n"
"s_mov_b32 m0, s50\n"
"v_and_b32 v182, 0xf0, v242\n"
"v_cvt_pk_bf16_f32 v200, v200, v201\n"
"v_mfma_f32_16x16x32_bf16 v[208:211], a[196:199], a[188:191], v[208:211]\n"
"buffer_load_dwordx4 v224, s[28:31], s48 offen lds\n"
"v_mfma_f32_16x16x32_bf16 v[160:163], a[192:195], a[160:163], v[160:163]\n"
"v_bfe_u32 v180, v242, 9, 1\n"
"v_lshl_add_u32 v180, v180, 8, v182\n"
"v_mfma_f32_16x16x32_bf16 v[160:163], a[196:199], a[164:167], v[160:163]\n"
"v_bfe_u32 v182, v242, 8, 1\n"
"v_cvt_pk_bf16_f32 v184, v184, v185\n"
"v_mfma_f32_16x16x32_bf16 v[164:167], a[200:203], a[160:163], v[164:167]\n"
"ds_read_b64_tr_b16 a[232:233], v245 offset:16384\n"
"v_xor_b32 v247, s74, v247\n"
"v_mfma_f32_16x16x32_bf16 v[164:167], a[204:207], a[164:167], v[164:167]\n"
"buffer_load_dwordx4 v225, s[28:31], s48 offen offset:1024 lds\n"
"v_mfma_f32_16x16x32_bf16 v[168:171], a[208:211], a[160:163], v[168:171]\n"
"v_cvt_pk_bf16_f32 v201, v202, v203\n"
"v_xor_b32 v253, s74, v253\n"
"v_mfma_f32_16x16x32_bf16 v[168:171], a[212:215], a[164:167], v[168:171]\n"
"v_cvt_pk_bf16_f32 v194, v196, v197\n"
"v_xor_b32 v244, s74, v244\n"
"v_mfma_f32_16x16x32_bf16 v[172:175], a[216:219], a[160:163], v[172:175]\n"
"v_lshl_add_u32 v180, v182, 3, v180\n"
"v_cvt_pk_bf16_f32 v185, v186, v187\n"
"v_mfma_f32_16x16x32_bf16 v[172:175], a[220:223], a[164:167], v[172:175]\n"
"v_cvt_pk_bf16_f32 v186, v188, v189\n"
"v_cvt_pk_bf16_f32 v187, v190, v191\n"
"v_mfma_f32_16x16x32_bf16 v[140:143], a[216:219], a[144:147], v[140:143]\n"
"v_cvt_pk_bf16_f32 v203, v206, v207\n"
"v_bfe_u32 v181, v242, 9, 1\n"
"v_mfma_f32_16x16x32_bf16 v[140:143], a[220:223], a[148:151], v[140:143]\n"
"buffer_load_dwordx4 v226, s[28:31], s48 offen offset:2048 lds\n"
"v_mfma_f32_16x16x32_bf16 v[136:139], a[208:211], a[144:147], v[136:139]\n"
"v_xor_b32_e64 v182, s42, 1\n"
"v_lshl_add_u32 v180, v182, 10, v180\n"
"v_mfma_f32_16x16x32_bf16 v[136:139], a[212:215], a[148:151], v[136:139]\n"
"v_cvt_pk_bf16_f32 v202, v204, v205\n"
"v_xor_b32 v252, s74, v252\n"
"v_mfma_f32_16x16x32_bf16 v[132:135], a[200:203], a[144:147], v[132:135]\n"
"v_add_u32 v180, 94208, v180\n"
"v_and_b32 v182, 0xf0, v242\n"
"v_mfma_f32_16x16x32_bf16 v[132:135], a[204:207], a[148:151], v[132:135]\n"
"ds_write2st64_b64 v180, v[184:185], v[186:187] offset0:64 offset1:65\n"
"v_pk_fma_f32 v[208:209], v[208:209], s[70:71], s[70:71] op_sel:[0,0,1] op_sel_hi:[1,0,1]\nv_pk_fma_f32 v[210:211], v[210:211], s[70:71], s[70:71] op_sel:[0,0,1] op_sel_hi:[1,0,1]\nv_pk_fma_f32 v[212:213], v[212:213], s[70:71], s[70:71] op_sel:[0,0,1] op_sel_hi:[1,0,1]\nv_pk_fma_f32 v[214:215], v[214:215], s[70:71], s[70:71] op_sel:[0,0,1] op_sel_hi:[1,0,1]\nv_pk_fma_f32 v[216:217], v[216:217], s[70:71], s[70:71] op_sel:[0,0,1] op_sel_hi:[1,0,1]\nv_pk_fma_f32 v[218:219], v[218:219], s[70:71], s[70:71] op_sel:[0,0,1] op_sel_hi:[1,0,1]\nv_pk_fma_f32 v[220:221], v[220:221], s[70:71], s[70:71] op_sel:[0,0,1] op_sel_hi:[1,0,1]\nv_pk_fma_f32 v[222:223], v[222:223], s[70:71], s[70:71] op_sel:[0,0,1] op_sel_hi:[1,0,1]\nv_pk_fma_f32 v[160:161], v[160:161], s[70:71], s[70:71] op_sel:[0,0,1] op_sel_hi:[1,0,1]\nv_pk_fma_f32 v[162:163], v[162:163], s[70:71], s[70:71] op_sel:[0,0,1] op_sel_hi:[1,0,1]\nv_pk_fma_f32 v[164:165], v[164:165], s[70:71], s[70:71] op_sel:[0,0,1] op_sel_hi:[1,0,1]\nv_pk_fma_f32 v[166:167], v[166:167], s[70:71], s[70:71] op_sel:[0,0,1] op_sel_hi:[1,0,1]\nv_pk_fma_f32 v[168:169], v[168:169], s[70:71], s[70:71] op_sel:[0,0,1] op_sel_hi:[1,0,1]\nv_pk_fma_f32 v[170:171], v[170:171], s[70:71], s[70:71] op_sel:[0,0,1] op_sel_hi:[1,0,1]\nv_pk_fma_f32 v[172:173], v[172:173], s[70:71], s[70:71] op_sel:[0,0,1] op_sel_hi:[1,0,1]\nv_pk_fma_f32 v[174:175], v[174:175], s[70:71], s[70:71] op_sel:[0,0,1] op_sel_hi:[1,0,1]\n"
"v_cvt_pk_bf16_f32 v216, v216, v217\n"
"v_mfma_f32_16x16x32_bf16 v[128:131], a[192:195], a[144:147], v[128:131]\n"
"v_cvt_pk_bf16_f32 v168, v168, v169\n"
"v_xor_b32 v250, s74, v250\n"
"v_mfma_f32_16x16x32_bf16 v[128:131], a[196:199], a[148:151], v[128:131]\n"
"buffer_load_dwordx4 v227, s[28:31], s48 offen offset:3072 lds\n"
"v_mfma_f32_16x16x32_bf16 v[144:147], a[192:195], a[152:155], v[144:147]\n"
"s_xor_b32 s50, s50, s74\n"
"v_cvt_pk_bf16_f32 v208, v208, v209\n"
"v_xor_b32 v251, s74, v251\n"
"v_mfma_f32_16x16x32_bf16 v[144:147], a[196:199], a[156:159], v[144:147]\n"
"v_cvt_pk_bf16_f32 v217, v218, v219\n"
"v_cvt_pk_bf16_f32 v169, v170, v171\n"
"v_mfma_f32_16x16x32_bf16 v[148:151], a[200:203], a[152:155], v[148:151]\n"
"v_cvt_pk_bf16_f32 v195, v198, v199\n"
"v_cvt_pk_bf16_f32 v209, v210, v211\n"
"v_mfma_f32_16x16x32_bf16 v[148:151], a[204:207], a[156:159], v[148:151]\n"
"ds_write2st64_b64 v180, v[192:193], v[194:195] offset0:88 offset1:89\n"
"v_pk_fma_f32 v[112:113], v[112:113], s[70:71], s[70:71] op_sel:[0,0,1] op_sel_hi:[1,0,1]\nv_pk_fma_f32 v[114:115], v[114:115], s[70:71], s[70:71] op_sel:[0,0,1] op_sel_hi:[1,0,1]\nv_pk_fma_f32 v[116:117], v[116:117], s[70:71], s[70:71] op_sel:[0,0,1] op_sel_hi:[1,0,1]\nv_pk_fma_f32 v[118:119], v[118:119], s[70:71], s[70:71] op_sel:[0,0,1] op_sel_hi:[1,0,1]\nv_pk_fma_f32 v[120:121], v[120:121], s[70:71], s[70:71] op_sel:[0,0,1] op_sel_hi:[1,0,1]\nv_pk_fma_f32 v[122:123], v[122:123], s[70:71], s[70:71] op_sel:[0,0,1] op_sel_hi:[1,0,1]\nv_pk_fma_f32 v[124:125], v[124:125], s[70:71], s[70:71] op_sel:[0,0,1] op_sel_hi:[1,0,1]\nv_pk_fma_f32 v[126:127], v[126:127], s[70:71], s[70:71] op_sel:[0,0,1] op_sel_hi:[1,0,1]\nv_pk_fma_f32 v[128:129], v[128:129], s[70:71], s[70:71] op_sel:[0,0,1] op_sel_hi:[1,0,1]\nv_pk_fma_f32 v[130:131], v[130:131], s[70:71], s[70:71] op_sel:[0,0,1] op_sel_hi:[1,0,1]\nv_pk_fma_f32 v[132:133], v[132:133], s[70:71], s[70:71] op_sel:[0,0,1] op_sel_hi:[1,0,1]\nv_pk_fma_f32 v[134:135], v[134:135], s[70:71], s[70:71] op_sel:[0,0,1] op_sel_hi:[1,0,1]\nv_pk_fma_f32 v[136:137], v[136:137], s[70:71], s[70:71] op_sel:[0,0,1] op_sel_hi:[1,0,1]\nv_pk_fma_f32 v[138:139], v[138:139], s[70:71], s[70:71] op_sel:[0,0,1] op_sel_hi:[1,0,1]\nv_pk_fma_f32 v[140:141], v[140:141], s[70:71], s[70:71] op_sel:[0,0,1] op_sel_hi:[1,0,1]\nv_pk_fma_f32 v[142:143], v[142:143], s[70:71], s[70:71] op_sel:[0,0,1] op_sel_hi:[1,0,1]\n"
"v_mfma_f32_16x16x32_bf16 v[152:155], a[208:211], a[152:155], v[152:155]\n"
"v_cvt_pk_bf16_f32 v218, v220, v221\n"
"v_cvt_pk_bf16_f32 v136, v136, v137\n"
"v_mfma_f32_16x16x32_bf16 v[152:155], a[212:215], a[156:159], v[152:155]\n"
"v_cvt_pk_bf16_f32 v137, v138, v139\n"
"v_cvt_pk_bf16_f32 v139, v142, v143\n"
"v_mfma_f32_16x16x32_bf16 v[156:159], a[216:219], a[152:155], v[156:159]\n"
"v_cvt_pk_bf16_f32 v128, v128, v129\n"
"v_cvt_pk_bf16_f32 v138, v140, v141\n"
"v_mfma_f32_16x16x32_bf16 v[156:159], a[220:223], a[156:159], v[156:159]\n"
"s_waitcnt lgkmcnt(1)\n"
"v_cvt_pk_bf16_f32 v112, v112, v113\n"
"v_xor_b32 v245, s74, v245\n"
"v_mfma_f32_16x16x32_bf16 v[96:99], a[224:227], a[184:187], v[96:99]\n"
"v_cvt_pk_bf16_f32 v120, v120, v121\n"
"v_cvt_pk_bf16_f32 v129, v130, v131\n"
"v_mfma_f32_16x16x32_bf16 v[96:99], a[228:231], a[188:191], v[96:99]\n"
"ds_write2st64_b64 v180, v[200:201], v[202:203] offset0:96 offset1:97\n"
"v_cvt_pk_bf16_f32 v219, v222, v223\n"
"v_cvt_pk_bf16_f32 v170, v172, v173\n"
"v_cvt_pk_bf16_f32 v131, v134, v135\n"
"v_lshl_add_u32 v181, v181, 8, v182\n"
"v_bfe_u32 v182, v242, 8, 1\n"
"v_lshl_add_u32 v181, v182, 9, v181\n"
"v_mfma_f32_16x16x32_bf16 v[100:103], a[232:235], a[184:187], v[100:103]\n"
"s_waitcnt vmcnt(10)\n"
"v_permlane16_swap_b32 v136, v138\n"
"v_mfma_f32_16x16x32_bf16 v[100:103], a[236:239], a[188:191], v[100:103]\n"
"s_barrier\n"
"ds_write2st64_b64 v180, v[216:217], v[218:219] offset0:128 offset1:129\n"
"v_pk_fma_f32 v[144:145], v[144:145], s[70:71], s[70:71] op_sel:[0,0,1] op_sel_hi:[1,0,1]\nv_pk_fma_f32 v[146:147], v[146:147], s[70:71], s[70:71] op_sel:[0,0,1] op_sel_hi:[1,0,1]\nv_pk_fma_f32 v[148:149], v[148:149], s[70:71], s[70:71] op_sel:[0,0,1] op_sel_hi:[1,0,1]\nv_pk_fma_f32 v[150:151], v[150:151], s[70:71], s[70:71] op_sel:[0,0,1] op_sel_hi:[1,0,1]\nv_pk_fma_f32 v[152:153], v[152:153], s[70:71], s[70:71] op_sel:[0,0,1] op_sel_hi:[1,0,1]\nv_pk_fma_f32 v[154:155], v[154:155], s[70:71], s[70:71] op_sel:[0,0,1] op_sel_hi:[1,0,1]\nv_pk_fma_f32 v[156:157], v[156:157], s[70:71], s[70:71] op_sel:[0,0,1] op_sel_hi:[1,0,1]\nv_pk_fma_f32 v[158:159], v[158:159], s[70:71], s[70:71] op_sel:[0,0,1] op_sel_hi:[1,0,1]\n"
"v_cvt_pk_bf16_f32 v144, v144, v145\n"
"v_mfma_f32_16x16x32_bf16 v[104:107], a[240:243], a[184:187], v[104:107]\n"
"s_mov_b32 m0, s51\n"
"v_cvt_pk_bf16_f32 v171, v174, v175\n"
"v_cvt_pk_bf16_f32 v121, v122, v123\n"
"v_mfma_f32_16x16x32_bf16 v[104:107], a[244:247], a[188:191], v[104:107]\n"
"buffer_load_dwordx4 v248, s[24:27], s49 offen lds\n"
"v_mfma_f32_16x16x32_bf16 v[108:111], a[248:251], a[184:187], v[108:111]\n"
"v_xor_b32_e64 v182, s42, 0\n"
"v_lshl_add_u32 v181, v182, 10, v181\n"
"v_mfma_f32_16x16x32_bf16 v[108:111], a[252:255], a[188:191], v[108:111]\n"
"ds_read_b64_tr_b16 a[218:219], v245 offset:1536\n"
"v_cvt_pk_bf16_f32 v211, v214, v215\n"
"v_mfma_f32_16x16x32_bf16 v[92:95], a[248:251], a[176:179], v[92:95]\n"
"v_cvt_pk_bf16_f32 v160, v160, v161\n"
"v_cvt_pk_bf16_f32 v145, v146, v147\n"
"v_mfma_f32_16x16x32_bf16 v[92:95], a[252:255], a[180:183], v[92:95]\n"
"ds_write2st64_b64 v180, v[176:177], v[178:179] offset0:0 offset1:1\n"
"v_cvt_pk_bf16_f32 v122, v124, v125\n"
"v_cvt_pk_bf16_f32 v123, v126, v127\n"
"v_cvt_pk_bf16_f32 v146, v148, v149\n"
"v_cvt_pk_bf16_f32 v152, v152, v153\n"
"v_cvt_pk_bf16_f32 v153, v154, v155\n"
"v_cvt_pk_bf16_f32 v155, v158, v159\n"
"v_mfma_f32_16x16x32_bf16 v[88:91], a[240:243], a[176:179], v[88:91]\n"
"v_cvt_pk_bf16_f32 v210, v212, v213\n"
"v_cvt_pk_bf16_f32 v113, v114, v115\n"
"v_mfma_f32_16x16x32_bf16 v[88:91], a[244:247], a[180:183], v[88:91]\n"
"buffer_load_dwordx4 v249, s[24:27], s49 offen offset:1024 lds\n"
"v_mfma_f32_16x16x32_bf16 v[84:87], a[232:235], a[176:179], v[84:87]\n"
"v_cvt_pk_bf16_f32 v161, v162, v163\n"
"v_cvt_pk_bf16_f32 v154, v156, v157\n"
"v_mfma_f32_16x16x32_bf16 v[84:87], a[236:239], a[180:183], v[84:87]\n"
"s_nop 0\n"
"v_permlane16_swap_b32 v152, v154\n"
"v_mfma_f32_16x16x32_bf16 v[80:83], a[224:227], a[176:179], v[80:83]\n"
"ds_read_b64_tr_b16 a[196:197], v244 offset:8192\n"
"ds_read_b64_tr_b16 a[214:215], v244 offset:9728\n"
"v_mfma_f32_16x16x32_bf16 v[80:83], a[228:231], a[180:183], v[80:83]\n"
"ds_write2st64_b64 v180, v[208:209], v[210:211] offset0:120 offset1:121\n"
"v_cvt_pk_bf16_f32 v114, v116, v117\n"
"v_permlane16_swap_b32 v137, v139\n"
"v_permlane16_swap_b32 v144, v146\n"
"v_mfma_f32_16x16x32_bf16 v[64:67], a[224:227], a[168:171], v[64:67]\n"
"v_permlane16_swap_b32 v121, v123\n"
"v_mfma_f32_16x16x32_bf16 v[64:67], a[228:231], a[172:175], v[64:67]\n"
"buffer_load_dwordx4 v254, s[24:27], s49 offen offset:2048 lds\n"
"v_mfma_f32_16x16x32_bf16 v[68:71], a[232:235], a[168:171], v[68:71]\n"
"v_permlane16_swap_b32 v120, v122\n"
"v_mfma_f32_16x16x32_bf16 v[68:71], a[236:239], a[172:175], v[68:71]\n"
"v_cvt_pk_bf16_f32 v115, v118, v119\n"
"v_accvgpr_write_b32 a24, v136\n"
"v_mfma_f32_16x16x32_bf16 v[72:75], a[240:243], a[168:171], v[72:75]\n"
"ds_read_b64_tr_b16 a[206:207], v245 offset:8704\n"
"v_add_u32 v181, 94208, v181\n"
"v_mfma_f32_16x16x32_bf16 v[72:75], a[244:247], a[172:175], v[72:75]\n"
"ds_read_b64_tr_b16 a[216:217], v245 offset:1024\n"
"v_permlane16_swap_b32 v112, v114\n"
"v_permlane16_swap_b32 v153, v155\n"
"v_pk_fma_f32 v[96:97], v[96:97], s[70:71], s[70:71] op_sel:[0,0,1] op_sel_hi:[1,0,1]\nv_pk_fma_f32 v[98:99], v[98:99], s[70:71], s[70:71] op_sel:[0,0,1] op_sel_hi:[1,0,1]\nv_pk_fma_f32 v[100:101], v[100:101], s[70:71], s[70:71] op_sel:[0,0,1] op_sel_hi:[1,0,1]\nv_pk_fma_f32 v[102:103], v[102:103], s[70:71], s[70:71] op_sel:[0,0,1] op_sel_hi:[1,0,1]\nv_pk_fma_f32 v[104:105], v[104:105], s[70:71], s[70:71] op_sel:[0,0,1] op_sel_hi:[1,0,1]\nv_pk_fma_f32 v[106:107], v[106:107], s[70:71], s[70:71] op_sel:[0,0,1] op_sel_hi:[1,0,1]\nv_pk_fma_f32 v[108:109], v[108:109], s[70:71], s[70:71] op_sel:[0,0,1] op_sel_hi:[1,0,1]\nv_pk_fma_f32 v[110:111], v[110:111], s[70:71], s[70:71] op_sel:[0,0,1] op_sel_hi:[1,0,1]\nv_pk_fma_f32 v[80:81], v[80:81], s[70:71], s[70:71] op_sel:[0,0,1] op_sel_hi:[1,0,1]\nv_pk_fma_f32 v[82:83], v[82:83], s[70:71], s[70:71] op_sel:[0,0,1] op_sel_hi:[1,0,1]\nv_pk_fma_f32 v[84:85], v[84:85], s[70:71], s[70:71] op_sel:[0,0,1] op_sel_hi:[1,0,1]\nv_pk_fma_f32 v[86:87], v[86:87], s[70:71], s[70:71] op_sel:[0,0,1] op_sel_hi:[1,0,1]\nv_pk_fma_f32 v[88:89], v[88:89], s[70:71], s[70:71] op_sel:[0,0,1] op_sel_hi:[1,0,1]\nv_pk_fma_f32 v[90:91], v[90:91], s[70:71], s[70:71] op_sel:[0,0,1] op_sel_hi:[1,0,1]\nv_pk_fma_f32 v[92:93], v[92:93], s[70:71], s[70:71] op_sel:[0,0,1] op_sel_hi:[1,0,1]\nv_pk_fma_f32 v[94:95], v[94:95], s[70:71], s[70:71] op_sel:[0,0,1] op_sel_hi:[1,0,1]\n"
"v_mfma_f32_16x16x32_bf16 v[76:79], a[248:251], a[168:171], v[76:79]\n"
"ds_read_b64_tr_b16 a[208:209], v244 offset:1024\n"
"v_cvt_pk_bf16_f32 v80, v80, v81\n"
"v_mfma_f32_16x16x32_bf16 v[76:79], a[252:255], a[172:175], v[76:79]\n"
"buffer_load_dwordx4 v255, s[24:27], s49 offen offset:3072 lds\n"
"v_mfma_f32_16x16x32_bf16 v[60:63], a[248:251], a[160:163], v[60:63]\n"
"ds_read_b64_tr_b16 a[192:193], v244 offset:0\n"
"ds_read_b64_tr_b16 a[222:223], v245 offset:9728\n"
"v_mfma_f32_16x16x32_bf16 v[60:63], a[252:255], a[164:167], v[60:63]\n"
"v_permlane16_swap_b32 v168, v170\n"
"v_mfma_f32_16x16x32_bf16 v[56:59], a[240:243], a[160:163], v[56:59]\n"
"v_permlane16_swap_b32 v113, v115\n"
"v_mfma_f32_16x16x32_bf16 v[56:59], a[244:247], a[164:167], v[56:59]\n"
"s_add_u32 s40, s49, 40960\n"
"ds_read_b64_tr_b16 a[210:211], v244 offset:1536\n"
"v_add_u32 v182, s75, v181\n"
"v_mfma_f32_16x16x32_bf16 v[52:55], a[232:235], a[160:163], v[52:55]\n"
"s_add_u32 m0, s51, 4096\n"
"v_cvt_pk_bf16_f32 v130, v132, v133\n"
"v_accvgpr_write_b32 a25, v137\n"
"v_mfma_f32_16x16x32_bf16 v[52:55], a[236:239], a[164:167], v[52:55]\n"
"buffer_load_dwordx4 v248, s[24:27], s40 offen lds\n"
"v_mfma_f32_16x16x32_bf16 v[48:51], a[224:227], a[160:163], v[48:51]\n"
"s_waitcnt vmcnt(63) lgkmcnt(3)\n"
"v_permlane16_swap_b32 v129, v131\n"
"v_mfma_f32_16x16x32_bf16 v[48:51], a[228:231], a[164:167], v[48:51]\n"
"s_barrier\n"
"v_mfma_f32_16x16x32_bf16 v[0:3], a[224:227], a[136:139], v[0:3]\n"
"ds_read_b128 a[36:39], v181 offset:61440\n"
"v_accvgpr_write_b32 a43, v155\n"
"v_mfma_f32_16x16x32_bf16 v[0:3], a[228:231], a[140:143], v[0:3]\n"
"ds_read_b128 a[28:31], v181 offset:49152\n"
"v_accvgpr_write_b32 a10, v122\n"
"v_mfma_f32_16x16x32_bf16 v[4:7], a[232:235], a[136:139], v[4:7]\n"
"v_cvt_pk_bf16_f32 v96, v96, v97\n"
"v_cvt_pk_bf16_f32 v104, v104, v105\n"
"v_mfma_f32_16x16x32_bf16 v[4:7], a[236:239], a[140:143], v[4:7]\n"
"buffer_load_dwordx4 v249, s[24:27], s40 offen offset:1024 lds\n"
"v_mfma_f32_16x16x32_bf16 v[8:11], a[240:243], a[136:139], v[8:11]\n"
"v_cvt_pk_bf16_f32 v97, v98, v99\n"
"v_cvt_pk_bf16_f32 v99, v102, v103\n"
"v_mfma_f32_16x16x32_bf16 v[8:11], a[244:247], a[140:143], v[8:11]\n"
"ds_read_b64_tr_b16 a[220:221], v245 offset:9216\n"
"ds_read_b128 a[12:15], v181 offset:32768\n"
"v_mfma_f32_16x16x32_bf16 v[12:15], a[248:251], a[136:139], v[12:15]\n"
"ds_read_b128 a[164:167], v253 offset:0\n"
"v_accvgpr_write_b32 a32, v144\n"
"v_mfma_f32_16x16x32_bf16 v[12:15], a[252:255], a[140:143], v[12:15]\n"
"ds_read_b128 a[140:143], v247 offset:0\n"
"v_accvgpr_write_b32 a3, v115\n"
"v_mfma_f32_16x16x32_bf16 v[28:31], a[248:251], a[144:147], v[28:31]\n"
"ds_read_b64_tr_b16 a[194:195], v244 offset:512\n"
"v_cvt_pk_bf16_f32 v162, v164, v165\n"
"v_mfma_f32_16x16x32_bf16 v[28:31], a[252:255], a[148:151], v[28:31]\n"
"buffer_load_dwordx4 v254, s[24:27], s40 offen offset:2048 lds\n"
"v_mfma_f32_16x16x32_bf16 v[24:27], a[240:243], a[144:147], v[24:27]\n"
"s_add_u32 s49, s49, 128\n"
"v_cvt_pk_bf16_f32 v147, v150, v151\n"
"v_accvgpr_write_b32 a41, v153\n"
"v_mfma_f32_16x16x32_bf16 v[24:27], a[244:247], a[148:151], v[24:27]\n"
"s_sub_u32 s41, s49, 1280\n"
"v_cvt_pk_bf16_f32 v98, v100, v101\n"
"v_cvt_pk_bf16_f32 v88, v88, v89\n"
"v_mfma_f32_16x16x32_bf16 v[20:23], a[232:235], a[144:147], v[20:23]\n"
"s_min_u32 s49, s49, s41\n"
"ds_read_b128 a[136:139], v246 offset:0\n"
"v_accvgpr_write_b32 a19, v131\n"
"v_mfma_f32_16x16x32_bf16 v[20:23], a[236:239], a[148:151], v[20:23]\n"
"s_xor_b32 s51, s51, s74\n"
"ds_read_b64_tr_b16 a[198:199], v244 offset:8704\n"
"ds_read_b128 a[44:47], v182 offset:0\n"
"v_mfma_f32_16x16x32_bf16 v[16:19], a[224:227], a[144:147], v[16:19]\n"
"ds_read_b64_tr_b16 a[212:213], v244 offset:9216\n"
"v_cvt_pk_bf16_f32 v163, v166, v167\n"
"v_mfma_f32_16x16x32_bf16 v[16:19], a[228:231], a[148:151], v[16:19]\n"
"ds_read_b128 a[148:151], v247 offset:2048\n"
"v_accvgpr_write_b32 a11, v123\n"
"v_mfma_f32_16x16x32_bf16 v[32:35], a[224:227], a[152:155], v[32:35]\n"
"ds_read_b128 a[4:7], v181 offset:0\n"
"v_and_b32 v102, 0xf0, v242\n"
"v_mfma_f32_16x16x32_bf16 v[32:35], a[228:231], a[156:159], v[32:35]\n"
"ds_read_b64_tr_b16 a[202:203], v245 offset:512\n"
"ds_read_b64_tr_b16 a[204:205], v245 offset:8192\n"
"v_mfma_f32_16x16x32_bf16 v[36:39], a[232:235], a[152:155], v[36:39]\n"
"ds_read_b64_tr_b16 a[200:201], v245 offset:0\n"
"v_cvt_pk_bf16_f32 v89, v90, v91\n"
"v_mfma_f32_16x16x32_bf16 v[36:39], a[236:239], a[156:159], v[36:39]\n"
"ds_read_b128 a[20:23], v181 offset:45056\n"
"v_accvgpr_write_b32 a27, v139\n"
"v_mfma_f32_16x16x32_bf16 v[40:43], a[240:243], a[152:155], v[40:43]\n"
"v_accvgpr_write_b32 a0, v112\n"
"v_accvgpr_write_b32 a1, v113\n"
"v_mfma_f32_16x16x32_bf16 v[40:43], a[244:247], a[156:159], v[40:43]\n"
"ds_read_b128 a[180:183], v251 offset:2048\n"
"v_accvgpr_write_b32 a2, v114\n"
"v_mfma_f32_16x16x32_bf16 v[44:47], a[248:251], a[152:155], v[44:47]\n"
"s_add_u32 s47, s47, 1\n"
"ds_read_b128 a[144:147], v246 offset:2048\n"
"v_accvgpr_write_b32 a17, v129\n"
"v_mfma_f32_16x16x32_bf16 v[44:47], a[252:255], a[156:159], v[44:47]\n"
"s_waitcnt lgkmcnt(7)\n"
"ds_read_b128 a[160:163], v252 offset:0\n"
"v_permlane16_swap_b32 v161, v163\n"
"v_permlane16_swap_b32 v169, v171\n"
"v_accvgpr_write_b32 a9, v121\n"
"v_permlane16_swap_b32 v128, v130\n"
"v_permlane16_swap_b32 v145, v147\n"
"v_accvgpr_write_b32 a42, v154\n"
"v_bfe_u32 v100, v242, 9, 1\n"
"v_cvt_pk_bf16_f32 v91, v94, v95\n"
"v_pk_fma_f32 v[0:1], v[0:1], s[70:71], s[70:71] op_sel:[0,0,1] op_sel_hi:[1,0,1]\nv_pk_fma_f32 v[2:3], v[2:3], s[70:71], s[70:71] op_sel:[0,0,1] op_sel_hi:[1,0,1]\nv_pk_fma_f32 v[4:5], v[4:5], s[70:71], s[70:71] op_sel:[0,0,1] op_sel_hi:[1,0,1]\nv_pk_fma_f32 v[6:7], v[6:7], s[70:71], s[70:71] op_sel:[0,0,1] op_sel_hi:[1,0,1]\nv_pk_fma_f32 v[8:9], v[8:9], s[70:71], s[70:71] op_sel:[0,0,1] op_sel_hi:[1,0,1]\nv_pk_fma_f32 v[10:11], v[10:11], s[70:71], s[70:71] op_sel:[0,0,1] op_sel_hi:[1,0,1]\nv_pk_fma_f32 v[12:13], v[12:13], s[70:71], s[70:71] op_sel:[0,0,1] op_sel_hi:[1,0,1]\nv_pk_fma_f32 v[14:15], v[14:15], s[70:71], s[70:71] op_sel:[0,0,1] op_sel_hi:[1,0,1]\nv_pk_fma_f32 v[16:17], v[16:17], s[70:71], s[70:71] op_sel:[0,0,1] op_sel_hi:[1,0,1]\nv_pk_fma_f32 v[18:19], v[18:19], s[70:71], s[70:71] op_sel:[0,0,1] op_sel_hi:[1,0,1]\nv_pk_fma_f32 v[20:21], v[20:21], s[70:71], s[70:71] op_sel:[0,0,1] op_sel_hi:[1,0,1]\nv_pk_fma_f32 v[22:23], v[22:23], s[70:71], s[70:71] op_sel:[0,0,1] op_sel_hi:[1,0,1]\nv_pk_fma_f32 v[24:25], v[24:25], s[70:71], s[70:71] op_sel:[0,0,1] op_sel_hi:[1,0,1]\nv_pk_fma_f32 v[26:27], v[26:27], s[70:71], s[70:71] op_sel:[0,0,1] op_sel_hi:[1,0,1]\nv_pk_fma_f32 v[28:29], v[28:29], s[70:71], s[70:71] op_sel:[0,0,1] op_sel_hi:[1,0,1]\nv_pk_fma_f32 v[30:31], v[30:31], s[70:71], s[70:71] op_sel:[0,0,1] op_sel_hi:[1,0,1]\n"
"v_cvt_pk_bf16_f32 v16, v16, v17\n"
"v_cvt_pk_bf16_f32 v24, v24, v25\n"
"v_cvt_pk_bf16_f32 v25, v26, v27\n"
"v_mfma_f32_16x16x32_bf16 v[112:115], a[192:195], a[136:139], v[228:231]\n"
"v_cvt_pk_bf16_f32 v0, v0, v1\n"
"v_cvt_pk_bf16_f32 v8, v8, v9\n"
"v_mfma_f32_16x16x32_bf16 v[112:115], a[196:199], a[140:143], v[112:115]\n"
"s_waitcnt lgkmcnt(4)\n"
"ds_write_b128 v243, v[168:171] offset:2048\n"
"v_permlane16_swap_b32 v160, v162\n"
"v_accvgpr_write_b32 a8, v120\n"
"v_cvt_pk_bf16_f32 v81, v82, v83\n"
"v_pk_fma_f32 v[64:65], v[64:65], s[70:71], s[70:71] op_sel:[0,0,1] op_sel_hi:[1,0,1]\nv_pk_fma_f32 v[66:67], v[66:67], s[70:71], s[70:71] op_sel:[0,0,1] op_sel_hi:[1,0,1]\nv_pk_fma_f32 v[68:69], v[68:69], s[70:71], s[70:71] op_sel:[0,0,1] op_sel_hi:[1,0,1]\nv_pk_fma_f32 v[70:71], v[70:71], s[70:71], s[70:71] op_sel:[0,0,1] op_sel_hi:[1,0,1]\nv_pk_fma_f32 v[72:73], v[72:73], s[70:71], s[70:71] op_sel:[0,0,1] op_sel_hi:[1,0,1]\nv_pk_fma_f32 v[74:75], v[74:75], s[70:71], s[70:71] op_sel:[0,0,1] op_sel_hi:[1,0,1]\nv_pk_fma_f32 v[76:77], v[76:77], s[70:71], s[70:71] op_sel:[0,0,1] op_sel_hi:[1,0,1]\nv_pk_fma_f32 v[78:79], v[78:79], s[70:71], s[70:71] op_sel:[0,0,1] op_sel_hi:[1,0,1]\nv_pk_fma_f32 v[48:49], v[48:49], s[70:71], s[70:71] op_sel:[0,0,1] op_sel_hi:[1,0,1]\nv_pk_fma_f32 v[50:51], v[50:51], s[70:71], s[70:71] op_sel:[0,0,1] op_sel_hi:[1,0,1]\nv_pk_fma_f32 v[52:53], v[52:53], s[70:71], s[70:71] op_sel:[0,0,1] op_sel_hi:[1,0,1]\nv_pk_fma_f32 v[54:55], v[54:55], s[70:71], s[70:71] op_sel:[0,0,1] op_sel_hi:[1,0,1]\nv_pk_fma_f32 v[56:57], v[56:57], s[70:71], s[70:71] op_sel:[0,0,1] op_sel_hi:[1,0,1]\nv_pk_fma_f32 v[58:59], v[58:59], s[70:71], s[70:71] op_sel:[0,0,1] op_sel_hi:[1,0,1]\nv_pk_fma_f32 v[60:61], v[60:61], s[70:71], s[70:71] op_sel:[0,0,1] op_sel_hi:[1,0,1]\nv_pk_fma_f32 v[62:63], v[62:63], s[70:71], s[70:71] op_sel:[0,0,1] op_sel_hi:[1,0,1]\n"
"v_cvt_pk_bf16_f32 v64, v64, v65\n"
"v_mfma_f32_16x16x32_bf16 v[116:119], a[200:203], a[136:139], v[228:231]\n"
"s_waitcnt vmcnt(11) lgkmcnt(4)\n"
"v_accvgpr_write_b32 a18, v130\n"
"v_accvgpr_write_b32 a26, v138\n"
"v_mfma_f32_16x16x32_bf16 v[116:119], a[204:207], a[140:143], v[116:119]\n"
"s_barrier\n"
"ds_write_b128 v243, v[160:163] offset:0\n"
"v_accvgpr_write_b32 a16, v128\n"
"v_accvgpr_write_b32 a33, v145\n"
"v_cvt_pk_bf16_f32 v1, v2, v3\n"
"v_pk_fma_f32 v[32:33], v[32:33], s[70:71], s[70:71] op_sel:[0,0,1] op_sel_hi:[1,0,1]\nv_pk_fma_f32 v[34:35], v[34:35], s[70:71], s[70:71] op_sel:[0,0,1] op_sel_hi:[1,0,1]\nv_pk_fma_f32 v[36:37], v[36:37], s[70:71], s[70:71] op_sel:[0,0,1] op_sel_hi:[1,0,1]\nv_pk_fma_f32 v[38:39], v[38:39], s[70:71], s[70:71] op_sel:[0,0,1] op_sel_hi:[1,0,1]\nv_pk_fma_f32 v[40:41], v[40:41], s[70:71], s[70:71] op_sel:[0,0,1] op_sel_hi:[1,0,1]\nv_pk_fma_f32 v[42:43], v[42:43], s[70:71], s[70:71] op_sel:[0,0,1] op_sel_hi:[1,0,1]\nv_pk_fma_f32 v[44:45], v[44:45], s[70:71], s[70:71] op_sel:[0,0,1] op_sel_hi:[1,0,1]\nv_pk_fma_f32 v[46:47], v[46:47], s[70:71], s[70:71] op_sel:[0,0,1] op_sel_hi:[1,0,1]\n"
"v_cvt_pk_bf16_f32 v32, v32, v33\n"
"v_mfma_f32_16x16x32_bf16 v[120:123], a[208:211], a[136:139], v[228:231]\n"
"s_add_u32 s40, s48, 256\n"
"s_xor_b32 m0, s50, 0xc000\n"
"ds_read_b128 a[152:155], v246 offset:4096\n"
"v_mfma_f32_16x16x32_bf16 v[120:123], a[212:215], a[140:143], v[120:123]\n"
"buffer_load_dwordx4 v224, s[28:31], s40 offen lds\n"
"v_mfma_f32_16x16x32_bf16 v[124:127], a[216:219], a[136:139], v[228:231]\n"
"v_accvgpr_write_b32 a40, v152\n"
"v_cvt_pk_bf16_f32 v40, v40, v41\n"
"v_mfma_f32_16x16x32_bf16 v[124:127], a[220:223], a[140:143], v[124:127]\n"
"s_waitcnt lgkmcnt(3)\n"
"ds_read_b128 a[168:171], v250 offset:0\n"
"v_cvt_pk_bf16_f32 v105, v106, v107\n"
"v_mfma_f32_16x16x32_bf16 v[140:143], a[216:219], a[144:147], v[228:231]\n"
"ds_read_b128 a[156:159], v247 offset:4096\n"
"v_accvgpr_write_b32 a34, v146\n"
"v_mfma_f32_16x16x32_bf16 v[140:143], a[220:223], a[148:151], v[140:143]\n"
"ds_read_b128 a[172:175], v251 offset:0\n"
"v_cvt_pk_bf16_f32 v2, v4, v5\n"
"v_mfma_f32_16x16x32_bf16 v[136:139], a[208:211], a[144:147], v[228:231]\n"
"ds_read_b64_tr_b16 a[224:225], v244 offset:16384\n"
"v_lshl_add_u32 v100, v100, 8, v102\n"
"v_mfma_f32_16x16x32_bf16 v[136:139], a[212:215], a[148:151], v[136:139]\n"
"buffer_load_dwordx4 v225, s[28:31], s40 offen offset:1024 lds\n"
"v_mfma_f32_16x16x32_bf16 v[132:135], a[200:203], a[144:147], v[228:231]\n"
"v_bfe_u32 v102, v242, 8, 1\n"
"v_cvt_pk_bf16_f32 v41, v42, v43\n"
"v_mfma_f32_16x16x32_bf16 v[132:135], a[204:207], a[148:151], v[132:135]\n"
"v_accvgpr_write_b32 a35, v147\n"
"v_cvt_pk_bf16_f32 v65, v66, v67\n"
"v_mfma_f32_16x16x32_bf16 v[128:131], a[192:195], a[144:147], v[228:231]\n"
"ds_read_b64_tr_b16 a[234:235], v245 offset:16896\n"
"v_cvt_pk_bf16_f32 v72, v72, v73\n"
"v_mfma_f32_16x16x32_bf16 v[128:131], a[196:199], a[148:151], v[128:131]\n"
"s_waitcnt lgkmcnt(4)\n"
"ds_read_b64_tr_b16 a[236:237], v245 offset:24576\n"
"v_bfe_u32 v101, v242, 9, 1\n"
"v_mfma_f32_16x16x32_bf16 v[144:147], a[192:195], a[152:155], v[228:231]\n"
"s_waitcnt lgkmcnt(4)\n"
"ds_read_b64_tr_b16 a[226:227], v244 offset:16896\n"
"v_cvt_pk_bf16_f32 v9, v10, v11\n"
"v_mfma_f32_16x16x32_bf16 v[144:147], a[196:199], a[156:159], v[144:147]\n"
"buffer_load_dwordx4 v226, s[28:31], s40 offen offset:2048 lds\n"
"v_mfma_f32_16x16x32_bf16 v[148:151], a[200:203], a[152:155], v[228:231]\n"
"v_cvt_pk_bf16_f32 v3, v6, v7\n"
"v_cvt_pk_bf16_f32 v17, v18, v19\n"
"v_mfma_f32_16x16x32_bf16 v[148:151], a[204:207], a[156:159], v[148:151]\n"
"v_cvt_pk_bf16_f32 v66, v68, v69\n"
"v_cvt_pk_bf16_f32 v73, v74, v75\n"
"v_mfma_f32_16x16x32_bf16 v[152:155], a[208:211], a[152:155], v[228:231]\n"
"ds_read_b64_tr_b16 a[252:253], v245 offset:25600\n"
"v_lshl_add_u32 v100, v102, 3, v100\n"
"v_mfma_f32_16x16x32_bf16 v[152:155], a[212:215], a[156:159], v[152:155]\n"
"ds_read_b64_tr_b16 a[244:245], v244 offset:25600\n"
"ds_read_b64_tr_b16 a[250:251], v245 offset:17920\n"
"v_mfma_f32_16x16x32_bf16 v[156:159], a[216:219], a[152:155], v[228:231]\n"
"v_cvt_pk_bf16_f32 v48, v48, v49\n"
"v_cvt_pk_bf16_f32 v43, v46, v47\n"
"v_mfma_f32_16x16x32_bf16 v[156:159], a[220:223], a[156:159], v[156:159]\n"
"buffer_load_dwordx4 v227, s[28:31], s40 offen offset:3072 lds\n"
"v_mfma_f32_16x16x32_bf16 v[172:175], a[216:219], a[160:163], v[228:231]\n"
"s_cmp_eq_u32 s47, 8\n"
"v_cvt_pk_bf16_f32 v75, v78, v79\n"
"v_cvt_pk_bf16_f32 v27, v30, v31\n"
"v_mfma_f32_16x16x32_bf16 v[172:175], a[220:223], a[164:167], v[172:175]\n"
"s_cselect_b32 s41, s52, s58\n"
"v_cvt_pk_bf16_f32 v56, v56, v57\n"
"v_permlane16_swap_b32 v1, v3\n"
"v_cvt_pk_bf16_f32 v11, v14, v15\n"
"v_cvt_pk_bf16_f32 v26, v28, v29\n"
"v_cvt_pk_bf16_f32 v33, v34, v35\n"
"v_mfma_f32_16x16x32_bf16 v[168:171], a[208:211], a[160:163], v[228:231]\n"
"s_add_u32 s48, s48, s41\n"
"ds_read_b128 a[176:179], v250 offset:2048\n"
"v_cvt_pk_bf16_f32 v83, v86, v87\n"
"v_mfma_f32_16x16x32_bf16 v[168:171], a[212:215], a[164:167], v[168:171]\n"
"s_sub_u32 s41, s48, 5242880\n"
"ds_read_b64_tr_b16 a[230:231], v244 offset:25088\n"
"ds_read_b64_tr_b16 a[248:249], v245 offset:17408\n"
"v_mfma_f32_16x16x32_bf16 v[164:167], a[200:203], a[160:163], v[228:231]\n"
"s_min_u32 s48, s48, s41\n"
"ds_read_b64_tr_b16 a[232:233], v245 offset:16384\n"
"v_xor_b32_e64 v102, s42, 1\n"
"v_mfma_f32_16x16x32_bf16 v[164:167], a[204:207], a[164:167], v[164:167]\n"
"ds_read_b128 a[184:187], v250 offset:4096\n"
"v_lshl_add_u32 v100, v102, 10, v100\n"
"v_mfma_f32_16x16x32_bf16 v[160:163], a[192:195], a[160:163], v[228:231]\n"
"s_mov_b32 m0, s50\n"
"v_permlane16_swap_b32 v9, v11\n"
"v_mfma_f32_16x16x32_bf16 v[160:163], a[196:199], a[164:167], v[160:163]\n"
"buffer_load_dwordx4 v224, s[28:31], s48 offen lds\n"
"v_mfma_f32_16x16x32_bf16 v[176:179], a[192:195], a[168:171], v[228:231]\n"
"s_waitcnt lgkmcnt(5)\n"
"v_cvt_pk_bf16_f32 v49, v50, v51\n"
"v_cvt_pk_bf16_f32 v34, v36, v37\n"
"v_mfma_f32_16x16x32_bf16 v[176:179], a[196:199], a[172:175], v[176:179]\n"
"v_cvt_pk_bf16_f32 v10, v12, v13\n"
"v_and_b32 v102, 0xf0, v242\n"
"v_mfma_f32_16x16x32_bf16 v[180:183], a[200:203], a[168:171], v[228:231]\n"
"ds_read_b64_tr_b16 a[240:241], v244 offset:17408\n"
"ds_read_b64_tr_b16 a[254:255], v245 offset:26112\n"
"v_mfma_f32_16x16x32_bf16 v[180:183], a[204:207], a[172:175], v[180:183]\n"
"buffer_load_dwordx4 v225, s[28:31], s48 offen offset:1024 lds\n"
"v_mfma_f32_16x16x32_bf16 v[184:187], a[208:211], a[168:171], v[228:231]\n"
"v_cvt_pk_bf16_f32 v90, v92, v93\n"
"v_cvt_pk_bf16_f32 v42, v44, v45\n"
"v_mfma_f32_16x16x32_bf16 v[184:187], a[212:215], a[172:175], v[184:187]\n"
"v_permlane16_swap_b32 v0, v2\n"
"v_mfma_f32_16x16x32_bf16 v[188:191], a[216:219], a[168:171], v[228:231]\n"
"ds_read_b128 a[188:191], v251 offset:4096\n"
"v_accvgpr_write_b32 a51, v3\n"
"v_mfma_f32_16x16x32_bf16 v[188:191], a[220:223], a[172:175], v[188:191]\n"
"s_waitcnt lgkmcnt(3)\n"
"ds_read_b64_tr_b16 a[238:239], v245 offset:25088\n"
"v_cvt_pk_bf16_f32 v67, v70, v71\n"
"v_mfma_f32_16x16x32_bf16 v[204:207], a[216:219], a[176:179], v[228:231]\n"
"ds_read_b64_tr_b16 a[246:247], v244 offset:26112\n"
"v_add_u32 v100, 94208, v100\n"
"v_mfma_f32_16x16x32_bf16 v[204:207], a[220:223], a[180:183], v[204:207]\n"
"buffer_load_dwordx4 v226, s[28:31], s48 offen offset:2048 lds\n"
"v_mfma_f32_16x16x32_bf16 v[200:203], a[208:211], a[176:179], v[228:231]\n"
"v_accvgpr_write_b32 a50, v2\n"
"v_lshl_add_u32 v101, v101, 8, v102\n"
"v_mfma_f32_16x16x32_bf16 v[200:203], a[212:215], a[180:183], v[200:203]\n"
"v_permlane16_swap_b32 v24, v26\n"
"v_mfma_f32_16x16x32_bf16 v[196:199], a[200:203], a[176:179], v[228:231]\n"
"ds_read_b64_tr_b16 a[228:229], v244 offset:24576\n"
"v_cvt_pk_bf16_f32 v106, v108, v109\n"
"v_mfma_f32_16x16x32_bf16 v[196:199], a[204:207], a[180:183], v[196:199]\n"
"ds_write2st64_b64 v100, v[96:97], v[98:99] offset0:120 offset1:121\n"
"v_accvgpr_write_b32 a49, v1\n"
"v_cvt_pk_bf16_f32 v19, v22, v23\n"
"v_permlane16_swap_b32 v25, v27\n"
"v_cvt_pk_bf16_f32 v35, v38, v39\n"
"v_mfma_f32_16x16x32_bf16 v[192:195], a[192:195], a[176:179], v[228:231]\n"
"v_cvt_pk_bf16_f32 v74, v76, v77\n"
"v_cvt_pk_bf16_f32 v18, v20, v21\n"
"v_mfma_f32_16x16x32_bf16 v[192:195], a[196:199], a[180:183], v[192:195]\n"
"buffer_load_dwordx4 v227, s[28:31], s48 offen offset:3072 lds\n"
"v_mfma_f32_16x16x32_bf16 v[208:211], a[192:195], a[184:187], v[228:231]\n"
"s_waitcnt lgkmcnt(2)\n"
"s_xor_b32 s50, s50, s74\n"
"v_permlane16_swap_b32 v33, v35\n"
"v_mfma_f32_16x16x32_bf16 v[208:211], a[196:199], a[188:191], v[208:211]\n"
"v_permlane16_swap_b32 v40, v42\n"
"v_mfma_f32_16x16x32_bf16 v[212:215], a[200:203], a[184:187], v[228:231]\n"
"v_permlane16_swap_b32 v17, v19\n"
"v_mfma_f32_16x16x32_bf16 v[212:215], a[204:207], a[188:191], v[212:215]\n"
"ds_read_b64_tr_b16 a[242:243], v244 offset:17920\n"
"v_cvt_pk_bf16_f32 v107, v110, v111\n"
"v_mfma_f32_16x16x32_bf16 v[216:219], a[208:211], a[184:187], v[228:231]\n"
"v_permlane16_swap_b32 v16, v18\n"
"v_mfma_f32_16x16x32_bf16 v[216:219], a[212:215], a[188:191], v[216:219]\n"
"ds_write2st64_b64 v100, v[104:105], v[106:107] offset0:128 offset1:129\n"
"v_cvt_pk_bf16_f32 v82, v84, v85\n"
"v_accvgpr_write_b32 a75, v27\n"
"v_permlane16_swap_b32 v41, v43\n"
"v_mfma_f32_16x16x32_bf16 v[220:223], a[216:219], a[184:187], v[228:231]\n"
"v_accvgpr_write_b32 a48, v0\n"
"v_accvgpr_write_b32 a64, v16\n"
"v_mfma_f32_16x16x32_bf16 v[220:223], a[220:223], a[188:191], v[220:223]\n"
"ds_write2st64_b64 v100, v[80:81], v[82:83] offset0:88 offset1:89\n"
"v_cvt_pk_bf16_f32 v57, v58, v59\n"
"v_cvt_pk_bf16_f32 v58, v60, v61\n"
"v_permlane16_swap_b32 v8, v10\n"
"v_accvgpr_write_b32 a57, v9\n"
"v_mfma_f32_16x16x32_bf16 v[0:3], a[224:227], a[136:139], v[228:231]\n"
"s_waitcnt lgkmcnt(3)\n"
"v_cvt_pk_bf16_f32 v50, v52, v53\n"
"v_xor_b32 v245, s74, v245\n"
"v_mfma_f32_16x16x32_bf16 v[0:3], a[228:231], a[140:143], v[0:3]\n"
"ds_write2st64_b64 v100, v[72:73], v[74:75] offset0:64 offset1:65\n"
"v_cvt_pk_bf16_f32 v59, v62, v63\n"
"v_accvgpr_write_b32 a58, v10\n"
"v_accvgpr_write_b32 a66, v18\n"
"v_accvgpr_write_b32 a81, v33\n"
"v_mfma_f32_16x16x32_bf16 v[4:7], a[232:235], a[136:139], v[228:231]\n"
"s_waitcnt vmcnt(8) lgkmcnt(3)\n"
"v_permlane16_swap_b32 v57, v59\n"
"v_mfma_f32_16x16x32_bf16 v[4:7], a[236:239], a[140:143], v[4:7]\n"
"s_barrier\n"
"ds_write2st64_b64 v100, v[88:89], v[90:91] offset0:96 offset1:97\n"
"v_accvgpr_write_b32 a56, v8\n"
"v_accvgpr_write_b32 a59, v11\n"
"v_accvgpr_write_b32 a72, v24\n"
"v_mfma_f32_16x16x32_bf16 v[8:11], a[240:243], a[136:139], v[228:231]\n"
"s_mov_b32 m0, s51\n"
"ds_read_b64_tr_b16 a[202:203], v245 offset:512\n"
"v_accvgpr_write_b32 a73, v25\n"
"v_mfma_f32_16x16x32_bf16 v[8:11], a[244:247], a[140:143], v[8:11]\n"
"buffer_load_dwordx4 v248, s[24:27], s49 offen lds\n"
"v_mfma_f32_16x16x32_bf16 v[12:15], a[248:251], a[136:139], v[228:231]\n"
"v_accvgpr_write_b32 a65, v17\n"
"v_accvgpr_write_b32 a67, v19\n"
"v_mfma_f32_16x16x32_bf16 v[12:15], a[252:255], a[140:143], v[12:15]\n"
"v_permlane16_swap_b32 v56, v58\n"
"v_mfma_f32_16x16x32_bf16 v[28:31], a[248:251], a[144:147], v[228:231]\n"
"v_accvgpr_write_b32 a74, v26\n"
"v_accvgpr_write_b32 a83, v35\n"
"v_mfma_f32_16x16x32_bf16 v[28:31], a[252:255], a[148:151], v[28:31]\n"
"ds_write2st64_b64 v100, v[64:65], v[66:67] offset0:0 offset1:1\n"
"v_permlane16_swap_b32 v32, v34\n"
"v_accvgpr_write_b32 a88, v40\n"
"v_mfma_f32_16x16x32_bf16 v[24:27], a[240:243], a[144:147], v[228:231]\n"
"v_accvgpr_write_b32 a89, v41\n"
"v_accvgpr_write_b32 a90, v42\n"
"v_mfma_f32_16x16x32_bf16 v[24:27], a[244:247], a[148:151], v[24:27]\n"
"buffer_load_dwordx4 v249, s[24:27], s49 offen offset:1024 lds\n"
"v_mfma_f32_16x16x32_bf16 v[20:23], a[232:235], a[144:147], v[228:231]\n"
"v_accvgpr_write_b32 a82, v34\n"
"v_accvgpr_write_b32 a91, v43\n"
"v_mfma_f32_16x16x32_bf16 v[20:23], a[236:239], a[148:151], v[20:23]\n"
"v_cvt_pk_bf16_f32 v51, v54, v55\n"
"v_accvgpr_write_b32 a80, v32\n"
"v_mfma_f32_16x16x32_bf16 v[16:19], a[224:227], a[144:147], v[228:231]\n"
"ds_read_b64_tr_b16 a[216:217], v245 offset:1024\n"
"v_xor_b32 v247, s74, v247\n"
"v_mfma_f32_16x16x32_bf16 v[16:19], a[228:231], a[148:151], v[16:19]\n"
"ds_write_b128 v243, v[56:59] offset:6144\n"
"v_mfma_f32_16x16x32_bf16 v[32:35], a[224:227], a[152:155], v[228:231]\n"
"v_permlane16_swap_b32 v48, v50\n"
"v_mfma_f32_16x16x32_bf16 v[32:35], a[228:231], a[156:159], v[32:35]\n"
"buffer_load_dwordx4 v254, s[24:27], s49 offen offset:2048 lds\n"
"v_mfma_f32_16x16x32_bf16 v[36:39], a[232:235], a[152:155], v[228:231]\n"
"s_waitcnt vmcnt(63) lgkmcnt(2)\n"
"v_permlane16_swap_b32 v49, v51\n"
"v_mfma_f32_16x16x32_bf16 v[36:39], a[236:239], a[156:159], v[36:39]\n"
"s_barrier\n"
"v_mfma_f32_16x16x32_bf16 v[40:43], a[240:243], a[152:155], v[228:231]\n"
"ds_read_b64_tr_b16 a[204:205], v245 offset:8192\n"
"ds_read_b128 a[140:143], v247 offset:0\n"
"v_mfma_f32_16x16x32_bf16 v[40:43], a[244:247], a[156:159], v[40:43]\n"
"ds_write_b128 v243, v[48:51] offset:4096\n"
"v_mfma_f32_16x16x32_bf16 v[44:47], a[248:251], a[152:155], v[228:231]\n"
"v_bfe_u32 v102, v242, 8, 1\n"
"v_lshl_add_u32 v101, v102, 9, v101\n"
"v_mfma_f32_16x16x32_bf16 v[44:47], a[252:255], a[156:159], v[44:47]\n"
"buffer_load_dwordx4 v255, s[24:27], s49 offen offset:3072 lds\n"
"v_mfma_f32_16x16x32_bf16 v[60:63], a[248:251], a[160:163], v[228:231]\n"
"v_xor_b32 v244, s74, v244\n"
"v_xor_b32_e64 v102, s42, 0\n"
"v_mfma_f32_16x16x32_bf16 v[60:63], a[252:255], a[164:167], v[60:63]\n"
"v_lshl_add_u32 v101, v102, 10, v101\n"
"v_add_u32 v101, 94208, v101\n"
"v_mfma_f32_16x16x32_bf16 v[56:59], a[240:243], a[160:163], v[228:231]\n"
"ds_read_b64_tr_b16 a[198:199], v244 offset:8704\n"
"ds_read_b64_tr_b16 a[220:221], v245 offset:9216\n"
"v_mfma_f32_16x16x32_bf16 v[56:59], a[244:247], a[164:167], v[56:59]\n"
"s_add_u32 s40, s49, 40960\n"
"ds_read_b128 a[52:55], v101 offset:0\n"
"v_xor_b32 v253, s74, v253\n"
"v_mfma_f32_16x16x32_bf16 v[52:55], a[232:235], a[160:163], v[228:231]\n"
"s_add_u32 m0, s51, 4096\n"
"ds_read_b64_tr_b16 a[206:207], v245 offset:8704\n"
"ds_read_b128 a[76:79], v101 offset:49152\n"
"v_mfma_f32_16x16x32_bf16 v[52:55], a[236:239], a[164:167], v[52:55]\n"
"buffer_load_dwordx4 v248, s[24:27], s40 offen lds\n"
"v_mfma_f32_16x16x32_bf16 v[48:51], a[224:227], a[160:163], v[228:231]\n"
"v_xor_b32 v246, s74, v246\n"
"v_add_u32 v102, s75, v101\n"
"v_mfma_f32_16x16x32_bf16 v[48:51], a[228:231], a[164:167], v[48:51]\n"
"ds_read_b64_tr_b16 a[192:193], v244 offset:0\n"
"ds_read_b128 a[68:71], v101 offset:45056\n"
"v_mfma_f32_16x16x32_bf16 v[64:67], a[224:227], a[168:171], v[228:231]\n"
"ds_read_b64_tr_b16 a[212:213], v244 offset:9216\n"
"ds_read_b128 a[164:167], v253 offset:0\n"
"v_mfma_f32_16x16x32_bf16 v[64:67], a[228:231], a[172:175], v[64:67]\n"
"ds_read_b64_tr_b16 a[208:209], v244 offset:1024\n"
"ds_read_b64_tr_b16 a[218:219], v245 offset:1536\n"
"v_mfma_f32_16x16x32_bf16 v[68:71], a[232:235], a[168:171], v[228:231]\n"
"ds_read_b64_tr_b16 a[200:201], v245 offset:0\n"
"ds_read_b128 a[84:87], v101 offset:61440\n"
"v_mfma_f32_16x16x32_bf16 v[68:71], a[236:239], a[172:175], v[68:71]\n"
"buffer_load_dwordx4 v249, s[24:27], s40 offen offset:1024 lds\n"
"v_mfma_f32_16x16x32_bf16 v[72:75], a[240:243], a[168:171], v[228:231]\n"
"ds_read_b64_tr_b16 a[214:215], v244 offset:9728\n"
"ds_read_b128 a[60:63], v101 offset:32768\n"
"v_mfma_f32_16x16x32_bf16 v[72:75], a[244:247], a[172:175], v[72:75]\n"
"v_xor_b32 v250, s74, v250\n"
"v_xor_b32 v251, s74, v251\n"
"v_mfma_f32_16x16x32_bf16 v[76:79], a[248:251], a[168:171], v[228:231]\n"
"ds_read_b128 a[92:95], v102 offset:0\n"
"ds_read_b128 a[168:171], v250 offset:0\n"
"v_mfma_f32_16x16x32_bf16 v[76:79], a[252:255], a[172:175], v[76:79]\n"
"ds_read_b128 a[152:155], v246 offset:4096\n"
"v_xor_b32 v252, s74, v252\n"
"v_mfma_f32_16x16x32_bf16 v[92:95], a[248:251], a[176:179], v[228:231]\n"
"ds_read_b64_tr_b16 a[222:223], v245 offset:9728\n"
"ds_read_b128 a[160:163], v252 offset:0\n"
"v_mfma_f32_16x16x32_bf16 v[92:95], a[252:255], a[180:183], v[92:95]\n"
"buffer_load_dwordx4 v254, s[24:27], s40 offen offset:2048 lds\n"
"v_mfma_f32_16x16x32_bf16 v[88:91], a[240:243], a[176:179], v[228:231]\n"
"s_add_u32 s49, s49, 128\n"
"ds_read_b128 a[144:147], v246 offset:2048\n"
"ds_read_b128 a[148:151], v247 offset:2048\n"
"v_mfma_f32_16x16x32_bf16 v[88:91], a[244:247], a[180:183], v[88:91]\n"
"s_sub_u32 s41, s49, 1280\n"
"ds_read_b128 a[136:139], v246 offset:0\n"
"v_mfma_f32_16x16x32_bf16 v[84:87], a[232:235], a[176:179], v[228:231]\n"
"s_min_u32 s49, s49, s41\n"
"ds_read_b64_tr_b16 a[196:197], v244 offset:8192\n"
"ds_read_b64_tr_b16 a[210:211], v244 offset:1536\n"
"v_mfma_f32_16x16x32_bf16 v[84:87], a[236:239], a[180:183], v[84:87]\n"
"s_xor_b32 s51, s51, s74\n"
"ds_read_b64_tr_b16 a[194:195], v244 offset:512\n"
"ds_read_b128 a[156:159], v247 offset:4096\n"
"v_mfma_f32_16x16x32_bf16 v[80:83], a[224:227], a[176:179], v[228:231]\n"
"ds_read_b128 a[176:179], v250 offset:2048\n"
"v_mfma_f32_16x16x32_bf16 v[80:83], a[228:231], a[180:183], v[80:83]\n"
"ds_read_b128 a[172:175], v251 offset:0\n"
"ds_read_b128 a[180:183], v251 offset:2048\n"
"v_mfma_f32_16x16x32_bf16 v[96:99], a[224:227], a[184:187], v[228:231]\n"
"v_mfma_f32_16x16x32_bf16 v[96:99], a[228:231], a[188:191], v[96:99]\n"
"v_mfma_f32_16x16x32_bf16 v[100:103], a[232:235], a[184:187], v[228:231]\n"
"v_mfma_f32_16x16x32_bf16 v[100:103], a[236:239], a[188:191], v[100:103]\n"
"v_mfma_f32_16x16x32_bf16 v[104:107], a[240:243], a[184:187], v[228:231]\n"
"v_mfma_f32_16x16x32_bf16 v[104:107], a[244:247], a[188:191], v[104:107]\n"
"v_mfma_f32_16x16x32_bf16 v[108:111], a[248:251], a[184:187], v[228:231]\n"
"s_add_u32 s47, s47, 1\n"
"ds_read_b128 a[184:187], v250 offset:4096\n"
"v_mfma_f32_16x16x32_bf16 v[108:111], a[252:255], a[188:191], v[108:111]\n"
"s_waitcnt lgkmcnt(1)\n"
"ds_read_b128 a[188:191], v251 offset:4096\n"
"s_mov_b32 s62, 18\n"
"s_branch LPE_%=\n"
"LPT2_%=:\n"
"v_mfma_f32_16x16x32_bf16 v[112:115], a[192:195], a[136:139], v[112:115]\n"
"s_add_u32 s78, s32, s96\ns_addc_u32 s79, s33, 0\n"
"s_mov_b64 s[80:81], 0\n"
"s_mov_b32 s82, 0\n"
"v_mfma_f32_16x16x32_bf16 v[112:115], a[196:199], a[140:143], v[112:115]\n"
"v_xor_b32 v248, 64, v240\n"
"v_mfma_f32_16x16x32_bf16 v[116:119], a[200:203], a[136:139], v[116:119]\n"
"s_waitcnt vmcnt(11)\n"
"s_load_dword s80, s[78:79], 0\n"
"v_mfma_f32_16x16x32_bf16 v[116:119], a[204:207], a[140:143], v[116:119]\n"
"s_barrier\n"
"v_mfma_f32_16x16x32_bf16 v[120:123], a[208:211], a[136:139], v[120:123]\n"
"s_add_u32 s40, s48, 256\n"
"s_xor_b32 m0, s50, 0xc000\n"
"ds_read_b64_tr_b16 a[238:239], v245 offset:25088\n"
"v_mfma_f32_16x16x32_bf16 v[120:123], a[212:215], a[140:143], v[120:123]\n"
"buffer_load_dwordx4 v224, s[28:31], s40 offen lds\n"
"v_mfma_f32_16x16x32_bf16 v[124:127], a[216:219], a[136:139], v[124:127]\n"
"v_mfma_f32_16x16x32_bf16 v[124:127], a[220:223], a[140:143], v[124:127]\n"
"v_mfma_f32_16x16x32_bf16 v[140:143], a[216:219], a[144:147], v[140:143]\n"
"ds_read_b64_tr_b16 a[244:245], v244 offset:25600\n"
"v_mfma_f32_16x16x32_bf16 v[140:143], a[220:223], a[148:151], v[140:143]\n"
"v_mfma_f32_16x16x32_bf16 v[136:139], a[208:211], a[144:147], v[136:139]\n"
"ds_read_b64_tr_b16 a[250:251], v245 offset:17920\n"
"v_bfe_i32 v241, v242, 4, 1\n"
"v_mfma_f32_16x16x32_bf16 v[136:139], a[212:215], a[148:151], v[136:139]\n"
"buffer_load_dwordx4 v225, s[28:31], s40 offen offset:1024 lds\n"
"v_mfma_f32_16x16x32_bf16 v[132:135], a[200:203], a[144:147], v[132:135]\n"
"s_add_u32 s82, s82, s80\n"
"v_mfma_f32_16x16x32_bf16 v[132:135], a[204:207], a[148:151], v[132:135]\n"
"s_add_u32 s82, s82, s80\n"
"v_mfma_f32_16x16x32_bf16 v[128:131], a[192:195], a[144:147], v[128:131]\n"
"s_add_u32 s82, s82, s80\n"
"ds_read_b64_tr_b16 a[230:231], v244 offset:25088\n"
"v_mfma_f32_16x16x32_bf16 v[128:131], a[196:199], a[148:151], v[128:131]\n"
"s_add_u32 s82, s82, s80\n"
"v_bfe_u32 v228, v242, 8, 2\n"
"v_mfma_f32_16x16x32_bf16 v[144:147], a[192:195], a[152:155], v[144:147]\n"
"s_add_u32 s82, s82, s80\n"
"ds_read_b64_tr_b16 a[242:243], v244 offset:17920\n"
"v_mfma_f32_16x16x32_bf16 v[144:147], a[196:199], a[156:159], v[144:147]\n"
"s_add_u32 s82, s82, s80\n"
"v_mfma_f32_16x16x32_bf16 v[148:151], a[200:203], a[152:155], v[148:151]\n"
"s_add_u32 s82, s82, s80\n"
"ds_read_b64_tr_b16 a[228:229], v244 offset:24576\n"
"v_mfma_f32_16x16x32_bf16 v[148:151], a[204:207], a[156:159], v[148:151]\n"
"s_add_u32 s82, s82, s80\n"
"buffer_load_dwordx4 v226, s[28:31], s40 offen offset:2048 lds\n"
"v_mfma_f32_16x16x32_bf16 v[152:155], a[208:211], a[152:155], v[152:155]\n"
"s_add_u32 s82, s82, s80\n"
"v_mfma_f32_16x16x32_bf16 v[152:155], a[212:215], a[156:159], v[152:155]\n"
"s_add_u32 s82, s82, s80\n"
"v_and_b32 v229, 0x60, v242\n"
"v_mfma_f32_16x16x32_bf16 v[156:159], a[216:219], a[152:155], v[156:159]\n"
"s_add_u32 s82, s82, s80\n"
"ds_read_b64_tr_b16 a[232:233], v245 offset:16384\n"
"v_mfma_f32_16x16x32_bf16 v[156:159], a[220:223], a[156:159], v[156:159]\n"
"s_add_u32 s82, s82, s80\n"
"v_mfma_f32_16x16x32_bf16 v[172:175], a[216:219], a[160:163], v[172:175]\n"
"s_add_u32 s82, s82, s80\n"
"ds_read_b64_tr_b16 a[234:235], v245 offset:16896\n"
"v_lshlrev_b32 v228, 4, v228\n"
"v_mfma_f32_16x16x32_bf16 v[172:175], a[220:223], a[164:167], v[172:175]\n"
"s_add_u32 s82, s82, s80\n"
"buffer_load_dwordx4 v227, s[28:31], s40 offen offset:3072 lds\n"
"v_mfma_f32_16x16x32_bf16 v[168:171], a[208:211], a[160:163], v[168:171]\n"
"s_add_u32 s82, s82, s80\n"
"s_mov_b32 s58, 122880\n"
"v_mfma_f32_16x16x32_bf16 v[168:171], a[212:215], a[164:167], v[168:171]\n"
"s_add_u32 s82, s82, s80\n"
"s_mov_b32 s47, 0\n"
"v_mfma_f32_16x16x32_bf16 v[164:167], a[200:203], a[160:163], v[164:167]\n"
"s_add_u32 s82, s82, s80\n"
"s_mov_b32 s52, 7168\n"
"ds_read_b64_tr_b16 a[246:247], v244 offset:26112\n"
"v_mfma_f32_16x16x32_bf16 v[164:167], a[204:207], a[164:167], v[164:167]\n"
"s_load_dword s81, s[78:79], 0x2000\n"
"s_mov_b32 s53, 14336\n"
"v_mfma_f32_16x16x32_bf16 v[160:163], a[192:195], a[160:163], v[160:163]\n"
"s_mov_b32 m0, s44\n"
"ds_read_b64_tr_b16 a[240:241], v244 offset:17408\n"
"v_mfma_f32_16x16x32_bf16 v[160:163], a[196:199], a[164:167], v[160:163]\n"
"buffer_load_dwordx4 v240, s[16:19], s56 offen nt lds\n"
"v_mfma_f32_16x16x32_bf16 v[176:179], a[192:195], a[168:171], v[176:179]\n"
"v_mfma_f32_16x16x32_bf16 v[176:179], a[196:199], a[172:175], v[176:179]\n"
"v_mfma_f32_16x16x32_bf16 v[180:183], a[200:203], a[168:171], v[180:183]\n"
"ds_read_b64_tr_b16 a[252:253], v245 offset:25600\n"
"v_mfma_f32_16x16x32_bf16 v[180:183], a[204:207], a[172:175], v[180:183]\n"
"v_mfma_f32_16x16x32_bf16 v[184:187], a[208:211], a[168:171], v[184:187]\n"
"ds_read_b64_tr_b16 a[236:237], v245 offset:24576\n"
"v_xor_b32 v228, v228, v229\n"
"v_mfma_f32_16x16x32_bf16 v[184:187], a[212:215], a[172:175], v[184:187]\n"
"v_mfma_f32_16x16x32_bf16 v[188:191], a[216:219], a[168:171], v[188:191]\n"
"s_add_u32 s40, s56, s52\n"
"ds_read_b64_tr_b16 a[226:227], v244 offset:16896\n"
"v_mfma_f32_16x16x32_bf16 v[188:191], a[220:223], a[172:175], v[188:191]\n"
"s_sub_u32 s82, s82, s81\n"
"buffer_load_dwordx4 v248, s[16:19], s40 offen offset:1024 nt lds\n"
"v_mfma_f32_16x16x32_bf16 v[204:207], a[216:219], a[176:179], v[204:207]\n"
"s_sub_u32 s82, s82, s81\n"
"v_mfma_f32_16x16x32_bf16 v[204:207], a[220:223], a[180:183], v[204:207]\n"
"s_sub_u32 s82, s82, s81\n"
"v_mfma_f32_16x16x32_bf16 v[200:203], a[208:211], a[176:179], v[200:203]\n"
"s_sub_u32 s82, s82, s81\n"
"ds_read_b64_tr_b16 a[248:249], v245 offset:17408\n"
"v_and_b32 v229, 0x30, v242\n"
"v_mfma_f32_16x16x32_bf16 v[200:203], a[212:215], a[180:183], v[200:203]\n"
"s_sub_u32 s82, s82, s81\n"
"v_mfma_f32_16x16x32_bf16 v[196:199], a[200:203], a[176:179], v[196:199]\n"
"s_sub_u32 s82, s82, s81\n"
"s_add_u32 s40, s56, s53\n"
"ds_read_b64_tr_b16 a[224:225], v244 offset:16384\n"
"v_mfma_f32_16x16x32_bf16 v[196:199], a[204:207], a[180:183], v[196:199]\n"
"s_sub_u32 s82, s82, s81\n"
"buffer_load_dwordx4 v240, s[16:19], s40 offen offset:2048 nt lds\n"
"v_mfma_f32_16x16x32_bf16 v[192:195], a[192:195], a[176:179], v[192:195]\n"
"s_sub_u32 s82, s82, s81\n"
"v_lshl_add_u32 v228, v229, 3, v228\n"
"v_mfma_f32_16x16x32_bf16 v[192:195], a[196:199], a[180:183], v[192:195]\n"
"s_waitcnt lgkmcnt(2)\n"
"s_sub_u32 s82, s82, s81\n"
"v_xor_b32 v244, s74, v244\n"
"v_mfma_f32_16x16x32_bf16 v[208:211], a[192:195], a[184:187], v[208:211]\n"
"s_sub_u32 s82, s82, s81\n"
"v_mfma_f32_16x16x32_bf16 v[208:211], a[196:199], a[188:191], v[208:211]\n"
"s_sub_u32 s82, s82, s81\n"
"ds_read_b64_tr_b16 a[254:255], v245 offset:26112\n"
"v_mfma_f32_16x16x32_bf16 v[212:215], a[200:203], a[184:187], v[212:215]\n"
"s_sub_u32 s82, s82, s81\n"
"s_add_u32 s40, s56, s54\n"
"v_mfma_f32_16x16x32_bf16 v[212:215], a[204:207], a[188:191], v[212:215]\n"
"s_sub_u32 s82, s82, s81\n"
"buffer_load_dwordx4 v248, s[16:19], s40 offen offset:3072 nt lds\n"
"v_mfma_f32_16x16x32_bf16 v[216:219], a[208:211], a[184:187], v[216:219]\n"
"s_sub_u32 s82, s82, s81\n"
"v_and_b32 v229, 0xc0, v242\n"
"v_xor_b32 v251, s74, v251\n"
"v_mfma_f32_16x16x32_bf16 v[216:219], a[212:215], a[188:191], v[216:219]\n"
"s_sub_u32 s82, s82, s81\n"
"v_mfma_f32_16x16x32_bf16 v[220:223], a[216:219], a[184:187], v[220:223]\n"
"s_sub_u32 s82, s82, s81\n"
"v_mfma_f32_16x16x32_bf16 v[220:223], a[220:223], a[188:191], v[220:223]\n"
"s_waitcnt lgkmcnt(1)\n"
"s_sub_u32 s82, s82, s81\n"
"v_mfma_f32_16x16x32_bf16 v[0:3], a[224:227], a[136:139], v[0:3]\n"
"v_mfma_f32_16x16x32_bf16 v[0:3], a[228:231], a[140:143], v[0:3]\n"
"v_lshl_add_u32 v228, v229, 4, v228\n"
"v_xor_b32 v245, s74, v245\n"
"v_mfma_f32_16x16x32_bf16 v[4:7], a[232:235], a[136:139], v[4:7]\n"
"s_waitcnt vmcnt(8) lgkmcnt(0)\n"
"v_mfma_f32_16x16x32_bf16 v[4:7], a[236:239], a[140:143], v[4:7]\n"
"s_barrier\n"
"v_mfma_f32_16x16x32_bf16 v[8:11], a[240:243], a[136:139], v[8:11]\n"
"s_add_u32 m0, s44, 65536\n"
"ds_read_b64_tr_b16 a[222:223], v245 offset:9728\n"
"v_mfma_f32_16x16x32_bf16 v[8:11], a[244:247], a[140:143], v[8:11]\n"
"buffer_load_dwordx4 v240, s[20:23], s56 offen nt lds\n"
"v_mfma_f32_16x16x32_bf16 v[12:15], a[248:251], a[136:139], v[12:15]\n"
"s_lshr_b32 s82, s82, 31\n"
"v_mfma_f32_16x16x32_bf16 v[12:15], a[252:255], a[140:143], v[12:15]\n"
"s_add_u32 s82, s82, s60\n"
"v_mfma_f32_16x16x32_bf16 v[28:31], a[248:251], a[144:147], v[28:31]\n"
"s_add_u32 s82, s82, 32\n"
"ds_read_b64_tr_b16 a[216:217], v245 offset:1024\n"
"v_xor_b32 v247, s74, v247\n"
"v_mfma_f32_16x16x32_bf16 v[28:31], a[252:255], a[148:151], v[28:31]\n"
"v_mov_b32 v254, s82\n"
"ds_read_b64_tr_b16 a[196:197], v244 offset:8192\n"
"v_mfma_f32_16x16x32_bf16 v[24:27], a[240:243], a[144:147], v[24:27]\n"
"v_lshlrev_b32_e64 v255, 2, s93\n"
"ds_read_b64_tr_b16 a[198:199], v244 offset:8704\n"
"v_xor_b32 v246, s74, v246\n"
"v_mfma_f32_16x16x32_bf16 v[24:27], a[244:247], a[148:151], v[24:27]\n"
"ds_read_b64_tr_b16 a[214:215], v244 offset:9728\n"
"v_mov_b32 v229, s43\n"
"v_mfma_f32_16x16x32_bf16 v[20:23], a[232:235], a[144:147], v[20:23]\n"
"s_add_u32 s40, s56, s52\n"
"ds_read_b64_tr_b16 a[204:205], v245 offset:8192\n"
"v_mfma_f32_16x16x32_bf16 v[20:23], a[236:239], a[148:151], v[20:23]\n"
"buffer_load_dwordx4 v248, s[20:23], s40 offen offset:1024 nt lds\n"
"v_mfma_f32_16x16x32_bf16 v[16:19], a[224:227], a[144:147], v[16:19]\n"
"v_mfma_f32_16x16x32_bf16 v[16:19], a[228:231], a[148:151], v[16:19]\n"
"s_mov_b64 exec, 1\n"
"global_store_dword v255, v254, s[6:7] offset:256 sc1\n"
"s_mov_b64 exec, -1\n"
"v_mfma_f32_16x16x32_bf16 v[32:35], a[224:227], a[152:155], v[32:35]\n"
"ds_read_b64_tr_b16 a[202:203], v245 offset:512\n"
"v_mfma_f32_16x16x32_bf16 v[32:35], a[228:231], a[156:159], v[32:35]\n"
"v_mfma_f32_16x16x32_bf16 v[36:39], a[232:235], a[152:155], v[36:39]\n"
"s_add_u32 s40, s56, s53\n"
"ds_read_b128 a[140:143], v247 offset:0\n"
"v_mfma_f32_16x16x32_bf16 v[36:39], a[236:239], a[156:159], v[36:39]\n"
"buffer_load_dwordx4 v240, s[20:23], s40 offen offset:2048 nt lds\n"
"v_mfma_f32_16x16x32_bf16 v[40:43], a[240:243], a[152:155], v[40:43]\n"
"v_xor_b32 v250, s74, v250\n"
"v_mfma_f32_16x16x32_bf16 v[40:43], a[244:247], a[156:159], v[40:43]\n"
"v_mfma_f32_16x16x32_bf16 v[44:47], a[248:251], a[152:155], v[44:47]\n"
"ds_read_b64_tr_b16 a[200:201], v245 offset:0\n"
"v_mul_u32_u24 v229, 0x81, v229\n"
"v_mfma_f32_16x16x32_bf16 v[44:47], a[252:255], a[156:159], v[44:47]\n"
"ds_read_b64_tr_b16 a[220:221], v245 offset:9216\n"
"v_mfma_f32_16x16x32_bf16 v[60:63], a[248:251], a[160:163], v[60:63]\n"
"s_add_u32 s40, s56, s54\n"
"ds_read_b64_tr_b16 a[210:211], v244 offset:1536\n"
"v_xor_b32 v253, s74, v253\n"
"v_mfma_f32_16x16x32_bf16 v[60:63], a[252:255], a[164:167], v[60:63]\n"
"buffer_load_dwordx4 v248, s[20:23], s40 offen offset:3072 nt lds\n"
"v_mfma_f32_16x16x32_bf16 v[56:59], a[240:243], a[160:163], v[56:59]\n"
"s_add_u32 s55, s55, s59\n"
"v_mfma_f32_16x16x32_bf16 v[56:59], a[244:247], a[164:167], v[56:59]\n"
"s_or_b32 s55, s55, s58\n"
"v_mfma_f32_16x16x32_bf16 v[52:55], a[232:235], a[160:163], v[52:55]\n"
"s_xor_b32 s56, s55, s57\n"
"ds_read_b128 a[136:139], v246 offset:0\n"
"v_mfma_f32_16x16x32_bf16 v[52:55], a[236:239], a[164:167], v[52:55]\n"
"v_mfma_f32_16x16x32_bf16 v[48:51], a[224:227], a[160:163], v[48:51]\n"
"s_add_u32 m0, s44, 16384\n"
"ds_read_b128 a[156:159], v247 offset:4096\n"
"v_mfma_f32_16x16x32_bf16 v[48:51], a[228:231], a[164:167], v[48:51]\n"
"buffer_load_dwordx4 v240, s[16:19], s56 offen nt lds\n"
"v_mfma_f32_16x16x32_bf16 v[64:67], a[224:227], a[168:171], v[64:67]\n"
"v_mfma_f32_16x16x32_bf16 v[64:67], a[228:231], a[172:175], v[64:67]\n"
"v_mfma_f32_16x16x32_bf16 v[68:71], a[232:235], a[168:171], v[68:71]\n"
"ds_read_b128 a[164:167], v253 offset:0\n"
"v_mfma_f32_16x16x32_bf16 v[68:71], a[236:239], a[172:175], v[68:71]\n"
"v_mfma_f32_16x16x32_bf16 v[72:75], a[240:243], a[168:171], v[72:75]\n"
"ds_read_b64_tr_b16 a[218:219], v245 offset:1536\n"
"v_mfma_f32_16x16x32_bf16 v[72:75], a[244:247], a[172:175], v[72:75]\n"
"ds_read_b64_tr_b16 a[194:195], v244 offset:512\n"
"v_mfma_f32_16x16x32_bf16 v[76:79], a[248:251], a[168:171], v[76:79]\n"
"s_add_u32 s40, s56, s52\n"
"ds_read_b64_tr_b16 a[206:207], v245 offset:8704\n"
"v_mfma_f32_16x16x32_bf16 v[76:79], a[252:255], a[172:175], v[76:79]\n"
"buffer_load_dwordx4 v248, s[16:19], s40 offen offset:1024 nt lds\n"
"v_mfma_f32_16x16x32_bf16 v[92:95], a[248:251], a[176:179], v[92:95]\n"
"v_mfma_f32_16x16x32_bf16 v[92:95], a[252:255], a[180:183], v[92:95]\n"
"v_mfma_f32_16x16x32_bf16 v[88:91], a[240:243], a[176:179], v[88:91]\n"
"ds_read_b64_tr_b16 a[208:209], v244 offset:1024\n"
"v_mfma_f32_16x16x32_bf16 v[88:91], a[244:247], a[180:183], v[88:91]\n"
"ds_read_b64_tr_b16 a[212:213], v244 offset:9216\n"
"v_mfma_f32_16x16x32_bf16 v[84:87], a[232:235], a[176:179], v[84:87]\n"
"s_add_u32 s40, s56, s53\n"
"ds_read_b64_tr_b16 a[192:193], v244 offset:0\n"
"v_mfma_f32_16x16x32_bf16 v[84:87], a[236:239], a[180:183], v[84:87]\n"
"buffer_load_dwordx4 v240, s[16:19], s40 offen offset:2048 nt lds\n"
"v_mfma_f32_16x16x32_bf16 v[80:83], a[224:227], a[176:179], v[80:83]\n"
"v_mfma_f32_16x16x32_bf16 v[80:83], a[228:231], a[180:183], v[80:83]\n"
"v_mfma_f32_16x16x32_bf16 v[96:99], a[224:227], a[184:187], v[96:99]\n"
"ds_read_b128 a[172:175], v251 offset:0\n"
"v_mfma_f32_16x16x32_bf16 v[96:99], a[228:231], a[188:191], v[96:99]\n"
"v_mfma_f32_16x16x32_bf16 v[100:103], a[232:235], a[184:187], v[100:103]\n"
"s_add_u32 s40, s56, s54\n"
"ds_read_b128 a[148:151], v247 offset:2048\n"
"v_mfma_f32_16x16x32_bf16 v[100:103], a[236:239], a[188:191], v[100:103]\n"
"buffer_load_dwordx4 v248, s[16:19], s40 offen offset:3072 nt lds\n"
"v_mfma_f32_16x16x32_bf16 v[104:107], a[240:243], a[184:187], v[104:107]\n"
"v_mfma_f32_16x16x32_bf16 v[104:107], a[244:247], a[188:191], v[104:107]\n"
"v_and_b32 v254, 0x20, v242\n"
"v_and_b32 v241, 0xaaaaaaaa, v241\n"
"v_mfma_f32_16x16x32_bf16 v[108:111], a[248:251], a[184:187], v[108:111]\n"
"ds_read_b128 a[168:171], v250 offset:0\n"
"v_mfma_f32_16x16x32_bf16 v[108:111], a[252:255], a[188:191], v[108:111]\n"
"s_waitcnt lgkmcnt(3)\n"
"v_mfma_f32_16x16x32_bf16 v[112:115], a[192:195], a[136:139], v[112:115]\n"
"ds_read_b128 a[184:187], v250 offset:4096\n"
"v_mfma_f32_16x16x32_bf16 v[112:115], a[196:199], a[140:143], v[112:115]\n"
"v_mfma_f32_16x16x32_bf16 v[116:119], a[200:203], a[136:139], v[116:119]\n"
"s_waitcnt vmcnt(13)\n"
"ds_read_b128 a[180:183], v251 offset:2048\n"
"v_and_b32 v249, 0xf0, v242\n"
"v_mfma_f32_16x16x32_bf16 v[116:119], a[204:207], a[140:143], v[116:119]\n"
"s_barrier\n"
"v_mfma_f32_16x16x32_bf16 v[120:123], a[208:211], a[136:139], v[120:123]\n"
"s_add_u32 m0, s44, 81920\n"
"ds_read_b128 a[176:179], v250 offset:2048\n"
"v_mfma_f32_16x16x32_bf16 v[120:123], a[212:215], a[140:143], v[120:123]\n"
"buffer_load_dwordx4 v240, s[20:23], s56 offen nt lds\n"
"v_mfma_f32_16x16x32_bf16 v[124:127], a[216:219], a[136:139], v[124:127]\n"
"v_bfe_u32 v255, v242, 9, 1\n"
"v_lshl_add_u32 v255, v255, 8, v249\n"
"v_mfma_f32_16x16x32_bf16 v[124:127], a[220:223], a[140:143], v[124:127]\n"
"s_waitcnt lgkmcnt(3)\n"
"v_xor_b32 v252, s74, v252\n"
"v_mfma_f32_16x16x32_bf16 v[188:191], a[216:219], a[168:171], v[188:191]\n"
"ds_read_b128 a[188:191], v251 offset:4096\n"
"v_xor_b32 v228, v228, v229\n"
"v_mfma_f32_16x16x32_bf16 v[188:191], a[220:223], a[172:175], v[188:191]\n"
"v_mfma_f32_16x16x32_bf16 v[184:187], a[208:211], a[168:171], v[184:187]\n"
"ds_read_b128 a[144:147], v246 offset:2048\n"
"v_add_u32 v254, -1, v254\n"
"v_mfma_f32_16x16x32_bf16 v[184:187], a[212:215], a[172:175], v[184:187]\n"
"v_mfma_f32_16x16x32_bf16 v[180:183], a[200:203], a[168:171], v[180:183]\n"
"ds_read_b64_tr_b16 a[234:235], v245 offset:16896\n"
"v_mfma_f32_16x16x32_bf16 v[180:183], a[204:207], a[172:175], v[180:183]\n"
"v_mfma_f32_16x16x32_bf16 v[176:179], a[192:195], a[168:171], v[176:179]\n"
"ds_read_b128 a[160:163], v252 offset:0\n"
"v_mfma_f32_16x16x32_bf16 v[176:179], a[196:199], a[172:175], v[176:179]\n"
"s_waitcnt lgkmcnt(4)\n"
"v_mfma_f32_16x16x32_bf16 v[192:195], a[192:195], a[176:179], v[192:195]\n"
"ds_read_b64_tr_b16 a[230:231], v244 offset:25088\n"
"v_or_b32 v241, 0x44444444, v241\n"
"v_mfma_f32_16x16x32_bf16 v[192:195], a[196:199], a[180:183], v[192:195]\n"
"v_bfe_u32 v249, v242, 8, 1\n"
"v_mfma_f32_16x16x32_bf16 v[196:199], a[200:203], a[176:179], v[196:199]\n"
"s_add_u32 s40, s56, s52\n"
"ds_read_b64_tr_b16 a[246:247], v244 offset:26112\n"
"v_mfma_f32_16x16x32_bf16 v[196:199], a[204:207], a[180:183], v[196:199]\n"
"buffer_load_dwordx4 v248, s[20:23], s40 offen offset:1024 nt lds\n"
"v_mfma_f32_16x16x32_bf16 v[200:203], a[208:211], a[176:179], v[200:203]\n"
"v_xor_b32 v229, 64, v228\n"
"v_mfma_f32_16x16x32_bf16 v[200:203], a[212:215], a[180:183], v[200:203]\n"
"v_mfma_f32_16x16x32_bf16 v[204:207], a[216:219], a[176:179], v[204:207]\n"
"ds_read_b64_tr_b16 a[240:241], v244 offset:17408\n"
"v_lshl_add_u32 v255, v249, 3, v255\n"
"v_mfma_f32_16x16x32_bf16 v[204:207], a[220:223], a[180:183], v[204:207]\n"
"v_xor_b32 v231, 0x2000, v229\n"
"v_xor_b32_e64 v249, s42, 1\n"
"v_mfma_f32_16x16x32_bf16 v[220:223], a[216:219], a[184:187], v[220:223]\n"
"s_waitcnt lgkmcnt(2)\n"
"ds_read_b64_tr_b16 a[232:233], v245 offset:16384\n"
"v_mfma_f32_16x16x32_bf16 v[220:223], a[220:223], a[188:191], v[220:223]\n"
"ds_read_b64_tr_b16 a[226:227], v244 offset:16896\n"
"v_mfma_f32_16x16x32_bf16 v[216:219], a[208:211], a[184:187], v[216:219]\n"
"ds_read_b64_tr_b16 a[224:225], v244 offset:16384\n"
"v_xor_b32 v230, 0x2000, v228\n"
"v_mfma_f32_16x16x32_bf16 v[216:219], a[212:215], a[188:191], v[216:219]\n"
"v_pk_fma_f32 v[176:177], v[176:177], s[70:71], s[70:71] op_sel:[0,0,1] op_sel_hi:[1,0,1]\nv_pk_fma_f32 v[178:179], v[178:179], s[70:71], s[70:71] op_sel:[0,0,1] op_sel_hi:[1,0,1]\nv_pk_fma_f32 v[180:181], v[180:181], s[70:71], s[70:71] op_sel:[0,0,1] op_sel_hi:[1,0,1]\nv_pk_fma_f32 v[182:183], v[182:183], s[70:71], s[70:71] op_sel:[0,0,1] op_sel_hi:[1,0,1]\nv_pk_fma_f32 v[184:185], v[184:185], s[70:71], s[70:71] op_sel:[0,0,1] op_sel_hi:[1,0,1]\nv_pk_fma_f32 v[186:187], v[186:187], s[70:71], s[70:71] op_sel:[0,0,1] op_sel_hi:[1,0,1]\nv_pk_fma_f32 v[188:189], v[188:189], s[70:71], s[70:71] op_sel:[0,0,1] op_sel_hi:[1,0,1]\nv_pk_fma_f32 v[190:191], v[190:191], s[70:71], s[70:71] op_sel:[0,0,1] op_sel_hi:[1,0,1]\nv_pk_fma_f32 v[192:193], v[192:193], s[70:71], s[70:71] op_sel:[0,0,1] op_sel_hi:[1,0,1]\nv_pk_fma_f32 v[194:195], v[194:195], s[70:71], s[70:71] op_sel:[0,0,1] op_sel_hi:[1,0,1]\nv_pk_fma_f32 v[196:197], v[196:197], s[70:71], s[70:71] op_sel:[0,0,1] op_sel_hi:[1,0,1]\nv_pk_fma_f32 v[198:199], v[198:199], s[70:71], s[70:71] op_sel:[0,0,1] op_sel_hi:[1,0,1]\nv_pk_fma_f32 v[200:201], v[200:201], s[70:71], s[70:71] op_sel:[0,0,1] op_sel_hi:[1,0,1]\nv_pk_fma_f32 v[202:203], v[202:203], s[70:71], s[70:71] op_sel:[0,0,1] op_sel_hi:[1,0,1]\nv_pk_fma_f32 v[204:205], v[204:205], s[70:71], s[70:71] op_sel:[0,0,1] op_sel_hi:[1,0,1]\nv_pk_fma_f32 v[206:207], v[206:207], s[70:71], s[70:71] op_sel:[0,0,1] op_sel_hi:[1,0,1]\n"
"v_mfma_f32_16x16x32_bf16 v[212:215], a[200:203], a[184:187], v[212:215]\n"
"ds_read_b64_tr_b16 a[252:253], v245 offset:25600\n"
"v_cvt_pk_bf16_f32 v192, v192, v193\n"
"v_mfma_f32_16x16x32_bf16 v[212:215], a[204:207], a[188:191], v[212:215]\n"
"v_cvt_pk_bf16_f32 v176, v176, v177\n"
"v_cvt_pk_bf16_f32 v193, v194, v195\n"
"v_mfma_f32_16x16x32_bf16 v[208:211], a[192:195], a[184:187], v[208:211]\n"
"s_add_u32 s40, s56, s53\n"
"ds_read_b64_tr_b16 a[248:249], v245 offset:17408\n"
"v_mfma_f32_16x16x32_bf16 v[208:211], a[196:199], a[188:191], v[208:211]\n"
"buffer_load_dwordx4 v240, s[20:23], s40 offen offset:2048 nt lds\n"
"v_mfma_f32_16x16x32_bf16 v[160:163], a[192:195], a[160:163], v[160:163]\n"
"v_cvt_pk_bf16_f32 v177, v178, v179\n"
"v_cvt_pk_bf16_f32 v184, v184, v185\n"
"v_mfma_f32_16x16x32_bf16 v[160:163], a[196:199], a[164:167], v[160:163]\n"
"v_cvt_pk_bf16_f32 v179, v182, v183\n"
"v_cvt_pk_bf16_f32 v185, v186, v187\n"
"v_mfma_f32_16x16x32_bf16 v[164:167], a[200:203], a[160:163], v[164:167]\n"
"ds_read_b64_tr_b16 a[244:245], v244 offset:25600\n"
"v_cvt_pk_bf16_f32 v200, v200, v201\n"
"v_mfma_f32_16x16x32_bf16 v[164:167], a[204:207], a[164:167], v[164:167]\n"
"v_cvt_pk_bf16_f32 v201, v202, v203\n"
"v_ashrrev_i32 v254, 31, v254\n"
"v_mfma_f32_16x16x32_bf16 v[168:171], a[208:211], a[160:163], v[168:171]\n"
"ds_read_b64_tr_b16 a[254:255], v245 offset:26112\n"
"v_cvt_pk_bf16_f32 v194, v196, v197\n"
"v_mfma_f32_16x16x32_bf16 v[168:171], a[212:215], a[164:167], v[168:171]\n"
"v_cvt_pk_bf16_f32 v202, v204, v205\n"
"v_mfma_f32_16x16x32_bf16 v[172:175], a[216:219], a[160:163], v[172:175]\n"
"ds_read_b64_tr_b16 a[242:243], v244 offset:17920\n"
"v_and_b32 v254, 0x3f803f80, v254\n"
"v_mfma_f32_16x16x32_bf16 v[172:175], a[220:223], a[164:167], v[172:175]\n"
"v_bfe_u32 v196, v242, 8, 2\n"
"v_lshl_add_u32 v196, v196, 4, s43\n"
"v_mfma_f32_16x16x32_bf16 v[140:143], a[216:219], a[144:147], v[140:143]\n"
"ds_read_b64_tr_b16 a[236:237], v245 offset:24576\n"
"v_lshl_add_u32 v255, v249, 12, v255\n"
"v_mfma_f32_16x16x32_bf16 v[140:143], a[220:223], a[148:151], v[140:143]\n"
"v_cvt_pk_bf16_f32 v203, v206, v207\n"
"v_mfma_f32_16x16x32_bf16 v[136:139], a[208:211], a[144:147], v[136:139]\n"
"ds_read_b128 a[152:155], v246 offset:4096\n"
"v_cvt_pk_bf16_f32 v178, v180, v181\n"
"v_mfma_f32_16x16x32_bf16 v[136:139], a[212:215], a[148:151], v[136:139]\n"
"v_pk_fma_f32 v[208:209], v[208:209], s[70:71], s[70:71] op_sel:[0,0,1] op_sel_hi:[1,0,1]\nv_pk_fma_f32 v[210:211], v[210:211], s[70:71], s[70:71] op_sel:[0,0,1] op_sel_hi:[1,0,1]\nv_pk_fma_f32 v[212:213], v[212:213], s[70:71], s[70:71] op_sel:[0,0,1] op_sel_hi:[1,0,1]\nv_pk_fma_f32 v[214:215], v[214:215], s[70:71], s[70:71] op_sel:[0,0,1] op_sel_hi:[1,0,1]\nv_pk_fma_f32 v[216:217], v[216:217], s[70:71], s[70:71] op_sel:[0,0,1] op_sel_hi:[1,0,1]\nv_pk_fma_f32 v[218:219], v[218:219], s[70:71], s[70:71] op_sel:[0,0,1] op_sel_hi:[1,0,1]\nv_pk_fma_f32 v[220:221], v[220:221], s[70:71], s[70:71] op_sel:[0,0,1] op_sel_hi:[1,0,1]\nv_pk_fma_f32 v[222:223], v[222:223], s[70:71], s[70:71] op_sel:[0,0,1] op_sel_hi:[1,0,1]\nv_pk_fma_f32 v[160:161], v[160:161], s[70:71], s[70:71] op_sel:[0,0,1] op_sel_hi:[1,0,1]\nv_pk_fma_f32 v[162:163], v[162:163], s[70:71], s[70:71] op_sel:[0,0,1] op_sel_hi:[1,0,1]\nv_pk_fma_f32 v[164:165], v[164:165], s[70:71], s[70:71] op_sel:[0,0,1] op_sel_hi:[1,0,1]\nv_pk_fma_f32 v[166:167], v[166:167], s[70:71], s[70:71] op_sel:[0,0,1] op_sel_hi:[1,0,1]\nv_pk_fma_f32 v[168:169], v[168:169], s[70:71], s[70:71] op_sel:[0,0,1] op_sel_hi:[1,0,1]\nv_pk_fma_f32 v[170:171], v[170:171], s[70:71], s[70:71] op_sel:[0,0,1] op_sel_hi:[1,0,1]\nv_pk_fma_f32 v[172:173], v[172:173], s[70:71], s[70:71] op_sel:[0,0,1] op_sel_hi:[1,0,1]\nv_pk_fma_f32 v[174:175], v[174:175], s[70:71], s[70:71] op_sel:[0,0,1] op_sel_hi:[1,0,1]\n"
"v_mfma_f32_16x16x32_bf16 v[132:135], a[200:203], a[144:147], v[132:135]\n"
"s_add_u32 s40, s56, s54\n"
"ds_read_b64_tr_b16 a[250:251], v245 offset:17920\n"
"v_cvt_pk_bf16_f32 v160, v160, v161\n"
"v_mfma_f32_16x16x32_bf16 v[132:135], a[204:207], a[148:151], v[132:135]\n"
"buffer_load_dwordx4 v248, s[20:23], s40 offen offset:3072 nt lds\n"
"v_mfma_f32_16x16x32_bf16 v[128:131], a[192:195], a[144:147], v[128:131]\n"
"s_add_u32 s55, s55, s59\n"
"v_cvt_pk_bf16_f32 v186, v188, v189\n"
"v_cvt_pk_bf16_f32 v216, v216, v217\n"
"v_mfma_f32_16x16x32_bf16 v[128:131], a[196:199], a[148:151], v[128:131]\n"
"s_waitcnt lgkmcnt(1)\n"
"s_or_b32 s55, s55, s58\n"
"v_cvt_pk_bf16_f32 v208, v208, v209\n"
"v_cvt_pk_bf16_f32 v161, v162, v163\n"
"v_mfma_f32_16x16x32_bf16 v[144:147], a[192:195], a[152:155], v[144:147]\n"
"s_xor_b32 s56, s55, s57\n"
"ds_read_b64_tr_b16 a[238:239], v245 offset:25088\n"
"v_cvt_pk_bf16_f32 v187, v190, v191\n"
"v_mfma_f32_16x16x32_bf16 v[144:147], a[196:199], a[156:159], v[144:147]\n"
"v_cvt_pk_bf16_f32 v217, v218, v219\n"
"v_cvt_pk_bf16_f32 v168, v168, v169\n"
"v_mfma_f32_16x16x32_bf16 v[148:151], a[200:203], a[152:155], v[148:151]\n"
"v_cvt_pk_bf16_f32 v195, v198, v199\n"
"v_xor_b32 v198, 0x400, v243\n"
"v_mfma_f32_16x16x32_bf16 v[148:151], a[204:207], a[156:159], v[148:151]\n"
"ds_read_b64_tr_b16 a[228:229], v244 offset:24576\n"
"v_cvt_pk_bf16_f32 v169, v170, v171\n"
"v_mfma_f32_16x16x32_bf16 v[152:155], a[208:211], a[152:155], v[152:155]\n"
"v_cvt_pk_bf16_f32 v163, v166, v167\n"
"v_cvt_pk_bf16_f32 v171, v174, v175\n"
"v_mfma_f32_16x16x32_bf16 v[152:155], a[212:215], a[156:159], v[152:155]\n"
"v_pk_fma_f32 v[112:113], v[112:113], s[70:71], s[70:71] op_sel:[0,0,1] op_sel_hi:[1,0,1]\nv_pk_fma_f32 v[114:115], v[114:115], s[70:71], s[70:71] op_sel:[0,0,1] op_sel_hi:[1,0,1]\nv_pk_fma_f32 v[116:117], v[116:117], s[70:71], s[70:71] op_sel:[0,0,1] op_sel_hi:[1,0,1]\nv_pk_fma_f32 v[118:119], v[118:119], s[70:71], s[70:71] op_sel:[0,0,1] op_sel_hi:[1,0,1]\nv_pk_fma_f32 v[120:121], v[120:121], s[70:71], s[70:71] op_sel:[0,0,1] op_sel_hi:[1,0,1]\nv_pk_fma_f32 v[122:123], v[122:123], s[70:71], s[70:71] op_sel:[0,0,1] op_sel_hi:[1,0,1]\nv_pk_fma_f32 v[124:125], v[124:125], s[70:71], s[70:71] op_sel:[0,0,1] op_sel_hi:[1,0,1]\nv_pk_fma_f32 v[126:127], v[126:127], s[70:71], s[70:71] op_sel:[0,0,1] op_sel_hi:[1,0,1]\nv_pk_fma_f32 v[128:129], v[128:129], s[70:71], s[70:71] op_sel:[0,0,1] op_sel_hi:[1,0,1]\nv_pk_fma_f32 v[130:131], v[130:131], s[70:71], s[70:71] op_sel:[0,0,1] op_sel_hi:[1,0,1]\nv_pk_fma_f32 v[132:133], v[132:133], s[70:71], s[70:71] op_sel:[0,0,1] op_sel_hi:[1,0,1]\nv_pk_fma_f32 v[134:135], v[134:135], s[70:71], s[70:71] op_sel:[0,0,1] op_sel_hi:[1,0,1]\nv_pk_fma_f32 v[136:137], v[136:137], s[70:71], s[70:71] op_sel:[0,0,1] op_sel_hi:[1,0,1]\nv_pk_fma_f32 v[138:139], v[138:139], s[70:71], s[70:71] op_sel:[0,0,1] op_sel_hi:[1,0,1]\nv_pk_fma_f32 v[140:141], v[140:141], s[70:71], s[70:71] op_sel:[0,0,1] op_sel_hi:[1,0,1]\nv_pk_fma_f32 v[142:143], v[142:143], s[70:71], s[70:71] op_sel:[0,0,1] op_sel_hi:[1,0,1]\n"
"v_mfma_f32_16x16x32_bf16 v[156:159], a[216:219], a[152:155], v[156:159]\n"
"v_cvt_pk_bf16_f32 v136, v136, v137\n"
"v_cvt_pk_bf16_f32 v137, v138, v139\n"
"v_mfma_f32_16x16x32_bf16 v[156:159], a[220:223], a[156:159], v[156:159]\n"
"v_xor_b32 v197, 64, v196\n"
"v_cvt_pk_bf16_f32 v120, v120, v121\n"
"v_mfma_f32_16x16x32_bf16 v[96:99], a[224:227], a[184:187], v[96:99]\n"
"s_waitcnt lgkmcnt(0)\n"
"s_add_u32 m0, s44, 32768\n"
"v_cvt_pk_bf16_f32 v209, v210, v211\n"
"v_cvt_pk_bf16_f32 v210, v212, v213\n"
"v_mfma_f32_16x16x32_bf16 v[96:99], a[228:231], a[188:191], v[96:99]\n"
"buffer_load_dwordx4 v240, s[16:19], s56 offen nt lds\n"
"v_mfma_f32_16x16x32_bf16 v[100:103], a[232:235], a[184:187], v[100:103]\n"
"s_waitcnt vmcnt(14)\n"
"v_cvt_pk_bf16_f32 v128, v128, v129\n"
"v_cvt_pk_bf16_f32 v129, v130, v131\n"
"v_mfma_f32_16x16x32_bf16 v[100:103], a[236:239], a[188:191], v[100:103]\n"
"s_barrier\n"
"v_mfma_f32_16x16x32_bf16 v[104:107], a[240:243], a[184:187], v[104:107]\n"
"ds_read_b128 a[196:199], v198 offset:0\n"
"v_cvt_pk_bf16_f32 v211, v214, v215\n"
"v_mfma_f32_16x16x32_bf16 v[104:107], a[244:247], a[188:191], v[104:107]\n"
"s_mov_b32 s74, 0x41400000\n"
"v_cvt_pk_bf16_f32 v138, v140, v141\n"
"v_cvt_pk_bf16_f32 v139, v142, v143\n"
"v_mfma_f32_16x16x32_bf16 v[108:111], a[248:251], a[184:187], v[108:111]\n"
"ds_read_b128 a[200:203], v243 offset:2048\n"
"v_cvt_pk_bf16_f32 v162, v164, v165\n"
"v_mfma_f32_16x16x32_bf16 v[108:111], a[252:255], a[188:191], v[108:111]\n"
"v_pk_fma_f32 v[144:145], v[144:145], s[70:71], s[70:71] op_sel:[0,0,1] op_sel_hi:[1,0,1]\nv_pk_fma_f32 v[146:147], v[146:147], s[70:71], s[70:71] op_sel:[0,0,1] op_sel_hi:[1,0,1]\nv_pk_fma_f32 v[148:149], v[148:149], s[70:71], s[70:71] op_sel:[0,0,1] op_sel_hi:[1,0,1]\nv_pk_fma_f32 v[150:151], v[150:151], s[70:71], s[70:71] op_sel:[0,0,1] op_sel_hi:[1,0,1]\nv_pk_fma_f32 v[152:153], v[152:153], s[70:71], s[70:71] op_sel:[0,0,1] op_sel_hi:[1,0,1]\nv_pk_fma_f32 v[154:155], v[154:155], s[70:71], s[70:71] op_sel:[0,0,1] op_sel_hi:[1,0,1]\nv_pk_fma_f32 v[156:157], v[156:157], s[70:71], s[70:71] op_sel:[0,0,1] op_sel_hi:[1,0,1]\nv_pk_fma_f32 v[158:159], v[158:159], s[70:71], s[70:71] op_sel:[0,0,1] op_sel_hi:[1,0,1]\n"
"v_mfma_f32_16x16x32_bf16 v[92:95], a[248:251], a[176:179], v[92:95]\n"
"v_permlane16_swap_b32 v161, v163\n"
"v_mfma_f32_16x16x32_bf16 v[92:95], a[252:255], a[180:183], v[92:95]\n"
"ds_write2st64_b64 v255, v[200:201], v[202:203] offset0:102 offset1:103\n"
"v_mfma_f32_16x16x32_bf16 v[88:91], a[240:243], a[176:179], v[88:91]\n"
"v_cvt_pk_bf16_f32 v144, v144, v145\n"
"v_cvt_pk_bf16_f32 v152, v152, v153\n"
"v_mfma_f32_16x16x32_bf16 v[88:91], a[244:247], a[180:183], v[88:91]\n"
"v_cvt_pk_bf16_f32 v145, v146, v147\n"
"v_cvt_pk_bf16_f32 v153, v154, v155\n"
"v_mfma_f32_16x16x32_bf16 v[84:87], a[232:235], a[176:179], v[84:87]\n"
"v_cvt_pk_bf16_f32 v130, v132, v133\n"
"v_cvt_pk_bf16_f32 v131, v134, v135\n"
"v_mfma_f32_16x16x32_bf16 v[84:87], a[236:239], a[180:183], v[84:87]\n"
"ds_write2st64_b64 v255, v[176:177], v[178:179] offset0:96 offset1:97\n"
"v_mfma_f32_16x16x32_bf16 v[80:83], a[224:227], a[176:179], v[80:83]\n"
"s_add_u32 s40, s56, s52\n"
"v_permlane16_swap_b32 v160, v162\n"
"v_mfma_f32_16x16x32_bf16 v[80:83], a[228:231], a[180:183], v[80:83]\n"
"buffer_load_dwordx4 v248, s[16:19], s40 offen offset:1024 nt lds\n"
"v_mfma_f32_16x16x32_bf16 v[64:67], a[224:227], a[168:171], v[64:67]\n"
"v_cvt_pk_bf16_f32 v218, v220, v221\n"
"v_cvt_pk_bf16_f32 v154, v156, v157\n"
"v_mfma_f32_16x16x32_bf16 v[64:67], a[228:231], a[172:175], v[64:67]\n"
"v_permlane16_swap_b32 v128, v130\n"
"v_mfma_f32_16x16x32_bf16 v[68:71], a[232:235], a[168:171], v[68:71]\n"
"ds_read_b128 v[212:215], v197 offset:0\n"
"v_cvt_pk_bf16_f32 v155, v158, v159\n"
"v_mfma_f32_16x16x32_bf16 v[68:71], a[236:239], a[172:175], v[68:71]\n"
"v_pk_fma_f32 v[96:97], v[96:97], s[70:71], s[70:71] op_sel:[0,0,1] op_sel_hi:[1,0,1]\nv_pk_fma_f32 v[98:99], v[98:99], s[70:71], s[70:71] op_sel:[0,0,1] op_sel_hi:[1,0,1]\nv_pk_fma_f32 v[100:101], v[100:101], s[70:71], s[70:71] op_sel:[0,0,1] op_sel_hi:[1,0,1]\nv_pk_fma_f32 v[102:103], v[102:103], s[70:71], s[70:71] op_sel:[0,0,1] op_sel_hi:[1,0,1]\nv_pk_fma_f32 v[104:105], v[104:105], s[70:71], s[70:71] op_sel:[0,0,1] op_sel_hi:[1,0,1]\nv_pk_fma_f32 v[106:107], v[106:107], s[70:71], s[70:71] op_sel:[0,0,1] op_sel_hi:[1,0,1]\nv_pk_fma_f32 v[108:109], v[108:109], s[70:71], s[70:71] op_sel:[0,0,1] op_sel_hi:[1,0,1]\nv_pk_fma_f32 v[110:111], v[110:111], s[70:71], s[70:71] op_sel:[0,0,1] op_sel_hi:[1,0,1]\nv_pk_fma_f32 v[80:81], v[80:81], s[70:71], s[70:71] op_sel:[0,0,1] op_sel_hi:[1,0,1]\nv_pk_fma_f32 v[82:83], v[82:83], s[70:71], s[70:71] op_sel:[0,0,1] op_sel_hi:[1,0,1]\nv_pk_fma_f32 v[84:85], v[84:85], s[70:71], s[70:71] op_sel:[0,0,1] op_sel_hi:[1,0,1]\nv_pk_fma_f32 v[86:87], v[86:87], s[70:71], s[70:71] op_sel:[0,0,1] op_sel_hi:[1,0,1]\nv_pk_fma_f32 v[88:89], v[88:89], s[70:71], s[70:71] op_sel:[0,0,1] op_sel_hi:[1,0,1]\nv_pk_fma_f32 v[90:91], v[90:91], s[70:71], s[70:71] op_sel:[0,0,1] op_sel_hi:[1,0,1]\nv_pk_fma_f32 v[92:93], v[92:93], s[70:71], s[70:71] op_sel:[0,0,1] op_sel_hi:[1,0,1]\nv_pk_fma_f32 v[94:95], v[94:95], s[70:71], s[70:71] op_sel:[0,0,1] op_sel_hi:[1,0,1]\n"
"v_mfma_f32_16x16x32_bf16 v[72:75], a[240:243], a[168:171], v[72:75]\n"
"v_permlane16_swap_b32 v137, v139\n"
"v_mfma_f32_16x16x32_bf16 v[72:75], a[244:247], a[172:175], v[72:75]\n"
"ds_write2st64_b64 v255, v[184:185], v[186:187] offset0:98 offset1:99\n"
"v_mfma_f32_16x16x32_bf16 v[76:79], a[248:251], a[168:171], v[76:79]\n"
"v_cvt_pk_bf16_f32 v121, v122, v123\n"
"v_cvt_pk_bf16_f32 v96, v96, v97\n"
"v_mfma_f32_16x16x32_bf16 v[76:79], a[252:255], a[172:175], v[76:79]\n"
"ds_read_b128 a[192:195], v243 offset:0\n"
"v_mov_b32 v199, 0\n"
"v_mfma_f32_16x16x32_bf16 v[60:63], a[248:251], a[160:163], v[60:63]\n"
"v_cvt_pk_bf16_f32 v104, v104, v105\n"
"v_cvt_pk_bf16_f32 v105, v106, v107\n"
"v_mfma_f32_16x16x32_bf16 v[60:63], a[252:255], a[164:167], v[60:63]\n"
"ds_write2st64_b64 v255, v[208:209], v[210:211] offset0:192 offset1:193\n"
"v_mfma_f32_16x16x32_bf16 v[56:59], a[240:243], a[160:163], v[56:59]\n"
"s_add_u32 s40, s56, s53\n"
"v_accvgpr_write_b32 a121, v137\n"
"v_cvt_pk_bf16_f32 v146, v148, v149\n"
"v_mfma_f32_16x16x32_bf16 v[56:59], a[244:247], a[164:167], v[56:59]\n"
"buffer_load_dwordx4 v240, s[16:19], s40 offen offset:2048 nt lds\n"
"v_mfma_f32_16x16x32_bf16 v[52:55], a[232:235], a[160:163], v[52:55]\n"
"v_cvt_pk_bf16_f32 v97, v98, v99\n"
"v_cvt_pk_bf16_f32 v88, v88, v89\n"
"v_mfma_f32_16x16x32_bf16 v[52:55], a[236:239], a[164:167], v[52:55]\n"
"v_cvt_pk_bf16_f32 v106, v108, v109\n"
"v_mov_b32 v204, 0\n"
"v_mfma_f32_16x16x32_bf16 v[48:51], a[224:227], a[160:163], v[48:51]\n"
"v_cvt_pk_bf16_f32 v219, v222, v223\n"
"v_mov_b32 v205, 0\n"
"v_mfma_f32_16x16x32_bf16 v[48:51], a[228:231], a[164:167], v[48:51]\n"
"ds_write_b128 v243, v[160:163] offset:8192\n"
"v_cvt_pk_bf16_f32 v170, v172, v173\n"
"v_mfma_f32_16x16x32_bf16 v[0:3], a[224:227], a[136:139], v[0:3]\n"
"v_permlane16_swap_b32 v153, v155\n"
"v_mfma_f32_16x16x32_bf16 v[0:3], a[228:231], a[140:143], v[0:3]\n"
"ds_write2st64_b64 v255, v[216:217], v[218:219] offset0:194 offset1:195\n"
"v_mfma_f32_16x16x32_bf16 v[4:7], a[232:235], a[136:139], v[4:7]\n"
"v_permlane16_swap_b32 v169, v171\n"
"v_mfma_f32_16x16x32_bf16 v[4:7], a[236:239], a[140:143], v[4:7]\n"
"v_pk_fma_f32 v[64:65], v[64:65], s[70:71], s[70:71] op_sel:[0,0,1] op_sel_hi:[1,0,1]\nv_pk_fma_f32 v[66:67], v[66:67], s[70:71], s[70:71] op_sel:[0,0,1] op_sel_hi:[1,0,1]\nv_pk_fma_f32 v[68:69], v[68:69], s[70:71], s[70:71] op_sel:[0,0,1] op_sel_hi:[1,0,1]\nv_pk_fma_f32 v[70:71], v[70:71], s[70:71], s[70:71] op_sel:[0,0,1] op_sel_hi:[1,0,1]\nv_pk_fma_f32 v[72:73], v[72:73], s[70:71], s[70:71] op_sel:[0,0,1] op_sel_hi:[1,0,1]\nv_pk_fma_f32 v[74:75], v[74:75], s[70:71], s[70:71] op_sel:[0,0,1] op_sel_hi:[1,0,1]\nv_pk_fma_f32 v[76:77], v[76:77], s[70:71], s[70:71] op_sel:[0,0,1] op_sel_hi:[1,0,1]\nv_pk_fma_f32 v[78:79], v[78:79], s[70:71], s[70:71] op_sel:[0,0,1] op_sel_hi:[1,0,1]\nv_pk_fma_f32 v[48:49], v[48:49], s[70:71], s[70:71] op_sel:[0,0,1] op_sel_hi:[1,0,1]\nv_pk_fma_f32 v[50:51], v[50:51], s[70:71], s[70:71] op_sel:[0,0,1] op_sel_hi:[1,0,1]\nv_pk_fma_f32 v[52:53], v[52:53], s[70:71], s[70:71] op_sel:[0,0,1] op_sel_hi:[1,0,1]\nv_pk_fma_f32 v[54:55], v[54:55], s[70:71], s[70:71] op_sel:[0,0,1] op_sel_hi:[1,0,1]\nv_pk_fma_f32 v[56:57], v[56:57], s[70:71], s[70:71] op_sel:[0,0,1] op_sel_hi:[1,0,1]\nv_pk_fma_f32 v[58:59], v[58:59], s[70:71], s[70:71] op_sel:[0,0,1] op_sel_hi:[1,0,1]\nv_pk_fma_f32 v[60:61], v[60:61], s[70:71], s[70:71] op_sel:[0,0,1] op_sel_hi:[1,0,1]\nv_pk_fma_f32 v[62:63], v[62:63], s[70:71], s[70:71] op_sel:[0,0,1] op_sel_hi:[1,0,1]\n"
"v_mfma_f32_16x16x32_bf16 v[8:11], a[240:243], a[136:139], v[8:11]\n"
"ds_read_b128 a[204:207], v198 offset:2048\n"
"v_cvt_pk_bf16_f32 v56, v56, v57\n"
"v_mfma_f32_16x16x32_bf16 v[8:11], a[244:247], a[140:143], v[8:11]\n"
"v_permlane16_swap_b32 v152, v154\n"
"v_mfma_f32_16x16x32_bf16 v[12:15], a[248:251], a[136:139], v[12:15]\n"
"v_permlane16_swap_b32 v144, v146\n"
"v_mfma_f32_16x16x32_bf16 v[12:15], a[252:255], a[140:143], v[12:15]\n"
"v_accvgpr_write_b32 a137, v153\n"
"v_cvt_pk_bf16_f32 v80, v80, v81\n"
"v_mfma_f32_16x16x32_bf16 v[28:31], a[248:251], a[144:147], v[28:31]\n"
"s_add_u32 s40, s56, s54\n"
"ds_read_b128 v[208:211], v196 offset:0\n"
"v_cvt_pk_bf16_f32 v147, v150, v151\n"
"v_mfma_f32_16x16x32_bf16 v[28:31], a[252:255], a[148:151], v[28:31]\n"
"buffer_load_dwordx4 v248, s[16:19], s40 offen offset:3072 nt lds\n"
"v_mfma_f32_16x16x32_bf16 v[24:27], a[240:243], a[144:147], v[24:27]\n"
"v_cvt_pk_bf16_f32 v107, v110, v111\n"
"v_cvt_pk_bf16_f32 v48, v48, v49\n"
"v_mfma_f32_16x16x32_bf16 v[24:27], a[244:247], a[148:151], v[24:27]\n"
"v_permlane16_swap_b32 v129, v131\n"
"v_mfma_f32_16x16x32_bf16 v[20:23], a[232:235], a[144:147], v[20:23]\n"
"v_permlane16_swap_b32 v136, v138\n"
"v_mfma_f32_16x16x32_bf16 v[20:23], a[236:239], a[148:151], v[20:23]\n"
"ds_read_b128 v[216:219], v196 offset:512\n"
"v_cvt_pk_bf16_f32 v64, v64, v65\n"
"v_mfma_f32_16x16x32_bf16 v[16:19], a[224:227], a[144:147], v[16:19]\n"
"v_cvt_pk_bf16_f32 v122, v124, v125\n"
"v_cvt_pk_bf16_f32 v72, v72, v73\n"
"v_mfma_f32_16x16x32_bf16 v[16:19], a[228:231], a[148:151], v[16:19]\n"
"v_accvgpr_write_b32 a113, v129\n"
"v_accvgpr_write_b32 a122, v138\n"
"v_mfma_f32_16x16x32_bf16 v[32:35], a[224:227], a[152:155], v[32:35]\n"
"v_cvt_pk_bf16_f32 v98, v100, v101\n"
"v_cvt_pk_bf16_f32 v65, v66, v67\n"
"v_mfma_f32_16x16x32_bf16 v[32:35], a[228:231], a[156:159], v[32:35]\n"
"ds_read_b128 v[220:223], v197 offset:512\n"
"v_accvgpr_write_b32 a138, v154\n"
"v_mfma_f32_16x16x32_bf16 v[36:39], a[232:235], a[152:155], v[36:39]\n"
"v_accvgpr_write_b32 a136, v152\n"
"v_cvt_pk_bf16_f32 v89, v90, v91\n"
"v_mfma_f32_16x16x32_bf16 v[36:39], a[236:239], a[156:159], v[36:39]\n"
"v_permlane16_swap_b32 v145, v147\n"
"v_mfma_f32_16x16x32_bf16 v[40:43], a[240:243], a[152:155], v[40:43]\n"
"v_accvgpr_write_b32 a128, v144\n"
"v_cvt_pk_bf16_f32 v99, v102, v103\n"
"v_mfma_f32_16x16x32_bf16 v[40:43], a[244:247], a[156:159], v[40:43]\n"
"ds_write2st64_b64 v255, v[192:193], v[194:195] offset0:100 offset1:101\n"
"v_pk_fma_f32 v[0:1], v[0:1], s[70:71], s[70:71] op_sel:[0,0,1] op_sel_hi:[1,0,1]\nv_pk_fma_f32 v[2:3], v[2:3], s[70:71], s[70:71] op_sel:[0,0,1] op_sel_hi:[1,0,1]\nv_pk_fma_f32 v[4:5], v[4:5], s[70:71], s[70:71] op_sel:[0,0,1] op_sel_hi:[1,0,1]\nv_pk_fma_f32 v[6:7], v[6:7], s[70:71], s[70:71] op_sel:[0,0,1] op_sel_hi:[1,0,1]\nv_pk_fma_f32 v[8:9], v[8:9], s[70:71], s[70:71] op_sel:[0,0,1] op_sel_hi:[1,0,1]\nv_pk_fma_f32 v[10:11], v[10:11], s[70:71], s[70:71] op_sel:[0,0,1] op_sel_hi:[1,0,1]\nv_pk_fma_f32 v[12:13], v[12:13], s[70:71], s[70:71] op_sel:[0,0,1] op_sel_hi:[1,0,1]\nv_pk_fma_f32 v[14:15], v[14:15], s[70:71], s[70:71] op_sel:[0,0,1] op_sel_hi:[1,0,1]\nv_pk_fma_f32 v[16:17], v[16:17], s[70:71], s[70:71] op_sel:[0,0,1] op_sel_hi:[1,0,1]\nv_pk_fma_f32 v[18:19], v[18:19], s[70:71], s[70:71] op_sel:[0,0,1] op_sel_hi:[1,0,1]\nv_pk_fma_f32 v[20:21], v[20:21], s[70:71], s[70:71] op_sel:[0,0,1] op_sel_hi:[1,0,1]\nv_pk_fma_f32 v[22:23], v[22:23], s[70:71], s[70:71] op_sel:[0,0,1] op_sel_hi:[1,0,1]\nv_pk_fma_f32 v[24:25], v[24:25], s[70:71], s[70:71] op_sel:[0,0,1] op_sel_hi:[1,0,1]\nv_pk_fma_f32 v[26:27], v[26:27], s[70:71], s[70:71] op_sel:[0,0,1] op_sel_hi:[1,0,1]\nv_pk_fma_f32 v[28:29], v[28:29], s[70:71], s[70:71] op_sel:[0,0,1] op_sel_hi:[1,0,1]\nv_pk_fma_f32 v[30:31], v[30:31], s[70:71], s[70:71] op_sel:[0,0,1] op_sel_hi:[1,0,1]\n"
"v_mfma_f32_16x16x32_bf16 v[44:47], a[248:251], a[152:155], v[44:47]\n"
"v_accvgpr_write_b32 a115, v131\n"
"v_accvgpr_write_b32 a120, v136\n"
"v_mfma_f32_16x16x32_bf16 v[44:47], a[252:255], a[156:159], v[44:47]\n"
"s_waitcnt lgkmcnt(2)\n"
"ds_write2st64_b64 v255, v[96:97], v[98:99] offset0:228 offset1:229\n"
"v_cvt_pk_bf16_f32 v81, v82, v83\n"
"v_mfma_f32_16x16x32_bf16 v[180:183], v[208:211], a[0:3], 0\n"
"v_accvgpr_write_b32 a112, v128\n"
"v_cvt_pk_bf16_f32 v91, v94, v95\n"
"v_mfma_f32_16x16x32_bf16 v[180:183], v[212:215], a[4:7], v[180:183]\n"
"v_permlane16_swap_b32 v168, v170\n"
"v_mfma_f32_16x16x32_bf16 v[180:183], v[216:219], a[8:11], v[180:183]\n"
"s_waitcnt lgkmcnt(2)\n"
"v_cvt_pk_bf16_f32 v73, v74, v75\n"
"v_cvt_pk_bf16_f32 v8, v8, v9\n"
"v_mfma_f32_16x16x32_bf16 v[180:183], v[220:223], a[12:15], v[180:183]\n"
"ds_write_b128 v243, v[168:171] offset:10240\n"
"v_pk_fma_f32 v[32:33], v[32:33], s[70:71], s[70:71] op_sel:[0,0,1] op_sel_hi:[1,0,1]\nv_pk_fma_f32 v[34:35], v[34:35], s[70:71], s[70:71] op_sel:[0,0,1] op_sel_hi:[1,0,1]\nv_pk_fma_f32 v[36:37], v[36:37], s[70:71], s[70:71] op_sel:[0,0,1] op_sel_hi:[1,0,1]\nv_pk_fma_f32 v[38:39], v[38:39], s[70:71], s[70:71] op_sel:[0,0,1] op_sel_hi:[1,0,1]\nv_pk_fma_f32 v[40:41], v[40:41], s[70:71], s[70:71] op_sel:[0,0,1] op_sel_hi:[1,0,1]\nv_pk_fma_f32 v[42:43], v[42:43], s[70:71], s[70:71] op_sel:[0,0,1] op_sel_hi:[1,0,1]\nv_pk_fma_f32 v[44:45], v[44:45], s[70:71], s[70:71] op_sel:[0,0,1] op_sel_hi:[1,0,1]\nv_pk_fma_f32 v[46:47], v[46:47], s[70:71], s[70:71] op_sel:[0,0,1] op_sel_hi:[1,0,1]\n"
"v_mfma_f32_16x16x32_bf16 v[184:187], v[208:211], a[16:19], 0\n"
"v_cvt_pk_bf16_f32 v49, v50, v51\n"
"v_cvt_pk_bf16_f32 v51, v54, v55\n"
"v_mfma_f32_16x16x32_bf16 v[184:187], v[212:215], a[20:23], v[184:187]\n"
"v_permlane16_swap_b32 v120, v122\n"
"v_mfma_f32_16x16x32_bf16 v[184:187], v[216:219], a[24:27], v[184:187]\n"
"v_cvt_pk_bf16_f32 v90, v92, v93\n"
"v_sub_f32 v183, s74, v183\n"
"v_mfma_f32_16x16x32_bf16 v[184:187], v[220:223], a[28:31], v[184:187]\n"
"ds_write2st64_b64 v255, v[104:105], v[106:107] offset0:230 offset1:231\n"
"v_cvt_pk_bf16_f32 v67, v70, v71\n"
"v_sub_f32 v182, s74, v182\n"
"v_mfma_f32_16x16x32_bf16 v[188:191], v[208:211], a[32:35], 0\n"
"v_cvt_pk_bf16_f32 v40, v40, v41\n"
"v_sub_f32 v180, s74, v180\n"
"v_mfma_f32_16x16x32_bf16 v[188:191], v[212:215], a[36:39], v[188:191]\n"
"v_accvgpr_write_b32 a139, v155\n"
"v_sub_f32 v181, s74, v181\n"
"v_mfma_f32_16x16x32_bf16 v[188:191], v[216:219], a[40:43], v[188:191]\n"
"v_cvt_pk_bf16_f32 v9, v10, v11\n"
"v_sub_f32 v186, s74, v186\n"
"v_mfma_f32_16x16x32_bf16 v[188:191], v[220:223], a[44:47], v[188:191]\n"
"v_cvt_pk_bf16_f32 v83, v86, v87\n"
"v_cvt_pk_bf16_f32 v57, v58, v59\n"
"v_mfma_f32_16x16x32_bf16 v[192:195], v[208:211], a[192:195], 0\n"
"ds_read_b128 v[208:211], v228 offset:0\n"
"v_sub_f32 v185, s74, v185\n"
"v_mfma_f32_16x16x32_bf16 v[192:195], v[212:215], a[196:199], v[192:195]\n"
"ds_read_b128 v[212:215], v229 offset:0\n"
"v_cvt_pk_bf16_f32 v41, v42, v43\n"
"v_mfma_f32_16x16x32_bf16 v[192:195], v[216:219], a[200:203], v[192:195]\n"
"ds_read_b128 v[216:219], v228 offset:512\n"
"v_sub_f32 v189, s74, v189\n"
"v_mfma_f32_16x16x32_bf16 v[192:195], v[220:223], a[204:207], v[192:195]\n"
"s_waitcnt lgkmcnt(2)\n"
"ds_read_b128 v[220:223], v229 offset:512\n"
"v_sub_f32 v184, s74, v184\n"
"v_mfma_f32_16x16x32_bf16 v[152:155], v[208:211], a[0:3], v[180:183]\n"
"s_waitcnt lgkmcnt(2)\n"
"v_cvt_pk_bf16_f32 v42, v44, v45\n"
"v_sub_f32 v187, s74, v187\n"
"v_mfma_f32_16x16x32_bf16 v[152:155], v[212:215], a[4:7], v[152:155]\n"
"s_waitcnt lgkmcnt(1)\n"
"v_accvgpr_write_b32 a129, v145\n"
"v_cvt_pk_bf16_f32 v82, v84, v85\n"
"v_mfma_f32_16x16x32_bf16 v[152:155], v[216:219], a[8:11], v[152:155]\n"
"s_waitcnt lgkmcnt(0)\n"
"v_cvt_pk_bf16_f32 v58, v60, v61\n"
"v_sub_f32 v188, s74, v188\n"
"v_mfma_f32_16x16x32_bf16 v[152:155], v[220:223], a[12:15], v[152:155]\n"
"v_cvt_pk_bf16_f32 v59, v62, v63\n"
"v_sub_f32 v190, s74, v190\n"
"v_mfma_f32_16x16x32_bf16 v[156:159], v[216:219], a[24:27], v[184:187]\n"
"v_cvt_pk_bf16_f32 v11, v14, v15\n"
"v_cvt_pk_bf16_f32 v43, v46, v47\n"
"v_mfma_f32_16x16x32_bf16 v[156:159], v[220:223], a[28:31], v[156:159]\n"
"v_sub_f32 v191, s74, v191\n"
"v_sub_f32 v192, s74, v192\n"
"v_mfma_f32_16x16x32_bf16 v[156:159], v[208:211], a[16:19], v[156:159]\n"
"v_cvt_pk_bf16_f32 v123, v126, v127\n"
"v_cvt_pk_bf16_f32 v10, v12, v13\n"
"v_mfma_f32_16x16x32_bf16 v[156:159], v[212:215], a[20:23], v[156:159]\n"
"ds_write2st64_b64 v255, v[88:89], v[90:91] offset0:226 offset1:227\n"
"v_permlane16_swap_b32 v57, v59\n"
"v_cvt_pk_bf16_f32 v16, v16, v17\n"
"v_accvgpr_write_b32 a241, v254\n"
"v_mov_b32 v196, 0\n"
"v_sub_f32 v193, s74, v193\n"
"v_exp_f32 v153, v153\n"
"v_mfma_f32_16x16x32_bf16 v[160:163], v[208:211], a[32:35], v[188:191]\n"
"v_sub_f32 v194, s74, v194\n"
"v_sub_f32 v195, s74, v195\n"
"v_mfma_f32_16x16x32_bf16 v[160:163], v[212:215], a[36:39], v[160:163]\n"
"v_exp_f32 v152, v152\n"
"v_mfma_f32_16x16x32_bf16 v[160:163], v[216:219], a[40:43], v[160:163]\n"
"v_exp_f32 v158, v158\n"
"v_mfma_f32_16x16x32_bf16 v[160:163], v[220:223], a[44:47], v[160:163]\n"
"v_exp_f32 v156, v156\n"
"v_mfma_f32_16x16x32_bf16 v[164:167], v[216:219], a[200:203], v[192:195]\n"
"ds_read_b128 v[216:219], v228 offset:4608\n"
"v_cvt_pk_bf16_f32 v74, v76, v77\n"
"v_mfma_f32_16x16x32_bf16 v[164:167], v[220:223], a[204:207], v[164:167]\n"
"ds_read_b128 v[220:223], v229 offset:4608\n"
"v_cvt_pk_bf16_f32 v66, v68, v69\n"
"v_mfma_f32_16x16x32_bf16 v[164:167], v[208:211], a[192:195], v[164:167]\n"
"ds_read_b128 v[208:211], v228 offset:4096\n"
"v_cvt_pk_bf16_f32 v24, v24, v25\n"
"v_mfma_f32_16x16x32_bf16 v[164:167], v[212:215], a[196:199], v[164:167]\n"
"s_waitcnt lgkmcnt(2)\n"
"ds_read_b128 v[212:215], v229 offset:4096\n"
"v_accvgpr_write_b32 a114, v130\n"
"v_mfma_f32_16x16x32_bf16 v[168:171], v[216:219], a[8:11], v[180:183]\n"
"s_waitcnt lgkmcnt(2)\n"
"v_exp_f32 v163, v163\n"
"v_mfma_f32_16x16x32_bf16 v[168:171], v[220:223], a[12:15], v[168:171]\n"
"s_waitcnt lgkmcnt(1)\n"
"v_exp_f32 v162, v162\n"
"v_mfma_f32_16x16x32_bf16 v[168:171], v[208:211], a[0:3], v[168:171]\n"
"s_waitcnt lgkmcnt(0)\n"
"v_exp_f32 v160, v160\n"
"v_mfma_f32_16x16x32_bf16 v[168:171], v[212:215], a[4:7], v[168:171]\n"
"ds_write2st64_b64 v255, v[80:81], v[82:83] offset0:224 offset1:225\n"
"v_permlane16_swap_b32 v121, v123\n"
"v_accvgpr_write_b32 a131, v147\n"
"v_exp_f32 v154, v154\n"
"v_exp_f32 v157, v157\n"
"v_exp_f32 v159, v159\n"
"v_exp_f32 v161, v161\n"
"v_exp_f32 v167, v167\n"
"v_mfma_f32_16x16x32_bf16 v[172:175], v[208:211], a[16:19], v[184:187]\n"
"v_exp_f32 v165, v165\n"
"v_mfma_f32_16x16x32_bf16 v[172:175], v[212:215], a[20:23], v[172:175]\n"
"v_exp_f32 v166, v166\n"
"v_mfma_f32_16x16x32_bf16 v[172:175], v[216:219], a[24:27], v[172:175]\n"
"v_exp_f32 v169, v169\n"
"v_mfma_f32_16x16x32_bf16 v[172:175], v[220:223], a[28:31], v[172:175]\n"
"v_exp_f32 v155, v155\n"
"v_mfma_f32_16x16x32_bf16 v[176:179], v[216:219], a[40:43], v[188:191]\n"
"v_cvt_pk_bf16_f32 v128, v152, v153\n"
"v_bfe_u32 v248, v242, 9, 1\n"
"v_mfma_f32_16x16x32_bf16 v[176:179], v[220:223], a[44:47], v[176:179]\n"
"v_exp_f32 v171, v171\n"
"v_mfma_f32_16x16x32_bf16 v[176:179], v[208:211], a[32:35], v[176:179]\n"
"v_cvt_pk_bf16_f32 v129, v154, v155\n"
"v_and_b32 v249, 0xf0, v242\n"
"v_mfma_f32_16x16x32_bf16 v[176:179], v[212:215], a[36:39], v[176:179]\n"
"ds_write2st64_b64 v255, v[64:65], v[66:67] offset0:196 offset1:197\n"
"v_cvt_pk_bf16_f32 v136, v156, v157\n"
"v_cvt_pk_bf16_f32 v137, v158, v159\n"
"v_cvt_pk_bf16_f32 v145, v162, v163\n"
"v_exp_f32 v170, v170\n"
"v_mfma_f32_16x16x32_bf16 v[152:155], v[208:211], a[192:195], v[192:195]\n"
"ds_read_b128 v[208:211], v230 offset:0\n"
"v_cvt_pk_bf16_f32 v125, v166, v167\n"
"v_mfma_f32_16x16x32_bf16 v[152:155], v[212:215], a[196:199], v[152:155]\n"
"ds_read_b128 v[212:215], v231 offset:0\n"
"v_accvgpr_write_b32 a123, v139\n"
"v_mfma_f32_16x16x32_bf16 v[152:155], v[216:219], a[200:203], v[152:155]\n"
"ds_read_b128 v[216:219], v230 offset:512\n"
"v_cvt_pk_bf16_f32 v50, v52, v53\n"
"v_mfma_f32_16x16x32_bf16 v[152:155], v[220:223], a[204:207], v[152:155]\n"
"s_waitcnt lgkmcnt(2)\n"
"ds_read_b128 v[220:223], v231 offset:512\n"
"v_permlane16_swap_b32 v40, v42\n"
"v_cvt_pk_bf16_f32 v144, v160, v161\n"
"v_exp_f32 v164, v164\n"
"v_exp_f32 v168, v168\n"
"v_cvt_pk_bf16_f32 v131, v170, v171\n"
"v_exp_f32 v172, v172\n"
"v_exp_f32 v173, v173\n"
"v_exp_f32 v174, v174\n"
"v_exp_f32 v176, v176\n"
"v_exp_f32 v179, v179\n"
"v_lshl_add_u32 v248, v248, 8, v249\n"
"v_mfma_f32_16x16x32_bf16 v[156:159], v[208:211], a[0:3], v[180:183]\n"
"s_waitcnt lgkmcnt(2)\n"
"v_cvt_pk_bf16_f32 v75, v78, v79\n"
"v_cvt_pk_bf16_f32 v130, v168, v169\n"
"v_mfma_f32_16x16x32_bf16 v[156:159], v[212:215], a[4:7], v[156:159]\n"
"s_waitcnt lgkmcnt(1)\n"
"v_bfe_u32 v249, v242, 8, 1\n"
"v_lshl_add_u32 v248, v249, 9, v248\n"
"v_mfma_f32_16x16x32_bf16 v[156:159], v[216:219], a[8:11], v[156:159]\n"
"s_waitcnt lgkmcnt(0)\n"
"v_exp_f32 v177, v177\n"
"v_mfma_f32_16x16x32_bf16 v[156:159], v[220:223], a[12:15], v[156:159]\n"
"v_mov_b32 v202, 0\n"
"v_cvt_pk_bf16_f32 v124, v164, v165\n"
"v_mfma_f32_16x16x32_bf16 v[160:163], v[216:219], a[24:27], v[184:187]\n"
"v_exp_f32 v155, v155\n"
"v_mfma_f32_16x16x32_bf16 v[160:163], v[220:223], a[28:31], v[160:163]\n"
"v_permlane16_swap_b32 v49, v51\n"
"v_mfma_f32_16x16x32_bf16 v[160:163], v[208:211], a[16:19], v[160:163]\n"
"v_mov_b32 v201, 0\n"
"v_xor_b32_e64 v249, s42, 0\n"
"v_mfma_f32_16x16x32_bf16 v[160:163], v[212:215], a[20:23], v[160:163]\n"
"ds_write2st64_b64 v255, v[72:73], v[74:75] offset0:198 offset1:199\n"
"v_permlane16_swap_b32 v48, v50\n"
"v_permlane16_swap_b32 v56, v58\n"
"v_cvt_pk_bf16_f32 v25, v26, v27\n"
"v_cvt_pk_bf16_f32 v138, v172, v173\n"
"v_exp_f32 v153, v153\n"
"v_exp_f32 v157, v157\n"
"v_mfma_f32_16x16x32_bf16 v[164:167], v[208:211], a[32:35], v[188:191]\n"
"ds_read_b128 v[208:211], v230 offset:4096\n"
"v_mov_b32 v197, 0\n"
"v_mfma_f32_16x16x32_bf16 v[164:167], v[212:215], a[36:39], v[164:167]\n"
"ds_read_b128 v[212:215], v231 offset:4096\n"
"v_accvgpr_write_b32 a243, v254\n"
"v_mfma_f32_16x16x32_bf16 v[164:167], v[216:219], a[40:43], v[164:167]\n"
"ds_read_b128 v[216:219], v230 offset:4608\n"
"v_lshl_add_u32 v248, v249, 12, v248\n"
"v_mfma_f32_16x16x32_bf16 v[164:167], v[220:223], a[44:47], v[164:167]\n"
"s_waitcnt lgkmcnt(2)\n"
"ds_read_b128 v[220:223], v231 offset:4608\n"
"v_accvgpr_write_b32 a104, v120\n"
"v_permlane16_swap_b32 v8, v10\n"
"v_permlane16_swap_b32 v9, v11\n"
"v_accvgpr_write_b32 a240, v254\n"
"v_exp_f32 v175, v175\n"
"v_exp_f32 v178, v178\n"
"v_exp_f32 v159, v159\n"
"v_exp_f32 v160, v160\n"
"v_exp_f32 v163, v163\n"
"v_mfma_f32_16x16x32_bf16 v[168:171], v[208:211], a[0:3], v[180:183]\n"
"s_waitcnt lgkmcnt(2)\n"
"v_exp_f32 v154, v154\n"
"v_mfma_f32_16x16x32_bf16 v[168:171], v[212:215], a[4:7], v[168:171]\n"
"s_waitcnt lgkmcnt(1)\n"
"v_accvgpr_write_b32 a186, v42\n"
"v_add_u32 v249, s75, v248\n"
"v_mfma_f32_16x16x32_bf16 v[168:171], v[216:219], a[8:11], v[168:171]\n"
"s_waitcnt lgkmcnt(0)\n"
"v_exp_f32 v152, v152\n"
"v_mfma_f32_16x16x32_bf16 v[168:171], v[220:223], a[12:15], v[168:171]\n"
"ds_write_b128 v243, v[48:51] offset:12288\n"
"v_accvgpr_write_b32 a130, v146\n"
"v_permlane16_swap_b32 v41, v43\n"
"v_cvt_pk_bf16_f32 v139, v174, v175\n"
"v_exp_f32 v164, v164\n"
"v_mfma_f32_16x16x32_bf16 v[172:175], v[216:219], a[24:27], v[184:187]\n"
"v_exp_f32 v156, v156\n"
"v_mfma_f32_16x16x32_bf16 v[172:175], v[220:223], a[28:31], v[172:175]\n"
"v_exp_f32 v158, v158\n"
"v_mfma_f32_16x16x32_bf16 v[172:175], v[208:211], a[16:19], v[172:175]\n"
"v_exp_f32 v168, v168\n"
"v_mfma_f32_16x16x32_bf16 v[172:175], v[212:215], a[20:23], v[172:175]\n"
"s_cbranch_vccnz LPCE0_%=\nLPKE0_%=:\n"
"ds_write_b128 v243, v[56:59] offset:14336\n"
"v_accvgpr_write_b32 a185, v41\n"
"v_cvt_pk_bf16_f32 v146, v176, v177\n"
"v_cvt_pk_bf16_f32 v147, v178, v179\n"
"v_cvt_pk_bf16_f32 v126, v152, v153\n"
"v_cvt_pk_bf16_f32 v133, v158, v159\n"
"v_exp_f32 v165, v165\n"
"v_mfma_f32_16x16x32_bf16 v[176:179], v[208:211], a[32:35], v[188:191]\n"
"v_cvt_pk_bf16_f32 v127, v154, v155\n"
"v_cvt_pk_bf16_f32 v132, v156, v157\n"
"v_mfma_f32_16x16x32_bf16 v[176:179], v[212:215], a[36:39], v[176:179]\n"
"v_exp_f32 v161, v161\n"
"v_mfma_f32_16x16x32_bf16 v[176:179], v[216:219], a[40:43], v[176:179]\n"
"v_cvt_pk_bf16_f32 v17, v18, v19\n"
"v_accvgpr_write_b32 a187, v43\n"
"v_mfma_f32_16x16x32_bf16 v[176:179], v[220:223], a[44:47], v[176:179]\n"
"v_cvt_pk_bf16_f32 v140, v160, v161\n"
"v_exp_f32 v162, v162\n"
"v_exp_f32 v167, v167\n"
"v_exp_f32 v169, v169\n"
"v_exp_f32 v172, v172\n"
"v_exp_f32 v173, v173\n"
"v_exp_f32 v174, v174\n"
"s_waitcnt vmcnt(8) lgkmcnt(0)\n"
"s_barrier\n"
"ds_read_b128 v[208:211], v228 offset:16384\n"
"ds_read_b128 v[212:215], v229 offset:16384\n"
"ds_read_b128 v[216:219], v228 offset:16896\n"
"ds_read_b128 v[220:223], v229 offset:16896\n"
"s_waitcnt lgkmcnt(3)\n"
"v_mfma_f32_16x16x32_bf16 v[152:155], v[208:211], a[0:3], v[180:183]\n"
"v_accvgpr_write_b32 a153, v9\n"
"v_cvt_pk_bf16_f32 v32, v32, v33\n"
"s_waitcnt lgkmcnt(2)\n"
"v_mfma_f32_16x16x32_bf16 v[152:155], v[212:215], a[4:7], v[152:155]\n"
"ds_read_b64_tr_b16 v[224:225], v239 offset:0\n"
"v_cvt_pk_bf16_f32 v141, v162, v163\n"
"s_waitcnt lgkmcnt(2)\n"
"v_mfma_f32_16x16x32_bf16 v[152:155], v[216:219], a[8:11], v[152:155]\n"
"s_waitcnt lgkmcnt(1)\n"
"ds_read_b128 a[188:191], v249 offset:52224\n"
"v_mov_b32 v203, 0\n"
"v_mfma_f32_16x16x32_bf16 v[152:155], v[220:223], a[12:15], v[152:155]\n"
"ds_read_b128 a[116:119], v248 offset:51200\n"
"v_cvt_pk_bf16_f32 v27, v30, v31\n"
"v_mfma_f32_16x16x32_bf16 v[156:159], v[216:219], a[24:27], v[184:187]\n"
"ds_read_b64_tr_b16 v[226:227], v239 offset:4096\n"
"ds_read_b128 a[180:183], v249 offset:51200\n"
"v_mfma_f32_16x16x32_bf16 v[156:159], v[220:223], a[28:31], v[156:159]\n"
"v_accvgpr_write_b32 a154, v10\n"
"v_accvgpr_write_b32 a184, v40\n"
"v_mfma_f32_16x16x32_bf16 v[156:159], v[208:211], a[16:19], v[156:159]\n"
"ds_read_b64_tr_b16 a[244:245], v234 offset:0\n"
"ds_read_b64_tr_b16 a[246:247], v234 offset:4096\n"
"v_mfma_f32_16x16x32_bf16 v[156:159], v[212:215], a[20:23], v[156:159]\n"
"ds_read_b128 a[100:103], v248 offset:49152\n"
"v_exp_f32 v166, v166\n"
"v_exp_f32 v175, v175\n"
"v_exp_f32 v176, v176\n"
"v_exp_f32 v177, v177\n"
"v_exp_f32 v154, v154\n"
"v_mfma_f32_16x16x32_bf16 v[160:163], v[208:211], a[32:35], v[188:191]\n"
"v_mov_b32 v198, 0\n"
"v_cvt_pk_bf16_f32 v148, v164, v165\n"
"v_mfma_f32_16x16x32_bf16 v[160:163], v[212:215], a[36:39], v[160:163]\n"
"ds_read_b64_tr_b16 a[250:251], v238 offset:4096\n"
"ds_read_b64_tr_b16 a[254:255], v235 offset:4096\n"
"v_mfma_f32_16x16x32_bf16 v[160:163], v[216:219], a[40:43], v[160:163]\n"
"ds_read_b64_tr_b16 a[248:249], v238 offset:0\n"
"ds_read_b128 a[156:159], v249 offset:35840\n"
"v_mfma_f32_16x16x32_bf16 v[160:163], v[220:223], a[44:47], v[160:163]\n"
"ds_read_b128 a[172:175], v249 offset:50176\n"
"v_accvgpr_write_b32 a155, v11\n"
"v_cvt_pk_bf16_f32 v149, v166, v167\n"
"v_cvt_pk_bf16_f32 v134, v168, v169\n"
"v_exp_f32 v170, v170\n"
"v_exp_f32 v171, v171\n"
"v_cvt_pk_bf16_f32 v143, v174, v175\n"
"v_exp_f32 v179, v179\n"
"v_mfma_f32_16x16x32_bf16 v[164:167], v[216:219], a[200:203], v[192:195]\n"
"ds_read_b128 a[164:167], v249 offset:49152\n"
"v_cvt_pk_bf16_f32 v142, v172, v173\n"
"v_mfma_f32_16x16x32_bf16 v[164:167], v[220:223], a[204:207], v[164:167]\n"
"ds_read_b64_tr_b16 a[252:253], v235 offset:0\n"
"v_cvt_pk_bf16_f32 v112, v112, v113\n"
"v_mfma_f32_16x16x32_bf16 v[164:167], v[208:211], a[192:195], v[164:167]\n"
"ds_read_b128 a[140:143], v249 offset:33792\n"
"v_cvt_pk_bf16_f32 v0, v0, v1\n"
"v_mfma_f32_16x16x32_bf16 v[164:167], v[212:215], a[196:199], v[164:167]\n"
"s_waitcnt lgkmcnt(8)\n"
"ds_read_b128 a[148:151], v249 offset:34816\n"
"v_accvgpr_write_b32 a152, v8\n"
"v_cvt_pk_bf16_f32 v33, v34, v35\n"
"v_cvt_pk_bf16_f32 v135, v170, v171\n"
"v_exp_f32 v178, v178\n"
"v_xor_b32 v207, 64, v240\n"
"v_exp_f32 v153, v153\n"
"v_exp_f32 v162, v162\n"
"v_mfma_f32_16x16x32_bf16 a[216:219], v[124:127], a[244:247], 0\n"
"ds_read_b128 a[108:111], v248 offset:50176\n"
"v_cvt_pk_bf16_f32 v1, v2, v3\n"
"v_mfma_f32_16x16x32_bf16 v[8:11], v[128:131], a[244:247], 0\n"
"s_waitcnt lgkmcnt(7)\n"
"ds_read_b128 a[124:127], v248 offset:52224\n"
"v_cvt_pk_bf16_f32 v34, v36, v37\n"
"v_mfma_f32_16x16x32_bf16 v[8:11], v[132:135], a[248:251], v[8:11]\n"
"ds_read_b128 v[216:219], v228 offset:20992\n"
"v_cvt_pk_bf16_f32 v26, v28, v29\n"
"v_mfma_f32_16x16x32_bf16 v[40:43], v[136:139], a[244:247], 0\n"
"ds_read_b128 a[132:135], v249 offset:32768\n"
"v_cvt_pk_bf16_f32 v150, v176, v177\n"
"v_mfma_f32_16x16x32_bf16 v[40:43], v[140:143], a[248:251], v[40:43]\n"
"v_cvt_pk_bf16_f32 v3, v6, v7\n"
"v_cvt_pk_bf16_f32 v151, v178, v179\n"
"v_mfma_f32_16x16x32_bf16 v[72:75], v[144:147], a[244:247], 0\n"
"ds_read_b128 v[208:211], v228 offset:20480\n"
"v_accvgpr_write_b32 a242, v254\n"
"v_mfma_f32_16x16x32_bf16 v[72:75], v[148:151], a[248:251], v[72:75]\n"
"ds_read_b128 v[220:223], v229 offset:20992\n"
"v_cvt_pk_bf16_f32 v113, v114, v115\n"
"v_mfma_f32_16x16x32_bf16 v[76:79], v[148:151], v[224:227], 0\n"
"s_waitcnt lgkmcnt(6)\n"
"v_exp_f32 v159, v159\n"
"v_mfma_f32_16x16x32_bf16 v[76:79], v[144:147], a[252:255], v[76:79]\n"
"v_permlane16_swap_b32 v24, v26\n"
"v_mfma_f32_16x16x32_bf16 v[44:47], v[140:143], v[224:227], 0\n"
"v_permlane16_swap_b32 v32, v34\n"
"v_mfma_f32_16x16x32_bf16 v[44:47], v[136:139], a[252:255], v[44:47]\n"
"ds_read_b128 v[212:215], v229 offset:20480\n"
"v_permlane16_swap_b32 v25, v27\n"
"v_cvt_pk_bf16_f32 v35, v38, v39\n"
"v_exp_f32 v155, v155\n"
"v_exp_f32 v160, v160\n"
"v_exp_f32 v167, v167\n"
"v_mfma_f32_16x16x32_bf16 v[12:15], v[132:135], v[224:227], 0\n"
"v_exp_f32 v164, v164\n"
"v_mfma_f32_16x16x32_bf16 v[12:15], v[128:131], a[252:255], v[12:15]\n"
"v_exp_f32 v166, v166\n"
"v_mfma_f32_16x16x32_bf16 a[220:223], v[124:127], a[252:255], 0\n"
"s_waitcnt lgkmcnt(2)\n"
"ds_read_b64_tr_b16 a[254:255], v235 offset:4608\n"
"v_accvgpr_write_b32 a169, v25\n"
"v_smfmac_f32_16x16x64_bf16 v[196:199], a[240:243], v[128:135], v241\n"
"s_add_u32 m0, s44, 98304\n"
"ds_read_b64_tr_b16 v[224:225], v239 offset:512\n"
"v_cvt_pk_bf16_f32 v97, v154, v155\n"
"v_mfma_f32_16x16x32_bf16 v[202:205], a[240:243], v[124:127], v[202:205]\n"
"buffer_load_dwordx4 v240, s[20:23], s56 offen nt lds\n"
"v_mfma_f32_16x16x32_bf16 v[168:171], v[216:219], a[8:11], v[180:183]\n"
"s_waitcnt lgkmcnt(3)\n"
"v_exp_f32 v152, v152\n"
"v_mfma_f32_16x16x32_bf16 v[168:171], v[220:223], a[12:15], v[168:171]\n"
"ds_read_b64_tr_b16 a[252:253], v235 offset:512\n"
"v_accvgpr_write_b32 a168, v24\n"
"v_mfma_f32_16x16x32_bf16 v[168:171], v[208:211], a[0:3], v[168:171]\n"
"s_waitcnt lgkmcnt(3)\n"
"ds_read_b64_tr_b16 a[244:245], v234 offset:512\n"
"v_cvt_pk_bf16_f32 v96, v152, v153\n"
"v_mfma_f32_16x16x32_bf16 v[168:171], v[212:215], a[4:7], v[168:171]\n"
"ds_read_b64_tr_b16 a[248:249], v238 offset:512\n"
"ds_read_b64_tr_b16 a[250:251], v238 offset:4608\n"
"v_mfma_f32_16x16x32_bf16 v[172:175], v[208:211], a[16:19], v[184:187]\n"
"ds_read_b64_tr_b16 a[246:247], v234 offset:4608\n"
"v_cvt_pk_bf16_f32 v115, v118, v119\n"
"v_mfma_f32_16x16x32_bf16 v[172:175], v[212:215], a[20:23], v[172:175]\n"
"v_exp_f32 v165, v165\n"
"v_mfma_f32_16x16x32_bf16 v[172:175], v[216:219], a[24:27], v[172:175]\n"
"s_add_u32 s40, s56, s52\n"
"v_exp_f32 v158, v158\n"
"v_mfma_f32_16x16x32_bf16 v[172:175], v[220:223], a[28:31], v[172:175]\n"
"buffer_load_dwordx4 v207, s[20:23], s40 offen offset:1024 nt lds\n"
"v_mfma_f32_16x16x32_bf16 v[176:179], v[216:219], a[40:43], v[188:191]\n"
"v_exp_f32 v157, v157\n"
"v_mfma_f32_16x16x32_bf16 v[176:179], v[220:223], a[44:47], v[176:179]\n"
"v_accvgpr_write_b32 a105, v121\n"
"v_accvgpr_write_b32 a170, v26\n"
"v_mfma_f32_16x16x32_bf16 v[176:179], v[208:211], a[32:35], v[176:179]\n"
"v_exp_f32 v156, v156\n"
"v_mfma_f32_16x16x32_bf16 v[176:179], v[212:215], a[36:39], v[176:179]\n"
"v_permlane16_swap_b32 v113, v115\n"
"v_cvt_pk_bf16_f32 v2, v4, v5\n"
"v_exp_f32 v161, v161\n"
"v_exp_f32 v163, v163\n"
"v_exp_f32 v172, v172\n"
"v_mfma_f32_16x16x32_bf16 v[152:155], v[208:211], a[192:195], v[192:195]\n"
"v_accvgpr_write_b32 a171, v27\n"
"v_mov_b32 v200, 0\n"
"v_mfma_f32_16x16x32_bf16 v[152:155], v[212:215], a[196:199], v[152:155]\n"
"ds_read_b64_tr_b16 v[226:227], v239 offset:4608\n"
"v_accvgpr_write_b32 a176, v32\n"
"v_mfma_f32_16x16x32_bf16 v[152:155], v[216:219], a[200:203], v[152:155]\n"
"ds_read_b128 v[212:215], v231 offset:16384\n"
"v_cvt_pk_bf16_f32 v105, v158, v159\n"
"v_mfma_f32_16x16x32_bf16 v[152:155], v[220:223], a[204:207], v[152:155]\n"
"s_waitcnt lgkmcnt(2)\n"
"v_permlane16_swap_b32 v0, v2\n"
"v_mfma_f32_16x16x32_bf16 a[232:235], v[124:127], a[244:247], 0\n"
"ds_read_b128 v[216:219], v230 offset:16896\n"
"v_cvt_pk_bf16_f32 v18, v20, v21\n"
"v_mfma_f32_16x16x32_bf16 v[24:27], v[128:131], a[244:247], 0\n"
"s_add_u32 s40, s56, s53\n"
"ds_read_b128 v[208:211], v230 offset:16384\n"
"v_cvt_pk_bf16_f32 v104, v156, v157\n"
"v_mfma_f32_16x16x32_bf16 v[24:27], v[132:135], a[248:251], v[24:27]\n"
"buffer_load_dwordx4 v240, s[20:23], s40 offen offset:2048 nt lds\n"
"v_mfma_f32_16x16x32_bf16 v[56:59], v[136:139], a[244:247], 0\n"
"v_exp_f32 v174, v174\n"
"v_mfma_f32_16x16x32_bf16 v[56:59], v[140:143], a[248:251], v[56:59]\n"
"v_accvgpr_write_b32 a97, v113\n"
"v_accvgpr_write_b32 a144, v0\n"
"v_mfma_f32_16x16x32_bf16 v[88:91], v[144:147], a[244:247], 0\n"
"v_exp_f32 v171, v171\n"
"v_mfma_f32_16x16x32_bf16 v[88:91], v[148:151], a[248:251], v[88:91]\n"
"s_waitcnt lgkmcnt(2)\n"
"ds_read_b128 v[220:223], v231 offset:16896\n"
"v_cvt_pk_bf16_f32 v114, v116, v117\n"
"v_mfma_f32_16x16x32_bf16 v[92:95], v[148:151], v[224:227], 0\n"
"s_add_u32 s40, s56, s54\n"
"v_permlane16_swap_b32 v33, v35\n"
"v_mfma_f32_16x16x32_bf16 v[92:95], v[144:147], a[252:255], v[92:95]\n"
"buffer_load_dwordx4 v207, s[20:23], s40 offen offset:3072 nt lds\n"
"v_permlane16_swap_b32 v112, v114\n"
"v_accvgpr_write_b32 a99, v115\n"
"v_cvt_pk_bf16_f32 v120, v164, v165\n"
"v_cvt_pk_bf16_f32 v121, v166, v167\n"
"v_exp_f32 v175, v175\n"
"v_exp_f32 v178, v178\n"
"v_mfma_f32_16x16x32_bf16 v[60:63], v[140:143], v[224:227], 0\n"
"s_add_u32 s55, s55, s59\n"
"v_permlane16_swap_b32 v1, v3\n"
"v_mfma_f32_16x16x32_bf16 v[60:63], v[136:139], a[252:255], v[60:63]\n"
"s_or_b32 s55, s55, s58\n"
"v_accvgpr_write_b32 a96, v112\n"
"v_accvgpr_write_b32 a178, v34\n"
"v_mfma_f32_16x16x32_bf16 v[28:31], v[132:135], v[224:227], 0\n"
"s_xor_b32 s56, s55, s57\n"
"ds_read_b64_tr_b16 v[224:225], v237 offset:0\n"
"v_accvgpr_write_b32 a179, v35\n"
"v_mfma_f32_16x16x32_bf16 v[28:31], v[128:131], a[252:255], v[28:31]\n"
"ds_read_b64_tr_b16 a[244:245], v232 offset:0\n"
"v_accvgpr_write_b32 a145, v1\n"
"v_mfma_f32_16x16x32_bf16 a[236:239], v[124:127], a[252:255], 0\n"
"v_exp_f32 v179, v179\n"
"v_smfmac_f32_16x16x64_bf16 v[198:201], a[240:243], v[136:143], v241\n"
"s_waitcnt lgkmcnt(2)\n"
"ds_read_b64_tr_b16 a[252:253], v233 offset:0\n"
"v_accvgpr_write_b32 a177, v33\n"
"v_mfma_f32_16x16x32_bf16 v[156:159], v[208:211], a[0:3], v[180:183]\n"
"ds_read_b64_tr_b16 a[246:247], v232 offset:4096\n"
"v_cvt_pk_bf16_f32 v113, v162, v163\n"
"v_mfma_f32_16x16x32_bf16 v[156:159], v[212:215], a[4:7], v[156:159]\n"
"ds_read_b64_tr_b16 a[250:251], v236 offset:4096\n"
"v_cvt_pk_bf16_f32 v112, v160, v161\n"
"v_mfma_f32_16x16x32_bf16 v[156:159], v[216:219], a[8:11], v[156:159]\n"
"s_add_u32 m0, s44, 49152\n"
"ds_read_b64_tr_b16 a[248:249], v236 offset:0\n"
"ds_read_b64_tr_b16 v[226:227], v237 offset:4096\n"
"v_mfma_f32_16x16x32_bf16 v[156:159], v[220:223], a[12:15], v[156:159]\n"
"buffer_load_dwordx4 v240, s[16:19], s56 offen nt lds\n"
"v_mfma_f32_16x16x32_bf16 v[160:163], v[216:219], a[24:27], v[184:187]\n"
"v_exp_f32 v169, v169\n"
"v_mfma_f32_16x16x32_bf16 v[160:163], v[220:223], a[28:31], v[160:163]\n"
"v_accvgpr_write_b32 a98, v114\n"
"v_accvgpr_write_b32 a147, v3\n"
"v_mfma_f32_16x16x32_bf16 v[160:163], v[208:211], a[16:19], v[160:163]\n"
"v_accvgpr_write_b32 a146, v2\n"
"v_cvt_pk_bf16_f32 v107, v174, v175\n"
"v_mfma_f32_16x16x32_bf16 v[160:163], v[212:215], a[20:23], v[160:163]\n"
"ds_read_b64_tr_b16 a[254:255], v233 offset:4096\n"
"v_cvt_pk_bf16_f32 v115, v178, v179\n"
"v_mfma_f32_16x16x32_bf16 v[164:167], v[208:211], a[32:35], v[188:191]\n"
"v_exp_f32 v158, v158\n"
"v_mfma_f32_16x16x32_bf16 v[164:167], v[212:215], a[36:39], v[164:167]\n"
"v_exp_f32 v168, v168\n"
"v_mfma_f32_16x16x32_bf16 v[164:167], v[216:219], a[40:43], v[164:167]\n"
"v_exp_f32 v170, v170\n"
"v_mfma_f32_16x16x32_bf16 v[164:167], v[220:223], a[44:47], v[164:167]\n"
"s_waitcnt lgkmcnt(1)\n"
"v_exp_f32 v159, v159\n"
"v_mfma_f32_16x16x32_bf16 a[208:211], v[124:127], a[244:247], 0\n"
"ds_read_b128 v[208:211], v230 offset:20480\n"
"v_cvt_pk_bf16_f32 v19, v22, v23\n"
"v_mfma_f32_16x16x32_bf16 v[0:3], v[128:131], a[244:247], 0\n"
"s_add_u32 s40, s56, s52\n"
"ds_read_b128 v[216:219], v230 offset:20992\n"
"v_cvt_pk_bf16_f32 v98, v168, v169\n"
"v_mfma_f32_16x16x32_bf16 v[0:3], v[132:135], a[248:251], v[0:3]\n"
"buffer_load_dwordx4 v207, s[16:19], s40 offen offset:1024 nt lds\n"
"v_mfma_f32_16x16x32_bf16 v[32:35], v[136:139], a[244:247], 0\n"
"v_exp_f32 v165, v165\n"
"v_mfma_f32_16x16x32_bf16 v[32:35], v[140:143], a[248:251], v[32:35]\n"
"v_exp_f32 v176, v176\n"
"v_mfma_f32_16x16x32_bf16 v[64:67], v[144:147], a[244:247], 0\n"
"ds_read_b128 v[212:215], v231 offset:20480\n"
"v_cvt_pk_bf16_f32 v99, v170, v171\n"
"v_mfma_f32_16x16x32_bf16 v[64:67], v[148:151], a[248:251], v[64:67]\n"
"ds_read_b128 v[220:223], v231 offset:20992\n"
"v_accvgpr_write_b32 a107, v123\n"
"v_permlane16_swap_b32 v16, v18\n"
"v_exp_f32 v173, v173\n"
"v_exp_f32 v154, v154\n"
"v_exp_f32 v155, v155\n"
"v_mfma_f32_16x16x32_bf16 v[68:71], v[148:151], v[224:227], 0\n"
"s_waitcnt lgkmcnt(4)\n"
"s_add_u32 s40, s56, s53\n"
"v_exp_f32 v164, v164\n"
"v_mfma_f32_16x16x32_bf16 v[68:71], v[144:147], a[252:255], v[68:71]\n"
"buffer_load_dwordx4 v240, s[16:19], s40 offen offset:2048 nt lds\n"
"v_mfma_f32_16x16x32_bf16 v[36:39], v[140:143], v[224:227], 0\n"
"v_exp_f32 v153, v153\n"
"v_mfma_f32_16x16x32_bf16 v[36:39], v[136:139], a[252:255], v[36:39]\n"
"v_exp_f32 v166, v166\n"
"v_mfma_f32_16x16x32_bf16 v[4:7], v[132:135], v[224:227], 0\n"
"v_permlane16_swap_b32 v17, v19\n"
"v_mfma_f32_16x16x32_bf16 v[4:7], v[128:131], a[252:255], v[4:7]\n"
"ds_read_b64_tr_b16 a[250:251], v236 offset:4608\n"
"v_cvt_pk_bf16_f32 v106, v172, v173\n"
"v_mfma_f32_16x16x32_bf16 a[212:215], v[124:127], a[252:255], 0\n"
"v_exp_f32 v162, v162\n"
"v_smfmac_f32_16x16x64_bf16 v[200:203], a[240:243], v[144:151], v241\n"
"s_waitcnt lgkmcnt(1)\n"
"ds_read_b64_tr_b16 a[246:247], v232 offset:4608\n"
"ds_read_b64_tr_b16 a[248:249], v236 offset:512\n"
"v_mfma_f32_16x16x32_bf16 v[168:171], v[208:211], a[0:3], v[180:183]\n"
"v_exp_f32 v177, v177\n"
"v_mfma_f32_16x16x32_bf16 v[168:171], v[212:215], a[4:7], v[168:171]\n"
"ds_read_b64_tr_b16 a[244:245], v232 offset:512\n"
"v_cvt_pk_bf16_f32 v114, v176, v177\n"
"v_mfma_f32_16x16x32_bf16 v[168:171], v[216:219], a[8:11], v[168:171]\n"
"s_add_u32 s40, s56, s54\n"
"v_exp_f32 v157, v157\n"
"v_mfma_f32_16x16x32_bf16 v[168:171], v[220:223], a[12:15], v[168:171]\n"
"buffer_load_dwordx4 v207, s[16:19], s40 offen offset:3072 nt lds\n"
"v_mfma_f32_16x16x32_bf16 v[172:175], v[216:219], a[24:27], v[184:187]\n"
"v_exp_f32 v167, v167\n"
"v_mfma_f32_16x16x32_bf16 v[172:175], v[220:223], a[28:31], v[172:175]\n"
"v_exp_f32 v160, v160\n"
"v_mfma_f32_16x16x32_bf16 v[172:175], v[208:211], a[16:19], v[172:175]\n"
"ds_read_b64_tr_b16 a[254:255], v233 offset:4608\n"
"v_accvgpr_write_b32 a162, v18\n"
"v_mfma_f32_16x16x32_bf16 v[172:175], v[212:215], a[20:23], v[172:175]\n"
"s_cbranch_vccnz LPCE1_%=\nLPKE1_%=:\n"
"v_bfe_u32 v249, v242, 4, 2\n"
"v_mfma_f32_16x16x32_bf16 v[176:179], v[208:211], a[32:35], v[188:191]\n"
"v_accvgpr_write_b32 a106, v122\n"
"v_bfe_u32 v248, v242, 8, 2\n"
"v_mfma_f32_16x16x32_bf16 v[176:179], v[212:215], a[36:39], v[176:179]\n"
"ds_read_b64_tr_b16 a[252:253], v233 offset:512\n"
"v_accvgpr_write_b32 a161, v17\n"
"v_mfma_f32_16x16x32_bf16 v[176:179], v[216:219], a[40:43], v[176:179]\n"
"ds_read_b64_tr_b16 v[224:225], v237 offset:512\n"
"v_accvgpr_write_b32 a160, v16\n"
"v_mfma_f32_16x16x32_bf16 v[176:179], v[220:223], a[44:47], v[176:179]\n"
"s_waitcnt lgkmcnt(3)\n"
"ds_read_b64_tr_b16 v[226:227], v237 offset:4608\n"
"v_accvgpr_write_b32 a163, v19\n"
"v_lshlrev_b32 v248, 6, v248\n"
"v_exp_f32 v152, v152\n"
"v_exp_f32 v163, v163\n"
"v_exp_f32 v169, v169\n"
"v_mfma_f32_16x16x32_bf16 a[224:227], v[124:127], a[244:247], 0\n"
"v_exp_f32 v156, v156\n"
"v_mfma_f32_16x16x32_bf16 v[16:19], v[128:131], a[244:247], 0\n"
"s_waitcnt vmcnt(8)\n"
"v_xor_b32 v248, v248, v242\n"
"v_bfe_u32 v254, v242, 6, 4\n"
"v_mfma_f32_16x16x32_bf16 v[16:19], v[132:135], a[248:251], v[16:19]\n"
"s_barrier\n"
"ds_read_b128 v[216:219], v228 offset:33280\n"
"v_mul_u32_u24 v249, 20, v249\n"
"v_cvt_pk_bf16_f32 v101, v158, v159\n"
"v_exp_f32 v176, v176\n"
"v_exp_f32 v177, v177\n"
"v_mfma_f32_16x16x32_bf16 v[48:51], v[136:139], a[244:247], 0\n"
"ds_read_b128 v[220:223], v229 offset:33280\n"
"v_cvt_pk_bf16_f32 v109, v162, v163\n"
"v_mfma_f32_16x16x32_bf16 v[48:51], v[140:143], a[248:251], v[48:51]\n"
"ds_read_b128 v[208:211], v228 offset:32768\n"
"ds_read_b128 v[212:215], v229 offset:32768\n"
"v_mfma_f32_16x16x32_bf16 v[80:83], v[144:147], a[244:247], 0\n"
"ds_read_b64_tr_b16 a[246:247], v234 offset:20480\n"
"v_cvt_pk_bf16_f32 v122, v152, v153\n"
"v_mfma_f32_16x16x32_bf16 v[80:83], v[148:151], a[248:251], v[80:83]\n"
"s_waitcnt lgkmcnt(5)\n"
"ds_read_b64_tr_b16 a[250:251], v238 offset:20480\n"
"v_cvt_pk_bf16_f32 v123, v154, v155\n"
"v_mfma_f32_16x16x32_bf16 v[84:87], v[148:151], v[224:227], 0\n"
"v_cvt_pk_bf16_f32 v116, v164, v165\n"
"v_cvt_pk_bf16_f32 v117, v166, v167\n"
"v_mfma_f32_16x16x32_bf16 v[84:87], v[144:147], a[252:255], v[84:87]\n"
"v_xor_b32 v249, v249, v254\n"
"v_cvt_pk_bf16_f32 v118, v176, v177\n"
"v_mfma_f32_16x16x32_bf16 v[52:55], v[140:143], v[224:227], 0\n"
"v_lshl_add_u32 v249, v249, 4, s44\n"
"v_cvt_pk_bf16_f32 v100, v156, v157\n"
"v_mfma_f32_16x16x32_bf16 v[52:55], v[136:139], a[252:255], v[52:55]\n"
"v_exp_f32 v161, v161\n"
"v_mfma_f32_16x16x32_bf16 v[20:23], v[132:135], v[224:227], 0\n"
"v_exp_f32 v174, v174\n"
"v_mfma_f32_16x16x32_bf16 v[20:23], v[128:131], a[252:255], v[20:23]\n"
"v_exp_f32 v168, v168\n"
"v_mfma_f32_16x16x32_bf16 a[228:231], v[124:127], a[252:255], 0\n"
"s_waitcnt lgkmcnt(1)\n"
"ds_read_b64_tr_b16 a[254:255], v235 offset:20480\n"
"s_branch LAY2_%=\n"
"LAL_%=:\n"
"v_mfma_f32_16x16x32_bf16 a[224:227], v[124:127], a[244:247], a[224:227]\n"
"v_exp_f32 v162, v162\n"
"v_mfma_f32_16x16x32_bf16 v[16:19], v[128:131], a[244:247], v[16:19]\n"
"s_waitcnt vmcnt(8)\n"
"v_exp_f32 v168, v168\n"
"v_mfma_f32_16x16x32_bf16 v[16:19], v[132:135], a[248:251], v[16:19]\n"
"s_barrier\n"
"v_mfma_f32_16x16x32_bf16 v[48:51], v[136:139], a[244:247], v[48:51]\n"
"ds_read_b128 v[208:211], v228 offset:0\n"
"v_cvt_pk_bf16_f32 v101, v158, v159\n"
"v_mfma_f32_16x16x32_bf16 v[48:51], v[140:143], a[248:251], v[48:51]\n"
"ds_read_b128 v[216:219], v228 offset:512\n"
"v_cvt_pk_bf16_f32 v109, v162, v163\n"
"v_mfma_f32_16x16x32_bf16 v[80:83], v[144:147], a[244:247], v[80:83]\n"
"ds_read_b128 v[220:223], v229 offset:512\n"
"v_cvt_pk_bf16_f32 v123, v154, v155\n"
"v_mfma_f32_16x16x32_bf16 v[80:83], v[148:151], a[248:251], v[80:83]\n"
"ds_read_b128 v[212:215], v229 offset:0\n"
"v_cvt_pk_bf16_f32 v117, v166, v167\n"
"v_mfma_f32_16x16x32_bf16 v[84:87], v[148:151], v[224:227], v[84:87]\n"
"v_exp_f32 v176, v176\n"
"v_mfma_f32_16x16x32_bf16 v[84:87], v[144:147], a[252:255], v[84:87]\n"
"v_exp_f32 v169, v169\n"
"v_mfma_f32_16x16x32_bf16 v[52:55], v[140:143], v[224:227], v[52:55]\n"
"ds_read_b64_tr_b16 a[246:247], v234 offset:53248\n"
"v_cvt_pk_bf16_f32 v116, v164, v165\n"
"v_mfma_f32_16x16x32_bf16 v[52:55], v[136:139], a[252:255], v[52:55]\n"
"v_exp_f32 v177, v177\n"
"v_mfma_f32_16x16x32_bf16 v[20:23], v[132:135], v[224:227], v[20:23]\n"
"ds_read_b64_tr_b16 a[250:251], v238 offset:53248\n"
"v_cvt_pk_bf16_f32 v118, v176, v177\n"
"v_mfma_f32_16x16x32_bf16 v[20:23], v[128:131], a[252:255], v[20:23]\n"
"v_exp_f32 v174, v174\n"
"v_mfma_f32_16x16x32_bf16 a[228:231], v[124:127], a[252:255], a[228:231]\n"
"s_waitcnt lgkmcnt(3)\n"
"ds_read_b64_tr_b16 a[254:255], v235 offset:53248\n"
"v_cvt_pk_bf16_f32 v100, v156, v157\n"
"v_mfma_f32_16x16x32_bf16 v[152:155], v[208:211], a[0:3], v[180:183]\n"
"s_waitcnt lgkmcnt(3)\n"
"ds_read_b64_tr_b16 a[248:249], v238 offset:49152\n"
"v_cvt_pk_bf16_f32 v102, v168, v169\n"
"v_mfma_f32_16x16x32_bf16 v[152:155], v[212:215], a[4:7], v[152:155]\n"
"v_exp_f32 v172, v172\n"
"v_mfma_f32_16x16x32_bf16 v[152:155], v[216:219], a[8:11], v[152:155]\n"
"ds_read_b64_tr_b16 a[244:245], v234 offset:49152\n"
"v_cvt_pk_bf16_f32 v108, v160, v161\n"
"v_mfma_f32_16x16x32_bf16 v[152:155], v[220:223], a[12:15], v[152:155]\n"
"v_exp_f32 v175, v175\n"
"v_mfma_f32_16x16x32_bf16 v[156:159], v[216:219], a[24:27], v[184:187]\n"
"ds_read_b64_tr_b16 a[252:253], v235 offset:49152\n"
"v_cvt_pk_bf16_f32 v111, v174, v175\n"
"v_mfma_f32_16x16x32_bf16 v[156:159], v[220:223], a[28:31], v[156:159]\n"
"ds_read_b64_tr_b16 v[224:225], v239 offset:49152\n"
"v_mfma_f32_16x16x32_bf16 v[156:159], v[208:211], a[16:19], v[156:159]\n"
"s_add_u32 m0, s44, 81920\n"
"ds_read_b64_tr_b16 v[226:227], v239 offset:53248\n"
"v_mfma_f32_16x16x32_bf16 v[156:159], v[212:215], a[20:23], v[156:159]\n"
"buffer_load_dwordx4 v240, s[20:23], s56 offen nt lds\n"
"v_mfma_f32_16x16x32_bf16 v[160:163], v[208:211], a[32:35], v[188:191]\n"
"v_exp_f32 v178, v178\n"
"v_mfma_f32_16x16x32_bf16 v[160:163], v[212:215], a[36:39], v[160:163]\n"
"v_exp_f32 v171, v171\n"
"v_mfma_f32_16x16x32_bf16 v[160:163], v[216:219], a[40:43], v[160:163]\n"
"s_add_u32 s40, s56, s52\n"
"v_mfma_f32_16x16x32_bf16 v[160:163], v[220:223], a[44:47], v[160:163]\n"
"buffer_load_dwordx4 v207, s[20:23], s40 offen offset:1024 nt lds\n"
"v_mfma_f32_16x16x32_bf16 v[164:167], v[216:219], a[200:203], v[192:195]\n"
"v_exp_f32 v170, v170\n"
"v_mfma_f32_16x16x32_bf16 v[164:167], v[220:223], a[204:207], v[164:167]\n"
"v_exp_f32 v153, v153\n"
"v_mfma_f32_16x16x32_bf16 v[164:167], v[208:211], a[192:195], v[164:167]\n"
"v_exp_f32 v179, v179\n"
"v_mfma_f32_16x16x32_bf16 v[164:167], v[212:215], a[196:199], v[164:167]\n"
"s_waitcnt lgkmcnt(0)\n"
"v_exp_f32 v158, v158\n"
"v_mfma_f32_16x16x32_bf16 a[216:219], v[120:123], a[244:247], a[216:219]\n"
"ds_read_b128 v[216:219], v228 offset:4608\n"
"v_cvt_pk_bf16_f32 v103, v170, v171\n"
"v_mfma_f32_16x16x32_bf16 v[8:11], v[96:99], a[244:247], v[8:11]\n"
"v_exp_f32 v173, v173\n"
"v_mfma_f32_16x16x32_bf16 v[8:11], v[100:103], a[248:251], v[8:11]\n"
"ds_read_b128 v[208:211], v228 offset:4096\n"
"v_cvt_pk_bf16_f32 v110, v172, v173\n"
"v_mfma_f32_16x16x32_bf16 v[40:43], v[104:107], a[244:247], v[40:43]\n"
"v_exp_f32 v162, v162\n"
"v_mfma_f32_16x16x32_bf16 v[40:43], v[108:111], a[248:251], v[40:43]\n"
"ds_read_b128 v[220:223], v229 offset:4608\n"
"v_cvt_pk_bf16_f32 v119, v178, v179\n"
"v_mfma_f32_16x16x32_bf16 v[72:75], v[112:115], a[244:247], v[72:75]\n"
"s_add_u32 s40, s56, s53\n"
"ds_read_b128 v[212:215], v229 offset:4096\n"
"v_mfma_f32_16x16x32_bf16 v[72:75], v[116:119], a[248:251], v[72:75]\n"
"buffer_load_dwordx4 v240, s[20:23], s40 offen offset:2048 nt lds\n"
"v_mfma_f32_16x16x32_bf16 v[76:79], v[116:119], v[224:227], v[76:79]\n"
"v_exp_f32 v163, v163\n"
"v_mfma_f32_16x16x32_bf16 v[76:79], v[112:115], a[252:255], v[76:79]\n"
"v_exp_f32 v156, v156\n"
"v_mfma_f32_16x16x32_bf16 v[44:47], v[108:111], v[224:227], v[44:47]\n"
"v_exp_f32 v160, v160\n"
"v_mfma_f32_16x16x32_bf16 v[44:47], v[104:107], a[252:255], v[44:47]\n"
"v_exp_f32 v155, v155\n"
"v_mfma_f32_16x16x32_bf16 v[12:15], v[100:103], v[224:227], v[12:15]\n"
"ds_read_b64_tr_b16 v[226:227], v239 offset:53760\n"
"v_mfma_f32_16x16x32_bf16 v[12:15], v[96:99], a[252:255], v[12:15]\n"
"ds_read_b64_tr_b16 a[248:249], v238 offset:49664\n"
"v_mfma_f32_16x16x32_bf16 a[220:223], v[120:123], a[252:255], a[220:223]\n"
"v_exp_f32 v154, v154\n"
"v_smfmac_f32_16x16x64_bf16 v[196:199], a[240:243], v[96:103], v241\n"
"ds_read_b64_tr_b16 a[250:251], v238 offset:53760\n"
"v_mfma_f32_16x16x32_bf16 v[202:205], a[240:243], v[120:123], v[202:205]\n"
"s_waitcnt lgkmcnt(3)\n"
"v_exp_f32 v152, v152\n"
"v_mfma_f32_16x16x32_bf16 v[168:171], v[216:219], a[8:11], v[180:183]\n"
"ds_read_b64_tr_b16 a[244:245], v234 offset:49664\n"
"v_cvt_pk_bf16_f32 v128, v152, v153\n"
"v_mfma_f32_16x16x32_bf16 v[168:171], v[220:223], a[12:15], v[168:171]\n"
"v_exp_f32 v165, v165\n"
"v_mfma_f32_16x16x32_bf16 v[168:171], v[208:211], a[0:3], v[168:171]\n"
"ds_read_b64_tr_b16 a[246:247], v234 offset:53760\n"
"v_cvt_pk_bf16_f32 v145, v162, v163\n"
"v_mfma_f32_16x16x32_bf16 v[168:171], v[212:215], a[4:7], v[168:171]\n"
"ds_read_b64_tr_b16 a[254:255], v235 offset:53760\n"
"v_mfma_f32_16x16x32_bf16 v[172:175], v[208:211], a[16:19], v[184:187]\n"
"ds_read_b64_tr_b16 v[224:225], v239 offset:49664\n"
"v_cvt_pk_bf16_f32 v129, v154, v155\n"
"v_mfma_f32_16x16x32_bf16 v[172:175], v[212:215], a[20:23], v[172:175]\n"
"v_mfma_f32_16x16x32_bf16 v[172:175], v[216:219], a[24:27], v[172:175]\n"
"s_add_u32 s40, s56, s54\n"
"ds_read_b64_tr_b16 a[252:253], v235 offset:49664\n"
"v_mfma_f32_16x16x32_bf16 v[172:175], v[220:223], a[28:31], v[172:175]\n"
"buffer_load_dwordx4 v207, s[20:23], s40 offen offset:3072 nt lds\n"
"v_mfma_f32_16x16x32_bf16 v[176:179], v[216:219], a[40:43], v[188:191]\n"
"s_add_u32 s55, s55, s59\n"
"v_exp_f32 v157, v157\n"
"v_mfma_f32_16x16x32_bf16 v[176:179], v[220:223], a[44:47], v[176:179]\n"
"s_or_b32 s55, s55, s58\n"
"v_exp_f32 v171, v171\n"
"v_mfma_f32_16x16x32_bf16 v[176:179], v[208:211], a[32:35], v[176:179]\n"
"s_xor_b32 s56, s55, s57\n"
"v_exp_f32 v159, v159\n"
"v_mfma_f32_16x16x32_bf16 v[176:179], v[212:215], a[36:39], v[176:179]\n"
"v_exp_f32 v167, v167\n"
"v_mfma_f32_16x16x32_bf16 v[152:155], v[208:211], a[192:195], v[192:195]\n"
"v_cvt_pk_bf16_f32 v137, v158, v159\n"
"v_mfma_f32_16x16x32_bf16 v[152:155], v[212:215], a[196:199], v[152:155]\n"
"ds_read_b128 v[208:211], v230 offset:0\n"
"v_cvt_pk_bf16_f32 v136, v156, v157\n"
"v_mfma_f32_16x16x32_bf16 v[152:155], v[216:219], a[200:203], v[152:155]\n"
"v_exp_f32 v166, v166\n"
"v_mfma_f32_16x16x32_bf16 v[152:155], v[220:223], a[204:207], v[152:155]\n"
"s_waitcnt lgkmcnt(1)\n"
"ds_read_b128 v[216:219], v230 offset:512\n"
"v_mfma_f32_16x16x32_bf16 a[232:235], v[120:123], a[244:247], a[232:235]\n"
"v_exp_f32 v169, v169\n"
"v_mfma_f32_16x16x32_bf16 v[24:27], v[96:99], a[244:247], v[24:27]\n"
"ds_read_b128 v[220:223], v231 offset:512\n"
"v_mfma_f32_16x16x32_bf16 v[24:27], v[100:103], a[248:251], v[24:27]\n"
"v_exp_f32 v164, v164\n"
"v_mfma_f32_16x16x32_bf16 v[56:59], v[104:107], a[244:247], v[56:59]\n"
"ds_read_b128 v[212:215], v231 offset:0\n"
"v_mfma_f32_16x16x32_bf16 v[56:59], v[108:111], a[248:251], v[56:59]\n"
"v_exp_f32 v178, v178\n"
"v_mfma_f32_16x16x32_bf16 v[88:91], v[112:115], a[244:247], v[88:91]\n"
"s_add_u32 m0, s44, 32768\n"
"ds_read_b64_tr_b16 a[246:247], v232 offset:53248\n"
"v_mfma_f32_16x16x32_bf16 v[88:91], v[116:119], a[248:251], v[88:91]\n"
"buffer_load_dwordx4 v240, s[16:19], s56 offen nt lds\n"
"v_mfma_f32_16x16x32_bf16 v[92:95], v[116:119], v[224:227], v[92:95]\n"
"v_exp_f32 v175, v175\n"
"v_mfma_f32_16x16x32_bf16 v[92:95], v[112:115], a[252:255], v[92:95]\n"
"v_exp_f32 v168, v168\n"
"v_mfma_f32_16x16x32_bf16 v[60:63], v[108:111], v[224:227], v[60:63]\n"
"v_exp_f32 v161, v161\n"
"v_mfma_f32_16x16x32_bf16 v[60:63], v[104:107], a[252:255], v[60:63]\n"
"v_exp_f32 v177, v177\n"
"v_mfma_f32_16x16x32_bf16 v[28:31], v[100:103], v[224:227], v[28:31]\n"
"ds_read_b64_tr_b16 v[226:227], v237 offset:53248\n"
"v_mfma_f32_16x16x32_bf16 v[28:31], v[96:99], a[252:255], v[28:31]\n"
"v_exp_f32 v172, v172\n"
"v_mfma_f32_16x16x32_bf16 a[236:239], v[120:123], a[252:255], a[236:239]\n"
"ds_read_b64_tr_b16 a[250:251], v236 offset:53248\n"
"v_cvt_pk_bf16_f32 v124, v164, v165\n"
"v_smfmac_f32_16x16x64_bf16 v[198:201], a[240:243], v[104:111], v241\n"
"s_waitcnt lgkmcnt(2)\n"
"ds_read_b64_tr_b16 a[252:253], v233 offset:49152\n"
"v_mfma_f32_16x16x32_bf16 v[156:159], v[208:211], a[0:3], v[180:183]\n"
"ds_read_b64_tr_b16 a[248:249], v236 offset:49152\n"
"v_cvt_pk_bf16_f32 v125, v166, v167\n"
"v_mfma_f32_16x16x32_bf16 v[156:159], v[212:215], a[4:7], v[156:159]\n"
"ds_read_b64_tr_b16 v[224:225], v237 offset:49152\n"
"v_cvt_pk_bf16_f32 v130, v168, v169\n"
"v_mfma_f32_16x16x32_bf16 v[156:159], v[216:219], a[8:11], v[156:159]\n"
"ds_read_b64_tr_b16 a[254:255], v233 offset:53248\n"
"v_cvt_pk_bf16_f32 v144, v160, v161\n"
"v_mfma_f32_16x16x32_bf16 v[156:159], v[220:223], a[12:15], v[156:159]\n"
"ds_read_b64_tr_b16 a[244:245], v232 offset:49152\n"
"v_mfma_f32_16x16x32_bf16 v[160:163], v[216:219], a[24:27], v[184:187]\n"
"v_exp_f32 v176, v176\n"
"v_mfma_f32_16x16x32_bf16 v[160:163], v[220:223], a[28:31], v[160:163]\n"
"v_exp_f32 v152, v152\n"
"v_mfma_f32_16x16x32_bf16 v[160:163], v[208:211], a[16:19], v[160:163]\n"
"s_add_u32 s40, s56, s52\n"
"v_cvt_pk_bf16_f32 v146, v176, v177\n"
"v_mfma_f32_16x16x32_bf16 v[160:163], v[212:215], a[20:23], v[160:163]\n"
"buffer_load_dwordx4 v207, s[16:19], s40 offen offset:1024 nt lds\n"
"v_mfma_f32_16x16x32_bf16 v[164:167], v[208:211], a[32:35], v[188:191]\n"
"v_exp_f32 v170, v170\n"
"v_mfma_f32_16x16x32_bf16 v[164:167], v[212:215], a[36:39], v[164:167]\n"
"v_exp_f32 v173, v173\n"
"v_mfma_f32_16x16x32_bf16 v[164:167], v[216:219], a[40:43], v[164:167]\n"
"v_exp_f32 v153, v153\n"
"v_mfma_f32_16x16x32_bf16 v[164:167], v[220:223], a[44:47], v[164:167]\n"
"s_waitcnt lgkmcnt(0)\n"
"v_exp_f32 v174, v174\n"
"v_mfma_f32_16x16x32_bf16 a[208:211], v[120:123], a[244:247], a[208:211]\n"
"ds_read_b128 v[208:211], v230 offset:4096\n"
"v_cvt_pk_bf16_f32 v139, v174, v175\n"
"v_mfma_f32_16x16x32_bf16 v[0:3], v[96:99], a[244:247], v[0:3]\n"
"v_exp_f32 v156, v156\n"
"v_mfma_f32_16x16x32_bf16 v[0:3], v[100:103], a[248:251], v[0:3]\n"
"ds_read_b128 v[220:223], v231 offset:4608\n"
"v_cvt_pk_bf16_f32 v138, v172, v173\n"
"v_mfma_f32_16x16x32_bf16 v[32:35], v[104:107], a[244:247], v[32:35]\n"
"v_exp_f32 v161, v161\n"
"v_mfma_f32_16x16x32_bf16 v[32:35], v[108:111], a[248:251], v[32:35]\n"
"ds_read_b128 v[216:219], v230 offset:4608\n"
"v_cvt_pk_bf16_f32 v126, v152, v153\n"
"v_mfma_f32_16x16x32_bf16 v[64:67], v[112:115], a[244:247], v[64:67]\n"
"s_add_u32 s40, s56, s53\n"
"ds_read_b128 v[212:215], v231 offset:4096\n"
"v_mfma_f32_16x16x32_bf16 v[64:67], v[116:119], a[248:251], v[64:67]\n"
"buffer_load_dwordx4 v240, s[16:19], s40 offen offset:2048 nt lds\n"
"v_mfma_f32_16x16x32_bf16 v[68:71], v[116:119], v[224:227], v[68:71]\n"
"v_exp_f32 v159, v159\n"
"v_mfma_f32_16x16x32_bf16 v[68:71], v[112:115], a[252:255], v[68:71]\n"
"v_exp_f32 v155, v155\n"
"v_mfma_f32_16x16x32_bf16 v[36:39], v[108:111], v[224:227], v[36:39]\n"
"v_exp_f32 v160, v160\n"
"v_mfma_f32_16x16x32_bf16 v[36:39], v[104:107], a[252:255], v[36:39]\n"
"v_exp_f32 v179, v179\n"
"v_mfma_f32_16x16x32_bf16 v[4:7], v[100:103], v[224:227], v[4:7]\n"
"ds_read_b64_tr_b16 a[244:245], v232 offset:49664\n"
"ds_read_b64_tr_b16 v[224:225], v237 offset:49664\n"
"v_mfma_f32_16x16x32_bf16 v[4:7], v[96:99], a[252:255], v[4:7]\n"
"v_exp_f32 v158, v158\n"
"v_mfma_f32_16x16x32_bf16 a[212:215], v[120:123], a[252:255], a[212:215]\n"
"ds_read_b64_tr_b16 a[246:247], v232 offset:53760\n"
"ds_read_b64_tr_b16 a[250:251], v236 offset:53760\n"
"v_smfmac_f32_16x16x64_bf16 v[200:203], a[240:243], v[112:119], v241\n"
"s_waitcnt lgkmcnt(4)\n"
"ds_read_b64_tr_b16 v[226:227], v237 offset:53760\n"
"v_cvt_pk_bf16_f32 v131, v170, v171\n"
"v_mfma_f32_16x16x32_bf16 v[168:171], v[208:211], a[0:3], v[180:183]\n"
"v_exp_f32 v164, v164\n"
"v_mfma_f32_16x16x32_bf16 v[168:171], v[212:215], a[4:7], v[168:171]\n"
"ds_read_b64_tr_b16 a[248:249], v236 offset:49664\n"
"ds_read_b64_tr_b16 a[254:255], v233 offset:53760\n"
"v_mfma_f32_16x16x32_bf16 v[168:171], v[216:219], a[8:11], v[168:171]\n"
"s_add_u32 s40, s56, s54\n"
"ds_read_b64_tr_b16 a[252:253], v233 offset:49664\n"
"v_mfma_f32_16x16x32_bf16 v[168:171], v[220:223], a[12:15], v[168:171]\n"
"buffer_load_dwordx4 v207, s[16:19], s40 offen offset:3072 nt lds\n"
"v_mfma_f32_16x16x32_bf16 v[172:175], v[216:219], a[24:27], v[184:187]\n"
"v_exp_f32 v154, v154\n"
"v_mfma_f32_16x16x32_bf16 v[172:175], v[220:223], a[28:31], v[172:175]\n"
"v_exp_f32 v167, v167\n"
"v_mfma_f32_16x16x32_bf16 v[172:175], v[208:211], a[16:19], v[172:175]\n"
"v_exp_f32 v163, v163\n"
"v_mfma_f32_16x16x32_bf16 v[172:175], v[212:215], a[20:23], v[172:175]\n"
"s_cbranch_vccnz LPCL0_%=\nLPKL0_%=:\n"
"v_cvt_pk_bf16_f32 v147, v178, v179\n"
"v_mfma_f32_16x16x32_bf16 v[176:179], v[208:211], a[32:35], v[188:191]\n"
"v_exp_f32 v165, v165\n"
"v_mfma_f32_16x16x32_bf16 v[176:179], v[212:215], a[36:39], v[176:179]\n"
"v_exp_f32 v157, v157\n"
"v_mfma_f32_16x16x32_bf16 v[176:179], v[216:219], a[40:43], v[176:179]\n"
"v_exp_f32 v166, v166\n"
"v_mfma_f32_16x16x32_bf16 v[176:179], v[220:223], a[44:47], v[176:179]\n"
"s_waitcnt lgkmcnt(0)\n"
"v_mfma_f32_16x16x32_bf16 a[224:227], v[120:123], a[244:247], a[224:227]\n"
"v_exp_f32 v162, v162\n"
"v_mfma_f32_16x16x32_bf16 v[16:19], v[96:99], a[244:247], v[16:19]\n"
"s_waitcnt vmcnt(8)\n"
"v_exp_f32 v168, v168\n"
"v_mfma_f32_16x16x32_bf16 v[16:19], v[100:103], a[248:251], v[16:19]\n"
"s_barrier\n"
"v_mfma_f32_16x16x32_bf16 v[48:51], v[104:107], a[244:247], v[48:51]\n"
"ds_read_b128 v[208:211], v228 offset:16384\n"
"v_cvt_pk_bf16_f32 v133, v158, v159\n"
"v_mfma_f32_16x16x32_bf16 v[48:51], v[108:111], a[248:251], v[48:51]\n"
"ds_read_b128 v[216:219], v228 offset:16896\n"
"v_cvt_pk_bf16_f32 v141, v162, v163\n"
"v_mfma_f32_16x16x32_bf16 v[80:83], v[112:115], a[244:247], v[80:83]\n"
"ds_read_b128 v[220:223], v229 offset:16896\n"
"v_cvt_pk_bf16_f32 v127, v154, v155\n"
"v_mfma_f32_16x16x32_bf16 v[80:83], v[116:119], a[248:251], v[80:83]\n"
"ds_read_b128 v[212:215], v229 offset:16384\n"
"v_cvt_pk_bf16_f32 v149, v166, v167\n"
"v_mfma_f32_16x16x32_bf16 v[84:87], v[116:119], v[224:227], v[84:87]\n"
"v_exp_f32 v176, v176\n"
"v_mfma_f32_16x16x32_bf16 v[84:87], v[112:115], a[252:255], v[84:87]\n"
"v_exp_f32 v169, v169\n"
"v_mfma_f32_16x16x32_bf16 v[52:55], v[108:111], v[224:227], v[52:55]\n"
"ds_read_b64_tr_b16 a[246:247], v234 offset:4096\n"
"v_cvt_pk_bf16_f32 v148, v164, v165\n"
"v_mfma_f32_16x16x32_bf16 v[52:55], v[104:107], a[252:255], v[52:55]\n"
"v_exp_f32 v177, v177\n"
"v_mfma_f32_16x16x32_bf16 v[20:23], v[100:103], v[224:227], v[20:23]\n"
"ds_read_b64_tr_b16 a[250:251], v238 offset:4096\n"
"v_cvt_pk_bf16_f32 v150, v176, v177\n"
"v_mfma_f32_16x16x32_bf16 v[20:23], v[96:99], a[252:255], v[20:23]\n"
"v_exp_f32 v174, v174\n"
"v_mfma_f32_16x16x32_bf16 a[228:231], v[120:123], a[252:255], a[228:231]\n"
"s_waitcnt lgkmcnt(3)\n"
"ds_read_b64_tr_b16 a[254:255], v235 offset:4096\n"
"v_cvt_pk_bf16_f32 v132, v156, v157\n"
"v_mfma_f32_16x16x32_bf16 v[152:155], v[208:211], a[0:3], v[180:183]\n"
"s_waitcnt lgkmcnt(3)\n"
"ds_read_b64_tr_b16 a[248:249], v238 offset:0\n"
"v_cvt_pk_bf16_f32 v134, v168, v169\n"
"v_mfma_f32_16x16x32_bf16 v[152:155], v[212:215], a[4:7], v[152:155]\n"
"v_exp_f32 v172, v172\n"
"v_mfma_f32_16x16x32_bf16 v[152:155], v[216:219], a[8:11], v[152:155]\n"
"ds_read_b64_tr_b16 a[244:245], v234 offset:0\n"
"v_cvt_pk_bf16_f32 v140, v160, v161\n"
"v_mfma_f32_16x16x32_bf16 v[152:155], v[220:223], a[12:15], v[152:155]\n"
"v_exp_f32 v175, v175\n"
"v_mfma_f32_16x16x32_bf16 v[156:159], v[216:219], a[24:27], v[184:187]\n"
"ds_read_b64_tr_b16 a[252:253], v235 offset:0\n"
"v_cvt_pk_bf16_f32 v143, v174, v175\n"
"v_mfma_f32_16x16x32_bf16 v[156:159], v[220:223], a[28:31], v[156:159]\n"
"ds_read_b64_tr_b16 v[224:225], v239 offset:0\n"
"v_mfma_f32_16x16x32_bf16 v[156:159], v[208:211], a[16:19], v[156:159]\n"
"s_add_u32 m0, s44, 98304\n"
"ds_read_b64_tr_b16 v[226:227], v239 offset:4096\n"
"v_mfma_f32_16x16x32_bf16 v[156:159], v[212:215], a[20:23], v[156:159]\n"
"buffer_load_dwordx4 v240, s[20:23], s56 offen nt lds\n"
"v_mfma_f32_16x16x32_bf16 v[160:163], v[208:211], a[32:35], v[188:191]\n"
"v_exp_f32 v178, v178\n"
"v_mfma_f32_16x16x32_bf16 v[160:163], v[212:215], a[36:39], v[160:163]\n"
"v_exp_f32 v171, v171\n"
"v_mfma_f32_16x16x32_bf16 v[160:163], v[216:219], a[40:43], v[160:163]\n"
"s_add_u32 s40, s56, s52\n"
"v_mfma_f32_16x16x32_bf16 v[160:163], v[220:223], a[44:47], v[160:163]\n"
"buffer_load_dwordx4 v207, s[20:23], s40 offen offset:1024 nt lds\n"
"v_mfma_f32_16x16x32_bf16 v[164:167], v[216:219], a[200:203], v[192:195]\n"
"v_exp_f32 v170, v170\n"
"v_mfma_f32_16x16x32_bf16 v[164:167], v[220:223], a[204:207], v[164:167]\n"
"v_exp_f32 v153, v153\n"
"v_mfma_f32_16x16x32_bf16 v[164:167], v[208:211], a[192:195], v[164:167]\n"
"v_exp_f32 v179, v179\n"
"v_mfma_f32_16x16x32_bf16 v[164:167], v[212:215], a[196:199], v[164:167]\n"
"s_waitcnt lgkmcnt(0)\n"
"v_exp_f32 v158, v158\n"
"v_mfma_f32_16x16x32_bf16 a[216:219], v[124:127], a[244:247], a[216:219]\n"
"ds_read_b128 v[216:219], v228 offset:20992\n"
"v_cvt_pk_bf16_f32 v135, v170, v171\n"
"v_mfma_f32_16x16x32_bf16 v[8:11], v[128:131], a[244:247], v[8:11]\n"
"v_exp_f32 v173, v173\n"
"v_mfma_f32_16x16x32_bf16 v[8:11], v[132:135], a[248:251], v[8:11]\n"
"ds_read_b128 v[208:211], v228 offset:20480\n"
"v_cvt_pk_bf16_f32 v142, v172, v173\n"
"v_mfma_f32_16x16x32_bf16 v[40:43], v[136:139], a[244:247], v[40:43]\n"
"v_exp_f32 v162, v162\n"
"v_mfma_f32_16x16x32_bf16 v[40:43], v[140:143], a[248:251], v[40:43]\n"
"ds_read_b128 v[220:223], v229 offset:20992\n"
"v_cvt_pk_bf16_f32 v151, v178, v179\n"
"v_mfma_f32_16x16x32_bf16 v[72:75], v[144:147], a[244:247], v[72:75]\n"
"s_add_u32 s40, s56, s53\n"
"ds_read_b128 v[212:215], v229 offset:20480\n"
"v_mfma_f32_16x16x32_bf16 v[72:75], v[148:151], a[248:251], v[72:75]\n"
"buffer_load_dwordx4 v240, s[20:23], s40 offen offset:2048 nt lds\n"
"v_mfma_f32_16x16x32_bf16 v[76:79], v[148:151], v[224:227], v[76:79]\n"
"v_exp_f32 v163, v163\n"
"v_mfma_f32_16x16x32_bf16 v[76:79], v[144:147], a[252:255], v[76:79]\n"
"v_exp_f32 v156, v156\n"
"v_mfma_f32_16x16x32_bf16 v[44:47], v[140:143], v[224:227], v[44:47]\n"
"v_exp_f32 v160, v160\n"
"v_mfma_f32_16x16x32_bf16 v[44:47], v[136:139], a[252:255], v[44:47]\n"
"v_exp_f32 v155, v155\n"
"v_mfma_f32_16x16x32_bf16 v[12:15], v[132:135], v[224:227], v[12:15]\n"
"ds_read_b64_tr_b16 v[226:227], v239 offset:4608\n"
"v_mfma_f32_16x16x32_bf16 v[12:15], v[128:131], a[252:255], v[12:15]\n"
"ds_read_b64_tr_b16 a[248:249], v238 offset:512\n"
"v_mfma_f32_16x16x32_bf16 a[220:223], v[124:127], a[252:255], a[220:223]\n"
"v_exp_f32 v154, v154\n"
"v_smfmac_f32_16x16x64_bf16 v[196:199], a[240:243], v[128:135], v241\n"
"ds_read_b64_tr_b16 a[250:251], v238 offset:4608\n"
"v_mfma_f32_16x16x32_bf16 v[202:205], a[240:243], v[124:127], v[202:205]\n"
"s_waitcnt lgkmcnt(3)\n"
"v_exp_f32 v152, v152\n"
"v_mfma_f32_16x16x32_bf16 v[168:171], v[216:219], a[8:11], v[180:183]\n"
"ds_read_b64_tr_b16 a[244:245], v234 offset:512\n"
"v_cvt_pk_bf16_f32 v96, v152, v153\n"
"v_mfma_f32_16x16x32_bf16 v[168:171], v[220:223], a[12:15], v[168:171]\n"
"v_exp_f32 v165, v165\n"
"v_mfma_f32_16x16x32_bf16 v[168:171], v[208:211], a[0:3], v[168:171]\n"
"ds_read_b64_tr_b16 a[246:247], v234 offset:4608\n"
"v_cvt_pk_bf16_f32 v113, v162, v163\n"
"v_mfma_f32_16x16x32_bf16 v[168:171], v[212:215], a[4:7], v[168:171]\n"
"ds_read_b64_tr_b16 a[254:255], v235 offset:4608\n"
"v_mfma_f32_16x16x32_bf16 v[172:175], v[208:211], a[16:19], v[184:187]\n"
"ds_read_b64_tr_b16 v[224:225], v239 offset:512\n"
"v_cvt_pk_bf16_f32 v97, v154, v155\n"
"v_mfma_f32_16x16x32_bf16 v[172:175], v[212:215], a[20:23], v[172:175]\n"
"v_mfma_f32_16x16x32_bf16 v[172:175], v[216:219], a[24:27], v[172:175]\n"
"s_add_u32 s40, s56, s54\n"
"ds_read_b64_tr_b16 a[252:253], v235 offset:512\n"
"v_mfma_f32_16x16x32_bf16 v[172:175], v[220:223], a[28:31], v[172:175]\n"
"buffer_load_dwordx4 v207, s[20:23], s40 offen offset:3072 nt lds\n"
"v_mfma_f32_16x16x32_bf16 v[176:179], v[216:219], a[40:43], v[188:191]\n"
"s_add_u32 s55, s55, s59\n"
"v_exp_f32 v157, v157\n"
"v_mfma_f32_16x16x32_bf16 v[176:179], v[220:223], a[44:47], v[176:179]\n"
"s_or_b32 s55, s55, s58\n"
"v_exp_f32 v171, v171\n"
"v_mfma_f32_16x16x32_bf16 v[176:179], v[208:211], a[32:35], v[176:179]\n"
"s_xor_b32 s56, s55, s57\n"
"v_exp_f32 v159, v159\n"
"v_mfma_f32_16x16x32_bf16 v[176:179], v[212:215], a[36:39], v[176:179]\n"
"v_exp_f32 v167, v167\n"
"v_mfma_f32_16x16x32_bf16 v[152:155], v[208:211], a[192:195], v[192:195]\n"
"v_cvt_pk_bf16_f32 v105, v158, v159\n"
"v_mfma_f32_16x16x32_bf16 v[152:155], v[212:215], a[196:199], v[152:155]\n"
"ds_read_b128 v[208:211], v230 offset:16384\n"
"v_cvt_pk_bf16_f32 v104, v156, v157\n"
"v_mfma_f32_16x16x32_bf16 v[152:155], v[216:219], a[200:203], v[152:155]\n"
"v_exp_f32 v166, v166\n"
"v_mfma_f32_16x16x32_bf16 v[152:155], v[220:223], a[204:207], v[152:155]\n"
"s_waitcnt lgkmcnt(1)\n"
"ds_read_b128 v[216:219], v230 offset:16896\n"
"v_mfma_f32_16x16x32_bf16 a[232:235], v[124:127], a[244:247], a[232:235]\n"
"v_exp_f32 v169, v169\n"
"v_mfma_f32_16x16x32_bf16 v[24:27], v[128:131], a[244:247], v[24:27]\n"
"ds_read_b128 v[220:223], v231 offset:16896\n"
"v_mfma_f32_16x16x32_bf16 v[24:27], v[132:135], a[248:251], v[24:27]\n"
"v_exp_f32 v164, v164\n"
"v_mfma_f32_16x16x32_bf16 v[56:59], v[136:139], a[244:247], v[56:59]\n"
"ds_read_b128 v[212:215], v231 offset:16384\n"
"v_mfma_f32_16x16x32_bf16 v[56:59], v[140:143], a[248:251], v[56:59]\n"
"v_exp_f32 v178, v178\n"
"v_mfma_f32_16x16x32_bf16 v[88:91], v[144:147], a[244:247], v[88:91]\n"
"s_add_u32 m0, s44, 49152\n"
"ds_read_b64_tr_b16 a[246:247], v232 offset:4096\n"
"v_mfma_f32_16x16x32_bf16 v[88:91], v[148:151], a[248:251], v[88:91]\n"
"buffer_load_dwordx4 v240, s[16:19], s56 offen nt lds\n"
"v_mfma_f32_16x16x32_bf16 v[92:95], v[148:151], v[224:227], v[92:95]\n"
"v_exp_f32 v175, v175\n"
"v_mfma_f32_16x16x32_bf16 v[92:95], v[144:147], a[252:255], v[92:95]\n"
"v_exp_f32 v168, v168\n"
"v_mfma_f32_16x16x32_bf16 v[60:63], v[140:143], v[224:227], v[60:63]\n"
"v_exp_f32 v161, v161\n"
"v_mfma_f32_16x16x32_bf16 v[60:63], v[136:139], a[252:255], v[60:63]\n"
"v_exp_f32 v177, v177\n"
"v_mfma_f32_16x16x32_bf16 v[28:31], v[132:135], v[224:227], v[28:31]\n"
"ds_read_b64_tr_b16 v[226:227], v237 offset:4096\n"
"v_mfma_f32_16x16x32_bf16 v[28:31], v[128:131], a[252:255], v[28:31]\n"
"v_exp_f32 v172, v172\n"
"v_mfma_f32_16x16x32_bf16 a[236:239], v[124:127], a[252:255], a[236:239]\n"
"ds_read_b64_tr_b16 a[250:251], v236 offset:4096\n"
"v_cvt_pk_bf16_f32 v120, v164, v165\n"
"v_smfmac_f32_16x16x64_bf16 v[198:201], a[240:243], v[136:143], v241\n"
"s_waitcnt lgkmcnt(2)\n"
"ds_read_b64_tr_b16 a[252:253], v233 offset:0\n"
"v_mfma_f32_16x16x32_bf16 v[156:159], v[208:211], a[0:3], v[180:183]\n"
"ds_read_b64_tr_b16 a[248:249], v236 offset:0\n"
"v_cvt_pk_bf16_f32 v121, v166, v167\n"
"v_mfma_f32_16x16x32_bf16 v[156:159], v[212:215], a[4:7], v[156:159]\n"
"ds_read_b64_tr_b16 v[224:225], v237 offset:0\n"
"v_cvt_pk_bf16_f32 v98, v168, v169\n"
"v_mfma_f32_16x16x32_bf16 v[156:159], v[216:219], a[8:11], v[156:159]\n"
"ds_read_b64_tr_b16 a[254:255], v233 offset:4096\n"
"v_cvt_pk_bf16_f32 v112, v160, v161\n"
"v_mfma_f32_16x16x32_bf16 v[156:159], v[220:223], a[12:15], v[156:159]\n"
"ds_read_b64_tr_b16 a[244:245], v232 offset:0\n"
"v_mfma_f32_16x16x32_bf16 v[160:163], v[216:219], a[24:27], v[184:187]\n"
"v_exp_f32 v176, v176\n"
"v_mfma_f32_16x16x32_bf16 v[160:163], v[220:223], a[28:31], v[160:163]\n"
"v_exp_f32 v152, v152\n"
"v_mfma_f32_16x16x32_bf16 v[160:163], v[208:211], a[16:19], v[160:163]\n"
"s_add_u32 s40, s56, s52\n"
"v_cvt_pk_bf16_f32 v114, v176, v177\n"
"v_mfma_f32_16x16x32_bf16 v[160:163], v[212:215], a[20:23], v[160:163]\n"
"buffer_load_dwordx4 v207, s[16:19], s40 offen offset:1024 nt lds\n"
"v_mfma_f32_16x16x32_bf16 v[164:167], v[208:211], a[32:35], v[188:191]\n"
"v_exp_f32 v170, v170\n"
"v_mfma_f32_16x16x32_bf16 v[164:167], v[212:215], a[36:39], v[164:167]\n"
"v_exp_f32 v173, v173\n"
"v_mfma_f32_16x16x32_bf16 v[164:167], v[216:219], a[40:43], v[164:167]\n"
"v_exp_f32 v153, v153\n"
"v_mfma_f32_16x16x32_bf16 v[164:167], v[220:223], a[44:47], v[164:167]\n"
"s_waitcnt lgkmcnt(0)\n"
"v_exp_f32 v174, v174\n"
"v_mfma_f32_16x16x32_bf16 a[208:211], v[124:127], a[244:247], a[208:211]\n"
"ds_read_b128 v[208:211], v230 offset:20480\n"
"v_cvt_pk_bf16_f32 v107, v174, v175\n"
"v_mfma_f32_16x16x32_bf16 v[0:3], v[128:131], a[244:247], v[0:3]\n"
"v_exp_f32 v156, v156\n"
"v_mfma_f32_16x16x32_bf16 v[0:3], v[132:135], a[248:251], v[0:3]\n"
"ds_read_b128 v[220:223], v231 offset:20992\n"
"v_cvt_pk_bf16_f32 v106, v172, v173\n"
"v_mfma_f32_16x16x32_bf16 v[32:35], v[136:139], a[244:247], v[32:35]\n"
"v_exp_f32 v161, v161\n"
"v_mfma_f32_16x16x32_bf16 v[32:35], v[140:143], a[248:251], v[32:35]\n"
"ds_read_b128 v[216:219], v230 offset:20992\n"
"v_cvt_pk_bf16_f32 v122, v152, v153\n"
"v_mfma_f32_16x16x32_bf16 v[64:67], v[144:147], a[244:247], v[64:67]\n"
"s_add_u32 s40, s56, s53\n"
"ds_read_b128 v[212:215], v231 offset:20480\n"
"v_mfma_f32_16x16x32_bf16 v[64:67], v[148:151], a[248:251], v[64:67]\n"
"buffer_load_dwordx4 v240, s[16:19], s40 offen offset:2048 nt lds\n"
"v_mfma_f32_16x16x32_bf16 v[68:71], v[148:151], v[224:227], v[68:71]\n"
"v_exp_f32 v159, v159\n"
"v_mfma_f32_16x16x32_bf16 v[68:71], v[144:147], a[252:255], v[68:71]\n"
"v_exp_f32 v155, v155\n"
"v_mfma_f32_16x16x32_bf16 v[36:39], v[140:143], v[224:227], v[36:39]\n"
"v_exp_f32 v160, v160\n"
"v_mfma_f32_16x16x32_bf16 v[36:39], v[136:139], a[252:255], v[36:39]\n"
"v_exp_f32 v179, v179\n"
"v_mfma_f32_16x16x32_bf16 v[4:7], v[132:135], v[224:227], v[4:7]\n"
"ds_read_b64_tr_b16 a[244:245], v232 offset:512\n"
"ds_read_b64_tr_b16 v[224:225], v237 offset:512\n"
"v_mfma_f32_16x16x32_bf16 v[4:7], v[128:131], a[252:255], v[4:7]\n"
"v_exp_f32 v158, v158\n"
"v_mfma_f32_16x16x32_bf16 a[212:215], v[124:127], a[252:255], a[212:215]\n"
"ds_read_b64_tr_b16 a[246:247], v232 offset:4608\n"
"ds_read_b64_tr_b16 a[250:251], v236 offset:4608\n"
"v_smfmac_f32_16x16x64_bf16 v[200:203], a[240:243], v[144:151], v241\n"
"s_waitcnt lgkmcnt(4)\n"
"ds_read_b64_tr_b16 v[226:227], v237 offset:4608\n"
"v_cvt_pk_bf16_f32 v99, v170, v171\n"
"v_mfma_f32_16x16x32_bf16 v[168:171], v[208:211], a[0:3], v[180:183]\n"
"v_exp_f32 v164, v164\n"
"v_mfma_f32_16x16x32_bf16 v[168:171], v[212:215], a[4:7], v[168:171]\n"
"ds_read_b64_tr_b16 a[248:249], v236 offset:512\n"
"ds_read_b64_tr_b16 a[254:255], v233 offset:4608\n"
"v_mfma_f32_16x16x32_bf16 v[168:171], v[216:219], a[8:11], v[168:171]\n"
"s_add_u32 s40, s56, s54\n"
"ds_read_b64_tr_b16 a[252:253], v233 offset:512\n"
"v_mfma_f32_16x16x32_bf16 v[168:171], v[220:223], a[12:15], v[168:171]\n"
"buffer_load_dwordx4 v207, s[16:19], s40 offen offset:3072 nt lds\n"
"v_mfma_f32_16x16x32_bf16 v[172:175], v[216:219], a[24:27], v[184:187]\n"
"v_exp_f32 v154, v154\n"
"v_mfma_f32_16x16x32_bf16 v[172:175], v[220:223], a[28:31], v[172:175]\n"
"v_exp_f32 v167, v167\n"
"v_mfma_f32_16x16x32_bf16 v[172:175], v[208:211], a[16:19], v[172:175]\n"
"v_exp_f32 v163, v163\n"
"v_mfma_f32_16x16x32_bf16 v[172:175], v[212:215], a[20:23], v[172:175]\n"
"s_cbranch_vccnz LPCL1_%=\nLPKL1_%=:\n"
"v_cvt_pk_bf16_f32 v115, v178, v179\n"
"v_mfma_f32_16x16x32_bf16 v[176:179], v[208:211], a[32:35], v[188:191]\n"
"v_exp_f32 v165, v165\n"
"v_mfma_f32_16x16x32_bf16 v[176:179], v[212:215], a[36:39], v[176:179]\n"
"v_exp_f32 v157, v157\n"
"v_mfma_f32_16x16x32_bf16 v[176:179], v[216:219], a[40:43], v[176:179]\n"
"v_exp_f32 v166, v166\n"
"v_mfma_f32_16x16x32_bf16 v[176:179], v[220:223], a[44:47], v[176:179]\n"
"s_waitcnt lgkmcnt(0)\n"
"v_mfma_f32_16x16x32_bf16 a[224:227], v[124:127], a[244:247], a[224:227]\n"
"v_exp_f32 v162, v162\n"
"v_mfma_f32_16x16x32_bf16 v[16:19], v[128:131], a[244:247], v[16:19]\n"
"s_waitcnt vmcnt(8)\n"
"v_exp_f32 v168, v168\n"
"v_mfma_f32_16x16x32_bf16 v[16:19], v[132:135], a[248:251], v[16:19]\n"
"s_barrier\n"
"v_mfma_f32_16x16x32_bf16 v[48:51], v[136:139], a[244:247], v[48:51]\n"
"ds_read_b128 v[208:211], v228 offset:32768\n"
"v_cvt_pk_bf16_f32 v101, v158, v159\n"
"v_mfma_f32_16x16x32_bf16 v[48:51], v[140:143], a[248:251], v[48:51]\n"
"ds_read_b128 v[216:219], v228 offset:33280\n"
"v_cvt_pk_bf16_f32 v109, v162, v163\n"
"v_mfma_f32_16x16x32_bf16 v[80:83], v[144:147], a[244:247], v[80:83]\n"
"ds_read_b128 v[220:223], v229 offset:33280\n"
"v_cvt_pk_bf16_f32 v123, v154, v155\n"
"v_mfma_f32_16x16x32_bf16 v[80:83], v[148:151], a[248:251], v[80:83]\n"
"ds_read_b128 v[212:215], v229 offset:32768\n"
"v_cvt_pk_bf16_f32 v117, v166, v167\n"
"v_mfma_f32_16x16x32_bf16 v[84:87], v[148:151], v[224:227], v[84:87]\n"
"v_exp_f32 v176, v176\n"
"v_mfma_f32_16x16x32_bf16 v[84:87], v[144:147], a[252:255], v[84:87]\n"
"v_exp_f32 v169, v169\n"
"v_mfma_f32_16x16x32_bf16 v[52:55], v[140:143], v[224:227], v[52:55]\n"
"ds_read_b64_tr_b16 a[246:247], v234 offset:20480\n"
"v_cvt_pk_bf16_f32 v116, v164, v165\n"
"v_mfma_f32_16x16x32_bf16 v[52:55], v[136:139], a[252:255], v[52:55]\n"
"v_exp_f32 v177, v177\n"
"v_mfma_f32_16x16x32_bf16 v[20:23], v[132:135], v[224:227], v[20:23]\n"
"ds_read_b64_tr_b16 a[250:251], v238 offset:20480\n"
"v_cvt_pk_bf16_f32 v118, v176, v177\n"
"v_mfma_f32_16x16x32_bf16 v[20:23], v[128:131], a[252:255], v[20:23]\n"
"v_exp_f32 v174, v174\n"
"v_mfma_f32_16x16x32_bf16 a[228:231], v[124:127], a[252:255], a[228:231]\n"
"s_waitcnt lgkmcnt(3)\n"
"ds_read_b64_tr_b16 a[254:255], v235 offset:20480\n"
"v_cvt_pk_bf16_f32 v100, v156, v157\n"
"LAY2_%=:\n"
"v_mfma_f32_16x16x32_bf16 v[152:155], v[208:211], a[0:3], v[180:183]\n"
"s_waitcnt lgkmcnt(3)\n"
"ds_read_b64_tr_b16 a[248:249], v238 offset:16384\n"
"v_cvt_pk_bf16_f32 v102, v168, v169\n"
"v_mfma_f32_16x16x32_bf16 v[152:155], v[212:215], a[4:7], v[152:155]\n"
"v_exp_f32 v172, v172\n"
"v_mfma_f32_16x16x32_bf16 v[152:155], v[216:219], a[8:11], v[152:155]\n"
"ds_read_b64_tr_b16 a[244:245], v234 offset:16384\n"
"v_cvt_pk_bf16_f32 v108, v160, v161\n"
"v_mfma_f32_16x16x32_bf16 v[152:155], v[220:223], a[12:15], v[152:155]\n"
"v_exp_f32 v175, v175\n"
"v_mfma_f32_16x16x32_bf16 v[156:159], v[216:219], a[24:27], v[184:187]\n"
"ds_read_b64_tr_b16 a[252:253], v235 offset:16384\n"
"v_cvt_pk_bf16_f32 v111, v174, v175\n"
"v_mfma_f32_16x16x32_bf16 v[156:159], v[220:223], a[28:31], v[156:159]\n"
"ds_read_b64_tr_b16 v[224:225], v239 offset:16384\n"
"v_mfma_f32_16x16x32_bf16 v[156:159], v[208:211], a[16:19], v[156:159]\n"
"s_add_u32 m0, s44, 114688\n"
"ds_read_b64_tr_b16 v[226:227], v239 offset:20480\n"
"v_mfma_f32_16x16x32_bf16 v[156:159], v[212:215], a[20:23], v[156:159]\n"
"buffer_load_dwordx4 v240, s[20:23], s56 offen nt lds\n"
"v_mfma_f32_16x16x32_bf16 v[160:163], v[208:211], a[32:35], v[188:191]\n"
"v_exp_f32 v178, v178\n"
"v_mfma_f32_16x16x32_bf16 v[160:163], v[212:215], a[36:39], v[160:163]\n"
"v_exp_f32 v171, v171\n"
"v_mfma_f32_16x16x32_bf16 v[160:163], v[216:219], a[40:43], v[160:163]\n"
"s_add_u32 s40, s56, s52\n"
"v_mfma_f32_16x16x32_bf16 v[160:163], v[220:223], a[44:47], v[160:163]\n"
"buffer_load_dwordx4 v207, s[20:23], s40 offen offset:1024 nt lds\n"
"v_mfma_f32_16x16x32_bf16 v[164:167], v[216:219], a[200:203], v[192:195]\n"
"v_exp_f32 v170, v170\n"
"v_mfma_f32_16x16x32_bf16 v[164:167], v[220:223], a[204:207], v[164:167]\n"
"v_exp_f32 v153, v153\n"
"v_mfma_f32_16x16x32_bf16 v[164:167], v[208:211], a[192:195], v[164:167]\n"
"v_exp_f32 v179, v179\n"
"v_mfma_f32_16x16x32_bf16 v[164:167], v[212:215], a[196:199], v[164:167]\n"
"s_waitcnt lgkmcnt(0)\n"
"v_exp_f32 v158, v158\n"
"v_mfma_f32_16x16x32_bf16 a[216:219], v[120:123], a[244:247], a[216:219]\n"
"ds_read_b128 v[216:219], v228 offset:37376\n"
"v_cvt_pk_bf16_f32 v103, v170, v171\n"
"v_mfma_f32_16x16x32_bf16 v[8:11], v[96:99], a[244:247], v[8:11]\n"
"v_exp_f32 v173, v173\n"
"v_mfma_f32_16x16x32_bf16 v[8:11], v[100:103], a[248:251], v[8:11]\n"
"ds_read_b128 v[208:211], v228 offset:36864\n"
"v_cvt_pk_bf16_f32 v110, v172, v173\n"
"v_mfma_f32_16x16x32_bf16 v[40:43], v[104:107], a[244:247], v[40:43]\n"
"v_exp_f32 v162, v162\n"
"v_mfma_f32_16x16x32_bf16 v[40:43], v[108:111], a[248:251], v[40:43]\n"
"ds_read_b128 v[220:223], v229 offset:37376\n"
"v_cvt_pk_bf16_f32 v119, v178, v179\n"
"v_mfma_f32_16x16x32_bf16 v[72:75], v[112:115], a[244:247], v[72:75]\n"
"s_add_u32 s40, s56, s53\n"
"ds_read_b128 v[212:215], v229 offset:36864\n"
"v_mfma_f32_16x16x32_bf16 v[72:75], v[116:119], a[248:251], v[72:75]\n"
"buffer_load_dwordx4 v240, s[20:23], s40 offen offset:2048 nt lds\n"
"v_mfma_f32_16x16x32_bf16 v[76:79], v[116:119], v[224:227], v[76:79]\n"
"v_exp_f32 v163, v163\n"
"v_mfma_f32_16x16x32_bf16 v[76:79], v[112:115], a[252:255], v[76:79]\n"
"v_exp_f32 v156, v156\n"
"v_mfma_f32_16x16x32_bf16 v[44:47], v[108:111], v[224:227], v[44:47]\n"
"v_exp_f32 v160, v160\n"
"v_mfma_f32_16x16x32_bf16 v[44:47], v[104:107], a[252:255], v[44:47]\n"
"v_exp_f32 v155, v155\n"
"v_mfma_f32_16x16x32_bf16 v[12:15], v[100:103], v[224:227], v[12:15]\n"
"ds_read_b64_tr_b16 v[226:227], v239 offset:20992\n"
"v_mfma_f32_16x16x32_bf16 v[12:15], v[96:99], a[252:255], v[12:15]\n"
"ds_read_b64_tr_b16 a[248:249], v238 offset:16896\n"
"v_mfma_f32_16x16x32_bf16 a[220:223], v[120:123], a[252:255], a[220:223]\n"
"v_exp_f32 v154, v154\n"
"v_smfmac_f32_16x16x64_bf16 v[196:199], a[240:243], v[96:103], v241\n"
"ds_read_b64_tr_b16 a[250:251], v238 offset:20992\n"
"v_mfma_f32_16x16x32_bf16 v[202:205], a[240:243], v[120:123], v[202:205]\n"
"s_waitcnt lgkmcnt(3)\n"
"v_exp_f32 v152, v152\n"
"v_mfma_f32_16x16x32_bf16 v[168:171], v[216:219], a[8:11], v[180:183]\n"
"ds_read_b64_tr_b16 a[244:245], v234 offset:16896\n"
"v_cvt_pk_bf16_f32 v128, v152, v153\n"
"v_mfma_f32_16x16x32_bf16 v[168:171], v[220:223], a[12:15], v[168:171]\n"
"v_exp_f32 v165, v165\n"
"v_mfma_f32_16x16x32_bf16 v[168:171], v[208:211], a[0:3], v[168:171]\n"
"ds_read_b64_tr_b16 a[246:247], v234 offset:20992\n"
"v_cvt_pk_bf16_f32 v145, v162, v163\n"
"v_mfma_f32_16x16x32_bf16 v[168:171], v[212:215], a[4:7], v[168:171]\n"
"ds_read_b64_tr_b16 a[254:255], v235 offset:20992\n"
"v_mfma_f32_16x16x32_bf16 v[172:175], v[208:211], a[16:19], v[184:187]\n"
"ds_read_b64_tr_b16 v[224:225], v239 offset:16896\n"
"v_cvt_pk_bf16_f32 v129, v154, v155\n"
"v_mfma_f32_16x16x32_bf16 v[172:175], v[212:215], a[20:23], v[172:175]\n"
"v_mfma_f32_16x16x32_bf16 v[172:175], v[216:219], a[24:27], v[172:175]\n"
"s_add_u32 s40, s56, s54\n"
"ds_read_b64_tr_b16 a[252:253], v235 offset:16896\n"
"v_mfma_f32_16x16x32_bf16 v[172:175], v[220:223], a[28:31], v[172:175]\n"
"buffer_load_dwordx4 v207, s[20:23], s40 offen offset:3072 nt lds\n"
"v_mfma_f32_16x16x32_bf16 v[176:179], v[216:219], a[40:43], v[188:191]\n"
"s_add_u32 s55, s55, s59\n"
"v_exp_f32 v157, v157\n"
"v_mfma_f32_16x16x32_bf16 v[176:179], v[220:223], a[44:47], v[176:179]\n"
"s_or_b32 s55, s55, s58\n"
"v_exp_f32 v171, v171\n"
"v_mfma_f32_16x16x32_bf16 v[176:179], v[208:211], a[32:35], v[176:179]\n"
"s_xor_b32 s56, s55, s57\n"
"v_exp_f32 v159, v159\n"
"v_mfma_f32_16x16x32_bf16 v[176:179], v[212:215], a[36:39], v[176:179]\n"
"s_cmp_ge_u32 s47, 15\n"
"v_exp_f32 v167, v167\n"
"v_mfma_f32_16x16x32_bf16 v[152:155], v[208:211], a[192:195], v[192:195]\n"
"s_cbranch_scc1 LSW_%=\nLSWK_%=:\n"
"v_cvt_pk_bf16_f32 v137, v158, v159\n"
"v_mfma_f32_16x16x32_bf16 v[152:155], v[212:215], a[196:199], v[152:155]\n"
"ds_read_b128 v[208:211], v230 offset:32768\n"
"v_cvt_pk_bf16_f32 v136, v156, v157\n"
"v_mfma_f32_16x16x32_bf16 v[152:155], v[216:219], a[200:203], v[152:155]\n"
"v_exp_f32 v166, v166\n"
"v_mfma_f32_16x16x32_bf16 v[152:155], v[220:223], a[204:207], v[152:155]\n"
"s_waitcnt lgkmcnt(1)\n"
"ds_read_b128 v[216:219], v230 offset:33280\n"
"v_mfma_f32_16x16x32_bf16 a[232:235], v[120:123], a[244:247], a[232:235]\n"
"v_exp_f32 v169, v169\n"
"v_mfma_f32_16x16x32_bf16 v[24:27], v[96:99], a[244:247], v[24:27]\n"
"ds_read_b128 v[220:223], v231 offset:33280\n"
"v_mfma_f32_16x16x32_bf16 v[24:27], v[100:103], a[248:251], v[24:27]\n"
"v_exp_f32 v164, v164\n"
"v_mfma_f32_16x16x32_bf16 v[56:59], v[104:107], a[244:247], v[56:59]\n"
"ds_read_b128 v[212:215], v231 offset:32768\n"
"v_mfma_f32_16x16x32_bf16 v[56:59], v[108:111], a[248:251], v[56:59]\n"
"v_exp_f32 v178, v178\n"
"v_mfma_f32_16x16x32_bf16 v[88:91], v[112:115], a[244:247], v[88:91]\n"
"s_mov_b32 m0, s44\n"
"ds_read_b64_tr_b16 a[246:247], v232 offset:20480\n"
"v_mfma_f32_16x16x32_bf16 v[88:91], v[116:119], a[248:251], v[88:91]\n"
"buffer_load_dwordx4 v240, s[16:19], s56 offen nt lds\n"
"v_mfma_f32_16x16x32_bf16 v[92:95], v[116:119], v[224:227], v[92:95]\n"
"v_exp_f32 v175, v175\n"
"v_mfma_f32_16x16x32_bf16 v[92:95], v[112:115], a[252:255], v[92:95]\n"
"v_exp_f32 v168, v168\n"
"v_mfma_f32_16x16x32_bf16 v[60:63], v[108:111], v[224:227], v[60:63]\n"
"v_exp_f32 v161, v161\n"
"v_mfma_f32_16x16x32_bf16 v[60:63], v[104:107], a[252:255], v[60:63]\n"
"v_exp_f32 v177, v177\n"
"v_mfma_f32_16x16x32_bf16 v[28:31], v[100:103], v[224:227], v[28:31]\n"
"ds_read_b64_tr_b16 v[226:227], v237 offset:20480\n"
"v_mfma_f32_16x16x32_bf16 v[28:31], v[96:99], a[252:255], v[28:31]\n"
"v_exp_f32 v172, v172\n"
"v_mfma_f32_16x16x32_bf16 a[236:239], v[120:123], a[252:255], a[236:239]\n"
"ds_read_b64_tr_b16 a[250:251], v236 offset:20480\n"
"v_cvt_pk_bf16_f32 v124, v164, v165\n"
"v_smfmac_f32_16x16x64_bf16 v[198:201], a[240:243], v[104:111], v241\n"
"s_waitcnt lgkmcnt(2)\n"
"ds_read_b64_tr_b16 a[252:253], v233 offset:16384\n"
"v_mfma_f32_16x16x32_bf16 v[156:159], v[208:211], a[0:3], v[180:183]\n"
"ds_read_b64_tr_b16 a[248:249], v236 offset:16384\n"
"v_cvt_pk_bf16_f32 v125, v166, v167\n"
"v_mfma_f32_16x16x32_bf16 v[156:159], v[212:215], a[4:7], v[156:159]\n"
"ds_read_b64_tr_b16 v[224:225], v237 offset:16384\n"
"v_cvt_pk_bf16_f32 v130, v168, v169\n"
"v_mfma_f32_16x16x32_bf16 v[156:159], v[216:219], a[8:11], v[156:159]\n"
"ds_read_b64_tr_b16 a[254:255], v233 offset:20480\n"
"v_cvt_pk_bf16_f32 v144, v160, v161\n"
"v_mfma_f32_16x16x32_bf16 v[156:159], v[220:223], a[12:15], v[156:159]\n"
"ds_read_b64_tr_b16 a[244:245], v232 offset:16384\n"
"v_mfma_f32_16x16x32_bf16 v[160:163], v[216:219], a[24:27], v[184:187]\n"
"v_exp_f32 v176, v176\n"
"v_mfma_f32_16x16x32_bf16 v[160:163], v[220:223], a[28:31], v[160:163]\n"
"v_exp_f32 v152, v152\n"
"v_mfma_f32_16x16x32_bf16 v[160:163], v[208:211], a[16:19], v[160:163]\n"
"s_add_u32 s40, s56, s52\n"
"v_cvt_pk_bf16_f32 v146, v176, v177\n"
"v_mfma_f32_16x16x32_bf16 v[160:163], v[212:215], a[20:23], v[160:163]\n"
"buffer_load_dwordx4 v207, s[16:19], s40 offen offset:1024 nt lds\n"
"v_mfma_f32_16x16x32_bf16 v[164:167], v[208:211], a[32:35], v[188:191]\n"
"v_exp_f32 v170, v170\n"
"v_mfma_f32_16x16x32_bf16 v[164:167], v[212:215], a[36:39], v[164:167]\n"
"v_exp_f32 v173, v173\n"
"v_mfma_f32_16x16x32_bf16 v[164:167], v[216:219], a[40:43], v[164:167]\n"
"v_exp_f32 v153, v153\n"
"v_mfma_f32_16x16x32_bf16 v[164:167], v[220:223], a[44:47], v[164:167]\n"
"s_waitcnt lgkmcnt(0)\n"
"v_exp_f32 v174, v174\n"
"v_mfma_f32_16x16x32_bf16 a[208:211], v[120:123], a[244:247], a[208:211]\n"
"ds_read_b128 v[208:211], v230 offset:36864\n"
"v_cvt_pk_bf16_f32 v139, v174, v175\n"
"v_mfma_f32_16x16x32_bf16 v[0:3], v[96:99], a[244:247], v[0:3]\n"
"v_exp_f32 v156, v156\n"
"v_mfma_f32_16x16x32_bf16 v[0:3], v[100:103], a[248:251], v[0:3]\n"
"ds_read_b128 v[220:223], v231 offset:37376\n"
"v_cvt_pk_bf16_f32 v138, v172, v173\n"
"v_mfma_f32_16x16x32_bf16 v[32:35], v[104:107], a[244:247], v[32:35]\n"
"v_exp_f32 v161, v161\n"
"v_mfma_f32_16x16x32_bf16 v[32:35], v[108:111], a[248:251], v[32:35]\n"
"ds_read_b128 v[216:219], v230 offset:37376\n"
"v_cvt_pk_bf16_f32 v126, v152, v153\n"
"v_mfma_f32_16x16x32_bf16 v[64:67], v[112:115], a[244:247], v[64:67]\n"
"s_add_u32 s40, s56, s53\n"
"ds_read_b128 v[212:215], v231 offset:36864\n"
"v_mfma_f32_16x16x32_bf16 v[64:67], v[116:119], a[248:251], v[64:67]\n"
"buffer_load_dwordx4 v240, s[16:19], s40 offen offset:2048 nt lds\n"
"v_mfma_f32_16x16x32_bf16 v[68:71], v[116:119], v[224:227], v[68:71]\n"
"v_exp_f32 v159, v159\n"
"v_mfma_f32_16x16x32_bf16 v[68:71], v[112:115], a[252:255], v[68:71]\n"
"v_exp_f32 v155, v155\n"
"v_mfma_f32_16x16x32_bf16 v[36:39], v[108:111], v[224:227], v[36:39]\n"
"v_exp_f32 v160, v160\n"
"v_mfma_f32_16x16x32_bf16 v[36:39], v[104:107], a[252:255], v[36:39]\n"
"v_exp_f32 v179, v179\n"
"v_mfma_f32_16x16x32_bf16 v[4:7], v[100:103], v[224:227], v[4:7]\n"
"ds_read_b64_tr_b16 a[244:245], v232 offset:16896\n"
"ds_read_b64_tr_b16 v[224:225], v237 offset:16896\n"
"v_mfma_f32_16x16x32_bf16 v[4:7], v[96:99], a[252:255], v[4:7]\n"
"v_exp_f32 v158, v158\n"
"v_mfma_f32_16x16x32_bf16 a[212:215], v[120:123], a[252:255], a[212:215]\n"
"ds_read_b64_tr_b16 a[246:247], v232 offset:20992\n"
"ds_read_b64_tr_b16 a[250:251], v236 offset:20992\n"
"v_smfmac_f32_16x16x64_bf16 v[200:203], a[240:243], v[112:119], v241\n"
"s_waitcnt lgkmcnt(4)\n"
"ds_read_b64_tr_b16 v[226:227], v237 offset:20992\n"
"v_cvt_pk_bf16_f32 v131, v170, v171\n"
"v_mfma_f32_16x16x32_bf16 v[168:171], v[208:211], a[0:3], v[180:183]\n"
"v_exp_f32 v164, v164\n"
"v_mfma_f32_16x16x32_bf16 v[168:171], v[212:215], a[4:7], v[168:171]\n"
"ds_read_b64_tr_b16 a[248:249], v236 offset:16896\n"
"ds_read_b64_tr_b16 a[254:255], v233 offset:20992\n"
"v_mfma_f32_16x16x32_bf16 v[168:171], v[216:219], a[8:11], v[168:171]\n"
"s_add_u32 s40, s56, s54\n"
"ds_read_b64_tr_b16 a[252:253], v233 offset:16896\n"
"v_mfma_f32_16x16x32_bf16 v[168:171], v[220:223], a[12:15], v[168:171]\n"
"buffer_load_dwordx4 v207, s[16:19], s40 offen offset:3072 nt lds\n"
"v_mfma_f32_16x16x32_bf16 v[172:175], v[216:219], a[24:27], v[184:187]\n"
"v_exp_f32 v154, v154\n"
"v_mfma_f32_16x16x32_bf16 v[172:175], v[220:223], a[28:31], v[172:175]\n"
"v_exp_f32 v167, v167\n"
"v_mfma_f32_16x16x32_bf16 v[172:175], v[208:211], a[16:19], v[172:175]\n"
"v_exp_f32 v163, v163\n"
"v_mfma_f32_16x16x32_bf16 v[172:175], v[212:215], a[20:23], v[172:175]\n"
"s_cbranch_vccnz LPCL2_%=\nLPKL2_%=:\n"
"v_cvt_pk_bf16_f32 v147, v178, v179\n"
"v_mfma_f32_16x16x32_bf16 v[176:179], v[208:211], a[32:35], v[188:191]\n"
"v_exp_f32 v165, v165\n"
"v_mfma_f32_16x16x32_bf16 v[176:179], v[212:215], a[36:39], v[176:179]\n"
"v_exp_f32 v157, v157\n"
"v_mfma_f32_16x16x32_bf16 v[176:179], v[216:219], a[40:43], v[176:179]\n"
"v_exp_f32 v166, v166\n"
"v_mfma_f32_16x16x32_bf16 v[176:179], v[220:223], a[44:47], v[176:179]\n"
"s_waitcnt lgkmcnt(0)\n"
"v_mfma_f32_16x16x32_bf16 a[224:227], v[120:123], a[244:247], a[224:227]\n"
"v_exp_f32 v162, v162\n"
"v_mfma_f32_16x16x32_bf16 v[16:19], v[96:99], a[244:247], v[16:19]\n"
"s_waitcnt vmcnt(8)\n"
"v_exp_f32 v168, v168\n"
"v_mfma_f32_16x16x32_bf16 v[16:19], v[100:103], a[248:251], v[16:19]\n"
"s_barrier\n"
"v_mfma_f32_16x16x32_bf16 v[48:51], v[104:107], a[244:247], v[48:51]\n"
"ds_read_b128 v[208:211], v228 offset:49152\n"
"v_cvt_pk_bf16_f32 v133, v158, v159\n"
"v_mfma_f32_16x16x32_bf16 v[48:51], v[108:111], a[248:251], v[48:51]\n"
"ds_read_b128 v[216:219], v228 offset:49664\n"
"v_cvt_pk_bf16_f32 v141, v162, v163\n"
"v_mfma_f32_16x16x32_bf16 v[80:83], v[112:115], a[244:247], v[80:83]\n"
"ds_read_b128 v[220:223], v229 offset:49664\n"
"v_cvt_pk_bf16_f32 v127, v154, v155\n"
"v_mfma_f32_16x16x32_bf16 v[80:83], v[116:119], a[248:251], v[80:83]\n"
"ds_read_b128 v[212:215], v229 offset:49152\n"
"v_cvt_pk_bf16_f32 v149, v166, v167\n"
"v_mfma_f32_16x16x32_bf16 v[84:87], v[116:119], v[224:227], v[84:87]\n"
"v_exp_f32 v176, v176\n"
"v_mfma_f32_16x16x32_bf16 v[84:87], v[112:115], a[252:255], v[84:87]\n"
"v_exp_f32 v169, v169\n"
"v_mfma_f32_16x16x32_bf16 v[52:55], v[108:111], v[224:227], v[52:55]\n"
"ds_read_b64_tr_b16 a[246:247], v234 offset:36864\n"
"v_cvt_pk_bf16_f32 v148, v164, v165\n"
"v_mfma_f32_16x16x32_bf16 v[52:55], v[104:107], a[252:255], v[52:55]\n"
"v_exp_f32 v177, v177\n"
"v_mfma_f32_16x16x32_bf16 v[20:23], v[100:103], v[224:227], v[20:23]\n"
"ds_read_b64_tr_b16 a[250:251], v238 offset:36864\n"
"v_cvt_pk_bf16_f32 v150, v176, v177\n"
"v_mfma_f32_16x16x32_bf16 v[20:23], v[96:99], a[252:255], v[20:23]\n"
"v_exp_f32 v174, v174\n"
"v_mfma_f32_16x16x32_bf16 a[228:231], v[120:123], a[252:255], a[228:231]\n"
"s_waitcnt lgkmcnt(3)\n"
"ds_read_b64_tr_b16 a[254:255], v235 offset:36864\n"
"v_cvt_pk_bf16_f32 v132, v156, v157\n"
"v_mfma_f32_16x16x32_bf16 v[152:155], v[208:211], a[0:3], v[180:183]\n"
"s_waitcnt lgkmcnt(3)\n"
"ds_read_b64_tr_b16 a[248:249], v238 offset:32768\n"
"v_cvt_pk_bf16_f32 v134, v168, v169\n"
"v_mfma_f32_16x16x32_bf16 v[152:155], v[212:215], a[4:7], v[152:155]\n"
"v_exp_f32 v172, v172\n"
"v_mfma_f32_16x16x32_bf16 v[152:155], v[216:219], a[8:11], v[152:155]\n"
"ds_read_b64_tr_b16 a[244:245], v234 offset:32768\n"
"v_cvt_pk_bf16_f32 v140, v160, v161\n"
"v_mfma_f32_16x16x32_bf16 v[152:155], v[220:223], a[12:15], v[152:155]\n"
"v_exp_f32 v175, v175\n"
"v_mfma_f32_16x16x32_bf16 v[156:159], v[216:219], a[24:27], v[184:187]\n"
"ds_read_b64_tr_b16 a[252:253], v235 offset:32768\n"
"v_cvt_pk_bf16_f32 v143, v174, v175\n"
"v_mfma_f32_16x16x32_bf16 v[156:159], v[220:223], a[28:31], v[156:159]\n"
"ds_read_b64_tr_b16 v[224:225], v239 offset:32768\n"
"v_mfma_f32_16x16x32_bf16 v[156:159], v[208:211], a[16:19], v[156:159]\n"
"s_add_u32 m0, s44, 65536\n"
"ds_read_b64_tr_b16 v[226:227], v239 offset:36864\n"
"v_mfma_f32_16x16x32_bf16 v[156:159], v[212:215], a[20:23], v[156:159]\n"
"buffer_load_dwordx4 v240, s[20:23], s56 offen nt lds\n"
"v_mfma_f32_16x16x32_bf16 v[160:163], v[208:211], a[32:35], v[188:191]\n"
"v_exp_f32 v178, v178\n"
"v_mfma_f32_16x16x32_bf16 v[160:163], v[212:215], a[36:39], v[160:163]\n"
"v_exp_f32 v171, v171\n"
"v_mfma_f32_16x16x32_bf16 v[160:163], v[216:219], a[40:43], v[160:163]\n"
"s_add_u32 s40, s56, s52\n"
"v_mfma_f32_16x16x32_bf16 v[160:163], v[220:223], a[44:47], v[160:163]\n"
"buffer_load_dwordx4 v207, s[20:23], s40 offen offset:1024 nt lds\n"
"v_mfma_f32_16x16x32_bf16 v[164:167], v[216:219], a[200:203], v[192:195]\n"
"v_exp_f32 v170, v170\n"
"v_mfma_f32_16x16x32_bf16 v[164:167], v[220:223], a[204:207], v[164:167]\n"
"v_exp_f32 v153, v153\n"
"v_mfma_f32_16x16x32_bf16 v[164:167], v[208:211], a[192:195], v[164:167]\n"
"v_exp_f32 v179, v179\n"
"v_mfma_f32_16x16x32_bf16 v[164:167], v[212:215], a[196:199], v[164:167]\n"
"s_waitcnt lgkmcnt(0)\n"
"v_exp_f32 v158, v158\n"
"v_mfma_f32_16x16x32_bf16 a[216:219], v[124:127], a[244:247], a[216:219]\n"
"ds_read_b128 v[216:219], v228 offset:53760\n"
"v_cvt_pk_bf16_f32 v135, v170, v171\n"
"v_mfma_f32_16x16x32_bf16 v[8:11], v[128:131], a[244:247], v[8:11]\n"
"v_exp_f32 v173, v173\n"
"v_mfma_f32_16x16x32_bf16 v[8:11], v[132:135], a[248:251], v[8:11]\n"
"ds_read_b128 v[208:211], v228 offset:53248\n"
"v_cvt_pk_bf16_f32 v142, v172, v173\n"
"v_mfma_f32_16x16x32_bf16 v[40:43], v[136:139], a[244:247], v[40:43]\n"
"v_exp_f32 v162, v162\n"
"v_mfma_f32_16x16x32_bf16 v[40:43], v[140:143], a[248:251], v[40:43]\n"
"ds_read_b128 v[220:223], v229 offset:53760\n"
"v_cvt_pk_bf16_f32 v151, v178, v179\n"
"v_mfma_f32_16x16x32_bf16 v[72:75], v[144:147], a[244:247], v[72:75]\n"
"s_add_u32 s40, s56, s53\n"
"ds_read_b128 v[212:215], v229 offset:53248\n"
"v_mfma_f32_16x16x32_bf16 v[72:75], v[148:151], a[248:251], v[72:75]\n"
"buffer_load_dwordx4 v240, s[20:23], s40 offen offset:2048 nt lds\n"
"v_mfma_f32_16x16x32_bf16 v[76:79], v[148:151], v[224:227], v[76:79]\n"
"v_exp_f32 v163, v163\n"
"v_mfma_f32_16x16x32_bf16 v[76:79], v[144:147], a[252:255], v[76:79]\n"
"v_exp_f32 v156, v156\n"
"v_mfma_f32_16x16x32_bf16 v[44:47], v[140:143], v[224:227], v[44:47]\n"
"v_exp_f32 v160, v160\n"
"v_mfma_f32_16x16x32_bf16 v[44:47], v[136:139], a[252:255], v[44:47]\n"
"v_exp_f32 v155, v155\n"
"v_mfma_f32_16x16x32_bf16 v[12:15], v[132:135], v[224:227], v[12:15]\n"
"ds_read_b64_tr_b16 v[226:227], v239 offset:37376\n"
"v_mfma_f32_16x16x32_bf16 v[12:15], v[128:131], a[252:255], v[12:15]\n"
"ds_read_b64_tr_b16 a[248:249], v238 offset:33280\n"
"v_mfma_f32_16x16x32_bf16 a[220:223], v[124:127], a[252:255], a[220:223]\n"
"v_exp_f32 v154, v154\n"
"v_smfmac_f32_16x16x64_bf16 v[196:199], a[240:243], v[128:135], v241\n"
"ds_read_b64_tr_b16 a[250:251], v238 offset:37376\n"
"v_mfma_f32_16x16x32_bf16 v[202:205], a[240:243], v[124:127], v[202:205]\n"
"s_waitcnt lgkmcnt(3)\n"
"v_exp_f32 v152, v152\n"
"v_mfma_f32_16x16x32_bf16 v[168:171], v[216:219], a[8:11], v[180:183]\n"
"ds_read_b64_tr_b16 a[244:245], v234 offset:33280\n"
"v_cvt_pk_bf16_f32 v96, v152, v153\n"
"v_mfma_f32_16x16x32_bf16 v[168:171], v[220:223], a[12:15], v[168:171]\n"
"v_exp_f32 v165, v165\n"
"v_mfma_f32_16x16x32_bf16 v[168:171], v[208:211], a[0:3], v[168:171]\n"
"ds_read_b64_tr_b16 a[246:247], v234 offset:37376\n"
"v_cvt_pk_bf16_f32 v113, v162, v163\n"
"v_mfma_f32_16x16x32_bf16 v[168:171], v[212:215], a[4:7], v[168:171]\n"
"ds_read_b64_tr_b16 a[254:255], v235 offset:37376\n"
"v_mfma_f32_16x16x32_bf16 v[172:175], v[208:211], a[16:19], v[184:187]\n"
"ds_read_b64_tr_b16 v[224:225], v239 offset:33280\n"
"v_cvt_pk_bf16_f32 v97, v154, v155\n"
"v_mfma_f32_16x16x32_bf16 v[172:175], v[212:215], a[20:23], v[172:175]\n"
"v_mfma_f32_16x16x32_bf16 v[172:175], v[216:219], a[24:27], v[172:175]\n"
"s_add_u32 s40, s56, s54\n"
"ds_read_b64_tr_b16 a[252:253], v235 offset:33280\n"
"v_mfma_f32_16x16x32_bf16 v[172:175], v[220:223], a[28:31], v[172:175]\n"
"buffer_load_dwordx4 v207, s[20:23], s40 offen offset:3072 nt lds\n"
"v_mfma_f32_16x16x32_bf16 v[176:179], v[216:219], a[40:43], v[188:191]\n"
"s_add_u32 s55, s55, s59\n"
"v_exp_f32 v157, v157\n"
"v_mfma_f32_16x16x32_bf16 v[176:179], v[220:223], a[44:47], v[176:179]\n"
"s_or_b32 s55, s55, s58\n"
"v_exp_f32 v171, v171\n"
"v_mfma_f32_16x16x32_bf16 v[176:179], v[208:211], a[32:35], v[176:179]\n"
"s_xor_b32 s56, s55, s57\n"
"v_exp_f32 v159, v159\n"
"v_mfma_f32_16x16x32_bf16 v[176:179], v[212:215], a[36:39], v[176:179]\n"
"v_exp_f32 v167, v167\n"
"v_mfma_f32_16x16x32_bf16 v[152:155], v[208:211], a[192:195], v[192:195]\n"
"v_cvt_pk_bf16_f32 v105, v158, v159\n"
"v_mfma_f32_16x16x32_bf16 v[152:155], v[212:215], a[196:199], v[152:155]\n"
"ds_read_b128 v[208:211], v230 offset:49152\n"
"v_cvt_pk_bf16_f32 v104, v156, v157\n"
"v_mfma_f32_16x16x32_bf16 v[152:155], v[216:219], a[200:203], v[152:155]\n"
"v_exp_f32 v166, v166\n"
"v_mfma_f32_16x16x32_bf16 v[152:155], v[220:223], a[204:207], v[152:155]\n"
"s_waitcnt lgkmcnt(1)\n"
"ds_read_b128 v[216:219], v230 offset:49664\n"
"v_mfma_f32_16x16x32_bf16 a[232:235], v[124:127], a[244:247], a[232:235]\n"
"v_exp_f32 v169, v169\n"
"v_mfma_f32_16x16x32_bf16 v[24:27], v[128:131], a[244:247], v[24:27]\n"
"ds_read_b128 v[220:223], v231 offset:49664\n"
"v_mfma_f32_16x16x32_bf16 v[24:27], v[132:135], a[248:251], v[24:27]\n"
"v_exp_f32 v164, v164\n"
"v_mfma_f32_16x16x32_bf16 v[56:59], v[136:139], a[244:247], v[56:59]\n"
"ds_read_b128 v[212:215], v231 offset:49152\n"
"v_mfma_f32_16x16x32_bf16 v[56:59], v[140:143], a[248:251], v[56:59]\n"
"v_exp_f32 v178, v178\n"
"v_mfma_f32_16x16x32_bf16 v[88:91], v[144:147], a[244:247], v[88:91]\n"
"s_add_u32 m0, s44, 16384\n"
"ds_read_b64_tr_b16 a[246:247], v232 offset:36864\n"
"v_mfma_f32_16x16x32_bf16 v[88:91], v[148:151], a[248:251], v[88:91]\n"
"buffer_load_dwordx4 v240, s[16:19], s56 offen nt lds\n"
"v_mfma_f32_16x16x32_bf16 v[92:95], v[148:151], v[224:227], v[92:95]\n"
"v_exp_f32 v175, v175\n"
"v_mfma_f32_16x16x32_bf16 v[92:95], v[144:147], a[252:255], v[92:95]\n"
"v_exp_f32 v168, v168\n"
"v_mfma_f32_16x16x32_bf16 v[60:63], v[140:143], v[224:227], v[60:63]\n"
"v_exp_f32 v161, v161\n"
"v_mfma_f32_16x16x32_bf16 v[60:63], v[136:139], a[252:255], v[60:63]\n"
"v_exp_f32 v177, v177\n"
"v_mfma_f32_16x16x32_bf16 v[28:31], v[132:135], v[224:227], v[28:31]\n"
"ds_read_b64_tr_b16 v[226:227], v237 offset:36864\n"
"v_mfma_f32_16x16x32_bf16 v[28:31], v[128:131], a[252:255], v[28:31]\n"
"v_exp_f32 v172, v172\n"
"v_mfma_f32_16x16x32_bf16 a[236:239], v[124:127], a[252:255], a[236:239]\n"
"ds_read_b64_tr_b16 a[250:251], v236 offset:36864\n"
"v_cvt_pk_bf16_f32 v120, v164, v165\n"
"v_smfmac_f32_16x16x64_bf16 v[198:201], a[240:243], v[136:143], v241\n"
"s_waitcnt lgkmcnt(2)\n"
"ds_read_b64_tr_b16 a[252:253], v233 offset:32768\n"
"v_mfma_f32_16x16x32_bf16 v[156:159], v[208:211], a[0:3], v[180:183]\n"
"ds_read_b64_tr_b16 a[248:249], v236 offset:32768\n"
"v_cvt_pk_bf16_f32 v121, v166, v167\n"
"v_mfma_f32_16x16x32_bf16 v[156:159], v[212:215], a[4:7], v[156:159]\n"
"ds_read_b64_tr_b16 v[224:225], v237 offset:32768\n"
"v_cvt_pk_bf16_f32 v98, v168, v169\n"
"v_mfma_f32_16x16x32_bf16 v[156:159], v[216:219], a[8:11], v[156:159]\n"
"ds_read_b64_tr_b16 a[254:255], v233 offset:36864\n"
"v_cvt_pk_bf16_f32 v112, v160, v161\n"
"v_mfma_f32_16x16x32_bf16 v[156:159], v[220:223], a[12:15], v[156:159]\n"
"ds_read_b64_tr_b16 a[244:245], v232 offset:32768\n"
"v_mfma_f32_16x16x32_bf16 v[160:163], v[216:219], a[24:27], v[184:187]\n"
"v_exp_f32 v176, v176\n"
"v_mfma_f32_16x16x32_bf16 v[160:163], v[220:223], a[28:31], v[160:163]\n"
"v_exp_f32 v152, v152\n"
"v_mfma_f32_16x16x32_bf16 v[160:163], v[208:211], a[16:19], v[160:163]\n"
"s_add_u32 s40, s56, s52\n"
"v_cvt_pk_bf16_f32 v114, v176, v177\n"
"v_mfma_f32_16x16x32_bf16 v[160:163], v[212:215], a[20:23], v[160:163]\n"
"buffer_load_dwordx4 v207, s[16:19], s40 offen offset:1024 nt lds\n"
"v_mfma_f32_16x16x32_bf16 v[164:167], v[208:211], a[32:35], v[188:191]\n"
"v_exp_f32 v170, v170\n"
"v_mfma_f32_16x16x32_bf16 v[164:167], v[212:215], a[36:39], v[164:167]\n"
"v_exp_f32 v173, v173\n"
"v_mfma_f32_16x16x32_bf16 v[164:167], v[216:219], a[40:43], v[164:167]\n"
"v_exp_f32 v153, v153\n"
"v_mfma_f32_16x16x32_bf16 v[164:167], v[220:223], a[44:47], v[164:167]\n"
"s_waitcnt lgkmcnt(0)\n"
"v_exp_f32 v174, v174\n"
"v_mfma_f32_16x16x32_bf16 a[208:211], v[124:127], a[244:247], a[208:211]\n"
"ds_read_b128 v[208:211], v230 offset:53248\n"
"v_cvt_pk_bf16_f32 v107, v174, v175\n"
"v_mfma_f32_16x16x32_bf16 v[0:3], v[128:131], a[244:247], v[0:3]\n"
"v_exp_f32 v156, v156\n"
"v_mfma_f32_16x16x32_bf16 v[0:3], v[132:135], a[248:251], v[0:3]\n"
"ds_read_b128 v[220:223], v231 offset:53760\n"
"v_cvt_pk_bf16_f32 v106, v172, v173\n"
"v_mfma_f32_16x16x32_bf16 v[32:35], v[136:139], a[244:247], v[32:35]\n"
"v_exp_f32 v161, v161\n"
"v_mfma_f32_16x16x32_bf16 v[32:35], v[140:143], a[248:251], v[32:35]\n"
"ds_read_b128 v[216:219], v230 offset:53760\n"
"v_cvt_pk_bf16_f32 v122, v152, v153\n"
"v_mfma_f32_16x16x32_bf16 v[64:67], v[144:147], a[244:247], v[64:67]\n"
"s_add_u32 s40, s56, s53\n"
"ds_read_b128 v[212:215], v231 offset:53248\n"
"v_mfma_f32_16x16x32_bf16 v[64:67], v[148:151], a[248:251], v[64:67]\n"
"buffer_load_dwordx4 v240, s[16:19], s40 offen offset:2048 nt lds\n"
"v_mfma_f32_16x16x32_bf16 v[68:71], v[148:151], v[224:227], v[68:71]\n"
"v_exp_f32 v159, v159\n"
"v_mfma_f32_16x16x32_bf16 v[68:71], v[144:147], a[252:255], v[68:71]\n"
"v_exp_f32 v155, v155\n"
"v_mfma_f32_16x16x32_bf16 v[36:39], v[140:143], v[224:227], v[36:39]\n"
"v_exp_f32 v160, v160\n"
"v_mfma_f32_16x16x32_bf16 v[36:39], v[136:139], a[252:255], v[36:39]\n"
"v_exp_f32 v179, v179\n"
"v_mfma_f32_16x16x32_bf16 v[4:7], v[132:135], v[224:227], v[4:7]\n"
"ds_read_b64_tr_b16 a[244:245], v232 offset:33280\n"
"ds_read_b64_tr_b16 v[224:225], v237 offset:33280\n"
"v_mfma_f32_16x16x32_bf16 v[4:7], v[128:131], a[252:255], v[4:7]\n"
"v_exp_f32 v158, v158\n"
"v_mfma_f32_16x16x32_bf16 a[212:215], v[124:127], a[252:255], a[212:215]\n"
"ds_read_b64_tr_b16 a[246:247], v232 offset:37376\n"
"ds_read_b64_tr_b16 a[250:251], v236 offset:37376\n"
"v_smfmac_f32_16x16x64_bf16 v[200:203], a[240:243], v[144:151], v241\n"
"s_waitcnt lgkmcnt(4)\n"
"ds_read_b64_tr_b16 v[226:227], v237 offset:37376\n"
"v_cvt_pk_bf16_f32 v99, v170, v171\n"
"v_mfma_f32_16x16x32_bf16 v[168:171], v[208:211], a[0:3], v[180:183]\n"
"v_exp_f32 v164, v164\n"
"v_mfma_f32_16x16x32_bf16 v[168:171], v[212:215], a[4:7], v[168:171]\n"
"ds_read_b64_tr_b16 a[248:249], v236 offset:33280\n"
"ds_read_b64_tr_b16 a[254:255], v233 offset:37376\n"
"v_mfma_f32_16x16x32_bf16 v[168:171], v[216:219], a[8:11], v[168:171]\n"
"s_add_u32 s40, s56, s54\n"
"ds_read_b64_tr_b16 a[252:253], v233 offset:33280\n"
"v_mfma_f32_16x16x32_bf16 v[168:171], v[220:223], a[12:15], v[168:171]\n"
"buffer_load_dwordx4 v207, s[16:19], s40 offen offset:3072 nt lds\n"
"v_mfma_f32_16x16x32_bf16 v[172:175], v[216:219], a[24:27], v[184:187]\n"
"v_exp_f32 v154, v154\n"
"v_mfma_f32_16x16x32_bf16 v[172:175], v[220:223], a[28:31], v[172:175]\n"
"v_exp_f32 v167, v167\n"
"v_mfma_f32_16x16x32_bf16 v[172:175], v[208:211], a[16:19], v[172:175]\n"
"v_exp_f32 v163, v163\n"
"v_mfma_f32_16x16x32_bf16 v[172:175], v[212:215], a[20:23], v[172:175]\n"
"s_cbranch_vccnz LPCL3_%=\nLPKL3_%=:\n"
"v_cvt_pk_bf16_f32 v115, v178, v179\n"
"v_mfma_f32_16x16x32_bf16 v[176:179], v[208:211], a[32:35], v[188:191]\n"
"s_add_u32 s47, s47, 1\n"
"v_exp_f32 v165, v165\n"
"v_mfma_f32_16x16x32_bf16 v[176:179], v[212:215], a[36:39], v[176:179]\n"
"v_exp_f32 v157, v157\n"
"v_mfma_f32_16x16x32_bf16 v[176:179], v[216:219], a[40:43], v[176:179]\n"
"s_and_b32 s40, s47, 3\n"
"v_exp_f32 v166, v166\n"
"v_mfma_f32_16x16x32_bf16 v[176:179], v[220:223], a[44:47], v[176:179]\n"
"s_waitcnt lgkmcnt(0)\n"
"s_cbranch_scc1 LAL_%=\n"
"s_cmp_eq_u32 s47, 8\n"
"s_cbranch_scc1 LTH1_%=\n"
"s_cmp_eq_u32 s47, 12\n"
"s_cbranch_scc1 LTH2_%=\n"
"s_cmp_eq_u32 s47, 16\n"
"s_cbranch_scc1 LAX_%=\n"
"LTH0_%=:\n"
"v_mfma_f32_16x16x32_bf16 a[224:227], v[124:127], a[244:247], a[224:227]\n"
"v_exp_f32 v171, v171\n"
"v_mfma_f32_16x16x32_bf16 v[16:19], v[128:131], a[244:247], v[16:19]\n"
"s_waitcnt vmcnt(8)\n"
"v_exp_f32 v172, v172\n"
"v_mfma_f32_16x16x32_bf16 v[16:19], v[132:135], a[248:251], v[16:19]\n"
"s_barrier\n"
"v_mfma_f32_16x16x32_bf16 v[48:51], v[136:139], a[244:247], v[48:51]\n"
"v_exp_f32 v169, v169\n"
"v_mfma_f32_16x16x32_bf16 v[48:51], v[140:143], a[248:251], v[48:51]\n"
"ds_read_b128 v[220:223], v229 offset:512\n"
"v_cvt_pk_bf16_f32 v117, v166, v167\n"
"v_exp_f32 v176, v176\n"
"v_exp_f32 v177, v177\n"
"v_exp_f32 v178, v178\n"
"v_mfma_f32_16x16x32_bf16 v[80:83], v[144:147], a[244:247], v[80:83]\n"
"ds_read_b64_tr_b16 a[244:245], v234 offset:49152\n"
"v_cvt_pk_bf16_f32 v108, v160, v161\n"
"v_mfma_f32_16x16x32_bf16 v[80:83], v[148:151], a[248:251], v[80:83]\n"
"ds_read_b64_tr_b16 a[246:247], v234 offset:53248\n"
"ds_read_b64_tr_b16 a[248:249], v238 offset:49152\n"
"v_exp_f32 v162, v162\n"
"v_exp_f32 v168, v168\n"
"v_exp_f32 v173, v173\n"
"v_exp_f32 v175, v175\n"
"v_mfma_f32_16x16x32_bf16 v[84:87], v[148:151], v[224:227], v[84:87]\n"
"ds_read_b64_tr_b16 a[250:251], v238 offset:53248\n"
"v_cvt_pk_bf16_f32 v116, v164, v165\n"
"v_mfma_f32_16x16x32_bf16 v[84:87], v[144:147], a[252:255], v[84:87]\n"
"v_exp_f32 v170, v170\n"
"v_mfma_f32_16x16x32_bf16 v[52:55], v[140:143], v[224:227], v[52:55]\n"
"v_cvt_pk_bf16_f32 v103, v170, v171\n"
"v_cvt_pk_bf16_f32 v110, v172, v173\n"
"v_mfma_f32_16x16x32_bf16 v[52:55], v[136:139], a[252:255], v[52:55]\n"
"v_cvt_pk_bf16_f32 v123, v154, v155\n"
"v_cvt_pk_bf16_f32 v101, v158, v159\n"
"v_mfma_f32_16x16x32_bf16 v[20:23], v[132:135], v[224:227], v[20:23]\n"
"v_cvt_pk_bf16_f32 v100, v156, v157\n"
"v_cvt_pk_bf16_f32 v102, v168, v169\n"
"v_mfma_f32_16x16x32_bf16 v[20:23], v[128:131], a[252:255], v[20:23]\n"
"v_exp_f32 v179, v179\n"
"v_mfma_f32_16x16x32_bf16 a[228:231], v[124:127], a[252:255], a[228:231]\n"
"s_waitcnt lgkmcnt(2)\n"
"ds_read_b64_tr_b16 a[252:253], v235 offset:49152\n"
"ds_read_b64_tr_b16 v[226:227], v239 offset:53248\n"
"v_mfma_f32_16x16x32_bf16 a[216:219], v[120:123], a[244:247], a[216:219]\n"
"s_add_u32 m0, s44, 81920\n"
"ds_read_b64_tr_b16 v[224:225], v239 offset:49152\n"
"v_cvt_pk_bf16_f32 v119, v178, v179\n"
"v_smfmac_f32_16x16x64_bf16 v[196:199], a[240:243], v[96:103], v241\n"
"buffer_load_dwordx4 v240, s[20:23], s56 offen nt lds\n"
"v_mfma_f32_16x16x32_bf16 v[8:11], v[96:99], a[244:247], v[8:11]\n"
"s_waitcnt lgkmcnt(3)\n"
"ds_read_b64_tr_b16 a[254:255], v235 offset:53248\n"
"v_cvt_pk_bf16_f32 v118, v176, v177\n"
"v_mfma_f32_16x16x32_bf16 v[8:11], v[100:103], a[248:251], v[8:11]\n"
"v_exp_f32 v174, v174\n"
"v_smfmac_f32_16x16x64_bf16 v[200:203], a[240:243], v[112:119], v241\n"
"v_cvt_pk_bf16_f32 v109, v162, v163\n"
"v_cvt_pk_bf16_f32 v111, v174, v175\n"
"v_mfma_f32_16x16x32_bf16 v[40:43], v[104:107], a[244:247], v[40:43]\n"
"ds_read_b128 v[212:215], v229 offset:0\n"
"v_and_b32 v245, 0x3f0, v242\n"
"v_mfma_f32_16x16x32_bf16 v[40:43], v[108:111], a[248:251], v[40:43]\n"
"v_lshlrev_b32_e64 v244, 24, s43\n"
"v_mul_f32 v244, 0x45800000, v244\n"
"v_smfmac_f32_16x16x64_bf16 v[198:201], a[240:243], v[104:111], v241\n"
"ds_read_b128 v[208:211], v228 offset:0\n"
"v_add_f32 v196, v196, v197\n"
"v_mfma_f32_16x16x32_bf16 v[72:75], v[112:115], a[244:247], v[72:75]\n"
"ds_read_b64_tr_b16 a[244:245], v234 offset:49664\n"
"v_lshrrev_b32 v245, 1, v245\n"
"v_mfma_f32_16x16x32_bf16 v[72:75], v[116:119], a[248:251], v[72:75]\n"
"ds_read_b64_tr_b16 a[246:247], v234 offset:53760\n"
"v_sub_u32 v245, v242, v245\n"
"v_mfma_f32_16x16x32_bf16 v[202:205], a[240:243], v[120:123], v[202:205]\n"
"s_waitcnt lgkmcnt(4)\n"
"ds_read_b64_tr_b16 a[248:249], v238 offset:49664\n"
"v_add_f32 v200, v200, v201\n"
"v_mfma_f32_16x16x32_bf16 v[76:79], v[116:119], v[224:227], v[76:79]\n"
"v_add_f32 v198, v198, v199\n"
"v_add_f32 v254, v196, v198\n"
"v_mfma_f32_16x16x32_bf16 v[76:79], v[112:115], a[252:255], v[76:79]\n"
"ds_read_b64_tr_b16 a[250:251], v238 offset:53760\n"
"v_xor_b32 v250, 0x1000, v245\n"
"v_mfma_f32_16x16x32_bf16 v[44:47], v[108:111], v[224:227], v[44:47]\n"
"ds_read_b128 v[216:219], v228 offset:512\n"
"v_add_f32 v254, v254, v200\n"
"v_mfma_f32_16x16x32_bf16 v[44:47], v[104:107], a[252:255], v[44:47]\n"
"v_add_f32 v244, v244, v202\n"
"v_add_f32 v254, v254, v202\n"
"v_mfma_f32_16x16x32_bf16 v[12:15], v[100:103], v[224:227], v[12:15]\n"
"s_add_u32 s40, s56, s52\n"
"ds_read_b64_tr_b16 v[226:227], v239 offset:53760\n"
"v_min3_f32 v244, v244, v196, v198\n"
"v_mfma_f32_16x16x32_bf16 v[12:15], v[96:99], a[252:255], v[12:15]\n"
"buffer_load_dwordx4 v207, s[20:23], s40 offen offset:1024 nt lds\n"
"v_min_f32 v244, v244, v200\n"
"v_rcp_f32 v244, v244\n"
"v_mfma_f32_16x16x32_bf16 a[220:223], v[120:123], a[252:255], a[220:223]\n"
"s_waitcnt lgkmcnt(4)\n"
"v_fmac_f32 v254, 0x68800000, v244\n"
"v_cmp_nge_f32 vcc, s45, v254\n"
"s_cbranch_vccnz LSLOW_%=\nLBACK0_%=:\ns_mov_b64 vcc, s[64:65]\n"
"v_mfma_f32_16x16x32_bf16 a[232:235], v[120:123], a[244:247], a[232:235]\n"
"v_rcp_f32 v246, v196\n"
"v_accvgpr_mov_b32 a2, a50\n"
"v_accvgpr_mov_b32 a7, a55\n"
"v_accvgpr_mov_b32 a10, a58\n"
"v_accvgpr_mov_b32 a15, a63\n"
"v_accvgpr_mov_b32 a22, a70\n"
"v_accvgpr_mov_b32 a30, a78\n"
"v_bfe_u32 v149, v242, 8, 2\n"
"v_lshl_add_u32 v149, v149, 4, s43\n"
"v_mfma_f32_16x16x32_bf16 v[24:27], v[96:99], a[244:247], v[24:27]\n"
"s_waitcnt lgkmcnt(2)\n"
"ds_read_b128 v[124:127], v149 offset:0\n"
"v_accvgpr_mov_b32 a28, a76\n"
"v_mfma_f32_16x16x32_bf16 v[24:27], v[100:103], a[248:251], v[24:27]\n"
"ds_read_b128 v[132:135], v149 offset:512\n"
"v_mov_b32_dpp v246, v246 row_ror:12 row_mask:0x2 bank_mask:0xf\n"
"v_accvgpr_mov_b32 a3, a51\n"
"v_accvgpr_mov_b32 a12, a60\n"
"v_accvgpr_mov_b32 a18, a66\n"
"v_accvgpr_mov_b32 a19, a67\n"
"v_accvgpr_mov_b32 a26, a74\n"
"v_accvgpr_mov_b32 a36, a84\n"
"v_xor_b32 v148, 0x400, v243\n"
"v_xor_b32 v150, 64, v149\n"
"v_mfma_f32_16x16x32_bf16 v[56:59], v[104:107], a[244:247], v[56:59]\n"
"ds_read_b128 v[128:131], v150 offset:0\n"
"v_accvgpr_mov_b32 a39, a87\n"
"v_mfma_f32_16x16x32_bf16 v[56:59], v[108:111], a[248:251], v[56:59]\n"
"ds_read_b128 v[136:139], v150 offset:512\n"
"v_rcp_f32 v247, v198\n"
"v_mov_b32_dpp v246, v246 row_ror:8 row_mask:0x4 bank_mask:0xf\n"
"v_accvgpr_mov_b32 a0, a48\n"
"v_accvgpr_mov_b32 a1, a49\n"
"v_accvgpr_mov_b32 a4, a52\n"
"v_accvgpr_mov_b32 a5, a53\n"
"v_accvgpr_mov_b32 a8, a56\n"
"v_accvgpr_mov_b32 a16, a64\n"
"v_accvgpr_mov_b32 a25, a73\n"
"v_mfma_f32_16x16x32_bf16 v[88:91], v[112:115], a[244:247], v[88:91]\n"
"v_accvgpr_mov_b32 a20, a68\n"
"v_accvgpr_mov_b32 a29, a77\n"
"v_mfma_f32_16x16x32_bf16 v[88:91], v[116:119], a[248:251], v[88:91]\n"
"ds_read_b128 a[192:195], v243 offset:4096\n"
"ds_read_b128 a[200:203], v243 offset:6144\n"
"v_accvgpr_mov_b32 a6, a54\n"
"v_accvgpr_mov_b32 a9, a57\n"
"v_accvgpr_mov_b32 a11, a59\n"
"v_accvgpr_mov_b32 a13, a61\n"
"v_accvgpr_mov_b32 a14, a62\n"
"v_accvgpr_mov_b32 a17, a65\n"
"v_accvgpr_mov_b32 a23, a71\n"
"v_accvgpr_mov_b32 a31, a79\n"
"v_accvgpr_mov_b32 a33, a81\n"
"v_accvgpr_mov_b32 a35, a83\n"
"v_accvgpr_mov_b32 a44, a92\n"
"v_accvgpr_mov_b32 a47, a95\n"
"s_waitcnt lgkmcnt(5)\n"
"v_mfma_f32_16x16x32_bf16 v[180:183], v[124:127], a[0:3], 0\n"
"v_xor_b32 v244, 0x11000, v242\n"
"s_waitcnt lgkmcnt(3)\n"
"v_mfma_f32_16x16x32_bf16 v[180:183], v[128:131], a[4:7], v[180:183]\n"
"v_accvgpr_mov_b32 a34, a82\n"
"v_accvgpr_mov_b32 a43, a91\n"
"v_mfma_f32_16x16x32_bf16 v[180:183], v[132:135], a[8:11], v[180:183]\n"
"s_waitcnt lgkmcnt(2)\n"
"ds_read_b128 a[204:207], v148 offset:6144\n"
"v_accvgpr_mov_b32 a37, a85\n"
"v_mfma_f32_16x16x32_bf16 v[180:183], v[136:139], a[12:15], v[180:183]\n"
"ds_read_b128 a[196:199], v148 offset:4096\n"
"v_accvgpr_mov_b32 a21, a69\n"
"v_accvgpr_mov_b32 a24, a72\n"
"v_accvgpr_mov_b32 a32, a80\n"
"v_accvgpr_mov_b32 a38, a86\n"
"v_accvgpr_mov_b32 a40, a88\n"
"v_accvgpr_mov_b32 a42, a90\n"
"v_accvgpr_mov_b32 a46, a94\n"
"v_mfma_f32_16x16x32_bf16 v[184:187], v[124:127], a[16:19], 0\n"
"v_mov_b32_dpp v246, v246 row_ror:4 row_mask:0x8 bank_mask:0xf\n"
"v_accvgpr_mov_b32 a27, a75\n"
"v_mfma_f32_16x16x32_bf16 v[184:187], v[128:131], a[20:23], v[184:187]\n"
"v_mov_b32_dpp v247, v247 row_ror:12 row_mask:0x2 bank_mask:0xf\n"
"v_mfma_f32_16x16x32_bf16 v[184:187], v[132:135], a[24:27], v[184:187]\n"
"s_add_u32 s40, s56, s53\n"
"ds_read_b64_tr_b16 v[224:225], v239 offset:49664\n"
"v_mfma_f32_16x16x32_bf16 v[184:187], v[136:139], a[28:31], v[184:187]\n"
"buffer_load_dwordx4 v240, s[20:23], s40 offen offset:2048 nt lds\n"
"v_accvgpr_mov_b32 a41, a89\n"
"v_sub_f32 v180, s74, v180\n"
"v_sub_f32 v181, s74, v181\n"
"v_sub_f32 v183, s74, v183\n"
"v_mul_f32_dpp v26, v246, v26 row_newbcast:2 row_mask:0xf bank_mask:0xf\n"
"v_mfma_f32_16x16x32_bf16 v[188:191], v[124:127], a[32:35], 0\n"
"v_mov_b32_dpp v247, v247 row_ror:8 row_mask:0x4 bank_mask:0xf\n"
"v_accvgpr_mov_b32 a45, a93\n"
"v_mfma_f32_16x16x32_bf16 v[188:191], v[128:131], a[36:39], v[188:191]\n"
"v_mov_b32 v252, v192\n"
"v_mfma_f32_16x16x32_bf16 v[188:191], v[132:135], a[40:43], v[188:191]\n"
"v_mfma_f32_16x16x32_bf16 v[188:191], v[136:139], a[44:47], v[188:191]\n"
"v_mul_f32_dpp v11, v246, v11 row_newbcast:3 row_mask:0xf bank_mask:0xf\n"
"s_waitcnt lgkmcnt(1)\n"
"v_mfma_f32_16x16x32_bf16 v[192:195], v[124:127], a[192:195], 0\n"
"ds_read_b64_tr_b16 a[252:253], v235 offset:49664\n"
"v_sub_f32 v185, s74, v185\n"
"v_mfma_f32_16x16x32_bf16 v[192:195], v[128:131], a[196:199], v[192:195]\n"
"ds_read_b64_tr_b16 a[248:249], v236 offset:49152\n"
"v_sub_f32 v182, s74, v182\n"
"v_mfma_f32_16x16x32_bf16 v[192:195], v[132:135], a[200:203], v[192:195]\n"
"v_sub_f32 v184, s74, v184\n"
"v_mfma_f32_16x16x32_bf16 v[192:195], v[136:139], a[204:207], v[192:195]\n"
"ds_read_b64_tr_b16 a[254:255], v235 offset:53760\n"
"ds_read_b64_tr_b16 a[246:247], v232 offset:53248\n"
"v_mov_b32 v253, v202\n"
"v_sub_f32 v186, s74, v186\n"
"v_mfma_f32_16x16x32_bf16 v[152:155], v[208:211], a[0:3], v[180:183]\n"
"v_mfma_f32_16x16x32_bf16 v[152:155], v[212:215], a[4:7], v[152:155]\n"
"v_sub_f32 v187, s74, v187\n"
"v_mfma_f32_16x16x32_bf16 v[152:155], v[216:219], a[8:11], v[152:155]\n"
"s_add_u32 s40, s56, s54\n"
"v_mfma_f32_16x16x32_bf16 v[152:155], v[220:223], a[12:15], v[152:155]\n"
"buffer_load_dwordx4 v207, s[20:23], s40 offen offset:3072 nt lds\n"
"v_mov_b32_dpp v247, v247 row_ror:4 row_mask:0x8 bank_mask:0xf\n"
"v_sub_f32 v189, s74, v189\n"
"v_sub_f32 v190, s74, v190\n"
"v_sub_f32 v191, s74, v191\n"
"v_sub_f32 v192, s74, v192\n"
"v_mfma_f32_16x16x32_bf16 v[156:159], v[216:219], a[24:27], v[184:187]\n"
"s_add_u32 s55, s55, s59\n"
"v_mfma_f32_16x16x32_bf16 v[156:159], v[220:223], a[28:31], v[156:159]\n"
"s_or_b32 s55, s55, s58\n"
"v_sub_f32 v188, s74, v188\n"
"v_mfma_f32_16x16x32_bf16 v[156:159], v[208:211], a[16:19], v[156:159]\n"
"s_xor_b32 s56, s55, s57\n"
"v_exp_f32 v153, v153\n"
"v_mfma_f32_16x16x32_bf16 v[156:159], v[212:215], a[20:23], v[156:159]\n"
"ds_write_b64 v250, v[252:253] offset:52224\n"
"v_sub_f32 v193, s74, v193\n"
"v_sub_f32 v194, s74, v194\n"
"v_exp_f32 v152, v152\n"
"v_mfma_f32_16x16x32_bf16 v[160:163], v[208:211], a[32:35], v[188:191]\n"
"v_mfma_f32_16x16x32_bf16 v[160:163], v[212:215], a[36:39], v[160:163]\n"
"v_rcp_f32 v206, v200\n"
"v_mfma_f32_16x16x32_bf16 v[160:163], v[216:219], a[40:43], v[160:163]\n"
"v_sub_f32 v195, s74, v195\n"
"v_mfma_f32_16x16x32_bf16 v[160:163], v[220:223], a[44:47], v[160:163]\n"
"ds_write_b128 v244, a[216:219] offset:32768\n"
"v_mul_f32_dpp v25, v246, v25 row_newbcast:1 row_mask:0xf bank_mask:0xf\n"
"v_mfma_f32_16x16x32_bf16 v[164:167], v[216:219], a[200:203], v[192:195]\n"
"v_mov_b32_dpp v206, v206 row_ror:12 row_mask:0x2 bank_mask:0xf\n"
"v_mfma_f32_16x16x32_bf16 v[164:167], v[220:223], a[204:207], v[164:167]\n"
"v_mfma_f32_16x16x32_bf16 v[164:167], v[208:211], a[192:195], v[164:167]\n"
"ds_read_b128 v[208:211], v228 offset:4096\n"
"v_mfma_f32_16x16x32_bf16 v[164:167], v[212:215], a[196:199], v[164:167]\n"
"ds_read_b128 v[216:219], v228 offset:4608\n"
"ds_read_b128 v[220:223], v229 offset:4608\n"
"v_mov_b32_dpp v206, v206 row_ror:8 row_mask:0x4 bank_mask:0xf\n"
"v_exp_f32 v155, v155\n"
"v_exp_f32 v160, v160\n"
"s_branch LTC_%=\n"
"LTH1_%=:\n"
"v_mfma_f32_16x16x32_bf16 a[224:227], v[124:127], a[244:247], a[224:227]\n"
"v_exp_f32 v171, v171\n"
"v_mfma_f32_16x16x32_bf16 v[16:19], v[128:131], a[244:247], v[16:19]\n"
"s_waitcnt vmcnt(8)\n"
"v_exp_f32 v172, v172\n"
"v_mfma_f32_16x16x32_bf16 v[16:19], v[132:135], a[248:251], v[16:19]\n"
"s_barrier\n"
"v_mfma_f32_16x16x32_bf16 v[48:51], v[136:139], a[244:247], v[48:51]\n"
"v_exp_f32 v169, v169\n"
"v_mfma_f32_16x16x32_bf16 v[48:51], v[140:143], a[248:251], v[48:51]\n"
"ds_read_b128 v[220:223], v229 offset:512\n"
"v_cvt_pk_bf16_f32 v117, v166, v167\n"
"v_exp_f32 v176, v176\n"
"v_exp_f32 v177, v177\n"
"v_exp_f32 v178, v178\n"
"v_mfma_f32_16x16x32_bf16 v[80:83], v[144:147], a[244:247], v[80:83]\n"
"ds_read_b64_tr_b16 a[244:245], v234 offset:49152\n"
"v_cvt_pk_bf16_f32 v108, v160, v161\n"
"v_mfma_f32_16x16x32_bf16 v[80:83], v[148:151], a[248:251], v[80:83]\n"
"ds_read_b64_tr_b16 a[246:247], v234 offset:53248\n"
"ds_read_b64_tr_b16 a[248:249], v238 offset:49152\n"
"v_exp_f32 v162, v162\n"
"v_exp_f32 v168, v168\n"
"v_exp_f32 v173, v173\n"
"v_exp_f32 v175, v175\n"
"v_mfma_f32_16x16x32_bf16 v[84:87], v[148:151], v[224:227], v[84:87]\n"
"ds_read_b64_tr_b16 a[250:251], v238 offset:53248\n"
"v_cvt_pk_bf16_f32 v116, v164, v165\n"
"v_mfma_f32_16x16x32_bf16 v[84:87], v[144:147], a[252:255], v[84:87]\n"
"v_exp_f32 v170, v170\n"
"v_mfma_f32_16x16x32_bf16 v[52:55], v[140:143], v[224:227], v[52:55]\n"
"v_cvt_pk_bf16_f32 v103, v170, v171\n"
"v_cvt_pk_bf16_f32 v110, v172, v173\n"
"v_mfma_f32_16x16x32_bf16 v[52:55], v[136:139], a[252:255], v[52:55]\n"
"v_cvt_pk_bf16_f32 v123, v154, v155\n"
"v_cvt_pk_bf16_f32 v101, v158, v159\n"
"v_mfma_f32_16x16x32_bf16 v[20:23], v[132:135], v[224:227], v[20:23]\n"
"v_cvt_pk_bf16_f32 v100, v156, v157\n"
"v_cvt_pk_bf16_f32 v102, v168, v169\n"
"v_mfma_f32_16x16x32_bf16 v[20:23], v[128:131], a[252:255], v[20:23]\n"
"v_exp_f32 v179, v179\n"
"v_mfma_f32_16x16x32_bf16 a[228:231], v[124:127], a[252:255], a[228:231]\n"
"s_waitcnt lgkmcnt(2)\n"
"ds_read_b64_tr_b16 a[252:253], v235 offset:49152\n"
"ds_read_b64_tr_b16 v[226:227], v239 offset:53248\n"
"v_mfma_f32_16x16x32_bf16 a[216:219], v[120:123], a[244:247], a[216:219]\n"
"s_add_u32 m0, s44, 81920\n"
"ds_read_b64_tr_b16 v[224:225], v239 offset:49152\n"
"v_cvt_pk_bf16_f32 v119, v178, v179\n"
"v_smfmac_f32_16x16x64_bf16 v[196:199], a[240:243], v[96:103], v241\n"
"buffer_load_dwordx4 v240, s[20:23], s56 offen nt lds\n"
"v_mfma_f32_16x16x32_bf16 v[8:11], v[96:99], a[244:247], v[8:11]\n"
"s_waitcnt lgkmcnt(3)\n"
"ds_read_b64_tr_b16 a[254:255], v235 offset:53248\n"
"v_cvt_pk_bf16_f32 v118, v176, v177\n"
"v_mfma_f32_16x16x32_bf16 v[8:11], v[100:103], a[248:251], v[8:11]\n"
"v_exp_f32 v174, v174\n"
"v_smfmac_f32_16x16x64_bf16 v[200:203], a[240:243], v[112:119], v241\n"
"v_cvt_pk_bf16_f32 v109, v162, v163\n"
"v_cvt_pk_bf16_f32 v111, v174, v175\n"
"v_mfma_f32_16x16x32_bf16 v[40:43], v[104:107], a[244:247], v[40:43]\n"
"ds_read_b128 v[212:215], v229 offset:0\n"
"v_and_b32 v245, 0x3f0, v242\n"
"v_mfma_f32_16x16x32_bf16 v[40:43], v[108:111], a[248:251], v[40:43]\n"
"v_lshlrev_b32_e64 v244, 24, s43\n"
"v_mul_f32 v244, 0x45800000, v244\n"
"v_smfmac_f32_16x16x64_bf16 v[198:201], a[240:243], v[104:111], v241\n"
"ds_read_b128 v[208:211], v228 offset:0\n"
"v_add_f32 v196, v196, v197\n"
"v_mfma_f32_16x16x32_bf16 v[72:75], v[112:115], a[244:247], v[72:75]\n"
"ds_read_b64_tr_b16 a[244:245], v234 offset:49664\n"
"v_lshrrev_b32 v245, 1, v245\n"
"v_mfma_f32_16x16x32_bf16 v[72:75], v[116:119], a[248:251], v[72:75]\n"
"ds_read_b64_tr_b16 a[246:247], v234 offset:53760\n"
"v_sub_u32 v245, v242, v245\n"
"v_mfma_f32_16x16x32_bf16 v[202:205], a[240:243], v[120:123], v[202:205]\n"
"s_waitcnt lgkmcnt(4)\n"
"ds_read_b64_tr_b16 a[248:249], v238 offset:49664\n"
"v_add_f32 v200, v200, v201\n"
"v_mfma_f32_16x16x32_bf16 v[76:79], v[116:119], v[224:227], v[76:79]\n"
"v_add_f32 v198, v198, v199\n"
"v_add_f32 v254, v196, v198\n"
"v_mfma_f32_16x16x32_bf16 v[76:79], v[112:115], a[252:255], v[76:79]\n"
"ds_read_b64_tr_b16 a[250:251], v238 offset:53760\n"
"v_xor_b32 v250, 0x1000, v245\n"
"v_mfma_f32_16x16x32_bf16 v[44:47], v[108:111], v[224:227], v[44:47]\n"
"ds_read_b128 v[216:219], v228 offset:512\n"
"v_add_f32 v254, v254, v200\n"
"v_mfma_f32_16x16x32_bf16 v[44:47], v[104:107], a[252:255], v[44:47]\n"
"v_add_f32 v244, v244, v202\n"
"v_add_f32 v254, v254, v202\n"
"v_mfma_f32_16x16x32_bf16 v[12:15], v[100:103], v[224:227], v[12:15]\n"
"s_add_u32 s40, s56, s52\n"
"ds_read_b64_tr_b16 v[226:227], v239 offset:53760\n"
"v_min3_f32 v244, v244, v196, v198\n"
"v_mfma_f32_16x16x32_bf16 v[12:15], v[96:99], a[252:255], v[12:15]\n"
"buffer_load_dwordx4 v207, s[20:23], s40 offen offset:1024 nt lds\n"
"v_min_f32 v244, v244, v200\n"
"v_rcp_f32 v244, v244\n"
"v_mfma_f32_16x16x32_bf16 a[220:223], v[120:123], a[252:255], a[220:223]\n"
"s_waitcnt lgkmcnt(4)\n"
"v_fmac_f32 v254, 0x68800000, v244\n"
"v_cmp_nge_f32 vcc, s45, v254\n"
"s_cbranch_vccnz LSLOW_%=\nLBACK1_%=:\ns_mov_b64 vcc, s[64:65]\n"
"v_mfma_f32_16x16x32_bf16 a[232:235], v[120:123], a[244:247], a[232:235]\n"
"v_rcp_f32 v246, v196\n"
"v_accvgpr_mov_b32 a2, a98\n"
"v_accvgpr_mov_b32 a7, a103\n"
"v_accvgpr_mov_b32 a10, a106\n"
"v_accvgpr_mov_b32 a15, a111\n"
"v_accvgpr_mov_b32 a22, a118\n"
"v_accvgpr_mov_b32 a30, a126\n"
"v_bfe_u32 v149, v242, 8, 2\n"
"v_lshl_add_u32 v149, v149, 4, s43\n"
"v_mfma_f32_16x16x32_bf16 v[24:27], v[96:99], a[244:247], v[24:27]\n"
"s_waitcnt lgkmcnt(2)\n"
"ds_read_b128 v[124:127], v149 offset:0\n"
"v_accvgpr_mov_b32 a28, a124\n"
"v_mfma_f32_16x16x32_bf16 v[24:27], v[100:103], a[248:251], v[24:27]\n"
"ds_read_b128 v[132:135], v149 offset:512\n"
"v_mov_b32_dpp v246, v246 row_ror:12 row_mask:0x2 bank_mask:0xf\n"
"v_accvgpr_mov_b32 a3, a99\n"
"v_accvgpr_mov_b32 a12, a108\n"
"v_accvgpr_mov_b32 a18, a114\n"
"v_accvgpr_mov_b32 a19, a115\n"
"v_accvgpr_mov_b32 a26, a122\n"
"v_accvgpr_mov_b32 a36, a132\n"
"v_xor_b32 v148, 0x400, v243\n"
"v_xor_b32 v150, 64, v149\n"
"v_accvgpr_mov_b32 a124, a76\n"
"v_mfma_f32_16x16x32_bf16 v[56:59], v[104:107], a[244:247], v[56:59]\n"
"ds_read_b128 v[128:131], v150 offset:0\n"
"v_accvgpr_mov_b32 a39, a135\n"
"v_mfma_f32_16x16x32_bf16 v[56:59], v[108:111], a[248:251], v[56:59]\n"
"ds_read_b128 v[136:139], v150 offset:512\n"
"v_rcp_f32 v247, v198\n"
"v_mov_b32_dpp v246, v246 row_ror:8 row_mask:0x4 bank_mask:0xf\n"
"v_accvgpr_mov_b32 a0, a96\n"
"v_accvgpr_mov_b32 a1, a97\n"
"v_accvgpr_mov_b32 a4, a100\n"
"v_accvgpr_mov_b32 a5, a101\n"
"v_accvgpr_mov_b32 a8, a104\n"
"v_accvgpr_mov_b32 a16, a112\n"
"v_accvgpr_mov_b32 a25, a121\n"
"v_mfma_f32_16x16x32_bf16 v[88:91], v[112:115], a[244:247], v[88:91]\n"
"v_accvgpr_mov_b32 a20, a116\n"
"v_accvgpr_mov_b32 a29, a125\n"
"v_mfma_f32_16x16x32_bf16 v[88:91], v[116:119], a[248:251], v[88:91]\n"
"s_waitcnt lgkmcnt(3)\n"
"ds_read_b128 a[192:195], v243 offset:4096\n"
"ds_read_b128 a[200:203], v243 offset:6144\n"
"v_accvgpr_mov_b32 a6, a102\n"
"v_accvgpr_mov_b32 a9, a105\n"
"v_accvgpr_mov_b32 a11, a107\n"
"v_accvgpr_mov_b32 a13, a109\n"
"v_accvgpr_mov_b32 a14, a110\n"
"v_accvgpr_mov_b32 a17, a113\n"
"v_accvgpr_mov_b32 a23, a119\n"
"v_accvgpr_mov_b32 a31, a127\n"
"v_accvgpr_mov_b32 a33, a129\n"
"v_accvgpr_mov_b32 a35, a131\n"
"v_accvgpr_mov_b32 a44, a140\n"
"v_accvgpr_mov_b32 a47, a143\n"
"v_accvgpr_mov_b32 a107, a59\n"
"v_mfma_f32_16x16x32_bf16 v[180:183], v[124:127], a[0:3], 0\n"
"s_waitcnt lgkmcnt(3)\n"
"v_xor_b32 v244, 0x11000, v242\n"
"v_accvgpr_mov_b32 a132, a84\n"
"v_mfma_f32_16x16x32_bf16 v[180:183], v[128:131], a[4:7], v[180:183]\n"
"v_accvgpr_mov_b32 a34, a130\n"
"v_accvgpr_mov_b32 a43, a139\n"
"v_mfma_f32_16x16x32_bf16 v[180:183], v[132:135], a[8:11], v[180:183]\n"
"s_waitcnt lgkmcnt(2)\n"
"ds_read_b128 a[204:207], v148 offset:6144\n"
"v_accvgpr_mov_b32 a37, a133\n"
"v_mfma_f32_16x16x32_bf16 v[180:183], v[136:139], a[12:15], v[180:183]\n"
"ds_read_b128 a[196:199], v148 offset:4096\n"
"v_accvgpr_mov_b32 a21, a117\n"
"v_accvgpr_mov_b32 a24, a120\n"
"v_accvgpr_mov_b32 a32, a128\n"
"v_accvgpr_mov_b32 a38, a134\n"
"v_accvgpr_mov_b32 a40, a136\n"
"v_accvgpr_mov_b32 a42, a138\n"
"v_accvgpr_mov_b32 a46, a142\n"
"v_accvgpr_mov_b32 a117, a69\n"
"v_accvgpr_mov_b32 a111, a63\n"
"v_mfma_f32_16x16x32_bf16 v[184:187], v[124:127], a[16:19], 0\n"
"v_mov_b32_dpp v246, v246 row_ror:4 row_mask:0x8 bank_mask:0xf\n"
"v_accvgpr_mov_b32 a27, a123\n"
"v_mfma_f32_16x16x32_bf16 v[184:187], v[128:131], a[20:23], v[184:187]\n"
"v_mov_b32_dpp v247, v247 row_ror:12 row_mask:0x2 bank_mask:0xf\n"
"v_accvgpr_mov_b32 a97, a49\n"
"v_mfma_f32_16x16x32_bf16 v[184:187], v[132:135], a[24:27], v[184:187]\n"
"s_add_u32 s40, s56, s53\n"
"ds_read_b64_tr_b16 v[224:225], v239 offset:49664\n"
"v_accvgpr_mov_b32 a112, a64\n"
"v_mfma_f32_16x16x32_bf16 v[184:187], v[136:139], a[28:31], v[184:187]\n"
"buffer_load_dwordx4 v240, s[20:23], s40 offen offset:2048 nt lds\n"
"v_accvgpr_mov_b32 a41, a137\n"
"v_accvgpr_mov_b32 a126, a78\n"
"v_accvgpr_mov_b32 a114, a66\n"
"v_accvgpr_mov_b32 a137, a89\n"
"v_sub_f32 v180, s74, v180\n"
"v_sub_f32 v181, s74, v181\n"
"v_sub_f32 v183, s74, v183\n"
"v_mul_f32_dpp v26, v246, v26 row_newbcast:2 row_mask:0xf bank_mask:0xf\n"
"v_mfma_f32_16x16x32_bf16 v[188:191], v[124:127], a[32:35], 0\n"
"v_mov_b32_dpp v247, v247 row_ror:8 row_mask:0x4 bank_mask:0xf\n"
"v_accvgpr_mov_b32 a45, a141\n"
"v_mfma_f32_16x16x32_bf16 v[188:191], v[128:131], a[36:39], v[188:191]\n"
"v_mov_b32 v252, v192\n"
"v_accvgpr_mov_b32 a133, a85\n"
"v_mfma_f32_16x16x32_bf16 v[188:191], v[132:135], a[40:43], v[188:191]\n"
"v_accvgpr_mov_b32 a118, a70\n"
"v_accvgpr_mov_b32 a105, a57\n"
"v_mfma_f32_16x16x32_bf16 v[188:191], v[136:139], a[44:47], v[188:191]\n"
"s_waitcnt lgkmcnt(1)\n"
"v_accvgpr_mov_b32 a109, a61\n"
"v_mul_f32_dpp v11, v246, v11 row_newbcast:3 row_mask:0xf bank_mask:0xf\n"
"v_mfma_f32_16x16x32_bf16 v[192:195], v[124:127], a[192:195], 0\n"
"ds_read_b64_tr_b16 a[252:253], v235 offset:49664\n"
"v_sub_f32 v185, s74, v185\n"
"v_mfma_f32_16x16x32_bf16 v[192:195], v[128:131], a[196:199], v[192:195]\n"
"ds_read_b64_tr_b16 a[248:249], v236 offset:49152\n"
"v_sub_f32 v182, s74, v182\n"
"v_mfma_f32_16x16x32_bf16 v[192:195], v[132:135], a[200:203], v[192:195]\n"
"v_accvgpr_mov_b32 a138, a90\n"
"v_sub_f32 v184, s74, v184\n"
"v_mfma_f32_16x16x32_bf16 v[192:195], v[136:139], a[204:207], v[192:195]\n"
"ds_read_b64_tr_b16 a[254:255], v235 offset:53760\n"
"ds_read_b64_tr_b16 a[246:247], v232 offset:53248\n"
"v_mov_b32 v253, v202\n"
"v_accvgpr_mov_b32 a116, a68\n"
"v_accvgpr_mov_b32 a125, a77\n"
"v_accvgpr_mov_b32 a113, a65\n"
"v_accvgpr_mov_b32 a128, a80\n"
"v_accvgpr_mov_b32 a130, a82\n"
"v_accvgpr_mov_b32 a136, a88\n"
"v_sub_f32 v186, s74, v186\n"
"v_mfma_f32_16x16x32_bf16 v[152:155], v[208:211], a[0:3], v[180:183]\n"
"v_accvgpr_mov_b32 a103, a55\n"
"v_accvgpr_mov_b32 a120, a72\n"
"v_mfma_f32_16x16x32_bf16 v[152:155], v[212:215], a[4:7], v[152:155]\n"
"v_accvgpr_mov_b32 a141, a93\n"
"v_sub_f32 v187, s74, v187\n"
"v_mfma_f32_16x16x32_bf16 v[152:155], v[216:219], a[8:11], v[152:155]\n"
"s_add_u32 s40, s56, s54\n"
"v_accvgpr_mov_b32 a127, a79\n"
"v_accvgpr_mov_b32 a140, a92\n"
"v_mfma_f32_16x16x32_bf16 v[152:155], v[220:223], a[12:15], v[152:155]\n"
"buffer_load_dwordx4 v207, s[20:23], s40 offen offset:3072 nt lds\n"
"v_mov_b32_dpp v247, v247 row_ror:4 row_mask:0x8 bank_mask:0xf\n"
"v_accvgpr_mov_b32 a121, a73\n"
"v_accvgpr_mov_b32 a122, a74\n"
"v_accvgpr_mov_b32 a123, a75\n"
"v_sub_f32 v189, s74, v189\n"
"v_sub_f32 v190, s74, v190\n"
"v_sub_f32 v191, s74, v191\n"
"v_sub_f32 v192, s74, v192\n"
"v_mfma_f32_16x16x32_bf16 v[156:159], v[216:219], a[24:27], v[184:187]\n"
"s_add_u32 s55, s55, s59\n"
"v_accvgpr_mov_b32 a143, a95\n"
"v_accvgpr_mov_b32 a115, a67\n"
"v_mfma_f32_16x16x32_bf16 v[156:159], v[220:223], a[28:31], v[156:159]\n"
"s_or_b32 s55, s55, s58\n"
"v_accvgpr_mov_b32 a101, a53\n"
"v_sub_f32 v188, s74, v188\n"
"v_mfma_f32_16x16x32_bf16 v[156:159], v[208:211], a[16:19], v[156:159]\n"
"s_xor_b32 s56, s55, s57\n"
"v_exp_f32 v153, v153\n"
"v_mfma_f32_16x16x32_bf16 v[156:159], v[212:215], a[20:23], v[156:159]\n"
"ds_write_b64 v250, v[252:253] offset:52224\n"
"v_accvgpr_mov_b32 a100, a52\n"
"v_accvgpr_mov_b32 a135, a87\n"
"v_accvgpr_mov_b32 a98, a50\n"
"v_sub_f32 v193, s74, v193\n"
"v_sub_f32 v194, s74, v194\n"
"v_exp_f32 v152, v152\n"
"v_mfma_f32_16x16x32_bf16 v[160:163], v[208:211], a[32:35], v[188:191]\n"
"v_accvgpr_mov_b32 a110, a62\n"
"v_accvgpr_mov_b32 a96, a48\n"
"v_mfma_f32_16x16x32_bf16 v[160:163], v[212:215], a[36:39], v[160:163]\n"
"v_rcp_f32 v206, v200\n"
"v_mfma_f32_16x16x32_bf16 v[160:163], v[216:219], a[40:43], v[160:163]\n"
"v_accvgpr_mov_b32 a142, a94\n"
"v_sub_f32 v195, s74, v195\n"
"v_mfma_f32_16x16x32_bf16 v[160:163], v[220:223], a[44:47], v[160:163]\n"
"ds_write_b128 v244, a[216:219] offset:32768\n"
"v_accvgpr_mov_b32 a102, a54\n"
"v_accvgpr_mov_b32 a108, a60\n"
"v_accvgpr_mov_b32 a99, a51\n"
"v_accvgpr_mov_b32 a129, a81\n"
"v_mul_f32_dpp v25, v246, v25 row_newbcast:1 row_mask:0xf bank_mask:0xf\n"
"v_mfma_f32_16x16x32_bf16 v[164:167], v[216:219], a[200:203], v[192:195]\n"
"v_mov_b32_dpp v206, v206 row_ror:12 row_mask:0x2 bank_mask:0xf\n"
"v_accvgpr_mov_b32 a119, a71\n"
"v_mfma_f32_16x16x32_bf16 v[164:167], v[220:223], a[204:207], v[164:167]\n"
"v_accvgpr_mov_b32 a131, a83\n"
"v_accvgpr_mov_b32 a139, a91\n"
"v_mfma_f32_16x16x32_bf16 v[164:167], v[208:211], a[192:195], v[164:167]\n"
"ds_read_b128 v[208:211], v228 offset:4096\n"
"v_accvgpr_mov_b32 a106, a58\n"
"v_mfma_f32_16x16x32_bf16 v[164:167], v[212:215], a[196:199], v[164:167]\n"
"s_waitcnt lgkmcnt(2)\n"
"ds_read_b128 v[216:219], v228 offset:4608\n"
"ds_read_b128 v[220:223], v229 offset:4608\n"
"v_mov_b32_dpp v206, v206 row_ror:8 row_mask:0x4 bank_mask:0xf\n"
"v_accvgpr_mov_b32 a134, a86\n"
"v_accvgpr_mov_b32 a104, a56\n"
"v_exp_f32 v155, v155\n"
"v_exp_f32 v160, v160\n"
"s_branch LTC_%=\n"
"LTH2_%=:\n"
"v_mfma_f32_16x16x32_bf16 a[224:227], v[124:127], a[244:247], a[224:227]\n"
"v_exp_f32 v171, v171\n"
"v_mfma_f32_16x16x32_bf16 v[16:19], v[128:131], a[244:247], v[16:19]\n"
"s_waitcnt vmcnt(8)\n"
"v_exp_f32 v172, v172\n"
"v_mfma_f32_16x16x32_bf16 v[16:19], v[132:135], a[248:251], v[16:19]\n"
"s_barrier\n"
"v_mfma_f32_16x16x32_bf16 v[48:51], v[136:139], a[244:247], v[48:51]\n"
"v_exp_f32 v169, v169\n"
"v_mfma_f32_16x16x32_bf16 v[48:51], v[140:143], a[248:251], v[48:51]\n"
"ds_read_b128 v[220:223], v229 offset:512\n"
"v_cvt_pk_bf16_f32 v117, v166, v167\n"
"v_exp_f32 v176, v176\n"
"v_exp_f32 v177, v177\n"
"v_exp_f32 v178, v178\n"
"v_mfma_f32_16x16x32_bf16 v[80:83], v[144:147], a[244:247], v[80:83]\n"
"ds_read_b64_tr_b16 a[244:245], v234 offset:49152\n"
"v_cvt_pk_bf16_f32 v108, v160, v161\n"
"v_mfma_f32_16x16x32_bf16 v[80:83], v[148:151], a[248:251], v[80:83]\n"
"ds_read_b64_tr_b16 a[246:247], v234 offset:53248\n"
"ds_read_b64_tr_b16 a[248:249], v238 offset:49152\n"
"v_exp_f32 v162, v162\n"
"v_exp_f32 v168, v168\n"
"v_exp_f32 v173, v173\n"
"v_exp_f32 v175, v175\n"
"v_mfma_f32_16x16x32_bf16 v[84:87], v[148:151], v[224:227], v[84:87]\n"
"ds_read_b64_tr_b16 a[250:251], v238 offset:53248\n"
"v_cvt_pk_bf16_f32 v116, v164, v165\n"
"v_mfma_f32_16x16x32_bf16 v[84:87], v[144:147], a[252:255], v[84:87]\n"
"v_exp_f32 v170, v170\n"
"v_mfma_f32_16x16x32_bf16 v[52:55], v[140:143], v[224:227], v[52:55]\n"
"v_cvt_pk_bf16_f32 v103, v170, v171\n"
"v_cvt_pk_bf16_f32 v110, v172, v173\n"
"v_mfma_f32_16x16x32_bf16 v[52:55], v[136:139], a[252:255], v[52:55]\n"
"v_cvt_pk_bf16_f32 v123, v154, v155\n"
"v_cvt_pk_bf16_f32 v101, v158, v159\n"
"v_mfma_f32_16x16x32_bf16 v[20:23], v[132:135], v[224:227], v[20:23]\n"
"v_cvt_pk_bf16_f32 v100, v156, v157\n"
"v_cvt_pk_bf16_f32 v102, v168, v169\n"
"v_mfma_f32_16x16x32_bf16 v[20:23], v[128:131], a[252:255], v[20:23]\n"
"v_exp_f32 v179, v179\n"
"v_mfma_f32_16x16x32_bf16 a[228:231], v[124:127], a[252:255], a[228:231]\n"
"s_waitcnt lgkmcnt(2)\n"
"ds_read_b64_tr_b16 a[252:253], v235 offset:49152\n"
"ds_read_b64_tr_b16 v[226:227], v239 offset:53248\n"
"v_mfma_f32_16x16x32_bf16 a[216:219], v[120:123], a[244:247], a[216:219]\n"
"s_add_u32 m0, s44, 81920\n"
"ds_read_b64_tr_b16 v[224:225], v239 offset:49152\n"
"v_cvt_pk_bf16_f32 v119, v178, v179\n"
"v_smfmac_f32_16x16x64_bf16 v[196:199], a[240:243], v[96:103], v241\n"
"buffer_load_dwordx4 v240, s[20:23], s56 offen nt lds\n"
"v_mfma_f32_16x16x32_bf16 v[8:11], v[96:99], a[244:247], v[8:11]\n"
"s_waitcnt lgkmcnt(3)\n"
"ds_read_b64_tr_b16 a[254:255], v235 offset:53248\n"
"v_cvt_pk_bf16_f32 v118, v176, v177\n"
"v_mfma_f32_16x16x32_bf16 v[8:11], v[100:103], a[248:251], v[8:11]\n"
"v_exp_f32 v174, v174\n"
"v_smfmac_f32_16x16x64_bf16 v[200:203], a[240:243], v[112:119], v241\n"
"v_cvt_pk_bf16_f32 v109, v162, v163\n"
"v_cvt_pk_bf16_f32 v111, v174, v175\n"
"v_mfma_f32_16x16x32_bf16 v[40:43], v[104:107], a[244:247], v[40:43]\n"
"ds_read_b128 v[212:215], v229 offset:0\n"
"v_and_b32 v245, 0x3f0, v242\n"
"v_mfma_f32_16x16x32_bf16 v[40:43], v[108:111], a[248:251], v[40:43]\n"
"v_lshlrev_b32_e64 v244, 24, s43\n"
"v_mul_f32 v244, 0x45800000, v244\n"
"v_smfmac_f32_16x16x64_bf16 v[198:201], a[240:243], v[104:111], v241\n"
"ds_read_b128 v[208:211], v228 offset:0\n"
"v_add_f32 v196, v196, v197\n"
"v_mfma_f32_16x16x32_bf16 v[72:75], v[112:115], a[244:247], v[72:75]\n"
"ds_read_b64_tr_b16 a[244:245], v234 offset:49664\n"
"v_lshrrev_b32 v245, 1, v245\n"
"v_mfma_f32_16x16x32_bf16 v[72:75], v[116:119], a[248:251], v[72:75]\n"
"ds_read_b64_tr_b16 a[246:247], v234 offset:53760\n"
"v_sub_u32 v245, v242, v245\n"
"v_mfma_f32_16x16x32_bf16 v[202:205], a[240:243], v[120:123], v[202:205]\n"
"s_waitcnt lgkmcnt(4)\n"
"ds_read_b64_tr_b16 a[248:249], v238 offset:49664\n"
"v_add_f32 v200, v200, v201\n"
"v_mfma_f32_16x16x32_bf16 v[76:79], v[116:119], v[224:227], v[76:79]\n"
"v_add_f32 v198, v198, v199\n"
"v_add_f32 v254, v196, v198\n"
"v_mfma_f32_16x16x32_bf16 v[76:79], v[112:115], a[252:255], v[76:79]\n"
"ds_read_b64_tr_b16 a[250:251], v238 offset:53760\n"
"v_xor_b32 v250, 0x1000, v245\n"
"v_mfma_f32_16x16x32_bf16 v[44:47], v[108:111], v[224:227], v[44:47]\n"
"ds_read_b128 v[216:219], v228 offset:512\n"
"v_add_f32 v254, v254, v200\n"
"v_mfma_f32_16x16x32_bf16 v[44:47], v[104:107], a[252:255], v[44:47]\n"
"v_add_f32 v244, v244, v202\n"
"v_add_f32 v254, v254, v202\n"
"v_mfma_f32_16x16x32_bf16 v[12:15], v[100:103], v[224:227], v[12:15]\n"
"s_add_u32 s40, s56, s52\n"
"ds_read_b64_tr_b16 v[226:227], v239 offset:53760\n"
"v_min3_f32 v244, v244, v196, v198\n"
"v_mfma_f32_16x16x32_bf16 v[12:15], v[96:99], a[252:255], v[12:15]\n"
"buffer_load_dwordx4 v207, s[20:23], s40 offen offset:1024 nt lds\n"
"v_min_f32 v244, v244, v200\n"
"v_rcp_f32 v244, v244\n"
"v_mfma_f32_16x16x32_bf16 a[220:223], v[120:123], a[252:255], a[220:223]\n"
"s_waitcnt lgkmcnt(4)\n"
"v_fmac_f32 v254, 0x68800000, v244\n"
"v_cmp_nge_f32 vcc, s45, v254\n"
"s_cbranch_vccnz LSLOW_%=\nLBACK2_%=:\ns_mov_b64 vcc, s[64:65]\n"
"v_mfma_f32_16x16x32_bf16 a[232:235], v[120:123], a[244:247], a[232:235]\n"
"v_rcp_f32 v246, v196\n"
"v_accvgpr_mov_b32 a2, a146\n"
"v_accvgpr_mov_b32 a7, a151\n"
"v_accvgpr_mov_b32 a10, a154\n"
"v_accvgpr_mov_b32 a15, a159\n"
"v_accvgpr_mov_b32 a22, a166\n"
"v_accvgpr_mov_b32 a30, a174\n"
"v_bfe_u32 v149, v242, 8, 2\n"
"v_lshl_add_u32 v149, v149, 4, s43\n"
"v_mfma_f32_16x16x32_bf16 v[24:27], v[96:99], a[244:247], v[24:27]\n"
"s_waitcnt lgkmcnt(2)\n"
"ds_read_b128 v[124:127], v149 offset:0\n"
"v_accvgpr_mov_b32 a28, a172\n"
"v_mfma_f32_16x16x32_bf16 v[24:27], v[100:103], a[248:251], v[24:27]\n"
"ds_read_b128 v[132:135], v149 offset:512\n"
"v_mov_b32_dpp v246, v246 row_ror:12 row_mask:0x2 bank_mask:0xf\n"
"v_accvgpr_mov_b32 a3, a147\n"
"v_accvgpr_mov_b32 a12, a156\n"
"v_accvgpr_mov_b32 a18, a162\n"
"v_accvgpr_mov_b32 a19, a163\n"
"v_accvgpr_mov_b32 a26, a170\n"
"v_accvgpr_mov_b32 a36, a180\n"
"v_xor_b32 v148, 0x400, v243\n"
"v_xor_b32 v150, 64, v149\n"
"v_accvgpr_mov_b32 a172, a76\n"
"v_mfma_f32_16x16x32_bf16 v[56:59], v[104:107], a[244:247], v[56:59]\n"
"ds_read_b128 v[128:131], v150 offset:0\n"
"v_accvgpr_mov_b32 a39, a183\n"
"v_mfma_f32_16x16x32_bf16 v[56:59], v[108:111], a[248:251], v[56:59]\n"
"ds_read_b128 v[136:139], v150 offset:512\n"
"v_rcp_f32 v247, v198\n"
"v_mov_b32_dpp v246, v246 row_ror:8 row_mask:0x4 bank_mask:0xf\n"
"v_accvgpr_mov_b32 a0, a144\n"
"v_accvgpr_mov_b32 a1, a145\n"
"v_accvgpr_mov_b32 a4, a148\n"
"v_accvgpr_mov_b32 a5, a149\n"
"v_accvgpr_mov_b32 a8, a152\n"
"v_accvgpr_mov_b32 a16, a160\n"
"v_accvgpr_mov_b32 a25, a169\n"
"v_mfma_f32_16x16x32_bf16 v[88:91], v[112:115], a[244:247], v[88:91]\n"
"v_accvgpr_mov_b32 a20, a164\n"
"v_accvgpr_mov_b32 a29, a173\n"
"v_mfma_f32_16x16x32_bf16 v[88:91], v[116:119], a[248:251], v[88:91]\n"
"s_waitcnt lgkmcnt(3)\n"
"ds_read_b128 a[192:195], v243 offset:4096\n"
"ds_read_b128 a[200:203], v243 offset:6144\n"
"v_accvgpr_mov_b32 a6, a150\n"
"v_accvgpr_mov_b32 a9, a153\n"
"v_accvgpr_mov_b32 a11, a155\n"
"v_accvgpr_mov_b32 a13, a157\n"
"v_accvgpr_mov_b32 a14, a158\n"
"v_accvgpr_mov_b32 a17, a161\n"
"v_accvgpr_mov_b32 a23, a167\n"
"v_accvgpr_mov_b32 a31, a175\n"
"v_accvgpr_mov_b32 a33, a177\n"
"v_accvgpr_mov_b32 a35, a179\n"
"v_accvgpr_mov_b32 a44, a188\n"
"v_accvgpr_mov_b32 a47, a191\n"
"v_accvgpr_mov_b32 a155, a59\n"
"v_mfma_f32_16x16x32_bf16 v[180:183], v[124:127], a[0:3], 0\n"
"s_waitcnt lgkmcnt(3)\n"
"v_xor_b32 v244, 0x11000, v242\n"
"v_accvgpr_mov_b32 a180, a84\n"
"v_mfma_f32_16x16x32_bf16 v[180:183], v[128:131], a[4:7], v[180:183]\n"
"v_accvgpr_mov_b32 a34, a178\n"
"v_accvgpr_mov_b32 a43, a187\n"
"v_mfma_f32_16x16x32_bf16 v[180:183], v[132:135], a[8:11], v[180:183]\n"
"s_waitcnt lgkmcnt(2)\n"
"ds_read_b128 a[204:207], v148 offset:6144\n"
"v_accvgpr_mov_b32 a37, a181\n"
"v_mfma_f32_16x16x32_bf16 v[180:183], v[136:139], a[12:15], v[180:183]\n"
"ds_read_b128 a[196:199], v148 offset:4096\n"
"v_accvgpr_mov_b32 a21, a165\n"
"v_accvgpr_mov_b32 a24, a168\n"
"v_accvgpr_mov_b32 a32, a176\n"
"v_accvgpr_mov_b32 a38, a182\n"
"v_accvgpr_mov_b32 a40, a184\n"
"v_accvgpr_mov_b32 a42, a186\n"
"v_accvgpr_mov_b32 a46, a190\n"
"v_accvgpr_mov_b32 a165, a69\n"
"v_accvgpr_mov_b32 a159, a63\n"
"v_mfma_f32_16x16x32_bf16 v[184:187], v[124:127], a[16:19], 0\n"
"v_mov_b32_dpp v246, v246 row_ror:4 row_mask:0x8 bank_mask:0xf\n"
"v_accvgpr_mov_b32 a27, a171\n"
"v_mfma_f32_16x16x32_bf16 v[184:187], v[128:131], a[20:23], v[184:187]\n"
"v_mov_b32_dpp v247, v247 row_ror:12 row_mask:0x2 bank_mask:0xf\n"
"v_accvgpr_mov_b32 a145, a49\n"
"v_mfma_f32_16x16x32_bf16 v[184:187], v[132:135], a[24:27], v[184:187]\n"
"s_add_u32 s40, s56, s53\n"
"ds_read_b64_tr_b16 v[224:225], v239 offset:49664\n"
"v_accvgpr_mov_b32 a160, a64\n"
"v_mfma_f32_16x16x32_bf16 v[184:187], v[136:139], a[28:31], v[184:187]\n"
"buffer_load_dwordx4 v240, s[20:23], s40 offen offset:2048 nt lds\n"
"v_accvgpr_mov_b32 a41, a185\n"
"v_accvgpr_mov_b32 a174, a78\n"
"v_accvgpr_mov_b32 a162, a66\n"
"v_accvgpr_mov_b32 a185, a89\n"
"v_sub_f32 v180, s74, v180\n"
"v_sub_f32 v181, s74, v181\n"
"v_sub_f32 v183, s74, v183\n"
"v_mul_f32_dpp v26, v246, v26 row_newbcast:2 row_mask:0xf bank_mask:0xf\n"
"v_mfma_f32_16x16x32_bf16 v[188:191], v[124:127], a[32:35], 0\n"
"v_mov_b32_dpp v247, v247 row_ror:8 row_mask:0x4 bank_mask:0xf\n"
"v_accvgpr_mov_b32 a45, a189\n"
"v_mfma_f32_16x16x32_bf16 v[188:191], v[128:131], a[36:39], v[188:191]\n"
"v_mov_b32 v252, v192\n"
"v_accvgpr_mov_b32 a181, a85\n"
"v_mfma_f32_16x16x32_bf16 v[188:191], v[132:135], a[40:43], v[188:191]\n"
"v_accvgpr_mov_b32 a166, a70\n"
"v_accvgpr_mov_b32 a153, a57\n"
"v_mfma_f32_16x16x32_bf16 v[188:191], v[136:139], a[44:47], v[188:191]\n"
"s_waitcnt lgkmcnt(1)\n"
"v_accvgpr_mov_b32 a157, a61\n"
"v_mul_f32_dpp v11, v246, v11 row_newbcast:3 row_mask:0xf bank_mask:0xf\n"
"v_mfma_f32_16x16x32_bf16 v[192:195], v[124:127], a[192:195], 0\n"
"ds_read_b64_tr_b16 a[252:253], v235 offset:49664\n"
"v_sub_f32 v185, s74, v185\n"
"v_mfma_f32_16x16x32_bf16 v[192:195], v[128:131], a[196:199], v[192:195]\n"
"ds_read_b64_tr_b16 a[248:249], v236 offset:49152\n"
"v_sub_f32 v182, s74, v182\n"
"v_mfma_f32_16x16x32_bf16 v[192:195], v[132:135], a[200:203], v[192:195]\n"
"v_accvgpr_mov_b32 a186, a90\n"
"v_sub_f32 v184, s74, v184\n"
"v_mfma_f32_16x16x32_bf16 v[192:195], v[136:139], a[204:207], v[192:195]\n"
"ds_read_b64_tr_b16 a[254:255], v235 offset:53760\n"
"ds_read_b64_tr_b16 a[246:247], v232 offset:53248\n"
"v_mov_b32 v253, v202\n"
"v_accvgpr_mov_b32 a164, a68\n"
"v_accvgpr_mov_b32 a173, a77\n"
"v_accvgpr_mov_b32 a161, a65\n"
"v_accvgpr_mov_b32 a176, a80\n"
"v_accvgpr_mov_b32 a178, a82\n"
"v_accvgpr_mov_b32 a184, a88\n"
"v_sub_f32 v186, s74, v186\n"
"v_mfma_f32_16x16x32_bf16 v[152:155], v[208:211], a[0:3], v[180:183]\n"
"v_accvgpr_mov_b32 a151, a55\n"
"v_accvgpr_mov_b32 a168, a72\n"
"v_mfma_f32_16x16x32_bf16 v[152:155], v[212:215], a[4:7], v[152:155]\n"
"v_accvgpr_mov_b32 a189, a93\n"
"v_sub_f32 v187, s74, v187\n"
"v_mfma_f32_16x16x32_bf16 v[152:155], v[216:219], a[8:11], v[152:155]\n"
"s_add_u32 s40, s56, s54\n"
"v_accvgpr_mov_b32 a175, a79\n"
"v_accvgpr_mov_b32 a188, a92\n"
"v_mfma_f32_16x16x32_bf16 v[152:155], v[220:223], a[12:15], v[152:155]\n"
"buffer_load_dwordx4 v207, s[20:23], s40 offen offset:3072 nt lds\n"
"v_mov_b32_dpp v247, v247 row_ror:4 row_mask:0x8 bank_mask:0xf\n"
"v_accvgpr_mov_b32 a169, a73\n"
"v_accvgpr_mov_b32 a170, a74\n"
"v_accvgpr_mov_b32 a171, a75\n"
"v_sub_f32 v189, s74, v189\n"
"v_sub_f32 v190, s74, v190\n"
"v_sub_f32 v191, s74, v191\n"
"v_sub_f32 v192, s74, v192\n"
"v_mfma_f32_16x16x32_bf16 v[156:159], v[216:219], a[24:27], v[184:187]\n"
"s_add_u32 s55, s55, s59\n"
"v_accvgpr_mov_b32 a191, a95\n"
"v_accvgpr_mov_b32 a163, a67\n"
"v_mfma_f32_16x16x32_bf16 v[156:159], v[220:223], a[28:31], v[156:159]\n"
"s_or_b32 s55, s55, s58\n"
"v_accvgpr_mov_b32 a149, a53\n"
"v_sub_f32 v188, s74, v188\n"
"v_mfma_f32_16x16x32_bf16 v[156:159], v[208:211], a[16:19], v[156:159]\n"
"s_xor_b32 s56, s55, s57\n"
"v_exp_f32 v153, v153\n"
"v_mfma_f32_16x16x32_bf16 v[156:159], v[212:215], a[20:23], v[156:159]\n"
"ds_write_b64 v250, v[252:253] offset:52224\n"
"v_accvgpr_mov_b32 a148, a52\n"
"v_accvgpr_mov_b32 a183, a87\n"
"v_accvgpr_mov_b32 a146, a50\n"
"v_sub_f32 v193, s74, v193\n"
"v_sub_f32 v194, s74, v194\n"
"v_exp_f32 v152, v152\n"
"v_mfma_f32_16x16x32_bf16 v[160:163], v[208:211], a[32:35], v[188:191]\n"
"v_accvgpr_mov_b32 a158, a62\n"
"v_accvgpr_mov_b32 a144, a48\n"
"v_mfma_f32_16x16x32_bf16 v[160:163], v[212:215], a[36:39], v[160:163]\n"
"v_rcp_f32 v206, v200\n"
"v_mfma_f32_16x16x32_bf16 v[160:163], v[216:219], a[40:43], v[160:163]\n"
"v_accvgpr_mov_b32 a190, a94\n"
"v_sub_f32 v195, s74, v195\n"
"v_mfma_f32_16x16x32_bf16 v[160:163], v[220:223], a[44:47], v[160:163]\n"
"ds_write_b128 v244, a[216:219] offset:32768\n"
"v_accvgpr_mov_b32 a150, a54\n"
"v_accvgpr_mov_b32 a156, a60\n"
"v_accvgpr_mov_b32 a147, a51\n"
"v_accvgpr_mov_b32 a177, a81\n"
"v_mul_f32_dpp v25, v246, v25 row_newbcast:1 row_mask:0xf bank_mask:0xf\n"
"v_mfma_f32_16x16x32_bf16 v[164:167], v[216:219], a[200:203], v[192:195]\n"
"v_mov_b32_dpp v206, v206 row_ror:12 row_mask:0x2 bank_mask:0xf\n"
"v_accvgpr_mov_b32 a167, a71\n"
"v_mfma_f32_16x16x32_bf16 v[164:167], v[220:223], a[204:207], v[164:167]\n"
"v_accvgpr_mov_b32 a179, a83\n"
"v_accvgpr_mov_b32 a187, a91\n"
"v_mfma_f32_16x16x32_bf16 v[164:167], v[208:211], a[192:195], v[164:167]\n"
"ds_read_b128 v[208:211], v228 offset:4096\n"
"v_accvgpr_mov_b32 a154, a58\n"
"v_mfma_f32_16x16x32_bf16 v[164:167], v[212:215], a[196:199], v[164:167]\n"
"s_waitcnt lgkmcnt(2)\n"
"ds_read_b128 v[216:219], v228 offset:4608\n"
"ds_read_b128 v[220:223], v229 offset:4608\n"
"v_mov_b32_dpp v206, v206 row_ror:8 row_mask:0x4 bank_mask:0xf\n"
"v_accvgpr_mov_b32 a182, a86\n"
"v_accvgpr_mov_b32 a152, a56\n"
"v_exp_f32 v155, v155\n"
"v_exp_f32 v160, v160\n"
"LTC_%=:\n"
"s_waitcnt lgkmcnt(4)\n"
"v_mfma_f32_16x16x32_bf16 v[92:95], v[116:119], v[224:227], v[92:95]\n"
"v_exp_f32 v154, v154\n"
"v_mfma_f32_16x16x32_bf16 v[92:95], v[112:115], a[252:255], v[92:95]\n"
"ds_read_b128 v[212:215], v229 offset:4096\n"
"v_mul_f32_dpp v24, v246, v24 row_newbcast:0 row_mask:0xf bank_mask:0xf\n"
"v_mfma_f32_16x16x32_bf16 v[60:63], v[108:111], v[224:227], v[60:63]\n"
"s_add_u32 m0, s44, 32768\n"
"v_exp_f32 v163, v163\n"
"v_mfma_f32_16x16x32_bf16 v[60:63], v[104:107], a[252:255], v[60:63]\n"
"buffer_load_dwordx4 v240, s[16:19], s56 offen nt lds\n"
"v_mul_f32_dpp v8, v246, v8 row_newbcast:0 row_mask:0xf bank_mask:0xf\n"
"v_exp_f32 v156, v156\n"
"v_exp_f32 v158, v158\n"
"v_exp_f32 v161, v161\n"
"v_exp_f32 v165, v165\n"
"v_mfma_f32_16x16x32_bf16 v[28:31], v[100:103], v[224:227], v[28:31]\n"
"v_cvt_pk_bf16_f32 v128, v152, v153\n"
"v_cvt_pk_bf16_f32 v129, v154, v155\n"
"v_mfma_f32_16x16x32_bf16 v[28:31], v[96:99], a[252:255], v[28:31]\n"
"v_mov_b32_dpp v206, v206 row_ror:4 row_mask:0x8 bank_mask:0xf\n"
"v_mul_f32_dpp v9, v246, v9 row_newbcast:1 row_mask:0xf bank_mask:0xf\n"
"v_mfma_f32_16x16x32_bf16 a[236:239], v[120:123], a[252:255], a[236:239]\n"
"s_waitcnt lgkmcnt(2)\n"
"v_exp_f32 v162, v162\n"
"v_mfma_f32_16x16x32_bf16 v[168:171], v[216:219], a[8:11], v[180:183]\n"
"s_waitcnt lgkmcnt(1)\n"
"v_exp_f32 v167, v167\n"
"v_mfma_f32_16x16x32_bf16 v[168:171], v[220:223], a[12:15], v[168:171]\n"
"v_mul_f32_dpp v12, v246, v12 row_newbcast:0 row_mask:0xf bank_mask:0xf\n"
"v_mul_f32_dpp v46, v247, v46 row_newbcast:2 row_mask:0xf bank_mask:0xf\n"
"v_mfma_f32_16x16x32_bf16 v[168:171], v[208:211], a[0:3], v[168:171]\n"
"s_waitcnt lgkmcnt(0)\n"
"ds_read_b64_tr_b16 a[244:245], v232 offset:49152\n"
"v_cvt_pk_bf16_f32 v145, v162, v163\n"
"v_mfma_f32_16x16x32_bf16 v[168:171], v[212:215], a[4:7], v[168:171]\n"
"v_exp_f32 v159, v159\n"
"v_mfma_f32_16x16x32_bf16 v[172:175], v[208:211], a[16:19], v[184:187]\n"
"ds_read_b64_tr_b16 a[252:253], v233 offset:49152\n"
"v_mul_f32_dpp v72, v206, v72 row_newbcast:0 row_mask:0xf bank_mask:0xf\n"
"v_mfma_f32_16x16x32_bf16 v[172:175], v[212:215], a[20:23], v[172:175]\n"
"v_exp_f32 v164, v164\n"
"v_mfma_f32_16x16x32_bf16 v[172:175], v[216:219], a[24:27], v[172:175]\n"
"ds_read_b64_tr_b16 a[250:251], v236 offset:53248\n"
"v_mul_f32_dpp v14, v246, v14 row_newbcast:2 row_mask:0xf bank_mask:0xf\n"
"v_mfma_f32_16x16x32_bf16 v[172:175], v[220:223], a[28:31], v[172:175]\n"
"ds_write_b128 v244, a[220:223] offset:33792\n"
"v_mul_f32_dpp v78, v206, v78 row_newbcast:2 row_mask:0xf bank_mask:0xf\n"
"v_exp_f32 v157, v157\n"
"v_mul_f32_dpp v89, v206, v89 row_newbcast:1 row_mask:0xf bank_mask:0xf\n"
"v_exp_f32 v168, v168\n"
"v_mfma_f32_16x16x32_bf16 v[176:179], v[216:219], a[40:43], v[188:191]\n"
"v_mul_f32_dpp v10, v246, v10 row_newbcast:2 row_mask:0xf bank_mask:0xf\n"
"v_mul_f32_dpp v40, v247, v40 row_newbcast:0 row_mask:0xf bank_mask:0xf\n"
"v_mfma_f32_16x16x32_bf16 v[176:179], v[220:223], a[44:47], v[176:179]\n"
"v_cvt_pk_bf16_f32 v137, v158, v159\n"
"v_mul_f32_dpp v28, v246, v28 row_newbcast:0 row_mask:0xf bank_mask:0xf\n"
"v_mfma_f32_16x16x32_bf16 v[176:179], v[208:211], a[32:35], v[176:179]\n"
"s_add_u32 s40, s56, s52\n"
"v_mul_f32_dpp v73, v206, v73 row_newbcast:1 row_mask:0xf bank_mask:0xf\n"
"v_cvt_pk_bf16_f32 v136, v156, v157\n"
"v_mfma_f32_16x16x32_bf16 v[176:179], v[212:215], a[36:39], v[176:179]\n"
"buffer_load_dwordx4 v207, s[16:19], s40 offen offset:1024 nt lds\n"
"v_mfma_f32_16x16x32_bf16 v[152:155], v[208:211], a[192:195], v[192:195]\n"
"v_mul_f32_dpp v76, v206, v76 row_newbcast:0 row_mask:0xf bank_mask:0xf\n"
"v_mul_f32_dpp v88, v206, v88 row_newbcast:0 row_mask:0xf bank_mask:0xf\n"
"v_mfma_f32_16x16x32_bf16 v[152:155], v[212:215], a[196:199], v[152:155]\n"
"ds_read_b128 v[208:211], v230 offset:0\n"
"v_cvt_pk_bf16_f32 v144, v160, v161\n"
"v_mfma_f32_16x16x32_bf16 v[152:155], v[216:219], a[200:203], v[152:155]\n"
"v_mul_f32_dpp v93, v206, v93 row_newbcast:1 row_mask:0xf bank_mask:0xf\n"
"v_cvt_pk_bf16_f32 v88, v88, v89\n"
"v_mfma_f32_16x16x32_bf16 v[152:155], v[220:223], a[204:207], v[152:155]\n"
"s_waitcnt lgkmcnt(1)\n"
"ds_read_b128 v[212:215], v231 offset:0\n"
"v_mul_f32_dpp v13, v246, v13 row_newbcast:1 row_mask:0xf bank_mask:0xf\n"
"v_mfma_f32_16x16x32_bf16 a[208:211], v[120:123], a[244:247], a[208:211]\n"
"v_exp_f32 v170, v170\n"
"v_mfma_f32_16x16x32_bf16 v[0:3], v[96:99], a[244:247], v[0:3]\n"
"ds_read_b128 v[216:219], v230 offset:512\n"
"v_cvt_pk_bf16_f32 v124, v164, v165\n"
"v_mfma_f32_16x16x32_bf16 v[0:3], v[100:103], a[248:251], v[0:3]\n"
"ds_read_b64_tr_b16 v[226:227], v237 offset:53248\n"
"v_mul_f32_dpp v15, v246, v15 row_newbcast:3 row_mask:0xf bank_mask:0xf\n"
"v_mul_f32_dpp v41, v247, v41 row_newbcast:1 row_mask:0xf bank_mask:0xf\n"
"v_mul_f32_dpp v42, v247, v42 row_newbcast:2 row_mask:0xf bank_mask:0xf\n"
"v_exp_f32 v166, v166\n"
"v_mul_f32_dpp v57, v247, v57 row_newbcast:1 row_mask:0xf bank_mask:0xf\n"
"v_mul_f32_dpp v91, v206, v91 row_newbcast:3 row_mask:0xf bank_mask:0xf\n"
"v_exp_f32 v172, v172\n"
"v_exp_f32 v177, v177\n"
"v_mfma_f32_16x16x32_bf16 v[32:35], v[104:107], a[244:247], v[32:35]\n"
"ds_read_b64_tr_b16 v[224:225], v237 offset:49152\n"
"ds_read_b128 v[220:223], v231 offset:512\n"
"v_mfma_f32_16x16x32_bf16 v[32:35], v[108:111], a[248:251], v[32:35]\n"
"ds_write_b128 v244, a[232:235] offset:34816\n"
"v_cvt_pk_bf16_f32 v8, v8, v9\n"
"v_mul_f32_dpp v75, v206, v75 row_newbcast:3 row_mask:0xf bank_mask:0xf\n"
"v_mul_f32_dpp v30, v246, v30 row_newbcast:2 row_mask:0xf bank_mask:0xf\n"
"v_mul_f32_dpp v94, v206, v94 row_newbcast:2 row_mask:0xf bank_mask:0xf\n"
"v_exp_f32 v176, v176\n"
"v_mfma_f32_16x16x32_bf16 v[64:67], v[112:115], a[244:247], v[64:67]\n"
"v_exp_f32 v173, v173\n"
"v_mfma_f32_16x16x32_bf16 v[64:67], v[116:119], a[248:251], v[64:67]\n"
"s_waitcnt lgkmcnt(6)\n"
"ds_write_b128 v244, a[236:239] offset:35840\n"
"v_mul_f32_dpp v77, v206, v77 row_newbcast:1 row_mask:0xf bank_mask:0xf\n"
"v_mul_f32_dpp v62, v247, v62 row_newbcast:2 row_mask:0xf bank_mask:0xf\n"
"v_exp_f32 v169, v169\n"
"v_cvt_pk_bf16_f32 v138, v172, v173\n"
"v_exp_f32 v153, v153\n"
"v_mfma_f32_16x16x32_bf16 v[156:159], v[208:211], a[0:3], v[180:183]\n"
"s_waitcnt lgkmcnt(6)\n"
"ds_read_b64_tr_b16 a[254:255], v233 offset:53248\n"
"v_mul_f32_dpp v63, v247, v63 row_newbcast:3 row_mask:0xf bank_mask:0xf\n"
"v_mfma_f32_16x16x32_bf16 v[156:159], v[212:215], a[4:7], v[156:159]\n"
"s_waitcnt lgkmcnt(6)\n"
"v_exp_f32 v174, v174\n"
"v_mfma_f32_16x16x32_bf16 v[156:159], v[216:219], a[8:11], v[156:159]\n"
"s_waitcnt lgkmcnt(3)\n"
"s_add_u32 s40, s56, s53\n"
"v_cvt_pk_bf16_f32 v40, v40, v41\n"
"v_mul_f32_dpp v0, v246, v0 row_newbcast:0 row_mask:0xf bank_mask:0xf\n"
"v_mfma_f32_16x16x32_bf16 v[156:159], v[220:223], a[12:15], v[156:159]\n"
"buffer_load_dwordx4 v240, s[16:19], s40 offen offset:2048 nt lds\n"
"v_cvt_pk_bf16_f32 v9, v10, v11\n"
"v_mul_f32_dpp v44, v247, v44 row_newbcast:0 row_mask:0xf bank_mask:0xf\n"
"v_cvt_pk_bf16_f32 v125, v166, v167\n"
"v_mul_f32_dpp v31, v246, v31 row_newbcast:3 row_mask:0xf bank_mask:0xf\n"
"v_exp_f32 v152, v152\n"
"v_mul_f32_dpp v34, v247, v34 row_newbcast:2 row_mask:0xf bank_mask:0xf\n"
"v_mov_b64 v[200:201], 0\n"
"v_mfma_f32_16x16x32_bf16 v[160:163], v[216:219], a[24:27], v[184:187]\n"
"v_mul_f32_dpp v60, v247, v60 row_newbcast:0 row_mask:0xf bank_mask:0xf\n"
"v_xor_b32 v244, 0x1000, v244\n"
"v_mfma_f32_16x16x32_bf16 v[160:163], v[220:223], a[28:31], v[160:163]\n"
"v_exp_f32 v178, v178\n"
"v_mfma_f32_16x16x32_bf16 v[160:163], v[208:211], a[16:19], v[160:163]\n"
"ds_read_b64_tr_b16 a[250:251], v236 offset:53760\n"
"v_mul_f32_dpp v33, v247, v33 row_newbcast:1 row_mask:0xf bank_mask:0xf\n"
"v_mfma_f32_16x16x32_bf16 v[160:163], v[212:215], a[20:23], v[160:163]\n"
"ds_read_b64_tr_b16 a[244:245], v232 offset:49664\n"
"v_mul_f32_dpp v43, v247, v43 row_newbcast:3 row_mask:0xf bank_mask:0xf\n"
"v_mfma_f32_16x16x32_bf16 v[164:167], v[208:211], a[32:35], v[188:191]\n"
"v_mul_f32_dpp v45, v247, v45 row_newbcast:1 row_mask:0xf bank_mask:0xf\n"
"v_cvt_pk_bf16_f32 v72, v72, v73\n"
"v_mfma_f32_16x16x32_bf16 v[164:167], v[212:215], a[36:39], v[164:167]\n"
"ds_read_b128 v[208:211], v230 offset:4096\n"
"v_mul_f32_dpp v74, v206, v74 row_newbcast:2 row_mask:0xf bank_mask:0xf\n"
"v_mfma_f32_16x16x32_bf16 v[164:167], v[216:219], a[40:43], v[164:167]\n"
"v_mul_f32_dpp v56, v247, v56 row_newbcast:0 row_mask:0xf bank_mask:0xf\n"
"v_mul_f32_dpp v58, v247, v58 row_newbcast:2 row_mask:0xf bank_mask:0xf\n"
"v_mfma_f32_16x16x32_bf16 v[164:167], v[220:223], a[44:47], v[164:167]\n"
"ds_read_b128 v[212:215], v231 offset:4096\n"
"ds_read_b128 v[220:223], v231 offset:4608\n"
"v_mul_f32_dpp v47, v247, v47 row_newbcast:3 row_mask:0xf bank_mask:0xf\n"
"v_cvt_pk_bf16_f32 v73, v74, v75\n"
"v_mul_f32_dpp v29, v246, v29 row_newbcast:1 row_mask:0xf bank_mask:0xf\n"
"v_exp_f32 v175, v175\n"
"v_exp_f32 v179, v179\n"
"v_mul_f32_dpp v1, v246, v1 row_newbcast:1 row_mask:0xf bank_mask:0xf\n"
"v_mul_f32_dpp v2, v246, v2 row_newbcast:2 row_mask:0xf bank_mask:0xf\n"
"v_mfma_f32_16x16x32_bf16 v[68:71], v[116:119], v[224:227], v[68:71]\n"
"s_waitcnt lgkmcnt(5)\n"
"v_exp_f32 v171, v171\n"
"v_mfma_f32_16x16x32_bf16 v[68:71], v[112:115], a[252:255], v[68:71]\n"
"ds_read_b128 v[216:219], v230 offset:4608\n"
"v_mul_f32_dpp v27, v246, v27 row_newbcast:3 row_mask:0xf bank_mask:0xf\n"
"v_mfma_f32_16x16x32_bf16 v[36:39], v[108:111], v[224:227], v[36:39]\n"
"s_add_u32 s40, s56, s54\n"
"v_cvt_pk_bf16_f32 v24, v24, v25\n"
"v_mul_f32_dpp v59, v247, v59 row_newbcast:3 row_mask:0xf bank_mask:0xf\n"
"v_mfma_f32_16x16x32_bf16 v[36:39], v[104:107], a[252:255], v[36:39]\n"
"buffer_load_dwordx4 v207, s[16:19], s40 offen offset:3072 nt lds\n"
"v_cvt_pk_bf16_f32 v74, v76, v77\n"
"v_cvt_pk_bf16_f32 v25, v26, v27\n"
"v_cvt_pk_bf16_f32 v130, v168, v169\n"
"v_exp_f32 v154, v154\n"
"v_exp_f32 v162, v162\n"
"v_mul_f32_dpp v3, v246, v3 row_newbcast:3 row_mask:0xf bank_mask:0xf\n"
"v_mfma_f32_16x16x32_bf16 v[4:7], v[100:103], v[224:227], v[4:7]\n"
"v_cvt_pk_bf16_f32 v10, v12, v13\n"
"v_cvt_pk_bf16_f32 v0, v0, v1\n"
"v_mfma_f32_16x16x32_bf16 v[4:7], v[96:99], a[252:255], v[4:7]\n"
"v_mul_f32_dpp v90, v206, v90 row_newbcast:2 row_mask:0xf bank_mask:0xf\n"
"v_mul_f32_dpp v92, v206, v92 row_newbcast:0 row_mask:0xf bank_mask:0xf\n"
"v_mfma_f32_16x16x32_bf16 a[212:215], v[120:123], a[252:255], a[212:215]\n"
"s_waitcnt lgkmcnt(3)\n"
"ds_read_b64_tr_b16 a[246:247], v232 offset:53760\n"
"ds_read_b64_tr_b16 v[224:225], v237 offset:49664\n"
"v_cvt_pk_bf16_f32 v11, v14, v15\n"
"v_cvt_pk_bf16_f32 v41, v42, v43\n"
"v_cvt_pk_bf16_f32 v43, v46, v47\n"
"v_cvt_pk_bf16_f32 v56, v56, v57\n"
"v_cvt_pk_bf16_f32 v131, v170, v171\n"
"v_cvt_pk_bf16_f32 v146, v176, v177\n"
"v_exp_f32 v167, v167\n"
"v_mfma_f32_16x16x32_bf16 v[168:171], v[208:211], a[0:3], v[180:183]\n"
"s_waitcnt lgkmcnt(3)\n"
"v_exp_f32 v159, v159\n"
"v_mfma_f32_16x16x32_bf16 v[168:171], v[212:215], a[4:7], v[168:171]\n"
"s_waitcnt lgkmcnt(2)\n"
"ds_read_b64_tr_b16 a[248:249], v236 offset:49664\n"
"ds_read_b64_tr_b16 a[254:255], v233 offset:53760\n"
"v_mfma_f32_16x16x32_bf16 v[168:171], v[216:219], a[8:11], v[168:171]\n"
"v_cvt_pk_bf16_f32 v89, v90, v91\n"
"v_cvt_pk_bf16_f32 v139, v174, v175\n"
"v_mfma_f32_16x16x32_bf16 v[168:171], v[220:223], a[12:15], v[168:171]\n"
"ds_write_b128 v248, v[8:11] offset:49152\n"
"v_mul_f32_dpp v79, v206, v79 row_newbcast:3 row_mask:0xf bank_mask:0xf\n"
"v_cvt_pk_bf16_f32 v147, v178, v179\n"
"v_exp_f32 v165, v165\n"
"v_mul_f32_dpp v6, v246, v6 row_newbcast:2 row_mask:0xf bank_mask:0xf\n"
"v_cvt_pk_bf16_f32 v1, v2, v3\n"
"v_mfma_f32_16x16x32_bf16 v[172:175], v[216:219], a[24:27], v[184:187]\n"
"ds_read_b64_tr_b16 v[226:227], v237 offset:53760\n"
"v_mul_f32_dpp v32, v247, v32 row_newbcast:0 row_mask:0xf bank_mask:0xf\n"
"v_mfma_f32_16x16x32_bf16 v[172:175], v[220:223], a[28:31], v[172:175]\n"
"v_mul_f32_dpp v39, v247, v39 row_newbcast:3 row_mask:0xf bank_mask:0xf\n"
"v_mul_f32_dpp v68, v206, v68 row_newbcast:0 row_mask:0xf bank_mask:0xf\n"
"v_mfma_f32_16x16x32_bf16 v[172:175], v[208:211], a[16:19], v[172:175]\n"
"ds_read_b64_tr_b16 a[252:253], v233 offset:49664\n"
"v_cvt_pk_bf16_f32 v75, v78, v79\n"
"v_mfma_f32_16x16x32_bf16 v[172:175], v[212:215], a[20:23], v[172:175]\n"
"s_cbranch_vccnz LPCT0_%=\nLPKT0_%=:\n"
"ds_read_b64_tr_b16 a[54:55], v249 offset:49160\n"
"v_mfma_f32_16x16x32_bf16 v[176:179], v[208:211], a[32:35], v[188:191]\n"
"v_cvt_pk_bf16_f32 v42, v44, v45\n"
"v_mul_f32_dpp v61, v247, v61 row_newbcast:1 row_mask:0xf bank_mask:0xf\n"
"v_mfma_f32_16x16x32_bf16 v[176:179], v[212:215], a[36:39], v[176:179]\n"
"ds_read_b64_tr_b16 a[52:53], v249 offset:49152\n"
"v_cvt_pk_bf16_f32 v57, v58, v59\n"
"v_mfma_f32_16x16x32_bf16 v[176:179], v[216:219], a[40:43], v[176:179]\n"
"v_exp_f32 v155, v155\n"
"v_mfma_f32_16x16x32_bf16 v[176:179], v[220:223], a[44:47], v[176:179]\n"
"s_waitcnt lgkmcnt(4)\n"
"ds_write_b128 v248, v[40:43] offset:50176\n"
"v_cvt_pk_bf16_f32 v27, v30, v31\n"
"v_cvt_pk_bf16_f32 v90, v92, v93\n"
"v_cvt_pk_bf16_f32 v126, v152, v153\n"
"v_mul_f32_dpp v7, v246, v7 row_newbcast:3 row_mask:0xf bank_mask:0xf\n"
"v_mul_f32_dpp v65, v206, v65 row_newbcast:1 row_mask:0xf bank_mask:0xf\n"
"v_mul_f32_dpp v67, v206, v67 row_newbcast:3 row_mask:0xf bank_mask:0xf\n"
"v_mul_f32_dpp v70, v206, v70 row_newbcast:2 row_mask:0xf bank_mask:0xf\n"
"v_mul_f32_dpp v71, v206, v71 row_newbcast:3 row_mask:0xf bank_mask:0xf\n"
"v_mfma_f32_16x16x32_bf16 a[224:227], v[120:123], a[244:247], a[224:227]\n"
"ds_write_b128 v248, v[72:75] offset:51200\n"
"v_cvt_pk_bf16_f32 v26, v28, v29\n"
"v_exp_f32 v166, v166\n"
"v_cvt_pk_bf16_f32 v3, v6, v7\n"
"v_mul_f32_dpp v66, v206, v66 row_newbcast:2 row_mask:0xf bank_mask:0xf\n"
"v_mfma_f32_16x16x32_bf16 v[16:19], v[96:99], a[244:247], v[16:19]\n"
"s_waitcnt vmcnt(8) lgkmcnt(4)\n"
"ds_read_b64_tr_b16 a[68:69], v249 offset:50176\n"
"v_mul_f32_dpp v69, v206, v69 row_newbcast:1 row_mask:0xf bank_mask:0xf\n"
"v_mfma_f32_16x16x32_bf16 v[16:19], v[100:103], a[248:251], v[16:19]\n"
"s_barrier\n"
"ds_read_b64_tr_b16 a[70:71], v249 offset:50184\n"
"ds_read_b128 v[212:215], v229 offset:16384\n"
"ds_read_b64 v[250:251], v245 offset:52224\n"
"v_mul_f32_dpp v5, v246, v5 row_newbcast:1 row_mask:0xf bank_mask:0xf\n"
"v_mul_f32_dpp v35, v247, v35 row_newbcast:3 row_mask:0xf bank_mask:0xf\n"
"v_exp_f32 v173, v173\n"
"v_exp_f32 v175, v175\n"
"v_mfma_f32_16x16x32_bf16 v[48:51], v[104:107], a[244:247], v[48:51]\n"
"v_cvt_pk_bf16_f32 v59, v62, v63\n"
"v_mul_f32_dpp v38, v247, v38 row_newbcast:2 row_mask:0xf bank_mask:0xf\n"
"v_mfma_f32_16x16x32_bf16 v[48:51], v[108:111], a[248:251], v[48:51]\n"
"ds_read_b128 v[208:211], v228 offset:16384\n"
"ds_read_b128 v[220:223], v229 offset:16896\n"
"v_mul_f32_dpp v95, v206, v95 row_newbcast:3 row_mask:0xf bank_mask:0xf\n"
"v_exp_f32 v161, v161\n"
"v_cvt_pk_bf16_f32 v149, v166, v167\n"
"v_cvt_pk_bf16_f32 v32, v32, v33\n"
"v_exp_f32 v170, v170\n"
"v_exp_f32 v172, v172\n"
"v_mfma_f32_16x16x32_bf16 v[80:83], v[112:115], a[244:247], v[80:83]\n"
"ds_read_b64_tr_b16 a[84:85], v249 offset:51200\n"
"ds_read_b64_tr_b16 a[86:87], v249 offset:51208\n"
"v_mfma_f32_16x16x32_bf16 v[80:83], v[116:119], a[248:251], v[80:83]\n"
"ds_read_b128 v[28:31], v244 offset:33792\n"
"v_mov_b64 v[198:199], 0\n"
"v_mfma_f32_16x16x32_bf16 v[84:87], v[116:119], v[224:227], v[84:87]\n"
"ds_write_b128 v248, v[24:27] offset:49152\n"
"v_cvt_pk_bf16_f32 v91, v94, v95\n"
"v_exp_f32 v156, v156\n"
"v_exp_f32 v157, v157\n"
"v_mul_f32_dpp v37, v247, v37 row_newbcast:1 row_mask:0xf bank_mask:0xf\n"
"v_mfma_f32_16x16x32_bf16 v[84:87], v[112:115], a[252:255], v[84:87]\n"
"ds_read_b64_tr_b16 a[60:61], v249 offset:49152\n"
"ds_read_b128 v[216:219], v228 offset:16896\n"
"v_cvt_pk_bf16_f32 v127, v154, v155\n"
"v_exp_f32 v160, v160\n"
"v_exp_f32 v164, v164\n"
"v_mul_f32_dpp v36, v247, v36 row_newbcast:0 row_mask:0xf bank_mask:0xf\n"
"v_exp_f32 v176, v176\n"
"v_mfma_f32_16x16x32_bf16 v[52:55], v[108:111], v[224:227], v[52:55]\n"
"ds_write_b128 v248, v[88:91] offset:51200\n"
"v_cvt_pk_bf16_f32 v58, v60, v61\n"
"v_exp_f32 v163, v163\n"
"v_cvt_pk_bf16_f32 v33, v34, v35\n"
"v_exp_f32 v169, v169\n"
"v_mfma_f32_16x16x32_bf16 v[52:55], v[104:107], a[252:255], v[52:55]\n"
"s_waitcnt lgkmcnt(9)\n"
"ds_write_b128 v248, v[56:59] offset:50176\n"
"v_mul_f32_dpp v4, v246, v4 row_newbcast:0 row_mask:0xf bank_mask:0xf\n"
"v_exp_f32 v168, v168\n"
"v_min_f32 v254, v252, v250\n"
"v_sub_f32 v252, v254, v252\n"
"v_mfma_f32_16x16x32_bf16 v[20:23], v[100:103], v[224:227], v[20:23]\n"
"ds_read_b64_tr_b16 a[92:93], v249 offset:51200\n"
"ds_read_b64_tr_b16 a[244:245], v234 offset:0\n"
"v_mfma_f32_16x16x32_bf16 v[20:23], v[96:99], a[252:255], v[20:23]\n"
"ds_read_b64_tr_b16 a[62:63], v249 offset:49160\n"
"ds_read_b64_tr_b16 a[78:79], v249 offset:50184\n"
"ds_read_b64_tr_b16 a[94:95], v249 offset:51208\n"
"v_cvt_pk_bf16_f32 v2, v4, v5\n"
"v_mul_f32_dpp v64, v206, v64 row_newbcast:0 row_mask:0xf bank_mask:0xf\n"
"v_cvt_pk_bf16_f32 v64, v64, v65\n"
"v_cvt_pk_bf16_f32 v65, v66, v67\n"
"v_cvt_pk_bf16_f32 v66, v68, v69\n"
"v_cvt_pk_bf16_f32 v67, v70, v71\n"
"v_exp_f32 v177, v177\n"
"v_mfma_f32_16x16x32_bf16 a[228:231], v[120:123], a[252:255], a[228:231]\n"
"s_waitcnt lgkmcnt(11)\n"
"ds_write_b128 v248, v[64:67] offset:51200\n"
"v_cvt_pk_bf16_f32 v132, v156, v157\n"
"v_cvt_pk_bf16_f32 v34, v36, v37\n"
"v_cvt_pk_bf16_f32 v35, v38, v39\n"
"v_exp_f32 v171, v171\n"
"v_exp_f32 v179, v179\n"
"v_sub_f32 v250, v254, v250\n"
"v_mul_f32_dpp v83, v206, v83 row_newbcast:3 row_mask:0xf bank_mask:0xf\n"
"v_mfma_f32_16x16x32_bf16 v[152:155], v[208:211], a[0:3], v[180:183]\n"
"ds_read_b64_tr_b16 a[76:77], v249 offset:50176\n"
"ds_read_b64_tr_b16 a[80:81], v249 offset:51200\n"
"v_mfma_f32_16x16x32_bf16 v[152:155], v[212:215], a[4:7], v[152:155]\n"
"s_waitcnt lgkmcnt(10)\n"
"ds_write_b128 v248, v[0:3] offset:49152\n"
"v_exp_f32 v158, v158\n"
"v_cvt_pk_bf16_f32 v140, v160, v161\n"
"v_cvt_pk_bf16_f32 v142, v172, v173\n"
"v_mul_f32_dpp v18, v246, v18 row_newbcast:2 row_mask:0xf bank_mask:0xf\n"
"v_mfma_f32_16x16x32_bf16 v[152:155], v[216:219], a[8:11], v[152:155]\n"
"ds_write_b128 v248, v[32:35] offset:50176\n"
"v_cvt_pk_bf16_f32 v141, v162, v163\n"
"v_exp_f32 v178, v178\n"
"v_exp_f32 v250, v250\n"
"v_mfma_f32_16x16x32_bf16 v[152:155], v[220:223], a[12:15], v[152:155]\n"
"ds_read_b64_tr_b16 a[50:51], v249 offset:49160\n"
"ds_read_b64_tr_b16 a[64:65], v249 offset:50176\n"
"ds_read_b128 v[24:27], v244 offset:32768\n"
"v_cvt_pk_bf16_f32 v133, v158, v159\n"
"v_cvt_pk_bf16_f32 v148, v164, v165\n"
"v_cvt_pk_bf16_f32 v150, v176, v177\n"
"v_exp_f32 v252, v252\n"
"v_mul_f32_dpp v19, v246, v19 row_newbcast:3 row_mask:0xf bank_mask:0xf\n"
"v_mul_f32_dpp v22, v246, v22 row_newbcast:2 row_mask:0xf bank_mask:0xf\n"
"v_mfma_f32_16x16x32_bf16 v[156:159], v[216:219], a[24:27], v[184:187]\n"
"ds_read_b64_tr_b16 a[66:67], v249 offset:50184\n"
"ds_read_b64_tr_b16 a[248:249], v238 offset:0\n"
"v_mfma_f32_16x16x32_bf16 v[156:159], v[220:223], a[28:31], v[156:159]\n"
"ds_read_b64_tr_b16 a[48:49], v249 offset:49152\n"
"ds_read_b128 v[60:63], v244 offset:35840\n"
"v_mfma_f32_16x16x32_bf16 v[156:159], v[208:211], a[16:19], v[156:159]\n"
"ds_read_b64_tr_b16 a[82:83], v249 offset:51208\n"
"ds_read_b128 v[56:59], v244 offset:34816\n"
"v_mfma_f32_16x16x32_bf16 v[156:159], v[212:215], a[20:23], v[156:159]\n"
"ds_read_b64_tr_b16 a[246:247], v234 offset:4096\n"
"ds_read_b64_tr_b16 v[226:227], v239 offset:4096\n"
"v_mfma_f32_16x16x32_bf16 v[160:163], v[208:211], a[32:35], v[188:191]\n"
"v_exp_f32 v174, v174\n"
"v_mfma_f32_16x16x32_bf16 v[160:163], v[212:215], a[36:39], v[160:163]\n"
"ds_read_b64_tr_b16 a[250:251], v238 offset:4096\n"
"v_mul_f32_dpp v49, v247, v49 row_newbcast:1 row_mask:0xf bank_mask:0xf\n"
"v_mfma_f32_16x16x32_bf16 v[160:163], v[216:219], a[40:43], v[160:163]\n"
"ds_read_b64_tr_b16 v[224:225], v239 offset:0\n"
"v_mov_b64 v[202:203], 0\n"
"v_mfma_f32_16x16x32_bf16 v[160:163], v[220:223], a[44:47], v[160:163]\n"
"v_exp_f32 v154, v154\n"
"v_mfma_f32_16x16x32_bf16 v[164:167], v[216:219], a[200:203], v[192:195]\n"
"v_cvt_pk_bf16_f32 v151, v178, v179\n"
"v_mul_f32 v253, v253, v252\n"
"v_mfma_f32_16x16x32_bf16 v[164:167], v[220:223], a[204:207], v[164:167]\n"
"v_cvt_pk_bf16_f32 v134, v168, v169\n"
"v_fmac_f32 v253, v251, v250\n"
"v_mfma_f32_16x16x32_bf16 v[164:167], v[208:211], a[192:195], v[164:167]\n"
"ds_read_b64_tr_b16 a[252:253], v235 offset:0\n"
"ds_read_b64_tr_b16 a[254:255], v235 offset:4096\n"
"v_mfma_f32_16x16x32_bf16 v[164:167], v[212:215], a[196:199], v[164:167]\n"
"s_waitcnt lgkmcnt(4)\n"
"v_cvt_pk_bf16_f32 v135, v170, v171\n"
"v_mfma_f32_16x16x32_bf16 a[216:219], v[124:127], a[244:247], 0\n"
"ds_read_b128 v[216:219], v228 offset:20992\n"
"v_cvt_pk_bf16_f32 v143, v174, v175\n"
"v_mfma_f32_16x16x32_bf16 v[8:11], v[128:131], a[244:247], 0\n"
"s_waitcnt lgkmcnt(4)\n"
"s_add_u32 m0, s44, 98304\n"
"v_rcp_f32 v253, v253\n"
"v_mfma_f32_16x16x32_bf16 v[8:11], v[132:135], a[248:251], v[8:11]\n"
"buffer_load_dwordx4 v240, s[20:23], s56 offen nt lds\n"
"v_exp_f32 v152, v152\n"
"v_exp_f32 v155, v155\n"
"v_exp_f32 v158, v158\n"
"v_exp_f32 v167, v167\n"
"v_mfma_f32_16x16x32_bf16 v[40:43], v[136:139], a[244:247], 0\n"
"v_accvgpr_read_b32 v254, a208\n"
"v_mul_f32_dpp v85, v206, v85 row_newbcast:1 row_mask:0xf bank_mask:0xf\n"
"v_mfma_f32_16x16x32_bf16 v[40:43], v[140:143], a[248:251], v[40:43]\n"
"v_mul_f32 v250, v250, v253\n"
"v_accvgpr_read_b32 v251, a211\n"
"v_mfma_f32_16x16x32_bf16 v[72:75], v[144:147], a[244:247], 0\n"
"ds_read_b128 v[220:223], v229 offset:20992\n"
"v_mul_f32_dpp v17, v246, v17 row_newbcast:1 row_mask:0xf bank_mask:0xf\n"
"v_mfma_f32_16x16x32_bf16 v[72:75], v[148:151], a[248:251], v[72:75]\n"
"s_waitcnt lgkmcnt(2)\n"
"v_exp_f32 v164, v164\n"
"v_mfma_f32_16x16x32_bf16 v[76:79], v[148:151], v[224:227], 0\n"
"v_exp_f32 v153, v153\n"
"v_mfma_f32_16x16x32_bf16 v[76:79], v[144:147], a[252:255], v[76:79]\n"
"v_mul_f32 v252, v252, v253\n"
"v_mov_b32_dpp v250, v250 row_ror:12 row_mask:0x2 bank_mask:0xf\n"
"v_mfma_f32_16x16x32_bf16 v[44:47], v[140:143], v[224:227], 0\n"
"ds_read_b64_tr_b16 a[244:245], v234 offset:512\n"
"v_mov_b32_dpp v252, v252 row_ror:12 row_mask:0x2 bank_mask:0xf\n"
"v_mfma_f32_16x16x32_bf16 v[44:47], v[136:139], a[252:255], v[44:47]\n"
"ds_read_b128 v[212:215], v229 offset:20480\n"
"v_mov_b32_dpp v250, v250 row_ror:8 row_mask:0x4 bank_mask:0xf\n"
"v_mfma_f32_16x16x32_bf16 v[12:15], v[132:135], v[224:227], 0\n"
"s_add_u32 s40, s56, s52\n"
"ds_read_b128 v[208:211], v228 offset:20480\n"
"v_cvt_pk_bf16_f32 v96, v152, v153\n"
"v_mfma_f32_16x16x32_bf16 v[12:15], v[128:131], a[252:255], v[12:15]\n"
"buffer_load_dwordx4 v207, s[20:23], s40 offen offset:1024 nt lds\n"
"v_mov_b32_dpp v252, v252 row_ror:8 row_mask:0x4 bank_mask:0xf\n"
"v_mov_b32_dpp v250, v250 row_ror:4 row_mask:0x8 bank_mask:0xf\n"
"v_accvgpr_read_b32 v255, a209\n"
"v_accvgpr_read_b32 v253, a210\n"
"v_mov_b64 v[196:197], 0\n"
"v_cvt_pk_bf16_f32 v97, v154, v155\n"
"v_exp_f32 v157, v157\n"
"v_mfma_f32_16x16x32_bf16 a[220:223], v[124:127], a[252:255], 0\n"
"v_mov_b32_dpp v252, v252 row_ror:4 row_mask:0x8 bank_mask:0xf\n"
"v_mul_f32_dpp v24, v250, v24 row_newbcast:0 row_mask:0xf bank_mask:0xf\n"
"v_mul_f32_dpp v25, v250, v25 row_newbcast:1 row_mask:0xf bank_mask:0xf\n"
"v_mul_f32_dpp v26, v250, v26 row_newbcast:2 row_mask:0xf bank_mask:0xf\n"
"v_mul_f32_dpp v29, v250, v29 row_newbcast:1 row_mask:0xf bank_mask:0xf\n"
"v_mul_f32_dpp v54, v247, v54 row_newbcast:2 row_mask:0xf bank_mask:0xf\n"
"v_exp_f32 v162, v162\n"
"v_exp_f32 v163, v163\n"
"v_smfmac_f32_16x16x64_bf16 v[196:199], a[240:243], v[128:135], v241\n"
"v_mul_f32_dpp v27, v250, v27 row_newbcast:3 row_mask:0xf bank_mask:0xf\n"
"v_fmac_f32_dpp v24, v252, v254 row_newbcast:0 row_mask:0xf bank_mask:0xf\n"
"v_mfma_f32_16x16x32_bf16 v[202:205], a[240:243], v[124:127], v[202:205]\n"
"s_waitcnt lgkmcnt(3)\n"
"ds_read_b64_tr_b16 a[252:253], v235 offset:512\n"
"ds_read_b64_tr_b16 a[254:255], v235 offset:4608\n"
"ds_read_b64_tr_b16 v[224:225], v239 offset:512\n"
"v_fmac_f32_dpp v25, v252, v255 row_newbcast:1 row_mask:0xf bank_mask:0xf\n"
"v_fmac_f32_dpp v26, v252, v253 row_newbcast:2 row_mask:0xf bank_mask:0xf\n"
"v_mul_f32_dpp v28, v250, v28 row_newbcast:0 row_mask:0xf bank_mask:0xf\n"
"v_mul_f32_dpp v30, v250, v30 row_newbcast:2 row_mask:0xf bank_mask:0xf\n"
"v_mul_f32_dpp v50, v247, v50 row_newbcast:2 row_mask:0xf bank_mask:0xf\n"
"v_mul_f32_dpp v58, v250, v58 row_newbcast:2 row_mask:0xf bank_mask:0xf\n"
"v_exp_f32 v156, v156\n"
"v_mfma_f32_16x16x32_bf16 v[168:171], v[216:219], a[8:11], v[180:183]\n"
"v_accvgpr_read_b32 v254, a212\n"
"v_accvgpr_read_b32 v253, a214\n"
"v_mfma_f32_16x16x32_bf16 v[168:171], v[220:223], a[12:15], v[168:171]\n"
"s_waitcnt lgkmcnt(3)\n"
"v_mul_f32_dpp v48, v247, v48 row_newbcast:0 row_mask:0xf bank_mask:0xf\n"
"v_mul_f32_dpp v57, v250, v57 row_newbcast:1 row_mask:0xf bank_mask:0xf\n"
"v_mfma_f32_16x16x32_bf16 v[168:171], v[208:211], a[0:3], v[168:171]\n"
"ds_read_b64_tr_b16 a[250:251], v238 offset:4608\n"
"v_mul_f32_dpp v56, v250, v56 row_newbcast:0 row_mask:0xf bank_mask:0xf\n"
"v_mfma_f32_16x16x32_bf16 v[168:171], v[212:215], a[4:7], v[168:171]\n"
"ds_read_b64_tr_b16 a[248:249], v238 offset:512\n"
"ds_read_b64_tr_b16 v[226:227], v239 offset:4608\n"
"v_fmac_f32_dpp v27, v252, v251 row_newbcast:3 row_mask:0xf bank_mask:0xf\n"
"v_mul_f32_dpp v31, v250, v31 row_newbcast:3 row_mask:0xf bank_mask:0xf\n"
"v_fmac_f32_dpp v28, v252, v254 row_newbcast:0 row_mask:0xf bank_mask:0xf\n"
"v_fmac_f32_dpp v30, v252, v253 row_newbcast:2 row_mask:0xf bank_mask:0xf\n"
"v_cvt_pk_bf16_f32 v24, v24, v25\n"
"v_cvt_pk_bf16_f32 v25, v26, v27\n"
"v_mul_f32_dpp v52, v247, v52 row_newbcast:0 row_mask:0xf bank_mask:0xf\n"
"v_accvgpr_read_b32 v254, a224\n"
"v_mul_f32_dpp v59, v250, v59 row_newbcast:3 row_mask:0xf bank_mask:0xf\n"
"v_mul_f32_dpp v60, v250, v60 row_newbcast:0 row_mask:0xf bank_mask:0xf\n"
"v_mul_f32_dpp v62, v250, v62 row_newbcast:2 row_mask:0xf bank_mask:0xf\n"
"v_mul_f32_dpp v63, v250, v63 row_newbcast:3 row_mask:0xf bank_mask:0xf\n"
"v_exp_f32 v159, v159\n"
"v_exp_f32 v161, v161\n"
"v_mfma_f32_16x16x32_bf16 v[172:175], v[208:211], a[16:19], v[184:187]\n"
"v_accvgpr_read_b32 v255, a213\n"
"v_accvgpr_read_b32 v251, a215\n"
"v_mfma_f32_16x16x32_bf16 v[172:175], v[212:215], a[20:23], v[172:175]\n"
"ds_read_b64_tr_b16 a[246:247], v234 offset:4608\n"
"v_fmac_f32_dpp v29, v252, v255 row_newbcast:1 row_mask:0xf bank_mask:0xf\n"
"v_mfma_f32_16x16x32_bf16 v[172:175], v[216:219], a[24:27], v[172:175]\n"
"s_add_u32 s40, s56, s53\n"
"v_xor_b32 v249, s75, v249\n"
"v_accvgpr_read_b32 v255, a225\n"
"v_mfma_f32_16x16x32_bf16 v[172:175], v[220:223], a[28:31], v[172:175]\n"
"buffer_load_dwordx4 v240, s[20:23], s40 offen offset:2048 nt lds\n"
"v_fmac_f32_dpp v31, v252, v251 row_newbcast:3 row_mask:0xf bank_mask:0xf\n"
"v_cvt_pk_bf16_f32 v27, v30, v31\n"
"v_mul_f32_dpp v16, v246, v16 row_newbcast:0 row_mask:0xf bank_mask:0xf\n"
"v_mul_f32_dpp v51, v247, v51 row_newbcast:3 row_mask:0xf bank_mask:0xf\n"
"v_accvgpr_read_b32 v253, a226\n"
"v_accvgpr_read_b32 v251, a227\n"
"v_fmac_f32_dpp v56, v252, v254 row_newbcast:0 row_mask:0xf bank_mask:0xf\n"
"v_accvgpr_read_b32 v254, a228\n"
"v_mfma_f32_16x16x32_bf16 v[176:179], v[216:219], a[40:43], v[188:191]\n"
"v_cvt_pk_bf16_f32 v26, v28, v29\n"
"v_fmac_f32_dpp v57, v252, v255 row_newbcast:1 row_mask:0xf bank_mask:0xf\n"
"v_mfma_f32_16x16x32_bf16 v[176:179], v[220:223], a[44:47], v[176:179]\n"
"v_fmac_f32_dpp v58, v252, v253 row_newbcast:2 row_mask:0xf bank_mask:0xf\n"
"v_accvgpr_read_b32 v255, a229\n"
"v_mfma_f32_16x16x32_bf16 v[176:179], v[208:211], a[32:35], v[176:179]\n"
"v_cvt_pk_bf16_f32 v16, v16, v17\n"
"v_mul_f32_dpp v61, v250, v61 row_newbcast:1 row_mask:0xf bank_mask:0xf\n"
"v_mfma_f32_16x16x32_bf16 v[176:179], v[212:215], a[36:39], v[176:179]\n"
"ds_write_b128 v243, v[24:27] offset:0\n"
"v_cvt_pk_bf16_f32 v48, v48, v49\n"
"v_fmac_f32_dpp v59, v252, v251 row_newbcast:3 row_mask:0xf bank_mask:0xf\n"
"v_accvgpr_read_b32 v253, a230\n"
"v_accvgpr_read_b32 v251, a231\n"
"v_fmac_f32_dpp v60, v252, v254 row_newbcast:0 row_mask:0xf bank_mask:0xf\n"
"v_fmac_f32_dpp v61, v252, v255 row_newbcast:1 row_mask:0xf bank_mask:0xf\n"
"v_cvt_pk_bf16_f32 v56, v56, v57\n"
"v_cvt_pk_bf16_f32 v57, v58, v59\n"
"v_cvt_pk_bf16_f32 v58, v60, v61\n"
"v_mfma_f32_16x16x32_bf16 v[152:155], v[208:211], a[192:195], v[192:195]\n"
"v_mul_f32_dpp v81, v206, v81 row_newbcast:1 row_mask:0xf bank_mask:0xf\n"
"v_mul_f32_dpp v82, v206, v82 row_newbcast:2 row_mask:0xf bank_mask:0xf\n"
"v_mul_f32_dpp v87, v206, v87 row_newbcast:3 row_mask:0xf bank_mask:0xf\n"
"v_fmac_f32_dpp v62, v252, v253 row_newbcast:2 row_mask:0xf bank_mask:0xf\n"
"v_fmac_f32_dpp v63, v252, v251 row_newbcast:3 row_mask:0xf bank_mask:0xf\n"
"v_cvt_pk_bf16_f32 v59, v62, v63\n"
"ds_write_b128 v243, v[56:59] offset:2048\n"
"v_mfma_f32_16x16x32_bf16 v[152:155], v[212:215], a[196:199], v[152:155]\n"
"v_exp_f32 v174, v174\n"
"v_mfma_f32_16x16x32_bf16 v[152:155], v[216:219], a[200:203], v[152:155]\n"
"v_exp_f32 v166, v166\n"
"v_mfma_f32_16x16x32_bf16 v[152:155], v[220:223], a[204:207], v[152:155]\n"
"s_waitcnt lgkmcnt(2)\n"
"ds_read_b128 v[212:215], v231 offset:16384\n"
"v_cvt_pk_bf16_f32 v105, v158, v159\n"
"v_mfma_f32_16x16x32_bf16 a[232:235], v[124:127], a[244:247], 0\n"
"v_mul_f32_dpp v20, v246, v20 row_newbcast:0 row_mask:0xf bank_mask:0xf\n"
"v_mul_f32_dpp v23, v246, v23 row_newbcast:3 row_mask:0xf bank_mask:0xf\n"
"v_mfma_f32_16x16x32_bf16 v[24:27], v[128:131], a[244:247], 0\n"
"s_add_u32 s40, s56, s54\n"
"ds_read_b128 v[220:223], v231 offset:16896\n"
"v_mul_f32_dpp v21, v246, v21 row_newbcast:1 row_mask:0xf bank_mask:0xf\n"
"v_mfma_f32_16x16x32_bf16 v[24:27], v[132:135], a[248:251], v[24:27]\n"
"buffer_load_dwordx4 v207, s[20:23], s40 offen offset:3072 nt lds\n"
"v_mfma_f32_16x16x32_bf16 v[56:59], v[136:139], a[244:247], 0\n"
"s_add_u32 s55, s55, s59\n"
"v_cvt_pk_bf16_f32 v17, v18, v19\n"
"v_cvt_pk_bf16_f32 v104, v156, v157\n"
"v_mfma_f32_16x16x32_bf16 v[56:59], v[140:143], a[248:251], v[56:59]\n"
"s_or_b32 s55, s55, s58\n"
"v_exp_f32 v178, v178\n"
"v_mfma_f32_16x16x32_bf16 v[88:91], v[144:147], a[244:247], 0\n"
"s_xor_b32 s56, s55, s57\n"
"ds_read_b128 v[208:211], v230 offset:16384\n"
"v_cvt_pk_bf16_f32 v49, v50, v51\n"
"v_mfma_f32_16x16x32_bf16 v[88:91], v[148:151], a[248:251], v[88:91]\n"
"ds_read_b128 v[216:219], v230 offset:16896\n"
"v_cvt_pk_bf16_f32 v18, v20, v21\n"
"v_mfma_f32_16x16x32_bf16 v[92:95], v[148:151], v[224:227], 0\n"
"v_exp_f32 v160, v160\n"
"v_mfma_f32_16x16x32_bf16 v[92:95], v[144:147], a[252:255], v[92:95]\n"
"v_exp_f32 v165, v165\n"
"v_mfma_f32_16x16x32_bf16 v[60:63], v[140:143], v[224:227], 0\n"
"s_add_u32 m0, s44, 49152\n"
"ds_read_b64_tr_b16 a[248:249], v236 offset:0\n"
"v_mul_f32_dpp v80, v206, v80 row_newbcast:0 row_mask:0xf bank_mask:0xf\n"
"v_mfma_f32_16x16x32_bf16 v[60:63], v[136:139], a[252:255], v[60:63]\n"
"buffer_load_dwordx4 v240, s[16:19], s56 offen nt lds\n"
"v_mfma_f32_16x16x32_bf16 v[28:31], v[132:135], v[224:227], 0\n"
"v_exp_f32 v177, v177\n"
"v_mfma_f32_16x16x32_bf16 v[28:31], v[128:131], a[252:255], v[28:31]\n"
"v_exp_f32 v169, v169\n"
"v_mfma_f32_16x16x32_bf16 a[236:239], v[124:127], a[252:255], 0\n"
"ds_read_b64_tr_b16 a[250:251], v236 offset:4096\n"
"ds_read_b64_tr_b16 v[224:225], v237 offset:0\n"
"v_smfmac_f32_16x16x64_bf16 v[198:201], a[240:243], v[136:143], v241\n"
"s_waitcnt lgkmcnt(3)\n"
"ds_read_b64_tr_b16 a[246:247], v232 offset:4096\n"
"v_cvt_pk_bf16_f32 v112, v160, v161\n"
"v_mfma_f32_16x16x32_bf16 v[156:159], v[208:211], a[0:3], v[180:183]\n"
"v_cvt_pk_bf16_f32 v80, v80, v81\n"
"v_cvt_pk_bf16_f32 v113, v162, v163\n"
"v_mfma_f32_16x16x32_bf16 v[156:159], v[212:215], a[4:7], v[156:159]\n"
"ds_read_b64_tr_b16 a[254:255], v233 offset:4096\n"
"v_xor_b32 v248, s75, v248\n"
"v_mfma_f32_16x16x32_bf16 v[156:159], v[216:219], a[8:11], v[156:159]\n"
"ds_read_b64_tr_b16 a[244:245], v232 offset:0\n"
"v_cvt_pk_bf16_f32 v120, v164, v165\n"
"v_mfma_f32_16x16x32_bf16 v[156:159], v[220:223], a[12:15], v[156:159]\n"
"v_exp_f32 v170, v170\n"
"v_mfma_f32_16x16x32_bf16 v[160:163], v[216:219], a[24:27], v[184:187]\n"
"v_exp_f32 v179, v179\n"
"v_mfma_f32_16x16x32_bf16 v[160:163], v[220:223], a[28:31], v[160:163]\n"
"ds_read_b64_tr_b16 v[226:227], v237 offset:4096\n"
"v_cvt_pk_bf16_f32 v121, v166, v167\n"
"v_mfma_f32_16x16x32_bf16 v[160:163], v[208:211], a[16:19], v[160:163]\n"
"s_add_u32 s40, s56, s52\n"
"v_exp_f32 v168, v168\n"
"v_mfma_f32_16x16x32_bf16 v[160:163], v[212:215], a[20:23], v[160:163]\n"
"buffer_load_dwordx4 v207, s[16:19], s40 offen offset:1024 nt lds\n"
"v_mfma_f32_16x16x32_bf16 v[164:167], v[208:211], a[32:35], v[188:191]\n"
"v_exp_f32 v176, v176\n"
"v_mfma_f32_16x16x32_bf16 v[164:167], v[212:215], a[36:39], v[164:167]\n"
"v_cvt_pk_bf16_f32 v19, v22, v23\n"
"v_mul_f32_dpp v53, v247, v53 row_newbcast:1 row_mask:0xf bank_mask:0xf\n"
"v_mfma_f32_16x16x32_bf16 v[164:167], v[216:219], a[40:43], v[164:167]\n"
"ds_read_b64_tr_b16 a[252:253], v233 offset:0\n"
"ds_read_b128 v[208:211], v230 offset:20480\n"
"v_mfma_f32_16x16x32_bf16 v[164:167], v[220:223], a[44:47], v[164:167]\n"
"s_waitcnt lgkmcnt(3)\n"
"v_exp_f32 v158, v158\n"
"v_mfma_f32_16x16x32_bf16 a[208:211], v[124:127], a[244:247], 0\n"
"ds_read_b128 v[220:223], v231 offset:20992\n"
"v_cvt_pk_bf16_f32 v81, v82, v83\n"
"v_mfma_f32_16x16x32_bf16 v[0:3], v[128:131], a[244:247], 0\n"
"ds_read_b128 v[212:215], v231 offset:20480\n"
"v_mul_f32_dpp v55, v247, v55 row_newbcast:3 row_mask:0xf bank_mask:0xf\n"
"v_mfma_f32_16x16x32_bf16 v[0:3], v[132:135], a[248:251], v[0:3]\n"
"v_exp_f32 v175, v175\n"
"v_mfma_f32_16x16x32_bf16 v[32:35], v[136:139], a[244:247], 0\n"
"s_add_u32 s40, s56, s53\n"
"ds_read_b128 v[216:219], v230 offset:20992\n"
"v_mul_f32_dpp v84, v206, v84 row_newbcast:0 row_mask:0xf bank_mask:0xf\n"
"v_mfma_f32_16x16x32_bf16 v[32:35], v[140:143], a[248:251], v[32:35]\n"
"buffer_load_dwordx4 v240, s[16:19], s40 offen offset:2048 nt lds\n"
"v_mfma_f32_16x16x32_bf16 v[64:67], v[144:147], a[244:247], 0\n"
"v_cvt_pk_bf16_f32 v50, v52, v53\n"
"v_mul_f32_dpp v86, v206, v86 row_newbcast:2 row_mask:0xf bank_mask:0xf\n"
"v_mfma_f32_16x16x32_bf16 v[64:67], v[148:151], a[248:251], v[64:67]\n"
"s_waitcnt lgkmcnt(3)\n"
"ds_write_b128 v248, v[16:19] offset:49152\n"
"v_cvt_pk_bf16_f32 v82, v84, v85\n"
"v_exp_f32 v172, v172\n"
"v_exp_f32 v152, v152\n"
"v_exp_f32 v159, v159\n"
"v_exp_f32 v161, v161\n"
"v_exp_f32 v164, v164\n"
"v_exp_f32 v167, v167\n"
"v_mfma_f32_16x16x32_bf16 v[68:71], v[148:151], v[224:227], 0\n"
"v_cvt_pk_bf16_f32 v51, v54, v55\n"
"v_cvt_pk_bf16_f32 v98, v168, v169\n"
"v_mfma_f32_16x16x32_bf16 v[68:71], v[144:147], a[252:255], v[68:71]\n"
"ds_write_b128 v248, v[48:51] offset:50176\n"
"v_exp_f32 v171, v171\n"
"v_exp_f32 v173, v173\n"
"v_exp_f32 v156, v156\n"
"v_exp_f32 v163, v163\n"
"v_mfma_f32_16x16x32_bf16 v[36:39], v[140:143], v[224:227], 0\n"
"ds_read_b64_tr_b16 a[246:247], v232 offset:4608\n"
"ds_read_b64_tr_b16 a[250:251], v236 offset:4608\n"
"v_mfma_f32_16x16x32_bf16 v[36:39], v[136:139], a[252:255], v[36:39]\n"
"ds_read_b64_tr_b16 a[74:75], v249 offset:50184\n"
"v_cvt_pk_bf16_f32 v107, v174, v175\n"
"v_mfma_f32_16x16x32_bf16 v[4:7], v[132:135], v[224:227], 0\n"
"ds_read_b64_tr_b16 a[56:57], v249 offset:49152\n"
"v_cvt_pk_bf16_f32 v83, v86, v87\n"
"v_mfma_f32_16x16x32_bf16 v[4:7], v[128:131], a[252:255], v[4:7]\n"
"ds_write_b128 v248, v[80:83] offset:51200\n"
"v_cvt_pk_bf16_f32 v99, v170, v171\n"
"v_exp_f32 v155, v155\n"
"v_exp_f32 v157, v157\n"
"v_mfma_f32_16x16x32_bf16 a[212:215], v[124:127], a[252:255], 0\n"
"s_add_u32 s40, s56, s54\n"
"ds_read_b64_tr_b16 a[58:59], v249 offset:49160\n"
"ds_read_b64_tr_b16 v[224:225], v237 offset:512\n"
"v_smfmac_f32_16x16x64_bf16 v[200:203], a[240:243], v[144:151], v241\n"
"buffer_load_dwordx4 v207, s[16:19], s40 offen offset:3072 nt lds\n"
"v_mfma_f32_16x16x32_bf16 v[168:171], v[208:211], a[0:3], v[180:183]\n"
"s_waitcnt lgkmcnt(8)\n"
"v_exp_f32 v153, v153\n"
"v_mfma_f32_16x16x32_bf16 v[168:171], v[212:215], a[4:7], v[168:171]\n"
"ds_read_b64_tr_b16 a[72:73], v249 offset:50176\n"
"ds_read_b64_tr_b16 a[90:91], v249 offset:51208\n"
"v_mfma_f32_16x16x32_bf16 v[168:171], v[216:219], a[8:11], v[168:171]\n"
"ds_read_b64_tr_b16 a[88:89], v249 offset:51200\n"
"v_cvt_pk_bf16_f32 v115, v178, v179\n"
"v_mfma_f32_16x16x32_bf16 v[168:171], v[220:223], a[12:15], v[168:171]\n"
"ds_read_b64_tr_b16 a[244:245], v232 offset:512\n"
"v_cvt_pk_bf16_f32 v106, v172, v173\n"
"v_mfma_f32_16x16x32_bf16 v[172:175], v[216:219], a[24:27], v[184:187]\n"
"ds_read_b64_tr_b16 a[248:249], v236 offset:512\n"
"v_cvt_pk_bf16_f32 v114, v176, v177\n"
"v_mfma_f32_16x16x32_bf16 v[172:175], v[220:223], a[28:31], v[172:175]\n"
"v_exp_f32 v162, v162\n"
"v_mfma_f32_16x16x32_bf16 v[172:175], v[208:211], a[16:19], v[172:175]\n"
"ds_read_b64_tr_b16 v[226:227], v237 offset:4608\n"
"v_cvt_pk_bf16_f32 v122, v152, v153\n"
"v_mfma_f32_16x16x32_bf16 v[172:175], v[212:215], a[20:23], v[172:175]\n"
"s_cbranch_vccnz LPCT1_%=\nLPKT1_%=:\n"
"ds_read_b64_tr_b16 a[252:253], v233 offset:512\n"
"v_mfma_f32_16x16x32_bf16 v[176:179], v[208:211], a[32:35], v[188:191]\n"
"v_exp_f32 v166, v166\n"
"v_mfma_f32_16x16x32_bf16 v[176:179], v[212:215], a[36:39], v[176:179]\n"
"ds_read_b64_tr_b16 a[254:255], v233 offset:4608\n"
"v_cvt_pk_bf16_f32 v109, v162, v163\n"
"v_mfma_f32_16x16x32_bf16 v[176:179], v[216:219], a[40:43], v[176:179]\n"
"v_exp_f32 v169, v169\n"
"v_mfma_f32_16x16x32_bf16 v[176:179], v[220:223], a[44:47], v[176:179]\n"
"s_waitcnt lgkmcnt(4)\n"
"v_cvt_pk_bf16_f32 v101, v158, v159\n"
"v_mfma_f32_16x16x32_bf16 a[224:227], v[124:127], a[244:247], 0\n"
"v_exp_f32 v154, v154\n"
"v_mfma_f32_16x16x32_bf16 v[16:19], v[128:131], a[244:247], 0\n"
"s_waitcnt vmcnt(8) lgkmcnt(3)\n"
"v_exp_f32 v168, v168\n"
"v_mfma_f32_16x16x32_bf16 v[16:19], v[132:135], a[248:251], v[16:19]\n"
"s_barrier\n"
"ds_read_b128 v[212:215], v229 offset:32768\n"
"ds_read_b128 v[216:219], v228 offset:33280\n"
"v_exp_f32 v165, v165\n"
"v_mfma_f32_16x16x32_bf16 v[48:51], v[136:139], a[244:247], 0\n"
"ds_read_b128 v[220:223], v229 offset:33280\n"
"v_xor_b32 v249, s75, v249\n"
"v_mfma_f32_16x16x32_bf16 v[48:51], v[140:143], a[248:251], v[48:51]\n"
"ds_read_b128 v[208:211], v228 offset:32768\n"
"v_add_u32 v243, 0x1000, v243\n"
"v_mfma_f32_16x16x32_bf16 v[80:83], v[144:147], a[244:247], 0\n"
"ds_read_b64_tr_b16 a[246:247], v234 offset:20480\n"
"v_cvt_pk_bf16_f32 v117, v166, v167\n"
"v_mfma_f32_16x16x32_bf16 v[80:83], v[148:151], a[248:251], v[80:83]\n"
"s_waitcnt lgkmcnt(5)\n"
"ds_read_b64_tr_b16 a[250:251], v238 offset:20480\n"
"v_xor_b32 v248, s75, v248\n"
"v_mfma_f32_16x16x32_bf16 v[84:87], v[148:151], v[224:227], 0\n"
"v_cvt_pk_bf16_f32 v100, v156, v157\n"
"v_cvt_pk_bf16_f32 v116, v164, v165\n"
"v_mfma_f32_16x16x32_bf16 v[84:87], v[144:147], a[252:255], v[84:87]\n"
"v_exp_f32 v160, v160\n"
"v_mfma_f32_16x16x32_bf16 v[52:55], v[140:143], v[224:227], 0\n"
"v_exp_f32 v176, v176\n"
"v_mfma_f32_16x16x32_bf16 v[52:55], v[136:139], a[252:255], v[52:55]\n"
"v_exp_f32 v177, v177\n"
"v_mfma_f32_16x16x32_bf16 v[20:23], v[132:135], v[224:227], 0\n"
"v_cvt_pk_bf16_f32 v123, v154, v155\n"
"v_cvt_pk_bf16_f32 v118, v176, v177\n"
"v_mfma_f32_16x16x32_bf16 v[20:23], v[128:131], a[252:255], v[20:23]\n"
"v_exp_f32 v174, v174\n"
"v_mfma_f32_16x16x32_bf16 a[228:231], v[124:127], a[252:255], 0\n"
"s_waitcnt lgkmcnt(1)\n"
"ds_read_b64_tr_b16 a[254:255], v235 offset:20480\n"
"s_branch LAY2_%=\n"
"LAX_%=:\n"
"v_mfma_f32_16x16x32_bf16 a[224:227], v[124:127], a[244:247], a[224:227]\n"
"v_exp_f32 v162, v162\n"
"v_mfma_f32_16x16x32_bf16 v[16:19], v[128:131], a[244:247], v[16:19]\n"
"s_waitcnt vmcnt(8)\n"
"v_exp_f32 v171, v171\n"
"v_mfma_f32_16x16x32_bf16 v[16:19], v[132:135], a[248:251], v[16:19]\n"
"s_barrier\n"
"v_exp_f32 v169, v169\n"
"v_exp_f32 v172, v172\n"
"v_exp_f32 v173, v173\n"
"v_exp_f32 v174, v174\n"
"v_lshlrev_b32_e64 v244, 24, s43\n"
"v_mfma_f32_16x16x32_bf16 v[48:51], v[136:139], a[244:247], v[48:51]\n"
"v_exp_f32 v170, v170\n"
"v_mfma_f32_16x16x32_bf16 v[48:51], v[140:143], a[248:251], v[48:51]\n"
"v_exp_f32 v179, v179\n"
"v_mfma_f32_16x16x32_bf16 v[80:83], v[144:147], a[244:247], v[80:83]\n"
"ds_read_b64_tr_b16 a[246:247], v234 offset:53248\n"
"v_cvt_pk_bf16_f32 v117, v166, v167\n"
"v_mfma_f32_16x16x32_bf16 v[80:83], v[148:151], a[248:251], v[80:83]\n"
"ds_read_b64_tr_b16 a[244:245], v234 offset:49152\n"
"v_cvt_pk_bf16_f32 v116, v164, v165\n"
"v_mfma_f32_16x16x32_bf16 v[84:87], v[148:151], v[224:227], v[84:87]\n"
"v_cvt_pk_bf16_f32 v100, v156, v157\n"
"v_accvgpr_read_b32 v253, a96\n"
"v_mfma_f32_16x16x32_bf16 v[84:87], v[144:147], a[252:255], v[84:87]\n"
"ds_read_b64_tr_b16 a[248:249], v238 offset:49152\n"
"ds_read_b64_tr_b16 a[250:251], v238 offset:53248\n"
"v_exp_f32 v168, v168\n"
"v_exp_f32 v175, v175\n"
"v_exp_f32 v176, v176\n"
"v_exp_f32 v177, v177\n"
"v_exp_f32 v178, v178\n"
"v_mfma_f32_16x16x32_bf16 v[52:55], v[140:143], v[224:227], v[52:55]\n"
"v_cvt_pk_bf16_f32 v102, v168, v169\n"
"v_cvt_pk_bf16_f32 v110, v172, v173\n"
"v_mfma_f32_16x16x32_bf16 v[52:55], v[136:139], a[252:255], v[52:55]\n"
"v_cvt_pk_bf16_f32 v103, v170, v171\n"
"v_and_b32 v253, 0x7fff0000, v253\n"
"v_mfma_f32_16x16x32_bf16 v[20:23], v[132:135], v[224:227], v[20:23]\n"
"ds_read_b64_tr_b16 v[224:225], v239 offset:49152\n"
"v_cvt_pk_bf16_f32 v101, v158, v159\n"
"v_mfma_f32_16x16x32_bf16 v[20:23], v[128:131], a[252:255], v[20:23]\n"
"v_cvt_pk_bf16_f32 v123, v154, v155\n"
"v_cvt_pk_bf16_f32 v111, v174, v175\n"
"v_mfma_f32_16x16x32_bf16 a[228:231], v[124:127], a[252:255], a[228:231]\n"
"s_waitcnt lgkmcnt(3)\n"
"ds_read_b64_tr_b16 a[254:255], v235 offset:53248\n"
"v_mul_f32 v244, 0x45800000, v244\n"
"v_mfma_f32_16x16x32_bf16 a[216:219], v[120:123], a[244:247], a[216:219]\n"
"s_mov_b64 s[28:29], s[8:9]\n"
"ds_read_b64_tr_b16 v[226:227], v239 offset:53248\n"
"v_cvt_pk_bf16_f32 v118, v176, v177\n"
"v_smfmac_f32_16x16x64_bf16 v[196:199], a[240:243], v[96:103], v241\n"
"s_mov_b32 s30, 655360\n"
"ds_read_b64_tr_b16 a[252:253], v235 offset:49152\n"
"v_cvt_pk_bf16_f32 v108, v160, v161\n"
"v_mfma_f32_16x16x32_bf16 v[8:11], v[96:99], a[244:247], v[8:11]\n"
"s_waitcnt lgkmcnt(4)\n"
"s_mov_b32 s71, s44\n"
"v_cvt_pk_bf16_f32 v119, v178, v179\n"
"v_max_u32_dpp v253, v253, v253 quad_perm:[1,0,3,2] row_mask:0xf bank_mask:0xf\n"
"v_mfma_f32_16x16x32_bf16 v[8:11], v[100:103], a[248:251], v[8:11]\n"
"s_mov_b32 s70, s95\n"
"v_and_b32 v252, 0x7fff0000, v252\n"
"v_and_b32 v245, 0x3f0, v242\n"
"v_smfmac_f32_16x16x64_bf16 v[200:203], a[240:243], v[112:119], v241\n"
"v_cvt_pk_bf16_f32 v109, v162, v163\n"
"v_max_u32_dpp v253, v253, v253 quad_perm:[2,3,0,1] row_mask:0xf bank_mask:0xf\n"
"v_mfma_f32_16x16x32_bf16 v[40:43], v[104:107], a[244:247], v[40:43]\n"
"v_add_f32 v196, v196, v197\n"
"v_lshrrev_b32 v245, 1, v245\n"
"v_mfma_f32_16x16x32_bf16 v[40:43], v[108:111], a[248:251], v[40:43]\n"
"s_add_u32 s41, s70, 327680\n"
"s_add_u32 m0, s71, 32768\n"
"v_max_u32_dpp v252, v252, v252 quad_perm:[1,0,3,2] row_mask:0xf bank_mask:0xf\n"
"v_smfmac_f32_16x16x64_bf16 v[198:201], a[240:243], v[104:111], v241\n"
"buffer_load_dwordx4 v240, s[28:31], s41 offen lds\n"
"v_mfma_f32_16x16x32_bf16 v[72:75], v[112:115], a[244:247], v[72:75]\n"
"v_max_u32_dpp v252, v252, v252 quad_perm:[2,3,0,1] row_mask:0xf bank_mask:0xf\n"
"v_max_u32_dpp v253, v253, v253 row_half_mirror row_mask:0xf bank_mask:0xf\n"
"v_mfma_f32_16x16x32_bf16 v[72:75], v[116:119], a[248:251], v[72:75]\n"
"ds_read_b64_tr_b16 a[244:245], v234 offset:49664\n"
"v_sub_u32 v245, v242, v245\n"
"v_mfma_f32_16x16x32_bf16 v[202:205], a[240:243], v[120:123], v[202:205]\n"
"s_waitcnt lgkmcnt(1)\n"
"ds_read_b64_tr_b16 a[246:247], v234 offset:53760\n"
"v_add_f32 v198, v198, v199\n"
"v_mfma_f32_16x16x32_bf16 v[76:79], v[116:119], v[224:227], v[76:79]\n"
"s_add_u32 s40, s41, s52\n"
"ds_read_b64_tr_b16 a[248:249], v238 offset:49664\n"
"ds_read_b64_tr_b16 a[250:251], v238 offset:53760\n"
"v_mfma_f32_16x16x32_bf16 v[76:79], v[112:115], a[252:255], v[76:79]\n"
"buffer_load_dwordx4 v240, s[28:31], s40 offen offset:1024 lds\n"
"v_mfma_f32_16x16x32_bf16 v[44:47], v[108:111], v[224:227], v[44:47]\n"
"v_add_f32 v200, v200, v201\n"
"v_add_f32 v244, v244, v202\n"
"v_mfma_f32_16x16x32_bf16 v[44:47], v[104:107], a[252:255], v[44:47]\n"
"v_add_f32 v254, v196, v198\n"
"v_min3_f32 v244, v244, v196, v198\n"
"v_min_f32 v244, v244, v200\n"
"v_rcp_f32 v244, v244\n"
"v_mfma_f32_16x16x32_bf16 v[12:15], v[100:103], v[224:227], v[12:15]\n"
"s_add_u32 s40, s41, s53\n"
"ds_read_b64_tr_b16 v[224:225], v239 offset:49664\n"
"ds_read_b64_tr_b16 v[226:227], v239 offset:53760\n"
"v_mfma_f32_16x16x32_bf16 v[12:15], v[96:99], a[252:255], v[12:15]\n"
"buffer_load_dwordx4 v240, s[28:31], s40 offen offset:2048 lds\n"
"v_min_u32_dpp v253, v253, v253 row_mirror row_mask:0xf bank_mask:0xf\n"
"v_add_f32 v254, v254, v200\n"
"v_add_f32 v254, v254, v202\n"
"v_fmac_f32 v254, 0x68800000, v244\n"
"v_cmp_nge_f32 vcc, s45, v254\n"
"v_bfe_u32 v254, v242, 8, 2\n"
"v_mfma_f32_16x16x32_bf16 a[220:223], v[120:123], a[252:255], a[220:223]\n"
"s_waitcnt lgkmcnt(4)\n"
"v_xor_b32 v244, 0x11000, v242\n"
"s_cbranch_vccnz LSLOW_%=\nLBACK3_%=:\ns_mov_b64 vcc, s[64:65]\n"
"v_mfma_f32_16x16x32_bf16 a[232:235], v[120:123], a[244:247], a[232:235]\n"
"ds_read_b64_tr_b16 a[252:253], v235 offset:49664\n"
"ds_read_b64_tr_b16 a[254:255], v235 offset:53760\n"
"v_min_u32_dpp v253, v253, v253 row_bcast:15 row_mask:0xa bank_mask:0xf\n"
"v_rcp_f32 v246, v196\n"
"v_rcp_f32 v247, v198\n"
"v_rcp_f32 v206, v200\n"
"v_xor_b32 v250, 0x1000, v245\n"
"v_mfma_f32_16x16x32_bf16 v[24:27], v[96:99], a[244:247], v[24:27]\n"
"s_waitcnt lgkmcnt(4)\n"
"v_mov_b32_dpp v206, v206 row_ror:12 row_mask:0x2 bank_mask:0xf\n"
"v_lshl_add_u32 v254, v254, 3, s43\n"
"v_mfma_f32_16x16x32_bf16 v[24:27], v[100:103], a[248:251], v[24:27]\n"
"ds_write_b128 v244, a[220:223] offset:33792\n"
"v_max_u32_dpp v252, v252, v252 row_half_mirror row_mask:0xf bank_mask:0xf\n"
"v_mov_b32_dpp v246, v246 row_ror:12 row_mask:0x2 bank_mask:0xf\n"
"v_mov_b32_dpp v206, v206 row_ror:8 row_mask:0x4 bank_mask:0xf\n"
"v_mfma_f32_16x16x32_bf16 v[56:59], v[104:107], a[244:247], v[56:59]\n"
"s_add_u32 s40, s41, s54\n"
"v_min_u32_dpp v252, v252, v252 row_mirror row_mask:0xf bank_mask:0xf\n"
"v_mov_b32_dpp v247, v247 row_ror:12 row_mask:0x2 bank_mask:0xf\n"
"v_mfma_f32_16x16x32_bf16 v[56:59], v[108:111], a[248:251], v[56:59]\n"
"buffer_load_dwordx4 v240, s[28:31], s40 offen offset:3072 lds\n"
"v_mfma_f32_16x16x32_bf16 v[88:91], v[112:115], a[244:247], v[88:91]\n"
"s_add_u32 s71, s44, s75\n"
"ds_read_b64_tr_b16 a[244:245], v232 offset:49152\n"
"ds_read_b64_tr_b16 a[246:247], v232 offset:53248\n"
"v_mfma_f32_16x16x32_bf16 v[88:91], v[116:119], a[248:251], v[88:91]\n"
"s_waitcnt lgkmcnt(5)\n"
"s_mov_b32 s70, s100\n"
"ds_write_b128 v244, a[232:235] offset:34816\n"
"v_mov_b32_dpp v246, v246 row_ror:8 row_mask:0x4 bank_mask:0xf\n"
"v_mov_b32_dpp v247, v247 row_ror:8 row_mask:0x4 bank_mask:0xf\n"
"v_mov_b32_dpp v206, v206 row_ror:4 row_mask:0x8 bank_mask:0xf\n"
"v_lshl_add_u32 v254, v254, 6, s75\n"
"v_sub_u32 v230, v234, v254\n"
"v_mfma_f32_16x16x32_bf16 v[92:95], v[116:119], v[224:227], v[92:95]\n"
"s_waitcnt lgkmcnt(4)\n"
"s_add_u32 s41, s70, 163840\n"
"s_add_u32 m0, s71, 16384\n"
"ds_read_b64_tr_b16 a[248:249], v236 offset:49152\n"
"v_mfma_f32_16x16x32_bf16 v[92:95], v[112:115], a[252:255], v[92:95]\n"
"buffer_load_dwordx4 v240, s[28:31], s41 offen lds\n"
"ds_read_b64_tr_b16 a[250:251], v236 offset:53248\n"
"v_min_u32_dpp v252, v252, v252 row_bcast:15 row_mask:0xa bank_mask:0xf\n"
"v_mov_b32_dpp v246, v246 row_ror:4 row_mask:0x8 bank_mask:0xf\n"
"v_mov_b32_dpp v247, v247 row_ror:4 row_mask:0x8 bank_mask:0xf\n"
"v_mul_f32_dpp v72, v206, v72 row_newbcast:0 row_mask:0xf bank_mask:0xf\n"
"v_mul_f32_dpp v74, v206, v74 row_newbcast:2 row_mask:0xf bank_mask:0xf\n"
"v_mul_f32_dpp v76, v206, v76 row_newbcast:0 row_mask:0xf bank_mask:0xf\n"
"v_mul_f32_dpp v77, v206, v77 row_newbcast:1 row_mask:0xf bank_mask:0xf\n"
"v_mfma_f32_16x16x32_bf16 v[60:63], v[108:111], v[224:227], v[60:63]\n"
"v_min_u32_dpp v252, v252, v252 row_bcast:31 row_mask:0xc bank_mask:0xf\n"
"v_mul_f32_dpp v8, v246, v8 row_newbcast:0 row_mask:0xf bank_mask:0xf\n"
"v_mfma_f32_16x16x32_bf16 v[60:63], v[104:107], a[252:255], v[60:63]\n"
"v_mul_f32_dpp v41, v247, v41 row_newbcast:1 row_mask:0xf bank_mask:0xf\n"
"v_mul_f32_dpp v59, v247, v59 row_newbcast:3 row_mask:0xf bank_mask:0xf\n"
"v_mfma_f32_16x16x32_bf16 v[28:31], v[100:103], v[224:227], v[28:31]\n"
"s_add_u32 s40, s41, s52\n"
"ds_read_b64_tr_b16 v[224:225], v237 offset:49152\n"
"v_min_u32_dpp v253, v253, v253 row_bcast:31 row_mask:0xc bank_mask:0xf\n"
"v_mfma_f32_16x16x32_bf16 v[28:31], v[96:99], a[252:255], v[28:31]\n"
"buffer_load_dwordx4 v240, s[28:31], s40 offen offset:1024 lds\n"
"v_mfma_f32_16x16x32_bf16 a[236:239], v[120:123], a[252:255], a[236:239]\n"
"s_waitcnt lgkmcnt(4)\n"
"v_mul_f32_dpp v25, v246, v25 row_newbcast:1 row_mask:0xf bank_mask:0xf\n"
"v_mul_f32_dpp v94, v206, v94 row_newbcast:2 row_mask:0xf bank_mask:0xf\n"
"v_mfma_f32_16x16x32_bf16 a[208:211], v[120:123], a[244:247], a[208:211]\n"
"ds_read_b64_tr_b16 a[252:253], v233 offset:49152\n"
"ds_read_b64_tr_b16 a[254:255], v233 offset:53248\n"
"v_mul_f32 v252, 0x43340000, v252\n"
"v_mul_f32 v252, v252, v253\n"
"v_mul_f32_dpp v10, v246, v10 row_newbcast:2 row_mask:0xf bank_mask:0xf\n"
"v_mul_f32_dpp v14, v246, v14 row_newbcast:2 row_mask:0xf bank_mask:0xf\n"
"v_mul_f32_dpp v40, v247, v40 row_newbcast:0 row_mask:0xf bank_mask:0xf\n"
"v_mul_f32_dpp v46, v247, v46 row_newbcast:2 row_mask:0xf bank_mask:0xf\n"
"v_mul_f32_dpp v73, v206, v73 row_newbcast:1 row_mask:0xf bank_mask:0xf\n"
"v_mul_f32_dpp v90, v206, v90 row_newbcast:2 row_mask:0xf bank_mask:0xf\n"
"v_mfma_f32_16x16x32_bf16 v[0:3], v[96:99], a[244:247], v[0:3]\n"
"s_waitcnt lgkmcnt(3)\n"
"ds_read_b64_tr_b16 v[226:227], v237 offset:53248\n"
"v_mul_f32_dpp v57, v247, v57 row_newbcast:1 row_mask:0xf bank_mask:0xf\n"
"v_mfma_f32_16x16x32_bf16 v[0:3], v[100:103], a[248:251], v[0:3]\n"
"ds_write_b128 v244, a[216:219] offset:32768\n"
"v_min_f32 v252, 0x71800000, v252\n"
"v_mul_f32_dpp v12, v246, v12 row_newbcast:0 row_mask:0xf bank_mask:0xf\n"
"v_mul_f32_dpp v45, v247, v45 row_newbcast:1 row_mask:0xf bank_mask:0xf\n"
"v_mul_f32_dpp v58, v247, v58 row_newbcast:2 row_mask:0xf bank_mask:0xf\n"
"v_mul_f32_dpp v92, v206, v92 row_newbcast:0 row_mask:0xf bank_mask:0xf\n"
"v_mfma_f32_16x16x32_bf16 v[32:35], v[104:107], a[244:247], v[32:35]\n"
"s_add_u32 s40, s41, s53\n"
"v_readlane_b32 s22, v252, 63\n"
"v_mul_f32_dpp v78, v206, v78 row_newbcast:2 row_mask:0xf bank_mask:0xf\n"
"v_mfma_f32_16x16x32_bf16 v[32:35], v[108:111], a[248:251], v[32:35]\n"
"buffer_load_dwordx4 v240, s[28:31], s40 offen offset:2048 lds\n"
"v_mfma_f32_16x16x32_bf16 v[64:67], v[112:115], a[244:247], v[64:67]\n"
"v_mul_f32_dpp v75, v206, v75 row_newbcast:3 row_mask:0xf bank_mask:0xf\n"
"v_mul_f32_dpp v61, v247, v61 row_newbcast:1 row_mask:0xf bank_mask:0xf\n"
"v_mfma_f32_16x16x32_bf16 v[64:67], v[116:119], a[248:251], v[64:67]\n"
"s_waitcnt lgkmcnt(1)\n"
"ds_read_b64_tr_b16 a[244:245], v232 offset:49664\n"
"ds_read_b64_tr_b16 a[246:247], v232 offset:53760\n"
"v_mov_b32 v253, v202\n"
"v_sub_u32 v228, v232, v254\n"
"v_mul_f32_dpp v9, v246, v9 row_newbcast:1 row_mask:0xf bank_mask:0xf\n"
"v_mul_f32_dpp v11, v246, v11 row_newbcast:3 row_mask:0xf bank_mask:0xf\n"
"v_mul_f32_dpp v15, v246, v15 row_newbcast:3 row_mask:0xf bank_mask:0xf\n"
"v_mul_f32_dpp v47, v247, v47 row_newbcast:3 row_mask:0xf bank_mask:0xf\n"
"v_cvt_pk_bf16_f32 v40, v40, v41\n"
"v_cvt_pk_bf16_f32 v72, v72, v73\n"
"v_mfma_f32_16x16x32_bf16 v[68:71], v[116:119], v[224:227], v[68:71]\n"
"s_add_u32 s40, s41, s54\n"
"ds_read_b64_tr_b16 a[248:249], v236 offset:49664\n"
"ds_read_b64_tr_b16 a[250:251], v236 offset:53760\n"
"v_mfma_f32_16x16x32_bf16 v[68:71], v[112:115], a[252:255], v[68:71]\n"
"buffer_load_dwordx4 v240, s[28:31], s40 offen offset:3072 lds\n"
"v_readlane_b32 s23, v252, 63\n"
"v_mul_f32_dpp v43, v247, v43 row_newbcast:3 row_mask:0xf bank_mask:0xf\n"
"v_mul_f32_dpp v24, v246, v24 row_newbcast:0 row_mask:0xf bank_mask:0xf\n"
"v_mul_f32_dpp v29, v246, v29 row_newbcast:1 row_mask:0xf bank_mask:0xf\n"
"v_mul_f32_dpp v56, v247, v56 row_newbcast:0 row_mask:0xf bank_mask:0xf\n"
"v_mul_f32_dpp v1, v246, v1 row_newbcast:1 row_mask:0xf bank_mask:0xf\n"
"v_accvgpr_read_b32 v255, a209\n"
"v_xor_b32 v232, 0x1000, v228\n"
"v_mfma_f32_16x16x32_bf16 v[36:39], v[108:111], v[224:227], v[36:39]\n"
"v_mov_b32 v252, v192\n"
"v_mul_f32_dpp v44, v247, v44 row_newbcast:0 row_mask:0xf bank_mask:0xf\n"
"v_mfma_f32_16x16x32_bf16 v[36:39], v[104:107], a[252:255], v[36:39]\n"
"ds_write_b128 v244, a[236:239] offset:35840\n"
"v_mul_f32_dpp v13, v246, v13 row_newbcast:1 row_mask:0xf bank_mask:0xf\n"
"v_mul_f32_dpp v42, v247, v42 row_newbcast:2 row_mask:0xf bank_mask:0xf\n"
"v_cvt_pk_bf16_f32 v41, v42, v43\n"
"v_cvt_pk_bf16_f32 v42, v44, v45\n"
"v_cvt_pk_bf16_f32 v56, v56, v57\n"
"v_mfma_f32_16x16x32_bf16 v[4:7], v[100:103], v[224:227], v[4:7]\n"
"ds_read_b64_tr_b16 v[224:225], v237 offset:49664\n"
"ds_read_b64_tr_b16 v[226:227], v237 offset:53760\n"
"v_mfma_f32_16x16x32_bf16 v[4:7], v[96:99], a[252:255], v[4:7]\n"
"ds_write_b64 v250, v[252:253] offset:52224\n"
"v_cvt_pk_bf16_f32 v8, v8, v9\n"
"v_cvt_pk_bf16_f32 v43, v46, v47\n"
"v_cvt_pk_bf16_f32 v73, v74, v75\n"
"v_mul_f32_dpp v28, v246, v28 row_newbcast:0 row_mask:0xf bank_mask:0xf\n"
"v_cvt_pk_bf16_f32 v57, v58, v59\n"
"v_mul_f32_dpp v0, v246, v0 row_newbcast:0 row_mask:0xf bank_mask:0xf\n"
"v_mul_f32_dpp v33, v247, v33 row_newbcast:1 row_mask:0xf bank_mask:0xf\n"
"v_mfma_f32_16x16x32_bf16 a[212:215], v[120:123], a[252:255], a[212:215]\n"
"s_waitcnt lgkmcnt(6)\n"
"ds_read_b64_tr_b16 a[252:253], v233 offset:49664\n"
"ds_read_b64_tr_b16 a[254:255], v233 offset:53760\n"
"v_cvt_pk_bf16_f32 v9, v10, v11\n"
"v_cvt_pk_bf16_f32 v74, v76, v77\n"
"v_mul_f32_dpp v60, v247, v60 row_newbcast:0 row_mask:0xf bank_mask:0xf\n"
"v_mul_f32_dpp v62, v247, v62 row_newbcast:2 row_mask:0xf bank_mask:0xf\n"
"v_mul_f32_dpp v88, v206, v88 row_newbcast:0 row_mask:0xf bank_mask:0xf\n"
"v_mul_f32_dpp v64, v206, v64 row_newbcast:0 row_mask:0xf bank_mask:0xf\n"
"v_mul_f32_dpp v66, v206, v66 row_newbcast:2 row_mask:0xf bank_mask:0xf\n"
"v_mfma_f32_16x16x32_bf16 a[224:227], v[120:123], a[244:247], a[224:227]\n"
"ds_write_b128 v248, v[40:43] offset:50176\n"
"v_sub_u32 v229, v233, v254\n"
"v_cvt_pk_bf16_f32 v11, v14, v15\n"
"v_mul_f32_dpp v27, v246, v27 row_newbcast:3 row_mask:0xf bank_mask:0xf\n"
"v_mul_f32_dpp v3, v246, v3 row_newbcast:3 row_mask:0xf bank_mask:0xf\n"
"v_mul_f32_dpp v68, v206, v68 row_newbcast:0 row_mask:0xf bank_mask:0xf\n"
"v_mfma_f32_16x16x32_bf16 v[16:19], v[96:99], a[244:247], v[16:19]\n"
"s_waitcnt vmcnt(8) lgkmcnt(1)\n"
"v_mul_f32_dpp v89, v206, v89 row_newbcast:1 row_mask:0xf bank_mask:0xf\n"
"v_add_u32 v236, s76, v249\n"
"v_mfma_f32_16x16x32_bf16 v[16:19], v[100:103], a[248:251], v[16:19]\n"
"s_barrier\n"
"ds_read_b64_tr_b16 a[22:23], v249 offset:50184\n"
"ds_read_b64_tr_b16 v[182:183], v228 offset:2048\n"
"ds_read_b64_tr_b16 v[216:217], v228 offset:24576\n"
"ds_read_b64_tr_b16 v[218:219], v228 offset:26624\n"
"ds_read_b64_tr_b16 a[202:203], v236 offset:2056\n"
"v_mul_f32_dpp v63, v247, v63 row_newbcast:3 row_mask:0xf bank_mask:0xf\n"
"v_mul_f32_dpp v91, v206, v91 row_newbcast:3 row_mask:0xf bank_mask:0xf\n"
"v_mul_f32_dpp v2, v246, v2 row_newbcast:2 row_mask:0xf bank_mask:0xf\n"
"v_mfma_f32_16x16x32_bf16 v[48:51], v[104:107], a[244:247], v[48:51]\n"
"ds_read_b64_tr_b16 v[192:193], v232 offset:8192\n"
"ds_read_b64_tr_b16 a[240:241], v236 offset:4096\n"
"v_mfma_f32_16x16x32_bf16 v[48:51], v[108:111], a[248:251], v[48:51]\n"
"ds_read_b64 v[250:251], v245 offset:52224\n"
"ds_read_b64_tr_b16 v[186:187], v232 offset:2048\n"
"ds_read_b64_tr_b16 v[194:195], v232 offset:10240\n"
"ds_read_b64_tr_b16 v[208:209], v228 offset:16384\n"
"v_cvt_pk_bf16_f32 v10, v12, v13\n"
"v_mul_f32_dpp v79, v206, v79 row_newbcast:3 row_mask:0xf bank_mask:0xf\n"
"v_cvt_pk_bf16_f32 v75, v78, v79\n"
"v_cvt_pk_bf16_f32 v88, v88, v89\n"
"v_cvt_pk_bf16_f32 v89, v90, v91\n"
"v_cvt_pk_bf16_f32 v0, v0, v1\n"
"v_mfma_f32_16x16x32_bf16 v[80:83], v[112:115], a[244:247], v[80:83]\n"
"ds_read_b64_tr_b16 v[180:181], v228 offset:0\n"
"ds_read_b64_tr_b16 v[188:189], v228 offset:8192\n"
"v_mfma_f32_16x16x32_bf16 v[80:83], v[116:119], a[248:251], v[80:83]\n"
"ds_write_b128 v248, v[72:75] offset:51200\n"
"v_mul_f32_dpp v30, v246, v30 row_newbcast:2 row_mask:0xf bank_mask:0xf\n"
"v_cvt_pk_bf16_f32 v24, v24, v25\n"
"v_cvt_pk_bf16_f32 v58, v60, v61\n"
"v_mul_f32_dpp v93, v206, v93 row_newbcast:1 row_mask:0xf bank_mask:0xf\n"
"v_mul_f32_dpp v36, v247, v36 row_newbcast:0 row_mask:0xf bank_mask:0xf\n"
"v_mfma_f32_16x16x32_bf16 v[84:87], v[116:119], v[224:227], v[84:87]\n"
"ds_write_b128 v248, v[8:11] offset:49152\n"
"v_mul_f32_dpp v26, v246, v26 row_newbcast:2 row_mask:0xf bank_mask:0xf\n"
"v_mul_f32_dpp v32, v247, v32 row_newbcast:0 row_mask:0xf bank_mask:0xf\n"
"v_mul_f32_dpp v65, v206, v65 row_newbcast:1 row_mask:0xf bank_mask:0xf\n"
"v_mul_f32_dpp v69, v206, v69 row_newbcast:1 row_mask:0xf bank_mask:0xf\n"
"v_xor_b32 v233, 0x1000, v229\n"
"v_mfma_f32_16x16x32_bf16 v[84:87], v[112:115], a[252:255], v[84:87]\n"
"ds_read_b64_tr_b16 a[6:7], v249 offset:49160\n"
"ds_read_b64_tr_b16 a[36:37], v249 offset:51200\n"
"ds_read_b64_tr_b16 v[210:211], v228 offset:18432\n"
"ds_read_b64_tr_b16 v[222:223], v232 offset:26624\n"
"v_mul_f32_dpp v31, v246, v31 row_newbcast:3 row_mask:0xf bank_mask:0xf\n"
"v_cvt_pk_bf16_f32 v25, v26, v27\n"
"v_cvt_pk_bf16_f32 v59, v62, v63\n"
"v_mul_f32_dpp v34, v247, v34 row_newbcast:2 row_mask:0xf bank_mask:0xf\n"
"v_mov_b64 v[196:197], s[22:23]\n"
"v_xor_b32 v237, 0x400, v236\n"
"v_mfma_f32_16x16x32_bf16 v[52:55], v[108:111], v[224:227], v[52:55]\n"
"ds_read_b64_tr_b16 v[184:185], v232 offset:0\n"
"ds_read_b64_tr_b16 v[212:213], v232 offset:16384\n"
"v_mfma_f32_16x16x32_bf16 v[52:55], v[104:107], a[252:255], v[52:55]\n"
"ds_read_b64_tr_b16 a[4:5], v249 offset:49152\n"
"ds_read_b64_tr_b16 a[20:21], v249 offset:50176\n"
"ds_read_b64_tr_b16 a[38:39], v249 offset:51208\n"
"ds_read_b64_tr_b16 v[220:221], v232 offset:24576\n"
"ds_read_b64_tr_b16 a[246:247], v237 offset:4104\n"
"v_cvt_pk_bf16_f32 v26, v28, v29\n"
"v_mul_f32_dpp v95, v206, v95 row_newbcast:3 row_mask:0xf bank_mask:0xf\n"
"v_cvt_pk_bf16_f32 v91, v94, v95\n"
"v_mul_f32_dpp v5, v246, v5 row_newbcast:1 row_mask:0xf bank_mask:0xf\n"
"v_mfma_f32_16x16x32_bf16 v[20:23], v[100:103], v[224:227], v[20:23]\n"
"ds_read_b64_tr_b16 v[190:191], v228 offset:10240\n"
"ds_read_b64_tr_b16 v[214:215], v232 offset:18432\n"
"v_mfma_f32_16x16x32_bf16 v[20:23], v[96:99], a[252:255], v[20:23]\n"
"ds_write_b128 v248, v[56:59] offset:50176\n"
"v_cvt_pk_bf16_f32 v27, v30, v31\n"
"v_mul_f32_dpp v4, v246, v4 row_newbcast:0 row_mask:0xf bank_mask:0xf\n"
"v_mul_f32_dpp v35, v247, v35 row_newbcast:3 row_mask:0xf bank_mask:0xf\n"
"v_cvt_pk_bf16_f32 v64, v64, v65\n"
"v_mov_b64 v[198:199], s[22:23]\n"
"v_mfma_f32_16x16x32_bf16 a[228:231], v[120:123], a[252:255], a[228:231]\n"
"s_waitcnt lgkmcnt(15)\n"
"ds_write_b128 v248, v[24:27] offset:49152\n"
"v_cvt_pk_bf16_f32 v90, v92, v93\n"
"v_mul_f32_dpp v6, v246, v6 row_newbcast:2 row_mask:0xf bank_mask:0xf\n"
"v_cvt_pk_bf16_f32 v1, v2, v3\n"
"v_cvt_pk_bf16_f32 v2, v4, v5\n"
"v_mul_f32_dpp v71, v206, v71 row_newbcast:3 row_mask:0xf bank_mask:0xf\n"
"v_mfma_f32_16x16x32_bf16 v[124:127], v[180:183], a[96:99], v[196:199]\n"
"s_waitcnt lgkmcnt(10)\n"
"ds_read_b64_tr_b16 a[14:15], v249 offset:49160\n"
"ds_read_b64_tr_b16 a[30:31], v249 offset:50184\n"
"v_mfma_f32_16x16x32_bf16 v[124:127], v[184:187], a[100:103], v[124:127]\n"
"s_waitcnt lgkmcnt(5)\n"
"ds_write_b128 v248, v[88:91] offset:51200\n"
"v_mul_f32_dpp v7, v246, v7 row_newbcast:3 row_mask:0xf bank_mask:0xf\n"
"v_cvt_pk_bf16_f32 v3, v6, v7\n"
"v_mul_f32_dpp v37, v247, v37 row_newbcast:1 row_mask:0xf bank_mask:0xf\n"
"v_cvt_pk_bf16_f32 v32, v32, v33\n"
"v_mul_f32_dpp v67, v206, v67 row_newbcast:3 row_mask:0xf bank_mask:0xf\n"
"v_mfma_f32_16x16x32_bf16 v[124:127], v[188:191], a[104:107], v[124:127]\n"
"ds_read_b64_tr_b16 a[12:13], v249 offset:49152\n"
"ds_read_b64_tr_b16 a[44:45], v249 offset:51200\n"
"ds_read_b64_tr_b16 a[46:47], v249 offset:51208\n"
"ds_read_b64_tr_b16 v[100:101], v233 offset:0\n"
"ds_read_b64_tr_b16 v[120:121], v229 offset:24576\n"
"v_mul_f32_dpp v38, v247, v38 row_newbcast:2 row_mask:0xf bank_mask:0xf\n"
"v_mul_f32_dpp v70, v206, v70 row_newbcast:2 row_mask:0xf bank_mask:0xf\n"
"v_cvt_pk_bf16_f32 v65, v66, v67\n"
"v_cvt_pk_bf16_f32 v66, v68, v69\n"
"v_cvt_pk_bf16_f32 v67, v70, v71\n"
"v_mfma_f32_16x16x32_bf16 v[124:127], v[192:195], a[108:111], v[124:127]\n"
"ds_write_b128 v248, v[0:3] offset:49152\n"
"v_mul_f32_dpp v39, v247, v39 row_newbcast:3 row_mask:0xf bank_mask:0xf\n"
"v_cvt_pk_bf16_f32 v33, v34, v35\n"
"v_cvt_pk_bf16_f32 v34, v36, v37\n"
"v_cvt_pk_bf16_f32 v35, v38, v39\n"
"v_xor_b32 v244, 0x1000, v244\n"
"v_mfma_f32_16x16x32_bf16 v[124:127], v[208:211], a[144:147], v[124:127]\n"
"s_waitcnt lgkmcnt(11)\n"
"ds_read_b64_tr_b16 a[28:29], v249 offset:50176\n"
"ds_read_b128 v[24:27], v244 offset:32768\n"
"v_mfma_f32_16x16x32_bf16 v[124:127], v[212:215], a[148:151], v[124:127]\n"
"ds_write_b128 v248, v[32:35] offset:50176\n"
"ds_write_b128 v248, v[64:67] offset:51200\n"
"v_mfma_f32_16x16x32_bf16 v[124:127], v[216:219], a[152:155], v[124:127]\n"
"ds_read_b64_tr_b16 a[0:1], v249 offset:49152\n"
"ds_read_b64_tr_b16 a[32:33], v249 offset:51200\n"
"v_mfma_f32_16x16x32_bf16 v[124:127], v[220:223], a[156:159], v[124:127]\n"
"ds_read_b64_tr_b16 a[16:17], v249 offset:50176\n"
"ds_read_b64_tr_b16 a[18:19], v249 offset:50184\n"
"ds_read_b128 v[56:59], v244 offset:34816\n"
"ds_read_b64_tr_b16 a[196:197], v237 offset:0\n"
"v_sub_u32 v231, v235, v254\n"
"v_mfma_f32_16x16x32_bf16 v[140:143], v[220:223], a[172:175], v[196:199]\n"
"ds_read_b64_tr_b16 a[2:3], v249 offset:49160\n"
"ds_read_b64_tr_b16 a[34:35], v249 offset:51208\n"
"v_mfma_f32_16x16x32_bf16 v[140:143], v[216:219], a[168:171], v[140:143]\n"
"ds_read_b128 v[28:31], v244 offset:33792\n"
"ds_read_b128 v[60:63], v244 offset:35840\n"
"v_mfma_f32_16x16x32_bf16 v[140:143], v[212:215], a[164:167], v[140:143]\n"
"ds_read_b64_tr_b16 a[200:201], v236 offset:2048\n"
"ds_read_b64_tr_b16 a[254:255], v237 offset:6152\n"
"v_mfma_f32_16x16x32_bf16 v[140:143], v[208:211], a[160:163], v[140:143]\n"
"ds_read_b64_tr_b16 a[206:207], v237 offset:2056\n"
"ds_read_b64_tr_b16 a[242:243], v236 offset:4104\n"
"v_mfma_f32_16x16x32_bf16 v[140:143], v[192:195], a[124:127], v[140:143]\n"
"ds_read_b64_tr_b16 a[244:245], v237 offset:4096\n"
"v_xor_b32 v234, 0x1000, v230\n"
"v_mfma_f32_16x16x32_bf16 v[140:143], v[188:191], a[120:123], v[140:143]\n"
"ds_read_b64_tr_b16 v[110:111], v233 offset:10240\n"
"ds_read_b64_tr_b16 a[250:251], v236 offset:6152\n"
"v_mfma_f32_16x16x32_bf16 v[140:143], v[184:187], a[116:119], v[140:143]\n"
"ds_read_b64_tr_b16 a[204:205], v237 offset:2048\n"
"ds_read_b64_tr_b16 v[102:103], v233 offset:2048\n"
"v_mfma_f32_16x16x32_bf16 v[140:143], v[180:183], a[112:115], v[140:143]\n"
"ds_read_b64_tr_b16 a[248:249], v236 offset:6144\n"
"ds_read_b64_tr_b16 a[252:253], v237 offset:6144\n"
"v_mfma_f32_16x16x32_bf16 v[156:159], v[180:183], a[128:131], v[196:199]\n"
"ds_read_b64_tr_b16 v[98:99], v229 offset:2048\n"
"ds_read_b64_tr_b16 v[116:117], v233 offset:16384\n"
"v_mfma_f32_16x16x32_bf16 v[156:159], v[184:187], a[132:135], v[156:159]\n"
"ds_read_b64_tr_b16 v[122:123], v229 offset:26624\n"
"ds_read_b64_tr_b16 v[224:225], v233 offset:24576\n"
"v_mfma_f32_16x16x32_bf16 v[156:159], v[188:191], a[136:139], v[156:159]\n"
"ds_read_b64_tr_b16 a[198:199], v237 offset:8\n"
"ds_read_b64_tr_b16 v[96:97], v229 offset:0\n"
"v_mfma_f32_16x16x32_bf16 v[156:159], v[192:195], a[140:143], v[156:159]\n"
"s_waitcnt lgkmcnt(13)\n"
"ds_read_b64_tr_b16 v[108:109], v233 offset:8192\n"
"v_mul_f32_dpp v23, v246, v23 row_newbcast:3 row_mask:0xf bank_mask:0xf\n"
"v_mfma_f32_16x16x32_bf16 v[156:159], v[208:211], a[176:179], v[156:159]\n"
"ds_read_b64_tr_b16 v[106:107], v229 offset:10240\n"
"ds_read_b64_tr_b16 v[112:113], v229 offset:16384\n"
"v_mfma_f32_16x16x32_bf16 v[156:159], v[212:215], a[180:183], v[156:159]\n"
"s_add_u32 s41, s95, 491520\n"
"ds_read_b64_tr_b16 a[192:193], v236 offset:0\n"
"ds_read_b64_tr_b16 v[118:119], v233 offset:18432\n"
"v_mfma_f32_16x16x32_bf16 v[156:159], v[216:219], a[184:187], v[156:159]\n"
"s_add_u32 m0, s44, 49152\n"
"ds_read_b64_tr_b16 a[194:195], v236 offset:8\n"
"ds_read_b64_tr_b16 v[104:105], v229 offset:8192\n"
"v_mfma_f32_16x16x32_bf16 v[156:159], v[220:223], a[188:191], v[156:159]\n"
"s_waitcnt lgkmcnt(13)\n"
"buffer_load_dwordx4 v240, s[28:31], s41 offen lds\n"
"v_mfma_f32_16x16x32_bf16 v[172:175], v[220:223], a[252:255], v[196:199]\n"
"s_add_u32 s40, s41, s52\n"
"v_min_f32 v254, v252, v250\n"
"v_sub_f32 v250, v254, v250\n"
"v_mfma_f32_16x16x32_bf16 v[172:175], v[216:219], a[248:251], v[172:175]\n"
"buffer_load_dwordx4 v240, s[28:31], s40 offen offset:1024 lds\n"
"v_mfma_f32_16x16x32_bf16 v[172:175], v[212:215], a[244:247], v[172:175]\n"
"s_add_u32 s40, s41, s53\n"
"ds_read_b64_tr_b16 v[114:115], v229 offset:18432\n"
"ds_read_b64_tr_b16 v[218:219], v230 offset:26624\n"
"v_mfma_f32_16x16x32_bf16 v[172:175], v[208:211], a[240:243], v[172:175]\n"
"buffer_load_dwordx4 v240, s[28:31], s40 offen offset:2048 lds\n"
"v_mfma_f32_16x16x32_bf16 v[172:175], v[192:195], a[204:207], v[172:175]\n"
"ds_read_b64_tr_b16 v[226:227], v233 offset:26624\n"
"ds_read_b64_tr_b16 a[232:233], v236 offset:10240\n"
"v_mfma_f32_16x16x32_bf16 v[172:175], v[188:191], a[200:203], v[172:175]\n"
"s_waitcnt lgkmcnt(6)\n"
"v_xor_b32 v235, 0x1000, v231\n"
"v_mul_f32_dpp v17, v246, v17 row_newbcast:1 row_mask:0xf bank_mask:0xf\n"
"v_mfma_f32_16x16x32_bf16 v[172:175], v[184:187], a[196:199], v[172:175]\n"
"s_waitcnt lgkmcnt(4)\n"
"s_add_u32 s40, s41, s54\n"
"ds_read_b64_tr_b16 v[222:223], v234 offset:26624\n"
"v_mul_f32_dpp v18, v246, v18 row_newbcast:2 row_mask:0xf bank_mask:0xf\n"
"v_mfma_f32_16x16x32_bf16 v[172:175], v[180:183], a[192:195], v[172:175]\n"
"buffer_load_dwordx4 v240, s[28:31], s40 offen offset:3072 lds\n"
"v_mfma_f32_16x16x32_bf16 v[128:131], v[96:99], a[96:99], v[196:199]\n"
"v_sub_f32 v252, v254, v252\n"
"v_mul_f32_dpp v16, v246, v16 row_newbcast:0 row_mask:0xf bank_mask:0xf\n"
"v_mfma_f32_16x16x32_bf16 v[128:131], v[100:103], a[100:103], v[128:131]\n"
"ds_read_b64_tr_b16 v[190:191], v230 offset:10240\n"
"v_mul_f32_dpp v86, v206, v86 row_newbcast:2 row_mask:0xf bank_mask:0xf\n"
"v_mfma_f32_16x16x32_bf16 v[128:131], v[104:107], a[104:107], v[128:131]\n"
"v_exp_f32 v252, v252\n"
"v_mfma_f32_16x16x32_bf16 v[128:131], v[108:111], a[108:111], v[128:131]\n"
"s_waitcnt lgkmcnt(4)\n"
"v_exp_f32 v250, v250\n"
"v_mfma_f32_16x16x32_bf16 v[128:131], v[112:115], a[144:147], v[128:131]\n"
"s_add_u32 s21, s60, 32\n"
"ds_read_b64_tr_b16 a[220:221], v237 offset:8192\n"
"v_mul_f32 v253, v253, v252\n"
"v_mfma_f32_16x16x32_bf16 v[128:131], v[116:119], a[148:151], v[128:131]\n"
"s_mov_b32 s69, 1\n"
"ds_read_b64_tr_b16 v[186:187], v234 offset:2048\n"
"v_fmac_f32 v253, v251, v250\n"
"v_mfma_f32_16x16x32_bf16 v[128:131], v[120:123], a[152:155], v[128:131]\n"
"s_waitcnt lgkmcnt(3)\n"
"v_bfe_u32 v201, v242, 2, 7\n"
"v_add_u32 v201, 0x100, v201\n"
"ds_read_b64_tr_b16 v[184:185], v234 offset:0\n"
"v_mfma_f32_16x16x32_bf16 v[128:131], v[224:227], a[156:159], v[128:131]\n"
"global_load_dword v200, v201, s[6:7] sc1\n"
"v_mfma_f32_16x16x32_bf16 v[144:147], v[224:227], a[172:175], v[196:199]\n"
"v_rcp_f32 v253, v253\n"
"v_mfma_f32_16x16x32_bf16 v[144:147], v[120:123], a[168:171], v[144:147]\n"
"v_mul_f32 v252, v252, v253\n"
"v_accvgpr_read_b32 v254, a208\n"
"v_mfma_f32_16x16x32_bf16 v[144:147], v[116:119], a[164:167], v[144:147]\n"
"ds_read_b64_tr_b16 v[188:189], v230 offset:8192\n"
"v_mul_f32 v250, v250, v253\n"
"v_mfma_f32_16x16x32_bf16 v[144:147], v[112:115], a[160:163], v[144:147]\n"
"s_lshl_b32 s20, s95, 9\n"
"ds_read_b64_tr_b16 v[194:195], v234 offset:10240\n"
"v_mov_b32_dpp v250, v250 row_ror:12 row_mask:0x2 bank_mask:0xf\n"
"v_mfma_f32_16x16x32_bf16 v[144:147], v[108:111], a[124:127], v[144:147]\n"
"s_lshl_b32 s19, s61, 7\n"
"v_mov_b32_dpp v250, v250 row_ror:8 row_mask:0x4 bank_mask:0xf\n"
"v_accvgpr_read_b32 v253, a210\n"
"v_mfma_f32_16x16x32_bf16 v[144:147], v[104:107], a[120:123], v[144:147]\n"
"s_lshl_b32 s91, s19, 9\n"
"v_mov_b32_dpp v252, v252 row_ror:12 row_mask:0x2 bank_mask:0xf\n"
"v_mov_b32_dpp v250, v250 row_ror:4 row_mask:0x8 bank_mask:0xf\n"
"v_mfma_f32_16x16x32_bf16 v[144:147], v[100:103], a[116:119], v[144:147]\n"
"ds_read_b64_tr_b16 v[214:215], v234 offset:18432\n"
"v_mul_f32_dpp v63, v250, v63 row_newbcast:3 row_mask:0xf bank_mask:0xf\n"
"v_mfma_f32_16x16x32_bf16 v[144:147], v[96:99], a[112:115], v[144:147]\n"
"ds_read_b64_tr_b16 v[208:209], v230 offset:16384\n"
"v_mul_f32_dpp v83, v206, v83 row_newbcast:3 row_mask:0xf bank_mask:0xf\n"
"v_mfma_f32_16x16x32_bf16 v[160:163], v[96:99], a[128:131], v[196:199]\n"
"ds_read_b64_tr_b16 v[192:193], v234 offset:8192\n"
"ds_read_b64_tr_b16 v[212:213], v234 offset:16384\n"
"v_mfma_f32_16x16x32_bf16 v[160:163], v[100:103], a[132:135], v[160:163]\n"
"v_accvgpr_read_b32 v251, a211\n"
"v_mul_f32_dpp v57, v250, v57 row_newbcast:1 row_mask:0xf bank_mask:0xf\n"
"v_mfma_f32_16x16x32_bf16 v[160:163], v[104:107], a[136:139], v[160:163]\n"
"ds_read_b64_tr_b16 v[182:183], v230 offset:2048\n"
"v_mul_f32_dpp v58, v250, v58 row_newbcast:2 row_mask:0xf bank_mask:0xf\n"
"v_mfma_f32_16x16x32_bf16 v[160:163], v[108:111], a[140:143], v[160:163]\n"
"ds_read_b64_tr_b16 v[180:181], v230 offset:0\n"
"v_mul_f32_dpp v31, v250, v31 row_newbcast:3 row_mask:0xf bank_mask:0xf\n"
"v_mfma_f32_16x16x32_bf16 v[160:163], v[112:115], a[176:179], v[160:163]\n"
"ds_read_b64_tr_b16 v[210:211], v230 offset:18432\n"
"v_mov_b32_dpp v252, v252 row_ror:8 row_mask:0x4 bank_mask:0xf\n"
"v_mfma_f32_16x16x32_bf16 v[160:163], v[116:119], a[180:183], v[160:163]\n"
"ds_read_b64_tr_b16 v[220:221], v234 offset:24576\n"
"v_mul_f32_dpp v26, v250, v26 row_newbcast:2 row_mask:0xf bank_mask:0xf\n"
"v_mfma_f32_16x16x32_bf16 v[160:163], v[120:123], a[184:187], v[160:163]\n"
"ds_read_b64_tr_b16 a[238:239], v237 offset:10248\n"
"v_mul_f32_dpp v30, v250, v30 row_newbcast:2 row_mask:0xf bank_mask:0xf\n"
"v_mfma_f32_16x16x32_bf16 v[160:163], v[224:227], a[188:191], v[160:163]\n"
"ds_read_b64_tr_b16 v[216:217], v230 offset:24576\n"
"v_mul_f32_dpp v28, v250, v28 row_newbcast:0 row_mask:0xf bank_mask:0xf\n"
"v_mfma_f32_16x16x32_bf16 v[176:179], v[224:227], a[252:255], v[196:199]\n"
"v_mov_b32_dpp v252, v252 row_ror:4 row_mask:0x8 bank_mask:0xf\n"
"v_mul_f32_dpp v61, v250, v61 row_newbcast:1 row_mask:0xf bank_mask:0xf\n"
"v_mfma_f32_16x16x32_bf16 v[176:179], v[120:123], a[248:251], v[176:179]\n"
"ds_read_b64_tr_b16 v[226:227], v235 offset:26624\n"
"v_mul_f32_dpp v59, v250, v59 row_newbcast:3 row_mask:0xf bank_mask:0xf\n"
"v_mfma_f32_16x16x32_bf16 v[176:179], v[116:119], a[244:247], v[176:179]\n"
"v_mul_f32_dpp v25, v250, v25 row_newbcast:1 row_mask:0xf bank_mask:0xf\n"
"v_mul_f32_dpp v29, v250, v29 row_newbcast:1 row_mask:0xf bank_mask:0xf\n"
"v_mfma_f32_16x16x32_bf16 v[176:179], v[112:115], a[240:243], v[176:179]\n"
"ds_read_b64_tr_b16 v[114:115], v231 offset:18432\n"
"v_mul_f32_dpp v19, v246, v19 row_newbcast:3 row_mask:0xf bank_mask:0xf\n"
"v_mfma_f32_16x16x32_bf16 v[176:179], v[108:111], a[204:207], v[176:179]\n"
"ds_read_b64_tr_b16 a[236:237], v237 offset:10240\n"
"v_mul_f32_dpp v27, v250, v27 row_newbcast:3 row_mask:0xf bank_mask:0xf\n"
"v_mfma_f32_16x16x32_bf16 v[176:179], v[104:107], a[200:203], v[176:179]\n"
"v_mul_f32_dpp v24, v250, v24 row_newbcast:0 row_mask:0xf bank_mask:0xf\n"
"v_fmac_f32_dpp v25, v252, v255 row_newbcast:1 row_mask:0xf bank_mask:0xf\n"
"v_mfma_f32_16x16x32_bf16 v[176:179], v[100:103], a[196:199], v[176:179]\n"
"ds_read_b64_tr_b16 a[234:235], v236 offset:10248\n"
"v_fmac_f32_dpp v27, v252, v251 row_newbcast:3 row_mask:0xf bank_mask:0xf\n"
"v_mfma_f32_16x16x32_bf16 v[176:179], v[96:99], a[192:195], v[176:179]\n"
"s_waitcnt lgkmcnt(4)\n"
"ds_read_b64_tr_b16 v[104:105], v231 offset:8192\n"
"ds_read_b64_tr_b16 v[106:107], v231 offset:10240\n"
"v_accvgpr_read_b32 v251, a215\n"
"v_mul_f32_dpp v55, v247, v55 row_newbcast:3 row_mask:0xf bank_mask:0xf\n"
"v_mul_f32_dpp v56, v250, v56 row_newbcast:0 row_mask:0xf bank_mask:0xf\n"
"v_mul_f32_dpp v62, v250, v62 row_newbcast:2 row_mask:0xf bank_mask:0xf\n"
"v_mfma_f32_16x16x32_bf16 v[132:135], v[180:183], a[96:99], v[196:199]\n"
"v_accvgpr_read_b32 v255, a213\n"
"v_fmac_f32_dpp v31, v252, v251 row_newbcast:3 row_mask:0xf bank_mask:0xf\n"
"v_mfma_f32_16x16x32_bf16 v[132:135], v[184:187], a[100:103], v[132:135]\n"
"ds_read_b64_tr_b16 v[108:109], v235 offset:8192\n"
"ds_read_b64_tr_b16 v[116:117], v235 offset:16384\n"
"v_mfma_f32_16x16x32_bf16 v[132:135], v[188:191], a[104:107], v[132:135]\n"
"ds_read_b64_tr_b16 v[102:103], v235 offset:2048\n"
"v_fmac_f32_dpp v26, v252, v253 row_newbcast:2 row_mask:0xf bank_mask:0xf\n"
"v_mfma_f32_16x16x32_bf16 v[132:135], v[192:195], a[108:111], v[132:135]\n"
"v_accvgpr_read_b32 v253, a214\n"
"v_mul_f32_dpp v60, v250, v60 row_newbcast:0 row_mask:0xf bank_mask:0xf\n"
"v_mfma_f32_16x16x32_bf16 v[132:135], v[208:211], a[144:147], v[132:135]\n"
"s_waitcnt vmcnt(0)\n"
"ds_read_b64_tr_b16 v[96:97], v231 offset:0\n"
"v_fmac_f32_dpp v24, v252, v254 row_newbcast:0 row_mask:0xf bank_mask:0xf\n"
"v_mfma_f32_16x16x32_bf16 v[132:135], v[212:215], a[148:151], v[132:135]\n"
"v_subrev_u32 v200, s21, v200\n"
"v_accvgpr_read_b32 v254, a212\n"
"v_mfma_f32_16x16x32_bf16 v[132:135], v[216:219], a[152:155], v[132:135]\n"
"v_cmp_lt_u32 vcc, s69, v200\n"
"v_accvgpr_read_b32 v251, a227\n"
"v_mfma_f32_16x16x32_bf16 v[132:135], v[220:223], a[156:159], v[132:135]\n"
"ds_read_b64_tr_b16 a[216:217], v236 offset:8192\n"
"ds_read_b64_tr_b16 v[118:119], v235 offset:18432\n"
"v_fmac_f32_dpp v28, v252, v254 row_newbcast:0 row_mask:0xf bank_mask:0xf\n"
"v_fmac_f32_dpp v29, v252, v255 row_newbcast:1 row_mask:0xf bank_mask:0xf\n"
"v_fmac_f32_dpp v30, v252, v253 row_newbcast:2 row_mask:0xf bank_mask:0xf\n"
"v_cvt_pk_bf16_f32 v24, v24, v25\n"
"v_cvt_pk_bf16_f32 v25, v26, v27\n"
"v_cvt_pk_bf16_f32 v26, v28, v29\n"
"v_cvt_pk_bf16_f32 v27, v30, v31\n"
"v_accvgpr_read_b32 v255, a225\n"
"v_accvgpr_read_b32 v253, a226\n"
"v_fmac_f32_dpp v59, v252, v251 row_newbcast:3 row_mask:0xf bank_mask:0xf\n"
"s_cbranch_vccnz LGV_%=\nLGVK_%=:\n"
"v_mfma_f32_16x16x32_bf16 v[148:151], v[220:223], a[172:175], v[196:199]\n"
"v_accvgpr_read_b32 v254, a224\n"
"v_fmac_f32_dpp v58, v252, v253 row_newbcast:2 row_mask:0xf bank_mask:0xf\n"
"v_mfma_f32_16x16x32_bf16 v[148:151], v[216:219], a[168:171], v[148:151]\n"
"v_add_u32_dpp v200, v200, v200 quad_perm:[1,0,3,2] row_mask:0xf bank_mask:0xf\n"
"v_accvgpr_read_b32 v253, a230\n"
"v_mfma_f32_16x16x32_bf16 v[148:151], v[212:215], a[164:167], v[148:151]\n"
"v_fmac_f32_dpp v56, v252, v254 row_newbcast:0 row_mask:0xf bank_mask:0xf\n"
"v_accvgpr_read_b32 v251, a231\n"
"v_mfma_f32_16x16x32_bf16 v[148:151], v[208:211], a[160:163], v[148:151]\n"
"v_add_u32_dpp v200, v200, v200 quad_perm:[2,3,0,1] row_mask:0xf bank_mask:0xf\n"
"ds_write_b128 v243, v[24:27] offset:0\n"
"v_fmac_f32_dpp v57, v252, v255 row_newbcast:1 row_mask:0xf bank_mask:0xf\n"
"v_accvgpr_read_b32 v254, a228\n"
"s_nop 1\n"
"v_fmac_f32_dpp v60, v252, v254 row_newbcast:0 row_mask:0xf bank_mask:0xf\n"
"v_fmac_f32_dpp v62, v252, v253 row_newbcast:2 row_mask:0xf bank_mask:0xf\n"
"v_fmac_f32_dpp v63, v252, v251 row_newbcast:3 row_mask:0xf bank_mask:0xf\n"
"v_cvt_pk_bf16_f32 v56, v56, v57\n"
"v_cvt_pk_bf16_f32 v57, v58, v59\n"
"v_cvt_pk_bf16_f32 v59, v62, v63\n"
"v_mfma_f32_16x16x32_bf16 v[148:151], v[192:195], a[124:127], v[148:151]\n"
"ds_read_b64_tr_b16 v[100:101], v235 offset:0\n"
"v_accvgpr_read_b32 v255, a229\n"
"v_mfma_f32_16x16x32_bf16 v[148:151], v[188:191], a[120:123], v[148:151]\n"
"v_cmp_lt_u32 vcc, 1, v200\n"
"v_fmac_f32_dpp v61, v252, v255 row_newbcast:1 row_mask:0xf bank_mask:0xf\n"
"v_mfma_f32_16x16x32_bf16 v[148:151], v[184:187], a[116:119], v[148:151]\n"
"ds_read_b64_tr_b16 v[98:99], v231 offset:2048\n"
"v_cvt_pk_bf16_f32 v58, v60, v61\n"
"v_mfma_f32_16x16x32_bf16 v[148:151], v[180:183], a[112:115], v[148:151]\n"
"ds_write_b128 v243, v[56:59] offset:2048\n"
"v_mul_f32_dpp v22, v246, v22 row_newbcast:2 row_mask:0xf bank_mask:0xf\n"
"v_mul_f32_dpp v48, v247, v48 row_newbcast:0 row_mask:0xf bank_mask:0xf\n"
"v_mul_f32_dpp v49, v247, v49 row_newbcast:1 row_mask:0xf bank_mask:0xf\n"
"v_mul_f32_dpp v87, v206, v87 row_newbcast:3 row_mask:0xf bank_mask:0xf\n"
"v_mfma_f32_16x16x32_bf16 v[164:167], v[180:183], a[128:131], v[196:199]\n"
"s_mov_b32 s47, 0\n"
"ds_read_b64_tr_b16 v[110:111], v235 offset:10240\n"
"v_bfe_u32 v254, v242, 4, 4\n"
"v_mfma_f32_16x16x32_bf16 v[164:167], v[184:187], a[132:135], v[164:167]\n"
"s_mov_b32 s89, vcc_lo\n"
"v_cvt_pk_bf16_f32 v16, v16, v17\n"
"v_mul_f32_dpp v81, v206, v81 row_newbcast:1 row_mask:0xf bank_mask:0xf\n"
"v_mfma_f32_16x16x32_bf16 v[164:167], v[188:191], a[136:139], v[164:167]\n"
"s_lshr_b32 s90, s89, s93\n"
"ds_read_b64_tr_b16 v[112:113], v231 offset:16384\n"
"v_xor_b32 v249, s75, v249\n"
"v_mfma_f32_16x16x32_bf16 v[164:167], v[192:195], a[140:143], v[164:167]\n"
"s_and_b32 s90, s90, 1\n"
"ds_read_b64_tr_b16 v[122:123], v231 offset:26624\n"
"v_mul_f32_dpp v51, v247, v51 row_newbcast:3 row_mask:0xf bank_mask:0xf\n"
"v_mfma_f32_16x16x32_bf16 v[164:167], v[208:211], a[176:179], v[164:167]\n"
"s_lshl_b32 s16, s61, 7\n"
"v_cvt_pk_bf16_f32 v17, v18, v19\n"
"v_cvt_pk_bf16_f32 v19, v22, v23\n"
"v_mfma_f32_16x16x32_bf16 v[164:167], v[212:215], a[180:183], v[164:167]\n"
"s_add_u32 s90, s90, s16\n"
"ds_read_b64_tr_b16 v[120:121], v231 offset:24576\n"
"ds_read_b64_tr_b16 v[224:225], v235 offset:24576\n"
"v_mfma_f32_16x16x32_bf16 v[164:167], v[216:219], a[184:187], v[164:167]\n"
"s_waitcnt vmcnt(63) lgkmcnt(5)\n"
"s_lshl_b32 s90, s90, 13\n"
"v_mul_f32_dpp v20, v246, v20 row_newbcast:0 row_mask:0xf bank_mask:0xf\n"
"v_mfma_f32_16x16x32_bf16 v[164:167], v[220:223], a[188:191], v[164:167]\n"
"s_barrier\n"
"s_add_u32 s16, s20, s90\n"
"v_mul_f32_dpp v50, v247, v50 row_newbcast:2 row_mask:0xf bank_mask:0xf\n"
"v_mul_f32_dpp v52, v247, v52 row_newbcast:0 row_mask:0xf bank_mask:0xf\n"
"v_cvt_pk_bf16_f32 v48, v48, v49\n"
"v_cvt_pk_bf16_f32 v49, v50, v51\n"
"v_mul_f32_dpp v80, v206, v80 row_newbcast:0 row_mask:0xf bank_mask:0xf\n"
"v_mfma_f32_16x16x32_bf16 v[136:139], v[96:99], a[96:99], v[196:199]\n"
"s_add_u32 s17, s16, 4096\n"
"ds_read_b64_tr_b16 v[180:181], v228 offset:32768\n"
"v_mul_f32_dpp v84, v206, v84 row_newbcast:0 row_mask:0xf bank_mask:0xf\n"
"v_mfma_f32_16x16x32_bf16 v[136:139], v[100:103], a[100:103], v[136:139]\n"
"s_mov_b32 s72, s61\n"
"ds_read_b64_tr_b16 v[182:183], v228 offset:34816\n"
"v_mul_f32_dpp v21, v246, v21 row_newbcast:1 row_mask:0xf bank_mask:0xf\n"
"v_mfma_f32_16x16x32_bf16 v[136:139], v[104:107], a[104:107], v[136:139]\n"
"s_waitcnt lgkmcnt(5)\n"
"s_mov_b32 s21, s60\n"
"ds_read_b64_tr_b16 a[208:209], v236 offset:12288\n"
"v_mul_f32_dpp v82, v206, v82 row_newbcast:2 row_mask:0xf bank_mask:0xf\n"
"v_mfma_f32_16x16x32_bf16 v[136:139], v[108:111], a[108:111], v[136:139]\n"
"s_mov_b32 s18, s77\n"
"v_cvt_pk_bf16_f32 v80, v80, v81\n"
"v_cvt_pk_bf16_f32 v81, v82, v83\n"
"v_mfma_f32_16x16x32_bf16 v[136:139], v[112:115], a[144:147], v[136:139]\n"
"s_mov_b32 s19, s77\n"
"v_mul_f32_dpp v85, v206, v85 row_newbcast:1 row_mask:0xf bank_mask:0xf\n"
"v_cvt_pk_bf16_f32 v83, v86, v87\n"
"v_mfma_f32_16x16x32_bf16 v[136:139], v[116:119], a[148:151], v[136:139]\n"
"s_waitcnt lgkmcnt(3)\n"
"s_mov_b32 s69, -1\n"
"ds_read_b64_tr_b16 v[218:219], v228 offset:59392\n"
"v_cvt_pk_bf16_f32 v18, v20, v21\n"
"v_mfma_f32_16x16x32_bf16 v[136:139], v[120:123], a[152:155], v[136:139]\n"
"s_mul_i32 s88, s61, 0x10011111\n"
"v_xor_b32 v248, s75, v248\n"
"v_cvt_pk_bf16_f32 v82, v84, v85\n"
"v_mfma_f32_16x16x32_bf16 v[136:139], v[224:227], a[156:159], v[136:139]\n"
"s_add_u32 s88, s88, 484675\n"
"ds_write_b128 v248, v[16:19] offset:49152\n"
"v_mul_f32_dpp v53, v247, v53 row_newbcast:1 row_mask:0xf bank_mask:0xf\n"
"v_mul_f32_dpp v54, v247, v54 row_newbcast:2 row_mask:0xf bank_mask:0xf\n"
"v_cvt_pk_bf16_f32 v50, v52, v53\n"
"v_cvt_pk_bf16_f32 v51, v54, v55\n"
"v_mfma_f32_16x16x32_bf16 v[152:155], v[224:227], a[172:175], v[196:199]\n"
"s_and_b32 s88, s88, 0x70077777\n"
"ds_read_b64_tr_b16 a[8:9], v249 offset:49152\n"
"ds_read_b64_tr_b16 a[10:11], v249 offset:49160\n"
"v_mfma_f32_16x16x32_bf16 v[152:155], v[120:123], a[168:171], v[152:155]\n"
"s_or_b32 s88, s88, 0x9800000\n"
"ds_write_b128 v248, v[48:51] offset:50176\n"
"ds_write_b128 v248, v[80:83] offset:51200\n"
"v_mfma_f32_16x16x32_bf16 v[152:155], v[116:119], a[164:167], v[152:155]\n"
"s_mov_b32 s84, s6\n"
"ds_read_b64_tr_b16 a[24:25], v249 offset:50176\n"
"ds_read_b64_tr_b16 a[26:27], v249 offset:50184\n"
"ds_read_b64_tr_b16 a[40:41], v249 offset:51200\n"
"ds_read_b64_tr_b16 a[42:43], v249 offset:51208\n"
"v_mfma_f32_16x16x32_bf16 v[152:155], v[112:115], a[160:163], v[152:155]\n"
"s_and_b32 s85, s7, 0xffff\n"
"ds_read_b64_tr_b16 v[188:189], v228 offset:40960\n"
"v_xor_b32 v248, s75, v248\n"
"v_mfma_f32_16x16x32_bf16 v[152:155], v[108:111], a[124:127], v[152:155]\n"
"s_mov_b32 s86, 128\n"
"ds_read_b64_tr_b16 v[184:185], v232 offset:32768\n"
"ds_read_b64_tr_b16 v[214:215], v232 offset:51200\n"
"v_mfma_f32_16x16x32_bf16 v[152:155], v[104:107], a[120:123], v[152:155]\n"
"s_mov_b32 s87, 131072\n"
"ds_read_b64_tr_b16 a[214:215], v237 offset:12296\n"
"ds_read_b64_tr_b16 v[190:191], v228 offset:43008\n"
"v_mfma_f32_16x16x32_bf16 v[152:155], v[100:103], a[116:119], v[152:155]\n"
"ds_read_b64_tr_b16 a[224:225], v236 offset:14336\n"
"ds_read_b64_tr_b16 v[212:213], v232 offset:49152\n"
"v_mfma_f32_16x16x32_bf16 v[152:155], v[96:99], a[112:115], v[152:155]\n"
"ds_read_b64_tr_b16 v[186:187], v232 offset:34816\n"
"v_mov_b64 v[84:85], 0\n"
"v_mfma_f32_16x16x32_bf16 v[168:171], v[96:99], a[128:131], v[196:199]\n"
"ds_read_b64_tr_b16 v[192:193], v232 offset:40960\n"
"ds_read_b64_tr_b16 v[222:223], v232 offset:59392\n"
"v_mfma_f32_16x16x32_bf16 v[168:171], v[100:103], a[132:135], v[168:171]\n"
"ds_read_b64_tr_b16 v[194:195], v232 offset:43008\n"
"v_bfe_u32 v255, v242, 8, 1\n"
"v_mfma_f32_16x16x32_bf16 v[168:171], v[104:107], a[136:139], v[168:171]\n"
"ds_read_b64_tr_b16 v[208:209], v228 offset:49152\n"
"ds_read_b64_tr_b16 v[210:211], v228 offset:51200\n"
"v_mfma_f32_16x16x32_bf16 v[168:171], v[108:111], a[140:143], v[168:171]\n"
"s_waitcnt lgkmcnt(12)\n"
"ds_read_b64_tr_b16 a[230:231], v237 offset:14344\n"
"ds_read_b64_tr_b16 v[216:217], v228 offset:57344\n"
"v_mfma_f32_16x16x32_bf16 v[168:171], v[112:115], a[176:179], v[168:171]\n"
"ds_read_b64_tr_b16 v[220:221], v232 offset:57344\n"
"v_xor_b32 v249, s75, v249\n"
"v_mfma_f32_16x16x32_bf16 v[168:171], v[116:119], a[180:183], v[168:171]\n"
"ds_read_b64_tr_b16 a[222:223], v237 offset:8200\n"
"ds_read_b64_tr_b16 a[226:227], v236 offset:14344\n"
"v_mfma_f32_16x16x32_bf16 v[168:171], v[120:123], a[184:187], v[168:171]\n"
"s_add_u32 s41, s70, 327680\n"
"s_add_u32 m0, s71, 32768\n"
"ds_read_b64_tr_b16 v[110:111], v233 offset:43008\n"
"v_mfma_f32_16x16x32_bf16 v[168:171], v[224:227], a[188:191], v[168:171]\n"
"buffer_load_dwordx4 v240, s[28:31], s41 offen lds\n"
"v_mfma_f32_16x16x32_bf16 v[124:127], v[180:183], a[48:51], v[124:127]\n"
"s_waitcnt lgkmcnt(9)\n"
"v_mul_u32_u24 v254, 2560, v254\n"
"v_mfma_f32_16x16x32_bf16 v[124:127], v[184:187], a[52:55], v[124:127]\n"
"v_mov_b64 v[32:33], s[22:23]\n"
"v_mov_b64 v[44:45], s[22:23]\n"
"v_mfma_f32_16x16x32_bf16 v[124:127], v[188:191], a[56:59], v[124:127]\n"
"s_waitcnt lgkmcnt(6)\n"
"s_add_u32 s40, s41, s52\n"
"ds_read_b64_tr_b16 a[212:213], v237 offset:12288\n"
"ds_read_b64_tr_b16 v[102:103], v233 offset:34816\n"
"v_mfma_f32_16x16x32_bf16 v[124:127], v[192:195], a[60:63], v[124:127]\n"
"buffer_load_dwordx4 v240, s[28:31], s40 offen offset:1024 lds\n"
"v_mfma_f32_16x16x32_bf16 v[124:127], v[208:211], a[0:3], v[124:127]\n"
"v_mov_b64 v[38:39], s[22:23]\n"
"v_mfma_f32_16x16x32_bf16 v[124:127], v[212:215], a[4:7], v[124:127]\n"
"s_waitcnt lgkmcnt(3)\n"
"v_mov_b64 v[244:245], 0\n"
"v_mfma_f32_16x16x32_bf16 v[124:127], v[216:219], a[8:11], v[124:127]\n"
"ds_read_b64_tr_b16 v[98:99], v229 offset:34816\n"
"ds_read_b64_tr_b16 v[116:117], v233 offset:49152\n"
"v_mfma_f32_16x16x32_bf16 v[124:127], v[220:223], a[12:15], v[124:127]\n"
"s_add_u32 s40, s41, s53\n"
"ds_read_b64_tr_b16 v[122:123], v229 offset:59392\n"
"ds_read_b64_tr_b16 v[224:225], v233 offset:57344\n"
"v_mfma_f32_16x16x32_bf16 v[140:143], v[220:223], a[28:31], v[140:143]\n"
"buffer_load_dwordx4 v240, s[28:31], s40 offen offset:2048 lds\n"
"v_mfma_f32_16x16x32_bf16 v[140:143], v[216:219], a[24:27], v[140:143]\n"
"v_mov_b64 v[16:17], s[22:23]\n"
"v_mov_b64 v[36:37], s[22:23]\n"
"v_mfma_f32_16x16x32_bf16 v[140:143], v[212:215], a[20:23], v[140:143]\n"
"v_mov_b64 v[246:247], 0\n"
"v_subrev_f32 v124, s22, v124\n"
"v_mfma_f32_16x16x32_bf16 v[140:143], v[208:211], a[16:19], v[140:143]\n"
"ds_read_b64_tr_b16 a[210:211], v236 offset:12296\n"
"ds_read_b64_tr_b16 v[106:107], v229 offset:43008\n"
"v_mfma_f32_16x16x32_bf16 v[140:143], v[192:195], a[76:79], v[140:143]\n"
"ds_read_b64_tr_b16 v[112:113], v229 offset:49152\n"
"v_xor_b32 v228, s75, v228\n"
"v_mfma_f32_16x16x32_bf16 v[140:143], v[188:191], a[72:75], v[140:143]\n"
"ds_read_b64_tr_b16 v[104:105], v229 offset:40960\n"
"v_mov_b64 v[26:27], s[22:23]\n"
"v_mfma_f32_16x16x32_bf16 v[140:143], v[184:187], a[68:71], v[140:143]\n"
"s_add_u32 s40, s41, s54\n"
"ds_read_b64_tr_b16 a[228:229], v237 offset:14336\n"
"ds_read_b64_tr_b16 v[118:119], v233 offset:51200\n"
"v_mfma_f32_16x16x32_bf16 v[140:143], v[180:183], a[64:67], v[140:143]\n"
"buffer_load_dwordx4 v240, s[28:31], s40 offen offset:3072 lds\n"
"v_mfma_f32_16x16x32_bf16 v[156:159], v[180:183], a[80:83], v[156:159]\n"
"s_add_u32 s41, s70, 491520\n"
"v_mov_b64 v[86:87], 0\n"
"v_mfma_f32_16x16x32_bf16 v[156:159], v[184:187], a[84:87], v[156:159]\n"
"s_add_u32 m0, s71, 49152\n"
"ds_read_b64_tr_b16 v[114:115], v229 offset:51200\n"
"ds_read_b64_tr_b16 v[226:227], v233 offset:59392\n"
"v_mfma_f32_16x16x32_bf16 v[156:159], v[188:191], a[88:91], v[156:159]\n"
"buffer_load_dwordx4 v240, s[28:31], s41 offen lds\n"
"v_mfma_f32_16x16x32_bf16 v[156:159], v[192:195], a[92:95], v[156:159]\n"
"v_and_b32 v243, 0x3f0, v242\n"
"v_subrev_f32 v142, s22, v142\n"
"v_mfma_f32_16x16x32_bf16 v[156:159], v[208:211], a[32:35], v[156:159]\n"
"v_mov_b64 v[6:7], s[22:23]\n"
"v_subrev_f32 v126, s22, v126\n"
"v_mfma_f32_16x16x32_bf16 v[156:159], v[212:215], a[36:39], v[156:159]\n"
"ds_read_b64_tr_b16 a[218:219], v236 offset:8200\n"
"ds_read_b64_tr_b16 v[96:97], v229 offset:32768\n"
"v_mfma_f32_16x16x32_bf16 v[156:159], v[216:219], a[40:43], v[156:159]\n"
"s_add_u32 s40, s41, s52\n"
"ds_read_b64_tr_b16 v[100:101], v233 offset:32768\n"
"ds_read_b64_tr_b16 v[108:109], v233 offset:40960\n"
"v_mfma_f32_16x16x32_bf16 v[156:159], v[220:223], a[44:47], v[156:159]\n"
"s_waitcnt lgkmcnt(6)\n"
"buffer_load_dwordx4 v240, s[28:31], s40 offen offset:1024 lds\n"
"v_mfma_f32_16x16x32_bf16 v[172:175], v[220:223], a[228:231], v[172:175]\n"
"v_subrev_f32 v143, s22, v143\n"
"v_xor_b32 v233, s75, v233\n"
"v_mfma_f32_16x16x32_bf16 v[172:175], v[216:219], a[224:227], v[172:175]\n"
"v_lshl_add_u32 v254, v255, 5, v254\n"
"v_subrev_f32 v125, s22, v125\n"
"v_mfma_f32_16x16x32_bf16 v[172:175], v[212:215], a[212:215], v[172:175]\n"
"s_add_u32 s40, s41, s53\n"
"ds_read_b64_tr_b16 v[120:121], v229 offset:57344\n"
"v_mfma_f32_16x16x32_bf16 v[172:175], v[208:211], a[208:211], v[172:175]\n"
"buffer_load_dwordx4 v240, s[28:31], s40 offen offset:2048 lds\n"
"v_mfma_f32_16x16x32_bf16 v[172:175], v[192:195], a[236:239], v[172:175]\n"
"v_add_u32 v236, s94, v243\n"
"v_mfma_f32_16x16x32_bf16 v[172:175], v[188:191], a[232:235], v[172:175]\n"
"v_bfe_u32 v255, v242, 9, 1\n"
"v_subrev_f32 v141, s22, v141\n"
"v_mfma_f32_16x16x32_bf16 v[172:175], v[184:187], a[220:223], v[172:175]\n"
"s_waitcnt lgkmcnt(1)\n"
"ds_read_b64_tr_b16 v[184:185], v234 offset:32768\n"
"v_mov_b64 v[54:55], s[22:23]\n"
"v_mfma_f32_16x16x32_bf16 v[172:175], v[180:183], a[216:219], v[172:175]\n"
"s_add_u32 s40, s41, s54\n"
"ds_read_b64_tr_b16 v[210:211], v230 offset:51200\n"
"v_mfma_f32_16x16x32_bf16 v[128:131], v[96:99], a[48:51], v[128:131]\n"
"buffer_load_dwordx4 v240, s[28:31], s40 offen offset:3072 lds\n"
"v_mfma_f32_16x16x32_bf16 v[128:131], v[100:103], a[52:55], v[128:131]\n"
"s_lshl_b32 s62, s70, 9\n"
"v_mov_b64 v[30:31], s[22:23]\n"
"v_xor_b32 v232, s75, v232\n"
"v_mfma_f32_16x16x32_bf16 v[128:131], v[104:107], a[56:59], v[128:131]\n"
"s_and_b32 s70, s88, 15\n"
"v_cvt_pk_bf16_f32 v124, v124, v125\n"
"v_mfma_f32_16x16x32_bf16 v[128:131], v[108:111], a[60:63], v[128:131]\n"
"s_lshr_b32 s88, s88, 4\n"
"ds_read_b64_tr_b16 v[218:219], v230 offset:59392\n"
"v_subrev_f32 v127, s22, v127\n"
"v_mfma_f32_16x16x32_bf16 v[128:131], v[112:115], a[0:3], v[128:131]\n"
"s_lshl_b32 s70, s70, 7\n"
"ds_read_b64_tr_b16 v[194:195], v234 offset:43008\n"
"v_bfe_u32 v237, v242, 4, 3\n"
"v_mfma_f32_16x16x32_bf16 v[128:131], v[116:119], a[4:7], v[128:131]\n"
"s_waitcnt lgkmcnt(4)\n"
"s_xor_b32 s71, s71, s75\n"
"v_lshl_add_u32 v243, s44, 2, v243\n"
"v_mov_b64 v[12:13], s[22:23]\n"
"v_mfma_f32_16x16x32_bf16 v[128:131], v[120:123], a[8:11], v[128:131]\n"
"v_lshlrev_b32_e64 v201, 2, s72\n"
"ds_read_b64_tr_b16 v[214:215], v234 offset:51200\n"
"v_cvt_pk_bf16_f32 v125, v126, v127\n"
"v_mfma_f32_16x16x32_bf16 v[128:131], v[224:227], a[12:15], v[128:131]\n"
"global_load_dword v200, v201, s[6:7] sc1\n"
"v_mfma_f32_16x16x32_bf16 v[144:147], v[224:227], a[28:31], v[144:147]\n"
"v_subrev_f32 v157, s22, v157\n"
"v_subrev_f32 v174, s22, v174\n"
"v_mfma_f32_16x16x32_bf16 v[144:147], v[120:123], a[24:27], v[144:147]\n"
"v_mov_b64 v[52:53], s[22:23]\n"
"v_subrev_f32 v156, s22, v156\n"
"v_mfma_f32_16x16x32_bf16 v[144:147], v[116:119], a[20:23], v[144:147]\n"
"ds_read_b64_tr_b16 v[188:189], v230 offset:40960\n"
"v_subrev_f32 v172, s22, v172\n"
"v_mfma_f32_16x16x32_bf16 v[144:147], v[112:115], a[16:19], v[144:147]\n"
"v_subrev_f32 v140, s22, v140\n"
"v_mfma_f32_16x16x32_bf16 v[144:147], v[108:111], a[76:79], v[144:147]\n"
"ds_read_b64_tr_b16 v[180:181], v230 offset:32768\n"
"v_subrev_f32 v130, s22, v130\n"
"v_mfma_f32_16x16x32_bf16 v[144:147], v[104:107], a[72:75], v[144:147]\n"
"v_lshl_add_u32 v254, v255, 4, v254\n"
"v_mov_b64 v[22:23], s[22:23]\n"
"v_mfma_f32_16x16x32_bf16 v[144:147], v[100:103], a[68:71], v[144:147]\n"
"ds_read_b64_tr_b16 v[192:193], v234 offset:40960\n"
"v_subrev_f32 v128, s22, v128\n"
"v_mfma_f32_16x16x32_bf16 v[144:147], v[96:99], a[64:67], v[144:147]\n"
"v_subrev_f32 v129, s22, v129\n"
"v_cvt_pk_bf16_f32 v126, v128, v129\n"
"v_mfma_f32_16x16x32_bf16 v[160:163], v[96:99], a[80:83], v[160:163]\n"
"ds_read_b64_tr_b16 v[186:187], v234 offset:34816\n"
"v_cvt_pk_bf16_f32 v156, v156, v157\n"
"v_mfma_f32_16x16x32_bf16 v[160:163], v[100:103], a[84:87], v[160:163]\n"
"ds_read_b64_tr_b16 v[190:191], v230 offset:43008\n"
"v_subrev_f32 v131, s22, v131\n"
"v_mfma_f32_16x16x32_bf16 v[160:163], v[104:107], a[88:91], v[160:163]\n"
"v_cvt_pk_bf16_f32 v127, v130, v131\n"
"v_subrev_f32 v146, s22, v146\n"
"v_mfma_f32_16x16x32_bf16 v[160:163], v[108:111], a[92:95], v[160:163]\n"
"ds_read_b64_tr_b16 v[220:221], v234 offset:57344\n"
"v_cvt_pk_bf16_f32 v140, v140, v141\n"
"v_mfma_f32_16x16x32_bf16 v[160:163], v[112:115], a[32:35], v[160:163]\n"
"v_lshl_add_u32 v248, v242, 8, s93\n"
"v_subrev_f32 v145, s22, v145\n"
"v_mfma_f32_16x16x32_bf16 v[160:163], v[116:119], a[36:39], v[160:163]\n"
"ds_read_b64_tr_b16 v[182:183], v230 offset:34816\n"
"v_cvt_pk_bf16_f32 v141, v142, v143\n"
"v_mfma_f32_16x16x32_bf16 v[160:163], v[120:123], a[40:43], v[160:163]\n"
"ds_read_b64_tr_b16 v[208:209], v230 offset:49152\n"
"v_subrev_f32 v159, s22, v159\n"
"v_mfma_f32_16x16x32_bf16 v[160:163], v[224:227], a[44:47], v[160:163]\n"
"buffer_store_dwordx4 v[124:127], v243, s[32:35], s16 offen sc1\n"
"v_mov_b64 v[24:25], s[22:23]\n"
"v_mfma_f32_16x16x32_bf16 v[176:179], v[224:227], a[228:231], v[176:179]\n"
"ds_read_b64_tr_b16 v[212:213], v234 offset:49152\n"
"v_mov_b64 v[50:51], s[22:23]\n"
"v_mfma_f32_16x16x32_bf16 v[176:179], v[120:123], a[224:227], v[176:179]\n"
"v_subrev_f32 v158, s22, v158\n"
"v_subrev_f32 v173, s22, v173\n"
"v_mfma_f32_16x16x32_bf16 v[176:179], v[116:119], a[212:215], v[176:179]\n"
"ds_read_b64_tr_b16 v[216:217], v230 offset:57344\n"
"v_mov_b64 v[94:95], 0\n"
"v_mfma_f32_16x16x32_bf16 v[176:179], v[112:115], a[208:211], v[176:179]\n"
"v_cvt_pk_bf16_f32 v172, v172, v173\n"
"v_mfma_f32_16x16x32_bf16 v[176:179], v[108:111], a[236:239], v[176:179]\n"
"ds_read_b64_tr_b16 v[112:113], v231 offset:49152\n"
"v_mov_b64 v[4:5], s[22:23]\n"
"v_mfma_f32_16x16x32_bf16 v[176:179], v[104:107], a[232:235], v[176:179]\n"
"ds_read_b64_tr_b16 v[222:223], v234 offset:59392\n"
"v_subrev_f32 v144, s22, v144\n"
"v_mfma_f32_16x16x32_bf16 v[176:179], v[100:103], a[220:223], v[176:179]\n"
"v_mov_b64 v[40:41], s[22:23]\n"
"v_subrev_f32 v147, s22, v147\n"
"v_mfma_f32_16x16x32_bf16 v[176:179], v[96:99], a[216:219], v[176:179]\n"
"s_waitcnt lgkmcnt(4)\n"
"ds_read_b64_tr_b16 v[224:225], v235 offset:57344\n"
"v_subrev_f32 v175, s22, v175\n"
"v_mfma_f32_16x16x32_bf16 v[132:135], v[180:183], a[48:51], v[132:135]\n"
"ds_read_b64_tr_b16 v[102:103], v235 offset:34816\n"
"v_mov_b32 v241, s60\n"
"v_mfma_f32_16x16x32_bf16 v[132:135], v[184:187], a[52:55], v[132:135]\n"
"ds_read_b64_tr_b16 v[96:97], v231 offset:32768\n"
"v_cvt_pk_bf16_f32 v157, v158, v159\n"
"v_mfma_f32_16x16x32_bf16 v[132:135], v[188:191], a[56:59], v[132:135]\n"
"ds_read_b64_tr_b16 v[114:115], v231 offset:51200\n"
"v_subrev_f32 v160, s22, v160\n"
"v_mfma_f32_16x16x32_bf16 v[132:135], v[192:195], a[60:63], v[132:135]\n"
"v_cvt_pk_bf16_f32 v142, v144, v145\n"
"v_subrev_f32 v161, s22, v161\n"
"v_mfma_f32_16x16x32_bf16 v[132:135], v[208:211], a[0:3], v[132:135]\n"
"s_waitcnt lgkmcnt(5)\n"
"ds_read_b64_tr_b16 v[98:99], v231 offset:34816\n"
"v_mfma_f32_16x16x32_bf16 v[132:135], v[212:215], a[4:7], v[132:135]\n"
"v_cvt_pk_bf16_f32 v143, v146, v147\n"
"v_mfma_f32_16x16x32_bf16 v[132:135], v[216:219], a[8:11], v[132:135]\n"
"s_waitcnt lgkmcnt(5)\n"
"ds_read_b64_tr_b16 v[116:117], v235 offset:49152\n"
"v_mov_b64 v[34:35], s[22:23]\n"
"v_mfma_f32_16x16x32_bf16 v[132:135], v[220:223], a[12:15], v[132:135]\n"
"buffer_store_dwordx4 v[140:143], v243, s[32:35], s16 offen offset:2048 sc1\n"
"ds_read_b64_tr_b16 v[106:107], v231 offset:43008\n"
"v_mfma_f32_16x16x32_bf16 v[148:151], v[220:223], a[28:31], v[148:151]\n"
"v_cvt_pk_bf16_f32 v173, v174, v175\n"
"v_subrev_f32 v176, s22, v176\n"
"v_mfma_f32_16x16x32_bf16 v[148:151], v[216:219], a[24:27], v[148:151]\n"
"ds_read_b64_tr_b16 v[100:101], v235 offset:32768\n"
"v_max_u32 v237, 1, v237\n"
"v_mfma_f32_16x16x32_bf16 v[148:151], v[212:215], a[20:23], v[148:151]\n"
"v_mov_b64 v[14:15], s[22:23]\n"
"v_mov_b64 v[46:47], s[22:23]\n"
"v_mfma_f32_16x16x32_bf16 v[148:151], v[208:211], a[16:19], v[148:151]\n"
"ds_read_b64_tr_b16 v[104:105], v231 offset:40960\n"
"v_mov_b64 v[42:43], s[22:23]\n"
"v_mfma_f32_16x16x32_bf16 v[148:151], v[192:195], a[76:79], v[148:151]\n"
"ds_read_b64_tr_b16 v[120:121], v231 offset:57344\n"
"v_mov_b64 v[2:3], s[22:23]\n"
"v_mfma_f32_16x16x32_bf16 v[148:151], v[188:191], a[72:75], v[148:151]\n"
"v_subrev_f32 v162, s22, v162\n"
"v_subrev_f32 v179, s22, v179\n"
"v_mfma_f32_16x16x32_bf16 v[148:151], v[184:187], a[68:71], v[148:151]\n"
"ds_read_b64_tr_b16 v[122:123], v231 offset:59392\n"
"v_xor_b32 v229, s75, v229\n"
"v_mfma_f32_16x16x32_bf16 v[148:151], v[180:183], a[64:67], v[148:151]\n"
"ds_read_b64_tr_b16 v[110:111], v235 offset:43008\n"
"v_add_u32 v238, s97, v254\n"
"v_mfma_f32_16x16x32_bf16 v[164:167], v[180:183], a[80:83], v[164:167]\n"
"v_add_u32 v252, s98, v254\n"
"v_add_u32 v241, 1, v241\n"
"v_mfma_f32_16x16x32_bf16 v[164:167], v[184:187], a[84:87], v[164:167]\n"
"ds_read_b64_tr_b16 v[108:109], v235 offset:40960\n"
"v_mov_b64 v[10:11], s[22:23]\n"
"v_mfma_f32_16x16x32_bf16 v[164:167], v[188:191], a[88:91], v[164:167]\n"
"v_mov_b64 v[28:29], s[22:23]\n"
"v_subrev_f32 v177, s22, v177\n"
"v_mfma_f32_16x16x32_bf16 v[164:167], v[192:195], a[92:95], v[164:167]\n"
"ds_read_b64_tr_b16 v[118:119], v235 offset:51200\n"
"v_subrev_f32 v132, s22, v132\n"
"v_mfma_f32_16x16x32_bf16 v[164:167], v[208:211], a[32:35], v[164:167]\n"
"v_subrev_f32 v163, s22, v163\n"
"v_subrev_f32 v135, s22, v135\n"
"v_mfma_f32_16x16x32_bf16 v[164:167], v[212:215], a[36:39], v[164:167]\n"
"ds_read_b64_tr_b16 v[226:227], v235 offset:59392\n"
"v_cvt_pk_bf16_f32 v159, v162, v163\n"
"v_mfma_f32_16x16x32_bf16 v[164:167], v[216:219], a[40:43], v[164:167]\n"
"v_xor_b32 v237, s61, v237\n"
"v_mov_b64 v[18:19], s[22:23]\n"
"v_mfma_f32_16x16x32_bf16 v[164:167], v[220:223], a[44:47], v[164:167]\n"
"s_barrier\n"
"s_waitcnt lgkmcnt(3)\n"
"v_mfma_f32_16x16x32_bf16 v[136:139], v[96:99], a[48:51], v[136:139]\n"
"ds_read_b64_tr_b16 v[214:215], v232 offset:18432\n"
"v_mfma_f32_16x16x32_bf16 v[136:139], v[100:103], a[52:55], v[136:139]\n"
"ds_read_b64_tr_b16 v[212:213], v232 offset:16384\n"
"v_cvt_pk_bf16_f32 v158, v160, v161\n"
"v_mfma_f32_16x16x32_bf16 v[136:139], v[104:107], a[56:59], v[136:139]\n"
"s_waitcnt lgkmcnt(4)\n"
"ds_read_b64_tr_b16 v[220:221], v232 offset:24576\n"
"v_subrev_f32 v178, s22, v178\n"
"v_mfma_f32_16x16x32_bf16 v[136:139], v[108:111], a[60:63], v[136:139]\n"
"ds_read_b64_tr_b16 v[180:181], v228 offset:0\n"
"v_mfma_f32_16x16x32_bf16 v[136:139], v[112:115], a[0:3], v[136:139]\n"
"s_waitcnt lgkmcnt(5)\n"
"ds_read_b64_tr_b16 v[194:195], v232 offset:10240\n"
"v_mfma_f32_16x16x32_bf16 v[136:139], v[116:119], a[4:7], v[136:139]\n"
"ds_read_b64_tr_b16 v[188:189], v228 offset:8192\n"
"v_add_u32 v253, s99, v254\n"
"v_mfma_f32_16x16x32_bf16 v[136:139], v[120:123], a[8:11], v[136:139]\n"
"s_waitcnt lgkmcnt(6)\n"
"ds_read_b64_tr_b16 v[184:185], v232 offset:0\n"
"v_subrev_f32 v133, s22, v133\n"
"v_mfma_f32_16x16x32_bf16 v[136:139], v[224:227], a[12:15], v[136:139]\n"
"buffer_store_dwordx4 v[156:159], v243, s[32:35], s17 offen sc1\n"
"ds_read_b64_tr_b16 v[222:223], v232 offset:26624\n"
"v_mfma_f32_16x16x32_bf16 v[152:155], v[224:227], a[28:31], v[152:155]\n"
"ds_read_b64_tr_b16 v[208:209], v228 offset:16384\n"
"v_mfma_f32_16x16x32_bf16 v[152:155], v[120:123], a[24:27], v[152:155]\n"
"ds_read_b64_tr_b16 v[190:191], v228 offset:10240\n"
"v_mfma_f32_16x16x32_bf16 v[152:155], v[116:119], a[20:23], v[152:155]\n"
"ds_read_b64_tr_b16 v[186:187], v232 offset:2048\n"
"v_subrev_f32 v134, s22, v134\n"
"v_mfma_f32_16x16x32_bf16 v[152:155], v[112:115], a[16:19], v[152:155]\n"
"ds_read_b64_tr_b16 v[218:219], v228 offset:26624\n"
"v_mfma_f32_16x16x32_bf16 v[152:155], v[108:111], a[76:79], v[152:155]\n"
"ds_read_b64_tr_b16 v[192:193], v232 offset:8192\n"
"v_mfma_f32_16x16x32_bf16 v[152:155], v[104:107], a[72:75], v[152:155]\n"
"ds_read_b64_tr_b16 v[182:183], v228 offset:2048\n"
"v_xor_b32 v239, 64, v238\n"
"v_mfma_f32_16x16x32_bf16 v[152:155], v[100:103], a[68:71], v[152:155]\n"
"ds_read_b64_tr_b16 v[210:211], v228 offset:18432\n"
"ds_read_b64_tr_b16 v[216:217], v228 offset:24576\n"
"v_mfma_f32_16x16x32_bf16 v[152:155], v[96:99], a[64:67], v[152:155]\n"
"v_mfma_f32_16x16x32_bf16 v[168:171], v[96:99], a[80:83], v[168:171]\n"
"ds_read_b64_tr_b16 v[96:97], v229 offset:0\n"
"ds_read_b64_tr_b16 v[98:99], v229 offset:2048\n"
"v_mfma_f32_16x16x32_bf16 v[168:171], v[100:103], a[84:87], v[168:171]\n"
"ds_read_b64_tr_b16 v[100:101], v233 offset:0\n"
"ds_read_b64_tr_b16 v[102:103], v233 offset:2048\n"
"v_mfma_f32_16x16x32_bf16 v[168:171], v[104:107], a[88:91], v[168:171]\n"
"ds_read_b64_tr_b16 v[104:105], v229 offset:8192\n"
"v_cvt_pk_bf16_f32 v175, v178, v179\n"
"v_mfma_f32_16x16x32_bf16 v[168:171], v[108:111], a[92:95], v[168:171]\n"
"v_mov_b64 v[20:21], s[22:23]\n"
"v_mov_b64 v[90:91], 0\n"
"v_mfma_f32_16x16x32_bf16 v[168:171], v[112:115], a[32:35], v[168:171]\n"
"v_mov_b64 v[88:89], 0\n"
"v_mov_b64 v[92:93], 0\n"
"v_mfma_f32_16x16x32_bf16 v[168:171], v[116:119], a[36:39], v[168:171]\n"
"v_mov_b64 v[48:49], s[22:23]\n"
"v_cvt_pk_bf16_f32 v132, v132, v133\n"
"v_mfma_f32_16x16x32_bf16 v[168:171], v[120:123], a[40:43], v[168:171]\n"
"s_add_u32 s47, s47, 1\n"
"v_mov_b64 v[0:1], s[22:23]\n"
"v_mov_b64 v[8:9], s[22:23]\n"
"v_mfma_f32_16x16x32_bf16 v[168:171], v[224:227], a[44:47], v[168:171]\n"
"s_waitcnt lgkmcnt(5)\n"
"v_lshlrev_b32 v237, 2, v237\n"
"v_cvt_pk_bf16_f32 v174, v176, v177\n"
"LGL_%=:\n"
"v_mfma_f32_16x16x32_bf16 v[124:127], v[180:183], a[96:99], v[196:199]\n"
"ds_read_b64_tr_b16 v[106:107], v229 offset:10240\n"
"v_cvt_pk_bf16_f32 v133, v134, v135\n"
"v_mfma_f32_16x16x32_bf16 v[124:127], v[184:187], a[100:103], v[124:127]\n"
"ds_read_b64_tr_b16 v[108:109], v233 offset:8192\n"
"v_subrev_f32 v136, s22, v136\n"
"v_mfma_f32_16x16x32_bf16 v[124:127], v[188:191], a[104:107], v[124:127]\n"
"ds_read_b64_tr_b16 v[110:111], v233 offset:10240\n"
"v_subrev_f32 v137, s22, v137\n"
"v_mfma_f32_16x16x32_bf16 v[124:127], v[192:195], a[108:111], v[124:127]\n"
"ds_read_b64_tr_b16 v[112:113], v229 offset:16384\n"
"v_subrev_f32 v138, s22, v138\n"
"v_mfma_f32_16x16x32_bf16 v[124:127], v[208:211], a[144:147], v[124:127]\n"
"ds_read_b64_tr_b16 v[114:115], v229 offset:18432\n"
"v_subrev_f32 v139, s22, v139\n"
"v_mfma_f32_16x16x32_bf16 v[124:127], v[212:215], a[148:151], v[124:127]\n"
"s_waitcnt lgkmcnt(7)\n"
"ds_read_b64_tr_b16 v[116:117], v233 offset:16384\n"
"v_cvt_pk_bf16_f32 v134, v136, v137\n"
"v_mfma_f32_16x16x32_bf16 v[124:127], v[216:219], a[152:155], v[124:127]\n"
"ds_read_b64_tr_b16 v[118:119], v233 offset:18432\n"
"v_cvt_pk_bf16_f32 v135, v138, v139\n"
"v_mfma_f32_16x16x32_bf16 v[124:127], v[220:223], a[156:159], v[124:127]\n"
"buffer_store_dwordx4 v[172:175], v243, s[32:35], s17 offen offset:2048 sc1\n"
"v_subrev_f32 v148, s22, v148\n"
"v_mfma_f32_16x16x32_bf16 v[140:143], v[220:223], a[172:175], v[196:199]\n"
"ds_read_b64_tr_b16 v[120:121], v229 offset:24576\n"
"v_subrev_f32 v149, s22, v149\n"
"v_mfma_f32_16x16x32_bf16 v[140:143], v[216:219], a[168:171], v[140:143]\n"
"ds_read_b64_tr_b16 v[122:123], v229 offset:26624\n"
"v_subrev_f32 v150, s22, v150\n"
"v_mfma_f32_16x16x32_bf16 v[140:143], v[212:215], a[164:167], v[140:143]\n"
"ds_read_b64_tr_b16 v[224:225], v233 offset:24576\n"
"v_subrev_f32 v151, s22, v151\n"
"v_mfma_f32_16x16x32_bf16 v[140:143], v[208:211], a[160:163], v[140:143]\n"
"ds_read_b64_tr_b16 v[226:227], v233 offset:26624\n"
"v_cvt_pk_bf16_f32 v148, v148, v149\n"
"v_mfma_f32_16x16x32_bf16 v[140:143], v[192:195], a[124:127], v[140:143]\n"
"v_cvt_pk_bf16_f32 v149, v150, v151\n"
"v_subrev_f32 v152, s22, v152\n"
"v_mfma_f32_16x16x32_bf16 v[140:143], v[188:191], a[120:123], v[140:143]\n"
"s_add_u32 s41, s70, 0\n"
"v_subrev_f32 v153, s22, v153\n"
"v_subrev_f32 v154, s22, v154\n"
"v_mfma_f32_16x16x32_bf16 v[140:143], v[184:187], a[116:119], v[140:143]\n"
"s_mov_b32 m0, s71\n"
"v_subrev_f32 v155, s22, v155\n"
"v_cvt_pk_bf16_f32 v150, v152, v153\n"
"v_mfma_f32_16x16x32_bf16 v[140:143], v[180:183], a[112:115], v[140:143]\n"
"s_waitcnt vmcnt(14)\n"
"buffer_load_dwordx4 v240, s[28:31], s41 offen lds\n"
"v_mfma_f32_16x16x32_bf16 v[156:159], v[180:183], a[128:131], v[196:199]\n"
"v_xor_b32 v230, s75, v230\n"
"v_cvt_pk_bf16_f32 v151, v154, v155\n"
"v_mfma_f32_16x16x32_bf16 v[156:159], v[184:187], a[132:135], v[156:159]\n"
"v_xor_b32 v234, s75, v234\n"
"v_subrev_f32 v164, s22, v164\n"
"v_mfma_f32_16x16x32_bf16 v[156:159], v[188:191], a[136:139], v[156:159]\n"
"v_subrev_f32 v165, s22, v165\n"
"v_subrev_f32 v166, s22, v166\n"
"v_mfma_f32_16x16x32_bf16 v[156:159], v[192:195], a[140:143], v[156:159]\n"
"v_subrev_f32 v167, s22, v167\n"
"v_cvt_pk_bf16_f32 v164, v164, v165\n"
"v_mfma_f32_16x16x32_bf16 v[156:159], v[208:211], a[176:179], v[156:159]\n"
"v_cvt_pk_bf16_f32 v165, v166, v167\n"
"v_subrev_f32 v168, s22, v168\n"
"v_mfma_f32_16x16x32_bf16 v[156:159], v[212:215], a[180:183], v[156:159]\n"
"v_subrev_f32 v169, s22, v169\n"
"v_subrev_f32 v170, s22, v170\n"
"v_mfma_f32_16x16x32_bf16 v[156:159], v[216:219], a[184:187], v[156:159]\n"
"v_subrev_f32 v171, s22, v171\n"
"v_cvt_pk_bf16_f32 v166, v168, v169\n"
"v_mfma_f32_16x16x32_bf16 v[156:159], v[220:223], a[188:191], v[156:159]\n"
"buffer_store_dwordx4 v[132:135], v243, s[32:35], s16 offen offset:1024 sc1\n"
"v_cvt_pk_bf16_f32 v167, v170, v171\n"
"v_mfma_f32_16x16x32_bf16 v[172:175], v[220:223], a[252:255], v[196:199]\n"
"ds_read_b64_tr_b16 v[220:221], v234 offset:24576\n"
"v_xor_b32 v231, s75, v231\n"
"v_mfma_f32_16x16x32_bf16 v[172:175], v[216:219], a[248:251], v[172:175]\n"
"ds_read_b64_tr_b16 v[216:217], v230 offset:24576\n"
"v_xor_b32 v235, s75, v235\n"
"v_mfma_f32_16x16x32_bf16 v[172:175], v[212:215], a[244:247], v[172:175]\n"
"ds_read_b64_tr_b16 v[212:213], v234 offset:16384\n"
"v_lshlrev_b32 v202, 16, v84\n"
"v_mfma_f32_16x16x32_bf16 v[172:175], v[208:211], a[240:243], v[172:175]\n"
"ds_read_b64_tr_b16 v[208:209], v230 offset:16384\n"
"v_and_b32 v84, s73, v84\n"
"v_mfma_f32_16x16x32_bf16 v[172:175], v[192:195], a[204:207], v[172:175]\n"
"ds_read_b64_tr_b16 v[192:193], v234 offset:8192\n"
"v_add_f32 v25, v25, v84\n"
"v_mfma_f32_16x16x32_bf16 v[172:175], v[188:191], a[200:203], v[172:175]\n"
"ds_read_b64_tr_b16 v[188:189], v230 offset:8192\n"
"v_lshlrev_b32 v203, 16, v85\n"
"v_mfma_f32_16x16x32_bf16 v[172:175], v[184:187], a[196:199], v[172:175]\n"
"s_add_u32 s40, s41, s52\n"
"ds_read_b64_tr_b16 v[184:185], v234 offset:0\n"
"v_and_b32 v85, s73, v85\n"
"v_mfma_f32_16x16x32_bf16 v[172:175], v[180:183], a[192:195], v[172:175]\n"
"s_waitcnt vmcnt(13) lgkmcnt(7)\n"
"buffer_load_dwordx4 v240, s[28:31], s40 offen offset:1024 lds\n"
"v_mfma_f32_16x16x32_bf16 v[128:131], v[96:99], a[96:99], v[196:199]\n"
"s_waitcnt lgkmcnt(6)\n"
"ds_read_b64_tr_b16 v[180:181], v230 offset:0\n"
"v_add_f32 v27, v27, v85\n"
"v_mfma_f32_16x16x32_bf16 v[128:131], v[100:103], a[100:103], v[128:131]\n"
"ds_read_b64_tr_b16 v[182:183], v230 offset:2048\n"
"v_lshlrev_b32 v204, 16, v86\n"
"v_mfma_f32_16x16x32_bf16 v[128:131], v[104:107], a[104:107], v[128:131]\n"
"ds_read_b64_tr_b16 v[186:187], v234 offset:2048\n"
"v_and_b32 v86, s73, v86\n"
"v_mfma_f32_16x16x32_bf16 v[128:131], v[108:111], a[108:111], v[128:131]\n"
"ds_read_b64_tr_b16 v[190:191], v230 offset:10240\n"
"v_add_f32 v29, v29, v86\n"
"v_mfma_f32_16x16x32_bf16 v[128:131], v[112:115], a[144:147], v[128:131]\n"
"s_waitcnt vmcnt(6)\n"
"ds_read_b64_tr_b16 v[194:195], v234 offset:10240\n"
"v_lshlrev_b32 v205, 16, v87\n"
"v_mfma_f32_16x16x32_bf16 v[128:131], v[116:119], a[148:151], v[128:131]\n"
"v_subrev_u32 v200, s21, v200\n"
"ds_read_b64_tr_b16 v[210:211], v230 offset:18432\n"
"v_mfma_f32_16x16x32_bf16 v[128:131], v[120:123], a[152:155], v[128:131]\n"
"v_cmp_lt_u32 vcc, s69, v200\n"
"ds_read_b64_tr_b16 v[214:215], v234 offset:18432\n"
"v_mfma_f32_16x16x32_bf16 v[128:131], v[224:227], a[156:159], v[128:131]\n"
"ds_read_b64_tr_b16 v[218:219], v230 offset:26624\n"
"s_cbranch_vccnz LGP_%=\nLGPK_%=:\n"
"v_mfma_f32_16x16x32_bf16 v[144:147], v[224:227], a[172:175], v[196:199]\n"
"s_sub_u32 s72, s72, 1\n"
"ds_read_b64_tr_b16 v[222:223], v234 offset:26624\n"
"v_and_b32 v87, s73, v87\n"
"v_mfma_f32_16x16x32_bf16 v[144:147], v[120:123], a[168:171], v[144:147]\n"
"s_and_b32 s72, s72, 7\n"
"v_add_f32 v24, v24, v202\n"
"v_add_f32 v31, v31, v87\n"
"v_mfma_f32_16x16x32_bf16 v[144:147], v[116:119], a[164:167], v[144:147]\n"
"v_add_f32 v26, v26, v203\n"
"v_lshlrev_b32 v206, 16, v88\n"
"v_mfma_f32_16x16x32_bf16 v[144:147], v[112:115], a[160:163], v[144:147]\n"
"buffer_load_dwordx4 v[72:75], v243, s[32:35], s18 offen sc1\n"
"v_mfma_f32_16x16x32_bf16 v[144:147], v[108:111], a[124:127], v[144:147]\n"
"v_and_b32 v88, s73, v88\n"
"v_add_f32 v33, v33, v88\n"
"v_mfma_f32_16x16x32_bf16 v[144:147], v[104:107], a[120:123], v[144:147]\n"
"v_lshlrev_b32 v207, 16, v89\n"
"v_and_b32 v89, s73, v89\n"
"v_mfma_f32_16x16x32_bf16 v[144:147], v[100:103], a[116:119], v[144:147]\n"
"v_add_f32 v35, v35, v89\n"
"v_lshlrev_b32 v202, 16, v90\n"
"v_mfma_f32_16x16x32_bf16 v[144:147], v[96:99], a[112:115], v[144:147]\n"
"buffer_store_dwordx4 v[148:151], v243, s[32:35], s16 offen offset:3072 sc1\n"
"v_and_b32 v90, s73, v90\n"
"v_mfma_f32_16x16x32_bf16 v[160:163], v[96:99], a[128:131], v[196:199]\n"
"v_add_f32 v37, v37, v90\n"
"v_lshlrev_b32 v203, 16, v91\n"
"v_mfma_f32_16x16x32_bf16 v[160:163], v[100:103], a[132:135], v[160:163]\n"
"v_and_b32 v91, s73, v91\n"
"v_add_f32 v39, v39, v91\n"
"v_mfma_f32_16x16x32_bf16 v[160:163], v[104:107], a[136:139], v[160:163]\n"
"v_add_f32 v28, v28, v204\n"
"v_add_f32 v30, v30, v205\n"
"v_mfma_f32_16x16x32_bf16 v[160:163], v[108:111], a[140:143], v[160:163]\n"
"v_add_f32 v32, v32, v206\n"
"v_add_f32 v34, v34, v207\n"
"v_mfma_f32_16x16x32_bf16 v[160:163], v[112:115], a[176:179], v[160:163]\n"
"v_lshlrev_b32 v204, 16, v92\n"
"v_and_b32 v92, s73, v92\n"
"v_mfma_f32_16x16x32_bf16 v[160:163], v[116:119], a[180:183], v[160:163]\n"
"v_add_f32 v41, v41, v92\n"
"v_lshlrev_b32 v205, 16, v93\n"
"v_mfma_f32_16x16x32_bf16 v[160:163], v[120:123], a[184:187], v[160:163]\n"
"s_add_u32 s40, s41, s53\n"
"v_and_b32 v93, s73, v93\n"
"v_add_f32 v43, v43, v93\n"
"v_mfma_f32_16x16x32_bf16 v[160:163], v[224:227], a[188:191], v[160:163]\n"
"buffer_load_dwordx4 v240, s[28:31], s40 offen offset:2048 lds\n"
"v_mfma_f32_16x16x32_bf16 v[176:179], v[224:227], a[252:255], v[196:199]\n"
"ds_read_b64_tr_b16 v[224:225], v235 offset:24576\n"
"v_lshlrev_b32 v206, 16, v94\n"
"v_mfma_f32_16x16x32_bf16 v[176:179], v[120:123], a[248:251], v[176:179]\n"
"ds_read_b64_tr_b16 v[120:121], v231 offset:24576\n"
"v_and_b32 v94, s73, v94\n"
"v_mfma_f32_16x16x32_bf16 v[176:179], v[116:119], a[244:247], v[176:179]\n"
"ds_read_b64_tr_b16 v[116:117], v235 offset:16384\n"
"v_add_f32 v45, v45, v94\n"
"v_mfma_f32_16x16x32_bf16 v[176:179], v[112:115], a[240:243], v[176:179]\n"
"buffer_load_dwordx4 v[76:79], v243, s[32:35], s18 offen offset:1024 sc1\n"
"v_mfma_f32_16x16x32_bf16 v[176:179], v[108:111], a[204:207], v[176:179]\n"
"ds_read_b64_tr_b16 v[108:109], v235 offset:8192\n"
"v_lshlrev_b32 v207, 16, v95\n"
"v_mfma_f32_16x16x32_bf16 v[176:179], v[104:107], a[200:203], v[176:179]\n"
"ds_read_b64_tr_b16 v[104:105], v231 offset:8192\n"
"v_and_b32 v95, s73, v95\n"
"v_mfma_f32_16x16x32_bf16 v[176:179], v[100:103], a[196:199], v[176:179]\n"
"s_add_u32 s40, s41, s54\n"
"ds_read_b64_tr_b16 v[100:101], v235 offset:0\n"
"v_add_f32 v47, v47, v95\n"
"v_mfma_f32_16x16x32_bf16 v[176:179], v[96:99], a[192:195], v[176:179]\n"
"s_waitcnt lgkmcnt(6)\n"
"buffer_load_dwordx4 v240, s[28:31], s40 offen offset:3072 lds\n"
"v_mfma_f32_16x16x32_bf16 v[132:135], v[180:183], a[96:99], v[196:199]\n"
"ds_read_b64_tr_b16 v[96:97], v231 offset:0\n"
"v_add_f32 v36, v36, v202\n"
"v_mfma_f32_16x16x32_bf16 v[132:135], v[184:187], a[100:103], v[132:135]\n"
"ds_read_b64_tr_b16 v[98:99], v231 offset:2048\n"
"v_add_f32 v38, v38, v203\n"
"v_mfma_f32_16x16x32_bf16 v[132:135], v[188:191], a[104:107], v[132:135]\n"
"ds_read_b64_tr_b16 v[102:103], v235 offset:2048\n"
"v_add_f32 v44, v44, v206\n"
"v_mfma_f32_16x16x32_bf16 v[132:135], v[192:195], a[108:111], v[132:135]\n"
"ds_read_b64_tr_b16 v[106:107], v231 offset:10240\n"
"v_add_f32 v46, v46, v207\n"
"v_mfma_f32_16x16x32_bf16 v[132:135], v[208:211], a[144:147], v[132:135]\n"
"ds_read_b64_tr_b16 v[110:111], v235 offset:10240\n"
"v_add_f32 v40, v40, v204\n"
"v_mfma_f32_16x16x32_bf16 v[132:135], v[212:215], a[148:151], v[132:135]\n"
"ds_read_b64_tr_b16 v[112:113], v231 offset:16384\n"
"v_add_f32 v42, v42, v205\n"
"v_mfma_f32_16x16x32_bf16 v[132:135], v[216:219], a[152:155], v[132:135]\n"
"ds_read_b64_tr_b16 v[114:115], v231 offset:18432\n"
"v_mfma_f32_16x16x32_bf16 v[132:135], v[220:223], a[156:159], v[132:135]\n"
"buffer_store_dwordx4 v[164:167], v243, s[32:35], s17 offen offset:1024 sc1\n"
"v_lshlrev_b32 v206, 16, v244\n"
"v_mfma_f32_16x16x32_bf16 v[148:151], v[220:223], a[172:175], v[196:199]\n"
"ds_read_b64_tr_b16 v[118:119], v235 offset:18432\n"
"v_and_b32 v244, s73, v244\n"
"v_mfma_f32_16x16x32_bf16 v[148:151], v[216:219], a[168:171], v[148:151]\n"
"ds_read_b64_tr_b16 v[122:123], v231 offset:26624\n"
"v_add_f32 v49, v49, v244\n"
"v_mfma_f32_16x16x32_bf16 v[148:151], v[212:215], a[164:167], v[148:151]\n"
"ds_read_b64_tr_b16 v[226:227], v235 offset:26624\n"
"v_lshlrev_b32 v207, 16, v245\n"
"v_mfma_f32_16x16x32_bf16 v[148:151], v[208:211], a[160:163], v[148:151]\n"
"buffer_load_dwordx4 v[80:83], v243, s[32:35], s18 offen offset:2048 sc1\n"
"v_mfma_f32_16x16x32_bf16 v[148:151], v[192:195], a[124:127], v[148:151]\n"
"v_and_b32 v245, s73, v245\n"
"v_add_f32 v51, v51, v245\n"
"v_mfma_f32_16x16x32_bf16 v[148:151], v[188:191], a[120:123], v[148:151]\n"
"s_add_u32 s41, s70, 163840\n"
"v_lshlrev_b32 v202, 16, v246\n"
"v_and_b32 v246, s73, v246\n"
"v_mfma_f32_16x16x32_bf16 v[148:151], v[184:187], a[116:119], v[148:151]\n"
"s_add_u32 m0, s71, 16384\n"
"v_add_f32 v53, v53, v246\n"
"v_lshlrev_b32 v203, 16, v247\n"
"v_mfma_f32_16x16x32_bf16 v[148:151], v[180:183], a[112:115], v[148:151]\n"
"buffer_load_dwordx4 v240, s[28:31], s41 offen lds\n"
"v_mfma_f32_16x16x32_bf16 v[164:167], v[180:183], a[128:131], v[196:199]\n"
"v_and_b32 v247, s73, v247\n"
"v_add_f32 v55, v55, v247\n"
"v_mfma_f32_16x16x32_bf16 v[164:167], v[184:187], a[132:135], v[164:167]\n"
"v_add_f32 v48, v48, v206\n"
"v_add_f32 v50, v50, v207\n"
"v_mfma_f32_16x16x32_bf16 v[164:167], v[188:191], a[136:139], v[164:167]\n"
"v_add_f32 v52, v52, v202\n"
"v_add_f32 v54, v54, v203\n"
"v_mfma_f32_16x16x32_bf16 v[164:167], v[192:195], a[140:143], v[164:167]\n"
"v_mfma_f32_16x16x32_bf16 v[164:167], v[208:211], a[176:179], v[164:167]\n"
"v_mfma_f32_16x16x32_bf16 v[164:167], v[212:215], a[180:183], v[164:167]\n"
"v_mfma_f32_16x16x32_bf16 v[164:167], v[216:219], a[184:187], v[164:167]\n"
"s_waitcnt vmcnt(12)\n"
"v_mfma_f32_16x16x32_bf16 v[164:167], v[220:223], a[188:191], v[164:167]\n"
"s_barrier\n"
"s_waitcnt lgkmcnt(0)\n"
"v_mfma_f32_16x16x32_bf16 v[136:139], v[96:99], a[96:99], v[196:199]\n"
"ds_read_b64_tr_b16 v[180:181], v228 offset:32768\n"
"v_mfma_f32_16x16x32_bf16 v[136:139], v[100:103], a[100:103], v[136:139]\n"
"ds_read_b64_tr_b16 v[182:183], v228 offset:34816\n"
"v_mfma_f32_16x16x32_bf16 v[136:139], v[104:107], a[104:107], v[136:139]\n"
"ds_read_b64_tr_b16 v[184:185], v232 offset:32768\n"
"v_mfma_f32_16x16x32_bf16 v[136:139], v[108:111], a[108:111], v[136:139]\n"
"ds_read_b64_tr_b16 v[186:187], v232 offset:34816\n"
"v_mfma_f32_16x16x32_bf16 v[136:139], v[112:115], a[144:147], v[136:139]\n"
"ds_read_b64_tr_b16 v[188:189], v228 offset:40960\n"
"v_mfma_f32_16x16x32_bf16 v[136:139], v[116:119], a[148:151], v[136:139]\n"
"ds_read_b64_tr_b16 v[190:191], v228 offset:43008\n"
"v_mfma_f32_16x16x32_bf16 v[136:139], v[120:123], a[152:155], v[136:139]\n"
"s_add_u32 s40, s41, s52\n"
"ds_read_b64_tr_b16 v[192:193], v232 offset:40960\n"
"v_mfma_f32_16x16x32_bf16 v[136:139], v[224:227], a[156:159], v[136:139]\n"
"buffer_load_dwordx4 v240, s[28:31], s40 offen offset:1024 lds\n"
"v_mfma_f32_16x16x32_bf16 v[152:155], v[224:227], a[172:175], v[196:199]\n"
"ds_read_b64_tr_b16 v[194:195], v232 offset:43008\n"
"v_mfma_f32_16x16x32_bf16 v[152:155], v[120:123], a[168:171], v[152:155]\n"
"ds_read_b64_tr_b16 v[208:209], v228 offset:49152\n"
"v_mfma_f32_16x16x32_bf16 v[152:155], v[116:119], a[164:167], v[152:155]\n"
"ds_read_b64_tr_b16 v[210:211], v228 offset:51200\n"
"v_mfma_f32_16x16x32_bf16 v[152:155], v[112:115], a[160:163], v[152:155]\n"
"ds_read_b64_tr_b16 v[212:213], v232 offset:49152\n"
"v_mfma_f32_16x16x32_bf16 v[152:155], v[108:111], a[124:127], v[152:155]\n"
"ds_read_b64_tr_b16 v[214:215], v232 offset:51200\n"
"v_mfma_f32_16x16x32_bf16 v[152:155], v[104:107], a[120:123], v[152:155]\n"
"ds_read_b64_tr_b16 v[216:217], v228 offset:57344\n"
"v_mfma_f32_16x16x32_bf16 v[152:155], v[100:103], a[116:119], v[152:155]\n"
"s_add_u32 s40, s41, s53\n"
"ds_read_b64_tr_b16 v[218:219], v228 offset:59392\n"
"v_xor_b32 v228, s75, v228\n"
"v_mfma_f32_16x16x32_bf16 v[152:155], v[96:99], a[112:115], v[152:155]\n"
"buffer_load_dwordx4 v240, s[28:31], s40 offen offset:2048 lds\n"
"v_mfma_f32_16x16x32_bf16 v[168:171], v[96:99], a[128:131], v[196:199]\n"
"ds_read_b64_tr_b16 v[220:221], v232 offset:57344\n"
"v_mfma_f32_16x16x32_bf16 v[168:171], v[100:103], a[132:135], v[168:171]\n"
"ds_read_b64_tr_b16 v[222:223], v232 offset:59392\n"
"v_xor_b32 v232, s75, v232\n"
"v_mfma_f32_16x16x32_bf16 v[168:171], v[104:107], a[136:139], v[168:171]\n"
"ds_read_b64_tr_b16 v[96:97], v229 offset:32768\n"
"v_mfma_f32_16x16x32_bf16 v[168:171], v[108:111], a[140:143], v[168:171]\n"
"ds_read_b64_tr_b16 v[98:99], v229 offset:34816\n"
"v_mfma_f32_16x16x32_bf16 v[168:171], v[112:115], a[176:179], v[168:171]\n"
"ds_read_b64_tr_b16 v[100:101], v233 offset:32768\n"
"v_mfma_f32_16x16x32_bf16 v[168:171], v[116:119], a[180:183], v[168:171]\n"
"ds_read_b64_tr_b16 v[102:103], v233 offset:34816\n"
"v_mfma_f32_16x16x32_bf16 v[168:171], v[120:123], a[184:187], v[168:171]\n"
"s_add_u32 s40, s41, s54\n"
"ds_read_b64_tr_b16 v[104:105], v229 offset:40960\n"
"v_mfma_f32_16x16x32_bf16 v[168:171], v[224:227], a[188:191], v[168:171]\n"
"s_waitcnt lgkmcnt(7)\n"
"buffer_load_dwordx4 v240, s[28:31], s40 offen offset:3072 lds\n"
"v_mfma_f32_16x16x32_bf16 v[124:127], v[180:183], a[48:51], v[124:127]\n"
"ds_read_b64_tr_b16 v[106:107], v229 offset:43008\n"
"v_mfma_f32_16x16x32_bf16 v[124:127], v[184:187], a[52:55], v[124:127]\n"
"ds_read_b64_tr_b16 v[108:109], v233 offset:40960\n"
"v_mfma_f32_16x16x32_bf16 v[124:127], v[188:191], a[56:59], v[124:127]\n"
"ds_read_b64_tr_b16 v[110:111], v233 offset:43008\n"
"v_mfma_f32_16x16x32_bf16 v[124:127], v[192:195], a[60:63], v[124:127]\n"
"ds_read_b64_tr_b16 v[112:113], v229 offset:49152\n"
"v_mfma_f32_16x16x32_bf16 v[124:127], v[208:211], a[0:3], v[124:127]\n"
"ds_read_b64_tr_b16 v[114:115], v229 offset:51200\n"
"v_mfma_f32_16x16x32_bf16 v[124:127], v[212:215], a[4:7], v[124:127]\n"
"s_add_u32 s41, s70, 327680\n"
"ds_read_b64_tr_b16 v[116:117], v233 offset:49152\n"
"v_mfma_f32_16x16x32_bf16 v[124:127], v[216:219], a[8:11], v[124:127]\n"
"s_waitcnt lgkmcnt(6)\n"
"s_add_u32 m0, s71, 32768\n"
"ds_read_b64_tr_b16 v[118:119], v233 offset:51200\n"
"v_mfma_f32_16x16x32_bf16 v[124:127], v[220:223], a[12:15], v[124:127]\n"
"buffer_load_dwordx4 v240, s[28:31], s41 offen lds\n"
"v_mfma_f32_16x16x32_bf16 v[140:143], v[220:223], a[28:31], v[140:143]\n"
"ds_read_b64_tr_b16 v[120:121], v229 offset:57344\n"
"v_mfma_f32_16x16x32_bf16 v[140:143], v[216:219], a[24:27], v[140:143]\n"
"ds_read_b64_tr_b16 v[122:123], v229 offset:59392\n"
"v_xor_b32 v229, s75, v229\n"
"v_mfma_f32_16x16x32_bf16 v[140:143], v[212:215], a[20:23], v[140:143]\n"
"ds_read_b64_tr_b16 v[224:225], v233 offset:57344\n"
"v_mfma_f32_16x16x32_bf16 v[140:143], v[208:211], a[16:19], v[140:143]\n"
"ds_read_b64_tr_b16 v[226:227], v233 offset:59392\n"
"v_xor_b32 v233, s75, v233\n"
"v_mfma_f32_16x16x32_bf16 v[140:143], v[192:195], a[76:79], v[140:143]\n"
"v_mfma_f32_16x16x32_bf16 v[140:143], v[188:191], a[72:75], v[140:143]\n"
"v_subrev_f32 v124, s22, v124\n"
"v_subrev_f32 v125, s22, v125\n"
"v_mfma_f32_16x16x32_bf16 v[140:143], v[184:187], a[68:71], v[140:143]\n"
"s_add_u32 s40, s41, s52\n"
"v_subrev_f32 v126, s22, v126\n"
"v_subrev_f32 v127, s22, v127\n"
"v_mfma_f32_16x16x32_bf16 v[140:143], v[180:183], a[64:67], v[140:143]\n"
"buffer_load_dwordx4 v240, s[28:31], s40 offen offset:1024 lds\n"
"v_mfma_f32_16x16x32_bf16 v[156:159], v[180:183], a[80:83], v[156:159]\n"
"v_cvt_pk_bf16_f32 v124, v124, v125\n"
"v_cvt_pk_bf16_f32 v125, v126, v127\n"
"v_mfma_f32_16x16x32_bf16 v[156:159], v[184:187], a[84:87], v[156:159]\n"
"v_mfma_f32_16x16x32_bf16 v[156:159], v[188:191], a[88:91], v[156:159]\n"
"v_mfma_f32_16x16x32_bf16 v[156:159], v[192:195], a[92:95], v[156:159]\n"
"v_mfma_f32_16x16x32_bf16 v[156:159], v[208:211], a[32:35], v[156:159]\n"
"v_mfma_f32_16x16x32_bf16 v[156:159], v[212:215], a[36:39], v[156:159]\n"
"v_subrev_f32 v140, s22, v140\n"
"v_subrev_f32 v141, s22, v141\n"
"v_mfma_f32_16x16x32_bf16 v[156:159], v[216:219], a[40:43], v[156:159]\n"
"s_waitcnt vmcnt(7)\n"
"v_subrev_f32 v142, s22, v142\n"
"v_subrev_f32 v143, s22, v143\n"
"v_mfma_f32_16x16x32_bf16 v[156:159], v[220:223], a[44:47], v[156:159]\n"
"s_barrier\n"
"buffer_store_dword v241, v248, s[84:87], 0 offen sc1\n"
"v_mfma_f32_16x16x32_bf16 v[172:175], v[220:223], a[228:231], v[172:175]\n"
"ds_read_b64_tr_b16 v[220:221], v234 offset:57344\n"
"v_cvt_pk_bf16_f32 v140, v140, v141\n"
"v_mfma_f32_16x16x32_bf16 v[172:175], v[216:219], a[224:227], v[172:175]\n"
"ds_read_b64_tr_b16 v[216:217], v230 offset:57344\n"
"v_cvt_pk_bf16_f32 v141, v142, v143\n"
"v_mfma_f32_16x16x32_bf16 v[172:175], v[212:215], a[212:215], v[172:175]\n"
"ds_read_b64_tr_b16 v[212:213], v234 offset:49152\n"
"v_add_u32 v241, 1, v241\n"
"v_mfma_f32_16x16x32_bf16 v[172:175], v[208:211], a[208:211], v[172:175]\n"
"buffer_load_dwordx4 v[84:87], v243, s[32:35], s18 offen offset:3072 sc1\n"
"v_mfma_f32_16x16x32_bf16 v[172:175], v[192:195], a[236:239], v[172:175]\n"
"ds_read_b64_tr_b16 v[192:193], v234 offset:40960\n"
"v_mfma_f32_16x16x32_bf16 v[172:175], v[188:191], a[232:235], v[172:175]\n"
"ds_read_b64_tr_b16 v[188:189], v230 offset:40960\n"
"v_subrev_f32 v156, s22, v156\n"
"v_mfma_f32_16x16x32_bf16 v[172:175], v[184:187], a[220:223], v[172:175]\n"
"s_add_u32 s40, s41, s53\n"
"ds_read_b64_tr_b16 v[184:185], v234 offset:32768\n"
"v_subrev_f32 v157, s22, v157\n"
"v_mfma_f32_16x16x32_bf16 v[172:175], v[180:183], a[216:219], v[172:175]\n"
"buffer_load_dwordx4 v240, s[28:31], s40 offen offset:2048 lds\n"
"v_mfma_f32_16x16x32_bf16 v[128:131], v[96:99], a[48:51], v[128:131]\n"
"s_add_u32 s16, s90, s62\n"
"ds_read_b64_tr_b16 v[180:181], v230 offset:32768\n"
"v_subrev_f32 v158, s22, v158\n"
"v_mfma_f32_16x16x32_bf16 v[128:131], v[100:103], a[52:55], v[128:131]\n"
"s_waitcnt lgkmcnt(5)\n"
"s_add_u32 s17, s16, 4096\n"
"ds_read_b64_tr_b16 v[182:183], v230 offset:34816\n"
"v_subrev_f32 v159, s22, v159\n"
"v_mfma_f32_16x16x32_bf16 v[128:131], v[104:107], a[56:59], v[128:131]\n"
"s_lshl_b32 s62, s70, 9\n"
"ds_read_b64_tr_b16 v[186:187], v234 offset:34816\n"
"v_cvt_pk_bf16_f32 v156, v156, v157\n"
"v_mfma_f32_16x16x32_bf16 v[128:131], v[108:111], a[60:63], v[128:131]\n"
"ds_read_b64_tr_b16 v[190:191], v230 offset:43008\n"
"v_cvt_pk_bf16_f32 v157, v158, v159\n"
"v_mfma_f32_16x16x32_bf16 v[128:131], v[112:115], a[0:3], v[128:131]\n"
"ds_read_b64_tr_b16 v[194:195], v234 offset:43008\n"
"v_lshlrev_b32 v202, 16, v72\n"
"v_mfma_f32_16x16x32_bf16 v[128:131], v[116:119], a[4:7], v[128:131]\n"
"ds_read_b64_tr_b16 v[208:209], v230 offset:49152\n"
"v_and_b32 v72, s73, v72\n"
"v_mfma_f32_16x16x32_bf16 v[128:131], v[120:123], a[8:11], v[128:131]\n"
"s_add_u32 s40, s41, s54\n"
"ds_read_b64_tr_b16 v[210:211], v230 offset:51200\n"
"v_add_f32 v0, v0, v202\n"
"v_mfma_f32_16x16x32_bf16 v[128:131], v[224:227], a[12:15], v[128:131]\n"
"buffer_load_dwordx4 v240, s[28:31], s40 offen offset:3072 lds\n"
"v_mfma_f32_16x16x32_bf16 v[144:147], v[224:227], a[28:31], v[144:147]\n"
"ds_read_b64_tr_b16 v[214:215], v234 offset:51200\n"
"v_add_f32 v1, v1, v72\n"
"v_mfma_f32_16x16x32_bf16 v[144:147], v[120:123], a[24:27], v[144:147]\n"
"ds_read_b64_tr_b16 v[218:219], v230 offset:59392\n"
"v_lshlrev_b32 v203, 16, v73\n"
"v_mfma_f32_16x16x32_bf16 v[144:147], v[116:119], a[20:23], v[144:147]\n"
"ds_read_b64_tr_b16 v[222:223], v234 offset:59392\n"
"v_and_b32 v73, s73, v73\n"
"v_mfma_f32_16x16x32_bf16 v[144:147], v[112:115], a[16:19], v[144:147]\n"
"buffer_load_dwordx4 v[88:91], v243, s[32:35], s19 offen sc1\n"
"v_mfma_f32_16x16x32_bf16 v[144:147], v[108:111], a[76:79], v[144:147]\n"
"v_add_f32 v2, v2, v203\n"
"v_add_f32 v3, v3, v73\n"
"v_mfma_f32_16x16x32_bf16 v[144:147], v[104:107], a[72:75], v[144:147]\n"
"s_add_u32 s41, s70, 491520\n"
"v_subrev_f32 v128, s22, v128\n"
"v_subrev_f32 v129, s22, v129\n"
"v_mfma_f32_16x16x32_bf16 v[144:147], v[100:103], a[68:71], v[144:147]\n"
"s_add_u32 m0, s71, 49152\n"
"v_subrev_f32 v130, s22, v130\n"
"v_subrev_f32 v131, s22, v131\n"
"v_mfma_f32_16x16x32_bf16 v[144:147], v[96:99], a[64:67], v[144:147]\n"
"buffer_load_dwordx4 v240, s[28:31], s41 offen lds\n"
"v_mfma_f32_16x16x32_bf16 v[160:163], v[96:99], a[80:83], v[160:163]\n"
"s_and_b32 s70, s88, 15\n"
"v_cvt_pk_bf16_f32 v126, v128, v129\n"
"v_cvt_pk_bf16_f32 v127, v130, v131\n"
"v_mfma_f32_16x16x32_bf16 v[160:163], v[100:103], a[84:87], v[160:163]\n"
"s_lshr_b32 s88, s88, 4\n"
"v_lshlrev_b32 v204, 16, v74\n"
"v_and_b32 v74, s73, v74\n"
"v_mfma_f32_16x16x32_bf16 v[160:163], v[104:107], a[88:91], v[160:163]\n"
"s_lshl_b32 s70, s70, 7\n"
"v_add_f32 v4, v4, v204\n"
"v_add_f32 v5, v5, v74\n"
"v_mfma_f32_16x16x32_bf16 v[160:163], v[108:111], a[92:95], v[160:163]\n"
"s_xor_b32 s71, s71, s75\n"
"v_lshlrev_b32 v205, 16, v75\n"
"v_and_b32 v75, s73, v75\n"
"v_mfma_f32_16x16x32_bf16 v[160:163], v[112:115], a[32:35], v[160:163]\n"
"v_add_f32 v6, v6, v205\n"
"v_add_f32 v7, v7, v75\n"
"v_mfma_f32_16x16x32_bf16 v[160:163], v[116:119], a[36:39], v[160:163]\n"
"v_subrev_f32 v144, s22, v144\n"
"v_subrev_f32 v145, s22, v145\n"
"v_mfma_f32_16x16x32_bf16 v[160:163], v[120:123], a[40:43], v[160:163]\n"
"v_subrev_f32 v146, s22, v146\n"
"v_subrev_f32 v147, s22, v147\n"
"v_mfma_f32_16x16x32_bf16 v[160:163], v[224:227], a[44:47], v[160:163]\n"
"buffer_store_dwordx4 v[124:127], v243, s[32:35], s16 offen sc1\n"
"v_cvt_pk_bf16_f32 v142, v144, v145\n"
"v_mfma_f32_16x16x32_bf16 v[176:179], v[224:227], a[228:231], v[176:179]\n"
"ds_read_b64_tr_b16 v[224:225], v235 offset:57344\n"
"v_cvt_pk_bf16_f32 v143, v146, v147\n"
"v_mfma_f32_16x16x32_bf16 v[176:179], v[120:123], a[224:227], v[176:179]\n"
"ds_read_b64_tr_b16 v[120:121], v231 offset:57344\n"
"v_lshlrev_b32 v206, 16, v76\n"
"v_mfma_f32_16x16x32_bf16 v[176:179], v[116:119], a[212:215], v[176:179]\n"
"ds_read_b64_tr_b16 v[116:117], v235 offset:49152\n"
"v_and_b32 v76, s73, v76\n"
"v_mfma_f32_16x16x32_bf16 v[176:179], v[112:115], a[208:211], v[176:179]\n"
"buffer_load_dwordx4 v[92:95], v243, s[32:35], s19 offen offset:1024 sc1\n"
"v_mfma_f32_16x16x32_bf16 v[176:179], v[108:111], a[236:239], v[176:179]\n"
"ds_read_b64_tr_b16 v[108:109], v235 offset:40960\n"
"v_add_f32 v8, v8, v206\n"
"v_mfma_f32_16x16x32_bf16 v[176:179], v[104:107], a[232:235], v[176:179]\n"
"ds_read_b64_tr_b16 v[104:105], v231 offset:40960\n"
"v_subrev_f32 v160, s22, v160\n"
"v_mfma_f32_16x16x32_bf16 v[176:179], v[100:103], a[220:223], v[176:179]\n"
"s_add_u32 s40, s41, s52\n"
"ds_read_b64_tr_b16 v[100:101], v235 offset:32768\n"
"v_subrev_f32 v161, s22, v161\n"
"v_mfma_f32_16x16x32_bf16 v[176:179], v[96:99], a[216:219], v[176:179]\n"
"s_waitcnt vmcnt(14) lgkmcnt(6)\n"
"buffer_load_dwordx4 v240, s[28:31], s40 offen offset:1024 lds\n"
"v_mfma_f32_16x16x32_bf16 v[132:135], v[180:183], a[48:51], v[132:135]\n"
"ds_read_b64_tr_b16 v[96:97], v231 offset:32768\n"
"v_subrev_f32 v162, s22, v162\n"
"v_mfma_f32_16x16x32_bf16 v[132:135], v[184:187], a[52:55], v[132:135]\n"
"ds_read_b64_tr_b16 v[98:99], v231 offset:34816\n"
"v_subrev_f32 v163, s22, v163\n"
"v_mfma_f32_16x16x32_bf16 v[132:135], v[188:191], a[56:59], v[132:135]\n"
"ds_read_b64_tr_b16 v[102:103], v235 offset:34816\n"
"v_cvt_pk_bf16_f32 v158, v160, v161\n"
"v_mfma_f32_16x16x32_bf16 v[132:135], v[192:195], a[60:63], v[132:135]\n"
"ds_read_b64_tr_b16 v[106:107], v231 offset:43008\n"
"v_cvt_pk_bf16_f32 v159, v162, v163\n"
"v_mfma_f32_16x16x32_bf16 v[132:135], v[208:211], a[0:3], v[132:135]\n"
"ds_read_b64_tr_b16 v[110:111], v235 offset:43008\n"
"v_add_f32 v9, v9, v76\n"
"v_mfma_f32_16x16x32_bf16 v[132:135], v[212:215], a[4:7], v[132:135]\n"
"ds_read_b64_tr_b16 v[112:113], v231 offset:49152\n"
"v_lshlrev_b32 v207, 16, v77\n"
"v_mfma_f32_16x16x32_bf16 v[132:135], v[216:219], a[8:11], v[132:135]\n"
"ds_read_b64_tr_b16 v[114:115], v231 offset:51200\n"
"v_and_b32 v77, s73, v77\n"
"v_mfma_f32_16x16x32_bf16 v[132:135], v[220:223], a[12:15], v[132:135]\n"
"buffer_store_dwordx4 v[140:143], v243, s[32:35], s16 offen offset:2048 sc1\n"
"v_add_f32 v10, v10, v207\n"
"v_mfma_f32_16x16x32_bf16 v[148:151], v[220:223], a[28:31], v[148:151]\n"
"ds_read_b64_tr_b16 v[118:119], v235 offset:51200\n"
"v_add_f32 v11, v11, v77\n"
"v_mfma_f32_16x16x32_bf16 v[148:151], v[216:219], a[24:27], v[148:151]\n"
"ds_read_b64_tr_b16 v[122:123], v231 offset:59392\n"
"v_lshlrev_b32 v202, 16, v78\n"
"v_mfma_f32_16x16x32_bf16 v[148:151], v[212:215], a[20:23], v[148:151]\n"
"ds_read_b64_tr_b16 v[226:227], v235 offset:59392\n"
"v_and_b32 v78, s73, v78\n"
"v_mfma_f32_16x16x32_bf16 v[148:151], v[208:211], a[16:19], v[148:151]\n"
"v_add_f32 v12, v12, v202\n"
"v_add_f32 v13, v13, v78\n"
"v_mfma_f32_16x16x32_bf16 v[148:151], v[192:195], a[76:79], v[148:151]\n"
"v_lshlrev_b32 v203, 16, v79\n"
"v_and_b32 v79, s73, v79\n"
"v_mfma_f32_16x16x32_bf16 v[148:151], v[188:191], a[72:75], v[148:151]\n"
"v_add_f32 v14, v14, v203\n"
"v_add_f32 v15, v15, v79\n"
"v_mfma_f32_16x16x32_bf16 v[148:151], v[184:187], a[68:71], v[148:151]\n"
"s_add_u32 s40, s41, s53\n"
"v_lshlrev_b32 v204, 16, v80\n"
"v_and_b32 v80, s73, v80\n"
"v_mfma_f32_16x16x32_bf16 v[148:151], v[180:183], a[64:67], v[148:151]\n"
"buffer_load_dwordx4 v240, s[28:31], s40 offen offset:2048 lds\n"
"v_mfma_f32_16x16x32_bf16 v[164:167], v[180:183], a[80:83], v[164:167]\n"
"v_add_f32 v16, v16, v204\n"
"v_add_f32 v17, v17, v80\n"
"v_mfma_f32_16x16x32_bf16 v[164:167], v[184:187], a[84:87], v[164:167]\n"
"v_lshlrev_b32 v205, 16, v81\n"
"v_and_b32 v81, s73, v81\n"
"v_mfma_f32_16x16x32_bf16 v[164:167], v[188:191], a[88:91], v[164:167]\n"
"v_add_f32 v18, v18, v205\n"
"v_add_f32 v19, v19, v81\n"
"v_mfma_f32_16x16x32_bf16 v[164:167], v[192:195], a[92:95], v[164:167]\n"
"buffer_load_dwordx4 v[244:247], v243, s[32:35], s19 offen offset:2048 sc1\n"
"v_mfma_f32_16x16x32_bf16 v[164:167], v[208:211], a[32:35], v[164:167]\n"
"s_lshl_b32 s18, s72, 4\n"
"v_lshlrev_b32 v206, 16, v82\n"
"v_and_b32 v82, s73, v82\n"
"v_mfma_f32_16x16x32_bf16 v[164:167], v[212:215], a[36:39], v[164:167]\n"
"s_lshl_b32 s19, s72, 2\n"
"v_add_f32 v20, v20, v206\n"
"v_add_f32 v21, v21, v82\n"
"v_mfma_f32_16x16x32_bf16 v[164:167], v[216:219], a[40:43], v[164:167]\n"
"s_waitcnt vmcnt(14)\n"
"s_lshr_b32 s19, s89, s19\n"
"v_lshlrev_b32 v207, 16, v83\n"
"v_mfma_f32_16x16x32_bf16 v[164:167], v[220:223], a[44:47], v[164:167]\n"
"s_barrier\n"
"s_and_b32 s19, s19, 1\n"
"s_waitcnt lgkmcnt(0)\n"
"v_mfma_f32_16x16x32_bf16 v[136:139], v[96:99], a[48:51], v[136:139]\n"
"s_lshl3_add_u32 s18, s18, s19\n"
"ds_read_b64_tr_b16 v[180:181], v228 offset:0\n"
"v_and_b32 v83, s73, v83\n"
"v_mfma_f32_16x16x32_bf16 v[136:139], v[100:103], a[52:55], v[136:139]\n"
"s_lshl_b32 s18, s18, 13\n"
"ds_read_b64_tr_b16 v[182:183], v228 offset:2048\n"
"v_add_f32 v22, v22, v207\n"
"v_mfma_f32_16x16x32_bf16 v[136:139], v[104:107], a[56:59], v[136:139]\n"
"s_add_u32 s18, s18, s91\n"
"v_lshlrev_b32_e64 v201, 2, s72\n"
"ds_read_b64_tr_b16 v[184:185], v232 offset:0\n"
"v_mfma_f32_16x16x32_bf16 v[136:139], v[108:111], a[60:63], v[136:139]\n"
"s_add_u32 s19, s18, 4096\n"
"global_load_dword v200, v201, s[6:7] sc1\n"
"v_mfma_f32_16x16x32_bf16 v[136:139], v[112:115], a[0:3], v[136:139]\n"
"s_mov_b32 s69, 15\n"
"ds_read_b64_tr_b16 v[186:187], v232 offset:2048\n"
"v_add_f32 v23, v23, v83\n"
"v_mfma_f32_16x16x32_bf16 v[136:139], v[116:119], a[4:7], v[136:139]\n"
"s_add_u32 s21, s21, 1\n"
"ds_read_b64_tr_b16 v[188:189], v228 offset:8192\n"
"v_subrev_f32 v172, s22, v172\n"
"v_mfma_f32_16x16x32_bf16 v[136:139], v[120:123], a[8:11], v[136:139]\n"
"ds_read_b64_tr_b16 v[190:191], v228 offset:10240\n"
"v_subrev_f32 v173, s22, v173\n"
"v_mfma_f32_16x16x32_bf16 v[136:139], v[224:227], a[12:15], v[136:139]\n"
"buffer_store_dwordx4 v[156:159], v243, s[32:35], s17 offen sc1\n"
"v_subrev_f32 v174, s22, v174\n"
"v_mfma_f32_16x16x32_bf16 v[152:155], v[224:227], a[28:31], v[152:155]\n"
"ds_read_b64_tr_b16 v[192:193], v232 offset:8192\n"
"v_subrev_f32 v175, s22, v175\n"
"v_mfma_f32_16x16x32_bf16 v[152:155], v[120:123], a[24:27], v[152:155]\n"
"ds_read_b64_tr_b16 v[194:195], v232 offset:10240\n"
"v_cvt_pk_bf16_f32 v172, v172, v173\n"
"v_mfma_f32_16x16x32_bf16 v[152:155], v[116:119], a[20:23], v[152:155]\n"
"ds_read_b64_tr_b16 v[208:209], v228 offset:16384\n"
"v_cvt_pk_bf16_f32 v173, v174, v175\n"
"v_mfma_f32_16x16x32_bf16 v[152:155], v[112:115], a[16:19], v[152:155]\n"
"ds_read_b64_tr_b16 v[210:211], v228 offset:18432\n"
"v_subrev_f32 v176, s22, v176\n"
"v_mfma_f32_16x16x32_bf16 v[152:155], v[108:111], a[76:79], v[152:155]\n"
"ds_read_b64_tr_b16 v[212:213], v232 offset:16384\n"
"v_subrev_f32 v177, s22, v177\n"
"v_mfma_f32_16x16x32_bf16 v[152:155], v[104:107], a[72:75], v[152:155]\n"
"ds_read_b64_tr_b16 v[214:215], v232 offset:18432\n"
"v_subrev_f32 v178, s22, v178\n"
"v_mfma_f32_16x16x32_bf16 v[152:155], v[100:103], a[68:71], v[152:155]\n"
"s_add_u32 s40, s41, s54\n"
"ds_read_b64_tr_b16 v[216:217], v228 offset:24576\n"
"v_subrev_f32 v179, s22, v179\n"
"v_mfma_f32_16x16x32_bf16 v[152:155], v[96:99], a[64:67], v[152:155]\n"
"buffer_load_dwordx4 v240, s[28:31], s40 offen offset:3072 lds\n"
"v_mfma_f32_16x16x32_bf16 v[168:171], v[96:99], a[80:83], v[168:171]\n"
"ds_read_b64_tr_b16 v[218:219], v228 offset:26624\n"
"v_cvt_pk_bf16_f32 v174, v176, v177\n"
"v_mfma_f32_16x16x32_bf16 v[168:171], v[100:103], a[84:87], v[168:171]\n"
"ds_read_b64_tr_b16 v[220:221], v232 offset:24576\n"
"v_cvt_pk_bf16_f32 v175, v178, v179\n"
"v_mfma_f32_16x16x32_bf16 v[168:171], v[104:107], a[88:91], v[168:171]\n"
"ds_read_b64_tr_b16 v[222:223], v232 offset:26624\n"
"v_subrev_f32 v132, s22, v132\n"
"v_mfma_f32_16x16x32_bf16 v[168:171], v[108:111], a[92:95], v[168:171]\n"
"ds_read_b64_tr_b16 v[96:97], v229 offset:0\n"
"v_subrev_f32 v133, s22, v133\n"
"v_mfma_f32_16x16x32_bf16 v[168:171], v[112:115], a[32:35], v[168:171]\n"
"ds_read_b64_tr_b16 v[98:99], v229 offset:2048\n"
"v_subrev_f32 v134, s22, v134\n"
"v_mfma_f32_16x16x32_bf16 v[168:171], v[116:119], a[36:39], v[168:171]\n"
"ds_read_b64_tr_b16 v[100:101], v233 offset:0\n"
"v_subrev_f32 v135, s22, v135\n"
"v_mfma_f32_16x16x32_bf16 v[168:171], v[120:123], a[40:43], v[168:171]\n"
"s_add_u32 s47, s47, 1\n"
"ds_read_b64_tr_b16 v[102:103], v233 offset:2048\n"
"v_cvt_pk_bf16_f32 v132, v132, v133\n"
"v_mfma_f32_16x16x32_bf16 v[168:171], v[224:227], a[44:47], v[168:171]\n"
"s_waitcnt lgkmcnt(7)\n"
"s_cmp_lg_u32 s47, 9\n"
"ds_read_b64_tr_b16 v[104:105], v229 offset:8192\n"
"s_cbranch_scc1 LGL_%=\n"
"v_mfma_f32_16x16x32_bf16 v[0:3], v[180:183], a[96:99], v[0:3]\n"
"v_xor_b32 v235, s75, v235\n"
"v_subrev_f32 v136, s22, v136\n"
"v_mfma_f32_16x16x32_bf16 v[0:3], v[184:187], a[100:103], v[0:3]\n"
"ds_read_b64_tr_b16 v[114:115], v229 offset:18432\n"
"v_cvt_pk_bf16_f32 v133, v134, v135\n"
"v_mfma_f32_16x16x32_bf16 v[0:3], v[188:191], a[104:107], v[0:3]\n"
"v_mov_b32 v201, v237\n"
"v_subrev_f32 v138, s22, v138\n"
"v_subrev_f32 v139, s22, v139\n"
"v_mfma_f32_16x16x32_bf16 v[0:3], v[192:195], a[108:111], v[0:3]\n"
"global_load_dword v200, v201, s[6:7] sc1\n"
"v_mfma_f32_16x16x32_bf16 v[0:3], v[208:211], a[144:147], v[0:3]\n"
"v_subrev_f32 v167, s22, v167\n"
"v_cvt_pk_bf16_f32 v135, v138, v139\n"
"v_mfma_f32_16x16x32_bf16 v[0:3], v[212:215], a[148:151], v[0:3]\n"
"s_waitcnt lgkmcnt(3)\n"
"v_subrev_f32 v148, s22, v148\n"
"v_subrev_f32 v137, s22, v137\n"
"v_mfma_f32_16x16x32_bf16 v[0:3], v[216:219], a[152:155], v[0:3]\n"
"v_subrev_f32 v165, s22, v165\n"
"v_subrev_f32 v168, s22, v168\n"
"v_mfma_f32_16x16x32_bf16 v[0:3], v[220:223], a[156:159], v[0:3]\n"
"buffer_store_dwordx4 v[172:175], v243, s[32:35], s17 offen offset:2048 sc1\n"
"v_subrev_f32 v151, s22, v151\n"
"v_cvt_pk_bf16_f32 v134, v136, v137\n"
"v_mfma_f32_16x16x32_bf16 v[16:19], v[220:223], a[172:175], v[16:19]\n"
"buffer_store_dwordx4 v[132:135], v243, s[32:35], s16 offen offset:1024 sc1\n"
"v_subrev_f32 v149, s22, v149\n"
"v_subrev_f32 v150, s22, v150\n"
"v_subrev_f32 v164, s22, v164\n"
"v_cvt_pk_bf16_f32 v164, v164, v165\n"
"v_subrev_f32 v153, s22, v153\n"
"v_subrev_f32 v155, s22, v155\n"
"v_mfma_f32_16x16x32_bf16 v[16:19], v[216:219], a[168:171], v[16:19]\n"
"v_cvt_pk_bf16_f32 v148, v148, v149\n"
"v_subrev_f32 v154, s22, v154\n"
"v_mfma_f32_16x16x32_bf16 v[16:19], v[212:215], a[164:167], v[16:19]\n"
"v_cvt_pk_bf16_f32 v149, v150, v151\n"
"v_subrev_f32 v170, s22, v170\n"
"v_mfma_f32_16x16x32_bf16 v[16:19], v[208:211], a[160:163], v[16:19]\n"
"v_subrev_f32 v166, s22, v166\n"
"v_subrev_f32 v171, s22, v171\n"
"v_mfma_f32_16x16x32_bf16 v[16:19], v[192:195], a[124:127], v[16:19]\n"
"v_cvt_pk_bf16_f32 v165, v166, v167\n"
"v_cvt_pk_bf16_f32 v167, v170, v171\n"
"v_mfma_f32_16x16x32_bf16 v[16:19], v[188:191], a[120:123], v[16:19]\n"
"v_subrev_f32 v152, s22, v152\n"
"v_cvt_pk_bf16_f32 v151, v154, v155\n"
"v_mfma_f32_16x16x32_bf16 v[16:19], v[184:187], a[116:119], v[16:19]\n"
"v_cvt_pk_bf16_f32 v150, v152, v153\n"
"v_subrev_f32 v169, s22, v169\n"
"v_mfma_f32_16x16x32_bf16 v[16:19], v[180:183], a[112:115], v[16:19]\n"
"s_waitcnt vmcnt(16)\n"
"buffer_store_dwordx4 v[148:151], v243, s[32:35], s16 offen offset:3072 sc1\n"
"v_cvt_pk_bf16_f32 v166, v168, v169\n"
"v_mfma_f32_16x16x32_bf16 v[32:35], v[180:183], a[128:131], v[32:35]\n"
"buffer_store_dwordx4 v[164:167], v243, s[32:35], s17 offen offset:1024 sc1\n"
"ds_read_b64_tr_b16 v[120:121], v229 offset:24576\n"
"v_mfma_f32_16x16x32_bf16 v[32:35], v[184:187], a[132:135], v[32:35]\n"
"ds_read_b64_tr_b16 v[224:225], v233 offset:24576\n"
"v_xor_b32 v234, s75, v234\n"
"v_mfma_f32_16x16x32_bf16 v[32:35], v[188:191], a[136:139], v[32:35]\n"
"ds_read_b64_tr_b16 v[122:123], v229 offset:26624\n"
"v_lshlrev_b32 v203, 16, v85\n"
"v_mfma_f32_16x16x32_bf16 v[32:35], v[192:195], a[140:143], v[32:35]\n"
"ds_read_b64_tr_b16 v[108:109], v233 offset:8192\n"
"v_lshlrev_b32 v202, 16, v84\n"
"v_mfma_f32_16x16x32_bf16 v[32:35], v[208:211], a[176:179], v[32:35]\n"
"ds_read_b64_tr_b16 v[226:227], v233 offset:26624\n"
"v_and_b32 v84, s73, v84\n"
"v_mfma_f32_16x16x32_bf16 v[32:35], v[212:215], a[180:183], v[32:35]\n"
"ds_read_b64_tr_b16 v[110:111], v233 offset:10240\n"
"v_bfe_u32 v161, v242, 8, 2\n"
"v_mfma_f32_16x16x32_bf16 v[32:35], v[216:219], a[184:187], v[32:35]\n"
"ds_read_b64_tr_b16 v[106:107], v229 offset:10240\n"
"v_lshlrev_b32 v205, 16, v87\n"
"v_mfma_f32_16x16x32_bf16 v[32:35], v[220:223], a[188:191], v[32:35]\n"
"v_xor_b32 v230, s75, v230\n"
"v_lshlrev_b32 v204, 16, v86\n"
"v_mfma_f32_16x16x32_bf16 v[48:51], v[220:223], a[252:255], v[48:51]\n"
"ds_read_b64_tr_b16 v[220:221], v234 offset:24576\n"
"v_add_f32 v26, v26, v203\n"
"v_mfma_f32_16x16x32_bf16 v[48:51], v[216:219], a[248:251], v[48:51]\n"
"ds_read_b64_tr_b16 v[112:113], v229 offset:16384\n"
"v_bfe_u32 v160, v242, 6, 2\n"
"v_mfma_f32_16x16x32_bf16 v[48:51], v[212:215], a[244:247], v[48:51]\n"
"ds_read_b64_tr_b16 v[116:117], v233 offset:16384\n"
"v_add_f32 v24, v24, v202\n"
"v_mfma_f32_16x16x32_bf16 v[48:51], v[208:211], a[240:243], v[48:51]\n"
"ds_read_b64_tr_b16 v[118:119], v233 offset:18432\n"
"v_add_f32 v25, v25, v84\n"
"v_mfma_f32_16x16x32_bf16 v[48:51], v[192:195], a[204:207], v[48:51]\n"
"ds_read_b64_tr_b16 v[210:211], v230 offset:18432\n"
"v_add_f32 v28, v28, v204\n"
"v_mfma_f32_16x16x32_bf16 v[48:51], v[188:191], a[200:203], v[48:51]\n"
"ds_read_b64_tr_b16 v[188:189], v230 offset:8192\n"
"v_and_b32 v86, s73, v86\n"
"v_mfma_f32_16x16x32_bf16 v[48:51], v[184:187], a[196:199], v[48:51]\n"
"ds_read_b64_tr_b16 v[194:195], v234 offset:10240\n"
"v_mov_b32 v164, 0\n"
"v_mfma_f32_16x16x32_bf16 v[48:51], v[180:183], a[192:195], v[48:51]\n"
"s_waitcnt vmcnt(15)\n"
"v_xor_b32 v160, v160, v161\n"
"v_lshlrev_b32 v202, 16, v90\n"
"v_mfma_f32_16x16x32_bf16 v[4:7], v[96:99], a[96:99], v[4:7]\n"
"s_waitcnt lgkmcnt(6)\n"
"ds_read_b64_tr_b16 v[222:223], v234 offset:26624\n"
"v_lshlrev_b32 v203, 16, v91\n"
"v_mfma_f32_16x16x32_bf16 v[4:7], v[100:103], a[100:103], v[4:7]\n"
"ds_read_b64_tr_b16 v[184:185], v234 offset:0\n"
"v_lshlrev_b32 v206, 16, v88\n"
"v_mfma_f32_16x16x32_bf16 v[4:7], v[104:107], a[104:107], v[4:7]\n"
"ds_read_b64_tr_b16 v[216:217], v230 offset:24576\n"
"v_mfma_f32_16x16x32_bf16 v[4:7], v[108:111], a[108:111], v[4:7]\n"
"s_waitcnt lgkmcnt(6)\n"
"ds_read_b64_tr_b16 v[218:219], v230 offset:26624\n"
"v_and_b32 v91, s73, v91\n"
"v_mfma_f32_16x16x32_bf16 v[4:7], v[112:115], a[144:147], v[4:7]\n"
"s_waitcnt vmcnt(4)\n"
"ds_read_b64_tr_b16 v[190:191], v230 offset:10240\n"
"v_lshlrev_b32 v207, 16, v89\n"
"v_mfma_f32_16x16x32_bf16 v[4:7], v[116:119], a[148:151], v[4:7]\n"
"v_subrev_u32 v200, s21, v200\n"
"ds_read_b64_tr_b16 v[208:209], v230 offset:16384\n"
"v_mfma_f32_16x16x32_bf16 v[4:7], v[120:123], a[152:155], v[4:7]\n"
"v_cmp_lt_u32 vcc, s69, v200\n"
"ds_read_b64_tr_b16 v[182:183], v230 offset:2048\n"
"v_mfma_f32_16x16x32_bf16 v[4:7], v[224:227], a[156:159], v[4:7]\n"
"ds_read_b64_tr_b16 v[180:181], v230 offset:0\n"
"s_cbranch_vccnz LGQ_%=\nLGQK_%=:\n"
"v_mfma_f32_16x16x32_bf16 v[20:23], v[224:227], a[172:175], v[20:23]\n"
"s_bfe_u32 s20, s89, 65536\ns_lshl_b32 s20, s20, 13\ns_add_u32 s20, s20, 0\n"
"ds_read_b64_tr_b16 v[214:215], v234 offset:18432\n"
"v_and_b32 v85, s73, v85\n"
"v_mfma_f32_16x16x32_bf16 v[20:23], v[120:123], a[168:171], v[20:23]\n"
"buffer_load_dwordx4 v[124:127], v236, s[32:35], s20 offen sc1\n"
"v_mfma_f32_16x16x32_bf16 v[20:23], v[116:119], a[164:167], v[20:23]\n"
"v_and_b32 v90, s73, v90\n"
"v_add_f32 v37, v37, v90\n"
"v_mfma_f32_16x16x32_bf16 v[20:23], v[112:115], a[160:163], v[20:23]\n"
"s_bfe_u32 s20, s89, 65540\ns_lshl_b32 s20, s20, 13\ns_add_u32 s20, s20, 1048576\n"
"v_add_f32 v36, v36, v202\n"
"v_add_f32 v38, v38, v203\n"
"v_mfma_f32_16x16x32_bf16 v[20:23], v[108:111], a[124:127], v[20:23]\n"
"buffer_load_dwordx4 v[128:131], v236, s[32:35], s20 offen sc1\n"
"v_mfma_f32_16x16x32_bf16 v[20:23], v[104:107], a[120:123], v[20:23]\n"
"v_add_u32 v160, -1, v160\n"
"v_add_f32 v39, v39, v91\n"
"v_mfma_f32_16x16x32_bf16 v[20:23], v[100:103], a[116:119], v[20:23]\n"
"s_bfe_u32 s20, s89, 65544\ns_lshl_b32 s20, s20, 13\ns_add_u32 s20, s20, 2097152\n"
"v_add_f32 v32, v32, v206\n"
"v_mfma_f32_16x16x32_bf16 v[20:23], v[96:99], a[112:115], v[20:23]\n"
"buffer_load_dwordx4 v[132:135], v236, s[32:35], s20 offen sc1\n"
"v_mfma_f32_16x16x32_bf16 v[36:39], v[96:99], a[128:131], v[36:39]\n"
"v_lshlrev_b32 v204, 16, v92\n"
"v_add_f32 v40, v40, v204\n"
"v_mfma_f32_16x16x32_bf16 v[36:39], v[100:103], a[132:135], v[36:39]\n"
"s_bfe_u32 s20, s89, 65548\ns_lshl_b32 s20, s20, 13\ns_add_u32 s20, s20, 3145728\n"
"ds_read_b64_tr_b16 v[212:213], v234 offset:16384\n"
"v_add_f32 v30, v30, v205\n"
"v_mfma_f32_16x16x32_bf16 v[36:39], v[104:107], a[136:139], v[36:39]\n"
"buffer_load_dwordx4 v[136:139], v236, s[32:35], s20 offen sc1\n"
"v_mfma_f32_16x16x32_bf16 v[36:39], v[108:111], a[140:143], v[36:39]\n"
"v_and_b32 v92, s73, v92\n"
"v_lshlrev_b32 v206, 16, v94\n"
"v_mfma_f32_16x16x32_bf16 v[36:39], v[112:115], a[176:179], v[36:39]\n"
"s_bfe_u32 s20, s89, 65552\ns_lshl_b32 s20, s20, 13\ns_add_u32 s20, s20, 4194304\n"
"ds_read_b64_tr_b16 v[186:187], v234 offset:2048\n"
"ds_read_b64_tr_b16 v[192:193], v234 offset:8192\n"
"v_mfma_f32_16x16x32_bf16 v[36:39], v[116:119], a[180:183], v[36:39]\n"
"buffer_load_dwordx4 v[140:143], v236, s[32:35], s20 offen sc1\n"
"v_mfma_f32_16x16x32_bf16 v[36:39], v[120:123], a[184:187], v[36:39]\n"
"v_and_b32 v94, s73, v94\n"
"v_mfma_f32_16x16x32_bf16 v[36:39], v[224:227], a[188:191], v[36:39]\n"
"s_bfe_u32 s20, s89, 65556\ns_lshl_b32 s20, s20, 13\ns_add_u32 s20, s20, 5242880\n"
"v_add_f32 v34, v34, v207\n"
"v_lshlrev_b32 v205, 16, v93\n"
"v_mfma_f32_16x16x32_bf16 v[52:55], v[224:227], a[252:255], v[52:55]\n"
"buffer_load_dwordx4 v[144:147], v236, s[32:35], s20 offen sc1\n"
"v_mfma_f32_16x16x32_bf16 v[52:55], v[120:123], a[248:251], v[52:55]\n"
"v_ashrrev_i32 v160, 31, v160\n"
"v_add_f32 v44, v44, v206\n"
"v_mfma_f32_16x16x32_bf16 v[52:55], v[116:119], a[244:247], v[52:55]\n"
"s_bfe_u32 s20, s89, 65560\ns_lshl_b32 s20, s20, 13\ns_add_u32 s20, s20, 6291456\n"
"v_xor_b32 v231, s75, v231\n"
"v_lshlrev_b32 v207, 16, v95\n"
"v_mfma_f32_16x16x32_bf16 v[52:55], v[112:115], a[240:243], v[52:55]\n"
"buffer_load_dwordx4 v[148:151], v236, s[32:35], s20 offen sc1\n"
"v_mfma_f32_16x16x32_bf16 v[52:55], v[108:111], a[204:207], v[52:55]\n"
"s_bfe_u32 s20, s89, 65564\ns_lshl_b32 s20, s20, 13\ns_add_u32 s20, s20, 7340032\n"
"v_and_b32 v87, s73, v87\n"
"v_and_b32 v95, s73, v95\n"
"v_mfma_f32_16x16x32_bf16 v[52:55], v[104:107], a[200:203], v[52:55]\n"
"buffer_load_dwordx4 v[152:155], v236, s[32:35], s20 offen sc1\n"
"v_mfma_f32_16x16x32_bf16 v[52:55], v[100:103], a[196:199], v[52:55]\n"
"s_waitcnt vmcnt(8)\n"
"s_add_u32 s21, s21, 1\n"
"v_and_b32 v88, s73, v88\n"
"v_mfma_f32_16x16x32_bf16 v[52:55], v[96:99], a[192:195], v[52:55]\n"
"s_barrier\n"
"s_mov_b32 s52, 40960\n"
"buffer_store_dword v241, v248, s[84:87], 0 offen sc1\n"
"s_waitcnt lgkmcnt(0)\n"
"v_mfma_f32_16x16x32_bf16 v[8:11], v[180:183], a[96:99], v[8:11]\n"
"s_mov_b32 s53, 81920\n"
"ds_read_b64_tr_b16 v[98:99], v231 offset:2048\n"
"v_add_f32 v42, v42, v205\n"
"v_mfma_f32_16x16x32_bf16 v[8:11], v[184:187], a[100:103], v[8:11]\n"
"ds_read_b64_tr_b16 v[112:113], v231 offset:16384\n"
"v_add_f32 v31, v31, v87\n"
"v_mfma_f32_16x16x32_bf16 v[8:11], v[188:191], a[104:107], v[8:11]\n"
"v_add_f32 v29, v29, v86\n"
"v_and_b32 v161, 16, v242\n"
"v_mfma_f32_16x16x32_bf16 v[8:11], v[192:195], a[108:111], v[8:11]\n"
"ds_read_b64_tr_b16 v[118:119], v235 offset:18432\n"
"v_and_b32 v160, 0x3f80, v160\n"
"v_mfma_f32_16x16x32_bf16 v[8:11], v[208:211], a[144:147], v[8:11]\n"
"ds_read_b64_tr_b16 v[102:103], v235 offset:2048\n"
"v_add_f32 v27, v27, v85\n"
"v_mfma_f32_16x16x32_bf16 v[8:11], v[212:215], a[148:151], v[8:11]\n"
"ds_read_b64_tr_b16 v[106:107], v231 offset:10240\n"
"v_lshlrev_b32 v160, v161, v160\n"
"v_mfma_f32_16x16x32_bf16 v[8:11], v[216:219], a[152:155], v[8:11]\n"
"ds_read_b64_tr_b16 v[96:97], v231 offset:0\n"
"v_and_b32 v93, s73, v93\n"
"v_mfma_f32_16x16x32_bf16 v[8:11], v[220:223], a[156:159], v[8:11]\n"
"ds_read_b64_tr_b16 v[108:109], v235 offset:8192\n"
"v_add_f32 v41, v41, v92\n"
"v_mfma_f32_16x16x32_bf16 v[24:27], v[220:223], a[172:175], v[24:27]\n"
"v_bfe_i32 v161, v242, 5, 1\n"
"v_lshlrev_b32 v206, 16, v244\n"
"v_mfma_f32_16x16x32_bf16 v[24:27], v[216:219], a[168:171], v[24:27]\n"
"ds_read_b64_tr_b16 v[114:115], v231 offset:18432\n"
"v_mfma_f32_16x16x32_bf16 v[24:27], v[212:215], a[164:167], v[24:27]\n"
"ds_read_b64_tr_b16 v[104:105], v231 offset:8192\n"
"v_lshlrev_b32 v203, 16, v247\n"
"v_mfma_f32_16x16x32_bf16 v[24:27], v[208:211], a[160:163], v[24:27]\n"
"ds_read_b64_tr_b16 v[116:117], v235 offset:16384\n"
"v_add_f32 v48, v48, v206\n"
"v_mfma_f32_16x16x32_bf16 v[24:27], v[192:195], a[124:127], v[24:27]\n"
"ds_read_b64_tr_b16 v[110:111], v235 offset:10240\n"
"v_and_b32 v89, s73, v89\n"
"v_mfma_f32_16x16x32_bf16 v[24:27], v[188:191], a[120:123], v[24:27]\n"
"v_add_f32 v45, v45, v94\n"
"v_add_f32 v47, v47, v95\n"
"v_mfma_f32_16x16x32_bf16 v[24:27], v[184:187], a[116:119], v[24:27]\n"
"ds_read_b64_tr_b16 v[100:101], v235 offset:0\n"
"v_add_f32 v43, v43, v93\n"
"v_mfma_f32_16x16x32_bf16 v[24:27], v[180:183], a[112:115], v[24:27]\n"
"ds_read_b64_tr_b16 v[122:123], v231 offset:26624\n"
"v_add_f32 v46, v46, v207\n"
"v_mfma_f32_16x16x32_bf16 v[40:43], v[180:183], a[128:131], v[40:43]\n"
"v_add_f32 v33, v33, v88\n"
"v_and_b32 v244, s73, v244\n"
"v_mfma_f32_16x16x32_bf16 v[40:43], v[184:187], a[132:135], v[40:43]\n"
"ds_read_b64_tr_b16 v[120:121], v231 offset:24576\n"
"v_lshlrev_b32 v207, 16, v245\n"
"v_mfma_f32_16x16x32_bf16 v[40:43], v[188:191], a[136:139], v[40:43]\n"
"v_and_b32 v163, v160, v161\n"
"v_lshlrev_b32 v202, 16, v246\n"
"v_mfma_f32_16x16x32_bf16 v[40:43], v[192:195], a[140:143], v[40:43]\n"
"ds_read_b64_tr_b16 v[224:225], v235 offset:24576\n"
"v_and_b32 v246, s73, v246\n"
"v_mfma_f32_16x16x32_bf16 v[40:43], v[208:211], a[176:179], v[40:43]\n"
"v_add_f32 v52, v52, v202\n"
"v_and_b32 v247, s73, v247\n"
"v_mfma_f32_16x16x32_bf16 v[40:43], v[212:215], a[180:183], v[40:43]\n"
"ds_read_b64_tr_b16 v[226:227], v235 offset:26624\n"
"v_and_b32 v245, s73, v245\n"
"v_mfma_f32_16x16x32_bf16 v[40:43], v[216:219], a[184:187], v[40:43]\n"
"v_mov_b32 v161, 0\n"
"v_add_f32 v54, v54, v203\n"
"v_mfma_f32_16x16x32_bf16 v[40:43], v[220:223], a[188:191], v[40:43]\n"
"s_barrier\n"
"s_waitcnt lgkmcnt(3)\n"
"v_mfma_f32_16x16x32_bf16 v[12:15], v[96:99], a[96:99], v[12:15]\n"
"ds_read_b64_tr_b16 v[184:185], v232 offset:32768\n"
"v_add_f32 v49, v49, v244\n"
"v_mfma_f32_16x16x32_bf16 v[12:15], v[100:103], a[100:103], v[12:15]\n"
"ds_read_b64_tr_b16 v[216:217], v228 offset:57344\n"
"v_mfma_f32_16x16x32_bf16 v[12:15], v[104:107], a[104:107], v[12:15]\n"
"ds_read_b64_tr_b16 v[188:189], v228 offset:40960\n"
"v_add_f32 v35, v35, v89\n"
"v_mfma_f32_16x16x32_bf16 v[12:15], v[108:111], a[108:111], v[12:15]\n"
"ds_read_b64_tr_b16 v[190:191], v228 offset:43008\n"
"v_mfma_f32_16x16x32_bf16 v[12:15], v[112:115], a[144:147], v[12:15]\n"
"ds_read_b64_tr_b16 v[192:193], v232 offset:40960\n"
"v_add_f32 v53, v53, v246\n"
"v_mfma_f32_16x16x32_bf16 v[12:15], v[116:119], a[148:151], v[12:15]\n"
"s_waitcnt lgkmcnt(5)\n"
"ds_read_b64_tr_b16 v[212:213], v232 offset:49152\n"
"v_add_f32 v51, v51, v245\n"
"v_mfma_f32_16x16x32_bf16 v[12:15], v[120:123], a[152:155], v[12:15]\n"
"ds_read_b64_tr_b16 v[182:183], v228 offset:34816\n"
"v_xor_b32 v162, v160, v163\n"
"v_mfma_f32_16x16x32_bf16 v[12:15], v[224:227], a[156:159], v[12:15]\n"
"global_load_dword v200, v201, s[6:7] sc1\n"
"v_mfma_f32_16x16x32_bf16 v[28:31], v[224:227], a[172:175], v[28:31]\n"
"v_add_f32 v50, v50, v207\n"
"v_add_f32 v55, v55, v247\n"
"v_mfma_f32_16x16x32_bf16 v[28:31], v[120:123], a[168:171], v[28:31]\n"
"s_waitcnt vmcnt(9)\n"
"v_lshlrev_b32 v58, 16, v125\n"
"v_lshlrev_b32 v62, 16, v127\n"
"v_mfma_f32_16x16x32_bf16 v[28:31], v[116:119], a[164:167], v[28:31]\n"
"ds_read_b64_tr_b16 v[186:187], v232 offset:34816\n"
"ds_read_b64_tr_b16 v[214:215], v232 offset:51200\n"
"v_mfma_f32_16x16x32_bf16 v[28:31], v[112:115], a[160:163], v[28:31]\n"
"ds_read_b64_tr_b16 v[194:195], v232 offset:43008\n"
"v_and_b32 v61, s73, v126\n"
"v_mfma_f32_16x16x32_bf16 v[28:31], v[108:111], a[124:127], v[28:31]\n"
"s_waitcnt vmcnt(8)\n"
"ds_read_b64_tr_b16 v[210:211], v228 offset:51200\n"
"v_and_b32 v57, s73, v124\n"
"v_mfma_f32_16x16x32_bf16 v[28:31], v[104:107], a[120:123], v[28:31]\n"
"ds_read_b64_tr_b16 v[222:223], v232 offset:59392\n"
"v_lshlrev_b32 v56, 16, v124\n"
"v_mfma_f32_16x16x32_bf16 v[28:31], v[100:103], a[116:119], v[28:31]\n"
"ds_read_b64_tr_b16 v[180:181], v228 offset:32768\n"
"v_lshlrev_b32 v205, 16, v129\n"
"v_mfma_f32_16x16x32_bf16 v[28:31], v[96:99], a[112:115], v[28:31]\n"
"s_waitcnt vmcnt(7)\n"
"v_and_b32 v59, s73, v125\n"
"v_lshlrev_b32 v206, 16, v130\n"
"v_mfma_f32_16x16x32_bf16 v[44:47], v[96:99], a[128:131], v[44:47]\n"
"ds_read_b64_tr_b16 v[220:221], v232 offset:57344\n"
"v_lshlrev_b32 v204, 16, v128\n"
"v_mfma_f32_16x16x32_bf16 v[44:47], v[100:103], a[132:135], v[44:47]\n"
"ds_read_b64_tr_b16 v[208:209], v228 offset:49152\n"
"v_and_b32 v130, s73, v130\n"
"v_mfma_f32_16x16x32_bf16 v[44:47], v[104:107], a[136:139], v[44:47]\n"
"s_waitcnt vmcnt(6)\n"
"ds_read_b64_tr_b16 v[102:103], v233 offset:34816\n"
"v_add_f32 v58, v58, v205\n"
"v_mfma_f32_16x16x32_bf16 v[44:47], v[108:111], a[140:143], v[44:47]\n"
"v_lshlrev_b32 v207, 16, v131\n"
"v_add_f32 v62, v62, v207\n"
"v_mfma_f32_16x16x32_bf16 v[44:47], v[112:115], a[176:179], v[44:47]\n"
"ds_read_b64_tr_b16 v[218:219], v228 offset:59392\n"
"v_and_b32 v131, s73, v131\n"
"v_mfma_f32_16x16x32_bf16 v[44:47], v[116:119], a[180:183], v[44:47]\n"
"s_waitcnt vmcnt(5)\n"
"ds_read_b64_tr_b16 v[96:97], v229 offset:32768\n"
"v_lshlrev_b32 v205, 16, v135\n"
"v_mfma_f32_16x16x32_bf16 v[44:47], v[120:123], a[184:187], v[44:47]\n"
"v_lshlrev_b32 v202, 16, v132\n"
"v_lshlrev_b32 v207, 16, v137\n"
"v_mfma_f32_16x16x32_bf16 v[44:47], v[224:227], a[188:191], v[44:47]\n"
"s_waitcnt lgkmcnt(5)\n"
"ds_read_b64_tr_b16 v[120:121], v229 offset:57344\n"
"v_add_f32 v56, v56, v204\n"
"v_mfma_f32_16x16x32_bf16 v[0:3], v[180:183], a[48:51], v[0:3]\n"
"s_waitcnt vmcnt(4)\n"
"ds_read_b64_tr_b16 v[116:117], v233 offset:49152\n"
"v_add_f32 v61, v61, v130\n"
"v_mfma_f32_16x16x32_bf16 v[0:3], v[184:187], a[52:55], v[0:3]\n"
"v_lshlrev_b32 v60, 16, v126\n"
"v_lshlrev_b32 v204, 16, v134\n"
"v_mfma_f32_16x16x32_bf16 v[0:3], v[188:191], a[56:59], v[0:3]\n"
"ds_read_b64_tr_b16 v[110:111], v233 offset:43008\n"
"v_add_f32 v56, v56, v202\n"
"v_mfma_f32_16x16x32_bf16 v[0:3], v[192:195], a[60:63], v[0:3]\n"
"s_waitcnt lgkmcnt(5)\n"
"s_waitcnt vmcnt(3)\n"
"ds_read_b64_tr_b16 v[112:113], v229 offset:49152\n"
"v_and_b32 v63, s73, v127\n"
"v_mfma_f32_16x16x32_bf16 v[0:3], v[208:211], a[0:3], v[0:3]\n"
"ds_read_b64_tr_b16 v[224:225], v233 offset:57344\n"
"v_and_b32 v134, s73, v134\n"
"v_mfma_f32_16x16x32_bf16 v[0:3], v[212:215], a[4:7], v[0:3]\n"
"s_waitcnt lgkmcnt(5)\n"
"s_waitcnt vmcnt(2)\n"
"ds_read_b64_tr_b16 v[108:109], v233 offset:40960\n"
"v_and_b32 v129, s73, v129\n"
"v_mfma_f32_16x16x32_bf16 v[0:3], v[216:219], a[8:11], v[0:3]\n"
"v_add_f32 v60, v60, v206\n"
"v_add_f32 v60, v60, v204\n"
"v_mfma_f32_16x16x32_bf16 v[0:3], v[220:223], a[12:15], v[0:3]\n"
"ds_read_b64_tr_b16 v[98:99], v229 offset:34816\n"
"ds_read_b64_tr_b16 v[122:123], v229 offset:59392\n"
"v_mfma_f32_16x16x32_bf16 v[16:19], v[220:223], a[28:31], v[16:19]\n"
"v_mov_b32 v165, 0\n"
"v_add_f32 v59, v59, v129\n"
"v_mfma_f32_16x16x32_bf16 v[16:19], v[216:219], a[24:27], v[16:19]\n"
"v_add_f32 v62, v62, v205\n"
"v_lshlrev_b32 v206, 16, v136\n"
"v_mfma_f32_16x16x32_bf16 v[16:19], v[212:215], a[20:23], v[16:19]\n"
"ds_read_b64_tr_b16 v[104:105], v229 offset:40960\n"
"ds_read_b64_tr_b16 v[226:227], v233 offset:59392\n"
"v_mfma_f32_16x16x32_bf16 v[16:19], v[208:211], a[16:19], v[16:19]\n"
"v_add_f32 v56, v56, v206\n"
"v_lshlrev_b32 v202, 16, v138\n"
"v_mfma_f32_16x16x32_bf16 v[16:19], v[192:195], a[76:79], v[16:19]\n"
"s_waitcnt vmcnt(0)\n"
"ds_read_b64_tr_b16 v[118:119], v233 offset:51200\n"
"v_subrev_f32 v2, s22, v2\n"
"v_mfma_f32_16x16x32_bf16 v[16:19], v[188:191], a[72:75], v[16:19]\n"
"v_subrev_u32 v200, s21, v200\n"
"ds_read_b64_tr_b16 v[114:115], v229 offset:51200\n"
"v_mfma_f32_16x16x32_bf16 v[16:19], v[184:187], a[68:71], v[16:19]\n"
"v_cmp_lt_u32 vcc, s69, v200\n"
"ds_read_b64_tr_b16 v[100:101], v233 offset:32768\n"
"v_mfma_f32_16x16x32_bf16 v[16:19], v[180:183], a[64:67], v[16:19]\n"
"v_lshlrev_b32 v206, 16, v142\n"
"s_cbranch_vccnz LGR_%=\nLGRK_%=:\n"
"v_mfma_f32_16x16x32_bf16 v[32:35], v[180:183], a[80:83], v[32:35]\n"
"v_subrev_f32 v3, s22, v3\n"
"s_cmp_eq_u32 s93, 0\ns_cbranch_scc1 LEP_%=\nLEPK_%=:\n"
"v_mfma_f32_16x16x32_bf16 v[32:35], v[184:187], a[84:87], v[32:35]\n"
"s_bfe_u32 s20, s89, 65536\ns_lshl_b32 s20, s20, 13\ns_add_u32 s20, s20, 65536\n"
"ds_read_b64_tr_b16 v[106:107], v229 offset:43008\n"
"v_and_b32 v128, s73, v128\n"
"v_mfma_f32_16x16x32_bf16 v[32:35], v[188:191], a[88:91], v[32:35]\n"
"buffer_load_dwordx4 v[72:75], v236, s[32:35], s20 offen sc1\n"
"v_mfma_f32_16x16x32_bf16 v[32:35], v[192:195], a[92:95], v[32:35]\n"
"v_lshlrev_b32 v203, 16, v133\n"
"v_lshlrev_b32 v205, 16, v141\n"
"v_mfma_f32_16x16x32_bf16 v[32:35], v[208:211], a[32:35], v[32:35]\n"
"s_bfe_u32 s20, s89, 65540\ns_lshl_b32 s20, s20, 13\ns_add_u32 s20, s20, 1114112\n"
"v_lshlrev_b32 v204, 16, v140\n"
"v_add_f32 v56, v56, v204\n"
"v_mfma_f32_16x16x32_bf16 v[32:35], v[212:215], a[36:39], v[32:35]\n"
"buffer_load_dwordx4 v[76:79], v236, s[32:35], s20 offen sc1\n"
"v_mfma_f32_16x16x32_bf16 v[32:35], v[216:219], a[40:43], v[32:35]\n"
"v_and_b32 v136, s73, v136\n"
"v_subrev_f32 v17, s22, v17\n"
"v_mfma_f32_16x16x32_bf16 v[32:35], v[220:223], a[44:47], v[32:35]\n"
"s_bfe_u32 s20, s89, 65544\ns_lshl_b32 s20, s20, 13\ns_add_u32 s20, s20, 2162688\n"
"v_add_f32 v61, v61, v134\n"
"v_and_b32 v141, s73, v141\n"
"v_mfma_f32_16x16x32_bf16 v[48:51], v[220:223], a[228:231], v[48:51]\n"
"buffer_load_dwordx4 v[80:83], v236, s[32:35], s20 offen sc1\n"
"v_mfma_f32_16x16x32_bf16 v[48:51], v[216:219], a[224:227], v[48:51]\n"
"v_add_f32 v58, v58, v203\n"
"v_subrev_f32 v19, s22, v19\n"
"v_mfma_f32_16x16x32_bf16 v[48:51], v[212:215], a[212:215], v[48:51]\n"
"s_bfe_u32 s20, s89, 65548\ns_lshl_b32 s20, s20, 13\ns_add_u32 s20, s20, 3211264\n"
"v_and_b32 v140, s73, v140\n"
"v_lshlrev_b32 v204, 16, v146\n"
"v_mfma_f32_16x16x32_bf16 v[48:51], v[208:211], a[208:211], v[48:51]\n"
"buffer_load_dwordx4 v[84:87], v236, s[32:35], s20 offen sc1\n"
"v_mfma_f32_16x16x32_bf16 v[48:51], v[192:195], a[236:239], v[48:51]\n"
"v_add_f32 v57, v57, v128\n"
"v_subrev_f32 v34, s22, v34\n"
"v_mfma_f32_16x16x32_bf16 v[48:51], v[188:191], a[232:235], v[48:51]\n"
"s_bfe_u32 s20, s89, 65552\ns_lshl_b32 s20, s20, 13\ns_add_u32 s20, s20, 4259840\n"
"ds_read_b64_tr_b16 v[214:215], v234 offset:51200\n"
"v_and_b32 v132, s73, v132\n"
"v_mfma_f32_16x16x32_bf16 v[48:51], v[184:187], a[220:223], v[48:51]\n"
"buffer_load_dwordx4 v[88:91], v236, s[32:35], s20 offen sc1\n"
"v_mfma_f32_16x16x32_bf16 v[48:51], v[180:183], a[216:219], v[48:51]\n"
"s_waitcnt lgkmcnt(1)\n"
"v_add_f32 v60, v60, v202\n"
"v_add_f32 v60, v60, v206\n"
"v_mfma_f32_16x16x32_bf16 v[4:7], v[96:99], a[48:51], v[4:7]\n"
"s_bfe_u32 s20, s89, 65556\ns_lshl_b32 s20, s20, 13\ns_add_u32 s20, s20, 5308416\n"
"v_lshlrev_b32 v206, 16, v148\n"
"v_subrev_f32 v35, s22, v35\n"
"v_mfma_f32_16x16x32_bf16 v[4:7], v[100:103], a[52:55], v[4:7]\n"
"buffer_load_dwordx4 v[92:95], v236, s[32:35], s20 offen sc1\n"
"v_mfma_f32_16x16x32_bf16 v[4:7], v[104:107], a[56:59], v[4:7]\n"
"v_add_f32 v60, v60, v204\n"
"v_subrev_f32 v0, s22, v0\n"
"v_mfma_f32_16x16x32_bf16 v[4:7], v[108:111], a[60:63], v[4:7]\n"
"s_bfe_u32 s20, s89, 65560\ns_lshl_b32 s20, s20, 13\ns_add_u32 s20, s20, 6356992\n"
"v_add_f32 v57, v57, v132\n"
"v_subrev_f32 v33, s22, v33\n"
"v_mfma_f32_16x16x32_bf16 v[4:7], v[112:115], a[0:3], v[4:7]\n"
"buffer_load_dwordx4 v[244:247], v236, s[32:35], s20 offen sc1\n"
"v_mfma_f32_16x16x32_bf16 v[4:7], v[116:119], a[4:7], v[4:7]\n"
"v_and_b32 v138, s73, v138\n"
"v_subrev_f32 v18, s22, v18\n"
"v_mfma_f32_16x16x32_bf16 v[4:7], v[120:123], a[8:11], v[4:7]\n"
"s_bfe_u32 s20, s89, 65564\ns_lshl_b32 s20, s20, 13\ns_add_u32 s20, s20, 7405568\n"
"v_and_b32 v135, s73, v135\n"
"v_subrev_f32 v49, s22, v49\n"
"v_mfma_f32_16x16x32_bf16 v[4:7], v[224:227], a[12:15], v[4:7]\n"
"buffer_load_dwordx4 v[156:159], v236, s[32:35], s20 offen sc1\n"
"v_add_f32 v58, v58, v207\n"
"v_lshlrev_b32 v203, 16, v139\n"
"v_add_f32 v58, v58, v205\n"
"v_and_b32 v142, s73, v142\n"
"v_lshlrev_b32 v202, 16, v144\n"
"v_add_f32 v56, v56, v202\n"
"v_add_f32 v56, v56, v206\n"
"v_subrev_f32 v1, s22, v1\n"
"v_mfma_f32_16x16x32_bf16 v[20:23], v[224:227], a[28:31], v[20:23]\n"
"v_add_f32 v63, v63, v131\n"
"v_lshlrev_b32 v204, 16, v152\n"
"v_mfma_f32_16x16x32_bf16 v[20:23], v[120:123], a[24:27], v[20:23]\n"
"v_and_b32 v133, s73, v133\n"
"v_add_f32 v59, v59, v133\n"
"v_mfma_f32_16x16x32_bf16 v[20:23], v[116:119], a[20:23], v[20:23]\n"
"ds_read_b64_tr_b16 v[218:219], v230 offset:59392\n"
"v_and_b32 v137, s73, v137\n"
"v_mfma_f32_16x16x32_bf16 v[20:23], v[112:115], a[16:19], v[20:23]\n"
"v_and_b32 v148, s73, v148\n"
"v_subrev_f32 v16, s22, v16\n"
"v_mfma_f32_16x16x32_bf16 v[20:23], v[108:111], a[76:79], v[20:23]\n"
"v_add_f32 v62, v62, v203\n"
"v_subrev_f32 v6, s22, v6\n"
"v_mfma_f32_16x16x32_bf16 v[20:23], v[104:107], a[72:75], v[20:23]\n"
"v_add_f32 v57, v57, v136\n"
"v_lshlrev_b32 v205, 16, v147\n"
"v_mfma_f32_16x16x32_bf16 v[20:23], v[100:103], a[68:71], v[20:23]\n"
"ds_read_b64_tr_b16 v[194:195], v234 offset:43008\n"
"v_subrev_f32 v32, s22, v32\n"
"v_mfma_f32_16x16x32_bf16 v[20:23], v[96:99], a[64:67], v[20:23]\n"
"ds_read_b64_tr_b16 v[184:185], v234 offset:32768\n"
"v_add_f32 v59, v59, v137\n"
"v_and_b32 v139, s73, v139\n"
"v_add_f32 v59, v59, v141\n"
"v_lshlrev_b32 v207, 16, v143\n"
"v_lshlrev_b32 v203, 16, v145\n"
"v_and_b32 v145, s73, v145\n"
"v_add_f32 v58, v58, v203\n"
"v_add_f32 v59, v59, v145\n"
"v_and_b32 v147, s73, v147\n"
"v_lshlrev_b32 v203, 16, v151\n"
"v_subrev_f32 v4, s22, v4\n"
"v_subrev_f32 v5, s22, v5\n"
"v_subrev_f32 v7, s22, v7\n"
"v_cvt_pk_bf16_f32 v0, v0, v1\n"
"v_cvt_pk_bf16_f32 v1, v2, v3\n"
"v_cvt_pk_bf16_f32 v3, v6, v7\n"
"v_mfma_f32_16x16x32_bf16 v[36:39], v[96:99], a[80:83], v[36:39]\n"
"v_add_f32 v56, v56, v204\n"
"v_cvt_pk_bf16_f32 v2, v4, v5\n"
"v_mfma_f32_16x16x32_bf16 v[36:39], v[100:103], a[84:87], v[36:39]\n"
"ds_read_b64_tr_b16 v[192:193], v234 offset:40960\n"
"v_add_f32 v61, v61, v138\n"
"v_mfma_f32_16x16x32_bf16 v[36:39], v[104:107], a[88:91], v[36:39]\n"
"v_add_f32 v57, v57, v140\n"
"v_subrev_f32 v23, s22, v23\n"
"v_mfma_f32_16x16x32_bf16 v[36:39], v[108:111], a[92:95], v[36:39]\n"
"ds_read_b64_tr_b16 v[180:181], v230 offset:32768\n"
"v_and_b32 v144, s73, v144\n"
"v_mfma_f32_16x16x32_bf16 v[36:39], v[112:115], a[32:35], v[36:39]\n"
"ds_read_b64_tr_b16 v[210:211], v230 offset:51200\n"
"v_add_f32 v62, v62, v207\n"
"v_add_f32 v62, v62, v205\n"
"v_lshlrev_b32 v207, 16, v149\n"
"v_add_f32 v58, v58, v207\n"
"v_lshlrev_b32 v202, 16, v150\n"
"v_permlane16_swap_b32 v0, v2\n"
"v_permlane16_swap_b32 v1, v3\n"
"v_subrev_f32 v20, s22, v20\n"
"v_cvt_pk_bf16_f32 v16, v16, v17\n"
"v_mfma_f32_16x16x32_bf16 v[36:39], v[116:119], a[36:39], v[36:39]\n"
"ds_read_b64_tr_b16 v[186:187], v234 offset:34816\n"
"v_add_f32 v63, v63, v135\n"
"v_mfma_f32_16x16x32_bf16 v[36:39], v[120:123], a[40:43], v[36:39]\n"
"ds_read_b64_tr_b16 v[188:189], v230 offset:40960\n"
"buffer_store_dwordx4 v[0:3], v238, s[36:39], 0 offen sc0 sc1\n"
"v_and_b32 v143, s73, v143\n"
"v_and_b32 v146, s73, v146\n"
"v_and_b32 v149, s73, v149\n"
"v_lshlrev_b32 v205, 16, v153\n"
"v_and_b32 v153, s73, v153\n"
"v_lshlrev_b32 v206, 16, v154\n"
"v_lshlrev_b32 v207, 16, v155\n"
"v_subrev_f32 v21, s22, v21\n"
"v_subrev_f32 v22, s22, v22\n"
"v_cvt_pk_bf16_f32 v17, v18, v19\n"
"v_cvt_pk_bf16_f32 v18, v20, v21\n"
"v_cvt_pk_bf16_f32 v32, v32, v33\n"
"v_mfma_f32_16x16x32_bf16 v[36:39], v[224:227], a[44:47], v[36:39]\n"
"ds_read_b64_tr_b16 v[182:183], v230 offset:34816\n"
"v_add_f32 v63, v63, v139\n"
"v_add_f32 v61, v61, v142\n"
"v_add_f32 v57, v57, v144\n"
"v_add_f32 v57, v57, v148\n"
"v_add_f32 v59, v59, v149\n"
"v_cvt_pk_bf16_f32 v19, v22, v23\n"
"v_permlane16_swap_b32 v16, v18\n"
"v_cvt_pk_bf16_f32 v33, v34, v35\n"
"v_mfma_f32_16x16x32_bf16 v[52:55], v[224:227], a[228:231], v[52:55]\n"
"ds_read_b64_tr_b16 v[190:191], v230 offset:43008\n"
"v_add_f32 v59, v59, v153\n"
"v_mfma_f32_16x16x32_bf16 v[52:55], v[120:123], a[224:227], v[52:55]\n"
"v_permlane16_swap_b32 v17, v19\n"
"v_mfma_f32_16x16x32_bf16 v[52:55], v[116:119], a[212:215], v[52:55]\n"
"ds_read_b64_tr_b16 v[208:209], v230 offset:49152\n"
"v_subrev_f32 v36, s22, v36\n"
"v_mfma_f32_16x16x32_bf16 v[52:55], v[112:115], a[208:211], v[52:55]\n"
"ds_read_b64_tr_b16 v[222:223], v234 offset:59392\n"
"buffer_store_dwordx4 v[16:19], v238, s[36:39], s52 offen sc0 sc1\n"
"v_add_f32 v63, v63, v143\n"
"v_add_f32 v60, v60, v202\n"
"v_add_f32 v62, v62, v203\n"
"v_and_b32 v152, s73, v152\n"
"v_subrev_f32 v38, s22, v38\n"
"v_subrev_f32 v39, s22, v39\n"
"v_mfma_f32_16x16x32_bf16 v[52:55], v[108:111], a[236:239], v[52:55]\n"
"v_and_b32 v155, s73, v155\n"
"v_cvt_pk_bf16_f32 v35, v38, v39\n"
"v_mfma_f32_16x16x32_bf16 v[52:55], v[104:107], a[232:235], v[52:55]\n"
"ds_read_b64_tr_b16 v[216:217], v230 offset:57344\n"
"v_subrev_f32 v37, s22, v37\n"
"v_mfma_f32_16x16x32_bf16 v[52:55], v[100:103], a[220:223], v[52:55]\n"
"v_and_b32 v150, s73, v150\n"
"v_and_b32 v154, s73, v154\n"
"v_mfma_f32_16x16x32_bf16 v[52:55], v[96:99], a[216:219], v[52:55]\n"
"s_waitcnt lgkmcnt(4)\n"
"ds_read_b64_tr_b16 v[212:213], v234 offset:49152\n"
"ds_read_b64_tr_b16 v[220:221], v234 offset:57344\n"
"v_add_f32 v63, v63, v147\n"
"v_and_b32 v151, s73, v151\n"
"v_add_f32 v63, v63, v151\n"
"v_add_f32 v57, v57, v152\n"
"v_add_f32 v58, v58, v205\n"
"v_add_f32 v60, v60, v206\n"
"v_add_f32 v62, v62, v207\n"
"v_cvt_pk_bf16_f32 v34, v36, v37\n"
"v_permlane16_swap_b32 v33, v35\n"
"v_mfma_f32_16x16x32_bf16 v[8:11], v[180:183], a[48:51], v[8:11]\n"
"v_cvt_pk_bf16_f32 v56, v56, v57\n"
"v_cvt_pk_bf16_f32 v57, v58, v59\n"
"v_mfma_f32_16x16x32_bf16 v[8:11], v[184:187], a[52:55], v[8:11]\n"
"s_waitcnt lgkmcnt(5)\n"
"v_permlane16_swap_b32 v32, v34\n"
"v_mfma_f32_16x16x32_bf16 v[8:11], v[188:191], a[56:59], v[8:11]\n"
"buffer_store_dwordx4 v[32:35], v238, s[36:39], s53 offen sc0 sc1\n"
"ds_read_b64_tr_b16 v[120:121], v231 offset:57344\n"
"v_add_f32 v61, v61, v146\n"
"v_add_f32 v61, v61, v150\n"
"v_add_f32 v61, v61, v154\n"
"v_add_f32 v63, v63, v155\n"
"v_cvt_pk_bf16_f32 v58, v60, v61\n"
"v_cvt_pk_bf16_f32 v59, v62, v63\n"
"v_mfma_f32_16x16x32_bf16 v[8:11], v[192:195], a[60:63], v[8:11]\n"
"s_waitcnt lgkmcnt(4)\n"
"v_permlane16_swap_b32 v57, v59\n"
"v_mfma_f32_16x16x32_bf16 v[8:11], v[208:211], a[0:3], v[8:11]\n"
"s_waitcnt lgkmcnt(2)\n"
"v_permlane16_swap_b32 v56, v58\n"
"v_mfma_f32_16x16x32_bf16 v[8:11], v[212:215], a[4:7], v[8:11]\n"
"buffer_store_dwordx4 v[56:59], v253, s[36:39], 0 offen sc0 sc1\n"
"v_subrev_f32 v48, s22, v48\n"
"v_subrev_f32 v50, s22, v50\n"
"v_subrev_f32 v51, s22, v51\n"
"v_mfma_f32_16x16x32_bf16 v[8:11], v[216:219], a[8:11], v[8:11]\n"
"s_waitcnt lgkmcnt(1)\n"
"ds_read_b64_tr_b16 v[112:113], v231 offset:49152\n"
"v_subrev_f32 v54, s22, v54\n"
"v_mfma_f32_16x16x32_bf16 v[8:11], v[220:223], a[12:15], v[8:11]\n"
"ds_read_b64_tr_b16 v[100:101], v235 offset:32768\n"
"v_subrev_f32 v52, s22, v52\n"
"v_mfma_f32_16x16x32_bf16 v[24:27], v[220:223], a[28:31], v[24:27]\n"
"ds_read_b64_tr_b16 v[102:103], v235 offset:34816\n"
"v_subrev_f32 v53, s22, v53\n"
"v_mfma_f32_16x16x32_bf16 v[24:27], v[216:219], a[24:27], v[24:27]\n"
"v_cvt_pk_bf16_f32 v48, v48, v49\n"
"v_cvt_pk_bf16_f32 v49, v50, v51\n"
"v_mfma_f32_16x16x32_bf16 v[24:27], v[212:215], a[20:23], v[24:27]\n"
"ds_read_b64_tr_b16 v[96:97], v231 offset:32768\n"
"v_cvt_pk_bf16_f32 v50, v52, v53\n"
"v_mfma_f32_16x16x32_bf16 v[24:27], v[208:211], a[16:19], v[24:27]\n"
"s_nop 0\n"
"v_permlane16_swap_b32 v48, v50\n"
"v_mfma_f32_16x16x32_bf16 v[24:27], v[192:195], a[76:79], v[24:27]\n"
"ds_read_b64_tr_b16 v[116:117], v235 offset:49152\n"
"v_subrev_f32 v55, s22, v55\n"
"v_mfma_f32_16x16x32_bf16 v[24:27], v[188:191], a[72:75], v[24:27]\n"
"ds_read_b64_tr_b16 v[98:99], v231 offset:34816\n"
"v_cvt_pk_bf16_f32 v51, v54, v55\n"
"v_mfma_f32_16x16x32_bf16 v[24:27], v[184:187], a[68:71], v[24:27]\n"
"s_nop 0\n"
"v_permlane16_swap_b32 v49, v51\n"
"v_mfma_f32_16x16x32_bf16 v[24:27], v[180:183], a[64:67], v[24:27]\n"
"buffer_store_dwordx4 v[48:51], v252, s[36:39], 0 offen sc0 sc1\n"
"ds_read_b64_tr_b16 v[104:105], v231 offset:40960\n"
"v_mfma_f32_16x16x32_bf16 v[40:43], v[180:183], a[80:83], v[40:43]\n"
"ds_read_b64_tr_b16 v[106:107], v231 offset:43008\n"
"ds_read_b64_tr_b16 v[108:109], v235 offset:40960\n"
"v_mfma_f32_16x16x32_bf16 v[40:43], v[184:187], a[84:87], v[40:43]\n"
"ds_read_b64_tr_b16 v[110:111], v235 offset:43008\n"
"v_mfma_f32_16x16x32_bf16 v[40:43], v[188:191], a[88:91], v[40:43]\n"
"ds_read_b64_tr_b16 v[114:115], v231 offset:51200\n"
"v_subrev_f32 v10, s22, v10\n"
"v_mfma_f32_16x16x32_bf16 v[40:43], v[192:195], a[92:95], v[40:43]\n"
"ds_read_b64_tr_b16 v[118:119], v235 offset:51200\n"
"v_mfma_f32_16x16x32_bf16 v[40:43], v[208:211], a[32:35], v[40:43]\n"
"ds_read_b64_tr_b16 v[122:123], v231 offset:59392\n"
"v_subrev_f32 v24, s22, v24\n"
"v_mfma_f32_16x16x32_bf16 v[40:43], v[212:215], a[36:39], v[40:43]\n"
"ds_read_b64_tr_b16 v[224:225], v235 offset:57344\n"
"ds_read_b64_tr_b16 v[226:227], v235 offset:59392\n"
"v_mfma_f32_16x16x32_bf16 v[40:43], v[216:219], a[40:43], v[40:43]\n"
"v_mfma_f32_16x16x32_bf16 v[40:43], v[220:223], a[44:47], v[40:43]\n"
"s_waitcnt lgkmcnt(8)\n"
"v_mov_b32 v160, 0\n"
"v_subrev_f32 v11, s22, v11\n"
"v_mfma_f32_16x16x32_bf16 v[12:15], v[96:99], a[48:51], v[12:15]\n"
"v_subrev_f32 v8, s22, v8\n"
"v_subrev_f32 v9, s22, v9\n"
"v_mfma_f32_16x16x32_bf16 v[12:15], v[100:103], a[52:55], v[12:15]\n"
"s_waitcnt lgkmcnt(5)\n"
"v_cvt_pk_bf16_f32 v8, v8, v9\n"
"v_mfma_f32_16x16x32_bf16 v[12:15], v[104:107], a[56:59], v[12:15]\n"
"v_subrev_f32 v27, s22, v27\n"
"v_mfma_f32_16x16x32_bf16 v[12:15], v[108:111], a[60:63], v[12:15]\n"
"s_waitcnt lgkmcnt(3)\n"
"v_mfma_f32_16x16x32_bf16 v[12:15], v[112:115], a[0:3], v[12:15]\n"
"v_mfma_f32_16x16x32_bf16 v[12:15], v[116:119], a[4:7], v[12:15]\n"
"s_waitcnt lgkmcnt(0)\n"
"v_mfma_f32_16x16x32_bf16 v[12:15], v[120:123], a[8:11], v[12:15]\n"
"v_subrev_f32 v40, s22, v40\n"
"v_mfma_f32_16x16x32_bf16 v[12:15], v[224:227], a[12:15], v[12:15]\n"
"s_waitcnt vmcnt(5)\n"
"v_mfma_f32_16x16x32_bf16 v[64:67], v[162:165], v[72:75], 0\n"
"v_mfma_f32_16x16x32_bf16 v[64:67], v[162:165], v[76:79], v[64:67]\n"
"v_cvt_pk_bf16_f32 v9, v10, v11\n"
"v_mfma_f32_16x16x32_bf16 v[64:67], v[162:165], v[80:83], v[64:67]\n"
"s_nop 2\n"
"v_subrev_f32 v12, s22, v12\n"
"v_mfma_f32_16x16x32_bf16 v[64:67], v[162:165], v[84:87], v[64:67]\n"
"v_subrev_f32 v25, s22, v25\n"
"v_mfma_f32_16x16x32_bf16 v[64:67], v[162:165], v[88:91], v[64:67]\n"
"v_subrev_f32 v13, s22, v13\n"
"v_mfma_f32_16x16x32_bf16 v[64:67], v[162:165], v[92:95], v[64:67]\n"
"v_subrev_f32 v15, s22, v15\n"
"v_mfma_f32_16x16x32_bf16 v[64:67], v[162:165], v[244:247], v[64:67]\n"
"v_subrev_f32 v14, s22, v14\n"
"v_mfma_f32_16x16x32_bf16 v[64:67], v[162:165], v[156:159], v[64:67]\n"
"v_subrev_f32 v42, s22, v42\n"
"v_mfma_f32_16x16x32_bf16 v[68:71], v[160:163], v[72:75], 0\n"
"v_cvt_pk_bf16_f32 v11, v14, v15\n"
"v_subrev_f32 v41, s22, v41\n"
"v_mfma_f32_16x16x32_bf16 v[68:71], v[160:163], v[76:79], v[68:71]\n"
"v_cvt_pk_bf16_f32 v10, v12, v13\n"
"v_cvt_pk_bf16_f32 v24, v24, v25\n"
"v_mfma_f32_16x16x32_bf16 v[68:71], v[160:163], v[80:83], v[68:71]\n"
"v_mfma_f32_16x16x32_bf16 v[68:71], v[160:163], v[84:87], v[68:71]\n"
"v_permlane16_swap_b32 v9, v11\n"
"v_mfma_f32_16x16x32_bf16 v[68:71], v[160:163], v[88:91], v[68:71]\n"
"v_mfma_f32_16x16x32_bf16 v[68:71], v[160:163], v[92:95], v[68:71]\n"
"v_cvt_pk_bf16_f32 v64, v64, v65\n"
"v_cvt_pk_bf16_f32 v40, v40, v41\n"
"v_mfma_f32_16x16x32_bf16 v[68:71], v[160:163], v[244:247], v[68:71]\n"
"v_permlane16_swap_b32 v8, v10\n"
"v_mfma_f32_16x16x32_bf16 v[68:71], v[160:163], v[156:159], v[68:71]\n"
"buffer_store_dwordx4 v[8:11], v239, s[36:39], 0 offen sc0 sc1\n"
"v_mfma_f32_16x16x32_bf16 v[28:31], v[224:227], a[28:31], v[28:31]\n"
"v_mfma_f32_16x16x32_bf16 v[28:31], v[120:123], a[24:27], v[28:31]\n"
"v_cvt_pk_bf16_f32 v65, v66, v67\n"
"v_subrev_f32 v43, s22, v43\n"
"v_mfma_f32_16x16x32_bf16 v[28:31], v[116:119], a[20:23], v[28:31]\n"
"v_mfma_f32_16x16x32_bf16 v[28:31], v[112:115], a[16:19], v[28:31]\n"
"v_mfma_f32_16x16x32_bf16 v[28:31], v[108:111], a[76:79], v[28:31]\n"
"v_cvt_pk_bf16_f32 v66, v68, v69\n"
"v_cvt_pk_bf16_f32 v67, v70, v71\n"
"v_mfma_f32_16x16x32_bf16 v[28:31], v[104:107], a[72:75], v[28:31]\n"
"v_permlane16_swap_b32 v64, v66\n"
"v_mfma_f32_16x16x32_bf16 v[28:31], v[100:103], a[68:71], v[28:31]\n"
"v_permlane16_swap_b32 v65, v67\n"
"v_mfma_f32_16x16x32_bf16 v[28:31], v[96:99], a[64:67], v[28:31]\n"
"buffer_store_dwordx4 v[64:67], v253, s[36:39], 0 offen offset:128 sc0 sc1\n"
"v_cvt_pk_bf16_f32 v41, v42, v43\n"
"v_mfma_f32_16x16x32_bf16 v[44:47], v[96:99], a[80:83], v[44:47]\n"
"v_subrev_f32 v26, s22, v26\n"
"v_cvt_pk_bf16_f32 v25, v26, v27\n"
"v_mfma_f32_16x16x32_bf16 v[44:47], v[100:103], a[84:87], v[44:47]\n"
"v_mfma_f32_16x16x32_bf16 v[44:47], v[104:107], a[88:91], v[44:47]\n"
"s_nop 0\n"
"v_subrev_f32 v30, s22, v30\n"
"v_subrev_f32 v31, s22, v31\n"
"v_mfma_f32_16x16x32_bf16 v[44:47], v[108:111], a[92:95], v[44:47]\n"
"v_subrev_f32 v28, s22, v28\n"
"v_subrev_f32 v29, s22, v29\n"
"v_mfma_f32_16x16x32_bf16 v[44:47], v[112:115], a[32:35], v[44:47]\n"
"v_cvt_pk_bf16_f32 v26, v28, v29\n"
"v_cvt_pk_bf16_f32 v27, v30, v31\n"
"v_mfma_f32_16x16x32_bf16 v[44:47], v[116:119], a[36:39], v[44:47]\n"
"s_nop 0\n"
"v_permlane16_swap_b32 v25, v27\n"
"v_mfma_f32_16x16x32_bf16 v[44:47], v[120:123], a[40:43], v[44:47]\n"
"v_permlane16_swap_b32 v24, v26\n"
"v_mfma_f32_16x16x32_bf16 v[44:47], v[224:227], a[44:47], v[44:47]\n"
"buffer_store_dwordx4 v[24:27], v239, s[36:39], s52 offen sc0 sc1\n"
"s_nop 6\n"
"v_subrev_f32 v44, s22, v44\n"
"v_subrev_f32 v45, s22, v45\n"
"v_subrev_f32 v46, s22, v46\n"
"v_subrev_f32 v47, s22, v47\n"
"v_cvt_pk_bf16_f32 v42, v44, v45\n"
"v_cvt_pk_bf16_f32 v43, v46, v47\n"
"s_nop 0\n"
"v_permlane16_swap_b32 v40, v42\n"
"v_permlane16_swap_b32 v41, v43\n"
"buffer_store_dwordx4 v[40:43], v239, s[36:39], s53 offen sc0 sc1\n"
"s_branch LFIN_%=\n"
"LSW_%=:\n"
"s_add_u32 s16, s8, s95\n"
"s_addc_u32 s17, s9, 0\n"
"s_sub_u32 s18, 655360, s95\n"
"s_add_u32 s20, s8, s100\n"
"s_addc_u32 s21, s9, 0\n"
"s_sub_u32 s22, 655360, s100\n"
"s_mov_b32 s56, 0\n"
"s_mov_b32 s55, 0\n"
"s_mov_b32 s58, 0\n"
"s_mov_b32 s57, 0\n"
"s_mov_b32 s59, 163840\n"
"s_mov_b32 s52, 9216\n"
"s_mov_b32 s53, 18432\n"
"s_mov_b32 s54, 27648\n"
"v_lshrrev_b32 v206, 7, v242\n"
"v_mul_u32_u24 v206, 1280, v206\n"
"v_lshrrev_b32 v240, 3, v242\n"
"v_and_b32 v240, 0x60, v240\n"
"v_and_b32 v207, 0x70, v242\n"
"v_xor_b32 v240, v240, v207\n"
"v_add_u32 v240, v206, v240\n"
"v_mov_b32 v207, v240\n"
"buffer_load_dword v252, v240, s[16:19], 0 offen\n"
"s_branch LSWK_%=\n"
"LGP_%=:\n"
"s_mov_b32 s80, 0x40000\n"
"LGPa_%=:\n"
"s_sleep 1\n"
"global_load_dword v200, v201, s[6:7] sc1\n"
"s_sub_u32 s80, s80, 1\n"
"s_cmp_eq_u32 s80, 0\n"
"s_waitcnt vmcnt(0)\n"
"s_cbranch_scc1 LGPK_%=\n"
"v_subrev_u32 v200, s21, v200\n"
"v_cmp_lt_u32 vcc, s69, v200\n"
"s_nop 4\n"
"s_cbranch_vccnz LGPa_%=\n"
"s_branch LGPK_%=\n"
"LGQ_%=:\n"
"s_mov_b32 s80, 0x40000\n"
"LGQa_%=:\n"
"s_sleep 1\n"
"global_load_dword v200, v201, s[6:7] sc1\n"
"s_sub_u32 s80, s80, 1\n"
"s_cmp_eq_u32 s80, 0\n"
"s_waitcnt vmcnt(0)\n"
"s_cbranch_scc1 LGQK_%=\n"
"v_subrev_u32 v200, s21, v200\n"
"v_cmp_lt_u32 vcc, s69, v200\n"
"s_nop 4\n"
"s_cbranch_vccnz LGQa_%=\n"
"s_branch LGQK_%=\n"
"LGR_%=:\n"
"s_mov_b32 s80, 0x40000\n"
"LGRa_%=:\n"
"s_sleep 1\n"
"global_load_dword v200, v201, s[6:7] sc1\n"
"s_sub_u32 s80, s80, 1\n"
"s_cmp_eq_u32 s80, 0\n"
"s_waitcnt vmcnt(0)\n"
"s_cbranch_scc1 LGRK_%=\n"
"v_subrev_u32 v200, s21, v200\n"
"v_cmp_lt_u32 vcc, s69, v200\n"
"s_nop 4\n"
"s_cbranch_vccnz LGRa_%=\n"
"s_branch LGRK_%=\n"
"LGV_%=:\n"
"s_mov_b32 s80, 0x40000\n"
"LGVa_%=:\n"
"s_sleep 1\n"
"global_load_dword v200, v201, s[6:7] sc1\n"
"s_sub_u32 s80, s80, 1\n"
"s_cmp_eq_u32 s80, 0\n"
"s_waitcnt vmcnt(0)\n"
"s_cbranch_scc1 LGVK_%=\n"
"v_subrev_u32 v200, s21, v200\n"
"v_cmp_lt_u32 vcc, s69, v200\n"
"s_nop 4\n"
"s_cbranch_vccnz LGVa_%=\n"
"s_branch LGVK_%=\n"
"LEP_%=:\n"
"s_add_u32 s92, s92, 1\n"
"v_mov_b32 v200, s92\n"
"v_mov_b32 v255, 0\n"
"s_mov_b64 exec, 1\n"
"global_store_dword v255, v200, s[6:7] offset:128 sc1\n"
"s_mov_b64 exec, -1\n"
"s_branch LEPK_%=\n"
"LPCE0_%=:\n"
"s_memrealtime s[80:81]\n"
"s_sub_u32 s82, s67, s66\n"
"s_cmp_gt_i32 s82, 0\n"
"s_cselect_b32 s83, s67, s66\n"
"s_waitcnt lgkmcnt(0)\n"
"s_mov_b32 s63, 0\n"
"s_mov_b32 s62, 118\n"
"s_sub_u32 s82, s83, s80\n"
"s_cmp_gt_i32 s82, 3\n"
"s_cbranch_scc0 LPEE0_%=\n"
"LPDE0_%=:\n"
"s_sleep 1\n"
"s_memrealtime s[80:81]\n"
"s_waitcnt lgkmcnt(0)\n"
"s_sub_u32 s82, s83, s80\n"
"s_cmp_gt_i32 s82, 3\n"
"s_cbranch_scc1 LPDE0_%=\n"
"LPEE0_%=:\n"
"s_add_u32 s66, s66, s72\n"
"s_add_u32 s66, s66, 4\n"
"s_add_u32 s67, s80, s72\n"
"s_sub_u32 s72, s72, 3\n"
"s_max_u32 s72, s72, s62\n"
"s_branch LPKE0_%=\n"
"LPCE1_%=:\n"
"s_memrealtime s[80:81]\n"
"s_sub_u32 s82, s67, s66\n"
"s_cmp_gt_i32 s82, 0\n"
"s_cselect_b32 s83, s67, s66\n"
"s_waitcnt lgkmcnt(0)\n"
"s_add_u32 s72, s62, 16\n"
"s_sub_u32 s82, s83, s80\n"
"s_cmp_gt_i32 s82, 3\n"
"s_cbranch_scc0 LPEE1_%=\n"
"LPDE1_%=:\n"
"s_sleep 1\n"
"s_memrealtime s[80:81]\n"
"s_waitcnt lgkmcnt(0)\n"
"s_sub_u32 s82, s83, s80\n"
"s_cmp_gt_i32 s82, 3\n"
"s_cbranch_scc1 LPDE1_%=\n"
"LPEE1_%=:\n"
"s_add_u32 s66, s66, s72\n"
"s_add_u32 s66, s66, 4\n"
"s_add_u32 s67, s80, s72\n"
"s_sub_u32 s72, s72, 3\n"
"s_max_u32 s72, s72, s62\n"
"s_branch LPKE1_%=\n"
"LPCT0_%=:\n"
"s_memrealtime s[80:81]\n"
"s_sub_u32 s82, s67, s66\n"
"s_cmp_gt_i32 s82, 0\n"
"s_cselect_b32 s83, s67, s66\n"
"s_waitcnt lgkmcnt(0)\n"
"s_sub_u32 s82, s83, s80\n"
"s_cmp_gt_i32 s82, 3\n"
"s_cbranch_scc0 LPET0_%=\n"
"LPDT0_%=:\n"
"s_sleep 1\n"
"s_memrealtime s[80:81]\n"
"s_waitcnt lgkmcnt(0)\n"
"s_sub_u32 s82, s83, s80\n"
"s_cmp_gt_i32 s82, 3\n"
"s_cbranch_scc1 LPDT0_%=\n"
"LPET0_%=:\n"
"s_add_u32 s66, s66, s72\n"
"s_add_u32 s66, s66, 4\n"
"s_add_u32 s67, s80, s72\n"
"s_sub_u32 s72, s72, 3\n"
"s_max_u32 s72, s72, s62\n"
"s_branch LPKT0_%=\n"
"LPCT1_%=:\n"
"s_memrealtime s[80:81]\n"
"s_sub_u32 s82, s67, s66\n"
"s_cmp_gt_i32 s82, 0\n"
"s_cselect_b32 s83, s67, s66\n"
"s_waitcnt lgkmcnt(0)\n"
"s_add_u32 s72, s62, 16\n"
"s_sub_u32 s82, s83, s80\n"
"s_cmp_gt_i32 s82, 3\n"
"s_cbranch_scc0 LPET1_%=\n"
"LPDT1_%=:\n"
"s_sleep 1\n"
"s_memrealtime s[80:81]\n"
"s_waitcnt lgkmcnt(0)\n"
"s_sub_u32 s82, s83, s80\n"
"s_cmp_gt_i32 s82, 3\n"
"s_cbranch_scc1 LPDT1_%=\n"
"LPET1_%=:\n"
"s_add_u32 s66, s66, s72\n"
"s_add_u32 s66, s66, 4\n"
"s_add_u32 s67, s80, s72\n"
"s_sub_u32 s72, s72, 3\n"
"s_max_u32 s72, s72, s62\n"
"s_branch LPKT1_%=\n"
"LPCL0_%=:\n"
"s_memrealtime s[80:81]\n"
"s_sub_u32 s82, s67, s66\n"
"s_cmp_gt_i32 s82, 0\n"
"s_cselect_b32 s83, s67, s66\n"
"s_waitcnt lgkmcnt(0)\n"
"s_sub_u32 s82, s80, s67\n"
"s_add_u32 s63, s63, 1\n"
"s_min_i32 s81, s63, 0\n"
"s_cmp_gt_i32 s82, 20\n"
"s_cselect_b32 s63, s63, s81\n"
"s_cmp_ge_i32 s63, 2\n"
"s_cbranch_scc0 LPRL0_%=\n"
"s_mov_b32 s63, -9\n"
"s_add_u32 s62, s62, 18\n"
"s_min_u32 s62, s62, 146\n"
"s_max_u32 s72, s72, s62\n"
"s_add_u32 s83, s80, 200\n"
"s_and_b32 s83, s83, -1\n"
"LPRL0_%=:\n"
"s_sub_u32 s82, s83, s80\n"
"s_cmp_gt_i32 s82, 3\n"
"s_cbranch_scc0 LPEL0_%=\n"
"LPDL0_%=:\n"
"s_sleep 1\n"
"s_memrealtime s[80:81]\n"
"s_waitcnt lgkmcnt(0)\n"
"s_sub_u32 s82, s83, s80\n"
"s_cmp_gt_i32 s82, 3\n"
"s_cbranch_scc1 LPDL0_%=\n"
"LPEL0_%=:\n"
"s_add_u32 s66, s66, s72\n"
"s_add_u32 s66, s66, 4\n"
"s_add_u32 s67, s80, s72\n"
"s_sub_u32 s72, s72, 3\n"
"s_max_u32 s72, s72, s62\n"
"s_branch LPKL0_%=\n"
"LPCL1_%=:\n"
"s_memrealtime s[80:81]\n"
"s_sub_u32 s82, s67, s66\n"
"s_cmp_gt_i32 s82, 0\n"
"s_cselect_b32 s83, s67, s66\n"
"s_waitcnt lgkmcnt(0)\n"
"s_sub_u32 s82, s80, s67\n"
"s_add_u32 s63, s63, 1\n"
"s_min_i32 s81, s63, 0\n"
"s_cmp_gt_i32 s82, 20\n"
"s_cselect_b32 s63, s63, s81\n"
"s_cmp_ge_i32 s63, 2\n"
"s_cbranch_scc0 LPRL1_%=\n"
"s_mov_b32 s63, -9\n"
"s_add_u32 s62, s62, 18\n"
"s_min_u32 s62, s62, 146\n"
"s_max_u32 s72, s72, s62\n"
"s_add_u32 s83, s80, 200\n"
"s_and_b32 s83, s83, -1\n"
"LPRL1_%=:\n"
"s_sub_u32 s82, s83, s80\n"
"s_cmp_gt_i32 s82, 3\n"
"s_cbranch_scc0 LPEL1_%=\n"
"LPDL1_%=:\n"
"s_sleep 1\n"
"s_memrealtime s[80:81]\n"
"s_waitcnt lgkmcnt(0)\n"
"s_sub_u32 s82, s83, s80\n"
"s_cmp_gt_i32 s82, 3\n"
"s_cbranch_scc1 LPDL1_%=\n"
"LPEL1_%=:\n"
"s_add_u32 s66, s66, s72\n"
"s_add_u32 s66, s66, 4\n"
"s_add_u32 s67, s80, s72\n"
"s_sub_u32 s72, s72, 3\n"
"s_max_u32 s72, s72, s62\n"
"s_branch LPKL1_%=\n"
"LPCL2_%=:\n"
"s_memrealtime s[80:81]\n"
"s_sub_u32 s82, s67, s66\n"
"s_cmp_gt_i32 s82, 0\n"
"s_cselect_b32 s83, s67, s66\n"
"s_waitcnt lgkmcnt(0)\n"
"s_sub_u32 s82, s80, s67\n"
"s_add_u32 s63, s63, 1\n"
"s_min_i32 s81, s63, 0\n"
"s_cmp_gt_i32 s82, 20\n"
"s_cselect_b32 s63, s63, s81\n"
"s_cmp_ge_i32 s63, 2\n"
"s_cbranch_scc0 LPRL2_%=\n"
"s_mov_b32 s63, -9\n"
"s_add_u32 s62, s62, 18\n"
"s_min_u32 s62, s62, 146\n"
"s_max_u32 s72, s72, s62\n"
"s_add_u32 s83, s80, 200\n"
"s_and_b32 s83, s83, -1\n"
"LPRL2_%=:\n"
"s_sub_u32 s82, s83, s80\n"
"s_cmp_gt_i32 s82, 3\n"
"s_cbranch_scc0 LPEL2_%=\n"
"LPDL2_%=:\n"
"s_sleep 1\n"
"s_memrealtime s[80:81]\n"
"s_waitcnt lgkmcnt(0)\n"
"s_sub_u32 s82, s83, s80\n"
"s_cmp_gt_i32 s82, 3\n"
"s_cbranch_scc1 LPDL2_%=\n"
"LPEL2_%=:\n"
"s_add_u32 s66, s66, s72\n"
"s_add_u32 s66, s66, 4\n"
"s_add_u32 s67, s80, s72\n"
"s_sub_u32 s72, s72, 3\n"
"s_max_u32 s72, s72, s62\n"
"s_branch LPKL2_%=\n"
"LPCL3_%=:\n"
"s_memrealtime s[80:81]\n"
"s_sub_u32 s82, s67, s66\n"
"s_cmp_gt_i32 s82, 0\n"
"s_cselect_b32 s83, s67, s66\n"
"s_waitcnt lgkmcnt(0)\n"
"s_sub_u32 s82, s80, s67\n"
"s_add_u32 s63, s63, 1\n"
"s_min_i32 s81, s63, 0\n"
"s_cmp_gt_i32 s82, 20\n"
"s_cselect_b32 s63, s63, s81\n"
"s_cmp_ge_i32 s63, 2\n"
"s_cbranch_scc0 LPRL3_%=\n"
"s_mov_b32 s63, -9\n"
"s_add_u32 s62, s62, 18\n"
"s_min_u32 s62, s62, 146\n"
"s_max_u32 s72, s72, s62\n"
"s_add_u32 s83, s80, 200\n"
"s_and_b32 s83, s83, -1\n"
"LPRL3_%=:\n"
"s_sub_u32 s82, s83, s80\n"
"s_cmp_gt_i32 s82, 3\n"
"s_cbranch_scc0 LPEL3_%=\n"
"LPDL3_%=:\n"
"s_sleep 1\n"
"s_memrealtime s[80:81]\n"
"s_waitcnt lgkmcnt(0)\n"
"s_sub_u32 s82, s83, s80\n"
"s_cmp_gt_i32 s82, 3\n"
"s_cbranch_scc1 LPDL3_%=\n"
"LPEL3_%=:\n"
"s_add_u32 s66, s66, s72\n"
"s_add_u32 s66, s66, 4\n"
"s_add_u32 s67, s80, s72\n"
"s_sub_u32 s72, s72, 3\n"
"s_max_u32 s72, s72, s62\n"
"s_branch LPKL3_%=\n"
"LPN_%=:\n"
"v_mfma_f32_16x16x32_bf16 v[112:115], a[192:195], a[136:139], v[112:115]\n"
"v_mfma_f32_16x16x32_bf16 v[112:115], a[196:199], a[140:143], v[112:115]\n"
"v_mfma_f32_16x16x32_bf16 v[116:119], a[200:203], a[136:139], v[116:119]\n"
"s_waitcnt vmcnt(11)\n"
"v_mfma_f32_16x16x32_bf16 v[116:119], a[204:207], a[140:143], v[116:119]\n"
"s_barrier\n"
"v_mfma_f32_16x16x32_bf16 v[120:123], a[208:211], a[136:139], v[120:123]\n"
"s_add_u32 s40, s48, 256\n"
"s_xor_b32 m0, s50, 0xc000\n"
"ds_read_b64_tr_b16 a[224:225], v244 offset:16384\n"
"v_mfma_f32_16x16x32_bf16 v[120:123], a[212:215], a[140:143], v[120:123]\n"
"buffer_load_dwordx4 v224, s[28:31], s40 offen nt lds\n"
"v_mfma_f32_16x16x32_bf16 v[124:127], a[216:219], a[136:139], v[124:127]\n"
"ds_read_b64_tr_b16 a[226:227], v244 offset:16896\n"
"v_mfma_f32_16x16x32_bf16 v[124:127], a[220:223], a[140:143], v[124:127]\n"
"ds_read_b64_tr_b16 a[228:229], v244 offset:24576\n"
"v_mfma_f32_16x16x32_bf16 v[140:143], a[216:219], a[144:147], v[140:143]\n"
"ds_read_b64_tr_b16 a[230:231], v244 offset:25088\n"
"v_mfma_f32_16x16x32_bf16 v[140:143], a[220:223], a[148:151], v[140:143]\n"
"ds_read_b64_tr_b16 a[232:233], v245 offset:16384\n"
"v_mfma_f32_16x16x32_bf16 v[136:139], a[208:211], a[144:147], v[136:139]\n"
"ds_read_b64_tr_b16 a[234:235], v245 offset:16896\n"
"v_mfma_f32_16x16x32_bf16 v[136:139], a[212:215], a[148:151], v[136:139]\n"
"buffer_load_dwordx4 v225, s[28:31], s40 offen offset:1024 nt lds\n"
"v_mfma_f32_16x16x32_bf16 v[132:135], a[200:203], a[144:147], v[132:135]\n"
"ds_read_b64_tr_b16 a[236:237], v245 offset:24576\n"
"v_mfma_f32_16x16x32_bf16 v[132:135], a[204:207], a[148:151], v[132:135]\n"
"ds_read_b64_tr_b16 a[238:239], v245 offset:25088\n"
"v_mfma_f32_16x16x32_bf16 v[128:131], a[192:195], a[144:147], v[128:131]\n"
"ds_read_b64_tr_b16 a[240:241], v244 offset:17408\n"
"v_mfma_f32_16x16x32_bf16 v[128:131], a[196:199], a[148:151], v[128:131]\n"
"ds_read_b64_tr_b16 a[242:243], v244 offset:17920\n"
"v_mfma_f32_16x16x32_bf16 v[144:147], a[192:195], a[152:155], v[144:147]\n"
"ds_read_b64_tr_b16 a[244:245], v244 offset:25600\n"
"v_mfma_f32_16x16x32_bf16 v[144:147], a[196:199], a[156:159], v[144:147]\n"
"buffer_load_dwordx4 v226, s[28:31], s40 offen offset:2048 nt lds\n"
"v_mfma_f32_16x16x32_bf16 v[148:151], a[200:203], a[152:155], v[148:151]\n"
"ds_read_b64_tr_b16 a[246:247], v244 offset:26112\n"
"v_mfma_f32_16x16x32_bf16 v[148:151], a[204:207], a[156:159], v[148:151]\n"
"ds_read_b64_tr_b16 a[248:249], v245 offset:17408\n"
"v_mfma_f32_16x16x32_bf16 v[152:155], a[208:211], a[152:155], v[152:155]\n"
"ds_read_b64_tr_b16 a[250:251], v245 offset:17920\n"
"v_mfma_f32_16x16x32_bf16 v[152:155], a[212:215], a[156:159], v[152:155]\n"
"ds_read_b64_tr_b16 a[252:253], v245 offset:25600\n"
"v_mfma_f32_16x16x32_bf16 v[156:159], a[216:219], a[152:155], v[156:159]\n"
"ds_read_b64_tr_b16 a[254:255], v245 offset:26112\n"
"v_mfma_f32_16x16x32_bf16 v[156:159], a[220:223], a[156:159], v[156:159]\n"
"buffer_load_dwordx4 v227, s[28:31], s40 offen offset:3072 nt lds\n"
"v_mfma_f32_16x16x32_bf16 v[172:175], a[216:219], a[160:163], v[172:175]\n"
"s_cmp_eq_u32 s47, 8\n"
"v_mfma_f32_16x16x32_bf16 v[172:175], a[220:223], a[164:167], v[172:175]\n"
"s_cselect_b32 s41, s52, s58\n"
"v_mfma_f32_16x16x32_bf16 v[168:171], a[208:211], a[160:163], v[168:171]\n"
"s_add_u32 s48, s48, s41\n"
"v_mfma_f32_16x16x32_bf16 v[168:171], a[212:215], a[164:167], v[168:171]\n"
"s_sub_u32 s41, s48, 5242880\n"
"v_mfma_f32_16x16x32_bf16 v[164:167], a[200:203], a[160:163], v[164:167]\n"
"s_min_u32 s48, s48, s41\n"
"v_mfma_f32_16x16x32_bf16 v[164:167], a[204:207], a[164:167], v[164:167]\n"
"v_xor_b32 v244, s74, v244\n"
"v_xor_b32 v246, s74, v246\n"
"v_mfma_f32_16x16x32_bf16 v[160:163], a[192:195], a[160:163], v[160:163]\n"
"s_mov_b32 m0, s50\n"
"v_xor_b32 v245, s74, v245\n"
"v_xor_b32 v247, s74, v247\n"
"v_mfma_f32_16x16x32_bf16 v[160:163], a[196:199], a[164:167], v[160:163]\n"
"buffer_load_dwordx4 v224, s[28:31], s48 offen nt lds\n"
"v_mfma_f32_16x16x32_bf16 v[176:179], a[192:195], a[168:171], v[176:179]\n"
"v_xor_b32 v252, s74, v252\n"
"v_xor_b32 v253, s74, v253\n"
"v_mfma_f32_16x16x32_bf16 v[176:179], a[196:199], a[172:175], v[176:179]\n"
"v_xor_b32 v250, s74, v250\n"
"v_xor_b32 v251, s74, v251\n"
"v_mfma_f32_16x16x32_bf16 v[180:183], a[200:203], a[168:171], v[180:183]\n"
"v_mfma_f32_16x16x32_bf16 v[180:183], a[204:207], a[172:175], v[180:183]\n"
"buffer_load_dwordx4 v225, s[28:31], s48 offen offset:1024 nt lds\n"
"v_mfma_f32_16x16x32_bf16 v[184:187], a[208:211], a[168:171], v[184:187]\n"
"v_mfma_f32_16x16x32_bf16 v[184:187], a[212:215], a[172:175], v[184:187]\n"
"v_mfma_f32_16x16x32_bf16 v[188:191], a[216:219], a[168:171], v[188:191]\n"
"v_mfma_f32_16x16x32_bf16 v[188:191], a[220:223], a[172:175], v[188:191]\n"
"v_mfma_f32_16x16x32_bf16 v[204:207], a[216:219], a[176:179], v[204:207]\n"
"v_mfma_f32_16x16x32_bf16 v[204:207], a[220:223], a[180:183], v[204:207]\n"
"buffer_load_dwordx4 v226, s[28:31], s48 offen offset:2048 nt lds\n"
"v_mfma_f32_16x16x32_bf16 v[200:203], a[208:211], a[176:179], v[200:203]\n"
"v_mfma_f32_16x16x32_bf16 v[200:203], a[212:215], a[180:183], v[200:203]\n"
"v_mfma_f32_16x16x32_bf16 v[196:199], a[200:203], a[176:179], v[196:199]\n"
"v_mfma_f32_16x16x32_bf16 v[196:199], a[204:207], a[180:183], v[196:199]\n"
"v_mfma_f32_16x16x32_bf16 v[192:195], a[192:195], a[176:179], v[192:195]\n"
"v_mfma_f32_16x16x32_bf16 v[192:195], a[196:199], a[180:183], v[192:195]\n"
"s_waitcnt lgkmcnt(0)\n"
"buffer_load_dwordx4 v227, s[28:31], s48 offen offset:3072 nt lds\n"
"v_mfma_f32_16x16x32_bf16 v[208:211], a[192:195], a[184:187], v[208:211]\n"
"s_xor_b32 s50, s50, s74\n"
"v_mfma_f32_16x16x32_bf16 v[208:211], a[196:199], a[188:191], v[208:211]\n"
"v_mfma_f32_16x16x32_bf16 v[212:215], a[200:203], a[184:187], v[212:215]\n"
"v_mfma_f32_16x16x32_bf16 v[212:215], a[204:207], a[188:191], v[212:215]\n"
"v_mfma_f32_16x16x32_bf16 v[216:219], a[208:211], a[184:187], v[216:219]\n"
"v_mfma_f32_16x16x32_bf16 v[216:219], a[212:215], a[188:191], v[216:219]\n"
"v_mfma_f32_16x16x32_bf16 v[220:223], a[216:219], a[184:187], v[220:223]\n"
"v_mfma_f32_16x16x32_bf16 v[220:223], a[220:223], a[188:191], v[220:223]\n"
"v_mfma_f32_16x16x32_bf16 v[0:3], a[224:227], a[136:139], v[0:3]\n"
"v_mfma_f32_16x16x32_bf16 v[0:3], a[228:231], a[140:143], v[0:3]\n"
"v_mfma_f32_16x16x32_bf16 v[4:7], a[232:235], a[136:139], v[4:7]\n"
"s_waitcnt vmcnt(8)\n"
"v_mfma_f32_16x16x32_bf16 v[4:7], a[236:239], a[140:143], v[4:7]\n"
"s_barrier\n"
"v_mfma_f32_16x16x32_bf16 v[8:11], a[240:243], a[136:139], v[8:11]\n"
"s_mov_b32 m0, s51\n"
"ds_read_b64_tr_b16 a[192:193], v244 offset:0\n"
"v_mfma_f32_16x16x32_bf16 v[8:11], a[244:247], a[140:143], v[8:11]\n"
"buffer_load_dword v242, s[80:83], 0 offen lds\n"
"s_add_u32 s80, s80, 680\n"
"s_addc_u32 s81, s81, 0\n"
"buffer_load_dwordx4 v248, s[24:27], s49 offen nt lds\n"
"v_mfma_f32_16x16x32_bf16 v[12:15], a[248:251], a[136:139], v[12:15]\n"
"ds_read_b64_tr_b16 a[194:195], v244 offset:512\n"
"v_mfma_f32_16x16x32_bf16 v[12:15], a[252:255], a[140:143], v[12:15]\n"
"ds_read_b128 a[136:139], v246 offset:0\n"
"v_mfma_f32_16x16x32_bf16 v[28:31], a[248:251], a[144:147], v[28:31]\n"
"ds_read_b64_tr_b16 a[196:197], v244 offset:8192\n"
"v_mfma_f32_16x16x32_bf16 v[28:31], a[252:255], a[148:151], v[28:31]\n"
"ds_read_b64_tr_b16 a[198:199], v244 offset:8704\n"
"v_mfma_f32_16x16x32_bf16 v[24:27], a[240:243], a[144:147], v[24:27]\n"
"ds_read_b128 a[140:143], v247 offset:0\n"
"v_mfma_f32_16x16x32_bf16 v[24:27], a[244:247], a[148:151], v[24:27]\n"
"buffer_load_dwordx4 v249, s[24:27], s49 offen offset:1024 nt lds\n"
"v_mfma_f32_16x16x32_bf16 v[20:23], a[232:235], a[144:147], v[20:23]\n"
"ds_read_b64_tr_b16 a[200:201], v245 offset:0\n"
"v_mfma_f32_16x16x32_bf16 v[20:23], a[236:239], a[148:151], v[20:23]\n"
"ds_read_b64_tr_b16 a[202:203], v245 offset:512\n"
"v_mfma_f32_16x16x32_bf16 v[16:19], a[224:227], a[144:147], v[16:19]\n"
"ds_read_b64_tr_b16 a[204:205], v245 offset:8192\n"
"v_mfma_f32_16x16x32_bf16 v[16:19], a[228:231], a[148:151], v[16:19]\n"
"ds_read_b64_tr_b16 a[206:207], v245 offset:8704\n"
"v_mfma_f32_16x16x32_bf16 v[32:35], a[224:227], a[152:155], v[32:35]\n"
"ds_read_b64_tr_b16 a[208:209], v244 offset:1024\n"
"v_mfma_f32_16x16x32_bf16 v[32:35], a[228:231], a[156:159], v[32:35]\n"
"buffer_load_dwordx4 v254, s[24:27], s49 offen offset:2048 nt lds\n"
"v_mfma_f32_16x16x32_bf16 v[36:39], a[232:235], a[152:155], v[36:39]\n"
"ds_read_b64_tr_b16 a[210:211], v244 offset:1536\n"
"v_mfma_f32_16x16x32_bf16 v[36:39], a[236:239], a[156:159], v[36:39]\n"
"ds_read_b64_tr_b16 a[212:213], v244 offset:9216\n"
"v_mfma_f32_16x16x32_bf16 v[40:43], a[240:243], a[152:155], v[40:43]\n"
"ds_read_b64_tr_b16 a[214:215], v244 offset:9728\n"
"v_mfma_f32_16x16x32_bf16 v[40:43], a[244:247], a[156:159], v[40:43]\n"
"ds_read_b64_tr_b16 a[216:217], v245 offset:1024\n"
"v_mfma_f32_16x16x32_bf16 v[44:47], a[248:251], a[152:155], v[44:47]\n"
"ds_read_b64_tr_b16 a[218:219], v245 offset:1536\n"
"v_mfma_f32_16x16x32_bf16 v[44:47], a[252:255], a[156:159], v[44:47]\n"
"buffer_load_dwordx4 v255, s[24:27], s49 offen offset:3072 nt lds\n"
"v_mfma_f32_16x16x32_bf16 v[60:63], a[248:251], a[160:163], v[60:63]\n"
"ds_read_b64_tr_b16 a[220:221], v245 offset:9216\n"
"v_mfma_f32_16x16x32_bf16 v[60:63], a[252:255], a[164:167], v[60:63]\n"
"ds_read_b64_tr_b16 a[222:223], v245 offset:9728\n"
"v_mfma_f32_16x16x32_bf16 v[56:59], a[240:243], a[160:163], v[56:59]\n"
"ds_read_b128 a[144:147], v246 offset:2048\n"
"v_mfma_f32_16x16x32_bf16 v[56:59], a[244:247], a[164:167], v[56:59]\n"
"s_add_u32 s40, s49, 40960\n"
"ds_read_b128 a[148:151], v247 offset:2048\n"
"v_mfma_f32_16x16x32_bf16 v[52:55], a[232:235], a[160:163], v[52:55]\n"
"s_add_u32 m0, s51, 4096\n"
"ds_read_b128 a[152:155], v246 offset:4096\n"
"v_mfma_f32_16x16x32_bf16 v[52:55], a[236:239], a[164:167], v[52:55]\n"
"buffer_load_dwordx4 v248, s[24:27], s40 offen nt lds\n"
"v_mfma_f32_16x16x32_bf16 v[48:51], a[224:227], a[160:163], v[48:51]\n"
"ds_read_b128 a[156:159], v247 offset:4096\n"
"v_mfma_f32_16x16x32_bf16 v[48:51], a[228:231], a[164:167], v[48:51]\n"
"ds_read_b128 a[160:163], v252 offset:0\n"
"v_mfma_f32_16x16x32_bf16 v[64:67], a[224:227], a[168:171], v[64:67]\n"
"ds_read_b128 a[164:167], v253 offset:0\n"
"v_mfma_f32_16x16x32_bf16 v[64:67], a[228:231], a[172:175], v[64:67]\n"
"v_mfma_f32_16x16x32_bf16 v[68:71], a[232:235], a[168:171], v[68:71]\n"
"v_mfma_f32_16x16x32_bf16 v[68:71], a[236:239], a[172:175], v[68:71]\n"
"buffer_load_dwordx4 v249, s[24:27], s40 offen offset:1024 nt lds\n"
"v_mfma_f32_16x16x32_bf16 v[72:75], a[240:243], a[168:171], v[72:75]\n"
"v_mfma_f32_16x16x32_bf16 v[72:75], a[244:247], a[172:175], v[72:75]\n"
"v_mfma_f32_16x16x32_bf16 v[76:79], a[248:251], a[168:171], v[76:79]\n"
"ds_read_b128 a[168:171], v250 offset:0\n"
"v_mfma_f32_16x16x32_bf16 v[76:79], a[252:255], a[172:175], v[76:79]\n"
"ds_read_b128 a[172:175], v251 offset:0\n"
"v_mfma_f32_16x16x32_bf16 v[92:95], a[248:251], a[176:179], v[92:95]\n"
"v_mfma_f32_16x16x32_bf16 v[92:95], a[252:255], a[180:183], v[92:95]\n"
"buffer_load_dwordx4 v254, s[24:27], s40 offen offset:2048 nt lds\n"
"v_mfma_f32_16x16x32_bf16 v[88:91], a[240:243], a[176:179], v[88:91]\n"
"s_add_u32 s49, s49, 128\n"
"v_mfma_f32_16x16x32_bf16 v[88:91], a[244:247], a[180:183], v[88:91]\n"
"s_sub_u32 s41, s49, 1280\n"
"v_mfma_f32_16x16x32_bf16 v[84:87], a[232:235], a[176:179], v[84:87]\n"
"s_min_u32 s49, s49, s41\n"
"v_mfma_f32_16x16x32_bf16 v[84:87], a[236:239], a[180:183], v[84:87]\n"
"s_xor_b32 s51, s51, s74\n"
"v_mfma_f32_16x16x32_bf16 v[80:83], a[224:227], a[176:179], v[80:83]\n"
"ds_read_b128 a[176:179], v250 offset:2048\n"
"v_mfma_f32_16x16x32_bf16 v[80:83], a[228:231], a[180:183], v[80:83]\n"
"ds_read_b128 a[180:183], v251 offset:2048\n"
"v_mfma_f32_16x16x32_bf16 v[96:99], a[224:227], a[184:187], v[96:99]\n"
"v_mfma_f32_16x16x32_bf16 v[96:99], a[228:231], a[188:191], v[96:99]\n"
"v_mfma_f32_16x16x32_bf16 v[100:103], a[232:235], a[184:187], v[100:103]\n"
"v_mfma_f32_16x16x32_bf16 v[100:103], a[236:239], a[188:191], v[100:103]\n"
"v_mfma_f32_16x16x32_bf16 v[104:107], a[240:243], a[184:187], v[104:107]\n"
"v_mfma_f32_16x16x32_bf16 v[104:107], a[244:247], a[188:191], v[104:107]\n"
"v_mfma_f32_16x16x32_bf16 v[108:111], a[248:251], a[184:187], v[108:111]\n"
"s_add_u32 s47, s47, 1\n"
"ds_read_b128 a[184:187], v250 offset:4096\n"
"v_mfma_f32_16x16x32_bf16 v[108:111], a[252:255], a[188:191], v[108:111]\n"
"s_waitcnt lgkmcnt(1)\n"
"s_cmp_lg_u32 s47, s62\n"
"ds_read_b128 a[188:191], v251 offset:4096\n"
"s_cbranch_scc1 LPN_%=\n"
"s_branch LPX_%=\n"
"LEND_%=:\n"
"LSLOW_%=:\n"
"s_waitcnt vmcnt(0) lgkmcnt(0)\n"
"s_nop 9\n"
"v_bfe_u32 v148, v242, 4, 4\n"
"v_bfe_u32 v149, v242, 8, 2\n"
"v_lshlrev_b32 v144, 8, v148\n"
"v_lshl_add_u32 v144, v149, 4, v144\n"
"v_add_u32 v144, s43, v144\n"
"v_xor_b32 v145, 64, v144\n"
"v_lshlrev_b32 v146, 1, v148\n"
"v_lshl_add_u32 v146, v149, 10, v146\n"
"v_add_u32 v146, s43, v146\n"
"v_xor_b32 v147, 64, v146\n"
"v_mov_b32 v152, 0xff7fffff\n"
"v_mov_b32 v155, 0\n"
"v_mov_b32 v0, 0\n"
"v_mov_b32 v1, 0\n"
"v_mov_b32 v2, 0\n"
"v_mov_b32 v3, 0\n"
"v_mov_b32 v4, 0\n"
"v_mov_b32 v5, 0\n"
"v_mov_b32 v6, 0\n"
"v_mov_b32 v7, 0\n"
"v_mov_b32 v8, 0\n"
"v_mov_b32 v9, 0\n"
"v_mov_b32 v10, 0\n"
"v_mov_b32 v11, 0\n"
"v_mov_b32 v12, 0\n"
"v_mov_b32 v13, 0\n"
"v_mov_b32 v14, 0\n"
"v_mov_b32 v15, 0\n"
"v_mov_b32 v16, 0\n"
"v_mov_b32 v17, 0\n"
"v_mov_b32 v18, 0\n"
"v_mov_b32 v19, 0\n"
"v_mov_b32 v20, 0\n"
"v_mov_b32 v21, 0\n"
"v_mov_b32 v22, 0\n"
"v_mov_b32 v23, 0\n"
"v_mov_b32 v24, 0\n"
"v_mov_b32 v25, 0\n"
"v_mov_b32 v26, 0\n"
"v_mov_b32 v27, 0\n"
"v_mov_b32 v28, 0\n"
"v_mov_b32 v29, 0\n"
"v_mov_b32 v30, 0\n"
"v_mov_b32 v31, 0\n"
"v_mov_b32 v156, 0xff7fffff\n"
"v_mov_b32 v159, 0\n"
"v_mov_b32 v32, 0\n"
"v_mov_b32 v33, 0\n"
"v_mov_b32 v34, 0\n"
"v_mov_b32 v35, 0\n"
"v_mov_b32 v36, 0\n"
"v_mov_b32 v37, 0\n"
"v_mov_b32 v38, 0\n"
"v_mov_b32 v39, 0\n"
"v_mov_b32 v40, 0\n"
"v_mov_b32 v41, 0\n"
"v_mov_b32 v42, 0\n"
"v_mov_b32 v43, 0\n"
"v_mov_b32 v44, 0\n"
"v_mov_b32 v45, 0\n"
"v_mov_b32 v46, 0\n"
"v_mov_b32 v47, 0\n"
"v_mov_b32 v48, 0\n"
"v_mov_b32 v49, 0\n"
"v_mov_b32 v50, 0\n"
"v_mov_b32 v51, 0\n"
"v_mov_b32 v52, 0\n"
"v_mov_b32 v53, 0\n"
"v_mov_b32 v54, 0\n"
"v_mov_b32 v55, 0\n"
"v_mov_b32 v56, 0\n"
"v_mov_b32 v57, 0\n"
"v_mov_b32 v58, 0\n"
"v_mov_b32 v59, 0\n"
"v_mov_b32 v60, 0\n"
"v_mov_b32 v61, 0\n"
"v_mov_b32 v62, 0\n"
"v_mov_b32 v63, 0\n"
"v_mov_b32 v160, 0xff7fffff\n"
"v_mov_b32 v163, 0\n"
"v_mov_b32 v64, 0\n"
"v_mov_b32 v65, 0\n"
"v_mov_b32 v66, 0\n"
"v_mov_b32 v67, 0\n"
"v_mov_b32 v68, 0\n"
"v_mov_b32 v69, 0\n"
"v_mov_b32 v70, 0\n"
"v_mov_b32 v71, 0\n"
"v_mov_b32 v72, 0\n"
"v_mov_b32 v73, 0\n"
"v_mov_b32 v74, 0\n"
"v_mov_b32 v75, 0\n"
"v_mov_b32 v76, 0\n"
"v_mov_b32 v77, 0\n"
"v_mov_b32 v78, 0\n"
"v_mov_b32 v79, 0\n"
"v_mov_b32 v80, 0\n"
"v_mov_b32 v81, 0\n"
"v_mov_b32 v82, 0\n"
"v_mov_b32 v83, 0\n"
"v_mov_b32 v84, 0\n"
"v_mov_b32 v85, 0\n"
"v_mov_b32 v86, 0\n"
"v_mov_b32 v87, 0\n"
"v_mov_b32 v88, 0\n"
"v_mov_b32 v89, 0\n"
"v_mov_b32 v90, 0\n"
"v_mov_b32 v91, 0\n"
"v_mov_b32 v92, 0\n"
"v_mov_b32 v93, 0\n"
"v_mov_b32 v94, 0\n"
"v_mov_b32 v95, 0\n"
"v_mov_b32 v164, 0xff7fffff\n"
"v_mov_b32 v167, 0\n"
"v_accvgpr_write_b32 a208, 0\n"
"v_accvgpr_write_b32 a209, 0\n"
"v_accvgpr_write_b32 a210, 0\n"
"v_accvgpr_write_b32 a211, 0\n"
"v_accvgpr_write_b32 a212, 0\n"
"v_accvgpr_write_b32 a213, 0\n"
"v_accvgpr_write_b32 a214, 0\n"
"v_accvgpr_write_b32 a215, 0\n"
"v_accvgpr_write_b32 a216, 0\n"
"v_accvgpr_write_b32 a217, 0\n"
"v_accvgpr_write_b32 a218, 0\n"
"v_accvgpr_write_b32 a219, 0\n"
"v_accvgpr_write_b32 a220, 0\n"
"v_accvgpr_write_b32 a221, 0\n"
"v_accvgpr_write_b32 a222, 0\n"
"v_accvgpr_write_b32 a223, 0\n"
"v_accvgpr_write_b32 a224, 0\n"
"v_accvgpr_write_b32 a225, 0\n"
"v_accvgpr_write_b32 a226, 0\n"
"v_accvgpr_write_b32 a227, 0\n"
"v_accvgpr_write_b32 a228, 0\n"
"v_accvgpr_write_b32 a229, 0\n"
"v_accvgpr_write_b32 a230, 0\n"
"v_accvgpr_write_b32 a231, 0\n"
"v_accvgpr_write_b32 a232, 0\n"
"v_accvgpr_write_b32 a233, 0\n"
"v_accvgpr_write_b32 a234, 0\n"
"v_accvgpr_write_b32 a235, 0\n"
"v_accvgpr_write_b32 a236, 0\n"
"v_accvgpr_write_b32 a237, 0\n"
"v_accvgpr_write_b32 a238, 0\n"
"v_accvgpr_write_b32 a239, 0\n"
"s_lshl_b32 s80, s47, 16\n"
"s_add_u32 s81, s80, 4096\n"
"s_mov_b32 s82, 0\n"
"LSLOOP_%=:\n"
"s_lshl_b32 s83, s82, 3\n"
"s_and_b32 s83, s83, 64\n"
"buffer_load_dwordx4 v[128:131], v144, s[84:87], s80 offen offset:0\n"
"buffer_load_dwordx4 v[132:135], v145, s[84:87], s80 offen offset:0\n"
"buffer_load_dwordx4 v[136:139], v144, s[84:87], s80 offen offset:128\n"
"buffer_load_dwordx4 v[140:143], v145, s[84:87], s80 offen offset:128\n"
"s_waitcnt vmcnt(0)\n"
"v_mfma_f32_16x16x32_bf16 v[96:99], v[128:131], a[0:3], 0\n"
"v_mfma_f32_16x16x32_bf16 v[96:99], v[132:135], a[4:7], v[96:99]\n"
"v_mfma_f32_16x16x32_bf16 v[96:99], v[136:139], a[8:11], v[96:99]\n"
"v_mfma_f32_16x16x32_bf16 v[96:99], v[140:143], a[12:15], v[96:99]\n"
"v_mfma_f32_16x16x32_bf16 v[104:107], v[128:131], a[16:19], 0\n"
"v_mfma_f32_16x16x32_bf16 v[104:107], v[132:135], a[20:23], v[104:107]\n"
"v_mfma_f32_16x16x32_bf16 v[104:107], v[136:139], a[24:27], v[104:107]\n"
"v_mfma_f32_16x16x32_bf16 v[104:107], v[140:143], a[28:31], v[104:107]\n"
"v_mfma_f32_16x16x32_bf16 v[112:115], v[128:131], a[32:35], 0\n"
"v_mfma_f32_16x16x32_bf16 v[112:115], v[132:135], a[36:39], v[112:115]\n"
"v_mfma_f32_16x16x32_bf16 v[112:115], v[136:139], a[40:43], v[112:115]\n"
"v_mfma_f32_16x16x32_bf16 v[112:115], v[140:143], a[44:47], v[112:115]\n"
"v_mfma_f32_16x16x32_bf16 v[120:123], v[128:131], a[192:195], 0\n"
"v_mfma_f32_16x16x32_bf16 v[120:123], v[132:135], a[196:199], v[120:123]\n"
"v_mfma_f32_16x16x32_bf16 v[120:123], v[136:139], a[200:203], v[120:123]\n"
"v_mfma_f32_16x16x32_bf16 v[120:123], v[140:143], a[204:207], v[120:123]\n"
"s_nop 7\n"
"buffer_load_dwordx4 v[128:131], v144, s[84:87], s81 offen offset:0\n"
"buffer_load_dwordx4 v[132:135], v145, s[84:87], s81 offen offset:0\n"
"buffer_load_dwordx4 v[136:139], v144, s[84:87], s81 offen offset:128\n"
"buffer_load_dwordx4 v[140:143], v145, s[84:87], s81 offen offset:128\n"
"s_waitcnt vmcnt(0)\n"
"v_mfma_f32_16x16x32_bf16 v[100:103], v[128:131], a[0:3], 0\n"
"v_mfma_f32_16x16x32_bf16 v[100:103], v[132:135], a[4:7], v[100:103]\n"
"v_mfma_f32_16x16x32_bf16 v[100:103], v[136:139], a[8:11], v[100:103]\n"
"v_mfma_f32_16x16x32_bf16 v[100:103], v[140:143], a[12:15], v[100:103]\n"
"v_mfma_f32_16x16x32_bf16 v[108:111], v[128:131], a[16:19], 0\n"
"v_mfma_f32_16x16x32_bf16 v[108:111], v[132:135], a[20:23], v[108:111]\n"
"v_mfma_f32_16x16x32_bf16 v[108:111], v[136:139], a[24:27], v[108:111]\n"
"v_mfma_f32_16x16x32_bf16 v[108:111], v[140:143], a[28:31], v[108:111]\n"
"v_mfma_f32_16x16x32_bf16 v[116:119], v[128:131], a[32:35], 0\n"
"v_mfma_f32_16x16x32_bf16 v[116:119], v[132:135], a[36:39], v[116:119]\n"
"v_mfma_f32_16x16x32_bf16 v[116:119], v[136:139], a[40:43], v[116:119]\n"
"v_mfma_f32_16x16x32_bf16 v[116:119], v[140:143], a[44:47], v[116:119]\n"
"v_mfma_f32_16x16x32_bf16 v[124:127], v[128:131], a[192:195], 0\n"
"v_mfma_f32_16x16x32_bf16 v[124:127], v[132:135], a[196:199], v[124:127]\n"
"v_mfma_f32_16x16x32_bf16 v[124:127], v[136:139], a[200:203], v[124:127]\n"
"v_mfma_f32_16x16x32_bf16 v[124:127], v[140:143], a[204:207], v[124:127]\n"
"s_nop 7\n"
"v_max3_f32 v153, v96, v97, v98\n"
"v_max3_f32 v153, v153, v99, v100\n"
"v_max3_f32 v153, v153, v101, v102\n"
"v_max3_f32 v153, v153, v103, v152\n"
"v_mov_b32 v154, v153\n"
"s_nop 1\n"
"v_permlane32_swap_b32 v153, v154\n"
"v_max_f32 v153, v153, v154\n"
"v_mov_b32 v154, v153\n"
"s_nop 1\n"
"v_permlane16_swap_b32 v153, v154\n"
"v_max_f32 v153, v153, v154\n"
"v_sub_f32 v154, v152, v153\n"
"v_exp_f32 v154, v154\n"
"v_mov_b32 v152, v153\n"
"v_sub_f32 v96, v96, v152\n"
"v_sub_f32 v97, v97, v152\n"
"v_sub_f32 v98, v98, v152\n"
"v_sub_f32 v99, v99, v152\n"
"v_sub_f32 v100, v100, v152\n"
"v_sub_f32 v101, v101, v152\n"
"v_sub_f32 v102, v102, v152\n"
"v_sub_f32 v103, v103, v152\n"
"v_exp_f32 v96, v96\n"
"v_exp_f32 v97, v97\n"
"v_exp_f32 v98, v98\n"
"v_exp_f32 v99, v99\n"
"v_exp_f32 v100, v100\n"
"v_exp_f32 v101, v101\n"
"v_exp_f32 v102, v102\n"
"v_exp_f32 v103, v103\n"
"v_mul_f32 v155, v155, v154\n"
"v_add_f32 v155, v155, v96\n"
"v_add_f32 v155, v155, v97\n"
"v_add_f32 v155, v155, v98\n"
"v_add_f32 v155, v155, v99\n"
"v_add_f32 v155, v155, v100\n"
"v_add_f32 v155, v155, v101\n"
"v_add_f32 v155, v155, v102\n"
"v_add_f32 v155, v155, v103\n"
"v_cvt_pk_bf16_f32 v96, v96, v97\n"
"v_cvt_pk_bf16_f32 v97, v98, v99\n"
"v_cvt_pk_bf16_f32 v98, v100, v101\n"
"v_cvt_pk_bf16_f32 v99, v102, v103\n"
"v_mov_b32_dpp v154, v154 row_ror:12 row_mask:0x2 bank_mask:0xf\n"
"s_nop 1\n"
"v_mov_b32_dpp v154, v154 row_ror:8 row_mask:0x4 bank_mask:0xf\n"
"s_nop 1\n"
"v_mov_b32_dpp v154, v154 row_ror:4 row_mask:0x8 bank_mask:0xf\n"
"s_nop 1\n"
"v_mul_f32_dpp v0, v154, v0 row_newbcast:0 row_mask:0xf bank_mask:0xf\n"
"v_mul_f32_dpp v1, v154, v1 row_newbcast:1 row_mask:0xf bank_mask:0xf\n"
"v_mul_f32_dpp v2, v154, v2 row_newbcast:2 row_mask:0xf bank_mask:0xf\n"
"v_mul_f32_dpp v3, v154, v3 row_newbcast:3 row_mask:0xf bank_mask:0xf\n"
"v_mul_f32_dpp v4, v154, v4 row_newbcast:0 row_mask:0xf bank_mask:0xf\n"
"v_mul_f32_dpp v5, v154, v5 row_newbcast:1 row_mask:0xf bank_mask:0xf\n"
"v_mul_f32_dpp v6, v154, v6 row_newbcast:2 row_mask:0xf bank_mask:0xf\n"
"v_mul_f32_dpp v7, v154, v7 row_newbcast:3 row_mask:0xf bank_mask:0xf\n"
"v_mul_f32_dpp v8, v154, v8 row_newbcast:0 row_mask:0xf bank_mask:0xf\n"
"v_mul_f32_dpp v9, v154, v9 row_newbcast:1 row_mask:0xf bank_mask:0xf\n"
"v_mul_f32_dpp v10, v154, v10 row_newbcast:2 row_mask:0xf bank_mask:0xf\n"
"v_mul_f32_dpp v11, v154, v11 row_newbcast:3 row_mask:0xf bank_mask:0xf\n"
"v_mul_f32_dpp v12, v154, v12 row_newbcast:0 row_mask:0xf bank_mask:0xf\n"
"v_mul_f32_dpp v13, v154, v13 row_newbcast:1 row_mask:0xf bank_mask:0xf\n"
"v_mul_f32_dpp v14, v154, v14 row_newbcast:2 row_mask:0xf bank_mask:0xf\n"
"v_mul_f32_dpp v15, v154, v15 row_newbcast:3 row_mask:0xf bank_mask:0xf\n"
"v_mul_f32_dpp v16, v154, v16 row_newbcast:0 row_mask:0xf bank_mask:0xf\n"
"v_mul_f32_dpp v17, v154, v17 row_newbcast:1 row_mask:0xf bank_mask:0xf\n"
"v_mul_f32_dpp v18, v154, v18 row_newbcast:2 row_mask:0xf bank_mask:0xf\n"
"v_mul_f32_dpp v19, v154, v19 row_newbcast:3 row_mask:0xf bank_mask:0xf\n"
"v_mul_f32_dpp v20, v154, v20 row_newbcast:0 row_mask:0xf bank_mask:0xf\n"
"v_mul_f32_dpp v21, v154, v21 row_newbcast:1 row_mask:0xf bank_mask:0xf\n"
"v_mul_f32_dpp v22, v154, v22 row_newbcast:2 row_mask:0xf bank_mask:0xf\n"
"v_mul_f32_dpp v23, v154, v23 row_newbcast:3 row_mask:0xf bank_mask:0xf\n"
"v_mul_f32_dpp v24, v154, v24 row_newbcast:0 row_mask:0xf bank_mask:0xf\n"
"v_mul_f32_dpp v25, v154, v25 row_newbcast:1 row_mask:0xf bank_mask:0xf\n"
"v_mul_f32_dpp v26, v154, v26 row_newbcast:2 row_mask:0xf bank_mask:0xf\n"
"v_mul_f32_dpp v27, v154, v27 row_newbcast:3 row_mask:0xf bank_mask:0xf\n"
"v_mul_f32_dpp v28, v154, v28 row_newbcast:0 row_mask:0xf bank_mask:0xf\n"
"v_mul_f32_dpp v29, v154, v29 row_newbcast:1 row_mask:0xf bank_mask:0xf\n"
"v_mul_f32_dpp v30, v154, v30 row_newbcast:2 row_mask:0xf bank_mask:0xf\n"
"v_mul_f32_dpp v31, v154, v31 row_newbcast:3 row_mask:0xf bank_mask:0xf\n"
"v_max3_f32 v157, v104, v105, v106\n"
"v_max3_f32 v157, v157, v107, v108\n"
"v_max3_f32 v157, v157, v109, v110\n"
"v_max3_f32 v157, v157, v111, v156\n"
"v_mov_b32 v158, v157\n"
"s_nop 1\n"
"v_permlane32_swap_b32 v157, v158\n"
"v_max_f32 v157, v157, v158\n"
"v_mov_b32 v158, v157\n"
"s_nop 1\n"
"v_permlane16_swap_b32 v157, v158\n"
"v_max_f32 v157, v157, v158\n"
"v_sub_f32 v158, v156, v157\n"
"v_exp_f32 v158, v158\n"
"v_mov_b32 v156, v157\n"
"v_sub_f32 v104, v104, v156\n"
"v_sub_f32 v105, v105, v156\n"
"v_sub_f32 v106, v106, v156\n"
"v_sub_f32 v107, v107, v156\n"
"v_sub_f32 v108, v108, v156\n"
"v_sub_f32 v109, v109, v156\n"
"v_sub_f32 v110, v110, v156\n"
"v_sub_f32 v111, v111, v156\n"
"v_exp_f32 v104, v104\n"
"v_exp_f32 v105, v105\n"
"v_exp_f32 v106, v106\n"
"v_exp_f32 v107, v107\n"
"v_exp_f32 v108, v108\n"
"v_exp_f32 v109, v109\n"
"v_exp_f32 v110, v110\n"
"v_exp_f32 v111, v111\n"
"v_mul_f32 v159, v159, v158\n"
"v_add_f32 v159, v159, v104\n"
"v_add_f32 v159, v159, v105\n"
"v_add_f32 v159, v159, v106\n"
"v_add_f32 v159, v159, v107\n"
"v_add_f32 v159, v159, v108\n"
"v_add_f32 v159, v159, v109\n"
"v_add_f32 v159, v159, v110\n"
"v_add_f32 v159, v159, v111\n"
"v_cvt_pk_bf16_f32 v104, v104, v105\n"
"v_cvt_pk_bf16_f32 v105, v106, v107\n"
"v_cvt_pk_bf16_f32 v106, v108, v109\n"
"v_cvt_pk_bf16_f32 v107, v110, v111\n"
"v_mov_b32_dpp v158, v158 row_ror:12 row_mask:0x2 bank_mask:0xf\n"
"s_nop 1\n"
"v_mov_b32_dpp v158, v158 row_ror:8 row_mask:0x4 bank_mask:0xf\n"
"s_nop 1\n"
"v_mov_b32_dpp v158, v158 row_ror:4 row_mask:0x8 bank_mask:0xf\n"
"s_nop 1\n"
"v_mul_f32_dpp v32, v158, v32 row_newbcast:0 row_mask:0xf bank_mask:0xf\n"
"v_mul_f32_dpp v33, v158, v33 row_newbcast:1 row_mask:0xf bank_mask:0xf\n"
"v_mul_f32_dpp v34, v158, v34 row_newbcast:2 row_mask:0xf bank_mask:0xf\n"
"v_mul_f32_dpp v35, v158, v35 row_newbcast:3 row_mask:0xf bank_mask:0xf\n"
"v_mul_f32_dpp v36, v158, v36 row_newbcast:0 row_mask:0xf bank_mask:0xf\n"
"v_mul_f32_dpp v37, v158, v37 row_newbcast:1 row_mask:0xf bank_mask:0xf\n"
"v_mul_f32_dpp v38, v158, v38 row_newbcast:2 row_mask:0xf bank_mask:0xf\n"
"v_mul_f32_dpp v39, v158, v39 row_newbcast:3 row_mask:0xf bank_mask:0xf\n"
"v_mul_f32_dpp v40, v158, v40 row_newbcast:0 row_mask:0xf bank_mask:0xf\n"
"v_mul_f32_dpp v41, v158, v41 row_newbcast:1 row_mask:0xf bank_mask:0xf\n"
"v_mul_f32_dpp v42, v158, v42 row_newbcast:2 row_mask:0xf bank_mask:0xf\n"
"v_mul_f32_dpp v43, v158, v43 row_newbcast:3 row_mask:0xf bank_mask:0xf\n"
"v_mul_f32_dpp v44, v158, v44 row_newbcast:0 row_mask:0xf bank_mask:0xf\n"
"v_mul_f32_dpp v45, v158, v45 row_newbcast:1 row_mask:0xf bank_mask:0xf\n"
"v_mul_f32_dpp v46, v158, v46 row_newbcast:2 row_mask:0xf bank_mask:0xf\n"
"v_mul_f32_dpp v47, v158, v47 row_newbcast:3 row_mask:0xf bank_mask:0xf\n"
"v_mul_f32_dpp v48, v158, v48 row_newbcast:0 row_mask:0xf bank_mask:0xf\n"
"v_mul_f32_dpp v49, v158, v49 row_newbcast:1 row_mask:0xf bank_mask:0xf\n"
"v_mul_f32_dpp v50, v158, v50 row_newbcast:2 row_mask:0xf bank_mask:0xf\n"
"v_mul_f32_dpp v51, v158, v51 row_newbcast:3 row_mask:0xf bank_mask:0xf\n"
"v_mul_f32_dpp v52, v158, v52 row_newbcast:0 row_mask:0xf bank_mask:0xf\n"
"v_mul_f32_dpp v53, v158, v53 row_newbcast:1 row_mask:0xf bank_mask:0xf\n"
"v_mul_f32_dpp v54, v158, v54 row_newbcast:2 row_mask:0xf bank_mask:0xf\n"
"v_mul_f32_dpp v55, v158, v55 row_newbcast:3 row_mask:0xf bank_mask:0xf\n"
"v_mul_f32_dpp v56, v158, v56 row_newbcast:0 row_mask:0xf bank_mask:0xf\n"
"v_mul_f32_dpp v57, v158, v57 row_newbcast:1 row_mask:0xf bank_mask:0xf\n"
"v_mul_f32_dpp v58, v158, v58 row_newbcast:2 row_mask:0xf bank_mask:0xf\n"
"v_mul_f32_dpp v59, v158, v59 row_newbcast:3 row_mask:0xf bank_mask:0xf\n"
"v_mul_f32_dpp v60, v158, v60 row_newbcast:0 row_mask:0xf bank_mask:0xf\n"
"v_mul_f32_dpp v61, v158, v61 row_newbcast:1 row_mask:0xf bank_mask:0xf\n"
"v_mul_f32_dpp v62, v158, v62 row_newbcast:2 row_mask:0xf bank_mask:0xf\n"
"v_mul_f32_dpp v63, v158, v63 row_newbcast:3 row_mask:0xf bank_mask:0xf\n"
"v_max3_f32 v161, v112, v113, v114\n"
"v_max3_f32 v161, v161, v115, v116\n"
"v_max3_f32 v161, v161, v117, v118\n"
"v_max3_f32 v161, v161, v119, v160\n"
"v_mov_b32 v162, v161\n"
"s_nop 1\n"
"v_permlane32_swap_b32 v161, v162\n"
"v_max_f32 v161, v161, v162\n"
"v_mov_b32 v162, v161\n"
"s_nop 1\n"
"v_permlane16_swap_b32 v161, v162\n"
"v_max_f32 v161, v161, v162\n"
"v_sub_f32 v162, v160, v161\n"
"v_exp_f32 v162, v162\n"
"v_mov_b32 v160, v161\n"
"v_sub_f32 v112, v112, v160\n"
"v_sub_f32 v113, v113, v160\n"
"v_sub_f32 v114, v114, v160\n"
"v_sub_f32 v115, v115, v160\n"
"v_sub_f32 v116, v116, v160\n"
"v_sub_f32 v117, v117, v160\n"
"v_sub_f32 v118, v118, v160\n"
"v_sub_f32 v119, v119, v160\n"
"v_exp_f32 v112, v112\n"
"v_exp_f32 v113, v113\n"
"v_exp_f32 v114, v114\n"
"v_exp_f32 v115, v115\n"
"v_exp_f32 v116, v116\n"
"v_exp_f32 v117, v117\n"
"v_exp_f32 v118, v118\n"
"v_exp_f32 v119, v119\n"
"v_mul_f32 v163, v163, v162\n"
"v_add_f32 v163, v163, v112\n"
"v_add_f32 v163, v163, v113\n"
"v_add_f32 v163, v163, v114\n"
"v_add_f32 v163, v163, v115\n"
"v_add_f32 v163, v163, v116\n"
"v_add_f32 v163, v163, v117\n"
"v_add_f32 v163, v163, v118\n"
"v_add_f32 v163, v163, v119\n"
"v_cvt_pk_bf16_f32 v112, v112, v113\n"
"v_cvt_pk_bf16_f32 v113, v114, v115\n"
"v_cvt_pk_bf16_f32 v114, v116, v117\n"
"v_cvt_pk_bf16_f32 v115, v118, v119\n"
"v_mov_b32_dpp v162, v162 row_ror:12 row_mask:0x2 bank_mask:0xf\n"
"s_nop 1\n"
"v_mov_b32_dpp v162, v162 row_ror:8 row_mask:0x4 bank_mask:0xf\n"
"s_nop 1\n"
"v_mov_b32_dpp v162, v162 row_ror:4 row_mask:0x8 bank_mask:0xf\n"
"s_nop 1\n"
"v_mul_f32_dpp v64, v162, v64 row_newbcast:0 row_mask:0xf bank_mask:0xf\n"
"v_mul_f32_dpp v65, v162, v65 row_newbcast:1 row_mask:0xf bank_mask:0xf\n"
"v_mul_f32_dpp v66, v162, v66 row_newbcast:2 row_mask:0xf bank_mask:0xf\n"
"v_mul_f32_dpp v67, v162, v67 row_newbcast:3 row_mask:0xf bank_mask:0xf\n"
"v_mul_f32_dpp v68, v162, v68 row_newbcast:0 row_mask:0xf bank_mask:0xf\n"
"v_mul_f32_dpp v69, v162, v69 row_newbcast:1 row_mask:0xf bank_mask:0xf\n"
"v_mul_f32_dpp v70, v162, v70 row_newbcast:2 row_mask:0xf bank_mask:0xf\n"
"v_mul_f32_dpp v71, v162, v71 row_newbcast:3 row_mask:0xf bank_mask:0xf\n"
"v_mul_f32_dpp v72, v162, v72 row_newbcast:0 row_mask:0xf bank_mask:0xf\n"
"v_mul_f32_dpp v73, v162, v73 row_newbcast:1 row_mask:0xf bank_mask:0xf\n"
"v_mul_f32_dpp v74, v162, v74 row_newbcast:2 row_mask:0xf bank_mask:0xf\n"
"v_mul_f32_dpp v75, v162, v75 row_newbcast:3 row_mask:0xf bank_mask:0xf\n"
"v_mul_f32_dpp v76, v162, v76 row_newbcast:0 row_mask:0xf bank_mask:0xf\n"
"v_mul_f32_dpp v77, v162, v77 row_newbcast:1 row_mask:0xf bank_mask:0xf\n"
"v_mul_f32_dpp v78, v162, v78 row_newbcast:2 row_mask:0xf bank_mask:0xf\n"
"v_mul_f32_dpp v79, v162, v79 row_newbcast:3 row_mask:0xf bank_mask:0xf\n"
"v_mul_f32_dpp v80, v162, v80 row_newbcast:0 row_mask:0xf bank_mask:0xf\n"
"v_mul_f32_dpp v81, v162, v81 row_newbcast:1 row_mask:0xf bank_mask:0xf\n"
"v_mul_f32_dpp v82, v162, v82 row_newbcast:2 row_mask:0xf bank_mask:0xf\n"
"v_mul_f32_dpp v83, v162, v83 row_newbcast:3 row_mask:0xf bank_mask:0xf\n"
"v_mul_f32_dpp v84, v162, v84 row_newbcast:0 row_mask:0xf bank_mask:0xf\n"
"v_mul_f32_dpp v85, v162, v85 row_newbcast:1 row_mask:0xf bank_mask:0xf\n"
"v_mul_f32_dpp v86, v162, v86 row_newbcast:2 row_mask:0xf bank_mask:0xf\n"
"v_mul_f32_dpp v87, v162, v87 row_newbcast:3 row_mask:0xf bank_mask:0xf\n"
"v_mul_f32_dpp v88, v162, v88 row_newbcast:0 row_mask:0xf bank_mask:0xf\n"
"v_mul_f32_dpp v89, v162, v89 row_newbcast:1 row_mask:0xf bank_mask:0xf\n"
"v_mul_f32_dpp v90, v162, v90 row_newbcast:2 row_mask:0xf bank_mask:0xf\n"
"v_mul_f32_dpp v91, v162, v91 row_newbcast:3 row_mask:0xf bank_mask:0xf\n"
"v_mul_f32_dpp v92, v162, v92 row_newbcast:0 row_mask:0xf bank_mask:0xf\n"
"v_mul_f32_dpp v93, v162, v93 row_newbcast:1 row_mask:0xf bank_mask:0xf\n"
"v_mul_f32_dpp v94, v162, v94 row_newbcast:2 row_mask:0xf bank_mask:0xf\n"
"v_mul_f32_dpp v95, v162, v95 row_newbcast:3 row_mask:0xf bank_mask:0xf\n"
"s_cmp_lg_u32 s83, s43\n"
"s_cbranch_scc1 LSK3A_%=\n"
"v_max3_f32 v165, v120, v121, v122\n"
"v_max3_f32 v165, v165, v123, v124\n"
"v_max3_f32 v165, v165, v125, v126\n"
"v_max3_f32 v165, v165, v127, v164\n"
"v_mov_b32 v166, v165\n"
"s_nop 1\n"
"v_permlane32_swap_b32 v165, v166\n"
"v_max_f32 v165, v165, v166\n"
"v_mov_b32 v166, v165\n"
"s_nop 1\n"
"v_permlane16_swap_b32 v165, v166\n"
"v_max_f32 v165, v165, v166\n"
"v_sub_f32 v166, v164, v165\n"
"v_exp_f32 v166, v166\n"
"v_mov_b32 v164, v165\n"
"v_sub_f32 v120, v120, v164\n"
"v_sub_f32 v121, v121, v164\n"
"v_sub_f32 v122, v122, v164\n"
"v_sub_f32 v123, v123, v164\n"
"v_sub_f32 v124, v124, v164\n"
"v_sub_f32 v125, v125, v164\n"
"v_sub_f32 v126, v126, v164\n"
"v_sub_f32 v127, v127, v164\n"
"v_exp_f32 v120, v120\n"
"v_exp_f32 v121, v121\n"
"v_exp_f32 v122, v122\n"
"v_exp_f32 v123, v123\n"
"v_exp_f32 v124, v124\n"
"v_exp_f32 v125, v125\n"
"v_exp_f32 v126, v126\n"
"v_exp_f32 v127, v127\n"
"v_mul_f32 v167, v167, v166\n"
"v_add_f32 v167, v167, v120\n"
"v_add_f32 v167, v167, v121\n"
"v_add_f32 v167, v167, v122\n"
"v_add_f32 v167, v167, v123\n"
"v_add_f32 v167, v167, v124\n"
"v_add_f32 v167, v167, v125\n"
"v_add_f32 v167, v167, v126\n"
"v_add_f32 v167, v167, v127\n"
"v_cvt_pk_bf16_f32 v120, v120, v121\n"
"v_cvt_pk_bf16_f32 v121, v122, v123\n"
"v_cvt_pk_bf16_f32 v122, v124, v125\n"
"v_cvt_pk_bf16_f32 v123, v126, v127\n"
"v_mov_b32_dpp v166, v166 row_ror:12 row_mask:0x2 bank_mask:0xf\n"
"s_nop 1\n"
"v_mov_b32_dpp v166, v166 row_ror:8 row_mask:0x4 bank_mask:0xf\n"
"s_nop 1\n"
"v_mov_b32_dpp v166, v166 row_ror:4 row_mask:0x8 bank_mask:0xf\n"
"v_accvgpr_read_b32 v124, a208\n"
"s_nop 1\n"
"v_mul_f32_dpp v124, v166, v124 row_newbcast:0 row_mask:0xf bank_mask:0xf\n"
"v_accvgpr_write_b32 a208, v124\n"
"v_accvgpr_read_b32 v125, a209\n"
"s_nop 1\n"
"v_mul_f32_dpp v125, v166, v125 row_newbcast:1 row_mask:0xf bank_mask:0xf\n"
"v_accvgpr_write_b32 a209, v125\n"
"v_accvgpr_read_b32 v126, a210\n"
"s_nop 1\n"
"v_mul_f32_dpp v126, v166, v126 row_newbcast:2 row_mask:0xf bank_mask:0xf\n"
"v_accvgpr_write_b32 a210, v126\n"
"v_accvgpr_read_b32 v127, a211\n"
"s_nop 1\n"
"v_mul_f32_dpp v127, v166, v127 row_newbcast:3 row_mask:0xf bank_mask:0xf\n"
"v_accvgpr_write_b32 a211, v127\n"
"v_accvgpr_read_b32 v124, a212\n"
"s_nop 1\n"
"v_mul_f32_dpp v124, v166, v124 row_newbcast:0 row_mask:0xf bank_mask:0xf\n"
"v_accvgpr_write_b32 a212, v124\n"
"v_accvgpr_read_b32 v125, a213\n"
"s_nop 1\n"
"v_mul_f32_dpp v125, v166, v125 row_newbcast:1 row_mask:0xf bank_mask:0xf\n"
"v_accvgpr_write_b32 a213, v125\n"
"v_accvgpr_read_b32 v126, a214\n"
"s_nop 1\n"
"v_mul_f32_dpp v126, v166, v126 row_newbcast:2 row_mask:0xf bank_mask:0xf\n"
"v_accvgpr_write_b32 a214, v126\n"
"v_accvgpr_read_b32 v127, a215\n"
"s_nop 1\n"
"v_mul_f32_dpp v127, v166, v127 row_newbcast:3 row_mask:0xf bank_mask:0xf\n"
"v_accvgpr_write_b32 a215, v127\n"
"v_accvgpr_read_b32 v124, a216\n"
"s_nop 1\n"
"v_mul_f32_dpp v124, v166, v124 row_newbcast:0 row_mask:0xf bank_mask:0xf\n"
"v_accvgpr_write_b32 a216, v124\n"
"v_accvgpr_read_b32 v125, a217\n"
"s_nop 1\n"
"v_mul_f32_dpp v125, v166, v125 row_newbcast:1 row_mask:0xf bank_mask:0xf\n"
"v_accvgpr_write_b32 a217, v125\n"
"v_accvgpr_read_b32 v126, a218\n"
"s_nop 1\n"
"v_mul_f32_dpp v126, v166, v126 row_newbcast:2 row_mask:0xf bank_mask:0xf\n"
"v_accvgpr_write_b32 a218, v126\n"
"v_accvgpr_read_b32 v127, a219\n"
"s_nop 1\n"
"v_mul_f32_dpp v127, v166, v127 row_newbcast:3 row_mask:0xf bank_mask:0xf\n"
"v_accvgpr_write_b32 a219, v127\n"
"v_accvgpr_read_b32 v124, a220\n"
"s_nop 1\n"
"v_mul_f32_dpp v124, v166, v124 row_newbcast:0 row_mask:0xf bank_mask:0xf\n"
"v_accvgpr_write_b32 a220, v124\n"
"v_accvgpr_read_b32 v125, a221\n"
"s_nop 1\n"
"v_mul_f32_dpp v125, v166, v125 row_newbcast:1 row_mask:0xf bank_mask:0xf\n"
"v_accvgpr_write_b32 a221, v125\n"
"v_accvgpr_read_b32 v126, a222\n"
"s_nop 1\n"
"v_mul_f32_dpp v126, v166, v126 row_newbcast:2 row_mask:0xf bank_mask:0xf\n"
"v_accvgpr_write_b32 a222, v126\n"
"v_accvgpr_read_b32 v127, a223\n"
"s_nop 1\n"
"v_mul_f32_dpp v127, v166, v127 row_newbcast:3 row_mask:0xf bank_mask:0xf\n"
"v_accvgpr_write_b32 a223, v127\n"
"v_accvgpr_read_b32 v124, a224\n"
"s_nop 1\n"
"v_mul_f32_dpp v124, v166, v124 row_newbcast:0 row_mask:0xf bank_mask:0xf\n"
"v_accvgpr_write_b32 a224, v124\n"
"v_accvgpr_read_b32 v125, a225\n"
"s_nop 1\n"
"v_mul_f32_dpp v125, v166, v125 row_newbcast:1 row_mask:0xf bank_mask:0xf\n"
"v_accvgpr_write_b32 a225, v125\n"
"v_accvgpr_read_b32 v126, a226\n"
"s_nop 1\n"
"v_mul_f32_dpp v126, v166, v126 row_newbcast:2 row_mask:0xf bank_mask:0xf\n"
"v_accvgpr_write_b32 a226, v126\n"
"v_accvgpr_read_b32 v127, a227\n"
"s_nop 1\n"
"v_mul_f32_dpp v127, v166, v127 row_newbcast:3 row_mask:0xf bank_mask:0xf\n"
"v_accvgpr_write_b32 a227, v127\n"
"v_accvgpr_read_b32 v124, a228\n"
"s_nop 1\n"
"v_mul_f32_dpp v124, v166, v124 row_newbcast:0 row_mask:0xf bank_mask:0xf\n"
"v_accvgpr_write_b32 a228, v124\n"
"v_accvgpr_read_b32 v125, a229\n"
"s_nop 1\n"
"v_mul_f32_dpp v125, v166, v125 row_newbcast:1 row_mask:0xf bank_mask:0xf\n"
"v_accvgpr_write_b32 a229, v125\n"
"v_accvgpr_read_b32 v126, a230\n"
"s_nop 1\n"
"v_mul_f32_dpp v126, v166, v126 row_newbcast:2 row_mask:0xf bank_mask:0xf\n"
"v_accvgpr_write_b32 a230, v126\n"
"v_accvgpr_read_b32 v127, a231\n"
"s_nop 1\n"
"v_mul_f32_dpp v127, v166, v127 row_newbcast:3 row_mask:0xf bank_mask:0xf\n"
"v_accvgpr_write_b32 a231, v127\n"
"v_accvgpr_read_b32 v124, a232\n"
"s_nop 1\n"
"v_mul_f32_dpp v124, v166, v124 row_newbcast:0 row_mask:0xf bank_mask:0xf\n"
"v_accvgpr_write_b32 a232, v124\n"
"v_accvgpr_read_b32 v125, a233\n"
"s_nop 1\n"
"v_mul_f32_dpp v125, v166, v125 row_newbcast:1 row_mask:0xf bank_mask:0xf\n"
"v_accvgpr_write_b32 a233, v125\n"
"v_accvgpr_read_b32 v126, a234\n"
"s_nop 1\n"
"v_mul_f32_dpp v126, v166, v126 row_newbcast:2 row_mask:0xf bank_mask:0xf\n"
"v_accvgpr_write_b32 a234, v126\n"
"v_accvgpr_read_b32 v127, a235\n"
"s_nop 1\n"
"v_mul_f32_dpp v127, v166, v127 row_newbcast:3 row_mask:0xf bank_mask:0xf\n"
"v_accvgpr_write_b32 a235, v127\n"
"v_accvgpr_read_b32 v124, a236\n"
"s_nop 1\n"
"v_mul_f32_dpp v124, v166, v124 row_newbcast:0 row_mask:0xf bank_mask:0xf\n"
"v_accvgpr_write_b32 a236, v124\n"
"v_accvgpr_read_b32 v125, a237\n"
"s_nop 1\n"
"v_mul_f32_dpp v125, v166, v125 row_newbcast:1 row_mask:0xf bank_mask:0xf\n"
"v_accvgpr_write_b32 a237, v125\n"
"v_accvgpr_read_b32 v126, a238\n"
"s_nop 1\n"
"v_mul_f32_dpp v126, v166, v126 row_newbcast:2 row_mask:0xf bank_mask:0xf\n"
"v_accvgpr_write_b32 a238, v126\n"
"v_accvgpr_read_b32 v127, a239\n"
"s_nop 1\n"
"v_mul_f32_dpp v127, v166, v127 row_newbcast:3 row_mask:0xf bank_mask:0xf\n"
"v_accvgpr_write_b32 a239, v127\n"
"LSK3A_%=:\n"
"s_nop 3\n"
"buffer_load_ushort v128, v146, s[88:91], s80 offen offset:0\n"
"buffer_load_ushort v129, v146, s[88:91], s80 offen offset:256\n"
"buffer_load_ushort v130, v146, s[88:91], s80 offen offset:512\n"
"buffer_load_ushort v131, v146, s[88:91], s80 offen offset:768\n"
"buffer_load_ushort v132, v146, s[88:91], s81 offen offset:0\n"
"buffer_load_ushort v133, v146, s[88:91], s81 offen offset:256\n"
"buffer_load_ushort v134, v146, s[88:91], s81 offen offset:512\n"
"buffer_load_ushort v135, v146, s[88:91], s81 offen offset:768\n"
"buffer_load_ushort v136, v146, s[88:91], s80 offen offset:32\n"
"buffer_load_ushort v137, v146, s[88:91], s80 offen offset:288\n"
"buffer_load_ushort v138, v146, s[88:91], s80 offen offset:544\n"
"buffer_load_ushort v139, v146, s[88:91], s80 offen offset:800\n"
"buffer_load_ushort v140, v146, s[88:91], s81 offen offset:32\n"
"buffer_load_ushort v141, v146, s[88:91], s81 offen offset:288\n"
"buffer_load_ushort v142, v146, s[88:91], s81 offen offset:544\n"
"buffer_load_ushort v143, v146, s[88:91], s81 offen offset:800\n"
"s_waitcnt vmcnt(0)\n"
"v_lshl_or_b32 v128, v129, 16, v128\n"
"v_lshl_or_b32 v129, v131, 16, v130\n"
"v_lshl_or_b32 v130, v133, 16, v132\n"
"v_lshl_or_b32 v131, v135, 16, v134\n"
"v_lshl_or_b32 v136, v137, 16, v136\n"
"v_lshl_or_b32 v137, v139, 16, v138\n"
"v_lshl_or_b32 v138, v141, 16, v140\n"
"v_lshl_or_b32 v139, v143, 16, v142\n"
"v_mfma_f32_16x16x32_bf16 v[0:3], v[96:99], v[128:131], v[0:3]\n"
"s_nop 0\n"
"v_mfma_f32_16x16x32_bf16 v[4:7], v[96:99], v[136:139], v[4:7]\n"
"v_mfma_f32_16x16x32_bf16 v[32:35], v[104:107], v[128:131], v[32:35]\n"
"v_mfma_f32_16x16x32_bf16 v[36:39], v[104:107], v[136:139], v[36:39]\n"
"v_mfma_f32_16x16x32_bf16 v[64:67], v[112:115], v[128:131], v[64:67]\n"
"v_mfma_f32_16x16x32_bf16 v[68:71], v[112:115], v[136:139], v[68:71]\n"
"s_cmp_lg_u32 s83, s43\n"
"s_cbranch_scc1 LSK3B0_%=\n"
"v_mfma_f32_16x16x32_bf16 a[208:211], v[120:123], v[128:131], a[208:211]\n"
"v_mfma_f32_16x16x32_bf16 a[212:215], v[120:123], v[136:139], a[212:215]\n"
"LSK3B0_%=:\n"
"s_nop 3\n"
"buffer_load_ushort v128, v147, s[88:91], s80 offen offset:0\n"
"buffer_load_ushort v129, v147, s[88:91], s80 offen offset:256\n"
"buffer_load_ushort v130, v147, s[88:91], s80 offen offset:512\n"
"buffer_load_ushort v131, v147, s[88:91], s80 offen offset:768\n"
"buffer_load_ushort v132, v147, s[88:91], s81 offen offset:0\n"
"buffer_load_ushort v133, v147, s[88:91], s81 offen offset:256\n"
"buffer_load_ushort v134, v147, s[88:91], s81 offen offset:512\n"
"buffer_load_ushort v135, v147, s[88:91], s81 offen offset:768\n"
"buffer_load_ushort v136, v147, s[88:91], s80 offen offset:32\n"
"buffer_load_ushort v137, v147, s[88:91], s80 offen offset:288\n"
"buffer_load_ushort v138, v147, s[88:91], s80 offen offset:544\n"
"buffer_load_ushort v139, v147, s[88:91], s80 offen offset:800\n"
"buffer_load_ushort v140, v147, s[88:91], s81 offen offset:32\n"
"buffer_load_ushort v141, v147, s[88:91], s81 offen offset:288\n"
"buffer_load_ushort v142, v147, s[88:91], s81 offen offset:544\n"
"buffer_load_ushort v143, v147, s[88:91], s81 offen offset:800\n"
"s_waitcnt vmcnt(0)\n"
"v_lshl_or_b32 v128, v129, 16, v128\n"
"v_lshl_or_b32 v129, v131, 16, v130\n"
"v_lshl_or_b32 v130, v133, 16, v132\n"
"v_lshl_or_b32 v131, v135, 16, v134\n"
"v_lshl_or_b32 v136, v137, 16, v136\n"
"v_lshl_or_b32 v137, v139, 16, v138\n"
"v_lshl_or_b32 v138, v141, 16, v140\n"
"v_lshl_or_b32 v139, v143, 16, v142\n"
"v_mfma_f32_16x16x32_bf16 v[8:11], v[96:99], v[128:131], v[8:11]\n"
"s_nop 0\n"
"v_mfma_f32_16x16x32_bf16 v[12:15], v[96:99], v[136:139], v[12:15]\n"
"v_mfma_f32_16x16x32_bf16 v[40:43], v[104:107], v[128:131], v[40:43]\n"
"v_mfma_f32_16x16x32_bf16 v[44:47], v[104:107], v[136:139], v[44:47]\n"
"v_mfma_f32_16x16x32_bf16 v[72:75], v[112:115], v[128:131], v[72:75]\n"
"v_mfma_f32_16x16x32_bf16 v[76:79], v[112:115], v[136:139], v[76:79]\n"
"s_cmp_lg_u32 s83, s43\n"
"s_cbranch_scc1 LSK3B1_%=\n"
"v_mfma_f32_16x16x32_bf16 a[216:219], v[120:123], v[128:131], a[216:219]\n"
"v_mfma_f32_16x16x32_bf16 a[220:223], v[120:123], v[136:139], a[220:223]\n"
"LSK3B1_%=:\n"
"s_nop 3\n"
"buffer_load_ushort v128, v146, s[88:91], s80 offen offset:128\n"
"buffer_load_ushort v129, v146, s[88:91], s80 offen offset:384\n"
"buffer_load_ushort v130, v146, s[88:91], s80 offen offset:640\n"
"buffer_load_ushort v131, v146, s[88:91], s80 offen offset:896\n"
"buffer_load_ushort v132, v146, s[88:91], s81 offen offset:128\n"
"buffer_load_ushort v133, v146, s[88:91], s81 offen offset:384\n"
"buffer_load_ushort v134, v146, s[88:91], s81 offen offset:640\n"
"buffer_load_ushort v135, v146, s[88:91], s81 offen offset:896\n"
"buffer_load_ushort v136, v146, s[88:91], s80 offen offset:160\n"
"buffer_load_ushort v137, v146, s[88:91], s80 offen offset:416\n"
"buffer_load_ushort v138, v146, s[88:91], s80 offen offset:672\n"
"buffer_load_ushort v139, v146, s[88:91], s80 offen offset:928\n"
"buffer_load_ushort v140, v146, s[88:91], s81 offen offset:160\n"
"buffer_load_ushort v141, v146, s[88:91], s81 offen offset:416\n"
"buffer_load_ushort v142, v146, s[88:91], s81 offen offset:672\n"
"buffer_load_ushort v143, v146, s[88:91], s81 offen offset:928\n"
"s_waitcnt vmcnt(0)\n"
"v_lshl_or_b32 v128, v129, 16, v128\n"
"v_lshl_or_b32 v129, v131, 16, v130\n"
"v_lshl_or_b32 v130, v133, 16, v132\n"
"v_lshl_or_b32 v131, v135, 16, v134\n"
"v_lshl_or_b32 v136, v137, 16, v136\n"
"v_lshl_or_b32 v137, v139, 16, v138\n"
"v_lshl_or_b32 v138, v141, 16, v140\n"
"v_lshl_or_b32 v139, v143, 16, v142\n"
"v_mfma_f32_16x16x32_bf16 v[16:19], v[96:99], v[128:131], v[16:19]\n"
"s_nop 0\n"
"v_mfma_f32_16x16x32_bf16 v[20:23], v[96:99], v[136:139], v[20:23]\n"
"v_mfma_f32_16x16x32_bf16 v[48:51], v[104:107], v[128:131], v[48:51]\n"
"v_mfma_f32_16x16x32_bf16 v[52:55], v[104:107], v[136:139], v[52:55]\n"
"v_mfma_f32_16x16x32_bf16 v[80:83], v[112:115], v[128:131], v[80:83]\n"
"v_mfma_f32_16x16x32_bf16 v[84:87], v[112:115], v[136:139], v[84:87]\n"
"s_cmp_lg_u32 s83, s43\n"
"s_cbranch_scc1 LSK3B2_%=\n"
"v_mfma_f32_16x16x32_bf16 a[224:227], v[120:123], v[128:131], a[224:227]\n"
"v_mfma_f32_16x16x32_bf16 a[228:231], v[120:123], v[136:139], a[228:231]\n"
"LSK3B2_%=:\n"
"s_nop 3\n"
"buffer_load_ushort v128, v147, s[88:91], s80 offen offset:128\n"
"buffer_load_ushort v129, v147, s[88:91], s80 offen offset:384\n"
"buffer_load_ushort v130, v147, s[88:91], s80 offen offset:640\n"
"buffer_load_ushort v131, v147, s[88:91], s80 offen offset:896\n"
"buffer_load_ushort v132, v147, s[88:91], s81 offen offset:128\n"
"buffer_load_ushort v133, v147, s[88:91], s81 offen offset:384\n"
"buffer_load_ushort v134, v147, s[88:91], s81 offen offset:640\n"
"buffer_load_ushort v135, v147, s[88:91], s81 offen offset:896\n"
"buffer_load_ushort v136, v147, s[88:91], s80 offen offset:160\n"
"buffer_load_ushort v137, v147, s[88:91], s80 offen offset:416\n"
"buffer_load_ushort v138, v147, s[88:91], s80 offen offset:672\n"
"buffer_load_ushort v139, v147, s[88:91], s80 offen offset:928\n"
"buffer_load_ushort v140, v147, s[88:91], s81 offen offset:160\n"
"buffer_load_ushort v141, v147, s[88:91], s81 offen offset:416\n"
"buffer_load_ushort v142, v147, s[88:91], s81 offen offset:672\n"
"buffer_load_ushort v143, v147, s[88:91], s81 offen offset:928\n"
"s_waitcnt vmcnt(0)\n"
"v_lshl_or_b32 v128, v129, 16, v128\n"
"v_lshl_or_b32 v129, v131, 16, v130\n"
"v_lshl_or_b32 v130, v133, 16, v132\n"
"v_lshl_or_b32 v131, v135, 16, v134\n"
"v_lshl_or_b32 v136, v137, 16, v136\n"
"v_lshl_or_b32 v137, v139, 16, v138\n"
"v_lshl_or_b32 v138, v141, 16, v140\n"
"v_lshl_or_b32 v139, v143, 16, v142\n"
"v_mfma_f32_16x16x32_bf16 v[24:27], v[96:99], v[128:131], v[24:27]\n"
"s_nop 0\n"
"v_mfma_f32_16x16x32_bf16 v[28:31], v[96:99], v[136:139], v[28:31]\n"
"v_mfma_f32_16x16x32_bf16 v[56:59], v[104:107], v[128:131], v[56:59]\n"
"v_mfma_f32_16x16x32_bf16 v[60:63], v[104:107], v[136:139], v[60:63]\n"
"v_mfma_f32_16x16x32_bf16 v[88:91], v[112:115], v[128:131], v[88:91]\n"
"v_mfma_f32_16x16x32_bf16 v[92:95], v[112:115], v[136:139], v[92:95]\n"
"s_cmp_lg_u32 s83, s43\n"
"s_cbranch_scc1 LSK3B3_%=\n"
"v_mfma_f32_16x16x32_bf16 a[232:235], v[120:123], v[128:131], a[232:235]\n"
"v_mfma_f32_16x16x32_bf16 a[236:239], v[120:123], v[136:139], a[236:239]\n"
"LSK3B3_%=:\n"
"s_nop 3\n"
"s_nop 9\n"
"s_add_u32 s80, s80, 8192\n"
"s_add_u32 s81, s81, 8192\n"
"s_add_u32 s82, s82, 1\n"
"s_cmp_lt_u32 s82, 32\n"
"s_cbranch_scc1 LSLOOP_%=\n"
"v_mov_b32 v154, v155\n"
"s_nop 1\n"
"v_permlane32_swap_b32 v155, v154\n"
"v_add_f32 v155, v155, v154\n"
"v_mov_b32 v154, v155\n"
"s_nop 1\n"
"v_permlane16_swap_b32 v155, v154\n"
"v_add_f32 v155, v155, v154\n"
"v_mov_b32 v196, v155\n"
"v_mov_b32 v158, v159\n"
"s_nop 1\n"
"v_permlane32_swap_b32 v159, v158\n"
"v_add_f32 v159, v159, v158\n"
"v_mov_b32 v158, v159\n"
"s_nop 1\n"
"v_permlane16_swap_b32 v159, v158\n"
"v_add_f32 v159, v159, v158\n"
"v_mov_b32 v198, v159\n"
"v_mov_b32 v162, v163\n"
"s_nop 1\n"
"v_permlane32_swap_b32 v163, v162\n"
"v_add_f32 v163, v163, v162\n"
"v_mov_b32 v162, v163\n"
"s_nop 1\n"
"v_permlane16_swap_b32 v163, v162\n"
"v_add_f32 v163, v163, v162\n"
"v_mov_b32 v200, v163\n"
"v_mov_b32 v166, v167\n"
"s_nop 1\n"
"v_permlane32_swap_b32 v167, v166\n"
"v_add_f32 v167, v167, v166\n"
"v_mov_b32 v166, v167\n"
"s_nop 1\n"
"v_permlane16_swap_b32 v167, v166\n"
"v_add_f32 v167, v167, v166\n"
"v_mov_b32 v202, v167\n"
"v_sub_f32 v192, 0, v164\n"
"v_mov_b32 v96, 0\n"
"v_mov_b32 v97, 0\n"
"v_mov_b32 v98, 0\n"
"v_mov_b32 v99, 0\n"
"v_mov_b32 v100, 0\n"
"v_mov_b32 v101, 0\n"
"v_mov_b32 v102, 0\n"
"v_mov_b32 v103, 0\n"
"v_mov_b32 v104, 0\n"
"v_mov_b32 v105, 0\n"
"v_mov_b32 v106, 0\n"
"v_mov_b32 v107, 0\n"
"v_mov_b32 v108, 0\n"
"v_mov_b32 v109, 0\n"
"v_mov_b32 v110, 0\n"
"v_mov_b32 v111, 0\n"
"v_mov_b32 v112, 0\n"
"v_mov_b32 v113, 0\n"
"v_mov_b32 v114, 0\n"
"v_mov_b32 v115, 0\n"
"v_mov_b32 v116, 0\n"
"v_mov_b32 v117, 0\n"
"v_mov_b32 v118, 0\n"
"v_mov_b32 v119, 0\n"
"v_mov_b32 v120, 0\n"
"v_mov_b32 v121, 0\n"
"v_mov_b32 v122, 0\n"
"v_mov_b32 v123, 0\n"
"v_mov_b32 v204, 0\n"
"v_mov_b32 v205, 0\n"
"s_nop 3\n"
"s_mov_b32 s68, 0\n"
"s_cmp_eq_u32 s47, 4\n"
"s_cbranch_scc1 LBACK0_%=\n"
"s_cmp_eq_u32 s47, 8\n"
"s_cbranch_scc1 LBACK1_%=\n"
"s_cmp_eq_u32 s47, 12\n"
"s_cbranch_scc1 LBACK2_%=\n"
"s_branch LBACK3_%=\n"
"LFIN_%=:\n"
    :: [kalo]"s"((unsigned)ka), [kahi]"s"((unsigned)(ka >> 32)), [wg]"s"(blockIdx.x), [wid]"s"(wid)
    : "v0","v1","v2","v3","v4","v5","v6","v7","v8","v9","v10","v11","v12","v13","v14","v15","v16","v17","v18","v19","v20","v21","v22","v23","v24","v25","v26","v27","v28","v29","v30","v31","v32","v33","v34","v35","v36","v37","v38","v39","v40","v41","v42","v43","v44","v45","v46","v47","v48","v49","v50","v51","v52","v53","v54","v55","v56","v57","v58","v59","v60","v61","v62","v63","v64","v65","v66","v67","v68","v69","v70","v71","v72","v73","v74","v75","v76","v77","v78","v79","v80","v81","v82","v83","v84","v85","v86","v87","v88","v89","v90","v91","v92","v93","v94","v95","v96","v97","v98","v99","v100","v101","v102","v103","v104","v105","v106","v107","v108","v109","v110","v111","v112","v113","v114","v115","v116","v117","v118","v119","v120","v121","v122","v123","v124","v125","v126","v127","v128","v129","v130","v131","v132","v133","v134","v135","v136","v137","v138","v139","v140","v141","v142","v143","v144","v145","v146","v147","v148","v149","v150","v151","v152","v153","v154","v155","v156","v157","v158","v159","v160","v161","v162","v163","v164","v165","v166","v167","v168","v169","v170","v171","v172","v173","v174","v175","v176","v177","v178","v179","v180","v181","v182","v183","v184","v185","v186","v187","v188","v189","v190","v191","v192","v193","v194","v195","v196","v197","v198","v199","v200","v201","v202","v203","v204","v205","v206","v207","v208","v209","v210","v211","v212","v213","v214","v215","v216","v217","v218","v219","v220","v221","v222","v223","v224","v225","v226","v227","v228","v229","v230","v231","v232","v233","v234","v235","v236","v237","v238","v239","v240","v241","v242","v243","v244","v245","v246","v247","v248","v249","v250","v251","v252","v253","v254","v255","a0","a1","a2","a3","a4","a5","a6","a7","a8","a9","a10","a11","a12","a13","a14","a15","a16","a17","a18","a19","a20","a21","a22","a23","a24","a25","a26","a27","a28","a29","a30","a31","a32","a33","a34","a35","a36","a37","a38","a39","a40","a41","a42","a43","a44","a45","a46","a47","a48","a49","a50","a51","a52","a53","a54","a55","a56","a57","a58","a59","a60","a61","a62","a63","a64","a65","a66","a67","a68","a69","a70","a71","a72","a73","a74","a75","a76","a77","a78","a79","a80","a81","a82","a83","a84","a85","a86","a87","a88","a89","a90","a91","a92","a93","a94","a95","a96","a97","a98","a99","a100","a101","a102","a103","a104","a105","a106","a107","a108","a109","a110","a111","a112","a113","a114","a115","a116","a117","a118","a119","a120","a121","a122","a123","a124","a125","a126","a127","a128","a129","a130","a131","a132","a133","a134","a135","a136","a137","a138","a139","a140","a141","a142","a143","a144","a145","a146","a147","a148","a149","a150","a151","a152","a153","a154","a155","a156","a157","a158","a159","a160","a161","a162","a163","a164","a165","a166","a167","a168","a169","a170","a171","a172","a173","a174","a175","a176","a177","a178","a179","a180","a181","a182","a183","a184","a185","a186","a187","a188","a189","a190","a191","a192","a193","a194","a195","a196","a197","a198","a199","a200","a201","a202","a203","a204","a205","a206","a207","a208","a209","a210","a211","a212","a213","a214","a215","a216","a217","a218","a219","a220","a221","a222","a223","a224","a225","a226","a227","a228","a229","a230","a231","a232","a233","a234","a235","a236","a237","a238","a239","a240","a241","a242","a243","a244","a245","a246","a247","a248","a249","a250","a251","a252","a253","a254","a255","s6","s7","s8","s9","s10","s11","s12","s13","s14","s15","s16","s17","s18","s19","s20","s21","s22","s23","s24","s25","s26","s27","s28","s29","s30","s31","s32","s33","s34","s35","s36","s37","s38","s39","s40","s41","s42","s43","s44","s45","s46","s47","s48","s49","s50","s51","s52","s53","s54","s55","s56","s57","s58","s59","s60","s61","s62","s63","s64","s65","s66","s67","s68","s69","s70","s71","s72","s73","s74","s75","s76","s77","s78","s79","s80","s81","s82","s83","s84","s85","s86","s87","s88","s89","s90","s91","s92","s93","s94","s95","s96","s97","s98","s99","s100","vcc","scc","m0","memory");
}
extern "C" void kernel_launch(void* const* d_in, const int* in_sizes, int n_in, void* d_out, int out_size, void* d_ws, size_t ws_size, hipStream_t stream) {
  char* xch = (char*)d_ws + ((ws_size - (size_t)270532608) & ~(size_t)0xfffff);
  (void)hipFuncSetAttribute((const void*)fused_attn, hipFuncAttributeMaxDynamicSharedMemorySize, 163840);
  hipLaunchKernelGGL(fused_attn, dim3(256), dim3(256), 163840, stream,
                     (const char*)d_in[0], (const char*)d_in[1], (const char*)d_in[2], (const char*)d_in[3], (const char*)d_in[4],
                     (const char*)d_in[5], (const char*)d_in[6], (const char*)d_in[7], (const char*)d_in[8], (const char*)d_in[9], xch, (char*)d_out, (char*)d_ws);
}
```

```cpp
#include <hip/hip_runtime.h>
#include <hip/hip_bf16.h>
#include <stdint.h>

__global__ __launch_bounds__(256, 1)
void fused_attn(const char* xA, const char* kA, const char* vA, const char* wA, const char* uA,
                const char* xB, const char* kB, const char* vB, const char* wB, const char* uB, char* xch, char* out, char* flags) {
  extern __shared__ __attribute__((aligned(16))) char lds[];
  const unsigned wid = __builtin_amdgcn_readfirstlane(threadIdx.x >> 6);
  const unsigned long long ka = (unsigned long long)__builtin_amdgcn_kernarg_segment_ptr();
  asm volatile(
"s_memtime s[64:65]\n"
"s_memrealtime s[66:67]\n"
"s_bfe_u32 s10, %[wg], 0x10002\n"
"s_mul_i32 s40, s10, 40\n"
"s_add_u32 s40, s40, %[kalo]\n"
"s_addc_u32 s41, %[kahi], 0\n"
"s_mov_b32 s78, %[kalo]\n"
"s_mov_b32 s79, %[kahi]\n"
"s_load_dwordx2 s[24:25], s[40:41], 0\n"
"s_load_dwordx2 s[28:29], s[40:41], 24\n"
"s_load_dwordx2 s[6:7], s[78:79], 0x60\n"
"s_load_dwordx2 s[16:17], s[40:41], 8\n"
"s_load_dwordx2 s[20:21], s[40:41], 16\n"
"s_load_dwordx2 s[8:9], s[40:41], 32\n"
"s_load_dwordx4 s[12:15], s[78:79], 0x50\n"
"s_mov_b32 s42, %[wid]\n"
"s_and_b32 s11, %[wg], 1\n"
"s_lshl_b32 s11, s11, 3\n"
"s_lshr_b32 s60, %[wg], 5\n"
"s_add_u32 s11, s11, s60\n"
"s_bfe_u32 s61, %[wg], 0x10001\n"
"s_bfe_u32 s60, %[wg], 0x20003\n"
"s_lshl2_add_u32 s61, s61, s60\n"
"s_getpc_b64 s[80:81]\n"
"LCP_%=:\n"
"s_mov_b32 s82, LEND_%=-LCP_%=\n"
"s_mov_b32 s83, 0x20000\n"
"v_mbcnt_lo_u32_b32 v2, -1, 0\n"
"v_lshlrev_b32 v2, 7, v2\n"
"s_lshr_b32 s60, %[wg], 3\n"
"s_lshl2_add_u32 s60, s60, s42\n"
"s_mul_i32 s60, s60, 640\n"
"s_mov_b64 exec, 31\n"
"buffer_load_dword v3, v2, s[80:83], s60 offen\n"
"s_mov_b64 exec, -1\n"
"s_mov_b32 s19, 0x20000\n"
"s_mov_b32 s23, 0x20000\n"
"s_mov_b32 s27, 0x20000\n"
"s_mov_b32 s31, 0x20000\n"
"s_mov_b32 s35, 0x20000\n"
"s_mov_b32 s39, 0x20000\n"
"s_mov_b32 s26, 286720\n"
"s_lshl_b32 s44, s42, 12\n"
"s_mov_b32 s50, s44\n"
"s_mul_i32 s51, s42, 7168\n"
"s_add_u32 s51, s51, 0x10000\n"
"s_mov_b32 s74, 0x8000\n"
"s_mov_b32 s58, 524288\n"
"s_mov_b32 s47, 0\n"
"s_add_u32 s60, s11, s61\n"
"s_and_b32 s60, s60, 0\n"
"s_mul_i32 s60, s60, 3\n"
"s_lshl_b32 s49, s60, 7\n"
"s_lshl_b32 s48, s60, 19\n"
"v_mbcnt_lo_u32_b32 v4, -1, 0\n"
"v_mbcnt_hi_u32_b32 v4, -1, v4\n"
"v_and_b32 v5, 15, v4\n"
"v_lshrrev_b32 v6, 4, v4\n"
"v_lshrrev_b32 v7, 3, v4\n"
"v_and_b32 v8, 7, v4\n"
"v_lshlrev_b32 v9, 1, v6\n"
"v_xor_b32 v9, v9, v8\n"
"v_lshlrev_b32 v9, 4, v9\n"
"s_mul_i32 s60, s42, 71680\n"
"v_mul_u32_u24 v10, 1280, v7\n"
"v_add3_u32 v248, v10, v9, s60\n"
"v_add_u32 v249, 9216, v248\n"
"v_add_u32 v254, 18432, v248\n"
"v_add_u32 v255, 27648, v248\n"
"v_and_b32 v10, 2, v7\n"
"v_xor_b32 v240, v10, v8\n"
"v_lshlrev_b32 v240, 4, v240\n"
"v_bfe_u32 v10, v7, 0, 1\n"
"v_lshl_add_u32 v240, v10, 8, v240\n"
"v_bfe_u32 v10, v7, 1, 1\n"
"v_lshl_add_u32 v240, v10, 9, v240\n"
"v_bfe_u32 v10, v7, 2, 1\n"
"v_lshl_add_u32 v240, v10, 7, v240\n"
"s_and_b32 s60, s42, 1\n"
"v_lshl_add_u32 v240, s60, 15, v240\n"
"s_lshr_b32 s60, s42, 1\n"
"v_lshl_add_u32 v240, s60, 16, v240\n"
"v_and_b32 v10, 1, v6\n"
"v_xor_b32 v10, v10, v8\n"
"v_lshlrev_b32 v10, 4, v10\n"
"v_lshl_add_u32 v10, v7, 13, v10\n"
"s_lshl_b32 s60, s42, 17\n"
"v_add_u32 v10, s60, v10\n"
"v_add_u32 v224, 1024, v10\n"
"v_add_u32 v225, 128, v10\n"
"v_xor_b32 v10, 64, v10\n"
"v_add_u32 v226, 64512, v10\n"
"v_add_u32 v227, 63616, v10\n"
"s_mul_i32 s40, s11, 286720\n"
"s_lshl_b32 s41, s61, 10\n"
"s_sub_u32 s30, 5243904, s41\n"
"s_sub_u32 s41, s41, 1024\n"
"s_ashr_i32 s60, s41, 31\n"
"s_lshl4_add_u32 s92, s10, s11\n"
"s_lshl_b32 s93, s92, 9\n"
"v_mov_b32 v0, 0\n"
"s_waitcnt lgkmcnt(0)\n"
"s_add_u32 s24, s24, s40\n"
"s_addc_u32 s25, s25, 0\n"
"s_and_b32 s25, s25, 0xffff\n"
"s_add_u32 s28, s28, s41\n"
"s_addc_u32 s29, s29, s60\n"
"s_and_b32 s29, s29, 0xffff\n"
"s_add_u32 s6, s6, s93\n"
"s_addc_u32 s7, s7, 0\n"
"buffer_load_dword v12, v248, s[24:27], s49 offen\n"
"buffer_load_dword v13, v224, s[28:31], s48 offen\n"
"global_load_dword v1, v0, s[6:7] offset:128 sc1\n"
"s_mov_b32 m0, s51\n"
"s_nop 0\n"
"buffer_load_dwordx4 v248, s[24:27], s49 offen lds\n"
"buffer_load_dwordx4 v249, s[24:27], s49 offen offset:1024 lds\n"
"buffer_load_dwordx4 v254, s[24:27], s49 offen offset:2048 lds\n"
"buffer_load_dwordx4 v255, s[24:27], s49 offen offset:3072 lds\n"
"s_add_u32 m0, s51, 4096\n"
"s_add_u32 s40, s49, 40960\n"
"buffer_load_dwordx4 v248, s[24:27], s40 offen lds\n"
"buffer_load_dwordx4 v249, s[24:27], s40 offen offset:1024 lds\n"
"buffer_load_dwordx4 v254, s[24:27], s40 offen offset:2048 lds\n"
"s_add_u32 s49, s49, 128\n"
"s_sub_u32 s41, s49, 1280\n"
"s_min_u32 s49, s49, s41\n"
"s_xor_b32 s51, s51, s74\n"
"s_mov_b32 m0, s50\n"
"s_nop 0\n"
"buffer_load_dwordx4 v224, s[28:31], s48 offen lds\n"
"buffer_load_dwordx4 v225, s[28:31], s48 offen offset:1024 lds\n"
"buffer_load_dwordx4 v226, s[28:31], s48 offen offset:2048 lds\n"
"buffer_load_dwordx4 v227, s[28:31], s48 offen offset:3072 lds\n"
"s_xor_b32 s50, s50, s74\n"
"s_lshl2_add_u32 s63, s11, s42\n"
"s_mul_i32 s63, s63, 10240\n"
"s_and_b32 s78, s42, 1\n"
"s_lshl_b32 s43, s78, 6\n"
"s_mov_b32 s77, 0x80000000\n"
"s_mov_b32 s75, 0x10000\n"
"s_mov_b32 s73, 0xffff0000\n"
"s_mov_b32 s46, 0x3e0293ee\n"
"s_mov_b32 s45, 0x63800000\n"
"s_mov_b32 s62, 9\n"
"v_lshl_add_u32 v242, v4, 4, s44\n"
"s_lshr_b32 s79, s42, 1\n"
"s_lshl4_add_u32 s76, s79, s78\n"
"s_lshl_b32 s76, s76, 10\n"
"s_add_u32 s76, s76, 0x20000\n"
"s_sub_u32 s76, s76, s44\n"
"v_lshlrev_b32 v9, 6, v6\n"
"v_xor_b32 v9, v9, v242\n"
"v_add_u32 v243, s76, v9\n"
"v_and_b32 v9, 6, v5\n"
"s_xor_b32 m0, s50, 0xc000\n"
"s_add_u32 s40, s48, 256\n"
"buffer_load_dwordx4 v224, s[28:31], s40 offen lds\n"
"v_xor_b32 v9, v9, v6\n"
"v_lshlrev_b32 v9, 4, v9\n"
"v_lshl_add_u32 v10, v5, 7, v9\n"
"s_mul_i32 s79, s42, 6144\n"
"s_add_u32 s79, s79, 0x10000\n"
"v_add_u32 v246, s79, v10\n"
"v_xor_b32 v247, 64, v246\n"
"s_lshr_b32 s79, s42, 1\n"
"s_lshl_b32 s79, s79, 11\n"
"buffer_load_dwordx4 v225, s[28:31], s40 offen offset:1024 lds\n"
"s_add_u32 s79, s79, 90112\n"
"v_add_u32 v252, s79, v10\n"
"v_xor_b32 v253, 64, v252\n"
"s_xor_b32 s79, s42, 1\n"
"s_mul_i32 s79, s79, 6144\n"
"s_add_u32 s79, s79, 0x10000\n"
"v_add_u32 v250, s79, v10\n"
"v_xor_b32 v251, 64, v250\n"
"s_mul_i32 s79, s78, 8256\n"
"buffer_load_dwordx4 v226, s[28:31], s40 offen offset:2048 lds\n"
"v_lshlrev_b32 v9, 10, v6\n"
"v_bfe_u32 v10, v4, 2, 2\n"
"v_lshl_add_u32 v9, v10, 7, v9\n"
"v_and_b32 v10, 3, v4\n"
"v_lshl_add_u32 v9, v10, 3, v9\n"
"v_bfe_u32 v10, v4, 3, 2\n"
"v_lshl_add_u32 v9, v10, 5, v9\n"
"s_add_u32 s79, s79, 0x10000\n"
"v_xor_b32 v232, s79, v9\n"
"buffer_load_dwordx4 v227, s[28:31], s40 offen offset:3072 lds\n"
"s_cmp_eq_u32 s47, 8\n"
"s_cselect_b32 s41, s52, s58\n"
"s_add_u32 s48, s48, s41\n"
"s_sub_u32 s41, s48, 5242880\n"
"s_min_u32 s48, s48, s41\n"
"v_xor_b32 v233, 32, v232\n"
"v_xor_b32 v234, 64, v232\n"
"v_xor_b32 v235, 96, v232\n"
"v_xor_b32 v236, 0x2000, v232\n"
"v_xor_b32 v237, 0x2000, v233\n"
"v_xor_b32 v238, 0x2000, v234\n"
"v_xor_b32 v239, 0x2000, v235\n"
"v_lshlrev_b32 v9, 11, v6\n"
"v_bfe_u32 v10, v4, 2, 2\n"
"s_mov_b32 m0, s50\n"
"s_nop 0\n"
"buffer_load_dwordx4 v224, s[28:31], s48 offen lds\n"
"v_lshl_add_u32 v9, v10, 7, v9\n"
"v_and_b32 v10, 1, v4\n"
"v_lshl_add_u32 v9, v10, 3, v9\n"
"v_bfe_u32 v10, v4, 1, 1\n"
"v_lshl_add_u32 v9, v10, 5, v9\n"
"v_bfe_u32 v10, v4, 3, 1\n"
"v_lshl_add_u32 v9, v10, 4, v9\n"
"v_bfe_u32 v10, v4, 4, 1\n"
"v_lshl_add_u32 v9, v10, 6, v9\n"
"buffer_load_dwordx4 v225, s[28:31], s48 offen offset:1024 lds\n"
"v_xor_b32 v244, s43, v9\n"
"v_xor_b32 v245, 16, v244\n"
"s_lshl3_add_u32 s79, s11, s61\n"
"s_lshl_b32 s79, s79, 20\n"
"s_sub_u32 s79, s79, 262144\n"
"s_ashr_i32 s60, s79, 31\n"
"s_add_u32 s16, s16, s79\n"
"s_addc_u32 s17, s17, s60\n"
"s_and_b32 s17, s17, 0xffff\n"
"buffer_load_dwordx4 v226, s[28:31], s48 offen offset:2048 lds\n"
"s_mov_b32 s18, 1310720\n"
"s_mov_b64 s[84:85], s[16:17]\n"
"s_mov_b64 s[86:87], s[18:19]\n"
"s_add_u32 s20, s20, s79\n"
"s_addc_u32 s21, s21, s60\n"
"s_and_b32 s21, s21, 0xffff\n"
"s_mov_b32 s22, 1310720\n"
"s_mov_b64 s[88:89], s[20:21]\n"
"s_mov_b64 s[90:91], s[22:23]\n"
"buffer_load_dwordx4 v227, s[28:31], s48 offen offset:3072 lds\n"
"s_xor_b32 s50, s50, s74\n"
"s_mov_b32 s57, 122880\n"
"s_bfe_u32 s55, %[wg], 65541\n"
"s_mul_i32 s55, s55, 1024\n"
"s_xor_b32 s57, s57, s55\n"
"s_bfe_u32 s55, %[wg], 65542\n"
"s_mul_i32 s55, s55, 2048\n"
"s_xor_b32 s57, s57, s55\n"
"s_and_b32 s54, %[wg], 144\n"
"s_bcnt1_i32_b32 s54, s54\n"
"s_mov_b32 m0, s51\n"
"s_nop 0\n"
"buffer_load_dwordx4 v248, s[24:27], s49 offen lds\n"
"s_and_b32 s54, s54, 1\n"
"s_lshl_b32 s54, s54, 13\n"
"s_xor_b32 s57, s57, s54\n"
"s_lshl_b32 s54, s54, 1\n"
"s_sub_u32 s54, 21504, s54\n"
"s_mov_b32 s55, 385024\n"
"s_xor_b32 s56, s55, s57\n"
"s_mov_b32 s59, 1024\n"
"s_add_u32 s52, s11, s61\n"
"buffer_load_dwordx4 v249, s[24:27], s49 offen offset:1024 lds\n"
"s_and_b32 s52, s52, 0\n"
"s_mul_i32 s52, s52, 3\n"
"s_add_u32 s52, s52, 1\n"
"s_lshl_b32 s53, s52, 7\n"
"s_lshl_b32 s52, s52, 19\n"
"s_add_u32 s52, s52, 512\n"
"s_mul_i32 s79, s61, 655360\n"
"s_add_u32 s8, s8, s79\n"
"s_addc_u32 s9, s9, 0\n"
"buffer_load_dwordx4 v254, s[24:27], s49 offen offset:2048 lds\n"
"s_and_b32 s9, s9, 0xffff\n"
"s_add_u32 s12, s12, 0x1fffff\n"
"s_addc_u32 s13, s13, 0\n"
"s_and_b32 s12, s12, 0xffe00000\n"
"s_mul_i32 s96, s42, 3\n"
"s_and_b32 s96, s96, 5\n"
"s_cmp_eq_u32 s42, 3\n"
"s_cselect_b32 s96, 5, s96\n"
"s_lshl_b32 s79, s61, 8\n"
"buffer_load_dwordx4 v255, s[24:27], s49 offen offset:3072 lds\n"
"s_add_u32 s96, s96, s79\n"
"s_add_u32 s96, s96, 160\n"
"s_lshl_b32 s96, s96, 12\n"
"s_mul_i32 s79, s92, 8388608\n"
"s_add_u32 s32, s12, s79\n"
"s_addc_u32 s33, s13, 0\n"
"s_and_b32 s33, s33, 0xffff\n"
"s_mov_b32 s34, 8388608\n"
"s_mul_i32 s79, s11, 573440\n"
"s_add_u32 m0, s51, 4096\n"
"s_add_u32 s40, s49, 40960\n"
"buffer_load_dwordx4 v248, s[24:27], s40 offen lds\n"
"s_mul_i32 s60, s10, 1280\n"
"s_sub_u32 s38, 573440, s60\n"
"s_add_u32 s79, s79, s60\n"
"s_add_u32 s36, s14, s79\n"
"s_addc_u32 s37, s15, 0\n"
"s_and_b32 s37, s37, 0xffff\n"
"s_lshl2_add_u32 s93, s61, s42\n"
"s_add_u32 s95, s61, 1\n"
"s_and_b32 s95, s95, 7\n"
"buffer_load_dwordx4 v249, s[24:27], s40 offen offset:1024 lds\n"
"s_lshl_b32 s95, s95, 7\n"
"s_add_u32 s100, s61, 2\n"
"s_and_b32 s100, s100, 7\n"
"s_lshl_b32 s100, s100, 7\n"
"s_lshl_b32 s60, s61, 7\n"
"s_add_u32 s60, s60, s43\n"
"s_mul_i32 s97, s42, 122880\n"
"s_add_u32 s97, s97, s60\n"
"s_lshr_b32 s98, s42, 1\n"
"buffer_load_dwordx4 v254, s[24:27], s40 offen offset:2048 lds\n"
"s_add_u32 s49, s49, 128\n"
"s_sub_u32 s41, s49, 1280\n"
"s_min_u32 s49, s49, s41\n"
"s_xor_b32 s51, s51, s74\n"
"s_add_u32 s98, s98, 12\n"
"s_mul_i32 s98, s98, 40960\n"
"s_add_u32 s98, s98, s60\n"
"s_mul_i32 s60, s93, 37\n"
"s_lshr_b32 s60, s60, 8\n"
"s_mul_i32 s79, s60, 7\n"
"s_sub_u32 s79, s93, s79\n"
"s_lshl_b32 s94, s60, 4\n"
"s_add_u32 s94, s94, s79\n"
"s_lshl_b32 s94, s94, 10\n"
"s_add_u32 s94, s94, 524288\n"
"s_mul_i32 s99, s60, 3\n"
"s_lshr_b32 s78, s79, 1\n"
"s_add_u32 s99, s99, s78\n"
"s_lshr_b32 s78, s60, 1\n"
"s_add_u32 s78, s78, 12\n"
"s_cmp_eq_u32 s79, 6\n"
"s_cselect_b32 s99, s78, s99\n"
"s_mul_i32 s99, s99, 40960\n"
"s_xor_b32 s79, s79, s60\n"
"s_and_b32 s79, s79, 1\n"
"s_lshl_b32 s79, s79, 6\n"
"s_add_u32 s99, s99, s79\n"
"s_add_u32 s99, s99, 1024\n"
"s_cmp_eq_u32 s61, 7\n"
"s_cselect_b32 s99, 0x40000000, s99\n"
"s_cselect_b32 s94, 0x40000000, s94\n"
"s_waitcnt vmcnt(27)\n"
"v_and_b32 v12, 0x7fff0000, v12\n"
"v_and_b32 v13, 0x7fff0000, v13\n"
"s_nop 0\n"
"v_max_u32_dpp v12, v12, v12 quad_perm:[1,0,3,2] row_mask:0xf bank_mask:0xf\n"
"v_max_u32_dpp v13, v13, v13 quad_perm:[1,0,3,2] row_mask:0xf bank_mask:0xf\n"
"s_nop 0\n"
"v_max_u32_dpp v12, v12, v12 quad_perm:[2,3,0,1] row_mask:0xf bank_mask:0xf\n"
"v_max_u32_dpp v13, v13, v13 quad_perm:[2,3,0,1] row_mask:0xf bank_mask:0xf\n"
"s_memtime s[80:81]\n"
"s_memrealtime s[82:83]\n"
"s_nop 0\n"
"v_max_u32_dpp v12, v12, v12 row_half_mirror row_mask:0xf bank_mask:0xf\n"
"v_max_u32_dpp v13, v13, v13 row_half_mirror row_mask:0xf bank_mask:0xf\n"
"s_nop 0\n"
"v_min_u32_dpp v12, v12, v12 row_mirror row_mask:0xf bank_mask:0xf\n"
"v_min_u32_dpp v13, v13, v13 row_mirror row_mask:0xf bank_mask:0xf\n"
"s_nop 0\n"
"v_min_u32_dpp v12, v12, v12 row_bcast:15 row_mask:0xa bank_mask:0xf\n"
"v_min_u32_dpp v13, v13, v13 row_bcast:15 row_mask:0xa bank_mask:0xf\n"
"s_nop 0\n"
"v_min_u32_dpp v12, v12, v12 row_bcast:31 row_mask:0xc bank_mask:0xf\n"
"v_min_u32_dpp v13, v13, v13 row_bcast:31 row_mask:0xc bank_mask:0xf\n"
"s_nop 0\n"
"v_mul_f32 v12, v12, v13\n"
"v_mul_f32 v12, 0x43c80000, v12\n"
"v_min_f32 v12, 0x71800000, v12\n"
"v_mul_f32 v13, s46, v12\n"
"s_nop 0\n"
"v_readlane_b32 s70, v12, 63\n"
"v_readlane_b32 s71, v12, 63\n"
"v_readlane_b32 s69, v13, 63\n"
"s_nop 1\n"
"v_mov_b64 v[228:229], s[70:71]\n"
"v_mov_b64 v[230:231], s[70:71]\n"
"s_waitcnt vmcnt(26)\n"
"v_readfirstlane_b32 s92, v1\n"
"v_mov_b64 v[0:1], s[70:71]\n"
"v_mov_b64 v[2:3], s[70:71]\n"
"s_mov_b32 s70, s46\n"
"s_xor_b32 s71, s69, s77\n"
"s_waitcnt lgkmcnt(0)\n"
"s_sub_u32 s80, s80, s64\n"
"s_sub_u32 s82, s82, s66\n"
"s_mul_i32 s80, s80, 10\n"
"s_mul_i32 s82, s82, 211\n"
"s_cmp_gt_u32 s80, s82\n"
"s_cselect_b32 s64, 1, 0\n"
"s_cselect_b32 s68, 655360, 0\n"
"s_cmp_eq_u32 s42, 0\n"
"s_cselect_b32 s64, s64, 0\n"
"s_mov_b32 s65, 0\n"
"s_add_u32 s66, s66, 2800\n"
"s_mov_b32 s67, s66\n"
"s_mov_b32 s72, 118\n"
"s_mov_b64 vcc, s[64:65]\n"
"s_sub_u32 s63, s63, s44\n"
"s_add_u32 s80, s8, s63\n"
"s_addc_u32 s81, s9, 0\n"
"s_add_u32 s82, s44, 704\n"
"s_cmp_lg_u32 s68, 0\n"
"s_cselect_b32 s82, s82, 0\n"
"s_mov_b32 s83, 0x20000\n"
"s_nop 2\n"
"s_mul_i32 s60, s92, 0x9e3779b1\n"
"s_add_u32 s60, s60, 0x7f4a7c15\n"
"s_waitcnt vmcnt(15)\n"
"s_barrier\n"
"ds_read_b128 a[136:139], v246 offset:0\n"
"ds_read_b128 a[140:143], v247 offset:0\n"
"ds_read_b64_tr_b16 a[192:193], v244 offset:0\n"
"ds_read_b64_tr_b16 a[194:195], v244 offset:512\n"
"ds_read_b64_tr_b16 a[196:197], v244 offset:8192\n"
"ds_read_b64_tr_b16 a[198:199], v244 offset:8704\n"
"ds_read_b64_tr_b16 a[200:201], v245 offset:0\n"
"ds_read_b64_tr_b16 a[202:203], v245 offset:512\n"
"ds_read_b64_tr_b16 a[204:205], v245 offset:8192\n"
"ds_read_b64_tr_b16 a[206:207], v245 offset:8704\n"
"ds_read_b64_tr_b16 a[208:209], v244 offset:1024\n"
"ds_read_b64_tr_b16 a[210:211], v244 offset:1536\n"
"ds_read_b64_tr_b16 a[212:213], v244 offset:9216\n"
"ds_read_b64_tr_b16 a[214:215], v244 offset:9728\n"
"s_waitcnt lgkmcnt(10)\n"
"v_mfma_f32_16x16x32_bf16 v[112:115], a[192:195], a[136:139], v[228:231]\n"
"ds_read_b64_tr_b16 a[216:217], v245 offset:1024\n"
"ds_read_b64_tr_b16 a[218:219], v245 offset:1536\n"
"s_waitcnt lgkmcnt(10)\n"
"v_mfma_f32_16x16x32_bf16 v[112:115], a[196:199], a[140:143], v[112:115]\n"
"ds_read_b64_tr_b16 a[220:221], v245 offset:9216\n"
"ds_read_b64_tr_b16 a[222:223], v245 offset:9728\n"
"s_waitcnt lgkmcnt(10)\n"
"v_mfma_f32_16x16x32_bf16 v[116:119], a[200:203], a[136:139], v[228:231]\n"
"s_waitcnt lgkmcnt(8)\n"
"ds_read_b128 a[144:147], v246 offset:2048\n"
"v_mfma_f32_16x16x32_bf16 v[116:119], a[204:207], a[140:143], v[116:119]\n"
"s_waitcnt lgkmcnt(7)\n"
"ds_read_b128 a[148:151], v247 offset:2048\n"
"v_mfma_f32_16x16x32_bf16 v[120:123], a[208:211], a[136:139], v[228:231]\n"
"s_waitcnt lgkmcnt(6)\n"
"ds_read_b128 a[152:155], v246 offset:4096\n"
"v_mfma_f32_16x16x32_bf16 v[120:123], a[212:215], a[140:143], v[120:123]\n"
"s_waitcnt lgkmcnt(5)\n"
"ds_read_b128 a[156:159], v247 offset:4096\n"
"v_mfma_f32_16x16x32_bf16 v[124:127], a[216:219], a[136:139], v[228:231]\n"
"s_waitcnt lgkmcnt(4)\n"
"ds_read_b128 a[160:163], v252 offset:0\n"
"v_mfma_f32_16x16x32_bf16 v[124:127], a[220:223], a[140:143], v[124:127]\n"
"s_waitcnt lgkmcnt(4)\n"
"ds_read_b128 a[164:167], v253 offset:0\n"
"v_mfma_f32_16x16x32_bf16 v[140:143], a[216:219], a[144:147], v[228:231]\n"
"s_waitcnt lgkmcnt(4)\n"
"ds_read_b128 a[168:171], v250 offset:0\n"
"v_mfma_f32_16x16x32_bf16 v[140:143], a[220:223], a[148:151], v[140:143]\n"
"ds_read_b128 a[172:175], v251 offset:0\n"
"v_mfma_f32_16x16x32_bf16 v[136:139], a[208:211], a[144:147], v[228:231]\n"
"s_waitcnt vmcnt(11)\n"
"ds_read_b128 a[176:179], v250 offset:2048\n"
"v_mfma_f32_16x16x32_bf16 v[136:139], a[212:215], a[148:151], v[136:139]\n"
"s_barrier\n"
"v_mfma_f32_16x16x32_bf16 v[132:135], a[200:203], a[144:147], v[228:231]\n"
"s_add_u32 s40, s48, 256\n"
"s_xor_b32 m0, s50, 0xc000\n"
"ds_read_b128 a[180:183], v251 offset:2048\n"
"v_mfma_f32_16x16x32_bf16 v[132:135], a[204:207], a[148:151], v[132:135]\n"
"buffer_load_dwordx4 v224, s[28:31], s40 offen lds\n"
"v_mfma_f32_16x16x32_bf16 v[128:131], a[192:195], a[144:147], v[228:231]\n"
"ds_read_b128 a[184:187], v250 offset:4096\n"
"v_mfma_f32_16x16x32_bf16 v[128:131], a[196:199], a[148:151], v[128:131]\n"
"s_waitcnt lgkmcnt(5)\n"
"ds_read_b128 a[188:191], v251 offset:4096\n"
"v_mfma_f32_16x16x32_bf16 v[144:147], a[192:195], a[152:155], v[228:231]\n"
"ds_read_b64_tr_b16 a[224:225], v244 offset:16384\n"
"v_mfma_f32_16x16x32_bf16 v[144:147], a[196:199], a[156:159], v[144:147]\n"
"ds_read_b64_tr_b16 a[226:227], v244 offset:16896\n"
"v_mfma_f32_16x16x32_bf16 v[148:151], a[200:203], a[152:155], v[228:231]\n"
"ds_read_b64_tr_b16 a[228:229], v244 offset:24576\n"
"v_mfma_f32_16x16x32_bf16 v[148:151], a[204:207], a[156:159], v[148:151]\n"
"buffer_load_dwordx4 v225, s[28:31], s40 offen offset:1024 lds\n"
"v_mfma_f32_16x16x32_bf16 v[152:155], a[208:211], a[152:155], v[228:231]\n"
"ds_read_b64_tr_b16 a[230:231], v244 offset:25088\n"
"v_mfma_f32_16x16x32_bf16 v[152:155], a[212:215], a[156:159], v[152:155]\n"
"ds_read_b64_tr_b16 a[232:233], v245 offset:16384\n"
"v_mfma_f32_16x16x32_bf16 v[156:159], a[216:219], a[152:155], v[228:231]\n"
"ds_read_b64_tr_b16 a[234:235], v245 offset:16896\n"
"v_mfma_f32_16x16x32_bf16 v[156:159], a[220:223], a[156:159], v[156:159]\n"
"ds_read_b64_tr_b16 a[236:237], v245 offset:24576\n"
"v_mfma_f32_16x16x32_bf16 v[172:175], a[216:219], a[160:163], v[228:231]\n"
"ds_read_b64_tr_b16 a[238:239], v245 offset:25088\n"
"v_mfma_f32_16x16x32_bf16 v[172:175], a[220:223], a[164:167], v[172:175]\n"
"buffer_load_dwordx4 v226, s[28:31], s40 offen offset:2048 lds\n"
"v_mfma_f32_16x16x32_bf16 v[168:171], a[208:211], a[160:163], v[228:231]\n"
"ds_read_b64_tr_b16 a[240:241], v244 offset:17408\n"
"v_mfma_f32_16x16x32_bf16 v[168:171], a[212:215], a[164:167], v[168:171]\n"
"ds_read_b64_tr_b16 a[242:243], v244 offset:17920\n"
"v_mfma_f32_16x16x32_bf16 v[164:167], a[200:203], a[160:163], v[228:231]\n"
"ds_read_b64_tr_b16 a[244:245], v244 offset:25600\n"
"v_mfma_f32_16x16x32_bf16 v[164:167], a[204:207], a[164:167], v[164:167]\n"
"buffer_load_dwordx4 v227, s[28:31], s40 offen offset:3072 lds\n"
"v_mfma_f32_16x16x32_bf16 v[160:163], a[192:195], a[160:163], v[228:231]\n"
"s_cmp_eq_u32 s47, 8\n"
"ds_read_b64_tr_b16 a[246:247], v244 offset:26112\n"
"v_xor_b32 v244, s74, v244\n"
"v_mfma_f32_16x16x32_bf16 v[160:163], a[196:199], a[164:167], v[160:163]\n"
"s_waitcnt lgkmcnt(5)\n"
"s_cselect_b32 s41, s52, s58\n"
"ds_read_b64_tr_b16 a[248:249], v245 offset:17408\n"
"v_xor_b32 v246, s74, v246\n"
"v_mfma_f32_16x16x32_bf16 v[176:179], a[192:195], a[168:171], v[228:231]\n"
"s_add_u32 s48, s48, s41\n"
"ds_read_b64_tr_b16 a[250:251], v245 offset:17920\n"
"v_xor_b32 v247, s74, v247\n"
"v_mfma_f32_16x16x32_bf16 v[176:179], a[196:199], a[172:175], v[176:179]\n"
"s_sub_u32 s41, s48, 5242880\n"
"ds_read_b64_tr_b16 a[252:253], v245 offset:25600\n"
"v_xor_b32 v252, s74, v252\n"
"v_mfma_f32_16x16x32_bf16 v[180:183], a[200:203], a[168:171], v[228:231]\n"
"s_min_u32 s48, s48, s41\n"
"ds_read_b64_tr_b16 a[254:255], v245 offset:26112\n"
"v_xor_b32 v245, s74, v245\n"
"v_mfma_f32_16x16x32_bf16 v[180:183], a[204:207], a[172:175], v[180:183]\n"
"v_xor_b32 v253, s74, v253\n"
"v_xor_b32 v250, s74, v250\n"
"v_mfma_f32_16x16x32_bf16 v[184:187], a[208:211], a[168:171], v[228:231]\n"
"s_mov_b32 m0, s50\n"
"v_xor_b32 v251, s74, v251\n"
"v_mfma_f32_16x16x32_bf16 v[184:187], a[212:215], a[172:175], v[184:187]\n"
"buffer_load_dwordx4 v224, s[28:31], s48 offen lds\n"
"v_mfma_f32_16x16x32_bf16 v[188:191], a[216:219], a[168:171], v[228:231]\n"
"v_mfma_f32_16x16x32_bf16 v[188:191], a[220:223], a[172:175], v[188:191]\n"
"v_mfma_f32_16x16x32_bf16 v[204:207], a[216:219], a[176:179], v[228:231]\n"
"v_mfma_f32_16x16x32_bf16 v[204:207], a[220:223], a[180:183], v[204:207]\n"
"buffer_load_dwordx4 v225, s[28:31], s48 offen offset:1024 lds\n"
"v_mfma_f32_16x16x32_bf16 v[200:203], a[208:211], a[176:179], v[228:231]\n"
"v_mfma_f32_16x16x32_bf16 v[200:203], a[212:215], a[180:183], v[200:203]\n"
"v_mfma_f32_16x16x32_bf16 v[196:199], a[200:203], a[176:179], v[228:231]\n"
"v_mfma_f32_16x16x32_bf16 v[196:199], a[204:207], a[180:183], v[196:199]\n"
"v_mfma_f32_16x16x32_bf16 v[192:195], a[192:195], a[176:179], v[228:231]\n"
"v_mfma_f32_16x16x32_bf16 v[192:195], a[196:199], a[180:183], v[192:195]\n"
"buffer_load_dwordx4 v226, s[28:31], s48 offen offset:2048 lds\n"
"v_mfma_f32_16x16x32_bf16 v[208:211], a[192:195], a[184:187], v[228:231]\n"
"v_mfma_f32_16x16x32_bf16 v[208:211], a[196:199], a[188:191], v[208:211]\n"
"v_mfma_f32_16x16x32_bf16 v[212:215], a[200:203], a[184:187], v[228:231]\n"
"v_mfma_f32_16x16x32_bf16 v[212:215], a[204:207], a[188:191], v[212:215]\n"
"v_mfma_f32_16x16x32_bf16 v[216:219], a[208:211], a[184:187], v[228:231]\n"
"v_mfma_f32_16x16x32_bf16 v[216:219], a[212:215], a[188:191], v[216:219]\n"
"buffer_load_dwordx4 v227, s[28:31], s48 offen offset:3072 lds\n"
"v_mfma_f32_16x16x32_bf16 v[220:223], a[216:219], a[184:187], v[228:231]\n"
"s_xor_b32 s50, s50, s74\n"
"v_mfma_f32_16x16x32_bf16 v[220:223], a[220:223], a[188:191], v[220:223]\n"
"v_mfma_f32_16x16x32_bf16 v[0:3], a[224:227], a[136:139], v[228:231]\n"
"v_mfma_f32_16x16x32_bf16 v[0:3], a[228:231], a[140:143], v[0:3]\n"
"v_mfma_f32_16x16x32_bf16 v[4:7], a[232:235], a[136:139], v[228:231]\n"
"s_waitcnt vmcnt(8) lgkmcnt(0)\n"
"v_mfma_f32_16x16x32_bf16 v[4:7], a[236:239], a[140:143], v[4:7]\n"
"s_barrier\n"
"v_mfma_f32_16x16x32_bf16 v[8:11], a[240:243], a[136:139], v[228:231]\n"
"s_mov_b32 m0, s51\n"
"ds_read_b64_tr_b16 a[192:193], v244 offset:0\n"
"v_mfma_f32_16x16x32_bf16 v[8:11], a[244:247], a[140:143], v[8:11]\n"
"buffer_load_dwordx4 v248, s[24:27], s49 offen lds\n"
"v_mfma_f32_16x16x32_bf16 v[12:15], a[248:251], a[136:139], v[228:231]\n"
"ds_read_b64_tr_b16 a[194:195], v244 offset:512\n"
"v_mfma_f32_16x16x32_bf16 v[12:15], a[252:255], a[140:143], v[12:15]\n"
"ds_read_b128 a[136:139], v246 offset:0\n"
"v_mfma_f32_16x16x32_bf16 v[28:31], a[248:251], a[144:147], v[228:231]\n"
"ds_read_b64_tr_b16 a[196:197], v244 offset:8192\n"
"v_mfma_f32_16x16x32_bf16 v[28:31], a[252:255], a[148:151], v[28:31]\n"
"ds_read_b64_tr_b16 a[198:199], v244 offset:8704\n"
"v_mfma_f32_16x16x32_bf16 v[24:27], a[240:243], a[144:147], v[228:231]\n"
"ds_read_b128 a[140:143], v247 offset:0\n"
"v_mfma_f32_16x16x32_bf16 v[24:27], a[244:247], a[148:151], v[24:27]\n"
"buffer_load_dwordx4 v249, s[24:27], s49 offen offset:1024 lds\n"
"v_mfma_f32_16x16x32_bf16 v[20:23], a[232:235], a[144:147], v[228:231]\n"
"ds_read_b64_tr_b16 a[200:201], v245 offset:0\n"
"v_mfma_f32_16x16x32_bf16 v[20:23], a[236:239], a[148:151], v[20:23]\n"
"ds_read_b64_tr_b16 a[202:203], v245 offset:512\n"
"v_mfma_f32_16x16x32_bf16 v[16:19], a[224:227], a[144:147], v[228:231]\n"
"ds_read_b64_tr_b16 a[204:205], v245 offset:8192\n"
"v_mfma_f32_16x16x32_bf16 v[16:19], a[228:231], a[148:151], v[16:19]\n"
"ds_read_b64_tr_b16 a[206:207], v245 offset:8704\n"
"v_mfma_f32_16x16x32_bf16 v[32:35], a[224:227], a[152:155], v[228:231]\n"
"ds_read_b64_tr_b16 a[208:209], v244 offset:1024\n"
"v_mfma_f32_16x16x32_bf16 v[32:35], a[228:231], a[156:159], v[32:35]\n"
"buffer_load_dwordx4 v254, s[24:27], s49 offen offset:2048 lds\n"
"v_mfma_f32_16x16x32_bf16 v[36:39], a[232:235], a[152:155], v[228:231]\n"
"ds_read_b64_tr_b16 a[210:211], v244 offset:1536\n"
"v_mfma_f32_16x16x32_bf16 v[36:39], a[236:239], a[156:159], v[36:39]\n"
"ds_read_b64_tr_b16 a[212:213], v244 offset:9216\n"
"v_mfma_f32_16x16x32_bf16 v[40:43], a[240:243], a[152:155], v[228:231]\n"
"ds_read_b64_tr_b16 a[214:215], v244 offset:9728\n"
"v_mfma_f32_16x16x32_bf16 v[40:43], a[244:247], a[156:159], v[40:43]\n"
"ds_read_b64_tr_b16 a[216:217], v245 offset:1024\n"
"v_mfma_f32_16x16x32_bf16 v[44:47], a[248:251], a[152:155], v[228:231]\n"
"ds_read_b64_tr_b16 a[218:219], v245 offset:1536\n"
"v_mfma_f32_16x16x32_bf16 v[44:47], a[252:255], a[156:159], v[44:47]\n"
"buffer_load_dwordx4 v255, s[24:27], s49 offen offset:3072 lds\n"
"v_mfma_f32_16x16x32_bf16 v[60:63], a[248:251], a[160:163], v[228:231]\n"
"ds_read_b64_tr_b16 a[220:221], v245 offset:9216\n"
"v_mfma_f32_16x16x32_bf16 v[60:63], a[252:255], a[164:167], v[60:63]\n"
"ds_read_b64_tr_b16 a[222:223], v245 offset:9728\n"
"v_mfma_f32_16x16x32_bf16 v[56:59], a[240:243], a[160:163], v[228:231]\n"
"ds_read_b128 a[144:147], v246 offset:2048\n"
"v_mfma_f32_16x16x32_bf16 v[56:59], a[244:247], a[164:167], v[56:59]\n"
"s_add_u32 s40, s49, 40960\n"
"ds_read_b128 a[148:151], v247 offset:2048\n"
"v_mfma_f32_16x16x32_bf16 v[52:55], a[232:235], a[160:163], v[228:231]\n"
"s_add_u32 m0, s51, 4096\n"
"ds_read_b128 a[152:155], v246 offset:4096\n"
"v_mfma_f32_16x16x32_bf16 v[52:55], a[236:239], a[164:167], v[52:55]\n"
"buffer_load_dwordx4 v248, s[24:27], s40 offen lds\n"
"v_mfma_f32_16x16x32_bf16 v[48:51], a[224:227], a[160:163], v[228:231]\n"
"ds_read_b128 a[156:159], v247 offset:4096\n"
"v_mfma_f32_16x16x32_bf16 v[48:51], a[228:231], a[164:167], v[48:51]\n"
"ds_read_b128 a[160:163], v252 offset:0\n"
"v_mfma_f32_16x16x32_bf16 v[64:67], a[224:227], a[168:171], v[228:231]\n"
"ds_read_b128 a[164:167], v253 offset:0\n"
"v_mfma_f32_16x16x32_bf16 v[64:67], a[228:231], a[172:175], v[64:67]\n"
"v_mfma_f32_16x16x32_bf16 v[68:71], a[232:235], a[168:171], v[228:231]\n"
"v_mfma_f32_16x16x32_bf16 v[68:71], a[236:239], a[172:175], v[68:71]\n"
"buffer_load_dwordx4 v249, s[24:27], s40 offen offset:1024 lds\n"
"v_mfma_f32_16x16x32_bf16 v[72:75], a[240:243], a[168:171], v[228:231]\n"
"v_mfma_f32_16x16x32_bf16 v[72:75], a[244:247], a[172:175], v[72:75]\n"
"v_mfma_f32_16x16x32_bf16 v[76:79], a[248:251], a[168:171], v[228:231]\n"
"ds_read_b128 a[168:171], v250 offset:0\n"
"v_mfma_f32_16x16x32_bf16 v[76:79], a[252:255], a[172:175], v[76:79]\n"
"ds_read_b128 a[172:175], v251 offset:0\n"
"v_mfma_f32_16x16x32_bf16 v[92:95], a[248:251], a[176:179], v[228:231]\n"
"v_mfma_f32_16x16x32_bf16 v[92:95], a[252:255], a[180:183], v[92:95]\n"
"buffer_load_dwordx4 v254, s[24:27], s40 offen offset:2048 lds\n"
"v_mfma_f32_16x16x32_bf16 v[88:91], a[240:243], a[176:179], v[228:231]\n"
"s_add_u32 s49, s49, 128\n"
"v_mfma_f32_16x16x32_bf16 v[88:91], a[244:247], a[180:183], v[88:91]\n"
"s_sub_u32 s41, s49, 1280\n"
"v_mfma_f32_16x16x32_bf16 v[84:87], a[232:235], a[176:179], v[228:231]\n"
"s_min_u32 s49, s49, s41\n"
"v_mfma_f32_16x16x32_bf16 v[84:87], a[236:239], a[180:183], v[84:87]\n"
"s_xor_b32 s51, s51, s74\n"
"v_mfma_f32_16x16x32_bf16 v[80:83], a[224:227], a[176:179], v[228:231]\n"
"ds_read_b128 a[176:179], v250 offset:2048\n"
"v_mfma_f32_16x16x32_bf16 v[80:83], a[228:231], a[180:183], v[80:83]\n"
"ds_read_b128 a[180:183], v251 offset:2048\n"
"v_mfma_f32_16x16x32_bf16 v[96:99], a[224:227], a[184:187], v[228:231]\n"
"v_mfma_f32_16x16x32_bf16 v[96:99], a[228:231], a[188:191], v[96:99]\n"
"v_mfma_f32_16x16x32_bf16 v[100:103], a[232:235], a[184:187], v[228:231]\n"
"v_mfma_f32_16x16x32_bf16 v[100:103], a[236:239], a[188:191], v[100:103]\n"
"v_mfma_f32_16x16x32_bf16 v[104:107], a[240:243], a[184:187], v[228:231]\n"
"v_mfma_f32_16x16x32_bf16 v[104:107], a[244:247], a[188:191], v[104:107]\n"
"v_mfma_f32_16x16x32_bf16 v[108:111], a[248:251], a[184:187], v[228:231]\n"
"s_add_u32 s47, s47, 1\n"
"ds_read_b128 a[184:187], v250 offset:4096\n"
"v_mfma_f32_16x16x32_bf16 v[108:111], a[252:255], a[188:191], v[108:111]\n"
"s_waitcnt lgkmcnt(1)\n"
"ds_read_b128 a[188:191], v251 offset:4096\n"
"LPE_%=:\n"
"s_cmp_lg_u32 s68, 0\n"
"s_cbranch_scc1 LPN_%=\n"
"LPL_%=:\n"
"v_mfma_f32_16x16x32_bf16 v[112:115], a[192:195], a[136:139], v[112:115]\n"
"v_mfma_f32_16x16x32_bf16 v[112:115], a[196:199], a[140:143], v[112:115]\n"
"v_mfma_f32_16x16x32_bf16 v[116:119], a[200:203], a[136:139], v[116:119]\n"
"s_waitcnt vmcnt(11)\n"
"v_mfma_f32_16x16x32_bf16 v[116:119], a[204:207], a[140:143], v[116:119]\n"
"s_barrier\n"
"v_mfma_f32_16x16x32_bf16 v[120:123], a[208:211], a[136:139], v[120:123]\n"
"s_add_u32 s40, s48, 256\n"
"s_xor_b32 m0, s50, 0xc000\n"
"ds_read_b64_tr_b16 a[224:225], v244 offset:16384\n"
"v_mfma_f32_16x16x32_bf16 v[120:123], a[212:215], a[140:143], v[120:123]\n"
"buffer_load_dwordx4 v224, s[28:31], s40 offen lds\n"
"v_mfma_f32_16x16x32_bf16 v[124:127], a[216:219], a[136:139], v[124:127]\n"
"ds_read_b64_tr_b16 a[226:227], v244 offset:16896\n"
"v_mfma_f32_16x16x32_bf16 v[124:127], a[220:223], a[140:143], v[124:127]\n"
"ds_read_b64_tr_b16 a[228:229], v244 offset:24576\n"
"v_mfma_f32_16x16x32_bf16 v[140:143], a[216:219], a[144:147], v[140:143]\n"
"ds_read_b64_tr_b16 a[230:231], v244 offset:25088\n"
"v_mfma_f32_16x16x32_bf16 v[140:143], a[220:223], a[148:151], v[140:143]\n"
"ds_read_b64_tr_b16 a[232:233], v245 offset:16384\n"
"v_mfma_f32_16x16x32_bf16 v[136:139], a[208:211], a[144:147], v[136:139]\n"
"ds_read_b64_tr_b16 a[234:235], v245 offset:16896\n"
"v_mfma_f32_16x16x32_bf16 v[136:139], a[212:215], a[148:151], v[136:139]\n"
"buffer_load_dwordx4 v225, s[28:31], s40 offen offset:1024 lds\n"
"v_mfma_f32_16x16x32_bf16 v[132:135], a[200:203], a[144:147], v[132:135]\n"
"ds_read_b64_tr_b16 a[236:237], v245 offset:24576\n"
"v_mfma_f32_16x16x32_bf16 v[132:135], a[204:207], a[148:151], v[132:135]\n"
"ds_read_b64_tr_b16 a[238:239], v245 offset:25088\n"
"v_mfma_f32_16x16x32_bf16 v[128:131], a[192:195], a[144:147], v[128:131]\n"
"ds_read_b64_tr_b16 a[240:241], v244 offset:17408\n"
"v_mfma_f32_16x16x32_bf16 v[128:131], a[196:199], a[148:151], v[128:131]\n"
"ds_read_b64_tr_b16 a[242:243], v244 offset:17920\n"
"v_mfma_f32_16x16x32_bf16 v[144:147], a[192:195], a[152:155], v[144:147]\n"
"ds_read_b64_tr_b16 a[244:245], v244 offset:25600\n"
"v_mfma_f32_16x16x32_bf16 v[144:147], a[196:199], a[156:159], v[144:147]\n"
"buffer_load_dwordx4 v226, s[28:31], s40 offen offset:2048 lds\n"
"v_mfma_f32_16x16x32_bf16 v[148:151], a[200:203], a[152:155], v[148:151]\n"
"ds_read_b64_tr_b16 a[246:247], v244 offset:26112\n"
"v_mfma_f32_16x16x32_bf16 v[148:151], a[204:207], a[156:159], v[148:151]\n"
"ds_read_b64_tr_b16 a[248:249], v245 offset:17408\n"
"v_mfma_f32_16x16x32_bf16 v[152:155], a[208:211], a[152:155], v[152:155]\n"
"ds_read_b64_tr_b16 a[250:251], v245 offset:17920\n"
"v_mfma_f32_16x16x32_bf16 v[152:155], a[212:215], a[156:159], v[152:155]\n"
"ds_read_b64_tr_b16 a[252:253], v245 offset:25600\n"
"v_mfma_f32_16x16x32_bf16 v[156:159], a[216:219], a[152:155], v[156:159]\n"
"ds_read_b64_tr_b16 a[254:255], v245 offset:26112\n"
"v_mfma_f32_16x16x32_bf16 v[156:159], a[220:223], a[156:159], v[156:159]\n"
"buffer_load_dwordx4 v227, s[28:31], s40 offen offset:3072 lds\n"
"v_mfma_f32_16x16x32_bf16 v[172:175], a[216:219], a[160:163], v[172:175]\n"
"s_cmp_eq_u32 s47, 8\n"
"v_mfma_f32_16x16x32_bf16 v[172:175], a[220:223], a[164:167], v[172:175]\n"
"s_cselect_b32 s41, s52, s58\n"
"v_mfma_f32_16x16x32_bf16 v[168:171], a[208:211], a[160:163], v[168:171]\n"
"s_add_u32 s48, s48, s41\n"
"v_mfma_f32_16x16x32_bf16 v[168:171], a[212:215], a[164:167], v[168:171]\n"
"s_sub_u32 s41, s48, 5242880\n"
"v_mfma_f32_16x16x32_bf16 v[164:167], a[200:203], a[160:163], v[164:167]\n"
"s_min_u32 s48, s48, s41\n"
"v_mfma_f32_16x16x32_bf16 v[164:167], a[204:207], a[164:167], v[164:167]\n"
"v_xor_b32 v244, s74, v244\n"
"v_xor_b32 v246, s74, v246\n"
"v_mfma_f32_16x16x32_bf16 v[160:163], a[192:195], a[160:163], v[160:163]\n"
"s_mov_b32 m0, s50\n"
"v_xor_b32 v245, s74, v245\n"
"v_xor_b32 v247, s74, v247\n"
"v_mfma_f32_16x16x32_bf16 v[160:163], a[196:199], a[164:167], v[160:163]\n"
"buffer_load_dwordx4 v224, s[28:31], s48 offen lds\n"
"v_mfma_f32_16x16x32_bf16 v[176:179], a[192:195], a[168:171], v[176:179]\n"
"v_xor_b32 v252, s74, v252\n"
"v_xor_b32 v253, s74, v253\n"
"v_mfma_f32_16x16x32_bf16 v[176:179], a[196:199], a[172:175], v[176:179]\n"
"v_xor_b32 v250, s74, v250\n"
"v_xor_b32 v251, s74, v251\n"
"v_mfma_f32_16x16x32_bf16 v[180:183], a[200:203], a[168:171], v[180:183]\n"
"v_mfma_f32_16x16x32_bf16 v[180:183], a[204:207], a[172:175], v[180:183]\n"
"buffer_load_dwordx4 v225, s[28:31], s48 offen offset:1024 lds\n"
"v_mfma_f32_16x16x32_bf16 v[184:187], a[208:211], a[168:171], v[184:187]\n"
"v_mfma_f32_16x16x32_bf16 v[184:187], a[212:215], a[172:175], v[184:187]\n"
"v_mfma_f32_16x16x32_bf16 v[188:191], a[216:219], a[168:171], v[188:191]\n"
"v_mfma_f32_16x16x32_bf16 v[188:191], a[220:223], a[172:175], v[188:191]\n"
"v_mfma_f32_16x16x32_bf16 v[204:207], a[216:219], a[176:179], v[204:207]\n"
"v_mfma_f32_16x16x32_bf16 v[204:207], a[220:223], a[180:183], v[204:207]\n"
"buffer_load_dwordx4 v226, s[28:31], s48 offen offset:2048 lds\n"
"v_mfma_f32_16x16x32_bf16 v[200:203], a[208:211], a[176:179], v[200:203]\n"
"v_mfma_f32_16x16x32_bf16 v[200:203], a[212:215], a[180:183], v[200:203]\n"
"v_mfma_f32_16x16x32_bf16 v[196:199], a[200:203], a[176:179], v[196:199]\n"
"v_mfma_f32_16x16x32_bf16 v[196:199], a[204:207], a[180:183], v[196:199]\n"
"v_mfma_f32_16x16x32_bf16 v[192:195], a[192:195], a[176:179], v[192:195]\n"
"v_mfma_f32_16x16x32_bf16 v[192:195], a[196:199], a[180:183], v[192:195]\n"
"s_waitcnt lgkmcnt(0)\n"
"buffer_load_dwordx4 v227, s[28:31], s48 offen offset:3072 lds\n"
"v_mfma_f32_16x16x32_bf16 v[208:211], a[192:195], a[184:187], v[208:211]\n"
"s_xor_b32 s50, s50, s74\n"
"v_mfma_f32_16x16x32_bf16 v[208:211], a[196:199], a[188:191], v[208:211]\n"
"v_mfma_f32_16x16x32_bf16 v[212:215], a[200:203], a[184:187], v[212:215]\n"
"v_mfma_f32_16x16x32_bf16 v[212:215], a[204:207], a[188:191], v[212:215]\n"
"v_mfma_f32_16x16x32_bf16 v[216:219], a[208:211], a[184:187], v[216:219]\n"
"v_mfma_f32_16x16x32_bf16 v[216:219], a[212:215], a[188:191], v[216:219]\n"
"v_mfma_f32_16x16x32_bf16 v[220:223], a[216:219], a[184:187], v[220:223]\n"
"v_mfma_f32_16x16x32_bf16 v[220:223], a[220:223], a[188:191], v[220:223]\n"
"v_mfma_f32_16x16x32_bf16 v[0:3], a[224:227], a[136:139], v[0:3]\n"
"v_mfma_f32_16x16x32_bf16 v[0:3], a[228:231], a[140:143], v[0:3]\n"
"v_mfma_f32_16x16x32_bf16 v[4:7], a[232:235], a[136:139], v[4:7]\n"
"s_waitcnt vmcnt(8)\n"
"v_mfma_f32_16x16x32_bf16 v[4:7], a[236:239], a[140:143], v[4:7]\n"
"s_barrier\n"
"v_mfma_f32_16x16x32_bf16 v[8:11], a[240:243], a[136:139], v[8:11]\n"
"s_mov_b32 m0, s51\n"
"ds_read_b64_tr_b16 a[192:193], v244 offset:0\n"
"v_mfma_f32_16x16x32_bf16 v[8:11], a[244:247], a[140:143], v[8:11]\n"
"buffer_load_dwordx4 v248, s[24:27], s49 offen lds\n"
"v_mfma_f32_16x16x32_bf16 v[12:15], a[248:251], a[136:139], v[12:15]\n"
"ds_read_b64_tr_b16 a[194:195], v244 offset:512\n"
"v_mfma_f32_16x16x32_bf16 v[12:15], a[252:255], a[140:143], v[12:15]\n"
"ds_read_b128 a[136:139], v246 offset:0\n"
"v_mfma_f32_16x16x32_bf16 v[28:31], a[248:251], a[144:147], v[28:31]\n"
"ds_read_b64_tr_b16 a[196:197], v244 offset:8192\n"
"v_mfma_f32_16x16x32_bf16 v[28:31], a[252:255], a[148:151], v[28:31]\n"
"ds_read_b64_tr_b16 a[198:199], v244 offset:8704\n"
"v_mfma_f32_16x16x32_bf16 v[24:27], a[240:243], a[144:147], v[24:27]\n"
"ds_read_b128 a[140:143], v247 offset:0\n"
"v_mfma_f32_16x16x32_bf16 v[24:27], a[244:247], a[148:151], v[24:27]\n"
"buffer_load_dwordx4 v249, s[24:27], s49 offen offset:1024 lds\n"
"v_mfma_f32_16x16x32_bf16 v[20:23], a[232:235], a[144:147], v[20:23]\n"
"ds_read_b64_tr_b16 a[200:201], v245 offset:0\n"
"v_mfma_f32_16x16x32_bf16 v[20:23], a[236:239], a[148:151], v[20:23]\n"
"ds_read_b64_tr_b16 a[202:203], v245 offset:512\n"
"v_mfma_f32_16x16x32_bf16 v[16:19], a[224:227], a[144:147], v[16:19]\n"
"ds_read_b64_tr_b16 a[204:205], v245 offset:8192\n"
"v_mfma_f32_16x16x32_bf16 v[16:19], a[228:231], a[148:151], v[16:19]\n"
"ds_read_b64_tr_b16 a[206:207], v245 offset:8704\n"
"v_mfma_f32_16x16x32_bf16 v[32:35], a[224:227], a[152:155], v[32:35]\n"
"ds_read_b64_tr_b16 a[208:209], v244 offset:1024\n"
"v_mfma_f32_16x16x32_bf16 v[32:35], a[228:231], a[156:159], v[32:35]\n"
"buffer_load_dwordx4 v254, s[24:27], s49 offen offset:2048 lds\n"
"v_mfma_f32_16x16x32_bf16 v[36:39], a[232:235], a[152:155], v[36:39]\n"
"ds_read_b64_tr_b16 a[210:211], v244 offset:1536\n"
"v_mfma_f32_16x16x32_bf16 v[36:39], a[236:239], a[156:159], v[36:39]\n"
"ds_read_b64_tr_b16 a[212:213], v244 offset:9216\n"
"v_mfma_f32_16x16x32_bf16 v[40:43], a[240:243], a[152:155], v[40:43]\n"
"ds_read_b64_tr_b16 a[214:215], v244 offset:9728\n"
"v_mfma_f32_16x16x32_bf16 v[40:43], a[244:247], a[156:159], v[40:43]\n"
"ds_read_b64_tr_b16 a[216:217], v245 offset:1024\n"
"v_mfma_f32_16x16x32_bf16 v[44:47], a[248:251], a[152:155], v[44:47]\n"
"ds_read_b64_tr_b16 a[218:219], v245 offset:1536\n"
"v_mfma_f32_16x16x32_bf16 v[44:47], a[252:255], a[156:159], v[44:47]\n"
"buffer_load_dwordx4 v255, s[24:27], s49 offen offset:3072 lds\n"
"v_mfma_f32_16x16x32_bf16 v[60:63], a[248:251], a[160:163], v[60:63]\n"
"ds_read_b64_tr_b16 a[220:221], v245 offset:9216\n"
"v_mfma_f32_16x16x32_bf16 v[60:63], a[252:255], a[164:167], v[60:63]\n"
"ds_read_b64_tr_b16 a[222:223], v245 offset:9728\n"
"v_mfma_f32_16x16x32_bf16 v[56:59], a[240:243], a[160:163], v[56:59]\n"
"ds_read_b128 a[144:147], v246 offset:2048\n"
"v_mfma_f32_16x16x32_bf16 v[56:59], a[244:247], a[164:167], v[56:59]\n"
"s_add_u32 s40, s49, 40960\n"
"ds_read_b128 a[148:151], v247 offset:2048\n"
"v_mfma_f32_16x16x32_bf16 v[52:55], a[232:235], a[160:163], v[52:55]\n"
"s_add_u32 m0, s51, 4096\n"
"ds_read_b128 a[152:155], v246 offset:4096\n"
"v_mfma_f32_16x16x32_bf16 v[52:55], a[236:239], a[164:167], v[52:55]\n"
"buffer_load_dwordx4 v248, s[24:27], s40 offen lds\n"
"v_mfma_f32_16x16x32_bf16 v[48:51], a[224:227], a[160:163], v[48:51]\n"
"ds_read_b128 a[156:159], v247 offset:4096\n"
"v_mfma_f32_16x16x32_bf16 v[48:51], a[228:231], a[164:167], v[48:51]\n"
"ds_read_b128 a[160:163], v252 offset:0\n"
"v_mfma_f32_16x16x32_bf16 v[64:67], a[224:227], a[168:171], v[64:67]\n"
"ds_read_b128 a[164:167], v253 offset:0\n"
"v_mfma_f32_16x16x32_bf16 v[64:67], a[228:231], a[172:175], v[64:67]\n"
"v_mfma_f32_16x16x32_bf16 v[68:71], a[232:235], a[168:171], v[68:71]\n"
"v_mfma_f32_16x16x32_bf16 v[68:71], a[236:239], a[172:175], v[68:71]\n"
"buffer_load_dwordx4 v249, s[24:27], s40 offen offset:1024 lds\n"
"v_mfma_f32_16x16x32_bf16 v[72:75], a[240:243], a[168:171], v[72:75]\n"
"v_mfma_f32_16x16x32_bf16 v[72:75], a[244:247], a[172:175], v[72:75]\n"
"v_mfma_f32_16x16x32_bf16 v[76:79], a[248:251], a[168:171], v[76:79]\n"
"ds_read_b128 a[168:171], v250 offset:0\n"
"v_mfma_f32_16x16x32_bf16 v[76:79], a[252:255], a[172:175], v[76:79]\n"
"ds_read_b128 a[172:175], v251 offset:0\n"
"v_mfma_f32_16x16x32_bf16 v[92:95], a[248:251], a[176:179], v[92:95]\n"
"v_mfma_f32_16x16x32_bf16 v[92:95], a[252:255], a[180:183], v[92:95]\n"
"buffer_load_dwordx4 v254, s[24:27], s40 offen offset:2048 lds\n"
"v_mfma_f32_16x16x32_bf16 v[88:91], a[240:243], a[176:179], v[88:91]\n"
"s_add_u32 s49, s49, 128\n"
"v_mfma_f32_16x16x32_bf16 v[88:91], a[244:247], a[180:183], v[88:91]\n"
"s_sub_u32 s41, s49, 1280\n"
"v_mfma_f32_16x16x32_bf16 v[84:87], a[232:235], a[176:179], v[84:87]\n"
"s_min_u32 s49, s49, s41\n"
"v_mfma_f32_16x16x32_bf16 v[84:87], a[236:239], a[180:183], v[84:87]\n"
"s_xor_b32 s51, s51, s74\n"
"v_mfma_f32_16x16x32_bf16 v[80:83], a[224:227], a[176:179], v[80:83]\n"
"ds_read_b128 a[176:179], v250 offset:2048\n"
"v_mfma_f32_16x16x32_bf16 v[80:83], a[228:231], a[180:183], v[80:83]\n"
"ds_read_b128 a[180:183], v251 offset:2048\n"
"v_mfma_f32_16x16x32_bf16 v[96:99], a[224:227], a[184:187], v[96:99]\n"
"v_mfma_f32_16x16x32_bf16 v[96:99], a[228:231], a[188:191], v[96:99]\n"
"v_mfma_f32_16x16x32_bf16 v[100:103], a[232:235], a[184:187], v[100:103]\n"
"v_mfma_f32_16x16x32_bf16 v[100:103], a[236:239], a[188:191], v[100:103]\n"
"v_mfma_f32_16x16x32_bf16 v[104:107], a[240:243], a[184:187], v[104:107]\n"
"v_mfma_f32_16x16x32_bf16 v[104:107], a[244:247], a[188:191], v[104:107]\n"
"v_mfma_f32_16x16x32_bf16 v[108:111], a[248:251], a[184:187], v[108:111]\n"
"s_add_u32 s47, s47, 1\n"
"ds_read_b128 a[184:187], v250 offset:4096\n"
"v_mfma_f32_16x16x32_bf16 v[108:111], a[252:255], a[188:191], v[108:111]\n"
"s_waitcnt lgkmcnt(1)\n"
"s_cmp_lg_u32 s47, s62\n"
"ds_read_b128 a[188:191], v251 offset:4096\n"
"s_cbranch_scc1 LPL_%=\n"
"LPX_%=:\n"
"s_cmp_eq_u32 s47, 18\n"
"s_cbranch_scc1 LPT2_%=\n"
"v_mfma_f32_16x16x32_bf16 v[112:115], a[192:195], a[136:139], v[112:115]\n"
"s_add_u32 s78, s96, 8192\n"
"v_mov_b32 v241, 1\n"
"v_mfma_f32_16x16x32_bf16 v[112:115], a[196:199], a[140:143], v[112:115]\n"
"s_mov_b64 exec, 1\n"
"buffer_store_dword v241, off, s[32:35], s96 sc0 sc1\n"
"buffer_store_dword v241, off, s[32:35], s78 sc0 sc1\n"
"s_mov_b64 exec, -1\n"
"v_mfma_f32_16x16x32_bf16 v[116:119], a[200:203], a[136:139], v[116:119]\n"
"s_waitcnt vmcnt(13)\n"
"v_mfma_f32_16x16x32_bf16 v[116:119], a[204:207], a[140:143], v[116:119]\n"
"s_barrier\n"
"v_mfma_f32_16x16x32_bf16 v[120:123], a[208:211], a[136:139], v[120:123]\n"
"s_add_u32 s40, s48, 256\n"
"s_xor_b32 m0, s50, 0xc000\n"
"ds_read_b64_tr_b16 a[238:239], v245 offset:25088\n"
"v_mfma_f32_16x16x32_bf16 v[120:123], a[212:215], a[140:143], v[120:123]\n"
"buffer_load_dwordx4 v224, s[28:31], s40 offen lds\n"
"v_mfma_f32_16x16x32_bf16 v[124:127], a[216:219], a[136:139], v[124:127]\n"
"v_mfma_f32_16x16x32_bf16 v[124:127], a[220:223], a[140:143], v[124:127]\n"
"v_mfma_f32_16x16x32_bf16 v[188:191], a[216:219], a[168:171], v[188:191]\n"
"ds_read_b64_tr_b16 a[254:255], v245 offset:26112\n"
"v_mfma_f32_16x16x32_bf16 v[188:191], a[220:223], a[172:175], v[188:191]\n"
"ds_read_b64_tr_b16 a[252:253], v245 offset:25600\n"
"v_mfma_f32_16x16x32_bf16 v[184:187], a[208:211], a[168:171], v[184:187]\n"
"ds_read_b64_tr_b16 a[224:225], v244 offset:16384\n"
"ds_read_b64_tr_b16 a[228:229], v244 offset:24576\n"
"v_mfma_f32_16x16x32_bf16 v[184:187], a[212:215], a[172:175], v[184:187]\n"
"buffer_load_dwordx4 v225, s[28:31], s40 offen offset:1024 lds\n"
"v_mfma_f32_16x16x32_bf16 v[180:183], a[200:203], a[168:171], v[180:183]\n"
"v_mfma_f32_16x16x32_bf16 v[180:183], a[204:207], a[172:175], v[180:183]\n"
"v_mfma_f32_16x16x32_bf16 v[176:179], a[192:195], a[168:171], v[176:179]\n"
"ds_read_b64_tr_b16 a[230:231], v244 offset:25088\n"
"v_mfma_f32_16x16x32_bf16 v[176:179], a[196:199], a[172:175], v[176:179]\n"
"ds_read_b64_tr_b16 a[244:245], v244 offset:25600\n"
"v_mfma_f32_16x16x32_bf16 v[192:195], a[192:195], a[176:179], v[192:195]\n"
"ds_read_b64_tr_b16 a[226:227], v244 offset:16896\n"
"ds_read_b64_tr_b16 a[250:251], v245 offset:17920\n"
"v_mfma_f32_16x16x32_bf16 v[192:195], a[196:199], a[180:183], v[192:195]\n"
"buffer_load_dwordx4 v226, s[28:31], s40 offen offset:2048 lds\n"
"v_mfma_f32_16x16x32_bf16 v[196:199], a[200:203], a[176:179], v[196:199]\n"
"v_mfma_f32_16x16x32_bf16 v[196:199], a[204:207], a[180:183], v[196:199]\n"
"v_mfma_f32_16x16x32_bf16 v[200:203], a[208:211], a[176:179], v[200:203]\n"
"ds_read_b64_tr_b16 a[236:237], v245 offset:24576\n"
"ds_read_b64_tr_b16 a[246:247], v244 offset:26112\n"
"v_mfma_f32_16x16x32_bf16 v[200:203], a[212:215], a[180:183], v[200:203]\n"
"ds_read_b64_tr_b16 a[240:241], v244 offset:17408\n"
"v_mfma_f32_16x16x32_bf16 v[204:207], a[216:219], a[176:179], v[204:207]\n"
"ds_read_b64_tr_b16 a[242:243], v244 offset:17920\n"
"ds_read_b64_tr_b16 a[248:249], v245 offset:17408\n"
"v_mfma_f32_16x16x32_bf16 v[204:207], a[220:223], a[180:183], v[204:207]\n"
"s_waitcnt lgkmcnt(7)\n"
"buffer_load_dwordx4 v227, s[28:31], s40 offen offset:3072 lds\n"
"v_mfma_f32_16x16x32_bf16 v[220:223], a[216:219], a[184:187], v[220:223]\n"
"s_cmp_eq_u32 s47, 8\n"
"v_mfma_f32_16x16x32_bf16 v[220:223], a[220:223], a[188:191], v[220:223]\n"
"s_cselect_b32 s41, s52, s58\n"
"s_nop 1\n"
"v_pk_fma_f32 v[176:177], v[176:177], s[70:71], s[70:71] op_sel:[0,0,1] op_sel_hi:[1,0,1]\nv_pk_fma_f32 v[178:179], v[178:179], s[70:71], s[70:71] op_sel:[0,0,1] op_sel_hi:[1,0,1]\nv_pk_fma_f32 v[180:181], v[180:181], s[70:71], s[70:71] op_sel:[0,0,1] op_sel_hi:[1,0,1]\nv_pk_fma_f32 v[182:183], v[182:183], s[70:71], s[70:71] op_sel:[0,0,1] op_sel_hi:[1,0,1]\nv_pk_fma_f32 v[184:185], v[184:185], s[70:71], s[70:71] op_sel:[0,0,1] op_sel_hi:[1,0,1]\nv_pk_fma_f32 v[186:187], v[186:187], s[70:71], s[70:71] op_sel:[0,0,1] op_sel_hi:[1,0,1]\nv_pk_fma_f32 v[188:189], v[188:189], s[70:71], s[70:71] op_sel:[0,0,1] op_sel_hi:[1,0,1]\nv_pk_fma_f32 v[190:191], v[190:191], s[70:71], s[70:71] op_sel:[0,0,1] op_sel_hi:[1,0,1]\nv_pk_fma_f32 v[192:193], v[192:193], s[70:71], s[70:71] op_sel:[0,0,1] op_sel_hi:[1,0,1]\nv_pk_fma_f32 v[194:195], v[194:195], s[70:71], s[70:71] op_sel:[0,0,1] op_sel_hi:[1,0,1]\nv_pk_fma_f32 v[196:197], v[196:197], s[70:71], s[70:71] op_sel:[0,0,1] op_sel_hi:[1,0,1]\nv_pk_fma_f32 v[198:199], v[198:199], s[70:71], s[70:71] op_sel:[0,0,1] op_sel_hi:[1,0,1]\nv_pk_fma_f32 v[200:201], v[200:201], s[70:71], s[70:71] op_sel:[0,0,1] op_sel_hi:[1,0,1]\nv_pk_fma_f32 v[202:203], v[202:203], s[70:71], s[70:71] op_sel:[0,0,1] op_sel_hi:[1,0,1]\nv_pk_fma_f32 v[204:205], v[204:205], s[70:71], s[70:71] op_sel:[0,0,1] op_sel_hi:[1,0,1]\nv_pk_fma_f32 v[206:207], v[206:207], s[70:71], s[70:71] op_sel:[0,0,1] op_sel_hi:[1,0,1]\n"
"v_mfma_f32_16x16x32_bf16 v[216:219], a[208:211], a[184:187], v[216:219]\n"
"s_add_u32 s48, s48, s41\n"
"v_cvt_pk_bf16_f32 v192, v192, v193\n"
"v_cvt_pk_bf16_f32 v193, v194, v195\n"
"v_mfma_f32_16x16x32_bf16 v[216:219], a[212:215], a[188:191], v[216:219]\n"
"s_sub_u32 s41, s48, 5242880\n"
"v_cvt_pk_bf16_f32 v176, v176, v177\n"
"v_cvt_pk_bf16_f32 v177, v178, v179\n"
"v_mfma_f32_16x16x32_bf16 v[212:215], a[200:203], a[184:187], v[212:215]\n"
"s_min_u32 s48, s48, s41\n"
"v_cvt_pk_bf16_f32 v178, v180, v181\n"
"v_cvt_pk_bf16_f32 v179, v182, v183\n"
"v_mfma_f32_16x16x32_bf16 v[212:215], a[204:207], a[188:191], v[212:215]\n"
"ds_read_b64_tr_b16 a[234:235], v245 offset:16896\n"
"v_xor_b32 v246, s74, v246\n"
"v_mfma_f32_16x16x32_bf16 v[208:211], a[192:195], a[184:187], v[208:211]\n"
"s_mov_b32 m0, s50\n"
"v_and_b32 v182, 0xf0, v242\n"
"v_cvt_pk_bf16_f32 v200, v200, v201\n"
"v_mfma_f32_16x16x32_bf16 v[208:211], a[196:199], a[188:191], v[208:211]\n"
"buffer_load_dwordx4 v224, s[28:31], s48 offen lds\n"
"v_mfma_f32_16x16x32_bf16 v[160:163], a[192:195], a[160:163], v[160:163]\n"
"v_bfe_u32 v180, v242, 9, 1\n"
"v_lshl_add_u32 v180, v180, 8, v182\n"
"v_mfma_f32_16x16x32_bf16 v[160:163], a[196:199], a[164:167], v[160:163]\n"
"v_bfe_u32 v182, v242, 8, 1\n"
"v_cvt_pk_bf16_f32 v184, v184, v185\n"
"v_mfma_f32_16x16x32_bf16 v[164:167], a[200:203], a[160:163], v[164:167]\n"
"ds_read_b64_tr_b16 a[232:233], v245 offset:16384\n"
"v_xor_b32 v247, s74, v247\n"
"v_mfma_f32_16x16x32_bf16 v[164:167], a[204:207], a[164:167], v[164:167]\n"
"buffer_load_dwordx4 v225, s[28:31], s48 offen offset:1024 lds\n"
"v_mfma_f32_16x16x32_bf16 v[168:171], a[208:211], a[160:163], v[168:171]\n"
"v_cvt_pk_bf16_f32 v201, v202, v203\n"
"v_xor_b32 v253, s74, v253\n"
"v_mfma_f32_16x16x32_bf16 v[168:171], a[212:215], a[164:167], v[168:171]\n"
"v_cvt_pk_bf16_f32 v194, v196, v197\n"
"v_xor_b32 v244, s74, v244\n"
"v_mfma_f32_16x16x32_bf16 v[172:175], a[216:219], a[160:163], v[172:175]\n"
"v_lshl_add_u32 v180, v182, 3, v180\n"
"v_cvt_pk_bf16_f32 v185, v186, v187\n"
"v_mfma_f32_16x16x32_bf16 v[172:175], a[220:223], a[164:167], v[172:175]\n"
"v_cvt_pk_bf16_f32 v186, v188, v189\n"
"v_cvt_pk_bf16_f32 v187, v190, v191\n"
"v_mfma_f32_16x16x32_bf16 v[140:143], a[216:219], a[144:147], v[140:143]\n"
"v_cvt_pk_bf16_f32 v203, v206, v207\n"
"v_bfe_u32 v181, v242, 9, 1\n"
"v_mfma_f32_16x16x32_bf16 v[140:143], a[220:223], a[148:151], v[140:143]\n"
"buffer_load_dwordx4 v226, s[28:31], s48 offen offset:2048 lds\n"
"v_mfma_f32_16x16x32_bf16 v[136:139], a[208:211], a[144:147], v[136:139]\n"
"v_xor_b32_e64 v182, s42, 1\n"
"v_lshl_add_u32 v180, v182, 10, v180\n"
"v_mfma_f32_16x16x32_bf16 v[136:139], a[212:215], a[148:151], v[136:139]\n"
"v_cvt_pk_bf16_f32 v202, v204, v205\n"
"v_xor_b32 v252, s74, v252\n"
"v_mfma_f32_16x16x32_bf16 v[132:135], a[200:203], a[144:147], v[132:135]\n"
"v_add_u32 v180, 94208, v180\n"
"v_and_b32 v182, 0xf0, v242\n"
"v_mfma_f32_16x16x32_bf16 v[132:135], a[204:207], a[148:151], v[132:135]\n"
"ds_write2st64_b64 v180, v[184:185], v[186:187] offset0:64 offset1:65\n"
"v_pk_fma_f32 v[208:209], v[208:209], s[70:71], s[70:71] op_sel:[0,0,1] op_sel_hi:[1,0,1]\nv_pk_fma_f32 v[210:211], v[210:211], s[70:71], s[70:71] op_sel:[0,0,1] op_sel_hi:[1,0,1]\nv_pk_fma_f32 v[212:213], v[212:213], s[70:71], s[70:71] op_sel:[0,0,1] op_sel_hi:[1,0,1]\nv_pk_fma_f32 v[214:215], v[214:215], s[70:71], s[70:71] op_sel:[0,0,1] op_sel_hi:[1,0,1]\nv_pk_fma_f32 v[216:217], v[216:217], s[70:71], s[70:71] op_sel:[0,0,1] op_sel_hi:[1,0,1]\nv_pk_fma_f32 v[218:219], v[218:219], s[70:71], s[70:71] op_sel:[0,0,1] op_sel_hi:[1,0,1]\nv_pk_fma_f32 v[220:221], v[220:221], s[70:71], s[70:71] op_sel:[0,0,1] op_sel_hi:[1,0,1]\nv_pk_fma_f32 v[222:223], v[222:223], s[70:71], s[70:71] op_sel:[0,0,1] op_sel_hi:[1,0,1]\nv_pk_fma_f32 v[160:161], v[160:161], s[70:71], s[70:71] op_sel:[0,0,1] op_sel_hi:[1,0,1]\nv_pk_fma_f32 v[162:163], v[162:163], s[70:71], s[70:71] op_sel:[0,0,1] op_sel_hi:[1,0,1]\nv_pk_fma_f32 v[164:165], v[164:165], s[70:71], s[70:71] op_sel:[0,0,1] op_sel_hi:[1,0,1]\nv_pk_fma_f32 v[166:167], v[166:167], s[70:71], s[70:71] op_sel:[0,0,1] op_sel_hi:[1,0,1]\nv_pk_fma_f32 v[168:169], v[168:169], s[70:71], s[70:71] op_sel:[0,0,1] op_sel_hi:[1,0,1]\nv_pk_fma_f32 v[170:171], v[170:171], s[70:71], s[70:71] op_sel:[0,0,1] op_sel_hi:[1,0,1]\nv_pk_fma_f32 v[172:173], v[172:173], s[70:71], s[70:71] op_sel:[0,0,1] op_sel_hi:[1,0,1]\nv_pk_fma_f32 v[174:175], v[174:175], s[70:71], s[70:71] op_sel:[0,0,1] op_sel_hi:[1,0,1]\n"
"v_cvt_pk_bf16_f32 v216, v216, v217\n"
"v_mfma_f32_16x16x32_bf16 v[128:131], a[192:195], a[144:147], v[128:131]\n"
"v_cvt_pk_bf16_f32 v168, v168, v169\n"
"v_xor_b32 v250, s74, v250\n"
"v_mfma_f32_16x16x32_bf16 v[128:131], a[196:199], a[148:151], v[128:131]\n"
"buffer_load_dwordx4 v227, s[28:31], s48 offen offset:3072 lds\n"
"v_mfma_f32_16x16x32_bf16 v[144:147], a[192:195], a[152:155], v[144:147]\n"
"s_xor_b32 s50, s50, s74\n"
"v_cvt_pk_bf16_f32 v208, v208, v209\n"
"v_xor_b32 v251, s74, v251\n"
"v_mfma_f32_16x16x32_bf16 v[144:147], a[196:199], a[156:159], v[144:147]\n"
"v_cvt_pk_bf16_f32 v217, v218, v219\n"
"v_cvt_pk_bf16_f32 v169, v170, v171\n"
"v_mfma_f32_16x16x32_bf16 v[148:151], a[200:203], a[152:155], v[148:151]\n"
"v_cvt_pk_bf16_f32 v195, v198, v199\n"
"v_cvt_pk_bf16_f32 v209, v210, v211\n"
"v_mfma_f32_16x16x32_bf16 v[148:151], a[204:207], a[156:159], v[148:151]\n"
"ds_write2st64_b64 v180, v[192:193], v[194:195] offset0:88 offset1:89\n"
"v_pk_fma_f32 v[112:113], v[112:113], s[70:71], s[70:71] op_sel:[0,0,1] op_sel_hi:[1,0,1]\nv_pk_fma_f32 v[114:115], v[114:115], s[70:71], s[70:71] op_sel:[0,0,1] op_sel_hi:[1,0,1]\nv_pk_fma_f32 v[116:117], v[116:117], s[70:71], s[70:71] op_sel:[0,0,1] op_sel_hi:[1,0,1]\nv_pk_fma_f32 v[118:119], v[118:119], s[70:71], s[70:71] op_sel:[0,0,1] op_sel_hi:[1,0,1]\nv_pk_fma_f32 v[120:121], v[120:121], s[70:71], s[70:71] op_sel:[0,0,1] op_sel_hi:[1,0,1]\nv_pk_fma_f32 v[122:123], v[122:123], s[70:71], s[70:71] op_sel:[0,0,1] op_sel_hi:[1,0,1]\nv_pk_fma_f32 v[124:125], v[124:125], s[70:71], s[70:71] op_sel:[0,0,1] op_sel_hi:[1,0,1]\nv_pk_fma_f32 v[126:127], v[126:127], s[70:71], s[70:71] op_sel:[0,0,1] op_sel_hi:[1,0,1]\nv_pk_fma_f32 v[128:129], v[128:129], s[70:71], s[70:71] op_sel:[0,0,1] op_sel_hi:[1,0,1]\nv_pk_fma_f32 v[130:131], v[130:131], s[70:71], s[70:71] op_sel:[0,0,1] op_sel_hi:[1,0,1]\nv_pk_fma_f32 v[132:133], v[132:133], s[70:71], s[70:71] op_sel:[0,0,1] op_sel_hi:[1,0,1]\nv_pk_fma_f32 v[134:135], v[134:135], s[70:71], s[70:71] op_sel:[0,0,1] op_sel_hi:[1,0,1]\nv_pk_fma_f32 v[136:137], v[136:137], s[70:71], s[70:71] op_sel:[0,0,1] op_sel_hi:[1,0,1]\nv_pk_fma_f32 v[138:139], v[138:139], s[70:71], s[70:71] op_sel:[0,0,1] op_sel_hi:[1,0,1]\nv_pk_fma_f32 v[140:141], v[140:141], s[70:71], s[70:71] op_sel:[0,0,1] op_sel_hi:[1,0,1]\nv_pk_fma_f32 v[142:143], v[142:143], s[70:71], s[70:71] op_sel:[0,0,1] op_sel_hi:[1,0,1]\n"
"v_mfma_f32_16x16x32_bf16 v[152:155], a[208:211], a[152:155], v[152:155]\n"
"v_cvt_pk_bf16_f32 v218, v220, v221\n"
"v_cvt_pk_bf16_f32 v136, v136, v137\n"
"v_mfma_f32_16x16x32_bf16 v[152:155], a[212:215], a[156:159], v[152:155]\n"
"v_cvt_pk_bf16_f32 v137, v138, v139\n"
"v_cvt_pk_bf16_f32 v139, v142, v143\n"
"v_mfma_f32_16x16x32_bf16 v[156:159], a[216:219], a[152:155], v[156:159]\n"
"v_cvt_pk_bf16_f32 v128, v128, v129\n"
"v_cvt_pk_bf16_f32 v138, v140, v141\n"
"v_mfma_f32_16x16x32_bf16 v[156:159], a[220:223], a[156:159], v[156:159]\n"
"s_waitcnt lgkmcnt(1)\n"
"v_cvt_pk_bf16_f32 v112, v112, v113\n"
"v_xor_b32 v245, s74, v245\n"
"v_mfma_f32_16x16x32_bf16 v[96:99], a[224:227], a[184:187], v[96:99]\n"
"v_cvt_pk_bf16_f32 v120, v120, v121\n"
"v_cvt_pk_bf16_f32 v129, v130, v131\n"
"v_mfma_f32_16x16x32_bf16 v[96:99], a[228:231], a[188:191], v[96:99]\n"
"ds_write2st64_b64 v180, v[200:201], v[202:203] offset0:96 offset1:97\n"
"v_cvt_pk_bf16_f32 v219, v222, v223\n"
"v_cvt_pk_bf16_f32 v170, v172, v173\n"
"v_cvt_pk_bf16_f32 v131, v134, v135\n"
"v_lshl_add_u32 v181, v181, 8, v182\n"
"v_bfe_u32 v182, v242, 8, 1\n"
"v_lshl_add_u32 v181, v182, 9, v181\n"
"v_mfma_f32_16x16x32_bf16 v[100:103], a[232:235], a[184:187], v[100:103]\n"
"s_waitcnt vmcnt(10)\n"
"v_permlane16_swap_b32 v136, v138\n"
"v_mfma_f32_16x16x32_bf16 v[100:103], a[236:239], a[188:191], v[100:103]\n"
"s_barrier\n"
"ds_write2st64_b64 v180, v[216:217], v[218:219] offset0:128 offset1:129\n"
"v_pk_fma_f32 v[144:145], v[144:145], s[70:71], s[70:71] op_sel:[0,0,1] op_sel_hi:[1,0,1]\nv_pk_fma_f32 v[146:147], v[146:147], s[70:71], s[70:71] op_sel:[0,0,1] op_sel_hi:[1,0,1]\nv_pk_fma_f32 v[148:149], v[148:149], s[70:71], s[70:71] op_sel:[0,0,1] op_sel_hi:[1,0,1]\nv_pk_fma_f32 v[150:151], v[150:151], s[70:71], s[70:71] op_sel:[0,0,1] op_sel_hi:[1,0,1]\nv_pk_fma_f32 v[152:153], v[152:153], s[70:71], s[70:71] op_sel:[0,0,1] op_sel_hi:[1,0,1]\nv_pk_fma_f32 v[154:155], v[154:155], s[70:71], s[70:71] op_sel:[0,0,1] op_sel_hi:[1,0,1]\nv_pk_fma_f32 v[156:157], v[156:157], s[70:71], s[70:71] op_sel:[0,0,1] op_sel_hi:[1,0,1]\nv_pk_fma_f32 v[158:159], v[158:159], s[70:71], s[70:71] op_sel:[0,0,1] op_sel_hi:[1,0,1]\n"
"v_cvt_pk_bf16_f32 v144, v144, v145\n"
"v_mfma_f32_16x16x32_bf16 v[104:107], a[240:243], a[184:187], v[104:107]\n"
"s_mov_b32 m0, s51\n"
"v_cvt_pk_bf16_f32 v171, v174, v175\n"
"v_cvt_pk_bf16_f32 v121, v122, v123\n"
"v_mfma_f32_16x16x32_bf16 v[104:107], a[244:247], a[188:191], v[104:107]\n"
"buffer_load_dwordx4 v248, s[24:27], s49 offen lds\n"
"v_mfma_f32_16x16x32_bf16 v[108:111], a[248:251], a[184:187], v[108:111]\n"
"v_xor_b32_e64 v182, s42, 0\n"
"v_lshl_add_u32 v181, v182, 10, v181\n"
"v_mfma_f32_16x16x32_bf16 v[108:111], a[252:255], a[188:191], v[108:111]\n"
"ds_read_b64_tr_b16 a[218:219], v245 offset:1536\n"
"v_cvt_pk_bf16_f32 v211, v214, v215\n"
"v_mfma_f32_16x16x32_bf16 v[92:95], a[248:251], a[176:179], v[92:95]\n"
"v_cvt_pk_bf16_f32 v160, v160, v161\n"
"v_cvt_pk_bf16_f32 v145, v146, v147\n"
"v_mfma_f32_16x16x32_bf16 v[92:95], a[252:255], a[180:183], v[92:95]\n"
"ds_write2st64_b64 v180, v[176:177], v[178:179] offset0:0 offset1:1\n"
"v_cvt_pk_bf16_f32 v122, v124, v125\n"
"v_cvt_pk_bf16_f32 v123, v126, v127\n"
"v_cvt_pk_bf16_f32 v146, v148, v149\n"
"v_cvt_pk_bf16_f32 v152, v152, v153\n"
"v_cvt_pk_bf16_f32 v153, v154, v155\n"
"v_cvt_pk_bf16_f32 v155, v158, v159\n"
"v_mfma_f32_16x16x32_bf16 v[88:91], a[240:243], a[176:179], v[88:91]\n"
"v_cvt_pk_bf16_f32 v210, v212, v213\n"
"v_cvt_pk_bf16_f32 v113, v114, v115\n"
"v_mfma_f32_16x16x32_bf16 v[88:91], a[244:247], a[180:183], v[88:91]\n"
"buffer_load_dwordx4 v249, s[24:27], s49 offen offset:1024 lds\n"
"v_mfma_f32_16x16x32_bf16 v[84:87], a[232:235], a[176:179], v[84:87]\n"
"v_cvt_pk_bf16_f32 v161, v162, v163\n"
"v_cvt_pk_bf16_f32 v154, v156, v157\n"
"v_mfma_f32_16x16x32_bf16 v[84:87], a[236:239], a[180:183], v[84:87]\n"
"s_nop 0\n"
"v_permlane16_swap_b32 v152, v154\n"
"v_mfma_f32_16x16x32_bf16 v[80:83], a[224:227], a[176:179], v[80:83]\n"
"ds_read_b64_tr_b16 a[196:197], v244 offset:8192\n"
"ds_read_b64_tr_b16 a[214:215], v244 offset:9728\n"
"v_mfma_f32_16x16x32_bf16 v[80:83], a[228:231], a[180:183], v[80:83]\n"
"ds_write2st64_b64 v180, v[208:209], v[210:211] offset0:120 offset1:121\n"
"v_cvt_pk_bf16_f32 v114, v116, v117\n"
"v_permlane16_swap_b32 v137, v139\n"
"v_permlane16_swap_b32 v144, v146\n"
"v_mfma_f32_16x16x32_bf16 v[64:67], a[224:227], a[168:171], v[64:67]\n"
"v_permlane16_swap_b32 v121, v123\n"
"v_mfma_f32_16x16x32_bf16 v[64:67], a[228:231], a[172:175], v[64:67]\n"
"buffer_load_dwordx4 v254, s[24:27], s49 offen offset:2048 lds\n"
"v_mfma_f32_16x16x32_bf16 v[68:71], a[232:235], a[168:171], v[68:71]\n"
"v_permlane16_swap_b32 v120, v122\n"
"v_mfma_f32_16x16x32_bf16 v[68:71], a[236:239], a[172:175], v[68:71]\n"
"v_cvt_pk_bf16_f32 v115, v118, v119\n"
"v_accvgpr_write_b32 a24, v136\n"
"v_mfma_f32_16x16x32_bf16 v[72:75], a[240:243], a[168:171], v[72:75]\n"
"ds_read_b64_tr_b16 a[206:207], v245 offset:8704\n"
"v_add_u32 v181, 94208, v181\n"
"v_mfma_f32_16x16x32_bf16 v[72:75], a[244:247], a[172:175], v[72:75]\n"
"ds_read_b64_tr_b16 a[216:217], v245 offset:1024\n"
"v_permlane16_swap_b32 v112, v114\n"
"v_permlane16_swap_b32 v153, v155\n"
"v_pk_fma_f32 v[96:97], v[96:97], s[70:71], s[70:71] op_sel:[0,0,1] op_sel_hi:[1,0,1]\nv_pk_fma_f32 v[98:99], v[98:99], s[70:71], s[70:71] op_sel:[0,0,1] op_sel_hi:[1,0,1]\nv_pk_fma_f32 v[100:101], v[100:101], s[70:71], s[70:71] op_sel:[0,0,1] op_sel_hi:[1,0,1]\nv_pk_fma_f32 v[102:103], v[102:103], s[70:71], s[70:71] op_sel:[0,0,1] op_sel_hi:[1,0,1]\nv_pk_fma_f32 v[104:105], v[104:105], s[70:71], s[70:71] op_sel:[0,0,1] op_sel_hi:[1,0,1]\nv_pk_fma_f32 v[106:107], v[106:107], s[70:71], s[70:71] op_sel:[0,0,1] op_sel_hi:[1,0,1]\nv_pk_fma_f32 v[108:109], v[108:109], s[70:71], s[70:71] op_sel:[0,0,1] op_sel_hi:[1,0,1]\nv_pk_fma_f32 v[110:111], v[110:111], s[70:71], s[70:71] op_sel:[0,0,1] op_sel_hi:[1,0,1]\nv_pk_fma_f32 v[80:81], v[80:81], s[70:71], s[70:71] op_sel:[0,0,1] op_sel_hi:[1,0,1]\nv_pk_fma_f32 v[82:83], v[82:83], s[70:71], s[70:71] op_sel:[0,0,1] op_sel_hi:[1,0,1]\nv_pk_fma_f32 v[84:85], v[84:85], s[70:71], s[70:71] op_sel:[0,0,1] op_sel_hi:[1,0,1]\nv_pk_fma_f32 v[86:87], v[86:87], s[70:71], s[70:71] op_sel:[0,0,1] op_sel_hi:[1,0,1]\nv_pk_fma_f32 v[88:89], v[88:89], s[70:71], s[70:71] op_sel:[0,0,1] op_sel_hi:[1,0,1]\nv_pk_fma_f32 v[90:91], v[90:91], s[70:71], s[70:71] op_sel:[0,0,1] op_sel_hi:[1,0,1]\nv_pk_fma_f32 v[92:93], v[92:93], s[70:71], s[70:71] op_sel:[0,0,1] op_sel_hi:[1,0,1]\nv_pk_fma_f32 v[94:95], v[94:95], s[70:71], s[70:71] op_sel:[0,0,1] op_sel_hi:[1,0,1]\n"
"v_mfma_f32_16x16x32_bf16 v[76:79], a[248:251], a[168:171], v[76:79]\n"
"ds_read_b64_tr_b16 a[208:209], v244 offset:1024\n"
"v_cvt_pk_bf16_f32 v80, v80, v81\n"
"v_mfma_f32_16x16x32_bf16 v[76:79], a[252:255], a[172:175], v[76:79]\n"
"buffer_load_dwordx4 v255, s[24:27], s49 offen offset:3072 lds\n"
"v_mfma_f32_16x16x32_bf16 v[60:63], a[248:251], a[160:163], v[60:63]\n"
"ds_read_b64_tr_b16 a[192:193], v244 offset:0\n"
"ds_read_b64_tr_b16 a[222:223], v245 offset:9728\n"
"v_mfma_f32_16x16x32_bf16 v[60:63], a[252:255], a[164:167], v[60:63]\n"
"v_permlane16_swap_b32 v168, v170\n"
"v_mfma_f32_16x16x32_bf16 v[56:59], a[240:243], a[160:163], v[56:59]\n"
"v_permlane16_swap_b32 v113, v115\n"
"v_mfma_f32_16x16x32_bf16 v[56:59], a[244:247], a[164:167], v[56:59]\n"
"s_add_u32 s40, s49, 40960\n"
"ds_read_b64_tr_b16 a[210:211], v244 offset:1536\n"
"v_add_u32 v182, s75, v181\n"
"v_mfma_f32_16x16x32_bf16 v[52:55], a[232:235], a[160:163], v[52:55]\n"
"s_add_u32 m0, s51, 4096\n"
"v_cvt_pk_bf16_f32 v130, v132, v133\n"
"v_accvgpr_write_b32 a25, v137\n"
"v_mfma_f32_16x16x32_bf16 v[52:55], a[236:239], a[164:167], v[52:55]\n"
"buffer_load_dwordx4 v248, s[24:27], s40 offen lds\n"
"v_mfma_f32_16x16x32_bf16 v[48:51], a[224:227], a[160:163], v[48:51]\n"
"s_waitcnt vmcnt(63) lgkmcnt(3)\n"
"v_permlane16_swap_b32 v129, v131\n"
"v_mfma_f32_16x16x32_bf16 v[48:51], a[228:231], a[164:167], v[48:51]\n"
"s_barrier\n"
"v_mfma_f32_16x16x32_bf16 v[0:3], a[224:227], a[136:139], v[0:3]\n"
"ds_read_b128 a[36:39], v181 offset:61440\n"
"v_accvgpr_write_b32 a43, v155\n"
"v_mfma_f32_16x16x32_bf16 v[0:3], a[228:231], a[140:143], v[0:3]\n"
"ds_read_b128 a[28:31], v181 offset:49152\n"
"v_accvgpr_write_b32 a10, v122\n"
"v_mfma_f32_16x16x32_bf16 v[4:7], a[232:235], a[136:139], v[4:7]\n"
"v_cvt_pk_bf16_f32 v96, v96, v97\n"
"v_cvt_pk_bf16_f32 v104, v104, v105\n"
"v_mfma_f32_16x16x32_bf16 v[4:7], a[236:239], a[140:143], v[4:7]\n"
"buffer_load_dwordx4 v249, s[24:27], s40 offen offset:1024 lds\n"
"v_mfma_f32_16x16x32_bf16 v[8:11], a[240:243], a[136:139], v[8:11]\n"
"v_cvt_pk_bf16_f32 v97, v98, v99\n"
"v_cvt_pk_bf16_f32 v99, v102, v103\n"
"v_mfma_f32_16x16x32_bf16 v[8:11], a[244:247], a[140:143], v[8:11]\n"
"ds_read_b64_tr_b16 a[220:221], v245 offset:9216\n"
"ds_read_b128 a[12:15], v181 offset:32768\n"
"v_mfma_f32_16x16x32_bf16 v[12:15], a[248:251], a[136:139], v[12:15]\n"
"ds_read_b128 a[164:167], v253 offset:0\n"
"v_accvgpr_write_b32 a32, v144\n"
"v_mfma_f32_16x16x32_bf16 v[12:15], a[252:255], a[140:143], v[12:15]\n"
"ds_read_b128 a[140:143], v247 offset:0\n"
"v_accvgpr_write_b32 a3, v115\n"
"v_mfma_f32_16x16x32_bf16 v[28:31], a[248:251], a[144:147], v[28:31]\n"
"ds_read_b64_tr_b16 a[194:195], v244 offset:512\n"
"v_cvt_pk_bf16_f32 v162, v164, v165\n"
"v_mfma_f32_16x16x32_bf16 v[28:31], a[252:255], a[148:151], v[28:31]\n"
"buffer_load_dwordx4 v254, s[24:27], s40 offen offset:2048 lds\n"
"v_mfma_f32_16x16x32_bf16 v[24:27], a[240:243], a[144:147], v[24:27]\n"
"s_add_u32 s49, s49, 128\n"
"v_cvt_pk_bf16_f32 v147, v150, v151\n"
"v_accvgpr_write_b32 a41, v153\n"
"v_mfma_f32_16x16x32_bf16 v[24:27], a[244:247], a[148:151], v[24:27]\n"
"s_sub_u32 s41, s49, 1280\n"
"v_cvt_pk_bf16_f32 v98, v100, v101\n"
"v_cvt_pk_bf16_f32 v88, v88, v89\n"
"v_mfma_f32_16x16x32_bf16 v[20:23], a[232:235], a[144:147], v[20:23]\n"
"s_min_u32 s49, s49, s41\n"
"ds_read_b128 a[136:139], v246 offset:0\n"
"v_accvgpr_write_b32 a19, v131\n"
"v_mfma_f32_16x16x32_bf16 v[20:23], a[236:239], a[148:151], v[20:23]\n"
"s_xor_b32 s51, s51, s74\n"
"ds_read_b64_tr_b16 a[198:199], v244 offset:8704\n"
"ds_read_b128 a[44:47], v182 offset:0\n"
"v_mfma_f32_16x16x32_bf16 v[16:19], a[224:227], a[144:147], v[16:19]\n"
"ds_read_b64_tr_b16 a[212:213], v244 offset:9216\n"
"v_cvt_pk_bf16_f32 v163, v166, v167\n"
"v_mfma_f32_16x16x32_bf16 v[16:19], a[228:231], a[148:151], v[16:19]\n"
"ds_read_b128 a[148:151], v247 offset:2048\n"
"v_accvgpr_write_b32 a11, v123\n"
"v_mfma_f32_16x16x32_bf16 v[32:35], a[224:227], a[152:155], v[32:35]\n"
"ds_read_b128 a[4:7], v181 offset:0\n"
"v_and_b32 v102, 0xf0, v242\n"
"v_mfma_f32_16x16x32_bf16 v[32:35], a[228:231], a[156:159], v[32:35]\n"
"ds_read_b64_tr_b16 a[202:203], v245 offset:512\n"
"ds_read_b64_tr_b16 a[204:205], v245 offset:8192\n"
"v_mfma_f32_16x16x32_bf16 v[36:39], a[232:235], a[152:155], v[36:39]\n"
"ds_read_b64_tr_b16 a[200:201], v245 offset:0\n"
"v_cvt_pk_bf16_f32 v89, v90, v91\n"
"v_mfma_f32_16x16x32_bf16 v[36:39], a[236:239], a[156:159], v[36:39]\n"
"ds_read_b128 a[20:23], v181 offset:45056\n"
"v_accvgpr_write_b32 a27, v139\n"
"v_mfma_f32_16x16x32_bf16 v[40:43], a[240:243], a[152:155], v[40:43]\n"
"v_accvgpr_write_b32 a0, v112\n"
"v_accvgpr_write_b32 a1, v113\n"
"v_mfma_f32_16x16x32_bf16 v[40:43], a[244:247], a[156:159], v[40:43]\n"
"ds_read_b128 a[180:183], v251 offset:2048\n"
"v_accvgpr_write_b32 a2, v114\n"
"v_mfma_f32_16x16x32_bf16 v[44:47], a[248:251], a[152:155], v[44:47]\n"
"s_add_u32 s47, s47, 1\n"
"ds_read_b128 a[144:147], v246 offset:2048\n"
"v_accvgpr_write_b32 a17, v129\n"
"v_mfma_f32_16x16x32_bf16 v[44:47], a[252:255], a[156:159], v[44:47]\n"
"s_waitcnt lgkmcnt(7)\n"
"ds_read_b128 a[160:163], v252 offset:0\n"
"v_permlane16_swap_b32 v161, v163\n"
"v_permlane16_swap_b32 v169, v171\n"
"v_accvgpr_write_b32 a9, v121\n"
"v_permlane16_swap_b32 v128, v130\n"
"v_permlane16_swap_b32 v145, v147\n"
"v_accvgpr_write_b32 a42, v154\n"
"v_bfe_u32 v100, v242, 9, 1\n"
"v_cvt_pk_bf16_f32 v91, v94, v95\n"
"v_pk_fma_f32 v[0:1], v[0:1], s[70:71], s[70:71] op_sel:[0,0,1] op_sel_hi:[1,0,1]\nv_pk_fma_f32 v[2:3], v[2:3], s[70:71], s[70:71] op_sel:[0,0,1] op_sel_hi:[1,0,1]\nv_pk_fma_f32 v[4:5], v[4:5], s[70:71], s[70:71] op_sel:[0,0,1] op_sel_hi:[1,0,1]\nv_pk_fma_f32 v[6:7], v[6:7], s[70:71], s[70:71] op_sel:[0,0,1] op_sel_hi:[1,0,1]\nv_pk_fma_f32 v[8:9], v[8:9], s[70:71], s[70:71] op_sel:[0,0,1] op_sel_hi:[1,0,1]\nv_pk_fma_f32 v[10:11], v[10:11], s[70:71], s[70:71] op_sel:[0,0,1] op_sel_hi:[1,0,1]\nv_pk_fma_f32 v[12:13], v[12:13], s[70:71], s[70:71] op_sel:[0,0,1] op_sel_hi:[1,0,1]\nv_pk_fma_f32 v[14:15], v[14:15], s[70:71], s[70:71] op_sel:[0,0,1] op_sel_hi:[1,0,1]\nv_pk_fma_f32 v[16:17], v[16:17], s[70:71], s[70:71] op_sel:[0,0,1] op_sel_hi:[1,0,1]\nv_pk_fma_f32 v[18:19], v[18:19], s[70:71], s[70:71] op_sel:[0,0,1] op_sel_hi:[1,0,1]\nv_pk_fma_f32 v[20:21], v[20:21], s[70:71], s[70:71] op_sel:[0,0,1] op_sel_hi:[1,0,1]\nv_pk_fma_f32 v[22:23], v[22:23], s[70:71], s[70:71] op_sel:[0,0,1] op_sel_hi:[1,0,1]\nv_pk_fma_f32 v[24:25], v[24:25], s[70:71], s[70:71] op_sel:[0,0,1] op_sel_hi:[1,0,1]\nv_pk_fma_f32 v[26:27], v[26:27], s[70:71], s[70:71] op_sel:[0,0,1] op_sel_hi:[1,0,1]\nv_pk_fma_f32 v[28:29], v[28:29], s[70:71], s[70:71] op_sel:[0,0,1] op_sel_hi:[1,0,1]\nv_pk_fma_f32 v[30:31], v[30:31], s[70:71], s[70:71] op_sel:[0,0,1] op_sel_hi:[1,0,1]\n"
"v_cvt_pk_bf16_f32 v16, v16, v17\n"
"v_cvt_pk_bf16_f32 v24, v24, v25\n"
"v_cvt_pk_bf16_f32 v25, v26, v27\n"
"v_mfma_f32_16x16x32_bf16 v[112:115], a[192:195], a[136:139], v[228:231]\n"
"v_cvt_pk_bf16_f32 v0, v0, v1\n"
"v_cvt_pk_bf16_f32 v8, v8, v9\n"
"v_mfma_f32_16x16x32_bf16 v[112:115], a[196:199], a[140:143], v[112:115]\n"
"s_waitcnt lgkmcnt(4)\n"
"ds_write_b128 v243, v[168:171] offset:2048\n"
"v_permlane16_swap_b32 v160, v162\n"
"v_accvgpr_write_b32 a8, v120\n"
"v_cvt_pk_bf16_f32 v81, v82, v83\n"
"v_pk_fma_f32 v[64:65], v[64:65], s[70:71], s[70:71] op_sel:[0,0,1] op_sel_hi:[1,0,1]\nv_pk_fma_f32 v[66:67], v[66:67], s[70:71], s[70:71] op_sel:[0,0,1] op_sel_hi:[1,0,1]\nv_pk_fma_f32 v[68:69], v[68:69], s[70:71], s[70:71] op_sel:[0,0,1] op_sel_hi:[1,0,1]\nv_pk_fma_f32 v[70:71], v[70:71], s[70:71], s[70:71] op_sel:[0,0,1] op_sel_hi:[1,0,1]\nv_pk_fma_f32 v[72:73], v[72:73], s[70:71], s[70:71] op_sel:[0,0,1] op_sel_hi:[1,0,1]\nv_pk_fma_f32 v[74:75], v[74:75], s[70:71], s[70:71] op_sel:[0,0,1] op_sel_hi:[1,0,1]\nv_pk_fma_f32 v[76:77], v[76:77], s[70:71], s[70:71] op_sel:[0,0,1] op_sel_hi:[1,0,1]\nv_pk_fma_f32 v[78:79], v[78:79], s[70:71], s[70:71] op_sel:[0,0,1] op_sel_hi:[1,0,1]\nv_pk_fma_f32 v[48:49], v[48:49], s[70:71], s[70:71] op_sel:[0,0,1] op_sel_hi:[1,0,1]\nv_pk_fma_f32 v[50:51], v[50:51], s[70:71], s[70:71] op_sel:[0,0,1] op_sel_hi:[1,0,1]\nv_pk_fma_f32 v[52:53], v[52:53], s[70:71], s[70:71] op_sel:[0,0,1] op_sel_hi:[1,0,1]\nv_pk_fma_f32 v[54:55], v[54:55], s[70:71], s[70:71] op_sel:[0,0,1] op_sel_hi:[1,0,1]\nv_pk_fma_f32 v[56:57], v[56:57], s[70:71], s[70:71] op_sel:[0,0,1] op_sel_hi:[1,0,1]\nv_pk_fma_f32 v[58:59], v[58:59], s[70:71], s[70:71] op_sel:[0,0,1] op_sel_hi:[1,0,1]\nv_pk_fma_f32 v[60:61], v[60:61], s[70:71], s[70:71] op_sel:[0,0,1] op_sel_hi:[1,0,1]\nv_pk_fma_f32 v[62:63], v[62:63], s[70:71], s[70:71] op_sel:[0,0,1] op_sel_hi:[1,0,1]\n"
"v_cvt_pk_bf16_f32 v64, v64, v65\n"
"v_mfma_f32_16x16x32_bf16 v[116:119], a[200:203], a[136:139], v[228:231]\n"
"s_waitcnt vmcnt(11) lgkmcnt(4)\n"
"v_accvgpr_write_b32 a18, v130\n"
"v_accvgpr_write_b32 a26, v138\n"
"v_mfma_f32_16x16x32_bf16 v[116:119], a[204:207], a[140:143], v[116:119]\n"
"s_barrier\n"
"ds_write_b128 v243, v[160:163] offset:0\n"
"v_accvgpr_write_b32 a16, v128\n"
"v_accvgpr_write_b32 a33, v145\n"
"v_cvt_pk_bf16_f32 v1, v2, v3\n"
"v_pk_fma_f32 v[32:33], v[32:33], s[70:71], s[70:71] op_sel:[0,0,1] op_sel_hi:[1,0,1]\nv_pk_fma_f32 v[34:35], v[34:35], s[70:71], s[70:71] op_sel:[0,0,1] op_sel_hi:[1,0,1]\nv_pk_fma_f32 v[36:37], v[36:37], s[70:71], s[70:71] op_sel:[0,0,1] op_sel_hi:[1,0,1]\nv_pk_fma_f32 v[38:39], v[38:39], s[70:71], s[70:71] op_sel:[0,0,1] op_sel_hi:[1,0,1]\nv_pk_fma_f32 v[40:41], v[40:41], s[70:71], s[70:71] op_sel:[0,0,1] op_sel_hi:[1,0,1]\nv_pk_fma_f32 v[42:43], v[42:43], s[70:71], s[70:71] op_sel:[0,0,1] op_sel_hi:[1,0,1]\nv_pk_fma_f32 v[44:45], v[44:45], s[70:71], s[70:71] op_sel:[0,0,1] op_sel_hi:[1,0,1]\nv_pk_fma_f32 v[46:47], v[46:47], s[70:71], s[70:71] op_sel:[0,0,1] op_sel_hi:[1,0,1]\n"
"v_cvt_pk_bf16_f32 v32, v32, v33\n"
"v_mfma_f32_16x16x32_bf16 v[120:123], a[208:211], a[136:139], v[228:231]\n"
"s_add_u32 s40, s48, 256\n"
"s_xor_b32 m0, s50, 0xc000\n"
"ds_read_b128 a[152:155], v246 offset:4096\n"
"v_mfma_f32_16x16x32_bf16 v[120:123], a[212:215], a[140:143], v[120:123]\n"
"buffer_load_dwordx4 v224, s[28:31], s40 offen lds\n"
"v_mfma_f32_16x16x32_bf16 v[124:127], a[216:219], a[136:139], v[228:231]\n"
"v_accvgpr_write_b32 a40, v152\n"
"v_cvt_pk_bf16_f32 v40, v40, v41\n"
"v_mfma_f32_16x16x32_bf16 v[124:127], a[220:223], a[140:143], v[124:127]\n"
"s_waitcnt lgkmcnt(3)\n"
"ds_read_b128 a[168:171], v250 offset:0\n"
"v_cvt_pk_bf16_f32 v105, v106, v107\n"
"v_mfma_f32_16x16x32_bf16 v[140:143], a[216:219], a[144:147], v[228:231]\n"
"ds_read_b128 a[156:159], v247 offset:4096\n"
"v_accvgpr_write_b32 a34, v146\n"
"v_mfma_f32_16x16x32_bf16 v[140:143], a[220:223], a[148:151], v[140:143]\n"
"ds_read_b128 a[172:175], v251 offset:0\n"
"v_cvt_pk_bf16_f32 v2, v4, v5\n"
"v_mfma_f32_16x16x32_bf16 v[136:139], a[208:211], a[144:147], v[228:231]\n"
"ds_read_b64_tr_b16 a[224:225], v244 offset:16384\n"
"v_lshl_add_u32 v100, v100, 8, v102\n"
"v_mfma_f32_16x16x32_bf16 v[136:139], a[212:215], a[148:151], v[136:139]\n"
"buffer_load_dwordx4 v225, s[28:31], s40 offen offset:1024 lds\n"
"v_mfma_f32_16x16x32_bf16 v[132:135], a[200:203], a[144:147], v[228:231]\n"
"v_bfe_u32 v102, v242, 8, 1\n"
"v_cvt_pk_bf16_f32 v41, v42, v43\n"
"v_mfma_f32_16x16x32_bf16 v[132:135], a[204:207], a[148:151], v[132:135]\n"
"v_accvgpr_write_b32 a35, v147\n"
"v_cvt_pk_bf16_f32 v65, v66, v67\n"
"v_mfma_f32_16x16x32_bf16 v[128:131], a[192:195], a[144:147], v[228:231]\n"
"ds_read_b64_tr_b16 a[234:235], v245 offset:16896\n"
"v_cvt_pk_bf16_f32 v72, v72, v73\n"
"v_mfma_f32_16x16x32_bf16 v[128:131], a[196:199], a[148:151], v[128:131]\n"
"s_waitcnt lgkmcnt(4)\n"
"ds_read_b64_tr_b16 a[236:237], v245 offset:24576\n"
"v_bfe_u32 v101, v242, 9, 1\n"
"v_mfma_f32_16x16x32_bf16 v[144:147], a[192:195], a[152:155], v[228:231]\n"
"s_waitcnt lgkmcnt(4)\n"
"ds_read_b64_tr_b16 a[226:227], v244 offset:16896\n"
"v_cvt_pk_bf16_f32 v9, v10, v11\n"
"v_mfma_f32_16x16x32_bf16 v[144:147], a[196:199], a[156:159], v[144:147]\n"
"buffer_load_dwordx4 v226, s[28:31], s40 offen offset:2048 lds\n"
"v_mfma_f32_16x16x32_bf16 v[148:151], a[200:203], a[152:155], v[228:231]\n"
"v_cvt_pk_bf16_f32 v3, v6, v7\n"
"v_cvt_pk_bf16_f32 v17, v18, v19\n"
"v_mfma_f32_16x16x32_bf16 v[148:151], a[204:207], a[156:159], v[148:151]\n"
"v_cvt_pk_bf16_f32 v66, v68, v69\n"
"v_cvt_pk_bf16_f32 v73, v74, v75\n"
"v_mfma_f32_16x16x32_bf16 v[152:155], a[208:211], a[152:155], v[228:231]\n"
"ds_read_b64_tr_b16 a[252:253], v245 offset:25600\n"
"v_lshl_add_u32 v100, v102, 3, v100\n"
"v_mfma_f32_16x16x32_bf16 v[152:155], a[212:215], a[156:159], v[152:155]\n"
"ds_read_b64_tr_b16 a[244:245], v244 offset:25600\n"
"ds_read_b64_tr_b16 a[250:251], v245 offset:17920\n"
"v_mfma_f32_16x16x32_bf16 v[156:159], a[216:219], a[152:155], v[228:231]\n"
"v_cvt_pk_bf16_f32 v48, v48, v49\n"
"v_cvt_pk_bf16_f32 v43, v46, v47\n"
"v_mfma_f32_16x16x32_bf16 v[156:159], a[220:223], a[156:159], v[156:159]\n"
"buffer_load_dwordx4 v227, s[28:31], s40 offen offset:3072 lds\n"
"v_mfma_f32_16x16x32_bf16 v[172:175], a[216:219], a[160:163], v[228:231]\n"
"s_cmp_eq_u32 s47, 8\n"
"v_cvt_pk_bf16_f32 v75, v78, v79\n"
"v_cvt_pk_bf16_f32 v27, v30, v31\n"
"v_mfma_f32_16x16x32_bf16 v[172:175], a[220:223], a[164:167], v[172:175]\n"
"s_cselect_b32 s41, s52, s58\n"
"v_cvt_pk_bf16_f32 v56, v56, v57\n"
"v_permlane16_swap_b32 v1, v3\n"
"v_cvt_pk_bf16_f32 v11, v14, v15\n"
"v_cvt_pk_bf16_f32 v26, v28, v29\n"
"v_cvt_pk_bf16_f32 v33, v34, v35\n"
"v_mfma_f32_16x16x32_bf16 v[168:171], a[208:211], a[160:163], v[228:231]\n"
"s_add_u32 s48, s48, s41\n"
"ds_read_b128 a[176:179], v250 offset:2048\n"
"v_cvt_pk_bf16_f32 v83, v86, v87\n"
"v_mfma_f32_16x16x32_bf16 v[168:171], a[212:215], a[164:167], v[168:171]\n"
"s_sub_u32 s41, s48, 5242880\n"
"ds_read_b64_tr_b16 a[230:231], v244 offset:25088\n"
"ds_read_b64_tr_b16 a[248:249], v245 offset:17408\n"
"v_mfma_f32_16x16x32_bf16 v[164:167], a[200:203], a[160:163], v[228:231]\n"
"s_min_u32 s48, s48, s41\n"
"ds_read_b64_tr_b16 a[232:233], v245 offset:16384\n"
"v_xor_b32_e64 v102, s42, 1\n"
"v_mfma_f32_16x16x32_bf16 v[164:167], a[204:207], a[164:167], v[164:167]\n"
"ds_read_b128 a[184:187], v250 offset:4096\n"
"v_lshl_add_u32 v100, v102, 10, v100\n"
"v_mfma_f32_16x16x32_bf16 v[160:163], a[192:195], a[160:163], v[228:231]\n"
"s_mov_b32 m0, s50\n"
"v_permlane16_swap_b32 v9, v11\n"
"v_mfma_f32_16x16x32_bf16 v[160:163], a[196:199], a[164:167], v[160:163]\n"
"buffer_load_dwordx4 v224, s[28:31], s48 offen lds\n"
"v_mfma_f32_16x16x32_bf16 v[176:179], a[192:195], a[168:171], v[228:231]\n"
"s_waitcnt lgkmcnt(5)\n"
"v_cvt_pk_bf16_f32 v49, v50, v51\n"
"v_cvt_pk_bf16_f32 v34, v36, v37\n"
"v_mfma_f32_16x16x32_bf16 v[176:179], a[196:199], a[172:175], v[176:179]\n"
"v_cvt_pk_bf16_f32 v10, v12, v13\n"
"v_and_b32 v102, 0xf0, v242\n"
"v_mfma_f32_16x16x32_bf16 v[180:183], a[200:203], a[168:171], v[228:231]\n"
"ds_read_b64_tr_b16 a[240:241], v244 offset:17408\n"
"ds_read_b64_tr_b16 a[254:255], v245 offset:26112\n"
"v_mfma_f32_16x16x32_bf16 v[180:183], a[204:207], a[172:175], v[180:183]\n"
"buffer_load_dwordx4 v225, s[28:31], s48 offen offset:1024 lds\n"
"v_mfma_f32_16x16x32_bf16 v[184:187], a[208:211], a[168:171], v[228:231]\n"
"v_cvt_pk_bf16_f32 v90, v92, v93\n"
"v_cvt_pk_bf16_f32 v42, v44, v45\n"
"v_mfma_f32_16x16x32_bf16 v[184:187], a[212:215], a[172:175], v[184:187]\n"
"v_permlane16_swap_b32 v0, v2\n"
"v_mfma_f32_16x16x32_bf16 v[188:191], a[216:219], a[168:171], v[228:231]\n"
"ds_read_b128 a[188:191], v251 offset:4096\n"
"v_accvgpr_write_b32 a51, v3\n"
"v_mfma_f32_16x16x32_bf16 v[188:191], a[220:223], a[172:175], v[188:191]\n"
"s_waitcnt lgkmcnt(3)\n"
"ds_read_b64_tr_b16 a[238:239], v245 offset:25088\n"
"v_cvt_pk_bf16_f32 v67, v70, v71\n"
"v_mfma_f32_16x16x32_bf16 v[204:207], a[216:219], a[176:179], v[228:231]\n"
"ds_read_b64_tr_b16 a[246:247], v244 offset:26112\n"
"v_add_u32 v100, 94208, v100\n"
"v_mfma_f32_16x16x32_bf16 v[204:207], a[220:223], a[180:183], v[204:207]\n"
"buffer_load_dwordx4 v226, s[28:31], s48 offen offset:2048 lds\n"
"v_mfma_f32_16x16x32_bf16 v[200:203], a[208:211], a[176:179], v[228:231]\n"
"v_accvgpr_write_b32 a50, v2\n"
"v_lshl_add_u32 v101, v101, 8, v102\n"
"v_mfma_f32_16x16x32_bf16 v[200:203], a[212:215], a[180:183], v[200:203]\n"
"v_permlane16_swap_b32 v24, v26\n"
"v_mfma_f32_16x16x32_bf16 v[196:199], a[200:203], a[176:179], v[228:231]\n"
"ds_read_b64_tr_b16 a[228:229], v244 offset:24576\n"
"v_cvt_pk_bf16_f32 v106, v108, v109\n"
"v_mfma_f32_16x16x32_bf16 v[196:199], a[204:207], a[180:183], v[196:199]\n"
"ds_write2st64_b64 v100, v[96:97], v[98:99] offset0:120 offset1:121\n"
"v_accvgpr_write_b32 a49, v1\n"
"v_cvt_pk_bf16_f32 v19, v22, v23\n"
"v_permlane16_swap_b32 v25, v27\n"
"v_cvt_pk_bf16_f32 v35, v38, v39\n"
"v_mfma_f32_16x16x32_bf16 v[192:195], a[192:195], a[176:179], v[228:231]\n"
"v_cvt_pk_bf16_f32 v74, v76, v77\n"
"v_cvt_pk_bf16_f32 v18, v20, v21\n"
"v_mfma_f32_16x16x32_bf16 v[192:195], a[196:199], a[180:183], v[192:195]\n"
"buffer_load_dwordx4 v227, s[28:31], s48 offen offset:3072 lds\n"
"v_mfma_f32_16x16x32_bf16 v[208:211], a[192:195], a[184:187], v[228:231]\n"
"s_waitcnt lgkmcnt(2)\n"
"s_xor_b32 s50, s50, s74\n"
"v_permlane16_swap_b32 v33, v35\n"
"v_mfma_f32_16x16x32_bf16 v[208:211], a[196:199], a[188:191], v[208:211]\n"
"v_permlane16_swap_b32 v40, v42\n"
"v_mfma_f32_16x16x32_bf16 v[212:215], a[200:203], a[184:187], v[228:231]\n"
"v_permlane16_swap_b32 v17, v19\n"
"v_mfma_f32_16x16x32_bf16 v[212:215], a[204:207], a[188:191], v[212:215]\n"
"ds_read_b64_tr_b16 a[242:243], v244 offset:17920\n"
"v_cvt_pk_bf16_f32 v107, v110, v111\n"
"v_mfma_f32_16x16x32_bf16 v[216:219], a[208:211], a[184:187], v[228:231]\n"
"v_permlane16_swap_b32 v16, v18\n"
"v_mfma_f32_16x16x32_bf16 v[216:219], a[212:215], a[188:191], v[216:219]\n"
"ds_write2st64_b64 v100, v[104:105], v[106:107] offset0:128 offset1:129\n"
"v_cvt_pk_bf16_f32 v82, v84, v85\n"
"v_accvgpr_write_b32 a75, v27\n"
"v_permlane16_swap_b32 v41, v43\n"
"v_mfma_f32_16x16x32_bf16 v[220:223], a[216:219], a[184:187], v[228:231]\n"
"v_accvgpr_write_b32 a48, v0\n"
"v_accvgpr_write_b32 a64, v16\n"
"v_mfma_f32_16x16x32_bf16 v[220:223], a[220:223], a[188:191], v[220:223]\n"
"ds_write2st64_b64 v100, v[80:81], v[82:83] offset0:88 offset1:89\n"
"v_cvt_pk_bf16_f32 v57, v58, v59\n"
"v_cvt_pk_bf16_f32 v58, v60, v61\n"
"v_permlane16_swap_b32 v8, v10\n"
"v_accvgpr_write_b32 a57, v9\n"
"v_mfma_f32_16x16x32_bf16 v[0:3], a[224:227], a[136:139], v[228:231]\n"
"s_waitcnt lgkmcnt(3)\n"
"v_cvt_pk_bf16_f32 v50, v52, v53\n"
"v_xor_b32 v245, s74, v245\n"
"v_mfma_f32_16x16x32_bf16 v[0:3], a[228:231], a[140:143], v[0:3]\n"
"ds_write2st64_b64 v100, v[72:73], v[74:75] offset0:64 offset1:65\n"
"v_cvt_pk_bf16_f32 v59, v62, v63\n"
"v_accvgpr_write_b32 a58, v10\n"
"v_accvgpr_write_b32 a66, v18\n"
"v_accvgpr_write_b32 a81, v33\n"
"v_mfma_f32_16x16x32_bf16 v[4:7], a[232:235], a[136:139], v[228:231]\n"
"s_waitcnt vmcnt(8) lgkmcnt(3)\n"
"v_permlane16_swap_b32 v57, v59\n"
"v_mfma_f32_16x16x32_bf16 v[4:7], a[236:239], a[140:143], v[4:7]\n"
"s_barrier\n"
"ds_write2st64_b64 v100, v[88:89], v[90:91] offset0:96 offset1:97\n"
"v_accvgpr_write_b32 a56, v8\n"
"v_accvgpr_write_b32 a59, v11\n"
"v_accvgpr_write_b32 a72, v24\n"
"v_mfma_f32_16x16x32_bf16 v[8:11], a[240:243], a[136:139], v[228:231]\n"
"s_mov_b32 m0, s51\n"
"ds_read_b64_tr_b16 a[202:203], v245 offset:512\n"
"v_accvgpr_write_b32 a73, v25\n"
"v_mfma_f32_16x16x32_bf16 v[8:11], a[244:247], a[140:143], v[8:11]\n"
"buffer_load_dwordx4 v248, s[24:27], s49 offen lds\n"
"v_mfma_f32_16x16x32_bf16 v[12:15], a[248:251], a[136:139], v[228:231]\n"
"v_accvgpr_write_b32 a65, v17\n"
"v_accvgpr_write_b32 a67, v19\n"
"v_mfma_f32_16x16x32_bf16 v[12:15], a[252:255], a[140:143], v[12:15]\n"
"v_permlane16_swap_b32 v56, v58\n"
"v_mfma_f32_16x16x32_bf16 v[28:31], a[248:251], a[144:147], v[228:231]\n"
"v_accvgpr_write_b32 a74, v26\n"
"v_accvgpr_write_b32 a83, v35\n"
"v_mfma_f32_16x16x32_bf16 v[28:31], a[252:255], a[148:151], v[28:31]\n"
"ds_write2st64_b64 v100, v[64:65], v[66:67] offset0:0 offset1:1\n"
"v_permlane16_swap_b32 v32, v34\n"
"v_accvgpr_write_b32 a88, v40\n"
"v_mfma_f32_16x16x32_bf16 v[24:27], a[240:243], a[144:147], v[228:231]\n"
"v_accvgpr_write_b32 a89, v41\n"
"v_accvgpr_write_b32 a90, v42\n"
"v_mfma_f32_16x16x32_bf16 v[24:27], a[244:247], a[148:151], v[24:27]\n"
"buffer_load_dwordx4 v249, s[24:27], s49 offen offset:1024 lds\n"
"v_mfma_f32_16x16x32_bf16 v[20:23], a[232:235], a[144:147], v[228:231]\n"
"v_accvgpr_write_b32 a82, v34\n"
"v_accvgpr_write_b32 a91, v43\n"
"v_mfma_f32_16x16x32_bf16 v[20:23], a[236:239], a[148:151], v[20:23]\n"
"v_cvt_pk_bf16_f32 v51, v54, v55\n"
"v_accvgpr_write_b32 a80, v32\n"
"v_mfma_f32_16x16x32_bf16 v[16:19], a[224:227], a[144:147], v[228:231]\n"
"ds_read_b64_tr_b16 a[216:217], v245 offset:1024\n"
"v_xor_b32 v247, s74, v247\n"
"v_mfma_f32_16x16x32_bf16 v[16:19], a[228:231], a[148:151], v[16:19]\n"
"ds_write_b128 v243, v[56:59] offset:6144\n"
"v_mfma_f32_16x16x32_bf16 v[32:35], a[224:227], a[152:155], v[228:231]\n"
"v_permlane16_swap_b32 v48, v50\n"
"v_mfma_f32_16x16x32_bf16 v[32:35], a[228:231], a[156:159], v[32:35]\n"
"buffer_load_dwordx4 v254, s[24:27], s49 offen offset:2048 lds\n"
"v_mfma_f32_16x16x32_bf16 v[36:39], a[232:235], a[152:155], v[228:231]\n"
"s_waitcnt vmcnt(63) lgkmcnt(2)\n"
"v_permlane16_swap_b32 v49, v51\n"
"v_mfma_f32_16x16x32_bf16 v[36:39], a[236:239], a[156:159], v[36:39]\n"
"s_barrier\n"
"v_mfma_f32_16x16x32_bf16 v[40:43], a[240:243], a[152:155], v[228:231]\n"
"ds_read_b64_tr_b16 a[204:205], v245 offset:8192\n"
"ds_read_b128 a[140:143], v247 offset:0\n"
"v_mfma_f32_16x16x32_bf16 v[40:43], a[244:247], a[156:159], v[40:43]\n"
"ds_write_b128 v243, v[48:51] offset:4096\n"
"v_mfma_f32_16x16x32_bf16 v[44:47], a[248:251], a[152:155], v[228:231]\n"
"v_bfe_u32 v102, v242, 8, 1\n"
"v_lshl_add_u32 v101, v102, 9, v101\n"
"v_mfma_f32_16x16x32_bf16 v[44:47], a[252:255], a[156:159], v[44:47]\n"
"buffer_load_dwordx4 v255, s[24:27], s49 offen offset:3072 lds\n"
"v_mfma_f32_16x16x32_bf16 v[60:63], a[248:251], a[160:163], v[228:231]\n"
"v_xor_b32 v244, s74, v244\n"
"v_xor_b32_e64 v102, s42, 0\n"
"v_mfma_f32_16x16x32_bf16 v[60:63], a[252:255], a[164:167], v[60:63]\n"
"v_lshl_add_u32 v101, v102, 10, v101\n"
"v_add_u32 v101, 94208, v101\n"
"v_mfma_f32_16x16x32_bf16 v[56:59], a[240:243], a[160:163], v[228:231]\n"
"ds_read_b64_tr_b16 a[198:199], v244 offset:8704\n"
"ds_read_b64_tr_b16 a[220:221], v245 offset:9216\n"
"v_mfma_f32_16x16x32_bf16 v[56:59], a[244:247], a[164:167], v[56:59]\n"
"s_add_u32 s40, s49, 40960\n"
"ds_read_b128 a[52:55], v101 offset:0\n"
"v_xor_b32 v253, s74, v253\n"
"v_mfma_f32_16x16x32_bf16 v[52:55], a[232:235], a[160:163], v[228:231]\n"
"s_add_u32 m0, s51, 4096\n"
"ds_read_b64_tr_b16 a[206:207], v245 offset:8704\n"
"ds_read_b128 a[76:79], v101 offset:49152\n"
"v_mfma_f32_16x16x32_bf16 v[52:55], a[236:239], a[164:167], v[52:55]\n"
"buffer_load_dwordx4 v248, s[24:27], s40 offen lds\n"
"v_mfma_f32_16x16x32_bf16 v[48:51], a[224:227], a[160:163], v[228:231]\n"
"v_xor_b32 v246, s74, v246\n"
"v_add_u32 v102, s75, v101\n"
"v_mfma_f32_16x16x32_bf16 v[48:51], a[228:231], a[164:167], v[48:51]\n"
"ds_read_b64_tr_b16 a[192:193], v244 offset:0\n"
"ds_read_b128 a[68:71], v101 offset:45056\n"
"v_mfma_f32_16x16x32_bf16 v[64:67], a[224:227], a[168:171], v[228:231]\n"
"ds_read_b64_tr_b16 a[212:213], v244 offset:9216\n"
"ds_read_b128 a[164:167], v253 offset:0\n"
"v_mfma_f32_16x16x32_bf16 v[64:67], a[228:231], a[172:175], v[64:67]\n"
"ds_read_b64_tr_b16 a[208:209], v244 offset:1024\n"
"ds_read_b64_tr_b16 a[218:219], v245 offset:1536\n"
"v_mfma_f32_16x16x32_bf16 v[68:71], a[232:235], a[168:171], v[228:231]\n"
"ds_read_b64_tr_b16 a[200:201], v245 offset:0\n"
"ds_read_b128 a[84:87], v101 offset:61440\n"
"v_mfma_f32_16x16x32_bf16 v[68:71], a[236:239], a[172:175], v[68:71]\n"
"buffer_load_dwordx4 v249, s[24:27], s40 offen offset:1024 lds\n"
"v_mfma_f32_16x16x32_bf16 v[72:75], a[240:243], a[168:171], v[228:231]\n"
"ds_read_b64_tr_b16 a[214:215], v244 offset:9728\n"
"ds_read_b128 a[60:63], v101 offset:32768\n"
"v_mfma_f32_16x16x32_bf16 v[72:75], a[244:247], a[172:175], v[72:75]\n"
"v_xor_b32 v250, s74, v250\n"
"v_xor_b32 v251, s74, v251\n"
"v_mfma_f32_16x16x32_bf16 v[76:79], a[248:251], a[168:171], v[228:231]\n"
"ds_read_b128 a[92:95], v102 offset:0\n"
"ds_read_b128 a[168:171], v250 offset:0\n"
"v_mfma_f32_16x16x32_bf16 v[76:79], a[252:255], a[172:175], v[76:79]\n"
"ds_read_b128 a[152:155], v246 offset:4096\n"
"v_xor_b32 v252, s74, v252\n"
"v_mfma_f32_16x16x32_bf16 v[92:95], a[248:251], a[176:179], v[228:231]\n"
"ds_read_b64_tr_b16 a[222:223], v245 offset:9728\n"
"ds_read_b128 a[160:163], v252 offset:0\n"
"v_mfma_f32_16x16x32_bf16 v[92:95], a[252:255], a[180:183], v[92:95]\n"
"buffer_load_dwordx4 v254, s[24:27], s40 offen offset:2048 lds\n"
"v_mfma_f32_16x16x32_bf16 v[88:91], a[240:243], a[176:179], v[228:231]\n"
"s_add_u32 s49, s49, 128\n"
"ds_read_b128 a[144:147], v246 offset:2048\n"
"ds_read_b128 a[148:151], v247 offset:2048\n"
"v_mfma_f32_16x16x32_bf16 v[88:91], a[244:247], a[180:183], v[88:91]\n"
"s_sub_u32 s41, s49, 1280\n"
"ds_read_b128 a[136:139], v246 offset:0\n"
"v_mfma_f32_16x16x32_bf16 v[84:87], a[232:235], a[176:179], v[228:231]\n"
"s_min_u32 s49, s49, s41\n"
"ds_read_b64_tr_b16 a[196:197], v244 offset:8192\n"
"ds_read_b64_tr_b16 a[210:211], v244 offset:1536\n"
"v_mfma_f32_16x16x32_bf16 v[84:87], a[236:239], a[180:183], v[84:87]\n"
"s_xor_b32 s51, s51, s74\n"
"ds_read_b64_tr_b16 a[194:195], v244 offset:512\n"
"ds_read_b128 a[156:159], v247 offset:4096\n"
"v_mfma_f32_16x16x32_bf16 v[80:83], a[224:227], a[176:179], v[228:231]\n"
"ds_read_b128 a[176:179], v250 offset:2048\n"
"v_mfma_f32_16x16x32_bf16 v[80:83], a[228:231], a[180:183], v[80:83]\n"
"ds_read_b128 a[172:175], v251 offset:0\n"
"ds_read_b128 a[180:183], v251 offset:2048\n"
"v_mfma_f32_16x16x32_bf16 v[96:99], a[224:227], a[184:187], v[228:231]\n"
"v_mfma_f32_16x16x32_bf16 v[96:99], a[228:231], a[188:191], v[96:99]\n"
"v_mfma_f32_16x16x32_bf16 v[100:103], a[232:235], a[184:187], v[228:231]\n"
"v_mfma_f32_16x16x32_bf16 v[100:103], a[236:239], a[188:191], v[100:103]\n"
"v_mfma_f32_16x16x32_bf16 v[104:107], a[240:243], a[184:187], v[228:231]\n"
"v_mfma_f32_16x16x32_bf16 v[104:107], a[244:247], a[188:191], v[104:107]\n"
"v_mfma_f32_16x16x32_bf16 v[108:111], a[248:251], a[184:187], v[228:231]\n"
"s_add_u32 s47, s47, 1\n"
"ds_read_b128 a[184:187], v250 offset:4096\n"
"v_mfma_f32_16x16x32_bf16 v[108:111], a[252:255], a[188:191], v[108:111]\n"
"s_waitcnt lgkmcnt(1)\n"
"ds_read_b128 a[188:191], v251 offset:4096\n"
"s_mov_b32 s62, 18\n"
"s_branch LPE_%=\n"
"LPT2_%=:\n"
"v_mfma_f32_16x16x32_bf16 v[112:115], a[192:195], a[136:139], v[112:115]\n"
"s_add_u32 s78, s32, s96\ns_addc_u32 s79, s33, 0\n"
"s_mov_b64 s[80:81], 0\n"
"s_mov_b32 s82, 0\n"
"v_mfma_f32_16x16x32_bf16 v[112:115], a[196:199], a[140:143], v[112:115]\n"
"v_xor_b32 v248, 64, v240\n"
"v_mfma_f32_16x16x32_bf16 v[116:119], a[200:203], a[136:139], v[116:119]\n"
"s_waitcnt vmcnt(11)\n"
"s_load_dword s80, s[78:79], 0\n"
"v_mfma_f32_16x16x32_bf16 v[116:119], a[204:207], a[140:143], v[116:119]\n"
"s_barrier\n"
"v_mfma_f32_16x16x32_bf16 v[120:123], a[208:211], a[136:139], v[120:123]\n"
"s_add_u32 s40, s48, 256\n"
"s_xor_b32 m0, s50, 0xc000\n"
"ds_read_b64_tr_b16 a[238:239], v245 offset:25088\n"
"v_mfma_f32_16x16x32_bf16 v[120:123], a[212:215], a[140:143], v[120:123]\n"
"buffer_load_dwordx4 v224, s[28:31], s40 offen lds\n"
"v_mfma_f32_16x16x32_bf16 v[124:127], a[216:219], a[136:139], v[124:127]\n"
"v_mfma_f32_16x16x32_bf16 v[124:127], a[220:223], a[140:143], v[124:127]\n"
"v_mfma_f32_16x16x32_bf16 v[140:143], a[216:219], a[144:147], v[140:143]\n"
"ds_read_b64_tr_b16 a[244:245], v244 offset:25600\n"
"v_mfma_f32_16x16x32_bf16 v[140:143], a[220:223], a[148:151], v[140:143]\n"
"v_mfma_f32_16x16x32_bf16 v[136:139], a[208:211], a[144:147], v[136:139]\n"
"ds_read_b64_tr_b16 a[250:251], v245 offset:17920\n"
"v_bfe_i32 v241, v242, 4, 1\n"
"v_mfma_f32_16x16x32_bf16 v[136:139], a[212:215], a[148:151], v[136:139]\n"
"buffer_load_dwordx4 v225, s[28:31], s40 offen offset:1024 lds\n"
"v_mfma_f32_16x16x32_bf16 v[132:135], a[200:203], a[144:147], v[132:135]\n"
"s_add_u32 s82, s82, s80\n"
"v_mfma_f32_16x16x32_bf16 v[132:135], a[204:207], a[148:151], v[132:135]\n"
"s_add_u32 s82, s82, s80\n"
"v_mfma_f32_16x16x32_bf16 v[128:131], a[192:195], a[144:147], v[128:131]\n"
"s_add_u32 s82, s82, s80\n"
"ds_read_b64_tr_b16 a[230:231], v244 offset:25088\n"
"v_mfma_f32_16x16x32_bf16 v[128:131], a[196:199], a[148:151], v[128:131]\n"
"s_add_u32 s82, s82, s80\n"
"v_bfe_u32 v228, v242, 8, 2\n"
"v_mfma_f32_16x16x32_bf16 v[144:147], a[192:195], a[152:155], v[144:147]\n"
"s_add_u32 s82, s82, s80\n"
"ds_read_b64_tr_b16 a[242:243], v244 offset:17920\n"
"v_mfma_f32_16x16x32_bf16 v[144:147], a[196:199], a[156:159], v[144:147]\n"
"s_add_u32 s82, s82, s80\n"
"v_mfma_f32_16x16x32_bf16 v[148:151], a[200:203], a[152:155], v[148:151]\n"
"s_add_u32 s82, s82, s80\n"
"ds_read_b64_tr_b16 a[228:229], v244 offset:24576\n"
"v_mfma_f32_16x16x32_bf16 v[148:151], a[204:207], a[156:159], v[148:151]\n"
"s_add_u32 s82, s82, s80\n"
"buffer_load_dwordx4 v226, s[28:31], s40 offen offset:2048 lds\n"
"v_mfma_f32_16x16x32_bf16 v[152:155], a[208:211], a[152:155], v[152:155]\n"
"s_add_u32 s82, s82, s80\n"
"v_mfma_f32_16x16x32_bf16 v[152:155], a[212:215], a[156:159], v[152:155]\n"
"s_add_u32 s82, s82, s80\n"
"v_and_b32 v229, 0x60, v242\n"
"v_mfma_f32_16x16x32_bf16 v[156:159], a[216:219], a[152:155], v[156:159]\n"
"s_add_u32 s82, s82, s80\n"
"ds_read_b64_tr_b16 a[232:233], v245 offset:16384\n"
"v_mfma_f32_16x16x32_bf16 v[156:159], a[220:223], a[156:159], v[156:159]\n"
"s_add_u32 s82, s82, s80\n"
"v_mfma_f32_16x16x32_bf16 v[172:175], a[216:219], a[160:163], v[172:175]\n"
"s_add_u32 s82, s82, s80\n"
"ds_read_b64_tr_b16 a[234:235], v245 offset:16896\n"
"v_lshlrev_b32 v228, 4, v228\n"
"v_mfma_f32_16x16x32_bf16 v[172:175], a[220:223], a[164:167], v[172:175]\n"
"s_add_u32 s82, s82, s80\n"
"buffer_load_dwordx4 v227, s[28:31], s40 offen offset:3072 lds\n"
"v_mfma_f32_16x16x32_bf16 v[168:171], a[208:211], a[160:163], v[168:171]\n"
"s_add_u32 s82, s82, s80\n"
"s_mov_b32 s58, 122880\n"
"v_mfma_f32_16x16x32_bf16 v[168:171], a[212:215], a[164:167], v[168:171]\n"
"s_add_u32 s82, s82, s80\n"
"s_mov_b32 s47, 0\n"
"v_mfma_f32_16x16x32_bf16 v[164:167], a[200:203], a[160:163], v[164:167]\n"
"s_add_u32 s82, s82, s80\n"
"s_sub_u32 s52, s54, 14336\n"
"ds_read_b64_tr_b16 a[246:247], v244 offset:26112\n"
"v_mfma_f32_16x16x32_bf16 v[164:167], a[204:207], a[164:167], v[164:167]\n"
"s_load_dword s81, s[78:79], 0x2000\n"
"s_mov_b32 s53, 14336\n"
"v_mfma_f32_16x16x32_bf16 v[160:163], a[192:195], a[160:163], v[160:163]\n"
"s_mov_b32 m0, s44\n"
"ds_read_b64_tr_b16 a[240:241], v244 offset:17408\n"
"v_mfma_f32_16x16x32_bf16 v[160:163], a[196:199], a[164:167], v[160:163]\n"
"buffer_load_dwordx4 v240, s[16:19], s56 offen nt lds\n"
"v_mfma_f32_16x16x32_bf16 v[176:179], a[192:195], a[168:171], v[176:179]\n"
"v_mfma_f32_16x16x32_bf16 v[176:179], a[196:199], a[172:175], v[176:179]\n"
"v_mfma_f32_16x16x32_bf16 v[180:183], a[200:203], a[168:171], v[180:183]\n"
"ds_read_b64_tr_b16 a[252:253], v245 offset:25600\n"
"v_mfma_f32_16x16x32_bf16 v[180:183], a[204:207], a[172:175], v[180:183]\n"
"v_mfma_f32_16x16x32_bf16 v[184:187], a[208:211], a[168:171], v[184:187]\n"
"ds_read_b64_tr_b16 a[236:237], v245 offset:24576\n"
"v_xor_b32 v228, v228, v229\n"
"v_mfma_f32_16x16x32_bf16 v[184:187], a[212:215], a[172:175], v[184:187]\n"
"v_mfma_f32_16x16x32_bf16 v[188:191], a[216:219], a[168:171], v[188:191]\n"
"s_add_u32 s40, s56, s52\n"
"ds_read_b64_tr_b16 a[226:227], v244 offset:16896\n"
"v_mfma_f32_16x16x32_bf16 v[188:191], a[220:223], a[172:175], v[188:191]\n"
"s_sub_u32 s82, s82, s81\n"
"buffer_load_dwordx4 v248, s[16:19], s40 offen offset:1024 nt lds\n"
"v_mfma_f32_16x16x32_bf16 v[204:207], a[216:219], a[176:179], v[204:207]\n"
"s_sub_u32 s82, s82, s81\n"
"v_mfma_f32_16x16x32_bf16 v[204:207], a[220:223], a[180:183], v[204:207]\n"
"s_sub_u32 s82, s82, s81\n"
"v_mfma_f32_16x16x32_bf16 v[200:203], a[208:211], a[176:179], v[200:203]\n"
"s_sub_u32 s82, s82, s81\n"
"ds_read_b64_tr_b16 a[248:249], v245 offset:17408\n"
"v_and_b32 v229, 0x30, v242\n"
"v_mfma_f32_16x16x32_bf16 v[200:203], a[212:215], a[180:183], v[200:203]\n"
"s_sub_u32 s82, s82, s81\n"
"v_mfma_f32_16x16x32_bf16 v[196:199], a[200:203], a[176:179], v[196:199]\n"
"s_sub_u32 s82, s82, s81\n"
"s_add_u32 s40, s56, s53\n"
"ds_read_b64_tr_b16 a[224:225], v244 offset:16384\n"
"v_mfma_f32_16x16x32_bf16 v[196:199], a[204:207], a[180:183], v[196:199]\n"
"s_sub_u32 s82, s82, s81\n"
"buffer_load_dwordx4 v240, s[16:19], s40 offen offset:2048 nt lds\n"
"v_mfma_f32_16x16x32_bf16 v[192:195], a[192:195], a[176:179], v[192:195]\n"
"s_sub_u32 s82, s82, s81\n"
"v_lshl_add_u32 v228, v229, 3, v228\n"
"v_mfma_f32_16x16x32_bf16 v[192:195], a[196:199], a[180:183], v[192:195]\n"
"s_waitcnt lgkmcnt(2)\n"
"s_sub_u32 s82, s82, s81\n"
"v_xor_b32 v244, s74, v244\n"
"v_mfma_f32_16x16x32_bf16 v[208:211], a[192:195], a[184:187], v[208:211]\n"
"s_sub_u32 s82, s82, s81\n"
"v_mfma_f32_16x16x32_bf16 v[208:211], a[196:199], a[188:191], v[208:211]\n"
"s_sub_u32 s82, s82, s81\n"
"ds_read_b64_tr_b16 a[254:255], v245 offset:26112\n"
"v_mfma_f32_16x16x32_bf16 v[212:215], a[200:203], a[184:187], v[212:215]\n"
"s_sub_u32 s82, s82, s81\n"
"s_add_u32 s40, s56, s54\n"
"v_mfma_f32_16x16x32_bf16 v[212:215], a[204:207], a[188:191], v[212:215]\n"
"s_sub_u32 s82, s82, s81\n"
"buffer_load_dwordx4 v248, s[16:19], s40 offen offset:3072 nt lds\n"
"v_mfma_f32_16x16x32_bf16 v[216:219], a[208:211], a[184:187], v[216:219]\n"
"s_sub_u32 s82, s82, s81\n"
"v_and_b32 v229, 0xc0, v242\n"
"v_xor_b32 v251, s74, v251\n"
"v_mfma_f32_16x16x32_bf16 v[216:219], a[212:215], a[188:191], v[216:219]\n"
"s_sub_u32 s82, s82, s81\n"
"v_mfma_f32_16x16x32_bf16 v[220:223], a[216:219], a[184:187], v[220:223]\n"
"s_sub_u32 s82, s82, s81\n"
"v_mfma_f32_16x16x32_bf16 v[220:223], a[220:223], a[188:191], v[220:223]\n"
"s_waitcnt lgkmcnt(1)\n"
"s_sub_u32 s82, s82, s81\n"
"v_mfma_f32_16x16x32_bf16 v[0:3], a[224:227], a[136:139], v[0:3]\n"
"v_mfma_f32_16x16x32_bf16 v[0:3], a[228:231], a[140:143], v[0:3]\n"
"v_lshl_add_u32 v228, v229, 4, v228\n"
"v_xor_b32 v245, s74, v245\n"
"v_mfma_f32_16x16x32_bf16 v[4:7], a[232:235], a[136:139], v[4:7]\n"
"s_waitcnt vmcnt(8) lgkmcnt(0)\n"
"v_mfma_f32_16x16x32_bf16 v[4:7], a[236:239], a[140:143], v[4:7]\n"
"s_barrier\n"
"v_mfma_f32_16x16x32_bf16 v[8:11], a[240:243], a[136:139], v[8:11]\n"
"s_add_u32 m0, s44, 65536\n"
"ds_read_b64_tr_b16 a[222:223], v245 offset:9728\n"
"v_mfma_f32_16x16x32_bf16 v[8:11], a[244:247], a[140:143], v[8:11]\n"
"buffer_load_dwordx4 v240, s[20:23], s56 offen nt lds\n"
"v_mfma_f32_16x16x32_bf16 v[12:15], a[248:251], a[136:139], v[12:15]\n"
"s_lshr_b32 s82, s82, 31\n"
"v_mfma_f32_16x16x32_bf16 v[12:15], a[252:255], a[140:143], v[12:15]\n"
"s_add_u32 s82, s82, s60\n"
"v_mfma_f32_16x16x32_bf16 v[28:31], a[248:251], a[144:147], v[28:31]\n"
"s_add_u32 s82, s82, 32\n"
"ds_read_b64_tr_b16 a[216:217], v245 offset:1024\n"
"v_xor_b32 v247, s74, v247\n"
"v_mfma_f32_16x16x32_bf16 v[28:31], a[252:255], a[148:151], v[28:31]\n"
"v_mov_b32 v254, s82\n"
"ds_read_b64_tr_b16 a[196:197], v244 offset:8192\n"
"v_mfma_f32_16x16x32_bf16 v[24:27], a[240:243], a[144:147], v[24:27]\n"
"v_lshlrev_b32_e64 v255, 2, s93\n"
"ds_read_b64_tr_b16 a[198:199], v244 offset:8704\n"
"v_xor_b32 v246, s74, v246\n"
"v_mfma_f32_16x16x32_bf16 v[24:27], a[244:247], a[148:151], v[24:27]\n"
"ds_read_b64_tr_b16 a[214:215], v244 offset:9728\n"
"v_mov_b32 v229, s43\n"
"v_mfma_f32_16x16x32_bf16 v[20:23], a[232:235], a[144:147], v[20:23]\n"
"s_add_u32 s40, s56, s52\n"
"ds_read_b64_tr_b16 a[204:205], v245 offset:8192\n"
"v_mfma_f32_16x16x32_bf16 v[20:23], a[236:239], a[148:151], v[20:23]\n"
"buffer_load_dwordx4 v248, s[20:23], s40 offen offset:1024 nt lds\n"
"v_mfma_f32_16x16x32_bf16 v[16:19], a[224:227], a[144:147], v[16:19]\n"
"v_mfma_f32_16x16x32_bf16 v[16:19], a[228:231], a[148:151], v[16:19]\n"
"s_mov_b64 exec, 1\n"
"global_store_dword v255, v254, s[6:7] offset:256 sc1\n"
"s_mov_b64 exec, -1\n"
"v_mfma_f32_16x16x32_bf16 v[32:35], a[224:227], a[152:155], v[32:35]\n"
"ds_read_b64_tr_b16 a[202:203], v245 offset:512\n"
"v_mfma_f32_16x16x32_bf16 v[32:35], a[228:231], a[156:159], v[32:35]\n"
"v_mfma_f32_16x16x32_bf16 v[36:39], a[232:235], a[152:155], v[36:39]\n"
"s_add_u32 s40, s56, s53\n"
"ds_read_b128 a[140:143], v247 offset:0\n"
"v_mfma_f32_16x16x32_bf16 v[36:39], a[236:239], a[156:159], v[36:39]\n"
"buffer_load_dwordx4 v240, s[20:23], s40 offen offset:2048 nt lds\n"
"v_mfma_f32_16x16x32_bf16 v[40:43], a[240:243], a[152:155], v[40:43]\n"
"v_xor_b32 v250, s74, v250\n"
"v_mfma_f32_16x16x32_bf16 v[40:43], a[244:247], a[156:159], v[40:43]\n"
"v_mfma_f32_16x16x32_bf16 v[44:47], a[248:251], a[152:155], v[44:47]\n"
"ds_read_b64_tr_b16 a[200:201], v245 offset:0\n"
"v_mul_u32_u24 v229, 0x81, v229\n"
"v_mfma_f32_16x16x32_bf16 v[44:47], a[252:255], a[156:159], v[44:47]\n"
"ds_read_b64_tr_b16 a[220:221], v245 offset:9216\n"
"v_mfma_f32_16x16x32_bf16 v[60:63], a[248:251], a[160:163], v[60:63]\n"
"s_add_u32 s40, s56, s54\n"
"ds_read_b64_tr_b16 a[210:211], v244 offset:1536\n"
"v_xor_b32 v253, s74, v253\n"
"v_mfma_f32_16x16x32_bf16 v[60:63], a[252:255], a[164:167], v[60:63]\n"
"buffer_load_dwordx4 v248, s[20:23], s40 offen offset:3072 nt lds\n"
"v_mfma_f32_16x16x32_bf16 v[56:59], a[240:243], a[160:163], v[56:59]\n"
"s_add_u32 s55, s55, s59\n"
"v_mfma_f32_16x16x32_bf16 v[56:59], a[244:247], a[164:167], v[56:59]\n"
"s_or_b32 s55, s55, s58\n"
"v_mfma_f32_16x16x32_bf16 v[52:55], a[232:235], a[160:163], v[52:55]\n"
"s_xor_b32 s56, s55, s57\n"
"ds_read_b128 a[136:139], v246 offset:0\n"
"v_mfma_f32_16x16x32_bf16 v[52:55], a[236:239], a[164:167], v[52:55]\n"
"v_mfma_f32_16x16x32_bf16 v[48:51], a[224:227], a[160:163], v[48:51]\n"
"s_add_u32 m0, s44, 16384\n"
"ds_read_b128 a[156:159], v247 offset:4096\n"
"v_mfma_f32_16x16x32_bf16 v[48:51], a[228:231], a[164:167], v[48:51]\n"
"buffer_load_dwordx4 v240, s[16:19], s56 offen nt lds\n"
"v_mfma_f32_16x16x32_bf16 v[64:67], a[224:227], a[168:171], v[64:67]\n"
"v_mfma_f32_16x16x32_bf16 v[64:67], a[228:231], a[172:175], v[64:67]\n"
"v_mfma_f32_16x16x32_bf16 v[68:71], a[232:235], a[168:171], v[68:71]\n"
"ds_read_b128 a[164:167], v253 offset:0\n"
"v_mfma_f32_16x16x32_bf16 v[68:71], a[236:239], a[172:175], v[68:71]\n"
"v_mfma_f32_16x16x32_bf16 v[72:75], a[240:243], a[168:171], v[72:75]\n"
"ds_read_b64_tr_b16 a[218:219], v245 offset:1536\n"
"v_mfma_f32_16x16x32_bf16 v[72:75], a[244:247], a[172:175], v[72:75]\n"
"ds_read_b64_tr_b16 a[194:195], v244 offset:512\n"
"v_mfma_f32_16x16x32_bf16 v[76:79], a[248:251], a[168:171], v[76:79]\n"
"s_add_u32 s40, s56, s52\n"
"ds_read_b64_tr_b16 a[206:207], v245 offset:8704\n"
"v_mfma_f32_16x16x32_bf16 v[76:79], a[252:255], a[172:175], v[76:79]\n"
"buffer_load_dwordx4 v248, s[16:19], s40 offen offset:1024 nt lds\n"
"v_mfma_f32_16x16x32_bf16 v[92:95], a[248:251], a[176:179], v[92:95]\n"
"v_mfma_f32_16x16x32_bf16 v[92:95], a[252:255], a[180:183], v[92:95]\n"
"v_mfma_f32_16x16x32_bf16 v[88:91], a[240:243], a[176:179], v[88:91]\n"
"ds_read_b64_tr_b16 a[208:209], v244 offset:1024\n"
"v_mfma_f32_16x16x32_bf16 v[88:91], a[244:247], a[180:183], v[88:91]\n"
"ds_read_b64_tr_b16 a[212:213], v244 offset:9216\n"
"v_mfma_f32_16x16x32_bf16 v[84:87], a[232:235], a[176:179], v[84:87]\n"
"s_add_u32 s40, s56, s53\n"
"ds_read_b64_tr_b16 a[192:193], v244 offset:0\n"
"v_mfma_f32_16x16x32_bf16 v[84:87], a[236:239], a[180:183], v[84:87]\n"
"buffer_load_dwordx4 v240, s[16:19], s40 offen offset:2048 nt lds\n"
"v_mfma_f32_16x16x32_bf16 v[80:83], a[224:227], a[176:179], v[80:83]\n"
"v_mfma_f32_16x16x32_bf16 v[80:83], a[228:231], a[180:183], v[80:83]\n"
"v_mfma_f32_16x16x32_bf16 v[96:99], a[224:227], a[184:187], v[96:99]\n"
"ds_read_b128 a[172:175], v251 offset:0\n"
"v_mfma_f32_16x16x32_bf16 v[96:99], a[228:231], a[188:191], v[96:99]\n"
"v_mfma_f32_16x16x32_bf16 v[100:103], a[232:235], a[184:187], v[100:103]\n"
"s_add_u32 s40, s56, s54\n"
"ds_read_b128 a[148:151], v247 offset:2048\n"
"v_mfma_f32_16x16x32_bf16 v[100:103], a[236:239], a[188:191], v[100:103]\n"
"buffer_load_dwordx4 v248, s[16:19], s40 offen offset:3072 nt lds\n"
"v_mfma_f32_16x16x32_bf16 v[104:107], a[240:243], a[184:187], v[104:107]\n"
"v_mfma_f32_16x16x32_bf16 v[104:107], a[244:247], a[188:191], v[104:107]\n"
"v_and_b32 v254, 0x20, v242\n"
"v_and_b32 v241, 0xaaaaaaaa, v241\n"
"v_mfma_f32_16x16x32_bf16 v[108:111], a[248:251], a[184:187], v[108:111]\n"
"ds_read_b128 a[168:171], v250 offset:0\n"
"v_mfma_f32_16x16x32_bf16 v[108:111], a[252:255], a[188:191], v[108:111]\n"
"s_waitcnt lgkmcnt(3)\n"
"v_mfma_f32_16x16x32_bf16 v[112:115], a[192:195], a[136:139], v[112:115]\n"
"ds_read_b128 a[184:187], v250 offset:4096\n"
"v_mfma_f32_16x16x32_bf16 v[112:115], a[196:199], a[140:143], v[112:115]\n"
"v_mfma_f32_16x16x32_bf16 v[116:119], a[200:203], a[136:139], v[116:119]\n"
"s_waitcnt vmcnt(13)\n"
"ds_read_b128 a[180:183], v251 offset:2048\n"
"v_and_b32 v249, 0xf0, v242\n"
"v_mfma_f32_16x16x32_bf16 v[116:119], a[204:207], a[140:143], v[116:119]\n"
"s_barrier\n"
"v_mfma_f32_16x16x32_bf16 v[120:123], a[208:211], a[136:139], v[120:123]\n"
"s_add_u32 m0, s44, 81920\n"
"ds_read_b128 a[176:179], v250 offset:2048\n"
"v_mfma_f32_16x16x32_bf16 v[120:123], a[212:215], a[140:143], v[120:123]\n"
"buffer_load_dwordx4 v240, s[20:23], s56 offen nt lds\n"
"v_mfma_f32_16x16x32_bf16 v[124:127], a[216:219], a[136:139], v[124:127]\n"
"v_bfe_u32 v255, v242, 9, 1\n"
"v_lshl_add_u32 v255, v255, 8, v249\n"
"v_mfma_f32_16x16x32_bf16 v[124:127], a[220:223], a[140:143], v[124:127]\n"
"s_waitcnt lgkmcnt(3)\n"
"v_xor_b32 v252, s74, v252\n"
"v_mfma_f32_16x16x32_bf16 v[188:191], a[216:219], a[168:171], v[188:191]\n"
"ds_read_b128 a[188:191], v251 offset:4096\n"
"v_xor_b32 v228, v228, v229\n"
"v_mfma_f32_16x16x32_bf16 v[188:191], a[220:223], a[172:175], v[188:191]\n"
"v_mfma_f32_16x16x32_bf16 v[184:187], a[208:211], a[168:171], v[184:187]\n"
"ds_read_b128 a[144:147], v246 offset:2048\n"
"v_add_u32 v254, -1, v254\n"
"v_mfma_f32_16x16x32_bf16 v[184:187], a[212:215], a[172:175], v[184:187]\n"
"v_mfma_f32_16x16x32_bf16 v[180:183], a[200:203], a[168:171], v[180:183]\n"
"ds_read_b64_tr_b16 a[234:235], v245 offset:16896\n"
"v_mfma_f32_16x16x32_bf16 v[180:183], a[204:207], a[172:175], v[180:183]\n"
"v_mfma_f32_16x16x32_bf16 v[176:179], a[192:195], a[168:171], v[176:179]\n"
"ds_read_b128 a[160:163], v252 offset:0\n"
"v_mfma_f32_16x16x32_bf16 v[176:179], a[196:199], a[172:175], v[176:179]\n"
"s_waitcnt lgkmcnt(4)\n"
"v_mfma_f32_16x16x32_bf16 v[192:195], a[192:195], a[176:179], v[192:195]\n"
"ds_read_b64_tr_b16 a[230:231], v244 offset:25088\n"
"v_or_b32 v241, 0x44444444, v241\n"
"v_mfma_f32_16x16x32_bf16 v[192:195], a[196:199], a[180:183], v[192:195]\n"
"v_bfe_u32 v249, v242, 8, 1\n"
"v_mfma_f32_16x16x32_bf16 v[196:199], a[200:203], a[176:179], v[196:199]\n"
"s_add_u32 s40, s56, s52\n"
"ds_read_b64_tr_b16 a[246:247], v244 offset:26112\n"
"v_mfma_f32_16x16x32_bf16 v[196:199], a[204:207], a[180:183], v[196:199]\n"
"buffer_load_dwordx4 v248, s[20:23], s40 offen offset:1024 nt lds\n"
"v_mfma_f32_16x16x32_bf16 v[200:203], a[208:211], a[176:179], v[200:203]\n"
"v_xor_b32 v229, 64, v228\n"
"v_mfma_f32_16x16x32_bf16 v[200:203], a[212:215], a[180:183], v[200:203]\n"
"v_mfma_f32_16x16x32_bf16 v[204:207], a[216:219], a[176:179], v[204:207]\n"
"ds_read_b64_tr_b16 a[240:241], v244 offset:17408\n"
"v_lshl_add_u32 v255, v249, 3, v255\n"
"v_mfma_f32_16x16x32_bf16 v[204:207], a[220:223], a[180:183], v[204:207]\n"
"v_xor_b32 v231, 0x2000, v229\n"
"v_xor_b32_e64 v249, s42, 1\n"
"v_mfma_f32_16x16x32_bf16 v[220:223], a[216:219], a[184:187], v[220:223]\n"
"s_waitcnt lgkmcnt(2)\n"
"ds_read_b64_tr_b16 a[232:233], v245 offset:16384\n"
"v_mfma_f32_16x16x32_bf16 v[220:223], a[220:223], a[188:191], v[220:223]\n"
"ds_read_b64_tr_b16 a[226:227], v244 offset:16896\n"
"v_mfma_f32_16x16x32_bf16 v[216:219], a[208:211], a[184:187], v[216:219]\n"
"ds_read_b64_tr_b16 a[224:225], v244 offset:16384\n"
"v_xor_b32 v230, 0x2000, v228\n"
"v_mfma_f32_16x16x32_bf16 v[216:219], a[212:215], a[188:191], v[216:219]\n"
"v_pk_fma_f32 v[176:177], v[176:177], s[70:71], s[70:71] op_sel:[0,0,1] op_sel_hi:[1,0,1]\nv_pk_fma_f32 v[178:179], v[178:179], s[70:71], s[70:71] op_sel:[0,0,1] op_sel_hi:[1,0,1]\nv_pk_fma_f32 v[180:181], v[180:181], s[70:71], s[70:71] op_sel:[0,0,1] op_sel_hi:[1,0,1]\nv_pk_fma_f32 v[182:183], v[182:183], s[70:71], s[70:71] op_sel:[0,0,1] op_sel_hi:[1,0,1]\nv_pk_fma_f32 v[184:185], v[184:185], s[70:71], s[70:71] op_sel:[0,0,1] op_sel_hi:[1,0,1]\nv_pk_fma_f32 v[186:187], v[186:187], s[70:71], s[70:71] op_sel:[0,0,1] op_sel_hi:[1,0,1]\nv_pk_fma_f32 v[188:189], v[188:189], s[70:71], s[70:71] op_sel:[0,0,1] op_sel_hi:[1,0,1]\nv_pk_fma_f32 v[190:191], v[190:191], s[70:71], s[70:71] op_sel:[0,0,1] op_sel_hi:[1,0,1]\nv_pk_fma_f32 v[192:193], v[192:193], s[70:71], s[70:71] op_sel:[0,0,1] op_sel_hi:[1,0,1]\nv_pk_fma_f32 v[194:195], v[194:195], s[70:71], s[70:71] op_sel:[0,0,1] op_sel_hi:[1,0,1]\nv_pk_fma_f32 v[196:197], v[196:197], s[70:71], s[70:71] op_sel:[0,0,1] op_sel_hi:[1,0,1]\nv_pk_fma_f32 v[198:199], v[198:199], s[70:71], s[70:71] op_sel:[0,0,1] op_sel_hi:[1,0,1]\nv_pk_fma_f32 v[200:201], v[200:201], s[70:71], s[70:71] op_sel:[0,0,1] op_sel_hi:[1,0,1]\nv_pk_fma_f32 v[202:203], v[202:203], s[70:71], s[70:71] op_sel:[0,0,1] op_sel_hi:[1,0,1]\nv_pk_fma_f32 v[204:205], v[204:205], s[70:71], s[70:71] op_sel:[0,0,1] op_sel_hi:[1,0,1]\nv_pk_fma_f32 v[206:207], v[206:207], s[70:71], s[70:71] op_sel:[0,0,1] op_sel_hi:[1,0,1]\n"
"v_mfma_f32_16x16x32_bf16 v[212:215], a[200:203], a[184:187], v[212:215]\n"
"ds_read_b64_tr_b16 a[252:253], v245 offset:25600\n"
"v_cvt_pk_bf16_f32 v192, v192, v193\n"
"v_mfma_f32_16x16x32_bf16 v[212:215], a[204:207], a[188:191], v[212:215]\n"
"v_cvt_pk_bf16_f32 v176, v176, v177\n"
"v_cvt_pk_bf16_f32 v193, v194, v195\n"
"v_mfma_f32_16x16x32_bf16 v[208:211], a[192:195], a[184:187], v[208:211]\n"
"s_add_u32 s40, s56, s53\n"
"ds_read_b64_tr_b16 a[248:249], v245 offset:17408\n"
"v_mfma_f32_16x16x32_bf16 v[208:211], a[196:199], a[188:191], v[208:211]\n"
"buffer_load_dwordx4 v240, s[20:23], s40 offen offset:2048 nt lds\n"
"v_mfma_f32_16x16x32_bf16 v[160:163], a[192:195], a[160:163], v[160:163]\n"
"v_cvt_pk_bf16_f32 v177, v178, v179\n"
"v_cvt_pk_bf16_f32 v184, v184, v185\n"
"v_mfma_f32_16x16x32_bf16 v[160:163], a[196:199], a[164:167], v[160:163]\n"
"v_cvt_pk_bf16_f32 v179, v182, v183\n"
"v_cvt_pk_bf16_f32 v185, v186, v187\n"
"v_mfma_f32_16x16x32_bf16 v[164:167], a[200:203], a[160:163], v[164:167]\n"
"ds_read_b64_tr_b16 a[244:245], v244 offset:25600\n"
"v_cvt_pk_bf16_f32 v200, v200, v201\n"
"v_mfma_f32_16x16x32_bf16 v[164:167], a[204:207], a[164:167], v[164:167]\n"
"v_cvt_pk_bf16_f32 v201, v202, v203\n"
"v_ashrrev_i32 v254, 31, v254\n"
"v_mfma_f32_16x16x32_bf16 v[168:171], a[208:211], a[160:163], v[168:171]\n"
"ds_read_b64_tr_b16 a[254:255], v245 offset:26112\n"
"v_cvt_pk_bf16_f32 v194, v196, v197\n"
"v_mfma_f32_16x16x32_bf16 v[168:171], a[212:215], a[164:167], v[168:171]\n"
"v_cvt_pk_bf16_f32 v202, v204, v205\n"
"v_mfma_f32_16x16x32_bf16 v[172:175], a[216:219], a[160:163], v[172:175]\n"
"ds_read_b64_tr_b16 a[242:243], v244 offset:17920\n"
"v_and_b32 v254, 0x3f803f80, v254\n"
"v_mfma_f32_16x16x32_bf16 v[172:175], a[220:223], a[164:167], v[172:175]\n"
"v_bfe_u32 v196, v242, 8, 2\n"
"v_lshl_add_u32 v196, v196, 4, s43\n"
"v_mfma_f32_16x16x32_bf16 v[140:143], a[216:219], a[144:147], v[140:143]\n"
"ds_read_b64_tr_b16 a[236:237], v245 offset:24576\n"
"v_lshl_add_u32 v255, v249, 12, v255\n"
"v_mfma_f32_16x16x32_bf16 v[140:143], a[220:223], a[148:151], v[140:143]\n"
"v_cvt_pk_bf16_f32 v203, v206, v207\n"
"v_mfma_f32_16x16x32_bf16 v[136:139], a[208:211], a[144:147], v[136:139]\n"
"ds_read_b128 a[152:155], v246 offset:4096\n"
"v_cvt_pk_bf16_f32 v178, v180, v181\n"
"v_mfma_f32_16x16x32_bf16 v[136:139], a[212:215], a[148:151], v[136:139]\n"
"v_pk_fma_f32 v[208:209], v[208:209], s[70:71], s[70:71] op_sel:[0,0,1] op_sel_hi:[1,0,1]\nv_pk_fma_f32 v[210:211], v[210:211], s[70:71], s[70:71] op_sel:[0,0,1] op_sel_hi:[1,0,1]\nv_pk_fma_f32 v[212:213], v[212:213], s[70:71], s[70:71] op_sel:[0,0,1] op_sel_hi:[1,0,1]\nv_pk_fma_f32 v[214:215], v[214:215], s[70:71], s[70:71] op_sel:[0,0,1] op_sel_hi:[1,0,1]\nv_pk_fma_f32 v[216:217], v[216:217], s[70:71], s[70:71] op_sel:[0,0,1] op_sel_hi:[1,0,1]\nv_pk_fma_f32 v[218:219], v[218:219], s[70:71], s[70:71] op_sel:[0,0,1] op_sel_hi:[1,0,1]\nv_pk_fma_f32 v[220:221], v[220:221], s[70:71], s[70:71] op_sel:[0,0,1] op_sel_hi:[1,0,1]\nv_pk_fma_f32 v[222:223], v[222:223], s[70:71], s[70:71] op_sel:[0,0,1] op_sel_hi:[1,0,1]\nv_pk_fma_f32 v[160:161], v[160:161], s[70:71], s[70:71] op_sel:[0,0,1] op_sel_hi:[1,0,1]\nv_pk_fma_f32 v[162:163], v[162:163], s[70:71], s[70:71] op_sel:[0,0,1] op_sel_hi:[1,0,1]\nv_pk_fma_f32 v[164:165], v[164:165], s[70:71], s[70:71] op_sel:[0,0,1] op_sel_hi:[1,0,1]\nv_pk_fma_f32 v[166:167], v[166:167], s[70:71], s[70:71] op_sel:[0,0,1] op_sel_hi:[1,0,1]\nv_pk_fma_f32 v[168:169], v[168:169], s[70:71], s[70:71] op_sel:[0,0,1] op_sel_hi:[1,0,1]\nv_pk_fma_f32 v[170:171], v[170:171], s[70:71], s[70:71] op_sel:[0,0,1] op_sel_hi:[1,0,1]\nv_pk_fma_f32 v[172:173], v[172:173], s[70:71], s[70:71] op_sel:[0,0,1] op_sel_hi:[1,0,1]\nv_pk_fma_f32 v[174:175], v[174:175], s[70:71], s[70:71] op_sel:[0,0,1] op_sel_hi:[1,0,1]\n"
"v_mfma_f32_16x16x32_bf16 v[132:135], a[200:203], a[144:147], v[132:135]\n"
"s_add_u32 s40, s56, s54\n"
"ds_read_b64_tr_b16 a[250:251], v245 offset:17920\n"
"v_cvt_pk_bf16_f32 v160, v160, v161\n"
"v_mfma_f32_16x16x32_bf16 v[132:135], a[204:207], a[148:151], v[132:135]\n"
"buffer_load_dwordx4 v248, s[20:23], s40 offen offset:3072 nt lds\n"
"v_mfma_f32_16x16x32_bf16 v[128:131], a[192:195], a[144:147], v[128:131]\n"
"s_add_u32 s55, s55, s59\n"
"v_cvt_pk_bf16_f32 v186, v188, v189\n"
"v_cvt_pk_bf16_f32 v216, v216, v217\n"
"v_mfma_f32_16x16x32_bf16 v[128:131], a[196:199], a[148:151], v[128:131]\n"
"s_waitcnt lgkmcnt(1)\n"
"s_or_b32 s55, s55, s58\n"
"v_cvt_pk_bf16_f32 v208, v208, v209\n"
"v_cvt_pk_bf16_f32 v161, v162, v163\n"
"v_mfma_f32_16x16x32_bf16 v[144:147], a[192:195], a[152:155], v[144:147]\n"
"s_xor_b32 s56, s55, s57\n"
"ds_read_b64_tr_b16 a[238:239], v245 offset:25088\n"
"v_cvt_pk_bf16_f32 v187, v190, v191\n"
"v_mfma_f32_16x16x32_bf16 v[144:147], a[196:199], a[156:159], v[144:147]\n"
"v_cvt_pk_bf16_f32 v217, v218, v219\n"
"v_cvt_pk_bf16_f32 v168, v168, v169\n"
"v_mfma_f32_16x16x32_bf16 v[148:151], a[200:203], a[152:155], v[148:151]\n"
"v_cvt_pk_bf16_f32 v195, v198, v199\n"
"v_xor_b32 v198, 0x400, v243\n"
"v_mfma_f32_16x16x32_bf16 v[148:151], a[204:207], a[156:159], v[148:151]\n"
"ds_read_b64_tr_b16 a[228:229], v244 offset:24576\n"
"v_cvt_pk_bf16_f32 v169, v170, v171\n"
"v_mfma_f32_16x16x32_bf16 v[152:155], a[208:211], a[152:155], v[152:155]\n"
"v_cvt_pk_bf16_f32 v163, v166, v167\n"
"v_cvt_pk_bf16_f32 v171, v174, v175\n"
"v_mfma_f32_16x16x32_bf16 v[152:155], a[212:215], a[156:159], v[152:155]\n"
"v_pk_fma_f32 v[112:113], v[112:113], s[70:71], s[70:71] op_sel:[0,0,1] op_sel_hi:[1,0,1]\nv_pk_fma_f32 v[114:115], v[114:115], s[70:71], s[70:71] op_sel:[0,0,1] op_sel_hi:[1,0,1]\nv_pk_fma_f32 v[116:117], v[116:117], s[70:71], s[70:71] op_sel:[0,0,1] op_sel_hi:[1,0,1]\nv_pk_fma_f32 v[118:119], v[118:119], s[70:71], s[70:71] op_sel:[0,0,1] op_sel_hi:[1,0,1]\nv_pk_fma_f32 v[120:121], v[120:121], s[70:71], s[70:71] op_sel:[0,0,1] op_sel_hi:[1,0,1]\nv_pk_fma_f32 v[122:123], v[122:123], s[70:71], s[70:71] op_sel:[0,0,1] op_sel_hi:[1,0,1]\nv_pk_fma_f32 v[124:125], v[124:125], s[70:71], s[70:71] op_sel:[0,0,1] op_sel_hi:[1,0,1]\nv_pk_fma_f32 v[126:127], v[126:127], s[70:71], s[70:71] op_sel:[0,0,1] op_sel_hi:[1,0,1]\nv_pk_fma_f32 v[128:129], v[128:129], s[70:71], s[70:71] op_sel:[0,0,1] op_sel_hi:[1,0,1]\nv_pk_fma_f32 v[130:131], v[130:131], s[70:71], s[70:71] op_sel:[0,0,1] op_sel_hi:[1,0,1]\nv_pk_fma_f32 v[132:133], v[132:133], s[70:71], s[70:71] op_sel:[0,0,1] op_sel_hi:[1,0,1]\nv_pk_fma_f32 v[134:135], v[134:135], s[70:71], s[70:71] op_sel:[0,0,1] op_sel_hi:[1,0,1]\nv_pk_fma_f32 v[136:137], v[136:137], s[70:71], s[70:71] op_sel:[0,0,1] op_sel_hi:[1,0,1]\nv_pk_fma_f32 v[138:139], v[138:139], s[70:71], s[70:71] op_sel:[0,0,1] op_sel_hi:[1,0,1]\nv_pk_fma_f32 v[140:141], v[140:141], s[70:71], s[70:71] op_sel:[0,0,1] op_sel_hi:[1,0,1]\nv_pk_fma_f32 v[142:143], v[142:143], s[70:71], s[70:71] op_sel:[0,0,1] op_sel_hi:[1,0,1]\n"
"v_mfma_f32_16x16x32_bf16 v[156:159], a[216:219], a[152:155], v[156:159]\n"
"v_cvt_pk_bf16_f32 v136, v136, v137\n"
"v_cvt_pk_bf16_f32 v137, v138, v139\n"
"v_mfma_f32_16x16x32_bf16 v[156:159], a[220:223], a[156:159], v[156:159]\n"
"v_xor_b32 v197, 64, v196\n"
"v_cvt_pk_bf16_f32 v120, v120, v121\n"
"v_mfma_f32_16x16x32_bf16 v[96:99], a[224:227], a[184:187], v[96:99]\n"
"s_waitcnt lgkmcnt(0)\n"
"s_add_u32 m0, s44, 32768\n"
"v_cvt_pk_bf16_f32 v209, v210, v211\n"
"v_cvt_pk_bf16_f32 v210, v212, v213\n"
"v_mfma_f32_16x16x32_bf16 v[96:99], a[228:231], a[188:191], v[96:99]\n"
"buffer_load_dwordx4 v240, s[16:19], s56 offen nt lds\n"
"v_mfma_f32_16x16x32_bf16 v[100:103], a[232:235], a[184:187], v[100:103]\n"
"s_waitcnt vmcnt(14)\n"
"v_cvt_pk_bf16_f32 v128, v128, v129\n"
"v_cvt_pk_bf16_f32 v129, v130, v131\n"
"v_mfma_f32_16x16x32_bf16 v[100:103], a[236:239], a[188:191], v[100:103]\n"
"s_barrier\n"
"v_mfma_f32_16x16x32_bf16 v[104:107], a[240:243], a[184:187], v[104:107]\n"
"ds_read_b128 a[196:199], v198 offset:0\n"
"v_cvt_pk_bf16_f32 v211, v214, v215\n"
"v_mfma_f32_16x16x32_bf16 v[104:107], a[244:247], a[188:191], v[104:107]\n"
"s_mov_b32 s74, 0x41400000\n"
"v_cvt_pk_bf16_f32 v138, v140, v141\n"
"v_cvt_pk_bf16_f32 v139, v142, v143\n"
"v_mfma_f32_16x16x32_bf16 v[108:111], a[248:251], a[184:187], v[108:111]\n"
"ds_read_b128 a[200:203], v243 offset:2048\n"
"v_cvt_pk_bf16_f32 v162, v164, v165\n"
"v_mfma_f32_16x16x32_bf16 v[108:111], a[252:255], a[188:191], v[108:111]\n"
"v_pk_fma_f32 v[144:145], v[144:145], s[70:71], s[70:71] op_sel:[0,0,1] op_sel_hi:[1,0,1]\nv_pk_fma_f32 v[146:147], v[146:147], s[70:71], s[70:71] op_sel:[0,0,1] op_sel_hi:[1,0,1]\nv_pk_fma_f32 v[148:149], v[148:149], s[70:71], s[70:71] op_sel:[0,0,1] op_sel_hi:[1,0,1]\nv_pk_fma_f32 v[150:151], v[150:151], s[70:71], s[70:71] op_sel:[0,0,1] op_sel_hi:[1,0,1]\nv_pk_fma_f32 v[152:153], v[152:153], s[70:71], s[70:71] op_sel:[0,0,1] op_sel_hi:[1,0,1]\nv_pk_fma_f32 v[154:155], v[154:155], s[70:71], s[70:71] op_sel:[0,0,1] op_sel_hi:[1,0,1]\nv_pk_fma_f32 v[156:157], v[156:157], s[70:71], s[70:71] op_sel:[0,0,1] op_sel_hi:[1,0,1]\nv_pk_fma_f32 v[158:159], v[158:159], s[70:71], s[70:71] op_sel:[0,0,1] op_sel_hi:[1,0,1]\n"
"v_mfma_f32_16x16x32_bf16 v[92:95], a[248:251], a[176:179], v[92:95]\n"
"v_permlane16_swap_b32 v161, v163\n"
"v_mfma_f32_16x16x32_bf16 v[92:95], a[252:255], a[180:183], v[92:95]\n"
"ds_write2st64_b64 v255, v[200:201], v[202:203] offset0:102 offset1:103\n"
"v_mfma_f32_16x16x32_bf16 v[88:91], a[240:243], a[176:179], v[88:91]\n"
"v_cvt_pk_bf16_f32 v144, v144, v145\n"
"v_cvt_pk_bf16_f32 v152, v152, v153\n"
"v_mfma_f32_16x16x32_bf16 v[88:91], a[244:247], a[180:183], v[88:91]\n"
"v_cvt_pk_bf16_f32 v145, v146, v147\n"
"v_cvt_pk_bf16_f32 v153, v154, v155\n"
"v_mfma_f32_16x16x32_bf16 v[84:87], a[232:235], a[176:179], v[84:87]\n"
"v_cvt_pk_bf16_f32 v130, v132, v133\n"
"v_cvt_pk_bf16_f32 v131, v134, v135\n"
"v_mfma_f32_16x16x32_bf16 v[84:87], a[236:239], a[180:183], v[84:87]\n"
"ds_write2st64_b64 v255, v[176:177], v[178:179] offset0:96 offset1:97\n"
"v_mfma_f32_16x16x32_bf16 v[80:83], a[224:227], a[176:179], v[80:83]\n"
"s_add_u32 s40, s56, s52\n"
"v_permlane16_swap_b32 v160, v162\n"
"v_mfma_f32_16x16x32_bf16 v[80:83], a[228:231], a[180:183], v[80:83]\n"
"buffer_load_dwordx4 v248, s[16:19], s40 offen offset:1024 nt lds\n"
"v_mfma_f32_16x16x32_bf16 v[64:67], a[224:227], a[168:171], v[64:67]\n"
"v_cvt_pk_bf16_f32 v218, v220, v221\n"
"v_cvt_pk_bf16_f32 v154, v156, v157\n"
"v_mfma_f32_16x16x32_bf16 v[64:67], a[228:231], a[172:175], v[64:67]\n"
"v_permlane16_swap_b32 v128, v130\n"
"v_mfma_f32_16x16x32_bf16 v[68:71], a[232:235], a[168:171], v[68:71]\n"
"ds_read_b128 v[212:215], v197 offset:0\n"
"v_cvt_pk_bf16_f32 v155, v158, v159\n"
"v_mfma_f32_16x16x32_bf16 v[68:71], a[236:239], a[172:175], v[68:71]\n"
"v_pk_fma_f32 v[96:97], v[96:97], s[70:71], s[70:71] op_sel:[0,0,1] op_sel_hi:[1,0,1]\nv_pk_fma_f32 v[98:99], v[98:99], s[70:71], s[70:71] op_sel:[0,0,1] op_sel_hi:[1,0,1]\nv_pk_fma_f32 v[100:101], v[100:101], s[70:71], s[70:71] op_sel:[0,0,1] op_sel_hi:[1,0,1]\nv_pk_fma_f32 v[102:103], v[102:103], s[70:71], s[70:71] op_sel:[0,0,1] op_sel_hi:[1,0,1]\nv_pk_fma_f32 v[104:105], v[104:105], s[70:71], s[70:71] op_sel:[0,0,1] op_sel_hi:[1,0,1]\nv_pk_fma_f32 v[106:107], v[106:107], s[70:71], s[70:71] op_sel:[0,0,1] op_sel_hi:[1,0,1]\nv_pk_fma_f32 v[108:109], v[108:109], s[70:71], s[70:71] op_sel:[0,0,1] op_sel_hi:[1,0,1]\nv_pk_fma_f32 v[110:111], v[110:111], s[70:71], s[70:71] op_sel:[0,0,1] op_sel_hi:[1,0,1]\nv_pk_fma_f32 v[80:81], v[80:81], s[70:71], s[70:71] op_sel:[0,0,1] op_sel_hi:[1,0,1]\nv_pk_fma_f32 v[82:83], v[82:83], s[70:71], s[70:71] op_sel:[0,0,1] op_sel_hi:[1,0,1]\nv_pk_fma_f32 v[84:85], v[84:85], s[70:71], s[70:71] op_sel:[0,0,1] op_sel_hi:[1,0,1]\nv_pk_fma_f32 v[86:87], v[86:87], s[70:71], s[70:71] op_sel:[0,0,1] op_sel_hi:[1,0,1]\nv_pk_fma_f32 v[88:89], v[88:89], s[70:71], s[70:71] op_sel:[0,0,1] op_sel_hi:[1,0,1]\nv_pk_fma_f32 v[90:91], v[90:91], s[70:71], s[70:71] op_sel:[0,0,1] op_sel_hi:[1,0,1]\nv_pk_fma_f32 v[92:93], v[92:93], s[70:71], s[70:71] op_sel:[0,0,1] op_sel_hi:[1,0,1]\nv_pk_fma_f32 v[94:95], v[94:95], s[70:71], s[70:71] op_sel:[0,0,1] op_sel_hi:[1,0,1]\n"
"v_mfma_f32_16x16x32_bf16 v[72:75], a[240:243], a[168:171], v[72:75]\n"
"v_permlane16_swap_b32 v137, v139\n"
"v_mfma_f32_16x16x32_bf16 v[72:75], a[244:247], a[172:175], v[72:75]\n"
"ds_write2st64_b64 v255, v[184:185], v[186:187] offset0:98 offset1:99\n"
"v_mfma_f32_16x16x32_bf16 v[76:79], a[248:251], a[168:171], v[76:79]\n"
"v_cvt_pk_bf16_f32 v121, v122, v123\n"
"v_cvt_pk_bf16_f32 v96, v96, v97\n"
"v_mfma_f32_16x16x32_bf16 v[76:79], a[252:255], a[172:175], v[76:79]\n"
"ds_read_b128 a[192:195], v243 offset:0\n"
"v_mov_b32 v199, 0\n"
"v_mfma_f32_16x16x32_bf16 v[60:63], a[248:251], a[160:163], v[60:63]\n"
"v_cvt_pk_bf16_f32 v104, v104, v105\n"
"v_cvt_pk_bf16_f32 v105, v106, v107\n"
"v_mfma_f32_16x16x32_bf16 v[60:63], a[252:255], a[164:167], v[60:63]\n"
"ds_write2st64_b64 v255, v[208:209], v[210:211] offset0:192 offset1:193\n"
"v_mfma_f32_16x16x32_bf16 v[56:59], a[240:243], a[160:163], v[56:59]\n"
"s_add_u32 s40, s56, s53\n"
"v_accvgpr_write_b32 a121, v137\n"
"v_cvt_pk_bf16_f32 v146, v148, v149\n"
"v_mfma_f32_16x16x32_bf16 v[56:59], a[244:247], a[164:167], v[56:59]\n"
"buffer_load_dwordx4 v240, s[16:19], s40 offen offset:2048 nt lds\n"
"v_mfma_f32_16x16x32_bf16 v[52:55], a[232:235], a[160:163], v[52:55]\n"
"v_cvt_pk_bf16_f32 v97, v98, v99\n"
"v_cvt_pk_bf16_f32 v88, v88, v89\n"
"v_mfma_f32_16x16x32_bf16 v[52:55], a[236:239], a[164:167], v[52:55]\n"
"v_cvt_pk_bf16_f32 v106, v108, v109\n"
"v_mov_b32 v204, 0\n"
"v_mfma_f32_16x16x32_bf16 v[48:51], a[224:227], a[160:163], v[48:51]\n"
"v_cvt_pk_bf16_f32 v219, v222, v223\n"
"v_mov_b32 v205, 0\n"
"v_mfma_f32_16x16x32_bf16 v[48:51], a[228:231], a[164:167], v[48:51]\n"
"ds_write_b128 v243, v[160:163] offset:8192\n"
"v_cvt_pk_bf16_f32 v170, v172, v173\n"
"v_mfma_f32_16x16x32_bf16 v[0:3], a[224:227], a[136:139], v[0:3]\n"
"v_permlane16_swap_b32 v153, v155\n"
"v_mfma_f32_16x16x32_bf16 v[0:3], a[228:231], a[140:143], v[0:3]\n"
"ds_write2st64_b64 v255, v[216:217], v[218:219] offset0:194 offset1:195\n"
"v_mfma_f32_16x16x32_bf16 v[4:7], a[232:235], a[136:139], v[4:7]\n"
"v_permlane16_swap_b32 v169, v171\n"
"v_mfma_f32_16x16x32_bf16 v[4:7], a[236:239], a[140:143], v[4:7]\n"
"v_pk_fma_f32 v[64:65], v[64:65], s[70:71], s[70:71] op_sel:[0,0,1] op_sel_hi:[1,0,1]\nv_pk_fma_f32 v[66:67], v[66:67], s[70:71], s[70:71] op_sel:[0,0,1] op_sel_hi:[1,0,1]\nv_pk_fma_f32 v[68:69], v[68:69], s[70:71], s[70:71] op_sel:[0,0,1] op_sel_hi:[1,0,1]\nv_pk_fma_f32 v[70:71], v[70:71], s[70:71], s[70:71] op_sel:[0,0,1] op_sel_hi:[1,0,1]\nv_pk_fma_f32 v[72:73], v[72:73], s[70:71], s[70:71] op_sel:[0,0,1] op_sel_hi:[1,0,1]\nv_pk_fma_f32 v[74:75], v[74:75], s[70:71], s[70:71] op_sel:[0,0,1] op_sel_hi:[1,0,1]\nv_pk_fma_f32 v[76:77], v[76:77], s[70:71], s[70:71] op_sel:[0,0,1] op_sel_hi:[1,0,1]\nv_pk_fma_f32 v[78:79], v[78:79], s[70:71], s[70:71] op_sel:[0,0,1] op_sel_hi:[1,0,1]\nv_pk_fma_f32 v[48:49], v[48:49], s[70:71], s[70:71] op_sel:[0,0,1] op_sel_hi:[1,0,1]\nv_pk_fma_f32 v[50:51], v[50:51], s[70:71], s[70:71] op_sel:[0,0,1] op_sel_hi:[1,0,1]\nv_pk_fma_f32 v[52:53], v[52:53], s[70:71], s[70:71] op_sel:[0,0,1] op_sel_hi:[1,0,1]\nv_pk_fma_f32 v[54:55], v[54:55], s[70:71], s[70:71] op_sel:[0,0,1] op_sel_hi:[1,0,1]\nv_pk_fma_f32 v[56:57], v[56:57], s[70:71], s[70:71] op_sel:[0,0,1] op_sel_hi:[1,0,1]\nv_pk_fma_f32 v[58:59], v[58:59], s[70:71], s[70:71] op_sel:[0,0,1] op_sel_hi:[1,0,1]\nv_pk_fma_f32 v[60:61], v[60:61], s[70:71], s[70:71] op_sel:[0,0,1] op_sel_hi:[1,0,1]\nv_pk_fma_f32 v[62:63], v[62:63], s[70:71], s[70:71] op_sel:[0,0,1] op_sel_hi:[1,0,1]\n"
"v_mfma_f32_16x16x32_bf16 v[8:11], a[240:243], a[136:139], v[8:11]\n"
"ds_read_b128 a[204:207], v198 offset:2048\n"
"v_cvt_pk_bf16_f32 v56, v56, v57\n"
"v_mfma_f32_16x16x32_bf16 v[8:11], a[244:247], a[140:143], v[8:11]\n"
"v_permlane16_swap_b32 v152, v154\n"
"v_mfma_f32_16x16x32_bf16 v[12:15], a[248:251], a[136:139], v[12:15]\n"
"v_permlane16_swap_b32 v144, v146\n"
"v_mfma_f32_16x16x32_bf16 v[12:15], a[252:255], a[140:143], v[12:15]\n"
"v_accvgpr_write_b32 a137, v153\n"
"v_cvt_pk_bf16_f32 v80, v80, v81\n"
"v_mfma_f32_16x16x32_bf16 v[28:31], a[248:251], a[144:147], v[28:31]\n"
"s_add_u32 s40, s56, s54\n"
"ds_read_b128 v[208:211], v196 offset:0\n"
"v_cvt_pk_bf16_f32 v147, v150, v151\n"
"v_mfma_f32_16x16x32_bf16 v[28:31], a[252:255], a[148:151], v[28:31]\n"
"buffer_load_dwordx4 v248, s[16:19], s40 offen offset:3072 nt lds\n"
"v_mfma_f32_16x16x32_bf16 v[24:27], a[240:243], a[144:147], v[24:27]\n"
"v_cvt_pk_bf16_f32 v107, v110, v111\n"
"v_cvt_pk_bf16_f32 v48, v48, v49\n"
"v_mfma_f32_16x16x32_bf16 v[24:27], a[244:247], a[148:151], v[24:27]\n"
"v_permlane16_swap_b32 v129, v131\n"
"v_mfma_f32_16x16x32_bf16 v[20:23], a[232:235], a[144:147], v[20:23]\n"
"v_permlane16_swap_b32 v136, v138\n"
"v_mfma_f32_16x16x32_bf16 v[20:23], a[236:239], a[148:151], v[20:23]\n"
"ds_read_b128 v[216:219], v196 offset:512\n"
"v_cvt_pk_bf16_f32 v64, v64, v65\n"
"v_mfma_f32_16x16x32_bf16 v[16:19], a[224:227], a[144:147], v[16:19]\n"
"v_cvt_pk_bf16_f32 v122, v124, v125\n"
"v_cvt_pk_bf16_f32 v72, v72, v73\n"
"v_mfma_f32_16x16x32_bf16 v[16:19], a[228:231], a[148:151], v[16:19]\n"
"v_accvgpr_write_b32 a113, v129\n"
"v_accvgpr_write_b32 a122, v138\n"
"v_mfma_f32_16x16x32_bf16 v[32:35], a[224:227], a[152:155], v[32:35]\n"
"v_cvt_pk_bf16_f32 v98, v100, v101\n"
"v_cvt_pk_bf16_f32 v65, v66, v67\n"
"v_mfma_f32_16x16x32_bf16 v[32:35], a[228:231], a[156:159], v[32:35]\n"
"ds_read_b128 v[220:223], v197 offset:512\n"
"v_accvgpr_write_b32 a138, v154\n"
"v_mfma_f32_16x16x32_bf16 v[36:39], a[232:235], a[152:155], v[36:39]\n"
"v_accvgpr_write_b32 a136, v152\n"
"v_cvt_pk_bf16_f32 v89, v90, v91\n"
"v_mfma_f32_16x16x32_bf16 v[36:39], a[236:239], a[156:159], v[36:39]\n"
"v_permlane16_swap_b32 v145, v147\n"
"v_mfma_f32_16x16x32_bf16 v[40:43], a[240:243], a[152:155], v[40:43]\n"
"v_accvgpr_write_b32 a128, v144\n"
"v_cvt_pk_bf16_f32 v99, v102, v103\n"
"v_mfma_f32_16x16x32_bf16 v[40:43], a[244:247], a[156:159], v[40:43]\n"
"ds_write2st64_b64 v255, v[192:193], v[194:195] offset0:100 offset1:101\n"
"v_pk_fma_f32 v[0:1], v[0:1], s[70:71], s[70:71] op_sel:[0,0,1] op_sel_hi:[1,0,1]\nv_pk_fma_f32 v[2:3], v[2:3], s[70:71], s[70:71] op_sel:[0,0,1] op_sel_hi:[1,0,1]\nv_pk_fma_f32 v[4:5], v[4:5], s[70:71], s[70:71] op_sel:[0,0,1] op_sel_hi:[1,0,1]\nv_pk_fma_f32 v[6:7], v[6:7], s[70:71], s[70:71] op_sel:[0,0,1] op_sel_hi:[1,0,1]\nv_pk_fma_f32 v[8:9], v[8:9], s[70:71], s[70:71] op_sel:[0,0,1] op_sel_hi:[1,0,1]\nv_pk_fma_f32 v[10:11], v[10:11], s[70:71], s[70:71] op_sel:[0,0,1] op_sel_hi:[1,0,1]\nv_pk_fma_f32 v[12:13], v[12:13], s[70:71], s[70:71] op_sel:[0,0,1] op_sel_hi:[1,0,1]\nv_pk_fma_f32 v[14:15], v[14:15], s[70:71], s[70:71] op_sel:[0,0,1] op_sel_hi:[1,0,1]\nv_pk_fma_f32 v[16:17], v[16:17], s[70:71], s[70:71] op_sel:[0,0,1] op_sel_hi:[1,0,1]\nv_pk_fma_f32 v[18:19], v[18:19], s[70:71], s[70:71] op_sel:[0,0,1] op_sel_hi:[1,0,1]\nv_pk_fma_f32 v[20:21], v[20:21], s[70:71], s[70:71] op_sel:[0,0,1] op_sel_hi:[1,0,1]\nv_pk_fma_f32 v[22:23], v[22:23], s[70:71], s[70:71] op_sel:[0,0,1] op_sel_hi:[1,0,1]\nv_pk_fma_f32 v[24:25], v[24:25], s[70:71], s[70:71] op_sel:[0,0,1] op_sel_hi:[1,0,1]\nv_pk_fma_f32 v[26:27], v[26:27], s[70:71], s[70:71] op_sel:[0,0,1] op_sel_hi:[1,0,1]\nv_pk_fma_f32 v[28:29], v[28:29], s[70:71], s[70:71] op_sel:[0,0,1] op_sel_hi:[1,0,1]\nv_pk_fma_f32 v[30:31], v[30:31], s[70:71], s[70:71] op_sel:[0,0,1] op_sel_hi:[1,0,1]\n"
"v_mfma_f32_16x16x32_bf16 v[44:47], a[248:251], a[152:155], v[44:47]\n"
"v_accvgpr_write_b32 a115, v131\n"
"v_accvgpr_write_b32 a120, v136\n"
"v_mfma_f32_16x16x32_bf16 v[44:47], a[252:255], a[156:159], v[44:47]\n"
"s_waitcnt lgkmcnt(2)\n"
"ds_write2st64_b64 v255, v[96:97], v[98:99] offset0:228 offset1:229\n"
"v_cvt_pk_bf16_f32 v81, v82, v83\n"
"v_mfma_f32_16x16x32_bf16 v[180:183], v[208:211], a[0:3], 0\n"
"v_accvgpr_write_b32 a112, v128\n"
"v_cvt_pk_bf16_f32 v91, v94, v95\n"
"v_mfma_f32_16x16x32_bf16 v[180:183], v[212:215], a[4:7], v[180:183]\n"
"v_permlane16_swap_b32 v168, v170\n"
"v_mfma_f32_16x16x32_bf16 v[180:183], v[216:219], a[8:11], v[180:183]\n"
"s_waitcnt lgkmcnt(2)\n"
"v_cvt_pk_bf16_f32 v73, v74, v75\n"
"v_cvt_pk_bf16_f32 v8, v8, v9\n"
"v_mfma_f32_16x16x32_bf16 v[180:183], v[220:223], a[12:15], v[180:183]\n"
"ds_write_b128 v243, v[168:171] offset:10240\n"
"v_pk_fma_f32 v[32:33], v[32:33], s[70:71], s[70:71] op_sel:[0,0,1] op_sel_hi:[1,0,1]\nv_pk_fma_f32 v[34:35], v[34:35], s[70:71], s[70:71] op_sel:[0,0,1] op_sel_hi:[1,0,1]\nv_pk_fma_f32 v[36:37], v[36:37], s[70:71], s[70:71] op_sel:[0,0,1] op_sel_hi:[1,0,1]\nv_pk_fma_f32 v[38:39], v[38:39], s[70:71], s[70:71] op_sel:[0,0,1] op_sel_hi:[1,0,1]\nv_pk_fma_f32 v[40:41], v[40:41], s[70:71], s[70:71] op_sel:[0,0,1] op_sel_hi:[1,0,1]\nv_pk_fma_f32 v[42:43], v[42:43], s[70:71], s[70:71] op_sel:[0,0,1] op_sel_hi:[1,0,1]\nv_pk_fma_f32 v[44:45], v[44:45], s[70:71], s[70:71] op_sel:[0,0,1] op_sel_hi:[1,0,1]\nv_pk_fma_f32 v[46:47], v[46:47], s[70:71], s[70:71] op_sel:[0,0,1] op_sel_hi:[1,0,1]\n"
"v_mfma_f32_16x16x32_bf16 v[184:187], v[208:211], a[16:19], 0\n"
"v_cvt_pk_bf16_f32 v49, v50, v51\n"
"v_cvt_pk_bf16_f32 v51, v54, v55\n"
"v_mfma_f32_16x16x32_bf16 v[184:187], v[212:215], a[20:23], v[184:187]\n"
"v_permlane16_swap_b32 v120, v122\n"
"v_mfma_f32_16x16x32_bf16 v[184:187], v[216:219], a[24:27], v[184:187]\n"
"v_cvt_pk_bf16_f32 v90, v92, v93\n"
"v_sub_f32 v183, s74, v183\n"
"v_mfma_f32_16x16x32_bf16 v[184:187], v[220:223], a[28:31], v[184:187]\n"
"ds_write2st64_b64 v255, v[104:105], v[106:107] offset0:230 offset1:231\n"
"v_cvt_pk_bf16_f32 v67, v70, v71\n"
"v_sub_f32 v182, s74, v182\n"
"v_mfma_f32_16x16x32_bf16 v[188:191], v[208:211], a[32:35], 0\n"
"v_cvt_pk_bf16_f32 v40, v40, v41\n"
"v_sub_f32 v180, s74, v180\n"
"v_mfma_f32_16x16x32_bf16 v[188:191], v[212:215], a[36:39], v[188:191]\n"
"v_accvgpr_write_b32 a139, v155\n"
"v_sub_f32 v181, s74, v181\n"
"v_mfma_f32_16x16x32_bf16 v[188:191], v[216:219], a[40:43], v[188:191]\n"
"v_cvt_pk_bf16_f32 v9, v10, v11\n"
"v_sub_f32 v186, s74, v186\n"
"v_mfma_f32_16x16x32_bf16 v[188:191], v[220:223], a[44:47], v[188:191]\n"
"v_cvt_pk_bf16_f32 v83, v86, v87\n"
"v_cvt_pk_bf16_f32 v57, v58, v59\n"
"v_mfma_f32_16x16x32_bf16 v[192:195], v[208:211], a[192:195], 0\n"
"ds_read_b128 v[208:211], v228 offset:0\n"
"v_sub_f32 v185, s74, v185\n"
"v_mfma_f32_16x16x32_bf16 v[192:195], v[212:215], a[196:199], v[192:195]\n"
"ds_read_b128 v[212:215], v229 offset:0\n"
"v_cvt_pk_bf16_f32 v41, v42, v43\n"
"v_mfma_f32_16x16x32_bf16 v[192:195], v[216:219], a[200:203], v[192:195]\n"
"ds_read_b128 v[216:219], v228 offset:512\n"
"v_sub_f32 v189, s74, v189\n"
"v_mfma_f32_16x16x32_bf16 v[192:195], v[220:223], a[204:207], v[192:195]\n"
"s_waitcnt lgkmcnt(2)\n"
"ds_read_b128 v[220:223], v229 offset:512\n"
"v_sub_f32 v184, s74, v184\n"
"v_mfma_f32_16x16x32_bf16 v[152:155], v[208:211], a[0:3], v[180:183]\n"
"s_waitcnt lgkmcnt(2)\n"
"v_cvt_pk_bf16_f32 v42, v44, v45\n"
"v_sub_f32 v187, s74, v187\n"
"v_mfma_f32_16x16x32_bf16 v[152:155], v[212:215], a[4:7], v[152:155]\n"
"s_waitcnt lgkmcnt(1)\n"
"v_accvgpr_write_b32 a129, v145\n"
"v_cvt_pk_bf16_f32 v82, v84, v85\n"
"v_mfma_f32_16x16x32_bf16 v[152:155], v[216:219], a[8:11], v[152:155]\n"
"s_waitcnt lgkmcnt(0)\n"
"v_cvt_pk_bf16_f32 v58, v60, v61\n"
"v_sub_f32 v188, s74, v188\n"
"v_mfma_f32_16x16x32_bf16 v[152:155], v[220:223], a[12:15], v[152:155]\n"
"v_cvt_pk_bf16_f32 v59, v62, v63\n"
"v_sub_f32 v190, s74, v190\n"
"v_mfma_f32_16x16x32_bf16 v[156:159], v[216:219], a[24:27], v[184:187]\n"
"v_cvt_pk_bf16_f32 v11, v14, v15\n"
"v_cvt_pk_bf16_f32 v43, v46, v47\n"
"v_mfma_f32_16x16x32_bf16 v[156:159], v[220:223], a[28:31], v[156:159]\n"
"v_sub_f32 v191, s74, v191\n"
"v_sub_f32 v192, s74, v192\n"
"v_mfma_f32_16x16x32_bf16 v[156:159], v[208:211], a[16:19], v[156:159]\n"
"v_cvt_pk_bf16_f32 v123, v126, v127\n"
"v_cvt_pk_bf16_f32 v10, v12, v13\n"
"v_mfma_f32_16x16x32_bf16 v[156:159], v[212:215], a[20:23], v[156:159]\n"
"ds_write2st64_b64 v255, v[88:89], v[90:91] offset0:226 offset1:227\n"
"v_permlane16_swap_b32 v57, v59\n"
"v_cvt_pk_bf16_f32 v16, v16, v17\n"
"v_accvgpr_write_b32 a241, v254\n"
"v_mov_b32 v196, 0\n"
"v_sub_f32 v193, s74, v193\n"
"v_exp_f32 v153, v153\n"
"v_mfma_f32_16x16x32_bf16 v[160:163], v[208:211], a[32:35], v[188:191]\n"
"v_sub_f32 v194, s74, v194\n"
"v_sub_f32 v195, s74, v195\n"
"v_mfma_f32_16x16x32_bf16 v[160:163], v[212:215], a[36:39], v[160:163]\n"
"v_exp_f32 v152, v152\n"
"v_mfma_f32_16x16x32_bf16 v[160:163], v[216:219], a[40:43], v[160:163]\n"
"v_exp_f32 v158, v158\n"
"v_mfma_f32_16x16x32_bf16 v[160:163], v[220:223], a[44:47], v[160:163]\n"
"v_exp_f32 v156, v156\n"
"v_mfma_f32_16x16x32_bf16 v[164:167], v[216:219], a[200:203], v[192:195]\n"
"ds_read_b128 v[216:219], v228 offset:4608\n"
"v_cvt_pk_bf16_f32 v74, v76, v77\n"
"v_mfma_f32_16x16x32_bf16 v[164:167], v[220:223], a[204:207], v[164:167]\n"
"ds_read_b128 v[220:223], v229 offset:4608\n"
"v_cvt_pk_bf16_f32 v66, v68, v69\n"
"v_mfma_f32_16x16x32_bf16 v[164:167], v[208:211], a[192:195], v[164:167]\n"
"ds_read_b128 v[208:211], v228 offset:4096\n"
"v_cvt_pk_bf16_f32 v24, v24, v25\n"
"v_mfma_f32_16x16x32_bf16 v[164:167], v[212:215], a[196:199], v[164:167]\n"
"s_waitcnt lgkmcnt(2)\n"
"ds_read_b128 v[212:215], v229 offset:4096\n"
"v_accvgpr_write_b32 a114, v130\n"
"v_mfma_f32_16x16x32_bf16 v[168:171], v[216:219], a[8:11], v[180:183]\n"
"s_waitcnt lgkmcnt(2)\n"
"v_exp_f32 v163, v163\n"
"v_mfma_f32_16x16x32_bf16 v[168:171], v[220:223], a[12:15], v[168:171]\n"
"s_waitcnt lgkmcnt(1)\n"
"v_exp_f32 v162, v162\n"
"v_mfma_f32_16x16x32_bf16 v[168:171], v[208:211], a[0:3], v[168:171]\n"
"s_waitcnt lgkmcnt(0)\n"
"v_exp_f32 v160, v160\n"
"v_mfma_f32_16x16x32_bf16 v[168:171], v[212:215], a[4:7], v[168:171]\n"
"ds_write2st64_b64 v255, v[80:81], v[82:83] offset0:224 offset1:225\n"
"v_permlane16_swap_b32 v121, v123\n"
"v_accvgpr_write_b32 a131, v147\n"
"v_exp_f32 v154, v154\n"
"v_exp_f32 v157, v157\n"
"v_exp_f32 v159, v159\n"
"v_exp_f32 v161, v161\n"
"v_exp_f32 v167, v167\n"
"v_mfma_f32_16x16x32_bf16 v[172:175], v[208:211], a[16:19], v[184:187]\n"
"v_exp_f32 v165, v165\n"
"v_mfma_f32_16x16x32_bf16 v[172:175], v[212:215], a[20:23], v[172:175]\n"
"v_exp_f32 v166, v166\n"
"v_mfma_f32_16x16x32_bf16 v[172:175], v[216:219], a[24:27], v[172:175]\n"
"v_exp_f32 v169, v169\n"
"v_mfma_f32_16x16x32_bf16 v[172:175], v[220:223], a[28:31], v[172:175]\n"
"v_exp_f32 v155, v155\n"
"v_mfma_f32_16x16x32_bf16 v[176:179], v[216:219], a[40:43], v[188:191]\n"
"v_cvt_pk_bf16_f32 v128, v152, v153\n"
"v_bfe_u32 v248, v242, 9, 1\n"
"v_mfma_f32_16x16x32_bf16 v[176:179], v[220:223], a[44:47], v[176:179]\n"
"v_exp_f32 v171, v171\n"
"v_mfma_f32_16x16x32_bf16 v[176:179], v[208:211], a[32:35], v[176:179]\n"
"v_cvt_pk_bf16_f32 v129, v154, v155\n"
"v_and_b32 v249, 0xf0, v242\n"
"v_mfma_f32_16x16x32_bf16 v[176:179], v[212:215], a[36:39], v[176:179]\n"
"ds_write2st64_b64 v255, v[64:65], v[66:67] offset0:196 offset1:197\n"
"v_cvt_pk_bf16_f32 v136, v156, v157\n"
"v_cvt_pk_bf16_f32 v137, v158, v159\n"
"v_cvt_pk_bf16_f32 v145, v162, v163\n"
"v_exp_f32 v170, v170\n"
"v_mfma_f32_16x16x32_bf16 v[152:155], v[208:211], a[192:195], v[192:195]\n"
"ds_read_b128 v[208:211], v230 offset:0\n"
"v_cvt_pk_bf16_f32 v125, v166, v167\n"
"v_mfma_f32_16x16x32_bf16 v[152:155], v[212:215], a[196:199], v[152:155]\n"
"ds_read_b128 v[212:215], v231 offset:0\n"
"v_accvgpr_write_b32 a123, v139\n"
"v_mfma_f32_16x16x32_bf16 v[152:155], v[216:219], a[200:203], v[152:155]\n"
"ds_read_b128 v[216:219], v230 offset:512\n"
"v_cvt_pk_bf16_f32 v50, v52, v53\n"
"v_mfma_f32_16x16x32_bf16 v[152:155], v[220:223], a[204:207], v[152:155]\n"
"s_waitcnt lgkmcnt(2)\n"
"ds_read_b128 v[220:223], v231 offset:512\n"
"v_permlane16_swap_b32 v40, v42\n"
"v_cvt_pk_bf16_f32 v144, v160, v161\n"
"v_exp_f32 v164, v164\n"
"v_exp_f32 v168, v168\n"
"v_cvt_pk_bf16_f32 v131, v170, v171\n"
"v_exp_f32 v172, v172\n"
"v_exp_f32 v173, v173\n"
"v_exp_f32 v174, v174\n"
"v_exp_f32 v176, v176\n"
"v_exp_f32 v179, v179\n"
"v_lshl_add_u32 v248, v248, 8, v249\n"
"v_mfma_f32_16x16x32_bf16 v[156:159], v[208:211], a[0:3], v[180:183]\n"
"s_waitcnt lgkmcnt(2)\n"
"v_cvt_pk_bf16_f32 v75, v78, v79\n"
"v_cvt_pk_bf16_f32 v130, v168, v169\n"
"v_mfma_f32_16x16x32_bf16 v[156:159], v[212:215], a[4:7], v[156:159]\n"
"s_waitcnt lgkmcnt(1)\n"
"v_bfe_u32 v249, v242, 8, 1\n"
"v_lshl_add_u32 v248, v249, 9, v248\n"
"v_mfma_f32_16x16x32_bf16 v[156:159], v[216:219], a[8:11], v[156:159]\n"
"s_waitcnt lgkmcnt(0)\n"
"v_exp_f32 v177, v177\n"
"v_mfma_f32_16x16x32_bf16 v[156:159], v[220:223], a[12:15], v[156:159]\n"
"v_mov_b32 v202, 0\n"
"v_cvt_pk_bf16_f32 v124, v164, v165\n"
"v_mfma_f32_16x16x32_bf16 v[160:163], v[216:219], a[24:27], v[184:187]\n"
"v_exp_f32 v155, v155\n"
"v_mfma_f32_16x16x32_bf16 v[160:163], v[220:223], a[28:31], v[160:163]\n"
"v_permlane16_swap_b32 v49, v51\n"
"v_mfma_f32_16x16x32_bf16 v[160:163], v[208:211], a[16:19], v[160:163]\n"
"v_mov_b32 v201, 0\n"
"v_xor_b32_e64 v249, s42, 0\n"
"v_mfma_f32_16x16x32_bf16 v[160:163], v[212:215], a[20:23], v[160:163]\n"
"ds_write2st64_b64 v255, v[72:73], v[74:75] offset0:198 offset1:199\n"
"v_permlane16_swap_b32 v48, v50\n"
"v_permlane16_swap_b32 v56, v58\n"
"v_cvt_pk_bf16_f32 v25, v26, v27\n"
"v_cvt_pk_bf16_f32 v138, v172, v173\n"
"v_exp_f32 v153, v153\n"
"v_exp_f32 v157, v157\n"
"v_mfma_f32_16x16x32_bf16 v[164:167], v[208:211], a[32:35], v[188:191]\n"
"ds_read_b128 v[208:211], v230 offset:4096\n"
"v_mov_b32 v197, 0\n"
"v_mfma_f32_16x16x32_bf16 v[164:167], v[212:215], a[36:39], v[164:167]\n"
"ds_read_b128 v[212:215], v231 offset:4096\n"
"v_accvgpr_write_b32 a243, v254\n"
"v_mfma_f32_16x16x32_bf16 v[164:167], v[216:219], a[40:43], v[164:167]\n"
"ds_read_b128 v[216:219], v230 offset:4608\n"
"v_lshl_add_u32 v248, v249, 12, v248\n"
"v_mfma_f32_16x16x32_bf16 v[164:167], v[220:223], a[44:47], v[164:167]\n"
"s_waitcnt lgkmcnt(2)\n"
"ds_read_b128 v[220:223], v231 offset:4608\n"
"v_accvgpr_write_b32 a104, v120\n"
"v_permlane16_swap_b32 v8, v10\n"
"v_permlane16_swap_b32 v9, v11\n"
"v_accvgpr_write_b32 a240, v254\n"
"v_exp_f32 v175, v175\n"
"v_exp_f32 v178, v178\n"
"v_exp_f32 v159, v159\n"
"v_exp_f32 v160, v160\n"
"v_exp_f32 v163, v163\n"
"v_mfma_f32_16x16x32_bf16 v[168:171], v[208:211], a[0:3], v[180:183]\n"
"s_waitcnt lgkmcnt(2)\n"
"v_exp_f32 v154, v154\n"
"v_mfma_f32_16x16x32_bf16 v[168:171], v[212:215], a[4:7], v[168:171]\n"
"s_waitcnt lgkmcnt(1)\n"
"v_accvgpr_write_b32 a186, v42\n"
"v_add_u32 v249, s75, v248\n"
"v_mfma_f32_16x16x32_bf16 v[168:171], v[216:219], a[8:11], v[168:171]\n"
"s_waitcnt lgkmcnt(0)\n"
"v_exp_f32 v152, v152\n"
"v_mfma_f32_16x16x32_bf16 v[168:171], v[220:223], a[12:15], v[168:171]\n"
"ds_write_b128 v243, v[48:51] offset:12288\n"
"v_accvgpr_write_b32 a130, v146\n"
"v_permlane16_swap_b32 v41, v43\n"
"v_cvt_pk_bf16_f32 v139, v174, v175\n"
"v_exp_f32 v164, v164\n"
"v_mfma_f32_16x16x32_bf16 v[172:175], v[216:219], a[24:27], v[184:187]\n"
"v_exp_f32 v156, v156\n"
"v_mfma_f32_16x16x32_bf16 v[172:175], v[220:223], a[28:31], v[172:175]\n"
"v_exp_f32 v158, v158\n"
"v_mfma_f32_16x16x32_bf16 v[172:175], v[208:211], a[16:19], v[172:175]\n"
"v_exp_f32 v168, v168\n"
"v_mfma_f32_16x16x32_bf16 v[172:175], v[212:215], a[20:23], v[172:175]\n"
"s_cbranch_vccnz LPCE0_%=\nLPKE0_%=:\n"
"ds_write_b128 v243, v[56:59] offset:14336\n"
"v_accvgpr_write_b32 a185, v41\n"
"v_cvt_pk_bf16_f32 v146, v176, v177\n"
"v_cvt_pk_bf16_f32 v147, v178, v179\n"
"v_cvt_pk_bf16_f32 v126, v152, v153\n"
"v_cvt_pk_bf16_f32 v133, v158, v159\n"
"v_exp_f32 v165, v165\n"
"v_mfma_f32_16x16x32_bf16 v[176:179], v[208:211], a[32:35], v[188:191]\n"
"v_cvt_pk_bf16_f32 v127, v154, v155\n"
"v_cvt_pk_bf16_f32 v132, v156, v157\n"
"v_mfma_f32_16x16x32_bf16 v[176:179], v[212:215], a[36:39], v[176:179]\n"
"v_exp_f32 v161, v161\n"
"v_mfma_f32_16x16x32_bf16 v[176:179], v[216:219], a[40:43], v[176:179]\n"
"v_cvt_pk_bf16_f32 v17, v18, v19\n"
"v_accvgpr_write_b32 a187, v43\n"
"v_mfma_f32_16x16x32_bf16 v[176:179], v[220:223], a[44:47], v[176:179]\n"
"v_cvt_pk_bf16_f32 v140, v160, v161\n"
"v_exp_f32 v162, v162\n"
"v_exp_f32 v167, v167\n"
"v_exp_f32 v169, v169\n"
"v_exp_f32 v172, v172\n"
"v_exp_f32 v173, v173\n"
"v_exp_f32 v174, v174\n"
"s_waitcnt vmcnt(8) lgkmcnt(0)\n"
"s_barrier\n"
"ds_read_b128 v[208:211], v228 offset:16384\n"
"ds_read_b128 v[212:215], v229 offset:16384\n"
"ds_read_b128 v[216:219], v228 offset:16896\n"
"ds_read_b128 v[220:223], v229 offset:16896\n"
"s_waitcnt lgkmcnt(3)\n"
"v_mfma_f32_16x16x32_bf16 v[152:155], v[208:211], a[0:3], v[180:183]\n"
"v_accvgpr_write_b32 a153, v9\n"
"v_cvt_pk_bf16_f32 v32, v32, v33\n"
"s_waitcnt lgkmcnt(2)\n"
"v_mfma_f32_16x16x32_bf16 v[152:155], v[212:215], a[4:7], v[152:155]\n"
"ds_read_b64_tr_b16 v[224:225], v239 offset:0\n"
"v_cvt_pk_bf16_f32 v141, v162, v163\n"
"s_waitcnt lgkmcnt(2)\n"
"v_mfma_f32_16x16x32_bf16 v[152:155], v[216:219], a[8:11], v[152:155]\n"
"s_waitcnt lgkmcnt(1)\n"
"ds_read_b128 a[188:191], v249 offset:52224\n"
"v_mov_b32 v203, 0\n"
"v_mfma_f32_16x16x32_bf16 v[152:155], v[220:223], a[12:15], v[152:155]\n"
"ds_read_b128 a[116:119], v248 offset:51200\n"
"v_cvt_pk_bf16_f32 v27, v30, v31\n"
"v_mfma_f32_16x16x32_bf16 v[156:159], v[216:219], a[24:27], v[184:187]\n"
"ds_read_b64_tr_b16 v[226:227], v239 offset:4096\n"
"ds_read_b128 a[180:183], v249 offset:51200\n"
"v_mfma_f32_16x16x32_bf16 v[156:159], v[220:223], a[28:31], v[156:159]\n"
"v_accvgpr_write_b32 a154, v10\n"
"v_accvgpr_write_b32 a184, v40\n"
"v_mfma_f32_16x16x32_bf16 v[156:159], v[208:211], a[16:19], v[156:159]\n"
"ds_read_b64_tr_b16 a[244:245], v234 offset:0\n"
"ds_read_b64_tr_b16 a[246:247], v234 offset:4096\n"
"v_mfma_f32_16x16x32_bf16 v[156:159], v[212:215], a[20:23], v[156:159]\n"
"ds_read_b128 a[100:103], v248 offset:49152\n"
"v_exp_f32 v166, v166\n"
"v_exp_f32 v175, v175\n"
"v_exp_f32 v176, v176\n"
"v_exp_f32 v177, v177\n"
"v_exp_f32 v154, v154\n"
"v_mfma_f32_16x16x32_bf16 v[160:163], v[208:211], a[32:35], v[188:191]\n"
"v_mov_b32 v198, 0\n"
"v_cvt_pk_bf16_f32 v148, v164, v165\n"
"v_mfma_f32_16x16x32_bf16 v[160:163], v[212:215], a[36:39], v[160:163]\n"
"ds_read_b64_tr_b16 a[250:251], v238 offset:4096\n"
"ds_read_b64_tr_b16 a[254:255], v235 offset:4096\n"
"v_mfma_f32_16x16x32_bf16 v[160:163], v[216:219], a[40:43], v[160:163]\n"
"ds_read_b64_tr_b16 a[248:249], v238 offset:0\n"
"ds_read_b128 a[156:159], v249 offset:35840\n"
"v_mfma_f32_16x16x32_bf16 v[160:163], v[220:223], a[44:47], v[160:163]\n"
"ds_read_b128 a[172:175], v249 offset:50176\n"
"v_accvgpr_write_b32 a155, v11\n"
"v_cvt_pk_bf16_f32 v149, v166, v167\n"
"v_cvt_pk_bf16_f32 v134, v168, v169\n"
"v_exp_f32 v170, v170\n"
"v_exp_f32 v171, v171\n"
"v_cvt_pk_bf16_f32 v143, v174, v175\n"
"v_exp_f32 v179, v179\n"
"v_mfma_f32_16x16x32_bf16 v[164:167], v[216:219], a[200:203], v[192:195]\n"
"ds_read_b128 a[164:167], v249 offset:49152\n"
"v_cvt_pk_bf16_f32 v142, v172, v173\n"
"v_mfma_f32_16x16x32_bf16 v[164:167], v[220:223], a[204:207], v[164:167]\n"
"ds_read_b64_tr_b16 a[252:253], v235 offset:0\n"
"v_cvt_pk_bf16_f32 v112, v112, v113\n"
"v_mfma_f32_16x16x32_bf16 v[164:167], v[208:211], a[192:195], v[164:167]\n"
"ds_read_b128 a[140:143], v249 offset:33792\n"
"v_cvt_pk_bf16_f32 v0, v0, v1\n"
"v_mfma_f32_16x16x32_bf16 v[164:167], v[212:215], a[196:199], v[164:167]\n"
"s_waitcnt lgkmcnt(8)\n"
"ds_read_b128 a[148:151], v249 offset:34816\n"
"v_accvgpr_write_b32 a152, v8\n"
"v_cvt_pk_bf16_f32 v33, v34, v35\n"
"v_cvt_pk_bf16_f32 v135, v170, v171\n"
"v_exp_f32 v178, v178\n"
"v_xor_b32 v207, 64, v240\n"
"v_exp_f32 v153, v153\n"
"v_exp_f32 v162, v162\n"
"v_mfma_f32_16x16x32_bf16 a[216:219], v[124:127], a[244:247], 0\n"
"ds_read_b128 a[108:111], v248 offset:50176\n"
"v_cvt_pk_bf16_f32 v1, v2, v3\n"
"v_mfma_f32_16x16x32_bf16 v[8:11], v[128:131], a[244:247], 0\n"
"s_waitcnt lgkmcnt(7)\n"
"ds_read_b128 a[124:127], v248 offset:52224\n"
"v_cvt_pk_bf16_f32 v34, v36, v37\n"
"v_mfma_f32_16x16x32_bf16 v[8:11], v[132:135], a[248:251], v[8:11]\n"
"ds_read_b128 v[216:219], v228 offset:20992\n"
"v_cvt_pk_bf16_f32 v26, v28, v29\n"
"v_mfma_f32_16x16x32_bf16 v[40:43], v[136:139], a[244:247], 0\n"
"ds_read_b128 a[132:135], v249 offset:32768\n"
"v_cvt_pk_bf16_f32 v150, v176, v177\n"
"v_mfma_f32_16x16x32_bf16 v[40:43], v[140:143], a[248:251], v[40:43]\n"
"v_cvt_pk_bf16_f32 v3, v6, v7\n"
"v_cvt_pk_bf16_f32 v151, v178, v179\n"
"v_mfma_f32_16x16x32_bf16 v[72:75], v[144:147], a[244:247], 0\n"
"ds_read_b128 v[208:211], v228 offset:20480\n"
"v_accvgpr_write_b32 a242, v254\n"
"v_mfma_f32_16x16x32_bf16 v[72:75], v[148:151], a[248:251], v[72:75]\n"
"ds_read_b128 v[220:223], v229 offset:20992\n"
"v_cvt_pk_bf16_f32 v113, v114, v115\n"
"v_mfma_f32_16x16x32_bf16 v[76:79], v[148:151], v[224:227], 0\n"
"s_waitcnt lgkmcnt(6)\n"
"v_exp_f32 v159, v159\n"
"v_mfma_f32_16x16x32_bf16 v[76:79], v[144:147], a[252:255], v[76:79]\n"
"v_permlane16_swap_b32 v24, v26\n"
"v_mfma_f32_16x16x32_bf16 v[44:47], v[140:143], v[224:227], 0\n"
"v_permlane16_swap_b32 v32, v34\n"
"v_mfma_f32_16x16x32_bf16 v[44:47], v[136:139], a[252:255], v[44:47]\n"
"ds_read_b128 v[212:215], v229 offset:20480\n"
"v_permlane16_swap_b32 v25, v27\n"
"v_cvt_pk_bf16_f32 v35, v38, v39\n"
"v_exp_f32 v155, v155\n"
"v_exp_f32 v160, v160\n"
"v_exp_f32 v167, v167\n"
"v_mfma_f32_16x16x32_bf16 v[12:15], v[132:135], v[224:227], 0\n"
"v_exp_f32 v164, v164\n"
"v_mfma_f32_16x16x32_bf16 v[12:15], v[128:131], a[252:255], v[12:15]\n"
"v_exp_f32 v166, v166\n"
"v_mfma_f32_16x16x32_bf16 a[220:223], v[124:127], a[252:255], 0\n"
"s_waitcnt lgkmcnt(2)\n"
"ds_read_b64_tr_b16 a[254:255], v235 offset:4608\n"
"v_accvgpr_write_b32 a169, v25\n"
"v_smfmac_f32_16x16x64_bf16 v[196:199], a[240:243], v[128:135], v241\n"
"s_add_u32 m0, s44, 98304\n"
"ds_read_b64_tr_b16 v[224:225], v239 offset:512\n"
"v_cvt_pk_bf16_f32 v97, v154, v155\n"
"v_mfma_f32_16x16x32_bf16 v[202:205], a[240:243], v[124:127], v[202:205]\n"
"buffer_load_dwordx4 v240, s[20:23], s56 offen nt lds\n"
"v_mfma_f32_16x16x32_bf16 v[168:171], v[216:219], a[8:11], v[180:183]\n"
"s_waitcnt lgkmcnt(3)\n"
"v_exp_f32 v152, v152\n"
"v_mfma_f32_16x16x32_bf16 v[168:171], v[220:223], a[12:15], v[168:171]\n"
"ds_read_b64_tr_b16 a[252:253], v235 offset:512\n"
"v_accvgpr_write_b32 a168, v24\n"
"v_mfma_f32_16x16x32_bf16 v[168:171], v[208:211], a[0:3], v[168:171]\n"
"s_waitcnt lgkmcnt(3)\n"
"ds_read_b64_tr_b16 a[244:245], v234 offset:512\n"
"v_cvt_pk_bf16_f32 v96, v152, v153\n"
"v_mfma_f32_16x16x32_bf16 v[168:171], v[212:215], a[4:7], v[168:171]\n"
"ds_read_b64_tr_b16 a[248:249], v238 offset:512\n"
"ds_read_b64_tr_b16 a[250:251], v238 offset:4608\n"
"v_mfma_f32_16x16x32_bf16 v[172:175], v[208:211], a[16:19], v[184:187]\n"
"ds_read_b64_tr_b16 a[246:247], v234 offset:4608\n"
"v_cvt_pk_bf16_f32 v115, v118, v119\n"
"v_mfma_f32_16x16x32_bf16 v[172:175], v[212:215], a[20:23], v[172:175]\n"
"v_exp_f32 v165, v165\n"
"v_mfma_f32_16x16x32_bf16 v[172:175], v[216:219], a[24:27], v[172:175]\n"
"s_add_u32 s40, s56, s52\n"
"v_exp_f32 v158, v158\n"
"v_mfma_f32_16x16x32_bf16 v[172:175], v[220:223], a[28:31], v[172:175]\n"
"buffer_load_dwordx4 v207, s[20:23], s40 offen offset:1024 nt lds\n"
"v_mfma_f32_16x16x32_bf16 v[176:179], v[216:219], a[40:43], v[188:191]\n"
"v_exp_f32 v157, v157\n"
"v_mfma_f32_16x16x32_bf16 v[176:179], v[220:223], a[44:47], v[176:179]\n"
"v_accvgpr_write_b32 a105, v121\n"
"v_accvgpr_write_b32 a170, v26\n"
"v_mfma_f32_16x16x32_bf16 v[176:179], v[208:211], a[32:35], v[176:179]\n"
"v_exp_f32 v156, v156\n"
"v_mfma_f32_16x16x32_bf16 v[176:179], v[212:215], a[36:39], v[176:179]\n"
"v_permlane16_swap_b32 v113, v115\n"
"v_cvt_pk_bf16_f32 v2, v4, v5\n"
"v_exp_f32 v161, v161\n"
"v_exp_f32 v163, v163\n"
"v_exp_f32 v172, v172\n"
"v_mfma_f32_16x16x32_bf16 v[152:155], v[208:211], a[192:195], v[192:195]\n"
"v_accvgpr_write_b32 a171, v27\n"
"v_mov_b32 v200, 0\n"
"v_mfma_f32_16x16x32_bf16 v[152:155], v[212:215], a[196:199], v[152:155]\n"
"ds_read_b64_tr_b16 v[226:227], v239 offset:4608\n"
"v_accvgpr_write_b32 a176, v32\n"
"v_mfma_f32_16x16x32_bf16 v[152:155], v[216:219], a[200:203], v[152:155]\n"
"ds_read_b128 v[212:215], v231 offset:16384\n"
"v_cvt_pk_bf16_f32 v105, v158, v159\n"
"v_mfma_f32_16x16x32_bf16 v[152:155], v[220:223], a[204:207], v[152:155]\n"
"s_waitcnt lgkmcnt(2)\n"
"v_permlane16_swap_b32 v0, v2\n"
"v_mfma_f32_16x16x32_bf16 a[232:235], v[124:127], a[244:247], 0\n"
"ds_read_b128 v[216:219], v230 offset:16896\n"
"v_cvt_pk_bf16_f32 v18, v20, v21\n"
"v_mfma_f32_16x16x32_bf16 v[24:27], v[128:131], a[244:247], 0\n"
"s_add_u32 s40, s56, s53\n"
"ds_read_b128 v[208:211], v230 offset:16384\n"
"v_cvt_pk_bf16_f32 v104, v156, v157\n"
"v_mfma_f32_16x16x32_bf16 v[24:27], v[132:135], a[248:251], v[24:27]\n"
"buffer_load_dwordx4 v240, s[20:23], s40 offen offset:2048 nt lds\n"
"v_mfma_f32_16x16x32_bf16 v[56:59], v[136:139], a[244:247], 0\n"
"v_exp_f32 v174, v174\n"
"v_mfma_f32_16x16x32_bf16 v[56:59], v[140:143], a[248:251], v[56:59]\n"
"v_accvgpr_write_b32 a97, v113\n"
"v_accvgpr_write_b32 a144, v0\n"
"v_mfma_f32_16x16x32_bf16 v[88:91], v[144:147], a[244:247], 0\n"
"v_exp_f32 v171, v171\n"
"v_mfma_f32_16x16x32_bf16 v[88:91], v[148:151], a[248:251], v[88:91]\n"
"s_waitcnt lgkmcnt(2)\n"
"ds_read_b128 v[220:223], v231 offset:16896\n"
"v_cvt_pk_bf16_f32 v114, v116, v117\n"
"v_mfma_f32_16x16x32_bf16 v[92:95], v[148:151], v[224:227], 0\n"
"s_add_u32 s40, s56, s54\n"
"v_permlane16_swap_b32 v33, v35\n"
"v_mfma_f32_16x16x32_bf16 v[92:95], v[144:147], a[252:255], v[92:95]\n"
"buffer_load_dwordx4 v207, s[20:23], s40 offen offset:3072 nt lds\n"
"v_permlane16_swap_b32 v112, v114\n"
"v_accvgpr_write_b32 a99, v115\n"
"v_cvt_pk_bf16_f32 v120, v164, v165\n"
"v_cvt_pk_bf16_f32 v121, v166, v167\n"
"v_exp_f32 v175, v175\n"
"v_exp_f32 v178, v178\n"
"v_mfma_f32_16x16x32_bf16 v[60:63], v[140:143], v[224:227], 0\n"
"s_add_u32 s55, s55, s59\n"
"v_permlane16_swap_b32 v1, v3\n"
"v_mfma_f32_16x16x32_bf16 v[60:63], v[136:139], a[252:255], v[60:63]\n"
"s_or_b32 s55, s55, s58\n"
"v_accvgpr_write_b32 a96, v112\n"
"v_accvgpr_write_b32 a178, v34\n"
"v_mfma_f32_16x16x32_bf16 v[28:31], v[132:135], v[224:227], 0\n"
"s_xor_b32 s56, s55, s57\n"
"ds_read_b64_tr_b16 v[224:225], v237 offset:0\n"
"v_accvgpr_write_b32 a179, v35\n"
"v_mfma_f32_16x16x32_bf16 v[28:31], v[128:131], a[252:255], v[28:31]\n"
"ds_read_b64_tr_b16 a[244:245], v232 offset:0\n"
"v_accvgpr_write_b32 a145, v1\n"
"v_mfma_f32_16x16x32_bf16 a[236:239], v[124:127], a[252:255], 0\n"
"v_exp_f32 v179, v179\n"
"v_smfmac_f32_16x16x64_bf16 v[198:201], a[240:243], v[136:143], v241\n"
"s_waitcnt lgkmcnt(2)\n"
"ds_read_b64_tr_b16 a[252:253], v233 offset:0\n"
"v_accvgpr_write_b32 a177, v33\n"
"v_mfma_f32_16x16x32_bf16 v[156:159], v[208:211], a[0:3], v[180:183]\n"
"ds_read_b64_tr_b16 a[246:247], v232 offset:4096\n"
"v_cvt_pk_bf16_f32 v113, v162, v163\n"
"v_mfma_f32_16x16x32_bf16 v[156:159], v[212:215], a[4:7], v[156:159]\n"
"ds_read_b64_tr_b16 a[250:251], v236 offset:4096\n"
"v_cvt_pk_bf16_f32 v112, v160, v161\n"
"v_mfma_f32_16x16x32_bf16 v[156:159], v[216:219], a[8:11], v[156:159]\n"
"s_add_u32 m0, s44, 49152\n"
"ds_read_b64_tr_b16 a[248:249], v236 offset:0\n"
"ds_read_b64_tr_b16 v[226:227], v237 offset:4096\n"
"v_mfma_f32_16x16x32_bf16 v[156:159], v[220:223], a[12:15], v[156:159]\n"
"buffer_load_dwordx4 v240, s[16:19], s56 offen nt lds\n"
"v_mfma_f32_16x16x32_bf16 v[160:163], v[216:219], a[24:27], v[184:187]\n"
"v_exp_f32 v169, v169\n"
"v_mfma_f32_16x16x32_bf16 v[160:163], v[220:223], a[28:31], v[160:163]\n"
"v_accvgpr_write_b32 a98, v114\n"
"v_accvgpr_write_b32 a147, v3\n"
"v_mfma_f32_16x16x32_bf16 v[160:163], v[208:211], a[16:19], v[160:163]\n"
"v_accvgpr_write_b32 a146, v2\n"
"v_cvt_pk_bf16_f32 v107, v174, v175\n"
"v_mfma_f32_16x16x32_bf16 v[160:163], v[212:215], a[20:23], v[160:163]\n"
"ds_read_b64_tr_b16 a[254:255], v233 offset:4096\n"
"v_cvt_pk_bf16_f32 v115, v178, v179\n"
"v_mfma_f32_16x16x32_bf16 v[164:167], v[208:211], a[32:35], v[188:191]\n"
"v_exp_f32 v158, v158\n"
"v_mfma_f32_16x16x32_bf16 v[164:167], v[212:215], a[36:39], v[164:167]\n"
"v_exp_f32 v168, v168\n"
"v_mfma_f32_16x16x32_bf16 v[164:167], v[216:219], a[40:43], v[164:167]\n"
"v_exp_f32 v170, v170\n"
"v_mfma_f32_16x16x32_bf16 v[164:167], v[220:223], a[44:47], v[164:167]\n"
"s_waitcnt lgkmcnt(1)\n"
"v_exp_f32 v159, v159\n"
"v_mfma_f32_16x16x32_bf16 a[208:211], v[124:127], a[244:247], 0\n"
"ds_read_b128 v[208:211], v230 offset:20480\n"
"v_cvt_pk_bf16_f32 v19, v22, v23\n"
"v_mfma_f32_16x16x32_bf16 v[0:3], v[128:131], a[244:247], 0\n"
"s_add_u32 s40, s56, s52\n"
"ds_read_b128 v[216:219], v230 offset:20992\n"
"v_cvt_pk_bf16_f32 v98, v168, v169\n"
"v_mfma_f32_16x16x32_bf16 v[0:3], v[132:135], a[248:251], v[0:3]\n"
"buffer_load_dwordx4 v207, s[16:19], s40 offen offset:1024 nt lds\n"
"v_mfma_f32_16x16x32_bf16 v[32:35], v[136:139], a[244:247], 0\n"
"v_exp_f32 v165, v165\n"
"v_mfma_f32_16x16x32_bf16 v[32:35], v[140:143], a[248:251], v[32:35]\n"
"v_exp_f32 v176, v176\n"
"v_mfma_f32_16x16x32_bf16 v[64:67], v[144:147], a[244:247], 0\n"
"ds_read_b128 v[212:215], v231 offset:20480\n"
"v_cvt_pk_bf16_f32 v99, v170, v171\n"
"v_mfma_f32_16x16x32_bf16 v[64:67], v[148:151], a[248:251], v[64:67]\n"
"ds_read_b128 v[220:223], v231 offset:20992\n"
"v_accvgpr_write_b32 a107, v123\n"
"v_permlane16_swap_b32 v16, v18\n"
"v_exp_f32 v173, v173\n"
"v_exp_f32 v154, v154\n"
"v_exp_f32 v155, v155\n"
"v_mfma_f32_16x16x32_bf16 v[68:71], v[148:151], v[224:227], 0\n"
"s_waitcnt lgkmcnt(4)\n"
"s_add_u32 s40, s56, s53\n"
"v_exp_f32 v164, v164\n"
"v_mfma_f32_16x16x32_bf16 v[68:71], v[144:147], a[252:255], v[68:71]\n"
"buffer_load_dwordx4 v240, s[16:19], s40 offen offset:2048 nt lds\n"
"v_mfma_f32_16x16x32_bf16 v[36:39], v[140:143], v[224:227], 0\n"
"v_exp_f32 v153, v153\n"
"v_mfma_f32_16x16x32_bf16 v[36:39], v[136:139], a[252:255], v[36:39]\n"
"v_exp_f32 v166, v166\n"
"v_mfma_f32_16x16x32_bf16 v[4:7], v[132:135], v[224:227], 0\n"
"v_permlane16_swap_b32 v17, v19\n"
"v_mfma_f32_16x16x32_bf16 v[4:7], v[128:131], a[252:255], v[4:7]\n"
"ds_read_b64_tr_b16 a[250:251], v236 offset:4608\n"
"v_cvt_pk_bf16_f32 v106, v172, v173\n"
"v_mfma_f32_16x16x32_bf16 a[212:215], v[124:127], a[252:255], 0\n"
"v_exp_f32 v162, v162\n"
"v_smfmac_f32_16x16x64_bf16 v[200:203], a[240:243], v[144:151], v241\n"
"s_waitcnt lgkmcnt(1)\n"
"ds_read_b64_tr_b16 a[246:247], v232 offset:4608\n"
"ds_read_b64_tr_b16 a[248:249], v236 offset:512\n"
"v_mfma_f32_16x16x32_bf16 v[168:171], v[208:211], a[0:3], v[180:183]\n"
"v_exp_f32 v177, v177\n"
"v_mfma_f32_16x16x32_bf16 v[168:171], v[212:215], a[4:7], v[168:171]\n"
"ds_read_b64_tr_b16 a[244:245], v232 offset:512\n"
"v_cvt_pk_bf16_f32 v114, v176, v177\n"
"v_mfma_f32_16x16x32_bf16 v[168:171], v[216:219], a[8:11], v[168:171]\n"
"s_add_u32 s40, s56, s54\n"
"v_exp_f32 v157, v157\n"
"v_mfma_f32_16x16x32_bf16 v[168:171], v[220:223], a[12:15], v[168:171]\n"
"buffer_load_dwordx4 v207, s[16:19], s40 offen offset:3072 nt lds\n"
"v_mfma_f32_16x16x32_bf16 v[172:175], v[216:219], a[24:27], v[184:187]\n"
"v_exp_f32 v167, v167\n"
"v_mfma_f32_16x16x32_bf16 v[172:175], v[220:223], a[28:31], v[172:175]\n"
"v_exp_f32 v160, v160\n"
"v_mfma_f32_16x16x32_bf16 v[172:175], v[208:211], a[16:19], v[172:175]\n"
"ds_read_b64_tr_b16 a[254:255], v233 offset:4608\n"
"v_accvgpr_write_b32 a162, v18\n"
"v_mfma_f32_16x16x32_bf16 v[172:175], v[212:215], a[20:23], v[172:175]\n"
"s_cbranch_vccnz LPCE1_%=\nLPKE1_%=:\n"
"v_bfe_u32 v249, v242, 4, 2\n"
"v_mfma_f32_16x16x32_bf16 v[176:179], v[208:211], a[32:35], v[188:191]\n"
"v_accvgpr_write_b32 a106, v122\n"
"v_bfe_u32 v248, v242, 8, 2\n"
"v_mfma_f32_16x16x32_bf16 v[176:179], v[212:215], a[36:39], v[176:179]\n"
"ds_read_b64_tr_b16 a[252:253], v233 offset:512\n"
"v_accvgpr_write_b32 a161, v17\n"
"v_mfma_f32_16x16x32_bf16 v[176:179], v[216:219], a[40:43], v[176:179]\n"
"ds_read_b64_tr_b16 v[224:225], v237 offset:512\n"
"v_accvgpr_write_b32 a160, v16\n"
"v_mfma_f32_16x16x32_bf16 v[176:179], v[220:223], a[44:47], v[176:179]\n"
"s_waitcnt lgkmcnt(3)\n"
"ds_read_b64_tr_b16 v[226:227], v237 offset:4608\n"
"v_accvgpr_write_b32 a163, v19\n"
"v_lshlrev_b32 v248, 6, v248\n"
"v_exp_f32 v152, v152\n"
"v_exp_f32 v163, v163\n"
"v_exp_f32 v169, v169\n"
"v_mfma_f32_16x16x32_bf16 a[224:227], v[124:127], a[244:247], 0\n"
"v_exp_f32 v156, v156\n"
"v_mfma_f32_16x16x32_bf16 v[16:19], v[128:131], a[244:247], 0\n"
"s_waitcnt vmcnt(8)\n"
"v_xor_b32 v248, v248, v242\n"
"v_bfe_u32 v254, v242, 6, 4\n"
"v_mfma_f32_16x16x32_bf16 v[16:19], v[132:135], a[248:251], v[16:19]\n"
"s_barrier\n"
"ds_read_b128 v[216:219], v228 offset:33280\n"
"v_mul_u32_u24 v249, 20, v249\n"
"v_cvt_pk_bf16_f32 v101, v158, v159\n"
"v_exp_f32 v176, v176\n"
"v_exp_f32 v177, v177\n"
"v_mfma_f32_16x16x32_bf16 v[48:51], v[136:139], a[244:247], 0\n"
"ds_read_b128 v[220:223], v229 offset:33280\n"
"v_cvt_pk_bf16_f32 v109, v162, v163\n"
"v_mfma_f32_16x16x32_bf16 v[48:51], v[140:143], a[248:251], v[48:51]\n"
"ds_read_b128 v[208:211], v228 offset:32768\n"
"ds_read_b128 v[212:215], v229 offset:32768\n"
"v_mfma_f32_16x16x32_bf16 v[80:83], v[144:147], a[244:247], 0\n"
"ds_read_b64_tr_b16 a[246:247], v234 offset:20480\n"
"v_cvt_pk_bf16_f32 v122, v152, v153\n"
"v_mfma_f32_16x16x32_bf16 v[80:83], v[148:151], a[248:251], v[80:83]\n"
"s_waitcnt lgkmcnt(5)\n"
"ds_read_b64_tr_b16 a[250:251], v238 offset:20480\n"
"v_cvt_pk_bf16_f32 v123, v154, v155\n"
"v_mfma_f32_16x16x32_bf16 v[84:87], v[148:151], v[224:227], 0\n"
"v_cvt_pk_bf16_f32 v116, v164, v165\n"
"v_cvt_pk_bf16_f32 v117, v166, v167\n"
"v_mfma_f32_16x16x32_bf16 v[84:87], v[144:147], a[252:255], v[84:87]\n"
"v_xor_b32 v249, v249, v254\n"
"v_cvt_pk_bf16_f32 v118, v176, v177\n"
"v_mfma_f32_16x16x32_bf16 v[52:55], v[140:143], v[224:227], 0\n"
"v_lshl_add_u32 v249, v249, 4, s44\n"
"v_cvt_pk_bf16_f32 v100, v156, v157\n"
"v_mfma_f32_16x16x32_bf16 v[52:55], v[136:139], a[252:255], v[52:55]\n"
"v_exp_f32 v161, v161\n"
"v_mfma_f32_16x16x32_bf16 v[20:23], v[132:135], v[224:227], 0\n"
"v_exp_f32 v174, v174\n"
"v_mfma_f32_16x16x32_bf16 v[20:23], v[128:131], a[252:255], v[20:23]\n"
"v_exp_f32 v168, v168\n"
"v_mfma_f32_16x16x32_bf16 a[228:231], v[124:127], a[252:255], 0\n"
"s_waitcnt lgkmcnt(1)\n"
"ds_read_b64_tr_b16 a[254:255], v235 offset:20480\n"
"s_branch LAY2_%=\n"
"LAL_%=:\n"
"v_mfma_f32_16x16x32_bf16 a[224:227], v[124:127], a[244:247], a[224:227]\n"
"v_exp_f32 v162, v162\n"
"v_mfma_f32_16x16x32_bf16 v[16:19], v[128:131], a[244:247], v[16:19]\n"
"s_waitcnt vmcnt(8)\n"
"v_exp_f32 v168, v168\n"
"v_mfma_f32_16x16x32_bf16 v[16:19], v[132:135], a[248:251], v[16:19]\n"
"s_barrier\n"
"v_mfma_f32_16x16x32_bf16 v[48:51], v[136:139], a[244:247], v[48:51]\n"
"ds_read_b128 v[208:211], v228 offset:0\n"
"v_cvt_pk_bf16_f32 v101, v158, v159\n"
"v_mfma_f32_16x16x32_bf16 v[48:51], v[140:143], a[248:251], v[48:51]\n"
"ds_read_b128 v[216:219], v228 offset:512\n"
"v_cvt_pk_bf16_f32 v109, v162, v163\n"
"v_mfma_f32_16x16x32_bf16 v[80:83], v[144:147], a[244:247], v[80:83]\n"
"ds_read_b128 v[220:223], v229 offset:512\n"
"v_cvt_pk_bf16_f32 v123, v154, v155\n"
"v_mfma_f32_16x16x32_bf16 v[80:83], v[148:151], a[248:251], v[80:83]\n"
"ds_read_b128 v[212:215], v229 offset:0\n"
"v_cvt_pk_bf16_f32 v117, v166, v167\n"
"v_mfma_f32_16x16x32_bf16 v[84:87], v[148:151], v[224:227], v[84:87]\n"
"v_exp_f32 v176, v176\n"
"v_mfma_f32_16x16x32_bf16 v[84:87], v[144:147], a[252:255], v[84:87]\n"
"v_exp_f32 v169, v169\n"
"v_mfma_f32_16x16x32_bf16 v[52:55], v[140:143], v[224:227], v[52:55]\n"
"ds_read_b64_tr_b16 a[246:247], v234 offset:53248\n"
"v_cvt_pk_bf16_f32 v116, v164, v165\n"
"v_mfma_f32_16x16x32_bf16 v[52:55], v[136:139], a[252:255], v[52:55]\n"
"v_exp_f32 v177, v177\n"
"v_mfma_f32_16x16x32_bf16 v[20:23], v[132:135], v[224:227], v[20:23]\n"
"ds_read_b64_tr_b16 a[250:251], v238 offset:53248\n"
"v_cvt_pk_bf16_f32 v118, v176, v177\n"
"v_mfma_f32_16x16x32_bf16 v[20:23], v[128:131], a[252:255], v[20:23]\n"
"v_exp_f32 v174, v174\n"
"v_mfma_f32_16x16x32_bf16 a[228:231], v[124:127], a[252:255], a[228:231]\n"
"s_waitcnt lgkmcnt(3)\n"
"ds_read_b64_tr_b16 a[254:255], v235 offset:53248\n"
"v_cvt_pk_bf16_f32 v100, v156, v157\n"
"v_mfma_f32_16x16x32_bf16 v[152:155], v[208:211], a[0:3], v[180:183]\n"
"s_waitcnt lgkmcnt(3)\n"
"ds_read_b64_tr_b16 a[248:249], v238 offset:49152\n"
"v_cvt_pk_bf16_f32 v102, v168, v169\n"
"v_mfma_f32_16x16x32_bf16 v[152:155], v[212:215], a[4:7], v[152:155]\n"
"v_exp_f32 v172, v172\n"
"v_mfma_f32_16x16x32_bf16 v[152:155], v[216:219], a[8:11], v[152:155]\n"
"ds_read_b64_tr_b16 a[244:245], v234 offset:49152\n"
"v_cvt_pk_bf16_f32 v108, v160, v161\n"
"v_mfma_f32_16x16x32_bf16 v[152:155], v[220:223], a[12:15], v[152:155]\n"
"v_exp_f32 v175, v175\n"
"v_mfma_f32_16x16x32_bf16 v[156:159], v[216:219], a[24:27], v[184:187]\n"
"ds_read_b64_tr_b16 a[252:253], v235 offset:49152\n"
"v_cvt_pk_bf16_f32 v111, v174, v175\n"
"v_mfma_f32_16x16x32_bf16 v[156:159], v[220:223], a[28:31], v[156:159]\n"
"ds_read_b64_tr_b16 v[224:225], v239 offset:49152\n"
"v_mfma_f32_16x16x32_bf16 v[156:159], v[208:211], a[16:19], v[156:159]\n"
"s_add_u32 m0, s44, 81920\n"
"ds_read_b64_tr_b16 v[226:227], v239 offset:53248\n"
"v_mfma_f32_16x16x32_bf16 v[156:159], v[212:215], a[20:23], v[156:159]\n"
"buffer_load_dwordx4 v240, s[20:23], s56 offen nt lds\n"
"v_mfma_f32_16x16x32_bf16 v[160:163], v[208:211], a[32:35], v[188:191]\n"
"v_exp_f32 v178, v178\n"
"v_mfma_f32_16x16x32_bf16 v[160:163], v[212:215], a[36:39], v[160:163]\n"
"v_exp_f32 v171, v171\n"
"v_mfma_f32_16x16x32_bf16 v[160:163], v[216:219], a[40:43], v[160:163]\n"
"s_add_u32 s40, s56, s52\n"
"v_mfma_f32_16x16x32_bf16 v[160:163], v[220:223], a[44:47], v[160:163]\n"
"buffer_load_dwordx4 v207, s[20:23], s40 offen offset:1024 nt lds\n"
"v_mfma_f32_16x16x32_bf16 v[164:167], v[216:219], a[200:203], v[192:195]\n"
"v_exp_f32 v170, v170\n"
"v_mfma_f32_16x16x32_bf16 v[164:167], v[220:223], a[204:207], v[164:167]\n"
"v_exp_f32 v153, v153\n"
"v_mfma_f32_16x16x32_bf16 v[164:167], v[208:211], a[192:195], v[164:167]\n"
"v_exp_f32 v179, v179\n"
"v_mfma_f32_16x16x32_bf16 v[164:167], v[212:215], a[196:199], v[164:167]\n"
"s_waitcnt lgkmcnt(0)\n"
"v_exp_f32 v158, v158\n"
"v_mfma_f32_16x16x32_bf16 a[216:219], v[120:123], a[244:247], a[216:219]\n"
"ds_read_b128 v[216:219], v228 offset:4608\n"
"v_cvt_pk_bf16_f32 v103, v170, v171\n"
"v_mfma_f32_16x16x32_bf16 v[8:11], v[96:99], a[244:247], v[8:11]\n"
"v_exp_f32 v173, v173\n"
"v_mfma_f32_16x16x32_bf16 v[8:11], v[100:103], a[248:251], v[8:11]\n"
"ds_read_b128 v[208:211], v228 offset:4096\n"
"v_cvt_pk_bf16_f32 v110, v172, v173\n"
"v_mfma_f32_16x16x32_bf16 v[40:43], v[104:107], a[244:247], v[40:43]\n"
"v_exp_f32 v162, v162\n"
"v_mfma_f32_16x16x32_bf16 v[40:43], v[108:111], a[248:251], v[40:43]\n"
"ds_read_b128 v[220:223], v229 offset:4608\n"
"v_cvt_pk_bf16_f32 v119, v178, v179\n"
"v_mfma_f32_16x16x32_bf16 v[72:75], v[112:115], a[244:247], v[72:75]\n"
"s_add_u32 s40, s56, s53\n"
"ds_read_b128 v[212:215], v229 offset:4096\n"
"v_mfma_f32_16x16x32_bf16 v[72:75], v[116:119], a[248:251], v[72:75]\n"
"buffer_load_dwordx4 v240, s[20:23], s40 offen offset:2048 nt lds\n"
"v_mfma_f32_16x16x32_bf16 v[76:79], v[116:119], v[224:227], v[76:79]\n"
"v_exp_f32 v163, v163\n"
"v_mfma_f32_16x16x32_bf16 v[76:79], v[112:115], a[252:255], v[76:79]\n"
"v_exp_f32 v156, v156\n"
"v_mfma_f32_16x16x32_bf16 v[44:47], v[108:111], v[224:227], v[44:47]\n"
"v_exp_f32 v160, v160\n"
"v_mfma_f32_16x16x32_bf16 v[44:47], v[104:107], a[252:255], v[44:47]\n"
"v_exp_f32 v155, v155\n"
"v_mfma_f32_16x16x32_bf16 v[12:15], v[100:103], v[224:227], v[12:15]\n"
"ds_read_b64_tr_b16 v[226:227], v239 offset:53760\n"
"v_mfma_f32_16x16x32_bf16 v[12:15], v[96:99], a[252:255], v[12:15]\n"
"ds_read_b64_tr_b16 a[248:249], v238 offset:49664\n"
"v_mfma_f32_16x16x32_bf16 a[220:223], v[120:123], a[252:255], a[220:223]\n"
"v_exp_f32 v154, v154\n"
"v_smfmac_f32_16x16x64_bf16 v[196:199], a[240:243], v[96:103], v241\n"
"ds_read_b64_tr_b16 a[250:251], v238 offset:53760\n"
"v_mfma_f32_16x16x32_bf16 v[202:205], a[240:243], v[120:123], v[202:205]\n"
"s_waitcnt lgkmcnt(3)\n"
"v_exp_f32 v152, v152\n"
"v_mfma_f32_16x16x32_bf16 v[168:171], v[216:219], a[8:11], v[180:183]\n"
"ds_read_b64_tr_b16 a[244:245], v234 offset:49664\n"
"v_cvt_pk_bf16_f32 v128, v152, v153\n"
"v_mfma_f32_16x16x32_bf16 v[168:171], v[220:223], a[12:15], v[168:171]\n"
"v_exp_f32 v165, v165\n"
"v_mfma_f32_16x16x32_bf16 v[168:171], v[208:211], a[0:3], v[168:171]\n"
"ds_read_b64_tr_b16 a[246:247], v234 offset:53760\n"
"v_cvt_pk_bf16_f32 v145, v162, v163\n"
"v_mfma_f32_16x16x32_bf16 v[168:171], v[212:215], a[4:7], v[168:171]\n"
"ds_read_b64_tr_b16 a[254:255], v235 offset:53760\n"
"v_mfma_f32_16x16x32_bf16 v[172:175], v[208:211], a[16:19], v[184:187]\n"
"ds_read_b64_tr_b16 v[224:225], v239 offset:49664\n"
"v_cvt_pk_bf16_f32 v129, v154, v155\n"
"v_mfma_f32_16x16x32_bf16 v[172:175], v[212:215], a[20:23], v[172:175]\n"
"v_mfma_f32_16x16x32_bf16 v[172:175], v[216:219], a[24:27], v[172:175]\n"
"s_add_u32 s40, s56, s54\n"
"ds_read_b64_tr_b16 a[252:253], v235 offset:49664\n"
"v_mfma_f32_16x16x32_bf16 v[172:175], v[220:223], a[28:31], v[172:175]\n"
"buffer_load_dwordx4 v207, s[20:23], s40 offen offset:3072 nt lds\n"
"v_mfma_f32_16x16x32_bf16 v[176:179], v[216:219], a[40:43], v[188:191]\n"
"s_add_u32 s55, s55, s59\n"
"v_exp_f32 v157, v157\n"
"v_mfma_f32_16x16x32_bf16 v[176:179], v[220:223], a[44:47], v[176:179]\n"
"s_or_b32 s55, s55, s58\n"
"v_exp_f32 v171, v171\n"
"v_mfma_f32_16x16x32_bf16 v[176:179], v[208:211], a[32:35], v[176:179]\n"
"s_xor_b32 s56, s55, s57\n"
"v_exp_f32 v159, v159\n"
"v_mfma_f32_16x16x32_bf16 v[176:179], v[212:215], a[36:39], v[176:179]\n"
"v_exp_f32 v167, v167\n"
"v_mfma_f32_16x16x32_bf16 v[152:155], v[208:211], a[192:195], v[192:195]\n"
"v_cvt_pk_bf16_f32 v137, v158, v159\n"
"v_mfma_f32_16x16x32_bf16 v[152:155], v[212:215], a[196:199], v[152:155]\n"
"ds_read_b128 v[208:211], v230 offset:0\n"
"v_cvt_pk_bf16_f32 v136, v156, v157\n"
"v_mfma_f32_16x16x32_bf16 v[152:155], v[216:219], a[200:203], v[152:155]\n"
"v_exp_f32 v166, v166\n"
"v_mfma_f32_16x16x32_bf16 v[152:155], v[220:223], a[204:207], v[152:155]\n"
"s_waitcnt lgkmcnt(1)\n"
"ds_read_b128 v[216:219], v230 offset:512\n"
"v_mfma_f32_16x16x32_bf16 a[232:235], v[120:123], a[244:247], a[232:235]\n"
"v_exp_f32 v169, v169\n"
"v_mfma_f32_16x16x32_bf16 v[24:27], v[96:99], a[244:247], v[24:27]\n"
"ds_read_b128 v[220:223], v231 offset:512\n"
"v_mfma_f32_16x16x32_bf16 v[24:27], v[100:103], a[248:251], v[24:27]\n"
"v_exp_f32 v164, v164\n"
"v_mfma_f32_16x16x32_bf16 v[56:59], v[104:107], a[244:247], v[56:59]\n"
"ds_read_b128 v[212:215], v231 offset:0\n"
"v_mfma_f32_16x16x32_bf16 v[56:59], v[108:111], a[248:251], v[56:59]\n"
"v_exp_f32 v178, v178\n"
"v_mfma_f32_16x16x32_bf16 v[88:91], v[112:115], a[244:247], v[88:91]\n"
"s_add_u32 m0, s44, 32768\n"
"ds_read_b64_tr_b16 a[246:247], v232 offset:53248\n"
"v_mfma_f32_16x16x32_bf16 v[88:91], v[116:119], a[248:251], v[88:91]\n"
"buffer_load_dwordx4 v240, s[16:19], s56 offen nt lds\n"
"v_mfma_f32_16x16x32_bf16 v[92:95], v[116:119], v[224:227], v[92:95]\n"
"v_exp_f32 v175, v175\n"
"v_mfma_f32_16x16x32_bf16 v[92:95], v[112:115], a[252:255], v[92:95]\n"
"v_exp_f32 v168, v168\n"
"v_mfma_f32_16x16x32_bf16 v[60:63], v[108:111], v[224:227], v[60:63]\n"
"v_exp_f32 v161, v161\n"
"v_mfma_f32_16x16x32_bf16 v[60:63], v[104:107], a[252:255], v[60:63]\n"
"v_exp_f32 v177, v177\n"
"v_mfma_f32_16x16x32_bf16 v[28:31], v[100:103], v[224:227], v[28:31]\n"
"ds_read_b64_tr_b16 v[226:227], v237 offset:53248\n"
"v_mfma_f32_16x16x32_bf16 v[28:31], v[96:99], a[252:255], v[28:31]\n"
"v_exp_f32 v172, v172\n"
"v_mfma_f32_16x16x32_bf16 a[236:239], v[120:123], a[252:255], a[236:239]\n"
"ds_read_b64_tr_b16 a[250:251], v236 offset:53248\n"
"v_cvt_pk_bf16_f32 v124, v164, v165\n"
"v_smfmac_f32_16x16x64_bf16 v[198:201], a[240:243], v[104:111], v241\n"
"s_waitcnt lgkmcnt(2)\n"
"ds_read_b64_tr_b16 a[252:253], v233 offset:49152\n"
"v_mfma_f32_16x16x32_bf16 v[156:159], v[208:211], a[0:3], v[180:183]\n"
"ds_read_b64_tr_b16 a[248:249], v236 offset:49152\n"
"v_cvt_pk_bf16_f32 v125, v166, v167\n"
"v_mfma_f32_16x16x32_bf16 v[156:159], v[212:215], a[4:7], v[156:159]\n"
"ds_read_b64_tr_b16 v[224:225], v237 offset:49152\n"
"v_cvt_pk_bf16_f32 v130, v168, v169\n"
"v_mfma_f32_16x16x32_bf16 v[156:159], v[216:219], a[8:11], v[156:159]\n"
"ds_read_b64_tr_b16 a[254:255], v233 offset:53248\n"
"v_cvt_pk_bf16_f32 v144, v160, v161\n"
"v_mfma_f32_16x16x32_bf16 v[156:159], v[220:223], a[12:15], v[156:159]\n"
"ds_read_b64_tr_b16 a[244:245], v232 offset:49152\n"
"v_mfma_f32_16x16x32_bf16 v[160:163], v[216:219], a[24:27], v[184:187]\n"
"v_exp_f32 v176, v176\n"
"v_mfma_f32_16x16x32_bf16 v[160:163], v[220:223], a[28:31], v[160:163]\n"
"v_exp_f32 v152, v152\n"
"v_mfma_f32_16x16x32_bf16 v[160:163], v[208:211], a[16:19], v[160:163]\n"
"s_add_u32 s40, s56, s52\n"
"v_cvt_pk_bf16_f32 v146, v176, v177\n"
"v_mfma_f32_16x16x32_bf16 v[160:163], v[212:215], a[20:23], v[160:163]\n"
"buffer_load_dwordx4 v207, s[16:19], s40 offen offset:1024 nt lds\n"
"v_mfma_f32_16x16x32_bf16 v[164:167], v[208:211], a[32:35], v[188:191]\n"
"v_exp_f32 v170, v170\n"
"v_mfma_f32_16x16x32_bf16 v[164:167], v[212:215], a[36:39], v[164:167]\n"
"v_exp_f32 v173, v173\n"
"v_mfma_f32_16x16x32_bf16 v[164:167], v[216:219], a[40:43], v[164:167]\n"
"v_exp_f32 v153, v153\n"
"v_mfma_f32_16x16x32_bf16 v[164:167], v[220:223], a[44:47], v[164:167]\n"
"s_waitcnt lgkmcnt(0)\n"
"v_exp_f32 v174, v174\n"
"v_mfma_f32_16x16x32_bf16 a[208:211], v[120:123], a[244:247], a[208:211]\n"
"ds_read_b128 v[208:211], v230 offset:4096\n"
"v_cvt_pk_bf16_f32 v139, v174, v175\n"
"v_mfma_f32_16x16x32_bf16 v[0:3], v[96:99], a[244:247], v[0:3]\n"
"v_exp_f32 v156, v156\n"
"v_mfma_f32_16x16x32_bf16 v[0:3], v[100:103], a[248:251], v[0:3]\n"
"ds_read_b128 v[220:223], v231 offset:4608\n"
"v_cvt_pk_bf16_f32 v138, v172, v173\n"
"v_mfma_f32_16x16x32_bf16 v[32:35], v[104:107], a[244:247], v[32:35]\n"
"v_exp_f32 v161, v161\n"
"v_mfma_f32_16x16x32_bf16 v[32:35], v[108:111], a[248:251], v[32:35]\n"
"ds_read_b128 v[216:219], v230 offset:4608\n"
"v_cvt_pk_bf16_f32 v126, v152, v153\n"
"v_mfma_f32_16x16x32_bf16 v[64:67], v[112:115], a[244:247], v[64:67]\n"
"s_add_u32 s40, s56, s53\n"
"ds_read_b128 v[212:215], v231 offset:4096\n"
"v_mfma_f32_16x16x32_bf16 v[64:67], v[116:119], a[248:251], v[64:67]\n"
"buffer_load_dwordx4 v240, s[16:19], s40 offen offset:2048 nt lds\n"
"v_mfma_f32_16x16x32_bf16 v[68:71], v[116:119], v[224:227], v[68:71]\n"
"v_exp_f32 v159, v159\n"
"v_mfma_f32_16x16x32_bf16 v[68:71], v[112:115], a[252:255], v[68:71]\n"
"v_exp_f32 v155, v155\n"
"v_mfma_f32_16x16x32_bf16 v[36:39], v[108:111], v[224:227], v[36:39]\n"
"v_exp_f32 v160, v160\n"
"v_mfma_f32_16x16x32_bf16 v[36:39], v[104:107], a[252:255], v[36:39]\n"
"v_exp_f32 v179, v179\n"
"v_mfma_f32_16x16x32_bf16 v[4:7], v[100:103], v[224:227], v[4:7]\n"
"ds_read_b64_tr_b16 a[244:245], v232 offset:49664\n"
"ds_read_b64_tr_b16 v[224:225], v237 offset:49664\n"
"v_mfma_f32_16x16x32_bf16 v[4:7], v[96:99], a[252:255], v[4:7]\n"
"v_exp_f32 v158, v158\n"
"v_mfma_f32_16x16x32_bf16 a[212:215], v[120:123], a[252:255], a[212:215]\n"
"ds_read_b64_tr_b16 a[246:247], v232 offset:53760\n"
"ds_read_b64_tr_b16 a[250:251], v236 offset:53760\n"
"v_smfmac_f32_16x16x64_bf16 v[200:203], a[240:243], v[112:119], v241\n"
"s_waitcnt lgkmcnt(4)\n"
"ds_read_b64_tr_b16 v[226:227], v237 offset:53760\n"
"v_cvt_pk_bf16_f32 v131, v170, v171\n"
"v_mfma_f32_16x16x32_bf16 v[168:171], v[208:211], a[0:3], v[180:183]\n"
"v_exp_f32 v164, v164\n"
"v_mfma_f32_16x16x32_bf16 v[168:171], v[212:215], a[4:7], v[168:171]\n"
"ds_read_b64_tr_b16 a[248:249], v236 offset:49664\n"
"ds_read_b64_tr_b16 a[254:255], v233 offset:53760\n"
"v_mfma_f32_16x16x32_bf16 v[168:171], v[216:219], a[8:11], v[168:171]\n"
"s_add_u32 s40, s56, s54\n"
"ds_read_b64_tr_b16 a[252:253], v233 offset:49664\n"
"v_mfma_f32_16x16x32_bf16 v[168:171], v[220:223], a[12:15], v[168:171]\n"
"buffer_load_dwordx4 v207, s[16:19], s40 offen offset:3072 nt lds\n"
"v_mfma_f32_16x16x32_bf16 v[172:175], v[216:219], a[24:27], v[184:187]\n"
"v_exp_f32 v154, v154\n"
"v_mfma_f32_16x16x32_bf16 v[172:175], v[220:223], a[28:31], v[172:175]\n"
"v_exp_f32 v167, v167\n"
"v_mfma_f32_16x16x32_bf16 v[172:175], v[208:211], a[16:19], v[172:175]\n"
"v_exp_f32 v163, v163\n"
"v_mfma_f32_16x16x32_bf16 v[172:175], v[212:215], a[20:23], v[172:175]\n"
"s_cbranch_vccnz LPCL0_%=\nLPKL0_%=:\n"
"v_cvt_pk_bf16_f32 v147, v178, v179\n"
"v_mfma_f32_16x16x32_bf16 v[176:179], v[208:211], a[32:35], v[188:191]\n"
"v_exp_f32 v165, v165\n"
"v_mfma_f32_16x16x32_bf16 v[176:179], v[212:215], a[36:39], v[176:179]\n"
"v_exp_f32 v157, v157\n"
"v_mfma_f32_16x16x32_bf16 v[176:179], v[216:219], a[40:43], v[176:179]\n"
"v_exp_f32 v166, v166\n"
"v_mfma_f32_16x16x32_bf16 v[176:179], v[220:223], a[44:47], v[176:179]\n"
"s_waitcnt lgkmcnt(0)\n"
"v_mfma_f32_16x16x32_bf16 a[224:227], v[120:123], a[244:247], a[224:227]\n"
"v_exp_f32 v162, v162\n"
"v_mfma_f32_16x16x32_bf16 v[16:19], v[96:99], a[244:247], v[16:19]\n"
"s_waitcnt vmcnt(8)\n"
"v_exp_f32 v168, v168\n"
"v_mfma_f32_16x16x32_bf16 v[16:19], v[100:103], a[248:251], v[16:19]\n"
"s_barrier\n"
"v_mfma_f32_16x16x32_bf16 v[48:51], v[104:107], a[244:247], v[48:51]\n"
"ds_read_b128 v[208:211], v228 offset:16384\n"
"v_cvt_pk_bf16_f32 v133, v158, v159\n"
"v_mfma_f32_16x16x32_bf16 v[48:51], v[108:111], a[248:251], v[48:51]\n"
"ds_read_b128 v[216:219], v228 offset:16896\n"
"v_cvt_pk_bf16_f32 v141, v162, v163\n"
"v_mfma_f32_16x16x32_bf16 v[80:83], v[112:115], a[244:247], v[80:83]\n"
"ds_read_b128 v[220:223], v229 offset:16896\n"
"v_cvt_pk_bf16_f32 v127, v154, v155\n"
"v_mfma_f32_16x16x32_bf16 v[80:83], v[116:119], a[248:251], v[80:83]\n"
"ds_read_b128 v[212:215], v229 offset:16384\n"
"v_cvt_pk_bf16_f32 v149, v166, v167\n"
"v_mfma_f32_16x16x32_bf16 v[84:87], v[116:119], v[224:227], v[84:87]\n"
"v_exp_f32 v176, v176\n"
"v_mfma_f32_16x16x32_bf16 v[84:87], v[112:115], a[252:255], v[84:87]\n"
"v_exp_f32 v169, v169\n"
"v_mfma_f32_16x16x32_bf16 v[52:55], v[108:111], v[224:227], v[52:55]\n"
"ds_read_b64_tr_b16 a[246:247], v234 offset:4096\n"
"v_cvt_pk_bf16_f32 v148, v164, v165\n"
"v_mfma_f32_16x16x32_bf16 v[52:55], v[104:107], a[252:255], v[52:55]\n"
"v_exp_f32 v177, v177\n"
"v_mfma_f32_16x16x32_bf16 v[20:23], v[100:103], v[224:227], v[20:23]\n"
"ds_read_b64_tr_b16 a[250:251], v238 offset:4096\n"
"v_cvt_pk_bf16_f32 v150, v176, v177\n"
"v_mfma_f32_16x16x32_bf16 v[20:23], v[96:99], a[252:255], v[20:23]\n"
"v_exp_f32 v174, v174\n"
"v_mfma_f32_16x16x32_bf16 a[228:231], v[120:123], a[252:255], a[228:231]\n"
"s_waitcnt lgkmcnt(3)\n"
"ds_read_b64_tr_b16 a[254:255], v235 offset:4096\n"
"v_cvt_pk_bf16_f32 v132, v156, v157\n"
"v_mfma_f32_16x16x32_bf16 v[152:155], v[208:211], a[0:3], v[180:183]\n"
"s_waitcnt lgkmcnt(3)\n"
"ds_read_b64_tr_b16 a[248:249], v238 offset:0\n"
"v_cvt_pk_bf16_f32 v134, v168, v169\n"
"v_mfma_f32_16x16x32_bf16 v[152:155], v[212:215], a[4:7], v[152:155]\n"
"v_exp_f32 v172, v172\n"
"v_mfma_f32_16x16x32_bf16 v[152:155], v[216:219], a[8:11], v[152:155]\n"
"ds_read_b64_tr_b16 a[244:245], v234 offset:0\n"
"v_cvt_pk_bf16_f32 v140, v160, v161\n"
"v_mfma_f32_16x16x32_bf16 v[152:155], v[220:223], a[12:15], v[152:155]\n"
"v_exp_f32 v175, v175\n"
"v_mfma_f32_16x16x32_bf16 v[156:159], v[216:219], a[24:27], v[184:187]\n"
"ds_read_b64_tr_b16 a[252:253], v235 offset:0\n"
"v_cvt_pk_bf16_f32 v143, v174, v175\n"
"v_mfma_f32_16x16x32_bf16 v[156:159], v[220:223], a[28:31], v[156:159]\n"
"ds_read_b64_tr_b16 v[224:225], v239 offset:0\n"
"v_mfma_f32_16x16x32_bf16 v[156:159], v[208:211], a[16:19], v[156:159]\n"
"s_add_u32 m0, s44, 98304\n"
"ds_read_b64_tr_b16 v[226:227], v239 offset:4096\n"
"v_mfma_f32_16x16x32_bf16 v[156:159], v[212:215], a[20:23], v[156:159]\n"
"buffer_load_dwordx4 v240, s[20:23], s56 offen nt lds\n"
"v_mfma_f32_16x16x32_bf16 v[160:163], v[208:211], a[32:35], v[188:191]\n"
"v_exp_f32 v178, v178\n"
"v_mfma_f32_16x16x32_bf16 v[160:163], v[212:215], a[36:39], v[160:163]\n"
"v_exp_f32 v171, v171\n"
"v_mfma_f32_16x16x32_bf16 v[160:163], v[216:219], a[40:43], v[160:163]\n"
"s_add_u32 s40, s56, s52\n"
"v_mfma_f32_16x16x32_bf16 v[160:163], v[220:223], a[44:47], v[160:163]\n"
"buffer_load_dwordx4 v207, s[20:23], s40 offen offset:1024 nt lds\n"
"v_mfma_f32_16x16x32_bf16 v[164:167], v[216:219], a[200:203], v[192:195]\n"
"v_exp_f32 v170, v170\n"
"v_mfma_f32_16x16x32_bf16 v[164:167], v[220:223], a[204:207], v[164:167]\n"
"v_exp_f32 v153, v153\n"
"v_mfma_f32_16x16x32_bf16 v[164:167], v[208:211], a[192:195], v[164:167]\n"
"v_exp_f32 v179, v179\n"
"v_mfma_f32_16x16x32_bf16 v[164:167], v[212:215], a[196:199], v[164:167]\n"
"s_waitcnt lgkmcnt(0)\n"
"v_exp_f32 v158, v158\n"
"v_mfma_f32_16x16x32_bf16 a[216:219], v[124:127], a[244:247], a[216:219]\n"
"ds_read_b128 v[216:219], v228 offset:20992\n"
"v_cvt_pk_bf16_f32 v135, v170, v171\n"
"v_mfma_f32_16x16x32_bf16 v[8:11], v[128:131], a[244:247], v[8:11]\n"
"v_exp_f32 v173, v173\n"
"v_mfma_f32_16x16x32_bf16 v[8:11], v[132:135], a[248:251], v[8:11]\n"
"ds_read_b128 v[208:211], v228 offset:20480\n"
"v_cvt_pk_bf16_f32 v142, v172, v173\n"
"v_mfma_f32_16x16x32_bf16 v[40:43], v[136:139], a[244:247], v[40:43]\n"
"v_exp_f32 v162, v162\n"
"v_mfma_f32_16x16x32_bf16 v[40:43], v[140:143], a[248:251], v[40:43]\n"
"ds_read_b128 v[220:223], v229 offset:20992\n"
"v_cvt_pk_bf16_f32 v151, v178, v179\n"
"v_mfma_f32_16x16x32_bf16 v[72:75], v[144:147], a[244:247], v[72:75]\n"
"s_add_u32 s40, s56, s53\n"
"ds_read_b128 v[212:215], v229 offset:20480\n"
"v_mfma_f32_16x16x32_bf16 v[72:75], v[148:151], a[248:251], v[72:75]\n"
"buffer_load_dwordx4 v240, s[20:23], s40 offen offset:2048 nt lds\n"
"v_mfma_f32_16x16x32_bf16 v[76:79], v[148:151], v[224:227], v[76:79]\n"
"v_exp_f32 v163, v163\n"
"v_mfma_f32_16x16x32_bf16 v[76:79], v[144:147], a[252:255], v[76:79]\n"
"v_exp_f32 v156, v156\n"
"v_mfma_f32_16x16x32_bf16 v[44:47], v[140:143], v[224:227], v[44:47]\n"
"v_exp_f32 v160, v160\n"
"v_mfma_f32_16x16x32_bf16 v[44:47], v[136:139], a[252:255], v[44:47]\n"
"v_exp_f32 v155, v155\n"
"v_mfma_f32_16x16x32_bf16 v[12:15], v[132:135], v[224:227], v[12:15]\n"
"ds_read_b64_tr_b16 v[226:227], v239 offset:4608\n"
"v_mfma_f32_16x16x32_bf16 v[12:15], v[128:131], a[252:255], v[12:15]\n"
"ds_read_b64_tr_b16 a[248:249], v238 offset:512\n"
"v_mfma_f32_16x16x32_bf16 a[220:223], v[124:127], a[252:255], a[220:223]\n"
"v_exp_f32 v154, v154\n"
"v_smfmac_f32_16x16x64_bf16 v[196:199], a[240:243], v[128:135], v241\n"
"ds_read_b64_tr_b16 a[250:251], v238 offset:4608\n"
"v_mfma_f32_16x16x32_bf16 v[202:205], a[240:243], v[124:127], v[202:205]\n"
"s_waitcnt lgkmcnt(3)\n"
"v_exp_f32 v152, v152\n"
"v_mfma_f32_16x16x32_bf16 v[168:171], v[216:219], a[8:11], v[180:183]\n"
"ds_read_b64_tr_b16 a[244:245], v234 offset:512\n"
"v_cvt_pk_bf16_f32 v96, v152, v153\n"
"v_mfma_f32_16x16x32_bf16 v[168:171], v[220:223], a[12:15], v[168:171]\n"
"v_exp_f32 v165, v165\n"
"v_mfma_f32_16x16x32_bf16 v[168:171], v[208:211], a[0:3], v[168:171]\n"
"ds_read_b64_tr_b16 a[246:247], v234 offset:4608\n"
"v_cvt_pk_bf16_f32 v113, v162, v163\n"
"v_mfma_f32_16x16x32_bf16 v[168:171], v[212:215], a[4:7], v[168:171]\n"
"ds_read_b64_tr_b16 a[254:255], v235 offset:4608\n"
"v_mfma_f32_16x16x32_bf16 v[172:175], v[208:211], a[16:19], v[184:187]\n"
"ds_read_b64_tr_b16 v[224:225], v239 offset:512\n"
"v_cvt_pk_bf16_f32 v97, v154, v155\n"
"v_mfma_f32_16x16x32_bf16 v[172:175], v[212:215], a[20:23], v[172:175]\n"
"v_mfma_f32_16x16x32_bf16 v[172:175], v[216:219], a[24:27], v[172:175]\n"
"s_add_u32 s40, s56, s54\n"
"ds_read_b64_tr_b16 a[252:253], v235 offset:512\n"
"v_mfma_f32_16x16x32_bf16 v[172:175], v[220:223], a[28:31], v[172:175]\n"
"buffer_load_dwordx4 v207, s[20:23], s40 offen offset:3072 nt lds\n"
"v_mfma_f32_16x16x32_bf16 v[176:179], v[216:219], a[40:43], v[188:191]\n"
"s_add_u32 s55, s55, s59\n"
"v_exp_f32 v157, v157\n"
"v_mfma_f32_16x16x32_bf16 v[176:179], v[220:223], a[44:47], v[176:179]\n"
"s_or_b32 s55, s55, s58\n"
"v_exp_f32 v171, v171\n"
"v_mfma_f32_16x16x32_bf16 v[176:179], v[208:211], a[32:35], v[176:179]\n"
"s_xor_b32 s56, s55, s57\n"
"v_exp_f32 v159, v159\n"
"v_mfma_f32_16x16x32_bf16 v[176:179], v[212:215], a[36:39], v[176:179]\n"
"v_exp_f32 v167, v167\n"
"v_mfma_f32_16x16x32_bf16 v[152:155], v[208:211], a[192:195], v[192:195]\n"
"v_cvt_pk_bf16_f32 v105, v158, v159\n"
"v_mfma_f32_16x16x32_bf16 v[152:155], v[212:215], a[196:199], v[152:155]\n"
"ds_read_b128 v[208:211], v230 offset:16384\n"
"v_cvt_pk_bf16_f32 v104, v156, v157\n"
"v_mfma_f32_16x16x32_bf16 v[152:155], v[216:219], a[200:203], v[152:155]\n"
"v_exp_f32 v166, v166\n"
"v_mfma_f32_16x16x32_bf16 v[152:155], v[220:223], a[204:207], v[152:155]\n"
"s_waitcnt lgkmcnt(1)\n"
"ds_read_b128 v[216:219], v230 offset:16896\n"
"v_mfma_f32_16x16x32_bf16 a[232:235], v[124:127], a[244:247], a[232:235]\n"
"v_exp_f32 v169, v169\n"
"v_mfma_f32_16x16x32_bf16 v[24:27], v[128:131], a[244:247], v[24:27]\n"
"ds_read_b128 v[220:223], v231 offset:16896\n"
"v_mfma_f32_16x16x32_bf16 v[24:27], v[132:135], a[248:251], v[24:27]\n"
"v_exp_f32 v164, v164\n"
"v_mfma_f32_16x16x32_bf16 v[56:59], v[136:139], a[244:247], v[56:59]\n"
"ds_read_b128 v[212:215], v231 offset:16384\n"
"v_mfma_f32_16x16x32_bf16 v[56:59], v[140:143], a[248:251], v[56:59]\n"
"v_exp_f32 v178, v178\n"
"v_mfma_f32_16x16x32_bf16 v[88:91], v[144:147], a[244:247], v[88:91]\n"
"s_add_u32 m0, s44, 49152\n"
"ds_read_b64_tr_b16 a[246:247], v232 offset:4096\n"
"v_mfma_f32_16x16x32_bf16 v[88:91], v[148:151], a[248:251], v[88:91]\n"
"buffer_load_dwordx4 v240, s[16:19], s56 offen nt lds\n"
"v_mfma_f32_16x16x32_bf16 v[92:95], v[148:151], v[224:227], v[92:95]\n"
"v_exp_f32 v175, v175\n"
"v_mfma_f32_16x16x32_bf16 v[92:95], v[144:147], a[252:255], v[92:95]\n"
"v_exp_f32 v168, v168\n"
"v_mfma_f32_16x16x32_bf16 v[60:63], v[140:143], v[224:227], v[60:63]\n"
"v_exp_f32 v161, v161\n"
"v_mfma_f32_16x16x32_bf16 v[60:63], v[136:139], a[252:255], v[60:63]\n"
"v_exp_f32 v177, v177\n"
"v_mfma_f32_16x16x32_bf16 v[28:31], v[132:135], v[224:227], v[28:31]\n"
"ds_read_b64_tr_b16 v[226:227], v237 offset:4096\n"
"v_mfma_f32_16x16x32_bf16 v[28:31], v[128:131], a[252:255], v[28:31]\n"
"v_exp_f32 v172, v172\n"
"v_mfma_f32_16x16x32_bf16 a[236:239], v[124:127], a[252:255], a[236:239]\n"
"ds_read_b64_tr_b16 a[250:251], v236 offset:4096\n"
"v_cvt_pk_bf16_f32 v120, v164, v165\n"
"v_smfmac_f32_16x16x64_bf16 v[198:201], a[240:243], v[136:143], v241\n"
"s_waitcnt lgkmcnt(2)\n"
"ds_read_b64_tr_b16 a[252:253], v233 offset:0\n"
"v_mfma_f32_16x16x32_bf16 v[156:159], v[208:211], a[0:3], v[180:183]\n"
"ds_read_b64_tr_b16 a[248:249], v236 offset:0\n"
"v_cvt_pk_bf16_f32 v121, v166, v167\n"
"v_mfma_f32_16x16x32_bf16 v[156:159], v[212:215], a[4:7], v[156:159]\n"
"ds_read_b64_tr_b16 v[224:225], v237 offset:0\n"
"v_cvt_pk_bf16_f32 v98, v168, v169\n"
"v_mfma_f32_16x16x32_bf16 v[156:159], v[216:219], a[8:11], v[156:159]\n"
"ds_read_b64_tr_b16 a[254:255], v233 offset:4096\n"
"v_cvt_pk_bf16_f32 v112, v160, v161\n"
"v_mfma_f32_16x16x32_bf16 v[156:159], v[220:223], a[12:15], v[156:159]\n"
"ds_read_b64_tr_b16 a[244:245], v232 offset:0\n"
"v_mfma_f32_16x16x32_bf16 v[160:163], v[216:219], a[24:27], v[184:187]\n"
"v_exp_f32 v176, v176\n"
"v_mfma_f32_16x16x32_bf16 v[160:163], v[220:223], a[28:31], v[160:163]\n"
"v_exp_f32 v152, v152\n"
"v_mfma_f32_16x16x32_bf16 v[160:163], v[208:211], a[16:19], v[160:163]\n"
"s_add_u32 s40, s56, s52\n"
"v_cvt_pk_bf16_f32 v114, v176, v177\n"
"v_mfma_f32_16x16x32_bf16 v[160:163], v[212:215], a[20:23], v[160:163]\n"
"buffer_load_dwordx4 v207, s[16:19], s40 offen offset:1024 nt lds\n"
"v_mfma_f32_16x16x32_bf16 v[164:167], v[208:211], a[32:35], v[188:191]\n"
"v_exp_f32 v170, v170\n"
"v_mfma_f32_16x16x32_bf16 v[164:167], v[212:215], a[36:39], v[164:167]\n"
"v_exp_f32 v173, v173\n"
"v_mfma_f32_16x16x32_bf16 v[164:167], v[216:219], a[40:43], v[164:167]\n"
"v_exp_f32 v153, v153\n"
"v_mfma_f32_16x16x32_bf16 v[164:167], v[220:223], a[44:47], v[164:167]\n"
"s_waitcnt lgkmcnt(0)\n"
"v_exp_f32 v174, v174\n"
"v_mfma_f32_16x16x32_bf16 a[208:211], v[124:127], a[244:247], a[208:211]\n"
"ds_read_b128 v[208:211], v230 offset:20480\n"
"v_cvt_pk_bf16_f32 v107, v174, v175\n"
"v_mfma_f32_16x16x32_bf16 v[0:3], v[128:131], a[244:247], v[0:3]\n"
"v_exp_f32 v156, v156\n"
"v_mfma_f32_16x16x32_bf16 v[0:3], v[132:135], a[248:251], v[0:3]\n"
"ds_read_b128 v[220:223], v231 offset:20992\n"
"v_cvt_pk_bf16_f32 v106, v172, v173\n"
"v_mfma_f32_16x16x32_bf16 v[32:35], v[136:139], a[244:247], v[32:35]\n"
"v_exp_f32 v161, v161\n"
"v_mfma_f32_16x16x32_bf16 v[32:35], v[140:143], a[248:251], v[32:35]\n"
"ds_read_b128 v[216:219], v230 offset:20992\n"
"v_cvt_pk_bf16_f32 v122, v152, v153\n"
"v_mfma_f32_16x16x32_bf16 v[64:67], v[144:147], a[244:247], v[64:67]\n"
"s_add_u32 s40, s56, s53\n"
"ds_read_b128 v[212:215], v231 offset:20480\n"
"v_mfma_f32_16x16x32_bf16 v[64:67], v[148:151], a[248:251], v[64:67]\n"
"buffer_load_dwordx4 v240, s[16:19], s40 offen offset:2048 nt lds\n"
"v_mfma_f32_16x16x32_bf16 v[68:71], v[148:151], v[224:227], v[68:71]\n"
"v_exp_f32 v159, v159\n"
"v_mfma_f32_16x16x32_bf16 v[68:71], v[144:147], a[252:255], v[68:71]\n"
"v_exp_f32 v155, v155\n"
"v_mfma_f32_16x16x32_bf16 v[36:39], v[140:143], v[224:227], v[36:39]\n"
"v_exp_f32 v160, v160\n"
"v_mfma_f32_16x16x32_bf16 v[36:39], v[136:139], a[252:255], v[36:39]\n"
"v_exp_f32 v179, v179\n"
"v_mfma_f32_16x16x32_bf16 v[4:7], v[132:135], v[224:227], v[4:7]\n"
"ds_read_b64_tr_b16 a[244:245], v232 offset:512\n"
"ds_read_b64_tr_b16 v[224:225], v237 offset:512\n"
"v_mfma_f32_16x16x32_bf16 v[4:7], v[128:131], a[252:255], v[4:7]\n"
"v_exp_f32 v158, v158\n"
"v_mfma_f32_16x16x32_bf16 a[212:215], v[124:127], a[252:255], a[212:215]\n"
"ds_read_b64_tr_b16 a[246:247], v232 offset:4608\n"
"ds_read_b64_tr_b16 a[250:251], v236 offset:4608\n"
"v_smfmac_f32_16x16x64_bf16 v[200:203], a[240:243], v[144:151], v241\n"
"s_waitcnt lgkmcnt(4)\n"
"ds_read_b64_tr_b16 v[226:227], v237 offset:4608\n"
"v_cvt_pk_bf16_f32 v99, v170, v171\n"
"v_mfma_f32_16x16x32_bf16 v[168:171], v[208:211], a[0:3], v[180:183]\n"
"v_exp_f32 v164, v164\n"
"v_mfma_f32_16x16x32_bf16 v[168:171], v[212:215], a[4:7], v[168:171]\n"
"ds_read_b64_tr_b16 a[248:249], v236 offset:512\n"
"ds_read_b64_tr_b16 a[254:255], v233 offset:4608\n"
"v_mfma_f32_16x16x32_bf16 v[168:171], v[216:219], a[8:11], v[168:171]\n"
"s_add_u32 s40, s56, s54\n"
"ds_read_b64_tr_b16 a[252:253], v233 offset:512\n"
"v_mfma_f32_16x16x32_bf16 v[168:171], v[220:223], a[12:15], v[168:171]\n"
"buffer_load_dwordx4 v207, s[16:19], s40 offen offset:3072 nt lds\n"
"v_mfma_f32_16x16x32_bf16 v[172:175], v[216:219], a[24:27], v[184:187]\n"
"v_exp_f32 v154, v154\n"
"v_mfma_f32_16x16x32_bf16 v[172:175], v[220:223], a[28:31], v[172:175]\n"
"v_exp_f32 v167, v167\n"
"v_mfma_f32_16x16x32_bf16 v[172:175], v[208:211], a[16:19], v[172:175]\n"
"v_exp_f32 v163, v163\n"
"v_mfma_f32_16x16x32_bf16 v[172:175], v[212:215], a[20:23], v[172:175]\n"
"s_cbranch_vccnz LPCL1_%=\nLPKL1_%=:\n"
"v_cvt_pk_bf16_f32 v115, v178, v179\n"
"v_mfma_f32_16x16x32_bf16 v[176:179], v[208:211], a[32:35], v[188:191]\n"
"v_exp_f32 v165, v165\n"
"v_mfma_f32_16x16x32_bf16 v[176:179], v[212:215], a[36:39], v[176:179]\n"
"v_exp_f32 v157, v157\n"
"v_mfma_f32_16x16x32_bf16 v[176:179], v[216:219], a[40:43], v[176:179]\n"
"v_exp_f32 v166, v166\n"
"v_mfma_f32_16x16x32_bf16 v[176:179], v[220:223], a[44:47], v[176:179]\n"
"s_waitcnt lgkmcnt(0)\n"
"v_mfma_f32_16x16x32_bf16 a[224:227], v[124:127], a[244:247], a[224:227]\n"
"v_exp_f32 v162, v162\n"
"v_mfma_f32_16x16x32_bf16 v[16:19], v[128:131], a[244:247], v[16:19]\n"
"s_waitcnt vmcnt(8)\n"
"v_exp_f32 v168, v168\n"
"v_mfma_f32_16x16x32_bf16 v[16:19], v[132:135], a[248:251], v[16:19]\n"
"s_barrier\n"
"v_mfma_f32_16x16x32_bf16 v[48:51], v[136:139], a[244:247], v[48:51]\n"
"ds_read_b128 v[208:211], v228 offset:32768\n"
"v_cvt_pk_bf16_f32 v101, v158, v159\n"
"v_mfma_f32_16x16x32_bf16 v[48:51], v[140:143], a[248:251], v[48:51]\n"
"ds_read_b128 v[216:219], v228 offset:33280\n"
"v_cvt_pk_bf16_f32 v109, v162, v163\n"
"v_mfma_f32_16x16x32_bf16 v[80:83], v[144:147], a[244:247], v[80:83]\n"
"ds_read_b128 v[220:223], v229 offset:33280\n"
"v_cvt_pk_bf16_f32 v123, v154, v155\n"
"v_mfma_f32_16x16x32_bf16 v[80:83], v[148:151], a[248:251], v[80:83]\n"
"ds_read_b128 v[212:215], v229 offset:32768\n"
"v_cvt_pk_bf16_f32 v117, v166, v167\n"
"v_mfma_f32_16x16x32_bf16 v[84:87], v[148:151], v[224:227], v[84:87]\n"
"v_exp_f32 v176, v176\n"
"v_mfma_f32_16x16x32_bf16 v[84:87], v[144:147], a[252:255], v[84:87]\n"
"v_exp_f32 v169, v169\n"
"v_mfma_f32_16x16x32_bf16 v[52:55], v[140:143], v[224:227], v[52:55]\n"
"ds_read_b64_tr_b16 a[246:247], v234 offset:20480\n"
"v_cvt_pk_bf16_f32 v116, v164, v165\n"
"v_mfma_f32_16x16x32_bf16 v[52:55], v[136:139], a[252:255], v[52:55]\n"
"v_exp_f32 v177, v177\n"
"v_mfma_f32_16x16x32_bf16 v[20:23], v[132:135], v[224:227], v[20:23]\n"
"ds_read_b64_tr_b16 a[250:251], v238 offset:20480\n"
"v_cvt_pk_bf16_f32 v118, v176, v177\n"
"v_mfma_f32_16x16x32_bf16 v[20:23], v[128:131], a[252:255], v[20:23]\n"
"v_exp_f32 v174, v174\n"
"v_mfma_f32_16x16x32_bf16 a[228:231], v[124:127], a[252:255], a[228:231]\n"
"s_waitcnt lgkmcnt(3)\n"
"ds_read_b64_tr_b16 a[254:255], v235 offset:20480\n"
"v_cvt_pk_bf16_f32 v100, v156, v157\n"
"LAY2_%=:\n"
"v_mfma_f32_16x16x32_bf16 v[152:155], v[208:211], a[0:3], v[180:183]\n"
"s_waitcnt lgkmcnt(3)\n"
"ds_read_b64_tr_b16 a[248:249], v238 offset:16384\n"
"v_cvt_pk_bf16_f32 v102, v168, v169\n"
"v_mfma_f32_16x16x32_bf16 v[152:155], v[212:215], a[4:7], v[152:155]\n"
"v_exp_f32 v172, v172\n"
"v_mfma_f32_16x16x32_bf16 v[152:155], v[216:219], a[8:11], v[152:155]\n"
"ds_read_b64_tr_b16 a[244:245], v234 offset:16384\n"
"v_cvt_pk_bf16_f32 v108, v160, v161\n"
"v_mfma_f32_16x16x32_bf16 v[152:155], v[220:223], a[12:15], v[152:155]\n"
"v_exp_f32 v175, v175\n"
"v_mfma_f32_16x16x32_bf16 v[156:159], v[216:219], a[24:27], v[184:187]\n"
"ds_read_b64_tr_b16 a[252:253], v235 offset:16384\n"
"v_cvt_pk_bf16_f32 v111, v174, v175\n"
"v_mfma_f32_16x16x32_bf16 v[156:159], v[220:223], a[28:31], v[156:159]\n"
"ds_read_b64_tr_b16 v[224:225], v239 offset:16384\n"
"v_mfma_f32_16x16x32_bf16 v[156:159], v[208:211], a[16:19], v[156:159]\n"
"s_add_u32 m0, s44, 114688\n"
"ds_read_b64_tr_b16 v[226:227], v239 offset:20480\n"
"v_mfma_f32_16x16x32_bf16 v[156:159], v[212:215], a[20:23], v[156:159]\n"
"buffer_load_dwordx4 v240, s[20:23], s56 offen nt lds\n"
"v_mfma_f32_16x16x32_bf16 v[160:163], v[208:211], a[32:35], v[188:191]\n"
"v_exp_f32 v178, v178\n"
"v_mfma_f32_16x16x32_bf16 v[160:163], v[212:215], a[36:39], v[160:163]\n"
"v_exp_f32 v171, v171\n"
"v_mfma_f32_16x16x32_bf16 v[160:163], v[216:219], a[40:43], v[160:163]\n"
"s_add_u32 s40, s56, s52\n"
"v_mfma_f32_16x16x32_bf16 v[160:163], v[220:223], a[44:47], v[160:163]\n"
"buffer_load_dwordx4 v207, s[20:23], s40 offen offset:1024 nt lds\n"
"v_mfma_f32_16x16x32_bf16 v[164:167], v[216:219], a[200:203], v[192:195]\n"
"v_exp_f32 v170, v170\n"
"v_mfma_f32_16x16x32_bf16 v[164:167], v[220:223], a[204:207], v[164:167]\n"
"v_exp_f32 v153, v153\n"
"v_mfma_f32_16x16x32_bf16 v[164:167], v[208:211], a[192:195], v[164:167]\n"
"v_exp_f32 v179, v179\n"
"v_mfma_f32_16x16x32_bf16 v[164:167], v[212:215], a[196:199], v[164:167]\n"
"s_waitcnt lgkmcnt(0)\n"
"v_exp_f32 v158, v158\n"
"v_mfma_f32_16x16x32_bf16 a[216:219], v[120:123], a[244:247], a[216:219]\n"
"ds_read_b128 v[216:219], v228 offset:37376\n"
"v_cvt_pk_bf16_f32 v103, v170, v171\n"
"v_mfma_f32_16x16x32_bf16 v[8:11], v[96:99], a[244:247], v[8:11]\n"
"v_exp_f32 v173, v173\n"
"v_mfma_f32_16x16x32_bf16 v[8:11], v[100:103], a[248:251], v[8:11]\n"
"ds_read_b128 v[208:211], v228 offset:36864\n"
"v_cvt_pk_bf16_f32 v110, v172, v173\n"
"v_mfma_f32_16x16x32_bf16 v[40:43], v[104:107], a[244:247], v[40:43]\n"
"v_exp_f32 v162, v162\n"
"v_mfma_f32_16x16x32_bf16 v[40:43], v[108:111], a[248:251], v[40:43]\n"
"ds_read_b128 v[220:223], v229 offset:37376\n"
"v_cvt_pk_bf16_f32 v119, v178, v179\n"
"v_mfma_f32_16x16x32_bf16 v[72:75], v[112:115], a[244:247], v[72:75]\n"
"s_add_u32 s40, s56, s53\n"
"ds_read_b128 v[212:215], v229 offset:36864\n"
"v_mfma_f32_16x16x32_bf16 v[72:75], v[116:119], a[248:251], v[72:75]\n"
"buffer_load_dwordx4 v240, s[20:23], s40 offen offset:2048 nt lds\n"
"v_mfma_f32_16x16x32_bf16 v[76:79], v[116:119], v[224:227], v[76:79]\n"
"v_exp_f32 v163, v163\n"
"v_mfma_f32_16x16x32_bf16 v[76:79], v[112:115], a[252:255], v[76:79]\n"
"v_exp_f32 v156, v156\n"
"v_mfma_f32_16x16x32_bf16 v[44:47], v[108:111], v[224:227], v[44:47]\n"
"v_exp_f32 v160, v160\n"
"v_mfma_f32_16x16x32_bf16 v[44:47], v[104:107], a[252:255], v[44:47]\n"
"v_exp_f32 v155, v155\n"
"v_mfma_f32_16x16x32_bf16 v[12:15], v[100:103], v[224:227], v[12:15]\n"
"ds_read_b64_tr_b16 v[226:227], v239 offset:20992\n"
"v_mfma_f32_16x16x32_bf16 v[12:15], v[96:99], a[252:255], v[12:15]\n"
"ds_read_b64_tr_b16 a[248:249], v238 offset:16896\n"
"v_mfma_f32_16x16x32_bf16 a[220:223], v[120:123], a[252:255], a[220:223]\n"
"v_exp_f32 v154, v154\n"
"v_smfmac_f32_16x16x64_bf16 v[196:199], a[240:243], v[96:103], v241\n"
"ds_read_b64_tr_b16 a[250:251], v238 offset:20992\n"
"v_mfma_f32_16x16x32_bf16 v[202:205], a[240:243], v[120:123], v[202:205]\n"
"s_waitcnt lgkmcnt(3)\n"
"v_exp_f32 v152, v152\n"
"v_mfma_f32_16x16x32_bf16 v[168:171], v[216:219], a[8:11], v[180:183]\n"
"ds_read_b64_tr_b16 a[244:245], v234 offset:16896\n"
"v_cvt_pk_bf16_f32 v128, v152, v153\n"
"v_mfma_f32_16x16x32_bf16 v[168:171], v[220:223], a[12:15], v[168:171]\n"
"v_exp_f32 v165, v165\n"
"v_mfma_f32_16x16x32_bf16 v[168:171], v[208:211], a[0:3], v[168:171]\n"
"ds_read_b64_tr_b16 a[246:247], v234 offset:20992\n"
"v_cvt_pk_bf16_f32 v145, v162, v163\n"
"v_mfma_f32_16x16x32_bf16 v[168:171], v[212:215], a[4:7], v[168:171]\n"
"ds_read_b64_tr_b16 a[254:255], v235 offset:20992\n"
"v_mfma_f32_16x16x32_bf16 v[172:175], v[208:211], a[16:19], v[184:187]\n"
"ds_read_b64_tr_b16 v[224:225], v239 offset:16896\n"
"v_cvt_pk_bf16_f32 v129, v154, v155\n"
"v_mfma_f32_16x16x32_bf16 v[172:175], v[212:215], a[20:23], v[172:175]\n"
"v_mfma_f32_16x16x32_bf16 v[172:175], v[216:219], a[24:27], v[172:175]\n"
"s_add_u32 s40, s56, s54\n"
"ds_read_b64_tr_b16 a[252:253], v235 offset:16896\n"
"v_mfma_f32_16x16x32_bf16 v[172:175], v[220:223], a[28:31], v[172:175]\n"
"buffer_load_dwordx4 v207, s[20:23], s40 offen offset:3072 nt lds\n"
"v_mfma_f32_16x16x32_bf16 v[176:179], v[216:219], a[40:43], v[188:191]\n"
"s_add_u32 s55, s55, s59\n"
"v_exp_f32 v157, v157\n"
"v_mfma_f32_16x16x32_bf16 v[176:179], v[220:223], a[44:47], v[176:179]\n"
"s_or_b32 s55, s55, s58\n"
"v_exp_f32 v171, v171\n"
"v_mfma_f32_16x16x32_bf16 v[176:179], v[208:211], a[32:35], v[176:179]\n"
"s_xor_b32 s56, s55, s57\n"
"v_exp_f32 v159, v159\n"
"v_mfma_f32_16x16x32_bf16 v[176:179], v[212:215], a[36:39], v[176:179]\n"
"s_cmp_ge_u32 s47, 15\n"
"v_exp_f32 v167, v167\n"
"v_mfma_f32_16x16x32_bf16 v[152:155], v[208:211], a[192:195], v[192:195]\n"
"s_cbranch_scc1 LSW_%=\nLSWK_%=:\n"
"v_cvt_pk_bf16_f32 v137, v158, v159\n"
"v_mfma_f32_16x16x32_bf16 v[152:155], v[212:215], a[196:199], v[152:155]\n"
"ds_read_b128 v[208:211], v230 offset:32768\n"
"v_cvt_pk_bf16_f32 v136, v156, v157\n"
"v_mfma_f32_16x16x32_bf16 v[152:155], v[216:219], a[200:203], v[152:155]\n"
"v_exp_f32 v166, v166\n"
"v_mfma_f32_16x16x32_bf16 v[152:155], v[220:223], a[204:207], v[152:155]\n"
"s_waitcnt lgkmcnt(1)\n"
"ds_read_b128 v[216:219], v230 offset:33280\n"
"v_mfma_f32_16x16x32_bf16 a[232:235], v[120:123], a[244:247], a[232:235]\n"
"v_exp_f32 v169, v169\n"
"v_mfma_f32_16x16x32_bf16 v[24:27], v[96:99], a[244:247], v[24:27]\n"
"ds_read_b128 v[220:223], v231 offset:33280\n"
"v_mfma_f32_16x16x32_bf16 v[24:27], v[100:103], a[248:251], v[24:27]\n"
"v_exp_f32 v164, v164\n"
"v_mfma_f32_16x16x32_bf16 v[56:59], v[104:107], a[244:247], v[56:59]\n"
"ds_read_b128 v[212:215], v231 offset:32768\n"
"v_mfma_f32_16x16x32_bf16 v[56:59], v[108:111], a[248:251], v[56:59]\n"
"v_exp_f32 v178, v178\n"
"v_mfma_f32_16x16x32_bf16 v[88:91], v[112:115], a[244:247], v[88:91]\n"
"s_mov_b32 m0, s44\n"
"ds_read_b64_tr_b16 a[246:247], v232 offset:20480\n"
"v_mfma_f32_16x16x32_bf16 v[88:91], v[116:119], a[248:251], v[88:91]\n"
"buffer_load_dwordx4 v240, s[16:19], s56 offen nt lds\n"
"v_mfma_f32_16x16x32_bf16 v[92:95], v[116:119], v[224:227], v[92:95]\n"
"v_exp_f32 v175, v175\n"
"v_mfma_f32_16x16x32_bf16 v[92:95], v[112:115], a[252:255], v[92:95]\n"
"v_exp_f32 v168, v168\n"
"v_mfma_f32_16x16x32_bf16 v[60:63], v[108:111], v[224:227], v[60:63]\n"
"v_exp_f32 v161, v161\n"
"v_mfma_f32_16x16x32_bf16 v[60:63], v[104:107], a[252:255], v[60:63]\n"
"v_exp_f32 v177, v177\n"
"v_mfma_f32_16x16x32_bf16 v[28:31], v[100:103], v[224:227], v[28:31]\n"
"ds_read_b64_tr_b16 v[226:227], v237 offset:20480\n"
"v_mfma_f32_16x16x32_bf16 v[28:31], v[96:99], a[252:255], v[28:31]\n"
"v_exp_f32 v172, v172\n"
"v_mfma_f32_16x16x32_bf16 a[236:239], v[120:123], a[252:255], a[236:239]\n"
"ds_read_b64_tr_b16 a[250:251], v236 offset:20480\n"
"v_cvt_pk_bf16_f32 v124, v164, v165\n"
"v_smfmac_f32_16x16x64_bf16 v[198:201], a[240:243], v[104:111], v241\n"
"s_waitcnt lgkmcnt(2)\n"
"ds_read_b64_tr_b16 a[252:253], v233 offset:16384\n"
"v_mfma_f32_16x16x32_bf16 v[156:159], v[208:211], a[0:3], v[180:183]\n"
"ds_read_b64_tr_b16 a[248:249], v236 offset:16384\n"
"v_cvt_pk_bf16_f32 v125, v166, v167\n"
"v_mfma_f32_16x16x32_bf16 v[156:159], v[212:215], a[4:7], v[156:159]\n"
"ds_read_b64_tr_b16 v[224:225], v237 offset:16384\n"
"v_cvt_pk_bf16_f32 v130, v168, v169\n"
"v_mfma_f32_16x16x32_bf16 v[156:159], v[216:219], a[8:11], v[156:159]\n"
"ds_read_b64_tr_b16 a[254:255], v233 offset:20480\n"
"v_cvt_pk_bf16_f32 v144, v160, v161\n"
"v_mfma_f32_16x16x32_bf16 v[156:159], v[220:223], a[12:15], v[156:159]\n"
"ds_read_b64_tr_b16 a[244:245], v232 offset:16384\n"
"v_mfma_f32_16x16x32_bf16 v[160:163], v[216:219], a[24:27], v[184:187]\n"
"v_exp_f32 v176, v176\n"
"v_mfma_f32_16x16x32_bf16 v[160:163], v[220:223], a[28:31], v[160:163]\n"
"v_exp_f32 v152, v152\n"
"v_mfma_f32_16x16x32_bf16 v[160:163], v[208:211], a[16:19], v[160:163]\n"
"s_add_u32 s40, s56, s52\n"
"v_cvt_pk_bf16_f32 v146, v176, v177\n"
"v_mfma_f32_16x16x32_bf16 v[160:163], v[212:215], a[20:23], v[160:163]\n"
"buffer_load_dwordx4 v207, s[16:19], s40 offen offset:1024 nt lds\n"
"v_mfma_f32_16x16x32_bf16 v[164:167], v[208:211], a[32:35], v[188:191]\n"
"v_exp_f32 v170, v170\n"
"v_mfma_f32_16x16x32_bf16 v[164:167], v[212:215], a[36:39], v[164:167]\n"
"v_exp_f32 v173, v173\n"
"v_mfma_f32_16x16x32_bf16 v[164:167], v[216:219], a[40:43], v[164:167]\n"
"v_exp_f32 v153, v153\n"
"v_mfma_f32_16x16x32_bf16 v[164:167], v[220:223], a[44:47], v[164:167]\n"
"s_waitcnt lgkmcnt(0)\n"
"v_exp_f32 v174, v174\n"
"v_mfma_f32_16x16x32_bf16 a[208:211], v[120:123], a[244:247], a[208:211]\n"
"ds_read_b128 v[208:211], v230 offset:36864\n"
"v_cvt_pk_bf16_f32 v139, v174, v175\n"
"v_mfma_f32_16x16x32_bf16 v[0:3], v[96:99], a[244:247], v[0:3]\n"
"v_exp_f32 v156, v156\n"
"v_mfma_f32_16x16x32_bf16 v[0:3], v[100:103], a[248:251], v[0:3]\n"
"ds_read_b128 v[220:223], v231 offset:37376\n"
"v_cvt_pk_bf16_f32 v138, v172, v173\n"
"v_mfma_f32_16x16x32_bf16 v[32:35], v[104:107], a[244:247], v[32:35]\n"
"v_exp_f32 v161, v161\n"
"v_mfma_f32_16x16x32_bf16 v[32:35], v[108:111], a[248:251], v[32:35]\n"
"ds_read_b128 v[216:219], v230 offset:37376\n"
"v_cvt_pk_bf16_f32 v126, v152, v153\n"
"v_mfma_f32_16x16x32_bf16 v[64:67], v[112:115], a[244:247], v[64:67]\n"
"s_add_u32 s40, s56, s53\n"
"ds_read_b128 v[212:215], v231 offset:36864\n"
"v_mfma_f32_16x16x32_bf16 v[64:67], v[116:119], a[248:251], v[64:67]\n"
"buffer_load_dwordx4 v240, s[16:19], s40 offen offset:2048 nt lds\n"
"v_mfma_f32_16x16x32_bf16 v[68:71], v[116:119], v[224:227], v[68:71]\n"
"v_exp_f32 v159, v159\n"
"v_mfma_f32_16x16x32_bf16 v[68:71], v[112:115], a[252:255], v[68:71]\n"
"v_exp_f32 v155, v155\n"
"v_mfma_f32_16x16x32_bf16 v[36:39], v[108:111], v[224:227], v[36:39]\n"
"v_exp_f32 v160, v160\n"
"v_mfma_f32_16x16x32_bf16 v[36:39], v[104:107], a[252:255], v[36:39]\n"
"v_exp_f32 v179, v179\n"
"v_mfma_f32_16x16x32_bf16 v[4:7], v[100:103], v[224:227], v[4:7]\n"
"ds_read_b64_tr_b16 a[244:245], v232 offset:16896\n"
"ds_read_b64_tr_b16 v[224:225], v237 offset:16896\n"
"v_mfma_f32_16x16x32_bf16 v[4:7], v[96:99], a[252:255], v[4:7]\n"
"v_exp_f32 v158, v158\n"
"v_mfma_f32_16x16x32_bf16 a[212:215], v[120:123], a[252:255], a[212:215]\n"
"ds_read_b64_tr_b16 a[246:247], v232 offset:20992\n"
"ds_read_b64_tr_b16 a[250:251], v236 offset:20992\n"
"v_smfmac_f32_16x16x64_bf16 v[200:203], a[240:243], v[112:119], v241\n"
"s_waitcnt lgkmcnt(4)\n"
"ds_read_b64_tr_b16 v[226:227], v237 offset:20992\n"
"v_cvt_pk_bf16_f32 v131, v170, v171\n"
"v_mfma_f32_16x16x32_bf16 v[168:171], v[208:211], a[0:3], v[180:183]\n"
"v_exp_f32 v164, v164\n"
"v_mfma_f32_16x16x32_bf16 v[168:171], v[212:215], a[4:7], v[168:171]\n"
"ds_read_b64_tr_b16 a[248:249], v236 offset:16896\n"
"ds_read_b64_tr_b16 a[254:255], v233 offset:20992\n"
"v_mfma_f32_16x16x32_bf16 v[168:171], v[216:219], a[8:11], v[168:171]\n"
"s_add_u32 s40, s56, s54\n"
"ds_read_b64_tr_b16 a[252:253], v233 offset:16896\n"
"v_mfma_f32_16x16x32_bf16 v[168:171], v[220:223], a[12:15], v[168:171]\n"
"buffer_load_dwordx4 v207, s[16:19], s40 offen offset:3072 nt lds\n"
"v_mfma_f32_16x16x32_bf16 v[172:175], v[216:219], a[24:27], v[184:187]\n"
"v_exp_f32 v154, v154\n"
"v_mfma_f32_16x16x32_bf16 v[172:175], v[220:223], a[28:31], v[172:175]\n"
"v_exp_f32 v167, v167\n"
"v_mfma_f32_16x16x32_bf16 v[172:175], v[208:211], a[16:19], v[172:175]\n"
"v_exp_f32 v163, v163\n"
"v_mfma_f32_16x16x32_bf16 v[172:175], v[212:215], a[20:23], v[172:175]\n"
"s_cbranch_vccnz LPCL2_%=\nLPKL2_%=:\n"
"v_cvt_pk_bf16_f32 v147, v178, v179\n"
"v_mfma_f32_16x16x32_bf16 v[176:179], v[208:211], a[32:35], v[188:191]\n"
"v_exp_f32 v165, v165\n"
"v_mfma_f32_16x16x32_bf16 v[176:179], v[212:215], a[36:39], v[176:179]\n"
"v_exp_f32 v157, v157\n"
"v_mfma_f32_16x16x32_bf16 v[176:179], v[216:219], a[40:43], v[176:179]\n"
"v_exp_f32 v166, v166\n"
"v_mfma_f32_16x16x32_bf16 v[176:179], v[220:223], a[44:47], v[176:179]\n"
"s_waitcnt lgkmcnt(0)\n"
"v_mfma_f32_16x16x32_bf16 a[224:227], v[120:123], a[244:247], a[224:227]\n"
"v_exp_f32 v162, v162\n"
"v_mfma_f32_16x16x32_bf16 v[16:19], v[96:99], a[244:247], v[16:19]\n"
"s_waitcnt vmcnt(8)\n"
"v_exp_f32 v168, v168\n"
"v_mfma_f32_16x16x32_bf16 v[16:19], v[100:103], a[248:251], v[16:19]\n"
"s_barrier\n"
"v_mfma_f32_16x16x32_bf16 v[48:51], v[104:107], a[244:247], v[48:51]\n"
"ds_read_b128 v[208:211], v228 offset:49152\n"
"v_cvt_pk_bf16_f32 v133, v158, v159\n"
"v_mfma_f32_16x16x32_bf16 v[48:51], v[108:111], a[248:251], v[48:51]\n"
"ds_read_b128 v[216:219], v228 offset:49664\n"
"v_cvt_pk_bf16_f32 v141, v162, v163\n"
"v_mfma_f32_16x16x32_bf16 v[80:83], v[112:115], a[244:247], v[80:83]\n"
"ds_read_b128 v[220:223], v229 offset:49664\n"
"v_cvt_pk_bf16_f32 v127, v154, v155\n"
"v_mfma_f32_16x16x32_bf16 v[80:83], v[116:119], a[248:251], v[80:83]\n"
"ds_read_b128 v[212:215], v229 offset:49152\n"
"v_cvt_pk_bf16_f32 v149, v166, v167\n"
"v_mfma_f32_16x16x32_bf16 v[84:87], v[116:119], v[224:227], v[84:87]\n"
"v_exp_f32 v176, v176\n"
"v_mfma_f32_16x16x32_bf16 v[84:87], v[112:115], a[252:255], v[84:87]\n"
"v_exp_f32 v169, v169\n"
"v_mfma_f32_16x16x32_bf16 v[52:55], v[108:111], v[224:227], v[52:55]\n"
"ds_read_b64_tr_b16 a[246:247], v234 offset:36864\n"
"v_cvt_pk_bf16_f32 v148, v164, v165\n"
"v_mfma_f32_16x16x32_bf16 v[52:55], v[104:107], a[252:255], v[52:55]\n"
"v_exp_f32 v177, v177\n"
"v_mfma_f32_16x16x32_bf16 v[20:23], v[100:103], v[224:227], v[20:23]\n"
"ds_read_b64_tr_b16 a[250:251], v238 offset:36864\n"
"v_cvt_pk_bf16_f32 v150, v176, v177\n"
"v_mfma_f32_16x16x32_bf16 v[20:23], v[96:99], a[252:255], v[20:23]\n"
"v_exp_f32 v174, v174\n"
"v_mfma_f32_16x16x32_bf16 a[228:231], v[120:123], a[252:255], a[228:231]\n"
"s_waitcnt lgkmcnt(3)\n"
"ds_read_b64_tr_b16 a[254:255], v235 offset:36864\n"
"v_cvt_pk_bf16_f32 v132, v156, v157\n"
"v_mfma_f32_16x16x32_bf16 v[152:155], v[208:211], a[0:3], v[180:183]\n"
"s_waitcnt lgkmcnt(3)\n"
"ds_read_b64_tr_b16 a[248:249], v238 offset:32768\n"
"v_cvt_pk_bf16_f32 v134, v168, v169\n"
"v_mfma_f32_16x16x32_bf16 v[152:155], v[212:215], a[4:7], v[152:155]\n"
"v_exp_f32 v172, v172\n"
"v_mfma_f32_16x16x32_bf16 v[152:155], v[216:219], a[8:11], v[152:155]\n"
"ds_read_b64_tr_b16 a[244:245], v234 offset:32768\n"
"v_cvt_pk_bf16_f32 v140, v160, v161\n"
"v_mfma_f32_16x16x32_bf16 v[152:155], v[220:223], a[12:15], v[152:155]\n"
"v_exp_f32 v175, v175\n"
"v_mfma_f32_16x16x32_bf16 v[156:159], v[216:219], a[24:27], v[184:187]\n"
"ds_read_b64_tr_b16 a[252:253], v235 offset:32768\n"
"v_cvt_pk_bf16_f32 v143, v174, v175\n"
"v_mfma_f32_16x16x32_bf16 v[156:159], v[220:223], a[28:31], v[156:159]\n"
"ds_read_b64_tr_b16 v[224:225], v239 offset:32768\n"
"v_mfma_f32_16x16x32_bf16 v[156:159], v[208:211], a[16:19], v[156:159]\n"
"s_add_u32 m0, s44, 65536\n"
"ds_read_b64_tr_b16 v[226:227], v239 offset:36864\n"
"v_mfma_f32_16x16x32_bf16 v[156:159], v[212:215], a[20:23], v[156:159]\n"
"buffer_load_dwordx4 v240, s[20:23], s56 offen nt lds\n"
"v_mfma_f32_16x16x32_bf16 v[160:163], v[208:211], a[32:35], v[188:191]\n"
"v_exp_f32 v178, v178\n"
"v_mfma_f32_16x16x32_bf16 v[160:163], v[212:215], a[36:39], v[160:163]\n"
"v_exp_f32 v171, v171\n"
"v_mfma_f32_16x16x32_bf16 v[160:163], v[216:219], a[40:43], v[160:163]\n"
"s_add_u32 s40, s56, s52\n"
"v_mfma_f32_16x16x32_bf16 v[160:163], v[220:223], a[44:47], v[160:163]\n"
"buffer_load_dwordx4 v207, s[20:23], s40 offen offset:1024 nt lds\n"
"v_mfma_f32_16x16x32_bf16 v[164:167], v[216:219], a[200:203], v[192:195]\n"
"v_exp_f32 v170, v170\n"
"v_mfma_f32_16x16x32_bf16 v[164:167], v[220:223], a[204:207], v[164:167]\n"
"v_exp_f32 v153, v153\n"
"v_mfma_f32_16x16x32_bf16 v[164:167], v[208:211], a[192:195], v[164:167]\n"
"v_exp_f32 v179, v179\n"
"v_mfma_f32_16x16x32_bf16 v[164:167], v[212:215], a[196:199], v[164:167]\n"
"s_waitcnt lgkmcnt(0)\n"
"v_exp_f32 v158, v158\n"
"v_mfma_f32_16x16x32_bf16 a[216:219], v[124:127], a[244:247], a[216:219]\n"
"ds_read_b128 v[216:219], v228 offset:53760\n"
"v_cvt_pk_bf16_f32 v135, v170, v171\n"
"v_mfma_f32_16x16x32_bf16 v[8:11], v[128:131], a[244:247], v[8:11]\n"
"v_exp_f32 v173, v173\n"
"v_mfma_f32_16x16x32_bf16 v[8:11], v[132:135], a[248:251], v[8:11]\n"
"ds_read_b128 v[208:211], v228 offset:53248\n"
"v_cvt_pk_bf16_f32 v142, v172, v173\n"
"v_mfma_f32_16x16x32_bf16 v[40:43], v[136:139], a[244:247], v[40:43]\n"
"v_exp_f32 v162, v162\n"
"v_mfma_f32_16x16x32_bf16 v[40:43], v[140:143], a[248:251], v[40:43]\n"
"ds_read_b128 v[220:223], v229 offset:53760\n"
"v_cvt_pk_bf16_f32 v151, v178, v179\n"
"v_mfma_f32_16x16x32_bf16 v[72:75], v[144:147], a[244:247], v[72:75]\n"
"s_add_u32 s40, s56, s53\n"
"ds_read_b128 v[212:215], v229 offset:53248\n"
"v_mfma_f32_16x16x32_bf16 v[72:75], v[148:151], a[248:251], v[72:75]\n"
"buffer_load_dwordx4 v240, s[20:23], s40 offen offset:2048 nt lds\n"
"v_mfma_f32_16x16x32_bf16 v[76:79], v[148:151], v[224:227], v[76:79]\n"
"v_exp_f32 v163, v163\n"
"v_mfma_f32_16x16x32_bf16 v[76:79], v[144:147], a[252:255], v[76:79]\n"
"v_exp_f32 v156, v156\n"
"v_mfma_f32_16x16x32_bf16 v[44:47], v[140:143], v[224:227], v[44:47]\n"
"v_exp_f32 v160, v160\n"
"v_mfma_f32_16x16x32_bf16 v[44:47], v[136:139], a[252:255], v[44:47]\n"
"v_exp_f32 v155, v155\n"
"v_mfma_f32_16x16x32_bf16 v[12:15], v[132:135], v[224:227], v[12:15]\n"
"ds_read_b64_tr_b16 v[226:227], v239 offset:37376\n"
"v_mfma_f32_16x16x32_bf16 v[12:15], v[128:131], a[252:255], v[12:15]\n"
"ds_read_b64_tr_b16 a[248:249], v238 offset:33280\n"
"v_mfma_f32_16x16x32_bf16 a[220:223], v[124:127], a[252:255], a[220:223]\n"
"v_exp_f32 v154, v154\n"
"v_smfmac_f32_16x16x64_bf16 v[196:199], a[240:243], v[128:135], v241\n"
"ds_read_b64_tr_b16 a[250:251], v238 offset:37376\n"
"v_mfma_f32_16x16x32_bf16 v[202:205], a[240:243], v[124:127], v[202:205]\n"
"s_waitcnt lgkmcnt(3)\n"
"v_exp_f32 v152, v152\n"
"v_mfma_f32_16x16x32_bf16 v[168:171], v[216:219], a[8:11], v[180:183]\n"
"ds_read_b64_tr_b16 a[244:245], v234 offset:33280\n"
"v_cvt_pk_bf16_f32 v96, v152, v153\n"
"v_mfma_f32_16x16x32_bf16 v[168:171], v[220:223], a[12:15], v[168:171]\n"
"v_exp_f32 v165, v165\n"
"v_mfma_f32_16x16x32_bf16 v[168:171], v[208:211], a[0:3], v[168:171]\n"
"ds_read_b64_tr_b16 a[246:247], v234 offset:37376\n"
"v_cvt_pk_bf16_f32 v113, v162, v163\n"
"v_mfma_f32_16x16x32_bf16 v[168:171], v[212:215], a[4:7], v[168:171]\n"
"ds_read_b64_tr_b16 a[254:255], v235 offset:37376\n"
"v_mfma_f32_16x16x32_bf16 v[172:175], v[208:211], a[16:19], v[184:187]\n"
"ds_read_b64_tr_b16 v[224:225], v239 offset:33280\n"
"v_cvt_pk_bf16_f32 v97, v154, v155\n"
"v_mfma_f32_16x16x32_bf16 v[172:175], v[212:215], a[20:23], v[172:175]\n"
"v_mfma_f32_16x16x32_bf16 v[172:175], v[216:219], a[24:27], v[172:175]\n"
"s_add_u32 s40, s56, s54\n"
"ds_read_b64_tr_b16 a[252:253], v235 offset:33280\n"
"v_mfma_f32_16x16x32_bf16 v[172:175], v[220:223], a[28:31], v[172:175]\n"
"buffer_load_dwordx4 v207, s[20:23], s40 offen offset:3072 nt lds\n"
"v_mfma_f32_16x16x32_bf16 v[176:179], v[216:219], a[40:43], v[188:191]\n"
"s_add_u32 s55, s55, s59\n"
"v_exp_f32 v157, v157\n"
"v_mfma_f32_16x16x32_bf16 v[176:179], v[220:223], a[44:47], v[176:179]\n"
"s_or_b32 s55, s55, s58\n"
"v_exp_f32 v171, v171\n"
"v_mfma_f32_16x16x32_bf16 v[176:179], v[208:211], a[32:35], v[176:179]\n"
"s_xor_b32 s56, s55, s57\n"
"v_exp_f32 v159, v159\n"
"v_mfma_f32_16x16x32_bf16 v[176:179], v[212:215], a[36:39], v[176:179]\n"
"v_exp_f32 v167, v167\n"
"v_mfma_f32_16x16x32_bf16 v[152:155], v[208:211], a[192:195], v[192:195]\n"
"v_cvt_pk_bf16_f32 v105, v158, v159\n"
"v_mfma_f32_16x16x32_bf16 v[152:155], v[212:215], a[196:199], v[152:155]\n"
"ds_read_b128 v[208:211], v230 offset:49152\n"
"v_cvt_pk_bf16_f32 v104, v156, v157\n"
"v_mfma_f32_16x16x32_bf16 v[152:155], v[216:219], a[200:203], v[152:155]\n"
"v_exp_f32 v166, v166\n"
"v_mfma_f32_16x16x32_bf16 v[152:155], v[220:223], a[204:207], v[152:155]\n"
"s_waitcnt lgkmcnt(1)\n"
"ds_read_b128 v[216:219], v230 offset:49664\n"
"v_mfma_f32_16x16x32_bf16 a[232:235], v[124:127], a[244:247], a[232:235]\n"
"v_exp_f32 v169, v169\n"
"v_mfma_f32_16x16x32_bf16 v[24:27], v[128:131], a[244:247], v[24:27]\n"
"ds_read_b128 v[220:223], v231 offset:49664\n"
"v_mfma_f32_16x16x32_bf16 v[24:27], v[132:135], a[248:251], v[24:27]\n"
"v_exp_f32 v164, v164\n"
"v_mfma_f32_16x16x32_bf16 v[56:59], v[136:139], a[244:247], v[56:59]\n"
"ds_read_b128 v[212:215], v231 offset:49152\n"
"v_mfma_f32_16x16x32_bf16 v[56:59], v[140:143], a[248:251], v[56:59]\n"
"v_exp_f32 v178, v178\n"
"v_mfma_f32_16x16x32_bf16 v[88:91], v[144:147], a[244:247], v[88:91]\n"
"s_add_u32 m0, s44, 16384\n"
"ds_read_b64_tr_b16 a[246:247], v232 offset:36864\n"
"v_mfma_f32_16x16x32_bf16 v[88:91], v[148:151], a[248:251], v[88:91]\n"
"buffer_load_dwordx4 v240, s[16:19], s56 offen nt lds\n"
"v_mfma_f32_16x16x32_bf16 v[92:95], v[148:151], v[224:227], v[92:95]\n"
"v_exp_f32 v175, v175\n"
"v_mfma_f32_16x16x32_bf16 v[92:95], v[144:147], a[252:255], v[92:95]\n"
"v_exp_f32 v168, v168\n"
"v_mfma_f32_16x16x32_bf16 v[60:63], v[140:143], v[224:227], v[60:63]\n"
"v_exp_f32 v161, v161\n"
"v_mfma_f32_16x16x32_bf16 v[60:63], v[136:139], a[252:255], v[60:63]\n"
"v_exp_f32 v177, v177\n"
"v_mfma_f32_16x16x32_bf16 v[28:31], v[132:135], v[224:227], v[28:31]\n"
"ds_read_b64_tr_b16 v[226:227], v237 offset:36864\n"
"v_mfma_f32_16x16x32_bf16 v[28:31], v[128:131], a[252:255], v[28:31]\n"
"v_exp_f32 v172, v172\n"
"v_mfma_f32_16x16x32_bf16 a[236:239], v[124:127], a[252:255], a[236:239]\n"
"ds_read_b64_tr_b16 a[250:251], v236 offset:36864\n"
"v_cvt_pk_bf16_f32 v120, v164, v165\n"
"v_smfmac_f32_16x16x64_bf16 v[198:201], a[240:243], v[136:143], v241\n"
"s_waitcnt lgkmcnt(2)\n"
"ds_read_b64_tr_b16 a[252:253], v233 offset:32768\n"
"v_mfma_f32_16x16x32_bf16 v[156:159], v[208:211], a[0:3], v[180:183]\n"
"ds_read_b64_tr_b16 a[248:249], v236 offset:32768\n"
"v_cvt_pk_bf16_f32 v121, v166, v167\n"
"v_mfma_f32_16x16x32_bf16 v[156:159], v[212:215], a[4:7], v[156:159]\n"
"ds_read_b64_tr_b16 v[224:225], v237 offset:32768\n"
"v_cvt_pk_bf16_f32 v98, v168, v169\n"
"v_mfma_f32_16x16x32_bf16 v[156:159], v[216:219], a[8:11], v[156:159]\n"
"ds_read_b64_tr_b16 a[254:255], v233 offset:36864\n"
"v_cvt_pk_bf16_f32 v112, v160, v161\n"
"v_mfma_f32_16x16x32_bf16 v[156:159], v[220:223], a[12:15], v[156:159]\n"
"ds_read_b64_tr_b16 a[244:245], v232 offset:32768\n"
"v_mfma_f32_16x16x32_bf16 v[160:163], v[216:219], a[24:27], v[184:187]\n"
"v_exp_f32 v176, v176\n"
"v_mfma_f32_16x16x32_bf16 v[160:163], v[220:223], a[28:31], v[160:163]\n"
"v_exp_f32 v152, v152\n"
"v_mfma_f32_16x16x32_bf16 v[160:163], v[208:211], a[16:19], v[160:163]\n"
"s_add_u32 s40, s56, s52\n"
"v_cvt_pk_bf16_f32 v114, v176, v177\n"
"v_mfma_f32_16x16x32_bf16 v[160:163], v[212:215], a[20:23], v[160:163]\n"
"buffer_load_dwordx4 v207, s[16:19], s40 offen offset:1024 nt lds\n"
"v_mfma_f32_16x16x32_bf16 v[164:167], v[208:211], a[32:35], v[188:191]\n"
"v_exp_f32 v170, v170\n"
"v_mfma_f32_16x16x32_bf16 v[164:167], v[212:215], a[36:39], v[164:167]\n"
"v_exp_f32 v173, v173\n"
"v_mfma_f32_16x16x32_bf16 v[164:167], v[216:219], a[40:43], v[164:167]\n"
"v_exp_f32 v153, v153\n"
"v_mfma_f32_16x16x32_bf16 v[164:167], v[220:223], a[44:47], v[164:167]\n"
"s_waitcnt lgkmcnt(0)\n"
"v_exp_f32 v174, v174\n"
"v_mfma_f32_16x16x32_bf16 a[208:211], v[124:127], a[244:247], a[208:211]\n"
"ds_read_b128 v[208:211], v230 offset:53248\n"
"v_cvt_pk_bf16_f32 v107, v174, v175\n"
"v_mfma_f32_16x16x32_bf16 v[0:3], v[128:131], a[244:247], v[0:3]\n"
"v_exp_f32 v156, v156\n"
"v_mfma_f32_16x16x32_bf16 v[0:3], v[132:135], a[248:251], v[0:3]\n"
"ds_read_b128 v[220:223], v231 offset:53760\n"
"v_cvt_pk_bf16_f32 v106, v172, v173\n"
"v_mfma_f32_16x16x32_bf16 v[32:35], v[136:139], a[244:247], v[32:35]\n"
"v_exp_f32 v161, v161\n"
"v_mfma_f32_16x16x32_bf16 v[32:35], v[140:143], a[248:251], v[32:35]\n"
"ds_read_b128 v[216:219], v230 offset:53760\n"
"v_cvt_pk_bf16_f32 v122, v152, v153\n"
"v_mfma_f32_16x16x32_bf16 v[64:67], v[144:147], a[244:247], v[64:67]\n"
"s_add_u32 s40, s56, s53\n"
"ds_read_b128 v[212:215], v231 offset:53248\n"
"v_mfma_f32_16x16x32_bf16 v[64:67], v[148:151], a[248:251], v[64:67]\n"
"buffer_load_dwordx4 v240, s[16:19], s40 offen offset:2048 nt lds\n"
"v_mfma_f32_16x16x32_bf16 v[68:71], v[148:151], v[224:227], v[68:71]\n"
"v_exp_f32 v159, v159\n"
"v_mfma_f32_16x16x32_bf16 v[68:71], v[144:147], a[252:255], v[68:71]\n"
"v_exp_f32 v155, v155\n"
"v_mfma_f32_16x16x32_bf16 v[36:39], v[140:143], v[224:227], v[36:39]\n"
"v_exp_f32 v160, v160\n"
"v_mfma_f32_16x16x32_bf16 v[36:39], v[136:139], a[252:255], v[36:39]\n"
"v_exp_f32 v179, v179\n"
"v_mfma_f32_16x16x32_bf16 v[4:7], v[132:135], v[224:227], v[4:7]\n"
"ds_read_b64_tr_b16 a[244:245], v232 offset:33280\n"
"ds_read_b64_tr_b16 v[224:225], v237 offset:33280\n"
"v_mfma_f32_16x16x32_bf16 v[4:7], v[128:131], a[252:255], v[4:7]\n"
"v_exp_f32 v158, v158\n"
"v_mfma_f32_16x16x32_bf16 a[212:215], v[124:127], a[252:255], a[212:215]\n"
"ds_read_b64_tr_b16 a[246:247], v232 offset:37376\n"
"ds_read_b64_tr_b16 a[250:251], v236 offset:37376\n"
"v_smfmac_f32_16x16x64_bf16 v[200:203], a[240:243], v[144:151], v241\n"
"s_waitcnt lgkmcnt(4)\n"
"ds_read_b64_tr_b16 v[226:227], v237 offset:37376\n"
"v_cvt_pk_bf16_f32 v99, v170, v171\n"
"v_mfma_f32_16x16x32_bf16 v[168:171], v[208:211], a[0:3], v[180:183]\n"
"v_exp_f32 v164, v164\n"
"v_mfma_f32_16x16x32_bf16 v[168:171], v[212:215], a[4:7], v[168:171]\n"
"ds_read_b64_tr_b16 a[248:249], v236 offset:33280\n"
"ds_read_b64_tr_b16 a[254:255], v233 offset:37376\n"
"v_mfma_f32_16x16x32_bf16 v[168:171], v[216:219], a[8:11], v[168:171]\n"
"s_add_u32 s40, s56, s54\n"
"ds_read_b64_tr_b16 a[252:253], v233 offset:33280\n"
"v_mfma_f32_16x16x32_bf16 v[168:171], v[220:223], a[12:15], v[168:171]\n"
"buffer_load_dwordx4 v207, s[16:19], s40 offen offset:3072 nt lds\n"
"v_mfma_f32_16x16x32_bf16 v[172:175], v[216:219], a[24:27], v[184:187]\n"
"v_exp_f32 v154, v154\n"
"v_mfma_f32_16x16x32_bf16 v[172:175], v[220:223], a[28:31], v[172:175]\n"
"v_exp_f32 v167, v167\n"
"v_mfma_f32_16x16x32_bf16 v[172:175], v[208:211], a[16:19], v[172:175]\n"
"v_exp_f32 v163, v163\n"
"v_mfma_f32_16x16x32_bf16 v[172:175], v[212:215], a[20:23], v[172:175]\n"
"s_cbranch_vccnz LPCL3_%=\nLPKL3_%=:\n"
"v_cvt_pk_bf16_f32 v115, v178, v179\n"
"v_mfma_f32_16x16x32_bf16 v[176:179], v[208:211], a[32:35], v[188:191]\n"
"s_add_u32 s47, s47, 1\n"
"v_exp_f32 v165, v165\n"
"v_mfma_f32_16x16x32_bf16 v[176:179], v[212:215], a[36:39], v[176:179]\n"
"v_exp_f32 v157, v157\n"
"v_mfma_f32_16x16x32_bf16 v[176:179], v[216:219], a[40:43], v[176:179]\n"
"s_and_b32 s40, s47, 3\n"
"v_exp_f32 v166, v166\n"
"v_mfma_f32_16x16x32_bf16 v[176:179], v[220:223], a[44:47], v[176:179]\n"
"s_waitcnt lgkmcnt(0)\n"
"s_cbranch_scc1 LAL_%=\n"
"s_cmp_eq_u32 s47, 8\n"
"s_cbranch_scc1 LTH1_%=\n"
"s_cmp_eq_u32 s47, 12\n"
"s_cbranch_scc1 LTH2_%=\n"
"s_cmp_eq_u32 s47, 16\n"
"s_cbranch_scc1 LAX_%=\n"
"LTH0_%=:\n"
"v_mfma_f32_16x16x32_bf16 a[224:227], v[124:127], a[244:247], a[224:227]\n"
"v_exp_f32 v171, v171\n"
"v_mfma_f32_16x16x32_bf16 v[16:19], v[128:131], a[244:247], v[16:19]\n"
"s_waitcnt vmcnt(8)\n"
"v_exp_f32 v172, v172\n"
"v_mfma_f32_16x16x32_bf16 v[16:19], v[132:135], a[248:251], v[16:19]\n"
"s_barrier\n"
"v_mfma_f32_16x16x32_bf16 v[48:51], v[136:139], a[244:247], v[48:51]\n"
"v_exp_f32 v169, v169\n"
"v_mfma_f32_16x16x32_bf16 v[48:51], v[140:143], a[248:251], v[48:51]\n"
"ds_read_b128 v[220:223], v229 offset:512\n"
"v_cvt_pk_bf16_f32 v117, v166, v167\n"
"v_exp_f32 v176, v176\n"
"v_exp_f32 v177, v177\n"
"v_exp_f32 v178, v178\n"
"v_mfma_f32_16x16x32_bf16 v[80:83], v[144:147], a[244:247], v[80:83]\n"
"ds_read_b64_tr_b16 a[244:245], v234 offset:49152\n"
"v_cvt_pk_bf16_f32 v108, v160, v161\n"
"v_mfma_f32_16x16x32_bf16 v[80:83], v[148:151], a[248:251], v[80:83]\n"
"ds_read_b64_tr_b16 a[246:247], v234 offset:53248\n"
"ds_read_b64_tr_b16 a[248:249], v238 offset:49152\n"
"v_exp_f32 v162, v162\n"
"v_exp_f32 v168, v168\n"
"v_exp_f32 v173, v173\n"
"v_exp_f32 v175, v175\n"
"v_mfma_f32_16x16x32_bf16 v[84:87], v[148:151], v[224:227], v[84:87]\n"
"ds_read_b64_tr_b16 a[250:251], v238 offset:53248\n"
"v_cvt_pk_bf16_f32 v116, v164, v165\n"
"v_mfma_f32_16x16x32_bf16 v[84:87], v[144:147], a[252:255], v[84:87]\n"
"v_exp_f32 v170, v170\n"
"v_mfma_f32_16x16x32_bf16 v[52:55], v[140:143], v[224:227], v[52:55]\n"
"v_cvt_pk_bf16_f32 v103, v170, v171\n"
"v_cvt_pk_bf16_f32 v110, v172, v173\n"
"v_mfma_f32_16x16x32_bf16 v[52:55], v[136:139], a[252:255], v[52:55]\n"
"v_cvt_pk_bf16_f32 v123, v154, v155\n"
"v_cvt_pk_bf16_f32 v101, v158, v159\n"
"v_mfma_f32_16x16x32_bf16 v[20:23], v[132:135], v[224:227], v[20:23]\n"
"v_cvt_pk_bf16_f32 v100, v156, v157\n"
"v_cvt_pk_bf16_f32 v102, v168, v169\n"
"v_mfma_f32_16x16x32_bf16 v[20:23], v[128:131], a[252:255], v[20:23]\n"
"v_exp_f32 v179, v179\n"
"v_mfma_f32_16x16x32_bf16 a[228:231], v[124:127], a[252:255], a[228:231]\n"
"s_waitcnt lgkmcnt(2)\n"
"ds_read_b64_tr_b16 a[252:253], v235 offset:49152\n"
"ds_read_b64_tr_b16 v[226:227], v239 offset:53248\n"
"v_mfma_f32_16x16x32_bf16 a[216:219], v[120:123], a[244:247], a[216:219]\n"
"s_add_u32 m0, s44, 81920\n"
"ds_read_b64_tr_b16 v[224:225], v239 offset:49152\n"
"v_cvt_pk_bf16_f32 v119, v178, v179\n"
"v_smfmac_f32_16x16x64_bf16 v[196:199], a[240:243], v[96:103], v241\n"
"buffer_load_dwordx4 v240, s[20:23], s56 offen nt lds\n"
"v_mfma_f32_16x16x32_bf16 v[8:11], v[96:99], a[244:247], v[8:11]\n"
"s_waitcnt lgkmcnt(3)\n"
"ds_read_b64_tr_b16 a[254:255], v235 offset:53248\n"
"v_cvt_pk_bf16_f32 v118, v176, v177\n"
"v_mfma_f32_16x16x32_bf16 v[8:11], v[100:103], a[248:251], v[8:11]\n"
"v_exp_f32 v174, v174\n"
"v_smfmac_f32_16x16x64_bf16 v[200:203], a[240:243], v[112:119], v241\n"
"v_cvt_pk_bf16_f32 v109, v162, v163\n"
"v_cvt_pk_bf16_f32 v111, v174, v175\n"
"v_mfma_f32_16x16x32_bf16 v[40:43], v[104:107], a[244:247], v[40:43]\n"
"ds_read_b128 v[212:215], v229 offset:0\n"
"v_and_b32 v245, 0x3f0, v242\n"
"v_mfma_f32_16x16x32_bf16 v[40:43], v[108:111], a[248:251], v[40:43]\n"
"v_lshlrev_b32_e64 v244, 24, s43\n"
"v_mul_f32 v244, 0x45800000, v244\n"
"v_smfmac_f32_16x16x64_bf16 v[198:201], a[240:243], v[104:111], v241\n"
"ds_read_b128 v[208:211], v228 offset:0\n"
"v_add_f32 v196, v196, v197\n"
"v_mfma_f32_16x16x32_bf16 v[72:75], v[112:115], a[244:247], v[72:75]\n"
"ds_read_b64_tr_b16 a[244:245], v234 offset:49664\n"
"v_lshrrev_b32 v245, 1, v245\n"
"v_mfma_f32_16x16x32_bf16 v[72:75], v[116:119], a[248:251], v[72:75]\n"
"ds_read_b64_tr_b16 a[246:247], v234 offset:53760\n"
"v_sub_u32 v245, v242, v245\n"
"v_mfma_f32_16x16x32_bf16 v[202:205], a[240:243], v[120:123], v[202:205]\n"
"s_waitcnt lgkmcnt(4)\n"
"ds_read_b64_tr_b16 a[248:249], v238 offset:49664\n"
"v_add_f32 v200, v200, v201\n"
"v_mfma_f32_16x16x32_bf16 v[76:79], v[116:119], v[224:227], v[76:79]\n"
"v_add_f32 v198, v198, v199\n"
"v_add_f32 v254, v196, v198\n"
"v_mfma_f32_16x16x32_bf16 v[76:79], v[112:115], a[252:255], v[76:79]\n"
"ds_read_b64_tr_b16 a[250:251], v238 offset:53760\n"
"v_xor_b32 v250, 0x1000, v245\n"
"v_mfma_f32_16x16x32_bf16 v[44:47], v[108:111], v[224:227], v[44:47]\n"
"ds_read_b128 v[216:219], v228 offset:512\n"
"v_add_f32 v254, v254, v200\n"
"v_mfma_f32_16x16x32_bf16 v[44:47], v[104:107], a[252:255], v[44:47]\n"
"v_add_f32 v244, v244, v202\n"
"v_add_f32 v254, v254, v202\n"
"v_mfma_f32_16x16x32_bf16 v[12:15], v[100:103], v[224:227], v[12:15]\n"
"s_add_u32 s40, s56, s52\n"
"ds_read_b64_tr_b16 v[226:227], v239 offset:53760\n"
"v_min3_f32 v244, v244, v196, v198\n"
"v_mfma_f32_16x16x32_bf16 v[12:15], v[96:99], a[252:255], v[12:15]\n"
"buffer_load_dwordx4 v207, s[20:23], s40 offen offset:1024 nt lds\n"
"v_min_f32 v244, v244, v200\n"
"v_rcp_f32 v244, v244\n"
"v_mfma_f32_16x16x32_bf16 a[220:223], v[120:123], a[252:255], a[220:223]\n"
"s_waitcnt lgkmcnt(4)\n"
"v_fmac_f32 v254, 0x68800000, v244\n"
"v_cmp_nge_f32 vcc, s45, v254\n"
"s_cbranch_vccnz LSLOW_%=\nLBACK0_%=:\ns_mov_b64 vcc, s[64:65]\n"
"v_mfma_f32_16x16x32_bf16 a[232:235], v[120:123], a[244:247], a[232:235]\n"
"v_rcp_f32 v246, v196\n"
"v_accvgpr_mov_b32 a2, a50\n"
"v_accvgpr_mov_b32 a7, a55\n"
"v_accvgpr_mov_b32 a10, a58\n"
"v_accvgpr_mov_b32 a15, a63\n"
"v_accvgpr_mov_b32 a22, a70\n"
"v_accvgpr_mov_b32 a30, a78\n"
"v_bfe_u32 v149, v242, 8, 2\n"
"v_lshl_add_u32 v149, v149, 4, s43\n"
"v_mfma_f32_16x16x32_bf16 v[24:27], v[96:99], a[244:247], v[24:27]\n"
"s_waitcnt lgkmcnt(2)\n"
"ds_read_b128 v[124:127], v149 offset:0\n"
"v_accvgpr_mov_b32 a28, a76\n"
"v_mfma_f32_16x16x32_bf16 v[24:27], v[100:103], a[248:251], v[24:27]\n"
"ds_read_b128 v[132:135], v149 offset:512\n"
"v_mov_b32_dpp v246, v246 row_ror:12 row_mask:0x2 bank_mask:0xf\n"
"v_accvgpr_mov_b32 a3, a51\n"
"v_accvgpr_mov_b32 a12, a60\n"
"v_accvgpr_mov_b32 a18, a66\n"
"v_accvgpr_mov_b32 a19, a67\n"
"v_accvgpr_mov_b32 a26, a74\n"
"v_accvgpr_mov_b32 a36, a84\n"
"v_xor_b32 v148, 0x400, v243\n"
"v_xor_b32 v150, 64, v149\n"
"v_mfma_f32_16x16x32_bf16 v[56:59], v[104:107], a[244:247], v[56:59]\n"
"ds_read_b128 v[128:131], v150 offset:0\n"
"v_accvgpr_mov_b32 a39, a87\n"
"v_mfma_f32_16x16x32_bf16 v[56:59], v[108:111], a[248:251], v[56:59]\n"
"ds_read_b128 v[136:139], v150 offset:512\n"
"v_rcp_f32 v247, v198\n"
"v_mov_b32_dpp v246, v246 row_ror:8 row_mask:0x4 bank_mask:0xf\n"
"v_accvgpr_mov_b32 a0, a48\n"
"v_accvgpr_mov_b32 a1, a49\n"
"v_accvgpr_mov_b32 a4, a52\n"
"v_accvgpr_mov_b32 a5, a53\n"
"v_accvgpr_mov_b32 a8, a56\n"
"v_accvgpr_mov_b32 a16, a64\n"
"v_accvgpr_mov_b32 a25, a73\n"
"v_mfma_f32_16x16x32_bf16 v[88:91], v[112:115], a[244:247], v[88:91]\n"
"v_accvgpr_mov_b32 a20, a68\n"
"v_accvgpr_mov_b32 a29, a77\n"
"v_mfma_f32_16x16x32_bf16 v[88:91], v[116:119], a[248:251], v[88:91]\n"
"ds_read_b128 a[192:195], v243 offset:4096\n"
"ds_read_b128 a[200:203], v243 offset:6144\n"
"v_accvgpr_mov_b32 a6, a54\n"
"v_accvgpr_mov_b32 a9, a57\n"
"v_accvgpr_mov_b32 a11, a59\n"
"v_accvgpr_mov_b32 a13, a61\n"
"v_accvgpr_mov_b32 a14, a62\n"
"v_accvgpr_mov_b32 a17, a65\n"
"v_accvgpr_mov_b32 a23, a71\n"
"v_accvgpr_mov_b32 a31, a79\n"
"v_accvgpr_mov_b32 a33, a81\n"
"v_accvgpr_mov_b32 a35, a83\n"
"v_accvgpr_mov_b32 a44, a92\n"
"v_accvgpr_mov_b32 a47, a95\n"
"s_waitcnt lgkmcnt(5)\n"
"v_mfma_f32_16x16x32_bf16 v[180:183], v[124:127], a[0:3], 0\n"
"v_xor_b32 v244, 0x11000, v242\n"
"s_waitcnt lgkmcnt(3)\n"
"v_mfma_f32_16x16x32_bf16 v[180:183], v[128:131], a[4:7], v[180:183]\n"
"v_accvgpr_mov_b32 a34, a82\n"
"v_accvgpr_mov_b32 a43, a91\n"
"v_mfma_f32_16x16x32_bf16 v[180:183], v[132:135], a[8:11], v[180:183]\n"
"s_waitcnt lgkmcnt(2)\n"
"ds_read_b128 a[204:207], v148 offset:6144\n"
"v_accvgpr_mov_b32 a37, a85\n"
"v_mfma_f32_16x16x32_bf16 v[180:183], v[136:139], a[12:15], v[180:183]\n"
"ds_read_b128 a[196:199], v148 offset:4096\n"
"v_accvgpr_mov_b32 a21, a69\n"
"v_accvgpr_mov_b32 a24, a72\n"
"v_accvgpr_mov_b32 a32, a80\n"
"v_accvgpr_mov_b32 a38, a86\n"
"v_accvgpr_mov_b32 a40, a88\n"
"v_accvgpr_mov_b32 a42, a90\n"
"v_accvgpr_mov_b32 a46, a94\n"
"v_mfma_f32_16x16x32_bf16 v[184:187], v[124:127], a[16:19], 0\n"
"v_mov_b32_dpp v246, v246 row_ror:4 row_mask:0x8 bank_mask:0xf\n"
"v_accvgpr_mov_b32 a27, a75\n"
"v_mfma_f32_16x16x32_bf16 v[184:187], v[128:131], a[20:23], v[184:187]\n"
"v_mov_b32_dpp v247, v247 row_ror:12 row_mask:0x2 bank_mask:0xf\n"
"v_mfma_f32_16x16x32_bf16 v[184:187], v[132:135], a[24:27], v[184:187]\n"
"s_add_u32 s40, s56, s53\n"
"ds_read_b64_tr_b16 v[224:225], v239 offset:49664\n"
"v_mfma_f32_16x16x32_bf16 v[184:187], v[136:139], a[28:31], v[184:187]\n"
"buffer_load_dwordx4 v240, s[20:23], s40 offen offset:2048 nt lds\n"
"v_accvgpr_mov_b32 a41, a89\n"
"v_sub_f32 v180, s74, v180\n"
"v_sub_f32 v181, s74, v181\n"
"v_sub_f32 v183, s74, v183\n"
"v_mul_f32_dpp v26, v246, v26 row_newbcast:2 row_mask:0xf bank_mask:0xf\n"
"v_mfma_f32_16x16x32_bf16 v[188:191], v[124:127], a[32:35], 0\n"
"v_mov_b32_dpp v247, v247 row_ror:8 row_mask:0x4 bank_mask:0xf\n"
"v_accvgpr_mov_b32 a45, a93\n"
"v_mfma_f32_16x16x32_bf16 v[188:191], v[128:131], a[36:39], v[188:191]\n"
"v_mov_b32 v252, v192\n"
"v_mfma_f32_16x16x32_bf16 v[188:191], v[132:135], a[40:43], v[188:191]\n"
"v_mfma_f32_16x16x32_bf16 v[188:191], v[136:139], a[44:47], v[188:191]\n"
"v_mul_f32_dpp v11, v246, v11 row_newbcast:3 row_mask:0xf bank_mask:0xf\n"
"s_waitcnt lgkmcnt(1)\n"
"v_mfma_f32_16x16x32_bf16 v[192:195], v[124:127], a[192:195], 0\n"
"ds_read_b64_tr_b16 a[252:253], v235 offset:49664\n"
"v_sub_f32 v185, s74, v185\n"
"v_mfma_f32_16x16x32_bf16 v[192:195], v[128:131], a[196:199], v[192:195]\n"
"ds_read_b64_tr_b16 a[248:249], v236 offset:49152\n"
"v_sub_f32 v182, s74, v182\n"
"v_mfma_f32_16x16x32_bf16 v[192:195], v[132:135], a[200:203], v[192:195]\n"
"v_sub_f32 v184, s74, v184\n"
"v_mfma_f32_16x16x32_bf16 v[192:195], v[136:139], a[204:207], v[192:195]\n"
"ds_read_b64_tr_b16 a[254:255], v235 offset:53760\n"
"ds_read_b64_tr_b16 a[246:247], v232 offset:53248\n"
"v_mov_b32 v253, v202\n"
"v_sub_f32 v186, s74, v186\n"
"v_mfma_f32_16x16x32_bf16 v[152:155], v[208:211], a[0:3], v[180:183]\n"
"v_mfma_f32_16x16x32_bf16 v[152:155], v[212:215], a[4:7], v[152:155]\n"
"v_sub_f32 v187, s74, v187\n"
"v_mfma_f32_16x16x32_bf16 v[152:155], v[216:219], a[8:11], v[152:155]\n"
"s_add_u32 s40, s56, s54\n"
"v_mfma_f32_16x16x32_bf16 v[152:155], v[220:223], a[12:15], v[152:155]\n"
"buffer_load_dwordx4 v207, s[20:23], s40 offen offset:3072 nt lds\n"
"v_mov_b32_dpp v247, v247 row_ror:4 row_mask:0x8 bank_mask:0xf\n"
"v_sub_f32 v189, s74, v189\n"
"v_sub_f32 v190, s74, v190\n"
"v_sub_f32 v191, s74, v191\n"
"v_sub_f32 v192, s74, v192\n"
"v_mfma_f32_16x16x32_bf16 v[156:159], v[216:219], a[24:27], v[184:187]\n"
"s_add_u32 s55, s55, s59\n"
"v_mfma_f32_16x16x32_bf16 v[156:159], v[220:223], a[28:31], v[156:159]\n"
"s_or_b32 s55, s55, s58\n"
"v_sub_f32 v188, s74, v188\n"
"v_mfma_f32_16x16x32_bf16 v[156:159], v[208:211], a[16:19], v[156:159]\n"
"s_xor_b32 s56, s55, s57\n"
"v_exp_f32 v153, v153\n"
"v_mfma_f32_16x16x32_bf16 v[156:159], v[212:215], a[20:23], v[156:159]\n"
"ds_write_b64 v250, v[252:253] offset:52224\n"
"v_sub_f32 v193, s74, v193\n"
"v_sub_f32 v194, s74, v194\n"
"v_exp_f32 v152, v152\n"
"v_mfma_f32_16x16x32_bf16 v[160:163], v[208:211], a[32:35], v[188:191]\n"
"v_mfma_f32_16x16x32_bf16 v[160:163], v[212:215], a[36:39], v[160:163]\n"
"v_rcp_f32 v206, v200\n"
"v_mfma_f32_16x16x32_bf16 v[160:163], v[216:219], a[40:43], v[160:163]\n"
"v_sub_f32 v195, s74, v195\n"
"v_mfma_f32_16x16x32_bf16 v[160:163], v[220:223], a[44:47], v[160:163]\n"
"ds_write_b128 v244, a[216:219] offset:32768\n"
"v_mul_f32_dpp v25, v246, v25 row_newbcast:1 row_mask:0xf bank_mask:0xf\n"
"v_mfma_f32_16x16x32_bf16 v[164:167], v[216:219], a[200:203], v[192:195]\n"
"v_mov_b32_dpp v206, v206 row_ror:12 row_mask:0x2 bank_mask:0xf\n"
"v_mfma_f32_16x16x32_bf16 v[164:167], v[220:223], a[204:207], v[164:167]\n"
"v_mfma_f32_16x16x32_bf16 v[164:167], v[208:211], a[192:195], v[164:167]\n"
"ds_read_b128 v[208:211], v228 offset:4096\n"
"v_mfma_f32_16x16x32_bf16 v[164:167], v[212:215], a[196:199], v[164:167]\n"
"ds_read_b128 v[216:219], v228 offset:4608\n"
"ds_read_b128 v[220:223], v229 offset:4608\n"
"v_mov_b32_dpp v206, v206 row_ror:8 row_mask:0x4 bank_mask:0xf\n"
"v_exp_f32 v155, v155\n"
"v_exp_f32 v160, v160\n"
"s_branch LTC_%=\n"
"LTH1_%=:\n"
"v_mfma_f32_16x16x32_bf16 a[224:227], v[124:127], a[244:247], a[224:227]\n"
"v_exp_f32 v171, v171\n"
"v_mfma_f32_16x16x32_bf16 v[16:19], v[128:131], a[244:247], v[16:19]\n"
"s_waitcnt vmcnt(8)\n"
"v_exp_f32 v172, v172\n"
"v_mfma_f32_16x16x32_bf16 v[16:19], v[132:135], a[248:251], v[16:19]\n"
"s_barrier\n"
"v_mfma_f32_16x16x32_bf16 v[48:51], v[136:139], a[244:247], v[48:51]\n"
"v_exp_f32 v169, v169\n"
"v_mfma_f32_16x16x32_bf16 v[48:51], v[140:143], a[248:251], v[48:51]\n"
"ds_read_b128 v[220:223], v229 offset:512\n"
"v_cvt_pk_bf16_f32 v117, v166, v167\n"
"v_exp_f32 v176, v176\n"
"v_exp_f32 v177, v177\n"
"v_exp_f32 v178, v178\n"
"v_mfma_f32_16x16x32_bf16 v[80:83], v[144:147], a[244:247], v[80:83]\n"
"ds_read_b64_tr_b16 a[244:245], v234 offset:49152\n"
"v_cvt_pk_bf16_f32 v108, v160, v161\n"
"v_mfma_f32_16x16x32_bf16 v[80:83], v[148:151], a[248:251], v[80:83]\n"
"ds_read_b64_tr_b16 a[246:247], v234 offset:53248\n"
"ds_read_b64_tr_b16 a[248:249], v238 offset:49152\n"
"v_exp_f32 v162, v162\n"
"v_exp_f32 v168, v168\n"
"v_exp_f32 v173, v173\n"
"v_exp_f32 v175, v175\n"
"v_mfma_f32_16x16x32_bf16 v[84:87], v[148:151], v[224:227], v[84:87]\n"
"ds_read_b64_tr_b16 a[250:251], v238 offset:53248\n"
"v_cvt_pk_bf16_f32 v116, v164, v165\n"
"v_mfma_f32_16x16x32_bf16 v[84:87], v[144:147], a[252:255], v[84:87]\n"
"v_exp_f32 v170, v170\n"
"v_mfma_f32_16x16x32_bf16 v[52:55], v[140:143], v[224:227], v[52:55]\n"
"v_cvt_pk_bf16_f32 v103, v170, v171\n"
"v_cvt_pk_bf16_f32 v110, v172, v173\n"
"v_mfma_f32_16x16x32_bf16 v[52:55], v[136:139], a[252:255], v[52:55]\n"
"v_cvt_pk_bf16_f32 v123, v154, v155\n"
"v_cvt_pk_bf16_f32 v101, v158, v159\n"
"v_mfma_f32_16x16x32_bf16 v[20:23], v[132:135], v[224:227], v[20:23]\n"
"v_cvt_pk_bf16_f32 v100, v156, v157\n"
"v_cvt_pk_bf16_f32 v102, v168, v169\n"
"v_mfma_f32_16x16x32_bf16 v[20:23], v[128:131], a[252:255], v[20:23]\n"
"v_exp_f32 v179, v179\n"
"v_mfma_f32_16x16x32_bf16 a[228:231], v[124:127], a[252:255], a[228:231]\n"
"s_waitcnt lgkmcnt(2)\n"
"ds_read_b64_tr_b16 a[252:253], v235 offset:49152\n"
"ds_read_b64_tr_b16 v[226:227], v239 offset:53248\n"
"v_mfma_f32_16x16x32_bf16 a[216:219], v[120:123], a[244:247], a[216:219]\n"
"s_add_u32 m0, s44, 81920\n"
"ds_read_b64_tr_b16 v[224:225], v239 offset:49152\n"
"v_cvt_pk_bf16_f32 v119, v178, v179\n"
"v_smfmac_f32_16x16x64_bf16 v[196:199], a[240:243], v[96:103], v241\n"
"buffer_load_dwordx4 v240, s[20:23], s56 offen nt lds\n"
"v_mfma_f32_16x16x32_bf16 v[8:11], v[96:99], a[244:247], v[8:11]\n"
"s_waitcnt lgkmcnt(3)\n"
"ds_read_b64_tr_b16 a[254:255], v235 offset:53248\n"
"v_cvt_pk_bf16_f32 v118, v176, v177\n"
"v_mfma_f32_16x16x32_bf16 v[8:11], v[100:103], a[248:251], v[8:11]\n"
"v_exp_f32 v174, v174\n"
"v_smfmac_f32_16x16x64_bf16 v[200:203], a[240:243], v[112:119], v241\n"
"v_cvt_pk_bf16_f32 v109, v162, v163\n"
"v_cvt_pk_bf16_f32 v111, v174, v175\n"
"v_mfma_f32_16x16x32_bf16 v[40:43], v[104:107], a[244:247], v[40:43]\n"
"ds_read_b128 v[212:215], v229 offset:0\n"
"v_and_b32 v245, 0x3f0, v242\n"
"v_mfma_f32_16x16x32_bf16 v[40:43], v[108:111], a[248:251], v[40:43]\n"
"v_lshlrev_b32_e64 v244, 24, s43\n"
"v_mul_f32 v244, 0x45800000, v244\n"
"v_smfmac_f32_16x16x64_bf16 v[198:201], a[240:243], v[104:111], v241\n"
"ds_read_b128 v[208:211], v228 offset:0\n"
"v_add_f32 v196, v196, v197\n"
"v_mfma_f32_16x16x32_bf16 v[72:75], v[112:115], a[244:247], v[72:75]\n"
"ds_read_b64_tr_b16 a[244:245], v234 offset:49664\n"
"v_lshrrev_b32 v245, 1, v245\n"
"v_mfma_f32_16x16x32_bf16 v[72:75], v[116:119], a[248:251], v[72:75]\n"
"ds_read_b64_tr_b16 a[246:247], v234 offset:53760\n"
"v_sub_u32 v245, v242, v245\n"
"v_mfma_f32_16x16x32_bf16 v[202:205], a[240:243], v[120:123], v[202:205]\n"
"s_waitcnt lgkmcnt(4)\n"
"ds_read_b64_tr_b16 a[248:249], v238 offset:49664\n"
"v_add_f32 v200, v200, v201\n"
"v_mfma_f32_16x16x32_bf16 v[76:79], v[116:119], v[224:227], v[76:79]\n"
"v_add_f32 v198, v198, v199\n"
"v_add_f32 v254, v196, v198\n"
"v_mfma_f32_16x16x32_bf16 v[76:79], v[112:115], a[252:255], v[76:79]\n"
"ds_read_b64_tr_b16 a[250:251], v238 offset:53760\n"
"v_xor_b32 v250, 0x1000, v245\n"
"v_mfma_f32_16x16x32_bf16 v[44:47], v[108:111], v[224:227], v[44:47]\n"
"ds_read_b128 v[216:219], v228 offset:512\n"
"v_add_f32 v254, v254, v200\n"
"v_mfma_f32_16x16x32_bf16 v[44:47], v[104:107], a[252:255], v[44:47]\n"
"v_add_f32 v244, v244, v202\n"
"v_add_f32 v254, v254, v202\n"
"v_mfma_f32_16x16x32_bf16 v[12:15], v[100:103], v[224:227], v[12:15]\n"
"s_add_u32 s40, s56, s52\n"
"ds_read_b64_tr_b16 v[226:227], v239 offset:53760\n"
"v_min3_f32 v244, v244, v196, v198\n"
"v_mfma_f32_16x16x32_bf16 v[12:15], v[96:99], a[252:255], v[12:15]\n"
"buffer_load_dwordx4 v207, s[20:23], s40 offen offset:1024 nt lds\n"
"v_min_f32 v244, v244, v200\n"
"v_rcp_f32 v244, v244\n"
"v_mfma_f32_16x16x32_bf16 a[220:223], v[120:123], a[252:255], a[220:223]\n"
"s_waitcnt lgkmcnt(4)\n"
"v_fmac_f32 v254, 0x68800000, v244\n"
"v_cmp_nge_f32 vcc, s45, v254\n"
"s_cbranch_vccnz LSLOW_%=\nLBACK1_%=:\ns_mov_b64 vcc, s[64:65]\n"
"v_mfma_f32_16x16x32_bf16 a[232:235], v[120:123], a[244:247], a[232:235]\n"
"v_rcp_f32 v246, v196\n"
"v_accvgpr_mov_b32 a2, a98\n"
"v_accvgpr_mov_b32 a7, a103\n"
"v_accvgpr_mov_b32 a10, a106\n"
"v_accvgpr_mov_b32 a15, a111\n"
"v_accvgpr_mov_b32 a22, a118\n"
"v_accvgpr_mov_b32 a30, a126\n"
"v_bfe_u32 v149, v242, 8, 2\n"
"v_lshl_add_u32 v149, v149, 4, s43\n"
"v_mfma_f32_16x16x32_bf16 v[24:27], v[96:99], a[244:247], v[24:27]\n"
"s_waitcnt lgkmcnt(2)\n"
"ds_read_b128 v[124:127], v149 offset:0\n"
"v_accvgpr_mov_b32 a28, a124\n"
"v_mfma_f32_16x16x32_bf16 v[24:27], v[100:103], a[248:251], v[24:27]\n"
"ds_read_b128 v[132:135], v149 offset:512\n"
"v_mov_b32_dpp v246, v246 row_ror:12 row_mask:0x2 bank_mask:0xf\n"
"v_accvgpr_mov_b32 a3, a99\n"
"v_accvgpr_mov_b32 a12, a108\n"
"v_accvgpr_mov_b32 a18, a114\n"
"v_accvgpr_mov_b32 a19, a115\n"
"v_accvgpr_mov_b32 a26, a122\n"
"v_accvgpr_mov_b32 a36, a132\n"
"v_xor_b32 v148, 0x400, v243\n"
"v_xor_b32 v150, 64, v149\n"
"v_accvgpr_mov_b32 a124, a76\n"
"v_mfma_f32_16x16x32_bf16 v[56:59], v[104:107], a[244:247], v[56:59]\n"
"ds_read_b128 v[128:131], v150 offset:0\n"
"v_accvgpr_mov_b32 a39, a135\n"
"v_mfma_f32_16x16x32_bf16 v[56:59], v[108:111], a[248:251], v[56:59]\n"
"ds_read_b128 v[136:139], v150 offset:512\n"
"v_rcp_f32 v247, v198\n"
"v_mov_b32_dpp v246, v246 row_ror:8 row_mask:0x4 bank_mask:0xf\n"
"v_accvgpr_mov_b32 a0, a96\n"
"v_accvgpr_mov_b32 a1, a97\n"
"v_accvgpr_mov_b32 a4, a100\n"
"v_accvgpr_mov_b32 a5, a101\n"
"v_accvgpr_mov_b32 a8, a104\n"
"v_accvgpr_mov_b32 a16, a112\n"
"v_accvgpr_mov_b32 a25, a121\n"
"v_mfma_f32_16x16x32_bf16 v[88:91], v[112:115], a[244:247], v[88:91]\n"
"v_accvgpr_mov_b32 a20, a116\n"
"v_accvgpr_mov_b32 a29, a125\n"
"v_mfma_f32_16x16x32_bf16 v[88:91], v[116:119], a[248:251], v[88:91]\n"
"s_waitcnt lgkmcnt(3)\n"
"ds_read_b128 a[192:195], v243 offset:4096\n"
"ds_read_b128 a[200:203], v243 offset:6144\n"
"v_accvgpr_mov_b32 a6, a102\n"
"v_accvgpr_mov_b32 a9, a105\n"
"v_accvgpr_mov_b32 a11, a107\n"
"v_accvgpr_mov_b32 a13, a109\n"
"v_accvgpr_mov_b32 a14, a110\n"
"v_accvgpr_mov_b32 a17, a113\n"
"v_accvgpr_mov_b32 a23, a119\n"
"v_accvgpr_mov_b32 a31, a127\n"
"v_accvgpr_mov_b32 a33, a129\n"
"v_accvgpr_mov_b32 a35, a131\n"
"v_accvgpr_mov_b32 a44, a140\n"
"v_accvgpr_mov_b32 a47, a143\n"
"v_accvgpr_mov_b32 a107, a59\n"
"v_mfma_f32_16x16x32_bf16 v[180:183], v[124:127], a[0:3], 0\n"
"s_waitcnt lgkmcnt(3)\n"
"v_xor_b32 v244, 0x11000, v242\n"
"v_accvgpr_mov_b32 a132, a84\n"
"v_mfma_f32_16x16x32_bf16 v[180:183], v[128:131], a[4:7], v[180:183]\n"
"v_accvgpr_mov_b32 a34, a130\n"
"v_accvgpr_mov_b32 a43, a139\n"
"v_mfma_f32_16x16x32_bf16 v[180:183], v[132:135], a[8:11], v[180:183]\n"
"s_waitcnt lgkmcnt(2)\n"
"ds_read_b128 a[204:207], v148 offset:6144\n"
"v_accvgpr_mov_b32 a37, a133\n"
"v_mfma_f32_16x16x32_bf16 v[180:183], v[136:139], a[12:15], v[180:183]\n"
"ds_read_b128 a[196:199], v148 offset:4096\n"
"v_accvgpr_mov_b32 a21, a117\n"
"v_accvgpr_mov_b32 a24, a120\n"
"v_accvgpr_mov_b32 a32, a128\n"
"v_accvgpr_mov_b32 a38, a134\n"
"v_accvgpr_mov_b32 a40, a136\n"
"v_accvgpr_mov_b32 a42, a138\n"
"v_accvgpr_mov_b32 a46, a142\n"
"v_accvgpr_mov_b32 a117, a69\n"
"v_accvgpr_mov_b32 a111, a63\n"
"v_mfma_f32_16x16x32_bf16 v[184:187], v[124:127], a[16:19], 0\n"
"v_mov_b32_dpp v246, v246 row_ror:4 row_mask:0x8 bank_mask:0xf\n"
"v_accvgpr_mov_b32 a27, a123\n"
"v_mfma_f32_16x16x32_bf16 v[184:187], v[128:131], a[20:23], v[184:187]\n"
"v_mov_b32_dpp v247, v247 row_ror:12 row_mask:0x2 bank_mask:0xf\n"
"v_accvgpr_mov_b32 a97, a49\n"
"v_mfma_f32_16x16x32_bf16 v[184:187], v[132:135], a[24:27], v[184:187]\n"
"s_add_u32 s40, s56, s53\n"
"ds_read_b64_tr_b16 v[224:225], v239 offset:49664\n"
"v_accvgpr_mov_b32 a112, a64\n"
"v_mfma_f32_16x16x32_bf16 v[184:187], v[136:139], a[28:31], v[184:187]\n"
"buffer_load_dwordx4 v240, s[20:23], s40 offen offset:2048 nt lds\n"
"v_accvgpr_mov_b32 a41, a137\n"
"v_accvgpr_mov_b32 a126, a78\n"
"v_accvgpr_mov_b32 a114, a66\n"
"v_accvgpr_mov_b32 a137, a89\n"
"v_sub_f32 v180, s74, v180\n"
"v_sub_f32 v181, s74, v181\n"
"v_sub_f32 v183, s74, v183\n"
"v_mul_f32_dpp v26, v246, v26 row_newbcast:2 row_mask:0xf bank_mask:0xf\n"
"v_mfma_f32_16x16x32_bf16 v[188:191], v[124:127], a[32:35], 0\n"
"v_mov_b32_dpp v247, v247 row_ror:8 row_mask:0x4 bank_mask:0xf\n"
"v_accvgpr_mov_b32 a45, a141\n"
"v_mfma_f32_16x16x32_bf16 v[188:191], v[128:131], a[36:39], v[188:191]\n"
"v_mov_b32 v252, v192\n"
"v_accvgpr_mov_b32 a133, a85\n"
"v_mfma_f32_16x16x32_bf16 v[188:191], v[132:135], a[40:43], v[188:191]\n"
"v_accvgpr_mov_b32 a118, a70\n"
"v_accvgpr_mov_b32 a105, a57\n"
"v_mfma_f32_16x16x32_bf16 v[188:191], v[136:139], a[44:47], v[188:191]\n"
"s_waitcnt lgkmcnt(1)\n"
"v_accvgpr_mov_b32 a109, a61\n"
"v_mul_f32_dpp v11, v246, v11 row_newbcast:3 row_mask:0xf bank_mask:0xf\n"
"v_mfma_f32_16x16x32_bf16 v[192:195], v[124:127], a[192:195], 0\n"
"ds_read_b64_tr_b16 a[252:253], v235 offset:49664\n"
"v_sub_f32 v185, s74, v185\n"
"v_mfma_f32_16x16x32_bf16 v[192:195], v[128:131], a[196:199], v[192:195]\n"
"ds_read_b64_tr_b16 a[248:249], v236 offset:49152\n"
"v_sub_f32 v182, s74, v182\n"
"v_mfma_f32_16x16x32_bf16 v[192:195], v[132:135], a[200:203], v[192:195]\n"
"v_accvgpr_mov_b32 a138, a90\n"
"v_sub_f32 v184, s74, v184\n"
"v_mfma_f32_16x16x32_bf16 v[192:195], v[136:139], a[204:207], v[192:195]\n"
"ds_read_b64_tr_b16 a[254:255], v235 offset:53760\n"
"ds_read_b64_tr_b16 a[246:247], v232 offset:53248\n"
"v_mov_b32 v253, v202\n"
"v_accvgpr_mov_b32 a116, a68\n"
"v_accvgpr_mov_b32 a125, a77\n"
"v_accvgpr_mov_b32 a113, a65\n"
"v_accvgpr_mov_b32 a128, a80\n"
"v_accvgpr_mov_b32 a130, a82\n"
"v_accvgpr_mov_b32 a136, a88\n"
"v_sub_f32 v186, s74, v186\n"
"v_mfma_f32_16x16x32_bf16 v[152:155], v[208:211], a[0:3], v[180:183]\n"
"v_accvgpr_mov_b32 a103, a55\n"
"v_accvgpr_mov_b32 a120, a72\n"
"v_mfma_f32_16x16x32_bf16 v[152:155], v[212:215], a[4:7], v[152:155]\n"
"v_accvgpr_mov_b32 a141, a93\n"
"v_sub_f32 v187, s74, v187\n"
"v_mfma_f32_16x16x32_bf16 v[152:155], v[216:219], a[8:11], v[152:155]\n"
"s_add_u32 s40, s56, s54\n"
"v_accvgpr_mov_b32 a127, a79\n"
"v_accvgpr_mov_b32 a140, a92\n"
"v_mfma_f32_16x16x32_bf16 v[152:155], v[220:223], a[12:15], v[152:155]\n"
"buffer_load_dwordx4 v207, s[20:23], s40 offen offset:3072 nt lds\n"
"v_mov_b32_dpp v247, v247 row_ror:4 row_mask:0x8 bank_mask:0xf\n"
"v_accvgpr_mov_b32 a121, a73\n"
"v_accvgpr_mov_b32 a122, a74\n"
"v_accvgpr_mov_b32 a123, a75\n"
"v_sub_f32 v189, s74, v189\n"
"v_sub_f32 v190, s74, v190\n"
"v_sub_f32 v191, s74, v191\n"
"v_sub_f32 v192, s74, v192\n"
"v_mfma_f32_16x16x32_bf16 v[156:159], v[216:219], a[24:27], v[184:187]\n"
"s_add_u32 s55, s55, s59\n"
"v_accvgpr_mov_b32 a143, a95\n"
"v_accvgpr_mov_b32 a115, a67\n"
"v_mfma_f32_16x16x32_bf16 v[156:159], v[220:223], a[28:31], v[156:159]\n"
"s_or_b32 s55, s55, s58\n"
"v_accvgpr_mov_b32 a101, a53\n"
"v_sub_f32 v188, s74, v188\n"
"v_mfma_f32_16x16x32_bf16 v[156:159], v[208:211], a[16:19], v[156:159]\n"
"s_xor_b32 s56, s55, s57\n"
"v_exp_f32 v153, v153\n"
"v_mfma_f32_16x16x32_bf16 v[156:159], v[212:215], a[20:23], v[156:159]\n"
"ds_write_b64 v250, v[252:253] offset:52224\n"
"v_accvgpr_mov_b32 a100, a52\n"
"v_accvgpr_mov_b32 a135, a87\n"
"v_accvgpr_mov_b32 a98, a50\n"
"v_sub_f32 v193, s74, v193\n"
"v_sub_f32 v194, s74, v194\n"
"v_exp_f32 v152, v152\n"
"v_mfma_f32_16x16x32_bf16 v[160:163], v[208:211], a[32:35], v[188:191]\n"
"v_accvgpr_mov_b32 a110, a62\n"
"v_accvgpr_mov_b32 a96, a48\n"
"v_mfma_f32_16x16x32_bf16 v[160:163], v[212:215], a[36:39], v[160:163]\n"
"v_rcp_f32 v206, v200\n"
"v_mfma_f32_16x16x32_bf16 v[160:163], v[216:219], a[40:43], v[160:163]\n"
"v_accvgpr_mov_b32 a142, a94\n"
"v_sub_f32 v195, s74, v195\n"
"v_mfma_f32_16x16x32_bf16 v[160:163], v[220:223], a[44:47], v[160:163]\n"
"ds_write_b128 v244, a[216:219] offset:32768\n"
"v_accvgpr_mov_b32 a102, a54\n"
"v_accvgpr_mov_b32 a108, a60\n"
"v_accvgpr_mov_b32 a99, a51\n"
"v_accvgpr_mov_b32 a129, a81\n"
"v_mul_f32_dpp v25, v246, v25 row_newbcast:1 row_mask:0xf bank_mask:0xf\n"
"v_mfma_f32_16x16x32_bf16 v[164:167], v[216:219], a[200:203], v[192:195]\n"
"v_mov_b32_dpp v206, v206 row_ror:12 row_mask:0x2 bank_mask:0xf\n"
"v_accvgpr_mov_b32 a119, a71\n"
"v_mfma_f32_16x16x32_bf16 v[164:167], v[220:223], a[204:207], v[164:167]\n"
"v_accvgpr_mov_b32 a131, a83\n"
"v_accvgpr_mov_b32 a139, a91\n"
"v_mfma_f32_16x16x32_bf16 v[164:167], v[208:211], a[192:195], v[164:167]\n"
"ds_read_b128 v[208:211], v228 offset:4096\n"
"v_accvgpr_mov_b32 a106, a58\n"
"v_mfma_f32_16x16x32_bf16 v[164:167], v[212:215], a[196:199], v[164:167]\n"
"s_waitcnt lgkmcnt(2)\n"
"ds_read_b128 v[216:219], v228 offset:4608\n"
"ds_read_b128 v[220:223], v229 offset:4608\n"
"v_mov_b32_dpp v206, v206 row_ror:8 row_mask:0x4 bank_mask:0xf\n"
"v_accvgpr_mov_b32 a134, a86\n"
"v_accvgpr_mov_b32 a104, a56\n"
"v_exp_f32 v155, v155\n"
"v_exp_f32 v160, v160\n"
"s_branch LTC_%=\n"
"LTH2_%=:\n"
"v_mfma_f32_16x16x32_bf16 a[224:227], v[124:127], a[244:247], a[224:227]\n"
"v_exp_f32 v171, v171\n"
"v_mfma_f32_16x16x32_bf16 v[16:19], v[128:131], a[244:247], v[16:19]\n"
"s_waitcnt vmcnt(8)\n"
"v_exp_f32 v172, v172\n"
"v_mfma_f32_16x16x32_bf16 v[16:19], v[132:135], a[248:251], v[16:19]\n"
"s_barrier\n"
"v_mfma_f32_16x16x32_bf16 v[48:51], v[136:139], a[244:247], v[48:51]\n"
"v_exp_f32 v169, v169\n"
"v_mfma_f32_16x16x32_bf16 v[48:51], v[140:143], a[248:251], v[48:51]\n"
"ds_read_b128 v[220:223], v229 offset:512\n"
"v_cvt_pk_bf16_f32 v117, v166, v167\n"
"v_exp_f32 v176, v176\n"
"v_exp_f32 v177, v177\n"
"v_exp_f32 v178, v178\n"
"v_mfma_f32_16x16x32_bf16 v[80:83], v[144:147], a[244:247], v[80:83]\n"
"ds_read_b64_tr_b16 a[244:245], v234 offset:49152\n"
"v_cvt_pk_bf16_f32 v108, v160, v161\n"
"v_mfma_f32_16x16x32_bf16 v[80:83], v[148:151], a[248:251], v[80:83]\n"
"ds_read_b64_tr_b16 a[246:247], v234 offset:53248\n"
"ds_read_b64_tr_b16 a[248:249], v238 offset:49152\n"
"v_exp_f32 v162, v162\n"
"v_exp_f32 v168, v168\n"
"v_exp_f32 v173, v173\n"
"v_exp_f32 v175, v175\n"
"v_mfma_f32_16x16x32_bf16 v[84:87], v[148:151], v[224:227], v[84:87]\n"
"ds_read_b64_tr_b16 a[250:251], v238 offset:53248\n"
"v_cvt_pk_bf16_f32 v116, v164, v165\n"
"v_mfma_f32_16x16x32_bf16 v[84:87], v[144:147], a[252:255], v[84:87]\n"
"v_exp_f32 v170, v170\n"
"v_mfma_f32_16x16x32_bf16 v[52:55], v[140:143], v[224:227], v[52:55]\n"
"v_cvt_pk_bf16_f32 v103, v170, v171\n"
"v_cvt_pk_bf16_f32 v110, v172, v173\n"
"v_mfma_f32_16x16x32_bf16 v[52:55], v[136:139], a[252:255], v[52:55]\n"
"v_cvt_pk_bf16_f32 v123, v154, v155\n"
"v_cvt_pk_bf16_f32 v101, v158, v159\n"
"v_mfma_f32_16x16x32_bf16 v[20:23], v[132:135], v[224:227], v[20:23]\n"
"v_cvt_pk_bf16_f32 v100, v156, v157\n"
"v_cvt_pk_bf16_f32 v102, v168, v169\n"
"v_mfma_f32_16x16x32_bf16 v[20:23], v[128:131], a[252:255], v[20:23]\n"
"v_exp_f32 v179, v179\n"
"v_mfma_f32_16x16x32_bf16 a[228:231], v[124:127], a[252:255], a[228:231]\n"
"s_waitcnt lgkmcnt(2)\n"
"ds_read_b64_tr_b16 a[252:253], v235 offset:49152\n"
"ds_read_b64_tr_b16 v[226:227], v239 offset:53248\n"
"v_mfma_f32_16x16x32_bf16 a[216:219], v[120:123], a[244:247], a[216:219]\n"
"s_add_u32 m0, s44, 81920\n"
"ds_read_b64_tr_b16 v[224:225], v239 offset:49152\n"
"v_cvt_pk_bf16_f32 v119, v178, v179\n"
"v_smfmac_f32_16x16x64_bf16 v[196:199], a[240:243], v[96:103], v241\n"
"buffer_load_dwordx4 v240, s[20:23], s56 offen nt lds\n"
"v_mfma_f32_16x16x32_bf16 v[8:11], v[96:99], a[244:247], v[8:11]\n"
"s_waitcnt lgkmcnt(3)\n"
"ds_read_b64_tr_b16 a[254:255], v235 offset:53248\n"
"v_cvt_pk_bf16_f32 v118, v176, v177\n"
"v_mfma_f32_16x16x32_bf16 v[8:11], v[100:103], a[248:251], v[8:11]\n"
"v_exp_f32 v174, v174\n"
"v_smfmac_f32_16x16x64_bf16 v[200:203], a[240:243], v[112:119], v241\n"
"v_cvt_pk_bf16_f32 v109, v162, v163\n"
"v_cvt_pk_bf16_f32 v111, v174, v175\n"
"v_mfma_f32_16x16x32_bf16 v[40:43], v[104:107], a[244:247], v[40:43]\n"
"ds_read_b128 v[212:215], v229 offset:0\n"
"v_and_b32 v245, 0x3f0, v242\n"
"v_mfma_f32_16x16x32_bf16 v[40:43], v[108:111], a[248:251], v[40:43]\n"
"v_lshlrev_b32_e64 v244, 24, s43\n"
"v_mul_f32 v244, 0x45800000, v244\n"
"v_smfmac_f32_16x16x64_bf16 v[198:201], a[240:243], v[104:111], v241\n"
"ds_read_b128 v[208:211], v228 offset:0\n"
"v_add_f32 v196, v196, v197\n"
"v_mfma_f32_16x16x32_bf16 v[72:75], v[112:115], a[244:247], v[72:75]\n"
"ds_read_b64_tr_b16 a[244:245], v234 offset:49664\n"
"v_lshrrev_b32 v245, 1, v245\n"
"v_mfma_f32_16x16x32_bf16 v[72:75], v[116:119], a[248:251], v[72:75]\n"
"ds_read_b64_tr_b16 a[246:247], v234 offset:53760\n"
"v_sub_u32 v245, v242, v245\n"
"v_mfma_f32_16x16x32_bf16 v[202:205], a[240:243], v[120:123], v[202:205]\n"
"s_waitcnt lgkmcnt(4)\n"
"ds_read_b64_tr_b16 a[248:249], v238 offset:49664\n"
"v_add_f32 v200, v200, v201\n"
"v_mfma_f32_16x16x32_bf16 v[76:79], v[116:119], v[224:227], v[76:79]\n"
"v_add_f32 v198, v198, v199\n"
"v_add_f32 v254, v196, v198\n"
"v_mfma_f32_16x16x32_bf16 v[76:79], v[112:115], a[252:255], v[76:79]\n"
"ds_read_b64_tr_b16 a[250:251], v238 offset:53760\n"
"v_xor_b32 v250, 0x1000, v245\n"
"v_mfma_f32_16x16x32_bf16 v[44:47], v[108:111], v[224:227], v[44:47]\n"
"ds_read_b128 v[216:219], v228 offset:512\n"
"v_add_f32 v254, v254, v200\n"
"v_mfma_f32_16x16x32_bf16 v[44:47], v[104:107], a[252:255], v[44:47]\n"
"v_add_f32 v244, v244, v202\n"
"v_add_f32 v254, v254, v202\n"
"v_mfma_f32_16x16x32_bf16 v[12:15], v[100:103], v[224:227], v[12:15]\n"
"s_add_u32 s40, s56, s52\n"
"ds_read_b64_tr_b16 v[226:227], v239 offset:53760\n"
"v_min3_f32 v244, v244, v196, v198\n"
"v_mfma_f32_16x16x32_bf16 v[12:15], v[96:99], a[252:255], v[12:15]\n"
"buffer_load_dwordx4 v207, s[20:23], s40 offen offset:1024 nt lds\n"
"v_min_f32 v244, v244, v200\n"
"v_rcp_f32 v244, v244\n"
"v_mfma_f32_16x16x32_bf16 a[220:223], v[120:123], a[252:255], a[220:223]\n"
"s_waitcnt lgkmcnt(4)\n"
"v_fmac_f32 v254, 0x68800000, v244\n"
"v_cmp_nge_f32 vcc, s45, v254\n"
"s_cbranch_vccnz LSLOW_%=\nLBACK2_%=:\ns_mov_b64 vcc, s[64:65]\n"
"v_mfma_f32_16x16x32_bf16 a[232:235], v[120:123], a[244:247], a[232:235]\n"
"v_rcp_f32 v246, v196\n"
"v_accvgpr_mov_b32 a2, a146\n"
"v_accvgpr_mov_b32 a7, a151\n"
"v_accvgpr_mov_b32 a10, a154\n"
"v_accvgpr_mov_b32 a15, a159\n"
"v_accvgpr_mov_b32 a22, a166\n"
"v_accvgpr_mov_b32 a30, a174\n"
"v_bfe_u32 v149, v242, 8, 2\n"
"v_lshl_add_u32 v149, v149, 4, s43\n"
"v_mfma_f32_16x16x32_bf16 v[24:27], v[96:99], a[244:247], v[24:27]\n"
"s_waitcnt lgkmcnt(2)\n"
"ds_read_b128 v[124:127], v149 offset:0\n"
"v_accvgpr_mov_b32 a28, a172\n"
"v_mfma_f32_16x16x32_bf16 v[24:27], v[100:103], a[248:251], v[24:27]\n"
"ds_read_b128 v[132:135], v149 offset:512\n"
"v_mov_b32_dpp v246, v246 row_ror:12 row_mask:0x2 bank_mask:0xf\n"
"v_accvgpr_mov_b32 a3, a147\n"
"v_accvgpr_mov_b32 a12, a156\n"
"v_accvgpr_mov_b32 a18, a162\n"
"v_accvgpr_mov_b32 a19, a163\n"
"v_accvgpr_mov_b32 a26, a170\n"
"v_accvgpr_mov_b32 a36, a180\n"
"v_xor_b32 v148, 0x400, v243\n"
"v_xor_b32 v150, 64, v149\n"
"v_accvgpr_mov_b32 a172, a76\n"
"v_mfma_f32_16x16x32_bf16 v[56:59], v[104:107], a[244:247], v[56:59]\n"
"ds_read_b128 v[128:131], v150 offset:0\n"
"v_accvgpr_mov_b32 a39, a183\n"
"v_mfma_f32_16x16x32_bf16 v[56:59], v[108:111], a[248:251], v[56:59]\n"
"ds_read_b128 v[136:139], v150 offset:512\n"
"v_rcp_f32 v247, v198\n"
"v_mov_b32_dpp v246, v246 row_ror:8 row_mask:0x4 bank_mask:0xf\n"
"v_accvgpr_mov_b32 a0, a144\n"
"v_accvgpr_mov_b32 a1, a145\n"
"v_accvgpr_mov_b32 a4, a148\n"
"v_accvgpr_mov_b32 a5, a149\n"
"v_accvgpr_mov_b32 a8, a152\n"
"v_accvgpr_mov_b32 a16, a160\n"
"v_accvgpr_mov_b32 a25, a169\n"
"v_mfma_f32_16x16x32_bf16 v[88:91], v[112:115], a[244:247], v[88:91]\n"
"v_accvgpr_mov_b32 a20, a164\n"
"v_accvgpr_mov_b32 a29, a173\n"
"v_mfma_f32_16x16x32_bf16 v[88:91], v[116:119], a[248:251], v[88:91]\n"
"s_waitcnt lgkmcnt(3)\n"
"ds_read_b128 a[192:195], v243 offset:4096\n"
"ds_read_b128 a[200:203], v243 offset:6144\n"
"v_accvgpr_mov_b32 a6, a150\n"
"v_accvgpr_mov_b32 a9, a153\n"
"v_accvgpr_mov_b32 a11, a155\n"
"v_accvgpr_mov_b32 a13, a157\n"
"v_accvgpr_mov_b32 a14, a158\n"
"v_accvgpr_mov_b32 a17, a161\n"
"v_accvgpr_mov_b32 a23, a167\n"
"v_accvgpr_mov_b32 a31, a175\n"
"v_accvgpr_mov_b32 a33, a177\n"
"v_accvgpr_mov_b32 a35, a179\n"
"v_accvgpr_mov_b32 a44, a188\n"
"v_accvgpr_mov_b32 a47, a191\n"
"v_accvgpr_mov_b32 a155, a59\n"
"v_mfma_f32_16x16x32_bf16 v[180:183], v[124:127], a[0:3], 0\n"
"s_waitcnt lgkmcnt(3)\n"
"v_xor_b32 v244, 0x11000, v242\n"
"v_accvgpr_mov_b32 a180, a84\n"
"v_mfma_f32_16x16x32_bf16 v[180:183], v[128:131], a[4:7], v[180:183]\n"
"v_accvgpr_mov_b32 a34, a178\n"
"v_accvgpr_mov_b32 a43, a187\n"
"v_mfma_f32_16x16x32_bf16 v[180:183], v[132:135], a[8:11], v[180:183]\n"
"s_waitcnt lgkmcnt(2)\n"
"ds_read_b128 a[204:207], v148 offset:6144\n"
"v_accvgpr_mov_b32 a37, a181\n"
"v_mfma_f32_16x16x32_bf16 v[180:183], v[136:139], a[12:15], v[180:183]\n"
"ds_read_b128 a[196:199], v148 offset:4096\n"
"v_accvgpr_mov_b32 a21, a165\n"
"v_accvgpr_mov_b32 a24, a168\n"
"v_accvgpr_mov_b32 a32, a176\n"
"v_accvgpr_mov_b32 a38, a182\n"
"v_accvgpr_mov_b32 a40, a184\n"
"v_accvgpr_mov_b32 a42, a186\n"
"v_accvgpr_mov_b32 a46, a190\n"
"v_accvgpr_mov_b32 a165, a69\n"
"v_accvgpr_mov_b32 a159, a63\n"
"v_mfma_f32_16x16x32_bf16 v[184:187], v[124:127], a[16:19], 0\n"
"v_mov_b32_dpp v246, v246 row_ror:4 row_mask:0x8 bank_mask:0xf\n"
"v_accvgpr_mov_b32 a27, a171\n"
"v_mfma_f32_16x16x32_bf16 v[184:187], v[128:131], a[20:23], v[184:187]\n"
"v_mov_b32_dpp v247, v247 row_ror:12 row_mask:0x2 bank_mask:0xf\n"
"v_accvgpr_mov_b32 a145, a49\n"
"v_mfma_f32_16x16x32_bf16 v[184:187], v[132:135], a[24:27], v[184:187]\n"
"s_add_u32 s40, s56, s53\n"
"ds_read_b64_tr_b16 v[224:225], v239 offset:49664\n"
"v_accvgpr_mov_b32 a160, a64\n"
"v_mfma_f32_16x16x32_bf16 v[184:187], v[136:139], a[28:31], v[184:187]\n"
"buffer_load_dwordx4 v240, s[20:23], s40 offen offset:2048 nt lds\n"
"v_accvgpr_mov_b32 a41, a185\n"
"v_accvgpr_mov_b32 a174, a78\n"
"v_accvgpr_mov_b32 a162, a66\n"
"v_accvgpr_mov_b32 a185, a89\n"
"v_sub_f32 v180, s74, v180\n"
"v_sub_f32 v181, s74, v181\n"
"v_sub_f32 v183, s74, v183\n"
"v_mul_f32_dpp v26, v246, v26 row_newbcast:2 row_mask:0xf bank_mask:0xf\n"
"v_mfma_f32_16x16x32_bf16 v[188:191], v[124:127], a[32:35], 0\n"
"v_mov_b32_dpp v247, v247 row_ror:8 row_mask:0x4 bank_mask:0xf\n"
"v_accvgpr_mov_b32 a45, a189\n"
"v_mfma_f32_16x16x32_bf16 v[188:191], v[128:131], a[36:39], v[188:191]\n"
"v_mov_b32 v252, v192\n"
"v_accvgpr_mov_b32 a181, a85\n"
"v_mfma_f32_16x16x32_bf16 v[188:191], v[132:135], a[40:43], v[188:191]\n"
"v_accvgpr_mov_b32 a166, a70\n"
"v_accvgpr_mov_b32 a153, a57\n"
"v_mfma_f32_16x16x32_bf16 v[188:191], v[136:139], a[44:47], v[188:191]\n"
"s_waitcnt lgkmcnt(1)\n"
"v_accvgpr_mov_b32 a157, a61\n"
"v_mul_f32_dpp v11, v246, v11 row_newbcast:3 row_mask:0xf bank_mask:0xf\n"
"v_mfma_f32_16x16x32_bf16 v[192:195], v[124:127], a[192:195], 0\n"
"ds_read_b64_tr_b16 a[252:253], v235 offset:49664\n"
"v_sub_f32 v185, s74, v185\n"
"v_mfma_f32_16x16x32_bf16 v[192:195], v[128:131], a[196:199], v[192:195]\n"
"ds_read_b64_tr_b16 a[248:249], v236 offset:49152\n"
"v_sub_f32 v182, s74, v182\n"
"v_mfma_f32_16x16x32_bf16 v[192:195], v[132:135], a[200:203], v[192:195]\n"
"v_accvgpr_mov_b32 a186, a90\n"
"v_sub_f32 v184, s74, v184\n"
"v_mfma_f32_16x16x32_bf16 v[192:195], v[136:139], a[204:207], v[192:195]\n"
"ds_read_b64_tr_b16 a[254:255], v235 offset:53760\n"
"ds_read_b64_tr_b16 a[246:247], v232 offset:53248\n"
"v_mov_b32 v253, v202\n"
"v_accvgpr_mov_b32 a164, a68\n"
"v_accvgpr_mov_b32 a173, a77\n"
"v_accvgpr_mov_b32 a161, a65\n"
"v_accvgpr_mov_b32 a176, a80\n"
"v_accvgpr_mov_b32 a178, a82\n"
"v_accvgpr_mov_b32 a184, a88\n"
"v_sub_f32 v186, s74, v186\n"
"v_mfma_f32_16x16x32_bf16 v[152:155], v[208:211], a[0:3], v[180:183]\n"
"v_accvgpr_mov_b32 a151, a55\n"
"v_accvgpr_mov_b32 a168, a72\n"
"v_mfma_f32_16x16x32_bf16 v[152:155], v[212:215], a[4:7], v[152:155]\n"
"v_accvgpr_mov_b32 a189, a93\n"
"v_sub_f32 v187, s74, v187\n"
"v_mfma_f32_16x16x32_bf16 v[152:155], v[216:219], a[8:11], v[152:155]\n"
"s_add_u32 s40, s56, s54\n"
"v_accvgpr_mov_b32 a175, a79\n"
"v_accvgpr_mov_b32 a188, a92\n"
"v_mfma_f32_16x16x32_bf16 v[152:155], v[220:223], a[12:15], v[152:155]\n"
"buffer_load_dwordx4 v207, s[20:23], s40 offen offset:3072 nt lds\n"
"v_mov_b32_dpp v247, v247 row_ror:4 row_mask:0x8 bank_mask:0xf\n"
"v_accvgpr_mov_b32 a169, a73\n"
"v_accvgpr_mov_b32 a170, a74\n"
"v_accvgpr_mov_b32 a171, a75\n"
"v_sub_f32 v189, s74, v189\n"
"v_sub_f32 v190, s74, v190\n"
"v_sub_f32 v191, s74, v191\n"
"v_sub_f32 v192, s74, v192\n"
"v_mfma_f32_16x16x32_bf16 v[156:159], v[216:219], a[24:27], v[184:187]\n"
"s_add_u32 s55, s55, s59\n"
"v_accvgpr_mov_b32 a191, a95\n"
"v_accvgpr_mov_b32 a163, a67\n"
"v_mfma_f32_16x16x32_bf16 v[156:159], v[220:223], a[28:31], v[156:159]\n"
"s_or_b32 s55, s55, s58\n"
"v_accvgpr_mov_b32 a149, a53\n"
"v_sub_f32 v188, s74, v188\n"
"v_mfma_f32_16x16x32_bf16 v[156:159], v[208:211], a[16:19], v[156:159]\n"
"s_xor_b32 s56, s55, s57\n"
"v_exp_f32 v153, v153\n"
"v_mfma_f32_16x16x32_bf16 v[156:159], v[212:215], a[20:23], v[156:159]\n"
"ds_write_b64 v250, v[252:253] offset:52224\n"
"v_accvgpr_mov_b32 a148, a52\n"
"v_accvgpr_mov_b32 a183, a87\n"
"v_accvgpr_mov_b32 a146, a50\n"
"v_sub_f32 v193, s74, v193\n"
"v_sub_f32 v194, s74, v194\n"
"v_exp_f32 v152, v152\n"
"v_mfma_f32_16x16x32_bf16 v[160:163], v[208:211], a[32:35], v[188:191]\n"
"v_accvgpr_mov_b32 a158, a62\n"
"v_accvgpr_mov_b32 a144, a48\n"
"v_mfma_f32_16x16x32_bf16 v[160:163], v[212:215], a[36:39], v[160:163]\n"
"v_rcp_f32 v206, v200\n"
"v_mfma_f32_16x16x32_bf16 v[160:163], v[216:219], a[40:43], v[160:163]\n"
"v_accvgpr_mov_b32 a190, a94\n"
"v_sub_f32 v195, s74, v195\n"
"v_mfma_f32_16x16x32_bf16 v[160:163], v[220:223], a[44:47], v[160:163]\n"
"ds_write_b128 v244, a[216:219] offset:32768\n"
"v_accvgpr_mov_b32 a150, a54\n"
"v_accvgpr_mov_b32 a156, a60\n"
"v_accvgpr_mov_b32 a147, a51\n"
"v_accvgpr_mov_b32 a177, a81\n"
"v_mul_f32_dpp v25, v246, v25 row_newbcast:1 row_mask:0xf bank_mask:0xf\n"
"v_mfma_f32_16x16x32_bf16 v[164:167], v[216:219], a[200:203], v[192:195]\n"
"v_mov_b32_dpp v206, v206 row_ror:12 row_mask:0x2 bank_mask:0xf\n"
"v_accvgpr_mov_b32 a167, a71\n"
"v_mfma_f32_16x16x32_bf16 v[164:167], v[220:223], a[204:207], v[164:167]\n"
"v_accvgpr_mov_b32 a179, a83\n"
"v_accvgpr_mov_b32 a187, a91\n"
"v_mfma_f32_16x16x32_bf16 v[164:167], v[208:211], a[192:195], v[164:167]\n"
"ds_read_b128 v[208:211], v228 offset:4096\n"
"v_accvgpr_mov_b32 a154, a58\n"
"v_mfma_f32_16x16x32_bf16 v[164:167], v[212:215], a[196:199], v[164:167]\n"
"s_waitcnt lgkmcnt(2)\n"
"ds_read_b128 v[216:219], v228 offset:4608\n"
"ds_read_b128 v[220:223], v229 offset:4608\n"
"v_mov_b32_dpp v206, v206 row_ror:8 row_mask:0x4 bank_mask:0xf\n"
"v_accvgpr_mov_b32 a182, a86\n"
"v_accvgpr_mov_b32 a152, a56\n"
"v_exp_f32 v155, v155\n"
"v_exp_f32 v160, v160\n"
"LTC_%=:\n"
"s_waitcnt lgkmcnt(4)\n"
"v_mfma_f32_16x16x32_bf16 v[92:95], v[116:119], v[224:227], v[92:95]\n"
"v_exp_f32 v154, v154\n"
"v_mfma_f32_16x16x32_bf16 v[92:95], v[112:115], a[252:255], v[92:95]\n"
"ds_read_b128 v[212:215], v229 offset:4096\n"
"v_mul_f32_dpp v24, v246, v24 row_newbcast:0 row_mask:0xf bank_mask:0xf\n"
"v_mfma_f32_16x16x32_bf16 v[60:63], v[108:111], v[224:227], v[60:63]\n"
"s_add_u32 m0, s44, 32768\n"
"v_exp_f32 v163, v163\n"
"v_mfma_f32_16x16x32_bf16 v[60:63], v[104:107], a[252:255], v[60:63]\n"
"buffer_load_dwordx4 v240, s[16:19], s56 offen nt lds\n"
"v_mul_f32_dpp v8, v246, v8 row_newbcast:0 row_mask:0xf bank_mask:0xf\n"
"v_exp_f32 v156, v156\n"
"v_exp_f32 v158, v158\n"
"v_exp_f32 v161, v161\n"
"v_exp_f32 v165, v165\n"
"v_mfma_f32_16x16x32_bf16 v[28:31], v[100:103], v[224:227], v[28:31]\n"
"v_cvt_pk_bf16_f32 v128, v152, v153\n"
"v_cvt_pk_bf16_f32 v129, v154, v155\n"
"v_mfma_f32_16x16x32_bf16 v[28:31], v[96:99], a[252:255], v[28:31]\n"
"v_mov_b32_dpp v206, v206 row_ror:4 row_mask:0x8 bank_mask:0xf\n"
"v_mul_f32_dpp v9, v246, v9 row_newbcast:1 row_mask:0xf bank_mask:0xf\n"
"v_mfma_f32_16x16x32_bf16 a[236:239], v[120:123], a[252:255], a[236:239]\n"
"s_waitcnt lgkmcnt(2)\n"
"v_exp_f32 v162, v162\n"
"v_mfma_f32_16x16x32_bf16 v[168:171], v[216:219], a[8:11], v[180:183]\n"
"s_waitcnt lgkmcnt(1)\n"
"v_exp_f32 v167, v167\n"
"v_mfma_f32_16x16x32_bf16 v[168:171], v[220:223], a[12:15], v[168:171]\n"
"v_mul_f32_dpp v12, v246, v12 row_newbcast:0 row_mask:0xf bank_mask:0xf\n"
"v_mul_f32_dpp v46, v247, v46 row_newbcast:2 row_mask:0xf bank_mask:0xf\n"
"v_mfma_f32_16x16x32_bf16 v[168:171], v[208:211], a[0:3], v[168:171]\n"
"s_waitcnt lgkmcnt(0)\n"
"ds_read_b64_tr_b16 a[244:245], v232 offset:49152\n"
"v_cvt_pk_bf16_f32 v145, v162, v163\n"
"v_mfma_f32_16x16x32_bf16 v[168:171], v[212:215], a[4:7], v[168:171]\n"
"v_exp_f32 v159, v159\n"
"v_mfma_f32_16x16x32_bf16 v[172:175], v[208:211], a[16:19], v[184:187]\n"
"ds_read_b64_tr_b16 a[252:253], v233 offset:49152\n"
"v_mul_f32_dpp v72, v206, v72 row_newbcast:0 row_mask:0xf bank_mask:0xf\n"
"v_mfma_f32_16x16x32_bf16 v[172:175], v[212:215], a[20:23], v[172:175]\n"
"v_exp_f32 v164, v164\n"
"v_mfma_f32_16x16x32_bf16 v[172:175], v[216:219], a[24:27], v[172:175]\n"
"ds_read_b64_tr_b16 a[250:251], v236 offset:53248\n"
"v_mul_f32_dpp v14, v246, v14 row_newbcast:2 row_mask:0xf bank_mask:0xf\n"
"v_mfma_f32_16x16x32_bf16 v[172:175], v[220:223], a[28:31], v[172:175]\n"
"ds_write_b128 v244, a[220:223] offset:33792\n"
"v_mul_f32_dpp v78, v206, v78 row_newbcast:2 row_mask:0xf bank_mask:0xf\n"
"v_exp_f32 v157, v157\n"
"v_mul_f32_dpp v89, v206, v89 row_newbcast:1 row_mask:0xf bank_mask:0xf\n"
"v_exp_f32 v168, v168\n"
"v_mfma_f32_16x16x32_bf16 v[176:179], v[216:219], a[40:43], v[188:191]\n"
"v_mul_f32_dpp v10, v246, v10 row_newbcast:2 row_mask:0xf bank_mask:0xf\n"
"v_mul_f32_dpp v40, v247, v40 row_newbcast:0 row_mask:0xf bank_mask:0xf\n"
"v_mfma_f32_16x16x32_bf16 v[176:179], v[220:223], a[44:47], v[176:179]\n"
"v_cvt_pk_bf16_f32 v137, v158, v159\n"
"v_mul_f32_dpp v28, v246, v28 row_newbcast:0 row_mask:0xf bank_mask:0xf\n"
"v_mfma_f32_16x16x32_bf16 v[176:179], v[208:211], a[32:35], v[176:179]\n"
"s_add_u32 s40, s56, s52\n"
"v_mul_f32_dpp v73, v206, v73 row_newbcast:1 row_mask:0xf bank_mask:0xf\n"
"v_cvt_pk_bf16_f32 v136, v156, v157\n"
"v_mfma_f32_16x16x32_bf16 v[176:179], v[212:215], a[36:39], v[176:179]\n"
"buffer_load_dwordx4 v207, s[16:19], s40 offen offset:1024 nt lds\n"
"v_mfma_f32_16x16x32_bf16 v[152:155], v[208:211], a[192:195], v[192:195]\n"
"v_mul_f32_dpp v76, v206, v76 row_newbcast:0 row_mask:0xf bank_mask:0xf\n"
"v_mul_f32_dpp v88, v206, v88 row_newbcast:0 row_mask:0xf bank_mask:0xf\n"
"v_mfma_f32_16x16x32_bf16 v[152:155], v[212:215], a[196:199], v[152:155]\n"
"ds_read_b128 v[208:211], v230 offset:0\n"
"v_cvt_pk_bf16_f32 v144, v160, v161\n"
"v_mfma_f32_16x16x32_bf16 v[152:155], v[216:219], a[200:203], v[152:155]\n"
"v_mul_f32_dpp v93, v206, v93 row_newbcast:1 row_mask:0xf bank_mask:0xf\n"
"v_cvt_pk_bf16_f32 v88, v88, v89\n"
"v_mfma_f32_16x16x32_bf16 v[152:155], v[220:223], a[204:207], v[152:155]\n"
"s_waitcnt lgkmcnt(1)\n"
"ds_read_b128 v[212:215], v231 offset:0\n"
"v_mul_f32_dpp v13, v246, v13 row_newbcast:1 row_mask:0xf bank_mask:0xf\n"
"v_mfma_f32_16x16x32_bf16 a[208:211], v[120:123], a[244:247], a[208:211]\n"
"v_exp_f32 v170, v170\n"
"v_mfma_f32_16x16x32_bf16 v[0:3], v[96:99], a[244:247], v[0:3]\n"
"ds_read_b128 v[216:219], v230 offset:512\n"
"v_cvt_pk_bf16_f32 v124, v164, v165\n"
"v_mfma_f32_16x16x32_bf16 v[0:3], v[100:103], a[248:251], v[0:3]\n"
"ds_read_b64_tr_b16 v[226:227], v237 offset:53248\n"
"v_mul_f32_dpp v15, v246, v15 row_newbcast:3 row_mask:0xf bank_mask:0xf\n"
"v_mul_f32_dpp v41, v247, v41 row_newbcast:1 row_mask:0xf bank_mask:0xf\n"
"v_mul_f32_dpp v42, v247, v42 row_newbcast:2 row_mask:0xf bank_mask:0xf\n"
"v_exp_f32 v166, v166\n"
"v_mul_f32_dpp v57, v247, v57 row_newbcast:1 row_mask:0xf bank_mask:0xf\n"
"v_mul_f32_dpp v91, v206, v91 row_newbcast:3 row_mask:0xf bank_mask:0xf\n"
"v_exp_f32 v172, v172\n"
"v_exp_f32 v177, v177\n"
"v_mfma_f32_16x16x32_bf16 v[32:35], v[104:107], a[244:247], v[32:35]\n"
"ds_read_b64_tr_b16 v[224:225], v237 offset:49152\n"
"ds_read_b128 v[220:223], v231 offset:512\n"
"v_mfma_f32_16x16x32_bf16 v[32:35], v[108:111], a[248:251], v[32:35]\n"
"ds_write_b128 v244, a[232:235] offset:34816\n"
"v_cvt_pk_bf16_f32 v8, v8, v9\n"
"v_mul_f32_dpp v75, v206, v75 row_newbcast:3 row_mask:0xf bank_mask:0xf\n"
"v_mul_f32_dpp v30, v246, v30 row_newbcast:2 row_mask:0xf bank_mask:0xf\n"
"v_mul_f32_dpp v94, v206, v94 row_newbcast:2 row_mask:0xf bank_mask:0xf\n"
"v_exp_f32 v176, v176\n"
"v_mfma_f32_16x16x32_bf16 v[64:67], v[112:115], a[244:247], v[64:67]\n"
"v_exp_f32 v173, v173\n"
"v_mfma_f32_16x16x32_bf16 v[64:67], v[116:119], a[248:251], v[64:67]\n"
"s_waitcnt lgkmcnt(6)\n"
"ds_write_b128 v244, a[236:239] offset:35840\n"
"v_mul_f32_dpp v77, v206, v77 row_newbcast:1 row_mask:0xf bank_mask:0xf\n"
"v_mul_f32_dpp v62, v247, v62 row_newbcast:2 row_mask:0xf bank_mask:0xf\n"
"v_exp_f32 v169, v169\n"
"v_cvt_pk_bf16_f32 v138, v172, v173\n"
"v_exp_f32 v153, v153\n"
"v_mfma_f32_16x16x32_bf16 v[156:159], v[208:211], a[0:3], v[180:183]\n"
"s_waitcnt lgkmcnt(6)\n"
"ds_read_b64_tr_b16 a[254:255], v233 offset:53248\n"
"v_mul_f32_dpp v63, v247, v63 row_newbcast:3 row_mask:0xf bank_mask:0xf\n"
"v_mfma_f32_16x16x32_bf16 v[156:159], v[212:215], a[4:7], v[156:159]\n"
"s_waitcnt lgkmcnt(6)\n"
"v_exp_f32 v174, v174\n"
"v_mfma_f32_16x16x32_bf16 v[156:159], v[216:219], a[8:11], v[156:159]\n"
"s_waitcnt lgkmcnt(3)\n"
"s_add_u32 s40, s56, s53\n"
"v_cvt_pk_bf16_f32 v40, v40, v41\n"
"v_mul_f32_dpp v0, v246, v0 row_newbcast:0 row_mask:0xf bank_mask:0xf\n"
"v_mfma_f32_16x16x32_bf16 v[156:159], v[220:223], a[12:15], v[156:159]\n"
"buffer_load_dwordx4 v240, s[16:19], s40 offen offset:2048 nt lds\n"
"v_cvt_pk_bf16_f32 v9, v10, v11\n"
"v_mul_f32_dpp v44, v247, v44 row_newbcast:0 row_mask:0xf bank_mask:0xf\n"
"v_cvt_pk_bf16_f32 v125, v166, v167\n"
"v_mul_f32_dpp v31, v246, v31 row_newbcast:3 row_mask:0xf bank_mask:0xf\n"
"v_exp_f32 v152, v152\n"
"v_mul_f32_dpp v34, v247, v34 row_newbcast:2 row_mask:0xf bank_mask:0xf\n"
"v_mov_b64 v[200:201], 0\n"
"v_mfma_f32_16x16x32_bf16 v[160:163], v[216:219], a[24:27], v[184:187]\n"
"v_mul_f32_dpp v60, v247, v60 row_newbcast:0 row_mask:0xf bank_mask:0xf\n"
"v_xor_b32 v244, 0x1000, v244\n"
"v_mfma_f32_16x16x32_bf16 v[160:163], v[220:223], a[28:31], v[160:163]\n"
"v_exp_f32 v178, v178\n"
"v_mfma_f32_16x16x32_bf16 v[160:163], v[208:211], a[16:19], v[160:163]\n"
"ds_read_b64_tr_b16 a[250:251], v236 offset:53760\n"
"v_mul_f32_dpp v33, v247, v33 row_newbcast:1 row_mask:0xf bank_mask:0xf\n"
"v_mfma_f32_16x16x32_bf16 v[160:163], v[212:215], a[20:23], v[160:163]\n"
"ds_read_b64_tr_b16 a[244:245], v232 offset:49664\n"
"v_mul_f32_dpp v43, v247, v43 row_newbcast:3 row_mask:0xf bank_mask:0xf\n"
"v_mfma_f32_16x16x32_bf16 v[164:167], v[208:211], a[32:35], v[188:191]\n"
"v_mul_f32_dpp v45, v247, v45 row_newbcast:1 row_mask:0xf bank_mask:0xf\n"
"v_cvt_pk_bf16_f32 v72, v72, v73\n"
"v_mfma_f32_16x16x32_bf16 v[164:167], v[212:215], a[36:39], v[164:167]\n"
"ds_read_b128 v[208:211], v230 offset:4096\n"
"v_mul_f32_dpp v74, v206, v74 row_newbcast:2 row_mask:0xf bank_mask:0xf\n"
"v_mfma_f32_16x16x32_bf16 v[164:167], v[216:219], a[40:43], v[164:167]\n"
"v_mul_f32_dpp v56, v247, v56 row_newbcast:0 row_mask:0xf bank_mask:0xf\n"
"v_mul_f32_dpp v58, v247, v58 row_newbcast:2 row_mask:0xf bank_mask:0xf\n"
"v_mfma_f32_16x16x32_bf16 v[164:167], v[220:223], a[44:47], v[164:167]\n"
"ds_read_b128 v[212:215], v231 offset:4096\n"
"ds_read_b128 v[220:223], v231 offset:4608\n"
"v_mul_f32_dpp v47, v247, v47 row_newbcast:3 row_mask:0xf bank_mask:0xf\n"
"v_cvt_pk_bf16_f32 v73, v74, v75\n"
"v_mul_f32_dpp v29, v246, v29 row_newbcast:1 row_mask:0xf bank_mask:0xf\n"
"v_exp_f32 v175, v175\n"
"v_exp_f32 v179, v179\n"
"v_mul_f32_dpp v1, v246, v1 row_newbcast:1 row_mask:0xf bank_mask:0xf\n"
"v_mul_f32_dpp v2, v246, v2 row_newbcast:2 row_mask:0xf bank_mask:0xf\n"
"v_mfma_f32_16x16x32_bf16 v[68:71], v[116:119], v[224:227], v[68:71]\n"
"s_waitcnt lgkmcnt(5)\n"
"v_exp_f32 v171, v171\n"
"v_mfma_f32_16x16x32_bf16 v[68:71], v[112:115], a[252:255], v[68:71]\n"
"ds_read_b128 v[216:219], v230 offset:4608\n"
"v_mul_f32_dpp v27, v246, v27 row_newbcast:3 row_mask:0xf bank_mask:0xf\n"
"v_mfma_f32_16x16x32_bf16 v[36:39], v[108:111], v[224:227], v[36:39]\n"
"s_add_u32 s40, s56, s54\n"
"v_cvt_pk_bf16_f32 v24, v24, v25\n"
"v_mul_f32_dpp v59, v247, v59 row_newbcast:3 row_mask:0xf bank_mask:0xf\n"
"v_mfma_f32_16x16x32_bf16 v[36:39], v[104:107], a[252:255], v[36:39]\n"
"buffer_load_dwordx4 v207, s[16:19], s40 offen offset:3072 nt lds\n"
"v_cvt_pk_bf16_f32 v74, v76, v77\n"
"v_cvt_pk_bf16_f32 v25, v26, v27\n"
"v_cvt_pk_bf16_f32 v130, v168, v169\n"
"v_exp_f32 v154, v154\n"
"v_exp_f32 v162, v162\n"
"v_mul_f32_dpp v3, v246, v3 row_newbcast:3 row_mask:0xf bank_mask:0xf\n"
"v_mfma_f32_16x16x32_bf16 v[4:7], v[100:103], v[224:227], v[4:7]\n"
"v_cvt_pk_bf16_f32 v10, v12, v13\n"
"v_cvt_pk_bf16_f32 v0, v0, v1\n"
"v_mfma_f32_16x16x32_bf16 v[4:7], v[96:99], a[252:255], v[4:7]\n"
"v_mul_f32_dpp v90, v206, v90 row_newbcast:2 row_mask:0xf bank_mask:0xf\n"
"v_mul_f32_dpp v92, v206, v92 row_newbcast:0 row_mask:0xf bank_mask:0xf\n"
"v_mfma_f32_16x16x32_bf16 a[212:215], v[120:123], a[252:255], a[212:215]\n"
"s_waitcnt lgkmcnt(3)\n"
"ds_read_b64_tr_b16 a[246:247], v232 offset:53760\n"
"ds_read_b64_tr_b16 v[224:225], v237 offset:49664\n"
"v_cvt_pk_bf16_f32 v11, v14, v15\n"
"v_cvt_pk_bf16_f32 v41, v42, v43\n"
"v_cvt_pk_bf16_f32 v43, v46, v47\n"
"v_cvt_pk_bf16_f32 v56, v56, v57\n"
"v_cvt_pk_bf16_f32 v131, v170, v171\n"
"v_cvt_pk_bf16_f32 v146, v176, v177\n"
"v_exp_f32 v167, v167\n"
"v_mfma_f32_16x16x32_bf16 v[168:171], v[208:211], a[0:3], v[180:183]\n"
"s_waitcnt lgkmcnt(3)\n"
"v_exp_f32 v159, v159\n"
"v_mfma_f32_16x16x32_bf16 v[168:171], v[212:215], a[4:7], v[168:171]\n"
"s_waitcnt lgkmcnt(2)\n"
"ds_read_b64_tr_b16 a[248:249], v236 offset:49664\n"
"ds_read_b64_tr_b16 a[254:255], v233 offset:53760\n"
"v_mfma_f32_16x16x32_bf16 v[168:171], v[216:219], a[8:11], v[168:171]\n"
"v_cvt_pk_bf16_f32 v89, v90, v91\n"
"v_cvt_pk_bf16_f32 v139, v174, v175\n"
"v_mfma_f32_16x16x32_bf16 v[168:171], v[220:223], a[12:15], v[168:171]\n"
"ds_write_b128 v248, v[8:11] offset:49152\n"
"v_mul_f32_dpp v79, v206, v79 row_newbcast:3 row_mask:0xf bank_mask:0xf\n"
"v_cvt_pk_bf16_f32 v147, v178, v179\n"
"v_exp_f32 v165, v165\n"
"v_mul_f32_dpp v6, v246, v6 row_newbcast:2 row_mask:0xf bank_mask:0xf\n"
"v_cvt_pk_bf16_f32 v1, v2, v3\n"
"v_mfma_f32_16x16x32_bf16 v[172:175], v[216:219], a[24:27], v[184:187]\n"
"ds_read_b64_tr_b16 v[226:227], v237 offset:53760\n"
"v_mul_f32_dpp v32, v247, v32 row_newbcast:0 row_mask:0xf bank_mask:0xf\n"
"v_mfma_f32_16x16x32_bf16 v[172:175], v[220:223], a[28:31], v[172:175]\n"
"v_mul_f32_dpp v39, v247, v39 row_newbcast:3 row_mask:0xf bank_mask:0xf\n"
"v_mul_f32_dpp v68, v206, v68 row_newbcast:0 row_mask:0xf bank_mask:0xf\n"
"v_mfma_f32_16x16x32_bf16 v[172:175], v[208:211], a[16:19], v[172:175]\n"
"ds_read_b64_tr_b16 a[252:253], v233 offset:49664\n"
"v_cvt_pk_bf16_f32 v75, v78, v79\n"
"v_mfma_f32_16x16x32_bf16 v[172:175], v[212:215], a[20:23], v[172:175]\n"
"s_cbranch_vccnz LPCT0_%=\nLPKT0_%=:\n"
"ds_read_b64_tr_b16 a[54:55], v249 offset:49160\n"
"v_mfma_f32_16x16x32_bf16 v[176:179], v[208:211], a[32:35], v[188:191]\n"
"v_cvt_pk_bf16_f32 v42, v44, v45\n"
"v_mul_f32_dpp v61, v247, v61 row_newbcast:1 row_mask:0xf bank_mask:0xf\n"
"v_mfma_f32_16x16x32_bf16 v[176:179], v[212:215], a[36:39], v[176:179]\n"
"ds_read_b64_tr_b16 a[52:53], v249 offset:49152\n"
"v_cvt_pk_bf16_f32 v57, v58, v59\n"
"v_mfma_f32_16x16x32_bf16 v[176:179], v[216:219], a[40:43], v[176:179]\n"
"v_exp_f32 v155, v155\n"
"v_mfma_f32_16x16x32_bf16 v[176:179], v[220:223], a[44:47], v[176:179]\n"
"s_waitcnt lgkmcnt(4)\n"
"ds_write_b128 v248, v[40:43] offset:50176\n"
"v_cvt_pk_bf16_f32 v27, v30, v31\n"
"v_cvt_pk_bf16_f32 v90, v92, v93\n"
"v_cvt_pk_bf16_f32 v126, v152, v153\n"
"v_mul_f32_dpp v7, v246, v7 row_newbcast:3 row_mask:0xf bank_mask:0xf\n"
"v_mul_f32_dpp v65, v206, v65 row_newbcast:1 row_mask:0xf bank_mask:0xf\n"
"v_mul_f32_dpp v67, v206, v67 row_newbcast:3 row_mask:0xf bank_mask:0xf\n"
"v_mul_f32_dpp v70, v206, v70 row_newbcast:2 row_mask:0xf bank_mask:0xf\n"
"v_mul_f32_dpp v71, v206, v71 row_newbcast:3 row_mask:0xf bank_mask:0xf\n"
"v_mfma_f32_16x16x32_bf16 a[224:227], v[120:123], a[244:247], a[224:227]\n"
"ds_write_b128 v248, v[72:75] offset:51200\n"
"v_cvt_pk_bf16_f32 v26, v28, v29\n"
"v_exp_f32 v166, v166\n"
"v_cvt_pk_bf16_f32 v3, v6, v7\n"
"v_mul_f32_dpp v66, v206, v66 row_newbcast:2 row_mask:0xf bank_mask:0xf\n"
"v_mfma_f32_16x16x32_bf16 v[16:19], v[96:99], a[244:247], v[16:19]\n"
"s_waitcnt vmcnt(8) lgkmcnt(4)\n"
"ds_read_b64_tr_b16 a[68:69], v249 offset:50176\n"
"v_mul_f32_dpp v69, v206, v69 row_newbcast:1 row_mask:0xf bank_mask:0xf\n"
"v_mfma_f32_16x16x32_bf16 v[16:19], v[100:103], a[248:251], v[16:19]\n"
"s_barrier\n"
"ds_read_b64_tr_b16 a[70:71], v249 offset:50184\n"
"ds_read_b128 v[212:215], v229 offset:16384\n"
"ds_read_b64 v[250:251], v245 offset:52224\n"
"v_mul_f32_dpp v5, v246, v5 row_newbcast:1 row_mask:0xf bank_mask:0xf\n"
"v_mul_f32_dpp v35, v247, v35 row_newbcast:3 row_mask:0xf bank_mask:0xf\n"
"v_exp_f32 v173, v173\n"
"v_exp_f32 v175, v175\n"
"v_mfma_f32_16x16x32_bf16 v[48:51], v[104:107], a[244:247], v[48:51]\n"
"v_cvt_pk_bf16_f32 v59, v62, v63\n"
"v_mul_f32_dpp v38, v247, v38 row_newbcast:2 row_mask:0xf bank_mask:0xf\n"
"v_mfma_f32_16x16x32_bf16 v[48:51], v[108:111], a[248:251], v[48:51]\n"
"ds_read_b128 v[208:211], v228 offset:16384\n"
"ds_read_b128 v[220:223], v229 offset:16896\n"
"v_mul_f32_dpp v95, v206, v95 row_newbcast:3 row_mask:0xf bank_mask:0xf\n"
"v_exp_f32 v161, v161\n"
"v_cvt_pk_bf16_f32 v149, v166, v167\n"
"v_cvt_pk_bf16_f32 v32, v32, v33\n"
"v_exp_f32 v170, v170\n"
"v_exp_f32 v172, v172\n"
"v_mfma_f32_16x16x32_bf16 v[80:83], v[112:115], a[244:247], v[80:83]\n"
"ds_read_b64_tr_b16 a[84:85], v249 offset:51200\n"
"ds_read_b64_tr_b16 a[86:87], v249 offset:51208\n"
"v_mfma_f32_16x16x32_bf16 v[80:83], v[116:119], a[248:251], v[80:83]\n"
"ds_read_b128 v[28:31], v244 offset:33792\n"
"v_mov_b64 v[198:199], 0\n"
"v_mfma_f32_16x16x32_bf16 v[84:87], v[116:119], v[224:227], v[84:87]\n"
"ds_write_b128 v248, v[24:27] offset:49152\n"
"v_cvt_pk_bf16_f32 v91, v94, v95\n"
"v_exp_f32 v156, v156\n"
"v_exp_f32 v157, v157\n"
"v_mul_f32_dpp v37, v247, v37 row_newbcast:1 row_mask:0xf bank_mask:0xf\n"
"v_mfma_f32_16x16x32_bf16 v[84:87], v[112:115], a[252:255], v[84:87]\n"
"ds_read_b64_tr_b16 a[60:61], v249 offset:49152\n"
"ds_read_b128 v[216:219], v228 offset:16896\n"
"v_cvt_pk_bf16_f32 v127, v154, v155\n"
"v_exp_f32 v160, v160\n"
"v_exp_f32 v164, v164\n"
"v_mul_f32_dpp v36, v247, v36 row_newbcast:0 row_mask:0xf bank_mask:0xf\n"
"v_exp_f32 v176, v176\n"
"v_mfma_f32_16x16x32_bf16 v[52:55], v[108:111], v[224:227], v[52:55]\n"
"ds_write_b128 v248, v[88:91] offset:51200\n"
"v_cvt_pk_bf16_f32 v58, v60, v61\n"
"v_exp_f32 v163, v163\n"
"v_cvt_pk_bf16_f32 v33, v34, v35\n"
"v_exp_f32 v169, v169\n"
"v_mfma_f32_16x16x32_bf16 v[52:55], v[104:107], a[252:255], v[52:55]\n"
"s_waitcnt lgkmcnt(9)\n"
"ds_write_b128 v248, v[56:59] offset:50176\n"
"v_mul_f32_dpp v4, v246, v4 row_newbcast:0 row_mask:0xf bank_mask:0xf\n"
"v_exp_f32 v168, v168\n"
"v_min_f32 v254, v252, v250\n"
"v_sub_f32 v252, v254, v252\n"
"v_mfma_f32_16x16x32_bf16 v[20:23], v[100:103], v[224:227], v[20:23]\n"
"ds_read_b64_tr_b16 a[92:93], v249 offset:51200\n"
"ds_read_b64_tr_b16 a[244:245], v234 offset:0\n"
"v_mfma_f32_16x16x32_bf16 v[20:23], v[96:99], a[252:255], v[20:23]\n"
"ds_read_b64_tr_b16 a[62:63], v249 offset:49160\n"
"ds_read_b64_tr_b16 a[78:79], v249 offset:50184\n"
"ds_read_b64_tr_b16 a[94:95], v249 offset:51208\n"
"v_cvt_pk_bf16_f32 v2, v4, v5\n"
"v_mul_f32_dpp v64, v206, v64 row_newbcast:0 row_mask:0xf bank_mask:0xf\n"
"v_cvt_pk_bf16_f32 v64, v64, v65\n"
"v_cvt_pk_bf16_f32 v65, v66, v67\n"
"v_cvt_pk_bf16_f32 v66, v68, v69\n"
"v_cvt_pk_bf16_f32 v67, v70, v71\n"
"v_exp_f32 v177, v177\n"
"v_mfma_f32_16x16x32_bf16 a[228:231], v[120:123], a[252:255], a[228:231]\n"
"s_waitcnt lgkmcnt(11)\n"
"ds_write_b128 v248, v[64:67] offset:51200\n"
"v_cvt_pk_bf16_f32 v132, v156, v157\n"
"v_cvt_pk_bf16_f32 v34, v36, v37\n"
"v_cvt_pk_bf16_f32 v35, v38, v39\n"
"v_exp_f32 v171, v171\n"
"v_exp_f32 v179, v179\n"
"v_sub_f32 v250, v254, v250\n"
"v_mul_f32_dpp v83, v206, v83 row_newbcast:3 row_mask:0xf bank_mask:0xf\n"
"v_mfma_f32_16x16x32_bf16 v[152:155], v[208:211], a[0:3], v[180:183]\n"
"ds_read_b64_tr_b16 a[76:77], v249 offset:50176\n"
"ds_read_b64_tr_b16 a[80:81], v249 offset:51200\n"
"v_mfma_f32_16x16x32_bf16 v[152:155], v[212:215], a[4:7], v[152:155]\n"
"s_waitcnt lgkmcnt(10)\n"
"ds_write_b128 v248, v[0:3] offset:49152\n"
"v_exp_f32 v158, v158\n"
"v_cvt_pk_bf16_f32 v140, v160, v161\n"
"v_cvt_pk_bf16_f32 v142, v172, v173\n"
"v_mul_f32_dpp v18, v246, v18 row_newbcast:2 row_mask:0xf bank_mask:0xf\n"
"v_mfma_f32_16x16x32_bf16 v[152:155], v[216:219], a[8:11], v[152:155]\n"
"ds_write_b128 v248, v[32:35] offset:50176\n"
"v_cvt_pk_bf16_f32 v141, v162, v163\n"
"v_exp_f32 v178, v178\n"
"v_exp_f32 v250, v250\n"
"v_mfma_f32_16x16x32_bf16 v[152:155], v[220:223], a[12:15], v[152:155]\n"
"ds_read_b64_tr_b16 a[50:51], v249 offset:49160\n"
"ds_read_b64_tr_b16 a[64:65], v249 offset:50176\n"
"ds_read_b128 v[24:27], v244 offset:32768\n"
"v_cvt_pk_bf16_f32 v133, v158, v159\n"
"v_cvt_pk_bf16_f32 v148, v164, v165\n"
"v_cvt_pk_bf16_f32 v150, v176, v177\n"
"v_exp_f32 v252, v252\n"
"v_mul_f32_dpp v19, v246, v19 row_newbcast:3 row_mask:0xf bank_mask:0xf\n"
"v_mul_f32_dpp v22, v246, v22 row_newbcast:2 row_mask:0xf bank_mask:0xf\n"
"v_mfma_f32_16x16x32_bf16 v[156:159], v[216:219], a[24:27], v[184:187]\n"
"ds_read_b64_tr_b16 a[66:67], v249 offset:50184\n"
"ds_read_b64_tr_b16 a[248:249], v238 offset:0\n"
"v_mfma_f32_16x16x32_bf16 v[156:159], v[220:223], a[28:31], v[156:159]\n"
"ds_read_b64_tr_b16 a[48:49], v249 offset:49152\n"
"ds_read_b128 v[60:63], v244 offset:35840\n"
"v_mfma_f32_16x16x32_bf16 v[156:159], v[208:211], a[16:19], v[156:159]\n"
"ds_read_b64_tr_b16 a[82:83], v249 offset:51208\n"
"ds_read_b128 v[56:59], v244 offset:34816\n"
"v_mfma_f32_16x16x32_bf16 v[156:159], v[212:215], a[20:23], v[156:159]\n"
"ds_read_b64_tr_b16 a[246:247], v234 offset:4096\n"
"ds_read_b64_tr_b16 v[226:227], v239 offset:4096\n"
"v_mfma_f32_16x16x32_bf16 v[160:163], v[208:211], a[32:35], v[188:191]\n"
"v_exp_f32 v174, v174\n"
"v_mfma_f32_16x16x32_bf16 v[160:163], v[212:215], a[36:39], v[160:163]\n"
"ds_read_b64_tr_b16 a[250:251], v238 offset:4096\n"
"v_mul_f32_dpp v49, v247, v49 row_newbcast:1 row_mask:0xf bank_mask:0xf\n"
"v_mfma_f32_16x16x32_bf16 v[160:163], v[216:219], a[40:43], v[160:163]\n"
"ds_read_b64_tr_b16 v[224:225], v239 offset:0\n"
"v_mov_b64 v[202:203], 0\n"
"v_mfma_f32_16x16x32_bf16 v[160:163], v[220:223], a[44:47], v[160:163]\n"
"v_exp_f32 v154, v154\n"
"v_mfma_f32_16x16x32_bf16 v[164:167], v[216:219], a[200:203], v[192:195]\n"
"v_cvt_pk_bf16_f32 v151, v178, v179\n"
"v_mul_f32 v253, v253, v252\n"
"v_mfma_f32_16x16x32_bf16 v[164:167], v[220:223], a[204:207], v[164:167]\n"
"v_cvt_pk_bf16_f32 v134, v168, v169\n"
"v_fmac_f32 v253, v251, v250\n"
"v_mfma_f32_16x16x32_bf16 v[164:167], v[208:211], a[192:195], v[164:167]\n"
"ds_read_b64_tr_b16 a[252:253], v235 offset:0\n"
"ds_read_b64_tr_b16 a[254:255], v235 offset:4096\n"
"v_mfma_f32_16x16x32_bf16 v[164:167], v[212:215], a[196:199], v[164:167]\n"
"s_waitcnt lgkmcnt(4)\n"
"v_cvt_pk_bf16_f32 v135, v170, v171\n"
"v_mfma_f32_16x16x32_bf16 a[216:219], v[124:127], a[244:247], 0\n"
"ds_read_b128 v[216:219], v228 offset:20992\n"
"v_cvt_pk_bf16_f32 v143, v174, v175\n"
"v_mfma_f32_16x16x32_bf16 v[8:11], v[128:131], a[244:247], 0\n"
"s_waitcnt lgkmcnt(4)\n"
"s_add_u32 m0, s44, 98304\n"
"v_rcp_f32 v253, v253\n"
"v_mfma_f32_16x16x32_bf16 v[8:11], v[132:135], a[248:251], v[8:11]\n"
"buffer_load_dwordx4 v240, s[20:23], s56 offen nt lds\n"
"v_exp_f32 v152, v152\n"
"v_exp_f32 v155, v155\n"
"v_exp_f32 v158, v158\n"
"v_exp_f32 v167, v167\n"
"v_mfma_f32_16x16x32_bf16 v[40:43], v[136:139], a[244:247], 0\n"
"v_accvgpr_read_b32 v254, a208\n"
"v_mul_f32_dpp v85, v206, v85 row_newbcast:1 row_mask:0xf bank_mask:0xf\n"
"v_mfma_f32_16x16x32_bf16 v[40:43], v[140:143], a[248:251], v[40:43]\n"
"v_mul_f32 v250, v250, v253\n"
"v_accvgpr_read_b32 v251, a211\n"
"v_mfma_f32_16x16x32_bf16 v[72:75], v[144:147], a[244:247], 0\n"
"ds_read_b128 v[220:223], v229 offset:20992\n"
"v_mul_f32_dpp v17, v246, v17 row_newbcast:1 row_mask:0xf bank_mask:0xf\n"
"v_mfma_f32_16x16x32_bf16 v[72:75], v[148:151], a[248:251], v[72:75]\n"
"s_waitcnt lgkmcnt(2)\n"
"v_exp_f32 v164, v164\n"
"v_mfma_f32_16x16x32_bf16 v[76:79], v[148:151], v[224:227], 0\n"
"v_exp_f32 v153, v153\n"
"v_mfma_f32_16x16x32_bf16 v[76:79], v[144:147], a[252:255], v[76:79]\n"
"v_mul_f32 v252, v252, v253\n"
"v_mov_b32_dpp v250, v250 row_ror:12 row_mask:0x2 bank_mask:0xf\n"
"v_mfma_f32_16x16x32_bf16 v[44:47], v[140:143], v[224:227], 0\n"
"ds_read_b64_tr_b16 a[244:245], v234 offset:512\n"
"v_mov_b32_dpp v252, v252 row_ror:12 row_mask:0x2 bank_mask:0xf\n"
"v_mfma_f32_16x16x32_bf16 v[44:47], v[136:139], a[252:255], v[44:47]\n"
"ds_read_b128 v[212:215], v229 offset:20480\n"
"v_mov_b32_dpp v250, v250 row_ror:8 row_mask:0x4 bank_mask:0xf\n"
"v_mfma_f32_16x16x32_bf16 v[12:15], v[132:135], v[224:227], 0\n"
"s_add_u32 s40, s56, s52\n"
"ds_read_b128 v[208:211], v228 offset:20480\n"
"v_cvt_pk_bf16_f32 v96, v152, v153\n"
"v_mfma_f32_16x16x32_bf16 v[12:15], v[128:131], a[252:255], v[12:15]\n"
"buffer_load_dwordx4 v207, s[20:23], s40 offen offset:1024 nt lds\n"
"v_mov_b32_dpp v252, v252 row_ror:8 row_mask:0x4 bank_mask:0xf\n"
"v_mov_b32_dpp v250, v250 row_ror:4 row_mask:0x8 bank_mask:0xf\n"
"v_accvgpr_read_b32 v255, a209\n"
"v_accvgpr_read_b32 v253, a210\n"
"v_mov_b64 v[196:197], 0\n"
"v_cvt_pk_bf16_f32 v97, v154, v155\n"
"v_exp_f32 v157, v157\n"
"v_mfma_f32_16x16x32_bf16 a[220:223], v[124:127], a[252:255], 0\n"
"v_mov_b32_dpp v252, v252 row_ror:4 row_mask:0x8 bank_mask:0xf\n"
"v_mul_f32_dpp v24, v250, v24 row_newbcast:0 row_mask:0xf bank_mask:0xf\n"
"v_mul_f32_dpp v25, v250, v25 row_newbcast:1 row_mask:0xf bank_mask:0xf\n"
"v_mul_f32_dpp v26, v250, v26 row_newbcast:2 row_mask:0xf bank_mask:0xf\n"
"v_mul_f32_dpp v29, v250, v29 row_newbcast:1 row_mask:0xf bank_mask:0xf\n"
"v_mul_f32_dpp v54, v247, v54 row_newbcast:2 row_mask:0xf bank_mask:0xf\n"
"v_exp_f32 v162, v162\n"
"v_exp_f32 v163, v163\n"
"v_smfmac_f32_16x16x64_bf16 v[196:199], a[240:243], v[128:135], v241\n"
"v_mul_f32_dpp v27, v250, v27 row_newbcast:3 row_mask:0xf bank_mask:0xf\n"
"v_fmac_f32_dpp v24, v252, v254 row_newbcast:0 row_mask:0xf bank_mask:0xf\n"
"v_mfma_f32_16x16x32_bf16 v[202:205], a[240:243], v[124:127], v[202:205]\n"
"s_waitcnt lgkmcnt(3)\n"
"ds_read_b64_tr_b16 a[252:253], v235 offset:512\n"
"ds_read_b64_tr_b16 a[254:255], v235 offset:4608\n"
"ds_read_b64_tr_b16 v[224:225], v239 offset:512\n"
"v_fmac_f32_dpp v25, v252, v255 row_newbcast:1 row_mask:0xf bank_mask:0xf\n"
"v_fmac_f32_dpp v26, v252, v253 row_newbcast:2 row_mask:0xf bank_mask:0xf\n"
"v_mul_f32_dpp v28, v250, v28 row_newbcast:0 row_mask:0xf bank_mask:0xf\n"
"v_mul_f32_dpp v30, v250, v30 row_newbcast:2 row_mask:0xf bank_mask:0xf\n"
"v_mul_f32_dpp v50, v247, v50 row_newbcast:2 row_mask:0xf bank_mask:0xf\n"
"v_mul_f32_dpp v58, v250, v58 row_newbcast:2 row_mask:0xf bank_mask:0xf\n"
"v_exp_f32 v156, v156\n"
"v_mfma_f32_16x16x32_bf16 v[168:171], v[216:219], a[8:11], v[180:183]\n"
"v_accvgpr_read_b32 v254, a212\n"
"v_accvgpr_read_b32 v253, a214\n"
"v_mfma_f32_16x16x32_bf16 v[168:171], v[220:223], a[12:15], v[168:171]\n"
"s_waitcnt lgkmcnt(3)\n"
"v_mul_f32_dpp v48, v247, v48 row_newbcast:0 row_mask:0xf bank_mask:0xf\n"
"v_mul_f32_dpp v57, v250, v57 row_newbcast:1 row_mask:0xf bank_mask:0xf\n"
"v_mfma_f32_16x16x32_bf16 v[168:171], v[208:211], a[0:3], v[168:171]\n"
"ds_read_b64_tr_b16 a[250:251], v238 offset:4608\n"
"v_mul_f32_dpp v56, v250, v56 row_newbcast:0 row_mask:0xf bank_mask:0xf\n"
"v_mfma_f32_16x16x32_bf16 v[168:171], v[212:215], a[4:7], v[168:171]\n"
"ds_read_b64_tr_b16 a[248:249], v238 offset:512\n"
"ds_read_b64_tr_b16 v[226:227], v239 offset:4608\n"
"v_fmac_f32_dpp v27, v252, v251 row_newbcast:3 row_mask:0xf bank_mask:0xf\n"
"v_mul_f32_dpp v31, v250, v31 row_newbcast:3 row_mask:0xf bank_mask:0xf\n"
"v_fmac_f32_dpp v28, v252, v254 row_newbcast:0 row_mask:0xf bank_mask:0xf\n"
"v_fmac_f32_dpp v30, v252, v253 row_newbcast:2 row_mask:0xf bank_mask:0xf\n"
"v_cvt_pk_bf16_f32 v24, v24, v25\n"
"v_cvt_pk_bf16_f32 v25, v26, v27\n"
"v_mul_f32_dpp v52, v247, v52 row_newbcast:0 row_mask:0xf bank_mask:0xf\n"
"v_accvgpr_read_b32 v254, a224\n"
"v_mul_f32_dpp v59, v250, v59 row_newbcast:3 row_mask:0xf bank_mask:0xf\n"
"v_mul_f32_dpp v60, v250, v60 row_newbcast:0 row_mask:0xf bank_mask:0xf\n"
"v_mul_f32_dpp v62, v250, v62 row_newbcast:2 row_mask:0xf bank_mask:0xf\n"
"v_mul_f32_dpp v63, v250, v63 row_newbcast:3 row_mask:0xf bank_mask:0xf\n"
"v_exp_f32 v159, v159\n"
"v_exp_f32 v161, v161\n"
"v_mfma_f32_16x16x32_bf16 v[172:175], v[208:211], a[16:19], v[184:187]\n"
"v_accvgpr_read_b32 v255, a213\n"
"v_accvgpr_read_b32 v251, a215\n"
"v_mfma_f32_16x16x32_bf16 v[172:175], v[212:215], a[20:23], v[172:175]\n"
"ds_read_b64_tr_b16 a[246:247], v234 offset:4608\n"
"v_fmac_f32_dpp v29, v252, v255 row_newbcast:1 row_mask:0xf bank_mask:0xf\n"
"v_mfma_f32_16x16x32_bf16 v[172:175], v[216:219], a[24:27], v[172:175]\n"
"s_add_u32 s40, s56, s53\n"
"v_xor_b32 v249, s75, v249\n"
"v_accvgpr_read_b32 v255, a225\n"
"v_mfma_f32_16x16x32_bf16 v[172:175], v[220:223], a[28:31], v[172:175]\n"
"buffer_load_dwordx4 v240, s[20:23], s40 offen offset:2048 nt lds\n"
"v_fmac_f32_dpp v31, v252, v251 row_newbcast:3 row_mask:0xf bank_mask:0xf\n"
"v_cvt_pk_bf16_f32 v27, v30, v31\n"
"v_mul_f32_dpp v16, v246, v16 row_newbcast:0 row_mask:0xf bank_mask:0xf\n"
"v_mul_f32_dpp v51, v247, v51 row_newbcast:3 row_mask:0xf bank_mask:0xf\n"
"v_accvgpr_read_b32 v253, a226\n"
"v_accvgpr_read_b32 v251, a227\n"
"v_fmac_f32_dpp v56, v252, v254 row_newbcast:0 row_mask:0xf bank_mask:0xf\n"
"v_accvgpr_read_b32 v254, a228\n"
"v_mfma_f32_16x16x32_bf16 v[176:179], v[216:219], a[40:43], v[188:191]\n"
"v_cvt_pk_bf16_f32 v26, v28, v29\n"
"v_fmac_f32_dpp v57, v252, v255 row_newbcast:1 row_mask:0xf bank_mask:0xf\n"
"v_mfma_f32_16x16x32_bf16 v[176:179], v[220:223], a[44:47], v[176:179]\n"
"v_fmac_f32_dpp v58, v252, v253 row_newbcast:2 row_mask:0xf bank_mask:0xf\n"
"v_accvgpr_read_b32 v255, a229\n"
"v_mfma_f32_16x16x32_bf16 v[176:179], v[208:211], a[32:35], v[176:179]\n"
"v_cvt_pk_bf16_f32 v16, v16, v17\n"
"v_mul_f32_dpp v61, v250, v61 row_newbcast:1 row_mask:0xf bank_mask:0xf\n"
"v_mfma_f32_16x16x32_bf16 v[176:179], v[212:215], a[36:39], v[176:179]\n"
"ds_write_b128 v243, v[24:27] offset:0\n"
"v_cvt_pk_bf16_f32 v48, v48, v49\n"
"v_fmac_f32_dpp v59, v252, v251 row_newbcast:3 row_mask:0xf bank_mask:0xf\n"
"v_accvgpr_read_b32 v253, a230\n"
"v_accvgpr_read_b32 v251, a231\n"
"v_fmac_f32_dpp v60, v252, v254 row_newbcast:0 row_mask:0xf bank_mask:0xf\n"
"v_fmac_f32_dpp v61, v252, v255 row_newbcast:1 row_mask:0xf bank_mask:0xf\n"
"v_cvt_pk_bf16_f32 v56, v56, v57\n"
"v_cvt_pk_bf16_f32 v57, v58, v59\n"
"v_cvt_pk_bf16_f32 v58, v60, v61\n"
"v_mfma_f32_16x16x32_bf16 v[152:155], v[208:211], a[192:195], v[192:195]\n"
"v_mul_f32_dpp v81, v206, v81 row_newbcast:1 row_mask:0xf bank_mask:0xf\n"
"v_mul_f32_dpp v82, v206, v82 row_newbcast:2 row_mask:0xf bank_mask:0xf\n"
"v_mul_f32_dpp v87, v206, v87 row_newbcast:3 row_mask:0xf bank_mask:0xf\n"
"v_fmac_f32_dpp v62, v252, v253 row_newbcast:2 row_mask:0xf bank_mask:0xf\n"
"v_fmac_f32_dpp v63, v252, v251 row_newbcast:3 row_mask:0xf bank_mask:0xf\n"
"v_cvt_pk_bf16_f32 v59, v62, v63\n"
"ds_write_b128 v243, v[56:59] offset:2048\n"
"v_mfma_f32_16x16x32_bf16 v[152:155], v[212:215], a[196:199], v[152:155]\n"
"v_exp_f32 v174, v174\n"
"v_mfma_f32_16x16x32_bf16 v[152:155], v[216:219], a[200:203], v[152:155]\n"
"v_exp_f32 v166, v166\n"
"v_mfma_f32_16x16x32_bf16 v[152:155], v[220:223], a[204:207], v[152:155]\n"
"s_waitcnt lgkmcnt(2)\n"
"ds_read_b128 v[212:215], v231 offset:16384\n"
"v_cvt_pk_bf16_f32 v105, v158, v159\n"
"v_mfma_f32_16x16x32_bf16 a[232:235], v[124:127], a[244:247], 0\n"
"v_mul_f32_dpp v20, v246, v20 row_newbcast:0 row_mask:0xf bank_mask:0xf\n"
"v_mul_f32_dpp v23, v246, v23 row_newbcast:3 row_mask:0xf bank_mask:0xf\n"
"v_mfma_f32_16x16x32_bf16 v[24:27], v[128:131], a[244:247], 0\n"
"s_add_u32 s40, s56, s54\n"
"ds_read_b128 v[220:223], v231 offset:16896\n"
"v_mul_f32_dpp v21, v246, v21 row_newbcast:1 row_mask:0xf bank_mask:0xf\n"
"v_mfma_f32_16x16x32_bf16 v[24:27], v[132:135], a[248:251], v[24:27]\n"
"buffer_load_dwordx4 v207, s[20:23], s40 offen offset:3072 nt lds\n"
"v_mfma_f32_16x16x32_bf16 v[56:59], v[136:139], a[244:247], 0\n"
"s_add_u32 s55, s55, s59\n"
"v_cvt_pk_bf16_f32 v17, v18, v19\n"
"v_cvt_pk_bf16_f32 v104, v156, v157\n"
"v_mfma_f32_16x16x32_bf16 v[56:59], v[140:143], a[248:251], v[56:59]\n"
"s_or_b32 s55, s55, s58\n"
"v_exp_f32 v178, v178\n"
"v_mfma_f32_16x16x32_bf16 v[88:91], v[144:147], a[244:247], 0\n"
"s_xor_b32 s56, s55, s57\n"
"ds_read_b128 v[208:211], v230 offset:16384\n"
"v_cvt_pk_bf16_f32 v49, v50, v51\n"
"v_mfma_f32_16x16x32_bf16 v[88:91], v[148:151], a[248:251], v[88:91]\n"
"ds_read_b128 v[216:219], v230 offset:16896\n"
"v_cvt_pk_bf16_f32 v18, v20, v21\n"
"v_mfma_f32_16x16x32_bf16 v[92:95], v[148:151], v[224:227], 0\n"
"v_exp_f32 v160, v160\n"
"v_mfma_f32_16x16x32_bf16 v[92:95], v[144:147], a[252:255], v[92:95]\n"
"v_exp_f32 v165, v165\n"
"v_mfma_f32_16x16x32_bf16 v[60:63], v[140:143], v[224:227], 0\n"
"s_add_u32 m0, s44, 49152\n"
"ds_read_b64_tr_b16 a[248:249], v236 offset:0\n"
"v_mul_f32_dpp v80, v206, v80 row_newbcast:0 row_mask:0xf bank_mask:0xf\n"
"v_mfma_f32_16x16x32_bf16 v[60:63], v[136:139], a[252:255], v[60:63]\n"
"buffer_load_dwordx4 v240, s[16:19], s56 offen nt lds\n"
"v_mfma_f32_16x16x32_bf16 v[28:31], v[132:135], v[224:227], 0\n"
"v_exp_f32 v177, v177\n"
"v_mfma_f32_16x16x32_bf16 v[28:31], v[128:131], a[252:255], v[28:31]\n"
"v_exp_f32 v169, v169\n"
"v_mfma_f32_16x16x32_bf16 a[236:239], v[124:127], a[252:255], 0\n"
"ds_read_b64_tr_b16 a[250:251], v236 offset:4096\n"
"ds_read_b64_tr_b16 v[224:225], v237 offset:0\n"
"v_smfmac_f32_16x16x64_bf16 v[198:201], a[240:243], v[136:143], v241\n"
"s_waitcnt lgkmcnt(3)\n"
"ds_read_b64_tr_b16 a[246:247], v232 offset:4096\n"
"v_cvt_pk_bf16_f32 v112, v160, v161\n"
"v_mfma_f32_16x16x32_bf16 v[156:159], v[208:211], a[0:3], v[180:183]\n"
"v_cvt_pk_bf16_f32 v80, v80, v81\n"
"v_cvt_pk_bf16_f32 v113, v162, v163\n"
"v_mfma_f32_16x16x32_bf16 v[156:159], v[212:215], a[4:7], v[156:159]\n"
"ds_read_b64_tr_b16 a[254:255], v233 offset:4096\n"
"v_xor_b32 v248, s75, v248\n"
"v_mfma_f32_16x16x32_bf16 v[156:159], v[216:219], a[8:11], v[156:159]\n"
"ds_read_b64_tr_b16 a[244:245], v232 offset:0\n"
"v_cvt_pk_bf16_f32 v120, v164, v165\n"
"v_mfma_f32_16x16x32_bf16 v[156:159], v[220:223], a[12:15], v[156:159]\n"
"v_exp_f32 v170, v170\n"
"v_mfma_f32_16x16x32_bf16 v[160:163], v[216:219], a[24:27], v[184:187]\n"
"v_exp_f32 v179, v179\n"
"v_mfma_f32_16x16x32_bf16 v[160:163], v[220:223], a[28:31], v[160:163]\n"
"ds_read_b64_tr_b16 v[226:227], v237 offset:4096\n"
"v_cvt_pk_bf16_f32 v121, v166, v167\n"
"v_mfma_f32_16x16x32_bf16 v[160:163], v[208:211], a[16:19], v[160:163]\n"
"s_add_u32 s40, s56, s52\n"
"v_exp_f32 v168, v168\n"
"v_mfma_f32_16x16x32_bf16 v[160:163], v[212:215], a[20:23], v[160:163]\n"
"buffer_load_dwordx4 v207, s[16:19], s40 offen offset:1024 nt lds\n"
"v_mfma_f32_16x16x32_bf16 v[164:167], v[208:211], a[32:35], v[188:191]\n"
"v_exp_f32 v176, v176\n"
"v_mfma_f32_16x16x32_bf16 v[164:167], v[212:215], a[36:39], v[164:167]\n"
"v_cvt_pk_bf16_f32 v19, v22, v23\n"
"v_mul_f32_dpp v53, v247, v53 row_newbcast:1 row_mask:0xf bank_mask:0xf\n"
"v_mfma_f32_16x16x32_bf16 v[164:167], v[216:219], a[40:43], v[164:167]\n"
"ds_read_b64_tr_b16 a[252:253], v233 offset:0\n"
"ds_read_b128 v[208:211], v230 offset:20480\n"
"v_mfma_f32_16x16x32_bf16 v[164:167], v[220:223], a[44:47], v[164:167]\n"
"s_waitcnt lgkmcnt(3)\n"
"v_exp_f32 v158, v158\n"
"v_mfma_f32_16x16x32_bf16 a[208:211], v[124:127], a[244:247], 0\n"
"ds_read_b128 v[220:223], v231 offset:20992\n"
"v_cvt_pk_bf16_f32 v81, v82, v83\n"
"v_mfma_f32_16x16x32_bf16 v[0:3], v[128:131], a[244:247], 0\n"
"ds_read_b128 v[212:215], v231 offset:20480\n"
"v_mul_f32_dpp v55, v247, v55 row_newbcast:3 row_mask:0xf bank_mask:0xf\n"
"v_mfma_f32_16x16x32_bf16 v[0:3], v[132:135], a[248:251], v[0:3]\n"
"v_exp_f32 v175, v175\n"
"v_mfma_f32_16x16x32_bf16 v[32:35], v[136:139], a[244:247], 0\n"
"s_add_u32 s40, s56, s53\n"
"ds_read_b128 v[216:219], v230 offset:20992\n"
"v_mul_f32_dpp v84, v206, v84 row_newbcast:0 row_mask:0xf bank_mask:0xf\n"
"v_mfma_f32_16x16x32_bf16 v[32:35], v[140:143], a[248:251], v[32:35]\n"
"buffer_load_dwordx4 v240, s[16:19], s40 offen offset:2048 nt lds\n"
"v_mfma_f32_16x16x32_bf16 v[64:67], v[144:147], a[244:247], 0\n"
"v_cvt_pk_bf16_f32 v50, v52, v53\n"
"v_mul_f32_dpp v86, v206, v86 row_newbcast:2 row_mask:0xf bank_mask:0xf\n"
"v_mfma_f32_16x16x32_bf16 v[64:67], v[148:151], a[248:251], v[64:67]\n"
"s_waitcnt lgkmcnt(3)\n"
"ds_write_b128 v248, v[16:19] offset:49152\n"
"v_cvt_pk_bf16_f32 v82, v84, v85\n"
"v_exp_f32 v172, v172\n"
"v_exp_f32 v152, v152\n"
"v_exp_f32 v159, v159\n"
"v_exp_f32 v161, v161\n"
"v_exp_f32 v164, v164\n"
"v_exp_f32 v167, v167\n"
"v_mfma_f32_16x16x32_bf16 v[68:71], v[148:151], v[224:227], 0\n"
"v_cvt_pk_bf16_f32 v51, v54, v55\n"
"v_cvt_pk_bf16_f32 v98, v168, v169\n"
"v_mfma_f32_16x16x32_bf16 v[68:71], v[144:147], a[252:255], v[68:71]\n"
"ds_write_b128 v248, v[48:51] offset:50176\n"
"v_exp_f32 v171, v171\n"
"v_exp_f32 v173, v173\n"
"v_exp_f32 v156, v156\n"
"v_exp_f32 v163, v163\n"
"v_mfma_f32_16x16x32_bf16 v[36:39], v[140:143], v[224:227], 0\n"
"ds_read_b64_tr_b16 a[246:247], v232 offset:4608\n"
"ds_read_b64_tr_b16 a[250:251], v236 offset:4608\n"
"v_mfma_f32_16x16x32_bf16 v[36:39], v[136:139], a[252:255], v[36:39]\n"
"ds_read_b64_tr_b16 a[74:75], v249 offset:50184\n"
"v_cvt_pk_bf16_f32 v107, v174, v175\n"
"v_mfma_f32_16x16x32_bf16 v[4:7], v[132:135], v[224:227], 0\n"
"ds_read_b64_tr_b16 a[56:57], v249 offset:49152\n"
"v_cvt_pk_bf16_f32 v83, v86, v87\n"
"v_mfma_f32_16x16x32_bf16 v[4:7], v[128:131], a[252:255], v[4:7]\n"
"ds_write_b128 v248, v[80:83] offset:51200\n"
"v_cvt_pk_bf16_f32 v99, v170, v171\n"
"v_exp_f32 v155, v155\n"
"v_exp_f32 v157, v157\n"
"v_mfma_f32_16x16x32_bf16 a[212:215], v[124:127], a[252:255], 0\n"
"s_add_u32 s40, s56, s54\n"
"ds_read_b64_tr_b16 a[58:59], v249 offset:49160\n"
"ds_read_b64_tr_b16 v[224:225], v237 offset:512\n"
"v_smfmac_f32_16x16x64_bf16 v[200:203], a[240:243], v[144:151], v241\n"
"buffer_load_dwordx4 v207, s[16:19], s40 offen offset:3072 nt lds\n"
"v_mfma_f32_16x16x32_bf16 v[168:171], v[208:211], a[0:3], v[180:183]\n"
"s_waitcnt lgkmcnt(8)\n"
"v_exp_f32 v153, v153\n"
"v_mfma_f32_16x16x32_bf16 v[168:171], v[212:215], a[4:7], v[168:171]\n"
"ds_read_b64_tr_b16 a[72:73], v249 offset:50176\n"
"ds_read_b64_tr_b16 a[90:91], v249 offset:51208\n"
"v_mfma_f32_16x16x32_bf16 v[168:171], v[216:219], a[8:11], v[168:171]\n"
"ds_read_b64_tr_b16 a[88:89], v249 offset:51200\n"
"v_cvt_pk_bf16_f32 v115, v178, v179\n"
"v_mfma_f32_16x16x32_bf16 v[168:171], v[220:223], a[12:15], v[168:171]\n"
"ds_read_b64_tr_b16 a[244:245], v232 offset:512\n"
"v_cvt_pk_bf16_f32 v106, v172, v173\n"
"v_mfma_f32_16x16x32_bf16 v[172:175], v[216:219], a[24:27], v[184:187]\n"
"ds_read_b64_tr_b16 a[248:249], v236 offset:512\n"
"v_cvt_pk_bf16_f32 v114, v176, v177\n"
"v_mfma_f32_16x16x32_bf16 v[172:175], v[220:223], a[28:31], v[172:175]\n"
"v_exp_f32 v162, v162\n"
"v_mfma_f32_16x16x32_bf16 v[172:175], v[208:211], a[16:19], v[172:175]\n"
"ds_read_b64_tr_b16 v[226:227], v237 offset:4608\n"
"v_cvt_pk_bf16_f32 v122, v152, v153\n"
"v_mfma_f32_16x16x32_bf16 v[172:175], v[212:215], a[20:23], v[172:175]\n"
"s_cbranch_vccnz LPCT1_%=\nLPKT1_%=:\n"
"ds_read_b64_tr_b16 a[252:253], v233 offset:512\n"
"v_mfma_f32_16x16x32_bf16 v[176:179], v[208:211], a[32:35], v[188:191]\n"
"v_exp_f32 v166, v166\n"
"v_mfma_f32_16x16x32_bf16 v[176:179], v[212:215], a[36:39], v[176:179]\n"
"ds_read_b64_tr_b16 a[254:255], v233 offset:4608\n"
"v_cvt_pk_bf16_f32 v109, v162, v163\n"
"v_mfma_f32_16x16x32_bf16 v[176:179], v[216:219], a[40:43], v[176:179]\n"
"v_exp_f32 v169, v169\n"
"v_mfma_f32_16x16x32_bf16 v[176:179], v[220:223], a[44:47], v[176:179]\n"
"s_waitcnt lgkmcnt(4)\n"
"v_cvt_pk_bf16_f32 v101, v158, v159\n"
"v_mfma_f32_16x16x32_bf16 a[224:227], v[124:127], a[244:247], 0\n"
"v_exp_f32 v154, v154\n"
"v_mfma_f32_16x16x32_bf16 v[16:19], v[128:131], a[244:247], 0\n"
"s_waitcnt vmcnt(8) lgkmcnt(3)\n"
"v_exp_f32 v168, v168\n"
"v_mfma_f32_16x16x32_bf16 v[16:19], v[132:135], a[248:251], v[16:19]\n"
"s_barrier\n"
"ds_read_b128 v[212:215], v229 offset:32768\n"
"ds_read_b128 v[216:219], v228 offset:33280\n"
"v_exp_f32 v165, v165\n"
"v_mfma_f32_16x16x32_bf16 v[48:51], v[136:139], a[244:247], 0\n"
"ds_read_b128 v[220:223], v229 offset:33280\n"
"v_xor_b32 v249, s75, v249\n"
"v_mfma_f32_16x16x32_bf16 v[48:51], v[140:143], a[248:251], v[48:51]\n"
"ds_read_b128 v[208:211], v228 offset:32768\n"
"v_add_u32 v243, 0x1000, v243\n"
"v_mfma_f32_16x16x32_bf16 v[80:83], v[144:147], a[244:247], 0\n"
"ds_read_b64_tr_b16 a[246:247], v234 offset:20480\n"
"v_cvt_pk_bf16_f32 v117, v166, v167\n"
"v_mfma_f32_16x16x32_bf16 v[80:83], v[148:151], a[248:251], v[80:83]\n"
"s_waitcnt lgkmcnt(5)\n"
"ds_read_b64_tr_b16 a[250:251], v238 offset:20480\n"
"v_xor_b32 v248, s75, v248\n"
"v_mfma_f32_16x16x32_bf16 v[84:87], v[148:151], v[224:227], 0\n"
"v_cvt_pk_bf16_f32 v100, v156, v157\n"
"v_cvt_pk_bf16_f32 v116, v164, v165\n"
"v_mfma_f32_16x16x32_bf16 v[84:87], v[144:147], a[252:255], v[84:87]\n"
"v_exp_f32 v160, v160\n"
"v_mfma_f32_16x16x32_bf16 v[52:55], v[140:143], v[224:227], 0\n"
"v_exp_f32 v176, v176\n"
"v_mfma_f32_16x16x32_bf16 v[52:55], v[136:139], a[252:255], v[52:55]\n"
"v_exp_f32 v177, v177\n"
"v_mfma_f32_16x16x32_bf16 v[20:23], v[132:135], v[224:227], 0\n"
"v_cvt_pk_bf16_f32 v123, v154, v155\n"
"v_cvt_pk_bf16_f32 v118, v176, v177\n"
"v_mfma_f32_16x16x32_bf16 v[20:23], v[128:131], a[252:255], v[20:23]\n"
"v_exp_f32 v174, v174\n"
"v_mfma_f32_16x16x32_bf16 a[228:231], v[124:127], a[252:255], 0\n"
"s_waitcnt lgkmcnt(1)\n"
"ds_read_b64_tr_b16 a[254:255], v235 offset:20480\n"
"s_branch LAY2_%=\n"
"LAX_%=:\n"
"v_mfma_f32_16x16x32_bf16 a[224:227], v[124:127], a[244:247], a[224:227]\n"
"v_exp_f32 v162, v162\n"
"v_mfma_f32_16x16x32_bf16 v[16:19], v[128:131], a[244:247], v[16:19]\n"
"s_waitcnt vmcnt(8)\n"
"v_exp_f32 v171, v171\n"
"v_mfma_f32_16x16x32_bf16 v[16:19], v[132:135], a[248:251], v[16:19]\n"
"s_barrier\n"
"v_exp_f32 v169, v169\n"
"v_exp_f32 v172, v172\n"
"v_exp_f32 v173, v173\n"
"v_exp_f32 v174, v174\n"
"v_lshlrev_b32_e64 v244, 24, s43\n"
"v_mfma_f32_16x16x32_bf16 v[48:51], v[136:139], a[244:247], v[48:51]\n"
"v_exp_f32 v170, v170\n"
"v_mfma_f32_16x16x32_bf16 v[48:51], v[140:143], a[248:251], v[48:51]\n"
"v_exp_f32 v179, v179\n"
"v_mfma_f32_16x16x32_bf16 v[80:83], v[144:147], a[244:247], v[80:83]\n"
"ds_read_b64_tr_b16 a[246:247], v234 offset:53248\n"
"v_cvt_pk_bf16_f32 v117, v166, v167\n"
"v_mfma_f32_16x16x32_bf16 v[80:83], v[148:151], a[248:251], v[80:83]\n"
"ds_read_b64_tr_b16 a[244:245], v234 offset:49152\n"
"v_cvt_pk_bf16_f32 v116, v164, v165\n"
"v_mfma_f32_16x16x32_bf16 v[84:87], v[148:151], v[224:227], v[84:87]\n"
"v_cvt_pk_bf16_f32 v100, v156, v157\n"
"v_accvgpr_read_b32 v253, a96\n"
"v_mfma_f32_16x16x32_bf16 v[84:87], v[144:147], a[252:255], v[84:87]\n"
"ds_read_b64_tr_b16 a[248:249], v238 offset:49152\n"
"ds_read_b64_tr_b16 a[250:251], v238 offset:53248\n"
"v_exp_f32 v168, v168\n"
"v_exp_f32 v175, v175\n"
"v_exp_f32 v176, v176\n"
"v_exp_f32 v177, v177\n"
"v_exp_f32 v178, v178\n"
"v_mfma_f32_16x16x32_bf16 v[52:55], v[140:143], v[224:227], v[52:55]\n"
"v_cvt_pk_bf16_f32 v102, v168, v169\n"
"v_cvt_pk_bf16_f32 v110, v172, v173\n"
"v_mfma_f32_16x16x32_bf16 v[52:55], v[136:139], a[252:255], v[52:55]\n"
"v_cvt_pk_bf16_f32 v103, v170, v171\n"
"v_and_b32 v253, 0x7fff0000, v253\n"
"v_mfma_f32_16x16x32_bf16 v[20:23], v[132:135], v[224:227], v[20:23]\n"
"ds_read_b64_tr_b16 v[224:225], v239 offset:49152\n"
"v_cvt_pk_bf16_f32 v101, v158, v159\n"
"v_mfma_f32_16x16x32_bf16 v[20:23], v[128:131], a[252:255], v[20:23]\n"
"v_cvt_pk_bf16_f32 v123, v154, v155\n"
"v_cvt_pk_bf16_f32 v111, v174, v175\n"
"v_mfma_f32_16x16x32_bf16 a[228:231], v[124:127], a[252:255], a[228:231]\n"
"s_waitcnt lgkmcnt(3)\n"
"ds_read_b64_tr_b16 a[254:255], v235 offset:53248\n"
"v_mul_f32 v244, 0x45800000, v244\n"
"v_mfma_f32_16x16x32_bf16 a[216:219], v[120:123], a[244:247], a[216:219]\n"
"s_mov_b64 s[28:29], s[8:9]\n"
"ds_read_b64_tr_b16 v[226:227], v239 offset:53248\n"
"v_cvt_pk_bf16_f32 v118, v176, v177\n"
"v_smfmac_f32_16x16x64_bf16 v[196:199], a[240:243], v[96:103], v241\n"
"s_mov_b32 s30, 655360\n"
"ds_read_b64_tr_b16 a[252:253], v235 offset:49152\n"
"v_cvt_pk_bf16_f32 v108, v160, v161\n"
"v_mfma_f32_16x16x32_bf16 v[8:11], v[96:99], a[244:247], v[8:11]\n"
"s_waitcnt lgkmcnt(4)\n"
"s_mov_b32 s71, s44\n"
"v_cvt_pk_bf16_f32 v119, v178, v179\n"
"v_max_u32_dpp v253, v253, v253 quad_perm:[1,0,3,2] row_mask:0xf bank_mask:0xf\n"
"v_mfma_f32_16x16x32_bf16 v[8:11], v[100:103], a[248:251], v[8:11]\n"
"s_mov_b32 s70, s95\n"
"v_and_b32 v252, 0x7fff0000, v252\n"
"v_and_b32 v245, 0x3f0, v242\n"
"v_smfmac_f32_16x16x64_bf16 v[200:203], a[240:243], v[112:119], v241\n"
"v_cvt_pk_bf16_f32 v109, v162, v163\n"
"v_max_u32_dpp v253, v253, v253 quad_perm:[2,3,0,1] row_mask:0xf bank_mask:0xf\n"
"v_mfma_f32_16x16x32_bf16 v[40:43], v[104:107], a[244:247], v[40:43]\n"
"v_add_f32 v196, v196, v197\n"
"v_lshrrev_b32 v245, 1, v245\n"
"v_mfma_f32_16x16x32_bf16 v[40:43], v[108:111], a[248:251], v[40:43]\n"
"s_add_u32 s41, s70, 327680\n"
"s_add_u32 m0, s71, 32768\n"
"v_max_u32_dpp v252, v252, v252 quad_perm:[1,0,3,2] row_mask:0xf bank_mask:0xf\n"
"v_smfmac_f32_16x16x64_bf16 v[198:201], a[240:243], v[104:111], v241\n"
"buffer_load_dwordx4 v240, s[28:31], s41 offen lds\n"
"v_mfma_f32_16x16x32_bf16 v[72:75], v[112:115], a[244:247], v[72:75]\n"
"v_max_u32_dpp v252, v252, v252 quad_perm:[2,3,0,1] row_mask:0xf bank_mask:0xf\n"
"v_max_u32_dpp v253, v253, v253 row_half_mirror row_mask:0xf bank_mask:0xf\n"
"v_mfma_f32_16x16x32_bf16 v[72:75], v[116:119], a[248:251], v[72:75]\n"
"ds_read_b64_tr_b16 a[244:245], v234 offset:49664\n"
"v_sub_u32 v245, v242, v245\n"
"v_mfma_f32_16x16x32_bf16 v[202:205], a[240:243], v[120:123], v[202:205]\n"
"s_waitcnt lgkmcnt(1)\n"
"ds_read_b64_tr_b16 a[246:247], v234 offset:53760\n"
"v_add_f32 v198, v198, v199\n"
"v_mfma_f32_16x16x32_bf16 v[76:79], v[116:119], v[224:227], v[76:79]\n"
"s_add_u32 s40, s41, s52\n"
"ds_read_b64_tr_b16 a[248:249], v238 offset:49664\n"
"ds_read_b64_tr_b16 a[250:251], v238 offset:53760\n"
"v_mfma_f32_16x16x32_bf16 v[76:79], v[112:115], a[252:255], v[76:79]\n"
"buffer_load_dwordx4 v240, s[28:31], s40 offen offset:1024 lds\n"
"v_mfma_f32_16x16x32_bf16 v[44:47], v[108:111], v[224:227], v[44:47]\n"
"v_add_f32 v200, v200, v201\n"
"v_add_f32 v244, v244, v202\n"
"v_mfma_f32_16x16x32_bf16 v[44:47], v[104:107], a[252:255], v[44:47]\n"
"v_add_f32 v254, v196, v198\n"
"v_min3_f32 v244, v244, v196, v198\n"
"v_min_f32 v244, v244, v200\n"
"v_rcp_f32 v244, v244\n"
"v_mfma_f32_16x16x32_bf16 v[12:15], v[100:103], v[224:227], v[12:15]\n"
"s_add_u32 s40, s41, s53\n"
"ds_read_b64_tr_b16 v[224:225], v239 offset:49664\n"
"ds_read_b64_tr_b16 v[226:227], v239 offset:53760\n"
"v_mfma_f32_16x16x32_bf16 v[12:15], v[96:99], a[252:255], v[12:15]\n"
"buffer_load_dwordx4 v240, s[28:31], s40 offen offset:2048 lds\n"
"v_min_u32_dpp v253, v253, v253 row_mirror row_mask:0xf bank_mask:0xf\n"
"v_add_f32 v254, v254, v200\n"
"v_add_f32 v254, v254, v202\n"
"v_fmac_f32 v254, 0x68800000, v244\n"
"v_cmp_nge_f32 vcc, s45, v254\n"
"v_bfe_u32 v254, v242, 8, 2\n"
"v_mfma_f32_16x16x32_bf16 a[220:223], v[120:123], a[252:255], a[220:223]\n"
"s_waitcnt lgkmcnt(4)\n"
"v_xor_b32 v244, 0x11000, v242\n"
"s_cbranch_vccnz LSLOW_%=\nLBACK3_%=:\ns_mov_b64 vcc, s[64:65]\n"
"v_mfma_f32_16x16x32_bf16 a[232:235], v[120:123], a[244:247], a[232:235]\n"
"ds_read_b64_tr_b16 a[252:253], v235 offset:49664\n"
"ds_read_b64_tr_b16 a[254:255], v235 offset:53760\n"
"v_min_u32_dpp v253, v253, v253 row_bcast:15 row_mask:0xa bank_mask:0xf\n"
"v_rcp_f32 v246, v196\n"
"v_rcp_f32 v247, v198\n"
"v_rcp_f32 v206, v200\n"
"v_xor_b32 v250, 0x1000, v245\n"
"v_mfma_f32_16x16x32_bf16 v[24:27], v[96:99], a[244:247], v[24:27]\n"
"s_waitcnt lgkmcnt(4)\n"
"v_mov_b32_dpp v206, v206 row_ror:12 row_mask:0x2 bank_mask:0xf\n"
"v_lshl_add_u32 v254, v254, 3, s43\n"
"v_mfma_f32_16x16x32_bf16 v[24:27], v[100:103], a[248:251], v[24:27]\n"
"ds_write_b128 v244, a[220:223] offset:33792\n"
"v_max_u32_dpp v252, v252, v252 row_half_mirror row_mask:0xf bank_mask:0xf\n"
"v_mov_b32_dpp v246, v246 row_ror:12 row_mask:0x2 bank_mask:0xf\n"
"v_mov_b32_dpp v206, v206 row_ror:8 row_mask:0x4 bank_mask:0xf\n"
"v_mfma_f32_16x16x32_bf16 v[56:59], v[104:107], a[244:247], v[56:59]\n"
"s_add_u32 s40, s41, s54\n"
"v_min_u32_dpp v252, v252, v252 row_mirror row_mask:0xf bank_mask:0xf\n"
"v_mov_b32_dpp v247, v247 row_ror:12 row_mask:0x2 bank_mask:0xf\n"
"v_mfma_f32_16x16x32_bf16 v[56:59], v[108:111], a[248:251], v[56:59]\n"
"buffer_load_dwordx4 v240, s[28:31], s40 offen offset:3072 lds\n"
"v_mfma_f32_16x16x32_bf16 v[88:91], v[112:115], a[244:247], v[88:91]\n"
"s_add_u32 s71, s44, s75\n"
"ds_read_b64_tr_b16 a[244:245], v232 offset:49152\n"
"ds_read_b64_tr_b16 a[246:247], v232 offset:53248\n"
"v_mfma_f32_16x16x32_bf16 v[88:91], v[116:119], a[248:251], v[88:91]\n"
"s_waitcnt lgkmcnt(5)\n"
"s_mov_b32 s70, s100\n"
"ds_write_b128 v244, a[232:235] offset:34816\n"
"v_mov_b32_dpp v246, v246 row_ror:8 row_mask:0x4 bank_mask:0xf\n"
"v_mov_b32_dpp v247, v247 row_ror:8 row_mask:0x4 bank_mask:0xf\n"
"v_mov_b32_dpp v206, v206 row_ror:4 row_mask:0x8 bank_mask:0xf\n"
"v_lshl_add_u32 v254, v254, 6, s75\n"
"v_sub_u32 v230, v234, v254\n"
"v_mfma_f32_16x16x32_bf16 v[92:95], v[116:119], v[224:227], v[92:95]\n"
"s_waitcnt lgkmcnt(4)\n"
"s_add_u32 s41, s70, 163840\n"
"s_add_u32 m0, s71, 16384\n"
"ds_read_b64_tr_b16 a[248:249], v236 offset:49152\n"
"v_mfma_f32_16x16x32_bf16 v[92:95], v[112:115], a[252:255], v[92:95]\n"
"buffer_load_dwordx4 v240, s[28:31], s41 offen lds\n"
"ds_read_b64_tr_b16 a[250:251], v236 offset:53248\n"
"v_min_u32_dpp v252, v252, v252 row_bcast:15 row_mask:0xa bank_mask:0xf\n"
"v_mov_b32_dpp v246, v246 row_ror:4 row_mask:0x8 bank_mask:0xf\n"
"v_mov_b32_dpp v247, v247 row_ror:4 row_mask:0x8 bank_mask:0xf\n"
"v_mul_f32_dpp v72, v206, v72 row_newbcast:0 row_mask:0xf bank_mask:0xf\n"
"v_mul_f32_dpp v74, v206, v74 row_newbcast:2 row_mask:0xf bank_mask:0xf\n"
"v_mul_f32_dpp v76, v206, v76 row_newbcast:0 row_mask:0xf bank_mask:0xf\n"
"v_mul_f32_dpp v77, v206, v77 row_newbcast:1 row_mask:0xf bank_mask:0xf\n"
"v_mfma_f32_16x16x32_bf16 v[60:63], v[108:111], v[224:227], v[60:63]\n"
"v_min_u32_dpp v252, v252, v252 row_bcast:31 row_mask:0xc bank_mask:0xf\n"
"v_mul_f32_dpp v8, v246, v8 row_newbcast:0 row_mask:0xf bank_mask:0xf\n"
"v_mfma_f32_16x16x32_bf16 v[60:63], v[104:107], a[252:255], v[60:63]\n"
"v_mul_f32_dpp v41, v247, v41 row_newbcast:1 row_mask:0xf bank_mask:0xf\n"
"v_mul_f32_dpp v59, v247, v59 row_newbcast:3 row_mask:0xf bank_mask:0xf\n"
"v_mfma_f32_16x16x32_bf16 v[28:31], v[100:103], v[224:227], v[28:31]\n"
"s_add_u32 s40, s41, s52\n"
"ds_read_b64_tr_b16 v[224:225], v237 offset:49152\n"
"v_min_u32_dpp v253, v253, v253 row_bcast:31 row_mask:0xc bank_mask:0xf\n"
"v_mfma_f32_16x16x32_bf16 v[28:31], v[96:99], a[252:255], v[28:31]\n"
"buffer_load_dwordx4 v240, s[28:31], s40 offen offset:1024 lds\n"
"v_mfma_f32_16x16x32_bf16 a[236:239], v[120:123], a[252:255], a[236:239]\n"
"s_waitcnt lgkmcnt(4)\n"
"v_mul_f32_dpp v25, v246, v25 row_newbcast:1 row_mask:0xf bank_mask:0xf\n"
"v_mul_f32_dpp v94, v206, v94 row_newbcast:2 row_mask:0xf bank_mask:0xf\n"
"v_mfma_f32_16x16x32_bf16 a[208:211], v[120:123], a[244:247], a[208:211]\n"
"ds_read_b64_tr_b16 a[252:253], v233 offset:49152\n"
"ds_read_b64_tr_b16 a[254:255], v233 offset:53248\n"
"v_mul_f32 v252, 0x43340000, v252\n"
"v_mul_f32 v252, v252, v253\n"
"v_mul_f32_dpp v10, v246, v10 row_newbcast:2 row_mask:0xf bank_mask:0xf\n"
"v_mul_f32_dpp v14, v246, v14 row_newbcast:2 row_mask:0xf bank_mask:0xf\n"
"v_mul_f32_dpp v40, v247, v40 row_newbcast:0 row_mask:0xf bank_mask:0xf\n"
"v_mul_f32_dpp v46, v247, v46 row_newbcast:2 row_mask:0xf bank_mask:0xf\n"
"v_mul_f32_dpp v73, v206, v73 row_newbcast:1 row_mask:0xf bank_mask:0xf\n"
"v_mul_f32_dpp v90, v206, v90 row_newbcast:2 row_mask:0xf bank_mask:0xf\n"
"v_mfma_f32_16x16x32_bf16 v[0:3], v[96:99], a[244:247], v[0:3]\n"
"s_waitcnt lgkmcnt(3)\n"
"ds_read_b64_tr_b16 v[226:227], v237 offset:53248\n"
"v_mul_f32_dpp v57, v247, v57 row_newbcast:1 row_mask:0xf bank_mask:0xf\n"
"v_mfma_f32_16x16x32_bf16 v[0:3], v[100:103], a[248:251], v[0:3]\n"
"ds_write_b128 v244, a[216:219] offset:32768\n"
"v_min_f32 v252, 0x71800000, v252\n"
"v_mul_f32_dpp v12, v246, v12 row_newbcast:0 row_mask:0xf bank_mask:0xf\n"
"v_mul_f32_dpp v45, v247, v45 row_newbcast:1 row_mask:0xf bank_mask:0xf\n"
"v_mul_f32_dpp v58, v247, v58 row_newbcast:2 row_mask:0xf bank_mask:0xf\n"
"v_mul_f32_dpp v92, v206, v92 row_newbcast:0 row_mask:0xf bank_mask:0xf\n"
"v_mfma_f32_16x16x32_bf16 v[32:35], v[104:107], a[244:247], v[32:35]\n"
"s_add_u32 s40, s41, s53\n"
"v_readlane_b32 s22, v252, 63\n"
"v_mul_f32_dpp v78, v206, v78 row_newbcast:2 row_mask:0xf bank_mask:0xf\n"
"v_mfma_f32_16x16x32_bf16 v[32:35], v[108:111], a[248:251], v[32:35]\n"
"buffer_load_dwordx4 v240, s[28:31], s40 offen offset:2048 lds\n"
"v_mfma_f32_16x16x32_bf16 v[64:67], v[112:115], a[244:247], v[64:67]\n"
"v_mul_f32_dpp v75, v206, v75 row_newbcast:3 row_mask:0xf bank_mask:0xf\n"
"v_mul_f32_dpp v61, v247, v61 row_newbcast:1 row_mask:0xf bank_mask:0xf\n"
"v_mfma_f32_16x16x32_bf16 v[64:67], v[116:119], a[248:251], v[64:67]\n"
"s_waitcnt lgkmcnt(1)\n"
"ds_read_b64_tr_b16 a[244:245], v232 offset:49664\n"
"ds_read_b64_tr_b16 a[246:247], v232 offset:53760\n"
"v_mov_b32 v253, v202\n"
"v_sub_u32 v228, v232, v254\n"
"v_mul_f32_dpp v9, v246, v9 row_newbcast:1 row_mask:0xf bank_mask:0xf\n"
"v_mul_f32_dpp v11, v246, v11 row_newbcast:3 row_mask:0xf bank_mask:0xf\n"
"v_mul_f32_dpp v15, v246, v15 row_newbcast:3 row_mask:0xf bank_mask:0xf\n"
"v_mul_f32_dpp v47, v247, v47 row_newbcast:3 row_mask:0xf bank_mask:0xf\n"
"v_cvt_pk_bf16_f32 v40, v40, v41\n"
"v_cvt_pk_bf16_f32 v72, v72, v73\n"
"v_mfma_f32_16x16x32_bf16 v[68:71], v[116:119], v[224:227], v[68:71]\n"
"s_add_u32 s40, s41, s54\n"
"ds_read_b64_tr_b16 a[248:249], v236 offset:49664\n"
"ds_read_b64_tr_b16 a[250:251], v236 offset:53760\n"
"v_mfma_f32_16x16x32_bf16 v[68:71], v[112:115], a[252:255], v[68:71]\n"
"buffer_load_dwordx4 v240, s[28:31], s40 offen offset:3072 lds\n"
"v_readlane_b32 s23, v252, 63\n"
"v_mul_f32_dpp v43, v247, v43 row_newbcast:3 row_mask:0xf bank_mask:0xf\n"
"v_mul_f32_dpp v24, v246, v24 row_newbcast:0 row_mask:0xf bank_mask:0xf\n"
"v_mul_f32_dpp v29, v246, v29 row_newbcast:1 row_mask:0xf bank_mask:0xf\n"
"v_mul_f32_dpp v56, v247, v56 row_newbcast:0 row_mask:0xf bank_mask:0xf\n"
"v_mul_f32_dpp v1, v246, v1 row_newbcast:1 row_mask:0xf bank_mask:0xf\n"
"v_accvgpr_read_b32 v255, a209\n"
"v_xor_b32 v232, 0x1000, v228\n"
"v_mfma_f32_16x16x32_bf16 v[36:39], v[108:111], v[224:227], v[36:39]\n"
"v_mov_b32 v252, v192\n"
"v_mul_f32_dpp v44, v247, v44 row_newbcast:0 row_mask:0xf bank_mask:0xf\n"
"v_mfma_f32_16x16x32_bf16 v[36:39], v[104:107], a[252:255], v[36:39]\n"
"ds_write_b128 v244, a[236:239] offset:35840\n"
"v_mul_f32_dpp v13, v246, v13 row_newbcast:1 row_mask:0xf bank_mask:0xf\n"
"v_mul_f32_dpp v42, v247, v42 row_newbcast:2 row_mask:0xf bank_mask:0xf\n"
"v_cvt_pk_bf16_f32 v41, v42, v43\n"
"v_cvt_pk_bf16_f32 v42, v44, v45\n"
"v_cvt_pk_bf16_f32 v56, v56, v57\n"
"v_mfma_f32_16x16x32_bf16 v[4:7], v[100:103], v[224:227], v[4:7]\n"
"ds_read_b64_tr_b16 v[224:225], v237 offset:49664\n"
"ds_read_b64_tr_b16 v[226:227], v237 offset:53760\n"
"v_mfma_f32_16x16x32_bf16 v[4:7], v[96:99], a[252:255], v[4:7]\n"
"ds_write_b64 v250, v[252:253] offset:52224\n"
"v_cvt_pk_bf16_f32 v8, v8, v9\n"
"v_cvt_pk_bf16_f32 v43, v46, v47\n"
"v_cvt_pk_bf16_f32 v73, v74, v75\n"
"v_mul_f32_dpp v28, v246, v28 row_newbcast:0 row_mask:0xf bank_mask:0xf\n"
"v_cvt_pk_bf16_f32 v57, v58, v59\n"
"v_mul_f32_dpp v0, v246, v0 row_newbcast:0 row_mask:0xf bank_mask:0xf\n"
"v_mul_f32_dpp v33, v247, v33 row_newbcast:1 row_mask:0xf bank_mask:0xf\n"
"v_mfma_f32_16x16x32_bf16 a[212:215], v[120:123], a[252:255], a[212:215]\n"
"s_waitcnt lgkmcnt(6)\n"
"ds_read_b64_tr_b16 a[252:253], v233 offset:49664\n"
"ds_read_b64_tr_b16 a[254:255], v233 offset:53760\n"
"v_cvt_pk_bf16_f32 v9, v10, v11\n"
"v_cvt_pk_bf16_f32 v74, v76, v77\n"
"v_mul_f32_dpp v60, v247, v60 row_newbcast:0 row_mask:0xf bank_mask:0xf\n"
"v_mul_f32_dpp v62, v247, v62 row_newbcast:2 row_mask:0xf bank_mask:0xf\n"
"v_mul_f32_dpp v88, v206, v88 row_newbcast:0 row_mask:0xf bank_mask:0xf\n"
"v_mul_f32_dpp v64, v206, v64 row_newbcast:0 row_mask:0xf bank_mask:0xf\n"
"v_mul_f32_dpp v66, v206, v66 row_newbcast:2 row_mask:0xf bank_mask:0xf\n"
"v_mfma_f32_16x16x32_bf16 a[224:227], v[120:123], a[244:247], a[224:227]\n"
"ds_write_b128 v248, v[40:43] offset:50176\n"
"v_sub_u32 v229, v233, v254\n"
"v_cvt_pk_bf16_f32 v11, v14, v15\n"
"v_mul_f32_dpp v27, v246, v27 row_newbcast:3 row_mask:0xf bank_mask:0xf\n"
"v_mul_f32_dpp v3, v246, v3 row_newbcast:3 row_mask:0xf bank_mask:0xf\n"
"v_mul_f32_dpp v68, v206, v68 row_newbcast:0 row_mask:0xf bank_mask:0xf\n"
"v_mfma_f32_16x16x32_bf16 v[16:19], v[96:99], a[244:247], v[16:19]\n"
"s_waitcnt vmcnt(8) lgkmcnt(1)\n"
"v_mul_f32_dpp v89, v206, v89 row_newbcast:1 row_mask:0xf bank_mask:0xf\n"
"v_add_u32 v236, s76, v249\n"
"v_mfma_f32_16x16x32_bf16 v[16:19], v[100:103], a[248:251], v[16:19]\n"
"s_barrier\n"
"ds_read_b64_tr_b16 a[22:23], v249 offset:50184\n"
"ds_read_b64_tr_b16 v[182:183], v228 offset:2048\n"
"ds_read_b64_tr_b16 v[216:217], v228 offset:24576\n"
"ds_read_b64_tr_b16 v[218:219], v228 offset:26624\n"
"ds_read_b64_tr_b16 a[202:203], v236 offset:2056\n"
"v_mul_f32_dpp v63, v247, v63 row_newbcast:3 row_mask:0xf bank_mask:0xf\n"
"v_mul_f32_dpp v91, v206, v91 row_newbcast:3 row_mask:0xf bank_mask:0xf\n"
"v_mul_f32_dpp v2, v246, v2 row_newbcast:2 row_mask:0xf bank_mask:0xf\n"
"v_mfma_f32_16x16x32_bf16 v[48:51], v[104:107], a[244:247], v[48:51]\n"
"ds_read_b64_tr_b16 v[192:193], v232 offset:8192\n"
"ds_read_b64_tr_b16 a[240:241], v236 offset:4096\n"
"v_mfma_f32_16x16x32_bf16 v[48:51], v[108:111], a[248:251], v[48:51]\n"
"ds_read_b64 v[250:251], v245 offset:52224\n"
"ds_read_b64_tr_b16 v[186:187], v232 offset:2048\n"
"ds_read_b64_tr_b16 v[194:195], v232 offset:10240\n"
"ds_read_b64_tr_b16 v[208:209], v228 offset:16384\n"
"v_cvt_pk_bf16_f32 v10, v12, v13\n"
"v_mul_f32_dpp v79, v206, v79 row_newbcast:3 row_mask:0xf bank_mask:0xf\n"
"v_cvt_pk_bf16_f32 v75, v78, v79\n"
"v_cvt_pk_bf16_f32 v88, v88, v89\n"
"v_cvt_pk_bf16_f32 v89, v90, v91\n"
"v_cvt_pk_bf16_f32 v0, v0, v1\n"
"v_mfma_f32_16x16x32_bf16 v[80:83], v[112:115], a[244:247], v[80:83]\n"
"ds_read_b64_tr_b16 v[180:181], v228 offset:0\n"
"ds_read_b64_tr_b16 v[188:189], v228 offset:8192\n"
"v_mfma_f32_16x16x32_bf16 v[80:83], v[116:119], a[248:251], v[80:83]\n"
"ds_write_b128 v248, v[72:75] offset:51200\n"
"v_mul_f32_dpp v30, v246, v30 row_newbcast:2 row_mask:0xf bank_mask:0xf\n"
"v_cvt_pk_bf16_f32 v24, v24, v25\n"
"v_cvt_pk_bf16_f32 v58, v60, v61\n"
"v_mul_f32_dpp v93, v206, v93 row_newbcast:1 row_mask:0xf bank_mask:0xf\n"
"v_mul_f32_dpp v36, v247, v36 row_newbcast:0 row_mask:0xf bank_mask:0xf\n"
"v_mfma_f32_16x16x32_bf16 v[84:87], v[116:119], v[224:227], v[84:87]\n"
"ds_write_b128 v248, v[8:11] offset:49152\n"
"v_mul_f32_dpp v26, v246, v26 row_newbcast:2 row_mask:0xf bank_mask:0xf\n"
"v_mul_f32_dpp v32, v247, v32 row_newbcast:0 row_mask:0xf bank_mask:0xf\n"
"v_mul_f32_dpp v65, v206, v65 row_newbcast:1 row_mask:0xf bank_mask:0xf\n"
"v_mul_f32_dpp v69, v206, v69 row_newbcast:1 row_mask:0xf bank_mask:0xf\n"
"v_xor_b32 v233, 0x1000, v229\n"
"v_mfma_f32_16x16x32_bf16 v[84:87], v[112:115], a[252:255], v[84:87]\n"
"ds_read_b64_tr_b16 a[6:7], v249 offset:49160\n"
"ds_read_b64_tr_b16 a[36:37], v249 offset:51200\n"
"ds_read_b64_tr_b16 v[210:211], v228 offset:18432\n"
"ds_read_b64_tr_b16 v[222:223], v232 offset:26624\n"
"v_mul_f32_dpp v31, v246, v31 row_newbcast:3 row_mask:0xf bank_mask:0xf\n"
"v_cvt_pk_bf16_f32 v25, v26, v27\n"
"v_cvt_pk_bf16_f32 v59, v62, v63\n"
"v_mul_f32_dpp v34, v247, v34 row_newbcast:2 row_mask:0xf bank_mask:0xf\n"
"v_mov_b64 v[196:197], s[22:23]\n"
"v_xor_b32 v237, 0x400, v236\n"
"v_mfma_f32_16x16x32_bf16 v[52:55], v[108:111], v[224:227], v[52:55]\n"
"ds_read_b64_tr_b16 v[184:185], v232 offset:0\n"
"ds_read_b64_tr_b16 v[212:213], v232 offset:16384\n"
"v_mfma_f32_16x16x32_bf16 v[52:55], v[104:107], a[252:255], v[52:55]\n"
"ds_read_b64_tr_b16 a[4:5], v249 offset:49152\n"
"ds_read_b64_tr_b16 a[20:21], v249 offset:50176\n"
"ds_read_b64_tr_b16 a[38:39], v249 offset:51208\n"
"ds_read_b64_tr_b16 v[220:221], v232 offset:24576\n"
"ds_read_b64_tr_b16 a[246:247], v237 offset:4104\n"
"v_cvt_pk_bf16_f32 v26, v28, v29\n"
"v_mul_f32_dpp v95, v206, v95 row_newbcast:3 row_mask:0xf bank_mask:0xf\n"
"v_cvt_pk_bf16_f32 v91, v94, v95\n"
"v_mul_f32_dpp v5, v246, v5 row_newbcast:1 row_mask:0xf bank_mask:0xf\n"
"v_mfma_f32_16x16x32_bf16 v[20:23], v[100:103], v[224:227], v[20:23]\n"
"ds_read_b64_tr_b16 v[190:191], v228 offset:10240\n"
"ds_read_b64_tr_b16 v[214:215], v232 offset:18432\n"
"v_mfma_f32_16x16x32_bf16 v[20:23], v[96:99], a[252:255], v[20:23]\n"
"ds_write_b128 v248, v[56:59] offset:50176\n"
"v_cvt_pk_bf16_f32 v27, v30, v31\n"
"v_mul_f32_dpp v4, v246, v4 row_newbcast:0 row_mask:0xf bank_mask:0xf\n"
"v_mul_f32_dpp v35, v247, v35 row_newbcast:3 row_mask:0xf bank_mask:0xf\n"
"v_cvt_pk_bf16_f32 v64, v64, v65\n"
"v_mov_b64 v[198:199], s[22:23]\n"
"v_mfma_f32_16x16x32_bf16 a[228:231], v[120:123], a[252:255], a[228:231]\n"
"s_waitcnt lgkmcnt(15)\n"
"ds_write_b128 v248, v[24:27] offset:49152\n"
"v_cvt_pk_bf16_f32 v90, v92, v93\n"
"v_mul_f32_dpp v6, v246, v6 row_newbcast:2 row_mask:0xf bank_mask:0xf\n"
"v_cvt_pk_bf16_f32 v1, v2, v3\n"
"v_cvt_pk_bf16_f32 v2, v4, v5\n"
"v_mul_f32_dpp v71, v206, v71 row_newbcast:3 row_mask:0xf bank_mask:0xf\n"
"v_mfma_f32_16x16x32_bf16 v[124:127], v[180:183], a[96:99], v[196:199]\n"
"s_waitcnt lgkmcnt(10)\n"
"ds_read_b64_tr_b16 a[14:15], v249 offset:49160\n"
"ds_read_b64_tr_b16 a[30:31], v249 offset:50184\n"
"v_mfma_f32_16x16x32_bf16 v[124:127], v[184:187], a[100:103], v[124:127]\n"
"s_waitcnt lgkmcnt(5)\n"
"ds_write_b128 v248, v[88:91] offset:51200\n"
"v_mul_f32_dpp v7, v246, v7 row_newbcast:3 row_mask:0xf bank_mask:0xf\n"
"v_cvt_pk_bf16_f32 v3, v6, v7\n"
"v_mul_f32_dpp v37, v247, v37 row_newbcast:1 row_mask:0xf bank_mask:0xf\n"
"v_cvt_pk_bf16_f32 v32, v32, v33\n"
"v_mul_f32_dpp v67, v206, v67 row_newbcast:3 row_mask:0xf bank_mask:0xf\n"
"v_mfma_f32_16x16x32_bf16 v[124:127], v[188:191], a[104:107], v[124:127]\n"
"ds_read_b64_tr_b16 a[12:13], v249 offset:49152\n"
"ds_read_b64_tr_b16 a[44:45], v249 offset:51200\n"
"ds_read_b64_tr_b16 a[46:47], v249 offset:51208\n"
"ds_read_b64_tr_b16 v[100:101], v233 offset:0\n"
"ds_read_b64_tr_b16 v[120:121], v229 offset:24576\n"
"v_mul_f32_dpp v38, v247, v38 row_newbcast:2 row_mask:0xf bank_mask:0xf\n"
"v_mul_f32_dpp v70, v206, v70 row_newbcast:2 row_mask:0xf bank_mask:0xf\n"
"v_cvt_pk_bf16_f32 v65, v66, v67\n"
"v_cvt_pk_bf16_f32 v66, v68, v69\n"
"v_cvt_pk_bf16_f32 v67, v70, v71\n"
"v_mfma_f32_16x16x32_bf16 v[124:127], v[192:195], a[108:111], v[124:127]\n"
"ds_write_b128 v248, v[0:3] offset:49152\n"
"v_mul_f32_dpp v39, v247, v39 row_newbcast:3 row_mask:0xf bank_mask:0xf\n"
"v_cvt_pk_bf16_f32 v33, v34, v35\n"
"v_cvt_pk_bf16_f32 v34, v36, v37\n"
"v_cvt_pk_bf16_f32 v35, v38, v39\n"
"v_xor_b32 v244, 0x1000, v244\n"
"v_mfma_f32_16x16x32_bf16 v[124:127], v[208:211], a[144:147], v[124:127]\n"
"s_waitcnt lgkmcnt(11)\n"
"ds_read_b64_tr_b16 a[28:29], v249 offset:50176\n"
"ds_read_b128 v[24:27], v244 offset:32768\n"
"v_mfma_f32_16x16x32_bf16 v[124:127], v[212:215], a[148:151], v[124:127]\n"
"ds_write_b128 v248, v[32:35] offset:50176\n"
"ds_write_b128 v248, v[64:67] offset:51200\n"
"v_mfma_f32_16x16x32_bf16 v[124:127], v[216:219], a[152:155], v[124:127]\n"
"ds_read_b64_tr_b16 a[0:1], v249 offset:49152\n"
"ds_read_b64_tr_b16 a[32:33], v249 offset:51200\n"
"v_mfma_f32_16x16x32_bf16 v[124:127], v[220:223], a[156:159], v[124:127]\n"
"ds_read_b64_tr_b16 a[16:17], v249 offset:50176\n"
"ds_read_b64_tr_b16 a[18:19], v249 offset:50184\n"
"ds_read_b128 v[56:59], v244 offset:34816\n"
"ds_read_b64_tr_b16 a[196:197], v237 offset:0\n"
"v_sub_u32 v231, v235, v254\n"
"v_mfma_f32_16x16x32_bf16 v[140:143], v[220:223], a[172:175], v[196:199]\n"
"ds_read_b64_tr_b16 a[2:3], v249 offset:49160\n"
"ds_read_b64_tr_b16 a[34:35], v249 offset:51208\n"
"v_mfma_f32_16x16x32_bf16 v[140:143], v[216:219], a[168:171], v[140:143]\n"
"ds_read_b128 v[28:31], v244 offset:33792\n"
"ds_read_b128 v[60:63], v244 offset:35840\n"
"v_mfma_f32_16x16x32_bf16 v[140:143], v[212:215], a[164:167], v[140:143]\n"
"ds_read_b64_tr_b16 a[200:201], v236 offset:2048\n"
"ds_read_b64_tr_b16 a[254:255], v237 offset:6152\n"
"v_mfma_f32_16x16x32_bf16 v[140:143], v[208:211], a[160:163], v[140:143]\n"
"ds_read_b64_tr_b16 a[206:207], v237 offset:2056\n"
"ds_read_b64_tr_b16 a[242:243], v236 offset:4104\n"
"v_mfma_f32_16x16x32_bf16 v[140:143], v[192:195], a[124:127], v[140:143]\n"
"ds_read_b64_tr_b16 a[244:245], v237 offset:4096\n"
"v_xor_b32 v234, 0x1000, v230\n"
"v_mfma_f32_16x16x32_bf16 v[140:143], v[188:191], a[120:123], v[140:143]\n"
"ds_read_b64_tr_b16 v[110:111], v233 offset:10240\n"
"ds_read_b64_tr_b16 a[250:251], v236 offset:6152\n"
"v_mfma_f32_16x16x32_bf16 v[140:143], v[184:187], a[116:119], v[140:143]\n"
"ds_read_b64_tr_b16 a[204:205], v237 offset:2048\n"
"ds_read_b64_tr_b16 v[102:103], v233 offset:2048\n"
"v_mfma_f32_16x16x32_bf16 v[140:143], v[180:183], a[112:115], v[140:143]\n"
"ds_read_b64_tr_b16 a[248:249], v236 offset:6144\n"
"ds_read_b64_tr_b16 a[252:253], v237 offset:6144\n"
"v_mfma_f32_16x16x32_bf16 v[156:159], v[180:183], a[128:131], v[196:199]\n"
"ds_read_b64_tr_b16 v[98:99], v229 offset:2048\n"
"ds_read_b64_tr_b16 v[116:117], v233 offset:16384\n"
"v_mfma_f32_16x16x32_bf16 v[156:159], v[184:187], a[132:135], v[156:159]\n"
"ds_read_b64_tr_b16 v[122:123], v229 offset:26624\n"
"ds_read_b64_tr_b16 v[224:225], v233 offset:24576\n"
"v_mfma_f32_16x16x32_bf16 v[156:159], v[188:191], a[136:139], v[156:159]\n"
"ds_read_b64_tr_b16 a[198:199], v237 offset:8\n"
"ds_read_b64_tr_b16 v[96:97], v229 offset:0\n"
"v_mfma_f32_16x16x32_bf16 v[156:159], v[192:195], a[140:143], v[156:159]\n"
"s_waitcnt lgkmcnt(13)\n"
"ds_read_b64_tr_b16 v[108:109], v233 offset:8192\n"
"v_mul_f32_dpp v23, v246, v23 row_newbcast:3 row_mask:0xf bank_mask:0xf\n"
"v_mfma_f32_16x16x32_bf16 v[156:159], v[208:211], a[176:179], v[156:159]\n"
"ds_read_b64_tr_b16 v[106:107], v229 offset:10240\n"
"ds_read_b64_tr_b16 v[112:113], v229 offset:16384\n"
"v_mfma_f32_16x16x32_bf16 v[156:159], v[212:215], a[180:183], v[156:159]\n"
"s_add_u32 s41, s95, 491520\n"
"ds_read_b64_tr_b16 a[192:193], v236 offset:0\n"
"ds_read_b64_tr_b16 v[118:119], v233 offset:18432\n"
"v_mfma_f32_16x16x32_bf16 v[156:159], v[216:219], a[184:187], v[156:159]\n"
"s_add_u32 m0, s44, 49152\n"
"ds_read_b64_tr_b16 a[194:195], v236 offset:8\n"
"ds_read_b64_tr_b16 v[104:105], v229 offset:8192\n"
"v_mfma_f32_16x16x32_bf16 v[156:159], v[220:223], a[188:191], v[156:159]\n"
"s_waitcnt lgkmcnt(13)\n"
"buffer_load_dwordx4 v240, s[28:31], s41 offen lds\n"
"v_mfma_f32_16x16x32_bf16 v[172:175], v[220:223], a[252:255], v[196:199]\n"
"s_add_u32 s40, s41, s52\n"
"v_min_f32 v254, v252, v250\n"
"v_sub_f32 v250, v254, v250\n"
"v_mfma_f32_16x16x32_bf16 v[172:175], v[216:219], a[248:251], v[172:175]\n"
"buffer_load_dwordx4 v240, s[28:31], s40 offen offset:1024 lds\n"
"v_mfma_f32_16x16x32_bf16 v[172:175], v[212:215], a[244:247], v[172:175]\n"
"s_add_u32 s40, s41, s53\n"
"ds_read_b64_tr_b16 v[114:115], v229 offset:18432\n"
"ds_read_b64_tr_b16 v[218:219], v230 offset:26624\n"
"v_mfma_f32_16x16x32_bf16 v[172:175], v[208:211], a[240:243], v[172:175]\n"
"buffer_load_dwordx4 v240, s[28:31], s40 offen offset:2048 lds\n"
"v_mfma_f32_16x16x32_bf16 v[172:175], v[192:195], a[204:207], v[172:175]\n"
"ds_read_b64_tr_b16 v[226:227], v233 offset:26624\n"
"ds_read_b64_tr_b16 a[232:233], v236 offset:10240\n"
"v_mfma_f32_16x16x32_bf16 v[172:175], v[188:191], a[200:203], v[172:175]\n"
"s_waitcnt lgkmcnt(6)\n"
"v_xor_b32 v235, 0x1000, v231\n"
"v_mul_f32_dpp v17, v246, v17 row_newbcast:1 row_mask:0xf bank_mask:0xf\n"
"v_mfma_f32_16x16x32_bf16 v[172:175], v[184:187], a[196:199], v[172:175]\n"
"s_waitcnt lgkmcnt(4)\n"
"s_add_u32 s40, s41, s54\n"
"ds_read_b64_tr_b16 v[222:223], v234 offset:26624\n"
"v_mul_f32_dpp v18, v246, v18 row_newbcast:2 row_mask:0xf bank_mask:0xf\n"
"v_mfma_f32_16x16x32_bf16 v[172:175], v[180:183], a[192:195], v[172:175]\n"
"buffer_load_dwordx4 v240, s[28:31], s40 offen offset:3072 lds\n"
"v_mfma_f32_16x16x32_bf16 v[128:131], v[96:99], a[96:99], v[196:199]\n"
"v_sub_f32 v252, v254, v252\n"
"v_mul_f32_dpp v16, v246, v16 row_newbcast:0 row_mask:0xf bank_mask:0xf\n"
"v_mfma_f32_16x16x32_bf16 v[128:131], v[100:103], a[100:103], v[128:131]\n"
"ds_read_b64_tr_b16 v[190:191], v230 offset:10240\n"
"v_mul_f32_dpp v86, v206, v86 row_newbcast:2 row_mask:0xf bank_mask:0xf\n"
"v_mfma_f32_16x16x32_bf16 v[128:131], v[104:107], a[104:107], v[128:131]\n"
"v_exp_f32 v252, v252\n"
"v_mfma_f32_16x16x32_bf16 v[128:131], v[108:111], a[108:111], v[128:131]\n"
"s_waitcnt lgkmcnt(4)\n"
"v_exp_f32 v250, v250\n"
"v_mfma_f32_16x16x32_bf16 v[128:131], v[112:115], a[144:147], v[128:131]\n"
"s_add_u32 s21, s60, 32\n"
"ds_read_b64_tr_b16 a[220:221], v237 offset:8192\n"
"v_mul_f32 v253, v253, v252\n"
"v_mfma_f32_16x16x32_bf16 v[128:131], v[116:119], a[148:151], v[128:131]\n"
"s_mov_b32 s69, 1\n"
"ds_read_b64_tr_b16 v[186:187], v234 offset:2048\n"
"v_fmac_f32 v253, v251, v250\n"
"v_mfma_f32_16x16x32_bf16 v[128:131], v[120:123], a[152:155], v[128:131]\n"
"s_waitcnt lgkmcnt(3)\n"
"v_bfe_u32 v201, v242, 2, 7\n"
"v_add_u32 v201, 0x100, v201\n"
"ds_read_b64_tr_b16 v[184:185], v234 offset:0\n"
"v_mfma_f32_16x16x32_bf16 v[128:131], v[224:227], a[156:159], v[128:131]\n"
"global_load_dword v200, v201, s[6:7] sc1\n"
"v_mfma_f32_16x16x32_bf16 v[144:147], v[224:227], a[172:175], v[196:199]\n"
"v_rcp_f32 v253, v253\n"
"v_mfma_f32_16x16x32_bf16 v[144:147], v[120:123], a[168:171], v[144:147]\n"
"v_mul_f32 v252, v252, v253\n"
"v_accvgpr_read_b32 v254, a208\n"
"v_mfma_f32_16x16x32_bf16 v[144:147], v[116:119], a[164:167], v[144:147]\n"
"ds_read_b64_tr_b16 v[188:189], v230 offset:8192\n"
"v_mul_f32 v250, v250, v253\n"
"v_mfma_f32_16x16x32_bf16 v[144:147], v[112:115], a[160:163], v[144:147]\n"
"s_lshl_b32 s20, s95, 9\n"
"ds_read_b64_tr_b16 v[194:195], v234 offset:10240\n"
"v_mov_b32_dpp v250, v250 row_ror:12 row_mask:0x2 bank_mask:0xf\n"
"v_mfma_f32_16x16x32_bf16 v[144:147], v[108:111], a[124:127], v[144:147]\n"
"s_lshl_b32 s19, s61, 7\n"
"v_mov_b32_dpp v250, v250 row_ror:8 row_mask:0x4 bank_mask:0xf\n"
"v_accvgpr_read_b32 v253, a210\n"
"v_mfma_f32_16x16x32_bf16 v[144:147], v[104:107], a[120:123], v[144:147]\n"
"s_lshl_b32 s91, s19, 9\n"
"v_mov_b32_dpp v252, v252 row_ror:12 row_mask:0x2 bank_mask:0xf\n"
"v_mov_b32_dpp v250, v250 row_ror:4 row_mask:0x8 bank_mask:0xf\n"
"v_mfma_f32_16x16x32_bf16 v[144:147], v[100:103], a[116:119], v[144:147]\n"
"ds_read_b64_tr_b16 v[214:215], v234 offset:18432\n"
"v_mul_f32_dpp v63, v250, v63 row_newbcast:3 row_mask:0xf bank_mask:0xf\n"
"v_mfma_f32_16x16x32_bf16 v[144:147], v[96:99], a[112:115], v[144:147]\n"
"ds_read_b64_tr_b16 v[208:209], v230 offset:16384\n"
"v_mul_f32_dpp v83, v206, v83 row_newbcast:3 row_mask:0xf bank_mask:0xf\n"
"v_mfma_f32_16x16x32_bf16 v[160:163], v[96:99], a[128:131], v[196:199]\n"
"ds_read_b64_tr_b16 v[192:193], v234 offset:8192\n"
"ds_read_b64_tr_b16 v[212:213], v234 offset:16384\n"
"v_mfma_f32_16x16x32_bf16 v[160:163], v[100:103], a[132:135], v[160:163]\n"
"v_accvgpr_read_b32 v251, a211\n"
"v_mul_f32_dpp v57, v250, v57 row_newbcast:1 row_mask:0xf bank_mask:0xf\n"
"v_mfma_f32_16x16x32_bf16 v[160:163], v[104:107], a[136:139], v[160:163]\n"
"ds_read_b64_tr_b16 v[182:183], v230 offset:2048\n"
"v_mul_f32_dpp v58, v250, v58 row_newbcast:2 row_mask:0xf bank_mask:0xf\n"
"v_mfma_f32_16x16x32_bf16 v[160:163], v[108:111], a[140:143], v[160:163]\n"
"ds_read_b64_tr_b16 v[180:181], v230 offset:0\n"
"v_mul_f32_dpp v31, v250, v31 row_newbcast:3 row_mask:0xf bank_mask:0xf\n"
"v_mfma_f32_16x16x32_bf16 v[160:163], v[112:115], a[176:179], v[160:163]\n"
"ds_read_b64_tr_b16 v[210:211], v230 offset:18432\n"
"v_mov_b32_dpp v252, v252 row_ror:8 row_mask:0x4 bank_mask:0xf\n"
"v_mfma_f32_16x16x32_bf16 v[160:163], v[116:119], a[180:183], v[160:163]\n"
"ds_read_b64_tr_b16 v[220:221], v234 offset:24576\n"
"v_mul_f32_dpp v26, v250, v26 row_newbcast:2 row_mask:0xf bank_mask:0xf\n"
"v_mfma_f32_16x16x32_bf16 v[160:163], v[120:123], a[184:187], v[160:163]\n"
"ds_read_b64_tr_b16 a[238:239], v237 offset:10248\n"
"v_mul_f32_dpp v30, v250, v30 row_newbcast:2 row_mask:0xf bank_mask:0xf\n"
"v_mfma_f32_16x16x32_bf16 v[160:163], v[224:227], a[188:191], v[160:163]\n"
"ds_read_b64_tr_b16 v[216:217], v230 offset:24576\n"
"v_mul_f32_dpp v28, v250, v28 row_newbcast:0 row_mask:0xf bank_mask:0xf\n"
"v_mfma_f32_16x16x32_bf16 v[176:179], v[224:227], a[252:255], v[196:199]\n"
"v_mov_b32_dpp v252, v252 row_ror:4 row_mask:0x8 bank_mask:0xf\n"
"v_mul_f32_dpp v61, v250, v61 row_newbcast:1 row_mask:0xf bank_mask:0xf\n"
"v_mfma_f32_16x16x32_bf16 v[176:179], v[120:123], a[248:251], v[176:179]\n"
"ds_read_b64_tr_b16 v[226:227], v235 offset:26624\n"
"v_mul_f32_dpp v59, v250, v59 row_newbcast:3 row_mask:0xf bank_mask:0xf\n"
"v_mfma_f32_16x16x32_bf16 v[176:179], v[116:119], a[244:247], v[176:179]\n"
"v_mul_f32_dpp v25, v250, v25 row_newbcast:1 row_mask:0xf bank_mask:0xf\n"
"v_mul_f32_dpp v29, v250, v29 row_newbcast:1 row_mask:0xf bank_mask:0xf\n"
"v_mfma_f32_16x16x32_bf16 v[176:179], v[112:115], a[240:243], v[176:179]\n"
"ds_read_b64_tr_b16 v[114:115], v231 offset:18432\n"
"v_mul_f32_dpp v19, v246, v19 row_newbcast:3 row_mask:0xf bank_mask:0xf\n"
"v_mfma_f32_16x16x32_bf16 v[176:179], v[108:111], a[204:207], v[176:179]\n"
"ds_read_b64_tr_b16 a[236:237], v237 offset:10240\n"
"v_mul_f32_dpp v27, v250, v27 row_newbcast:3 row_mask:0xf bank_mask:0xf\n"
"v_mfma_f32_16x16x32_bf16 v[176:179], v[104:107], a[200:203], v[176:179]\n"
"v_mul_f32_dpp v24, v250, v24 row_newbcast:0 row_mask:0xf bank_mask:0xf\n"
"v_fmac_f32_dpp v25, v252, v255 row_newbcast:1 row_mask:0xf bank_mask:0xf\n"
"v_mfma_f32_16x16x32_bf16 v[176:179], v[100:103], a[196:199], v[176:179]\n"
"ds_read_b64_tr_b16 a[234:235], v236 offset:10248\n"
"v_fmac_f32_dpp v27, v252, v251 row_newbcast:3 row_mask:0xf bank_mask:0xf\n"
"v_mfma_f32_16x16x32_bf16 v[176:179], v[96:99], a[192:195], v[176:179]\n"
"s_waitcnt lgkmcnt(4)\n"
"ds_read_b64_tr_b16 v[104:105], v231 offset:8192\n"
"ds_read_b64_tr_b16 v[106:107], v231 offset:10240\n"
"v_accvgpr_read_b32 v251, a215\n"
"v_mul_f32_dpp v55, v247, v55 row_newbcast:3 row_mask:0xf bank_mask:0xf\n"
"v_mul_f32_dpp v56, v250, v56 row_newbcast:0 row_mask:0xf bank_mask:0xf\n"
"v_mul_f32_dpp v62, v250, v62 row_newbcast:2 row_mask:0xf bank_mask:0xf\n"
"v_mfma_f32_16x16x32_bf16 v[132:135], v[180:183], a[96:99], v[196:199]\n"
"v_accvgpr_read_b32 v255, a213\n"
"v_fmac_f32_dpp v31, v252, v251 row_newbcast:3 row_mask:0xf bank_mask:0xf\n"
"v_mfma_f32_16x16x32_bf16 v[132:135], v[184:187], a[100:103], v[132:135]\n"
"ds_read_b64_tr_b16 v[108:109], v235 offset:8192\n"
"ds_read_b64_tr_b16 v[116:117], v235 offset:16384\n"
"v_mfma_f32_16x16x32_bf16 v[132:135], v[188:191], a[104:107], v[132:135]\n"
"ds_read_b64_tr_b16 v[102:103], v235 offset:2048\n"
"v_fmac_f32_dpp v26, v252, v253 row_newbcast:2 row_mask:0xf bank_mask:0xf\n"
"v_mfma_f32_16x16x32_bf16 v[132:135], v[192:195], a[108:111], v[132:135]\n"
"v_accvgpr_read_b32 v253, a214\n"
"v_mul_f32_dpp v60, v250, v60 row_newbcast:0 row_mask:0xf bank_mask:0xf\n"
"v_mfma_f32_16x16x32_bf16 v[132:135], v[208:211], a[144:147], v[132:135]\n"
"s_waitcnt vmcnt(0)\n"
"ds_read_b64_tr_b16 v[96:97], v231 offset:0\n"
"v_fmac_f32_dpp v24, v252, v254 row_newbcast:0 row_mask:0xf bank_mask:0xf\n"
"v_mfma_f32_16x16x32_bf16 v[132:135], v[212:215], a[148:151], v[132:135]\n"
"v_subrev_u32 v200, s21, v200\n"
"v_accvgpr_read_b32 v254, a212\n"
"v_mfma_f32_16x16x32_bf16 v[132:135], v[216:219], a[152:155], v[132:135]\n"
"v_cmp_lt_u32 vcc, s69, v200\n"
"v_accvgpr_read_b32 v251, a227\n"
"v_mfma_f32_16x16x32_bf16 v[132:135], v[220:223], a[156:159], v[132:135]\n"
"ds_read_b64_tr_b16 a[216:217], v236 offset:8192\n"
"ds_read_b64_tr_b16 v[118:119], v235 offset:18432\n"
"v_fmac_f32_dpp v28, v252, v254 row_newbcast:0 row_mask:0xf bank_mask:0xf\n"
"v_fmac_f32_dpp v29, v252, v255 row_newbcast:1 row_mask:0xf bank_mask:0xf\n"
"v_fmac_f32_dpp v30, v252, v253 row_newbcast:2 row_mask:0xf bank_mask:0xf\n"
"v_cvt_pk_bf16_f32 v24, v24, v25\n"
"v_cvt_pk_bf16_f32 v25, v26, v27\n"
"v_cvt_pk_bf16_f32 v26, v28, v29\n"
"v_cvt_pk_bf16_f32 v27, v30, v31\n"
"v_accvgpr_read_b32 v255, a225\n"
"v_accvgpr_read_b32 v253, a226\n"
"v_fmac_f32_dpp v59, v252, v251 row_newbcast:3 row_mask:0xf bank_mask:0xf\n"
"s_cbranch_vccnz LGV_%=\nLGVK_%=:\n"
"v_mfma_f32_16x16x32_bf16 v[148:151], v[220:223], a[172:175], v[196:199]\n"
"v_accvgpr_read_b32 v254, a224\n"
"v_fmac_f32_dpp v58, v252, v253 row_newbcast:2 row_mask:0xf bank_mask:0xf\n"
"v_mfma_f32_16x16x32_bf16 v[148:151], v[216:219], a[168:171], v[148:151]\n"
"v_add_u32_dpp v200, v200, v200 quad_perm:[1,0,3,2] row_mask:0xf bank_mask:0xf\n"
"v_accvgpr_read_b32 v253, a230\n"
"v_mfma_f32_16x16x32_bf16 v[148:151], v[212:215], a[164:167], v[148:151]\n"
"v_fmac_f32_dpp v56, v252, v254 row_newbcast:0 row_mask:0xf bank_mask:0xf\n"
"v_accvgpr_read_b32 v251, a231\n"
"v_mfma_f32_16x16x32_bf16 v[148:151], v[208:211], a[160:163], v[148:151]\n"
"v_add_u32_dpp v200, v200, v200 quad_perm:[2,3,0,1] row_mask:0xf bank_mask:0xf\n"
"ds_write_b128 v243, v[24:27] offset:0\n"
"v_fmac_f32_dpp v57, v252, v255 row_newbcast:1 row_mask:0xf bank_mask:0xf\n"
"v_accvgpr_read_b32 v254, a228\n"
"s_nop 1\n"
"v_fmac_f32_dpp v60, v252, v254 row_newbcast:0 row_mask:0xf bank_mask:0xf\n"
"v_fmac_f32_dpp v62, v252, v253 row_newbcast:2 row_mask:0xf bank_mask:0xf\n"
"v_fmac_f32_dpp v63, v252, v251 row_newbcast:3 row_mask:0xf bank_mask:0xf\n"
"v_cvt_pk_bf16_f32 v56, v56, v57\n"
"v_cvt_pk_bf16_f32 v57, v58, v59\n"
"v_cvt_pk_bf16_f32 v59, v62, v63\n"
"v_mfma_f32_16x16x32_bf16 v[148:151], v[192:195], a[124:127], v[148:151]\n"
"ds_read_b64_tr_b16 v[100:101], v235 offset:0\n"
"v_accvgpr_read_b32 v255, a229\n"
"v_mfma_f32_16x16x32_bf16 v[148:151], v[188:191], a[120:123], v[148:151]\n"
"v_cmp_lt_u32 vcc, 1, v200\n"
"v_fmac_f32_dpp v61, v252, v255 row_newbcast:1 row_mask:0xf bank_mask:0xf\n"
"v_mfma_f32_16x16x32_bf16 v[148:151], v[184:187], a[116:119], v[148:151]\n"
"ds_read_b64_tr_b16 v[98:99], v231 offset:2048\n"
"v_cvt_pk_bf16_f32 v58, v60, v61\n"
"v_mfma_f32_16x16x32_bf16 v[148:151], v[180:183], a[112:115], v[148:151]\n"
"ds_write_b128 v243, v[56:59] offset:2048\n"
"v_mul_f32_dpp v22, v246, v22 row_newbcast:2 row_mask:0xf bank_mask:0xf\n"
"v_mul_f32_dpp v48, v247, v48 row_newbcast:0 row_mask:0xf bank_mask:0xf\n"
"v_mul_f32_dpp v49, v247, v49 row_newbcast:1 row_mask:0xf bank_mask:0xf\n"
"v_mul_f32_dpp v87, v206, v87 row_newbcast:3 row_mask:0xf bank_mask:0xf\n"
"v_mfma_f32_16x16x32_bf16 v[164:167], v[180:183], a[128:131], v[196:199]\n"
"s_mov_b32 s47, 0\n"
"ds_read_b64_tr_b16 v[110:111], v235 offset:10240\n"
"v_bfe_u32 v254, v242, 4, 4\n"
"v_mfma_f32_16x16x32_bf16 v[164:167], v[184:187], a[132:135], v[164:167]\n"
"s_mov_b32 s89, vcc_lo\n"
"v_cvt_pk_bf16_f32 v16, v16, v17\n"
"v_mul_f32_dpp v81, v206, v81 row_newbcast:1 row_mask:0xf bank_mask:0xf\n"
"v_mfma_f32_16x16x32_bf16 v[164:167], v[188:191], a[136:139], v[164:167]\n"
"s_lshr_b32 s90, s89, s93\n"
"ds_read_b64_tr_b16 v[112:113], v231 offset:16384\n"
"v_xor_b32 v249, s75, v249\n"
"v_mfma_f32_16x16x32_bf16 v[164:167], v[192:195], a[140:143], v[164:167]\n"
"s_and_b32 s90, s90, 1\n"
"ds_read_b64_tr_b16 v[122:123], v231 offset:26624\n"
"v_mul_f32_dpp v51, v247, v51 row_newbcast:3 row_mask:0xf bank_mask:0xf\n"
"v_mfma_f32_16x16x32_bf16 v[164:167], v[208:211], a[176:179], v[164:167]\n"
"s_lshl_b32 s16, s61, 7\n"
"v_cvt_pk_bf16_f32 v17, v18, v19\n"
"v_cvt_pk_bf16_f32 v19, v22, v23\n"
"v_mfma_f32_16x16x32_bf16 v[164:167], v[212:215], a[180:183], v[164:167]\n"
"s_add_u32 s90, s90, s16\n"
"ds_read_b64_tr_b16 v[120:121], v231 offset:24576\n"
"ds_read_b64_tr_b16 v[224:225], v235 offset:24576\n"
"v_mfma_f32_16x16x32_bf16 v[164:167], v[216:219], a[184:187], v[164:167]\n"
"s_waitcnt vmcnt(63) lgkmcnt(5)\n"
"s_lshl_b32 s90, s90, 13\n"
"v_mul_f32_dpp v20, v246, v20 row_newbcast:0 row_mask:0xf bank_mask:0xf\n"
"v_mfma_f32_16x16x32_bf16 v[164:167], v[220:223], a[188:191], v[164:167]\n"
"s_barrier\n"
"s_add_u32 s16, s20, s90\n"
"v_mul_f32_dpp v50, v247, v50 row_newbcast:2 row_mask:0xf bank_mask:0xf\n"
"v_mul_f32_dpp v52, v247, v52 row_newbcast:0 row_mask:0xf bank_mask:0xf\n"
"v_cvt_pk_bf16_f32 v48, v48, v49\n"
"v_cvt_pk_bf16_f32 v49, v50, v51\n"
"v_mul_f32_dpp v80, v206, v80 row_newbcast:0 row_mask:0xf bank_mask:0xf\n"
"v_mfma_f32_16x16x32_bf16 v[136:139], v[96:99], a[96:99], v[196:199]\n"
"s_add_u32 s17, s16, 4096\n"
"ds_read_b64_tr_b16 v[180:181], v228 offset:32768\n"
"v_mul_f32_dpp v84, v206, v84 row_newbcast:0 row_mask:0xf bank_mask:0xf\n"
"v_mfma_f32_16x16x32_bf16 v[136:139], v[100:103], a[100:103], v[136:139]\n"
"s_mov_b32 s72, s61\n"
"ds_read_b64_tr_b16 v[182:183], v228 offset:34816\n"
"v_mul_f32_dpp v21, v246, v21 row_newbcast:1 row_mask:0xf bank_mask:0xf\n"
"v_mfma_f32_16x16x32_bf16 v[136:139], v[104:107], a[104:107], v[136:139]\n"
"s_waitcnt lgkmcnt(5)\n"
"s_mov_b32 s21, s60\n"
"ds_read_b64_tr_b16 a[208:209], v236 offset:12288\n"
"v_mul_f32_dpp v82, v206, v82 row_newbcast:2 row_mask:0xf bank_mask:0xf\n"
"v_mfma_f32_16x16x32_bf16 v[136:139], v[108:111], a[108:111], v[136:139]\n"
"s_mov_b32 s18, s77\n"
"v_cvt_pk_bf16_f32 v80, v80, v81\n"
"v_cvt_pk_bf16_f32 v81, v82, v83\n"
"v_mfma_f32_16x16x32_bf16 v[136:139], v[112:115], a[144:147], v[136:139]\n"
"s_mov_b32 s19, s77\n"
"v_mul_f32_dpp v85, v206, v85 row_newbcast:1 row_mask:0xf bank_mask:0xf\n"
"v_cvt_pk_bf16_f32 v83, v86, v87\n"
"v_mfma_f32_16x16x32_bf16 v[136:139], v[116:119], a[148:151], v[136:139]\n"
"s_waitcnt lgkmcnt(3)\n"
"s_mov_b32 s69, -1\n"
"ds_read_b64_tr_b16 v[218:219], v228 offset:59392\n"
"v_cvt_pk_bf16_f32 v18, v20, v21\n"
"v_mfma_f32_16x16x32_bf16 v[136:139], v[120:123], a[152:155], v[136:139]\n"
"s_mul_i32 s88, s61, 0x10011111\n"
"v_xor_b32 v248, s75, v248\n"
"v_cvt_pk_bf16_f32 v82, v84, v85\n"
"v_mfma_f32_16x16x32_bf16 v[136:139], v[224:227], a[156:159], v[136:139]\n"
"s_add_u32 s88, s88, 484675\n"
"ds_write_b128 v248, v[16:19] offset:49152\n"
"v_mul_f32_dpp v53, v247, v53 row_newbcast:1 row_mask:0xf bank_mask:0xf\n"
"v_mul_f32_dpp v54, v247, v54 row_newbcast:2 row_mask:0xf bank_mask:0xf\n"
"v_cvt_pk_bf16_f32 v50, v52, v53\n"
"v_cvt_pk_bf16_f32 v51, v54, v55\n"
"v_mfma_f32_16x16x32_bf16 v[152:155], v[224:227], a[172:175], v[196:199]\n"
"s_and_b32 s88, s88, 0x70077777\n"
"ds_read_b64_tr_b16 a[8:9], v249 offset:49152\n"
"ds_read_b64_tr_b16 a[10:11], v249 offset:49160\n"
"v_mfma_f32_16x16x32_bf16 v[152:155], v[120:123], a[168:171], v[152:155]\n"
"s_or_b32 s88, s88, 0x9800000\n"
"ds_write_b128 v248, v[48:51] offset:50176\n"
"ds_write_b128 v248, v[80:83] offset:51200\n"
"v_mfma_f32_16x16x32_bf16 v[152:155], v[116:119], a[164:167], v[152:155]\n"
"s_mov_b32 s84, s6\n"
"ds_read_b64_tr_b16 a[24:25], v249 offset:50176\n"
"ds_read_b64_tr_b16 a[26:27], v249 offset:50184\n"
"ds_read_b64_tr_b16 a[40:41], v249 offset:51200\n"
"ds_read_b64_tr_b16 a[42:43], v249 offset:51208\n"
"v_mfma_f32_16x16x32_bf16 v[152:155], v[112:115], a[160:163], v[152:155]\n"
"s_and_b32 s85, s7, 0xffff\n"
"ds_read_b64_tr_b16 v[188:189], v228 offset:40960\n"
"v_xor_b32 v248, s75, v248\n"
"v_mfma_f32_16x16x32_bf16 v[152:155], v[108:111], a[124:127], v[152:155]\n"
"s_mov_b32 s86, 128\n"
"ds_read_b64_tr_b16 v[184:185], v232 offset:32768\n"
"ds_read_b64_tr_b16 v[214:215], v232 offset:51200\n"
"v_mfma_f32_16x16x32_bf16 v[152:155], v[104:107], a[120:123], v[152:155]\n"
"s_mov_b32 s87, 131072\n"
"ds_read_b64_tr_b16 a[214:215], v237 offset:12296\n"
"ds_read_b64_tr_b16 v[190:191], v228 offset:43008\n"
"v_mfma_f32_16x16x32_bf16 v[152:155], v[100:103], a[116:119], v[152:155]\n"
"ds_read_b64_tr_b16 a[224:225], v236 offset:14336\n"
"ds_read_b64_tr_b16 v[212:213], v232 offset:49152\n"
"v_mfma_f32_16x16x32_bf16 v[152:155], v[96:99], a[112:115], v[152:155]\n"
"ds_read_b64_tr_b16 v[186:187], v232 offset:34816\n"
"v_mov_b64 v[84:85], 0\n"
"v_mfma_f32_16x16x32_bf16 v[168:171], v[96:99], a[128:131], v[196:199]\n"
"ds_read_b64_tr_b16 v[192:193], v232 offset:40960\n"
"ds_read_b64_tr_b16 v[222:223], v232 offset:59392\n"
"v_mfma_f32_16x16x32_bf16 v[168:171], v[100:103], a[132:135], v[168:171]\n"
"ds_read_b64_tr_b16 v[194:195], v232 offset:43008\n"
"v_bfe_u32 v255, v242, 8, 1\n"
"v_mfma_f32_16x16x32_bf16 v[168:171], v[104:107], a[136:139], v[168:171]\n"
"ds_read_b64_tr_b16 v[208:209], v228 offset:49152\n"
"ds_read_b64_tr_b16 v[210:211], v228 offset:51200\n"
"v_mfma_f32_16x16x32_bf16 v[168:171], v[108:111], a[140:143], v[168:171]\n"
"s_waitcnt lgkmcnt(12)\n"
"ds_read_b64_tr_b16 a[230:231], v237 offset:14344\n"
"ds_read_b64_tr_b16 v[216:217], v228 offset:57344\n"
"v_mfma_f32_16x16x32_bf16 v[168:171], v[112:115], a[176:179], v[168:171]\n"
"ds_read_b64_tr_b16 v[220:221], v232 offset:57344\n"
"v_xor_b32 v249, s75, v249\n"
"v_mfma_f32_16x16x32_bf16 v[168:171], v[116:119], a[180:183], v[168:171]\n"
"ds_read_b64_tr_b16 a[222:223], v237 offset:8200\n"
"ds_read_b64_tr_b16 a[226:227], v236 offset:14344\n"
"v_mfma_f32_16x16x32_bf16 v[168:171], v[120:123], a[184:187], v[168:171]\n"
"s_add_u32 s41, s70, 327680\n"
"s_add_u32 m0, s71, 32768\n"
"ds_read_b64_tr_b16 v[110:111], v233 offset:43008\n"
"v_mfma_f32_16x16x32_bf16 v[168:171], v[224:227], a[188:191], v[168:171]\n"
"buffer_load_dwordx4 v240, s[28:31], s41 offen lds\n"
"v_mfma_f32_16x16x32_bf16 v[124:127], v[180:183], a[48:51], v[124:127]\n"
"s_waitcnt lgkmcnt(9)\n"
"v_mul_u32_u24 v254, 2560, v254\n"
"v_mfma_f32_16x16x32_bf16 v[124:127], v[184:187], a[52:55], v[124:127]\n"
"v_mov_b64 v[32:33], s[22:23]\n"
"v_mov_b64 v[44:45], s[22:23]\n"
"v_mfma_f32_16x16x32_bf16 v[124:127], v[188:191], a[56:59], v[124:127]\n"
"s_waitcnt lgkmcnt(6)\n"
"s_add_u32 s40, s41, s52\n"
"ds_read_b64_tr_b16 a[212:213], v237 offset:12288\n"
"ds_read_b64_tr_b16 v[102:103], v233 offset:34816\n"
"v_mfma_f32_16x16x32_bf16 v[124:127], v[192:195], a[60:63], v[124:127]\n"
"buffer_load_dwordx4 v240, s[28:31], s40 offen offset:1024 lds\n"
"v_mfma_f32_16x16x32_bf16 v[124:127], v[208:211], a[0:3], v[124:127]\n"
"v_mov_b64 v[38:39], s[22:23]\n"
"v_mfma_f32_16x16x32_bf16 v[124:127], v[212:215], a[4:7], v[124:127]\n"
"s_waitcnt lgkmcnt(3)\n"
"v_mov_b64 v[244:245], 0\n"
"v_mfma_f32_16x16x32_bf16 v[124:127], v[216:219], a[8:11], v[124:127]\n"
"ds_read_b64_tr_b16 v[98:99], v229 offset:34816\n"
"ds_read_b64_tr_b16 v[116:117], v233 offset:49152\n"
"v_mfma_f32_16x16x32_bf16 v[124:127], v[220:223], a[12:15], v[124:127]\n"
"s_add_u32 s40, s41, s53\n"
"ds_read_b64_tr_b16 v[122:123], v229 offset:59392\n"
"ds_read_b64_tr_b16 v[224:225], v233 offset:57344\n"
"v_mfma_f32_16x16x32_bf16 v[140:143], v[220:223], a[28:31], v[140:143]\n"
"buffer_load_dwordx4 v240, s[28:31], s40 offen offset:2048 lds\n"
"v_mfma_f32_16x16x32_bf16 v[140:143], v[216:219], a[24:27], v[140:143]\n"
"v_mov_b64 v[16:17], s[22:23]\n"
"v_mov_b64 v[36:37], s[22:23]\n"
"v_mfma_f32_16x16x32_bf16 v[140:143], v[212:215], a[20:23], v[140:143]\n"
"v_mov_b64 v[246:247], 0\n"
"v_subrev_f32 v124, s22, v124\n"
"v_mfma_f32_16x16x32_bf16 v[140:143], v[208:211], a[16:19], v[140:143]\n"
"ds_read_b64_tr_b16 a[210:211], v236 offset:12296\n"
"ds_read_b64_tr_b16 v[106:107], v229 offset:43008\n"
"v_mfma_f32_16x16x32_bf16 v[140:143], v[192:195], a[76:79], v[140:143]\n"
"ds_read_b64_tr_b16 v[112:113], v229 offset:49152\n"
"v_xor_b32 v228, s75, v228\n"
"v_mfma_f32_16x16x32_bf16 v[140:143], v[188:191], a[72:75], v[140:143]\n"
"ds_read_b64_tr_b16 v[104:105], v229 offset:40960\n"
"v_mov_b64 v[26:27], s[22:23]\n"
"v_mfma_f32_16x16x32_bf16 v[140:143], v[184:187], a[68:71], v[140:143]\n"
"s_add_u32 s40, s41, s54\n"
"ds_read_b64_tr_b16 a[228:229], v237 offset:14336\n"
"ds_read_b64_tr_b16 v[118:119], v233 offset:51200\n"
"v_mfma_f32_16x16x32_bf16 v[140:143], v[180:183], a[64:67], v[140:143]\n"
"buffer_load_dwordx4 v240, s[28:31], s40 offen offset:3072 lds\n"
"v_mfma_f32_16x16x32_bf16 v[156:159], v[180:183], a[80:83], v[156:159]\n"
"s_add_u32 s41, s70, 491520\n"
"v_mov_b64 v[86:87], 0\n"
"v_mfma_f32_16x16x32_bf16 v[156:159], v[184:187], a[84:87], v[156:159]\n"
"s_add_u32 m0, s71, 49152\n"
"ds_read_b64_tr_b16 v[114:115], v229 offset:51200\n"
"ds_read_b64_tr_b16 v[226:227], v233 offset:59392\n"
"v_mfma_f32_16x16x32_bf16 v[156:159], v[188:191], a[88:91], v[156:159]\n"
"buffer_load_dwordx4 v240, s[28:31], s41 offen lds\n"
"v_mfma_f32_16x16x32_bf16 v[156:159], v[192:195], a[92:95], v[156:159]\n"
"v_and_b32 v243, 0x3f0, v242\n"
"v_subrev_f32 v142, s22, v142\n"
"v_mfma_f32_16x16x32_bf16 v[156:159], v[208:211], a[32:35], v[156:159]\n"
"v_mov_b64 v[6:7], s[22:23]\n"
"v_subrev_f32 v126, s22, v126\n"
"v_mfma_f32_16x16x32_bf16 v[156:159], v[212:215], a[36:39], v[156:159]\n"
"ds_read_b64_tr_b16 a[218:219], v236 offset:8200\n"
"ds_read_b64_tr_b16 v[96:97], v229 offset:32768\n"
"v_mfma_f32_16x16x32_bf16 v[156:159], v[216:219], a[40:43], v[156:159]\n"
"s_add_u32 s40, s41, s52\n"
"ds_read_b64_tr_b16 v[100:101], v233 offset:32768\n"
"ds_read_b64_tr_b16 v[108:109], v233 offset:40960\n"
"v_mfma_f32_16x16x32_bf16 v[156:159], v[220:223], a[44:47], v[156:159]\n"
"s_waitcnt lgkmcnt(6)\n"
"buffer_load_dwordx4 v240, s[28:31], s40 offen offset:1024 lds\n"
"v_mfma_f32_16x16x32_bf16 v[172:175], v[220:223], a[228:231], v[172:175]\n"
"v_subrev_f32 v143, s22, v143\n"
"v_xor_b32 v233, s75, v233\n"
"v_mfma_f32_16x16x32_bf16 v[172:175], v[216:219], a[224:227], v[172:175]\n"
"v_lshl_add_u32 v254, v255, 5, v254\n"
"v_subrev_f32 v125, s22, v125\n"
"v_mfma_f32_16x16x32_bf16 v[172:175], v[212:215], a[212:215], v[172:175]\n"
"s_add_u32 s40, s41, s53\n"
"ds_read_b64_tr_b16 v[120:121], v229 offset:57344\n"
"v_mfma_f32_16x16x32_bf16 v[172:175], v[208:211], a[208:211], v[172:175]\n"
"buffer_load_dwordx4 v240, s[28:31], s40 offen offset:2048 lds\n"
"v_mfma_f32_16x16x32_bf16 v[172:175], v[192:195], a[236:239], v[172:175]\n"
"v_add_u32 v236, s94, v243\n"
"v_mfma_f32_16x16x32_bf16 v[172:175], v[188:191], a[232:235], v[172:175]\n"
"v_bfe_u32 v255, v242, 9, 1\n"
"v_subrev_f32 v141, s22, v141\n"
"v_mfma_f32_16x16x32_bf16 v[172:175], v[184:187], a[220:223], v[172:175]\n"
"s_waitcnt lgkmcnt(1)\n"
"ds_read_b64_tr_b16 v[184:185], v234 offset:32768\n"
"v_mov_b64 v[54:55], s[22:23]\n"
"v_mfma_f32_16x16x32_bf16 v[172:175], v[180:183], a[216:219], v[172:175]\n"
"s_add_u32 s40, s41, s54\n"
"ds_read_b64_tr_b16 v[210:211], v230 offset:51200\n"
"v_mfma_f32_16x16x32_bf16 v[128:131], v[96:99], a[48:51], v[128:131]\n"
"buffer_load_dwordx4 v240, s[28:31], s40 offen offset:3072 lds\n"
"v_mfma_f32_16x16x32_bf16 v[128:131], v[100:103], a[52:55], v[128:131]\n"
"s_lshl_b32 s62, s70, 9\n"
"v_mov_b64 v[30:31], s[22:23]\n"
"v_xor_b32 v232, s75, v232\n"
"v_mfma_f32_16x16x32_bf16 v[128:131], v[104:107], a[56:59], v[128:131]\n"
"s_and_b32 s70, s88, 15\n"
"v_cvt_pk_bf16_f32 v124, v124, v125\n"
"v_mfma_f32_16x16x32_bf16 v[128:131], v[108:111], a[60:63], v[128:131]\n"
"s_lshr_b32 s88, s88, 4\n"
"ds_read_b64_tr_b16 v[218:219], v230 offset:59392\n"
"v_subrev_f32 v127, s22, v127\n"
"v_mfma_f32_16x16x32_bf16 v[128:131], v[112:115], a[0:3], v[128:131]\n"
"s_lshl_b32 s70, s70, 7\n"
"ds_read_b64_tr_b16 v[194:195], v234 offset:43008\n"
"v_bfe_u32 v237, v242, 4, 3\n"
"v_mfma_f32_16x16x32_bf16 v[128:131], v[116:119], a[4:7], v[128:131]\n"
"s_waitcnt lgkmcnt(4)\n"
"s_xor_b32 s71, s71, s75\n"
"v_lshl_add_u32 v243, s44, 2, v243\n"
"v_mov_b64 v[12:13], s[22:23]\n"
"v_mfma_f32_16x16x32_bf16 v[128:131], v[120:123], a[8:11], v[128:131]\n"
"v_lshlrev_b32_e64 v201, 2, s72\n"
"ds_read_b64_tr_b16 v[214:215], v234 offset:51200\n"
"v_cvt_pk_bf16_f32 v125, v126, v127\n"
"v_mfma_f32_16x16x32_bf16 v[128:131], v[224:227], a[12:15], v[128:131]\n"
"global_load_dword v200, v201, s[6:7] sc1\n"
"v_mfma_f32_16x16x32_bf16 v[144:147], v[224:227], a[28:31], v[144:147]\n"
"v_subrev_f32 v157, s22, v157\n"
"v_subrev_f32 v174, s22, v174\n"
"v_mfma_f32_16x16x32_bf16 v[144:147], v[120:123], a[24:27], v[144:147]\n"
"v_mov_b64 v[52:53], s[22:23]\n"
"v_subrev_f32 v156, s22, v156\n"
"v_mfma_f32_16x16x32_bf16 v[144:147], v[116:119], a[20:23], v[144:147]\n"
"ds_read_b64_tr_b16 v[188:189], v230 offset:40960\n"
"v_subrev_f32 v172, s22, v172\n"
"v_mfma_f32_16x16x32_bf16 v[144:147], v[112:115], a[16:19], v[144:147]\n"
"v_subrev_f32 v140, s22, v140\n"
"v_mfma_f32_16x16x32_bf16 v[144:147], v[108:111], a[76:79], v[144:147]\n"
"ds_read_b64_tr_b16 v[180:181], v230 offset:32768\n"
"v_subrev_f32 v130, s22, v130\n"
"v_mfma_f32_16x16x32_bf16 v[144:147], v[104:107], a[72:75], v[144:147]\n"
"v_lshl_add_u32 v254, v255, 4, v254\n"
"v_mov_b64 v[22:23], s[22:23]\n"
"v_mfma_f32_16x16x32_bf16 v[144:147], v[100:103], a[68:71], v[144:147]\n"
"ds_read_b64_tr_b16 v[192:193], v234 offset:40960\n"
"v_subrev_f32 v128, s22, v128\n"
"v_mfma_f32_16x16x32_bf16 v[144:147], v[96:99], a[64:67], v[144:147]\n"
"v_subrev_f32 v129, s22, v129\n"
"v_cvt_pk_bf16_f32 v126, v128, v129\n"
"v_mfma_f32_16x16x32_bf16 v[160:163], v[96:99], a[80:83], v[160:163]\n"
"ds_read_b64_tr_b16 v[186:187], v234 offset:34816\n"
"v_cvt_pk_bf16_f32 v156, v156, v157\n"
"v_mfma_f32_16x16x32_bf16 v[160:163], v[100:103], a[84:87], v[160:163]\n"
"ds_read_b64_tr_b16 v[190:191], v230 offset:43008\n"
"v_subrev_f32 v131, s22, v131\n"
"v_mfma_f32_16x16x32_bf16 v[160:163], v[104:107], a[88:91], v[160:163]\n"
"v_cvt_pk_bf16_f32 v127, v130, v131\n"
"v_subrev_f32 v146, s22, v146\n"
"v_mfma_f32_16x16x32_bf16 v[160:163], v[108:111], a[92:95], v[160:163]\n"
"ds_read_b64_tr_b16 v[220:221], v234 offset:57344\n"
"v_cvt_pk_bf16_f32 v140, v140, v141\n"
"v_mfma_f32_16x16x32_bf16 v[160:163], v[112:115], a[32:35], v[160:163]\n"
"v_lshl_add_u32 v248, v242, 8, s93\n"
"v_subrev_f32 v145, s22, v145\n"
"v_mfma_f32_16x16x32_bf16 v[160:163], v[116:119], a[36:39], v[160:163]\n"
"ds_read_b64_tr_b16 v[182:183], v230 offset:34816\n"
"v_cvt_pk_bf16_f32 v141, v142, v143\n"
"v_mfma_f32_16x16x32_bf16 v[160:163], v[120:123], a[40:43], v[160:163]\n"
"ds_read_b64_tr_b16 v[208:209], v230 offset:49152\n"
"v_subrev_f32 v159, s22, v159\n"
"v_mfma_f32_16x16x32_bf16 v[160:163], v[224:227], a[44:47], v[160:163]\n"
"buffer_store_dwordx4 v[124:127], v243, s[32:35], s16 offen sc1\n"
"v_mov_b64 v[24:25], s[22:23]\n"
"v_mfma_f32_16x16x32_bf16 v[176:179], v[224:227], a[228:231], v[176:179]\n"
"ds_read_b64_tr_b16 v[212:213], v234 offset:49152\n"
"v_mov_b64 v[50:51], s[22:23]\n"
"v_mfma_f32_16x16x32_bf16 v[176:179], v[120:123], a[224:227], v[176:179]\n"
"v_subrev_f32 v158, s22, v158\n"
"v_subrev_f32 v173, s22, v173\n"
"v_mfma_f32_16x16x32_bf16 v[176:179], v[116:119], a[212:215], v[176:179]\n"
"ds_read_b64_tr_b16 v[216:217], v230 offset:57344\n"
"v_mov_b64 v[94:95], 0\n"
"v_mfma_f32_16x16x32_bf16 v[176:179], v[112:115], a[208:211], v[176:179]\n"
"v_cvt_pk_bf16_f32 v172, v172, v173\n"
"v_mfma_f32_16x16x32_bf16 v[176:179], v[108:111], a[236:239], v[176:179]\n"
"ds_read_b64_tr_b16 v[112:113], v231 offset:49152\n"
"v_mov_b64 v[4:5], s[22:23]\n"
"v_mfma_f32_16x16x32_bf16 v[176:179], v[104:107], a[232:235], v[176:179]\n"
"ds_read_b64_tr_b16 v[222:223], v234 offset:59392\n"
"v_subrev_f32 v144, s22, v144\n"
"v_mfma_f32_16x16x32_bf16 v[176:179], v[100:103], a[220:223], v[176:179]\n"
"v_mov_b64 v[40:41], s[22:23]\n"
"v_subrev_f32 v147, s22, v147\n"
"v_mfma_f32_16x16x32_bf16 v[176:179], v[96:99], a[216:219], v[176:179]\n"
"s_waitcnt lgkmcnt(4)\n"
"ds_read_b64_tr_b16 v[224:225], v235 offset:57344\n"
"v_subrev_f32 v175, s22, v175\n"
"v_mfma_f32_16x16x32_bf16 v[132:135], v[180:183], a[48:51], v[132:135]\n"
"ds_read_b64_tr_b16 v[102:103], v235 offset:34816\n"
"v_mov_b32 v241, s60\n"
"v_mfma_f32_16x16x32_bf16 v[132:135], v[184:187], a[52:55], v[132:135]\n"
"ds_read_b64_tr_b16 v[96:97], v231 offset:32768\n"
"v_cvt_pk_bf16_f32 v157, v158, v159\n"
"v_mfma_f32_16x16x32_bf16 v[132:135], v[188:191], a[56:59], v[132:135]\n"
"ds_read_b64_tr_b16 v[114:115], v231 offset:51200\n"
"v_subrev_f32 v160, s22, v160\n"
"v_mfma_f32_16x16x32_bf16 v[132:135], v[192:195], a[60:63], v[132:135]\n"
"v_cvt_pk_bf16_f32 v142, v144, v145\n"
"v_subrev_f32 v161, s22, v161\n"
"v_mfma_f32_16x16x32_bf16 v[132:135], v[208:211], a[0:3], v[132:135]\n"
"s_waitcnt lgkmcnt(5)\n"
"ds_read_b64_tr_b16 v[98:99], v231 offset:34816\n"
"v_mfma_f32_16x16x32_bf16 v[132:135], v[212:215], a[4:7], v[132:135]\n"
"v_cvt_pk_bf16_f32 v143, v146, v147\n"
"v_mfma_f32_16x16x32_bf16 v[132:135], v[216:219], a[8:11], v[132:135]\n"
"s_waitcnt lgkmcnt(5)\n"
"ds_read_b64_tr_b16 v[116:117], v235 offset:49152\n"
"v_mov_b64 v[34:35], s[22:23]\n"
"v_mfma_f32_16x16x32_bf16 v[132:135], v[220:223], a[12:15], v[132:135]\n"
"buffer_store_dwordx4 v[140:143], v243, s[32:35], s16 offen offset:2048 sc1\n"
"ds_read_b64_tr_b16 v[106:107], v231 offset:43008\n"
"v_mfma_f32_16x16x32_bf16 v[148:151], v[220:223], a[28:31], v[148:151]\n"
"v_cvt_pk_bf16_f32 v173, v174, v175\n"
"v_subrev_f32 v176, s22, v176\n"
"v_mfma_f32_16x16x32_bf16 v[148:151], v[216:219], a[24:27], v[148:151]\n"
"ds_read_b64_tr_b16 v[100:101], v235 offset:32768\n"
"v_max_u32 v237, 1, v237\n"
"v_mfma_f32_16x16x32_bf16 v[148:151], v[212:215], a[20:23], v[148:151]\n"
"v_mov_b64 v[14:15], s[22:23]\n"
"v_mov_b64 v[46:47], s[22:23]\n"
"v_mfma_f32_16x16x32_bf16 v[148:151], v[208:211], a[16:19], v[148:151]\n"
"ds_read_b64_tr_b16 v[104:105], v231 offset:40960\n"
"v_mov_b64 v[42:43], s[22:23]\n"
"v_mfma_f32_16x16x32_bf16 v[148:151], v[192:195], a[76:79], v[148:151]\n"
"ds_read_b64_tr_b16 v[120:121], v231 offset:57344\n"
"v_mov_b64 v[2:3], s[22:23]\n"
"v_mfma_f32_16x16x32_bf16 v[148:151], v[188:191], a[72:75], v[148:151]\n"
"v_subrev_f32 v162, s22, v162\n"
"v_subrev_f32 v179, s22, v179\n"
"v_mfma_f32_16x16x32_bf16 v[148:151], v[184:187], a[68:71], v[148:151]\n"
"ds_read_b64_tr_b16 v[122:123], v231 offset:59392\n"
"v_xor_b32 v229, s75, v229\n"
"v_mfma_f32_16x16x32_bf16 v[148:151], v[180:183], a[64:67], v[148:151]\n"
"ds_read_b64_tr_b16 v[110:111], v235 offset:43008\n"
"v_add_u32 v238, s97, v254\n"
"v_mfma_f32_16x16x32_bf16 v[164:167], v[180:183], a[80:83], v[164:167]\n"
"v_add_u32 v252, s98, v254\n"
"v_add_u32 v241, 1, v241\n"
"v_mfma_f32_16x16x32_bf16 v[164:167], v[184:187], a[84:87], v[164:167]\n"
"ds_read_b64_tr_b16 v[108:109], v235 offset:40960\n"
"v_mov_b64 v[10:11], s[22:23]\n"
"v_mfma_f32_16x16x32_bf16 v[164:167], v[188:191], a[88:91], v[164:167]\n"
"v_mov_b64 v[28:29], s[22:23]\n"
"v_subrev_f32 v177, s22, v177\n"
"v_mfma_f32_16x16x32_bf16 v[164:167], v[192:195], a[92:95], v[164:167]\n"
"ds_read_b64_tr_b16 v[118:119], v235 offset:51200\n"
"v_subrev_f32 v132, s22, v132\n"
"v_mfma_f32_16x16x32_bf16 v[164:167], v[208:211], a[32:35], v[164:167]\n"
"v_subrev_f32 v163, s22, v163\n"
"v_subrev_f32 v135, s22, v135\n"
"v_mfma_f32_16x16x32_bf16 v[164:167], v[212:215], a[36:39], v[164:167]\n"
"ds_read_b64_tr_b16 v[226:227], v235 offset:59392\n"
"v_cvt_pk_bf16_f32 v159, v162, v163\n"
"v_mfma_f32_16x16x32_bf16 v[164:167], v[216:219], a[40:43], v[164:167]\n"
"v_xor_b32 v237, s61, v237\n"
"v_mov_b64 v[18:19], s[22:23]\n"
"v_mfma_f32_16x16x32_bf16 v[164:167], v[220:223], a[44:47], v[164:167]\n"
"s_barrier\n"
"s_waitcnt lgkmcnt(3)\n"
"v_mfma_f32_16x16x32_bf16 v[136:139], v[96:99], a[48:51], v[136:139]\n"
"ds_read_b64_tr_b16 v[214:215], v232 offset:18432\n"
"v_mfma_f32_16x16x32_bf16 v[136:139], v[100:103], a[52:55], v[136:139]\n"
"ds_read_b64_tr_b16 v[212:213], v232 offset:16384\n"
"v_cvt_pk_bf16_f32 v158, v160, v161\n"
"v_mfma_f32_16x16x32_bf16 v[136:139], v[104:107], a[56:59], v[136:139]\n"
"s_waitcnt lgkmcnt(4)\n"
"ds_read_b64_tr_b16 v[220:221], v232 offset:24576\n"
"v_subrev_f32 v178, s22, v178\n"
"v_mfma_f32_16x16x32_bf16 v[136:139], v[108:111], a[60:63], v[136:139]\n"
"ds_read_b64_tr_b16 v[180:181], v228 offset:0\n"
"v_mfma_f32_16x16x32_bf16 v[136:139], v[112:115], a[0:3], v[136:139]\n"
"s_waitcnt lgkmcnt(5)\n"
"ds_read_b64_tr_b16 v[194:195], v232 offset:10240\n"
"v_mfma_f32_16x16x32_bf16 v[136:139], v[116:119], a[4:7], v[136:139]\n"
"ds_read_b64_tr_b16 v[188:189], v228 offset:8192\n"
"v_add_u32 v253, s99, v254\n"
"v_mfma_f32_16x16x32_bf16 v[136:139], v[120:123], a[8:11], v[136:139]\n"
"s_waitcnt lgkmcnt(6)\n"
"ds_read_b64_tr_b16 v[184:185], v232 offset:0\n"
"v_subrev_f32 v133, s22, v133\n"
"v_mfma_f32_16x16x32_bf16 v[136:139], v[224:227], a[12:15], v[136:139]\n"
"buffer_store_dwordx4 v[156:159], v243, s[32:35], s17 offen sc1\n"
"ds_read_b64_tr_b16 v[222:223], v232 offset:26624\n"
"v_mfma_f32_16x16x32_bf16 v[152:155], v[224:227], a[28:31], v[152:155]\n"
"ds_read_b64_tr_b16 v[208:209], v228 offset:16384\n"
"v_mfma_f32_16x16x32_bf16 v[152:155], v[120:123], a[24:27], v[152:155]\n"
"ds_read_b64_tr_b16 v[190:191], v228 offset:10240\n"
"v_mfma_f32_16x16x32_bf16 v[152:155], v[116:119], a[20:23], v[152:155]\n"
"ds_read_b64_tr_b16 v[186:187], v232 offset:2048\n"
"v_subrev_f32 v134, s22, v134\n"
"v_mfma_f32_16x16x32_bf16 v[152:155], v[112:115], a[16:19], v[152:155]\n"
"ds_read_b64_tr_b16 v[218:219], v228 offset:26624\n"
"v_mfma_f32_16x16x32_bf16 v[152:155], v[108:111], a[76:79], v[152:155]\n"
"ds_read_b64_tr_b16 v[192:193], v232 offset:8192\n"
"v_mfma_f32_16x16x32_bf16 v[152:155], v[104:107], a[72:75], v[152:155]\n"
"ds_read_b64_tr_b16 v[182:183], v228 offset:2048\n"
"v_xor_b32 v239, 64, v238\n"
"v_mfma_f32_16x16x32_bf16 v[152:155], v[100:103], a[68:71], v[152:155]\n"
"ds_read_b64_tr_b16 v[210:211], v228 offset:18432\n"
"ds_read_b64_tr_b16 v[216:217], v228 offset:24576\n"
"v_mfma_f32_16x16x32_bf16 v[152:155], v[96:99], a[64:67], v[152:155]\n"
"v_mfma_f32_16x16x32_bf16 v[168:171], v[96:99], a[80:83], v[168:171]\n"
"ds_read_b64_tr_b16 v[96:97], v229 offset:0\n"
"ds_read_b64_tr_b16 v[98:99], v229 offset:2048\n"
"v_mfma_f32_16x16x32_bf16 v[168:171], v[100:103], a[84:87], v[168:171]\n"
"ds_read_b64_tr_b16 v[100:101], v233 offset:0\n"
"ds_read_b64_tr_b16 v[102:103], v233 offset:2048\n"
"v_mfma_f32_16x16x32_bf16 v[168:171], v[104:107], a[88:91], v[168:171]\n"
"ds_read_b64_tr_b16 v[104:105], v229 offset:8192\n"
"v_cvt_pk_bf16_f32 v175, v178, v179\n"
"v_mfma_f32_16x16x32_bf16 v[168:171], v[108:111], a[92:95], v[168:171]\n"
"v_mov_b64 v[20:21], s[22:23]\n"
"v_mov_b64 v[90:91], 0\n"
"v_mfma_f32_16x16x32_bf16 v[168:171], v[112:115], a[32:35], v[168:171]\n"
"v_mov_b64 v[88:89], 0\n"
"v_mov_b64 v[92:93], 0\n"
"v_mfma_f32_16x16x32_bf16 v[168:171], v[116:119], a[36:39], v[168:171]\n"
"v_mov_b64 v[48:49], s[22:23]\n"
"v_cvt_pk_bf16_f32 v132, v132, v133\n"
"v_mfma_f32_16x16x32_bf16 v[168:171], v[120:123], a[40:43], v[168:171]\n"
"s_add_u32 s47, s47, 1\n"
"v_mov_b64 v[0:1], s[22:23]\n"
"v_mov_b64 v[8:9], s[22:23]\n"
"v_mfma_f32_16x16x32_bf16 v[168:171], v[224:227], a[44:47], v[168:171]\n"
"s_waitcnt lgkmcnt(5)\n"
"v_lshlrev_b32 v237, 2, v237\n"
"v_cvt_pk_bf16_f32 v174, v176, v177\n"
"LGL_%=:\n"
"v_mfma_f32_16x16x32_bf16 v[124:127], v[180:183], a[96:99], v[196:199]\n"
"ds_read_b64_tr_b16 v[106:107], v229 offset:10240\n"
"v_cvt_pk_bf16_f32 v133, v134, v135\n"
"v_mfma_f32_16x16x32_bf16 v[124:127], v[184:187], a[100:103], v[124:127]\n"
"ds_read_b64_tr_b16 v[108:109], v233 offset:8192\n"
"v_subrev_f32 v136, s22, v136\n"
"v_mfma_f32_16x16x32_bf16 v[124:127], v[188:191], a[104:107], v[124:127]\n"
"ds_read_b64_tr_b16 v[110:111], v233 offset:10240\n"
"v_subrev_f32 v137, s22, v137\n"
"v_mfma_f32_16x16x32_bf16 v[124:127], v[192:195], a[108:111], v[124:127]\n"
"ds_read_b64_tr_b16 v[112:113], v229 offset:16384\n"
"v_subrev_f32 v138, s22, v138\n"
"v_mfma_f32_16x16x32_bf16 v[124:127], v[208:211], a[144:147], v[124:127]\n"
"ds_read_b64_tr_b16 v[114:115], v229 offset:18432\n"
"v_subrev_f32 v139, s22, v139\n"
"v_mfma_f32_16x16x32_bf16 v[124:127], v[212:215], a[148:151], v[124:127]\n"
"s_waitcnt lgkmcnt(7)\n"
"ds_read_b64_tr_b16 v[116:117], v233 offset:16384\n"
"v_cvt_pk_bf16_f32 v134, v136, v137\n"
"v_mfma_f32_16x16x32_bf16 v[124:127], v[216:219], a[152:155], v[124:127]\n"
"ds_read_b64_tr_b16 v[118:119], v233 offset:18432\n"
"v_cvt_pk_bf16_f32 v135, v138, v139\n"
"v_mfma_f32_16x16x32_bf16 v[124:127], v[220:223], a[156:159], v[124:127]\n"
"buffer_store_dwordx4 v[172:175], v243, s[32:35], s17 offen offset:2048 sc1\n"
"v_subrev_f32 v148, s22, v148\n"
"v_mfma_f32_16x16x32_bf16 v[140:143], v[220:223], a[172:175], v[196:199]\n"
"ds_read_b64_tr_b16 v[120:121], v229 offset:24576\n"
"v_subrev_f32 v149, s22, v149\n"
"v_mfma_f32_16x16x32_bf16 v[140:143], v[216:219], a[168:171], v[140:143]\n"
"ds_read_b64_tr_b16 v[122:123], v229 offset:26624\n"
"v_subrev_f32 v150, s22, v150\n"
"v_mfma_f32_16x16x32_bf16 v[140:143], v[212:215], a[164:167], v[140:143]\n"
"ds_read_b64_tr_b16 v[224:225], v233 offset:24576\n"
"v_subrev_f32 v151, s22, v151\n"
"v_mfma_f32_16x16x32_bf16 v[140:143], v[208:211], a[160:163], v[140:143]\n"
"ds_read_b64_tr_b16 v[226:227], v233 offset:26624\n"
"v_cvt_pk_bf16_f32 v148, v148, v149\n"
"v_mfma_f32_16x16x32_bf16 v[140:143], v[192:195], a[124:127], v[140:143]\n"
"v_cvt_pk_bf16_f32 v149, v150, v151\n"
"v_subrev_f32 v152, s22, v152\n"
"v_mfma_f32_16x16x32_bf16 v[140:143], v[188:191], a[120:123], v[140:143]\n"
"s_add_u32 s41, s70, 0\n"
"v_subrev_f32 v153, s22, v153\n"
"v_subrev_f32 v154, s22, v154\n"
"v_mfma_f32_16x16x32_bf16 v[140:143], v[184:187], a[116:119], v[140:143]\n"
"s_mov_b32 m0, s71\n"
"v_subrev_f32 v155, s22, v155\n"
"v_cvt_pk_bf16_f32 v150, v152, v153\n"
"v_mfma_f32_16x16x32_bf16 v[140:143], v[180:183], a[112:115], v[140:143]\n"
"s_waitcnt vmcnt(14)\n"
"buffer_load_dwordx4 v240, s[28:31], s41 offen lds\n"
"v_mfma_f32_16x16x32_bf16 v[156:159], v[180:183], a[128:131], v[196:199]\n"
"v_xor_b32 v230, s75, v230\n"
"v_cvt_pk_bf16_f32 v151, v154, v155\n"
"v_mfma_f32_16x16x32_bf16 v[156:159], v[184:187], a[132:135], v[156:159]\n"
"v_xor_b32 v234, s75, v234\n"
"v_subrev_f32 v164, s22, v164\n"
"v_mfma_f32_16x16x32_bf16 v[156:159], v[188:191], a[136:139], v[156:159]\n"
"v_subrev_f32 v165, s22, v165\n"
"v_subrev_f32 v166, s22, v166\n"
"v_mfma_f32_16x16x32_bf16 v[156:159], v[192:195], a[140:143], v[156:159]\n"
"v_subrev_f32 v167, s22, v167\n"
"v_cvt_pk_bf16_f32 v164, v164, v165\n"
"v_mfma_f32_16x16x32_bf16 v[156:159], v[208:211], a[176:179], v[156:159]\n"
"v_cvt_pk_bf16_f32 v165, v166, v167\n"
"v_subrev_f32 v168, s22, v168\n"
"v_mfma_f32_16x16x32_bf16 v[156:159], v[212:215], a[180:183], v[156:159]\n"
"v_subrev_f32 v169, s22, v169\n"
"v_subrev_f32 v170, s22, v170\n"
"v_mfma_f32_16x16x32_bf16 v[156:159], v[216:219], a[184:187], v[156:159]\n"
"v_subrev_f32 v171, s22, v171\n"
"v_cvt_pk_bf16_f32 v166, v168, v169\n"
"v_mfma_f32_16x16x32_bf16 v[156:159], v[220:223], a[188:191], v[156:159]\n"
"buffer_store_dwordx4 v[132:135], v243, s[32:35], s16 offen offset:1024 sc1\n"
"v_cvt_pk_bf16_f32 v167, v170, v171\n"
"v_mfma_f32_16x16x32_bf16 v[172:175], v[220:223], a[252:255], v[196:199]\n"
"ds_read_b64_tr_b16 v[220:221], v234 offset:24576\n"
"v_xor_b32 v231, s75, v231\n"
"v_mfma_f32_16x16x32_bf16 v[172:175], v[216:219], a[248:251], v[172:175]\n"
"ds_read_b64_tr_b16 v[216:217], v230 offset:24576\n"
"v_xor_b32 v235, s75, v235\n"
"v_mfma_f32_16x16x32_bf16 v[172:175], v[212:215], a[244:247], v[172:175]\n"
"ds_read_b64_tr_b16 v[212:213], v234 offset:16384\n"
"v_lshlrev_b32 v202, 16, v84\n"
"v_mfma_f32_16x16x32_bf16 v[172:175], v[208:211], a[240:243], v[172:175]\n"
"ds_read_b64_tr_b16 v[208:209], v230 offset:16384\n"
"v_and_b32 v84, s73, v84\n"
"v_mfma_f32_16x16x32_bf16 v[172:175], v[192:195], a[204:207], v[172:175]\n"
"ds_read_b64_tr_b16 v[192:193], v234 offset:8192\n"
"v_add_f32 v25, v25, v84\n"
"v_mfma_f32_16x16x32_bf16 v[172:175], v[188:191], a[200:203], v[172:175]\n"
"ds_read_b64_tr_b16 v[188:189], v230 offset:8192\n"
"v_lshlrev_b32 v203, 16, v85\n"
"v_mfma_f32_16x16x32_bf16 v[172:175], v[184:187], a[196:199], v[172:175]\n"
"s_add_u32 s40, s41, s52\n"
"ds_read_b64_tr_b16 v[184:185], v234 offset:0\n"
"v_and_b32 v85, s73, v85\n"
"v_mfma_f32_16x16x32_bf16 v[172:175], v[180:183], a[192:195], v[172:175]\n"
"s_waitcnt vmcnt(13) lgkmcnt(7)\n"
"buffer_load_dwordx4 v240, s[28:31], s40 offen offset:1024 lds\n"
"v_mfma_f32_16x16x32_bf16 v[128:131], v[96:99], a[96:99], v[196:199]\n"
"s_waitcnt lgkmcnt(6)\n"
"ds_read_b64_tr_b16 v[180:181], v230 offset:0\n"
"v_add_f32 v27, v27, v85\n"
"v_mfma_f32_16x16x32_bf16 v[128:131], v[100:103], a[100:103], v[128:131]\n"
"ds_read_b64_tr_b16 v[182:183], v230 offset:2048\n"
"v_lshlrev_b32 v204, 16, v86\n"
"v_mfma_f32_16x16x32_bf16 v[128:131], v[104:107], a[104:107], v[128:131]\n"
"ds_read_b64_tr_b16 v[186:187], v234 offset:2048\n"
"v_and_b32 v86, s73, v86\n"
"v_mfma_f32_16x16x32_bf16 v[128:131], v[108:111], a[108:111], v[128:131]\n"
"ds_read_b64_tr_b16 v[190:191], v230 offset:10240\n"
"v_add_f32 v29, v29, v86\n"
"v_mfma_f32_16x16x32_bf16 v[128:131], v[112:115], a[144:147], v[128:131]\n"
"s_waitcnt vmcnt(6)\n"
"ds_read_b64_tr_b16 v[194:195], v234 offset:10240\n"
"v_lshlrev_b32 v205, 16, v87\n"
"v_mfma_f32_16x16x32_bf16 v[128:131], v[116:119], a[148:151], v[128:131]\n"
"v_subrev_u32 v200, s21, v200\n"
"ds_read_b64_tr_b16 v[210:211], v230 offset:18432\n"
"v_mfma_f32_16x16x32_bf16 v[128:131], v[120:123], a[152:155], v[128:131]\n"
"v_cmp_lt_u32 vcc, s69, v200\n"
"ds_read_b64_tr_b16 v[214:215], v234 offset:18432\n"
"v_mfma_f32_16x16x32_bf16 v[128:131], v[224:227], a[156:159], v[128:131]\n"
"ds_read_b64_tr_b16 v[218:219], v230 offset:26624\n"
"s_cbranch_vccnz LGP_%=\nLGPK_%=:\n"
"v_mfma_f32_16x16x32_bf16 v[144:147], v[224:227], a[172:175], v[196:199]\n"
"s_sub_u32 s72, s72, 1\n"
"ds_read_b64_tr_b16 v[222:223], v234 offset:26624\n"
"v_and_b32 v87, s73, v87\n"
"v_mfma_f32_16x16x32_bf16 v[144:147], v[120:123], a[168:171], v[144:147]\n"
"s_and_b32 s72, s72, 7\n"
"v_add_f32 v24, v24, v202\n"
"v_add_f32 v31, v31, v87\n"
"v_mfma_f32_16x16x32_bf16 v[144:147], v[116:119], a[164:167], v[144:147]\n"
"v_add_f32 v26, v26, v203\n"
"v_lshlrev_b32 v206, 16, v88\n"
"v_mfma_f32_16x16x32_bf16 v[144:147], v[112:115], a[160:163], v[144:147]\n"
"buffer_load_dwordx4 v[72:75], v243, s[32:35], s18 offen sc1\n"
"v_mfma_f32_16x16x32_bf16 v[144:147], v[108:111], a[124:127], v[144:147]\n"
"v_and_b32 v88, s73, v88\n"
"v_add_f32 v33, v33, v88\n"
"v_mfma_f32_16x16x32_bf16 v[144:147], v[104:107], a[120:123], v[144:147]\n"
"v_lshlrev_b32 v207, 16, v89\n"
"v_and_b32 v89, s73, v89\n"
"v_mfma_f32_16x16x32_bf16 v[144:147], v[100:103], a[116:119], v[144:147]\n"
"v_add_f32 v35, v35, v89\n"
"v_lshlrev_b32 v202, 16, v90\n"
"v_mfma_f32_16x16x32_bf16 v[144:147], v[96:99], a[112:115], v[144:147]\n"
"buffer_store_dwordx4 v[148:151], v243, s[32:35], s16 offen offset:3072 sc1\n"
"v_and_b32 v90, s73, v90\n"
"v_mfma_f32_16x16x32_bf16 v[160:163], v[96:99], a[128:131], v[196:199]\n"
"v_add_f32 v37, v37, v90\n"
"v_lshlrev_b32 v203, 16, v91\n"
"v_mfma_f32_16x16x32_bf16 v[160:163], v[100:103], a[132:135], v[160:163]\n"
"v_and_b32 v91, s73, v91\n"
"v_add_f32 v39, v39, v91\n"
"v_mfma_f32_16x16x32_bf16 v[160:163], v[104:107], a[136:139], v[160:163]\n"
"v_add_f32 v28, v28, v204\n"
"v_add_f32 v30, v30, v205\n"
"v_mfma_f32_16x16x32_bf16 v[160:163], v[108:111], a[140:143], v[160:163]\n"
"v_add_f32 v32, v32, v206\n"
"v_add_f32 v34, v34, v207\n"
"v_mfma_f32_16x16x32_bf16 v[160:163], v[112:115], a[176:179], v[160:163]\n"
"v_lshlrev_b32 v204, 16, v92\n"
"v_and_b32 v92, s73, v92\n"
"v_mfma_f32_16x16x32_bf16 v[160:163], v[116:119], a[180:183], v[160:163]\n"
"v_add_f32 v41, v41, v92\n"
"v_lshlrev_b32 v205, 16, v93\n"
"v_mfma_f32_16x16x32_bf16 v[160:163], v[120:123], a[184:187], v[160:163]\n"
"s_add_u32 s40, s41, s53\n"
"v_and_b32 v93, s73, v93\n"
"v_add_f32 v43, v43, v93\n"
"v_mfma_f32_16x16x32_bf16 v[160:163], v[224:227], a[188:191], v[160:163]\n"
"buffer_load_dwordx4 v240, s[28:31], s40 offen offset:2048 lds\n"
"v_mfma_f32_16x16x32_bf16 v[176:179], v[224:227], a[252:255], v[196:199]\n"
"ds_read_b64_tr_b16 v[224:225], v235 offset:24576\n"
"v_lshlrev_b32 v206, 16, v94\n"
"v_mfma_f32_16x16x32_bf16 v[176:179], v[120:123], a[248:251], v[176:179]\n"
"ds_read_b64_tr_b16 v[120:121], v231 offset:24576\n"
"v_and_b32 v94, s73, v94\n"
"v_mfma_f32_16x16x32_bf16 v[176:179], v[116:119], a[244:247], v[176:179]\n"
"ds_read_b64_tr_b16 v[116:117], v235 offset:16384\n"
"v_add_f32 v45, v45, v94\n"
"v_mfma_f32_16x16x32_bf16 v[176:179], v[112:115], a[240:243], v[176:179]\n"
"buffer_load_dwordx4 v[76:79], v243, s[32:35], s18 offen offset:1024 sc1\n"
"v_mfma_f32_16x16x32_bf16 v[176:179], v[108:111], a[204:207], v[176:179]\n"
"ds_read_b64_tr_b16 v[108:109], v235 offset:8192\n"
"v_lshlrev_b32 v207, 16, v95\n"
"v_mfma_f32_16x16x32_bf16 v[176:179], v[104:107], a[200:203], v[176:179]\n"
"ds_read_b64_tr_b16 v[104:105], v231 offset:8192\n"
"v_and_b32 v95, s73, v95\n"
"v_mfma_f32_16x16x32_bf16 v[176:179], v[100:103], a[196:199], v[176:179]\n"
"s_add_u32 s40, s41, s54\n"
"ds_read_b64_tr_b16 v[100:101], v235 offset:0\n"
"v_add_f32 v47, v47, v95\n"
"v_mfma_f32_16x16x32_bf16 v[176:179], v[96:99], a[192:195], v[176:179]\n"
"s_waitcnt lgkmcnt(6)\n"
"buffer_load_dwordx4 v240, s[28:31], s40 offen offset:3072 lds\n"
"v_mfma_f32_16x16x32_bf16 v[132:135], v[180:183], a[96:99], v[196:199]\n"
"ds_read_b64_tr_b16 v[96:97], v231 offset:0\n"
"v_add_f32 v36, v36, v202\n"
"v_mfma_f32_16x16x32_bf16 v[132:135], v[184:187], a[100:103], v[132:135]\n"
"ds_read_b64_tr_b16 v[98:99], v231 offset:2048\n"
"v_add_f32 v38, v38, v203\n"
"v_mfma_f32_16x16x32_bf16 v[132:135], v[188:191], a[104:107], v[132:135]\n"
"ds_read_b64_tr_b16 v[102:103], v235 offset:2048\n"
"v_add_f32 v44, v44, v206\n"
"v_mfma_f32_16x16x32_bf16 v[132:135], v[192:195], a[108:111], v[132:135]\n"
"ds_read_b64_tr_b16 v[106:107], v231 offset:10240\n"
"v_add_f32 v46, v46, v207\n"
"v_mfma_f32_16x16x32_bf16 v[132:135], v[208:211], a[144:147], v[132:135]\n"
"ds_read_b64_tr_b16 v[110:111], v235 offset:10240\n"
"v_add_f32 v40, v40, v204\n"
"v_mfma_f32_16x16x32_bf16 v[132:135], v[212:215], a[148:151], v[132:135]\n"
"ds_read_b64_tr_b16 v[112:113], v231 offset:16384\n"
"v_add_f32 v42, v42, v205\n"
"v_mfma_f32_16x16x32_bf16 v[132:135], v[216:219], a[152:155], v[132:135]\n"
"ds_read_b64_tr_b16 v[114:115], v231 offset:18432\n"
"v_mfma_f32_16x16x32_bf16 v[132:135], v[220:223], a[156:159], v[132:135]\n"
"buffer_store_dwordx4 v[164:167], v243, s[32:35], s17 offen offset:1024 sc1\n"
"v_lshlrev_b32 v206, 16, v244\n"
"v_mfma_f32_16x16x32_bf16 v[148:151], v[220:223], a[172:175], v[196:199]\n"
"ds_read_b64_tr_b16 v[118:119], v235 offset:18432\n"
"v_and_b32 v244, s73, v244\n"
"v_mfma_f32_16x16x32_bf16 v[148:151], v[216:219], a[168:171], v[148:151]\n"
"ds_read_b64_tr_b16 v[122:123], v231 offset:26624\n"
"v_add_f32 v49, v49, v244\n"
"v_mfma_f32_16x16x32_bf16 v[148:151], v[212:215], a[164:167], v[148:151]\n"
"ds_read_b64_tr_b16 v[226:227], v235 offset:26624\n"
"v_lshlrev_b32 v207, 16, v245\n"
"v_mfma_f32_16x16x32_bf16 v[148:151], v[208:211], a[160:163], v[148:151]\n"
"buffer_load_dwordx4 v[80:83], v243, s[32:35], s18 offen offset:2048 sc1\n"
"v_mfma_f32_16x16x32_bf16 v[148:151], v[192:195], a[124:127], v[148:151]\n"
"v_and_b32 v245, s73, v245\n"
"v_add_f32 v51, v51, v245\n"
"v_mfma_f32_16x16x32_bf16 v[148:151], v[188:191], a[120:123], v[148:151]\n"
"s_add_u32 s41, s70, 163840\n"
"v_lshlrev_b32 v202, 16, v246\n"
"v_and_b32 v246, s73, v246\n"
"v_mfma_f32_16x16x32_bf16 v[148:151], v[184:187], a[116:119], v[148:151]\n"
"s_add_u32 m0, s71, 16384\n"
"v_add_f32 v53, v53, v246\n"
"v_lshlrev_b32 v203, 16, v247\n"
"v_mfma_f32_16x16x32_bf16 v[148:151], v[180:183], a[112:115], v[148:151]\n"
"buffer_load_dwordx4 v240, s[28:31], s41 offen lds\n"
"v_mfma_f32_16x16x32_bf16 v[164:167], v[180:183], a[128:131], v[196:199]\n"
"v_and_b32 v247, s73, v247\n"
"v_add_f32 v55, v55, v247\n"
"v_mfma_f32_16x16x32_bf16 v[164:167], v[184:187], a[132:135], v[164:167]\n"
"v_add_f32 v48, v48, v206\n"
"v_add_f32 v50, v50, v207\n"
"v_mfma_f32_16x16x32_bf16 v[164:167], v[188:191], a[136:139], v[164:167]\n"
"v_add_f32 v52, v52, v202\n"
"v_add_f32 v54, v54, v203\n"
"v_mfma_f32_16x16x32_bf16 v[164:167], v[192:195], a[140:143], v[164:167]\n"
"v_mfma_f32_16x16x32_bf16 v[164:167], v[208:211], a[176:179], v[164:167]\n"
"v_mfma_f32_16x16x32_bf16 v[164:167], v[212:215], a[180:183], v[164:167]\n"
"v_mfma_f32_16x16x32_bf16 v[164:167], v[216:219], a[184:187], v[164:167]\n"
"s_waitcnt vmcnt(12)\n"
"v_mfma_f32_16x16x32_bf16 v[164:167], v[220:223], a[188:191], v[164:167]\n"
"s_barrier\n"
"s_waitcnt lgkmcnt(0)\n"
"v_mfma_f32_16x16x32_bf16 v[136:139], v[96:99], a[96:99], v[196:199]\n"
"ds_read_b64_tr_b16 v[180:181], v228 offset:32768\n"
"v_mfma_f32_16x16x32_bf16 v[136:139], v[100:103], a[100:103], v[136:139]\n"
"ds_read_b64_tr_b16 v[182:183], v228 offset:34816\n"
"v_mfma_f32_16x16x32_bf16 v[136:139], v[104:107], a[104:107], v[136:139]\n"
"ds_read_b64_tr_b16 v[184:185], v232 offset:32768\n"
"v_mfma_f32_16x16x32_bf16 v[136:139], v[108:111], a[108:111], v[136:139]\n"
"ds_read_b64_tr_b16 v[186:187], v232 offset:34816\n"
"v_mfma_f32_16x16x32_bf16 v[136:139], v[112:115], a[144:147], v[136:139]\n"
"ds_read_b64_tr_b16 v[188:189], v228 offset:40960\n"
"v_mfma_f32_16x16x32_bf16 v[136:139], v[116:119], a[148:151], v[136:139]\n"
"ds_read_b64_tr_b16 v[190:191], v228 offset:43008\n"
"v_mfma_f32_16x16x32_bf16 v[136:139], v[120:123], a[152:155], v[136:139]\n"
"s_add_u32 s40, s41, s52\n"
"ds_read_b64_tr_b16 v[192:193], v232 offset:40960\n"
"v_mfma_f32_16x16x32_bf16 v[136:139], v[224:227], a[156:159], v[136:139]\n"
"buffer_load_dwordx4 v240, s[28:31], s40 offen offset:1024 lds\n"
"v_mfma_f32_16x16x32_bf16 v[152:155], v[224:227], a[172:175], v[196:199]\n"
"ds_read_b64_tr_b16 v[194:195], v232 offset:43008\n"
"v_mfma_f32_16x16x32_bf16 v[152:155], v[120:123], a[168:171], v[152:155]\n"
"ds_read_b64_tr_b16 v[208:209], v228 offset:49152\n"
"v_mfma_f32_16x16x32_bf16 v[152:155], v[116:119], a[164:167], v[152:155]\n"
"ds_read_b64_tr_b16 v[210:211], v228 offset:51200\n"
"v_mfma_f32_16x16x32_bf16 v[152:155], v[112:115], a[160:163], v[152:155]\n"
"ds_read_b64_tr_b16 v[212:213], v232 offset:49152\n"
"v_mfma_f32_16x16x32_bf16 v[152:155], v[108:111], a[124:127], v[152:155]\n"
"ds_read_b64_tr_b16 v[214:215], v232 offset:51200\n"
"v_mfma_f32_16x16x32_bf16 v[152:155], v[104:107], a[120:123], v[152:155]\n"
"ds_read_b64_tr_b16 v[216:217], v228 offset:57344\n"
"v_mfma_f32_16x16x32_bf16 v[152:155], v[100:103], a[116:119], v[152:155]\n"
"s_add_u32 s40, s41, s53\n"
"ds_read_b64_tr_b16 v[218:219], v228 offset:59392\n"
"v_xor_b32 v228, s75, v228\n"
"v_mfma_f32_16x16x32_bf16 v[152:155], v[96:99], a[112:115], v[152:155]\n"
"buffer_load_dwordx4 v240, s[28:31], s40 offen offset:2048 lds\n"
"v_mfma_f32_16x16x32_bf16 v[168:171], v[96:99], a[128:131], v[196:199]\n"
"ds_read_b64_tr_b16 v[220:221], v232 offset:57344\n"
"v_mfma_f32_16x16x32_bf16 v[168:171], v[100:103], a[132:135], v[168:171]\n"
"ds_read_b64_tr_b16 v[222:223], v232 offset:59392\n"
"v_xor_b32 v232, s75, v232\n"
"v_mfma_f32_16x16x32_bf16 v[168:171], v[104:107], a[136:139], v[168:171]\n"
"ds_read_b64_tr_b16 v[96:97], v229 offset:32768\n"
"v_mfma_f32_16x16x32_bf16 v[168:171], v[108:111], a[140:143], v[168:171]\n"
"ds_read_b64_tr_b16 v[98:99], v229 offset:34816\n"
"v_mfma_f32_16x16x32_bf16 v[168:171], v[112:115], a[176:179], v[168:171]\n"
"ds_read_b64_tr_b16 v[100:101], v233 offset:32768\n"
"v_mfma_f32_16x16x32_bf16 v[168:171], v[116:119], a[180:183], v[168:171]\n"
"ds_read_b64_tr_b16 v[102:103], v233 offset:34816\n"
"v_mfma_f32_16x16x32_bf16 v[168:171], v[120:123], a[184:187], v[168:171]\n"
"s_add_u32 s40, s41, s54\n"
"ds_read_b64_tr_b16 v[104:105], v229 offset:40960\n"
"v_mfma_f32_16x16x32_bf16 v[168:171], v[224:227], a[188:191], v[168:171]\n"
"s_waitcnt lgkmcnt(7)\n"
"buffer_load_dwordx4 v240, s[28:31], s40 offen offset:3072 lds\n"
"v_mfma_f32_16x16x32_bf16 v[124:127], v[180:183], a[48:51], v[124:127]\n"
"ds_read_b64_tr_b16 v[106:107], v229 offset:43008\n"
"v_mfma_f32_16x16x32_bf16 v[124:127], v[184:187], a[52:55], v[124:127]\n"
"ds_read_b64_tr_b16 v[108:109], v233 offset:40960\n"
"v_mfma_f32_16x16x32_bf16 v[124:127], v[188:191], a[56:59], v[124:127]\n"
"ds_read_b64_tr_b16 v[110:111], v233 offset:43008\n"
"v_mfma_f32_16x16x32_bf16 v[124:127], v[192:195], a[60:63], v[124:127]\n"
"ds_read_b64_tr_b16 v[112:113], v229 offset:49152\n"
"v_mfma_f32_16x16x32_bf16 v[124:127], v[208:211], a[0:3], v[124:127]\n"
"ds_read_b64_tr_b16 v[114:115], v229 offset:51200\n"
"v_mfma_f32_16x16x32_bf16 v[124:127], v[212:215], a[4:7], v[124:127]\n"
"s_add_u32 s41, s70, 327680\n"
"ds_read_b64_tr_b16 v[116:117], v233 offset:49152\n"
"v_mfma_f32_16x16x32_bf16 v[124:127], v[216:219], a[8:11], v[124:127]\n"
"s_waitcnt lgkmcnt(6)\n"
"s_add_u32 m0, s71, 32768\n"
"ds_read_b64_tr_b16 v[118:119], v233 offset:51200\n"
"v_mfma_f32_16x16x32_bf16 v[124:127], v[220:223], a[12:15], v[124:127]\n"
"buffer_load_dwordx4 v240, s[28:31], s41 offen lds\n"
"v_mfma_f32_16x16x32_bf16 v[140:143], v[220:223], a[28:31], v[140:143]\n"
"ds_read_b64_tr_b16 v[120:121], v229 offset:57344\n"
"v_mfma_f32_16x16x32_bf16 v[140:143], v[216:219], a[24:27], v[140:143]\n"
"ds_read_b64_tr_b16 v[122:123], v229 offset:59392\n"
"v_xor_b32 v229, s75, v229\n"
"v_mfma_f32_16x16x32_bf16 v[140:143], v[212:215], a[20:23], v[140:143]\n"
"ds_read_b64_tr_b16 v[224:225], v233 offset:57344\n"
"v_mfma_f32_16x16x32_bf16 v[140:143], v[208:211], a[16:19], v[140:143]\n"
"ds_read_b64_tr_b16 v[226:227], v233 offset:59392\n"
"v_xor_b32 v233, s75, v233\n"
"v_mfma_f32_16x16x32_bf16 v[140:143], v[192:195], a[76:79], v[140:143]\n"
"v_mfma_f32_16x16x32_bf16 v[140:143], v[188:191], a[72:75], v[140:143]\n"
"v_subrev_f32 v124, s22, v124\n"
"v_subrev_f32 v125, s22, v125\n"
"v_mfma_f32_16x16x32_bf16 v[140:143], v[184:187], a[68:71], v[140:143]\n"
"s_add_u32 s40, s41, s52\n"
"v_subrev_f32 v126, s22, v126\n"
"v_subrev_f32 v127, s22, v127\n"
"v_mfma_f32_16x16x32_bf16 v[140:143], v[180:183], a[64:67], v[140:143]\n"
"buffer_load_dwordx4 v240, s[28:31], s40 offen offset:1024 lds\n"
"v_mfma_f32_16x16x32_bf16 v[156:159], v[180:183], a[80:83], v[156:159]\n"
"v_cvt_pk_bf16_f32 v124, v124, v125\n"
"v_cvt_pk_bf16_f32 v125, v126, v127\n"
"v_mfma_f32_16x16x32_bf16 v[156:159], v[184:187], a[84:87], v[156:159]\n"
"v_mfma_f32_16x16x32_bf16 v[156:159], v[188:191], a[88:91], v[156:159]\n"
"v_mfma_f32_16x16x32_bf16 v[156:159], v[192:195], a[92:95], v[156:159]\n"
"v_mfma_f32_16x16x32_bf16 v[156:159], v[208:211], a[32:35], v[156:159]\n"
"v_mfma_f32_16x16x32_bf16 v[156:159], v[212:215], a[36:39], v[156:159]\n"
"v_subrev_f32 v140, s22, v140\n"
"v_subrev_f32 v141, s22, v141\n"
"v_mfma_f32_16x16x32_bf16 v[156:159], v[216:219], a[40:43], v[156:159]\n"
"s_waitcnt vmcnt(7)\n"
"v_subrev_f32 v142, s22, v142\n"
"v_subrev_f32 v143, s22, v143\n"
"v_mfma_f32_16x16x32_bf16 v[156:159], v[220:223], a[44:47], v[156:159]\n"
"s_barrier\n"
"buffer_store_dword v241, v248, s[84:87], 0 offen sc1\n"
"v_mfma_f32_16x16x32_bf16 v[172:175], v[220:223], a[228:231], v[172:175]\n"
"ds_read_b64_tr_b16 v[220:221], v234 offset:57344\n"
"v_cvt_pk_bf16_f32 v140, v140, v141\n"
"v_mfma_f32_16x16x32_bf16 v[172:175], v[216:219], a[224:227], v[172:175]\n"
"ds_read_b64_tr_b16 v[216:217], v230 offset:57344\n"
"v_cvt_pk_bf16_f32 v141, v142, v143\n"
"v_mfma_f32_16x16x32_bf16 v[172:175], v[212:215], a[212:215], v[172:175]\n"
"ds_read_b64_tr_b16 v[212:213], v234 offset:49152\n"
"v_add_u32 v241, 1, v241\n"
"v_mfma_f32_16x16x32_bf16 v[172:175], v[208:211], a[208:211], v[172:175]\n"
"buffer_load_dwordx4 v[84:87], v243, s[32:35], s18 offen offset:3072 sc1\n"
"v_mfma_f32_16x16x32_bf16 v[172:175], v[192:195], a[236:239], v[172:175]\n"
"ds_read_b64_tr_b16 v[192:193], v234 offset:40960\n"
"v_mfma_f32_16x16x32_bf16 v[172:175], v[188:191], a[232:235], v[172:175]\n"
"ds_read_b64_tr_b16 v[188:189], v230 offset:40960\n"
"v_subrev_f32 v156, s22, v156\n"
"v_mfma_f32_16x16x32_bf16 v[172:175], v[184:187], a[220:223], v[172:175]\n"
"s_add_u32 s40, s41, s53\n"
"ds_read_b64_tr_b16 v[184:185], v234 offset:32768\n"
"v_subrev_f32 v157, s22, v157\n"
"v_mfma_f32_16x16x32_bf16 v[172:175], v[180:183], a[216:219], v[172:175]\n"
"buffer_load_dwordx4 v240, s[28:31], s40 offen offset:2048 lds\n"
"v_mfma_f32_16x16x32_bf16 v[128:131], v[96:99], a[48:51], v[128:131]\n"
"s_add_u32 s16, s90, s62\n"
"ds_read_b64_tr_b16 v[180:181], v230 offset:32768\n"
"v_subrev_f32 v158, s22, v158\n"
"v_mfma_f32_16x16x32_bf16 v[128:131], v[100:103], a[52:55], v[128:131]\n"
"s_waitcnt lgkmcnt(5)\n"
"s_add_u32 s17, s16, 4096\n"
"ds_read_b64_tr_b16 v[182:183], v230 offset:34816\n"
"v_subrev_f32 v159, s22, v159\n"
"v_mfma_f32_16x16x32_bf16 v[128:131], v[104:107], a[56:59], v[128:131]\n"
"s_lshl_b32 s62, s70, 9\n"
"ds_read_b64_tr_b16 v[186:187], v234 offset:34816\n"
"v_cvt_pk_bf16_f32 v156, v156, v157\n"
"v_mfma_f32_16x16x32_bf16 v[128:131], v[108:111], a[60:63], v[128:131]\n"
"ds_read_b64_tr_b16 v[190:191], v230 offset:43008\n"
"v_cvt_pk_bf16_f32 v157, v158, v159\n"
"v_mfma_f32_16x16x32_bf16 v[128:131], v[112:115], a[0:3], v[128:131]\n"
"ds_read_b64_tr_b16 v[194:195], v234 offset:43008\n"
"v_lshlrev_b32 v202, 16, v72\n"
"v_mfma_f32_16x16x32_bf16 v[128:131], v[116:119], a[4:7], v[128:131]\n"
"ds_read_b64_tr_b16 v[208:209], v230 offset:49152\n"
"v_and_b32 v72, s73, v72\n"
"v_mfma_f32_16x16x32_bf16 v[128:131], v[120:123], a[8:11], v[128:131]\n"
"s_add_u32 s40, s41, s54\n"
"ds_read_b64_tr_b16 v[210:211], v230 offset:51200\n"
"v_add_f32 v0, v0, v202\n"
"v_mfma_f32_16x16x32_bf16 v[128:131], v[224:227], a[12:15], v[128:131]\n"
"buffer_load_dwordx4 v240, s[28:31], s40 offen offset:3072 lds\n"
"v_mfma_f32_16x16x32_bf16 v[144:147], v[224:227], a[28:31], v[144:147]\n"
"ds_read_b64_tr_b16 v[214:215], v234 offset:51200\n"
"v_add_f32 v1, v1, v72\n"
"v_mfma_f32_16x16x32_bf16 v[144:147], v[120:123], a[24:27], v[144:147]\n"
"ds_read_b64_tr_b16 v[218:219], v230 offset:59392\n"
"v_lshlrev_b32 v203, 16, v73\n"
"v_mfma_f32_16x16x32_bf16 v[144:147], v[116:119], a[20:23], v[144:147]\n"
"ds_read_b64_tr_b16 v[222:223], v234 offset:59392\n"
"v_and_b32 v73, s73, v73\n"
"v_mfma_f32_16x16x32_bf16 v[144:147], v[112:115], a[16:19], v[144:147]\n"
"buffer_load_dwordx4 v[88:91], v243, s[32:35], s19 offen sc1\n"
"v_mfma_f32_16x16x32_bf16 v[144:147], v[108:111], a[76:79], v[144:147]\n"
"v_add_f32 v2, v2, v203\n"
"v_add_f32 v3, v3, v73\n"
"v_mfma_f32_16x16x32_bf16 v[144:147], v[104:107], a[72:75], v[144:147]\n"
"s_add_u32 s41, s70, 491520\n"
"v_subrev_f32 v128, s22, v128\n"
"v_subrev_f32 v129, s22, v129\n"
"v_mfma_f32_16x16x32_bf16 v[144:147], v[100:103], a[68:71], v[144:147]\n"
"s_add_u32 m0, s71, 49152\n"
"v_subrev_f32 v130, s22, v130\n"
"v_subrev_f32 v131, s22, v131\n"
"v_mfma_f32_16x16x32_bf16 v[144:147], v[96:99], a[64:67], v[144:147]\n"
"buffer_load_dwordx4 v240, s[28:31], s41 offen lds\n"
"v_mfma_f32_16x16x32_bf16 v[160:163], v[96:99], a[80:83], v[160:163]\n"
"s_and_b32 s70, s88, 15\n"
"v_cvt_pk_bf16_f32 v126, v128, v129\n"
"v_cvt_pk_bf16_f32 v127, v130, v131\n"
"v_mfma_f32_16x16x32_bf16 v[160:163], v[100:103], a[84:87], v[160:163]\n"
"s_lshr_b32 s88, s88, 4\n"
"v_lshlrev_b32 v204, 16, v74\n"
"v_and_b32 v74, s73, v74\n"
"v_mfma_f32_16x16x32_bf16 v[160:163], v[104:107], a[88:91], v[160:163]\n"
"s_lshl_b32 s70, s70, 7\n"
"v_add_f32 v4, v4, v204\n"
"v_add_f32 v5, v5, v74\n"
"v_mfma_f32_16x16x32_bf16 v[160:163], v[108:111], a[92:95], v[160:163]\n"
"s_xor_b32 s71, s71, s75\n"
"v_lshlrev_b32 v205, 16, v75\n"
"v_and_b32 v75, s73, v75\n"
"v_mfma_f32_16x16x32_bf16 v[160:163], v[112:115], a[32:35], v[160:163]\n"
"v_add_f32 v6, v6, v205\n"
"v_add_f32 v7, v7, v75\n"
"v_mfma_f32_16x16x32_bf16 v[160:163], v[116:119], a[36:39], v[160:163]\n"
"v_subrev_f32 v144, s22, v144\n"
"v_subrev_f32 v145, s22, v145\n"
"v_mfma_f32_16x16x32_bf16 v[160:163], v[120:123], a[40:43], v[160:163]\n"
"v_subrev_f32 v146, s22, v146\n"
"v_subrev_f32 v147, s22, v147\n"
"v_mfma_f32_16x16x32_bf16 v[160:163], v[224:227], a[44:47], v[160:163]\n"
"buffer_store_dwordx4 v[124:127], v243, s[32:35], s16 offen sc1\n"
"v_cvt_pk_bf16_f32 v142, v144, v145\n"
"v_mfma_f32_16x16x32_bf16 v[176:179], v[224:227], a[228:231], v[176:179]\n"
"ds_read_b64_tr_b16 v[224:225], v235 offset:57344\n"
"v_cvt_pk_bf16_f32 v143, v146, v147\n"
"v_mfma_f32_16x16x32_bf16 v[176:179], v[120:123], a[224:227], v[176:179]\n"
"ds_read_b64_tr_b16 v[120:121], v231 offset:57344\n"
"v_lshlrev_b32 v206, 16, v76\n"
"v_mfma_f32_16x16x32_bf16 v[176:179], v[116:119], a[212:215], v[176:179]\n"
"ds_read_b64_tr_b16 v[116:117], v235 offset:49152\n"
"v_and_b32 v76, s73, v76\n"
"v_mfma_f32_16x16x32_bf16 v[176:179], v[112:115], a[208:211], v[176:179]\n"
"buffer_load_dwordx4 v[92:95], v243, s[32:35], s19 offen offset:1024 sc1\n"
"v_mfma_f32_16x16x32_bf16 v[176:179], v[108:111], a[236:239], v[176:179]\n"
"ds_read_b64_tr_b16 v[108:109], v235 offset:40960\n"
"v_add_f32 v8, v8, v206\n"
"v_mfma_f32_16x16x32_bf16 v[176:179], v[104:107], a[232:235], v[176:179]\n"
"ds_read_b64_tr_b16 v[104:105], v231 offset:40960\n"
"v_subrev_f32 v160, s22, v160\n"
"v_mfma_f32_16x16x32_bf16 v[176:179], v[100:103], a[220:223], v[176:179]\n"
"s_add_u32 s40, s41, s52\n"
"ds_read_b64_tr_b16 v[100:101], v235 offset:32768\n"
"v_subrev_f32 v161, s22, v161\n"
"v_mfma_f32_16x16x32_bf16 v[176:179], v[96:99], a[216:219], v[176:179]\n"
"s_waitcnt vmcnt(14) lgkmcnt(6)\n"
"buffer_load_dwordx4 v240, s[28:31], s40 offen offset:1024 lds\n"
"v_mfma_f32_16x16x32_bf16 v[132:135], v[180:183], a[48:51], v[132:135]\n"
"ds_read_b64_tr_b16 v[96:97], v231 offset:32768\n"
"v_subrev_f32 v162, s22, v162\n"
"v_mfma_f32_16x16x32_bf16 v[132:135], v[184:187], a[52:55], v[132:135]\n"
"ds_read_b64_tr_b16 v[98:99], v231 offset:34816\n"
"v_subrev_f32 v163, s22, v163\n"
"v_mfma_f32_16x16x32_bf16 v[132:135], v[188:191], a[56:59], v[132:135]\n"
"ds_read_b64_tr_b16 v[102:103], v235 offset:34816\n"
"v_cvt_pk_bf16_f32 v158, v160, v161\n"
"v_mfma_f32_16x16x32_bf16 v[132:135], v[192:195], a[60:63], v[132:135]\n"
"ds_read_b64_tr_b16 v[106:107], v231 offset:43008\n"
"v_cvt_pk_bf16_f32 v159, v162, v163\n"
"v_mfma_f32_16x16x32_bf16 v[132:135], v[208:211], a[0:3], v[132:135]\n"
"ds_read_b64_tr_b16 v[110:111], v235 offset:43008\n"
"v_add_f32 v9, v9, v76\n"
"v_mfma_f32_16x16x32_bf16 v[132:135], v[212:215], a[4:7], v[132:135]\n"
"ds_read_b64_tr_b16 v[112:113], v231 offset:49152\n"
"v_lshlrev_b32 v207, 16, v77\n"
"v_mfma_f32_16x16x32_bf16 v[132:135], v[216:219], a[8:11], v[132:135]\n"
"ds_read_b64_tr_b16 v[114:115], v231 offset:51200\n"
"v_and_b32 v77, s73, v77\n"
"v_mfma_f32_16x16x32_bf16 v[132:135], v[220:223], a[12:15], v[132:135]\n"
"buffer_store_dwordx4 v[140:143], v243, s[32:35], s16 offen offset:2048 sc1\n"
"v_add_f32 v10, v10, v207\n"
"v_mfma_f32_16x16x32_bf16 v[148:151], v[220:223], a[28:31], v[148:151]\n"
"ds_read_b64_tr_b16 v[118:119], v235 offset:51200\n"
"v_add_f32 v11, v11, v77\n"
"v_mfma_f32_16x16x32_bf16 v[148:151], v[216:219], a[24:27], v[148:151]\n"
"ds_read_b64_tr_b16 v[122:123], v231 offset:59392\n"
"v_lshlrev_b32 v202, 16, v78\n"
"v_mfma_f32_16x16x32_bf16 v[148:151], v[212:215], a[20:23], v[148:151]\n"
"ds_read_b64_tr_b16 v[226:227], v235 offset:59392\n"
"v_and_b32 v78, s73, v78\n"
"v_mfma_f32_16x16x32_bf16 v[148:151], v[208:211], a[16:19], v[148:151]\n"
"v_add_f32 v12, v12, v202\n"
"v_add_f32 v13, v13, v78\n"
"v_mfma_f32_16x16x32_bf16 v[148:151], v[192:195], a[76:79], v[148:151]\n"
"v_lshlrev_b32 v203, 16, v79\n"
"v_and_b32 v79, s73, v79\n"
"v_mfma_f32_16x16x32_bf16 v[148:151], v[188:191], a[72:75], v[148:151]\n"
"v_add_f32 v14, v14, v203\n"
"v_add_f32 v15, v15, v79\n"
"v_mfma_f32_16x16x32_bf16 v[148:151], v[184:187], a[68:71], v[148:151]\n"
"s_add_u32 s40, s41, s53\n"
"v_lshlrev_b32 v204, 16, v80\n"
"v_and_b32 v80, s73, v80\n"
"v_mfma_f32_16x16x32_bf16 v[148:151], v[180:183], a[64:67], v[148:151]\n"
"buffer_load_dwordx4 v240, s[28:31], s40 offen offset:2048 lds\n"
"v_mfma_f32_16x16x32_bf16 v[164:167], v[180:183], a[80:83], v[164:167]\n"
"v_add_f32 v16, v16, v204\n"
"v_add_f32 v17, v17, v80\n"
"v_mfma_f32_16x16x32_bf16 v[164:167], v[184:187], a[84:87], v[164:167]\n"
"v_lshlrev_b32 v205, 16, v81\n"
"v_and_b32 v81, s73, v81\n"
"v_mfma_f32_16x16x32_bf16 v[164:167], v[188:191], a[88:91], v[164:167]\n"
"v_add_f32 v18, v18, v205\n"
"v_add_f32 v19, v19, v81\n"
"v_mfma_f32_16x16x32_bf16 v[164:167], v[192:195], a[92:95], v[164:167]\n"
"buffer_load_dwordx4 v[244:247], v243, s[32:35], s19 offen offset:2048 sc1\n"
"v_mfma_f32_16x16x32_bf16 v[164:167], v[208:211], a[32:35], v[164:167]\n"
"s_lshl_b32 s18, s72, 4\n"
"v_lshlrev_b32 v206, 16, v82\n"
"v_and_b32 v82, s73, v82\n"
"v_mfma_f32_16x16x32_bf16 v[164:167], v[212:215], a[36:39], v[164:167]\n"
"s_lshl_b32 s19, s72, 2\n"
"v_add_f32 v20, v20, v206\n"
"v_add_f32 v21, v21, v82\n"
"v_mfma_f32_16x16x32_bf16 v[164:167], v[216:219], a[40:43], v[164:167]\n"
"s_waitcnt vmcnt(14)\n"
"s_lshr_b32 s19, s89, s19\n"
"v_lshlrev_b32 v207, 16, v83\n"
"v_mfma_f32_16x16x32_bf16 v[164:167], v[220:223], a[44:47], v[164:167]\n"
"s_barrier\n"
"s_and_b32 s19, s19, 1\n"
"s_waitcnt lgkmcnt(0)\n"
"v_mfma_f32_16x16x32_bf16 v[136:139], v[96:99], a[48:51], v[136:139]\n"
"s_lshl3_add_u32 s18, s18, s19\n"
"ds_read_b64_tr_b16 v[180:181], v228 offset:0\n"
"v_and_b32 v83, s73, v83\n"
"v_mfma_f32_16x16x32_bf16 v[136:139], v[100:103], a[52:55], v[136:139]\n"
"s_lshl_b32 s18, s18, 13\n"
"ds_read_b64_tr_b16 v[182:183], v228 offset:2048\n"
"v_add_f32 v22, v22, v207\n"
"v_mfma_f32_16x16x32_bf16 v[136:139], v[104:107], a[56:59], v[136:139]\n"
"s_add_u32 s18, s18, s91\n"
"v_lshlrev_b32_e64 v201, 2, s72\n"
"ds_read_b64_tr_b16 v[184:185], v232 offset:0\n"
"v_mfma_f32_16x16x32_bf16 v[136:139], v[108:111], a[60:63], v[136:139]\n"
"s_add_u32 s19, s18, 4096\n"
"global_load_dword v200, v201, s[6:7] sc1\n"
"v_mfma_f32_16x16x32_bf16 v[136:139], v[112:115], a[0:3], v[136:139]\n"
"s_mov_b32 s69, 15\n"
"ds_read_b64_tr_b16 v[186:187], v232 offset:2048\n"
"v_add_f32 v23, v23, v83\n"
"v_mfma_f32_16x16x32_bf16 v[136:139], v[116:119], a[4:7], v[136:139]\n"
"s_add_u32 s21, s21, 1\n"
"ds_read_b64_tr_b16 v[188:189], v228 offset:8192\n"
"v_subrev_f32 v172, s22, v172\n"
"v_mfma_f32_16x16x32_bf16 v[136:139], v[120:123], a[8:11], v[136:139]\n"
"ds_read_b64_tr_b16 v[190:191], v228 offset:10240\n"
"v_subrev_f32 v173, s22, v173\n"
"v_mfma_f32_16x16x32_bf16 v[136:139], v[224:227], a[12:15], v[136:139]\n"
"buffer_store_dwordx4 v[156:159], v243, s[32:35], s17 offen sc1\n"
"v_subrev_f32 v174, s22, v174\n"
"v_mfma_f32_16x16x32_bf16 v[152:155], v[224:227], a[28:31], v[152:155]\n"
"ds_read_b64_tr_b16 v[192:193], v232 offset:8192\n"
"v_subrev_f32 v175, s22, v175\n"
"v_mfma_f32_16x16x32_bf16 v[152:155], v[120:123], a[24:27], v[152:155]\n"
"ds_read_b64_tr_b16 v[194:195], v232 offset:10240\n"
"v_cvt_pk_bf16_f32 v172, v172, v173\n"
"v_mfma_f32_16x16x32_bf16 v[152:155], v[116:119], a[20:23], v[152:155]\n"
"ds_read_b64_tr_b16 v[208:209], v228 offset:16384\n"
"v_cvt_pk_bf16_f32 v173, v174, v175\n"
"v_mfma_f32_16x16x32_bf16 v[152:155], v[112:115], a[16:19], v[152:155]\n"
"ds_read_b64_tr_b16 v[210:211], v228 offset:18432\n"
"v_subrev_f32 v176, s22, v176\n"
"v_mfma_f32_16x16x32_bf16 v[152:155], v[108:111], a[76:79], v[152:155]\n"
"ds_read_b64_tr_b16 v[212:213], v232 offset:16384\n"
"v_subrev_f32 v177, s22, v177\n"
"v_mfma_f32_16x16x32_bf16 v[152:155], v[104:107], a[72:75], v[152:155]\n"
"ds_read_b64_tr_b16 v[214:215], v232 offset:18432\n"
"v_subrev_f32 v178, s22, v178\n"
"v_mfma_f32_16x16x32_bf16 v[152:155], v[100:103], a[68:71], v[152:155]\n"
"s_add_u32 s40, s41, s54\n"
"ds_read_b64_tr_b16 v[216:217], v228 offset:24576\n"
"v_subrev_f32 v179, s22, v179\n"
"v_mfma_f32_16x16x32_bf16 v[152:155], v[96:99], a[64:67], v[152:155]\n"
"buffer_load_dwordx4 v240, s[28:31], s40 offen offset:3072 lds\n"
"v_mfma_f32_16x16x32_bf16 v[168:171], v[96:99], a[80:83], v[168:171]\n"
"ds_read_b64_tr_b16 v[218:219], v228 offset:26624\n"
"v_cvt_pk_bf16_f32 v174, v176, v177\n"
"v_mfma_f32_16x16x32_bf16 v[168:171], v[100:103], a[84:87], v[168:171]\n"
"ds_read_b64_tr_b16 v[220:221], v232 offset:24576\n"
"v_cvt_pk_bf16_f32 v175, v178, v179\n"
"v_mfma_f32_16x16x32_bf16 v[168:171], v[104:107], a[88:91], v[168:171]\n"
"ds_read_b64_tr_b16 v[222:223], v232 offset:26624\n"
"v_subrev_f32 v132, s22, v132\n"
"v_mfma_f32_16x16x32_bf16 v[168:171], v[108:111], a[92:95], v[168:171]\n"
"ds_read_b64_tr_b16 v[96:97], v229 offset:0\n"
"v_subrev_f32 v133, s22, v133\n"
"v_mfma_f32_16x16x32_bf16 v[168:171], v[112:115], a[32:35], v[168:171]\n"
"ds_read_b64_tr_b16 v[98:99], v229 offset:2048\n"
"v_subrev_f32 v134, s22, v134\n"
"v_mfma_f32_16x16x32_bf16 v[168:171], v[116:119], a[36:39], v[168:171]\n"
"ds_read_b64_tr_b16 v[100:101], v233 offset:0\n"
"v_subrev_f32 v135, s22, v135\n"
"v_mfma_f32_16x16x32_bf16 v[168:171], v[120:123], a[40:43], v[168:171]\n"
"s_add_u32 s47, s47, 1\n"
"ds_read_b64_tr_b16 v[102:103], v233 offset:2048\n"
"v_cvt_pk_bf16_f32 v132, v132, v133\n"
"v_mfma_f32_16x16x32_bf16 v[168:171], v[224:227], a[44:47], v[168:171]\n"
"s_waitcnt lgkmcnt(7)\n"
"s_cmp_lg_u32 s47, 9\n"
"ds_read_b64_tr_b16 v[104:105], v229 offset:8192\n"
"s_cbranch_scc1 LGL_%=\n"
"v_mfma_f32_16x16x32_bf16 v[0:3], v[180:183], a[96:99], v[0:3]\n"
"v_xor_b32 v235, s75, v235\n"
"v_subrev_f32 v136, s22, v136\n"
"v_mfma_f32_16x16x32_bf16 v[0:3], v[184:187], a[100:103], v[0:3]\n"
"ds_read_b64_tr_b16 v[114:115], v229 offset:18432\n"
"v_cvt_pk_bf16_f32 v133, v134, v135\n"
"v_mfma_f32_16x16x32_bf16 v[0:3], v[188:191], a[104:107], v[0:3]\n"
"v_mov_b32 v201, v237\n"
"v_subrev_f32 v138, s22, v138\n"
"v_subrev_f32 v139, s22, v139\n"
"v_mfma_f32_16x16x32_bf16 v[0:3], v[192:195], a[108:111], v[0:3]\n"
"global_load_dword v200, v201, s[6:7] sc1\n"
"v_mfma_f32_16x16x32_bf16 v[0:3], v[208:211], a[144:147], v[0:3]\n"
"v_subrev_f32 v167, s22, v167\n"
"v_cvt_pk_bf16_f32 v135, v138, v139\n"
"v_mfma_f32_16x16x32_bf16 v[0:3], v[212:215], a[148:151], v[0:3]\n"
"s_waitcnt lgkmcnt(3)\n"
"v_subrev_f32 v148, s22, v148\n"
"v_subrev_f32 v137, s22, v137\n"
"v_mfma_f32_16x16x32_bf16 v[0:3], v[216:219], a[152:155], v[0:3]\n"
"v_subrev_f32 v165, s22, v165\n"
"v_subrev_f32 v168, s22, v168\n"
"v_mfma_f32_16x16x32_bf16 v[0:3], v[220:223], a[156:159], v[0:3]\n"
"buffer_store_dwordx4 v[172:175], v243, s[32:35], s17 offen offset:2048 sc1\n"
"v_subrev_f32 v151, s22, v151\n"
"v_cvt_pk_bf16_f32 v134, v136, v137\n"
"v_mfma_f32_16x16x32_bf16 v[16:19], v[220:223], a[172:175], v[16:19]\n"
"buffer_store_dwordx4 v[132:135], v243, s[32:35], s16 offen offset:1024 sc1\n"
"v_subrev_f32 v149, s22, v149\n"
"v_subrev_f32 v150, s22, v150\n"
"v_subrev_f32 v164, s22, v164\n"
"v_cvt_pk_bf16_f32 v164, v164, v165\n"
"v_subrev_f32 v153, s22, v153\n"
"v_subrev_f32 v155, s22, v155\n"
"v_mfma_f32_16x16x32_bf16 v[16:19], v[216:219], a[168:171], v[16:19]\n"
"v_cvt_pk_bf16_f32 v148, v148, v149\n"
"v_subrev_f32 v154, s22, v154\n"
"v_mfma_f32_16x16x32_bf16 v[16:19], v[212:215], a[164:167], v[16:19]\n"
"v_cvt_pk_bf16_f32 v149, v150, v151\n"
"v_subrev_f32 v170, s22, v170\n"
"v_mfma_f32_16x16x32_bf16 v[16:19], v[208:211], a[160:163], v[16:19]\n"
"v_subrev_f32 v166, s22, v166\n"
"v_subrev_f32 v171, s22, v171\n"
"v_mfma_f32_16x16x32_bf16 v[16:19], v[192:195], a[124:127], v[16:19]\n"
"v_cvt_pk_bf16_f32 v165, v166, v167\n"
"v_cvt_pk_bf16_f32 v167, v170, v171\n"
"v_mfma_f32_16x16x32_bf16 v[16:19], v[188:191], a[120:123], v[16:19]\n"
"v_subrev_f32 v152, s22, v152\n"
"v_cvt_pk_bf16_f32 v151, v154, v155\n"
"v_mfma_f32_16x16x32_bf16 v[16:19], v[184:187], a[116:119], v[16:19]\n"
"v_cvt_pk_bf16_f32 v150, v152, v153\n"
"v_subrev_f32 v169, s22, v169\n"
"v_mfma_f32_16x16x32_bf16 v[16:19], v[180:183], a[112:115], v[16:19]\n"
"s_waitcnt vmcnt(16)\n"
"buffer_store_dwordx4 v[148:151], v243, s[32:35], s16 offen offset:3072 sc1\n"
"v_cvt_pk_bf16_f32 v166, v168, v169\n"
"v_mfma_f32_16x16x32_bf16 v[32:35], v[180:183], a[128:131], v[32:35]\n"
"buffer_store_dwordx4 v[164:167], v243, s[32:35], s17 offen offset:1024 sc1\n"
"ds_read_b64_tr_b16 v[120:121], v229 offset:24576\n"
"v_mfma_f32_16x16x32_bf16 v[32:35], v[184:187], a[132:135], v[32:35]\n"
"ds_read_b64_tr_b16 v[224:225], v233 offset:24576\n"
"v_xor_b32 v234, s75, v234\n"
"v_mfma_f32_16x16x32_bf16 v[32:35], v[188:191], a[136:139], v[32:35]\n"
"ds_read_b64_tr_b16 v[122:123], v229 offset:26624\n"
"v_lshlrev_b32 v203, 16, v85\n"
"v_mfma_f32_16x16x32_bf16 v[32:35], v[192:195], a[140:143], v[32:35]\n"
"ds_read_b64_tr_b16 v[108:109], v233 offset:8192\n"
"v_lshlrev_b32 v202, 16, v84\n"
"v_mfma_f32_16x16x32_bf16 v[32:35], v[208:211], a[176:179], v[32:35]\n"
"ds_read_b64_tr_b16 v[226:227], v233 offset:26624\n"
"v_and_b32 v84, s73, v84\n"
"v_mfma_f32_16x16x32_bf16 v[32:35], v[212:215], a[180:183], v[32:35]\n"
"ds_read_b64_tr_b16 v[110:111], v233 offset:10240\n"
"v_bfe_u32 v161, v242, 8, 2\n"
"v_mfma_f32_16x16x32_bf16 v[32:35], v[216:219], a[184:187], v[32:35]\n"
"ds_read_b64_tr_b16 v[106:107], v229 offset:10240\n"
"v_lshlrev_b32 v205, 16, v87\n"
"v_mfma_f32_16x16x32_bf16 v[32:35], v[220:223], a[188:191], v[32:35]\n"
"v_xor_b32 v230, s75, v230\n"
"v_lshlrev_b32 v204, 16, v86\n"
"v_mfma_f32_16x16x32_bf16 v[48:51], v[220:223], a[252:255], v[48:51]\n"
"ds_read_b64_tr_b16 v[220:221], v234 offset:24576\n"
"v_add_f32 v26, v26, v203\n"
"v_mfma_f32_16x16x32_bf16 v[48:51], v[216:219], a[248:251], v[48:51]\n"
"ds_read_b64_tr_b16 v[112:113], v229 offset:16384\n"
"v_bfe_u32 v160, v242, 6, 2\n"
"v_mfma_f32_16x16x32_bf16 v[48:51], v[212:215], a[244:247], v[48:51]\n"
"ds_read_b64_tr_b16 v[116:117], v233 offset:16384\n"
"v_add_f32 v24, v24, v202\n"
"v_mfma_f32_16x16x32_bf16 v[48:51], v[208:211], a[240:243], v[48:51]\n"
"ds_read_b64_tr_b16 v[118:119], v233 offset:18432\n"
"v_add_f32 v25, v25, v84\n"
"v_mfma_f32_16x16x32_bf16 v[48:51], v[192:195], a[204:207], v[48:51]\n"
"ds_read_b64_tr_b16 v[210:211], v230 offset:18432\n"
"v_add_f32 v28, v28, v204\n"
"v_mfma_f32_16x16x32_bf16 v[48:51], v[188:191], a[200:203], v[48:51]\n"
"ds_read_b64_tr_b16 v[188:189], v230 offset:8192\n"
"v_and_b32 v86, s73, v86\n"
"v_mfma_f32_16x16x32_bf16 v[48:51], v[184:187], a[196:199], v[48:51]\n"
"ds_read_b64_tr_b16 v[194:195], v234 offset:10240\n"
"v_mov_b32 v164, 0\n"
"v_mfma_f32_16x16x32_bf16 v[48:51], v[180:183], a[192:195], v[48:51]\n"
"s_waitcnt vmcnt(15)\n"
"v_xor_b32 v160, v160, v161\n"
"v_lshlrev_b32 v202, 16, v90\n"
"v_mfma_f32_16x16x32_bf16 v[4:7], v[96:99], a[96:99], v[4:7]\n"
"s_waitcnt lgkmcnt(6)\n"
"ds_read_b64_tr_b16 v[222:223], v234 offset:26624\n"
"v_lshlrev_b32 v203, 16, v91\n"
"v_mfma_f32_16x16x32_bf16 v[4:7], v[100:103], a[100:103], v[4:7]\n"
"ds_read_b64_tr_b16 v[184:185], v234 offset:0\n"
"v_lshlrev_b32 v206, 16, v88\n"
"v_mfma_f32_16x16x32_bf16 v[4:7], v[104:107], a[104:107], v[4:7]\n"
"ds_read_b64_tr_b16 v[216:217], v230 offset:24576\n"
"v_mfma_f32_16x16x32_bf16 v[4:7], v[108:111], a[108:111], v[4:7]\n"
"s_waitcnt lgkmcnt(6)\n"
"ds_read_b64_tr_b16 v[218:219], v230 offset:26624\n"
"v_and_b32 v91, s73, v91\n"
"v_mfma_f32_16x16x32_bf16 v[4:7], v[112:115], a[144:147], v[4:7]\n"
"s_waitcnt vmcnt(4)\n"
"ds_read_b64_tr_b16 v[190:191], v230 offset:10240\n"
"v_lshlrev_b32 v207, 16, v89\n"
"v_mfma_f32_16x16x32_bf16 v[4:7], v[116:119], a[148:151], v[4:7]\n"
"v_subrev_u32 v200, s21, v200\n"
"ds_read_b64_tr_b16 v[208:209], v230 offset:16384\n"
"v_mfma_f32_16x16x32_bf16 v[4:7], v[120:123], a[152:155], v[4:7]\n"
"v_cmp_lt_u32 vcc, s69, v200\n"
"ds_read_b64_tr_b16 v[182:183], v230 offset:2048\n"
"v_mfma_f32_16x16x32_bf16 v[4:7], v[224:227], a[156:159], v[4:7]\n"
"ds_read_b64_tr_b16 v[180:181], v230 offset:0\n"
"s_cbranch_vccnz LGQ_%=\nLGQK_%=:\n"
"v_mfma_f32_16x16x32_bf16 v[20:23], v[224:227], a[172:175], v[20:23]\n"
"s_bfe_u32 s20, s89, 65536\ns_lshl_b32 s20, s20, 13\ns_add_u32 s20, s20, 0\n"
"ds_read_b64_tr_b16 v[214:215], v234 offset:18432\n"
"v_and_b32 v85, s73, v85\n"
"v_mfma_f32_16x16x32_bf16 v[20:23], v[120:123], a[168:171], v[20:23]\n"
"buffer_load_dwordx4 v[124:127], v236, s[32:35], s20 offen sc1\n"
"v_mfma_f32_16x16x32_bf16 v[20:23], v[116:119], a[164:167], v[20:23]\n"
"v_and_b32 v90, s73, v90\n"
"v_add_f32 v37, v37, v90\n"
"v_mfma_f32_16x16x32_bf16 v[20:23], v[112:115], a[160:163], v[20:23]\n"
"s_bfe_u32 s20, s89, 65540\ns_lshl_b32 s20, s20, 13\ns_add_u32 s20, s20, 1048576\n"
"v_add_f32 v36, v36, v202\n"
"v_add_f32 v38, v38, v203\n"
"v_mfma_f32_16x16x32_bf16 v[20:23], v[108:111], a[124:127], v[20:23]\n"
"buffer_load_dwordx4 v[128:131], v236, s[32:35], s20 offen sc1\n"
"v_mfma_f32_16x16x32_bf16 v[20:23], v[104:107], a[120:123], v[20:23]\n"
"v_add_u32 v160, -1, v160\n"
"v_add_f32 v39, v39, v91\n"
"v_mfma_f32_16x16x32_bf16 v[20:23], v[100:103], a[116:119], v[20:23]\n"
"s_bfe_u32 s20, s89, 65544\ns_lshl_b32 s20, s20, 13\ns_add_u32 s20, s20, 2097152\n"
"v_add_f32 v32, v32, v206\n"
"v_mfma_f32_16x16x32_bf16 v[20:23], v[96:99], a[112:115], v[20:23]\n"
"buffer_load_dwordx4 v[132:135], v236, s[32:35], s20 offen sc1\n"
"v_mfma_f32_16x16x32_bf16 v[36:39], v[96:99], a[128:131], v[36:39]\n"
"v_lshlrev_b32 v204, 16, v92\n"
"v_add_f32 v40, v40, v204\n"
"v_mfma_f32_16x16x32_bf16 v[36:39], v[100:103], a[132:135], v[36:39]\n"
"s_bfe_u32 s20, s89, 65548\ns_lshl_b32 s20, s20, 13\ns_add_u32 s20, s20, 3145728\n"
"ds_read_b64_tr_b16 v[212:213], v234 offset:16384\n"
"v_add_f32 v30, v30, v205\n"
"v_mfma_f32_16x16x32_bf16 v[36:39], v[104:107], a[136:139], v[36:39]\n"
"buffer_load_dwordx4 v[136:139], v236, s[32:35], s20 offen sc1\n"
"v_mfma_f32_16x16x32_bf16 v[36:39], v[108:111], a[140:143], v[36:39]\n"
"v_and_b32 v92, s73, v92\n"
"v_lshlrev_b32 v206, 16, v94\n"
"v_mfma_f32_16x16x32_bf16 v[36:39], v[112:115], a[176:179], v[36:39]\n"
"s_bfe_u32 s20, s89, 65552\ns_lshl_b32 s20, s20, 13\ns_add_u32 s20, s20, 4194304\n"
"ds_read_b64_tr_b16 v[186:187], v234 offset:2048\n"
"ds_read_b64_tr_b16 v[192:193], v234 offset:8192\n"
"v_mfma_f32_16x16x32_bf16 v[36:39], v[116:119], a[180:183], v[36:39]\n"
"buffer_load_dwordx4 v[140:143], v236, s[32:35], s20 offen sc1\n"
"v_mfma_f32_16x16x32_bf16 v[36:39], v[120:123], a[184:187], v[36:39]\n"
"v_and_b32 v94, s73, v94\n"
"v_mfma_f32_16x16x32_bf16 v[36:39], v[224:227], a[188:191], v[36:39]\n"
"s_bfe_u32 s20, s89, 65556\ns_lshl_b32 s20, s20, 13\ns_add_u32 s20, s20, 5242880\n"
"v_add_f32 v34, v34, v207\n"
"v_lshlrev_b32 v205, 16, v93\n"
"v_mfma_f32_16x16x32_bf16 v[52:55], v[224:227], a[252:255], v[52:55]\n"
"buffer_load_dwordx4 v[144:147], v236, s[32:35], s20 offen sc1\n"
"v_mfma_f32_16x16x32_bf16 v[52:55], v[120:123], a[248:251], v[52:55]\n"
"v_ashrrev_i32 v160, 31, v160\n"
"v_add_f32 v44, v44, v206\n"
"v_mfma_f32_16x16x32_bf16 v[52:55], v[116:119], a[244:247], v[52:55]\n"
"s_bfe_u32 s20, s89, 65560\ns_lshl_b32 s20, s20, 13\ns_add_u32 s20, s20, 6291456\n"
"v_xor_b32 v231, s75, v231\n"
"v_lshlrev_b32 v207, 16, v95\n"
"v_mfma_f32_16x16x32_bf16 v[52:55], v[112:115], a[240:243], v[52:55]\n"
"buffer_load_dwordx4 v[148:151], v236, s[32:35], s20 offen sc1\n"
"v_mfma_f32_16x16x32_bf16 v[52:55], v[108:111], a[204:207], v[52:55]\n"
"s_bfe_u32 s20, s89, 65564\ns_lshl_b32 s20, s20, 13\ns_add_u32 s20, s20, 7340032\n"
"v_and_b32 v87, s73, v87\n"
"v_and_b32 v95, s73, v95\n"
"v_mfma_f32_16x16x32_bf16 v[52:55], v[104:107], a[200:203], v[52:55]\n"
"buffer_load_dwordx4 v[152:155], v236, s[32:35], s20 offen sc1\n"
"v_mfma_f32_16x16x32_bf16 v[52:55], v[100:103], a[196:199], v[52:55]\n"
"s_waitcnt vmcnt(8)\n"
"s_add_u32 s21, s21, 1\n"
"v_and_b32 v88, s73, v88\n"
"v_mfma_f32_16x16x32_bf16 v[52:55], v[96:99], a[192:195], v[52:55]\n"
"s_barrier\n"
"s_mov_b32 s52, 40960\n"
"buffer_store_dword v241, v248, s[84:87], 0 offen sc1\n"
"s_waitcnt lgkmcnt(0)\n"
"v_mfma_f32_16x16x32_bf16 v[8:11], v[180:183], a[96:99], v[8:11]\n"
"s_mov_b32 s53, 81920\n"
"ds_read_b64_tr_b16 v[98:99], v231 offset:2048\n"
"v_add_f32 v42, v42, v205\n"
"v_mfma_f32_16x16x32_bf16 v[8:11], v[184:187], a[100:103], v[8:11]\n"
"ds_read_b64_tr_b16 v[112:113], v231 offset:16384\n"
"v_add_f32 v31, v31, v87\n"
"v_mfma_f32_16x16x32_bf16 v[8:11], v[188:191], a[104:107], v[8:11]\n"
"v_add_f32 v29, v29, v86\n"
"v_and_b32 v161, 16, v242\n"
"v_mfma_f32_16x16x32_bf16 v[8:11], v[192:195], a[108:111], v[8:11]\n"
"ds_read_b64_tr_b16 v[118:119], v235 offset:18432\n"
"v_and_b32 v160, 0x3f80, v160\n"
"v_mfma_f32_16x16x32_bf16 v[8:11], v[208:211], a[144:147], v[8:11]\n"
"ds_read_b64_tr_b16 v[102:103], v235 offset:2048\n"
"v_add_f32 v27, v27, v85\n"
"v_mfma_f32_16x16x32_bf16 v[8:11], v[212:215], a[148:151], v[8:11]\n"
"ds_read_b64_tr_b16 v[106:107], v231 offset:10240\n"
"v_lshlrev_b32 v160, v161, v160\n"
"v_mfma_f32_16x16x32_bf16 v[8:11], v[216:219], a[152:155], v[8:11]\n"
"ds_read_b64_tr_b16 v[96:97], v231 offset:0\n"
"v_and_b32 v93, s73, v93\n"
"v_mfma_f32_16x16x32_bf16 v[8:11], v[220:223], a[156:159], v[8:11]\n"
"ds_read_b64_tr_b16 v[108:109], v235 offset:8192\n"
"v_add_f32 v41, v41, v92\n"
"v_mfma_f32_16x16x32_bf16 v[24:27], v[220:223], a[172:175], v[24:27]\n"
"v_bfe_i32 v161, v242, 5, 1\n"
"v_lshlrev_b32 v206, 16, v244\n"
"v_mfma_f32_16x16x32_bf16 v[24:27], v[216:219], a[168:171], v[24:27]\n"
"ds_read_b64_tr_b16 v[114:115], v231 offset:18432\n"
"v_mfma_f32_16x16x32_bf16 v[24:27], v[212:215], a[164:167], v[24:27]\n"
"ds_read_b64_tr_b16 v[104:105], v231 offset:8192\n"
"v_lshlrev_b32 v203, 16, v247\n"
"v_mfma_f32_16x16x32_bf16 v[24:27], v[208:211], a[160:163], v[24:27]\n"
"ds_read_b64_tr_b16 v[116:117], v235 offset:16384\n"
"v_add_f32 v48, v48, v206\n"
"v_mfma_f32_16x16x32_bf16 v[24:27], v[192:195], a[124:127], v[24:27]\n"
"ds_read_b64_tr_b16 v[110:111], v235 offset:10240\n"
"v_and_b32 v89, s73, v89\n"
"v_mfma_f32_16x16x32_bf16 v[24:27], v[188:191], a[120:123], v[24:27]\n"
"v_add_f32 v45, v45, v94\n"
"v_add_f32 v47, v47, v95\n"
"v_mfma_f32_16x16x32_bf16 v[24:27], v[184:187], a[116:119], v[24:27]\n"
"ds_read_b64_tr_b16 v[100:101], v235 offset:0\n"
"v_add_f32 v43, v43, v93\n"
"v_mfma_f32_16x16x32_bf16 v[24:27], v[180:183], a[112:115], v[24:27]\n"
"ds_read_b64_tr_b16 v[122:123], v231 offset:26624\n"
"v_add_f32 v46, v46, v207\n"
"v_mfma_f32_16x16x32_bf16 v[40:43], v[180:183], a[128:131], v[40:43]\n"
"v_add_f32 v33, v33, v88\n"
"v_and_b32 v244, s73, v244\n"
"v_mfma_f32_16x16x32_bf16 v[40:43], v[184:187], a[132:135], v[40:43]\n"
"ds_read_b64_tr_b16 v[120:121], v231 offset:24576\n"
"v_lshlrev_b32 v207, 16, v245\n"
"v_mfma_f32_16x16x32_bf16 v[40:43], v[188:191], a[136:139], v[40:43]\n"
"v_and_b32 v163, v160, v161\n"
"v_lshlrev_b32 v202, 16, v246\n"
"v_mfma_f32_16x16x32_bf16 v[40:43], v[192:195], a[140:143], v[40:43]\n"
"ds_read_b64_tr_b16 v[224:225], v235 offset:24576\n"
"v_and_b32 v246, s73, v246\n"
"v_mfma_f32_16x16x32_bf16 v[40:43], v[208:211], a[176:179], v[40:43]\n"
"v_add_f32 v52, v52, v202\n"
"v_and_b32 v247, s73, v247\n"
"v_mfma_f32_16x16x32_bf16 v[40:43], v[212:215], a[180:183], v[40:43]\n"
"ds_read_b64_tr_b16 v[226:227], v235 offset:26624\n"
"v_and_b32 v245, s73, v245\n"
"v_mfma_f32_16x16x32_bf16 v[40:43], v[216:219], a[184:187], v[40:43]\n"
"v_mov_b32 v161, 0\n"
"v_add_f32 v54, v54, v203\n"
"v_mfma_f32_16x16x32_bf16 v[40:43], v[220:223], a[188:191], v[40:43]\n"
"s_barrier\n"
"s_waitcnt lgkmcnt(3)\n"
"v_mfma_f32_16x16x32_bf16 v[12:15], v[96:99], a[96:99], v[12:15]\n"
"ds_read_b64_tr_b16 v[184:185], v232 offset:32768\n"
"v_add_f32 v49, v49, v244\n"
"v_mfma_f32_16x16x32_bf16 v[12:15], v[100:103], a[100:103], v[12:15]\n"
"ds_read_b64_tr_b16 v[216:217], v228 offset:57344\n"
"v_mfma_f32_16x16x32_bf16 v[12:15], v[104:107], a[104:107], v[12:15]\n"
"ds_read_b64_tr_b16 v[188:189], v228 offset:40960\n"
"v_add_f32 v35, v35, v89\n"
"v_mfma_f32_16x16x32_bf16 v[12:15], v[108:111], a[108:111], v[12:15]\n"
"ds_read_b64_tr_b16 v[190:191], v228 offset:43008\n"
"v_mfma_f32_16x16x32_bf16 v[12:15], v[112:115], a[144:147], v[12:15]\n"
"ds_read_b64_tr_b16 v[192:193], v232 offset:40960\n"
"v_add_f32 v53, v53, v246\n"
"v_mfma_f32_16x16x32_bf16 v[12:15], v[116:119], a[148:151], v[12:15]\n"
"s_waitcnt lgkmcnt(5)\n"
"ds_read_b64_tr_b16 v[212:213], v232 offset:49152\n"
"v_add_f32 v51, v51, v245\n"
"v_mfma_f32_16x16x32_bf16 v[12:15], v[120:123], a[152:155], v[12:15]\n"
"ds_read_b64_tr_b16 v[182:183], v228 offset:34816\n"
"v_xor_b32 v162, v160, v163\n"
"v_mfma_f32_16x16x32_bf16 v[12:15], v[224:227], a[156:159], v[12:15]\n"
"global_load_dword v200, v201, s[6:7] sc1\n"
"v_mfma_f32_16x16x32_bf16 v[28:31], v[224:227], a[172:175], v[28:31]\n"
"v_add_f32 v50, v50, v207\n"
"v_add_f32 v55, v55, v247\n"
"v_mfma_f32_16x16x32_bf16 v[28:31], v[120:123], a[168:171], v[28:31]\n"
"s_waitcnt vmcnt(9)\n"
"v_lshlrev_b32 v58, 16, v125\n"
"v_lshlrev_b32 v62, 16, v127\n"
"v_mfma_f32_16x16x32_bf16 v[28:31], v[116:119], a[164:167], v[28:31]\n"
"ds_read_b64_tr_b16 v[186:187], v232 offset:34816\n"
"ds_read_b64_tr_b16 v[214:215], v232 offset:51200\n"
"v_mfma_f32_16x16x32_bf16 v[28:31], v[112:115], a[160:163], v[28:31]\n"
"ds_read_b64_tr_b16 v[194:195], v232 offset:43008\n"
"v_and_b32 v61, s73, v126\n"
"v_mfma_f32_16x16x32_bf16 v[28:31], v[108:111], a[124:127], v[28:31]\n"
"s_waitcnt vmcnt(8)\n"
"ds_read_b64_tr_b16 v[210:211], v228 offset:51200\n"
"v_and_b32 v57, s73, v124\n"
"v_mfma_f32_16x16x32_bf16 v[28:31], v[104:107], a[120:123], v[28:31]\n"
"ds_read_b64_tr_b16 v[222:223], v232 offset:59392\n"
"v_lshlrev_b32 v56, 16, v124\n"
"v_mfma_f32_16x16x32_bf16 v[28:31], v[100:103], a[116:119], v[28:31]\n"
"ds_read_b64_tr_b16 v[180:181], v228 offset:32768\n"
"v_lshlrev_b32 v205, 16, v129\n"
"v_mfma_f32_16x16x32_bf16 v[28:31], v[96:99], a[112:115], v[28:31]\n"
"s_waitcnt vmcnt(7)\n"
"v_and_b32 v59, s73, v125\n"
"v_lshlrev_b32 v206, 16, v130\n"
"v_mfma_f32_16x16x32_bf16 v[44:47], v[96:99], a[128:131], v[44:47]\n"
"ds_read_b64_tr_b16 v[220:221], v232 offset:57344\n"
"v_lshlrev_b32 v204, 16, v128\n"
"v_mfma_f32_16x16x32_bf16 v[44:47], v[100:103], a[132:135], v[44:47]\n"
"ds_read_b64_tr_b16 v[208:209], v228 offset:49152\n"
"v_and_b32 v130, s73, v130\n"
"v_mfma_f32_16x16x32_bf16 v[44:47], v[104:107], a[136:139], v[44:47]\n"
"s_waitcnt vmcnt(6)\n"
"ds_read_b64_tr_b16 v[102:103], v233 offset:34816\n"
"v_add_f32 v58, v58, v205\n"
"v_mfma_f32_16x16x32_bf16 v[44:47], v[108:111], a[140:143], v[44:47]\n"
"v_lshlrev_b32 v207, 16, v131\n"
"v_add_f32 v62, v62, v207\n"
"v_mfma_f32_16x16x32_bf16 v[44:47], v[112:115], a[176:179], v[44:47]\n"
"ds_read_b64_tr_b16 v[218:219], v228 offset:59392\n"
"v_and_b32 v131, s73, v131\n"
"v_mfma_f32_16x16x32_bf16 v[44:47], v[116:119], a[180:183], v[44:47]\n"
"s_waitcnt vmcnt(5)\n"
"ds_read_b64_tr_b16 v[96:97], v229 offset:32768\n"
"v_lshlrev_b32 v205, 16, v135\n"
"v_mfma_f32_16x16x32_bf16 v[44:47], v[120:123], a[184:187], v[44:47]\n"
"v_lshlrev_b32 v202, 16, v132\n"
"v_lshlrev_b32 v207, 16, v137\n"
"v_mfma_f32_16x16x32_bf16 v[44:47], v[224:227], a[188:191], v[44:47]\n"
"s_waitcnt lgkmcnt(5)\n"
"ds_read_b64_tr_b16 v[120:121], v229 offset:57344\n"
"v_add_f32 v56, v56, v204\n"
"v_mfma_f32_16x16x32_bf16 v[0:3], v[180:183], a[48:51], v[0:3]\n"
"s_waitcnt vmcnt(4)\n"
"ds_read_b64_tr_b16 v[116:117], v233 offset:49152\n"
"v_add_f32 v61, v61, v130\n"
"v_mfma_f32_16x16x32_bf16 v[0:3], v[184:187], a[52:55], v[0:3]\n"
"v_lshlrev_b32 v60, 16, v126\n"
"v_lshlrev_b32 v204, 16, v134\n"
"v_mfma_f32_16x16x32_bf16 v[0:3], v[188:191], a[56:59], v[0:3]\n"
"ds_read_b64_tr_b16 v[110:111], v233 offset:43008\n"
"v_add_f32 v56, v56, v202\n"
"v_mfma_f32_16x16x32_bf16 v[0:3], v[192:195], a[60:63], v[0:3]\n"
"s_waitcnt lgkmcnt(5)\n"
"s_waitcnt vmcnt(3)\n"
"ds_read_b64_tr_b16 v[112:113], v229 offset:49152\n"
"v_and_b32 v63, s73, v127\n"
"v_mfma_f32_16x16x32_bf16 v[0:3], v[208:211], a[0:3], v[0:3]\n"
"ds_read_b64_tr_b16 v[224:225], v233 offset:57344\n"
"v_and_b32 v134, s73, v134\n"
"v_mfma_f32_16x16x32_bf16 v[0:3], v[212:215], a[4:7], v[0:3]\n"
"s_waitcnt lgkmcnt(5)\n"
"s_waitcnt vmcnt(2)\n"
"ds_read_b64_tr_b16 v[108:109], v233 offset:40960\n"
"v_and_b32 v129, s73, v129\n"
"v_mfma_f32_16x16x32_bf16 v[0:3], v[216:219], a[8:11], v[0:3]\n"
"v_add_f32 v60, v60, v206\n"
"v_add_f32 v60, v60, v204\n"
"v_mfma_f32_16x16x32_bf16 v[0:3], v[220:223], a[12:15], v[0:3]\n"
"ds_read_b64_tr_b16 v[98:99], v229 offset:34816\n"
"ds_read_b64_tr_b16 v[122:123], v229 offset:59392\n"
"v_mfma_f32_16x16x32_bf16 v[16:19], v[220:223], a[28:31], v[16:19]\n"
"v_mov_b32 v165, 0\n"
"v_add_f32 v59, v59, v129\n"
"v_mfma_f32_16x16x32_bf16 v[16:19], v[216:219], a[24:27], v[16:19]\n"
"v_add_f32 v62, v62, v205\n"
"v_lshlrev_b32 v206, 16, v136\n"
"v_mfma_f32_16x16x32_bf16 v[16:19], v[212:215], a[20:23], v[16:19]\n"
"ds_read_b64_tr_b16 v[104:105], v229 offset:40960\n"
"ds_read_b64_tr_b16 v[226:227], v233 offset:59392\n"
"v_mfma_f32_16x16x32_bf16 v[16:19], v[208:211], a[16:19], v[16:19]\n"
"v_add_f32 v56, v56, v206\n"
"v_lshlrev_b32 v202, 16, v138\n"
"v_mfma_f32_16x16x32_bf16 v[16:19], v[192:195], a[76:79], v[16:19]\n"
"s_waitcnt vmcnt(0)\n"
"ds_read_b64_tr_b16 v[118:119], v233 offset:51200\n"
"v_subrev_f32 v2, s22, v2\n"
"v_mfma_f32_16x16x32_bf16 v[16:19], v[188:191], a[72:75], v[16:19]\n"
"v_subrev_u32 v200, s21, v200\n"
"ds_read_b64_tr_b16 v[114:115], v229 offset:51200\n"
"v_mfma_f32_16x16x32_bf16 v[16:19], v[184:187], a[68:71], v[16:19]\n"
"v_cmp_lt_u32 vcc, s69, v200\n"
"ds_read_b64_tr_b16 v[100:101], v233 offset:32768\n"
"v_mfma_f32_16x16x32_bf16 v[16:19], v[180:183], a[64:67], v[16:19]\n"
"v_lshlrev_b32 v206, 16, v142\n"
"s_cbranch_vccnz LGR_%=\nLGRK_%=:\n"
"v_mfma_f32_16x16x32_bf16 v[32:35], v[180:183], a[80:83], v[32:35]\n"
"v_subrev_f32 v3, s22, v3\n"
"s_cmp_eq_u32 s93, 0\ns_cbranch_scc1 LEP_%=\nLEPK_%=:\n"
"v_mfma_f32_16x16x32_bf16 v[32:35], v[184:187], a[84:87], v[32:35]\n"
"s_bfe_u32 s20, s89, 65536\ns_lshl_b32 s20, s20, 13\ns_add_u32 s20, s20, 65536\n"
"ds_read_b64_tr_b16 v[106:107], v229 offset:43008\n"
"v_and_b32 v128, s73, v128\n"
"v_mfma_f32_16x16x32_bf16 v[32:35], v[188:191], a[88:91], v[32:35]\n"
"buffer_load_dwordx4 v[72:75], v236, s[32:35], s20 offen sc1\n"
"v_mfma_f32_16x16x32_bf16 v[32:35], v[192:195], a[92:95], v[32:35]\n"
"v_lshlrev_b32 v203, 16, v133\n"
"v_lshlrev_b32 v205, 16, v141\n"
"v_mfma_f32_16x16x32_bf16 v[32:35], v[208:211], a[32:35], v[32:35]\n"
"s_bfe_u32 s20, s89, 65540\ns_lshl_b32 s20, s20, 13\ns_add_u32 s20, s20, 1114112\n"
"v_lshlrev_b32 v204, 16, v140\n"
"v_add_f32 v56, v56, v204\n"
"v_mfma_f32_16x16x32_bf16 v[32:35], v[212:215], a[36:39], v[32:35]\n"
"buffer_load_dwordx4 v[76:79], v236, s[32:35], s20 offen sc1\n"
"v_mfma_f32_16x16x32_bf16 v[32:35], v[216:219], a[40:43], v[32:35]\n"
"v_and_b32 v136, s73, v136\n"
"v_subrev_f32 v17, s22, v17\n"
"v_mfma_f32_16x16x32_bf16 v[32:35], v[220:223], a[44:47], v[32:35]\n"
"s_bfe_u32 s20, s89, 65544\ns_lshl_b32 s20, s20, 13\ns_add_u32 s20, s20, 2162688\n"
"v_add_f32 v61, v61, v134\n"
"v_and_b32 v141, s73, v141\n"
"v_mfma_f32_16x16x32_bf16 v[48:51], v[220:223], a[228:231], v[48:51]\n"
"buffer_load_dwordx4 v[80:83], v236, s[32:35], s20 offen sc1\n"
"v_mfma_f32_16x16x32_bf16 v[48:51], v[216:219], a[224:227], v[48:51]\n"
"v_add_f32 v58, v58, v203\n"
"v_subrev_f32 v19, s22, v19\n"
"v_mfma_f32_16x16x32_bf16 v[48:51], v[212:215], a[212:215], v[48:51]\n"
"s_bfe_u32 s20, s89, 65548\ns_lshl_b32 s20, s20, 13\ns_add_u32 s20, s20, 3211264\n"
"v_and_b32 v140, s73, v140\n"
"v_lshlrev_b32 v204, 16, v146\n"
"v_mfma_f32_16x16x32_bf16 v[48:51], v[208:211], a[208:211], v[48:51]\n"
"buffer_load_dwordx4 v[84:87], v236, s[32:35], s20 offen sc1\n"
"v_mfma_f32_16x16x32_bf16 v[48:51], v[192:195], a[236:239], v[48:51]\n"
"v_add_f32 v57, v57, v128\n"
"v_subrev_f32 v34, s22, v34\n"
"v_mfma_f32_16x16x32_bf16 v[48:51], v[188:191], a[232:235], v[48:51]\n"
"s_bfe_u32 s20, s89, 65552\ns_lshl_b32 s20, s20, 13\ns_add_u32 s20, s20, 4259840\n"
"ds_read_b64_tr_b16 v[214:215], v234 offset:51200\n"
"v_and_b32 v132, s73, v132\n"
"v_mfma_f32_16x16x32_bf16 v[48:51], v[184:187], a[220:223], v[48:51]\n"
"buffer_load_dwordx4 v[88:91], v236, s[32:35], s20 offen sc1\n"
"v_mfma_f32_16x16x32_bf16 v[48:51], v[180:183], a[216:219], v[48:51]\n"
"s_waitcnt lgkmcnt(1)\n"
"v_add_f32 v60, v60, v202\n"
"v_add_f32 v60, v60, v206\n"
"v_mfma_f32_16x16x32_bf16 v[4:7], v[96:99], a[48:51], v[4:7]\n"
"s_bfe_u32 s20, s89, 65556\ns_lshl_b32 s20, s20, 13\ns_add_u32 s20, s20, 5308416\n"
"v_lshlrev_b32 v206, 16, v148\n"
"v_subrev_f32 v35, s22, v35\n"
"v_mfma_f32_16x16x32_bf16 v[4:7], v[100:103], a[52:55], v[4:7]\n"
"buffer_load_dwordx4 v[92:95], v236, s[32:35], s20 offen sc1\n"
"v_mfma_f32_16x16x32_bf16 v[4:7], v[104:107], a[56:59], v[4:7]\n"
"v_add_f32 v60, v60, v204\n"
"v_subrev_f32 v0, s22, v0\n"
"v_mfma_f32_16x16x32_bf16 v[4:7], v[108:111], a[60:63], v[4:7]\n"
"s_bfe_u32 s20, s89, 65560\ns_lshl_b32 s20, s20, 13\ns_add_u32 s20, s20, 6356992\n"
"v_add_f32 v57, v57, v132\n"
"v_subrev_f32 v33, s22, v33\n"
"v_mfma_f32_16x16x32_bf16 v[4:7], v[112:115], a[0:3], v[4:7]\n"
"buffer_load_dwordx4 v[244:247], v236, s[32:35], s20 offen sc1\n"
"v_mfma_f32_16x16x32_bf16 v[4:7], v[116:119], a[4:7], v[4:7]\n"
"v_and_b32 v138, s73, v138\n"
"v_subrev_f32 v18, s22, v18\n"
"v_mfma_f32_16x16x32_bf16 v[4:7], v[120:123], a[8:11], v[4:7]\n"
"s_bfe_u32 s20, s89, 65564\ns_lshl_b32 s20, s20, 13\ns_add_u32 s20, s20, 7405568\n"
"v_and_b32 v135, s73, v135\n"
"v_subrev_f32 v49, s22, v49\n"
"v_mfma_f32_16x16x32_bf16 v[4:7], v[224:227], a[12:15], v[4:7]\n"
"buffer_load_dwordx4 v[156:159], v236, s[32:35], s20 offen sc1\n"
"v_add_f32 v58, v58, v207\n"
"v_lshlrev_b32 v203, 16, v139\n"
"v_add_f32 v58, v58, v205\n"
"v_and_b32 v142, s73, v142\n"
"v_lshlrev_b32 v202, 16, v144\n"
"v_add_f32 v56, v56, v202\n"
"v_add_f32 v56, v56, v206\n"
"v_subrev_f32 v1, s22, v1\n"
"v_mfma_f32_16x16x32_bf16 v[20:23], v[224:227], a[28:31], v[20:23]\n"
"v_add_f32 v63, v63, v131\n"
"v_lshlrev_b32 v204, 16, v152\n"
"v_mfma_f32_16x16x32_bf16 v[20:23], v[120:123], a[24:27], v[20:23]\n"
"v_and_b32 v133, s73, v133\n"
"v_add_f32 v59, v59, v133\n"
"v_mfma_f32_16x16x32_bf16 v[20:23], v[116:119], a[20:23], v[20:23]\n"
"ds_read_b64_tr_b16 v[218:219], v230 offset:59392\n"
"v_and_b32 v137, s73, v137\n"
"v_mfma_f32_16x16x32_bf16 v[20:23], v[112:115], a[16:19], v[20:23]\n"
"v_and_b32 v148, s73, v148\n"
"v_subrev_f32 v16, s22, v16\n"
"v_mfma_f32_16x16x32_bf16 v[20:23], v[108:111], a[76:79], v[20:23]\n"
"v_add_f32 v62, v62, v203\n"
"v_subrev_f32 v6, s22, v6\n"
"v_mfma_f32_16x16x32_bf16 v[20:23], v[104:107], a[72:75], v[20:23]\n"
"v_add_f32 v57, v57, v136\n"
"v_lshlrev_b32 v205, 16, v147\n"
"v_mfma_f32_16x16x32_bf16 v[20:23], v[100:103], a[68:71], v[20:23]\n"
"ds_read_b64_tr_b16 v[194:195], v234 offset:43008\n"
"v_subrev_f32 v32, s22, v32\n"
"v_mfma_f32_16x16x32_bf16 v[20:23], v[96:99], a[64:67], v[20:23]\n"
"ds_read_b64_tr_b16 v[184:185], v234 offset:32768\n"
"v_add_f32 v59, v59, v137\n"
"v_and_b32 v139, s73, v139\n"
"v_add_f32 v59, v59, v141\n"
"v_lshlrev_b32 v207, 16, v143\n"
"v_lshlrev_b32 v203, 16, v145\n"
"v_and_b32 v145, s73, v145\n"
"v_add_f32 v58, v58, v203\n"
"v_add_f32 v59, v59, v145\n"
"v_and_b32 v147, s73, v147\n"
"v_lshlrev_b32 v203, 16, v151\n"
"v_subrev_f32 v4, s22, v4\n"
"v_subrev_f32 v5, s22, v5\n"
"v_subrev_f32 v7, s22, v7\n"
"v_cvt_pk_bf16_f32 v0, v0, v1\n"
"v_cvt_pk_bf16_f32 v1, v2, v3\n"
"v_cvt_pk_bf16_f32 v3, v6, v7\n"
"v_mfma_f32_16x16x32_bf16 v[36:39], v[96:99], a[80:83], v[36:39]\n"
"v_add_f32 v56, v56, v204\n"
"v_cvt_pk_bf16_f32 v2, v4, v5\n"
"v_mfma_f32_16x16x32_bf16 v[36:39], v[100:103], a[84:87], v[36:39]\n"
"ds_read_b64_tr_b16 v[192:193], v234 offset:40960\n"
"v_add_f32 v61, v61, v138\n"
"v_mfma_f32_16x16x32_bf16 v[36:39], v[104:107], a[88:91], v[36:39]\n"
"v_add_f32 v57, v57, v140\n"
"v_subrev_f32 v23, s22, v23\n"
"v_mfma_f32_16x16x32_bf16 v[36:39], v[108:111], a[92:95], v[36:39]\n"
"ds_read_b64_tr_b16 v[180:181], v230 offset:32768\n"
"v_and_b32 v144, s73, v144\n"
"v_mfma_f32_16x16x32_bf16 v[36:39], v[112:115], a[32:35], v[36:39]\n"
"ds_read_b64_tr_b16 v[210:211], v230 offset:51200\n"
"v_add_f32 v62, v62, v207\n"
"v_add_f32 v62, v62, v205\n"
"v_lshlrev_b32 v207, 16, v149\n"
"v_add_f32 v58, v58, v207\n"
"v_lshlrev_b32 v202, 16, v150\n"
"v_permlane16_swap_b32 v0, v2\n"
"v_permlane16_swap_b32 v1, v3\n"
"v_subrev_f32 v20, s22, v20\n"
"v_cvt_pk_bf16_f32 v16, v16, v17\n"
"v_mfma_f32_16x16x32_bf16 v[36:39], v[116:119], a[36:39], v[36:39]\n"
"ds_read_b64_tr_b16 v[186:187], v234 offset:34816\n"
"v_add_f32 v63, v63, v135\n"
"v_mfma_f32_16x16x32_bf16 v[36:39], v[120:123], a[40:43], v[36:39]\n"
"ds_read_b64_tr_b16 v[188:189], v230 offset:40960\n"
"buffer_store_dwordx4 v[0:3], v238, s[36:39], 0 offen sc0 sc1\n"
"v_and_b32 v143, s73, v143\n"
"v_and_b32 v146, s73, v146\n"
"v_and_b32 v149, s73, v149\n"
"v_lshlrev_b32 v205, 16, v153\n"
"v_and_b32 v153, s73, v153\n"
"v_lshlrev_b32 v206, 16, v154\n"
"v_lshlrev_b32 v207, 16, v155\n"
"v_subrev_f32 v21, s22, v21\n"
"v_subrev_f32 v22, s22, v22\n"
"v_cvt_pk_bf16_f32 v17, v18, v19\n"
"v_cvt_pk_bf16_f32 v18, v20, v21\n"
"v_cvt_pk_bf16_f32 v32, v32, v33\n"
"v_mfma_f32_16x16x32_bf16 v[36:39], v[224:227], a[44:47], v[36:39]\n"
"ds_read_b64_tr_b16 v[182:183], v230 offset:34816\n"
"v_add_f32 v63, v63, v139\n"
"v_add_f32 v61, v61, v142\n"
"v_add_f32 v57, v57, v144\n"
"v_add_f32 v57, v57, v148\n"
"v_add_f32 v59, v59, v149\n"
"v_cvt_pk_bf16_f32 v19, v22, v23\n"
"v_permlane16_swap_b32 v16, v18\n"
"v_cvt_pk_bf16_f32 v33, v34, v35\n"
"v_mfma_f32_16x16x32_bf16 v[52:55], v[224:227], a[228:231], v[52:55]\n"
"ds_read_b64_tr_b16 v[190:191], v230 offset:43008\n"
"v_add_f32 v59, v59, v153\n"
"v_mfma_f32_16x16x32_bf16 v[52:55], v[120:123], a[224:227], v[52:55]\n"
"v_permlane16_swap_b32 v17, v19\n"
"v_mfma_f32_16x16x32_bf16 v[52:55], v[116:119], a[212:215], v[52:55]\n"
"ds_read_b64_tr_b16 v[208:209], v230 offset:49152\n"
"v_subrev_f32 v36, s22, v36\n"
"v_mfma_f32_16x16x32_bf16 v[52:55], v[112:115], a[208:211], v[52:55]\n"
"ds_read_b64_tr_b16 v[222:223], v234 offset:59392\n"
"buffer_store_dwordx4 v[16:19], v238, s[36:39], s52 offen sc0 sc1\n"
"v_add_f32 v63, v63, v143\n"
"v_add_f32 v60, v60, v202\n"
"v_add_f32 v62, v62, v203\n"
"v_and_b32 v152, s73, v152\n"
"v_subrev_f32 v38, s22, v38\n"
"v_subrev_f32 v39, s22, v39\n"
"v_mfma_f32_16x16x32_bf16 v[52:55], v[108:111], a[236:239], v[52:55]\n"
"v_and_b32 v155, s73, v155\n"
"v_cvt_pk_bf16_f32 v35, v38, v39\n"
"v_mfma_f32_16x16x32_bf16 v[52:55], v[104:107], a[232:235], v[52:55]\n"
"ds_read_b64_tr_b16 v[216:217], v230 offset:57344\n"
"v_subrev_f32 v37, s22, v37\n"
"v_mfma_f32_16x16x32_bf16 v[52:55], v[100:103], a[220:223], v[52:55]\n"
"v_and_b32 v150, s73, v150\n"
"v_and_b32 v154, s73, v154\n"
"v_mfma_f32_16x16x32_bf16 v[52:55], v[96:99], a[216:219], v[52:55]\n"
"s_waitcnt lgkmcnt(4)\n"
"ds_read_b64_tr_b16 v[212:213], v234 offset:49152\n"
"ds_read_b64_tr_b16 v[220:221], v234 offset:57344\n"
"v_add_f32 v63, v63, v147\n"
"v_and_b32 v151, s73, v151\n"
"v_add_f32 v63, v63, v151\n"
"v_add_f32 v57, v57, v152\n"
"v_add_f32 v58, v58, v205\n"
"v_add_f32 v60, v60, v206\n"
"v_add_f32 v62, v62, v207\n"
"v_cvt_pk_bf16_f32 v34, v36, v37\n"
"v_permlane16_swap_b32 v33, v35\n"
"v_mfma_f32_16x16x32_bf16 v[8:11], v[180:183], a[48:51], v[8:11]\n"
"v_cvt_pk_bf16_f32 v56, v56, v57\n"
"v_cvt_pk_bf16_f32 v57, v58, v59\n"
"v_mfma_f32_16x16x32_bf16 v[8:11], v[184:187], a[52:55], v[8:11]\n"
"s_waitcnt lgkmcnt(5)\n"
"v_permlane16_swap_b32 v32, v34\n"
"v_mfma_f32_16x16x32_bf16 v[8:11], v[188:191], a[56:59], v[8:11]\n"
"buffer_store_dwordx4 v[32:35], v238, s[36:39], s53 offen sc0 sc1\n"
"ds_read_b64_tr_b16 v[120:121], v231 offset:57344\n"
"v_add_f32 v61, v61, v146\n"
"v_add_f32 v61, v61, v150\n"
"v_add_f32 v61, v61, v154\n"
"v_add_f32 v63, v63, v155\n"
"v_cvt_pk_bf16_f32 v58, v60, v61\n"
"v_cvt_pk_bf16_f32 v59, v62, v63\n"
"v_mfma_f32_16x16x32_bf16 v[8:11], v[192:195], a[60:63], v[8:11]\n"
"s_waitcnt lgkmcnt(4)\n"
"v_permlane16_swap_b32 v57, v59\n"
"v_mfma_f32_16x16x32_bf16 v[8:11], v[208:211], a[0:3], v[8:11]\n"
"s_waitcnt lgkmcnt(2)\n"
"v_permlane16_swap_b32 v56, v58\n"
"v_mfma_f32_16x16x32_bf16 v[8:11], v[212:215], a[4:7], v[8:11]\n"
"buffer_store_dwordx4 v[56:59], v253, s[36:39], 0 offen sc0 sc1\n"
"v_subrev_f32 v48, s22, v48\n"
"v_subrev_f32 v50, s22, v50\n"
"v_subrev_f32 v51, s22, v51\n"
"v_mfma_f32_16x16x32_bf16 v[8:11], v[216:219], a[8:11], v[8:11]\n"
"s_waitcnt lgkmcnt(1)\n"
"ds_read_b64_tr_b16 v[112:113], v231 offset:49152\n"
"v_subrev_f32 v54, s22, v54\n"
"v_mfma_f32_16x16x32_bf16 v[8:11], v[220:223], a[12:15], v[8:11]\n"
"ds_read_b64_tr_b16 v[100:101], v235 offset:32768\n"
"v_subrev_f32 v52, s22, v52\n"
"v_mfma_f32_16x16x32_bf16 v[24:27], v[220:223], a[28:31], v[24:27]\n"
"ds_read_b64_tr_b16 v[102:103], v235 offset:34816\n"
"v_subrev_f32 v53, s22, v53\n"
"v_mfma_f32_16x16x32_bf16 v[24:27], v[216:219], a[24:27], v[24:27]\n"
"v_cvt_pk_bf16_f32 v48, v48, v49\n"
"v_cvt_pk_bf16_f32 v49, v50, v51\n"
"v_mfma_f32_16x16x32_bf16 v[24:27], v[212:215], a[20:23], v[24:27]\n"
"ds_read_b64_tr_b16 v[96:97], v231 offset:32768\n"
"v_cvt_pk_bf16_f32 v50, v52, v53\n"
"v_mfma_f32_16x16x32_bf16 v[24:27], v[208:211], a[16:19], v[24:27]\n"
"s_nop 0\n"
"v_permlane16_swap_b32 v48, v50\n"
"v_mfma_f32_16x16x32_bf16 v[24:27], v[192:195], a[76:79], v[24:27]\n"
"ds_read_b64_tr_b16 v[116:117], v235 offset:49152\n"
"v_subrev_f32 v55, s22, v55\n"
"v_mfma_f32_16x16x32_bf16 v[24:27], v[188:191], a[72:75], v[24:27]\n"
"ds_read_b64_tr_b16 v[98:99], v231 offset:34816\n"
"v_cvt_pk_bf16_f32 v51, v54, v55\n"
"v_mfma_f32_16x16x32_bf16 v[24:27], v[184:187], a[68:71], v[24:27]\n"
"s_nop 0\n"
"v_permlane16_swap_b32 v49, v51\n"
"v_mfma_f32_16x16x32_bf16 v[24:27], v[180:183], a[64:67], v[24:27]\n"
"buffer_store_dwordx4 v[48:51], v252, s[36:39], 0 offen sc0 sc1\n"
"ds_read_b64_tr_b16 v[104:105], v231 offset:40960\n"
"v_mfma_f32_16x16x32_bf16 v[40:43], v[180:183], a[80:83], v[40:43]\n"
"ds_read_b64_tr_b16 v[106:107], v231 offset:43008\n"
"ds_read_b64_tr_b16 v[108:109], v235 offset:40960\n"
"v_mfma_f32_16x16x32_bf16 v[40:43], v[184:187], a[84:87], v[40:43]\n"
"ds_read_b64_tr_b16 v[110:111], v235 offset:43008\n"
"v_mfma_f32_16x16x32_bf16 v[40:43], v[188:191], a[88:91], v[40:43]\n"
"ds_read_b64_tr_b16 v[114:115], v231 offset:51200\n"
"v_subrev_f32 v10, s22, v10\n"
"v_mfma_f32_16x16x32_bf16 v[40:43], v[192:195], a[92:95], v[40:43]\n"
"ds_read_b64_tr_b16 v[118:119], v235 offset:51200\n"
"v_mfma_f32_16x16x32_bf16 v[40:43], v[208:211], a[32:35], v[40:43]\n"
"ds_read_b64_tr_b16 v[122:123], v231 offset:59392\n"
"v_subrev_f32 v24, s22, v24\n"
"v_mfma_f32_16x16x32_bf16 v[40:43], v[212:215], a[36:39], v[40:43]\n"
"ds_read_b64_tr_b16 v[224:225], v235 offset:57344\n"
"ds_read_b64_tr_b16 v[226:227], v235 offset:59392\n"
"v_mfma_f32_16x16x32_bf16 v[40:43], v[216:219], a[40:43], v[40:43]\n"
"v_mfma_f32_16x16x32_bf16 v[40:43], v[220:223], a[44:47], v[40:43]\n"
"s_waitcnt lgkmcnt(8)\n"
"v_mov_b32 v160, 0\n"
"v_subrev_f32 v11, s22, v11\n"
"v_mfma_f32_16x16x32_bf16 v[12:15], v[96:99], a[48:51], v[12:15]\n"
"v_subrev_f32 v8, s22, v8\n"
"v_subrev_f32 v9, s22, v9\n"
"v_mfma_f32_16x16x32_bf16 v[12:15], v[100:103], a[52:55], v[12:15]\n"
"s_waitcnt lgkmcnt(5)\n"
"v_cvt_pk_bf16_f32 v8, v8, v9\n"
"v_mfma_f32_16x16x32_bf16 v[12:15], v[104:107], a[56:59], v[12:15]\n"
"v_subrev_f32 v27, s22, v27\n"
"v_mfma_f32_16x16x32_bf16 v[12:15], v[108:111], a[60:63], v[12:15]\n"
"s_waitcnt lgkmcnt(3)\n"
"v_mfma_f32_16x16x32_bf16 v[12:15], v[112:115], a[0:3], v[12:15]\n"
"v_mfma_f32_16x16x32_bf16 v[12:15], v[116:119], a[4:7], v[12:15]\n"
"s_waitcnt lgkmcnt(0)\n"
"v_mfma_f32_16x16x32_bf16 v[12:15], v[120:123], a[8:11], v[12:15]\n"
"v_subrev_f32 v40, s22, v40\n"
"v_mfma_f32_16x16x32_bf16 v[12:15], v[224:227], a[12:15], v[12:15]\n"
"s_waitcnt vmcnt(5)\n"
"v_mfma_f32_16x16x32_bf16 v[64:67], v[162:165], v[72:75], 0\n"
"v_mfma_f32_16x16x32_bf16 v[64:67], v[162:165], v[76:79], v[64:67]\n"
"v_cvt_pk_bf16_f32 v9, v10, v11\n"
"v_mfma_f32_16x16x32_bf16 v[64:67], v[162:165], v[80:83], v[64:67]\n"
"s_nop 2\n"
"v_subrev_f32 v12, s22, v12\n"
"v_mfma_f32_16x16x32_bf16 v[64:67], v[162:165], v[84:87], v[64:67]\n"
"v_subrev_f32 v25, s22, v25\n"
"v_mfma_f32_16x16x32_bf16 v[64:67], v[162:165], v[88:91], v[64:67]\n"
"v_subrev_f32 v13, s22, v13\n"
"v_mfma_f32_16x16x32_bf16 v[64:67], v[162:165], v[92:95], v[64:67]\n"
"v_subrev_f32 v15, s22, v15\n"
"v_mfma_f32_16x16x32_bf16 v[64:67], v[162:165], v[244:247], v[64:67]\n"
"v_subrev_f32 v14, s22, v14\n"
"v_mfma_f32_16x16x32_bf16 v[64:67], v[162:165], v[156:159], v[64:67]\n"
"v_subrev_f32 v42, s22, v42\n"
"v_mfma_f32_16x16x32_bf16 v[68:71], v[160:163], v[72:75], 0\n"
"v_cvt_pk_bf16_f32 v11, v14, v15\n"
"v_subrev_f32 v41, s22, v41\n"
"v_mfma_f32_16x16x32_bf16 v[68:71], v[160:163], v[76:79], v[68:71]\n"
"v_cvt_pk_bf16_f32 v10, v12, v13\n"
"v_cvt_pk_bf16_f32 v24, v24, v25\n"
"v_mfma_f32_16x16x32_bf16 v[68:71], v[160:163], v[80:83], v[68:71]\n"
"v_mfma_f32_16x16x32_bf16 v[68:71], v[160:163], v[84:87], v[68:71]\n"
"v_permlane16_swap_b32 v9, v11\n"
"v_mfma_f32_16x16x32_bf16 v[68:71], v[160:163], v[88:91], v[68:71]\n"
"v_mfma_f32_16x16x32_bf16 v[68:71], v[160:163], v[92:95], v[68:71]\n"
"v_cvt_pk_bf16_f32 v64, v64, v65\n"
"v_cvt_pk_bf16_f32 v40, v40, v41\n"
"v_mfma_f32_16x16x32_bf16 v[68:71], v[160:163], v[244:247], v[68:71]\n"
"v_permlane16_swap_b32 v8, v10\n"
"v_mfma_f32_16x16x32_bf16 v[68:71], v[160:163], v[156:159], v[68:71]\n"
"buffer_store_dwordx4 v[8:11], v239, s[36:39], 0 offen sc0 sc1\n"
"v_mfma_f32_16x16x32_bf16 v[28:31], v[224:227], a[28:31], v[28:31]\n"
"v_mfma_f32_16x16x32_bf16 v[28:31], v[120:123], a[24:27], v[28:31]\n"
"v_cvt_pk_bf16_f32 v65, v66, v67\n"
"v_subrev_f32 v43, s22, v43\n"
"v_mfma_f32_16x16x32_bf16 v[28:31], v[116:119], a[20:23], v[28:31]\n"
"v_mfma_f32_16x16x32_bf16 v[28:31], v[112:115], a[16:19], v[28:31]\n"
"v_mfma_f32_16x16x32_bf16 v[28:31], v[108:111], a[76:79], v[28:31]\n"
"v_cvt_pk_bf16_f32 v66, v68, v69\n"
"v_cvt_pk_bf16_f32 v67, v70, v71\n"
"v_mfma_f32_16x16x32_bf16 v[28:31], v[104:107], a[72:75], v[28:31]\n"
"v_permlane16_swap_b32 v64, v66\n"
"v_mfma_f32_16x16x32_bf16 v[28:31], v[100:103], a[68:71], v[28:31]\n"
"v_permlane16_swap_b32 v65, v67\n"
"v_mfma_f32_16x16x32_bf16 v[28:31], v[96:99], a[64:67], v[28:31]\n"
"buffer_store_dwordx4 v[64:67], v253, s[36:39], 0 offen offset:128 sc0 sc1\n"
"v_cvt_pk_bf16_f32 v41, v42, v43\n"
"v_mfma_f32_16x16x32_bf16 v[44:47], v[96:99], a[80:83], v[44:47]\n"
"v_subrev_f32 v26, s22, v26\n"
"v_cvt_pk_bf16_f32 v25, v26, v27\n"
"v_mfma_f32_16x16x32_bf16 v[44:47], v[100:103], a[84:87], v[44:47]\n"
"v_mfma_f32_16x16x32_bf16 v[44:47], v[104:107], a[88:91], v[44:47]\n"
"s_nop 0\n"
"v_subrev_f32 v30, s22, v30\n"
"v_subrev_f32 v31, s22, v31\n"
"v_mfma_f32_16x16x32_bf16 v[44:47], v[108:111], a[92:95], v[44:47]\n"
"v_subrev_f32 v28, s22, v28\n"
"v_subrev_f32 v29, s22, v29\n"
"v_mfma_f32_16x16x32_bf16 v[44:47], v[112:115], a[32:35], v[44:47]\n"
"v_cvt_pk_bf16_f32 v26, v28, v29\n"
"v_cvt_pk_bf16_f32 v27, v30, v31\n"
"v_mfma_f32_16x16x32_bf16 v[44:47], v[116:119], a[36:39], v[44:47]\n"
"s_nop 0\n"
"v_permlane16_swap_b32 v25, v27\n"
"v_mfma_f32_16x16x32_bf16 v[44:47], v[120:123], a[40:43], v[44:47]\n"
"v_permlane16_swap_b32 v24, v26\n"
"v_mfma_f32_16x16x32_bf16 v[44:47], v[224:227], a[44:47], v[44:47]\n"
"buffer_store_dwordx4 v[24:27], v239, s[36:39], s52 offen sc0 sc1\n"
"s_nop 6\n"
"v_subrev_f32 v44, s22, v44\n"
"v_subrev_f32 v45, s22, v45\n"
"v_subrev_f32 v46, s22, v46\n"
"v_subrev_f32 v47, s22, v47\n"
"v_cvt_pk_bf16_f32 v42, v44, v45\n"
"v_cvt_pk_bf16_f32 v43, v46, v47\n"
"s_nop 0\n"
"v_permlane16_swap_b32 v40, v42\n"
"v_permlane16_swap_b32 v41, v43\n"
"buffer_store_dwordx4 v[40:43], v239, s[36:39], s53 offen sc0 sc1\n"
"s_branch LFIN_%=\n"
"LSW_%=:\n"
"s_add_u32 s16, s8, s95\n"
"s_addc_u32 s17, s9, 0\n"
"s_sub_u32 s18, 655360, s95\n"
"s_add_u32 s20, s8, s100\n"
"s_addc_u32 s21, s9, 0\n"
"s_sub_u32 s22, 655360, s100\n"
"s_mov_b32 s56, 0\n"
"s_mov_b32 s55, 0\n"
"s_mov_b32 s58, 0\n"
"s_mov_b32 s57, 0\n"
"s_mov_b32 s59, 163840\n"
"s_mov_b32 s52, 9216\n"
"s_mov_b32 s53, 18432\n"
"s_mov_b32 s54, 27648\n"
"v_lshrrev_b32 v206, 7, v242\n"
"v_mul_u32_u24 v206, 1280, v206\n"
"v_lshrrev_b32 v240, 3, v242\n"
"v_and_b32 v240, 0x60, v240\n"
"v_and_b32 v207, 0x70, v242\n"
"v_xor_b32 v240, v240, v207\n"
"v_add_u32 v240, v206, v240\n"
"v_mov_b32 v207, v240\n"
"buffer_load_dword v252, v240, s[16:19], 0 offen\n"
"s_branch LSWK_%=\n"
"LGP_%=:\n"
"s_mov_b32 s80, 0x40000\n"
"LGPa_%=:\n"
"s_sleep 1\n"
"global_load_dword v200, v201, s[6:7] sc1\n"
"s_sub_u32 s80, s80, 1\n"
"s_cmp_eq_u32 s80, 0\n"
"s_waitcnt vmcnt(0)\n"
"s_cbranch_scc1 LGPK_%=\n"
"v_subrev_u32 v200, s21, v200\n"
"v_cmp_lt_u32 vcc, s69, v200\n"
"s_nop 4\n"
"s_cbranch_vccnz LGPa_%=\n"
"s_branch LGPK_%=\n"
"LGQ_%=:\n"
"s_mov_b32 s80, 0x40000\n"
"LGQa_%=:\n"
"s_sleep 1\n"
"global_load_dword v200, v201, s[6:7] sc1\n"
"s_sub_u32 s80, s80, 1\n"
"s_cmp_eq_u32 s80, 0\n"
"s_waitcnt vmcnt(0)\n"
"s_cbranch_scc1 LGQK_%=\n"
"v_subrev_u32 v200, s21, v200\n"
"v_cmp_lt_u32 vcc, s69, v200\n"
"s_nop 4\n"
"s_cbranch_vccnz LGQa_%=\n"
"s_branch LGQK_%=\n"
"LGR_%=:\n"
"s_mov_b32 s80, 0x40000\n"
"LGRa_%=:\n"
"s_sleep 1\n"
"global_load_dword v200, v201, s[6:7] sc1\n"
"s_sub_u32 s80, s80, 1\n"
"s_cmp_eq_u32 s80, 0\n"
"s_waitcnt vmcnt(0)\n"
"s_cbranch_scc1 LGRK_%=\n"
"v_subrev_u32 v200, s21, v200\n"
"v_cmp_lt_u32 vcc, s69, v200\n"
"s_nop 4\n"
"s_cbranch_vccnz LGRa_%=\n"
"s_branch LGRK_%=\n"
"LGV_%=:\n"
"s_mov_b32 s80, 0x40000\n"
"LGVa_%=:\n"
"s_sleep 1\n"
"global_load_dword v200, v201, s[6:7] sc1\n"
"s_sub_u32 s80, s80, 1\n"
"s_cmp_eq_u32 s80, 0\n"
"s_waitcnt vmcnt(0)\n"
"s_cbranch_scc1 LGVK_%=\n"
"v_subrev_u32 v200, s21, v200\n"
"v_cmp_lt_u32 vcc, s69, v200\n"
"s_nop 4\n"
"s_cbranch_vccnz LGVa_%=\n"
"s_branch LGVK_%=\n"
"LEP_%=:\n"
"s_add_u32 s92, s92, 1\n"
"v_mov_b32 v200, s92\n"
"v_mov_b32 v255, 0\n"
"s_mov_b64 exec, 1\n"
"global_store_dword v255, v200, s[6:7] offset:128 sc1\n"
"s_mov_b64 exec, -1\n"
"s_branch LEPK_%=\n"
"LPCE0_%=:\n"
"s_memrealtime s[80:81]\n"
"s_sub_u32 s82, s67, s66\n"
"s_cmp_gt_i32 s82, 0\n"
"s_cselect_b32 s83, s67, s66\n"
"s_waitcnt lgkmcnt(0)\n"
"s_mov_b32 s63, 0\n"
"s_mov_b32 s62, 118\n"
"s_sub_u32 s82, s83, s80\n"
"s_cmp_gt_i32 s82, 3\n"
"s_cbranch_scc0 LPEE0_%=\n"
"LPDE0_%=:\n"
"s_sleep 1\n"
"s_memrealtime s[80:81]\n"
"s_waitcnt lgkmcnt(0)\n"
"s_sub_u32 s82, s83, s80\n"
"s_cmp_gt_i32 s82, 3\n"
"s_cbranch_scc1 LPDE0_%=\n"
"LPEE0_%=:\n"
"s_add_u32 s66, s66, s72\n"
"s_add_u32 s66, s66, 4\n"
"s_add_u32 s67, s80, s72\n"
"s_sub_u32 s72, s72, 3\n"
"s_max_u32 s72, s72, s62\n"
"s_branch LPKE0_%=\n"
"LPCE1_%=:\n"
"s_memrealtime s[80:81]\n"
"s_sub_u32 s82, s67, s66\n"
"s_cmp_gt_i32 s82, 0\n"
"s_cselect_b32 s83, s67, s66\n"
"s_waitcnt lgkmcnt(0)\n"
"s_add_u32 s72, s62, 16\n"
"s_sub_u32 s82, s83, s80\n"
"s_cmp_gt_i32 s82, 3\n"
"s_cbranch_scc0 LPEE1_%=\n"
"LPDE1_%=:\n"
"s_sleep 1\n"
"s_memrealtime s[80:81]\n"
"s_waitcnt lgkmcnt(0)\n"
"s_sub_u32 s82, s83, s80\n"
"s_cmp_gt_i32 s82, 3\n"
"s_cbranch_scc1 LPDE1_%=\n"
"LPEE1_%=:\n"
"s_add_u32 s66, s66, s72\n"
"s_add_u32 s66, s66, 4\n"
"s_add_u32 s67, s80, s72\n"
"s_sub_u32 s72, s72, 3\n"
"s_max_u32 s72, s72, s62\n"
"s_branch LPKE1_%=\n"
"LPCT0_%=:\n"
"s_memrealtime s[80:81]\n"
"s_sub_u32 s82, s67, s66\n"
"s_cmp_gt_i32 s82, 0\n"
"s_cselect_b32 s83, s67, s66\n"
"s_waitcnt lgkmcnt(0)\n"
"s_sub_u32 s82, s83, s80\n"
"s_cmp_gt_i32 s82, 3\n"
"s_cbranch_scc0 LPET0_%=\n"
"LPDT0_%=:\n"
"s_sleep 1\n"
"s_memrealtime s[80:81]\n"
"s_waitcnt lgkmcnt(0)\n"
"s_sub_u32 s82, s83, s80\n"
"s_cmp_gt_i32 s82, 3\n"
"s_cbranch_scc1 LPDT0_%=\n"
"LPET0_%=:\n"
"s_add_u32 s66, s66, s72\n"
"s_add_u32 s66, s66, 4\n"
"s_add_u32 s67, s80, s72\n"
"s_sub_u32 s72, s72, 3\n"
"s_max_u32 s72, s72, s62\n"
"s_branch LPKT0_%=\n"
"LPCT1_%=:\n"
"s_memrealtime s[80:81]\n"
"s_sub_u32 s82, s67, s66\n"
"s_cmp_gt_i32 s82, 0\n"
"s_cselect_b32 s83, s67, s66\n"
"s_waitcnt lgkmcnt(0)\n"
"s_add_u32 s72, s62, 16\n"
"s_sub_u32 s82, s83, s80\n"
"s_cmp_gt_i32 s82, 3\n"
"s_cbranch_scc0 LPET1_%=\n"
"LPDT1_%=:\n"
"s_sleep 1\n"
"s_memrealtime s[80:81]\n"
"s_waitcnt lgkmcnt(0)\n"
"s_sub_u32 s82, s83, s80\n"
"s_cmp_gt_i32 s82, 3\n"
"s_cbranch_scc1 LPDT1_%=\n"
"LPET1_%=:\n"
"s_add_u32 s66, s66, s72\n"
"s_add_u32 s66, s66, 4\n"
"s_add_u32 s67, s80, s72\n"
"s_sub_u32 s72, s72, 3\n"
"s_max_u32 s72, s72, s62\n"
"s_branch LPKT1_%=\n"
"LPCL0_%=:\n"
"s_memrealtime s[80:81]\n"
"s_sub_u32 s82, s67, s66\n"
"s_cmp_gt_i32 s82, 0\n"
"s_cselect_b32 s83, s67, s66\n"
"s_waitcnt lgkmcnt(0)\n"
"s_sub_u32 s82, s80, s67\n"
"s_add_u32 s63, s63, 1\n"
"s_min_i32 s81, s63, 0\n"
"s_cmp_gt_i32 s82, 20\n"
"s_cselect_b32 s63, s63, s81\n"
"s_cmp_ge_i32 s63, 2\n"
"s_cbranch_scc0 LPRL0_%=\n"
"s_mov_b32 s63, -9\n"
"s_add_u32 s62, s62, 18\n"
"s_min_u32 s62, s62, 146\n"
"s_max_u32 s72, s72, s62\n"
"s_add_u32 s83, s80, 200\n"
"s_and_b32 s83, s83, -1\n"
"LPRL0_%=:\n"
"s_sub_u32 s82, s83, s80\n"
"s_cmp_gt_i32 s82, 3\n"
"s_cbranch_scc0 LPEL0_%=\n"
"LPDL0_%=:\n"
"s_sleep 1\n"
"s_memrealtime s[80:81]\n"
"s_waitcnt lgkmcnt(0)\n"
"s_sub_u32 s82, s83, s80\n"
"s_cmp_gt_i32 s82, 3\n"
"s_cbranch_scc1 LPDL0_%=\n"
"LPEL0_%=:\n"
"s_add_u32 s66, s66, s72\n"
"s_add_u32 s66, s66, 4\n"
"s_add_u32 s67, s80, s72\n"
"s_sub_u32 s72, s72, 3\n"
"s_max_u32 s72, s72, s62\n"
"s_branch LPKL0_%=\n"
"LPCL1_%=:\n"
"s_memrealtime s[80:81]\n"
"s_sub_u32 s82, s67, s66\n"
"s_cmp_gt_i32 s82, 0\n"
"s_cselect_b32 s83, s67, s66\n"
"s_waitcnt lgkmcnt(0)\n"
"s_sub_u32 s82, s80, s67\n"
"s_add_u32 s63, s63, 1\n"
"s_min_i32 s81, s63, 0\n"
"s_cmp_gt_i32 s82, 20\n"
"s_cselect_b32 s63, s63, s81\n"
"s_cmp_ge_i32 s63, 2\n"
"s_cbranch_scc0 LPRL1_%=\n"
"s_mov_b32 s63, -9\n"
"s_add_u32 s62, s62, 18\n"
"s_min_u32 s62, s62, 146\n"
"s_max_u32 s72, s72, s62\n"
"s_add_u32 s83, s80, 200\n"
"s_and_b32 s83, s83, -1\n"
"LPRL1_%=:\n"
"s_sub_u32 s82, s83, s80\n"
"s_cmp_gt_i32 s82, 3\n"
"s_cbranch_scc0 LPEL1_%=\n"
"LPDL1_%=:\n"
"s_sleep 1\n"
"s_memrealtime s[80:81]\n"
"s_waitcnt lgkmcnt(0)\n"
"s_sub_u32 s82, s83, s80\n"
"s_cmp_gt_i32 s82, 3\n"
"s_cbranch_scc1 LPDL1_%=\n"
"LPEL1_%=:\n"
"s_add_u32 s66, s66, s72\n"
"s_add_u32 s66, s66, 4\n"
"s_add_u32 s67, s80, s72\n"
"s_sub_u32 s72, s72, 3\n"
"s_max_u32 s72, s72, s62\n"
"s_branch LPKL1_%=\n"
"LPCL2_%=:\n"
"s_memrealtime s[80:81]\n"
"s_sub_u32 s82, s67, s66\n"
"s_cmp_gt_i32 s82, 0\n"
"s_cselect_b32 s83, s67, s66\n"
"s_waitcnt lgkmcnt(0)\n"
"s_sub_u32 s82, s80, s67\n"
"s_add_u32 s63, s63, 1\n"
"s_min_i32 s81, s63, 0\n"
"s_cmp_gt_i32 s82, 20\n"
"s_cselect_b32 s63, s63, s81\n"
"s_cmp_ge_i32 s63, 2\n"
"s_cbranch_scc0 LPRL2_%=\n"
"s_mov_b32 s63, -9\n"
"s_add_u32 s62, s62, 18\n"
"s_min_u32 s62, s62, 146\n"
"s_max_u32 s72, s72, s62\n"
"s_add_u32 s83, s80, 200\n"
"s_and_b32 s83, s83, -1\n"
"LPRL2_%=:\n"
"s_sub_u32 s82, s83, s80\n"
"s_cmp_gt_i32 s82, 3\n"
"s_cbranch_scc0 LPEL2_%=\n"
"LPDL2_%=:\n"
"s_sleep 1\n"
"s_memrealtime s[80:81]\n"
"s_waitcnt lgkmcnt(0)\n"
"s_sub_u32 s82, s83, s80\n"
"s_cmp_gt_i32 s82, 3\n"
"s_cbranch_scc1 LPDL2_%=\n"
"LPEL2_%=:\n"
"s_add_u32 s66, s66, s72\n"
"s_add_u32 s66, s66, 4\n"
"s_add_u32 s67, s80, s72\n"
"s_sub_u32 s72, s72, 3\n"
"s_max_u32 s72, s72, s62\n"
"s_branch LPKL2_%=\n"
"LPCL3_%=:\n"
"s_memrealtime s[80:81]\n"
"s_sub_u32 s82, s67, s66\n"
"s_cmp_gt_i32 s82, 0\n"
"s_cselect_b32 s83, s67, s66\n"
"s_waitcnt lgkmcnt(0)\n"
"s_sub_u32 s82, s80, s67\n"
"s_add_u32 s63, s63, 1\n"
"s_min_i32 s81, s63, 0\n"
"s_cmp_gt_i32 s82, 20\n"
"s_cselect_b32 s63, s63, s81\n"
"s_cmp_ge_i32 s63, 2\n"
"s_cbranch_scc0 LPRL3_%=\n"
"s_mov_b32 s63, -9\n"
"s_add_u32 s62, s62, 18\n"
"s_min_u32 s62, s62, 146\n"
"s_max_u32 s72, s72, s62\n"
"s_add_u32 s83, s80, 200\n"
"s_and_b32 s83, s83, -1\n"
"LPRL3_%=:\n"
"s_sub_u32 s82, s83, s80\n"
"s_cmp_gt_i32 s82, 3\n"
"s_cbranch_scc0 LPEL3_%=\n"
"LPDL3_%=:\n"
"s_sleep 1\n"
"s_memrealtime s[80:81]\n"
"s_waitcnt lgkmcnt(0)\n"
"s_sub_u32 s82, s83, s80\n"
"s_cmp_gt_i32 s82, 3\n"
"s_cbranch_scc1 LPDL3_%=\n"
"LPEL3_%=:\n"
"s_add_u32 s66, s66, s72\n"
"s_add_u32 s66, s66, 4\n"
"s_add_u32 s67, s80, s72\n"
"s_sub_u32 s72, s72, 3\n"
"s_max_u32 s72, s72, s62\n"
"s_branch LPKL3_%=\n"
"LPN_%=:\n"
"v_mfma_f32_16x16x32_bf16 v[112:115], a[192:195], a[136:139], v[112:115]\n"
"v_mfma_f32_16x16x32_bf16 v[112:115], a[196:199], a[140:143], v[112:115]\n"
"v_mfma_f32_16x16x32_bf16 v[116:119], a[200:203], a[136:139], v[116:119]\n"
"s_waitcnt vmcnt(11)\n"
"v_mfma_f32_16x16x32_bf16 v[116:119], a[204:207], a[140:143], v[116:119]\n"
"s_barrier\n"
"v_mfma_f32_16x16x32_bf16 v[120:123], a[208:211], a[136:139], v[120:123]\n"
"s_add_u32 s40, s48, 256\n"
"s_xor_b32 m0, s50, 0xc000\n"
"ds_read_b64_tr_b16 a[224:225], v244 offset:16384\n"
"v_mfma_f32_16x16x32_bf16 v[120:123], a[212:215], a[140:143], v[120:123]\n"
"buffer_load_dwordx4 v224, s[28:31], s40 offen nt lds\n"
"v_mfma_f32_16x16x32_bf16 v[124:127], a[216:219], a[136:139], v[124:127]\n"
"ds_read_b64_tr_b16 a[226:227], v244 offset:16896\n"
"v_mfma_f32_16x16x32_bf16 v[124:127], a[220:223], a[140:143], v[124:127]\n"
"ds_read_b64_tr_b16 a[228:229], v244 offset:24576\n"
"v_mfma_f32_16x16x32_bf16 v[140:143], a[216:219], a[144:147], v[140:143]\n"
"ds_read_b64_tr_b16 a[230:231], v244 offset:25088\n"
"v_mfma_f32_16x16x32_bf16 v[140:143], a[220:223], a[148:151], v[140:143]\n"
"ds_read_b64_tr_b16 a[232:233], v245 offset:16384\n"
"v_mfma_f32_16x16x32_bf16 v[136:139], a[208:211], a[144:147], v[136:139]\n"
"ds_read_b64_tr_b16 a[234:235], v245 offset:16896\n"
"v_mfma_f32_16x16x32_bf16 v[136:139], a[212:215], a[148:151], v[136:139]\n"
"buffer_load_dwordx4 v225, s[28:31], s40 offen offset:1024 nt lds\n"
"v_mfma_f32_16x16x32_bf16 v[132:135], a[200:203], a[144:147], v[132:135]\n"
"ds_read_b64_tr_b16 a[236:237], v245 offset:24576\n"
"v_mfma_f32_16x16x32_bf16 v[132:135], a[204:207], a[148:151], v[132:135]\n"
"ds_read_b64_tr_b16 a[238:239], v245 offset:25088\n"
"v_mfma_f32_16x16x32_bf16 v[128:131], a[192:195], a[144:147], v[128:131]\n"
"ds_read_b64_tr_b16 a[240:241], v244 offset:17408\n"
"v_mfma_f32_16x16x32_bf16 v[128:131], a[196:199], a[148:151], v[128:131]\n"
"ds_read_b64_tr_b16 a[242:243], v244 offset:17920\n"
"v_mfma_f32_16x16x32_bf16 v[144:147], a[192:195], a[152:155], v[144:147]\n"
"ds_read_b64_tr_b16 a[244:245], v244 offset:25600\n"
"v_mfma_f32_16x16x32_bf16 v[144:147], a[196:199], a[156:159], v[144:147]\n"
"buffer_load_dwordx4 v226, s[28:31], s40 offen offset:2048 nt lds\n"
"v_mfma_f32_16x16x32_bf16 v[148:151], a[200:203], a[152:155], v[148:151]\n"
"ds_read_b64_tr_b16 a[246:247], v244 offset:26112\n"
"v_mfma_f32_16x16x32_bf16 v[148:151], a[204:207], a[156:159], v[148:151]\n"
"ds_read_b64_tr_b16 a[248:249], v245 offset:17408\n"
"v_mfma_f32_16x16x32_bf16 v[152:155], a[208:211], a[152:155], v[152:155]\n"
"ds_read_b64_tr_b16 a[250:251], v245 offset:17920\n"
"v_mfma_f32_16x16x32_bf16 v[152:155], a[212:215], a[156:159], v[152:155]\n"
"ds_read_b64_tr_b16 a[252:253], v245 offset:25600\n"
"v_mfma_f32_16x16x32_bf16 v[156:159], a[216:219], a[152:155], v[156:159]\n"
"ds_read_b64_tr_b16 a[254:255], v245 offset:26112\n"
"v_mfma_f32_16x16x32_bf16 v[156:159], a[220:223], a[156:159], v[156:159]\n"
"buffer_load_dwordx4 v227, s[28:31], s40 offen offset:3072 nt lds\n"
"v_mfma_f32_16x16x32_bf16 v[172:175], a[216:219], a[160:163], v[172:175]\n"
"s_cmp_eq_u32 s47, 8\n"
"v_mfma_f32_16x16x32_bf16 v[172:175], a[220:223], a[164:167], v[172:175]\n"
"s_cselect_b32 s41, s52, s58\n"
"v_mfma_f32_16x16x32_bf16 v[168:171], a[208:211], a[160:163], v[168:171]\n"
"s_add_u32 s48, s48, s41\n"
"v_mfma_f32_16x16x32_bf16 v[168:171], a[212:215], a[164:167], v[168:171]\n"
"s_sub_u32 s41, s48, 5242880\n"
"v_mfma_f32_16x16x32_bf16 v[164:167], a[200:203], a[160:163], v[164:167]\n"
"s_min_u32 s48, s48, s41\n"
"v_mfma_f32_16x16x32_bf16 v[164:167], a[204:207], a[164:167], v[164:167]\n"
"v_xor_b32 v244, s74, v244\n"
"v_xor_b32 v246, s74, v246\n"
"v_mfma_f32_16x16x32_bf16 v[160:163], a[192:195], a[160:163], v[160:163]\n"
"s_mov_b32 m0, s50\n"
"v_xor_b32 v245, s74, v245\n"
"v_xor_b32 v247, s74, v247\n"
"v_mfma_f32_16x16x32_bf16 v[160:163], a[196:199], a[164:167], v[160:163]\n"
"buffer_load_dwordx4 v224, s[28:31], s48 offen nt lds\n"
"v_mfma_f32_16x16x32_bf16 v[176:179], a[192:195], a[168:171], v[176:179]\n"
"v_xor_b32 v252, s74, v252\n"
"v_xor_b32 v253, s74, v253\n"
"v_mfma_f32_16x16x32_bf16 v[176:179], a[196:199], a[172:175], v[176:179]\n"
"v_xor_b32 v250, s74, v250\n"
"v_xor_b32 v251, s74, v251\n"
"v_mfma_f32_16x16x32_bf16 v[180:183], a[200:203], a[168:171], v[180:183]\n"
"v_mfma_f32_16x16x32_bf16 v[180:183], a[204:207], a[172:175], v[180:183]\n"
"buffer_load_dwordx4 v225, s[28:31], s48 offen offset:1024 nt lds\n"
"v_mfma_f32_16x16x32_bf16 v[184:187], a[208:211], a[168:171], v[184:187]\n"
"v_mfma_f32_16x16x32_bf16 v[184:187], a[212:215], a[172:175], v[184:187]\n"
"v_mfma_f32_16x16x32_bf16 v[188:191], a[216:219], a[168:171], v[188:191]\n"
"v_mfma_f32_16x16x32_bf16 v[188:191], a[220:223], a[172:175], v[188:191]\n"
"v_mfma_f32_16x16x32_bf16 v[204:207], a[216:219], a[176:179], v[204:207]\n"
"v_mfma_f32_16x16x32_bf16 v[204:207], a[220:223], a[180:183], v[204:207]\n"
"buffer_load_dwordx4 v226, s[28:31], s48 offen offset:2048 nt lds\n"
"v_mfma_f32_16x16x32_bf16 v[200:203], a[208:211], a[176:179], v[200:203]\n"
"v_mfma_f32_16x16x32_bf16 v[200:203], a[212:215], a[180:183], v[200:203]\n"
"v_mfma_f32_16x16x32_bf16 v[196:199], a[200:203], a[176:179], v[196:199]\n"
"v_mfma_f32_16x16x32_bf16 v[196:199], a[204:207], a[180:183], v[196:199]\n"
"v_mfma_f32_16x16x32_bf16 v[192:195], a[192:195], a[176:179], v[192:195]\n"
"v_mfma_f32_16x16x32_bf16 v[192:195], a[196:199], a[180:183], v[192:195]\n"
"s_waitcnt lgkmcnt(0)\n"
"buffer_load_dwordx4 v227, s[28:31], s48 offen offset:3072 nt lds\n"
"v_mfma_f32_16x16x32_bf16 v[208:211], a[192:195], a[184:187], v[208:211]\n"
"s_xor_b32 s50, s50, s74\n"
"v_mfma_f32_16x16x32_bf16 v[208:211], a[196:199], a[188:191], v[208:211]\n"
"v_mfma_f32_16x16x32_bf16 v[212:215], a[200:203], a[184:187], v[212:215]\n"
"v_mfma_f32_16x16x32_bf16 v[212:215], a[204:207], a[188:191], v[212:215]\n"
"v_mfma_f32_16x16x32_bf16 v[216:219], a[208:211], a[184:187], v[216:219]\n"
"v_mfma_f32_16x16x32_bf16 v[216:219], a[212:215], a[188:191], v[216:219]\n"
"v_mfma_f32_16x16x32_bf16 v[220:223], a[216:219], a[184:187], v[220:223]\n"
"v_mfma_f32_16x16x32_bf16 v[220:223], a[220:223], a[188:191], v[220:223]\n"
"v_mfma_f32_16x16x32_bf16 v[0:3], a[224:227], a[136:139], v[0:3]\n"
"v_mfma_f32_16x16x32_bf16 v[0:3], a[228:231], a[140:143], v[0:3]\n"
"v_mfma_f32_16x16x32_bf16 v[4:7], a[232:235], a[136:139], v[4:7]\n"
"s_waitcnt vmcnt(8)\n"
"v_mfma_f32_16x16x32_bf16 v[4:7], a[236:239], a[140:143], v[4:7]\n"
"s_barrier\n"
"v_mfma_f32_16x16x32_bf16 v[8:11], a[240:243], a[136:139], v[8:11]\n"
"s_mov_b32 m0, s51\n"
"ds_read_b64_tr_b16 a[192:193], v244 offset:0\n"
"v_mfma_f32_16x16x32_bf16 v[8:11], a[244:247], a[140:143], v[8:11]\n"
"buffer_load_dword v242, s[80:83], 0 offen lds\n"
"s_add_u32 s80, s80, 680\n"
"s_addc_u32 s81, s81, 0\n"
"buffer_load_dwordx4 v248, s[24:27], s49 offen nt lds\n"
"v_mfma_f32_16x16x32_bf16 v[12:15], a[248:251], a[136:139], v[12:15]\n"
"ds_read_b64_tr_b16 a[194:195], v244 offset:512\n"
"v_mfma_f32_16x16x32_bf16 v[12:15], a[252:255], a[140:143], v[12:15]\n"
"ds_read_b128 a[136:139], v246 offset:0\n"
"v_mfma_f32_16x16x32_bf16 v[28:31], a[248:251], a[144:147], v[28:31]\n"
"ds_read_b64_tr_b16 a[196:197], v244 offset:8192\n"
"v_mfma_f32_16x16x32_bf16 v[28:31], a[252:255], a[148:151], v[28:31]\n"
"ds_read_b64_tr_b16 a[198:199], v244 offset:8704\n"
"v_mfma_f32_16x16x32_bf16 v[24:27], a[240:243], a[144:147], v[24:27]\n"
"ds_read_b128 a[140:143], v247 offset:0\n"
"v_mfma_f32_16x16x32_bf16 v[24:27], a[244:247], a[148:151], v[24:27]\n"
"buffer_load_dwordx4 v249, s[24:27], s49 offen offset:1024 nt lds\n"
"v_mfma_f32_16x16x32_bf16 v[20:23], a[232:235], a[144:147], v[20:23]\n"
"ds_read_b64_tr_b16 a[200:201], v245 offset:0\n"
"v_mfma_f32_16x16x32_bf16 v[20:23], a[236:239], a[148:151], v[20:23]\n"
"ds_read_b64_tr_b16 a[202:203], v245 offset:512\n"
"v_mfma_f32_16x16x32_bf16 v[16:19], a[224:227], a[144:147], v[16:19]\n"
"ds_read_b64_tr_b16 a[204:205], v245 offset:8192\n"
"v_mfma_f32_16x16x32_bf16 v[16:19], a[228:231], a[148:151], v[16:19]\n"
"ds_read_b64_tr_b16 a[206:207], v245 offset:8704\n"
"v_mfma_f32_16x16x32_bf16 v[32:35], a[224:227], a[152:155], v[32:35]\n"
"ds_read_b64_tr_b16 a[208:209], v244 offset:1024\n"
"v_mfma_f32_16x16x32_bf16 v[32:35], a[228:231], a[156:159], v[32:35]\n"
"buffer_load_dwordx4 v254, s[24:27], s49 offen offset:2048 nt lds\n"
"v_mfma_f32_16x16x32_bf16 v[36:39], a[232:235], a[152:155], v[36:39]\n"
"ds_read_b64_tr_b16 a[210:211], v244 offset:1536\n"
"v_mfma_f32_16x16x32_bf16 v[36:39], a[236:239], a[156:159], v[36:39]\n"
"ds_read_b64_tr_b16 a[212:213], v244 offset:9216\n"
"v_mfma_f32_16x16x32_bf16 v[40:43], a[240:243], a[152:155], v[40:43]\n"
"ds_read_b64_tr_b16 a[214:215], v244 offset:9728\n"
"v_mfma_f32_16x16x32_bf16 v[40:43], a[244:247], a[156:159], v[40:43]\n"
"ds_read_b64_tr_b16 a[216:217], v245 offset:1024\n"
"v_mfma_f32_16x16x32_bf16 v[44:47], a[248:251], a[152:155], v[44:47]\n"
"ds_read_b64_tr_b16 a[218:219], v245 offset:1536\n"
"v_mfma_f32_16x16x32_bf16 v[44:47], a[252:255], a[156:159], v[44:47]\n"
"buffer_load_dwordx4 v255, s[24:27], s49 offen offset:3072 nt lds\n"
"v_mfma_f32_16x16x32_bf16 v[60:63], a[248:251], a[160:163], v[60:63]\n"
"ds_read_b64_tr_b16 a[220:221], v245 offset:9216\n"
"v_mfma_f32_16x16x32_bf16 v[60:63], a[252:255], a[164:167], v[60:63]\n"
"ds_read_b64_tr_b16 a[222:223], v245 offset:9728\n"
"v_mfma_f32_16x16x32_bf16 v[56:59], a[240:243], a[160:163], v[56:59]\n"
"ds_read_b128 a[144:147], v246 offset:2048\n"
"v_mfma_f32_16x16x32_bf16 v[56:59], a[244:247], a[164:167], v[56:59]\n"
"s_add_u32 s40, s49, 40960\n"
"ds_read_b128 a[148:151], v247 offset:2048\n"
"v_mfma_f32_16x16x32_bf16 v[52:55], a[232:235], a[160:163], v[52:55]\n"
"s_add_u32 m0, s51, 4096\n"
"ds_read_b128 a[152:155], v246 offset:4096\n"
"v_mfma_f32_16x16x32_bf16 v[52:55], a[236:239], a[164:167], v[52:55]\n"
"buffer_load_dwordx4 v248, s[24:27], s40 offen nt lds\n"
"v_mfma_f32_16x16x32_bf16 v[48:51], a[224:227], a[160:163], v[48:51]\n"
"ds_read_b128 a[156:159], v247 offset:4096\n"
"v_mfma_f32_16x16x32_bf16 v[48:51], a[228:231], a[164:167], v[48:51]\n"
"ds_read_b128 a[160:163], v252 offset:0\n"
"v_mfma_f32_16x16x32_bf16 v[64:67], a[224:227], a[168:171], v[64:67]\n"
"ds_read_b128 a[164:167], v253 offset:0\n"
"v_mfma_f32_16x16x32_bf16 v[64:67], a[228:231], a[172:175], v[64:67]\n"
"v_mfma_f32_16x16x32_bf16 v[68:71], a[232:235], a[168:171], v[68:71]\n"
"v_mfma_f32_16x16x32_bf16 v[68:71], a[236:239], a[172:175], v[68:71]\n"
"buffer_load_dwordx4 v249, s[24:27], s40 offen offset:1024 nt lds\n"
"v_mfma_f32_16x16x32_bf16 v[72:75], a[240:243], a[168:171], v[72:75]\n"
"v_mfma_f32_16x16x32_bf16 v[72:75], a[244:247], a[172:175], v[72:75]\n"
"v_mfma_f32_16x16x32_bf16 v[76:79], a[248:251], a[168:171], v[76:79]\n"
"ds_read_b128 a[168:171], v250 offset:0\n"
"v_mfma_f32_16x16x32_bf16 v[76:79], a[252:255], a[172:175], v[76:79]\n"
"ds_read_b128 a[172:175], v251 offset:0\n"
"v_mfma_f32_16x16x32_bf16 v[92:95], a[248:251], a[176:179], v[92:95]\n"
"v_mfma_f32_16x16x32_bf16 v[92:95], a[252:255], a[180:183], v[92:95]\n"
"buffer_load_dwordx4 v254, s[24:27], s40 offen offset:2048 nt lds\n"
"v_mfma_f32_16x16x32_bf16 v[88:91], a[240:243], a[176:179], v[88:91]\n"
"s_add_u32 s49, s49, 128\n"
"v_mfma_f32_16x16x32_bf16 v[88:91], a[244:247], a[180:183], v[88:91]\n"
"s_sub_u32 s41, s49, 1280\n"
"v_mfma_f32_16x16x32_bf16 v[84:87], a[232:235], a[176:179], v[84:87]\n"
"s_min_u32 s49, s49, s41\n"
"v_mfma_f32_16x16x32_bf16 v[84:87], a[236:239], a[180:183], v[84:87]\n"
"s_xor_b32 s51, s51, s74\n"
"v_mfma_f32_16x16x32_bf16 v[80:83], a[224:227], a[176:179], v[80:83]\n"
"ds_read_b128 a[176:179], v250 offset:2048\n"
"v_mfma_f32_16x16x32_bf16 v[80:83], a[228:231], a[180:183], v[80:83]\n"
"ds_read_b128 a[180:183], v251 offset:2048\n"
"v_mfma_f32_16x16x32_bf16 v[96:99], a[224:227], a[184:187], v[96:99]\n"
"v_mfma_f32_16x16x32_bf16 v[96:99], a[228:231], a[188:191], v[96:99]\n"
"v_mfma_f32_16x16x32_bf16 v[100:103], a[232:235], a[184:187], v[100:103]\n"
"v_mfma_f32_16x16x32_bf16 v[100:103], a[236:239], a[188:191], v[100:103]\n"
"v_mfma_f32_16x16x32_bf16 v[104:107], a[240:243], a[184:187], v[104:107]\n"
"v_mfma_f32_16x16x32_bf16 v[104:107], a[244:247], a[188:191], v[104:107]\n"
"v_mfma_f32_16x16x32_bf16 v[108:111], a[248:251], a[184:187], v[108:111]\n"
"s_add_u32 s47, s47, 1\n"
"ds_read_b128 a[184:187], v250 offset:4096\n"
"v_mfma_f32_16x16x32_bf16 v[108:111], a[252:255], a[188:191], v[108:111]\n"
"s_waitcnt lgkmcnt(1)\n"
"s_cmp_lg_u32 s47, s62\n"
"ds_read_b128 a[188:191], v251 offset:4096\n"
"s_cbranch_scc1 LPN_%=\n"
"s_branch LPX_%=\n"
"LEND_%=:\n"
"LSLOW_%=:\n"
"s_waitcnt vmcnt(0) lgkmcnt(0)\n"
"s_nop 9\n"
"v_bfe_u32 v148, v242, 4, 4\n"
"v_bfe_u32 v149, v242, 8, 2\n"
"v_lshlrev_b32 v144, 8, v148\n"
"v_lshl_add_u32 v144, v149, 4, v144\n"
"v_add_u32 v144, s43, v144\n"
"v_xor_b32 v145, 64, v144\n"
"v_lshlrev_b32 v146, 1, v148\n"
"v_lshl_add_u32 v146, v149, 10, v146\n"
"v_add_u32 v146, s43, v146\n"
"v_xor_b32 v147, 64, v146\n"
"v_mov_b32 v152, 0xff7fffff\n"
"v_mov_b32 v155, 0\n"
"v_mov_b32 v0, 0\n"
"v_mov_b32 v1, 0\n"
"v_mov_b32 v2, 0\n"
"v_mov_b32 v3, 0\n"
"v_mov_b32 v4, 0\n"
"v_mov_b32 v5, 0\n"
"v_mov_b32 v6, 0\n"
"v_mov_b32 v7, 0\n"
"v_mov_b32 v8, 0\n"
"v_mov_b32 v9, 0\n"
"v_mov_b32 v10, 0\n"
"v_mov_b32 v11, 0\n"
"v_mov_b32 v12, 0\n"
"v_mov_b32 v13, 0\n"
"v_mov_b32 v14, 0\n"
"v_mov_b32 v15, 0\n"
"v_mov_b32 v16, 0\n"
"v_mov_b32 v17, 0\n"
"v_mov_b32 v18, 0\n"
"v_mov_b32 v19, 0\n"
"v_mov_b32 v20, 0\n"
"v_mov_b32 v21, 0\n"
"v_mov_b32 v22, 0\n"
"v_mov_b32 v23, 0\n"
"v_mov_b32 v24, 0\n"
"v_mov_b32 v25, 0\n"
"v_mov_b32 v26, 0\n"
"v_mov_b32 v27, 0\n"
"v_mov_b32 v28, 0\n"
"v_mov_b32 v29, 0\n"
"v_mov_b32 v30, 0\n"
"v_mov_b32 v31, 0\n"
"v_mov_b32 v156, 0xff7fffff\n"
"v_mov_b32 v159, 0\n"
"v_mov_b32 v32, 0\n"
"v_mov_b32 v33, 0\n"
"v_mov_b32 v34, 0\n"
"v_mov_b32 v35, 0\n"
"v_mov_b32 v36, 0\n"
"v_mov_b32 v37, 0\n"
"v_mov_b32 v38, 0\n"
"v_mov_b32 v39, 0\n"
"v_mov_b32 v40, 0\n"
"v_mov_b32 v41, 0\n"
"v_mov_b32 v42, 0\n"
"v_mov_b32 v43, 0\n"
"v_mov_b32 v44, 0\n"
"v_mov_b32 v45, 0\n"
"v_mov_b32 v46, 0\n"
"v_mov_b32 v47, 0\n"
"v_mov_b32 v48, 0\n"
"v_mov_b32 v49, 0\n"
"v_mov_b32 v50, 0\n"
"v_mov_b32 v51, 0\n"
"v_mov_b32 v52, 0\n"
"v_mov_b32 v53, 0\n"
"v_mov_b32 v54, 0\n"
"v_mov_b32 v55, 0\n"
"v_mov_b32 v56, 0\n"
"v_mov_b32 v57, 0\n"
"v_mov_b32 v58, 0\n"
"v_mov_b32 v59, 0\n"
"v_mov_b32 v60, 0\n"
"v_mov_b32 v61, 0\n"
"v_mov_b32 v62, 0\n"
"v_mov_b32 v63, 0\n"
"v_mov_b32 v160, 0xff7fffff\n"
"v_mov_b32 v163, 0\n"
"v_mov_b32 v64, 0\n"
"v_mov_b32 v65, 0\n"
"v_mov_b32 v66, 0\n"
"v_mov_b32 v67, 0\n"
"v_mov_b32 v68, 0\n"
"v_mov_b32 v69, 0\n"
"v_mov_b32 v70, 0\n"
"v_mov_b32 v71, 0\n"
"v_mov_b32 v72, 0\n"
"v_mov_b32 v73, 0\n"
"v_mov_b32 v74, 0\n"
"v_mov_b32 v75, 0\n"
"v_mov_b32 v76, 0\n"
"v_mov_b32 v77, 0\n"
"v_mov_b32 v78, 0\n"
"v_mov_b32 v79, 0\n"
"v_mov_b32 v80, 0\n"
"v_mov_b32 v81, 0\n"
"v_mov_b32 v82, 0\n"
"v_mov_b32 v83, 0\n"
"v_mov_b32 v84, 0\n"
"v_mov_b32 v85, 0\n"
"v_mov_b32 v86, 0\n"
"v_mov_b32 v87, 0\n"
"v_mov_b32 v88, 0\n"
"v_mov_b32 v89, 0\n"
"v_mov_b32 v90, 0\n"
"v_mov_b32 v91, 0\n"
"v_mov_b32 v92, 0\n"
"v_mov_b32 v93, 0\n"
"v_mov_b32 v94, 0\n"
"v_mov_b32 v95, 0\n"
"v_mov_b32 v164, 0xff7fffff\n"
"v_mov_b32 v167, 0\n"
"v_accvgpr_write_b32 a208, 0\n"
"v_accvgpr_write_b32 a209, 0\n"
"v_accvgpr_write_b32 a210, 0\n"
"v_accvgpr_write_b32 a211, 0\n"
"v_accvgpr_write_b32 a212, 0\n"
"v_accvgpr_write_b32 a213, 0\n"
"v_accvgpr_write_b32 a214, 0\n"
"v_accvgpr_write_b32 a215, 0\n"
"v_accvgpr_write_b32 a216, 0\n"
"v_accvgpr_write_b32 a217, 0\n"
"v_accvgpr_write_b32 a218, 0\n"
"v_accvgpr_write_b32 a219, 0\n"
"v_accvgpr_write_b32 a220, 0\n"
"v_accvgpr_write_b32 a221, 0\n"
"v_accvgpr_write_b32 a222, 0\n"
"v_accvgpr_write_b32 a223, 0\n"
"v_accvgpr_write_b32 a224, 0\n"
"v_accvgpr_write_b32 a225, 0\n"
"v_accvgpr_write_b32 a226, 0\n"
"v_accvgpr_write_b32 a227, 0\n"
"v_accvgpr_write_b32 a228, 0\n"
"v_accvgpr_write_b32 a229, 0\n"
"v_accvgpr_write_b32 a230, 0\n"
"v_accvgpr_write_b32 a231, 0\n"
"v_accvgpr_write_b32 a232, 0\n"
"v_accvgpr_write_b32 a233, 0\n"
"v_accvgpr_write_b32 a234, 0\n"
"v_accvgpr_write_b32 a235, 0\n"
"v_accvgpr_write_b32 a236, 0\n"
"v_accvgpr_write_b32 a237, 0\n"
"v_accvgpr_write_b32 a238, 0\n"
"v_accvgpr_write_b32 a239, 0\n"
"s_lshl_b32 s80, s47, 16\n"
"s_add_u32 s81, s80, 4096\n"
"s_mov_b32 s82, 0\n"
"LSLOOP_%=:\n"
"s_lshl_b32 s83, s82, 3\n"
"s_and_b32 s83, s83, 64\n"
"buffer_load_dwordx4 v[128:131], v144, s[84:87], s80 offen offset:0\n"
"buffer_load_dwordx4 v[132:135], v145, s[84:87], s80 offen offset:0\n"
"buffer_load_dwordx4 v[136:139], v144, s[84:87], s80 offen offset:128\n"
"buffer_load_dwordx4 v[140:143], v145, s[84:87], s80 offen offset:128\n"
"s_waitcnt vmcnt(0)\n"
"v_mfma_f32_16x16x32_bf16 v[96:99], v[128:131], a[0:3], 0\n"
"v_mfma_f32_16x16x32_bf16 v[96:99], v[132:135], a[4:7], v[96:99]\n"
"v_mfma_f32_16x16x32_bf16 v[96:99], v[136:139], a[8:11], v[96:99]\n"
"v_mfma_f32_16x16x32_bf16 v[96:99], v[140:143], a[12:15], v[96:99]\n"
"v_mfma_f32_16x16x32_bf16 v[104:107], v[128:131], a[16:19], 0\n"
"v_mfma_f32_16x16x32_bf16 v[104:107], v[132:135], a[20:23], v[104:107]\n"
"v_mfma_f32_16x16x32_bf16 v[104:107], v[136:139], a[24:27], v[104:107]\n"
"v_mfma_f32_16x16x32_bf16 v[104:107], v[140:143], a[28:31], v[104:107]\n"
"v_mfma_f32_16x16x32_bf16 v[112:115], v[128:131], a[32:35], 0\n"
"v_mfma_f32_16x16x32_bf16 v[112:115], v[132:135], a[36:39], v[112:115]\n"
"v_mfma_f32_16x16x32_bf16 v[112:115], v[136:139], a[40:43], v[112:115]\n"
"v_mfma_f32_16x16x32_bf16 v[112:115], v[140:143], a[44:47], v[112:115]\n"
"v_mfma_f32_16x16x32_bf16 v[120:123], v[128:131], a[192:195], 0\n"
"v_mfma_f32_16x16x32_bf16 v[120:123], v[132:135], a[196:199], v[120:123]\n"
"v_mfma_f32_16x16x32_bf16 v[120:123], v[136:139], a[200:203], v[120:123]\n"
"v_mfma_f32_16x16x32_bf16 v[120:123], v[140:143], a[204:207], v[120:123]\n"
"s_nop 7\n"
"buffer_load_dwordx4 v[128:131], v144, s[84:87], s81 offen offset:0\n"
"buffer_load_dwordx4 v[132:135], v145, s[84:87], s81 offen offset:0\n"
"buffer_load_dwordx4 v[136:139], v144, s[84:87], s81 offen offset:128\n"
"buffer_load_dwordx4 v[140:143], v145, s[84:87], s81 offen offset:128\n"
"s_waitcnt vmcnt(0)\n"
"v_mfma_f32_16x16x32_bf16 v[100:103], v[128:131], a[0:3], 0\n"
"v_mfma_f32_16x16x32_bf16 v[100:103], v[132:135], a[4:7], v[100:103]\n"
"v_mfma_f32_16x16x32_bf16 v[100:103], v[136:139], a[8:11], v[100:103]\n"
"v_mfma_f32_16x16x32_bf16 v[100:103], v[140:143], a[12:15], v[100:103]\n"
"v_mfma_f32_16x16x32_bf16 v[108:111], v[128:131], a[16:19], 0\n"
"v_mfma_f32_16x16x32_bf16 v[108:111], v[132:135], a[20:23], v[108:111]\n"
"v_mfma_f32_16x16x32_bf16 v[108:111], v[136:139], a[24:27], v[108:111]\n"
"v_mfma_f32_16x16x32_bf16 v[108:111], v[140:143], a[28:31], v[108:111]\n"
"v_mfma_f32_16x16x32_bf16 v[116:119], v[128:131], a[32:35], 0\n"
"v_mfma_f32_16x16x32_bf16 v[116:119], v[132:135], a[36:39], v[116:119]\n"
"v_mfma_f32_16x16x32_bf16 v[116:119], v[136:139], a[40:43], v[116:119]\n"
"v_mfma_f32_16x16x32_bf16 v[116:119], v[140:143], a[44:47], v[116:119]\n"
"v_mfma_f32_16x16x32_bf16 v[124:127], v[128:131], a[192:195], 0\n"
"v_mfma_f32_16x16x32_bf16 v[124:127], v[132:135], a[196:199], v[124:127]\n"
"v_mfma_f32_16x16x32_bf16 v[124:127], v[136:139], a[200:203], v[124:127]\n"
"v_mfma_f32_16x16x32_bf16 v[124:127], v[140:143], a[204:207], v[124:127]\n"
"s_nop 7\n"
"v_max3_f32 v153, v96, v97, v98\n"
"v_max3_f32 v153, v153, v99, v100\n"
"v_max3_f32 v153, v153, v101, v102\n"
"v_max3_f32 v153, v153, v103, v152\n"
"v_mov_b32 v154, v153\n"
"s_nop 1\n"
"v_permlane32_swap_b32 v153, v154\n"
"v_max_f32 v153, v153, v154\n"
"v_mov_b32 v154, v153\n"
"s_nop 1\n"
"v_permlane16_swap_b32 v153, v154\n"
"v_max_f32 v153, v153, v154\n"
"v_sub_f32 v154, v152, v153\n"
"v_exp_f32 v154, v154\n"
"v_mov_b32 v152, v153\n"
"v_sub_f32 v96, v96, v152\n"
"v_sub_f32 v97, v97, v152\n"
"v_sub_f32 v98, v98, v152\n"
"v_sub_f32 v99, v99, v152\n"
"v_sub_f32 v100, v100, v152\n"
"v_sub_f32 v101, v101, v152\n"
"v_sub_f32 v102, v102, v152\n"
"v_sub_f32 v103, v103, v152\n"
"v_exp_f32 v96, v96\n"
"v_exp_f32 v97, v97\n"
"v_exp_f32 v98, v98\n"
"v_exp_f32 v99, v99\n"
"v_exp_f32 v100, v100\n"
"v_exp_f32 v101, v101\n"
"v_exp_f32 v102, v102\n"
"v_exp_f32 v103, v103\n"
"v_mul_f32 v155, v155, v154\n"
"v_add_f32 v155, v155, v96\n"
"v_add_f32 v155, v155, v97\n"
"v_add_f32 v155, v155, v98\n"
"v_add_f32 v155, v155, v99\n"
"v_add_f32 v155, v155, v100\n"
"v_add_f32 v155, v155, v101\n"
"v_add_f32 v155, v155, v102\n"
"v_add_f32 v155, v155, v103\n"
"v_cvt_pk_bf16_f32 v96, v96, v97\n"
"v_cvt_pk_bf16_f32 v97, v98, v99\n"
"v_cvt_pk_bf16_f32 v98, v100, v101\n"
"v_cvt_pk_bf16_f32 v99, v102, v103\n"
"v_mov_b32_dpp v154, v154 row_ror:12 row_mask:0x2 bank_mask:0xf\n"
"s_nop 1\n"
"v_mov_b32_dpp v154, v154 row_ror:8 row_mask:0x4 bank_mask:0xf\n"
"s_nop 1\n"
"v_mov_b32_dpp v154, v154 row_ror:4 row_mask:0x8 bank_mask:0xf\n"
"s_nop 1\n"
"v_mul_f32_dpp v0, v154, v0 row_newbcast:0 row_mask:0xf bank_mask:0xf\n"
"v_mul_f32_dpp v1, v154, v1 row_newbcast:1 row_mask:0xf bank_mask:0xf\n"
"v_mul_f32_dpp v2, v154, v2 row_newbcast:2 row_mask:0xf bank_mask:0xf\n"
"v_mul_f32_dpp v3, v154, v3 row_newbcast:3 row_mask:0xf bank_mask:0xf\n"
"v_mul_f32_dpp v4, v154, v4 row_newbcast:0 row_mask:0xf bank_mask:0xf\n"
"v_mul_f32_dpp v5, v154, v5 row_newbcast:1 row_mask:0xf bank_mask:0xf\n"
"v_mul_f32_dpp v6, v154, v6 row_newbcast:2 row_mask:0xf bank_mask:0xf\n"
"v_mul_f32_dpp v7, v154, v7 row_newbcast:3 row_mask:0xf bank_mask:0xf\n"
"v_mul_f32_dpp v8, v154, v8 row_newbcast:0 row_mask:0xf bank_mask:0xf\n"
"v_mul_f32_dpp v9, v154, v9 row_newbcast:1 row_mask:0xf bank_mask:0xf\n"
"v_mul_f32_dpp v10, v154, v10 row_newbcast:2 row_mask:0xf bank_mask:0xf\n"
"v_mul_f32_dpp v11, v154, v11 row_newbcast:3 row_mask:0xf bank_mask:0xf\n"
"v_mul_f32_dpp v12, v154, v12 row_newbcast:0 row_mask:0xf bank_mask:0xf\n"
"v_mul_f32_dpp v13, v154, v13 row_newbcast:1 row_mask:0xf bank_mask:0xf\n"
"v_mul_f32_dpp v14, v154, v14 row_newbcast:2 row_mask:0xf bank_mask:0xf\n"
"v_mul_f32_dpp v15, v154, v15 row_newbcast:3 row_mask:0xf bank_mask:0xf\n"
"v_mul_f32_dpp v16, v154, v16 row_newbcast:0 row_mask:0xf bank_mask:0xf\n"
"v_mul_f32_dpp v17, v154, v17 row_newbcast:1 row_mask:0xf bank_mask:0xf\n"
"v_mul_f32_dpp v18, v154, v18 row_newbcast:2 row_mask:0xf bank_mask:0xf\n"
"v_mul_f32_dpp v19, v154, v19 row_newbcast:3 row_mask:0xf bank_mask:0xf\n"
"v_mul_f32_dpp v20, v154, v20 row_newbcast:0 row_mask:0xf bank_mask:0xf\n"
"v_mul_f32_dpp v21, v154, v21 row_newbcast:1 row_mask:0xf bank_mask:0xf\n"
"v_mul_f32_dpp v22, v154, v22 row_newbcast:2 row_mask:0xf bank_mask:0xf\n"
"v_mul_f32_dpp v23, v154, v23 row_newbcast:3 row_mask:0xf bank_mask:0xf\n"
"v_mul_f32_dpp v24, v154, v24 row_newbcast:0 row_mask:0xf bank_mask:0xf\n"
"v_mul_f32_dpp v25, v154, v25 row_newbcast:1 row_mask:0xf bank_mask:0xf\n"
"v_mul_f32_dpp v26, v154, v26 row_newbcast:2 row_mask:0xf bank_mask:0xf\n"
"v_mul_f32_dpp v27, v154, v27 row_newbcast:3 row_mask:0xf bank_mask:0xf\n"
"v_mul_f32_dpp v28, v154, v28 row_newbcast:0 row_mask:0xf bank_mask:0xf\n"
"v_mul_f32_dpp v29, v154, v29 row_newbcast:1 row_mask:0xf bank_mask:0xf\n"
"v_mul_f32_dpp v30, v154, v30 row_newbcast:2 row_mask:0xf bank_mask:0xf\n"
"v_mul_f32_dpp v31, v154, v31 row_newbcast:3 row_mask:0xf bank_mask:0xf\n"
"v_max3_f32 v157, v104, v105, v106\n"
"v_max3_f32 v157, v157, v107, v108\n"
"v_max3_f32 v157, v157, v109, v110\n"
"v_max3_f32 v157, v157, v111, v156\n"
"v_mov_b32 v158, v157\n"
"s_nop 1\n"
"v_permlane32_swap_b32 v157, v158\n"
"v_max_f32 v157, v157, v158\n"
"v_mov_b32 v158, v157\n"
"s_nop 1\n"
"v_permlane16_swap_b32 v157, v158\n"
"v_max_f32 v157, v157, v158\n"
"v_sub_f32 v158, v156, v157\n"
"v_exp_f32 v158, v158\n"
"v_mov_b32 v156, v157\n"
"v_sub_f32 v104, v104, v156\n"
"v_sub_f32 v105, v105, v156\n"
"v_sub_f32 v106, v106, v156\n"
"v_sub_f32 v107, v107, v156\n"
"v_sub_f32 v108, v108, v156\n"
"v_sub_f32 v109, v109, v156\n"
"v_sub_f32 v110, v110, v156\n"
"v_sub_f32 v111, v111, v156\n"
"v_exp_f32 v104, v104\n"
"v_exp_f32 v105, v105\n"
"v_exp_f32 v106, v106\n"
"v_exp_f32 v107, v107\n"
"v_exp_f32 v108, v108\n"
"v_exp_f32 v109, v109\n"
"v_exp_f32 v110, v110\n"
"v_exp_f32 v111, v111\n"
"v_mul_f32 v159, v159, v158\n"
"v_add_f32 v159, v159, v104\n"
"v_add_f32 v159, v159, v105\n"
"v_add_f32 v159, v159, v106\n"
"v_add_f32 v159, v159, v107\n"
"v_add_f32 v159, v159, v108\n"
"v_add_f32 v159, v159, v109\n"
"v_add_f32 v159, v159, v110\n"
"v_add_f32 v159, v159, v111\n"
"v_cvt_pk_bf16_f32 v104, v104, v105\n"
"v_cvt_pk_bf16_f32 v105, v106, v107\n"
"v_cvt_pk_bf16_f32 v106, v108, v109\n"
"v_cvt_pk_bf16_f32 v107, v110, v111\n"
"v_mov_b32_dpp v158, v158 row_ror:12 row_mask:0x2 bank_mask:0xf\n"
"s_nop 1\n"
"v_mov_b32_dpp v158, v158 row_ror:8 row_mask:0x4 bank_mask:0xf\n"
"s_nop 1\n"
"v_mov_b32_dpp v158, v158 row_ror:4 row_mask:0x8 bank_mask:0xf\n"
"s_nop 1\n"
"v_mul_f32_dpp v32, v158, v32 row_newbcast:0 row_mask:0xf bank_mask:0xf\n"
"v_mul_f32_dpp v33, v158, v33 row_newbcast:1 row_mask:0xf bank_mask:0xf\n"
"v_mul_f32_dpp v34, v158, v34 row_newbcast:2 row_mask:0xf bank_mask:0xf\n"
"v_mul_f32_dpp v35, v158, v35 row_newbcast:3 row_mask:0xf bank_mask:0xf\n"
"v_mul_f32_dpp v36, v158, v36 row_newbcast:0 row_mask:0xf bank_mask:0xf\n"
"v_mul_f32_dpp v37, v158, v37 row_newbcast:1 row_mask:0xf bank_mask:0xf\n"
"v_mul_f32_dpp v38, v158, v38 row_newbcast:2 row_mask:0xf bank_mask:0xf\n"
"v_mul_f32_dpp v39, v158, v39 row_newbcast:3 row_mask:0xf bank_mask:0xf\n"
"v_mul_f32_dpp v40, v158, v40 row_newbcast:0 row_mask:0xf bank_mask:0xf\n"
"v_mul_f32_dpp v41, v158, v41 row_newbcast:1 row_mask:0xf bank_mask:0xf\n"
"v_mul_f32_dpp v42, v158, v42 row_newbcast:2 row_mask:0xf bank_mask:0xf\n"
"v_mul_f32_dpp v43, v158, v43 row_newbcast:3 row_mask:0xf bank_mask:0xf\n"
"v_mul_f32_dpp v44, v158, v44 row_newbcast:0 row_mask:0xf bank_mask:0xf\n"
"v_mul_f32_dpp v45, v158, v45 row_newbcast:1 row_mask:0xf bank_mask:0xf\n"
"v_mul_f32_dpp v46, v158, v46 row_newbcast:2 row_mask:0xf bank_mask:0xf\n"
"v_mul_f32_dpp v47, v158, v47 row_newbcast:3 row_mask:0xf bank_mask:0xf\n"
"v_mul_f32_dpp v48, v158, v48 row_newbcast:0 row_mask:0xf bank_mask:0xf\n"
"v_mul_f32_dpp v49, v158, v49 row_newbcast:1 row_mask:0xf bank_mask:0xf\n"
"v_mul_f32_dpp v50, v158, v50 row_newbcast:2 row_mask:0xf bank_mask:0xf\n"
"v_mul_f32_dpp v51, v158, v51 row_newbcast:3 row_mask:0xf bank_mask:0xf\n"
"v_mul_f32_dpp v52, v158, v52 row_newbcast:0 row_mask:0xf bank_mask:0xf\n"
"v_mul_f32_dpp v53, v158, v53 row_newbcast:1 row_mask:0xf bank_mask:0xf\n"
"v_mul_f32_dpp v54, v158, v54 row_newbcast:2 row_mask:0xf bank_mask:0xf\n"
"v_mul_f32_dpp v55, v158, v55 row_newbcast:3 row_mask:0xf bank_mask:0xf\n"
"v_mul_f32_dpp v56, v158, v56 row_newbcast:0 row_mask:0xf bank_mask:0xf\n"
"v_mul_f32_dpp v57, v158, v57 row_newbcast:1 row_mask:0xf bank_mask:0xf\n"
"v_mul_f32_dpp v58, v158, v58 row_newbcast:2 row_mask:0xf bank_mask:0xf\n"
"v_mul_f32_dpp v59, v158, v59 row_newbcast:3 row_mask:0xf bank_mask:0xf\n"
"v_mul_f32_dpp v60, v158, v60 row_newbcast:0 row_mask:0xf bank_mask:0xf\n"
"v_mul_f32_dpp v61, v158, v61 row_newbcast:1 row_mask:0xf bank_mask:0xf\n"
"v_mul_f32_dpp v62, v158, v62 row_newbcast:2 row_mask:0xf bank_mask:0xf\n"
"v_mul_f32_dpp v63, v158, v63 row_newbcast:3 row_mask:0xf bank_mask:0xf\n"
"v_max3_f32 v161, v112, v113, v114\n"
"v_max3_f32 v161, v161, v115, v116\n"
"v_max3_f32 v161, v161, v117, v118\n"
"v_max3_f32 v161, v161, v119, v160\n"
"v_mov_b32 v162, v161\n"
"s_nop 1\n"
"v_permlane32_swap_b32 v161, v162\n"
"v_max_f32 v161, v161, v162\n"
"v_mov_b32 v162, v161\n"
"s_nop 1\n"
"v_permlane16_swap_b32 v161, v162\n"
"v_max_f32 v161, v161, v162\n"
"v_sub_f32 v162, v160, v161\n"
"v_exp_f32 v162, v162\n"
"v_mov_b32 v160, v161\n"
"v_sub_f32 v112, v112, v160\n"
"v_sub_f32 v113, v113, v160\n"
"v_sub_f32 v114, v114, v160\n"
"v_sub_f32 v115, v115, v160\n"
"v_sub_f32 v116, v116, v160\n"
"v_sub_f32 v117, v117, v160\n"
"v_sub_f32 v118, v118, v160\n"
"v_sub_f32 v119, v119, v160\n"
"v_exp_f32 v112, v112\n"
"v_exp_f32 v113, v113\n"
"v_exp_f32 v114, v114\n"
"v_exp_f32 v115, v115\n"
"v_exp_f32 v116, v116\n"
"v_exp_f32 v117, v117\n"
"v_exp_f32 v118, v118\n"
"v_exp_f32 v119, v119\n"
"v_mul_f32 v163, v163, v162\n"
"v_add_f32 v163, v163, v112\n"
"v_add_f32 v163, v163, v113\n"
"v_add_f32 v163, v163, v114\n"
"v_add_f32 v163, v163, v115\n"
"v_add_f32 v163, v163, v116\n"
"v_add_f32 v163, v163, v117\n"
"v_add_f32 v163, v163, v118\n"
"v_add_f32 v163, v163, v119\n"
"v_cvt_pk_bf16_f32 v112, v112, v113\n"
"v_cvt_pk_bf16_f32 v113, v114, v115\n"
"v_cvt_pk_bf16_f32 v114, v116, v117\n"
"v_cvt_pk_bf16_f32 v115, v118, v119\n"
"v_mov_b32_dpp v162, v162 row_ror:12 row_mask:0x2 bank_mask:0xf\n"
"s_nop 1\n"
"v_mov_b32_dpp v162, v162 row_ror:8 row_mask:0x4 bank_mask:0xf\n"
"s_nop 1\n"
"v_mov_b32_dpp v162, v162 row_ror:4 row_mask:0x8 bank_mask:0xf\n"
"s_nop 1\n"
"v_mul_f32_dpp v64, v162, v64 row_newbcast:0 row_mask:0xf bank_mask:0xf\n"
"v_mul_f32_dpp v65, v162, v65 row_newbcast:1 row_mask:0xf bank_mask:0xf\n"
"v_mul_f32_dpp v66, v162, v66 row_newbcast:2 row_mask:0xf bank_mask:0xf\n"
"v_mul_f32_dpp v67, v162, v67 row_newbcast:3 row_mask:0xf bank_mask:0xf\n"
"v_mul_f32_dpp v68, v162, v68 row_newbcast:0 row_mask:0xf bank_mask:0xf\n"
"v_mul_f32_dpp v69, v162, v69 row_newbcast:1 row_mask:0xf bank_mask:0xf\n"
"v_mul_f32_dpp v70, v162, v70 row_newbcast:2 row_mask:0xf bank_mask:0xf\n"
"v_mul_f32_dpp v71, v162, v71 row_newbcast:3 row_mask:0xf bank_mask:0xf\n"
"v_mul_f32_dpp v72, v162, v72 row_newbcast:0 row_mask:0xf bank_mask:0xf\n"
"v_mul_f32_dpp v73, v162, v73 row_newbcast:1 row_mask:0xf bank_mask:0xf\n"
"v_mul_f32_dpp v74, v162, v74 row_newbcast:2 row_mask:0xf bank_mask:0xf\n"
"v_mul_f32_dpp v75, v162, v75 row_newbcast:3 row_mask:0xf bank_mask:0xf\n"
"v_mul_f32_dpp v76, v162, v76 row_newbcast:0 row_mask:0xf bank_mask:0xf\n"
"v_mul_f32_dpp v77, v162, v77 row_newbcast:1 row_mask:0xf bank_mask:0xf\n"
"v_mul_f32_dpp v78, v162, v78 row_newbcast:2 row_mask:0xf bank_mask:0xf\n"
"v_mul_f32_dpp v79, v162, v79 row_newbcast:3 row_mask:0xf bank_mask:0xf\n"
"v_mul_f32_dpp v80, v162, v80 row_newbcast:0 row_mask:0xf bank_mask:0xf\n"
"v_mul_f32_dpp v81, v162, v81 row_newbcast:1 row_mask:0xf bank_mask:0xf\n"
"v_mul_f32_dpp v82, v162, v82 row_newbcast:2 row_mask:0xf bank_mask:0xf\n"
"v_mul_f32_dpp v83, v162, v83 row_newbcast:3 row_mask:0xf bank_mask:0xf\n"
"v_mul_f32_dpp v84, v162, v84 row_newbcast:0 row_mask:0xf bank_mask:0xf\n"
"v_mul_f32_dpp v85, v162, v85 row_newbcast:1 row_mask:0xf bank_mask:0xf\n"
"v_mul_f32_dpp v86, v162, v86 row_newbcast:2 row_mask:0xf bank_mask:0xf\n"
"v_mul_f32_dpp v87, v162, v87 row_newbcast:3 row_mask:0xf bank_mask:0xf\n"
"v_mul_f32_dpp v88, v162, v88 row_newbcast:0 row_mask:0xf bank_mask:0xf\n"
"v_mul_f32_dpp v89, v162, v89 row_newbcast:1 row_mask:0xf bank_mask:0xf\n"
"v_mul_f32_dpp v90, v162, v90 row_newbcast:2 row_mask:0xf bank_mask:0xf\n"
"v_mul_f32_dpp v91, v162, v91 row_newbcast:3 row_mask:0xf bank_mask:0xf\n"
"v_mul_f32_dpp v92, v162, v92 row_newbcast:0 row_mask:0xf bank_mask:0xf\n"
"v_mul_f32_dpp v93, v162, v93 row_newbcast:1 row_mask:0xf bank_mask:0xf\n"
"v_mul_f32_dpp v94, v162, v94 row_newbcast:2 row_mask:0xf bank_mask:0xf\n"
"v_mul_f32_dpp v95, v162, v95 row_newbcast:3 row_mask:0xf bank_mask:0xf\n"
"s_cmp_lg_u32 s83, s43\n"
"s_cbranch_scc1 LSK3A_%=\n"
"v_max3_f32 v165, v120, v121, v122\n"
"v_max3_f32 v165, v165, v123, v124\n"
"v_max3_f32 v165, v165, v125, v126\n"
"v_max3_f32 v165, v165, v127, v164\n"
"v_mov_b32 v166, v165\n"
"s_nop 1\n"
"v_permlane32_swap_b32 v165, v166\n"
"v_max_f32 v165, v165, v166\n"
"v_mov_b32 v166, v165\n"
"s_nop 1\n"
"v_permlane16_swap_b32 v165, v166\n"
"v_max_f32 v165, v165, v166\n"
"v_sub_f32 v166, v164, v165\n"
"v_exp_f32 v166, v166\n"
"v_mov_b32 v164, v165\n"
"v_sub_f32 v120, v120, v164\n"
"v_sub_f32 v121, v121, v164\n"
"v_sub_f32 v122, v122, v164\n"
"v_sub_f32 v123, v123, v164\n"
"v_sub_f32 v124, v124, v164\n"
"v_sub_f32 v125, v125, v164\n"
"v_sub_f32 v126, v126, v164\n"
"v_sub_f32 v127, v127, v164\n"
"v_exp_f32 v120, v120\n"
"v_exp_f32 v121, v121\n"
"v_exp_f32 v122, v122\n"
"v_exp_f32 v123, v123\n"
"v_exp_f32 v124, v124\n"
"v_exp_f32 v125, v125\n"
"v_exp_f32 v126, v126\n"
"v_exp_f32 v127, v127\n"
"v_mul_f32 v167, v167, v166\n"
"v_add_f32 v167, v167, v120\n"
"v_add_f32 v167, v167, v121\n"
"v_add_f32 v167, v167, v122\n"
"v_add_f32 v167, v167, v123\n"
"v_add_f32 v167, v167, v124\n"
"v_add_f32 v167, v167, v125\n"
"v_add_f32 v167, v167, v126\n"
"v_add_f32 v167, v167, v127\n"
"v_cvt_pk_bf16_f32 v120, v120, v121\n"
"v_cvt_pk_bf16_f32 v121, v122, v123\n"
"v_cvt_pk_bf16_f32 v122, v124, v125\n"
"v_cvt_pk_bf16_f32 v123, v126, v127\n"
"v_mov_b32_dpp v166, v166 row_ror:12 row_mask:0x2 bank_mask:0xf\n"
"s_nop 1\n"
"v_mov_b32_dpp v166, v166 row_ror:8 row_mask:0x4 bank_mask:0xf\n"
"s_nop 1\n"
"v_mov_b32_dpp v166, v166 row_ror:4 row_mask:0x8 bank_mask:0xf\n"
"v_accvgpr_read_b32 v124, a208\n"
"s_nop 1\n"
"v_mul_f32_dpp v124, v166, v124 row_newbcast:0 row_mask:0xf bank_mask:0xf\n"
"v_accvgpr_write_b32 a208, v124\n"
"v_accvgpr_read_b32 v125, a209\n"
"s_nop 1\n"
"v_mul_f32_dpp v125, v166, v125 row_newbcast:1 row_mask:0xf bank_mask:0xf\n"
"v_accvgpr_write_b32 a209, v125\n"
"v_accvgpr_read_b32 v126, a210\n"
"s_nop 1\n"
"v_mul_f32_dpp v126, v166, v126 row_newbcast:2 row_mask:0xf bank_mask:0xf\n"
"v_accvgpr_write_b32 a210, v126\n"
"v_accvgpr_read_b32 v127, a211\n"
"s_nop 1\n"
"v_mul_f32_dpp v127, v166, v127 row_newbcast:3 row_mask:0xf bank_mask:0xf\n"
"v_accvgpr_write_b32 a211, v127\n"
"v_accvgpr_read_b32 v124, a212\n"
"s_nop 1\n"
"v_mul_f32_dpp v124, v166, v124 row_newbcast:0 row_mask:0xf bank_mask:0xf\n"
"v_accvgpr_write_b32 a212, v124\n"
"v_accvgpr_read_b32 v125, a213\n"
"s_nop 1\n"
"v_mul_f32_dpp v125, v166, v125 row_newbcast:1 row_mask:0xf bank_mask:0xf\n"
"v_accvgpr_write_b32 a213, v125\n"
"v_accvgpr_read_b32 v126, a214\n"
"s_nop 1\n"
"v_mul_f32_dpp v126, v166, v126 row_newbcast:2 row_mask:0xf bank_mask:0xf\n"
"v_accvgpr_write_b32 a214, v126\n"
"v_accvgpr_read_b32 v127, a215\n"
"s_nop 1\n"
"v_mul_f32_dpp v127, v166, v127 row_newbcast:3 row_mask:0xf bank_mask:0xf\n"
"v_accvgpr_write_b32 a215, v127\n"
"v_accvgpr_read_b32 v124, a216\n"
"s_nop 1\n"
"v_mul_f32_dpp v124, v166, v124 row_newbcast:0 row_mask:0xf bank_mask:0xf\n"
"v_accvgpr_write_b32 a216, v124\n"
"v_accvgpr_read_b32 v125, a217\n"
"s_nop 1\n"
"v_mul_f32_dpp v125, v166, v125 row_newbcast:1 row_mask:0xf bank_mask:0xf\n"
"v_accvgpr_write_b32 a217, v125\n"
"v_accvgpr_read_b32 v126, a218\n"
"s_nop 1\n"
"v_mul_f32_dpp v126, v166, v126 row_newbcast:2 row_mask:0xf bank_mask:0xf\n"
"v_accvgpr_write_b32 a218, v126\n"
"v_accvgpr_read_b32 v127, a219\n"
"s_nop 1\n"
"v_mul_f32_dpp v127, v166, v127 row_newbcast:3 row_mask:0xf bank_mask:0xf\n"
"v_accvgpr_write_b32 a219, v127\n"
"v_accvgpr_read_b32 v124, a220\n"
"s_nop 1\n"
"v_mul_f32_dpp v124, v166, v124 row_newbcast:0 row_mask:0xf bank_mask:0xf\n"
"v_accvgpr_write_b32 a220, v124\n"
"v_accvgpr_read_b32 v125, a221\n"
"s_nop 1\n"
"v_mul_f32_dpp v125, v166, v125 row_newbcast:1 row_mask:0xf bank_mask:0xf\n"
"v_accvgpr_write_b32 a221, v125\n"
"v_accvgpr_read_b32 v126, a222\n"
"s_nop 1\n"
"v_mul_f32_dpp v126, v166, v126 row_newbcast:2 row_mask:0xf bank_mask:0xf\n"
"v_accvgpr_write_b32 a222, v126\n"
"v_accvgpr_read_b32 v127, a223\n"
"s_nop 1\n"
"v_mul_f32_dpp v127, v166, v127 row_newbcast:3 row_mask:0xf bank_mask:0xf\n"
"v_accvgpr_write_b32 a223, v127\n"
"v_accvgpr_read_b32 v124, a224\n"
"s_nop 1\n"
"v_mul_f32_dpp v124, v166, v124 row_newbcast:0 row_mask:0xf bank_mask:0xf\n"
"v_accvgpr_write_b32 a224, v124\n"
"v_accvgpr_read_b32 v125, a225\n"
"s_nop 1\n"
"v_mul_f32_dpp v125, v166, v125 row_newbcast:1 row_mask:0xf bank_mask:0xf\n"
"v_accvgpr_write_b32 a225, v125\n"
"v_accvgpr_read_b32 v126, a226\n"
"s_nop 1\n"
"v_mul_f32_dpp v126, v166, v126 row_newbcast:2 row_mask:0xf bank_mask:0xf\n"
"v_accvgpr_write_b32 a226, v126\n"
"v_accvgpr_read_b32 v127, a227\n"
"s_nop 1\n"
"v_mul_f32_dpp v127, v166, v127 row_newbcast:3 row_mask:0xf bank_mask:0xf\n"
"v_accvgpr_write_b32 a227, v127\n"
"v_accvgpr_read_b32 v124, a228\n"
"s_nop 1\n"
"v_mul_f32_dpp v124, v166, v124 row_newbcast:0 row_mask:0xf bank_mask:0xf\n"
"v_accvgpr_write_b32 a228, v124\n"
"v_accvgpr_read_b32 v125, a229\n"
"s_nop 1\n"
"v_mul_f32_dpp v125, v166, v125 row_newbcast:1 row_mask:0xf bank_mask:0xf\n"
"v_accvgpr_write_b32 a229, v125\n"
"v_accvgpr_read_b32 v126, a230\n"
"s_nop 1\n"
"v_mul_f32_dpp v126, v166, v126 row_newbcast:2 row_mask:0xf bank_mask:0xf\n"
"v_accvgpr_write_b32 a230, v126\n"
"v_accvgpr_read_b32 v127, a231\n"
"s_nop 1\n"
"v_mul_f32_dpp v127, v166, v127 row_newbcast:3 row_mask:0xf bank_mask:0xf\n"
"v_accvgpr_write_b32 a231, v127\n"
"v_accvgpr_read_b32 v124, a232\n"
"s_nop 1\n"
"v_mul_f32_dpp v124, v166, v124 row_newbcast:0 row_mask:0xf bank_mask:0xf\n"
"v_accvgpr_write_b32 a232, v124\n"
"v_accvgpr_read_b32 v125, a233\n"
"s_nop 1\n"
"v_mul_f32_dpp v125, v166, v125 row_newbcast:1 row_mask:0xf bank_mask:0xf\n"
"v_accvgpr_write_b32 a233, v125\n"
"v_accvgpr_read_b32 v126, a234\n"
"s_nop 1\n"
"v_mul_f32_dpp v126, v166, v126 row_newbcast:2 row_mask:0xf bank_mask:0xf\n"
"v_accvgpr_write_b32 a234, v126\n"
"v_accvgpr_read_b32 v127, a235\n"
"s_nop 1\n"
"v_mul_f32_dpp v127, v166, v127 row_newbcast:3 row_mask:0xf bank_mask:0xf\n"
"v_accvgpr_write_b32 a235, v127\n"
"v_accvgpr_read_b32 v124, a236\n"
"s_nop 1\n"
"v_mul_f32_dpp v124, v166, v124 row_newbcast:0 row_mask:0xf bank_mask:0xf\n"
"v_accvgpr_write_b32 a236, v124\n"
"v_accvgpr_read_b32 v125, a237\n"
"s_nop 1\n"
"v_mul_f32_dpp v125, v166, v125 row_newbcast:1 row_mask:0xf bank_mask:0xf\n"
"v_accvgpr_write_b32 a237, v125\n"
"v_accvgpr_read_b32 v126, a238\n"
"s_nop 1\n"
"v_mul_f32_dpp v126, v166, v126 row_newbcast:2 row_mask:0xf bank_mask:0xf\n"
"v_accvgpr_write_b32 a238, v126\n"
"v_accvgpr_read_b32 v127, a239\n"
"s_nop 1\n"
"v_mul_f32_dpp v127, v166, v127 row_newbcast:3 row_mask:0xf bank_mask:0xf\n"
"v_accvgpr_write_b32 a239, v127\n"
"LSK3A_%=:\n"
"s_nop 3\n"
"buffer_load_ushort v128, v146, s[88:91], s80 offen offset:0\n"
"buffer_load_ushort v129, v146, s[88:91], s80 offen offset:256\n"
"buffer_load_ushort v130, v146, s[88:91], s80 offen offset:512\n"
"buffer_load_ushort v131, v146, s[88:91], s80 offen offset:768\n"
"buffer_load_ushort v132, v146, s[88:91], s81 offen offset:0\n"
"buffer_load_ushort v133, v146, s[88:91], s81 offen offset:256\n"
"buffer_load_ushort v134, v146, s[88:91], s81 offen offset:512\n"
"buffer_load_ushort v135, v146, s[88:91], s81 offen offset:768\n"
"buffer_load_ushort v136, v146, s[88:91], s80 offen offset:32\n"
"buffer_load_ushort v137, v146, s[88:91], s80 offen offset:288\n"
"buffer_load_ushort v138, v146, s[88:91], s80 offen offset:544\n"
"buffer_load_ushort v139, v146, s[88:91], s80 offen offset:800\n"
"buffer_load_ushort v140, v146, s[88:91], s81 offen offset:32\n"
"buffer_load_ushort v141, v146, s[88:91], s81 offen offset:288\n"
"buffer_load_ushort v142, v146, s[88:91], s81 offen offset:544\n"
"buffer_load_ushort v143, v146, s[88:91], s81 offen offset:800\n"
"s_waitcnt vmcnt(0)\n"
"v_lshl_or_b32 v128, v129, 16, v128\n"
"v_lshl_or_b32 v129, v131, 16, v130\n"
"v_lshl_or_b32 v130, v133, 16, v132\n"
"v_lshl_or_b32 v131, v135, 16, v134\n"
"v_lshl_or_b32 v136, v137, 16, v136\n"
"v_lshl_or_b32 v137, v139, 16, v138\n"
"v_lshl_or_b32 v138, v141, 16, v140\n"
"v_lshl_or_b32 v139, v143, 16, v142\n"
"v_mfma_f32_16x16x32_bf16 v[0:3], v[96:99], v[128:131], v[0:3]\n"
"s_nop 0\n"
"v_mfma_f32_16x16x32_bf16 v[4:7], v[96:99], v[136:139], v[4:7]\n"
"v_mfma_f32_16x16x32_bf16 v[32:35], v[104:107], v[128:131], v[32:35]\n"
"v_mfma_f32_16x16x32_bf16 v[36:39], v[104:107], v[136:139], v[36:39]\n"
"v_mfma_f32_16x16x32_bf16 v[64:67], v[112:115], v[128:131], v[64:67]\n"
"v_mfma_f32_16x16x32_bf16 v[68:71], v[112:115], v[136:139], v[68:71]\n"
"s_cmp_lg_u32 s83, s43\n"
"s_cbranch_scc1 LSK3B0_%=\n"
"v_mfma_f32_16x16x32_bf16 a[208:211], v[120:123], v[128:131], a[208:211]\n"
"v_mfma_f32_16x16x32_bf16 a[212:215], v[120:123], v[136:139], a[212:215]\n"
"LSK3B0_%=:\n"
"s_nop 3\n"
"buffer_load_ushort v128, v147, s[88:91], s80 offen offset:0\n"
"buffer_load_ushort v129, v147, s[88:91], s80 offen offset:256\n"
"buffer_load_ushort v130, v147, s[88:91], s80 offen offset:512\n"
"buffer_load_ushort v131, v147, s[88:91], s80 offen offset:768\n"
"buffer_load_ushort v132, v147, s[88:91], s81 offen offset:0\n"
"buffer_load_ushort v133, v147, s[88:91], s81 offen offset:256\n"
"buffer_load_ushort v134, v147, s[88:91], s81 offen offset:512\n"
"buffer_load_ushort v135, v147, s[88:91], s81 offen offset:768\n"
"buffer_load_ushort v136, v147, s[88:91], s80 offen offset:32\n"
"buffer_load_ushort v137, v147, s[88:91], s80 offen offset:288\n"
"buffer_load_ushort v138, v147, s[88:91], s80 offen offset:544\n"
"buffer_load_ushort v139, v147, s[88:91], s80 offen offset:800\n"
"buffer_load_ushort v140, v147, s[88:91], s81 offen offset:32\n"
"buffer_load_ushort v141, v147, s[88:91], s81 offen offset:288\n"
"buffer_load_ushort v142, v147, s[88:91], s81 offen offset:544\n"
"buffer_load_ushort v143, v147, s[88:91], s81 offen offset:800\n"
"s_waitcnt vmcnt(0)\n"
"v_lshl_or_b32 v128, v129, 16, v128\n"
"v_lshl_or_b32 v129, v131, 16, v130\n"
"v_lshl_or_b32 v130, v133, 16, v132\n"
"v_lshl_or_b32 v131, v135, 16, v134\n"
"v_lshl_or_b32 v136, v137, 16, v136\n"
"v_lshl_or_b32 v137, v139, 16, v138\n"
"v_lshl_or_b32 v138, v141, 16, v140\n"
"v_lshl_or_b32 v139, v143, 16, v142\n"
"v_mfma_f32_16x16x32_bf16 v[8:11], v[96:99], v[128:131], v[8:11]\n"
"s_nop 0\n"
"v_mfma_f32_16x16x32_bf16 v[12:15], v[96:99], v[136:139], v[12:15]\n"
"v_mfma_f32_16x16x32_bf16 v[40:43], v[104:107], v[128:131], v[40:43]\n"
"v_mfma_f32_16x16x32_bf16 v[44:47], v[104:107], v[136:139], v[44:47]\n"
"v_mfma_f32_16x16x32_bf16 v[72:75], v[112:115], v[128:131], v[72:75]\n"
"v_mfma_f32_16x16x32_bf16 v[76:79], v[112:115], v[136:139], v[76:79]\n"
"s_cmp_lg_u32 s83, s43\n"
"s_cbranch_scc1 LSK3B1_%=\n"
"v_mfma_f32_16x16x32_bf16 a[216:219], v[120:123], v[128:131], a[216:219]\n"
"v_mfma_f32_16x16x32_bf16 a[220:223], v[120:123], v[136:139], a[220:223]\n"
"LSK3B1_%=:\n"
"s_nop 3\n"
"buffer_load_ushort v128, v146, s[88:91], s80 offen offset:128\n"
"buffer_load_ushort v129, v146, s[88:91], s80 offen offset:384\n"
"buffer_load_ushort v130, v146, s[88:91], s80 offen offset:640\n"
"buffer_load_ushort v131, v146, s[88:91], s80 offen offset:896\n"
"buffer_load_ushort v132, v146, s[88:91], s81 offen offset:128\n"
"buffer_load_ushort v133, v146, s[88:91], s81 offen offset:384\n"
"buffer_load_ushort v134, v146, s[88:91], s81 offen offset:640\n"
"buffer_load_ushort v135, v146, s[88:91], s81 offen offset:896\n"
"buffer_load_ushort v136, v146, s[88:91], s80 offen offset:160\n"
"buffer_load_ushort v137, v146, s[88:91], s80 offen offset:416\n"
"buffer_load_ushort v138, v146, s[88:91], s80 offen offset:672\n"
"buffer_load_ushort v139, v146, s[88:91], s80 offen offset:928\n"
"buffer_load_ushort v140, v146, s[88:91], s81 offen offset:160\n"
"buffer_load_ushort v141, v146, s[88:91], s81 offen offset:416\n"
"buffer_load_ushort v142, v146, s[88:91], s81 offen offset:672\n"
"buffer_load_ushort v143, v146, s[88:91], s81 offen offset:928\n"
"s_waitcnt vmcnt(0)\n"
"v_lshl_or_b32 v128, v129, 16, v128\n"
"v_lshl_or_b32 v129, v131, 16, v130\n"
"v_lshl_or_b32 v130, v133, 16, v132\n"
"v_lshl_or_b32 v131, v135, 16, v134\n"
"v_lshl_or_b32 v136, v137, 16, v136\n"
"v_lshl_or_b32 v137, v139, 16, v138\n"
"v_lshl_or_b32 v138, v141, 16, v140\n"
"v_lshl_or_b32 v139, v143, 16, v142\n"
"v_mfma_f32_16x16x32_bf16 v[16:19], v[96:99], v[128:131], v[16:19]\n"
"s_nop 0\n"
"v_mfma_f32_16x16x32_bf16 v[20:23], v[96:99], v[136:139], v[20:23]\n"
"v_mfma_f32_16x16x32_bf16 v[48:51], v[104:107], v[128:131], v[48:51]\n"
"v_mfma_f32_16x16x32_bf16 v[52:55], v[104:107], v[136:139], v[52:55]\n"
"v_mfma_f32_16x16x32_bf16 v[80:83], v[112:115], v[128:131], v[80:83]\n"
"v_mfma_f32_16x16x32_bf16 v[84:87], v[112:115], v[136:139], v[84:87]\n"
"s_cmp_lg_u32 s83, s43\n"
"s_cbranch_scc1 LSK3B2_%=\n"
"v_mfma_f32_16x16x32_bf16 a[224:227], v[120:123], v[128:131], a[224:227]\n"
"v_mfma_f32_16x16x32_bf16 a[228:231], v[120:123], v[136:139], a[228:231]\n"
"LSK3B2_%=:\n"
"s_nop 3\n"
"buffer_load_ushort v128, v147, s[88:91], s80 offen offset:128\n"
"buffer_load_ushort v129, v147, s[88:91], s80 offen offset:384\n"
"buffer_load_ushort v130, v147, s[88:91], s80 offen offset:640\n"
"buffer_load_ushort v131, v147, s[88:91], s80 offen offset:896\n"
"buffer_load_ushort v132, v147, s[88:91], s81 offen offset:128\n"
"buffer_load_ushort v133, v147, s[88:91], s81 offen offset:384\n"
"buffer_load_ushort v134, v147, s[88:91], s81 offen offset:640\n"
"buffer_load_ushort v135, v147, s[88:91], s81 offen offset:896\n"
"buffer_load_ushort v136, v147, s[88:91], s80 offen offset:160\n"
"buffer_load_ushort v137, v147, s[88:91], s80 offen offset:416\n"
"buffer_load_ushort v138, v147, s[88:91], s80 offen offset:672\n"
"buffer_load_ushort v139, v147, s[88:91], s80 offen offset:928\n"
"buffer_load_ushort v140, v147, s[88:91], s81 offen offset:160\n"
"buffer_load_ushort v141, v147, s[88:91], s81 offen offset:416\n"
"buffer_load_ushort v142, v147, s[88:91], s81 offen offset:672\n"
"buffer_load_ushort v143, v147, s[88:91], s81 offen offset:928\n"
"s_waitcnt vmcnt(0)\n"
"v_lshl_or_b32 v128, v129, 16, v128\n"
"v_lshl_or_b32 v129, v131, 16, v130\n"
"v_lshl_or_b32 v130, v133, 16, v132\n"
"v_lshl_or_b32 v131, v135, 16, v134\n"
"v_lshl_or_b32 v136, v137, 16, v136\n"
"v_lshl_or_b32 v137, v139, 16, v138\n"
"v_lshl_or_b32 v138, v141, 16, v140\n"
"v_lshl_or_b32 v139, v143, 16, v142\n"
"v_mfma_f32_16x16x32_bf16 v[24:27], v[96:99], v[128:131], v[24:27]\n"
"s_nop 0\n"
"v_mfma_f32_16x16x32_bf16 v[28:31], v[96:99], v[136:139], v[28:31]\n"
"v_mfma_f32_16x16x32_bf16 v[56:59], v[104:107], v[128:131], v[56:59]\n"
"v_mfma_f32_16x16x32_bf16 v[60:63], v[104:107], v[136:139], v[60:63]\n"
"v_mfma_f32_16x16x32_bf16 v[88:91], v[112:115], v[128:131], v[88:91]\n"
"v_mfma_f32_16x16x32_bf16 v[92:95], v[112:115], v[136:139], v[92:95]\n"
"s_cmp_lg_u32 s83, s43\n"
"s_cbranch_scc1 LSK3B3_%=\n"
"v_mfma_f32_16x16x32_bf16 a[232:235], v[120:123], v[128:131], a[232:235]\n"
"v_mfma_f32_16x16x32_bf16 a[236:239], v[120:123], v[136:139], a[236:239]\n"
"LSK3B3_%=:\n"
"s_nop 3\n"
"s_nop 9\n"
"s_add_u32 s80, s80, 8192\n"
"s_add_u32 s81, s81, 8192\n"
"s_add_u32 s82, s82, 1\n"
"s_cmp_lt_u32 s82, 32\n"
"s_cbranch_scc1 LSLOOP_%=\n"
"v_mov_b32 v154, v155\n"
"s_nop 1\n"
"v_permlane32_swap_b32 v155, v154\n"
"v_add_f32 v155, v155, v154\n"
"v_mov_b32 v154, v155\n"
"s_nop 1\n"
"v_permlane16_swap_b32 v155, v154\n"
"v_add_f32 v155, v155, v154\n"
"v_mov_b32 v196, v155\n"
"v_mov_b32 v158, v159\n"
"s_nop 1\n"
"v_permlane32_swap_b32 v159, v158\n"
"v_add_f32 v159, v159, v158\n"
"v_mov_b32 v158, v159\n"
"s_nop 1\n"
"v_permlane16_swap_b32 v159, v158\n"
"v_add_f32 v159, v159, v158\n"
"v_mov_b32 v198, v159\n"
"v_mov_b32 v162, v163\n"
"s_nop 1\n"
"v_permlane32_swap_b32 v163, v162\n"
"v_add_f32 v163, v163, v162\n"
"v_mov_b32 v162, v163\n"
"s_nop 1\n"
"v_permlane16_swap_b32 v163, v162\n"
"v_add_f32 v163, v163, v162\n"
"v_mov_b32 v200, v163\n"
"v_mov_b32 v166, v167\n"
"s_nop 1\n"
"v_permlane32_swap_b32 v167, v166\n"
"v_add_f32 v167, v167, v166\n"
"v_mov_b32 v166, v167\n"
"s_nop 1\n"
"v_permlane16_swap_b32 v167, v166\n"
"v_add_f32 v167, v167, v166\n"
"v_mov_b32 v202, v167\n"
"v_sub_f32 v192, 0, v164\n"
"v_mov_b32 v96, 0\n"
"v_mov_b32 v97, 0\n"
"v_mov_b32 v98, 0\n"
"v_mov_b32 v99, 0\n"
"v_mov_b32 v100, 0\n"
"v_mov_b32 v101, 0\n"
"v_mov_b32 v102, 0\n"
"v_mov_b32 v103, 0\n"
"v_mov_b32 v104, 0\n"
"v_mov_b32 v105, 0\n"
"v_mov_b32 v106, 0\n"
"v_mov_b32 v107, 0\n"
"v_mov_b32 v108, 0\n"
"v_mov_b32 v109, 0\n"
"v_mov_b32 v110, 0\n"
"v_mov_b32 v111, 0\n"
"v_mov_b32 v112, 0\n"
"v_mov_b32 v113, 0\n"
"v_mov_b32 v114, 0\n"
"v_mov_b32 v115, 0\n"
"v_mov_b32 v116, 0\n"
"v_mov_b32 v117, 0\n"
"v_mov_b32 v118, 0\n"
"v_mov_b32 v119, 0\n"
"v_mov_b32 v120, 0\n"
"v_mov_b32 v121, 0\n"
"v_mov_b32 v122, 0\n"
"v_mov_b32 v123, 0\n"
"v_mov_b32 v204, 0\n"
"v_mov_b32 v205, 0\n"
"s_nop 3\n"
"s_mov_b32 s68, 0\n"
"s_cmp_eq_u32 s47, 4\n"
"s_cbranch_scc1 LBACK0_%=\n"
"s_cmp_eq_u32 s47, 8\n"
"s_cbranch_scc1 LBACK1_%=\n"
"s_cmp_eq_u32 s47, 12\n"
"s_cbranch_scc1 LBACK2_%=\n"
"s_branch LBACK3_%=\n"
"LFIN_%=:\n"
    :: [kalo]"s"((unsigned)ka), [kahi]"s"((unsigned)(ka >> 32)), [wg]"s"(blockIdx.x), [wid]"s"(wid)
    : "v0","v1","v2","v3","v4","v5","v6","v7","v8","v9","v10","v11","v12","v13","v14","v15","v16","v17","v18","v19","v20","v21","v22","v23","v24","v25","v26","v27","v28","v29","v30","v31","v32","v33","v34","v35","v36","v37","v38","v39","v40","v41","v42","v43","v44","v45","v46","v47","v48","v49","v50","v51","v52","v53","v54","v55","v56","v57","v58","v59","v60","v61","v62","v63","v64","v65","v66","v67","v68","v69","v70","v71","v72","v73","v74","v75","v76","v77","v78","v79","v80","v81","v82","v83","v84","v85","v86","v87","v88","v89","v90","v91","v92","v93","v94","v95","v96","v97","v98","v99","v100","v101","v102","v103","v104","v105","v106","v107","v108","v109","v110","v111","v112","v113","v114","v115","v116","v117","v118","v119","v120","v121","v122","v123","v124","v125","v126","v127","v128","v129","v130","v131","v132","v133","v134","v135","v136","v137","v138","v139","v140","v141","v142","v143","v144","v145","v146","v147","v148","v149","v150","v151","v152","v153","v154","v155","v156","v157","v158","v159","v160","v161","v162","v163","v164","v165","v166","v167","v168","v169","v170","v171","v172","v173","v174","v175","v176","v177","v178","v179","v180","v181","v182","v183","v184","v185","v186","v187","v188","v189","v190","v191","v192","v193","v194","v195","v196","v197","v198","v199","v200","v201","v202","v203","v204","v205","v206","v207","v208","v209","v210","v211","v212","v213","v214","v215","v216","v217","v218","v219","v220","v221","v222","v223","v224","v225","v226","v227","v228","v229","v230","v231","v232","v233","v234","v235","v236","v237","v238","v239","v240","v241","v242","v243","v244","v245","v246","v247","v248","v249","v250","v251","v252","v253","v254","v255","a0","a1","a2","a3","a4","a5","a6","a7","a8","a9","a10","a11","a12","a13","a14","a15","a16","a17","a18","a19","a20","a21","a22","a23","a24","a25","a26","a27","a28","a29","a30","a31","a32","a33","a34","a35","a36","a37","a38","a39","a40","a41","a42","a43","a44","a45","a46","a47","a48","a49","a50","a51","a52","a53","a54","a55","a56","a57","a58","a59","a60","a61","a62","a63","a64","a65","a66","a67","a68","a69","a70","a71","a72","a73","a74","a75","a76","a77","a78","a79","a80","a81","a82","a83","a84","a85","a86","a87","a88","a89","a90","a91","a92","a93","a94","a95","a96","a97","a98","a99","a100","a101","a102","a103","a104","a105","a106","a107","a108","a109","a110","a111","a112","a113","a114","a115","a116","a117","a118","a119","a120","a121","a122","a123","a124","a125","a126","a127","a128","a129","a130","a131","a132","a133","a134","a135","a136","a137","a138","a139","a140","a141","a142","a143","a144","a145","a146","a147","a148","a149","a150","a151","a152","a153","a154","a155","a156","a157","a158","a159","a160","a161","a162","a163","a164","a165","a166","a167","a168","a169","a170","a171","a172","a173","a174","a175","a176","a177","a178","a179","a180","a181","a182","a183","a184","a185","a186","a187","a188","a189","a190","a191","a192","a193","a194","a195","a196","a197","a198","a199","a200","a201","a202","a203","a204","a205","a206","a207","a208","a209","a210","a211","a212","a213","a214","a215","a216","a217","a218","a219","a220","a221","a222","a223","a224","a225","a226","a227","a228","a229","a230","a231","a232","a233","a234","a235","a236","a237","a238","a239","a240","a241","a242","a243","a244","a245","a246","a247","a248","a249","a250","a251","a252","a253","a254","a255","s6","s7","s8","s9","s10","s11","s12","s13","s14","s15","s16","s17","s18","s19","s20","s21","s22","s23","s24","s25","s26","s27","s28","s29","s30","s31","s32","s33","s34","s35","s36","s37","s38","s39","s40","s41","s42","s43","s44","s45","s46","s47","s48","s49","s50","s51","s52","s53","s54","s55","s56","s57","s58","s59","s60","s61","s62","s63","s64","s65","s66","s67","s68","s69","s70","s71","s72","s73","s74","s75","s76","s77","s78","s79","s80","s81","s82","s83","s84","s85","s86","s87","s88","s89","s90","s91","s92","s93","s94","s95","s96","s97","s98","s99","s100","vcc","scc","m0","memory");
}
extern "C" void kernel_launch(void* const* d_in, const int* in_sizes, int n_in, void* d_out, int out_size, void* d_ws, size_t ws_size, hipStream_t stream) {
  char* xch = (char*)d_ws + ((ws_size - (size_t)270532608) & ~(size_t)0xfffff);
  (void)hipFuncSetAttribute((const void*)fused_attn, hipFuncAttributeMaxDynamicSharedMemorySize, 163840);
  hipLaunchKernelGGL(fused_attn, dim3(256), dim3(256), 163840, stream,
                     (const char*)d_in[0], (const char*)d_in[1], (const char*)d_in[2], (const char*)d_in[3], (const char*)d_in[4],
                     (const char*)d_in[5], (const char*)d_in[6], (const char*)d_in[7], (const char*)d_in[8], (const char*)d_in[9], xch, (char*)d_out, (char*)d_ws);
}
```

```cpp
#include <hip/hip_runtime.h>
#include <hip/hip_bf16.h>
#include <stdint.h>

__global__ __launch_bounds__(256, 1)
void fused_attn(const char* xA, const char* kA, const char* vA, const char* wA, const char* uA,
                const char* xB, const char* kB, const char* vB, const char* wB, const char* uB, char* xch, char* out, char* flags) {
  extern __shared__ __attribute__((aligned(16))) char lds[];
  const unsigned wid = __builtin_amdgcn_readfirstlane(threadIdx.x >> 6);
  const unsigned long long ka = (unsigned long long)__builtin_amdgcn_kernarg_segment_ptr();
  asm volatile(
"s_memtime s[64:65]\n"
"s_memrealtime s[66:67]\n"
"s_bfe_u32 s10, %[wg], 0x10002\n"
"s_mul_i32 s40, s10, 40\n"
"s_add_u32 s40, s40, %[kalo]\n"
"s_addc_u32 s41, %[kahi], 0\n"
"s_mov_b32 s78, %[kalo]\n"
"s_mov_b32 s79, %[kahi]\n"
"s_load_dwordx2 s[24:25], s[40:41], 0\n"
"s_load_dwordx2 s[28:29], s[40:41], 24\n"
"s_load_dwordx2 s[6:7], s[78:79], 0x60\n"
"s_load_dwordx2 s[16:17], s[40:41], 8\n"
"s_load_dwordx2 s[20:21], s[40:41], 16\n"
"s_load_dwordx2 s[8:9], s[40:41], 32\n"
"s_load_dwordx4 s[12:15], s[78:79], 0x50\n"
"s_mov_b32 s42, %[wid]\n"
"s_and_b32 s11, %[wg], 1\n"
"s_lshl_b32 s11, s11, 3\n"
"s_lshr_b32 s60, %[wg], 5\n"
"s_add_u32 s11, s11, s60\n"
"s_bfe_u32 s61, %[wg], 0x10001\n"
"s_bfe_u32 s60, %[wg], 0x20003\n"
"s_lshl2_add_u32 s61, s61, s60\n"
"s_getpc_b64 s[80:81]\n"
"LCP_%=:\n"
"s_mov_b32 s82, LEND_%=-LCP_%=\n"
"s_mov_b32 s83, 0x20000\n"
"v_mbcnt_lo_u32_b32 v2, -1, 0\n"
"v_lshlrev_b32 v2, 7, v2\n"
"s_lshr_b32 s60, %[wg], 3\n"
"s_lshl2_add_u32 s60, s60, s42\n"
"s_mul_i32 s60, s60, 640\n"
"s_mov_b64 exec, 31\n"
"buffer_load_dword v3, v2, s[80:83], s60 offen\n"
"s_mov_b64 exec, -1\n"
"s_mov_b32 s19, 0x20000\n"
"s_mov_b32 s23, 0x20000\n"
"s_mov_b32 s27, 0x20000\n"
"s_mov_b32 s31, 0x20000\n"
"s_mov_b32 s35, 0x20000\n"
"s_mov_b32 s39, 0x20000\n"
"s_mov_b32 s26, 286720\n"
"s_lshl_b32 s44, s42, 12\n"
"s_mov_b32 s50, s44\n"
"s_mul_i32 s51, s42, 7168\n"
"s_add_u32 s51, s51, 0x10000\n"
"s_mov_b32 s74, 0x8000\n"
"s_mov_b32 s58, 524288\n"
"s_mov_b32 s47, 0\n"
"s_add_u32 s60, s11, s61\n"
"s_and_b32 s60, s60, 0\n"
"s_mul_i32 s60, s60, 3\n"
"s_lshl_b32 s49, s60, 7\n"
"s_lshl_b32 s48, s60, 19\n"
"v_mbcnt_lo_u32_b32 v4, -1, 0\n"
"v_mbcnt_hi_u32_b32 v4, -1, v4\n"
"v_and_b32 v5, 15, v4\n"
"v_lshrrev_b32 v6, 4, v4\n"
"v_lshrrev_b32 v7, 3, v4\n"
"v_and_b32 v8, 7, v4\n"
"v_lshlrev_b32 v9, 1, v6\n"
"v_xor_b32 v9, v9, v8\n"
"v_lshlrev_b32 v9, 4, v9\n"
"s_mul_i32 s60, s42, 71680\n"
"v_mul_u32_u24 v10, 1280, v7\n"
"v_add3_u32 v248, v10, v9, s60\n"
"v_add_u32 v249, 9216, v248\n"
"v_add_u32 v254, 18432, v248\n"
"v_add_u32 v255, 27648, v248\n"
"v_and_b32 v10, 2, v7\n"
"v_xor_b32 v240, v10, v8\n"
"v_lshlrev_b32 v240, 4, v240\n"
"v_bfe_u32 v10, v7, 0, 1\n"
"v_lshl_add_u32 v240, v10, 8, v240\n"
"v_bfe_u32 v10, v7, 1, 1\n"
"v_lshl_add_u32 v240, v10, 9, v240\n"
"v_bfe_u32 v10, v7, 2, 1\n"
"v_lshl_add_u32 v240, v10, 7, v240\n"
"s_and_b32 s60, s42, 1\n"
"v_lshl_add_u32 v240, s60, 15, v240\n"
"s_lshr_b32 s60, s42, 1\n"
"v_lshl_add_u32 v240, s60, 16, v240\n"
"v_and_b32 v10, 1, v6\n"
"v_xor_b32 v10, v10, v8\n"
"v_lshlrev_b32 v10, 4, v10\n"
"v_lshl_add_u32 v10, v7, 13, v10\n"
"s_lshl_b32 s60, s42, 17\n"
"v_add_u32 v10, s60, v10\n"
"v_add_u32 v224, 1024, v10\n"
"v_add_u32 v225, 128, v10\n"
"v_xor_b32 v10, 64, v10\n"
"v_add_u32 v226, 64512, v10\n"
"v_add_u32 v227, 63616, v10\n"
"s_mul_i32 s40, s11, 286720\n"
"s_lshl_b32 s41, s61, 10\n"
"s_sub_u32 s30, 5243904, s41\n"
"s_sub_u32 s41, s41, 1024\n"
"s_ashr_i32 s60, s41, 31\n"
"s_lshl4_add_u32 s92, s10, s11\n"
"s_lshl_b32 s93, s92, 9\n"
"v_mov_b32 v0, 0\n"
"s_waitcnt lgkmcnt(0)\n"
"s_add_u32 s24, s24, s40\n"
"s_addc_u32 s25, s25, 0\n"
"s_and_b32 s25, s25, 0xffff\n"
"s_add_u32 s28, s28, s41\n"
"s_addc_u32 s29, s29, s60\n"
"s_and_b32 s29, s29, 0xffff\n"
"s_add_u32 s6, s6, s93\n"
"s_addc_u32 s7, s7, 0\n"
"buffer_load_dword v12, v248, s[24:27], s49 offen\n"
"buffer_load_dword v13, v224, s[28:31], s48 offen\n"
"global_load_dword v1, v0, s[6:7] offset:128 sc1\n"
"s_mov_b32 m0, s51\n"
"s_nop 0\n"
"buffer_load_dwordx4 v248, s[24:27], s49 offen lds\n"
"buffer_load_dwordx4 v249, s[24:27], s49 offen offset:1024 lds\n"
"buffer_load_dwordx4 v254, s[24:27], s49 offen offset:2048 lds\n"
"buffer_load_dwordx4 v255, s[24:27], s49 offen offset:3072 lds\n"
"s_add_u32 m0, s51, 4096\n"
"s_add_u32 s40, s49, 40960\n"
"buffer_load_dwordx4 v248, s[24:27], s40 offen lds\n"
"buffer_load_dwordx4 v249, s[24:27], s40 offen offset:1024 lds\n"
"buffer_load_dwordx4 v254, s[24:27], s40 offen offset:2048 lds\n"
"s_add_u32 s49, s49, 128\n"
"s_sub_u32 s41, s49, 1280\n"
"s_min_u32 s49, s49, s41\n"
"s_xor_b32 s51, s51, s74\n"
"s_mov_b32 m0, s50\n"
"s_nop 0\n"
"buffer_load_dwordx4 v224, s[28:31], s48 offen lds\n"
"buffer_load_dwordx4 v225, s[28:31], s48 offen offset:1024 lds\n"
"buffer_load_dwordx4 v226, s[28:31], s48 offen offset:2048 lds\n"
"buffer_load_dwordx4 v227, s[28:31], s48 offen offset:3072 lds\n"
"s_xor_b32 s50, s50, s74\n"
"s_lshl2_add_u32 s63, s11, s42\n"
"s_mul_i32 s63, s63, 10240\n"
"s_and_b32 s78, s42, 1\n"
"s_lshl_b32 s43, s78, 6\n"
"s_mov_b32 s77, 0x80000000\n"
"s_mov_b32 s75, 0x10000\n"
"s_mov_b32 s73, 0xffff0000\n"
"s_mov_b32 s46, 0x3e0293ee\n"
"s_mov_b32 s45, 0x63800000\n"
"s_mov_b32 s62, 9\n"
"v_lshl_add_u32 v242, v4, 4, s44\n"
"s_lshr_b32 s79, s42, 1\n"
"s_lshl4_add_u32 s76, s79, s78\n"
"s_lshl_b32 s76, s76, 10\n"
"s_add_u32 s76, s76, 0x20000\n"
"s_sub_u32 s76, s76, s44\n"
"v_lshlrev_b32 v9, 6, v6\n"
"v_xor_b32 v9, v9, v242\n"
"v_add_u32 v243, s76, v9\n"
"v_and_b32 v9, 6, v5\n"
"s_xor_b32 m0, s50, 0xc000\n"
"s_add_u32 s40, s48, 256\n"
"buffer_load_dwordx4 v224, s[28:31], s40 offen lds\n"
"v_xor_b32 v9, v9, v6\n"
"v_lshlrev_b32 v9, 4, v9\n"
"v_lshl_add_u32 v10, v5, 7, v9\n"
"s_mul_i32 s79, s42, 6144\n"
"s_add_u32 s79, s79, 0x10000\n"
"v_add_u32 v246, s79, v10\n"
"v_xor_b32 v247, 64, v246\n"
"s_lshr_b32 s79, s42, 1\n"
"s_lshl_b32 s79, s79, 11\n"
"buffer_load_dwordx4 v225, s[28:31], s40 offen offset:1024 lds\n"
"s_add_u32 s79, s79, 90112\n"
"v_add_u32 v252, s79, v10\n"
"v_xor_b32 v253, 64, v252\n"
"s_xor_b32 s79, s42, 1\n"
"s_mul_i32 s79, s79, 6144\n"
"s_add_u32 s79, s79, 0x10000\n"
"v_add_u32 v250, s79, v10\n"
"v_xor_b32 v251, 64, v250\n"
"s_mul_i32 s79, s78, 8256\n"
"buffer_load_dwordx4 v226, s[28:31], s40 offen offset:2048 lds\n"
"v_lshlrev_b32 v9, 10, v6\n"
"v_bfe_u32 v10, v4, 2, 2\n"
"v_lshl_add_u32 v9, v10, 7, v9\n"
"v_and_b32 v10, 3, v4\n"
"v_lshl_add_u32 v9, v10, 3, v9\n"
"v_bfe_u32 v10, v4, 3, 2\n"
"v_lshl_add_u32 v9, v10, 5, v9\n"
"s_add_u32 s79, s79, 0x10000\n"
"v_xor_b32 v232, s79, v9\n"
"buffer_load_dwordx4 v227, s[28:31], s40 offen offset:3072 lds\n"
"s_cmp_eq_u32 s47, 8\n"
"s_cselect_b32 s41, s52, s58\n"
"s_add_u32 s48, s48, s41\n"
"s_sub_u32 s41, s48, 5242880\n"
"s_min_u32 s48, s48, s41\n"
"v_xor_b32 v233, 32, v232\n"
"v_xor_b32 v234, 64, v232\n"
"v_xor_b32 v235, 96, v232\n"
"v_xor_b32 v236, 0x2000, v232\n"
"v_xor_b32 v237, 0x2000, v233\n"
"v_xor_b32 v238, 0x2000, v234\n"
"v_xor_b32 v239, 0x2000, v235\n"
"v_lshlrev_b32 v9, 11, v6\n"
"v_bfe_u32 v10, v4, 2, 2\n"
"s_mov_b32 m0, s50\n"
"s_nop 0\n"
"buffer_load_dwordx4 v224, s[28:31], s48 offen lds\n"
"v_lshl_add_u32 v9, v10, 7, v9\n"
"v_and_b32 v10, 1, v4\n"
"v_lshl_add_u32 v9, v10, 3, v9\n"
"v_bfe_u32 v10, v4, 1, 1\n"
"v_lshl_add_u32 v9, v10, 5, v9\n"
"v_bfe_u32 v10, v4, 3, 1\n"
"v_lshl_add_u32 v9, v10, 4, v9\n"
"v_bfe_u32 v10, v4, 4, 1\n"
"v_lshl_add_u32 v9, v10, 6, v9\n"
"buffer_load_dwordx4 v225, s[28:31], s48 offen offset:1024 lds\n"
"v_xor_b32 v244, s43, v9\n"
"v_xor_b32 v245, 16, v244\n"
"s_lshl3_add_u32 s79, s11, s61\n"
"s_lshl_b32 s79, s79, 20\n"
"s_sub_u32 s79, s79, 262144\n"
"s_ashr_i32 s60, s79, 31\n"
"s_add_u32 s16, s16, s79\n"
"s_addc_u32 s17, s17, s60\n"
"s_and_b32 s17, s17, 0xffff\n"
"buffer_load_dwordx4 v226, s[28:31], s48 offen offset:2048 lds\n"
"s_mov_b32 s18, 1310720\n"
"s_mov_b64 s[84:85], s[16:17]\n"
"s_mov_b64 s[86:87], s[18:19]\n"
"s_add_u32 s20, s20, s79\n"
"s_addc_u32 s21, s21, s60\n"
"s_and_b32 s21, s21, 0xffff\n"
"s_mov_b32 s22, 1310720\n"
"s_mov_b64 s[88:89], s[20:21]\n"
"s_mov_b64 s[90:91], s[22:23]\n"
"buffer_load_dwordx4 v227, s[28:31], s48 offen offset:3072 lds\n"
"s_xor_b32 s50, s50, s74\n"
"s_mov_b32 s57, 122880\n"
"s_bfe_u32 s55, %[wg], 65541\n"
"s_mul_i32 s55, s55, 1024\n"
"s_xor_b32 s57, s57, s55\n"
"s_bfe_u32 s55, %[wg], 65542\n"
"s_mul_i32 s55, s55, 2048\n"
"s_xor_b32 s57, s57, s55\n"
"s_and_b32 s54, %[wg], 144\n"
"s_bcnt1_i32_b32 s54, s54\n"
"s_mov_b32 m0, s51\n"
"s_nop 0\n"
"buffer_load_dwordx4 v248, s[24:27], s49 offen lds\n"
"s_and_b32 s54, s54, 1\n"
"s_lshl_b32 s54, s54, 13\n"
"s_xor_b32 s57, s57, s54\n"
"s_lshl_b32 s54, s54, 1\n"
"s_sub_u32 s54, 21504, s54\n"
"s_mov_b32 s55, 385024\n"
"s_xor_b32 s56, s55, s57\n"
"s_mov_b32 s59, 1024\n"
"s_add_u32 s52, s11, s61\n"
"buffer_load_dwordx4 v249, s[24:27], s49 offen offset:1024 lds\n"
"s_and_b32 s52, s52, 0\n"
"s_mul_i32 s52, s52, 3\n"
"s_add_u32 s52, s52, 1\n"
"s_lshl_b32 s53, s52, 7\n"
"s_lshl_b32 s52, s52, 19\n"
"s_add_u32 s52, s52, 512\n"
"s_mul_i32 s79, s61, 655360\n"
"s_add_u32 s8, s8, s79\n"
"s_addc_u32 s9, s9, 0\n"
"buffer_load_dwordx4 v254, s[24:27], s49 offen offset:2048 lds\n"
"s_and_b32 s9, s9, 0xffff\n"
"s_add_u32 s12, s12, 0x1fffff\n"
"s_addc_u32 s13, s13, 0\n"
"s_and_b32 s12, s12, 0xffe00000\n"
"s_mul_i32 s96, s42, 3\n"
"s_and_b32 s96, s96, 5\n"
"s_cmp_eq_u32 s42, 3\n"
"s_cselect_b32 s96, 5, s96\n"
"s_lshl_b32 s79, s61, 8\n"
"buffer_load_dwordx4 v255, s[24:27], s49 offen offset:3072 lds\n"
"s_add_u32 s96, s96, s79\n"
"s_add_u32 s96, s96, 160\n"
"s_lshl_b32 s96, s96, 12\n"
"s_mul_i32 s79, s92, 8388608\n"
"s_add_u32 s32, s12, s79\n"
"s_addc_u32 s33, s13, 0\n"
"s_and_b32 s33, s33, 0xffff\n"
"s_mov_b32 s34, 8388608\n"
"s_mul_i32 s79, s11, 573440\n"
"s_add_u32 m0, s51, 4096\n"
"s_add_u32 s40, s49, 40960\n"
"buffer_load_dwordx4 v248, s[24:27], s40 offen lds\n"
"s_mul_i32 s60, s10, 1280\n"
"s_sub_u32 s38, 573440, s60\n"
"s_add_u32 s79, s79, s60\n"
"s_add_u32 s36, s14, s79\n"
"s_addc_u32 s37, s15, 0\n"
"s_and_b32 s37, s37, 0xffff\n"
"s_lshl2_add_u32 s93, s61, s42\n"
"s_add_u32 s95, s61, 1\n"
"s_and_b32 s95, s95, 7\n"
"buffer_load_dwordx4 v249, s[24:27], s40 offen offset:1024 lds\n"
"s_lshl_b32 s95, s95, 7\n"
"s_add_u32 s100, s61, 2\n"
"s_and_b32 s100, s100, 7\n"
"s_lshl_b32 s100, s100, 7\n"
"s_lshl_b32 s60, s61, 7\n"
"s_add_u32 s60, s60, s43\n"
"s_mul_i32 s97, s42, 122880\n"
"s_add_u32 s97, s97, s60\n"
"s_lshr_b32 s98, s42, 1\n"
"buffer_load_dwordx4 v254, s[24:27], s40 offen offset:2048 lds\n"
"s_add_u32 s49, s49, 128\n"
"s_sub_u32 s41, s49, 1280\n"
"s_min_u32 s49, s49, s41\n"
"s_xor_b32 s51, s51, s74\n"
"s_add_u32 s98, s98, 12\n"
"s_mul_i32 s98, s98, 40960\n"
"s_add_u32 s98, s98, s60\n"
"s_mul_i32 s60, s93, 37\n"
"s_lshr_b32 s60, s60, 8\n"
"s_mul_i32 s79, s60, 7\n"
"s_sub_u32 s79, s93, s79\n"
"s_lshl_b32 s94, s60, 4\n"
"s_add_u32 s94, s94, s79\n"
"s_lshl_b32 s94, s94, 10\n"
"s_add_u32 s94, s94, 524288\n"
"s_mul_i32 s99, s60, 3\n"
"s_lshr_b32 s78, s79, 1\n"
"s_add_u32 s99, s99, s78\n"
"s_lshr_b32 s78, s60, 1\n"
"s_add_u32 s78, s78, 12\n"
"s_cmp_eq_u32 s79, 6\n"
"s_cselect_b32 s99, s78, s99\n"
"s_mul_i32 s99, s99, 40960\n"
"s_xor_b32 s79, s79, s60\n"
"s_and_b32 s79, s79, 1\n"
"s_lshl_b32 s79, s79, 6\n"
"s_add_u32 s99, s99, s79\n"
"s_add_u32 s99, s99, 1024\n"
"s_cmp_eq_u32 s61, 7\n"
"s_cselect_b32 s99, 0x40000000, s99\n"
"s_cselect_b32 s94, 0x40000000, s94\n"
"s_waitcnt vmcnt(27)\n"
"v_and_b32 v12, 0x7fff0000, v12\n"
"v_and_b32 v13, 0x7fff0000, v13\n"
"s_nop 0\n"
"v_max_u32_dpp v12, v12, v12 quad_perm:[1,0,3,2] row_mask:0xf bank_mask:0xf\n"
"v_max_u32_dpp v13, v13, v13 quad_perm:[1,0,3,2] row_mask:0xf bank_mask:0xf\n"
"s_nop 0\n"
"v_max_u32_dpp v12, v12, v12 quad_perm:[2,3,0,1] row_mask:0xf bank_mask:0xf\n"
"v_max_u32_dpp v13, v13, v13 quad_perm:[2,3,0,1] row_mask:0xf bank_mask:0xf\n"
"s_memtime s[80:81]\n"
"s_memrealtime s[82:83]\n"
"s_nop 0\n"
"v_max_u32_dpp v12, v12, v12 row_half_mirror row_mask:0xf bank_mask:0xf\n"
"v_max_u32_dpp v13, v13, v13 row_half_mirror row_mask:0xf bank_mask:0xf\n"
"s_nop 0\n"
"v_min_u32_dpp v12, v12, v12 row_mirror row_mask:0xf bank_mask:0xf\n"
"v_min_u32_dpp v13, v13, v13 row_mirror row_mask:0xf bank_mask:0xf\n"
"s_nop 0\n"
"v_min_u32_dpp v12, v12, v12 row_bcast:15 row_mask:0xa bank_mask:0xf\n"
"v_min_u32_dpp v13, v13, v13 row_bcast:15 row_mask:0xa bank_mask:0xf\n"
"s_nop 0\n"
"v_min_u32_dpp v12, v12, v12 row_bcast:31 row_mask:0xc bank_mask:0xf\n"
"v_min_u32_dpp v13, v13, v13 row_bcast:31 row_mask:0xc bank_mask:0xf\n"
"s_nop 0\n"
"v_mul_f32 v12, v12, v13\n"
"v_mul_f32 v12, 0x43c80000, v12\n"
"v_min_f32 v12, 0x71800000, v12\n"
"v_mul_f32 v13, s46, v12\n"
"s_nop 0\n"
"v_readlane_b32 s70, v12, 63\n"
"v_readlane_b32 s71, v12, 63\n"
"v_readlane_b32 s69, v13, 63\n"
"s_nop 1\n"
"v_mov_b64 v[228:229], s[70:71]\n"
"v_mov_b64 v[230:231], s[70:71]\n"
"s_waitcnt vmcnt(26)\n"
"v_readfirstlane_b32 s92, v1\n"
"v_mov_b64 v[0:1], s[70:71]\n"
"v_mov_b64 v[2:3], s[70:71]\n"
"s_mov_b32 s70, s46\n"
"s_xor_b32 s71, s69, s77\n"
"s_waitcnt lgkmcnt(0)\n"
"s_sub_u32 s80, s80, s64\n"
"s_sub_u32 s82, s82, s66\n"
"s_mul_i32 s80, s80, 10\n"
"s_mul_i32 s82, s82, 205\n"
"s_max_u32 s82, s82, 37000\n"
"s_cmp_gt_u32 s80, s82\n"
"s_cselect_b32 s64, 1, 0\n"
"s_cselect_b32 s68, 655360, 0\n"
"s_cmp_eq_u32 s42, 0\n"
"s_cselect_b32 s64, s64, 0\n"
"s_mov_b32 s65, 0\n"
"s_add_u32 s66, s66, 2800\n"
"s_mov_b32 s67, s66\n"
"s_mov_b32 s72, 118\n"
"s_mov_b64 vcc, s[64:65]\n"
"s_sub_u32 s63, s63, s44\n"
"s_add_u32 s80, s8, s63\n"
"s_addc_u32 s81, s9, 0\n"
"s_add_u32 s82, s44, 704\n"
"s_cmp_lg_u32 s68, 0\n"
"s_cselect_b32 s82, s82, 0\n"
"s_mov_b32 s83, 0x20000\n"
"s_nop 2\n"
"s_mul_i32 s60, s92, 0x9e3779b1\n"
"s_add_u32 s60, s60, 0x7f4a7c15\n"
"s_waitcnt vmcnt(15)\n"
"s_barrier\n"
"ds_read_b128 a[136:139], v246 offset:0\n"
"ds_read_b128 a[140:143], v247 offset:0\n"
"ds_read_b64_tr_b16 a[192:193], v244 offset:0\n"
"ds_read_b64_tr_b16 a[194:195], v244 offset:512\n"
"ds_read_b64_tr_b16 a[196:197], v244 offset:8192\n"
"ds_read_b64_tr_b16 a[198:199], v244 offset:8704\n"
"ds_read_b64_tr_b16 a[200:201], v245 offset:0\n"
"ds_read_b64_tr_b16 a[202:203], v245 offset:512\n"
"ds_read_b64_tr_b16 a[204:205], v245 offset:8192\n"
"ds_read_b64_tr_b16 a[206:207], v245 offset:8704\n"
"ds_read_b64_tr_b16 a[208:209], v244 offset:1024\n"
"ds_read_b64_tr_b16 a[210:211], v244 offset:1536\n"
"ds_read_b64_tr_b16 a[212:213], v244 offset:9216\n"
"ds_read_b64_tr_b16 a[214:215], v244 offset:9728\n"
"s_waitcnt lgkmcnt(10)\n"
"v_mfma_f32_16x16x32_bf16 v[112:115], a[192:195], a[136:139], v[228:231]\n"
"ds_read_b64_tr_b16 a[216:217], v245 offset:1024\n"
"ds_read_b64_tr_b16 a[218:219], v245 offset:1536\n"
"s_waitcnt lgkmcnt(10)\n"
"v_mfma_f32_16x16x32_bf16 v[112:115], a[196:199], a[140:143], v[112:115]\n"
"ds_read_b64_tr_b16 a[220:221], v245 offset:9216\n"
"ds_read_b64_tr_b16 a[222:223], v245 offset:9728\n"
"s_waitcnt lgkmcnt(10)\n"
"v_mfma_f32_16x16x32_bf16 v[116:119], a[200:203], a[136:139], v[228:231]\n"
"s_waitcnt lgkmcnt(8)\n"
"ds_read_b128 a[144:147], v246 offset:2048\n"
"v_mfma_f32_16x16x32_bf16 v[116:119], a[204:207], a[140:143], v[116:119]\n"
"s_waitcnt lgkmcnt(7)\n"
"ds_read_b128 a[148:151], v247 offset:2048\n"
"v_mfma_f32_16x16x32_bf16 v[120:123], a[208:211], a[136:139], v[228:231]\n"
"s_waitcnt lgkmcnt(6)\n"
"ds_read_b128 a[152:155], v246 offset:4096\n"
"v_mfma_f32_16x16x32_bf16 v[120:123], a[212:215], a[140:143], v[120:123]\n"
"s_waitcnt lgkmcnt(5)\n"
"ds_read_b128 a[156:159], v247 offset:4096\n"
"v_mfma_f32_16x16x32_bf16 v[124:127], a[216:219], a[136:139], v[228:231]\n"
"s_waitcnt lgkmcnt(4)\n"
"ds_read_b128 a[160:163], v252 offset:0\n"
"v_mfma_f32_16x16x32_bf16 v[124:127], a[220:223], a[140:143], v[124:127]\n"
"s_waitcnt lgkmcnt(4)\n"
"ds_read_b128 a[164:167], v253 offset:0\n"
"v_mfma_f32_16x16x32_bf16 v[140:143], a[216:219], a[144:147], v[228:231]\n"
"s_waitcnt lgkmcnt(4)\n"
"ds_read_b128 a[168:171], v250 offset:0\n"
"v_mfma_f32_16x16x32_bf16 v[140:143], a[220:223], a[148:151], v[140:143]\n"
"ds_read_b128 a[172:175], v251 offset:0\n"
"v_mfma_f32_16x16x32_bf16 v[136:139], a[208:211], a[144:147], v[228:231]\n"
"s_waitcnt vmcnt(11)\n"
"ds_read_b128 a[176:179], v250 offset:2048\n"
"v_mfma_f32_16x16x32_bf16 v[136:139], a[212:215], a[148:151], v[136:139]\n"
"s_barrier\n"
"v_mfma_f32_16x16x32_bf16 v[132:135], a[200:203], a[144:147], v[228:231]\n"
"s_add_u32 s40, s48, 256\n"
"s_xor_b32 m0, s50, 0xc000\n"
"ds_read_b128 a[180:183], v251 offset:2048\n"
"v_mfma_f32_16x16x32_bf16 v[132:135], a[204:207], a[148:151], v[132:135]\n"
"buffer_load_dwordx4 v224, s[28:31], s40 offen lds\n"
"v_mfma_f32_16x16x32_bf16 v[128:131], a[192:195], a[144:147], v[228:231]\n"
"ds_read_b128 a[184:187], v250 offset:4096\n"
"v_mfma_f32_16x16x32_bf16 v[128:131], a[196:199], a[148:151], v[128:131]\n"
"s_waitcnt lgkmcnt(5)\n"
"ds_read_b128 a[188:191], v251 offset:4096\n"
"v_mfma_f32_16x16x32_bf16 v[144:147], a[192:195], a[152:155], v[228:231]\n"
"ds_read_b64_tr_b16 a[224:225], v244 offset:16384\n"
"v_mfma_f32_16x16x32_bf16 v[144:147], a[196:199], a[156:159], v[144:147]\n"
"ds_read_b64_tr_b16 a[226:227], v244 offset:16896\n"
"v_mfma_f32_16x16x32_bf16 v[148:151], a[200:203], a[152:155], v[228:231]\n"
"ds_read_b64_tr_b16 a[228:229], v244 offset:24576\n"
"v_mfma_f32_16x16x32_bf16 v[148:151], a[204:207], a[156:159], v[148:151]\n"
"buffer_load_dwordx4 v225, s[28:31], s40 offen offset:1024 lds\n"
"v_mfma_f32_16x16x32_bf16 v[152:155], a[208:211], a[152:155], v[228:231]\n"
"ds_read_b64_tr_b16 a[230:231], v244 offset:25088\n"
"v_mfma_f32_16x16x32_bf16 v[152:155], a[212:215], a[156:159], v[152:155]\n"
"ds_read_b64_tr_b16 a[232:233], v245 offset:16384\n"
"v_mfma_f32_16x16x32_bf16 v[156:159], a[216:219], a[152:155], v[228:231]\n"
"ds_read_b64_tr_b16 a[234:235], v245 offset:16896\n"
"v_mfma_f32_16x16x32_bf16 v[156:159], a[220:223], a[156:159], v[156:159]\n"
"ds_read_b64_tr_b16 a[236:237], v245 offset:24576\n"
"v_mfma_f32_16x16x32_bf16 v[172:175], a[216:219], a[160:163], v[228:231]\n"
"ds_read_b64_tr_b16 a[238:239], v245 offset:25088\n"
"v_mfma_f32_16x16x32_bf16 v[172:175], a[220:223], a[164:167], v[172:175]\n"
"buffer_load_dwordx4 v226, s[28:31], s40 offen offset:2048 lds\n"
"v_mfma_f32_16x16x32_bf16 v[168:171], a[208:211], a[160:163], v[228:231]\n"
"ds_read_b64_tr_b16 a[240:241], v244 offset:17408\n"
"v_mfma_f32_16x16x32_bf16 v[168:171], a[212:215], a[164:167], v[168:171]\n"
"ds_read_b64_tr_b16 a[242:243], v244 offset:17920\n"
"v_mfma_f32_16x16x32_bf16 v[164:167], a[200:203], a[160:163], v[228:231]\n"
"ds_read_b64_tr_b16 a[244:245], v244 offset:25600\n"
"v_mfma_f32_16x16x32_bf16 v[164:167], a[204:207], a[164:167], v[164:167]\n"
"buffer_load_dwordx4 v227, s[28:31], s40 offen offset:3072 lds\n"
"v_mfma_f32_16x16x32_bf16 v[160:163], a[192:195], a[160:163], v[228:231]\n"
"s_cmp_eq_u32 s47, 8\n"
"ds_read_b64_tr_b16 a[246:247], v244 offset:26112\n"
"v_xor_b32 v244, s74, v244\n"
"v_mfma_f32_16x16x32_bf16 v[160:163], a[196:199], a[164:167], v[160:163]\n"
"s_waitcnt lgkmcnt(5)\n"
"s_cselect_b32 s41, s52, s58\n"
"ds_read_b64_tr_b16 a[248:249], v245 offset:17408\n"
"v_xor_b32 v246, s74, v246\n"
"v_mfma_f32_16x16x32_bf16 v[176:179], a[192:195], a[168:171], v[228:231]\n"
"s_add_u32 s48, s48, s41\n"
"ds_read_b64_tr_b16 a[250:251], v245 offset:17920\n"
"v_xor_b32 v247, s74, v247\n"
"v_mfma_f32_16x16x32_bf16 v[176:179], a[196:199], a[172:175], v[176:179]\n"
"s_sub_u32 s41, s48, 5242880\n"
"ds_read_b64_tr_b16 a[252:253], v245 offset:25600\n"
"v_xor_b32 v252, s74, v252\n"
"v_mfma_f32_16x16x32_bf16 v[180:183], a[200:203], a[168:171], v[228:231]\n"
"s_min_u32 s48, s48, s41\n"
"ds_read_b64_tr_b16 a[254:255], v245 offset:26112\n"
"v_xor_b32 v245, s74, v245\n"
"v_mfma_f32_16x16x32_bf16 v[180:183], a[204:207], a[172:175], v[180:183]\n"
"v_xor_b32 v253, s74, v253\n"
"v_xor_b32 v250, s74, v250\n"
"v_mfma_f32_16x16x32_bf16 v[184:187], a[208:211], a[168:171], v[228:231]\n"
"s_mov_b32 m0, s50\n"
"v_xor_b32 v251, s74, v251\n"
"v_mfma_f32_16x16x32_bf16 v[184:187], a[212:215], a[172:175], v[184:187]\n"
"buffer_load_dwordx4 v224, s[28:31], s48 offen lds\n"
"v_mfma_f32_16x16x32_bf16 v[188:191], a[216:219], a[168:171], v[228:231]\n"
"v_mfma_f32_16x16x32_bf16 v[188:191], a[220:223], a[172:175], v[188:191]\n"
"v_mfma_f32_16x16x32_bf16 v[204:207], a[216:219], a[176:179], v[228:231]\n"
"v_mfma_f32_16x16x32_bf16 v[204:207], a[220:223], a[180:183], v[204:207]\n"
"buffer_load_dwordx4 v225, s[28:31], s48 offen offset:1024 lds\n"
"v_mfma_f32_16x16x32_bf16 v[200:203], a[208:211], a[176:179], v[228:231]\n"
"v_mfma_f32_16x16x32_bf16 v[200:203], a[212:215], a[180:183], v[200:203]\n"
"v_mfma_f32_16x16x32_bf16 v[196:199], a[200:203], a[176:179], v[228:231]\n"
"v_mfma_f32_16x16x32_bf16 v[196:199], a[204:207], a[180:183], v[196:199]\n"
"v_mfma_f32_16x16x32_bf16 v[192:195], a[192:195], a[176:179], v[228:231]\n"
"v_mfma_f32_16x16x32_bf16 v[192:195], a[196:199], a[180:183], v[192:195]\n"
"buffer_load_dwordx4 v226, s[28:31], s48 offen offset:2048 lds\n"
"v_mfma_f32_16x16x32_bf16 v[208:211], a[192:195], a[184:187], v[228:231]\n"
"v_mfma_f32_16x16x32_bf16 v[208:211], a[196:199], a[188:191], v[208:211]\n"
"v_mfma_f32_16x16x32_bf16 v[212:215], a[200:203], a[184:187], v[228:231]\n"
"v_mfma_f32_16x16x32_bf16 v[212:215], a[204:207], a[188:191], v[212:215]\n"
"v_mfma_f32_16x16x32_bf16 v[216:219], a[208:211], a[184:187], v[228:231]\n"
"v_mfma_f32_16x16x32_bf16 v[216:219], a[212:215], a[188:191], v[216:219]\n"
"buffer_load_dwordx4 v227, s[28:31], s48 offen offset:3072 lds\n"
"v_mfma_f32_16x16x32_bf16 v[220:223], a[216:219], a[184:187], v[228:231]\n"
"s_xor_b32 s50, s50, s74\n"
"v_mfma_f32_16x16x32_bf16 v[220:223], a[220:223], a[188:191], v[220:223]\n"
"v_mfma_f32_16x16x32_bf16 v[0:3], a[224:227], a[136:139], v[228:231]\n"
"v_mfma_f32_16x16x32_bf16 v[0:3], a[228:231], a[140:143], v[0:3]\n"
"v_mfma_f32_16x16x32_bf16 v[4:7], a[232:235], a[136:139], v[228:231]\n"
"s_waitcnt vmcnt(8) lgkmcnt(0)\n"
"v_mfma_f32_16x16x32_bf16 v[4:7], a[236:239], a[140:143], v[4:7]\n"
"s_barrier\n"
"v_mfma_f32_16x16x32_bf16 v[8:11], a[240:243], a[136:139], v[228:231]\n"
"s_mov_b32 m0, s51\n"
"ds_read_b64_tr_b16 a[192:193], v244 offset:0\n"
"v_mfma_f32_16x16x32_bf16 v[8:11], a[244:247], a[140:143], v[8:11]\n"
"buffer_load_dwordx4 v248, s[24:27], s49 offen lds\n"
"v_mfma_f32_16x16x32_bf16 v[12:15], a[248:251], a[136:139], v[228:231]\n"
"ds_read_b64_tr_b16 a[194:195], v244 offset:512\n"
"v_mfma_f32_16x16x32_bf16 v[12:15], a[252:255], a[140:143], v[12:15]\n"
"ds_read_b128 a[136:139], v246 offset:0\n"
"v_mfma_f32_16x16x32_bf16 v[28:31], a[248:251], a[144:147], v[228:231]\n"
"ds_read_b64_tr_b16 a[196:197], v244 offset:8192\n"
"v_mfma_f32_16x16x32_bf16 v[28:31], a[252:255], a[148:151], v[28:31]\n"
"ds_read_b64_tr_b16 a[198:199], v244 offset:8704\n"
"v_mfma_f32_16x16x32_bf16 v[24:27], a[240:243], a[144:147], v[228:231]\n"
"ds_read_b128 a[140:143], v247 offset:0\n"
"v_mfma_f32_16x16x32_bf16 v[24:27], a[244:247], a[148:151], v[24:27]\n"
"buffer_load_dwordx4 v249, s[24:27], s49 offen offset:1024 lds\n"
"v_mfma_f32_16x16x32_bf16 v[20:23], a[232:235], a[144:147], v[228:231]\n"
"ds_read_b64_tr_b16 a[200:201], v245 offset:0\n"
"v_mfma_f32_16x16x32_bf16 v[20:23], a[236:239], a[148:151], v[20:23]\n"
"ds_read_b64_tr_b16 a[202:203], v245 offset:512\n"
"v_mfma_f32_16x16x32_bf16 v[16:19], a[224:227], a[144:147], v[228:231]\n"
"ds_read_b64_tr_b16 a[204:205], v245 offset:8192\n"
"v_mfma_f32_16x16x32_bf16 v[16:19], a[228:231], a[148:151], v[16:19]\n"
"ds_read_b64_tr_b16 a[206:207], v245 offset:8704\n"
"v_mfma_f32_16x16x32_bf16 v[32:35], a[224:227], a[152:155], v[228:231]\n"
"ds_read_b64_tr_b16 a[208:209], v244 offset:1024\n"
"v_mfma_f32_16x16x32_bf16 v[32:35], a[228:231], a[156:159], v[32:35]\n"
"buffer_load_dwordx4 v254, s[24:27], s49 offen offset:2048 lds\n"
"v_mfma_f32_16x16x32_bf16 v[36:39], a[232:235], a[152:155], v[228:231]\n"
"ds_read_b64_tr_b16 a[210:211], v244 offset:1536\n"
"v_mfma_f32_16x16x32_bf16 v[36:39], a[236:239], a[156:159], v[36:39]\n"
"ds_read_b64_tr_b16 a[212:213], v244 offset:9216\n"
"v_mfma_f32_16x16x32_bf16 v[40:43], a[240:243], a[152:155], v[228:231]\n"
"ds_read_b64_tr_b16 a[214:215], v244 offset:9728\n"
"v_mfma_f32_16x16x32_bf16 v[40:43], a[244:247], a[156:159], v[40:43]\n"
"ds_read_b64_tr_b16 a[216:217], v245 offset:1024\n"
"v_mfma_f32_16x16x32_bf16 v[44:47], a[248:251], a[152:155], v[228:231]\n"
"ds_read_b64_tr_b16 a[218:219], v245 offset:1536\n"
"v_mfma_f32_16x16x32_bf16 v[44:47], a[252:255], a[156:159], v[44:47]\n"
"buffer_load_dwordx4 v255, s[24:27], s49 offen offset:3072 lds\n"
"v_mfma_f32_16x16x32_bf16 v[60:63], a[248:251], a[160:163], v[228:231]\n"
"ds_read_b64_tr_b16 a[220:221], v245 offset:9216\n"
"v_mfma_f32_16x16x32_bf16 v[60:63], a[252:255], a[164:167], v[60:63]\n"
"ds_read_b64_tr_b16 a[222:223], v245 offset:9728\n"
"v_mfma_f32_16x16x32_bf16 v[56:59], a[240:243], a[160:163], v[228:231]\n"
"ds_read_b128 a[144:147], v246 offset:2048\n"
"v_mfma_f32_16x16x32_bf16 v[56:59], a[244:247], a[164:167], v[56:59]\n"
"s_add_u32 s40, s49, 40960\n"
"ds_read_b128 a[148:151], v247 offset:2048\n"
"v_mfma_f32_16x16x32_bf16 v[52:55], a[232:235], a[160:163], v[228:231]\n"
"s_add_u32 m0, s51, 4096\n"
"ds_read_b128 a[152:155], v246 offset:4096\n"
"v_mfma_f32_16x16x32_bf16 v[52:55], a[236:239], a[164:167], v[52:55]\n"
"buffer_load_dwordx4 v248, s[24:27], s40 offen lds\n"
"v_mfma_f32_16x16x32_bf16 v[48:51], a[224:227], a[160:163], v[228:231]\n"
"ds_read_b128 a[156:159], v247 offset:4096\n"
"v_mfma_f32_16x16x32_bf16 v[48:51], a[228:231], a[164:167], v[48:51]\n"
"ds_read_b128 a[160:163], v252 offset:0\n"
"v_mfma_f32_16x16x32_bf16 v[64:67], a[224:227], a[168:171], v[228:231]\n"
"ds_read_b128 a[164:167], v253 offset:0\n"
"v_mfma_f32_16x16x32_bf16 v[64:67], a[228:231], a[172:175], v[64:67]\n"
"v_mfma_f32_16x16x32_bf16 v[68:71], a[232:235], a[168:171], v[228:231]\n"
"v_mfma_f32_16x16x32_bf16 v[68:71], a[236:239], a[172:175], v[68:71]\n"
"buffer_load_dwordx4 v249, s[24:27], s40 offen offset:1024 lds\n"
"v_mfma_f32_16x16x32_bf16 v[72:75], a[240:243], a[168:171], v[228:231]\n"
"v_mfma_f32_16x16x32_bf16 v[72:75], a[244:247], a[172:175], v[72:75]\n"
"v_mfma_f32_16x16x32_bf16 v[76:79], a[248:251], a[168:171], v[228:231]\n"
"ds_read_b128 a[168:171], v250 offset:0\n"
"v_mfma_f32_16x16x32_bf16 v[76:79], a[252:255], a[172:175], v[76:79]\n"
"ds_read_b128 a[172:175], v251 offset:0\n"
"v_mfma_f32_16x16x32_bf16 v[92:95], a[248:251], a[176:179], v[228:231]\n"
"v_mfma_f32_16x16x32_bf16 v[92:95], a[252:255], a[180:183], v[92:95]\n"
"buffer_load_dwordx4 v254, s[24:27], s40 offen offset:2048 lds\n"
"v_mfma_f32_16x16x32_bf16 v[88:91], a[240:243], a[176:179], v[228:231]\n"
"s_add_u32 s49, s49, 128\n"
"v_mfma_f32_16x16x32_bf16 v[88:91], a[244:247], a[180:183], v[88:91]\n"
"s_sub_u32 s41, s49, 1280\n"
"v_mfma_f32_16x16x32_bf16 v[84:87], a[232:235], a[176:179], v[228:231]\n"
"s_min_u32 s49, s49, s41\n"
"v_mfma_f32_16x16x32_bf16 v[84:87], a[236:239], a[180:183], v[84:87]\n"
"s_xor_b32 s51, s51, s74\n"
"v_mfma_f32_16x16x32_bf16 v[80:83], a[224:227], a[176:179], v[228:231]\n"
"ds_read_b128 a[176:179], v250 offset:2048\n"
"v_mfma_f32_16x16x32_bf16 v[80:83], a[228:231], a[180:183], v[80:83]\n"
"ds_read_b128 a[180:183], v251 offset:2048\n"
"v_mfma_f32_16x16x32_bf16 v[96:99], a[224:227], a[184:187], v[228:231]\n"
"v_mfma_f32_16x16x32_bf16 v[96:99], a[228:231], a[188:191], v[96:99]\n"
"v_mfma_f32_16x16x32_bf16 v[100:103], a[232:235], a[184:187], v[228:231]\n"
"v_mfma_f32_16x16x32_bf16 v[100:103], a[236:239], a[188:191], v[100:103]\n"
"v_mfma_f32_16x16x32_bf16 v[104:107], a[240:243], a[184:187], v[228:231]\n"
"v_mfma_f32_16x16x32_bf16 v[104:107], a[244:247], a[188:191], v[104:107]\n"
"v_mfma_f32_16x16x32_bf16 v[108:111], a[248:251], a[184:187], v[228:231]\n"
"s_add_u32 s47, s47, 1\n"
"ds_read_b128 a[184:187], v250 offset:4096\n"
"v_mfma_f32_16x16x32_bf16 v[108:111], a[252:255], a[188:191], v[108:111]\n"
"s_waitcnt lgkmcnt(1)\n"
"ds_read_b128 a[188:191], v251 offset:4096\n"
"LPE_%=:\n"
"s_cmp_lg_u32 s68, 0\n"
"s_cbranch_scc1 LPN_%=\n"
"LPL_%=:\n"
"v_mfma_f32_16x16x32_bf16 v[112:115], a[192:195], a[136:139], v[112:115]\n"
"v_mfma_f32_16x16x32_bf16 v[112:115], a[196:199], a[140:143], v[112:115]\n"
"v_mfma_f32_16x16x32_bf16 v[116:119], a[200:203], a[136:139], v[116:119]\n"
"s_waitcnt vmcnt(11)\n"
"v_mfma_f32_16x16x32_bf16 v[116:119], a[204:207], a[140:143], v[116:119]\n"
"s_barrier\n"
"v_mfma_f32_16x16x32_bf16 v[120:123], a[208:211], a[136:139], v[120:123]\n"
"s_add_u32 s40, s48, 256\n"
"s_xor_b32 m0, s50, 0xc000\n"
"ds_read_b64_tr_b16 a[224:225], v244 offset:16384\n"
"v_mfma_f32_16x16x32_bf16 v[120:123], a[212:215], a[140:143], v[120:123]\n"
"buffer_load_dwordx4 v224, s[28:31], s40 offen lds\n"
"v_mfma_f32_16x16x32_bf16 v[124:127], a[216:219], a[136:139], v[124:127]\n"
"ds_read_b64_tr_b16 a[226:227], v244 offset:16896\n"
"v_mfma_f32_16x16x32_bf16 v[124:127], a[220:223], a[140:143], v[124:127]\n"
"ds_read_b64_tr_b16 a[228:229], v244 offset:24576\n"
"v_mfma_f32_16x16x32_bf16 v[140:143], a[216:219], a[144:147], v[140:143]\n"
"ds_read_b64_tr_b16 a[230:231], v244 offset:25088\n"
"v_mfma_f32_16x16x32_bf16 v[140:143], a[220:223], a[148:151], v[140:143]\n"
"ds_read_b64_tr_b16 a[232:233], v245 offset:16384\n"
"v_mfma_f32_16x16x32_bf16 v[136:139], a[208:211], a[144:147], v[136:139]\n"
"ds_read_b64_tr_b16 a[234:235], v245 offset:16896\n"
"v_mfma_f32_16x16x32_bf16 v[136:139], a[212:215], a[148:151], v[136:139]\n"
"buffer_load_dwordx4 v225, s[28:31], s40 offen offset:1024 lds\n"
"v_mfma_f32_16x16x32_bf16 v[132:135], a[200:203], a[144:147], v[132:135]\n"
"ds_read_b64_tr_b16 a[236:237], v245 offset:24576\n"
"v_mfma_f32_16x16x32_bf16 v[132:135], a[204:207], a[148:151], v[132:135]\n"
"ds_read_b64_tr_b16 a[238:239], v245 offset:25088\n"
"v_mfma_f32_16x16x32_bf16 v[128:131], a[192:195], a[144:147], v[128:131]\n"
"ds_read_b64_tr_b16 a[240:241], v244 offset:17408\n"
"v_mfma_f32_16x16x32_bf16 v[128:131], a[196:199], a[148:151], v[128:131]\n"
"ds_read_b64_tr_b16 a[242:243], v244 offset:17920\n"
"v_mfma_f32_16x16x32_bf16 v[144:147], a[192:195], a[152:155], v[144:147]\n"
"ds_read_b64_tr_b16 a[244:245], v244 offset:25600\n"
"v_mfma_f32_16x16x32_bf16 v[144:147], a[196:199], a[156:159], v[144:147]\n"
"buffer_load_dwordx4 v226, s[28:31], s40 offen offset:2048 lds\n"
"v_mfma_f32_16x16x32_bf16 v[148:151], a[200:203], a[152:155], v[148:151]\n"
"ds_read_b64_tr_b16 a[246:247], v244 offset:26112\n"
"v_mfma_f32_16x16x32_bf16 v[148:151], a[204:207], a[156:159], v[148:151]\n"
"ds_read_b64_tr_b16 a[248:249], v245 offset:17408\n"
"v_mfma_f32_16x16x32_bf16 v[152:155], a[208:211], a[152:155], v[152:155]\n"
"ds_read_b64_tr_b16 a[250:251], v245 offset:17920\n"
"v_mfma_f32_16x16x32_bf16 v[152:155], a[212:215], a[156:159], v[152:155]\n"
"ds_read_b64_tr_b16 a[252:253], v245 offset:25600\n"
"v_mfma_f32_16x16x32_bf16 v[156:159], a[216:219], a[152:155], v[156:159]\n"
"ds_read_b64_tr_b16 a[254:255], v245 offset:26112\n"
"v_mfma_f32_16x16x32_bf16 v[156:159], a[220:223], a[156:159], v[156:159]\n"
"buffer_load_dwordx4 v227, s[28:31], s40 offen offset:3072 lds\n"
"v_mfma_f32_16x16x32_bf16 v[172:175], a[216:219], a[160:163], v[172:175]\n"
"s_cmp_eq_u32 s47, 8\n"
"v_mfma_f32_16x16x32_bf16 v[172:175], a[220:223], a[164:167], v[172:175]\n"
"s_cselect_b32 s41, s52, s58\n"
"v_mfma_f32_16x16x32_bf16 v[168:171], a[208:211], a[160:163], v[168:171]\n"
"s_add_u32 s48, s48, s41\n"
"v_mfma_f32_16x16x32_bf16 v[168:171], a[212:215], a[164:167], v[168:171]\n"
"s_sub_u32 s41, s48, 5242880\n"
"v_mfma_f32_16x16x32_bf16 v[164:167], a[200:203], a[160:163], v[164:167]\n"
"s_min_u32 s48, s48, s41\n"
"v_mfma_f32_16x16x32_bf16 v[164:167], a[204:207], a[164:167], v[164:167]\n"
"v_xor_b32 v244, s74, v244\n"
"v_xor_b32 v246, s74, v246\n"
"v_mfma_f32_16x16x32_bf16 v[160:163], a[192:195], a[160:163], v[160:163]\n"
"s_mov_b32 m0, s50\n"
"v_xor_b32 v245, s74, v245\n"
"v_xor_b32 v247, s74, v247\n"
"v_mfma_f32_16x16x32_bf16 v[160:163], a[196:199], a[164:167], v[160:163]\n"
"buffer_load_dwordx4 v224, s[28:31], s48 offen lds\n"
"v_mfma_f32_16x16x32_bf16 v[176:179], a[192:195], a[168:171], v[176:179]\n"
"v_xor_b32 v252, s74, v252\n"
"v_xor_b32 v253, s74, v253\n"
"v_mfma_f32_16x16x32_bf16 v[176:179], a[196:199], a[172:175], v[176:179]\n"
"v_xor_b32 v250, s74, v250\n"
"v_xor_b32 v251, s74, v251\n"
"v_mfma_f32_16x16x32_bf16 v[180:183], a[200:203], a[168:171], v[180:183]\n"
"v_mfma_f32_16x16x32_bf16 v[180:183], a[204:207], a[172:175], v[180:183]\n"
"buffer_load_dwordx4 v225, s[28:31], s48 offen offset:1024 lds\n"
"v_mfma_f32_16x16x32_bf16 v[184:187], a[208:211], a[168:171], v[184:187]\n"
"v_mfma_f32_16x16x32_bf16 v[184:187], a[212:215], a[172:175], v[184:187]\n"
"v_mfma_f32_16x16x32_bf16 v[188:191], a[216:219], a[168:171], v[188:191]\n"
"v_mfma_f32_16x16x32_bf16 v[188:191], a[220:223], a[172:175], v[188:191]\n"
"v_mfma_f32_16x16x32_bf16 v[204:207], a[216:219], a[176:179], v[204:207]\n"
"v_mfma_f32_16x16x32_bf16 v[204:207], a[220:223], a[180:183], v[204:207]\n"
"buffer_load_dwordx4 v226, s[28:31], s48 offen offset:2048 lds\n"
"v_mfma_f32_16x16x32_bf16 v[200:203], a[208:211], a[176:179], v[200:203]\n"
"v_mfma_f32_16x16x32_bf16 v[200:203], a[212:215], a[180:183], v[200:203]\n"
"v_mfma_f32_16x16x32_bf16 v[196:199], a[200:203], a[176:179], v[196:199]\n"
"v_mfma_f32_16x16x32_bf16 v[196:199], a[204:207], a[180:183], v[196:199]\n"
"v_mfma_f32_16x16x32_bf16 v[192:195], a[192:195], a[176:179], v[192:195]\n"
"v_mfma_f32_16x16x32_bf16 v[192:195], a[196:199], a[180:183], v[192:195]\n"
"s_waitcnt lgkmcnt(0)\n"
"buffer_load_dwordx4 v227, s[28:31], s48 offen offset:3072 lds\n"
"v_mfma_f32_16x16x32_bf16 v[208:211], a[192:195], a[184:187], v[208:211]\n"
"s_xor_b32 s50, s50, s74\n"
"v_mfma_f32_16x16x32_bf16 v[208:211], a[196:199], a[188:191], v[208:211]\n"
"v_mfma_f32_16x16x32_bf16 v[212:215], a[200:203], a[184:187], v[212:215]\n"
"v_mfma_f32_16x16x32_bf16 v[212:215], a[204:207], a[188:191], v[212:215]\n"
"v_mfma_f32_16x16x32_bf16 v[216:219], a[208:211], a[184:187], v[216:219]\n"
"v_mfma_f32_16x16x32_bf16 v[216:219], a[212:215], a[188:191], v[216:219]\n"
"v_mfma_f32_16x16x32_bf16 v[220:223], a[216:219], a[184:187], v[220:223]\n"
"v_mfma_f32_16x16x32_bf16 v[220:223], a[220:223], a[188:191], v[220:223]\n"
"v_mfma_f32_16x16x32_bf16 v[0:3], a[224:227], a[136:139], v[0:3]\n"
"v_mfma_f32_16x16x32_bf16 v[0:3], a[228:231], a[140:143], v[0:3]\n"
"v_mfma_f32_16x16x32_bf16 v[4:7], a[232:235], a[136:139], v[4:7]\n"
"s_waitcnt vmcnt(8)\n"
"v_mfma_f32_16x16x32_bf16 v[4:7], a[236:239], a[140:143], v[4:7]\n"
"s_barrier\n"
"v_mfma_f32_16x16x32_bf16 v[8:11], a[240:243], a[136:139], v[8:11]\n"
"s_mov_b32 m0, s51\n"
"ds_read_b64_tr_b16 a[192:193], v244 offset:0\n"
"v_mfma_f32_16x16x32_bf16 v[8:11], a[244:247], a[140:143], v[8:11]\n"
"buffer_load_dwordx4 v248, s[24:27], s49 offen lds\n"
"v_mfma_f32_16x16x32_bf16 v[12:15], a[248:251], a[136:139], v[12:15]\n"
"ds_read_b64_tr_b16 a[194:195], v244 offset:512\n"
"v_mfma_f32_16x16x32_bf16 v[12:15], a[252:255], a[140:143], v[12:15]\n"
"ds_read_b128 a[136:139], v246 offset:0\n"
"v_mfma_f32_16x16x32_bf16 v[28:31], a[248:251], a[144:147], v[28:31]\n"
"ds_read_b64_tr_b16 a[196:197], v244 offset:8192\n"
"v_mfma_f32_16x16x32_bf16 v[28:31], a[252:255], a[148:151], v[28:31]\n"
"ds_read_b64_tr_b16 a[198:199], v244 offset:8704\n"
"v_mfma_f32_16x16x32_bf16 v[24:27], a[240:243], a[144:147], v[24:27]\n"
"ds_read_b128 a[140:143], v247 offset:0\n"
"v_mfma_f32_16x16x32_bf16 v[24:27], a[244:247], a[148:151], v[24:27]\n"
"buffer_load_dwordx4 v249, s[24:27], s49 offen offset:1024 lds\n"
"v_mfma_f32_16x16x32_bf16 v[20:23], a[232:235], a[144:147], v[20:23]\n"
"ds_read_b64_tr_b16 a[200:201], v245 offset:0\n"
"v_mfma_f32_16x16x32_bf16 v[20:23], a[236:239], a[148:151], v[20:23]\n"
"ds_read_b64_tr_b16 a[202:203], v245 offset:512\n"
"v_mfma_f32_16x16x32_bf16 v[16:19], a[224:227], a[144:147], v[16:19]\n"
"ds_read_b64_tr_b16 a[204:205], v245 offset:8192\n"
"v_mfma_f32_16x16x32_bf16 v[16:19], a[228:231], a[148:151], v[16:19]\n"
"ds_read_b64_tr_b16 a[206:207], v245 offset:8704\n"
"v_mfma_f32_16x16x32_bf16 v[32:35], a[224:227], a[152:155], v[32:35]\n"
"ds_read_b64_tr_b16 a[208:209], v244 offset:1024\n"
"v_mfma_f32_16x16x32_bf16 v[32:35], a[228:231], a[156:159], v[32:35]\n"
"buffer_load_dwordx4 v254, s[24:27], s49 offen offset:2048 lds\n"
"v_mfma_f32_16x16x32_bf16 v[36:39], a[232:235], a[152:155], v[36:39]\n"
"ds_read_b64_tr_b16 a[210:211], v244 offset:1536\n"
"v_mfma_f32_16x16x32_bf16 v[36:39], a[236:239], a[156:159], v[36:39]\n"
"ds_read_b64_tr_b16 a[212:213], v244 offset:9216\n"
"v_mfma_f32_16x16x32_bf16 v[40:43], a[240:243], a[152:155], v[40:43]\n"
"ds_read_b64_tr_b16 a[214:215], v244 offset:9728\n"
"v_mfma_f32_16x16x32_bf16 v[40:43], a[244:247], a[156:159], v[40:43]\n"
"ds_read_b64_tr_b16 a[216:217], v245 offset:1024\n"
"v_mfma_f32_16x16x32_bf16 v[44:47], a[248:251], a[152:155], v[44:47]\n"
"ds_read_b64_tr_b16 a[218:219], v245 offset:1536\n"
"v_mfma_f32_16x16x32_bf16 v[44:47], a[252:255], a[156:159], v[44:47]\n"
"buffer_load_dwordx4 v255, s[24:27], s49 offen offset:3072 lds\n"
"v_mfma_f32_16x16x32_bf16 v[60:63], a[248:251], a[160:163], v[60:63]\n"
"ds_read_b64_tr_b16 a[220:221], v245 offset:9216\n"
"v_mfma_f32_16x16x32_bf16 v[60:63], a[252:255], a[164:167], v[60:63]\n"
"ds_read_b64_tr_b16 a[222:223], v245 offset:9728\n"
"v_mfma_f32_16x16x32_bf16 v[56:59], a[240:243], a[160:163], v[56:59]\n"
"ds_read_b128 a[144:147], v246 offset:2048\n"
"v_mfma_f32_16x16x32_bf16 v[56:59], a[244:247], a[164:167], v[56:59]\n"
"s_add_u32 s40, s49, 40960\n"
"ds_read_b128 a[148:151], v247 offset:2048\n"
"v_mfma_f32_16x16x32_bf16 v[52:55], a[232:235], a[160:163], v[52:55]\n"
"s_add_u32 m0, s51, 4096\n"
"ds_read_b128 a[152:155], v246 offset:4096\n"
"v_mfma_f32_16x16x32_bf16 v[52:55], a[236:239], a[164:167], v[52:55]\n"
"buffer_load_dwordx4 v248, s[24:27], s40 offen lds\n"
"v_mfma_f32_16x16x32_bf16 v[48:51], a[224:227], a[160:163], v[48:51]\n"
"ds_read_b128 a[156:159], v247 offset:4096\n"
"v_mfma_f32_16x16x32_bf16 v[48:51], a[228:231], a[164:167], v[48:51]\n"
"ds_read_b128 a[160:163], v252 offset:0\n"
"v_mfma_f32_16x16x32_bf16 v[64:67], a[224:227], a[168:171], v[64:67]\n"
"ds_read_b128 a[164:167], v253 offset:0\n"
"v_mfma_f32_16x16x32_bf16 v[64:67], a[228:231], a[172:175], v[64:67]\n"
"v_mfma_f32_16x16x32_bf16 v[68:71], a[232:235], a[168:171], v[68:71]\n"
"v_mfma_f32_16x16x32_bf16 v[68:71], a[236:239], a[172:175], v[68:71]\n"
"buffer_load_dwordx4 v249, s[24:27], s40 offen offset:1024 lds\n"
"v_mfma_f32_16x16x32_bf16 v[72:75], a[240:243], a[168:171], v[72:75]\n"
"v_mfma_f32_16x16x32_bf16 v[72:75], a[244:247], a[172:175], v[72:75]\n"
"v_mfma_f32_16x16x32_bf16 v[76:79], a[248:251], a[168:171], v[76:79]\n"
"ds_read_b128 a[168:171], v250 offset:0\n"
"v_mfma_f32_16x16x32_bf16 v[76:79], a[252:255], a[172:175], v[76:79]\n"
"ds_read_b128 a[172:175], v251 offset:0\n"
"v_mfma_f32_16x16x32_bf16 v[92:95], a[248:251], a[176:179], v[92:95]\n"
"v_mfma_f32_16x16x32_bf16 v[92:95], a[252:255], a[180:183], v[92:95]\n"
"buffer_load_dwordx4 v254, s[24:27], s40 offen offset:2048 lds\n"
"v_mfma_f32_16x16x32_bf16 v[88:91], a[240:243], a[176:179], v[88:91]\n"
"s_add_u32 s49, s49, 128\n"
"v_mfma_f32_16x16x32_bf16 v[88:91], a[244:247], a[180:183], v[88:91]\n"
"s_sub_u32 s41, s49, 1280\n"
"v_mfma_f32_16x16x32_bf16 v[84:87], a[232:235], a[176:179], v[84:87]\n"
"s_min_u32 s49, s49, s41\n"
"v_mfma_f32_16x16x32_bf16 v[84:87], a[236:239], a[180:183], v[84:87]\n"
"s_xor_b32 s51, s51, s74\n"
"v_mfma_f32_16x16x32_bf16 v[80:83], a[224:227], a[176:179], v[80:83]\n"
"ds_read_b128 a[176:179], v250 offset:2048\n"
"v_mfma_f32_16x16x32_bf16 v[80:83], a[228:231], a[180:183], v[80:83]\n"
"ds_read_b128 a[180:183], v251 offset:2048\n"
"v_mfma_f32_16x16x32_bf16 v[96:99], a[224:227], a[184:187], v[96:99]\n"
"v_mfma_f32_16x16x32_bf16 v[96:99], a[228:231], a[188:191], v[96:99]\n"
"v_mfma_f32_16x16x32_bf16 v[100:103], a[232:235], a[184:187], v[100:103]\n"
"v_mfma_f32_16x16x32_bf16 v[100:103], a[236:239], a[188:191], v[100:103]\n"
"v_mfma_f32_16x16x32_bf16 v[104:107], a[240:243], a[184:187], v[104:107]\n"
"v_mfma_f32_16x16x32_bf16 v[104:107], a[244:247], a[188:191], v[104:107]\n"
"v_mfma_f32_16x16x32_bf16 v[108:111], a[248:251], a[184:187], v[108:111]\n"
"s_add_u32 s47, s47, 1\n"
"ds_read_b128 a[184:187], v250 offset:4096\n"
"v_mfma_f32_16x16x32_bf16 v[108:111], a[252:255], a[188:191], v[108:111]\n"
"s_waitcnt lgkmcnt(1)\n"
"s_cmp_lg_u32 s47, s62\n"
"ds_read_b128 a[188:191], v251 offset:4096\n"
"s_cbranch_scc1 LPL_%=\n"
"LPX_%=:\n"
"s_cmp_eq_u32 s47, 18\n"
"s_cbranch_scc1 LPT2_%=\n"
"v_mfma_f32_16x16x32_bf16 v[112:115], a[192:195], a[136:139], v[112:115]\n"
"s_add_u32 s78, s96, 8192\n"
"v_mov_b32 v241, 1\n"
"v_mfma_f32_16x16x32_bf16 v[112:115], a[196:199], a[140:143], v[112:115]\n"
"s_mov_b64 exec, 1\n"
"buffer_store_dword v241, off, s[32:35], s96 sc0 sc1\n"
"buffer_store_dword v241, off, s[32:35], s78 sc0 sc1\n"
"s_mov_b64 exec, -1\n"
"v_mfma_f32_16x16x32_bf16 v[116:119], a[200:203], a[136:139], v[116:119]\n"
"s_waitcnt vmcnt(13)\n"
"v_mfma_f32_16x16x32_bf16 v[116:119], a[204:207], a[140:143], v[116:119]\n"
"s_barrier\n"
"v_mfma_f32_16x16x32_bf16 v[120:123], a[208:211], a[136:139], v[120:123]\n"
"s_add_u32 s40, s48, 256\n"
"s_xor_b32 m0, s50, 0xc000\n"
"ds_read_b64_tr_b16 a[238:239], v245 offset:25088\n"
"v_mfma_f32_16x16x32_bf16 v[120:123], a[212:215], a[140:143], v[120:123]\n"
"buffer_load_dwordx4 v224, s[28:31], s40 offen lds\n"
"v_mfma_f32_16x16x32_bf16 v[124:127], a[216:219], a[136:139], v[124:127]\n"
"v_mfma_f32_16x16x32_bf16 v[124:127], a[220:223], a[140:143], v[124:127]\n"
"v_mfma_f32_16x16x32_bf16 v[188:191], a[216:219], a[168:171], v[188:191]\n"
"ds_read_b64_tr_b16 a[254:255], v245 offset:26112\n"
"v_mfma_f32_16x16x32_bf16 v[188:191], a[220:223], a[172:175], v[188:191]\n"
"ds_read_b64_tr_b16 a[252:253], v245 offset:25600\n"
"v_mfma_f32_16x16x32_bf16 v[184:187], a[208:211], a[168:171], v[184:187]\n"
"ds_read_b64_tr_b16 a[224:225], v244 offset:16384\n"
"ds_read_b64_tr_b16 a[228:229], v244 offset:24576\n"
"v_mfma_f32_16x16x32_bf16 v[184:187], a[212:215], a[172:175], v[184:187]\n"
"buffer_load_dwordx4 v225, s[28:31], s40 offen offset:1024 lds\n"
"v_mfma_f32_16x16x32_bf16 v[180:183], a[200:203], a[168:171], v[180:183]\n"
"v_mfma_f32_16x16x32_bf16 v[180:183], a[204:207], a[172:175], v[180:183]\n"
"v_mfma_f32_16x16x32_bf16 v[176:179], a[192:195], a[168:171], v[176:179]\n"
"ds_read_b64_tr_b16 a[230:231], v244 offset:25088\n"
"v_mfma_f32_16x16x32_bf16 v[176:179], a[196:199], a[172:175], v[176:179]\n"
"ds_read_b64_tr_b16 a[244:245], v244 offset:25600\n"
"v_mfma_f32_16x16x32_bf16 v[192:195], a[192:195], a[176:179], v[192:195]\n"
"ds_read_b64_tr_b16 a[226:227], v244 offset:16896\n"
"ds_read_b64_tr_b16 a[250:251], v245 offset:17920\n"
"v_mfma_f32_16x16x32_bf16 v[192:195], a[196:199], a[180:183], v[192:195]\n"
"buffer_load_dwordx4 v226, s[28:31], s40 offen offset:2048 lds\n"
"v_mfma_f32_16x16x32_bf16 v[196:199], a[200:203], a[176:179], v[196:199]\n"
"v_mfma_f32_16x16x32_bf16 v[196:199], a[204:207], a[180:183], v[196:199]\n"
"v_mfma_f32_16x16x32_bf16 v[200:203], a[208:211], a[176:179], v[200:203]\n"
"ds_read_b64_tr_b16 a[236:237], v245 offset:24576\n"
"ds_read_b64_tr_b16 a[246:247], v244 offset:26112\n"
"v_mfma_f32_16x16x32_bf16 v[200:203], a[212:215], a[180:183], v[200:203]\n"
"ds_read_b64_tr_b16 a[240:241], v244 offset:17408\n"
"v_mfma_f32_16x16x32_bf16 v[204:207], a[216:219], a[176:179], v[204:207]\n"
"ds_read_b64_tr_b16 a[242:243], v244 offset:17920\n"
"ds_read_b64_tr_b16 a[248:249], v245 offset:17408\n"
"v_mfma_f32_16x16x32_bf16 v[204:207], a[220:223], a[180:183], v[204:207]\n"
"s_waitcnt lgkmcnt(7)\n"
"buffer_load_dwordx4 v227, s[28:31], s40 offen offset:3072 lds\n"
"v_mfma_f32_16x16x32_bf16 v[220:223], a[216:219], a[184:187], v[220:223]\n"
"s_cmp_eq_u32 s47, 8\n"
"v_mfma_f32_16x16x32_bf16 v[220:223], a[220:223], a[188:191], v[220:223]\n"
"s_cselect_b32 s41, s52, s58\n"
"s_nop 1\n"
"v_pk_fma_f32 v[176:177], v[176:177], s[70:71], s[70:71] op_sel:[0,0,1] op_sel_hi:[1,0,1]\nv_pk_fma_f32 v[178:179], v[178:179], s[70:71], s[70:71] op_sel:[0,0,1] op_sel_hi:[1,0,1]\nv_pk_fma_f32 v[180:181], v[180:181], s[70:71], s[70:71] op_sel:[0,0,1] op_sel_hi:[1,0,1]\nv_pk_fma_f32 v[182:183], v[182:183], s[70:71], s[70:71] op_sel:[0,0,1] op_sel_hi:[1,0,1]\nv_pk_fma_f32 v[184:185], v[184:185], s[70:71], s[70:71] op_sel:[0,0,1] op_sel_hi:[1,0,1]\nv_pk_fma_f32 v[186:187], v[186:187], s[70:71], s[70:71] op_sel:[0,0,1] op_sel_hi:[1,0,1]\nv_pk_fma_f32 v[188:189], v[188:189], s[70:71], s[70:71] op_sel:[0,0,1] op_sel_hi:[1,0,1]\nv_pk_fma_f32 v[190:191], v[190:191], s[70:71], s[70:71] op_sel:[0,0,1] op_sel_hi:[1,0,1]\nv_pk_fma_f32 v[192:193], v[192:193], s[70:71], s[70:71] op_sel:[0,0,1] op_sel_hi:[1,0,1]\nv_pk_fma_f32 v[194:195], v[194:195], s[70:71], s[70:71] op_sel:[0,0,1] op_sel_hi:[1,0,1]\nv_pk_fma_f32 v[196:197], v[196:197], s[70:71], s[70:71] op_sel:[0,0,1] op_sel_hi:[1,0,1]\nv_pk_fma_f32 v[198:199], v[198:199], s[70:71], s[70:71] op_sel:[0,0,1] op_sel_hi:[1,0,1]\nv_pk_fma_f32 v[200:201], v[200:201], s[70:71], s[70:71] op_sel:[0,0,1] op_sel_hi:[1,0,1]\nv_pk_fma_f32 v[202:203], v[202:203], s[70:71], s[70:71] op_sel:[0,0,1] op_sel_hi:[1,0,1]\nv_pk_fma_f32 v[204:205], v[204:205], s[70:71], s[70:71] op_sel:[0,0,1] op_sel_hi:[1,0,1]\nv_pk_fma_f32 v[206:207], v[206:207], s[70:71], s[70:71] op_sel:[0,0,1] op_sel_hi:[1,0,1]\n"
"v_mfma_f32_16x16x32_bf16 v[216:219], a[208:211], a[184:187], v[216:219]\n"
"s_add_u32 s48, s48, s41\n"
"v_cvt_pk_bf16_f32 v192, v192, v193\n"
"v_cvt_pk_bf16_f32 v193, v194, v195\n"
"v_mfma_f32_16x16x32_bf16 v[216:219], a[212:215], a[188:191], v[216:219]\n"
"s_sub_u32 s41, s48, 5242880\n"
"v_cvt_pk_bf16_f32 v176, v176, v177\n"
"v_cvt_pk_bf16_f32 v177, v178, v179\n"
"v_mfma_f32_16x16x32_bf16 v[212:215], a[200:203], a[184:187], v[212:215]\n"
"s_min_u32 s48, s48, s41\n"
"v_cvt_pk_bf16_f32 v178, v180, v181\n"
"v_cvt_pk_bf16_f32 v179, v182, v183\n"
"v_mfma_f32_16x16x32_bf16 v[212:215], a[204:207], a[188:191], v[212:215]\n"
"ds_read_b64_tr_b16 a[234:235], v245 offset:16896\n"
"v_xor_b32 v246, s74, v246\n"
"v_mfma_f32_16x16x32_bf16 v[208:211], a[192:195], a[184:187], v[208:211]\n"
"s_mov_b32 m0, s50\n"
"v_and_b32 v182, 0xf0, v242\n"
"v_cvt_pk_bf16_f32 v200, v200, v201\n"
"v_mfma_f32_16x16x32_bf16 v[208:211], a[196:199], a[188:191], v[208:211]\n"
"buffer_load_dwordx4 v224, s[28:31], s48 offen lds\n"
"v_mfma_f32_16x16x32_bf16 v[160:163], a[192:195], a[160:163], v[160:163]\n"
"v_bfe_u32 v180, v242, 9, 1\n"
"v_lshl_add_u32 v180, v180, 8, v182\n"
"v_mfma_f32_16x16x32_bf16 v[160:163], a[196:199], a[164:167], v[160:163]\n"
"v_bfe_u32 v182, v242, 8, 1\n"
"v_cvt_pk_bf16_f32 v184, v184, v185\n"
"v_mfma_f32_16x16x32_bf16 v[164:167], a[200:203], a[160:163], v[164:167]\n"
"ds_read_b64_tr_b16 a[232:233], v245 offset:16384\n"
"v_xor_b32 v247, s74, v247\n"
"v_mfma_f32_16x16x32_bf16 v[164:167], a[204:207], a[164:167], v[164:167]\n"
"buffer_load_dwordx4 v225, s[28:31], s48 offen offset:1024 lds\n"
"v_mfma_f32_16x16x32_bf16 v[168:171], a[208:211], a[160:163], v[168:171]\n"
"v_cvt_pk_bf16_f32 v201, v202, v203\n"
"v_xor_b32 v253, s74, v253\n"
"v_mfma_f32_16x16x32_bf16 v[168:171], a[212:215], a[164:167], v[168:171]\n"
"v_cvt_pk_bf16_f32 v194, v196, v197\n"
"v_xor_b32 v244, s74, v244\n"
"v_mfma_f32_16x16x32_bf16 v[172:175], a[216:219], a[160:163], v[172:175]\n"
"v_lshl_add_u32 v180, v182, 3, v180\n"
"v_cvt_pk_bf16_f32 v185, v186, v187\n"
"v_mfma_f32_16x16x32_bf16 v[172:175], a[220:223], a[164:167], v[172:175]\n"
"v_cvt_pk_bf16_f32 v186, v188, v189\n"
"v_cvt_pk_bf16_f32 v187, v190, v191\n"
"v_mfma_f32_16x16x32_bf16 v[140:143], a[216:219], a[144:147], v[140:143]\n"
"v_cvt_pk_bf16_f32 v203, v206, v207\n"
"v_bfe_u32 v181, v242, 9, 1\n"
"v_mfma_f32_16x16x32_bf16 v[140:143], a[220:223], a[148:151], v[140:143]\n"
"buffer_load_dwordx4 v226, s[28:31], s48 offen offset:2048 lds\n"
"v_mfma_f32_16x16x32_bf16 v[136:139], a[208:211], a[144:147], v[136:139]\n"
"v_xor_b32_e64 v182, s42, 1\n"
"v_lshl_add_u32 v180, v182, 10, v180\n"
"v_mfma_f32_16x16x32_bf16 v[136:139], a[212:215], a[148:151], v[136:139]\n"
"v_cvt_pk_bf16_f32 v202, v204, v205\n"
"v_xor_b32 v252, s74, v252\n"
"v_mfma_f32_16x16x32_bf16 v[132:135], a[200:203], a[144:147], v[132:135]\n"
"v_add_u32 v180, 94208, v180\n"
"v_and_b32 v182, 0xf0, v242\n"
"v_mfma_f32_16x16x32_bf16 v[132:135], a[204:207], a[148:151], v[132:135]\n"
"ds_write2st64_b64 v180, v[184:185], v[186:187] offset0:64 offset1:65\n"
"v_pk_fma_f32 v[208:209], v[208:209], s[70:71], s[70:71] op_sel:[0,0,1] op_sel_hi:[1,0,1]\nv_pk_fma_f32 v[210:211], v[210:211], s[70:71], s[70:71] op_sel:[0,0,1] op_sel_hi:[1,0,1]\nv_pk_fma_f32 v[212:213], v[212:213], s[70:71], s[70:71] op_sel:[0,0,1] op_sel_hi:[1,0,1]\nv_pk_fma_f32 v[214:215], v[214:215], s[70:71], s[70:71] op_sel:[0,0,1] op_sel_hi:[1,0,1]\nv_pk_fma_f32 v[216:217], v[216:217], s[70:71], s[70:71] op_sel:[0,0,1] op_sel_hi:[1,0,1]\nv_pk_fma_f32 v[218:219], v[218:219], s[70:71], s[70:71] op_sel:[0,0,1] op_sel_hi:[1,0,1]\nv_pk_fma_f32 v[220:221], v[220:221], s[70:71], s[70:71] op_sel:[0,0,1] op_sel_hi:[1,0,1]\nv_pk_fma_f32 v[222:223], v[222:223], s[70:71], s[70:71] op_sel:[0,0,1] op_sel_hi:[1,0,1]\nv_pk_fma_f32 v[160:161], v[160:161], s[70:71], s[70:71] op_sel:[0,0,1] op_sel_hi:[1,0,1]\nv_pk_fma_f32 v[162:163], v[162:163], s[70:71], s[70:71] op_sel:[0,0,1] op_sel_hi:[1,0,1]\nv_pk_fma_f32 v[164:165], v[164:165], s[70:71], s[70:71] op_sel:[0,0,1] op_sel_hi:[1,0,1]\nv_pk_fma_f32 v[166:167], v[166:167], s[70:71], s[70:71] op_sel:[0,0,1] op_sel_hi:[1,0,1]\nv_pk_fma_f32 v[168:169], v[168:169], s[70:71], s[70:71] op_sel:[0,0,1] op_sel_hi:[1,0,1]\nv_pk_fma_f32 v[170:171], v[170:171], s[70:71], s[70:71] op_sel:[0,0,1] op_sel_hi:[1,0,1]\nv_pk_fma_f32 v[172:173], v[172:173], s[70:71], s[70:71] op_sel:[0,0,1] op_sel_hi:[1,0,1]\nv_pk_fma_f32 v[174:175], v[174:175], s[70:71], s[70:71] op_sel:[0,0,1] op_sel_hi:[1,0,1]\n"
"v_cvt_pk_bf16_f32 v216, v216, v217\n"
"v_mfma_f32_16x16x32_bf16 v[128:131], a[192:195], a[144:147], v[128:131]\n"
"v_cvt_pk_bf16_f32 v168, v168, v169\n"
"v_xor_b32 v250, s74, v250\n"
"v_mfma_f32_16x16x32_bf16 v[128:131], a[196:199], a[148:151], v[128:131]\n"
"buffer_load_dwordx4 v227, s[28:31], s48 offen offset:3072 lds\n"
"v_mfma_f32_16x16x32_bf16 v[144:147], a[192:195], a[152:155], v[144:147]\n"
"s_xor_b32 s50, s50, s74\n"
"v_cvt_pk_bf16_f32 v208, v208, v209\n"
"v_xor_b32 v251, s74, v251\n"
"v_mfma_f32_16x16x32_bf16 v[144:147], a[196:199], a[156:159], v[144:147]\n"
"v_cvt_pk_bf16_f32 v217, v218, v219\n"
"v_cvt_pk_bf16_f32 v169, v170, v171\n"
"v_mfma_f32_16x16x32_bf16 v[148:151], a[200:203], a[152:155], v[148:151]\n"
"v_cvt_pk_bf16_f32 v195, v198, v199\n"
"v_cvt_pk_bf16_f32 v209, v210, v211\n"
"v_mfma_f32_16x16x32_bf16 v[148:151], a[204:207], a[156:159], v[148:151]\n"
"ds_write2st64_b64 v180, v[192:193], v[194:195] offset0:88 offset1:89\n"
"v_pk_fma_f32 v[112:113], v[112:113], s[70:71], s[70:71] op_sel:[0,0,1] op_sel_hi:[1,0,1]\nv_pk_fma_f32 v[114:115], v[114:115], s[70:71], s[70:71] op_sel:[0,0,1] op_sel_hi:[1,0,1]\nv_pk_fma_f32 v[116:117], v[116:117], s[70:71], s[70:71] op_sel:[0,0,1] op_sel_hi:[1,0,1]\nv_pk_fma_f32 v[118:119], v[118:119], s[70:71], s[70:71] op_sel:[0,0,1] op_sel_hi:[1,0,1]\nv_pk_fma_f32 v[120:121], v[120:121], s[70:71], s[70:71] op_sel:[0,0,1] op_sel_hi:[1,0,1]\nv_pk_fma_f32 v[122:123], v[122:123], s[70:71], s[70:71] op_sel:[0,0,1] op_sel_hi:[1,0,1]\nv_pk_fma_f32 v[124:125], v[124:125], s[70:71], s[70:71] op_sel:[0,0,1] op_sel_hi:[1,0,1]\nv_pk_fma_f32 v[126:127], v[126:127], s[70:71], s[70:71] op_sel:[0,0,1] op_sel_hi:[1,0,1]\nv_pk_fma_f32 v[128:129], v[128:129], s[70:71], s[70:71] op_sel:[0,0,1] op_sel_hi:[1,0,1]\nv_pk_fma_f32 v[130:131], v[130:131], s[70:71], s[70:71] op_sel:[0,0,1] op_sel_hi:[1,0,1]\nv_pk_fma_f32 v[132:133], v[132:133], s[70:71], s[70:71] op_sel:[0,0,1] op_sel_hi:[1,0,1]\nv_pk_fma_f32 v[134:135], v[134:135], s[70:71], s[70:71] op_sel:[0,0,1] op_sel_hi:[1,0,1]\nv_pk_fma_f32 v[136:137], v[136:137], s[70:71], s[70:71] op_sel:[0,0,1] op_sel_hi:[1,0,1]\nv_pk_fma_f32 v[138:139], v[138:139], s[70:71], s[70:71] op_sel:[0,0,1] op_sel_hi:[1,0,1]\nv_pk_fma_f32 v[140:141], v[140:141], s[70:71], s[70:71] op_sel:[0,0,1] op_sel_hi:[1,0,1]\nv_pk_fma_f32 v[142:143], v[142:143], s[70:71], s[70:71] op_sel:[0,0,1] op_sel_hi:[1,0,1]\n"
"v_mfma_f32_16x16x32_bf16 v[152:155], a[208:211], a[152:155], v[152:155]\n"
"v_cvt_pk_bf16_f32 v218, v220, v221\n"
"v_cvt_pk_bf16_f32 v136, v136, v137\n"
"v_mfma_f32_16x16x32_bf16 v[152:155], a[212:215], a[156:159], v[152:155]\n"
"v_cvt_pk_bf16_f32 v137, v138, v139\n"
"v_cvt_pk_bf16_f32 v139, v142, v143\n"
"v_mfma_f32_16x16x32_bf16 v[156:159], a[216:219], a[152:155], v[156:159]\n"
"v_cvt_pk_bf16_f32 v128, v128, v129\n"
"v_cvt_pk_bf16_f32 v138, v140, v141\n"
"v_mfma_f32_16x16x32_bf16 v[156:159], a[220:223], a[156:159], v[156:159]\n"
"s_waitcnt lgkmcnt(1)\n"
"v_cvt_pk_bf16_f32 v112, v112, v113\n"
"v_xor_b32 v245, s74, v245\n"
"v_mfma_f32_16x16x32_bf16 v[96:99], a[224:227], a[184:187], v[96:99]\n"
"v_cvt_pk_bf16_f32 v120, v120, v121\n"
"v_cvt_pk_bf16_f32 v129, v130, v131\n"
"v_mfma_f32_16x16x32_bf16 v[96:99], a[228:231], a[188:191], v[96:99]\n"
"ds_write2st64_b64 v180, v[200:201], v[202:203] offset0:96 offset1:97\n"
"v_cvt_pk_bf16_f32 v219, v222, v223\n"
"v_cvt_pk_bf16_f32 v170, v172, v173\n"
"v_cvt_pk_bf16_f32 v131, v134, v135\n"
"v_lshl_add_u32 v181, v181, 8, v182\n"
"v_bfe_u32 v182, v242, 8, 1\n"
"v_lshl_add_u32 v181, v182, 9, v181\n"
"v_mfma_f32_16x16x32_bf16 v[100:103], a[232:235], a[184:187], v[100:103]\n"
"s_waitcnt vmcnt(10)\n"
"v_permlane16_swap_b32 v136, v138\n"
"v_mfma_f32_16x16x32_bf16 v[100:103], a[236:239], a[188:191], v[100:103]\n"
"s_barrier\n"
"ds_write2st64_b64 v180, v[216:217], v[218:219] offset0:128 offset1:129\n"
"v_pk_fma_f32 v[144:145], v[144:145], s[70:71], s[70:71] op_sel:[0,0,1] op_sel_hi:[1,0,1]\nv_pk_fma_f32 v[146:147], v[146:147], s[70:71], s[70:71] op_sel:[0,0,1] op_sel_hi:[1,0,1]\nv_pk_fma_f32 v[148:149], v[148:149], s[70:71], s[70:71] op_sel:[0,0,1] op_sel_hi:[1,0,1]\nv_pk_fma_f32 v[150:151], v[150:151], s[70:71], s[70:71] op_sel:[0,0,1] op_sel_hi:[1,0,1]\nv_pk_fma_f32 v[152:153], v[152:153], s[70:71], s[70:71] op_sel:[0,0,1] op_sel_hi:[1,0,1]\nv_pk_fma_f32 v[154:155], v[154:155], s[70:71], s[70:71] op_sel:[0,0,1] op_sel_hi:[1,0,1]\nv_pk_fma_f32 v[156:157], v[156:157], s[70:71], s[70:71] op_sel:[0,0,1] op_sel_hi:[1,0,1]\nv_pk_fma_f32 v[158:159], v[158:159], s[70:71], s[70:71] op_sel:[0,0,1] op_sel_hi:[1,0,1]\n"
"v_cvt_pk_bf16_f32 v144, v144, v145\n"
"v_mfma_f32_16x16x32_bf16 v[104:107], a[240:243], a[184:187], v[104:107]\n"
"s_mov_b32 m0, s51\n"
"v_cvt_pk_bf16_f32 v171, v174, v175\n"
"v_cvt_pk_bf16_f32 v121, v122, v123\n"
"v_mfma_f32_16x16x32_bf16 v[104:107], a[244:247], a[188:191], v[104:107]\n"
"buffer_load_dwordx4 v248, s[24:27], s49 offen lds\n"
"v_mfma_f32_16x16x32_bf16 v[108:111], a[248:251], a[184:187], v[108:111]\n"
"v_xor_b32_e64 v182, s42, 0\n"
"v_lshl_add_u32 v181, v182, 10, v181\n"
"v_mfma_f32_16x16x32_bf16 v[108:111], a[252:255], a[188:191], v[108:111]\n"
"ds_read_b64_tr_b16 a[218:219], v245 offset:1536\n"
"v_cvt_pk_bf16_f32 v211, v214, v215\n"
"v_mfma_f32_16x16x32_bf16 v[92:95], a[248:251], a[176:179], v[92:95]\n"
"v_cvt_pk_bf16_f32 v160, v160, v161\n"
"v_cvt_pk_bf16_f32 v145, v146, v147\n"
"v_mfma_f32_16x16x32_bf16 v[92:95], a[252:255], a[180:183], v[92:95]\n"
"ds_write2st64_b64 v180, v[176:177], v[178:179] offset0:0 offset1:1\n"
"v_cvt_pk_bf16_f32 v122, v124, v125\n"
"v_cvt_pk_bf16_f32 v123, v126, v127\n"
"v_cvt_pk_bf16_f32 v146, v148, v149\n"
"v_cvt_pk_bf16_f32 v152, v152, v153\n"
"v_cvt_pk_bf16_f32 v153, v154, v155\n"
"v_cvt_pk_bf16_f32 v155, v158, v159\n"
"v_mfma_f32_16x16x32_bf16 v[88:91], a[240:243], a[176:179], v[88:91]\n"
"v_cvt_pk_bf16_f32 v210, v212, v213\n"
"v_cvt_pk_bf16_f32 v113, v114, v115\n"
"v_mfma_f32_16x16x32_bf16 v[88:91], a[244:247], a[180:183], v[88:91]\n"
"buffer_load_dwordx4 v249, s[24:27], s49 offen offset:1024 lds\n"
"v_mfma_f32_16x16x32_bf16 v[84:87], a[232:235], a[176:179], v[84:87]\n"
"v_cvt_pk_bf16_f32 v161, v162, v163\n"
"v_cvt_pk_bf16_f32 v154, v156, v157\n"
"v_mfma_f32_16x16x32_bf16 v[84:87], a[236:239], a[180:183], v[84:87]\n"
"s_nop 0\n"
"v_permlane16_swap_b32 v152, v154\n"
"v_mfma_f32_16x16x32_bf16 v[80:83], a[224:227], a[176:179], v[80:83]\n"
"ds_read_b64_tr_b16 a[196:197], v244 offset:8192\n"
"ds_read_b64_tr_b16 a[214:215], v244 offset:9728\n"
"v_mfma_f32_16x16x32_bf16 v[80:83], a[228:231], a[180:183], v[80:83]\n"
"ds_write2st64_b64 v180, v[208:209], v[210:211] offset0:120 offset1:121\n"
"v_cvt_pk_bf16_f32 v114, v116, v117\n"
"v_permlane16_swap_b32 v137, v139\n"
"v_permlane16_swap_b32 v144, v146\n"
"v_mfma_f32_16x16x32_bf16 v[64:67], a[224:227], a[168:171], v[64:67]\n"
"v_permlane16_swap_b32 v121, v123\n"
"v_mfma_f32_16x16x32_bf16 v[64:67], a[228:231], a[172:175], v[64:67]\n"
"buffer_load_dwordx4 v254, s[24:27], s49 offen offset:2048 lds\n"
"v_mfma_f32_16x16x32_bf16 v[68:71], a[232:235], a[168:171], v[68:71]\n"
"v_permlane16_swap_b32 v120, v122\n"
"v_mfma_f32_16x16x32_bf16 v[68:71], a[236:239], a[172:175], v[68:71]\n"
"v_cvt_pk_bf16_f32 v115, v118, v119\n"
"v_accvgpr_write_b32 a24, v136\n"
"v_mfma_f32_16x16x32_bf16 v[72:75], a[240:243], a[168:171], v[72:75]\n"
"ds_read_b64_tr_b16 a[206:207], v245 offset:8704\n"
"v_add_u32 v181, 94208, v181\n"
"v_mfma_f32_16x16x32_bf16 v[72:75], a[244:247], a[172:175], v[72:75]\n"
"ds_read_b64_tr_b16 a[216:217], v245 offset:1024\n"
"v_permlane16_swap_b32 v112, v114\n"
"v_permlane16_swap_b32 v153, v155\n"
"v_pk_fma_f32 v[96:97], v[96:97], s[70:71], s[70:71] op_sel:[0,0,1] op_sel_hi:[1,0,1]\nv_pk_fma_f32 v[98:99], v[98:99], s[70:71], s[70:71] op_sel:[0,0,1] op_sel_hi:[1,0,1]\nv_pk_fma_f32 v[100:101], v[100:101], s[70:71], s[70:71] op_sel:[0,0,1] op_sel_hi:[1,0,1]\nv_pk_fma_f32 v[102:103], v[102:103], s[70:71], s[70:71] op_sel:[0,0,1] op_sel_hi:[1,0,1]\nv_pk_fma_f32 v[104:105], v[104:105], s[70:71], s[70:71] op_sel:[0,0,1] op_sel_hi:[1,0,1]\nv_pk_fma_f32 v[106:107], v[106:107], s[70:71], s[70:71] op_sel:[0,0,1] op_sel_hi:[1,0,1]\nv_pk_fma_f32 v[108:109], v[108:109], s[70:71], s[70:71] op_sel:[0,0,1] op_sel_hi:[1,0,1]\nv_pk_fma_f32 v[110:111], v[110:111], s[70:71], s[70:71] op_sel:[0,0,1] op_sel_hi:[1,0,1]\nv_pk_fma_f32 v[80:81], v[80:81], s[70:71], s[70:71] op_sel:[0,0,1] op_sel_hi:[1,0,1]\nv_pk_fma_f32 v[82:83], v[82:83], s[70:71], s[70:71] op_sel:[0,0,1] op_sel_hi:[1,0,1]\nv_pk_fma_f32 v[84:85], v[84:85], s[70:71], s[70:71] op_sel:[0,0,1] op_sel_hi:[1,0,1]\nv_pk_fma_f32 v[86:87], v[86:87], s[70:71], s[70:71] op_sel:[0,0,1] op_sel_hi:[1,0,1]\nv_pk_fma_f32 v[88:89], v[88:89], s[70:71], s[70:71] op_sel:[0,0,1] op_sel_hi:[1,0,1]\nv_pk_fma_f32 v[90:91], v[90:91], s[70:71], s[70:71] op_sel:[0,0,1] op_sel_hi:[1,0,1]\nv_pk_fma_f32 v[92:93], v[92:93], s[70:71], s[70:71] op_sel:[0,0,1] op_sel_hi:[1,0,1]\nv_pk_fma_f32 v[94:95], v[94:95], s[70:71], s[70:71] op_sel:[0,0,1] op_sel_hi:[1,0,1]\n"
"v_mfma_f32_16x16x32_bf16 v[76:79], a[248:251], a[168:171], v[76:79]\n"
"ds_read_b64_tr_b16 a[208:209], v244 offset:1024\n"
"v_cvt_pk_bf16_f32 v80, v80, v81\n"
"v_mfma_f32_16x16x32_bf16 v[76:79], a[252:255], a[172:175], v[76:79]\n"
"buffer_load_dwordx4 v255, s[24:27], s49 offen offset:3072 lds\n"
"v_mfma_f32_16x16x32_bf16 v[60:63], a[248:251], a[160:163], v[60:63]\n"
"ds_read_b64_tr_b16 a[192:193], v244 offset:0\n"
"ds_read_b64_tr_b16 a[222:223], v245 offset:9728\n"
"v_mfma_f32_16x16x32_bf16 v[60:63], a[252:255], a[164:167], v[60:63]\n"
"v_permlane16_swap_b32 v168, v170\n"
"v_mfma_f32_16x16x32_bf16 v[56:59], a[240:243], a[160:163], v[56:59]\n"
"v_permlane16_swap_b32 v113, v115\n"
"v_mfma_f32_16x16x32_bf16 v[56:59], a[244:247], a[164:167], v[56:59]\n"
"s_add_u32 s40, s49, 40960\n"
"ds_read_b64_tr_b16 a[210:211], v244 offset:1536\n"
"v_add_u32 v182, s75, v181\n"
"v_mfma_f32_16x16x32_bf16 v[52:55], a[232:235], a[160:163], v[52:55]\n"
"s_add_u32 m0, s51, 4096\n"
"v_cvt_pk_bf16_f32 v130, v132, v133\n"
"v_accvgpr_write_b32 a25, v137\n"
"v_mfma_f32_16x16x32_bf16 v[52:55], a[236:239], a[164:167], v[52:55]\n"
"buffer_load_dwordx4 v248, s[24:27], s40 offen lds\n"
"v_mfma_f32_16x16x32_bf16 v[48:51], a[224:227], a[160:163], v[48:51]\n"
"s_waitcnt vmcnt(63) lgkmcnt(3)\n"
"v_permlane16_swap_b32 v129, v131\n"
"v_mfma_f32_16x16x32_bf16 v[48:51], a[228:231], a[164:167], v[48:51]\n"
"s_barrier\n"
"v_mfma_f32_16x16x32_bf16 v[0:3], a[224:227], a[136:139], v[0:3]\n"
"ds_read_b128 a[36:39], v181 offset:61440\n"
"v_accvgpr_write_b32 a43, v155\n"
"v_mfma_f32_16x16x32_bf16 v[0:3], a[228:231], a[140:143], v[0:3]\n"
"ds_read_b128 a[28:31], v181 offset:49152\n"
"v_accvgpr_write_b32 a10, v122\n"
"v_mfma_f32_16x16x32_bf16 v[4:7], a[232:235], a[136:139], v[4:7]\n"
"v_cvt_pk_bf16_f32 v96, v96, v97\n"
"v_cvt_pk_bf16_f32 v104, v104, v105\n"
"v_mfma_f32_16x16x32_bf16 v[4:7], a[236:239], a[140:143], v[4:7]\n"
"buffer_load_dwordx4 v249, s[24:27], s40 offen offset:1024 lds\n"
"v_mfma_f32_16x16x32_bf16 v[8:11], a[240:243], a[136:139], v[8:11]\n"
"v_cvt_pk_bf16_f32 v97, v98, v99\n"
"v_cvt_pk_bf16_f32 v99, v102, v103\n"
"v_mfma_f32_16x16x32_bf16 v[8:11], a[244:247], a[140:143], v[8:11]\n"
"ds_read_b64_tr_b16 a[220:221], v245 offset:9216\n"
"ds_read_b128 a[12:15], v181 offset:32768\n"
"v_mfma_f32_16x16x32_bf16 v[12:15], a[248:251], a[136:139], v[12:15]\n"
"ds_read_b128 a[164:167], v253 offset:0\n"
"v_accvgpr_write_b32 a32, v144\n"
"v_mfma_f32_16x16x32_bf16 v[12:15], a[252:255], a[140:143], v[12:15]\n"
"ds_read_b128 a[140:143], v247 offset:0\n"
"v_accvgpr_write_b32 a3, v115\n"
"v_mfma_f32_16x16x32_bf16 v[28:31], a[248:251], a[144:147], v[28:31]\n"
"ds_read_b64_tr_b16 a[194:195], v244 offset:512\n"
"v_cvt_pk_bf16_f32 v162, v164, v165\n"
"v_mfma_f32_16x16x32_bf16 v[28:31], a[252:255], a[148:151], v[28:31]\n"
"buffer_load_dwordx4 v254, s[24:27], s40 offen offset:2048 lds\n"
"v_mfma_f32_16x16x32_bf16 v[24:27], a[240:243], a[144:147], v[24:27]\n"
"s_add_u32 s49, s49, 128\n"
"v_cvt_pk_bf16_f32 v147, v150, v151\n"
"v_accvgpr_write_b32 a41, v153\n"
"v_mfma_f32_16x16x32_bf16 v[24:27], a[244:247], a[148:151], v[24:27]\n"
"s_sub_u32 s41, s49, 1280\n"
"v_cvt_pk_bf16_f32 v98, v100, v101\n"
"v_cvt_pk_bf16_f32 v88, v88, v89\n"
"v_mfma_f32_16x16x32_bf16 v[20:23], a[232:235], a[144:147], v[20:23]\n"
"s_min_u32 s49, s49, s41\n"
"ds_read_b128 a[136:139], v246 offset:0\n"
"v_accvgpr_write_b32 a19, v131\n"
"v_mfma_f32_16x16x32_bf16 v[20:23], a[236:239], a[148:151], v[20:23]\n"
"s_xor_b32 s51, s51, s74\n"
"ds_read_b64_tr_b16 a[198:199], v244 offset:8704\n"
"ds_read_b128 a[44:47], v182 offset:0\n"
"v_mfma_f32_16x16x32_bf16 v[16:19], a[224:227], a[144:147], v[16:19]\n"
"ds_read_b64_tr_b16 a[212:213], v244 offset:9216\n"
"v_cvt_pk_bf16_f32 v163, v166, v167\n"
"v_mfma_f32_16x16x32_bf16 v[16:19], a[228:231], a[148:151], v[16:19]\n"
"ds_read_b128 a[148:151], v247 offset:2048\n"
"v_accvgpr_write_b32 a11, v123\n"
"v_mfma_f32_16x16x32_bf16 v[32:35], a[224:227], a[152:155], v[32:35]\n"
"ds_read_b128 a[4:7], v181 offset:0\n"
"v_and_b32 v102, 0xf0, v242\n"
"v_mfma_f32_16x16x32_bf16 v[32:35], a[228:231], a[156:159], v[32:35]\n"
"ds_read_b64_tr_b16 a[202:203], v245 offset:512\n"
"ds_read_b64_tr_b16 a[204:205], v245 offset:8192\n"
"v_mfma_f32_16x16x32_bf16 v[36:39], a[232:235], a[152:155], v[36:39]\n"
"ds_read_b64_tr_b16 a[200:201], v245 offset:0\n"
"v_cvt_pk_bf16_f32 v89, v90, v91\n"
"v_mfma_f32_16x16x32_bf16 v[36:39], a[236:239], a[156:159], v[36:39]\n"
"ds_read_b128 a[20:23], v181 offset:45056\n"
"v_accvgpr_write_b32 a27, v139\n"
"v_mfma_f32_16x16x32_bf16 v[40:43], a[240:243], a[152:155], v[40:43]\n"
"v_accvgpr_write_b32 a0, v112\n"
"v_accvgpr_write_b32 a1, v113\n"
"v_mfma_f32_16x16x32_bf16 v[40:43], a[244:247], a[156:159], v[40:43]\n"
"ds_read_b128 a[180:183], v251 offset:2048\n"
"v_accvgpr_write_b32 a2, v114\n"
"v_mfma_f32_16x16x32_bf16 v[44:47], a[248:251], a[152:155], v[44:47]\n"
"s_add_u32 s47, s47, 1\n"
"ds_read_b128 a[144:147], v246 offset:2048\n"
"v_accvgpr_write_b32 a17, v129\n"
"v_mfma_f32_16x16x32_bf16 v[44:47], a[252:255], a[156:159], v[44:47]\n"
"s_waitcnt lgkmcnt(7)\n"
"ds_read_b128 a[160:163], v252 offset:0\n"
"v_permlane16_swap_b32 v161, v163\n"
"v_permlane16_swap_b32 v169, v171\n"
"v_accvgpr_write_b32 a9, v121\n"
"v_permlane16_swap_b32 v128, v130\n"
"v_permlane16_swap_b32 v145, v147\n"
"v_accvgpr_write_b32 a42, v154\n"
"v_bfe_u32 v100, v242, 9, 1\n"
"v_cvt_pk_bf16_f32 v91, v94, v95\n"
"v_pk_fma_f32 v[0:1], v[0:1], s[70:71], s[70:71] op_sel:[0,0,1] op_sel_hi:[1,0,1]\nv_pk_fma_f32 v[2:3], v[2:3], s[70:71], s[70:71] op_sel:[0,0,1] op_sel_hi:[1,0,1]\nv_pk_fma_f32 v[4:5], v[4:5], s[70:71], s[70:71] op_sel:[0,0,1] op_sel_hi:[1,0,1]\nv_pk_fma_f32 v[6:7], v[6:7], s[70:71], s[70:71] op_sel:[0,0,1] op_sel_hi:[1,0,1]\nv_pk_fma_f32 v[8:9], v[8:9], s[70:71], s[70:71] op_sel:[0,0,1] op_sel_hi:[1,0,1]\nv_pk_fma_f32 v[10:11], v[10:11], s[70:71], s[70:71] op_sel:[0,0,1] op_sel_hi:[1,0,1]\nv_pk_fma_f32 v[12:13], v[12:13], s[70:71], s[70:71] op_sel:[0,0,1] op_sel_hi:[1,0,1]\nv_pk_fma_f32 v[14:15], v[14:15], s[70:71], s[70:71] op_sel:[0,0,1] op_sel_hi:[1,0,1]\nv_pk_fma_f32 v[16:17], v[16:17], s[70:71], s[70:71] op_sel:[0,0,1] op_sel_hi:[1,0,1]\nv_pk_fma_f32 v[18:19], v[18:19], s[70:71], s[70:71] op_sel:[0,0,1] op_sel_hi:[1,0,1]\nv_pk_fma_f32 v[20:21], v[20:21], s[70:71], s[70:71] op_sel:[0,0,1] op_sel_hi:[1,0,1]\nv_pk_fma_f32 v[22:23], v[22:23], s[70:71], s[70:71] op_sel:[0,0,1] op_sel_hi:[1,0,1]\nv_pk_fma_f32 v[24:25], v[24:25], s[70:71], s[70:71] op_sel:[0,0,1] op_sel_hi:[1,0,1]\nv_pk_fma_f32 v[26:27], v[26:27], s[70:71], s[70:71] op_sel:[0,0,1] op_sel_hi:[1,0,1]\nv_pk_fma_f32 v[28:29], v[28:29], s[70:71], s[70:71] op_sel:[0,0,1] op_sel_hi:[1,0,1]\nv_pk_fma_f32 v[30:31], v[30:31], s[70:71], s[70:71] op_sel:[0,0,1] op_sel_hi:[1,0,1]\n"
"v_cvt_pk_bf16_f32 v16, v16, v17\n"
"v_cvt_pk_bf16_f32 v24, v24, v25\n"
"v_cvt_pk_bf16_f32 v25, v26, v27\n"
"v_mfma_f32_16x16x32_bf16 v[112:115], a[192:195], a[136:139], v[228:231]\n"
"v_cvt_pk_bf16_f32 v0, v0, v1\n"
"v_cvt_pk_bf16_f32 v8, v8, v9\n"
"v_mfma_f32_16x16x32_bf16 v[112:115], a[196:199], a[140:143], v[112:115]\n"
"s_waitcnt lgkmcnt(4)\n"
"ds_write_b128 v243, v[168:171] offset:2048\n"
"v_permlane16_swap_b32 v160, v162\n"
"v_accvgpr_write_b32 a8, v120\n"
"v_cvt_pk_bf16_f32 v81, v82, v83\n"
"v_pk_fma_f32 v[64:65], v[64:65], s[70:71], s[70:71] op_sel:[0,0,1] op_sel_hi:[1,0,1]\nv_pk_fma_f32 v[66:67], v[66:67], s[70:71], s[70:71] op_sel:[0,0,1] op_sel_hi:[1,0,1]\nv_pk_fma_f32 v[68:69], v[68:69], s[70:71], s[70:71] op_sel:[0,0,1] op_sel_hi:[1,0,1]\nv_pk_fma_f32 v[70:71], v[70:71], s[70:71], s[70:71] op_sel:[0,0,1] op_sel_hi:[1,0,1]\nv_pk_fma_f32 v[72:73], v[72:73], s[70:71], s[70:71] op_sel:[0,0,1] op_sel_hi:[1,0,1]\nv_pk_fma_f32 v[74:75], v[74:75], s[70:71], s[70:71] op_sel:[0,0,1] op_sel_hi:[1,0,1]\nv_pk_fma_f32 v[76:77], v[76:77], s[70:71], s[70:71] op_sel:[0,0,1] op_sel_hi:[1,0,1]\nv_pk_fma_f32 v[78:79], v[78:79], s[70:71], s[70:71] op_sel:[0,0,1] op_sel_hi:[1,0,1]\nv_pk_fma_f32 v[48:49], v[48:49], s[70:71], s[70:71] op_sel:[0,0,1] op_sel_hi:[1,0,1]\nv_pk_fma_f32 v[50:51], v[50:51], s[70:71], s[70:71] op_sel:[0,0,1] op_sel_hi:[1,0,1]\nv_pk_fma_f32 v[52:53], v[52:53], s[70:71], s[70:71] op_sel:[0,0,1] op_sel_hi:[1,0,1]\nv_pk_fma_f32 v[54:55], v[54:55], s[70:71], s[70:71] op_sel:[0,0,1] op_sel_hi:[1,0,1]\nv_pk_fma_f32 v[56:57], v[56:57], s[70:71], s[70:71] op_sel:[0,0,1] op_sel_hi:[1,0,1]\nv_pk_fma_f32 v[58:59], v[58:59], s[70:71], s[70:71] op_sel:[0,0,1] op_sel_hi:[1,0,1]\nv_pk_fma_f32 v[60:61], v[60:61], s[70:71], s[70:71] op_sel:[0,0,1] op_sel_hi:[1,0,1]\nv_pk_fma_f32 v[62:63], v[62:63], s[70:71], s[70:71] op_sel:[0,0,1] op_sel_hi:[1,0,1]\n"
"v_cvt_pk_bf16_f32 v64, v64, v65\n"
"v_mfma_f32_16x16x32_bf16 v[116:119], a[200:203], a[136:139], v[228:231]\n"
"s_waitcnt vmcnt(11) lgkmcnt(4)\n"
"v_accvgpr_write_b32 a18, v130\n"
"v_accvgpr_write_b32 a26, v138\n"
"v_mfma_f32_16x16x32_bf16 v[116:119], a[204:207], a[140:143], v[116:119]\n"
"s_barrier\n"
"ds_write_b128 v243, v[160:163] offset:0\n"
"v_accvgpr_write_b32 a16, v128\n"
"v_accvgpr_write_b32 a33, v145\n"
"v_cvt_pk_bf16_f32 v1, v2, v3\n"
"v_pk_fma_f32 v[32:33], v[32:33], s[70:71], s[70:71] op_sel:[0,0,1] op_sel_hi:[1,0,1]\nv_pk_fma_f32 v[34:35], v[34:35], s[70:71], s[70:71] op_sel:[0,0,1] op_sel_hi:[1,0,1]\nv_pk_fma_f32 v[36:37], v[36:37], s[70:71], s[70:71] op_sel:[0,0,1] op_sel_hi:[1,0,1]\nv_pk_fma_f32 v[38:39], v[38:39], s[70:71], s[70:71] op_sel:[0,0,1] op_sel_hi:[1,0,1]\nv_pk_fma_f32 v[40:41], v[40:41], s[70:71], s[70:71] op_sel:[0,0,1] op_sel_hi:[1,0,1]\nv_pk_fma_f32 v[42:43], v[42:43], s[70:71], s[70:71] op_sel:[0,0,1] op_sel_hi:[1,0,1]\nv_pk_fma_f32 v[44:45], v[44:45], s[70:71], s[70:71] op_sel:[0,0,1] op_sel_hi:[1,0,1]\nv_pk_fma_f32 v[46:47], v[46:47], s[70:71], s[70:71] op_sel:[0,0,1] op_sel_hi:[1,0,1]\n"
"v_cvt_pk_bf16_f32 v32, v32, v33\n"
"v_mfma_f32_16x16x32_bf16 v[120:123], a[208:211], a[136:139], v[228:231]\n"
"s_add_u32 s40, s48, 256\n"
"s_xor_b32 m0, s50, 0xc000\n"
"ds_read_b128 a[152:155], v246 offset:4096\n"
"v_mfma_f32_16x16x32_bf16 v[120:123], a[212:215], a[140:143], v[120:123]\n"
"buffer_load_dwordx4 v224, s[28:31], s40 offen lds\n"
"v_mfma_f32_16x16x32_bf16 v[124:127], a[216:219], a[136:139], v[228:231]\n"
"v_accvgpr_write_b32 a40, v152\n"
"v_cvt_pk_bf16_f32 v40, v40, v41\n"
"v_mfma_f32_16x16x32_bf16 v[124:127], a[220:223], a[140:143], v[124:127]\n"
"s_waitcnt lgkmcnt(3)\n"
"ds_read_b128 a[168:171], v250 offset:0\n"
"v_cvt_pk_bf16_f32 v105, v106, v107\n"
"v_mfma_f32_16x16x32_bf16 v[140:143], a[216:219], a[144:147], v[228:231]\n"
"ds_read_b128 a[156:159], v247 offset:4096\n"
"v_accvgpr_write_b32 a34, v146\n"
"v_mfma_f32_16x16x32_bf16 v[140:143], a[220:223], a[148:151], v[140:143]\n"
"ds_read_b128 a[172:175], v251 offset:0\n"
"v_cvt_pk_bf16_f32 v2, v4, v5\n"
"v_mfma_f32_16x16x32_bf16 v[136:139], a[208:211], a[144:147], v[228:231]\n"
"ds_read_b64_tr_b16 a[224:225], v244 offset:16384\n"
"v_lshl_add_u32 v100, v100, 8, v102\n"
"v_mfma_f32_16x16x32_bf16 v[136:139], a[212:215], a[148:151], v[136:139]\n"
"buffer_load_dwordx4 v225, s[28:31], s40 offen offset:1024 lds\n"
"v_mfma_f32_16x16x32_bf16 v[132:135], a[200:203], a[144:147], v[228:231]\n"
"v_bfe_u32 v102, v242, 8, 1\n"
"v_cvt_pk_bf16_f32 v41, v42, v43\n"
"v_mfma_f32_16x16x32_bf16 v[132:135], a[204:207], a[148:151], v[132:135]\n"
"v_accvgpr_write_b32 a35, v147\n"
"v_cvt_pk_bf16_f32 v65, v66, v67\n"
"v_mfma_f32_16x16x32_bf16 v[128:131], a[192:195], a[144:147], v[228:231]\n"
"ds_read_b64_tr_b16 a[234:235], v245 offset:16896\n"
"v_cvt_pk_bf16_f32 v72, v72, v73\n"
"v_mfma_f32_16x16x32_bf16 v[128:131], a[196:199], a[148:151], v[128:131]\n"
"s_waitcnt lgkmcnt(4)\n"
"ds_read_b64_tr_b16 a[236:237], v245 offset:24576\n"
"v_bfe_u32 v101, v242, 9, 1\n"
"v_mfma_f32_16x16x32_bf16 v[144:147], a[192:195], a[152:155], v[228:231]\n"
"s_waitcnt lgkmcnt(4)\n"
"ds_read_b64_tr_b16 a[226:227], v244 offset:16896\n"
"v_cvt_pk_bf16_f32 v9, v10, v11\n"
"v_mfma_f32_16x16x32_bf16 v[144:147], a[196:199], a[156:159], v[144:147]\n"
"buffer_load_dwordx4 v226, s[28:31], s40 offen offset:2048 lds\n"
"v_mfma_f32_16x16x32_bf16 v[148:151], a[200:203], a[152:155], v[228:231]\n"
"v_cvt_pk_bf16_f32 v3, v6, v7\n"
"v_cvt_pk_bf16_f32 v17, v18, v19\n"
"v_mfma_f32_16x16x32_bf16 v[148:151], a[204:207], a[156:159], v[148:151]\n"
"v_cvt_pk_bf16_f32 v66, v68, v69\n"
"v_cvt_pk_bf16_f32 v73, v74, v75\n"
"v_mfma_f32_16x16x32_bf16 v[152:155], a[208:211], a[152:155], v[228:231]\n"
"ds_read_b64_tr_b16 a[252:253], v245 offset:25600\n"
"v_lshl_add_u32 v100, v102, 3, v100\n"
"v_mfma_f32_16x16x32_bf16 v[152:155], a[212:215], a[156:159], v[152:155]\n"
"ds_read_b64_tr_b16 a[244:245], v244 offset:25600\n"
"ds_read_b64_tr_b16 a[250:251], v245 offset:17920\n"
"v_mfma_f32_16x16x32_bf16 v[156:159], a[216:219], a[152:155], v[228:231]\n"
"v_cvt_pk_bf16_f32 v48, v48, v49\n"
"v_cvt_pk_bf16_f32 v43, v46, v47\n"
"v_mfma_f32_16x16x32_bf16 v[156:159], a[220:223], a[156:159], v[156:159]\n"
"buffer_load_dwordx4 v227, s[28:31], s40 offen offset:3072 lds\n"
"v_mfma_f32_16x16x32_bf16 v[172:175], a[216:219], a[160:163], v[228:231]\n"
"s_cmp_eq_u32 s47, 8\n"
"v_cvt_pk_bf16_f32 v75, v78, v79\n"
"v_cvt_pk_bf16_f32 v27, v30, v31\n"
"v_mfma_f32_16x16x32_bf16 v[172:175], a[220:223], a[164:167], v[172:175]\n"
"s_cselect_b32 s41, s52, s58\n"
"v_cvt_pk_bf16_f32 v56, v56, v57\n"
"v_permlane16_swap_b32 v1, v3\n"
"v_cvt_pk_bf16_f32 v11, v14, v15\n"
"v_cvt_pk_bf16_f32 v26, v28, v29\n"
"v_cvt_pk_bf16_f32 v33, v34, v35\n"
"v_mfma_f32_16x16x32_bf16 v[168:171], a[208:211], a[160:163], v[228:231]\n"
"s_add_u32 s48, s48, s41\n"
"ds_read_b128 a[176:179], v250 offset:2048\n"
"v_cvt_pk_bf16_f32 v83, v86, v87\n"
"v_mfma_f32_16x16x32_bf16 v[168:171], a[212:215], a[164:167], v[168:171]\n"
"s_sub_u32 s41, s48, 5242880\n"
"ds_read_b64_tr_b16 a[230:231], v244 offset:25088\n"
"ds_read_b64_tr_b16 a[248:249], v245 offset:17408\n"
"v_mfma_f32_16x16x32_bf16 v[164:167], a[200:203], a[160:163], v[228:231]\n"
"s_min_u32 s48, s48, s41\n"
"ds_read_b64_tr_b16 a[232:233], v245 offset:16384\n"
"v_xor_b32_e64 v102, s42, 1\n"
"v_mfma_f32_16x16x32_bf16 v[164:167], a[204:207], a[164:167], v[164:167]\n"
"ds_read_b128 a[184:187], v250 offset:4096\n"
"v_lshl_add_u32 v100, v102, 10, v100\n"
"v_mfma_f32_16x16x32_bf16 v[160:163], a[192:195], a[160:163], v[228:231]\n"
"s_mov_b32 m0, s50\n"
"v_permlane16_swap_b32 v9, v11\n"
"v_mfma_f32_16x16x32_bf16 v[160:163], a[196:199], a[164:167], v[160:163]\n"
"buffer_load_dwordx4 v224, s[28:31], s48 offen lds\n"
"v_mfma_f32_16x16x32_bf16 v[176:179], a[192:195], a[168:171], v[228:231]\n"
"s_waitcnt lgkmcnt(5)\n"
"v_cvt_pk_bf16_f32 v49, v50, v51\n"
"v_cvt_pk_bf16_f32 v34, v36, v37\n"
"v_mfma_f32_16x16x32_bf16 v[176:179], a[196:199], a[172:175], v[176:179]\n"
"v_cvt_pk_bf16_f32 v10, v12, v13\n"
"v_and_b32 v102, 0xf0, v242\n"
"v_mfma_f32_16x16x32_bf16 v[180:183], a[200:203], a[168:171], v[228:231]\n"
"ds_read_b64_tr_b16 a[240:241], v244 offset:17408\n"
"ds_read_b64_tr_b16 a[254:255], v245 offset:26112\n"
"v_mfma_f32_16x16x32_bf16 v[180:183], a[204:207], a[172:175], v[180:183]\n"
"buffer_load_dwordx4 v225, s[28:31], s48 offen offset:1024 lds\n"
"v_mfma_f32_16x16x32_bf16 v[184:187], a[208:211], a[168:171], v[228:231]\n"
"v_cvt_pk_bf16_f32 v90, v92, v93\n"
"v_cvt_pk_bf16_f32 v42, v44, v45\n"
"v_mfma_f32_16x16x32_bf16 v[184:187], a[212:215], a[172:175], v[184:187]\n"
"v_permlane16_swap_b32 v0, v2\n"
"v_mfma_f32_16x16x32_bf16 v[188:191], a[216:219], a[168:171], v[228:231]\n"
"ds_read_b128 a[188:191], v251 offset:4096\n"
"v_accvgpr_write_b32 a51, v3\n"
"v_mfma_f32_16x16x32_bf16 v[188:191], a[220:223], a[172:175], v[188:191]\n"
"s_waitcnt lgkmcnt(3)\n"
"ds_read_b64_tr_b16 a[238:239], v245 offset:25088\n"
"v_cvt_pk_bf16_f32 v67, v70, v71\n"
"v_mfma_f32_16x16x32_bf16 v[204:207], a[216:219], a[176:179], v[228:231]\n"
"ds_read_b64_tr_b16 a[246:247], v244 offset:26112\n"
"v_add_u32 v100, 94208, v100\n"
"v_mfma_f32_16x16x32_bf16 v[204:207], a[220:223], a[180:183], v[204:207]\n"
"buffer_load_dwordx4 v226, s[28:31], s48 offen offset:2048 lds\n"
"v_mfma_f32_16x16x32_bf16 v[200:203], a[208:211], a[176:179], v[228:231]\n"
"v_accvgpr_write_b32 a50, v2\n"
"v_lshl_add_u32 v101, v101, 8, v102\n"
"v_mfma_f32_16x16x32_bf16 v[200:203], a[212:215], a[180:183], v[200:203]\n"
"v_permlane16_swap_b32 v24, v26\n"
"v_mfma_f32_16x16x32_bf16 v[196:199], a[200:203], a[176:179], v[228:231]\n"
"ds_read_b64_tr_b16 a[228:229], v244 offset:24576\n"
"v_cvt_pk_bf16_f32 v106, v108, v109\n"
"v_mfma_f32_16x16x32_bf16 v[196:199], a[204:207], a[180:183], v[196:199]\n"
"ds_write2st64_b64 v100, v[96:97], v[98:99] offset0:120 offset1:121\n"
"v_accvgpr_write_b32 a49, v1\n"
"v_cvt_pk_bf16_f32 v19, v22, v23\n"
"v_permlane16_swap_b32 v25, v27\n"
"v_cvt_pk_bf16_f32 v35, v38, v39\n"
"v_mfma_f32_16x16x32_bf16 v[192:195], a[192:195], a[176:179], v[228:231]\n"
"v_cvt_pk_bf16_f32 v74, v76, v77\n"
"v_cvt_pk_bf16_f32 v18, v20, v21\n"
"v_mfma_f32_16x16x32_bf16 v[192:195], a[196:199], a[180:183], v[192:195]\n"
"buffer_load_dwordx4 v227, s[28:31], s48 offen offset:3072 lds\n"
"v_mfma_f32_16x16x32_bf16 v[208:211], a[192:195], a[184:187], v[228:231]\n"
"s_waitcnt lgkmcnt(2)\n"
"s_xor_b32 s50, s50, s74\n"
"v_permlane16_swap_b32 v33, v35\n"
"v_mfma_f32_16x16x32_bf16 v[208:211], a[196:199], a[188:191], v[208:211]\n"
"v_permlane16_swap_b32 v40, v42\n"
"v_mfma_f32_16x16x32_bf16 v[212:215], a[200:203], a[184:187], v[228:231]\n"
"v_permlane16_swap_b32 v17, v19\n"
"v_mfma_f32_16x16x32_bf16 v[212:215], a[204:207], a[188:191], v[212:215]\n"
"ds_read_b64_tr_b16 a[242:243], v244 offset:17920\n"
"v_cvt_pk_bf16_f32 v107, v110, v111\n"
"v_mfma_f32_16x16x32_bf16 v[216:219], a[208:211], a[184:187], v[228:231]\n"
"v_permlane16_swap_b32 v16, v18\n"
"v_mfma_f32_16x16x32_bf16 v[216:219], a[212:215], a[188:191], v[216:219]\n"
"ds_write2st64_b64 v100, v[104:105], v[106:107] offset0:128 offset1:129\n"
"v_cvt_pk_bf16_f32 v82, v84, v85\n"
"v_accvgpr_write_b32 a75, v27\n"
"v_permlane16_swap_b32 v41, v43\n"
"v_mfma_f32_16x16x32_bf16 v[220:223], a[216:219], a[184:187], v[228:231]\n"
"v_accvgpr_write_b32 a48, v0\n"
"v_accvgpr_write_b32 a64, v16\n"
"v_mfma_f32_16x16x32_bf16 v[220:223], a[220:223], a[188:191], v[220:223]\n"
"ds_write2st64_b64 v100, v[80:81], v[82:83] offset0:88 offset1:89\n"
"v_cvt_pk_bf16_f32 v57, v58, v59\n"
"v_cvt_pk_bf16_f32 v58, v60, v61\n"
"v_permlane16_swap_b32 v8, v10\n"
"v_accvgpr_write_b32 a57, v9\n"
"v_mfma_f32_16x16x32_bf16 v[0:3], a[224:227], a[136:139], v[228:231]\n"
"s_waitcnt lgkmcnt(3)\n"
"v_cvt_pk_bf16_f32 v50, v52, v53\n"
"v_xor_b32 v245, s74, v245\n"
"v_mfma_f32_16x16x32_bf16 v[0:3], a[228:231], a[140:143], v[0:3]\n"
"ds_write2st64_b64 v100, v[72:73], v[74:75] offset0:64 offset1:65\n"
"v_cvt_pk_bf16_f32 v59, v62, v63\n"
"v_accvgpr_write_b32 a58, v10\n"
"v_accvgpr_write_b32 a66, v18\n"
"v_accvgpr_write_b32 a81, v33\n"
"v_mfma_f32_16x16x32_bf16 v[4:7], a[232:235], a[136:139], v[228:231]\n"
"s_waitcnt vmcnt(8) lgkmcnt(3)\n"
"v_permlane16_swap_b32 v57, v59\n"
"v_mfma_f32_16x16x32_bf16 v[4:7], a[236:239], a[140:143], v[4:7]\n"
"s_barrier\n"
"ds_write2st64_b64 v100, v[88:89], v[90:91] offset0:96 offset1:97\n"
"v_accvgpr_write_b32 a56, v8\n"
"v_accvgpr_write_b32 a59, v11\n"
"v_accvgpr_write_b32 a72, v24\n"
"v_mfma_f32_16x16x32_bf16 v[8:11], a[240:243], a[136:139], v[228:231]\n"
"s_mov_b32 m0, s51\n"
"ds_read_b64_tr_b16 a[202:203], v245 offset:512\n"
"v_accvgpr_write_b32 a73, v25\n"
"v_mfma_f32_16x16x32_bf16 v[8:11], a[244:247], a[140:143], v[8:11]\n"
"buffer_load_dwordx4 v248, s[24:27], s49 offen lds\n"
"v_mfma_f32_16x16x32_bf16 v[12:15], a[248:251], a[136:139], v[228:231]\n"
"v_accvgpr_write_b32 a65, v17\n"
"v_accvgpr_write_b32 a67, v19\n"
"v_mfma_f32_16x16x32_bf16 v[12:15], a[252:255], a[140:143], v[12:15]\n"
"v_permlane16_swap_b32 v56, v58\n"
"v_mfma_f32_16x16x32_bf16 v[28:31], a[248:251], a[144:147], v[228:231]\n"
"v_accvgpr_write_b32 a74, v26\n"
"v_accvgpr_write_b32 a83, v35\n"
"v_mfma_f32_16x16x32_bf16 v[28:31], a[252:255], a[148:151], v[28:31]\n"
"ds_write2st64_b64 v100, v[64:65], v[66:67] offset0:0 offset1:1\n"
"v_permlane16_swap_b32 v32, v34\n"
"v_accvgpr_write_b32 a88, v40\n"
"v_mfma_f32_16x16x32_bf16 v[24:27], a[240:243], a[144:147], v[228:231]\n"
"v_accvgpr_write_b32 a89, v41\n"
"v_accvgpr_write_b32 a90, v42\n"
"v_mfma_f32_16x16x32_bf16 v[24:27], a[244:247], a[148:151], v[24:27]\n"
"buffer_load_dwordx4 v249, s[24:27], s49 offen offset:1024 lds\n"
"v_mfma_f32_16x16x32_bf16 v[20:23], a[232:235], a[144:147], v[228:231]\n"
"v_accvgpr_write_b32 a82, v34\n"
"v_accvgpr_write_b32 a91, v43\n"
"v_mfma_f32_16x16x32_bf16 v[20:23], a[236:239], a[148:151], v[20:23]\n"
"v_cvt_pk_bf16_f32 v51, v54, v55\n"
"v_accvgpr_write_b32 a80, v32\n"
"v_mfma_f32_16x16x32_bf16 v[16:19], a[224:227], a[144:147], v[228:231]\n"
"ds_read_b64_tr_b16 a[216:217], v245 offset:1024\n"
"v_xor_b32 v247, s74, v247\n"
"v_mfma_f32_16x16x32_bf16 v[16:19], a[228:231], a[148:151], v[16:19]\n"
"ds_write_b128 v243, v[56:59] offset:6144\n"
"v_mfma_f32_16x16x32_bf16 v[32:35], a[224:227], a[152:155], v[228:231]\n"
"v_permlane16_swap_b32 v48, v50\n"
"v_mfma_f32_16x16x32_bf16 v[32:35], a[228:231], a[156:159], v[32:35]\n"
"buffer_load_dwordx4 v254, s[24:27], s49 offen offset:2048 lds\n"
"v_mfma_f32_16x16x32_bf16 v[36:39], a[232:235], a[152:155], v[228:231]\n"
"s_waitcnt vmcnt(63) lgkmcnt(2)\n"
"v_permlane16_swap_b32 v49, v51\n"
"v_mfma_f32_16x16x32_bf16 v[36:39], a[236:239], a[156:159], v[36:39]\n"
"s_barrier\n"
"v_mfma_f32_16x16x32_bf16 v[40:43], a[240:243], a[152:155], v[228:231]\n"
"ds_read_b64_tr_b16 a[204:205], v245 offset:8192\n"
"ds_read_b128 a[140:143], v247 offset:0\n"
"v_mfma_f32_16x16x32_bf16 v[40:43], a[244:247], a[156:159], v[40:43]\n"
"ds_write_b128 v243, v[48:51] offset:4096\n"
"v_mfma_f32_16x16x32_bf16 v[44:47], a[248:251], a[152:155], v[228:231]\n"
"v_bfe_u32 v102, v242, 8, 1\n"
"v_lshl_add_u32 v101, v102, 9, v101\n"
"v_mfma_f32_16x16x32_bf16 v[44:47], a[252:255], a[156:159], v[44:47]\n"
"buffer_load_dwordx4 v255, s[24:27], s49 offen offset:3072 lds\n"
"v_mfma_f32_16x16x32_bf16 v[60:63], a[248:251], a[160:163], v[228:231]\n"
"v_xor_b32 v244, s74, v244\n"
"v_xor_b32_e64 v102, s42, 0\n"
"v_mfma_f32_16x16x32_bf16 v[60:63], a[252:255], a[164:167], v[60:63]\n"
"v_lshl_add_u32 v101, v102, 10, v101\n"
"v_add_u32 v101, 94208, v101\n"
"v_mfma_f32_16x16x32_bf16 v[56:59], a[240:243], a[160:163], v[228:231]\n"
"ds_read_b64_tr_b16 a[198:199], v244 offset:8704\n"
"ds_read_b64_tr_b16 a[220:221], v245 offset:9216\n"
"v_mfma_f32_16x16x32_bf16 v[56:59], a[244:247], a[164:167], v[56:59]\n"
"s_add_u32 s40, s49, 40960\n"
"ds_read_b128 a[52:55], v101 offset:0\n"
"v_xor_b32 v253, s74, v253\n"
"v_mfma_f32_16x16x32_bf16 v[52:55], a[232:235], a[160:163], v[228:231]\n"
"s_add_u32 m0, s51, 4096\n"
"ds_read_b64_tr_b16 a[206:207], v245 offset:8704\n"
"ds_read_b128 a[76:79], v101 offset:49152\n"
"v_mfma_f32_16x16x32_bf16 v[52:55], a[236:239], a[164:167], v[52:55]\n"
"buffer_load_dwordx4 v248, s[24:27], s40 offen lds\n"
"v_mfma_f32_16x16x32_bf16 v[48:51], a[224:227], a[160:163], v[228:231]\n"
"v_xor_b32 v246, s74, v246\n"
"v_add_u32 v102, s75, v101\n"
"v_mfma_f32_16x16x32_bf16 v[48:51], a[228:231], a[164:167], v[48:51]\n"
"ds_read_b64_tr_b16 a[192:193], v244 offset:0\n"
"ds_read_b128 a[68:71], v101 offset:45056\n"
"v_mfma_f32_16x16x32_bf16 v[64:67], a[224:227], a[168:171], v[228:231]\n"
"ds_read_b64_tr_b16 a[212:213], v244 offset:9216\n"
"ds_read_b128 a[164:167], v253 offset:0\n"
"v_mfma_f32_16x16x32_bf16 v[64:67], a[228:231], a[172:175], v[64:67]\n"
"ds_read_b64_tr_b16 a[208:209], v244 offset:1024\n"
"ds_read_b64_tr_b16 a[218:219], v245 offset:1536\n"
"v_mfma_f32_16x16x32_bf16 v[68:71], a[232:235], a[168:171], v[228:231]\n"
"ds_read_b64_tr_b16 a[200:201], v245 offset:0\n"
"ds_read_b128 a[84:87], v101 offset:61440\n"
"v_mfma_f32_16x16x32_bf16 v[68:71], a[236:239], a[172:175], v[68:71]\n"
"buffer_load_dwordx4 v249, s[24:27], s40 offen offset:1024 lds\n"
"v_mfma_f32_16x16x32_bf16 v[72:75], a[240:243], a[168:171], v[228:231]\n"
"ds_read_b64_tr_b16 a[214:215], v244 offset:9728\n"
"ds_read_b128 a[60:63], v101 offset:32768\n"
"v_mfma_f32_16x16x32_bf16 v[72:75], a[244:247], a[172:175], v[72:75]\n"
"v_xor_b32 v250, s74, v250\n"
"v_xor_b32 v251, s74, v251\n"
"v_mfma_f32_16x16x32_bf16 v[76:79], a[248:251], a[168:171], v[228:231]\n"
"ds_read_b128 a[92:95], v102 offset:0\n"
"ds_read_b128 a[168:171], v250 offset:0\n"
"v_mfma_f32_16x16x32_bf16 v[76:79], a[252:255], a[172:175], v[76:79]\n"
"ds_read_b128 a[152:155], v246 offset:4096\n"
"v_xor_b32 v252, s74, v252\n"
"v_mfma_f32_16x16x32_bf16 v[92:95], a[248:251], a[176:179], v[228:231]\n"
"ds_read_b64_tr_b16 a[222:223], v245 offset:9728\n"
"ds_read_b128 a[160:163], v252 offset:0\n"
"v_mfma_f32_16x16x32_bf16 v[92:95], a[252:255], a[180:183], v[92:95]\n"
"buffer_load_dwordx4 v254, s[24:27], s40 offen offset:2048 lds\n"
"v_mfma_f32_16x16x32_bf16 v[88:91], a[240:243], a[176:179], v[228:231]\n"
"s_add_u32 s49, s49, 128\n"
"ds_read_b128 a[144:147], v246 offset:2048\n"
"ds_read_b128 a[148:151], v247 offset:2048\n"
"v_mfma_f32_16x16x32_bf16 v[88:91], a[244:247], a[180:183], v[88:91]\n"
"s_sub_u32 s41, s49, 1280\n"
"ds_read_b128 a[136:139], v246 offset:0\n"
"v_mfma_f32_16x16x32_bf16 v[84:87], a[232:235], a[176:179], v[228:231]\n"
"s_min_u32 s49, s49, s41\n"
"ds_read_b64_tr_b16 a[196:197], v244 offset:8192\n"
"ds_read_b64_tr_b16 a[210:211], v244 offset:1536\n"
"v_mfma_f32_16x16x32_bf16 v[84:87], a[236:239], a[180:183], v[84:87]\n"
"s_xor_b32 s51, s51, s74\n"
"ds_read_b64_tr_b16 a[194:195], v244 offset:512\n"
"ds_read_b128 a[156:159], v247 offset:4096\n"
"v_mfma_f32_16x16x32_bf16 v[80:83], a[224:227], a[176:179], v[228:231]\n"
"ds_read_b128 a[176:179], v250 offset:2048\n"
"v_mfma_f32_16x16x32_bf16 v[80:83], a[228:231], a[180:183], v[80:83]\n"
"ds_read_b128 a[172:175], v251 offset:0\n"
"ds_read_b128 a[180:183], v251 offset:2048\n"
"v_mfma_f32_16x16x32_bf16 v[96:99], a[224:227], a[184:187], v[228:231]\n"
"v_mfma_f32_16x16x32_bf16 v[96:99], a[228:231], a[188:191], v[96:99]\n"
"v_mfma_f32_16x16x32_bf16 v[100:103], a[232:235], a[184:187], v[228:231]\n"
"v_mfma_f32_16x16x32_bf16 v[100:103], a[236:239], a[188:191], v[100:103]\n"
"v_mfma_f32_16x16x32_bf16 v[104:107], a[240:243], a[184:187], v[228:231]\n"
"v_mfma_f32_16x16x32_bf16 v[104:107], a[244:247], a[188:191], v[104:107]\n"
"v_mfma_f32_16x16x32_bf16 v[108:111], a[248:251], a[184:187], v[228:231]\n"
"s_add_u32 s47, s47, 1\n"
"ds_read_b128 a[184:187], v250 offset:4096\n"
"v_mfma_f32_16x16x32_bf16 v[108:111], a[252:255], a[188:191], v[108:111]\n"
"s_waitcnt lgkmcnt(1)\n"
"ds_read_b128 a[188:191], v251 offset:4096\n"
"s_mov_b32 s62, 18\n"
"s_branch LPE_%=\n"
"LPT2_%=:\n"
"v_mfma_f32_16x16x32_bf16 v[112:115], a[192:195], a[136:139], v[112:115]\n"
"s_add_u32 s78, s32, s96\ns_addc_u32 s79, s33, 0\n"
"s_mov_b64 s[80:81], 0\n"
"s_mov_b32 s82, 0\n"
"v_mfma_f32_16x16x32_bf16 v[112:115], a[196:199], a[140:143], v[112:115]\n"
"v_xor_b32 v248, 64, v240\n"
"v_mfma_f32_16x16x32_bf16 v[116:119], a[200:203], a[136:139], v[116:119]\n"
"s_waitcnt vmcnt(11)\n"
"s_load_dword s80, s[78:79], 0\n"
"v_mfma_f32_16x16x32_bf16 v[116:119], a[204:207], a[140:143], v[116:119]\n"
"s_barrier\n"
"v_mfma_f32_16x16x32_bf16 v[120:123], a[208:211], a[136:139], v[120:123]\n"
"s_add_u32 s40, s48, 256\n"
"s_xor_b32 m0, s50, 0xc000\n"
"ds_read_b64_tr_b16 a[238:239], v245 offset:25088\n"
"v_mfma_f32_16x16x32_bf16 v[120:123], a[212:215], a[140:143], v[120:123]\n"
"buffer_load_dwordx4 v224, s[28:31], s40 offen lds\n"
"v_mfma_f32_16x16x32_bf16 v[124:127], a[216:219], a[136:139], v[124:127]\n"
"v_mfma_f32_16x16x32_bf16 v[124:127], a[220:223], a[140:143], v[124:127]\n"
"v_mfma_f32_16x16x32_bf16 v[140:143], a[216:219], a[144:147], v[140:143]\n"
"ds_read_b64_tr_b16 a[244:245], v244 offset:25600\n"
"v_mfma_f32_16x16x32_bf16 v[140:143], a[220:223], a[148:151], v[140:143]\n"
"v_mfma_f32_16x16x32_bf16 v[136:139], a[208:211], a[144:147], v[136:139]\n"
"ds_read_b64_tr_b16 a[250:251], v245 offset:17920\n"
"v_bfe_i32 v241, v242, 4, 1\n"
"v_mfma_f32_16x16x32_bf16 v[136:139], a[212:215], a[148:151], v[136:139]\n"
"buffer_load_dwordx4 v225, s[28:31], s40 offen offset:1024 lds\n"
"v_mfma_f32_16x16x32_bf16 v[132:135], a[200:203], a[144:147], v[132:135]\n"
"s_add_u32 s82, s82, s80\n"
"v_mfma_f32_16x16x32_bf16 v[132:135], a[204:207], a[148:151], v[132:135]\n"
"s_add_u32 s82, s82, s80\n"
"v_mfma_f32_16x16x32_bf16 v[128:131], a[192:195], a[144:147], v[128:131]\n"
"s_add_u32 s82, s82, s80\n"
"ds_read_b64_tr_b16 a[230:231], v244 offset:25088\n"
"v_mfma_f32_16x16x32_bf16 v[128:131], a[196:199], a[148:151], v[128:131]\n"
"s_add_u32 s82, s82, s80\n"
"v_bfe_u32 v228, v242, 8, 2\n"
"v_mfma_f32_16x16x32_bf16 v[144:147], a[192:195], a[152:155], v[144:147]\n"
"s_add_u32 s82, s82, s80\n"
"ds_read_b64_tr_b16 a[242:243], v244 offset:17920\n"
"v_mfma_f32_16x16x32_bf16 v[144:147], a[196:199], a[156:159], v[144:147]\n"
"s_add_u32 s82, s82, s80\n"
"v_mfma_f32_16x16x32_bf16 v[148:151], a[200:203], a[152:155], v[148:151]\n"
"s_add_u32 s82, s82, s80\n"
"ds_read_b64_tr_b16 a[228:229], v244 offset:24576\n"
"v_mfma_f32_16x16x32_bf16 v[148:151], a[204:207], a[156:159], v[148:151]\n"
"s_add_u32 s82, s82, s80\n"
"buffer_load_dwordx4 v226, s[28:31], s40 offen offset:2048 lds\n"
"v_mfma_f32_16x16x32_bf16 v[152:155], a[208:211], a[152:155], v[152:155]\n"
"s_add_u32 s82, s82, s80\n"
"v_mfma_f32_16x16x32_bf16 v[152:155], a[212:215], a[156:159], v[152:155]\n"
"s_add_u32 s82, s82, s80\n"
"v_and_b32 v229, 0x60, v242\n"
"v_mfma_f32_16x16x32_bf16 v[156:159], a[216:219], a[152:155], v[156:159]\n"
"s_add_u32 s82, s82, s80\n"
"ds_read_b64_tr_b16 a[232:233], v245 offset:16384\n"
"v_mfma_f32_16x16x32_bf16 v[156:159], a[220:223], a[156:159], v[156:159]\n"
"s_add_u32 s82, s82, s80\n"
"v_mfma_f32_16x16x32_bf16 v[172:175], a[216:219], a[160:163], v[172:175]\n"
"s_add_u32 s82, s82, s80\n"
"ds_read_b64_tr_b16 a[234:235], v245 offset:16896\n"
"v_lshlrev_b32 v228, 4, v228\n"
"v_mfma_f32_16x16x32_bf16 v[172:175], a[220:223], a[164:167], v[172:175]\n"
"s_add_u32 s82, s82, s80\n"
"buffer_load_dwordx4 v227, s[28:31], s40 offen offset:3072 lds\n"
"v_mfma_f32_16x16x32_bf16 v[168:171], a[208:211], a[160:163], v[168:171]\n"
"s_add_u32 s82, s82, s80\n"
"s_mov_b32 s58, 122880\n"
"v_mfma_f32_16x16x32_bf16 v[168:171], a[212:215], a[164:167], v[168:171]\n"
"s_add_u32 s82, s82, s80\n"
"s_mov_b32 s47, 0\n"
"v_mfma_f32_16x16x32_bf16 v[164:167], a[200:203], a[160:163], v[164:167]\n"
"s_add_u32 s82, s82, s80\n"
"s_sub_u32 s52, s54, 14336\n"
"ds_read_b64_tr_b16 a[246:247], v244 offset:26112\n"
"v_mfma_f32_16x16x32_bf16 v[164:167], a[204:207], a[164:167], v[164:167]\n"
"s_load_dword s81, s[78:79], 0x2000\n"
"s_mov_b32 s53, 14336\n"
"v_mfma_f32_16x16x32_bf16 v[160:163], a[192:195], a[160:163], v[160:163]\n"
"s_mov_b32 m0, s44\n"
"ds_read_b64_tr_b16 a[240:241], v244 offset:17408\n"
"v_mfma_f32_16x16x32_bf16 v[160:163], a[196:199], a[164:167], v[160:163]\n"
"buffer_load_dwordx4 v240, s[16:19], s56 offen nt lds\n"
"v_mfma_f32_16x16x32_bf16 v[176:179], a[192:195], a[168:171], v[176:179]\n"
"v_mfma_f32_16x16x32_bf16 v[176:179], a[196:199], a[172:175], v[176:179]\n"
"v_mfma_f32_16x16x32_bf16 v[180:183], a[200:203], a[168:171], v[180:183]\n"
"ds_read_b64_tr_b16 a[252:253], v245 offset:25600\n"
"v_mfma_f32_16x16x32_bf16 v[180:183], a[204:207], a[172:175], v[180:183]\n"
"v_mfma_f32_16x16x32_bf16 v[184:187], a[208:211], a[168:171], v[184:187]\n"
"ds_read_b64_tr_b16 a[236:237], v245 offset:24576\n"
"v_xor_b32 v228, v228, v229\n"
"v_mfma_f32_16x16x32_bf16 v[184:187], a[212:215], a[172:175], v[184:187]\n"
"v_mfma_f32_16x16x32_bf16 v[188:191], a[216:219], a[168:171], v[188:191]\n"
"s_add_u32 s40, s56, s52\n"
"ds_read_b64_tr_b16 a[226:227], v244 offset:16896\n"
"v_mfma_f32_16x16x32_bf16 v[188:191], a[220:223], a[172:175], v[188:191]\n"
"s_sub_u32 s82, s82, s81\n"
"buffer_load_dwordx4 v248, s[16:19], s40 offen offset:1024 nt lds\n"
"v_mfma_f32_16x16x32_bf16 v[204:207], a[216:219], a[176:179], v[204:207]\n"
"s_sub_u32 s82, s82, s81\n"
"v_mfma_f32_16x16x32_bf16 v[204:207], a[220:223], a[180:183], v[204:207]\n"
"s_sub_u32 s82, s82, s81\n"
"v_mfma_f32_16x16x32_bf16 v[200:203], a[208:211], a[176:179], v[200:203]\n"
"s_sub_u32 s82, s82, s81\n"
"ds_read_b64_tr_b16 a[248:249], v245 offset:17408\n"
"v_and_b32 v229, 0x30, v242\n"
"v_mfma_f32_16x16x32_bf16 v[200:203], a[212:215], a[180:183], v[200:203]\n"
"s_sub_u32 s82, s82, s81\n"
"v_mfma_f32_16x16x32_bf16 v[196:199], a[200:203], a[176:179], v[196:199]\n"
"s_sub_u32 s82, s82, s81\n"
"s_add_u32 s40, s56, s53\n"
"ds_read_b64_tr_b16 a[224:225], v244 offset:16384\n"
"v_mfma_f32_16x16x32_bf16 v[196:199], a[204:207], a[180:183], v[196:199]\n"
"s_sub_u32 s82, s82, s81\n"
"buffer_load_dwordx4 v240, s[16:19], s40 offen offset:2048 nt lds\n"
"v_mfma_f32_16x16x32_bf16 v[192:195], a[192:195], a[176:179], v[192:195]\n"
"s_sub_u32 s82, s82, s81\n"
"v_lshl_add_u32 v228, v229, 3, v228\n"
"v_mfma_f32_16x16x32_bf16 v[192:195], a[196:199], a[180:183], v[192:195]\n"
"s_waitcnt lgkmcnt(2)\n"
"s_sub_u32 s82, s82, s81\n"
"v_xor_b32 v244, s74, v244\n"
"v_mfma_f32_16x16x32_bf16 v[208:211], a[192:195], a[184:187], v[208:211]\n"
"s_sub_u32 s82, s82, s81\n"
"v_mfma_f32_16x16x32_bf16 v[208:211], a[196:199], a[188:191], v[208:211]\n"
"s_sub_u32 s82, s82, s81\n"
"ds_read_b64_tr_b16 a[254:255], v245 offset:26112\n"
"v_mfma_f32_16x16x32_bf16 v[212:215], a[200:203], a[184:187], v[212:215]\n"
"s_sub_u32 s82, s82, s81\n"
"s_add_u32 s40, s56, s54\n"
"v_mfma_f32_16x16x32_bf16 v[212:215], a[204:207], a[188:191], v[212:215]\n"
"s_sub_u32 s82, s82, s81\n"
"buffer_load_dwordx4 v248, s[16:19], s40 offen offset:3072 nt lds\n"
"v_mfma_f32_16x16x32_bf16 v[216:219], a[208:211], a[184:187], v[216:219]\n"
"s_sub_u32 s82, s82, s81\n"
"v_and_b32 v229, 0xc0, v242\n"
"v_xor_b32 v251, s74, v251\n"
"v_mfma_f32_16x16x32_bf16 v[216:219], a[212:215], a[188:191], v[216:219]\n"
"s_sub_u32 s82, s82, s81\n"
"v_mfma_f32_16x16x32_bf16 v[220:223], a[216:219], a[184:187], v[220:223]\n"
"s_sub_u32 s82, s82, s81\n"
"v_mfma_f32_16x16x32_bf16 v[220:223], a[220:223], a[188:191], v[220:223]\n"
"s_waitcnt lgkmcnt(1)\n"
"s_sub_u32 s82, s82, s81\n"
"v_mfma_f32_16x16x32_bf16 v[0:3], a[224:227], a[136:139], v[0:3]\n"
"v_mfma_f32_16x16x32_bf16 v[0:3], a[228:231], a[140:143], v[0:3]\n"
"v_lshl_add_u32 v228, v229, 4, v228\n"
"v_xor_b32 v245, s74, v245\n"
"v_mfma_f32_16x16x32_bf16 v[4:7], a[232:235], a[136:139], v[4:7]\n"
"s_waitcnt vmcnt(8) lgkmcnt(0)\n"
"v_mfma_f32_16x16x32_bf16 v[4:7], a[236:239], a[140:143], v[4:7]\n"
"s_barrier\n"
"v_mfma_f32_16x16x32_bf16 v[8:11], a[240:243], a[136:139], v[8:11]\n"
"s_add_u32 m0, s44, 65536\n"
"ds_read_b64_tr_b16 a[222:223], v245 offset:9728\n"
"v_mfma_f32_16x16x32_bf16 v[8:11], a[244:247], a[140:143], v[8:11]\n"
"buffer_load_dwordx4 v240, s[20:23], s56 offen nt lds\n"
"v_mfma_f32_16x16x32_bf16 v[12:15], a[248:251], a[136:139], v[12:15]\n"
"s_lshr_b32 s82, s82, 31\n"
"v_mfma_f32_16x16x32_bf16 v[12:15], a[252:255], a[140:143], v[12:15]\n"
"s_add_u32 s82, s82, s60\n"
"v_mfma_f32_16x16x32_bf16 v[28:31], a[248:251], a[144:147], v[28:31]\n"
"s_add_u32 s82, s82, 32\n"
"ds_read_b64_tr_b16 a[216:217], v245 offset:1024\n"
"v_xor_b32 v247, s74, v247\n"
"v_mfma_f32_16x16x32_bf16 v[28:31], a[252:255], a[148:151], v[28:31]\n"
"v_mov_b32 v254, s82\n"
"ds_read_b64_tr_b16 a[196:197], v244 offset:8192\n"
"v_mfma_f32_16x16x32_bf16 v[24:27], a[240:243], a[144:147], v[24:27]\n"
"v_lshlrev_b32_e64 v255, 2, s93\n"
"ds_read_b64_tr_b16 a[198:199], v244 offset:8704\n"
"v_xor_b32 v246, s74, v246\n"
"v_mfma_f32_16x16x32_bf16 v[24:27], a[244:247], a[148:151], v[24:27]\n"
"ds_read_b64_tr_b16 a[214:215], v244 offset:9728\n"
"v_mov_b32 v229, s43\n"
"v_mfma_f32_16x16x32_bf16 v[20:23], a[232:235], a[144:147], v[20:23]\n"
"s_add_u32 s40, s56, s52\n"
"ds_read_b64_tr_b16 a[204:205], v245 offset:8192\n"
"v_mfma_f32_16x16x32_bf16 v[20:23], a[236:239], a[148:151], v[20:23]\n"
"buffer_load_dwordx4 v248, s[20:23], s40 offen offset:1024 nt lds\n"
"v_mfma_f32_16x16x32_bf16 v[16:19], a[224:227], a[144:147], v[16:19]\n"
"v_mfma_f32_16x16x32_bf16 v[16:19], a[228:231], a[148:151], v[16:19]\n"
"s_mov_b64 exec, 1\n"
"global_store_dword v255, v254, s[6:7] offset:256 sc1\n"
"s_mov_b64 exec, -1\n"
"v_mfma_f32_16x16x32_bf16 v[32:35], a[224:227], a[152:155], v[32:35]\n"
"ds_read_b64_tr_b16 a[202:203], v245 offset:512\n"
"v_mfma_f32_16x16x32_bf16 v[32:35], a[228:231], a[156:159], v[32:35]\n"
"v_mfma_f32_16x16x32_bf16 v[36:39], a[232:235], a[152:155], v[36:39]\n"
"s_add_u32 s40, s56, s53\n"
"ds_read_b128 a[140:143], v247 offset:0\n"
"v_mfma_f32_16x16x32_bf16 v[36:39], a[236:239], a[156:159], v[36:39]\n"
"buffer_load_dwordx4 v240, s[20:23], s40 offen offset:2048 nt lds\n"
"v_mfma_f32_16x16x32_bf16 v[40:43], a[240:243], a[152:155], v[40:43]\n"
"v_xor_b32 v250, s74, v250\n"
"v_mfma_f32_16x16x32_bf16 v[40:43], a[244:247], a[156:159], v[40:43]\n"
"v_mfma_f32_16x16x32_bf16 v[44:47], a[248:251], a[152:155], v[44:47]\n"
"ds_read_b64_tr_b16 a[200:201], v245 offset:0\n"
"v_mul_u32_u24 v229, 0x81, v229\n"
"v_mfma_f32_16x16x32_bf16 v[44:47], a[252:255], a[156:159], v[44:47]\n"
"ds_read_b64_tr_b16 a[220:221], v245 offset:9216\n"
"v_mfma_f32_16x16x32_bf16 v[60:63], a[248:251], a[160:163], v[60:63]\n"
"s_add_u32 s40, s56, s54\n"
"ds_read_b64_tr_b16 a[210:211], v244 offset:1536\n"
"v_xor_b32 v253, s74, v253\n"
"v_mfma_f32_16x16x32_bf16 v[60:63], a[252:255], a[164:167], v[60:63]\n"
"buffer_load_dwordx4 v248, s[20:23], s40 offen offset:3072 nt lds\n"
"v_mfma_f32_16x16x32_bf16 v[56:59], a[240:243], a[160:163], v[56:59]\n"
"s_add_u32 s55, s55, s59\n"
"v_mfma_f32_16x16x32_bf16 v[56:59], a[244:247], a[164:167], v[56:59]\n"
"s_or_b32 s55, s55, s58\n"
"v_mfma_f32_16x16x32_bf16 v[52:55], a[232:235], a[160:163], v[52:55]\n"
"s_xor_b32 s56, s55, s57\n"
"ds_read_b128 a[136:139], v246 offset:0\n"
"v_mfma_f32_16x16x32_bf16 v[52:55], a[236:239], a[164:167], v[52:55]\n"
"v_mfma_f32_16x16x32_bf16 v[48:51], a[224:227], a[160:163], v[48:51]\n"
"s_add_u32 m0, s44, 16384\n"
"ds_read_b128 a[156:159], v247 offset:4096\n"
"v_mfma_f32_16x16x32_bf16 v[48:51], a[228:231], a[164:167], v[48:51]\n"
"buffer_load_dwordx4 v240, s[16:19], s56 offen nt lds\n"
"v_mfma_f32_16x16x32_bf16 v[64:67], a[224:227], a[168:171], v[64:67]\n"
"v_mfma_f32_16x16x32_bf16 v[64:67], a[228:231], a[172:175], v[64:67]\n"
"v_mfma_f32_16x16x32_bf16 v[68:71], a[232:235], a[168:171], v[68:71]\n"
"ds_read_b128 a[164:167], v253 offset:0\n"
"v_mfma_f32_16x16x32_bf16 v[68:71], a[236:239], a[172:175], v[68:71]\n"
"v_mfma_f32_16x16x32_bf16 v[72:75], a[240:243], a[168:171], v[72:75]\n"
"ds_read_b64_tr_b16 a[218:219], v245 offset:1536\n"
"v_mfma_f32_16x16x32_bf16 v[72:75], a[244:247], a[172:175], v[72:75]\n"
"ds_read_b64_tr_b16 a[194:195], v244 offset:512\n"
"v_mfma_f32_16x16x32_bf16 v[76:79], a[248:251], a[168:171], v[76:79]\n"
"s_add_u32 s40, s56, s52\n"
"ds_read_b64_tr_b16 a[206:207], v245 offset:8704\n"
"v_mfma_f32_16x16x32_bf16 v[76:79], a[252:255], a[172:175], v[76:79]\n"
"buffer_load_dwordx4 v248, s[16:19], s40 offen offset:1024 nt lds\n"
"v_mfma_f32_16x16x32_bf16 v[92:95], a[248:251], a[176:179], v[92:95]\n"
"v_mfma_f32_16x16x32_bf16 v[92:95], a[252:255], a[180:183], v[92:95]\n"
"v_mfma_f32_16x16x32_bf16 v[88:91], a[240:243], a[176:179], v[88:91]\n"
"ds_read_b64_tr_b16 a[208:209], v244 offset:1024\n"
"v_mfma_f32_16x16x32_bf16 v[88:91], a[244:247], a[180:183], v[88:91]\n"
"ds_read_b64_tr_b16 a[212:213], v244 offset:9216\n"
"v_mfma_f32_16x16x32_bf16 v[84:87], a[232:235], a[176:179], v[84:87]\n"
"s_add_u32 s40, s56, s53\n"
"ds_read_b64_tr_b16 a[192:193], v244 offset:0\n"
"v_mfma_f32_16x16x32_bf16 v[84:87], a[236:239], a[180:183], v[84:87]\n"
"buffer_load_dwordx4 v240, s[16:19], s40 offen offset:2048 nt lds\n"
"v_mfma_f32_16x16x32_bf16 v[80:83], a[224:227], a[176:179], v[80:83]\n"
"v_mfma_f32_16x16x32_bf16 v[80:83], a[228:231], a[180:183], v[80:83]\n"
"v_mfma_f32_16x16x32_bf16 v[96:99], a[224:227], a[184:187], v[96:99]\n"
"ds_read_b128 a[172:175], v251 offset:0\n"
"v_mfma_f32_16x16x32_bf16 v[96:99], a[228:231], a[188:191], v[96:99]\n"
"v_mfma_f32_16x16x32_bf16 v[100:103], a[232:235], a[184:187], v[100:103]\n"
"s_add_u32 s40, s56, s54\n"
"ds_read_b128 a[148:151], v247 offset:2048\n"
"v_mfma_f32_16x16x32_bf16 v[100:103], a[236:239], a[188:191], v[100:103]\n"
"buffer_load_dwordx4 v248, s[16:19], s40 offen offset:3072 nt lds\n"
"v_mfma_f32_16x16x32_bf16 v[104:107], a[240:243], a[184:187], v[104:107]\n"
"v_mfma_f32_16x16x32_bf16 v[104:107], a[244:247], a[188:191], v[104:107]\n"
"v_and_b32 v254, 0x20, v242\n"
"v_and_b32 v241, 0xaaaaaaaa, v241\n"
"v_mfma_f32_16x16x32_bf16 v[108:111], a[248:251], a[184:187], v[108:111]\n"
"ds_read_b128 a[168:171], v250 offset:0\n"
"v_mfma_f32_16x16x32_bf16 v[108:111], a[252:255], a[188:191], v[108:111]\n"
"s_waitcnt lgkmcnt(3)\n"
"v_mfma_f32_16x16x32_bf16 v[112:115], a[192:195], a[136:139], v[112:115]\n"
"ds_read_b128 a[184:187], v250 offset:4096\n"
"v_mfma_f32_16x16x32_bf16 v[112:115], a[196:199], a[140:143], v[112:115]\n"
"v_mfma_f32_16x16x32_bf16 v[116:119], a[200:203], a[136:139], v[116:119]\n"
"s_waitcnt vmcnt(13)\n"
"ds_read_b128 a[180:183], v251 offset:2048\n"
"v_and_b32 v249, 0xf0, v242\n"
"v_mfma_f32_16x16x32_bf16 v[116:119], a[204:207], a[140:143], v[116:119]\n"
"s_barrier\n"
"v_mfma_f32_16x16x32_bf16 v[120:123], a[208:211], a[136:139], v[120:123]\n"
"s_add_u32 m0, s44, 81920\n"
"ds_read_b128 a[176:179], v250 offset:2048\n"
"v_mfma_f32_16x16x32_bf16 v[120:123], a[212:215], a[140:143], v[120:123]\n"
"buffer_load_dwordx4 v240, s[20:23], s56 offen nt lds\n"
"v_mfma_f32_16x16x32_bf16 v[124:127], a[216:219], a[136:139], v[124:127]\n"
"v_bfe_u32 v255, v242, 9, 1\n"
"v_lshl_add_u32 v255, v255, 8, v249\n"
"v_mfma_f32_16x16x32_bf16 v[124:127], a[220:223], a[140:143], v[124:127]\n"
"s_waitcnt lgkmcnt(3)\n"
"v_xor_b32 v252, s74, v252\n"
"v_mfma_f32_16x16x32_bf16 v[188:191], a[216:219], a[168:171], v[188:191]\n"
"ds_read_b128 a[188:191], v251 offset:4096\n"
"v_xor_b32 v228, v228, v229\n"
"v_mfma_f32_16x16x32_bf16 v[188:191], a[220:223], a[172:175], v[188:191]\n"
"v_mfma_f32_16x16x32_bf16 v[184:187], a[208:211], a[168:171], v[184:187]\n"
"ds_read_b128 a[144:147], v246 offset:2048\n"
"v_add_u32 v254, -1, v254\n"
"v_mfma_f32_16x16x32_bf16 v[184:187], a[212:215], a[172:175], v[184:187]\n"
"v_mfma_f32_16x16x32_bf16 v[180:183], a[200:203], a[168:171], v[180:183]\n"
"ds_read_b64_tr_b16 a[234:235], v245 offset:16896\n"
"v_mfma_f32_16x16x32_bf16 v[180:183], a[204:207], a[172:175], v[180:183]\n"
"v_mfma_f32_16x16x32_bf16 v[176:179], a[192:195], a[168:171], v[176:179]\n"
"ds_read_b128 a[160:163], v252 offset:0\n"
"v_mfma_f32_16x16x32_bf16 v[176:179], a[196:199], a[172:175], v[176:179]\n"
"s_waitcnt lgkmcnt(4)\n"
"v_mfma_f32_16x16x32_bf16 v[192:195], a[192:195], a[176:179], v[192:195]\n"
"ds_read_b64_tr_b16 a[230:231], v244 offset:25088\n"
"v_or_b32 v241, 0x44444444, v241\n"
"v_mfma_f32_16x16x32_bf16 v[192:195], a[196:199], a[180:183], v[192:195]\n"
"v_bfe_u32 v249, v242, 8, 1\n"
"v_mfma_f32_16x16x32_bf16 v[196:199], a[200:203], a[176:179], v[196:199]\n"
"s_add_u32 s40, s56, s52\n"
"ds_read_b64_tr_b16 a[246:247], v244 offset:26112\n"
"v_mfma_f32_16x16x32_bf16 v[196:199], a[204:207], a[180:183], v[196:199]\n"
"buffer_load_dwordx4 v248, s[20:23], s40 offen offset:1024 nt lds\n"
"v_mfma_f32_16x16x32_bf16 v[200:203], a[208:211], a[176:179], v[200:203]\n"
"v_xor_b32 v229, 64, v228\n"
"v_mfma_f32_16x16x32_bf16 v[200:203], a[212:215], a[180:183], v[200:203]\n"
"v_mfma_f32_16x16x32_bf16 v[204:207], a[216:219], a[176:179], v[204:207]\n"
"ds_read_b64_tr_b16 a[240:241], v244 offset:17408\n"
"v_lshl_add_u32 v255, v249, 3, v255\n"
"v_mfma_f32_16x16x32_bf16 v[204:207], a[220:223], a[180:183], v[204:207]\n"
"v_xor_b32 v231, 0x2000, v229\n"
"v_xor_b32_e64 v249, s42, 1\n"
"v_mfma_f32_16x16x32_bf16 v[220:223], a[216:219], a[184:187], v[220:223]\n"
"s_waitcnt lgkmcnt(2)\n"
"ds_read_b64_tr_b16 a[232:233], v245 offset:16384\n"
"v_mfma_f32_16x16x32_bf16 v[220:223], a[220:223], a[188:191], v[220:223]\n"
"ds_read_b64_tr_b16 a[226:227], v244 offset:16896\n"
"v_mfma_f32_16x16x32_bf16 v[216:219], a[208:211], a[184:187], v[216:219]\n"
"ds_read_b64_tr_b16 a[224:225], v244 offset:16384\n"
"v_xor_b32 v230, 0x2000, v228\n"
"v_mfma_f32_16x16x32_bf16 v[216:219], a[212:215], a[188:191], v[216:219]\n"
"v_pk_fma_f32 v[176:177], v[176:177], s[70:71], s[70:71] op_sel:[0,0,1] op_sel_hi:[1,0,1]\nv_pk_fma_f32 v[178:179], v[178:179], s[70:71], s[70:71] op_sel:[0,0,1] op_sel_hi:[1,0,1]\nv_pk_fma_f32 v[180:181], v[180:181], s[70:71], s[70:71] op_sel:[0,0,1] op_sel_hi:[1,0,1]\nv_pk_fma_f32 v[182:183], v[182:183], s[70:71], s[70:71] op_sel:[0,0,1] op_sel_hi:[1,0,1]\nv_pk_fma_f32 v[184:185], v[184:185], s[70:71], s[70:71] op_sel:[0,0,1] op_sel_hi:[1,0,1]\nv_pk_fma_f32 v[186:187], v[186:187], s[70:71], s[70:71] op_sel:[0,0,1] op_sel_hi:[1,0,1]\nv_pk_fma_f32 v[188:189], v[188:189], s[70:71], s[70:71] op_sel:[0,0,1] op_sel_hi:[1,0,1]\nv_pk_fma_f32 v[190:191], v[190:191], s[70:71], s[70:71] op_sel:[0,0,1] op_sel_hi:[1,0,1]\nv_pk_fma_f32 v[192:193], v[192:193], s[70:71], s[70:71] op_sel:[0,0,1] op_sel_hi:[1,0,1]\nv_pk_fma_f32 v[194:195], v[194:195], s[70:71], s[70:71] op_sel:[0,0,1] op_sel_hi:[1,0,1]\nv_pk_fma_f32 v[196:197], v[196:197], s[70:71], s[70:71] op_sel:[0,0,1] op_sel_hi:[1,0,1]\nv_pk_fma_f32 v[198:199], v[198:199], s[70:71], s[70:71] op_sel:[0,0,1] op_sel_hi:[1,0,1]\nv_pk_fma_f32 v[200:201], v[200:201], s[70:71], s[70:71] op_sel:[0,0,1] op_sel_hi:[1,0,1]\nv_pk_fma_f32 v[202:203], v[202:203], s[70:71], s[70:71] op_sel:[0,0,1] op_sel_hi:[1,0,1]\nv_pk_fma_f32 v[204:205], v[204:205], s[70:71], s[70:71] op_sel:[0,0,1] op_sel_hi:[1,0,1]\nv_pk_fma_f32 v[206:207], v[206:207], s[70:71], s[70:71] op_sel:[0,0,1] op_sel_hi:[1,0,1]\n"
"v_mfma_f32_16x16x32_bf16 v[212:215], a[200:203], a[184:187], v[212:215]\n"
"ds_read_b64_tr_b16 a[252:253], v245 offset:25600\n"
"v_cvt_pk_bf16_f32 v192, v192, v193\n"
"v_mfma_f32_16x16x32_bf16 v[212:215], a[204:207], a[188:191], v[212:215]\n"
"v_cvt_pk_bf16_f32 v176, v176, v177\n"
"v_cvt_pk_bf16_f32 v193, v194, v195\n"
"v_mfma_f32_16x16x32_bf16 v[208:211], a[192:195], a[184:187], v[208:211]\n"
"s_add_u32 s40, s56, s53\n"
"ds_read_b64_tr_b16 a[248:249], v245 offset:17408\n"
"v_mfma_f32_16x16x32_bf16 v[208:211], a[196:199], a[188:191], v[208:211]\n"
"buffer_load_dwordx4 v240, s[20:23], s40 offen offset:2048 nt lds\n"
"v_mfma_f32_16x16x32_bf16 v[160:163], a[192:195], a[160:163], v[160:163]\n"
"v_cvt_pk_bf16_f32 v177, v178, v179\n"
"v_cvt_pk_bf16_f32 v184, v184, v185\n"
"v_mfma_f32_16x16x32_bf16 v[160:163], a[196:199], a[164:167], v[160:163]\n"
"v_cvt_pk_bf16_f32 v179, v182, v183\n"
"v_cvt_pk_bf16_f32 v185, v186, v187\n"
"v_mfma_f32_16x16x32_bf16 v[164:167], a[200:203], a[160:163], v[164:167]\n"
"ds_read_b64_tr_b16 a[244:245], v244 offset:25600\n"
"v_cvt_pk_bf16_f32 v200, v200, v201\n"
"v_mfma_f32_16x16x32_bf16 v[164:167], a[204:207], a[164:167], v[164:167]\n"
"v_cvt_pk_bf16_f32 v201, v202, v203\n"
"v_ashrrev_i32 v254, 31, v254\n"
"v_mfma_f32_16x16x32_bf16 v[168:171], a[208:211], a[160:163], v[168:171]\n"
"ds_read_b64_tr_b16 a[254:255], v245 offset:26112\n"
"v_cvt_pk_bf16_f32 v194, v196, v197\n"
"v_mfma_f32_16x16x32_bf16 v[168:171], a[212:215], a[164:167], v[168:171]\n"
"v_cvt_pk_bf16_f32 v202, v204, v205\n"
"v_mfma_f32_16x16x32_bf16 v[172:175], a[216:219], a[160:163], v[172:175]\n"
"ds_read_b64_tr_b16 a[242:243], v244 offset:17920\n"
"v_and_b32 v254, 0x3f803f80, v254\n"
"v_mfma_f32_16x16x32_bf16 v[172:175], a[220:223], a[164:167], v[172:175]\n"
"v_bfe_u32 v196, v242, 8, 2\n"
"v_lshl_add_u32 v196, v196, 4, s43\n"
"v_mfma_f32_16x16x32_bf16 v[140:143], a[216:219], a[144:147], v[140:143]\n"
"ds_read_b64_tr_b16 a[236:237], v245 offset:24576\n"
"v_lshl_add_u32 v255, v249, 12, v255\n"
"v_mfma_f32_16x16x32_bf16 v[140:143], a[220:223], a[148:151], v[140:143]\n"
"v_cvt_pk_bf16_f32 v203, v206, v207\n"
"v_mfma_f32_16x16x32_bf16 v[136:139], a[208:211], a[144:147], v[136:139]\n"
"ds_read_b128 a[152:155], v246 offset:4096\n"
"v_cvt_pk_bf16_f32 v178, v180, v181\n"
"v_mfma_f32_16x16x32_bf16 v[136:139], a[212:215], a[148:151], v[136:139]\n"
"v_pk_fma_f32 v[208:209], v[208:209], s[70:71], s[70:71] op_sel:[0,0,1] op_sel_hi:[1,0,1]\nv_pk_fma_f32 v[210:211], v[210:211], s[70:71], s[70:71] op_sel:[0,0,1] op_sel_hi:[1,0,1]\nv_pk_fma_f32 v[212:213], v[212:213], s[70:71], s[70:71] op_sel:[0,0,1] op_sel_hi:[1,0,1]\nv_pk_fma_f32 v[214:215], v[214:215], s[70:71], s[70:71] op_sel:[0,0,1] op_sel_hi:[1,0,1]\nv_pk_fma_f32 v[216:217], v[216:217], s[70:71], s[70:71] op_sel:[0,0,1] op_sel_hi:[1,0,1]\nv_pk_fma_f32 v[218:219], v[218:219], s[70:71], s[70:71] op_sel:[0,0,1] op_sel_hi:[1,0,1]\nv_pk_fma_f32 v[220:221], v[220:221], s[70:71], s[70:71] op_sel:[0,0,1] op_sel_hi:[1,0,1]\nv_pk_fma_f32 v[222:223], v[222:223], s[70:71], s[70:71] op_sel:[0,0,1] op_sel_hi:[1,0,1]\nv_pk_fma_f32 v[160:161], v[160:161], s[70:71], s[70:71] op_sel:[0,0,1] op_sel_hi:[1,0,1]\nv_pk_fma_f32 v[162:163], v[162:163], s[70:71], s[70:71] op_sel:[0,0,1] op_sel_hi:[1,0,1]\nv_pk_fma_f32 v[164:165], v[164:165], s[70:71], s[70:71] op_sel:[0,0,1] op_sel_hi:[1,0,1]\nv_pk_fma_f32 v[166:167], v[166:167], s[70:71], s[70:71] op_sel:[0,0,1] op_sel_hi:[1,0,1]\nv_pk_fma_f32 v[168:169], v[168:169], s[70:71], s[70:71] op_sel:[0,0,1] op_sel_hi:[1,0,1]\nv_pk_fma_f32 v[170:171], v[170:171], s[70:71], s[70:71] op_sel:[0,0,1] op_sel_hi:[1,0,1]\nv_pk_fma_f32 v[172:173], v[172:173], s[70:71], s[70:71] op_sel:[0,0,1] op_sel_hi:[1,0,1]\nv_pk_fma_f32 v[174:175], v[174:175], s[70:71], s[70:71] op_sel:[0,0,1] op_sel_hi:[1,0,1]\n"
"v_mfma_f32_16x16x32_bf16 v[132:135], a[200:203], a[144:147], v[132:135]\n"
"s_add_u32 s40, s56, s54\n"
"ds_read_b64_tr_b16 a[250:251], v245 offset:17920\n"
"v_cvt_pk_bf16_f32 v160, v160, v161\n"
"v_mfma_f32_16x16x32_bf16 v[132:135], a[204:207], a[148:151], v[132:135]\n"
"buffer_load_dwordx4 v248, s[20:23], s40 offen offset:3072 nt lds\n"
"v_mfma_f32_16x16x32_bf16 v[128:131], a[192:195], a[144:147], v[128:131]\n"
"s_add_u32 s55, s55, s59\n"
"v_cvt_pk_bf16_f32 v186, v188, v189\n"
"v_cvt_pk_bf16_f32 v216, v216, v217\n"
"v_mfma_f32_16x16x32_bf16 v[128:131], a[196:199], a[148:151], v[128:131]\n"
"s_waitcnt lgkmcnt(1)\n"
"s_or_b32 s55, s55, s58\n"
"v_cvt_pk_bf16_f32 v208, v208, v209\n"
"v_cvt_pk_bf16_f32 v161, v162, v163\n"
"v_mfma_f32_16x16x32_bf16 v[144:147], a[192:195], a[152:155], v[144:147]\n"
"s_xor_b32 s56, s55, s57\n"
"ds_read_b64_tr_b16 a[238:239], v245 offset:25088\n"
"v_cvt_pk_bf16_f32 v187, v190, v191\n"
"v_mfma_f32_16x16x32_bf16 v[144:147], a[196:199], a[156:159], v[144:147]\n"
"v_cvt_pk_bf16_f32 v217, v218, v219\n"
"v_cvt_pk_bf16_f32 v168, v168, v169\n"
"v_mfma_f32_16x16x32_bf16 v[148:151], a[200:203], a[152:155], v[148:151]\n"
"v_cvt_pk_bf16_f32 v195, v198, v199\n"
"v_xor_b32 v198, 0x400, v243\n"
"v_mfma_f32_16x16x32_bf16 v[148:151], a[204:207], a[156:159], v[148:151]\n"
"ds_read_b64_tr_b16 a[228:229], v244 offset:24576\n"
"v_cvt_pk_bf16_f32 v169, v170, v171\n"
"v_mfma_f32_16x16x32_bf16 v[152:155], a[208:211], a[152:155], v[152:155]\n"
"v_cvt_pk_bf16_f32 v163, v166, v167\n"
"v_cvt_pk_bf16_f32 v171, v174, v175\n"
"v_mfma_f32_16x16x32_bf16 v[152:155], a[212:215], a[156:159], v[152:155]\n"
"v_pk_fma_f32 v[112:113], v[112:113], s[70:71], s[70:71] op_sel:[0,0,1] op_sel_hi:[1,0,1]\nv_pk_fma_f32 v[114:115], v[114:115], s[70:71], s[70:71] op_sel:[0,0,1] op_sel_hi:[1,0,1]\nv_pk_fma_f32 v[116:117], v[116:117], s[70:71], s[70:71] op_sel:[0,0,1] op_sel_hi:[1,0,1]\nv_pk_fma_f32 v[118:119], v[118:119], s[70:71], s[70:71] op_sel:[0,0,1] op_sel_hi:[1,0,1]\nv_pk_fma_f32 v[120:121], v[120:121], s[70:71], s[70:71] op_sel:[0,0,1] op_sel_hi:[1,0,1]\nv_pk_fma_f32 v[122:123], v[122:123], s[70:71], s[70:71] op_sel:[0,0,1] op_sel_hi:[1,0,1]\nv_pk_fma_f32 v[124:125], v[124:125], s[70:71], s[70:71] op_sel:[0,0,1] op_sel_hi:[1,0,1]\nv_pk_fma_f32 v[126:127], v[126:127], s[70:71], s[70:71] op_sel:[0,0,1] op_sel_hi:[1,0,1]\nv_pk_fma_f32 v[128:129], v[128:129], s[70:71], s[70:71] op_sel:[0,0,1] op_sel_hi:[1,0,1]\nv_pk_fma_f32 v[130:131], v[130:131], s[70:71], s[70:71] op_sel:[0,0,1] op_sel_hi:[1,0,1]\nv_pk_fma_f32 v[132:133], v[132:133], s[70:71], s[70:71] op_sel:[0,0,1] op_sel_hi:[1,0,1]\nv_pk_fma_f32 v[134:135], v[134:135], s[70:71], s[70:71] op_sel:[0,0,1] op_sel_hi:[1,0,1]\nv_pk_fma_f32 v[136:137], v[136:137], s[70:71], s[70:71] op_sel:[0,0,1] op_sel_hi:[1,0,1]\nv_pk_fma_f32 v[138:139], v[138:139], s[70:71], s[70:71] op_sel:[0,0,1] op_sel_hi:[1,0,1]\nv_pk_fma_f32 v[140:141], v[140:141], s[70:71], s[70:71] op_sel:[0,0,1] op_sel_hi:[1,0,1]\nv_pk_fma_f32 v[142:143], v[142:143], s[70:71], s[70:71] op_sel:[0,0,1] op_sel_hi:[1,0,1]\n"
"v_mfma_f32_16x16x32_bf16 v[156:159], a[216:219], a[152:155], v[156:159]\n"
"v_cvt_pk_bf16_f32 v136, v136, v137\n"
"v_cvt_pk_bf16_f32 v137, v138, v139\n"
"v_mfma_f32_16x16x32_bf16 v[156:159], a[220:223], a[156:159], v[156:159]\n"
"v_xor_b32 v197, 64, v196\n"
"v_cvt_pk_bf16_f32 v120, v120, v121\n"
"v_mfma_f32_16x16x32_bf16 v[96:99], a[224:227], a[184:187], v[96:99]\n"
"s_waitcnt lgkmcnt(0)\n"
"s_add_u32 m0, s44, 32768\n"
"v_cvt_pk_bf16_f32 v209, v210, v211\n"
"v_cvt_pk_bf16_f32 v210, v212, v213\n"
"v_mfma_f32_16x16x32_bf16 v[96:99], a[228:231], a[188:191], v[96:99]\n"
"buffer_load_dwordx4 v240, s[16:19], s56 offen nt lds\n"
"v_mfma_f32_16x16x32_bf16 v[100:103], a[232:235], a[184:187], v[100:103]\n"
"s_waitcnt vmcnt(14)\n"
"v_cvt_pk_bf16_f32 v128, v128, v129\n"
"v_cvt_pk_bf16_f32 v129, v130, v131\n"
"v_mfma_f32_16x16x32_bf16 v[100:103], a[236:239], a[188:191], v[100:103]\n"
"s_barrier\n"
"v_mfma_f32_16x16x32_bf16 v[104:107], a[240:243], a[184:187], v[104:107]\n"
"ds_read_b128 a[196:199], v198 offset:0\n"
"v_cvt_pk_bf16_f32 v211, v214, v215\n"
"v_mfma_f32_16x16x32_bf16 v[104:107], a[244:247], a[188:191], v[104:107]\n"
"s_mov_b32 s74, 0x41400000\n"
"v_cvt_pk_bf16_f32 v138, v140, v141\n"
"v_cvt_pk_bf16_f32 v139, v142, v143\n"
"v_mfma_f32_16x16x32_bf16 v[108:111], a[248:251], a[184:187], v[108:111]\n"
"ds_read_b128 a[200:203], v243 offset:2048\n"
"v_cvt_pk_bf16_f32 v162, v164, v165\n"
"v_mfma_f32_16x16x32_bf16 v[108:111], a[252:255], a[188:191], v[108:111]\n"
"v_pk_fma_f32 v[144:145], v[144:145], s[70:71], s[70:71] op_sel:[0,0,1] op_sel_hi:[1,0,1]\nv_pk_fma_f32 v[146:147], v[146:147], s[70:71], s[70:71] op_sel:[0,0,1] op_sel_hi:[1,0,1]\nv_pk_fma_f32 v[148:149], v[148:149], s[70:71], s[70:71] op_sel:[0,0,1] op_sel_hi:[1,0,1]\nv_pk_fma_f32 v[150:151], v[150:151], s[70:71], s[70:71] op_sel:[0,0,1] op_sel_hi:[1,0,1]\nv_pk_fma_f32 v[152:153], v[152:153], s[70:71], s[70:71] op_sel:[0,0,1] op_sel_hi:[1,0,1]\nv_pk_fma_f32 v[154:155], v[154:155], s[70:71], s[70:71] op_sel:[0,0,1] op_sel_hi:[1,0,1]\nv_pk_fma_f32 v[156:157], v[156:157], s[70:71], s[70:71] op_sel:[0,0,1] op_sel_hi:[1,0,1]\nv_pk_fma_f32 v[158:159], v[158:159], s[70:71], s[70:71] op_sel:[0,0,1] op_sel_hi:[1,0,1]\n"
"v_mfma_f32_16x16x32_bf16 v[92:95], a[248:251], a[176:179], v[92:95]\n"
"v_permlane16_swap_b32 v161, v163\n"
"v_mfma_f32_16x16x32_bf16 v[92:95], a[252:255], a[180:183], v[92:95]\n"
"ds_write2st64_b64 v255, v[200:201], v[202:203] offset0:102 offset1:103\n"
"v_mfma_f32_16x16x32_bf16 v[88:91], a[240:243], a[176:179], v[88:91]\n"
"v_cvt_pk_bf16_f32 v144, v144, v145\n"
"v_cvt_pk_bf16_f32 v152, v152, v153\n"
"v_mfma_f32_16x16x32_bf16 v[88:91], a[244:247], a[180:183], v[88:91]\n"
"v_cvt_pk_bf16_f32 v145, v146, v147\n"
"v_cvt_pk_bf16_f32 v153, v154, v155\n"
"v_mfma_f32_16x16x32_bf16 v[84:87], a[232:235], a[176:179], v[84:87]\n"
"v_cvt_pk_bf16_f32 v130, v132, v133\n"
"v_cvt_pk_bf16_f32 v131, v134, v135\n"
"v_mfma_f32_16x16x32_bf16 v[84:87], a[236:239], a[180:183], v[84:87]\n"
"ds_write2st64_b64 v255, v[176:177], v[178:179] offset0:96 offset1:97\n"
"v_mfma_f32_16x16x32_bf16 v[80:83], a[224:227], a[176:179], v[80:83]\n"
"s_add_u32 s40, s56, s52\n"
"v_permlane16_swap_b32 v160, v162\n"
"v_mfma_f32_16x16x32_bf16 v[80:83], a[228:231], a[180:183], v[80:83]\n"
"buffer_load_dwordx4 v248, s[16:19], s40 offen offset:1024 nt lds\n"
"v_mfma_f32_16x16x32_bf16 v[64:67], a[224:227], a[168:171], v[64:67]\n"
"v_cvt_pk_bf16_f32 v218, v220, v221\n"
"v_cvt_pk_bf16_f32 v154, v156, v157\n"
"v_mfma_f32_16x16x32_bf16 v[64:67], a[228:231], a[172:175], v[64:67]\n"
"v_permlane16_swap_b32 v128, v130\n"
"v_mfma_f32_16x16x32_bf16 v[68:71], a[232:235], a[168:171], v[68:71]\n"
"ds_read_b128 v[212:215], v197 offset:0\n"
"v_cvt_pk_bf16_f32 v155, v158, v159\n"
"v_mfma_f32_16x16x32_bf16 v[68:71], a[236:239], a[172:175], v[68:71]\n"
"v_pk_fma_f32 v[96:97], v[96:97], s[70:71], s[70:71] op_sel:[0,0,1] op_sel_hi:[1,0,1]\nv_pk_fma_f32 v[98:99], v[98:99], s[70:71], s[70:71] op_sel:[0,0,1] op_sel_hi:[1,0,1]\nv_pk_fma_f32 v[100:101], v[100:101], s[70:71], s[70:71] op_sel:[0,0,1] op_sel_hi:[1,0,1]\nv_pk_fma_f32 v[102:103], v[102:103], s[70:71], s[70:71] op_sel:[0,0,1] op_sel_hi:[1,0,1]\nv_pk_fma_f32 v[104:105], v[104:105], s[70:71], s[70:71] op_sel:[0,0,1] op_sel_hi:[1,0,1]\nv_pk_fma_f32 v[106:107], v[106:107], s[70:71], s[70:71] op_sel:[0,0,1] op_sel_hi:[1,0,1]\nv_pk_fma_f32 v[108:109], v[108:109], s[70:71], s[70:71] op_sel:[0,0,1] op_sel_hi:[1,0,1]\nv_pk_fma_f32 v[110:111], v[110:111], s[70:71], s[70:71] op_sel:[0,0,1] op_sel_hi:[1,0,1]\nv_pk_fma_f32 v[80:81], v[80:81], s[70:71], s[70:71] op_sel:[0,0,1] op_sel_hi:[1,0,1]\nv_pk_fma_f32 v[82:83], v[82:83], s[70:71], s[70:71] op_sel:[0,0,1] op_sel_hi:[1,0,1]\nv_pk_fma_f32 v[84:85], v[84:85], s[70:71], s[70:71] op_sel:[0,0,1] op_sel_hi:[1,0,1]\nv_pk_fma_f32 v[86:87], v[86:87], s[70:71], s[70:71] op_sel:[0,0,1] op_sel_hi:[1,0,1]\nv_pk_fma_f32 v[88:89], v[88:89], s[70:71], s[70:71] op_sel:[0,0,1] op_sel_hi:[1,0,1]\nv_pk_fma_f32 v[90:91], v[90:91], s[70:71], s[70:71] op_sel:[0,0,1] op_sel_hi:[1,0,1]\nv_pk_fma_f32 v[92:93], v[92:93], s[70:71], s[70:71] op_sel:[0,0,1] op_sel_hi:[1,0,1]\nv_pk_fma_f32 v[94:95], v[94:95], s[70:71], s[70:71] op_sel:[0,0,1] op_sel_hi:[1,0,1]\n"
"v_mfma_f32_16x16x32_bf16 v[72:75], a[240:243], a[168:171], v[72:75]\n"
"v_permlane16_swap_b32 v137, v139\n"
"v_mfma_f32_16x16x32_bf16 v[72:75], a[244:247], a[172:175], v[72:75]\n"
"ds_write2st64_b64 v255, v[184:185], v[186:187] offset0:98 offset1:99\n"
"v_mfma_f32_16x16x32_bf16 v[76:79], a[248:251], a[168:171], v[76:79]\n"
"v_cvt_pk_bf16_f32 v121, v122, v123\n"
"v_cvt_pk_bf16_f32 v96, v96, v97\n"
"v_mfma_f32_16x16x32_bf16 v[76:79], a[252:255], a[172:175], v[76:79]\n"
"ds_read_b128 a[192:195], v243 offset:0\n"
"v_mov_b32 v199, 0\n"
"v_mfma_f32_16x16x32_bf16 v[60:63], a[248:251], a[160:163], v[60:63]\n"
"v_cvt_pk_bf16_f32 v104, v104, v105\n"
"v_cvt_pk_bf16_f32 v105, v106, v107\n"
"v_mfma_f32_16x16x32_bf16 v[60:63], a[252:255], a[164:167], v[60:63]\n"
"ds_write2st64_b64 v255, v[208:209], v[210:211] offset0:192 offset1:193\n"
"v_mfma_f32_16x16x32_bf16 v[56:59], a[240:243], a[160:163], v[56:59]\n"
"s_add_u32 s40, s56, s53\n"
"v_accvgpr_write_b32 a121, v137\n"
"v_cvt_pk_bf16_f32 v146, v148, v149\n"
"v_mfma_f32_16x16x32_bf16 v[56:59], a[244:247], a[164:167], v[56:59]\n"
"buffer_load_dwordx4 v240, s[16:19], s40 offen offset:2048 nt lds\n"
"v_mfma_f32_16x16x32_bf16 v[52:55], a[232:235], a[160:163], v[52:55]\n"
"v_cvt_pk_bf16_f32 v97, v98, v99\n"
"v_cvt_pk_bf16_f32 v88, v88, v89\n"
"v_mfma_f32_16x16x32_bf16 v[52:55], a[236:239], a[164:167], v[52:55]\n"
"v_cvt_pk_bf16_f32 v106, v108, v109\n"
"v_mov_b32 v204, 0\n"
"v_mfma_f32_16x16x32_bf16 v[48:51], a[224:227], a[160:163], v[48:51]\n"
"v_cvt_pk_bf16_f32 v219, v222, v223\n"
"v_mov_b32 v205, 0\n"
"v_mfma_f32_16x16x32_bf16 v[48:51], a[228:231], a[164:167], v[48:51]\n"
"ds_write_b128 v243, v[160:163] offset:8192\n"
"v_cvt_pk_bf16_f32 v170, v172, v173\n"
"v_mfma_f32_16x16x32_bf16 v[0:3], a[224:227], a[136:139], v[0:3]\n"
"v_permlane16_swap_b32 v153, v155\n"
"v_mfma_f32_16x16x32_bf16 v[0:3], a[228:231], a[140:143], v[0:3]\n"
"ds_write2st64_b64 v255, v[216:217], v[218:219] offset0:194 offset1:195\n"
"v_mfma_f32_16x16x32_bf16 v[4:7], a[232:235], a[136:139], v[4:7]\n"
"v_permlane16_swap_b32 v169, v171\n"
"v_mfma_f32_16x16x32_bf16 v[4:7], a[236:239], a[140:143], v[4:7]\n"
"v_pk_fma_f32 v[64:65], v[64:65], s[70:71], s[70:71] op_sel:[0,0,1] op_sel_hi:[1,0,1]\nv_pk_fma_f32 v[66:67], v[66:67], s[70:71], s[70:71] op_sel:[0,0,1] op_sel_hi:[1,0,1]\nv_pk_fma_f32 v[68:69], v[68:69], s[70:71], s[70:71] op_sel:[0,0,1] op_sel_hi:[1,0,1]\nv_pk_fma_f32 v[70:71], v[70:71], s[70:71], s[70:71] op_sel:[0,0,1] op_sel_hi:[1,0,1]\nv_pk_fma_f32 v[72:73], v[72:73], s[70:71], s[70:71] op_sel:[0,0,1] op_sel_hi:[1,0,1]\nv_pk_fma_f32 v[74:75], v[74:75], s[70:71], s[70:71] op_sel:[0,0,1] op_sel_hi:[1,0,1]\nv_pk_fma_f32 v[76:77], v[76:77], s[70:71], s[70:71] op_sel:[0,0,1] op_sel_hi:[1,0,1]\nv_pk_fma_f32 v[78:79], v[78:79], s[70:71], s[70:71] op_sel:[0,0,1] op_sel_hi:[1,0,1]\nv_pk_fma_f32 v[48:49], v[48:49], s[70:71], s[70:71] op_sel:[0,0,1] op_sel_hi:[1,0,1]\nv_pk_fma_f32 v[50:51], v[50:51], s[70:71], s[70:71] op_sel:[0,0,1] op_sel_hi:[1,0,1]\nv_pk_fma_f32 v[52:53], v[52:53], s[70:71], s[70:71] op_sel:[0,0,1] op_sel_hi:[1,0,1]\nv_pk_fma_f32 v[54:55], v[54:55], s[70:71], s[70:71] op_sel:[0,0,1] op_sel_hi:[1,0,1]\nv_pk_fma_f32 v[56:57], v[56:57], s[70:71], s[70:71] op_sel:[0,0,1] op_sel_hi:[1,0,1]\nv_pk_fma_f32 v[58:59], v[58:59], s[70:71], s[70:71] op_sel:[0,0,1] op_sel_hi:[1,0,1]\nv_pk_fma_f32 v[60:61], v[60:61], s[70:71], s[70:71] op_sel:[0,0,1] op_sel_hi:[1,0,1]\nv_pk_fma_f32 v[62:63], v[62:63], s[70:71], s[70:71] op_sel:[0,0,1] op_sel_hi:[1,0,1]\n"
"v_mfma_f32_16x16x32_bf16 v[8:11], a[240:243], a[136:139], v[8:11]\n"
"ds_read_b128 a[204:207], v198 offset:2048\n"
"v_cvt_pk_bf16_f32 v56, v56, v57\n"
"v_mfma_f32_16x16x32_bf16 v[8:11], a[244:247], a[140:143], v[8:11]\n"
"v_permlane16_swap_b32 v152, v154\n"
"v_mfma_f32_16x16x32_bf16 v[12:15], a[248:251], a[136:139], v[12:15]\n"
"v_permlane16_swap_b32 v144, v146\n"
"v_mfma_f32_16x16x32_bf16 v[12:15], a[252:255], a[140:143], v[12:15]\n"
"v_accvgpr_write_b32 a137, v153\n"
"v_cvt_pk_bf16_f32 v80, v80, v81\n"
"v_mfma_f32_16x16x32_bf16 v[28:31], a[248:251], a[144:147], v[28:31]\n"
"s_add_u32 s40, s56, s54\n"
"ds_read_b128 v[208:211], v196 offset:0\n"
"v_cvt_pk_bf16_f32 v147, v150, v151\n"
"v_mfma_f32_16x16x32_bf16 v[28:31], a[252:255], a[148:151], v[28:31]\n"
"buffer_load_dwordx4 v248, s[16:19], s40 offen offset:3072 nt lds\n"
"v_mfma_f32_16x16x32_bf16 v[24:27], a[240:243], a[144:147], v[24:27]\n"
"v_cvt_pk_bf16_f32 v107, v110, v111\n"
"v_cvt_pk_bf16_f32 v48, v48, v49\n"
"v_mfma_f32_16x16x32_bf16 v[24:27], a[244:247], a[148:151], v[24:27]\n"
"v_permlane16_swap_b32 v129, v131\n"
"v_mfma_f32_16x16x32_bf16 v[20:23], a[232:235], a[144:147], v[20:23]\n"
"v_permlane16_swap_b32 v136, v138\n"
"v_mfma_f32_16x16x32_bf16 v[20:23], a[236:239], a[148:151], v[20:23]\n"
"ds_read_b128 v[216:219], v196 offset:512\n"
"v_cvt_pk_bf16_f32 v64, v64, v65\n"
"v_mfma_f32_16x16x32_bf16 v[16:19], a[224:227], a[144:147], v[16:19]\n"
"v_cvt_pk_bf16_f32 v122, v124, v125\n"
"v_cvt_pk_bf16_f32 v72, v72, v73\n"
"v_mfma_f32_16x16x32_bf16 v[16:19], a[228:231], a[148:151], v[16:19]\n"
"v_accvgpr_write_b32 a113, v129\n"
"v_accvgpr_write_b32 a122, v138\n"
"v_mfma_f32_16x16x32_bf16 v[32:35], a[224:227], a[152:155], v[32:35]\n"
"v_cvt_pk_bf16_f32 v98, v100, v101\n"
"v_cvt_pk_bf16_f32 v65, v66, v67\n"
"v_mfma_f32_16x16x32_bf16 v[32:35], a[228:231], a[156:159], v[32:35]\n"
"ds_read_b128 v[220:223], v197 offset:512\n"
"v_accvgpr_write_b32 a138, v154\n"
"v_mfma_f32_16x16x32_bf16 v[36:39], a[232:235], a[152:155], v[36:39]\n"
"v_accvgpr_write_b32 a136, v152\n"
"v_cvt_pk_bf16_f32 v89, v90, v91\n"
"v_mfma_f32_16x16x32_bf16 v[36:39], a[236:239], a[156:159], v[36:39]\n"
"v_permlane16_swap_b32 v145, v147\n"
"v_mfma_f32_16x16x32_bf16 v[40:43], a[240:243], a[152:155], v[40:43]\n"
"v_accvgpr_write_b32 a128, v144\n"
"v_cvt_pk_bf16_f32 v99, v102, v103\n"
"v_mfma_f32_16x16x32_bf16 v[40:43], a[244:247], a[156:159], v[40:43]\n"
"ds_write2st64_b64 v255, v[192:193], v[194:195] offset0:100 offset1:101\n"
"v_pk_fma_f32 v[0:1], v[0:1], s[70:71], s[70:71] op_sel:[0,0,1] op_sel_hi:[1,0,1]\nv_pk_fma_f32 v[2:3], v[2:3], s[70:71], s[70:71] op_sel:[0,0,1] op_sel_hi:[1,0,1]\nv_pk_fma_f32 v[4:5], v[4:5], s[70:71], s[70:71] op_sel:[0,0,1] op_sel_hi:[1,0,1]\nv_pk_fma_f32 v[6:7], v[6:7], s[70:71], s[70:71] op_sel:[0,0,1] op_sel_hi:[1,0,1]\nv_pk_fma_f32 v[8:9], v[8:9], s[70:71], s[70:71] op_sel:[0,0,1] op_sel_hi:[1,0,1]\nv_pk_fma_f32 v[10:11], v[10:11], s[70:71], s[70:71] op_sel:[0,0,1] op_sel_hi:[1,0,1]\nv_pk_fma_f32 v[12:13], v[12:13], s[70:71], s[70:71] op_sel:[0,0,1] op_sel_hi:[1,0,1]\nv_pk_fma_f32 v[14:15], v[14:15], s[70:71], s[70:71] op_sel:[0,0,1] op_sel_hi:[1,0,1]\nv_pk_fma_f32 v[16:17], v[16:17], s[70:71], s[70:71] op_sel:[0,0,1] op_sel_hi:[1,0,1]\nv_pk_fma_f32 v[18:19], v[18:19], s[70:71], s[70:71] op_sel:[0,0,1] op_sel_hi:[1,0,1]\nv_pk_fma_f32 v[20:21], v[20:21], s[70:71], s[70:71] op_sel:[0,0,1] op_sel_hi:[1,0,1]\nv_pk_fma_f32 v[22:23], v[22:23], s[70:71], s[70:71] op_sel:[0,0,1] op_sel_hi:[1,0,1]\nv_pk_fma_f32 v[24:25], v[24:25], s[70:71], s[70:71] op_sel:[0,0,1] op_sel_hi:[1,0,1]\nv_pk_fma_f32 v[26:27], v[26:27], s[70:71], s[70:71] op_sel:[0,0,1] op_sel_hi:[1,0,1]\nv_pk_fma_f32 v[28:29], v[28:29], s[70:71], s[70:71] op_sel:[0,0,1] op_sel_hi:[1,0,1]\nv_pk_fma_f32 v[30:31], v[30:31], s[70:71], s[70:71] op_sel:[0,0,1] op_sel_hi:[1,0,1]\n"
"v_mfma_f32_16x16x32_bf16 v[44:47], a[248:251], a[152:155], v[44:47]\n"
"v_accvgpr_write_b32 a115, v131\n"
"v_accvgpr_write_b32 a120, v136\n"
"v_mfma_f32_16x16x32_bf16 v[44:47], a[252:255], a[156:159], v[44:47]\n"
"s_waitcnt lgkmcnt(2)\n"
"ds_write2st64_b64 v255, v[96:97], v[98:99] offset0:228 offset1:229\n"
"v_cvt_pk_bf16_f32 v81, v82, v83\n"
"v_mfma_f32_16x16x32_bf16 v[180:183], v[208:211], a[0:3], 0\n"
"v_accvgpr_write_b32 a112, v128\n"
"v_cvt_pk_bf16_f32 v91, v94, v95\n"
"v_mfma_f32_16x16x32_bf16 v[180:183], v[212:215], a[4:7], v[180:183]\n"
"v_permlane16_swap_b32 v168, v170\n"
"v_mfma_f32_16x16x32_bf16 v[180:183], v[216:219], a[8:11], v[180:183]\n"
"s_waitcnt lgkmcnt(2)\n"
"v_cvt_pk_bf16_f32 v73, v74, v75\n"
"v_cvt_pk_bf16_f32 v8, v8, v9\n"
"v_mfma_f32_16x16x32_bf16 v[180:183], v[220:223], a[12:15], v[180:183]\n"
"ds_write_b128 v243, v[168:171] offset:10240\n"
"v_pk_fma_f32 v[32:33], v[32:33], s[70:71], s[70:71] op_sel:[0,0,1] op_sel_hi:[1,0,1]\nv_pk_fma_f32 v[34:35], v[34:35], s[70:71], s[70:71] op_sel:[0,0,1] op_sel_hi:[1,0,1]\nv_pk_fma_f32 v[36:37], v[36:37], s[70:71], s[70:71] op_sel:[0,0,1] op_sel_hi:[1,0,1]\nv_pk_fma_f32 v[38:39], v[38:39], s[70:71], s[70:71] op_sel:[0,0,1] op_sel_hi:[1,0,1]\nv_pk_fma_f32 v[40:41], v[40:41], s[70:71], s[70:71] op_sel:[0,0,1] op_sel_hi:[1,0,1]\nv_pk_fma_f32 v[42:43], v[42:43], s[70:71], s[70:71] op_sel:[0,0,1] op_sel_hi:[1,0,1]\nv_pk_fma_f32 v[44:45], v[44:45], s[70:71], s[70:71] op_sel:[0,0,1] op_sel_hi:[1,0,1]\nv_pk_fma_f32 v[46:47], v[46:47], s[70:71], s[70:71] op_sel:[0,0,1] op_sel_hi:[1,0,1]\n"
"v_mfma_f32_16x16x32_bf16 v[184:187], v[208:211], a[16:19], 0\n"
"v_cvt_pk_bf16_f32 v49, v50, v51\n"
"v_cvt_pk_bf16_f32 v51, v54, v55\n"
"v_mfma_f32_16x16x32_bf16 v[184:187], v[212:215], a[20:23], v[184:187]\n"
"v_permlane16_swap_b32 v120, v122\n"
"v_mfma_f32_16x16x32_bf16 v[184:187], v[216:219], a[24:27], v[184:187]\n"
"v_cvt_pk_bf16_f32 v90, v92, v93\n"
"v_sub_f32 v183, s74, v183\n"
"v_mfma_f32_16x16x32_bf16 v[184:187], v[220:223], a[28:31], v[184:187]\n"
"ds_write2st64_b64 v255, v[104:105], v[106:107] offset0:230 offset1:231\n"
"v_cvt_pk_bf16_f32 v67, v70, v71\n"
"v_sub_f32 v182, s74, v182\n"
"v_mfma_f32_16x16x32_bf16 v[188:191], v[208:211], a[32:35], 0\n"
"v_cvt_pk_bf16_f32 v40, v40, v41\n"
"v_sub_f32 v180, s74, v180\n"
"v_mfma_f32_16x16x32_bf16 v[188:191], v[212:215], a[36:39], v[188:191]\n"
"v_accvgpr_write_b32 a139, v155\n"
"v_sub_f32 v181, s74, v181\n"
"v_mfma_f32_16x16x32_bf16 v[188:191], v[216:219], a[40:43], v[188:191]\n"
"v_cvt_pk_bf16_f32 v9, v10, v11\n"
"v_sub_f32 v186, s74, v186\n"
"v_mfma_f32_16x16x32_bf16 v[188:191], v[220:223], a[44:47], v[188:191]\n"
"v_cvt_pk_bf16_f32 v83, v86, v87\n"
"v_cvt_pk_bf16_f32 v57, v58, v59\n"
"v_mfma_f32_16x16x32_bf16 v[192:195], v[208:211], a[192:195], 0\n"
"ds_read_b128 v[208:211], v228 offset:0\n"
"v_sub_f32 v185, s74, v185\n"
"v_mfma_f32_16x16x32_bf16 v[192:195], v[212:215], a[196:199], v[192:195]\n"
"ds_read_b128 v[212:215], v229 offset:0\n"
"v_cvt_pk_bf16_f32 v41, v42, v43\n"
"v_mfma_f32_16x16x32_bf16 v[192:195], v[216:219], a[200:203], v[192:195]\n"
"ds_read_b128 v[216:219], v228 offset:512\n"
"v_sub_f32 v189, s74, v189\n"
"v_mfma_f32_16x16x32_bf16 v[192:195], v[220:223], a[204:207], v[192:195]\n"
"s_waitcnt lgkmcnt(2)\n"
"ds_read_b128 v[220:223], v229 offset:512\n"
"v_sub_f32 v184, s74, v184\n"
"v_mfma_f32_16x16x32_bf16 v[152:155], v[208:211], a[0:3], v[180:183]\n"
"s_waitcnt lgkmcnt(2)\n"
"v_cvt_pk_bf16_f32 v42, v44, v45\n"
"v_sub_f32 v187, s74, v187\n"
"v_mfma_f32_16x16x32_bf16 v[152:155], v[212:215], a[4:7], v[152:155]\n"
"s_waitcnt lgkmcnt(1)\n"
"v_accvgpr_write_b32 a129, v145\n"
"v_cvt_pk_bf16_f32 v82, v84, v85\n"
"v_mfma_f32_16x16x32_bf16 v[152:155], v[216:219], a[8:11], v[152:155]\n"
"s_waitcnt lgkmcnt(0)\n"
"v_cvt_pk_bf16_f32 v58, v60, v61\n"
"v_sub_f32 v188, s74, v188\n"
"v_mfma_f32_16x16x32_bf16 v[152:155], v[220:223], a[12:15], v[152:155]\n"
"v_cvt_pk_bf16_f32 v59, v62, v63\n"
"v_sub_f32 v190, s74, v190\n"
"v_mfma_f32_16x16x32_bf16 v[156:159], v[216:219], a[24:27], v[184:187]\n"
"v_cvt_pk_bf16_f32 v11, v14, v15\n"
"v_cvt_pk_bf16_f32 v43, v46, v47\n"
"v_mfma_f32_16x16x32_bf16 v[156:159], v[220:223], a[28:31], v[156:159]\n"
"v_sub_f32 v191, s74, v191\n"
"v_sub_f32 v192, s74, v192\n"
"v_mfma_f32_16x16x32_bf16 v[156:159], v[208:211], a[16:19], v[156:159]\n"
"v_cvt_pk_bf16_f32 v123, v126, v127\n"
"v_cvt_pk_bf16_f32 v10, v12, v13\n"
"v_mfma_f32_16x16x32_bf16 v[156:159], v[212:215], a[20:23], v[156:159]\n"
"ds_write2st64_b64 v255, v[88:89], v[90:91] offset0:226 offset1:227\n"
"v_permlane16_swap_b32 v57, v59\n"
"v_cvt_pk_bf16_f32 v16, v16, v17\n"
"v_accvgpr_write_b32 a241, v254\n"
"v_mov_b32 v196, 0\n"
"v_sub_f32 v193, s74, v193\n"
"v_exp_f32 v153, v153\n"
"v_mfma_f32_16x16x32_bf16 v[160:163], v[208:211], a[32:35], v[188:191]\n"
"v_sub_f32 v194, s74, v194\n"
"v_sub_f32 v195, s74, v195\n"
"v_mfma_f32_16x16x32_bf16 v[160:163], v[212:215], a[36:39], v[160:163]\n"
"v_exp_f32 v152, v152\n"
"v_mfma_f32_16x16x32_bf16 v[160:163], v[216:219], a[40:43], v[160:163]\n"
"v_exp_f32 v158, v158\n"
"v_mfma_f32_16x16x32_bf16 v[160:163], v[220:223], a[44:47], v[160:163]\n"
"v_exp_f32 v156, v156\n"
"v_mfma_f32_16x16x32_bf16 v[164:167], v[216:219], a[200:203], v[192:195]\n"
"ds_read_b128 v[216:219], v228 offset:4608\n"
"v_cvt_pk_bf16_f32 v74, v76, v77\n"
"v_mfma_f32_16x16x32_bf16 v[164:167], v[220:223], a[204:207], v[164:167]\n"
"ds_read_b128 v[220:223], v229 offset:4608\n"
"v_cvt_pk_bf16_f32 v66, v68, v69\n"
"v_mfma_f32_16x16x32_bf16 v[164:167], v[208:211], a[192:195], v[164:167]\n"
"ds_read_b128 v[208:211], v228 offset:4096\n"
"v_cvt_pk_bf16_f32 v24, v24, v25\n"
"v_mfma_f32_16x16x32_bf16 v[164:167], v[212:215], a[196:199], v[164:167]\n"
"s_waitcnt lgkmcnt(2)\n"
"ds_read_b128 v[212:215], v229 offset:4096\n"
"v_accvgpr_write_b32 a114, v130\n"
"v_mfma_f32_16x16x32_bf16 v[168:171], v[216:219], a[8:11], v[180:183]\n"
"s_waitcnt lgkmcnt(2)\n"
"v_exp_f32 v163, v163\n"
"v_mfma_f32_16x16x32_bf16 v[168:171], v[220:223], a[12:15], v[168:171]\n"
"s_waitcnt lgkmcnt(1)\n"
"v_exp_f32 v162, v162\n"
"v_mfma_f32_16x16x32_bf16 v[168:171], v[208:211], a[0:3], v[168:171]\n"
"s_waitcnt lgkmcnt(0)\n"
"v_exp_f32 v160, v160\n"
"v_mfma_f32_16x16x32_bf16 v[168:171], v[212:215], a[4:7], v[168:171]\n"
"ds_write2st64_b64 v255, v[80:81], v[82:83] offset0:224 offset1:225\n"
"v_permlane16_swap_b32 v121, v123\n"
"v_accvgpr_write_b32 a131, v147\n"
"v_exp_f32 v154, v154\n"
"v_exp_f32 v157, v157\n"
"v_exp_f32 v159, v159\n"
"v_exp_f32 v161, v161\n"
"v_exp_f32 v167, v167\n"
"v_mfma_f32_16x16x32_bf16 v[172:175], v[208:211], a[16:19], v[184:187]\n"
"v_exp_f32 v165, v165\n"
"v_mfma_f32_16x16x32_bf16 v[172:175], v[212:215], a[20:23], v[172:175]\n"
"v_exp_f32 v166, v166\n"
"v_mfma_f32_16x16x32_bf16 v[172:175], v[216:219], a[24:27], v[172:175]\n"
"v_exp_f32 v169, v169\n"
"v_mfma_f32_16x16x32_bf16 v[172:175], v[220:223], a[28:31], v[172:175]\n"
"v_exp_f32 v155, v155\n"
"v_mfma_f32_16x16x32_bf16 v[176:179], v[216:219], a[40:43], v[188:191]\n"
"v_cvt_pk_bf16_f32 v128, v152, v153\n"
"v_bfe_u32 v248, v242, 9, 1\n"
"v_mfma_f32_16x16x32_bf16 v[176:179], v[220:223], a[44:47], v[176:179]\n"
"v_exp_f32 v171, v171\n"
"v_mfma_f32_16x16x32_bf16 v[176:179], v[208:211], a[32:35], v[176:179]\n"
"v_cvt_pk_bf16_f32 v129, v154, v155\n"
"v_and_b32 v249, 0xf0, v242\n"
"v_mfma_f32_16x16x32_bf16 v[176:179], v[212:215], a[36:39], v[176:179]\n"
"ds_write2st64_b64 v255, v[64:65], v[66:67] offset0:196 offset1:197\n"
"v_cvt_pk_bf16_f32 v136, v156, v157\n"
"v_cvt_pk_bf16_f32 v137, v158, v159\n"
"v_cvt_pk_bf16_f32 v145, v162, v163\n"
"v_exp_f32 v170, v170\n"
"v_mfma_f32_16x16x32_bf16 v[152:155], v[208:211], a[192:195], v[192:195]\n"
"ds_read_b128 v[208:211], v230 offset:0\n"
"v_cvt_pk_bf16_f32 v125, v166, v167\n"
"v_mfma_f32_16x16x32_bf16 v[152:155], v[212:215], a[196:199], v[152:155]\n"
"ds_read_b128 v[212:215], v231 offset:0\n"
"v_accvgpr_write_b32 a123, v139\n"
"v_mfma_f32_16x16x32_bf16 v[152:155], v[216:219], a[200:203], v[152:155]\n"
"ds_read_b128 v[216:219], v230 offset:512\n"
"v_cvt_pk_bf16_f32 v50, v52, v53\n"
"v_mfma_f32_16x16x32_bf16 v[152:155], v[220:223], a[204:207], v[152:155]\n"
"s_waitcnt lgkmcnt(2)\n"
"ds_read_b128 v[220:223], v231 offset:512\n"
"v_permlane16_swap_b32 v40, v42\n"
"v_cvt_pk_bf16_f32 v144, v160, v161\n"
"v_exp_f32 v164, v164\n"
"v_exp_f32 v168, v168\n"
"v_cvt_pk_bf16_f32 v131, v170, v171\n"
"v_exp_f32 v172, v172\n"
"v_exp_f32 v173, v173\n"
"v_exp_f32 v174, v174\n"
"v_exp_f32 v176, v176\n"
"v_exp_f32 v179, v179\n"
"v_lshl_add_u32 v248, v248, 8, v249\n"
"v_mfma_f32_16x16x32_bf16 v[156:159], v[208:211], a[0:3], v[180:183]\n"
"s_waitcnt lgkmcnt(2)\n"
"v_cvt_pk_bf16_f32 v75, v78, v79\n"
"v_cvt_pk_bf16_f32 v130, v168, v169\n"
"v_mfma_f32_16x16x32_bf16 v[156:159], v[212:215], a[4:7], v[156:159]\n"
"s_waitcnt lgkmcnt(1)\n"
"v_bfe_u32 v249, v242, 8, 1\n"
"v_lshl_add_u32 v248, v249, 9, v248\n"
"v_mfma_f32_16x16x32_bf16 v[156:159], v[216:219], a[8:11], v[156:159]\n"
"s_waitcnt lgkmcnt(0)\n"
"v_exp_f32 v177, v177\n"
"v_mfma_f32_16x16x32_bf16 v[156:159], v[220:223], a[12:15], v[156:159]\n"
"v_mov_b32 v202, 0\n"
"v_cvt_pk_bf16_f32 v124, v164, v165\n"
"v_mfma_f32_16x16x32_bf16 v[160:163], v[216:219], a[24:27], v[184:187]\n"
"v_exp_f32 v155, v155\n"
"v_mfma_f32_16x16x32_bf16 v[160:163], v[220:223], a[28:31], v[160:163]\n"
"v_permlane16_swap_b32 v49, v51\n"
"v_mfma_f32_16x16x32_bf16 v[160:163], v[208:211], a[16:19], v[160:163]\n"
"v_mov_b32 v201, 0\n"
"v_xor_b32_e64 v249, s42, 0\n"
"v_mfma_f32_16x16x32_bf16 v[160:163], v[212:215], a[20:23], v[160:163]\n"
"ds_write2st64_b64 v255, v[72:73], v[74:75] offset0:198 offset1:199\n"
"v_permlane16_swap_b32 v48, v50\n"
"v_permlane16_swap_b32 v56, v58\n"
"v_cvt_pk_bf16_f32 v25, v26, v27\n"
"v_cvt_pk_bf16_f32 v138, v172, v173\n"
"v_exp_f32 v153, v153\n"
"v_exp_f32 v157, v157\n"
"v_mfma_f32_16x16x32_bf16 v[164:167], v[208:211], a[32:35], v[188:191]\n"
"ds_read_b128 v[208:211], v230 offset:4096\n"
"v_mov_b32 v197, 0\n"
"v_mfma_f32_16x16x32_bf16 v[164:167], v[212:215], a[36:39], v[164:167]\n"
"ds_read_b128 v[212:215], v231 offset:4096\n"
"v_accvgpr_write_b32 a243, v254\n"
"v_mfma_f32_16x16x32_bf16 v[164:167], v[216:219], a[40:43], v[164:167]\n"
"ds_read_b128 v[216:219], v230 offset:4608\n"
"v_lshl_add_u32 v248, v249, 12, v248\n"
"v_mfma_f32_16x16x32_bf16 v[164:167], v[220:223], a[44:47], v[164:167]\n"
"s_waitcnt lgkmcnt(2)\n"
"ds_read_b128 v[220:223], v231 offset:4608\n"
"v_accvgpr_write_b32 a104, v120\n"
"v_permlane16_swap_b32 v8, v10\n"
"v_permlane16_swap_b32 v9, v11\n"
"v_accvgpr_write_b32 a240, v254\n"
"v_exp_f32 v175, v175\n"
"v_exp_f32 v178, v178\n"
"v_exp_f32 v159, v159\n"
"v_exp_f32 v160, v160\n"
"v_exp_f32 v163, v163\n"
"v_mfma_f32_16x16x32_bf16 v[168:171], v[208:211], a[0:3], v[180:183]\n"
"s_waitcnt lgkmcnt(2)\n"
"v_exp_f32 v154, v154\n"
"v_mfma_f32_16x16x32_bf16 v[168:171], v[212:215], a[4:7], v[168:171]\n"
"s_waitcnt lgkmcnt(1)\n"
"v_accvgpr_write_b32 a186, v42\n"
"v_add_u32 v249, s75, v248\n"
"v_mfma_f32_16x16x32_bf16 v[168:171], v[216:219], a[8:11], v[168:171]\n"
"s_waitcnt lgkmcnt(0)\n"
"v_exp_f32 v152, v152\n"
"v_mfma_f32_16x16x32_bf16 v[168:171], v[220:223], a[12:15], v[168:171]\n"
"ds_write_b128 v243, v[48:51] offset:12288\n"
"v_accvgpr_write_b32 a130, v146\n"
"v_permlane16_swap_b32 v41, v43\n"
"v_cvt_pk_bf16_f32 v139, v174, v175\n"
"v_exp_f32 v164, v164\n"
"v_mfma_f32_16x16x32_bf16 v[172:175], v[216:219], a[24:27], v[184:187]\n"
"v_exp_f32 v156, v156\n"
"v_mfma_f32_16x16x32_bf16 v[172:175], v[220:223], a[28:31], v[172:175]\n"
"v_exp_f32 v158, v158\n"
"v_mfma_f32_16x16x32_bf16 v[172:175], v[208:211], a[16:19], v[172:175]\n"
"v_exp_f32 v168, v168\n"
"v_mfma_f32_16x16x32_bf16 v[172:175], v[212:215], a[20:23], v[172:175]\n"
"s_cbranch_vccnz LPCE0_%=\nLPKE0_%=:\n"
"ds_write_b128 v243, v[56:59] offset:14336\n"
"v_accvgpr_write_b32 a185, v41\n"
"v_cvt_pk_bf16_f32 v146, v176, v177\n"
"v_cvt_pk_bf16_f32 v147, v178, v179\n"
"v_cvt_pk_bf16_f32 v126, v152, v153\n"
"v_cvt_pk_bf16_f32 v133, v158, v159\n"
"v_exp_f32 v165, v165\n"
"v_mfma_f32_16x16x32_bf16 v[176:179], v[208:211], a[32:35], v[188:191]\n"
"v_cvt_pk_bf16_f32 v127, v154, v155\n"
"v_cvt_pk_bf16_f32 v132, v156, v157\n"
"v_mfma_f32_16x16x32_bf16 v[176:179], v[212:215], a[36:39], v[176:179]\n"
"v_exp_f32 v161, v161\n"
"v_mfma_f32_16x16x32_bf16 v[176:179], v[216:219], a[40:43], v[176:179]\n"
"v_cvt_pk_bf16_f32 v17, v18, v19\n"
"v_accvgpr_write_b32 a187, v43\n"
"v_mfma_f32_16x16x32_bf16 v[176:179], v[220:223], a[44:47], v[176:179]\n"
"v_cvt_pk_bf16_f32 v140, v160, v161\n"
"v_exp_f32 v162, v162\n"
"v_exp_f32 v167, v167\n"
"v_exp_f32 v169, v169\n"
"v_exp_f32 v172, v172\n"
"v_exp_f32 v173, v173\n"
"v_exp_f32 v174, v174\n"
"s_waitcnt vmcnt(8) lgkmcnt(0)\n"
"s_barrier\n"
"ds_read_b128 v[208:211], v228 offset:16384\n"
"ds_read_b128 v[212:215], v229 offset:16384\n"
"ds_read_b128 v[216:219], v228 offset:16896\n"
"ds_read_b128 v[220:223], v229 offset:16896\n"
"s_waitcnt lgkmcnt(3)\n"
"v_mfma_f32_16x16x32_bf16 v[152:155], v[208:211], a[0:3], v[180:183]\n"
"v_accvgpr_write_b32 a153, v9\n"
"v_cvt_pk_bf16_f32 v32, v32, v33\n"
"s_waitcnt lgkmcnt(2)\n"
"v_mfma_f32_16x16x32_bf16 v[152:155], v[212:215], a[4:7], v[152:155]\n"
"ds_read_b64_tr_b16 v[224:225], v239 offset:0\n"
"v_cvt_pk_bf16_f32 v141, v162, v163\n"
"s_waitcnt lgkmcnt(2)\n"
"v_mfma_f32_16x16x32_bf16 v[152:155], v[216:219], a[8:11], v[152:155]\n"
"s_waitcnt lgkmcnt(1)\n"
"ds_read_b128 a[188:191], v249 offset:52224\n"
"v_mov_b32 v203, 0\n"
"v_mfma_f32_16x16x32_bf16 v[152:155], v[220:223], a[12:15], v[152:155]\n"
"ds_read_b128 a[116:119], v248 offset:51200\n"
"v_cvt_pk_bf16_f32 v27, v30, v31\n"
"v_mfma_f32_16x16x32_bf16 v[156:159], v[216:219], a[24:27], v[184:187]\n"
"ds_read_b64_tr_b16 v[226:227], v239 offset:4096\n"
"ds_read_b128 a[180:183], v249 offset:51200\n"
"v_mfma_f32_16x16x32_bf16 v[156:159], v[220:223], a[28:31], v[156:159]\n"
"v_accvgpr_write_b32 a154, v10\n"
"v_accvgpr_write_b32 a184, v40\n"
"v_mfma_f32_16x16x32_bf16 v[156:159], v[208:211], a[16:19], v[156:159]\n"
"ds_read_b64_tr_b16 a[244:245], v234 offset:0\n"
"ds_read_b64_tr_b16 a[246:247], v234 offset:4096\n"
"v_mfma_f32_16x16x32_bf16 v[156:159], v[212:215], a[20:23], v[156:159]\n"
"ds_read_b128 a[100:103], v248 offset:49152\n"
"v_exp_f32 v166, v166\n"
"v_exp_f32 v175, v175\n"
"v_exp_f32 v176, v176\n"
"v_exp_f32 v177, v177\n"
"v_exp_f32 v154, v154\n"
"v_mfma_f32_16x16x32_bf16 v[160:163], v[208:211], a[32:35], v[188:191]\n"
"v_mov_b32 v198, 0\n"
"v_cvt_pk_bf16_f32 v148, v164, v165\n"
"v_mfma_f32_16x16x32_bf16 v[160:163], v[212:215], a[36:39], v[160:163]\n"
"ds_read_b64_tr_b16 a[250:251], v238 offset:4096\n"
"ds_read_b64_tr_b16 a[254:255], v235 offset:4096\n"
"v_mfma_f32_16x16x32_bf16 v[160:163], v[216:219], a[40:43], v[160:163]\n"
"ds_read_b64_tr_b16 a[248:249], v238 offset:0\n"
"ds_read_b128 a[156:159], v249 offset:35840\n"
"v_mfma_f32_16x16x32_bf16 v[160:163], v[220:223], a[44:47], v[160:163]\n"
"ds_read_b128 a[172:175], v249 offset:50176\n"
"v_accvgpr_write_b32 a155, v11\n"
"v_cvt_pk_bf16_f32 v149, v166, v167\n"
"v_cvt_pk_bf16_f32 v134, v168, v169\n"
"v_exp_f32 v170, v170\n"
"v_exp_f32 v171, v171\n"
"v_cvt_pk_bf16_f32 v143, v174, v175\n"
"v_exp_f32 v179, v179\n"
"v_mfma_f32_16x16x32_bf16 v[164:167], v[216:219], a[200:203], v[192:195]\n"
"ds_read_b128 a[164:167], v249 offset:49152\n"
"v_cvt_pk_bf16_f32 v142, v172, v173\n"
"v_mfma_f32_16x16x32_bf16 v[164:167], v[220:223], a[204:207], v[164:167]\n"
"ds_read_b64_tr_b16 a[252:253], v235 offset:0\n"
"v_cvt_pk_bf16_f32 v112, v112, v113\n"
"v_mfma_f32_16x16x32_bf16 v[164:167], v[208:211], a[192:195], v[164:167]\n"
"ds_read_b128 a[140:143], v249 offset:33792\n"
"v_cvt_pk_bf16_f32 v0, v0, v1\n"
"v_mfma_f32_16x16x32_bf16 v[164:167], v[212:215], a[196:199], v[164:167]\n"
"s_waitcnt lgkmcnt(8)\n"
"ds_read_b128 a[148:151], v249 offset:34816\n"
"v_accvgpr_write_b32 a152, v8\n"
"v_cvt_pk_bf16_f32 v33, v34, v35\n"
"v_cvt_pk_bf16_f32 v135, v170, v171\n"
"v_exp_f32 v178, v178\n"
"v_xor_b32 v207, 64, v240\n"
"v_exp_f32 v153, v153\n"
"v_exp_f32 v162, v162\n"
"v_mfma_f32_16x16x32_bf16 a[216:219], v[124:127], a[244:247], 0\n"
"ds_read_b128 a[108:111], v248 offset:50176\n"
"v_cvt_pk_bf16_f32 v1, v2, v3\n"
"v_mfma_f32_16x16x32_bf16 v[8:11], v[128:131], a[244:247], 0\n"
"s_waitcnt lgkmcnt(7)\n"
"ds_read_b128 a[124:127], v248 offset:52224\n"
"v_cvt_pk_bf16_f32 v34, v36, v37\n"
"v_mfma_f32_16x16x32_bf16 v[8:11], v[132:135], a[248:251], v[8:11]\n"
"ds_read_b128 v[216:219], v228 offset:20992\n"
"v_cvt_pk_bf16_f32 v26, v28, v29\n"
"v_mfma_f32_16x16x32_bf16 v[40:43], v[136:139], a[244:247], 0\n"
"ds_read_b128 a[132:135], v249 offset:32768\n"
"v_cvt_pk_bf16_f32 v150, v176, v177\n"
"v_mfma_f32_16x16x32_bf16 v[40:43], v[140:143], a[248:251], v[40:43]\n"
"v_cvt_pk_bf16_f32 v3, v6, v7\n"
"v_cvt_pk_bf16_f32 v151, v178, v179\n"
"v_mfma_f32_16x16x32_bf16 v[72:75], v[144:147], a[244:247], 0\n"
"ds_read_b128 v[208:211], v228 offset:20480\n"
"v_accvgpr_write_b32 a242, v254\n"
"v_mfma_f32_16x16x32_bf16 v[72:75], v[148:151], a[248:251], v[72:75]\n"
"ds_read_b128 v[220:223], v229 offset:20992\n"
"v_cvt_pk_bf16_f32 v113, v114, v115\n"
"v_mfma_f32_16x16x32_bf16 v[76:79], v[148:151], v[224:227], 0\n"
"s_waitcnt lgkmcnt(6)\n"
"v_exp_f32 v159, v159\n"
"v_mfma_f32_16x16x32_bf16 v[76:79], v[144:147], a[252:255], v[76:79]\n"
"v_permlane16_swap_b32 v24, v26\n"
"v_mfma_f32_16x16x32_bf16 v[44:47], v[140:143], v[224:227], 0\n"
"v_permlane16_swap_b32 v32, v34\n"
"v_mfma_f32_16x16x32_bf16 v[44:47], v[136:139], a[252:255], v[44:47]\n"
"ds_read_b128 v[212:215], v229 offset:20480\n"
"v_permlane16_swap_b32 v25, v27\n"
"v_cvt_pk_bf16_f32 v35, v38, v39\n"
"v_exp_f32 v155, v155\n"
"v_exp_f32 v160, v160\n"
"v_exp_f32 v167, v167\n"
"v_mfma_f32_16x16x32_bf16 v[12:15], v[132:135], v[224:227], 0\n"
"v_exp_f32 v164, v164\n"
"v_mfma_f32_16x16x32_bf16 v[12:15], v[128:131], a[252:255], v[12:15]\n"
"v_exp_f32 v166, v166\n"
"v_mfma_f32_16x16x32_bf16 a[220:223], v[124:127], a[252:255], 0\n"
"s_waitcnt lgkmcnt(2)\n"
"ds_read_b64_tr_b16 a[254:255], v235 offset:4608\n"
"v_accvgpr_write_b32 a169, v25\n"
"v_smfmac_f32_16x16x64_bf16 v[196:199], a[240:243], v[128:135], v241\n"
"s_add_u32 m0, s44, 98304\n"
"ds_read_b64_tr_b16 v[224:225], v239 offset:512\n"
"v_cvt_pk_bf16_f32 v97, v154, v155\n"
"v_mfma_f32_16x16x32_bf16 v[202:205], a[240:243], v[124:127], v[202:205]\n"
"buffer_load_dwordx4 v240, s[20:23], s56 offen nt lds\n"
"v_mfma_f32_16x16x32_bf16 v[168:171], v[216:219], a[8:11], v[180:183]\n"
"s_waitcnt lgkmcnt(3)\n"
"v_exp_f32 v152, v152\n"
"v_mfma_f32_16x16x32_bf16 v[168:171], v[220:223], a[12:15], v[168:171]\n"
"ds_read_b64_tr_b16 a[252:253], v235 offset:512\n"
"v_accvgpr_write_b32 a168, v24\n"
"v_mfma_f32_16x16x32_bf16 v[168:171], v[208:211], a[0:3], v[168:171]\n"
"s_waitcnt lgkmcnt(3)\n"
"ds_read_b64_tr_b16 a[244:245], v234 offset:512\n"
"v_cvt_pk_bf16_f32 v96, v152, v153\n"
"v_mfma_f32_16x16x32_bf16 v[168:171], v[212:215], a[4:7], v[168:171]\n"
"ds_read_b64_tr_b16 a[248:249], v238 offset:512\n"
"ds_read_b64_tr_b16 a[250:251], v238 offset:4608\n"
"v_mfma_f32_16x16x32_bf16 v[172:175], v[208:211], a[16:19], v[184:187]\n"
"ds_read_b64_tr_b16 a[246:247], v234 offset:4608\n"
"v_cvt_pk_bf16_f32 v115, v118, v119\n"
"v_mfma_f32_16x16x32_bf16 v[172:175], v[212:215], a[20:23], v[172:175]\n"
"v_exp_f32 v165, v165\n"
"v_mfma_f32_16x16x32_bf16 v[172:175], v[216:219], a[24:27], v[172:175]\n"
"s_add_u32 s40, s56, s52\n"
"v_exp_f32 v158, v158\n"
"v_mfma_f32_16x16x32_bf16 v[172:175], v[220:223], a[28:31], v[172:175]\n"
"buffer_load_dwordx4 v207, s[20:23], s40 offen offset:1024 nt lds\n"
"v_mfma_f32_16x16x32_bf16 v[176:179], v[216:219], a[40:43], v[188:191]\n"
"v_exp_f32 v157, v157\n"
"v_mfma_f32_16x16x32_bf16 v[176:179], v[220:223], a[44:47], v[176:179]\n"
"v_accvgpr_write_b32 a105, v121\n"
"v_accvgpr_write_b32 a170, v26\n"
"v_mfma_f32_16x16x32_bf16 v[176:179], v[208:211], a[32:35], v[176:179]\n"
"v_exp_f32 v156, v156\n"
"v_mfma_f32_16x16x32_bf16 v[176:179], v[212:215], a[36:39], v[176:179]\n"
"v_permlane16_swap_b32 v113, v115\n"
"v_cvt_pk_bf16_f32 v2, v4, v5\n"
"v_exp_f32 v161, v161\n"
"v_exp_f32 v163, v163\n"
"v_exp_f32 v172, v172\n"
"v_mfma_f32_16x16x32_bf16 v[152:155], v[208:211], a[192:195], v[192:195]\n"
"v_accvgpr_write_b32 a171, v27\n"
"v_mov_b32 v200, 0\n"
"v_mfma_f32_16x16x32_bf16 v[152:155], v[212:215], a[196:199], v[152:155]\n"
"ds_read_b64_tr_b16 v[226:227], v239 offset:4608\n"
"v_accvgpr_write_b32 a176, v32\n"
"v_mfma_f32_16x16x32_bf16 v[152:155], v[216:219], a[200:203], v[152:155]\n"
"ds_read_b128 v[212:215], v231 offset:16384\n"
"v_cvt_pk_bf16_f32 v105, v158, v159\n"
"v_mfma_f32_16x16x32_bf16 v[152:155], v[220:223], a[204:207], v[152:155]\n"
"s_waitcnt lgkmcnt(2)\n"
"v_permlane16_swap_b32 v0, v2\n"
"v_mfma_f32_16x16x32_bf16 a[232:235], v[124:127], a[244:247], 0\n"
"ds_read_b128 v[216:219], v230 offset:16896\n"
"v_cvt_pk_bf16_f32 v18, v20, v21\n"
"v_mfma_f32_16x16x32_bf16 v[24:27], v[128:131], a[244:247], 0\n"
"s_add_u32 s40, s56, s53\n"
"ds_read_b128 v[208:211], v230 offset:16384\n"
"v_cvt_pk_bf16_f32 v104, v156, v157\n"
"v_mfma_f32_16x16x32_bf16 v[24:27], v[132:135], a[248:251], v[24:27]\n"
"buffer_load_dwordx4 v240, s[20:23], s40 offen offset:2048 nt lds\n"
"v_mfma_f32_16x16x32_bf16 v[56:59], v[136:139], a[244:247], 0\n"
"v_exp_f32 v174, v174\n"
"v_mfma_f32_16x16x32_bf16 v[56:59], v[140:143], a[248:251], v[56:59]\n"
"v_accvgpr_write_b32 a97, v113\n"
"v_accvgpr_write_b32 a144, v0\n"
"v_mfma_f32_16x16x32_bf16 v[88:91], v[144:147], a[244:247], 0\n"
"v_exp_f32 v171, v171\n"
"v_mfma_f32_16x16x32_bf16 v[88:91], v[148:151], a[248:251], v[88:91]\n"
"s_waitcnt lgkmcnt(2)\n"
"ds_read_b128 v[220:223], v231 offset:16896\n"
"v_cvt_pk_bf16_f32 v114, v116, v117\n"
"v_mfma_f32_16x16x32_bf16 v[92:95], v[148:151], v[224:227], 0\n"
"s_add_u32 s40, s56, s54\n"
"v_permlane16_swap_b32 v33, v35\n"
"v_mfma_f32_16x16x32_bf16 v[92:95], v[144:147], a[252:255], v[92:95]\n"
"buffer_load_dwordx4 v207, s[20:23], s40 offen offset:3072 nt lds\n"
"v_permlane16_swap_b32 v112, v114\n"
"v_accvgpr_write_b32 a99, v115\n"
"v_cvt_pk_bf16_f32 v120, v164, v165\n"
"v_cvt_pk_bf16_f32 v121, v166, v167\n"
"v_exp_f32 v175, v175\n"
"v_exp_f32 v178, v178\n"
"v_mfma_f32_16x16x32_bf16 v[60:63], v[140:143], v[224:227], 0\n"
"s_add_u32 s55, s55, s59\n"
"v_permlane16_swap_b32 v1, v3\n"
"v_mfma_f32_16x16x32_bf16 v[60:63], v[136:139], a[252:255], v[60:63]\n"
"s_or_b32 s55, s55, s58\n"
"v_accvgpr_write_b32 a96, v112\n"
"v_accvgpr_write_b32 a178, v34\n"
"v_mfma_f32_16x16x32_bf16 v[28:31], v[132:135], v[224:227], 0\n"
"s_xor_b32 s56, s55, s57\n"
"ds_read_b64_tr_b16 v[224:225], v237 offset:0\n"
"v_accvgpr_write_b32 a179, v35\n"
"v_mfma_f32_16x16x32_bf16 v[28:31], v[128:131], a[252:255], v[28:31]\n"
"ds_read_b64_tr_b16 a[244:245], v232 offset:0\n"
"v_accvgpr_write_b32 a145, v1\n"
"v_mfma_f32_16x16x32_bf16 a[236:239], v[124:127], a[252:255], 0\n"
"v_exp_f32 v179, v179\n"
"v_smfmac_f32_16x16x64_bf16 v[198:201], a[240:243], v[136:143], v241\n"
"s_waitcnt lgkmcnt(2)\n"
"ds_read_b64_tr_b16 a[252:253], v233 offset:0\n"
"v_accvgpr_write_b32 a177, v33\n"
"v_mfma_f32_16x16x32_bf16 v[156:159], v[208:211], a[0:3], v[180:183]\n"
"ds_read_b64_tr_b16 a[246:247], v232 offset:4096\n"
"v_cvt_pk_bf16_f32 v113, v162, v163\n"
"v_mfma_f32_16x16x32_bf16 v[156:159], v[212:215], a[4:7], v[156:159]\n"
"ds_read_b64_tr_b16 a[250:251], v236 offset:4096\n"
"v_cvt_pk_bf16_f32 v112, v160, v161\n"
"v_mfma_f32_16x16x32_bf16 v[156:159], v[216:219], a[8:11], v[156:159]\n"
"s_add_u32 m0, s44, 49152\n"
"ds_read_b64_tr_b16 a[248:249], v236 offset:0\n"
"ds_read_b64_tr_b16 v[226:227], v237 offset:4096\n"
"v_mfma_f32_16x16x32_bf16 v[156:159], v[220:223], a[12:15], v[156:159]\n"
"buffer_load_dwordx4 v240, s[16:19], s56 offen nt lds\n"
"v_mfma_f32_16x16x32_bf16 v[160:163], v[216:219], a[24:27], v[184:187]\n"
"v_exp_f32 v169, v169\n"
"v_mfma_f32_16x16x32_bf16 v[160:163], v[220:223], a[28:31], v[160:163]\n"
"v_accvgpr_write_b32 a98, v114\n"
"v_accvgpr_write_b32 a147, v3\n"
"v_mfma_f32_16x16x32_bf16 v[160:163], v[208:211], a[16:19], v[160:163]\n"
"v_accvgpr_write_b32 a146, v2\n"
"v_cvt_pk_bf16_f32 v107, v174, v175\n"
"v_mfma_f32_16x16x32_bf16 v[160:163], v[212:215], a[20:23], v[160:163]\n"
"ds_read_b64_tr_b16 a[254:255], v233 offset:4096\n"
"v_cvt_pk_bf16_f32 v115, v178, v179\n"
"v_mfma_f32_16x16x32_bf16 v[164:167], v[208:211], a[32:35], v[188:191]\n"
"v_exp_f32 v158, v158\n"
"v_mfma_f32_16x16x32_bf16 v[164:167], v[212:215], a[36:39], v[164:167]\n"
"v_exp_f32 v168, v168\n"
"v_mfma_f32_16x16x32_bf16 v[164:167], v[216:219], a[40:43], v[164:167]\n"
"v_exp_f32 v170, v170\n"
"v_mfma_f32_16x16x32_bf16 v[164:167], v[220:223], a[44:47], v[164:167]\n"
"s_waitcnt lgkmcnt(1)\n"
"v_exp_f32 v159, v159\n"
"v_mfma_f32_16x16x32_bf16 a[208:211], v[124:127], a[244:247], 0\n"
"ds_read_b128 v[208:211], v230 offset:20480\n"
"v_cvt_pk_bf16_f32 v19, v22, v23\n"
"v_mfma_f32_16x16x32_bf16 v[0:3], v[128:131], a[244:247], 0\n"
"s_add_u32 s40, s56, s52\n"
"ds_read_b128 v[216:219], v230 offset:20992\n"
"v_cvt_pk_bf16_f32 v98, v168, v169\n"
"v_mfma_f32_16x16x32_bf16 v[0:3], v[132:135], a[248:251], v[0:3]\n"
"buffer_load_dwordx4 v207, s[16:19], s40 offen offset:1024 nt lds\n"
"v_mfma_f32_16x16x32_bf16 v[32:35], v[136:139], a[244:247], 0\n"
"v_exp_f32 v165, v165\n"
"v_mfma_f32_16x16x32_bf16 v[32:35], v[140:143], a[248:251], v[32:35]\n"
"v_exp_f32 v176, v176\n"
"v_mfma_f32_16x16x32_bf16 v[64:67], v[144:147], a[244:247], 0\n"
"ds_read_b128 v[212:215], v231 offset:20480\n"
"v_cvt_pk_bf16_f32 v99, v170, v171\n"
"v_mfma_f32_16x16x32_bf16 v[64:67], v[148:151], a[248:251], v[64:67]\n"
"ds_read_b128 v[220:223], v231 offset:20992\n"
"v_accvgpr_write_b32 a107, v123\n"
"v_permlane16_swap_b32 v16, v18\n"
"v_exp_f32 v173, v173\n"
"v_exp_f32 v154, v154\n"
"v_exp_f32 v155, v155\n"
"v_mfma_f32_16x16x32_bf16 v[68:71], v[148:151], v[224:227], 0\n"
"s_waitcnt lgkmcnt(4)\n"
"s_add_u32 s40, s56, s53\n"
"v_exp_f32 v164, v164\n"
"v_mfma_f32_16x16x32_bf16 v[68:71], v[144:147], a[252:255], v[68:71]\n"
"buffer_load_dwordx4 v240, s[16:19], s40 offen offset:2048 nt lds\n"
"v_mfma_f32_16x16x32_bf16 v[36:39], v[140:143], v[224:227], 0\n"
"v_exp_f32 v153, v153\n"
"v_mfma_f32_16x16x32_bf16 v[36:39], v[136:139], a[252:255], v[36:39]\n"
"v_exp_f32 v166, v166\n"
"v_mfma_f32_16x16x32_bf16 v[4:7], v[132:135], v[224:227], 0\n"
"v_permlane16_swap_b32 v17, v19\n"
"v_mfma_f32_16x16x32_bf16 v[4:7], v[128:131], a[252:255], v[4:7]\n"
"ds_read_b64_tr_b16 a[250:251], v236 offset:4608\n"
"v_cvt_pk_bf16_f32 v106, v172, v173\n"
"v_mfma_f32_16x16x32_bf16 a[212:215], v[124:127], a[252:255], 0\n"
"v_exp_f32 v162, v162\n"
"v_smfmac_f32_16x16x64_bf16 v[200:203], a[240:243], v[144:151], v241\n"
"s_waitcnt lgkmcnt(1)\n"
"ds_read_b64_tr_b16 a[246:247], v232 offset:4608\n"
"ds_read_b64_tr_b16 a[248:249], v236 offset:512\n"
"v_mfma_f32_16x16x32_bf16 v[168:171], v[208:211], a[0:3], v[180:183]\n"
"v_exp_f32 v177, v177\n"
"v_mfma_f32_16x16x32_bf16 v[168:171], v[212:215], a[4:7], v[168:171]\n"
"ds_read_b64_tr_b16 a[244:245], v232 offset:512\n"
"v_cvt_pk_bf16_f32 v114, v176, v177\n"
"v_mfma_f32_16x16x32_bf16 v[168:171], v[216:219], a[8:11], v[168:171]\n"
"s_add_u32 s40, s56, s54\n"
"v_exp_f32 v157, v157\n"
"v_mfma_f32_16x16x32_bf16 v[168:171], v[220:223], a[12:15], v[168:171]\n"
"buffer_load_dwordx4 v207, s[16:19], s40 offen offset:3072 nt lds\n"
"v_mfma_f32_16x16x32_bf16 v[172:175], v[216:219], a[24:27], v[184:187]\n"
"v_exp_f32 v167, v167\n"
"v_mfma_f32_16x16x32_bf16 v[172:175], v[220:223], a[28:31], v[172:175]\n"
"v_exp_f32 v160, v160\n"
"v_mfma_f32_16x16x32_bf16 v[172:175], v[208:211], a[16:19], v[172:175]\n"
"ds_read_b64_tr_b16 a[254:255], v233 offset:4608\n"
"v_accvgpr_write_b32 a162, v18\n"
"v_mfma_f32_16x16x32_bf16 v[172:175], v[212:215], a[20:23], v[172:175]\n"
"s_cbranch_vccnz LPCE1_%=\nLPKE1_%=:\n"
"v_bfe_u32 v249, v242, 4, 2\n"
"v_mfma_f32_16x16x32_bf16 v[176:179], v[208:211], a[32:35], v[188:191]\n"
"v_accvgpr_write_b32 a106, v122\n"
"v_bfe_u32 v248, v242, 8, 2\n"
"v_mfma_f32_16x16x32_bf16 v[176:179], v[212:215], a[36:39], v[176:179]\n"
"ds_read_b64_tr_b16 a[252:253], v233 offset:512\n"
"v_accvgpr_write_b32 a161, v17\n"
"v_mfma_f32_16x16x32_bf16 v[176:179], v[216:219], a[40:43], v[176:179]\n"
"ds_read_b64_tr_b16 v[224:225], v237 offset:512\n"
"v_accvgpr_write_b32 a160, v16\n"
"v_mfma_f32_16x16x32_bf16 v[176:179], v[220:223], a[44:47], v[176:179]\n"
"s_waitcnt lgkmcnt(3)\n"
"ds_read_b64_tr_b16 v[226:227], v237 offset:4608\n"
"v_accvgpr_write_b32 a163, v19\n"
"v_lshlrev_b32 v248, 6, v248\n"
"v_exp_f32 v152, v152\n"
"v_exp_f32 v163, v163\n"
"v_exp_f32 v169, v169\n"
"v_mfma_f32_16x16x32_bf16 a[224:227], v[124:127], a[244:247], 0\n"
"v_exp_f32 v156, v156\n"
"v_mfma_f32_16x16x32_bf16 v[16:19], v[128:131], a[244:247], 0\n"
"s_waitcnt vmcnt(8)\n"
"v_xor_b32 v248, v248, v242\n"
"v_bfe_u32 v254, v242, 6, 4\n"
"v_mfma_f32_16x16x32_bf16 v[16:19], v[132:135], a[248:251], v[16:19]\n"
"s_barrier\n"
"ds_read_b128 v[216:219], v228 offset:33280\n"
"v_mul_u32_u24 v249, 20, v249\n"
"v_cvt_pk_bf16_f32 v101, v158, v159\n"
"v_exp_f32 v176, v176\n"
"v_exp_f32 v177, v177\n"
"v_mfma_f32_16x16x32_bf16 v[48:51], v[136:139], a[244:247], 0\n"
"ds_read_b128 v[220:223], v229 offset:33280\n"
"v_cvt_pk_bf16_f32 v109, v162, v163\n"
"v_mfma_f32_16x16x32_bf16 v[48:51], v[140:143], a[248:251], v[48:51]\n"
"ds_read_b128 v[208:211], v228 offset:32768\n"
"ds_read_b128 v[212:215], v229 offset:32768\n"
"v_mfma_f32_16x16x32_bf16 v[80:83], v[144:147], a[244:247], 0\n"
"ds_read_b64_tr_b16 a[246:247], v234 offset:20480\n"
"v_cvt_pk_bf16_f32 v122, v152, v153\n"
"v_mfma_f32_16x16x32_bf16 v[80:83], v[148:151], a[248:251], v[80:83]\n"
"s_waitcnt lgkmcnt(5)\n"
"ds_read_b64_tr_b16 a[250:251], v238 offset:20480\n"
"v_cvt_pk_bf16_f32 v123, v154, v155\n"
"v_mfma_f32_16x16x32_bf16 v[84:87], v[148:151], v[224:227], 0\n"
"v_cvt_pk_bf16_f32 v116, v164, v165\n"
"v_cvt_pk_bf16_f32 v117, v166, v167\n"
"v_mfma_f32_16x16x32_bf16 v[84:87], v[144:147], a[252:255], v[84:87]\n"
"v_xor_b32 v249, v249, v254\n"
"v_cvt_pk_bf16_f32 v118, v176, v177\n"
"v_mfma_f32_16x16x32_bf16 v[52:55], v[140:143], v[224:227], 0\n"
"v_lshl_add_u32 v249, v249, 4, s44\n"
"v_cvt_pk_bf16_f32 v100, v156, v157\n"
"v_mfma_f32_16x16x32_bf16 v[52:55], v[136:139], a[252:255], v[52:55]\n"
"v_exp_f32 v161, v161\n"
"v_mfma_f32_16x16x32_bf16 v[20:23], v[132:135], v[224:227], 0\n"
"v_exp_f32 v174, v174\n"
"v_mfma_f32_16x16x32_bf16 v[20:23], v[128:131], a[252:255], v[20:23]\n"
"v_exp_f32 v168, v168\n"
"v_mfma_f32_16x16x32_bf16 a[228:231], v[124:127], a[252:255], 0\n"
"s_waitcnt lgkmcnt(1)\n"
"ds_read_b64_tr_b16 a[254:255], v235 offset:20480\n"
"s_branch LAY2_%=\n"
"LAL_%=:\n"
"v_mfma_f32_16x16x32_bf16 a[224:227], v[124:127], a[244:247], a[224:227]\n"
"v_exp_f32 v162, v162\n"
"v_mfma_f32_16x16x32_bf16 v[16:19], v[128:131], a[244:247], v[16:19]\n"
"s_waitcnt vmcnt(8)\n"
"v_exp_f32 v168, v168\n"
"v_mfma_f32_16x16x32_bf16 v[16:19], v[132:135], a[248:251], v[16:19]\n"
"s_barrier\n"
"v_mfma_f32_16x16x32_bf16 v[48:51], v[136:139], a[244:247], v[48:51]\n"
"ds_read_b128 v[208:211], v228 offset:0\n"
"v_cvt_pk_bf16_f32 v101, v158, v159\n"
"v_mfma_f32_16x16x32_bf16 v[48:51], v[140:143], a[248:251], v[48:51]\n"
"ds_read_b128 v[216:219], v228 offset:512\n"
"v_cvt_pk_bf16_f32 v109, v162, v163\n"
"v_mfma_f32_16x16x32_bf16 v[80:83], v[144:147], a[244:247], v[80:83]\n"
"ds_read_b128 v[220:223], v229 offset:512\n"
"v_cvt_pk_bf16_f32 v123, v154, v155\n"
"v_mfma_f32_16x16x32_bf16 v[80:83], v[148:151], a[248:251], v[80:83]\n"
"ds_read_b128 v[212:215], v229 offset:0\n"
"v_cvt_pk_bf16_f32 v117, v166, v167\n"
"v_mfma_f32_16x16x32_bf16 v[84:87], v[148:151], v[224:227], v[84:87]\n"
"v_exp_f32 v176, v176\n"
"v_mfma_f32_16x16x32_bf16 v[84:87], v[144:147], a[252:255], v[84:87]\n"
"v_exp_f32 v169, v169\n"
"v_mfma_f32_16x16x32_bf16 v[52:55], v[140:143], v[224:227], v[52:55]\n"
"ds_read_b64_tr_b16 a[246:247], v234 offset:53248\n"
"v_cvt_pk_bf16_f32 v116, v164, v165\n"
"v_mfma_f32_16x16x32_bf16 v[52:55], v[136:139], a[252:255], v[52:55]\n"
"v_exp_f32 v177, v177\n"
"v_mfma_f32_16x16x32_bf16 v[20:23], v[132:135], v[224:227], v[20:23]\n"
"ds_read_b64_tr_b16 a[250:251], v238 offset:53248\n"
"v_cvt_pk_bf16_f32 v118, v176, v177\n"
"v_mfma_f32_16x16x32_bf16 v[20:23], v[128:131], a[252:255], v[20:23]\n"
"v_exp_f32 v174, v174\n"
"v_mfma_f32_16x16x32_bf16 a[228:231], v[124:127], a[252:255], a[228:231]\n"
"s_waitcnt lgkmcnt(3)\n"
"ds_read_b64_tr_b16 a[254:255], v235 offset:53248\n"
"v_cvt_pk_bf16_f32 v100, v156, v157\n"
"v_mfma_f32_16x16x32_bf16 v[152:155], v[208:211], a[0:3], v[180:183]\n"
"s_waitcnt lgkmcnt(3)\n"
"ds_read_b64_tr_b16 a[248:249], v238 offset:49152\n"
"v_cvt_pk_bf16_f32 v102, v168, v169\n"
"v_mfma_f32_16x16x32_bf16 v[152:155], v[212:215], a[4:7], v[152:155]\n"
"v_exp_f32 v172, v172\n"
"v_mfma_f32_16x16x32_bf16 v[152:155], v[216:219], a[8:11], v[152:155]\n"
"ds_read_b64_tr_b16 a[244:245], v234 offset:49152\n"
"v_cvt_pk_bf16_f32 v108, v160, v161\n"
"v_mfma_f32_16x16x32_bf16 v[152:155], v[220:223], a[12:15], v[152:155]\n"
"v_exp_f32 v175, v175\n"
"v_mfma_f32_16x16x32_bf16 v[156:159], v[216:219], a[24:27], v[184:187]\n"
"ds_read_b64_tr_b16 a[252:253], v235 offset:49152\n"
"v_cvt_pk_bf16_f32 v111, v174, v175\n"
"v_mfma_f32_16x16x32_bf16 v[156:159], v[220:223], a[28:31], v[156:159]\n"
"ds_read_b64_tr_b16 v[224:225], v239 offset:49152\n"
"v_mfma_f32_16x16x32_bf16 v[156:159], v[208:211], a[16:19], v[156:159]\n"
"s_add_u32 m0, s44, 81920\n"
"ds_read_b64_tr_b16 v[226:227], v239 offset:53248\n"
"v_mfma_f32_16x16x32_bf16 v[156:159], v[212:215], a[20:23], v[156:159]\n"
"buffer_load_dwordx4 v240, s[20:23], s56 offen nt lds\n"
"v_mfma_f32_16x16x32_bf16 v[160:163], v[208:211], a[32:35], v[188:191]\n"
"v_exp_f32 v178, v178\n"
"v_mfma_f32_16x16x32_bf16 v[160:163], v[212:215], a[36:39], v[160:163]\n"
"v_exp_f32 v171, v171\n"
"v_mfma_f32_16x16x32_bf16 v[160:163], v[216:219], a[40:43], v[160:163]\n"
"s_add_u32 s40, s56, s52\n"
"v_mfma_f32_16x16x32_bf16 v[160:163], v[220:223], a[44:47], v[160:163]\n"
"buffer_load_dwordx4 v207, s[20:23], s40 offen offset:1024 nt lds\n"
"v_mfma_f32_16x16x32_bf16 v[164:167], v[216:219], a[200:203], v[192:195]\n"
"v_exp_f32 v170, v170\n"
"v_mfma_f32_16x16x32_bf16 v[164:167], v[220:223], a[204:207], v[164:167]\n"
"v_exp_f32 v153, v153\n"
"v_mfma_f32_16x16x32_bf16 v[164:167], v[208:211], a[192:195], v[164:167]\n"
"v_exp_f32 v179, v179\n"
"v_mfma_f32_16x16x32_bf16 v[164:167], v[212:215], a[196:199], v[164:167]\n"
"s_waitcnt lgkmcnt(0)\n"
"v_exp_f32 v158, v158\n"
"v_mfma_f32_16x16x32_bf16 a[216:219], v[120:123], a[244:247], a[216:219]\n"
"ds_read_b128 v[216:219], v228 offset:4608\n"
"v_cvt_pk_bf16_f32 v103, v170, v171\n"
"v_mfma_f32_16x16x32_bf16 v[8:11], v[96:99], a[244:247], v[8:11]\n"
"v_exp_f32 v173, v173\n"
"v_mfma_f32_16x16x32_bf16 v[8:11], v[100:103], a[248:251], v[8:11]\n"
"ds_read_b128 v[208:211], v228 offset:4096\n"
"v_cvt_pk_bf16_f32 v110, v172, v173\n"
"v_mfma_f32_16x16x32_bf16 v[40:43], v[104:107], a[244:247], v[40:43]\n"
"v_exp_f32 v162, v162\n"
"v_mfma_f32_16x16x32_bf16 v[40:43], v[108:111], a[248:251], v[40:43]\n"
"ds_read_b128 v[220:223], v229 offset:4608\n"
"v_cvt_pk_bf16_f32 v119, v178, v179\n"
"v_mfma_f32_16x16x32_bf16 v[72:75], v[112:115], a[244:247], v[72:75]\n"
"s_add_u32 s40, s56, s53\n"
"ds_read_b128 v[212:215], v229 offset:4096\n"
"v_mfma_f32_16x16x32_bf16 v[72:75], v[116:119], a[248:251], v[72:75]\n"
"buffer_load_dwordx4 v240, s[20:23], s40 offen offset:2048 nt lds\n"
"v_mfma_f32_16x16x32_bf16 v[76:79], v[116:119], v[224:227], v[76:79]\n"
"v_exp_f32 v163, v163\n"
"v_mfma_f32_16x16x32_bf16 v[76:79], v[112:115], a[252:255], v[76:79]\n"
"v_exp_f32 v156, v156\n"
"v_mfma_f32_16x16x32_bf16 v[44:47], v[108:111], v[224:227], v[44:47]\n"
"v_exp_f32 v160, v160\n"
"v_mfma_f32_16x16x32_bf16 v[44:47], v[104:107], a[252:255], v[44:47]\n"
"v_exp_f32 v155, v155\n"
"v_mfma_f32_16x16x32_bf16 v[12:15], v[100:103], v[224:227], v[12:15]\n"
"ds_read_b64_tr_b16 v[226:227], v239 offset:53760\n"
"v_mfma_f32_16x16x32_bf16 v[12:15], v[96:99], a[252:255], v[12:15]\n"
"ds_read_b64_tr_b16 a[248:249], v238 offset:49664\n"
"v_mfma_f32_16x16x32_bf16 a[220:223], v[120:123], a[252:255], a[220:223]\n"
"v_exp_f32 v154, v154\n"
"v_smfmac_f32_16x16x64_bf16 v[196:199], a[240:243], v[96:103], v241\n"
"ds_read_b64_tr_b16 a[250:251], v238 offset:53760\n"
"v_mfma_f32_16x16x32_bf16 v[202:205], a[240:243], v[120:123], v[202:205]\n"
"s_waitcnt lgkmcnt(3)\n"
"v_exp_f32 v152, v152\n"
"v_mfma_f32_16x16x32_bf16 v[168:171], v[216:219], a[8:11], v[180:183]\n"
"ds_read_b64_tr_b16 a[244:245], v234 offset:49664\n"
"v_cvt_pk_bf16_f32 v128, v152, v153\n"
"v_mfma_f32_16x16x32_bf16 v[168:171], v[220:223], a[12:15], v[168:171]\n"
"v_exp_f32 v165, v165\n"
"v_mfma_f32_16x16x32_bf16 v[168:171], v[208:211], a[0:3], v[168:171]\n"
"ds_read_b64_tr_b16 a[246:247], v234 offset:53760\n"
"v_cvt_pk_bf16_f32 v145, v162, v163\n"
"v_mfma_f32_16x16x32_bf16 v[168:171], v[212:215], a[4:7], v[168:171]\n"
"ds_read_b64_tr_b16 a[254:255], v235 offset:53760\n"
"v_mfma_f32_16x16x32_bf16 v[172:175], v[208:211], a[16:19], v[184:187]\n"
"ds_read_b64_tr_b16 v[224:225], v239 offset:49664\n"
"v_cvt_pk_bf16_f32 v129, v154, v155\n"
"v_mfma_f32_16x16x32_bf16 v[172:175], v[212:215], a[20:23], v[172:175]\n"
"v_mfma_f32_16x16x32_bf16 v[172:175], v[216:219], a[24:27], v[172:175]\n"
"s_add_u32 s40, s56, s54\n"
"ds_read_b64_tr_b16 a[252:253], v235 offset:49664\n"
"v_mfma_f32_16x16x32_bf16 v[172:175], v[220:223], a[28:31], v[172:175]\n"
"buffer_load_dwordx4 v207, s[20:23], s40 offen offset:3072 nt lds\n"
"v_mfma_f32_16x16x32_bf16 v[176:179], v[216:219], a[40:43], v[188:191]\n"
"s_add_u32 s55, s55, s59\n"
"v_exp_f32 v157, v157\n"
"v_mfma_f32_16x16x32_bf16 v[176:179], v[220:223], a[44:47], v[176:179]\n"
"s_or_b32 s55, s55, s58\n"
"v_exp_f32 v171, v171\n"
"v_mfma_f32_16x16x32_bf16 v[176:179], v[208:211], a[32:35], v[176:179]\n"
"s_xor_b32 s56, s55, s57\n"
"v_exp_f32 v159, v159\n"
"v_mfma_f32_16x16x32_bf16 v[176:179], v[212:215], a[36:39], v[176:179]\n"
"v_exp_f32 v167, v167\n"
"v_mfma_f32_16x16x32_bf16 v[152:155], v[208:211], a[192:195], v[192:195]\n"
"v_cvt_pk_bf16_f32 v137, v158, v159\n"
"v_mfma_f32_16x16x32_bf16 v[152:155], v[212:215], a[196:199], v[152:155]\n"
"ds_read_b128 v[208:211], v230 offset:0\n"
"v_cvt_pk_bf16_f32 v136, v156, v157\n"
"v_mfma_f32_16x16x32_bf16 v[152:155], v[216:219], a[200:203], v[152:155]\n"
"v_exp_f32 v166, v166\n"
"v_mfma_f32_16x16x32_bf16 v[152:155], v[220:223], a[204:207], v[152:155]\n"
"s_waitcnt lgkmcnt(1)\n"
"ds_read_b128 v[216:219], v230 offset:512\n"
"v_mfma_f32_16x16x32_bf16 a[232:235], v[120:123], a[244:247], a[232:235]\n"
"v_exp_f32 v169, v169\n"
"v_mfma_f32_16x16x32_bf16 v[24:27], v[96:99], a[244:247], v[24:27]\n"
"ds_read_b128 v[220:223], v231 offset:512\n"
"v_mfma_f32_16x16x32_bf16 v[24:27], v[100:103], a[248:251], v[24:27]\n"
"v_exp_f32 v164, v164\n"
"v_mfma_f32_16x16x32_bf16 v[56:59], v[104:107], a[244:247], v[56:59]\n"
"ds_read_b128 v[212:215], v231 offset:0\n"
"v_mfma_f32_16x16x32_bf16 v[56:59], v[108:111], a[248:251], v[56:59]\n"
"v_exp_f32 v178, v178\n"
"v_mfma_f32_16x16x32_bf16 v[88:91], v[112:115], a[244:247], v[88:91]\n"
"s_add_u32 m0, s44, 32768\n"
"ds_read_b64_tr_b16 a[246:247], v232 offset:53248\n"
"v_mfma_f32_16x16x32_bf16 v[88:91], v[116:119], a[248:251], v[88:91]\n"
"buffer_load_dwordx4 v240, s[16:19], s56 offen nt lds\n"
"v_mfma_f32_16x16x32_bf16 v[92:95], v[116:119], v[224:227], v[92:95]\n"
"v_exp_f32 v175, v175\n"
"v_mfma_f32_16x16x32_bf16 v[92:95], v[112:115], a[252:255], v[92:95]\n"
"v_exp_f32 v168, v168\n"
"v_mfma_f32_16x16x32_bf16 v[60:63], v[108:111], v[224:227], v[60:63]\n"
"v_exp_f32 v161, v161\n"
"v_mfma_f32_16x16x32_bf16 v[60:63], v[104:107], a[252:255], v[60:63]\n"
"v_exp_f32 v177, v177\n"
"v_mfma_f32_16x16x32_bf16 v[28:31], v[100:103], v[224:227], v[28:31]\n"
"ds_read_b64_tr_b16 v[226:227], v237 offset:53248\n"
"v_mfma_f32_16x16x32_bf16 v[28:31], v[96:99], a[252:255], v[28:31]\n"
"v_exp_f32 v172, v172\n"
"v_mfma_f32_16x16x32_bf16 a[236:239], v[120:123], a[252:255], a[236:239]\n"
"ds_read_b64_tr_b16 a[250:251], v236 offset:53248\n"
"v_cvt_pk_bf16_f32 v124, v164, v165\n"
"v_smfmac_f32_16x16x64_bf16 v[198:201], a[240:243], v[104:111], v241\n"
"s_waitcnt lgkmcnt(2)\n"
"ds_read_b64_tr_b16 a[252:253], v233 offset:49152\n"
"v_mfma_f32_16x16x32_bf16 v[156:159], v[208:211], a[0:3], v[180:183]\n"
"ds_read_b64_tr_b16 a[248:249], v236 offset:49152\n"
"v_cvt_pk_bf16_f32 v125, v166, v167\n"
"v_mfma_f32_16x16x32_bf16 v[156:159], v[212:215], a[4:7], v[156:159]\n"
"ds_read_b64_tr_b16 v[224:225], v237 offset:49152\n"
"v_cvt_pk_bf16_f32 v130, v168, v169\n"
"v_mfma_f32_16x16x32_bf16 v[156:159], v[216:219], a[8:11], v[156:159]\n"
"ds_read_b64_tr_b16 a[254:255], v233 offset:53248\n"
"v_cvt_pk_bf16_f32 v144, v160, v161\n"
"v_mfma_f32_16x16x32_bf16 v[156:159], v[220:223], a[12:15], v[156:159]\n"
"ds_read_b64_tr_b16 a[244:245], v232 offset:49152\n"
"v_mfma_f32_16x16x32_bf16 v[160:163], v[216:219], a[24:27], v[184:187]\n"
"v_exp_f32 v176, v176\n"
"v_mfma_f32_16x16x32_bf16 v[160:163], v[220:223], a[28:31], v[160:163]\n"
"v_exp_f32 v152, v152\n"
"v_mfma_f32_16x16x32_bf16 v[160:163], v[208:211], a[16:19], v[160:163]\n"
"s_add_u32 s40, s56, s52\n"
"v_cvt_pk_bf16_f32 v146, v176, v177\n"
"v_mfma_f32_16x16x32_bf16 v[160:163], v[212:215], a[20:23], v[160:163]\n"
"buffer_load_dwordx4 v207, s[16:19], s40 offen offset:1024 nt lds\n"
"v_mfma_f32_16x16x32_bf16 v[164:167], v[208:211], a[32:35], v[188:191]\n"
"v_exp_f32 v170, v170\n"
"v_mfma_f32_16x16x32_bf16 v[164:167], v[212:215], a[36:39], v[164:167]\n"
"v_exp_f32 v173, v173\n"
"v_mfma_f32_16x16x32_bf16 v[164:167], v[216:219], a[40:43], v[164:167]\n"
"v_exp_f32 v153, v153\n"
"v_mfma_f32_16x16x32_bf16 v[164:167], v[220:223], a[44:47], v[164:167]\n"
"s_waitcnt lgkmcnt(0)\n"
"v_exp_f32 v174, v174\n"
"v_mfma_f32_16x16x32_bf16 a[208:211], v[120:123], a[244:247], a[208:211]\n"
"ds_read_b128 v[208:211], v230 offset:4096\n"
"v_cvt_pk_bf16_f32 v139, v174, v175\n"
"v_mfma_f32_16x16x32_bf16 v[0:3], v[96:99], a[244:247], v[0:3]\n"
"v_exp_f32 v156, v156\n"
"v_mfma_f32_16x16x32_bf16 v[0:3], v[100:103], a[248:251], v[0:3]\n"
"ds_read_b128 v[220:223], v231 offset:4608\n"
"v_cvt_pk_bf16_f32 v138, v172, v173\n"
"v_mfma_f32_16x16x32_bf16 v[32:35], v[104:107], a[244:247], v[32:35]\n"
"v_exp_f32 v161, v161\n"
"v_mfma_f32_16x16x32_bf16 v[32:35], v[108:111], a[248:251], v[32:35]\n"
"ds_read_b128 v[216:219], v230 offset:4608\n"
"v_cvt_pk_bf16_f32 v126, v152, v153\n"
"v_mfma_f32_16x16x32_bf16 v[64:67], v[112:115], a[244:247], v[64:67]\n"
"s_add_u32 s40, s56, s53\n"
"ds_read_b128 v[212:215], v231 offset:4096\n"
"v_mfma_f32_16x16x32_bf16 v[64:67], v[116:119], a[248:251], v[64:67]\n"
"buffer_load_dwordx4 v240, s[16:19], s40 offen offset:2048 nt lds\n"
"v_mfma_f32_16x16x32_bf16 v[68:71], v[116:119], v[224:227], v[68:71]\n"
"v_exp_f32 v159, v159\n"
"v_mfma_f32_16x16x32_bf16 v[68:71], v[112:115], a[252:255], v[68:71]\n"
"v_exp_f32 v155, v155\n"
"v_mfma_f32_16x16x32_bf16 v[36:39], v[108:111], v[224:227], v[36:39]\n"
"v_exp_f32 v160, v160\n"
"v_mfma_f32_16x16x32_bf16 v[36:39], v[104:107], a[252:255], v[36:39]\n"
"v_exp_f32 v179, v179\n"
"v_mfma_f32_16x16x32_bf16 v[4:7], v[100:103], v[224:227], v[4:7]\n"
"ds_read_b64_tr_b16 a[244:245], v232 offset:49664\n"
"ds_read_b64_tr_b16 v[224:225], v237 offset:49664\n"
"v_mfma_f32_16x16x32_bf16 v[4:7], v[96:99], a[252:255], v[4:7]\n"
"v_exp_f32 v158, v158\n"
"v_mfma_f32_16x16x32_bf16 a[212:215], v[120:123], a[252:255], a[212:215]\n"
"ds_read_b64_tr_b16 a[246:247], v232 offset:53760\n"
"ds_read_b64_tr_b16 a[250:251], v236 offset:53760\n"
"v_smfmac_f32_16x16x64_bf16 v[200:203], a[240:243], v[112:119], v241\n"
"s_waitcnt lgkmcnt(4)\n"
"ds_read_b64_tr_b16 v[226:227], v237 offset:53760\n"
"v_cvt_pk_bf16_f32 v131, v170, v171\n"
"v_mfma_f32_16x16x32_bf16 v[168:171], v[208:211], a[0:3], v[180:183]\n"
"v_exp_f32 v164, v164\n"
"v_mfma_f32_16x16x32_bf16 v[168:171], v[212:215], a[4:7], v[168:171]\n"
"ds_read_b64_tr_b16 a[248:249], v236 offset:49664\n"
"ds_read_b64_tr_b16 a[254:255], v233 offset:53760\n"
"v_mfma_f32_16x16x32_bf16 v[168:171], v[216:219], a[8:11], v[168:171]\n"
"s_add_u32 s40, s56, s54\n"
"ds_read_b64_tr_b16 a[252:253], v233 offset:49664\n"
"v_mfma_f32_16x16x32_bf16 v[168:171], v[220:223], a[12:15], v[168:171]\n"
"buffer_load_dwordx4 v207, s[16:19], s40 offen offset:3072 nt lds\n"
"v_mfma_f32_16x16x32_bf16 v[172:175], v[216:219], a[24:27], v[184:187]\n"
"v_exp_f32 v154, v154\n"
"v_mfma_f32_16x16x32_bf16 v[172:175], v[220:223], a[28:31], v[172:175]\n"
"v_exp_f32 v167, v167\n"
"v_mfma_f32_16x16x32_bf16 v[172:175], v[208:211], a[16:19], v[172:175]\n"
"v_exp_f32 v163, v163\n"
"v_mfma_f32_16x16x32_bf16 v[172:175], v[212:215], a[20:23], v[172:175]\n"
"s_cbranch_vccnz LPCL0_%=\nLPKL0_%=:\n"
"v_cvt_pk_bf16_f32 v147, v178, v179\n"
"v_mfma_f32_16x16x32_bf16 v[176:179], v[208:211], a[32:35], v[188:191]\n"
"v_exp_f32 v165, v165\n"
"v_mfma_f32_16x16x32_bf16 v[176:179], v[212:215], a[36:39], v[176:179]\n"
"v_exp_f32 v157, v157\n"
"v_mfma_f32_16x16x32_bf16 v[176:179], v[216:219], a[40:43], v[176:179]\n"
"v_exp_f32 v166, v166\n"
"v_mfma_f32_16x16x32_bf16 v[176:179], v[220:223], a[44:47], v[176:179]\n"
"s_waitcnt lgkmcnt(0)\n"
"v_mfma_f32_16x16x32_bf16 a[224:227], v[120:123], a[244:247], a[224:227]\n"
"v_exp_f32 v162, v162\n"
"v_mfma_f32_16x16x32_bf16 v[16:19], v[96:99], a[244:247], v[16:19]\n"
"s_waitcnt vmcnt(8)\n"
"v_exp_f32 v168, v168\n"
"v_mfma_f32_16x16x32_bf16 v[16:19], v[100:103], a[248:251], v[16:19]\n"
"s_barrier\n"
"v_mfma_f32_16x16x32_bf16 v[48:51], v[104:107], a[244:247], v[48:51]\n"
"ds_read_b128 v[208:211], v228 offset:16384\n"
"v_cvt_pk_bf16_f32 v133, v158, v159\n"
"v_mfma_f32_16x16x32_bf16 v[48:51], v[108:111], a[248:251], v[48:51]\n"
"ds_read_b128 v[216:219], v228 offset:16896\n"
"v_cvt_pk_bf16_f32 v141, v162, v163\n"
"v_mfma_f32_16x16x32_bf16 v[80:83], v[112:115], a[244:247], v[80:83]\n"
"ds_read_b128 v[220:223], v229 offset:16896\n"
"v_cvt_pk_bf16_f32 v127, v154, v155\n"
"v_mfma_f32_16x16x32_bf16 v[80:83], v[116:119], a[248:251], v[80:83]\n"
"ds_read_b128 v[212:215], v229 offset:16384\n"
"v_cvt_pk_bf16_f32 v149, v166, v167\n"
"v_mfma_f32_16x16x32_bf16 v[84:87], v[116:119], v[224:227], v[84:87]\n"
"v_exp_f32 v176, v176\n"
"v_mfma_f32_16x16x32_bf16 v[84:87], v[112:115], a[252:255], v[84:87]\n"
"v_exp_f32 v169, v169\n"
"v_mfma_f32_16x16x32_bf16 v[52:55], v[108:111], v[224:227], v[52:55]\n"
"ds_read_b64_tr_b16 a[246:247], v234 offset:4096\n"
"v_cvt_pk_bf16_f32 v148, v164, v165\n"
"v_mfma_f32_16x16x32_bf16 v[52:55], v[104:107], a[252:255], v[52:55]\n"
"v_exp_f32 v177, v177\n"
"v_mfma_f32_16x16x32_bf16 v[20:23], v[100:103], v[224:227], v[20:23]\n"
"ds_read_b64_tr_b16 a[250:251], v238 offset:4096\n"
"v_cvt_pk_bf16_f32 v150, v176, v177\n"
"v_mfma_f32_16x16x32_bf16 v[20:23], v[96:99], a[252:255], v[20:23]\n"
"v_exp_f32 v174, v174\n"
"v_mfma_f32_16x16x32_bf16 a[228:231], v[120:123], a[252:255], a[228:231]\n"
"s_waitcnt lgkmcnt(3)\n"
"ds_read_b64_tr_b16 a[254:255], v235 offset:4096\n"
"v_cvt_pk_bf16_f32 v132, v156, v157\n"
"v_mfma_f32_16x16x32_bf16 v[152:155], v[208:211], a[0:3], v[180:183]\n"
"s_waitcnt lgkmcnt(3)\n"
"ds_read_b64_tr_b16 a[248:249], v238 offset:0\n"
"v_cvt_pk_bf16_f32 v134, v168, v169\n"
"v_mfma_f32_16x16x32_bf16 v[152:155], v[212:215], a[4:7], v[152:155]\n"
"v_exp_f32 v172, v172\n"
"v_mfma_f32_16x16x32_bf16 v[152:155], v[216:219], a[8:11], v[152:155]\n"
"ds_read_b64_tr_b16 a[244:245], v234 offset:0\n"
"v_cvt_pk_bf16_f32 v140, v160, v161\n"
"v_mfma_f32_16x16x32_bf16 v[152:155], v[220:223], a[12:15], v[152:155]\n"
"v_exp_f32 v175, v175\n"
"v_mfma_f32_16x16x32_bf16 v[156:159], v[216:219], a[24:27], v[184:187]\n"
"ds_read_b64_tr_b16 a[252:253], v235 offset:0\n"
"v_cvt_pk_bf16_f32 v143, v174, v175\n"
"v_mfma_f32_16x16x32_bf16 v[156:159], v[220:223], a[28:31], v[156:159]\n"
"ds_read_b64_tr_b16 v[224:225], v239 offset:0\n"
"v_mfma_f32_16x16x32_bf16 v[156:159], v[208:211], a[16:19], v[156:159]\n"
"s_add_u32 m0, s44, 98304\n"
"ds_read_b64_tr_b16 v[226:227], v239 offset:4096\n"
"v_mfma_f32_16x16x32_bf16 v[156:159], v[212:215], a[20:23], v[156:159]\n"
"buffer_load_dwordx4 v240, s[20:23], s56 offen nt lds\n"
"v_mfma_f32_16x16x32_bf16 v[160:163], v[208:211], a[32:35], v[188:191]\n"
"v_exp_f32 v178, v178\n"
"v_mfma_f32_16x16x32_bf16 v[160:163], v[212:215], a[36:39], v[160:163]\n"
"v_exp_f32 v171, v171\n"
"v_mfma_f32_16x16x32_bf16 v[160:163], v[216:219], a[40:43], v[160:163]\n"
"s_add_u32 s40, s56, s52\n"
"v_mfma_f32_16x16x32_bf16 v[160:163], v[220:223], a[44:47], v[160:163]\n"
"buffer_load_dwordx4 v207, s[20:23], s40 offen offset:1024 nt lds\n"
"v_mfma_f32_16x16x32_bf16 v[164:167], v[216:219], a[200:203], v[192:195]\n"
"v_exp_f32 v170, v170\n"
"v_mfma_f32_16x16x32_bf16 v[164:167], v[220:223], a[204:207], v[164:167]\n"
"v_exp_f32 v153, v153\n"
"v_mfma_f32_16x16x32_bf16 v[164:167], v[208:211], a[192:195], v[164:167]\n"
"v_exp_f32 v179, v179\n"
"v_mfma_f32_16x16x32_bf16 v[164:167], v[212:215], a[196:199], v[164:167]\n"
"s_waitcnt lgkmcnt(0)\n"
"v_exp_f32 v158, v158\n"
"v_mfma_f32_16x16x32_bf16 a[216:219], v[124:127], a[244:247], a[216:219]\n"
"ds_read_b128 v[216:219], v228 offset:20992\n"
"v_cvt_pk_bf16_f32 v135, v170, v171\n"
"v_mfma_f32_16x16x32_bf16 v[8:11], v[128:131], a[244:247], v[8:11]\n"
"v_exp_f32 v173, v173\n"
"v_mfma_f32_16x16x32_bf16 v[8:11], v[132:135], a[248:251], v[8:11]\n"
"ds_read_b128 v[208:211], v228 offset:20480\n"
"v_cvt_pk_bf16_f32 v142, v172, v173\n"
"v_mfma_f32_16x16x32_bf16 v[40:43], v[136:139], a[244:247], v[40:43]\n"
"v_exp_f32 v162, v162\n"
"v_mfma_f32_16x16x32_bf16 v[40:43], v[140:143], a[248:251], v[40:43]\n"
"ds_read_b128 v[220:223], v229 offset:20992\n"
"v_cvt_pk_bf16_f32 v151, v178, v179\n"
"v_mfma_f32_16x16x32_bf16 v[72:75], v[144:147], a[244:247], v[72:75]\n"
"s_add_u32 s40, s56, s53\n"
"ds_read_b128 v[212:215], v229 offset:20480\n"
"v_mfma_f32_16x16x32_bf16 v[72:75], v[148:151], a[248:251], v[72:75]\n"
"buffer_load_dwordx4 v240, s[20:23], s40 offen offset:2048 nt lds\n"
"v_mfma_f32_16x16x32_bf16 v[76:79], v[148:151], v[224:227], v[76:79]\n"
"v_exp_f32 v163, v163\n"
"v_mfma_f32_16x16x32_bf16 v[76:79], v[144:147], a[252:255], v[76:79]\n"
"v_exp_f32 v156, v156\n"
"v_mfma_f32_16x16x32_bf16 v[44:47], v[140:143], v[224:227], v[44:47]\n"
"v_exp_f32 v160, v160\n"
"v_mfma_f32_16x16x32_bf16 v[44:47], v[136:139], a[252:255], v[44:47]\n"
"v_exp_f32 v155, v155\n"
"v_mfma_f32_16x16x32_bf16 v[12:15], v[132:135], v[224:227], v[12:15]\n"
"ds_read_b64_tr_b16 v[226:227], v239 offset:4608\n"
"v_mfma_f32_16x16x32_bf16 v[12:15], v[128:131], a[252:255], v[12:15]\n"
"ds_read_b64_tr_b16 a[248:249], v238 offset:512\n"
"v_mfma_f32_16x16x32_bf16 a[220:223], v[124:127], a[252:255], a[220:223]\n"
"v_exp_f32 v154, v154\n"
"v_smfmac_f32_16x16x64_bf16 v[196:199], a[240:243], v[128:135], v241\n"
"ds_read_b64_tr_b16 a[250:251], v238 offset:4608\n"
"v_mfma_f32_16x16x32_bf16 v[202:205], a[240:243], v[124:127], v[202:205]\n"
"s_waitcnt lgkmcnt(3)\n"
"v_exp_f32 v152, v152\n"
"v_mfma_f32_16x16x32_bf16 v[168:171], v[216:219], a[8:11], v[180:183]\n"
"ds_read_b64_tr_b16 a[244:245], v234 offset:512\n"
"v_cvt_pk_bf16_f32 v96, v152, v153\n"
"v_mfma_f32_16x16x32_bf16 v[168:171], v[220:223], a[12:15], v[168:171]\n"
"v_exp_f32 v165, v165\n"
"v_mfma_f32_16x16x32_bf16 v[168:171], v[208:211], a[0:3], v[168:171]\n"
"ds_read_b64_tr_b16 a[246:247], v234 offset:4608\n"
"v_cvt_pk_bf16_f32 v113, v162, v163\n"
"v_mfma_f32_16x16x32_bf16 v[168:171], v[212:215], a[4:7], v[168:171]\n"
"ds_read_b64_tr_b16 a[254:255], v235 offset:4608\n"
"v_mfma_f32_16x16x32_bf16 v[172:175], v[208:211], a[16:19], v[184:187]\n"
"ds_read_b64_tr_b16 v[224:225], v239 offset:512\n"
"v_cvt_pk_bf16_f32 v97, v154, v155\n"
"v_mfma_f32_16x16x32_bf16 v[172:175], v[212:215], a[20:23], v[172:175]\n"
"v_mfma_f32_16x16x32_bf16 v[172:175], v[216:219], a[24:27], v[172:175]\n"
"s_add_u32 s40, s56, s54\n"
"ds_read_b64_tr_b16 a[252:253], v235 offset:512\n"
"v_mfma_f32_16x16x32_bf16 v[172:175], v[220:223], a[28:31], v[172:175]\n"
"buffer_load_dwordx4 v207, s[20:23], s40 offen offset:3072 nt lds\n"
"v_mfma_f32_16x16x32_bf16 v[176:179], v[216:219], a[40:43], v[188:191]\n"
"s_add_u32 s55, s55, s59\n"
"v_exp_f32 v157, v157\n"
"v_mfma_f32_16x16x32_bf16 v[176:179], v[220:223], a[44:47], v[176:179]\n"
"s_or_b32 s55, s55, s58\n"
"v_exp_f32 v171, v171\n"
"v_mfma_f32_16x16x32_bf16 v[176:179], v[208:211], a[32:35], v[176:179]\n"
"s_xor_b32 s56, s55, s57\n"
"v_exp_f32 v159, v159\n"
"v_mfma_f32_16x16x32_bf16 v[176:179], v[212:215], a[36:39], v[176:179]\n"
"v_exp_f32 v167, v167\n"
"v_mfma_f32_16x16x32_bf16 v[152:155], v[208:211], a[192:195], v[192:195]\n"
"v_cvt_pk_bf16_f32 v105, v158, v159\n"
"v_mfma_f32_16x16x32_bf16 v[152:155], v[212:215], a[196:199], v[152:155]\n"
"ds_read_b128 v[208:211], v230 offset:16384\n"
"v_cvt_pk_bf16_f32 v104, v156, v157\n"
"v_mfma_f32_16x16x32_bf16 v[152:155], v[216:219], a[200:203], v[152:155]\n"
"v_exp_f32 v166, v166\n"
"v_mfma_f32_16x16x32_bf16 v[152:155], v[220:223], a[204:207], v[152:155]\n"
"s_waitcnt lgkmcnt(1)\n"
"ds_read_b128 v[216:219], v230 offset:16896\n"
"v_mfma_f32_16x16x32_bf16 a[232:235], v[124:127], a[244:247], a[232:235]\n"
"v_exp_f32 v169, v169\n"
"v_mfma_f32_16x16x32_bf16 v[24:27], v[128:131], a[244:247], v[24:27]\n"
"ds_read_b128 v[220:223], v231 offset:16896\n"
"v_mfma_f32_16x16x32_bf16 v[24:27], v[132:135], a[248:251], v[24:27]\n"
"v_exp_f32 v164, v164\n"
"v_mfma_f32_16x16x32_bf16 v[56:59], v[136:139], a[244:247], v[56:59]\n"
"ds_read_b128 v[212:215], v231 offset:16384\n"
"v_mfma_f32_16x16x32_bf16 v[56:59], v[140:143], a[248:251], v[56:59]\n"
"v_exp_f32 v178, v178\n"
"v_mfma_f32_16x16x32_bf16 v[88:91], v[144:147], a[244:247], v[88:91]\n"
"s_add_u32 m0, s44, 49152\n"
"ds_read_b64_tr_b16 a[246:247], v232 offset:4096\n"
"v_mfma_f32_16x16x32_bf16 v[88:91], v[148:151], a[248:251], v[88:91]\n"
"buffer_load_dwordx4 v240, s[16:19], s56 offen nt lds\n"
"v_mfma_f32_16x16x32_bf16 v[92:95], v[148:151], v[224:227], v[92:95]\n"
"v_exp_f32 v175, v175\n"
"v_mfma_f32_16x16x32_bf16 v[92:95], v[144:147], a[252:255], v[92:95]\n"
"v_exp_f32 v168, v168\n"
"v_mfma_f32_16x16x32_bf16 v[60:63], v[140:143], v[224:227], v[60:63]\n"
"v_exp_f32 v161, v161\n"
"v_mfma_f32_16x16x32_bf16 v[60:63], v[136:139], a[252:255], v[60:63]\n"
"v_exp_f32 v177, v177\n"
"v_mfma_f32_16x16x32_bf16 v[28:31], v[132:135], v[224:227], v[28:31]\n"
"ds_read_b64_tr_b16 v[226:227], v237 offset:4096\n"
"v_mfma_f32_16x16x32_bf16 v[28:31], v[128:131], a[252:255], v[28:31]\n"
"v_exp_f32 v172, v172\n"
"v_mfma_f32_16x16x32_bf16 a[236:239], v[124:127], a[252:255], a[236:239]\n"
"ds_read_b64_tr_b16 a[250:251], v236 offset:4096\n"
"v_cvt_pk_bf16_f32 v120, v164, v165\n"
"v_smfmac_f32_16x16x64_bf16 v[198:201], a[240:243], v[136:143], v241\n"
"s_waitcnt lgkmcnt(2)\n"
"ds_read_b64_tr_b16 a[252:253], v233 offset:0\n"
"v_mfma_f32_16x16x32_bf16 v[156:159], v[208:211], a[0:3], v[180:183]\n"
"ds_read_b64_tr_b16 a[248:249], v236 offset:0\n"
"v_cvt_pk_bf16_f32 v121, v166, v167\n"
"v_mfma_f32_16x16x32_bf16 v[156:159], v[212:215], a[4:7], v[156:159]\n"
"ds_read_b64_tr_b16 v[224:225], v237 offset:0\n"
"v_cvt_pk_bf16_f32 v98, v168, v169\n"
"v_mfma_f32_16x16x32_bf16 v[156:159], v[216:219], a[8:11], v[156:159]\n"
"ds_read_b64_tr_b16 a[254:255], v233 offset:4096\n"
"v_cvt_pk_bf16_f32 v112, v160, v161\n"
"v_mfma_f32_16x16x32_bf16 v[156:159], v[220:223], a[12:15], v[156:159]\n"
"ds_read_b64_tr_b16 a[244:245], v232 offset:0\n"
"v_mfma_f32_16x16x32_bf16 v[160:163], v[216:219], a[24:27], v[184:187]\n"
"v_exp_f32 v176, v176\n"
"v_mfma_f32_16x16x32_bf16 v[160:163], v[220:223], a[28:31], v[160:163]\n"
"v_exp_f32 v152, v152\n"
"v_mfma_f32_16x16x32_bf16 v[160:163], v[208:211], a[16:19], v[160:163]\n"
"s_add_u32 s40, s56, s52\n"
"v_cvt_pk_bf16_f32 v114, v176, v177\n"
"v_mfma_f32_16x16x32_bf16 v[160:163], v[212:215], a[20:23], v[160:163]\n"
"buffer_load_dwordx4 v207, s[16:19], s40 offen offset:1024 nt lds\n"
"v_mfma_f32_16x16x32_bf16 v[164:167], v[208:211], a[32:35], v[188:191]\n"
"v_exp_f32 v170, v170\n"
"v_mfma_f32_16x16x32_bf16 v[164:167], v[212:215], a[36:39], v[164:167]\n"
"v_exp_f32 v173, v173\n"
"v_mfma_f32_16x16x32_bf16 v[164:167], v[216:219], a[40:43], v[164:167]\n"
"v_exp_f32 v153, v153\n"
"v_mfma_f32_16x16x32_bf16 v[164:167], v[220:223], a[44:47], v[164:167]\n"
"s_waitcnt lgkmcnt(0)\n"
"v_exp_f32 v174, v174\n"
"v_mfma_f32_16x16x32_bf16 a[208:211], v[124:127], a[244:247], a[208:211]\n"
"ds_read_b128 v[208:211], v230 offset:20480\n"
"v_cvt_pk_bf16_f32 v107, v174, v175\n"
"v_mfma_f32_16x16x32_bf16 v[0:3], v[128:131], a[244:247], v[0:3]\n"
"v_exp_f32 v156, v156\n"
"v_mfma_f32_16x16x32_bf16 v[0:3], v[132:135], a[248:251], v[0:3]\n"
"ds_read_b128 v[220:223], v231 offset:20992\n"
"v_cvt_pk_bf16_f32 v106, v172, v173\n"
"v_mfma_f32_16x16x32_bf16 v[32:35], v[136:139], a[244:247], v[32:35]\n"
"v_exp_f32 v161, v161\n"
"v_mfma_f32_16x16x32_bf16 v[32:35], v[140:143], a[248:251], v[32:35]\n"
"ds_read_b128 v[216:219], v230 offset:20992\n"
"v_cvt_pk_bf16_f32 v122, v152, v153\n"
"v_mfma_f32_16x16x32_bf16 v[64:67], v[144:147], a[244:247], v[64:67]\n"
"s_add_u32 s40, s56, s53\n"
"ds_read_b128 v[212:215], v231 offset:20480\n"
"v_mfma_f32_16x16x32_bf16 v[64:67], v[148:151], a[248:251], v[64:67]\n"
"buffer_load_dwordx4 v240, s[16:19], s40 offen offset:2048 nt lds\n"
"v_mfma_f32_16x16x32_bf16 v[68:71], v[148:151], v[224:227], v[68:71]\n"
"v_exp_f32 v159, v159\n"
"v_mfma_f32_16x16x32_bf16 v[68:71], v[144:147], a[252:255], v[68:71]\n"
"v_exp_f32 v155, v155\n"
"v_mfma_f32_16x16x32_bf16 v[36:39], v[140:143], v[224:227], v[36:39]\n"
"v_exp_f32 v160, v160\n"
"v_mfma_f32_16x16x32_bf16 v[36:39], v[136:139], a[252:255], v[36:39]\n"
"v_exp_f32 v179, v179\n"
"v_mfma_f32_16x16x32_bf16 v[4:7], v[132:135], v[224:227], v[4:7]\n"
"ds_read_b64_tr_b16 a[244:245], v232 offset:512\n"
"ds_read_b64_tr_b16 v[224:225], v237 offset:512\n"
"v_mfma_f32_16x16x32_bf16 v[4:7], v[128:131], a[252:255], v[4:7]\n"
"v_exp_f32 v158, v158\n"
"v_mfma_f32_16x16x32_bf16 a[212:215], v[124:127], a[252:255], a[212:215]\n"
"ds_read_b64_tr_b16 a[246:247], v232 offset:4608\n"
"ds_read_b64_tr_b16 a[250:251], v236 offset:4608\n"
"v_smfmac_f32_16x16x64_bf16 v[200:203], a[240:243], v[144:151], v241\n"
"s_waitcnt lgkmcnt(4)\n"
"ds_read_b64_tr_b16 v[226:227], v237 offset:4608\n"
"v_cvt_pk_bf16_f32 v99, v170, v171\n"
"v_mfma_f32_16x16x32_bf16 v[168:171], v[208:211], a[0:3], v[180:183]\n"
"v_exp_f32 v164, v164\n"
"v_mfma_f32_16x16x32_bf16 v[168:171], v[212:215], a[4:7], v[168:171]\n"
"ds_read_b64_tr_b16 a[248:249], v236 offset:512\n"
"ds_read_b64_tr_b16 a[254:255], v233 offset:4608\n"
"v_mfma_f32_16x16x32_bf16 v[168:171], v[216:219], a[8:11], v[168:171]\n"
"s_add_u32 s40, s56, s54\n"
"ds_read_b64_tr_b16 a[252:253], v233 offset:512\n"
"v_mfma_f32_16x16x32_bf16 v[168:171], v[220:223], a[12:15], v[168:171]\n"
"buffer_load_dwordx4 v207, s[16:19], s40 offen offset:3072 nt lds\n"
"v_mfma_f32_16x16x32_bf16 v[172:175], v[216:219], a[24:27], v[184:187]\n"
"v_exp_f32 v154, v154\n"
"v_mfma_f32_16x16x32_bf16 v[172:175], v[220:223], a[28:31], v[172:175]\n"
"v_exp_f32 v167, v167\n"
"v_mfma_f32_16x16x32_bf16 v[172:175], v[208:211], a[16:19], v[172:175]\n"
"v_exp_f32 v163, v163\n"
"v_mfma_f32_16x16x32_bf16 v[172:175], v[212:215], a[20:23], v[172:175]\n"
"s_cbranch_vccnz LPCL1_%=\nLPKL1_%=:\n"
"v_cvt_pk_bf16_f32 v115, v178, v179\n"
"v_mfma_f32_16x16x32_bf16 v[176:179], v[208:211], a[32:35], v[188:191]\n"
"v_exp_f32 v165, v165\n"
"v_mfma_f32_16x16x32_bf16 v[176:179], v[212:215], a[36:39], v[176:179]\n"
"v_exp_f32 v157, v157\n"
"v_mfma_f32_16x16x32_bf16 v[176:179], v[216:219], a[40:43], v[176:179]\n"
"v_exp_f32 v166, v166\n"
"v_mfma_f32_16x16x32_bf16 v[176:179], v[220:223], a[44:47], v[176:179]\n"
"s_waitcnt lgkmcnt(0)\n"
"v_mfma_f32_16x16x32_bf16 a[224:227], v[124:127], a[244:247], a[224:227]\n"
"v_exp_f32 v162, v162\n"
"v_mfma_f32_16x16x32_bf16 v[16:19], v[128:131], a[244:247], v[16:19]\n"
"s_waitcnt vmcnt(8)\n"
"v_exp_f32 v168, v168\n"
"v_mfma_f32_16x16x32_bf16 v[16:19], v[132:135], a[248:251], v[16:19]\n"
"s_barrier\n"
"v_mfma_f32_16x16x32_bf16 v[48:51], v[136:139], a[244:247], v[48:51]\n"
"ds_read_b128 v[208:211], v228 offset:32768\n"
"v_cvt_pk_bf16_f32 v101, v158, v159\n"
"v_mfma_f32_16x16x32_bf16 v[48:51], v[140:143], a[248:251], v[48:51]\n"
"ds_read_b128 v[216:219], v228 offset:33280\n"
"v_cvt_pk_bf16_f32 v109, v162, v163\n"
"v_mfma_f32_16x16x32_bf16 v[80:83], v[144:147], a[244:247], v[80:83]\n"
"ds_read_b128 v[220:223], v229 offset:33280\n"
"v_cvt_pk_bf16_f32 v123, v154, v155\n"
"v_mfma_f32_16x16x32_bf16 v[80:83], v[148:151], a[248:251], v[80:83]\n"
"ds_read_b128 v[212:215], v229 offset:32768\n"
"v_cvt_pk_bf16_f32 v117, v166, v167\n"
"v_mfma_f32_16x16x32_bf16 v[84:87], v[148:151], v[224:227], v[84:87]\n"
"v_exp_f32 v176, v176\n"
"v_mfma_f32_16x16x32_bf16 v[84:87], v[144:147], a[252:255], v[84:87]\n"
"v_exp_f32 v169, v169\n"
"v_mfma_f32_16x16x32_bf16 v[52:55], v[140:143], v[224:227], v[52:55]\n"
"ds_read_b64_tr_b16 a[246:247], v234 offset:20480\n"
"v_cvt_pk_bf16_f32 v116, v164, v165\n"
"v_mfma_f32_16x16x32_bf16 v[52:55], v[136:139], a[252:255], v[52:55]\n"
"v_exp_f32 v177, v177\n"
"v_mfma_f32_16x16x32_bf16 v[20:23], v[132:135], v[224:227], v[20:23]\n"
"ds_read_b64_tr_b16 a[250:251], v238 offset:20480\n"
"v_cvt_pk_bf16_f32 v118, v176, v177\n"
"v_mfma_f32_16x16x32_bf16 v[20:23], v[128:131], a[252:255], v[20:23]\n"
"v_exp_f32 v174, v174\n"
"v_mfma_f32_16x16x32_bf16 a[228:231], v[124:127], a[252:255], a[228:231]\n"
"s_waitcnt lgkmcnt(3)\n"
"ds_read_b64_tr_b16 a[254:255], v235 offset:20480\n"
"v_cvt_pk_bf16_f32 v100, v156, v157\n"
"LAY2_%=:\n"
"v_mfma_f32_16x16x32_bf16 v[152:155], v[208:211], a[0:3], v[180:183]\n"
"s_waitcnt lgkmcnt(3)\n"
"ds_read_b64_tr_b16 a[248:249], v238 offset:16384\n"
"v_cvt_pk_bf16_f32 v102, v168, v169\n"
"v_mfma_f32_16x16x32_bf16 v[152:155], v[212:215], a[4:7], v[152:155]\n"
"v_exp_f32 v172, v172\n"
"v_mfma_f32_16x16x32_bf16 v[152:155], v[216:219], a[8:11], v[152:155]\n"
"ds_read_b64_tr_b16 a[244:245], v234 offset:16384\n"
"v_cvt_pk_bf16_f32 v108, v160, v161\n"
"v_mfma_f32_16x16x32_bf16 v[152:155], v[220:223], a[12:15], v[152:155]\n"
"v_exp_f32 v175, v175\n"
"v_mfma_f32_16x16x32_bf16 v[156:159], v[216:219], a[24:27], v[184:187]\n"
"ds_read_b64_tr_b16 a[252:253], v235 offset:16384\n"
"v_cvt_pk_bf16_f32 v111, v174, v175\n"
"v_mfma_f32_16x16x32_bf16 v[156:159], v[220:223], a[28:31], v[156:159]\n"
"ds_read_b64_tr_b16 v[224:225], v239 offset:16384\n"
"v_mfma_f32_16x16x32_bf16 v[156:159], v[208:211], a[16:19], v[156:159]\n"
"s_add_u32 m0, s44, 114688\n"
"ds_read_b64_tr_b16 v[226:227], v239 offset:20480\n"
"v_mfma_f32_16x16x32_bf16 v[156:159], v[212:215], a[20:23], v[156:159]\n"
"buffer_load_dwordx4 v240, s[20:23], s56 offen nt lds\n"
"v_mfma_f32_16x16x32_bf16 v[160:163], v[208:211], a[32:35], v[188:191]\n"
"v_exp_f32 v178, v178\n"
"v_mfma_f32_16x16x32_bf16 v[160:163], v[212:215], a[36:39], v[160:163]\n"
"v_exp_f32 v171, v171\n"
"v_mfma_f32_16x16x32_bf16 v[160:163], v[216:219], a[40:43], v[160:163]\n"
"s_add_u32 s40, s56, s52\n"
"v_mfma_f32_16x16x32_bf16 v[160:163], v[220:223], a[44:47], v[160:163]\n"
"buffer_load_dwordx4 v207, s[20:23], s40 offen offset:1024 nt lds\n"
"v_mfma_f32_16x16x32_bf16 v[164:167], v[216:219], a[200:203], v[192:195]\n"
"v_exp_f32 v170, v170\n"
"v_mfma_f32_16x16x32_bf16 v[164:167], v[220:223], a[204:207], v[164:167]\n"
"v_exp_f32 v153, v153\n"
"v_mfma_f32_16x16x32_bf16 v[164:167], v[208:211], a[192:195], v[164:167]\n"
"v_exp_f32 v179, v179\n"
"v_mfma_f32_16x16x32_bf16 v[164:167], v[212:215], a[196:199], v[164:167]\n"
"s_waitcnt lgkmcnt(0)\n"
"v_exp_f32 v158, v158\n"
"v_mfma_f32_16x16x32_bf16 a[216:219], v[120:123], a[244:247], a[216:219]\n"
"ds_read_b128 v[216:219], v228 offset:37376\n"
"v_cvt_pk_bf16_f32 v103, v170, v171\n"
"v_mfma_f32_16x16x32_bf16 v[8:11], v[96:99], a[244:247], v[8:11]\n"
"v_exp_f32 v173, v173\n"
"v_mfma_f32_16x16x32_bf16 v[8:11], v[100:103], a[248:251], v[8:11]\n"
"ds_read_b128 v[208:211], v228 offset:36864\n"
"v_cvt_pk_bf16_f32 v110, v172, v173\n"
"v_mfma_f32_16x16x32_bf16 v[40:43], v[104:107], a[244:247], v[40:43]\n"
"v_exp_f32 v162, v162\n"
"v_mfma_f32_16x16x32_bf16 v[40:43], v[108:111], a[248:251], v[40:43]\n"
"ds_read_b128 v[220:223], v229 offset:37376\n"
"v_cvt_pk_bf16_f32 v119, v178, v179\n"
"v_mfma_f32_16x16x32_bf16 v[72:75], v[112:115], a[244:247], v[72:75]\n"
"s_add_u32 s40, s56, s53\n"
"ds_read_b128 v[212:215], v229 offset:36864\n"
"v_mfma_f32_16x16x32_bf16 v[72:75], v[116:119], a[248:251], v[72:75]\n"
"buffer_load_dwordx4 v240, s[20:23], s40 offen offset:2048 nt lds\n"
"v_mfma_f32_16x16x32_bf16 v[76:79], v[116:119], v[224:227], v[76:79]\n"
"v_exp_f32 v163, v163\n"
"v_mfma_f32_16x16x32_bf16 v[76:79], v[112:115], a[252:255], v[76:79]\n"
"v_exp_f32 v156, v156\n"
"v_mfma_f32_16x16x32_bf16 v[44:47], v[108:111], v[224:227], v[44:47]\n"
"v_exp_f32 v160, v160\n"
"v_mfma_f32_16x16x32_bf16 v[44:47], v[104:107], a[252:255], v[44:47]\n"
"v_exp_f32 v155, v155\n"
"v_mfma_f32_16x16x32_bf16 v[12:15], v[100:103], v[224:227], v[12:15]\n"
"ds_read_b64_tr_b16 v[226:227], v239 offset:20992\n"
"v_mfma_f32_16x16x32_bf16 v[12:15], v[96:99], a[252:255], v[12:15]\n"
"ds_read_b64_tr_b16 a[248:249], v238 offset:16896\n"
"v_mfma_f32_16x16x32_bf16 a[220:223], v[120:123], a[252:255], a[220:223]\n"
"v_exp_f32 v154, v154\n"
"v_smfmac_f32_16x16x64_bf16 v[196:199], a[240:243], v[96:103], v241\n"
"ds_read_b64_tr_b16 a[250:251], v238 offset:20992\n"
"v_mfma_f32_16x16x32_bf16 v[202:205], a[240:243], v[120:123], v[202:205]\n"
"s_waitcnt lgkmcnt(3)\n"
"v_exp_f32 v152, v152\n"
"v_mfma_f32_16x16x32_bf16 v[168:171], v[216:219], a[8:11], v[180:183]\n"
"ds_read_b64_tr_b16 a[244:245], v234 offset:16896\n"
"v_cvt_pk_bf16_f32 v128, v152, v153\n"
"v_mfma_f32_16x16x32_bf16 v[168:171], v[220:223], a[12:15], v[168:171]\n"
"v_exp_f32 v165, v165\n"
"v_mfma_f32_16x16x32_bf16 v[168:171], v[208:211], a[0:3], v[168:171]\n"
"ds_read_b64_tr_b16 a[246:247], v234 offset:20992\n"
"v_cvt_pk_bf16_f32 v145, v162, v163\n"
"v_mfma_f32_16x16x32_bf16 v[168:171], v[212:215], a[4:7], v[168:171]\n"
"ds_read_b64_tr_b16 a[254:255], v235 offset:20992\n"
"v_mfma_f32_16x16x32_bf16 v[172:175], v[208:211], a[16:19], v[184:187]\n"
"ds_read_b64_tr_b16 v[224:225], v239 offset:16896\n"
"v_cvt_pk_bf16_f32 v129, v154, v155\n"
"v_mfma_f32_16x16x32_bf16 v[172:175], v[212:215], a[20:23], v[172:175]\n"
"v_mfma_f32_16x16x32_bf16 v[172:175], v[216:219], a[24:27], v[172:175]\n"
"s_add_u32 s40, s56, s54\n"
"ds_read_b64_tr_b16 a[252:253], v235 offset:16896\n"
"v_mfma_f32_16x16x32_bf16 v[172:175], v[220:223], a[28:31], v[172:175]\n"
"buffer_load_dwordx4 v207, s[20:23], s40 offen offset:3072 nt lds\n"
"v_mfma_f32_16x16x32_bf16 v[176:179], v[216:219], a[40:43], v[188:191]\n"
"s_add_u32 s55, s55, s59\n"
"v_exp_f32 v157, v157\n"
"v_mfma_f32_16x16x32_bf16 v[176:179], v[220:223], a[44:47], v[176:179]\n"
"s_or_b32 s55, s55, s58\n"
"v_exp_f32 v171, v171\n"
"v_mfma_f32_16x16x32_bf16 v[176:179], v[208:211], a[32:35], v[176:179]\n"
"s_xor_b32 s56, s55, s57\n"
"v_exp_f32 v159, v159\n"
"v_mfma_f32_16x16x32_bf16 v[176:179], v[212:215], a[36:39], v[176:179]\n"
"s_cmp_ge_u32 s47, 15\n"
"v_exp_f32 v167, v167\n"
"v_mfma_f32_16x16x32_bf16 v[152:155], v[208:211], a[192:195], v[192:195]\n"
"s_cbranch_scc1 LSW_%=\nLSWK_%=:\n"
"v_cvt_pk_bf16_f32 v137, v158, v159\n"
"v_mfma_f32_16x16x32_bf16 v[152:155], v[212:215], a[196:199], v[152:155]\n"
"ds_read_b128 v[208:211], v230 offset:32768\n"
"v_cvt_pk_bf16_f32 v136, v156, v157\n"
"v_mfma_f32_16x16x32_bf16 v[152:155], v[216:219], a[200:203], v[152:155]\n"
"v_exp_f32 v166, v166\n"
"v_mfma_f32_16x16x32_bf16 v[152:155], v[220:223], a[204:207], v[152:155]\n"
"s_waitcnt lgkmcnt(1)\n"
"ds_read_b128 v[216:219], v230 offset:33280\n"
"v_mfma_f32_16x16x32_bf16 a[232:235], v[120:123], a[244:247], a[232:235]\n"
"v_exp_f32 v169, v169\n"
"v_mfma_f32_16x16x32_bf16 v[24:27], v[96:99], a[244:247], v[24:27]\n"
"ds_read_b128 v[220:223], v231 offset:33280\n"
"v_mfma_f32_16x16x32_bf16 v[24:27], v[100:103], a[248:251], v[24:27]\n"
"v_exp_f32 v164, v164\n"
"v_mfma_f32_16x16x32_bf16 v[56:59], v[104:107], a[244:247], v[56:59]\n"
"ds_read_b128 v[212:215], v231 offset:32768\n"
"v_mfma_f32_16x16x32_bf16 v[56:59], v[108:111], a[248:251], v[56:59]\n"
"v_exp_f32 v178, v178\n"
"v_mfma_f32_16x16x32_bf16 v[88:91], v[112:115], a[244:247], v[88:91]\n"
"s_mov_b32 m0, s44\n"
"ds_read_b64_tr_b16 a[246:247], v232 offset:20480\n"
"v_mfma_f32_16x16x32_bf16 v[88:91], v[116:119], a[248:251], v[88:91]\n"
"buffer_load_dwordx4 v240, s[16:19], s56 offen nt lds\n"
"v_mfma_f32_16x16x32_bf16 v[92:95], v[116:119], v[224:227], v[92:95]\n"
"v_exp_f32 v175, v175\n"
"v_mfma_f32_16x16x32_bf16 v[92:95], v[112:115], a[252:255], v[92:95]\n"
"v_exp_f32 v168, v168\n"
"v_mfma_f32_16x16x32_bf16 v[60:63], v[108:111], v[224:227], v[60:63]\n"
"v_exp_f32 v161, v161\n"
"v_mfma_f32_16x16x32_bf16 v[60:63], v[104:107], a[252:255], v[60:63]\n"
"v_exp_f32 v177, v177\n"
"v_mfma_f32_16x16x32_bf16 v[28:31], v[100:103], v[224:227], v[28:31]\n"
"ds_read_b64_tr_b16 v[226:227], v237 offset:20480\n"
"v_mfma_f32_16x16x32_bf16 v[28:31], v[96:99], a[252:255], v[28:31]\n"
"v_exp_f32 v172, v172\n"
"v_mfma_f32_16x16x32_bf16 a[236:239], v[120:123], a[252:255], a[236:239]\n"
"ds_read_b64_tr_b16 a[250:251], v236 offset:20480\n"
"v_cvt_pk_bf16_f32 v124, v164, v165\n"
"v_smfmac_f32_16x16x64_bf16 v[198:201], a[240:243], v[104:111], v241\n"
"s_waitcnt lgkmcnt(2)\n"
"ds_read_b64_tr_b16 a[252:253], v233 offset:16384\n"
"v_mfma_f32_16x16x32_bf16 v[156:159], v[208:211], a[0:3], v[180:183]\n"
"ds_read_b64_tr_b16 a[248:249], v236 offset:16384\n"
"v_cvt_pk_bf16_f32 v125, v166, v167\n"
"v_mfma_f32_16x16x32_bf16 v[156:159], v[212:215], a[4:7], v[156:159]\n"
"ds_read_b64_tr_b16 v[224:225], v237 offset:16384\n"
"v_cvt_pk_bf16_f32 v130, v168, v169\n"
"v_mfma_f32_16x16x32_bf16 v[156:159], v[216:219], a[8:11], v[156:159]\n"
"ds_read_b64_tr_b16 a[254:255], v233 offset:20480\n"
"v_cvt_pk_bf16_f32 v144, v160, v161\n"
"v_mfma_f32_16x16x32_bf16 v[156:159], v[220:223], a[12:15], v[156:159]\n"
"ds_read_b64_tr_b16 a[244:245], v232 offset:16384\n"
"v_mfma_f32_16x16x32_bf16 v[160:163], v[216:219], a[24:27], v[184:187]\n"
"v_exp_f32 v176, v176\n"
"v_mfma_f32_16x16x32_bf16 v[160:163], v[220:223], a[28:31], v[160:163]\n"
"v_exp_f32 v152, v152\n"
"v_mfma_f32_16x16x32_bf16 v[160:163], v[208:211], a[16:19], v[160:163]\n"
"s_add_u32 s40, s56, s52\n"
"v_cvt_pk_bf16_f32 v146, v176, v177\n"
"v_mfma_f32_16x16x32_bf16 v[160:163], v[212:215], a[20:23], v[160:163]\n"
"buffer_load_dwordx4 v207, s[16:19], s40 offen offset:1024 nt lds\n"
"v_mfma_f32_16x16x32_bf16 v[164:167], v[208:211], a[32:35], v[188:191]\n"
"v_exp_f32 v170, v170\n"
"v_mfma_f32_16x16x32_bf16 v[164:167], v[212:215], a[36:39], v[164:167]\n"
"v_exp_f32 v173, v173\n"
"v_mfma_f32_16x16x32_bf16 v[164:167], v[216:219], a[40:43], v[164:167]\n"
"v_exp_f32 v153, v153\n"
"v_mfma_f32_16x16x32_bf16 v[164:167], v[220:223], a[44:47], v[164:167]\n"
"s_waitcnt lgkmcnt(0)\n"
"v_exp_f32 v174, v174\n"
"v_mfma_f32_16x16x32_bf16 a[208:211], v[120:123], a[244:247], a[208:211]\n"
"ds_read_b128 v[208:211], v230 offset:36864\n"
"v_cvt_pk_bf16_f32 v139, v174, v175\n"
"v_mfma_f32_16x16x32_bf16 v[0:3], v[96:99], a[244:247], v[0:3]\n"
"v_exp_f32 v156, v156\n"
"v_mfma_f32_16x16x32_bf16 v[0:3], v[100:103], a[248:251], v[0:3]\n"
"ds_read_b128 v[220:223], v231 offset:37376\n"
"v_cvt_pk_bf16_f32 v138, v172, v173\n"
"v_mfma_f32_16x16x32_bf16 v[32:35], v[104:107], a[244:247], v[32:35]\n"
"v_exp_f32 v161, v161\n"
"v_mfma_f32_16x16x32_bf16 v[32:35], v[108:111], a[248:251], v[32:35]\n"
"ds_read_b128 v[216:219], v230 offset:37376\n"
"v_cvt_pk_bf16_f32 v126, v152, v153\n"
"v_mfma_f32_16x16x32_bf16 v[64:67], v[112:115], a[244:247], v[64:67]\n"
"s_add_u32 s40, s56, s53\n"
"ds_read_b128 v[212:215], v231 offset:36864\n"
"v_mfma_f32_16x16x32_bf16 v[64:67], v[116:119], a[248:251], v[64:67]\n"
"buffer_load_dwordx4 v240, s[16:19], s40 offen offset:2048 nt lds\n"
"v_mfma_f32_16x16x32_bf16 v[68:71], v[116:119], v[224:227], v[68:71]\n"
"v_exp_f32 v159, v159\n"
"v_mfma_f32_16x16x32_bf16 v[68:71], v[112:115], a[252:255], v[68:71]\n"
"v_exp_f32 v155, v155\n"
"v_mfma_f32_16x16x32_bf16 v[36:39], v[108:111], v[224:227], v[36:39]\n"
"v_exp_f32 v160, v160\n"
"v_mfma_f32_16x16x32_bf16 v[36:39], v[104:107], a[252:255], v[36:39]\n"
"v_exp_f32 v179, v179\n"
"v_mfma_f32_16x16x32_bf16 v[4:7], v[100:103], v[224:227], v[4:7]\n"
"ds_read_b64_tr_b16 a[244:245], v232 offset:16896\n"
"ds_read_b64_tr_b16 v[224:225], v237 offset:16896\n"
"v_mfma_f32_16x16x32_bf16 v[4:7], v[96:99], a[252:255], v[4:7]\n"
"v_exp_f32 v158, v158\n"
"v_mfma_f32_16x16x32_bf16 a[212:215], v[120:123], a[252:255], a[212:215]\n"
"ds_read_b64_tr_b16 a[246:247], v232 offset:20992\n"
"ds_read_b64_tr_b16 a[250:251], v236 offset:20992\n"
"v_smfmac_f32_16x16x64_bf16 v[200:203], a[240:243], v[112:119], v241\n"
"s_waitcnt lgkmcnt(4)\n"
"ds_read_b64_tr_b16 v[226:227], v237 offset:20992\n"
"v_cvt_pk_bf16_f32 v131, v170, v171\n"
"v_mfma_f32_16x16x32_bf16 v[168:171], v[208:211], a[0:3], v[180:183]\n"
"v_exp_f32 v164, v164\n"
"v_mfma_f32_16x16x32_bf16 v[168:171], v[212:215], a[4:7], v[168:171]\n"
"ds_read_b64_tr_b16 a[248:249], v236 offset:16896\n"
"ds_read_b64_tr_b16 a[254:255], v233 offset:20992\n"
"v_mfma_f32_16x16x32_bf16 v[168:171], v[216:219], a[8:11], v[168:171]\n"
"s_add_u32 s40, s56, s54\n"
"ds_read_b64_tr_b16 a[252:253], v233 offset:16896\n"
"v_mfma_f32_16x16x32_bf16 v[168:171], v[220:223], a[12:15], v[168:171]\n"
"buffer_load_dwordx4 v207, s[16:19], s40 offen offset:3072 nt lds\n"
"v_mfma_f32_16x16x32_bf16 v[172:175], v[216:219], a[24:27], v[184:187]\n"
"v_exp_f32 v154, v154\n"
"v_mfma_f32_16x16x32_bf16 v[172:175], v[220:223], a[28:31], v[172:175]\n"
"v_exp_f32 v167, v167\n"
"v_mfma_f32_16x16x32_bf16 v[172:175], v[208:211], a[16:19], v[172:175]\n"
"v_exp_f32 v163, v163\n"
"v_mfma_f32_16x16x32_bf16 v[172:175], v[212:215], a[20:23], v[172:175]\n"
"s_cbranch_vccnz LPCL2_%=\nLPKL2_%=:\n"
"v_cvt_pk_bf16_f32 v147, v178, v179\n"
"v_mfma_f32_16x16x32_bf16 v[176:179], v[208:211], a[32:35], v[188:191]\n"
"v_exp_f32 v165, v165\n"
"v_mfma_f32_16x16x32_bf16 v[176:179], v[212:215], a[36:39], v[176:179]\n"
"v_exp_f32 v157, v157\n"
"v_mfma_f32_16x16x32_bf16 v[176:179], v[216:219], a[40:43], v[176:179]\n"
"v_exp_f32 v166, v166\n"
"v_mfma_f32_16x16x32_bf16 v[176:179], v[220:223], a[44:47], v[176:179]\n"
"s_waitcnt lgkmcnt(0)\n"
"v_mfma_f32_16x16x32_bf16 a[224:227], v[120:123], a[244:247], a[224:227]\n"
"v_exp_f32 v162, v162\n"
"v_mfma_f32_16x16x32_bf16 v[16:19], v[96:99], a[244:247], v[16:19]\n"
"s_waitcnt vmcnt(8)\n"
"v_exp_f32 v168, v168\n"
"v_mfma_f32_16x16x32_bf16 v[16:19], v[100:103], a[248:251], v[16:19]\n"
"s_barrier\n"
"v_mfma_f32_16x16x32_bf16 v[48:51], v[104:107], a[244:247], v[48:51]\n"
"ds_read_b128 v[208:211], v228 offset:49152\n"
"v_cvt_pk_bf16_f32 v133, v158, v159\n"
"v_mfma_f32_16x16x32_bf16 v[48:51], v[108:111], a[248:251], v[48:51]\n"
"ds_read_b128 v[216:219], v228 offset:49664\n"
"v_cvt_pk_bf16_f32 v141, v162, v163\n"
"v_mfma_f32_16x16x32_bf16 v[80:83], v[112:115], a[244:247], v[80:83]\n"
"ds_read_b128 v[220:223], v229 offset:49664\n"
"v_cvt_pk_bf16_f32 v127, v154, v155\n"
"v_mfma_f32_16x16x32_bf16 v[80:83], v[116:119], a[248:251], v[80:83]\n"
"ds_read_b128 v[212:215], v229 offset:49152\n"
"v_cvt_pk_bf16_f32 v149, v166, v167\n"
"v_mfma_f32_16x16x32_bf16 v[84:87], v[116:119], v[224:227], v[84:87]\n"
"v_exp_f32 v176, v176\n"
"v_mfma_f32_16x16x32_bf16 v[84:87], v[112:115], a[252:255], v[84:87]\n"
"v_exp_f32 v169, v169\n"
"v_mfma_f32_16x16x32_bf16 v[52:55], v[108:111], v[224:227], v[52:55]\n"
"ds_read_b64_tr_b16 a[246:247], v234 offset:36864\n"
"v_cvt_pk_bf16_f32 v148, v164, v165\n"
"v_mfma_f32_16x16x32_bf16 v[52:55], v[104:107], a[252:255], v[52:55]\n"
"v_exp_f32 v177, v177\n"
"v_mfma_f32_16x16x32_bf16 v[20:23], v[100:103], v[224:227], v[20:23]\n"
"ds_read_b64_tr_b16 a[250:251], v238 offset:36864\n"
"v_cvt_pk_bf16_f32 v150, v176, v177\n"
"v_mfma_f32_16x16x32_bf16 v[20:23], v[96:99], a[252:255], v[20:23]\n"
"v_exp_f32 v174, v174\n"
"v_mfma_f32_16x16x32_bf16 a[228:231], v[120:123], a[252:255], a[228:231]\n"
"s_waitcnt lgkmcnt(3)\n"
"ds_read_b64_tr_b16 a[254:255], v235 offset:36864\n"
"v_cvt_pk_bf16_f32 v132, v156, v157\n"
"v_mfma_f32_16x16x32_bf16 v[152:155], v[208:211], a[0:3], v[180:183]\n"
"s_waitcnt lgkmcnt(3)\n"
"ds_read_b64_tr_b16 a[248:249], v238 offset:32768\n"
"v_cvt_pk_bf16_f32 v134, v168, v169\n"
"v_mfma_f32_16x16x32_bf16 v[152:155], v[212:215], a[4:7], v[152:155]\n"
"v_exp_f32 v172, v172\n"
"v_mfma_f32_16x16x32_bf16 v[152:155], v[216:219], a[8:11], v[152:155]\n"
"ds_read_b64_tr_b16 a[244:245], v234 offset:32768\n"
"v_cvt_pk_bf16_f32 v140, v160, v161\n"
"v_mfma_f32_16x16x32_bf16 v[152:155], v[220:223], a[12:15], v[152:155]\n"
"v_exp_f32 v175, v175\n"
"v_mfma_f32_16x16x32_bf16 v[156:159], v[216:219], a[24:27], v[184:187]\n"
"ds_read_b64_tr_b16 a[252:253], v235 offset:32768\n"
"v_cvt_pk_bf16_f32 v143, v174, v175\n"
"v_mfma_f32_16x16x32_bf16 v[156:159], v[220:223], a[28:31], v[156:159]\n"
"ds_read_b64_tr_b16 v[224:225], v239 offset:32768\n"
"v_mfma_f32_16x16x32_bf16 v[156:159], v[208:211], a[16:19], v[156:159]\n"
"s_add_u32 m0, s44, 65536\n"
"ds_read_b64_tr_b16 v[226:227], v239 offset:36864\n"
"v_mfma_f32_16x16x32_bf16 v[156:159], v[212:215], a[20:23], v[156:159]\n"
"buffer_load_dwordx4 v240, s[20:23], s56 offen nt lds\n"
"v_mfma_f32_16x16x32_bf16 v[160:163], v[208:211], a[32:35], v[188:191]\n"
"v_exp_f32 v178, v178\n"
"v_mfma_f32_16x16x32_bf16 v[160:163], v[212:215], a[36:39], v[160:163]\n"
"v_exp_f32 v171, v171\n"
"v_mfma_f32_16x16x32_bf16 v[160:163], v[216:219], a[40:43], v[160:163]\n"
"s_add_u32 s40, s56, s52\n"
"v_mfma_f32_16x16x32_bf16 v[160:163], v[220:223], a[44:47], v[160:163]\n"
"buffer_load_dwordx4 v207, s[20:23], s40 offen offset:1024 nt lds\n"
"v_mfma_f32_16x16x32_bf16 v[164:167], v[216:219], a[200:203], v[192:195]\n"
"v_exp_f32 v170, v170\n"
"v_mfma_f32_16x16x32_bf16 v[164:167], v[220:223], a[204:207], v[164:167]\n"
"v_exp_f32 v153, v153\n"
"v_mfma_f32_16x16x32_bf16 v[164:167], v[208:211], a[192:195], v[164:167]\n"
"v_exp_f32 v179, v179\n"
"v_mfma_f32_16x16x32_bf16 v[164:167], v[212:215], a[196:199], v[164:167]\n"
"s_waitcnt lgkmcnt(0)\n"
"v_exp_f32 v158, v158\n"
"v_mfma_f32_16x16x32_bf16 a[216:219], v[124:127], a[244:247], a[216:219]\n"
"ds_read_b128 v[216:219], v228 offset:53760\n"
"v_cvt_pk_bf16_f32 v135, v170, v171\n"
"v_mfma_f32_16x16x32_bf16 v[8:11], v[128:131], a[244:247], v[8:11]\n"
"v_exp_f32 v173, v173\n"
"v_mfma_f32_16x16x32_bf16 v[8:11], v[132:135], a[248:251], v[8:11]\n"
"ds_read_b128 v[208:211], v228 offset:53248\n"
"v_cvt_pk_bf16_f32 v142, v172, v173\n"
"v_mfma_f32_16x16x32_bf16 v[40:43], v[136:139], a[244:247], v[40:43]\n"
"v_exp_f32 v162, v162\n"
"v_mfma_f32_16x16x32_bf16 v[40:43], v[140:143], a[248:251], v[40:43]\n"
"ds_read_b128 v[220:223], v229 offset:53760\n"
"v_cvt_pk_bf16_f32 v151, v178, v179\n"
"v_mfma_f32_16x16x32_bf16 v[72:75], v[144:147], a[244:247], v[72:75]\n"
"s_add_u32 s40, s56, s53\n"
"ds_read_b128 v[212:215], v229 offset:53248\n"
"v_mfma_f32_16x16x32_bf16 v[72:75], v[148:151], a[248:251], v[72:75]\n"
"buffer_load_dwordx4 v240, s[20:23], s40 offen offset:2048 nt lds\n"
"v_mfma_f32_16x16x32_bf16 v[76:79], v[148:151], v[224:227], v[76:79]\n"
"v_exp_f32 v163, v163\n"
"v_mfma_f32_16x16x32_bf16 v[76:79], v[144:147], a[252:255], v[76:79]\n"
"v_exp_f32 v156, v156\n"
"v_mfma_f32_16x16x32_bf16 v[44:47], v[140:143], v[224:227], v[44:47]\n"
"v_exp_f32 v160, v160\n"
"v_mfma_f32_16x16x32_bf16 v[44:47], v[136:139], a[252:255], v[44:47]\n"
"v_exp_f32 v155, v155\n"
"v_mfma_f32_16x16x32_bf16 v[12:15], v[132:135], v[224:227], v[12:15]\n"
"ds_read_b64_tr_b16 v[226:227], v239 offset:37376\n"
"v_mfma_f32_16x16x32_bf16 v[12:15], v[128:131], a[252:255], v[12:15]\n"
"ds_read_b64_tr_b16 a[248:249], v238 offset:33280\n"
"v_mfma_f32_16x16x32_bf16 a[220:223], v[124:127], a[252:255], a[220:223]\n"
"v_exp_f32 v154, v154\n"
"v_smfmac_f32_16x16x64_bf16 v[196:199], a[240:243], v[128:135], v241\n"
"ds_read_b64_tr_b16 a[250:251], v238 offset:37376\n"
"v_mfma_f32_16x16x32_bf16 v[202:205], a[240:243], v[124:127], v[202:205]\n"
"s_waitcnt lgkmcnt(3)\n"
"v_exp_f32 v152, v152\n"
"v_mfma_f32_16x16x32_bf16 v[168:171], v[216:219], a[8:11], v[180:183]\n"
"ds_read_b64_tr_b16 a[244:245], v234 offset:33280\n"
"v_cvt_pk_bf16_f32 v96, v152, v153\n"
"v_mfma_f32_16x16x32_bf16 v[168:171], v[220:223], a[12:15], v[168:171]\n"
"v_exp_f32 v165, v165\n"
"v_mfma_f32_16x16x32_bf16 v[168:171], v[208:211], a[0:3], v[168:171]\n"
"ds_read_b64_tr_b16 a[246:247], v234 offset:37376\n"
"v_cvt_pk_bf16_f32 v113, v162, v163\n"
"v_mfma_f32_16x16x32_bf16 v[168:171], v[212:215], a[4:7], v[168:171]\n"
"ds_read_b64_tr_b16 a[254:255], v235 offset:37376\n"
"v_mfma_f32_16x16x32_bf16 v[172:175], v[208:211], a[16:19], v[184:187]\n"
"ds_read_b64_tr_b16 v[224:225], v239 offset:33280\n"
"v_cvt_pk_bf16_f32 v97, v154, v155\n"
"v_mfma_f32_16x16x32_bf16 v[172:175], v[212:215], a[20:23], v[172:175]\n"
"v_mfma_f32_16x16x32_bf16 v[172:175], v[216:219], a[24:27], v[172:175]\n"
"s_add_u32 s40, s56, s54\n"
"ds_read_b64_tr_b16 a[252:253], v235 offset:33280\n"
"v_mfma_f32_16x16x32_bf16 v[172:175], v[220:223], a[28:31], v[172:175]\n"
"buffer_load_dwordx4 v207, s[20:23], s40 offen offset:3072 nt lds\n"
"v_mfma_f32_16x16x32_bf16 v[176:179], v[216:219], a[40:43], v[188:191]\n"
"s_add_u32 s55, s55, s59\n"
"v_exp_f32 v157, v157\n"
"v_mfma_f32_16x16x32_bf16 v[176:179], v[220:223], a[44:47], v[176:179]\n"
"s_or_b32 s55, s55, s58\n"
"v_exp_f32 v171, v171\n"
"v_mfma_f32_16x16x32_bf16 v[176:179], v[208:211], a[32:35], v[176:179]\n"
"s_xor_b32 s56, s55, s57\n"
"v_exp_f32 v159, v159\n"
"v_mfma_f32_16x16x32_bf16 v[176:179], v[212:215], a[36:39], v[176:179]\n"
"v_exp_f32 v167, v167\n"
"v_mfma_f32_16x16x32_bf16 v[152:155], v[208:211], a[192:195], v[192:195]\n"
"v_cvt_pk_bf16_f32 v105, v158, v159\n"
"v_mfma_f32_16x16x32_bf16 v[152:155], v[212:215], a[196:199], v[152:155]\n"
"ds_read_b128 v[208:211], v230 offset:49152\n"
"v_cvt_pk_bf16_f32 v104, v156, v157\n"
"v_mfma_f32_16x16x32_bf16 v[152:155], v[216:219], a[200:203], v[152:155]\n"
"v_exp_f32 v166, v166\n"
"v_mfma_f32_16x16x32_bf16 v[152:155], v[220:223], a[204:207], v[152:155]\n"
"s_waitcnt lgkmcnt(1)\n"
"ds_read_b128 v[216:219], v230 offset:49664\n"
"v_mfma_f32_16x16x32_bf16 a[232:235], v[124:127], a[244:247], a[232:235]\n"
"v_exp_f32 v169, v169\n"
"v_mfma_f32_16x16x32_bf16 v[24:27], v[128:131], a[244:247], v[24:27]\n"
"ds_read_b128 v[220:223], v231 offset:49664\n"
"v_mfma_f32_16x16x32_bf16 v[24:27], v[132:135], a[248:251], v[24:27]\n"
"v_exp_f32 v164, v164\n"
"v_mfma_f32_16x16x32_bf16 v[56:59], v[136:139], a[244:247], v[56:59]\n"
"ds_read_b128 v[212:215], v231 offset:49152\n"
"v_mfma_f32_16x16x32_bf16 v[56:59], v[140:143], a[248:251], v[56:59]\n"
"v_exp_f32 v178, v178\n"
"v_mfma_f32_16x16x32_bf16 v[88:91], v[144:147], a[244:247], v[88:91]\n"
"s_add_u32 m0, s44, 16384\n"
"ds_read_b64_tr_b16 a[246:247], v232 offset:36864\n"
"v_mfma_f32_16x16x32_bf16 v[88:91], v[148:151], a[248:251], v[88:91]\n"
"buffer_load_dwordx4 v240, s[16:19], s56 offen nt lds\n"
"v_mfma_f32_16x16x32_bf16 v[92:95], v[148:151], v[224:227], v[92:95]\n"
"v_exp_f32 v175, v175\n"
"v_mfma_f32_16x16x32_bf16 v[92:95], v[144:147], a[252:255], v[92:95]\n"
"v_exp_f32 v168, v168\n"
"v_mfma_f32_16x16x32_bf16 v[60:63], v[140:143], v[224:227], v[60:63]\n"
"v_exp_f32 v161, v161\n"
"v_mfma_f32_16x16x32_bf16 v[60:63], v[136:139], a[252:255], v[60:63]\n"
"v_exp_f32 v177, v177\n"
"v_mfma_f32_16x16x32_bf16 v[28:31], v[132:135], v[224:227], v[28:31]\n"
"ds_read_b64_tr_b16 v[226:227], v237 offset:36864\n"
"v_mfma_f32_16x16x32_bf16 v[28:31], v[128:131], a[252:255], v[28:31]\n"
"v_exp_f32 v172, v172\n"
"v_mfma_f32_16x16x32_bf16 a[236:239], v[124:127], a[252:255], a[236:239]\n"
"ds_read_b64_tr_b16 a[250:251], v236 offset:36864\n"
"v_cvt_pk_bf16_f32 v120, v164, v165\n"
"v_smfmac_f32_16x16x64_bf16 v[198:201], a[240:243], v[136:143], v241\n"
"s_waitcnt lgkmcnt(2)\n"
"ds_read_b64_tr_b16 a[252:253], v233 offset:32768\n"
"v_mfma_f32_16x16x32_bf16 v[156:159], v[208:211], a[0:3], v[180:183]\n"
"ds_read_b64_tr_b16 a[248:249], v236 offset:32768\n"
"v_cvt_pk_bf16_f32 v121, v166, v167\n"
"v_mfma_f32_16x16x32_bf16 v[156:159], v[212:215], a[4:7], v[156:159]\n"
"ds_read_b64_tr_b16 v[224:225], v237 offset:32768\n"
"v_cvt_pk_bf16_f32 v98, v168, v169\n"
"v_mfma_f32_16x16x32_bf16 v[156:159], v[216:219], a[8:11], v[156:159]\n"
"ds_read_b64_tr_b16 a[254:255], v233 offset:36864\n"
"v_cvt_pk_bf16_f32 v112, v160, v161\n"
"v_mfma_f32_16x16x32_bf16 v[156:159], v[220:223], a[12:15], v[156:159]\n"
"ds_read_b64_tr_b16 a[244:245], v232 offset:32768\n"
"v_mfma_f32_16x16x32_bf16 v[160:163], v[216:219], a[24:27], v[184:187]\n"
"v_exp_f32 v176, v176\n"
"v_mfma_f32_16x16x32_bf16 v[160:163], v[220:223], a[28:31], v[160:163]\n"
"v_exp_f32 v152, v152\n"
"v_mfma_f32_16x16x32_bf16 v[160:163], v[208:211], a[16:19], v[160:163]\n"
"s_add_u32 s40, s56, s52\n"
"v_cvt_pk_bf16_f32 v114, v176, v177\n"
"v_mfma_f32_16x16x32_bf16 v[160:163], v[212:215], a[20:23], v[160:163]\n"
"buffer_load_dwordx4 v207, s[16:19], s40 offen offset:1024 nt lds\n"
"v_mfma_f32_16x16x32_bf16 v[164:167], v[208:211], a[32:35], v[188:191]\n"
"v_exp_f32 v170, v170\n"
"v_mfma_f32_16x16x32_bf16 v[164:167], v[212:215], a[36:39], v[164:167]\n"
"v_exp_f32 v173, v173\n"
"v_mfma_f32_16x16x32_bf16 v[164:167], v[216:219], a[40:43], v[164:167]\n"
"v_exp_f32 v153, v153\n"
"v_mfma_f32_16x16x32_bf16 v[164:167], v[220:223], a[44:47], v[164:167]\n"
"s_waitcnt lgkmcnt(0)\n"
"v_exp_f32 v174, v174\n"
"v_mfma_f32_16x16x32_bf16 a[208:211], v[124:127], a[244:247], a[208:211]\n"
"ds_read_b128 v[208:211], v230 offset:53248\n"
"v_cvt_pk_bf16_f32 v107, v174, v175\n"
"v_mfma_f32_16x16x32_bf16 v[0:3], v[128:131], a[244:247], v[0:3]\n"
"v_exp_f32 v156, v156\n"
"v_mfma_f32_16x16x32_bf16 v[0:3], v[132:135], a[248:251], v[0:3]\n"
"ds_read_b128 v[220:223], v231 offset:53760\n"
"v_cvt_pk_bf16_f32 v106, v172, v173\n"
"v_mfma_f32_16x16x32_bf16 v[32:35], v[136:139], a[244:247], v[32:35]\n"
"v_exp_f32 v161, v161\n"
"v_mfma_f32_16x16x32_bf16 v[32:35], v[140:143], a[248:251], v[32:35]\n"
"ds_read_b128 v[216:219], v230 offset:53760\n"
"v_cvt_pk_bf16_f32 v122, v152, v153\n"
"v_mfma_f32_16x16x32_bf16 v[64:67], v[144:147], a[244:247], v[64:67]\n"
"s_add_u32 s40, s56, s53\n"
"ds_read_b128 v[212:215], v231 offset:53248\n"
"v_mfma_f32_16x16x32_bf16 v[64:67], v[148:151], a[248:251], v[64:67]\n"
"buffer_load_dwordx4 v240, s[16:19], s40 offen offset:2048 nt lds\n"
"v_mfma_f32_16x16x32_bf16 v[68:71], v[148:151], v[224:227], v[68:71]\n"
"v_exp_f32 v159, v159\n"
"v_mfma_f32_16x16x32_bf16 v[68:71], v[144:147], a[252:255], v[68:71]\n"
"v_exp_f32 v155, v155\n"
"v_mfma_f32_16x16x32_bf16 v[36:39], v[140:143], v[224:227], v[36:39]\n"
"v_exp_f32 v160, v160\n"
"v_mfma_f32_16x16x32_bf16 v[36:39], v[136:139], a[252:255], v[36:39]\n"
"v_exp_f32 v179, v179\n"
"v_mfma_f32_16x16x32_bf16 v[4:7], v[132:135], v[224:227], v[4:7]\n"
"ds_read_b64_tr_b16 a[244:245], v232 offset:33280\n"
"ds_read_b64_tr_b16 v[224:225], v237 offset:33280\n"
"v_mfma_f32_16x16x32_bf16 v[4:7], v[128:131], a[252:255], v[4:7]\n"
"v_exp_f32 v158, v158\n"
"v_mfma_f32_16x16x32_bf16 a[212:215], v[124:127], a[252:255], a[212:215]\n"
"ds_read_b64_tr_b16 a[246:247], v232 offset:37376\n"
"ds_read_b64_tr_b16 a[250:251], v236 offset:37376\n"
"v_smfmac_f32_16x16x64_bf16 v[200:203], a[240:243], v[144:151], v241\n"
"s_waitcnt lgkmcnt(4)\n"
"ds_read_b64_tr_b16 v[226:227], v237 offset:37376\n"
"v_cvt_pk_bf16_f32 v99, v170, v171\n"
"v_mfma_f32_16x16x32_bf16 v[168:171], v[208:211], a[0:3], v[180:183]\n"
"v_exp_f32 v164, v164\n"
"v_mfma_f32_16x16x32_bf16 v[168:171], v[212:215], a[4:7], v[168:171]\n"
"ds_read_b64_tr_b16 a[248:249], v236 offset:33280\n"
"ds_read_b64_tr_b16 a[254:255], v233 offset:37376\n"
"v_mfma_f32_16x16x32_bf16 v[168:171], v[216:219], a[8:11], v[168:171]\n"
"s_add_u32 s40, s56, s54\n"
"ds_read_b64_tr_b16 a[252:253], v233 offset:33280\n"
"v_mfma_f32_16x16x32_bf16 v[168:171], v[220:223], a[12:15], v[168:171]\n"
"buffer_load_dwordx4 v207, s[16:19], s40 offen offset:3072 nt lds\n"
"v_mfma_f32_16x16x32_bf16 v[172:175], v[216:219], a[24:27], v[184:187]\n"
"v_exp_f32 v154, v154\n"
"v_mfma_f32_16x16x32_bf16 v[172:175], v[220:223], a[28:31], v[172:175]\n"
"v_exp_f32 v167, v167\n"
"v_mfma_f32_16x16x32_bf16 v[172:175], v[208:211], a[16:19], v[172:175]\n"
"v_exp_f32 v163, v163\n"
"v_mfma_f32_16x16x32_bf16 v[172:175], v[212:215], a[20:23], v[172:175]\n"
"s_cbranch_vccnz LPCL3_%=\nLPKL3_%=:\n"
"v_cvt_pk_bf16_f32 v115, v178, v179\n"
"v_mfma_f32_16x16x32_bf16 v[176:179], v[208:211], a[32:35], v[188:191]\n"
"s_add_u32 s47, s47, 1\n"
"v_exp_f32 v165, v165\n"
"v_mfma_f32_16x16x32_bf16 v[176:179], v[212:215], a[36:39], v[176:179]\n"
"v_exp_f32 v157, v157\n"
"v_mfma_f32_16x16x32_bf16 v[176:179], v[216:219], a[40:43], v[176:179]\n"
"s_and_b32 s40, s47, 3\n"
"v_exp_f32 v166, v166\n"
"v_mfma_f32_16x16x32_bf16 v[176:179], v[220:223], a[44:47], v[176:179]\n"
"s_waitcnt lgkmcnt(0)\n"
"s_cbranch_scc1 LAL_%=\n"
"s_cmp_eq_u32 s47, 8\n"
"s_cbranch_scc1 LTH1_%=\n"
"s_cmp_eq_u32 s47, 12\n"
"s_cbranch_scc1 LTH2_%=\n"
"s_cmp_eq_u32 s47, 16\n"
"s_cbranch_scc1 LAX_%=\n"
"LTH0_%=:\n"
"v_mfma_f32_16x16x32_bf16 a[224:227], v[124:127], a[244:247], a[224:227]\n"
"v_exp_f32 v171, v171\n"
"v_mfma_f32_16x16x32_bf16 v[16:19], v[128:131], a[244:247], v[16:19]\n"
"s_waitcnt vmcnt(8)\n"
"v_exp_f32 v172, v172\n"
"v_mfma_f32_16x16x32_bf16 v[16:19], v[132:135], a[248:251], v[16:19]\n"
"s_barrier\n"
"v_mfma_f32_16x16x32_bf16 v[48:51], v[136:139], a[244:247], v[48:51]\n"
"v_exp_f32 v169, v169\n"
"v_mfma_f32_16x16x32_bf16 v[48:51], v[140:143], a[248:251], v[48:51]\n"
"ds_read_b128 v[220:223], v229 offset:512\n"
"v_cvt_pk_bf16_f32 v117, v166, v167\n"
"v_exp_f32 v176, v176\n"
"v_exp_f32 v177, v177\n"
"v_exp_f32 v178, v178\n"
"v_mfma_f32_16x16x32_bf16 v[80:83], v[144:147], a[244:247], v[80:83]\n"
"ds_read_b64_tr_b16 a[244:245], v234 offset:49152\n"
"v_cvt_pk_bf16_f32 v108, v160, v161\n"
"v_mfma_f32_16x16x32_bf16 v[80:83], v[148:151], a[248:251], v[80:83]\n"
"ds_read_b64_tr_b16 a[246:247], v234 offset:53248\n"
"ds_read_b64_tr_b16 a[248:249], v238 offset:49152\n"
"v_exp_f32 v162, v162\n"
"v_exp_f32 v168, v168\n"
"v_exp_f32 v173, v173\n"
"v_exp_f32 v175, v175\n"
"v_mfma_f32_16x16x32_bf16 v[84:87], v[148:151], v[224:227], v[84:87]\n"
"ds_read_b64_tr_b16 a[250:251], v238 offset:53248\n"
"v_cvt_pk_bf16_f32 v116, v164, v165\n"
"v_mfma_f32_16x16x32_bf16 v[84:87], v[144:147], a[252:255], v[84:87]\n"
"v_exp_f32 v170, v170\n"
"v_mfma_f32_16x16x32_bf16 v[52:55], v[140:143], v[224:227], v[52:55]\n"
"v_cvt_pk_bf16_f32 v103, v170, v171\n"
"v_cvt_pk_bf16_f32 v110, v172, v173\n"
"v_mfma_f32_16x16x32_bf16 v[52:55], v[136:139], a[252:255], v[52:55]\n"
"v_cvt_pk_bf16_f32 v123, v154, v155\n"
"v_cvt_pk_bf16_f32 v101, v158, v159\n"
"v_mfma_f32_16x16x32_bf16 v[20:23], v[132:135], v[224:227], v[20:23]\n"
"v_cvt_pk_bf16_f32 v100, v156, v157\n"
"v_cvt_pk_bf16_f32 v102, v168, v169\n"
"v_mfma_f32_16x16x32_bf16 v[20:23], v[128:131], a[252:255], v[20:23]\n"
"v_exp_f32 v179, v179\n"
"v_mfma_f32_16x16x32_bf16 a[228:231], v[124:127], a[252:255], a[228:231]\n"
"s_waitcnt lgkmcnt(2)\n"
"ds_read_b64_tr_b16 a[252:253], v235 offset:49152\n"
"ds_read_b64_tr_b16 v[226:227], v239 offset:53248\n"
"v_mfma_f32_16x16x32_bf16 a[216:219], v[120:123], a[244:247], a[216:219]\n"
"s_add_u32 m0, s44, 81920\n"
"ds_read_b64_tr_b16 v[224:225], v239 offset:49152\n"
"v_cvt_pk_bf16_f32 v119, v178, v179\n"
"v_smfmac_f32_16x16x64_bf16 v[196:199], a[240:243], v[96:103], v241\n"
"buffer_load_dwordx4 v240, s[20:23], s56 offen nt lds\n"
"v_mfma_f32_16x16x32_bf16 v[8:11], v[96:99], a[244:247], v[8:11]\n"
"s_waitcnt lgkmcnt(3)\n"
"ds_read_b64_tr_b16 a[254:255], v235 offset:53248\n"
"v_cvt_pk_bf16_f32 v118, v176, v177\n"
"v_mfma_f32_16x16x32_bf16 v[8:11], v[100:103], a[248:251], v[8:11]\n"
"v_exp_f32 v174, v174\n"
"v_smfmac_f32_16x16x64_bf16 v[200:203], a[240:243], v[112:119], v241\n"
"v_cvt_pk_bf16_f32 v109, v162, v163\n"
"v_cvt_pk_bf16_f32 v111, v174, v175\n"
"v_mfma_f32_16x16x32_bf16 v[40:43], v[104:107], a[244:247], v[40:43]\n"
"ds_read_b128 v[212:215], v229 offset:0\n"
"v_and_b32 v245, 0x3f0, v242\n"
"v_mfma_f32_16x16x32_bf16 v[40:43], v[108:111], a[248:251], v[40:43]\n"
"v_lshlrev_b32_e64 v244, 24, s43\n"
"v_mul_f32 v244, 0x45800000, v244\n"
"v_smfmac_f32_16x16x64_bf16 v[198:201], a[240:243], v[104:111], v241\n"
"ds_read_b128 v[208:211], v228 offset:0\n"
"v_add_f32 v196, v196, v197\n"
"v_mfma_f32_16x16x32_bf16 v[72:75], v[112:115], a[244:247], v[72:75]\n"
"ds_read_b64_tr_b16 a[244:245], v234 offset:49664\n"
"v_lshrrev_b32 v245, 1, v245\n"
"v_mfma_f32_16x16x32_bf16 v[72:75], v[116:119], a[248:251], v[72:75]\n"
"ds_read_b64_tr_b16 a[246:247], v234 offset:53760\n"
"v_sub_u32 v245, v242, v245\n"
"v_mfma_f32_16x16x32_bf16 v[202:205], a[240:243], v[120:123], v[202:205]\n"
"s_waitcnt lgkmcnt(4)\n"
"ds_read_b64_tr_b16 a[248:249], v238 offset:49664\n"
"v_add_f32 v200, v200, v201\n"
"v_mfma_f32_16x16x32_bf16 v[76:79], v[116:119], v[224:227], v[76:79]\n"
"v_add_f32 v198, v198, v199\n"
"v_add_f32 v254, v196, v198\n"
"v_mfma_f32_16x16x32_bf16 v[76:79], v[112:115], a[252:255], v[76:79]\n"
"ds_read_b64_tr_b16 a[250:251], v238 offset:53760\n"
"v_xor_b32 v250, 0x1000, v245\n"
"v_mfma_f32_16x16x32_bf16 v[44:47], v[108:111], v[224:227], v[44:47]\n"
"ds_read_b128 v[216:219], v228 offset:512\n"
"v_add_f32 v254, v254, v200\n"
"v_mfma_f32_16x16x32_bf16 v[44:47], v[104:107], a[252:255], v[44:47]\n"
"v_add_f32 v244, v244, v202\n"
"v_add_f32 v254, v254, v202\n"
"v_mfma_f32_16x16x32_bf16 v[12:15], v[100:103], v[224:227], v[12:15]\n"
"s_add_u32 s40, s56, s52\n"
"ds_read_b64_tr_b16 v[226:227], v239 offset:53760\n"
"v_min3_f32 v244, v244, v196, v198\n"
"v_mfma_f32_16x16x32_bf16 v[12:15], v[96:99], a[252:255], v[12:15]\n"
"buffer_load_dwordx4 v207, s[20:23], s40 offen offset:1024 nt lds\n"
"v_min_f32 v244, v244, v200\n"
"v_rcp_f32 v244, v244\n"
"v_mfma_f32_16x16x32_bf16 a[220:223], v[120:123], a[252:255], a[220:223]\n"
"s_waitcnt lgkmcnt(4)\n"
"v_fmac_f32 v254, 0x68800000, v244\n"
"v_cmp_nge_f32 vcc, s45, v254\n"
"s_cbranch_vccnz LSLOW_%=\nLBACK0_%=:\ns_mov_b64 vcc, s[64:65]\n"
"v_mfma_f32_16x16x32_bf16 a[232:235], v[120:123], a[244:247], a[232:235]\n"
"v_rcp_f32 v246, v196\n"
"v_accvgpr_mov_b32 a2, a50\n"
"v_accvgpr_mov_b32 a7, a55\n"
"v_accvgpr_mov_b32 a10, a58\n"
"v_accvgpr_mov_b32 a15, a63\n"
"v_accvgpr_mov_b32 a22, a70\n"
"v_accvgpr_mov_b32 a30, a78\n"
"v_bfe_u32 v149, v242, 8, 2\n"
"v_lshl_add_u32 v149, v149, 4, s43\n"
"v_mfma_f32_16x16x32_bf16 v[24:27], v[96:99], a[244:247], v[24:27]\n"
"s_waitcnt lgkmcnt(2)\n"
"ds_read_b128 v[124:127], v149 offset:0\n"
"v_accvgpr_mov_b32 a28, a76\n"
"v_mfma_f32_16x16x32_bf16 v[24:27], v[100:103], a[248:251], v[24:27]\n"
"ds_read_b128 v[132:135], v149 offset:512\n"
"v_mov_b32_dpp v246, v246 row_ror:12 row_mask:0x2 bank_mask:0xf\n"
"v_accvgpr_mov_b32 a3, a51\n"
"v_accvgpr_mov_b32 a12, a60\n"
"v_accvgpr_mov_b32 a18, a66\n"
"v_accvgpr_mov_b32 a19, a67\n"
"v_accvgpr_mov_b32 a26, a74\n"
"v_accvgpr_mov_b32 a36, a84\n"
"v_xor_b32 v148, 0x400, v243\n"
"v_xor_b32 v150, 64, v149\n"
"v_mfma_f32_16x16x32_bf16 v[56:59], v[104:107], a[244:247], v[56:59]\n"
"ds_read_b128 v[128:131], v150 offset:0\n"
"v_accvgpr_mov_b32 a39, a87\n"
"v_mfma_f32_16x16x32_bf16 v[56:59], v[108:111], a[248:251], v[56:59]\n"
"ds_read_b128 v[136:139], v150 offset:512\n"
"v_rcp_f32 v247, v198\n"
"v_mov_b32_dpp v246, v246 row_ror:8 row_mask:0x4 bank_mask:0xf\n"
"v_accvgpr_mov_b32 a0, a48\n"
"v_accvgpr_mov_b32 a1, a49\n"
"v_accvgpr_mov_b32 a4, a52\n"
"v_accvgpr_mov_b32 a5, a53\n"
"v_accvgpr_mov_b32 a8, a56\n"
"v_accvgpr_mov_b32 a16, a64\n"
"v_accvgpr_mov_b32 a25, a73\n"
"v_mfma_f32_16x16x32_bf16 v[88:91], v[112:115], a[244:247], v[88:91]\n"
"v_accvgpr_mov_b32 a20, a68\n"
"v_accvgpr_mov_b32 a29, a77\n"
"v_mfma_f32_16x16x32_bf16 v[88:91], v[116:119], a[248:251], v[88:91]\n"
"ds_read_b128 a[192:195], v243 offset:4096\n"
"ds_read_b128 a[200:203], v243 offset:6144\n"
"v_accvgpr_mov_b32 a6, a54\n"
"v_accvgpr_mov_b32 a9, a57\n"
"v_accvgpr_mov_b32 a11, a59\n"
"v_accvgpr_mov_b32 a13, a61\n"
"v_accvgpr_mov_b32 a14, a62\n"
"v_accvgpr_mov_b32 a17, a65\n"
"v_accvgpr_mov_b32 a23, a71\n"
"v_accvgpr_mov_b32 a31, a79\n"
"v_accvgpr_mov_b32 a33, a81\n"
"v_accvgpr_mov_b32 a35, a83\n"
"v_accvgpr_mov_b32 a44, a92\n"
"v_accvgpr_mov_b32 a47, a95\n"
"s_waitcnt lgkmcnt(5)\n"
"v_mfma_f32_16x16x32_bf16 v[180:183], v[124:127], a[0:3], 0\n"
"v_xor_b32 v244, 0x11000, v242\n"
"s_waitcnt lgkmcnt(3)\n"
"v_mfma_f32_16x16x32_bf16 v[180:183], v[128:131], a[4:7], v[180:183]\n"
"v_accvgpr_mov_b32 a34, a82\n"
"v_accvgpr_mov_b32 a43, a91\n"
"v_mfma_f32_16x16x32_bf16 v[180:183], v[132:135], a[8:11], v[180:183]\n"
"s_waitcnt lgkmcnt(2)\n"
"ds_read_b128 a[204:207], v148 offset:6144\n"
"v_accvgpr_mov_b32 a37, a85\n"
"v_mfma_f32_16x16x32_bf16 v[180:183], v[136:139], a[12:15], v[180:183]\n"
"ds_read_b128 a[196:199], v148 offset:4096\n"
"v_accvgpr_mov_b32 a21, a69\n"
"v_accvgpr_mov_b32 a24, a72\n"
"v_accvgpr_mov_b32 a32, a80\n"
"v_accvgpr_mov_b32 a38, a86\n"
"v_accvgpr_mov_b32 a40, a88\n"
"v_accvgpr_mov_b32 a42, a90\n"
"v_accvgpr_mov_b32 a46, a94\n"
"v_mfma_f32_16x16x32_bf16 v[184:187], v[124:127], a[16:19], 0\n"
"v_mov_b32_dpp v246, v246 row_ror:4 row_mask:0x8 bank_mask:0xf\n"
"v_accvgpr_mov_b32 a27, a75\n"
"v_mfma_f32_16x16x32_bf16 v[184:187], v[128:131], a[20:23], v[184:187]\n"
"v_mov_b32_dpp v247, v247 row_ror:12 row_mask:0x2 bank_mask:0xf\n"
"v_mfma_f32_16x16x32_bf16 v[184:187], v[132:135], a[24:27], v[184:187]\n"
"s_add_u32 s40, s56, s53\n"
"ds_read_b64_tr_b16 v[224:225], v239 offset:49664\n"
"v_mfma_f32_16x16x32_bf16 v[184:187], v[136:139], a[28:31], v[184:187]\n"
"buffer_load_dwordx4 v240, s[20:23], s40 offen offset:2048 nt lds\n"
"v_accvgpr_mov_b32 a41, a89\n"
"v_sub_f32 v180, s74, v180\n"
"v_sub_f32 v181, s74, v181\n"
"v_sub_f32 v183, s74, v183\n"
"v_mul_f32_dpp v26, v246, v26 row_newbcast:2 row_mask:0xf bank_mask:0xf\n"
"v_mfma_f32_16x16x32_bf16 v[188:191], v[124:127], a[32:35], 0\n"
"v_mov_b32_dpp v247, v247 row_ror:8 row_mask:0x4 bank_mask:0xf\n"
"v_accvgpr_mov_b32 a45, a93\n"
"v_mfma_f32_16x16x32_bf16 v[188:191], v[128:131], a[36:39], v[188:191]\n"
"v_mov_b32 v252, v192\n"
"v_mfma_f32_16x16x32_bf16 v[188:191], v[132:135], a[40:43], v[188:191]\n"
"v_mfma_f32_16x16x32_bf16 v[188:191], v[136:139], a[44:47], v[188:191]\n"
"v_mul_f32_dpp v11, v246, v11 row_newbcast:3 row_mask:0xf bank_mask:0xf\n"
"s_waitcnt lgkmcnt(1)\n"
"v_mfma_f32_16x16x32_bf16 v[192:195], v[124:127], a[192:195], 0\n"
"ds_read_b64_tr_b16 a[252:253], v235 offset:49664\n"
"v_sub_f32 v185, s74, v185\n"
"v_mfma_f32_16x16x32_bf16 v[192:195], v[128:131], a[196:199], v[192:195]\n"
"ds_read_b64_tr_b16 a[248:249], v236 offset:49152\n"
"v_sub_f32 v182, s74, v182\n"
"v_mfma_f32_16x16x32_bf16 v[192:195], v[132:135], a[200:203], v[192:195]\n"
"v_sub_f32 v184, s74, v184\n"
"v_mfma_f32_16x16x32_bf16 v[192:195], v[136:139], a[204:207], v[192:195]\n"
"ds_read_b64_tr_b16 a[254:255], v235 offset:53760\n"
"ds_read_b64_tr_b16 a[246:247], v232 offset:53248\n"
"v_mov_b32 v253, v202\n"
"v_sub_f32 v186, s74, v186\n"
"v_mfma_f32_16x16x32_bf16 v[152:155], v[208:211], a[0:3], v[180:183]\n"
"v_mfma_f32_16x16x32_bf16 v[152:155], v[212:215], a[4:7], v[152:155]\n"
"v_sub_f32 v187, s74, v187\n"
"v_mfma_f32_16x16x32_bf16 v[152:155], v[216:219], a[8:11], v[152:155]\n"
"s_add_u32 s40, s56, s54\n"
"v_mfma_f32_16x16x32_bf16 v[152:155], v[220:223], a[12:15], v[152:155]\n"
"buffer_load_dwordx4 v207, s[20:23], s40 offen offset:3072 nt lds\n"
"v_mov_b32_dpp v247, v247 row_ror:4 row_mask:0x8 bank_mask:0xf\n"
"v_sub_f32 v189, s74, v189\n"
"v_sub_f32 v190, s74, v190\n"
"v_sub_f32 v191, s74, v191\n"
"v_sub_f32 v192, s74, v192\n"
"v_mfma_f32_16x16x32_bf16 v[156:159], v[216:219], a[24:27], v[184:187]\n"
"s_add_u32 s55, s55, s59\n"
"v_mfma_f32_16x16x32_bf16 v[156:159], v[220:223], a[28:31], v[156:159]\n"
"s_or_b32 s55, s55, s58\n"
"v_sub_f32 v188, s74, v188\n"
"v_mfma_f32_16x16x32_bf16 v[156:159], v[208:211], a[16:19], v[156:159]\n"
"s_xor_b32 s56, s55, s57\n"
"v_exp_f32 v153, v153\n"
"v_mfma_f32_16x16x32_bf16 v[156:159], v[212:215], a[20:23], v[156:159]\n"
"ds_write_b64 v250, v[252:253] offset:52224\n"
"v_sub_f32 v193, s74, v193\n"
"v_sub_f32 v194, s74, v194\n"
"v_exp_f32 v152, v152\n"
"v_mfma_f32_16x16x32_bf16 v[160:163], v[208:211], a[32:35], v[188:191]\n"
"v_mfma_f32_16x16x32_bf16 v[160:163], v[212:215], a[36:39], v[160:163]\n"
"v_rcp_f32 v206, v200\n"
"v_mfma_f32_16x16x32_bf16 v[160:163], v[216:219], a[40:43], v[160:163]\n"
"v_sub_f32 v195, s74, v195\n"
"v_mfma_f32_16x16x32_bf16 v[160:163], v[220:223], a[44:47], v[160:163]\n"
"ds_write_b128 v244, a[216:219] offset:32768\n"
"v_mul_f32_dpp v25, v246, v25 row_newbcast:1 row_mask:0xf bank_mask:0xf\n"
"v_mfma_f32_16x16x32_bf16 v[164:167], v[216:219], a[200:203], v[192:195]\n"
"v_mov_b32_dpp v206, v206 row_ror:12 row_mask:0x2 bank_mask:0xf\n"
"v_mfma_f32_16x16x32_bf16 v[164:167], v[220:223], a[204:207], v[164:167]\n"
"v_mfma_f32_16x16x32_bf16 v[164:167], v[208:211], a[192:195], v[164:167]\n"
"ds_read_b128 v[208:211], v228 offset:4096\n"
"v_mfma_f32_16x16x32_bf16 v[164:167], v[212:215], a[196:199], v[164:167]\n"
"ds_read_b128 v[216:219], v228 offset:4608\n"
"ds_read_b128 v[220:223], v229 offset:4608\n"
"v_mov_b32_dpp v206, v206 row_ror:8 row_mask:0x4 bank_mask:0xf\n"
"v_exp_f32 v155, v155\n"
"v_exp_f32 v160, v160\n"
"s_branch LTC_%=\n"
"LTH1_%=:\n"
"v_mfma_f32_16x16x32_bf16 a[224:227], v[124:127], a[244:247], a[224:227]\n"
"v_exp_f32 v171, v171\n"
"v_mfma_f32_16x16x32_bf16 v[16:19], v[128:131], a[244:247], v[16:19]\n"
"s_waitcnt vmcnt(8)\n"
"v_exp_f32 v172, v172\n"
"v_mfma_f32_16x16x32_bf16 v[16:19], v[132:135], a[248:251], v[16:19]\n"
"s_barrier\n"
"v_mfma_f32_16x16x32_bf16 v[48:51], v[136:139], a[244:247], v[48:51]\n"
"v_exp_f32 v169, v169\n"
"v_mfma_f32_16x16x32_bf16 v[48:51], v[140:143], a[248:251], v[48:51]\n"
"ds_read_b128 v[220:223], v229 offset:512\n"
"v_cvt_pk_bf16_f32 v117, v166, v167\n"
"v_exp_f32 v176, v176\n"
"v_exp_f32 v177, v177\n"
"v_exp_f32 v178, v178\n"
"v_mfma_f32_16x16x32_bf16 v[80:83], v[144:147], a[244:247], v[80:83]\n"
"ds_read_b64_tr_b16 a[244:245], v234 offset:49152\n"
"v_cvt_pk_bf16_f32 v108, v160, v161\n"
"v_mfma_f32_16x16x32_bf16 v[80:83], v[148:151], a[248:251], v[80:83]\n"
"ds_read_b64_tr_b16 a[246:247], v234 offset:53248\n"
"ds_read_b64_tr_b16 a[248:249], v238 offset:49152\n"
"v_exp_f32 v162, v162\n"
"v_exp_f32 v168, v168\n"
"v_exp_f32 v173, v173\n"
"v_exp_f32 v175, v175\n"
"v_mfma_f32_16x16x32_bf16 v[84:87], v[148:151], v[224:227], v[84:87]\n"
"ds_read_b64_tr_b16 a[250:251], v238 offset:53248\n"
"v_cvt_pk_bf16_f32 v116, v164, v165\n"
"v_mfma_f32_16x16x32_bf16 v[84:87], v[144:147], a[252:255], v[84:87]\n"
"v_exp_f32 v170, v170\n"
"v_mfma_f32_16x16x32_bf16 v[52:55], v[140:143], v[224:227], v[52:55]\n"
"v_cvt_pk_bf16_f32 v103, v170, v171\n"
"v_cvt_pk_bf16_f32 v110, v172, v173\n"
"v_mfma_f32_16x16x32_bf16 v[52:55], v[136:139], a[252:255], v[52:55]\n"
"v_cvt_pk_bf16_f32 v123, v154, v155\n"
"v_cvt_pk_bf16_f32 v101, v158, v159\n"
"v_mfma_f32_16x16x32_bf16 v[20:23], v[132:135], v[224:227], v[20:23]\n"
"v_cvt_pk_bf16_f32 v100, v156, v157\n"
"v_cvt_pk_bf16_f32 v102, v168, v169\n"
"v_mfma_f32_16x16x32_bf16 v[20:23], v[128:131], a[252:255], v[20:23]\n"
"v_exp_f32 v179, v179\n"
"v_mfma_f32_16x16x32_bf16 a[228:231], v[124:127], a[252:255], a[228:231]\n"
"s_waitcnt lgkmcnt(2)\n"
"ds_read_b64_tr_b16 a[252:253], v235 offset:49152\n"
"ds_read_b64_tr_b16 v[226:227], v239 offset:53248\n"
"v_mfma_f32_16x16x32_bf16 a[216:219], v[120:123], a[244:247], a[216:219]\n"
"s_add_u32 m0, s44, 81920\n"
"ds_read_b64_tr_b16 v[224:225], v239 offset:49152\n"
"v_cvt_pk_bf16_f32 v119, v178, v179\n"
"v_smfmac_f32_16x16x64_bf16 v[196:199], a[240:243], v[96:103], v241\n"
"buffer_load_dwordx4 v240, s[20:23], s56 offen nt lds\n"
"v_mfma_f32_16x16x32_bf16 v[8:11], v[96:99], a[244:247], v[8:11]\n"
"s_waitcnt lgkmcnt(3)\n"
"ds_read_b64_tr_b16 a[254:255], v235 offset:53248\n"
"v_cvt_pk_bf16_f32 v118, v176, v177\n"
"v_mfma_f32_16x16x32_bf16 v[8:11], v[100:103], a[248:251], v[8:11]\n"
"v_exp_f32 v174, v174\n"
"v_smfmac_f32_16x16x64_bf16 v[200:203], a[240:243], v[112:119], v241\n"
"v_cvt_pk_bf16_f32 v109, v162, v163\n"
"v_cvt_pk_bf16_f32 v111, v174, v175\n"
"v_mfma_f32_16x16x32_bf16 v[40:43], v[104:107], a[244:247], v[40:43]\n"
"ds_read_b128 v[212:215], v229 offset:0\n"
"v_and_b32 v245, 0x3f0, v242\n"
"v_mfma_f32_16x16x32_bf16 v[40:43], v[108:111], a[248:251], v[40:43]\n"
"v_lshlrev_b32_e64 v244, 24, s43\n"
"v_mul_f32 v244, 0x45800000, v244\n"
"v_smfmac_f32_16x16x64_bf16 v[198:201], a[240:243], v[104:111], v241\n"
"ds_read_b128 v[208:211], v228 offset:0\n"
"v_add_f32 v196, v196, v197\n"
"v_mfma_f32_16x16x32_bf16 v[72:75], v[112:115], a[244:247], v[72:75]\n"
"ds_read_b64_tr_b16 a[244:245], v234 offset:49664\n"
"v_lshrrev_b32 v245, 1, v245\n"
"v_mfma_f32_16x16x32_bf16 v[72:75], v[116:119], a[248:251], v[72:75]\n"
"ds_read_b64_tr_b16 a[246:247], v234 offset:53760\n"
"v_sub_u32 v245, v242, v245\n"
"v_mfma_f32_16x16x32_bf16 v[202:205], a[240:243], v[120:123], v[202:205]\n"
"s_waitcnt lgkmcnt(4)\n"
"ds_read_b64_tr_b16 a[248:249], v238 offset:49664\n"
"v_add_f32 v200, v200, v201\n"
"v_mfma_f32_16x16x32_bf16 v[76:79], v[116:119], v[224:227], v[76:79]\n"
"v_add_f32 v198, v198, v199\n"
"v_add_f32 v254, v196, v198\n"
"v_mfma_f32_16x16x32_bf16 v[76:79], v[112:115], a[252:255], v[76:79]\n"
"ds_read_b64_tr_b16 a[250:251], v238 offset:53760\n"
"v_xor_b32 v250, 0x1000, v245\n"
"v_mfma_f32_16x16x32_bf16 v[44:47], v[108:111], v[224:227], v[44:47]\n"
"ds_read_b128 v[216:219], v228 offset:512\n"
"v_add_f32 v254, v254, v200\n"
"v_mfma_f32_16x16x32_bf16 v[44:47], v[104:107], a[252:255], v[44:47]\n"
"v_add_f32 v244, v244, v202\n"
"v_add_f32 v254, v254, v202\n"
"v_mfma_f32_16x16x32_bf16 v[12:15], v[100:103], v[224:227], v[12:15]\n"
"s_add_u32 s40, s56, s52\n"
"ds_read_b64_tr_b16 v[226:227], v239 offset:53760\n"
"v_min3_f32 v244, v244, v196, v198\n"
"v_mfma_f32_16x16x32_bf16 v[12:15], v[96:99], a[252:255], v[12:15]\n"
"buffer_load_dwordx4 v207, s[20:23], s40 offen offset:1024 nt lds\n"
"v_min_f32 v244, v244, v200\n"
"v_rcp_f32 v244, v244\n"
"v_mfma_f32_16x16x32_bf16 a[220:223], v[120:123], a[252:255], a[220:223]\n"
"s_waitcnt lgkmcnt(4)\n"
"v_fmac_f32 v254, 0x68800000, v244\n"
"v_cmp_nge_f32 vcc, s45, v254\n"
"s_cbranch_vccnz LSLOW_%=\nLBACK1_%=:\ns_mov_b64 vcc, s[64:65]\n"
"v_mfma_f32_16x16x32_bf16 a[232:235], v[120:123], a[244:247], a[232:235]\n"
"v_rcp_f32 v246, v196\n"
"v_accvgpr_mov_b32 a2, a98\n"
"v_accvgpr_mov_b32 a7, a103\n"
"v_accvgpr_mov_b32 a10, a106\n"
"v_accvgpr_mov_b32 a15, a111\n"
"v_accvgpr_mov_b32 a22, a118\n"
"v_accvgpr_mov_b32 a30, a126\n"
"v_bfe_u32 v149, v242, 8, 2\n"
"v_lshl_add_u32 v149, v149, 4, s43\n"
"v_mfma_f32_16x16x32_bf16 v[24:27], v[96:99], a[244:247], v[24:27]\n"
"s_waitcnt lgkmcnt(2)\n"
"ds_read_b128 v[124:127], v149 offset:0\n"
"v_accvgpr_mov_b32 a28, a124\n"
"v_mfma_f32_16x16x32_bf16 v[24:27], v[100:103], a[248:251], v[24:27]\n"
"ds_read_b128 v[132:135], v149 offset:512\n"
"v_mov_b32_dpp v246, v246 row_ror:12 row_mask:0x2 bank_mask:0xf\n"
"v_accvgpr_mov_b32 a3, a99\n"
"v_accvgpr_mov_b32 a12, a108\n"
"v_accvgpr_mov_b32 a18, a114\n"
"v_accvgpr_mov_b32 a19, a115\n"
"v_accvgpr_mov_b32 a26, a122\n"
"v_accvgpr_mov_b32 a36, a132\n"
"v_xor_b32 v148, 0x400, v243\n"
"v_xor_b32 v150, 64, v149\n"
"v_accvgpr_mov_b32 a124, a76\n"
"v_mfma_f32_16x16x32_bf16 v[56:59], v[104:107], a[244:247], v[56:59]\n"
"ds_read_b128 v[128:131], v150 offset:0\n"
"v_accvgpr_mov_b32 a39, a135\n"
"v_mfma_f32_16x16x32_bf16 v[56:59], v[108:111], a[248:251], v[56:59]\n"
"ds_read_b128 v[136:139], v150 offset:512\n"
"v_rcp_f32 v247, v198\n"
"v_mov_b32_dpp v246, v246 row_ror:8 row_mask:0x4 bank_mask:0xf\n"
"v_accvgpr_mov_b32 a0, a96\n"
"v_accvgpr_mov_b32 a1, a97\n"
"v_accvgpr_mov_b32 a4, a100\n"
"v_accvgpr_mov_b32 a5, a101\n"
"v_accvgpr_mov_b32 a8, a104\n"
"v_accvgpr_mov_b32 a16, a112\n"
"v_accvgpr_mov_b32 a25, a121\n"
"v_mfma_f32_16x16x32_bf16 v[88:91], v[112:115], a[244:247], v[88:91]\n"
"v_accvgpr_mov_b32 a20, a116\n"
"v_accvgpr_mov_b32 a29, a125\n"
"v_mfma_f32_16x16x32_bf16 v[88:91], v[116:119], a[248:251], v[88:91]\n"
"s_waitcnt lgkmcnt(3)\n"
"ds_read_b128 a[192:195], v243 offset:4096\n"
"ds_read_b128 a[200:203], v243 offset:6144\n"
"v_accvgpr_mov_b32 a6, a102\n"
"v_accvgpr_mov_b32 a9, a105\n"
"v_accvgpr_mov_b32 a11, a107\n"
"v_accvgpr_mov_b32 a13, a109\n"
"v_accvgpr_mov_b32 a14, a110\n"
"v_accvgpr_mov_b32 a17, a113\n"
"v_accvgpr_mov_b32 a23, a119\n"
"v_accvgpr_mov_b32 a31, a127\n"
"v_accvgpr_mov_b32 a33, a129\n"
"v_accvgpr_mov_b32 a35, a131\n"
"v_accvgpr_mov_b32 a44, a140\n"
"v_accvgpr_mov_b32 a47, a143\n"
"v_accvgpr_mov_b32 a107, a59\n"
"v_mfma_f32_16x16x32_bf16 v[180:183], v[124:127], a[0:3], 0\n"
"s_waitcnt lgkmcnt(3)\n"
"v_xor_b32 v244, 0x11000, v242\n"
"v_accvgpr_mov_b32 a132, a84\n"
"v_mfma_f32_16x16x32_bf16 v[180:183], v[128:131], a[4:7], v[180:183]\n"
"v_accvgpr_mov_b32 a34, a130\n"
"v_accvgpr_mov_b32 a43, a139\n"
"v_mfma_f32_16x16x32_bf16 v[180:183], v[132:135], a[8:11], v[180:183]\n"
"s_waitcnt lgkmcnt(2)\n"
"ds_read_b128 a[204:207], v148 offset:6144\n"
"v_accvgpr_mov_b32 a37, a133\n"
"v_mfma_f32_16x16x32_bf16 v[180:183], v[136:139], a[12:15], v[180:183]\n"
"ds_read_b128 a[196:199], v148 offset:4096\n"
"v_accvgpr_mov_b32 a21, a117\n"
"v_accvgpr_mov_b32 a24, a120\n"
"v_accvgpr_mov_b32 a32, a128\n"
"v_accvgpr_mov_b32 a38, a134\n"
"v_accvgpr_mov_b32 a40, a136\n"
"v_accvgpr_mov_b32 a42, a138\n"
"v_accvgpr_mov_b32 a46, a142\n"
"v_accvgpr_mov_b32 a117, a69\n"
"v_accvgpr_mov_b32 a111, a63\n"
"v_mfma_f32_16x16x32_bf16 v[184:187], v[124:127], a[16:19], 0\n"
"v_mov_b32_dpp v246, v246 row_ror:4 row_mask:0x8 bank_mask:0xf\n"
"v_accvgpr_mov_b32 a27, a123\n"
"v_mfma_f32_16x16x32_bf16 v[184:187], v[128:131], a[20:23], v[184:187]\n"
"v_mov_b32_dpp v247, v247 row_ror:12 row_mask:0x2 bank_mask:0xf\n"
"v_accvgpr_mov_b32 a97, a49\n"
"v_mfma_f32_16x16x32_bf16 v[184:187], v[132:135], a[24:27], v[184:187]\n"
"s_add_u32 s40, s56, s53\n"
"ds_read_b64_tr_b16 v[224:225], v239 offset:49664\n"
"v_accvgpr_mov_b32 a112, a64\n"
"v_mfma_f32_16x16x32_bf16 v[184:187], v[136:139], a[28:31], v[184:187]\n"
"buffer_load_dwordx4 v240, s[20:23], s40 offen offset:2048 nt lds\n"
"v_accvgpr_mov_b32 a41, a137\n"
"v_accvgpr_mov_b32 a126, a78\n"
"v_accvgpr_mov_b32 a114, a66\n"
"v_accvgpr_mov_b32 a137, a89\n"
"v_sub_f32 v180, s74, v180\n"
"v_sub_f32 v181, s74, v181\n"
"v_sub_f32 v183, s74, v183\n"
"v_mul_f32_dpp v26, v246, v26 row_newbcast:2 row_mask:0xf bank_mask:0xf\n"
"v_mfma_f32_16x16x32_bf16 v[188:191], v[124:127], a[32:35], 0\n"
"v_mov_b32_dpp v247, v247 row_ror:8 row_mask:0x4 bank_mask:0xf\n"
"v_accvgpr_mov_b32 a45, a141\n"
"v_mfma_f32_16x16x32_bf16 v[188:191], v[128:131], a[36:39], v[188:191]\n"
"v_mov_b32 v252, v192\n"
"v_accvgpr_mov_b32 a133, a85\n"
"v_mfma_f32_16x16x32_bf16 v[188:191], v[132:135], a[40:43], v[188:191]\n"
"v_accvgpr_mov_b32 a118, a70\n"
"v_accvgpr_mov_b32 a105, a57\n"
"v_mfma_f32_16x16x32_bf16 v[188:191], v[136:139], a[44:47], v[188:191]\n"
"s_waitcnt lgkmcnt(1)\n"
"v_accvgpr_mov_b32 a109, a61\n"
"v_mul_f32_dpp v11, v246, v11 row_newbcast:3 row_mask:0xf bank_mask:0xf\n"
"v_mfma_f32_16x16x32_bf16 v[192:195], v[124:127], a[192:195], 0\n"
"ds_read_b64_tr_b16 a[252:253], v235 offset:49664\n"
"v_sub_f32 v185, s74, v185\n"
"v_mfma_f32_16x16x32_bf16 v[192:195], v[128:131], a[196:199], v[192:195]\n"
"ds_read_b64_tr_b16 a[248:249], v236 offset:49152\n"
"v_sub_f32 v182, s74, v182\n"
"v_mfma_f32_16x16x32_bf16 v[192:195], v[132:135], a[200:203], v[192:195]\n"
"v_accvgpr_mov_b32 a138, a90\n"
"v_sub_f32 v184, s74, v184\n"
"v_mfma_f32_16x16x32_bf16 v[192:195], v[136:139], a[204:207], v[192:195]\n"
"ds_read_b64_tr_b16 a[254:255], v235 offset:53760\n"
"ds_read_b64_tr_b16 a[246:247], v232 offset:53248\n"
"v_mov_b32 v253, v202\n"
"v_accvgpr_mov_b32 a116, a68\n"
"v_accvgpr_mov_b32 a125, a77\n"
"v_accvgpr_mov_b32 a113, a65\n"
"v_accvgpr_mov_b32 a128, a80\n"
"v_accvgpr_mov_b32 a130, a82\n"
"v_accvgpr_mov_b32 a136, a88\n"
"v_sub_f32 v186, s74, v186\n"
"v_mfma_f32_16x16x32_bf16 v[152:155], v[208:211], a[0:3], v[180:183]\n"
"v_accvgpr_mov_b32 a103, a55\n"
"v_accvgpr_mov_b32 a120, a72\n"
"v_mfma_f32_16x16x32_bf16 v[152:155], v[212:215], a[4:7], v[152:155]\n"
"v_accvgpr_mov_b32 a141, a93\n"
"v_sub_f32 v187, s74, v187\n"
"v_mfma_f32_16x16x32_bf16 v[152:155], v[216:219], a[8:11], v[152:155]\n"
"s_add_u32 s40, s56, s54\n"
"v_accvgpr_mov_b32 a127, a79\n"
"v_accvgpr_mov_b32 a140, a92\n"
"v_mfma_f32_16x16x32_bf16 v[152:155], v[220:223], a[12:15], v[152:155]\n"
"buffer_load_dwordx4 v207, s[20:23], s40 offen offset:3072 nt lds\n"
"v_mov_b32_dpp v247, v247 row_ror:4 row_mask:0x8 bank_mask:0xf\n"
"v_accvgpr_mov_b32 a121, a73\n"
"v_accvgpr_mov_b32 a122, a74\n"
"v_accvgpr_mov_b32 a123, a75\n"
"v_sub_f32 v189, s74, v189\n"
"v_sub_f32 v190, s74, v190\n"
"v_sub_f32 v191, s74, v191\n"
"v_sub_f32 v192, s74, v192\n"
"v_mfma_f32_16x16x32_bf16 v[156:159], v[216:219], a[24:27], v[184:187]\n"
"s_add_u32 s55, s55, s59\n"
"v_accvgpr_mov_b32 a143, a95\n"
"v_accvgpr_mov_b32 a115, a67\n"
"v_mfma_f32_16x16x32_bf16 v[156:159], v[220:223], a[28:31], v[156:159]\n"
"s_or_b32 s55, s55, s58\n"
"v_accvgpr_mov_b32 a101, a53\n"
"v_sub_f32 v188, s74, v188\n"
"v_mfma_f32_16x16x32_bf16 v[156:159], v[208:211], a[16:19], v[156:159]\n"
"s_xor_b32 s56, s55, s57\n"
"v_exp_f32 v153, v153\n"
"v_mfma_f32_16x16x32_bf16 v[156:159], v[212:215], a[20:23], v[156:159]\n"
"ds_write_b64 v250, v[252:253] offset:52224\n"
"v_accvgpr_mov_b32 a100, a52\n"
"v_accvgpr_mov_b32 a135, a87\n"
"v_accvgpr_mov_b32 a98, a50\n"
"v_sub_f32 v193, s74, v193\n"
"v_sub_f32 v194, s74, v194\n"
"v_exp_f32 v152, v152\n"
"v_mfma_f32_16x16x32_bf16 v[160:163], v[208:211], a[32:35], v[188:191]\n"
"v_accvgpr_mov_b32 a110, a62\n"
"v_accvgpr_mov_b32 a96, a48\n"
"v_mfma_f32_16x16x32_bf16 v[160:163], v[212:215], a[36:39], v[160:163]\n"
"v_rcp_f32 v206, v200\n"
"v_mfma_f32_16x16x32_bf16 v[160:163], v[216:219], a[40:43], v[160:163]\n"
"v_accvgpr_mov_b32 a142, a94\n"
"v_sub_f32 v195, s74, v195\n"
"v_mfma_f32_16x16x32_bf16 v[160:163], v[220:223], a[44:47], v[160:163]\n"
"ds_write_b128 v244, a[216:219] offset:32768\n"
"v_accvgpr_mov_b32 a102, a54\n"
"v_accvgpr_mov_b32 a108, a60\n"
"v_accvgpr_mov_b32 a99, a51\n"
"v_accvgpr_mov_b32 a129, a81\n"
"v_mul_f32_dpp v25, v246, v25 row_newbcast:1 row_mask:0xf bank_mask:0xf\n"
"v_mfma_f32_16x16x32_bf16 v[164:167], v[216:219], a[200:203], v[192:195]\n"
"v_mov_b32_dpp v206, v206 row_ror:12 row_mask:0x2 bank_mask:0xf\n"
"v_accvgpr_mov_b32 a119, a71\n"
"v_mfma_f32_16x16x32_bf16 v[164:167], v[220:223], a[204:207], v[164:167]\n"
"v_accvgpr_mov_b32 a131, a83\n"
"v_accvgpr_mov_b32 a139, a91\n"
"v_mfma_f32_16x16x32_bf16 v[164:167], v[208:211], a[192:195], v[164:167]\n"
"ds_read_b128 v[208:211], v228 offset:4096\n"
"v_accvgpr_mov_b32 a106, a58\n"
"v_mfma_f32_16x16x32_bf16 v[164:167], v[212:215], a[196:199], v[164:167]\n"
"s_waitcnt lgkmcnt(2)\n"
"ds_read_b128 v[216:219], v228 offset:4608\n"
"ds_read_b128 v[220:223], v229 offset:4608\n"
"v_mov_b32_dpp v206, v206 row_ror:8 row_mask:0x4 bank_mask:0xf\n"
"v_accvgpr_mov_b32 a134, a86\n"
"v_accvgpr_mov_b32 a104, a56\n"
"v_exp_f32 v155, v155\n"
"v_exp_f32 v160, v160\n"
"s_branch LTC_%=\n"
"LTH2_%=:\n"
"v_mfma_f32_16x16x32_bf16 a[224:227], v[124:127], a[244:247], a[224:227]\n"
"v_exp_f32 v171, v171\n"
"v_mfma_f32_16x16x32_bf16 v[16:19], v[128:131], a[244:247], v[16:19]\n"
"s_waitcnt vmcnt(8)\n"
"v_exp_f32 v172, v172\n"
"v_mfma_f32_16x16x32_bf16 v[16:19], v[132:135], a[248:251], v[16:19]\n"
"s_barrier\n"
"v_mfma_f32_16x16x32_bf16 v[48:51], v[136:139], a[244:247], v[48:51]\n"
"v_exp_f32 v169, v169\n"
"v_mfma_f32_16x16x32_bf16 v[48:51], v[140:143], a[248:251], v[48:51]\n"
"ds_read_b128 v[220:223], v229 offset:512\n"
"v_cvt_pk_bf16_f32 v117, v166, v167\n"
"v_exp_f32 v176, v176\n"
"v_exp_f32 v177, v177\n"
"v_exp_f32 v178, v178\n"
"v_mfma_f32_16x16x32_bf16 v[80:83], v[144:147], a[244:247], v[80:83]\n"
"ds_read_b64_tr_b16 a[244:245], v234 offset:49152\n"
"v_cvt_pk_bf16_f32 v108, v160, v161\n"
"v_mfma_f32_16x16x32_bf16 v[80:83], v[148:151], a[248:251], v[80:83]\n"
"ds_read_b64_tr_b16 a[246:247], v234 offset:53248\n"
"ds_read_b64_tr_b16 a[248:249], v238 offset:49152\n"
"v_exp_f32 v162, v162\n"
"v_exp_f32 v168, v168\n"
"v_exp_f32 v173, v173\n"
"v_exp_f32 v175, v175\n"
"v_mfma_f32_16x16x32_bf16 v[84:87], v[148:151], v[224:227], v[84:87]\n"
"ds_read_b64_tr_b16 a[250:251], v238 offset:53248\n"
"v_cvt_pk_bf16_f32 v116, v164, v165\n"
"v_mfma_f32_16x16x32_bf16 v[84:87], v[144:147], a[252:255], v[84:87]\n"
"v_exp_f32 v170, v170\n"
"v_mfma_f32_16x16x32_bf16 v[52:55], v[140:143], v[224:227], v[52:55]\n"
"v_cvt_pk_bf16_f32 v103, v170, v171\n"
"v_cvt_pk_bf16_f32 v110, v172, v173\n"
"v_mfma_f32_16x16x32_bf16 v[52:55], v[136:139], a[252:255], v[52:55]\n"
"v_cvt_pk_bf16_f32 v123, v154, v155\n"
"v_cvt_pk_bf16_f32 v101, v158, v159\n"
"v_mfma_f32_16x16x32_bf16 v[20:23], v[132:135], v[224:227], v[20:23]\n"
"v_cvt_pk_bf16_f32 v100, v156, v157\n"
"v_cvt_pk_bf16_f32 v102, v168, v169\n"
"v_mfma_f32_16x16x32_bf16 v[20:23], v[128:131], a[252:255], v[20:23]\n"
"v_exp_f32 v179, v179\n"
"v_mfma_f32_16x16x32_bf16 a[228:231], v[124:127], a[252:255], a[228:231]\n"
"s_waitcnt lgkmcnt(2)\n"
"ds_read_b64_tr_b16 a[252:253], v235 offset:49152\n"
"ds_read_b64_tr_b16 v[226:227], v239 offset:53248\n"
"v_mfma_f32_16x16x32_bf16 a[216:219], v[120:123], a[244:247], a[216:219]\n"
"s_add_u32 m0, s44, 81920\n"
"ds_read_b64_tr_b16 v[224:225], v239 offset:49152\n"
"v_cvt_pk_bf16_f32 v119, v178, v179\n"
"v_smfmac_f32_16x16x64_bf16 v[196:199], a[240:243], v[96:103], v241\n"
"buffer_load_dwordx4 v240, s[20:23], s56 offen nt lds\n"
"v_mfma_f32_16x16x32_bf16 v[8:11], v[96:99], a[244:247], v[8:11]\n"
"s_waitcnt lgkmcnt(3)\n"
"ds_read_b64_tr_b16 a[254:255], v235 offset:53248\n"
"v_cvt_pk_bf16_f32 v118, v176, v177\n"
"v_mfma_f32_16x16x32_bf16 v[8:11], v[100:103], a[248:251], v[8:11]\n"
"v_exp_f32 v174, v174\n"
"v_smfmac_f32_16x16x64_bf16 v[200:203], a[240:243], v[112:119], v241\n"
"v_cvt_pk_bf16_f32 v109, v162, v163\n"
"v_cvt_pk_bf16_f32 v111, v174, v175\n"
"v_mfma_f32_16x16x32_bf16 v[40:43], v[104:107], a[244:247], v[40:43]\n"
"ds_read_b128 v[212:215], v229 offset:0\n"
"v_and_b32 v245, 0x3f0, v242\n"
"v_mfma_f32_16x16x32_bf16 v[40:43], v[108:111], a[248:251], v[40:43]\n"
"v_lshlrev_b32_e64 v244, 24, s43\n"
"v_mul_f32 v244, 0x45800000, v244\n"
"v_smfmac_f32_16x16x64_bf16 v[198:201], a[240:243], v[104:111], v241\n"
"ds_read_b128 v[208:211], v228 offset:0\n"
"v_add_f32 v196, v196, v197\n"
"v_mfma_f32_16x16x32_bf16 v[72:75], v[112:115], a[244:247], v[72:75]\n"
"ds_read_b64_tr_b16 a[244:245], v234 offset:49664\n"
"v_lshrrev_b32 v245, 1, v245\n"
"v_mfma_f32_16x16x32_bf16 v[72:75], v[116:119], a[248:251], v[72:75]\n"
"ds_read_b64_tr_b16 a[246:247], v234 offset:53760\n"
"v_sub_u32 v245, v242, v245\n"
"v_mfma_f32_16x16x32_bf16 v[202:205], a[240:243], v[120:123], v[202:205]\n"
"s_waitcnt lgkmcnt(4)\n"
"ds_read_b64_tr_b16 a[248:249], v238 offset:49664\n"
"v_add_f32 v200, v200, v201\n"
"v_mfma_f32_16x16x32_bf16 v[76:79], v[116:119], v[224:227], v[76:79]\n"
"v_add_f32 v198, v198, v199\n"
"v_add_f32 v254, v196, v198\n"
"v_mfma_f32_16x16x32_bf16 v[76:79], v[112:115], a[252:255], v[76:79]\n"
"ds_read_b64_tr_b16 a[250:251], v238 offset:53760\n"
"v_xor_b32 v250, 0x1000, v245\n"
"v_mfma_f32_16x16x32_bf16 v[44:47], v[108:111], v[224:227], v[44:47]\n"
"ds_read_b128 v[216:219], v228 offset:512\n"
"v_add_f32 v254, v254, v200\n"
"v_mfma_f32_16x16x32_bf16 v[44:47], v[104:107], a[252:255], v[44:47]\n"
"v_add_f32 v244, v244, v202\n"
"v_add_f32 v254, v254, v202\n"
"v_mfma_f32_16x16x32_bf16 v[12:15], v[100:103], v[224:227], v[12:15]\n"
"s_add_u32 s40, s56, s52\n"
"ds_read_b64_tr_b16 v[226:227], v239 offset:53760\n"
"v_min3_f32 v244, v244, v196, v198\n"
"v_mfma_f32_16x16x32_bf16 v[12:15], v[96:99], a[252:255], v[12:15]\n"
"buffer_load_dwordx4 v207, s[20:23], s40 offen offset:1024 nt lds\n"
"v_min_f32 v244, v244, v200\n"
"v_rcp_f32 v244, v244\n"
"v_mfma_f32_16x16x32_bf16 a[220:223], v[120:123], a[252:255], a[220:223]\n"
"s_waitcnt lgkmcnt(4)\n"
"v_fmac_f32 v254, 0x68800000, v244\n"
"v_cmp_nge_f32 vcc, s45, v254\n"
"s_cbranch_vccnz LSLOW_%=\nLBACK2_%=:\ns_mov_b64 vcc, s[64:65]\n"
"v_mfma_f32_16x16x32_bf16 a[232:235], v[120:123], a[244:247], a[232:235]\n"
"v_rcp_f32 v246, v196\n"
"v_accvgpr_mov_b32 a2, a146\n"
"v_accvgpr_mov_b32 a7, a151\n"
"v_accvgpr_mov_b32 a10, a154\n"
"v_accvgpr_mov_b32 a15, a159\n"
"v_accvgpr_mov_b32 a22, a166\n"
"v_accvgpr_mov_b32 a30, a174\n"
"v_bfe_u32 v149, v242, 8, 2\n"
"v_lshl_add_u32 v149, v149, 4, s43\n"
"v_mfma_f32_16x16x32_bf16 v[24:27], v[96:99], a[244:247], v[24:27]\n"
"s_waitcnt lgkmcnt(2)\n"
"ds_read_b128 v[124:127], v149 offset:0\n"
"v_accvgpr_mov_b32 a28, a172\n"
"v_mfma_f32_16x16x32_bf16 v[24:27], v[100:103], a[248:251], v[24:27]\n"
"ds_read_b128 v[132:135], v149 offset:512\n"
"v_mov_b32_dpp v246, v246 row_ror:12 row_mask:0x2 bank_mask:0xf\n"
"v_accvgpr_mov_b32 a3, a147\n"
"v_accvgpr_mov_b32 a12, a156\n"
"v_accvgpr_mov_b32 a18, a162\n"
"v_accvgpr_mov_b32 a19, a163\n"
"v_accvgpr_mov_b32 a26, a170\n"
"v_accvgpr_mov_b32 a36, a180\n"
"v_xor_b32 v148, 0x400, v243\n"
"v_xor_b32 v150, 64, v149\n"
"v_accvgpr_mov_b32 a172, a76\n"
"v_mfma_f32_16x16x32_bf16 v[56:59], v[104:107], a[244:247], v[56:59]\n"
"ds_read_b128 v[128:131], v150 offset:0\n"
"v_accvgpr_mov_b32 a39, a183\n"
"v_mfma_f32_16x16x32_bf16 v[56:59], v[108:111], a[248:251], v[56:59]\n"
"ds_read_b128 v[136:139], v150 offset:512\n"
"v_rcp_f32 v247, v198\n"
"v_mov_b32_dpp v246, v246 row_ror:8 row_mask:0x4 bank_mask:0xf\n"
"v_accvgpr_mov_b32 a0, a144\n"
"v_accvgpr_mov_b32 a1, a145\n"
"v_accvgpr_mov_b32 a4, a148\n"
"v_accvgpr_mov_b32 a5, a149\n"
"v_accvgpr_mov_b32 a8, a152\n"
"v_accvgpr_mov_b32 a16, a160\n"
"v_accvgpr_mov_b32 a25, a169\n"
"v_mfma_f32_16x16x32_bf16 v[88:91], v[112:115], a[244:247], v[88:91]\n"
"v_accvgpr_mov_b32 a20, a164\n"
"v_accvgpr_mov_b32 a29, a173\n"
"v_mfma_f32_16x16x32_bf16 v[88:91], v[116:119], a[248:251], v[88:91]\n"
"s_waitcnt lgkmcnt(3)\n"
"ds_read_b128 a[192:195], v243 offset:4096\n"
"ds_read_b128 a[200:203], v243 offset:6144\n"
"v_accvgpr_mov_b32 a6, a150\n"
"v_accvgpr_mov_b32 a9, a153\n"
"v_accvgpr_mov_b32 a11, a155\n"
"v_accvgpr_mov_b32 a13, a157\n"
"v_accvgpr_mov_b32 a14, a158\n"
"v_accvgpr_mov_b32 a17, a161\n"
"v_accvgpr_mov_b32 a23, a167\n"
"v_accvgpr_mov_b32 a31, a175\n"
"v_accvgpr_mov_b32 a33, a177\n"
"v_accvgpr_mov_b32 a35, a179\n"
"v_accvgpr_mov_b32 a44, a188\n"
"v_accvgpr_mov_b32 a47, a191\n"
"v_accvgpr_mov_b32 a155, a59\n"
"v_mfma_f32_16x16x32_bf16 v[180:183], v[124:127], a[0:3], 0\n"
"s_waitcnt lgkmcnt(3)\n"
"v_xor_b32 v244, 0x11000, v242\n"
"v_accvgpr_mov_b32 a180, a84\n"
"v_mfma_f32_16x16x32_bf16 v[180:183], v[128:131], a[4:7], v[180:183]\n"
"v_accvgpr_mov_b32 a34, a178\n"
"v_accvgpr_mov_b32 a43, a187\n"
"v_mfma_f32_16x16x32_bf16 v[180:183], v[132:135], a[8:11], v[180:183]\n"
"s_waitcnt lgkmcnt(2)\n"
"ds_read_b128 a[204:207], v148 offset:6144\n"
"v_accvgpr_mov_b32 a37, a181\n"
"v_mfma_f32_16x16x32_bf16 v[180:183], v[136:139], a[12:15], v[180:183]\n"
"ds_read_b128 a[196:199], v148 offset:4096\n"
"v_accvgpr_mov_b32 a21, a165\n"
"v_accvgpr_mov_b32 a24, a168\n"
"v_accvgpr_mov_b32 a32, a176\n"
"v_accvgpr_mov_b32 a38, a182\n"
"v_accvgpr_mov_b32 a40, a184\n"
"v_accvgpr_mov_b32 a42, a186\n"
"v_accvgpr_mov_b32 a46, a190\n"
"v_accvgpr_mov_b32 a165, a69\n"
"v_accvgpr_mov_b32 a159, a63\n"
"v_mfma_f32_16x16x32_bf16 v[184:187], v[124:127], a[16:19], 0\n"
"v_mov_b32_dpp v246, v246 row_ror:4 row_mask:0x8 bank_mask:0xf\n"
"v_accvgpr_mov_b32 a27, a171\n"
"v_mfma_f32_16x16x32_bf16 v[184:187], v[128:131], a[20:23], v[184:187]\n"
"v_mov_b32_dpp v247, v247 row_ror:12 row_mask:0x2 bank_mask:0xf\n"
"v_accvgpr_mov_b32 a145, a49\n"
"v_mfma_f32_16x16x32_bf16 v[184:187], v[132:135], a[24:27], v[184:187]\n"
"s_add_u32 s40, s56, s53\n"
"ds_read_b64_tr_b16 v[224:225], v239 offset:49664\n"
"v_accvgpr_mov_b32 a160, a64\n"
"v_mfma_f32_16x16x32_bf16 v[184:187], v[136:139], a[28:31], v[184:187]\n"
"buffer_load_dwordx4 v240, s[20:23], s40 offen offset:2048 nt lds\n"
"v_accvgpr_mov_b32 a41, a185\n"
"v_accvgpr_mov_b32 a174, a78\n"
"v_accvgpr_mov_b32 a162, a66\n"
"v_accvgpr_mov_b32 a185, a89\n"
"v_sub_f32 v180, s74, v180\n"
"v_sub_f32 v181, s74, v181\n"
"v_sub_f32 v183, s74, v183\n"
"v_mul_f32_dpp v26, v246, v26 row_newbcast:2 row_mask:0xf bank_mask:0xf\n"
"v_mfma_f32_16x16x32_bf16 v[188:191], v[124:127], a[32:35], 0\n"
"v_mov_b32_dpp v247, v247 row_ror:8 row_mask:0x4 bank_mask:0xf\n"
"v_accvgpr_mov_b32 a45, a189\n"
"v_mfma_f32_16x16x32_bf16 v[188:191], v[128:131], a[36:39], v[188:191]\n"
"v_mov_b32 v252, v192\n"
"v_accvgpr_mov_b32 a181, a85\n"
"v_mfma_f32_16x16x32_bf16 v[188:191], v[132:135], a[40:43], v[188:191]\n"
"v_accvgpr_mov_b32 a166, a70\n"
"v_accvgpr_mov_b32 a153, a57\n"
"v_mfma_f32_16x16x32_bf16 v[188:191], v[136:139], a[44:47], v[188:191]\n"
"s_waitcnt lgkmcnt(1)\n"
"v_accvgpr_mov_b32 a157, a61\n"
"v_mul_f32_dpp v11, v246, v11 row_newbcast:3 row_mask:0xf bank_mask:0xf\n"
"v_mfma_f32_16x16x32_bf16 v[192:195], v[124:127], a[192:195], 0\n"
"ds_read_b64_tr_b16 a[252:253], v235 offset:49664\n"
"v_sub_f32 v185, s74, v185\n"
"v_mfma_f32_16x16x32_bf16 v[192:195], v[128:131], a[196:199], v[192:195]\n"
"ds_read_b64_tr_b16 a[248:249], v236 offset:49152\n"
"v_sub_f32 v182, s74, v182\n"
"v_mfma_f32_16x16x32_bf16 v[192:195], v[132:135], a[200:203], v[192:195]\n"
"v_accvgpr_mov_b32 a186, a90\n"
"v_sub_f32 v184, s74, v184\n"
"v_mfma_f32_16x16x32_bf16 v[192:195], v[136:139], a[204:207], v[192:195]\n"
"ds_read_b64_tr_b16 a[254:255], v235 offset:53760\n"
"ds_read_b64_tr_b16 a[246:247], v232 offset:53248\n"
"v_mov_b32 v253, v202\n"
"v_accvgpr_mov_b32 a164, a68\n"
"v_accvgpr_mov_b32 a173, a77\n"
"v_accvgpr_mov_b32 a161, a65\n"
"v_accvgpr_mov_b32 a176, a80\n"
"v_accvgpr_mov_b32 a178, a82\n"
"v_accvgpr_mov_b32 a184, a88\n"
"v_sub_f32 v186, s74, v186\n"
"v_mfma_f32_16x16x32_bf16 v[152:155], v[208:211], a[0:3], v[180:183]\n"
"v_accvgpr_mov_b32 a151, a55\n"
"v_accvgpr_mov_b32 a168, a72\n"
"v_mfma_f32_16x16x32_bf16 v[152:155], v[212:215], a[4:7], v[152:155]\n"
"v_accvgpr_mov_b32 a189, a93\n"
"v_sub_f32 v187, s74, v187\n"
"v_mfma_f32_16x16x32_bf16 v[152:155], v[216:219], a[8:11], v[152:155]\n"
"s_add_u32 s40, s56, s54\n"
"v_accvgpr_mov_b32 a175, a79\n"
"v_accvgpr_mov_b32 a188, a92\n"
"v_mfma_f32_16x16x32_bf16 v[152:155], v[220:223], a[12:15], v[152:155]\n"
"buffer_load_dwordx4 v207, s[20:23], s40 offen offset:3072 nt lds\n"
"v_mov_b32_dpp v247, v247 row_ror:4 row_mask:0x8 bank_mask:0xf\n"
"v_accvgpr_mov_b32 a169, a73\n"
"v_accvgpr_mov_b32 a170, a74\n"
"v_accvgpr_mov_b32 a171, a75\n"
"v_sub_f32 v189, s74, v189\n"
"v_sub_f32 v190, s74, v190\n"
"v_sub_f32 v191, s74, v191\n"
"v_sub_f32 v192, s74, v192\n"
"v_mfma_f32_16x16x32_bf16 v[156:159], v[216:219], a[24:27], v[184:187]\n"
"s_add_u32 s55, s55, s59\n"
"v_accvgpr_mov_b32 a191, a95\n"
"v_accvgpr_mov_b32 a163, a67\n"
"v_mfma_f32_16x16x32_bf16 v[156:159], v[220:223], a[28:31], v[156:159]\n"
"s_or_b32 s55, s55, s58\n"
"v_accvgpr_mov_b32 a149, a53\n"
"v_sub_f32 v188, s74, v188\n"
"v_mfma_f32_16x16x32_bf16 v[156:159], v[208:211], a[16:19], v[156:159]\n"
"s_xor_b32 s56, s55, s57\n"
"v_exp_f32 v153, v153\n"
"v_mfma_f32_16x16x32_bf16 v[156:159], v[212:215], a[20:23], v[156:159]\n"
"ds_write_b64 v250, v[252:253] offset:52224\n"
"v_accvgpr_mov_b32 a148, a52\n"
"v_accvgpr_mov_b32 a183, a87\n"
"v_accvgpr_mov_b32 a146, a50\n"
"v_sub_f32 v193, s74, v193\n"
"v_sub_f32 v194, s74, v194\n"
"v_exp_f32 v152, v152\n"
"v_mfma_f32_16x16x32_bf16 v[160:163], v[208:211], a[32:35], v[188:191]\n"
"v_accvgpr_mov_b32 a158, a62\n"
"v_accvgpr_mov_b32 a144, a48\n"
"v_mfma_f32_16x16x32_bf16 v[160:163], v[212:215], a[36:39], v[160:163]\n"
"v_rcp_f32 v206, v200\n"
"v_mfma_f32_16x16x32_bf16 v[160:163], v[216:219], a[40:43], v[160:163]\n"
"v_accvgpr_mov_b32 a190, a94\n"
"v_sub_f32 v195, s74, v195\n"
"v_mfma_f32_16x16x32_bf16 v[160:163], v[220:223], a[44:47], v[160:163]\n"
"ds_write_b128 v244, a[216:219] offset:32768\n"
"v_accvgpr_mov_b32 a150, a54\n"
"v_accvgpr_mov_b32 a156, a60\n"
"v_accvgpr_mov_b32 a147, a51\n"
"v_accvgpr_mov_b32 a177, a81\n"
"v_mul_f32_dpp v25, v246, v25 row_newbcast:1 row_mask:0xf bank_mask:0xf\n"
"v_mfma_f32_16x16x32_bf16 v[164:167], v[216:219], a[200:203], v[192:195]\n"
"v_mov_b32_dpp v206, v206 row_ror:12 row_mask:0x2 bank_mask:0xf\n"
"v_accvgpr_mov_b32 a167, a71\n"
"v_mfma_f32_16x16x32_bf16 v[164:167], v[220:223], a[204:207], v[164:167]\n"
"v_accvgpr_mov_b32 a179, a83\n"
"v_accvgpr_mov_b32 a187, a91\n"
"v_mfma_f32_16x16x32_bf16 v[164:167], v[208:211], a[192:195], v[164:167]\n"
"ds_read_b128 v[208:211], v228 offset:4096\n"
"v_accvgpr_mov_b32 a154, a58\n"
"v_mfma_f32_16x16x32_bf16 v[164:167], v[212:215], a[196:199], v[164:167]\n"
"s_waitcnt lgkmcnt(2)\n"
"ds_read_b128 v[216:219], v228 offset:4608\n"
"ds_read_b128 v[220:223], v229 offset:4608\n"
"v_mov_b32_dpp v206, v206 row_ror:8 row_mask:0x4 bank_mask:0xf\n"
"v_accvgpr_mov_b32 a182, a86\n"
"v_accvgpr_mov_b32 a152, a56\n"
"v_exp_f32 v155, v155\n"
"v_exp_f32 v160, v160\n"
"LTC_%=:\n"
"s_waitcnt lgkmcnt(4)\n"
"v_mfma_f32_16x16x32_bf16 v[92:95], v[116:119], v[224:227], v[92:95]\n"
"v_exp_f32 v154, v154\n"
"v_mfma_f32_16x16x32_bf16 v[92:95], v[112:115], a[252:255], v[92:95]\n"
"ds_read_b128 v[212:215], v229 offset:4096\n"
"v_mul_f32_dpp v24, v246, v24 row_newbcast:0 row_mask:0xf bank_mask:0xf\n"
"v_mfma_f32_16x16x32_bf16 v[60:63], v[108:111], v[224:227], v[60:63]\n"
"s_add_u32 m0, s44, 32768\n"
"v_exp_f32 v163, v163\n"
"v_mfma_f32_16x16x32_bf16 v[60:63], v[104:107], a[252:255], v[60:63]\n"
"buffer_load_dwordx4 v240, s[16:19], s56 offen nt lds\n"
"v_mul_f32_dpp v8, v246, v8 row_newbcast:0 row_mask:0xf bank_mask:0xf\n"
"v_exp_f32 v156, v156\n"
"v_exp_f32 v158, v158\n"
"v_exp_f32 v161, v161\n"
"v_exp_f32 v165, v165\n"
"v_mfma_f32_16x16x32_bf16 v[28:31], v[100:103], v[224:227], v[28:31]\n"
"v_cvt_pk_bf16_f32 v128, v152, v153\n"
"v_cvt_pk_bf16_f32 v129, v154, v155\n"
"v_mfma_f32_16x16x32_bf16 v[28:31], v[96:99], a[252:255], v[28:31]\n"
"v_mov_b32_dpp v206, v206 row_ror:4 row_mask:0x8 bank_mask:0xf\n"
"v_mul_f32_dpp v9, v246, v9 row_newbcast:1 row_mask:0xf bank_mask:0xf\n"
"v_mfma_f32_16x16x32_bf16 a[236:239], v[120:123], a[252:255], a[236:239]\n"
"s_waitcnt lgkmcnt(2)\n"
"v_exp_f32 v162, v162\n"
"v_mfma_f32_16x16x32_bf16 v[168:171], v[216:219], a[8:11], v[180:183]\n"
"s_waitcnt lgkmcnt(1)\n"
"v_exp_f32 v167, v167\n"
"v_mfma_f32_16x16x32_bf16 v[168:171], v[220:223], a[12:15], v[168:171]\n"
"v_mul_f32_dpp v12, v246, v12 row_newbcast:0 row_mask:0xf bank_mask:0xf\n"
"v_mul_f32_dpp v46, v247, v46 row_newbcast:2 row_mask:0xf bank_mask:0xf\n"
"v_mfma_f32_16x16x32_bf16 v[168:171], v[208:211], a[0:3], v[168:171]\n"
"s_waitcnt lgkmcnt(0)\n"
"ds_read_b64_tr_b16 a[244:245], v232 offset:49152\n"
"v_cvt_pk_bf16_f32 v145, v162, v163\n"
"v_mfma_f32_16x16x32_bf16 v[168:171], v[212:215], a[4:7], v[168:171]\n"
"v_exp_f32 v159, v159\n"
"v_mfma_f32_16x16x32_bf16 v[172:175], v[208:211], a[16:19], v[184:187]\n"
"ds_read_b64_tr_b16 a[252:253], v233 offset:49152\n"
"v_mul_f32_dpp v72, v206, v72 row_newbcast:0 row_mask:0xf bank_mask:0xf\n"
"v_mfma_f32_16x16x32_bf16 v[172:175], v[212:215], a[20:23], v[172:175]\n"
"v_exp_f32 v164, v164\n"
"v_mfma_f32_16x16x32_bf16 v[172:175], v[216:219], a[24:27], v[172:175]\n"
"ds_read_b64_tr_b16 a[250:251], v236 offset:53248\n"
"v_mul_f32_dpp v14, v246, v14 row_newbcast:2 row_mask:0xf bank_mask:0xf\n"
"v_mfma_f32_16x16x32_bf16 v[172:175], v[220:223], a[28:31], v[172:175]\n"
"ds_write_b128 v244, a[220:223] offset:33792\n"
"v_mul_f32_dpp v78, v206, v78 row_newbcast:2 row_mask:0xf bank_mask:0xf\n"
"v_exp_f32 v157, v157\n"
"v_mul_f32_dpp v89, v206, v89 row_newbcast:1 row_mask:0xf bank_mask:0xf\n"
"v_exp_f32 v168, v168\n"
"v_mfma_f32_16x16x32_bf16 v[176:179], v[216:219], a[40:43], v[188:191]\n"
"v_mul_f32_dpp v10, v246, v10 row_newbcast:2 row_mask:0xf bank_mask:0xf\n"
"v_mul_f32_dpp v40, v247, v40 row_newbcast:0 row_mask:0xf bank_mask:0xf\n"
"v_mfma_f32_16x16x32_bf16 v[176:179], v[220:223], a[44:47], v[176:179]\n"
"v_cvt_pk_bf16_f32 v137, v158, v159\n"
"v_mul_f32_dpp v28, v246, v28 row_newbcast:0 row_mask:0xf bank_mask:0xf\n"
"v_mfma_f32_16x16x32_bf16 v[176:179], v[208:211], a[32:35], v[176:179]\n"
"s_add_u32 s40, s56, s52\n"
"v_mul_f32_dpp v73, v206, v73 row_newbcast:1 row_mask:0xf bank_mask:0xf\n"
"v_cvt_pk_bf16_f32 v136, v156, v157\n"
"v_mfma_f32_16x16x32_bf16 v[176:179], v[212:215], a[36:39], v[176:179]\n"
"buffer_load_dwordx4 v207, s[16:19], s40 offen offset:1024 nt lds\n"
"v_mfma_f32_16x16x32_bf16 v[152:155], v[208:211], a[192:195], v[192:195]\n"
"v_mul_f32_dpp v76, v206, v76 row_newbcast:0 row_mask:0xf bank_mask:0xf\n"
"v_mul_f32_dpp v88, v206, v88 row_newbcast:0 row_mask:0xf bank_mask:0xf\n"
"v_mfma_f32_16x16x32_bf16 v[152:155], v[212:215], a[196:199], v[152:155]\n"
"ds_read_b128 v[208:211], v230 offset:0\n"
"v_cvt_pk_bf16_f32 v144, v160, v161\n"
"v_mfma_f32_16x16x32_bf16 v[152:155], v[216:219], a[200:203], v[152:155]\n"
"v_mul_f32_dpp v93, v206, v93 row_newbcast:1 row_mask:0xf bank_mask:0xf\n"
"v_cvt_pk_bf16_f32 v88, v88, v89\n"
"v_mfma_f32_16x16x32_bf16 v[152:155], v[220:223], a[204:207], v[152:155]\n"
"s_waitcnt lgkmcnt(1)\n"
"ds_read_b128 v[212:215], v231 offset:0\n"
"v_mul_f32_dpp v13, v246, v13 row_newbcast:1 row_mask:0xf bank_mask:0xf\n"
"v_mfma_f32_16x16x32_bf16 a[208:211], v[120:123], a[244:247], a[208:211]\n"
"v_exp_f32 v170, v170\n"
"v_mfma_f32_16x16x32_bf16 v[0:3], v[96:99], a[244:247], v[0:3]\n"
"ds_read_b128 v[216:219], v230 offset:512\n"
"v_cvt_pk_bf16_f32 v124, v164, v165\n"
"v_mfma_f32_16x16x32_bf16 v[0:3], v[100:103], a[248:251], v[0:3]\n"
"ds_read_b64_tr_b16 v[226:227], v237 offset:53248\n"
"v_mul_f32_dpp v15, v246, v15 row_newbcast:3 row_mask:0xf bank_mask:0xf\n"
"v_mul_f32_dpp v41, v247, v41 row_newbcast:1 row_mask:0xf bank_mask:0xf\n"
"v_mul_f32_dpp v42, v247, v42 row_newbcast:2 row_mask:0xf bank_mask:0xf\n"
"v_exp_f32 v166, v166\n"
"v_mul_f32_dpp v57, v247, v57 row_newbcast:1 row_mask:0xf bank_mask:0xf\n"
"v_mul_f32_dpp v91, v206, v91 row_newbcast:3 row_mask:0xf bank_mask:0xf\n"
"v_exp_f32 v172, v172\n"
"v_exp_f32 v177, v177\n"
"v_mfma_f32_16x16x32_bf16 v[32:35], v[104:107], a[244:247], v[32:35]\n"
"ds_read_b64_tr_b16 v[224:225], v237 offset:49152\n"
"ds_read_b128 v[220:223], v231 offset:512\n"
"v_mfma_f32_16x16x32_bf16 v[32:35], v[108:111], a[248:251], v[32:35]\n"
"ds_write_b128 v244, a[232:235] offset:34816\n"
"v_cvt_pk_bf16_f32 v8, v8, v9\n"
"v_mul_f32_dpp v75, v206, v75 row_newbcast:3 row_mask:0xf bank_mask:0xf\n"
"v_mul_f32_dpp v30, v246, v30 row_newbcast:2 row_mask:0xf bank_mask:0xf\n"
"v_mul_f32_dpp v94, v206, v94 row_newbcast:2 row_mask:0xf bank_mask:0xf\n"
"v_exp_f32 v176, v176\n"
"v_mfma_f32_16x16x32_bf16 v[64:67], v[112:115], a[244:247], v[64:67]\n"
"v_exp_f32 v173, v173\n"
"v_mfma_f32_16x16x32_bf16 v[64:67], v[116:119], a[248:251], v[64:67]\n"
"s_waitcnt lgkmcnt(6)\n"
"ds_write_b128 v244, a[236:239] offset:35840\n"
"v_mul_f32_dpp v77, v206, v77 row_newbcast:1 row_mask:0xf bank_mask:0xf\n"
"v_mul_f32_dpp v62, v247, v62 row_newbcast:2 row_mask:0xf bank_mask:0xf\n"
"v_exp_f32 v169, v169\n"
"v_cvt_pk_bf16_f32 v138, v172, v173\n"
"v_exp_f32 v153, v153\n"
"v_mfma_f32_16x16x32_bf16 v[156:159], v[208:211], a[0:3], v[180:183]\n"
"s_waitcnt lgkmcnt(6)\n"
"ds_read_b64_tr_b16 a[254:255], v233 offset:53248\n"
"v_mul_f32_dpp v63, v247, v63 row_newbcast:3 row_mask:0xf bank_mask:0xf\n"
"v_mfma_f32_16x16x32_bf16 v[156:159], v[212:215], a[4:7], v[156:159]\n"
"s_waitcnt lgkmcnt(6)\n"
"v_exp_f32 v174, v174\n"
"v_mfma_f32_16x16x32_bf16 v[156:159], v[216:219], a[8:11], v[156:159]\n"
"s_waitcnt lgkmcnt(3)\n"
"s_add_u32 s40, s56, s53\n"
"v_cvt_pk_bf16_f32 v40, v40, v41\n"
"v_mul_f32_dpp v0, v246, v0 row_newbcast:0 row_mask:0xf bank_mask:0xf\n"
"v_mfma_f32_16x16x32_bf16 v[156:159], v[220:223], a[12:15], v[156:159]\n"
"buffer_load_dwordx4 v240, s[16:19], s40 offen offset:2048 nt lds\n"
"v_cvt_pk_bf16_f32 v9, v10, v11\n"
"v_mul_f32_dpp v44, v247, v44 row_newbcast:0 row_mask:0xf bank_mask:0xf\n"
"v_cvt_pk_bf16_f32 v125, v166, v167\n"
"v_mul_f32_dpp v31, v246, v31 row_newbcast:3 row_mask:0xf bank_mask:0xf\n"
"v_exp_f32 v152, v152\n"
"v_mul_f32_dpp v34, v247, v34 row_newbcast:2 row_mask:0xf bank_mask:0xf\n"
"v_mov_b64 v[200:201], 0\n"
"v_mfma_f32_16x16x32_bf16 v[160:163], v[216:219], a[24:27], v[184:187]\n"
"v_mul_f32_dpp v60, v247, v60 row_newbcast:0 row_mask:0xf bank_mask:0xf\n"
"v_xor_b32 v244, 0x1000, v244\n"
"v_mfma_f32_16x16x32_bf16 v[160:163], v[220:223], a[28:31], v[160:163]\n"
"v_exp_f32 v178, v178\n"
"v_mfma_f32_16x16x32_bf16 v[160:163], v[208:211], a[16:19], v[160:163]\n"
"ds_read_b64_tr_b16 a[250:251], v236 offset:53760\n"
"v_mul_f32_dpp v33, v247, v33 row_newbcast:1 row_mask:0xf bank_mask:0xf\n"
"v_mfma_f32_16x16x32_bf16 v[160:163], v[212:215], a[20:23], v[160:163]\n"
"ds_read_b64_tr_b16 a[244:245], v232 offset:49664\n"
"v_mul_f32_dpp v43, v247, v43 row_newbcast:3 row_mask:0xf bank_mask:0xf\n"
"v_mfma_f32_16x16x32_bf16 v[164:167], v[208:211], a[32:35], v[188:191]\n"
"v_mul_f32_dpp v45, v247, v45 row_newbcast:1 row_mask:0xf bank_mask:0xf\n"
"v_cvt_pk_bf16_f32 v72, v72, v73\n"
"v_mfma_f32_16x16x32_bf16 v[164:167], v[212:215], a[36:39], v[164:167]\n"
"ds_read_b128 v[208:211], v230 offset:4096\n"
"v_mul_f32_dpp v74, v206, v74 row_newbcast:2 row_mask:0xf bank_mask:0xf\n"
"v_mfma_f32_16x16x32_bf16 v[164:167], v[216:219], a[40:43], v[164:167]\n"
"v_mul_f32_dpp v56, v247, v56 row_newbcast:0 row_mask:0xf bank_mask:0xf\n"
"v_mul_f32_dpp v58, v247, v58 row_newbcast:2 row_mask:0xf bank_mask:0xf\n"
"v_mfma_f32_16x16x32_bf16 v[164:167], v[220:223], a[44:47], v[164:167]\n"
"ds_read_b128 v[212:215], v231 offset:4096\n"
"ds_read_b128 v[220:223], v231 offset:4608\n"
"v_mul_f32_dpp v47, v247, v47 row_newbcast:3 row_mask:0xf bank_mask:0xf\n"
"v_cvt_pk_bf16_f32 v73, v74, v75\n"
"v_mul_f32_dpp v29, v246, v29 row_newbcast:1 row_mask:0xf bank_mask:0xf\n"
"v_exp_f32 v175, v175\n"
"v_exp_f32 v179, v179\n"
"v_mul_f32_dpp v1, v246, v1 row_newbcast:1 row_mask:0xf bank_mask:0xf\n"
"v_mul_f32_dpp v2, v246, v2 row_newbcast:2 row_mask:0xf bank_mask:0xf\n"
"v_mfma_f32_16x16x32_bf16 v[68:71], v[116:119], v[224:227], v[68:71]\n"
"s_waitcnt lgkmcnt(5)\n"
"v_exp_f32 v171, v171\n"
"v_mfma_f32_16x16x32_bf16 v[68:71], v[112:115], a[252:255], v[68:71]\n"
"ds_read_b128 v[216:219], v230 offset:4608\n"
"v_mul_f32_dpp v27, v246, v27 row_newbcast:3 row_mask:0xf bank_mask:0xf\n"
"v_mfma_f32_16x16x32_bf16 v[36:39], v[108:111], v[224:227], v[36:39]\n"
"s_add_u32 s40, s56, s54\n"
"v_cvt_pk_bf16_f32 v24, v24, v25\n"
"v_mul_f32_dpp v59, v247, v59 row_newbcast:3 row_mask:0xf bank_mask:0xf\n"
"v_mfma_f32_16x16x32_bf16 v[36:39], v[104:107], a[252:255], v[36:39]\n"
"buffer_load_dwordx4 v207, s[16:19], s40 offen offset:3072 nt lds\n"
"v_cvt_pk_bf16_f32 v74, v76, v77\n"
"v_cvt_pk_bf16_f32 v25, v26, v27\n"
"v_cvt_pk_bf16_f32 v130, v168, v169\n"
"v_exp_f32 v154, v154\n"
"v_exp_f32 v162, v162\n"
"v_mul_f32_dpp v3, v246, v3 row_newbcast:3 row_mask:0xf bank_mask:0xf\n"
"v_mfma_f32_16x16x32_bf16 v[4:7], v[100:103], v[224:227], v[4:7]\n"
"v_cvt_pk_bf16_f32 v10, v12, v13\n"
"v_cvt_pk_bf16_f32 v0, v0, v1\n"
"v_mfma_f32_16x16x32_bf16 v[4:7], v[96:99], a[252:255], v[4:7]\n"
"v_mul_f32_dpp v90, v206, v90 row_newbcast:2 row_mask:0xf bank_mask:0xf\n"
"v_mul_f32_dpp v92, v206, v92 row_newbcast:0 row_mask:0xf bank_mask:0xf\n"
"v_mfma_f32_16x16x32_bf16 a[212:215], v[120:123], a[252:255], a[212:215]\n"
"s_waitcnt lgkmcnt(3)\n"
"ds_read_b64_tr_b16 a[246:247], v232 offset:53760\n"
"ds_read_b64_tr_b16 v[224:225], v237 offset:49664\n"
"v_cvt_pk_bf16_f32 v11, v14, v15\n"
"v_cvt_pk_bf16_f32 v41, v42, v43\n"
"v_cvt_pk_bf16_f32 v43, v46, v47\n"
"v_cvt_pk_bf16_f32 v56, v56, v57\n"
"v_cvt_pk_bf16_f32 v131, v170, v171\n"
"v_cvt_pk_bf16_f32 v146, v176, v177\n"
"v_exp_f32 v167, v167\n"
"v_mfma_f32_16x16x32_bf16 v[168:171], v[208:211], a[0:3], v[180:183]\n"
"s_waitcnt lgkmcnt(3)\n"
"v_exp_f32 v159, v159\n"
"v_mfma_f32_16x16x32_bf16 v[168:171], v[212:215], a[4:7], v[168:171]\n"
"s_waitcnt lgkmcnt(2)\n"
"ds_read_b64_tr_b16 a[248:249], v236 offset:49664\n"
"ds_read_b64_tr_b16 a[254:255], v233 offset:53760\n"
"v_mfma_f32_16x16x32_bf16 v[168:171], v[216:219], a[8:11], v[168:171]\n"
"v_cvt_pk_bf16_f32 v89, v90, v91\n"
"v_cvt_pk_bf16_f32 v139, v174, v175\n"
"v_mfma_f32_16x16x32_bf16 v[168:171], v[220:223], a[12:15], v[168:171]\n"
"ds_write_b128 v248, v[8:11] offset:49152\n"
"v_mul_f32_dpp v79, v206, v79 row_newbcast:3 row_mask:0xf bank_mask:0xf\n"
"v_cvt_pk_bf16_f32 v147, v178, v179\n"
"v_exp_f32 v165, v165\n"
"v_mul_f32_dpp v6, v246, v6 row_newbcast:2 row_mask:0xf bank_mask:0xf\n"
"v_cvt_pk_bf16_f32 v1, v2, v3\n"
"v_mfma_f32_16x16x32_bf16 v[172:175], v[216:219], a[24:27], v[184:187]\n"
"ds_read_b64_tr_b16 v[226:227], v237 offset:53760\n"
"v_mul_f32_dpp v32, v247, v32 row_newbcast:0 row_mask:0xf bank_mask:0xf\n"
"v_mfma_f32_16x16x32_bf16 v[172:175], v[220:223], a[28:31], v[172:175]\n"
"v_mul_f32_dpp v39, v247, v39 row_newbcast:3 row_mask:0xf bank_mask:0xf\n"
"v_mul_f32_dpp v68, v206, v68 row_newbcast:0 row_mask:0xf bank_mask:0xf\n"
"v_mfma_f32_16x16x32_bf16 v[172:175], v[208:211], a[16:19], v[172:175]\n"
"ds_read_b64_tr_b16 a[252:253], v233 offset:49664\n"
"v_cvt_pk_bf16_f32 v75, v78, v79\n"
"v_mfma_f32_16x16x32_bf16 v[172:175], v[212:215], a[20:23], v[172:175]\n"
"s_cbranch_vccnz LPCT0_%=\nLPKT0_%=:\n"
"ds_read_b64_tr_b16 a[54:55], v249 offset:49160\n"
"v_mfma_f32_16x16x32_bf16 v[176:179], v[208:211], a[32:35], v[188:191]\n"
"v_cvt_pk_bf16_f32 v42, v44, v45\n"
"v_mul_f32_dpp v61, v247, v61 row_newbcast:1 row_mask:0xf bank_mask:0xf\n"
"v_mfma_f32_16x16x32_bf16 v[176:179], v[212:215], a[36:39], v[176:179]\n"
"ds_read_b64_tr_b16 a[52:53], v249 offset:49152\n"
"v_cvt_pk_bf16_f32 v57, v58, v59\n"
"v_mfma_f32_16x16x32_bf16 v[176:179], v[216:219], a[40:43], v[176:179]\n"
"v_exp_f32 v155, v155\n"
"v_mfma_f32_16x16x32_bf16 v[176:179], v[220:223], a[44:47], v[176:179]\n"
"s_waitcnt lgkmcnt(4)\n"
"ds_write_b128 v248, v[40:43] offset:50176\n"
"v_cvt_pk_bf16_f32 v27, v30, v31\n"
"v_cvt_pk_bf16_f32 v90, v92, v93\n"
"v_cvt_pk_bf16_f32 v126, v152, v153\n"
"v_mul_f32_dpp v7, v246, v7 row_newbcast:3 row_mask:0xf bank_mask:0xf\n"
"v_mul_f32_dpp v65, v206, v65 row_newbcast:1 row_mask:0xf bank_mask:0xf\n"
"v_mul_f32_dpp v67, v206, v67 row_newbcast:3 row_mask:0xf bank_mask:0xf\n"
"v_mul_f32_dpp v70, v206, v70 row_newbcast:2 row_mask:0xf bank_mask:0xf\n"
"v_mul_f32_dpp v71, v206, v71 row_newbcast:3 row_mask:0xf bank_mask:0xf\n"
"v_mfma_f32_16x16x32_bf16 a[224:227], v[120:123], a[244:247], a[224:227]\n"
"ds_write_b128 v248, v[72:75] offset:51200\n"
"v_cvt_pk_bf16_f32 v26, v28, v29\n"
"v_exp_f32 v166, v166\n"
"v_cvt_pk_bf16_f32 v3, v6, v7\n"
"v_mul_f32_dpp v66, v206, v66 row_newbcast:2 row_mask:0xf bank_mask:0xf\n"
"v_mfma_f32_16x16x32_bf16 v[16:19], v[96:99], a[244:247], v[16:19]\n"
"s_waitcnt vmcnt(8) lgkmcnt(4)\n"
"ds_read_b64_tr_b16 a[68:69], v249 offset:50176\n"
"v_mul_f32_dpp v69, v206, v69 row_newbcast:1 row_mask:0xf bank_mask:0xf\n"
"v_mfma_f32_16x16x32_bf16 v[16:19], v[100:103], a[248:251], v[16:19]\n"
"s_barrier\n"
"ds_read_b64_tr_b16 a[70:71], v249 offset:50184\n"
"ds_read_b128 v[212:215], v229 offset:16384\n"
"ds_read_b64 v[250:251], v245 offset:52224\n"
"v_mul_f32_dpp v5, v246, v5 row_newbcast:1 row_mask:0xf bank_mask:0xf\n"
"v_mul_f32_dpp v35, v247, v35 row_newbcast:3 row_mask:0xf bank_mask:0xf\n"
"v_exp_f32 v173, v173\n"
"v_exp_f32 v175, v175\n"
"v_mfma_f32_16x16x32_bf16 v[48:51], v[104:107], a[244:247], v[48:51]\n"
"v_cvt_pk_bf16_f32 v59, v62, v63\n"
"v_mul_f32_dpp v38, v247, v38 row_newbcast:2 row_mask:0xf bank_mask:0xf\n"
"v_mfma_f32_16x16x32_bf16 v[48:51], v[108:111], a[248:251], v[48:51]\n"
"ds_read_b128 v[208:211], v228 offset:16384\n"
"ds_read_b128 v[220:223], v229 offset:16896\n"
"v_mul_f32_dpp v95, v206, v95 row_newbcast:3 row_mask:0xf bank_mask:0xf\n"
"v_exp_f32 v161, v161\n"
"v_cvt_pk_bf16_f32 v149, v166, v167\n"
"v_cvt_pk_bf16_f32 v32, v32, v33\n"
"v_exp_f32 v170, v170\n"
"v_exp_f32 v172, v172\n"
"v_mfma_f32_16x16x32_bf16 v[80:83], v[112:115], a[244:247], v[80:83]\n"
"ds_read_b64_tr_b16 a[84:85], v249 offset:51200\n"
"ds_read_b64_tr_b16 a[86:87], v249 offset:51208\n"
"v_mfma_f32_16x16x32_bf16 v[80:83], v[116:119], a[248:251], v[80:83]\n"
"ds_read_b128 v[28:31], v244 offset:33792\n"
"v_mov_b64 v[198:199], 0\n"
"v_mfma_f32_16x16x32_bf16 v[84:87], v[116:119], v[224:227], v[84:87]\n"
"ds_write_b128 v248, v[24:27] offset:49152\n"
"v_cvt_pk_bf16_f32 v91, v94, v95\n"
"v_exp_f32 v156, v156\n"
"v_exp_f32 v157, v157\n"
"v_mul_f32_dpp v37, v247, v37 row_newbcast:1 row_mask:0xf bank_mask:0xf\n"
"v_mfma_f32_16x16x32_bf16 v[84:87], v[112:115], a[252:255], v[84:87]\n"
"ds_read_b64_tr_b16 a[60:61], v249 offset:49152\n"
"ds_read_b128 v[216:219], v228 offset:16896\n"
"v_cvt_pk_bf16_f32 v127, v154, v155\n"
"v_exp_f32 v160, v160\n"
"v_exp_f32 v164, v164\n"
"v_mul_f32_dpp v36, v247, v36 row_newbcast:0 row_mask:0xf bank_mask:0xf\n"
"v_exp_f32 v176, v176\n"
"v_mfma_f32_16x16x32_bf16 v[52:55], v[108:111], v[224:227], v[52:55]\n"
"ds_write_b128 v248, v[88:91] offset:51200\n"
"v_cvt_pk_bf16_f32 v58, v60, v61\n"
"v_exp_f32 v163, v163\n"
"v_cvt_pk_bf16_f32 v33, v34, v35\n"
"v_exp_f32 v169, v169\n"
"v_mfma_f32_16x16x32_bf16 v[52:55], v[104:107], a[252:255], v[52:55]\n"
"s_waitcnt lgkmcnt(9)\n"
"ds_write_b128 v248, v[56:59] offset:50176\n"
"v_mul_f32_dpp v4, v246, v4 row_newbcast:0 row_mask:0xf bank_mask:0xf\n"
"v_exp_f32 v168, v168\n"
"v_min_f32 v254, v252, v250\n"
"v_sub_f32 v252, v254, v252\n"
"v_mfma_f32_16x16x32_bf16 v[20:23], v[100:103], v[224:227], v[20:23]\n"
"ds_read_b64_tr_b16 a[92:93], v249 offset:51200\n"
"ds_read_b64_tr_b16 a[244:245], v234 offset:0\n"
"v_mfma_f32_16x16x32_bf16 v[20:23], v[96:99], a[252:255], v[20:23]\n"
"ds_read_b64_tr_b16 a[62:63], v249 offset:49160\n"
"ds_read_b64_tr_b16 a[78:79], v249 offset:50184\n"
"ds_read_b64_tr_b16 a[94:95], v249 offset:51208\n"
"v_cvt_pk_bf16_f32 v2, v4, v5\n"
"v_mul_f32_dpp v64, v206, v64 row_newbcast:0 row_mask:0xf bank_mask:0xf\n"
"v_cvt_pk_bf16_f32 v64, v64, v65\n"
"v_cvt_pk_bf16_f32 v65, v66, v67\n"
"v_cvt_pk_bf16_f32 v66, v68, v69\n"
"v_cvt_pk_bf16_f32 v67, v70, v71\n"
"v_exp_f32 v177, v177\n"
"v_mfma_f32_16x16x32_bf16 a[228:231], v[120:123], a[252:255], a[228:231]\n"
"s_waitcnt lgkmcnt(11)\n"
"ds_write_b128 v248, v[64:67] offset:51200\n"
"v_cvt_pk_bf16_f32 v132, v156, v157\n"
"v_cvt_pk_bf16_f32 v34, v36, v37\n"
"v_cvt_pk_bf16_f32 v35, v38, v39\n"
"v_exp_f32 v171, v171\n"
"v_exp_f32 v179, v179\n"
"v_sub_f32 v250, v254, v250\n"
"v_mul_f32_dpp v83, v206, v83 row_newbcast:3 row_mask:0xf bank_mask:0xf\n"
"v_mfma_f32_16x16x32_bf16 v[152:155], v[208:211], a[0:3], v[180:183]\n"
"ds_read_b64_tr_b16 a[76:77], v249 offset:50176\n"
"ds_read_b64_tr_b16 a[80:81], v249 offset:51200\n"
"v_mfma_f32_16x16x32_bf16 v[152:155], v[212:215], a[4:7], v[152:155]\n"
"s_waitcnt lgkmcnt(10)\n"
"ds_write_b128 v248, v[0:3] offset:49152\n"
"v_exp_f32 v158, v158\n"
"v_cvt_pk_bf16_f32 v140, v160, v161\n"
"v_cvt_pk_bf16_f32 v142, v172, v173\n"
"v_mul_f32_dpp v18, v246, v18 row_newbcast:2 row_mask:0xf bank_mask:0xf\n"
"v_mfma_f32_16x16x32_bf16 v[152:155], v[216:219], a[8:11], v[152:155]\n"
"ds_write_b128 v248, v[32:35] offset:50176\n"
"v_cvt_pk_bf16_f32 v141, v162, v163\n"
"v_exp_f32 v178, v178\n"
"v_exp_f32 v250, v250\n"
"v_mfma_f32_16x16x32_bf16 v[152:155], v[220:223], a[12:15], v[152:155]\n"
"ds_read_b64_tr_b16 a[50:51], v249 offset:49160\n"
"ds_read_b64_tr_b16 a[64:65], v249 offset:50176\n"
"ds_read_b128 v[24:27], v244 offset:32768\n"
"v_cvt_pk_bf16_f32 v133, v158, v159\n"
"v_cvt_pk_bf16_f32 v148, v164, v165\n"
"v_cvt_pk_bf16_f32 v150, v176, v177\n"
"v_exp_f32 v252, v252\n"
"v_mul_f32_dpp v19, v246, v19 row_newbcast:3 row_mask:0xf bank_mask:0xf\n"
"v_mul_f32_dpp v22, v246, v22 row_newbcast:2 row_mask:0xf bank_mask:0xf\n"
"v_mfma_f32_16x16x32_bf16 v[156:159], v[216:219], a[24:27], v[184:187]\n"
"ds_read_b64_tr_b16 a[66:67], v249 offset:50184\n"
"ds_read_b64_tr_b16 a[248:249], v238 offset:0\n"
"v_mfma_f32_16x16x32_bf16 v[156:159], v[220:223], a[28:31], v[156:159]\n"
"ds_read_b64_tr_b16 a[48:49], v249 offset:49152\n"
"ds_read_b128 v[60:63], v244 offset:35840\n"
"v_mfma_f32_16x16x32_bf16 v[156:159], v[208:211], a[16:19], v[156:159]\n"
"ds_read_b64_tr_b16 a[82:83], v249 offset:51208\n"
"ds_read_b128 v[56:59], v244 offset:34816\n"
"v_mfma_f32_16x16x32_bf16 v[156:159], v[212:215], a[20:23], v[156:159]\n"
"ds_read_b64_tr_b16 a[246:247], v234 offset:4096\n"
"ds_read_b64_tr_b16 v[226:227], v239 offset:4096\n"
"v_mfma_f32_16x16x32_bf16 v[160:163], v[208:211], a[32:35], v[188:191]\n"
"v_exp_f32 v174, v174\n"
"v_mfma_f32_16x16x32_bf16 v[160:163], v[212:215], a[36:39], v[160:163]\n"
"ds_read_b64_tr_b16 a[250:251], v238 offset:4096\n"
"v_mul_f32_dpp v49, v247, v49 row_newbcast:1 row_mask:0xf bank_mask:0xf\n"
"v_mfma_f32_16x16x32_bf16 v[160:163], v[216:219], a[40:43], v[160:163]\n"
"ds_read_b64_tr_b16 v[224:225], v239 offset:0\n"
"v_mov_b64 v[202:203], 0\n"
"v_mfma_f32_16x16x32_bf16 v[160:163], v[220:223], a[44:47], v[160:163]\n"
"v_exp_f32 v154, v154\n"
"v_mfma_f32_16x16x32_bf16 v[164:167], v[216:219], a[200:203], v[192:195]\n"
"v_cvt_pk_bf16_f32 v151, v178, v179\n"
"v_mul_f32 v253, v253, v252\n"
"v_mfma_f32_16x16x32_bf16 v[164:167], v[220:223], a[204:207], v[164:167]\n"
"v_cvt_pk_bf16_f32 v134, v168, v169\n"
"v_fmac_f32 v253, v251, v250\n"
"v_mfma_f32_16x16x32_bf16 v[164:167], v[208:211], a[192:195], v[164:167]\n"
"ds_read_b64_tr_b16 a[252:253], v235 offset:0\n"
"ds_read_b64_tr_b16 a[254:255], v235 offset:4096\n"
"v_mfma_f32_16x16x32_bf16 v[164:167], v[212:215], a[196:199], v[164:167]\n"
"s_waitcnt lgkmcnt(4)\n"
"v_cvt_pk_bf16_f32 v135, v170, v171\n"
"v_mfma_f32_16x16x32_bf16 a[216:219], v[124:127], a[244:247], 0\n"
"ds_read_b128 v[216:219], v228 offset:20992\n"
"v_cvt_pk_bf16_f32 v143, v174, v175\n"
"v_mfma_f32_16x16x32_bf16 v[8:11], v[128:131], a[244:247], 0\n"
"s_waitcnt lgkmcnt(4)\n"
"s_add_u32 m0, s44, 98304\n"
"v_rcp_f32 v253, v253\n"
"v_mfma_f32_16x16x32_bf16 v[8:11], v[132:135], a[248:251], v[8:11]\n"
"buffer_load_dwordx4 v240, s[20:23], s56 offen nt lds\n"
"v_exp_f32 v152, v152\n"
"v_exp_f32 v155, v155\n"
"v_exp_f32 v158, v158\n"
"v_exp_f32 v167, v167\n"
"v_mfma_f32_16x16x32_bf16 v[40:43], v[136:139], a[244:247], 0\n"
"v_accvgpr_read_b32 v254, a208\n"
"v_mul_f32_dpp v85, v206, v85 row_newbcast:1 row_mask:0xf bank_mask:0xf\n"
"v_mfma_f32_16x16x32_bf16 v[40:43], v[140:143], a[248:251], v[40:43]\n"
"v_mul_f32 v250, v250, v253\n"
"v_accvgpr_read_b32 v251, a211\n"
"v_mfma_f32_16x16x32_bf16 v[72:75], v[144:147], a[244:247], 0\n"
"ds_read_b128 v[220:223], v229 offset:20992\n"
"v_mul_f32_dpp v17, v246, v17 row_newbcast:1 row_mask:0xf bank_mask:0xf\n"
"v_mfma_f32_16x16x32_bf16 v[72:75], v[148:151], a[248:251], v[72:75]\n"
"s_waitcnt lgkmcnt(2)\n"
"v_exp_f32 v164, v164\n"
"v_mfma_f32_16x16x32_bf16 v[76:79], v[148:151], v[224:227], 0\n"
"v_exp_f32 v153, v153\n"
"v_mfma_f32_16x16x32_bf16 v[76:79], v[144:147], a[252:255], v[76:79]\n"
"v_mul_f32 v252, v252, v253\n"
"v_mov_b32_dpp v250, v250 row_ror:12 row_mask:0x2 bank_mask:0xf\n"
"v_mfma_f32_16x16x32_bf16 v[44:47], v[140:143], v[224:227], 0\n"
"ds_read_b64_tr_b16 a[244:245], v234 offset:512\n"
"v_mov_b32_dpp v252, v252 row_ror:12 row_mask:0x2 bank_mask:0xf\n"
"v_mfma_f32_16x16x32_bf16 v[44:47], v[136:139], a[252:255], v[44:47]\n"
"ds_read_b128 v[212:215], v229 offset:20480\n"
"v_mov_b32_dpp v250, v250 row_ror:8 row_mask:0x4 bank_mask:0xf\n"
"v_mfma_f32_16x16x32_bf16 v[12:15], v[132:135], v[224:227], 0\n"
"s_add_u32 s40, s56, s52\n"
"ds_read_b128 v[208:211], v228 offset:20480\n"
"v_cvt_pk_bf16_f32 v96, v152, v153\n"
"v_mfma_f32_16x16x32_bf16 v[12:15], v[128:131], a[252:255], v[12:15]\n"
"buffer_load_dwordx4 v207, s[20:23], s40 offen offset:1024 nt lds\n"
"v_mov_b32_dpp v252, v252 row_ror:8 row_mask:0x4 bank_mask:0xf\n"
"v_mov_b32_dpp v250, v250 row_ror:4 row_mask:0x8 bank_mask:0xf\n"
"v_accvgpr_read_b32 v255, a209\n"
"v_accvgpr_read_b32 v253, a210\n"
"v_mov_b64 v[196:197], 0\n"
"v_cvt_pk_bf16_f32 v97, v154, v155\n"
"v_exp_f32 v157, v157\n"
"v_mfma_f32_16x16x32_bf16 a[220:223], v[124:127], a[252:255], 0\n"
"v_mov_b32_dpp v252, v252 row_ror:4 row_mask:0x8 bank_mask:0xf\n"
"v_mul_f32_dpp v24, v250, v24 row_newbcast:0 row_mask:0xf bank_mask:0xf\n"
"v_mul_f32_dpp v25, v250, v25 row_newbcast:1 row_mask:0xf bank_mask:0xf\n"
"v_mul_f32_dpp v26, v250, v26 row_newbcast:2 row_mask:0xf bank_mask:0xf\n"
"v_mul_f32_dpp v29, v250, v29 row_newbcast:1 row_mask:0xf bank_mask:0xf\n"
"v_mul_f32_dpp v54, v247, v54 row_newbcast:2 row_mask:0xf bank_mask:0xf\n"
"v_exp_f32 v162, v162\n"
"v_exp_f32 v163, v163\n"
"v_smfmac_f32_16x16x64_bf16 v[196:199], a[240:243], v[128:135], v241\n"
"v_mul_f32_dpp v27, v250, v27 row_newbcast:3 row_mask:0xf bank_mask:0xf\n"
"v_fmac_f32_dpp v24, v252, v254 row_newbcast:0 row_mask:0xf bank_mask:0xf\n"
"v_mfma_f32_16x16x32_bf16 v[202:205], a[240:243], v[124:127], v[202:205]\n"
"s_waitcnt lgkmcnt(3)\n"
"ds_read_b64_tr_b16 a[252:253], v235 offset:512\n"
"ds_read_b64_tr_b16 a[254:255], v235 offset:4608\n"
"ds_read_b64_tr_b16 v[224:225], v239 offset:512\n"
"v_fmac_f32_dpp v25, v252, v255 row_newbcast:1 row_mask:0xf bank_mask:0xf\n"
"v_fmac_f32_dpp v26, v252, v253 row_newbcast:2 row_mask:0xf bank_mask:0xf\n"
"v_mul_f32_dpp v28, v250, v28 row_newbcast:0 row_mask:0xf bank_mask:0xf\n"
"v_mul_f32_dpp v30, v250, v30 row_newbcast:2 row_mask:0xf bank_mask:0xf\n"
"v_mul_f32_dpp v50, v247, v50 row_newbcast:2 row_mask:0xf bank_mask:0xf\n"
"v_mul_f32_dpp v58, v250, v58 row_newbcast:2 row_mask:0xf bank_mask:0xf\n"
"v_exp_f32 v156, v156\n"
"v_mfma_f32_16x16x32_bf16 v[168:171], v[216:219], a[8:11], v[180:183]\n"
"v_accvgpr_read_b32 v254, a212\n"
"v_accvgpr_read_b32 v253, a214\n"
"v_mfma_f32_16x16x32_bf16 v[168:171], v[220:223], a[12:15], v[168:171]\n"
"s_waitcnt lgkmcnt(3)\n"
"v_mul_f32_dpp v48, v247, v48 row_newbcast:0 row_mask:0xf bank_mask:0xf\n"
"v_mul_f32_dpp v57, v250, v57 row_newbcast:1 row_mask:0xf bank_mask:0xf\n"
"v_mfma_f32_16x16x32_bf16 v[168:171], v[208:211], a[0:3], v[168:171]\n"
"ds_read_b64_tr_b16 a[250:251], v238 offset:4608\n"
"v_mul_f32_dpp v56, v250, v56 row_newbcast:0 row_mask:0xf bank_mask:0xf\n"
"v_mfma_f32_16x16x32_bf16 v[168:171], v[212:215], a[4:7], v[168:171]\n"
"ds_read_b64_tr_b16 a[248:249], v238 offset:512\n"
"ds_read_b64_tr_b16 v[226:227], v239 offset:4608\n"
"v_fmac_f32_dpp v27, v252, v251 row_newbcast:3 row_mask:0xf bank_mask:0xf\n"
"v_mul_f32_dpp v31, v250, v31 row_newbcast:3 row_mask:0xf bank_mask:0xf\n"
"v_fmac_f32_dpp v28, v252, v254 row_newbcast:0 row_mask:0xf bank_mask:0xf\n"
"v_fmac_f32_dpp v30, v252, v253 row_newbcast:2 row_mask:0xf bank_mask:0xf\n"
"v_cvt_pk_bf16_f32 v24, v24, v25\n"
"v_cvt_pk_bf16_f32 v25, v26, v27\n"
"v_mul_f32_dpp v52, v247, v52 row_newbcast:0 row_mask:0xf bank_mask:0xf\n"
"v_accvgpr_read_b32 v254, a224\n"
"v_mul_f32_dpp v59, v250, v59 row_newbcast:3 row_mask:0xf bank_mask:0xf\n"
"v_mul_f32_dpp v60, v250, v60 row_newbcast:0 row_mask:0xf bank_mask:0xf\n"
"v_mul_f32_dpp v62, v250, v62 row_newbcast:2 row_mask:0xf bank_mask:0xf\n"
"v_mul_f32_dpp v63, v250, v63 row_newbcast:3 row_mask:0xf bank_mask:0xf\n"
"v_exp_f32 v159, v159\n"
"v_exp_f32 v161, v161\n"
"v_mfma_f32_16x16x32_bf16 v[172:175], v[208:211], a[16:19], v[184:187]\n"
"v_accvgpr_read_b32 v255, a213\n"
"v_accvgpr_read_b32 v251, a215\n"
"v_mfma_f32_16x16x32_bf16 v[172:175], v[212:215], a[20:23], v[172:175]\n"
"ds_read_b64_tr_b16 a[246:247], v234 offset:4608\n"
"v_fmac_f32_dpp v29, v252, v255 row_newbcast:1 row_mask:0xf bank_mask:0xf\n"
"v_mfma_f32_16x16x32_bf16 v[172:175], v[216:219], a[24:27], v[172:175]\n"
"s_add_u32 s40, s56, s53\n"
"v_xor_b32 v249, s75, v249\n"
"v_accvgpr_read_b32 v255, a225\n"
"v_mfma_f32_16x16x32_bf16 v[172:175], v[220:223], a[28:31], v[172:175]\n"
"buffer_load_dwordx4 v240, s[20:23], s40 offen offset:2048 nt lds\n"
"v_fmac_f32_dpp v31, v252, v251 row_newbcast:3 row_mask:0xf bank_mask:0xf\n"
"v_cvt_pk_bf16_f32 v27, v30, v31\n"
"v_mul_f32_dpp v16, v246, v16 row_newbcast:0 row_mask:0xf bank_mask:0xf\n"
"v_mul_f32_dpp v51, v247, v51 row_newbcast:3 row_mask:0xf bank_mask:0xf\n"
"v_accvgpr_read_b32 v253, a226\n"
"v_accvgpr_read_b32 v251, a227\n"
"v_fmac_f32_dpp v56, v252, v254 row_newbcast:0 row_mask:0xf bank_mask:0xf\n"
"v_accvgpr_read_b32 v254, a228\n"
"v_mfma_f32_16x16x32_bf16 v[176:179], v[216:219], a[40:43], v[188:191]\n"
"v_cvt_pk_bf16_f32 v26, v28, v29\n"
"v_fmac_f32_dpp v57, v252, v255 row_newbcast:1 row_mask:0xf bank_mask:0xf\n"
"v_mfma_f32_16x16x32_bf16 v[176:179], v[220:223], a[44:47], v[176:179]\n"
"v_fmac_f32_dpp v58, v252, v253 row_newbcast:2 row_mask:0xf bank_mask:0xf\n"
"v_accvgpr_read_b32 v255, a229\n"
"v_mfma_f32_16x16x32_bf16 v[176:179], v[208:211], a[32:35], v[176:179]\n"
"v_cvt_pk_bf16_f32 v16, v16, v17\n"
"v_mul_f32_dpp v61, v250, v61 row_newbcast:1 row_mask:0xf bank_mask:0xf\n"
"v_mfma_f32_16x16x32_bf16 v[176:179], v[212:215], a[36:39], v[176:179]\n"
"ds_write_b128 v243, v[24:27] offset:0\n"
"v_cvt_pk_bf16_f32 v48, v48, v49\n"
"v_fmac_f32_dpp v59, v252, v251 row_newbcast:3 row_mask:0xf bank_mask:0xf\n"
"v_accvgpr_read_b32 v253, a230\n"
"v_accvgpr_read_b32 v251, a231\n"
"v_fmac_f32_dpp v60, v252, v254 row_newbcast:0 row_mask:0xf bank_mask:0xf\n"
"v_fmac_f32_dpp v61, v252, v255 row_newbcast:1 row_mask:0xf bank_mask:0xf\n"
"v_cvt_pk_bf16_f32 v56, v56, v57\n"
"v_cvt_pk_bf16_f32 v57, v58, v59\n"
"v_cvt_pk_bf16_f32 v58, v60, v61\n"
"v_mfma_f32_16x16x32_bf16 v[152:155], v[208:211], a[192:195], v[192:195]\n"
"v_mul_f32_dpp v81, v206, v81 row_newbcast:1 row_mask:0xf bank_mask:0xf\n"
"v_mul_f32_dpp v82, v206, v82 row_newbcast:2 row_mask:0xf bank_mask:0xf\n"
"v_mul_f32_dpp v87, v206, v87 row_newbcast:3 row_mask:0xf bank_mask:0xf\n"
"v_fmac_f32_dpp v62, v252, v253 row_newbcast:2 row_mask:0xf bank_mask:0xf\n"
"v_fmac_f32_dpp v63, v252, v251 row_newbcast:3 row_mask:0xf bank_mask:0xf\n"
"v_cvt_pk_bf16_f32 v59, v62, v63\n"
"ds_write_b128 v243, v[56:59] offset:2048\n"
"v_mfma_f32_16x16x32_bf16 v[152:155], v[212:215], a[196:199], v[152:155]\n"
"v_exp_f32 v174, v174\n"
"v_mfma_f32_16x16x32_bf16 v[152:155], v[216:219], a[200:203], v[152:155]\n"
"v_exp_f32 v166, v166\n"
"v_mfma_f32_16x16x32_bf16 v[152:155], v[220:223], a[204:207], v[152:155]\n"
"s_waitcnt lgkmcnt(2)\n"
"ds_read_b128 v[212:215], v231 offset:16384\n"
"v_cvt_pk_bf16_f32 v105, v158, v159\n"
"v_mfma_f32_16x16x32_bf16 a[232:235], v[124:127], a[244:247], 0\n"
"v_mul_f32_dpp v20, v246, v20 row_newbcast:0 row_mask:0xf bank_mask:0xf\n"
"v_mul_f32_dpp v23, v246, v23 row_newbcast:3 row_mask:0xf bank_mask:0xf\n"
"v_mfma_f32_16x16x32_bf16 v[24:27], v[128:131], a[244:247], 0\n"
"s_add_u32 s40, s56, s54\n"
"ds_read_b128 v[220:223], v231 offset:16896\n"
"v_mul_f32_dpp v21, v246, v21 row_newbcast:1 row_mask:0xf bank_mask:0xf\n"
"v_mfma_f32_16x16x32_bf16 v[24:27], v[132:135], a[248:251], v[24:27]\n"
"buffer_load_dwordx4 v207, s[20:23], s40 offen offset:3072 nt lds\n"
"v_mfma_f32_16x16x32_bf16 v[56:59], v[136:139], a[244:247], 0\n"
"s_add_u32 s55, s55, s59\n"
"v_cvt_pk_bf16_f32 v17, v18, v19\n"
"v_cvt_pk_bf16_f32 v104, v156, v157\n"
"v_mfma_f32_16x16x32_bf16 v[56:59], v[140:143], a[248:251], v[56:59]\n"
"s_or_b32 s55, s55, s58\n"
"v_exp_f32 v178, v178\n"
"v_mfma_f32_16x16x32_bf16 v[88:91], v[144:147], a[244:247], 0\n"
"s_xor_b32 s56, s55, s57\n"
"ds_read_b128 v[208:211], v230 offset:16384\n"
"v_cvt_pk_bf16_f32 v49, v50, v51\n"
"v_mfma_f32_16x16x32_bf16 v[88:91], v[148:151], a[248:251], v[88:91]\n"
"ds_read_b128 v[216:219], v230 offset:16896\n"
"v_cvt_pk_bf16_f32 v18, v20, v21\n"
"v_mfma_f32_16x16x32_bf16 v[92:95], v[148:151], v[224:227], 0\n"
"v_exp_f32 v160, v160\n"
"v_mfma_f32_16x16x32_bf16 v[92:95], v[144:147], a[252:255], v[92:95]\n"
"v_exp_f32 v165, v165\n"
"v_mfma_f32_16x16x32_bf16 v[60:63], v[140:143], v[224:227], 0\n"
"s_add_u32 m0, s44, 49152\n"
"ds_read_b64_tr_b16 a[248:249], v236 offset:0\n"
"v_mul_f32_dpp v80, v206, v80 row_newbcast:0 row_mask:0xf bank_mask:0xf\n"
"v_mfma_f32_16x16x32_bf16 v[60:63], v[136:139], a[252:255], v[60:63]\n"
"buffer_load_dwordx4 v240, s[16:19], s56 offen nt lds\n"
"v_mfma_f32_16x16x32_bf16 v[28:31], v[132:135], v[224:227], 0\n"
"v_exp_f32 v177, v177\n"
"v_mfma_f32_16x16x32_bf16 v[28:31], v[128:131], a[252:255], v[28:31]\n"
"v_exp_f32 v169, v169\n"
"v_mfma_f32_16x16x32_bf16 a[236:239], v[124:127], a[252:255], 0\n"
"ds_read_b64_tr_b16 a[250:251], v236 offset:4096\n"
"ds_read_b64_tr_b16 v[224:225], v237 offset:0\n"
"v_smfmac_f32_16x16x64_bf16 v[198:201], a[240:243], v[136:143], v241\n"
"s_waitcnt lgkmcnt(3)\n"
"ds_read_b64_tr_b16 a[246:247], v232 offset:4096\n"
"v_cvt_pk_bf16_f32 v112, v160, v161\n"
"v_mfma_f32_16x16x32_bf16 v[156:159], v[208:211], a[0:3], v[180:183]\n"
"v_cvt_pk_bf16_f32 v80, v80, v81\n"
"v_cvt_pk_bf16_f32 v113, v162, v163\n"
"v_mfma_f32_16x16x32_bf16 v[156:159], v[212:215], a[4:7], v[156:159]\n"
"ds_read_b64_tr_b16 a[254:255], v233 offset:4096\n"
"v_xor_b32 v248, s75, v248\n"
"v_mfma_f32_16x16x32_bf16 v[156:159], v[216:219], a[8:11], v[156:159]\n"
"ds_read_b64_tr_b16 a[244:245], v232 offset:0\n"
"v_cvt_pk_bf16_f32 v120, v164, v165\n"
"v_mfma_f32_16x16x32_bf16 v[156:159], v[220:223], a[12:15], v[156:159]\n"
"v_exp_f32 v170, v170\n"
"v_mfma_f32_16x16x32_bf16 v[160:163], v[216:219], a[24:27], v[184:187]\n"
"v_exp_f32 v179, v179\n"
"v_mfma_f32_16x16x32_bf16 v[160:163], v[220:223], a[28:31], v[160:163]\n"
"ds_read_b64_tr_b16 v[226:227], v237 offset:4096\n"
"v_cvt_pk_bf16_f32 v121, v166, v167\n"
"v_mfma_f32_16x16x32_bf16 v[160:163], v[208:211], a[16:19], v[160:163]\n"
"s_add_u32 s40, s56, s52\n"
"v_exp_f32 v168, v168\n"
"v_mfma_f32_16x16x32_bf16 v[160:163], v[212:215], a[20:23], v[160:163]\n"
"buffer_load_dwordx4 v207, s[16:19], s40 offen offset:1024 nt lds\n"
"v_mfma_f32_16x16x32_bf16 v[164:167], v[208:211], a[32:35], v[188:191]\n"
"v_exp_f32 v176, v176\n"
"v_mfma_f32_16x16x32_bf16 v[164:167], v[212:215], a[36:39], v[164:167]\n"
"v_cvt_pk_bf16_f32 v19, v22, v23\n"
"v_mul_f32_dpp v53, v247, v53 row_newbcast:1 row_mask:0xf bank_mask:0xf\n"
"v_mfma_f32_16x16x32_bf16 v[164:167], v[216:219], a[40:43], v[164:167]\n"
"ds_read_b64_tr_b16 a[252:253], v233 offset:0\n"
"ds_read_b128 v[208:211], v230 offset:20480\n"
"v_mfma_f32_16x16x32_bf16 v[164:167], v[220:223], a[44:47], v[164:167]\n"
"s_waitcnt lgkmcnt(3)\n"
"v_exp_f32 v158, v158\n"
"v_mfma_f32_16x16x32_bf16 a[208:211], v[124:127], a[244:247], 0\n"
"ds_read_b128 v[220:223], v231 offset:20992\n"
"v_cvt_pk_bf16_f32 v81, v82, v83\n"
"v_mfma_f32_16x16x32_bf16 v[0:3], v[128:131], a[244:247], 0\n"
"ds_read_b128 v[212:215], v231 offset:20480\n"
"v_mul_f32_dpp v55, v247, v55 row_newbcast:3 row_mask:0xf bank_mask:0xf\n"
"v_mfma_f32_16x16x32_bf16 v[0:3], v[132:135], a[248:251], v[0:3]\n"
"v_exp_f32 v175, v175\n"
"v_mfma_f32_16x16x32_bf16 v[32:35], v[136:139], a[244:247], 0\n"
"s_add_u32 s40, s56, s53\n"
"ds_read_b128 v[216:219], v230 offset:20992\n"
"v_mul_f32_dpp v84, v206, v84 row_newbcast:0 row_mask:0xf bank_mask:0xf\n"
"v_mfma_f32_16x16x32_bf16 v[32:35], v[140:143], a[248:251], v[32:35]\n"
"buffer_load_dwordx4 v240, s[16:19], s40 offen offset:2048 nt lds\n"
"v_mfma_f32_16x16x32_bf16 v[64:67], v[144:147], a[244:247], 0\n"
"v_cvt_pk_bf16_f32 v50, v52, v53\n"
"v_mul_f32_dpp v86, v206, v86 row_newbcast:2 row_mask:0xf bank_mask:0xf\n"
"v_mfma_f32_16x16x32_bf16 v[64:67], v[148:151], a[248:251], v[64:67]\n"
"s_waitcnt lgkmcnt(3)\n"
"ds_write_b128 v248, v[16:19] offset:49152\n"
"v_cvt_pk_bf16_f32 v82, v84, v85\n"
"v_exp_f32 v172, v172\n"
"v_exp_f32 v152, v152\n"
"v_exp_f32 v159, v159\n"
"v_exp_f32 v161, v161\n"
"v_exp_f32 v164, v164\n"
"v_exp_f32 v167, v167\n"
"v_mfma_f32_16x16x32_bf16 v[68:71], v[148:151], v[224:227], 0\n"
"v_cvt_pk_bf16_f32 v51, v54, v55\n"
"v_cvt_pk_bf16_f32 v98, v168, v169\n"
"v_mfma_f32_16x16x32_bf16 v[68:71], v[144:147], a[252:255], v[68:71]\n"
"ds_write_b128 v248, v[48:51] offset:50176\n"
"v_exp_f32 v171, v171\n"
"v_exp_f32 v173, v173\n"
"v_exp_f32 v156, v156\n"
"v_exp_f32 v163, v163\n"
"v_mfma_f32_16x16x32_bf16 v[36:39], v[140:143], v[224:227], 0\n"
"ds_read_b64_tr_b16 a[246:247], v232 offset:4608\n"
"ds_read_b64_tr_b16 a[250:251], v236 offset:4608\n"
"v_mfma_f32_16x16x32_bf16 v[36:39], v[136:139], a[252:255], v[36:39]\n"
"ds_read_b64_tr_b16 a[74:75], v249 offset:50184\n"
"v_cvt_pk_bf16_f32 v107, v174, v175\n"
"v_mfma_f32_16x16x32_bf16 v[4:7], v[132:135], v[224:227], 0\n"
"ds_read_b64_tr_b16 a[56:57], v249 offset:49152\n"
"v_cvt_pk_bf16_f32 v83, v86, v87\n"
"v_mfma_f32_16x16x32_bf16 v[4:7], v[128:131], a[252:255], v[4:7]\n"
"ds_write_b128 v248, v[80:83] offset:51200\n"
"v_cvt_pk_bf16_f32 v99, v170, v171\n"
"v_exp_f32 v155, v155\n"
"v_exp_f32 v157, v157\n"
"v_mfma_f32_16x16x32_bf16 a[212:215], v[124:127], a[252:255], 0\n"
"s_add_u32 s40, s56, s54\n"
"ds_read_b64_tr_b16 a[58:59], v249 offset:49160\n"
"ds_read_b64_tr_b16 v[224:225], v237 offset:512\n"
"v_smfmac_f32_16x16x64_bf16 v[200:203], a[240:243], v[144:151], v241\n"
"buffer_load_dwordx4 v207, s[16:19], s40 offen offset:3072 nt lds\n"
"v_mfma_f32_16x16x32_bf16 v[168:171], v[208:211], a[0:3], v[180:183]\n"
"s_waitcnt lgkmcnt(8)\n"
"v_exp_f32 v153, v153\n"
"v_mfma_f32_16x16x32_bf16 v[168:171], v[212:215], a[4:7], v[168:171]\n"
"ds_read_b64_tr_b16 a[72:73], v249 offset:50176\n"
"ds_read_b64_tr_b16 a[90:91], v249 offset:51208\n"
"v_mfma_f32_16x16x32_bf16 v[168:171], v[216:219], a[8:11], v[168:171]\n"
"ds_read_b64_tr_b16 a[88:89], v249 offset:51200\n"
"v_cvt_pk_bf16_f32 v115, v178, v179\n"
"v_mfma_f32_16x16x32_bf16 v[168:171], v[220:223], a[12:15], v[168:171]\n"
"ds_read_b64_tr_b16 a[244:245], v232 offset:512\n"
"v_cvt_pk_bf16_f32 v106, v172, v173\n"
"v_mfma_f32_16x16x32_bf16 v[172:175], v[216:219], a[24:27], v[184:187]\n"
"ds_read_b64_tr_b16 a[248:249], v236 offset:512\n"
"v_cvt_pk_bf16_f32 v114, v176, v177\n"
"v_mfma_f32_16x16x32_bf16 v[172:175], v[220:223], a[28:31], v[172:175]\n"
"v_exp_f32 v162, v162\n"
"v_mfma_f32_16x16x32_bf16 v[172:175], v[208:211], a[16:19], v[172:175]\n"
"ds_read_b64_tr_b16 v[226:227], v237 offset:4608\n"
"v_cvt_pk_bf16_f32 v122, v152, v153\n"
"v_mfma_f32_16x16x32_bf16 v[172:175], v[212:215], a[20:23], v[172:175]\n"
"s_cbranch_vccnz LPCT1_%=\nLPKT1_%=:\n"
"ds_read_b64_tr_b16 a[252:253], v233 offset:512\n"
"v_mfma_f32_16x16x32_bf16 v[176:179], v[208:211], a[32:35], v[188:191]\n"
"v_exp_f32 v166, v166\n"
"v_mfma_f32_16x16x32_bf16 v[176:179], v[212:215], a[36:39], v[176:179]\n"
"ds_read_b64_tr_b16 a[254:255], v233 offset:4608\n"
"v_cvt_pk_bf16_f32 v109, v162, v163\n"
"v_mfma_f32_16x16x32_bf16 v[176:179], v[216:219], a[40:43], v[176:179]\n"
"v_exp_f32 v169, v169\n"
"v_mfma_f32_16x16x32_bf16 v[176:179], v[220:223], a[44:47], v[176:179]\n"
"s_waitcnt lgkmcnt(4)\n"
"v_cvt_pk_bf16_f32 v101, v158, v159\n"
"v_mfma_f32_16x16x32_bf16 a[224:227], v[124:127], a[244:247], 0\n"
"v_exp_f32 v154, v154\n"
"v_mfma_f32_16x16x32_bf16 v[16:19], v[128:131], a[244:247], 0\n"
"s_waitcnt vmcnt(8) lgkmcnt(3)\n"
"v_exp_f32 v168, v168\n"
"v_mfma_f32_16x16x32_bf16 v[16:19], v[132:135], a[248:251], v[16:19]\n"
"s_barrier\n"
"ds_read_b128 v[212:215], v229 offset:32768\n"
"ds_read_b128 v[216:219], v228 offset:33280\n"
"v_exp_f32 v165, v165\n"
"v_mfma_f32_16x16x32_bf16 v[48:51], v[136:139], a[244:247], 0\n"
"ds_read_b128 v[220:223], v229 offset:33280\n"
"v_xor_b32 v249, s75, v249\n"
"v_mfma_f32_16x16x32_bf16 v[48:51], v[140:143], a[248:251], v[48:51]\n"
"ds_read_b128 v[208:211], v228 offset:32768\n"
"v_add_u32 v243, 0x1000, v243\n"
"v_mfma_f32_16x16x32_bf16 v[80:83], v[144:147], a[244:247], 0\n"
"ds_read_b64_tr_b16 a[246:247], v234 offset:20480\n"
"v_cvt_pk_bf16_f32 v117, v166, v167\n"
"v_mfma_f32_16x16x32_bf16 v[80:83], v[148:151], a[248:251], v[80:83]\n"
"s_waitcnt lgkmcnt(5)\n"
"ds_read_b64_tr_b16 a[250:251], v238 offset:20480\n"
"v_xor_b32 v248, s75, v248\n"
"v_mfma_f32_16x16x32_bf16 v[84:87], v[148:151], v[224:227], 0\n"
"v_cvt_pk_bf16_f32 v100, v156, v157\n"
"v_cvt_pk_bf16_f32 v116, v164, v165\n"
"v_mfma_f32_16x16x32_bf16 v[84:87], v[144:147], a[252:255], v[84:87]\n"
"v_exp_f32 v160, v160\n"
"v_mfma_f32_16x16x32_bf16 v[52:55], v[140:143], v[224:227], 0\n"
"v_exp_f32 v176, v176\n"
"v_mfma_f32_16x16x32_bf16 v[52:55], v[136:139], a[252:255], v[52:55]\n"
"v_exp_f32 v177, v177\n"
"v_mfma_f32_16x16x32_bf16 v[20:23], v[132:135], v[224:227], 0\n"
"v_cvt_pk_bf16_f32 v123, v154, v155\n"
"v_cvt_pk_bf16_f32 v118, v176, v177\n"
"v_mfma_f32_16x16x32_bf16 v[20:23], v[128:131], a[252:255], v[20:23]\n"
"v_exp_f32 v174, v174\n"
"v_mfma_f32_16x16x32_bf16 a[228:231], v[124:127], a[252:255], 0\n"
"s_waitcnt lgkmcnt(1)\n"
"ds_read_b64_tr_b16 a[254:255], v235 offset:20480\n"
"s_branch LAY2_%=\n"
"LAX_%=:\n"
"v_mfma_f32_16x16x32_bf16 a[224:227], v[124:127], a[244:247], a[224:227]\n"
"v_exp_f32 v162, v162\n"
"v_mfma_f32_16x16x32_bf16 v[16:19], v[128:131], a[244:247], v[16:19]\n"
"s_waitcnt vmcnt(8)\n"
"v_exp_f32 v171, v171\n"
"v_mfma_f32_16x16x32_bf16 v[16:19], v[132:135], a[248:251], v[16:19]\n"
"s_barrier\n"
"v_exp_f32 v169, v169\n"
"v_exp_f32 v172, v172\n"
"v_exp_f32 v173, v173\n"
"v_exp_f32 v174, v174\n"
"v_lshlrev_b32_e64 v244, 24, s43\n"
"v_mfma_f32_16x16x32_bf16 v[48:51], v[136:139], a[244:247], v[48:51]\n"
"v_exp_f32 v170, v170\n"
"v_mfma_f32_16x16x32_bf16 v[48:51], v[140:143], a[248:251], v[48:51]\n"
"v_exp_f32 v179, v179\n"
"v_mfma_f32_16x16x32_bf16 v[80:83], v[144:147], a[244:247], v[80:83]\n"
"ds_read_b64_tr_b16 a[246:247], v234 offset:53248\n"
"v_cvt_pk_bf16_f32 v117, v166, v167\n"
"v_mfma_f32_16x16x32_bf16 v[80:83], v[148:151], a[248:251], v[80:83]\n"
"ds_read_b64_tr_b16 a[244:245], v234 offset:49152\n"
"v_cvt_pk_bf16_f32 v116, v164, v165\n"
"v_mfma_f32_16x16x32_bf16 v[84:87], v[148:151], v[224:227], v[84:87]\n"
"v_cvt_pk_bf16_f32 v100, v156, v157\n"
"v_accvgpr_read_b32 v253, a96\n"
"v_mfma_f32_16x16x32_bf16 v[84:87], v[144:147], a[252:255], v[84:87]\n"
"ds_read_b64_tr_b16 a[248:249], v238 offset:49152\n"
"ds_read_b64_tr_b16 a[250:251], v238 offset:53248\n"
"v_exp_f32 v168, v168\n"
"v_exp_f32 v175, v175\n"
"v_exp_f32 v176, v176\n"
"v_exp_f32 v177, v177\n"
"v_exp_f32 v178, v178\n"
"v_mfma_f32_16x16x32_bf16 v[52:55], v[140:143], v[224:227], v[52:55]\n"
"v_cvt_pk_bf16_f32 v102, v168, v169\n"
"v_cvt_pk_bf16_f32 v110, v172, v173\n"
"v_mfma_f32_16x16x32_bf16 v[52:55], v[136:139], a[252:255], v[52:55]\n"
"v_cvt_pk_bf16_f32 v103, v170, v171\n"
"v_and_b32 v253, 0x7fff0000, v253\n"
"v_mfma_f32_16x16x32_bf16 v[20:23], v[132:135], v[224:227], v[20:23]\n"
"ds_read_b64_tr_b16 v[224:225], v239 offset:49152\n"
"v_cvt_pk_bf16_f32 v101, v158, v159\n"
"v_mfma_f32_16x16x32_bf16 v[20:23], v[128:131], a[252:255], v[20:23]\n"
"v_cvt_pk_bf16_f32 v123, v154, v155\n"
"v_cvt_pk_bf16_f32 v111, v174, v175\n"
"v_mfma_f32_16x16x32_bf16 a[228:231], v[124:127], a[252:255], a[228:231]\n"
"s_waitcnt lgkmcnt(3)\n"
"ds_read_b64_tr_b16 a[254:255], v235 offset:53248\n"
"v_mul_f32 v244, 0x45800000, v244\n"
"v_mfma_f32_16x16x32_bf16 a[216:219], v[120:123], a[244:247], a[216:219]\n"
"s_mov_b64 s[28:29], s[8:9]\n"
"ds_read_b64_tr_b16 v[226:227], v239 offset:53248\n"
"v_cvt_pk_bf16_f32 v118, v176, v177\n"
"v_smfmac_f32_16x16x64_bf16 v[196:199], a[240:243], v[96:103], v241\n"
"s_mov_b32 s30, 655360\n"
"ds_read_b64_tr_b16 a[252:253], v235 offset:49152\n"
"v_cvt_pk_bf16_f32 v108, v160, v161\n"
"v_mfma_f32_16x16x32_bf16 v[8:11], v[96:99], a[244:247], v[8:11]\n"
"s_waitcnt lgkmcnt(4)\n"
"s_mov_b32 s71, s44\n"
"v_cvt_pk_bf16_f32 v119, v178, v179\n"
"v_max_u32_dpp v253, v253, v253 quad_perm:[1,0,3,2] row_mask:0xf bank_mask:0xf\n"
"v_mfma_f32_16x16x32_bf16 v[8:11], v[100:103], a[248:251], v[8:11]\n"
"s_mov_b32 s70, s95\n"
"v_and_b32 v252, 0x7fff0000, v252\n"
"v_and_b32 v245, 0x3f0, v242\n"
"v_smfmac_f32_16x16x64_bf16 v[200:203], a[240:243], v[112:119], v241\n"
"v_cvt_pk_bf16_f32 v109, v162, v163\n"
"v_max_u32_dpp v253, v253, v253 quad_perm:[2,3,0,1] row_mask:0xf bank_mask:0xf\n"
"v_mfma_f32_16x16x32_bf16 v[40:43], v[104:107], a[244:247], v[40:43]\n"
"v_add_f32 v196, v196, v197\n"
"v_lshrrev_b32 v245, 1, v245\n"
"v_mfma_f32_16x16x32_bf16 v[40:43], v[108:111], a[248:251], v[40:43]\n"
"s_add_u32 s41, s70, 327680\n"
"s_add_u32 m0, s71, 32768\n"
"v_max_u32_dpp v252, v252, v252 quad_perm:[1,0,3,2] row_mask:0xf bank_mask:0xf\n"
"v_smfmac_f32_16x16x64_bf16 v[198:201], a[240:243], v[104:111], v241\n"
"buffer_load_dwordx4 v240, s[28:31], s41 offen lds\n"
"v_mfma_f32_16x16x32_bf16 v[72:75], v[112:115], a[244:247], v[72:75]\n"
"v_max_u32_dpp v252, v252, v252 quad_perm:[2,3,0,1] row_mask:0xf bank_mask:0xf\n"
"v_max_u32_dpp v253, v253, v253 row_half_mirror row_mask:0xf bank_mask:0xf\n"
"v_mfma_f32_16x16x32_bf16 v[72:75], v[116:119], a[248:251], v[72:75]\n"
"ds_read_b64_tr_b16 a[244:245], v234 offset:49664\n"
"v_sub_u32 v245, v242, v245\n"
"v_mfma_f32_16x16x32_bf16 v[202:205], a[240:243], v[120:123], v[202:205]\n"
"s_waitcnt lgkmcnt(1)\n"
"ds_read_b64_tr_b16 a[246:247], v234 offset:53760\n"
"v_add_f32 v198, v198, v199\n"
"v_mfma_f32_16x16x32_bf16 v[76:79], v[116:119], v[224:227], v[76:79]\n"
"s_add_u32 s40, s41, s52\n"
"ds_read_b64_tr_b16 a[248:249], v238 offset:49664\n"
"ds_read_b64_tr_b16 a[250:251], v238 offset:53760\n"
"v_mfma_f32_16x16x32_bf16 v[76:79], v[112:115], a[252:255], v[76:79]\n"
"buffer_load_dwordx4 v240, s[28:31], s40 offen offset:1024 lds\n"
"v_mfma_f32_16x16x32_bf16 v[44:47], v[108:111], v[224:227], v[44:47]\n"
"v_add_f32 v200, v200, v201\n"
"v_add_f32 v244, v244, v202\n"
"v_mfma_f32_16x16x32_bf16 v[44:47], v[104:107], a[252:255], v[44:47]\n"
"v_add_f32 v254, v196, v198\n"
"v_min3_f32 v244, v244, v196, v198\n"
"v_min_f32 v244, v244, v200\n"
"v_rcp_f32 v244, v244\n"
"v_mfma_f32_16x16x32_bf16 v[12:15], v[100:103], v[224:227], v[12:15]\n"
"s_add_u32 s40, s41, s53\n"
"ds_read_b64_tr_b16 v[224:225], v239 offset:49664\n"
"ds_read_b64_tr_b16 v[226:227], v239 offset:53760\n"
"v_mfma_f32_16x16x32_bf16 v[12:15], v[96:99], a[252:255], v[12:15]\n"
"buffer_load_dwordx4 v240, s[28:31], s40 offen offset:2048 lds\n"
"v_min_u32_dpp v253, v253, v253 row_mirror row_mask:0xf bank_mask:0xf\n"
"v_add_f32 v254, v254, v200\n"
"v_add_f32 v254, v254, v202\n"
"v_fmac_f32 v254, 0x68800000, v244\n"
"v_cmp_nge_f32 vcc, s45, v254\n"
"v_bfe_u32 v254, v242, 8, 2\n"
"v_mfma_f32_16x16x32_bf16 a[220:223], v[120:123], a[252:255], a[220:223]\n"
"s_waitcnt lgkmcnt(4)\n"
"v_xor_b32 v244, 0x11000, v242\n"
"s_cbranch_vccnz LSLOW_%=\nLBACK3_%=:\ns_mov_b64 vcc, s[64:65]\n"
"v_mfma_f32_16x16x32_bf16 a[232:235], v[120:123], a[244:247], a[232:235]\n"
"ds_read_b64_tr_b16 a[252:253], v235 offset:49664\n"
"ds_read_b64_tr_b16 a[254:255], v235 offset:53760\n"
"v_min_u32_dpp v253, v253, v253 row_bcast:15 row_mask:0xa bank_mask:0xf\n"
"v_rcp_f32 v246, v196\n"
"v_rcp_f32 v247, v198\n"
"v_rcp_f32 v206, v200\n"
"v_xor_b32 v250, 0x1000, v245\n"
"v_mfma_f32_16x16x32_bf16 v[24:27], v[96:99], a[244:247], v[24:27]\n"
"s_waitcnt lgkmcnt(4)\n"
"v_mov_b32_dpp v206, v206 row_ror:12 row_mask:0x2 bank_mask:0xf\n"
"v_lshl_add_u32 v254, v254, 3, s43\n"
"v_mfma_f32_16x16x32_bf16 v[24:27], v[100:103], a[248:251], v[24:27]\n"
"ds_write_b128 v244, a[220:223] offset:33792\n"
"v_max_u32_dpp v252, v252, v252 row_half_mirror row_mask:0xf bank_mask:0xf\n"
"v_mov_b32_dpp v246, v246 row_ror:12 row_mask:0x2 bank_mask:0xf\n"
"v_mov_b32_dpp v206, v206 row_ror:8 row_mask:0x4 bank_mask:0xf\n"
"v_mfma_f32_16x16x32_bf16 v[56:59], v[104:107], a[244:247], v[56:59]\n"
"s_add_u32 s40, s41, s54\n"
"v_min_u32_dpp v252, v252, v252 row_mirror row_mask:0xf bank_mask:0xf\n"
"v_mov_b32_dpp v247, v247 row_ror:12 row_mask:0x2 bank_mask:0xf\n"
"v_mfma_f32_16x16x32_bf16 v[56:59], v[108:111], a[248:251], v[56:59]\n"
"buffer_load_dwordx4 v240, s[28:31], s40 offen offset:3072 lds\n"
"v_mfma_f32_16x16x32_bf16 v[88:91], v[112:115], a[244:247], v[88:91]\n"
"s_add_u32 s71, s44, s75\n"
"ds_read_b64_tr_b16 a[244:245], v232 offset:49152\n"
"ds_read_b64_tr_b16 a[246:247], v232 offset:53248\n"
"v_mfma_f32_16x16x32_bf16 v[88:91], v[116:119], a[248:251], v[88:91]\n"
"s_waitcnt lgkmcnt(5)\n"
"s_mov_b32 s70, s100\n"
"ds_write_b128 v244, a[232:235] offset:34816\n"
"v_mov_b32_dpp v246, v246 row_ror:8 row_mask:0x4 bank_mask:0xf\n"
"v_mov_b32_dpp v247, v247 row_ror:8 row_mask:0x4 bank_mask:0xf\n"
"v_mov_b32_dpp v206, v206 row_ror:4 row_mask:0x8 bank_mask:0xf\n"
"v_lshl_add_u32 v254, v254, 6, s75\n"
"v_sub_u32 v230, v234, v254\n"
"v_mfma_f32_16x16x32_bf16 v[92:95], v[116:119], v[224:227], v[92:95]\n"
"s_waitcnt lgkmcnt(4)\n"
"s_add_u32 s41, s70, 163840\n"
"s_add_u32 m0, s71, 16384\n"
"ds_read_b64_tr_b16 a[248:249], v236 offset:49152\n"
"v_mfma_f32_16x16x32_bf16 v[92:95], v[112:115], a[252:255], v[92:95]\n"
"buffer_load_dwordx4 v240, s[28:31], s41 offen lds\n"
"ds_read_b64_tr_b16 a[250:251], v236 offset:53248\n"
"v_min_u32_dpp v252, v252, v252 row_bcast:15 row_mask:0xa bank_mask:0xf\n"
"v_mov_b32_dpp v246, v246 row_ror:4 row_mask:0x8 bank_mask:0xf\n"
"v_mov_b32_dpp v247, v247 row_ror:4 row_mask:0x8 bank_mask:0xf\n"
"v_mul_f32_dpp v72, v206, v72 row_newbcast:0 row_mask:0xf bank_mask:0xf\n"
"v_mul_f32_dpp v74, v206, v74 row_newbcast:2 row_mask:0xf bank_mask:0xf\n"
"v_mul_f32_dpp v76, v206, v76 row_newbcast:0 row_mask:0xf bank_mask:0xf\n"
"v_mul_f32_dpp v77, v206, v77 row_newbcast:1 row_mask:0xf bank_mask:0xf\n"
"v_mfma_f32_16x16x32_bf16 v[60:63], v[108:111], v[224:227], v[60:63]\n"
"v_min_u32_dpp v252, v252, v252 row_bcast:31 row_mask:0xc bank_mask:0xf\n"
"v_mul_f32_dpp v8, v246, v8 row_newbcast:0 row_mask:0xf bank_mask:0xf\n"
"v_mfma_f32_16x16x32_bf16 v[60:63], v[104:107], a[252:255], v[60:63]\n"
"v_mul_f32_dpp v41, v247, v41 row_newbcast:1 row_mask:0xf bank_mask:0xf\n"
"v_mul_f32_dpp v59, v247, v59 row_newbcast:3 row_mask:0xf bank_mask:0xf\n"
"v_mfma_f32_16x16x32_bf16 v[28:31], v[100:103], v[224:227], v[28:31]\n"
"s_add_u32 s40, s41, s52\n"
"ds_read_b64_tr_b16 v[224:225], v237 offset:49152\n"
"v_min_u32_dpp v253, v253, v253 row_bcast:31 row_mask:0xc bank_mask:0xf\n"
"v_mfma_f32_16x16x32_bf16 v[28:31], v[96:99], a[252:255], v[28:31]\n"
"buffer_load_dwordx4 v240, s[28:31], s40 offen offset:1024 lds\n"
"v_mfma_f32_16x16x32_bf16 a[236:239], v[120:123], a[252:255], a[236:239]\n"
"s_waitcnt lgkmcnt(4)\n"
"v_mul_f32_dpp v25, v246, v25 row_newbcast:1 row_mask:0xf bank_mask:0xf\n"
"v_mul_f32_dpp v94, v206, v94 row_newbcast:2 row_mask:0xf bank_mask:0xf\n"
"v_mfma_f32_16x16x32_bf16 a[208:211], v[120:123], a[244:247], a[208:211]\n"
"ds_read_b64_tr_b16 a[252:253], v233 offset:49152\n"
"ds_read_b64_tr_b16 a[254:255], v233 offset:53248\n"
"v_mul_f32 v252, 0x43340000, v252\n"
"v_mul_f32 v252, v252, v253\n"
"v_mul_f32_dpp v10, v246, v10 row_newbcast:2 row_mask:0xf bank_mask:0xf\n"
"v_mul_f32_dpp v14, v246, v14 row_newbcast:2 row_mask:0xf bank_mask:0xf\n"
"v_mul_f32_dpp v40, v247, v40 row_newbcast:0 row_mask:0xf bank_mask:0xf\n"
"v_mul_f32_dpp v46, v247, v46 row_newbcast:2 row_mask:0xf bank_mask:0xf\n"
"v_mul_f32_dpp v73, v206, v73 row_newbcast:1 row_mask:0xf bank_mask:0xf\n"
"v_mul_f32_dpp v90, v206, v90 row_newbcast:2 row_mask:0xf bank_mask:0xf\n"
"v_mfma_f32_16x16x32_bf16 v[0:3], v[96:99], a[244:247], v[0:3]\n"
"s_waitcnt lgkmcnt(3)\n"
"ds_read_b64_tr_b16 v[226:227], v237 offset:53248\n"
"v_mul_f32_dpp v57, v247, v57 row_newbcast:1 row_mask:0xf bank_mask:0xf\n"
"v_mfma_f32_16x16x32_bf16 v[0:3], v[100:103], a[248:251], v[0:3]\n"
"ds_write_b128 v244, a[216:219] offset:32768\n"
"v_min_f32 v252, 0x71800000, v252\n"
"v_mul_f32_dpp v12, v246, v12 row_newbcast:0 row_mask:0xf bank_mask:0xf\n"
"v_mul_f32_dpp v45, v247, v45 row_newbcast:1 row_mask:0xf bank_mask:0xf\n"
"v_mul_f32_dpp v58, v247, v58 row_newbcast:2 row_mask:0xf bank_mask:0xf\n"
"v_mul_f32_dpp v92, v206, v92 row_newbcast:0 row_mask:0xf bank_mask:0xf\n"
"v_mfma_f32_16x16x32_bf16 v[32:35], v[104:107], a[244:247], v[32:35]\n"
"s_add_u32 s40, s41, s53\n"
"v_readlane_b32 s22, v252, 63\n"
"v_mul_f32_dpp v78, v206, v78 row_newbcast:2 row_mask:0xf bank_mask:0xf\n"
"v_mfma_f32_16x16x32_bf16 v[32:35], v[108:111], a[248:251], v[32:35]\n"
"buffer_load_dwordx4 v240, s[28:31], s40 offen offset:2048 lds\n"
"v_mfma_f32_16x16x32_bf16 v[64:67], v[112:115], a[244:247], v[64:67]\n"
"v_mul_f32_dpp v75, v206, v75 row_newbcast:3 row_mask:0xf bank_mask:0xf\n"
"v_mul_f32_dpp v61, v247, v61 row_newbcast:1 row_mask:0xf bank_mask:0xf\n"
"v_mfma_f32_16x16x32_bf16 v[64:67], v[116:119], a[248:251], v[64:67]\n"
"s_waitcnt lgkmcnt(1)\n"
"ds_read_b64_tr_b16 a[244:245], v232 offset:49664\n"
"ds_read_b64_tr_b16 a[246:247], v232 offset:53760\n"
"v_mov_b32 v253, v202\n"
"v_sub_u32 v228, v232, v254\n"
"v_mul_f32_dpp v9, v246, v9 row_newbcast:1 row_mask:0xf bank_mask:0xf\n"
"v_mul_f32_dpp v11, v246, v11 row_newbcast:3 row_mask:0xf bank_mask:0xf\n"
"v_mul_f32_dpp v15, v246, v15 row_newbcast:3 row_mask:0xf bank_mask:0xf\n"
"v_mul_f32_dpp v47, v247, v47 row_newbcast:3 row_mask:0xf bank_mask:0xf\n"
"v_cvt_pk_bf16_f32 v40, v40, v41\n"
"v_cvt_pk_bf16_f32 v72, v72, v73\n"
"v_mfma_f32_16x16x32_bf16 v[68:71], v[116:119], v[224:227], v[68:71]\n"
"s_add_u32 s40, s41, s54\n"
"ds_read_b64_tr_b16 a[248:249], v236 offset:49664\n"
"ds_read_b64_tr_b16 a[250:251], v236 offset:53760\n"
"v_mfma_f32_16x16x32_bf16 v[68:71], v[112:115], a[252:255], v[68:71]\n"
"buffer_load_dwordx4 v240, s[28:31], s40 offen offset:3072 lds\n"
"v_readlane_b32 s23, v252, 63\n"
"v_mul_f32_dpp v43, v247, v43 row_newbcast:3 row_mask:0xf bank_mask:0xf\n"
"v_mul_f32_dpp v24, v246, v24 row_newbcast:0 row_mask:0xf bank_mask:0xf\n"
"v_mul_f32_dpp v29, v246, v29 row_newbcast:1 row_mask:0xf bank_mask:0xf\n"
"v_mul_f32_dpp v56, v247, v56 row_newbcast:0 row_mask:0xf bank_mask:0xf\n"
"v_mul_f32_dpp v1, v246, v1 row_newbcast:1 row_mask:0xf bank_mask:0xf\n"
"v_accvgpr_read_b32 v255, a209\n"
"v_xor_b32 v232, 0x1000, v228\n"
"v_mfma_f32_16x16x32_bf16 v[36:39], v[108:111], v[224:227], v[36:39]\n"
"v_mov_b32 v252, v192\n"
"v_mul_f32_dpp v44, v247, v44 row_newbcast:0 row_mask:0xf bank_mask:0xf\n"
"v_mfma_f32_16x16x32_bf16 v[36:39], v[104:107], a[252:255], v[36:39]\n"
"ds_write_b128 v244, a[236:239] offset:35840\n"
"v_mul_f32_dpp v13, v246, v13 row_newbcast:1 row_mask:0xf bank_mask:0xf\n"
"v_mul_f32_dpp v42, v247, v42 row_newbcast:2 row_mask:0xf bank_mask:0xf\n"
"v_cvt_pk_bf16_f32 v41, v42, v43\n"
"v_cvt_pk_bf16_f32 v42, v44, v45\n"
"v_cvt_pk_bf16_f32 v56, v56, v57\n"
"v_mfma_f32_16x16x32_bf16 v[4:7], v[100:103], v[224:227], v[4:7]\n"
"ds_read_b64_tr_b16 v[224:225], v237 offset:49664\n"
"ds_read_b64_tr_b16 v[226:227], v237 offset:53760\n"
"v_mfma_f32_16x16x32_bf16 v[4:7], v[96:99], a[252:255], v[4:7]\n"
"ds_write_b64 v250, v[252:253] offset:52224\n"
"v_cvt_pk_bf16_f32 v8, v8, v9\n"
"v_cvt_pk_bf16_f32 v43, v46, v47\n"
"v_cvt_pk_bf16_f32 v73, v74, v75\n"
"v_mul_f32_dpp v28, v246, v28 row_newbcast:0 row_mask:0xf bank_mask:0xf\n"
"v_cvt_pk_bf16_f32 v57, v58, v59\n"
"v_mul_f32_dpp v0, v246, v0 row_newbcast:0 row_mask:0xf bank_mask:0xf\n"
"v_mul_f32_dpp v33, v247, v33 row_newbcast:1 row_mask:0xf bank_mask:0xf\n"
"v_mfma_f32_16x16x32_bf16 a[212:215], v[120:123], a[252:255], a[212:215]\n"
"s_waitcnt lgkmcnt(6)\n"
"ds_read_b64_tr_b16 a[252:253], v233 offset:49664\n"
"ds_read_b64_tr_b16 a[254:255], v233 offset:53760\n"
"v_cvt_pk_bf16_f32 v9, v10, v11\n"
"v_cvt_pk_bf16_f32 v74, v76, v77\n"
"v_mul_f32_dpp v60, v247, v60 row_newbcast:0 row_mask:0xf bank_mask:0xf\n"
"v_mul_f32_dpp v62, v247, v62 row_newbcast:2 row_mask:0xf bank_mask:0xf\n"
"v_mul_f32_dpp v88, v206, v88 row_newbcast:0 row_mask:0xf bank_mask:0xf\n"
"v_mul_f32_dpp v64, v206, v64 row_newbcast:0 row_mask:0xf bank_mask:0xf\n"
"v_mul_f32_dpp v66, v206, v66 row_newbcast:2 row_mask:0xf bank_mask:0xf\n"
"v_mfma_f32_16x16x32_bf16 a[224:227], v[120:123], a[244:247], a[224:227]\n"
"ds_write_b128 v248, v[40:43] offset:50176\n"
"v_sub_u32 v229, v233, v254\n"
"v_cvt_pk_bf16_f32 v11, v14, v15\n"
"v_mul_f32_dpp v27, v246, v27 row_newbcast:3 row_mask:0xf bank_mask:0xf\n"
"v_mul_f32_dpp v3, v246, v3 row_newbcast:3 row_mask:0xf bank_mask:0xf\n"
"v_mul_f32_dpp v68, v206, v68 row_newbcast:0 row_mask:0xf bank_mask:0xf\n"
"v_mfma_f32_16x16x32_bf16 v[16:19], v[96:99], a[244:247], v[16:19]\n"
"s_waitcnt vmcnt(8) lgkmcnt(1)\n"
"v_mul_f32_dpp v89, v206, v89 row_newbcast:1 row_mask:0xf bank_mask:0xf\n"
"v_add_u32 v236, s76, v249\n"
"v_mfma_f32_16x16x32_bf16 v[16:19], v[100:103], a[248:251], v[16:19]\n"
"s_barrier\n"
"ds_read_b64_tr_b16 a[22:23], v249 offset:50184\n"
"ds_read_b64_tr_b16 v[182:183], v228 offset:2048\n"
"ds_read_b64_tr_b16 v[216:217], v228 offset:24576\n"
"ds_read_b64_tr_b16 v[218:219], v228 offset:26624\n"
"ds_read_b64_tr_b16 a[202:203], v236 offset:2056\n"
"v_mul_f32_dpp v63, v247, v63 row_newbcast:3 row_mask:0xf bank_mask:0xf\n"
"v_mul_f32_dpp v91, v206, v91 row_newbcast:3 row_mask:0xf bank_mask:0xf\n"
"v_mul_f32_dpp v2, v246, v2 row_newbcast:2 row_mask:0xf bank_mask:0xf\n"
"v_mfma_f32_16x16x32_bf16 v[48:51], v[104:107], a[244:247], v[48:51]\n"
"ds_read_b64_tr_b16 v[192:193], v232 offset:8192\n"
"ds_read_b64_tr_b16 a[240:241], v236 offset:4096\n"
"v_mfma_f32_16x16x32_bf16 v[48:51], v[108:111], a[248:251], v[48:51]\n"
"ds_read_b64 v[250:251], v245 offset:52224\n"
"ds_read_b64_tr_b16 v[186:187], v232 offset:2048\n"
"ds_read_b64_tr_b16 v[194:195], v232 offset:10240\n"
"ds_read_b64_tr_b16 v[208:209], v228 offset:16384\n"
"v_cvt_pk_bf16_f32 v10, v12, v13\n"
"v_mul_f32_dpp v79, v206, v79 row_newbcast:3 row_mask:0xf bank_mask:0xf\n"
"v_cvt_pk_bf16_f32 v75, v78, v79\n"
"v_cvt_pk_bf16_f32 v88, v88, v89\n"
"v_cvt_pk_bf16_f32 v89, v90, v91\n"
"v_cvt_pk_bf16_f32 v0, v0, v1\n"
"v_mfma_f32_16x16x32_bf16 v[80:83], v[112:115], a[244:247], v[80:83]\n"
"ds_read_b64_tr_b16 v[180:181], v228 offset:0\n"
"ds_read_b64_tr_b16 v[188:189], v228 offset:8192\n"
"v_mfma_f32_16x16x32_bf16 v[80:83], v[116:119], a[248:251], v[80:83]\n"
"ds_write_b128 v248, v[72:75] offset:51200\n"
"v_mul_f32_dpp v30, v246, v30 row_newbcast:2 row_mask:0xf bank_mask:0xf\n"
"v_cvt_pk_bf16_f32 v24, v24, v25\n"
"v_cvt_pk_bf16_f32 v58, v60, v61\n"
"v_mul_f32_dpp v93, v206, v93 row_newbcast:1 row_mask:0xf bank_mask:0xf\n"
"v_mul_f32_dpp v36, v247, v36 row_newbcast:0 row_mask:0xf bank_mask:0xf\n"
"v_mfma_f32_16x16x32_bf16 v[84:87], v[116:119], v[224:227], v[84:87]\n"
"ds_write_b128 v248, v[8:11] offset:49152\n"
"v_mul_f32_dpp v26, v246, v26 row_newbcast:2 row_mask:0xf bank_mask:0xf\n"
"v_mul_f32_dpp v32, v247, v32 row_newbcast:0 row_mask:0xf bank_mask:0xf\n"
"v_mul_f32_dpp v65, v206, v65 row_newbcast:1 row_mask:0xf bank_mask:0xf\n"
"v_mul_f32_dpp v69, v206, v69 row_newbcast:1 row_mask:0xf bank_mask:0xf\n"
"v_xor_b32 v233, 0x1000, v229\n"
"v_mfma_f32_16x16x32_bf16 v[84:87], v[112:115], a[252:255], v[84:87]\n"
"ds_read_b64_tr_b16 a[6:7], v249 offset:49160\n"
"ds_read_b64_tr_b16 a[36:37], v249 offset:51200\n"
"ds_read_b64_tr_b16 v[210:211], v228 offset:18432\n"
"ds_read_b64_tr_b16 v[222:223], v232 offset:26624\n"
"v_mul_f32_dpp v31, v246, v31 row_newbcast:3 row_mask:0xf bank_mask:0xf\n"
"v_cvt_pk_bf16_f32 v25, v26, v27\n"
"v_cvt_pk_bf16_f32 v59, v62, v63\n"
"v_mul_f32_dpp v34, v247, v34 row_newbcast:2 row_mask:0xf bank_mask:0xf\n"
"v_mov_b64 v[196:197], s[22:23]\n"
"v_xor_b32 v237, 0x400, v236\n"
"v_mfma_f32_16x16x32_bf16 v[52:55], v[108:111], v[224:227], v[52:55]\n"
"ds_read_b64_tr_b16 v[184:185], v232 offset:0\n"
"ds_read_b64_tr_b16 v[212:213], v232 offset:16384\n"
"v_mfma_f32_16x16x32_bf16 v[52:55], v[104:107], a[252:255], v[52:55]\n"
"ds_read_b64_tr_b16 a[4:5], v249 offset:49152\n"
"ds_read_b64_tr_b16 a[20:21], v249 offset:50176\n"
"ds_read_b64_tr_b16 a[38:39], v249 offset:51208\n"
"ds_read_b64_tr_b16 v[220:221], v232 offset:24576\n"
"ds_read_b64_tr_b16 a[246:247], v237 offset:4104\n"
"v_cvt_pk_bf16_f32 v26, v28, v29\n"
"v_mul_f32_dpp v95, v206, v95 row_newbcast:3 row_mask:0xf bank_mask:0xf\n"
"v_cvt_pk_bf16_f32 v91, v94, v95\n"
"v_mul_f32_dpp v5, v246, v5 row_newbcast:1 row_mask:0xf bank_mask:0xf\n"
"v_mfma_f32_16x16x32_bf16 v[20:23], v[100:103], v[224:227], v[20:23]\n"
"ds_read_b64_tr_b16 v[190:191], v228 offset:10240\n"
"ds_read_b64_tr_b16 v[214:215], v232 offset:18432\n"
"v_mfma_f32_16x16x32_bf16 v[20:23], v[96:99], a[252:255], v[20:23]\n"
"ds_write_b128 v248, v[56:59] offset:50176\n"
"v_cvt_pk_bf16_f32 v27, v30, v31\n"
"v_mul_f32_dpp v4, v246, v4 row_newbcast:0 row_mask:0xf bank_mask:0xf\n"
"v_mul_f32_dpp v35, v247, v35 row_newbcast:3 row_mask:0xf bank_mask:0xf\n"
"v_cvt_pk_bf16_f32 v64, v64, v65\n"
"v_mov_b64 v[198:199], s[22:23]\n"
"v_mfma_f32_16x16x32_bf16 a[228:231], v[120:123], a[252:255], a[228:231]\n"
"s_waitcnt lgkmcnt(15)\n"
"ds_write_b128 v248, v[24:27] offset:49152\n"
"v_cvt_pk_bf16_f32 v90, v92, v93\n"
"v_mul_f32_dpp v6, v246, v6 row_newbcast:2 row_mask:0xf bank_mask:0xf\n"
"v_cvt_pk_bf16_f32 v1, v2, v3\n"
"v_cvt_pk_bf16_f32 v2, v4, v5\n"
"v_mul_f32_dpp v71, v206, v71 row_newbcast:3 row_mask:0xf bank_mask:0xf\n"
"v_mfma_f32_16x16x32_bf16 v[124:127], v[180:183], a[96:99], v[196:199]\n"
"s_waitcnt lgkmcnt(10)\n"
"ds_read_b64_tr_b16 a[14:15], v249 offset:49160\n"
"ds_read_b64_tr_b16 a[30:31], v249 offset:50184\n"
"v_mfma_f32_16x16x32_bf16 v[124:127], v[184:187], a[100:103], v[124:127]\n"
"s_waitcnt lgkmcnt(5)\n"
"ds_write_b128 v248, v[88:91] offset:51200\n"
"v_mul_f32_dpp v7, v246, v7 row_newbcast:3 row_mask:0xf bank_mask:0xf\n"
"v_cvt_pk_bf16_f32 v3, v6, v7\n"
"v_mul_f32_dpp v37, v247, v37 row_newbcast:1 row_mask:0xf bank_mask:0xf\n"
"v_cvt_pk_bf16_f32 v32, v32, v33\n"
"v_mul_f32_dpp v67, v206, v67 row_newbcast:3 row_mask:0xf bank_mask:0xf\n"
"v_mfma_f32_16x16x32_bf16 v[124:127], v[188:191], a[104:107], v[124:127]\n"
"ds_read_b64_tr_b16 a[12:13], v249 offset:49152\n"
"ds_read_b64_tr_b16 a[44:45], v249 offset:51200\n"
"ds_read_b64_tr_b16 a[46:47], v249 offset:51208\n"
"ds_read_b64_tr_b16 v[100:101], v233 offset:0\n"
"ds_read_b64_tr_b16 v[120:121], v229 offset:24576\n"
"v_mul_f32_dpp v38, v247, v38 row_newbcast:2 row_mask:0xf bank_mask:0xf\n"
"v_mul_f32_dpp v70, v206, v70 row_newbcast:2 row_mask:0xf bank_mask:0xf\n"
"v_cvt_pk_bf16_f32 v65, v66, v67\n"
"v_cvt_pk_bf16_f32 v66, v68, v69\n"
"v_cvt_pk_bf16_f32 v67, v70, v71\n"
"v_mfma_f32_16x16x32_bf16 v[124:127], v[192:195], a[108:111], v[124:127]\n"
"ds_write_b128 v248, v[0:3] offset:49152\n"
"v_mul_f32_dpp v39, v247, v39 row_newbcast:3 row_mask:0xf bank_mask:0xf\n"
"v_cvt_pk_bf16_f32 v33, v34, v35\n"
"v_cvt_pk_bf16_f32 v34, v36, v37\n"
"v_cvt_pk_bf16_f32 v35, v38, v39\n"
"v_xor_b32 v244, 0x1000, v244\n"
"v_mfma_f32_16x16x32_bf16 v[124:127], v[208:211], a[144:147], v[124:127]\n"
"s_waitcnt lgkmcnt(11)\n"
"ds_read_b64_tr_b16 a[28:29], v249 offset:50176\n"
"ds_read_b128 v[24:27], v244 offset:32768\n"
"v_mfma_f32_16x16x32_bf16 v[124:127], v[212:215], a[148:151], v[124:127]\n"
"ds_write_b128 v248, v[32:35] offset:50176\n"
"ds_write_b128 v248, v[64:67] offset:51200\n"
"v_mfma_f32_16x16x32_bf16 v[124:127], v[216:219], a[152:155], v[124:127]\n"
"ds_read_b64_tr_b16 a[0:1], v249 offset:49152\n"
"ds_read_b64_tr_b16 a[32:33], v249 offset:51200\n"
"v_mfma_f32_16x16x32_bf16 v[124:127], v[220:223], a[156:159], v[124:127]\n"
"ds_read_b64_tr_b16 a[16:17], v249 offset:50176\n"
"ds_read_b64_tr_b16 a[18:19], v249 offset:50184\n"
"ds_read_b128 v[56:59], v244 offset:34816\n"
"ds_read_b64_tr_b16 a[196:197], v237 offset:0\n"
"v_sub_u32 v231, v235, v254\n"
"v_mfma_f32_16x16x32_bf16 v[140:143], v[220:223], a[172:175], v[196:199]\n"
"ds_read_b64_tr_b16 a[2:3], v249 offset:49160\n"
"ds_read_b64_tr_b16 a[34:35], v249 offset:51208\n"
"v_mfma_f32_16x16x32_bf16 v[140:143], v[216:219], a[168:171], v[140:143]\n"
"ds_read_b128 v[28:31], v244 offset:33792\n"
"ds_read_b128 v[60:63], v244 offset:35840\n"
"v_mfma_f32_16x16x32_bf16 v[140:143], v[212:215], a[164:167], v[140:143]\n"
"ds_read_b64_tr_b16 a[200:201], v236 offset:2048\n"
"ds_read_b64_tr_b16 a[254:255], v237 offset:6152\n"
"v_mfma_f32_16x16x32_bf16 v[140:143], v[208:211], a[160:163], v[140:143]\n"
"ds_read_b64_tr_b16 a[206:207], v237 offset:2056\n"
"ds_read_b64_tr_b16 a[242:243], v236 offset:4104\n"
"v_mfma_f32_16x16x32_bf16 v[140:143], v[192:195], a[124:127], v[140:143]\n"
"ds_read_b64_tr_b16 a[244:245], v237 offset:4096\n"
"v_xor_b32 v234, 0x1000, v230\n"
"v_mfma_f32_16x16x32_bf16 v[140:143], v[188:191], a[120:123], v[140:143]\n"
"ds_read_b64_tr_b16 v[110:111], v233 offset:10240\n"
"ds_read_b64_tr_b16 a[250:251], v236 offset:6152\n"
"v_mfma_f32_16x16x32_bf16 v[140:143], v[184:187], a[116:119], v[140:143]\n"
"ds_read_b64_tr_b16 a[204:205], v237 offset:2048\n"
"ds_read_b64_tr_b16 v[102:103], v233 offset:2048\n"
"v_mfma_f32_16x16x32_bf16 v[140:143], v[180:183], a[112:115], v[140:143]\n"
"ds_read_b64_tr_b16 a[248:249], v236 offset:6144\n"
"ds_read_b64_tr_b16 a[252:253], v237 offset:6144\n"
"v_mfma_f32_16x16x32_bf16 v[156:159], v[180:183], a[128:131], v[196:199]\n"
"ds_read_b64_tr_b16 v[98:99], v229 offset:2048\n"
"ds_read_b64_tr_b16 v[116:117], v233 offset:16384\n"
"v_mfma_f32_16x16x32_bf16 v[156:159], v[184:187], a[132:135], v[156:159]\n"
"ds_read_b64_tr_b16 v[122:123], v229 offset:26624\n"
"ds_read_b64_tr_b16 v[224:225], v233 offset:24576\n"
"v_mfma_f32_16x16x32_bf16 v[156:159], v[188:191], a[136:139], v[156:159]\n"
"ds_read_b64_tr_b16 a[198:199], v237 offset:8\n"
"ds_read_b64_tr_b16 v[96:97], v229 offset:0\n"
"v_mfma_f32_16x16x32_bf16 v[156:159], v[192:195], a[140:143], v[156:159]\n"
"s_waitcnt lgkmcnt(13)\n"
"ds_read_b64_tr_b16 v[108:109], v233 offset:8192\n"
"v_mul_f32_dpp v23, v246, v23 row_newbcast:3 row_mask:0xf bank_mask:0xf\n"
"v_mfma_f32_16x16x32_bf16 v[156:159], v[208:211], a[176:179], v[156:159]\n"
"ds_read_b64_tr_b16 v[106:107], v229 offset:10240\n"
"ds_read_b64_tr_b16 v[112:113], v229 offset:16384\n"
"v_mfma_f32_16x16x32_bf16 v[156:159], v[212:215], a[180:183], v[156:159]\n"
"s_add_u32 s41, s95, 491520\n"
"ds_read_b64_tr_b16 a[192:193], v236 offset:0\n"
"ds_read_b64_tr_b16 v[118:119], v233 offset:18432\n"
"v_mfma_f32_16x16x32_bf16 v[156:159], v[216:219], a[184:187], v[156:159]\n"
"s_add_u32 m0, s44, 49152\n"
"ds_read_b64_tr_b16 a[194:195], v236 offset:8\n"
"ds_read_b64_tr_b16 v[104:105], v229 offset:8192\n"
"v_mfma_f32_16x16x32_bf16 v[156:159], v[220:223], a[188:191], v[156:159]\n"
"s_waitcnt lgkmcnt(13)\n"
"buffer_load_dwordx4 v240, s[28:31], s41 offen lds\n"
"v_mfma_f32_16x16x32_bf16 v[172:175], v[220:223], a[252:255], v[196:199]\n"
"s_add_u32 s40, s41, s52\n"
"v_min_f32 v254, v252, v250\n"
"v_sub_f32 v250, v254, v250\n"
"v_mfma_f32_16x16x32_bf16 v[172:175], v[216:219], a[248:251], v[172:175]\n"
"buffer_load_dwordx4 v240, s[28:31], s40 offen offset:1024 lds\n"
"v_mfma_f32_16x16x32_bf16 v[172:175], v[212:215], a[244:247], v[172:175]\n"
"s_add_u32 s40, s41, s53\n"
"ds_read_b64_tr_b16 v[114:115], v229 offset:18432\n"
"ds_read_b64_tr_b16 v[218:219], v230 offset:26624\n"
"v_mfma_f32_16x16x32_bf16 v[172:175], v[208:211], a[240:243], v[172:175]\n"
"buffer_load_dwordx4 v240, s[28:31], s40 offen offset:2048 lds\n"
"v_mfma_f32_16x16x32_bf16 v[172:175], v[192:195], a[204:207], v[172:175]\n"
"ds_read_b64_tr_b16 v[226:227], v233 offset:26624\n"
"ds_read_b64_tr_b16 a[232:233], v236 offset:10240\n"
"v_mfma_f32_16x16x32_bf16 v[172:175], v[188:191], a[200:203], v[172:175]\n"
"s_waitcnt lgkmcnt(6)\n"
"v_xor_b32 v235, 0x1000, v231\n"
"v_mul_f32_dpp v17, v246, v17 row_newbcast:1 row_mask:0xf bank_mask:0xf\n"
"v_mfma_f32_16x16x32_bf16 v[172:175], v[184:187], a[196:199], v[172:175]\n"
"s_waitcnt lgkmcnt(4)\n"
"s_add_u32 s40, s41, s54\n"
"ds_read_b64_tr_b16 v[222:223], v234 offset:26624\n"
"v_mul_f32_dpp v18, v246, v18 row_newbcast:2 row_mask:0xf bank_mask:0xf\n"
"v_mfma_f32_16x16x32_bf16 v[172:175], v[180:183], a[192:195], v[172:175]\n"
"buffer_load_dwordx4 v240, s[28:31], s40 offen offset:3072 lds\n"
"v_mfma_f32_16x16x32_bf16 v[128:131], v[96:99], a[96:99], v[196:199]\n"
"v_sub_f32 v252, v254, v252\n"
"v_mul_f32_dpp v16, v246, v16 row_newbcast:0 row_mask:0xf bank_mask:0xf\n"
"v_mfma_f32_16x16x32_bf16 v[128:131], v[100:103], a[100:103], v[128:131]\n"
"ds_read_b64_tr_b16 v[190:191], v230 offset:10240\n"
"v_mul_f32_dpp v86, v206, v86 row_newbcast:2 row_mask:0xf bank_mask:0xf\n"
"v_mfma_f32_16x16x32_bf16 v[128:131], v[104:107], a[104:107], v[128:131]\n"
"v_exp_f32 v252, v252\n"
"v_mfma_f32_16x16x32_bf16 v[128:131], v[108:111], a[108:111], v[128:131]\n"
"s_waitcnt lgkmcnt(4)\n"
"v_exp_f32 v250, v250\n"
"v_mfma_f32_16x16x32_bf16 v[128:131], v[112:115], a[144:147], v[128:131]\n"
"s_add_u32 s21, s60, 32\n"
"ds_read_b64_tr_b16 a[220:221], v237 offset:8192\n"
"v_mul_f32 v253, v253, v252\n"
"v_mfma_f32_16x16x32_bf16 v[128:131], v[116:119], a[148:151], v[128:131]\n"
"s_mov_b32 s69, 1\n"
"ds_read_b64_tr_b16 v[186:187], v234 offset:2048\n"
"v_fmac_f32 v253, v251, v250\n"
"v_mfma_f32_16x16x32_bf16 v[128:131], v[120:123], a[152:155], v[128:131]\n"
"s_waitcnt lgkmcnt(3)\n"
"v_bfe_u32 v201, v242, 2, 7\n"
"v_add_u32 v201, 0x100, v201\n"
"ds_read_b64_tr_b16 v[184:185], v234 offset:0\n"
"v_mfma_f32_16x16x32_bf16 v[128:131], v[224:227], a[156:159], v[128:131]\n"
"global_load_dword v200, v201, s[6:7] sc1\n"
"v_mfma_f32_16x16x32_bf16 v[144:147], v[224:227], a[172:175], v[196:199]\n"
"v_rcp_f32 v253, v253\n"
"v_mfma_f32_16x16x32_bf16 v[144:147], v[120:123], a[168:171], v[144:147]\n"
"v_mul_f32 v252, v252, v253\n"
"v_accvgpr_read_b32 v254, a208\n"
"v_mfma_f32_16x16x32_bf16 v[144:147], v[116:119], a[164:167], v[144:147]\n"
"ds_read_b64_tr_b16 v[188:189], v230 offset:8192\n"
"v_mul_f32 v250, v250, v253\n"
"v_mfma_f32_16x16x32_bf16 v[144:147], v[112:115], a[160:163], v[144:147]\n"
"s_lshl_b32 s20, s95, 9\n"
"ds_read_b64_tr_b16 v[194:195], v234 offset:10240\n"
"v_mov_b32_dpp v250, v250 row_ror:12 row_mask:0x2 bank_mask:0xf\n"
"v_mfma_f32_16x16x32_bf16 v[144:147], v[108:111], a[124:127], v[144:147]\n"
"s_lshl_b32 s19, s61, 7\n"
"v_mov_b32_dpp v250, v250 row_ror:8 row_mask:0x4 bank_mask:0xf\n"
"v_accvgpr_read_b32 v253, a210\n"
"v_mfma_f32_16x16x32_bf16 v[144:147], v[104:107], a[120:123], v[144:147]\n"
"s_lshl_b32 s91, s19, 9\n"
"v_mov_b32_dpp v252, v252 row_ror:12 row_mask:0x2 bank_mask:0xf\n"
"v_mov_b32_dpp v250, v250 row_ror:4 row_mask:0x8 bank_mask:0xf\n"
"v_mfma_f32_16x16x32_bf16 v[144:147], v[100:103], a[116:119], v[144:147]\n"
"ds_read_b64_tr_b16 v[214:215], v234 offset:18432\n"
"v_mul_f32_dpp v63, v250, v63 row_newbcast:3 row_mask:0xf bank_mask:0xf\n"
"v_mfma_f32_16x16x32_bf16 v[144:147], v[96:99], a[112:115], v[144:147]\n"
"ds_read_b64_tr_b16 v[208:209], v230 offset:16384\n"
"v_mul_f32_dpp v83, v206, v83 row_newbcast:3 row_mask:0xf bank_mask:0xf\n"
"v_mfma_f32_16x16x32_bf16 v[160:163], v[96:99], a[128:131], v[196:199]\n"
"ds_read_b64_tr_b16 v[192:193], v234 offset:8192\n"
"ds_read_b64_tr_b16 v[212:213], v234 offset:16384\n"
"v_mfma_f32_16x16x32_bf16 v[160:163], v[100:103], a[132:135], v[160:163]\n"
"v_accvgpr_read_b32 v251, a211\n"
"v_mul_f32_dpp v57, v250, v57 row_newbcast:1 row_mask:0xf bank_mask:0xf\n"
"v_mfma_f32_16x16x32_bf16 v[160:163], v[104:107], a[136:139], v[160:163]\n"
"ds_read_b64_tr_b16 v[182:183], v230 offset:2048\n"
"v_mul_f32_dpp v58, v250, v58 row_newbcast:2 row_mask:0xf bank_mask:0xf\n"
"v_mfma_f32_16x16x32_bf16 v[160:163], v[108:111], a[140:143], v[160:163]\n"
"ds_read_b64_tr_b16 v[180:181], v230 offset:0\n"
"v_mul_f32_dpp v31, v250, v31 row_newbcast:3 row_mask:0xf bank_mask:0xf\n"
"v_mfma_f32_16x16x32_bf16 v[160:163], v[112:115], a[176:179], v[160:163]\n"
"ds_read_b64_tr_b16 v[210:211], v230 offset:18432\n"
"v_mov_b32_dpp v252, v252 row_ror:8 row_mask:0x4 bank_mask:0xf\n"
"v_mfma_f32_16x16x32_bf16 v[160:163], v[116:119], a[180:183], v[160:163]\n"
"ds_read_b64_tr_b16 v[220:221], v234 offset:24576\n"
"v_mul_f32_dpp v26, v250, v26 row_newbcast:2 row_mask:0xf bank_mask:0xf\n"
"v_mfma_f32_16x16x32_bf16 v[160:163], v[120:123], a[184:187], v[160:163]\n"
"ds_read_b64_tr_b16 a[238:239], v237 offset:10248\n"
"v_mul_f32_dpp v30, v250, v30 row_newbcast:2 row_mask:0xf bank_mask:0xf\n"
"v_mfma_f32_16x16x32_bf16 v[160:163], v[224:227], a[188:191], v[160:163]\n"
"ds_read_b64_tr_b16 v[216:217], v230 offset:24576\n"
"v_mul_f32_dpp v28, v250, v28 row_newbcast:0 row_mask:0xf bank_mask:0xf\n"
"v_mfma_f32_16x16x32_bf16 v[176:179], v[224:227], a[252:255], v[196:199]\n"
"v_mov_b32_dpp v252, v252 row_ror:4 row_mask:0x8 bank_mask:0xf\n"
"v_mul_f32_dpp v61, v250, v61 row_newbcast:1 row_mask:0xf bank_mask:0xf\n"
"v_mfma_f32_16x16x32_bf16 v[176:179], v[120:123], a[248:251], v[176:179]\n"
"ds_read_b64_tr_b16 v[226:227], v235 offset:26624\n"
"v_mul_f32_dpp v59, v250, v59 row_newbcast:3 row_mask:0xf bank_mask:0xf\n"
"v_mfma_f32_16x16x32_bf16 v[176:179], v[116:119], a[244:247], v[176:179]\n"
"v_mul_f32_dpp v25, v250, v25 row_newbcast:1 row_mask:0xf bank_mask:0xf\n"
"v_mul_f32_dpp v29, v250, v29 row_newbcast:1 row_mask:0xf bank_mask:0xf\n"
"v_mfma_f32_16x16x32_bf16 v[176:179], v[112:115], a[240:243], v[176:179]\n"
"ds_read_b64_tr_b16 v[114:115], v231 offset:18432\n"
"v_mul_f32_dpp v19, v246, v19 row_newbcast:3 row_mask:0xf bank_mask:0xf\n"
"v_mfma_f32_16x16x32_bf16 v[176:179], v[108:111], a[204:207], v[176:179]\n"
"ds_read_b64_tr_b16 a[236:237], v237 offset:10240\n"
"v_mul_f32_dpp v27, v250, v27 row_newbcast:3 row_mask:0xf bank_mask:0xf\n"
"v_mfma_f32_16x16x32_bf16 v[176:179], v[104:107], a[200:203], v[176:179]\n"
"v_mul_f32_dpp v24, v250, v24 row_newbcast:0 row_mask:0xf bank_mask:0xf\n"
"v_fmac_f32_dpp v25, v252, v255 row_newbcast:1 row_mask:0xf bank_mask:0xf\n"
"v_mfma_f32_16x16x32_bf16 v[176:179], v[100:103], a[196:199], v[176:179]\n"
"ds_read_b64_tr_b16 a[234:235], v236 offset:10248\n"
"v_fmac_f32_dpp v27, v252, v251 row_newbcast:3 row_mask:0xf bank_mask:0xf\n"
"v_mfma_f32_16x16x32_bf16 v[176:179], v[96:99], a[192:195], v[176:179]\n"
"s_waitcnt lgkmcnt(4)\n"
"ds_read_b64_tr_b16 v[104:105], v231 offset:8192\n"
"ds_read_b64_tr_b16 v[106:107], v231 offset:10240\n"
"v_accvgpr_read_b32 v251, a215\n"
"v_mul_f32_dpp v55, v247, v55 row_newbcast:3 row_mask:0xf bank_mask:0xf\n"
"v_mul_f32_dpp v56, v250, v56 row_newbcast:0 row_mask:0xf bank_mask:0xf\n"
"v_mul_f32_dpp v62, v250, v62 row_newbcast:2 row_mask:0xf bank_mask:0xf\n"
"v_mfma_f32_16x16x32_bf16 v[132:135], v[180:183], a[96:99], v[196:199]\n"
"v_accvgpr_read_b32 v255, a213\n"
"v_fmac_f32_dpp v31, v252, v251 row_newbcast:3 row_mask:0xf bank_mask:0xf\n"
"v_mfma_f32_16x16x32_bf16 v[132:135], v[184:187], a[100:103], v[132:135]\n"
"ds_read_b64_tr_b16 v[108:109], v235 offset:8192\n"
"ds_read_b64_tr_b16 v[116:117], v235 offset:16384\n"
"v_mfma_f32_16x16x32_bf16 v[132:135], v[188:191], a[104:107], v[132:135]\n"
"ds_read_b64_tr_b16 v[102:103], v235 offset:2048\n"
"v_fmac_f32_dpp v26, v252, v253 row_newbcast:2 row_mask:0xf bank_mask:0xf\n"
"v_mfma_f32_16x16x32_bf16 v[132:135], v[192:195], a[108:111], v[132:135]\n"
"v_accvgpr_read_b32 v253, a214\n"
"v_mul_f32_dpp v60, v250, v60 row_newbcast:0 row_mask:0xf bank_mask:0xf\n"
"v_mfma_f32_16x16x32_bf16 v[132:135], v[208:211], a[144:147], v[132:135]\n"
"s_waitcnt vmcnt(0)\n"
"ds_read_b64_tr_b16 v[96:97], v231 offset:0\n"
"v_fmac_f32_dpp v24, v252, v254 row_newbcast:0 row_mask:0xf bank_mask:0xf\n"
"v_mfma_f32_16x16x32_bf16 v[132:135], v[212:215], a[148:151], v[132:135]\n"
"v_subrev_u32 v200, s21, v200\n"
"v_accvgpr_read_b32 v254, a212\n"
"v_mfma_f32_16x16x32_bf16 v[132:135], v[216:219], a[152:155], v[132:135]\n"
"v_cmp_lt_u32 vcc, s69, v200\n"
"v_accvgpr_read_b32 v251, a227\n"
"v_mfma_f32_16x16x32_bf16 v[132:135], v[220:223], a[156:159], v[132:135]\n"
"ds_read_b64_tr_b16 a[216:217], v236 offset:8192\n"
"ds_read_b64_tr_b16 v[118:119], v235 offset:18432\n"
"v_fmac_f32_dpp v28, v252, v254 row_newbcast:0 row_mask:0xf bank_mask:0xf\n"
"v_fmac_f32_dpp v29, v252, v255 row_newbcast:1 row_mask:0xf bank_mask:0xf\n"
"v_fmac_f32_dpp v30, v252, v253 row_newbcast:2 row_mask:0xf bank_mask:0xf\n"
"v_cvt_pk_bf16_f32 v24, v24, v25\n"
"v_cvt_pk_bf16_f32 v25, v26, v27\n"
"v_cvt_pk_bf16_f32 v26, v28, v29\n"
"v_cvt_pk_bf16_f32 v27, v30, v31\n"
"v_accvgpr_read_b32 v255, a225\n"
"v_accvgpr_read_b32 v253, a226\n"
"v_fmac_f32_dpp v59, v252, v251 row_newbcast:3 row_mask:0xf bank_mask:0xf\n"
"s_cbranch_vccnz LGV_%=\nLGVK_%=:\n"
"v_mfma_f32_16x16x32_bf16 v[148:151], v[220:223], a[172:175], v[196:199]\n"
"v_accvgpr_read_b32 v254, a224\n"
"v_fmac_f32_dpp v58, v252, v253 row_newbcast:2 row_mask:0xf bank_mask:0xf\n"
"v_mfma_f32_16x16x32_bf16 v[148:151], v[216:219], a[168:171], v[148:151]\n"
"v_add_u32_dpp v200, v200, v200 quad_perm:[1,0,3,2] row_mask:0xf bank_mask:0xf\n"
"v_accvgpr_read_b32 v253, a230\n"
"v_mfma_f32_16x16x32_bf16 v[148:151], v[212:215], a[164:167], v[148:151]\n"
"v_fmac_f32_dpp v56, v252, v254 row_newbcast:0 row_mask:0xf bank_mask:0xf\n"
"v_accvgpr_read_b32 v251, a231\n"
"v_mfma_f32_16x16x32_bf16 v[148:151], v[208:211], a[160:163], v[148:151]\n"
"v_add_u32_dpp v200, v200, v200 quad_perm:[2,3,0,1] row_mask:0xf bank_mask:0xf\n"
"ds_write_b128 v243, v[24:27] offset:0\n"
"v_fmac_f32_dpp v57, v252, v255 row_newbcast:1 row_mask:0xf bank_mask:0xf\n"
"v_accvgpr_read_b32 v254, a228\n"
"s_nop 1\n"
"v_fmac_f32_dpp v60, v252, v254 row_newbcast:0 row_mask:0xf bank_mask:0xf\n"
"v_fmac_f32_dpp v62, v252, v253 row_newbcast:2 row_mask:0xf bank_mask:0xf\n"
"v_fmac_f32_dpp v63, v252, v251 row_newbcast:3 row_mask:0xf bank_mask:0xf\n"
"v_cvt_pk_bf16_f32 v56, v56, v57\n"
"v_cvt_pk_bf16_f32 v57, v58, v59\n"
"v_cvt_pk_bf16_f32 v59, v62, v63\n"
"v_mfma_f32_16x16x32_bf16 v[148:151], v[192:195], a[124:127], v[148:151]\n"
"ds_read_b64_tr_b16 v[100:101], v235 offset:0\n"
"v_accvgpr_read_b32 v255, a229\n"
"v_mfma_f32_16x16x32_bf16 v[148:151], v[188:191], a[120:123], v[148:151]\n"
"v_cmp_lt_u32 vcc, 1, v200\n"
"v_fmac_f32_dpp v61, v252, v255 row_newbcast:1 row_mask:0xf bank_mask:0xf\n"
"v_mfma_f32_16x16x32_bf16 v[148:151], v[184:187], a[116:119], v[148:151]\n"
"ds_read_b64_tr_b16 v[98:99], v231 offset:2048\n"
"v_cvt_pk_bf16_f32 v58, v60, v61\n"
"v_mfma_f32_16x16x32_bf16 v[148:151], v[180:183], a[112:115], v[148:151]\n"
"ds_write_b128 v243, v[56:59] offset:2048\n"
"v_mul_f32_dpp v22, v246, v22 row_newbcast:2 row_mask:0xf bank_mask:0xf\n"
"v_mul_f32_dpp v48, v247, v48 row_newbcast:0 row_mask:0xf bank_mask:0xf\n"
"v_mul_f32_dpp v49, v247, v49 row_newbcast:1 row_mask:0xf bank_mask:0xf\n"
"v_mul_f32_dpp v87, v206, v87 row_newbcast:3 row_mask:0xf bank_mask:0xf\n"
"v_mfma_f32_16x16x32_bf16 v[164:167], v[180:183], a[128:131], v[196:199]\n"
"s_mov_b32 s47, 0\n"
"ds_read_b64_tr_b16 v[110:111], v235 offset:10240\n"
"v_bfe_u32 v254, v242, 4, 4\n"
"v_mfma_f32_16x16x32_bf16 v[164:167], v[184:187], a[132:135], v[164:167]\n"
"s_mov_b32 s89, vcc_lo\n"
"v_cvt_pk_bf16_f32 v16, v16, v17\n"
"v_mul_f32_dpp v81, v206, v81 row_newbcast:1 row_mask:0xf bank_mask:0xf\n"
"v_mfma_f32_16x16x32_bf16 v[164:167], v[188:191], a[136:139], v[164:167]\n"
"s_lshr_b32 s90, s89, s93\n"
"ds_read_b64_tr_b16 v[112:113], v231 offset:16384\n"
"v_xor_b32 v249, s75, v249\n"
"v_mfma_f32_16x16x32_bf16 v[164:167], v[192:195], a[140:143], v[164:167]\n"
"s_and_b32 s90, s90, 1\n"
"ds_read_b64_tr_b16 v[122:123], v231 offset:26624\n"
"v_mul_f32_dpp v51, v247, v51 row_newbcast:3 row_mask:0xf bank_mask:0xf\n"
"v_mfma_f32_16x16x32_bf16 v[164:167], v[208:211], a[176:179], v[164:167]\n"
"s_lshl_b32 s16, s61, 7\n"
"v_cvt_pk_bf16_f32 v17, v18, v19\n"
"v_cvt_pk_bf16_f32 v19, v22, v23\n"
"v_mfma_f32_16x16x32_bf16 v[164:167], v[212:215], a[180:183], v[164:167]\n"
"s_add_u32 s90, s90, s16\n"
"ds_read_b64_tr_b16 v[120:121], v231 offset:24576\n"
"ds_read_b64_tr_b16 v[224:225], v235 offset:24576\n"
"v_mfma_f32_16x16x32_bf16 v[164:167], v[216:219], a[184:187], v[164:167]\n"
"s_waitcnt vmcnt(63) lgkmcnt(5)\n"
"s_lshl_b32 s90, s90, 13\n"
"v_mul_f32_dpp v20, v246, v20 row_newbcast:0 row_mask:0xf bank_mask:0xf\n"
"v_mfma_f32_16x16x32_bf16 v[164:167], v[220:223], a[188:191], v[164:167]\n"
"s_barrier\n"
"s_add_u32 s16, s20, s90\n"
"v_mul_f32_dpp v50, v247, v50 row_newbcast:2 row_mask:0xf bank_mask:0xf\n"
"v_mul_f32_dpp v52, v247, v52 row_newbcast:0 row_mask:0xf bank_mask:0xf\n"
"v_cvt_pk_bf16_f32 v48, v48, v49\n"
"v_cvt_pk_bf16_f32 v49, v50, v51\n"
"v_mul_f32_dpp v80, v206, v80 row_newbcast:0 row_mask:0xf bank_mask:0xf\n"
"v_mfma_f32_16x16x32_bf16 v[136:139], v[96:99], a[96:99], v[196:199]\n"
"s_add_u32 s17, s16, 4096\n"
"ds_read_b64_tr_b16 v[180:181], v228 offset:32768\n"
"v_mul_f32_dpp v84, v206, v84 row_newbcast:0 row_mask:0xf bank_mask:0xf\n"
"v_mfma_f32_16x16x32_bf16 v[136:139], v[100:103], a[100:103], v[136:139]\n"
"s_mov_b32 s72, s61\n"
"ds_read_b64_tr_b16 v[182:183], v228 offset:34816\n"
"v_mul_f32_dpp v21, v246, v21 row_newbcast:1 row_mask:0xf bank_mask:0xf\n"
"v_mfma_f32_16x16x32_bf16 v[136:139], v[104:107], a[104:107], v[136:139]\n"
"s_waitcnt lgkmcnt(5)\n"
"s_mov_b32 s21, s60\n"
"ds_read_b64_tr_b16 a[208:209], v236 offset:12288\n"
"v_mul_f32_dpp v82, v206, v82 row_newbcast:2 row_mask:0xf bank_mask:0xf\n"
"v_mfma_f32_16x16x32_bf16 v[136:139], v[108:111], a[108:111], v[136:139]\n"
"s_mov_b32 s18, s77\n"
"v_cvt_pk_bf16_f32 v80, v80, v81\n"
"v_cvt_pk_bf16_f32 v81, v82, v83\n"
"v_mfma_f32_16x16x32_bf16 v[136:139], v[112:115], a[144:147], v[136:139]\n"
"s_mov_b32 s19, s77\n"
"v_mul_f32_dpp v85, v206, v85 row_newbcast:1 row_mask:0xf bank_mask:0xf\n"
"v_cvt_pk_bf16_f32 v83, v86, v87\n"
"v_mfma_f32_16x16x32_bf16 v[136:139], v[116:119], a[148:151], v[136:139]\n"
"s_waitcnt lgkmcnt(3)\n"
"s_mov_b32 s69, -1\n"
"ds_read_b64_tr_b16 v[218:219], v228 offset:59392\n"
"v_cvt_pk_bf16_f32 v18, v20, v21\n"
"v_mfma_f32_16x16x32_bf16 v[136:139], v[120:123], a[152:155], v[136:139]\n"
"s_mul_i32 s88, s61, 0x10011111\n"
"v_xor_b32 v248, s75, v248\n"
"v_cvt_pk_bf16_f32 v82, v84, v85\n"
"v_mfma_f32_16x16x32_bf16 v[136:139], v[224:227], a[156:159], v[136:139]\n"
"s_add_u32 s88, s88, 484675\n"
"ds_write_b128 v248, v[16:19] offset:49152\n"
"v_mul_f32_dpp v53, v247, v53 row_newbcast:1 row_mask:0xf bank_mask:0xf\n"
"v_mul_f32_dpp v54, v247, v54 row_newbcast:2 row_mask:0xf bank_mask:0xf\n"
"v_cvt_pk_bf16_f32 v50, v52, v53\n"
"v_cvt_pk_bf16_f32 v51, v54, v55\n"
"v_mfma_f32_16x16x32_bf16 v[152:155], v[224:227], a[172:175], v[196:199]\n"
"s_and_b32 s88, s88, 0x70077777\n"
"ds_read_b64_tr_b16 a[8:9], v249 offset:49152\n"
"ds_read_b64_tr_b16 a[10:11], v249 offset:49160\n"
"v_mfma_f32_16x16x32_bf16 v[152:155], v[120:123], a[168:171], v[152:155]\n"
"s_or_b32 s88, s88, 0x9800000\n"
"ds_write_b128 v248, v[48:51] offset:50176\n"
"ds_write_b128 v248, v[80:83] offset:51200\n"
"v_mfma_f32_16x16x32_bf16 v[152:155], v[116:119], a[164:167], v[152:155]\n"
"s_mov_b32 s84, s6\n"
"ds_read_b64_tr_b16 a[24:25], v249 offset:50176\n"
"ds_read_b64_tr_b16 a[26:27], v249 offset:50184\n"
"ds_read_b64_tr_b16 a[40:41], v249 offset:51200\n"
"ds_read_b64_tr_b16 a[42:43], v249 offset:51208\n"
"v_mfma_f32_16x16x32_bf16 v[152:155], v[112:115], a[160:163], v[152:155]\n"
"s_and_b32 s85, s7, 0xffff\n"
"ds_read_b64_tr_b16 v[188:189], v228 offset:40960\n"
"v_xor_b32 v248, s75, v248\n"
"v_mfma_f32_16x16x32_bf16 v[152:155], v[108:111], a[124:127], v[152:155]\n"
"s_mov_b32 s86, 128\n"
"ds_read_b64_tr_b16 v[184:185], v232 offset:32768\n"
"ds_read_b64_tr_b16 v[214:215], v232 offset:51200\n"
"v_mfma_f32_16x16x32_bf16 v[152:155], v[104:107], a[120:123], v[152:155]\n"
"s_mov_b32 s87, 131072\n"
"ds_read_b64_tr_b16 a[214:215], v237 offset:12296\n"
"ds_read_b64_tr_b16 v[190:191], v228 offset:43008\n"
"v_mfma_f32_16x16x32_bf16 v[152:155], v[100:103], a[116:119], v[152:155]\n"
"ds_read_b64_tr_b16 a[224:225], v236 offset:14336\n"
"ds_read_b64_tr_b16 v[212:213], v232 offset:49152\n"
"v_mfma_f32_16x16x32_bf16 v[152:155], v[96:99], a[112:115], v[152:155]\n"
"ds_read_b64_tr_b16 v[186:187], v232 offset:34816\n"
"v_mov_b64 v[84:85], 0\n"
"v_mfma_f32_16x16x32_bf16 v[168:171], v[96:99], a[128:131], v[196:199]\n"
"ds_read_b64_tr_b16 v[192:193], v232 offset:40960\n"
"ds_read_b64_tr_b16 v[222:223], v232 offset:59392\n"
"v_mfma_f32_16x16x32_bf16 v[168:171], v[100:103], a[132:135], v[168:171]\n"
"ds_read_b64_tr_b16 v[194:195], v232 offset:43008\n"
"v_bfe_u32 v255, v242, 8, 1\n"
"v_mfma_f32_16x16x32_bf16 v[168:171], v[104:107], a[136:139], v[168:171]\n"
"ds_read_b64_tr_b16 v[208:209], v228 offset:49152\n"
"ds_read_b64_tr_b16 v[210:211], v228 offset:51200\n"
"v_mfma_f32_16x16x32_bf16 v[168:171], v[108:111], a[140:143], v[168:171]\n"
"s_waitcnt lgkmcnt(12)\n"
"ds_read_b64_tr_b16 a[230:231], v237 offset:14344\n"
"ds_read_b64_tr_b16 v[216:217], v228 offset:57344\n"
"v_mfma_f32_16x16x32_bf16 v[168:171], v[112:115], a[176:179], v[168:171]\n"
"ds_read_b64_tr_b16 v[220:221], v232 offset:57344\n"
"v_xor_b32 v249, s75, v249\n"
"v_mfma_f32_16x16x32_bf16 v[168:171], v[116:119], a[180:183], v[168:171]\n"
"ds_read_b64_tr_b16 a[222:223], v237 offset:8200\n"
"ds_read_b64_tr_b16 a[226:227], v236 offset:14344\n"
"v_mfma_f32_16x16x32_bf16 v[168:171], v[120:123], a[184:187], v[168:171]\n"
"s_add_u32 s41, s70, 327680\n"
"s_add_u32 m0, s71, 32768\n"
"ds_read_b64_tr_b16 v[110:111], v233 offset:43008\n"
"v_mfma_f32_16x16x32_bf16 v[168:171], v[224:227], a[188:191], v[168:171]\n"
"buffer_load_dwordx4 v240, s[28:31], s41 offen lds\n"
"v_mfma_f32_16x16x32_bf16 v[124:127], v[180:183], a[48:51], v[124:127]\n"
"s_waitcnt lgkmcnt(9)\n"
"v_mul_u32_u24 v254, 2560, v254\n"
"v_mfma_f32_16x16x32_bf16 v[124:127], v[184:187], a[52:55], v[124:127]\n"
"v_mov_b64 v[32:33], s[22:23]\n"
"v_mov_b64 v[44:45], s[22:23]\n"
"v_mfma_f32_16x16x32_bf16 v[124:127], v[188:191], a[56:59], v[124:127]\n"
"s_waitcnt lgkmcnt(6)\n"
"s_add_u32 s40, s41, s52\n"
"ds_read_b64_tr_b16 a[212:213], v237 offset:12288\n"
"ds_read_b64_tr_b16 v[102:103], v233 offset:34816\n"
"v_mfma_f32_16x16x32_bf16 v[124:127], v[192:195], a[60:63], v[124:127]\n"
"buffer_load_dwordx4 v240, s[28:31], s40 offen offset:1024 lds\n"
"v_mfma_f32_16x16x32_bf16 v[124:127], v[208:211], a[0:3], v[124:127]\n"
"v_mov_b64 v[38:39], s[22:23]\n"
"v_mfma_f32_16x16x32_bf16 v[124:127], v[212:215], a[4:7], v[124:127]\n"
"s_waitcnt lgkmcnt(3)\n"
"v_mov_b64 v[244:245], 0\n"
"v_mfma_f32_16x16x32_bf16 v[124:127], v[216:219], a[8:11], v[124:127]\n"
"ds_read_b64_tr_b16 v[98:99], v229 offset:34816\n"
"ds_read_b64_tr_b16 v[116:117], v233 offset:49152\n"
"v_mfma_f32_16x16x32_bf16 v[124:127], v[220:223], a[12:15], v[124:127]\n"
"s_add_u32 s40, s41, s53\n"
"ds_read_b64_tr_b16 v[122:123], v229 offset:59392\n"
"ds_read_b64_tr_b16 v[224:225], v233 offset:57344\n"
"v_mfma_f32_16x16x32_bf16 v[140:143], v[220:223], a[28:31], v[140:143]\n"
"buffer_load_dwordx4 v240, s[28:31], s40 offen offset:2048 lds\n"
"v_mfma_f32_16x16x32_bf16 v[140:143], v[216:219], a[24:27], v[140:143]\n"
"v_mov_b64 v[16:17], s[22:23]\n"
"v_mov_b64 v[36:37], s[22:23]\n"
"v_mfma_f32_16x16x32_bf16 v[140:143], v[212:215], a[20:23], v[140:143]\n"
"v_mov_b64 v[246:247], 0\n"
"v_subrev_f32 v124, s22, v124\n"
"v_mfma_f32_16x16x32_bf16 v[140:143], v[208:211], a[16:19], v[140:143]\n"
"ds_read_b64_tr_b16 a[210:211], v236 offset:12296\n"
"ds_read_b64_tr_b16 v[106:107], v229 offset:43008\n"
"v_mfma_f32_16x16x32_bf16 v[140:143], v[192:195], a[76:79], v[140:143]\n"
"ds_read_b64_tr_b16 v[112:113], v229 offset:49152\n"
"v_xor_b32 v228, s75, v228\n"
"v_mfma_f32_16x16x32_bf16 v[140:143], v[188:191], a[72:75], v[140:143]\n"
"ds_read_b64_tr_b16 v[104:105], v229 offset:40960\n"
"v_mov_b64 v[26:27], s[22:23]\n"
"v_mfma_f32_16x16x32_bf16 v[140:143], v[184:187], a[68:71], v[140:143]\n"
"s_add_u32 s40, s41, s54\n"
"ds_read_b64_tr_b16 a[228:229], v237 offset:14336\n"
"ds_read_b64_tr_b16 v[118:119], v233 offset:51200\n"
"v_mfma_f32_16x16x32_bf16 v[140:143], v[180:183], a[64:67], v[140:143]\n"
"buffer_load_dwordx4 v240, s[28:31], s40 offen offset:3072 lds\n"
"v_mfma_f32_16x16x32_bf16 v[156:159], v[180:183], a[80:83], v[156:159]\n"
"s_add_u32 s41, s70, 491520\n"
"v_mov_b64 v[86:87], 0\n"
"v_mfma_f32_16x16x32_bf16 v[156:159], v[184:187], a[84:87], v[156:159]\n"
"s_add_u32 m0, s71, 49152\n"
"ds_read_b64_tr_b16 v[114:115], v229 offset:51200\n"
"ds_read_b64_tr_b16 v[226:227], v233 offset:59392\n"
"v_mfma_f32_16x16x32_bf16 v[156:159], v[188:191], a[88:91], v[156:159]\n"
"buffer_load_dwordx4 v240, s[28:31], s41 offen lds\n"
"v_mfma_f32_16x16x32_bf16 v[156:159], v[192:195], a[92:95], v[156:159]\n"
"v_and_b32 v243, 0x3f0, v242\n"
"v_subrev_f32 v142, s22, v142\n"
"v_mfma_f32_16x16x32_bf16 v[156:159], v[208:211], a[32:35], v[156:159]\n"
"v_mov_b64 v[6:7], s[22:23]\n"
"v_subrev_f32 v126, s22, v126\n"
"v_mfma_f32_16x16x32_bf16 v[156:159], v[212:215], a[36:39], v[156:159]\n"
"ds_read_b64_tr_b16 a[218:219], v236 offset:8200\n"
"ds_read_b64_tr_b16 v[96:97], v229 offset:32768\n"
"v_mfma_f32_16x16x32_bf16 v[156:159], v[216:219], a[40:43], v[156:159]\n"
"s_add_u32 s40, s41, s52\n"
"ds_read_b64_tr_b16 v[100:101], v233 offset:32768\n"
"ds_read_b64_tr_b16 v[108:109], v233 offset:40960\n"
"v_mfma_f32_16x16x32_bf16 v[156:159], v[220:223], a[44:47], v[156:159]\n"
"s_waitcnt lgkmcnt(6)\n"
"buffer_load_dwordx4 v240, s[28:31], s40 offen offset:1024 lds\n"
"v_mfma_f32_16x16x32_bf16 v[172:175], v[220:223], a[228:231], v[172:175]\n"
"v_subrev_f32 v143, s22, v143\n"
"v_xor_b32 v233, s75, v233\n"
"v_mfma_f32_16x16x32_bf16 v[172:175], v[216:219], a[224:227], v[172:175]\n"
"v_lshl_add_u32 v254, v255, 5, v254\n"
"v_subrev_f32 v125, s22, v125\n"
"v_mfma_f32_16x16x32_bf16 v[172:175], v[212:215], a[212:215], v[172:175]\n"
"s_add_u32 s40, s41, s53\n"
"ds_read_b64_tr_b16 v[120:121], v229 offset:57344\n"
"v_mfma_f32_16x16x32_bf16 v[172:175], v[208:211], a[208:211], v[172:175]\n"
"buffer_load_dwordx4 v240, s[28:31], s40 offen offset:2048 lds\n"
"v_mfma_f32_16x16x32_bf16 v[172:175], v[192:195], a[236:239], v[172:175]\n"
"v_add_u32 v236, s94, v243\n"
"v_mfma_f32_16x16x32_bf16 v[172:175], v[188:191], a[232:235], v[172:175]\n"
"v_bfe_u32 v255, v242, 9, 1\n"
"v_subrev_f32 v141, s22, v141\n"
"v_mfma_f32_16x16x32_bf16 v[172:175], v[184:187], a[220:223], v[172:175]\n"
"s_waitcnt lgkmcnt(1)\n"
"ds_read_b64_tr_b16 v[184:185], v234 offset:32768\n"
"v_mov_b64 v[54:55], s[22:23]\n"
"v_mfma_f32_16x16x32_bf16 v[172:175], v[180:183], a[216:219], v[172:175]\n"
"s_add_u32 s40, s41, s54\n"
"ds_read_b64_tr_b16 v[210:211], v230 offset:51200\n"
"v_mfma_f32_16x16x32_bf16 v[128:131], v[96:99], a[48:51], v[128:131]\n"
"buffer_load_dwordx4 v240, s[28:31], s40 offen offset:3072 lds\n"
"v_mfma_f32_16x16x32_bf16 v[128:131], v[100:103], a[52:55], v[128:131]\n"
"s_lshl_b32 s62, s70, 9\n"
"v_mov_b64 v[30:31], s[22:23]\n"
"v_xor_b32 v232, s75, v232\n"
"v_mfma_f32_16x16x32_bf16 v[128:131], v[104:107], a[56:59], v[128:131]\n"
"s_and_b32 s70, s88, 15\n"
"v_cvt_pk_bf16_f32 v124, v124, v125\n"
"v_mfma_f32_16x16x32_bf16 v[128:131], v[108:111], a[60:63], v[128:131]\n"
"s_lshr_b32 s88, s88, 4\n"
"ds_read_b64_tr_b16 v[218:219], v230 offset:59392\n"
"v_subrev_f32 v127, s22, v127\n"
"v_mfma_f32_16x16x32_bf16 v[128:131], v[112:115], a[0:3], v[128:131]\n"
"s_lshl_b32 s70, s70, 7\n"
"ds_read_b64_tr_b16 v[194:195], v234 offset:43008\n"
"v_bfe_u32 v237, v242, 4, 3\n"
"v_mfma_f32_16x16x32_bf16 v[128:131], v[116:119], a[4:7], v[128:131]\n"
"s_waitcnt lgkmcnt(4)\n"
"s_xor_b32 s71, s71, s75\n"
"v_lshl_add_u32 v243, s44, 2, v243\n"
"v_mov_b64 v[12:13], s[22:23]\n"
"v_mfma_f32_16x16x32_bf16 v[128:131], v[120:123], a[8:11], v[128:131]\n"
"v_lshlrev_b32_e64 v201, 2, s72\n"
"ds_read_b64_tr_b16 v[214:215], v234 offset:51200\n"
"v_cvt_pk_bf16_f32 v125, v126, v127\n"
"v_mfma_f32_16x16x32_bf16 v[128:131], v[224:227], a[12:15], v[128:131]\n"
"global_load_dword v200, v201, s[6:7] sc1\n"
"v_mfma_f32_16x16x32_bf16 v[144:147], v[224:227], a[28:31], v[144:147]\n"
"v_subrev_f32 v157, s22, v157\n"
"v_subrev_f32 v174, s22, v174\n"
"v_mfma_f32_16x16x32_bf16 v[144:147], v[120:123], a[24:27], v[144:147]\n"
"v_mov_b64 v[52:53], s[22:23]\n"
"v_subrev_f32 v156, s22, v156\n"
"v_mfma_f32_16x16x32_bf16 v[144:147], v[116:119], a[20:23], v[144:147]\n"
"ds_read_b64_tr_b16 v[188:189], v230 offset:40960\n"
"v_subrev_f32 v172, s22, v172\n"
"v_mfma_f32_16x16x32_bf16 v[144:147], v[112:115], a[16:19], v[144:147]\n"
"v_subrev_f32 v140, s22, v140\n"
"v_mfma_f32_16x16x32_bf16 v[144:147], v[108:111], a[76:79], v[144:147]\n"
"ds_read_b64_tr_b16 v[180:181], v230 offset:32768\n"
"v_subrev_f32 v130, s22, v130\n"
"v_mfma_f32_16x16x32_bf16 v[144:147], v[104:107], a[72:75], v[144:147]\n"
"v_lshl_add_u32 v254, v255, 4, v254\n"
"v_mov_b64 v[22:23], s[22:23]\n"
"v_mfma_f32_16x16x32_bf16 v[144:147], v[100:103], a[68:71], v[144:147]\n"
"ds_read_b64_tr_b16 v[192:193], v234 offset:40960\n"
"v_subrev_f32 v128, s22, v128\n"
"v_mfma_f32_16x16x32_bf16 v[144:147], v[96:99], a[64:67], v[144:147]\n"
"v_subrev_f32 v129, s22, v129\n"
"v_cvt_pk_bf16_f32 v126, v128, v129\n"
"v_mfma_f32_16x16x32_bf16 v[160:163], v[96:99], a[80:83], v[160:163]\n"
"ds_read_b64_tr_b16 v[186:187], v234 offset:34816\n"
"v_cvt_pk_bf16_f32 v156, v156, v157\n"
"v_mfma_f32_16x16x32_bf16 v[160:163], v[100:103], a[84:87], v[160:163]\n"
"ds_read_b64_tr_b16 v[190:191], v230 offset:43008\n"
"v_subrev_f32 v131, s22, v131\n"
"v_mfma_f32_16x16x32_bf16 v[160:163], v[104:107], a[88:91], v[160:163]\n"
"v_cvt_pk_bf16_f32 v127, v130, v131\n"
"v_subrev_f32 v146, s22, v146\n"
"v_mfma_f32_16x16x32_bf16 v[160:163], v[108:111], a[92:95], v[160:163]\n"
"ds_read_b64_tr_b16 v[220:221], v234 offset:57344\n"
"v_cvt_pk_bf16_f32 v140, v140, v141\n"
"v_mfma_f32_16x16x32_bf16 v[160:163], v[112:115], a[32:35], v[160:163]\n"
"v_lshl_add_u32 v248, v242, 8, s93\n"
"v_subrev_f32 v145, s22, v145\n"
"v_mfma_f32_16x16x32_bf16 v[160:163], v[116:119], a[36:39], v[160:163]\n"
"ds_read_b64_tr_b16 v[182:183], v230 offset:34816\n"
"v_cvt_pk_bf16_f32 v141, v142, v143\n"
"v_mfma_f32_16x16x32_bf16 v[160:163], v[120:123], a[40:43], v[160:163]\n"
"ds_read_b64_tr_b16 v[208:209], v230 offset:49152\n"
"v_subrev_f32 v159, s22, v159\n"
"v_mfma_f32_16x16x32_bf16 v[160:163], v[224:227], a[44:47], v[160:163]\n"
"buffer_store_dwordx4 v[124:127], v243, s[32:35], s16 offen sc1\n"
"v_mov_b64 v[24:25], s[22:23]\n"
"v_mfma_f32_16x16x32_bf16 v[176:179], v[224:227], a[228:231], v[176:179]\n"
"ds_read_b64_tr_b16 v[212:213], v234 offset:49152\n"
"v_mov_b64 v[50:51], s[22:23]\n"
"v_mfma_f32_16x16x32_bf16 v[176:179], v[120:123], a[224:227], v[176:179]\n"
"v_subrev_f32 v158, s22, v158\n"
"v_subrev_f32 v173, s22, v173\n"
"v_mfma_f32_16x16x32_bf16 v[176:179], v[116:119], a[212:215], v[176:179]\n"
"ds_read_b64_tr_b16 v[216:217], v230 offset:57344\n"
"v_mov_b64 v[94:95], 0\n"
"v_mfma_f32_16x16x32_bf16 v[176:179], v[112:115], a[208:211], v[176:179]\n"
"v_cvt_pk_bf16_f32 v172, v172, v173\n"
"v_mfma_f32_16x16x32_bf16 v[176:179], v[108:111], a[236:239], v[176:179]\n"
"ds_read_b64_tr_b16 v[112:113], v231 offset:49152\n"
"v_mov_b64 v[4:5], s[22:23]\n"
"v_mfma_f32_16x16x32_bf16 v[176:179], v[104:107], a[232:235], v[176:179]\n"
"ds_read_b64_tr_b16 v[222:223], v234 offset:59392\n"
"v_subrev_f32 v144, s22, v144\n"
"v_mfma_f32_16x16x32_bf16 v[176:179], v[100:103], a[220:223], v[176:179]\n"
"v_mov_b64 v[40:41], s[22:23]\n"
"v_subrev_f32 v147, s22, v147\n"
"v_mfma_f32_16x16x32_bf16 v[176:179], v[96:99], a[216:219], v[176:179]\n"
"s_waitcnt lgkmcnt(4)\n"
"ds_read_b64_tr_b16 v[224:225], v235 offset:57344\n"
"v_subrev_f32 v175, s22, v175\n"
"v_mfma_f32_16x16x32_bf16 v[132:135], v[180:183], a[48:51], v[132:135]\n"
"ds_read_b64_tr_b16 v[102:103], v235 offset:34816\n"
"v_mov_b32 v241, s60\n"
"v_mfma_f32_16x16x32_bf16 v[132:135], v[184:187], a[52:55], v[132:135]\n"
"ds_read_b64_tr_b16 v[96:97], v231 offset:32768\n"
"v_cvt_pk_bf16_f32 v157, v158, v159\n"
"v_mfma_f32_16x16x32_bf16 v[132:135], v[188:191], a[56:59], v[132:135]\n"
"ds_read_b64_tr_b16 v[114:115], v231 offset:51200\n"
"v_subrev_f32 v160, s22, v160\n"
"v_mfma_f32_16x16x32_bf16 v[132:135], v[192:195], a[60:63], v[132:135]\n"
"v_cvt_pk_bf16_f32 v142, v144, v145\n"
"v_subrev_f32 v161, s22, v161\n"
"v_mfma_f32_16x16x32_bf16 v[132:135], v[208:211], a[0:3], v[132:135]\n"
"s_waitcnt lgkmcnt(5)\n"
"ds_read_b64_tr_b16 v[98:99], v231 offset:34816\n"
"v_mfma_f32_16x16x32_bf16 v[132:135], v[212:215], a[4:7], v[132:135]\n"
"v_cvt_pk_bf16_f32 v143, v146, v147\n"
"v_mfma_f32_16x16x32_bf16 v[132:135], v[216:219], a[8:11], v[132:135]\n"
"s_waitcnt lgkmcnt(5)\n"
"ds_read_b64_tr_b16 v[116:117], v235 offset:49152\n"
"v_mov_b64 v[34:35], s[22:23]\n"
"v_mfma_f32_16x16x32_bf16 v[132:135], v[220:223], a[12:15], v[132:135]\n"
"buffer_store_dwordx4 v[140:143], v243, s[32:35], s16 offen offset:2048 sc1\n"
"ds_read_b64_tr_b16 v[106:107], v231 offset:43008\n"
"v_mfma_f32_16x16x32_bf16 v[148:151], v[220:223], a[28:31], v[148:151]\n"
"v_cvt_pk_bf16_f32 v173, v174, v175\n"
"v_subrev_f32 v176, s22, v176\n"
"v_mfma_f32_16x16x32_bf16 v[148:151], v[216:219], a[24:27], v[148:151]\n"
"ds_read_b64_tr_b16 v[100:101], v235 offset:32768\n"
"v_max_u32 v237, 1, v237\n"
"v_mfma_f32_16x16x32_bf16 v[148:151], v[212:215], a[20:23], v[148:151]\n"
"v_mov_b64 v[14:15], s[22:23]\n"
"v_mov_b64 v[46:47], s[22:23]\n"
"v_mfma_f32_16x16x32_bf16 v[148:151], v[208:211], a[16:19], v[148:151]\n"
"ds_read_b64_tr_b16 v[104:105], v231 offset:40960\n"
"v_mov_b64 v[42:43], s[22:23]\n"
"v_mfma_f32_16x16x32_bf16 v[148:151], v[192:195], a[76:79], v[148:151]\n"
"ds_read_b64_tr_b16 v[120:121], v231 offset:57344\n"
"v_mov_b64 v[2:3], s[22:23]\n"
"v_mfma_f32_16x16x32_bf16 v[148:151], v[188:191], a[72:75], v[148:151]\n"
"v_subrev_f32 v162, s22, v162\n"
"v_subrev_f32 v179, s22, v179\n"
"v_mfma_f32_16x16x32_bf16 v[148:151], v[184:187], a[68:71], v[148:151]\n"
"ds_read_b64_tr_b16 v[122:123], v231 offset:59392\n"
"v_xor_b32 v229, s75, v229\n"
"v_mfma_f32_16x16x32_bf16 v[148:151], v[180:183], a[64:67], v[148:151]\n"
"ds_read_b64_tr_b16 v[110:111], v235 offset:43008\n"
"v_add_u32 v238, s97, v254\n"
"v_mfma_f32_16x16x32_bf16 v[164:167], v[180:183], a[80:83], v[164:167]\n"
"v_add_u32 v252, s98, v254\n"
"v_add_u32 v241, 1, v241\n"
"v_mfma_f32_16x16x32_bf16 v[164:167], v[184:187], a[84:87], v[164:167]\n"
"ds_read_b64_tr_b16 v[108:109], v235 offset:40960\n"
"v_mov_b64 v[10:11], s[22:23]\n"
"v_mfma_f32_16x16x32_bf16 v[164:167], v[188:191], a[88:91], v[164:167]\n"
"v_mov_b64 v[28:29], s[22:23]\n"
"v_subrev_f32 v177, s22, v177\n"
"v_mfma_f32_16x16x32_bf16 v[164:167], v[192:195], a[92:95], v[164:167]\n"
"ds_read_b64_tr_b16 v[118:119], v235 offset:51200\n"
"v_subrev_f32 v132, s22, v132\n"
"v_mfma_f32_16x16x32_bf16 v[164:167], v[208:211], a[32:35], v[164:167]\n"
"v_subrev_f32 v163, s22, v163\n"
"v_subrev_f32 v135, s22, v135\n"
"v_mfma_f32_16x16x32_bf16 v[164:167], v[212:215], a[36:39], v[164:167]\n"
"ds_read_b64_tr_b16 v[226:227], v235 offset:59392\n"
"v_cvt_pk_bf16_f32 v159, v162, v163\n"
"v_mfma_f32_16x16x32_bf16 v[164:167], v[216:219], a[40:43], v[164:167]\n"
"v_xor_b32 v237, s61, v237\n"
"v_mov_b64 v[18:19], s[22:23]\n"
"v_mfma_f32_16x16x32_bf16 v[164:167], v[220:223], a[44:47], v[164:167]\n"
"s_barrier\n"
"s_waitcnt lgkmcnt(3)\n"
"v_mfma_f32_16x16x32_bf16 v[136:139], v[96:99], a[48:51], v[136:139]\n"
"ds_read_b64_tr_b16 v[214:215], v232 offset:18432\n"
"v_mfma_f32_16x16x32_bf16 v[136:139], v[100:103], a[52:55], v[136:139]\n"
"ds_read_b64_tr_b16 v[212:213], v232 offset:16384\n"
"v_cvt_pk_bf16_f32 v158, v160, v161\n"
"v_mfma_f32_16x16x32_bf16 v[136:139], v[104:107], a[56:59], v[136:139]\n"
"s_waitcnt lgkmcnt(4)\n"
"ds_read_b64_tr_b16 v[220:221], v232 offset:24576\n"
"v_subrev_f32 v178, s22, v178\n"
"v_mfma_f32_16x16x32_bf16 v[136:139], v[108:111], a[60:63], v[136:139]\n"
"ds_read_b64_tr_b16 v[180:181], v228 offset:0\n"
"v_mfma_f32_16x16x32_bf16 v[136:139], v[112:115], a[0:3], v[136:139]\n"
"s_waitcnt lgkmcnt(5)\n"
"ds_read_b64_tr_b16 v[194:195], v232 offset:10240\n"
"v_mfma_f32_16x16x32_bf16 v[136:139], v[116:119], a[4:7], v[136:139]\n"
"ds_read_b64_tr_b16 v[188:189], v228 offset:8192\n"
"v_add_u32 v253, s99, v254\n"
"v_mfma_f32_16x16x32_bf16 v[136:139], v[120:123], a[8:11], v[136:139]\n"
"s_waitcnt lgkmcnt(6)\n"
"ds_read_b64_tr_b16 v[184:185], v232 offset:0\n"
"v_subrev_f32 v133, s22, v133\n"
"v_mfma_f32_16x16x32_bf16 v[136:139], v[224:227], a[12:15], v[136:139]\n"
"buffer_store_dwordx4 v[156:159], v243, s[32:35], s17 offen sc1\n"
"ds_read_b64_tr_b16 v[222:223], v232 offset:26624\n"
"v_mfma_f32_16x16x32_bf16 v[152:155], v[224:227], a[28:31], v[152:155]\n"
"ds_read_b64_tr_b16 v[208:209], v228 offset:16384\n"
"v_mfma_f32_16x16x32_bf16 v[152:155], v[120:123], a[24:27], v[152:155]\n"
"ds_read_b64_tr_b16 v[190:191], v228 offset:10240\n"
"v_mfma_f32_16x16x32_bf16 v[152:155], v[116:119], a[20:23], v[152:155]\n"
"ds_read_b64_tr_b16 v[186:187], v232 offset:2048\n"
"v_subrev_f32 v134, s22, v134\n"
"v_mfma_f32_16x16x32_bf16 v[152:155], v[112:115], a[16:19], v[152:155]\n"
"ds_read_b64_tr_b16 v[218:219], v228 offset:26624\n"
"v_mfma_f32_16x16x32_bf16 v[152:155], v[108:111], a[76:79], v[152:155]\n"
"ds_read_b64_tr_b16 v[192:193], v232 offset:8192\n"
"v_mfma_f32_16x16x32_bf16 v[152:155], v[104:107], a[72:75], v[152:155]\n"
"ds_read_b64_tr_b16 v[182:183], v228 offset:2048\n"
"v_xor_b32 v239, 64, v238\n"
"v_mfma_f32_16x16x32_bf16 v[152:155], v[100:103], a[68:71], v[152:155]\n"
"ds_read_b64_tr_b16 v[210:211], v228 offset:18432\n"
"ds_read_b64_tr_b16 v[216:217], v228 offset:24576\n"
"v_mfma_f32_16x16x32_bf16 v[152:155], v[96:99], a[64:67], v[152:155]\n"
"v_mfma_f32_16x16x32_bf16 v[168:171], v[96:99], a[80:83], v[168:171]\n"
"ds_read_b64_tr_b16 v[96:97], v229 offset:0\n"
"ds_read_b64_tr_b16 v[98:99], v229 offset:2048\n"
"v_mfma_f32_16x16x32_bf16 v[168:171], v[100:103], a[84:87], v[168:171]\n"
"ds_read_b64_tr_b16 v[100:101], v233 offset:0\n"
"ds_read_b64_tr_b16 v[102:103], v233 offset:2048\n"
"v_mfma_f32_16x16x32_bf16 v[168:171], v[104:107], a[88:91], v[168:171]\n"
"ds_read_b64_tr_b16 v[104:105], v229 offset:8192\n"
"v_cvt_pk_bf16_f32 v175, v178, v179\n"
"v_mfma_f32_16x16x32_bf16 v[168:171], v[108:111], a[92:95], v[168:171]\n"
"v_mov_b64 v[20:21], s[22:23]\n"
"v_mov_b64 v[90:91], 0\n"
"v_mfma_f32_16x16x32_bf16 v[168:171], v[112:115], a[32:35], v[168:171]\n"
"v_mov_b64 v[88:89], 0\n"
"v_mov_b64 v[92:93], 0\n"
"v_mfma_f32_16x16x32_bf16 v[168:171], v[116:119], a[36:39], v[168:171]\n"
"v_mov_b64 v[48:49], s[22:23]\n"
"v_cvt_pk_bf16_f32 v132, v132, v133\n"
"v_mfma_f32_16x16x32_bf16 v[168:171], v[120:123], a[40:43], v[168:171]\n"
"s_add_u32 s47, s47, 1\n"
"v_mov_b64 v[0:1], s[22:23]\n"
"v_mov_b64 v[8:9], s[22:23]\n"
"v_mfma_f32_16x16x32_bf16 v[168:171], v[224:227], a[44:47], v[168:171]\n"
"s_waitcnt lgkmcnt(5)\n"
"v_lshlrev_b32 v237, 2, v237\n"
"v_cvt_pk_bf16_f32 v174, v176, v177\n"
"LGL_%=:\n"
"v_mfma_f32_16x16x32_bf16 v[124:127], v[180:183], a[96:99], v[196:199]\n"
"ds_read_b64_tr_b16 v[106:107], v229 offset:10240\n"
"v_cvt_pk_bf16_f32 v133, v134, v135\n"
"v_mfma_f32_16x16x32_bf16 v[124:127], v[184:187], a[100:103], v[124:127]\n"
"ds_read_b64_tr_b16 v[108:109], v233 offset:8192\n"
"v_subrev_f32 v136, s22, v136\n"
"v_mfma_f32_16x16x32_bf16 v[124:127], v[188:191], a[104:107], v[124:127]\n"
"ds_read_b64_tr_b16 v[110:111], v233 offset:10240\n"
"v_subrev_f32 v137, s22, v137\n"
"v_mfma_f32_16x16x32_bf16 v[124:127], v[192:195], a[108:111], v[124:127]\n"
"ds_read_b64_tr_b16 v[112:113], v229 offset:16384\n"
"v_subrev_f32 v138, s22, v138\n"
"v_mfma_f32_16x16x32_bf16 v[124:127], v[208:211], a[144:147], v[124:127]\n"
"ds_read_b64_tr_b16 v[114:115], v229 offset:18432\n"
"v_subrev_f32 v139, s22, v139\n"
"v_mfma_f32_16x16x32_bf16 v[124:127], v[212:215], a[148:151], v[124:127]\n"
"s_waitcnt lgkmcnt(7)\n"
"ds_read_b64_tr_b16 v[116:117], v233 offset:16384\n"
"v_cvt_pk_bf16_f32 v134, v136, v137\n"
"v_mfma_f32_16x16x32_bf16 v[124:127], v[216:219], a[152:155], v[124:127]\n"
"ds_read_b64_tr_b16 v[118:119], v233 offset:18432\n"
"v_cvt_pk_bf16_f32 v135, v138, v139\n"
"v_mfma_f32_16x16x32_bf16 v[124:127], v[220:223], a[156:159], v[124:127]\n"
"buffer_store_dwordx4 v[172:175], v243, s[32:35], s17 offen offset:2048 sc1\n"
"v_subrev_f32 v148, s22, v148\n"
"v_mfma_f32_16x16x32_bf16 v[140:143], v[220:223], a[172:175], v[196:199]\n"
"ds_read_b64_tr_b16 v[120:121], v229 offset:24576\n"
"v_subrev_f32 v149, s22, v149\n"
"v_mfma_f32_16x16x32_bf16 v[140:143], v[216:219], a[168:171], v[140:143]\n"
"ds_read_b64_tr_b16 v[122:123], v229 offset:26624\n"
"v_subrev_f32 v150, s22, v150\n"
"v_mfma_f32_16x16x32_bf16 v[140:143], v[212:215], a[164:167], v[140:143]\n"
"ds_read_b64_tr_b16 v[224:225], v233 offset:24576\n"
"v_subrev_f32 v151, s22, v151\n"
"v_mfma_f32_16x16x32_bf16 v[140:143], v[208:211], a[160:163], v[140:143]\n"
"ds_read_b64_tr_b16 v[226:227], v233 offset:26624\n"
"v_cvt_pk_bf16_f32 v148, v148, v149\n"
"v_mfma_f32_16x16x32_bf16 v[140:143], v[192:195], a[124:127], v[140:143]\n"
"v_cvt_pk_bf16_f32 v149, v150, v151\n"
"v_subrev_f32 v152, s22, v152\n"
"v_mfma_f32_16x16x32_bf16 v[140:143], v[188:191], a[120:123], v[140:143]\n"
"s_add_u32 s41, s70, 0\n"
"v_subrev_f32 v153, s22, v153\n"
"v_subrev_f32 v154, s22, v154\n"
"v_mfma_f32_16x16x32_bf16 v[140:143], v[184:187], a[116:119], v[140:143]\n"
"s_mov_b32 m0, s71\n"
"v_subrev_f32 v155, s22, v155\n"
"v_cvt_pk_bf16_f32 v150, v152, v153\n"
"v_mfma_f32_16x16x32_bf16 v[140:143], v[180:183], a[112:115], v[140:143]\n"
"s_waitcnt vmcnt(14)\n"
"buffer_load_dwordx4 v240, s[28:31], s41 offen lds\n"
"v_mfma_f32_16x16x32_bf16 v[156:159], v[180:183], a[128:131], v[196:199]\n"
"v_xor_b32 v230, s75, v230\n"
"v_cvt_pk_bf16_f32 v151, v154, v155\n"
"v_mfma_f32_16x16x32_bf16 v[156:159], v[184:187], a[132:135], v[156:159]\n"
"v_xor_b32 v234, s75, v234\n"
"v_subrev_f32 v164, s22, v164\n"
"v_mfma_f32_16x16x32_bf16 v[156:159], v[188:191], a[136:139], v[156:159]\n"
"v_subrev_f32 v165, s22, v165\n"
"v_subrev_f32 v166, s22, v166\n"
"v_mfma_f32_16x16x32_bf16 v[156:159], v[192:195], a[140:143], v[156:159]\n"
"v_subrev_f32 v167, s22, v167\n"
"v_cvt_pk_bf16_f32 v164, v164, v165\n"
"v_mfma_f32_16x16x32_bf16 v[156:159], v[208:211], a[176:179], v[156:159]\n"
"v_cvt_pk_bf16_f32 v165, v166, v167\n"
"v_subrev_f32 v168, s22, v168\n"
"v_mfma_f32_16x16x32_bf16 v[156:159], v[212:215], a[180:183], v[156:159]\n"
"v_subrev_f32 v169, s22, v169\n"
"v_subrev_f32 v170, s22, v170\n"
"v_mfma_f32_16x16x32_bf16 v[156:159], v[216:219], a[184:187], v[156:159]\n"
"v_subrev_f32 v171, s22, v171\n"
"v_cvt_pk_bf16_f32 v166, v168, v169\n"
"v_mfma_f32_16x16x32_bf16 v[156:159], v[220:223], a[188:191], v[156:159]\n"
"buffer_store_dwordx4 v[132:135], v243, s[32:35], s16 offen offset:1024 sc1\n"
"v_cvt_pk_bf16_f32 v167, v170, v171\n"
"v_mfma_f32_16x16x32_bf16 v[172:175], v[220:223], a[252:255], v[196:199]\n"
"ds_read_b64_tr_b16 v[220:221], v234 offset:24576\n"
"v_xor_b32 v231, s75, v231\n"
"v_mfma_f32_16x16x32_bf16 v[172:175], v[216:219], a[248:251], v[172:175]\n"
"ds_read_b64_tr_b16 v[216:217], v230 offset:24576\n"
"v_xor_b32 v235, s75, v235\n"
"v_mfma_f32_16x16x32_bf16 v[172:175], v[212:215], a[244:247], v[172:175]\n"
"ds_read_b64_tr_b16 v[212:213], v234 offset:16384\n"
"v_lshlrev_b32 v202, 16, v84\n"
"v_mfma_f32_16x16x32_bf16 v[172:175], v[208:211], a[240:243], v[172:175]\n"
"ds_read_b64_tr_b16 v[208:209], v230 offset:16384\n"
"v_and_b32 v84, s73, v84\n"
"v_mfma_f32_16x16x32_bf16 v[172:175], v[192:195], a[204:207], v[172:175]\n"
"ds_read_b64_tr_b16 v[192:193], v234 offset:8192\n"
"v_add_f32 v25, v25, v84\n"
"v_mfma_f32_16x16x32_bf16 v[172:175], v[188:191], a[200:203], v[172:175]\n"
"ds_read_b64_tr_b16 v[188:189], v230 offset:8192\n"
"v_lshlrev_b32 v203, 16, v85\n"
"v_mfma_f32_16x16x32_bf16 v[172:175], v[184:187], a[196:199], v[172:175]\n"
"s_add_u32 s40, s41, s52\n"
"ds_read_b64_tr_b16 v[184:185], v234 offset:0\n"
"v_and_b32 v85, s73, v85\n"
"v_mfma_f32_16x16x32_bf16 v[172:175], v[180:183], a[192:195], v[172:175]\n"
"s_waitcnt vmcnt(13) lgkmcnt(7)\n"
"buffer_load_dwordx4 v240, s[28:31], s40 offen offset:1024 lds\n"
"v_mfma_f32_16x16x32_bf16 v[128:131], v[96:99], a[96:99], v[196:199]\n"
"s_waitcnt lgkmcnt(6)\n"
"ds_read_b64_tr_b16 v[180:181], v230 offset:0\n"
"v_add_f32 v27, v27, v85\n"
"v_mfma_f32_16x16x32_bf16 v[128:131], v[100:103], a[100:103], v[128:131]\n"
"ds_read_b64_tr_b16 v[182:183], v230 offset:2048\n"
"v_lshlrev_b32 v204, 16, v86\n"
"v_mfma_f32_16x16x32_bf16 v[128:131], v[104:107], a[104:107], v[128:131]\n"
"ds_read_b64_tr_b16 v[186:187], v234 offset:2048\n"
"v_and_b32 v86, s73, v86\n"
"v_mfma_f32_16x16x32_bf16 v[128:131], v[108:111], a[108:111], v[128:131]\n"
"ds_read_b64_tr_b16 v[190:191], v230 offset:10240\n"
"v_add_f32 v29, v29, v86\n"
"v_mfma_f32_16x16x32_bf16 v[128:131], v[112:115], a[144:147], v[128:131]\n"
"s_waitcnt vmcnt(6)\n"
"ds_read_b64_tr_b16 v[194:195], v234 offset:10240\n"
"v_lshlrev_b32 v205, 16, v87\n"
"v_mfma_f32_16x16x32_bf16 v[128:131], v[116:119], a[148:151], v[128:131]\n"
"v_subrev_u32 v200, s21, v200\n"
"ds_read_b64_tr_b16 v[210:211], v230 offset:18432\n"
"v_mfma_f32_16x16x32_bf16 v[128:131], v[120:123], a[152:155], v[128:131]\n"
"v_cmp_lt_u32 vcc, s69, v200\n"
"ds_read_b64_tr_b16 v[214:215], v234 offset:18432\n"
"v_mfma_f32_16x16x32_bf16 v[128:131], v[224:227], a[156:159], v[128:131]\n"
"ds_read_b64_tr_b16 v[218:219], v230 offset:26624\n"
"s_cbranch_vccnz LGP_%=\nLGPK_%=:\n"
"v_mfma_f32_16x16x32_bf16 v[144:147], v[224:227], a[172:175], v[196:199]\n"
"s_sub_u32 s72, s72, 1\n"
"ds_read_b64_tr_b16 v[222:223], v234 offset:26624\n"
"v_and_b32 v87, s73, v87\n"
"v_mfma_f32_16x16x32_bf16 v[144:147], v[120:123], a[168:171], v[144:147]\n"
"s_and_b32 s72, s72, 7\n"
"v_add_f32 v24, v24, v202\n"
"v_add_f32 v31, v31, v87\n"
"v_mfma_f32_16x16x32_bf16 v[144:147], v[116:119], a[164:167], v[144:147]\n"
"v_add_f32 v26, v26, v203\n"
"v_lshlrev_b32 v206, 16, v88\n"
"v_mfma_f32_16x16x32_bf16 v[144:147], v[112:115], a[160:163], v[144:147]\n"
"buffer_load_dwordx4 v[72:75], v243, s[32:35], s18 offen sc1\n"
"v_mfma_f32_16x16x32_bf16 v[144:147], v[108:111], a[124:127], v[144:147]\n"
"v_and_b32 v88, s73, v88\n"
"v_add_f32 v33, v33, v88\n"
"v_mfma_f32_16x16x32_bf16 v[144:147], v[104:107], a[120:123], v[144:147]\n"
"v_lshlrev_b32 v207, 16, v89\n"
"v_and_b32 v89, s73, v89\n"
"v_mfma_f32_16x16x32_bf16 v[144:147], v[100:103], a[116:119], v[144:147]\n"
"v_add_f32 v35, v35, v89\n"
"v_lshlrev_b32 v202, 16, v90\n"
"v_mfma_f32_16x16x32_bf16 v[144:147], v[96:99], a[112:115], v[144:147]\n"
"buffer_store_dwordx4 v[148:151], v243, s[32:35], s16 offen offset:3072 sc1\n"
"v_and_b32 v90, s73, v90\n"
"v_mfma_f32_16x16x32_bf16 v[160:163], v[96:99], a[128:131], v[196:199]\n"
"v_add_f32 v37, v37, v90\n"
"v_lshlrev_b32 v203, 16, v91\n"
"v_mfma_f32_16x16x32_bf16 v[160:163], v[100:103], a[132:135], v[160:163]\n"
"v_and_b32 v91, s73, v91\n"
"v_add_f32 v39, v39, v91\n"
"v_mfma_f32_16x16x32_bf16 v[160:163], v[104:107], a[136:139], v[160:163]\n"
"v_add_f32 v28, v28, v204\n"
"v_add_f32 v30, v30, v205\n"
"v_mfma_f32_16x16x32_bf16 v[160:163], v[108:111], a[140:143], v[160:163]\n"
"v_add_f32 v32, v32, v206\n"
"v_add_f32 v34, v34, v207\n"
"v_mfma_f32_16x16x32_bf16 v[160:163], v[112:115], a[176:179], v[160:163]\n"
"v_lshlrev_b32 v204, 16, v92\n"
"v_and_b32 v92, s73, v92\n"
"v_mfma_f32_16x16x32_bf16 v[160:163], v[116:119], a[180:183], v[160:163]\n"
"v_add_f32 v41, v41, v92\n"
"v_lshlrev_b32 v205, 16, v93\n"
"v_mfma_f32_16x16x32_bf16 v[160:163], v[120:123], a[184:187], v[160:163]\n"
"s_add_u32 s40, s41, s53\n"
"v_and_b32 v93, s73, v93\n"
"v_add_f32 v43, v43, v93\n"
"v_mfma_f32_16x16x32_bf16 v[160:163], v[224:227], a[188:191], v[160:163]\n"
"buffer_load_dwordx4 v240, s[28:31], s40 offen offset:2048 lds\n"
"v_mfma_f32_16x16x32_bf16 v[176:179], v[224:227], a[252:255], v[196:199]\n"
"ds_read_b64_tr_b16 v[224:225], v235 offset:24576\n"
"v_lshlrev_b32 v206, 16, v94\n"
"v_mfma_f32_16x16x32_bf16 v[176:179], v[120:123], a[248:251], v[176:179]\n"
"ds_read_b64_tr_b16 v[120:121], v231 offset:24576\n"
"v_and_b32 v94, s73, v94\n"
"v_mfma_f32_16x16x32_bf16 v[176:179], v[116:119], a[244:247], v[176:179]\n"
"ds_read_b64_tr_b16 v[116:117], v235 offset:16384\n"
"v_add_f32 v45, v45, v94\n"
"v_mfma_f32_16x16x32_bf16 v[176:179], v[112:115], a[240:243], v[176:179]\n"
"buffer_load_dwordx4 v[76:79], v243, s[32:35], s18 offen offset:1024 sc1\n"
"v_mfma_f32_16x16x32_bf16 v[176:179], v[108:111], a[204:207], v[176:179]\n"
"ds_read_b64_tr_b16 v[108:109], v235 offset:8192\n"
"v_lshlrev_b32 v207, 16, v95\n"
"v_mfma_f32_16x16x32_bf16 v[176:179], v[104:107], a[200:203], v[176:179]\n"
"ds_read_b64_tr_b16 v[104:105], v231 offset:8192\n"
"v_and_b32 v95, s73, v95\n"
"v_mfma_f32_16x16x32_bf16 v[176:179], v[100:103], a[196:199], v[176:179]\n"
"s_add_u32 s40, s41, s54\n"
"ds_read_b64_tr_b16 v[100:101], v235 offset:0\n"
"v_add_f32 v47, v47, v95\n"
"v_mfma_f32_16x16x32_bf16 v[176:179], v[96:99], a[192:195], v[176:179]\n"
"s_waitcnt lgkmcnt(6)\n"
"buffer_load_dwordx4 v240, s[28:31], s40 offen offset:3072 lds\n"
"v_mfma_f32_16x16x32_bf16 v[132:135], v[180:183], a[96:99], v[196:199]\n"
"ds_read_b64_tr_b16 v[96:97], v231 offset:0\n"
"v_add_f32 v36, v36, v202\n"
"v_mfma_f32_16x16x32_bf16 v[132:135], v[184:187], a[100:103], v[132:135]\n"
"ds_read_b64_tr_b16 v[98:99], v231 offset:2048\n"
"v_add_f32 v38, v38, v203\n"
"v_mfma_f32_16x16x32_bf16 v[132:135], v[188:191], a[104:107], v[132:135]\n"
"ds_read_b64_tr_b16 v[102:103], v235 offset:2048\n"
"v_add_f32 v44, v44, v206\n"
"v_mfma_f32_16x16x32_bf16 v[132:135], v[192:195], a[108:111], v[132:135]\n"
"ds_read_b64_tr_b16 v[106:107], v231 offset:10240\n"
"v_add_f32 v46, v46, v207\n"
"v_mfma_f32_16x16x32_bf16 v[132:135], v[208:211], a[144:147], v[132:135]\n"
"ds_read_b64_tr_b16 v[110:111], v235 offset:10240\n"
"v_add_f32 v40, v40, v204\n"
"v_mfma_f32_16x16x32_bf16 v[132:135], v[212:215], a[148:151], v[132:135]\n"
"ds_read_b64_tr_b16 v[112:113], v231 offset:16384\n"
"v_add_f32 v42, v42, v205\n"
"v_mfma_f32_16x16x32_bf16 v[132:135], v[216:219], a[152:155], v[132:135]\n"
"ds_read_b64_tr_b16 v[114:115], v231 offset:18432\n"
"v_mfma_f32_16x16x32_bf16 v[132:135], v[220:223], a[156:159], v[132:135]\n"
"buffer_store_dwordx4 v[164:167], v243, s[32:35], s17 offen offset:1024 sc1\n"
"v_lshlrev_b32 v206, 16, v244\n"
"v_mfma_f32_16x16x32_bf16 v[148:151], v[220:223], a[172:175], v[196:199]\n"
"ds_read_b64_tr_b16 v[118:119], v235 offset:18432\n"
"v_and_b32 v244, s73, v244\n"
"v_mfma_f32_16x16x32_bf16 v[148:151], v[216:219], a[168:171], v[148:151]\n"
"ds_read_b64_tr_b16 v[122:123], v231 offset:26624\n"
"v_add_f32 v49, v49, v244\n"
"v_mfma_f32_16x16x32_bf16 v[148:151], v[212:215], a[164:167], v[148:151]\n"
"ds_read_b64_tr_b16 v[226:227], v235 offset:26624\n"
"v_lshlrev_b32 v207, 16, v245\n"
"v_mfma_f32_16x16x32_bf16 v[148:151], v[208:211], a[160:163], v[148:151]\n"
"buffer_load_dwordx4 v[80:83], v243, s[32:35], s18 offen offset:2048 sc1\n"
"v_mfma_f32_16x16x32_bf16 v[148:151], v[192:195], a[124:127], v[148:151]\n"
"v_and_b32 v245, s73, v245\n"
"v_add_f32 v51, v51, v245\n"
"v_mfma_f32_16x16x32_bf16 v[148:151], v[188:191], a[120:123], v[148:151]\n"
"s_add_u32 s41, s70, 163840\n"
"v_lshlrev_b32 v202, 16, v246\n"
"v_and_b32 v246, s73, v246\n"
"v_mfma_f32_16x16x32_bf16 v[148:151], v[184:187], a[116:119], v[148:151]\n"
"s_add_u32 m0, s71, 16384\n"
"v_add_f32 v53, v53, v246\n"
"v_lshlrev_b32 v203, 16, v247\n"
"v_mfma_f32_16x16x32_bf16 v[148:151], v[180:183], a[112:115], v[148:151]\n"
"buffer_load_dwordx4 v240, s[28:31], s41 offen lds\n"
"v_mfma_f32_16x16x32_bf16 v[164:167], v[180:183], a[128:131], v[196:199]\n"
"v_and_b32 v247, s73, v247\n"
"v_add_f32 v55, v55, v247\n"
"v_mfma_f32_16x16x32_bf16 v[164:167], v[184:187], a[132:135], v[164:167]\n"
"v_add_f32 v48, v48, v206\n"
"v_add_f32 v50, v50, v207\n"
"v_mfma_f32_16x16x32_bf16 v[164:167], v[188:191], a[136:139], v[164:167]\n"
"v_add_f32 v52, v52, v202\n"
"v_add_f32 v54, v54, v203\n"
"v_mfma_f32_16x16x32_bf16 v[164:167], v[192:195], a[140:143], v[164:167]\n"
"v_mfma_f32_16x16x32_bf16 v[164:167], v[208:211], a[176:179], v[164:167]\n"
"v_mfma_f32_16x16x32_bf16 v[164:167], v[212:215], a[180:183], v[164:167]\n"
"v_mfma_f32_16x16x32_bf16 v[164:167], v[216:219], a[184:187], v[164:167]\n"
"s_waitcnt vmcnt(12)\n"
"v_mfma_f32_16x16x32_bf16 v[164:167], v[220:223], a[188:191], v[164:167]\n"
"s_barrier\n"
"s_waitcnt lgkmcnt(0)\n"
"v_mfma_f32_16x16x32_bf16 v[136:139], v[96:99], a[96:99], v[196:199]\n"
"ds_read_b64_tr_b16 v[180:181], v228 offset:32768\n"
"v_mfma_f32_16x16x32_bf16 v[136:139], v[100:103], a[100:103], v[136:139]\n"
"ds_read_b64_tr_b16 v[182:183], v228 offset:34816\n"
"v_mfma_f32_16x16x32_bf16 v[136:139], v[104:107], a[104:107], v[136:139]\n"
"ds_read_b64_tr_b16 v[184:185], v232 offset:32768\n"
"v_mfma_f32_16x16x32_bf16 v[136:139], v[108:111], a[108:111], v[136:139]\n"
"ds_read_b64_tr_b16 v[186:187], v232 offset:34816\n"
"v_mfma_f32_16x16x32_bf16 v[136:139], v[112:115], a[144:147], v[136:139]\n"
"ds_read_b64_tr_b16 v[188:189], v228 offset:40960\n"
"v_mfma_f32_16x16x32_bf16 v[136:139], v[116:119], a[148:151], v[136:139]\n"
"ds_read_b64_tr_b16 v[190:191], v228 offset:43008\n"
"v_mfma_f32_16x16x32_bf16 v[136:139], v[120:123], a[152:155], v[136:139]\n"
"s_add_u32 s40, s41, s52\n"
"ds_read_b64_tr_b16 v[192:193], v232 offset:40960\n"
"v_mfma_f32_16x16x32_bf16 v[136:139], v[224:227], a[156:159], v[136:139]\n"
"buffer_load_dwordx4 v240, s[28:31], s40 offen offset:1024 lds\n"
"v_mfma_f32_16x16x32_bf16 v[152:155], v[224:227], a[172:175], v[196:199]\n"
"ds_read_b64_tr_b16 v[194:195], v232 offset:43008\n"
"v_mfma_f32_16x16x32_bf16 v[152:155], v[120:123], a[168:171], v[152:155]\n"
"ds_read_b64_tr_b16 v[208:209], v228 offset:49152\n"
"v_mfma_f32_16x16x32_bf16 v[152:155], v[116:119], a[164:167], v[152:155]\n"
"ds_read_b64_tr_b16 v[210:211], v228 offset:51200\n"
"v_mfma_f32_16x16x32_bf16 v[152:155], v[112:115], a[160:163], v[152:155]\n"
"ds_read_b64_tr_b16 v[212:213], v232 offset:49152\n"
"v_mfma_f32_16x16x32_bf16 v[152:155], v[108:111], a[124:127], v[152:155]\n"
"ds_read_b64_tr_b16 v[214:215], v232 offset:51200\n"
"v_mfma_f32_16x16x32_bf16 v[152:155], v[104:107], a[120:123], v[152:155]\n"
"ds_read_b64_tr_b16 v[216:217], v228 offset:57344\n"
"v_mfma_f32_16x16x32_bf16 v[152:155], v[100:103], a[116:119], v[152:155]\n"
"s_add_u32 s40, s41, s53\n"
"ds_read_b64_tr_b16 v[218:219], v228 offset:59392\n"
"v_xor_b32 v228, s75, v228\n"
"v_mfma_f32_16x16x32_bf16 v[152:155], v[96:99], a[112:115], v[152:155]\n"
"buffer_load_dwordx4 v240, s[28:31], s40 offen offset:2048 lds\n"
"v_mfma_f32_16x16x32_bf16 v[168:171], v[96:99], a[128:131], v[196:199]\n"
"ds_read_b64_tr_b16 v[220:221], v232 offset:57344\n"
"v_mfma_f32_16x16x32_bf16 v[168:171], v[100:103], a[132:135], v[168:171]\n"
"ds_read_b64_tr_b16 v[222:223], v232 offset:59392\n"
"v_xor_b32 v232, s75, v232\n"
"v_mfma_f32_16x16x32_bf16 v[168:171], v[104:107], a[136:139], v[168:171]\n"
"ds_read_b64_tr_b16 v[96:97], v229 offset:32768\n"
"v_mfma_f32_16x16x32_bf16 v[168:171], v[108:111], a[140:143], v[168:171]\n"
"ds_read_b64_tr_b16 v[98:99], v229 offset:34816\n"
"v_mfma_f32_16x16x32_bf16 v[168:171], v[112:115], a[176:179], v[168:171]\n"
"ds_read_b64_tr_b16 v[100:101], v233 offset:32768\n"
"v_mfma_f32_16x16x32_bf16 v[168:171], v[116:119], a[180:183], v[168:171]\n"
"ds_read_b64_tr_b16 v[102:103], v233 offset:34816\n"
"v_mfma_f32_16x16x32_bf16 v[168:171], v[120:123], a[184:187], v[168:171]\n"
"s_add_u32 s40, s41, s54\n"
"ds_read_b64_tr_b16 v[104:105], v229 offset:40960\n"
"v_mfma_f32_16x16x32_bf16 v[168:171], v[224:227], a[188:191], v[168:171]\n"
"s_waitcnt lgkmcnt(7)\n"
"buffer_load_dwordx4 v240, s[28:31], s40 offen offset:3072 lds\n"
"v_mfma_f32_16x16x32_bf16 v[124:127], v[180:183], a[48:51], v[124:127]\n"
"ds_read_b64_tr_b16 v[106:107], v229 offset:43008\n"
"v_mfma_f32_16x16x32_bf16 v[124:127], v[184:187], a[52:55], v[124:127]\n"
"ds_read_b64_tr_b16 v[108:109], v233 offset:40960\n"
"v_mfma_f32_16x16x32_bf16 v[124:127], v[188:191], a[56:59], v[124:127]\n"
"ds_read_b64_tr_b16 v[110:111], v233 offset:43008\n"
"v_mfma_f32_16x16x32_bf16 v[124:127], v[192:195], a[60:63], v[124:127]\n"
"ds_read_b64_tr_b16 v[112:113], v229 offset:49152\n"
"v_mfma_f32_16x16x32_bf16 v[124:127], v[208:211], a[0:3], v[124:127]\n"
"ds_read_b64_tr_b16 v[114:115], v229 offset:51200\n"
"v_mfma_f32_16x16x32_bf16 v[124:127], v[212:215], a[4:7], v[124:127]\n"
"s_add_u32 s41, s70, 327680\n"
"ds_read_b64_tr_b16 v[116:117], v233 offset:49152\n"
"v_mfma_f32_16x16x32_bf16 v[124:127], v[216:219], a[8:11], v[124:127]\n"
"s_waitcnt lgkmcnt(6)\n"
"s_add_u32 m0, s71, 32768\n"
"ds_read_b64_tr_b16 v[118:119], v233 offset:51200\n"
"v_mfma_f32_16x16x32_bf16 v[124:127], v[220:223], a[12:15], v[124:127]\n"
"buffer_load_dwordx4 v240, s[28:31], s41 offen lds\n"
"v_mfma_f32_16x16x32_bf16 v[140:143], v[220:223], a[28:31], v[140:143]\n"
"ds_read_b64_tr_b16 v[120:121], v229 offset:57344\n"
"v_mfma_f32_16x16x32_bf16 v[140:143], v[216:219], a[24:27], v[140:143]\n"
"ds_read_b64_tr_b16 v[122:123], v229 offset:59392\n"
"v_xor_b32 v229, s75, v229\n"
"v_mfma_f32_16x16x32_bf16 v[140:143], v[212:215], a[20:23], v[140:143]\n"
"ds_read_b64_tr_b16 v[224:225], v233 offset:57344\n"
"v_mfma_f32_16x16x32_bf16 v[140:143], v[208:211], a[16:19], v[140:143]\n"
"ds_read_b64_tr_b16 v[226:227], v233 offset:59392\n"
"v_xor_b32 v233, s75, v233\n"
"v_mfma_f32_16x16x32_bf16 v[140:143], v[192:195], a[76:79], v[140:143]\n"
"v_mfma_f32_16x16x32_bf16 v[140:143], v[188:191], a[72:75], v[140:143]\n"
"v_subrev_f32 v124, s22, v124\n"
"v_subrev_f32 v125, s22, v125\n"
"v_mfma_f32_16x16x32_bf16 v[140:143], v[184:187], a[68:71], v[140:143]\n"
"s_add_u32 s40, s41, s52\n"
"v_subrev_f32 v126, s22, v126\n"
"v_subrev_f32 v127, s22, v127\n"
"v_mfma_f32_16x16x32_bf16 v[140:143], v[180:183], a[64:67], v[140:143]\n"
"buffer_load_dwordx4 v240, s[28:31], s40 offen offset:1024 lds\n"
"v_mfma_f32_16x16x32_bf16 v[156:159], v[180:183], a[80:83], v[156:159]\n"
"v_cvt_pk_bf16_f32 v124, v124, v125\n"
"v_cvt_pk_bf16_f32 v125, v126, v127\n"
"v_mfma_f32_16x16x32_bf16 v[156:159], v[184:187], a[84:87], v[156:159]\n"
"v_mfma_f32_16x16x32_bf16 v[156:159], v[188:191], a[88:91], v[156:159]\n"
"v_mfma_f32_16x16x32_bf16 v[156:159], v[192:195], a[92:95], v[156:159]\n"
"v_mfma_f32_16x16x32_bf16 v[156:159], v[208:211], a[32:35], v[156:159]\n"
"v_mfma_f32_16x16x32_bf16 v[156:159], v[212:215], a[36:39], v[156:159]\n"
"v_subrev_f32 v140, s22, v140\n"
"v_subrev_f32 v141, s22, v141\n"
"v_mfma_f32_16x16x32_bf16 v[156:159], v[216:219], a[40:43], v[156:159]\n"
"s_waitcnt vmcnt(7)\n"
"v_subrev_f32 v142, s22, v142\n"
"v_subrev_f32 v143, s22, v143\n"
"v_mfma_f32_16x16x32_bf16 v[156:159], v[220:223], a[44:47], v[156:159]\n"
"s_barrier\n"
"buffer_store_dword v241, v248, s[84:87], 0 offen sc1\n"
"v_mfma_f32_16x16x32_bf16 v[172:175], v[220:223], a[228:231], v[172:175]\n"
"ds_read_b64_tr_b16 v[220:221], v234 offset:57344\n"
"v_cvt_pk_bf16_f32 v140, v140, v141\n"
"v_mfma_f32_16x16x32_bf16 v[172:175], v[216:219], a[224:227], v[172:175]\n"
"ds_read_b64_tr_b16 v[216:217], v230 offset:57344\n"
"v_cvt_pk_bf16_f32 v141, v142, v143\n"
"v_mfma_f32_16x16x32_bf16 v[172:175], v[212:215], a[212:215], v[172:175]\n"
"ds_read_b64_tr_b16 v[212:213], v234 offset:49152\n"
"v_add_u32 v241, 1, v241\n"
"v_mfma_f32_16x16x32_bf16 v[172:175], v[208:211], a[208:211], v[172:175]\n"
"buffer_load_dwordx4 v[84:87], v243, s[32:35], s18 offen offset:3072 sc1\n"
"v_mfma_f32_16x16x32_bf16 v[172:175], v[192:195], a[236:239], v[172:175]\n"
"ds_read_b64_tr_b16 v[192:193], v234 offset:40960\n"
"v_mfma_f32_16x16x32_bf16 v[172:175], v[188:191], a[232:235], v[172:175]\n"
"ds_read_b64_tr_b16 v[188:189], v230 offset:40960\n"
"v_subrev_f32 v156, s22, v156\n"
"v_mfma_f32_16x16x32_bf16 v[172:175], v[184:187], a[220:223], v[172:175]\n"
"s_add_u32 s40, s41, s53\n"
"ds_read_b64_tr_b16 v[184:185], v234 offset:32768\n"
"v_subrev_f32 v157, s22, v157\n"
"v_mfma_f32_16x16x32_bf16 v[172:175], v[180:183], a[216:219], v[172:175]\n"
"buffer_load_dwordx4 v240, s[28:31], s40 offen offset:2048 lds\n"
"v_mfma_f32_16x16x32_bf16 v[128:131], v[96:99], a[48:51], v[128:131]\n"
"s_add_u32 s16, s90, s62\n"
"ds_read_b64_tr_b16 v[180:181], v230 offset:32768\n"
"v_subrev_f32 v158, s22, v158\n"
"v_mfma_f32_16x16x32_bf16 v[128:131], v[100:103], a[52:55], v[128:131]\n"
"s_waitcnt lgkmcnt(5)\n"
"s_add_u32 s17, s16, 4096\n"
"ds_read_b64_tr_b16 v[182:183], v230 offset:34816\n"
"v_subrev_f32 v159, s22, v159\n"
"v_mfma_f32_16x16x32_bf16 v[128:131], v[104:107], a[56:59], v[128:131]\n"
"s_lshl_b32 s62, s70, 9\n"
"ds_read_b64_tr_b16 v[186:187], v234 offset:34816\n"
"v_cvt_pk_bf16_f32 v156, v156, v157\n"
"v_mfma_f32_16x16x32_bf16 v[128:131], v[108:111], a[60:63], v[128:131]\n"
"ds_read_b64_tr_b16 v[190:191], v230 offset:43008\n"
"v_cvt_pk_bf16_f32 v157, v158, v159\n"
"v_mfma_f32_16x16x32_bf16 v[128:131], v[112:115], a[0:3], v[128:131]\n"
"ds_read_b64_tr_b16 v[194:195], v234 offset:43008\n"
"v_lshlrev_b32 v202, 16, v72\n"
"v_mfma_f32_16x16x32_bf16 v[128:131], v[116:119], a[4:7], v[128:131]\n"
"ds_read_b64_tr_b16 v[208:209], v230 offset:49152\n"
"v_and_b32 v72, s73, v72\n"
"v_mfma_f32_16x16x32_bf16 v[128:131], v[120:123], a[8:11], v[128:131]\n"
"s_add_u32 s40, s41, s54\n"
"ds_read_b64_tr_b16 v[210:211], v230 offset:51200\n"
"v_add_f32 v0, v0, v202\n"
"v_mfma_f32_16x16x32_bf16 v[128:131], v[224:227], a[12:15], v[128:131]\n"
"buffer_load_dwordx4 v240, s[28:31], s40 offen offset:3072 lds\n"
"v_mfma_f32_16x16x32_bf16 v[144:147], v[224:227], a[28:31], v[144:147]\n"
"ds_read_b64_tr_b16 v[214:215], v234 offset:51200\n"
"v_add_f32 v1, v1, v72\n"
"v_mfma_f32_16x16x32_bf16 v[144:147], v[120:123], a[24:27], v[144:147]\n"
"ds_read_b64_tr_b16 v[218:219], v230 offset:59392\n"
"v_lshlrev_b32 v203, 16, v73\n"
"v_mfma_f32_16x16x32_bf16 v[144:147], v[116:119], a[20:23], v[144:147]\n"
"ds_read_b64_tr_b16 v[222:223], v234 offset:59392\n"
"v_and_b32 v73, s73, v73\n"
"v_mfma_f32_16x16x32_bf16 v[144:147], v[112:115], a[16:19], v[144:147]\n"
"buffer_load_dwordx4 v[88:91], v243, s[32:35], s19 offen sc1\n"
"v_mfma_f32_16x16x32_bf16 v[144:147], v[108:111], a[76:79], v[144:147]\n"
"v_add_f32 v2, v2, v203\n"
"v_add_f32 v3, v3, v73\n"
"v_mfma_f32_16x16x32_bf16 v[144:147], v[104:107], a[72:75], v[144:147]\n"
"s_add_u32 s41, s70, 491520\n"
"v_subrev_f32 v128, s22, v128\n"
"v_subrev_f32 v129, s22, v129\n"
"v_mfma_f32_16x16x32_bf16 v[144:147], v[100:103], a[68:71], v[144:147]\n"
"s_add_u32 m0, s71, 49152\n"
"v_subrev_f32 v130, s22, v130\n"
"v_subrev_f32 v131, s22, v131\n"
"v_mfma_f32_16x16x32_bf16 v[144:147], v[96:99], a[64:67], v[144:147]\n"
"buffer_load_dwordx4 v240, s[28:31], s41 offen lds\n"
"v_mfma_f32_16x16x32_bf16 v[160:163], v[96:99], a[80:83], v[160:163]\n"
"s_and_b32 s70, s88, 15\n"
"v_cvt_pk_bf16_f32 v126, v128, v129\n"
"v_cvt_pk_bf16_f32 v127, v130, v131\n"
"v_mfma_f32_16x16x32_bf16 v[160:163], v[100:103], a[84:87], v[160:163]\n"
"s_lshr_b32 s88, s88, 4\n"
"v_lshlrev_b32 v204, 16, v74\n"
"v_and_b32 v74, s73, v74\n"
"v_mfma_f32_16x16x32_bf16 v[160:163], v[104:107], a[88:91], v[160:163]\n"
"s_lshl_b32 s70, s70, 7\n"
"v_add_f32 v4, v4, v204\n"
"v_add_f32 v5, v5, v74\n"
"v_mfma_f32_16x16x32_bf16 v[160:163], v[108:111], a[92:95], v[160:163]\n"
"s_xor_b32 s71, s71, s75\n"
"v_lshlrev_b32 v205, 16, v75\n"
"v_and_b32 v75, s73, v75\n"
"v_mfma_f32_16x16x32_bf16 v[160:163], v[112:115], a[32:35], v[160:163]\n"
"v_add_f32 v6, v6, v205\n"
"v_add_f32 v7, v7, v75\n"
"v_mfma_f32_16x16x32_bf16 v[160:163], v[116:119], a[36:39], v[160:163]\n"
"v_subrev_f32 v144, s22, v144\n"
"v_subrev_f32 v145, s22, v145\n"
"v_mfma_f32_16x16x32_bf16 v[160:163], v[120:123], a[40:43], v[160:163]\n"
"v_subrev_f32 v146, s22, v146\n"
"v_subrev_f32 v147, s22, v147\n"
"v_mfma_f32_16x16x32_bf16 v[160:163], v[224:227], a[44:47], v[160:163]\n"
"buffer_store_dwordx4 v[124:127], v243, s[32:35], s16 offen sc1\n"
"v_cvt_pk_bf16_f32 v142, v144, v145\n"
"v_mfma_f32_16x16x32_bf16 v[176:179], v[224:227], a[228:231], v[176:179]\n"
"ds_read_b64_tr_b16 v[224:225], v235 offset:57344\n"
"v_cvt_pk_bf16_f32 v143, v146, v147\n"
"v_mfma_f32_16x16x32_bf16 v[176:179], v[120:123], a[224:227], v[176:179]\n"
"ds_read_b64_tr_b16 v[120:121], v231 offset:57344\n"
"v_lshlrev_b32 v206, 16, v76\n"
"v_mfma_f32_16x16x32_bf16 v[176:179], v[116:119], a[212:215], v[176:179]\n"
"ds_read_b64_tr_b16 v[116:117], v235 offset:49152\n"
"v_and_b32 v76, s73, v76\n"
"v_mfma_f32_16x16x32_bf16 v[176:179], v[112:115], a[208:211], v[176:179]\n"
"buffer_load_dwordx4 v[92:95], v243, s[32:35], s19 offen offset:1024 sc1\n"
"v_mfma_f32_16x16x32_bf16 v[176:179], v[108:111], a[236:239], v[176:179]\n"
"ds_read_b64_tr_b16 v[108:109], v235 offset:40960\n"
"v_add_f32 v8, v8, v206\n"
"v_mfma_f32_16x16x32_bf16 v[176:179], v[104:107], a[232:235], v[176:179]\n"
"ds_read_b64_tr_b16 v[104:105], v231 offset:40960\n"
"v_subrev_f32 v160, s22, v160\n"
"v_mfma_f32_16x16x32_bf16 v[176:179], v[100:103], a[220:223], v[176:179]\n"
"s_add_u32 s40, s41, s52\n"
"ds_read_b64_tr_b16 v[100:101], v235 offset:32768\n"
"v_subrev_f32 v161, s22, v161\n"
"v_mfma_f32_16x16x32_bf16 v[176:179], v[96:99], a[216:219], v[176:179]\n"
"s_waitcnt vmcnt(14) lgkmcnt(6)\n"
"buffer_load_dwordx4 v240, s[28:31], s40 offen offset:1024 lds\n"
"v_mfma_f32_16x16x32_bf16 v[132:135], v[180:183], a[48:51], v[132:135]\n"
"ds_read_b64_tr_b16 v[96:97], v231 offset:32768\n"
"v_subrev_f32 v162, s22, v162\n"
"v_mfma_f32_16x16x32_bf16 v[132:135], v[184:187], a[52:55], v[132:135]\n"
"ds_read_b64_tr_b16 v[98:99], v231 offset:34816\n"
"v_subrev_f32 v163, s22, v163\n"
"v_mfma_f32_16x16x32_bf16 v[132:135], v[188:191], a[56:59], v[132:135]\n"
"ds_read_b64_tr_b16 v[102:103], v235 offset:34816\n"
"v_cvt_pk_bf16_f32 v158, v160, v161\n"
"v_mfma_f32_16x16x32_bf16 v[132:135], v[192:195], a[60:63], v[132:135]\n"
"ds_read_b64_tr_b16 v[106:107], v231 offset:43008\n"
"v_cvt_pk_bf16_f32 v159, v162, v163\n"
"v_mfma_f32_16x16x32_bf16 v[132:135], v[208:211], a[0:3], v[132:135]\n"
"ds_read_b64_tr_b16 v[110:111], v235 offset:43008\n"
"v_add_f32 v9, v9, v76\n"
"v_mfma_f32_16x16x32_bf16 v[132:135], v[212:215], a[4:7], v[132:135]\n"
"ds_read_b64_tr_b16 v[112:113], v231 offset:49152\n"
"v_lshlrev_b32 v207, 16, v77\n"
"v_mfma_f32_16x16x32_bf16 v[132:135], v[216:219], a[8:11], v[132:135]\n"
"ds_read_b64_tr_b16 v[114:115], v231 offset:51200\n"
"v_and_b32 v77, s73, v77\n"
"v_mfma_f32_16x16x32_bf16 v[132:135], v[220:223], a[12:15], v[132:135]\n"
"buffer_store_dwordx4 v[140:143], v243, s[32:35], s16 offen offset:2048 sc1\n"
"v_add_f32 v10, v10, v207\n"
"v_mfma_f32_16x16x32_bf16 v[148:151], v[220:223], a[28:31], v[148:151]\n"
"ds_read_b64_tr_b16 v[118:119], v235 offset:51200\n"
"v_add_f32 v11, v11, v77\n"
"v_mfma_f32_16x16x32_bf16 v[148:151], v[216:219], a[24:27], v[148:151]\n"
"ds_read_b64_tr_b16 v[122:123], v231 offset:59392\n"
"v_lshlrev_b32 v202, 16, v78\n"
"v_mfma_f32_16x16x32_bf16 v[148:151], v[212:215], a[20:23], v[148:151]\n"
"ds_read_b64_tr_b16 v[226:227], v235 offset:59392\n"
"v_and_b32 v78, s73, v78\n"
"v_mfma_f32_16x16x32_bf16 v[148:151], v[208:211], a[16:19], v[148:151]\n"
"v_add_f32 v12, v12, v202\n"
"v_add_f32 v13, v13, v78\n"
"v_mfma_f32_16x16x32_bf16 v[148:151], v[192:195], a[76:79], v[148:151]\n"
"v_lshlrev_b32 v203, 16, v79\n"
"v_and_b32 v79, s73, v79\n"
"v_mfma_f32_16x16x32_bf16 v[148:151], v[188:191], a[72:75], v[148:151]\n"
"v_add_f32 v14, v14, v203\n"
"v_add_f32 v15, v15, v79\n"
"v_mfma_f32_16x16x32_bf16 v[148:151], v[184:187], a[68:71], v[148:151]\n"
"s_add_u32 s40, s41, s53\n"
"v_lshlrev_b32 v204, 16, v80\n"
"v_and_b32 v80, s73, v80\n"
"v_mfma_f32_16x16x32_bf16 v[148:151], v[180:183], a[64:67], v[148:151]\n"
"buffer_load_dwordx4 v240, s[28:31], s40 offen offset:2048 lds\n"
"v_mfma_f32_16x16x32_bf16 v[164:167], v[180:183], a[80:83], v[164:167]\n"
"v_add_f32 v16, v16, v204\n"
"v_add_f32 v17, v17, v80\n"
"v_mfma_f32_16x16x32_bf16 v[164:167], v[184:187], a[84:87], v[164:167]\n"
"v_lshlrev_b32 v205, 16, v81\n"
"v_and_b32 v81, s73, v81\n"
"v_mfma_f32_16x16x32_bf16 v[164:167], v[188:191], a[88:91], v[164:167]\n"
"v_add_f32 v18, v18, v205\n"
"v_add_f32 v19, v19, v81\n"
"v_mfma_f32_16x16x32_bf16 v[164:167], v[192:195], a[92:95], v[164:167]\n"
"buffer_load_dwordx4 v[244:247], v243, s[32:35], s19 offen offset:2048 sc1\n"
"v_mfma_f32_16x16x32_bf16 v[164:167], v[208:211], a[32:35], v[164:167]\n"
"s_lshl_b32 s18, s72, 4\n"
"v_lshlrev_b32 v206, 16, v82\n"
"v_and_b32 v82, s73, v82\n"
"v_mfma_f32_16x16x32_bf16 v[164:167], v[212:215], a[36:39], v[164:167]\n"
"s_lshl_b32 s19, s72, 2\n"
"v_add_f32 v20, v20, v206\n"
"v_add_f32 v21, v21, v82\n"
"v_mfma_f32_16x16x32_bf16 v[164:167], v[216:219], a[40:43], v[164:167]\n"
"s_waitcnt vmcnt(14)\n"
"s_lshr_b32 s19, s89, s19\n"
"v_lshlrev_b32 v207, 16, v83\n"
"v_mfma_f32_16x16x32_bf16 v[164:167], v[220:223], a[44:47], v[164:167]\n"
"s_barrier\n"
"s_and_b32 s19, s19, 1\n"
"s_waitcnt lgkmcnt(0)\n"
"v_mfma_f32_16x16x32_bf16 v[136:139], v[96:99], a[48:51], v[136:139]\n"
"s_lshl3_add_u32 s18, s18, s19\n"
"ds_read_b64_tr_b16 v[180:181], v228 offset:0\n"
"v_and_b32 v83, s73, v83\n"
"v_mfma_f32_16x16x32_bf16 v[136:139], v[100:103], a[52:55], v[136:139]\n"
"s_lshl_b32 s18, s18, 13\n"
"ds_read_b64_tr_b16 v[182:183], v228 offset:2048\n"
"v_add_f32 v22, v22, v207\n"
"v_mfma_f32_16x16x32_bf16 v[136:139], v[104:107], a[56:59], v[136:139]\n"
"s_add_u32 s18, s18, s91\n"
"v_lshlrev_b32_e64 v201, 2, s72\n"
"ds_read_b64_tr_b16 v[184:185], v232 offset:0\n"
"v_mfma_f32_16x16x32_bf16 v[136:139], v[108:111], a[60:63], v[136:139]\n"
"s_add_u32 s19, s18, 4096\n"
"global_load_dword v200, v201, s[6:7] sc1\n"
"v_mfma_f32_16x16x32_bf16 v[136:139], v[112:115], a[0:3], v[136:139]\n"
"s_mov_b32 s69, 15\n"
"ds_read_b64_tr_b16 v[186:187], v232 offset:2048\n"
"v_add_f32 v23, v23, v83\n"
"v_mfma_f32_16x16x32_bf16 v[136:139], v[116:119], a[4:7], v[136:139]\n"
"s_add_u32 s21, s21, 1\n"
"ds_read_b64_tr_b16 v[188:189], v228 offset:8192\n"
"v_subrev_f32 v172, s22, v172\n"
"v_mfma_f32_16x16x32_bf16 v[136:139], v[120:123], a[8:11], v[136:139]\n"
"ds_read_b64_tr_b16 v[190:191], v228 offset:10240\n"
"v_subrev_f32 v173, s22, v173\n"
"v_mfma_f32_16x16x32_bf16 v[136:139], v[224:227], a[12:15], v[136:139]\n"
"buffer_store_dwordx4 v[156:159], v243, s[32:35], s17 offen sc1\n"
"v_subrev_f32 v174, s22, v174\n"
"v_mfma_f32_16x16x32_bf16 v[152:155], v[224:227], a[28:31], v[152:155]\n"
"ds_read_b64_tr_b16 v[192:193], v232 offset:8192\n"
"v_subrev_f32 v175, s22, v175\n"
"v_mfma_f32_16x16x32_bf16 v[152:155], v[120:123], a[24:27], v[152:155]\n"
"ds_read_b64_tr_b16 v[194:195], v232 offset:10240\n"
"v_cvt_pk_bf16_f32 v172, v172, v173\n"
"v_mfma_f32_16x16x32_bf16 v[152:155], v[116:119], a[20:23], v[152:155]\n"
"ds_read_b64_tr_b16 v[208:209], v228 offset:16384\n"
"v_cvt_pk_bf16_f32 v173, v174, v175\n"
"v_mfma_f32_16x16x32_bf16 v[152:155], v[112:115], a[16:19], v[152:155]\n"
"ds_read_b64_tr_b16 v[210:211], v228 offset:18432\n"
"v_subrev_f32 v176, s22, v176\n"
"v_mfma_f32_16x16x32_bf16 v[152:155], v[108:111], a[76:79], v[152:155]\n"
"ds_read_b64_tr_b16 v[212:213], v232 offset:16384\n"
"v_subrev_f32 v177, s22, v177\n"
"v_mfma_f32_16x16x32_bf16 v[152:155], v[104:107], a[72:75], v[152:155]\n"
"ds_read_b64_tr_b16 v[214:215], v232 offset:18432\n"
"v_subrev_f32 v178, s22, v178\n"
"v_mfma_f32_16x16x32_bf16 v[152:155], v[100:103], a[68:71], v[152:155]\n"
"s_add_u32 s40, s41, s54\n"
"ds_read_b64_tr_b16 v[216:217], v228 offset:24576\n"
"v_subrev_f32 v179, s22, v179\n"
"v_mfma_f32_16x16x32_bf16 v[152:155], v[96:99], a[64:67], v[152:155]\n"
"buffer_load_dwordx4 v240, s[28:31], s40 offen offset:3072 lds\n"
"v_mfma_f32_16x16x32_bf16 v[168:171], v[96:99], a[80:83], v[168:171]\n"
"ds_read_b64_tr_b16 v[218:219], v228 offset:26624\n"
"v_cvt_pk_bf16_f32 v174, v176, v177\n"
"v_mfma_f32_16x16x32_bf16 v[168:171], v[100:103], a[84:87], v[168:171]\n"
"ds_read_b64_tr_b16 v[220:221], v232 offset:24576\n"
"v_cvt_pk_bf16_f32 v175, v178, v179\n"
"v_mfma_f32_16x16x32_bf16 v[168:171], v[104:107], a[88:91], v[168:171]\n"
"ds_read_b64_tr_b16 v[222:223], v232 offset:26624\n"
"v_subrev_f32 v132, s22, v132\n"
"v_mfma_f32_16x16x32_bf16 v[168:171], v[108:111], a[92:95], v[168:171]\n"
"ds_read_b64_tr_b16 v[96:97], v229 offset:0\n"
"v_subrev_f32 v133, s22, v133\n"
"v_mfma_f32_16x16x32_bf16 v[168:171], v[112:115], a[32:35], v[168:171]\n"
"ds_read_b64_tr_b16 v[98:99], v229 offset:2048\n"
"v_subrev_f32 v134, s22, v134\n"
"v_mfma_f32_16x16x32_bf16 v[168:171], v[116:119], a[36:39], v[168:171]\n"
"ds_read_b64_tr_b16 v[100:101], v233 offset:0\n"
"v_subrev_f32 v135, s22, v135\n"
"v_mfma_f32_16x16x32_bf16 v[168:171], v[120:123], a[40:43], v[168:171]\n"
"s_add_u32 s47, s47, 1\n"
"ds_read_b64_tr_b16 v[102:103], v233 offset:2048\n"
"v_cvt_pk_bf16_f32 v132, v132, v133\n"
"v_mfma_f32_16x16x32_bf16 v[168:171], v[224:227], a[44:47], v[168:171]\n"
"s_waitcnt lgkmcnt(7)\n"
"s_cmp_lg_u32 s47, 9\n"
"ds_read_b64_tr_b16 v[104:105], v229 offset:8192\n"
"s_cbranch_scc1 LGL_%=\n"
"v_mfma_f32_16x16x32_bf16 v[0:3], v[180:183], a[96:99], v[0:3]\n"
"v_xor_b32 v235, s75, v235\n"
"v_subrev_f32 v136, s22, v136\n"
"v_mfma_f32_16x16x32_bf16 v[0:3], v[184:187], a[100:103], v[0:3]\n"
"ds_read_b64_tr_b16 v[114:115], v229 offset:18432\n"
"v_cvt_pk_bf16_f32 v133, v134, v135\n"
"v_mfma_f32_16x16x32_bf16 v[0:3], v[188:191], a[104:107], v[0:3]\n"
"v_mov_b32 v201, v237\n"
"v_subrev_f32 v138, s22, v138\n"
"v_subrev_f32 v139, s22, v139\n"
"v_mfma_f32_16x16x32_bf16 v[0:3], v[192:195], a[108:111], v[0:3]\n"
"global_load_dword v200, v201, s[6:7] sc1\n"
"v_mfma_f32_16x16x32_bf16 v[0:3], v[208:211], a[144:147], v[0:3]\n"
"v_subrev_f32 v167, s22, v167\n"
"v_cvt_pk_bf16_f32 v135, v138, v139\n"
"v_mfma_f32_16x16x32_bf16 v[0:3], v[212:215], a[148:151], v[0:3]\n"
"s_waitcnt lgkmcnt(3)\n"
"v_subrev_f32 v148, s22, v148\n"
"v_subrev_f32 v137, s22, v137\n"
"v_mfma_f32_16x16x32_bf16 v[0:3], v[216:219], a[152:155], v[0:3]\n"
"v_subrev_f32 v165, s22, v165\n"
"v_subrev_f32 v168, s22, v168\n"
"v_mfma_f32_16x16x32_bf16 v[0:3], v[220:223], a[156:159], v[0:3]\n"
"buffer_store_dwordx4 v[172:175], v243, s[32:35], s17 offen offset:2048 sc1\n"
"v_subrev_f32 v151, s22, v151\n"
"v_cvt_pk_bf16_f32 v134, v136, v137\n"
"v_mfma_f32_16x16x32_bf16 v[16:19], v[220:223], a[172:175], v[16:19]\n"
"buffer_store_dwordx4 v[132:135], v243, s[32:35], s16 offen offset:1024 sc1\n"
"v_subrev_f32 v149, s22, v149\n"
"v_subrev_f32 v150, s22, v150\n"
"v_subrev_f32 v164, s22, v164\n"
"v_cvt_pk_bf16_f32 v164, v164, v165\n"
"v_subrev_f32 v153, s22, v153\n"
"v_subrev_f32 v155, s22, v155\n"
"v_mfma_f32_16x16x32_bf16 v[16:19], v[216:219], a[168:171], v[16:19]\n"
"v_cvt_pk_bf16_f32 v148, v148, v149\n"
"v_subrev_f32 v154, s22, v154\n"
"v_mfma_f32_16x16x32_bf16 v[16:19], v[212:215], a[164:167], v[16:19]\n"
"v_cvt_pk_bf16_f32 v149, v150, v151\n"
"v_subrev_f32 v170, s22, v170\n"
"v_mfma_f32_16x16x32_bf16 v[16:19], v[208:211], a[160:163], v[16:19]\n"
"v_subrev_f32 v166, s22, v166\n"
"v_subrev_f32 v171, s22, v171\n"
"v_mfma_f32_16x16x32_bf16 v[16:19], v[192:195], a[124:127], v[16:19]\n"
"v_cvt_pk_bf16_f32 v165, v166, v167\n"
"v_cvt_pk_bf16_f32 v167, v170, v171\n"
"v_mfma_f32_16x16x32_bf16 v[16:19], v[188:191], a[120:123], v[16:19]\n"
"v_subrev_f32 v152, s22, v152\n"
"v_cvt_pk_bf16_f32 v151, v154, v155\n"
"v_mfma_f32_16x16x32_bf16 v[16:19], v[184:187], a[116:119], v[16:19]\n"
"v_cvt_pk_bf16_f32 v150, v152, v153\n"
"v_subrev_f32 v169, s22, v169\n"
"v_mfma_f32_16x16x32_bf16 v[16:19], v[180:183], a[112:115], v[16:19]\n"
"s_waitcnt vmcnt(16)\n"
"buffer_store_dwordx4 v[148:151], v243, s[32:35], s16 offen offset:3072 sc1\n"
"v_cvt_pk_bf16_f32 v166, v168, v169\n"
"v_mfma_f32_16x16x32_bf16 v[32:35], v[180:183], a[128:131], v[32:35]\n"
"buffer_store_dwordx4 v[164:167], v243, s[32:35], s17 offen offset:1024 sc1\n"
"ds_read_b64_tr_b16 v[120:121], v229 offset:24576\n"
"v_mfma_f32_16x16x32_bf16 v[32:35], v[184:187], a[132:135], v[32:35]\n"
"ds_read_b64_tr_b16 v[224:225], v233 offset:24576\n"
"v_xor_b32 v234, s75, v234\n"
"v_mfma_f32_16x16x32_bf16 v[32:35], v[188:191], a[136:139], v[32:35]\n"
"ds_read_b64_tr_b16 v[122:123], v229 offset:26624\n"
"v_lshlrev_b32 v203, 16, v85\n"
"v_mfma_f32_16x16x32_bf16 v[32:35], v[192:195], a[140:143], v[32:35]\n"
"ds_read_b64_tr_b16 v[108:109], v233 offset:8192\n"
"v_lshlrev_b32 v202, 16, v84\n"
"v_mfma_f32_16x16x32_bf16 v[32:35], v[208:211], a[176:179], v[32:35]\n"
"ds_read_b64_tr_b16 v[226:227], v233 offset:26624\n"
"v_and_b32 v84, s73, v84\n"
"v_mfma_f32_16x16x32_bf16 v[32:35], v[212:215], a[180:183], v[32:35]\n"
"ds_read_b64_tr_b16 v[110:111], v233 offset:10240\n"
"v_bfe_u32 v161, v242, 8, 2\n"
"v_mfma_f32_16x16x32_bf16 v[32:35], v[216:219], a[184:187], v[32:35]\n"
"ds_read_b64_tr_b16 v[106:107], v229 offset:10240\n"
"v_lshlrev_b32 v205, 16, v87\n"
"v_mfma_f32_16x16x32_bf16 v[32:35], v[220:223], a[188:191], v[32:35]\n"
"v_xor_b32 v230, s75, v230\n"
"v_lshlrev_b32 v204, 16, v86\n"
"v_mfma_f32_16x16x32_bf16 v[48:51], v[220:223], a[252:255], v[48:51]\n"
"ds_read_b64_tr_b16 v[220:221], v234 offset:24576\n"
"v_add_f32 v26, v26, v203\n"
"v_mfma_f32_16x16x32_bf16 v[48:51], v[216:219], a[248:251], v[48:51]\n"
"ds_read_b64_tr_b16 v[112:113], v229 offset:16384\n"
"v_bfe_u32 v160, v242, 6, 2\n"
"v_mfma_f32_16x16x32_bf16 v[48:51], v[212:215], a[244:247], v[48:51]\n"
"ds_read_b64_tr_b16 v[116:117], v233 offset:16384\n"
"v_add_f32 v24, v24, v202\n"
"v_mfma_f32_16x16x32_bf16 v[48:51], v[208:211], a[240:243], v[48:51]\n"
"ds_read_b64_tr_b16 v[118:119], v233 offset:18432\n"
"v_add_f32 v25, v25, v84\n"
"v_mfma_f32_16x16x32_bf16 v[48:51], v[192:195], a[204:207], v[48:51]\n"
"ds_read_b64_tr_b16 v[210:211], v230 offset:18432\n"
"v_add_f32 v28, v28, v204\n"
"v_mfma_f32_16x16x32_bf16 v[48:51], v[188:191], a[200:203], v[48:51]\n"
"ds_read_b64_tr_b16 v[188:189], v230 offset:8192\n"
"v_and_b32 v86, s73, v86\n"
"v_mfma_f32_16x16x32_bf16 v[48:51], v[184:187], a[196:199], v[48:51]\n"
"ds_read_b64_tr_b16 v[194:195], v234 offset:10240\n"
"v_mov_b32 v164, 0\n"
"v_mfma_f32_16x16x32_bf16 v[48:51], v[180:183], a[192:195], v[48:51]\n"
"s_waitcnt vmcnt(15)\n"
"v_xor_b32 v160, v160, v161\n"
"v_lshlrev_b32 v202, 16, v90\n"
"v_mfma_f32_16x16x32_bf16 v[4:7], v[96:99], a[96:99], v[4:7]\n"
"s_waitcnt lgkmcnt(6)\n"
"ds_read_b64_tr_b16 v[222:223], v234 offset:26624\n"
"v_lshlrev_b32 v203, 16, v91\n"
"v_mfma_f32_16x16x32_bf16 v[4:7], v[100:103], a[100:103], v[4:7]\n"
"ds_read_b64_tr_b16 v[184:185], v234 offset:0\n"
"v_lshlrev_b32 v206, 16, v88\n"
"v_mfma_f32_16x16x32_bf16 v[4:7], v[104:107], a[104:107], v[4:7]\n"
"ds_read_b64_tr_b16 v[216:217], v230 offset:24576\n"
"v_mfma_f32_16x16x32_bf16 v[4:7], v[108:111], a[108:111], v[4:7]\n"
"s_waitcnt lgkmcnt(6)\n"
"ds_read_b64_tr_b16 v[218:219], v230 offset:26624\n"
"v_and_b32 v91, s73, v91\n"
"v_mfma_f32_16x16x32_bf16 v[4:7], v[112:115], a[144:147], v[4:7]\n"
"s_waitcnt vmcnt(4)\n"
"ds_read_b64_tr_b16 v[190:191], v230 offset:10240\n"
"v_lshlrev_b32 v207, 16, v89\n"
"v_mfma_f32_16x16x32_bf16 v[4:7], v[116:119], a[148:151], v[4:7]\n"
"v_subrev_u32 v200, s21, v200\n"
"ds_read_b64_tr_b16 v[208:209], v230 offset:16384\n"
"v_mfma_f32_16x16x32_bf16 v[4:7], v[120:123], a[152:155], v[4:7]\n"
"v_cmp_lt_u32 vcc, s69, v200\n"
"ds_read_b64_tr_b16 v[182:183], v230 offset:2048\n"
"v_mfma_f32_16x16x32_bf16 v[4:7], v[224:227], a[156:159], v[4:7]\n"
"ds_read_b64_tr_b16 v[180:181], v230 offset:0\n"
"s_cbranch_vccnz LGQ_%=\nLGQK_%=:\n"
"v_mfma_f32_16x16x32_bf16 v[20:23], v[224:227], a[172:175], v[20:23]\n"
"s_bfe_u32 s20, s89, 65536\ns_lshl_b32 s20, s20, 13\ns_add_u32 s20, s20, 0\n"
"ds_read_b64_tr_b16 v[214:215], v234 offset:18432\n"
"v_and_b32 v85, s73, v85\n"
"v_mfma_f32_16x16x32_bf16 v[20:23], v[120:123], a[168:171], v[20:23]\n"
"buffer_load_dwordx4 v[124:127], v236, s[32:35], s20 offen sc1\n"
"v_mfma_f32_16x16x32_bf16 v[20:23], v[116:119], a[164:167], v[20:23]\n"
"v_and_b32 v90, s73, v90\n"
"v_add_f32 v37, v37, v90\n"
"v_mfma_f32_16x16x32_bf16 v[20:23], v[112:115], a[160:163], v[20:23]\n"
"s_bfe_u32 s20, s89, 65540\ns_lshl_b32 s20, s20, 13\ns_add_u32 s20, s20, 1048576\n"
"v_add_f32 v36, v36, v202\n"
"v_add_f32 v38, v38, v203\n"
"v_mfma_f32_16x16x32_bf16 v[20:23], v[108:111], a[124:127], v[20:23]\n"
"buffer_load_dwordx4 v[128:131], v236, s[32:35], s20 offen sc1\n"
"v_mfma_f32_16x16x32_bf16 v[20:23], v[104:107], a[120:123], v[20:23]\n"
"v_add_u32 v160, -1, v160\n"
"v_add_f32 v39, v39, v91\n"
"v_mfma_f32_16x16x32_bf16 v[20:23], v[100:103], a[116:119], v[20:23]\n"
"s_bfe_u32 s20, s89, 65544\ns_lshl_b32 s20, s20, 13\ns_add_u32 s20, s20, 2097152\n"
"v_add_f32 v32, v32, v206\n"
"v_mfma_f32_16x16x32_bf16 v[20:23], v[96:99], a[112:115], v[20:23]\n"
"buffer_load_dwordx4 v[132:135], v236, s[32:35], s20 offen sc1\n"
"v_mfma_f32_16x16x32_bf16 v[36:39], v[96:99], a[128:131], v[36:39]\n"
"v_lshlrev_b32 v204, 16, v92\n"
"v_add_f32 v40, v40, v204\n"
"v_mfma_f32_16x16x32_bf16 v[36:39], v[100:103], a[132:135], v[36:39]\n"
"s_bfe_u32 s20, s89, 65548\ns_lshl_b32 s20, s20, 13\ns_add_u32 s20, s20, 3145728\n"
"ds_read_b64_tr_b16 v[212:213], v234 offset:16384\n"
"v_add_f32 v30, v30, v205\n"
"v_mfma_f32_16x16x32_bf16 v[36:39], v[104:107], a[136:139], v[36:39]\n"
"buffer_load_dwordx4 v[136:139], v236, s[32:35], s20 offen sc1\n"
"v_mfma_f32_16x16x32_bf16 v[36:39], v[108:111], a[140:143], v[36:39]\n"
"v_and_b32 v92, s73, v92\n"
"v_lshlrev_b32 v206, 16, v94\n"
"v_mfma_f32_16x16x32_bf16 v[36:39], v[112:115], a[176:179], v[36:39]\n"
"s_bfe_u32 s20, s89, 65552\ns_lshl_b32 s20, s20, 13\ns_add_u32 s20, s20, 4194304\n"
"ds_read_b64_tr_b16 v[186:187], v234 offset:2048\n"
"ds_read_b64_tr_b16 v[192:193], v234 offset:8192\n"
"v_mfma_f32_16x16x32_bf16 v[36:39], v[116:119], a[180:183], v[36:39]\n"
"buffer_load_dwordx4 v[140:143], v236, s[32:35], s20 offen sc1\n"
"v_mfma_f32_16x16x32_bf16 v[36:39], v[120:123], a[184:187], v[36:39]\n"
"v_and_b32 v94, s73, v94\n"
"v_mfma_f32_16x16x32_bf16 v[36:39], v[224:227], a[188:191], v[36:39]\n"
"s_bfe_u32 s20, s89, 65556\ns_lshl_b32 s20, s20, 13\ns_add_u32 s20, s20, 5242880\n"
"v_add_f32 v34, v34, v207\n"
"v_lshlrev_b32 v205, 16, v93\n"
"v_mfma_f32_16x16x32_bf16 v[52:55], v[224:227], a[252:255], v[52:55]\n"
"buffer_load_dwordx4 v[144:147], v236, s[32:35], s20 offen sc1\n"
"v_mfma_f32_16x16x32_bf16 v[52:55], v[120:123], a[248:251], v[52:55]\n"
"v_ashrrev_i32 v160, 31, v160\n"
"v_add_f32 v44, v44, v206\n"
"v_mfma_f32_16x16x32_bf16 v[52:55], v[116:119], a[244:247], v[52:55]\n"
"s_bfe_u32 s20, s89, 65560\ns_lshl_b32 s20, s20, 13\ns_add_u32 s20, s20, 6291456\n"
"v_xor_b32 v231, s75, v231\n"
"v_lshlrev_b32 v207, 16, v95\n"
"v_mfma_f32_16x16x32_bf16 v[52:55], v[112:115], a[240:243], v[52:55]\n"
"buffer_load_dwordx4 v[148:151], v236, s[32:35], s20 offen sc1\n"
"v_mfma_f32_16x16x32_bf16 v[52:55], v[108:111], a[204:207], v[52:55]\n"
"s_bfe_u32 s20, s89, 65564\ns_lshl_b32 s20, s20, 13\ns_add_u32 s20, s20, 7340032\n"
"v_and_b32 v87, s73, v87\n"
"v_and_b32 v95, s73, v95\n"
"v_mfma_f32_16x16x32_bf16 v[52:55], v[104:107], a[200:203], v[52:55]\n"
"buffer_load_dwordx4 v[152:155], v236, s[32:35], s20 offen sc1\n"
"v_mfma_f32_16x16x32_bf16 v[52:55], v[100:103], a[196:199], v[52:55]\n"
"s_waitcnt vmcnt(8)\n"
"s_add_u32 s21, s21, 1\n"
"v_and_b32 v88, s73, v88\n"
"v_mfma_f32_16x16x32_bf16 v[52:55], v[96:99], a[192:195], v[52:55]\n"
"s_barrier\n"
"s_mov_b32 s52, 40960\n"
"buffer_store_dword v241, v248, s[84:87], 0 offen sc1\n"
"s_waitcnt lgkmcnt(0)\n"
"v_mfma_f32_16x16x32_bf16 v[8:11], v[180:183], a[96:99], v[8:11]\n"
"s_mov_b32 s53, 81920\n"
"ds_read_b64_tr_b16 v[98:99], v231 offset:2048\n"
"v_add_f32 v42, v42, v205\n"
"v_mfma_f32_16x16x32_bf16 v[8:11], v[184:187], a[100:103], v[8:11]\n"
"ds_read_b64_tr_b16 v[112:113], v231 offset:16384\n"
"v_add_f32 v31, v31, v87\n"
"v_mfma_f32_16x16x32_bf16 v[8:11], v[188:191], a[104:107], v[8:11]\n"
"v_add_f32 v29, v29, v86\n"
"v_and_b32 v161, 16, v242\n"
"v_mfma_f32_16x16x32_bf16 v[8:11], v[192:195], a[108:111], v[8:11]\n"
"ds_read_b64_tr_b16 v[118:119], v235 offset:18432\n"
"v_and_b32 v160, 0x3f80, v160\n"
"v_mfma_f32_16x16x32_bf16 v[8:11], v[208:211], a[144:147], v[8:11]\n"
"ds_read_b64_tr_b16 v[102:103], v235 offset:2048\n"
"v_add_f32 v27, v27, v85\n"
"v_mfma_f32_16x16x32_bf16 v[8:11], v[212:215], a[148:151], v[8:11]\n"
"ds_read_b64_tr_b16 v[106:107], v231 offset:10240\n"
"v_lshlrev_b32 v160, v161, v160\n"
"v_mfma_f32_16x16x32_bf16 v[8:11], v[216:219], a[152:155], v[8:11]\n"
"ds_read_b64_tr_b16 v[96:97], v231 offset:0\n"
"v_and_b32 v93, s73, v93\n"
"v_mfma_f32_16x16x32_bf16 v[8:11], v[220:223], a[156:159], v[8:11]\n"
"ds_read_b64_tr_b16 v[108:109], v235 offset:8192\n"
"v_add_f32 v41, v41, v92\n"
"v_mfma_f32_16x16x32_bf16 v[24:27], v[220:223], a[172:175], v[24:27]\n"
"v_bfe_i32 v161, v242, 5, 1\n"
"v_lshlrev_b32 v206, 16, v244\n"
"v_mfma_f32_16x16x32_bf16 v[24:27], v[216:219], a[168:171], v[24:27]\n"
"ds_read_b64_tr_b16 v[114:115], v231 offset:18432\n"
"v_mfma_f32_16x16x32_bf16 v[24:27], v[212:215], a[164:167], v[24:27]\n"
"ds_read_b64_tr_b16 v[104:105], v231 offset:8192\n"
"v_lshlrev_b32 v203, 16, v247\n"
"v_mfma_f32_16x16x32_bf16 v[24:27], v[208:211], a[160:163], v[24:27]\n"
"ds_read_b64_tr_b16 v[116:117], v235 offset:16384\n"
"v_add_f32 v48, v48, v206\n"
"v_mfma_f32_16x16x32_bf16 v[24:27], v[192:195], a[124:127], v[24:27]\n"
"ds_read_b64_tr_b16 v[110:111], v235 offset:10240\n"
"v_and_b32 v89, s73, v89\n"
"v_mfma_f32_16x16x32_bf16 v[24:27], v[188:191], a[120:123], v[24:27]\n"
"v_add_f32 v45, v45, v94\n"
"v_add_f32 v47, v47, v95\n"
"v_mfma_f32_16x16x32_bf16 v[24:27], v[184:187], a[116:119], v[24:27]\n"
"ds_read_b64_tr_b16 v[100:101], v235 offset:0\n"
"v_add_f32 v43, v43, v93\n"
"v_mfma_f32_16x16x32_bf16 v[24:27], v[180:183], a[112:115], v[24:27]\n"
"ds_read_b64_tr_b16 v[122:123], v231 offset:26624\n"
"v_add_f32 v46, v46, v207\n"
"v_mfma_f32_16x16x32_bf16 v[40:43], v[180:183], a[128:131], v[40:43]\n"
"v_add_f32 v33, v33, v88\n"
"v_and_b32 v244, s73, v244\n"
"v_mfma_f32_16x16x32_bf16 v[40:43], v[184:187], a[132:135], v[40:43]\n"
"ds_read_b64_tr_b16 v[120:121], v231 offset:24576\n"
"v_lshlrev_b32 v207, 16, v245\n"
"v_mfma_f32_16x16x32_bf16 v[40:43], v[188:191], a[136:139], v[40:43]\n"
"v_and_b32 v163, v160, v161\n"
"v_lshlrev_b32 v202, 16, v246\n"
"v_mfma_f32_16x16x32_bf16 v[40:43], v[192:195], a[140:143], v[40:43]\n"
"ds_read_b64_tr_b16 v[224:225], v235 offset:24576\n"
"v_and_b32 v246, s73, v246\n"
"v_mfma_f32_16x16x32_bf16 v[40:43], v[208:211], a[176:179], v[40:43]\n"
"v_add_f32 v52, v52, v202\n"
"v_and_b32 v247, s73, v247\n"
"v_mfma_f32_16x16x32_bf16 v[40:43], v[212:215], a[180:183], v[40:43]\n"
"ds_read_b64_tr_b16 v[226:227], v235 offset:26624\n"
"v_and_b32 v245, s73, v245\n"
"v_mfma_f32_16x16x32_bf16 v[40:43], v[216:219], a[184:187], v[40:43]\n"
"v_mov_b32 v161, 0\n"
"v_add_f32 v54, v54, v203\n"
"v_mfma_f32_16x16x32_bf16 v[40:43], v[220:223], a[188:191], v[40:43]\n"
"s_barrier\n"
"s_waitcnt lgkmcnt(3)\n"
"v_mfma_f32_16x16x32_bf16 v[12:15], v[96:99], a[96:99], v[12:15]\n"
"ds_read_b64_tr_b16 v[184:185], v232 offset:32768\n"
"v_add_f32 v49, v49, v244\n"
"v_mfma_f32_16x16x32_bf16 v[12:15], v[100:103], a[100:103], v[12:15]\n"
"ds_read_b64_tr_b16 v[216:217], v228 offset:57344\n"
"v_mfma_f32_16x16x32_bf16 v[12:15], v[104:107], a[104:107], v[12:15]\n"
"ds_read_b64_tr_b16 v[188:189], v228 offset:40960\n"
"v_add_f32 v35, v35, v89\n"
"v_mfma_f32_16x16x32_bf16 v[12:15], v[108:111], a[108:111], v[12:15]\n"
"ds_read_b64_tr_b16 v[190:191], v228 offset:43008\n"
"v_mfma_f32_16x16x32_bf16 v[12:15], v[112:115], a[144:147], v[12:15]\n"
"ds_read_b64_tr_b16 v[192:193], v232 offset:40960\n"
"v_add_f32 v53, v53, v246\n"
"v_mfma_f32_16x16x32_bf16 v[12:15], v[116:119], a[148:151], v[12:15]\n"
"s_waitcnt lgkmcnt(5)\n"
"ds_read_b64_tr_b16 v[212:213], v232 offset:49152\n"
"v_add_f32 v51, v51, v245\n"
"v_mfma_f32_16x16x32_bf16 v[12:15], v[120:123], a[152:155], v[12:15]\n"
"ds_read_b64_tr_b16 v[182:183], v228 offset:34816\n"
"v_xor_b32 v162, v160, v163\n"
"v_mfma_f32_16x16x32_bf16 v[12:15], v[224:227], a[156:159], v[12:15]\n"
"global_load_dword v200, v201, s[6:7] sc1\n"
"v_mfma_f32_16x16x32_bf16 v[28:31], v[224:227], a[172:175], v[28:31]\n"
"v_add_f32 v50, v50, v207\n"
"v_add_f32 v55, v55, v247\n"
"v_mfma_f32_16x16x32_bf16 v[28:31], v[120:123], a[168:171], v[28:31]\n"
"s_waitcnt vmcnt(9)\n"
"v_lshlrev_b32 v58, 16, v125\n"
"v_lshlrev_b32 v62, 16, v127\n"
"v_mfma_f32_16x16x32_bf16 v[28:31], v[116:119], a[164:167], v[28:31]\n"
"ds_read_b64_tr_b16 v[186:187], v232 offset:34816\n"
"ds_read_b64_tr_b16 v[214:215], v232 offset:51200\n"
"v_mfma_f32_16x16x32_bf16 v[28:31], v[112:115], a[160:163], v[28:31]\n"
"ds_read_b64_tr_b16 v[194:195], v232 offset:43008\n"
"v_and_b32 v61, s73, v126\n"
"v_mfma_f32_16x16x32_bf16 v[28:31], v[108:111], a[124:127], v[28:31]\n"
"s_waitcnt vmcnt(8)\n"
"ds_read_b64_tr_b16 v[210:211], v228 offset:51200\n"
"v_and_b32 v57, s73, v124\n"
"v_mfma_f32_16x16x32_bf16 v[28:31], v[104:107], a[120:123], v[28:31]\n"
"ds_read_b64_tr_b16 v[222:223], v232 offset:59392\n"
"v_lshlrev_b32 v56, 16, v124\n"
"v_mfma_f32_16x16x32_bf16 v[28:31], v[100:103], a[116:119], v[28:31]\n"
"ds_read_b64_tr_b16 v[180:181], v228 offset:32768\n"
"v_lshlrev_b32 v205, 16, v129\n"
"v_mfma_f32_16x16x32_bf16 v[28:31], v[96:99], a[112:115], v[28:31]\n"
"s_waitcnt vmcnt(7)\n"
"v_and_b32 v59, s73, v125\n"
"v_lshlrev_b32 v206, 16, v130\n"
"v_mfma_f32_16x16x32_bf16 v[44:47], v[96:99], a[128:131], v[44:47]\n"
"ds_read_b64_tr_b16 v[220:221], v232 offset:57344\n"
"v_lshlrev_b32 v204, 16, v128\n"
"v_mfma_f32_16x16x32_bf16 v[44:47], v[100:103], a[132:135], v[44:47]\n"
"ds_read_b64_tr_b16 v[208:209], v228 offset:49152\n"
"v_and_b32 v130, s73, v130\n"
"v_mfma_f32_16x16x32_bf16 v[44:47], v[104:107], a[136:139], v[44:47]\n"
"s_waitcnt vmcnt(6)\n"
"ds_read_b64_tr_b16 v[102:103], v233 offset:34816\n"
"v_add_f32 v58, v58, v205\n"
"v_mfma_f32_16x16x32_bf16 v[44:47], v[108:111], a[140:143], v[44:47]\n"
"v_lshlrev_b32 v207, 16, v131\n"
"v_add_f32 v62, v62, v207\n"
"v_mfma_f32_16x16x32_bf16 v[44:47], v[112:115], a[176:179], v[44:47]\n"
"ds_read_b64_tr_b16 v[218:219], v228 offset:59392\n"
"v_and_b32 v131, s73, v131\n"
"v_mfma_f32_16x16x32_bf16 v[44:47], v[116:119], a[180:183], v[44:47]\n"
"s_waitcnt vmcnt(5)\n"
"ds_read_b64_tr_b16 v[96:97], v229 offset:32768\n"
"v_lshlrev_b32 v205, 16, v135\n"
"v_mfma_f32_16x16x32_bf16 v[44:47], v[120:123], a[184:187], v[44:47]\n"
"v_lshlrev_b32 v202, 16, v132\n"
"v_lshlrev_b32 v207, 16, v137\n"
"v_mfma_f32_16x16x32_bf16 v[44:47], v[224:227], a[188:191], v[44:47]\n"
"s_waitcnt lgkmcnt(5)\n"
"ds_read_b64_tr_b16 v[120:121], v229 offset:57344\n"
"v_add_f32 v56, v56, v204\n"
"v_mfma_f32_16x16x32_bf16 v[0:3], v[180:183], a[48:51], v[0:3]\n"
"s_waitcnt vmcnt(4)\n"
"ds_read_b64_tr_b16 v[116:117], v233 offset:49152\n"
"v_add_f32 v61, v61, v130\n"
"v_mfma_f32_16x16x32_bf16 v[0:3], v[184:187], a[52:55], v[0:3]\n"
"v_lshlrev_b32 v60, 16, v126\n"
"v_lshlrev_b32 v204, 16, v134\n"
"v_mfma_f32_16x16x32_bf16 v[0:3], v[188:191], a[56:59], v[0:3]\n"
"ds_read_b64_tr_b16 v[110:111], v233 offset:43008\n"
"v_add_f32 v56, v56, v202\n"
"v_mfma_f32_16x16x32_bf16 v[0:3], v[192:195], a[60:63], v[0:3]\n"
"s_waitcnt lgkmcnt(5)\n"
"s_waitcnt vmcnt(3)\n"
"ds_read_b64_tr_b16 v[112:113], v229 offset:49152\n"
"v_and_b32 v63, s73, v127\n"
"v_mfma_f32_16x16x32_bf16 v[0:3], v[208:211], a[0:3], v[0:3]\n"
"ds_read_b64_tr_b16 v[224:225], v233 offset:57344\n"
"v_and_b32 v134, s73, v134\n"
"v_mfma_f32_16x16x32_bf16 v[0:3], v[212:215], a[4:7], v[0:3]\n"
"s_waitcnt lgkmcnt(5)\n"
"s_waitcnt vmcnt(2)\n"
"ds_read_b64_tr_b16 v[108:109], v233 offset:40960\n"
"v_and_b32 v129, s73, v129\n"
"v_mfma_f32_16x16x32_bf16 v[0:3], v[216:219], a[8:11], v[0:3]\n"
"v_add_f32 v60, v60, v206\n"
"v_add_f32 v60, v60, v204\n"
"v_mfma_f32_16x16x32_bf16 v[0:3], v[220:223], a[12:15], v[0:3]\n"
"ds_read_b64_tr_b16 v[98:99], v229 offset:34816\n"
"ds_read_b64_tr_b16 v[122:123], v229 offset:59392\n"
"v_mfma_f32_16x16x32_bf16 v[16:19], v[220:223], a[28:31], v[16:19]\n"
"v_mov_b32 v165, 0\n"
"v_add_f32 v59, v59, v129\n"
"v_mfma_f32_16x16x32_bf16 v[16:19], v[216:219], a[24:27], v[16:19]\n"
"v_add_f32 v62, v62, v205\n"
"v_lshlrev_b32 v206, 16, v136\n"
"v_mfma_f32_16x16x32_bf16 v[16:19], v[212:215], a[20:23], v[16:19]\n"
"ds_read_b64_tr_b16 v[104:105], v229 offset:40960\n"
"ds_read_b64_tr_b16 v[226:227], v233 offset:59392\n"
"v_mfma_f32_16x16x32_bf16 v[16:19], v[208:211], a[16:19], v[16:19]\n"
"v_add_f32 v56, v56, v206\n"
"v_lshlrev_b32 v202, 16, v138\n"
"v_mfma_f32_16x16x32_bf16 v[16:19], v[192:195], a[76:79], v[16:19]\n"
"s_waitcnt vmcnt(0)\n"
"ds_read_b64_tr_b16 v[118:119], v233 offset:51200\n"
"v_subrev_f32 v2, s22, v2\n"
"v_mfma_f32_16x16x32_bf16 v[16:19], v[188:191], a[72:75], v[16:19]\n"
"v_subrev_u32 v200, s21, v200\n"
"ds_read_b64_tr_b16 v[114:115], v229 offset:51200\n"
"v_mfma_f32_16x16x32_bf16 v[16:19], v[184:187], a[68:71], v[16:19]\n"
"v_cmp_lt_u32 vcc, s69, v200\n"
"ds_read_b64_tr_b16 v[100:101], v233 offset:32768\n"
"v_mfma_f32_16x16x32_bf16 v[16:19], v[180:183], a[64:67], v[16:19]\n"
"v_lshlrev_b32 v206, 16, v142\n"
"s_cbranch_vccnz LGR_%=\nLGRK_%=:\n"
"v_mfma_f32_16x16x32_bf16 v[32:35], v[180:183], a[80:83], v[32:35]\n"
"v_subrev_f32 v3, s22, v3\n"
"s_cmp_eq_u32 s93, 0\ns_cbranch_scc1 LEP_%=\nLEPK_%=:\n"
"v_mfma_f32_16x16x32_bf16 v[32:35], v[184:187], a[84:87], v[32:35]\n"
"s_bfe_u32 s20, s89, 65536\ns_lshl_b32 s20, s20, 13\ns_add_u32 s20, s20, 65536\n"
"ds_read_b64_tr_b16 v[106:107], v229 offset:43008\n"
"v_and_b32 v128, s73, v128\n"
"v_mfma_f32_16x16x32_bf16 v[32:35], v[188:191], a[88:91], v[32:35]\n"
"buffer_load_dwordx4 v[72:75], v236, s[32:35], s20 offen sc1\n"
"v_mfma_f32_16x16x32_bf16 v[32:35], v[192:195], a[92:95], v[32:35]\n"
"v_lshlrev_b32 v203, 16, v133\n"
"v_lshlrev_b32 v205, 16, v141\n"
"v_mfma_f32_16x16x32_bf16 v[32:35], v[208:211], a[32:35], v[32:35]\n"
"s_bfe_u32 s20, s89, 65540\ns_lshl_b32 s20, s20, 13\ns_add_u32 s20, s20, 1114112\n"
"v_lshlrev_b32 v204, 16, v140\n"
"v_add_f32 v56, v56, v204\n"
"v_mfma_f32_16x16x32_bf16 v[32:35], v[212:215], a[36:39], v[32:35]\n"
"buffer_load_dwordx4 v[76:79], v236, s[32:35], s20 offen sc1\n"
"v_mfma_f32_16x16x32_bf16 v[32:35], v[216:219], a[40:43], v[32:35]\n"
"v_and_b32 v136, s73, v136\n"
"v_subrev_f32 v17, s22, v17\n"
"v_mfma_f32_16x16x32_bf16 v[32:35], v[220:223], a[44:47], v[32:35]\n"
"s_bfe_u32 s20, s89, 65544\ns_lshl_b32 s20, s20, 13\ns_add_u32 s20, s20, 2162688\n"
"v_add_f32 v61, v61, v134\n"
"v_and_b32 v141, s73, v141\n"
"v_mfma_f32_16x16x32_bf16 v[48:51], v[220:223], a[228:231], v[48:51]\n"
"buffer_load_dwordx4 v[80:83], v236, s[32:35], s20 offen sc1\n"
"v_mfma_f32_16x16x32_bf16 v[48:51], v[216:219], a[224:227], v[48:51]\n"
"v_add_f32 v58, v58, v203\n"
"v_subrev_f32 v19, s22, v19\n"
"v_mfma_f32_16x16x32_bf16 v[48:51], v[212:215], a[212:215], v[48:51]\n"
"s_bfe_u32 s20, s89, 65548\ns_lshl_b32 s20, s20, 13\ns_add_u32 s20, s20, 3211264\n"
"v_and_b32 v140, s73, v140\n"
"v_lshlrev_b32 v204, 16, v146\n"
"v_mfma_f32_16x16x32_bf16 v[48:51], v[208:211], a[208:211], v[48:51]\n"
"buffer_load_dwordx4 v[84:87], v236, s[32:35], s20 offen sc1\n"
"v_mfma_f32_16x16x32_bf16 v[48:51], v[192:195], a[236:239], v[48:51]\n"
"v_add_f32 v57, v57, v128\n"
"v_subrev_f32 v34, s22, v34\n"
"v_mfma_f32_16x16x32_bf16 v[48:51], v[188:191], a[232:235], v[48:51]\n"
"s_bfe_u32 s20, s89, 65552\ns_lshl_b32 s20, s20, 13\ns_add_u32 s20, s20, 4259840\n"
"ds_read_b64_tr_b16 v[214:215], v234 offset:51200\n"
"v_and_b32 v132, s73, v132\n"
"v_mfma_f32_16x16x32_bf16 v[48:51], v[184:187], a[220:223], v[48:51]\n"
"buffer_load_dwordx4 v[88:91], v236, s[32:35], s20 offen sc1\n"
"v_mfma_f32_16x16x32_bf16 v[48:51], v[180:183], a[216:219], v[48:51]\n"
"s_waitcnt lgkmcnt(1)\n"
"v_add_f32 v60, v60, v202\n"
"v_add_f32 v60, v60, v206\n"
"v_mfma_f32_16x16x32_bf16 v[4:7], v[96:99], a[48:51], v[4:7]\n"
"s_bfe_u32 s20, s89, 65556\ns_lshl_b32 s20, s20, 13\ns_add_u32 s20, s20, 5308416\n"
"v_lshlrev_b32 v206, 16, v148\n"
"v_subrev_f32 v35, s22, v35\n"
"v_mfma_f32_16x16x32_bf16 v[4:7], v[100:103], a[52:55], v[4:7]\n"
"buffer_load_dwordx4 v[92:95], v236, s[32:35], s20 offen sc1\n"
"v_mfma_f32_16x16x32_bf16 v[4:7], v[104:107], a[56:59], v[4:7]\n"
"v_add_f32 v60, v60, v204\n"
"v_subrev_f32 v0, s22, v0\n"
"v_mfma_f32_16x16x32_bf16 v[4:7], v[108:111], a[60:63], v[4:7]\n"
"s_bfe_u32 s20, s89, 65560\ns_lshl_b32 s20, s20, 13\ns_add_u32 s20, s20, 6356992\n"
"v_add_f32 v57, v57, v132\n"
"v_subrev_f32 v33, s22, v33\n"
"v_mfma_f32_16x16x32_bf16 v[4:7], v[112:115], a[0:3], v[4:7]\n"
"buffer_load_dwordx4 v[244:247], v236, s[32:35], s20 offen sc1\n"
"v_mfma_f32_16x16x32_bf16 v[4:7], v[116:119], a[4:7], v[4:7]\n"
"v_and_b32 v138, s73, v138\n"
"v_subrev_f32 v18, s22, v18\n"
"v_mfma_f32_16x16x32_bf16 v[4:7], v[120:123], a[8:11], v[4:7]\n"
"s_bfe_u32 s20, s89, 65564\ns_lshl_b32 s20, s20, 13\ns_add_u32 s20, s20, 7405568\n"
"v_and_b32 v135, s73, v135\n"
"v_subrev_f32 v49, s22, v49\n"
"v_mfma_f32_16x16x32_bf16 v[4:7], v[224:227], a[12:15], v[4:7]\n"
"buffer_load_dwordx4 v[156:159], v236, s[32:35], s20 offen sc1\n"
"v_add_f32 v58, v58, v207\n"
"v_lshlrev_b32 v203, 16, v139\n"
"v_add_f32 v58, v58, v205\n"
"v_and_b32 v142, s73, v142\n"
"v_lshlrev_b32 v202, 16, v144\n"
"v_add_f32 v56, v56, v202\n"
"v_add_f32 v56, v56, v206\n"
"v_subrev_f32 v1, s22, v1\n"
"v_mfma_f32_16x16x32_bf16 v[20:23], v[224:227], a[28:31], v[20:23]\n"
"v_add_f32 v63, v63, v131\n"
"v_lshlrev_b32 v204, 16, v152\n"
"v_mfma_f32_16x16x32_bf16 v[20:23], v[120:123], a[24:27], v[20:23]\n"
"v_and_b32 v133, s73, v133\n"
"v_add_f32 v59, v59, v133\n"
"v_mfma_f32_16x16x32_bf16 v[20:23], v[116:119], a[20:23], v[20:23]\n"
"ds_read_b64_tr_b16 v[218:219], v230 offset:59392\n"
"v_and_b32 v137, s73, v137\n"
"v_mfma_f32_16x16x32_bf16 v[20:23], v[112:115], a[16:19], v[20:23]\n"
"v_and_b32 v148, s73, v148\n"
"v_subrev_f32 v16, s22, v16\n"
"v_mfma_f32_16x16x32_bf16 v[20:23], v[108:111], a[76:79], v[20:23]\n"
"v_add_f32 v62, v62, v203\n"
"v_subrev_f32 v6, s22, v6\n"
"v_mfma_f32_16x16x32_bf16 v[20:23], v[104:107], a[72:75], v[20:23]\n"
"v_add_f32 v57, v57, v136\n"
"v_lshlrev_b32 v205, 16, v147\n"
"v_mfma_f32_16x16x32_bf16 v[20:23], v[100:103], a[68:71], v[20:23]\n"
"ds_read_b64_tr_b16 v[194:195], v234 offset:43008\n"
"v_subrev_f32 v32, s22, v32\n"
"v_mfma_f32_16x16x32_bf16 v[20:23], v[96:99], a[64:67], v[20:23]\n"
"ds_read_b64_tr_b16 v[184:185], v234 offset:32768\n"
"v_add_f32 v59, v59, v137\n"
"v_and_b32 v139, s73, v139\n"
"v_add_f32 v59, v59, v141\n"
"v_lshlrev_b32 v207, 16, v143\n"
"v_lshlrev_b32 v203, 16, v145\n"
"v_and_b32 v145, s73, v145\n"
"v_add_f32 v58, v58, v203\n"
"v_add_f32 v59, v59, v145\n"
"v_and_b32 v147, s73, v147\n"
"v_lshlrev_b32 v203, 16, v151\n"
"v_subrev_f32 v4, s22, v4\n"
"v_subrev_f32 v5, s22, v5\n"
"v_subrev_f32 v7, s22, v7\n"
"v_cvt_pk_bf16_f32 v0, v0, v1\n"
"v_cvt_pk_bf16_f32 v1, v2, v3\n"
"v_cvt_pk_bf16_f32 v3, v6, v7\n"
"v_mfma_f32_16x16x32_bf16 v[36:39], v[96:99], a[80:83], v[36:39]\n"
"v_add_f32 v56, v56, v204\n"
"v_cvt_pk_bf16_f32 v2, v4, v5\n"
"v_mfma_f32_16x16x32_bf16 v[36:39], v[100:103], a[84:87], v[36:39]\n"
"ds_read_b64_tr_b16 v[192:193], v234 offset:40960\n"
"v_add_f32 v61, v61, v138\n"
"v_mfma_f32_16x16x32_bf16 v[36:39], v[104:107], a[88:91], v[36:39]\n"
"v_add_f32 v57, v57, v140\n"
"v_subrev_f32 v23, s22, v23\n"
"v_mfma_f32_16x16x32_bf16 v[36:39], v[108:111], a[92:95], v[36:39]\n"
"ds_read_b64_tr_b16 v[180:181], v230 offset:32768\n"
"v_and_b32 v144, s73, v144\n"
"v_mfma_f32_16x16x32_bf16 v[36:39], v[112:115], a[32:35], v[36:39]\n"
"ds_read_b64_tr_b16 v[210:211], v230 offset:51200\n"
"v_add_f32 v62, v62, v207\n"
"v_add_f32 v62, v62, v205\n"
"v_lshlrev_b32 v207, 16, v149\n"
"v_add_f32 v58, v58, v207\n"
"v_lshlrev_b32 v202, 16, v150\n"
"v_permlane16_swap_b32 v0, v2\n"
"v_permlane16_swap_b32 v1, v3\n"
"v_subrev_f32 v20, s22, v20\n"
"v_cvt_pk_bf16_f32 v16, v16, v17\n"
"v_mfma_f32_16x16x32_bf16 v[36:39], v[116:119], a[36:39], v[36:39]\n"
"ds_read_b64_tr_b16 v[186:187], v234 offset:34816\n"
"v_add_f32 v63, v63, v135\n"
"v_mfma_f32_16x16x32_bf16 v[36:39], v[120:123], a[40:43], v[36:39]\n"
"ds_read_b64_tr_b16 v[188:189], v230 offset:40960\n"
"buffer_store_dwordx4 v[0:3], v238, s[36:39], 0 offen sc0 sc1\n"
"v_and_b32 v143, s73, v143\n"
"v_and_b32 v146, s73, v146\n"
"v_and_b32 v149, s73, v149\n"
"v_lshlrev_b32 v205, 16, v153\n"
"v_and_b32 v153, s73, v153\n"
"v_lshlrev_b32 v206, 16, v154\n"
"v_lshlrev_b32 v207, 16, v155\n"
"v_subrev_f32 v21, s22, v21\n"
"v_subrev_f32 v22, s22, v22\n"
"v_cvt_pk_bf16_f32 v17, v18, v19\n"
"v_cvt_pk_bf16_f32 v18, v20, v21\n"
"v_cvt_pk_bf16_f32 v32, v32, v33\n"
"v_mfma_f32_16x16x32_bf16 v[36:39], v[224:227], a[44:47], v[36:39]\n"
"ds_read_b64_tr_b16 v[182:183], v230 offset:34816\n"
"v_add_f32 v63, v63, v139\n"
"v_add_f32 v61, v61, v142\n"
"v_add_f32 v57, v57, v144\n"
"v_add_f32 v57, v57, v148\n"
"v_add_f32 v59, v59, v149\n"
"v_cvt_pk_bf16_f32 v19, v22, v23\n"
"v_permlane16_swap_b32 v16, v18\n"
"v_cvt_pk_bf16_f32 v33, v34, v35\n"
"v_mfma_f32_16x16x32_bf16 v[52:55], v[224:227], a[228:231], v[52:55]\n"
"ds_read_b64_tr_b16 v[190:191], v230 offset:43008\n"
"v_add_f32 v59, v59, v153\n"
"v_mfma_f32_16x16x32_bf16 v[52:55], v[120:123], a[224:227], v[52:55]\n"
"v_permlane16_swap_b32 v17, v19\n"
"v_mfma_f32_16x16x32_bf16 v[52:55], v[116:119], a[212:215], v[52:55]\n"
"ds_read_b64_tr_b16 v[208:209], v230 offset:49152\n"
"v_subrev_f32 v36, s22, v36\n"
"v_mfma_f32_16x16x32_bf16 v[52:55], v[112:115], a[208:211], v[52:55]\n"
"ds_read_b64_tr_b16 v[222:223], v234 offset:59392\n"
"buffer_store_dwordx4 v[16:19], v238, s[36:39], s52 offen sc0 sc1\n"
"v_add_f32 v63, v63, v143\n"
"v_add_f32 v60, v60, v202\n"
"v_add_f32 v62, v62, v203\n"
"v_and_b32 v152, s73, v152\n"
"v_subrev_f32 v38, s22, v38\n"
"v_subrev_f32 v39, s22, v39\n"
"v_mfma_f32_16x16x32_bf16 v[52:55], v[108:111], a[236:239], v[52:55]\n"
"v_and_b32 v155, s73, v155\n"
"v_cvt_pk_bf16_f32 v35, v38, v39\n"
"v_mfma_f32_16x16x32_bf16 v[52:55], v[104:107], a[232:235], v[52:55]\n"
"ds_read_b64_tr_b16 v[216:217], v230 offset:57344\n"
"v_subrev_f32 v37, s22, v37\n"
"v_mfma_f32_16x16x32_bf16 v[52:55], v[100:103], a[220:223], v[52:55]\n"
"v_and_b32 v150, s73, v150\n"
"v_and_b32 v154, s73, v154\n"
"v_mfma_f32_16x16x32_bf16 v[52:55], v[96:99], a[216:219], v[52:55]\n"
"s_waitcnt lgkmcnt(4)\n"
"ds_read_b64_tr_b16 v[212:213], v234 offset:49152\n"
"ds_read_b64_tr_b16 v[220:221], v234 offset:57344\n"
"v_add_f32 v63, v63, v147\n"
"v_and_b32 v151, s73, v151\n"
"v_add_f32 v63, v63, v151\n"
"v_add_f32 v57, v57, v152\n"
"v_add_f32 v58, v58, v205\n"
"v_add_f32 v60, v60, v206\n"
"v_add_f32 v62, v62, v207\n"
"v_cvt_pk_bf16_f32 v34, v36, v37\n"
"v_permlane16_swap_b32 v33, v35\n"
"v_mfma_f32_16x16x32_bf16 v[8:11], v[180:183], a[48:51], v[8:11]\n"
"v_cvt_pk_bf16_f32 v56, v56, v57\n"
"v_cvt_pk_bf16_f32 v57, v58, v59\n"
"v_mfma_f32_16x16x32_bf16 v[8:11], v[184:187], a[52:55], v[8:11]\n"
"s_waitcnt lgkmcnt(5)\n"
"v_permlane16_swap_b32 v32, v34\n"
"v_mfma_f32_16x16x32_bf16 v[8:11], v[188:191], a[56:59], v[8:11]\n"
"buffer_store_dwordx4 v[32:35], v238, s[36:39], s53 offen sc0 sc1\n"
"ds_read_b64_tr_b16 v[120:121], v231 offset:57344\n"
"v_add_f32 v61, v61, v146\n"
"v_add_f32 v61, v61, v150\n"
"v_add_f32 v61, v61, v154\n"
"v_add_f32 v63, v63, v155\n"
"v_cvt_pk_bf16_f32 v58, v60, v61\n"
"v_cvt_pk_bf16_f32 v59, v62, v63\n"
"v_mfma_f32_16x16x32_bf16 v[8:11], v[192:195], a[60:63], v[8:11]\n"
"s_waitcnt lgkmcnt(4)\n"
"v_permlane16_swap_b32 v57, v59\n"
"v_mfma_f32_16x16x32_bf16 v[8:11], v[208:211], a[0:3], v[8:11]\n"
"s_waitcnt lgkmcnt(2)\n"
"v_permlane16_swap_b32 v56, v58\n"
"v_mfma_f32_16x16x32_bf16 v[8:11], v[212:215], a[4:7], v[8:11]\n"
"buffer_store_dwordx4 v[56:59], v253, s[36:39], 0 offen sc0 sc1\n"
"v_subrev_f32 v48, s22, v48\n"
"v_subrev_f32 v50, s22, v50\n"
"v_subrev_f32 v51, s22, v51\n"
"v_mfma_f32_16x16x32_bf16 v[8:11], v[216:219], a[8:11], v[8:11]\n"
"s_waitcnt lgkmcnt(1)\n"
"ds_read_b64_tr_b16 v[112:113], v231 offset:49152\n"
"v_subrev_f32 v54, s22, v54\n"
"v_mfma_f32_16x16x32_bf16 v[8:11], v[220:223], a[12:15], v[8:11]\n"
"ds_read_b64_tr_b16 v[100:101], v235 offset:32768\n"
"v_subrev_f32 v52, s22, v52\n"
"v_mfma_f32_16x16x32_bf16 v[24:27], v[220:223], a[28:31], v[24:27]\n"
"ds_read_b64_tr_b16 v[102:103], v235 offset:34816\n"
"v_subrev_f32 v53, s22, v53\n"
"v_mfma_f32_16x16x32_bf16 v[24:27], v[216:219], a[24:27], v[24:27]\n"
"v_cvt_pk_bf16_f32 v48, v48, v49\n"
"v_cvt_pk_bf16_f32 v49, v50, v51\n"
"v_mfma_f32_16x16x32_bf16 v[24:27], v[212:215], a[20:23], v[24:27]\n"
"ds_read_b64_tr_b16 v[96:97], v231 offset:32768\n"
"v_cvt_pk_bf16_f32 v50, v52, v53\n"
"v_mfma_f32_16x16x32_bf16 v[24:27], v[208:211], a[16:19], v[24:27]\n"
"s_nop 0\n"
"v_permlane16_swap_b32 v48, v50\n"
"v_mfma_f32_16x16x32_bf16 v[24:27], v[192:195], a[76:79], v[24:27]\n"
"ds_read_b64_tr_b16 v[116:117], v235 offset:49152\n"
"v_subrev_f32 v55, s22, v55\n"
"v_mfma_f32_16x16x32_bf16 v[24:27], v[188:191], a[72:75], v[24:27]\n"
"ds_read_b64_tr_b16 v[98:99], v231 offset:34816\n"
"v_cvt_pk_bf16_f32 v51, v54, v55\n"
"v_mfma_f32_16x16x32_bf16 v[24:27], v[184:187], a[68:71], v[24:27]\n"
"s_nop 0\n"
"v_permlane16_swap_b32 v49, v51\n"
"v_mfma_f32_16x16x32_bf16 v[24:27], v[180:183], a[64:67], v[24:27]\n"
"buffer_store_dwordx4 v[48:51], v252, s[36:39], 0 offen sc0 sc1\n"
"ds_read_b64_tr_b16 v[104:105], v231 offset:40960\n"
"v_mfma_f32_16x16x32_bf16 v[40:43], v[180:183], a[80:83], v[40:43]\n"
"ds_read_b64_tr_b16 v[106:107], v231 offset:43008\n"
"ds_read_b64_tr_b16 v[108:109], v235 offset:40960\n"
"v_mfma_f32_16x16x32_bf16 v[40:43], v[184:187], a[84:87], v[40:43]\n"
"ds_read_b64_tr_b16 v[110:111], v235 offset:43008\n"
"v_mfma_f32_16x16x32_bf16 v[40:43], v[188:191], a[88:91], v[40:43]\n"
"ds_read_b64_tr_b16 v[114:115], v231 offset:51200\n"
"v_subrev_f32 v10, s22, v10\n"
"v_mfma_f32_16x16x32_bf16 v[40:43], v[192:195], a[92:95], v[40:43]\n"
"ds_read_b64_tr_b16 v[118:119], v235 offset:51200\n"
"v_mfma_f32_16x16x32_bf16 v[40:43], v[208:211], a[32:35], v[40:43]\n"
"ds_read_b64_tr_b16 v[122:123], v231 offset:59392\n"
"v_subrev_f32 v24, s22, v24\n"
"v_mfma_f32_16x16x32_bf16 v[40:43], v[212:215], a[36:39], v[40:43]\n"
"ds_read_b64_tr_b16 v[224:225], v235 offset:57344\n"
"ds_read_b64_tr_b16 v[226:227], v235 offset:59392\n"
"v_mfma_f32_16x16x32_bf16 v[40:43], v[216:219], a[40:43], v[40:43]\n"
"v_mfma_f32_16x16x32_bf16 v[40:43], v[220:223], a[44:47], v[40:43]\n"
"s_waitcnt lgkmcnt(8)\n"
"v_mov_b32 v160, 0\n"
"v_subrev_f32 v11, s22, v11\n"
"v_mfma_f32_16x16x32_bf16 v[12:15], v[96:99], a[48:51], v[12:15]\n"
"v_subrev_f32 v8, s22, v8\n"
"v_subrev_f32 v9, s22, v9\n"
"v_mfma_f32_16x16x32_bf16 v[12:15], v[100:103], a[52:55], v[12:15]\n"
"s_waitcnt lgkmcnt(5)\n"
"v_cvt_pk_bf16_f32 v8, v8, v9\n"
"v_mfma_f32_16x16x32_bf16 v[12:15], v[104:107], a[56:59], v[12:15]\n"
"v_subrev_f32 v27, s22, v27\n"
"v_mfma_f32_16x16x32_bf16 v[12:15], v[108:111], a[60:63], v[12:15]\n"
"s_waitcnt lgkmcnt(3)\n"
"v_mfma_f32_16x16x32_bf16 v[12:15], v[112:115], a[0:3], v[12:15]\n"
"v_mfma_f32_16x16x32_bf16 v[12:15], v[116:119], a[4:7], v[12:15]\n"
"s_waitcnt lgkmcnt(0)\n"
"v_mfma_f32_16x16x32_bf16 v[12:15], v[120:123], a[8:11], v[12:15]\n"
"v_subrev_f32 v40, s22, v40\n"
"v_mfma_f32_16x16x32_bf16 v[12:15], v[224:227], a[12:15], v[12:15]\n"
"s_waitcnt vmcnt(5)\n"
"v_mfma_f32_16x16x32_bf16 v[64:67], v[162:165], v[72:75], 0\n"
"v_mfma_f32_16x16x32_bf16 v[64:67], v[162:165], v[76:79], v[64:67]\n"
"v_cvt_pk_bf16_f32 v9, v10, v11\n"
"v_mfma_f32_16x16x32_bf16 v[64:67], v[162:165], v[80:83], v[64:67]\n"
"s_nop 2\n"
"v_subrev_f32 v12, s22, v12\n"
"v_mfma_f32_16x16x32_bf16 v[64:67], v[162:165], v[84:87], v[64:67]\n"
"v_subrev_f32 v25, s22, v25\n"
"v_mfma_f32_16x16x32_bf16 v[64:67], v[162:165], v[88:91], v[64:67]\n"
"v_subrev_f32 v13, s22, v13\n"
"v_mfma_f32_16x16x32_bf16 v[64:67], v[162:165], v[92:95], v[64:67]\n"
"v_subrev_f32 v15, s22, v15\n"
"v_mfma_f32_16x16x32_bf16 v[64:67], v[162:165], v[244:247], v[64:67]\n"
"v_subrev_f32 v14, s22, v14\n"
"v_mfma_f32_16x16x32_bf16 v[64:67], v[162:165], v[156:159], v[64:67]\n"
"v_subrev_f32 v42, s22, v42\n"
"v_mfma_f32_16x16x32_bf16 v[68:71], v[160:163], v[72:75], 0\n"
"v_cvt_pk_bf16_f32 v11, v14, v15\n"
"v_subrev_f32 v41, s22, v41\n"
"v_mfma_f32_16x16x32_bf16 v[68:71], v[160:163], v[76:79], v[68:71]\n"
"v_cvt_pk_bf16_f32 v10, v12, v13\n"
"v_cvt_pk_bf16_f32 v24, v24, v25\n"
"v_mfma_f32_16x16x32_bf16 v[68:71], v[160:163], v[80:83], v[68:71]\n"
"v_mfma_f32_16x16x32_bf16 v[68:71], v[160:163], v[84:87], v[68:71]\n"
"v_permlane16_swap_b32 v9, v11\n"
"v_mfma_f32_16x16x32_bf16 v[68:71], v[160:163], v[88:91], v[68:71]\n"
"v_mfma_f32_16x16x32_bf16 v[68:71], v[160:163], v[92:95], v[68:71]\n"
"v_cvt_pk_bf16_f32 v64, v64, v65\n"
"v_cvt_pk_bf16_f32 v40, v40, v41\n"
"v_mfma_f32_16x16x32_bf16 v[68:71], v[160:163], v[244:247], v[68:71]\n"
"v_permlane16_swap_b32 v8, v10\n"
"v_mfma_f32_16x16x32_bf16 v[68:71], v[160:163], v[156:159], v[68:71]\n"
"buffer_store_dwordx4 v[8:11], v239, s[36:39], 0 offen sc0 sc1\n"
"v_mfma_f32_16x16x32_bf16 v[28:31], v[224:227], a[28:31], v[28:31]\n"
"v_mfma_f32_16x16x32_bf16 v[28:31], v[120:123], a[24:27], v[28:31]\n"
"v_cvt_pk_bf16_f32 v65, v66, v67\n"
"v_subrev_f32 v43, s22, v43\n"
"v_mfma_f32_16x16x32_bf16 v[28:31], v[116:119], a[20:23], v[28:31]\n"
"v_mfma_f32_16x16x32_bf16 v[28:31], v[112:115], a[16:19], v[28:31]\n"
"v_mfma_f32_16x16x32_bf16 v[28:31], v[108:111], a[76:79], v[28:31]\n"
"v_cvt_pk_bf16_f32 v66, v68, v69\n"
"v_cvt_pk_bf16_f32 v67, v70, v71\n"
"v_mfma_f32_16x16x32_bf16 v[28:31], v[104:107], a[72:75], v[28:31]\n"
"v_permlane16_swap_b32 v64, v66\n"
"v_mfma_f32_16x16x32_bf16 v[28:31], v[100:103], a[68:71], v[28:31]\n"
"v_permlane16_swap_b32 v65, v67\n"
"v_mfma_f32_16x16x32_bf16 v[28:31], v[96:99], a[64:67], v[28:31]\n"
"buffer_store_dwordx4 v[64:67], v253, s[36:39], 0 offen offset:128 sc0 sc1\n"
"v_cvt_pk_bf16_f32 v41, v42, v43\n"
"v_mfma_f32_16x16x32_bf16 v[44:47], v[96:99], a[80:83], v[44:47]\n"
"v_subrev_f32 v26, s22, v26\n"
"v_cvt_pk_bf16_f32 v25, v26, v27\n"
"v_mfma_f32_16x16x32_bf16 v[44:47], v[100:103], a[84:87], v[44:47]\n"
"v_mfma_f32_16x16x32_bf16 v[44:47], v[104:107], a[88:91], v[44:47]\n"
"s_nop 0\n"
"v_subrev_f32 v30, s22, v30\n"
"v_subrev_f32 v31, s22, v31\n"
"v_mfma_f32_16x16x32_bf16 v[44:47], v[108:111], a[92:95], v[44:47]\n"
"v_subrev_f32 v28, s22, v28\n"
"v_subrev_f32 v29, s22, v29\n"
"v_mfma_f32_16x16x32_bf16 v[44:47], v[112:115], a[32:35], v[44:47]\n"
"v_cvt_pk_bf16_f32 v26, v28, v29\n"
"v_cvt_pk_bf16_f32 v27, v30, v31\n"
"v_mfma_f32_16x16x32_bf16 v[44:47], v[116:119], a[36:39], v[44:47]\n"
"s_nop 0\n"
"v_permlane16_swap_b32 v25, v27\n"
"v_mfma_f32_16x16x32_bf16 v[44:47], v[120:123], a[40:43], v[44:47]\n"
"v_permlane16_swap_b32 v24, v26\n"
"v_mfma_f32_16x16x32_bf16 v[44:47], v[224:227], a[44:47], v[44:47]\n"
"buffer_store_dwordx4 v[24:27], v239, s[36:39], s52 offen sc0 sc1\n"
"s_nop 6\n"
"v_subrev_f32 v44, s22, v44\n"
"v_subrev_f32 v45, s22, v45\n"
"v_subrev_f32 v46, s22, v46\n"
"v_subrev_f32 v47, s22, v47\n"
"v_cvt_pk_bf16_f32 v42, v44, v45\n"
"v_cvt_pk_bf16_f32 v43, v46, v47\n"
"s_nop 0\n"
"v_permlane16_swap_b32 v40, v42\n"
"v_permlane16_swap_b32 v41, v43\n"
"buffer_store_dwordx4 v[40:43], v239, s[36:39], s53 offen sc0 sc1\n"
"s_branch LFIN_%=\n"
"LSW_%=:\n"
"s_add_u32 s16, s8, s95\n"
"s_addc_u32 s17, s9, 0\n"
"s_sub_u32 s18, 655360, s95\n"
"s_add_u32 s20, s8, s100\n"
"s_addc_u32 s21, s9, 0\n"
"s_sub_u32 s22, 655360, s100\n"
"s_mov_b32 s56, 0\n"
"s_mov_b32 s55, 0\n"
"s_mov_b32 s58, 0\n"
"s_mov_b32 s57, 0\n"
"s_mov_b32 s59, 163840\n"
"s_mov_b32 s52, 9216\n"
"s_mov_b32 s53, 18432\n"
"s_mov_b32 s54, 27648\n"
"v_lshrrev_b32 v206, 7, v242\n"
"v_mul_u32_u24 v206, 1280, v206\n"
"v_lshrrev_b32 v240, 3, v242\n"
"v_and_b32 v240, 0x60, v240\n"
"v_and_b32 v207, 0x70, v242\n"
"v_xor_b32 v240, v240, v207\n"
"v_add_u32 v240, v206, v240\n"
"v_mov_b32 v207, v240\n"
"buffer_load_dword v252, v240, s[16:19], 0 offen\n"
"s_branch LSWK_%=\n"
"LGP_%=:\n"
"s_mov_b32 s80, 0x40000\n"
"LGPa_%=:\n"
"s_sleep 1\n"
"global_load_dword v200, v201, s[6:7] sc1\n"
"s_sub_u32 s80, s80, 1\n"
"s_cmp_eq_u32 s80, 0\n"
"s_waitcnt vmcnt(0)\n"
"s_cbranch_scc1 LGPK_%=\n"
"v_subrev_u32 v200, s21, v200\n"
"v_cmp_lt_u32 vcc, s69, v200\n"
"s_nop 4\n"
"s_cbranch_vccnz LGPa_%=\n"
"s_branch LGPK_%=\n"
"LGQ_%=:\n"
"s_mov_b32 s80, 0x40000\n"
"LGQa_%=:\n"
"s_sleep 1\n"
"global_load_dword v200, v201, s[6:7] sc1\n"
"s_sub_u32 s80, s80, 1\n"
"s_cmp_eq_u32 s80, 0\n"
"s_waitcnt vmcnt(0)\n"
"s_cbranch_scc1 LGQK_%=\n"
"v_subrev_u32 v200, s21, v200\n"
"v_cmp_lt_u32 vcc, s69, v200\n"
"s_nop 4\n"
"s_cbranch_vccnz LGQa_%=\n"
"s_branch LGQK_%=\n"
"LGR_%=:\n"
"s_mov_b32 s80, 0x40000\n"
"LGRa_%=:\n"
"s_sleep 1\n"
"global_load_dword v200, v201, s[6:7] sc1\n"
"s_sub_u32 s80, s80, 1\n"
"s_cmp_eq_u32 s80, 0\n"
"s_waitcnt vmcnt(0)\n"
"s_cbranch_scc1 LGRK_%=\n"
"v_subrev_u32 v200, s21, v200\n"
"v_cmp_lt_u32 vcc, s69, v200\n"
"s_nop 4\n"
"s_cbranch_vccnz LGRa_%=\n"
"s_branch LGRK_%=\n"
"LGV_%=:\n"
"s_mov_b32 s80, 0x40000\n"
"LGVa_%=:\n"
"s_sleep 1\n"
"global_load_dword v200, v201, s[6:7] sc1\n"
"s_sub_u32 s80, s80, 1\n"
"s_cmp_eq_u32 s80, 0\n"
"s_waitcnt vmcnt(0)\n"
"s_cbranch_scc1 LGVK_%=\n"
"v_subrev_u32 v200, s21, v200\n"
"v_cmp_lt_u32 vcc, s69, v200\n"
"s_nop 4\n"
"s_cbranch_vccnz LGVa_%=\n"
"s_branch LGVK_%=\n"
"LEP_%=:\n"
"s_add_u32 s92, s92, 1\n"
"v_mov_b32 v200, s92\n"
"v_mov_b32 v255, 0\n"
"s_mov_b64 exec, 1\n"
"global_store_dword v255, v200, s[6:7] offset:128 sc1\n"
"s_mov_b64 exec, -1\n"
"s_branch LEPK_%=\n"
"LPCE0_%=:\n"
"s_memrealtime s[80:81]\n"
"s_sub_u32 s82, s67, s66\n"
"s_cmp_gt_i32 s82, 0\n"
"s_cselect_b32 s83, s67, s66\n"
"s_waitcnt lgkmcnt(0)\n"
"s_mov_b32 s63, 0\n"
"s_mov_b32 s62, 118\n"
"s_sub_u32 s82, s83, s80\n"
"s_cmp_gt_i32 s82, 3\n"
"s_cbranch_scc0 LPEE0_%=\n"
"LPDE0_%=:\n"
"s_sleep 1\n"
"s_memrealtime s[80:81]\n"
"s_waitcnt lgkmcnt(0)\n"
"s_sub_u32 s82, s83, s80\n"
"s_cmp_gt_i32 s82, 3\n"
"s_cbranch_scc1 LPDE0_%=\n"
"LPEE0_%=:\n"
"s_add_u32 s66, s66, s72\n"
"s_add_u32 s66, s66, 4\n"
"s_add_u32 s67, s80, s72\n"
"s_sub_u32 s72, s72, 3\n"
"s_max_u32 s72, s72, s62\n"
"s_branch LPKE0_%=\n"
"LPCE1_%=:\n"
"s_memrealtime s[80:81]\n"
"s_sub_u32 s82, s67, s66\n"
"s_cmp_gt_i32 s82, 0\n"
"s_cselect_b32 s83, s67, s66\n"
"s_waitcnt lgkmcnt(0)\n"
"s_add_u32 s72, s62, 16\n"
"s_sub_u32 s82, s83, s80\n"
"s_cmp_gt_i32 s82, 3\n"
"s_cbranch_scc0 LPEE1_%=\n"
"LPDE1_%=:\n"
"s_sleep 1\n"
"s_memrealtime s[80:81]\n"
"s_waitcnt lgkmcnt(0)\n"
"s_sub_u32 s82, s83, s80\n"
"s_cmp_gt_i32 s82, 3\n"
"s_cbranch_scc1 LPDE1_%=\n"
"LPEE1_%=:\n"
"s_add_u32 s66, s66, s72\n"
"s_add_u32 s66, s66, 4\n"
"s_add_u32 s67, s80, s72\n"
"s_sub_u32 s72, s72, 3\n"
"s_max_u32 s72, s72, s62\n"
"s_branch LPKE1_%=\n"
"LPCT0_%=:\n"
"s_memrealtime s[80:81]\n"
"s_sub_u32 s82, s67, s66\n"
"s_cmp_gt_i32 s82, 0\n"
"s_cselect_b32 s83, s67, s66\n"
"s_waitcnt lgkmcnt(0)\n"
"s_sub_u32 s82, s83, s80\n"
"s_cmp_gt_i32 s82, 3\n"
"s_cbranch_scc0 LPET0_%=\n"
"LPDT0_%=:\n"
"s_sleep 1\n"
"s_memrealtime s[80:81]\n"
"s_waitcnt lgkmcnt(0)\n"
"s_sub_u32 s82, s83, s80\n"
"s_cmp_gt_i32 s82, 3\n"
"s_cbranch_scc1 LPDT0_%=\n"
"LPET0_%=:\n"
"s_add_u32 s66, s66, s72\n"
"s_add_u32 s66, s66, 4\n"
"s_add_u32 s67, s80, s72\n"
"s_sub_u32 s72, s72, 3\n"
"s_max_u32 s72, s72, s62\n"
"s_branch LPKT0_%=\n"
"LPCT1_%=:\n"
"s_memrealtime s[80:81]\n"
"s_sub_u32 s82, s67, s66\n"
"s_cmp_gt_i32 s82, 0\n"
"s_cselect_b32 s83, s67, s66\n"
"s_waitcnt lgkmcnt(0)\n"
"s_add_u32 s72, s62, 16\n"
"s_sub_u32 s82, s83, s80\n"
"s_cmp_gt_i32 s82, 3\n"
"s_cbranch_scc0 LPET1_%=\n"
"LPDT1_%=:\n"
"s_sleep 1\n"
"s_memrealtime s[80:81]\n"
"s_waitcnt lgkmcnt(0)\n"
"s_sub_u32 s82, s83, s80\n"
"s_cmp_gt_i32 s82, 3\n"
"s_cbranch_scc1 LPDT1_%=\n"
"LPET1_%=:\n"
"s_add_u32 s66, s66, s72\n"
"s_add_u32 s66, s66, 4\n"
"s_add_u32 s67, s80, s72\n"
"s_sub_u32 s72, s72, 3\n"
"s_max_u32 s72, s72, s62\n"
"s_branch LPKT1_%=\n"
"LPCL0_%=:\n"
"s_memrealtime s[80:81]\n"
"s_sub_u32 s82, s67, s66\n"
"s_cmp_gt_i32 s82, 0\n"
"s_cselect_b32 s83, s67, s66\n"
"s_waitcnt lgkmcnt(0)\n"
"s_sub_u32 s82, s83, s80\n"
"s_cmp_gt_i32 s82, 3\n"
"s_cbranch_scc0 LPEL0_%=\n"
"LPDL0_%=:\n"
"s_sleep 1\n"
"s_memrealtime s[80:81]\n"
"s_waitcnt lgkmcnt(0)\n"
"s_sub_u32 s82, s83, s80\n"
"s_cmp_gt_i32 s82, 3\n"
"s_cbranch_scc1 LPDL0_%=\n"
"LPEL0_%=:\n"
"s_add_u32 s66, s66, s72\n"
"s_add_u32 s66, s66, 4\n"
"s_add_u32 s67, s80, s72\n"
"s_sub_u32 s72, s72, 3\n"
"s_max_u32 s72, s72, s62\n"
"s_branch LPKL0_%=\n"
"LPCL1_%=:\n"
"s_memrealtime s[80:81]\n"
"s_sub_u32 s82, s67, s66\n"
"s_cmp_gt_i32 s82, 0\n"
"s_cselect_b32 s83, s67, s66\n"
"s_waitcnt lgkmcnt(0)\n"
"s_sub_u32 s82, s83, s80\n"
"s_cmp_gt_i32 s82, 3\n"
"s_cbranch_scc0 LPEL1_%=\n"
"LPDL1_%=:\n"
"s_sleep 1\n"
"s_memrealtime s[80:81]\n"
"s_waitcnt lgkmcnt(0)\n"
"s_sub_u32 s82, s83, s80\n"
"s_cmp_gt_i32 s82, 3\n"
"s_cbranch_scc1 LPDL1_%=\n"
"LPEL1_%=:\n"
"s_add_u32 s66, s66, s72\n"
"s_add_u32 s66, s66, 4\n"
"s_add_u32 s67, s80, s72\n"
"s_sub_u32 s72, s72, 3\n"
"s_max_u32 s72, s72, s62\n"
"s_branch LPKL1_%=\n"
"LPCL2_%=:\n"
"s_memrealtime s[80:81]\n"
"s_sub_u32 s82, s67, s66\n"
"s_cmp_gt_i32 s82, 0\n"
"s_cselect_b32 s83, s67, s66\n"
"s_waitcnt lgkmcnt(0)\n"
"s_sub_u32 s82, s83, s80\n"
"s_cmp_gt_i32 s82, 3\n"
"s_cbranch_scc0 LPEL2_%=\n"
"LPDL2_%=:\n"
"s_sleep 1\n"
"s_memrealtime s[80:81]\n"
"s_waitcnt lgkmcnt(0)\n"
"s_sub_u32 s82, s83, s80\n"
"s_cmp_gt_i32 s82, 3\n"
"s_cbranch_scc1 LPDL2_%=\n"
"LPEL2_%=:\n"
"s_add_u32 s66, s66, s72\n"
"s_add_u32 s66, s66, 4\n"
"s_add_u32 s67, s80, s72\n"
"s_sub_u32 s72, s72, 3\n"
"s_max_u32 s72, s72, s62\n"
"s_branch LPKL2_%=\n"
"LPCL3_%=:\n"
"s_memrealtime s[80:81]\n"
"s_sub_u32 s82, s67, s66\n"
"s_cmp_gt_i32 s82, 0\n"
"s_cselect_b32 s83, s67, s66\n"
"s_waitcnt lgkmcnt(0)\n"
"s_sub_u32 s82, s83, s80\n"
"s_cmp_gt_i32 s82, 3\n"
"s_cbranch_scc0 LPEL3_%=\n"
"LPDL3_%=:\n"
"s_sleep 1\n"
"s_memrealtime s[80:81]\n"
"s_waitcnt lgkmcnt(0)\n"
"s_sub_u32 s82, s83, s80\n"
"s_cmp_gt_i32 s82, 3\n"
"s_cbranch_scc1 LPDL3_%=\n"
"LPEL3_%=:\n"
"s_add_u32 s66, s66, s72\n"
"s_add_u32 s66, s66, 4\n"
"s_add_u32 s67, s80, s72\n"
"s_sub_u32 s72, s72, 3\n"
"s_max_u32 s72, s72, s62\n"
"s_branch LPKL3_%=\n"
"LPN_%=:\n"
"v_mfma_f32_16x16x32_bf16 v[112:115], a[192:195], a[136:139], v[112:115]\n"
"v_mfma_f32_16x16x32_bf16 v[112:115], a[196:199], a[140:143], v[112:115]\n"
"v_mfma_f32_16x16x32_bf16 v[116:119], a[200:203], a[136:139], v[116:119]\n"
"s_waitcnt vmcnt(11)\n"
"v_mfma_f32_16x16x32_bf16 v[116:119], a[204:207], a[140:143], v[116:119]\n"
"s_barrier\n"
"v_mfma_f32_16x16x32_bf16 v[120:123], a[208:211], a[136:139], v[120:123]\n"
"s_add_u32 s40, s48, 256\n"
"s_xor_b32 m0, s50, 0xc000\n"
"ds_read_b64_tr_b16 a[224:225], v244 offset:16384\n"
"v_mfma_f32_16x16x32_bf16 v[120:123], a[212:215], a[140:143], v[120:123]\n"
"buffer_load_dwordx4 v224, s[28:31], s40 offen nt lds\n"
"v_mfma_f32_16x16x32_bf16 v[124:127], a[216:219], a[136:139], v[124:127]\n"
"ds_read_b64_tr_b16 a[226:227], v244 offset:16896\n"
"v_mfma_f32_16x16x32_bf16 v[124:127], a[220:223], a[140:143], v[124:127]\n"
"ds_read_b64_tr_b16 a[228:229], v244 offset:24576\n"
"v_mfma_f32_16x16x32_bf16 v[140:143], a[216:219], a[144:147], v[140:143]\n"
"ds_read_b64_tr_b16 a[230:231], v244 offset:25088\n"
"v_mfma_f32_16x16x32_bf16 v[140:143], a[220:223], a[148:151], v[140:143]\n"
"ds_read_b64_tr_b16 a[232:233], v245 offset:16384\n"
"v_mfma_f32_16x16x32_bf16 v[136:139], a[208:211], a[144:147], v[136:139]\n"
"ds_read_b64_tr_b16 a[234:235], v245 offset:16896\n"
"v_mfma_f32_16x16x32_bf16 v[136:139], a[212:215], a[148:151], v[136:139]\n"
"buffer_load_dwordx4 v225, s[28:31], s40 offen offset:1024 nt lds\n"
"v_mfma_f32_16x16x32_bf16 v[132:135], a[200:203], a[144:147], v[132:135]\n"
"ds_read_b64_tr_b16 a[236:237], v245 offset:24576\n"
"v_mfma_f32_16x16x32_bf16 v[132:135], a[204:207], a[148:151], v[132:135]\n"
"ds_read_b64_tr_b16 a[238:239], v245 offset:25088\n"
"v_mfma_f32_16x16x32_bf16 v[128:131], a[192:195], a[144:147], v[128:131]\n"
"ds_read_b64_tr_b16 a[240:241], v244 offset:17408\n"
"v_mfma_f32_16x16x32_bf16 v[128:131], a[196:199], a[148:151], v[128:131]\n"
"ds_read_b64_tr_b16 a[242:243], v244 offset:17920\n"
"v_mfma_f32_16x16x32_bf16 v[144:147], a[192:195], a[152:155], v[144:147]\n"
"ds_read_b64_tr_b16 a[244:245], v244 offset:25600\n"
"v_mfma_f32_16x16x32_bf16 v[144:147], a[196:199], a[156:159], v[144:147]\n"
"buffer_load_dwordx4 v226, s[28:31], s40 offen offset:2048 nt lds\n"
"v_mfma_f32_16x16x32_bf16 v[148:151], a[200:203], a[152:155], v[148:151]\n"
"ds_read_b64_tr_b16 a[246:247], v244 offset:26112\n"
"v_mfma_f32_16x16x32_bf16 v[148:151], a[204:207], a[156:159], v[148:151]\n"
"ds_read_b64_tr_b16 a[248:249], v245 offset:17408\n"
"v_mfma_f32_16x16x32_bf16 v[152:155], a[208:211], a[152:155], v[152:155]\n"
"ds_read_b64_tr_b16 a[250:251], v245 offset:17920\n"
"v_mfma_f32_16x16x32_bf16 v[152:155], a[212:215], a[156:159], v[152:155]\n"
"ds_read_b64_tr_b16 a[252:253], v245 offset:25600\n"
"v_mfma_f32_16x16x32_bf16 v[156:159], a[216:219], a[152:155], v[156:159]\n"
"ds_read_b64_tr_b16 a[254:255], v245 offset:26112\n"
"v_mfma_f32_16x16x32_bf16 v[156:159], a[220:223], a[156:159], v[156:159]\n"
"buffer_load_dwordx4 v227, s[28:31], s40 offen offset:3072 nt lds\n"
"v_mfma_f32_16x16x32_bf16 v[172:175], a[216:219], a[160:163], v[172:175]\n"
"s_cmp_eq_u32 s47, 8\n"
"v_mfma_f32_16x16x32_bf16 v[172:175], a[220:223], a[164:167], v[172:175]\n"
"s_cselect_b32 s41, s52, s58\n"
"v_mfma_f32_16x16x32_bf16 v[168:171], a[208:211], a[160:163], v[168:171]\n"
"s_add_u32 s48, s48, s41\n"
"v_mfma_f32_16x16x32_bf16 v[168:171], a[212:215], a[164:167], v[168:171]\n"
"s_sub_u32 s41, s48, 5242880\n"
"v_mfma_f32_16x16x32_bf16 v[164:167], a[200:203], a[160:163], v[164:167]\n"
"s_min_u32 s48, s48, s41\n"
"v_mfma_f32_16x16x32_bf16 v[164:167], a[204:207], a[164:167], v[164:167]\n"
"v_xor_b32 v244, s74, v244\n"
"v_xor_b32 v246, s74, v246\n"
"v_mfma_f32_16x16x32_bf16 v[160:163], a[192:195], a[160:163], v[160:163]\n"
"s_mov_b32 m0, s50\n"
"v_xor_b32 v245, s74, v245\n"
"v_xor_b32 v247, s74, v247\n"
"v_mfma_f32_16x16x32_bf16 v[160:163], a[196:199], a[164:167], v[160:163]\n"
"buffer_load_dwordx4 v224, s[28:31], s48 offen nt lds\n"
"v_mfma_f32_16x16x32_bf16 v[176:179], a[192:195], a[168:171], v[176:179]\n"
"v_xor_b32 v252, s74, v252\n"
"v_xor_b32 v253, s74, v253\n"
"v_mfma_f32_16x16x32_bf16 v[176:179], a[196:199], a[172:175], v[176:179]\n"
"v_xor_b32 v250, s74, v250\n"
"v_xor_b32 v251, s74, v251\n"
"v_mfma_f32_16x16x32_bf16 v[180:183], a[200:203], a[168:171], v[180:183]\n"
"v_mfma_f32_16x16x32_bf16 v[180:183], a[204:207], a[172:175], v[180:183]\n"
"buffer_load_dwordx4 v225, s[28:31], s48 offen offset:1024 nt lds\n"
"v_mfma_f32_16x16x32_bf16 v[184:187], a[208:211], a[168:171], v[184:187]\n"
"v_mfma_f32_16x16x32_bf16 v[184:187], a[212:215], a[172:175], v[184:187]\n"
"v_mfma_f32_16x16x32_bf16 v[188:191], a[216:219], a[168:171], v[188:191]\n"
"v_mfma_f32_16x16x32_bf16 v[188:191], a[220:223], a[172:175], v[188:191]\n"
"v_mfma_f32_16x16x32_bf16 v[204:207], a[216:219], a[176:179], v[204:207]\n"
"v_mfma_f32_16x16x32_bf16 v[204:207], a[220:223], a[180:183], v[204:207]\n"
"buffer_load_dwordx4 v226, s[28:31], s48 offen offset:2048 nt lds\n"
"v_mfma_f32_16x16x32_bf16 v[200:203], a[208:211], a[176:179], v[200:203]\n"
"v_mfma_f32_16x16x32_bf16 v[200:203], a[212:215], a[180:183], v[200:203]\n"
"v_mfma_f32_16x16x32_bf16 v[196:199], a[200:203], a[176:179], v[196:199]\n"
"v_mfma_f32_16x16x32_bf16 v[196:199], a[204:207], a[180:183], v[196:199]\n"
"v_mfma_f32_16x16x32_bf16 v[192:195], a[192:195], a[176:179], v[192:195]\n"
"v_mfma_f32_16x16x32_bf16 v[192:195], a[196:199], a[180:183], v[192:195]\n"
"s_waitcnt lgkmcnt(0)\n"
"buffer_load_dwordx4 v227, s[28:31], s48 offen offset:3072 nt lds\n"
"v_mfma_f32_16x16x32_bf16 v[208:211], a[192:195], a[184:187], v[208:211]\n"
"s_xor_b32 s50, s50, s74\n"
"v_mfma_f32_16x16x32_bf16 v[208:211], a[196:199], a[188:191], v[208:211]\n"
"v_mfma_f32_16x16x32_bf16 v[212:215], a[200:203], a[184:187], v[212:215]\n"
"v_mfma_f32_16x16x32_bf16 v[212:215], a[204:207], a[188:191], v[212:215]\n"
"v_mfma_f32_16x16x32_bf16 v[216:219], a[208:211], a[184:187], v[216:219]\n"
"v_mfma_f32_16x16x32_bf16 v[216:219], a[212:215], a[188:191], v[216:219]\n"
"v_mfma_f32_16x16x32_bf16 v[220:223], a[216:219], a[184:187], v[220:223]\n"
"v_mfma_f32_16x16x32_bf16 v[220:223], a[220:223], a[188:191], v[220:223]\n"
"v_mfma_f32_16x16x32_bf16 v[0:3], a[224:227], a[136:139], v[0:3]\n"
"v_mfma_f32_16x16x32_bf16 v[0:3], a[228:231], a[140:143], v[0:3]\n"
"v_mfma_f32_16x16x32_bf16 v[4:7], a[232:235], a[136:139], v[4:7]\n"
"s_waitcnt vmcnt(8)\n"
"v_mfma_f32_16x16x32_bf16 v[4:7], a[236:239], a[140:143], v[4:7]\n"
"s_barrier\n"
"v_mfma_f32_16x16x32_bf16 v[8:11], a[240:243], a[136:139], v[8:11]\n"
"s_mov_b32 m0, s51\n"
"ds_read_b64_tr_b16 a[192:193], v244 offset:0\n"
"v_mfma_f32_16x16x32_bf16 v[8:11], a[244:247], a[140:143], v[8:11]\n"
"buffer_load_dword v242, s[80:83], 0 offen lds\n"
"s_add_u32 s80, s80, 680\n"
"s_addc_u32 s81, s81, 0\n"
"buffer_load_dwordx4 v248, s[24:27], s49 offen nt lds\n"
"v_mfma_f32_16x16x32_bf16 v[12:15], a[248:251], a[136:139], v[12:15]\n"
"ds_read_b64_tr_b16 a[194:195], v244 offset:512\n"
"v_mfma_f32_16x16x32_bf16 v[12:15], a[252:255], a[140:143], v[12:15]\n"
"ds_read_b128 a[136:139], v246 offset:0\n"
"v_mfma_f32_16x16x32_bf16 v[28:31], a[248:251], a[144:147], v[28:31]\n"
"ds_read_b64_tr_b16 a[196:197], v244 offset:8192\n"
"v_mfma_f32_16x16x32_bf16 v[28:31], a[252:255], a[148:151], v[28:31]\n"
"ds_read_b64_tr_b16 a[198:199], v244 offset:8704\n"
"v_mfma_f32_16x16x32_bf16 v[24:27], a[240:243], a[144:147], v[24:27]\n"
"ds_read_b128 a[140:143], v247 offset:0\n"
"v_mfma_f32_16x16x32_bf16 v[24:27], a[244:247], a[148:151], v[24:27]\n"
"buffer_load_dwordx4 v249, s[24:27], s49 offen offset:1024 nt lds\n"
"v_mfma_f32_16x16x32_bf16 v[20:23], a[232:235], a[144:147], v[20:23]\n"
"ds_read_b64_tr_b16 a[200:201], v245 offset:0\n"
"v_mfma_f32_16x16x32_bf16 v[20:23], a[236:239], a[148:151], v[20:23]\n"
"ds_read_b64_tr_b16 a[202:203], v245 offset:512\n"
"v_mfma_f32_16x16x32_bf16 v[16:19], a[224:227], a[144:147], v[16:19]\n"
"ds_read_b64_tr_b16 a[204:205], v245 offset:8192\n"
"v_mfma_f32_16x16x32_bf16 v[16:19], a[228:231], a[148:151], v[16:19]\n"
"ds_read_b64_tr_b16 a[206:207], v245 offset:8704\n"
"v_mfma_f32_16x16x32_bf16 v[32:35], a[224:227], a[152:155], v[32:35]\n"
"ds_read_b64_tr_b16 a[208:209], v244 offset:1024\n"
"v_mfma_f32_16x16x32_bf16 v[32:35], a[228:231], a[156:159], v[32:35]\n"
"buffer_load_dwordx4 v254, s[24:27], s49 offen offset:2048 nt lds\n"
"v_mfma_f32_16x16x32_bf16 v[36:39], a[232:235], a[152:155], v[36:39]\n"
"ds_read_b64_tr_b16 a[210:211], v244 offset:1536\n"
"v_mfma_f32_16x16x32_bf16 v[36:39], a[236:239], a[156:159], v[36:39]\n"
"ds_read_b64_tr_b16 a[212:213], v244 offset:9216\n"
"v_mfma_f32_16x16x32_bf16 v[40:43], a[240:243], a[152:155], v[40:43]\n"
"ds_read_b64_tr_b16 a[214:215], v244 offset:9728\n"
"v_mfma_f32_16x16x32_bf16 v[40:43], a[244:247], a[156:159], v[40:43]\n"
"ds_read_b64_tr_b16 a[216:217], v245 offset:1024\n"
"v_mfma_f32_16x16x32_bf16 v[44:47], a[248:251], a[152:155], v[44:47]\n"
"ds_read_b64_tr_b16 a[218:219], v245 offset:1536\n"
"v_mfma_f32_16x16x32_bf16 v[44:47], a[252:255], a[156:159], v[44:47]\n"
"buffer_load_dwordx4 v255, s[24:27], s49 offen offset:3072 nt lds\n"
"v_mfma_f32_16x16x32_bf16 v[60:63], a[248:251], a[160:163], v[60:63]\n"
"ds_read_b64_tr_b16 a[220:221], v245 offset:9216\n"
"v_mfma_f32_16x16x32_bf16 v[60:63], a[252:255], a[164:167], v[60:63]\n"
"ds_read_b64_tr_b16 a[222:223], v245 offset:9728\n"
"v_mfma_f32_16x16x32_bf16 v[56:59], a[240:243], a[160:163], v[56:59]\n"
"ds_read_b128 a[144:147], v246 offset:2048\n"
"v_mfma_f32_16x16x32_bf16 v[56:59], a[244:247], a[164:167], v[56:59]\n"
"s_add_u32 s40, s49, 40960\n"
"ds_read_b128 a[148:151], v247 offset:2048\n"
"v_mfma_f32_16x16x32_bf16 v[52:55], a[232:235], a[160:163], v[52:55]\n"
"s_add_u32 m0, s51, 4096\n"
"ds_read_b128 a[152:155], v246 offset:4096\n"
"v_mfma_f32_16x16x32_bf16 v[52:55], a[236:239], a[164:167], v[52:55]\n"
"buffer_load_dwordx4 v248, s[24:27], s40 offen nt lds\n"
"v_mfma_f32_16x16x32_bf16 v[48:51], a[224:227], a[160:163], v[48:51]\n"
"ds_read_b128 a[156:159], v247 offset:4096\n"
"v_mfma_f32_16x16x32_bf16 v[48:51], a[228:231], a[164:167], v[48:51]\n"
"ds_read_b128 a[160:163], v252 offset:0\n"
"v_mfma_f32_16x16x32_bf16 v[64:67], a[224:227], a[168:171], v[64:67]\n"
"ds_read_b128 a[164:167], v253 offset:0\n"
"v_mfma_f32_16x16x32_bf16 v[64:67], a[228:231], a[172:175], v[64:67]\n"
"v_mfma_f32_16x16x32_bf16 v[68:71], a[232:235], a[168:171], v[68:71]\n"
"v_mfma_f32_16x16x32_bf16 v[68:71], a[236:239], a[172:175], v[68:71]\n"
"buffer_load_dwordx4 v249, s[24:27], s40 offen offset:1024 nt lds\n"
"v_mfma_f32_16x16x32_bf16 v[72:75], a[240:243], a[168:171], v[72:75]\n"
"v_mfma_f32_16x16x32_bf16 v[72:75], a[244:247], a[172:175], v[72:75]\n"
"v_mfma_f32_16x16x32_bf16 v[76:79], a[248:251], a[168:171], v[76:79]\n"
"ds_read_b128 a[168:171], v250 offset:0\n"
"v_mfma_f32_16x16x32_bf16 v[76:79], a[252:255], a[172:175], v[76:79]\n"
"ds_read_b128 a[172:175], v251 offset:0\n"
"v_mfma_f32_16x16x32_bf16 v[92:95], a[248:251], a[176:179], v[92:95]\n"
"v_mfma_f32_16x16x32_bf16 v[92:95], a[252:255], a[180:183], v[92:95]\n"
"buffer_load_dwordx4 v254, s[24:27], s40 offen offset:2048 nt lds\n"
"v_mfma_f32_16x16x32_bf16 v[88:91], a[240:243], a[176:179], v[88:91]\n"
"s_add_u32 s49, s49, 128\n"
"v_mfma_f32_16x16x32_bf16 v[88:91], a[244:247], a[180:183], v[88:91]\n"
"s_sub_u32 s41, s49, 1280\n"
"v_mfma_f32_16x16x32_bf16 v[84:87], a[232:235], a[176:179], v[84:87]\n"
"s_min_u32 s49, s49, s41\n"
"v_mfma_f32_16x16x32_bf16 v[84:87], a[236:239], a[180:183], v[84:87]\n"
"s_xor_b32 s51, s51, s74\n"
"v_mfma_f32_16x16x32_bf16 v[80:83], a[224:227], a[176:179], v[80:83]\n"
"ds_read_b128 a[176:179], v250 offset:2048\n"
"v_mfma_f32_16x16x32_bf16 v[80:83], a[228:231], a[180:183], v[80:83]\n"
"ds_read_b128 a[180:183], v251 offset:2048\n"
"v_mfma_f32_16x16x32_bf16 v[96:99], a[224:227], a[184:187], v[96:99]\n"
"v_mfma_f32_16x16x32_bf16 v[96:99], a[228:231], a[188:191], v[96:99]\n"
"v_mfma_f32_16x16x32_bf16 v[100:103], a[232:235], a[184:187], v[100:103]\n"
"v_mfma_f32_16x16x32_bf16 v[100:103], a[236:239], a[188:191], v[100:103]\n"
"v_mfma_f32_16x16x32_bf16 v[104:107], a[240:243], a[184:187], v[104:107]\n"
"v_mfma_f32_16x16x32_bf16 v[104:107], a[244:247], a[188:191], v[104:107]\n"
"v_mfma_f32_16x16x32_bf16 v[108:111], a[248:251], a[184:187], v[108:111]\n"
"s_add_u32 s47, s47, 1\n"
"ds_read_b128 a[184:187], v250 offset:4096\n"
"v_mfma_f32_16x16x32_bf16 v[108:111], a[252:255], a[188:191], v[108:111]\n"
"s_waitcnt lgkmcnt(1)\n"
"s_cmp_lg_u32 s47, s62\n"
"ds_read_b128 a[188:191], v251 offset:4096\n"
"s_cbranch_scc1 LPN_%=\n"
"s_branch LPX_%=\n"
"LEND_%=:\n"
"LSLOW_%=:\n"
"s_waitcnt vmcnt(0) lgkmcnt(0)\n"
"s_nop 9\n"
"v_bfe_u32 v148, v242, 4, 4\n"
"v_bfe_u32 v149, v242, 8, 2\n"
"v_lshlrev_b32 v144, 8, v148\n"
"v_lshl_add_u32 v144, v149, 4, v144\n"
"v_add_u32 v144, s43, v144\n"
"v_xor_b32 v145, 64, v144\n"
"v_lshlrev_b32 v146, 1, v148\n"
"v_lshl_add_u32 v146, v149, 10, v146\n"
"v_add_u32 v146, s43, v146\n"
"v_xor_b32 v147, 64, v146\n"
"v_mov_b32 v152, 0xff7fffff\n"
"v_mov_b32 v155, 0\n"
"v_mov_b32 v0, 0\n"
"v_mov_b32 v1, 0\n"
"v_mov_b32 v2, 0\n"
"v_mov_b32 v3, 0\n"
"v_mov_b32 v4, 0\n"
"v_mov_b32 v5, 0\n"
"v_mov_b32 v6, 0\n"
"v_mov_b32 v7, 0\n"
"v_mov_b32 v8, 0\n"
"v_mov_b32 v9, 0\n"
"v_mov_b32 v10, 0\n"
"v_mov_b32 v11, 0\n"
"v_mov_b32 v12, 0\n"
"v_mov_b32 v13, 0\n"
"v_mov_b32 v14, 0\n"
"v_mov_b32 v15, 0\n"
"v_mov_b32 v16, 0\n"
"v_mov_b32 v17, 0\n"
"v_mov_b32 v18, 0\n"
"v_mov_b32 v19, 0\n"
"v_mov_b32 v20, 0\n"
"v_mov_b32 v21, 0\n"
"v_mov_b32 v22, 0\n"
"v_mov_b32 v23, 0\n"
"v_mov_b32 v24, 0\n"
"v_mov_b32 v25, 0\n"
"v_mov_b32 v26, 0\n"
"v_mov_b32 v27, 0\n"
"v_mov_b32 v28, 0\n"
"v_mov_b32 v29, 0\n"
"v_mov_b32 v30, 0\n"
"v_mov_b32 v31, 0\n"
"v_mov_b32 v156, 0xff7fffff\n"
"v_mov_b32 v159, 0\n"
"v_mov_b32 v32, 0\n"
"v_mov_b32 v33, 0\n"
"v_mov_b32 v34, 0\n"
"v_mov_b32 v35, 0\n"
"v_mov_b32 v36, 0\n"
"v_mov_b32 v37, 0\n"
"v_mov_b32 v38, 0\n"
"v_mov_b32 v39, 0\n"
"v_mov_b32 v40, 0\n"
"v_mov_b32 v41, 0\n"
"v_mov_b32 v42, 0\n"
"v_mov_b32 v43, 0\n"
"v_mov_b32 v44, 0\n"
"v_mov_b32 v45, 0\n"
"v_mov_b32 v46, 0\n"
"v_mov_b32 v47, 0\n"
"v_mov_b32 v48, 0\n"
"v_mov_b32 v49, 0\n"
"v_mov_b32 v50, 0\n"
"v_mov_b32 v51, 0\n"
"v_mov_b32 v52, 0\n"
"v_mov_b32 v53, 0\n"
"v_mov_b32 v54, 0\n"
"v_mov_b32 v55, 0\n"
"v_mov_b32 v56, 0\n"
"v_mov_b32 v57, 0\n"
"v_mov_b32 v58, 0\n"
"v_mov_b32 v59, 0\n"
"v_mov_b32 v60, 0\n"
"v_mov_b32 v61, 0\n"
"v_mov_b32 v62, 0\n"
"v_mov_b32 v63, 0\n"
"v_mov_b32 v160, 0xff7fffff\n"
"v_mov_b32 v163, 0\n"
"v_mov_b32 v64, 0\n"
"v_mov_b32 v65, 0\n"
"v_mov_b32 v66, 0\n"
"v_mov_b32 v67, 0\n"
"v_mov_b32 v68, 0\n"
"v_mov_b32 v69, 0\n"
"v_mov_b32 v70, 0\n"
"v_mov_b32 v71, 0\n"
"v_mov_b32 v72, 0\n"
"v_mov_b32 v73, 0\n"
"v_mov_b32 v74, 0\n"
"v_mov_b32 v75, 0\n"
"v_mov_b32 v76, 0\n"
"v_mov_b32 v77, 0\n"
"v_mov_b32 v78, 0\n"
"v_mov_b32 v79, 0\n"
"v_mov_b32 v80, 0\n"
"v_mov_b32 v81, 0\n"
"v_mov_b32 v82, 0\n"
"v_mov_b32 v83, 0\n"
"v_mov_b32 v84, 0\n"
"v_mov_b32 v85, 0\n"
"v_mov_b32 v86, 0\n"
"v_mov_b32 v87, 0\n"
"v_mov_b32 v88, 0\n"
"v_mov_b32 v89, 0\n"
"v_mov_b32 v90, 0\n"
"v_mov_b32 v91, 0\n"
"v_mov_b32 v92, 0\n"
"v_mov_b32 v93, 0\n"
"v_mov_b32 v94, 0\n"
"v_mov_b32 v95, 0\n"
"v_mov_b32 v164, 0xff7fffff\n"
"v_mov_b32 v167, 0\n"
"v_accvgpr_write_b32 a208, 0\n"
"v_accvgpr_write_b32 a209, 0\n"
"v_accvgpr_write_b32 a210, 0\n"
"v_accvgpr_write_b32 a211, 0\n"
"v_accvgpr_write_b32 a212, 0\n"
"v_accvgpr_write_b32 a213, 0\n"
"v_accvgpr_write_b32 a214, 0\n"
"v_accvgpr_write_b32 a215, 0\n"
"v_accvgpr_write_b32 a216, 0\n"
"v_accvgpr_write_b32 a217, 0\n"
"v_accvgpr_write_b32 a218, 0\n"
"v_accvgpr_write_b32 a219, 0\n"
"v_accvgpr_write_b32 a220, 0\n"
"v_accvgpr_write_b32 a221, 0\n"
"v_accvgpr_write_b32 a222, 0\n"
"v_accvgpr_write_b32 a223, 0\n"
"v_accvgpr_write_b32 a224, 0\n"
"v_accvgpr_write_b32 a225, 0\n"
"v_accvgpr_write_b32 a226, 0\n"
"v_accvgpr_write_b32 a227, 0\n"
"v_accvgpr_write_b32 a228, 0\n"
"v_accvgpr_write_b32 a229, 0\n"
"v_accvgpr_write_b32 a230, 0\n"
"v_accvgpr_write_b32 a231, 0\n"
"v_accvgpr_write_b32 a232, 0\n"
"v_accvgpr_write_b32 a233, 0\n"
"v_accvgpr_write_b32 a234, 0\n"
"v_accvgpr_write_b32 a235, 0\n"
"v_accvgpr_write_b32 a236, 0\n"
"v_accvgpr_write_b32 a237, 0\n"
"v_accvgpr_write_b32 a238, 0\n"
"v_accvgpr_write_b32 a239, 0\n"
"s_lshl_b32 s80, s47, 16\n"
"s_add_u32 s81, s80, 4096\n"
"s_mov_b32 s82, 0\n"
"LSLOOP_%=:\n"
"s_lshl_b32 s83, s82, 3\n"
"s_and_b32 s83, s83, 64\n"
"buffer_load_dwordx4 v[128:131], v144, s[84:87], s80 offen offset:0\n"
"buffer_load_dwordx4 v[132:135], v145, s[84:87], s80 offen offset:0\n"
"buffer_load_dwordx4 v[136:139], v144, s[84:87], s80 offen offset:128\n"
"buffer_load_dwordx4 v[140:143], v145, s[84:87], s80 offen offset:128\n"
"s_waitcnt vmcnt(0)\n"
"v_mfma_f32_16x16x32_bf16 v[96:99], v[128:131], a[0:3], 0\n"
"v_mfma_f32_16x16x32_bf16 v[96:99], v[132:135], a[4:7], v[96:99]\n"
"v_mfma_f32_16x16x32_bf16 v[96:99], v[136:139], a[8:11], v[96:99]\n"
"v_mfma_f32_16x16x32_bf16 v[96:99], v[140:143], a[12:15], v[96:99]\n"
"v_mfma_f32_16x16x32_bf16 v[104:107], v[128:131], a[16:19], 0\n"
"v_mfma_f32_16x16x32_bf16 v[104:107], v[132:135], a[20:23], v[104:107]\n"
"v_mfma_f32_16x16x32_bf16 v[104:107], v[136:139], a[24:27], v[104:107]\n"
"v_mfma_f32_16x16x32_bf16 v[104:107], v[140:143], a[28:31], v[104:107]\n"
"v_mfma_f32_16x16x32_bf16 v[112:115], v[128:131], a[32:35], 0\n"
"v_mfma_f32_16x16x32_bf16 v[112:115], v[132:135], a[36:39], v[112:115]\n"
"v_mfma_f32_16x16x32_bf16 v[112:115], v[136:139], a[40:43], v[112:115]\n"
"v_mfma_f32_16x16x32_bf16 v[112:115], v[140:143], a[44:47], v[112:115]\n"
"v_mfma_f32_16x16x32_bf16 v[120:123], v[128:131], a[192:195], 0\n"
"v_mfma_f32_16x16x32_bf16 v[120:123], v[132:135], a[196:199], v[120:123]\n"
"v_mfma_f32_16x16x32_bf16 v[120:123], v[136:139], a[200:203], v[120:123]\n"
"v_mfma_f32_16x16x32_bf16 v[120:123], v[140:143], a[204:207], v[120:123]\n"
"s_nop 7\n"
"buffer_load_dwordx4 v[128:131], v144, s[84:87], s81 offen offset:0\n"
"buffer_load_dwordx4 v[132:135], v145, s[84:87], s81 offen offset:0\n"
"buffer_load_dwordx4 v[136:139], v144, s[84:87], s81 offen offset:128\n"
"buffer_load_dwordx4 v[140:143], v145, s[84:87], s81 offen offset:128\n"
"s_waitcnt vmcnt(0)\n"
"v_mfma_f32_16x16x32_bf16 v[100:103], v[128:131], a[0:3], 0\n"
"v_mfma_f32_16x16x32_bf16 v[100:103], v[132:135], a[4:7], v[100:103]\n"
"v_mfma_f32_16x16x32_bf16 v[100:103], v[136:139], a[8:11], v[100:103]\n"
"v_mfma_f32_16x16x32_bf16 v[100:103], v[140:143], a[12:15], v[100:103]\n"
"v_mfma_f32_16x16x32_bf16 v[108:111], v[128:131], a[16:19], 0\n"
"v_mfma_f32_16x16x32_bf16 v[108:111], v[132:135], a[20:23], v[108:111]\n"
"v_mfma_f32_16x16x32_bf16 v[108:111], v[136:139], a[24:27], v[108:111]\n"
"v_mfma_f32_16x16x32_bf16 v[108:111], v[140:143], a[28:31], v[108:111]\n"
"v_mfma_f32_16x16x32_bf16 v[116:119], v[128:131], a[32:35], 0\n"
"v_mfma_f32_16x16x32_bf16 v[116:119], v[132:135], a[36:39], v[116:119]\n"
"v_mfma_f32_16x16x32_bf16 v[116:119], v[136:139], a[40:43], v[116:119]\n"
"v_mfma_f32_16x16x32_bf16 v[116:119], v[140:143], a[44:47], v[116:119]\n"
"v_mfma_f32_16x16x32_bf16 v[124:127], v[128:131], a[192:195], 0\n"
"v_mfma_f32_16x16x32_bf16 v[124:127], v[132:135], a[196:199], v[124:127]\n"
"v_mfma_f32_16x16x32_bf16 v[124:127], v[136:139], a[200:203], v[124:127]\n"
"v_mfma_f32_16x16x32_bf16 v[124:127], v[140:143], a[204:207], v[124:127]\n"
"s_nop 7\n"
"v_max3_f32 v153, v96, v97, v98\n"
"v_max3_f32 v153, v153, v99, v100\n"
"v_max3_f32 v153, v153, v101, v102\n"
"v_max3_f32 v153, v153, v103, v152\n"
"v_mov_b32 v154, v153\n"
"s_nop 1\n"
"v_permlane32_swap_b32 v153, v154\n"
"v_max_f32 v153, v153, v154\n"
"v_mov_b32 v154, v153\n"
"s_nop 1\n"
"v_permlane16_swap_b32 v153, v154\n"
"v_max_f32 v153, v153, v154\n"
"v_sub_f32 v154, v152, v153\n"
"v_exp_f32 v154, v154\n"
"v_mov_b32 v152, v153\n"
"v_sub_f32 v96, v96, v152\n"
"v_sub_f32 v97, v97, v152\n"
"v_sub_f32 v98, v98, v152\n"
"v_sub_f32 v99, v99, v152\n"
"v_sub_f32 v100, v100, v152\n"
"v_sub_f32 v101, v101, v152\n"
"v_sub_f32 v102, v102, v152\n"
"v_sub_f32 v103, v103, v152\n"
"v_exp_f32 v96, v96\n"
"v_exp_f32 v97, v97\n"
"v_exp_f32 v98, v98\n"
"v_exp_f32 v99, v99\n"
"v_exp_f32 v100, v100\n"
"v_exp_f32 v101, v101\n"
"v_exp_f32 v102, v102\n"
"v_exp_f32 v103, v103\n"
"v_mul_f32 v155, v155, v154\n"
"v_add_f32 v155, v155, v96\n"
"v_add_f32 v155, v155, v97\n"
"v_add_f32 v155, v155, v98\n"
"v_add_f32 v155, v155, v99\n"
"v_add_f32 v155, v155, v100\n"
"v_add_f32 v155, v155, v101\n"
"v_add_f32 v155, v155, v102\n"
"v_add_f32 v155, v155, v103\n"
"v_cvt_pk_bf16_f32 v96, v96, v97\n"
"v_cvt_pk_bf16_f32 v97, v98, v99\n"
"v_cvt_pk_bf16_f32 v98, v100, v101\n"
"v_cvt_pk_bf16_f32 v99, v102, v103\n"
"v_mov_b32_dpp v154, v154 row_ror:12 row_mask:0x2 bank_mask:0xf\n"
"s_nop 1\n"
"v_mov_b32_dpp v154, v154 row_ror:8 row_mask:0x4 bank_mask:0xf\n"
"s_nop 1\n"
"v_mov_b32_dpp v154, v154 row_ror:4 row_mask:0x8 bank_mask:0xf\n"
"s_nop 1\n"
"v_mul_f32_dpp v0, v154, v0 row_newbcast:0 row_mask:0xf bank_mask:0xf\n"
"v_mul_f32_dpp v1, v154, v1 row_newbcast:1 row_mask:0xf bank_mask:0xf\n"
"v_mul_f32_dpp v2, v154, v2 row_newbcast:2 row_mask:0xf bank_mask:0xf\n"
"v_mul_f32_dpp v3, v154, v3 row_newbcast:3 row_mask:0xf bank_mask:0xf\n"
"v_mul_f32_dpp v4, v154, v4 row_newbcast:0 row_mask:0xf bank_mask:0xf\n"
"v_mul_f32_dpp v5, v154, v5 row_newbcast:1 row_mask:0xf bank_mask:0xf\n"
"v_mul_f32_dpp v6, v154, v6 row_newbcast:2 row_mask:0xf bank_mask:0xf\n"
"v_mul_f32_dpp v7, v154, v7 row_newbcast:3 row_mask:0xf bank_mask:0xf\n"
"v_mul_f32_dpp v8, v154, v8 row_newbcast:0 row_mask:0xf bank_mask:0xf\n"
"v_mul_f32_dpp v9, v154, v9 row_newbcast:1 row_mask:0xf bank_mask:0xf\n"
"v_mul_f32_dpp v10, v154, v10 row_newbcast:2 row_mask:0xf bank_mask:0xf\n"
"v_mul_f32_dpp v11, v154, v11 row_newbcast:3 row_mask:0xf bank_mask:0xf\n"
"v_mul_f32_dpp v12, v154, v12 row_newbcast:0 row_mask:0xf bank_mask:0xf\n"
"v_mul_f32_dpp v13, v154, v13 row_newbcast:1 row_mask:0xf bank_mask:0xf\n"
"v_mul_f32_dpp v14, v154, v14 row_newbcast:2 row_mask:0xf bank_mask:0xf\n"
"v_mul_f32_dpp v15, v154, v15 row_newbcast:3 row_mask:0xf bank_mask:0xf\n"
"v_mul_f32_dpp v16, v154, v16 row_newbcast:0 row_mask:0xf bank_mask:0xf\n"
"v_mul_f32_dpp v17, v154, v17 row_newbcast:1 row_mask:0xf bank_mask:0xf\n"
"v_mul_f32_dpp v18, v154, v18 row_newbcast:2 row_mask:0xf bank_mask:0xf\n"
"v_mul_f32_dpp v19, v154, v19 row_newbcast:3 row_mask:0xf bank_mask:0xf\n"
"v_mul_f32_dpp v20, v154, v20 row_newbcast:0 row_mask:0xf bank_mask:0xf\n"
"v_mul_f32_dpp v21, v154, v21 row_newbcast:1 row_mask:0xf bank_mask:0xf\n"
"v_mul_f32_dpp v22, v154, v22 row_newbcast:2 row_mask:0xf bank_mask:0xf\n"
"v_mul_f32_dpp v23, v154, v23 row_newbcast:3 row_mask:0xf bank_mask:0xf\n"
"v_mul_f32_dpp v24, v154, v24 row_newbcast:0 row_mask:0xf bank_mask:0xf\n"
"v_mul_f32_dpp v25, v154, v25 row_newbcast:1 row_mask:0xf bank_mask:0xf\n"
"v_mul_f32_dpp v26, v154, v26 row_newbcast:2 row_mask:0xf bank_mask:0xf\n"
"v_mul_f32_dpp v27, v154, v27 row_newbcast:3 row_mask:0xf bank_mask:0xf\n"
"v_mul_f32_dpp v28, v154, v28 row_newbcast:0 row_mask:0xf bank_mask:0xf\n"
"v_mul_f32_dpp v29, v154, v29 row_newbcast:1 row_mask:0xf bank_mask:0xf\n"
"v_mul_f32_dpp v30, v154, v30 row_newbcast:2 row_mask:0xf bank_mask:0xf\n"
"v_mul_f32_dpp v31, v154, v31 row_newbcast:3 row_mask:0xf bank_mask:0xf\n"
"v_max3_f32 v157, v104, v105, v106\n"
"v_max3_f32 v157, v157, v107, v108\n"
"v_max3_f32 v157, v157, v109, v110\n"
"v_max3_f32 v157, v157, v111, v156\n"
"v_mov_b32 v158, v157\n"
"s_nop 1\n"
"v_permlane32_swap_b32 v157, v158\n"
"v_max_f32 v157, v157, v158\n"
"v_mov_b32 v158, v157\n"
"s_nop 1\n"
"v_permlane16_swap_b32 v157, v158\n"
"v_max_f32 v157, v157, v158\n"
"v_sub_f32 v158, v156, v157\n"
"v_exp_f32 v158, v158\n"
"v_mov_b32 v156, v157\n"
"v_sub_f32 v104, v104, v156\n"
"v_sub_f32 v105, v105, v156\n"
"v_sub_f32 v106, v106, v156\n"
"v_sub_f32 v107, v107, v156\n"
"v_sub_f32 v108, v108, v156\n"
"v_sub_f32 v109, v109, v156\n"
"v_sub_f32 v110, v110, v156\n"
"v_sub_f32 v111, v111, v156\n"
"v_exp_f32 v104, v104\n"
"v_exp_f32 v105, v105\n"
"v_exp_f32 v106, v106\n"
"v_exp_f32 v107, v107\n"
"v_exp_f32 v108, v108\n"
"v_exp_f32 v109, v109\n"
"v_exp_f32 v110, v110\n"
"v_exp_f32 v111, v111\n"
"v_mul_f32 v159, v159, v158\n"
"v_add_f32 v159, v159, v104\n"
"v_add_f32 v159, v159, v105\n"
"v_add_f32 v159, v159, v106\n"
"v_add_f32 v159, v159, v107\n"
"v_add_f32 v159, v159, v108\n"
"v_add_f32 v159, v159, v109\n"
"v_add_f32 v159, v159, v110\n"
"v_add_f32 v159, v159, v111\n"
"v_cvt_pk_bf16_f32 v104, v104, v105\n"
"v_cvt_pk_bf16_f32 v105, v106, v107\n"
"v_cvt_pk_bf16_f32 v106, v108, v109\n"
"v_cvt_pk_bf16_f32 v107, v110, v111\n"
"v_mov_b32_dpp v158, v158 row_ror:12 row_mask:0x2 bank_mask:0xf\n"
"s_nop 1\n"
"v_mov_b32_dpp v158, v158 row_ror:8 row_mask:0x4 bank_mask:0xf\n"
"s_nop 1\n"
"v_mov_b32_dpp v158, v158 row_ror:4 row_mask:0x8 bank_mask:0xf\n"
"s_nop 1\n"
"v_mul_f32_dpp v32, v158, v32 row_newbcast:0 row_mask:0xf bank_mask:0xf\n"
"v_mul_f32_dpp v33, v158, v33 row_newbcast:1 row_mask:0xf bank_mask:0xf\n"
"v_mul_f32_dpp v34, v158, v34 row_newbcast:2 row_mask:0xf bank_mask:0xf\n"
"v_mul_f32_dpp v35, v158, v35 row_newbcast:3 row_mask:0xf bank_mask:0xf\n"
"v_mul_f32_dpp v36, v158, v36 row_newbcast:0 row_mask:0xf bank_mask:0xf\n"
"v_mul_f32_dpp v37, v158, v37 row_newbcast:1 row_mask:0xf bank_mask:0xf\n"
"v_mul_f32_dpp v38, v158, v38 row_newbcast:2 row_mask:0xf bank_mask:0xf\n"
"v_mul_f32_dpp v39, v158, v39 row_newbcast:3 row_mask:0xf bank_mask:0xf\n"
"v_mul_f32_dpp v40, v158, v40 row_newbcast:0 row_mask:0xf bank_mask:0xf\n"
"v_mul_f32_dpp v41, v158, v41 row_newbcast:1 row_mask:0xf bank_mask:0xf\n"
"v_mul_f32_dpp v42, v158, v42 row_newbcast:2 row_mask:0xf bank_mask:0xf\n"
"v_mul_f32_dpp v43, v158, v43 row_newbcast:3 row_mask:0xf bank_mask:0xf\n"
"v_mul_f32_dpp v44, v158, v44 row_newbcast:0 row_mask:0xf bank_mask:0xf\n"
"v_mul_f32_dpp v45, v158, v45 row_newbcast:1 row_mask:0xf bank_mask:0xf\n"
"v_mul_f32_dpp v46, v158, v46 row_newbcast:2 row_mask:0xf bank_mask:0xf\n"
"v_mul_f32_dpp v47, v158, v47 row_newbcast:3 row_mask:0xf bank_mask:0xf\n"
"v_mul_f32_dpp v48, v158, v48 row_newbcast:0 row_mask:0xf bank_mask:0xf\n"
"v_mul_f32_dpp v49, v158, v49 row_newbcast:1 row_mask:0xf bank_mask:0xf\n"
"v_mul_f32_dpp v50, v158, v50 row_newbcast:2 row_mask:0xf bank_mask:0xf\n"
"v_mul_f32_dpp v51, v158, v51 row_newbcast:3 row_mask:0xf bank_mask:0xf\n"
"v_mul_f32_dpp v52, v158, v52 row_newbcast:0 row_mask:0xf bank_mask:0xf\n"
"v_mul_f32_dpp v53, v158, v53 row_newbcast:1 row_mask:0xf bank_mask:0xf\n"
"v_mul_f32_dpp v54, v158, v54 row_newbcast:2 row_mask:0xf bank_mask:0xf\n"
"v_mul_f32_dpp v55, v158, v55 row_newbcast:3 row_mask:0xf bank_mask:0xf\n"
"v_mul_f32_dpp v56, v158, v56 row_newbcast:0 row_mask:0xf bank_mask:0xf\n"
"v_mul_f32_dpp v57, v158, v57 row_newbcast:1 row_mask:0xf bank_mask:0xf\n"
"v_mul_f32_dpp v58, v158, v58 row_newbcast:2 row_mask:0xf bank_mask:0xf\n"
"v_mul_f32_dpp v59, v158, v59 row_newbcast:3 row_mask:0xf bank_mask:0xf\n"
"v_mul_f32_dpp v60, v158, v60 row_newbcast:0 row_mask:0xf bank_mask:0xf\n"
"v_mul_f32_dpp v61, v158, v61 row_newbcast:1 row_mask:0xf bank_mask:0xf\n"
"v_mul_f32_dpp v62, v158, v62 row_newbcast:2 row_mask:0xf bank_mask:0xf\n"
"v_mul_f32_dpp v63, v158, v63 row_newbcast:3 row_mask:0xf bank_mask:0xf\n"
"v_max3_f32 v161, v112, v113, v114\n"
"v_max3_f32 v161, v161, v115, v116\n"
"v_max3_f32 v161, v161, v117, v118\n"
"v_max3_f32 v161, v161, v119, v160\n"
"v_mov_b32 v162, v161\n"
"s_nop 1\n"
"v_permlane32_swap_b32 v161, v162\n"
"v_max_f32 v161, v161, v162\n"
"v_mov_b32 v162, v161\n"
"s_nop 1\n"
"v_permlane16_swap_b32 v161, v162\n"
"v_max_f32 v161, v161, v162\n"
"v_sub_f32 v162, v160, v161\n"
"v_exp_f32 v162, v162\n"
"v_mov_b32 v160, v161\n"
"v_sub_f32 v112, v112, v160\n"
"v_sub_f32 v113, v113, v160\n"
"v_sub_f32 v114, v114, v160\n"
"v_sub_f32 v115, v115, v160\n"
"v_sub_f32 v116, v116, v160\n"
"v_sub_f32 v117, v117, v160\n"
"v_sub_f32 v118, v118, v160\n"
"v_sub_f32 v119, v119, v160\n"
"v_exp_f32 v112, v112\n"
"v_exp_f32 v113, v113\n"
"v_exp_f32 v114, v114\n"
"v_exp_f32 v115, v115\n"
"v_exp_f32 v116, v116\n"
"v_exp_f32 v117, v117\n"
"v_exp_f32 v118, v118\n"
"v_exp_f32 v119, v119\n"
"v_mul_f32 v163, v163, v162\n"
"v_add_f32 v163, v163, v112\n"
"v_add_f32 v163, v163, v113\n"
"v_add_f32 v163, v163, v114\n"
"v_add_f32 v163, v163, v115\n"
"v_add_f32 v163, v163, v116\n"
"v_add_f32 v163, v163, v117\n"
"v_add_f32 v163, v163, v118\n"
"v_add_f32 v163, v163, v119\n"
"v_cvt_pk_bf16_f32 v112, v112, v113\n"
"v_cvt_pk_bf16_f32 v113, v114, v115\n"
"v_cvt_pk_bf16_f32 v114, v116, v117\n"
"v_cvt_pk_bf16_f32 v115, v118, v119\n"
"v_mov_b32_dpp v162, v162 row_ror:12 row_mask:0x2 bank_mask:0xf\n"
"s_nop 1\n"
"v_mov_b32_dpp v162, v162 row_ror:8 row_mask:0x4 bank_mask:0xf\n"
"s_nop 1\n"
"v_mov_b32_dpp v162, v162 row_ror:4 row_mask:0x8 bank_mask:0xf\n"
"s_nop 1\n"
"v_mul_f32_dpp v64, v162, v64 row_newbcast:0 row_mask:0xf bank_mask:0xf\n"
"v_mul_f32_dpp v65, v162, v65 row_newbcast:1 row_mask:0xf bank_mask:0xf\n"
"v_mul_f32_dpp v66, v162, v66 row_newbcast:2 row_mask:0xf bank_mask:0xf\n"
"v_mul_f32_dpp v67, v162, v67 row_newbcast:3 row_mask:0xf bank_mask:0xf\n"
"v_mul_f32_dpp v68, v162, v68 row_newbcast:0 row_mask:0xf bank_mask:0xf\n"
"v_mul_f32_dpp v69, v162, v69 row_newbcast:1 row_mask:0xf bank_mask:0xf\n"
"v_mul_f32_dpp v70, v162, v70 row_newbcast:2 row_mask:0xf bank_mask:0xf\n"
"v_mul_f32_dpp v71, v162, v71 row_newbcast:3 row_mask:0xf bank_mask:0xf\n"
"v_mul_f32_dpp v72, v162, v72 row_newbcast:0 row_mask:0xf bank_mask:0xf\n"
"v_mul_f32_dpp v73, v162, v73 row_newbcast:1 row_mask:0xf bank_mask:0xf\n"
"v_mul_f32_dpp v74, v162, v74 row_newbcast:2 row_mask:0xf bank_mask:0xf\n"
"v_mul_f32_dpp v75, v162, v75 row_newbcast:3 row_mask:0xf bank_mask:0xf\n"
"v_mul_f32_dpp v76, v162, v76 row_newbcast:0 row_mask:0xf bank_mask:0xf\n"
"v_mul_f32_dpp v77, v162, v77 row_newbcast:1 row_mask:0xf bank_mask:0xf\n"
"v_mul_f32_dpp v78, v162, v78 row_newbcast:2 row_mask:0xf bank_mask:0xf\n"
"v_mul_f32_dpp v79, v162, v79 row_newbcast:3 row_mask:0xf bank_mask:0xf\n"
"v_mul_f32_dpp v80, v162, v80 row_newbcast:0 row_mask:0xf bank_mask:0xf\n"
"v_mul_f32_dpp v81, v162, v81 row_newbcast:1 row_mask:0xf bank_mask:0xf\n"
"v_mul_f32_dpp v82, v162, v82 row_newbcast:2 row_mask:0xf bank_mask:0xf\n"
"v_mul_f32_dpp v83, v162, v83 row_newbcast:3 row_mask:0xf bank_mask:0xf\n"
"v_mul_f32_dpp v84, v162, v84 row_newbcast:0 row_mask:0xf bank_mask:0xf\n"
"v_mul_f32_dpp v85, v162, v85 row_newbcast:1 row_mask:0xf bank_mask:0xf\n"
"v_mul_f32_dpp v86, v162, v86 row_newbcast:2 row_mask:0xf bank_mask:0xf\n"
"v_mul_f32_dpp v87, v162, v87 row_newbcast:3 row_mask:0xf bank_mask:0xf\n"
"v_mul_f32_dpp v88, v162, v88 row_newbcast:0 row_mask:0xf bank_mask:0xf\n"
"v_mul_f32_dpp v89, v162, v89 row_newbcast:1 row_mask:0xf bank_mask:0xf\n"
"v_mul_f32_dpp v90, v162, v90 row_newbcast:2 row_mask:0xf bank_mask:0xf\n"
"v_mul_f32_dpp v91, v162, v91 row_newbcast:3 row_mask:0xf bank_mask:0xf\n"
"v_mul_f32_dpp v92, v162, v92 row_newbcast:0 row_mask:0xf bank_mask:0xf\n"
"v_mul_f32_dpp v93, v162, v93 row_newbcast:1 row_mask:0xf bank_mask:0xf\n"
"v_mul_f32_dpp v94, v162, v94 row_newbcast:2 row_mask:0xf bank_mask:0xf\n"
"v_mul_f32_dpp v95, v162, v95 row_newbcast:3 row_mask:0xf bank_mask:0xf\n"
"s_cmp_lg_u32 s83, s43\n"
"s_cbranch_scc1 LSK3A_%=\n"
"v_max3_f32 v165, v120, v121, v122\n"
"v_max3_f32 v165, v165, v123, v124\n"
"v_max3_f32 v165, v165, v125, v126\n"
"v_max3_f32 v165, v165, v127, v164\n"
"v_mov_b32 v166, v165\n"
"s_nop 1\n"
"v_permlane32_swap_b32 v165, v166\n"
"v_max_f32 v165, v165, v166\n"
"v_mov_b32 v166, v165\n"
"s_nop 1\n"
"v_permlane16_swap_b32 v165, v166\n"
"v_max_f32 v165, v165, v166\n"
"v_sub_f32 v166, v164, v165\n"
"v_exp_f32 v166, v166\n"
"v_mov_b32 v164, v165\n"
"v_sub_f32 v120, v120, v164\n"
"v_sub_f32 v121, v121, v164\n"
"v_sub_f32 v122, v122, v164\n"
"v_sub_f32 v123, v123, v164\n"
"v_sub_f32 v124, v124, v164\n"
"v_sub_f32 v125, v125, v164\n"
"v_sub_f32 v126, v126, v164\n"
"v_sub_f32 v127, v127, v164\n"
"v_exp_f32 v120, v120\n"
"v_exp_f32 v121, v121\n"
"v_exp_f32 v122, v122\n"
"v_exp_f32 v123, v123\n"
"v_exp_f32 v124, v124\n"
"v_exp_f32 v125, v125\n"
"v_exp_f32 v126, v126\n"
"v_exp_f32 v127, v127\n"
"v_mul_f32 v167, v167, v166\n"
"v_add_f32 v167, v167, v120\n"
"v_add_f32 v167, v167, v121\n"
"v_add_f32 v167, v167, v122\n"
"v_add_f32 v167, v167, v123\n"
"v_add_f32 v167, v167, v124\n"
"v_add_f32 v167, v167, v125\n"
"v_add_f32 v167, v167, v126\n"
"v_add_f32 v167, v167, v127\n"
"v_cvt_pk_bf16_f32 v120, v120, v121\n"
"v_cvt_pk_bf16_f32 v121, v122, v123\n"
"v_cvt_pk_bf16_f32 v122, v124, v125\n"
"v_cvt_pk_bf16_f32 v123, v126, v127\n"
"v_mov_b32_dpp v166, v166 row_ror:12 row_mask:0x2 bank_mask:0xf\n"
"s_nop 1\n"
"v_mov_b32_dpp v166, v166 row_ror:8 row_mask:0x4 bank_mask:0xf\n"
"s_nop 1\n"
"v_mov_b32_dpp v166, v166 row_ror:4 row_mask:0x8 bank_mask:0xf\n"
"v_accvgpr_read_b32 v124, a208\n"
"s_nop 1\n"
"v_mul_f32_dpp v124, v166, v124 row_newbcast:0 row_mask:0xf bank_mask:0xf\n"
"v_accvgpr_write_b32 a208, v124\n"
"v_accvgpr_read_b32 v125, a209\n"
"s_nop 1\n"
"v_mul_f32_dpp v125, v166, v125 row_newbcast:1 row_mask:0xf bank_mask:0xf\n"
"v_accvgpr_write_b32 a209, v125\n"
"v_accvgpr_read_b32 v126, a210\n"
"s_nop 1\n"
"v_mul_f32_dpp v126, v166, v126 row_newbcast:2 row_mask:0xf bank_mask:0xf\n"
"v_accvgpr_write_b32 a210, v126\n"
"v_accvgpr_read_b32 v127, a211\n"
"s_nop 1\n"
"v_mul_f32_dpp v127, v166, v127 row_newbcast:3 row_mask:0xf bank_mask:0xf\n"
"v_accvgpr_write_b32 a211, v127\n"
"v_accvgpr_read_b32 v124, a212\n"
"s_nop 1\n"
"v_mul_f32_dpp v124, v166, v124 row_newbcast:0 row_mask:0xf bank_mask:0xf\n"
"v_accvgpr_write_b32 a212, v124\n"
"v_accvgpr_read_b32 v125, a213\n"
"s_nop 1\n"
"v_mul_f32_dpp v125, v166, v125 row_newbcast:1 row_mask:0xf bank_mask:0xf\n"
"v_accvgpr_write_b32 a213, v125\n"
"v_accvgpr_read_b32 v126, a214\n"
"s_nop 1\n"
"v_mul_f32_dpp v126, v166, v126 row_newbcast:2 row_mask:0xf bank_mask:0xf\n"
"v_accvgpr_write_b32 a214, v126\n"
"v_accvgpr_read_b32 v127, a215\n"
"s_nop 1\n"
"v_mul_f32_dpp v127, v166, v127 row_newbcast:3 row_mask:0xf bank_mask:0xf\n"
"v_accvgpr_write_b32 a215, v127\n"
"v_accvgpr_read_b32 v124, a216\n"
"s_nop 1\n"
"v_mul_f32_dpp v124, v166, v124 row_newbcast:0 row_mask:0xf bank_mask:0xf\n"
"v_accvgpr_write_b32 a216, v124\n"
"v_accvgpr_read_b32 v125, a217\n"
"s_nop 1\n"
"v_mul_f32_dpp v125, v166, v125 row_newbcast:1 row_mask:0xf bank_mask:0xf\n"
"v_accvgpr_write_b32 a217, v125\n"
"v_accvgpr_read_b32 v126, a218\n"
"s_nop 1\n"
"v_mul_f32_dpp v126, v166, v126 row_newbcast:2 row_mask:0xf bank_mask:0xf\n"
"v_accvgpr_write_b32 a218, v126\n"
"v_accvgpr_read_b32 v127, a219\n"
"s_nop 1\n"
"v_mul_f32_dpp v127, v166, v127 row_newbcast:3 row_mask:0xf bank_mask:0xf\n"
"v_accvgpr_write_b32 a219, v127\n"
"v_accvgpr_read_b32 v124, a220\n"
"s_nop 1\n"
"v_mul_f32_dpp v124, v166, v124 row_newbcast:0 row_mask:0xf bank_mask:0xf\n"
"v_accvgpr_write_b32 a220, v124\n"
"v_accvgpr_read_b32 v125, a221\n"
"s_nop 1\n"
"v_mul_f32_dpp v125, v166, v125 row_newbcast:1 row_mask:0xf bank_mask:0xf\n"
"v_accvgpr_write_b32 a221, v125\n"
"v_accvgpr_read_b32 v126, a222\n"
"s_nop 1\n"
"v_mul_f32_dpp v126, v166, v126 row_newbcast:2 row_mask:0xf bank_mask:0xf\n"
"v_accvgpr_write_b32 a222, v126\n"
"v_accvgpr_read_b32 v127, a223\n"
"s_nop 1\n"
"v_mul_f32_dpp v127, v166, v127 row_newbcast:3 row_mask:0xf bank_mask:0xf\n"
"v_accvgpr_write_b32 a223, v127\n"
"v_accvgpr_read_b32 v124, a224\n"
"s_nop 1\n"
"v_mul_f32_dpp v124, v166, v124 row_newbcast:0 row_mask:0xf bank_mask:0xf\n"
"v_accvgpr_write_b32 a224, v124\n"
"v_accvgpr_read_b32 v125, a225\n"
"s_nop 1\n"
"v_mul_f32_dpp v125, v166, v125 row_newbcast:1 row_mask:0xf bank_mask:0xf\n"
"v_accvgpr_write_b32 a225, v125\n"
"v_accvgpr_read_b32 v126, a226\n"
"s_nop 1\n"
"v_mul_f32_dpp v126, v166, v126 row_newbcast:2 row_mask:0xf bank_mask:0xf\n"
"v_accvgpr_write_b32 a226, v126\n"
"v_accvgpr_read_b32 v127, a227\n"
"s_nop 1\n"
"v_mul_f32_dpp v127, v166, v127 row_newbcast:3 row_mask:0xf bank_mask:0xf\n"
"v_accvgpr_write_b32 a227, v127\n"
"v_accvgpr_read_b32 v124, a228\n"
"s_nop 1\n"
"v_mul_f32_dpp v124, v166, v124 row_newbcast:0 row_mask:0xf bank_mask:0xf\n"
"v_accvgpr_write_b32 a228, v124\n"
"v_accvgpr_read_b32 v125, a229\n"
"s_nop 1\n"
"v_mul_f32_dpp v125, v166, v125 row_newbcast:1 row_mask:0xf bank_mask:0xf\n"
"v_accvgpr_write_b32 a229, v125\n"
"v_accvgpr_read_b32 v126, a230\n"
"s_nop 1\n"
"v_mul_f32_dpp v126, v166, v126 row_newbcast:2 row_mask:0xf bank_mask:0xf\n"
"v_accvgpr_write_b32 a230, v126\n"
"v_accvgpr_read_b32 v127, a231\n"
"s_nop 1\n"
"v_mul_f32_dpp v127, v166, v127 row_newbcast:3 row_mask:0xf bank_mask:0xf\n"
"v_accvgpr_write_b32 a231, v127\n"
"v_accvgpr_read_b32 v124, a232\n"
"s_nop 1\n"
"v_mul_f32_dpp v124, v166, v124 row_newbcast:0 row_mask:0xf bank_mask:0xf\n"
"v_accvgpr_write_b32 a232, v124\n"
"v_accvgpr_read_b32 v125, a233\n"
"s_nop 1\n"
"v_mul_f32_dpp v125, v166, v125 row_newbcast:1 row_mask:0xf bank_mask:0xf\n"
"v_accvgpr_write_b32 a233, v125\n"
"v_accvgpr_read_b32 v126, a234\n"
"s_nop 1\n"
"v_mul_f32_dpp v126, v166, v126 row_newbcast:2 row_mask:0xf bank_mask:0xf\n"
"v_accvgpr_write_b32 a234, v126\n"
"v_accvgpr_read_b32 v127, a235\n"
"s_nop 1\n"
"v_mul_f32_dpp v127, v166, v127 row_newbcast:3 row_mask:0xf bank_mask:0xf\n"
"v_accvgpr_write_b32 a235, v127\n"
"v_accvgpr_read_b32 v124, a236\n"
"s_nop 1\n"
"v_mul_f32_dpp v124, v166, v124 row_newbcast:0 row_mask:0xf bank_mask:0xf\n"
"v_accvgpr_write_b32 a236, v124\n"
"v_accvgpr_read_b32 v125, a237\n"
"s_nop 1\n"
"v_mul_f32_dpp v125, v166, v125 row_newbcast:1 row_mask:0xf bank_mask:0xf\n"
"v_accvgpr_write_b32 a237, v125\n"
"v_accvgpr_read_b32 v126, a238\n"
"s_nop 1\n"
"v_mul_f32_dpp v126, v166, v126 row_newbcast:2 row_mask:0xf bank_mask:0xf\n"
"v_accvgpr_write_b32 a238, v126\n"
"v_accvgpr_read_b32 v127, a239\n"
"s_nop 1\n"
"v_mul_f32_dpp v127, v166, v127 row_newbcast:3 row_mask:0xf bank_mask:0xf\n"
"v_accvgpr_write_b32 a239, v127\n"
"LSK3A_%=:\n"
"s_nop 3\n"
"buffer_load_ushort v128, v146, s[88:91], s80 offen offset:0\n"
"buffer_load_ushort v129, v146, s[88:91], s80 offen offset:256\n"
"buffer_load_ushort v130, v146, s[88:91], s80 offen offset:512\n"
"buffer_load_ushort v131, v146, s[88:91], s80 offen offset:768\n"
"buffer_load_ushort v132, v146, s[88:91], s81 offen offset:0\n"
"buffer_load_ushort v133, v146, s[88:91], s81 offen offset:256\n"
"buffer_load_ushort v134, v146, s[88:91], s81 offen offset:512\n"
"buffer_load_ushort v135, v146, s[88:91], s81 offen offset:768\n"
"buffer_load_ushort v136, v146, s[88:91], s80 offen offset:32\n"
"buffer_load_ushort v137, v146, s[88:91], s80 offen offset:288\n"
"buffer_load_ushort v138, v146, s[88:91], s80 offen offset:544\n"
"buffer_load_ushort v139, v146, s[88:91], s80 offen offset:800\n"
"buffer_load_ushort v140, v146, s[88:91], s81 offen offset:32\n"
"buffer_load_ushort v141, v146, s[88:91], s81 offen offset:288\n"
"buffer_load_ushort v142, v146, s[88:91], s81 offen offset:544\n"
"buffer_load_ushort v143, v146, s[88:91], s81 offen offset:800\n"
"s_waitcnt vmcnt(0)\n"
"v_lshl_or_b32 v128, v129, 16, v128\n"
"v_lshl_or_b32 v129, v131, 16, v130\n"
"v_lshl_or_b32 v130, v133, 16, v132\n"
"v_lshl_or_b32 v131, v135, 16, v134\n"
"v_lshl_or_b32 v136, v137, 16, v136\n"
"v_lshl_or_b32 v137, v139, 16, v138\n"
"v_lshl_or_b32 v138, v141, 16, v140\n"
"v_lshl_or_b32 v139, v143, 16, v142\n"
"v_mfma_f32_16x16x32_bf16 v[0:3], v[96:99], v[128:131], v[0:3]\n"
"s_nop 0\n"
"v_mfma_f32_16x16x32_bf16 v[4:7], v[96:99], v[136:139], v[4:7]\n"
"v_mfma_f32_16x16x32_bf16 v[32:35], v[104:107], v[128:131], v[32:35]\n"
"v_mfma_f32_16x16x32_bf16 v[36:39], v[104:107], v[136:139], v[36:39]\n"
"v_mfma_f32_16x16x32_bf16 v[64:67], v[112:115], v[128:131], v[64:67]\n"
"v_mfma_f32_16x16x32_bf16 v[68:71], v[112:115], v[136:139], v[68:71]\n"
"s_cmp_lg_u32 s83, s43\n"
"s_cbranch_scc1 LSK3B0_%=\n"
"v_mfma_f32_16x16x32_bf16 a[208:211], v[120:123], v[128:131], a[208:211]\n"
"v_mfma_f32_16x16x32_bf16 a[212:215], v[120:123], v[136:139], a[212:215]\n"
"LSK3B0_%=:\n"
"s_nop 3\n"
"buffer_load_ushort v128, v147, s[88:91], s80 offen offset:0\n"
"buffer_load_ushort v129, v147, s[88:91], s80 offen offset:256\n"
"buffer_load_ushort v130, v147, s[88:91], s80 offen offset:512\n"
"buffer_load_ushort v131, v147, s[88:91], s80 offen offset:768\n"
"buffer_load_ushort v132, v147, s[88:91], s81 offen offset:0\n"
"buffer_load_ushort v133, v147, s[88:91], s81 offen offset:256\n"
"buffer_load_ushort v134, v147, s[88:91], s81 offen offset:512\n"
"buffer_load_ushort v135, v147, s[88:91], s81 offen offset:768\n"
"buffer_load_ushort v136, v147, s[88:91], s80 offen offset:32\n"
"buffer_load_ushort v137, v147, s[88:91], s80 offen offset:288\n"
"buffer_load_ushort v138, v147, s[88:91], s80 offen offset:544\n"
"buffer_load_ushort v139, v147, s[88:91], s80 offen offset:800\n"
"buffer_load_ushort v140, v147, s[88:91], s81 offen offset:32\n"
"buffer_load_ushort v141, v147, s[88:91], s81 offen offset:288\n"
"buffer_load_ushort v142, v147, s[88:91], s81 offen offset:544\n"
"buffer_load_ushort v143, v147, s[88:91], s81 offen offset:800\n"
"s_waitcnt vmcnt(0)\n"
"v_lshl_or_b32 v128, v129, 16, v128\n"
"v_lshl_or_b32 v129, v131, 16, v130\n"
"v_lshl_or_b32 v130, v133, 16, v132\n"
"v_lshl_or_b32 v131, v135, 16, v134\n"
"v_lshl_or_b32 v136, v137, 16, v136\n"
"v_lshl_or_b32 v137, v139, 16, v138\n"
"v_lshl_or_b32 v138, v141, 16, v140\n"
"v_lshl_or_b32 v139, v143, 16, v142\n"
"v_mfma_f32_16x16x32_bf16 v[8:11], v[96:99], v[128:131], v[8:11]\n"
"s_nop 0\n"
"v_mfma_f32_16x16x32_bf16 v[12:15], v[96:99], v[136:139], v[12:15]\n"
"v_mfma_f32_16x16x32_bf16 v[40:43], v[104:107], v[128:131], v[40:43]\n"
"v_mfma_f32_16x16x32_bf16 v[44:47], v[104:107], v[136:139], v[44:47]\n"
"v_mfma_f32_16x16x32_bf16 v[72:75], v[112:115], v[128:131], v[72:75]\n"
"v_mfma_f32_16x16x32_bf16 v[76:79], v[112:115], v[136:139], v[76:79]\n"
"s_cmp_lg_u32 s83, s43\n"
"s_cbranch_scc1 LSK3B1_%=\n"
"v_mfma_f32_16x16x32_bf16 a[216:219], v[120:123], v[128:131], a[216:219]\n"
"v_mfma_f32_16x16x32_bf16 a[220:223], v[120:123], v[136:139], a[220:223]\n"
"LSK3B1_%=:\n"
"s_nop 3\n"
"buffer_load_ushort v128, v146, s[88:91], s80 offen offset:128\n"
"buffer_load_ushort v129, v146, s[88:91], s80 offen offset:384\n"
"buffer_load_ushort v130, v146, s[88:91], s80 offen offset:640\n"
"buffer_load_ushort v131, v146, s[88:91], s80 offen offset:896\n"
"buffer_load_ushort v132, v146, s[88:91], s81 offen offset:128\n"
"buffer_load_ushort v133, v146, s[88:91], s81 offen offset:384\n"
"buffer_load_ushort v134, v146, s[88:91], s81 offen offset:640\n"
"buffer_load_ushort v135, v146, s[88:91], s81 offen offset:896\n"
"buffer_load_ushort v136, v146, s[88:91], s80 offen offset:160\n"
"buffer_load_ushort v137, v146, s[88:91], s80 offen offset:416\n"
"buffer_load_ushort v138, v146, s[88:91], s80 offen offset:672\n"
"buffer_load_ushort v139, v146, s[88:91], s80 offen offset:928\n"
"buffer_load_ushort v140, v146, s[88:91], s81 offen offset:160\n"
"buffer_load_ushort v141, v146, s[88:91], s81 offen offset:416\n"
"buffer_load_ushort v142, v146, s[88:91], s81 offen offset:672\n"
"buffer_load_ushort v143, v146, s[88:91], s81 offen offset:928\n"
"s_waitcnt vmcnt(0)\n"
"v_lshl_or_b32 v128, v129, 16, v128\n"
"v_lshl_or_b32 v129, v131, 16, v130\n"
"v_lshl_or_b32 v130, v133, 16, v132\n"
"v_lshl_or_b32 v131, v135, 16, v134\n"
"v_lshl_or_b32 v136, v137, 16, v136\n"
"v_lshl_or_b32 v137, v139, 16, v138\n"
"v_lshl_or_b32 v138, v141, 16, v140\n"
"v_lshl_or_b32 v139, v143, 16, v142\n"
"v_mfma_f32_16x16x32_bf16 v[16:19], v[96:99], v[128:131], v[16:19]\n"
"s_nop 0\n"
"v_mfma_f32_16x16x32_bf16 v[20:23], v[96:99], v[136:139], v[20:23]\n"
"v_mfma_f32_16x16x32_bf16 v[48:51], v[104:107], v[128:131], v[48:51]\n"
"v_mfma_f32_16x16x32_bf16 v[52:55], v[104:107], v[136:139], v[52:55]\n"
"v_mfma_f32_16x16x32_bf16 v[80:83], v[112:115], v[128:131], v[80:83]\n"
"v_mfma_f32_16x16x32_bf16 v[84:87], v[112:115], v[136:139], v[84:87]\n"
"s_cmp_lg_u32 s83, s43\n"
"s_cbranch_scc1 LSK3B2_%=\n"
"v_mfma_f32_16x16x32_bf16 a[224:227], v[120:123], v[128:131], a[224:227]\n"
"v_mfma_f32_16x16x32_bf16 a[228:231], v[120:123], v[136:139], a[228:231]\n"
"LSK3B2_%=:\n"
"s_nop 3\n"
"buffer_load_ushort v128, v147, s[88:91], s80 offen offset:128\n"
"buffer_load_ushort v129, v147, s[88:91], s80 offen offset:384\n"
"buffer_load_ushort v130, v147, s[88:91], s80 offen offset:640\n"
"buffer_load_ushort v131, v147, s[88:91], s80 offen offset:896\n"
"buffer_load_ushort v132, v147, s[88:91], s81 offen offset:128\n"
"buffer_load_ushort v133, v147, s[88:91], s81 offen offset:384\n"
"buffer_load_ushort v134, v147, s[88:91], s81 offen offset:640\n"
"buffer_load_ushort v135, v147, s[88:91], s81 offen offset:896\n"
"buffer_load_ushort v136, v147, s[88:91], s80 offen offset:160\n"
"buffer_load_ushort v137, v147, s[88:91], s80 offen offset:416\n"
"buffer_load_ushort v138, v147, s[88:91], s80 offen offset:672\n"
"buffer_load_ushort v139, v147, s[88:91], s80 offen offset:928\n"
"buffer_load_ushort v140, v147, s[88:91], s81 offen offset:160\n"
"buffer_load_ushort v141, v147, s[88:91], s81 offen offset:416\n"
"buffer_load_ushort v142, v147, s[88:91], s81 offen offset:672\n"
"buffer_load_ushort v143, v147, s[88:91], s81 offen offset:928\n"
"s_waitcnt vmcnt(0)\n"
"v_lshl_or_b32 v128, v129, 16, v128\n"
"v_lshl_or_b32 v129, v131, 16, v130\n"
"v_lshl_or_b32 v130, v133, 16, v132\n"
"v_lshl_or_b32 v131, v135, 16, v134\n"
"v_lshl_or_b32 v136, v137, 16, v136\n"
"v_lshl_or_b32 v137, v139, 16, v138\n"
"v_lshl_or_b32 v138, v141, 16, v140\n"
"v_lshl_or_b32 v139, v143, 16, v142\n"
"v_mfma_f32_16x16x32_bf16 v[24:27], v[96:99], v[128:131], v[24:27]\n"
"s_nop 0\n"
"v_mfma_f32_16x16x32_bf16 v[28:31], v[96:99], v[136:139], v[28:31]\n"
"v_mfma_f32_16x16x32_bf16 v[56:59], v[104:107], v[128:131], v[56:59]\n"
"v_mfma_f32_16x16x32_bf16 v[60:63], v[104:107], v[136:139], v[60:63]\n"
"v_mfma_f32_16x16x32_bf16 v[88:91], v[112:115], v[128:131], v[88:91]\n"
"v_mfma_f32_16x16x32_bf16 v[92:95], v[112:115], v[136:139], v[92:95]\n"
"s_cmp_lg_u32 s83, s43\n"
"s_cbranch_scc1 LSK3B3_%=\n"
"v_mfma_f32_16x16x32_bf16 a[232:235], v[120:123], v[128:131], a[232:235]\n"
"v_mfma_f32_16x16x32_bf16 a[236:239], v[120:123], v[136:139], a[236:239]\n"
"LSK3B3_%=:\n"
"s_nop 3\n"
"s_nop 9\n"
"s_add_u32 s80, s80, 8192\n"
"s_add_u32 s81, s81, 8192\n"
"s_add_u32 s82, s82, 1\n"
"s_cmp_lt_u32 s82, 32\n"
"s_cbranch_scc1 LSLOOP_%=\n"
"v_mov_b32 v154, v155\n"
"s_nop 1\n"
"v_permlane32_swap_b32 v155, v154\n"
"v_add_f32 v155, v155, v154\n"
"v_mov_b32 v154, v155\n"
"s_nop 1\n"
"v_permlane16_swap_b32 v155, v154\n"
"v_add_f32 v155, v155, v154\n"
"v_mov_b32 v196, v155\n"
"v_mov_b32 v158, v159\n"
"s_nop 1\n"
"v_permlane32_swap_b32 v159, v158\n"
"v_add_f32 v159, v159, v158\n"
"v_mov_b32 v158, v159\n"
"s_nop 1\n"
"v_permlane16_swap_b32 v159, v158\n"
"v_add_f32 v159, v159, v158\n"
"v_mov_b32 v198, v159\n"
"v_mov_b32 v162, v163\n"
"s_nop 1\n"
"v_permlane32_swap_b32 v163, v162\n"
"v_add_f32 v163, v163, v162\n"
"v_mov_b32 v162, v163\n"
"s_nop 1\n"
"v_permlane16_swap_b32 v163, v162\n"
"v_add_f32 v163, v163, v162\n"
"v_mov_b32 v200, v163\n"
"v_mov_b32 v166, v167\n"
"s_nop 1\n"
"v_permlane32_swap_b32 v167, v166\n"
"v_add_f32 v167, v167, v166\n"
"v_mov_b32 v166, v167\n"
"s_nop 1\n"
"v_permlane16_swap_b32 v167, v166\n"
"v_add_f32 v167, v167, v166\n"
"v_mov_b32 v202, v167\n"
"v_sub_f32 v192, 0, v164\n"
"v_mov_b32 v96, 0\n"
"v_mov_b32 v97, 0\n"
"v_mov_b32 v98, 0\n"
"v_mov_b32 v99, 0\n"
"v_mov_b32 v100, 0\n"
"v_mov_b32 v101, 0\n"
"v_mov_b32 v102, 0\n"
"v_mov_b32 v103, 0\n"
"v_mov_b32 v104, 0\n"
"v_mov_b32 v105, 0\n"
"v_mov_b32 v106, 0\n"
"v_mov_b32 v107, 0\n"
"v_mov_b32 v108, 0\n"
"v_mov_b32 v109, 0\n"
"v_mov_b32 v110, 0\n"
"v_mov_b32 v111, 0\n"
"v_mov_b32 v112, 0\n"
"v_mov_b32 v113, 0\n"
"v_mov_b32 v114, 0\n"
"v_mov_b32 v115, 0\n"
"v_mov_b32 v116, 0\n"
"v_mov_b32 v117, 0\n"
"v_mov_b32 v118, 0\n"
"v_mov_b32 v119, 0\n"
"v_mov_b32 v120, 0\n"
"v_mov_b32 v121, 0\n"
"v_mov_b32 v122, 0\n"
"v_mov_b32 v123, 0\n"
"v_mov_b32 v204, 0\n"
"v_mov_b32 v205, 0\n"
"s_nop 3\n"
"s_mov_b32 s68, 0\n"
"s_cmp_eq_u32 s47, 4\n"
"s_cbranch_scc1 LBACK0_%=\n"
"s_cmp_eq_u32 s47, 8\n"
"s_cbranch_scc1 LBACK1_%=\n"
"s_cmp_eq_u32 s47, 12\n"
"s_cbranch_scc1 LBACK2_%=\n"
"s_branch LBACK3_%=\n"
"LFIN_%=:\n"
    :: [kalo]"s"((unsigned)ka), [kahi]"s"((unsigned)(ka >> 32)), [wg]"s"(blockIdx.x), [wid]"s"(wid)
    : "v0","v1","v2","v3","v4","v5","v6","v7","v8","v9","v10","v11","v12","v13","v14","v15","v16","v17","v18","v19","v20","v21","v22","v23","v24","v25","v26","v27","v28","v29","v30","v31","v32","v33","v34","v35","v36","v37","v38","v39","v40","v41","v42","v43","v44","v45","v46","v47","v48","v49","v50","v51","v52","v53","v54","v55","v56","v57","v58","v59","v60","v61","v62","v63","v64","v65","v66","v67","v68","v69","v70","v71","v72","v73","v74","v75","v76","v77","v78","v79","v80","v81","v82","v83","v84","v85","v86","v87","v88","v89","v90","v91","v92","v93","v94","v95","v96","v97","v98","v99","v100","v101","v102","v103","v104","v105","v106","v107","v108","v109","v110","v111","v112","v113","v114","v115","v116","v117","v118","v119","v120","v121","v122","v123","v124","v125","v126","v127","v128","v129","v130","v131","v132","v133","v134","v135","v136","v137","v138","v139","v140","v141","v142","v143","v144","v145","v146","v147","v148","v149","v150","v151","v152","v153","v154","v155","v156","v157","v158","v159","v160","v161","v162","v163","v164","v165","v166","v167","v168","v169","v170","v171","v172","v173","v174","v175","v176","v177","v178","v179","v180","v181","v182","v183","v184","v185","v186","v187","v188","v189","v190","v191","v192","v193","v194","v195","v196","v197","v198","v199","v200","v201","v202","v203","v204","v205","v206","v207","v208","v209","v210","v211","v212","v213","v214","v215","v216","v217","v218","v219","v220","v221","v222","v223","v224","v225","v226","v227","v228","v229","v230","v231","v232","v233","v234","v235","v236","v237","v238","v239","v240","v241","v242","v243","v244","v245","v246","v247","v248","v249","v250","v251","v252","v253","v254","v255","a0","a1","a2","a3","a4","a5","a6","a7","a8","a9","a10","a11","a12","a13","a14","a15","a16","a17","a18","a19","a20","a21","a22","a23","a24","a25","a26","a27","a28","a29","a30","a31","a32","a33","a34","a35","a36","a37","a38","a39","a40","a41","a42","a43","a44","a45","a46","a47","a48","a49","a50","a51","a52","a53","a54","a55","a56","a57","a58","a59","a60","a61","a62","a63","a64","a65","a66","a67","a68","a69","a70","a71","a72","a73","a74","a75","a76","a77","a78","a79","a80","a81","a82","a83","a84","a85","a86","a87","a88","a89","a90","a91","a92","a93","a94","a95","a96","a97","a98","a99","a100","a101","a102","a103","a104","a105","a106","a107","a108","a109","a110","a111","a112","a113","a114","a115","a116","a117","a118","a119","a120","a121","a122","a123","a124","a125","a126","a127","a128","a129","a130","a131","a132","a133","a134","a135","a136","a137","a138","a139","a140","a141","a142","a143","a144","a145","a146","a147","a148","a149","a150","a151","a152","a153","a154","a155","a156","a157","a158","a159","a160","a161","a162","a163","a164","a165","a166","a167","a168","a169","a170","a171","a172","a173","a174","a175","a176","a177","a178","a179","a180","a181","a182","a183","a184","a185","a186","a187","a188","a189","a190","a191","a192","a193","a194","a195","a196","a197","a198","a199","a200","a201","a202","a203","a204","a205","a206","a207","a208","a209","a210","a211","a212","a213","a214","a215","a216","a217","a218","a219","a220","a221","a222","a223","a224","a225","a226","a227","a228","a229","a230","a231","a232","a233","a234","a235","a236","a237","a238","a239","a240","a241","a242","a243","a244","a245","a246","a247","a248","a249","a250","a251","a252","a253","a254","a255","s6","s7","s8","s9","s10","s11","s12","s13","s14","s15","s16","s17","s18","s19","s20","s21","s22","s23","s24","s25","s26","s27","s28","s29","s30","s31","s32","s33","s34","s35","s36","s37","s38","s39","s40","s41","s42","s43","s44","s45","s46","s47","s48","s49","s50","s51","s52","s53","s54","s55","s56","s57","s58","s59","s60","s61","s62","s63","s64","s65","s66","s67","s68","s69","s70","s71","s72","s73","s74","s75","s76","s77","s78","s79","s80","s81","s82","s83","s84","s85","s86","s87","s88","s89","s90","s91","s92","s93","s94","s95","s96","s97","s98","s99","s100","vcc","scc","m0","memory");
}
extern "C" void kernel_launch(void* const* d_in, const int* in_sizes, int n_in, void* d_out, int out_size, void* d_ws, size_t ws_size, hipStream_t stream) {
  char* xch = (char*)d_ws + ((ws_size - (size_t)270532608) & ~(size_t)0xfffff);
  (void)hipFuncSetAttribute((const void*)fused_attn, hipFuncAttributeMaxDynamicSharedMemorySize, 163840);
  hipLaunchKernelGGL(fused_attn, dim3(256), dim3(256), 163840, stream,
                     (const char*)d_in[0], (const char*)d_in[1], (const char*)d_in[2], (const char*)d_in[3], (const char*)d_in[4],
                     (const char*)d_in[5], (const char*)d_in[6], (const char*)d_in[7], (const char*)d_in[8], (const char*)d_in[9], xch, (char*)d_out, (char*)d_ws);
}
```

```cpp
#include <hip/hip_runtime.h>
#include <hip/hip_bf16.h>
#include <stdint.h>

__global__ __launch_bounds__(256, 1)
void fused_attn(const char* xA, const char* kA, const char* vA, const char* wA, const char* uA,
                const char* xB, const char* kB, const char* vB, const char* wB, const char* uB, char* xch, char* out, char* flags) {
  extern __shared__ __attribute__((aligned(16))) char lds[];
  const unsigned wid = __builtin_amdgcn_readfirstlane(threadIdx.x >> 6);
  const unsigned long long ka = (unsigned long long)__builtin_amdgcn_kernarg_segment_ptr();
  asm volatile(
"s_memtime s[64:65]\n"
"s_memrealtime s[66:67]\n"
"s_bfe_u32 s10, %[wg], 0x10002\n"
"s_mul_i32 s40, s10, 40\n"
"s_add_u32 s40, s40, %[kalo]\n"
"s_addc_u32 s41, %[kahi], 0\n"
"s_mov_b32 s78, %[kalo]\n"
"s_mov_b32 s79, %[kahi]\n"
"s_load_dwordx2 s[24:25], s[40:41], 0\n"
"s_load_dwordx2 s[28:29], s[40:41], 24\n"
"s_load_dwordx2 s[6:7], s[78:79], 0x60\n"
"s_load_dwordx2 s[16:17], s[40:41], 8\n"
"s_load_dwordx2 s[20:21], s[40:41], 16\n"
"s_load_dwordx2 s[8:9], s[40:41], 32\n"
"s_load_dwordx4 s[12:15], s[78:79], 0x50\n"
"s_mov_b32 s42, %[wid]\n"
"s_and_b32 s11, %[wg], 1\n"
"s_lshl_b32 s11, s11, 3\n"
"s_lshr_b32 s60, %[wg], 5\n"
"s_add_u32 s11, s11, s60\n"
"s_bfe_u32 s61, %[wg], 0x10001\n"
"s_bfe_u32 s60, %[wg], 0x20003\n"
"s_lshl2_add_u32 s61, s61, s60\n"
"s_getpc_b64 s[80:81]\n"
"LCP_%=:\n"
"s_mov_b32 s82, LEND_%=-LCP_%=\n"
"s_mov_b32 s83, 0x20000\n"
"v_mbcnt_lo_u32_b32 v2, -1, 0\n"
"v_lshlrev_b32 v2, 7, v2\n"
"s_lshr_b32 s60, %[wg], 3\n"
"s_lshl2_add_u32 s60, s60, s42\n"
"s_mul_i32 s60, s60, 640\n"
"s_mov_b64 exec, 31\n"
"buffer_load_dword v3, v2, s[80:83], s60 offen\n"
"s_mov_b64 exec, -1\n"
"s_mov_b32 s19, 0x20000\n"
"s_mov_b32 s23, 0x20000\n"
"s_mov_b32 s27, 0x20000\n"
"s_mov_b32 s31, 0x20000\n"
"s_mov_b32 s35, 0x20000\n"
"s_mov_b32 s39, 0x20000\n"
"s_mov_b32 s26, 286720\n"
"s_lshl_b32 s44, s42, 12\n"
"s_mov_b32 s50, s44\n"
"s_mul_i32 s51, s42, 7168\n"
"s_add_u32 s51, s51, 0x10000\n"
"s_mov_b32 s74, 0x8000\n"
"s_mov_b32 s58, 524288\n"
"s_mov_b32 s47, 0\n"
"s_add_u32 s60, s11, s61\n"
"s_and_b32 s60, s60, 0\n"
"s_mul_i32 s60, s60, 3\n"
"s_lshl_b32 s49, s60, 7\n"
"s_lshl_b32 s48, s60, 19\n"
"v_mbcnt_lo_u32_b32 v4, -1, 0\n"
"v_mbcnt_hi_u32_b32 v4, -1, v4\n"
"v_and_b32 v5, 15, v4\n"
"v_lshrrev_b32 v6, 4, v4\n"
"v_lshrrev_b32 v7, 3, v4\n"
"v_and_b32 v8, 7, v4\n"
"v_lshlrev_b32 v9, 1, v6\n"
"v_xor_b32 v9, v9, v8\n"
"v_lshlrev_b32 v9, 4, v9\n"
"s_mul_i32 s60, s42, 71680\n"
"v_mul_u32_u24 v10, 1280, v7\n"
"v_add3_u32 v248, v10, v9, s60\n"
"v_add_u32 v249, 9216, v248\n"
"v_add_u32 v254, 18432, v248\n"
"v_add_u32 v255, 27648, v248\n"
"v_and_b32 v10, 2, v7\n"
"v_xor_b32 v240, v10, v8\n"
"v_lshlrev_b32 v240, 4, v240\n"
"v_bfe_u32 v10, v7, 0, 1\n"
"v_lshl_add_u32 v240, v10, 8, v240\n"
"v_bfe_u32 v10, v7, 1, 1\n"
"v_lshl_add_u32 v240, v10, 9, v240\n"
"v_bfe_u32 v10, v7, 2, 1\n"
"v_lshl_add_u32 v240, v10, 7, v240\n"
"s_and_b32 s60, s42, 1\n"
"v_lshl_add_u32 v240, s60, 15, v240\n"
"s_lshr_b32 s60, s42, 1\n"
"v_lshl_add_u32 v240, s60, 16, v240\n"
"v_and_b32 v10, 1, v6\n"
"v_xor_b32 v10, v10, v8\n"
"v_lshlrev_b32 v10, 4, v10\n"
"v_lshl_add_u32 v10, v7, 13, v10\n"
"s_lshl_b32 s60, s42, 17\n"
"v_add_u32 v10, s60, v10\n"
"v_add_u32 v224, 1024, v10\n"
"v_add_u32 v225, 128, v10\n"
"v_xor_b32 v10, 64, v10\n"
"v_add_u32 v226, 64512, v10\n"
"v_add_u32 v227, 63616, v10\n"
"s_mul_i32 s40, s11, 286720\n"
"s_lshl_b32 s41, s61, 10\n"
"s_sub_u32 s30, 5243904, s41\n"
"s_sub_u32 s41, s41, 1024\n"
"s_ashr_i32 s60, s41, 31\n"
"s_lshl4_add_u32 s92, s10, s11\n"
"s_lshl_b32 s93, s92, 9\n"
"v_mov_b32 v0, 0\n"
"s_waitcnt lgkmcnt(0)\n"
"s_add_u32 s24, s24, s40\n"
"s_addc_u32 s25, s25, 0\n"
"s_and_b32 s25, s25, 0xffff\n"
"s_add_u32 s28, s28, s41\n"
"s_addc_u32 s29, s29, s60\n"
"s_and_b32 s29, s29, 0xffff\n"
"s_add_u32 s6, s6, s93\n"
"s_addc_u32 s7, s7, 0\n"
"buffer_load_dword v12, v248, s[24:27], s49 offen\n"
"buffer_load_dword v13, v224, s[28:31], s48 offen\n"
"global_load_dword v1, v0, s[6:7] offset:128 sc1\n"
"s_mov_b32 m0, s51\n"
"s_nop 0\n"
"buffer_load_dwordx4 v248, s[24:27], s49 offen lds\n"
"buffer_load_dwordx4 v249, s[24:27], s49 offen offset:1024 lds\n"
"buffer_load_dwordx4 v254, s[24:27], s49 offen offset:2048 lds\n"
"buffer_load_dwordx4 v255, s[24:27], s49 offen offset:3072 lds\n"
"s_add_u32 m0, s51, 4096\n"
"s_add_u32 s40, s49, 40960\n"
"buffer_load_dwordx4 v248, s[24:27], s40 offen lds\n"
"buffer_load_dwordx4 v249, s[24:27], s40 offen offset:1024 lds\n"
"buffer_load_dwordx4 v254, s[24:27], s40 offen offset:2048 lds\n"
"s_add_u32 s49, s49, 128\n"
"s_sub_u32 s41, s49, 1280\n"
"s_min_u32 s49, s49, s41\n"
"s_xor_b32 s51, s51, s74\n"
"s_mov_b32 m0, s50\n"
"s_nop 0\n"
"buffer_load_dwordx4 v224, s[28:31], s48 offen lds\n"
"buffer_load_dwordx4 v225, s[28:31], s48 offen offset:1024 lds\n"
"buffer_load_dwordx4 v226, s[28:31], s48 offen offset:2048 lds\n"
"buffer_load_dwordx4 v227, s[28:31], s48 offen offset:3072 lds\n"
"s_xor_b32 s50, s50, s74\n"
"s_lshl2_add_u32 s63, s11, s42\n"
"s_mul_i32 s63, s63, 10240\n"
"s_and_b32 s78, s42, 1\n"
"s_lshl_b32 s43, s78, 6\n"
"s_mov_b32 s77, 0x80000000\n"
"s_mov_b32 s75, 0x10000\n"
"s_mov_b32 s73, 0xffff0000\n"
"s_mov_b32 s46, 0x3e0293ee\n"
"s_mov_b32 s45, 0x63800000\n"
"s_mov_b32 s62, 9\n"
"v_lshl_add_u32 v242, v4, 4, s44\n"
"s_lshr_b32 s79, s42, 1\n"
"s_lshl4_add_u32 s76, s79, s78\n"
"s_lshl_b32 s76, s76, 10\n"
"s_add_u32 s76, s76, 0x20000\n"
"s_sub_u32 s76, s76, s44\n"
"v_lshlrev_b32 v9, 6, v6\n"
"v_xor_b32 v9, v9, v242\n"
"v_add_u32 v243, s76, v9\n"
"v_and_b32 v9, 6, v5\n"
"s_xor_b32 m0, s50, 0xc000\n"
"s_add_u32 s40, s48, 256\n"
"buffer_load_dwordx4 v224, s[28:31], s40 offen lds\n"
"v_xor_b32 v9, v9, v6\n"
"v_lshlrev_b32 v9, 4, v9\n"
"v_lshl_add_u32 v10, v5, 7, v9\n"
"s_mul_i32 s79, s42, 6144\n"
"s_add_u32 s79, s79, 0x10000\n"
"v_add_u32 v246, s79, v10\n"
"v_xor_b32 v247, 64, v246\n"
"s_lshr_b32 s79, s42, 1\n"
"s_lshl_b32 s79, s79, 11\n"
"buffer_load_dwordx4 v225, s[28:31], s40 offen offset:1024 lds\n"
"s_add_u32 s79, s79, 90112\n"
"v_add_u32 v252, s79, v10\n"
"v_xor_b32 v253, 64, v252\n"
"s_xor_b32 s79, s42, 1\n"
"s_mul_i32 s79, s79, 6144\n"
"s_add_u32 s79, s79, 0x10000\n"
"v_add_u32 v250, s79, v10\n"
"v_xor_b32 v251, 64, v250\n"
"s_mul_i32 s79, s78, 8256\n"
"buffer_load_dwordx4 v226, s[28:31], s40 offen offset:2048 lds\n"
"v_lshlrev_b32 v9, 10, v6\n"
"v_bfe_u32 v10, v4, 2, 2\n"
"v_lshl_add_u32 v9, v10, 7, v9\n"
"v_and_b32 v10, 3, v4\n"
"v_lshl_add_u32 v9, v10, 3, v9\n"
"v_bfe_u32 v10, v4, 3, 2\n"
"v_lshl_add_u32 v9, v10, 5, v9\n"
"s_add_u32 s79, s79, 0x10000\n"
"v_xor_b32 v232, s79, v9\n"
"buffer_load_dwordx4 v227, s[28:31], s40 offen offset:3072 lds\n"
"s_cmp_eq_u32 s47, 8\n"
"s_cselect_b32 s41, s52, s58\n"
"s_add_u32 s48, s48, s41\n"
"s_sub_u32 s41, s48, 5242880\n"
"s_min_u32 s48, s48, s41\n"
"v_xor_b32 v233, 32, v232\n"
"v_xor_b32 v234, 64, v232\n"
"v_xor_b32 v235, 96, v232\n"
"v_xor_b32 v236, 0x2000, v232\n"
"v_xor_b32 v237, 0x2000, v233\n"
"v_xor_b32 v238, 0x2000, v234\n"
"v_xor_b32 v239, 0x2000, v235\n"
"v_lshlrev_b32 v9, 11, v6\n"
"v_bfe_u32 v10, v4, 2, 2\n"
"s_mov_b32 m0, s50\n"
"s_nop 0\n"
"buffer_load_dwordx4 v224, s[28:31], s48 offen lds\n"
"v_lshl_add_u32 v9, v10, 7, v9\n"
"v_and_b32 v10, 1, v4\n"
"v_lshl_add_u32 v9, v10, 3, v9\n"
"v_bfe_u32 v10, v4, 1, 1\n"
"v_lshl_add_u32 v9, v10, 5, v9\n"
"v_bfe_u32 v10, v4, 3, 1\n"
"v_lshl_add_u32 v9, v10, 4, v9\n"
"v_bfe_u32 v10, v4, 4, 1\n"
"v_lshl_add_u32 v9, v10, 6, v9\n"
"buffer_load_dwordx4 v225, s[28:31], s48 offen offset:1024 lds\n"
"v_xor_b32 v244, s43, v9\n"
"v_xor_b32 v245, 16, v244\n"
"s_lshl3_add_u32 s79, s11, s61\n"
"s_lshl_b32 s79, s79, 20\n"
"s_sub_u32 s79, s79, 262144\n"
"s_ashr_i32 s60, s79, 31\n"
"s_add_u32 s16, s16, s79\n"
"s_addc_u32 s17, s17, s60\n"
"s_and_b32 s17, s17, 0xffff\n"
"buffer_load_dwordx4 v226, s[28:31], s48 offen offset:2048 lds\n"
"s_mov_b32 s18, 1310720\n"
"s_mov_b64 s[84:85], s[16:17]\n"
"s_mov_b64 s[86:87], s[18:19]\n"
"s_add_u32 s20, s20, s79\n"
"s_addc_u32 s21, s21, s60\n"
"s_and_b32 s21, s21, 0xffff\n"
"s_mov_b32 s22, 1310720\n"
"s_mov_b64 s[88:89], s[20:21]\n"
"s_mov_b64 s[90:91], s[22:23]\n"
"buffer_load_dwordx4 v227, s[28:31], s48 offen offset:3072 lds\n"
"s_xor_b32 s50, s50, s74\n"
"s_mov_b32 s57, 122880\n"
"s_bfe_u32 s55, %[wg], 65541\n"
"s_mul_i32 s55, s55, 1024\n"
"s_xor_b32 s57, s57, s55\n"
"s_bfe_u32 s55, %[wg], 65542\n"
"s_mul_i32 s55, s55, 2048\n"
"s_xor_b32 s57, s57, s55\n"
"s_and_b32 s54, %[wg], 144\n"
"s_bcnt1_i32_b32 s54, s54\n"
"s_mov_b32 m0, s51\n"
"s_nop 0\n"
"buffer_load_dwordx4 v248, s[24:27], s49 offen lds\n"
"s_and_b32 s54, s54, 1\n"
"s_lshl_b32 s54, s54, 13\n"
"s_xor_b32 s57, s57, s54\n"
"s_lshl_b32 s54, s54, 1\n"
"s_sub_u32 s54, 21504, s54\n"
"s_mov_b32 s55, 385024\n"
"s_xor_b32 s56, s55, s57\n"
"s_mov_b32 s59, 1024\n"
"s_add_u32 s52, s11, s61\n"
"buffer_load_dwordx4 v249, s[24:27], s49 offen offset:1024 lds\n"
"s_and_b32 s52, s52, 0\n"
"s_mul_i32 s52, s52, 3\n"
"s_add_u32 s52, s52, 1\n"
"s_lshl_b32 s53, s52, 7\n"
"s_lshl_b32 s52, s52, 19\n"
"s_add_u32 s52, s52, 512\n"
"s_mul_i32 s79, s61, 655360\n"
"s_add_u32 s8, s8, s79\n"
"s_addc_u32 s9, s9, 0\n"
"buffer_load_dwordx4 v254, s[24:27], s49 offen offset:2048 lds\n"
"s_and_b32 s9, s9, 0xffff\n"
"s_add_u32 s12, s12, 0x1fffff\n"
"s_addc_u32 s13, s13, 0\n"
"s_and_b32 s12, s12, 0xffe00000\n"
"s_mul_i32 s96, s42, 3\n"
"s_and_b32 s96, s96, 5\n"
"s_cmp_eq_u32 s42, 3\n"
"s_cselect_b32 s96, 5, s96\n"
"s_lshl_b32 s79, s61, 8\n"
"buffer_load_dwordx4 v255, s[24:27], s49 offen offset:3072 lds\n"
"s_add_u32 s96, s96, s79\n"
"s_add_u32 s96, s96, 160\n"
"s_lshl_b32 s96, s96, 12\n"
"s_mul_i32 s79, s92, 8388608\n"
"s_add_u32 s32, s12, s79\n"
"s_addc_u32 s33, s13, 0\n"
"s_and_b32 s33, s33, 0xffff\n"
"s_mov_b32 s34, 8388608\n"
"s_mul_i32 s79, s11, 573440\n"
"s_add_u32 m0, s51, 4096\n"
"s_add_u32 s40, s49, 40960\n"
"buffer_load_dwordx4 v248, s[24:27], s40 offen lds\n"
"s_mul_i32 s60, s10, 1280\n"
"s_sub_u32 s38, 573440, s60\n"
"s_add_u32 s79, s79, s60\n"
"s_add_u32 s36, s14, s79\n"
"s_addc_u32 s37, s15, 0\n"
"s_and_b32 s37, s37, 0xffff\n"
"s_lshl2_add_u32 s93, s61, s42\n"
"s_add_u32 s95, s61, 1\n"
"s_and_b32 s95, s95, 7\n"
"buffer_load_dwordx4 v249, s[24:27], s40 offen offset:1024 lds\n"
"s_lshl_b32 s95, s95, 7\n"
"s_add_u32 s100, s61, 2\n"
"s_and_b32 s100, s100, 7\n"
"s_lshl_b32 s100, s100, 7\n"
"s_lshl_b32 s60, s61, 7\n"
"s_add_u32 s60, s60, s43\n"
"s_mul_i32 s97, s42, 122880\n"
"s_add_u32 s97, s97, s60\n"
"s_lshr_b32 s98, s42, 1\n"
"buffer_load_dwordx4 v254, s[24:27], s40 offen offset:2048 lds\n"
"s_add_u32 s49, s49, 128\n"
"s_sub_u32 s41, s49, 1280\n"
"s_min_u32 s49, s49, s41\n"
"s_xor_b32 s51, s51, s74\n"
"s_add_u32 s98, s98, 12\n"
"s_mul_i32 s98, s98, 40960\n"
"s_add_u32 s98, s98, s60\n"
"s_mul_i32 s60, s93, 37\n"
"s_lshr_b32 s60, s60, 8\n"
"s_mul_i32 s79, s60, 7\n"
"s_sub_u32 s79, s93, s79\n"
"s_lshl_b32 s94, s60, 4\n"
"s_add_u32 s94, s94, s79\n"
"s_lshl_b32 s94, s94, 10\n"
"s_add_u32 s94, s94, 524288\n"
"s_mul_i32 s99, s60, 3\n"
"s_lshr_b32 s78, s79, 1\n"
"s_add_u32 s99, s99, s78\n"
"s_lshr_b32 s78, s60, 1\n"
"s_add_u32 s78, s78, 12\n"
"s_cmp_eq_u32 s79, 6\n"
"s_cselect_b32 s99, s78, s99\n"
"s_mul_i32 s99, s99, 40960\n"
"s_xor_b32 s79, s79, s60\n"
"s_and_b32 s79, s79, 1\n"
"s_lshl_b32 s79, s79, 6\n"
"s_add_u32 s99, s99, s79\n"
"s_add_u32 s99, s99, 1024\n"
"s_cmp_eq_u32 s61, 7\n"
"s_cselect_b32 s99, 0x40000000, s99\n"
"s_cselect_b32 s94, 0x40000000, s94\n"
"s_waitcnt vmcnt(27)\n"
"v_and_b32 v12, 0x7fff0000, v12\n"
"v_and_b32 v13, 0x7fff0000, v13\n"
"s_nop 0\n"
"v_max_u32_dpp v12, v12, v12 quad_perm:[1,0,3,2] row_mask:0xf bank_mask:0xf\n"
"v_max_u32_dpp v13, v13, v13 quad_perm:[1,0,3,2] row_mask:0xf bank_mask:0xf\n"
"s_nop 0\n"
"v_max_u32_dpp v12, v12, v12 quad_perm:[2,3,0,1] row_mask:0xf bank_mask:0xf\n"
"v_max_u32_dpp v13, v13, v13 quad_perm:[2,3,0,1] row_mask:0xf bank_mask:0xf\n"
"s_memtime s[80:81]\n"
"s_memrealtime s[82:83]\n"
"s_nop 0\n"
"v_max_u32_dpp v12, v12, v12 row_half_mirror row_mask:0xf bank_mask:0xf\n"
"v_max_u32_dpp v13, v13, v13 row_half_mirror row_mask:0xf bank_mask:0xf\n"
"s_nop 0\n"
"v_min_u32_dpp v12, v12, v12 row_mirror row_mask:0xf bank_mask:0xf\n"
"v_min_u32_dpp v13, v13, v13 row_mirror row_mask:0xf bank_mask:0xf\n"
"s_nop 0\n"
"v_min_u32_dpp v12, v12, v12 row_bcast:15 row_mask:0xa bank_mask:0xf\n"
"v_min_u32_dpp v13, v13, v13 row_bcast:15 row_mask:0xa bank_mask:0xf\n"
"s_nop 0\n"
"v_min_u32_dpp v12, v12, v12 row_bcast:31 row_mask:0xc bank_mask:0xf\n"
"v_min_u32_dpp v13, v13, v13 row_bcast:31 row_mask:0xc bank_mask:0xf\n"
"s_nop 0\n"
"v_mul_f32 v12, v12, v13\n"
"v_mul_f32 v12, 0x43c80000, v12\n"
"v_min_f32 v12, 0x71800000, v12\n"
"v_mul_f32 v13, s46, v12\n"
"s_nop 0\n"
"v_readlane_b32 s70, v12, 63\n"
"v_readlane_b32 s71, v12, 63\n"
"v_readlane_b32 s69, v13, 63\n"
"s_nop 1\n"
"v_mov_b64 v[228:229], s[70:71]\n"
"v_mov_b64 v[230:231], s[70:71]\n"
"s_waitcnt vmcnt(26)\n"
"v_readfirstlane_b32 s92, v1\n"
"v_mov_b64 v[0:1], s[70:71]\n"
"v_mov_b64 v[2:3], s[70:71]\n"
"s_mov_b32 s70, s46\n"
"s_xor_b32 s71, s69, s77\n"
"s_waitcnt lgkmcnt(0)\n"
"s_sub_u32 s80, s80, s64\n"
"s_sub_u32 s82, s82, s66\n"
"s_mul_i32 s80, s80, 10\n"
"s_mul_i32 s82, s82, 205\n"
"s_max_u32 s82, s82, 37000\n"
"s_cmp_gt_u32 s80, s82\n"
"s_mov_b32 s78, s64\n"
"s_cselect_b32 s64, 1, 0\n"
"s_cselect_b32 s68, 655360, 0\n"
"s_cmp_eq_u32 s42, 0\n"
"s_cselect_b32 s64, s64, 0\n"
"s_mov_b32 s65, 0\n"
"s_add_u32 s66, s66, 2800\n"
"s_mov_b32 s67, s66\n"
"s_mov_b32 s72, 114\n"
"s_mov_b64 vcc, s[64:65]\n"
"s_mov_b32 s65, s78\n"
"s_mov_b64 s[80:81], s[8:9]\n"
"s_mov_b32 s82, 0\n"
"s_mov_b32 s83, 0x20000\n"
"s_nop 2\n"
"s_mul_i32 s60, s92, 0x9e3779b1\n"
"s_add_u32 s60, s60, 0x7f4a7c15\n"
"s_waitcnt vmcnt(15)\n"
"s_barrier\n"
"ds_read_b128 a[136:139], v246 offset:0\n"
"ds_read_b128 a[140:143], v247 offset:0\n"
"ds_read_b64_tr_b16 a[192:193], v244 offset:0\n"
"ds_read_b64_tr_b16 a[194:195], v244 offset:512\n"
"ds_read_b64_tr_b16 a[196:197], v244 offset:8192\n"
"ds_read_b64_tr_b16 a[198:199], v244 offset:8704\n"
"ds_read_b64_tr_b16 a[200:201], v245 offset:0\n"
"ds_read_b64_tr_b16 a[202:203], v245 offset:512\n"
"ds_read_b64_tr_b16 a[204:205], v245 offset:8192\n"
"ds_read_b64_tr_b16 a[206:207], v245 offset:8704\n"
"ds_read_b64_tr_b16 a[208:209], v244 offset:1024\n"
"ds_read_b64_tr_b16 a[210:211], v244 offset:1536\n"
"ds_read_b64_tr_b16 a[212:213], v244 offset:9216\n"
"ds_read_b64_tr_b16 a[214:215], v244 offset:9728\n"
"s_waitcnt lgkmcnt(10)\n"
"v_mfma_f32_16x16x32_bf16 v[112:115], a[192:195], a[136:139], v[228:231]\n"
"ds_read_b64_tr_b16 a[216:217], v245 offset:1024\n"
"ds_read_b64_tr_b16 a[218:219], v245 offset:1536\n"
"s_waitcnt lgkmcnt(10)\n"
"v_mfma_f32_16x16x32_bf16 v[112:115], a[196:199], a[140:143], v[112:115]\n"
"ds_read_b64_tr_b16 a[220:221], v245 offset:9216\n"
"ds_read_b64_tr_b16 a[222:223], v245 offset:9728\n"
"s_waitcnt lgkmcnt(10)\n"
"v_mfma_f32_16x16x32_bf16 v[116:119], a[200:203], a[136:139], v[228:231]\n"
"s_waitcnt lgkmcnt(8)\n"
"ds_read_b128 a[144:147], v246 offset:2048\n"
"v_mfma_f32_16x16x32_bf16 v[116:119], a[204:207], a[140:143], v[116:119]\n"
"s_waitcnt lgkmcnt(7)\n"
"ds_read_b128 a[148:151], v247 offset:2048\n"
"v_mfma_f32_16x16x32_bf16 v[120:123], a[208:211], a[136:139], v[228:231]\n"
"s_waitcnt lgkmcnt(6)\n"
"ds_read_b128 a[152:155], v246 offset:4096\n"
"v_mfma_f32_16x16x32_bf16 v[120:123], a[212:215], a[140:143], v[120:123]\n"
"s_waitcnt lgkmcnt(5)\n"
"ds_read_b128 a[156:159], v247 offset:4096\n"
"v_mfma_f32_16x16x32_bf16 v[124:127], a[216:219], a[136:139], v[228:231]\n"
"s_waitcnt lgkmcnt(4)\n"
"ds_read_b128 a[160:163], v252 offset:0\n"
"v_mfma_f32_16x16x32_bf16 v[124:127], a[220:223], a[140:143], v[124:127]\n"
"s_waitcnt lgkmcnt(4)\n"
"ds_read_b128 a[164:167], v253 offset:0\n"
"v_mfma_f32_16x16x32_bf16 v[140:143], a[216:219], a[144:147], v[228:231]\n"
"s_waitcnt lgkmcnt(4)\n"
"ds_read_b128 a[168:171], v250 offset:0\n"
"v_mfma_f32_16x16x32_bf16 v[140:143], a[220:223], a[148:151], v[140:143]\n"
"ds_read_b128 a[172:175], v251 offset:0\n"
"v_mfma_f32_16x16x32_bf16 v[136:139], a[208:211], a[144:147], v[228:231]\n"
"s_waitcnt vmcnt(11)\n"
"ds_read_b128 a[176:179], v250 offset:2048\n"
"v_mfma_f32_16x16x32_bf16 v[136:139], a[212:215], a[148:151], v[136:139]\n"
"s_barrier\n"
"v_mfma_f32_16x16x32_bf16 v[132:135], a[200:203], a[144:147], v[228:231]\n"
"s_add_u32 s40, s48, 256\n"
"s_xor_b32 m0, s50, 0xc000\n"
"ds_read_b128 a[180:183], v251 offset:2048\n"
"v_mfma_f32_16x16x32_bf16 v[132:135], a[204:207], a[148:151], v[132:135]\n"
"buffer_load_dwordx4 v224, s[28:31], s40 offen lds\n"
"v_mfma_f32_16x16x32_bf16 v[128:131], a[192:195], a[144:147], v[228:231]\n"
"ds_read_b128 a[184:187], v250 offset:4096\n"
"v_mfma_f32_16x16x32_bf16 v[128:131], a[196:199], a[148:151], v[128:131]\n"
"s_waitcnt lgkmcnt(5)\n"
"ds_read_b128 a[188:191], v251 offset:4096\n"
"v_mfma_f32_16x16x32_bf16 v[144:147], a[192:195], a[152:155], v[228:231]\n"
"ds_read_b64_tr_b16 a[224:225], v244 offset:16384\n"
"v_mfma_f32_16x16x32_bf16 v[144:147], a[196:199], a[156:159], v[144:147]\n"
"ds_read_b64_tr_b16 a[226:227], v244 offset:16896\n"
"v_mfma_f32_16x16x32_bf16 v[148:151], a[200:203], a[152:155], v[228:231]\n"
"ds_read_b64_tr_b16 a[228:229], v244 offset:24576\n"
"v_mfma_f32_16x16x32_bf16 v[148:151], a[204:207], a[156:159], v[148:151]\n"
"buffer_load_dwordx4 v225, s[28:31], s40 offen offset:1024 lds\n"
"v_mfma_f32_16x16x32_bf16 v[152:155], a[208:211], a[152:155], v[228:231]\n"
"ds_read_b64_tr_b16 a[230:231], v244 offset:25088\n"
"v_mfma_f32_16x16x32_bf16 v[152:155], a[212:215], a[156:159], v[152:155]\n"
"ds_read_b64_tr_b16 a[232:233], v245 offset:16384\n"
"v_mfma_f32_16x16x32_bf16 v[156:159], a[216:219], a[152:155], v[228:231]\n"
"ds_read_b64_tr_b16 a[234:235], v245 offset:16896\n"
"v_mfma_f32_16x16x32_bf16 v[156:159], a[220:223], a[156:159], v[156:159]\n"
"ds_read_b64_tr_b16 a[236:237], v245 offset:24576\n"
"v_mfma_f32_16x16x32_bf16 v[172:175], a[216:219], a[160:163], v[228:231]\n"
"ds_read_b64_tr_b16 a[238:239], v245 offset:25088\n"
"v_mfma_f32_16x16x32_bf16 v[172:175], a[220:223], a[164:167], v[172:175]\n"
"buffer_load_dwordx4 v226, s[28:31], s40 offen offset:2048 lds\n"
"v_mfma_f32_16x16x32_bf16 v[168:171], a[208:211], a[160:163], v[228:231]\n"
"ds_read_b64_tr_b16 a[240:241], v244 offset:17408\n"
"v_mfma_f32_16x16x32_bf16 v[168:171], a[212:215], a[164:167], v[168:171]\n"
"ds_read_b64_tr_b16 a[242:243], v244 offset:17920\n"
"v_mfma_f32_16x16x32_bf16 v[164:167], a[200:203], a[160:163], v[228:231]\n"
"ds_read_b64_tr_b16 a[244:245], v244 offset:25600\n"
"v_mfma_f32_16x16x32_bf16 v[164:167], a[204:207], a[164:167], v[164:167]\n"
"buffer_load_dwordx4 v227, s[28:31], s40 offen offset:3072 lds\n"
"v_mfma_f32_16x16x32_bf16 v[160:163], a[192:195], a[160:163], v[228:231]\n"
"s_cmp_eq_u32 s47, 8\n"
"ds_read_b64_tr_b16 a[246:247], v244 offset:26112\n"
"v_xor_b32 v244, s74, v244\n"
"v_mfma_f32_16x16x32_bf16 v[160:163], a[196:199], a[164:167], v[160:163]\n"
"s_waitcnt lgkmcnt(5)\n"
"s_cselect_b32 s41, s52, s58\n"
"ds_read_b64_tr_b16 a[248:249], v245 offset:17408\n"
"v_xor_b32 v246, s74, v246\n"
"v_mfma_f32_16x16x32_bf16 v[176:179], a[192:195], a[168:171], v[228:231]\n"
"s_add_u32 s48, s48, s41\n"
"ds_read_b64_tr_b16 a[250:251], v245 offset:17920\n"
"v_xor_b32 v247, s74, v247\n"
"v_mfma_f32_16x16x32_bf16 v[176:179], a[196:199], a[172:175], v[176:179]\n"
"s_sub_u32 s41, s48, 5242880\n"
"ds_read_b64_tr_b16 a[252:253], v245 offset:25600\n"
"v_xor_b32 v252, s74, v252\n"
"v_mfma_f32_16x16x32_bf16 v[180:183], a[200:203], a[168:171], v[228:231]\n"
"s_min_u32 s48, s48, s41\n"
"ds_read_b64_tr_b16 a[254:255], v245 offset:26112\n"
"v_xor_b32 v245, s74, v245\n"
"v_mfma_f32_16x16x32_bf16 v[180:183], a[204:207], a[172:175], v[180:183]\n"
"v_xor_b32 v253, s74, v253\n"
"v_xor_b32 v250, s74, v250\n"
"v_mfma_f32_16x16x32_bf16 v[184:187], a[208:211], a[168:171], v[228:231]\n"
"s_mov_b32 m0, s50\n"
"v_xor_b32 v251, s74, v251\n"
"v_mfma_f32_16x16x32_bf16 v[184:187], a[212:215], a[172:175], v[184:187]\n"
"buffer_load_dwordx4 v224, s[28:31], s48 offen lds\n"
"v_mfma_f32_16x16x32_bf16 v[188:191], a[216:219], a[168:171], v[228:231]\n"
"v_mfma_f32_16x16x32_bf16 v[188:191], a[220:223], a[172:175], v[188:191]\n"
"v_mfma_f32_16x16x32_bf16 v[204:207], a[216:219], a[176:179], v[228:231]\n"
"v_mfma_f32_16x16x32_bf16 v[204:207], a[220:223], a[180:183], v[204:207]\n"
"buffer_load_dwordx4 v225, s[28:31], s48 offen offset:1024 lds\n"
"v_mfma_f32_16x16x32_bf16 v[200:203], a[208:211], a[176:179], v[228:231]\n"
"v_mfma_f32_16x16x32_bf16 v[200:203], a[212:215], a[180:183], v[200:203]\n"
"v_mfma_f32_16x16x32_bf16 v[196:199], a[200:203], a[176:179], v[228:231]\n"
"v_mfma_f32_16x16x32_bf16 v[196:199], a[204:207], a[180:183], v[196:199]\n"
"v_mfma_f32_16x16x32_bf16 v[192:195], a[192:195], a[176:179], v[228:231]\n"
"v_mfma_f32_16x16x32_bf16 v[192:195], a[196:199], a[180:183], v[192:195]\n"
"buffer_load_dwordx4 v226, s[28:31], s48 offen offset:2048 lds\n"
"v_mfma_f32_16x16x32_bf16 v[208:211], a[192:195], a[184:187], v[228:231]\n"
"v_mfma_f32_16x16x32_bf16 v[208:211], a[196:199], a[188:191], v[208:211]\n"
"v_mfma_f32_16x16x32_bf16 v[212:215], a[200:203], a[184:187], v[228:231]\n"
"v_mfma_f32_16x16x32_bf16 v[212:215], a[204:207], a[188:191], v[212:215]\n"
"v_mfma_f32_16x16x32_bf16 v[216:219], a[208:211], a[184:187], v[228:231]\n"
"v_mfma_f32_16x16x32_bf16 v[216:219], a[212:215], a[188:191], v[216:219]\n"
"buffer_load_dwordx4 v227, s[28:31], s48 offen offset:3072 lds\n"
"v_mfma_f32_16x16x32_bf16 v[220:223], a[216:219], a[184:187], v[228:231]\n"
"s_xor_b32 s50, s50, s74\n"
"v_mfma_f32_16x16x32_bf16 v[220:223], a[220:223], a[188:191], v[220:223]\n"
"v_mfma_f32_16x16x32_bf16 v[0:3], a[224:227], a[136:139], v[228:231]\n"
"v_mfma_f32_16x16x32_bf16 v[0:3], a[228:231], a[140:143], v[0:3]\n"
"v_mfma_f32_16x16x32_bf16 v[4:7], a[232:235], a[136:139], v[228:231]\n"
"s_waitcnt vmcnt(8) lgkmcnt(0)\n"
"v_mfma_f32_16x16x32_bf16 v[4:7], a[236:239], a[140:143], v[4:7]\n"
"s_barrier\n"
"v_mfma_f32_16x16x32_bf16 v[8:11], a[240:243], a[136:139], v[228:231]\n"
"s_mov_b32 m0, s51\n"
"ds_read_b64_tr_b16 a[192:193], v244 offset:0\n"
"v_mfma_f32_16x16x32_bf16 v[8:11], a[244:247], a[140:143], v[8:11]\n"
"buffer_load_dwordx4 v248, s[24:27], s49 offen lds\n"
"v_mfma_f32_16x16x32_bf16 v[12:15], a[248:251], a[136:139], v[228:231]\n"
"ds_read_b64_tr_b16 a[194:195], v244 offset:512\n"
"v_mfma_f32_16x16x32_bf16 v[12:15], a[252:255], a[140:143], v[12:15]\n"
"ds_read_b128 a[136:139], v246 offset:0\n"
"v_mfma_f32_16x16x32_bf16 v[28:31], a[248:251], a[144:147], v[228:231]\n"
"ds_read_b64_tr_b16 a[196:197], v244 offset:8192\n"
"v_mfma_f32_16x16x32_bf16 v[28:31], a[252:255], a[148:151], v[28:31]\n"
"ds_read_b64_tr_b16 a[198:199], v244 offset:8704\n"
"v_mfma_f32_16x16x32_bf16 v[24:27], a[240:243], a[144:147], v[228:231]\n"
"ds_read_b128 a[140:143], v247 offset:0\n"
"v_mfma_f32_16x16x32_bf16 v[24:27], a[244:247], a[148:151], v[24:27]\n"
"buffer_load_dwordx4 v249, s[24:27], s49 offen offset:1024 lds\n"
"v_mfma_f32_16x16x32_bf16 v[20:23], a[232:235], a[144:147], v[228:231]\n"
"ds_read_b64_tr_b16 a[200:201], v245 offset:0\n"
"v_mfma_f32_16x16x32_bf16 v[20:23], a[236:239], a[148:151], v[20:23]\n"
"ds_read_b64_tr_b16 a[202:203], v245 offset:512\n"
"v_mfma_f32_16x16x32_bf16 v[16:19], a[224:227], a[144:147], v[228:231]\n"
"ds_read_b64_tr_b16 a[204:205], v245 offset:8192\n"
"v_mfma_f32_16x16x32_bf16 v[16:19], a[228:231], a[148:151], v[16:19]\n"
"ds_read_b64_tr_b16 a[206:207], v245 offset:8704\n"
"v_mfma_f32_16x16x32_bf16 v[32:35], a[224:227], a[152:155], v[228:231]\n"
"ds_read_b64_tr_b16 a[208:209], v244 offset:1024\n"
"v_mfma_f32_16x16x32_bf16 v[32:35], a[228:231], a[156:159], v[32:35]\n"
"buffer_load_dwordx4 v254, s[24:27], s49 offen offset:2048 lds\n"
"v_mfma_f32_16x16x32_bf16 v[36:39], a[232:235], a[152:155], v[228:231]\n"
"ds_read_b64_tr_b16 a[210:211], v244 offset:1536\n"
"v_mfma_f32_16x16x32_bf16 v[36:39], a[236:239], a[156:159], v[36:39]\n"
"ds_read_b64_tr_b16 a[212:213], v244 offset:9216\n"
"v_mfma_f32_16x16x32_bf16 v[40:43], a[240:243], a[152:155], v[228:231]\n"
"ds_read_b64_tr_b16 a[214:215], v244 offset:9728\n"
"v_mfma_f32_16x16x32_bf16 v[40:43], a[244:247], a[156:159], v[40:43]\n"
"ds_read_b64_tr_b16 a[216:217], v245 offset:1024\n"
"v_mfma_f32_16x16x32_bf16 v[44:47], a[248:251], a[152:155], v[228:231]\n"
"ds_read_b64_tr_b16 a[218:219], v245 offset:1536\n"
"v_mfma_f32_16x16x32_bf16 v[44:47], a[252:255], a[156:159], v[44:47]\n"
"buffer_load_dwordx4 v255, s[24:27], s49 offen offset:3072 lds\n"
"v_mfma_f32_16x16x32_bf16 v[60:63], a[248:251], a[160:163], v[228:231]\n"
"ds_read_b64_tr_b16 a[220:221], v245 offset:9216\n"
"v_mfma_f32_16x16x32_bf16 v[60:63], a[252:255], a[164:167], v[60:63]\n"
"ds_read_b64_tr_b16 a[222:223], v245 offset:9728\n"
"v_mfma_f32_16x16x32_bf16 v[56:59], a[240:243], a[160:163], v[228:231]\n"
"ds_read_b128 a[144:147], v246 offset:2048\n"
"v_mfma_f32_16x16x32_bf16 v[56:59], a[244:247], a[164:167], v[56:59]\n"
"s_add_u32 s40, s49, 40960\n"
"ds_read_b128 a[148:151], v247 offset:2048\n"
"v_mfma_f32_16x16x32_bf16 v[52:55], a[232:235], a[160:163], v[228:231]\n"
"s_add_u32 m0, s51, 4096\n"
"ds_read_b128 a[152:155], v246 offset:4096\n"
"v_mfma_f32_16x16x32_bf16 v[52:55], a[236:239], a[164:167], v[52:55]\n"
"buffer_load_dwordx4 v248, s[24:27], s40 offen lds\n"
"v_mfma_f32_16x16x32_bf16 v[48:51], a[224:227], a[160:163], v[228:231]\n"
"ds_read_b128 a[156:159], v247 offset:4096\n"
"v_mfma_f32_16x16x32_bf16 v[48:51], a[228:231], a[164:167], v[48:51]\n"
"ds_read_b128 a[160:163], v252 offset:0\n"
"v_mfma_f32_16x16x32_bf16 v[64:67], a[224:227], a[168:171], v[228:231]\n"
"ds_read_b128 a[164:167], v253 offset:0\n"
"v_mfma_f32_16x16x32_bf16 v[64:67], a[228:231], a[172:175], v[64:67]\n"
"v_mfma_f32_16x16x32_bf16 v[68:71], a[232:235], a[168:171], v[228:231]\n"
"v_mfma_f32_16x16x32_bf16 v[68:71], a[236:239], a[172:175], v[68:71]\n"
"buffer_load_dwordx4 v249, s[24:27], s40 offen offset:1024 lds\n"
"v_mfma_f32_16x16x32_bf16 v[72:75], a[240:243], a[168:171], v[228:231]\n"
"v_mfma_f32_16x16x32_bf16 v[72:75], a[244:247], a[172:175], v[72:75]\n"
"v_mfma_f32_16x16x32_bf16 v[76:79], a[248:251], a[168:171], v[228:231]\n"
"ds_read_b128 a[168:171], v250 offset:0\n"
"v_mfma_f32_16x16x32_bf16 v[76:79], a[252:255], a[172:175], v[76:79]\n"
"ds_read_b128 a[172:175], v251 offset:0\n"
"v_mfma_f32_16x16x32_bf16 v[92:95], a[248:251], a[176:179], v[228:231]\n"
"v_mfma_f32_16x16x32_bf16 v[92:95], a[252:255], a[180:183], v[92:95]\n"
"buffer_load_dwordx4 v254, s[24:27], s40 offen offset:2048 lds\n"
"v_mfma_f32_16x16x32_bf16 v[88:91], a[240:243], a[176:179], v[228:231]\n"
"s_add_u32 s49, s49, 128\n"
"v_mfma_f32_16x16x32_bf16 v[88:91], a[244:247], a[180:183], v[88:91]\n"
"s_sub_u32 s41, s49, 1280\n"
"v_mfma_f32_16x16x32_bf16 v[84:87], a[232:235], a[176:179], v[228:231]\n"
"s_min_u32 s49, s49, s41\n"
"v_mfma_f32_16x16x32_bf16 v[84:87], a[236:239], a[180:183], v[84:87]\n"
"s_xor_b32 s51, s51, s74\n"
"v_mfma_f32_16x16x32_bf16 v[80:83], a[224:227], a[176:179], v[228:231]\n"
"ds_read_b128 a[176:179], v250 offset:2048\n"
"v_mfma_f32_16x16x32_bf16 v[80:83], a[228:231], a[180:183], v[80:83]\n"
"ds_read_b128 a[180:183], v251 offset:2048\n"
"v_mfma_f32_16x16x32_bf16 v[96:99], a[224:227], a[184:187], v[228:231]\n"
"v_mfma_f32_16x16x32_bf16 v[96:99], a[228:231], a[188:191], v[96:99]\n"
"v_mfma_f32_16x16x32_bf16 v[100:103], a[232:235], a[184:187], v[228:231]\n"
"v_mfma_f32_16x16x32_bf16 v[100:103], a[236:239], a[188:191], v[100:103]\n"
"v_mfma_f32_16x16x32_bf16 v[104:107], a[240:243], a[184:187], v[228:231]\n"
"v_mfma_f32_16x16x32_bf16 v[104:107], a[244:247], a[188:191], v[104:107]\n"
"v_mfma_f32_16x16x32_bf16 v[108:111], a[248:251], a[184:187], v[228:231]\n"
"s_add_u32 s47, s47, 1\n"
"ds_read_b128 a[184:187], v250 offset:4096\n"
"v_mfma_f32_16x16x32_bf16 v[108:111], a[252:255], a[188:191], v[108:111]\n"
"s_waitcnt lgkmcnt(1)\n"
"ds_read_b128 a[188:191], v251 offset:4096\n"
"LPE_%=:\n"
"s_cmp_lg_u32 s68, 0\n"
"s_cbranch_scc1 LPN_%=\n"
"LPL_%=:\n"
"v_mfma_f32_16x16x32_bf16 v[112:115], a[192:195], a[136:139], v[112:115]\n"
"v_mfma_f32_16x16x32_bf16 v[112:115], a[196:199], a[140:143], v[112:115]\n"
"v_mfma_f32_16x16x32_bf16 v[116:119], a[200:203], a[136:139], v[116:119]\n"
"s_waitcnt vmcnt(11)\n"
"v_mfma_f32_16x16x32_bf16 v[116:119], a[204:207], a[140:143], v[116:119]\n"
"s_barrier\n"
"v_mfma_f32_16x16x32_bf16 v[120:123], a[208:211], a[136:139], v[120:123]\n"
"s_add_u32 s40, s48, 256\n"
"s_xor_b32 m0, s50, 0xc000\n"
"ds_read_b64_tr_b16 a[224:225], v244 offset:16384\n"
"v_mfma_f32_16x16x32_bf16 v[120:123], a[212:215], a[140:143], v[120:123]\n"
"buffer_load_dwordx4 v224, s[28:31], s40 offen lds\n"
"v_mfma_f32_16x16x32_bf16 v[124:127], a[216:219], a[136:139], v[124:127]\n"
"ds_read_b64_tr_b16 a[226:227], v244 offset:16896\n"
"v_mfma_f32_16x16x32_bf16 v[124:127], a[220:223], a[140:143], v[124:127]\n"
"ds_read_b64_tr_b16 a[228:229], v244 offset:24576\n"
"v_mfma_f32_16x16x32_bf16 v[140:143], a[216:219], a[144:147], v[140:143]\n"
"ds_read_b64_tr_b16 a[230:231], v244 offset:25088\n"
"v_mfma_f32_16x16x32_bf16 v[140:143], a[220:223], a[148:151], v[140:143]\n"
"ds_read_b64_tr_b16 a[232:233], v245 offset:16384\n"
"v_mfma_f32_16x16x32_bf16 v[136:139], a[208:211], a[144:147], v[136:139]\n"
"ds_read_b64_tr_b16 a[234:235], v245 offset:16896\n"
"v_mfma_f32_16x16x32_bf16 v[136:139], a[212:215], a[148:151], v[136:139]\n"
"buffer_load_dwordx4 v225, s[28:31], s40 offen offset:1024 lds\n"
"v_mfma_f32_16x16x32_bf16 v[132:135], a[200:203], a[144:147], v[132:135]\n"
"ds_read_b64_tr_b16 a[236:237], v245 offset:24576\n"
"v_mfma_f32_16x16x32_bf16 v[132:135], a[204:207], a[148:151], v[132:135]\n"
"ds_read_b64_tr_b16 a[238:239], v245 offset:25088\n"
"v_mfma_f32_16x16x32_bf16 v[128:131], a[192:195], a[144:147], v[128:131]\n"
"ds_read_b64_tr_b16 a[240:241], v244 offset:17408\n"
"v_mfma_f32_16x16x32_bf16 v[128:131], a[196:199], a[148:151], v[128:131]\n"
"ds_read_b64_tr_b16 a[242:243], v244 offset:17920\n"
"v_mfma_f32_16x16x32_bf16 v[144:147], a[192:195], a[152:155], v[144:147]\n"
"ds_read_b64_tr_b16 a[244:245], v244 offset:25600\n"
"v_mfma_f32_16x16x32_bf16 v[144:147], a[196:199], a[156:159], v[144:147]\n"
"buffer_load_dwordx4 v226, s[28:31], s40 offen offset:2048 lds\n"
"v_mfma_f32_16x16x32_bf16 v[148:151], a[200:203], a[152:155], v[148:151]\n"
"ds_read_b64_tr_b16 a[246:247], v244 offset:26112\n"
"v_mfma_f32_16x16x32_bf16 v[148:151], a[204:207], a[156:159], v[148:151]\n"
"ds_read_b64_tr_b16 a[248:249], v245 offset:17408\n"
"v_mfma_f32_16x16x32_bf16 v[152:155], a[208:211], a[152:155], v[152:155]\n"
"ds_read_b64_tr_b16 a[250:251], v245 offset:17920\n"
"v_mfma_f32_16x16x32_bf16 v[152:155], a[212:215], a[156:159], v[152:155]\n"
"ds_read_b64_tr_b16 a[252:253], v245 offset:25600\n"
"v_mfma_f32_16x16x32_bf16 v[156:159], a[216:219], a[152:155], v[156:159]\n"
"ds_read_b64_tr_b16 a[254:255], v245 offset:26112\n"
"v_mfma_f32_16x16x32_bf16 v[156:159], a[220:223], a[156:159], v[156:159]\n"
"buffer_load_dwordx4 v227, s[28:31], s40 offen offset:3072 lds\n"
"v_mfma_f32_16x16x32_bf16 v[172:175], a[216:219], a[160:163], v[172:175]\n"
"s_cmp_eq_u32 s47, 8\n"
"v_mfma_f32_16x16x32_bf16 v[172:175], a[220:223], a[164:167], v[172:175]\n"
"s_cselect_b32 s41, s52, s58\n"
"v_mfma_f32_16x16x32_bf16 v[168:171], a[208:211], a[160:163], v[168:171]\n"
"s_add_u32 s48, s48, s41\n"
"v_mfma_f32_16x16x32_bf16 v[168:171], a[212:215], a[164:167], v[168:171]\n"
"s_sub_u32 s41, s48, 5242880\n"
"v_mfma_f32_16x16x32_bf16 v[164:167], a[200:203], a[160:163], v[164:167]\n"
"s_min_u32 s48, s48, s41\n"
"v_mfma_f32_16x16x32_bf16 v[164:167], a[204:207], a[164:167], v[164:167]\n"
"v_xor_b32 v244, s74, v244\n"
"v_xor_b32 v246, s74, v246\n"
"v_mfma_f32_16x16x32_bf16 v[160:163], a[192:195], a[160:163], v[160:163]\n"
"s_mov_b32 m0, s50\n"
"v_xor_b32 v245, s74, v245\n"
"v_xor_b32 v247, s74, v247\n"
"v_mfma_f32_16x16x32_bf16 v[160:163], a[196:199], a[164:167], v[160:163]\n"
"buffer_load_dwordx4 v224, s[28:31], s48 offen lds\n"
"v_mfma_f32_16x16x32_bf16 v[176:179], a[192:195], a[168:171], v[176:179]\n"
"v_xor_b32 v252, s74, v252\n"
"v_xor_b32 v253, s74, v253\n"
"v_mfma_f32_16x16x32_bf16 v[176:179], a[196:199], a[172:175], v[176:179]\n"
"v_xor_b32 v250, s74, v250\n"
"v_xor_b32 v251, s74, v251\n"
"v_mfma_f32_16x16x32_bf16 v[180:183], a[200:203], a[168:171], v[180:183]\n"
"v_mfma_f32_16x16x32_bf16 v[180:183], a[204:207], a[172:175], v[180:183]\n"
"buffer_load_dwordx4 v225, s[28:31], s48 offen offset:1024 lds\n"
"v_mfma_f32_16x16x32_bf16 v[184:187], a[208:211], a[168:171], v[184:187]\n"
"v_mfma_f32_16x16x32_bf16 v[184:187], a[212:215], a[172:175], v[184:187]\n"
"v_mfma_f32_16x16x32_bf16 v[188:191], a[216:219], a[168:171], v[188:191]\n"
"v_mfma_f32_16x16x32_bf16 v[188:191], a[220:223], a[172:175], v[188:191]\n"
"v_mfma_f32_16x16x32_bf16 v[204:207], a[216:219], a[176:179], v[204:207]\n"
"v_mfma_f32_16x16x32_bf16 v[204:207], a[220:223], a[180:183], v[204:207]\n"
"buffer_load_dwordx4 v226, s[28:31], s48 offen offset:2048 lds\n"
"v_mfma_f32_16x16x32_bf16 v[200:203], a[208:211], a[176:179], v[200:203]\n"
"v_mfma_f32_16x16x32_bf16 v[200:203], a[212:215], a[180:183], v[200:203]\n"
"v_mfma_f32_16x16x32_bf16 v[196:199], a[200:203], a[176:179], v[196:199]\n"
"v_mfma_f32_16x16x32_bf16 v[196:199], a[204:207], a[180:183], v[196:199]\n"
"v_mfma_f32_16x16x32_bf16 v[192:195], a[192:195], a[176:179], v[192:195]\n"
"v_mfma_f32_16x16x32_bf16 v[192:195], a[196:199], a[180:183], v[192:195]\n"
"s_waitcnt lgkmcnt(0)\n"
"buffer_load_dwordx4 v227, s[28:31], s48 offen offset:3072 lds\n"
"v_mfma_f32_16x16x32_bf16 v[208:211], a[192:195], a[184:187], v[208:211]\n"
"s_xor_b32 s50, s50, s74\n"
"v_mfma_f32_16x16x32_bf16 v[208:211], a[196:199], a[188:191], v[208:211]\n"
"v_mfma_f32_16x16x32_bf16 v[212:215], a[200:203], a[184:187], v[212:215]\n"
"v_mfma_f32_16x16x32_bf16 v[212:215], a[204:207], a[188:191], v[212:215]\n"
"v_mfma_f32_16x16x32_bf16 v[216:219], a[208:211], a[184:187], v[216:219]\n"
"v_mfma_f32_16x16x32_bf16 v[216:219], a[212:215], a[188:191], v[216:219]\n"
"v_mfma_f32_16x16x32_bf16 v[220:223], a[216:219], a[184:187], v[220:223]\n"
"v_mfma_f32_16x16x32_bf16 v[220:223], a[220:223], a[188:191], v[220:223]\n"
"v_mfma_f32_16x16x32_bf16 v[0:3], a[224:227], a[136:139], v[0:3]\n"
"v_mfma_f32_16x16x32_bf16 v[0:3], a[228:231], a[140:143], v[0:3]\n"
"v_mfma_f32_16x16x32_bf16 v[4:7], a[232:235], a[136:139], v[4:7]\n"
"s_waitcnt vmcnt(8)\n"
"v_mfma_f32_16x16x32_bf16 v[4:7], a[236:239], a[140:143], v[4:7]\n"
"s_barrier\n"
"v_mfma_f32_16x16x32_bf16 v[8:11], a[240:243], a[136:139], v[8:11]\n"
"s_mov_b32 m0, s51\n"
"ds_read_b64_tr_b16 a[192:193], v244 offset:0\n"
"v_mfma_f32_16x16x32_bf16 v[8:11], a[244:247], a[140:143], v[8:11]\n"
"buffer_load_dwordx4 v248, s[24:27], s49 offen lds\n"
"v_mfma_f32_16x16x32_bf16 v[12:15], a[248:251], a[136:139], v[12:15]\n"
"ds_read_b64_tr_b16 a[194:195], v244 offset:512\n"
"v_mfma_f32_16x16x32_bf16 v[12:15], a[252:255], a[140:143], v[12:15]\n"
"ds_read_b128 a[136:139], v246 offset:0\n"
"v_mfma_f32_16x16x32_bf16 v[28:31], a[248:251], a[144:147], v[28:31]\n"
"ds_read_b64_tr_b16 a[196:197], v244 offset:8192\n"
"v_mfma_f32_16x16x32_bf16 v[28:31], a[252:255], a[148:151], v[28:31]\n"
"ds_read_b64_tr_b16 a[198:199], v244 offset:8704\n"
"v_mfma_f32_16x16x32_bf16 v[24:27], a[240:243], a[144:147], v[24:27]\n"
"ds_read_b128 a[140:143], v247 offset:0\n"
"v_mfma_f32_16x16x32_bf16 v[24:27], a[244:247], a[148:151], v[24:27]\n"
"buffer_load_dwordx4 v249, s[24:27], s49 offen offset:1024 lds\n"
"v_mfma_f32_16x16x32_bf16 v[20:23], a[232:235], a[144:147], v[20:23]\n"
"ds_read_b64_tr_b16 a[200:201], v245 offset:0\n"
"v_mfma_f32_16x16x32_bf16 v[20:23], a[236:239], a[148:151], v[20:23]\n"
"ds_read_b64_tr_b16 a[202:203], v245 offset:512\n"
"v_mfma_f32_16x16x32_bf16 v[16:19], a[224:227], a[144:147], v[16:19]\n"
"ds_read_b64_tr_b16 a[204:205], v245 offset:8192\n"
"v_mfma_f32_16x16x32_bf16 v[16:19], a[228:231], a[148:151], v[16:19]\n"
"ds_read_b64_tr_b16 a[206:207], v245 offset:8704\n"
"v_mfma_f32_16x16x32_bf16 v[32:35], a[224:227], a[152:155], v[32:35]\n"
"ds_read_b64_tr_b16 a[208:209], v244 offset:1024\n"
"v_mfma_f32_16x16x32_bf16 v[32:35], a[228:231], a[156:159], v[32:35]\n"
"buffer_load_dwordx4 v254, s[24:27], s49 offen offset:2048 lds\n"
"v_mfma_f32_16x16x32_bf16 v[36:39], a[232:235], a[152:155], v[36:39]\n"
"ds_read_b64_tr_b16 a[210:211], v244 offset:1536\n"
"v_mfma_f32_16x16x32_bf16 v[36:39], a[236:239], a[156:159], v[36:39]\n"
"ds_read_b64_tr_b16 a[212:213], v244 offset:9216\n"
"v_mfma_f32_16x16x32_bf16 v[40:43], a[240:243], a[152:155], v[40:43]\n"
"ds_read_b64_tr_b16 a[214:215], v244 offset:9728\n"
"v_mfma_f32_16x16x32_bf16 v[40:43], a[244:247], a[156:159], v[40:43]\n"
"ds_read_b64_tr_b16 a[216:217], v245 offset:1024\n"
"v_mfma_f32_16x16x32_bf16 v[44:47], a[248:251], a[152:155], v[44:47]\n"
"ds_read_b64_tr_b16 a[218:219], v245 offset:1536\n"
"v_mfma_f32_16x16x32_bf16 v[44:47], a[252:255], a[156:159], v[44:47]\n"
"buffer_load_dwordx4 v255, s[24:27], s49 offen offset:3072 lds\n"
"v_mfma_f32_16x16x32_bf16 v[60:63], a[248:251], a[160:163], v[60:63]\n"
"ds_read_b64_tr_b16 a[220:221], v245 offset:9216\n"
"v_mfma_f32_16x16x32_bf16 v[60:63], a[252:255], a[164:167], v[60:63]\n"
"ds_read_b64_tr_b16 a[222:223], v245 offset:9728\n"
"v_mfma_f32_16x16x32_bf16 v[56:59], a[240:243], a[160:163], v[56:59]\n"
"ds_read_b128 a[144:147], v246 offset:2048\n"
"v_mfma_f32_16x16x32_bf16 v[56:59], a[244:247], a[164:167], v[56:59]\n"
"s_add_u32 s40, s49, 40960\n"
"ds_read_b128 a[148:151], v247 offset:2048\n"
"v_mfma_f32_16x16x32_bf16 v[52:55], a[232:235], a[160:163], v[52:55]\n"
"s_add_u32 m0, s51, 4096\n"
"ds_read_b128 a[152:155], v246 offset:4096\n"
"v_mfma_f32_16x16x32_bf16 v[52:55], a[236:239], a[164:167], v[52:55]\n"
"buffer_load_dwordx4 v248, s[24:27], s40 offen lds\n"
"v_mfma_f32_16x16x32_bf16 v[48:51], a[224:227], a[160:163], v[48:51]\n"
"ds_read_b128 a[156:159], v247 offset:4096\n"
"v_mfma_f32_16x16x32_bf16 v[48:51], a[228:231], a[164:167], v[48:51]\n"
"ds_read_b128 a[160:163], v252 offset:0\n"
"v_mfma_f32_16x16x32_bf16 v[64:67], a[224:227], a[168:171], v[64:67]\n"
"ds_read_b128 a[164:167], v253 offset:0\n"
"v_mfma_f32_16x16x32_bf16 v[64:67], a[228:231], a[172:175], v[64:67]\n"
"v_mfma_f32_16x16x32_bf16 v[68:71], a[232:235], a[168:171], v[68:71]\n"
"v_mfma_f32_16x16x32_bf16 v[68:71], a[236:239], a[172:175], v[68:71]\n"
"buffer_load_dwordx4 v249, s[24:27], s40 offen offset:1024 lds\n"
"v_mfma_f32_16x16x32_bf16 v[72:75], a[240:243], a[168:171], v[72:75]\n"
"v_mfma_f32_16x16x32_bf16 v[72:75], a[244:247], a[172:175], v[72:75]\n"
"v_mfma_f32_16x16x32_bf16 v[76:79], a[248:251], a[168:171], v[76:79]\n"
"ds_read_b128 a[168:171], v250 offset:0\n"
"v_mfma_f32_16x16x32_bf16 v[76:79], a[252:255], a[172:175], v[76:79]\n"
"ds_read_b128 a[172:175], v251 offset:0\n"
"v_mfma_f32_16x16x32_bf16 v[92:95], a[248:251], a[176:179], v[92:95]\n"
"v_mfma_f32_16x16x32_bf16 v[92:95], a[252:255], a[180:183], v[92:95]\n"
"buffer_load_dwordx4 v254, s[24:27], s40 offen offset:2048 lds\n"
"v_mfma_f32_16x16x32_bf16 v[88:91], a[240:243], a[176:179], v[88:91]\n"
"s_add_u32 s49, s49, 128\n"
"v_mfma_f32_16x16x32_bf16 v[88:91], a[244:247], a[180:183], v[88:91]\n"
"s_sub_u32 s41, s49, 1280\n"
"v_mfma_f32_16x16x32_bf16 v[84:87], a[232:235], a[176:179], v[84:87]\n"
"s_min_u32 s49, s49, s41\n"
"v_mfma_f32_16x16x32_bf16 v[84:87], a[236:239], a[180:183], v[84:87]\n"
"s_xor_b32 s51, s51, s74\n"
"v_mfma_f32_16x16x32_bf16 v[80:83], a[224:227], a[176:179], v[80:83]\n"
"ds_read_b128 a[176:179], v250 offset:2048\n"
"v_mfma_f32_16x16x32_bf16 v[80:83], a[228:231], a[180:183], v[80:83]\n"
"ds_read_b128 a[180:183], v251 offset:2048\n"
"v_mfma_f32_16x16x32_bf16 v[96:99], a[224:227], a[184:187], v[96:99]\n"
"v_mfma_f32_16x16x32_bf16 v[96:99], a[228:231], a[188:191], v[96:99]\n"
"v_mfma_f32_16x16x32_bf16 v[100:103], a[232:235], a[184:187], v[100:103]\n"
"v_mfma_f32_16x16x32_bf16 v[100:103], a[236:239], a[188:191], v[100:103]\n"
"v_mfma_f32_16x16x32_bf16 v[104:107], a[240:243], a[184:187], v[104:107]\n"
"v_mfma_f32_16x16x32_bf16 v[104:107], a[244:247], a[188:191], v[104:107]\n"
"v_mfma_f32_16x16x32_bf16 v[108:111], a[248:251], a[184:187], v[108:111]\n"
"s_add_u32 s47, s47, 1\n"
"ds_read_b128 a[184:187], v250 offset:4096\n"
"v_mfma_f32_16x16x32_bf16 v[108:111], a[252:255], a[188:191], v[108:111]\n"
"s_waitcnt lgkmcnt(1)\n"
"s_cmp_lg_u32 s47, s62\n"
"ds_read_b128 a[188:191], v251 offset:4096\n"
"s_cbranch_scc1 LPL_%=\n"
"LPX_%=:\n"
"s_cmp_eq_u32 s47, 18\n"
"s_cbranch_scc1 LPT2_%=\n"
"v_mfma_f32_16x16x32_bf16 v[112:115], a[192:195], a[136:139], v[112:115]\n"
"s_add_u32 s78, s96, 8192\n"
"v_mov_b32 v241, 1\n"
"v_mfma_f32_16x16x32_bf16 v[112:115], a[196:199], a[140:143], v[112:115]\n"
"s_mov_b64 exec, 1\n"
"buffer_store_dword v241, off, s[32:35], s96 sc0 sc1\n"
"buffer_store_dword v241, off, s[32:35], s78 sc0 sc1\n"
"s_mov_b64 exec, -1\n"
"v_mfma_f32_16x16x32_bf16 v[116:119], a[200:203], a[136:139], v[116:119]\n"
"s_waitcnt vmcnt(13)\n"
"v_mfma_f32_16x16x32_bf16 v[116:119], a[204:207], a[140:143], v[116:119]\n"
"s_barrier\n"
"v_mfma_f32_16x16x32_bf16 v[120:123], a[208:211], a[136:139], v[120:123]\n"
"s_add_u32 s40, s48, 256\n"
"s_xor_b32 m0, s50, 0xc000\n"
"ds_read_b64_tr_b16 a[232:233], v245 offset:16384\n"
"v_mfma_f32_16x16x32_bf16 v[120:123], a[212:215], a[140:143], v[120:123]\n"
"buffer_load_dwordx4 v224, s[28:31], s40 offen lds\n"
"v_mfma_f32_16x16x32_bf16 v[124:127], a[216:219], a[136:139], v[124:127]\n"
"v_mfma_f32_16x16x32_bf16 v[124:127], a[220:223], a[140:143], v[124:127]\n"
"s_memtime s[78:79]\n"
"v_mfma_f32_16x16x32_bf16 v[188:191], a[216:219], a[168:171], v[188:191]\n"
"ds_read_b64_tr_b16 a[224:225], v244 offset:16384\n"
"v_mfma_f32_16x16x32_bf16 v[188:191], a[220:223], a[172:175], v[188:191]\n"
"ds_read_b64_tr_b16 a[252:253], v245 offset:25600\n"
"v_mfma_f32_16x16x32_bf16 v[184:187], a[208:211], a[168:171], v[184:187]\n"
"ds_read_b64_tr_b16 a[254:255], v245 offset:26112\n"
"v_mfma_f32_16x16x32_bf16 v[184:187], a[212:215], a[172:175], v[184:187]\n"
"buffer_load_dwordx4 v225, s[28:31], s40 offen offset:1024 lds\n"
"v_mfma_f32_16x16x32_bf16 v[180:183], a[200:203], a[168:171], v[180:183]\n"
"v_mfma_f32_16x16x32_bf16 v[180:183], a[204:207], a[172:175], v[180:183]\n"
"v_mfma_f32_16x16x32_bf16 v[176:179], a[192:195], a[168:171], v[176:179]\n"
"ds_read_b64_tr_b16 a[238:239], v245 offset:25088\n"
"ds_read_b64_tr_b16 a[246:247], v244 offset:26112\n"
"v_mfma_f32_16x16x32_bf16 v[176:179], a[196:199], a[172:175], v[176:179]\n"
"ds_read_b64_tr_b16 a[240:241], v244 offset:17408\n"
"v_mfma_f32_16x16x32_bf16 v[192:195], a[192:195], a[176:179], v[192:195]\n"
"ds_read_b64_tr_b16 a[226:227], v244 offset:16896\n"
"ds_read_b64_tr_b16 a[250:251], v245 offset:17920\n"
"v_mfma_f32_16x16x32_bf16 v[192:195], a[196:199], a[180:183], v[192:195]\n"
"buffer_load_dwordx4 v226, s[28:31], s40 offen offset:2048 lds\n"
"v_mfma_f32_16x16x32_bf16 v[196:199], a[200:203], a[176:179], v[196:199]\n"
"v_bfe_u32 v241, v242, 4, 6\n"
"v_mfma_f32_16x16x32_bf16 v[196:199], a[204:207], a[180:183], v[196:199]\n"
"v_mul_u32_u24 v241, 24, v241\n"
"v_mfma_f32_16x16x32_bf16 v[200:203], a[208:211], a[176:179], v[200:203]\n"
"ds_read_b64_tr_b16 a[234:235], v245 offset:16896\n"
"ds_read_b64_tr_b16 a[236:237], v245 offset:24576\n"
"v_mfma_f32_16x16x32_bf16 v[200:203], a[212:215], a[180:183], v[200:203]\n"
"ds_read_b64_tr_b16 a[248:249], v245 offset:17408\n"
"v_mfma_f32_16x16x32_bf16 v[204:207], a[216:219], a[176:179], v[204:207]\n"
"ds_read_b64_tr_b16 a[228:229], v244 offset:24576\n"
"ds_read_b64_tr_b16 a[244:245], v244 offset:25600\n"
"v_mfma_f32_16x16x32_bf16 v[204:207], a[220:223], a[180:183], v[204:207]\n"
"s_waitcnt lgkmcnt(7)\n"
"buffer_load_dwordx4 v227, s[28:31], s40 offen offset:3072 lds\n"
"v_mfma_f32_16x16x32_bf16 v[220:223], a[216:219], a[184:187], v[220:223]\n"
"s_cmp_eq_u32 s47, 8\n"
"v_mfma_f32_16x16x32_bf16 v[220:223], a[220:223], a[188:191], v[220:223]\n"
"s_cselect_b32 s41, s52, s58\n"
"s_nop 1\n"
"v_pk_fma_f32 v[176:177], v[176:177], s[70:71], s[70:71] op_sel:[0,0,1] op_sel_hi:[1,0,1]\nv_pk_fma_f32 v[178:179], v[178:179], s[70:71], s[70:71] op_sel:[0,0,1] op_sel_hi:[1,0,1]\nv_pk_fma_f32 v[180:181], v[180:181], s[70:71], s[70:71] op_sel:[0,0,1] op_sel_hi:[1,0,1]\nv_pk_fma_f32 v[182:183], v[182:183], s[70:71], s[70:71] op_sel:[0,0,1] op_sel_hi:[1,0,1]\nv_pk_fma_f32 v[184:185], v[184:185], s[70:71], s[70:71] op_sel:[0,0,1] op_sel_hi:[1,0,1]\nv_pk_fma_f32 v[186:187], v[186:187], s[70:71], s[70:71] op_sel:[0,0,1] op_sel_hi:[1,0,1]\nv_pk_fma_f32 v[188:189], v[188:189], s[70:71], s[70:71] op_sel:[0,0,1] op_sel_hi:[1,0,1]\nv_pk_fma_f32 v[190:191], v[190:191], s[70:71], s[70:71] op_sel:[0,0,1] op_sel_hi:[1,0,1]\nv_pk_fma_f32 v[192:193], v[192:193], s[70:71], s[70:71] op_sel:[0,0,1] op_sel_hi:[1,0,1]\nv_pk_fma_f32 v[194:195], v[194:195], s[70:71], s[70:71] op_sel:[0,0,1] op_sel_hi:[1,0,1]\nv_pk_fma_f32 v[196:197], v[196:197], s[70:71], s[70:71] op_sel:[0,0,1] op_sel_hi:[1,0,1]\nv_pk_fma_f32 v[198:199], v[198:199], s[70:71], s[70:71] op_sel:[0,0,1] op_sel_hi:[1,0,1]\nv_pk_fma_f32 v[200:201], v[200:201], s[70:71], s[70:71] op_sel:[0,0,1] op_sel_hi:[1,0,1]\nv_pk_fma_f32 v[202:203], v[202:203], s[70:71], s[70:71] op_sel:[0,0,1] op_sel_hi:[1,0,1]\nv_pk_fma_f32 v[204:205], v[204:205], s[70:71], s[70:71] op_sel:[0,0,1] op_sel_hi:[1,0,1]\nv_pk_fma_f32 v[206:207], v[206:207], s[70:71], s[70:71] op_sel:[0,0,1] op_sel_hi:[1,0,1]\n"
"v_mfma_f32_16x16x32_bf16 v[216:219], a[208:211], a[184:187], v[216:219]\n"
"s_add_u32 s48, s48, s41\n"
"v_cvt_pk_bf16_f32 v176, v176, v177\n"
"v_cvt_pk_bf16_f32 v200, v200, v201\n"
"v_mfma_f32_16x16x32_bf16 v[216:219], a[212:215], a[188:191], v[216:219]\n"
"s_sub_u32 s41, s48, 5242880\n"
"ds_read_b64_tr_b16 a[242:243], v244 offset:17920\n"
"v_cvt_pk_bf16_f32 v177, v178, v179\n"
"v_mfma_f32_16x16x32_bf16 v[212:215], a[200:203], a[184:187], v[212:215]\n"
"s_min_u32 s48, s48, s41\n"
"v_cvt_pk_bf16_f32 v178, v180, v181\n"
"v_cvt_pk_bf16_f32 v179, v182, v183\n"
"v_mfma_f32_16x16x32_bf16 v[212:215], a[204:207], a[188:191], v[212:215]\n"
"v_cvt_pk_bf16_f32 v192, v192, v193\n"
"v_bfe_u32 v181, v242, 9, 1\n"
"v_mfma_f32_16x16x32_bf16 v[208:211], a[192:195], a[184:187], v[208:211]\n"
"s_mov_b32 m0, s50\n"
"v_cvt_pk_bf16_f32 v184, v184, v185\n"
"v_xor_b32 v246, s74, v246\n"
"v_mfma_f32_16x16x32_bf16 v[208:211], a[196:199], a[188:191], v[208:211]\n"
"buffer_load_dwordx4 v224, s[28:31], s48 offen lds\n"
"v_mfma_f32_16x16x32_bf16 v[160:163], a[192:195], a[160:163], v[160:163]\n"
"v_bfe_u32 v180, v242, 9, 1\n"
"v_and_b32 v182, 0xf0, v242\n"
"v_mfma_f32_16x16x32_bf16 v[160:163], a[196:199], a[164:167], v[160:163]\n"
"v_xor_b32 v251, s74, v251\n"
"v_xor_b32 v253, s74, v253\n"
"v_mfma_f32_16x16x32_bf16 v[164:167], a[200:203], a[160:163], v[164:167]\n"
"v_cvt_pk_bf16_f32 v185, v186, v187\n"
"v_cvt_pk_bf16_f32 v201, v202, v203\n"
"v_mfma_f32_16x16x32_bf16 v[164:167], a[204:207], a[164:167], v[164:167]\n"
"buffer_load_dwordx4 v225, s[28:31], s48 offen offset:1024 lds\n"
"v_mfma_f32_16x16x32_bf16 v[168:171], a[208:211], a[160:163], v[168:171]\n"
"v_lshl_add_u32 v180, v180, 8, v182\n"
"v_cvt_pk_bf16_f32 v193, v194, v195\n"
"v_mfma_f32_16x16x32_bf16 v[168:171], a[212:215], a[164:167], v[168:171]\n"
"v_cvt_pk_bf16_f32 v194, v196, v197\n"
"v_cvt_pk_bf16_f32 v203, v206, v207\n"
"v_mfma_f32_16x16x32_bf16 v[172:175], a[216:219], a[160:163], v[172:175]\n"
"v_bfe_u32 v182, v242, 8, 1\n"
"v_cvt_pk_bf16_f32 v195, v198, v199\n"
"v_mfma_f32_16x16x32_bf16 v[172:175], a[220:223], a[164:167], v[172:175]\n"
"v_lshl_add_u32 v180, v182, 3, v180\n"
"v_cvt_pk_bf16_f32 v187, v190, v191\n"
"v_mfma_f32_16x16x32_bf16 v[140:143], a[216:219], a[144:147], v[140:143]\n"
"v_xor_b32_e64 v182, s42, 1\n"
"v_cvt_pk_bf16_f32 v202, v204, v205\n"
"v_mfma_f32_16x16x32_bf16 v[140:143], a[220:223], a[148:151], v[140:143]\n"
"buffer_load_dwordx4 v226, s[28:31], s48 offen offset:2048 lds\n"
"v_mfma_f32_16x16x32_bf16 v[136:139], a[208:211], a[144:147], v[136:139]\n"
"v_cvt_pk_bf16_f32 v186, v188, v189\n"
"v_xor_b32 v247, s74, v247\n"
"v_mfma_f32_16x16x32_bf16 v[136:139], a[212:215], a[148:151], v[136:139]\n"
"v_pk_fma_f32 v[208:209], v[208:209], s[70:71], s[70:71] op_sel:[0,0,1] op_sel_hi:[1,0,1]\nv_pk_fma_f32 v[210:211], v[210:211], s[70:71], s[70:71] op_sel:[0,0,1] op_sel_hi:[1,0,1]\nv_pk_fma_f32 v[212:213], v[212:213], s[70:71], s[70:71] op_sel:[0,0,1] op_sel_hi:[1,0,1]\nv_pk_fma_f32 v[214:215], v[214:215], s[70:71], s[70:71] op_sel:[0,0,1] op_sel_hi:[1,0,1]\nv_pk_fma_f32 v[216:217], v[216:217], s[70:71], s[70:71] op_sel:[0,0,1] op_sel_hi:[1,0,1]\nv_pk_fma_f32 v[218:219], v[218:219], s[70:71], s[70:71] op_sel:[0,0,1] op_sel_hi:[1,0,1]\nv_pk_fma_f32 v[220:221], v[220:221], s[70:71], s[70:71] op_sel:[0,0,1] op_sel_hi:[1,0,1]\nv_pk_fma_f32 v[222:223], v[222:223], s[70:71], s[70:71] op_sel:[0,0,1] op_sel_hi:[1,0,1]\nv_pk_fma_f32 v[160:161], v[160:161], s[70:71], s[70:71] op_sel:[0,0,1] op_sel_hi:[1,0,1]\nv_pk_fma_f32 v[162:163], v[162:163], s[70:71], s[70:71] op_sel:[0,0,1] op_sel_hi:[1,0,1]\nv_pk_fma_f32 v[164:165], v[164:165], s[70:71], s[70:71] op_sel:[0,0,1] op_sel_hi:[1,0,1]\nv_pk_fma_f32 v[166:167], v[166:167], s[70:71], s[70:71] op_sel:[0,0,1] op_sel_hi:[1,0,1]\nv_pk_fma_f32 v[168:169], v[168:169], s[70:71], s[70:71] op_sel:[0,0,1] op_sel_hi:[1,0,1]\nv_pk_fma_f32 v[170:171], v[170:171], s[70:71], s[70:71] op_sel:[0,0,1] op_sel_hi:[1,0,1]\nv_pk_fma_f32 v[172:173], v[172:173], s[70:71], s[70:71] op_sel:[0,0,1] op_sel_hi:[1,0,1]\nv_pk_fma_f32 v[174:175], v[174:175], s[70:71], s[70:71] op_sel:[0,0,1] op_sel_hi:[1,0,1]\n"
"v_mfma_f32_16x16x32_bf16 v[132:135], a[200:203], a[144:147], v[132:135]\n"
"v_cvt_pk_bf16_f32 v216, v216, v217\n"
"v_cvt_pk_bf16_f32 v217, v218, v219\n"
"v_mfma_f32_16x16x32_bf16 v[132:135], a[204:207], a[148:151], v[132:135]\n"
"ds_read_b64_tr_b16 a[230:231], v244 offset:25088\n"
"v_lshl_add_u32 v180, v182, 10, v180\n"
"v_mfma_f32_16x16x32_bf16 v[128:131], a[192:195], a[144:147], v[128:131]\n"
"v_add_u32 v180, 94208, v180\n"
"v_cvt_pk_bf16_f32 v160, v160, v161\n"
"v_mfma_f32_16x16x32_bf16 v[128:131], a[196:199], a[148:151], v[128:131]\n"
"buffer_load_dwordx4 v227, s[28:31], s48 offen offset:3072 lds\n"
"v_mfma_f32_16x16x32_bf16 v[144:147], a[192:195], a[152:155], v[144:147]\n"
"s_xor_b32 s50, s50, s74\n"
"v_cvt_pk_bf16_f32 v208, v208, v209\n"
"v_cvt_pk_bf16_f32 v218, v220, v221\n"
"v_mfma_f32_16x16x32_bf16 v[144:147], a[196:199], a[156:159], v[144:147]\n"
"v_cvt_pk_bf16_f32 v209, v210, v211\n"
"v_cvt_pk_bf16_f32 v168, v168, v169\n"
"v_mfma_f32_16x16x32_bf16 v[148:151], a[200:203], a[152:155], v[148:151]\n"
"v_cvt_pk_bf16_f32 v219, v222, v223\n"
"v_xor_b32 v244, s74, v244\n"
"v_mfma_f32_16x16x32_bf16 v[148:151], a[204:207], a[156:159], v[148:151]\n"
"ds_write2st64_b64 v180, v[176:177], v[178:179] offset0:0 offset1:1\n"
"v_pk_fma_f32 v[112:113], v[112:113], s[70:71], s[70:71] op_sel:[0,0,1] op_sel_hi:[1,0,1]\nv_pk_fma_f32 v[114:115], v[114:115], s[70:71], s[70:71] op_sel:[0,0,1] op_sel_hi:[1,0,1]\nv_pk_fma_f32 v[116:117], v[116:117], s[70:71], s[70:71] op_sel:[0,0,1] op_sel_hi:[1,0,1]\nv_pk_fma_f32 v[118:119], v[118:119], s[70:71], s[70:71] op_sel:[0,0,1] op_sel_hi:[1,0,1]\nv_pk_fma_f32 v[120:121], v[120:121], s[70:71], s[70:71] op_sel:[0,0,1] op_sel_hi:[1,0,1]\nv_pk_fma_f32 v[122:123], v[122:123], s[70:71], s[70:71] op_sel:[0,0,1] op_sel_hi:[1,0,1]\nv_pk_fma_f32 v[124:125], v[124:125], s[70:71], s[70:71] op_sel:[0,0,1] op_sel_hi:[1,0,1]\nv_pk_fma_f32 v[126:127], v[126:127], s[70:71], s[70:71] op_sel:[0,0,1] op_sel_hi:[1,0,1]\nv_pk_fma_f32 v[128:129], v[128:129], s[70:71], s[70:71] op_sel:[0,0,1] op_sel_hi:[1,0,1]\nv_pk_fma_f32 v[130:131], v[130:131], s[70:71], s[70:71] op_sel:[0,0,1] op_sel_hi:[1,0,1]\nv_pk_fma_f32 v[132:133], v[132:133], s[70:71], s[70:71] op_sel:[0,0,1] op_sel_hi:[1,0,1]\nv_pk_fma_f32 v[134:135], v[134:135], s[70:71], s[70:71] op_sel:[0,0,1] op_sel_hi:[1,0,1]\nv_pk_fma_f32 v[136:137], v[136:137], s[70:71], s[70:71] op_sel:[0,0,1] op_sel_hi:[1,0,1]\nv_pk_fma_f32 v[138:139], v[138:139], s[70:71], s[70:71] op_sel:[0,0,1] op_sel_hi:[1,0,1]\nv_pk_fma_f32 v[140:141], v[140:141], s[70:71], s[70:71] op_sel:[0,0,1] op_sel_hi:[1,0,1]\nv_pk_fma_f32 v[142:143], v[142:143], s[70:71], s[70:71] op_sel:[0,0,1] op_sel_hi:[1,0,1]\n"
"v_mfma_f32_16x16x32_bf16 v[152:155], a[208:211], a[152:155], v[152:155]\n"
"v_cvt_pk_bf16_f32 v120, v120, v121\n"
"v_cvt_pk_bf16_f32 v136, v136, v137\n"
"v_mfma_f32_16x16x32_bf16 v[152:155], a[212:215], a[156:159], v[152:155]\n"
"ds_write2st64_b64 v180, v[192:193], v[194:195] offset0:88 offset1:89\n"
"v_and_b32 v182, 0xf0, v242\n"
"v_mfma_f32_16x16x32_bf16 v[156:159], a[216:219], a[152:155], v[156:159]\n"
"v_cvt_pk_bf16_f32 v169, v170, v171\n"
"v_lshl_add_u32 v181, v181, 8, v182\n"
"v_mfma_f32_16x16x32_bf16 v[156:159], a[220:223], a[156:159], v[156:159]\n"
"s_waitcnt lgkmcnt(2)\n"
"ds_write2st64_b64 v180, v[200:201], v[202:203] offset0:96 offset1:97\n"
"v_cvt_pk_bf16_f32 v112, v112, v113\n"
"v_xor_b32 v245, s74, v245\n"
"v_bfe_u32 v182, v242, 8, 1\n"
"v_lshl_add_u32 v181, v182, 9, v181\n"
"v_mfma_f32_16x16x32_bf16 v[96:99], a[224:227], a[184:187], v[96:99]\n"
"v_cvt_pk_bf16_f32 v161, v162, v163\n"
"v_xor_b32 v252, s74, v252\n"
"v_mfma_f32_16x16x32_bf16 v[96:99], a[228:231], a[188:191], v[96:99]\n"
"ds_write2st64_b64 v180, v[184:185], v[186:187] offset0:64 offset1:65\n"
"v_cvt_pk_bf16_f32 v137, v138, v139\n"
"v_mfma_f32_16x16x32_bf16 v[100:103], a[232:235], a[184:187], v[100:103]\n"
"s_waitcnt vmcnt(10)\n"
"v_cvt_pk_bf16_f32 v163, v166, v167\n"
"v_cvt_pk_bf16_f32 v113, v114, v115\n"
"v_mfma_f32_16x16x32_bf16 v[100:103], a[236:239], a[188:191], v[100:103]\n"
"s_barrier\n"
"ds_write2st64_b64 v180, v[216:217], v[218:219] offset0:128 offset1:129\n"
"v_pk_fma_f32 v[144:145], v[144:145], s[70:71], s[70:71] op_sel:[0,0,1] op_sel_hi:[1,0,1]\nv_pk_fma_f32 v[146:147], v[146:147], s[70:71], s[70:71] op_sel:[0,0,1] op_sel_hi:[1,0,1]\nv_pk_fma_f32 v[148:149], v[148:149], s[70:71], s[70:71] op_sel:[0,0,1] op_sel_hi:[1,0,1]\nv_pk_fma_f32 v[150:151], v[150:151], s[70:71], s[70:71] op_sel:[0,0,1] op_sel_hi:[1,0,1]\nv_pk_fma_f32 v[152:153], v[152:153], s[70:71], s[70:71] op_sel:[0,0,1] op_sel_hi:[1,0,1]\nv_pk_fma_f32 v[154:155], v[154:155], s[70:71], s[70:71] op_sel:[0,0,1] op_sel_hi:[1,0,1]\nv_pk_fma_f32 v[156:157], v[156:157], s[70:71], s[70:71] op_sel:[0,0,1] op_sel_hi:[1,0,1]\nv_pk_fma_f32 v[158:159], v[158:159], s[70:71], s[70:71] op_sel:[0,0,1] op_sel_hi:[1,0,1]\n"
"v_mfma_f32_16x16x32_bf16 v[104:107], a[240:243], a[184:187], v[104:107]\n"
"s_mov_b32 m0, s51\n"
"ds_read_b64_tr_b16 a[222:223], v245 offset:9728\n"
"v_cvt_pk_bf16_f32 v128, v128, v129\n"
"v_mfma_f32_16x16x32_bf16 v[104:107], a[244:247], a[188:191], v[104:107]\n"
"buffer_load_dwordx4 v248, s[24:27], s49 offen lds\n"
"v_mfma_f32_16x16x32_bf16 v[108:111], a[248:251], a[184:187], v[108:111]\n"
"v_cvt_pk_bf16_f32 v139, v142, v143\n"
"v_cvt_pk_bf16_f32 v144, v144, v145\n"
"v_mfma_f32_16x16x32_bf16 v[108:111], a[252:255], a[188:191], v[108:111]\n"
"v_cvt_pk_bf16_f32 v138, v140, v141\n"
"v_xor_b32 v250, s74, v250\n"
"v_mfma_f32_16x16x32_bf16 v[92:95], a[248:251], a[176:179], v[92:95]\n"
"ds_read_b64_tr_b16 a[218:219], v245 offset:1536\n"
"v_cvt_pk_bf16_f32 v152, v152, v153\n"
"v_mfma_f32_16x16x32_bf16 v[92:95], a[252:255], a[180:183], v[92:95]\n"
"v_permlane16_swap_b32 v137, v139\n"
"v_mfma_f32_16x16x32_bf16 v[88:91], a[240:243], a[176:179], v[88:91]\n"
"ds_read_b64_tr_b16 a[204:205], v245 offset:8192\n"
"v_xor_b32_e64 v182, s42, 0\n"
"v_mfma_f32_16x16x32_bf16 v[88:91], a[244:247], a[180:183], v[88:91]\n"
"buffer_load_dwordx4 v249, s[24:27], s49 offen offset:1024 lds\n"
"v_mfma_f32_16x16x32_bf16 v[84:87], a[232:235], a[176:179], v[84:87]\n"
"v_cvt_pk_bf16_f32 v129, v130, v131\n"
"v_cvt_pk_bf16_f32 v130, v132, v133\n"
"v_mfma_f32_16x16x32_bf16 v[84:87], a[236:239], a[180:183], v[84:87]\n"
"v_cvt_pk_bf16_f32 v211, v214, v215\n"
"v_cvt_pk_bf16_f32 v171, v174, v175\n"
"v_mfma_f32_16x16x32_bf16 v[80:83], a[224:227], a[176:179], v[80:83]\n"
"ds_read_b64_tr_b16 a[210:211], v244 offset:1536\n"
"v_cvt_pk_bf16_f32 v153, v154, v155\n"
"v_mfma_f32_16x16x32_bf16 v[80:83], a[228:231], a[180:183], v[80:83]\n"
"v_permlane16_swap_b32 v136, v138\n"
"v_mfma_f32_16x16x32_bf16 v[64:67], a[224:227], a[168:171], v[64:67]\n"
"v_cvt_pk_bf16_f32 v210, v212, v213\n"
"v_accvgpr_write_b32 a25, v137\n"
"v_mfma_f32_16x16x32_bf16 v[64:67], a[228:231], a[172:175], v[64:67]\n"
"buffer_load_dwordx4 v254, s[24:27], s49 offen offset:2048 lds\n"
"v_mfma_f32_16x16x32_bf16 v[68:71], a[232:235], a[168:171], v[68:71]\n"
"v_cvt_pk_bf16_f32 v121, v122, v123\n"
"v_cvt_pk_bf16_f32 v145, v146, v147\n"
"v_mfma_f32_16x16x32_bf16 v[68:71], a[236:239], a[172:175], v[68:71]\n"
"v_permlane16_swap_b32 v169, v171\n"
"v_mfma_f32_16x16x32_bf16 v[72:75], a[240:243], a[168:171], v[72:75]\n"
"ds_read_b64_tr_b16 a[216:217], v245 offset:1024\n"
"v_accvgpr_write_b32 a27, v139\n"
"v_mfma_f32_16x16x32_bf16 v[72:75], a[244:247], a[172:175], v[72:75]\n"
"ds_write2st64_b64 v180, v[208:209], v[210:211] offset0:120 offset1:121\n"
"v_cvt_pk_bf16_f32 v155, v158, v159\n"
"v_pk_fma_f32 v[96:97], v[96:97], s[70:71], s[70:71] op_sel:[0,0,1] op_sel_hi:[1,0,1]\nv_pk_fma_f32 v[98:99], v[98:99], s[70:71], s[70:71] op_sel:[0,0,1] op_sel_hi:[1,0,1]\nv_pk_fma_f32 v[100:101], v[100:101], s[70:71], s[70:71] op_sel:[0,0,1] op_sel_hi:[1,0,1]\nv_pk_fma_f32 v[102:103], v[102:103], s[70:71], s[70:71] op_sel:[0,0,1] op_sel_hi:[1,0,1]\nv_pk_fma_f32 v[104:105], v[104:105], s[70:71], s[70:71] op_sel:[0,0,1] op_sel_hi:[1,0,1]\nv_pk_fma_f32 v[106:107], v[106:107], s[70:71], s[70:71] op_sel:[0,0,1] op_sel_hi:[1,0,1]\nv_pk_fma_f32 v[108:109], v[108:109], s[70:71], s[70:71] op_sel:[0,0,1] op_sel_hi:[1,0,1]\nv_pk_fma_f32 v[110:111], v[110:111], s[70:71], s[70:71] op_sel:[0,0,1] op_sel_hi:[1,0,1]\nv_pk_fma_f32 v[80:81], v[80:81], s[70:71], s[70:71] op_sel:[0,0,1] op_sel_hi:[1,0,1]\nv_pk_fma_f32 v[82:83], v[82:83], s[70:71], s[70:71] op_sel:[0,0,1] op_sel_hi:[1,0,1]\nv_pk_fma_f32 v[84:85], v[84:85], s[70:71], s[70:71] op_sel:[0,0,1] op_sel_hi:[1,0,1]\nv_pk_fma_f32 v[86:87], v[86:87], s[70:71], s[70:71] op_sel:[0,0,1] op_sel_hi:[1,0,1]\nv_pk_fma_f32 v[88:89], v[88:89], s[70:71], s[70:71] op_sel:[0,0,1] op_sel_hi:[1,0,1]\nv_pk_fma_f32 v[90:91], v[90:91], s[70:71], s[70:71] op_sel:[0,0,1] op_sel_hi:[1,0,1]\nv_pk_fma_f32 v[92:93], v[92:93], s[70:71], s[70:71] op_sel:[0,0,1] op_sel_hi:[1,0,1]\nv_pk_fma_f32 v[94:95], v[94:95], s[70:71], s[70:71] op_sel:[0,0,1] op_sel_hi:[1,0,1]\n"
"v_cvt_pk_bf16_f32 v96, v96, v97\n"
"v_mfma_f32_16x16x32_bf16 v[76:79], a[248:251], a[168:171], v[76:79]\n"
"ds_read_b64_tr_b16 a[198:199], v244 offset:8704\n"
"v_cvt_pk_bf16_f32 v104, v104, v105\n"
"v_mfma_f32_16x16x32_bf16 v[76:79], a[252:255], a[172:175], v[76:79]\n"
"buffer_load_dwordx4 v255, s[24:27], s49 offen offset:3072 lds\n"
"v_mfma_f32_16x16x32_bf16 v[60:63], a[248:251], a[160:163], v[60:63]\n"
"v_cvt_pk_bf16_f32 v122, v124, v125\n"
"v_cvt_pk_bf16_f32 v131, v134, v135\n"
"v_mfma_f32_16x16x32_bf16 v[60:63], a[252:255], a[164:167], v[60:63]\n"
"ds_read_b64_tr_b16 a[196:197], v244 offset:8192\n"
"v_cvt_pk_bf16_f32 v115, v118, v119\n"
"v_mfma_f32_16x16x32_bf16 v[56:59], a[240:243], a[160:163], v[56:59]\n"
"v_cvt_pk_bf16_f32 v154, v156, v157\n"
"v_cvt_pk_bf16_f32 v97, v98, v99\n"
"v_mfma_f32_16x16x32_bf16 v[56:59], a[244:247], a[164:167], v[56:59]\n"
"s_add_u32 s40, s49, 40960\n"
"ds_read_b128 a[176:179], v250 offset:2048\n"
"v_lshl_add_u32 v181, v182, 10, v181\n"
"v_mfma_f32_16x16x32_bf16 v[52:55], a[232:235], a[160:163], v[52:55]\n"
"s_add_u32 m0, s51, 4096\n"
"ds_read_b64_tr_b16 a[214:215], v244 offset:9728\n"
"ds_read_b64_tr_b16 a[220:221], v245 offset:9216\n"
"v_mfma_f32_16x16x32_bf16 v[52:55], a[236:239], a[164:167], v[52:55]\n"
"buffer_load_dwordx4 v248, s[24:27], s40 offen lds\n"
"v_cvt_pk_bf16_f32 v162, v164, v165\n"
"v_permlane16_swap_b32 v113, v115\n"
"v_cvt_pk_bf16_f32 v123, v126, v127\n"
"v_permlane16_swap_b32 v128, v130\n"
"v_permlane16_swap_b32 v129, v131\n"
"v_add_u32 v181, 94208, v181\n"
"v_cvt_pk_bf16_f32 v99, v102, v103\n"
"v_mfma_f32_16x16x32_bf16 v[48:51], a[224:227], a[160:163], v[48:51]\n"
"s_waitcnt lgkmcnt(0)\n"
"v_permlane16_swap_b32 v152, v154\n"
"v_mfma_f32_16x16x32_bf16 v[48:51], a[228:231], a[164:167], v[48:51]\n"
"s_barrier\n"
"ds_read_b64_tr_b16 a[194:195], v244 offset:512\n"
"ds_read_b64_tr_b16 a[200:201], v245 offset:0\n"
"ds_read_b64_tr_b16 a[206:207], v245 offset:8704\n"
"v_permlane16_swap_b32 v161, v163\n"
"v_cvt_pk_bf16_f32 v114, v116, v117\n"
"v_permlane16_swap_b32 v120, v122\n"
"v_accvgpr_write_b32 a24, v136\n"
"v_cvt_pk_bf16_f32 v146, v148, v149\n"
"v_mfma_f32_16x16x32_bf16 v[0:3], a[224:227], a[136:139], v[0:3]\n"
"v_add_u32 v182, s75, v181\n"
"v_cvt_pk_bf16_f32 v80, v80, v81\n"
"v_mfma_f32_16x16x32_bf16 v[0:3], a[228:231], a[140:143], v[0:3]\n"
"ds_read_b128 a[28:31], v181 offset:49152\n"
"v_cvt_pk_bf16_f32 v105, v106, v107\n"
"v_mfma_f32_16x16x32_bf16 v[4:7], a[232:235], a[136:139], v[4:7]\n"
"ds_read_b64_tr_b16 a[202:203], v245 offset:512\n"
"ds_read_b64_tr_b16 a[212:213], v244 offset:9216\n"
"v_mfma_f32_16x16x32_bf16 v[4:7], a[236:239], a[140:143], v[4:7]\n"
"buffer_load_dwordx4 v249, s[24:27], s40 offen offset:1024 lds\n"
"v_permlane16_swap_b32 v121, v123\n"
"v_permlane16_swap_b32 v153, v155\n"
"v_accvgpr_write_b32 a41, v153\n"
"v_cvt_pk_bf16_f32 v98, v100, v101\n"
"v_pk_fma_f32 v[64:65], v[64:65], s[70:71], s[70:71] op_sel:[0,0,1] op_sel_hi:[1,0,1]\nv_pk_fma_f32 v[66:67], v[66:67], s[70:71], s[70:71] op_sel:[0,0,1] op_sel_hi:[1,0,1]\nv_pk_fma_f32 v[68:69], v[68:69], s[70:71], s[70:71] op_sel:[0,0,1] op_sel_hi:[1,0,1]\nv_pk_fma_f32 v[70:71], v[70:71], s[70:71], s[70:71] op_sel:[0,0,1] op_sel_hi:[1,0,1]\nv_pk_fma_f32 v[72:73], v[72:73], s[70:71], s[70:71] op_sel:[0,0,1] op_sel_hi:[1,0,1]\nv_pk_fma_f32 v[74:75], v[74:75], s[70:71], s[70:71] op_sel:[0,0,1] op_sel_hi:[1,0,1]\nv_pk_fma_f32 v[76:77], v[76:77], s[70:71], s[70:71] op_sel:[0,0,1] op_sel_hi:[1,0,1]\nv_pk_fma_f32 v[78:79], v[78:79], s[70:71], s[70:71] op_sel:[0,0,1] op_sel_hi:[1,0,1]\nv_pk_fma_f32 v[48:49], v[48:49], s[70:71], s[70:71] op_sel:[0,0,1] op_sel_hi:[1,0,1]\nv_pk_fma_f32 v[50:51], v[50:51], s[70:71], s[70:71] op_sel:[0,0,1] op_sel_hi:[1,0,1]\nv_pk_fma_f32 v[52:53], v[52:53], s[70:71], s[70:71] op_sel:[0,0,1] op_sel_hi:[1,0,1]\nv_pk_fma_f32 v[54:55], v[54:55], s[70:71], s[70:71] op_sel:[0,0,1] op_sel_hi:[1,0,1]\nv_pk_fma_f32 v[56:57], v[56:57], s[70:71], s[70:71] op_sel:[0,0,1] op_sel_hi:[1,0,1]\nv_pk_fma_f32 v[58:59], v[58:59], s[70:71], s[70:71] op_sel:[0,0,1] op_sel_hi:[1,0,1]\nv_pk_fma_f32 v[60:61], v[60:61], s[70:71], s[70:71] op_sel:[0,0,1] op_sel_hi:[1,0,1]\nv_pk_fma_f32 v[62:63], v[62:63], s[70:71], s[70:71] op_sel:[0,0,1] op_sel_hi:[1,0,1]\n"
"v_cvt_pk_bf16_f32 v64, v64, v65\n"
"v_mfma_f32_16x16x32_bf16 v[8:11], a[240:243], a[136:139], v[8:11]\n"
"v_permlane16_swap_b32 v112, v114\n"
"v_mfma_f32_16x16x32_bf16 v[8:11], a[244:247], a[140:143], v[8:11]\n"
"v_permlane16_swap_b32 v144, v146\n"
"v_mfma_f32_16x16x32_bf16 v[12:15], a[248:251], a[136:139], v[12:15]\n"
"ds_read_b64_tr_b16 a[192:193], v244 offset:0\n"
"v_accvgpr_write_b32 a8, v120\n"
"v_mfma_f32_16x16x32_bf16 v[12:15], a[252:255], a[140:143], v[12:15]\n"
"ds_read_b128 a[36:39], v181 offset:61440\n"
"v_cvt_pk_bf16_f32 v170, v172, v173\n"
"v_mfma_f32_16x16x32_bf16 v[28:31], a[248:251], a[144:147], v[28:31]\n"
"ds_read_b64_tr_b16 a[208:209], v244 offset:1024\n"
"v_accvgpr_write_b32 a16, v128\n"
"v_mfma_f32_16x16x32_bf16 v[28:31], a[252:255], a[148:151], v[28:31]\n"
"buffer_load_dwordx4 v254, s[24:27], s40 offen offset:2048 lds\n"
"v_mfma_f32_16x16x32_bf16 v[24:27], a[240:243], a[144:147], v[24:27]\n"
"s_add_u32 s49, s49, 128\n"
"v_accvgpr_write_b32 a1, v113\n"
"v_cvt_pk_bf16_f32 v147, v150, v151\n"
"v_mfma_f32_16x16x32_bf16 v[24:27], a[244:247], a[148:151], v[24:27]\n"
"s_sub_u32 s41, s49, 1280\n"
"ds_read_b128 a[164:167], v253 offset:0\n"
"v_accvgpr_write_b32 a40, v152\n"
"v_mfma_f32_16x16x32_bf16 v[20:23], a[232:235], a[144:147], v[20:23]\n"
"s_min_u32 s49, s49, s41\n"
"ds_read_b128 a[4:7], v181 offset:0\n"
"v_accvgpr_write_b32 a11, v123\n"
"v_mfma_f32_16x16x32_bf16 v[20:23], a[236:239], a[148:151], v[20:23]\n"
"s_xor_b32 s51, s51, s74\n"
"ds_read_b128 a[12:15], v181 offset:32768\n"
"v_accvgpr_write_b32 a0, v112\n"
"v_mfma_f32_16x16x32_bf16 v[16:19], a[224:227], a[144:147], v[16:19]\n"
"ds_read_b128 a[136:139], v246 offset:0\n"
"v_bfe_u32 v100, v242, 9, 1\n"
"v_mfma_f32_16x16x32_bf16 v[16:19], a[228:231], a[148:151], v[16:19]\n"
"v_accvgpr_write_b32 a26, v138\n"
"v_accvgpr_write_b32 a32, v144\n"
"v_mfma_f32_16x16x32_bf16 v[32:35], a[224:227], a[152:155], v[32:35]\n"
"ds_read_b128 a[140:143], v247 offset:0\n"
"v_cvt_pk_bf16_f32 v56, v56, v57\n"
"v_mfma_f32_16x16x32_bf16 v[32:35], a[228:231], a[156:159], v[32:35]\n"
"ds_read_b128 a[188:191], v251 offset:4096\n"
"v_accvgpr_write_b32 a43, v155\n"
"v_mfma_f32_16x16x32_bf16 v[36:39], a[232:235], a[152:155], v[36:39]\n"
"ds_read_b128 a[20:23], v181 offset:45056\n"
"v_accvgpr_write_b32 a9, v121\n"
"v_mfma_f32_16x16x32_bf16 v[36:39], a[236:239], a[156:159], v[36:39]\n"
"ds_read_b128 a[44:47], v182 offset:0\n"
"v_accvgpr_write_b32 a17, v129\n"
"v_mfma_f32_16x16x32_bf16 v[40:43], a[240:243], a[152:155], v[40:43]\n"
"v_accvgpr_write_b32 a2, v114\n"
"v_accvgpr_write_b32 a3, v115\n"
"v_mfma_f32_16x16x32_bf16 v[40:43], a[244:247], a[156:159], v[40:43]\n"
"ds_read_b128 a[184:187], v250 offset:4096\n"
"v_cvt_pk_bf16_f32 v65, v66, v67\n"
"v_mfma_f32_16x16x32_bf16 v[44:47], a[248:251], a[152:155], v[44:47]\n"
"s_add_u32 s47, s47, 1\n"
"ds_read_b128 a[144:147], v246 offset:2048\n"
"v_accvgpr_write_b32 a10, v122\n"
"v_mfma_f32_16x16x32_bf16 v[44:47], a[252:255], a[156:159], v[44:47]\n"
"s_waitcnt lgkmcnt(6)\n"
"ds_read_b128 a[148:151], v247 offset:2048\n"
"v_permlane16_swap_b32 v160, v162\n"
"v_permlane16_swap_b32 v168, v170\n"
"v_permlane16_swap_b32 v145, v147\n"
"v_pk_fma_f32 v[0:1], v[0:1], s[70:71], s[70:71] op_sel:[0,0,1] op_sel_hi:[1,0,1]\nv_pk_fma_f32 v[2:3], v[2:3], s[70:71], s[70:71] op_sel:[0,0,1] op_sel_hi:[1,0,1]\nv_pk_fma_f32 v[4:5], v[4:5], s[70:71], s[70:71] op_sel:[0,0,1] op_sel_hi:[1,0,1]\nv_pk_fma_f32 v[6:7], v[6:7], s[70:71], s[70:71] op_sel:[0,0,1] op_sel_hi:[1,0,1]\nv_pk_fma_f32 v[8:9], v[8:9], s[70:71], s[70:71] op_sel:[0,0,1] op_sel_hi:[1,0,1]\nv_pk_fma_f32 v[10:11], v[10:11], s[70:71], s[70:71] op_sel:[0,0,1] op_sel_hi:[1,0,1]\nv_pk_fma_f32 v[12:13], v[12:13], s[70:71], s[70:71] op_sel:[0,0,1] op_sel_hi:[1,0,1]\nv_pk_fma_f32 v[14:15], v[14:15], s[70:71], s[70:71] op_sel:[0,0,1] op_sel_hi:[1,0,1]\nv_pk_fma_f32 v[16:17], v[16:17], s[70:71], s[70:71] op_sel:[0,0,1] op_sel_hi:[1,0,1]\nv_pk_fma_f32 v[18:19], v[18:19], s[70:71], s[70:71] op_sel:[0,0,1] op_sel_hi:[1,0,1]\nv_pk_fma_f32 v[20:21], v[20:21], s[70:71], s[70:71] op_sel:[0,0,1] op_sel_hi:[1,0,1]\nv_pk_fma_f32 v[22:23], v[22:23], s[70:71], s[70:71] op_sel:[0,0,1] op_sel_hi:[1,0,1]\nv_pk_fma_f32 v[24:25], v[24:25], s[70:71], s[70:71] op_sel:[0,0,1] op_sel_hi:[1,0,1]\nv_pk_fma_f32 v[26:27], v[26:27], s[70:71], s[70:71] op_sel:[0,0,1] op_sel_hi:[1,0,1]\nv_pk_fma_f32 v[28:29], v[28:29], s[70:71], s[70:71] op_sel:[0,0,1] op_sel_hi:[1,0,1]\nv_pk_fma_f32 v[30:31], v[30:31], s[70:71], s[70:71] op_sel:[0,0,1] op_sel_hi:[1,0,1]\n"
"v_cvt_pk_bf16_f32 v8, v8, v9\n"
"v_cvt_pk_bf16_f32 v9, v10, v11\n"
"v_mfma_f32_16x16x32_bf16 v[112:115], a[192:195], a[136:139], v[228:231]\n"
"s_waitcnt lgkmcnt(6)\n"
"v_accvgpr_write_b32 a19, v131\n"
"v_cvt_pk_bf16_f32 v0, v0, v1\n"
"v_mfma_f32_16x16x32_bf16 v[112:115], a[196:199], a[140:143], v[112:115]\n"
"v_accvgpr_write_b32 a18, v130\n"
"v_cvt_pk_bf16_f32 v1, v2, v3\n"
"v_mfma_f32_16x16x32_bf16 v[116:119], a[200:203], a[136:139], v[228:231]\n"
"s_waitcnt vmcnt(11) lgkmcnt(3)\n"
"ds_read_b128 a[168:171], v250 offset:0\n"
"v_bfe_u32 v101, v242, 9, 1\n"
"v_mfma_f32_16x16x32_bf16 v[116:119], a[204:207], a[140:143], v[116:119]\n"
"s_barrier\n"
"ds_write_b128 v243, v[168:171] offset:2048\n"
"v_accvgpr_write_b32 a33, v145\n"
"v_cvt_pk_bf16_f32 v57, v58, v59\n"
"v_cvt_pk_bf16_f32 v10, v12, v13\n"
"v_cvt_pk_bf16_f32 v16, v16, v17\n"
"v_pk_fma_f32 v[32:33], v[32:33], s[70:71], s[70:71] op_sel:[0,0,1] op_sel_hi:[1,0,1]\nv_pk_fma_f32 v[34:35], v[34:35], s[70:71], s[70:71] op_sel:[0,0,1] op_sel_hi:[1,0,1]\nv_pk_fma_f32 v[36:37], v[36:37], s[70:71], s[70:71] op_sel:[0,0,1] op_sel_hi:[1,0,1]\nv_pk_fma_f32 v[38:39], v[38:39], s[70:71], s[70:71] op_sel:[0,0,1] op_sel_hi:[1,0,1]\nv_pk_fma_f32 v[40:41], v[40:41], s[70:71], s[70:71] op_sel:[0,0,1] op_sel_hi:[1,0,1]\nv_pk_fma_f32 v[42:43], v[42:43], s[70:71], s[70:71] op_sel:[0,0,1] op_sel_hi:[1,0,1]\nv_pk_fma_f32 v[44:45], v[44:45], s[70:71], s[70:71] op_sel:[0,0,1] op_sel_hi:[1,0,1]\nv_pk_fma_f32 v[46:47], v[46:47], s[70:71], s[70:71] op_sel:[0,0,1] op_sel_hi:[1,0,1]\n"
"v_mfma_f32_16x16x32_bf16 v[120:123], a[208:211], a[136:139], v[228:231]\n"
"s_add_u32 s40, s48, 256\n"
"s_xor_b32 m0, s50, 0xc000\n"
"ds_read_b128 a[152:155], v246 offset:4096\n"
"v_mfma_f32_16x16x32_bf16 v[120:123], a[212:215], a[140:143], v[120:123]\n"
"buffer_load_dwordx4 v224, s[28:31], s40 offen lds\n"
"v_mfma_f32_16x16x32_bf16 v[124:127], a[216:219], a[136:139], v[228:231]\n"
"v_accvgpr_write_b32 a35, v147\n"
"v_and_b32 v102, 0xf0, v242\n"
"v_mfma_f32_16x16x32_bf16 v[124:127], a[220:223], a[140:143], v[124:127]\n"
"s_waitcnt lgkmcnt(3)\n"
"ds_read_b128 a[156:159], v247 offset:4096\n"
"v_cvt_pk_bf16_f32 v88, v88, v89\n"
"v_mfma_f32_16x16x32_bf16 v[140:143], a[216:219], a[144:147], v[228:231]\n"
"ds_read_b128 a[160:163], v252 offset:0\n"
"v_cvt_pk_bf16_f32 v11, v14, v15\n"
"v_mfma_f32_16x16x32_bf16 v[140:143], a[220:223], a[148:151], v[140:143]\n"
"ds_read_b64_tr_b16 a[250:251], v245 offset:17920\n"
"ds_read_b64_tr_b16 a[252:253], v245 offset:25600\n"
"v_mfma_f32_16x16x32_bf16 v[136:139], a[208:211], a[144:147], v[228:231]\n"
"v_accvgpr_write_b32 a42, v154\n"
"v_cvt_pk_bf16_f32 v48, v48, v49\n"
"v_mfma_f32_16x16x32_bf16 v[136:139], a[212:215], a[148:151], v[136:139]\n"
"buffer_load_dwordx4 v225, s[28:31], s40 offen offset:1024 lds\n"
"v_mfma_f32_16x16x32_bf16 v[132:135], a[200:203], a[144:147], v[228:231]\n"
"v_accvgpr_write_b32 a34, v146\n"
"v_cvt_pk_bf16_f32 v32, v32, v33\n"
"v_mfma_f32_16x16x32_bf16 v[132:135], a[204:207], a[148:151], v[132:135]\n"
"v_permlane16_swap_b32 v9, v11\n"
"v_mfma_f32_16x16x32_bf16 v[128:131], a[192:195], a[144:147], v[228:231]\n"
"ds_read_b64_tr_b16 a[238:239], v245 offset:25088\n"
"v_cvt_pk_bf16_f32 v2, v4, v5\n"
"v_mfma_f32_16x16x32_bf16 v[128:131], a[196:199], a[148:151], v[128:131]\n"
"s_waitcnt lgkmcnt(4)\n"
"ds_write_b128 v243, v[160:163] offset:0\n"
"v_lshl_add_u32 v100, v100, 8, v102\n"
"v_bfe_u32 v102, v242, 8, 1\n"
"v_cvt_pk_bf16_f32 v106, v108, v109\n"
"v_cvt_pk_bf16_f32 v49, v50, v51\n"
"v_cvt_pk_bf16_f32 v3, v6, v7\n"
"v_cvt_pk_bf16_f32 v17, v18, v19\n"
"v_cvt_pk_bf16_f32 v24, v24, v25\n"
"v_mfma_f32_16x16x32_bf16 v[144:147], a[192:195], a[152:155], v[228:231]\n"
"ds_read_b64_tr_b16 a[236:237], v245 offset:24576\n"
"ds_read_b64_tr_b16 a[254:255], v245 offset:26112\n"
"v_mfma_f32_16x16x32_bf16 v[144:147], a[196:199], a[156:159], v[144:147]\n"
"buffer_load_dwordx4 v226, s[28:31], s40 offen offset:2048 lds\n"
"v_mfma_f32_16x16x32_bf16 v[148:151], a[200:203], a[152:155], v[228:231]\n"
"v_accvgpr_write_b32 a57, v9\n"
"v_cvt_pk_bf16_f32 v25, v26, v27\n"
"v_mfma_f32_16x16x32_bf16 v[148:151], a[204:207], a[156:159], v[148:151]\n"
"v_cvt_pk_bf16_f32 v33, v34, v35\n"
"v_cvt_pk_bf16_f32 v40, v40, v41\n"
"v_mfma_f32_16x16x32_bf16 v[152:155], a[208:211], a[152:155], v[228:231]\n"
"v_permlane16_swap_b32 v8, v10\n"
"v_mfma_f32_16x16x32_bf16 v[152:155], a[212:215], a[156:159], v[152:155]\n"
"ds_read_b128 a[172:175], v251 offset:0\n"
"ds_read_b64_tr_b16 a[240:241], v244 offset:17408\n"
"v_mfma_f32_16x16x32_bf16 v[156:159], a[216:219], a[152:155], v[228:231]\n"
"v_cvt_pk_bf16_f32 v89, v90, v91\n"
"v_cvt_pk_bf16_f32 v67, v70, v71\n"
"v_mfma_f32_16x16x32_bf16 v[156:159], a[220:223], a[156:159], v[156:159]\n"
"s_waitcnt lgkmcnt(4)\n"
"buffer_load_dwordx4 v227, s[28:31], s40 offen offset:3072 lds\n"
"v_mfma_f32_16x16x32_bf16 v[172:175], a[216:219], a[160:163], v[228:231]\n"
"s_cmp_eq_u32 s47, 8\n"
"v_cvt_pk_bf16_f32 v81, v82, v83\n"
"v_accvgpr_write_b32 a56, v8\n"
"v_mfma_f32_16x16x32_bf16 v[172:175], a[220:223], a[164:167], v[172:175]\n"
"s_cselect_b32 s41, s52, s58\n"
"v_cvt_pk_bf16_f32 v72, v72, v73\n"
"v_cvt_pk_bf16_f32 v35, v38, v39\n"
"v_mfma_f32_16x16x32_bf16 v[168:171], a[208:211], a[160:163], v[228:231]\n"
"s_add_u32 s48, s48, s41\n"
"ds_read_b64_tr_b16 a[228:229], v244 offset:24576\n"
"ds_read_b64_tr_b16 a[234:235], v245 offset:16896\n"
"v_mfma_f32_16x16x32_bf16 v[168:171], a[212:215], a[164:167], v[168:171]\n"
"s_sub_u32 s41, s48, 5242880\n"
"ds_read_b128 a[180:183], v251 offset:2048\n"
"v_cvt_pk_bf16_f32 v18, v20, v21\n"
"v_mfma_f32_16x16x32_bf16 v[164:167], a[200:203], a[160:163], v[228:231]\n"
"s_min_u32 s48, s48, s41\n"
"ds_read_b64_tr_b16 a[232:233], v245 offset:16384\n"
"v_cvt_pk_bf16_f32 v41, v42, v43\n"
"v_mfma_f32_16x16x32_bf16 v[164:167], a[204:207], a[164:167], v[164:167]\n"
"ds_read_b64_tr_b16 a[248:249], v245 offset:17408\n"
"v_cvt_pk_bf16_f32 v58, v60, v61\n"
"v_mfma_f32_16x16x32_bf16 v[160:163], a[192:195], a[160:163], v[228:231]\n"
"s_mov_b32 m0, s50\n"
"ds_read_b64_tr_b16 a[226:227], v244 offset:16896\n"
"v_cvt_pk_bf16_f32 v34, v36, v37\n"
"v_mfma_f32_16x16x32_bf16 v[160:163], a[196:199], a[164:167], v[160:163]\n"
"buffer_load_dwordx4 v224, s[28:31], s48 offen lds\n"
"v_mfma_f32_16x16x32_bf16 v[176:179], a[192:195], a[168:171], v[228:231]\n"
"s_waitcnt lgkmcnt(6)\n"
"v_cvt_pk_bf16_f32 v59, v62, v63\n"
"v_cvt_pk_bf16_f32 v26, v28, v29\n"
"v_mfma_f32_16x16x32_bf16 v[176:179], a[196:199], a[172:175], v[176:179]\n"
"v_permlane16_swap_b32 v0, v2\n"
"v_mfma_f32_16x16x32_bf16 v[180:183], a[200:203], a[168:171], v[228:231]\n"
"v_lshl_add_u32 v100, v102, 3, v100\n"
"v_xor_b32_e64 v102, s42, 1\n"
"v_mfma_f32_16x16x32_bf16 v[180:183], a[204:207], a[172:175], v[180:183]\n"
"buffer_load_dwordx4 v225, s[28:31], s48 offen offset:1024 lds\n"
"v_mfma_f32_16x16x32_bf16 v[184:187], a[208:211], a[168:171], v[228:231]\n"
"v_permlane16_swap_b32 v1, v3\n"
"v_mfma_f32_16x16x32_bf16 v[184:187], a[212:215], a[172:175], v[184:187]\n"
"v_accvgpr_write_b32 a48, v0\n"
"v_accvgpr_write_b32 a49, v1\n"
"v_mfma_f32_16x16x32_bf16 v[188:191], a[216:219], a[168:171], v[228:231]\n"
"ds_read_b64_tr_b16 a[230:231], v244 offset:25088\n"
"v_cvt_pk_bf16_f32 v107, v110, v111\n"
"v_mfma_f32_16x16x32_bf16 v[188:191], a[220:223], a[172:175], v[188:191]\n"
"v_cvt_pk_bf16_f32 v82, v84, v85\n"
"v_cvt_pk_bf16_f32 v83, v86, v87\n"
"v_mfma_f32_16x16x32_bf16 v[204:207], a[216:219], a[176:179], v[228:231]\n"
"s_waitcnt lgkmcnt(1)\n"
"ds_read_b64_tr_b16 a[242:243], v244 offset:17920\n"
"v_cvt_pk_bf16_f32 v73, v74, v75\n"
"v_mfma_f32_16x16x32_bf16 v[204:207], a[220:223], a[180:183], v[204:207]\n"
"buffer_load_dwordx4 v226, s[28:31], s48 offen offset:2048 lds\n"
"v_mfma_f32_16x16x32_bf16 v[200:203], a[208:211], a[176:179], v[228:231]\n"
"v_permlane16_swap_b32 v24, v26\n"
"v_mfma_f32_16x16x32_bf16 v[200:203], a[212:215], a[180:183], v[200:203]\n"
"ds_read_b64_tr_b16 a[246:247], v244 offset:26112\n"
"v_accvgpr_write_b32 a50, v2\n"
"v_mfma_f32_16x16x32_bf16 v[196:199], a[200:203], a[176:179], v[228:231]\n"
"v_permlane16_swap_b32 v57, v59\n"
"v_mfma_f32_16x16x32_bf16 v[196:199], a[204:207], a[180:183], v[196:199]\n"
"ds_read_b64_tr_b16 a[244:245], v244 offset:25600\n"
"v_cvt_pk_bf16_f32 v90, v92, v93\n"
"v_mfma_f32_16x16x32_bf16 v[192:195], a[192:195], a[176:179], v[228:231]\n"
"ds_read_b64_tr_b16 a[224:225], v244 offset:16384\n"
"v_accvgpr_write_b32 a74, v26\n"
"v_mfma_f32_16x16x32_bf16 v[192:195], a[196:199], a[180:183], v[192:195]\n"
"buffer_load_dwordx4 v227, s[28:31], s48 offen offset:3072 lds\n"
"v_mfma_f32_16x16x32_bf16 v[208:211], a[192:195], a[184:187], v[228:231]\n"
"s_xor_b32 s50, s50, s74\n"
"v_cvt_pk_bf16_f32 v75, v78, v79\n"
"v_cvt_pk_bf16_f32 v27, v30, v31\n"
"v_mfma_f32_16x16x32_bf16 v[208:211], a[196:199], a[188:191], v[208:211]\n"
"v_cvt_pk_bf16_f32 v91, v94, v95\n"
"v_cvt_pk_bf16_f32 v19, v22, v23\n"
"v_mfma_f32_16x16x32_bf16 v[212:215], a[200:203], a[184:187], v[228:231]\n"
"v_lshl_add_u32 v100, v102, 10, v100\n"
"v_add_u32 v100, 94208, v100\n"
"v_mfma_f32_16x16x32_bf16 v[212:215], a[204:207], a[188:191], v[212:215]\n"
"ds_write2st64_b64 v100, v[96:97], v[98:99] offset0:120 offset1:121\n"
"v_accvgpr_write_b32 a51, v3\n"
"v_accvgpr_write_b32 a58, v10\n"
"v_permlane16_swap_b32 v25, v27\n"
"v_mfma_f32_16x16x32_bf16 v[216:219], a[208:211], a[184:187], v[228:231]\n"
"v_permlane16_swap_b32 v56, v58\n"
"v_mfma_f32_16x16x32_bf16 v[216:219], a[212:215], a[188:191], v[216:219]\n"
"ds_write2st64_b64 v100, v[88:89], v[90:91] offset0:96 offset1:97\n"
"v_permlane16_swap_b32 v32, v34\n"
"v_and_b32 v102, 0xf0, v242\n"
"v_mfma_f32_16x16x32_bf16 v[220:223], a[216:219], a[184:187], v[228:231]\n"
"v_accvgpr_write_b32 a73, v25\n"
"v_cvt_pk_bf16_f32 v43, v46, v47\n"
"v_mfma_f32_16x16x32_bf16 v[220:223], a[220:223], a[188:191], v[220:223]\n"
"s_waitcnt lgkmcnt(2)\n"
"ds_write2st64_b64 v100, v[80:81], v[82:83] offset0:88 offset1:89\n"
"v_cvt_pk_bf16_f32 v66, v68, v69\n"
"v_cvt_pk_bf16_f32 v51, v54, v55\n"
"v_permlane16_swap_b32 v17, v19\n"
"v_mfma_f32_16x16x32_bf16 v[0:3], a[224:227], a[136:139], v[228:231]\n"
"v_accvgpr_write_b32 a59, v11\n"
"v_cvt_pk_bf16_f32 v42, v44, v45\n"
"v_mfma_f32_16x16x32_bf16 v[0:3], a[228:231], a[140:143], v[0:3]\n"
"ds_write2st64_b64 v100, v[104:105], v[106:107] offset0:128 offset1:129\n"
"v_cvt_pk_bf16_f32 v74, v76, v77\n"
"v_accvgpr_write_b32 a67, v19\n"
"v_accvgpr_write_b32 a72, v24\n"
"v_mfma_f32_16x16x32_bf16 v[4:7], a[232:235], a[136:139], v[228:231]\n"
"s_waitcnt vmcnt(8)\n"
"v_cvt_pk_bf16_f32 v50, v52, v53\n"
"v_accvgpr_write_b32 a75, v27\n"
"v_mfma_f32_16x16x32_bf16 v[4:7], a[236:239], a[140:143], v[4:7]\n"
"s_barrier\n"
"ds_write2st64_b64 v100, v[64:65], v[66:67] offset0:0 offset1:1\n"
"v_permlane16_swap_b32 v16, v18\n"
"v_mfma_f32_16x16x32_bf16 v[8:11], a[240:243], a[136:139], v[228:231]\n"
"s_mov_b32 m0, s51\n"
"v_permlane16_swap_b32 v41, v43\n"
"v_mfma_f32_16x16x32_bf16 v[8:11], a[244:247], a[140:143], v[8:11]\n"
"buffer_load_dwordx4 v248, s[24:27], s49 offen lds\n"
"v_mfma_f32_16x16x32_bf16 v[12:15], a[248:251], a[136:139], v[228:231]\n"
"v_permlane16_swap_b32 v40, v42\n"
"v_mfma_f32_16x16x32_bf16 v[12:15], a[252:255], a[140:143], v[12:15]\n"
"v_accvgpr_write_b32 a65, v17\n"
"v_accvgpr_write_b32 a91, v43\n"
"v_mfma_f32_16x16x32_bf16 v[28:31], a[248:251], a[144:147], v[228:231]\n"
"v_accvgpr_write_b32 a66, v18\n"
"v_accvgpr_write_b32 a89, v41\n"
"v_mfma_f32_16x16x32_bf16 v[28:31], a[252:255], a[148:151], v[28:31]\n"
"ds_write2st64_b64 v100, v[72:73], v[74:75] offset0:64 offset1:65\n"
"v_accvgpr_write_b32 a64, v16\n"
"v_lshl_add_u32 v101, v101, 8, v102\n"
"v_mfma_f32_16x16x32_bf16 v[24:27], a[240:243], a[144:147], v[228:231]\n"
"v_accvgpr_write_b32 a80, v32\n"
"v_accvgpr_write_b32 a88, v40\n"
"v_mfma_f32_16x16x32_bf16 v[24:27], a[244:247], a[148:151], v[24:27]\n"
"buffer_load_dwordx4 v249, s[24:27], s49 offen offset:1024 lds\n"
"v_mfma_f32_16x16x32_bf16 v[20:23], a[232:235], a[144:147], v[228:231]\n"
"v_accvgpr_write_b32 a82, v34\n"
"v_xor_b32 v247, s74, v247\n"
"v_mfma_f32_16x16x32_bf16 v[20:23], a[236:239], a[148:151], v[20:23]\n"
"v_permlane16_swap_b32 v33, v35\n"
"v_mfma_f32_16x16x32_bf16 v[16:19], a[224:227], a[144:147], v[228:231]\n"
"v_accvgpr_write_b32 a81, v33\n"
"v_xor_b32 v245, s74, v245\n"
"v_mfma_f32_16x16x32_bf16 v[16:19], a[228:231], a[148:151], v[16:19]\n"
"ds_write_b128 v243, v[56:59] offset:6144\n"
"v_permlane16_swap_b32 v48, v50\n"
"v_accvgpr_write_b32 a83, v35\n"
"v_mfma_f32_16x16x32_bf16 v[32:35], a[224:227], a[152:155], v[228:231]\n"
"v_accvgpr_write_b32 a90, v42\n"
"v_xor_b32 v244, s74, v244\n"
"v_mfma_f32_16x16x32_bf16 v[32:35], a[228:231], a[156:159], v[32:35]\n"
"buffer_load_dwordx4 v254, s[24:27], s49 offen offset:2048 lds\n"
"v_mfma_f32_16x16x32_bf16 v[36:39], a[232:235], a[152:155], v[228:231]\n"
"s_waitcnt vmcnt(63) lgkmcnt(1)\n"
"v_permlane16_swap_b32 v49, v51\n"
"v_mfma_f32_16x16x32_bf16 v[36:39], a[236:239], a[156:159], v[36:39]\n"
"s_barrier\n"
"ds_write_b128 v243, v[48:51] offset:4096\n"
"v_mfma_f32_16x16x32_bf16 v[40:43], a[240:243], a[152:155], v[228:231]\n"
"v_xor_b32 v246, s74, v246\n"
"v_bfe_u32 v102, v242, 8, 1\n"
"v_mfma_f32_16x16x32_bf16 v[40:43], a[244:247], a[156:159], v[40:43]\n"
"ds_read_b128 a[136:139], v246 offset:0\n"
"v_lshl_add_u32 v101, v102, 9, v101\n"
"v_mfma_f32_16x16x32_bf16 v[44:47], a[248:251], a[152:155], v[228:231]\n"
"ds_read_b64_tr_b16 a[218:219], v245 offset:1536\n"
"ds_read_b128 a[144:147], v246 offset:2048\n"
"v_mfma_f32_16x16x32_bf16 v[44:47], a[252:255], a[156:159], v[44:47]\n"
"buffer_load_dwordx4 v255, s[24:27], s49 offen offset:3072 lds\n"
"v_mfma_f32_16x16x32_bf16 v[60:63], a[248:251], a[160:163], v[228:231]\n"
"v_xor_b32_e64 v102, s42, 0\n"
"v_lshl_add_u32 v101, v102, 10, v101\n"
"v_mfma_f32_16x16x32_bf16 v[60:63], a[252:255], a[164:167], v[60:63]\n"
"ds_read_b64_tr_b16 a[192:193], v244 offset:0\n"
"ds_read_b64_tr_b16 a[220:221], v245 offset:9216\n"
"v_mfma_f32_16x16x32_bf16 v[56:59], a[240:243], a[160:163], v[228:231]\n"
"ds_read_b128 a[148:151], v247 offset:2048\n"
"v_add_u32 v101, 94208, v101\n"
"v_mfma_f32_16x16x32_bf16 v[56:59], a[244:247], a[164:167], v[56:59]\n"
"s_add_u32 s40, s49, 40960\n"
"ds_read_b128 a[68:71], v101 offset:45056\n"
"v_xor_b32 v253, s74, v253\n"
"v_mfma_f32_16x16x32_bf16 v[52:55], a[232:235], a[160:163], v[228:231]\n"
"s_add_u32 m0, s51, 4096\n"
"ds_read_b64_tr_b16 a[198:199], v244 offset:8704\n"
"ds_read_b128 a[84:87], v101 offset:61440\n"
"v_mfma_f32_16x16x32_bf16 v[52:55], a[236:239], a[164:167], v[52:55]\n"
"buffer_load_dwordx4 v248, s[24:27], s40 offen lds\n"
"v_mfma_f32_16x16x32_bf16 v[48:51], a[224:227], a[160:163], v[228:231]\n"
"ds_read_b64_tr_b16 a[194:195], v244 offset:512\n"
"ds_read_b128 a[156:159], v247 offset:4096\n"
"v_mfma_f32_16x16x32_bf16 v[48:51], a[228:231], a[164:167], v[48:51]\n"
"v_xor_b32 v251, s74, v251\n"
"v_xor_b32 v252, s74, v252\n"
"v_mfma_f32_16x16x32_bf16 v[64:67], a[224:227], a[168:171], v[228:231]\n"
"ds_read_b128 a[140:143], v247 offset:0\n"
"ds_read_b128 a[52:55], v101 offset:0\n"
"v_mfma_f32_16x16x32_bf16 v[64:67], a[228:231], a[172:175], v[64:67]\n"
"ds_read_b128 a[60:63], v101 offset:32768\n"
"v_add_u32 v102, s75, v101\n"
"v_mfma_f32_16x16x32_bf16 v[68:71], a[232:235], a[168:171], v[228:231]\n"
"ds_read_b64_tr_b16 a[200:201], v245 offset:0\n"
"ds_read_b128 a[92:95], v102 offset:0\n"
"v_mfma_f32_16x16x32_bf16 v[68:71], a[236:239], a[172:175], v[68:71]\n"
"buffer_load_dwordx4 v249, s[24:27], s40 offen offset:1024 lds\n"
"v_mfma_f32_16x16x32_bf16 v[72:75], a[240:243], a[168:171], v[228:231]\n"
"ds_read_b64_tr_b16 a[202:203], v245 offset:512\n"
"ds_read_b128 a[76:79], v101 offset:49152\n"
"v_mfma_f32_16x16x32_bf16 v[72:75], a[244:247], a[172:175], v[72:75]\n"
"ds_read_b128 a[152:155], v246 offset:4096\n"
"v_mfma_f32_16x16x32_bf16 v[76:79], a[248:251], a[168:171], v[228:231]\n"
"ds_read_b64_tr_b16 a[206:207], v245 offset:8704\n"
"ds_read_b128 a[160:163], v252 offset:0\n"
"v_mfma_f32_16x16x32_bf16 v[76:79], a[252:255], a[172:175], v[76:79]\n"
"ds_read_b64_tr_b16 a[208:209], v244 offset:1024\n"
"ds_read_b64_tr_b16 a[214:215], v244 offset:9728\n"
"v_mfma_f32_16x16x32_bf16 v[92:95], a[248:251], a[176:179], v[228:231]\n"
"ds_read_b64_tr_b16 a[216:217], v245 offset:1024\n"
"ds_read_b128 a[164:167], v253 offset:0\n"
"v_mfma_f32_16x16x32_bf16 v[92:95], a[252:255], a[180:183], v[92:95]\n"
"buffer_load_dwordx4 v254, s[24:27], s40 offen offset:2048 lds\n"
"v_mfma_f32_16x16x32_bf16 v[88:91], a[240:243], a[176:179], v[228:231]\n"
"s_add_u32 s49, s49, 128\n"
"v_xor_b32 v250, s74, v250\n"
"v_mfma_f32_16x16x32_bf16 v[88:91], a[244:247], a[180:183], v[88:91]\n"
"s_sub_u32 s41, s49, 1280\n"
"ds_read_b64_tr_b16 a[222:223], v245 offset:9728\n"
"ds_read_b128 a[168:171], v250 offset:0\n"
"v_mfma_f32_16x16x32_bf16 v[84:87], a[232:235], a[176:179], v[228:231]\n"
"s_min_u32 s49, s49, s41\n"
"ds_read_b64_tr_b16 a[204:205], v245 offset:8192\n"
"ds_read_b128 a[172:175], v251 offset:0\n"
"v_mfma_f32_16x16x32_bf16 v[84:87], a[236:239], a[180:183], v[84:87]\n"
"s_xor_b32 s51, s51, s74\n"
"ds_read_b64_tr_b16 a[196:197], v244 offset:8192\n"
"ds_read_b64_tr_b16 a[212:213], v244 offset:9216\n"
"v_mfma_f32_16x16x32_bf16 v[80:83], a[224:227], a[176:179], v[228:231]\n"
"ds_read_b64_tr_b16 a[210:211], v244 offset:1536\n"
"ds_read_b128 a[176:179], v250 offset:2048\n"
"v_mfma_f32_16x16x32_bf16 v[80:83], a[228:231], a[180:183], v[80:83]\n"
"s_sub_u32 s78, s78, s65\n"
"s_mov_b32 s65, 0\n"
"ds_read_b128 a[180:183], v251 offset:2048\n"
"v_mfma_f32_16x16x32_bf16 v[96:99], a[224:227], a[184:187], v[228:231]\n"
"s_cmp_gt_u32 s78, 22000\n"
"v_mfma_f32_16x16x32_bf16 v[96:99], a[228:231], a[188:191], v[96:99]\n"
"s_cselect_b32 s68, 1504, 0\n"
"s_cmp_eq_u32 s42, 0\n"
"v_mfma_f32_16x16x32_bf16 v[100:103], a[232:235], a[184:187], v[228:231]\n"
"s_cselect_b32 s64, s68, 0\n"
"s_add_u32 s80, s8, s63\n"
"v_mfma_f32_16x16x32_bf16 v[100:103], a[236:239], a[188:191], v[100:103]\n"
"s_addc_u32 s81, s9, 0\n"
"s_mov_b32 s82, s68\n"
"v_mfma_f32_16x16x32_bf16 v[104:107], a[240:243], a[184:187], v[228:231]\n"
"s_mov_b64 vcc, s[64:65]\n"
"v_mfma_f32_16x16x32_bf16 v[104:107], a[244:247], a[188:191], v[104:107]\n"
"v_mfma_f32_16x16x32_bf16 v[108:111], a[248:251], a[184:187], v[228:231]\n"
"s_add_u32 s47, s47, 1\n"
"ds_read_b128 a[184:187], v250 offset:4096\n"
"v_mfma_f32_16x16x32_bf16 v[108:111], a[252:255], a[188:191], v[108:111]\n"
"s_waitcnt lgkmcnt(1)\n"
"ds_read_b128 a[188:191], v251 offset:4096\n"
"s_mov_b32 s62, 18\n"
"s_branch LPE_%=\n"
"LPT2_%=:\n"
"v_mfma_f32_16x16x32_bf16 v[112:115], a[192:195], a[136:139], v[112:115]\n"
"s_add_u32 s78, s32, s96\ns_addc_u32 s79, s33, 0\n"
"s_mov_b64 s[80:81], 0\n"
"s_mov_b32 s82, 0\n"
"v_mfma_f32_16x16x32_bf16 v[112:115], a[196:199], a[140:143], v[112:115]\n"
"v_xor_b32 v248, 64, v240\n"
"v_mfma_f32_16x16x32_bf16 v[116:119], a[200:203], a[136:139], v[116:119]\n"
"s_waitcnt vmcnt(11)\n"
"s_load_dword s80, s[78:79], 0\n"
"v_mfma_f32_16x16x32_bf16 v[116:119], a[204:207], a[140:143], v[116:119]\n"
"s_barrier\n"
"v_mfma_f32_16x16x32_bf16 v[120:123], a[208:211], a[136:139], v[120:123]\n"
"s_add_u32 s40, s48, 256\n"
"s_xor_b32 m0, s50, 0xc000\n"
"ds_read_b64_tr_b16 a[238:239], v245 offset:25088\n"
"v_mfma_f32_16x16x32_bf16 v[120:123], a[212:215], a[140:143], v[120:123]\n"
"buffer_load_dwordx4 v224, s[28:31], s40 offen lds\n"
"v_mfma_f32_16x16x32_bf16 v[124:127], a[216:219], a[136:139], v[124:127]\n"
"v_mfma_f32_16x16x32_bf16 v[124:127], a[220:223], a[140:143], v[124:127]\n"
"v_mfma_f32_16x16x32_bf16 v[140:143], a[216:219], a[144:147], v[140:143]\n"
"ds_read_b64_tr_b16 a[244:245], v244 offset:25600\n"
"v_mfma_f32_16x16x32_bf16 v[140:143], a[220:223], a[148:151], v[140:143]\n"
"v_mfma_f32_16x16x32_bf16 v[136:139], a[208:211], a[144:147], v[136:139]\n"
"ds_read_b64_tr_b16 a[250:251], v245 offset:17920\n"
"v_bfe_i32 v241, v242, 4, 1\n"
"v_mfma_f32_16x16x32_bf16 v[136:139], a[212:215], a[148:151], v[136:139]\n"
"buffer_load_dwordx4 v225, s[28:31], s40 offen offset:1024 lds\n"
"v_mfma_f32_16x16x32_bf16 v[132:135], a[200:203], a[144:147], v[132:135]\n"
"s_add_u32 s82, s82, s80\n"
"v_mfma_f32_16x16x32_bf16 v[132:135], a[204:207], a[148:151], v[132:135]\n"
"s_add_u32 s82, s82, s80\n"
"v_mfma_f32_16x16x32_bf16 v[128:131], a[192:195], a[144:147], v[128:131]\n"
"s_add_u32 s82, s82, s80\n"
"ds_read_b64_tr_b16 a[230:231], v244 offset:25088\n"
"v_mfma_f32_16x16x32_bf16 v[128:131], a[196:199], a[148:151], v[128:131]\n"
"s_add_u32 s82, s82, s80\n"
"v_bfe_u32 v228, v242, 8, 2\n"
"v_mfma_f32_16x16x32_bf16 v[144:147], a[192:195], a[152:155], v[144:147]\n"
"s_add_u32 s82, s82, s80\n"
"ds_read_b64_tr_b16 a[242:243], v244 offset:17920\n"
"v_mfma_f32_16x16x32_bf16 v[144:147], a[196:199], a[156:159], v[144:147]\n"
"s_add_u32 s82, s82, s80\n"
"v_mfma_f32_16x16x32_bf16 v[148:151], a[200:203], a[152:155], v[148:151]\n"
"s_add_u32 s82, s82, s80\n"
"ds_read_b64_tr_b16 a[228:229], v244 offset:24576\n"
"v_mfma_f32_16x16x32_bf16 v[148:151], a[204:207], a[156:159], v[148:151]\n"
"s_add_u32 s82, s82, s80\n"
"buffer_load_dwordx4 v226, s[28:31], s40 offen offset:2048 lds\n"
"v_mfma_f32_16x16x32_bf16 v[152:155], a[208:211], a[152:155], v[152:155]\n"
"s_add_u32 s82, s82, s80\n"
"v_mfma_f32_16x16x32_bf16 v[152:155], a[212:215], a[156:159], v[152:155]\n"
"s_add_u32 s82, s82, s80\n"
"v_and_b32 v229, 0x60, v242\n"
"v_mfma_f32_16x16x32_bf16 v[156:159], a[216:219], a[152:155], v[156:159]\n"
"s_add_u32 s82, s82, s80\n"
"ds_read_b64_tr_b16 a[232:233], v245 offset:16384\n"
"v_mfma_f32_16x16x32_bf16 v[156:159], a[220:223], a[156:159], v[156:159]\n"
"s_add_u32 s82, s82, s80\n"
"v_mfma_f32_16x16x32_bf16 v[172:175], a[216:219], a[160:163], v[172:175]\n"
"s_add_u32 s82, s82, s80\n"
"ds_read_b64_tr_b16 a[234:235], v245 offset:16896\n"
"v_lshlrev_b32 v228, 4, v228\n"
"v_mfma_f32_16x16x32_bf16 v[172:175], a[220:223], a[164:167], v[172:175]\n"
"s_add_u32 s82, s82, s80\n"
"buffer_load_dwordx4 v227, s[28:31], s40 offen offset:3072 lds\n"
"v_mfma_f32_16x16x32_bf16 v[168:171], a[208:211], a[160:163], v[168:171]\n"
"s_add_u32 s82, s82, s80\n"
"s_mov_b32 s58, 122880\n"
"v_mfma_f32_16x16x32_bf16 v[168:171], a[212:215], a[164:167], v[168:171]\n"
"s_add_u32 s82, s82, s80\n"
"s_mov_b32 s47, 0\n"
"v_mfma_f32_16x16x32_bf16 v[164:167], a[200:203], a[160:163], v[164:167]\n"
"s_add_u32 s82, s82, s80\n"
"s_sub_u32 s52, s54, 14336\n"
"ds_read_b64_tr_b16 a[246:247], v244 offset:26112\n"
"v_mfma_f32_16x16x32_bf16 v[164:167], a[204:207], a[164:167], v[164:167]\n"
"s_load_dword s81, s[78:79], 0x2000\n"
"s_mov_b32 s53, 14336\n"
"v_mfma_f32_16x16x32_bf16 v[160:163], a[192:195], a[160:163], v[160:163]\n"
"s_mov_b32 m0, s44\n"
"ds_read_b64_tr_b16 a[240:241], v244 offset:17408\n"
"v_mfma_f32_16x16x32_bf16 v[160:163], a[196:199], a[164:167], v[160:163]\n"
"buffer_load_dwordx4 v240, s[16:19], s56 offen nt lds\n"
"v_mfma_f32_16x16x32_bf16 v[176:179], a[192:195], a[168:171], v[176:179]\n"
"v_mfma_f32_16x16x32_bf16 v[176:179], a[196:199], a[172:175], v[176:179]\n"
"v_mfma_f32_16x16x32_bf16 v[180:183], a[200:203], a[168:171], v[180:183]\n"
"ds_read_b64_tr_b16 a[252:253], v245 offset:25600\n"
"v_mfma_f32_16x16x32_bf16 v[180:183], a[204:207], a[172:175], v[180:183]\n"
"v_mfma_f32_16x16x32_bf16 v[184:187], a[208:211], a[168:171], v[184:187]\n"
"ds_read_b64_tr_b16 a[236:237], v245 offset:24576\n"
"v_xor_b32 v228, v228, v229\n"
"v_mfma_f32_16x16x32_bf16 v[184:187], a[212:215], a[172:175], v[184:187]\n"
"v_mfma_f32_16x16x32_bf16 v[188:191], a[216:219], a[168:171], v[188:191]\n"
"s_add_u32 s40, s56, s52\n"
"ds_read_b64_tr_b16 a[226:227], v244 offset:16896\n"
"v_mfma_f32_16x16x32_bf16 v[188:191], a[220:223], a[172:175], v[188:191]\n"
"s_sub_u32 s82, s82, s81\n"
"buffer_load_dwordx4 v248, s[16:19], s40 offen offset:1024 nt lds\n"
"v_mfma_f32_16x16x32_bf16 v[204:207], a[216:219], a[176:179], v[204:207]\n"
"s_sub_u32 s82, s82, s81\n"
"v_mfma_f32_16x16x32_bf16 v[204:207], a[220:223], a[180:183], v[204:207]\n"
"s_sub_u32 s82, s82, s81\n"
"v_mfma_f32_16x16x32_bf16 v[200:203], a[208:211], a[176:179], v[200:203]\n"
"s_sub_u32 s82, s82, s81\n"
"ds_read_b64_tr_b16 a[248:249], v245 offset:17408\n"
"v_and_b32 v229, 0x30, v242\n"
"v_mfma_f32_16x16x32_bf16 v[200:203], a[212:215], a[180:183], v[200:203]\n"
"s_sub_u32 s82, s82, s81\n"
"v_mfma_f32_16x16x32_bf16 v[196:199], a[200:203], a[176:179], v[196:199]\n"
"s_sub_u32 s82, s82, s81\n"
"s_add_u32 s40, s56, s53\n"
"ds_read_b64_tr_b16 a[224:225], v244 offset:16384\n"
"v_mfma_f32_16x16x32_bf16 v[196:199], a[204:207], a[180:183], v[196:199]\n"
"s_sub_u32 s82, s82, s81\n"
"buffer_load_dwordx4 v240, s[16:19], s40 offen offset:2048 nt lds\n"
"v_mfma_f32_16x16x32_bf16 v[192:195], a[192:195], a[176:179], v[192:195]\n"
"s_sub_u32 s82, s82, s81\n"
"v_lshl_add_u32 v228, v229, 3, v228\n"
"v_mfma_f32_16x16x32_bf16 v[192:195], a[196:199], a[180:183], v[192:195]\n"
"s_waitcnt lgkmcnt(2)\n"
"s_sub_u32 s82, s82, s81\n"
"v_xor_b32 v244, s74, v244\n"
"v_mfma_f32_16x16x32_bf16 v[208:211], a[192:195], a[184:187], v[208:211]\n"
"s_sub_u32 s82, s82, s81\n"
"v_mfma_f32_16x16x32_bf16 v[208:211], a[196:199], a[188:191], v[208:211]\n"
"s_sub_u32 s82, s82, s81\n"
"ds_read_b64_tr_b16 a[254:255], v245 offset:26112\n"
"v_mfma_f32_16x16x32_bf16 v[212:215], a[200:203], a[184:187], v[212:215]\n"
"s_sub_u32 s82, s82, s81\n"
"s_add_u32 s40, s56, s54\n"
"v_mfma_f32_16x16x32_bf16 v[212:215], a[204:207], a[188:191], v[212:215]\n"
"s_sub_u32 s82, s82, s81\n"
"buffer_load_dwordx4 v248, s[16:19], s40 offen offset:3072 nt lds\n"
"v_mfma_f32_16x16x32_bf16 v[216:219], a[208:211], a[184:187], v[216:219]\n"
"s_sub_u32 s82, s82, s81\n"
"v_and_b32 v229, 0xc0, v242\n"
"v_xor_b32 v251, s74, v251\n"
"v_mfma_f32_16x16x32_bf16 v[216:219], a[212:215], a[188:191], v[216:219]\n"
"s_sub_u32 s82, s82, s81\n"
"v_mfma_f32_16x16x32_bf16 v[220:223], a[216:219], a[184:187], v[220:223]\n"
"s_sub_u32 s82, s82, s81\n"
"v_mfma_f32_16x16x32_bf16 v[220:223], a[220:223], a[188:191], v[220:223]\n"
"s_waitcnt lgkmcnt(1)\n"
"s_sub_u32 s82, s82, s81\n"
"v_mfma_f32_16x16x32_bf16 v[0:3], a[224:227], a[136:139], v[0:3]\n"
"v_mfma_f32_16x16x32_bf16 v[0:3], a[228:231], a[140:143], v[0:3]\n"
"v_lshl_add_u32 v228, v229, 4, v228\n"
"v_xor_b32 v245, s74, v245\n"
"v_mfma_f32_16x16x32_bf16 v[4:7], a[232:235], a[136:139], v[4:7]\n"
"s_waitcnt vmcnt(8) lgkmcnt(0)\n"
"v_mfma_f32_16x16x32_bf16 v[4:7], a[236:239], a[140:143], v[4:7]\n"
"s_barrier\n"
"v_mfma_f32_16x16x32_bf16 v[8:11], a[240:243], a[136:139], v[8:11]\n"
"s_add_u32 m0, s44, 65536\n"
"ds_read_b64_tr_b16 a[222:223], v245 offset:9728\n"
"v_mfma_f32_16x16x32_bf16 v[8:11], a[244:247], a[140:143], v[8:11]\n"
"buffer_load_dwordx4 v240, s[20:23], s56 offen nt lds\n"
"v_mfma_f32_16x16x32_bf16 v[12:15], a[248:251], a[136:139], v[12:15]\n"
"s_lshr_b32 s82, s82, 31\n"
"v_mfma_f32_16x16x32_bf16 v[12:15], a[252:255], a[140:143], v[12:15]\n"
"s_add_u32 s82, s82, s60\n"
"v_mfma_f32_16x16x32_bf16 v[28:31], a[248:251], a[144:147], v[28:31]\n"
"s_add_u32 s82, s82, 32\n"
"ds_read_b64_tr_b16 a[216:217], v245 offset:1024\n"
"v_xor_b32 v247, s74, v247\n"
"v_mfma_f32_16x16x32_bf16 v[28:31], a[252:255], a[148:151], v[28:31]\n"
"v_mov_b32 v254, s82\n"
"ds_read_b64_tr_b16 a[196:197], v244 offset:8192\n"
"v_mfma_f32_16x16x32_bf16 v[24:27], a[240:243], a[144:147], v[24:27]\n"
"v_lshlrev_b32_e64 v255, 2, s93\n"
"ds_read_b64_tr_b16 a[198:199], v244 offset:8704\n"
"v_xor_b32 v246, s74, v246\n"
"v_mfma_f32_16x16x32_bf16 v[24:27], a[244:247], a[148:151], v[24:27]\n"
"ds_read_b64_tr_b16 a[214:215], v244 offset:9728\n"
"v_mov_b32 v229, s43\n"
"v_mfma_f32_16x16x32_bf16 v[20:23], a[232:235], a[144:147], v[20:23]\n"
"s_add_u32 s40, s56, s52\n"
"ds_read_b64_tr_b16 a[204:205], v245 offset:8192\n"
"v_mfma_f32_16x16x32_bf16 v[20:23], a[236:239], a[148:151], v[20:23]\n"
"buffer_load_dwordx4 v248, s[20:23], s40 offen offset:1024 nt lds\n"
"v_mfma_f32_16x16x32_bf16 v[16:19], a[224:227], a[144:147], v[16:19]\n"
"v_mfma_f32_16x16x32_bf16 v[16:19], a[228:231], a[148:151], v[16:19]\n"
"s_mov_b64 exec, 1\n"
"global_store_dword v255, v254, s[6:7] offset:256 sc1\n"
"s_mov_b64 exec, -1\n"
"v_mfma_f32_16x16x32_bf16 v[32:35], a[224:227], a[152:155], v[32:35]\n"
"ds_read_b64_tr_b16 a[202:203], v245 offset:512\n"
"v_mfma_f32_16x16x32_bf16 v[32:35], a[228:231], a[156:159], v[32:35]\n"
"v_mfma_f32_16x16x32_bf16 v[36:39], a[232:235], a[152:155], v[36:39]\n"
"s_add_u32 s40, s56, s53\n"
"ds_read_b128 a[140:143], v247 offset:0\n"
"v_mfma_f32_16x16x32_bf16 v[36:39], a[236:239], a[156:159], v[36:39]\n"
"buffer_load_dwordx4 v240, s[20:23], s40 offen offset:2048 nt lds\n"
"v_mfma_f32_16x16x32_bf16 v[40:43], a[240:243], a[152:155], v[40:43]\n"
"v_xor_b32 v250, s74, v250\n"
"v_mfma_f32_16x16x32_bf16 v[40:43], a[244:247], a[156:159], v[40:43]\n"
"v_mfma_f32_16x16x32_bf16 v[44:47], a[248:251], a[152:155], v[44:47]\n"
"ds_read_b64_tr_b16 a[200:201], v245 offset:0\n"
"v_mul_u32_u24 v229, 0x81, v229\n"
"v_mfma_f32_16x16x32_bf16 v[44:47], a[252:255], a[156:159], v[44:47]\n"
"ds_read_b64_tr_b16 a[220:221], v245 offset:9216\n"
"v_mfma_f32_16x16x32_bf16 v[60:63], a[248:251], a[160:163], v[60:63]\n"
"s_add_u32 s40, s56, s54\n"
"ds_read_b64_tr_b16 a[210:211], v244 offset:1536\n"
"v_xor_b32 v253, s74, v253\n"
"v_mfma_f32_16x16x32_bf16 v[60:63], a[252:255], a[164:167], v[60:63]\n"
"buffer_load_dwordx4 v248, s[20:23], s40 offen offset:3072 nt lds\n"
"v_mfma_f32_16x16x32_bf16 v[56:59], a[240:243], a[160:163], v[56:59]\n"
"s_add_u32 s55, s55, s59\n"
"v_mfma_f32_16x16x32_bf16 v[56:59], a[244:247], a[164:167], v[56:59]\n"
"s_or_b32 s55, s55, s58\n"
"v_mfma_f32_16x16x32_bf16 v[52:55], a[232:235], a[160:163], v[52:55]\n"
"s_xor_b32 s56, s55, s57\n"
"ds_read_b128 a[136:139], v246 offset:0\n"
"v_mfma_f32_16x16x32_bf16 v[52:55], a[236:239], a[164:167], v[52:55]\n"
"v_mfma_f32_16x16x32_bf16 v[48:51], a[224:227], a[160:163], v[48:51]\n"
"s_add_u32 m0, s44, 16384\n"
"ds_read_b128 a[156:159], v247 offset:4096\n"
"v_mfma_f32_16x16x32_bf16 v[48:51], a[228:231], a[164:167], v[48:51]\n"
"buffer_load_dwordx4 v240, s[16:19], s56 offen nt lds\n"
"v_mfma_f32_16x16x32_bf16 v[64:67], a[224:227], a[168:171], v[64:67]\n"
"v_mfma_f32_16x16x32_bf16 v[64:67], a[228:231], a[172:175], v[64:67]\n"
"v_mfma_f32_16x16x32_bf16 v[68:71], a[232:235], a[168:171], v[68:71]\n"
"ds_read_b128 a[164:167], v253 offset:0\n"
"v_mfma_f32_16x16x32_bf16 v[68:71], a[236:239], a[172:175], v[68:71]\n"
"v_mfma_f32_16x16x32_bf16 v[72:75], a[240:243], a[168:171], v[72:75]\n"
"ds_read_b64_tr_b16 a[218:219], v245 offset:1536\n"
"v_mfma_f32_16x16x32_bf16 v[72:75], a[244:247], a[172:175], v[72:75]\n"
"ds_read_b64_tr_b16 a[194:195], v244 offset:512\n"
"v_mfma_f32_16x16x32_bf16 v[76:79], a[248:251], a[168:171], v[76:79]\n"
"s_add_u32 s40, s56, s52\n"
"ds_read_b64_tr_b16 a[206:207], v245 offset:8704\n"
"v_mfma_f32_16x16x32_bf16 v[76:79], a[252:255], a[172:175], v[76:79]\n"
"buffer_load_dwordx4 v248, s[16:19], s40 offen offset:1024 nt lds\n"
"v_mfma_f32_16x16x32_bf16 v[92:95], a[248:251], a[176:179], v[92:95]\n"
"v_mfma_f32_16x16x32_bf16 v[92:95], a[252:255], a[180:183], v[92:95]\n"
"v_mfma_f32_16x16x32_bf16 v[88:91], a[240:243], a[176:179], v[88:91]\n"
"ds_read_b64_tr_b16 a[208:209], v244 offset:1024\n"
"v_mfma_f32_16x16x32_bf16 v[88:91], a[244:247], a[180:183], v[88:91]\n"
"ds_read_b64_tr_b16 a[212:213], v244 offset:9216\n"
"v_mfma_f32_16x16x32_bf16 v[84:87], a[232:235], a[176:179], v[84:87]\n"
"s_add_u32 s40, s56, s53\n"
"ds_read_b64_tr_b16 a[192:193], v244 offset:0\n"
"v_mfma_f32_16x16x32_bf16 v[84:87], a[236:239], a[180:183], v[84:87]\n"
"buffer_load_dwordx4 v240, s[16:19], s40 offen offset:2048 nt lds\n"
"v_mfma_f32_16x16x32_bf16 v[80:83], a[224:227], a[176:179], v[80:83]\n"
"v_mfma_f32_16x16x32_bf16 v[80:83], a[228:231], a[180:183], v[80:83]\n"
"v_mfma_f32_16x16x32_bf16 v[96:99], a[224:227], a[184:187], v[96:99]\n"
"ds_read_b128 a[172:175], v251 offset:0\n"
"v_mfma_f32_16x16x32_bf16 v[96:99], a[228:231], a[188:191], v[96:99]\n"
"v_mfma_f32_16x16x32_bf16 v[100:103], a[232:235], a[184:187], v[100:103]\n"
"s_add_u32 s40, s56, s54\n"
"ds_read_b128 a[148:151], v247 offset:2048\n"
"v_mfma_f32_16x16x32_bf16 v[100:103], a[236:239], a[188:191], v[100:103]\n"
"buffer_load_dwordx4 v248, s[16:19], s40 offen offset:3072 nt lds\n"
"v_mfma_f32_16x16x32_bf16 v[104:107], a[240:243], a[184:187], v[104:107]\n"
"v_mfma_f32_16x16x32_bf16 v[104:107], a[244:247], a[188:191], v[104:107]\n"
"v_and_b32 v254, 0x20, v242\n"
"v_and_b32 v241, 0xaaaaaaaa, v241\n"
"v_mfma_f32_16x16x32_bf16 v[108:111], a[248:251], a[184:187], v[108:111]\n"
"ds_read_b128 a[168:171], v250 offset:0\n"
"v_mfma_f32_16x16x32_bf16 v[108:111], a[252:255], a[188:191], v[108:111]\n"
"s_waitcnt lgkmcnt(3)\n"
"v_mfma_f32_16x16x32_bf16 v[112:115], a[192:195], a[136:139], v[112:115]\n"
"ds_read_b128 a[184:187], v250 offset:4096\n"
"v_mfma_f32_16x16x32_bf16 v[112:115], a[196:199], a[140:143], v[112:115]\n"
"v_mfma_f32_16x16x32_bf16 v[116:119], a[200:203], a[136:139], v[116:119]\n"
"s_waitcnt vmcnt(13)\n"
"ds_read_b128 a[180:183], v251 offset:2048\n"
"v_and_b32 v249, 0xf0, v242\n"
"v_mfma_f32_16x16x32_bf16 v[116:119], a[204:207], a[140:143], v[116:119]\n"
"s_barrier\n"
"v_mfma_f32_16x16x32_bf16 v[120:123], a[208:211], a[136:139], v[120:123]\n"
"s_add_u32 m0, s44, 81920\n"
"ds_read_b128 a[176:179], v250 offset:2048\n"
"v_mfma_f32_16x16x32_bf16 v[120:123], a[212:215], a[140:143], v[120:123]\n"
"buffer_load_dwordx4 v240, s[20:23], s56 offen nt lds\n"
"v_mfma_f32_16x16x32_bf16 v[124:127], a[216:219], a[136:139], v[124:127]\n"
"v_bfe_u32 v255, v242, 9, 1\n"
"v_lshl_add_u32 v255, v255, 8, v249\n"
"v_mfma_f32_16x16x32_bf16 v[124:127], a[220:223], a[140:143], v[124:127]\n"
"s_waitcnt lgkmcnt(3)\n"
"v_xor_b32 v252, s74, v252\n"
"v_mfma_f32_16x16x32_bf16 v[188:191], a[216:219], a[168:171], v[188:191]\n"
"ds_read_b128 a[188:191], v251 offset:4096\n"
"v_xor_b32 v228, v228, v229\n"
"v_mfma_f32_16x16x32_bf16 v[188:191], a[220:223], a[172:175], v[188:191]\n"
"v_mfma_f32_16x16x32_bf16 v[184:187], a[208:211], a[168:171], v[184:187]\n"
"ds_read_b128 a[144:147], v246 offset:2048\n"
"v_add_u32 v254, -1, v254\n"
"v_mfma_f32_16x16x32_bf16 v[184:187], a[212:215], a[172:175], v[184:187]\n"
"v_mfma_f32_16x16x32_bf16 v[180:183], a[200:203], a[168:171], v[180:183]\n"
"ds_read_b64_tr_b16 a[234:235], v245 offset:16896\n"
"v_mfma_f32_16x16x32_bf16 v[180:183], a[204:207], a[172:175], v[180:183]\n"
"v_mfma_f32_16x16x32_bf16 v[176:179], a[192:195], a[168:171], v[176:179]\n"
"ds_read_b128 a[160:163], v252 offset:0\n"
"v_mfma_f32_16x16x32_bf16 v[176:179], a[196:199], a[172:175], v[176:179]\n"
"s_waitcnt lgkmcnt(4)\n"
"v_mfma_f32_16x16x32_bf16 v[192:195], a[192:195], a[176:179], v[192:195]\n"
"ds_read_b64_tr_b16 a[230:231], v244 offset:25088\n"
"v_or_b32 v241, 0x44444444, v241\n"
"v_mfma_f32_16x16x32_bf16 v[192:195], a[196:199], a[180:183], v[192:195]\n"
"v_bfe_u32 v249, v242, 8, 1\n"
"v_mfma_f32_16x16x32_bf16 v[196:199], a[200:203], a[176:179], v[196:199]\n"
"s_add_u32 s40, s56, s52\n"
"ds_read_b64_tr_b16 a[246:247], v244 offset:26112\n"
"v_mfma_f32_16x16x32_bf16 v[196:199], a[204:207], a[180:183], v[196:199]\n"
"buffer_load_dwordx4 v248, s[20:23], s40 offen offset:1024 nt lds\n"
"v_mfma_f32_16x16x32_bf16 v[200:203], a[208:211], a[176:179], v[200:203]\n"
"v_xor_b32 v229, 64, v228\n"
"v_mfma_f32_16x16x32_bf16 v[200:203], a[212:215], a[180:183], v[200:203]\n"
"v_mfma_f32_16x16x32_bf16 v[204:207], a[216:219], a[176:179], v[204:207]\n"
"ds_read_b64_tr_b16 a[240:241], v244 offset:17408\n"
"v_lshl_add_u32 v255, v249, 3, v255\n"
"v_mfma_f32_16x16x32_bf16 v[204:207], a[220:223], a[180:183], v[204:207]\n"
"v_xor_b32 v231, 0x2000, v229\n"
"v_xor_b32_e64 v249, s42, 1\n"
"v_mfma_f32_16x16x32_bf16 v[220:223], a[216:219], a[184:187], v[220:223]\n"
"s_waitcnt lgkmcnt(2)\n"
"ds_read_b64_tr_b16 a[232:233], v245 offset:16384\n"
"v_mfma_f32_16x16x32_bf16 v[220:223], a[220:223], a[188:191], v[220:223]\n"
"ds_read_b64_tr_b16 a[226:227], v244 offset:16896\n"
"v_mfma_f32_16x16x32_bf16 v[216:219], a[208:211], a[184:187], v[216:219]\n"
"ds_read_b64_tr_b16 a[224:225], v244 offset:16384\n"
"v_xor_b32 v230, 0x2000, v228\n"
"v_mfma_f32_16x16x32_bf16 v[216:219], a[212:215], a[188:191], v[216:219]\n"
"v_pk_fma_f32 v[176:177], v[176:177], s[70:71], s[70:71] op_sel:[0,0,1] op_sel_hi:[1,0,1]\nv_pk_fma_f32 v[178:179], v[178:179], s[70:71], s[70:71] op_sel:[0,0,1] op_sel_hi:[1,0,1]\nv_pk_fma_f32 v[180:181], v[180:181], s[70:71], s[70:71] op_sel:[0,0,1] op_sel_hi:[1,0,1]\nv_pk_fma_f32 v[182:183], v[182:183], s[70:71], s[70:71] op_sel:[0,0,1] op_sel_hi:[1,0,1]\nv_pk_fma_f32 v[184:185], v[184:185], s[70:71], s[70:71] op_sel:[0,0,1] op_sel_hi:[1,0,1]\nv_pk_fma_f32 v[186:187], v[186:187], s[70:71], s[70:71] op_sel:[0,0,1] op_sel_hi:[1,0,1]\nv_pk_fma_f32 v[188:189], v[188:189], s[70:71], s[70:71] op_sel:[0,0,1] op_sel_hi:[1,0,1]\nv_pk_fma_f32 v[190:191], v[190:191], s[70:71], s[70:71] op_sel:[0,0,1] op_sel_hi:[1,0,1]\nv_pk_fma_f32 v[192:193], v[192:193], s[70:71], s[70:71] op_sel:[0,0,1] op_sel_hi:[1,0,1]\nv_pk_fma_f32 v[194:195], v[194:195], s[70:71], s[70:71] op_sel:[0,0,1] op_sel_hi:[1,0,1]\nv_pk_fma_f32 v[196:197], v[196:197], s[70:71], s[70:71] op_sel:[0,0,1] op_sel_hi:[1,0,1]\nv_pk_fma_f32 v[198:199], v[198:199], s[70:71], s[70:71] op_sel:[0,0,1] op_sel_hi:[1,0,1]\nv_pk_fma_f32 v[200:201], v[200:201], s[70:71], s[70:71] op_sel:[0,0,1] op_sel_hi:[1,0,1]\nv_pk_fma_f32 v[202:203], v[202:203], s[70:71], s[70:71] op_sel:[0,0,1] op_sel_hi:[1,0,1]\nv_pk_fma_f32 v[204:205], v[204:205], s[70:71], s[70:71] op_sel:[0,0,1] op_sel_hi:[1,0,1]\nv_pk_fma_f32 v[206:207], v[206:207], s[70:71], s[70:71] op_sel:[0,0,1] op_sel_hi:[1,0,1]\n"
"v_mfma_f32_16x16x32_bf16 v[212:215], a[200:203], a[184:187], v[212:215]\n"
"ds_read_b64_tr_b16 a[252:253], v245 offset:25600\n"
"v_cvt_pk_bf16_f32 v192, v192, v193\n"
"v_mfma_f32_16x16x32_bf16 v[212:215], a[204:207], a[188:191], v[212:215]\n"
"v_cvt_pk_bf16_f32 v176, v176, v177\n"
"v_cvt_pk_bf16_f32 v193, v194, v195\n"
"v_mfma_f32_16x16x32_bf16 v[208:211], a[192:195], a[184:187], v[208:211]\n"
"s_add_u32 s40, s56, s53\n"
"ds_read_b64_tr_b16 a[248:249], v245 offset:17408\n"
"v_mfma_f32_16x16x32_bf16 v[208:211], a[196:199], a[188:191], v[208:211]\n"
"buffer_load_dwordx4 v240, s[20:23], s40 offen offset:2048 nt lds\n"
"v_mfma_f32_16x16x32_bf16 v[160:163], a[192:195], a[160:163], v[160:163]\n"
"v_cvt_pk_bf16_f32 v177, v178, v179\n"
"v_cvt_pk_bf16_f32 v184, v184, v185\n"
"v_mfma_f32_16x16x32_bf16 v[160:163], a[196:199], a[164:167], v[160:163]\n"
"v_cvt_pk_bf16_f32 v179, v182, v183\n"
"v_cvt_pk_bf16_f32 v185, v186, v187\n"
"v_mfma_f32_16x16x32_bf16 v[164:167], a[200:203], a[160:163], v[164:167]\n"
"ds_read_b64_tr_b16 a[244:245], v244 offset:25600\n"
"v_cvt_pk_bf16_f32 v200, v200, v201\n"
"v_mfma_f32_16x16x32_bf16 v[164:167], a[204:207], a[164:167], v[164:167]\n"
"v_cvt_pk_bf16_f32 v201, v202, v203\n"
"v_ashrrev_i32 v254, 31, v254\n"
"v_mfma_f32_16x16x32_bf16 v[168:171], a[208:211], a[160:163], v[168:171]\n"
"ds_read_b64_tr_b16 a[254:255], v245 offset:26112\n"
"v_cvt_pk_bf16_f32 v194, v196, v197\n"
"v_mfma_f32_16x16x32_bf16 v[168:171], a[212:215], a[164:167], v[168:171]\n"
"v_cvt_pk_bf16_f32 v202, v204, v205\n"
"v_mfma_f32_16x16x32_bf16 v[172:175], a[216:219], a[160:163], v[172:175]\n"
"ds_read_b64_tr_b16 a[242:243], v244 offset:17920\n"
"v_and_b32 v254, 0x3f803f80, v254\n"
"v_mfma_f32_16x16x32_bf16 v[172:175], a[220:223], a[164:167], v[172:175]\n"
"v_bfe_u32 v196, v242, 8, 2\n"
"v_lshl_add_u32 v196, v196, 4, s43\n"
"v_mfma_f32_16x16x32_bf16 v[140:143], a[216:219], a[144:147], v[140:143]\n"
"ds_read_b64_tr_b16 a[236:237], v245 offset:24576\n"
"v_lshl_add_u32 v255, v249, 12, v255\n"
"v_mfma_f32_16x16x32_bf16 v[140:143], a[220:223], a[148:151], v[140:143]\n"
"v_cvt_pk_bf16_f32 v203, v206, v207\n"
"v_mfma_f32_16x16x32_bf16 v[136:139], a[208:211], a[144:147], v[136:139]\n"
"ds_read_b128 a[152:155], v246 offset:4096\n"
"v_cvt_pk_bf16_f32 v178, v180, v181\n"
"v_mfma_f32_16x16x32_bf16 v[136:139], a[212:215], a[148:151], v[136:139]\n"
"v_pk_fma_f32 v[208:209], v[208:209], s[70:71], s[70:71] op_sel:[0,0,1] op_sel_hi:[1,0,1]\nv_pk_fma_f32 v[210:211], v[210:211], s[70:71], s[70:71] op_sel:[0,0,1] op_sel_hi:[1,0,1]\nv_pk_fma_f32 v[212:213], v[212:213], s[70:71], s[70:71] op_sel:[0,0,1] op_sel_hi:[1,0,1]\nv_pk_fma_f32 v[214:215], v[214:215], s[70:71], s[70:71] op_sel:[0,0,1] op_sel_hi:[1,0,1]\nv_pk_fma_f32 v[216:217], v[216:217], s[70:71], s[70:71] op_sel:[0,0,1] op_sel_hi:[1,0,1]\nv_pk_fma_f32 v[218:219], v[218:219], s[70:71], s[70:71] op_sel:[0,0,1] op_sel_hi:[1,0,1]\nv_pk_fma_f32 v[220:221], v[220:221], s[70:71], s[70:71] op_sel:[0,0,1] op_sel_hi:[1,0,1]\nv_pk_fma_f32 v[222:223], v[222:223], s[70:71], s[70:71] op_sel:[0,0,1] op_sel_hi:[1,0,1]\nv_pk_fma_f32 v[160:161], v[160:161], s[70:71], s[70:71] op_sel:[0,0,1] op_sel_hi:[1,0,1]\nv_pk_fma_f32 v[162:163], v[162:163], s[70:71], s[70:71] op_sel:[0,0,1] op_sel_hi:[1,0,1]\nv_pk_fma_f32 v[164:165], v[164:165], s[70:71], s[70:71] op_sel:[0,0,1] op_sel_hi:[1,0,1]\nv_pk_fma_f32 v[166:167], v[166:167], s[70:71], s[70:71] op_sel:[0,0,1] op_sel_hi:[1,0,1]\nv_pk_fma_f32 v[168:169], v[168:169], s[70:71], s[70:71] op_sel:[0,0,1] op_sel_hi:[1,0,1]\nv_pk_fma_f32 v[170:171], v[170:171], s[70:71], s[70:71] op_sel:[0,0,1] op_sel_hi:[1,0,1]\nv_pk_fma_f32 v[172:173], v[172:173], s[70:71], s[70:71] op_sel:[0,0,1] op_sel_hi:[1,0,1]\nv_pk_fma_f32 v[174:175], v[174:175], s[70:71], s[70:71] op_sel:[0,0,1] op_sel_hi:[1,0,1]\n"
"v_mfma_f32_16x16x32_bf16 v[132:135], a[200:203], a[144:147], v[132:135]\n"
"s_add_u32 s40, s56, s54\n"
"ds_read_b64_tr_b16 a[250:251], v245 offset:17920\n"
"v_cvt_pk_bf16_f32 v160, v160, v161\n"
"v_mfma_f32_16x16x32_bf16 v[132:135], a[204:207], a[148:151], v[132:135]\n"
"buffer_load_dwordx4 v248, s[20:23], s40 offen offset:3072 nt lds\n"
"v_mfma_f32_16x16x32_bf16 v[128:131], a[192:195], a[144:147], v[128:131]\n"
"s_add_u32 s55, s55, s59\n"
"v_cvt_pk_bf16_f32 v186, v188, v189\n"
"v_cvt_pk_bf16_f32 v216, v216, v217\n"
"v_mfma_f32_16x16x32_bf16 v[128:131], a[196:199], a[148:151], v[128:131]\n"
"s_waitcnt lgkmcnt(1)\n"
"s_or_b32 s55, s55, s58\n"
"v_cvt_pk_bf16_f32 v208, v208, v209\n"
"v_cvt_pk_bf16_f32 v161, v162, v163\n"
"v_mfma_f32_16x16x32_bf16 v[144:147], a[192:195], a[152:155], v[144:147]\n"
"s_xor_b32 s56, s55, s57\n"
"ds_read_b64_tr_b16 a[238:239], v245 offset:25088\n"
"v_cvt_pk_bf16_f32 v187, v190, v191\n"
"v_mfma_f32_16x16x32_bf16 v[144:147], a[196:199], a[156:159], v[144:147]\n"
"v_cvt_pk_bf16_f32 v217, v218, v219\n"
"v_cvt_pk_bf16_f32 v168, v168, v169\n"
"v_mfma_f32_16x16x32_bf16 v[148:151], a[200:203], a[152:155], v[148:151]\n"
"v_cvt_pk_bf16_f32 v195, v198, v199\n"
"v_xor_b32 v198, 0x400, v243\n"
"v_mfma_f32_16x16x32_bf16 v[148:151], a[204:207], a[156:159], v[148:151]\n"
"ds_read_b64_tr_b16 a[228:229], v244 offset:24576\n"
"v_cvt_pk_bf16_f32 v169, v170, v171\n"
"v_mfma_f32_16x16x32_bf16 v[152:155], a[208:211], a[152:155], v[152:155]\n"
"v_cvt_pk_bf16_f32 v163, v166, v167\n"
"v_cvt_pk_bf16_f32 v171, v174, v175\n"
"v_mfma_f32_16x16x32_bf16 v[152:155], a[212:215], a[156:159], v[152:155]\n"
"v_pk_fma_f32 v[112:113], v[112:113], s[70:71], s[70:71] op_sel:[0,0,1] op_sel_hi:[1,0,1]\nv_pk_fma_f32 v[114:115], v[114:115], s[70:71], s[70:71] op_sel:[0,0,1] op_sel_hi:[1,0,1]\nv_pk_fma_f32 v[116:117], v[116:117], s[70:71], s[70:71] op_sel:[0,0,1] op_sel_hi:[1,0,1]\nv_pk_fma_f32 v[118:119], v[118:119], s[70:71], s[70:71] op_sel:[0,0,1] op_sel_hi:[1,0,1]\nv_pk_fma_f32 v[120:121], v[120:121], s[70:71], s[70:71] op_sel:[0,0,1] op_sel_hi:[1,0,1]\nv_pk_fma_f32 v[122:123], v[122:123], s[70:71], s[70:71] op_sel:[0,0,1] op_sel_hi:[1,0,1]\nv_pk_fma_f32 v[124:125], v[124:125], s[70:71], s[70:71] op_sel:[0,0,1] op_sel_hi:[1,0,1]\nv_pk_fma_f32 v[126:127], v[126:127], s[70:71], s[70:71] op_sel:[0,0,1] op_sel_hi:[1,0,1]\nv_pk_fma_f32 v[128:129], v[128:129], s[70:71], s[70:71] op_sel:[0,0,1] op_sel_hi:[1,0,1]\nv_pk_fma_f32 v[130:131], v[130:131], s[70:71], s[70:71] op_sel:[0,0,1] op_sel_hi:[1,0,1]\nv_pk_fma_f32 v[132:133], v[132:133], s[70:71], s[70:71] op_sel:[0,0,1] op_sel_hi:[1,0,1]\nv_pk_fma_f32 v[134:135], v[134:135], s[70:71], s[70:71] op_sel:[0,0,1] op_sel_hi:[1,0,1]\nv_pk_fma_f32 v[136:137], v[136:137], s[70:71], s[70:71] op_sel:[0,0,1] op_sel_hi:[1,0,1]\nv_pk_fma_f32 v[138:139], v[138:139], s[70:71], s[70:71] op_sel:[0,0,1] op_sel_hi:[1,0,1]\nv_pk_fma_f32 v[140:141], v[140:141], s[70:71], s[70:71] op_sel:[0,0,1] op_sel_hi:[1,0,1]\nv_pk_fma_f32 v[142:143], v[142:143], s[70:71], s[70:71] op_sel:[0,0,1] op_sel_hi:[1,0,1]\n"
"v_mfma_f32_16x16x32_bf16 v[156:159], a[216:219], a[152:155], v[156:159]\n"
"v_cvt_pk_bf16_f32 v136, v136, v137\n"
"v_cvt_pk_bf16_f32 v137, v138, v139\n"
"v_mfma_f32_16x16x32_bf16 v[156:159], a[220:223], a[156:159], v[156:159]\n"
"v_xor_b32 v197, 64, v196\n"
"v_cvt_pk_bf16_f32 v120, v120, v121\n"
"v_mfma_f32_16x16x32_bf16 v[96:99], a[224:227], a[184:187], v[96:99]\n"
"s_waitcnt lgkmcnt(0)\n"
"s_add_u32 m0, s44, 32768\n"
"v_cvt_pk_bf16_f32 v209, v210, v211\n"
"v_cvt_pk_bf16_f32 v210, v212, v213\n"
"v_mfma_f32_16x16x32_bf16 v[96:99], a[228:231], a[188:191], v[96:99]\n"
"buffer_load_dwordx4 v240, s[16:19], s56 offen nt lds\n"
"v_mfma_f32_16x16x32_bf16 v[100:103], a[232:235], a[184:187], v[100:103]\n"
"s_waitcnt vmcnt(14)\n"
"v_cvt_pk_bf16_f32 v128, v128, v129\n"
"v_cvt_pk_bf16_f32 v129, v130, v131\n"
"v_mfma_f32_16x16x32_bf16 v[100:103], a[236:239], a[188:191], v[100:103]\n"
"s_barrier\n"
"v_mfma_f32_16x16x32_bf16 v[104:107], a[240:243], a[184:187], v[104:107]\n"
"ds_read_b128 a[196:199], v198 offset:0\n"
"v_cvt_pk_bf16_f32 v211, v214, v215\n"
"v_mfma_f32_16x16x32_bf16 v[104:107], a[244:247], a[188:191], v[104:107]\n"
"s_mov_b32 s74, 0x41400000\n"
"v_cvt_pk_bf16_f32 v138, v140, v141\n"
"v_cvt_pk_bf16_f32 v139, v142, v143\n"
"v_mfma_f32_16x16x32_bf16 v[108:111], a[248:251], a[184:187], v[108:111]\n"
"ds_read_b128 a[200:203], v243 offset:2048\n"
"v_cvt_pk_bf16_f32 v162, v164, v165\n"
"v_mfma_f32_16x16x32_bf16 v[108:111], a[252:255], a[188:191], v[108:111]\n"
"v_pk_fma_f32 v[144:145], v[144:145], s[70:71], s[70:71] op_sel:[0,0,1] op_sel_hi:[1,0,1]\nv_pk_fma_f32 v[146:147], v[146:147], s[70:71], s[70:71] op_sel:[0,0,1] op_sel_hi:[1,0,1]\nv_pk_fma_f32 v[148:149], v[148:149], s[70:71], s[70:71] op_sel:[0,0,1] op_sel_hi:[1,0,1]\nv_pk_fma_f32 v[150:151], v[150:151], s[70:71], s[70:71] op_sel:[0,0,1] op_sel_hi:[1,0,1]\nv_pk_fma_f32 v[152:153], v[152:153], s[70:71], s[70:71] op_sel:[0,0,1] op_sel_hi:[1,0,1]\nv_pk_fma_f32 v[154:155], v[154:155], s[70:71], s[70:71] op_sel:[0,0,1] op_sel_hi:[1,0,1]\nv_pk_fma_f32 v[156:157], v[156:157], s[70:71], s[70:71] op_sel:[0,0,1] op_sel_hi:[1,0,1]\nv_pk_fma_f32 v[158:159], v[158:159], s[70:71], s[70:71] op_sel:[0,0,1] op_sel_hi:[1,0,1]\n"
"v_mfma_f32_16x16x32_bf16 v[92:95], a[248:251], a[176:179], v[92:95]\n"
"v_permlane16_swap_b32 v161, v163\n"
"v_mfma_f32_16x16x32_bf16 v[92:95], a[252:255], a[180:183], v[92:95]\n"
"ds_write2st64_b64 v255, v[200:201], v[202:203] offset0:102 offset1:103\n"
"v_mfma_f32_16x16x32_bf16 v[88:91], a[240:243], a[176:179], v[88:91]\n"
"v_cvt_pk_bf16_f32 v144, v144, v145\n"
"v_cvt_pk_bf16_f32 v152, v152, v153\n"
"v_mfma_f32_16x16x32_bf16 v[88:91], a[244:247], a[180:183], v[88:91]\n"
"v_cvt_pk_bf16_f32 v145, v146, v147\n"
"v_cvt_pk_bf16_f32 v153, v154, v155\n"
"v_mfma_f32_16x16x32_bf16 v[84:87], a[232:235], a[176:179], v[84:87]\n"
"v_cvt_pk_bf16_f32 v130, v132, v133\n"
"v_cvt_pk_bf16_f32 v131, v134, v135\n"
"v_mfma_f32_16x16x32_bf16 v[84:87], a[236:239], a[180:183], v[84:87]\n"
"ds_write2st64_b64 v255, v[176:177], v[178:179] offset0:96 offset1:97\n"
"v_mfma_f32_16x16x32_bf16 v[80:83], a[224:227], a[176:179], v[80:83]\n"
"s_add_u32 s40, s56, s52\n"
"v_permlane16_swap_b32 v160, v162\n"
"v_mfma_f32_16x16x32_bf16 v[80:83], a[228:231], a[180:183], v[80:83]\n"
"buffer_load_dwordx4 v248, s[16:19], s40 offen offset:1024 nt lds\n"
"v_mfma_f32_16x16x32_bf16 v[64:67], a[224:227], a[168:171], v[64:67]\n"
"v_cvt_pk_bf16_f32 v218, v220, v221\n"
"v_cvt_pk_bf16_f32 v154, v156, v157\n"
"v_mfma_f32_16x16x32_bf16 v[64:67], a[228:231], a[172:175], v[64:67]\n"
"v_permlane16_swap_b32 v128, v130\n"
"v_mfma_f32_16x16x32_bf16 v[68:71], a[232:235], a[168:171], v[68:71]\n"
"ds_read_b128 v[212:215], v197 offset:0\n"
"v_cvt_pk_bf16_f32 v155, v158, v159\n"
"v_mfma_f32_16x16x32_bf16 v[68:71], a[236:239], a[172:175], v[68:71]\n"
"v_pk_fma_f32 v[96:97], v[96:97], s[70:71], s[70:71] op_sel:[0,0,1] op_sel_hi:[1,0,1]\nv_pk_fma_f32 v[98:99], v[98:99], s[70:71], s[70:71] op_sel:[0,0,1] op_sel_hi:[1,0,1]\nv_pk_fma_f32 v[100:101], v[100:101], s[70:71], s[70:71] op_sel:[0,0,1] op_sel_hi:[1,0,1]\nv_pk_fma_f32 v[102:103], v[102:103], s[70:71], s[70:71] op_sel:[0,0,1] op_sel_hi:[1,0,1]\nv_pk_fma_f32 v[104:105], v[104:105], s[70:71], s[70:71] op_sel:[0,0,1] op_sel_hi:[1,0,1]\nv_pk_fma_f32 v[106:107], v[106:107], s[70:71], s[70:71] op_sel:[0,0,1] op_sel_hi:[1,0,1]\nv_pk_fma_f32 v[108:109], v[108:109], s[70:71], s[70:71] op_sel:[0,0,1] op_sel_hi:[1,0,1]\nv_pk_fma_f32 v[110:111], v[110:111], s[70:71], s[70:71] op_sel:[0,0,1] op_sel_hi:[1,0,1]\nv_pk_fma_f32 v[80:81], v[80:81], s[70:71], s[70:71] op_sel:[0,0,1] op_sel_hi:[1,0,1]\nv_pk_fma_f32 v[82:83], v[82:83], s[70:71], s[70:71] op_sel:[0,0,1] op_sel_hi:[1,0,1]\nv_pk_fma_f32 v[84:85], v[84:85], s[70:71], s[70:71] op_sel:[0,0,1] op_sel_hi:[1,0,1]\nv_pk_fma_f32 v[86:87], v[86:87], s[70:71], s[70:71] op_sel:[0,0,1] op_sel_hi:[1,0,1]\nv_pk_fma_f32 v[88:89], v[88:89], s[70:71], s[70:71] op_sel:[0,0,1] op_sel_hi:[1,0,1]\nv_pk_fma_f32 v[90:91], v[90:91], s[70:71], s[70:71] op_sel:[0,0,1] op_sel_hi:[1,0,1]\nv_pk_fma_f32 v[92:93], v[92:93], s[70:71], s[70:71] op_sel:[0,0,1] op_sel_hi:[1,0,1]\nv_pk_fma_f32 v[94:95], v[94:95], s[70:71], s[70:71] op_sel:[0,0,1] op_sel_hi:[1,0,1]\n"
"v_mfma_f32_16x16x32_bf16 v[72:75], a[240:243], a[168:171], v[72:75]\n"
"v_permlane16_swap_b32 v137, v139\n"
"v_mfma_f32_16x16x32_bf16 v[72:75], a[244:247], a[172:175], v[72:75]\n"
"ds_write2st64_b64 v255, v[184:185], v[186:187] offset0:98 offset1:99\n"
"v_mfma_f32_16x16x32_bf16 v[76:79], a[248:251], a[168:171], v[76:79]\n"
"v_cvt_pk_bf16_f32 v121, v122, v123\n"
"v_cvt_pk_bf16_f32 v96, v96, v97\n"
"v_mfma_f32_16x16x32_bf16 v[76:79], a[252:255], a[172:175], v[76:79]\n"
"ds_read_b128 a[192:195], v243 offset:0\n"
"v_mov_b32 v199, 0\n"
"v_mfma_f32_16x16x32_bf16 v[60:63], a[248:251], a[160:163], v[60:63]\n"
"v_cvt_pk_bf16_f32 v104, v104, v105\n"
"v_cvt_pk_bf16_f32 v105, v106, v107\n"
"v_mfma_f32_16x16x32_bf16 v[60:63], a[252:255], a[164:167], v[60:63]\n"
"ds_write2st64_b64 v255, v[208:209], v[210:211] offset0:192 offset1:193\n"
"v_mfma_f32_16x16x32_bf16 v[56:59], a[240:243], a[160:163], v[56:59]\n"
"s_add_u32 s40, s56, s53\n"
"v_accvgpr_write_b32 a121, v137\n"
"v_cvt_pk_bf16_f32 v146, v148, v149\n"
"v_mfma_f32_16x16x32_bf16 v[56:59], a[244:247], a[164:167], v[56:59]\n"
"buffer_load_dwordx4 v240, s[16:19], s40 offen offset:2048 nt lds\n"
"v_mfma_f32_16x16x32_bf16 v[52:55], a[232:235], a[160:163], v[52:55]\n"
"v_cvt_pk_bf16_f32 v97, v98, v99\n"
"v_cvt_pk_bf16_f32 v88, v88, v89\n"
"v_mfma_f32_16x16x32_bf16 v[52:55], a[236:239], a[164:167], v[52:55]\n"
"v_cvt_pk_bf16_f32 v106, v108, v109\n"
"v_mov_b32 v204, 0\n"
"v_mfma_f32_16x16x32_bf16 v[48:51], a[224:227], a[160:163], v[48:51]\n"
"v_cvt_pk_bf16_f32 v219, v222, v223\n"
"v_mov_b32 v205, 0\n"
"v_mfma_f32_16x16x32_bf16 v[48:51], a[228:231], a[164:167], v[48:51]\n"
"ds_write_b128 v243, v[160:163] offset:8192\n"
"v_cvt_pk_bf16_f32 v170, v172, v173\n"
"v_mfma_f32_16x16x32_bf16 v[0:3], a[224:227], a[136:139], v[0:3]\n"
"v_permlane16_swap_b32 v153, v155\n"
"v_mfma_f32_16x16x32_bf16 v[0:3], a[228:231], a[140:143], v[0:3]\n"
"ds_write2st64_b64 v255, v[216:217], v[218:219] offset0:194 offset1:195\n"
"v_mfma_f32_16x16x32_bf16 v[4:7], a[232:235], a[136:139], v[4:7]\n"
"v_permlane16_swap_b32 v169, v171\n"
"v_mfma_f32_16x16x32_bf16 v[4:7], a[236:239], a[140:143], v[4:7]\n"
"v_pk_fma_f32 v[64:65], v[64:65], s[70:71], s[70:71] op_sel:[0,0,1] op_sel_hi:[1,0,1]\nv_pk_fma_f32 v[66:67], v[66:67], s[70:71], s[70:71] op_sel:[0,0,1] op_sel_hi:[1,0,1]\nv_pk_fma_f32 v[68:69], v[68:69], s[70:71], s[70:71] op_sel:[0,0,1] op_sel_hi:[1,0,1]\nv_pk_fma_f32 v[70:71], v[70:71], s[70:71], s[70:71] op_sel:[0,0,1] op_sel_hi:[1,0,1]\nv_pk_fma_f32 v[72:73], v[72:73], s[70:71], s[70:71] op_sel:[0,0,1] op_sel_hi:[1,0,1]\nv_pk_fma_f32 v[74:75], v[74:75], s[70:71], s[70:71] op_sel:[0,0,1] op_sel_hi:[1,0,1]\nv_pk_fma_f32 v[76:77], v[76:77], s[70:71], s[70:71] op_sel:[0,0,1] op_sel_hi:[1,0,1]\nv_pk_fma_f32 v[78:79], v[78:79], s[70:71], s[70:71] op_sel:[0,0,1] op_sel_hi:[1,0,1]\nv_pk_fma_f32 v[48:49], v[48:49], s[70:71], s[70:71] op_sel:[0,0,1] op_sel_hi:[1,0,1]\nv_pk_fma_f32 v[50:51], v[50:51], s[70:71], s[70:71] op_sel:[0,0,1] op_sel_hi:[1,0,1]\nv_pk_fma_f32 v[52:53], v[52:53], s[70:71], s[70:71] op_sel:[0,0,1] op_sel_hi:[1,0,1]\nv_pk_fma_f32 v[54:55], v[54:55], s[70:71], s[70:71] op_sel:[0,0,1] op_sel_hi:[1,0,1]\nv_pk_fma_f32 v[56:57], v[56:57], s[70:71], s[70:71] op_sel:[0,0,1] op_sel_hi:[1,0,1]\nv_pk_fma_f32 v[58:59], v[58:59], s[70:71], s[70:71] op_sel:[0,0,1] op_sel_hi:[1,0,1]\nv_pk_fma_f32 v[60:61], v[60:61], s[70:71], s[70:71] op_sel:[0,0,1] op_sel_hi:[1,0,1]\nv_pk_fma_f32 v[62:63], v[62:63], s[70:71], s[70:71] op_sel:[0,0,1] op_sel_hi:[1,0,1]\n"
"v_mfma_f32_16x16x32_bf16 v[8:11], a[240:243], a[136:139], v[8:11]\n"
"ds_read_b128 a[204:207], v198 offset:2048\n"
"v_cvt_pk_bf16_f32 v56, v56, v57\n"
"v_mfma_f32_16x16x32_bf16 v[8:11], a[244:247], a[140:143], v[8:11]\n"
"v_permlane16_swap_b32 v152, v154\n"
"v_mfma_f32_16x16x32_bf16 v[12:15], a[248:251], a[136:139], v[12:15]\n"
"v_permlane16_swap_b32 v144, v146\n"
"v_mfma_f32_16x16x32_bf16 v[12:15], a[252:255], a[140:143], v[12:15]\n"
"v_accvgpr_write_b32 a137, v153\n"
"v_cvt_pk_bf16_f32 v80, v80, v81\n"
"v_mfma_f32_16x16x32_bf16 v[28:31], a[248:251], a[144:147], v[28:31]\n"
"s_add_u32 s40, s56, s54\n"
"ds_read_b128 v[208:211], v196 offset:0\n"
"v_cvt_pk_bf16_f32 v147, v150, v151\n"
"v_mfma_f32_16x16x32_bf16 v[28:31], a[252:255], a[148:151], v[28:31]\n"
"buffer_load_dwordx4 v248, s[16:19], s40 offen offset:3072 nt lds\n"
"v_mfma_f32_16x16x32_bf16 v[24:27], a[240:243], a[144:147], v[24:27]\n"
"v_cvt_pk_bf16_f32 v107, v110, v111\n"
"v_cvt_pk_bf16_f32 v48, v48, v49\n"
"v_mfma_f32_16x16x32_bf16 v[24:27], a[244:247], a[148:151], v[24:27]\n"
"v_permlane16_swap_b32 v129, v131\n"
"v_mfma_f32_16x16x32_bf16 v[20:23], a[232:235], a[144:147], v[20:23]\n"
"v_permlane16_swap_b32 v136, v138\n"
"v_mfma_f32_16x16x32_bf16 v[20:23], a[236:239], a[148:151], v[20:23]\n"
"ds_read_b128 v[216:219], v196 offset:512\n"
"v_cvt_pk_bf16_f32 v64, v64, v65\n"
"v_mfma_f32_16x16x32_bf16 v[16:19], a[224:227], a[144:147], v[16:19]\n"
"v_cvt_pk_bf16_f32 v122, v124, v125\n"
"v_cvt_pk_bf16_f32 v72, v72, v73\n"
"v_mfma_f32_16x16x32_bf16 v[16:19], a[228:231], a[148:151], v[16:19]\n"
"v_accvgpr_write_b32 a113, v129\n"
"v_accvgpr_write_b32 a122, v138\n"
"v_mfma_f32_16x16x32_bf16 v[32:35], a[224:227], a[152:155], v[32:35]\n"
"v_cvt_pk_bf16_f32 v98, v100, v101\n"
"v_cvt_pk_bf16_f32 v65, v66, v67\n"
"v_mfma_f32_16x16x32_bf16 v[32:35], a[228:231], a[156:159], v[32:35]\n"
"ds_read_b128 v[220:223], v197 offset:512\n"
"v_accvgpr_write_b32 a138, v154\n"
"v_mfma_f32_16x16x32_bf16 v[36:39], a[232:235], a[152:155], v[36:39]\n"
"v_accvgpr_write_b32 a136, v152\n"
"v_cvt_pk_bf16_f32 v89, v90, v91\n"
"v_mfma_f32_16x16x32_bf16 v[36:39], a[236:239], a[156:159], v[36:39]\n"
"v_permlane16_swap_b32 v145, v147\n"
"v_mfma_f32_16x16x32_bf16 v[40:43], a[240:243], a[152:155], v[40:43]\n"
"v_accvgpr_write_b32 a128, v144\n"
"v_cvt_pk_bf16_f32 v99, v102, v103\n"
"v_mfma_f32_16x16x32_bf16 v[40:43], a[244:247], a[156:159], v[40:43]\n"
"ds_write2st64_b64 v255, v[192:193], v[194:195] offset0:100 offset1:101\n"
"v_pk_fma_f32 v[0:1], v[0:1], s[70:71], s[70:71] op_sel:[0,0,1] op_sel_hi:[1,0,1]\nv_pk_fma_f32 v[2:3], v[2:3], s[70:71], s[70:71] op_sel:[0,0,1] op_sel_hi:[1,0,1]\nv_pk_fma_f32 v[4:5], v[4:5], s[70:71], s[70:71] op_sel:[0,0,1] op_sel_hi:[1,0,1]\nv_pk_fma_f32 v[6:7], v[6:7], s[70:71], s[70:71] op_sel:[0,0,1] op_sel_hi:[1,0,1]\nv_pk_fma_f32 v[8:9], v[8:9], s[70:71], s[70:71] op_sel:[0,0,1] op_sel_hi:[1,0,1]\nv_pk_fma_f32 v[10:11], v[10:11], s[70:71], s[70:71] op_sel:[0,0,1] op_sel_hi:[1,0,1]\nv_pk_fma_f32 v[12:13], v[12:13], s[70:71], s[70:71] op_sel:[0,0,1] op_sel_hi:[1,0,1]\nv_pk_fma_f32 v[14:15], v[14:15], s[70:71], s[70:71] op_sel:[0,0,1] op_sel_hi:[1,0,1]\nv_pk_fma_f32 v[16:17], v[16:17], s[70:71], s[70:71] op_sel:[0,0,1] op_sel_hi:[1,0,1]\nv_pk_fma_f32 v[18:19], v[18:19], s[70:71], s[70:71] op_sel:[0,0,1] op_sel_hi:[1,0,1]\nv_pk_fma_f32 v[20:21], v[20:21], s[70:71], s[70:71] op_sel:[0,0,1] op_sel_hi:[1,0,1]\nv_pk_fma_f32 v[22:23], v[22:23], s[70:71], s[70:71] op_sel:[0,0,1] op_sel_hi:[1,0,1]\nv_pk_fma_f32 v[24:25], v[24:25], s[70:71], s[70:71] op_sel:[0,0,1] op_sel_hi:[1,0,1]\nv_pk_fma_f32 v[26:27], v[26:27], s[70:71], s[70:71] op_sel:[0,0,1] op_sel_hi:[1,0,1]\nv_pk_fma_f32 v[28:29], v[28:29], s[70:71], s[70:71] op_sel:[0,0,1] op_sel_hi:[1,0,1]\nv_pk_fma_f32 v[30:31], v[30:31], s[70:71], s[70:71] op_sel:[0,0,1] op_sel_hi:[1,0,1]\n"
"v_mfma_f32_16x16x32_bf16 v[44:47], a[248:251], a[152:155], v[44:47]\n"
"v_accvgpr_write_b32 a115, v131\n"
"v_accvgpr_write_b32 a120, v136\n"
"v_mfma_f32_16x16x32_bf16 v[44:47], a[252:255], a[156:159], v[44:47]\n"
"s_waitcnt lgkmcnt(2)\n"
"ds_write2st64_b64 v255, v[96:97], v[98:99] offset0:228 offset1:229\n"
"v_cvt_pk_bf16_f32 v81, v82, v83\n"
"v_mfma_f32_16x16x32_bf16 v[180:183], v[208:211], a[0:3], 0\n"
"v_accvgpr_write_b32 a112, v128\n"
"v_cvt_pk_bf16_f32 v91, v94, v95\n"
"v_mfma_f32_16x16x32_bf16 v[180:183], v[212:215], a[4:7], v[180:183]\n"
"v_permlane16_swap_b32 v168, v170\n"
"v_mfma_f32_16x16x32_bf16 v[180:183], v[216:219], a[8:11], v[180:183]\n"
"s_waitcnt lgkmcnt(2)\n"
"v_cvt_pk_bf16_f32 v73, v74, v75\n"
"v_cvt_pk_bf16_f32 v8, v8, v9\n"
"v_mfma_f32_16x16x32_bf16 v[180:183], v[220:223], a[12:15], v[180:183]\n"
"ds_write_b128 v243, v[168:171] offset:10240\n"
"v_pk_fma_f32 v[32:33], v[32:33], s[70:71], s[70:71] op_sel:[0,0,1] op_sel_hi:[1,0,1]\nv_pk_fma_f32 v[34:35], v[34:35], s[70:71], s[70:71] op_sel:[0,0,1] op_sel_hi:[1,0,1]\nv_pk_fma_f32 v[36:37], v[36:37], s[70:71], s[70:71] op_sel:[0,0,1] op_sel_hi:[1,0,1]\nv_pk_fma_f32 v[38:39], v[38:39], s[70:71], s[70:71] op_sel:[0,0,1] op_sel_hi:[1,0,1]\nv_pk_fma_f32 v[40:41], v[40:41], s[70:71], s[70:71] op_sel:[0,0,1] op_sel_hi:[1,0,1]\nv_pk_fma_f32 v[42:43], v[42:43], s[70:71], s[70:71] op_sel:[0,0,1] op_sel_hi:[1,0,1]\nv_pk_fma_f32 v[44:45], v[44:45], s[70:71], s[70:71] op_sel:[0,0,1] op_sel_hi:[1,0,1]\nv_pk_fma_f32 v[46:47], v[46:47], s[70:71], s[70:71] op_sel:[0,0,1] op_sel_hi:[1,0,1]\n"
"v_mfma_f32_16x16x32_bf16 v[184:187], v[208:211], a[16:19], 0\n"
"v_cvt_pk_bf16_f32 v49, v50, v51\n"
"v_cvt_pk_bf16_f32 v51, v54, v55\n"
"v_mfma_f32_16x16x32_bf16 v[184:187], v[212:215], a[20:23], v[184:187]\n"
"v_permlane16_swap_b32 v120, v122\n"
"v_mfma_f32_16x16x32_bf16 v[184:187], v[216:219], a[24:27], v[184:187]\n"
"v_cvt_pk_bf16_f32 v90, v92, v93\n"
"v_sub_f32 v183, s74, v183\n"
"v_mfma_f32_16x16x32_bf16 v[184:187], v[220:223], a[28:31], v[184:187]\n"
"ds_write2st64_b64 v255, v[104:105], v[106:107] offset0:230 offset1:231\n"
"v_cvt_pk_bf16_f32 v67, v70, v71\n"
"v_sub_f32 v182, s74, v182\n"
"v_mfma_f32_16x16x32_bf16 v[188:191], v[208:211], a[32:35], 0\n"
"v_cvt_pk_bf16_f32 v40, v40, v41\n"
"v_sub_f32 v180, s74, v180\n"
"v_mfma_f32_16x16x32_bf16 v[188:191], v[212:215], a[36:39], v[188:191]\n"
"v_accvgpr_write_b32 a139, v155\n"
"v_sub_f32 v181, s74, v181\n"
"v_mfma_f32_16x16x32_bf16 v[188:191], v[216:219], a[40:43], v[188:191]\n"
"v_cvt_pk_bf16_f32 v9, v10, v11\n"
"v_sub_f32 v186, s74, v186\n"
"v_mfma_f32_16x16x32_bf16 v[188:191], v[220:223], a[44:47], v[188:191]\n"
"v_cvt_pk_bf16_f32 v83, v86, v87\n"
"v_cvt_pk_bf16_f32 v57, v58, v59\n"
"v_mfma_f32_16x16x32_bf16 v[192:195], v[208:211], a[192:195], 0\n"
"ds_read_b128 v[208:211], v228 offset:0\n"
"v_sub_f32 v185, s74, v185\n"
"v_mfma_f32_16x16x32_bf16 v[192:195], v[212:215], a[196:199], v[192:195]\n"
"ds_read_b128 v[212:215], v229 offset:0\n"
"v_cvt_pk_bf16_f32 v41, v42, v43\n"
"v_mfma_f32_16x16x32_bf16 v[192:195], v[216:219], a[200:203], v[192:195]\n"
"ds_read_b128 v[216:219], v228 offset:512\n"
"v_sub_f32 v189, s74, v189\n"
"v_mfma_f32_16x16x32_bf16 v[192:195], v[220:223], a[204:207], v[192:195]\n"
"s_waitcnt lgkmcnt(2)\n"
"ds_read_b128 v[220:223], v229 offset:512\n"
"v_sub_f32 v184, s74, v184\n"
"v_mfma_f32_16x16x32_bf16 v[152:155], v[208:211], a[0:3], v[180:183]\n"
"s_waitcnt lgkmcnt(2)\n"
"v_cvt_pk_bf16_f32 v42, v44, v45\n"
"v_sub_f32 v187, s74, v187\n"
"v_mfma_f32_16x16x32_bf16 v[152:155], v[212:215], a[4:7], v[152:155]\n"
"s_waitcnt lgkmcnt(1)\n"
"v_accvgpr_write_b32 a129, v145\n"
"v_cvt_pk_bf16_f32 v82, v84, v85\n"
"v_mfma_f32_16x16x32_bf16 v[152:155], v[216:219], a[8:11], v[152:155]\n"
"s_waitcnt lgkmcnt(0)\n"
"v_cvt_pk_bf16_f32 v58, v60, v61\n"
"v_sub_f32 v188, s74, v188\n"
"v_mfma_f32_16x16x32_bf16 v[152:155], v[220:223], a[12:15], v[152:155]\n"
"v_cvt_pk_bf16_f32 v59, v62, v63\n"
"v_sub_f32 v190, s74, v190\n"
"v_mfma_f32_16x16x32_bf16 v[156:159], v[216:219], a[24:27], v[184:187]\n"
"v_cvt_pk_bf16_f32 v11, v14, v15\n"
"v_cvt_pk_bf16_f32 v43, v46, v47\n"
"v_mfma_f32_16x16x32_bf16 v[156:159], v[220:223], a[28:31], v[156:159]\n"
"v_sub_f32 v191, s74, v191\n"
"v_sub_f32 v192, s74, v192\n"
"v_mfma_f32_16x16x32_bf16 v[156:159], v[208:211], a[16:19], v[156:159]\n"
"v_cvt_pk_bf16_f32 v123, v126, v127\n"
"v_cvt_pk_bf16_f32 v10, v12, v13\n"
"v_mfma_f32_16x16x32_bf16 v[156:159], v[212:215], a[20:23], v[156:159]\n"
"ds_write2st64_b64 v255, v[88:89], v[90:91] offset0:226 offset1:227\n"
"v_permlane16_swap_b32 v57, v59\n"
"v_cvt_pk_bf16_f32 v16, v16, v17\n"
"v_accvgpr_write_b32 a241, v254\n"
"v_mov_b32 v196, 0\n"
"v_sub_f32 v193, s74, v193\n"
"v_exp_f32 v153, v153\n"
"v_mfma_f32_16x16x32_bf16 v[160:163], v[208:211], a[32:35], v[188:191]\n"
"v_sub_f32 v194, s74, v194\n"
"v_sub_f32 v195, s74, v195\n"
"v_mfma_f32_16x16x32_bf16 v[160:163], v[212:215], a[36:39], v[160:163]\n"
"v_exp_f32 v152, v152\n"
"v_mfma_f32_16x16x32_bf16 v[160:163], v[216:219], a[40:43], v[160:163]\n"
"v_exp_f32 v158, v158\n"
"v_mfma_f32_16x16x32_bf16 v[160:163], v[220:223], a[44:47], v[160:163]\n"
"v_exp_f32 v156, v156\n"
"v_mfma_f32_16x16x32_bf16 v[164:167], v[216:219], a[200:203], v[192:195]\n"
"ds_read_b128 v[216:219], v228 offset:4608\n"
"v_cvt_pk_bf16_f32 v74, v76, v77\n"
"v_mfma_f32_16x16x32_bf16 v[164:167], v[220:223], a[204:207], v[164:167]\n"
"ds_read_b128 v[220:223], v229 offset:4608\n"
"v_cvt_pk_bf16_f32 v66, v68, v69\n"
"v_mfma_f32_16x16x32_bf16 v[164:167], v[208:211], a[192:195], v[164:167]\n"
"ds_read_b128 v[208:211], v228 offset:4096\n"
"v_cvt_pk_bf16_f32 v24, v24, v25\n"
"v_mfma_f32_16x16x32_bf16 v[164:167], v[212:215], a[196:199], v[164:167]\n"
"s_waitcnt lgkmcnt(2)\n"
"ds_read_b128 v[212:215], v229 offset:4096\n"
"v_accvgpr_write_b32 a114, v130\n"
"v_mfma_f32_16x16x32_bf16 v[168:171], v[216:219], a[8:11], v[180:183]\n"
"s_waitcnt lgkmcnt(2)\n"
"v_exp_f32 v163, v163\n"
"v_mfma_f32_16x16x32_bf16 v[168:171], v[220:223], a[12:15], v[168:171]\n"
"s_waitcnt lgkmcnt(1)\n"
"v_exp_f32 v162, v162\n"
"v_mfma_f32_16x16x32_bf16 v[168:171], v[208:211], a[0:3], v[168:171]\n"
"s_waitcnt lgkmcnt(0)\n"
"v_exp_f32 v160, v160\n"
"v_mfma_f32_16x16x32_bf16 v[168:171], v[212:215], a[4:7], v[168:171]\n"
"ds_write2st64_b64 v255, v[80:81], v[82:83] offset0:224 offset1:225\n"
"v_permlane16_swap_b32 v121, v123\n"
"v_accvgpr_write_b32 a131, v147\n"
"v_exp_f32 v154, v154\n"
"v_exp_f32 v157, v157\n"
"v_exp_f32 v159, v159\n"
"v_exp_f32 v161, v161\n"
"v_exp_f32 v167, v167\n"
"v_mfma_f32_16x16x32_bf16 v[172:175], v[208:211], a[16:19], v[184:187]\n"
"v_exp_f32 v165, v165\n"
"v_mfma_f32_16x16x32_bf16 v[172:175], v[212:215], a[20:23], v[172:175]\n"
"v_exp_f32 v166, v166\n"
"v_mfma_f32_16x16x32_bf16 v[172:175], v[216:219], a[24:27], v[172:175]\n"
"v_exp_f32 v169, v169\n"
"v_mfma_f32_16x16x32_bf16 v[172:175], v[220:223], a[28:31], v[172:175]\n"
"v_exp_f32 v155, v155\n"
"v_mfma_f32_16x16x32_bf16 v[176:179], v[216:219], a[40:43], v[188:191]\n"
"v_cvt_pk_bf16_f32 v128, v152, v153\n"
"v_bfe_u32 v248, v242, 9, 1\n"
"v_mfma_f32_16x16x32_bf16 v[176:179], v[220:223], a[44:47], v[176:179]\n"
"v_exp_f32 v171, v171\n"
"v_mfma_f32_16x16x32_bf16 v[176:179], v[208:211], a[32:35], v[176:179]\n"
"v_cvt_pk_bf16_f32 v129, v154, v155\n"
"v_and_b32 v249, 0xf0, v242\n"
"v_mfma_f32_16x16x32_bf16 v[176:179], v[212:215], a[36:39], v[176:179]\n"
"ds_write2st64_b64 v255, v[64:65], v[66:67] offset0:196 offset1:197\n"
"v_cvt_pk_bf16_f32 v136, v156, v157\n"
"v_cvt_pk_bf16_f32 v137, v158, v159\n"
"v_cvt_pk_bf16_f32 v145, v162, v163\n"
"v_exp_f32 v170, v170\n"
"v_mfma_f32_16x16x32_bf16 v[152:155], v[208:211], a[192:195], v[192:195]\n"
"ds_read_b128 v[208:211], v230 offset:0\n"
"v_cvt_pk_bf16_f32 v125, v166, v167\n"
"v_mfma_f32_16x16x32_bf16 v[152:155], v[212:215], a[196:199], v[152:155]\n"
"ds_read_b128 v[212:215], v231 offset:0\n"
"v_accvgpr_write_b32 a123, v139\n"
"v_mfma_f32_16x16x32_bf16 v[152:155], v[216:219], a[200:203], v[152:155]\n"
"ds_read_b128 v[216:219], v230 offset:512\n"
"v_cvt_pk_bf16_f32 v50, v52, v53\n"
"v_mfma_f32_16x16x32_bf16 v[152:155], v[220:223], a[204:207], v[152:155]\n"
"s_waitcnt lgkmcnt(2)\n"
"ds_read_b128 v[220:223], v231 offset:512\n"
"v_permlane16_swap_b32 v40, v42\n"
"v_cvt_pk_bf16_f32 v144, v160, v161\n"
"v_exp_f32 v164, v164\n"
"v_exp_f32 v168, v168\n"
"v_cvt_pk_bf16_f32 v131, v170, v171\n"
"v_exp_f32 v172, v172\n"
"v_exp_f32 v173, v173\n"
"v_exp_f32 v174, v174\n"
"v_exp_f32 v176, v176\n"
"v_exp_f32 v179, v179\n"
"v_lshl_add_u32 v248, v248, 8, v249\n"
"v_mfma_f32_16x16x32_bf16 v[156:159], v[208:211], a[0:3], v[180:183]\n"
"s_waitcnt lgkmcnt(2)\n"
"v_cvt_pk_bf16_f32 v75, v78, v79\n"
"v_cvt_pk_bf16_f32 v130, v168, v169\n"
"v_mfma_f32_16x16x32_bf16 v[156:159], v[212:215], a[4:7], v[156:159]\n"
"s_waitcnt lgkmcnt(1)\n"
"v_bfe_u32 v249, v242, 8, 1\n"
"v_lshl_add_u32 v248, v249, 9, v248\n"
"v_mfma_f32_16x16x32_bf16 v[156:159], v[216:219], a[8:11], v[156:159]\n"
"s_waitcnt lgkmcnt(0)\n"
"v_exp_f32 v177, v177\n"
"v_mfma_f32_16x16x32_bf16 v[156:159], v[220:223], a[12:15], v[156:159]\n"
"v_mov_b32 v202, 0\n"
"v_cvt_pk_bf16_f32 v124, v164, v165\n"
"v_mfma_f32_16x16x32_bf16 v[160:163], v[216:219], a[24:27], v[184:187]\n"
"v_exp_f32 v155, v155\n"
"v_mfma_f32_16x16x32_bf16 v[160:163], v[220:223], a[28:31], v[160:163]\n"
"v_permlane16_swap_b32 v49, v51\n"
"v_mfma_f32_16x16x32_bf16 v[160:163], v[208:211], a[16:19], v[160:163]\n"
"v_mov_b32 v201, 0\n"
"v_xor_b32_e64 v249, s42, 0\n"
"v_mfma_f32_16x16x32_bf16 v[160:163], v[212:215], a[20:23], v[160:163]\n"
"ds_write2st64_b64 v255, v[72:73], v[74:75] offset0:198 offset1:199\n"
"v_permlane16_swap_b32 v48, v50\n"
"v_permlane16_swap_b32 v56, v58\n"
"v_cvt_pk_bf16_f32 v25, v26, v27\n"
"v_cvt_pk_bf16_f32 v138, v172, v173\n"
"v_exp_f32 v153, v153\n"
"v_exp_f32 v157, v157\n"
"v_mfma_f32_16x16x32_bf16 v[164:167], v[208:211], a[32:35], v[188:191]\n"
"ds_read_b128 v[208:211], v230 offset:4096\n"
"v_mov_b32 v197, 0\n"
"v_mfma_f32_16x16x32_bf16 v[164:167], v[212:215], a[36:39], v[164:167]\n"
"ds_read_b128 v[212:215], v231 offset:4096\n"
"v_accvgpr_write_b32 a243, v254\n"
"v_mfma_f32_16x16x32_bf16 v[164:167], v[216:219], a[40:43], v[164:167]\n"
"ds_read_b128 v[216:219], v230 offset:4608\n"
"v_lshl_add_u32 v248, v249, 12, v248\n"
"v_mfma_f32_16x16x32_bf16 v[164:167], v[220:223], a[44:47], v[164:167]\n"
"s_waitcnt lgkmcnt(2)\n"
"ds_read_b128 v[220:223], v231 offset:4608\n"
"v_accvgpr_write_b32 a104, v120\n"
"v_permlane16_swap_b32 v8, v10\n"
"v_permlane16_swap_b32 v9, v11\n"
"v_accvgpr_write_b32 a240, v254\n"
"v_exp_f32 v175, v175\n"
"v_exp_f32 v178, v178\n"
"v_exp_f32 v159, v159\n"
"v_exp_f32 v160, v160\n"
"v_exp_f32 v163, v163\n"
"v_mfma_f32_16x16x32_bf16 v[168:171], v[208:211], a[0:3], v[180:183]\n"
"s_waitcnt lgkmcnt(2)\n"
"v_exp_f32 v154, v154\n"
"v_mfma_f32_16x16x32_bf16 v[168:171], v[212:215], a[4:7], v[168:171]\n"
"s_waitcnt lgkmcnt(1)\n"
"v_accvgpr_write_b32 a186, v42\n"
"v_add_u32 v249, s75, v248\n"
"v_mfma_f32_16x16x32_bf16 v[168:171], v[216:219], a[8:11], v[168:171]\n"
"s_waitcnt lgkmcnt(0)\n"
"v_exp_f32 v152, v152\n"
"v_mfma_f32_16x16x32_bf16 v[168:171], v[220:223], a[12:15], v[168:171]\n"
"ds_write_b128 v243, v[48:51] offset:12288\n"
"v_accvgpr_write_b32 a130, v146\n"
"v_permlane16_swap_b32 v41, v43\n"
"v_cvt_pk_bf16_f32 v139, v174, v175\n"
"v_exp_f32 v164, v164\n"
"v_mfma_f32_16x16x32_bf16 v[172:175], v[216:219], a[24:27], v[184:187]\n"
"v_exp_f32 v156, v156\n"
"v_mfma_f32_16x16x32_bf16 v[172:175], v[220:223], a[28:31], v[172:175]\n"
"v_exp_f32 v158, v158\n"
"v_mfma_f32_16x16x32_bf16 v[172:175], v[208:211], a[16:19], v[172:175]\n"
"v_exp_f32 v168, v168\n"
"v_mfma_f32_16x16x32_bf16 v[172:175], v[212:215], a[20:23], v[172:175]\n"
"s_cbranch_vccnz LPCE0_%=\nLPKE0_%=:\n"
"ds_write_b128 v243, v[56:59] offset:14336\n"
"v_accvgpr_write_b32 a185, v41\n"
"v_cvt_pk_bf16_f32 v146, v176, v177\n"
"v_cvt_pk_bf16_f32 v147, v178, v179\n"
"v_cvt_pk_bf16_f32 v126, v152, v153\n"
"v_cvt_pk_bf16_f32 v133, v158, v159\n"
"v_exp_f32 v165, v165\n"
"v_mfma_f32_16x16x32_bf16 v[176:179], v[208:211], a[32:35], v[188:191]\n"
"v_cvt_pk_bf16_f32 v127, v154, v155\n"
"v_cvt_pk_bf16_f32 v132, v156, v157\n"
"v_mfma_f32_16x16x32_bf16 v[176:179], v[212:215], a[36:39], v[176:179]\n"
"v_exp_f32 v161, v161\n"
"v_mfma_f32_16x16x32_bf16 v[176:179], v[216:219], a[40:43], v[176:179]\n"
"v_cvt_pk_bf16_f32 v17, v18, v19\n"
"v_accvgpr_write_b32 a187, v43\n"
"v_mfma_f32_16x16x32_bf16 v[176:179], v[220:223], a[44:47], v[176:179]\n"
"v_cvt_pk_bf16_f32 v140, v160, v161\n"
"v_exp_f32 v162, v162\n"
"v_exp_f32 v167, v167\n"
"v_exp_f32 v169, v169\n"
"v_exp_f32 v172, v172\n"
"v_exp_f32 v173, v173\n"
"v_exp_f32 v174, v174\n"
"s_waitcnt vmcnt(8) lgkmcnt(0)\n"
"s_barrier\n"
"ds_read_b128 v[208:211], v228 offset:16384\n"
"ds_read_b128 v[212:215], v229 offset:16384\n"
"ds_read_b128 v[216:219], v228 offset:16896\n"
"ds_read_b128 v[220:223], v229 offset:16896\n"
"s_waitcnt lgkmcnt(3)\n"
"v_mfma_f32_16x16x32_bf16 v[152:155], v[208:211], a[0:3], v[180:183]\n"
"v_accvgpr_write_b32 a153, v9\n"
"v_cvt_pk_bf16_f32 v32, v32, v33\n"
"s_waitcnt lgkmcnt(2)\n"
"v_mfma_f32_16x16x32_bf16 v[152:155], v[212:215], a[4:7], v[152:155]\n"
"ds_read_b64_tr_b16 v[224:225], v239 offset:0\n"
"v_cvt_pk_bf16_f32 v141, v162, v163\n"
"s_waitcnt lgkmcnt(2)\n"
"v_mfma_f32_16x16x32_bf16 v[152:155], v[216:219], a[8:11], v[152:155]\n"
"s_waitcnt lgkmcnt(1)\n"
"ds_read_b128 a[188:191], v249 offset:52224\n"
"v_mov_b32 v203, 0\n"
"v_mfma_f32_16x16x32_bf16 v[152:155], v[220:223], a[12:15], v[152:155]\n"
"ds_read_b128 a[116:119], v248 offset:51200\n"
"v_cvt_pk_bf16_f32 v27, v30, v31\n"
"v_mfma_f32_16x16x32_bf16 v[156:159], v[216:219], a[24:27], v[184:187]\n"
"ds_read_b64_tr_b16 v[226:227], v239 offset:4096\n"
"ds_read_b128 a[180:183], v249 offset:51200\n"
"v_mfma_f32_16x16x32_bf16 v[156:159], v[220:223], a[28:31], v[156:159]\n"
"v_accvgpr_write_b32 a154, v10\n"
"v_accvgpr_write_b32 a184, v40\n"
"v_mfma_f32_16x16x32_bf16 v[156:159], v[208:211], a[16:19], v[156:159]\n"
"ds_read_b64_tr_b16 a[244:245], v234 offset:0\n"
"ds_read_b64_tr_b16 a[246:247], v234 offset:4096\n"
"v_mfma_f32_16x16x32_bf16 v[156:159], v[212:215], a[20:23], v[156:159]\n"
"ds_read_b128 a[100:103], v248 offset:49152\n"
"v_exp_f32 v166, v166\n"
"v_exp_f32 v175, v175\n"
"v_exp_f32 v176, v176\n"
"v_exp_f32 v177, v177\n"
"v_exp_f32 v154, v154\n"
"v_mfma_f32_16x16x32_bf16 v[160:163], v[208:211], a[32:35], v[188:191]\n"
"v_mov_b32 v198, 0\n"
"v_cvt_pk_bf16_f32 v148, v164, v165\n"
"v_mfma_f32_16x16x32_bf16 v[160:163], v[212:215], a[36:39], v[160:163]\n"
"ds_read_b64_tr_b16 a[250:251], v238 offset:4096\n"
"ds_read_b64_tr_b16 a[254:255], v235 offset:4096\n"
"v_mfma_f32_16x16x32_bf16 v[160:163], v[216:219], a[40:43], v[160:163]\n"
"ds_read_b64_tr_b16 a[248:249], v238 offset:0\n"
"ds_read_b128 a[156:159], v249 offset:35840\n"
"v_mfma_f32_16x16x32_bf16 v[160:163], v[220:223], a[44:47], v[160:163]\n"
"ds_read_b128 a[172:175], v249 offset:50176\n"
"v_accvgpr_write_b32 a155, v11\n"
"v_cvt_pk_bf16_f32 v149, v166, v167\n"
"v_cvt_pk_bf16_f32 v134, v168, v169\n"
"v_exp_f32 v170, v170\n"
"v_exp_f32 v171, v171\n"
"v_cvt_pk_bf16_f32 v143, v174, v175\n"
"v_exp_f32 v179, v179\n"
"v_mfma_f32_16x16x32_bf16 v[164:167], v[216:219], a[200:203], v[192:195]\n"
"ds_read_b128 a[164:167], v249 offset:49152\n"
"v_cvt_pk_bf16_f32 v142, v172, v173\n"
"v_mfma_f32_16x16x32_bf16 v[164:167], v[220:223], a[204:207], v[164:167]\n"
"ds_read_b64_tr_b16 a[252:253], v235 offset:0\n"
"v_cvt_pk_bf16_f32 v112, v112, v113\n"
"v_mfma_f32_16x16x32_bf16 v[164:167], v[208:211], a[192:195], v[164:167]\n"
"ds_read_b128 a[140:143], v249 offset:33792\n"
"v_cvt_pk_bf16_f32 v0, v0, v1\n"
"v_mfma_f32_16x16x32_bf16 v[164:167], v[212:215], a[196:199], v[164:167]\n"
"s_waitcnt lgkmcnt(8)\n"
"ds_read_b128 a[148:151], v249 offset:34816\n"
"v_accvgpr_write_b32 a152, v8\n"
"v_cvt_pk_bf16_f32 v33, v34, v35\n"
"v_cvt_pk_bf16_f32 v135, v170, v171\n"
"v_exp_f32 v178, v178\n"
"v_xor_b32 v207, 64, v240\n"
"v_exp_f32 v153, v153\n"
"v_exp_f32 v162, v162\n"
"v_mfma_f32_16x16x32_bf16 a[216:219], v[124:127], a[244:247], 0\n"
"ds_read_b128 a[108:111], v248 offset:50176\n"
"v_cvt_pk_bf16_f32 v1, v2, v3\n"
"v_mfma_f32_16x16x32_bf16 v[8:11], v[128:131], a[244:247], 0\n"
"s_waitcnt lgkmcnt(7)\n"
"ds_read_b128 a[124:127], v248 offset:52224\n"
"v_cvt_pk_bf16_f32 v34, v36, v37\n"
"v_mfma_f32_16x16x32_bf16 v[8:11], v[132:135], a[248:251], v[8:11]\n"
"ds_read_b128 v[216:219], v228 offset:20992\n"
"v_cvt_pk_bf16_f32 v26, v28, v29\n"
"v_mfma_f32_16x16x32_bf16 v[40:43], v[136:139], a[244:247], 0\n"
"ds_read_b128 a[132:135], v249 offset:32768\n"
"v_cvt_pk_bf16_f32 v150, v176, v177\n"
"v_mfma_f32_16x16x32_bf16 v[40:43], v[140:143], a[248:251], v[40:43]\n"
"v_cvt_pk_bf16_f32 v3, v6, v7\n"
"v_cvt_pk_bf16_f32 v151, v178, v179\n"
"v_mfma_f32_16x16x32_bf16 v[72:75], v[144:147], a[244:247], 0\n"
"ds_read_b128 v[208:211], v228 offset:20480\n"
"v_accvgpr_write_b32 a242, v254\n"
"v_mfma_f32_16x16x32_bf16 v[72:75], v[148:151], a[248:251], v[72:75]\n"
"ds_read_b128 v[220:223], v229 offset:20992\n"
"v_cvt_pk_bf16_f32 v113, v114, v115\n"
"v_mfma_f32_16x16x32_bf16 v[76:79], v[148:151], v[224:227], 0\n"
"s_waitcnt lgkmcnt(6)\n"
"v_exp_f32 v159, v159\n"
"v_mfma_f32_16x16x32_bf16 v[76:79], v[144:147], a[252:255], v[76:79]\n"
"v_permlane16_swap_b32 v24, v26\n"
"v_mfma_f32_16x16x32_bf16 v[44:47], v[140:143], v[224:227], 0\n"
"v_permlane16_swap_b32 v32, v34\n"
"v_mfma_f32_16x16x32_bf16 v[44:47], v[136:139], a[252:255], v[44:47]\n"
"ds_read_b128 v[212:215], v229 offset:20480\n"
"v_permlane16_swap_b32 v25, v27\n"
"v_cvt_pk_bf16_f32 v35, v38, v39\n"
"v_exp_f32 v155, v155\n"
"v_exp_f32 v160, v160\n"
"v_exp_f32 v167, v167\n"
"v_mfma_f32_16x16x32_bf16 v[12:15], v[132:135], v[224:227], 0\n"
"v_exp_f32 v164, v164\n"
"v_mfma_f32_16x16x32_bf16 v[12:15], v[128:131], a[252:255], v[12:15]\n"
"v_exp_f32 v166, v166\n"
"v_mfma_f32_16x16x32_bf16 a[220:223], v[124:127], a[252:255], 0\n"
"s_waitcnt lgkmcnt(2)\n"
"ds_read_b64_tr_b16 a[254:255], v235 offset:4608\n"
"v_accvgpr_write_b32 a169, v25\n"
"v_smfmac_f32_16x16x64_bf16 v[196:199], a[240:243], v[128:135], v241\n"
"s_add_u32 m0, s44, 98304\n"
"ds_read_b64_tr_b16 v[224:225], v239 offset:512\n"
"v_cvt_pk_bf16_f32 v97, v154, v155\n"
"v_mfma_f32_16x16x32_bf16 v[202:205], a[240:243], v[124:127], v[202:205]\n"
"buffer_load_dwordx4 v240, s[20:23], s56 offen nt lds\n"
"v_mfma_f32_16x16x32_bf16 v[168:171], v[216:219], a[8:11], v[180:183]\n"
"s_waitcnt lgkmcnt(3)\n"
"v_exp_f32 v152, v152\n"
"v_mfma_f32_16x16x32_bf16 v[168:171], v[220:223], a[12:15], v[168:171]\n"
"ds_read_b64_tr_b16 a[252:253], v235 offset:512\n"
"v_accvgpr_write_b32 a168, v24\n"
"v_mfma_f32_16x16x32_bf16 v[168:171], v[208:211], a[0:3], v[168:171]\n"
"s_waitcnt lgkmcnt(3)\n"
"ds_read_b64_tr_b16 a[244:245], v234 offset:512\n"
"v_cvt_pk_bf16_f32 v96, v152, v153\n"
"v_mfma_f32_16x16x32_bf16 v[168:171], v[212:215], a[4:7], v[168:171]\n"
"ds_read_b64_tr_b16 a[248:249], v238 offset:512\n"
"ds_read_b64_tr_b16 a[250:251], v238 offset:4608\n"
"v_mfma_f32_16x16x32_bf16 v[172:175], v[208:211], a[16:19], v[184:187]\n"
"ds_read_b64_tr_b16 a[246:247], v234 offset:4608\n"
"v_cvt_pk_bf16_f32 v115, v118, v119\n"
"v_mfma_f32_16x16x32_bf16 v[172:175], v[212:215], a[20:23], v[172:175]\n"
"v_exp_f32 v165, v165\n"
"v_mfma_f32_16x16x32_bf16 v[172:175], v[216:219], a[24:27], v[172:175]\n"
"s_add_u32 s40, s56, s52\n"
"v_exp_f32 v158, v158\n"
"v_mfma_f32_16x16x32_bf16 v[172:175], v[220:223], a[28:31], v[172:175]\n"
"buffer_load_dwordx4 v207, s[20:23], s40 offen offset:1024 nt lds\n"
"v_mfma_f32_16x16x32_bf16 v[176:179], v[216:219], a[40:43], v[188:191]\n"
"v_exp_f32 v157, v157\n"
"v_mfma_f32_16x16x32_bf16 v[176:179], v[220:223], a[44:47], v[176:179]\n"
"v_accvgpr_write_b32 a105, v121\n"
"v_accvgpr_write_b32 a170, v26\n"
"v_mfma_f32_16x16x32_bf16 v[176:179], v[208:211], a[32:35], v[176:179]\n"
"v_exp_f32 v156, v156\n"
"v_mfma_f32_16x16x32_bf16 v[176:179], v[212:215], a[36:39], v[176:179]\n"
"v_permlane16_swap_b32 v113, v115\n"
"v_cvt_pk_bf16_f32 v2, v4, v5\n"
"v_exp_f32 v161, v161\n"
"v_exp_f32 v163, v163\n"
"v_exp_f32 v172, v172\n"
"v_mfma_f32_16x16x32_bf16 v[152:155], v[208:211], a[192:195], v[192:195]\n"
"v_accvgpr_write_b32 a171, v27\n"
"v_mov_b32 v200, 0\n"
"v_mfma_f32_16x16x32_bf16 v[152:155], v[212:215], a[196:199], v[152:155]\n"
"ds_read_b64_tr_b16 v[226:227], v239 offset:4608\n"
"v_accvgpr_write_b32 a176, v32\n"
"v_mfma_f32_16x16x32_bf16 v[152:155], v[216:219], a[200:203], v[152:155]\n"
"ds_read_b128 v[212:215], v231 offset:16384\n"
"v_cvt_pk_bf16_f32 v105, v158, v159\n"
"v_mfma_f32_16x16x32_bf16 v[152:155], v[220:223], a[204:207], v[152:155]\n"
"s_waitcnt lgkmcnt(2)\n"
"v_permlane16_swap_b32 v0, v2\n"
"v_mfma_f32_16x16x32_bf16 a[232:235], v[124:127], a[244:247], 0\n"
"ds_read_b128 v[216:219], v230 offset:16896\n"
"v_cvt_pk_bf16_f32 v18, v20, v21\n"
"v_mfma_f32_16x16x32_bf16 v[24:27], v[128:131], a[244:247], 0\n"
"s_add_u32 s40, s56, s53\n"
"ds_read_b128 v[208:211], v230 offset:16384\n"
"v_cvt_pk_bf16_f32 v104, v156, v157\n"
"v_mfma_f32_16x16x32_bf16 v[24:27], v[132:135], a[248:251], v[24:27]\n"
"buffer_load_dwordx4 v240, s[20:23], s40 offen offset:2048 nt lds\n"
"v_mfma_f32_16x16x32_bf16 v[56:59], v[136:139], a[244:247], 0\n"
"v_exp_f32 v174, v174\n"
"v_mfma_f32_16x16x32_bf16 v[56:59], v[140:143], a[248:251], v[56:59]\n"
"v_accvgpr_write_b32 a97, v113\n"
"v_accvgpr_write_b32 a144, v0\n"
"v_mfma_f32_16x16x32_bf16 v[88:91], v[144:147], a[244:247], 0\n"
"v_exp_f32 v171, v171\n"
"v_mfma_f32_16x16x32_bf16 v[88:91], v[148:151], a[248:251], v[88:91]\n"
"s_waitcnt lgkmcnt(2)\n"
"ds_read_b128 v[220:223], v231 offset:16896\n"
"v_cvt_pk_bf16_f32 v114, v116, v117\n"
"v_mfma_f32_16x16x32_bf16 v[92:95], v[148:151], v[224:227], 0\n"
"s_add_u32 s40, s56, s54\n"
"v_permlane16_swap_b32 v33, v35\n"
"v_mfma_f32_16x16x32_bf16 v[92:95], v[144:147], a[252:255], v[92:95]\n"
"buffer_load_dwordx4 v207, s[20:23], s40 offen offset:3072 nt lds\n"
"v_permlane16_swap_b32 v112, v114\n"
"v_accvgpr_write_b32 a99, v115\n"
"v_cvt_pk_bf16_f32 v120, v164, v165\n"
"v_cvt_pk_bf16_f32 v121, v166, v167\n"
"v_exp_f32 v175, v175\n"
"v_exp_f32 v178, v178\n"
"v_mfma_f32_16x16x32_bf16 v[60:63], v[140:143], v[224:227], 0\n"
"s_add_u32 s55, s55, s59\n"
"v_permlane16_swap_b32 v1, v3\n"
"v_mfma_f32_16x16x32_bf16 v[60:63], v[136:139], a[252:255], v[60:63]\n"
"s_or_b32 s55, s55, s58\n"
"v_accvgpr_write_b32 a96, v112\n"
"v_accvgpr_write_b32 a178, v34\n"
"v_mfma_f32_16x16x32_bf16 v[28:31], v[132:135], v[224:227], 0\n"
"s_xor_b32 s56, s55, s57\n"
"ds_read_b64_tr_b16 v[224:225], v237 offset:0\n"
"v_accvgpr_write_b32 a179, v35\n"
"v_mfma_f32_16x16x32_bf16 v[28:31], v[128:131], a[252:255], v[28:31]\n"
"ds_read_b64_tr_b16 a[244:245], v232 offset:0\n"
"v_accvgpr_write_b32 a145, v1\n"
"v_mfma_f32_16x16x32_bf16 a[236:239], v[124:127], a[252:255], 0\n"
"v_exp_f32 v179, v179\n"
"v_smfmac_f32_16x16x64_bf16 v[198:201], a[240:243], v[136:143], v241\n"
"s_waitcnt lgkmcnt(2)\n"
"ds_read_b64_tr_b16 a[252:253], v233 offset:0\n"
"v_accvgpr_write_b32 a177, v33\n"
"v_mfma_f32_16x16x32_bf16 v[156:159], v[208:211], a[0:3], v[180:183]\n"
"ds_read_b64_tr_b16 a[246:247], v232 offset:4096\n"
"v_cvt_pk_bf16_f32 v113, v162, v163\n"
"v_mfma_f32_16x16x32_bf16 v[156:159], v[212:215], a[4:7], v[156:159]\n"
"ds_read_b64_tr_b16 a[250:251], v236 offset:4096\n"
"v_cvt_pk_bf16_f32 v112, v160, v161\n"
"v_mfma_f32_16x16x32_bf16 v[156:159], v[216:219], a[8:11], v[156:159]\n"
"s_add_u32 m0, s44, 49152\n"
"ds_read_b64_tr_b16 a[248:249], v236 offset:0\n"
"ds_read_b64_tr_b16 v[226:227], v237 offset:4096\n"
"v_mfma_f32_16x16x32_bf16 v[156:159], v[220:223], a[12:15], v[156:159]\n"
"buffer_load_dwordx4 v240, s[16:19], s56 offen nt lds\n"
"v_mfma_f32_16x16x32_bf16 v[160:163], v[216:219], a[24:27], v[184:187]\n"
"v_exp_f32 v169, v169\n"
"v_mfma_f32_16x16x32_bf16 v[160:163], v[220:223], a[28:31], v[160:163]\n"
"v_accvgpr_write_b32 a98, v114\n"
"v_accvgpr_write_b32 a147, v3\n"
"v_mfma_f32_16x16x32_bf16 v[160:163], v[208:211], a[16:19], v[160:163]\n"
"v_accvgpr_write_b32 a146, v2\n"
"v_cvt_pk_bf16_f32 v107, v174, v175\n"
"v_mfma_f32_16x16x32_bf16 v[160:163], v[212:215], a[20:23], v[160:163]\n"
"ds_read_b64_tr_b16 a[254:255], v233 offset:4096\n"
"v_cvt_pk_bf16_f32 v115, v178, v179\n"
"v_mfma_f32_16x16x32_bf16 v[164:167], v[208:211], a[32:35], v[188:191]\n"
"v_exp_f32 v158, v158\n"
"v_mfma_f32_16x16x32_bf16 v[164:167], v[212:215], a[36:39], v[164:167]\n"
"v_exp_f32 v168, v168\n"
"v_mfma_f32_16x16x32_bf16 v[164:167], v[216:219], a[40:43], v[164:167]\n"
"v_exp_f32 v170, v170\n"
"v_mfma_f32_16x16x32_bf16 v[164:167], v[220:223], a[44:47], v[164:167]\n"
"s_waitcnt lgkmcnt(1)\n"
"v_exp_f32 v159, v159\n"
"v_mfma_f32_16x16x32_bf16 a[208:211], v[124:127], a[244:247], 0\n"
"ds_read_b128 v[208:211], v230 offset:20480\n"
"v_cvt_pk_bf16_f32 v19, v22, v23\n"
"v_mfma_f32_16x16x32_bf16 v[0:3], v[128:131], a[244:247], 0\n"
"s_add_u32 s40, s56, s52\n"
"ds_read_b128 v[216:219], v230 offset:20992\n"
"v_cvt_pk_bf16_f32 v98, v168, v169\n"
"v_mfma_f32_16x16x32_bf16 v[0:3], v[132:135], a[248:251], v[0:3]\n"
"buffer_load_dwordx4 v207, s[16:19], s40 offen offset:1024 nt lds\n"
"v_mfma_f32_16x16x32_bf16 v[32:35], v[136:139], a[244:247], 0\n"
"v_exp_f32 v165, v165\n"
"v_mfma_f32_16x16x32_bf16 v[32:35], v[140:143], a[248:251], v[32:35]\n"
"v_exp_f32 v176, v176\n"
"v_mfma_f32_16x16x32_bf16 v[64:67], v[144:147], a[244:247], 0\n"
"ds_read_b128 v[212:215], v231 offset:20480\n"
"v_cvt_pk_bf16_f32 v99, v170, v171\n"
"v_mfma_f32_16x16x32_bf16 v[64:67], v[148:151], a[248:251], v[64:67]\n"
"ds_read_b128 v[220:223], v231 offset:20992\n"
"v_accvgpr_write_b32 a107, v123\n"
"v_permlane16_swap_b32 v16, v18\n"
"v_exp_f32 v173, v173\n"
"v_exp_f32 v154, v154\n"
"v_exp_f32 v155, v155\n"
"v_mfma_f32_16x16x32_bf16 v[68:71], v[148:151], v[224:227], 0\n"
"s_waitcnt lgkmcnt(4)\n"
"s_add_u32 s40, s56, s53\n"
"v_exp_f32 v164, v164\n"
"v_mfma_f32_16x16x32_bf16 v[68:71], v[144:147], a[252:255], v[68:71]\n"
"buffer_load_dwordx4 v240, s[16:19], s40 offen offset:2048 nt lds\n"
"v_mfma_f32_16x16x32_bf16 v[36:39], v[140:143], v[224:227], 0\n"
"v_exp_f32 v153, v153\n"
"v_mfma_f32_16x16x32_bf16 v[36:39], v[136:139], a[252:255], v[36:39]\n"
"v_exp_f32 v166, v166\n"
"v_mfma_f32_16x16x32_bf16 v[4:7], v[132:135], v[224:227], 0\n"
"v_permlane16_swap_b32 v17, v19\n"
"v_mfma_f32_16x16x32_bf16 v[4:7], v[128:131], a[252:255], v[4:7]\n"
"ds_read_b64_tr_b16 a[250:251], v236 offset:4608\n"
"v_cvt_pk_bf16_f32 v106, v172, v173\n"
"v_mfma_f32_16x16x32_bf16 a[212:215], v[124:127], a[252:255], 0\n"
"v_exp_f32 v162, v162\n"
"v_smfmac_f32_16x16x64_bf16 v[200:203], a[240:243], v[144:151], v241\n"
"s_waitcnt lgkmcnt(1)\n"
"ds_read_b64_tr_b16 a[246:247], v232 offset:4608\n"
"ds_read_b64_tr_b16 a[248:249], v236 offset:512\n"
"v_mfma_f32_16x16x32_bf16 v[168:171], v[208:211], a[0:3], v[180:183]\n"
"v_exp_f32 v177, v177\n"
"v_mfma_f32_16x16x32_bf16 v[168:171], v[212:215], a[4:7], v[168:171]\n"
"ds_read_b64_tr_b16 a[244:245], v232 offset:512\n"
"v_cvt_pk_bf16_f32 v114, v176, v177\n"
"v_mfma_f32_16x16x32_bf16 v[168:171], v[216:219], a[8:11], v[168:171]\n"
"s_add_u32 s40, s56, s54\n"
"v_exp_f32 v157, v157\n"
"v_mfma_f32_16x16x32_bf16 v[168:171], v[220:223], a[12:15], v[168:171]\n"
"buffer_load_dwordx4 v207, s[16:19], s40 offen offset:3072 nt lds\n"
"v_mfma_f32_16x16x32_bf16 v[172:175], v[216:219], a[24:27], v[184:187]\n"
"v_exp_f32 v167, v167\n"
"v_mfma_f32_16x16x32_bf16 v[172:175], v[220:223], a[28:31], v[172:175]\n"
"v_exp_f32 v160, v160\n"
"v_mfma_f32_16x16x32_bf16 v[172:175], v[208:211], a[16:19], v[172:175]\n"
"ds_read_b64_tr_b16 a[254:255], v233 offset:4608\n"
"v_accvgpr_write_b32 a162, v18\n"
"v_mfma_f32_16x16x32_bf16 v[172:175], v[212:215], a[20:23], v[172:175]\n"
"s_cbranch_vccnz LPCE1_%=\nLPKE1_%=:\n"
"v_bfe_u32 v249, v242, 4, 2\n"
"v_mfma_f32_16x16x32_bf16 v[176:179], v[208:211], a[32:35], v[188:191]\n"
"v_accvgpr_write_b32 a106, v122\n"
"v_bfe_u32 v248, v242, 8, 2\n"
"v_mfma_f32_16x16x32_bf16 v[176:179], v[212:215], a[36:39], v[176:179]\n"
"ds_read_b64_tr_b16 a[252:253], v233 offset:512\n"
"v_accvgpr_write_b32 a161, v17\n"
"v_mfma_f32_16x16x32_bf16 v[176:179], v[216:219], a[40:43], v[176:179]\n"
"ds_read_b64_tr_b16 v[224:225], v237 offset:512\n"
"v_accvgpr_write_b32 a160, v16\n"
"v_mfma_f32_16x16x32_bf16 v[176:179], v[220:223], a[44:47], v[176:179]\n"
"s_waitcnt lgkmcnt(3)\n"
"ds_read_b64_tr_b16 v[226:227], v237 offset:4608\n"
"v_accvgpr_write_b32 a163, v19\n"
"v_lshlrev_b32 v248, 6, v248\n"
"v_exp_f32 v152, v152\n"
"v_exp_f32 v163, v163\n"
"v_exp_f32 v169, v169\n"
"v_mfma_f32_16x16x32_bf16 a[224:227], v[124:127], a[244:247], 0\n"
"v_exp_f32 v156, v156\n"
"v_mfma_f32_16x16x32_bf16 v[16:19], v[128:131], a[244:247], 0\n"
"s_waitcnt vmcnt(8)\n"
"v_xor_b32 v248, v248, v242\n"
"v_bfe_u32 v254, v242, 6, 4\n"
"v_mfma_f32_16x16x32_bf16 v[16:19], v[132:135], a[248:251], v[16:19]\n"
"s_barrier\n"
"ds_read_b128 v[216:219], v228 offset:33280\n"
"v_mul_u32_u24 v249, 20, v249\n"
"v_cvt_pk_bf16_f32 v101, v158, v159\n"
"v_exp_f32 v176, v176\n"
"v_exp_f32 v177, v177\n"
"v_mfma_f32_16x16x32_bf16 v[48:51], v[136:139], a[244:247], 0\n"
"ds_read_b128 v[220:223], v229 offset:33280\n"
"v_cvt_pk_bf16_f32 v109, v162, v163\n"
"v_mfma_f32_16x16x32_bf16 v[48:51], v[140:143], a[248:251], v[48:51]\n"
"ds_read_b128 v[208:211], v228 offset:32768\n"
"ds_read_b128 v[212:215], v229 offset:32768\n"
"v_mfma_f32_16x16x32_bf16 v[80:83], v[144:147], a[244:247], 0\n"
"ds_read_b64_tr_b16 a[246:247], v234 offset:20480\n"
"v_cvt_pk_bf16_f32 v122, v152, v153\n"
"v_mfma_f32_16x16x32_bf16 v[80:83], v[148:151], a[248:251], v[80:83]\n"
"s_waitcnt lgkmcnt(5)\n"
"ds_read_b64_tr_b16 a[250:251], v238 offset:20480\n"
"v_cvt_pk_bf16_f32 v123, v154, v155\n"
"v_mfma_f32_16x16x32_bf16 v[84:87], v[148:151], v[224:227], 0\n"
"v_cvt_pk_bf16_f32 v116, v164, v165\n"
"v_cvt_pk_bf16_f32 v117, v166, v167\n"
"v_mfma_f32_16x16x32_bf16 v[84:87], v[144:147], a[252:255], v[84:87]\n"
"v_xor_b32 v249, v249, v254\n"
"v_cvt_pk_bf16_f32 v118, v176, v177\n"
"v_mfma_f32_16x16x32_bf16 v[52:55], v[140:143], v[224:227], 0\n"
"v_lshl_add_u32 v249, v249, 4, s44\n"
"v_cvt_pk_bf16_f32 v100, v156, v157\n"
"v_mfma_f32_16x16x32_bf16 v[52:55], v[136:139], a[252:255], v[52:55]\n"
"v_exp_f32 v161, v161\n"
"v_mfma_f32_16x16x32_bf16 v[20:23], v[132:135], v[224:227], 0\n"
"v_exp_f32 v174, v174\n"
"v_mfma_f32_16x16x32_bf16 v[20:23], v[128:131], a[252:255], v[20:23]\n"
"v_exp_f32 v168, v168\n"
"v_mfma_f32_16x16x32_bf16 a[228:231], v[124:127], a[252:255], 0\n"
"s_waitcnt lgkmcnt(1)\n"
"ds_read_b64_tr_b16 a[254:255], v235 offset:20480\n"
"s_branch LAY2_%=\n"
"LAL_%=:\n"
"v_mfma_f32_16x16x32_bf16 a[224:227], v[124:127], a[244:247], a[224:227]\n"
"v_exp_f32 v162, v162\n"
"v_mfma_f32_16x16x32_bf16 v[16:19], v[128:131], a[244:247], v[16:19]\n"
"s_waitcnt vmcnt(8)\n"
"v_exp_f32 v168, v168\n"
"v_mfma_f32_16x16x32_bf16 v[16:19], v[132:135], a[248:251], v[16:19]\n"
"s_barrier\n"
"v_mfma_f32_16x16x32_bf16 v[48:51], v[136:139], a[244:247], v[48:51]\n"
"ds_read_b128 v[208:211], v228 offset:0\n"
"v_cvt_pk_bf16_f32 v101, v158, v159\n"
"v_mfma_f32_16x16x32_bf16 v[48:51], v[140:143], a[248:251], v[48:51]\n"
"ds_read_b128 v[216:219], v228 offset:512\n"
"v_cvt_pk_bf16_f32 v109, v162, v163\n"
"v_mfma_f32_16x16x32_bf16 v[80:83], v[144:147], a[244:247], v[80:83]\n"
"ds_read_b128 v[220:223], v229 offset:512\n"
"v_cvt_pk_bf16_f32 v123, v154, v155\n"
"v_mfma_f32_16x16x32_bf16 v[80:83], v[148:151], a[248:251], v[80:83]\n"
"ds_read_b128 v[212:215], v229 offset:0\n"
"v_cvt_pk_bf16_f32 v117, v166, v167\n"
"v_mfma_f32_16x16x32_bf16 v[84:87], v[148:151], v[224:227], v[84:87]\n"
"v_exp_f32 v176, v176\n"
"v_mfma_f32_16x16x32_bf16 v[84:87], v[144:147], a[252:255], v[84:87]\n"
"v_exp_f32 v169, v169\n"
"v_mfma_f32_16x16x32_bf16 v[52:55], v[140:143], v[224:227], v[52:55]\n"
"ds_read_b64_tr_b16 a[246:247], v234 offset:53248\n"
"v_cvt_pk_bf16_f32 v116, v164, v165\n"
"v_mfma_f32_16x16x32_bf16 v[52:55], v[136:139], a[252:255], v[52:55]\n"
"v_exp_f32 v177, v177\n"
"v_mfma_f32_16x16x32_bf16 v[20:23], v[132:135], v[224:227], v[20:23]\n"
"ds_read_b64_tr_b16 a[250:251], v238 offset:53248\n"
"v_cvt_pk_bf16_f32 v118, v176, v177\n"
"v_mfma_f32_16x16x32_bf16 v[20:23], v[128:131], a[252:255], v[20:23]\n"
"v_exp_f32 v174, v174\n"
"v_mfma_f32_16x16x32_bf16 a[228:231], v[124:127], a[252:255], a[228:231]\n"
"s_waitcnt lgkmcnt(3)\n"
"ds_read_b64_tr_b16 a[254:255], v235 offset:53248\n"
"v_cvt_pk_bf16_f32 v100, v156, v157\n"
"v_mfma_f32_16x16x32_bf16 v[152:155], v[208:211], a[0:3], v[180:183]\n"
"s_waitcnt lgkmcnt(3)\n"
"ds_read_b64_tr_b16 a[248:249], v238 offset:49152\n"
"v_cvt_pk_bf16_f32 v102, v168, v169\n"
"v_mfma_f32_16x16x32_bf16 v[152:155], v[212:215], a[4:7], v[152:155]\n"
"v_exp_f32 v172, v172\n"
"v_mfma_f32_16x16x32_bf16 v[152:155], v[216:219], a[8:11], v[152:155]\n"
"ds_read_b64_tr_b16 a[244:245], v234 offset:49152\n"
"v_cvt_pk_bf16_f32 v108, v160, v161\n"
"v_mfma_f32_16x16x32_bf16 v[152:155], v[220:223], a[12:15], v[152:155]\n"
"v_exp_f32 v175, v175\n"
"v_mfma_f32_16x16x32_bf16 v[156:159], v[216:219], a[24:27], v[184:187]\n"
"ds_read_b64_tr_b16 a[252:253], v235 offset:49152\n"
"v_cvt_pk_bf16_f32 v111, v174, v175\n"
"v_mfma_f32_16x16x32_bf16 v[156:159], v[220:223], a[28:31], v[156:159]\n"
"ds_read_b64_tr_b16 v[224:225], v239 offset:49152\n"
"v_mfma_f32_16x16x32_bf16 v[156:159], v[208:211], a[16:19], v[156:159]\n"
"s_add_u32 m0, s44, 81920\n"
"ds_read_b64_tr_b16 v[226:227], v239 offset:53248\n"
"v_mfma_f32_16x16x32_bf16 v[156:159], v[212:215], a[20:23], v[156:159]\n"
"buffer_load_dwordx4 v240, s[20:23], s56 offen nt lds\n"
"v_mfma_f32_16x16x32_bf16 v[160:163], v[208:211], a[32:35], v[188:191]\n"
"v_exp_f32 v178, v178\n"
"v_mfma_f32_16x16x32_bf16 v[160:163], v[212:215], a[36:39], v[160:163]\n"
"v_exp_f32 v171, v171\n"
"v_mfma_f32_16x16x32_bf16 v[160:163], v[216:219], a[40:43], v[160:163]\n"
"s_add_u32 s40, s56, s52\n"
"v_mfma_f32_16x16x32_bf16 v[160:163], v[220:223], a[44:47], v[160:163]\n"
"buffer_load_dwordx4 v207, s[20:23], s40 offen offset:1024 nt lds\n"
"v_mfma_f32_16x16x32_bf16 v[164:167], v[216:219], a[200:203], v[192:195]\n"
"v_exp_f32 v170, v170\n"
"v_mfma_f32_16x16x32_bf16 v[164:167], v[220:223], a[204:207], v[164:167]\n"
"v_exp_f32 v153, v153\n"
"v_mfma_f32_16x16x32_bf16 v[164:167], v[208:211], a[192:195], v[164:167]\n"
"v_exp_f32 v179, v179\n"
"v_mfma_f32_16x16x32_bf16 v[164:167], v[212:215], a[196:199], v[164:167]\n"
"s_waitcnt lgkmcnt(0)\n"
"v_exp_f32 v158, v158\n"
"v_mfma_f32_16x16x32_bf16 a[216:219], v[120:123], a[244:247], a[216:219]\n"
"ds_read_b128 v[216:219], v228 offset:4608\n"
"v_cvt_pk_bf16_f32 v103, v170, v171\n"
"v_mfma_f32_16x16x32_bf16 v[8:11], v[96:99], a[244:247], v[8:11]\n"
"v_exp_f32 v173, v173\n"
"v_mfma_f32_16x16x32_bf16 v[8:11], v[100:103], a[248:251], v[8:11]\n"
"ds_read_b128 v[208:211], v228 offset:4096\n"
"v_cvt_pk_bf16_f32 v110, v172, v173\n"
"v_mfma_f32_16x16x32_bf16 v[40:43], v[104:107], a[244:247], v[40:43]\n"
"v_exp_f32 v162, v162\n"
"v_mfma_f32_16x16x32_bf16 v[40:43], v[108:111], a[248:251], v[40:43]\n"
"ds_read_b128 v[220:223], v229 offset:4608\n"
"v_cvt_pk_bf16_f32 v119, v178, v179\n"
"v_mfma_f32_16x16x32_bf16 v[72:75], v[112:115], a[244:247], v[72:75]\n"
"s_add_u32 s40, s56, s53\n"
"ds_read_b128 v[212:215], v229 offset:4096\n"
"v_mfma_f32_16x16x32_bf16 v[72:75], v[116:119], a[248:251], v[72:75]\n"
"buffer_load_dwordx4 v240, s[20:23], s40 offen offset:2048 nt lds\n"
"v_mfma_f32_16x16x32_bf16 v[76:79], v[116:119], v[224:227], v[76:79]\n"
"v_exp_f32 v163, v163\n"
"v_mfma_f32_16x16x32_bf16 v[76:79], v[112:115], a[252:255], v[76:79]\n"
"v_exp_f32 v156, v156\n"
"v_mfma_f32_16x16x32_bf16 v[44:47], v[108:111], v[224:227], v[44:47]\n"
"v_exp_f32 v160, v160\n"
"v_mfma_f32_16x16x32_bf16 v[44:47], v[104:107], a[252:255], v[44:47]\n"
"v_exp_f32 v155, v155\n"
"v_mfma_f32_16x16x32_bf16 v[12:15], v[100:103], v[224:227], v[12:15]\n"
"ds_read_b64_tr_b16 v[226:227], v239 offset:53760\n"
"v_mfma_f32_16x16x32_bf16 v[12:15], v[96:99], a[252:255], v[12:15]\n"
"ds_read_b64_tr_b16 a[248:249], v238 offset:49664\n"
"v_mfma_f32_16x16x32_bf16 a[220:223], v[120:123], a[252:255], a[220:223]\n"
"v_exp_f32 v154, v154\n"
"v_smfmac_f32_16x16x64_bf16 v[196:199], a[240:243], v[96:103], v241\n"
"ds_read_b64_tr_b16 a[250:251], v238 offset:53760\n"
"v_mfma_f32_16x16x32_bf16 v[202:205], a[240:243], v[120:123], v[202:205]\n"
"s_waitcnt lgkmcnt(3)\n"
"v_exp_f32 v152, v152\n"
"v_mfma_f32_16x16x32_bf16 v[168:171], v[216:219], a[8:11], v[180:183]\n"
"ds_read_b64_tr_b16 a[244:245], v234 offset:49664\n"
"v_cvt_pk_bf16_f32 v128, v152, v153\n"
"v_mfma_f32_16x16x32_bf16 v[168:171], v[220:223], a[12:15], v[168:171]\n"
"v_exp_f32 v165, v165\n"
"v_mfma_f32_16x16x32_bf16 v[168:171], v[208:211], a[0:3], v[168:171]\n"
"ds_read_b64_tr_b16 a[246:247], v234 offset:53760\n"
"v_cvt_pk_bf16_f32 v145, v162, v163\n"
"v_mfma_f32_16x16x32_bf16 v[168:171], v[212:215], a[4:7], v[168:171]\n"
"ds_read_b64_tr_b16 a[254:255], v235 offset:53760\n"
"v_mfma_f32_16x16x32_bf16 v[172:175], v[208:211], a[16:19], v[184:187]\n"
"ds_read_b64_tr_b16 v[224:225], v239 offset:49664\n"
"v_cvt_pk_bf16_f32 v129, v154, v155\n"
"v_mfma_f32_16x16x32_bf16 v[172:175], v[212:215], a[20:23], v[172:175]\n"
"v_mfma_f32_16x16x32_bf16 v[172:175], v[216:219], a[24:27], v[172:175]\n"
"s_add_u32 s40, s56, s54\n"
"ds_read_b64_tr_b16 a[252:253], v235 offset:49664\n"
"v_mfma_f32_16x16x32_bf16 v[172:175], v[220:223], a[28:31], v[172:175]\n"
"buffer_load_dwordx4 v207, s[20:23], s40 offen offset:3072 nt lds\n"
"v_mfma_f32_16x16x32_bf16 v[176:179], v[216:219], a[40:43], v[188:191]\n"
"s_add_u32 s55, s55, s59\n"
"v_exp_f32 v157, v157\n"
"v_mfma_f32_16x16x32_bf16 v[176:179], v[220:223], a[44:47], v[176:179]\n"
"s_or_b32 s55, s55, s58\n"
"v_exp_f32 v171, v171\n"
"v_mfma_f32_16x16x32_bf16 v[176:179], v[208:211], a[32:35], v[176:179]\n"
"s_xor_b32 s56, s55, s57\n"
"v_exp_f32 v159, v159\n"
"v_mfma_f32_16x16x32_bf16 v[176:179], v[212:215], a[36:39], v[176:179]\n"
"v_exp_f32 v167, v167\n"
"v_mfma_f32_16x16x32_bf16 v[152:155], v[208:211], a[192:195], v[192:195]\n"
"v_cvt_pk_bf16_f32 v137, v158, v159\n"
"v_mfma_f32_16x16x32_bf16 v[152:155], v[212:215], a[196:199], v[152:155]\n"
"ds_read_b128 v[208:211], v230 offset:0\n"
"v_cvt_pk_bf16_f32 v136, v156, v157\n"
"v_mfma_f32_16x16x32_bf16 v[152:155], v[216:219], a[200:203], v[152:155]\n"
"v_exp_f32 v166, v166\n"
"v_mfma_f32_16x16x32_bf16 v[152:155], v[220:223], a[204:207], v[152:155]\n"
"s_waitcnt lgkmcnt(1)\n"
"ds_read_b128 v[216:219], v230 offset:512\n"
"v_mfma_f32_16x16x32_bf16 a[232:235], v[120:123], a[244:247], a[232:235]\n"
"v_exp_f32 v169, v169\n"
"v_mfma_f32_16x16x32_bf16 v[24:27], v[96:99], a[244:247], v[24:27]\n"
"ds_read_b128 v[220:223], v231 offset:512\n"
"v_mfma_f32_16x16x32_bf16 v[24:27], v[100:103], a[248:251], v[24:27]\n"
"v_exp_f32 v164, v164\n"
"v_mfma_f32_16x16x32_bf16 v[56:59], v[104:107], a[244:247], v[56:59]\n"
"ds_read_b128 v[212:215], v231 offset:0\n"
"v_mfma_f32_16x16x32_bf16 v[56:59], v[108:111], a[248:251], v[56:59]\n"
"v_exp_f32 v178, v178\n"
"v_mfma_f32_16x16x32_bf16 v[88:91], v[112:115], a[244:247], v[88:91]\n"
"s_add_u32 m0, s44, 32768\n"
"ds_read_b64_tr_b16 a[246:247], v232 offset:53248\n"
"v_mfma_f32_16x16x32_bf16 v[88:91], v[116:119], a[248:251], v[88:91]\n"
"buffer_load_dwordx4 v240, s[16:19], s56 offen nt lds\n"
"v_mfma_f32_16x16x32_bf16 v[92:95], v[116:119], v[224:227], v[92:95]\n"
"v_exp_f32 v175, v175\n"
"v_mfma_f32_16x16x32_bf16 v[92:95], v[112:115], a[252:255], v[92:95]\n"
"v_exp_f32 v168, v168\n"
"v_mfma_f32_16x16x32_bf16 v[60:63], v[108:111], v[224:227], v[60:63]\n"
"v_exp_f32 v161, v161\n"
"v_mfma_f32_16x16x32_bf16 v[60:63], v[104:107], a[252:255], v[60:63]\n"
"v_exp_f32 v177, v177\n"
"v_mfma_f32_16x16x32_bf16 v[28:31], v[100:103], v[224:227], v[28:31]\n"
"ds_read_b64_tr_b16 v[226:227], v237 offset:53248\n"
"v_mfma_f32_16x16x32_bf16 v[28:31], v[96:99], a[252:255], v[28:31]\n"
"v_exp_f32 v172, v172\n"
"v_mfma_f32_16x16x32_bf16 a[236:239], v[120:123], a[252:255], a[236:239]\n"
"ds_read_b64_tr_b16 a[250:251], v236 offset:53248\n"
"v_cvt_pk_bf16_f32 v124, v164, v165\n"
"v_smfmac_f32_16x16x64_bf16 v[198:201], a[240:243], v[104:111], v241\n"
"s_waitcnt lgkmcnt(2)\n"
"ds_read_b64_tr_b16 a[252:253], v233 offset:49152\n"
"v_mfma_f32_16x16x32_bf16 v[156:159], v[208:211], a[0:3], v[180:183]\n"
"ds_read_b64_tr_b16 a[248:249], v236 offset:49152\n"
"v_cvt_pk_bf16_f32 v125, v166, v167\n"
"v_mfma_f32_16x16x32_bf16 v[156:159], v[212:215], a[4:7], v[156:159]\n"
"ds_read_b64_tr_b16 v[224:225], v237 offset:49152\n"
"v_cvt_pk_bf16_f32 v130, v168, v169\n"
"v_mfma_f32_16x16x32_bf16 v[156:159], v[216:219], a[8:11], v[156:159]\n"
"ds_read_b64_tr_b16 a[254:255], v233 offset:53248\n"
"v_cvt_pk_bf16_f32 v144, v160, v161\n"
"v_mfma_f32_16x16x32_bf16 v[156:159], v[220:223], a[12:15], v[156:159]\n"
"ds_read_b64_tr_b16 a[244:245], v232 offset:49152\n"
"v_mfma_f32_16x16x32_bf16 v[160:163], v[216:219], a[24:27], v[184:187]\n"
"v_exp_f32 v176, v176\n"
"v_mfma_f32_16x16x32_bf16 v[160:163], v[220:223], a[28:31], v[160:163]\n"
"v_exp_f32 v152, v152\n"
"v_mfma_f32_16x16x32_bf16 v[160:163], v[208:211], a[16:19], v[160:163]\n"
"s_add_u32 s40, s56, s52\n"
"v_cvt_pk_bf16_f32 v146, v176, v177\n"
"v_mfma_f32_16x16x32_bf16 v[160:163], v[212:215], a[20:23], v[160:163]\n"
"buffer_load_dwordx4 v207, s[16:19], s40 offen offset:1024 nt lds\n"
"v_mfma_f32_16x16x32_bf16 v[164:167], v[208:211], a[32:35], v[188:191]\n"
"v_exp_f32 v170, v170\n"
"v_mfma_f32_16x16x32_bf16 v[164:167], v[212:215], a[36:39], v[164:167]\n"
"v_exp_f32 v173, v173\n"
"v_mfma_f32_16x16x32_bf16 v[164:167], v[216:219], a[40:43], v[164:167]\n"
"v_exp_f32 v153, v153\n"
"v_mfma_f32_16x16x32_bf16 v[164:167], v[220:223], a[44:47], v[164:167]\n"
"s_waitcnt lgkmcnt(0)\n"
"v_exp_f32 v174, v174\n"
"v_mfma_f32_16x16x32_bf16 a[208:211], v[120:123], a[244:247], a[208:211]\n"
"ds_read_b128 v[208:211], v230 offset:4096\n"
"v_cvt_pk_bf16_f32 v139, v174, v175\n"
"v_mfma_f32_16x16x32_bf16 v[0:3], v[96:99], a[244:247], v[0:3]\n"
"v_exp_f32 v156, v156\n"
"v_mfma_f32_16x16x32_bf16 v[0:3], v[100:103], a[248:251], v[0:3]\n"
"ds_read_b128 v[220:223], v231 offset:4608\n"
"v_cvt_pk_bf16_f32 v138, v172, v173\n"
"v_mfma_f32_16x16x32_bf16 v[32:35], v[104:107], a[244:247], v[32:35]\n"
"v_exp_f32 v161, v161\n"
"v_mfma_f32_16x16x32_bf16 v[32:35], v[108:111], a[248:251], v[32:35]\n"
"ds_read_b128 v[216:219], v230 offset:4608\n"
"v_cvt_pk_bf16_f32 v126, v152, v153\n"
"v_mfma_f32_16x16x32_bf16 v[64:67], v[112:115], a[244:247], v[64:67]\n"
"s_add_u32 s40, s56, s53\n"
"ds_read_b128 v[212:215], v231 offset:4096\n"
"v_mfma_f32_16x16x32_bf16 v[64:67], v[116:119], a[248:251], v[64:67]\n"
"buffer_load_dwordx4 v240, s[16:19], s40 offen offset:2048 nt lds\n"
"v_mfma_f32_16x16x32_bf16 v[68:71], v[116:119], v[224:227], v[68:71]\n"
"v_exp_f32 v159, v159\n"
"v_mfma_f32_16x16x32_bf16 v[68:71], v[112:115], a[252:255], v[68:71]\n"
"v_exp_f32 v155, v155\n"
"v_mfma_f32_16x16x32_bf16 v[36:39], v[108:111], v[224:227], v[36:39]\n"
"v_exp_f32 v160, v160\n"
"v_mfma_f32_16x16x32_bf16 v[36:39], v[104:107], a[252:255], v[36:39]\n"
"v_exp_f32 v179, v179\n"
"v_mfma_f32_16x16x32_bf16 v[4:7], v[100:103], v[224:227], v[4:7]\n"
"ds_read_b64_tr_b16 a[244:245], v232 offset:49664\n"
"ds_read_b64_tr_b16 v[224:225], v237 offset:49664\n"
"v_mfma_f32_16x16x32_bf16 v[4:7], v[96:99], a[252:255], v[4:7]\n"
"v_exp_f32 v158, v158\n"
"v_mfma_f32_16x16x32_bf16 a[212:215], v[120:123], a[252:255], a[212:215]\n"
"ds_read_b64_tr_b16 a[246:247], v232 offset:53760\n"
"ds_read_b64_tr_b16 a[250:251], v236 offset:53760\n"
"v_smfmac_f32_16x16x64_bf16 v[200:203], a[240:243], v[112:119], v241\n"
"s_waitcnt lgkmcnt(4)\n"
"ds_read_b64_tr_b16 v[226:227], v237 offset:53760\n"
"v_cvt_pk_bf16_f32 v131, v170, v171\n"
"v_mfma_f32_16x16x32_bf16 v[168:171], v[208:211], a[0:3], v[180:183]\n"
"v_exp_f32 v164, v164\n"
"v_mfma_f32_16x16x32_bf16 v[168:171], v[212:215], a[4:7], v[168:171]\n"
"ds_read_b64_tr_b16 a[248:249], v236 offset:49664\n"
"ds_read_b64_tr_b16 a[254:255], v233 offset:53760\n"
"v_mfma_f32_16x16x32_bf16 v[168:171], v[216:219], a[8:11], v[168:171]\n"
"s_add_u32 s40, s56, s54\n"
"ds_read_b64_tr_b16 a[252:253], v233 offset:49664\n"
"v_mfma_f32_16x16x32_bf16 v[168:171], v[220:223], a[12:15], v[168:171]\n"
"buffer_load_dwordx4 v207, s[16:19], s40 offen offset:3072 nt lds\n"
"v_mfma_f32_16x16x32_bf16 v[172:175], v[216:219], a[24:27], v[184:187]\n"
"v_exp_f32 v154, v154\n"
"v_mfma_f32_16x16x32_bf16 v[172:175], v[220:223], a[28:31], v[172:175]\n"
"v_exp_f32 v167, v167\n"
"v_mfma_f32_16x16x32_bf16 v[172:175], v[208:211], a[16:19], v[172:175]\n"
"v_exp_f32 v163, v163\n"
"v_mfma_f32_16x16x32_bf16 v[172:175], v[212:215], a[20:23], v[172:175]\n"
"s_cbranch_vccnz LPCL0_%=\nLPKL0_%=:\n"
"v_cvt_pk_bf16_f32 v147, v178, v179\n"
"v_mfma_f32_16x16x32_bf16 v[176:179], v[208:211], a[32:35], v[188:191]\n"
"v_exp_f32 v165, v165\n"
"v_mfma_f32_16x16x32_bf16 v[176:179], v[212:215], a[36:39], v[176:179]\n"
"v_exp_f32 v157, v157\n"
"v_mfma_f32_16x16x32_bf16 v[176:179], v[216:219], a[40:43], v[176:179]\n"
"v_exp_f32 v166, v166\n"
"v_mfma_f32_16x16x32_bf16 v[176:179], v[220:223], a[44:47], v[176:179]\n"
"s_waitcnt lgkmcnt(0)\n"
"v_mfma_f32_16x16x32_bf16 a[224:227], v[120:123], a[244:247], a[224:227]\n"
"v_exp_f32 v162, v162\n"
"v_mfma_f32_16x16x32_bf16 v[16:19], v[96:99], a[244:247], v[16:19]\n"
"s_waitcnt vmcnt(8)\n"
"v_exp_f32 v168, v168\n"
"v_mfma_f32_16x16x32_bf16 v[16:19], v[100:103], a[248:251], v[16:19]\n"
"s_barrier\n"
"v_mfma_f32_16x16x32_bf16 v[48:51], v[104:107], a[244:247], v[48:51]\n"
"ds_read_b128 v[208:211], v228 offset:16384\n"
"v_cvt_pk_bf16_f32 v133, v158, v159\n"
"v_mfma_f32_16x16x32_bf16 v[48:51], v[108:111], a[248:251], v[48:51]\n"
"ds_read_b128 v[216:219], v228 offset:16896\n"
"v_cvt_pk_bf16_f32 v141, v162, v163\n"
"v_mfma_f32_16x16x32_bf16 v[80:83], v[112:115], a[244:247], v[80:83]\n"
"ds_read_b128 v[220:223], v229 offset:16896\n"
"v_cvt_pk_bf16_f32 v127, v154, v155\n"
"v_mfma_f32_16x16x32_bf16 v[80:83], v[116:119], a[248:251], v[80:83]\n"
"ds_read_b128 v[212:215], v229 offset:16384\n"
"v_cvt_pk_bf16_f32 v149, v166, v167\n"
"v_mfma_f32_16x16x32_bf16 v[84:87], v[116:119], v[224:227], v[84:87]\n"
"v_exp_f32 v176, v176\n"
"v_mfma_f32_16x16x32_bf16 v[84:87], v[112:115], a[252:255], v[84:87]\n"
"v_exp_f32 v169, v169\n"
"v_mfma_f32_16x16x32_bf16 v[52:55], v[108:111], v[224:227], v[52:55]\n"
"ds_read_b64_tr_b16 a[246:247], v234 offset:4096\n"
"v_cvt_pk_bf16_f32 v148, v164, v165\n"
"v_mfma_f32_16x16x32_bf16 v[52:55], v[104:107], a[252:255], v[52:55]\n"
"v_exp_f32 v177, v177\n"
"v_mfma_f32_16x16x32_bf16 v[20:23], v[100:103], v[224:227], v[20:23]\n"
"ds_read_b64_tr_b16 a[250:251], v238 offset:4096\n"
"v_cvt_pk_bf16_f32 v150, v176, v177\n"
"v_mfma_f32_16x16x32_bf16 v[20:23], v[96:99], a[252:255], v[20:23]\n"
"v_exp_f32 v174, v174\n"
"v_mfma_f32_16x16x32_bf16 a[228:231], v[120:123], a[252:255], a[228:231]\n"
"s_waitcnt lgkmcnt(3)\n"
"ds_read_b64_tr_b16 a[254:255], v235 offset:4096\n"
"v_cvt_pk_bf16_f32 v132, v156, v157\n"
"v_mfma_f32_16x16x32_bf16 v[152:155], v[208:211], a[0:3], v[180:183]\n"
"s_waitcnt lgkmcnt(3)\n"
"ds_read_b64_tr_b16 a[248:249], v238 offset:0\n"
"v_cvt_pk_bf16_f32 v134, v168, v169\n"
"v_mfma_f32_16x16x32_bf16 v[152:155], v[212:215], a[4:7], v[152:155]\n"
"v_exp_f32 v172, v172\n"
"v_mfma_f32_16x16x32_bf16 v[152:155], v[216:219], a[8:11], v[152:155]\n"
"ds_read_b64_tr_b16 a[244:245], v234 offset:0\n"
"v_cvt_pk_bf16_f32 v140, v160, v161\n"
"v_mfma_f32_16x16x32_bf16 v[152:155], v[220:223], a[12:15], v[152:155]\n"
"v_exp_f32 v175, v175\n"
"v_mfma_f32_16x16x32_bf16 v[156:159], v[216:219], a[24:27], v[184:187]\n"
"ds_read_b64_tr_b16 a[252:253], v235 offset:0\n"
"v_cvt_pk_bf16_f32 v143, v174, v175\n"
"v_mfma_f32_16x16x32_bf16 v[156:159], v[220:223], a[28:31], v[156:159]\n"
"ds_read_b64_tr_b16 v[224:225], v239 offset:0\n"
"v_mfma_f32_16x16x32_bf16 v[156:159], v[208:211], a[16:19], v[156:159]\n"
"s_add_u32 m0, s44, 98304\n"
"ds_read_b64_tr_b16 v[226:227], v239 offset:4096\n"
"v_mfma_f32_16x16x32_bf16 v[156:159], v[212:215], a[20:23], v[156:159]\n"
"buffer_load_dwordx4 v240, s[20:23], s56 offen nt lds\n"
"v_mfma_f32_16x16x32_bf16 v[160:163], v[208:211], a[32:35], v[188:191]\n"
"v_exp_f32 v178, v178\n"
"v_mfma_f32_16x16x32_bf16 v[160:163], v[212:215], a[36:39], v[160:163]\n"
"v_exp_f32 v171, v171\n"
"v_mfma_f32_16x16x32_bf16 v[160:163], v[216:219], a[40:43], v[160:163]\n"
"s_add_u32 s40, s56, s52\n"
"v_mfma_f32_16x16x32_bf16 v[160:163], v[220:223], a[44:47], v[160:163]\n"
"buffer_load_dwordx4 v207, s[20:23], s40 offen offset:1024 nt lds\n"
"v_mfma_f32_16x16x32_bf16 v[164:167], v[216:219], a[200:203], v[192:195]\n"
"v_exp_f32 v170, v170\n"
"v_mfma_f32_16x16x32_bf16 v[164:167], v[220:223], a[204:207], v[164:167]\n"
"v_exp_f32 v153, v153\n"
"v_mfma_f32_16x16x32_bf16 v[164:167], v[208:211], a[192:195], v[164:167]\n"
"v_exp_f32 v179, v179\n"
"v_mfma_f32_16x16x32_bf16 v[164:167], v[212:215], a[196:199], v[164:167]\n"
"s_waitcnt lgkmcnt(0)\n"
"v_exp_f32 v158, v158\n"
"v_mfma_f32_16x16x32_bf16 a[216:219], v[124:127], a[244:247], a[216:219]\n"
"ds_read_b128 v[216:219], v228 offset:20992\n"
"v_cvt_pk_bf16_f32 v135, v170, v171\n"
"v_mfma_f32_16x16x32_bf16 v[8:11], v[128:131], a[244:247], v[8:11]\n"
"v_exp_f32 v173, v173\n"
"v_mfma_f32_16x16x32_bf16 v[8:11], v[132:135], a[248:251], v[8:11]\n"
"ds_read_b128 v[208:211], v228 offset:20480\n"
"v_cvt_pk_bf16_f32 v142, v172, v173\n"
"v_mfma_f32_16x16x32_bf16 v[40:43], v[136:139], a[244:247], v[40:43]\n"
"v_exp_f32 v162, v162\n"
"v_mfma_f32_16x16x32_bf16 v[40:43], v[140:143], a[248:251], v[40:43]\n"
"ds_read_b128 v[220:223], v229 offset:20992\n"
"v_cvt_pk_bf16_f32 v151, v178, v179\n"
"v_mfma_f32_16x16x32_bf16 v[72:75], v[144:147], a[244:247], v[72:75]\n"
"s_add_u32 s40, s56, s53\n"
"ds_read_b128 v[212:215], v229 offset:20480\n"
"v_mfma_f32_16x16x32_bf16 v[72:75], v[148:151], a[248:251], v[72:75]\n"
"buffer_load_dwordx4 v240, s[20:23], s40 offen offset:2048 nt lds\n"
"v_mfma_f32_16x16x32_bf16 v[76:79], v[148:151], v[224:227], v[76:79]\n"
"v_exp_f32 v163, v163\n"
"v_mfma_f32_16x16x32_bf16 v[76:79], v[144:147], a[252:255], v[76:79]\n"
"v_exp_f32 v156, v156\n"
"v_mfma_f32_16x16x32_bf16 v[44:47], v[140:143], v[224:227], v[44:47]\n"
"v_exp_f32 v160, v160\n"
"v_mfma_f32_16x16x32_bf16 v[44:47], v[136:139], a[252:255], v[44:47]\n"
"v_exp_f32 v155, v155\n"
"v_mfma_f32_16x16x32_bf16 v[12:15], v[132:135], v[224:227], v[12:15]\n"
"ds_read_b64_tr_b16 v[226:227], v239 offset:4608\n"
"v_mfma_f32_16x16x32_bf16 v[12:15], v[128:131], a[252:255], v[12:15]\n"
"ds_read_b64_tr_b16 a[248:249], v238 offset:512\n"
"v_mfma_f32_16x16x32_bf16 a[220:223], v[124:127], a[252:255], a[220:223]\n"
"v_exp_f32 v154, v154\n"
"v_smfmac_f32_16x16x64_bf16 v[196:199], a[240:243], v[128:135], v241\n"
"ds_read_b64_tr_b16 a[250:251], v238 offset:4608\n"
"v_mfma_f32_16x16x32_bf16 v[202:205], a[240:243], v[124:127], v[202:205]\n"
"s_waitcnt lgkmcnt(3)\n"
"v_exp_f32 v152, v152\n"
"v_mfma_f32_16x16x32_bf16 v[168:171], v[216:219], a[8:11], v[180:183]\n"
"ds_read_b64_tr_b16 a[244:245], v234 offset:512\n"
"v_cvt_pk_bf16_f32 v96, v152, v153\n"
"v_mfma_f32_16x16x32_bf16 v[168:171], v[220:223], a[12:15], v[168:171]\n"
"v_exp_f32 v165, v165\n"
"v_mfma_f32_16x16x32_bf16 v[168:171], v[208:211], a[0:3], v[168:171]\n"
"ds_read_b64_tr_b16 a[246:247], v234 offset:4608\n"
"v_cvt_pk_bf16_f32 v113, v162, v163\n"
"v_mfma_f32_16x16x32_bf16 v[168:171], v[212:215], a[4:7], v[168:171]\n"
"ds_read_b64_tr_b16 a[254:255], v235 offset:4608\n"
"v_mfma_f32_16x16x32_bf16 v[172:175], v[208:211], a[16:19], v[184:187]\n"
"ds_read_b64_tr_b16 v[224:225], v239 offset:512\n"
"v_cvt_pk_bf16_f32 v97, v154, v155\n"
"v_mfma_f32_16x16x32_bf16 v[172:175], v[212:215], a[20:23], v[172:175]\n"
"v_mfma_f32_16x16x32_bf16 v[172:175], v[216:219], a[24:27], v[172:175]\n"
"s_add_u32 s40, s56, s54\n"
"ds_read_b64_tr_b16 a[252:253], v235 offset:512\n"
"v_mfma_f32_16x16x32_bf16 v[172:175], v[220:223], a[28:31], v[172:175]\n"
"buffer_load_dwordx4 v207, s[20:23], s40 offen offset:3072 nt lds\n"
"v_mfma_f32_16x16x32_bf16 v[176:179], v[216:219], a[40:43], v[188:191]\n"
"s_add_u32 s55, s55, s59\n"
"v_exp_f32 v157, v157\n"
"v_mfma_f32_16x16x32_bf16 v[176:179], v[220:223], a[44:47], v[176:179]\n"
"s_or_b32 s55, s55, s58\n"
"v_exp_f32 v171, v171\n"
"v_mfma_f32_16x16x32_bf16 v[176:179], v[208:211], a[32:35], v[176:179]\n"
"s_xor_b32 s56, s55, s57\n"
"v_exp_f32 v159, v159\n"
"v_mfma_f32_16x16x32_bf16 v[176:179], v[212:215], a[36:39], v[176:179]\n"
"v_exp_f32 v167, v167\n"
"v_mfma_f32_16x16x32_bf16 v[152:155], v[208:211], a[192:195], v[192:195]\n"
"v_cvt_pk_bf16_f32 v105, v158, v159\n"
"v_mfma_f32_16x16x32_bf16 v[152:155], v[212:215], a[196:199], v[152:155]\n"
"ds_read_b128 v[208:211], v230 offset:16384\n"
"v_cvt_pk_bf16_f32 v104, v156, v157\n"
"v_mfma_f32_16x16x32_bf16 v[152:155], v[216:219], a[200:203], v[152:155]\n"
"v_exp_f32 v166, v166\n"
"v_mfma_f32_16x16x32_bf16 v[152:155], v[220:223], a[204:207], v[152:155]\n"
"s_waitcnt lgkmcnt(1)\n"
"ds_read_b128 v[216:219], v230 offset:16896\n"
"v_mfma_f32_16x16x32_bf16 a[232:235], v[124:127], a[244:247], a[232:235]\n"
"v_exp_f32 v169, v169\n"
"v_mfma_f32_16x16x32_bf16 v[24:27], v[128:131], a[244:247], v[24:27]\n"
"ds_read_b128 v[220:223], v231 offset:16896\n"
"v_mfma_f32_16x16x32_bf16 v[24:27], v[132:135], a[248:251], v[24:27]\n"
"v_exp_f32 v164, v164\n"
"v_mfma_f32_16x16x32_bf16 v[56:59], v[136:139], a[244:247], v[56:59]\n"
"ds_read_b128 v[212:215], v231 offset:16384\n"
"v_mfma_f32_16x16x32_bf16 v[56:59], v[140:143], a[248:251], v[56:59]\n"
"v_exp_f32 v178, v178\n"
"v_mfma_f32_16x16x32_bf16 v[88:91], v[144:147], a[244:247], v[88:91]\n"
"s_add_u32 m0, s44, 49152\n"
"ds_read_b64_tr_b16 a[246:247], v232 offset:4096\n"
"v_mfma_f32_16x16x32_bf16 v[88:91], v[148:151], a[248:251], v[88:91]\n"
"buffer_load_dwordx4 v240, s[16:19], s56 offen nt lds\n"
"v_mfma_f32_16x16x32_bf16 v[92:95], v[148:151], v[224:227], v[92:95]\n"
"v_exp_f32 v175, v175\n"
"v_mfma_f32_16x16x32_bf16 v[92:95], v[144:147], a[252:255], v[92:95]\n"
"v_exp_f32 v168, v168\n"
"v_mfma_f32_16x16x32_bf16 v[60:63], v[140:143], v[224:227], v[60:63]\n"
"v_exp_f32 v161, v161\n"
"v_mfma_f32_16x16x32_bf16 v[60:63], v[136:139], a[252:255], v[60:63]\n"
"v_exp_f32 v177, v177\n"
"v_mfma_f32_16x16x32_bf16 v[28:31], v[132:135], v[224:227], v[28:31]\n"
"ds_read_b64_tr_b16 v[226:227], v237 offset:4096\n"
"v_mfma_f32_16x16x32_bf16 v[28:31], v[128:131], a[252:255], v[28:31]\n"
"v_exp_f32 v172, v172\n"
"v_mfma_f32_16x16x32_bf16 a[236:239], v[124:127], a[252:255], a[236:239]\n"
"ds_read_b64_tr_b16 a[250:251], v236 offset:4096\n"
"v_cvt_pk_bf16_f32 v120, v164, v165\n"
"v_smfmac_f32_16x16x64_bf16 v[198:201], a[240:243], v[136:143], v241\n"
"s_waitcnt lgkmcnt(2)\n"
"ds_read_b64_tr_b16 a[252:253], v233 offset:0\n"
"v_mfma_f32_16x16x32_bf16 v[156:159], v[208:211], a[0:3], v[180:183]\n"
"ds_read_b64_tr_b16 a[248:249], v236 offset:0\n"
"v_cvt_pk_bf16_f32 v121, v166, v167\n"
"v_mfma_f32_16x16x32_bf16 v[156:159], v[212:215], a[4:7], v[156:159]\n"
"ds_read_b64_tr_b16 v[224:225], v237 offset:0\n"
"v_cvt_pk_bf16_f32 v98, v168, v169\n"
"v_mfma_f32_16x16x32_bf16 v[156:159], v[216:219], a[8:11], v[156:159]\n"
"ds_read_b64_tr_b16 a[254:255], v233 offset:4096\n"
"v_cvt_pk_bf16_f32 v112, v160, v161\n"
"v_mfma_f32_16x16x32_bf16 v[156:159], v[220:223], a[12:15], v[156:159]\n"
"ds_read_b64_tr_b16 a[244:245], v232 offset:0\n"
"v_mfma_f32_16x16x32_bf16 v[160:163], v[216:219], a[24:27], v[184:187]\n"
"v_exp_f32 v176, v176\n"
"v_mfma_f32_16x16x32_bf16 v[160:163], v[220:223], a[28:31], v[160:163]\n"
"v_exp_f32 v152, v152\n"
"v_mfma_f32_16x16x32_bf16 v[160:163], v[208:211], a[16:19], v[160:163]\n"
"s_add_u32 s40, s56, s52\n"
"v_cvt_pk_bf16_f32 v114, v176, v177\n"
"v_mfma_f32_16x16x32_bf16 v[160:163], v[212:215], a[20:23], v[160:163]\n"
"buffer_load_dwordx4 v207, s[16:19], s40 offen offset:1024 nt lds\n"
"v_mfma_f32_16x16x32_bf16 v[164:167], v[208:211], a[32:35], v[188:191]\n"
"v_exp_f32 v170, v170\n"
"v_mfma_f32_16x16x32_bf16 v[164:167], v[212:215], a[36:39], v[164:167]\n"
"v_exp_f32 v173, v173\n"
"v_mfma_f32_16x16x32_bf16 v[164:167], v[216:219], a[40:43], v[164:167]\n"
"v_exp_f32 v153, v153\n"
"v_mfma_f32_16x16x32_bf16 v[164:167], v[220:223], a[44:47], v[164:167]\n"
"s_waitcnt lgkmcnt(0)\n"
"v_exp_f32 v174, v174\n"
"v_mfma_f32_16x16x32_bf16 a[208:211], v[124:127], a[244:247], a[208:211]\n"
"ds_read_b128 v[208:211], v230 offset:20480\n"
"v_cvt_pk_bf16_f32 v107, v174, v175\n"
"v_mfma_f32_16x16x32_bf16 v[0:3], v[128:131], a[244:247], v[0:3]\n"
"v_exp_f32 v156, v156\n"
"v_mfma_f32_16x16x32_bf16 v[0:3], v[132:135], a[248:251], v[0:3]\n"
"ds_read_b128 v[220:223], v231 offset:20992\n"
"v_cvt_pk_bf16_f32 v106, v172, v173\n"
"v_mfma_f32_16x16x32_bf16 v[32:35], v[136:139], a[244:247], v[32:35]\n"
"v_exp_f32 v161, v161\n"
"v_mfma_f32_16x16x32_bf16 v[32:35], v[140:143], a[248:251], v[32:35]\n"
"ds_read_b128 v[216:219], v230 offset:20992\n"
"v_cvt_pk_bf16_f32 v122, v152, v153\n"
"v_mfma_f32_16x16x32_bf16 v[64:67], v[144:147], a[244:247], v[64:67]\n"
"s_add_u32 s40, s56, s53\n"
"ds_read_b128 v[212:215], v231 offset:20480\n"
"v_mfma_f32_16x16x32_bf16 v[64:67], v[148:151], a[248:251], v[64:67]\n"
"buffer_load_dwordx4 v240, s[16:19], s40 offen offset:2048 nt lds\n"
"v_mfma_f32_16x16x32_bf16 v[68:71], v[148:151], v[224:227], v[68:71]\n"
"v_exp_f32 v159, v159\n"
"v_mfma_f32_16x16x32_bf16 v[68:71], v[144:147], a[252:255], v[68:71]\n"
"v_exp_f32 v155, v155\n"
"v_mfma_f32_16x16x32_bf16 v[36:39], v[140:143], v[224:227], v[36:39]\n"
"v_exp_f32 v160, v160\n"
"v_mfma_f32_16x16x32_bf16 v[36:39], v[136:139], a[252:255], v[36:39]\n"
"v_exp_f32 v179, v179\n"
"v_mfma_f32_16x16x32_bf16 v[4:7], v[132:135], v[224:227], v[4:7]\n"
"ds_read_b64_tr_b16 a[244:245], v232 offset:512\n"
"ds_read_b64_tr_b16 v[224:225], v237 offset:512\n"
"v_mfma_f32_16x16x32_bf16 v[4:7], v[128:131], a[252:255], v[4:7]\n"
"v_exp_f32 v158, v158\n"
"v_mfma_f32_16x16x32_bf16 a[212:215], v[124:127], a[252:255], a[212:215]\n"
"ds_read_b64_tr_b16 a[246:247], v232 offset:4608\n"
"ds_read_b64_tr_b16 a[250:251], v236 offset:4608\n"
"v_smfmac_f32_16x16x64_bf16 v[200:203], a[240:243], v[144:151], v241\n"
"s_waitcnt lgkmcnt(4)\n"
"ds_read_b64_tr_b16 v[226:227], v237 offset:4608\n"
"v_cvt_pk_bf16_f32 v99, v170, v171\n"
"v_mfma_f32_16x16x32_bf16 v[168:171], v[208:211], a[0:3], v[180:183]\n"
"v_exp_f32 v164, v164\n"
"v_mfma_f32_16x16x32_bf16 v[168:171], v[212:215], a[4:7], v[168:171]\n"
"ds_read_b64_tr_b16 a[248:249], v236 offset:512\n"
"ds_read_b64_tr_b16 a[254:255], v233 offset:4608\n"
"v_mfma_f32_16x16x32_bf16 v[168:171], v[216:219], a[8:11], v[168:171]\n"
"s_add_u32 s40, s56, s54\n"
"ds_read_b64_tr_b16 a[252:253], v233 offset:512\n"
"v_mfma_f32_16x16x32_bf16 v[168:171], v[220:223], a[12:15], v[168:171]\n"
"buffer_load_dwordx4 v207, s[16:19], s40 offen offset:3072 nt lds\n"
"v_mfma_f32_16x16x32_bf16 v[172:175], v[216:219], a[24:27], v[184:187]\n"
"v_exp_f32 v154, v154\n"
"v_mfma_f32_16x16x32_bf16 v[172:175], v[220:223], a[28:31], v[172:175]\n"
"v_exp_f32 v167, v167\n"
"v_mfma_f32_16x16x32_bf16 v[172:175], v[208:211], a[16:19], v[172:175]\n"
"v_exp_f32 v163, v163\n"
"v_mfma_f32_16x16x32_bf16 v[172:175], v[212:215], a[20:23], v[172:175]\n"
"s_cbranch_vccnz LPCL1_%=\nLPKL1_%=:\n"
"v_cvt_pk_bf16_f32 v115, v178, v179\n"
"v_mfma_f32_16x16x32_bf16 v[176:179], v[208:211], a[32:35], v[188:191]\n"
"v_exp_f32 v165, v165\n"
"v_mfma_f32_16x16x32_bf16 v[176:179], v[212:215], a[36:39], v[176:179]\n"
"v_exp_f32 v157, v157\n"
"v_mfma_f32_16x16x32_bf16 v[176:179], v[216:219], a[40:43], v[176:179]\n"
"v_exp_f32 v166, v166\n"
"v_mfma_f32_16x16x32_bf16 v[176:179], v[220:223], a[44:47], v[176:179]\n"
"s_waitcnt lgkmcnt(0)\n"
"v_mfma_f32_16x16x32_bf16 a[224:227], v[124:127], a[244:247], a[224:227]\n"
"v_exp_f32 v162, v162\n"
"v_mfma_f32_16x16x32_bf16 v[16:19], v[128:131], a[244:247], v[16:19]\n"
"s_waitcnt vmcnt(8)\n"
"v_exp_f32 v168, v168\n"
"v_mfma_f32_16x16x32_bf16 v[16:19], v[132:135], a[248:251], v[16:19]\n"
"s_barrier\n"
"v_mfma_f32_16x16x32_bf16 v[48:51], v[136:139], a[244:247], v[48:51]\n"
"ds_read_b128 v[208:211], v228 offset:32768\n"
"v_cvt_pk_bf16_f32 v101, v158, v159\n"
"v_mfma_f32_16x16x32_bf16 v[48:51], v[140:143], a[248:251], v[48:51]\n"
"ds_read_b128 v[216:219], v228 offset:33280\n"
"v_cvt_pk_bf16_f32 v109, v162, v163\n"
"v_mfma_f32_16x16x32_bf16 v[80:83], v[144:147], a[244:247], v[80:83]\n"
"ds_read_b128 v[220:223], v229 offset:33280\n"
"v_cvt_pk_bf16_f32 v123, v154, v155\n"
"v_mfma_f32_16x16x32_bf16 v[80:83], v[148:151], a[248:251], v[80:83]\n"
"ds_read_b128 v[212:215], v229 offset:32768\n"
"v_cvt_pk_bf16_f32 v117, v166, v167\n"
"v_mfma_f32_16x16x32_bf16 v[84:87], v[148:151], v[224:227], v[84:87]\n"
"v_exp_f32 v176, v176\n"
"v_mfma_f32_16x16x32_bf16 v[84:87], v[144:147], a[252:255], v[84:87]\n"
"v_exp_f32 v169, v169\n"
"v_mfma_f32_16x16x32_bf16 v[52:55], v[140:143], v[224:227], v[52:55]\n"
"ds_read_b64_tr_b16 a[246:247], v234 offset:20480\n"
"v_cvt_pk_bf16_f32 v116, v164, v165\n"
"v_mfma_f32_16x16x32_bf16 v[52:55], v[136:139], a[252:255], v[52:55]\n"
"v_exp_f32 v177, v177\n"
"v_mfma_f32_16x16x32_bf16 v[20:23], v[132:135], v[224:227], v[20:23]\n"
"ds_read_b64_tr_b16 a[250:251], v238 offset:20480\n"
"v_cvt_pk_bf16_f32 v118, v176, v177\n"
"v_mfma_f32_16x16x32_bf16 v[20:23], v[128:131], a[252:255], v[20:23]\n"
"v_exp_f32 v174, v174\n"
"v_mfma_f32_16x16x32_bf16 a[228:231], v[124:127], a[252:255], a[228:231]\n"
"s_waitcnt lgkmcnt(3)\n"
"ds_read_b64_tr_b16 a[254:255], v235 offset:20480\n"
"v_cvt_pk_bf16_f32 v100, v156, v157\n"
"LAY2_%=:\n"
"v_mfma_f32_16x16x32_bf16 v[152:155], v[208:211], a[0:3], v[180:183]\n"
"s_waitcnt lgkmcnt(3)\n"
"ds_read_b64_tr_b16 a[248:249], v238 offset:16384\n"
"v_cvt_pk_bf16_f32 v102, v168, v169\n"
"v_mfma_f32_16x16x32_bf16 v[152:155], v[212:215], a[4:7], v[152:155]\n"
"v_exp_f32 v172, v172\n"
"v_mfma_f32_16x16x32_bf16 v[152:155], v[216:219], a[8:11], v[152:155]\n"
"ds_read_b64_tr_b16 a[244:245], v234 offset:16384\n"
"v_cvt_pk_bf16_f32 v108, v160, v161\n"
"v_mfma_f32_16x16x32_bf16 v[152:155], v[220:223], a[12:15], v[152:155]\n"
"v_exp_f32 v175, v175\n"
"v_mfma_f32_16x16x32_bf16 v[156:159], v[216:219], a[24:27], v[184:187]\n"
"ds_read_b64_tr_b16 a[252:253], v235 offset:16384\n"
"v_cvt_pk_bf16_f32 v111, v174, v175\n"
"v_mfma_f32_16x16x32_bf16 v[156:159], v[220:223], a[28:31], v[156:159]\n"
"ds_read_b64_tr_b16 v[224:225], v239 offset:16384\n"
"v_mfma_f32_16x16x32_bf16 v[156:159], v[208:211], a[16:19], v[156:159]\n"
"s_add_u32 m0, s44, 114688\n"
"ds_read_b64_tr_b16 v[226:227], v239 offset:20480\n"
"v_mfma_f32_16x16x32_bf16 v[156:159], v[212:215], a[20:23], v[156:159]\n"
"buffer_load_dwordx4 v240, s[20:23], s56 offen nt lds\n"
"v_mfma_f32_16x16x32_bf16 v[160:163], v[208:211], a[32:35], v[188:191]\n"
"v_exp_f32 v178, v178\n"
"v_mfma_f32_16x16x32_bf16 v[160:163], v[212:215], a[36:39], v[160:163]\n"
"v_exp_f32 v171, v171\n"
"v_mfma_f32_16x16x32_bf16 v[160:163], v[216:219], a[40:43], v[160:163]\n"
"s_add_u32 s40, s56, s52\n"
"v_mfma_f32_16x16x32_bf16 v[160:163], v[220:223], a[44:47], v[160:163]\n"
"buffer_load_dwordx4 v207, s[20:23], s40 offen offset:1024 nt lds\n"
"v_mfma_f32_16x16x32_bf16 v[164:167], v[216:219], a[200:203], v[192:195]\n"
"v_exp_f32 v170, v170\n"
"v_mfma_f32_16x16x32_bf16 v[164:167], v[220:223], a[204:207], v[164:167]\n"
"v_exp_f32 v153, v153\n"
"v_mfma_f32_16x16x32_bf16 v[164:167], v[208:211], a[192:195], v[164:167]\n"
"v_exp_f32 v179, v179\n"
"v_mfma_f32_16x16x32_bf16 v[164:167], v[212:215], a[196:199], v[164:167]\n"
"s_waitcnt lgkmcnt(0)\n"
"v_exp_f32 v158, v158\n"
"v_mfma_f32_16x16x32_bf16 a[216:219], v[120:123], a[244:247], a[216:219]\n"
"ds_read_b128 v[216:219], v228 offset:37376\n"
"v_cvt_pk_bf16_f32 v103, v170, v171\n"
"v_mfma_f32_16x16x32_bf16 v[8:11], v[96:99], a[244:247], v[8:11]\n"
"v_exp_f32 v173, v173\n"
"v_mfma_f32_16x16x32_bf16 v[8:11], v[100:103], a[248:251], v[8:11]\n"
"ds_read_b128 v[208:211], v228 offset:36864\n"
"v_cvt_pk_bf16_f32 v110, v172, v173\n"
"v_mfma_f32_16x16x32_bf16 v[40:43], v[104:107], a[244:247], v[40:43]\n"
"v_exp_f32 v162, v162\n"
"v_mfma_f32_16x16x32_bf16 v[40:43], v[108:111], a[248:251], v[40:43]\n"
"ds_read_b128 v[220:223], v229 offset:37376\n"
"v_cvt_pk_bf16_f32 v119, v178, v179\n"
"v_mfma_f32_16x16x32_bf16 v[72:75], v[112:115], a[244:247], v[72:75]\n"
"s_add_u32 s40, s56, s53\n"
"ds_read_b128 v[212:215], v229 offset:36864\n"
"v_mfma_f32_16x16x32_bf16 v[72:75], v[116:119], a[248:251], v[72:75]\n"
"buffer_load_dwordx4 v240, s[20:23], s40 offen offset:2048 nt lds\n"
"v_mfma_f32_16x16x32_bf16 v[76:79], v[116:119], v[224:227], v[76:79]\n"
"v_exp_f32 v163, v163\n"
"v_mfma_f32_16x16x32_bf16 v[76:79], v[112:115], a[252:255], v[76:79]\n"
"v_exp_f32 v156, v156\n"
"v_mfma_f32_16x16x32_bf16 v[44:47], v[108:111], v[224:227], v[44:47]\n"
"v_exp_f32 v160, v160\n"
"v_mfma_f32_16x16x32_bf16 v[44:47], v[104:107], a[252:255], v[44:47]\n"
"v_exp_f32 v155, v155\n"
"v_mfma_f32_16x16x32_bf16 v[12:15], v[100:103], v[224:227], v[12:15]\n"
"ds_read_b64_tr_b16 v[226:227], v239 offset:20992\n"
"v_mfma_f32_16x16x32_bf16 v[12:15], v[96:99], a[252:255], v[12:15]\n"
"ds_read_b64_tr_b16 a[248:249], v238 offset:16896\n"
"v_mfma_f32_16x16x32_bf16 a[220:223], v[120:123], a[252:255], a[220:223]\n"
"v_exp_f32 v154, v154\n"
"v_smfmac_f32_16x16x64_bf16 v[196:199], a[240:243], v[96:103], v241\n"
"ds_read_b64_tr_b16 a[250:251], v238 offset:20992\n"
"v_mfma_f32_16x16x32_bf16 v[202:205], a[240:243], v[120:123], v[202:205]\n"
"s_waitcnt lgkmcnt(3)\n"
"v_exp_f32 v152, v152\n"
"v_mfma_f32_16x16x32_bf16 v[168:171], v[216:219], a[8:11], v[180:183]\n"
"ds_read_b64_tr_b16 a[244:245], v234 offset:16896\n"
"v_cvt_pk_bf16_f32 v128, v152, v153\n"
"v_mfma_f32_16x16x32_bf16 v[168:171], v[220:223], a[12:15], v[168:171]\n"
"v_exp_f32 v165, v165\n"
"v_mfma_f32_16x16x32_bf16 v[168:171], v[208:211], a[0:3], v[168:171]\n"
"ds_read_b64_tr_b16 a[246:247], v234 offset:20992\n"
"v_cvt_pk_bf16_f32 v145, v162, v163\n"
"v_mfma_f32_16x16x32_bf16 v[168:171], v[212:215], a[4:7], v[168:171]\n"
"ds_read_b64_tr_b16 a[254:255], v235 offset:20992\n"
"v_mfma_f32_16x16x32_bf16 v[172:175], v[208:211], a[16:19], v[184:187]\n"
"ds_read_b64_tr_b16 v[224:225], v239 offset:16896\n"
"v_cvt_pk_bf16_f32 v129, v154, v155\n"
"v_mfma_f32_16x16x32_bf16 v[172:175], v[212:215], a[20:23], v[172:175]\n"
"v_mfma_f32_16x16x32_bf16 v[172:175], v[216:219], a[24:27], v[172:175]\n"
"s_add_u32 s40, s56, s54\n"
"ds_read_b64_tr_b16 a[252:253], v235 offset:16896\n"
"v_mfma_f32_16x16x32_bf16 v[172:175], v[220:223], a[28:31], v[172:175]\n"
"buffer_load_dwordx4 v207, s[20:23], s40 offen offset:3072 nt lds\n"
"v_mfma_f32_16x16x32_bf16 v[176:179], v[216:219], a[40:43], v[188:191]\n"
"s_add_u32 s55, s55, s59\n"
"v_exp_f32 v157, v157\n"
"v_mfma_f32_16x16x32_bf16 v[176:179], v[220:223], a[44:47], v[176:179]\n"
"s_or_b32 s55, s55, s58\n"
"v_exp_f32 v171, v171\n"
"v_mfma_f32_16x16x32_bf16 v[176:179], v[208:211], a[32:35], v[176:179]\n"
"s_xor_b32 s56, s55, s57\n"
"v_exp_f32 v159, v159\n"
"v_mfma_f32_16x16x32_bf16 v[176:179], v[212:215], a[36:39], v[176:179]\n"
"s_cmp_ge_u32 s47, 15\n"
"v_exp_f32 v167, v167\n"
"v_mfma_f32_16x16x32_bf16 v[152:155], v[208:211], a[192:195], v[192:195]\n"
"s_cbranch_scc1 LSW_%=\nLSWK_%=:\n"
"v_cvt_pk_bf16_f32 v137, v158, v159\n"
"v_mfma_f32_16x16x32_bf16 v[152:155], v[212:215], a[196:199], v[152:155]\n"
"ds_read_b128 v[208:211], v230 offset:32768\n"
"v_cvt_pk_bf16_f32 v136, v156, v157\n"
"v_mfma_f32_16x16x32_bf16 v[152:155], v[216:219], a[200:203], v[152:155]\n"
"v_exp_f32 v166, v166\n"
"v_mfma_f32_16x16x32_bf16 v[152:155], v[220:223], a[204:207], v[152:155]\n"
"s_waitcnt lgkmcnt(1)\n"
"ds_read_b128 v[216:219], v230 offset:33280\n"
"v_mfma_f32_16x16x32_bf16 a[232:235], v[120:123], a[244:247], a[232:235]\n"
"v_exp_f32 v169, v169\n"
"v_mfma_f32_16x16x32_bf16 v[24:27], v[96:99], a[244:247], v[24:27]\n"
"ds_read_b128 v[220:223], v231 offset:33280\n"
"v_mfma_f32_16x16x32_bf16 v[24:27], v[100:103], a[248:251], v[24:27]\n"
"v_exp_f32 v164, v164\n"
"v_mfma_f32_16x16x32_bf16 v[56:59], v[104:107], a[244:247], v[56:59]\n"
"ds_read_b128 v[212:215], v231 offset:32768\n"
"v_mfma_f32_16x16x32_bf16 v[56:59], v[108:111], a[248:251], v[56:59]\n"
"v_exp_f32 v178, v178\n"
"v_mfma_f32_16x16x32_bf16 v[88:91], v[112:115], a[244:247], v[88:91]\n"
"s_mov_b32 m0, s44\n"
"ds_read_b64_tr_b16 a[246:247], v232 offset:20480\n"
"v_mfma_f32_16x16x32_bf16 v[88:91], v[116:119], a[248:251], v[88:91]\n"
"buffer_load_dwordx4 v240, s[16:19], s56 offen nt lds\n"
"v_mfma_f32_16x16x32_bf16 v[92:95], v[116:119], v[224:227], v[92:95]\n"
"v_exp_f32 v175, v175\n"
"v_mfma_f32_16x16x32_bf16 v[92:95], v[112:115], a[252:255], v[92:95]\n"
"v_exp_f32 v168, v168\n"
"v_mfma_f32_16x16x32_bf16 v[60:63], v[108:111], v[224:227], v[60:63]\n"
"v_exp_f32 v161, v161\n"
"v_mfma_f32_16x16x32_bf16 v[60:63], v[104:107], a[252:255], v[60:63]\n"
"v_exp_f32 v177, v177\n"
"v_mfma_f32_16x16x32_bf16 v[28:31], v[100:103], v[224:227], v[28:31]\n"
"ds_read_b64_tr_b16 v[226:227], v237 offset:20480\n"
"v_mfma_f32_16x16x32_bf16 v[28:31], v[96:99], a[252:255], v[28:31]\n"
"v_exp_f32 v172, v172\n"
"v_mfma_f32_16x16x32_bf16 a[236:239], v[120:123], a[252:255], a[236:239]\n"
"ds_read_b64_tr_b16 a[250:251], v236 offset:20480\n"
"v_cvt_pk_bf16_f32 v124, v164, v165\n"
"v_smfmac_f32_16x16x64_bf16 v[198:201], a[240:243], v[104:111], v241\n"
"s_waitcnt lgkmcnt(2)\n"
"ds_read_b64_tr_b16 a[252:253], v233 offset:16384\n"
"v_mfma_f32_16x16x32_bf16 v[156:159], v[208:211], a[0:3], v[180:183]\n"
"ds_read_b64_tr_b16 a[248:249], v236 offset:16384\n"
"v_cvt_pk_bf16_f32 v125, v166, v167\n"
"v_mfma_f32_16x16x32_bf16 v[156:159], v[212:215], a[4:7], v[156:159]\n"
"ds_read_b64_tr_b16 v[224:225], v237 offset:16384\n"
"v_cvt_pk_bf16_f32 v130, v168, v169\n"
"v_mfma_f32_16x16x32_bf16 v[156:159], v[216:219], a[8:11], v[156:159]\n"
"ds_read_b64_tr_b16 a[254:255], v233 offset:20480\n"
"v_cvt_pk_bf16_f32 v144, v160, v161\n"
"v_mfma_f32_16x16x32_bf16 v[156:159], v[220:223], a[12:15], v[156:159]\n"
"ds_read_b64_tr_b16 a[244:245], v232 offset:16384\n"
"v_mfma_f32_16x16x32_bf16 v[160:163], v[216:219], a[24:27], v[184:187]\n"
"v_exp_f32 v176, v176\n"
"v_mfma_f32_16x16x32_bf16 v[160:163], v[220:223], a[28:31], v[160:163]\n"
"v_exp_f32 v152, v152\n"
"v_mfma_f32_16x16x32_bf16 v[160:163], v[208:211], a[16:19], v[160:163]\n"
"s_add_u32 s40, s56, s52\n"
"v_cvt_pk_bf16_f32 v146, v176, v177\n"
"v_mfma_f32_16x16x32_bf16 v[160:163], v[212:215], a[20:23], v[160:163]\n"
"buffer_load_dwordx4 v207, s[16:19], s40 offen offset:1024 nt lds\n"
"v_mfma_f32_16x16x32_bf16 v[164:167], v[208:211], a[32:35], v[188:191]\n"
"v_exp_f32 v170, v170\n"
"v_mfma_f32_16x16x32_bf16 v[164:167], v[212:215], a[36:39], v[164:167]\n"
"v_exp_f32 v173, v173\n"
"v_mfma_f32_16x16x32_bf16 v[164:167], v[216:219], a[40:43], v[164:167]\n"
"v_exp_f32 v153, v153\n"
"v_mfma_f32_16x16x32_bf16 v[164:167], v[220:223], a[44:47], v[164:167]\n"
"s_waitcnt lgkmcnt(0)\n"
"v_exp_f32 v174, v174\n"
"v_mfma_f32_16x16x32_bf16 a[208:211], v[120:123], a[244:247], a[208:211]\n"
"ds_read_b128 v[208:211], v230 offset:36864\n"
"v_cvt_pk_bf16_f32 v139, v174, v175\n"
"v_mfma_f32_16x16x32_bf16 v[0:3], v[96:99], a[244:247], v[0:3]\n"
"v_exp_f32 v156, v156\n"
"v_mfma_f32_16x16x32_bf16 v[0:3], v[100:103], a[248:251], v[0:3]\n"
"ds_read_b128 v[220:223], v231 offset:37376\n"
"v_cvt_pk_bf16_f32 v138, v172, v173\n"
"v_mfma_f32_16x16x32_bf16 v[32:35], v[104:107], a[244:247], v[32:35]\n"
"v_exp_f32 v161, v161\n"
"v_mfma_f32_16x16x32_bf16 v[32:35], v[108:111], a[248:251], v[32:35]\n"
"ds_read_b128 v[216:219], v230 offset:37376\n"
"v_cvt_pk_bf16_f32 v126, v152, v153\n"
"v_mfma_f32_16x16x32_bf16 v[64:67], v[112:115], a[244:247], v[64:67]\n"
"s_add_u32 s40, s56, s53\n"
"ds_read_b128 v[212:215], v231 offset:36864\n"
"v_mfma_f32_16x16x32_bf16 v[64:67], v[116:119], a[248:251], v[64:67]\n"
"buffer_load_dwordx4 v240, s[16:19], s40 offen offset:2048 nt lds\n"
"v_mfma_f32_16x16x32_bf16 v[68:71], v[116:119], v[224:227], v[68:71]\n"
"v_exp_f32 v159, v159\n"
"v_mfma_f32_16x16x32_bf16 v[68:71], v[112:115], a[252:255], v[68:71]\n"
"v_exp_f32 v155, v155\n"
"v_mfma_f32_16x16x32_bf16 v[36:39], v[108:111], v[224:227], v[36:39]\n"
"v_exp_f32 v160, v160\n"
"v_mfma_f32_16x16x32_bf16 v[36:39], v[104:107], a[252:255], v[36:39]\n"
"v_exp_f32 v179, v179\n"
"v_mfma_f32_16x16x32_bf16 v[4:7], v[100:103], v[224:227], v[4:7]\n"
"ds_read_b64_tr_b16 a[244:245], v232 offset:16896\n"
"ds_read_b64_tr_b16 v[224:225], v237 offset:16896\n"
"v_mfma_f32_16x16x32_bf16 v[4:7], v[96:99], a[252:255], v[4:7]\n"
"v_exp_f32 v158, v158\n"
"v_mfma_f32_16x16x32_bf16 a[212:215], v[120:123], a[252:255], a[212:215]\n"
"ds_read_b64_tr_b16 a[246:247], v232 offset:20992\n"
"ds_read_b64_tr_b16 a[250:251], v236 offset:20992\n"
"v_smfmac_f32_16x16x64_bf16 v[200:203], a[240:243], v[112:119], v241\n"
"s_waitcnt lgkmcnt(4)\n"
"ds_read_b64_tr_b16 v[226:227], v237 offset:20992\n"
"v_cvt_pk_bf16_f32 v131, v170, v171\n"
"v_mfma_f32_16x16x32_bf16 v[168:171], v[208:211], a[0:3], v[180:183]\n"
"v_exp_f32 v164, v164\n"
"v_mfma_f32_16x16x32_bf16 v[168:171], v[212:215], a[4:7], v[168:171]\n"
"ds_read_b64_tr_b16 a[248:249], v236 offset:16896\n"
"ds_read_b64_tr_b16 a[254:255], v233 offset:20992\n"
"v_mfma_f32_16x16x32_bf16 v[168:171], v[216:219], a[8:11], v[168:171]\n"
"s_add_u32 s40, s56, s54\n"
"ds_read_b64_tr_b16 a[252:253], v233 offset:16896\n"
"v_mfma_f32_16x16x32_bf16 v[168:171], v[220:223], a[12:15], v[168:171]\n"
"buffer_load_dwordx4 v207, s[16:19], s40 offen offset:3072 nt lds\n"
"v_mfma_f32_16x16x32_bf16 v[172:175], v[216:219], a[24:27], v[184:187]\n"
"v_exp_f32 v154, v154\n"
"v_mfma_f32_16x16x32_bf16 v[172:175], v[220:223], a[28:31], v[172:175]\n"
"v_exp_f32 v167, v167\n"
"v_mfma_f32_16x16x32_bf16 v[172:175], v[208:211], a[16:19], v[172:175]\n"
"v_exp_f32 v163, v163\n"
"v_mfma_f32_16x16x32_bf16 v[172:175], v[212:215], a[20:23], v[172:175]\n"
"s_cbranch_vccnz LPCL2_%=\nLPKL2_%=:\n"
"v_cvt_pk_bf16_f32 v147, v178, v179\n"
"v_mfma_f32_16x16x32_bf16 v[176:179], v[208:211], a[32:35], v[188:191]\n"
"v_exp_f32 v165, v165\n"
"v_mfma_f32_16x16x32_bf16 v[176:179], v[212:215], a[36:39], v[176:179]\n"
"v_exp_f32 v157, v157\n"
"v_mfma_f32_16x16x32_bf16 v[176:179], v[216:219], a[40:43], v[176:179]\n"
"v_exp_f32 v166, v166\n"
"v_mfma_f32_16x16x32_bf16 v[176:179], v[220:223], a[44:47], v[176:179]\n"
"s_waitcnt lgkmcnt(0)\n"
"v_mfma_f32_16x16x32_bf16 a[224:227], v[120:123], a[244:247], a[224:227]\n"
"v_exp_f32 v162, v162\n"
"v_mfma_f32_16x16x32_bf16 v[16:19], v[96:99], a[244:247], v[16:19]\n"
"s_waitcnt vmcnt(8)\n"
"v_exp_f32 v168, v168\n"
"v_mfma_f32_16x16x32_bf16 v[16:19], v[100:103], a[248:251], v[16:19]\n"
"s_barrier\n"
"v_mfma_f32_16x16x32_bf16 v[48:51], v[104:107], a[244:247], v[48:51]\n"
"ds_read_b128 v[208:211], v228 offset:49152\n"
"v_cvt_pk_bf16_f32 v133, v158, v159\n"
"v_mfma_f32_16x16x32_bf16 v[48:51], v[108:111], a[248:251], v[48:51]\n"
"ds_read_b128 v[216:219], v228 offset:49664\n"
"v_cvt_pk_bf16_f32 v141, v162, v163\n"
"v_mfma_f32_16x16x32_bf16 v[80:83], v[112:115], a[244:247], v[80:83]\n"
"ds_read_b128 v[220:223], v229 offset:49664\n"
"v_cvt_pk_bf16_f32 v127, v154, v155\n"
"v_mfma_f32_16x16x32_bf16 v[80:83], v[116:119], a[248:251], v[80:83]\n"
"ds_read_b128 v[212:215], v229 offset:49152\n"
"v_cvt_pk_bf16_f32 v149, v166, v167\n"
"v_mfma_f32_16x16x32_bf16 v[84:87], v[116:119], v[224:227], v[84:87]\n"
"v_exp_f32 v176, v176\n"
"v_mfma_f32_16x16x32_bf16 v[84:87], v[112:115], a[252:255], v[84:87]\n"
"v_exp_f32 v169, v169\n"
"v_mfma_f32_16x16x32_bf16 v[52:55], v[108:111], v[224:227], v[52:55]\n"
"ds_read_b64_tr_b16 a[246:247], v234 offset:36864\n"
"v_cvt_pk_bf16_f32 v148, v164, v165\n"
"v_mfma_f32_16x16x32_bf16 v[52:55], v[104:107], a[252:255], v[52:55]\n"
"v_exp_f32 v177, v177\n"
"v_mfma_f32_16x16x32_bf16 v[20:23], v[100:103], v[224:227], v[20:23]\n"
"ds_read_b64_tr_b16 a[250:251], v238 offset:36864\n"
"v_cvt_pk_bf16_f32 v150, v176, v177\n"
"v_mfma_f32_16x16x32_bf16 v[20:23], v[96:99], a[252:255], v[20:23]\n"
"v_exp_f32 v174, v174\n"
"v_mfma_f32_16x16x32_bf16 a[228:231], v[120:123], a[252:255], a[228:231]\n"
"s_waitcnt lgkmcnt(3)\n"
"ds_read_b64_tr_b16 a[254:255], v235 offset:36864\n"
"v_cvt_pk_bf16_f32 v132, v156, v157\n"
"v_mfma_f32_16x16x32_bf16 v[152:155], v[208:211], a[0:3], v[180:183]\n"
"s_waitcnt lgkmcnt(3)\n"
"ds_read_b64_tr_b16 a[248:249], v238 offset:32768\n"
"v_cvt_pk_bf16_f32 v134, v168, v169\n"
"v_mfma_f32_16x16x32_bf16 v[152:155], v[212:215], a[4:7], v[152:155]\n"
"v_exp_f32 v172, v172\n"
"v_mfma_f32_16x16x32_bf16 v[152:155], v[216:219], a[8:11], v[152:155]\n"
"ds_read_b64_tr_b16 a[244:245], v234 offset:32768\n"
"v_cvt_pk_bf16_f32 v140, v160, v161\n"
"v_mfma_f32_16x16x32_bf16 v[152:155], v[220:223], a[12:15], v[152:155]\n"
"v_exp_f32 v175, v175\n"
"v_mfma_f32_16x16x32_bf16 v[156:159], v[216:219], a[24:27], v[184:187]\n"
"ds_read_b64_tr_b16 a[252:253], v235 offset:32768\n"
"v_cvt_pk_bf16_f32 v143, v174, v175\n"
"v_mfma_f32_16x16x32_bf16 v[156:159], v[220:223], a[28:31], v[156:159]\n"
"ds_read_b64_tr_b16 v[224:225], v239 offset:32768\n"
"v_mfma_f32_16x16x32_bf16 v[156:159], v[208:211], a[16:19], v[156:159]\n"
"s_add_u32 m0, s44, 65536\n"
"ds_read_b64_tr_b16 v[226:227], v239 offset:36864\n"
"v_mfma_f32_16x16x32_bf16 v[156:159], v[212:215], a[20:23], v[156:159]\n"
"buffer_load_dwordx4 v240, s[20:23], s56 offen nt lds\n"
"v_mfma_f32_16x16x32_bf16 v[160:163], v[208:211], a[32:35], v[188:191]\n"
"v_exp_f32 v178, v178\n"
"v_mfma_f32_16x16x32_bf16 v[160:163], v[212:215], a[36:39], v[160:163]\n"
"v_exp_f32 v171, v171\n"
"v_mfma_f32_16x16x32_bf16 v[160:163], v[216:219], a[40:43], v[160:163]\n"
"s_add_u32 s40, s56, s52\n"
"v_mfma_f32_16x16x32_bf16 v[160:163], v[220:223], a[44:47], v[160:163]\n"
"buffer_load_dwordx4 v207, s[20:23], s40 offen offset:1024 nt lds\n"
"v_mfma_f32_16x16x32_bf16 v[164:167], v[216:219], a[200:203], v[192:195]\n"
"v_exp_f32 v170, v170\n"
"v_mfma_f32_16x16x32_bf16 v[164:167], v[220:223], a[204:207], v[164:167]\n"
"v_exp_f32 v153, v153\n"
"v_mfma_f32_16x16x32_bf16 v[164:167], v[208:211], a[192:195], v[164:167]\n"
"v_exp_f32 v179, v179\n"
"v_mfma_f32_16x16x32_bf16 v[164:167], v[212:215], a[196:199], v[164:167]\n"
"s_waitcnt lgkmcnt(0)\n"
"v_exp_f32 v158, v158\n"
"v_mfma_f32_16x16x32_bf16 a[216:219], v[124:127], a[244:247], a[216:219]\n"
"ds_read_b128 v[216:219], v228 offset:53760\n"
"v_cvt_pk_bf16_f32 v135, v170, v171\n"
"v_mfma_f32_16x16x32_bf16 v[8:11], v[128:131], a[244:247], v[8:11]\n"
"v_exp_f32 v173, v173\n"
"v_mfma_f32_16x16x32_bf16 v[8:11], v[132:135], a[248:251], v[8:11]\n"
"ds_read_b128 v[208:211], v228 offset:53248\n"
"v_cvt_pk_bf16_f32 v142, v172, v173\n"
"v_mfma_f32_16x16x32_bf16 v[40:43], v[136:139], a[244:247], v[40:43]\n"
"v_exp_f32 v162, v162\n"
"v_mfma_f32_16x16x32_bf16 v[40:43], v[140:143], a[248:251], v[40:43]\n"
"ds_read_b128 v[220:223], v229 offset:53760\n"
"v_cvt_pk_bf16_f32 v151, v178, v179\n"
"v_mfma_f32_16x16x32_bf16 v[72:75], v[144:147], a[244:247], v[72:75]\n"
"s_add_u32 s40, s56, s53\n"
"ds_read_b128 v[212:215], v229 offset:53248\n"
"v_mfma_f32_16x16x32_bf16 v[72:75], v[148:151], a[248:251], v[72:75]\n"
"buffer_load_dwordx4 v240, s[20:23], s40 offen offset:2048 nt lds\n"
"v_mfma_f32_16x16x32_bf16 v[76:79], v[148:151], v[224:227], v[76:79]\n"
"v_exp_f32 v163, v163\n"
"v_mfma_f32_16x16x32_bf16 v[76:79], v[144:147], a[252:255], v[76:79]\n"
"v_exp_f32 v156, v156\n"
"v_mfma_f32_16x16x32_bf16 v[44:47], v[140:143], v[224:227], v[44:47]\n"
"v_exp_f32 v160, v160\n"
"v_mfma_f32_16x16x32_bf16 v[44:47], v[136:139], a[252:255], v[44:47]\n"
"v_exp_f32 v155, v155\n"
"v_mfma_f32_16x16x32_bf16 v[12:15], v[132:135], v[224:227], v[12:15]\n"
"ds_read_b64_tr_b16 v[226:227], v239 offset:37376\n"
"v_mfma_f32_16x16x32_bf16 v[12:15], v[128:131], a[252:255], v[12:15]\n"
"ds_read_b64_tr_b16 a[248:249], v238 offset:33280\n"
"v_mfma_f32_16x16x32_bf16 a[220:223], v[124:127], a[252:255], a[220:223]\n"
"v_exp_f32 v154, v154\n"
"v_smfmac_f32_16x16x64_bf16 v[196:199], a[240:243], v[128:135], v241\n"
"ds_read_b64_tr_b16 a[250:251], v238 offset:37376\n"
"v_mfma_f32_16x16x32_bf16 v[202:205], a[240:243], v[124:127], v[202:205]\n"
"s_waitcnt lgkmcnt(3)\n"
"v_exp_f32 v152, v152\n"
"v_mfma_f32_16x16x32_bf16 v[168:171], v[216:219], a[8:11], v[180:183]\n"
"ds_read_b64_tr_b16 a[244:245], v234 offset:33280\n"
"v_cvt_pk_bf16_f32 v96, v152, v153\n"
"v_mfma_f32_16x16x32_bf16 v[168:171], v[220:223], a[12:15], v[168:171]\n"
"v_exp_f32 v165, v165\n"
"v_mfma_f32_16x16x32_bf16 v[168:171], v[208:211], a[0:3], v[168:171]\n"
"ds_read_b64_tr_b16 a[246:247], v234 offset:37376\n"
"v_cvt_pk_bf16_f32 v113, v162, v163\n"
"v_mfma_f32_16x16x32_bf16 v[168:171], v[212:215], a[4:7], v[168:171]\n"
"ds_read_b64_tr_b16 a[254:255], v235 offset:37376\n"
"v_mfma_f32_16x16x32_bf16 v[172:175], v[208:211], a[16:19], v[184:187]\n"
"ds_read_b64_tr_b16 v[224:225], v239 offset:33280\n"
"v_cvt_pk_bf16_f32 v97, v154, v155\n"
"v_mfma_f32_16x16x32_bf16 v[172:175], v[212:215], a[20:23], v[172:175]\n"
"v_mfma_f32_16x16x32_bf16 v[172:175], v[216:219], a[24:27], v[172:175]\n"
"s_add_u32 s40, s56, s54\n"
"ds_read_b64_tr_b16 a[252:253], v235 offset:33280\n"
"v_mfma_f32_16x16x32_bf16 v[172:175], v[220:223], a[28:31], v[172:175]\n"
"buffer_load_dwordx4 v207, s[20:23], s40 offen offset:3072 nt lds\n"
"v_mfma_f32_16x16x32_bf16 v[176:179], v[216:219], a[40:43], v[188:191]\n"
"s_add_u32 s55, s55, s59\n"
"v_exp_f32 v157, v157\n"
"v_mfma_f32_16x16x32_bf16 v[176:179], v[220:223], a[44:47], v[176:179]\n"
"s_or_b32 s55, s55, s58\n"
"v_exp_f32 v171, v171\n"
"v_mfma_f32_16x16x32_bf16 v[176:179], v[208:211], a[32:35], v[176:179]\n"
"s_xor_b32 s56, s55, s57\n"
"v_exp_f32 v159, v159\n"
"v_mfma_f32_16x16x32_bf16 v[176:179], v[212:215], a[36:39], v[176:179]\n"
"v_exp_f32 v167, v167\n"
"v_mfma_f32_16x16x32_bf16 v[152:155], v[208:211], a[192:195], v[192:195]\n"
"v_cvt_pk_bf16_f32 v105, v158, v159\n"
"v_mfma_f32_16x16x32_bf16 v[152:155], v[212:215], a[196:199], v[152:155]\n"
"ds_read_b128 v[208:211], v230 offset:49152\n"
"v_cvt_pk_bf16_f32 v104, v156, v157\n"
"v_mfma_f32_16x16x32_bf16 v[152:155], v[216:219], a[200:203], v[152:155]\n"
"v_exp_f32 v166, v166\n"
"v_mfma_f32_16x16x32_bf16 v[152:155], v[220:223], a[204:207], v[152:155]\n"
"s_waitcnt lgkmcnt(1)\n"
"ds_read_b128 v[216:219], v230 offset:49664\n"
"v_mfma_f32_16x16x32_bf16 a[232:235], v[124:127], a[244:247], a[232:235]\n"
"v_exp_f32 v169, v169\n"
"v_mfma_f32_16x16x32_bf16 v[24:27], v[128:131], a[244:247], v[24:27]\n"
"ds_read_b128 v[220:223], v231 offset:49664\n"
"v_mfma_f32_16x16x32_bf16 v[24:27], v[132:135], a[248:251], v[24:27]\n"
"v_exp_f32 v164, v164\n"
"v_mfma_f32_16x16x32_bf16 v[56:59], v[136:139], a[244:247], v[56:59]\n"
"ds_read_b128 v[212:215], v231 offset:49152\n"
"v_mfma_f32_16x16x32_bf16 v[56:59], v[140:143], a[248:251], v[56:59]\n"
"v_exp_f32 v178, v178\n"
"v_mfma_f32_16x16x32_bf16 v[88:91], v[144:147], a[244:247], v[88:91]\n"
"s_add_u32 m0, s44, 16384\n"
"ds_read_b64_tr_b16 a[246:247], v232 offset:36864\n"
"v_mfma_f32_16x16x32_bf16 v[88:91], v[148:151], a[248:251], v[88:91]\n"
"buffer_load_dwordx4 v240, s[16:19], s56 offen nt lds\n"
"v_mfma_f32_16x16x32_bf16 v[92:95], v[148:151], v[224:227], v[92:95]\n"
"v_exp_f32 v175, v175\n"
"v_mfma_f32_16x16x32_bf16 v[92:95], v[144:147], a[252:255], v[92:95]\n"
"v_exp_f32 v168, v168\n"
"v_mfma_f32_16x16x32_bf16 v[60:63], v[140:143], v[224:227], v[60:63]\n"
"v_exp_f32 v161, v161\n"
"v_mfma_f32_16x16x32_bf16 v[60:63], v[136:139], a[252:255], v[60:63]\n"
"v_exp_f32 v177, v177\n"
"v_mfma_f32_16x16x32_bf16 v[28:31], v[132:135], v[224:227], v[28:31]\n"
"ds_read_b64_tr_b16 v[226:227], v237 offset:36864\n"
"v_mfma_f32_16x16x32_bf16 v[28:31], v[128:131], a[252:255], v[28:31]\n"
"v_exp_f32 v172, v172\n"
"v_mfma_f32_16x16x32_bf16 a[236:239], v[124:127], a[252:255], a[236:239]\n"
"ds_read_b64_tr_b16 a[250:251], v236 offset:36864\n"
"v_cvt_pk_bf16_f32 v120, v164, v165\n"
"v_smfmac_f32_16x16x64_bf16 v[198:201], a[240:243], v[136:143], v241\n"
"s_waitcnt lgkmcnt(2)\n"
"ds_read_b64_tr_b16 a[252:253], v233 offset:32768\n"
"v_mfma_f32_16x16x32_bf16 v[156:159], v[208:211], a[0:3], v[180:183]\n"
"ds_read_b64_tr_b16 a[248:249], v236 offset:32768\n"
"v_cvt_pk_bf16_f32 v121, v166, v167\n"
"v_mfma_f32_16x16x32_bf16 v[156:159], v[212:215], a[4:7], v[156:159]\n"
"ds_read_b64_tr_b16 v[224:225], v237 offset:32768\n"
"v_cvt_pk_bf16_f32 v98, v168, v169\n"
"v_mfma_f32_16x16x32_bf16 v[156:159], v[216:219], a[8:11], v[156:159]\n"
"ds_read_b64_tr_b16 a[254:255], v233 offset:36864\n"
"v_cvt_pk_bf16_f32 v112, v160, v161\n"
"v_mfma_f32_16x16x32_bf16 v[156:159], v[220:223], a[12:15], v[156:159]\n"
"ds_read_b64_tr_b16 a[244:245], v232 offset:32768\n"
"v_mfma_f32_16x16x32_bf16 v[160:163], v[216:219], a[24:27], v[184:187]\n"
"v_exp_f32 v176, v176\n"
"v_mfma_f32_16x16x32_bf16 v[160:163], v[220:223], a[28:31], v[160:163]\n"
"v_exp_f32 v152, v152\n"
"v_mfma_f32_16x16x32_bf16 v[160:163], v[208:211], a[16:19], v[160:163]\n"
"s_add_u32 s40, s56, s52\n"
"v_cvt_pk_bf16_f32 v114, v176, v177\n"
"v_mfma_f32_16x16x32_bf16 v[160:163], v[212:215], a[20:23], v[160:163]\n"
"buffer_load_dwordx4 v207, s[16:19], s40 offen offset:1024 nt lds\n"
"v_mfma_f32_16x16x32_bf16 v[164:167], v[208:211], a[32:35], v[188:191]\n"
"v_exp_f32 v170, v170\n"
"v_mfma_f32_16x16x32_bf16 v[164:167], v[212:215], a[36:39], v[164:167]\n"
"v_exp_f32 v173, v173\n"
"v_mfma_f32_16x16x32_bf16 v[164:167], v[216:219], a[40:43], v[164:167]\n"
"v_exp_f32 v153, v153\n"
"v_mfma_f32_16x16x32_bf16 v[164:167], v[220:223], a[44:47], v[164:167]\n"
"s_waitcnt lgkmcnt(0)\n"
"v_exp_f32 v174, v174\n"
"v_mfma_f32_16x16x32_bf16 a[208:211], v[124:127], a[244:247], a[208:211]\n"
"ds_read_b128 v[208:211], v230 offset:53248\n"
"v_cvt_pk_bf16_f32 v107, v174, v175\n"
"v_mfma_f32_16x16x32_bf16 v[0:3], v[128:131], a[244:247], v[0:3]\n"
"v_exp_f32 v156, v156\n"
"v_mfma_f32_16x16x32_bf16 v[0:3], v[132:135], a[248:251], v[0:3]\n"
"ds_read_b128 v[220:223], v231 offset:53760\n"
"v_cvt_pk_bf16_f32 v106, v172, v173\n"
"v_mfma_f32_16x16x32_bf16 v[32:35], v[136:139], a[244:247], v[32:35]\n"
"v_exp_f32 v161, v161\n"
"v_mfma_f32_16x16x32_bf16 v[32:35], v[140:143], a[248:251], v[32:35]\n"
"ds_read_b128 v[216:219], v230 offset:53760\n"
"v_cvt_pk_bf16_f32 v122, v152, v153\n"
"v_mfma_f32_16x16x32_bf16 v[64:67], v[144:147], a[244:247], v[64:67]\n"
"s_add_u32 s40, s56, s53\n"
"ds_read_b128 v[212:215], v231 offset:53248\n"
"v_mfma_f32_16x16x32_bf16 v[64:67], v[148:151], a[248:251], v[64:67]\n"
"buffer_load_dwordx4 v240, s[16:19], s40 offen offset:2048 nt lds\n"
"v_mfma_f32_16x16x32_bf16 v[68:71], v[148:151], v[224:227], v[68:71]\n"
"v_exp_f32 v159, v159\n"
"v_mfma_f32_16x16x32_bf16 v[68:71], v[144:147], a[252:255], v[68:71]\n"
"v_exp_f32 v155, v155\n"
"v_mfma_f32_16x16x32_bf16 v[36:39], v[140:143], v[224:227], v[36:39]\n"
"v_exp_f32 v160, v160\n"
"v_mfma_f32_16x16x32_bf16 v[36:39], v[136:139], a[252:255], v[36:39]\n"
"v_exp_f32 v179, v179\n"
"v_mfma_f32_16x16x32_bf16 v[4:7], v[132:135], v[224:227], v[4:7]\n"
"ds_read_b64_tr_b16 a[244:245], v232 offset:33280\n"
"ds_read_b64_tr_b16 v[224:225], v237 offset:33280\n"
"v_mfma_f32_16x16x32_bf16 v[4:7], v[128:131], a[252:255], v[4:7]\n"
"v_exp_f32 v158, v158\n"
"v_mfma_f32_16x16x32_bf16 a[212:215], v[124:127], a[252:255], a[212:215]\n"
"ds_read_b64_tr_b16 a[246:247], v232 offset:37376\n"
"ds_read_b64_tr_b16 a[250:251], v236 offset:37376\n"
"v_smfmac_f32_16x16x64_bf16 v[200:203], a[240:243], v[144:151], v241\n"
"s_waitcnt lgkmcnt(4)\n"
"ds_read_b64_tr_b16 v[226:227], v237 offset:37376\n"
"v_cvt_pk_bf16_f32 v99, v170, v171\n"
"v_mfma_f32_16x16x32_bf16 v[168:171], v[208:211], a[0:3], v[180:183]\n"
"v_exp_f32 v164, v164\n"
"v_mfma_f32_16x16x32_bf16 v[168:171], v[212:215], a[4:7], v[168:171]\n"
"ds_read_b64_tr_b16 a[248:249], v236 offset:33280\n"
"ds_read_b64_tr_b16 a[254:255], v233 offset:37376\n"
"v_mfma_f32_16x16x32_bf16 v[168:171], v[216:219], a[8:11], v[168:171]\n"
"s_add_u32 s40, s56, s54\n"
"ds_read_b64_tr_b16 a[252:253], v233 offset:33280\n"
"v_mfma_f32_16x16x32_bf16 v[168:171], v[220:223], a[12:15], v[168:171]\n"
"buffer_load_dwordx4 v207, s[16:19], s40 offen offset:3072 nt lds\n"
"v_mfma_f32_16x16x32_bf16 v[172:175], v[216:219], a[24:27], v[184:187]\n"
"v_exp_f32 v154, v154\n"
"v_mfma_f32_16x16x32_bf16 v[172:175], v[220:223], a[28:31], v[172:175]\n"
"v_exp_f32 v167, v167\n"
"v_mfma_f32_16x16x32_bf16 v[172:175], v[208:211], a[16:19], v[172:175]\n"
"v_exp_f32 v163, v163\n"
"v_mfma_f32_16x16x32_bf16 v[172:175], v[212:215], a[20:23], v[172:175]\n"
"s_cbranch_vccnz LPCL3_%=\nLPKL3_%=:\n"
"v_cvt_pk_bf16_f32 v115, v178, v179\n"
"v_mfma_f32_16x16x32_bf16 v[176:179], v[208:211], a[32:35], v[188:191]\n"
"s_add_u32 s47, s47, 1\n"
"v_exp_f32 v165, v165\n"
"v_mfma_f32_16x16x32_bf16 v[176:179], v[212:215], a[36:39], v[176:179]\n"
"v_exp_f32 v157, v157\n"
"v_mfma_f32_16x16x32_bf16 v[176:179], v[216:219], a[40:43], v[176:179]\n"
"s_and_b32 s40, s47, 3\n"
"v_exp_f32 v166, v166\n"
"v_mfma_f32_16x16x32_bf16 v[176:179], v[220:223], a[44:47], v[176:179]\n"
"s_waitcnt lgkmcnt(0)\n"
"s_cbranch_scc1 LAL_%=\n"
"s_cmp_eq_u32 s47, 8\n"
"s_cbranch_scc1 LTH1_%=\n"
"s_cmp_eq_u32 s47, 12\n"
"s_cbranch_scc1 LTH2_%=\n"
"s_cmp_eq_u32 s47, 16\n"
"s_cbranch_scc1 LAX_%=\n"
"LTH0_%=:\n"
"v_mfma_f32_16x16x32_bf16 a[224:227], v[124:127], a[244:247], a[224:227]\n"
"v_exp_f32 v171, v171\n"
"v_mfma_f32_16x16x32_bf16 v[16:19], v[128:131], a[244:247], v[16:19]\n"
"s_waitcnt vmcnt(8)\n"
"v_exp_f32 v172, v172\n"
"v_mfma_f32_16x16x32_bf16 v[16:19], v[132:135], a[248:251], v[16:19]\n"
"s_barrier\n"
"v_mfma_f32_16x16x32_bf16 v[48:51], v[136:139], a[244:247], v[48:51]\n"
"v_exp_f32 v169, v169\n"
"v_mfma_f32_16x16x32_bf16 v[48:51], v[140:143], a[248:251], v[48:51]\n"
"ds_read_b128 v[220:223], v229 offset:512\n"
"v_cvt_pk_bf16_f32 v117, v166, v167\n"
"v_exp_f32 v176, v176\n"
"v_exp_f32 v177, v177\n"
"v_exp_f32 v178, v178\n"
"v_mfma_f32_16x16x32_bf16 v[80:83], v[144:147], a[244:247], v[80:83]\n"
"ds_read_b64_tr_b16 a[244:245], v234 offset:49152\n"
"v_cvt_pk_bf16_f32 v108, v160, v161\n"
"v_mfma_f32_16x16x32_bf16 v[80:83], v[148:151], a[248:251], v[80:83]\n"
"ds_read_b64_tr_b16 a[246:247], v234 offset:53248\n"
"ds_read_b64_tr_b16 a[248:249], v238 offset:49152\n"
"v_exp_f32 v162, v162\n"
"v_exp_f32 v168, v168\n"
"v_exp_f32 v173, v173\n"
"v_exp_f32 v175, v175\n"
"v_mfma_f32_16x16x32_bf16 v[84:87], v[148:151], v[224:227], v[84:87]\n"
"ds_read_b64_tr_b16 a[250:251], v238 offset:53248\n"
"v_cvt_pk_bf16_f32 v116, v164, v165\n"
"v_mfma_f32_16x16x32_bf16 v[84:87], v[144:147], a[252:255], v[84:87]\n"
"v_exp_f32 v170, v170\n"
"v_mfma_f32_16x16x32_bf16 v[52:55], v[140:143], v[224:227], v[52:55]\n"
"v_cvt_pk_bf16_f32 v103, v170, v171\n"
"v_cvt_pk_bf16_f32 v110, v172, v173\n"
"v_mfma_f32_16x16x32_bf16 v[52:55], v[136:139], a[252:255], v[52:55]\n"
"v_cvt_pk_bf16_f32 v123, v154, v155\n"
"v_cvt_pk_bf16_f32 v101, v158, v159\n"
"v_mfma_f32_16x16x32_bf16 v[20:23], v[132:135], v[224:227], v[20:23]\n"
"v_cvt_pk_bf16_f32 v100, v156, v157\n"
"v_cvt_pk_bf16_f32 v102, v168, v169\n"
"v_mfma_f32_16x16x32_bf16 v[20:23], v[128:131], a[252:255], v[20:23]\n"
"v_exp_f32 v179, v179\n"
"v_mfma_f32_16x16x32_bf16 a[228:231], v[124:127], a[252:255], a[228:231]\n"
"s_waitcnt lgkmcnt(2)\n"
"ds_read_b64_tr_b16 a[252:253], v235 offset:49152\n"
"ds_read_b64_tr_b16 v[226:227], v239 offset:53248\n"
"v_mfma_f32_16x16x32_bf16 a[216:219], v[120:123], a[244:247], a[216:219]\n"
"s_add_u32 m0, s44, 81920\n"
"ds_read_b64_tr_b16 v[224:225], v239 offset:49152\n"
"v_cvt_pk_bf16_f32 v119, v178, v179\n"
"v_smfmac_f32_16x16x64_bf16 v[196:199], a[240:243], v[96:103], v241\n"
"buffer_load_dwordx4 v240, s[20:23], s56 offen nt lds\n"
"v_mfma_f32_16x16x32_bf16 v[8:11], v[96:99], a[244:247], v[8:11]\n"
"s_waitcnt lgkmcnt(3)\n"
"ds_read_b64_tr_b16 a[254:255], v235 offset:53248\n"
"v_cvt_pk_bf16_f32 v118, v176, v177\n"
"v_mfma_f32_16x16x32_bf16 v[8:11], v[100:103], a[248:251], v[8:11]\n"
"v_exp_f32 v174, v174\n"
"v_smfmac_f32_16x16x64_bf16 v[200:203], a[240:243], v[112:119], v241\n"
"v_cvt_pk_bf16_f32 v109, v162, v163\n"
"v_cvt_pk_bf16_f32 v111, v174, v175\n"
"v_mfma_f32_16x16x32_bf16 v[40:43], v[104:107], a[244:247], v[40:43]\n"
"ds_read_b128 v[212:215], v229 offset:0\n"
"v_and_b32 v245, 0x3f0, v242\n"
"v_mfma_f32_16x16x32_bf16 v[40:43], v[108:111], a[248:251], v[40:43]\n"
"v_lshlrev_b32_e64 v244, 24, s43\n"
"v_mul_f32 v244, 0x45800000, v244\n"
"v_smfmac_f32_16x16x64_bf16 v[198:201], a[240:243], v[104:111], v241\n"
"ds_read_b128 v[208:211], v228 offset:0\n"
"v_add_f32 v196, v196, v197\n"
"v_mfma_f32_16x16x32_bf16 v[72:75], v[112:115], a[244:247], v[72:75]\n"
"ds_read_b64_tr_b16 a[244:245], v234 offset:49664\n"
"v_lshrrev_b32 v245, 1, v245\n"
"v_mfma_f32_16x16x32_bf16 v[72:75], v[116:119], a[248:251], v[72:75]\n"
"ds_read_b64_tr_b16 a[246:247], v234 offset:53760\n"
"v_sub_u32 v245, v242, v245\n"
"v_mfma_f32_16x16x32_bf16 v[202:205], a[240:243], v[120:123], v[202:205]\n"
"s_waitcnt lgkmcnt(4)\n"
"ds_read_b64_tr_b16 a[248:249], v238 offset:49664\n"
"v_add_f32 v200, v200, v201\n"
"v_mfma_f32_16x16x32_bf16 v[76:79], v[116:119], v[224:227], v[76:79]\n"
"v_add_f32 v198, v198, v199\n"
"v_add_f32 v254, v196, v198\n"
"v_mfma_f32_16x16x32_bf16 v[76:79], v[112:115], a[252:255], v[76:79]\n"
"ds_read_b64_tr_b16 a[250:251], v238 offset:53760\n"
"v_xor_b32 v250, 0x1000, v245\n"
"v_mfma_f32_16x16x32_bf16 v[44:47], v[108:111], v[224:227], v[44:47]\n"
"ds_read_b128 v[216:219], v228 offset:512\n"
"v_add_f32 v254, v254, v200\n"
"v_mfma_f32_16x16x32_bf16 v[44:47], v[104:107], a[252:255], v[44:47]\n"
"v_add_f32 v244, v244, v202\n"
"v_add_f32 v254, v254, v202\n"
"v_mfma_f32_16x16x32_bf16 v[12:15], v[100:103], v[224:227], v[12:15]\n"
"s_add_u32 s40, s56, s52\n"
"ds_read_b64_tr_b16 v[226:227], v239 offset:53760\n"
"v_min3_f32 v244, v244, v196, v198\n"
"v_mfma_f32_16x16x32_bf16 v[12:15], v[96:99], a[252:255], v[12:15]\n"
"buffer_load_dwordx4 v207, s[20:23], s40 offen offset:1024 nt lds\n"
"v_min_f32 v244, v244, v200\n"
"v_rcp_f32 v244, v244\n"
"v_mfma_f32_16x16x32_bf16 a[220:223], v[120:123], a[252:255], a[220:223]\n"
"s_waitcnt lgkmcnt(4)\n"
"v_fmac_f32 v254, 0x68800000, v244\n"
"v_cmp_nge_f32 vcc, s45, v254\n"
"s_cbranch_vccnz LSLOW_%=\nLBACK0_%=:\ns_mov_b64 vcc, s[64:65]\n"
"v_mfma_f32_16x16x32_bf16 a[232:235], v[120:123], a[244:247], a[232:235]\n"
"v_rcp_f32 v246, v196\n"
"v_accvgpr_mov_b32 a2, a50\n"
"v_accvgpr_mov_b32 a7, a55\n"
"v_accvgpr_mov_b32 a10, a58\n"
"v_accvgpr_mov_b32 a15, a63\n"
"v_accvgpr_mov_b32 a22, a70\n"
"v_accvgpr_mov_b32 a30, a78\n"
"v_bfe_u32 v149, v242, 8, 2\n"
"v_lshl_add_u32 v149, v149, 4, s43\n"
"v_mfma_f32_16x16x32_bf16 v[24:27], v[96:99], a[244:247], v[24:27]\n"
"s_waitcnt lgkmcnt(2)\n"
"ds_read_b128 v[124:127], v149 offset:0\n"
"v_accvgpr_mov_b32 a28, a76\n"
"v_mfma_f32_16x16x32_bf16 v[24:27], v[100:103], a[248:251], v[24:27]\n"
"ds_read_b128 v[132:135], v149 offset:512\n"
"v_mov_b32_dpp v246, v246 row_ror:12 row_mask:0x2 bank_mask:0xf\n"
"v_accvgpr_mov_b32 a3, a51\n"
"v_accvgpr_mov_b32 a12, a60\n"
"v_accvgpr_mov_b32 a18, a66\n"
"v_accvgpr_mov_b32 a19, a67\n"
"v_accvgpr_mov_b32 a26, a74\n"
"v_accvgpr_mov_b32 a36, a84\n"
"v_xor_b32 v148, 0x400, v243\n"
"v_xor_b32 v150, 64, v149\n"
"v_mfma_f32_16x16x32_bf16 v[56:59], v[104:107], a[244:247], v[56:59]\n"
"ds_read_b128 v[128:131], v150 offset:0\n"
"v_accvgpr_mov_b32 a39, a87\n"
"v_mfma_f32_16x16x32_bf16 v[56:59], v[108:111], a[248:251], v[56:59]\n"
"ds_read_b128 v[136:139], v150 offset:512\n"
"v_rcp_f32 v247, v198\n"
"v_mov_b32_dpp v246, v246 row_ror:8 row_mask:0x4 bank_mask:0xf\n"
"v_accvgpr_mov_b32 a0, a48\n"
"v_accvgpr_mov_b32 a1, a49\n"
"v_accvgpr_mov_b32 a4, a52\n"
"v_accvgpr_mov_b32 a5, a53\n"
"v_accvgpr_mov_b32 a8, a56\n"
"v_accvgpr_mov_b32 a16, a64\n"
"v_accvgpr_mov_b32 a25, a73\n"
"v_mfma_f32_16x16x32_bf16 v[88:91], v[112:115], a[244:247], v[88:91]\n"
"v_accvgpr_mov_b32 a20, a68\n"
"v_accvgpr_mov_b32 a29, a77\n"
"v_mfma_f32_16x16x32_bf16 v[88:91], v[116:119], a[248:251], v[88:91]\n"
"ds_read_b128 a[192:195], v243 offset:4096\n"
"ds_read_b128 a[200:203], v243 offset:6144\n"
"v_accvgpr_mov_b32 a6, a54\n"
"v_accvgpr_mov_b32 a9, a57\n"
"v_accvgpr_mov_b32 a11, a59\n"
"v_accvgpr_mov_b32 a13, a61\n"
"v_accvgpr_mov_b32 a14, a62\n"
"v_accvgpr_mov_b32 a17, a65\n"
"v_accvgpr_mov_b32 a23, a71\n"
"v_accvgpr_mov_b32 a31, a79\n"
"v_accvgpr_mov_b32 a33, a81\n"
"v_accvgpr_mov_b32 a35, a83\n"
"v_accvgpr_mov_b32 a44, a92\n"
"v_accvgpr_mov_b32 a47, a95\n"
"s_waitcnt lgkmcnt(5)\n"
"v_mfma_f32_16x16x32_bf16 v[180:183], v[124:127], a[0:3], 0\n"
"v_xor_b32 v244, 0x11000, v242\n"
"s_waitcnt lgkmcnt(3)\n"
"v_mfma_f32_16x16x32_bf16 v[180:183], v[128:131], a[4:7], v[180:183]\n"
"v_accvgpr_mov_b32 a34, a82\n"
"v_accvgpr_mov_b32 a43, a91\n"
"v_mfma_f32_16x16x32_bf16 v[180:183], v[132:135], a[8:11], v[180:183]\n"
"s_waitcnt lgkmcnt(2)\n"
"ds_read_b128 a[204:207], v148 offset:6144\n"
"v_accvgpr_mov_b32 a37, a85\n"
"v_mfma_f32_16x16x32_bf16 v[180:183], v[136:139], a[12:15], v[180:183]\n"
"ds_read_b128 a[196:199], v148 offset:4096\n"
"v_accvgpr_mov_b32 a21, a69\n"
"v_accvgpr_mov_b32 a24, a72\n"
"v_accvgpr_mov_b32 a32, a80\n"
"v_accvgpr_mov_b32 a38, a86\n"
"v_accvgpr_mov_b32 a40, a88\n"
"v_accvgpr_mov_b32 a42, a90\n"
"v_accvgpr_mov_b32 a46, a94\n"
"v_mfma_f32_16x16x32_bf16 v[184:187], v[124:127], a[16:19], 0\n"
"v_mov_b32_dpp v246, v246 row_ror:4 row_mask:0x8 bank_mask:0xf\n"
"v_accvgpr_mov_b32 a27, a75\n"
"v_mfma_f32_16x16x32_bf16 v[184:187], v[128:131], a[20:23], v[184:187]\n"
"v_mov_b32_dpp v247, v247 row_ror:12 row_mask:0x2 bank_mask:0xf\n"
"v_mfma_f32_16x16x32_bf16 v[184:187], v[132:135], a[24:27], v[184:187]\n"
"s_add_u32 s40, s56, s53\n"
"ds_read_b64_tr_b16 v[224:225], v239 offset:49664\n"
"v_mfma_f32_16x16x32_bf16 v[184:187], v[136:139], a[28:31], v[184:187]\n"
"buffer_load_dwordx4 v240, s[20:23], s40 offen offset:2048 nt lds\n"
"v_accvgpr_mov_b32 a41, a89\n"
"v_sub_f32 v180, s74, v180\n"
"v_sub_f32 v181, s74, v181\n"
"v_sub_f32 v183, s74, v183\n"
"v_mul_f32_dpp v26, v246, v26 row_newbcast:2 row_mask:0xf bank_mask:0xf\n"
"v_mfma_f32_16x16x32_bf16 v[188:191], v[124:127], a[32:35], 0\n"
"v_mov_b32_dpp v247, v247 row_ror:8 row_mask:0x4 bank_mask:0xf\n"
"v_accvgpr_mov_b32 a45, a93\n"
"v_mfma_f32_16x16x32_bf16 v[188:191], v[128:131], a[36:39], v[188:191]\n"
"v_mov_b32 v252, v192\n"
"v_mfma_f32_16x16x32_bf16 v[188:191], v[132:135], a[40:43], v[188:191]\n"
"v_mfma_f32_16x16x32_bf16 v[188:191], v[136:139], a[44:47], v[188:191]\n"
"v_mul_f32_dpp v11, v246, v11 row_newbcast:3 row_mask:0xf bank_mask:0xf\n"
"s_waitcnt lgkmcnt(1)\n"
"v_mfma_f32_16x16x32_bf16 v[192:195], v[124:127], a[192:195], 0\n"
"ds_read_b64_tr_b16 a[252:253], v235 offset:49664\n"
"v_sub_f32 v185, s74, v185\n"
"v_mfma_f32_16x16x32_bf16 v[192:195], v[128:131], a[196:199], v[192:195]\n"
"ds_read_b64_tr_b16 a[248:249], v236 offset:49152\n"
"v_sub_f32 v182, s74, v182\n"
"v_mfma_f32_16x16x32_bf16 v[192:195], v[132:135], a[200:203], v[192:195]\n"
"v_sub_f32 v184, s74, v184\n"
"v_mfma_f32_16x16x32_bf16 v[192:195], v[136:139], a[204:207], v[192:195]\n"
"ds_read_b64_tr_b16 a[254:255], v235 offset:53760\n"
"ds_read_b64_tr_b16 a[246:247], v232 offset:53248\n"
"v_mov_b32 v253, v202\n"
"v_sub_f32 v186, s74, v186\n"
"v_mfma_f32_16x16x32_bf16 v[152:155], v[208:211], a[0:3], v[180:183]\n"
"v_mfma_f32_16x16x32_bf16 v[152:155], v[212:215], a[4:7], v[152:155]\n"
"v_sub_f32 v187, s74, v187\n"
"v_mfma_f32_16x16x32_bf16 v[152:155], v[216:219], a[8:11], v[152:155]\n"
"s_add_u32 s40, s56, s54\n"
"v_mfma_f32_16x16x32_bf16 v[152:155], v[220:223], a[12:15], v[152:155]\n"
"buffer_load_dwordx4 v207, s[20:23], s40 offen offset:3072 nt lds\n"
"v_mov_b32_dpp v247, v247 row_ror:4 row_mask:0x8 bank_mask:0xf\n"
"v_sub_f32 v189, s74, v189\n"
"v_sub_f32 v190, s74, v190\n"
"v_sub_f32 v191, s74, v191\n"
"v_sub_f32 v192, s74, v192\n"
"v_mfma_f32_16x16x32_bf16 v[156:159], v[216:219], a[24:27], v[184:187]\n"
"s_add_u32 s55, s55, s59\n"
"v_mfma_f32_16x16x32_bf16 v[156:159], v[220:223], a[28:31], v[156:159]\n"
"s_or_b32 s55, s55, s58\n"
"v_sub_f32 v188, s74, v188\n"
"v_mfma_f32_16x16x32_bf16 v[156:159], v[208:211], a[16:19], v[156:159]\n"
"s_xor_b32 s56, s55, s57\n"
"v_exp_f32 v153, v153\n"
"v_mfma_f32_16x16x32_bf16 v[156:159], v[212:215], a[20:23], v[156:159]\n"
"ds_write_b64 v250, v[252:253] offset:52224\n"
"v_sub_f32 v193, s74, v193\n"
"v_sub_f32 v194, s74, v194\n"
"v_exp_f32 v152, v152\n"
"v_mfma_f32_16x16x32_bf16 v[160:163], v[208:211], a[32:35], v[188:191]\n"
"v_mfma_f32_16x16x32_bf16 v[160:163], v[212:215], a[36:39], v[160:163]\n"
"v_rcp_f32 v206, v200\n"
"v_mfma_f32_16x16x32_bf16 v[160:163], v[216:219], a[40:43], v[160:163]\n"
"v_sub_f32 v195, s74, v195\n"
"v_mfma_f32_16x16x32_bf16 v[160:163], v[220:223], a[44:47], v[160:163]\n"
"ds_write_b128 v244, a[216:219] offset:32768\n"
"v_mul_f32_dpp v25, v246, v25 row_newbcast:1 row_mask:0xf bank_mask:0xf\n"
"v_mfma_f32_16x16x32_bf16 v[164:167], v[216:219], a[200:203], v[192:195]\n"
"v_mov_b32_dpp v206, v206 row_ror:12 row_mask:0x2 bank_mask:0xf\n"
"v_mfma_f32_16x16x32_bf16 v[164:167], v[220:223], a[204:207], v[164:167]\n"
"v_mfma_f32_16x16x32_bf16 v[164:167], v[208:211], a[192:195], v[164:167]\n"
"ds_read_b128 v[208:211], v228 offset:4096\n"
"v_mfma_f32_16x16x32_bf16 v[164:167], v[212:215], a[196:199], v[164:167]\n"
"ds_read_b128 v[216:219], v228 offset:4608\n"
"ds_read_b128 v[220:223], v229 offset:4608\n"
"v_mov_b32_dpp v206, v206 row_ror:8 row_mask:0x4 bank_mask:0xf\n"
"v_exp_f32 v155, v155\n"
"v_exp_f32 v160, v160\n"
"s_branch LTC_%=\n"
"LTH1_%=:\n"
"v_mfma_f32_16x16x32_bf16 a[224:227], v[124:127], a[244:247], a[224:227]\n"
"v_exp_f32 v171, v171\n"
"v_mfma_f32_16x16x32_bf16 v[16:19], v[128:131], a[244:247], v[16:19]\n"
"s_waitcnt vmcnt(8)\n"
"v_exp_f32 v172, v172\n"
"v_mfma_f32_16x16x32_bf16 v[16:19], v[132:135], a[248:251], v[16:19]\n"
"s_barrier\n"
"v_mfma_f32_16x16x32_bf16 v[48:51], v[136:139], a[244:247], v[48:51]\n"
"v_exp_f32 v169, v169\n"
"v_mfma_f32_16x16x32_bf16 v[48:51], v[140:143], a[248:251], v[48:51]\n"
"ds_read_b128 v[220:223], v229 offset:512\n"
"v_cvt_pk_bf16_f32 v117, v166, v167\n"
"v_exp_f32 v176, v176\n"
"v_exp_f32 v177, v177\n"
"v_exp_f32 v178, v178\n"
"v_mfma_f32_16x16x32_bf16 v[80:83], v[144:147], a[244:247], v[80:83]\n"
"ds_read_b64_tr_b16 a[244:245], v234 offset:49152\n"
"v_cvt_pk_bf16_f32 v108, v160, v161\n"
"v_mfma_f32_16x16x32_bf16 v[80:83], v[148:151], a[248:251], v[80:83]\n"
"ds_read_b64_tr_b16 a[246:247], v234 offset:53248\n"
"ds_read_b64_tr_b16 a[248:249], v238 offset:49152\n"
"v_exp_f32 v162, v162\n"
"v_exp_f32 v168, v168\n"
"v_exp_f32 v173, v173\n"
"v_exp_f32 v175, v175\n"
"v_mfma_f32_16x16x32_bf16 v[84:87], v[148:151], v[224:227], v[84:87]\n"
"ds_read_b64_tr_b16 a[250:251], v238 offset:53248\n"
"v_cvt_pk_bf16_f32 v116, v164, v165\n"
"v_mfma_f32_16x16x32_bf16 v[84:87], v[144:147], a[252:255], v[84:87]\n"
"v_exp_f32 v170, v170\n"
"v_mfma_f32_16x16x32_bf16 v[52:55], v[140:143], v[224:227], v[52:55]\n"
"v_cvt_pk_bf16_f32 v103, v170, v171\n"
"v_cvt_pk_bf16_f32 v110, v172, v173\n"
"v_mfma_f32_16x16x32_bf16 v[52:55], v[136:139], a[252:255], v[52:55]\n"
"v_cvt_pk_bf16_f32 v123, v154, v155\n"
"v_cvt_pk_bf16_f32 v101, v158, v159\n"
"v_mfma_f32_16x16x32_bf16 v[20:23], v[132:135], v[224:227], v[20:23]\n"
"v_cvt_pk_bf16_f32 v100, v156, v157\n"
"v_cvt_pk_bf16_f32 v102, v168, v169\n"
"v_mfma_f32_16x16x32_bf16 v[20:23], v[128:131], a[252:255], v[20:23]\n"
"v_exp_f32 v179, v179\n"
"v_mfma_f32_16x16x32_bf16 a[228:231], v[124:127], a[252:255], a[228:231]\n"
"s_waitcnt lgkmcnt(2)\n"
"ds_read_b64_tr_b16 a[252:253], v235 offset:49152\n"
"ds_read_b64_tr_b16 v[226:227], v239 offset:53248\n"
"v_mfma_f32_16x16x32_bf16 a[216:219], v[120:123], a[244:247], a[216:219]\n"
"s_add_u32 m0, s44, 81920\n"
"ds_read_b64_tr_b16 v[224:225], v239 offset:49152\n"
"v_cvt_pk_bf16_f32 v119, v178, v179\n"
"v_smfmac_f32_16x16x64_bf16 v[196:199], a[240:243], v[96:103], v241\n"
"buffer_load_dwordx4 v240, s[20:23], s56 offen nt lds\n"
"v_mfma_f32_16x16x32_bf16 v[8:11], v[96:99], a[244:247], v[8:11]\n"
"s_waitcnt lgkmcnt(3)\n"
"ds_read_b64_tr_b16 a[254:255], v235 offset:53248\n"
"v_cvt_pk_bf16_f32 v118, v176, v177\n"
"v_mfma_f32_16x16x32_bf16 v[8:11], v[100:103], a[248:251], v[8:11]\n"
"v_exp_f32 v174, v174\n"
"v_smfmac_f32_16x16x64_bf16 v[200:203], a[240:243], v[112:119], v241\n"
"v_cvt_pk_bf16_f32 v109, v162, v163\n"
"v_cvt_pk_bf16_f32 v111, v174, v175\n"
"v_mfma_f32_16x16x32_bf16 v[40:43], v[104:107], a[244:247], v[40:43]\n"
"ds_read_b128 v[212:215], v229 offset:0\n"
"v_and_b32 v245, 0x3f0, v242\n"
"v_mfma_f32_16x16x32_bf16 v[40:43], v[108:111], a[248:251], v[40:43]\n"
"v_lshlrev_b32_e64 v244, 24, s43\n"
"v_mul_f32 v244, 0x45800000, v244\n"
"v_smfmac_f32_16x16x64_bf16 v[198:201], a[240:243], v[104:111], v241\n"
"ds_read_b128 v[208:211], v228 offset:0\n"
"v_add_f32 v196, v196, v197\n"
"v_mfma_f32_16x16x32_bf16 v[72:75], v[112:115], a[244:247], v[72:75]\n"
"ds_read_b64_tr_b16 a[244:245], v234 offset:49664\n"
"v_lshrrev_b32 v245, 1, v245\n"
"v_mfma_f32_16x16x32_bf16 v[72:75], v[116:119], a[248:251], v[72:75]\n"
"ds_read_b64_tr_b16 a[246:247], v234 offset:53760\n"
"v_sub_u32 v245, v242, v245\n"
"v_mfma_f32_16x16x32_bf16 v[202:205], a[240:243], v[120:123], v[202:205]\n"
"s_waitcnt lgkmcnt(4)\n"
"ds_read_b64_tr_b16 a[248:249], v238 offset:49664\n"
"v_add_f32 v200, v200, v201\n"
"v_mfma_f32_16x16x32_bf16 v[76:79], v[116:119], v[224:227], v[76:79]\n"
"v_add_f32 v198, v198, v199\n"
"v_add_f32 v254, v196, v198\n"
"v_mfma_f32_16x16x32_bf16 v[76:79], v[112:115], a[252:255], v[76:79]\n"
"ds_read_b64_tr_b16 a[250:251], v238 offset:53760\n"
"v_xor_b32 v250, 0x1000, v245\n"
"v_mfma_f32_16x16x32_bf16 v[44:47], v[108:111], v[224:227], v[44:47]\n"
"ds_read_b128 v[216:219], v228 offset:512\n"
"v_add_f32 v254, v254, v200\n"
"v_mfma_f32_16x16x32_bf16 v[44:47], v[104:107], a[252:255], v[44:47]\n"
"v_add_f32 v244, v244, v202\n"
"v_add_f32 v254, v254, v202\n"
"v_mfma_f32_16x16x32_bf16 v[12:15], v[100:103], v[224:227], v[12:15]\n"
"s_add_u32 s40, s56, s52\n"
"ds_read_b64_tr_b16 v[226:227], v239 offset:53760\n"
"v_min3_f32 v244, v244, v196, v198\n"
"v_mfma_f32_16x16x32_bf16 v[12:15], v[96:99], a[252:255], v[12:15]\n"
"buffer_load_dwordx4 v207, s[20:23], s40 offen offset:1024 nt lds\n"
"v_min_f32 v244, v244, v200\n"
"v_rcp_f32 v244, v244\n"
"v_mfma_f32_16x16x32_bf16 a[220:223], v[120:123], a[252:255], a[220:223]\n"
"s_waitcnt lgkmcnt(4)\n"
"v_fmac_f32 v254, 0x68800000, v244\n"
"v_cmp_nge_f32 vcc, s45, v254\n"
"s_cbranch_vccnz LSLOW_%=\nLBACK1_%=:\ns_mov_b64 vcc, s[64:65]\n"
"v_mfma_f32_16x16x32_bf16 a[232:235], v[120:123], a[244:247], a[232:235]\n"
"v_rcp_f32 v246, v196\n"
"v_accvgpr_mov_b32 a2, a98\n"
"v_accvgpr_mov_b32 a7, a103\n"
"v_accvgpr_mov_b32 a10, a106\n"
"v_accvgpr_mov_b32 a15, a111\n"
"v_accvgpr_mov_b32 a22, a118\n"
"v_accvgpr_mov_b32 a30, a126\n"
"v_bfe_u32 v149, v242, 8, 2\n"
"v_lshl_add_u32 v149, v149, 4, s43\n"
"v_mfma_f32_16x16x32_bf16 v[24:27], v[96:99], a[244:247], v[24:27]\n"
"s_waitcnt lgkmcnt(2)\n"
"ds_read_b128 v[124:127], v149 offset:0\n"
"v_accvgpr_mov_b32 a28, a124\n"
"v_mfma_f32_16x16x32_bf16 v[24:27], v[100:103], a[248:251], v[24:27]\n"
"ds_read_b128 v[132:135], v149 offset:512\n"
"v_mov_b32_dpp v246, v246 row_ror:12 row_mask:0x2 bank_mask:0xf\n"
"v_accvgpr_mov_b32 a3, a99\n"
"v_accvgpr_mov_b32 a12, a108\n"
"v_accvgpr_mov_b32 a18, a114\n"
"v_accvgpr_mov_b32 a19, a115\n"
"v_accvgpr_mov_b32 a26, a122\n"
"v_accvgpr_mov_b32 a36, a132\n"
"v_xor_b32 v148, 0x400, v243\n"
"v_xor_b32 v150, 64, v149\n"
"v_accvgpr_mov_b32 a124, a76\n"
"v_mfma_f32_16x16x32_bf16 v[56:59], v[104:107], a[244:247], v[56:59]\n"
"ds_read_b128 v[128:131], v150 offset:0\n"
"v_accvgpr_mov_b32 a39, a135\n"
"v_mfma_f32_16x16x32_bf16 v[56:59], v[108:111], a[248:251], v[56:59]\n"
"ds_read_b128 v[136:139], v150 offset:512\n"
"v_rcp_f32 v247, v198\n"
"v_mov_b32_dpp v246, v246 row_ror:8 row_mask:0x4 bank_mask:0xf\n"
"v_accvgpr_mov_b32 a0, a96\n"
"v_accvgpr_mov_b32 a1, a97\n"
"v_accvgpr_mov_b32 a4, a100\n"
"v_accvgpr_mov_b32 a5, a101\n"
"v_accvgpr_mov_b32 a8, a104\n"
"v_accvgpr_mov_b32 a16, a112\n"
"v_accvgpr_mov_b32 a25, a121\n"
"v_mfma_f32_16x16x32_bf16 v[88:91], v[112:115], a[244:247], v[88:91]\n"
"v_accvgpr_mov_b32 a20, a116\n"
"v_accvgpr_mov_b32 a29, a125\n"
"v_mfma_f32_16x16x32_bf16 v[88:91], v[116:119], a[248:251], v[88:91]\n"
"s_waitcnt lgkmcnt(3)\n"
"ds_read_b128 a[192:195], v243 offset:4096\n"
"ds_read_b128 a[200:203], v243 offset:6144\n"
"v_accvgpr_mov_b32 a6, a102\n"
"v_accvgpr_mov_b32 a9, a105\n"
"v_accvgpr_mov_b32 a11, a107\n"
"v_accvgpr_mov_b32 a13, a109\n"
"v_accvgpr_mov_b32 a14, a110\n"
"v_accvgpr_mov_b32 a17, a113\n"
"v_accvgpr_mov_b32 a23, a119\n"
"v_accvgpr_mov_b32 a31, a127\n"
"v_accvgpr_mov_b32 a33, a129\n"
"v_accvgpr_mov_b32 a35, a131\n"
"v_accvgpr_mov_b32 a44, a140\n"
"v_accvgpr_mov_b32 a47, a143\n"
"v_accvgpr_mov_b32 a107, a59\n"
"v_mfma_f32_16x16x32_bf16 v[180:183], v[124:127], a[0:3], 0\n"
"s_waitcnt lgkmcnt(3)\n"
"v_xor_b32 v244, 0x11000, v242\n"
"v_accvgpr_mov_b32 a132, a84\n"
"v_mfma_f32_16x16x32_bf16 v[180:183], v[128:131], a[4:7], v[180:183]\n"
"v_accvgpr_mov_b32 a34, a130\n"
"v_accvgpr_mov_b32 a43, a139\n"
"v_mfma_f32_16x16x32_bf16 v[180:183], v[132:135], a[8:11], v[180:183]\n"
"s_waitcnt lgkmcnt(2)\n"
"ds_read_b128 a[204:207], v148 offset:6144\n"
"v_accvgpr_mov_b32 a37, a133\n"
"v_mfma_f32_16x16x32_bf16 v[180:183], v[136:139], a[12:15], v[180:183]\n"
"ds_read_b128 a[196:199], v148 offset:4096\n"
"v_accvgpr_mov_b32 a21, a117\n"
"v_accvgpr_mov_b32 a24, a120\n"
"v_accvgpr_mov_b32 a32, a128\n"
"v_accvgpr_mov_b32 a38, a134\n"
"v_accvgpr_mov_b32 a40, a136\n"
"v_accvgpr_mov_b32 a42, a138\n"
"v_accvgpr_mov_b32 a46, a142\n"
"v_accvgpr_mov_b32 a117, a69\n"
"v_accvgpr_mov_b32 a111, a63\n"
"v_mfma_f32_16x16x32_bf16 v[184:187], v[124:127], a[16:19], 0\n"
"v_mov_b32_dpp v246, v246 row_ror:4 row_mask:0x8 bank_mask:0xf\n"
"v_accvgpr_mov_b32 a27, a123\n"
"v_mfma_f32_16x16x32_bf16 v[184:187], v[128:131], a[20:23], v[184:187]\n"
"v_mov_b32_dpp v247, v247 row_ror:12 row_mask:0x2 bank_mask:0xf\n"
"v_accvgpr_mov_b32 a97, a49\n"
"v_mfma_f32_16x16x32_bf16 v[184:187], v[132:135], a[24:27], v[184:187]\n"
"s_add_u32 s40, s56, s53\n"
"ds_read_b64_tr_b16 v[224:225], v239 offset:49664\n"
"v_accvgpr_mov_b32 a112, a64\n"
"v_mfma_f32_16x16x32_bf16 v[184:187], v[136:139], a[28:31], v[184:187]\n"
"buffer_load_dwordx4 v240, s[20:23], s40 offen offset:2048 nt lds\n"
"v_accvgpr_mov_b32 a41, a137\n"
"v_accvgpr_mov_b32 a126, a78\n"
"v_accvgpr_mov_b32 a114, a66\n"
"v_accvgpr_mov_b32 a137, a89\n"
"v_sub_f32 v180, s74, v180\n"
"v_sub_f32 v181, s74, v181\n"
"v_sub_f32 v183, s74, v183\n"
"v_mul_f32_dpp v26, v246, v26 row_newbcast:2 row_mask:0xf bank_mask:0xf\n"
"v_mfma_f32_16x16x32_bf16 v[188:191], v[124:127], a[32:35], 0\n"
"v_mov_b32_dpp v247, v247 row_ror:8 row_mask:0x4 bank_mask:0xf\n"
"v_accvgpr_mov_b32 a45, a141\n"
"v_mfma_f32_16x16x32_bf16 v[188:191], v[128:131], a[36:39], v[188:191]\n"
"v_mov_b32 v252, v192\n"
"v_accvgpr_mov_b32 a133, a85\n"
"v_mfma_f32_16x16x32_bf16 v[188:191], v[132:135], a[40:43], v[188:191]\n"
"v_accvgpr_mov_b32 a118, a70\n"
"v_accvgpr_mov_b32 a105, a57\n"
"v_mfma_f32_16x16x32_bf16 v[188:191], v[136:139], a[44:47], v[188:191]\n"
"s_waitcnt lgkmcnt(1)\n"
"v_accvgpr_mov_b32 a109, a61\n"
"v_mul_f32_dpp v11, v246, v11 row_newbcast:3 row_mask:0xf bank_mask:0xf\n"
"v_mfma_f32_16x16x32_bf16 v[192:195], v[124:127], a[192:195], 0\n"
"ds_read_b64_tr_b16 a[252:253], v235 offset:49664\n"
"v_sub_f32 v185, s74, v185\n"
"v_mfma_f32_16x16x32_bf16 v[192:195], v[128:131], a[196:199], v[192:195]\n"
"ds_read_b64_tr_b16 a[248:249], v236 offset:49152\n"
"v_sub_f32 v182, s74, v182\n"
"v_mfma_f32_16x16x32_bf16 v[192:195], v[132:135], a[200:203], v[192:195]\n"
"v_accvgpr_mov_b32 a138, a90\n"
"v_sub_f32 v184, s74, v184\n"
"v_mfma_f32_16x16x32_bf16 v[192:195], v[136:139], a[204:207], v[192:195]\n"
"ds_read_b64_tr_b16 a[254:255], v235 offset:53760\n"
"ds_read_b64_tr_b16 a[246:247], v232 offset:53248\n"
"v_mov_b32 v253, v202\n"
"v_accvgpr_mov_b32 a116, a68\n"
"v_accvgpr_mov_b32 a125, a77\n"
"v_accvgpr_mov_b32 a113, a65\n"
"v_accvgpr_mov_b32 a128, a80\n"
"v_accvgpr_mov_b32 a130, a82\n"
"v_accvgpr_mov_b32 a136, a88\n"
"v_sub_f32 v186, s74, v186\n"
"v_mfma_f32_16x16x32_bf16 v[152:155], v[208:211], a[0:3], v[180:183]\n"
"v_accvgpr_mov_b32 a103, a55\n"
"v_accvgpr_mov_b32 a120, a72\n"
"v_mfma_f32_16x16x32_bf16 v[152:155], v[212:215], a[4:7], v[152:155]\n"
"v_accvgpr_mov_b32 a141, a93\n"
"v_sub_f32 v187, s74, v187\n"
"v_mfma_f32_16x16x32_bf16 v[152:155], v[216:219], a[8:11], v[152:155]\n"
"s_add_u32 s40, s56, s54\n"
"v_accvgpr_mov_b32 a127, a79\n"
"v_accvgpr_mov_b32 a140, a92\n"
"v_mfma_f32_16x16x32_bf16 v[152:155], v[220:223], a[12:15], v[152:155]\n"
"buffer_load_dwordx4 v207, s[20:23], s40 offen offset:3072 nt lds\n"
"v_mov_b32_dpp v247, v247 row_ror:4 row_mask:0x8 bank_mask:0xf\n"
"v_accvgpr_mov_b32 a121, a73\n"
"v_accvgpr_mov_b32 a122, a74\n"
"v_accvgpr_mov_b32 a123, a75\n"
"v_sub_f32 v189, s74, v189\n"
"v_sub_f32 v190, s74, v190\n"
"v_sub_f32 v191, s74, v191\n"
"v_sub_f32 v192, s74, v192\n"
"v_mfma_f32_16x16x32_bf16 v[156:159], v[216:219], a[24:27], v[184:187]\n"
"s_add_u32 s55, s55, s59\n"
"v_accvgpr_mov_b32 a143, a95\n"
"v_accvgpr_mov_b32 a115, a67\n"
"v_mfma_f32_16x16x32_bf16 v[156:159], v[220:223], a[28:31], v[156:159]\n"
"s_or_b32 s55, s55, s58\n"
"v_accvgpr_mov_b32 a101, a53\n"
"v_sub_f32 v188, s74, v188\n"
"v_mfma_f32_16x16x32_bf16 v[156:159], v[208:211], a[16:19], v[156:159]\n"
"s_xor_b32 s56, s55, s57\n"
"v_exp_f32 v153, v153\n"
"v_mfma_f32_16x16x32_bf16 v[156:159], v[212:215], a[20:23], v[156:159]\n"
"ds_write_b64 v250, v[252:253] offset:52224\n"
"v_accvgpr_mov_b32 a100, a52\n"
"v_accvgpr_mov_b32 a135, a87\n"
"v_accvgpr_mov_b32 a98, a50\n"
"v_sub_f32 v193, s74, v193\n"
"v_sub_f32 v194, s74, v194\n"
"v_exp_f32 v152, v152\n"
"v_mfma_f32_16x16x32_bf16 v[160:163], v[208:211], a[32:35], v[188:191]\n"
"v_accvgpr_mov_b32 a110, a62\n"
"v_accvgpr_mov_b32 a96, a48\n"
"v_mfma_f32_16x16x32_bf16 v[160:163], v[212:215], a[36:39], v[160:163]\n"
"v_rcp_f32 v206, v200\n"
"v_mfma_f32_16x16x32_bf16 v[160:163], v[216:219], a[40:43], v[160:163]\n"
"v_accvgpr_mov_b32 a142, a94\n"
"v_sub_f32 v195, s74, v195\n"
"v_mfma_f32_16x16x32_bf16 v[160:163], v[220:223], a[44:47], v[160:163]\n"
"ds_write_b128 v244, a[216:219] offset:32768\n"
"v_accvgpr_mov_b32 a102, a54\n"
"v_accvgpr_mov_b32 a108, a60\n"
"v_accvgpr_mov_b32 a99, a51\n"
"v_accvgpr_mov_b32 a129, a81\n"
"v_mul_f32_dpp v25, v246, v25 row_newbcast:1 row_mask:0xf bank_mask:0xf\n"
"v_mfma_f32_16x16x32_bf16 v[164:167], v[216:219], a[200:203], v[192:195]\n"
"v_mov_b32_dpp v206, v206 row_ror:12 row_mask:0x2 bank_mask:0xf\n"
"v_accvgpr_mov_b32 a119, a71\n"
"v_mfma_f32_16x16x32_bf16 v[164:167], v[220:223], a[204:207], v[164:167]\n"
"v_accvgpr_mov_b32 a131, a83\n"
"v_accvgpr_mov_b32 a139, a91\n"
"v_mfma_f32_16x16x32_bf16 v[164:167], v[208:211], a[192:195], v[164:167]\n"
"ds_read_b128 v[208:211], v228 offset:4096\n"
"v_accvgpr_mov_b32 a106, a58\n"
"v_mfma_f32_16x16x32_bf16 v[164:167], v[212:215], a[196:199], v[164:167]\n"
"s_waitcnt lgkmcnt(2)\n"
"ds_read_b128 v[216:219], v228 offset:4608\n"
"ds_read_b128 v[220:223], v229 offset:4608\n"
"v_mov_b32_dpp v206, v206 row_ror:8 row_mask:0x4 bank_mask:0xf\n"
"v_accvgpr_mov_b32 a134, a86\n"
"v_accvgpr_mov_b32 a104, a56\n"
"v_exp_f32 v155, v155\n"
"v_exp_f32 v160, v160\n"
"s_branch LTC_%=\n"
"LTH2_%=:\n"
"v_mfma_f32_16x16x32_bf16 a[224:227], v[124:127], a[244:247], a[224:227]\n"
"v_exp_f32 v171, v171\n"
"v_mfma_f32_16x16x32_bf16 v[16:19], v[128:131], a[244:247], v[16:19]\n"
"s_waitcnt vmcnt(8)\n"
"v_exp_f32 v172, v172\n"
"v_mfma_f32_16x16x32_bf16 v[16:19], v[132:135], a[248:251], v[16:19]\n"
"s_barrier\n"
"v_mfma_f32_16x16x32_bf16 v[48:51], v[136:139], a[244:247], v[48:51]\n"
"v_exp_f32 v169, v169\n"
"v_mfma_f32_16x16x32_bf16 v[48:51], v[140:143], a[248:251], v[48:51]\n"
"ds_read_b128 v[220:223], v229 offset:512\n"
"v_cvt_pk_bf16_f32 v117, v166, v167\n"
"v_exp_f32 v176, v176\n"
"v_exp_f32 v177, v177\n"
"v_exp_f32 v178, v178\n"
"v_mfma_f32_16x16x32_bf16 v[80:83], v[144:147], a[244:247], v[80:83]\n"
"ds_read_b64_tr_b16 a[244:245], v234 offset:49152\n"
"v_cvt_pk_bf16_f32 v108, v160, v161\n"
"v_mfma_f32_16x16x32_bf16 v[80:83], v[148:151], a[248:251], v[80:83]\n"
"ds_read_b64_tr_b16 a[246:247], v234 offset:53248\n"
"ds_read_b64_tr_b16 a[248:249], v238 offset:49152\n"
"v_exp_f32 v162, v162\n"
"v_exp_f32 v168, v168\n"
"v_exp_f32 v173, v173\n"
"v_exp_f32 v175, v175\n"
"v_mfma_f32_16x16x32_bf16 v[84:87], v[148:151], v[224:227], v[84:87]\n"
"ds_read_b64_tr_b16 a[250:251], v238 offset:53248\n"
"v_cvt_pk_bf16_f32 v116, v164, v165\n"
"v_mfma_f32_16x16x32_bf16 v[84:87], v[144:147], a[252:255], v[84:87]\n"
"v_exp_f32 v170, v170\n"
"v_mfma_f32_16x16x32_bf16 v[52:55], v[140:143], v[224:227], v[52:55]\n"
"v_cvt_pk_bf16_f32 v103, v170, v171\n"
"v_cvt_pk_bf16_f32 v110, v172, v173\n"
"v_mfma_f32_16x16x32_bf16 v[52:55], v[136:139], a[252:255], v[52:55]\n"
"v_cvt_pk_bf16_f32 v123, v154, v155\n"
"v_cvt_pk_bf16_f32 v101, v158, v159\n"
"v_mfma_f32_16x16x32_bf16 v[20:23], v[132:135], v[224:227], v[20:23]\n"
"v_cvt_pk_bf16_f32 v100, v156, v157\n"
"v_cvt_pk_bf16_f32 v102, v168, v169\n"
"v_mfma_f32_16x16x32_bf16 v[20:23], v[128:131], a[252:255], v[20:23]\n"
"v_exp_f32 v179, v179\n"
"v_mfma_f32_16x16x32_bf16 a[228:231], v[124:127], a[252:255], a[228:231]\n"
"s_waitcnt lgkmcnt(2)\n"
"ds_read_b64_tr_b16 a[252:253], v235 offset:49152\n"
"ds_read_b64_tr_b16 v[226:227], v239 offset:53248\n"
"v_mfma_f32_16x16x32_bf16 a[216:219], v[120:123], a[244:247], a[216:219]\n"
"s_add_u32 m0, s44, 81920\n"
"ds_read_b64_tr_b16 v[224:225], v239 offset:49152\n"
"v_cvt_pk_bf16_f32 v119, v178, v179\n"
"v_smfmac_f32_16x16x64_bf16 v[196:199], a[240:243], v[96:103], v241\n"
"buffer_load_dwordx4 v240, s[20:23], s56 offen nt lds\n"
"v_mfma_f32_16x16x32_bf16 v[8:11], v[96:99], a[244:247], v[8:11]\n"
"s_waitcnt lgkmcnt(3)\n"
"ds_read_b64_tr_b16 a[254:255], v235 offset:53248\n"
"v_cvt_pk_bf16_f32 v118, v176, v177\n"
"v_mfma_f32_16x16x32_bf16 v[8:11], v[100:103], a[248:251], v[8:11]\n"
"v_exp_f32 v174, v174\n"
"v_smfmac_f32_16x16x64_bf16 v[200:203], a[240:243], v[112:119], v241\n"
"v_cvt_pk_bf16_f32 v109, v162, v163\n"
"v_cvt_pk_bf16_f32 v111, v174, v175\n"
"v_mfma_f32_16x16x32_bf16 v[40:43], v[104:107], a[244:247], v[40:43]\n"
"ds_read_b128 v[212:215], v229 offset:0\n"
"v_and_b32 v245, 0x3f0, v242\n"
"v_mfma_f32_16x16x32_bf16 v[40:43], v[108:111], a[248:251], v[40:43]\n"
"v_lshlrev_b32_e64 v244, 24, s43\n"
"v_mul_f32 v244, 0x45800000, v244\n"
"v_smfmac_f32_16x16x64_bf16 v[198:201], a[240:243], v[104:111], v241\n"
"ds_read_b128 v[208:211], v228 offset:0\n"
"v_add_f32 v196, v196, v197\n"
"v_mfma_f32_16x16x32_bf16 v[72:75], v[112:115], a[244:247], v[72:75]\n"
"ds_read_b64_tr_b16 a[244:245], v234 offset:49664\n"
"v_lshrrev_b32 v245, 1, v245\n"
"v_mfma_f32_16x16x32_bf16 v[72:75], v[116:119], a[248:251], v[72:75]\n"
"ds_read_b64_tr_b16 a[246:247], v234 offset:53760\n"
"v_sub_u32 v245, v242, v245\n"
"v_mfma_f32_16x16x32_bf16 v[202:205], a[240:243], v[120:123], v[202:205]\n"
"s_waitcnt lgkmcnt(4)\n"
"ds_read_b64_tr_b16 a[248:249], v238 offset:49664\n"
"v_add_f32 v200, v200, v201\n"
"v_mfma_f32_16x16x32_bf16 v[76:79], v[116:119], v[224:227], v[76:79]\n"
"v_add_f32 v198, v198, v199\n"
"v_add_f32 v254, v196, v198\n"
"v_mfma_f32_16x16x32_bf16 v[76:79], v[112:115], a[252:255], v[76:79]\n"
"ds_read_b64_tr_b16 a[250:251], v238 offset:53760\n"
"v_xor_b32 v250, 0x1000, v245\n"
"v_mfma_f32_16x16x32_bf16 v[44:47], v[108:111], v[224:227], v[44:47]\n"
"ds_read_b128 v[216:219], v228 offset:512\n"
"v_add_f32 v254, v254, v200\n"
"v_mfma_f32_16x16x32_bf16 v[44:47], v[104:107], a[252:255], v[44:47]\n"
"v_add_f32 v244, v244, v202\n"
"v_add_f32 v254, v254, v202\n"
"v_mfma_f32_16x16x32_bf16 v[12:15], v[100:103], v[224:227], v[12:15]\n"
"s_add_u32 s40, s56, s52\n"
"ds_read_b64_tr_b16 v[226:227], v239 offset:53760\n"
"v_min3_f32 v244, v244, v196, v198\n"
"v_mfma_f32_16x16x32_bf16 v[12:15], v[96:99], a[252:255], v[12:15]\n"
"buffer_load_dwordx4 v207, s[20:23], s40 offen offset:1024 nt lds\n"
"v_min_f32 v244, v244, v200\n"
"v_rcp_f32 v244, v244\n"
"v_mfma_f32_16x16x32_bf16 a[220:223], v[120:123], a[252:255], a[220:223]\n"
"s_waitcnt lgkmcnt(4)\n"
"v_fmac_f32 v254, 0x68800000, v244\n"
"v_cmp_nge_f32 vcc, s45, v254\n"
"s_cbranch_vccnz LSLOW_%=\nLBACK2_%=:\ns_mov_b64 vcc, s[64:65]\n"
"v_mfma_f32_16x16x32_bf16 a[232:235], v[120:123], a[244:247], a[232:235]\n"
"v_rcp_f32 v246, v196\n"
"v_accvgpr_mov_b32 a2, a146\n"
"v_accvgpr_mov_b32 a7, a151\n"
"v_accvgpr_mov_b32 a10, a154\n"
"v_accvgpr_mov_b32 a15, a159\n"
"v_accvgpr_mov_b32 a22, a166\n"
"v_accvgpr_mov_b32 a30, a174\n"
"v_bfe_u32 v149, v242, 8, 2\n"
"v_lshl_add_u32 v149, v149, 4, s43\n"
"v_mfma_f32_16x16x32_bf16 v[24:27], v[96:99], a[244:247], v[24:27]\n"
"s_waitcnt lgkmcnt(2)\n"
"ds_read_b128 v[124:127], v149 offset:0\n"
"v_accvgpr_mov_b32 a28, a172\n"
"v_mfma_f32_16x16x32_bf16 v[24:27], v[100:103], a[248:251], v[24:27]\n"
"ds_read_b128 v[132:135], v149 offset:512\n"
"v_mov_b32_dpp v246, v246 row_ror:12 row_mask:0x2 bank_mask:0xf\n"
"v_accvgpr_mov_b32 a3, a147\n"
"v_accvgpr_mov_b32 a12, a156\n"
"v_accvgpr_mov_b32 a18, a162\n"
"v_accvgpr_mov_b32 a19, a163\n"
"v_accvgpr_mov_b32 a26, a170\n"
"v_accvgpr_mov_b32 a36, a180\n"
"v_xor_b32 v148, 0x400, v243\n"
"v_xor_b32 v150, 64, v149\n"
"v_accvgpr_mov_b32 a172, a76\n"
"v_mfma_f32_16x16x32_bf16 v[56:59], v[104:107], a[244:247], v[56:59]\n"
"ds_read_b128 v[128:131], v150 offset:0\n"
"v_accvgpr_mov_b32 a39, a183\n"
"v_mfma_f32_16x16x32_bf16 v[56:59], v[108:111], a[248:251], v[56:59]\n"
"ds_read_b128 v[136:139], v150 offset:512\n"
"v_rcp_f32 v247, v198\n"
"v_mov_b32_dpp v246, v246 row_ror:8 row_mask:0x4 bank_mask:0xf\n"
"v_accvgpr_mov_b32 a0, a144\n"
"v_accvgpr_mov_b32 a1, a145\n"
"v_accvgpr_mov_b32 a4, a148\n"
"v_accvgpr_mov_b32 a5, a149\n"
"v_accvgpr_mov_b32 a8, a152\n"
"v_accvgpr_mov_b32 a16, a160\n"
"v_accvgpr_mov_b32 a25, a169\n"
"v_mfma_f32_16x16x32_bf16 v[88:91], v[112:115], a[244:247], v[88:91]\n"
"v_accvgpr_mov_b32 a20, a164\n"
"v_accvgpr_mov_b32 a29, a173\n"
"v_mfma_f32_16x16x32_bf16 v[88:91], v[116:119], a[248:251], v[88:91]\n"
"s_waitcnt lgkmcnt(3)\n"
"ds_read_b128 a[192:195], v243 offset:4096\n"
"ds_read_b128 a[200:203], v243 offset:6144\n"
"v_accvgpr_mov_b32 a6, a150\n"
"v_accvgpr_mov_b32 a9, a153\n"
"v_accvgpr_mov_b32 a11, a155\n"
"v_accvgpr_mov_b32 a13, a157\n"
"v_accvgpr_mov_b32 a14, a158\n"
"v_accvgpr_mov_b32 a17, a161\n"
"v_accvgpr_mov_b32 a23, a167\n"
"v_accvgpr_mov_b32 a31, a175\n"
"v_accvgpr_mov_b32 a33, a177\n"
"v_accvgpr_mov_b32 a35, a179\n"
"v_accvgpr_mov_b32 a44, a188\n"
"v_accvgpr_mov_b32 a47, a191\n"
"v_accvgpr_mov_b32 a155, a59\n"
"v_mfma_f32_16x16x32_bf16 v[180:183], v[124:127], a[0:3], 0\n"
"s_waitcnt lgkmcnt(3)\n"
"v_xor_b32 v244, 0x11000, v242\n"
"v_accvgpr_mov_b32 a180, a84\n"
"v_mfma_f32_16x16x32_bf16 v[180:183], v[128:131], a[4:7], v[180:183]\n"
"v_accvgpr_mov_b32 a34, a178\n"
"v_accvgpr_mov_b32 a43, a187\n"
"v_mfma_f32_16x16x32_bf16 v[180:183], v[132:135], a[8:11], v[180:183]\n"
"s_waitcnt lgkmcnt(2)\n"
"ds_read_b128 a[204:207], v148 offset:6144\n"
"v_accvgpr_mov_b32 a37, a181\n"
"v_mfma_f32_16x16x32_bf16 v[180:183], v[136:139], a[12:15], v[180:183]\n"
"ds_read_b128 a[196:199], v148 offset:4096\n"
"v_accvgpr_mov_b32 a21, a165\n"
"v_accvgpr_mov_b32 a24, a168\n"
"v_accvgpr_mov_b32 a32, a176\n"
"v_accvgpr_mov_b32 a38, a182\n"
"v_accvgpr_mov_b32 a40, a184\n"
"v_accvgpr_mov_b32 a42, a186\n"
"v_accvgpr_mov_b32 a46, a190\n"
"v_accvgpr_mov_b32 a165, a69\n"
"v_accvgpr_mov_b32 a159, a63\n"
"v_mfma_f32_16x16x32_bf16 v[184:187], v[124:127], a[16:19], 0\n"
"v_mov_b32_dpp v246, v246 row_ror:4 row_mask:0x8 bank_mask:0xf\n"
"v_accvgpr_mov_b32 a27, a171\n"
"v_mfma_f32_16x16x32_bf16 v[184:187], v[128:131], a[20:23], v[184:187]\n"
"v_mov_b32_dpp v247, v247 row_ror:12 row_mask:0x2 bank_mask:0xf\n"
"v_accvgpr_mov_b32 a145, a49\n"
"v_mfma_f32_16x16x32_bf16 v[184:187], v[132:135], a[24:27], v[184:187]\n"
"s_add_u32 s40, s56, s53\n"
"ds_read_b64_tr_b16 v[224:225], v239 offset:49664\n"
"v_accvgpr_mov_b32 a160, a64\n"
"v_mfma_f32_16x16x32_bf16 v[184:187], v[136:139], a[28:31], v[184:187]\n"
"buffer_load_dwordx4 v240, s[20:23], s40 offen offset:2048 nt lds\n"
"v_accvgpr_mov_b32 a41, a185\n"
"v_accvgpr_mov_b32 a174, a78\n"
"v_accvgpr_mov_b32 a162, a66\n"
"v_accvgpr_mov_b32 a185, a89\n"
"v_sub_f32 v180, s74, v180\n"
"v_sub_f32 v181, s74, v181\n"
"v_sub_f32 v183, s74, v183\n"
"v_mul_f32_dpp v26, v246, v26 row_newbcast:2 row_mask:0xf bank_mask:0xf\n"
"v_mfma_f32_16x16x32_bf16 v[188:191], v[124:127], a[32:35], 0\n"
"v_mov_b32_dpp v247, v247 row_ror:8 row_mask:0x4 bank_mask:0xf\n"
"v_accvgpr_mov_b32 a45, a189\n"
"v_mfma_f32_16x16x32_bf16 v[188:191], v[128:131], a[36:39], v[188:191]\n"
"v_mov_b32 v252, v192\n"
"v_accvgpr_mov_b32 a181, a85\n"
"v_mfma_f32_16x16x32_bf16 v[188:191], v[132:135], a[40:43], v[188:191]\n"
"v_accvgpr_mov_b32 a166, a70\n"
"v_accvgpr_mov_b32 a153, a57\n"
"v_mfma_f32_16x16x32_bf16 v[188:191], v[136:139], a[44:47], v[188:191]\n"
"s_waitcnt lgkmcnt(1)\n"
"v_accvgpr_mov_b32 a157, a61\n"
"v_mul_f32_dpp v11, v246, v11 row_newbcast:3 row_mask:0xf bank_mask:0xf\n"
"v_mfma_f32_16x16x32_bf16 v[192:195], v[124:127], a[192:195], 0\n"
"ds_read_b64_tr_b16 a[252:253], v235 offset:49664\n"
"v_sub_f32 v185, s74, v185\n"
"v_mfma_f32_16x16x32_bf16 v[192:195], v[128:131], a[196:199], v[192:195]\n"
"ds_read_b64_tr_b16 a[248:249], v236 offset:49152\n"
"v_sub_f32 v182, s74, v182\n"
"v_mfma_f32_16x16x32_bf16 v[192:195], v[132:135], a[200:203], v[192:195]\n"
"v_accvgpr_mov_b32 a186, a90\n"
"v_sub_f32 v184, s74, v184\n"
"v_mfma_f32_16x16x32_bf16 v[192:195], v[136:139], a[204:207], v[192:195]\n"
"ds_read_b64_tr_b16 a[254:255], v235 offset:53760\n"
"ds_read_b64_tr_b16 a[246:247], v232 offset:53248\n"
"v_mov_b32 v253, v202\n"
"v_accvgpr_mov_b32 a164, a68\n"
"v_accvgpr_mov_b32 a173, a77\n"
"v_accvgpr_mov_b32 a161, a65\n"
"v_accvgpr_mov_b32 a176, a80\n"
"v_accvgpr_mov_b32 a178, a82\n"
"v_accvgpr_mov_b32 a184, a88\n"
"v_sub_f32 v186, s74, v186\n"
"v_mfma_f32_16x16x32_bf16 v[152:155], v[208:211], a[0:3], v[180:183]\n"
"v_accvgpr_mov_b32 a151, a55\n"
"v_accvgpr_mov_b32 a168, a72\n"
"v_mfma_f32_16x16x32_bf16 v[152:155], v[212:215], a[4:7], v[152:155]\n"
"v_accvgpr_mov_b32 a189, a93\n"
"v_sub_f32 v187, s74, v187\n"
"v_mfma_f32_16x16x32_bf16 v[152:155], v[216:219], a[8:11], v[152:155]\n"
"s_add_u32 s40, s56, s54\n"
"v_accvgpr_mov_b32 a175, a79\n"
"v_accvgpr_mov_b32 a188, a92\n"
"v_mfma_f32_16x16x32_bf16 v[152:155], v[220:223], a[12:15], v[152:155]\n"
"buffer_load_dwordx4 v207, s[20:23], s40 offen offset:3072 nt lds\n"
"v_mov_b32_dpp v247, v247 row_ror:4 row_mask:0x8 bank_mask:0xf\n"
"v_accvgpr_mov_b32 a169, a73\n"
"v_accvgpr_mov_b32 a170, a74\n"
"v_accvgpr_mov_b32 a171, a75\n"
"v_sub_f32 v189, s74, v189\n"
"v_sub_f32 v190, s74, v190\n"
"v_sub_f32 v191, s74, v191\n"
"v_sub_f32 v192, s74, v192\n"
"v_mfma_f32_16x16x32_bf16 v[156:159], v[216:219], a[24:27], v[184:187]\n"
"s_add_u32 s55, s55, s59\n"
"v_accvgpr_mov_b32 a191, a95\n"
"v_accvgpr_mov_b32 a163, a67\n"
"v_mfma_f32_16x16x32_bf16 v[156:159], v[220:223], a[28:31], v[156:159]\n"
"s_or_b32 s55, s55, s58\n"
"v_accvgpr_mov_b32 a149, a53\n"
"v_sub_f32 v188, s74, v188\n"
"v_mfma_f32_16x16x32_bf16 v[156:159], v[208:211], a[16:19], v[156:159]\n"
"s_xor_b32 s56, s55, s57\n"
"v_exp_f32 v153, v153\n"
"v_mfma_f32_16x16x32_bf16 v[156:159], v[212:215], a[20:23], v[156:159]\n"
"ds_write_b64 v250, v[252:253] offset:52224\n"
"v_accvgpr_mov_b32 a148, a52\n"
"v_accvgpr_mov_b32 a183, a87\n"
"v_accvgpr_mov_b32 a146, a50\n"
"v_sub_f32 v193, s74, v193\n"
"v_sub_f32 v194, s74, v194\n"
"v_exp_f32 v152, v152\n"
"v_mfma_f32_16x16x32_bf16 v[160:163], v[208:211], a[32:35], v[188:191]\n"
"v_accvgpr_mov_b32 a158, a62\n"
"v_accvgpr_mov_b32 a144, a48\n"
"v_mfma_f32_16x16x32_bf16 v[160:163], v[212:215], a[36:39], v[160:163]\n"
"v_rcp_f32 v206, v200\n"
"v_mfma_f32_16x16x32_bf16 v[160:163], v[216:219], a[40:43], v[160:163]\n"
"v_accvgpr_mov_b32 a190, a94\n"
"v_sub_f32 v195, s74, v195\n"
"v_mfma_f32_16x16x32_bf16 v[160:163], v[220:223], a[44:47], v[160:163]\n"
"ds_write_b128 v244, a[216:219] offset:32768\n"
"v_accvgpr_mov_b32 a150, a54\n"
"v_accvgpr_mov_b32 a156, a60\n"
"v_accvgpr_mov_b32 a147, a51\n"
"v_accvgpr_mov_b32 a177, a81\n"
"v_mul_f32_dpp v25, v246, v25 row_newbcast:1 row_mask:0xf bank_mask:0xf\n"
"v_mfma_f32_16x16x32_bf16 v[164:167], v[216:219], a[200:203], v[192:195]\n"
"v_mov_b32_dpp v206, v206 row_ror:12 row_mask:0x2 bank_mask:0xf\n"
"v_accvgpr_mov_b32 a167, a71\n"
"v_mfma_f32_16x16x32_bf16 v[164:167], v[220:223], a[204:207], v[164:167]\n"
"v_accvgpr_mov_b32 a179, a83\n"
"v_accvgpr_mov_b32 a187, a91\n"
"v_mfma_f32_16x16x32_bf16 v[164:167], v[208:211], a[192:195], v[164:167]\n"
"ds_read_b128 v[208:211], v228 offset:4096\n"
"v_accvgpr_mov_b32 a154, a58\n"
"v_mfma_f32_16x16x32_bf16 v[164:167], v[212:215], a[196:199], v[164:167]\n"
"s_waitcnt lgkmcnt(2)\n"
"ds_read_b128 v[216:219], v228 offset:4608\n"
"ds_read_b128 v[220:223], v229 offset:4608\n"
"v_mov_b32_dpp v206, v206 row_ror:8 row_mask:0x4 bank_mask:0xf\n"
"v_accvgpr_mov_b32 a182, a86\n"
"v_accvgpr_mov_b32 a152, a56\n"
"v_exp_f32 v155, v155\n"
"v_exp_f32 v160, v160\n"
"LTC_%=:\n"
"s_waitcnt lgkmcnt(4)\n"
"v_mfma_f32_16x16x32_bf16 v[92:95], v[116:119], v[224:227], v[92:95]\n"
"v_exp_f32 v154, v154\n"
"v_mfma_f32_16x16x32_bf16 v[92:95], v[112:115], a[252:255], v[92:95]\n"
"ds_read_b128 v[212:215], v229 offset:4096\n"
"v_mul_f32_dpp v24, v246, v24 row_newbcast:0 row_mask:0xf bank_mask:0xf\n"
"v_mfma_f32_16x16x32_bf16 v[60:63], v[108:111], v[224:227], v[60:63]\n"
"s_add_u32 m0, s44, 32768\n"
"v_exp_f32 v163, v163\n"
"v_mfma_f32_16x16x32_bf16 v[60:63], v[104:107], a[252:255], v[60:63]\n"
"buffer_load_dwordx4 v240, s[16:19], s56 offen nt lds\n"
"v_mul_f32_dpp v8, v246, v8 row_newbcast:0 row_mask:0xf bank_mask:0xf\n"
"v_exp_f32 v156, v156\n"
"v_exp_f32 v158, v158\n"
"v_exp_f32 v161, v161\n"
"v_exp_f32 v165, v165\n"
"v_mfma_f32_16x16x32_bf16 v[28:31], v[100:103], v[224:227], v[28:31]\n"
"v_cvt_pk_bf16_f32 v128, v152, v153\n"
"v_cvt_pk_bf16_f32 v129, v154, v155\n"
"v_mfma_f32_16x16x32_bf16 v[28:31], v[96:99], a[252:255], v[28:31]\n"
"v_mov_b32_dpp v206, v206 row_ror:4 row_mask:0x8 bank_mask:0xf\n"
"v_mul_f32_dpp v9, v246, v9 row_newbcast:1 row_mask:0xf bank_mask:0xf\n"
"v_mfma_f32_16x16x32_bf16 a[236:239], v[120:123], a[252:255], a[236:239]\n"
"s_waitcnt lgkmcnt(2)\n"
"v_exp_f32 v162, v162\n"
"v_mfma_f32_16x16x32_bf16 v[168:171], v[216:219], a[8:11], v[180:183]\n"
"s_waitcnt lgkmcnt(1)\n"
"v_exp_f32 v167, v167\n"
"v_mfma_f32_16x16x32_bf16 v[168:171], v[220:223], a[12:15], v[168:171]\n"
"v_mul_f32_dpp v12, v246, v12 row_newbcast:0 row_mask:0xf bank_mask:0xf\n"
"v_mul_f32_dpp v46, v247, v46 row_newbcast:2 row_mask:0xf bank_mask:0xf\n"
"v_mfma_f32_16x16x32_bf16 v[168:171], v[208:211], a[0:3], v[168:171]\n"
"s_waitcnt lgkmcnt(0)\n"
"ds_read_b64_tr_b16 a[244:245], v232 offset:49152\n"
"v_cvt_pk_bf16_f32 v145, v162, v163\n"
"v_mfma_f32_16x16x32_bf16 v[168:171], v[212:215], a[4:7], v[168:171]\n"
"v_exp_f32 v159, v159\n"
"v_mfma_f32_16x16x32_bf16 v[172:175], v[208:211], a[16:19], v[184:187]\n"
"ds_read_b64_tr_b16 a[252:253], v233 offset:49152\n"
"v_mul_f32_dpp v72, v206, v72 row_newbcast:0 row_mask:0xf bank_mask:0xf\n"
"v_mfma_f32_16x16x32_bf16 v[172:175], v[212:215], a[20:23], v[172:175]\n"
"v_exp_f32 v164, v164\n"
"v_mfma_f32_16x16x32_bf16 v[172:175], v[216:219], a[24:27], v[172:175]\n"
"ds_read_b64_tr_b16 a[250:251], v236 offset:53248\n"
"v_mul_f32_dpp v14, v246, v14 row_newbcast:2 row_mask:0xf bank_mask:0xf\n"
"v_mfma_f32_16x16x32_bf16 v[172:175], v[220:223], a[28:31], v[172:175]\n"
"ds_write_b128 v244, a[220:223] offset:33792\n"
"v_mul_f32_dpp v78, v206, v78 row_newbcast:2 row_mask:0xf bank_mask:0xf\n"
"v_exp_f32 v157, v157\n"
"v_mul_f32_dpp v89, v206, v89 row_newbcast:1 row_mask:0xf bank_mask:0xf\n"
"v_exp_f32 v168, v168\n"
"v_mfma_f32_16x16x32_bf16 v[176:179], v[216:219], a[40:43], v[188:191]\n"
"v_mul_f32_dpp v10, v246, v10 row_newbcast:2 row_mask:0xf bank_mask:0xf\n"
"v_mul_f32_dpp v40, v247, v40 row_newbcast:0 row_mask:0xf bank_mask:0xf\n"
"v_mfma_f32_16x16x32_bf16 v[176:179], v[220:223], a[44:47], v[176:179]\n"
"v_cvt_pk_bf16_f32 v137, v158, v159\n"
"v_mul_f32_dpp v28, v246, v28 row_newbcast:0 row_mask:0xf bank_mask:0xf\n"
"v_mfma_f32_16x16x32_bf16 v[176:179], v[208:211], a[32:35], v[176:179]\n"
"s_add_u32 s40, s56, s52\n"
"v_mul_f32_dpp v73, v206, v73 row_newbcast:1 row_mask:0xf bank_mask:0xf\n"
"v_cvt_pk_bf16_f32 v136, v156, v157\n"
"v_mfma_f32_16x16x32_bf16 v[176:179], v[212:215], a[36:39], v[176:179]\n"
"buffer_load_dwordx4 v207, s[16:19], s40 offen offset:1024 nt lds\n"
"v_mfma_f32_16x16x32_bf16 v[152:155], v[208:211], a[192:195], v[192:195]\n"
"v_mul_f32_dpp v76, v206, v76 row_newbcast:0 row_mask:0xf bank_mask:0xf\n"
"v_mul_f32_dpp v88, v206, v88 row_newbcast:0 row_mask:0xf bank_mask:0xf\n"
"v_mfma_f32_16x16x32_bf16 v[152:155], v[212:215], a[196:199], v[152:155]\n"
"ds_read_b128 v[208:211], v230 offset:0\n"
"v_cvt_pk_bf16_f32 v144, v160, v161\n"
"v_mfma_f32_16x16x32_bf16 v[152:155], v[216:219], a[200:203], v[152:155]\n"
"v_mul_f32_dpp v93, v206, v93 row_newbcast:1 row_mask:0xf bank_mask:0xf\n"
"v_cvt_pk_bf16_f32 v88, v88, v89\n"
"v_mfma_f32_16x16x32_bf16 v[152:155], v[220:223], a[204:207], v[152:155]\n"
"s_waitcnt lgkmcnt(1)\n"
"ds_read_b128 v[212:215], v231 offset:0\n"
"v_mul_f32_dpp v13, v246, v13 row_newbcast:1 row_mask:0xf bank_mask:0xf\n"
"v_mfma_f32_16x16x32_bf16 a[208:211], v[120:123], a[244:247], a[208:211]\n"
"v_exp_f32 v170, v170\n"
"v_mfma_f32_16x16x32_bf16 v[0:3], v[96:99], a[244:247], v[0:3]\n"
"ds_read_b128 v[216:219], v230 offset:512\n"
"v_cvt_pk_bf16_f32 v124, v164, v165\n"
"v_mfma_f32_16x16x32_bf16 v[0:3], v[100:103], a[248:251], v[0:3]\n"
"ds_read_b64_tr_b16 v[226:227], v237 offset:53248\n"
"v_mul_f32_dpp v15, v246, v15 row_newbcast:3 row_mask:0xf bank_mask:0xf\n"
"v_mul_f32_dpp v41, v247, v41 row_newbcast:1 row_mask:0xf bank_mask:0xf\n"
"v_mul_f32_dpp v42, v247, v42 row_newbcast:2 row_mask:0xf bank_mask:0xf\n"
"v_exp_f32 v166, v166\n"
"v_mul_f32_dpp v57, v247, v57 row_newbcast:1 row_mask:0xf bank_mask:0xf\n"
"v_mul_f32_dpp v91, v206, v91 row_newbcast:3 row_mask:0xf bank_mask:0xf\n"
"v_exp_f32 v172, v172\n"
"v_exp_f32 v177, v177\n"
"v_mfma_f32_16x16x32_bf16 v[32:35], v[104:107], a[244:247], v[32:35]\n"
"ds_read_b64_tr_b16 v[224:225], v237 offset:49152\n"
"ds_read_b128 v[220:223], v231 offset:512\n"
"v_mfma_f32_16x16x32_bf16 v[32:35], v[108:111], a[248:251], v[32:35]\n"
"ds_write_b128 v244, a[232:235] offset:34816\n"
"v_cvt_pk_bf16_f32 v8, v8, v9\n"
"v_mul_f32_dpp v75, v206, v75 row_newbcast:3 row_mask:0xf bank_mask:0xf\n"
"v_mul_f32_dpp v30, v246, v30 row_newbcast:2 row_mask:0xf bank_mask:0xf\n"
"v_mul_f32_dpp v94, v206, v94 row_newbcast:2 row_mask:0xf bank_mask:0xf\n"
"v_exp_f32 v176, v176\n"
"v_mfma_f32_16x16x32_bf16 v[64:67], v[112:115], a[244:247], v[64:67]\n"
"v_exp_f32 v173, v173\n"
"v_mfma_f32_16x16x32_bf16 v[64:67], v[116:119], a[248:251], v[64:67]\n"
"s_waitcnt lgkmcnt(6)\n"
"ds_write_b128 v244, a[236:239] offset:35840\n"
"v_mul_f32_dpp v77, v206, v77 row_newbcast:1 row_mask:0xf bank_mask:0xf\n"
"v_mul_f32_dpp v62, v247, v62 row_newbcast:2 row_mask:0xf bank_mask:0xf\n"
"v_exp_f32 v169, v169\n"
"v_cvt_pk_bf16_f32 v138, v172, v173\n"
"v_exp_f32 v153, v153\n"
"v_mfma_f32_16x16x32_bf16 v[156:159], v[208:211], a[0:3], v[180:183]\n"
"s_waitcnt lgkmcnt(6)\n"
"ds_read_b64_tr_b16 a[254:255], v233 offset:53248\n"
"v_mul_f32_dpp v63, v247, v63 row_newbcast:3 row_mask:0xf bank_mask:0xf\n"
"v_mfma_f32_16x16x32_bf16 v[156:159], v[212:215], a[4:7], v[156:159]\n"
"s_waitcnt lgkmcnt(6)\n"
"v_exp_f32 v174, v174\n"
"v_mfma_f32_16x16x32_bf16 v[156:159], v[216:219], a[8:11], v[156:159]\n"
"s_waitcnt lgkmcnt(3)\n"
"s_add_u32 s40, s56, s53\n"
"v_cvt_pk_bf16_f32 v40, v40, v41\n"
"v_mul_f32_dpp v0, v246, v0 row_newbcast:0 row_mask:0xf bank_mask:0xf\n"
"v_mfma_f32_16x16x32_bf16 v[156:159], v[220:223], a[12:15], v[156:159]\n"
"buffer_load_dwordx4 v240, s[16:19], s40 offen offset:2048 nt lds\n"
"v_cvt_pk_bf16_f32 v9, v10, v11\n"
"v_mul_f32_dpp v44, v247, v44 row_newbcast:0 row_mask:0xf bank_mask:0xf\n"
"v_cvt_pk_bf16_f32 v125, v166, v167\n"
"v_mul_f32_dpp v31, v246, v31 row_newbcast:3 row_mask:0xf bank_mask:0xf\n"
"v_exp_f32 v152, v152\n"
"v_mul_f32_dpp v34, v247, v34 row_newbcast:2 row_mask:0xf bank_mask:0xf\n"
"v_mov_b64 v[200:201], 0\n"
"v_mfma_f32_16x16x32_bf16 v[160:163], v[216:219], a[24:27], v[184:187]\n"
"v_mul_f32_dpp v60, v247, v60 row_newbcast:0 row_mask:0xf bank_mask:0xf\n"
"v_xor_b32 v244, 0x1000, v244\n"
"v_mfma_f32_16x16x32_bf16 v[160:163], v[220:223], a[28:31], v[160:163]\n"
"v_exp_f32 v178, v178\n"
"v_mfma_f32_16x16x32_bf16 v[160:163], v[208:211], a[16:19], v[160:163]\n"
"ds_read_b64_tr_b16 a[250:251], v236 offset:53760\n"
"v_mul_f32_dpp v33, v247, v33 row_newbcast:1 row_mask:0xf bank_mask:0xf\n"
"v_mfma_f32_16x16x32_bf16 v[160:163], v[212:215], a[20:23], v[160:163]\n"
"ds_read_b64_tr_b16 a[244:245], v232 offset:49664\n"
"v_mul_f32_dpp v43, v247, v43 row_newbcast:3 row_mask:0xf bank_mask:0xf\n"
"v_mfma_f32_16x16x32_bf16 v[164:167], v[208:211], a[32:35], v[188:191]\n"
"v_mul_f32_dpp v45, v247, v45 row_newbcast:1 row_mask:0xf bank_mask:0xf\n"
"v_cvt_pk_bf16_f32 v72, v72, v73\n"
"v_mfma_f32_16x16x32_bf16 v[164:167], v[212:215], a[36:39], v[164:167]\n"
"ds_read_b128 v[208:211], v230 offset:4096\n"
"v_mul_f32_dpp v74, v206, v74 row_newbcast:2 row_mask:0xf bank_mask:0xf\n"
"v_mfma_f32_16x16x32_bf16 v[164:167], v[216:219], a[40:43], v[164:167]\n"
"v_mul_f32_dpp v56, v247, v56 row_newbcast:0 row_mask:0xf bank_mask:0xf\n"
"v_mul_f32_dpp v58, v247, v58 row_newbcast:2 row_mask:0xf bank_mask:0xf\n"
"v_mfma_f32_16x16x32_bf16 v[164:167], v[220:223], a[44:47], v[164:167]\n"
"ds_read_b128 v[212:215], v231 offset:4096\n"
"ds_read_b128 v[220:223], v231 offset:4608\n"
"v_mul_f32_dpp v47, v247, v47 row_newbcast:3 row_mask:0xf bank_mask:0xf\n"
"v_cvt_pk_bf16_f32 v73, v74, v75\n"
"v_mul_f32_dpp v29, v246, v29 row_newbcast:1 row_mask:0xf bank_mask:0xf\n"
"v_exp_f32 v175, v175\n"
"v_exp_f32 v179, v179\n"
"v_mul_f32_dpp v1, v246, v1 row_newbcast:1 row_mask:0xf bank_mask:0xf\n"
"v_mul_f32_dpp v2, v246, v2 row_newbcast:2 row_mask:0xf bank_mask:0xf\n"
"v_mfma_f32_16x16x32_bf16 v[68:71], v[116:119], v[224:227], v[68:71]\n"
"s_waitcnt lgkmcnt(5)\n"
"v_exp_f32 v171, v171\n"
"v_mfma_f32_16x16x32_bf16 v[68:71], v[112:115], a[252:255], v[68:71]\n"
"ds_read_b128 v[216:219], v230 offset:4608\n"
"v_mul_f32_dpp v27, v246, v27 row_newbcast:3 row_mask:0xf bank_mask:0xf\n"
"v_mfma_f32_16x16x32_bf16 v[36:39], v[108:111], v[224:227], v[36:39]\n"
"s_add_u32 s40, s56, s54\n"
"v_cvt_pk_bf16_f32 v24, v24, v25\n"
"v_mul_f32_dpp v59, v247, v59 row_newbcast:3 row_mask:0xf bank_mask:0xf\n"
"v_mfma_f32_16x16x32_bf16 v[36:39], v[104:107], a[252:255], v[36:39]\n"
"buffer_load_dwordx4 v207, s[16:19], s40 offen offset:3072 nt lds\n"
"v_cvt_pk_bf16_f32 v74, v76, v77\n"
"v_cvt_pk_bf16_f32 v25, v26, v27\n"
"v_cvt_pk_bf16_f32 v130, v168, v169\n"
"v_exp_f32 v154, v154\n"
"v_exp_f32 v162, v162\n"
"v_mul_f32_dpp v3, v246, v3 row_newbcast:3 row_mask:0xf bank_mask:0xf\n"
"v_mfma_f32_16x16x32_bf16 v[4:7], v[100:103], v[224:227], v[4:7]\n"
"v_cvt_pk_bf16_f32 v10, v12, v13\n"
"v_cvt_pk_bf16_f32 v0, v0, v1\n"
"v_mfma_f32_16x16x32_bf16 v[4:7], v[96:99], a[252:255], v[4:7]\n"
"v_mul_f32_dpp v90, v206, v90 row_newbcast:2 row_mask:0xf bank_mask:0xf\n"
"v_mul_f32_dpp v92, v206, v92 row_newbcast:0 row_mask:0xf bank_mask:0xf\n"
"v_mfma_f32_16x16x32_bf16 a[212:215], v[120:123], a[252:255], a[212:215]\n"
"s_waitcnt lgkmcnt(3)\n"
"ds_read_b64_tr_b16 a[246:247], v232 offset:53760\n"
"ds_read_b64_tr_b16 v[224:225], v237 offset:49664\n"
"v_cvt_pk_bf16_f32 v11, v14, v15\n"
"v_cvt_pk_bf16_f32 v41, v42, v43\n"
"v_cvt_pk_bf16_f32 v43, v46, v47\n"
"v_cvt_pk_bf16_f32 v56, v56, v57\n"
"v_cvt_pk_bf16_f32 v131, v170, v171\n"
"v_cvt_pk_bf16_f32 v146, v176, v177\n"
"v_exp_f32 v167, v167\n"
"v_mfma_f32_16x16x32_bf16 v[168:171], v[208:211], a[0:3], v[180:183]\n"
"s_waitcnt lgkmcnt(3)\n"
"v_exp_f32 v159, v159\n"
"v_mfma_f32_16x16x32_bf16 v[168:171], v[212:215], a[4:7], v[168:171]\n"
"s_waitcnt lgkmcnt(2)\n"
"ds_read_b64_tr_b16 a[248:249], v236 offset:49664\n"
"ds_read_b64_tr_b16 a[254:255], v233 offset:53760\n"
"v_mfma_f32_16x16x32_bf16 v[168:171], v[216:219], a[8:11], v[168:171]\n"
"v_cvt_pk_bf16_f32 v89, v90, v91\n"
"v_cvt_pk_bf16_f32 v139, v174, v175\n"
"v_mfma_f32_16x16x32_bf16 v[168:171], v[220:223], a[12:15], v[168:171]\n"
"ds_write_b128 v248, v[8:11] offset:49152\n"
"v_mul_f32_dpp v79, v206, v79 row_newbcast:3 row_mask:0xf bank_mask:0xf\n"
"v_cvt_pk_bf16_f32 v147, v178, v179\n"
"v_exp_f32 v165, v165\n"
"v_mul_f32_dpp v6, v246, v6 row_newbcast:2 row_mask:0xf bank_mask:0xf\n"
"v_cvt_pk_bf16_f32 v1, v2, v3\n"
"v_mfma_f32_16x16x32_bf16 v[172:175], v[216:219], a[24:27], v[184:187]\n"
"ds_read_b64_tr_b16 v[226:227], v237 offset:53760\n"
"v_mul_f32_dpp v32, v247, v32 row_newbcast:0 row_mask:0xf bank_mask:0xf\n"
"v_mfma_f32_16x16x32_bf16 v[172:175], v[220:223], a[28:31], v[172:175]\n"
"v_mul_f32_dpp v39, v247, v39 row_newbcast:3 row_mask:0xf bank_mask:0xf\n"
"v_mul_f32_dpp v68, v206, v68 row_newbcast:0 row_mask:0xf bank_mask:0xf\n"
"v_mfma_f32_16x16x32_bf16 v[172:175], v[208:211], a[16:19], v[172:175]\n"
"ds_read_b64_tr_b16 a[252:253], v233 offset:49664\n"
"v_cvt_pk_bf16_f32 v75, v78, v79\n"
"v_mfma_f32_16x16x32_bf16 v[172:175], v[212:215], a[20:23], v[172:175]\n"
"s_cbranch_vccnz LPCT0_%=\nLPKT0_%=:\n"
"ds_read_b64_tr_b16 a[54:55], v249 offset:49160\n"
"v_mfma_f32_16x16x32_bf16 v[176:179], v[208:211], a[32:35], v[188:191]\n"
"v_cvt_pk_bf16_f32 v42, v44, v45\n"
"v_mul_f32_dpp v61, v247, v61 row_newbcast:1 row_mask:0xf bank_mask:0xf\n"
"v_mfma_f32_16x16x32_bf16 v[176:179], v[212:215], a[36:39], v[176:179]\n"
"ds_read_b64_tr_b16 a[52:53], v249 offset:49152\n"
"v_cvt_pk_bf16_f32 v57, v58, v59\n"
"v_mfma_f32_16x16x32_bf16 v[176:179], v[216:219], a[40:43], v[176:179]\n"
"v_exp_f32 v155, v155\n"
"v_mfma_f32_16x16x32_bf16 v[176:179], v[220:223], a[44:47], v[176:179]\n"
"s_waitcnt lgkmcnt(4)\n"
"ds_write_b128 v248, v[40:43] offset:50176\n"
"v_cvt_pk_bf16_f32 v27, v30, v31\n"
"v_cvt_pk_bf16_f32 v90, v92, v93\n"
"v_cvt_pk_bf16_f32 v126, v152, v153\n"
"v_mul_f32_dpp v7, v246, v7 row_newbcast:3 row_mask:0xf bank_mask:0xf\n"
"v_mul_f32_dpp v65, v206, v65 row_newbcast:1 row_mask:0xf bank_mask:0xf\n"
"v_mul_f32_dpp v67, v206, v67 row_newbcast:3 row_mask:0xf bank_mask:0xf\n"
"v_mul_f32_dpp v70, v206, v70 row_newbcast:2 row_mask:0xf bank_mask:0xf\n"
"v_mul_f32_dpp v71, v206, v71 row_newbcast:3 row_mask:0xf bank_mask:0xf\n"
"v_mfma_f32_16x16x32_bf16 a[224:227], v[120:123], a[244:247], a[224:227]\n"
"ds_write_b128 v248, v[72:75] offset:51200\n"
"v_cvt_pk_bf16_f32 v26, v28, v29\n"
"v_exp_f32 v166, v166\n"
"v_cvt_pk_bf16_f32 v3, v6, v7\n"
"v_mul_f32_dpp v66, v206, v66 row_newbcast:2 row_mask:0xf bank_mask:0xf\n"
"v_mfma_f32_16x16x32_bf16 v[16:19], v[96:99], a[244:247], v[16:19]\n"
"s_waitcnt vmcnt(8) lgkmcnt(4)\n"
"ds_read_b64_tr_b16 a[68:69], v249 offset:50176\n"
"v_mul_f32_dpp v69, v206, v69 row_newbcast:1 row_mask:0xf bank_mask:0xf\n"
"v_mfma_f32_16x16x32_bf16 v[16:19], v[100:103], a[248:251], v[16:19]\n"
"s_barrier\n"
"ds_read_b64_tr_b16 a[70:71], v249 offset:50184\n"
"ds_read_b128 v[212:215], v229 offset:16384\n"
"ds_read_b64 v[250:251], v245 offset:52224\n"
"v_mul_f32_dpp v5, v246, v5 row_newbcast:1 row_mask:0xf bank_mask:0xf\n"
"v_mul_f32_dpp v35, v247, v35 row_newbcast:3 row_mask:0xf bank_mask:0xf\n"
"v_exp_f32 v173, v173\n"
"v_exp_f32 v175, v175\n"
"v_mfma_f32_16x16x32_bf16 v[48:51], v[104:107], a[244:247], v[48:51]\n"
"v_cvt_pk_bf16_f32 v59, v62, v63\n"
"v_mul_f32_dpp v38, v247, v38 row_newbcast:2 row_mask:0xf bank_mask:0xf\n"
"v_mfma_f32_16x16x32_bf16 v[48:51], v[108:111], a[248:251], v[48:51]\n"
"ds_read_b128 v[208:211], v228 offset:16384\n"
"ds_read_b128 v[220:223], v229 offset:16896\n"
"v_mul_f32_dpp v95, v206, v95 row_newbcast:3 row_mask:0xf bank_mask:0xf\n"
"v_exp_f32 v161, v161\n"
"v_cvt_pk_bf16_f32 v149, v166, v167\n"
"v_cvt_pk_bf16_f32 v32, v32, v33\n"
"v_exp_f32 v170, v170\n"
"v_exp_f32 v172, v172\n"
"v_mfma_f32_16x16x32_bf16 v[80:83], v[112:115], a[244:247], v[80:83]\n"
"ds_read_b64_tr_b16 a[84:85], v249 offset:51200\n"
"ds_read_b64_tr_b16 a[86:87], v249 offset:51208\n"
"v_mfma_f32_16x16x32_bf16 v[80:83], v[116:119], a[248:251], v[80:83]\n"
"ds_read_b128 v[28:31], v244 offset:33792\n"
"v_mov_b64 v[198:199], 0\n"
"v_mfma_f32_16x16x32_bf16 v[84:87], v[116:119], v[224:227], v[84:87]\n"
"ds_write_b128 v248, v[24:27] offset:49152\n"
"v_cvt_pk_bf16_f32 v91, v94, v95\n"
"v_exp_f32 v156, v156\n"
"v_exp_f32 v157, v157\n"
"v_mul_f32_dpp v37, v247, v37 row_newbcast:1 row_mask:0xf bank_mask:0xf\n"
"v_mfma_f32_16x16x32_bf16 v[84:87], v[112:115], a[252:255], v[84:87]\n"
"ds_read_b64_tr_b16 a[60:61], v249 offset:49152\n"
"ds_read_b128 v[216:219], v228 offset:16896\n"
"v_cvt_pk_bf16_f32 v127, v154, v155\n"
"v_exp_f32 v160, v160\n"
"v_exp_f32 v164, v164\n"
"v_mul_f32_dpp v36, v247, v36 row_newbcast:0 row_mask:0xf bank_mask:0xf\n"
"v_exp_f32 v176, v176\n"
"v_mfma_f32_16x16x32_bf16 v[52:55], v[108:111], v[224:227], v[52:55]\n"
"ds_write_b128 v248, v[88:91] offset:51200\n"
"v_cvt_pk_bf16_f32 v58, v60, v61\n"
"v_exp_f32 v163, v163\n"
"v_cvt_pk_bf16_f32 v33, v34, v35\n"
"v_exp_f32 v169, v169\n"
"v_mfma_f32_16x16x32_bf16 v[52:55], v[104:107], a[252:255], v[52:55]\n"
"s_waitcnt lgkmcnt(9)\n"
"ds_write_b128 v248, v[56:59] offset:50176\n"
"v_mul_f32_dpp v4, v246, v4 row_newbcast:0 row_mask:0xf bank_mask:0xf\n"
"v_exp_f32 v168, v168\n"
"v_min_f32 v254, v252, v250\n"
"v_sub_f32 v252, v254, v252\n"
"v_mfma_f32_16x16x32_bf16 v[20:23], v[100:103], v[224:227], v[20:23]\n"
"ds_read_b64_tr_b16 a[92:93], v249 offset:51200\n"
"ds_read_b64_tr_b16 a[244:245], v234 offset:0\n"
"v_mfma_f32_16x16x32_bf16 v[20:23], v[96:99], a[252:255], v[20:23]\n"
"ds_read_b64_tr_b16 a[62:63], v249 offset:49160\n"
"ds_read_b64_tr_b16 a[78:79], v249 offset:50184\n"
"ds_read_b64_tr_b16 a[94:95], v249 offset:51208\n"
"v_cvt_pk_bf16_f32 v2, v4, v5\n"
"v_mul_f32_dpp v64, v206, v64 row_newbcast:0 row_mask:0xf bank_mask:0xf\n"
"v_cvt_pk_bf16_f32 v64, v64, v65\n"
"v_cvt_pk_bf16_f32 v65, v66, v67\n"
"v_cvt_pk_bf16_f32 v66, v68, v69\n"
"v_cvt_pk_bf16_f32 v67, v70, v71\n"
"v_exp_f32 v177, v177\n"
"v_mfma_f32_16x16x32_bf16 a[228:231], v[120:123], a[252:255], a[228:231]\n"
"s_waitcnt lgkmcnt(11)\n"
"ds_write_b128 v248, v[64:67] offset:51200\n"
"v_cvt_pk_bf16_f32 v132, v156, v157\n"
"v_cvt_pk_bf16_f32 v34, v36, v37\n"
"v_cvt_pk_bf16_f32 v35, v38, v39\n"
"v_exp_f32 v171, v171\n"
"v_exp_f32 v179, v179\n"
"v_sub_f32 v250, v254, v250\n"
"v_mul_f32_dpp v83, v206, v83 row_newbcast:3 row_mask:0xf bank_mask:0xf\n"
"v_mfma_f32_16x16x32_bf16 v[152:155], v[208:211], a[0:3], v[180:183]\n"
"ds_read_b64_tr_b16 a[76:77], v249 offset:50176\n"
"ds_read_b64_tr_b16 a[80:81], v249 offset:51200\n"
"v_mfma_f32_16x16x32_bf16 v[152:155], v[212:215], a[4:7], v[152:155]\n"
"s_waitcnt lgkmcnt(10)\n"
"ds_write_b128 v248, v[0:3] offset:49152\n"
"v_exp_f32 v158, v158\n"
"v_cvt_pk_bf16_f32 v140, v160, v161\n"
"v_cvt_pk_bf16_f32 v142, v172, v173\n"
"v_mul_f32_dpp v18, v246, v18 row_newbcast:2 row_mask:0xf bank_mask:0xf\n"
"v_mfma_f32_16x16x32_bf16 v[152:155], v[216:219], a[8:11], v[152:155]\n"
"ds_write_b128 v248, v[32:35] offset:50176\n"
"v_cvt_pk_bf16_f32 v141, v162, v163\n"
"v_exp_f32 v178, v178\n"
"v_exp_f32 v250, v250\n"
"v_mfma_f32_16x16x32_bf16 v[152:155], v[220:223], a[12:15], v[152:155]\n"
"ds_read_b64_tr_b16 a[50:51], v249 offset:49160\n"
"ds_read_b64_tr_b16 a[64:65], v249 offset:50176\n"
"ds_read_b128 v[24:27], v244 offset:32768\n"
"v_cvt_pk_bf16_f32 v133, v158, v159\n"
"v_cvt_pk_bf16_f32 v148, v164, v165\n"
"v_cvt_pk_bf16_f32 v150, v176, v177\n"
"v_exp_f32 v252, v252\n"
"v_mul_f32_dpp v19, v246, v19 row_newbcast:3 row_mask:0xf bank_mask:0xf\n"
"v_mul_f32_dpp v22, v246, v22 row_newbcast:2 row_mask:0xf bank_mask:0xf\n"
"v_mfma_f32_16x16x32_bf16 v[156:159], v[216:219], a[24:27], v[184:187]\n"
"ds_read_b64_tr_b16 a[66:67], v249 offset:50184\n"
"ds_read_b64_tr_b16 a[248:249], v238 offset:0\n"
"v_mfma_f32_16x16x32_bf16 v[156:159], v[220:223], a[28:31], v[156:159]\n"
"ds_read_b64_tr_b16 a[48:49], v249 offset:49152\n"
"ds_read_b128 v[60:63], v244 offset:35840\n"
"v_mfma_f32_16x16x32_bf16 v[156:159], v[208:211], a[16:19], v[156:159]\n"
"ds_read_b64_tr_b16 a[82:83], v249 offset:51208\n"
"ds_read_b128 v[56:59], v244 offset:34816\n"
"v_mfma_f32_16x16x32_bf16 v[156:159], v[212:215], a[20:23], v[156:159]\n"
"ds_read_b64_tr_b16 a[246:247], v234 offset:4096\n"
"ds_read_b64_tr_b16 v[226:227], v239 offset:4096\n"
"v_mfma_f32_16x16x32_bf16 v[160:163], v[208:211], a[32:35], v[188:191]\n"
"v_exp_f32 v174, v174\n"
"v_mfma_f32_16x16x32_bf16 v[160:163], v[212:215], a[36:39], v[160:163]\n"
"ds_read_b64_tr_b16 a[250:251], v238 offset:4096\n"
"v_mul_f32_dpp v49, v247, v49 row_newbcast:1 row_mask:0xf bank_mask:0xf\n"
"v_mfma_f32_16x16x32_bf16 v[160:163], v[216:219], a[40:43], v[160:163]\n"
"ds_read_b64_tr_b16 v[224:225], v239 offset:0\n"
"v_mov_b64 v[202:203], 0\n"
"v_mfma_f32_16x16x32_bf16 v[160:163], v[220:223], a[44:47], v[160:163]\n"
"v_exp_f32 v154, v154\n"
"v_mfma_f32_16x16x32_bf16 v[164:167], v[216:219], a[200:203], v[192:195]\n"
"v_cvt_pk_bf16_f32 v151, v178, v179\n"
"v_mul_f32 v253, v253, v252\n"
"v_mfma_f32_16x16x32_bf16 v[164:167], v[220:223], a[204:207], v[164:167]\n"
"v_cvt_pk_bf16_f32 v134, v168, v169\n"
"v_fmac_f32 v253, v251, v250\n"
"v_mfma_f32_16x16x32_bf16 v[164:167], v[208:211], a[192:195], v[164:167]\n"
"ds_read_b64_tr_b16 a[252:253], v235 offset:0\n"
"ds_read_b64_tr_b16 a[254:255], v235 offset:4096\n"
"v_mfma_f32_16x16x32_bf16 v[164:167], v[212:215], a[196:199], v[164:167]\n"
"s_waitcnt lgkmcnt(4)\n"
"v_cvt_pk_bf16_f32 v135, v170, v171\n"
"v_mfma_f32_16x16x32_bf16 a[216:219], v[124:127], a[244:247], 0\n"
"ds_read_b128 v[216:219], v228 offset:20992\n"
"v_cvt_pk_bf16_f32 v143, v174, v175\n"
"v_mfma_f32_16x16x32_bf16 v[8:11], v[128:131], a[244:247], 0\n"
"s_waitcnt lgkmcnt(4)\n"
"s_add_u32 m0, s44, 98304\n"
"v_rcp_f32 v253, v253\n"
"v_mfma_f32_16x16x32_bf16 v[8:11], v[132:135], a[248:251], v[8:11]\n"
"buffer_load_dwordx4 v240, s[20:23], s56 offen nt lds\n"
"v_exp_f32 v152, v152\n"
"v_exp_f32 v155, v155\n"
"v_exp_f32 v158, v158\n"
"v_exp_f32 v167, v167\n"
"v_mfma_f32_16x16x32_bf16 v[40:43], v[136:139], a[244:247], 0\n"
"v_accvgpr_read_b32 v254, a208\n"
"v_mul_f32_dpp v85, v206, v85 row_newbcast:1 row_mask:0xf bank_mask:0xf\n"
"v_mfma_f32_16x16x32_bf16 v[40:43], v[140:143], a[248:251], v[40:43]\n"
"v_mul_f32 v250, v250, v253\n"
"v_accvgpr_read_b32 v251, a211\n"
"v_mfma_f32_16x16x32_bf16 v[72:75], v[144:147], a[244:247], 0\n"
"ds_read_b128 v[220:223], v229 offset:20992\n"
"v_mul_f32_dpp v17, v246, v17 row_newbcast:1 row_mask:0xf bank_mask:0xf\n"
"v_mfma_f32_16x16x32_bf16 v[72:75], v[148:151], a[248:251], v[72:75]\n"
"s_waitcnt lgkmcnt(2)\n"
"v_exp_f32 v164, v164\n"
"v_mfma_f32_16x16x32_bf16 v[76:79], v[148:151], v[224:227], 0\n"
"v_exp_f32 v153, v153\n"
"v_mfma_f32_16x16x32_bf16 v[76:79], v[144:147], a[252:255], v[76:79]\n"
"v_mul_f32 v252, v252, v253\n"
"v_mov_b32_dpp v250, v250 row_ror:12 row_mask:0x2 bank_mask:0xf\n"
"v_mfma_f32_16x16x32_bf16 v[44:47], v[140:143], v[224:227], 0\n"
"ds_read_b64_tr_b16 a[244:245], v234 offset:512\n"
"v_mov_b32_dpp v252, v252 row_ror:12 row_mask:0x2 bank_mask:0xf\n"
"v_mfma_f32_16x16x32_bf16 v[44:47], v[136:139], a[252:255], v[44:47]\n"
"ds_read_b128 v[212:215], v229 offset:20480\n"
"v_mov_b32_dpp v250, v250 row_ror:8 row_mask:0x4 bank_mask:0xf\n"
"v_mfma_f32_16x16x32_bf16 v[12:15], v[132:135], v[224:227], 0\n"
"s_add_u32 s40, s56, s52\n"
"ds_read_b128 v[208:211], v228 offset:20480\n"
"v_cvt_pk_bf16_f32 v96, v152, v153\n"
"v_mfma_f32_16x16x32_bf16 v[12:15], v[128:131], a[252:255], v[12:15]\n"
"buffer_load_dwordx4 v207, s[20:23], s40 offen offset:1024 nt lds\n"
"v_mov_b32_dpp v252, v252 row_ror:8 row_mask:0x4 bank_mask:0xf\n"
"v_mov_b32_dpp v250, v250 row_ror:4 row_mask:0x8 bank_mask:0xf\n"
"v_accvgpr_read_b32 v255, a209\n"
"v_accvgpr_read_b32 v253, a210\n"
"v_mov_b64 v[196:197], 0\n"
"v_cvt_pk_bf16_f32 v97, v154, v155\n"
"v_exp_f32 v157, v157\n"
"v_mfma_f32_16x16x32_bf16 a[220:223], v[124:127], a[252:255], 0\n"
"v_mov_b32_dpp v252, v252 row_ror:4 row_mask:0x8 bank_mask:0xf\n"
"v_mul_f32_dpp v24, v250, v24 row_newbcast:0 row_mask:0xf bank_mask:0xf\n"
"v_mul_f32_dpp v25, v250, v25 row_newbcast:1 row_mask:0xf bank_mask:0xf\n"
"v_mul_f32_dpp v26, v250, v26 row_newbcast:2 row_mask:0xf bank_mask:0xf\n"
"v_mul_f32_dpp v29, v250, v29 row_newbcast:1 row_mask:0xf bank_mask:0xf\n"
"v_mul_f32_dpp v54, v247, v54 row_newbcast:2 row_mask:0xf bank_mask:0xf\n"
"v_exp_f32 v162, v162\n"
"v_exp_f32 v163, v163\n"
"v_smfmac_f32_16x16x64_bf16 v[196:199], a[240:243], v[128:135], v241\n"
"v_mul_f32_dpp v27, v250, v27 row_newbcast:3 row_mask:0xf bank_mask:0xf\n"
"v_fmac_f32_dpp v24, v252, v254 row_newbcast:0 row_mask:0xf bank_mask:0xf\n"
"v_mfma_f32_16x16x32_bf16 v[202:205], a[240:243], v[124:127], v[202:205]\n"
"s_waitcnt lgkmcnt(3)\n"
"ds_read_b64_tr_b16 a[252:253], v235 offset:512\n"
"ds_read_b64_tr_b16 a[254:255], v235 offset:4608\n"
"ds_read_b64_tr_b16 v[224:225], v239 offset:512\n"
"v_fmac_f32_dpp v25, v252, v255 row_newbcast:1 row_mask:0xf bank_mask:0xf\n"
"v_fmac_f32_dpp v26, v252, v253 row_newbcast:2 row_mask:0xf bank_mask:0xf\n"
"v_mul_f32_dpp v28, v250, v28 row_newbcast:0 row_mask:0xf bank_mask:0xf\n"
"v_mul_f32_dpp v30, v250, v30 row_newbcast:2 row_mask:0xf bank_mask:0xf\n"
"v_mul_f32_dpp v50, v247, v50 row_newbcast:2 row_mask:0xf bank_mask:0xf\n"
"v_mul_f32_dpp v58, v250, v58 row_newbcast:2 row_mask:0xf bank_mask:0xf\n"
"v_exp_f32 v156, v156\n"
"v_mfma_f32_16x16x32_bf16 v[168:171], v[216:219], a[8:11], v[180:183]\n"
"v_accvgpr_read_b32 v254, a212\n"
"v_accvgpr_read_b32 v253, a214\n"
"v_mfma_f32_16x16x32_bf16 v[168:171], v[220:223], a[12:15], v[168:171]\n"
"s_waitcnt lgkmcnt(3)\n"
"v_mul_f32_dpp v48, v247, v48 row_newbcast:0 row_mask:0xf bank_mask:0xf\n"
"v_mul_f32_dpp v57, v250, v57 row_newbcast:1 row_mask:0xf bank_mask:0xf\n"
"v_mfma_f32_16x16x32_bf16 v[168:171], v[208:211], a[0:3], v[168:171]\n"
"ds_read_b64_tr_b16 a[250:251], v238 offset:4608\n"
"v_mul_f32_dpp v56, v250, v56 row_newbcast:0 row_mask:0xf bank_mask:0xf\n"
"v_mfma_f32_16x16x32_bf16 v[168:171], v[212:215], a[4:7], v[168:171]\n"
"ds_read_b64_tr_b16 a[248:249], v238 offset:512\n"
"ds_read_b64_tr_b16 v[226:227], v239 offset:4608\n"
"v_fmac_f32_dpp v27, v252, v251 row_newbcast:3 row_mask:0xf bank_mask:0xf\n"
"v_mul_f32_dpp v31, v250, v31 row_newbcast:3 row_mask:0xf bank_mask:0xf\n"
"v_fmac_f32_dpp v28, v252, v254 row_newbcast:0 row_mask:0xf bank_mask:0xf\n"
"v_fmac_f32_dpp v30, v252, v253 row_newbcast:2 row_mask:0xf bank_mask:0xf\n"
"v_cvt_pk_bf16_f32 v24, v24, v25\n"
"v_cvt_pk_bf16_f32 v25, v26, v27\n"
"v_mul_f32_dpp v52, v247, v52 row_newbcast:0 row_mask:0xf bank_mask:0xf\n"
"v_accvgpr_read_b32 v254, a224\n"
"v_mul_f32_dpp v59, v250, v59 row_newbcast:3 row_mask:0xf bank_mask:0xf\n"
"v_mul_f32_dpp v60, v250, v60 row_newbcast:0 row_mask:0xf bank_mask:0xf\n"
"v_mul_f32_dpp v62, v250, v62 row_newbcast:2 row_mask:0xf bank_mask:0xf\n"
"v_mul_f32_dpp v63, v250, v63 row_newbcast:3 row_mask:0xf bank_mask:0xf\n"
"v_exp_f32 v159, v159\n"
"v_exp_f32 v161, v161\n"
"v_mfma_f32_16x16x32_bf16 v[172:175], v[208:211], a[16:19], v[184:187]\n"
"v_accvgpr_read_b32 v255, a213\n"
"v_accvgpr_read_b32 v251, a215\n"
"v_mfma_f32_16x16x32_bf16 v[172:175], v[212:215], a[20:23], v[172:175]\n"
"ds_read_b64_tr_b16 a[246:247], v234 offset:4608\n"
"v_fmac_f32_dpp v29, v252, v255 row_newbcast:1 row_mask:0xf bank_mask:0xf\n"
"v_mfma_f32_16x16x32_bf16 v[172:175], v[216:219], a[24:27], v[172:175]\n"
"s_add_u32 s40, s56, s53\n"
"v_xor_b32 v249, s75, v249\n"
"v_accvgpr_read_b32 v255, a225\n"
"v_mfma_f32_16x16x32_bf16 v[172:175], v[220:223], a[28:31], v[172:175]\n"
"buffer_load_dwordx4 v240, s[20:23], s40 offen offset:2048 nt lds\n"
"v_fmac_f32_dpp v31, v252, v251 row_newbcast:3 row_mask:0xf bank_mask:0xf\n"
"v_cvt_pk_bf16_f32 v27, v30, v31\n"
"v_mul_f32_dpp v16, v246, v16 row_newbcast:0 row_mask:0xf bank_mask:0xf\n"
"v_mul_f32_dpp v51, v247, v51 row_newbcast:3 row_mask:0xf bank_mask:0xf\n"
"v_accvgpr_read_b32 v253, a226\n"
"v_accvgpr_read_b32 v251, a227\n"
"v_fmac_f32_dpp v56, v252, v254 row_newbcast:0 row_mask:0xf bank_mask:0xf\n"
"v_accvgpr_read_b32 v254, a228\n"
"v_mfma_f32_16x16x32_bf16 v[176:179], v[216:219], a[40:43], v[188:191]\n"
"v_cvt_pk_bf16_f32 v26, v28, v29\n"
"v_fmac_f32_dpp v57, v252, v255 row_newbcast:1 row_mask:0xf bank_mask:0xf\n"
"v_mfma_f32_16x16x32_bf16 v[176:179], v[220:223], a[44:47], v[176:179]\n"
"v_fmac_f32_dpp v58, v252, v253 row_newbcast:2 row_mask:0xf bank_mask:0xf\n"
"v_accvgpr_read_b32 v255, a229\n"
"v_mfma_f32_16x16x32_bf16 v[176:179], v[208:211], a[32:35], v[176:179]\n"
"v_cvt_pk_bf16_f32 v16, v16, v17\n"
"v_mul_f32_dpp v61, v250, v61 row_newbcast:1 row_mask:0xf bank_mask:0xf\n"
"v_mfma_f32_16x16x32_bf16 v[176:179], v[212:215], a[36:39], v[176:179]\n"
"ds_write_b128 v243, v[24:27] offset:0\n"
"v_cvt_pk_bf16_f32 v48, v48, v49\n"
"v_fmac_f32_dpp v59, v252, v251 row_newbcast:3 row_mask:0xf bank_mask:0xf\n"
"v_accvgpr_read_b32 v253, a230\n"
"v_accvgpr_read_b32 v251, a231\n"
"v_fmac_f32_dpp v60, v252, v254 row_newbcast:0 row_mask:0xf bank_mask:0xf\n"
"v_fmac_f32_dpp v61, v252, v255 row_newbcast:1 row_mask:0xf bank_mask:0xf\n"
"v_cvt_pk_bf16_f32 v56, v56, v57\n"
"v_cvt_pk_bf16_f32 v57, v58, v59\n"
"v_cvt_pk_bf16_f32 v58, v60, v61\n"
"v_mfma_f32_16x16x32_bf16 v[152:155], v[208:211], a[192:195], v[192:195]\n"
"v_mul_f32_dpp v81, v206, v81 row_newbcast:1 row_mask:0xf bank_mask:0xf\n"
"v_mul_f32_dpp v82, v206, v82 row_newbcast:2 row_mask:0xf bank_mask:0xf\n"
"v_mul_f32_dpp v87, v206, v87 row_newbcast:3 row_mask:0xf bank_mask:0xf\n"
"v_fmac_f32_dpp v62, v252, v253 row_newbcast:2 row_mask:0xf bank_mask:0xf\n"
"v_fmac_f32_dpp v63, v252, v251 row_newbcast:3 row_mask:0xf bank_mask:0xf\n"
"v_cvt_pk_bf16_f32 v59, v62, v63\n"
"ds_write_b128 v243, v[56:59] offset:2048\n"
"v_mfma_f32_16x16x32_bf16 v[152:155], v[212:215], a[196:199], v[152:155]\n"
"v_exp_f32 v174, v174\n"
"v_mfma_f32_16x16x32_bf16 v[152:155], v[216:219], a[200:203], v[152:155]\n"
"v_exp_f32 v166, v166\n"
"v_mfma_f32_16x16x32_bf16 v[152:155], v[220:223], a[204:207], v[152:155]\n"
"s_waitcnt lgkmcnt(2)\n"
"ds_read_b128 v[212:215], v231 offset:16384\n"
"v_cvt_pk_bf16_f32 v105, v158, v159\n"
"v_mfma_f32_16x16x32_bf16 a[232:235], v[124:127], a[244:247], 0\n"
"v_mul_f32_dpp v20, v246, v20 row_newbcast:0 row_mask:0xf bank_mask:0xf\n"
"v_mul_f32_dpp v23, v246, v23 row_newbcast:3 row_mask:0xf bank_mask:0xf\n"
"v_mfma_f32_16x16x32_bf16 v[24:27], v[128:131], a[244:247], 0\n"
"s_add_u32 s40, s56, s54\n"
"ds_read_b128 v[220:223], v231 offset:16896\n"
"v_mul_f32_dpp v21, v246, v21 row_newbcast:1 row_mask:0xf bank_mask:0xf\n"
"v_mfma_f32_16x16x32_bf16 v[24:27], v[132:135], a[248:251], v[24:27]\n"
"buffer_load_dwordx4 v207, s[20:23], s40 offen offset:3072 nt lds\n"
"v_mfma_f32_16x16x32_bf16 v[56:59], v[136:139], a[244:247], 0\n"
"s_add_u32 s55, s55, s59\n"
"v_cvt_pk_bf16_f32 v17, v18, v19\n"
"v_cvt_pk_bf16_f32 v104, v156, v157\n"
"v_mfma_f32_16x16x32_bf16 v[56:59], v[140:143], a[248:251], v[56:59]\n"
"s_or_b32 s55, s55, s58\n"
"v_exp_f32 v178, v178\n"
"v_mfma_f32_16x16x32_bf16 v[88:91], v[144:147], a[244:247], 0\n"
"s_xor_b32 s56, s55, s57\n"
"ds_read_b128 v[208:211], v230 offset:16384\n"
"v_cvt_pk_bf16_f32 v49, v50, v51\n"
"v_mfma_f32_16x16x32_bf16 v[88:91], v[148:151], a[248:251], v[88:91]\n"
"ds_read_b128 v[216:219], v230 offset:16896\n"
"v_cvt_pk_bf16_f32 v18, v20, v21\n"
"v_mfma_f32_16x16x32_bf16 v[92:95], v[148:151], v[224:227], 0\n"
"v_exp_f32 v160, v160\n"
"v_mfma_f32_16x16x32_bf16 v[92:95], v[144:147], a[252:255], v[92:95]\n"
"v_exp_f32 v165, v165\n"
"v_mfma_f32_16x16x32_bf16 v[60:63], v[140:143], v[224:227], 0\n"
"s_add_u32 m0, s44, 49152\n"
"ds_read_b64_tr_b16 a[248:249], v236 offset:0\n"
"v_mul_f32_dpp v80, v206, v80 row_newbcast:0 row_mask:0xf bank_mask:0xf\n"
"v_mfma_f32_16x16x32_bf16 v[60:63], v[136:139], a[252:255], v[60:63]\n"
"buffer_load_dwordx4 v240, s[16:19], s56 offen nt lds\n"
"v_mfma_f32_16x16x32_bf16 v[28:31], v[132:135], v[224:227], 0\n"
"v_exp_f32 v177, v177\n"
"v_mfma_f32_16x16x32_bf16 v[28:31], v[128:131], a[252:255], v[28:31]\n"
"v_exp_f32 v169, v169\n"
"v_mfma_f32_16x16x32_bf16 a[236:239], v[124:127], a[252:255], 0\n"
"ds_read_b64_tr_b16 a[250:251], v236 offset:4096\n"
"ds_read_b64_tr_b16 v[224:225], v237 offset:0\n"
"v_smfmac_f32_16x16x64_bf16 v[198:201], a[240:243], v[136:143], v241\n"
"s_waitcnt lgkmcnt(3)\n"
"ds_read_b64_tr_b16 a[246:247], v232 offset:4096\n"
"v_cvt_pk_bf16_f32 v112, v160, v161\n"
"v_mfma_f32_16x16x32_bf16 v[156:159], v[208:211], a[0:3], v[180:183]\n"
"v_cvt_pk_bf16_f32 v80, v80, v81\n"
"v_cvt_pk_bf16_f32 v113, v162, v163\n"
"v_mfma_f32_16x16x32_bf16 v[156:159], v[212:215], a[4:7], v[156:159]\n"
"ds_read_b64_tr_b16 a[254:255], v233 offset:4096\n"
"v_xor_b32 v248, s75, v248\n"
"v_mfma_f32_16x16x32_bf16 v[156:159], v[216:219], a[8:11], v[156:159]\n"
"ds_read_b64_tr_b16 a[244:245], v232 offset:0\n"
"v_cvt_pk_bf16_f32 v120, v164, v165\n"
"v_mfma_f32_16x16x32_bf16 v[156:159], v[220:223], a[12:15], v[156:159]\n"
"v_exp_f32 v170, v170\n"
"v_mfma_f32_16x16x32_bf16 v[160:163], v[216:219], a[24:27], v[184:187]\n"
"v_exp_f32 v179, v179\n"
"v_mfma_f32_16x16x32_bf16 v[160:163], v[220:223], a[28:31], v[160:163]\n"
"ds_read_b64_tr_b16 v[226:227], v237 offset:4096\n"
"v_cvt_pk_bf16_f32 v121, v166, v167\n"
"v_mfma_f32_16x16x32_bf16 v[160:163], v[208:211], a[16:19], v[160:163]\n"
"s_add_u32 s40, s56, s52\n"
"v_exp_f32 v168, v168\n"
"v_mfma_f32_16x16x32_bf16 v[160:163], v[212:215], a[20:23], v[160:163]\n"
"buffer_load_dwordx4 v207, s[16:19], s40 offen offset:1024 nt lds\n"
"v_mfma_f32_16x16x32_bf16 v[164:167], v[208:211], a[32:35], v[188:191]\n"
"v_exp_f32 v176, v176\n"
"v_mfma_f32_16x16x32_bf16 v[164:167], v[212:215], a[36:39], v[164:167]\n"
"v_cvt_pk_bf16_f32 v19, v22, v23\n"
"v_mul_f32_dpp v53, v247, v53 row_newbcast:1 row_mask:0xf bank_mask:0xf\n"
"v_mfma_f32_16x16x32_bf16 v[164:167], v[216:219], a[40:43], v[164:167]\n"
"ds_read_b64_tr_b16 a[252:253], v233 offset:0\n"
"ds_read_b128 v[208:211], v230 offset:20480\n"
"v_mfma_f32_16x16x32_bf16 v[164:167], v[220:223], a[44:47], v[164:167]\n"
"s_waitcnt lgkmcnt(3)\n"
"v_exp_f32 v158, v158\n"
"v_mfma_f32_16x16x32_bf16 a[208:211], v[124:127], a[244:247], 0\n"
"ds_read_b128 v[220:223], v231 offset:20992\n"
"v_cvt_pk_bf16_f32 v81, v82, v83\n"
"v_mfma_f32_16x16x32_bf16 v[0:3], v[128:131], a[244:247], 0\n"
"ds_read_b128 v[212:215], v231 offset:20480\n"
"v_mul_f32_dpp v55, v247, v55 row_newbcast:3 row_mask:0xf bank_mask:0xf\n"
"v_mfma_f32_16x16x32_bf16 v[0:3], v[132:135], a[248:251], v[0:3]\n"
"v_exp_f32 v175, v175\n"
"v_mfma_f32_16x16x32_bf16 v[32:35], v[136:139], a[244:247], 0\n"
"s_add_u32 s40, s56, s53\n"
"ds_read_b128 v[216:219], v230 offset:20992\n"
"v_mul_f32_dpp v84, v206, v84 row_newbcast:0 row_mask:0xf bank_mask:0xf\n"
"v_mfma_f32_16x16x32_bf16 v[32:35], v[140:143], a[248:251], v[32:35]\n"
"buffer_load_dwordx4 v240, s[16:19], s40 offen offset:2048 nt lds\n"
"v_mfma_f32_16x16x32_bf16 v[64:67], v[144:147], a[244:247], 0\n"
"v_cvt_pk_bf16_f32 v50, v52, v53\n"
"v_mul_f32_dpp v86, v206, v86 row_newbcast:2 row_mask:0xf bank_mask:0xf\n"
"v_mfma_f32_16x16x32_bf16 v[64:67], v[148:151], a[248:251], v[64:67]\n"
"s_waitcnt lgkmcnt(3)\n"
"ds_write_b128 v248, v[16:19] offset:49152\n"
"v_cvt_pk_bf16_f32 v82, v84, v85\n"
"v_exp_f32 v172, v172\n"
"v_exp_f32 v152, v152\n"
"v_exp_f32 v159, v159\n"
"v_exp_f32 v161, v161\n"
"v_exp_f32 v164, v164\n"
"v_exp_f32 v167, v167\n"
"v_mfma_f32_16x16x32_bf16 v[68:71], v[148:151], v[224:227], 0\n"
"v_cvt_pk_bf16_f32 v51, v54, v55\n"
"v_cvt_pk_bf16_f32 v98, v168, v169\n"
"v_mfma_f32_16x16x32_bf16 v[68:71], v[144:147], a[252:255], v[68:71]\n"
"ds_write_b128 v248, v[48:51] offset:50176\n"
"v_exp_f32 v171, v171\n"
"v_exp_f32 v173, v173\n"
"v_exp_f32 v156, v156\n"
"v_exp_f32 v163, v163\n"
"v_mfma_f32_16x16x32_bf16 v[36:39], v[140:143], v[224:227], 0\n"
"ds_read_b64_tr_b16 a[246:247], v232 offset:4608\n"
"ds_read_b64_tr_b16 a[250:251], v236 offset:4608\n"
"v_mfma_f32_16x16x32_bf16 v[36:39], v[136:139], a[252:255], v[36:39]\n"
"ds_read_b64_tr_b16 a[74:75], v249 offset:50184\n"
"v_cvt_pk_bf16_f32 v107, v174, v175\n"
"v_mfma_f32_16x16x32_bf16 v[4:7], v[132:135], v[224:227], 0\n"
"ds_read_b64_tr_b16 a[56:57], v249 offset:49152\n"
"v_cvt_pk_bf16_f32 v83, v86, v87\n"
"v_mfma_f32_16x16x32_bf16 v[4:7], v[128:131], a[252:255], v[4:7]\n"
"ds_write_b128 v248, v[80:83] offset:51200\n"
"v_cvt_pk_bf16_f32 v99, v170, v171\n"
"v_exp_f32 v155, v155\n"
"v_exp_f32 v157, v157\n"
"v_mfma_f32_16x16x32_bf16 a[212:215], v[124:127], a[252:255], 0\n"
"s_add_u32 s40, s56, s54\n"
"ds_read_b64_tr_b16 a[58:59], v249 offset:49160\n"
"ds_read_b64_tr_b16 v[224:225], v237 offset:512\n"
"v_smfmac_f32_16x16x64_bf16 v[200:203], a[240:243], v[144:151], v241\n"
"buffer_load_dwordx4 v207, s[16:19], s40 offen offset:3072 nt lds\n"
"v_mfma_f32_16x16x32_bf16 v[168:171], v[208:211], a[0:3], v[180:183]\n"
"s_waitcnt lgkmcnt(8)\n"
"v_exp_f32 v153, v153\n"
"v_mfma_f32_16x16x32_bf16 v[168:171], v[212:215], a[4:7], v[168:171]\n"
"ds_read_b64_tr_b16 a[72:73], v249 offset:50176\n"
"ds_read_b64_tr_b16 a[90:91], v249 offset:51208\n"
"v_mfma_f32_16x16x32_bf16 v[168:171], v[216:219], a[8:11], v[168:171]\n"
"ds_read_b64_tr_b16 a[88:89], v249 offset:51200\n"
"v_cvt_pk_bf16_f32 v115, v178, v179\n"
"v_mfma_f32_16x16x32_bf16 v[168:171], v[220:223], a[12:15], v[168:171]\n"
"ds_read_b64_tr_b16 a[244:245], v232 offset:512\n"
"v_cvt_pk_bf16_f32 v106, v172, v173\n"
"v_mfma_f32_16x16x32_bf16 v[172:175], v[216:219], a[24:27], v[184:187]\n"
"ds_read_b64_tr_b16 a[248:249], v236 offset:512\n"
"v_cvt_pk_bf16_f32 v114, v176, v177\n"
"v_mfma_f32_16x16x32_bf16 v[172:175], v[220:223], a[28:31], v[172:175]\n"
"v_exp_f32 v162, v162\n"
"v_mfma_f32_16x16x32_bf16 v[172:175], v[208:211], a[16:19], v[172:175]\n"
"ds_read_b64_tr_b16 v[226:227], v237 offset:4608\n"
"v_cvt_pk_bf16_f32 v122, v152, v153\n"
"v_mfma_f32_16x16x32_bf16 v[172:175], v[212:215], a[20:23], v[172:175]\n"
"s_cbranch_vccnz LPCT1_%=\nLPKT1_%=:\n"
"ds_read_b64_tr_b16 a[252:253], v233 offset:512\n"
"v_mfma_f32_16x16x32_bf16 v[176:179], v[208:211], a[32:35], v[188:191]\n"
"v_exp_f32 v166, v166\n"
"v_mfma_f32_16x16x32_bf16 v[176:179], v[212:215], a[36:39], v[176:179]\n"
"ds_read_b64_tr_b16 a[254:255], v233 offset:4608\n"
"v_cvt_pk_bf16_f32 v109, v162, v163\n"
"v_mfma_f32_16x16x32_bf16 v[176:179], v[216:219], a[40:43], v[176:179]\n"
"v_exp_f32 v169, v169\n"
"v_mfma_f32_16x16x32_bf16 v[176:179], v[220:223], a[44:47], v[176:179]\n"
"s_waitcnt lgkmcnt(4)\n"
"v_cvt_pk_bf16_f32 v101, v158, v159\n"
"v_mfma_f32_16x16x32_bf16 a[224:227], v[124:127], a[244:247], 0\n"
"v_exp_f32 v154, v154\n"
"v_mfma_f32_16x16x32_bf16 v[16:19], v[128:131], a[244:247], 0\n"
"s_waitcnt vmcnt(8) lgkmcnt(3)\n"
"v_exp_f32 v168, v168\n"
"v_mfma_f32_16x16x32_bf16 v[16:19], v[132:135], a[248:251], v[16:19]\n"
"s_barrier\n"
"ds_read_b128 v[212:215], v229 offset:32768\n"
"ds_read_b128 v[216:219], v228 offset:33280\n"
"v_exp_f32 v165, v165\n"
"v_mfma_f32_16x16x32_bf16 v[48:51], v[136:139], a[244:247], 0\n"
"ds_read_b128 v[220:223], v229 offset:33280\n"
"v_xor_b32 v249, s75, v249\n"
"v_mfma_f32_16x16x32_bf16 v[48:51], v[140:143], a[248:251], v[48:51]\n"
"ds_read_b128 v[208:211], v228 offset:32768\n"
"v_add_u32 v243, 0x1000, v243\n"
"v_mfma_f32_16x16x32_bf16 v[80:83], v[144:147], a[244:247], 0\n"
"ds_read_b64_tr_b16 a[246:247], v234 offset:20480\n"
"v_cvt_pk_bf16_f32 v117, v166, v167\n"
"v_mfma_f32_16x16x32_bf16 v[80:83], v[148:151], a[248:251], v[80:83]\n"
"s_waitcnt lgkmcnt(5)\n"
"ds_read_b64_tr_b16 a[250:251], v238 offset:20480\n"
"v_xor_b32 v248, s75, v248\n"
"v_mfma_f32_16x16x32_bf16 v[84:87], v[148:151], v[224:227], 0\n"
"v_cvt_pk_bf16_f32 v100, v156, v157\n"
"v_cvt_pk_bf16_f32 v116, v164, v165\n"
"v_mfma_f32_16x16x32_bf16 v[84:87], v[144:147], a[252:255], v[84:87]\n"
"v_exp_f32 v160, v160\n"
"v_mfma_f32_16x16x32_bf16 v[52:55], v[140:143], v[224:227], 0\n"
"v_exp_f32 v176, v176\n"
"v_mfma_f32_16x16x32_bf16 v[52:55], v[136:139], a[252:255], v[52:55]\n"
"v_exp_f32 v177, v177\n"
"v_mfma_f32_16x16x32_bf16 v[20:23], v[132:135], v[224:227], 0\n"
"v_cvt_pk_bf16_f32 v123, v154, v155\n"
"v_cvt_pk_bf16_f32 v118, v176, v177\n"
"v_mfma_f32_16x16x32_bf16 v[20:23], v[128:131], a[252:255], v[20:23]\n"
"v_exp_f32 v174, v174\n"
"v_mfma_f32_16x16x32_bf16 a[228:231], v[124:127], a[252:255], 0\n"
"s_waitcnt lgkmcnt(1)\n"
"ds_read_b64_tr_b16 a[254:255], v235 offset:20480\n"
"s_branch LAY2_%=\n"
"LAX_%=:\n"
"v_mfma_f32_16x16x32_bf16 a[224:227], v[124:127], a[244:247], a[224:227]\n"
"v_exp_f32 v162, v162\n"
"v_mfma_f32_16x16x32_bf16 v[16:19], v[128:131], a[244:247], v[16:19]\n"
"s_waitcnt vmcnt(8)\n"
"v_exp_f32 v171, v171\n"
"v_mfma_f32_16x16x32_bf16 v[16:19], v[132:135], a[248:251], v[16:19]\n"
"s_barrier\n"
"v_exp_f32 v169, v169\n"
"v_exp_f32 v172, v172\n"
"v_exp_f32 v173, v173\n"
"v_exp_f32 v174, v174\n"
"v_lshlrev_b32_e64 v244, 24, s43\n"
"v_mfma_f32_16x16x32_bf16 v[48:51], v[136:139], a[244:247], v[48:51]\n"
"v_exp_f32 v170, v170\n"
"v_mfma_f32_16x16x32_bf16 v[48:51], v[140:143], a[248:251], v[48:51]\n"
"v_exp_f32 v179, v179\n"
"v_mfma_f32_16x16x32_bf16 v[80:83], v[144:147], a[244:247], v[80:83]\n"
"ds_read_b64_tr_b16 a[246:247], v234 offset:53248\n"
"v_cvt_pk_bf16_f32 v117, v166, v167\n"
"v_mfma_f32_16x16x32_bf16 v[80:83], v[148:151], a[248:251], v[80:83]\n"
"ds_read_b64_tr_b16 a[244:245], v234 offset:49152\n"
"v_cvt_pk_bf16_f32 v116, v164, v165\n"
"v_mfma_f32_16x16x32_bf16 v[84:87], v[148:151], v[224:227], v[84:87]\n"
"v_cvt_pk_bf16_f32 v100, v156, v157\n"
"v_accvgpr_read_b32 v253, a96\n"
"v_mfma_f32_16x16x32_bf16 v[84:87], v[144:147], a[252:255], v[84:87]\n"
"ds_read_b64_tr_b16 a[248:249], v238 offset:49152\n"
"ds_read_b64_tr_b16 a[250:251], v238 offset:53248\n"
"v_exp_f32 v168, v168\n"
"v_exp_f32 v175, v175\n"
"v_exp_f32 v176, v176\n"
"v_exp_f32 v177, v177\n"
"v_exp_f32 v178, v178\n"
"v_mfma_f32_16x16x32_bf16 v[52:55], v[140:143], v[224:227], v[52:55]\n"
"v_cvt_pk_bf16_f32 v102, v168, v169\n"
"v_cvt_pk_bf16_f32 v110, v172, v173\n"
"v_mfma_f32_16x16x32_bf16 v[52:55], v[136:139], a[252:255], v[52:55]\n"
"v_cvt_pk_bf16_f32 v103, v170, v171\n"
"v_and_b32 v253, 0x7fff0000, v253\n"
"v_mfma_f32_16x16x32_bf16 v[20:23], v[132:135], v[224:227], v[20:23]\n"
"ds_read_b64_tr_b16 v[224:225], v239 offset:49152\n"
"v_cvt_pk_bf16_f32 v101, v158, v159\n"
"v_mfma_f32_16x16x32_bf16 v[20:23], v[128:131], a[252:255], v[20:23]\n"
"v_cvt_pk_bf16_f32 v123, v154, v155\n"
"v_cvt_pk_bf16_f32 v111, v174, v175\n"
"v_mfma_f32_16x16x32_bf16 a[228:231], v[124:127], a[252:255], a[228:231]\n"
"s_waitcnt lgkmcnt(3)\n"
"ds_read_b64_tr_b16 a[254:255], v235 offset:53248\n"
"v_mul_f32 v244, 0x45800000, v244\n"
"v_mfma_f32_16x16x32_bf16 a[216:219], v[120:123], a[244:247], a[216:219]\n"
"s_mov_b64 s[28:29], s[8:9]\n"
"ds_read_b64_tr_b16 v[226:227], v239 offset:53248\n"
"v_cvt_pk_bf16_f32 v118, v176, v177\n"
"v_smfmac_f32_16x16x64_bf16 v[196:199], a[240:243], v[96:103], v241\n"
"s_mov_b32 s30, 655360\n"
"ds_read_b64_tr_b16 a[252:253], v235 offset:49152\n"
"v_cvt_pk_bf16_f32 v108, v160, v161\n"
"v_mfma_f32_16x16x32_bf16 v[8:11], v[96:99], a[244:247], v[8:11]\n"
"s_waitcnt lgkmcnt(4)\n"
"s_mov_b32 s71, s44\n"
"v_cvt_pk_bf16_f32 v119, v178, v179\n"
"v_max_u32_dpp v253, v253, v253 quad_perm:[1,0,3,2] row_mask:0xf bank_mask:0xf\n"
"v_mfma_f32_16x16x32_bf16 v[8:11], v[100:103], a[248:251], v[8:11]\n"
"s_mov_b32 s70, s95\n"
"v_and_b32 v252, 0x7fff0000, v252\n"
"v_and_b32 v245, 0x3f0, v242\n"
"v_smfmac_f32_16x16x64_bf16 v[200:203], a[240:243], v[112:119], v241\n"
"v_cvt_pk_bf16_f32 v109, v162, v163\n"
"v_max_u32_dpp v253, v253, v253 quad_perm:[2,3,0,1] row_mask:0xf bank_mask:0xf\n"
"v_mfma_f32_16x16x32_bf16 v[40:43], v[104:107], a[244:247], v[40:43]\n"
"v_add_f32 v196, v196, v197\n"
"v_lshrrev_b32 v245, 1, v245\n"
"v_mfma_f32_16x16x32_bf16 v[40:43], v[108:111], a[248:251], v[40:43]\n"
"s_add_u32 s41, s70, 327680\n"
"s_add_u32 m0, s71, 32768\n"
"v_max_u32_dpp v252, v252, v252 quad_perm:[1,0,3,2] row_mask:0xf bank_mask:0xf\n"
"v_smfmac_f32_16x16x64_bf16 v[198:201], a[240:243], v[104:111], v241\n"
"buffer_load_dwordx4 v240, s[28:31], s41 offen lds\n"
"v_mfma_f32_16x16x32_bf16 v[72:75], v[112:115], a[244:247], v[72:75]\n"
"v_max_u32_dpp v252, v252, v252 quad_perm:[2,3,0,1] row_mask:0xf bank_mask:0xf\n"
"v_max_u32_dpp v253, v253, v253 row_half_mirror row_mask:0xf bank_mask:0xf\n"
"v_mfma_f32_16x16x32_bf16 v[72:75], v[116:119], a[248:251], v[72:75]\n"
"ds_read_b64_tr_b16 a[244:245], v234 offset:49664\n"
"v_sub_u32 v245, v242, v245\n"
"v_mfma_f32_16x16x32_bf16 v[202:205], a[240:243], v[120:123], v[202:205]\n"
"s_waitcnt lgkmcnt(1)\n"
"ds_read_b64_tr_b16 a[246:247], v234 offset:53760\n"
"v_add_f32 v198, v198, v199\n"
"v_mfma_f32_16x16x32_bf16 v[76:79], v[116:119], v[224:227], v[76:79]\n"
"s_add_u32 s40, s41, s52\n"
"ds_read_b64_tr_b16 a[248:249], v238 offset:49664\n"
"ds_read_b64_tr_b16 a[250:251], v238 offset:53760\n"
"v_mfma_f32_16x16x32_bf16 v[76:79], v[112:115], a[252:255], v[76:79]\n"
"buffer_load_dwordx4 v240, s[28:31], s40 offen offset:1024 lds\n"
"v_mfma_f32_16x16x32_bf16 v[44:47], v[108:111], v[224:227], v[44:47]\n"
"v_add_f32 v200, v200, v201\n"
"v_add_f32 v244, v244, v202\n"
"v_mfma_f32_16x16x32_bf16 v[44:47], v[104:107], a[252:255], v[44:47]\n"
"v_add_f32 v254, v196, v198\n"
"v_min3_f32 v244, v244, v196, v198\n"
"v_min_f32 v244, v244, v200\n"
"v_rcp_f32 v244, v244\n"
"v_mfma_f32_16x16x32_bf16 v[12:15], v[100:103], v[224:227], v[12:15]\n"
"s_add_u32 s40, s41, s53\n"
"ds_read_b64_tr_b16 v[224:225], v239 offset:49664\n"
"ds_read_b64_tr_b16 v[226:227], v239 offset:53760\n"
"v_mfma_f32_16x16x32_bf16 v[12:15], v[96:99], a[252:255], v[12:15]\n"
"buffer_load_dwordx4 v240, s[28:31], s40 offen offset:2048 lds\n"
"v_min_u32_dpp v253, v253, v253 row_mirror row_mask:0xf bank_mask:0xf\n"
"v_add_f32 v254, v254, v200\n"
"v_add_f32 v254, v254, v202\n"
"v_fmac_f32 v254, 0x68800000, v244\n"
"v_cmp_nge_f32 vcc, s45, v254\n"
"v_bfe_u32 v254, v242, 8, 2\n"
"v_mfma_f32_16x16x32_bf16 a[220:223], v[120:123], a[252:255], a[220:223]\n"
"s_waitcnt lgkmcnt(4)\n"
"v_xor_b32 v244, 0x11000, v242\n"
"s_cbranch_vccnz LSLOW_%=\nLBACK3_%=:\ns_mov_b64 vcc, s[64:65]\n"
"v_mfma_f32_16x16x32_bf16 a[232:235], v[120:123], a[244:247], a[232:235]\n"
"ds_read_b64_tr_b16 a[252:253], v235 offset:49664\n"
"ds_read_b64_tr_b16 a[254:255], v235 offset:53760\n"
"v_min_u32_dpp v253, v253, v253 row_bcast:15 row_mask:0xa bank_mask:0xf\n"
"v_rcp_f32 v246, v196\n"
"v_rcp_f32 v247, v198\n"
"v_rcp_f32 v206, v200\n"
"v_xor_b32 v250, 0x1000, v245\n"
"v_mfma_f32_16x16x32_bf16 v[24:27], v[96:99], a[244:247], v[24:27]\n"
"s_waitcnt lgkmcnt(4)\n"
"v_mov_b32_dpp v206, v206 row_ror:12 row_mask:0x2 bank_mask:0xf\n"
"v_lshl_add_u32 v254, v254, 3, s43\n"
"v_mfma_f32_16x16x32_bf16 v[24:27], v[100:103], a[248:251], v[24:27]\n"
"ds_write_b128 v244, a[220:223] offset:33792\n"
"v_max_u32_dpp v252, v252, v252 row_half_mirror row_mask:0xf bank_mask:0xf\n"
"v_mov_b32_dpp v246, v246 row_ror:12 row_mask:0x2 bank_mask:0xf\n"
"v_mov_b32_dpp v206, v206 row_ror:8 row_mask:0x4 bank_mask:0xf\n"
"v_mfma_f32_16x16x32_bf16 v[56:59], v[104:107], a[244:247], v[56:59]\n"
"s_add_u32 s40, s41, s54\n"
"v_min_u32_dpp v252, v252, v252 row_mirror row_mask:0xf bank_mask:0xf\n"
"v_mov_b32_dpp v247, v247 row_ror:12 row_mask:0x2 bank_mask:0xf\n"
"v_mfma_f32_16x16x32_bf16 v[56:59], v[108:111], a[248:251], v[56:59]\n"
"buffer_load_dwordx4 v240, s[28:31], s40 offen offset:3072 lds\n"
"v_mfma_f32_16x16x32_bf16 v[88:91], v[112:115], a[244:247], v[88:91]\n"
"s_add_u32 s71, s44, s75\n"
"ds_read_b64_tr_b16 a[244:245], v232 offset:49152\n"
"ds_read_b64_tr_b16 a[246:247], v232 offset:53248\n"
"v_mfma_f32_16x16x32_bf16 v[88:91], v[116:119], a[248:251], v[88:91]\n"
"s_waitcnt lgkmcnt(5)\n"
"s_mov_b32 s70, s100\n"
"ds_write_b128 v244, a[232:235] offset:34816\n"
"v_mov_b32_dpp v246, v246 row_ror:8 row_mask:0x4 bank_mask:0xf\n"
"v_mov_b32_dpp v247, v247 row_ror:8 row_mask:0x4 bank_mask:0xf\n"
"v_mov_b32_dpp v206, v206 row_ror:4 row_mask:0x8 bank_mask:0xf\n"
"v_lshl_add_u32 v254, v254, 6, s75\n"
"v_sub_u32 v230, v234, v254\n"
"v_mfma_f32_16x16x32_bf16 v[92:95], v[116:119], v[224:227], v[92:95]\n"
"s_waitcnt lgkmcnt(4)\n"
"s_add_u32 s41, s70, 163840\n"
"s_add_u32 m0, s71, 16384\n"
"ds_read_b64_tr_b16 a[248:249], v236 offset:49152\n"
"v_mfma_f32_16x16x32_bf16 v[92:95], v[112:115], a[252:255], v[92:95]\n"
"buffer_load_dwordx4 v240, s[28:31], s41 offen lds\n"
"ds_read_b64_tr_b16 a[250:251], v236 offset:53248\n"
"v_min_u32_dpp v252, v252, v252 row_bcast:15 row_mask:0xa bank_mask:0xf\n"
"v_mov_b32_dpp v246, v246 row_ror:4 row_mask:0x8 bank_mask:0xf\n"
"v_mov_b32_dpp v247, v247 row_ror:4 row_mask:0x8 bank_mask:0xf\n"
"v_mul_f32_dpp v72, v206, v72 row_newbcast:0 row_mask:0xf bank_mask:0xf\n"
"v_mul_f32_dpp v74, v206, v74 row_newbcast:2 row_mask:0xf bank_mask:0xf\n"
"v_mul_f32_dpp v76, v206, v76 row_newbcast:0 row_mask:0xf bank_mask:0xf\n"
"v_mul_f32_dpp v77, v206, v77 row_newbcast:1 row_mask:0xf bank_mask:0xf\n"
"v_mfma_f32_16x16x32_bf16 v[60:63], v[108:111], v[224:227], v[60:63]\n"
"v_min_u32_dpp v252, v252, v252 row_bcast:31 row_mask:0xc bank_mask:0xf\n"
"v_mul_f32_dpp v8, v246, v8 row_newbcast:0 row_mask:0xf bank_mask:0xf\n"
"v_mfma_f32_16x16x32_bf16 v[60:63], v[104:107], a[252:255], v[60:63]\n"
"v_mul_f32_dpp v41, v247, v41 row_newbcast:1 row_mask:0xf bank_mask:0xf\n"
"v_mul_f32_dpp v59, v247, v59 row_newbcast:3 row_mask:0xf bank_mask:0xf\n"
"v_mfma_f32_16x16x32_bf16 v[28:31], v[100:103], v[224:227], v[28:31]\n"
"s_add_u32 s40, s41, s52\n"
"ds_read_b64_tr_b16 v[224:225], v237 offset:49152\n"
"v_min_u32_dpp v253, v253, v253 row_bcast:31 row_mask:0xc bank_mask:0xf\n"
"v_mfma_f32_16x16x32_bf16 v[28:31], v[96:99], a[252:255], v[28:31]\n"
"buffer_load_dwordx4 v240, s[28:31], s40 offen offset:1024 lds\n"
"v_mfma_f32_16x16x32_bf16 a[236:239], v[120:123], a[252:255], a[236:239]\n"
"s_waitcnt lgkmcnt(4)\n"
"v_mul_f32_dpp v25, v246, v25 row_newbcast:1 row_mask:0xf bank_mask:0xf\n"
"v_mul_f32_dpp v94, v206, v94 row_newbcast:2 row_mask:0xf bank_mask:0xf\n"
"v_mfma_f32_16x16x32_bf16 a[208:211], v[120:123], a[244:247], a[208:211]\n"
"ds_read_b64_tr_b16 a[252:253], v233 offset:49152\n"
"ds_read_b64_tr_b16 a[254:255], v233 offset:53248\n"
"v_mul_f32 v252, 0x43340000, v252\n"
"v_mul_f32 v252, v252, v253\n"
"v_mul_f32_dpp v10, v246, v10 row_newbcast:2 row_mask:0xf bank_mask:0xf\n"
"v_mul_f32_dpp v14, v246, v14 row_newbcast:2 row_mask:0xf bank_mask:0xf\n"
"v_mul_f32_dpp v40, v247, v40 row_newbcast:0 row_mask:0xf bank_mask:0xf\n"
"v_mul_f32_dpp v46, v247, v46 row_newbcast:2 row_mask:0xf bank_mask:0xf\n"
"v_mul_f32_dpp v73, v206, v73 row_newbcast:1 row_mask:0xf bank_mask:0xf\n"
"v_mul_f32_dpp v90, v206, v90 row_newbcast:2 row_mask:0xf bank_mask:0xf\n"
"v_mfma_f32_16x16x32_bf16 v[0:3], v[96:99], a[244:247], v[0:3]\n"
"s_waitcnt lgkmcnt(3)\n"
"ds_read_b64_tr_b16 v[226:227], v237 offset:53248\n"
"v_mul_f32_dpp v57, v247, v57 row_newbcast:1 row_mask:0xf bank_mask:0xf\n"
"v_mfma_f32_16x16x32_bf16 v[0:3], v[100:103], a[248:251], v[0:3]\n"
"ds_write_b128 v244, a[216:219] offset:32768\n"
"v_min_f32 v252, 0x71800000, v252\n"
"v_mul_f32_dpp v12, v246, v12 row_newbcast:0 row_mask:0xf bank_mask:0xf\n"
"v_mul_f32_dpp v45, v247, v45 row_newbcast:1 row_mask:0xf bank_mask:0xf\n"
"v_mul_f32_dpp v58, v247, v58 row_newbcast:2 row_mask:0xf bank_mask:0xf\n"
"v_mul_f32_dpp v92, v206, v92 row_newbcast:0 row_mask:0xf bank_mask:0xf\n"
"v_mfma_f32_16x16x32_bf16 v[32:35], v[104:107], a[244:247], v[32:35]\n"
"s_add_u32 s40, s41, s53\n"
"v_readlane_b32 s22, v252, 63\n"
"v_mul_f32_dpp v78, v206, v78 row_newbcast:2 row_mask:0xf bank_mask:0xf\n"
"v_mfma_f32_16x16x32_bf16 v[32:35], v[108:111], a[248:251], v[32:35]\n"
"buffer_load_dwordx4 v240, s[28:31], s40 offen offset:2048 lds\n"
"v_mfma_f32_16x16x32_bf16 v[64:67], v[112:115], a[244:247], v[64:67]\n"
"v_mul_f32_dpp v75, v206, v75 row_newbcast:3 row_mask:0xf bank_mask:0xf\n"
"v_mul_f32_dpp v61, v247, v61 row_newbcast:1 row_mask:0xf bank_mask:0xf\n"
"v_mfma_f32_16x16x32_bf16 v[64:67], v[116:119], a[248:251], v[64:67]\n"
"s_waitcnt lgkmcnt(1)\n"
"ds_read_b64_tr_b16 a[244:245], v232 offset:49664\n"
"ds_read_b64_tr_b16 a[246:247], v232 offset:53760\n"
"v_mov_b32 v253, v202\n"
"v_sub_u32 v228, v232, v254\n"
"v_mul_f32_dpp v9, v246, v9 row_newbcast:1 row_mask:0xf bank_mask:0xf\n"
"v_mul_f32_dpp v11, v246, v11 row_newbcast:3 row_mask:0xf bank_mask:0xf\n"
"v_mul_f32_dpp v15, v246, v15 row_newbcast:3 row_mask:0xf bank_mask:0xf\n"
"v_mul_f32_dpp v47, v247, v47 row_newbcast:3 row_mask:0xf bank_mask:0xf\n"
"v_cvt_pk_bf16_f32 v40, v40, v41\n"
"v_cvt_pk_bf16_f32 v72, v72, v73\n"
"v_mfma_f32_16x16x32_bf16 v[68:71], v[116:119], v[224:227], v[68:71]\n"
"s_add_u32 s40, s41, s54\n"
"ds_read_b64_tr_b16 a[248:249], v236 offset:49664\n"
"ds_read_b64_tr_b16 a[250:251], v236 offset:53760\n"
"v_mfma_f32_16x16x32_bf16 v[68:71], v[112:115], a[252:255], v[68:71]\n"
"buffer_load_dwordx4 v240, s[28:31], s40 offen offset:3072 lds\n"
"v_readlane_b32 s23, v252, 63\n"
"v_mul_f32_dpp v43, v247, v43 row_newbcast:3 row_mask:0xf bank_mask:0xf\n"
"v_mul_f32_dpp v24, v246, v24 row_newbcast:0 row_mask:0xf bank_mask:0xf\n"
"v_mul_f32_dpp v29, v246, v29 row_newbcast:1 row_mask:0xf bank_mask:0xf\n"
"v_mul_f32_dpp v56, v247, v56 row_newbcast:0 row_mask:0xf bank_mask:0xf\n"
"v_mul_f32_dpp v1, v246, v1 row_newbcast:1 row_mask:0xf bank_mask:0xf\n"
"v_accvgpr_read_b32 v255, a209\n"
"v_xor_b32 v232, 0x1000, v228\n"
"v_mfma_f32_16x16x32_bf16 v[36:39], v[108:111], v[224:227], v[36:39]\n"
"v_mov_b32 v252, v192\n"
"v_mul_f32_dpp v44, v247, v44 row_newbcast:0 row_mask:0xf bank_mask:0xf\n"
"v_mfma_f32_16x16x32_bf16 v[36:39], v[104:107], a[252:255], v[36:39]\n"
"ds_write_b128 v244, a[236:239] offset:35840\n"
"v_mul_f32_dpp v13, v246, v13 row_newbcast:1 row_mask:0xf bank_mask:0xf\n"
"v_mul_f32_dpp v42, v247, v42 row_newbcast:2 row_mask:0xf bank_mask:0xf\n"
"v_cvt_pk_bf16_f32 v41, v42, v43\n"
"v_cvt_pk_bf16_f32 v42, v44, v45\n"
"v_cvt_pk_bf16_f32 v56, v56, v57\n"
"v_mfma_f32_16x16x32_bf16 v[4:7], v[100:103], v[224:227], v[4:7]\n"
"ds_read_b64_tr_b16 v[224:225], v237 offset:49664\n"
"ds_read_b64_tr_b16 v[226:227], v237 offset:53760\n"
"v_mfma_f32_16x16x32_bf16 v[4:7], v[96:99], a[252:255], v[4:7]\n"
"ds_write_b64 v250, v[252:253] offset:52224\n"
"v_cvt_pk_bf16_f32 v8, v8, v9\n"
"v_cvt_pk_bf16_f32 v43, v46, v47\n"
"v_cvt_pk_bf16_f32 v73, v74, v75\n"
"v_mul_f32_dpp v28, v246, v28 row_newbcast:0 row_mask:0xf bank_mask:0xf\n"
"v_cvt_pk_bf16_f32 v57, v58, v59\n"
"v_mul_f32_dpp v0, v246, v0 row_newbcast:0 row_mask:0xf bank_mask:0xf\n"
"v_mul_f32_dpp v33, v247, v33 row_newbcast:1 row_mask:0xf bank_mask:0xf\n"
"v_mfma_f32_16x16x32_bf16 a[212:215], v[120:123], a[252:255], a[212:215]\n"
"s_waitcnt lgkmcnt(6)\n"
"ds_read_b64_tr_b16 a[252:253], v233 offset:49664\n"
"ds_read_b64_tr_b16 a[254:255], v233 offset:53760\n"
"v_cvt_pk_bf16_f32 v9, v10, v11\n"
"v_cvt_pk_bf16_f32 v74, v76, v77\n"
"v_mul_f32_dpp v60, v247, v60 row_newbcast:0 row_mask:0xf bank_mask:0xf\n"
"v_mul_f32_dpp v62, v247, v62 row_newbcast:2 row_mask:0xf bank_mask:0xf\n"
"v_mul_f32_dpp v88, v206, v88 row_newbcast:0 row_mask:0xf bank_mask:0xf\n"
"v_mul_f32_dpp v64, v206, v64 row_newbcast:0 row_mask:0xf bank_mask:0xf\n"
"v_mul_f32_dpp v66, v206, v66 row_newbcast:2 row_mask:0xf bank_mask:0xf\n"
"v_mfma_f32_16x16x32_bf16 a[224:227], v[120:123], a[244:247], a[224:227]\n"
"ds_write_b128 v248, v[40:43] offset:50176\n"
"v_sub_u32 v229, v233, v254\n"
"v_cvt_pk_bf16_f32 v11, v14, v15\n"
"v_mul_f32_dpp v27, v246, v27 row_newbcast:3 row_mask:0xf bank_mask:0xf\n"
"v_mul_f32_dpp v3, v246, v3 row_newbcast:3 row_mask:0xf bank_mask:0xf\n"
"v_mul_f32_dpp v68, v206, v68 row_newbcast:0 row_mask:0xf bank_mask:0xf\n"
"v_mfma_f32_16x16x32_bf16 v[16:19], v[96:99], a[244:247], v[16:19]\n"
"s_waitcnt vmcnt(8) lgkmcnt(1)\n"
"v_mul_f32_dpp v89, v206, v89 row_newbcast:1 row_mask:0xf bank_mask:0xf\n"
"v_add_u32 v236, s76, v249\n"
"v_mfma_f32_16x16x32_bf16 v[16:19], v[100:103], a[248:251], v[16:19]\n"
"s_barrier\n"
"ds_read_b64_tr_b16 a[22:23], v249 offset:50184\n"
"ds_read_b64_tr_b16 v[182:183], v228 offset:2048\n"
"ds_read_b64_tr_b16 v[216:217], v228 offset:24576\n"
"ds_read_b64_tr_b16 v[218:219], v228 offset:26624\n"
"ds_read_b64_tr_b16 a[202:203], v236 offset:2056\n"
"v_mul_f32_dpp v63, v247, v63 row_newbcast:3 row_mask:0xf bank_mask:0xf\n"
"v_mul_f32_dpp v91, v206, v91 row_newbcast:3 row_mask:0xf bank_mask:0xf\n"
"v_mul_f32_dpp v2, v246, v2 row_newbcast:2 row_mask:0xf bank_mask:0xf\n"
"v_mfma_f32_16x16x32_bf16 v[48:51], v[104:107], a[244:247], v[48:51]\n"
"ds_read_b64_tr_b16 v[192:193], v232 offset:8192\n"
"ds_read_b64_tr_b16 a[240:241], v236 offset:4096\n"
"v_mfma_f32_16x16x32_bf16 v[48:51], v[108:111], a[248:251], v[48:51]\n"
"ds_read_b64 v[250:251], v245 offset:52224\n"
"ds_read_b64_tr_b16 v[186:187], v232 offset:2048\n"
"ds_read_b64_tr_b16 v[194:195], v232 offset:10240\n"
"ds_read_b64_tr_b16 v[208:209], v228 offset:16384\n"
"v_cvt_pk_bf16_f32 v10, v12, v13\n"
"v_mul_f32_dpp v79, v206, v79 row_newbcast:3 row_mask:0xf bank_mask:0xf\n"
"v_cvt_pk_bf16_f32 v75, v78, v79\n"
"v_cvt_pk_bf16_f32 v88, v88, v89\n"
"v_cvt_pk_bf16_f32 v89, v90, v91\n"
"v_cvt_pk_bf16_f32 v0, v0, v1\n"
"v_mfma_f32_16x16x32_bf16 v[80:83], v[112:115], a[244:247], v[80:83]\n"
"ds_read_b64_tr_b16 v[180:181], v228 offset:0\n"
"ds_read_b64_tr_b16 v[188:189], v228 offset:8192\n"
"v_mfma_f32_16x16x32_bf16 v[80:83], v[116:119], a[248:251], v[80:83]\n"
"ds_write_b128 v248, v[72:75] offset:51200\n"
"v_mul_f32_dpp v30, v246, v30 row_newbcast:2 row_mask:0xf bank_mask:0xf\n"
"v_cvt_pk_bf16_f32 v24, v24, v25\n"
"v_cvt_pk_bf16_f32 v58, v60, v61\n"
"v_mul_f32_dpp v93, v206, v93 row_newbcast:1 row_mask:0xf bank_mask:0xf\n"
"v_mul_f32_dpp v36, v247, v36 row_newbcast:0 row_mask:0xf bank_mask:0xf\n"
"v_mfma_f32_16x16x32_bf16 v[84:87], v[116:119], v[224:227], v[84:87]\n"
"ds_write_b128 v248, v[8:11] offset:49152\n"
"v_mul_f32_dpp v26, v246, v26 row_newbcast:2 row_mask:0xf bank_mask:0xf\n"
"v_mul_f32_dpp v32, v247, v32 row_newbcast:0 row_mask:0xf bank_mask:0xf\n"
"v_mul_f32_dpp v65, v206, v65 row_newbcast:1 row_mask:0xf bank_mask:0xf\n"
"v_mul_f32_dpp v69, v206, v69 row_newbcast:1 row_mask:0xf bank_mask:0xf\n"
"v_xor_b32 v233, 0x1000, v229\n"
"v_mfma_f32_16x16x32_bf16 v[84:87], v[112:115], a[252:255], v[84:87]\n"
"ds_read_b64_tr_b16 a[6:7], v249 offset:49160\n"
"ds_read_b64_tr_b16 a[36:37], v249 offset:51200\n"
"ds_read_b64_tr_b16 v[210:211], v228 offset:18432\n"
"ds_read_b64_tr_b16 v[222:223], v232 offset:26624\n"
"v_mul_f32_dpp v31, v246, v31 row_newbcast:3 row_mask:0xf bank_mask:0xf\n"
"v_cvt_pk_bf16_f32 v25, v26, v27\n"
"v_cvt_pk_bf16_f32 v59, v62, v63\n"
"v_mul_f32_dpp v34, v247, v34 row_newbcast:2 row_mask:0xf bank_mask:0xf\n"
"v_mov_b64 v[196:197], s[22:23]\n"
"v_xor_b32 v237, 0x400, v236\n"
"v_mfma_f32_16x16x32_bf16 v[52:55], v[108:111], v[224:227], v[52:55]\n"
"ds_read_b64_tr_b16 v[184:185], v232 offset:0\n"
"ds_read_b64_tr_b16 v[212:213], v232 offset:16384\n"
"v_mfma_f32_16x16x32_bf16 v[52:55], v[104:107], a[252:255], v[52:55]\n"
"ds_read_b64_tr_b16 a[4:5], v249 offset:49152\n"
"ds_read_b64_tr_b16 a[20:21], v249 offset:50176\n"
"ds_read_b64_tr_b16 a[38:39], v249 offset:51208\n"
"ds_read_b64_tr_b16 v[220:221], v232 offset:24576\n"
"ds_read_b64_tr_b16 a[246:247], v237 offset:4104\n"
"v_cvt_pk_bf16_f32 v26, v28, v29\n"
"v_mul_f32_dpp v95, v206, v95 row_newbcast:3 row_mask:0xf bank_mask:0xf\n"
"v_cvt_pk_bf16_f32 v91, v94, v95\n"
"v_mul_f32_dpp v5, v246, v5 row_newbcast:1 row_mask:0xf bank_mask:0xf\n"
"v_mfma_f32_16x16x32_bf16 v[20:23], v[100:103], v[224:227], v[20:23]\n"
"ds_read_b64_tr_b16 v[190:191], v228 offset:10240\n"
"ds_read_b64_tr_b16 v[214:215], v232 offset:18432\n"
"v_mfma_f32_16x16x32_bf16 v[20:23], v[96:99], a[252:255], v[20:23]\n"
"ds_write_b128 v248, v[56:59] offset:50176\n"
"v_cvt_pk_bf16_f32 v27, v30, v31\n"
"v_mul_f32_dpp v4, v246, v4 row_newbcast:0 row_mask:0xf bank_mask:0xf\n"
"v_mul_f32_dpp v35, v247, v35 row_newbcast:3 row_mask:0xf bank_mask:0xf\n"
"v_cvt_pk_bf16_f32 v64, v64, v65\n"
"v_mov_b64 v[198:199], s[22:23]\n"
"v_mfma_f32_16x16x32_bf16 a[228:231], v[120:123], a[252:255], a[228:231]\n"
"s_waitcnt lgkmcnt(15)\n"
"ds_write_b128 v248, v[24:27] offset:49152\n"
"v_cvt_pk_bf16_f32 v90, v92, v93\n"
"v_mul_f32_dpp v6, v246, v6 row_newbcast:2 row_mask:0xf bank_mask:0xf\n"
"v_cvt_pk_bf16_f32 v1, v2, v3\n"
"v_cvt_pk_bf16_f32 v2, v4, v5\n"
"v_mul_f32_dpp v71, v206, v71 row_newbcast:3 row_mask:0xf bank_mask:0xf\n"
"v_mfma_f32_16x16x32_bf16 v[124:127], v[180:183], a[96:99], v[196:199]\n"
"s_waitcnt lgkmcnt(10)\n"
"ds_read_b64_tr_b16 a[14:15], v249 offset:49160\n"
"ds_read_b64_tr_b16 a[30:31], v249 offset:50184\n"
"v_mfma_f32_16x16x32_bf16 v[124:127], v[184:187], a[100:103], v[124:127]\n"
"s_waitcnt lgkmcnt(5)\n"
"ds_write_b128 v248, v[88:91] offset:51200\n"
"v_mul_f32_dpp v7, v246, v7 row_newbcast:3 row_mask:0xf bank_mask:0xf\n"
"v_cvt_pk_bf16_f32 v3, v6, v7\n"
"v_mul_f32_dpp v37, v247, v37 row_newbcast:1 row_mask:0xf bank_mask:0xf\n"
"v_cvt_pk_bf16_f32 v32, v32, v33\n"
"v_mul_f32_dpp v67, v206, v67 row_newbcast:3 row_mask:0xf bank_mask:0xf\n"
"v_mfma_f32_16x16x32_bf16 v[124:127], v[188:191], a[104:107], v[124:127]\n"
"ds_read_b64_tr_b16 a[12:13], v249 offset:49152\n"
"ds_read_b64_tr_b16 a[44:45], v249 offset:51200\n"
"ds_read_b64_tr_b16 a[46:47], v249 offset:51208\n"
"ds_read_b64_tr_b16 v[100:101], v233 offset:0\n"
"ds_read_b64_tr_b16 v[120:121], v229 offset:24576\n"
"v_mul_f32_dpp v38, v247, v38 row_newbcast:2 row_mask:0xf bank_mask:0xf\n"
"v_mul_f32_dpp v70, v206, v70 row_newbcast:2 row_mask:0xf bank_mask:0xf\n"
"v_cvt_pk_bf16_f32 v65, v66, v67\n"
"v_cvt_pk_bf16_f32 v66, v68, v69\n"
"v_cvt_pk_bf16_f32 v67, v70, v71\n"
"v_mfma_f32_16x16x32_bf16 v[124:127], v[192:195], a[108:111], v[124:127]\n"
"ds_write_b128 v248, v[0:3] offset:49152\n"
"v_mul_f32_dpp v39, v247, v39 row_newbcast:3 row_mask:0xf bank_mask:0xf\n"
"v_cvt_pk_bf16_f32 v33, v34, v35\n"
"v_cvt_pk_bf16_f32 v34, v36, v37\n"
"v_cvt_pk_bf16_f32 v35, v38, v39\n"
"v_xor_b32 v244, 0x1000, v244\n"
"v_mfma_f32_16x16x32_bf16 v[124:127], v[208:211], a[144:147], v[124:127]\n"
"s_waitcnt lgkmcnt(11)\n"
"ds_read_b64_tr_b16 a[28:29], v249 offset:50176\n"
"ds_read_b128 v[24:27], v244 offset:32768\n"
"v_mfma_f32_16x16x32_bf16 v[124:127], v[212:215], a[148:151], v[124:127]\n"
"ds_write_b128 v248, v[32:35] offset:50176\n"
"ds_write_b128 v248, v[64:67] offset:51200\n"
"v_mfma_f32_16x16x32_bf16 v[124:127], v[216:219], a[152:155], v[124:127]\n"
"ds_read_b64_tr_b16 a[0:1], v249 offset:49152\n"
"ds_read_b64_tr_b16 a[32:33], v249 offset:51200\n"
"v_mfma_f32_16x16x32_bf16 v[124:127], v[220:223], a[156:159], v[124:127]\n"
"ds_read_b64_tr_b16 a[16:17], v249 offset:50176\n"
"ds_read_b64_tr_b16 a[18:19], v249 offset:50184\n"
"ds_read_b128 v[56:59], v244 offset:34816\n"
"ds_read_b64_tr_b16 a[196:197], v237 offset:0\n"
"v_sub_u32 v231, v235, v254\n"
"v_mfma_f32_16x16x32_bf16 v[140:143], v[220:223], a[172:175], v[196:199]\n"
"ds_read_b64_tr_b16 a[2:3], v249 offset:49160\n"
"ds_read_b64_tr_b16 a[34:35], v249 offset:51208\n"
"v_mfma_f32_16x16x32_bf16 v[140:143], v[216:219], a[168:171], v[140:143]\n"
"ds_read_b128 v[28:31], v244 offset:33792\n"
"ds_read_b128 v[60:63], v244 offset:35840\n"
"v_mfma_f32_16x16x32_bf16 v[140:143], v[212:215], a[164:167], v[140:143]\n"
"ds_read_b64_tr_b16 a[200:201], v236 offset:2048\n"
"ds_read_b64_tr_b16 a[254:255], v237 offset:6152\n"
"v_mfma_f32_16x16x32_bf16 v[140:143], v[208:211], a[160:163], v[140:143]\n"
"ds_read_b64_tr_b16 a[206:207], v237 offset:2056\n"
"ds_read_b64_tr_b16 a[242:243], v236 offset:4104\n"
"v_mfma_f32_16x16x32_bf16 v[140:143], v[192:195], a[124:127], v[140:143]\n"
"ds_read_b64_tr_b16 a[244:245], v237 offset:4096\n"
"v_xor_b32 v234, 0x1000, v230\n"
"v_mfma_f32_16x16x32_bf16 v[140:143], v[188:191], a[120:123], v[140:143]\n"
"ds_read_b64_tr_b16 v[110:111], v233 offset:10240\n"
"ds_read_b64_tr_b16 a[250:251], v236 offset:6152\n"
"v_mfma_f32_16x16x32_bf16 v[140:143], v[184:187], a[116:119], v[140:143]\n"
"ds_read_b64_tr_b16 a[204:205], v237 offset:2048\n"
"ds_read_b64_tr_b16 v[102:103], v233 offset:2048\n"
"v_mfma_f32_16x16x32_bf16 v[140:143], v[180:183], a[112:115], v[140:143]\n"
"ds_read_b64_tr_b16 a[248:249], v236 offset:6144\n"
"ds_read_b64_tr_b16 a[252:253], v237 offset:6144\n"
"v_mfma_f32_16x16x32_bf16 v[156:159], v[180:183], a[128:131], v[196:199]\n"
"ds_read_b64_tr_b16 v[98:99], v229 offset:2048\n"
"ds_read_b64_tr_b16 v[116:117], v233 offset:16384\n"
"v_mfma_f32_16x16x32_bf16 v[156:159], v[184:187], a[132:135], v[156:159]\n"
"ds_read_b64_tr_b16 v[122:123], v229 offset:26624\n"
"ds_read_b64_tr_b16 v[224:225], v233 offset:24576\n"
"v_mfma_f32_16x16x32_bf16 v[156:159], v[188:191], a[136:139], v[156:159]\n"
"ds_read_b64_tr_b16 a[198:199], v237 offset:8\n"
"ds_read_b64_tr_b16 v[96:97], v229 offset:0\n"
"v_mfma_f32_16x16x32_bf16 v[156:159], v[192:195], a[140:143], v[156:159]\n"
"s_waitcnt lgkmcnt(13)\n"
"ds_read_b64_tr_b16 v[108:109], v233 offset:8192\n"
"v_mul_f32_dpp v23, v246, v23 row_newbcast:3 row_mask:0xf bank_mask:0xf\n"
"v_mfma_f32_16x16x32_bf16 v[156:159], v[208:211], a[176:179], v[156:159]\n"
"ds_read_b64_tr_b16 v[106:107], v229 offset:10240\n"
"ds_read_b64_tr_b16 v[112:113], v229 offset:16384\n"
"v_mfma_f32_16x16x32_bf16 v[156:159], v[212:215], a[180:183], v[156:159]\n"
"s_add_u32 s41, s95, 491520\n"
"ds_read_b64_tr_b16 a[192:193], v236 offset:0\n"
"ds_read_b64_tr_b16 v[118:119], v233 offset:18432\n"
"v_mfma_f32_16x16x32_bf16 v[156:159], v[216:219], a[184:187], v[156:159]\n"
"s_add_u32 m0, s44, 49152\n"
"ds_read_b64_tr_b16 a[194:195], v236 offset:8\n"
"ds_read_b64_tr_b16 v[104:105], v229 offset:8192\n"
"v_mfma_f32_16x16x32_bf16 v[156:159], v[220:223], a[188:191], v[156:159]\n"
"s_waitcnt lgkmcnt(13)\n"
"buffer_load_dwordx4 v240, s[28:31], s41 offen lds\n"
"v_mfma_f32_16x16x32_bf16 v[172:175], v[220:223], a[252:255], v[196:199]\n"
"s_add_u32 s40, s41, s52\n"
"v_min_f32 v254, v252, v250\n"
"v_sub_f32 v250, v254, v250\n"
"v_mfma_f32_16x16x32_bf16 v[172:175], v[216:219], a[248:251], v[172:175]\n"
"buffer_load_dwordx4 v240, s[28:31], s40 offen offset:1024 lds\n"
"v_mfma_f32_16x16x32_bf16 v[172:175], v[212:215], a[244:247], v[172:175]\n"
"s_add_u32 s40, s41, s53\n"
"ds_read_b64_tr_b16 v[114:115], v229 offset:18432\n"
"ds_read_b64_tr_b16 v[218:219], v230 offset:26624\n"
"v_mfma_f32_16x16x32_bf16 v[172:175], v[208:211], a[240:243], v[172:175]\n"
"buffer_load_dwordx4 v240, s[28:31], s40 offen offset:2048 lds\n"
"v_mfma_f32_16x16x32_bf16 v[172:175], v[192:195], a[204:207], v[172:175]\n"
"ds_read_b64_tr_b16 v[226:227], v233 offset:26624\n"
"ds_read_b64_tr_b16 a[232:233], v236 offset:10240\n"
"v_mfma_f32_16x16x32_bf16 v[172:175], v[188:191], a[200:203], v[172:175]\n"
"s_waitcnt lgkmcnt(6)\n"
"v_xor_b32 v235, 0x1000, v231\n"
"v_mul_f32_dpp v17, v246, v17 row_newbcast:1 row_mask:0xf bank_mask:0xf\n"
"v_mfma_f32_16x16x32_bf16 v[172:175], v[184:187], a[196:199], v[172:175]\n"
"s_waitcnt lgkmcnt(4)\n"
"s_add_u32 s40, s41, s54\n"
"ds_read_b64_tr_b16 v[222:223], v234 offset:26624\n"
"v_mul_f32_dpp v18, v246, v18 row_newbcast:2 row_mask:0xf bank_mask:0xf\n"
"v_mfma_f32_16x16x32_bf16 v[172:175], v[180:183], a[192:195], v[172:175]\n"
"buffer_load_dwordx4 v240, s[28:31], s40 offen offset:3072 lds\n"
"v_mfma_f32_16x16x32_bf16 v[128:131], v[96:99], a[96:99], v[196:199]\n"
"v_sub_f32 v252, v254, v252\n"
"v_mul_f32_dpp v16, v246, v16 row_newbcast:0 row_mask:0xf bank_mask:0xf\n"
"v_mfma_f32_16x16x32_bf16 v[128:131], v[100:103], a[100:103], v[128:131]\n"
"ds_read_b64_tr_b16 v[190:191], v230 offset:10240\n"
"v_mul_f32_dpp v86, v206, v86 row_newbcast:2 row_mask:0xf bank_mask:0xf\n"
"v_mfma_f32_16x16x32_bf16 v[128:131], v[104:107], a[104:107], v[128:131]\n"
"v_exp_f32 v252, v252\n"
"v_mfma_f32_16x16x32_bf16 v[128:131], v[108:111], a[108:111], v[128:131]\n"
"s_waitcnt lgkmcnt(4)\n"
"v_exp_f32 v250, v250\n"
"v_mfma_f32_16x16x32_bf16 v[128:131], v[112:115], a[144:147], v[128:131]\n"
"s_add_u32 s21, s60, 32\n"
"ds_read_b64_tr_b16 a[220:221], v237 offset:8192\n"
"v_mul_f32 v253, v253, v252\n"
"v_mfma_f32_16x16x32_bf16 v[128:131], v[116:119], a[148:151], v[128:131]\n"
"s_mov_b32 s69, 1\n"
"ds_read_b64_tr_b16 v[186:187], v234 offset:2048\n"
"v_fmac_f32 v253, v251, v250\n"
"v_mfma_f32_16x16x32_bf16 v[128:131], v[120:123], a[152:155], v[128:131]\n"
"s_waitcnt lgkmcnt(3)\n"
"v_bfe_u32 v201, v242, 2, 7\n"
"v_add_u32 v201, 0x100, v201\n"
"ds_read_b64_tr_b16 v[184:185], v234 offset:0\n"
"v_mfma_f32_16x16x32_bf16 v[128:131], v[224:227], a[156:159], v[128:131]\n"
"global_load_dword v200, v201, s[6:7] sc1\n"
"v_mfma_f32_16x16x32_bf16 v[144:147], v[224:227], a[172:175], v[196:199]\n"
"v_rcp_f32 v253, v253\n"
"v_mfma_f32_16x16x32_bf16 v[144:147], v[120:123], a[168:171], v[144:147]\n"
"v_mul_f32 v252, v252, v253\n"
"v_accvgpr_read_b32 v254, a208\n"
"v_mfma_f32_16x16x32_bf16 v[144:147], v[116:119], a[164:167], v[144:147]\n"
"ds_read_b64_tr_b16 v[188:189], v230 offset:8192\n"
"v_mul_f32 v250, v250, v253\n"
"v_mfma_f32_16x16x32_bf16 v[144:147], v[112:115], a[160:163], v[144:147]\n"
"s_lshl_b32 s20, s95, 9\n"
"ds_read_b64_tr_b16 v[194:195], v234 offset:10240\n"
"v_mov_b32_dpp v250, v250 row_ror:12 row_mask:0x2 bank_mask:0xf\n"
"v_mfma_f32_16x16x32_bf16 v[144:147], v[108:111], a[124:127], v[144:147]\n"
"s_lshl_b32 s19, s61, 7\n"
"v_mov_b32_dpp v250, v250 row_ror:8 row_mask:0x4 bank_mask:0xf\n"
"v_accvgpr_read_b32 v253, a210\n"
"v_mfma_f32_16x16x32_bf16 v[144:147], v[104:107], a[120:123], v[144:147]\n"
"s_lshl_b32 s91, s19, 9\n"
"v_mov_b32_dpp v252, v252 row_ror:12 row_mask:0x2 bank_mask:0xf\n"
"v_mov_b32_dpp v250, v250 row_ror:4 row_mask:0x8 bank_mask:0xf\n"
"v_mfma_f32_16x16x32_bf16 v[144:147], v[100:103], a[116:119], v[144:147]\n"
"ds_read_b64_tr_b16 v[214:215], v234 offset:18432\n"
"v_mul_f32_dpp v63, v250, v63 row_newbcast:3 row_mask:0xf bank_mask:0xf\n"
"v_mfma_f32_16x16x32_bf16 v[144:147], v[96:99], a[112:115], v[144:147]\n"
"ds_read_b64_tr_b16 v[208:209], v230 offset:16384\n"
"v_mul_f32_dpp v83, v206, v83 row_newbcast:3 row_mask:0xf bank_mask:0xf\n"
"v_mfma_f32_16x16x32_bf16 v[160:163], v[96:99], a[128:131], v[196:199]\n"
"ds_read_b64_tr_b16 v[192:193], v234 offset:8192\n"
"ds_read_b64_tr_b16 v[212:213], v234 offset:16384\n"
"v_mfma_f32_16x16x32_bf16 v[160:163], v[100:103], a[132:135], v[160:163]\n"
"v_accvgpr_read_b32 v251, a211\n"
"v_mul_f32_dpp v57, v250, v57 row_newbcast:1 row_mask:0xf bank_mask:0xf\n"
"v_mfma_f32_16x16x32_bf16 v[160:163], v[104:107], a[136:139], v[160:163]\n"
"ds_read_b64_tr_b16 v[182:183], v230 offset:2048\n"
"v_mul_f32_dpp v58, v250, v58 row_newbcast:2 row_mask:0xf bank_mask:0xf\n"
"v_mfma_f32_16x16x32_bf16 v[160:163], v[108:111], a[140:143], v[160:163]\n"
"ds_read_b64_tr_b16 v[180:181], v230 offset:0\n"
"v_mul_f32_dpp v31, v250, v31 row_newbcast:3 row_mask:0xf bank_mask:0xf\n"
"v_mfma_f32_16x16x32_bf16 v[160:163], v[112:115], a[176:179], v[160:163]\n"
"ds_read_b64_tr_b16 v[210:211], v230 offset:18432\n"
"v_mov_b32_dpp v252, v252 row_ror:8 row_mask:0x4 bank_mask:0xf\n"
"v_mfma_f32_16x16x32_bf16 v[160:163], v[116:119], a[180:183], v[160:163]\n"
"ds_read_b64_tr_b16 v[220:221], v234 offset:24576\n"
"v_mul_f32_dpp v26, v250, v26 row_newbcast:2 row_mask:0xf bank_mask:0xf\n"
"v_mfma_f32_16x16x32_bf16 v[160:163], v[120:123], a[184:187], v[160:163]\n"
"ds_read_b64_tr_b16 a[238:239], v237 offset:10248\n"
"v_mul_f32_dpp v30, v250, v30 row_newbcast:2 row_mask:0xf bank_mask:0xf\n"
"v_mfma_f32_16x16x32_bf16 v[160:163], v[224:227], a[188:191], v[160:163]\n"
"ds_read_b64_tr_b16 v[216:217], v230 offset:24576\n"
"v_mul_f32_dpp v28, v250, v28 row_newbcast:0 row_mask:0xf bank_mask:0xf\n"
"v_mfma_f32_16x16x32_bf16 v[176:179], v[224:227], a[252:255], v[196:199]\n"
"v_mov_b32_dpp v252, v252 row_ror:4 row_mask:0x8 bank_mask:0xf\n"
"v_mul_f32_dpp v61, v250, v61 row_newbcast:1 row_mask:0xf bank_mask:0xf\n"
"v_mfma_f32_16x16x32_bf16 v[176:179], v[120:123], a[248:251], v[176:179]\n"
"ds_read_b64_tr_b16 v[226:227], v235 offset:26624\n"
"v_mul_f32_dpp v59, v250, v59 row_newbcast:3 row_mask:0xf bank_mask:0xf\n"
"v_mfma_f32_16x16x32_bf16 v[176:179], v[116:119], a[244:247], v[176:179]\n"
"v_mul_f32_dpp v25, v250, v25 row_newbcast:1 row_mask:0xf bank_mask:0xf\n"
"v_mul_f32_dpp v29, v250, v29 row_newbcast:1 row_mask:0xf bank_mask:0xf\n"
"v_mfma_f32_16x16x32_bf16 v[176:179], v[112:115], a[240:243], v[176:179]\n"
"ds_read_b64_tr_b16 v[114:115], v231 offset:18432\n"
"v_mul_f32_dpp v19, v246, v19 row_newbcast:3 row_mask:0xf bank_mask:0xf\n"
"v_mfma_f32_16x16x32_bf16 v[176:179], v[108:111], a[204:207], v[176:179]\n"
"ds_read_b64_tr_b16 a[236:237], v237 offset:10240\n"
"v_mul_f32_dpp v27, v250, v27 row_newbcast:3 row_mask:0xf bank_mask:0xf\n"
"v_mfma_f32_16x16x32_bf16 v[176:179], v[104:107], a[200:203], v[176:179]\n"
"v_mul_f32_dpp v24, v250, v24 row_newbcast:0 row_mask:0xf bank_mask:0xf\n"
"v_fmac_f32_dpp v25, v252, v255 row_newbcast:1 row_mask:0xf bank_mask:0xf\n"
"v_mfma_f32_16x16x32_bf16 v[176:179], v[100:103], a[196:199], v[176:179]\n"
"ds_read_b64_tr_b16 a[234:235], v236 offset:10248\n"
"v_fmac_f32_dpp v27, v252, v251 row_newbcast:3 row_mask:0xf bank_mask:0xf\n"
"v_mfma_f32_16x16x32_bf16 v[176:179], v[96:99], a[192:195], v[176:179]\n"
"s_waitcnt lgkmcnt(4)\n"
"ds_read_b64_tr_b16 v[104:105], v231 offset:8192\n"
"ds_read_b64_tr_b16 v[106:107], v231 offset:10240\n"
"v_accvgpr_read_b32 v251, a215\n"
"v_mul_f32_dpp v55, v247, v55 row_newbcast:3 row_mask:0xf bank_mask:0xf\n"
"v_mul_f32_dpp v56, v250, v56 row_newbcast:0 row_mask:0xf bank_mask:0xf\n"
"v_mul_f32_dpp v62, v250, v62 row_newbcast:2 row_mask:0xf bank_mask:0xf\n"
"v_mfma_f32_16x16x32_bf16 v[132:135], v[180:183], a[96:99], v[196:199]\n"
"v_accvgpr_read_b32 v255, a213\n"
"v_fmac_f32_dpp v31, v252, v251 row_newbcast:3 row_mask:0xf bank_mask:0xf\n"
"v_mfma_f32_16x16x32_bf16 v[132:135], v[184:187], a[100:103], v[132:135]\n"
"ds_read_b64_tr_b16 v[108:109], v235 offset:8192\n"
"ds_read_b64_tr_b16 v[116:117], v235 offset:16384\n"
"v_mfma_f32_16x16x32_bf16 v[132:135], v[188:191], a[104:107], v[132:135]\n"
"ds_read_b64_tr_b16 v[102:103], v235 offset:2048\n"
"v_fmac_f32_dpp v26, v252, v253 row_newbcast:2 row_mask:0xf bank_mask:0xf\n"
"v_mfma_f32_16x16x32_bf16 v[132:135], v[192:195], a[108:111], v[132:135]\n"
"v_accvgpr_read_b32 v253, a214\n"
"v_mul_f32_dpp v60, v250, v60 row_newbcast:0 row_mask:0xf bank_mask:0xf\n"
"v_mfma_f32_16x16x32_bf16 v[132:135], v[208:211], a[144:147], v[132:135]\n"
"s_waitcnt vmcnt(0)\n"
"ds_read_b64_tr_b16 v[96:97], v231 offset:0\n"
"v_fmac_f32_dpp v24, v252, v254 row_newbcast:0 row_mask:0xf bank_mask:0xf\n"
"v_mfma_f32_16x16x32_bf16 v[132:135], v[212:215], a[148:151], v[132:135]\n"
"v_subrev_u32 v200, s21, v200\n"
"v_accvgpr_read_b32 v254, a212\n"
"v_mfma_f32_16x16x32_bf16 v[132:135], v[216:219], a[152:155], v[132:135]\n"
"v_cmp_lt_u32 vcc, s69, v200\n"
"v_accvgpr_read_b32 v251, a227\n"
"v_mfma_f32_16x16x32_bf16 v[132:135], v[220:223], a[156:159], v[132:135]\n"
"ds_read_b64_tr_b16 a[216:217], v236 offset:8192\n"
"ds_read_b64_tr_b16 v[118:119], v235 offset:18432\n"
"v_fmac_f32_dpp v28, v252, v254 row_newbcast:0 row_mask:0xf bank_mask:0xf\n"
"v_fmac_f32_dpp v29, v252, v255 row_newbcast:1 row_mask:0xf bank_mask:0xf\n"
"v_fmac_f32_dpp v30, v252, v253 row_newbcast:2 row_mask:0xf bank_mask:0xf\n"
"v_cvt_pk_bf16_f32 v24, v24, v25\n"
"v_cvt_pk_bf16_f32 v25, v26, v27\n"
"v_cvt_pk_bf16_f32 v26, v28, v29\n"
"v_cvt_pk_bf16_f32 v27, v30, v31\n"
"v_accvgpr_read_b32 v255, a225\n"
"v_accvgpr_read_b32 v253, a226\n"
"v_fmac_f32_dpp v59, v252, v251 row_newbcast:3 row_mask:0xf bank_mask:0xf\n"
"s_cbranch_vccnz LGV_%=\nLGVK_%=:\n"
"v_mfma_f32_16x16x32_bf16 v[148:151], v[220:223], a[172:175], v[196:199]\n"
"v_accvgpr_read_b32 v254, a224\n"
"v_fmac_f32_dpp v58, v252, v253 row_newbcast:2 row_mask:0xf bank_mask:0xf\n"
"v_mfma_f32_16x16x32_bf16 v[148:151], v[216:219], a[168:171], v[148:151]\n"
"v_add_u32_dpp v200, v200, v200 quad_perm:[1,0,3,2] row_mask:0xf bank_mask:0xf\n"
"v_accvgpr_read_b32 v253, a230\n"
"v_mfma_f32_16x16x32_bf16 v[148:151], v[212:215], a[164:167], v[148:151]\n"
"v_fmac_f32_dpp v56, v252, v254 row_newbcast:0 row_mask:0xf bank_mask:0xf\n"
"v_accvgpr_read_b32 v251, a231\n"
"v_mfma_f32_16x16x32_bf16 v[148:151], v[208:211], a[160:163], v[148:151]\n"
"v_add_u32_dpp v200, v200, v200 quad_perm:[2,3,0,1] row_mask:0xf bank_mask:0xf\n"
"ds_write_b128 v243, v[24:27] offset:0\n"
"v_fmac_f32_dpp v57, v252, v255 row_newbcast:1 row_mask:0xf bank_mask:0xf\n"
"v_accvgpr_read_b32 v254, a228\n"
"s_nop 1\n"
"v_fmac_f32_dpp v60, v252, v254 row_newbcast:0 row_mask:0xf bank_mask:0xf\n"
"v_fmac_f32_dpp v62, v252, v253 row_newbcast:2 row_mask:0xf bank_mask:0xf\n"
"v_fmac_f32_dpp v63, v252, v251 row_newbcast:3 row_mask:0xf bank_mask:0xf\n"
"v_cvt_pk_bf16_f32 v56, v56, v57\n"
"v_cvt_pk_bf16_f32 v57, v58, v59\n"
"v_cvt_pk_bf16_f32 v59, v62, v63\n"
"v_mfma_f32_16x16x32_bf16 v[148:151], v[192:195], a[124:127], v[148:151]\n"
"ds_read_b64_tr_b16 v[100:101], v235 offset:0\n"
"v_accvgpr_read_b32 v255, a229\n"
"v_mfma_f32_16x16x32_bf16 v[148:151], v[188:191], a[120:123], v[148:151]\n"
"v_cmp_lt_u32 vcc, 1, v200\n"
"v_fmac_f32_dpp v61, v252, v255 row_newbcast:1 row_mask:0xf bank_mask:0xf\n"
"v_mfma_f32_16x16x32_bf16 v[148:151], v[184:187], a[116:119], v[148:151]\n"
"ds_read_b64_tr_b16 v[98:99], v231 offset:2048\n"
"v_cvt_pk_bf16_f32 v58, v60, v61\n"
"v_mfma_f32_16x16x32_bf16 v[148:151], v[180:183], a[112:115], v[148:151]\n"
"ds_write_b128 v243, v[56:59] offset:2048\n"
"v_mul_f32_dpp v22, v246, v22 row_newbcast:2 row_mask:0xf bank_mask:0xf\n"
"v_mul_f32_dpp v48, v247, v48 row_newbcast:0 row_mask:0xf bank_mask:0xf\n"
"v_mul_f32_dpp v49, v247, v49 row_newbcast:1 row_mask:0xf bank_mask:0xf\n"
"v_mul_f32_dpp v87, v206, v87 row_newbcast:3 row_mask:0xf bank_mask:0xf\n"
"v_mfma_f32_16x16x32_bf16 v[164:167], v[180:183], a[128:131], v[196:199]\n"
"s_mov_b32 s47, 0\n"
"ds_read_b64_tr_b16 v[110:111], v235 offset:10240\n"
"v_bfe_u32 v254, v242, 4, 4\n"
"v_mfma_f32_16x16x32_bf16 v[164:167], v[184:187], a[132:135], v[164:167]\n"
"s_mov_b32 s89, vcc_lo\n"
"v_cvt_pk_bf16_f32 v16, v16, v17\n"
"v_mul_f32_dpp v81, v206, v81 row_newbcast:1 row_mask:0xf bank_mask:0xf\n"
"v_mfma_f32_16x16x32_bf16 v[164:167], v[188:191], a[136:139], v[164:167]\n"
"s_lshr_b32 s90, s89, s93\n"
"ds_read_b64_tr_b16 v[112:113], v231 offset:16384\n"
"v_xor_b32 v249, s75, v249\n"
"v_mfma_f32_16x16x32_bf16 v[164:167], v[192:195], a[140:143], v[164:167]\n"
"s_and_b32 s90, s90, 1\n"
"ds_read_b64_tr_b16 v[122:123], v231 offset:26624\n"
"v_mul_f32_dpp v51, v247, v51 row_newbcast:3 row_mask:0xf bank_mask:0xf\n"
"v_mfma_f32_16x16x32_bf16 v[164:167], v[208:211], a[176:179], v[164:167]\n"
"s_lshl_b32 s16, s61, 7\n"
"v_cvt_pk_bf16_f32 v17, v18, v19\n"
"v_cvt_pk_bf16_f32 v19, v22, v23\n"
"v_mfma_f32_16x16x32_bf16 v[164:167], v[212:215], a[180:183], v[164:167]\n"
"s_add_u32 s90, s90, s16\n"
"ds_read_b64_tr_b16 v[120:121], v231 offset:24576\n"
"ds_read_b64_tr_b16 v[224:225], v235 offset:24576\n"
"v_mfma_f32_16x16x32_bf16 v[164:167], v[216:219], a[184:187], v[164:167]\n"
"s_waitcnt vmcnt(63) lgkmcnt(5)\n"
"s_lshl_b32 s90, s90, 13\n"
"v_mul_f32_dpp v20, v246, v20 row_newbcast:0 row_mask:0xf bank_mask:0xf\n"
"v_mfma_f32_16x16x32_bf16 v[164:167], v[220:223], a[188:191], v[164:167]\n"
"s_barrier\n"
"s_add_u32 s16, s20, s90\n"
"v_mul_f32_dpp v50, v247, v50 row_newbcast:2 row_mask:0xf bank_mask:0xf\n"
"v_mul_f32_dpp v52, v247, v52 row_newbcast:0 row_mask:0xf bank_mask:0xf\n"
"v_cvt_pk_bf16_f32 v48, v48, v49\n"
"v_cvt_pk_bf16_f32 v49, v50, v51\n"
"v_mul_f32_dpp v80, v206, v80 row_newbcast:0 row_mask:0xf bank_mask:0xf\n"
"v_mfma_f32_16x16x32_bf16 v[136:139], v[96:99], a[96:99], v[196:199]\n"
"s_add_u32 s17, s16, 4096\n"
"ds_read_b64_tr_b16 v[180:181], v228 offset:32768\n"
"v_mul_f32_dpp v84, v206, v84 row_newbcast:0 row_mask:0xf bank_mask:0xf\n"
"v_mfma_f32_16x16x32_bf16 v[136:139], v[100:103], a[100:103], v[136:139]\n"
"s_mov_b32 s72, s61\n"
"ds_read_b64_tr_b16 v[182:183], v228 offset:34816\n"
"v_mul_f32_dpp v21, v246, v21 row_newbcast:1 row_mask:0xf bank_mask:0xf\n"
"v_mfma_f32_16x16x32_bf16 v[136:139], v[104:107], a[104:107], v[136:139]\n"
"s_waitcnt lgkmcnt(5)\n"
"s_mov_b32 s21, s60\n"
"ds_read_b64_tr_b16 a[208:209], v236 offset:12288\n"
"v_mul_f32_dpp v82, v206, v82 row_newbcast:2 row_mask:0xf bank_mask:0xf\n"
"v_mfma_f32_16x16x32_bf16 v[136:139], v[108:111], a[108:111], v[136:139]\n"
"s_mov_b32 s18, s77\n"
"v_cvt_pk_bf16_f32 v80, v80, v81\n"
"v_cvt_pk_bf16_f32 v81, v82, v83\n"
"v_mfma_f32_16x16x32_bf16 v[136:139], v[112:115], a[144:147], v[136:139]\n"
"s_mov_b32 s19, s77\n"
"v_mul_f32_dpp v85, v206, v85 row_newbcast:1 row_mask:0xf bank_mask:0xf\n"
"v_cvt_pk_bf16_f32 v83, v86, v87\n"
"v_mfma_f32_16x16x32_bf16 v[136:139], v[116:119], a[148:151], v[136:139]\n"
"s_waitcnt lgkmcnt(3)\n"
"s_mov_b32 s69, -1\n"
"ds_read_b64_tr_b16 v[218:219], v228 offset:59392\n"
"v_cvt_pk_bf16_f32 v18, v20, v21\n"
"v_mfma_f32_16x16x32_bf16 v[136:139], v[120:123], a[152:155], v[136:139]\n"
"s_mul_i32 s88, s61, 0x10011111\n"
"v_xor_b32 v248, s75, v248\n"
"v_cvt_pk_bf16_f32 v82, v84, v85\n"
"v_mfma_f32_16x16x32_bf16 v[136:139], v[224:227], a[156:159], v[136:139]\n"
"s_add_u32 s88, s88, 484675\n"
"ds_write_b128 v248, v[16:19] offset:49152\n"
"v_mul_f32_dpp v53, v247, v53 row_newbcast:1 row_mask:0xf bank_mask:0xf\n"
"v_mul_f32_dpp v54, v247, v54 row_newbcast:2 row_mask:0xf bank_mask:0xf\n"
"v_cvt_pk_bf16_f32 v50, v52, v53\n"
"v_cvt_pk_bf16_f32 v51, v54, v55\n"
"v_mfma_f32_16x16x32_bf16 v[152:155], v[224:227], a[172:175], v[196:199]\n"
"s_and_b32 s88, s88, 0x70077777\n"
"ds_read_b64_tr_b16 a[8:9], v249 offset:49152\n"
"ds_read_b64_tr_b16 a[10:11], v249 offset:49160\n"
"v_mfma_f32_16x16x32_bf16 v[152:155], v[120:123], a[168:171], v[152:155]\n"
"s_or_b32 s88, s88, 0x9800000\n"
"ds_write_b128 v248, v[48:51] offset:50176\n"
"ds_write_b128 v248, v[80:83] offset:51200\n"
"v_mfma_f32_16x16x32_bf16 v[152:155], v[116:119], a[164:167], v[152:155]\n"
"s_mov_b32 s84, s6\n"
"ds_read_b64_tr_b16 a[24:25], v249 offset:50176\n"
"ds_read_b64_tr_b16 a[26:27], v249 offset:50184\n"
"ds_read_b64_tr_b16 a[40:41], v249 offset:51200\n"
"ds_read_b64_tr_b16 a[42:43], v249 offset:51208\n"
"v_mfma_f32_16x16x32_bf16 v[152:155], v[112:115], a[160:163], v[152:155]\n"
"s_and_b32 s85, s7, 0xffff\n"
"ds_read_b64_tr_b16 v[188:189], v228 offset:40960\n"
"v_xor_b32 v248, s75, v248\n"
"v_mfma_f32_16x16x32_bf16 v[152:155], v[108:111], a[124:127], v[152:155]\n"
"s_mov_b32 s86, 128\n"
"ds_read_b64_tr_b16 v[184:185], v232 offset:32768\n"
"ds_read_b64_tr_b16 v[214:215], v232 offset:51200\n"
"v_mfma_f32_16x16x32_bf16 v[152:155], v[104:107], a[120:123], v[152:155]\n"
"s_mov_b32 s87, 131072\n"
"ds_read_b64_tr_b16 a[214:215], v237 offset:12296\n"
"ds_read_b64_tr_b16 v[190:191], v228 offset:43008\n"
"v_mfma_f32_16x16x32_bf16 v[152:155], v[100:103], a[116:119], v[152:155]\n"
"ds_read_b64_tr_b16 a[224:225], v236 offset:14336\n"
"ds_read_b64_tr_b16 v[212:213], v232 offset:49152\n"
"v_mfma_f32_16x16x32_bf16 v[152:155], v[96:99], a[112:115], v[152:155]\n"
"ds_read_b64_tr_b16 v[186:187], v232 offset:34816\n"
"v_mov_b64 v[84:85], 0\n"
"v_mfma_f32_16x16x32_bf16 v[168:171], v[96:99], a[128:131], v[196:199]\n"
"ds_read_b64_tr_b16 v[192:193], v232 offset:40960\n"
"ds_read_b64_tr_b16 v[222:223], v232 offset:59392\n"
"v_mfma_f32_16x16x32_bf16 v[168:171], v[100:103], a[132:135], v[168:171]\n"
"ds_read_b64_tr_b16 v[194:195], v232 offset:43008\n"
"v_bfe_u32 v255, v242, 8, 1\n"
"v_mfma_f32_16x16x32_bf16 v[168:171], v[104:107], a[136:139], v[168:171]\n"
"ds_read_b64_tr_b16 v[208:209], v228 offset:49152\n"
"ds_read_b64_tr_b16 v[210:211], v228 offset:51200\n"
"v_mfma_f32_16x16x32_bf16 v[168:171], v[108:111], a[140:143], v[168:171]\n"
"s_waitcnt lgkmcnt(12)\n"
"ds_read_b64_tr_b16 a[230:231], v237 offset:14344\n"
"ds_read_b64_tr_b16 v[216:217], v228 offset:57344\n"
"v_mfma_f32_16x16x32_bf16 v[168:171], v[112:115], a[176:179], v[168:171]\n"
"ds_read_b64_tr_b16 v[220:221], v232 offset:57344\n"
"v_xor_b32 v249, s75, v249\n"
"v_mfma_f32_16x16x32_bf16 v[168:171], v[116:119], a[180:183], v[168:171]\n"
"ds_read_b64_tr_b16 a[222:223], v237 offset:8200\n"
"ds_read_b64_tr_b16 a[226:227], v236 offset:14344\n"
"v_mfma_f32_16x16x32_bf16 v[168:171], v[120:123], a[184:187], v[168:171]\n"
"s_add_u32 s41, s70, 327680\n"
"s_add_u32 m0, s71, 32768\n"
"ds_read_b64_tr_b16 v[110:111], v233 offset:43008\n"
"v_mfma_f32_16x16x32_bf16 v[168:171], v[224:227], a[188:191], v[168:171]\n"
"buffer_load_dwordx4 v240, s[28:31], s41 offen lds\n"
"v_mfma_f32_16x16x32_bf16 v[124:127], v[180:183], a[48:51], v[124:127]\n"
"s_waitcnt lgkmcnt(9)\n"
"v_mul_u32_u24 v254, 2560, v254\n"
"v_mfma_f32_16x16x32_bf16 v[124:127], v[184:187], a[52:55], v[124:127]\n"
"v_mov_b64 v[32:33], s[22:23]\n"
"v_mov_b64 v[44:45], s[22:23]\n"
"v_mfma_f32_16x16x32_bf16 v[124:127], v[188:191], a[56:59], v[124:127]\n"
"s_waitcnt lgkmcnt(6)\n"
"s_add_u32 s40, s41, s52\n"
"ds_read_b64_tr_b16 a[212:213], v237 offset:12288\n"
"ds_read_b64_tr_b16 v[102:103], v233 offset:34816\n"
"v_mfma_f32_16x16x32_bf16 v[124:127], v[192:195], a[60:63], v[124:127]\n"
"buffer_load_dwordx4 v240, s[28:31], s40 offen offset:1024 lds\n"
"v_mfma_f32_16x16x32_bf16 v[124:127], v[208:211], a[0:3], v[124:127]\n"
"v_mov_b64 v[38:39], s[22:23]\n"
"v_mfma_f32_16x16x32_bf16 v[124:127], v[212:215], a[4:7], v[124:127]\n"
"s_waitcnt lgkmcnt(3)\n"
"v_mov_b64 v[244:245], 0\n"
"v_mfma_f32_16x16x32_bf16 v[124:127], v[216:219], a[8:11], v[124:127]\n"
"ds_read_b64_tr_b16 v[98:99], v229 offset:34816\n"
"ds_read_b64_tr_b16 v[116:117], v233 offset:49152\n"
"v_mfma_f32_16x16x32_bf16 v[124:127], v[220:223], a[12:15], v[124:127]\n"
"s_add_u32 s40, s41, s53\n"
"ds_read_b64_tr_b16 v[122:123], v229 offset:59392\n"
"ds_read_b64_tr_b16 v[224:225], v233 offset:57344\n"
"v_mfma_f32_16x16x32_bf16 v[140:143], v[220:223], a[28:31], v[140:143]\n"
"buffer_load_dwordx4 v240, s[28:31], s40 offen offset:2048 lds\n"
"v_mfma_f32_16x16x32_bf16 v[140:143], v[216:219], a[24:27], v[140:143]\n"
"v_mov_b64 v[16:17], s[22:23]\n"
"v_mov_b64 v[36:37], s[22:23]\n"
"v_mfma_f32_16x16x32_bf16 v[140:143], v[212:215], a[20:23], v[140:143]\n"
"v_mov_b64 v[246:247], 0\n"
"v_subrev_f32 v124, s22, v124\n"
"v_mfma_f32_16x16x32_bf16 v[140:143], v[208:211], a[16:19], v[140:143]\n"
"ds_read_b64_tr_b16 a[210:211], v236 offset:12296\n"
"ds_read_b64_tr_b16 v[106:107], v229 offset:43008\n"
"v_mfma_f32_16x16x32_bf16 v[140:143], v[192:195], a[76:79], v[140:143]\n"
"ds_read_b64_tr_b16 v[112:113], v229 offset:49152\n"
"v_xor_b32 v228, s75, v228\n"
"v_mfma_f32_16x16x32_bf16 v[140:143], v[188:191], a[72:75], v[140:143]\n"
"ds_read_b64_tr_b16 v[104:105], v229 offset:40960\n"
"v_mov_b64 v[26:27], s[22:23]\n"
"v_mfma_f32_16x16x32_bf16 v[140:143], v[184:187], a[68:71], v[140:143]\n"
"s_add_u32 s40, s41, s54\n"
"ds_read_b64_tr_b16 a[228:229], v237 offset:14336\n"
"ds_read_b64_tr_b16 v[118:119], v233 offset:51200\n"
"v_mfma_f32_16x16x32_bf16 v[140:143], v[180:183], a[64:67], v[140:143]\n"
"buffer_load_dwordx4 v240, s[28:31], s40 offen offset:3072 lds\n"
"v_mfma_f32_16x16x32_bf16 v[156:159], v[180:183], a[80:83], v[156:159]\n"
"s_add_u32 s41, s70, 491520\n"
"v_mov_b64 v[86:87], 0\n"
"v_mfma_f32_16x16x32_bf16 v[156:159], v[184:187], a[84:87], v[156:159]\n"
"s_add_u32 m0, s71, 49152\n"
"ds_read_b64_tr_b16 v[114:115], v229 offset:51200\n"
"ds_read_b64_tr_b16 v[226:227], v233 offset:59392\n"
"v_mfma_f32_16x16x32_bf16 v[156:159], v[188:191], a[88:91], v[156:159]\n"
"buffer_load_dwordx4 v240, s[28:31], s41 offen lds\n"
"v_mfma_f32_16x16x32_bf16 v[156:159], v[192:195], a[92:95], v[156:159]\n"
"v_and_b32 v243, 0x3f0, v242\n"
"v_subrev_f32 v142, s22, v142\n"
"v_mfma_f32_16x16x32_bf16 v[156:159], v[208:211], a[32:35], v[156:159]\n"
"v_mov_b64 v[6:7], s[22:23]\n"
"v_subrev_f32 v126, s22, v126\n"
"v_mfma_f32_16x16x32_bf16 v[156:159], v[212:215], a[36:39], v[156:159]\n"
"ds_read_b64_tr_b16 a[218:219], v236 offset:8200\n"
"ds_read_b64_tr_b16 v[96:97], v229 offset:32768\n"
"v_mfma_f32_16x16x32_bf16 v[156:159], v[216:219], a[40:43], v[156:159]\n"
"s_add_u32 s40, s41, s52\n"
"ds_read_b64_tr_b16 v[100:101], v233 offset:32768\n"
"ds_read_b64_tr_b16 v[108:109], v233 offset:40960\n"
"v_mfma_f32_16x16x32_bf16 v[156:159], v[220:223], a[44:47], v[156:159]\n"
"s_waitcnt lgkmcnt(6)\n"
"buffer_load_dwordx4 v240, s[28:31], s40 offen offset:1024 lds\n"
"v_mfma_f32_16x16x32_bf16 v[172:175], v[220:223], a[228:231], v[172:175]\n"
"v_subrev_f32 v143, s22, v143\n"
"v_xor_b32 v233, s75, v233\n"
"v_mfma_f32_16x16x32_bf16 v[172:175], v[216:219], a[224:227], v[172:175]\n"
"v_lshl_add_u32 v254, v255, 5, v254\n"
"v_subrev_f32 v125, s22, v125\n"
"v_mfma_f32_16x16x32_bf16 v[172:175], v[212:215], a[212:215], v[172:175]\n"
"s_add_u32 s40, s41, s53\n"
"ds_read_b64_tr_b16 v[120:121], v229 offset:57344\n"
"v_mfma_f32_16x16x32_bf16 v[172:175], v[208:211], a[208:211], v[172:175]\n"
"buffer_load_dwordx4 v240, s[28:31], s40 offen offset:2048 lds\n"
"v_mfma_f32_16x16x32_bf16 v[172:175], v[192:195], a[236:239], v[172:175]\n"
"v_add_u32 v236, s94, v243\n"
"v_mfma_f32_16x16x32_bf16 v[172:175], v[188:191], a[232:235], v[172:175]\n"
"v_bfe_u32 v255, v242, 9, 1\n"
"v_subrev_f32 v141, s22, v141\n"
"v_mfma_f32_16x16x32_bf16 v[172:175], v[184:187], a[220:223], v[172:175]\n"
"s_waitcnt lgkmcnt(1)\n"
"ds_read_b64_tr_b16 v[184:185], v234 offset:32768\n"
"v_mov_b64 v[54:55], s[22:23]\n"
"v_mfma_f32_16x16x32_bf16 v[172:175], v[180:183], a[216:219], v[172:175]\n"
"s_add_u32 s40, s41, s54\n"
"ds_read_b64_tr_b16 v[210:211], v230 offset:51200\n"
"v_mfma_f32_16x16x32_bf16 v[128:131], v[96:99], a[48:51], v[128:131]\n"
"buffer_load_dwordx4 v240, s[28:31], s40 offen offset:3072 lds\n"
"v_mfma_f32_16x16x32_bf16 v[128:131], v[100:103], a[52:55], v[128:131]\n"
"s_lshl_b32 s62, s70, 9\n"
"v_mov_b64 v[30:31], s[22:23]\n"
"v_xor_b32 v232, s75, v232\n"
"v_mfma_f32_16x16x32_bf16 v[128:131], v[104:107], a[56:59], v[128:131]\n"
"s_and_b32 s70, s88, 15\n"
"v_cvt_pk_bf16_f32 v124, v124, v125\n"
"v_mfma_f32_16x16x32_bf16 v[128:131], v[108:111], a[60:63], v[128:131]\n"
"s_lshr_b32 s88, s88, 4\n"
"ds_read_b64_tr_b16 v[218:219], v230 offset:59392\n"
"v_subrev_f32 v127, s22, v127\n"
"v_mfma_f32_16x16x32_bf16 v[128:131], v[112:115], a[0:3], v[128:131]\n"
"s_lshl_b32 s70, s70, 7\n"
"ds_read_b64_tr_b16 v[194:195], v234 offset:43008\n"
"v_bfe_u32 v237, v242, 4, 3\n"
"v_mfma_f32_16x16x32_bf16 v[128:131], v[116:119], a[4:7], v[128:131]\n"
"s_waitcnt lgkmcnt(4)\n"
"s_xor_b32 s71, s71, s75\n"
"v_lshl_add_u32 v243, s44, 2, v243\n"
"v_mov_b64 v[12:13], s[22:23]\n"
"v_mfma_f32_16x16x32_bf16 v[128:131], v[120:123], a[8:11], v[128:131]\n"
"v_lshlrev_b32_e64 v201, 2, s72\n"
"ds_read_b64_tr_b16 v[214:215], v234 offset:51200\n"
"v_cvt_pk_bf16_f32 v125, v126, v127\n"
"v_mfma_f32_16x16x32_bf16 v[128:131], v[224:227], a[12:15], v[128:131]\n"
"global_load_dword v200, v201, s[6:7] sc1\n"
"v_mfma_f32_16x16x32_bf16 v[144:147], v[224:227], a[28:31], v[144:147]\n"
"v_subrev_f32 v157, s22, v157\n"
"v_subrev_f32 v174, s22, v174\n"
"v_mfma_f32_16x16x32_bf16 v[144:147], v[120:123], a[24:27], v[144:147]\n"
"v_mov_b64 v[52:53], s[22:23]\n"
"v_subrev_f32 v156, s22, v156\n"
"v_mfma_f32_16x16x32_bf16 v[144:147], v[116:119], a[20:23], v[144:147]\n"
"ds_read_b64_tr_b16 v[188:189], v230 offset:40960\n"
"v_subrev_f32 v172, s22, v172\n"
"v_mfma_f32_16x16x32_bf16 v[144:147], v[112:115], a[16:19], v[144:147]\n"
"v_subrev_f32 v140, s22, v140\n"
"v_mfma_f32_16x16x32_bf16 v[144:147], v[108:111], a[76:79], v[144:147]\n"
"ds_read_b64_tr_b16 v[180:181], v230 offset:32768\n"
"v_subrev_f32 v130, s22, v130\n"
"v_mfma_f32_16x16x32_bf16 v[144:147], v[104:107], a[72:75], v[144:147]\n"
"v_lshl_add_u32 v254, v255, 4, v254\n"
"v_mov_b64 v[22:23], s[22:23]\n"
"v_mfma_f32_16x16x32_bf16 v[144:147], v[100:103], a[68:71], v[144:147]\n"
"ds_read_b64_tr_b16 v[192:193], v234 offset:40960\n"
"v_subrev_f32 v128, s22, v128\n"
"v_mfma_f32_16x16x32_bf16 v[144:147], v[96:99], a[64:67], v[144:147]\n"
"v_subrev_f32 v129, s22, v129\n"
"v_cvt_pk_bf16_f32 v126, v128, v129\n"
"v_mfma_f32_16x16x32_bf16 v[160:163], v[96:99], a[80:83], v[160:163]\n"
"ds_read_b64_tr_b16 v[186:187], v234 offset:34816\n"
"v_cvt_pk_bf16_f32 v156, v156, v157\n"
"v_mfma_f32_16x16x32_bf16 v[160:163], v[100:103], a[84:87], v[160:163]\n"
"ds_read_b64_tr_b16 v[190:191], v230 offset:43008\n"
"v_subrev_f32 v131, s22, v131\n"
"v_mfma_f32_16x16x32_bf16 v[160:163], v[104:107], a[88:91], v[160:163]\n"
"v_cvt_pk_bf16_f32 v127, v130, v131\n"
"v_subrev_f32 v146, s22, v146\n"
"v_mfma_f32_16x16x32_bf16 v[160:163], v[108:111], a[92:95], v[160:163]\n"
"ds_read_b64_tr_b16 v[220:221], v234 offset:57344\n"
"v_cvt_pk_bf16_f32 v140, v140, v141\n"
"v_mfma_f32_16x16x32_bf16 v[160:163], v[112:115], a[32:35], v[160:163]\n"
"v_lshl_add_u32 v248, v242, 8, s93\n"
"v_subrev_f32 v145, s22, v145\n"
"v_mfma_f32_16x16x32_bf16 v[160:163], v[116:119], a[36:39], v[160:163]\n"
"ds_read_b64_tr_b16 v[182:183], v230 offset:34816\n"
"v_cvt_pk_bf16_f32 v141, v142, v143\n"
"v_mfma_f32_16x16x32_bf16 v[160:163], v[120:123], a[40:43], v[160:163]\n"
"ds_read_b64_tr_b16 v[208:209], v230 offset:49152\n"
"v_subrev_f32 v159, s22, v159\n"
"v_mfma_f32_16x16x32_bf16 v[160:163], v[224:227], a[44:47], v[160:163]\n"
"buffer_store_dwordx4 v[124:127], v243, s[32:35], s16 offen sc1\n"
"v_mov_b64 v[24:25], s[22:23]\n"
"v_mfma_f32_16x16x32_bf16 v[176:179], v[224:227], a[228:231], v[176:179]\n"
"ds_read_b64_tr_b16 v[212:213], v234 offset:49152\n"
"v_mov_b64 v[50:51], s[22:23]\n"
"v_mfma_f32_16x16x32_bf16 v[176:179], v[120:123], a[224:227], v[176:179]\n"
"v_subrev_f32 v158, s22, v158\n"
"v_subrev_f32 v173, s22, v173\n"
"v_mfma_f32_16x16x32_bf16 v[176:179], v[116:119], a[212:215], v[176:179]\n"
"ds_read_b64_tr_b16 v[216:217], v230 offset:57344\n"
"v_mov_b64 v[94:95], 0\n"
"v_mfma_f32_16x16x32_bf16 v[176:179], v[112:115], a[208:211], v[176:179]\n"
"v_cvt_pk_bf16_f32 v172, v172, v173\n"
"v_mfma_f32_16x16x32_bf16 v[176:179], v[108:111], a[236:239], v[176:179]\n"
"ds_read_b64_tr_b16 v[112:113], v231 offset:49152\n"
"v_mov_b64 v[4:5], s[22:23]\n"
"v_mfma_f32_16x16x32_bf16 v[176:179], v[104:107], a[232:235], v[176:179]\n"
"ds_read_b64_tr_b16 v[222:223], v234 offset:59392\n"
"v_subrev_f32 v144, s22, v144\n"
"v_mfma_f32_16x16x32_bf16 v[176:179], v[100:103], a[220:223], v[176:179]\n"
"v_mov_b64 v[40:41], s[22:23]\n"
"v_subrev_f32 v147, s22, v147\n"
"v_mfma_f32_16x16x32_bf16 v[176:179], v[96:99], a[216:219], v[176:179]\n"
"s_waitcnt lgkmcnt(4)\n"
"ds_read_b64_tr_b16 v[224:225], v235 offset:57344\n"
"v_subrev_f32 v175, s22, v175\n"
"v_mfma_f32_16x16x32_bf16 v[132:135], v[180:183], a[48:51], v[132:135]\n"
"ds_read_b64_tr_b16 v[102:103], v235 offset:34816\n"
"v_mov_b32 v241, s60\n"
"v_mfma_f32_16x16x32_bf16 v[132:135], v[184:187], a[52:55], v[132:135]\n"
"ds_read_b64_tr_b16 v[96:97], v231 offset:32768\n"
"v_cvt_pk_bf16_f32 v157, v158, v159\n"
"v_mfma_f32_16x16x32_bf16 v[132:135], v[188:191], a[56:59], v[132:135]\n"
"ds_read_b64_tr_b16 v[114:115], v231 offset:51200\n"
"v_subrev_f32 v160, s22, v160\n"
"v_mfma_f32_16x16x32_bf16 v[132:135], v[192:195], a[60:63], v[132:135]\n"
"v_cvt_pk_bf16_f32 v142, v144, v145\n"
"v_subrev_f32 v161, s22, v161\n"
"v_mfma_f32_16x16x32_bf16 v[132:135], v[208:211], a[0:3], v[132:135]\n"
"s_waitcnt lgkmcnt(5)\n"
"ds_read_b64_tr_b16 v[98:99], v231 offset:34816\n"
"v_mfma_f32_16x16x32_bf16 v[132:135], v[212:215], a[4:7], v[132:135]\n"
"v_cvt_pk_bf16_f32 v143, v146, v147\n"
"v_mfma_f32_16x16x32_bf16 v[132:135], v[216:219], a[8:11], v[132:135]\n"
"s_waitcnt lgkmcnt(5)\n"
"ds_read_b64_tr_b16 v[116:117], v235 offset:49152\n"
"v_mov_b64 v[34:35], s[22:23]\n"
"v_mfma_f32_16x16x32_bf16 v[132:135], v[220:223], a[12:15], v[132:135]\n"
"buffer_store_dwordx4 v[140:143], v243, s[32:35], s16 offen offset:2048 sc1\n"
"ds_read_b64_tr_b16 v[106:107], v231 offset:43008\n"
"v_mfma_f32_16x16x32_bf16 v[148:151], v[220:223], a[28:31], v[148:151]\n"
"v_cvt_pk_bf16_f32 v173, v174, v175\n"
"v_subrev_f32 v176, s22, v176\n"
"v_mfma_f32_16x16x32_bf16 v[148:151], v[216:219], a[24:27], v[148:151]\n"
"ds_read_b64_tr_b16 v[100:101], v235 offset:32768\n"
"v_max_u32 v237, 1, v237\n"
"v_mfma_f32_16x16x32_bf16 v[148:151], v[212:215], a[20:23], v[148:151]\n"
"v_mov_b64 v[14:15], s[22:23]\n"
"v_mov_b64 v[46:47], s[22:23]\n"
"v_mfma_f32_16x16x32_bf16 v[148:151], v[208:211], a[16:19], v[148:151]\n"
"ds_read_b64_tr_b16 v[104:105], v231 offset:40960\n"
"v_mov_b64 v[42:43], s[22:23]\n"
"v_mfma_f32_16x16x32_bf16 v[148:151], v[192:195], a[76:79], v[148:151]\n"
"ds_read_b64_tr_b16 v[120:121], v231 offset:57344\n"
"v_mov_b64 v[2:3], s[22:23]\n"
"v_mfma_f32_16x16x32_bf16 v[148:151], v[188:191], a[72:75], v[148:151]\n"
"v_subrev_f32 v162, s22, v162\n"
"v_subrev_f32 v179, s22, v179\n"
"v_mfma_f32_16x16x32_bf16 v[148:151], v[184:187], a[68:71], v[148:151]\n"
"ds_read_b64_tr_b16 v[122:123], v231 offset:59392\n"
"v_xor_b32 v229, s75, v229\n"
"v_mfma_f32_16x16x32_bf16 v[148:151], v[180:183], a[64:67], v[148:151]\n"
"ds_read_b64_tr_b16 v[110:111], v235 offset:43008\n"
"v_add_u32 v238, s97, v254\n"
"v_mfma_f32_16x16x32_bf16 v[164:167], v[180:183], a[80:83], v[164:167]\n"
"v_add_u32 v252, s98, v254\n"
"v_add_u32 v241, 1, v241\n"
"v_mfma_f32_16x16x32_bf16 v[164:167], v[184:187], a[84:87], v[164:167]\n"
"ds_read_b64_tr_b16 v[108:109], v235 offset:40960\n"
"v_mov_b64 v[10:11], s[22:23]\n"
"v_mfma_f32_16x16x32_bf16 v[164:167], v[188:191], a[88:91], v[164:167]\n"
"v_mov_b64 v[28:29], s[22:23]\n"
"v_subrev_f32 v177, s22, v177\n"
"v_mfma_f32_16x16x32_bf16 v[164:167], v[192:195], a[92:95], v[164:167]\n"
"ds_read_b64_tr_b16 v[118:119], v235 offset:51200\n"
"v_subrev_f32 v132, s22, v132\n"
"v_mfma_f32_16x16x32_bf16 v[164:167], v[208:211], a[32:35], v[164:167]\n"
"v_subrev_f32 v163, s22, v163\n"
"v_subrev_f32 v135, s22, v135\n"
"v_mfma_f32_16x16x32_bf16 v[164:167], v[212:215], a[36:39], v[164:167]\n"
"ds_read_b64_tr_b16 v[226:227], v235 offset:59392\n"
"v_cvt_pk_bf16_f32 v159, v162, v163\n"
"v_mfma_f32_16x16x32_bf16 v[164:167], v[216:219], a[40:43], v[164:167]\n"
"v_xor_b32 v237, s61, v237\n"
"v_mov_b64 v[18:19], s[22:23]\n"
"v_mfma_f32_16x16x32_bf16 v[164:167], v[220:223], a[44:47], v[164:167]\n"
"s_barrier\n"
"s_waitcnt lgkmcnt(3)\n"
"v_mfma_f32_16x16x32_bf16 v[136:139], v[96:99], a[48:51], v[136:139]\n"
"ds_read_b64_tr_b16 v[214:215], v232 offset:18432\n"
"v_mfma_f32_16x16x32_bf16 v[136:139], v[100:103], a[52:55], v[136:139]\n"
"ds_read_b64_tr_b16 v[212:213], v232 offset:16384\n"
"v_cvt_pk_bf16_f32 v158, v160, v161\n"
"v_mfma_f32_16x16x32_bf16 v[136:139], v[104:107], a[56:59], v[136:139]\n"
"s_waitcnt lgkmcnt(4)\n"
"ds_read_b64_tr_b16 v[220:221], v232 offset:24576\n"
"v_subrev_f32 v178, s22, v178\n"
"v_mfma_f32_16x16x32_bf16 v[136:139], v[108:111], a[60:63], v[136:139]\n"
"ds_read_b64_tr_b16 v[180:181], v228 offset:0\n"
"v_mfma_f32_16x16x32_bf16 v[136:139], v[112:115], a[0:3], v[136:139]\n"
"s_waitcnt lgkmcnt(5)\n"
"ds_read_b64_tr_b16 v[194:195], v232 offset:10240\n"
"v_mfma_f32_16x16x32_bf16 v[136:139], v[116:119], a[4:7], v[136:139]\n"
"ds_read_b64_tr_b16 v[188:189], v228 offset:8192\n"
"v_add_u32 v253, s99, v254\n"
"v_mfma_f32_16x16x32_bf16 v[136:139], v[120:123], a[8:11], v[136:139]\n"
"s_waitcnt lgkmcnt(6)\n"
"ds_read_b64_tr_b16 v[184:185], v232 offset:0\n"
"v_subrev_f32 v133, s22, v133\n"
"v_mfma_f32_16x16x32_bf16 v[136:139], v[224:227], a[12:15], v[136:139]\n"
"buffer_store_dwordx4 v[156:159], v243, s[32:35], s17 offen sc1\n"
"ds_read_b64_tr_b16 v[222:223], v232 offset:26624\n"
"v_mfma_f32_16x16x32_bf16 v[152:155], v[224:227], a[28:31], v[152:155]\n"
"ds_read_b64_tr_b16 v[208:209], v228 offset:16384\n"
"v_mfma_f32_16x16x32_bf16 v[152:155], v[120:123], a[24:27], v[152:155]\n"
"ds_read_b64_tr_b16 v[190:191], v228 offset:10240\n"
"v_mfma_f32_16x16x32_bf16 v[152:155], v[116:119], a[20:23], v[152:155]\n"
"ds_read_b64_tr_b16 v[186:187], v232 offset:2048\n"
"v_subrev_f32 v134, s22, v134\n"
"v_mfma_f32_16x16x32_bf16 v[152:155], v[112:115], a[16:19], v[152:155]\n"
"ds_read_b64_tr_b16 v[218:219], v228 offset:26624\n"
"v_mfma_f32_16x16x32_bf16 v[152:155], v[108:111], a[76:79], v[152:155]\n"
"ds_read_b64_tr_b16 v[192:193], v232 offset:8192\n"
"v_mfma_f32_16x16x32_bf16 v[152:155], v[104:107], a[72:75], v[152:155]\n"
"ds_read_b64_tr_b16 v[182:183], v228 offset:2048\n"
"v_xor_b32 v239, 64, v238\n"
"v_mfma_f32_16x16x32_bf16 v[152:155], v[100:103], a[68:71], v[152:155]\n"
"ds_read_b64_tr_b16 v[210:211], v228 offset:18432\n"
"ds_read_b64_tr_b16 v[216:217], v228 offset:24576\n"
"v_mfma_f32_16x16x32_bf16 v[152:155], v[96:99], a[64:67], v[152:155]\n"
"v_mfma_f32_16x16x32_bf16 v[168:171], v[96:99], a[80:83], v[168:171]\n"
"ds_read_b64_tr_b16 v[96:97], v229 offset:0\n"
"ds_read_b64_tr_b16 v[98:99], v229 offset:2048\n"
"v_mfma_f32_16x16x32_bf16 v[168:171], v[100:103], a[84:87], v[168:171]\n"
"ds_read_b64_tr_b16 v[100:101], v233 offset:0\n"
"ds_read_b64_tr_b16 v[102:103], v233 offset:2048\n"
"v_mfma_f32_16x16x32_bf16 v[168:171], v[104:107], a[88:91], v[168:171]\n"
"ds_read_b64_tr_b16 v[104:105], v229 offset:8192\n"
"v_cvt_pk_bf16_f32 v175, v178, v179\n"
"v_mfma_f32_16x16x32_bf16 v[168:171], v[108:111], a[92:95], v[168:171]\n"
"v_mov_b64 v[20:21], s[22:23]\n"
"v_mov_b64 v[90:91], 0\n"
"v_mfma_f32_16x16x32_bf16 v[168:171], v[112:115], a[32:35], v[168:171]\n"
"v_mov_b64 v[88:89], 0\n"
"v_mov_b64 v[92:93], 0\n"
"v_mfma_f32_16x16x32_bf16 v[168:171], v[116:119], a[36:39], v[168:171]\n"
"v_mov_b64 v[48:49], s[22:23]\n"
"v_cvt_pk_bf16_f32 v132, v132, v133\n"
"v_mfma_f32_16x16x32_bf16 v[168:171], v[120:123], a[40:43], v[168:171]\n"
"s_add_u32 s47, s47, 1\n"
"v_mov_b64 v[0:1], s[22:23]\n"
"v_mov_b64 v[8:9], s[22:23]\n"
"v_mfma_f32_16x16x32_bf16 v[168:171], v[224:227], a[44:47], v[168:171]\n"
"s_waitcnt lgkmcnt(5)\n"
"v_lshlrev_b32 v237, 2, v237\n"
"v_cvt_pk_bf16_f32 v174, v176, v177\n"
"LGL_%=:\n"
"v_mfma_f32_16x16x32_bf16 v[124:127], v[180:183], a[96:99], v[196:199]\n"
"ds_read_b64_tr_b16 v[106:107], v229 offset:10240\n"
"v_cvt_pk_bf16_f32 v133, v134, v135\n"
"v_mfma_f32_16x16x32_bf16 v[124:127], v[184:187], a[100:103], v[124:127]\n"
"ds_read_b64_tr_b16 v[108:109], v233 offset:8192\n"
"v_subrev_f32 v136, s22, v136\n"
"v_mfma_f32_16x16x32_bf16 v[124:127], v[188:191], a[104:107], v[124:127]\n"
"ds_read_b64_tr_b16 v[110:111], v233 offset:10240\n"
"v_subrev_f32 v137, s22, v137\n"
"v_mfma_f32_16x16x32_bf16 v[124:127], v[192:195], a[108:111], v[124:127]\n"
"ds_read_b64_tr_b16 v[112:113], v229 offset:16384\n"
"v_subrev_f32 v138, s22, v138\n"
"v_mfma_f32_16x16x32_bf16 v[124:127], v[208:211], a[144:147], v[124:127]\n"
"ds_read_b64_tr_b16 v[114:115], v229 offset:18432\n"
"v_subrev_f32 v139, s22, v139\n"
"v_mfma_f32_16x16x32_bf16 v[124:127], v[212:215], a[148:151], v[124:127]\n"
"s_waitcnt lgkmcnt(7)\n"
"ds_read_b64_tr_b16 v[116:117], v233 offset:16384\n"
"v_cvt_pk_bf16_f32 v134, v136, v137\n"
"v_mfma_f32_16x16x32_bf16 v[124:127], v[216:219], a[152:155], v[124:127]\n"
"ds_read_b64_tr_b16 v[118:119], v233 offset:18432\n"
"v_cvt_pk_bf16_f32 v135, v138, v139\n"
"v_mfma_f32_16x16x32_bf16 v[124:127], v[220:223], a[156:159], v[124:127]\n"
"buffer_store_dwordx4 v[172:175], v243, s[32:35], s17 offen offset:2048 sc1\n"
"v_subrev_f32 v148, s22, v148\n"
"v_mfma_f32_16x16x32_bf16 v[140:143], v[220:223], a[172:175], v[196:199]\n"
"ds_read_b64_tr_b16 v[120:121], v229 offset:24576\n"
"v_subrev_f32 v149, s22, v149\n"
"v_mfma_f32_16x16x32_bf16 v[140:143], v[216:219], a[168:171], v[140:143]\n"
"ds_read_b64_tr_b16 v[122:123], v229 offset:26624\n"
"v_subrev_f32 v150, s22, v150\n"
"v_mfma_f32_16x16x32_bf16 v[140:143], v[212:215], a[164:167], v[140:143]\n"
"ds_read_b64_tr_b16 v[224:225], v233 offset:24576\n"
"v_subrev_f32 v151, s22, v151\n"
"v_mfma_f32_16x16x32_bf16 v[140:143], v[208:211], a[160:163], v[140:143]\n"
"ds_read_b64_tr_b16 v[226:227], v233 offset:26624\n"
"v_cvt_pk_bf16_f32 v148, v148, v149\n"
"v_mfma_f32_16x16x32_bf16 v[140:143], v[192:195], a[124:127], v[140:143]\n"
"v_cvt_pk_bf16_f32 v149, v150, v151\n"
"v_subrev_f32 v152, s22, v152\n"
"v_mfma_f32_16x16x32_bf16 v[140:143], v[188:191], a[120:123], v[140:143]\n"
"s_add_u32 s41, s70, 0\n"
"v_subrev_f32 v153, s22, v153\n"
"v_subrev_f32 v154, s22, v154\n"
"v_mfma_f32_16x16x32_bf16 v[140:143], v[184:187], a[116:119], v[140:143]\n"
"s_mov_b32 m0, s71\n"
"v_subrev_f32 v155, s22, v155\n"
"v_cvt_pk_bf16_f32 v150, v152, v153\n"
"v_mfma_f32_16x16x32_bf16 v[140:143], v[180:183], a[112:115], v[140:143]\n"
"s_waitcnt vmcnt(14)\n"
"buffer_load_dwordx4 v240, s[28:31], s41 offen lds\n"
"v_mfma_f32_16x16x32_bf16 v[156:159], v[180:183], a[128:131], v[196:199]\n"
"v_xor_b32 v230, s75, v230\n"
"v_cvt_pk_bf16_f32 v151, v154, v155\n"
"v_mfma_f32_16x16x32_bf16 v[156:159], v[184:187], a[132:135], v[156:159]\n"
"v_xor_b32 v234, s75, v234\n"
"v_subrev_f32 v164, s22, v164\n"
"v_mfma_f32_16x16x32_bf16 v[156:159], v[188:191], a[136:139], v[156:159]\n"
"v_subrev_f32 v165, s22, v165\n"
"v_subrev_f32 v166, s22, v166\n"
"v_mfma_f32_16x16x32_bf16 v[156:159], v[192:195], a[140:143], v[156:159]\n"
"v_subrev_f32 v167, s22, v167\n"
"v_cvt_pk_bf16_f32 v164, v164, v165\n"
"v_mfma_f32_16x16x32_bf16 v[156:159], v[208:211], a[176:179], v[156:159]\n"
"v_cvt_pk_bf16_f32 v165, v166, v167\n"
"v_subrev_f32 v168, s22, v168\n"
"v_mfma_f32_16x16x32_bf16 v[156:159], v[212:215], a[180:183], v[156:159]\n"
"v_subrev_f32 v169, s22, v169\n"
"v_subrev_f32 v170, s22, v170\n"
"v_mfma_f32_16x16x32_bf16 v[156:159], v[216:219], a[184:187], v[156:159]\n"
"v_subrev_f32 v171, s22, v171\n"
"v_cvt_pk_bf16_f32 v166, v168, v169\n"
"v_mfma_f32_16x16x32_bf16 v[156:159], v[220:223], a[188:191], v[156:159]\n"
"buffer_store_dwordx4 v[132:135], v243, s[32:35], s16 offen offset:1024 sc1\n"
"v_cvt_pk_bf16_f32 v167, v170, v171\n"
"v_mfma_f32_16x16x32_bf16 v[172:175], v[220:223], a[252:255], v[196:199]\n"
"ds_read_b64_tr_b16 v[220:221], v234 offset:24576\n"
"v_xor_b32 v231, s75, v231\n"
"v_mfma_f32_16x16x32_bf16 v[172:175], v[216:219], a[248:251], v[172:175]\n"
"ds_read_b64_tr_b16 v[216:217], v230 offset:24576\n"
"v_xor_b32 v235, s75, v235\n"
"v_mfma_f32_16x16x32_bf16 v[172:175], v[212:215], a[244:247], v[172:175]\n"
"ds_read_b64_tr_b16 v[212:213], v234 offset:16384\n"
"v_lshlrev_b32 v202, 16, v84\n"
"v_mfma_f32_16x16x32_bf16 v[172:175], v[208:211], a[240:243], v[172:175]\n"
"ds_read_b64_tr_b16 v[208:209], v230 offset:16384\n"
"v_and_b32 v84, s73, v84\n"
"v_mfma_f32_16x16x32_bf16 v[172:175], v[192:195], a[204:207], v[172:175]\n"
"ds_read_b64_tr_b16 v[192:193], v234 offset:8192\n"
"v_add_f32 v25, v25, v84\n"
"v_mfma_f32_16x16x32_bf16 v[172:175], v[188:191], a[200:203], v[172:175]\n"
"ds_read_b64_tr_b16 v[188:189], v230 offset:8192\n"
"v_lshlrev_b32 v203, 16, v85\n"
"v_mfma_f32_16x16x32_bf16 v[172:175], v[184:187], a[196:199], v[172:175]\n"
"s_add_u32 s40, s41, s52\n"
"ds_read_b64_tr_b16 v[184:185], v234 offset:0\n"
"v_and_b32 v85, s73, v85\n"
"v_mfma_f32_16x16x32_bf16 v[172:175], v[180:183], a[192:195], v[172:175]\n"
"s_waitcnt vmcnt(13) lgkmcnt(7)\n"
"buffer_load_dwordx4 v240, s[28:31], s40 offen offset:1024 lds\n"
"v_mfma_f32_16x16x32_bf16 v[128:131], v[96:99], a[96:99], v[196:199]\n"
"s_waitcnt lgkmcnt(6)\n"
"ds_read_b64_tr_b16 v[180:181], v230 offset:0\n"
"v_add_f32 v27, v27, v85\n"
"v_mfma_f32_16x16x32_bf16 v[128:131], v[100:103], a[100:103], v[128:131]\n"
"ds_read_b64_tr_b16 v[182:183], v230 offset:2048\n"
"v_lshlrev_b32 v204, 16, v86\n"
"v_mfma_f32_16x16x32_bf16 v[128:131], v[104:107], a[104:107], v[128:131]\n"
"ds_read_b64_tr_b16 v[186:187], v234 offset:2048\n"
"v_and_b32 v86, s73, v86\n"
"v_mfma_f32_16x16x32_bf16 v[128:131], v[108:111], a[108:111], v[128:131]\n"
"ds_read_b64_tr_b16 v[190:191], v230 offset:10240\n"
"v_add_f32 v29, v29, v86\n"
"v_mfma_f32_16x16x32_bf16 v[128:131], v[112:115], a[144:147], v[128:131]\n"
"s_waitcnt vmcnt(6)\n"
"ds_read_b64_tr_b16 v[194:195], v234 offset:10240\n"
"v_lshlrev_b32 v205, 16, v87\n"
"v_mfma_f32_16x16x32_bf16 v[128:131], v[116:119], a[148:151], v[128:131]\n"
"v_subrev_u32 v200, s21, v200\n"
"ds_read_b64_tr_b16 v[210:211], v230 offset:18432\n"
"v_mfma_f32_16x16x32_bf16 v[128:131], v[120:123], a[152:155], v[128:131]\n"
"v_cmp_lt_u32 vcc, s69, v200\n"
"ds_read_b64_tr_b16 v[214:215], v234 offset:18432\n"
"v_mfma_f32_16x16x32_bf16 v[128:131], v[224:227], a[156:159], v[128:131]\n"
"ds_read_b64_tr_b16 v[218:219], v230 offset:26624\n"
"s_cbranch_vccnz LGP_%=\nLGPK_%=:\n"
"v_mfma_f32_16x16x32_bf16 v[144:147], v[224:227], a[172:175], v[196:199]\n"
"s_sub_u32 s72, s72, 1\n"
"ds_read_b64_tr_b16 v[222:223], v234 offset:26624\n"
"v_and_b32 v87, s73, v87\n"
"v_mfma_f32_16x16x32_bf16 v[144:147], v[120:123], a[168:171], v[144:147]\n"
"s_and_b32 s72, s72, 7\n"
"v_add_f32 v24, v24, v202\n"
"v_add_f32 v31, v31, v87\n"
"v_mfma_f32_16x16x32_bf16 v[144:147], v[116:119], a[164:167], v[144:147]\n"
"v_add_f32 v26, v26, v203\n"
"v_lshlrev_b32 v206, 16, v88\n"
"v_mfma_f32_16x16x32_bf16 v[144:147], v[112:115], a[160:163], v[144:147]\n"
"buffer_load_dwordx4 v[72:75], v243, s[32:35], s18 offen sc1\n"
"v_mfma_f32_16x16x32_bf16 v[144:147], v[108:111], a[124:127], v[144:147]\n"
"v_and_b32 v88, s73, v88\n"
"v_add_f32 v33, v33, v88\n"
"v_mfma_f32_16x16x32_bf16 v[144:147], v[104:107], a[120:123], v[144:147]\n"
"v_lshlrev_b32 v207, 16, v89\n"
"v_and_b32 v89, s73, v89\n"
"v_mfma_f32_16x16x32_bf16 v[144:147], v[100:103], a[116:119], v[144:147]\n"
"v_add_f32 v35, v35, v89\n"
"v_lshlrev_b32 v202, 16, v90\n"
"v_mfma_f32_16x16x32_bf16 v[144:147], v[96:99], a[112:115], v[144:147]\n"
"buffer_store_dwordx4 v[148:151], v243, s[32:35], s16 offen offset:3072 sc1\n"
"v_and_b32 v90, s73, v90\n"
"v_mfma_f32_16x16x32_bf16 v[160:163], v[96:99], a[128:131], v[196:199]\n"
"v_add_f32 v37, v37, v90\n"
"v_lshlrev_b32 v203, 16, v91\n"
"v_mfma_f32_16x16x32_bf16 v[160:163], v[100:103], a[132:135], v[160:163]\n"
"v_and_b32 v91, s73, v91\n"
"v_add_f32 v39, v39, v91\n"
"v_mfma_f32_16x16x32_bf16 v[160:163], v[104:107], a[136:139], v[160:163]\n"
"v_add_f32 v28, v28, v204\n"
"v_add_f32 v30, v30, v205\n"
"v_mfma_f32_16x16x32_bf16 v[160:163], v[108:111], a[140:143], v[160:163]\n"
"v_add_f32 v32, v32, v206\n"
"v_add_f32 v34, v34, v207\n"
"v_mfma_f32_16x16x32_bf16 v[160:163], v[112:115], a[176:179], v[160:163]\n"
"v_lshlrev_b32 v204, 16, v92\n"
"v_and_b32 v92, s73, v92\n"
"v_mfma_f32_16x16x32_bf16 v[160:163], v[116:119], a[180:183], v[160:163]\n"
"v_add_f32 v41, v41, v92\n"
"v_lshlrev_b32 v205, 16, v93\n"
"v_mfma_f32_16x16x32_bf16 v[160:163], v[120:123], a[184:187], v[160:163]\n"
"s_add_u32 s40, s41, s53\n"
"v_and_b32 v93, s73, v93\n"
"v_add_f32 v43, v43, v93\n"
"v_mfma_f32_16x16x32_bf16 v[160:163], v[224:227], a[188:191], v[160:163]\n"
"buffer_load_dwordx4 v240, s[28:31], s40 offen offset:2048 lds\n"
"v_mfma_f32_16x16x32_bf16 v[176:179], v[224:227], a[252:255], v[196:199]\n"
"ds_read_b64_tr_b16 v[224:225], v235 offset:24576\n"
"v_lshlrev_b32 v206, 16, v94\n"
"v_mfma_f32_16x16x32_bf16 v[176:179], v[120:123], a[248:251], v[176:179]\n"
"ds_read_b64_tr_b16 v[120:121], v231 offset:24576\n"
"v_and_b32 v94, s73, v94\n"
"v_mfma_f32_16x16x32_bf16 v[176:179], v[116:119], a[244:247], v[176:179]\n"
"ds_read_b64_tr_b16 v[116:117], v235 offset:16384\n"
"v_add_f32 v45, v45, v94\n"
"v_mfma_f32_16x16x32_bf16 v[176:179], v[112:115], a[240:243], v[176:179]\n"
"buffer_load_dwordx4 v[76:79], v243, s[32:35], s18 offen offset:1024 sc1\n"
"v_mfma_f32_16x16x32_bf16 v[176:179], v[108:111], a[204:207], v[176:179]\n"
"ds_read_b64_tr_b16 v[108:109], v235 offset:8192\n"
"v_lshlrev_b32 v207, 16, v95\n"
"v_mfma_f32_16x16x32_bf16 v[176:179], v[104:107], a[200:203], v[176:179]\n"
"ds_read_b64_tr_b16 v[104:105], v231 offset:8192\n"
"v_and_b32 v95, s73, v95\n"
"v_mfma_f32_16x16x32_bf16 v[176:179], v[100:103], a[196:199], v[176:179]\n"
"s_add_u32 s40, s41, s54\n"
"ds_read_b64_tr_b16 v[100:101], v235 offset:0\n"
"v_add_f32 v47, v47, v95\n"
"v_mfma_f32_16x16x32_bf16 v[176:179], v[96:99], a[192:195], v[176:179]\n"
"s_waitcnt lgkmcnt(6)\n"
"buffer_load_dwordx4 v240, s[28:31], s40 offen offset:3072 lds\n"
"v_mfma_f32_16x16x32_bf16 v[132:135], v[180:183], a[96:99], v[196:199]\n"
"ds_read_b64_tr_b16 v[96:97], v231 offset:0\n"
"v_add_f32 v36, v36, v202\n"
"v_mfma_f32_16x16x32_bf16 v[132:135], v[184:187], a[100:103], v[132:135]\n"
"ds_read_b64_tr_b16 v[98:99], v231 offset:2048\n"
"v_add_f32 v38, v38, v203\n"
"v_mfma_f32_16x16x32_bf16 v[132:135], v[188:191], a[104:107], v[132:135]\n"
"ds_read_b64_tr_b16 v[102:103], v235 offset:2048\n"
"v_add_f32 v44, v44, v206\n"
"v_mfma_f32_16x16x32_bf16 v[132:135], v[192:195], a[108:111], v[132:135]\n"
"ds_read_b64_tr_b16 v[106:107], v231 offset:10240\n"
"v_add_f32 v46, v46, v207\n"
"v_mfma_f32_16x16x32_bf16 v[132:135], v[208:211], a[144:147], v[132:135]\n"
"ds_read_b64_tr_b16 v[110:111], v235 offset:10240\n"
"v_add_f32 v40, v40, v204\n"
"v_mfma_f32_16x16x32_bf16 v[132:135], v[212:215], a[148:151], v[132:135]\n"
"ds_read_b64_tr_b16 v[112:113], v231 offset:16384\n"
"v_add_f32 v42, v42, v205\n"
"v_mfma_f32_16x16x32_bf16 v[132:135], v[216:219], a[152:155], v[132:135]\n"
"ds_read_b64_tr_b16 v[114:115], v231 offset:18432\n"
"v_mfma_f32_16x16x32_bf16 v[132:135], v[220:223], a[156:159], v[132:135]\n"
"buffer_store_dwordx4 v[164:167], v243, s[32:35], s17 offen offset:1024 sc1\n"
"v_lshlrev_b32 v206, 16, v244\n"
"v_mfma_f32_16x16x32_bf16 v[148:151], v[220:223], a[172:175], v[196:199]\n"
"ds_read_b64_tr_b16 v[118:119], v235 offset:18432\n"
"v_and_b32 v244, s73, v244\n"
"v_mfma_f32_16x16x32_bf16 v[148:151], v[216:219], a[168:171], v[148:151]\n"
"ds_read_b64_tr_b16 v[122:123], v231 offset:26624\n"
"v_add_f32 v49, v49, v244\n"
"v_mfma_f32_16x16x32_bf16 v[148:151], v[212:215], a[164:167], v[148:151]\n"
"ds_read_b64_tr_b16 v[226:227], v235 offset:26624\n"
"v_lshlrev_b32 v207, 16, v245\n"
"v_mfma_f32_16x16x32_bf16 v[148:151], v[208:211], a[160:163], v[148:151]\n"
"buffer_load_dwordx4 v[80:83], v243, s[32:35], s18 offen offset:2048 sc1\n"
"v_mfma_f32_16x16x32_bf16 v[148:151], v[192:195], a[124:127], v[148:151]\n"
"v_and_b32 v245, s73, v245\n"
"v_add_f32 v51, v51, v245\n"
"v_mfma_f32_16x16x32_bf16 v[148:151], v[188:191], a[120:123], v[148:151]\n"
"s_add_u32 s41, s70, 163840\n"
"v_lshlrev_b32 v202, 16, v246\n"
"v_and_b32 v246, s73, v246\n"
"v_mfma_f32_16x16x32_bf16 v[148:151], v[184:187], a[116:119], v[148:151]\n"
"s_add_u32 m0, s71, 16384\n"
"v_add_f32 v53, v53, v246\n"
"v_lshlrev_b32 v203, 16, v247\n"
"v_mfma_f32_16x16x32_bf16 v[148:151], v[180:183], a[112:115], v[148:151]\n"
"buffer_load_dwordx4 v240, s[28:31], s41 offen lds\n"
"v_mfma_f32_16x16x32_bf16 v[164:167], v[180:183], a[128:131], v[196:199]\n"
"v_and_b32 v247, s73, v247\n"
"v_add_f32 v55, v55, v247\n"
"v_mfma_f32_16x16x32_bf16 v[164:167], v[184:187], a[132:135], v[164:167]\n"
"v_add_f32 v48, v48, v206\n"
"v_add_f32 v50, v50, v207\n"
"v_mfma_f32_16x16x32_bf16 v[164:167], v[188:191], a[136:139], v[164:167]\n"
"v_add_f32 v52, v52, v202\n"
"v_add_f32 v54, v54, v203\n"
"v_mfma_f32_16x16x32_bf16 v[164:167], v[192:195], a[140:143], v[164:167]\n"
"v_mfma_f32_16x16x32_bf16 v[164:167], v[208:211], a[176:179], v[164:167]\n"
"v_mfma_f32_16x16x32_bf16 v[164:167], v[212:215], a[180:183], v[164:167]\n"
"v_mfma_f32_16x16x32_bf16 v[164:167], v[216:219], a[184:187], v[164:167]\n"
"s_waitcnt vmcnt(12)\n"
"v_mfma_f32_16x16x32_bf16 v[164:167], v[220:223], a[188:191], v[164:167]\n"
"s_barrier\n"
"s_waitcnt lgkmcnt(0)\n"
"v_mfma_f32_16x16x32_bf16 v[136:139], v[96:99], a[96:99], v[196:199]\n"
"ds_read_b64_tr_b16 v[180:181], v228 offset:32768\n"
"v_mfma_f32_16x16x32_bf16 v[136:139], v[100:103], a[100:103], v[136:139]\n"
"ds_read_b64_tr_b16 v[182:183], v228 offset:34816\n"
"v_mfma_f32_16x16x32_bf16 v[136:139], v[104:107], a[104:107], v[136:139]\n"
"ds_read_b64_tr_b16 v[184:185], v232 offset:32768\n"
"v_mfma_f32_16x16x32_bf16 v[136:139], v[108:111], a[108:111], v[136:139]\n"
"ds_read_b64_tr_b16 v[186:187], v232 offset:34816\n"
"v_mfma_f32_16x16x32_bf16 v[136:139], v[112:115], a[144:147], v[136:139]\n"
"ds_read_b64_tr_b16 v[188:189], v228 offset:40960\n"
"v_mfma_f32_16x16x32_bf16 v[136:139], v[116:119], a[148:151], v[136:139]\n"
"ds_read_b64_tr_b16 v[190:191], v228 offset:43008\n"
"v_mfma_f32_16x16x32_bf16 v[136:139], v[120:123], a[152:155], v[136:139]\n"
"s_add_u32 s40, s41, s52\n"
"ds_read_b64_tr_b16 v[192:193], v232 offset:40960\n"
"v_mfma_f32_16x16x32_bf16 v[136:139], v[224:227], a[156:159], v[136:139]\n"
"buffer_load_dwordx4 v240, s[28:31], s40 offen offset:1024 lds\n"
"v_mfma_f32_16x16x32_bf16 v[152:155], v[224:227], a[172:175], v[196:199]\n"
"ds_read_b64_tr_b16 v[194:195], v232 offset:43008\n"
"v_mfma_f32_16x16x32_bf16 v[152:155], v[120:123], a[168:171], v[152:155]\n"
"ds_read_b64_tr_b16 v[208:209], v228 offset:49152\n"
"v_mfma_f32_16x16x32_bf16 v[152:155], v[116:119], a[164:167], v[152:155]\n"
"ds_read_b64_tr_b16 v[210:211], v228 offset:51200\n"
"v_mfma_f32_16x16x32_bf16 v[152:155], v[112:115], a[160:163], v[152:155]\n"
"ds_read_b64_tr_b16 v[212:213], v232 offset:49152\n"
"v_mfma_f32_16x16x32_bf16 v[152:155], v[108:111], a[124:127], v[152:155]\n"
"ds_read_b64_tr_b16 v[214:215], v232 offset:51200\n"
"v_mfma_f32_16x16x32_bf16 v[152:155], v[104:107], a[120:123], v[152:155]\n"
"ds_read_b64_tr_b16 v[216:217], v228 offset:57344\n"
"v_mfma_f32_16x16x32_bf16 v[152:155], v[100:103], a[116:119], v[152:155]\n"
"s_add_u32 s40, s41, s53\n"
"ds_read_b64_tr_b16 v[218:219], v228 offset:59392\n"
"v_xor_b32 v228, s75, v228\n"
"v_mfma_f32_16x16x32_bf16 v[152:155], v[96:99], a[112:115], v[152:155]\n"
"buffer_load_dwordx4 v240, s[28:31], s40 offen offset:2048 lds\n"
"v_mfma_f32_16x16x32_bf16 v[168:171], v[96:99], a[128:131], v[196:199]\n"
"ds_read_b64_tr_b16 v[220:221], v232 offset:57344\n"
"v_mfma_f32_16x16x32_bf16 v[168:171], v[100:103], a[132:135], v[168:171]\n"
"ds_read_b64_tr_b16 v[222:223], v232 offset:59392\n"
"v_xor_b32 v232, s75, v232\n"
"v_mfma_f32_16x16x32_bf16 v[168:171], v[104:107], a[136:139], v[168:171]\n"
"ds_read_b64_tr_b16 v[96:97], v229 offset:32768\n"
"v_mfma_f32_16x16x32_bf16 v[168:171], v[108:111], a[140:143], v[168:171]\n"
"ds_read_b64_tr_b16 v[98:99], v229 offset:34816\n"
"v_mfma_f32_16x16x32_bf16 v[168:171], v[112:115], a[176:179], v[168:171]\n"
"ds_read_b64_tr_b16 v[100:101], v233 offset:32768\n"
"v_mfma_f32_16x16x32_bf16 v[168:171], v[116:119], a[180:183], v[168:171]\n"
"ds_read_b64_tr_b16 v[102:103], v233 offset:34816\n"
"v_mfma_f32_16x16x32_bf16 v[168:171], v[120:123], a[184:187], v[168:171]\n"
"s_add_u32 s40, s41, s54\n"
"ds_read_b64_tr_b16 v[104:105], v229 offset:40960\n"
"v_mfma_f32_16x16x32_bf16 v[168:171], v[224:227], a[188:191], v[168:171]\n"
"s_waitcnt lgkmcnt(7)\n"
"buffer_load_dwordx4 v240, s[28:31], s40 offen offset:3072 lds\n"
"v_mfma_f32_16x16x32_bf16 v[124:127], v[180:183], a[48:51], v[124:127]\n"
"ds_read_b64_tr_b16 v[106:107], v229 offset:43008\n"
"v_mfma_f32_16x16x32_bf16 v[124:127], v[184:187], a[52:55], v[124:127]\n"
"ds_read_b64_tr_b16 v[108:109], v233 offset:40960\n"
"v_mfma_f32_16x16x32_bf16 v[124:127], v[188:191], a[56:59], v[124:127]\n"
"ds_read_b64_tr_b16 v[110:111], v233 offset:43008\n"
"v_mfma_f32_16x16x32_bf16 v[124:127], v[192:195], a[60:63], v[124:127]\n"
"ds_read_b64_tr_b16 v[112:113], v229 offset:49152\n"
"v_mfma_f32_16x16x32_bf16 v[124:127], v[208:211], a[0:3], v[124:127]\n"
"ds_read_b64_tr_b16 v[114:115], v229 offset:51200\n"
"v_mfma_f32_16x16x32_bf16 v[124:127], v[212:215], a[4:7], v[124:127]\n"
"s_add_u32 s41, s70, 327680\n"
"ds_read_b64_tr_b16 v[116:117], v233 offset:49152\n"
"v_mfma_f32_16x16x32_bf16 v[124:127], v[216:219], a[8:11], v[124:127]\n"
"s_waitcnt lgkmcnt(6)\n"
"s_add_u32 m0, s71, 32768\n"
"ds_read_b64_tr_b16 v[118:119], v233 offset:51200\n"
"v_mfma_f32_16x16x32_bf16 v[124:127], v[220:223], a[12:15], v[124:127]\n"
"buffer_load_dwordx4 v240, s[28:31], s41 offen lds\n"
"v_mfma_f32_16x16x32_bf16 v[140:143], v[220:223], a[28:31], v[140:143]\n"
"ds_read_b64_tr_b16 v[120:121], v229 offset:57344\n"
"v_mfma_f32_16x16x32_bf16 v[140:143], v[216:219], a[24:27], v[140:143]\n"
"ds_read_b64_tr_b16 v[122:123], v229 offset:59392\n"
"v_xor_b32 v229, s75, v229\n"
"v_mfma_f32_16x16x32_bf16 v[140:143], v[212:215], a[20:23], v[140:143]\n"
"ds_read_b64_tr_b16 v[224:225], v233 offset:57344\n"
"v_mfma_f32_16x16x32_bf16 v[140:143], v[208:211], a[16:19], v[140:143]\n"
"ds_read_b64_tr_b16 v[226:227], v233 offset:59392\n"
"v_xor_b32 v233, s75, v233\n"
"v_mfma_f32_16x16x32_bf16 v[140:143], v[192:195], a[76:79], v[140:143]\n"
"v_mfma_f32_16x16x32_bf16 v[140:143], v[188:191], a[72:75], v[140:143]\n"
"v_subrev_f32 v124, s22, v124\n"
"v_subrev_f32 v125, s22, v125\n"
"v_mfma_f32_16x16x32_bf16 v[140:143], v[184:187], a[68:71], v[140:143]\n"
"s_add_u32 s40, s41, s52\n"
"v_subrev_f32 v126, s22, v126\n"
"v_subrev_f32 v127, s22, v127\n"
"v_mfma_f32_16x16x32_bf16 v[140:143], v[180:183], a[64:67], v[140:143]\n"
"buffer_load_dwordx4 v240, s[28:31], s40 offen offset:1024 lds\n"
"v_mfma_f32_16x16x32_bf16 v[156:159], v[180:183], a[80:83], v[156:159]\n"
"v_cvt_pk_bf16_f32 v124, v124, v125\n"
"v_cvt_pk_bf16_f32 v125, v126, v127\n"
"v_mfma_f32_16x16x32_bf16 v[156:159], v[184:187], a[84:87], v[156:159]\n"
"v_mfma_f32_16x16x32_bf16 v[156:159], v[188:191], a[88:91], v[156:159]\n"
"v_mfma_f32_16x16x32_bf16 v[156:159], v[192:195], a[92:95], v[156:159]\n"
"v_mfma_f32_16x16x32_bf16 v[156:159], v[208:211], a[32:35], v[156:159]\n"
"v_mfma_f32_16x16x32_bf16 v[156:159], v[212:215], a[36:39], v[156:159]\n"
"v_subrev_f32 v140, s22, v140\n"
"v_subrev_f32 v141, s22, v141\n"
"v_mfma_f32_16x16x32_bf16 v[156:159], v[216:219], a[40:43], v[156:159]\n"
"s_waitcnt vmcnt(7)\n"
"v_subrev_f32 v142, s22, v142\n"
"v_subrev_f32 v143, s22, v143\n"
"v_mfma_f32_16x16x32_bf16 v[156:159], v[220:223], a[44:47], v[156:159]\n"
"s_barrier\n"
"buffer_store_dword v241, v248, s[84:87], 0 offen sc1\n"
"v_mfma_f32_16x16x32_bf16 v[172:175], v[220:223], a[228:231], v[172:175]\n"
"ds_read_b64_tr_b16 v[220:221], v234 offset:57344\n"
"v_cvt_pk_bf16_f32 v140, v140, v141\n"
"v_mfma_f32_16x16x32_bf16 v[172:175], v[216:219], a[224:227], v[172:175]\n"
"ds_read_b64_tr_b16 v[216:217], v230 offset:57344\n"
"v_cvt_pk_bf16_f32 v141, v142, v143\n"
"v_mfma_f32_16x16x32_bf16 v[172:175], v[212:215], a[212:215], v[172:175]\n"
"ds_read_b64_tr_b16 v[212:213], v234 offset:49152\n"
"v_add_u32 v241, 1, v241\n"
"v_mfma_f32_16x16x32_bf16 v[172:175], v[208:211], a[208:211], v[172:175]\n"
"buffer_load_dwordx4 v[84:87], v243, s[32:35], s18 offen offset:3072 sc1\n"
"v_mfma_f32_16x16x32_bf16 v[172:175], v[192:195], a[236:239], v[172:175]\n"
"ds_read_b64_tr_b16 v[192:193], v234 offset:40960\n"
"v_mfma_f32_16x16x32_bf16 v[172:175], v[188:191], a[232:235], v[172:175]\n"
"ds_read_b64_tr_b16 v[188:189], v230 offset:40960\n"
"v_subrev_f32 v156, s22, v156\n"
"v_mfma_f32_16x16x32_bf16 v[172:175], v[184:187], a[220:223], v[172:175]\n"
"s_add_u32 s40, s41, s53\n"
"ds_read_b64_tr_b16 v[184:185], v234 offset:32768\n"
"v_subrev_f32 v157, s22, v157\n"
"v_mfma_f32_16x16x32_bf16 v[172:175], v[180:183], a[216:219], v[172:175]\n"
"buffer_load_dwordx4 v240, s[28:31], s40 offen offset:2048 lds\n"
"v_mfma_f32_16x16x32_bf16 v[128:131], v[96:99], a[48:51], v[128:131]\n"
"s_add_u32 s16, s90, s62\n"
"ds_read_b64_tr_b16 v[180:181], v230 offset:32768\n"
"v_subrev_f32 v158, s22, v158\n"
"v_mfma_f32_16x16x32_bf16 v[128:131], v[100:103], a[52:55], v[128:131]\n"
"s_waitcnt lgkmcnt(5)\n"
"s_add_u32 s17, s16, 4096\n"
"ds_read_b64_tr_b16 v[182:183], v230 offset:34816\n"
"v_subrev_f32 v159, s22, v159\n"
"v_mfma_f32_16x16x32_bf16 v[128:131], v[104:107], a[56:59], v[128:131]\n"
"s_lshl_b32 s62, s70, 9\n"
"ds_read_b64_tr_b16 v[186:187], v234 offset:34816\n"
"v_cvt_pk_bf16_f32 v156, v156, v157\n"
"v_mfma_f32_16x16x32_bf16 v[128:131], v[108:111], a[60:63], v[128:131]\n"
"ds_read_b64_tr_b16 v[190:191], v230 offset:43008\n"
"v_cvt_pk_bf16_f32 v157, v158, v159\n"
"v_mfma_f32_16x16x32_bf16 v[128:131], v[112:115], a[0:3], v[128:131]\n"
"ds_read_b64_tr_b16 v[194:195], v234 offset:43008\n"
"v_lshlrev_b32 v202, 16, v72\n"
"v_mfma_f32_16x16x32_bf16 v[128:131], v[116:119], a[4:7], v[128:131]\n"
"ds_read_b64_tr_b16 v[208:209], v230 offset:49152\n"
"v_and_b32 v72, s73, v72\n"
"v_mfma_f32_16x16x32_bf16 v[128:131], v[120:123], a[8:11], v[128:131]\n"
"s_add_u32 s40, s41, s54\n"
"ds_read_b64_tr_b16 v[210:211], v230 offset:51200\n"
"v_add_f32 v0, v0, v202\n"
"v_mfma_f32_16x16x32_bf16 v[128:131], v[224:227], a[12:15], v[128:131]\n"
"buffer_load_dwordx4 v240, s[28:31], s40 offen offset:3072 lds\n"
"v_mfma_f32_16x16x32_bf16 v[144:147], v[224:227], a[28:31], v[144:147]\n"
"ds_read_b64_tr_b16 v[214:215], v234 offset:51200\n"
"v_add_f32 v1, v1, v72\n"
"v_mfma_f32_16x16x32_bf16 v[144:147], v[120:123], a[24:27], v[144:147]\n"
"ds_read_b64_tr_b16 v[218:219], v230 offset:59392\n"
"v_lshlrev_b32 v203, 16, v73\n"
"v_mfma_f32_16x16x32_bf16 v[144:147], v[116:119], a[20:23], v[144:147]\n"
"ds_read_b64_tr_b16 v[222:223], v234 offset:59392\n"
"v_and_b32 v73, s73, v73\n"
"v_mfma_f32_16x16x32_bf16 v[144:147], v[112:115], a[16:19], v[144:147]\n"
"buffer_load_dwordx4 v[88:91], v243, s[32:35], s19 offen sc1\n"
"v_mfma_f32_16x16x32_bf16 v[144:147], v[108:111], a[76:79], v[144:147]\n"
"v_add_f32 v2, v2, v203\n"
"v_add_f32 v3, v3, v73\n"
"v_mfma_f32_16x16x32_bf16 v[144:147], v[104:107], a[72:75], v[144:147]\n"
"s_add_u32 s41, s70, 491520\n"
"v_subrev_f32 v128, s22, v128\n"
"v_subrev_f32 v129, s22, v129\n"
"v_mfma_f32_16x16x32_bf16 v[144:147], v[100:103], a[68:71], v[144:147]\n"
"s_add_u32 m0, s71, 49152\n"
"v_subrev_f32 v130, s22, v130\n"
"v_subrev_f32 v131, s22, v131\n"
"v_mfma_f32_16x16x32_bf16 v[144:147], v[96:99], a[64:67], v[144:147]\n"
"buffer_load_dwordx4 v240, s[28:31], s41 offen lds\n"
"v_mfma_f32_16x16x32_bf16 v[160:163], v[96:99], a[80:83], v[160:163]\n"
"s_and_b32 s70, s88, 15\n"
"v_cvt_pk_bf16_f32 v126, v128, v129\n"
"v_cvt_pk_bf16_f32 v127, v130, v131\n"
"v_mfma_f32_16x16x32_bf16 v[160:163], v[100:103], a[84:87], v[160:163]\n"
"s_lshr_b32 s88, s88, 4\n"
"v_lshlrev_b32 v204, 16, v74\n"
"v_and_b32 v74, s73, v74\n"
"v_mfma_f32_16x16x32_bf16 v[160:163], v[104:107], a[88:91], v[160:163]\n"
"s_lshl_b32 s70, s70, 7\n"
"v_add_f32 v4, v4, v204\n"
"v_add_f32 v5, v5, v74\n"
"v_mfma_f32_16x16x32_bf16 v[160:163], v[108:111], a[92:95], v[160:163]\n"
"s_xor_b32 s71, s71, s75\n"
"v_lshlrev_b32 v205, 16, v75\n"
"v_and_b32 v75, s73, v75\n"
"v_mfma_f32_16x16x32_bf16 v[160:163], v[112:115], a[32:35], v[160:163]\n"
"v_add_f32 v6, v6, v205\n"
"v_add_f32 v7, v7, v75\n"
"v_mfma_f32_16x16x32_bf16 v[160:163], v[116:119], a[36:39], v[160:163]\n"
"v_subrev_f32 v144, s22, v144\n"
"v_subrev_f32 v145, s22, v145\n"
"v_mfma_f32_16x16x32_bf16 v[160:163], v[120:123], a[40:43], v[160:163]\n"
"v_subrev_f32 v146, s22, v146\n"
"v_subrev_f32 v147, s22, v147\n"
"v_mfma_f32_16x16x32_bf16 v[160:163], v[224:227], a[44:47], v[160:163]\n"
"buffer_store_dwordx4 v[124:127], v243, s[32:35], s16 offen sc1\n"
"v_cvt_pk_bf16_f32 v142, v144, v145\n"
"v_mfma_f32_16x16x32_bf16 v[176:179], v[224:227], a[228:231], v[176:179]\n"
"ds_read_b64_tr_b16 v[224:225], v235 offset:57344\n"
"v_cvt_pk_bf16_f32 v143, v146, v147\n"
"v_mfma_f32_16x16x32_bf16 v[176:179], v[120:123], a[224:227], v[176:179]\n"
"ds_read_b64_tr_b16 v[120:121], v231 offset:57344\n"
"v_lshlrev_b32 v206, 16, v76\n"
"v_mfma_f32_16x16x32_bf16 v[176:179], v[116:119], a[212:215], v[176:179]\n"
"ds_read_b64_tr_b16 v[116:117], v235 offset:49152\n"
"v_and_b32 v76, s73, v76\n"
"v_mfma_f32_16x16x32_bf16 v[176:179], v[112:115], a[208:211], v[176:179]\n"
"buffer_load_dwordx4 v[92:95], v243, s[32:35], s19 offen offset:1024 sc1\n"
"v_mfma_f32_16x16x32_bf16 v[176:179], v[108:111], a[236:239], v[176:179]\n"
"ds_read_b64_tr_b16 v[108:109], v235 offset:40960\n"
"v_add_f32 v8, v8, v206\n"
"v_mfma_f32_16x16x32_bf16 v[176:179], v[104:107], a[232:235], v[176:179]\n"
"ds_read_b64_tr_b16 v[104:105], v231 offset:40960\n"
"v_subrev_f32 v160, s22, v160\n"
"v_mfma_f32_16x16x32_bf16 v[176:179], v[100:103], a[220:223], v[176:179]\n"
"s_add_u32 s40, s41, s52\n"
"ds_read_b64_tr_b16 v[100:101], v235 offset:32768\n"
"v_subrev_f32 v161, s22, v161\n"
"v_mfma_f32_16x16x32_bf16 v[176:179], v[96:99], a[216:219], v[176:179]\n"
"s_waitcnt vmcnt(14) lgkmcnt(6)\n"
"buffer_load_dwordx4 v240, s[28:31], s40 offen offset:1024 lds\n"
"v_mfma_f32_16x16x32_bf16 v[132:135], v[180:183], a[48:51], v[132:135]\n"
"ds_read_b64_tr_b16 v[96:97], v231 offset:32768\n"
"v_subrev_f32 v162, s22, v162\n"
"v_mfma_f32_16x16x32_bf16 v[132:135], v[184:187], a[52:55], v[132:135]\n"
"ds_read_b64_tr_b16 v[98:99], v231 offset:34816\n"
"v_subrev_f32 v163, s22, v163\n"
"v_mfma_f32_16x16x32_bf16 v[132:135], v[188:191], a[56:59], v[132:135]\n"
"ds_read_b64_tr_b16 v[102:103], v235 offset:34816\n"
"v_cvt_pk_bf16_f32 v158, v160, v161\n"
"v_mfma_f32_16x16x32_bf16 v[132:135], v[192:195], a[60:63], v[132:135]\n"
"ds_read_b64_tr_b16 v[106:107], v231 offset:43008\n"
"v_cvt_pk_bf16_f32 v159, v162, v163\n"
"v_mfma_f32_16x16x32_bf16 v[132:135], v[208:211], a[0:3], v[132:135]\n"
"ds_read_b64_tr_b16 v[110:111], v235 offset:43008\n"
"v_add_f32 v9, v9, v76\n"
"v_mfma_f32_16x16x32_bf16 v[132:135], v[212:215], a[4:7], v[132:135]\n"
"ds_read_b64_tr_b16 v[112:113], v231 offset:49152\n"
"v_lshlrev_b32 v207, 16, v77\n"
"v_mfma_f32_16x16x32_bf16 v[132:135], v[216:219], a[8:11], v[132:135]\n"
"ds_read_b64_tr_b16 v[114:115], v231 offset:51200\n"
"v_and_b32 v77, s73, v77\n"
"v_mfma_f32_16x16x32_bf16 v[132:135], v[220:223], a[12:15], v[132:135]\n"
"buffer_store_dwordx4 v[140:143], v243, s[32:35], s16 offen offset:2048 sc1\n"
"v_add_f32 v10, v10, v207\n"
"v_mfma_f32_16x16x32_bf16 v[148:151], v[220:223], a[28:31], v[148:151]\n"
"ds_read_b64_tr_b16 v[118:119], v235 offset:51200\n"
"v_add_f32 v11, v11, v77\n"
"v_mfma_f32_16x16x32_bf16 v[148:151], v[216:219], a[24:27], v[148:151]\n"
"ds_read_b64_tr_b16 v[122:123], v231 offset:59392\n"
"v_lshlrev_b32 v202, 16, v78\n"
"v_mfma_f32_16x16x32_bf16 v[148:151], v[212:215], a[20:23], v[148:151]\n"
"ds_read_b64_tr_b16 v[226:227], v235 offset:59392\n"
"v_and_b32 v78, s73, v78\n"
"v_mfma_f32_16x16x32_bf16 v[148:151], v[208:211], a[16:19], v[148:151]\n"
"v_add_f32 v12, v12, v202\n"
"v_add_f32 v13, v13, v78\n"
"v_mfma_f32_16x16x32_bf16 v[148:151], v[192:195], a[76:79], v[148:151]\n"
"v_lshlrev_b32 v203, 16, v79\n"
"v_and_b32 v79, s73, v79\n"
"v_mfma_f32_16x16x32_bf16 v[148:151], v[188:191], a[72:75], v[148:151]\n"
"v_add_f32 v14, v14, v203\n"
"v_add_f32 v15, v15, v79\n"
"v_mfma_f32_16x16x32_bf16 v[148:151], v[184:187], a[68:71], v[148:151]\n"
"s_add_u32 s40, s41, s53\n"
"v_lshlrev_b32 v204, 16, v80\n"
"v_and_b32 v80, s73, v80\n"
"v_mfma_f32_16x16x32_bf16 v[148:151], v[180:183], a[64:67], v[148:151]\n"
"buffer_load_dwordx4 v240, s[28:31], s40 offen offset:2048 lds\n"
"v_mfma_f32_16x16x32_bf16 v[164:167], v[180:183], a[80:83], v[164:167]\n"
"v_add_f32 v16, v16, v204\n"
"v_add_f32 v17, v17, v80\n"
"v_mfma_f32_16x16x32_bf16 v[164:167], v[184:187], a[84:87], v[164:167]\n"
"v_lshlrev_b32 v205, 16, v81\n"
"v_and_b32 v81, s73, v81\n"
"v_mfma_f32_16x16x32_bf16 v[164:167], v[188:191], a[88:91], v[164:167]\n"
"v_add_f32 v18, v18, v205\n"
"v_add_f32 v19, v19, v81\n"
"v_mfma_f32_16x16x32_bf16 v[164:167], v[192:195], a[92:95], v[164:167]\n"
"buffer_load_dwordx4 v[244:247], v243, s[32:35], s19 offen offset:2048 sc1\n"
"v_mfma_f32_16x16x32_bf16 v[164:167], v[208:211], a[32:35], v[164:167]\n"
"s_lshl_b32 s18, s72, 4\n"
"v_lshlrev_b32 v206, 16, v82\n"
"v_and_b32 v82, s73, v82\n"
"v_mfma_f32_16x16x32_bf16 v[164:167], v[212:215], a[36:39], v[164:167]\n"
"s_lshl_b32 s19, s72, 2\n"
"v_add_f32 v20, v20, v206\n"
"v_add_f32 v21, v21, v82\n"
"v_mfma_f32_16x16x32_bf16 v[164:167], v[216:219], a[40:43], v[164:167]\n"
"s_waitcnt vmcnt(14)\n"
"s_lshr_b32 s19, s89, s19\n"
"v_lshlrev_b32 v207, 16, v83\n"
"v_mfma_f32_16x16x32_bf16 v[164:167], v[220:223], a[44:47], v[164:167]\n"
"s_barrier\n"
"s_and_b32 s19, s19, 1\n"
"s_waitcnt lgkmcnt(0)\n"
"v_mfma_f32_16x16x32_bf16 v[136:139], v[96:99], a[48:51], v[136:139]\n"
"s_lshl3_add_u32 s18, s18, s19\n"
"ds_read_b64_tr_b16 v[180:181], v228 offset:0\n"
"v_and_b32 v83, s73, v83\n"
"v_mfma_f32_16x16x32_bf16 v[136:139], v[100:103], a[52:55], v[136:139]\n"
"s_lshl_b32 s18, s18, 13\n"
"ds_read_b64_tr_b16 v[182:183], v228 offset:2048\n"
"v_add_f32 v22, v22, v207\n"
"v_mfma_f32_16x16x32_bf16 v[136:139], v[104:107], a[56:59], v[136:139]\n"
"s_add_u32 s18, s18, s91\n"
"v_lshlrev_b32_e64 v201, 2, s72\n"
"ds_read_b64_tr_b16 v[184:185], v232 offset:0\n"
"v_mfma_f32_16x16x32_bf16 v[136:139], v[108:111], a[60:63], v[136:139]\n"
"s_add_u32 s19, s18, 4096\n"
"global_load_dword v200, v201, s[6:7] sc1\n"
"v_mfma_f32_16x16x32_bf16 v[136:139], v[112:115], a[0:3], v[136:139]\n"
"s_mov_b32 s69, 15\n"
"ds_read_b64_tr_b16 v[186:187], v232 offset:2048\n"
"v_add_f32 v23, v23, v83\n"
"v_mfma_f32_16x16x32_bf16 v[136:139], v[116:119], a[4:7], v[136:139]\n"
"s_add_u32 s21, s21, 1\n"
"ds_read_b64_tr_b16 v[188:189], v228 offset:8192\n"
"v_subrev_f32 v172, s22, v172\n"
"v_mfma_f32_16x16x32_bf16 v[136:139], v[120:123], a[8:11], v[136:139]\n"
"ds_read_b64_tr_b16 v[190:191], v228 offset:10240\n"
"v_subrev_f32 v173, s22, v173\n"
"v_mfma_f32_16x16x32_bf16 v[136:139], v[224:227], a[12:15], v[136:139]\n"
"buffer_store_dwordx4 v[156:159], v243, s[32:35], s17 offen sc1\n"
"v_subrev_f32 v174, s22, v174\n"
"v_mfma_f32_16x16x32_bf16 v[152:155], v[224:227], a[28:31], v[152:155]\n"
"ds_read_b64_tr_b16 v[192:193], v232 offset:8192\n"
"v_subrev_f32 v175, s22, v175\n"
"v_mfma_f32_16x16x32_bf16 v[152:155], v[120:123], a[24:27], v[152:155]\n"
"ds_read_b64_tr_b16 v[194:195], v232 offset:10240\n"
"v_cvt_pk_bf16_f32 v172, v172, v173\n"
"v_mfma_f32_16x16x32_bf16 v[152:155], v[116:119], a[20:23], v[152:155]\n"
"ds_read_b64_tr_b16 v[208:209], v228 offset:16384\n"
"v_cvt_pk_bf16_f32 v173, v174, v175\n"
"v_mfma_f32_16x16x32_bf16 v[152:155], v[112:115], a[16:19], v[152:155]\n"
"ds_read_b64_tr_b16 v[210:211], v228 offset:18432\n"
"v_subrev_f32 v176, s22, v176\n"
"v_mfma_f32_16x16x32_bf16 v[152:155], v[108:111], a[76:79], v[152:155]\n"
"ds_read_b64_tr_b16 v[212:213], v232 offset:16384\n"
"v_subrev_f32 v177, s22, v177\n"
"v_mfma_f32_16x16x32_bf16 v[152:155], v[104:107], a[72:75], v[152:155]\n"
"ds_read_b64_tr_b16 v[214:215], v232 offset:18432\n"
"v_subrev_f32 v178, s22, v178\n"
"v_mfma_f32_16x16x32_bf16 v[152:155], v[100:103], a[68:71], v[152:155]\n"
"s_add_u32 s40, s41, s54\n"
"ds_read_b64_tr_b16 v[216:217], v228 offset:24576\n"
"v_subrev_f32 v179, s22, v179\n"
"v_mfma_f32_16x16x32_bf16 v[152:155], v[96:99], a[64:67], v[152:155]\n"
"buffer_load_dwordx4 v240, s[28:31], s40 offen offset:3072 lds\n"
"v_mfma_f32_16x16x32_bf16 v[168:171], v[96:99], a[80:83], v[168:171]\n"
"ds_read_b64_tr_b16 v[218:219], v228 offset:26624\n"
"v_cvt_pk_bf16_f32 v174, v176, v177\n"
"v_mfma_f32_16x16x32_bf16 v[168:171], v[100:103], a[84:87], v[168:171]\n"
"ds_read_b64_tr_b16 v[220:221], v232 offset:24576\n"
"v_cvt_pk_bf16_f32 v175, v178, v179\n"
"v_mfma_f32_16x16x32_bf16 v[168:171], v[104:107], a[88:91], v[168:171]\n"
"ds_read_b64_tr_b16 v[222:223], v232 offset:26624\n"
"v_subrev_f32 v132, s22, v132\n"
"v_mfma_f32_16x16x32_bf16 v[168:171], v[108:111], a[92:95], v[168:171]\n"
"ds_read_b64_tr_b16 v[96:97], v229 offset:0\n"
"v_subrev_f32 v133, s22, v133\n"
"v_mfma_f32_16x16x32_bf16 v[168:171], v[112:115], a[32:35], v[168:171]\n"
"ds_read_b64_tr_b16 v[98:99], v229 offset:2048\n"
"v_subrev_f32 v134, s22, v134\n"
"v_mfma_f32_16x16x32_bf16 v[168:171], v[116:119], a[36:39], v[168:171]\n"
"ds_read_b64_tr_b16 v[100:101], v233 offset:0\n"
"v_subrev_f32 v135, s22, v135\n"
"v_mfma_f32_16x16x32_bf16 v[168:171], v[120:123], a[40:43], v[168:171]\n"
"s_add_u32 s47, s47, 1\n"
"ds_read_b64_tr_b16 v[102:103], v233 offset:2048\n"
"v_cvt_pk_bf16_f32 v132, v132, v133\n"
"v_mfma_f32_16x16x32_bf16 v[168:171], v[224:227], a[44:47], v[168:171]\n"
"s_waitcnt lgkmcnt(7)\n"
"s_cmp_lg_u32 s47, 9\n"
"ds_read_b64_tr_b16 v[104:105], v229 offset:8192\n"
"s_cbranch_scc1 LGL_%=\n"
"v_mfma_f32_16x16x32_bf16 v[0:3], v[180:183], a[96:99], v[0:3]\n"
"v_xor_b32 v235, s75, v235\n"
"v_subrev_f32 v136, s22, v136\n"
"v_mfma_f32_16x16x32_bf16 v[0:3], v[184:187], a[100:103], v[0:3]\n"
"ds_read_b64_tr_b16 v[114:115], v229 offset:18432\n"
"v_cvt_pk_bf16_f32 v133, v134, v135\n"
"v_mfma_f32_16x16x32_bf16 v[0:3], v[188:191], a[104:107], v[0:3]\n"
"v_mov_b32 v201, v237\n"
"v_subrev_f32 v138, s22, v138\n"
"v_subrev_f32 v139, s22, v139\n"
"v_mfma_f32_16x16x32_bf16 v[0:3], v[192:195], a[108:111], v[0:3]\n"
"global_load_dword v200, v201, s[6:7] sc1\n"
"v_mfma_f32_16x16x32_bf16 v[0:3], v[208:211], a[144:147], v[0:3]\n"
"v_subrev_f32 v167, s22, v167\n"
"v_cvt_pk_bf16_f32 v135, v138, v139\n"
"v_mfma_f32_16x16x32_bf16 v[0:3], v[212:215], a[148:151], v[0:3]\n"
"s_waitcnt lgkmcnt(3)\n"
"v_subrev_f32 v148, s22, v148\n"
"v_subrev_f32 v137, s22, v137\n"
"v_mfma_f32_16x16x32_bf16 v[0:3], v[216:219], a[152:155], v[0:3]\n"
"v_subrev_f32 v165, s22, v165\n"
"v_subrev_f32 v168, s22, v168\n"
"v_mfma_f32_16x16x32_bf16 v[0:3], v[220:223], a[156:159], v[0:3]\n"
"buffer_store_dwordx4 v[172:175], v243, s[32:35], s17 offen offset:2048 sc1\n"
"v_subrev_f32 v151, s22, v151\n"
"v_cvt_pk_bf16_f32 v134, v136, v137\n"
"v_mfma_f32_16x16x32_bf16 v[16:19], v[220:223], a[172:175], v[16:19]\n"
"buffer_store_dwordx4 v[132:135], v243, s[32:35], s16 offen offset:1024 sc1\n"
"v_subrev_f32 v149, s22, v149\n"
"v_subrev_f32 v150, s22, v150\n"
"v_subrev_f32 v164, s22, v164\n"
"v_cvt_pk_bf16_f32 v164, v164, v165\n"
"v_subrev_f32 v153, s22, v153\n"
"v_subrev_f32 v155, s22, v155\n"
"v_mfma_f32_16x16x32_bf16 v[16:19], v[216:219], a[168:171], v[16:19]\n"
"v_cvt_pk_bf16_f32 v148, v148, v149\n"
"v_subrev_f32 v154, s22, v154\n"
"v_mfma_f32_16x16x32_bf16 v[16:19], v[212:215], a[164:167], v[16:19]\n"
"v_cvt_pk_bf16_f32 v149, v150, v151\n"
"v_subrev_f32 v170, s22, v170\n"
"v_mfma_f32_16x16x32_bf16 v[16:19], v[208:211], a[160:163], v[16:19]\n"
"v_subrev_f32 v166, s22, v166\n"
"v_subrev_f32 v171, s22, v171\n"
"v_mfma_f32_16x16x32_bf16 v[16:19], v[192:195], a[124:127], v[16:19]\n"
"v_cvt_pk_bf16_f32 v165, v166, v167\n"
"v_cvt_pk_bf16_f32 v167, v170, v171\n"
"v_mfma_f32_16x16x32_bf16 v[16:19], v[188:191], a[120:123], v[16:19]\n"
"v_subrev_f32 v152, s22, v152\n"
"v_cvt_pk_bf16_f32 v151, v154, v155\n"
"v_mfma_f32_16x16x32_bf16 v[16:19], v[184:187], a[116:119], v[16:19]\n"
"v_cvt_pk_bf16_f32 v150, v152, v153\n"
"v_subrev_f32 v169, s22, v169\n"
"v_mfma_f32_16x16x32_bf16 v[16:19], v[180:183], a[112:115], v[16:19]\n"
"s_waitcnt vmcnt(16)\n"
"buffer_store_dwordx4 v[148:151], v243, s[32:35], s16 offen offset:3072 sc1\n"
"v_cvt_pk_bf16_f32 v166, v168, v169\n"
"v_mfma_f32_16x16x32_bf16 v[32:35], v[180:183], a[128:131], v[32:35]\n"
"buffer_store_dwordx4 v[164:167], v243, s[32:35], s17 offen offset:1024 sc1\n"
"ds_read_b64_tr_b16 v[120:121], v229 offset:24576\n"
"v_mfma_f32_16x16x32_bf16 v[32:35], v[184:187], a[132:135], v[32:35]\n"
"ds_read_b64_tr_b16 v[224:225], v233 offset:24576\n"
"v_xor_b32 v234, s75, v234\n"
"v_mfma_f32_16x16x32_bf16 v[32:35], v[188:191], a[136:139], v[32:35]\n"
"ds_read_b64_tr_b16 v[122:123], v229 offset:26624\n"
"v_lshlrev_b32 v203, 16, v85\n"
"v_mfma_f32_16x16x32_bf16 v[32:35], v[192:195], a[140:143], v[32:35]\n"
"ds_read_b64_tr_b16 v[108:109], v233 offset:8192\n"
"v_lshlrev_b32 v202, 16, v84\n"
"v_mfma_f32_16x16x32_bf16 v[32:35], v[208:211], a[176:179], v[32:35]\n"
"ds_read_b64_tr_b16 v[226:227], v233 offset:26624\n"
"v_and_b32 v84, s73, v84\n"
"v_mfma_f32_16x16x32_bf16 v[32:35], v[212:215], a[180:183], v[32:35]\n"
"ds_read_b64_tr_b16 v[110:111], v233 offset:10240\n"
"v_bfe_u32 v161, v242, 8, 2\n"
"v_mfma_f32_16x16x32_bf16 v[32:35], v[216:219], a[184:187], v[32:35]\n"
"ds_read_b64_tr_b16 v[106:107], v229 offset:10240\n"
"v_lshlrev_b32 v205, 16, v87\n"
"v_mfma_f32_16x16x32_bf16 v[32:35], v[220:223], a[188:191], v[32:35]\n"
"v_xor_b32 v230, s75, v230\n"
"v_lshlrev_b32 v204, 16, v86\n"
"v_mfma_f32_16x16x32_bf16 v[48:51], v[220:223], a[252:255], v[48:51]\n"
"ds_read_b64_tr_b16 v[220:221], v234 offset:24576\n"
"v_add_f32 v26, v26, v203\n"
"v_mfma_f32_16x16x32_bf16 v[48:51], v[216:219], a[248:251], v[48:51]\n"
"ds_read_b64_tr_b16 v[112:113], v229 offset:16384\n"
"v_bfe_u32 v160, v242, 6, 2\n"
"v_mfma_f32_16x16x32_bf16 v[48:51], v[212:215], a[244:247], v[48:51]\n"
"ds_read_b64_tr_b16 v[116:117], v233 offset:16384\n"
"v_add_f32 v24, v24, v202\n"
"v_mfma_f32_16x16x32_bf16 v[48:51], v[208:211], a[240:243], v[48:51]\n"
"ds_read_b64_tr_b16 v[118:119], v233 offset:18432\n"
"v_add_f32 v25, v25, v84\n"
"v_mfma_f32_16x16x32_bf16 v[48:51], v[192:195], a[204:207], v[48:51]\n"
"ds_read_b64_tr_b16 v[210:211], v230 offset:18432\n"
"v_add_f32 v28, v28, v204\n"
"v_mfma_f32_16x16x32_bf16 v[48:51], v[188:191], a[200:203], v[48:51]\n"
"ds_read_b64_tr_b16 v[188:189], v230 offset:8192\n"
"v_and_b32 v86, s73, v86\n"
"v_mfma_f32_16x16x32_bf16 v[48:51], v[184:187], a[196:199], v[48:51]\n"
"ds_read_b64_tr_b16 v[194:195], v234 offset:10240\n"
"v_mov_b32 v164, 0\n"
"v_mfma_f32_16x16x32_bf16 v[48:51], v[180:183], a[192:195], v[48:51]\n"
"s_waitcnt vmcnt(15)\n"
"v_xor_b32 v160, v160, v161\n"
"v_lshlrev_b32 v202, 16, v90\n"
"v_mfma_f32_16x16x32_bf16 v[4:7], v[96:99], a[96:99], v[4:7]\n"
"s_waitcnt lgkmcnt(6)\n"
"ds_read_b64_tr_b16 v[222:223], v234 offset:26624\n"
"v_lshlrev_b32 v203, 16, v91\n"
"v_mfma_f32_16x16x32_bf16 v[4:7], v[100:103], a[100:103], v[4:7]\n"
"ds_read_b64_tr_b16 v[184:185], v234 offset:0\n"
"v_lshlrev_b32 v206, 16, v88\n"
"v_mfma_f32_16x16x32_bf16 v[4:7], v[104:107], a[104:107], v[4:7]\n"
"ds_read_b64_tr_b16 v[216:217], v230 offset:24576\n"
"v_mfma_f32_16x16x32_bf16 v[4:7], v[108:111], a[108:111], v[4:7]\n"
"s_waitcnt lgkmcnt(6)\n"
"ds_read_b64_tr_b16 v[218:219], v230 offset:26624\n"
"v_and_b32 v91, s73, v91\n"
"v_mfma_f32_16x16x32_bf16 v[4:7], v[112:115], a[144:147], v[4:7]\n"
"s_waitcnt vmcnt(4)\n"
"ds_read_b64_tr_b16 v[190:191], v230 offset:10240\n"
"v_lshlrev_b32 v207, 16, v89\n"
"v_mfma_f32_16x16x32_bf16 v[4:7], v[116:119], a[148:151], v[4:7]\n"
"v_subrev_u32 v200, s21, v200\n"
"ds_read_b64_tr_b16 v[208:209], v230 offset:16384\n"
"v_mfma_f32_16x16x32_bf16 v[4:7], v[120:123], a[152:155], v[4:7]\n"
"v_cmp_lt_u32 vcc, s69, v200\n"
"ds_read_b64_tr_b16 v[182:183], v230 offset:2048\n"
"v_mfma_f32_16x16x32_bf16 v[4:7], v[224:227], a[156:159], v[4:7]\n"
"ds_read_b64_tr_b16 v[180:181], v230 offset:0\n"
"s_cbranch_vccnz LGQ_%=\nLGQK_%=:\n"
"v_mfma_f32_16x16x32_bf16 v[20:23], v[224:227], a[172:175], v[20:23]\n"
"s_bfe_u32 s20, s89, 65536\ns_lshl_b32 s20, s20, 13\ns_add_u32 s20, s20, 0\n"
"ds_read_b64_tr_b16 v[214:215], v234 offset:18432\n"
"v_and_b32 v85, s73, v85\n"
"v_mfma_f32_16x16x32_bf16 v[20:23], v[120:123], a[168:171], v[20:23]\n"
"buffer_load_dwordx4 v[124:127], v236, s[32:35], s20 offen sc1\n"
"v_mfma_f32_16x16x32_bf16 v[20:23], v[116:119], a[164:167], v[20:23]\n"
"v_and_b32 v90, s73, v90\n"
"v_add_f32 v37, v37, v90\n"
"v_mfma_f32_16x16x32_bf16 v[20:23], v[112:115], a[160:163], v[20:23]\n"
"s_bfe_u32 s20, s89, 65540\ns_lshl_b32 s20, s20, 13\ns_add_u32 s20, s20, 1048576\n"
"v_add_f32 v36, v36, v202\n"
"v_add_f32 v38, v38, v203\n"
"v_mfma_f32_16x16x32_bf16 v[20:23], v[108:111], a[124:127], v[20:23]\n"
"buffer_load_dwordx4 v[128:131], v236, s[32:35], s20 offen sc1\n"
"v_mfma_f32_16x16x32_bf16 v[20:23], v[104:107], a[120:123], v[20:23]\n"
"v_add_u32 v160, -1, v160\n"
"v_add_f32 v39, v39, v91\n"
"v_mfma_f32_16x16x32_bf16 v[20:23], v[100:103], a[116:119], v[20:23]\n"
"s_bfe_u32 s20, s89, 65544\ns_lshl_b32 s20, s20, 13\ns_add_u32 s20, s20, 2097152\n"
"v_add_f32 v32, v32, v206\n"
"v_mfma_f32_16x16x32_bf16 v[20:23], v[96:99], a[112:115], v[20:23]\n"
"buffer_load_dwordx4 v[132:135], v236, s[32:35], s20 offen sc1\n"
"v_mfma_f32_16x16x32_bf16 v[36:39], v[96:99], a[128:131], v[36:39]\n"
"v_lshlrev_b32 v204, 16, v92\n"
"v_add_f32 v40, v40, v204\n"
"v_mfma_f32_16x16x32_bf16 v[36:39], v[100:103], a[132:135], v[36:39]\n"
"s_bfe_u32 s20, s89, 65548\ns_lshl_b32 s20, s20, 13\ns_add_u32 s20, s20, 3145728\n"
"ds_read_b64_tr_b16 v[212:213], v234 offset:16384\n"
"v_add_f32 v30, v30, v205\n"
"v_mfma_f32_16x16x32_bf16 v[36:39], v[104:107], a[136:139], v[36:39]\n"
"buffer_load_dwordx4 v[136:139], v236, s[32:35], s20 offen sc1\n"
"v_mfma_f32_16x16x32_bf16 v[36:39], v[108:111], a[140:143], v[36:39]\n"
"v_and_b32 v92, s73, v92\n"
"v_lshlrev_b32 v206, 16, v94\n"
"v_mfma_f32_16x16x32_bf16 v[36:39], v[112:115], a[176:179], v[36:39]\n"
"s_bfe_u32 s20, s89, 65552\ns_lshl_b32 s20, s20, 13\ns_add_u32 s20, s20, 4194304\n"
"ds_read_b64_tr_b16 v[186:187], v234 offset:2048\n"
"ds_read_b64_tr_b16 v[192:193], v234 offset:8192\n"
"v_mfma_f32_16x16x32_bf16 v[36:39], v[116:119], a[180:183], v[36:39]\n"
"buffer_load_dwordx4 v[140:143], v236, s[32:35], s20 offen sc1\n"
"v_mfma_f32_16x16x32_bf16 v[36:39], v[120:123], a[184:187], v[36:39]\n"
"v_and_b32 v94, s73, v94\n"
"v_mfma_f32_16x16x32_bf16 v[36:39], v[224:227], a[188:191], v[36:39]\n"
"s_bfe_u32 s20, s89, 65556\ns_lshl_b32 s20, s20, 13\ns_add_u32 s20, s20, 5242880\n"
"v_add_f32 v34, v34, v207\n"
"v_lshlrev_b32 v205, 16, v93\n"
"v_mfma_f32_16x16x32_bf16 v[52:55], v[224:227], a[252:255], v[52:55]\n"
"buffer_load_dwordx4 v[144:147], v236, s[32:35], s20 offen sc1\n"
"v_mfma_f32_16x16x32_bf16 v[52:55], v[120:123], a[248:251], v[52:55]\n"
"v_ashrrev_i32 v160, 31, v160\n"
"v_add_f32 v44, v44, v206\n"
"v_mfma_f32_16x16x32_bf16 v[52:55], v[116:119], a[244:247], v[52:55]\n"
"s_bfe_u32 s20, s89, 65560\ns_lshl_b32 s20, s20, 13\ns_add_u32 s20, s20, 6291456\n"
"v_xor_b32 v231, s75, v231\n"
"v_lshlrev_b32 v207, 16, v95\n"
"v_mfma_f32_16x16x32_bf16 v[52:55], v[112:115], a[240:243], v[52:55]\n"
"buffer_load_dwordx4 v[148:151], v236, s[32:35], s20 offen sc1\n"
"v_mfma_f32_16x16x32_bf16 v[52:55], v[108:111], a[204:207], v[52:55]\n"
"s_bfe_u32 s20, s89, 65564\ns_lshl_b32 s20, s20, 13\ns_add_u32 s20, s20, 7340032\n"
"v_and_b32 v87, s73, v87\n"
"v_and_b32 v95, s73, v95\n"
"v_mfma_f32_16x16x32_bf16 v[52:55], v[104:107], a[200:203], v[52:55]\n"
"buffer_load_dwordx4 v[152:155], v236, s[32:35], s20 offen sc1\n"
"v_mfma_f32_16x16x32_bf16 v[52:55], v[100:103], a[196:199], v[52:55]\n"
"s_waitcnt vmcnt(8)\n"
"s_add_u32 s21, s21, 1\n"
"v_and_b32 v88, s73, v88\n"
"v_mfma_f32_16x16x32_bf16 v[52:55], v[96:99], a[192:195], v[52:55]\n"
"s_barrier\n"
"s_mov_b32 s52, 40960\n"
"buffer_store_dword v241, v248, s[84:87], 0 offen sc1\n"
"s_waitcnt lgkmcnt(0)\n"
"v_mfma_f32_16x16x32_bf16 v[8:11], v[180:183], a[96:99], v[8:11]\n"
"s_mov_b32 s53, 81920\n"
"ds_read_b64_tr_b16 v[98:99], v231 offset:2048\n"
"v_add_f32 v42, v42, v205\n"
"v_mfma_f32_16x16x32_bf16 v[8:11], v[184:187], a[100:103], v[8:11]\n"
"ds_read_b64_tr_b16 v[112:113], v231 offset:16384\n"
"v_add_f32 v31, v31, v87\n"
"v_mfma_f32_16x16x32_bf16 v[8:11], v[188:191], a[104:107], v[8:11]\n"
"v_add_f32 v29, v29, v86\n"
"v_and_b32 v161, 16, v242\n"
"v_mfma_f32_16x16x32_bf16 v[8:11], v[192:195], a[108:111], v[8:11]\n"
"ds_read_b64_tr_b16 v[118:119], v235 offset:18432\n"
"v_and_b32 v160, 0x3f80, v160\n"
"v_mfma_f32_16x16x32_bf16 v[8:11], v[208:211], a[144:147], v[8:11]\n"
"ds_read_b64_tr_b16 v[102:103], v235 offset:2048\n"
"v_add_f32 v27, v27, v85\n"
"v_mfma_f32_16x16x32_bf16 v[8:11], v[212:215], a[148:151], v[8:11]\n"
"ds_read_b64_tr_b16 v[106:107], v231 offset:10240\n"
"v_lshlrev_b32 v160, v161, v160\n"
"v_mfma_f32_16x16x32_bf16 v[8:11], v[216:219], a[152:155], v[8:11]\n"
"ds_read_b64_tr_b16 v[96:97], v231 offset:0\n"
"v_and_b32 v93, s73, v93\n"
"v_mfma_f32_16x16x32_bf16 v[8:11], v[220:223], a[156:159], v[8:11]\n"
"ds_read_b64_tr_b16 v[108:109], v235 offset:8192\n"
"v_add_f32 v41, v41, v92\n"
"v_mfma_f32_16x16x32_bf16 v[24:27], v[220:223], a[172:175], v[24:27]\n"
"v_bfe_i32 v161, v242, 5, 1\n"
"v_lshlrev_b32 v206, 16, v244\n"
"v_mfma_f32_16x16x32_bf16 v[24:27], v[216:219], a[168:171], v[24:27]\n"
"ds_read_b64_tr_b16 v[114:115], v231 offset:18432\n"
"v_mfma_f32_16x16x32_bf16 v[24:27], v[212:215], a[164:167], v[24:27]\n"
"ds_read_b64_tr_b16 v[104:105], v231 offset:8192\n"
"v_lshlrev_b32 v203, 16, v247\n"
"v_mfma_f32_16x16x32_bf16 v[24:27], v[208:211], a[160:163], v[24:27]\n"
"ds_read_b64_tr_b16 v[116:117], v235 offset:16384\n"
"v_add_f32 v48, v48, v206\n"
"v_mfma_f32_16x16x32_bf16 v[24:27], v[192:195], a[124:127], v[24:27]\n"
"ds_read_b64_tr_b16 v[110:111], v235 offset:10240\n"
"v_and_b32 v89, s73, v89\n"
"v_mfma_f32_16x16x32_bf16 v[24:27], v[188:191], a[120:123], v[24:27]\n"
"v_add_f32 v45, v45, v94\n"
"v_add_f32 v47, v47, v95\n"
"v_mfma_f32_16x16x32_bf16 v[24:27], v[184:187], a[116:119], v[24:27]\n"
"ds_read_b64_tr_b16 v[100:101], v235 offset:0\n"
"v_add_f32 v43, v43, v93\n"
"v_mfma_f32_16x16x32_bf16 v[24:27], v[180:183], a[112:115], v[24:27]\n"
"ds_read_b64_tr_b16 v[122:123], v231 offset:26624\n"
"v_add_f32 v46, v46, v207\n"
"v_mfma_f32_16x16x32_bf16 v[40:43], v[180:183], a[128:131], v[40:43]\n"
"v_add_f32 v33, v33, v88\n"
"v_and_b32 v244, s73, v244\n"
"v_mfma_f32_16x16x32_bf16 v[40:43], v[184:187], a[132:135], v[40:43]\n"
"ds_read_b64_tr_b16 v[120:121], v231 offset:24576\n"
"v_lshlrev_b32 v207, 16, v245\n"
"v_mfma_f32_16x16x32_bf16 v[40:43], v[188:191], a[136:139], v[40:43]\n"
"v_and_b32 v163, v160, v161\n"
"v_lshlrev_b32 v202, 16, v246\n"
"v_mfma_f32_16x16x32_bf16 v[40:43], v[192:195], a[140:143], v[40:43]\n"
"ds_read_b64_tr_b16 v[224:225], v235 offset:24576\n"
"v_and_b32 v246, s73, v246\n"
"v_mfma_f32_16x16x32_bf16 v[40:43], v[208:211], a[176:179], v[40:43]\n"
"v_add_f32 v52, v52, v202\n"
"v_and_b32 v247, s73, v247\n"
"v_mfma_f32_16x16x32_bf16 v[40:43], v[212:215], a[180:183], v[40:43]\n"
"ds_read_b64_tr_b16 v[226:227], v235 offset:26624\n"
"v_and_b32 v245, s73, v245\n"
"v_mfma_f32_16x16x32_bf16 v[40:43], v[216:219], a[184:187], v[40:43]\n"
"v_mov_b32 v161, 0\n"
"v_add_f32 v54, v54, v203\n"
"v_mfma_f32_16x16x32_bf16 v[40:43], v[220:223], a[188:191], v[40:43]\n"
"s_barrier\n"
"s_waitcnt lgkmcnt(3)\n"
"v_mfma_f32_16x16x32_bf16 v[12:15], v[96:99], a[96:99], v[12:15]\n"
"ds_read_b64_tr_b16 v[184:185], v232 offset:32768\n"
"v_add_f32 v49, v49, v244\n"
"v_mfma_f32_16x16x32_bf16 v[12:15], v[100:103], a[100:103], v[12:15]\n"
"ds_read_b64_tr_b16 v[216:217], v228 offset:57344\n"
"v_mfma_f32_16x16x32_bf16 v[12:15], v[104:107], a[104:107], v[12:15]\n"
"ds_read_b64_tr_b16 v[188:189], v228 offset:40960\n"
"v_add_f32 v35, v35, v89\n"
"v_mfma_f32_16x16x32_bf16 v[12:15], v[108:111], a[108:111], v[12:15]\n"
"ds_read_b64_tr_b16 v[190:191], v228 offset:43008\n"
"v_mfma_f32_16x16x32_bf16 v[12:15], v[112:115], a[144:147], v[12:15]\n"
"ds_read_b64_tr_b16 v[192:193], v232 offset:40960\n"
"v_add_f32 v53, v53, v246\n"
"v_mfma_f32_16x16x32_bf16 v[12:15], v[116:119], a[148:151], v[12:15]\n"
"s_waitcnt lgkmcnt(5)\n"
"ds_read_b64_tr_b16 v[212:213], v232 offset:49152\n"
"v_add_f32 v51, v51, v245\n"
"v_mfma_f32_16x16x32_bf16 v[12:15], v[120:123], a[152:155], v[12:15]\n"
"ds_read_b64_tr_b16 v[182:183], v228 offset:34816\n"
"v_xor_b32 v162, v160, v163\n"
"v_mfma_f32_16x16x32_bf16 v[12:15], v[224:227], a[156:159], v[12:15]\n"
"global_load_dword v200, v201, s[6:7] sc1\n"
"v_mfma_f32_16x16x32_bf16 v[28:31], v[224:227], a[172:175], v[28:31]\n"
"v_add_f32 v50, v50, v207\n"
"v_add_f32 v55, v55, v247\n"
"v_mfma_f32_16x16x32_bf16 v[28:31], v[120:123], a[168:171], v[28:31]\n"
"s_waitcnt vmcnt(9)\n"
"v_lshlrev_b32 v58, 16, v125\n"
"v_lshlrev_b32 v62, 16, v127\n"
"v_mfma_f32_16x16x32_bf16 v[28:31], v[116:119], a[164:167], v[28:31]\n"
"ds_read_b64_tr_b16 v[186:187], v232 offset:34816\n"
"ds_read_b64_tr_b16 v[214:215], v232 offset:51200\n"
"v_mfma_f32_16x16x32_bf16 v[28:31], v[112:115], a[160:163], v[28:31]\n"
"ds_read_b64_tr_b16 v[194:195], v232 offset:43008\n"
"v_and_b32 v61, s73, v126\n"
"v_mfma_f32_16x16x32_bf16 v[28:31], v[108:111], a[124:127], v[28:31]\n"
"s_waitcnt vmcnt(8)\n"
"ds_read_b64_tr_b16 v[210:211], v228 offset:51200\n"
"v_and_b32 v57, s73, v124\n"
"v_mfma_f32_16x16x32_bf16 v[28:31], v[104:107], a[120:123], v[28:31]\n"
"ds_read_b64_tr_b16 v[222:223], v232 offset:59392\n"
"v_lshlrev_b32 v56, 16, v124\n"
"v_mfma_f32_16x16x32_bf16 v[28:31], v[100:103], a[116:119], v[28:31]\n"
"ds_read_b64_tr_b16 v[180:181], v228 offset:32768\n"
"v_lshlrev_b32 v205, 16, v129\n"
"v_mfma_f32_16x16x32_bf16 v[28:31], v[96:99], a[112:115], v[28:31]\n"
"s_waitcnt vmcnt(7)\n"
"v_and_b32 v59, s73, v125\n"
"v_lshlrev_b32 v206, 16, v130\n"
"v_mfma_f32_16x16x32_bf16 v[44:47], v[96:99], a[128:131], v[44:47]\n"
"ds_read_b64_tr_b16 v[220:221], v232 offset:57344\n"
"v_lshlrev_b32 v204, 16, v128\n"
"v_mfma_f32_16x16x32_bf16 v[44:47], v[100:103], a[132:135], v[44:47]\n"
"ds_read_b64_tr_b16 v[208:209], v228 offset:49152\n"
"v_and_b32 v130, s73, v130\n"
"v_mfma_f32_16x16x32_bf16 v[44:47], v[104:107], a[136:139], v[44:47]\n"
"s_waitcnt vmcnt(6)\n"
"ds_read_b64_tr_b16 v[102:103], v233 offset:34816\n"
"v_add_f32 v58, v58, v205\n"
"v_mfma_f32_16x16x32_bf16 v[44:47], v[108:111], a[140:143], v[44:47]\n"
"v_lshlrev_b32 v207, 16, v131\n"
"v_add_f32 v62, v62, v207\n"
"v_mfma_f32_16x16x32_bf16 v[44:47], v[112:115], a[176:179], v[44:47]\n"
"ds_read_b64_tr_b16 v[218:219], v228 offset:59392\n"
"v_and_b32 v131, s73, v131\n"
"v_mfma_f32_16x16x32_bf16 v[44:47], v[116:119], a[180:183], v[44:47]\n"
"s_waitcnt vmcnt(5)\n"
"ds_read_b64_tr_b16 v[96:97], v229 offset:32768\n"
"v_lshlrev_b32 v205, 16, v135\n"
"v_mfma_f32_16x16x32_bf16 v[44:47], v[120:123], a[184:187], v[44:47]\n"
"v_lshlrev_b32 v202, 16, v132\n"
"v_lshlrev_b32 v207, 16, v137\n"
"v_mfma_f32_16x16x32_bf16 v[44:47], v[224:227], a[188:191], v[44:47]\n"
"s_waitcnt lgkmcnt(5)\n"
"ds_read_b64_tr_b16 v[120:121], v229 offset:57344\n"
"v_add_f32 v56, v56, v204\n"
"v_mfma_f32_16x16x32_bf16 v[0:3], v[180:183], a[48:51], v[0:3]\n"
"s_waitcnt vmcnt(4)\n"
"ds_read_b64_tr_b16 v[116:117], v233 offset:49152\n"
"v_add_f32 v61, v61, v130\n"
"v_mfma_f32_16x16x32_bf16 v[0:3], v[184:187], a[52:55], v[0:3]\n"
"v_lshlrev_b32 v60, 16, v126\n"
"v_lshlrev_b32 v204, 16, v134\n"
"v_mfma_f32_16x16x32_bf16 v[0:3], v[188:191], a[56:59], v[0:3]\n"
"ds_read_b64_tr_b16 v[110:111], v233 offset:43008\n"
"v_add_f32 v56, v56, v202\n"
"v_mfma_f32_16x16x32_bf16 v[0:3], v[192:195], a[60:63], v[0:3]\n"
"s_waitcnt lgkmcnt(5)\n"
"s_waitcnt vmcnt(3)\n"
"ds_read_b64_tr_b16 v[112:113], v229 offset:49152\n"
"v_and_b32 v63, s73, v127\n"
"v_mfma_f32_16x16x32_bf16 v[0:3], v[208:211], a[0:3], v[0:3]\n"
"ds_read_b64_tr_b16 v[224:225], v233 offset:57344\n"
"v_and_b32 v134, s73, v134\n"
"v_mfma_f32_16x16x32_bf16 v[0:3], v[212:215], a[4:7], v[0:3]\n"
"s_waitcnt lgkmcnt(5)\n"
"s_waitcnt vmcnt(2)\n"
"ds_read_b64_tr_b16 v[108:109], v233 offset:40960\n"
"v_and_b32 v129, s73, v129\n"
"v_mfma_f32_16x16x32_bf16 v[0:3], v[216:219], a[8:11], v[0:3]\n"
"v_add_f32 v60, v60, v206\n"
"v_add_f32 v60, v60, v204\n"
"v_mfma_f32_16x16x32_bf16 v[0:3], v[220:223], a[12:15], v[0:3]\n"
"ds_read_b64_tr_b16 v[98:99], v229 offset:34816\n"
"ds_read_b64_tr_b16 v[122:123], v229 offset:59392\n"
"v_mfma_f32_16x16x32_bf16 v[16:19], v[220:223], a[28:31], v[16:19]\n"
"v_mov_b32 v165, 0\n"
"v_add_f32 v59, v59, v129\n"
"v_mfma_f32_16x16x32_bf16 v[16:19], v[216:219], a[24:27], v[16:19]\n"
"v_add_f32 v62, v62, v205\n"
"v_lshlrev_b32 v206, 16, v136\n"
"v_mfma_f32_16x16x32_bf16 v[16:19], v[212:215], a[20:23], v[16:19]\n"
"ds_read_b64_tr_b16 v[104:105], v229 offset:40960\n"
"ds_read_b64_tr_b16 v[226:227], v233 offset:59392\n"
"v_mfma_f32_16x16x32_bf16 v[16:19], v[208:211], a[16:19], v[16:19]\n"
"v_add_f32 v56, v56, v206\n"
"v_lshlrev_b32 v202, 16, v138\n"
"v_mfma_f32_16x16x32_bf16 v[16:19], v[192:195], a[76:79], v[16:19]\n"
"s_waitcnt vmcnt(0)\n"
"ds_read_b64_tr_b16 v[118:119], v233 offset:51200\n"
"v_subrev_f32 v2, s22, v2\n"
"v_mfma_f32_16x16x32_bf16 v[16:19], v[188:191], a[72:75], v[16:19]\n"
"v_subrev_u32 v200, s21, v200\n"
"ds_read_b64_tr_b16 v[114:115], v229 offset:51200\n"
"v_mfma_f32_16x16x32_bf16 v[16:19], v[184:187], a[68:71], v[16:19]\n"
"v_cmp_lt_u32 vcc, s69, v200\n"
"ds_read_b64_tr_b16 v[100:101], v233 offset:32768\n"
"v_mfma_f32_16x16x32_bf16 v[16:19], v[180:183], a[64:67], v[16:19]\n"
"v_lshlrev_b32 v206, 16, v142\n"
"s_cbranch_vccnz LGR_%=\nLGRK_%=:\n"
"v_mfma_f32_16x16x32_bf16 v[32:35], v[180:183], a[80:83], v[32:35]\n"
"v_subrev_f32 v3, s22, v3\n"
"s_cmp_eq_u32 s93, 0\ns_cbranch_scc1 LEP_%=\nLEPK_%=:\n"
"v_mfma_f32_16x16x32_bf16 v[32:35], v[184:187], a[84:87], v[32:35]\n"
"s_bfe_u32 s20, s89, 65536\ns_lshl_b32 s20, s20, 13\ns_add_u32 s20, s20, 65536\n"
"ds_read_b64_tr_b16 v[106:107], v229 offset:43008\n"
"v_and_b32 v128, s73, v128\n"
"v_mfma_f32_16x16x32_bf16 v[32:35], v[188:191], a[88:91], v[32:35]\n"
"buffer_load_dwordx4 v[72:75], v236, s[32:35], s20 offen sc1\n"
"v_mfma_f32_16x16x32_bf16 v[32:35], v[192:195], a[92:95], v[32:35]\n"
"v_lshlrev_b32 v203, 16, v133\n"
"v_lshlrev_b32 v205, 16, v141\n"
"v_mfma_f32_16x16x32_bf16 v[32:35], v[208:211], a[32:35], v[32:35]\n"
"s_bfe_u32 s20, s89, 65540\ns_lshl_b32 s20, s20, 13\ns_add_u32 s20, s20, 1114112\n"
"v_lshlrev_b32 v204, 16, v140\n"
"v_add_f32 v56, v56, v204\n"
"v_mfma_f32_16x16x32_bf16 v[32:35], v[212:215], a[36:39], v[32:35]\n"
"buffer_load_dwordx4 v[76:79], v236, s[32:35], s20 offen sc1\n"
"v_mfma_f32_16x16x32_bf16 v[32:35], v[216:219], a[40:43], v[32:35]\n"
"v_and_b32 v136, s73, v136\n"
"v_subrev_f32 v17, s22, v17\n"
"v_mfma_f32_16x16x32_bf16 v[32:35], v[220:223], a[44:47], v[32:35]\n"
"s_bfe_u32 s20, s89, 65544\ns_lshl_b32 s20, s20, 13\ns_add_u32 s20, s20, 2162688\n"
"v_add_f32 v61, v61, v134\n"
"v_and_b32 v141, s73, v141\n"
"v_mfma_f32_16x16x32_bf16 v[48:51], v[220:223], a[228:231], v[48:51]\n"
"buffer_load_dwordx4 v[80:83], v236, s[32:35], s20 offen sc1\n"
"v_mfma_f32_16x16x32_bf16 v[48:51], v[216:219], a[224:227], v[48:51]\n"
"v_add_f32 v58, v58, v203\n"
"v_subrev_f32 v19, s22, v19\n"
"v_mfma_f32_16x16x32_bf16 v[48:51], v[212:215], a[212:215], v[48:51]\n"
"s_bfe_u32 s20, s89, 65548\ns_lshl_b32 s20, s20, 13\ns_add_u32 s20, s20, 3211264\n"
"v_and_b32 v140, s73, v140\n"
"v_lshlrev_b32 v204, 16, v146\n"
"v_mfma_f32_16x16x32_bf16 v[48:51], v[208:211], a[208:211], v[48:51]\n"
"buffer_load_dwordx4 v[84:87], v236, s[32:35], s20 offen sc1\n"
"v_mfma_f32_16x16x32_bf16 v[48:51], v[192:195], a[236:239], v[48:51]\n"
"v_add_f32 v57, v57, v128\n"
"v_subrev_f32 v34, s22, v34\n"
"v_mfma_f32_16x16x32_bf16 v[48:51], v[188:191], a[232:235], v[48:51]\n"
"s_bfe_u32 s20, s89, 65552\ns_lshl_b32 s20, s20, 13\ns_add_u32 s20, s20, 4259840\n"
"ds_read_b64_tr_b16 v[214:215], v234 offset:51200\n"
"v_and_b32 v132, s73, v132\n"
"v_mfma_f32_16x16x32_bf16 v[48:51], v[184:187], a[220:223], v[48:51]\n"
"buffer_load_dwordx4 v[88:91], v236, s[32:35], s20 offen sc1\n"
"v_mfma_f32_16x16x32_bf16 v[48:51], v[180:183], a[216:219], v[48:51]\n"
"s_waitcnt lgkmcnt(1)\n"
"v_add_f32 v60, v60, v202\n"
"v_add_f32 v60, v60, v206\n"
"v_mfma_f32_16x16x32_bf16 v[4:7], v[96:99], a[48:51], v[4:7]\n"
"s_bfe_u32 s20, s89, 65556\ns_lshl_b32 s20, s20, 13\ns_add_u32 s20, s20, 5308416\n"
"v_lshlrev_b32 v206, 16, v148\n"
"v_subrev_f32 v35, s22, v35\n"
"v_mfma_f32_16x16x32_bf16 v[4:7], v[100:103], a[52:55], v[4:7]\n"
"buffer_load_dwordx4 v[92:95], v236, s[32:35], s20 offen sc1\n"
"v_mfma_f32_16x16x32_bf16 v[4:7], v[104:107], a[56:59], v[4:7]\n"
"v_add_f32 v60, v60, v204\n"
"v_subrev_f32 v0, s22, v0\n"
"v_mfma_f32_16x16x32_bf16 v[4:7], v[108:111], a[60:63], v[4:7]\n"
"s_bfe_u32 s20, s89, 65560\ns_lshl_b32 s20, s20, 13\ns_add_u32 s20, s20, 6356992\n"
"v_add_f32 v57, v57, v132\n"
"v_subrev_f32 v33, s22, v33\n"
"v_mfma_f32_16x16x32_bf16 v[4:7], v[112:115], a[0:3], v[4:7]\n"
"buffer_load_dwordx4 v[244:247], v236, s[32:35], s20 offen sc1\n"
"v_mfma_f32_16x16x32_bf16 v[4:7], v[116:119], a[4:7], v[4:7]\n"
"v_and_b32 v138, s73, v138\n"
"v_subrev_f32 v18, s22, v18\n"
"v_mfma_f32_16x16x32_bf16 v[4:7], v[120:123], a[8:11], v[4:7]\n"
"s_bfe_u32 s20, s89, 65564\ns_lshl_b32 s20, s20, 13\ns_add_u32 s20, s20, 7405568\n"
"v_and_b32 v135, s73, v135\n"
"v_subrev_f32 v49, s22, v49\n"
"v_mfma_f32_16x16x32_bf16 v[4:7], v[224:227], a[12:15], v[4:7]\n"
"buffer_load_dwordx4 v[156:159], v236, s[32:35], s20 offen sc1\n"
"v_add_f32 v58, v58, v207\n"
"v_lshlrev_b32 v203, 16, v139\n"
"v_add_f32 v58, v58, v205\n"
"v_and_b32 v142, s73, v142\n"
"v_lshlrev_b32 v202, 16, v144\n"
"v_add_f32 v56, v56, v202\n"
"v_add_f32 v56, v56, v206\n"
"v_subrev_f32 v1, s22, v1\n"
"v_mfma_f32_16x16x32_bf16 v[20:23], v[224:227], a[28:31], v[20:23]\n"
"v_add_f32 v63, v63, v131\n"
"v_lshlrev_b32 v204, 16, v152\n"
"v_mfma_f32_16x16x32_bf16 v[20:23], v[120:123], a[24:27], v[20:23]\n"
"v_and_b32 v133, s73, v133\n"
"v_add_f32 v59, v59, v133\n"
"v_mfma_f32_16x16x32_bf16 v[20:23], v[116:119], a[20:23], v[20:23]\n"
"ds_read_b64_tr_b16 v[218:219], v230 offset:59392\n"
"v_and_b32 v137, s73, v137\n"
"v_mfma_f32_16x16x32_bf16 v[20:23], v[112:115], a[16:19], v[20:23]\n"
"v_and_b32 v148, s73, v148\n"
"v_subrev_f32 v16, s22, v16\n"
"v_mfma_f32_16x16x32_bf16 v[20:23], v[108:111], a[76:79], v[20:23]\n"
"v_add_f32 v62, v62, v203\n"
"v_subrev_f32 v6, s22, v6\n"
"v_mfma_f32_16x16x32_bf16 v[20:23], v[104:107], a[72:75], v[20:23]\n"
"v_add_f32 v57, v57, v136\n"
"v_lshlrev_b32 v205, 16, v147\n"
"v_mfma_f32_16x16x32_bf16 v[20:23], v[100:103], a[68:71], v[20:23]\n"
"ds_read_b64_tr_b16 v[194:195], v234 offset:43008\n"
"v_subrev_f32 v32, s22, v32\n"
"v_mfma_f32_16x16x32_bf16 v[20:23], v[96:99], a[64:67], v[20:23]\n"
"ds_read_b64_tr_b16 v[184:185], v234 offset:32768\n"
"v_add_f32 v59, v59, v137\n"
"v_and_b32 v139, s73, v139\n"
"v_add_f32 v59, v59, v141\n"
"v_lshlrev_b32 v207, 16, v143\n"
"v_lshlrev_b32 v203, 16, v145\n"
"v_and_b32 v145, s73, v145\n"
"v_add_f32 v58, v58, v203\n"
"v_add_f32 v59, v59, v145\n"
"v_and_b32 v147, s73, v147\n"
"v_lshlrev_b32 v203, 16, v151\n"
"v_subrev_f32 v4, s22, v4\n"
"v_subrev_f32 v5, s22, v5\n"
"v_subrev_f32 v7, s22, v7\n"
"v_cvt_pk_bf16_f32 v0, v0, v1\n"
"v_cvt_pk_bf16_f32 v1, v2, v3\n"
"v_cvt_pk_bf16_f32 v3, v6, v7\n"
"v_mfma_f32_16x16x32_bf16 v[36:39], v[96:99], a[80:83], v[36:39]\n"
"v_add_f32 v56, v56, v204\n"
"v_cvt_pk_bf16_f32 v2, v4, v5\n"
"v_mfma_f32_16x16x32_bf16 v[36:39], v[100:103], a[84:87], v[36:39]\n"
"ds_read_b64_tr_b16 v[192:193], v234 offset:40960\n"
"v_add_f32 v61, v61, v138\n"
"v_mfma_f32_16x16x32_bf16 v[36:39], v[104:107], a[88:91], v[36:39]\n"
"v_add_f32 v57, v57, v140\n"
"v_subrev_f32 v23, s22, v23\n"
"v_mfma_f32_16x16x32_bf16 v[36:39], v[108:111], a[92:95], v[36:39]\n"
"ds_read_b64_tr_b16 v[180:181], v230 offset:32768\n"
"v_and_b32 v144, s73, v144\n"
"v_mfma_f32_16x16x32_bf16 v[36:39], v[112:115], a[32:35], v[36:39]\n"
"ds_read_b64_tr_b16 v[210:211], v230 offset:51200\n"
"v_add_f32 v62, v62, v207\n"
"v_add_f32 v62, v62, v205\n"
"v_lshlrev_b32 v207, 16, v149\n"
"v_add_f32 v58, v58, v207\n"
"v_lshlrev_b32 v202, 16, v150\n"
"v_permlane16_swap_b32 v0, v2\n"
"v_permlane16_swap_b32 v1, v3\n"
"v_subrev_f32 v20, s22, v20\n"
"v_cvt_pk_bf16_f32 v16, v16, v17\n"
"v_mfma_f32_16x16x32_bf16 v[36:39], v[116:119], a[36:39], v[36:39]\n"
"ds_read_b64_tr_b16 v[186:187], v234 offset:34816\n"
"v_add_f32 v63, v63, v135\n"
"v_mfma_f32_16x16x32_bf16 v[36:39], v[120:123], a[40:43], v[36:39]\n"
"ds_read_b64_tr_b16 v[188:189], v230 offset:40960\n"
"buffer_store_dwordx4 v[0:3], v238, s[36:39], 0 offen sc0 sc1\n"
"v_and_b32 v143, s73, v143\n"
"v_and_b32 v146, s73, v146\n"
"v_and_b32 v149, s73, v149\n"
"v_lshlrev_b32 v205, 16, v153\n"
"v_and_b32 v153, s73, v153\n"
"v_lshlrev_b32 v206, 16, v154\n"
"v_lshlrev_b32 v207, 16, v155\n"
"v_subrev_f32 v21, s22, v21\n"
"v_subrev_f32 v22, s22, v22\n"
"v_cvt_pk_bf16_f32 v17, v18, v19\n"
"v_cvt_pk_bf16_f32 v18, v20, v21\n"
"v_cvt_pk_bf16_f32 v32, v32, v33\n"
"v_mfma_f32_16x16x32_bf16 v[36:39], v[224:227], a[44:47], v[36:39]\n"
"ds_read_b64_tr_b16 v[182:183], v230 offset:34816\n"
"v_add_f32 v63, v63, v139\n"
"v_add_f32 v61, v61, v142\n"
"v_add_f32 v57, v57, v144\n"
"v_add_f32 v57, v57, v148\n"
"v_add_f32 v59, v59, v149\n"
"v_cvt_pk_bf16_f32 v19, v22, v23\n"
"v_permlane16_swap_b32 v16, v18\n"
"v_cvt_pk_bf16_f32 v33, v34, v35\n"
"v_mfma_f32_16x16x32_bf16 v[52:55], v[224:227], a[228:231], v[52:55]\n"
"ds_read_b64_tr_b16 v[190:191], v230 offset:43008\n"
"v_add_f32 v59, v59, v153\n"
"v_mfma_f32_16x16x32_bf16 v[52:55], v[120:123], a[224:227], v[52:55]\n"
"v_permlane16_swap_b32 v17, v19\n"
"v_mfma_f32_16x16x32_bf16 v[52:55], v[116:119], a[212:215], v[52:55]\n"
"ds_read_b64_tr_b16 v[208:209], v230 offset:49152\n"
"v_subrev_f32 v36, s22, v36\n"
"v_mfma_f32_16x16x32_bf16 v[52:55], v[112:115], a[208:211], v[52:55]\n"
"ds_read_b64_tr_b16 v[222:223], v234 offset:59392\n"
"buffer_store_dwordx4 v[16:19], v238, s[36:39], s52 offen sc0 sc1\n"
"v_add_f32 v63, v63, v143\n"
"v_add_f32 v60, v60, v202\n"
"v_add_f32 v62, v62, v203\n"
"v_and_b32 v152, s73, v152\n"
"v_subrev_f32 v38, s22, v38\n"
"v_subrev_f32 v39, s22, v39\n"
"v_mfma_f32_16x16x32_bf16 v[52:55], v[108:111], a[236:239], v[52:55]\n"
"v_and_b32 v155, s73, v155\n"
"v_cvt_pk_bf16_f32 v35, v38, v39\n"
"v_mfma_f32_16x16x32_bf16 v[52:55], v[104:107], a[232:235], v[52:55]\n"
"ds_read_b64_tr_b16 v[216:217], v230 offset:57344\n"
"v_subrev_f32 v37, s22, v37\n"
"v_mfma_f32_16x16x32_bf16 v[52:55], v[100:103], a[220:223], v[52:55]\n"
"v_and_b32 v150, s73, v150\n"
"v_and_b32 v154, s73, v154\n"
"v_mfma_f32_16x16x32_bf16 v[52:55], v[96:99], a[216:219], v[52:55]\n"
"s_waitcnt lgkmcnt(4)\n"
"ds_read_b64_tr_b16 v[212:213], v234 offset:49152\n"
"ds_read_b64_tr_b16 v[220:221], v234 offset:57344\n"
"v_add_f32 v63, v63, v147\n"
"v_and_b32 v151, s73, v151\n"
"v_add_f32 v63, v63, v151\n"
"v_add_f32 v57, v57, v152\n"
"v_add_f32 v58, v58, v205\n"
"v_add_f32 v60, v60, v206\n"
"v_add_f32 v62, v62, v207\n"
"v_cvt_pk_bf16_f32 v34, v36, v37\n"
"v_permlane16_swap_b32 v33, v35\n"
"v_mfma_f32_16x16x32_bf16 v[8:11], v[180:183], a[48:51], v[8:11]\n"
"v_cvt_pk_bf16_f32 v56, v56, v57\n"
"v_cvt_pk_bf16_f32 v57, v58, v59\n"
"v_mfma_f32_16x16x32_bf16 v[8:11], v[184:187], a[52:55], v[8:11]\n"
"s_waitcnt lgkmcnt(5)\n"
"v_permlane16_swap_b32 v32, v34\n"
"v_mfma_f32_16x16x32_bf16 v[8:11], v[188:191], a[56:59], v[8:11]\n"
"buffer_store_dwordx4 v[32:35], v238, s[36:39], s53 offen sc0 sc1\n"
"ds_read_b64_tr_b16 v[120:121], v231 offset:57344\n"
"v_add_f32 v61, v61, v146\n"
"v_add_f32 v61, v61, v150\n"
"v_add_f32 v61, v61, v154\n"
"v_add_f32 v63, v63, v155\n"
"v_cvt_pk_bf16_f32 v58, v60, v61\n"
"v_cvt_pk_bf16_f32 v59, v62, v63\n"
"v_mfma_f32_16x16x32_bf16 v[8:11], v[192:195], a[60:63], v[8:11]\n"
"s_waitcnt lgkmcnt(4)\n"
"v_permlane16_swap_b32 v57, v59\n"
"v_mfma_f32_16x16x32_bf16 v[8:11], v[208:211], a[0:3], v[8:11]\n"
"s_waitcnt lgkmcnt(2)\n"
"v_permlane16_swap_b32 v56, v58\n"
"v_mfma_f32_16x16x32_bf16 v[8:11], v[212:215], a[4:7], v[8:11]\n"
"buffer_store_dwordx4 v[56:59], v253, s[36:39], 0 offen sc0 sc1\n"
"v_subrev_f32 v48, s22, v48\n"
"v_subrev_f32 v50, s22, v50\n"
"v_subrev_f32 v51, s22, v51\n"
"v_mfma_f32_16x16x32_bf16 v[8:11], v[216:219], a[8:11], v[8:11]\n"
"s_waitcnt lgkmcnt(1)\n"
"ds_read_b64_tr_b16 v[112:113], v231 offset:49152\n"
"v_subrev_f32 v54, s22, v54\n"
"v_mfma_f32_16x16x32_bf16 v[8:11], v[220:223], a[12:15], v[8:11]\n"
"ds_read_b64_tr_b16 v[100:101], v235 offset:32768\n"
"v_subrev_f32 v52, s22, v52\n"
"v_mfma_f32_16x16x32_bf16 v[24:27], v[220:223], a[28:31], v[24:27]\n"
"ds_read_b64_tr_b16 v[102:103], v235 offset:34816\n"
"v_subrev_f32 v53, s22, v53\n"
"v_mfma_f32_16x16x32_bf16 v[24:27], v[216:219], a[24:27], v[24:27]\n"
"v_cvt_pk_bf16_f32 v48, v48, v49\n"
"v_cvt_pk_bf16_f32 v49, v50, v51\n"
"v_mfma_f32_16x16x32_bf16 v[24:27], v[212:215], a[20:23], v[24:27]\n"
"ds_read_b64_tr_b16 v[96:97], v231 offset:32768\n"
"v_cvt_pk_bf16_f32 v50, v52, v53\n"
"v_mfma_f32_16x16x32_bf16 v[24:27], v[208:211], a[16:19], v[24:27]\n"
"s_nop 0\n"
"v_permlane16_swap_b32 v48, v50\n"
"v_mfma_f32_16x16x32_bf16 v[24:27], v[192:195], a[76:79], v[24:27]\n"
"ds_read_b64_tr_b16 v[116:117], v235 offset:49152\n"
"v_subrev_f32 v55, s22, v55\n"
"v_mfma_f32_16x16x32_bf16 v[24:27], v[188:191], a[72:75], v[24:27]\n"
"ds_read_b64_tr_b16 v[98:99], v231 offset:34816\n"
"v_cvt_pk_bf16_f32 v51, v54, v55\n"
"v_mfma_f32_16x16x32_bf16 v[24:27], v[184:187], a[68:71], v[24:27]\n"
"s_nop 0\n"
"v_permlane16_swap_b32 v49, v51\n"
"v_mfma_f32_16x16x32_bf16 v[24:27], v[180:183], a[64:67], v[24:27]\n"
"buffer_store_dwordx4 v[48:51], v252, s[36:39], 0 offen sc0 sc1\n"
"ds_read_b64_tr_b16 v[104:105], v231 offset:40960\n"
"v_mfma_f32_16x16x32_bf16 v[40:43], v[180:183], a[80:83], v[40:43]\n"
"ds_read_b64_tr_b16 v[106:107], v231 offset:43008\n"
"ds_read_b64_tr_b16 v[108:109], v235 offset:40960\n"
"v_mfma_f32_16x16x32_bf16 v[40:43], v[184:187], a[84:87], v[40:43]\n"
"ds_read_b64_tr_b16 v[110:111], v235 offset:43008\n"
"v_mfma_f32_16x16x32_bf16 v[40:43], v[188:191], a[88:91], v[40:43]\n"
"ds_read_b64_tr_b16 v[114:115], v231 offset:51200\n"
"v_subrev_f32 v10, s22, v10\n"
"v_mfma_f32_16x16x32_bf16 v[40:43], v[192:195], a[92:95], v[40:43]\n"
"ds_read_b64_tr_b16 v[118:119], v235 offset:51200\n"
"v_mfma_f32_16x16x32_bf16 v[40:43], v[208:211], a[32:35], v[40:43]\n"
"ds_read_b64_tr_b16 v[122:123], v231 offset:59392\n"
"v_subrev_f32 v24, s22, v24\n"
"v_mfma_f32_16x16x32_bf16 v[40:43], v[212:215], a[36:39], v[40:43]\n"
"ds_read_b64_tr_b16 v[224:225], v235 offset:57344\n"
"ds_read_b64_tr_b16 v[226:227], v235 offset:59392\n"
"v_mfma_f32_16x16x32_bf16 v[40:43], v[216:219], a[40:43], v[40:43]\n"
"v_mfma_f32_16x16x32_bf16 v[40:43], v[220:223], a[44:47], v[40:43]\n"
"s_waitcnt lgkmcnt(8)\n"
"v_mov_b32 v160, 0\n"
"v_subrev_f32 v11, s22, v11\n"
"v_mfma_f32_16x16x32_bf16 v[12:15], v[96:99], a[48:51], v[12:15]\n"
"v_subrev_f32 v8, s22, v8\n"
"v_subrev_f32 v9, s22, v9\n"
"v_mfma_f32_16x16x32_bf16 v[12:15], v[100:103], a[52:55], v[12:15]\n"
"s_waitcnt lgkmcnt(5)\n"
"v_cvt_pk_bf16_f32 v8, v8, v9\n"
"v_mfma_f32_16x16x32_bf16 v[12:15], v[104:107], a[56:59], v[12:15]\n"
"v_subrev_f32 v27, s22, v27\n"
"v_mfma_f32_16x16x32_bf16 v[12:15], v[108:111], a[60:63], v[12:15]\n"
"s_waitcnt lgkmcnt(3)\n"
"v_mfma_f32_16x16x32_bf16 v[12:15], v[112:115], a[0:3], v[12:15]\n"
"v_mfma_f32_16x16x32_bf16 v[12:15], v[116:119], a[4:7], v[12:15]\n"
"s_waitcnt lgkmcnt(0)\n"
"v_mfma_f32_16x16x32_bf16 v[12:15], v[120:123], a[8:11], v[12:15]\n"
"v_subrev_f32 v40, s22, v40\n"
"v_mfma_f32_16x16x32_bf16 v[12:15], v[224:227], a[12:15], v[12:15]\n"
"s_waitcnt vmcnt(5)\n"
"v_mfma_f32_16x16x32_bf16 v[64:67], v[162:165], v[72:75], 0\n"
"v_mfma_f32_16x16x32_bf16 v[64:67], v[162:165], v[76:79], v[64:67]\n"
"v_cvt_pk_bf16_f32 v9, v10, v11\n"
"v_mfma_f32_16x16x32_bf16 v[64:67], v[162:165], v[80:83], v[64:67]\n"
"s_nop 2\n"
"v_subrev_f32 v12, s22, v12\n"
"v_mfma_f32_16x16x32_bf16 v[64:67], v[162:165], v[84:87], v[64:67]\n"
"v_subrev_f32 v25, s22, v25\n"
"v_mfma_f32_16x16x32_bf16 v[64:67], v[162:165], v[88:91], v[64:67]\n"
"v_subrev_f32 v13, s22, v13\n"
"v_mfma_f32_16x16x32_bf16 v[64:67], v[162:165], v[92:95], v[64:67]\n"
"v_subrev_f32 v15, s22, v15\n"
"v_mfma_f32_16x16x32_bf16 v[64:67], v[162:165], v[244:247], v[64:67]\n"
"v_subrev_f32 v14, s22, v14\n"
"v_mfma_f32_16x16x32_bf16 v[64:67], v[162:165], v[156:159], v[64:67]\n"
"v_subrev_f32 v42, s22, v42\n"
"v_mfma_f32_16x16x32_bf16 v[68:71], v[160:163], v[72:75], 0\n"
"v_cvt_pk_bf16_f32 v11, v14, v15\n"
"v_subrev_f32 v41, s22, v41\n"
"v_mfma_f32_16x16x32_bf16 v[68:71], v[160:163], v[76:79], v[68:71]\n"
"v_cvt_pk_bf16_f32 v10, v12, v13\n"
"v_cvt_pk_bf16_f32 v24, v24, v25\n"
"v_mfma_f32_16x16x32_bf16 v[68:71], v[160:163], v[80:83], v[68:71]\n"
"v_mfma_f32_16x16x32_bf16 v[68:71], v[160:163], v[84:87], v[68:71]\n"
"v_permlane16_swap_b32 v9, v11\n"
"v_mfma_f32_16x16x32_bf16 v[68:71], v[160:163], v[88:91], v[68:71]\n"
"v_mfma_f32_16x16x32_bf16 v[68:71], v[160:163], v[92:95], v[68:71]\n"
"v_cvt_pk_bf16_f32 v64, v64, v65\n"
"v_cvt_pk_bf16_f32 v40, v40, v41\n"
"v_mfma_f32_16x16x32_bf16 v[68:71], v[160:163], v[244:247], v[68:71]\n"
"v_permlane16_swap_b32 v8, v10\n"
"v_mfma_f32_16x16x32_bf16 v[68:71], v[160:163], v[156:159], v[68:71]\n"
"buffer_store_dwordx4 v[8:11], v239, s[36:39], 0 offen sc0 sc1\n"
"v_mfma_f32_16x16x32_bf16 v[28:31], v[224:227], a[28:31], v[28:31]\n"
"v_mfma_f32_16x16x32_bf16 v[28:31], v[120:123], a[24:27], v[28:31]\n"
"v_cvt_pk_bf16_f32 v65, v66, v67\n"
"v_subrev_f32 v43, s22, v43\n"
"v_mfma_f32_16x16x32_bf16 v[28:31], v[116:119], a[20:23], v[28:31]\n"
"v_mfma_f32_16x16x32_bf16 v[28:31], v[112:115], a[16:19], v[28:31]\n"
"v_mfma_f32_16x16x32_bf16 v[28:31], v[108:111], a[76:79], v[28:31]\n"
"v_cvt_pk_bf16_f32 v66, v68, v69\n"
"v_cvt_pk_bf16_f32 v67, v70, v71\n"
"v_mfma_f32_16x16x32_bf16 v[28:31], v[104:107], a[72:75], v[28:31]\n"
"v_permlane16_swap_b32 v64, v66\n"
"v_mfma_f32_16x16x32_bf16 v[28:31], v[100:103], a[68:71], v[28:31]\n"
"v_permlane16_swap_b32 v65, v67\n"
"v_mfma_f32_16x16x32_bf16 v[28:31], v[96:99], a[64:67], v[28:31]\n"
"buffer_store_dwordx4 v[64:67], v253, s[36:39], 0 offen offset:128 sc0 sc1\n"
"v_cvt_pk_bf16_f32 v41, v42, v43\n"
"v_mfma_f32_16x16x32_bf16 v[44:47], v[96:99], a[80:83], v[44:47]\n"
"v_subrev_f32 v26, s22, v26\n"
"v_cvt_pk_bf16_f32 v25, v26, v27\n"
"v_mfma_f32_16x16x32_bf16 v[44:47], v[100:103], a[84:87], v[44:47]\n"
"v_mfma_f32_16x16x32_bf16 v[44:47], v[104:107], a[88:91], v[44:47]\n"
"s_nop 0\n"
"v_subrev_f32 v30, s22, v30\n"
"v_subrev_f32 v31, s22, v31\n"
"v_mfma_f32_16x16x32_bf16 v[44:47], v[108:111], a[92:95], v[44:47]\n"
"v_subrev_f32 v28, s22, v28\n"
"v_subrev_f32 v29, s22, v29\n"
"v_mfma_f32_16x16x32_bf16 v[44:47], v[112:115], a[32:35], v[44:47]\n"
"v_cvt_pk_bf16_f32 v26, v28, v29\n"
"v_cvt_pk_bf16_f32 v27, v30, v31\n"
"v_mfma_f32_16x16x32_bf16 v[44:47], v[116:119], a[36:39], v[44:47]\n"
"s_nop 0\n"
"v_permlane16_swap_b32 v25, v27\n"
"v_mfma_f32_16x16x32_bf16 v[44:47], v[120:123], a[40:43], v[44:47]\n"
"v_permlane16_swap_b32 v24, v26\n"
"v_mfma_f32_16x16x32_bf16 v[44:47], v[224:227], a[44:47], v[44:47]\n"
"buffer_store_dwordx4 v[24:27], v239, s[36:39], s52 offen sc0 sc1\n"
"s_nop 6\n"
"v_subrev_f32 v44, s22, v44\n"
"v_subrev_f32 v45, s22, v45\n"
"v_subrev_f32 v46, s22, v46\n"
"v_subrev_f32 v47, s22, v47\n"
"v_cvt_pk_bf16_f32 v42, v44, v45\n"
"v_cvt_pk_bf16_f32 v43, v46, v47\n"
"s_nop 0\n"
"v_permlane16_swap_b32 v40, v42\n"
"v_permlane16_swap_b32 v41, v43\n"
"buffer_store_dwordx4 v[40:43], v239, s[36:39], s53 offen sc0 sc1\n"
"s_branch LFIN_%=\n"
"LSW_%=:\n"
"s_add_u32 s16, s8, s95\n"
"s_addc_u32 s17, s9, 0\n"
"s_sub_u32 s18, 655360, s95\n"
"s_add_u32 s20, s8, s100\n"
"s_addc_u32 s21, s9, 0\n"
"s_sub_u32 s22, 655360, s100\n"
"s_mov_b32 s56, 0\n"
"s_mov_b32 s55, 0\n"
"s_mov_b32 s58, 0\n"
"s_mov_b32 s57, 0\n"
"s_mov_b32 s59, 163840\n"
"s_mov_b32 s52, 9216\n"
"s_mov_b32 s53, 18432\n"
"s_mov_b32 s54, 27648\n"
"v_lshrrev_b32 v206, 7, v242\n"
"v_mul_u32_u24 v206, 1280, v206\n"
"v_lshrrev_b32 v240, 3, v242\n"
"v_and_b32 v240, 0x60, v240\n"
"v_and_b32 v207, 0x70, v242\n"
"v_xor_b32 v240, v240, v207\n"
"v_add_u32 v240, v206, v240\n"
"v_mov_b32 v207, v240\n"
"buffer_load_dword v252, v240, s[16:19], 0 offen\n"
"s_branch LSWK_%=\n"
"LGP_%=:\n"
"s_mov_b32 s80, 0x40000\n"
"LGPa_%=:\n"
"s_sleep 1\n"
"global_load_dword v200, v201, s[6:7] sc1\n"
"s_sub_u32 s80, s80, 1\n"
"s_cmp_eq_u32 s80, 0\n"
"s_waitcnt vmcnt(0)\n"
"s_cbranch_scc1 LGPK_%=\n"
"v_subrev_u32 v200, s21, v200\n"
"v_cmp_lt_u32 vcc, s69, v200\n"
"s_nop 4\n"
"s_cbranch_vccnz LGPa_%=\n"
"s_branch LGPK_%=\n"
"LGQ_%=:\n"
"s_mov_b32 s80, 0x40000\n"
"LGQa_%=:\n"
"s_sleep 1\n"
"global_load_dword v200, v201, s[6:7] sc1\n"
"s_sub_u32 s80, s80, 1\n"
"s_cmp_eq_u32 s80, 0\n"
"s_waitcnt vmcnt(0)\n"
"s_cbranch_scc1 LGQK_%=\n"
"v_subrev_u32 v200, s21, v200\n"
"v_cmp_lt_u32 vcc, s69, v200\n"
"s_nop 4\n"
"s_cbranch_vccnz LGQa_%=\n"
"s_branch LGQK_%=\n"
"LGR_%=:\n"
"s_mov_b32 s80, 0x40000\n"
"LGRa_%=:\n"
"s_sleep 1\n"
"global_load_dword v200, v201, s[6:7] sc1\n"
"s_sub_u32 s80, s80, 1\n"
"s_cmp_eq_u32 s80, 0\n"
"s_waitcnt vmcnt(0)\n"
"s_cbranch_scc1 LGRK_%=\n"
"v_subrev_u32 v200, s21, v200\n"
"v_cmp_lt_u32 vcc, s69, v200\n"
"s_nop 4\n"
"s_cbranch_vccnz LGRa_%=\n"
"s_branch LGRK_%=\n"
"LGV_%=:\n"
"s_mov_b32 s80, 0x40000\n"
"LGVa_%=:\n"
"s_sleep 1\n"
"global_load_dword v200, v201, s[6:7] sc1\n"
"s_sub_u32 s80, s80, 1\n"
"s_cmp_eq_u32 s80, 0\n"
"s_waitcnt vmcnt(0)\n"
"s_cbranch_scc1 LGVK_%=\n"
"v_subrev_u32 v200, s21, v200\n"
"v_cmp_lt_u32 vcc, s69, v200\n"
"s_nop 4\n"
"s_cbranch_vccnz LGVa_%=\n"
"s_branch LGVK_%=\n"
"LEP_%=:\n"
"s_add_u32 s92, s92, 1\n"
"v_mov_b32 v200, s92\n"
"v_mov_b32 v255, 0\n"
"s_mov_b64 exec, 1\n"
"global_store_dword v255, v200, s[6:7] offset:128 sc1\n"
"s_mov_b64 exec, -1\n"
"s_branch LEPK_%=\n"
"LPCE0_%=:\n"
"s_memrealtime s[80:81]\n"
"s_sub_u32 s82, s67, s66\n"
"s_cmp_gt_i32 s82, 0\n"
"s_cselect_b32 s83, s67, s66\n"
"s_waitcnt lgkmcnt(0)\n"
"s_mov_b32 s63, 0\n"
"s_mov_b32 s62, 114\n"
"s_sub_u32 s82, s83, s80\n"
"s_cmp_gt_i32 s82, 3\n"
"s_cbranch_scc0 LPEE0_%=\n"
"LPDE0_%=:\n"
"s_sleep 1\n"
"s_memrealtime s[80:81]\n"
"s_waitcnt lgkmcnt(0)\n"
"s_sub_u32 s82, s83, s80\n"
"s_cmp_gt_i32 s82, 3\n"
"s_cbranch_scc1 LPDE0_%=\n"
"LPEE0_%=:\n"
"s_add_u32 s66, s66, s72\n"
"s_add_u32 s66, s66, 4\n"
"s_add_u32 s67, s80, s72\n"
"s_sub_u32 s72, s72, 4\n"
"s_max_u32 s72, s72, s62\n"
"s_branch LPKE0_%=\n"
"LPCE1_%=:\n"
"s_memrealtime s[80:81]\n"
"s_sub_u32 s82, s67, s66\n"
"s_cmp_gt_i32 s82, 0\n"
"s_cselect_b32 s83, s67, s66\n"
"s_waitcnt lgkmcnt(0)\n"
"s_add_u32 s72, s62, 20\n"
"s_sub_u32 s82, s83, s80\n"
"s_cmp_gt_i32 s82, 3\n"
"s_cbranch_scc0 LPEE1_%=\n"
"LPDE1_%=:\n"
"s_sleep 1\n"
"s_memrealtime s[80:81]\n"
"s_waitcnt lgkmcnt(0)\n"
"s_sub_u32 s82, s83, s80\n"
"s_cmp_gt_i32 s82, 3\n"
"s_cbranch_scc1 LPDE1_%=\n"
"LPEE1_%=:\n"
"s_add_u32 s66, s66, s72\n"
"s_add_u32 s66, s66, 4\n"
"s_add_u32 s67, s80, s72\n"
"s_sub_u32 s72, s72, 4\n"
"s_max_u32 s72, s72, s62\n"
"s_branch LPKE1_%=\n"
"LPCT0_%=:\n"
"s_memrealtime s[80:81]\n"
"s_sub_u32 s82, s67, s66\n"
"s_cmp_gt_i32 s82, 0\n"
"s_cselect_b32 s83, s67, s66\n"
"s_waitcnt lgkmcnt(0)\n"
"s_sub_u32 s82, s83, s80\n"
"s_cmp_gt_i32 s82, 3\n"
"s_cbranch_scc0 LPET0_%=\n"
"LPDT0_%=:\n"
"s_sleep 1\n"
"s_memrealtime s[80:81]\n"
"s_waitcnt lgkmcnt(0)\n"
"s_sub_u32 s82, s83, s80\n"
"s_cmp_gt_i32 s82, 3\n"
"s_cbranch_scc1 LPDT0_%=\n"
"LPET0_%=:\n"
"s_add_u32 s66, s66, s72\n"
"s_add_u32 s66, s66, 4\n"
"s_add_u32 s67, s80, s72\n"
"s_sub_u32 s72, s72, 4\n"
"s_max_u32 s72, s72, s62\n"
"s_branch LPKT0_%=\n"
"LPCT1_%=:\n"
"s_memrealtime s[80:81]\n"
"s_sub_u32 s82, s67, s66\n"
"s_cmp_gt_i32 s82, 0\n"
"s_cselect_b32 s83, s67, s66\n"
"s_waitcnt lgkmcnt(0)\n"
"s_add_u32 s72, s62, 20\n"
"s_sub_u32 s82, s83, s80\n"
"s_cmp_gt_i32 s82, 3\n"
"s_cbranch_scc0 LPET1_%=\n"
"LPDT1_%=:\n"
"s_sleep 1\n"
"s_memrealtime s[80:81]\n"
"s_waitcnt lgkmcnt(0)\n"
"s_sub_u32 s82, s83, s80\n"
"s_cmp_gt_i32 s82, 3\n"
"s_cbranch_scc1 LPDT1_%=\n"
"LPET1_%=:\n"
"s_add_u32 s66, s66, s72\n"
"s_add_u32 s66, s66, 4\n"
"s_add_u32 s67, s80, s72\n"
"s_sub_u32 s72, s72, 4\n"
"s_max_u32 s72, s72, s62\n"
"s_branch LPKT1_%=\n"
"LPCL0_%=:\n"
"s_memrealtime s[80:81]\n"
"s_sub_u32 s82, s67, s66\n"
"s_cmp_gt_i32 s82, 0\n"
"s_cselect_b32 s83, s67, s66\n"
"s_waitcnt lgkmcnt(0)\n"
"s_sub_u32 s82, s83, s80\n"
"s_cmp_gt_i32 s82, 3\n"
"s_cbranch_scc0 LPEL0_%=\n"
"LPDL0_%=:\n"
"s_sleep 1\n"
"s_memrealtime s[80:81]\n"
"s_waitcnt lgkmcnt(0)\n"
"s_sub_u32 s82, s83, s80\n"
"s_cmp_gt_i32 s82, 3\n"
"s_cbranch_scc1 LPDL0_%=\n"
"LPEL0_%=:\n"
"s_add_u32 s66, s66, s72\n"
"s_add_u32 s66, s66, 4\n"
"s_add_u32 s67, s80, s72\n"
"s_sub_u32 s72, s72, 4\n"
"s_max_u32 s72, s72, s62\n"
"s_branch LPKL0_%=\n"
"LPCL1_%=:\n"
"s_memrealtime s[80:81]\n"
"s_sub_u32 s82, s67, s66\n"
"s_cmp_gt_i32 s82, 0\n"
"s_cselect_b32 s83, s67, s66\n"
"s_waitcnt lgkmcnt(0)\n"
"s_sub_u32 s82, s83, s80\n"
"s_cmp_gt_i32 s82, 3\n"
"s_cbranch_scc0 LPEL1_%=\n"
"LPDL1_%=:\n"
"s_sleep 1\n"
"s_memrealtime s[80:81]\n"
"s_waitcnt lgkmcnt(0)\n"
"s_sub_u32 s82, s83, s80\n"
"s_cmp_gt_i32 s82, 3\n"
"s_cbranch_scc1 LPDL1_%=\n"
"LPEL1_%=:\n"
"s_add_u32 s66, s66, s72\n"
"s_add_u32 s66, s66, 4\n"
"s_add_u32 s67, s80, s72\n"
"s_sub_u32 s72, s72, 4\n"
"s_max_u32 s72, s72, s62\n"
"s_branch LPKL1_%=\n"
"LPCL2_%=:\n"
"s_memrealtime s[80:81]\n"
"s_sub_u32 s82, s67, s66\n"
"s_cmp_gt_i32 s82, 0\n"
"s_cselect_b32 s83, s67, s66\n"
"s_waitcnt lgkmcnt(0)\n"
"s_sub_u32 s82, s83, s80\n"
"s_cmp_gt_i32 s82, 3\n"
"s_cbranch_scc0 LPEL2_%=\n"
"LPDL2_%=:\n"
"s_sleep 1\n"
"s_memrealtime s[80:81]\n"
"s_waitcnt lgkmcnt(0)\n"
"s_sub_u32 s82, s83, s80\n"
"s_cmp_gt_i32 s82, 3\n"
"s_cbranch_scc1 LPDL2_%=\n"
"LPEL2_%=:\n"
"s_add_u32 s66, s66, s72\n"
"s_add_u32 s66, s66, 4\n"
"s_add_u32 s67, s80, s72\n"
"s_sub_u32 s72, s72, 4\n"
"s_max_u32 s72, s72, s62\n"
"s_branch LPKL2_%=\n"
"LPCL3_%=:\n"
"s_memrealtime s[80:81]\n"
"s_sub_u32 s82, s67, s66\n"
"s_cmp_gt_i32 s82, 0\n"
"s_cselect_b32 s83, s67, s66\n"
"s_waitcnt lgkmcnt(0)\n"
"s_sub_u32 s82, s83, s80\n"
"s_cmp_gt_i32 s82, 3\n"
"s_cbranch_scc0 LPEL3_%=\n"
"LPDL3_%=:\n"
"s_sleep 1\n"
"s_memrealtime s[80:81]\n"
"s_waitcnt lgkmcnt(0)\n"
"s_sub_u32 s82, s83, s80\n"
"s_cmp_gt_i32 s82, 3\n"
"s_cbranch_scc1 LPDL3_%=\n"
"LPEL3_%=:\n"
"s_add_u32 s66, s66, s72\n"
"s_add_u32 s66, s66, 4\n"
"s_add_u32 s67, s80, s72\n"
"s_sub_u32 s72, s72, 4\n"
"s_max_u32 s72, s72, s62\n"
"s_branch LPKL3_%=\n"
"LPN_%=:\n"
"v_mfma_f32_16x16x32_bf16 v[112:115], a[192:195], a[136:139], v[112:115]\n"
"v_mfma_f32_16x16x32_bf16 v[112:115], a[196:199], a[140:143], v[112:115]\n"
"v_mfma_f32_16x16x32_bf16 v[116:119], a[200:203], a[136:139], v[116:119]\n"
"s_waitcnt vmcnt(11)\n"
"v_mfma_f32_16x16x32_bf16 v[116:119], a[204:207], a[140:143], v[116:119]\n"
"s_barrier\n"
"v_mfma_f32_16x16x32_bf16 v[120:123], a[208:211], a[136:139], v[120:123]\n"
"s_add_u32 s40, s48, 256\n"
"s_xor_b32 m0, s50, 0xc000\n"
"ds_read_b64_tr_b16 a[224:225], v244 offset:16384\n"
"v_mfma_f32_16x16x32_bf16 v[120:123], a[212:215], a[140:143], v[120:123]\n"
"buffer_load_dwordx4 v224, s[28:31], s40 offen nt lds\n"
"v_mfma_f32_16x16x32_bf16 v[124:127], a[216:219], a[136:139], v[124:127]\n"
"ds_read_b64_tr_b16 a[226:227], v244 offset:16896\n"
"v_mfma_f32_16x16x32_bf16 v[124:127], a[220:223], a[140:143], v[124:127]\n"
"ds_read_b64_tr_b16 a[228:229], v244 offset:24576\n"
"v_mfma_f32_16x16x32_bf16 v[140:143], a[216:219], a[144:147], v[140:143]\n"
"ds_read_b64_tr_b16 a[230:231], v244 offset:25088\n"
"v_mfma_f32_16x16x32_bf16 v[140:143], a[220:223], a[148:151], v[140:143]\n"
"ds_read_b64_tr_b16 a[232:233], v245 offset:16384\n"
"v_mfma_f32_16x16x32_bf16 v[136:139], a[208:211], a[144:147], v[136:139]\n"
"ds_read_b64_tr_b16 a[234:235], v245 offset:16896\n"
"v_mfma_f32_16x16x32_bf16 v[136:139], a[212:215], a[148:151], v[136:139]\n"
"buffer_load_dwordx4 v225, s[28:31], s40 offen offset:1024 nt lds\n"
"v_mfma_f32_16x16x32_bf16 v[132:135], a[200:203], a[144:147], v[132:135]\n"
"ds_read_b64_tr_b16 a[236:237], v245 offset:24576\n"
"v_mfma_f32_16x16x32_bf16 v[132:135], a[204:207], a[148:151], v[132:135]\n"
"ds_read_b64_tr_b16 a[238:239], v245 offset:25088\n"
"v_mfma_f32_16x16x32_bf16 v[128:131], a[192:195], a[144:147], v[128:131]\n"
"ds_read_b64_tr_b16 a[240:241], v244 offset:17408\n"
"v_mfma_f32_16x16x32_bf16 v[128:131], a[196:199], a[148:151], v[128:131]\n"
"ds_read_b64_tr_b16 a[242:243], v244 offset:17920\n"
"v_mfma_f32_16x16x32_bf16 v[144:147], a[192:195], a[152:155], v[144:147]\n"
"ds_read_b64_tr_b16 a[244:245], v244 offset:25600\n"
"v_mfma_f32_16x16x32_bf16 v[144:147], a[196:199], a[156:159], v[144:147]\n"
"buffer_load_dwordx4 v226, s[28:31], s40 offen offset:2048 nt lds\n"
"v_mfma_f32_16x16x32_bf16 v[148:151], a[200:203], a[152:155], v[148:151]\n"
"ds_read_b64_tr_b16 a[246:247], v244 offset:26112\n"
"v_mfma_f32_16x16x32_bf16 v[148:151], a[204:207], a[156:159], v[148:151]\n"
"ds_read_b64_tr_b16 a[248:249], v245 offset:17408\n"
"v_mfma_f32_16x16x32_bf16 v[152:155], a[208:211], a[152:155], v[152:155]\n"
"ds_read_b64_tr_b16 a[250:251], v245 offset:17920\n"
"v_mfma_f32_16x16x32_bf16 v[152:155], a[212:215], a[156:159], v[152:155]\n"
"ds_read_b64_tr_b16 a[252:253], v245 offset:25600\n"
"v_mfma_f32_16x16x32_bf16 v[156:159], a[216:219], a[152:155], v[156:159]\n"
"ds_read_b64_tr_b16 a[254:255], v245 offset:26112\n"
"v_mfma_f32_16x16x32_bf16 v[156:159], a[220:223], a[156:159], v[156:159]\n"
"buffer_load_dwordx4 v227, s[28:31], s40 offen offset:3072 nt lds\n"
"v_mfma_f32_16x16x32_bf16 v[172:175], a[216:219], a[160:163], v[172:175]\n"
"s_cmp_eq_u32 s47, 8\n"
"v_mfma_f32_16x16x32_bf16 v[172:175], a[220:223], a[164:167], v[172:175]\n"
"s_cselect_b32 s41, s52, s58\n"
"v_mfma_f32_16x16x32_bf16 v[168:171], a[208:211], a[160:163], v[168:171]\n"
"s_add_u32 s48, s48, s41\n"
"v_mfma_f32_16x16x32_bf16 v[168:171], a[212:215], a[164:167], v[168:171]\n"
"s_sub_u32 s41, s48, 5242880\n"
"v_mfma_f32_16x16x32_bf16 v[164:167], a[200:203], a[160:163], v[164:167]\n"
"s_min_u32 s48, s48, s41\n"
"v_mfma_f32_16x16x32_bf16 v[164:167], a[204:207], a[164:167], v[164:167]\n"
"v_xor_b32 v244, s74, v244\n"
"v_xor_b32 v246, s74, v246\n"
"v_mfma_f32_16x16x32_bf16 v[160:163], a[192:195], a[160:163], v[160:163]\n"
"s_mov_b32 m0, s50\n"
"v_xor_b32 v245, s74, v245\n"
"v_xor_b32 v247, s74, v247\n"
"v_mfma_f32_16x16x32_bf16 v[160:163], a[196:199], a[164:167], v[160:163]\n"
"buffer_load_dwordx4 v224, s[28:31], s48 offen nt lds\n"
"v_mfma_f32_16x16x32_bf16 v[176:179], a[192:195], a[168:171], v[176:179]\n"
"v_xor_b32 v252, s74, v252\n"
"v_xor_b32 v253, s74, v253\n"
"v_mfma_f32_16x16x32_bf16 v[176:179], a[196:199], a[172:175], v[176:179]\n"
"v_xor_b32 v250, s74, v250\n"
"v_xor_b32 v251, s74, v251\n"
"v_mfma_f32_16x16x32_bf16 v[180:183], a[200:203], a[168:171], v[180:183]\n"
"v_mfma_f32_16x16x32_bf16 v[180:183], a[204:207], a[172:175], v[180:183]\n"
"buffer_load_dwordx4 v225, s[28:31], s48 offen offset:1024 nt lds\n"
"v_mfma_f32_16x16x32_bf16 v[184:187], a[208:211], a[168:171], v[184:187]\n"
"v_mfma_f32_16x16x32_bf16 v[184:187], a[212:215], a[172:175], v[184:187]\n"
"v_mfma_f32_16x16x32_bf16 v[188:191], a[216:219], a[168:171], v[188:191]\n"
"v_mfma_f32_16x16x32_bf16 v[188:191], a[220:223], a[172:175], v[188:191]\n"
"v_mfma_f32_16x16x32_bf16 v[204:207], a[216:219], a[176:179], v[204:207]\n"
"v_mfma_f32_16x16x32_bf16 v[204:207], a[220:223], a[180:183], v[204:207]\n"
"buffer_load_dwordx4 v226, s[28:31], s48 offen offset:2048 nt lds\n"
"v_mfma_f32_16x16x32_bf16 v[200:203], a[208:211], a[176:179], v[200:203]\n"
"v_mfma_f32_16x16x32_bf16 v[200:203], a[212:215], a[180:183], v[200:203]\n"
"v_mfma_f32_16x16x32_bf16 v[196:199], a[200:203], a[176:179], v[196:199]\n"
"v_mfma_f32_16x16x32_bf16 v[196:199], a[204:207], a[180:183], v[196:199]\n"
"v_mfma_f32_16x16x32_bf16 v[192:195], a[192:195], a[176:179], v[192:195]\n"
"v_mfma_f32_16x16x32_bf16 v[192:195], a[196:199], a[180:183], v[192:195]\n"
"s_waitcnt lgkmcnt(0)\n"
"buffer_load_dwordx4 v227, s[28:31], s48 offen offset:3072 nt lds\n"
"v_mfma_f32_16x16x32_bf16 v[208:211], a[192:195], a[184:187], v[208:211]\n"
"s_xor_b32 s50, s50, s74\n"
"v_mfma_f32_16x16x32_bf16 v[208:211], a[196:199], a[188:191], v[208:211]\n"
"v_mfma_f32_16x16x32_bf16 v[212:215], a[200:203], a[184:187], v[212:215]\n"
"v_mfma_f32_16x16x32_bf16 v[212:215], a[204:207], a[188:191], v[212:215]\n"
"v_mfma_f32_16x16x32_bf16 v[216:219], a[208:211], a[184:187], v[216:219]\n"
"v_mfma_f32_16x16x32_bf16 v[216:219], a[212:215], a[188:191], v[216:219]\n"
"v_mfma_f32_16x16x32_bf16 v[220:223], a[216:219], a[184:187], v[220:223]\n"
"v_mfma_f32_16x16x32_bf16 v[220:223], a[220:223], a[188:191], v[220:223]\n"
"v_mfma_f32_16x16x32_bf16 v[0:3], a[224:227], a[136:139], v[0:3]\n"
"v_mfma_f32_16x16x32_bf16 v[0:3], a[228:231], a[140:143], v[0:3]\n"
"v_mfma_f32_16x16x32_bf16 v[4:7], a[232:235], a[136:139], v[4:7]\n"
"s_waitcnt vmcnt(8)\n"
"v_mfma_f32_16x16x32_bf16 v[4:7], a[236:239], a[140:143], v[4:7]\n"
"s_barrier\n"
"v_mfma_f32_16x16x32_bf16 v[8:11], a[240:243], a[136:139], v[8:11]\n"
"s_mov_b32 m0, s51\n"
"ds_read_b64_tr_b16 a[192:193], v244 offset:0\n"
"v_mfma_f32_16x16x32_bf16 v[8:11], a[244:247], a[140:143], v[8:11]\n"
"buffer_load_dword v241, s[80:83], 0 offen lds\n"
"s_add_u32 s80, s80, 1456\n"
"s_addc_u32 s81, s81, 0\n"
"buffer_load_dwordx4 v248, s[24:27], s49 offen lds\n"
"v_mfma_f32_16x16x32_bf16 v[12:15], a[248:251], a[136:139], v[12:15]\n"
"ds_read_b64_tr_b16 a[194:195], v244 offset:512\n"
"v_mfma_f32_16x16x32_bf16 v[12:15], a[252:255], a[140:143], v[12:15]\n"
"ds_read_b128 a[136:139], v246 offset:0\n"
"v_mfma_f32_16x16x32_bf16 v[28:31], a[248:251], a[144:147], v[28:31]\n"
"ds_read_b64_tr_b16 a[196:197], v244 offset:8192\n"
"v_mfma_f32_16x16x32_bf16 v[28:31], a[252:255], a[148:151], v[28:31]\n"
"ds_read_b64_tr_b16 a[198:199], v244 offset:8704\n"
"v_mfma_f32_16x16x32_bf16 v[24:27], a[240:243], a[144:147], v[24:27]\n"
"ds_read_b128 a[140:143], v247 offset:0\n"
"v_mfma_f32_16x16x32_bf16 v[24:27], a[244:247], a[148:151], v[24:27]\n"
"buffer_load_dwordx4 v249, s[24:27], s49 offen offset:1024 lds\n"
"v_mfma_f32_16x16x32_bf16 v[20:23], a[232:235], a[144:147], v[20:23]\n"
"ds_read_b64_tr_b16 a[200:201], v245 offset:0\n"
"v_mfma_f32_16x16x32_bf16 v[20:23], a[236:239], a[148:151], v[20:23]\n"
"ds_read_b64_tr_b16 a[202:203], v245 offset:512\n"
"v_mfma_f32_16x16x32_bf16 v[16:19], a[224:227], a[144:147], v[16:19]\n"
"ds_read_b64_tr_b16 a[204:205], v245 offset:8192\n"
"v_mfma_f32_16x16x32_bf16 v[16:19], a[228:231], a[148:151], v[16:19]\n"
"ds_read_b64_tr_b16 a[206:207], v245 offset:8704\n"
"v_mfma_f32_16x16x32_bf16 v[32:35], a[224:227], a[152:155], v[32:35]\n"
"ds_read_b64_tr_b16 a[208:209], v244 offset:1024\n"
"v_mfma_f32_16x16x32_bf16 v[32:35], a[228:231], a[156:159], v[32:35]\n"
"buffer_load_dwordx4 v254, s[24:27], s49 offen offset:2048 lds\n"
"v_mfma_f32_16x16x32_bf16 v[36:39], a[232:235], a[152:155], v[36:39]\n"
"ds_read_b64_tr_b16 a[210:211], v244 offset:1536\n"
"v_mfma_f32_16x16x32_bf16 v[36:39], a[236:239], a[156:159], v[36:39]\n"
"ds_read_b64_tr_b16 a[212:213], v244 offset:9216\n"
"v_mfma_f32_16x16x32_bf16 v[40:43], a[240:243], a[152:155], v[40:43]\n"
"ds_read_b64_tr_b16 a[214:215], v244 offset:9728\n"
"v_mfma_f32_16x16x32_bf16 v[40:43], a[244:247], a[156:159], v[40:43]\n"
"ds_read_b64_tr_b16 a[216:217], v245 offset:1024\n"
"v_mfma_f32_16x16x32_bf16 v[44:47], a[248:251], a[152:155], v[44:47]\n"
"ds_read_b64_tr_b16 a[218:219], v245 offset:1536\n"
"v_mfma_f32_16x16x32_bf16 v[44:47], a[252:255], a[156:159], v[44:47]\n"
"buffer_load_dwordx4 v255, s[24:27], s49 offen offset:3072 lds\n"
"v_mfma_f32_16x16x32_bf16 v[60:63], a[248:251], a[160:163], v[60:63]\n"
"ds_read_b64_tr_b16 a[220:221], v245 offset:9216\n"
"v_mfma_f32_16x16x32_bf16 v[60:63], a[252:255], a[164:167], v[60:63]\n"
"ds_read_b64_tr_b16 a[222:223], v245 offset:9728\n"
"v_mfma_f32_16x16x32_bf16 v[56:59], a[240:243], a[160:163], v[56:59]\n"
"ds_read_b128 a[144:147], v246 offset:2048\n"
"v_mfma_f32_16x16x32_bf16 v[56:59], a[244:247], a[164:167], v[56:59]\n"
"s_add_u32 s40, s49, 40960\n"
"ds_read_b128 a[148:151], v247 offset:2048\n"
"v_mfma_f32_16x16x32_bf16 v[52:55], a[232:235], a[160:163], v[52:55]\n"
"s_add_u32 m0, s51, 4096\n"
"ds_read_b128 a[152:155], v246 offset:4096\n"
"v_mfma_f32_16x16x32_bf16 v[52:55], a[236:239], a[164:167], v[52:55]\n"
"buffer_load_dwordx4 v248, s[24:27], s40 offen lds\n"
"v_mfma_f32_16x16x32_bf16 v[48:51], a[224:227], a[160:163], v[48:51]\n"
"ds_read_b128 a[156:159], v247 offset:4096\n"
"v_mfma_f32_16x16x32_bf16 v[48:51], a[228:231], a[164:167], v[48:51]\n"
"ds_read_b128 a[160:163], v252 offset:0\n"
"v_mfma_f32_16x16x32_bf16 v[64:67], a[224:227], a[168:171], v[64:67]\n"
"ds_read_b128 a[164:167], v253 offset:0\n"
"v_mfma_f32_16x16x32_bf16 v[64:67], a[228:231], a[172:175], v[64:67]\n"
"v_mfma_f32_16x16x32_bf16 v[68:71], a[232:235], a[168:171], v[68:71]\n"
"v_mfma_f32_16x16x32_bf16 v[68:71], a[236:239], a[172:175], v[68:71]\n"
"buffer_load_dwordx4 v249, s[24:27], s40 offen offset:1024 lds\n"
"v_mfma_f32_16x16x32_bf16 v[72:75], a[240:243], a[168:171], v[72:75]\n"
"v_mfma_f32_16x16x32_bf16 v[72:75], a[244:247], a[172:175], v[72:75]\n"
"v_mfma_f32_16x16x32_bf16 v[76:79], a[248:251], a[168:171], v[76:79]\n"
"ds_read_b128 a[168:171], v250 offset:0\n"
"v_mfma_f32_16x16x32_bf16 v[76:79], a[252:255], a[172:175], v[76:79]\n"
"ds_read_b128 a[172:175], v251 offset:0\n"
"v_mfma_f32_16x16x32_bf16 v[92:95], a[248:251], a[176:179], v[92:95]\n"
"v_mfma_f32_16x16x32_bf16 v[92:95], a[252:255], a[180:183], v[92:95]\n"
"buffer_load_dwordx4 v254, s[24:27], s40 offen offset:2048 lds\n"
"v_mfma_f32_16x16x32_bf16 v[88:91], a[240:243], a[176:179], v[88:91]\n"
"s_add_u32 s49, s49, 128\n"
"v_mfma_f32_16x16x32_bf16 v[88:91], a[244:247], a[180:183], v[88:91]\n"
"s_sub_u32 s41, s49, 1280\n"
"v_mfma_f32_16x16x32_bf16 v[84:87], a[232:235], a[176:179], v[84:87]\n"
"s_min_u32 s49, s49, s41\n"
"v_mfma_f32_16x16x32_bf16 v[84:87], a[236:239], a[180:183], v[84:87]\n"
"s_xor_b32 s51, s51, s74\n"
"v_mfma_f32_16x16x32_bf16 v[80:83], a[224:227], a[176:179], v[80:83]\n"
"ds_read_b128 a[176:179], v250 offset:2048\n"
"v_mfma_f32_16x16x32_bf16 v[80:83], a[228:231], a[180:183], v[80:83]\n"
"ds_read_b128 a[180:183], v251 offset:2048\n"
"v_mfma_f32_16x16x32_bf16 v[96:99], a[224:227], a[184:187], v[96:99]\n"
"v_mfma_f32_16x16x32_bf16 v[96:99], a[228:231], a[188:191], v[96:99]\n"
"v_mfma_f32_16x16x32_bf16 v[100:103], a[232:235], a[184:187], v[100:103]\n"
"v_mfma_f32_16x16x32_bf16 v[100:103], a[236:239], a[188:191], v[100:103]\n"
"v_mfma_f32_16x16x32_bf16 v[104:107], a[240:243], a[184:187], v[104:107]\n"
"v_mfma_f32_16x16x32_bf16 v[104:107], a[244:247], a[188:191], v[104:107]\n"
"v_mfma_f32_16x16x32_bf16 v[108:111], a[248:251], a[184:187], v[108:111]\n"
"s_add_u32 s47, s47, 1\n"
"ds_read_b128 a[184:187], v250 offset:4096\n"
"v_mfma_f32_16x16x32_bf16 v[108:111], a[252:255], a[188:191], v[108:111]\n"
"s_waitcnt lgkmcnt(1)\n"
"s_cmp_lg_u32 s47, s62\n"
"ds_read_b128 a[188:191], v251 offset:4096\n"
"s_cbranch_scc1 LPN_%=\n"
"s_branch LPX_%=\n"
"LEND_%=:\n"
"LSLOW_%=:\n"
"s_waitcnt vmcnt(0) lgkmcnt(0)\n"
"s_nop 9\n"
"v_bfe_u32 v148, v242, 4, 4\n"
"v_bfe_u32 v149, v242, 8, 2\n"
"v_lshlrev_b32 v144, 8, v148\n"
"v_lshl_add_u32 v144, v149, 4, v144\n"
"v_add_u32 v144, s43, v144\n"
"v_xor_b32 v145, 64, v144\n"
"v_lshlrev_b32 v146, 1, v148\n"
"v_lshl_add_u32 v146, v149, 10, v146\n"
"v_add_u32 v146, s43, v146\n"
"v_xor_b32 v147, 64, v146\n"
"v_mov_b32 v152, 0xff7fffff\n"
"v_mov_b32 v155, 0\n"
"v_mov_b32 v0, 0\n"
"v_mov_b32 v1, 0\n"
"v_mov_b32 v2, 0\n"
"v_mov_b32 v3, 0\n"
"v_mov_b32 v4, 0\n"
"v_mov_b32 v5, 0\n"
"v_mov_b32 v6, 0\n"
"v_mov_b32 v7, 0\n"
"v_mov_b32 v8, 0\n"
"v_mov_b32 v9, 0\n"
"v_mov_b32 v10, 0\n"
"v_mov_b32 v11, 0\n"
"v_mov_b32 v12, 0\n"
"v_mov_b32 v13, 0\n"
"v_mov_b32 v14, 0\n"
"v_mov_b32 v15, 0\n"
"v_mov_b32 v16, 0\n"
"v_mov_b32 v17, 0\n"
"v_mov_b32 v18, 0\n"
"v_mov_b32 v19, 0\n"
"v_mov_b32 v20, 0\n"
"v_mov_b32 v21, 0\n"
"v_mov_b32 v22, 0\n"
"v_mov_b32 v23, 0\n"
"v_mov_b32 v24, 0\n"
"v_mov_b32 v25, 0\n"
"v_mov_b32 v26, 0\n"
"v_mov_b32 v27, 0\n"
"v_mov_b32 v28, 0\n"
"v_mov_b32 v29, 0\n"
"v_mov_b32 v30, 0\n"
"v_mov_b32 v31, 0\n"
"v_mov_b32 v156, 0xff7fffff\n"
"v_mov_b32 v159, 0\n"
"v_mov_b32 v32, 0\n"
"v_mov_b32 v33, 0\n"
"v_mov_b32 v34, 0\n"
"v_mov_b32 v35, 0\n"
"v_mov_b32 v36, 0\n"
"v_mov_b32 v37, 0\n"
"v_mov_b32 v38, 0\n"
"v_mov_b32 v39, 0\n"
"v_mov_b32 v40, 0\n"
"v_mov_b32 v41, 0\n"
"v_mov_b32 v42, 0\n"
"v_mov_b32 v43, 0\n"
"v_mov_b32 v44, 0\n"
"v_mov_b32 v45, 0\n"
"v_mov_b32 v46, 0\n"
"v_mov_b32 v47, 0\n"
"v_mov_b32 v48, 0\n"
"v_mov_b32 v49, 0\n"
"v_mov_b32 v50, 0\n"
"v_mov_b32 v51, 0\n"
"v_mov_b32 v52, 0\n"
"v_mov_b32 v53, 0\n"
"v_mov_b32 v54, 0\n"
"v_mov_b32 v55, 0\n"
"v_mov_b32 v56, 0\n"
"v_mov_b32 v57, 0\n"
"v_mov_b32 v58, 0\n"
"v_mov_b32 v59, 0\n"
"v_mov_b32 v60, 0\n"
"v_mov_b32 v61, 0\n"
"v_mov_b32 v62, 0\n"
"v_mov_b32 v63, 0\n"
"v_mov_b32 v160, 0xff7fffff\n"
"v_mov_b32 v163, 0\n"
"v_mov_b32 v64, 0\n"
"v_mov_b32 v65, 0\n"
"v_mov_b32 v66, 0\n"
"v_mov_b32 v67, 0\n"
"v_mov_b32 v68, 0\n"
"v_mov_b32 v69, 0\n"
"v_mov_b32 v70, 0\n"
"v_mov_b32 v71, 0\n"
"v_mov_b32 v72, 0\n"
"v_mov_b32 v73, 0\n"
"v_mov_b32 v74, 0\n"
"v_mov_b32 v75, 0\n"
"v_mov_b32 v76, 0\n"
"v_mov_b32 v77, 0\n"
"v_mov_b32 v78, 0\n"
"v_mov_b32 v79, 0\n"
"v_mov_b32 v80, 0\n"
"v_mov_b32 v81, 0\n"
"v_mov_b32 v82, 0\n"
"v_mov_b32 v83, 0\n"
"v_mov_b32 v84, 0\n"
"v_mov_b32 v85, 0\n"
"v_mov_b32 v86, 0\n"
"v_mov_b32 v87, 0\n"
"v_mov_b32 v88, 0\n"
"v_mov_b32 v89, 0\n"
"v_mov_b32 v90, 0\n"
"v_mov_b32 v91, 0\n"
"v_mov_b32 v92, 0\n"
"v_mov_b32 v93, 0\n"
"v_mov_b32 v94, 0\n"
"v_mov_b32 v95, 0\n"
"v_mov_b32 v164, 0xff7fffff\n"
"v_mov_b32 v167, 0\n"
"v_accvgpr_write_b32 a208, 0\n"
"v_accvgpr_write_b32 a209, 0\n"
"v_accvgpr_write_b32 a210, 0\n"
"v_accvgpr_write_b32 a211, 0\n"
"v_accvgpr_write_b32 a212, 0\n"
"v_accvgpr_write_b32 a213, 0\n"
"v_accvgpr_write_b32 a214, 0\n"
"v_accvgpr_write_b32 a215, 0\n"
"v_accvgpr_write_b32 a216, 0\n"
"v_accvgpr_write_b32 a217, 0\n"
"v_accvgpr_write_b32 a218, 0\n"
"v_accvgpr_write_b32 a219, 0\n"
"v_accvgpr_write_b32 a220, 0\n"
"v_accvgpr_write_b32 a221, 0\n"
"v_accvgpr_write_b32 a222, 0\n"
"v_accvgpr_write_b32 a223, 0\n"
"v_accvgpr_write_b32 a224, 0\n"
"v_accvgpr_write_b32 a225, 0\n"
"v_accvgpr_write_b32 a226, 0\n"
"v_accvgpr_write_b32 a227, 0\n"
"v_accvgpr_write_b32 a228, 0\n"
"v_accvgpr_write_b32 a229, 0\n"
"v_accvgpr_write_b32 a230, 0\n"
"v_accvgpr_write_b32 a231, 0\n"
"v_accvgpr_write_b32 a232, 0\n"
"v_accvgpr_write_b32 a233, 0\n"
"v_accvgpr_write_b32 a234, 0\n"
"v_accvgpr_write_b32 a235, 0\n"
"v_accvgpr_write_b32 a236, 0\n"
"v_accvgpr_write_b32 a237, 0\n"
"v_accvgpr_write_b32 a238, 0\n"
"v_accvgpr_write_b32 a239, 0\n"
"s_lshl_b32 s80, s47, 16\n"
"s_add_u32 s81, s80, 4096\n"
"s_mov_b32 s82, 0\n"
"LSLOOP_%=:\n"
"s_lshl_b32 s83, s82, 3\n"
"s_and_b32 s83, s83, 64\n"
"buffer_load_dwordx4 v[128:131], v144, s[84:87], s80 offen offset:0\n"
"buffer_load_dwordx4 v[132:135], v145, s[84:87], s80 offen offset:0\n"
"buffer_load_dwordx4 v[136:139], v144, s[84:87], s80 offen offset:128\n"
"buffer_load_dwordx4 v[140:143], v145, s[84:87], s80 offen offset:128\n"
"s_waitcnt vmcnt(0)\n"
"v_mfma_f32_16x16x32_bf16 v[96:99], v[128:131], a[0:3], 0\n"
"v_mfma_f32_16x16x32_bf16 v[96:99], v[132:135], a[4:7], v[96:99]\n"
"v_mfma_f32_16x16x32_bf16 v[96:99], v[136:139], a[8:11], v[96:99]\n"
"v_mfma_f32_16x16x32_bf16 v[96:99], v[140:143], a[12:15], v[96:99]\n"
"v_mfma_f32_16x16x32_bf16 v[104:107], v[128:131], a[16:19], 0\n"
"v_mfma_f32_16x16x32_bf16 v[104:107], v[132:135], a[20:23], v[104:107]\n"
"v_mfma_f32_16x16x32_bf16 v[104:107], v[136:139], a[24:27], v[104:107]\n"
"v_mfma_f32_16x16x32_bf16 v[104:107], v[140:143], a[28:31], v[104:107]\n"
"v_mfma_f32_16x16x32_bf16 v[112:115], v[128:131], a[32:35], 0\n"
"v_mfma_f32_16x16x32_bf16 v[112:115], v[132:135], a[36:39], v[112:115]\n"
"v_mfma_f32_16x16x32_bf16 v[112:115], v[136:139], a[40:43], v[112:115]\n"
"v_mfma_f32_16x16x32_bf16 v[112:115], v[140:143], a[44:47], v[112:115]\n"
"v_mfma_f32_16x16x32_bf16 v[120:123], v[128:131], a[192:195], 0\n"
"v_mfma_f32_16x16x32_bf16 v[120:123], v[132:135], a[196:199], v[120:123]\n"
"v_mfma_f32_16x16x32_bf16 v[120:123], v[136:139], a[200:203], v[120:123]\n"
"v_mfma_f32_16x16x32_bf16 v[120:123], v[140:143], a[204:207], v[120:123]\n"
"s_nop 7\n"
"buffer_load_dwordx4 v[128:131], v144, s[84:87], s81 offen offset:0\n"
"buffer_load_dwordx4 v[132:135], v145, s[84:87], s81 offen offset:0\n"
"buffer_load_dwordx4 v[136:139], v144, s[84:87], s81 offen offset:128\n"
"buffer_load_dwordx4 v[140:143], v145, s[84:87], s81 offen offset:128\n"
"s_waitcnt vmcnt(0)\n"
"v_mfma_f32_16x16x32_bf16 v[100:103], v[128:131], a[0:3], 0\n"
"v_mfma_f32_16x16x32_bf16 v[100:103], v[132:135], a[4:7], v[100:103]\n"
"v_mfma_f32_16x16x32_bf16 v[100:103], v[136:139], a[8:11], v[100:103]\n"
"v_mfma_f32_16x16x32_bf16 v[100:103], v[140:143], a[12:15], v[100:103]\n"
"v_mfma_f32_16x16x32_bf16 v[108:111], v[128:131], a[16:19], 0\n"
"v_mfma_f32_16x16x32_bf16 v[108:111], v[132:135], a[20:23], v[108:111]\n"
"v_mfma_f32_16x16x32_bf16 v[108:111], v[136:139], a[24:27], v[108:111]\n"
"v_mfma_f32_16x16x32_bf16 v[108:111], v[140:143], a[28:31], v[108:111]\n"
"v_mfma_f32_16x16x32_bf16 v[116:119], v[128:131], a[32:35], 0\n"
"v_mfma_f32_16x16x32_bf16 v[116:119], v[132:135], a[36:39], v[116:119]\n"
"v_mfma_f32_16x16x32_bf16 v[116:119], v[136:139], a[40:43], v[116:119]\n"
"v_mfma_f32_16x16x32_bf16 v[116:119], v[140:143], a[44:47], v[116:119]\n"
"v_mfma_f32_16x16x32_bf16 v[124:127], v[128:131], a[192:195], 0\n"
"v_mfma_f32_16x16x32_bf16 v[124:127], v[132:135], a[196:199], v[124:127]\n"
"v_mfma_f32_16x16x32_bf16 v[124:127], v[136:139], a[200:203], v[124:127]\n"
"v_mfma_f32_16x16x32_bf16 v[124:127], v[140:143], a[204:207], v[124:127]\n"
"s_nop 7\n"
"v_max3_f32 v153, v96, v97, v98\n"
"v_max3_f32 v153, v153, v99, v100\n"
"v_max3_f32 v153, v153, v101, v102\n"
"v_max3_f32 v153, v153, v103, v152\n"
"v_mov_b32 v154, v153\n"
"s_nop 1\n"
"v_permlane32_swap_b32 v153, v154\n"
"v_max_f32 v153, v153, v154\n"
"v_mov_b32 v154, v153\n"
"s_nop 1\n"
"v_permlane16_swap_b32 v153, v154\n"
"v_max_f32 v153, v153, v154\n"
"v_sub_f32 v154, v152, v153\n"
"v_exp_f32 v154, v154\n"
"v_mov_b32 v152, v153\n"
"v_sub_f32 v96, v96, v152\n"
"v_sub_f32 v97, v97, v152\n"
"v_sub_f32 v98, v98, v152\n"
"v_sub_f32 v99, v99, v152\n"
"v_sub_f32 v100, v100, v152\n"
"v_sub_f32 v101, v101, v152\n"
"v_sub_f32 v102, v102, v152\n"
"v_sub_f32 v103, v103, v152\n"
"v_exp_f32 v96, v96\n"
"v_exp_f32 v97, v97\n"
"v_exp_f32 v98, v98\n"
"v_exp_f32 v99, v99\n"
"v_exp_f32 v100, v100\n"
"v_exp_f32 v101, v101\n"
"v_exp_f32 v102, v102\n"
"v_exp_f32 v103, v103\n"
"v_mul_f32 v155, v155, v154\n"
"v_add_f32 v155, v155, v96\n"
"v_add_f32 v155, v155, v97\n"
"v_add_f32 v155, v155, v98\n"
"v_add_f32 v155, v155, v99\n"
"v_add_f32 v155, v155, v100\n"
"v_add_f32 v155, v155, v101\n"
"v_add_f32 v155, v155, v102\n"
"v_add_f32 v155, v155, v103\n"
"v_cvt_pk_bf16_f32 v96, v96, v97\n"
"v_cvt_pk_bf16_f32 v97, v98, v99\n"
"v_cvt_pk_bf16_f32 v98, v100, v101\n"
"v_cvt_pk_bf16_f32 v99, v102, v103\n"
"v_mov_b32_dpp v154, v154 row_ror:12 row_mask:0x2 bank_mask:0xf\n"
"s_nop 1\n"
"v_mov_b32_dpp v154, v154 row_ror:8 row_mask:0x4 bank_mask:0xf\n"
"s_nop 1\n"
"v_mov_b32_dpp v154, v154 row_ror:4 row_mask:0x8 bank_mask:0xf\n"
"s_nop 1\n"
"v_mul_f32_dpp v0, v154, v0 row_newbcast:0 row_mask:0xf bank_mask:0xf\n"
"v_mul_f32_dpp v1, v154, v1 row_newbcast:1 row_mask:0xf bank_mask:0xf\n"
"v_mul_f32_dpp v2, v154, v2 row_newbcast:2 row_mask:0xf bank_mask:0xf\n"
"v_mul_f32_dpp v3, v154, v3 row_newbcast:3 row_mask:0xf bank_mask:0xf\n"
"v_mul_f32_dpp v4, v154, v4 row_newbcast:0 row_mask:0xf bank_mask:0xf\n"
"v_mul_f32_dpp v5, v154, v5 row_newbcast:1 row_mask:0xf bank_mask:0xf\n"
"v_mul_f32_dpp v6, v154, v6 row_newbcast:2 row_mask:0xf bank_mask:0xf\n"
"v_mul_f32_dpp v7, v154, v7 row_newbcast:3 row_mask:0xf bank_mask:0xf\n"
"v_mul_f32_dpp v8, v154, v8 row_newbcast:0 row_mask:0xf bank_mask:0xf\n"
"v_mul_f32_dpp v9, v154, v9 row_newbcast:1 row_mask:0xf bank_mask:0xf\n"
"v_mul_f32_dpp v10, v154, v10 row_newbcast:2 row_mask:0xf bank_mask:0xf\n"
"v_mul_f32_dpp v11, v154, v11 row_newbcast:3 row_mask:0xf bank_mask:0xf\n"
"v_mul_f32_dpp v12, v154, v12 row_newbcast:0 row_mask:0xf bank_mask:0xf\n"
"v_mul_f32_dpp v13, v154, v13 row_newbcast:1 row_mask:0xf bank_mask:0xf\n"
"v_mul_f32_dpp v14, v154, v14 row_newbcast:2 row_mask:0xf bank_mask:0xf\n"
"v_mul_f32_dpp v15, v154, v15 row_newbcast:3 row_mask:0xf bank_mask:0xf\n"
"v_mul_f32_dpp v16, v154, v16 row_newbcast:0 row_mask:0xf bank_mask:0xf\n"
"v_mul_f32_dpp v17, v154, v17 row_newbcast:1 row_mask:0xf bank_mask:0xf\n"
"v_mul_f32_dpp v18, v154, v18 row_newbcast:2 row_mask:0xf bank_mask:0xf\n"
"v_mul_f32_dpp v19, v154, v19 row_newbcast:3 row_mask:0xf bank_mask:0xf\n"
"v_mul_f32_dpp v20, v154, v20 row_newbcast:0 row_mask:0xf bank_mask:0xf\n"
"v_mul_f32_dpp v21, v154, v21 row_newbcast:1 row_mask:0xf bank_mask:0xf\n"
"v_mul_f32_dpp v22, v154, v22 row_newbcast:2 row_mask:0xf bank_mask:0xf\n"
"v_mul_f32_dpp v23, v154, v23 row_newbcast:3 row_mask:0xf bank_mask:0xf\n"
"v_mul_f32_dpp v24, v154, v24 row_newbcast:0 row_mask:0xf bank_mask:0xf\n"
"v_mul_f32_dpp v25, v154, v25 row_newbcast:1 row_mask:0xf bank_mask:0xf\n"
"v_mul_f32_dpp v26, v154, v26 row_newbcast:2 row_mask:0xf bank_mask:0xf\n"
"v_mul_f32_dpp v27, v154, v27 row_newbcast:3 row_mask:0xf bank_mask:0xf\n"
"v_mul_f32_dpp v28, v154, v28 row_newbcast:0 row_mask:0xf bank_mask:0xf\n"
"v_mul_f32_dpp v29, v154, v29 row_newbcast:1 row_mask:0xf bank_mask:0xf\n"
"v_mul_f32_dpp v30, v154, v30 row_newbcast:2 row_mask:0xf bank_mask:0xf\n"
"v_mul_f32_dpp v31, v154, v31 row_newbcast:3 row_mask:0xf bank_mask:0xf\n"
"v_max3_f32 v157, v104, v105, v106\n"
"v_max3_f32 v157, v157, v107, v108\n"
"v_max3_f32 v157, v157, v109, v110\n"
"v_max3_f32 v157, v157, v111, v156\n"
"v_mov_b32 v158, v157\n"
"s_nop 1\n"
"v_permlane32_swap_b32 v157, v158\n"
"v_max_f32 v157, v157, v158\n"
"v_mov_b32 v158, v157\n"
"s_nop 1\n"
"v_permlane16_swap_b32 v157, v158\n"
"v_max_f32 v157, v157, v158\n"
"v_sub_f32 v158, v156, v157\n"
"v_exp_f32 v158, v158\n"
"v_mov_b32 v156, v157\n"
"v_sub_f32 v104, v104, v156\n"
"v_sub_f32 v105, v105, v156\n"
"v_sub_f32 v106, v106, v156\n"
"v_sub_f32 v107, v107, v156\n"
"v_sub_f32 v108, v108, v156\n"
"v_sub_f32 v109, v109, v156\n"
"v_sub_f32 v110, v110, v156\n"
"v_sub_f32 v111, v111, v156\n"
"v_exp_f32 v104, v104\n"
"v_exp_f32 v105, v105\n"
"v_exp_f32 v106, v106\n"
"v_exp_f32 v107, v107\n"
"v_exp_f32 v108, v108\n"
"v_exp_f32 v109, v109\n"
"v_exp_f32 v110, v110\n"
"v_exp_f32 v111, v111\n"
"v_mul_f32 v159, v159, v158\n"
"v_add_f32 v159, v159, v104\n"
"v_add_f32 v159, v159, v105\n"
"v_add_f32 v159, v159, v106\n"
"v_add_f32 v159, v159, v107\n"
"v_add_f32 v159, v159, v108\n"
"v_add_f32 v159, v159, v109\n"
"v_add_f32 v159, v159, v110\n"
"v_add_f32 v159, v159, v111\n"
"v_cvt_pk_bf16_f32 v104, v104, v105\n"
"v_cvt_pk_bf16_f32 v105, v106, v107\n"
"v_cvt_pk_bf16_f32 v106, v108, v109\n"
"v_cvt_pk_bf16_f32 v107, v110, v111\n"
"v_mov_b32_dpp v158, v158 row_ror:12 row_mask:0x2 bank_mask:0xf\n"
"s_nop 1\n"
"v_mov_b32_dpp v158, v158 row_ror:8 row_mask:0x4 bank_mask:0xf\n"
"s_nop 1\n"
"v_mov_b32_dpp v158, v158 row_ror:4 row_mask:0x8 bank_mask:0xf\n"
"s_nop 1\n"
"v_mul_f32_dpp v32, v158, v32 row_newbcast:0 row_mask:0xf bank_mask:0xf\n"
"v_mul_f32_dpp v33, v158, v33 row_newbcast:1 row_mask:0xf bank_mask:0xf\n"
"v_mul_f32_dpp v34, v158, v34 row_newbcast:2 row_mask:0xf bank_mask:0xf\n"
"v_mul_f32_dpp v35, v158, v35 row_newbcast:3 row_mask:0xf bank_mask:0xf\n"
"v_mul_f32_dpp v36, v158, v36 row_newbcast:0 row_mask:0xf bank_mask:0xf\n"
"v_mul_f32_dpp v37, v158, v37 row_newbcast:1 row_mask:0xf bank_mask:0xf\n"
"v_mul_f32_dpp v38, v158, v38 row_newbcast:2 row_mask:0xf bank_mask:0xf\n"
"v_mul_f32_dpp v39, v158, v39 row_newbcast:3 row_mask:0xf bank_mask:0xf\n"
"v_mul_f32_dpp v40, v158, v40 row_newbcast:0 row_mask:0xf bank_mask:0xf\n"
"v_mul_f32_dpp v41, v158, v41 row_newbcast:1 row_mask:0xf bank_mask:0xf\n"
"v_mul_f32_dpp v42, v158, v42 row_newbcast:2 row_mask:0xf bank_mask:0xf\n"
"v_mul_f32_dpp v43, v158, v43 row_newbcast:3 row_mask:0xf bank_mask:0xf\n"
"v_mul_f32_dpp v44, v158, v44 row_newbcast:0 row_mask:0xf bank_mask:0xf\n"
"v_mul_f32_dpp v45, v158, v45 row_newbcast:1 row_mask:0xf bank_mask:0xf\n"
"v_mul_f32_dpp v46, v158, v46 row_newbcast:2 row_mask:0xf bank_mask:0xf\n"
"v_mul_f32_dpp v47, v158, v47 row_newbcast:3 row_mask:0xf bank_mask:0xf\n"
"v_mul_f32_dpp v48, v158, v48 row_newbcast:0 row_mask:0xf bank_mask:0xf\n"
"v_mul_f32_dpp v49, v158, v49 row_newbcast:1 row_mask:0xf bank_mask:0xf\n"
"v_mul_f32_dpp v50, v158, v50 row_newbcast:2 row_mask:0xf bank_mask:0xf\n"
"v_mul_f32_dpp v51, v158, v51 row_newbcast:3 row_mask:0xf bank_mask:0xf\n"
"v_mul_f32_dpp v52, v158, v52 row_newbcast:0 row_mask:0xf bank_mask:0xf\n"
"v_mul_f32_dpp v53, v158, v53 row_newbcast:1 row_mask:0xf bank_mask:0xf\n"
"v_mul_f32_dpp v54, v158, v54 row_newbcast:2 row_mask:0xf bank_mask:0xf\n"
"v_mul_f32_dpp v55, v158, v55 row_newbcast:3 row_mask:0xf bank_mask:0xf\n"
"v_mul_f32_dpp v56, v158, v56 row_newbcast:0 row_mask:0xf bank_mask:0xf\n"
"v_mul_f32_dpp v57, v158, v57 row_newbcast:1 row_mask:0xf bank_mask:0xf\n"
"v_mul_f32_dpp v58, v158, v58 row_newbcast:2 row_mask:0xf bank_mask:0xf\n"
"v_mul_f32_dpp v59, v158, v59 row_newbcast:3 row_mask:0xf bank_mask:0xf\n"
"v_mul_f32_dpp v60, v158, v60 row_newbcast:0 row_mask:0xf bank_mask:0xf\n"
"v_mul_f32_dpp v61, v158, v61 row_newbcast:1 row_mask:0xf bank_mask:0xf\n"
"v_mul_f32_dpp v62, v158, v62 row_newbcast:2 row_mask:0xf bank_mask:0xf\n"
"v_mul_f32_dpp v63, v158, v63 row_newbcast:3 row_mask:0xf bank_mask:0xf\n"
"v_max3_f32 v161, v112, v113, v114\n"
"v_max3_f32 v161, v161, v115, v116\n"
"v_max3_f32 v161, v161, v117, v118\n"
"v_max3_f32 v161, v161, v119, v160\n"
"v_mov_b32 v162, v161\n"
"s_nop 1\n"
"v_permlane32_swap_b32 v161, v162\n"
"v_max_f32 v161, v161, v162\n"
"v_mov_b32 v162, v161\n"
"s_nop 1\n"
"v_permlane16_swap_b32 v161, v162\n"
"v_max_f32 v161, v161, v162\n"
"v_sub_f32 v162, v160, v161\n"
"v_exp_f32 v162, v162\n"
"v_mov_b32 v160, v161\n"
"v_sub_f32 v112, v112, v160\n"
"v_sub_f32 v113, v113, v160\n"
"v_sub_f32 v114, v114, v160\n"
"v_sub_f32 v115, v115, v160\n"
"v_sub_f32 v116, v116, v160\n"
"v_sub_f32 v117, v117, v160\n"
"v_sub_f32 v118, v118, v160\n"
"v_sub_f32 v119, v119, v160\n"
"v_exp_f32 v112, v112\n"
"v_exp_f32 v113, v113\n"
"v_exp_f32 v114, v114\n"
"v_exp_f32 v115, v115\n"
"v_exp_f32 v116, v116\n"
"v_exp_f32 v117, v117\n"
"v_exp_f32 v118, v118\n"
"v_exp_f32 v119, v119\n"
"v_mul_f32 v163, v163, v162\n"
"v_add_f32 v163, v163, v112\n"
"v_add_f32 v163, v163, v113\n"
"v_add_f32 v163, v163, v114\n"
"v_add_f32 v163, v163, v115\n"
"v_add_f32 v163, v163, v116\n"
"v_add_f32 v163, v163, v117\n"
"v_add_f32 v163, v163, v118\n"
"v_add_f32 v163, v163, v119\n"
"v_cvt_pk_bf16_f32 v112, v112, v113\n"
"v_cvt_pk_bf16_f32 v113, v114, v115\n"
"v_cvt_pk_bf16_f32 v114, v116, v117\n"
"v_cvt_pk_bf16_f32 v115, v118, v119\n"
"v_mov_b32_dpp v162, v162 row_ror:12 row_mask:0x2 bank_mask:0xf\n"
"s_nop 1\n"
"v_mov_b32_dpp v162, v162 row_ror:8 row_mask:0x4 bank_mask:0xf\n"
"s_nop 1\n"
"v_mov_b32_dpp v162, v162 row_ror:4 row_mask:0x8 bank_mask:0xf\n"
"s_nop 1\n"
"v_mul_f32_dpp v64, v162, v64 row_newbcast:0 row_mask:0xf bank_mask:0xf\n"
"v_mul_f32_dpp v65, v162, v65 row_newbcast:1 row_mask:0xf bank_mask:0xf\n"
"v_mul_f32_dpp v66, v162, v66 row_newbcast:2 row_mask:0xf bank_mask:0xf\n"
"v_mul_f32_dpp v67, v162, v67 row_newbcast:3 row_mask:0xf bank_mask:0xf\n"
"v_mul_f32_dpp v68, v162, v68 row_newbcast:0 row_mask:0xf bank_mask:0xf\n"
"v_mul_f32_dpp v69, v162, v69 row_newbcast:1 row_mask:0xf bank_mask:0xf\n"
"v_mul_f32_dpp v70, v162, v70 row_newbcast:2 row_mask:0xf bank_mask:0xf\n"
"v_mul_f32_dpp v71, v162, v71 row_newbcast:3 row_mask:0xf bank_mask:0xf\n"
"v_mul_f32_dpp v72, v162, v72 row_newbcast:0 row_mask:0xf bank_mask:0xf\n"
"v_mul_f32_dpp v73, v162, v73 row_newbcast:1 row_mask:0xf bank_mask:0xf\n"
"v_mul_f32_dpp v74, v162, v74 row_newbcast:2 row_mask:0xf bank_mask:0xf\n"
"v_mul_f32_dpp v75, v162, v75 row_newbcast:3 row_mask:0xf bank_mask:0xf\n"
"v_mul_f32_dpp v76, v162, v76 row_newbcast:0 row_mask:0xf bank_mask:0xf\n"
"v_mul_f32_dpp v77, v162, v77 row_newbcast:1 row_mask:0xf bank_mask:0xf\n"
"v_mul_f32_dpp v78, v162, v78 row_newbcast:2 row_mask:0xf bank_mask:0xf\n"
"v_mul_f32_dpp v79, v162, v79 row_newbcast:3 row_mask:0xf bank_mask:0xf\n"
"v_mul_f32_dpp v80, v162, v80 row_newbcast:0 row_mask:0xf bank_mask:0xf\n"
"v_mul_f32_dpp v81, v162, v81 row_newbcast:1 row_mask:0xf bank_mask:0xf\n"
"v_mul_f32_dpp v82, v162, v82 row_newbcast:2 row_mask:0xf bank_mask:0xf\n"
"v_mul_f32_dpp v83, v162, v83 row_newbcast:3 row_mask:0xf bank_mask:0xf\n"
"v_mul_f32_dpp v84, v162, v84 row_newbcast:0 row_mask:0xf bank_mask:0xf\n"
"v_mul_f32_dpp v85, v162, v85 row_newbcast:1 row_mask:0xf bank_mask:0xf\n"
"v_mul_f32_dpp v86, v162, v86 row_newbcast:2 row_mask:0xf bank_mask:0xf\n"
"v_mul_f32_dpp v87, v162, v87 row_newbcast:3 row_mask:0xf bank_mask:0xf\n"
"v_mul_f32_dpp v88, v162, v88 row_newbcast:0 row_mask:0xf bank_mask:0xf\n"
"v_mul_f32_dpp v89, v162, v89 row_newbcast:1 row_mask:0xf bank_mask:0xf\n"
"v_mul_f32_dpp v90, v162, v90 row_newbcast:2 row_mask:0xf bank_mask:0xf\n"
"v_mul_f32_dpp v91, v162, v91 row_newbcast:3 row_mask:0xf bank_mask:0xf\n"
"v_mul_f32_dpp v92, v162, v92 row_newbcast:0 row_mask:0xf bank_mask:0xf\n"
"v_mul_f32_dpp v93, v162, v93 row_newbcast:1 row_mask:0xf bank_mask:0xf\n"
"v_mul_f32_dpp v94, v162, v94 row_newbcast:2 row_mask:0xf bank_mask:0xf\n"
"v_mul_f32_dpp v95, v162, v95 row_newbcast:3 row_mask:0xf bank_mask:0xf\n"
"s_cmp_lg_u32 s83, s43\n"
"s_cbranch_scc1 LSK3A_%=\n"
"v_max3_f32 v165, v120, v121, v122\n"
"v_max3_f32 v165, v165, v123, v124\n"
"v_max3_f32 v165, v165, v125, v126\n"
"v_max3_f32 v165, v165, v127, v164\n"
"v_mov_b32 v166, v165\n"
"s_nop 1\n"
"v_permlane32_swap_b32 v165, v166\n"
"v_max_f32 v165, v165, v166\n"
"v_mov_b32 v166, v165\n"
"s_nop 1\n"
"v_permlane16_swap_b32 v165, v166\n"
"v_max_f32 v165, v165, v166\n"
"v_sub_f32 v166, v164, v165\n"
"v_exp_f32 v166, v166\n"
"v_mov_b32 v164, v165\n"
"v_sub_f32 v120, v120, v164\n"
"v_sub_f32 v121, v121, v164\n"
"v_sub_f32 v122, v122, v164\n"
"v_sub_f32 v123, v123, v164\n"
"v_sub_f32 v124, v124, v164\n"
"v_sub_f32 v125, v125, v164\n"
"v_sub_f32 v126, v126, v164\n"
"v_sub_f32 v127, v127, v164\n"
"v_exp_f32 v120, v120\n"
"v_exp_f32 v121, v121\n"
"v_exp_f32 v122, v122\n"
"v_exp_f32 v123, v123\n"
"v_exp_f32 v124, v124\n"
"v_exp_f32 v125, v125\n"
"v_exp_f32 v126, v126\n"
"v_exp_f32 v127, v127\n"
"v_mul_f32 v167, v167, v166\n"
"v_add_f32 v167, v167, v120\n"
"v_add_f32 v167, v167, v121\n"
"v_add_f32 v167, v167, v122\n"
"v_add_f32 v167, v167, v123\n"
"v_add_f32 v167, v167, v124\n"
"v_add_f32 v167, v167, v125\n"
"v_add_f32 v167, v167, v126\n"
"v_add_f32 v167, v167, v127\n"
"v_cvt_pk_bf16_f32 v120, v120, v121\n"
"v_cvt_pk_bf16_f32 v121, v122, v123\n"
"v_cvt_pk_bf16_f32 v122, v124, v125\n"
"v_cvt_pk_bf16_f32 v123, v126, v127\n"
"v_mov_b32_dpp v166, v166 row_ror:12 row_mask:0x2 bank_mask:0xf\n"
"s_nop 1\n"
"v_mov_b32_dpp v166, v166 row_ror:8 row_mask:0x4 bank_mask:0xf\n"
"s_nop 1\n"
"v_mov_b32_dpp v166, v166 row_ror:4 row_mask:0x8 bank_mask:0xf\n"
"v_accvgpr_read_b32 v124, a208\n"
"s_nop 1\n"
"v_mul_f32_dpp v124, v166, v124 row_newbcast:0 row_mask:0xf bank_mask:0xf\n"
"v_accvgpr_write_b32 a208, v124\n"
"v_accvgpr_read_b32 v125, a209\n"
"s_nop 1\n"
"v_mul_f32_dpp v125, v166, v125 row_newbcast:1 row_mask:0xf bank_mask:0xf\n"
"v_accvgpr_write_b32 a209, v125\n"
"v_accvgpr_read_b32 v126, a210\n"
"s_nop 1\n"
"v_mul_f32_dpp v126, v166, v126 row_newbcast:2 row_mask:0xf bank_mask:0xf\n"
"v_accvgpr_write_b32 a210, v126\n"
"v_accvgpr_read_b32 v127, a211\n"
"s_nop 1\n"
"v_mul_f32_dpp v127, v166, v127 row_newbcast:3 row_mask:0xf bank_mask:0xf\n"
"v_accvgpr_write_b32 a211, v127\n"
"v_accvgpr_read_b32 v124, a212\n"
"s_nop 1\n"
"v_mul_f32_dpp v124, v166, v124 row_newbcast:0 row_mask:0xf bank_mask:0xf\n"
"v_accvgpr_write_b32 a212, v124\n"
"v_accvgpr_read_b32 v125, a213\n"
"s_nop 1\n"
"v_mul_f32_dpp v125, v166, v125 row_newbcast:1 row_mask:0xf bank_mask:0xf\n"
"v_accvgpr_write_b32 a213, v125\n"
"v_accvgpr_read_b32 v126, a214\n"
"s_nop 1\n"
"v_mul_f32_dpp v126, v166, v126 row_newbcast:2 row_mask:0xf bank_mask:0xf\n"
"v_accvgpr_write_b32 a214, v126\n"
"v_accvgpr_read_b32 v127, a215\n"
"s_nop 1\n"
"v_mul_f32_dpp v127, v166, v127 row_newbcast:3 row_mask:0xf bank_mask:0xf\n"
"v_accvgpr_write_b32 a215, v127\n"
"v_accvgpr_read_b32 v124, a216\n"
"s_nop 1\n"
"v_mul_f32_dpp v124, v166, v124 row_newbcast:0 row_mask:0xf bank_mask:0xf\n"
"v_accvgpr_write_b32 a216, v124\n"
"v_accvgpr_read_b32 v125, a217\n"
"s_nop 1\n"
"v_mul_f32_dpp v125, v166, v125 row_newbcast:1 row_mask:0xf bank_mask:0xf\n"
"v_accvgpr_write_b32 a217, v125\n"
"v_accvgpr_read_b32 v126, a218\n"
"s_nop 1\n"
"v_mul_f32_dpp v126, v166, v126 row_newbcast:2 row_mask:0xf bank_mask:0xf\n"
"v_accvgpr_write_b32 a218, v126\n"
"v_accvgpr_read_b32 v127, a219\n"
"s_nop 1\n"
"v_mul_f32_dpp v127, v166, v127 row_newbcast:3 row_mask:0xf bank_mask:0xf\n"
"v_accvgpr_write_b32 a219, v127\n"
"v_accvgpr_read_b32 v124, a220\n"
"s_nop 1\n"
"v_mul_f32_dpp v124, v166, v124 row_newbcast:0 row_mask:0xf bank_mask:0xf\n"
"v_accvgpr_write_b32 a220, v124\n"
"v_accvgpr_read_b32 v125, a221\n"
"s_nop 1\n"
"v_mul_f32_dpp v125, v166, v125 row_newbcast:1 row_mask:0xf bank_mask:0xf\n"
"v_accvgpr_write_b32 a221, v125\n"
"v_accvgpr_read_b32 v126, a222\n"
"s_nop 1\n"
"v_mul_f32_dpp v126, v166, v126 row_newbcast:2 row_mask:0xf bank_mask:0xf\n"
"v_accvgpr_write_b32 a222, v126\n"
"v_accvgpr_read_b32 v127, a223\n"
"s_nop 1\n"
"v_mul_f32_dpp v127, v166, v127 row_newbcast:3 row_mask:0xf bank_mask:0xf\n"
"v_accvgpr_write_b32 a223, v127\n"
"v_accvgpr_read_b32 v124, a224\n"
"s_nop 1\n"
"v_mul_f32_dpp v124, v166, v124 row_newbcast:0 row_mask:0xf bank_mask:0xf\n"
"v_accvgpr_write_b32 a224, v124\n"
"v_accvgpr_read_b32 v125, a225\n"
"s_nop 1\n"
"v_mul_f32_dpp v125, v166, v125 row_newbcast:1 row_mask:0xf bank_mask:0xf\n"
"v_accvgpr_write_b32 a225, v125\n"
"v_accvgpr_read_b32 v126, a226\n"
"s_nop 1\n"
"v_mul_f32_dpp v126, v166, v126 row_newbcast:2 row_mask:0xf bank_mask:0xf\n"
"v_accvgpr_write_b32 a226, v126\n"
"v_accvgpr_read_b32 v127, a227\n"
"s_nop 1\n"
"v_mul_f32_dpp v127, v166, v127 row_newbcast:3 row_mask:0xf bank_mask:0xf\n"
"v_accvgpr_write_b32 a227, v127\n"
"v_accvgpr_read_b32 v124, a228\n"
"s_nop 1\n"
"v_mul_f32_dpp v124, v166, v124 row_newbcast:0 row_mask:0xf bank_mask:0xf\n"
"v_accvgpr_write_b32 a228, v124\n"
"v_accvgpr_read_b32 v125, a229\n"
"s_nop 1\n"
"v_mul_f32_dpp v125, v166, v125 row_newbcast:1 row_mask:0xf bank_mask:0xf\n"
"v_accvgpr_write_b32 a229, v125\n"
"v_accvgpr_read_b32 v126, a230\n"
"s_nop 1\n"
"v_mul_f32_dpp v126, v166, v126 row_newbcast:2 row_mask:0xf bank_mask:0xf\n"
"v_accvgpr_write_b32 a230, v126\n"
"v_accvgpr_read_b32 v127, a231\n"
"s_nop 1\n"
"v_mul_f32_dpp v127, v166, v127 row_newbcast:3 row_mask:0xf bank_mask:0xf\n"
"v_accvgpr_write_b32 a231, v127\n"
"v_accvgpr_read_b32 v124, a232\n"
"s_nop 1\n"
"v_mul_f32_dpp v124, v166, v124 row_newbcast:0 row_mask:0xf bank_mask:0xf\n"
"v_accvgpr_write_b32 a232, v124\n"
"v_accvgpr_read_b32 v125, a233\n"
"s_nop 1\n"
"v_mul_f32_dpp v125, v166, v125 row_newbcast:1 row_mask:0xf bank_mask:0xf\n"
"v_accvgpr_write_b32 a233, v125\n"
"v_accvgpr_read_b32 v126, a234\n"
"s_nop 1\n"
"v_mul_f32_dpp v126, v166, v126 row_newbcast:2 row_mask:0xf bank_mask:0xf\n"
"v_accvgpr_write_b32 a234, v126\n"
"v_accvgpr_read_b32 v127, a235\n"
"s_nop 1\n"
"v_mul_f32_dpp v127, v166, v127 row_newbcast:3 row_mask:0xf bank_mask:0xf\n"
"v_accvgpr_write_b32 a235, v127\n"
"v_accvgpr_read_b32 v124, a236\n"
"s_nop 1\n"
"v_mul_f32_dpp v124, v166, v124 row_newbcast:0 row_mask:0xf bank_mask:0xf\n"
"v_accvgpr_write_b32 a236, v124\n"
"v_accvgpr_read_b32 v125, a237\n"
"s_nop 1\n"
"v_mul_f32_dpp v125, v166, v125 row_newbcast:1 row_mask:0xf bank_mask:0xf\n"
"v_accvgpr_write_b32 a237, v125\n"
"v_accvgpr_read_b32 v126, a238\n"
"s_nop 1\n"
"v_mul_f32_dpp v126, v166, v126 row_newbcast:2 row_mask:0xf bank_mask:0xf\n"
"v_accvgpr_write_b32 a238, v126\n"
"v_accvgpr_read_b32 v127, a239\n"
"s_nop 1\n"
"v_mul_f32_dpp v127, v166, v127 row_newbcast:3 row_mask:0xf bank_mask:0xf\n"
"v_accvgpr_write_b32 a239, v127\n"
"LSK3A_%=:\n"
"s_nop 3\n"
"buffer_load_ushort v128, v146, s[88:91], s80 offen offset:0\n"
"buffer_load_ushort v129, v146, s[88:91], s80 offen offset:256\n"
"buffer_load_ushort v130, v146, s[88:91], s80 offen offset:512\n"
"buffer_load_ushort v131, v146, s[88:91], s80 offen offset:768\n"
"buffer_load_ushort v132, v146, s[88:91], s81 offen offset:0\n"
"buffer_load_ushort v133, v146, s[88:91], s81 offen offset:256\n"
"buffer_load_ushort v134, v146, s[88:91], s81 offen offset:512\n"
"buffer_load_ushort v135, v146, s[88:91], s81 offen offset:768\n"
"buffer_load_ushort v136, v146, s[88:91], s80 offen offset:32\n"
"buffer_load_ushort v137, v146, s[88:91], s80 offen offset:288\n"
"buffer_load_ushort v138, v146, s[88:91], s80 offen offset:544\n"
"buffer_load_ushort v139, v146, s[88:91], s80 offen offset:800\n"
"buffer_load_ushort v140, v146, s[88:91], s81 offen offset:32\n"
"buffer_load_ushort v141, v146, s[88:91], s81 offen offset:288\n"
"buffer_load_ushort v142, v146, s[88:91], s81 offen offset:544\n"
"buffer_load_ushort v143, v146, s[88:91], s81 offen offset:800\n"
"s_waitcnt vmcnt(0)\n"
"v_lshl_or_b32 v128, v129, 16, v128\n"
"v_lshl_or_b32 v129, v131, 16, v130\n"
"v_lshl_or_b32 v130, v133, 16, v132\n"
"v_lshl_or_b32 v131, v135, 16, v134\n"
"v_lshl_or_b32 v136, v137, 16, v136\n"
"v_lshl_or_b32 v137, v139, 16, v138\n"
"v_lshl_or_b32 v138, v141, 16, v140\n"
"v_lshl_or_b32 v139, v143, 16, v142\n"
"v_mfma_f32_16x16x32_bf16 v[0:3], v[96:99], v[128:131], v[0:3]\n"
"s_nop 0\n"
"v_mfma_f32_16x16x32_bf16 v[4:7], v[96:99], v[136:139], v[4:7]\n"
"v_mfma_f32_16x16x32_bf16 v[32:35], v[104:107], v[128:131], v[32:35]\n"
"v_mfma_f32_16x16x32_bf16 v[36:39], v[104:107], v[136:139], v[36:39]\n"
"v_mfma_f32_16x16x32_bf16 v[64:67], v[112:115], v[128:131], v[64:67]\n"
"v_mfma_f32_16x16x32_bf16 v[68:71], v[112:115], v[136:139], v[68:71]\n"
"s_cmp_lg_u32 s83, s43\n"
"s_cbranch_scc1 LSK3B0_%=\n"
"v_mfma_f32_16x16x32_bf16 a[208:211], v[120:123], v[128:131], a[208:211]\n"
"v_mfma_f32_16x16x32_bf16 a[212:215], v[120:123], v[136:139], a[212:215]\n"
"LSK3B0_%=:\n"
"s_nop 3\n"
"buffer_load_ushort v128, v147, s[88:91], s80 offen offset:0\n"
"buffer_load_ushort v129, v147, s[88:91], s80 offen offset:256\n"
"buffer_load_ushort v130, v147, s[88:91], s80 offen offset:512\n"
"buffer_load_ushort v131, v147, s[88:91], s80 offen offset:768\n"
"buffer_load_ushort v132, v147, s[88:91], s81 offen offset:0\n"
"buffer_load_ushort v133, v147, s[88:91], s81 offen offset:256\n"
"buffer_load_ushort v134, v147, s[88:91], s81 offen offset:512\n"
"buffer_load_ushort v135, v147, s[88:91], s81 offen offset:768\n"
"buffer_load_ushort v136, v147, s[88:91], s80 offen offset:32\n"
"buffer_load_ushort v137, v147, s[88:91], s80 offen offset:288\n"
"buffer_load_ushort v138, v147, s[88:91], s80 offen offset:544\n"
"buffer_load_ushort v139, v147, s[88:91], s80 offen offset:800\n"
"buffer_load_ushort v140, v147, s[88:91], s81 offen offset:32\n"
"buffer_load_ushort v141, v147, s[88:91], s81 offen offset:288\n"
"buffer_load_ushort v142, v147, s[88:91], s81 offen offset:544\n"
"buffer_load_ushort v143, v147, s[88:91], s81 offen offset:800\n"
"s_waitcnt vmcnt(0)\n"
"v_lshl_or_b32 v128, v129, 16, v128\n"
"v_lshl_or_b32 v129, v131, 16, v130\n"
"v_lshl_or_b32 v130, v133, 16, v132\n"
"v_lshl_or_b32 v131, v135, 16, v134\n"
"v_lshl_or_b32 v136, v137, 16, v136\n"
"v_lshl_or_b32 v137, v139, 16, v138\n"
"v_lshl_or_b32 v138, v141, 16, v140\n"
"v_lshl_or_b32 v139, v143, 16, v142\n"
"v_mfma_f32_16x16x32_bf16 v[8:11], v[96:99], v[128:131], v[8:11]\n"
"s_nop 0\n"
"v_mfma_f32_16x16x32_bf16 v[12:15], v[96:99], v[136:139], v[12:15]\n"
"v_mfma_f32_16x16x32_bf16 v[40:43], v[104:107], v[128:131], v[40:43]\n"
"v_mfma_f32_16x16x32_bf16 v[44:47], v[104:107], v[136:139], v[44:47]\n"
"v_mfma_f32_16x16x32_bf16 v[72:75], v[112:115], v[128:131], v[72:75]\n"
"v_mfma_f32_16x16x32_bf16 v[76:79], v[112:115], v[136:139], v[76:79]\n"
"s_cmp_lg_u32 s83, s43\n"
"s_cbranch_scc1 LSK3B1_%=\n"
"v_mfma_f32_16x16x32_bf16 a[216:219], v[120:123], v[128:131], a[216:219]\n"
"v_mfma_f32_16x16x32_bf16 a[220:223], v[120:123], v[136:139], a[220:223]\n"
"LSK3B1_%=:\n"
"s_nop 3\n"
"buffer_load_ushort v128, v146, s[88:91], s80 offen offset:128\n"
"buffer_load_ushort v129, v146, s[88:91], s80 offen offset:384\n"
"buffer_load_ushort v130, v146, s[88:91], s80 offen offset:640\n"
"buffer_load_ushort v131, v146, s[88:91], s80 offen offset:896\n"
"buffer_load_ushort v132, v146, s[88:91], s81 offen offset:128\n"
"buffer_load_ushort v133, v146, s[88:91], s81 offen offset:384\n"
"buffer_load_ushort v134, v146, s[88:91], s81 offen offset:640\n"
"buffer_load_ushort v135, v146, s[88:91], s81 offen offset:896\n"
"buffer_load_ushort v136, v146, s[88:91], s80 offen offset:160\n"
"buffer_load_ushort v137, v146, s[88:91], s80 offen offset:416\n"
"buffer_load_ushort v138, v146, s[88:91], s80 offen offset:672\n"
"buffer_load_ushort v139, v146, s[88:91], s80 offen offset:928\n"
"buffer_load_ushort v140, v146, s[88:91], s81 offen offset:160\n"
"buffer_load_ushort v141, v146, s[88:91], s81 offen offset:416\n"
"buffer_load_ushort v142, v146, s[88:91], s81 offen offset:672\n"
"buffer_load_ushort v143, v146, s[88:91], s81 offen offset:928\n"
"s_waitcnt vmcnt(0)\n"
"v_lshl_or_b32 v128, v129, 16, v128\n"
"v_lshl_or_b32 v129, v131, 16, v130\n"
"v_lshl_or_b32 v130, v133, 16, v132\n"
"v_lshl_or_b32 v131, v135, 16, v134\n"
"v_lshl_or_b32 v136, v137, 16, v136\n"
"v_lshl_or_b32 v137, v139, 16, v138\n"
"v_lshl_or_b32 v138, v141, 16, v140\n"
"v_lshl_or_b32 v139, v143, 16, v142\n"
"v_mfma_f32_16x16x32_bf16 v[16:19], v[96:99], v[128:131], v[16:19]\n"
"s_nop 0\n"
"v_mfma_f32_16x16x32_bf16 v[20:23], v[96:99], v[136:139], v[20:23]\n"
"v_mfma_f32_16x16x32_bf16 v[48:51], v[104:107], v[128:131], v[48:51]\n"
"v_mfma_f32_16x16x32_bf16 v[52:55], v[104:107], v[136:139], v[52:55]\n"
"v_mfma_f32_16x16x32_bf16 v[80:83], v[112:115], v[128:131], v[80:83]\n"
"v_mfma_f32_16x16x32_bf16 v[84:87], v[112:115], v[136:139], v[84:87]\n"
"s_cmp_lg_u32 s83, s43\n"
"s_cbranch_scc1 LSK3B2_%=\n"
"v_mfma_f32_16x16x32_bf16 a[224:227], v[120:123], v[128:131], a[224:227]\n"
"v_mfma_f32_16x16x32_bf16 a[228:231], v[120:123], v[136:139], a[228:231]\n"
"LSK3B2_%=:\n"
"s_nop 3\n"
"buffer_load_ushort v128, v147, s[88:91], s80 offen offset:128\n"
"buffer_load_ushort v129, v147, s[88:91], s80 offen offset:384\n"
"buffer_load_ushort v130, v147, s[88:91], s80 offen offset:640\n"
"buffer_load_ushort v131, v147, s[88:91], s80 offen offset:896\n"
"buffer_load_ushort v132, v147, s[88:91], s81 offen offset:128\n"
"buffer_load_ushort v133, v147, s[88:91], s81 offen offset:384\n"
"buffer_load_ushort v134, v147, s[88:91], s81 offen offset:640\n"
"buffer_load_ushort v135, v147, s[88:91], s81 offen offset:896\n"
"buffer_load_ushort v136, v147, s[88:91], s80 offen offset:160\n"
"buffer_load_ushort v137, v147, s[88:91], s80 offen offset:416\n"
"buffer_load_ushort v138, v147, s[88:91], s80 offen offset:672\n"
"buffer_load_ushort v139, v147, s[88:91], s80 offen offset:928\n"
"buffer_load_ushort v140, v147, s[88:91], s81 offen offset:160\n"
"buffer_load_ushort v141, v147, s[88:91], s81 offen offset:416\n"
"buffer_load_ushort v142, v147, s[88:91], s81 offen offset:672\n"
"buffer_load_ushort v143, v147, s[88:91], s81 offen offset:928\n"
"s_waitcnt vmcnt(0)\n"
"v_lshl_or_b32 v128, v129, 16, v128\n"
"v_lshl_or_b32 v129, v131, 16, v130\n"
"v_lshl_or_b32 v130, v133, 16, v132\n"
"v_lshl_or_b32 v131, v135, 16, v134\n"
"v_lshl_or_b32 v136, v137, 16, v136\n"
"v_lshl_or_b32 v137, v139, 16, v138\n"
"v_lshl_or_b32 v138, v141, 16, v140\n"
"v_lshl_or_b32 v139, v143, 16, v142\n"
"v_mfma_f32_16x16x32_bf16 v[24:27], v[96:99], v[128:131], v[24:27]\n"
"s_nop 0\n"
"v_mfma_f32_16x16x32_bf16 v[28:31], v[96:99], v[136:139], v[28:31]\n"
"v_mfma_f32_16x16x32_bf16 v[56:59], v[104:107], v[128:131], v[56:59]\n"
"v_mfma_f32_16x16x32_bf16 v[60:63], v[104:107], v[136:139], v[60:63]\n"
"v_mfma_f32_16x16x32_bf16 v[88:91], v[112:115], v[128:131], v[88:91]\n"
"v_mfma_f32_16x16x32_bf16 v[92:95], v[112:115], v[136:139], v[92:95]\n"
"s_cmp_lg_u32 s83, s43\n"
"s_cbranch_scc1 LSK3B3_%=\n"
"v_mfma_f32_16x16x32_bf16 a[232:235], v[120:123], v[128:131], a[232:235]\n"
"v_mfma_f32_16x16x32_bf16 a[236:239], v[120:123], v[136:139], a[236:239]\n"
"LSK3B3_%=:\n"
"s_nop 3\n"
"s_nop 9\n"
"s_add_u32 s80, s80, 8192\n"
"s_add_u32 s81, s81, 8192\n"
"s_add_u32 s82, s82, 1\n"
"s_cmp_lt_u32 s82, 32\n"
"s_cbranch_scc1 LSLOOP_%=\n"
"v_mov_b32 v154, v155\n"
"s_nop 1\n"
"v_permlane32_swap_b32 v155, v154\n"
"v_add_f32 v155, v155, v154\n"
"v_mov_b32 v154, v155\n"
"s_nop 1\n"
"v_permlane16_swap_b32 v155, v154\n"
"v_add_f32 v155, v155, v154\n"
"v_mov_b32 v196, v155\n"
"v_mov_b32 v158, v159\n"
"s_nop 1\n"
"v_permlane32_swap_b32 v159, v158\n"
"v_add_f32 v159, v159, v158\n"
"v_mov_b32 v158, v159\n"
"s_nop 1\n"
"v_permlane16_swap_b32 v159, v158\n"
"v_add_f32 v159, v159, v158\n"
"v_mov_b32 v198, v159\n"
"v_mov_b32 v162, v163\n"
"s_nop 1\n"
"v_permlane32_swap_b32 v163, v162\n"
"v_add_f32 v163, v163, v162\n"
"v_mov_b32 v162, v163\n"
"s_nop 1\n"
"v_permlane16_swap_b32 v163, v162\n"
"v_add_f32 v163, v163, v162\n"
"v_mov_b32 v200, v163\n"
"v_mov_b32 v166, v167\n"
"s_nop 1\n"
"v_permlane32_swap_b32 v167, v166\n"
"v_add_f32 v167, v167, v166\n"
"v_mov_b32 v166, v167\n"
"s_nop 1\n"
"v_permlane16_swap_b32 v167, v166\n"
"v_add_f32 v167, v167, v166\n"
"v_mov_b32 v202, v167\n"
"v_sub_f32 v192, 0, v164\n"
"v_mov_b32 v96, 0\n"
"v_mov_b32 v97, 0\n"
"v_mov_b32 v98, 0\n"
"v_mov_b32 v99, 0\n"
"v_mov_b32 v100, 0\n"
"v_mov_b32 v101, 0\n"
"v_mov_b32 v102, 0\n"
"v_mov_b32 v103, 0\n"
"v_mov_b32 v104, 0\n"
"v_mov_b32 v105, 0\n"
"v_mov_b32 v106, 0\n"
"v_mov_b32 v107, 0\n"
"v_mov_b32 v108, 0\n"
"v_mov_b32 v109, 0\n"
"v_mov_b32 v110, 0\n"
"v_mov_b32 v111, 0\n"
"v_mov_b32 v112, 0\n"
"v_mov_b32 v113, 0\n"
"v_mov_b32 v114, 0\n"
"v_mov_b32 v115, 0\n"
"v_mov_b32 v116, 0\n"
"v_mov_b32 v117, 0\n"
"v_mov_b32 v118, 0\n"
"v_mov_b32 v119, 0\n"
"v_mov_b32 v120, 0\n"
"v_mov_b32 v121, 0\n"
"v_mov_b32 v122, 0\n"
"v_mov_b32 v123, 0\n"
"v_mov_b32 v204, 0\n"
"v_mov_b32 v205, 0\n"
"s_nop 3\n"
"s_mov_b32 s68, 0\n"
"s_cmp_eq_u32 s47, 4\n"
"s_cbranch_scc1 LBACK0_%=\n"
"s_cmp_eq_u32 s47, 8\n"
"s_cbranch_scc1 LBACK1_%=\n"
"s_cmp_eq_u32 s47, 12\n"
"s_cbranch_scc1 LBACK2_%=\n"
"s_branch LBACK3_%=\n"
"LFIN_%=:\n"
    :: [kalo]"s"((unsigned)ka), [kahi]"s"((unsigned)(ka >> 32)), [wg]"s"(blockIdx.x), [wid]"s"(wid)
    : "v0","v1","v2","v3","v4","v5","v6","v7","v8","v9","v10","v11","v12","v13","v14","v15","v16","v17","v18","v19","v20","v21","v22","v23","v24","v25","v26","v27","v28","v29","v30","v31","v32","v33","v34","v35","v36","v37","v38","v39","v40","v41","v42","v43","v44","v45","v46","v47","v48","v49","v50","v51","v52","v53","v54","v55","v56","v57","v58","v59","v60","v61","v62","v63","v64","v65","v66","v67","v68","v69","v70","v71","v72","v73","v74","v75","v76","v77","v78","v79","v80","v81","v82","v83","v84","v85","v86","v87","v88","v89","v90","v91","v92","v93","v94","v95","v96","v97","v98","v99","v100","v101","v102","v103","v104","v105","v106","v107","v108","v109","v110","v111","v112","v113","v114","v115","v116","v117","v118","v119","v120","v121","v122","v123","v124","v125","v126","v127","v128","v129","v130","v131","v132","v133","v134","v135","v136","v137","v138","v139","v140","v141","v142","v143","v144","v145","v146","v147","v148","v149","v150","v151","v152","v153","v154","v155","v156","v157","v158","v159","v160","v161","v162","v163","v164","v165","v166","v167","v168","v169","v170","v171","v172","v173","v174","v175","v176","v177","v178","v179","v180","v181","v182","v183","v184","v185","v186","v187","v188","v189","v190","v191","v192","v193","v194","v195","v196","v197","v198","v199","v200","v201","v202","v203","v204","v205","v206","v207","v208","v209","v210","v211","v212","v213","v214","v215","v216","v217","v218","v219","v220","v221","v222","v223","v224","v225","v226","v227","v228","v229","v230","v231","v232","v233","v234","v235","v236","v237","v238","v239","v240","v241","v242","v243","v244","v245","v246","v247","v248","v249","v250","v251","v252","v253","v254","v255","a0","a1","a2","a3","a4","a5","a6","a7","a8","a9","a10","a11","a12","a13","a14","a15","a16","a17","a18","a19","a20","a21","a22","a23","a24","a25","a26","a27","a28","a29","a30","a31","a32","a33","a34","a35","a36","a37","a38","a39","a40","a41","a42","a43","a44","a45","a46","a47","a48","a49","a50","a51","a52","a53","a54","a55","a56","a57","a58","a59","a60","a61","a62","a63","a64","a65","a66","a67","a68","a69","a70","a71","a72","a73","a74","a75","a76","a77","a78","a79","a80","a81","a82","a83","a84","a85","a86","a87","a88","a89","a90","a91","a92","a93","a94","a95","a96","a97","a98","a99","a100","a101","a102","a103","a104","a105","a106","a107","a108","a109","a110","a111","a112","a113","a114","a115","a116","a117","a118","a119","a120","a121","a122","a123","a124","a125","a126","a127","a128","a129","a130","a131","a132","a133","a134","a135","a136","a137","a138","a139","a140","a141","a142","a143","a144","a145","a146","a147","a148","a149","a150","a151","a152","a153","a154","a155","a156","a157","a158","a159","a160","a161","a162","a163","a164","a165","a166","a167","a168","a169","a170","a171","a172","a173","a174","a175","a176","a177","a178","a179","a180","a181","a182","a183","a184","a185","a186","a187","a188","a189","a190","a191","a192","a193","a194","a195","a196","a197","a198","a199","a200","a201","a202","a203","a204","a205","a206","a207","a208","a209","a210","a211","a212","a213","a214","a215","a216","a217","a218","a219","a220","a221","a222","a223","a224","a225","a226","a227","a228","a229","a230","a231","a232","a233","a234","a235","a236","a237","a238","a239","a240","a241","a242","a243","a244","a245","a246","a247","a248","a249","a250","a251","a252","a253","a254","a255","s6","s7","s8","s9","s10","s11","s12","s13","s14","s15","s16","s17","s18","s19","s20","s21","s22","s23","s24","s25","s26","s27","s28","s29","s30","s31","s32","s33","s34","s35","s36","s37","s38","s39","s40","s41","s42","s43","s44","s45","s46","s47","s48","s49","s50","s51","s52","s53","s54","s55","s56","s57","s58","s59","s60","s61","s62","s63","s64","s65","s66","s67","s68","s69","s70","s71","s72","s73","s74","s75","s76","s77","s78","s79","s80","s81","s82","s83","s84","s85","s86","s87","s88","s89","s90","s91","s92","s93","s94","s95","s96","s97","s98","s99","s100","vcc","scc","m0","memory");
}
extern "C" void kernel_launch(void* const* d_in, const int* in_sizes, int n_in, void* d_out, int out_size, void* d_ws, size_t ws_size, hipStream_t stream) {
  char* xch = (char*)d_ws + ((ws_size - (size_t)270532608) & ~(size_t)0xfffff);
  (void)hipFuncSetAttribute((const void*)fused_attn, hipFuncAttributeMaxDynamicSharedMemorySize, 163840);
  hipLaunchKernelGGL(fused_attn, dim3(256), dim3(256), 163840, stream,
                     (const char*)d_in[0], (const char*)d_in[1], (const char*)d_in[2], (const char*)d_in[3], (const char*)d_in[4],
                     (const char*)d_in[5], (const char*)d_in[6], (const char*)d_in[7], (const char*)d_in[8], (const char*)d_in[9], xch, (char*)d_out, (char*)d_ws);
}
```

```cpp
#include <hip/hip_runtime.h>
#include <hip/hip_bf16.h>
#include <stdint.h>

__global__ __launch_bounds__(256, 1)
void fused_attn(const char* xA, const char* kA, const char* vA, const char* wA, const char* uA,
                const char* xB, const char* kB, const char* vB, const char* wB, const char* uB, char* xch, char* out, char* flags) {
  extern __shared__ __attribute__((aligned(16))) char lds[];
  const unsigned wid = __builtin_amdgcn_readfirstlane(threadIdx.x >> 6);
  const unsigned long long ka = (unsigned long long)__builtin_amdgcn_kernarg_segment_ptr();
  asm volatile(
"s_memtime s[64:65]\n"
"s_memrealtime s[66:67]\n"
"s_bfe_u32 s10, %[wg], 0x10002\n"
"s_mul_i32 s40, s10, 40\n"
"s_add_u32 s40, s40, %[kalo]\n"
"s_addc_u32 s41, %[kahi], 0\n"
"s_mov_b32 s78, %[kalo]\n"
"s_mov_b32 s79, %[kahi]\n"
"s_load_dwordx2 s[24:25], s[40:41], 0\n"
"s_load_dwordx2 s[28:29], s[40:41], 24\n"
"s_load_dwordx2 s[6:7], s[78:79], 0x60\n"
"s_load_dwordx2 s[16:17], s[40:41], 8\n"
"s_load_dwordx2 s[20:21], s[40:41], 16\n"
"s_load_dwordx2 s[8:9], s[40:41], 32\n"
"s_load_dwordx4 s[12:15], s[78:79], 0x50\n"
"s_mov_b32 s42, %[wid]\n"
"s_and_b32 s11, %[wg], 1\n"
"s_lshl_b32 s11, s11, 3\n"
"s_lshr_b32 s60, %[wg], 5\n"
"s_add_u32 s11, s11, s60\n"
"s_bfe_u32 s61, %[wg], 0x10001\n"
"s_bfe_u32 s60, %[wg], 0x20003\n"
"s_lshl2_add_u32 s61, s61, s60\n"
"s_getpc_b64 s[80:81]\n"
"LCP_%=:\n"
"s_mov_b32 s82, LEND_%=-LCP_%=\n"
"s_mov_b32 s83, 0x20000\n"
"v_mbcnt_lo_u32_b32 v2, -1, 0\n"
"v_lshlrev_b32 v2, 7, v2\n"
"s_lshr_b32 s60, %[wg], 3\n"
"s_lshl2_add_u32 s60, s60, s42\n"
"s_mul_i32 s60, s60, 640\n"
"s_mov_b64 exec, 31\n"
"buffer_load_dword v3, v2, s[80:83], s60 offen\n"
"s_mov_b64 exec, -1\n"
"s_mov_b32 s19, 0x20000\n"
"s_mov_b32 s23, 0x20000\n"
"s_mov_b32 s27, 0x20000\n"
"s_mov_b32 s31, 0x20000\n"
"s_mov_b32 s35, 0x20000\n"
"s_mov_b32 s39, 0x20000\n"
"s_mov_b32 s26, 286720\n"
"s_lshl_b32 s44, s42, 12\n"
"s_mov_b32 s50, s44\n"
"s_mul_i32 s51, s42, 7168\n"
"s_add_u32 s51, s51, 0x10000\n"
"s_mov_b32 s74, 0x8000\n"
"s_mov_b32 s58, 524288\n"
"s_mov_b32 s47, 0\n"
"s_add_u32 s60, s11, s61\n"
"s_and_b32 s60, s60, 0\n"
"s_mul_i32 s60, s60, 3\n"
"s_lshl_b32 s49, s60, 7\n"
"s_lshl_b32 s48, s60, 19\n"
"v_mbcnt_lo_u32_b32 v4, -1, 0\n"
"v_mbcnt_hi_u32_b32 v4, -1, v4\n"
"v_and_b32 v5, 15, v4\n"
"v_lshrrev_b32 v6, 4, v4\n"
"v_lshrrev_b32 v7, 3, v4\n"
"v_and_b32 v8, 7, v4\n"
"v_lshlrev_b32 v9, 1, v6\n"
"v_xor_b32 v9, v9, v8\n"
"v_lshlrev_b32 v9, 4, v9\n"
"s_mul_i32 s60, s42, 71680\n"
"v_mul_u32_u24 v10, 1280, v7\n"
"v_add3_u32 v248, v10, v9, s60\n"
"v_add_u32 v249, 9216, v248\n"
"v_add_u32 v254, 18432, v248\n"
"v_add_u32 v255, 27648, v248\n"
"v_and_b32 v10, 2, v7\n"
"v_xor_b32 v240, v10, v8\n"
"v_lshlrev_b32 v240, 4, v240\n"
"v_bfe_u32 v10, v7, 0, 1\n"
"v_lshl_add_u32 v240, v10, 8, v240\n"
"v_bfe_u32 v10, v7, 1, 1\n"
"v_lshl_add_u32 v240, v10, 9, v240\n"
"v_bfe_u32 v10, v7, 2, 1\n"
"v_lshl_add_u32 v240, v10, 7, v240\n"
"s_and_b32 s60, s42, 1\n"
"v_lshl_add_u32 v240, s60, 15, v240\n"
"s_lshr_b32 s60, s42, 1\n"
"v_lshl_add_u32 v240, s60, 16, v240\n"
"v_and_b32 v10, 1, v6\n"
"v_xor_b32 v10, v10, v8\n"
"v_lshlrev_b32 v10, 4, v10\n"
"v_lshl_add_u32 v10, v7, 13, v10\n"
"s_lshl_b32 s60, s42, 17\n"
"v_add_u32 v10, s60, v10\n"
"v_add_u32 v224, 1024, v10\n"
"v_add_u32 v225, 128, v10\n"
"v_xor_b32 v10, 64, v10\n"
"v_add_u32 v226, 64512, v10\n"
"v_add_u32 v227, 63616, v10\n"
"s_mul_i32 s40, s11, 286720\n"
"s_lshl_b32 s41, s61, 10\n"
"s_sub_u32 s30, 5243904, s41\n"
"s_sub_u32 s41, s41, 1024\n"
"s_ashr_i32 s60, s41, 31\n"
"s_lshl4_add_u32 s92, s10, s11\n"
"s_lshl_b32 s93, s92, 9\n"
"v_mov_b32 v0, 0\n"
"s_waitcnt lgkmcnt(0)\n"
"s_add_u32 s24, s24, s40\n"
"s_addc_u32 s25, s25, 0\n"
"s_and_b32 s25, s25, 0xffff\n"
"s_add_u32 s28, s28, s41\n"
"s_addc_u32 s29, s29, s60\n"
"s_and_b32 s29, s29, 0xffff\n"
"s_add_u32 s6, s6, s93\n"
"s_addc_u32 s7, s7, 0\n"
"buffer_load_dword v12, v248, s[24:27], s49 offen\n"
"buffer_load_dword v13, v224, s[28:31], s48 offen\n"
"global_load_dword v1, v0, s[6:7] offset:128 sc1\n"
"s_mov_b32 m0, s51\n"
"s_nop 0\n"
"buffer_load_dwordx4 v248, s[24:27], s49 offen lds\n"
"buffer_load_dwordx4 v249, s[24:27], s49 offen offset:1024 lds\n"
"buffer_load_dwordx4 v254, s[24:27], s49 offen offset:2048 lds\n"
"buffer_load_dwordx4 v255, s[24:27], s49 offen offset:3072 lds\n"
"s_add_u32 m0, s51, 4096\n"
"s_add_u32 s40, s49, 40960\n"
"buffer_load_dwordx4 v248, s[24:27], s40 offen lds\n"
"buffer_load_dwordx4 v249, s[24:27], s40 offen offset:1024 lds\n"
"buffer_load_dwordx4 v254, s[24:27], s40 offen offset:2048 lds\n"
"s_add_u32 s49, s49, 128\n"
"s_sub_u32 s41, s49, 1280\n"
"s_min_u32 s49, s49, s41\n"
"s_xor_b32 s51, s51, s74\n"
"s_mov_b32 m0, s50\n"
"s_nop 0\n"
"buffer_load_dwordx4 v224, s[28:31], s48 offen lds\n"
"buffer_load_dwordx4 v225, s[28:31], s48 offen offset:1024 lds\n"
"buffer_load_dwordx4 v226, s[28:31], s48 offen offset:2048 lds\n"
"buffer_load_dwordx4 v227, s[28:31], s48 offen offset:3072 lds\n"
"s_xor_b32 s50, s50, s74\n"
"s_lshl2_add_u32 s63, s11, s42\n"
"s_mul_i32 s63, s63, 10240\n"
"s_and_b32 s78, s42, 1\n"
"s_lshl_b32 s43, s78, 6\n"
"s_mov_b32 s77, 0x80000000\n"
"s_mov_b32 s75, 0x10000\n"
"s_mov_b32 s73, 0xffff0000\n"
"s_mov_b32 s46, 0x3e0293ee\n"
"s_mov_b32 s45, 0x63800000\n"
"s_mov_b32 s62, 9\n"
"v_lshl_add_u32 v242, v4, 4, s44\n"
"s_lshr_b32 s79, s42, 1\n"
"s_lshl4_add_u32 s76, s79, s78\n"
"s_lshl_b32 s76, s76, 10\n"
"s_add_u32 s76, s76, 0x20000\n"
"s_sub_u32 s76, s76, s44\n"
"v_lshlrev_b32 v9, 6, v6\n"
"v_xor_b32 v9, v9, v242\n"
"v_add_u32 v243, s76, v9\n"
"v_and_b32 v9, 6, v5\n"
"s_xor_b32 m0, s50, 0xc000\n"
"s_add_u32 s40, s48, 256\n"
"buffer_load_dwordx4 v224, s[28:31], s40 offen lds\n"
"v_xor_b32 v9, v9, v6\n"
"v_lshlrev_b32 v9, 4, v9\n"
"v_lshl_add_u32 v10, v5, 7, v9\n"
"s_mul_i32 s79, s42, 6144\n"
"s_add_u32 s79, s79, 0x10000\n"
"v_add_u32 v246, s79, v10\n"
"v_xor_b32 v247, 64, v246\n"
"s_lshr_b32 s79, s42, 1\n"
"s_lshl_b32 s79, s79, 11\n"
"buffer_load_dwordx4 v225, s[28:31], s40 offen offset:1024 lds\n"
"s_add_u32 s79, s79, 90112\n"
"v_add_u32 v252, s79, v10\n"
"v_xor_b32 v253, 64, v252\n"
"s_xor_b32 s79, s42, 1\n"
"s_mul_i32 s79, s79, 6144\n"
"s_add_u32 s79, s79, 0x10000\n"
"v_add_u32 v250, s79, v10\n"
"v_xor_b32 v251, 64, v250\n"
"s_mul_i32 s79, s78, 8256\n"
"buffer_load_dwordx4 v226, s[28:31], s40 offen offset:2048 lds\n"
"v_lshlrev_b32 v9, 10, v6\n"
"v_bfe_u32 v10, v4, 2, 2\n"
"v_lshl_add_u32 v9, v10, 7, v9\n"
"v_and_b32 v10, 3, v4\n"
"v_lshl_add_u32 v9, v10, 3, v9\n"
"v_bfe_u32 v10, v4, 3, 2\n"
"v_lshl_add_u32 v9, v10, 5, v9\n"
"s_add_u32 s79, s79, 0x10000\n"
"v_xor_b32 v232, s79, v9\n"
"buffer_load_dwordx4 v227, s[28:31], s40 offen offset:3072 lds\n"
"s_cmp_eq_u32 s47, 8\n"
"s_cselect_b32 s41, s52, s58\n"
"s_add_u32 s48, s48, s41\n"
"s_sub_u32 s41, s48, 5242880\n"
"s_min_u32 s48, s48, s41\n"
"v_xor_b32 v233, 32, v232\n"
"v_xor_b32 v234, 64, v232\n"
"v_xor_b32 v235, 96, v232\n"
"v_xor_b32 v236, 0x2000, v232\n"
"v_xor_b32 v237, 0x2000, v233\n"
"v_xor_b32 v238, 0x2000, v234\n"
"v_xor_b32 v239, 0x2000, v235\n"
"v_lshlrev_b32 v9, 11, v6\n"
"v_bfe_u32 v10, v4, 2, 2\n"
"s_mov_b32 m0, s50\n"
"s_nop 0\n"
"buffer_load_dwordx4 v224, s[28:31], s48 offen lds\n"
"v_lshl_add_u32 v9, v10, 7, v9\n"
"v_and_b32 v10, 1, v4\n"
"v_lshl_add_u32 v9, v10, 3, v9\n"
"v_bfe_u32 v10, v4, 1, 1\n"
"v_lshl_add_u32 v9, v10, 5, v9\n"
"v_bfe_u32 v10, v4, 3, 1\n"
"v_lshl_add_u32 v9, v10, 4, v9\n"
"v_bfe_u32 v10, v4, 4, 1\n"
"v_lshl_add_u32 v9, v10, 6, v9\n"
"buffer_load_dwordx4 v225, s[28:31], s48 offen offset:1024 lds\n"
"v_xor_b32 v244, s43, v9\n"
"v_xor_b32 v245, 16, v244\n"
"s_lshl3_add_u32 s79, s11, s61\n"
"s_lshl_b32 s79, s79, 20\n"
"s_sub_u32 s79, s79, 262144\n"
"s_ashr_i32 s60, s79, 31\n"
"s_add_u32 s16, s16, s79\n"
"s_addc_u32 s17, s17, s60\n"
"s_and_b32 s17, s17, 0xffff\n"
"buffer_load_dwordx4 v226, s[28:31], s48 offen offset:2048 lds\n"
"s_mov_b32 s18, 1310720\n"
"s_mov_b64 s[84:85], s[16:17]\n"
"s_mov_b64 s[86:87], s[18:19]\n"
"s_add_u32 s20, s20, s79\n"
"s_addc_u32 s21, s21, s60\n"
"s_and_b32 s21, s21, 0xffff\n"
"s_mov_b32 s22, 1310720\n"
"s_mov_b64 s[88:89], s[20:21]\n"
"s_mov_b64 s[90:91], s[22:23]\n"
"buffer_load_dwordx4 v227, s[28:31], s48 offen offset:3072 lds\n"
"s_xor_b32 s50, s50, s74\n"
"s_mov_b32 s57, 122880\n"
"s_bfe_u32 s55, %[wg], 65541\n"
"s_mul_i32 s55, s55, 1024\n"
"s_xor_b32 s57, s57, s55\n"
"s_bfe_u32 s55, %[wg], 65542\n"
"s_mul_i32 s55, s55, 2048\n"
"s_xor_b32 s57, s57, s55\n"
"s_and_b32 s54, %[wg], 144\n"
"s_bcnt1_i32_b32 s54, s54\n"
"s_mov_b32 m0, s51\n"
"s_nop 0\n"
"buffer_load_dwordx4 v248, s[24:27], s49 offen lds\n"
"s_and_b32 s54, s54, 1\n"
"s_lshl_b32 s54, s54, 13\n"
"s_xor_b32 s57, s57, s54\n"
"s_lshl_b32 s54, s54, 1\n"
"s_sub_u32 s54, 21504, s54\n"
"s_mov_b32 s55, 385024\n"
"s_xor_b32 s56, s55, s57\n"
"s_mov_b32 s59, 1024\n"
"s_add_u32 s52, s11, s61\n"
"buffer_load_dwordx4 v249, s[24:27], s49 offen offset:1024 lds\n"
"s_and_b32 s52, s52, 0\n"
"s_mul_i32 s52, s52, 3\n"
"s_add_u32 s52, s52, 1\n"
"s_lshl_b32 s53, s52, 7\n"
"s_lshl_b32 s52, s52, 19\n"
"s_add_u32 s52, s52, 512\n"
"s_mul_i32 s79, s61, 655360\n"
"s_add_u32 s8, s8, s79\n"
"s_addc_u32 s9, s9, 0\n"
"buffer_load_dwordx4 v254, s[24:27], s49 offen offset:2048 lds\n"
"s_and_b32 s9, s9, 0xffff\n"
"s_add_u32 s12, s12, 0x1fffff\n"
"s_addc_u32 s13, s13, 0\n"
"s_and_b32 s12, s12, 0xffe00000\n"
"s_mul_i32 s96, s42, 3\n"
"s_and_b32 s96, s96, 5\n"
"s_cmp_eq_u32 s42, 3\n"
"s_cselect_b32 s96, 5, s96\n"
"s_lshl_b32 s79, s61, 8\n"
"buffer_load_dwordx4 v255, s[24:27], s49 offen offset:3072 lds\n"
"s_add_u32 s96, s96, s79\n"
"s_add_u32 s96, s96, 160\n"
"s_lshl_b32 s96, s96, 12\n"
"s_mul_i32 s79, s92, 8388608\n"
"s_add_u32 s32, s12, s79\n"
"s_addc_u32 s33, s13, 0\n"
"s_and_b32 s33, s33, 0xffff\n"
"s_mov_b32 s34, 8388608\n"
"s_mul_i32 s79, s11, 573440\n"
"s_add_u32 m0, s51, 4096\n"
"s_add_u32 s40, s49, 40960\n"
"buffer_load_dwordx4 v248, s[24:27], s40 offen lds\n"
"s_mul_i32 s60, s10, 1280\n"
"s_sub_u32 s38, 573440, s60\n"
"s_add_u32 s79, s79, s60\n"
"s_add_u32 s36, s14, s79\n"
"s_addc_u32 s37, s15, 0\n"
"s_and_b32 s37, s37, 0xffff\n"
"s_lshl2_add_u32 s93, s61, s42\n"
"s_add_u32 s95, s61, 1\n"
"s_and_b32 s95, s95, 7\n"
"buffer_load_dwordx4 v249, s[24:27], s40 offen offset:1024 lds\n"
"s_lshl_b32 s95, s95, 7\n"
"s_add_u32 s100, s61, 2\n"
"s_and_b32 s100, s100, 7\n"
"s_lshl_b32 s100, s100, 7\n"
"s_lshl_b32 s60, s61, 7\n"
"s_add_u32 s60, s60, s43\n"
"s_mul_i32 s97, s42, 122880\n"
"s_add_u32 s97, s97, s60\n"
"s_lshr_b32 s98, s42, 1\n"
"buffer_load_dwordx4 v254, s[24:27], s40 offen offset:2048 lds\n"
"s_add_u32 s49, s49, 128\n"
"s_sub_u32 s41, s49, 1280\n"
"s_min_u32 s49, s49, s41\n"
"s_xor_b32 s51, s51, s74\n"
"s_add_u32 s98, s98, 12\n"
"s_mul_i32 s98, s98, 40960\n"
"s_add_u32 s98, s98, s60\n"
"s_mul_i32 s60, s93, 37\n"
"s_lshr_b32 s60, s60, 8\n"
"s_mul_i32 s79, s60, 7\n"
"s_sub_u32 s79, s93, s79\n"
"s_lshl_b32 s94, s60, 4\n"
"s_add_u32 s94, s94, s79\n"
"s_lshl_b32 s94, s94, 10\n"
"s_add_u32 s94, s94, 524288\n"
"s_mul_i32 s99, s60, 3\n"
"s_lshr_b32 s78, s79, 1\n"
"s_add_u32 s99, s99, s78\n"
"s_lshr_b32 s78, s60, 1\n"
"s_add_u32 s78, s78, 12\n"
"s_cmp_eq_u32 s79, 6\n"
"s_cselect_b32 s99, s78, s99\n"
"s_mul_i32 s99, s99, 40960\n"
"s_xor_b32 s79, s79, s60\n"
"s_and_b32 s79, s79, 1\n"
"s_lshl_b32 s79, s79, 6\n"
"s_add_u32 s99, s99, s79\n"
"s_add_u32 s99, s99, 1024\n"
"s_cmp_eq_u32 s61, 7\n"
"s_cselect_b32 s99, 0x40000000, s99\n"
"s_cselect_b32 s94, 0x40000000, s94\n"
"s_waitcnt vmcnt(27)\n"
"v_and_b32 v12, 0x7fff0000, v12\n"
"v_and_b32 v13, 0x7fff0000, v13\n"
"s_nop 0\n"
"v_max_u32_dpp v12, v12, v12 quad_perm:[1,0,3,2] row_mask:0xf bank_mask:0xf\n"
"v_max_u32_dpp v13, v13, v13 quad_perm:[1,0,3,2] row_mask:0xf bank_mask:0xf\n"
"s_nop 0\n"
"v_max_u32_dpp v12, v12, v12 quad_perm:[2,3,0,1] row_mask:0xf bank_mask:0xf\n"
"v_max_u32_dpp v13, v13, v13 quad_perm:[2,3,0,1] row_mask:0xf bank_mask:0xf\n"
"s_nop 0\n"
"v_max_u32_dpp v12, v12, v12 row_half_mirror row_mask:0xf bank_mask:0xf\n"
"v_max_u32_dpp v13, v13, v13 row_half_mirror row_mask:0xf bank_mask:0xf\n"
"s_nop 0\n"
"v_min_u32_dpp v12, v12, v12 row_mirror row_mask:0xf bank_mask:0xf\n"
"v_min_u32_dpp v13, v13, v13 row_mirror row_mask:0xf bank_mask:0xf\n"
"s_nop 0\n"
"v_min_u32_dpp v12, v12, v12 row_bcast:15 row_mask:0xa bank_mask:0xf\n"
"v_min_u32_dpp v13, v13, v13 row_bcast:15 row_mask:0xa bank_mask:0xf\n"
"s_nop 0\n"
"v_min_u32_dpp v12, v12, v12 row_bcast:31 row_mask:0xc bank_mask:0xf\n"
"v_min_u32_dpp v13, v13, v13 row_bcast:31 row_mask:0xc bank_mask:0xf\n"
"s_nop 0\n"
"v_mul_f32 v12, v12, v13\n"
"v_mul_f32 v12, 0x43c80000, v12\n"
"v_min_f32 v12, 0x71800000, v12\n"
"v_mul_f32 v13, s46, v12\n"
"s_nop 0\n"
"v_readlane_b32 s70, v12, 63\n"
"v_readlane_b32 s71, v12, 63\n"
"v_readlane_b32 s69, v13, 63\n"
"s_nop 1\n"
"v_mov_b64 v[228:229], s[70:71]\n"
"v_mov_b64 v[230:231], s[70:71]\n"
"s_waitcnt vmcnt(26)\n"
"v_readfirstlane_b32 s92, v1\n"
"v_mov_b64 v[0:1], s[70:71]\n"
"v_mov_b64 v[2:3], s[70:71]\n"
"s_mov_b32 s70, s46\n"
"s_xor_b32 s71, s69, s77\n"
"s_mov_b32 s65, s64\n"
"s_add_u32 s66, s66, 2800\n"
"s_mov_b32 s67, s66\n"
"s_mov_b32 s72, 118\n"
"s_mov_b32 s83, 0x20000\n"
"s_nop 2\n"
"s_mul_i32 s60, s92, 0x9e3779b1\n"
"s_add_u32 s60, s60, 0x7f4a7c15\n"
"s_waitcnt vmcnt(15)\n"
"s_barrier\n"
"ds_read_b128 a[136:139], v246 offset:0\n"
"ds_read_b128 a[140:143], v247 offset:0\n"
"ds_read_b64_tr_b16 a[192:193], v244 offset:0\n"
"ds_read_b64_tr_b16 a[194:195], v244 offset:512\n"
"ds_read_b64_tr_b16 a[196:197], v244 offset:8192\n"
"ds_read_b64_tr_b16 a[198:199], v244 offset:8704\n"
"ds_read_b64_tr_b16 a[200:201], v245 offset:0\n"
"ds_read_b64_tr_b16 a[202:203], v245 offset:512\n"
"ds_read_b64_tr_b16 a[204:205], v245 offset:8192\n"
"ds_read_b64_tr_b16 a[206:207], v245 offset:8704\n"
"ds_read_b64_tr_b16 a[208:209], v244 offset:1024\n"
"ds_read_b64_tr_b16 a[210:211], v244 offset:1536\n"
"ds_read_b64_tr_b16 a[212:213], v244 offset:9216\n"
"ds_read_b64_tr_b16 a[214:215], v244 offset:9728\n"
"s_waitcnt lgkmcnt(10)\n"
"v_mfma_f32_16x16x32_bf16 v[112:115], a[192:195], a[136:139], v[228:231]\n"
"ds_read_b64_tr_b16 a[216:217], v245 offset:1024\n"
"ds_read_b64_tr_b16 a[218:219], v245 offset:1536\n"
"s_waitcnt lgkmcnt(10)\n"
"v_mfma_f32_16x16x32_bf16 v[112:115], a[196:199], a[140:143], v[112:115]\n"
"ds_read_b64_tr_b16 a[220:221], v245 offset:9216\n"
"ds_read_b64_tr_b16 a[222:223], v245 offset:9728\n"
"s_waitcnt lgkmcnt(10)\n"
"v_mfma_f32_16x16x32_bf16 v[116:119], a[200:203], a[136:139], v[228:231]\n"
"s_waitcnt lgkmcnt(8)\n"
"ds_read_b128 a[144:147], v246 offset:2048\n"
"v_mfma_f32_16x16x32_bf16 v[116:119], a[204:207], a[140:143], v[116:119]\n"
"s_waitcnt lgkmcnt(7)\n"
"ds_read_b128 a[148:151], v247 offset:2048\n"
"v_mfma_f32_16x16x32_bf16 v[120:123], a[208:211], a[136:139], v[228:231]\n"
"s_waitcnt lgkmcnt(6)\n"
"ds_read_b128 a[152:155], v246 offset:4096\n"
"v_mfma_f32_16x16x32_bf16 v[120:123], a[212:215], a[140:143], v[120:123]\n"
"s_waitcnt lgkmcnt(5)\n"
"ds_read_b128 a[156:159], v247 offset:4096\n"
"v_mfma_f32_16x16x32_bf16 v[124:127], a[216:219], a[136:139], v[228:231]\n"
"s_waitcnt lgkmcnt(4)\n"
"ds_read_b128 a[160:163], v252 offset:0\n"
"v_mfma_f32_16x16x32_bf16 v[124:127], a[220:223], a[140:143], v[124:127]\n"
"s_waitcnt lgkmcnt(4)\n"
"ds_read_b128 a[164:167], v253 offset:0\n"
"v_mfma_f32_16x16x32_bf16 v[140:143], a[216:219], a[144:147], v[228:231]\n"
"s_waitcnt lgkmcnt(4)\n"
"ds_read_b128 a[168:171], v250 offset:0\n"
"v_mfma_f32_16x16x32_bf16 v[140:143], a[220:223], a[148:151], v[140:143]\n"
"ds_read_b128 a[172:175], v251 offset:0\n"
"v_mfma_f32_16x16x32_bf16 v[136:139], a[208:211], a[144:147], v[228:231]\n"
"s_waitcnt vmcnt(11)\n"
"ds_read_b128 a[176:179], v250 offset:2048\n"
"v_mfma_f32_16x16x32_bf16 v[136:139], a[212:215], a[148:151], v[136:139]\n"
"s_barrier\n"
"v_mfma_f32_16x16x32_bf16 v[132:135], a[200:203], a[144:147], v[228:231]\n"
"s_add_u32 s40, s48, 256\n"
"s_xor_b32 m0, s50, 0xc000\n"
"ds_read_b128 a[180:183], v251 offset:2048\n"
"v_mfma_f32_16x16x32_bf16 v[132:135], a[204:207], a[148:151], v[132:135]\n"
"buffer_load_dwordx4 v224, s[28:31], s40 offen lds\n"
"v_mfma_f32_16x16x32_bf16 v[128:131], a[192:195], a[144:147], v[228:231]\n"
"ds_read_b128 a[184:187], v250 offset:4096\n"
"v_mfma_f32_16x16x32_bf16 v[128:131], a[196:199], a[148:151], v[128:131]\n"
"s_waitcnt lgkmcnt(5)\n"
"ds_read_b128 a[188:191], v251 offset:4096\n"
"v_mfma_f32_16x16x32_bf16 v[144:147], a[192:195], a[152:155], v[228:231]\n"
"ds_read_b64_tr_b16 a[224:225], v244 offset:16384\n"
"v_mfma_f32_16x16x32_bf16 v[144:147], a[196:199], a[156:159], v[144:147]\n"
"ds_read_b64_tr_b16 a[226:227], v244 offset:16896\n"
"v_mfma_f32_16x16x32_bf16 v[148:151], a[200:203], a[152:155], v[228:231]\n"
"ds_read_b64_tr_b16 a[228:229], v244 offset:24576\n"
"v_mfma_f32_16x16x32_bf16 v[148:151], a[204:207], a[156:159], v[148:151]\n"
"buffer_load_dwordx4 v225, s[28:31], s40 offen offset:1024 lds\n"
"v_mfma_f32_16x16x32_bf16 v[152:155], a[208:211], a[152:155], v[228:231]\n"
"ds_read_b64_tr_b16 a[230:231], v244 offset:25088\n"
"v_mfma_f32_16x16x32_bf16 v[152:155], a[212:215], a[156:159], v[152:155]\n"
"ds_read_b64_tr_b16 a[232:233], v245 offset:16384\n"
"v_mfma_f32_16x16x32_bf16 v[156:159], a[216:219], a[152:155], v[228:231]\n"
"ds_read_b64_tr_b16 a[234:235], v245 offset:16896\n"
"v_mfma_f32_16x16x32_bf16 v[156:159], a[220:223], a[156:159], v[156:159]\n"
"ds_read_b64_tr_b16 a[236:237], v245 offset:24576\n"
"v_mfma_f32_16x16x32_bf16 v[172:175], a[216:219], a[160:163], v[228:231]\n"
"ds_read_b64_tr_b16 a[238:239], v245 offset:25088\n"
"v_mfma_f32_16x16x32_bf16 v[172:175], a[220:223], a[164:167], v[172:175]\n"
"buffer_load_dwordx4 v226, s[28:31], s40 offen offset:2048 lds\n"
"v_mfma_f32_16x16x32_bf16 v[168:171], a[208:211], a[160:163], v[228:231]\n"
"ds_read_b64_tr_b16 a[240:241], v244 offset:17408\n"
"v_mfma_f32_16x16x32_bf16 v[168:171], a[212:215], a[164:167], v[168:171]\n"
"ds_read_b64_tr_b16 a[242:243], v244 offset:17920\n"
"v_mfma_f32_16x16x32_bf16 v[164:167], a[200:203], a[160:163], v[228:231]\n"
"ds_read_b64_tr_b16 a[244:245], v244 offset:25600\n"
"v_mfma_f32_16x16x32_bf16 v[164:167], a[204:207], a[164:167], v[164:167]\n"
"buffer_load_dwordx4 v227, s[28:31], s40 offen offset:3072 lds\n"
"v_mfma_f32_16x16x32_bf16 v[160:163], a[192:195], a[160:163], v[228:231]\n"
"s_cmp_eq_u32 s47, 8\n"
"ds_read_b64_tr_b16 a[246:247], v244 offset:26112\n"
"v_xor_b32 v244, s74, v244\n"
"v_mfma_f32_16x16x32_bf16 v[160:163], a[196:199], a[164:167], v[160:163]\n"
"s_waitcnt lgkmcnt(5)\n"
"s_cselect_b32 s41, s52, s58\n"
"ds_read_b64_tr_b16 a[248:249], v245 offset:17408\n"
"v_xor_b32 v246, s74, v246\n"
"v_mfma_f32_16x16x32_bf16 v[176:179], a[192:195], a[168:171], v[228:231]\n"
"s_add_u32 s48, s48, s41\n"
"ds_read_b64_tr_b16 a[250:251], v245 offset:17920\n"
"v_xor_b32 v247, s74, v247\n"
"v_mfma_f32_16x16x32_bf16 v[176:179], a[196:199], a[172:175], v[176:179]\n"
"s_sub_u32 s41, s48, 5242880\n"
"ds_read_b64_tr_b16 a[252:253], v245 offset:25600\n"
"v_xor_b32 v252, s74, v252\n"
"v_mfma_f32_16x16x32_bf16 v[180:183], a[200:203], a[168:171], v[228:231]\n"
"s_min_u32 s48, s48, s41\n"
"ds_read_b64_tr_b16 a[254:255], v245 offset:26112\n"
"v_xor_b32 v245, s74, v245\n"
"v_mfma_f32_16x16x32_bf16 v[180:183], a[204:207], a[172:175], v[180:183]\n"
"v_xor_b32 v253, s74, v253\n"
"v_xor_b32 v250, s74, v250\n"
"v_mfma_f32_16x16x32_bf16 v[184:187], a[208:211], a[168:171], v[228:231]\n"
"s_mov_b32 m0, s50\n"
"v_xor_b32 v251, s74, v251\n"
"v_mfma_f32_16x16x32_bf16 v[184:187], a[212:215], a[172:175], v[184:187]\n"
"buffer_load_dwordx4 v224, s[28:31], s48 offen lds\n"
"v_mfma_f32_16x16x32_bf16 v[188:191], a[216:219], a[168:171], v[228:231]\n"
"v_mfma_f32_16x16x32_bf16 v[188:191], a[220:223], a[172:175], v[188:191]\n"
"v_mfma_f32_16x16x32_bf16 v[204:207], a[216:219], a[176:179], v[228:231]\n"
"v_mfma_f32_16x16x32_bf16 v[204:207], a[220:223], a[180:183], v[204:207]\n"
"buffer_load_dwordx4 v225, s[28:31], s48 offen offset:1024 lds\n"
"v_mfma_f32_16x16x32_bf16 v[200:203], a[208:211], a[176:179], v[228:231]\n"
"v_mfma_f32_16x16x32_bf16 v[200:203], a[212:215], a[180:183], v[200:203]\n"
"v_mfma_f32_16x16x32_bf16 v[196:199], a[200:203], a[176:179], v[228:231]\n"
"v_mfma_f32_16x16x32_bf16 v[196:199], a[204:207], a[180:183], v[196:199]\n"
"v_mfma_f32_16x16x32_bf16 v[192:195], a[192:195], a[176:179], v[228:231]\n"
"v_mfma_f32_16x16x32_bf16 v[192:195], a[196:199], a[180:183], v[192:195]\n"
"buffer_load_dwordx4 v226, s[28:31], s48 offen offset:2048 lds\n"
"v_mfma_f32_16x16x32_bf16 v[208:211], a[192:195], a[184:187], v[228:231]\n"
"v_mfma_f32_16x16x32_bf16 v[208:211], a[196:199], a[188:191], v[208:211]\n"
"v_mfma_f32_16x16x32_bf16 v[212:215], a[200:203], a[184:187], v[228:231]\n"
"v_mfma_f32_16x16x32_bf16 v[212:215], a[204:207], a[188:191], v[212:215]\n"
"v_mfma_f32_16x16x32_bf16 v[216:219], a[208:211], a[184:187], v[228:231]\n"
"v_mfma_f32_16x16x32_bf16 v[216:219], a[212:215], a[188:191], v[216:219]\n"
"buffer_load_dwordx4 v227, s[28:31], s48 offen offset:3072 lds\n"
"v_mfma_f32_16x16x32_bf16 v[220:223], a[216:219], a[184:187], v[228:231]\n"
"s_xor_b32 s50, s50, s74\n"
"v_mfma_f32_16x16x32_bf16 v[220:223], a[220:223], a[188:191], v[220:223]\n"
"v_mfma_f32_16x16x32_bf16 v[0:3], a[224:227], a[136:139], v[228:231]\n"
"v_mfma_f32_16x16x32_bf16 v[0:3], a[228:231], a[140:143], v[0:3]\n"
"v_mfma_f32_16x16x32_bf16 v[4:7], a[232:235], a[136:139], v[228:231]\n"
"s_waitcnt vmcnt(8) lgkmcnt(0)\n"
"v_mfma_f32_16x16x32_bf16 v[4:7], a[236:239], a[140:143], v[4:7]\n"
"s_barrier\n"
"v_mfma_f32_16x16x32_bf16 v[8:11], a[240:243], a[136:139], v[228:231]\n"
"s_mov_b32 m0, s51\n"
"ds_read_b64_tr_b16 a[192:193], v244 offset:0\n"
"v_mfma_f32_16x16x32_bf16 v[8:11], a[244:247], a[140:143], v[8:11]\n"
"buffer_load_dwordx4 v248, s[24:27], s49 offen lds\n"
"v_mfma_f32_16x16x32_bf16 v[12:15], a[248:251], a[136:139], v[228:231]\n"
"ds_read_b64_tr_b16 a[194:195], v244 offset:512\n"
"v_mfma_f32_16x16x32_bf16 v[12:15], a[252:255], a[140:143], v[12:15]\n"
"ds_read_b128 a[136:139], v246 offset:0\n"
"v_mfma_f32_16x16x32_bf16 v[28:31], a[248:251], a[144:147], v[228:231]\n"
"ds_read_b64_tr_b16 a[196:197], v244 offset:8192\n"
"v_mfma_f32_16x16x32_bf16 v[28:31], a[252:255], a[148:151], v[28:31]\n"
"ds_read_b64_tr_b16 a[198:199], v244 offset:8704\n"
"v_mfma_f32_16x16x32_bf16 v[24:27], a[240:243], a[144:147], v[228:231]\n"
"ds_read_b128 a[140:143], v247 offset:0\n"
"v_mfma_f32_16x16x32_bf16 v[24:27], a[244:247], a[148:151], v[24:27]\n"
"buffer_load_dwordx4 v249, s[24:27], s49 offen offset:1024 lds\n"
"v_mfma_f32_16x16x32_bf16 v[20:23], a[232:235], a[144:147], v[228:231]\n"
"ds_read_b64_tr_b16 a[200:201], v245 offset:0\n"
"v_mfma_f32_16x16x32_bf16 v[20:23], a[236:239], a[148:151], v[20:23]\n"
"ds_read_b64_tr_b16 a[202:203], v245 offset:512\n"
"v_mfma_f32_16x16x32_bf16 v[16:19], a[224:227], a[144:147], v[228:231]\n"
"ds_read_b64_tr_b16 a[204:205], v245 offset:8192\n"
"v_mfma_f32_16x16x32_bf16 v[16:19], a[228:231], a[148:151], v[16:19]\n"
"ds_read_b64_tr_b16 a[206:207], v245 offset:8704\n"
"v_mfma_f32_16x16x32_bf16 v[32:35], a[224:227], a[152:155], v[228:231]\n"
"ds_read_b64_tr_b16 a[208:209], v244 offset:1024\n"
"v_mfma_f32_16x16x32_bf16 v[32:35], a[228:231], a[156:159], v[32:35]\n"
"buffer_load_dwordx4 v254, s[24:27], s49 offen offset:2048 lds\n"
"v_mfma_f32_16x16x32_bf16 v[36:39], a[232:235], a[152:155], v[228:231]\n"
"ds_read_b64_tr_b16 a[210:211], v244 offset:1536\n"
"v_mfma_f32_16x16x32_bf16 v[36:39], a[236:239], a[156:159], v[36:39]\n"
"ds_read_b64_tr_b16 a[212:213], v244 offset:9216\n"
"v_mfma_f32_16x16x32_bf16 v[40:43], a[240:243], a[152:155], v[228:231]\n"
"ds_read_b64_tr_b16 a[214:215], v244 offset:9728\n"
"v_mfma_f32_16x16x32_bf16 v[40:43], a[244:247], a[156:159], v[40:43]\n"
"ds_read_b64_tr_b16 a[216:217], v245 offset:1024\n"
"v_mfma_f32_16x16x32_bf16 v[44:47], a[248:251], a[152:155], v[228:231]\n"
"ds_read_b64_tr_b16 a[218:219], v245 offset:1536\n"
"v_mfma_f32_16x16x32_bf16 v[44:47], a[252:255], a[156:159], v[44:47]\n"
"buffer_load_dwordx4 v255, s[24:27], s49 offen offset:3072 lds\n"
"v_mfma_f32_16x16x32_bf16 v[60:63], a[248:251], a[160:163], v[228:231]\n"
"ds_read_b64_tr_b16 a[220:221], v245 offset:9216\n"
"v_mfma_f32_16x16x32_bf16 v[60:63], a[252:255], a[164:167], v[60:63]\n"
"ds_read_b64_tr_b16 a[222:223], v245 offset:9728\n"
"v_mfma_f32_16x16x32_bf16 v[56:59], a[240:243], a[160:163], v[228:231]\n"
"ds_read_b128 a[144:147], v246 offset:2048\n"
"v_mfma_f32_16x16x32_bf16 v[56:59], a[244:247], a[164:167], v[56:59]\n"
"s_add_u32 s40, s49, 40960\n"
"ds_read_b128 a[148:151], v247 offset:2048\n"
"v_mfma_f32_16x16x32_bf16 v[52:55], a[232:235], a[160:163], v[228:231]\n"
"s_add_u32 m0, s51, 4096\n"
"ds_read_b128 a[152:155], v246 offset:4096\n"
"v_mfma_f32_16x16x32_bf16 v[52:55], a[236:239], a[164:167], v[52:55]\n"
"buffer_load_dwordx4 v248, s[24:27], s40 offen lds\n"
"v_mfma_f32_16x16x32_bf16 v[48:51], a[224:227], a[160:163], v[228:231]\n"
"ds_read_b128 a[156:159], v247 offset:4096\n"
"v_mfma_f32_16x16x32_bf16 v[48:51], a[228:231], a[164:167], v[48:51]\n"
"ds_read_b128 a[160:163], v252 offset:0\n"
"v_mfma_f32_16x16x32_bf16 v[64:67], a[224:227], a[168:171], v[228:231]\n"
"ds_read_b128 a[164:167], v253 offset:0\n"
"v_mfma_f32_16x16x32_bf16 v[64:67], a[228:231], a[172:175], v[64:67]\n"
"v_mfma_f32_16x16x32_bf16 v[68:71], a[232:235], a[168:171], v[228:231]\n"
"v_mfma_f32_16x16x32_bf16 v[68:71], a[236:239], a[172:175], v[68:71]\n"
"buffer_load_dwordx4 v249, s[24:27], s40 offen offset:1024 lds\n"
"v_mfma_f32_16x16x32_bf16 v[72:75], a[240:243], a[168:171], v[228:231]\n"
"v_mfma_f32_16x16x32_bf16 v[72:75], a[244:247], a[172:175], v[72:75]\n"
"v_mfma_f32_16x16x32_bf16 v[76:79], a[248:251], a[168:171], v[228:231]\n"
"ds_read_b128 a[168:171], v250 offset:0\n"
"v_mfma_f32_16x16x32_bf16 v[76:79], a[252:255], a[172:175], v[76:79]\n"
"ds_read_b128 a[172:175], v251 offset:0\n"
"v_mfma_f32_16x16x32_bf16 v[92:95], a[248:251], a[176:179], v[228:231]\n"
"v_mfma_f32_16x16x32_bf16 v[92:95], a[252:255], a[180:183], v[92:95]\n"
"buffer_load_dwordx4 v254, s[24:27], s40 offen offset:2048 lds\n"
"v_mfma_f32_16x16x32_bf16 v[88:91], a[240:243], a[176:179], v[228:231]\n"
"s_add_u32 s49, s49, 128\n"
"v_mfma_f32_16x16x32_bf16 v[88:91], a[244:247], a[180:183], v[88:91]\n"
"s_sub_u32 s41, s49, 1280\n"
"v_mfma_f32_16x16x32_bf16 v[84:87], a[232:235], a[176:179], v[228:231]\n"
"s_min_u32 s49, s49, s41\n"
"v_mfma_f32_16x16x32_bf16 v[84:87], a[236:239], a[180:183], v[84:87]\n"
"s_xor_b32 s51, s51, s74\n"
"v_mfma_f32_16x16x32_bf16 v[80:83], a[224:227], a[176:179], v[228:231]\n"
"ds_read_b128 a[176:179], v250 offset:2048\n"
"v_mfma_f32_16x16x32_bf16 v[80:83], a[228:231], a[180:183], v[80:83]\n"
"ds_read_b128 a[180:183], v251 offset:2048\n"
"v_mfma_f32_16x16x32_bf16 v[96:99], a[224:227], a[184:187], v[228:231]\n"
"v_mfma_f32_16x16x32_bf16 v[96:99], a[228:231], a[188:191], v[96:99]\n"
"v_mfma_f32_16x16x32_bf16 v[100:103], a[232:235], a[184:187], v[228:231]\n"
"v_mfma_f32_16x16x32_bf16 v[100:103], a[236:239], a[188:191], v[100:103]\n"
"v_mfma_f32_16x16x32_bf16 v[104:107], a[240:243], a[184:187], v[228:231]\n"
"v_mfma_f32_16x16x32_bf16 v[104:107], a[244:247], a[188:191], v[104:107]\n"
"v_mfma_f32_16x16x32_bf16 v[108:111], a[248:251], a[184:187], v[228:231]\n"
"s_add_u32 s47, s47, 1\n"
"ds_read_b128 a[184:187], v250 offset:4096\n"
"v_mfma_f32_16x16x32_bf16 v[108:111], a[252:255], a[188:191], v[108:111]\n"
"s_waitcnt lgkmcnt(1)\n"
"ds_read_b128 a[188:191], v251 offset:4096\n"
"LPL_%=:\n"
"v_mfma_f32_16x16x32_bf16 v[112:115], a[192:195], a[136:139], v[112:115]\n"
"v_mfma_f32_16x16x32_bf16 v[112:115], a[196:199], a[140:143], v[112:115]\n"
"v_mfma_f32_16x16x32_bf16 v[116:119], a[200:203], a[136:139], v[116:119]\n"
"s_waitcnt vmcnt(11)\n"
"v_mfma_f32_16x16x32_bf16 v[116:119], a[204:207], a[140:143], v[116:119]\n"
"s_barrier\n"
"v_mfma_f32_16x16x32_bf16 v[120:123], a[208:211], a[136:139], v[120:123]\n"
"s_add_u32 s40, s48, 256\n"
"s_xor_b32 m0, s50, 0xc000\n"
"ds_read_b64_tr_b16 a[224:225], v244 offset:16384\n"
"v_mfma_f32_16x16x32_bf16 v[120:123], a[212:215], a[140:143], v[120:123]\n"
"buffer_load_dwordx4 v224, s[28:31], s40 offen lds\n"
"v_mfma_f32_16x16x32_bf16 v[124:127], a[216:219], a[136:139], v[124:127]\n"
"ds_read_b64_tr_b16 a[226:227], v244 offset:16896\n"
"v_mfma_f32_16x16x32_bf16 v[124:127], a[220:223], a[140:143], v[124:127]\n"
"ds_read_b64_tr_b16 a[228:229], v244 offset:24576\n"
"v_mfma_f32_16x16x32_bf16 v[140:143], a[216:219], a[144:147], v[140:143]\n"
"ds_read_b64_tr_b16 a[230:231], v244 offset:25088\n"
"v_mfma_f32_16x16x32_bf16 v[140:143], a[220:223], a[148:151], v[140:143]\n"
"ds_read_b64_tr_b16 a[232:233], v245 offset:16384\n"
"v_mfma_f32_16x16x32_bf16 v[136:139], a[208:211], a[144:147], v[136:139]\n"
"ds_read_b64_tr_b16 a[234:235], v245 offset:16896\n"
"v_mfma_f32_16x16x32_bf16 v[136:139], a[212:215], a[148:151], v[136:139]\n"
"buffer_load_dwordx4 v225, s[28:31], s40 offen offset:1024 lds\n"
"v_mfma_f32_16x16x32_bf16 v[132:135], a[200:203], a[144:147], v[132:135]\n"
"ds_read_b64_tr_b16 a[236:237], v245 offset:24576\n"
"v_mfma_f32_16x16x32_bf16 v[132:135], a[204:207], a[148:151], v[132:135]\n"
"ds_read_b64_tr_b16 a[238:239], v245 offset:25088\n"
"v_mfma_f32_16x16x32_bf16 v[128:131], a[192:195], a[144:147], v[128:131]\n"
"ds_read_b64_tr_b16 a[240:241], v244 offset:17408\n"
"v_mfma_f32_16x16x32_bf16 v[128:131], a[196:199], a[148:151], v[128:131]\n"
"ds_read_b64_tr_b16 a[242:243], v244 offset:17920\n"
"v_mfma_f32_16x16x32_bf16 v[144:147], a[192:195], a[152:155], v[144:147]\n"
"ds_read_b64_tr_b16 a[244:245], v244 offset:25600\n"
"v_mfma_f32_16x16x32_bf16 v[144:147], a[196:199], a[156:159], v[144:147]\n"
"buffer_load_dwordx4 v226, s[28:31], s40 offen offset:2048 lds\n"
"v_mfma_f32_16x16x32_bf16 v[148:151], a[200:203], a[152:155], v[148:151]\n"
"ds_read_b64_tr_b16 a[246:247], v244 offset:26112\n"
"v_mfma_f32_16x16x32_bf16 v[148:151], a[204:207], a[156:159], v[148:151]\n"
"ds_read_b64_tr_b16 a[248:249], v245 offset:17408\n"
"v_mfma_f32_16x16x32_bf16 v[152:155], a[208:211], a[152:155], v[152:155]\n"
"ds_read_b64_tr_b16 a[250:251], v245 offset:17920\n"
"v_mfma_f32_16x16x32_bf16 v[152:155], a[212:215], a[156:159], v[152:155]\n"
"ds_read_b64_tr_b16 a[252:253], v245 offset:25600\n"
"v_mfma_f32_16x16x32_bf16 v[156:159], a[216:219], a[152:155], v[156:159]\n"
"ds_read_b64_tr_b16 a[254:255], v245 offset:26112\n"
"v_mfma_f32_16x16x32_bf16 v[156:159], a[220:223], a[156:159], v[156:159]\n"
"buffer_load_dwordx4 v227, s[28:31], s40 offen offset:3072 lds\n"
"v_mfma_f32_16x16x32_bf16 v[172:175], a[216:219], a[160:163], v[172:175]\n"
"s_cmp_eq_u32 s47, 8\n"
"v_mfma_f32_16x16x32_bf16 v[172:175], a[220:223], a[164:167], v[172:175]\n"
"s_cselect_b32 s41, s52, s58\n"
"v_mfma_f32_16x16x32_bf16 v[168:171], a[208:211], a[160:163], v[168:171]\n"
"s_add_u32 s48, s48, s41\n"
"v_mfma_f32_16x16x32_bf16 v[168:171], a[212:215], a[164:167], v[168:171]\n"
"s_sub_u32 s41, s48, 5242880\n"
"v_mfma_f32_16x16x32_bf16 v[164:167], a[200:203], a[160:163], v[164:167]\n"
"s_min_u32 s48, s48, s41\n"
"v_mfma_f32_16x16x32_bf16 v[164:167], a[204:207], a[164:167], v[164:167]\n"
"v_xor_b32 v244, s74, v244\n"
"v_xor_b32 v246, s74, v246\n"
"v_mfma_f32_16x16x32_bf16 v[160:163], a[192:195], a[160:163], v[160:163]\n"
"s_mov_b32 m0, s50\n"
"v_xor_b32 v245, s74, v245\n"
"v_xor_b32 v247, s74, v247\n"
"v_mfma_f32_16x16x32_bf16 v[160:163], a[196:199], a[164:167], v[160:163]\n"
"buffer_load_dwordx4 v224, s[28:31], s48 offen lds\n"
"v_mfma_f32_16x16x32_bf16 v[176:179], a[192:195], a[168:171], v[176:179]\n"
"v_xor_b32 v252, s74, v252\n"
"v_xor_b32 v253, s74, v253\n"
"v_mfma_f32_16x16x32_bf16 v[176:179], a[196:199], a[172:175], v[176:179]\n"
"v_xor_b32 v250, s74, v250\n"
"v_xor_b32 v251, s74, v251\n"
"v_mfma_f32_16x16x32_bf16 v[180:183], a[200:203], a[168:171], v[180:183]\n"
"v_mfma_f32_16x16x32_bf16 v[180:183], a[204:207], a[172:175], v[180:183]\n"
"buffer_load_dwordx4 v225, s[28:31], s48 offen offset:1024 lds\n"
"v_mfma_f32_16x16x32_bf16 v[184:187], a[208:211], a[168:171], v[184:187]\n"
"v_mfma_f32_16x16x32_bf16 v[184:187], a[212:215], a[172:175], v[184:187]\n"
"v_mfma_f32_16x16x32_bf16 v[188:191], a[216:219], a[168:171], v[188:191]\n"
"v_mfma_f32_16x16x32_bf16 v[188:191], a[220:223], a[172:175], v[188:191]\n"
"v_mfma_f32_16x16x32_bf16 v[204:207], a[216:219], a[176:179], v[204:207]\n"
"v_mfma_f32_16x16x32_bf16 v[204:207], a[220:223], a[180:183], v[204:207]\n"
"buffer_load_dwordx4 v226, s[28:31], s48 offen offset:2048 lds\n"
"v_mfma_f32_16x16x32_bf16 v[200:203], a[208:211], a[176:179], v[200:203]\n"
"v_mfma_f32_16x16x32_bf16 v[200:203], a[212:215], a[180:183], v[200:203]\n"
"v_mfma_f32_16x16x32_bf16 v[196:199], a[200:203], a[176:179], v[196:199]\n"
"v_mfma_f32_16x16x32_bf16 v[196:199], a[204:207], a[180:183], v[196:199]\n"
"v_mfma_f32_16x16x32_bf16 v[192:195], a[192:195], a[176:179], v[192:195]\n"
"v_mfma_f32_16x16x32_bf16 v[192:195], a[196:199], a[180:183], v[192:195]\n"
"s_waitcnt lgkmcnt(0)\n"
"buffer_load_dwordx4 v227, s[28:31], s48 offen offset:3072 lds\n"
"v_mfma_f32_16x16x32_bf16 v[208:211], a[192:195], a[184:187], v[208:211]\n"
"s_xor_b32 s50, s50, s74\n"
"v_mfma_f32_16x16x32_bf16 v[208:211], a[196:199], a[188:191], v[208:211]\n"
"v_mfma_f32_16x16x32_bf16 v[212:215], a[200:203], a[184:187], v[212:215]\n"
"v_mfma_f32_16x16x32_bf16 v[212:215], a[204:207], a[188:191], v[212:215]\n"
"v_mfma_f32_16x16x32_bf16 v[216:219], a[208:211], a[184:187], v[216:219]\n"
"v_mfma_f32_16x16x32_bf16 v[216:219], a[212:215], a[188:191], v[216:219]\n"
"v_mfma_f32_16x16x32_bf16 v[220:223], a[216:219], a[184:187], v[220:223]\n"
"v_mfma_f32_16x16x32_bf16 v[220:223], a[220:223], a[188:191], v[220:223]\n"
"v_mfma_f32_16x16x32_bf16 v[0:3], a[224:227], a[136:139], v[0:3]\n"
"v_mfma_f32_16x16x32_bf16 v[0:3], a[228:231], a[140:143], v[0:3]\n"
"v_mfma_f32_16x16x32_bf16 v[4:7], a[232:235], a[136:139], v[4:7]\n"
"s_waitcnt vmcnt(8)\n"
"v_mfma_f32_16x16x32_bf16 v[4:7], a[236:239], a[140:143], v[4:7]\n"
"s_barrier\n"
"v_mfma_f32_16x16x32_bf16 v[8:11], a[240:243], a[136:139], v[8:11]\n"
"s_mov_b32 m0, s51\n"
"ds_read_b64_tr_b16 a[192:193], v244 offset:0\n"
"v_mfma_f32_16x16x32_bf16 v[8:11], a[244:247], a[140:143], v[8:11]\n"
"buffer_load_dwordx4 v248, s[24:27], s49 offen lds\n"
"v_mfma_f32_16x16x32_bf16 v[12:15], a[248:251], a[136:139], v[12:15]\n"
"ds_read_b64_tr_b16 a[194:195], v244 offset:512\n"
"v_mfma_f32_16x16x32_bf16 v[12:15], a[252:255], a[140:143], v[12:15]\n"
"ds_read_b128 a[136:139], v246 offset:0\n"
"v_mfma_f32_16x16x32_bf16 v[28:31], a[248:251], a[144:147], v[28:31]\n"
"ds_read_b64_tr_b16 a[196:197], v244 offset:8192\n"
"v_mfma_f32_16x16x32_bf16 v[28:31], a[252:255], a[148:151], v[28:31]\n"
"ds_read_b64_tr_b16 a[198:199], v244 offset:8704\n"
"v_mfma_f32_16x16x32_bf16 v[24:27], a[240:243], a[144:147], v[24:27]\n"
"ds_read_b128 a[140:143], v247 offset:0\n"
"v_mfma_f32_16x16x32_bf16 v[24:27], a[244:247], a[148:151], v[24:27]\n"
"buffer_load_dwordx4 v249, s[24:27], s49 offen offset:1024 lds\n"
"v_mfma_f32_16x16x32_bf16 v[20:23], a[232:235], a[144:147], v[20:23]\n"
"ds_read_b64_tr_b16 a[200:201], v245 offset:0\n"
"v_mfma_f32_16x16x32_bf16 v[20:23], a[236:239], a[148:151], v[20:23]\n"
"ds_read_b64_tr_b16 a[202:203], v245 offset:512\n"
"v_mfma_f32_16x16x32_bf16 v[16:19], a[224:227], a[144:147], v[16:19]\n"
"ds_read_b64_tr_b16 a[204:205], v245 offset:8192\n"
"v_mfma_f32_16x16x32_bf16 v[16:19], a[228:231], a[148:151], v[16:19]\n"
"ds_read_b64_tr_b16 a[206:207], v245 offset:8704\n"
"v_mfma_f32_16x16x32_bf16 v[32:35], a[224:227], a[152:155], v[32:35]\n"
"ds_read_b64_tr_b16 a[208:209], v244 offset:1024\n"
"v_mfma_f32_16x16x32_bf16 v[32:35], a[228:231], a[156:159], v[32:35]\n"
"buffer_load_dwordx4 v254, s[24:27], s49 offen offset:2048 lds\n"
"v_mfma_f32_16x16x32_bf16 v[36:39], a[232:235], a[152:155], v[36:39]\n"
"ds_read_b64_tr_b16 a[210:211], v244 offset:1536\n"
"v_mfma_f32_16x16x32_bf16 v[36:39], a[236:239], a[156:159], v[36:39]\n"
"ds_read_b64_tr_b16 a[212:213], v244 offset:9216\n"
"v_mfma_f32_16x16x32_bf16 v[40:43], a[240:243], a[152:155], v[40:43]\n"
"ds_read_b64_tr_b16 a[214:215], v244 offset:9728\n"
"v_mfma_f32_16x16x32_bf16 v[40:43], a[244:247], a[156:159], v[40:43]\n"
"ds_read_b64_tr_b16 a[216:217], v245 offset:1024\n"
"v_mfma_f32_16x16x32_bf16 v[44:47], a[248:251], a[152:155], v[44:47]\n"
"ds_read_b64_tr_b16 a[218:219], v245 offset:1536\n"
"v_mfma_f32_16x16x32_bf16 v[44:47], a[252:255], a[156:159], v[44:47]\n"
"buffer_load_dwordx4 v255, s[24:27], s49 offen offset:3072 lds\n"
"v_mfma_f32_16x16x32_bf16 v[60:63], a[248:251], a[160:163], v[60:63]\n"
"ds_read_b64_tr_b16 a[220:221], v245 offset:9216\n"
"v_mfma_f32_16x16x32_bf16 v[60:63], a[252:255], a[164:167], v[60:63]\n"
"ds_read_b64_tr_b16 a[222:223], v245 offset:9728\n"
"v_mfma_f32_16x16x32_bf16 v[56:59], a[240:243], a[160:163], v[56:59]\n"
"ds_read_b128 a[144:147], v246 offset:2048\n"
"v_mfma_f32_16x16x32_bf16 v[56:59], a[244:247], a[164:167], v[56:59]\n"
"s_add_u32 s40, s49, 40960\n"
"ds_read_b128 a[148:151], v247 offset:2048\n"
"v_mfma_f32_16x16x32_bf16 v[52:55], a[232:235], a[160:163], v[52:55]\n"
"s_add_u32 m0, s51, 4096\n"
"ds_read_b128 a[152:155], v246 offset:4096\n"
"v_mfma_f32_16x16x32_bf16 v[52:55], a[236:239], a[164:167], v[52:55]\n"
"buffer_load_dwordx4 v248, s[24:27], s40 offen lds\n"
"v_mfma_f32_16x16x32_bf16 v[48:51], a[224:227], a[160:163], v[48:51]\n"
"ds_read_b128 a[156:159], v247 offset:4096\n"
"v_mfma_f32_16x16x32_bf16 v[48:51], a[228:231], a[164:167], v[48:51]\n"
"ds_read_b128 a[160:163], v252 offset:0\n"
"v_mfma_f32_16x16x32_bf16 v[64:67], a[224:227], a[168:171], v[64:67]\n"
"ds_read_b128 a[164:167], v253 offset:0\n"
"v_mfma_f32_16x16x32_bf16 v[64:67], a[228:231], a[172:175], v[64:67]\n"
"v_mfma_f32_16x16x32_bf16 v[68:71], a[232:235], a[168:171], v[68:71]\n"
"v_mfma_f32_16x16x32_bf16 v[68:71], a[236:239], a[172:175], v[68:71]\n"
"buffer_load_dwordx4 v249, s[24:27], s40 offen offset:1024 lds\n"
"v_mfma_f32_16x16x32_bf16 v[72:75], a[240:243], a[168:171], v[72:75]\n"
"v_mfma_f32_16x16x32_bf16 v[72:75], a[244:247], a[172:175], v[72:75]\n"
"v_mfma_f32_16x16x32_bf16 v[76:79], a[248:251], a[168:171], v[76:79]\n"
"ds_read_b128 a[168:171], v250 offset:0\n"
"v_mfma_f32_16x16x32_bf16 v[76:79], a[252:255], a[172:175], v[76:79]\n"
"ds_read_b128 a[172:175], v251 offset:0\n"
"v_mfma_f32_16x16x32_bf16 v[92:95], a[248:251], a[176:179], v[92:95]\n"
"v_mfma_f32_16x16x32_bf16 v[92:95], a[252:255], a[180:183], v[92:95]\n"
"buffer_load_dwordx4 v254, s[24:27], s40 offen offset:2048 lds\n"
"v_mfma_f32_16x16x32_bf16 v[88:91], a[240:243], a[176:179], v[88:91]\n"
"s_add_u32 s49, s49, 128\n"
"v_mfma_f32_16x16x32_bf16 v[88:91], a[244:247], a[180:183], v[88:91]\n"
"s_sub_u32 s41, s49, 1280\n"
"v_mfma_f32_16x16x32_bf16 v[84:87], a[232:235], a[176:179], v[84:87]\n"
"s_min_u32 s49, s49, s41\n"
"v_mfma_f32_16x16x32_bf16 v[84:87], a[236:239], a[180:183], v[84:87]\n"
"s_xor_b32 s51, s51, s74\n"
"v_mfma_f32_16x16x32_bf16 v[80:83], a[224:227], a[176:179], v[80:83]\n"
"ds_read_b128 a[176:179], v250 offset:2048\n"
"v_mfma_f32_16x16x32_bf16 v[80:83], a[228:231], a[180:183], v[80:83]\n"
"ds_read_b128 a[180:183], v251 offset:2048\n"
"v_mfma_f32_16x16x32_bf16 v[96:99], a[224:227], a[184:187], v[96:99]\n"
"v_mfma_f32_16x16x32_bf16 v[96:99], a[228:231], a[188:191], v[96:99]\n"
"v_mfma_f32_16x16x32_bf16 v[100:103], a[232:235], a[184:187], v[100:103]\n"
"v_mfma_f32_16x16x32_bf16 v[100:103], a[236:239], a[188:191], v[100:103]\n"
"v_mfma_f32_16x16x32_bf16 v[104:107], a[240:243], a[184:187], v[104:107]\n"
"v_mfma_f32_16x16x32_bf16 v[104:107], a[244:247], a[188:191], v[104:107]\n"
"v_mfma_f32_16x16x32_bf16 v[108:111], a[248:251], a[184:187], v[108:111]\n"
"s_add_u32 s47, s47, 1\n"
"ds_read_b128 a[184:187], v250 offset:4096\n"
"v_mfma_f32_16x16x32_bf16 v[108:111], a[252:255], a[188:191], v[108:111]\n"
"s_waitcnt lgkmcnt(1)\n"
"s_cmp_lg_u32 s47, s62\n"
"ds_read_b128 a[188:191], v251 offset:4096\n"
"s_cbranch_scc1 LPL_%=\n"
"LPX_%=:\n"
"s_cmp_eq_u32 s47, 18\n"
"s_cbranch_scc1 LPT2_%=\n"
"v_mfma_f32_16x16x32_bf16 v[112:115], a[192:195], a[136:139], v[112:115]\n"
"s_add_u32 s78, s96, 8192\n"
"v_mov_b32 v241, 1\n"
"v_mfma_f32_16x16x32_bf16 v[112:115], a[196:199], a[140:143], v[112:115]\n"
"s_mov_b64 exec, 1\n"
"buffer_store_dword v241, off, s[32:35], s96 sc0 sc1\n"
"buffer_store_dword v241, off, s[32:35], s78 sc0 sc1\n"
"s_mov_b64 exec, -1\n"
"v_mfma_f32_16x16x32_bf16 v[116:119], a[200:203], a[136:139], v[116:119]\n"
"s_waitcnt vmcnt(13)\n"
"v_mfma_f32_16x16x32_bf16 v[116:119], a[204:207], a[140:143], v[116:119]\n"
"s_barrier\n"
"v_mfma_f32_16x16x32_bf16 v[120:123], a[208:211], a[136:139], v[120:123]\n"
"s_add_u32 s40, s48, 256\n"
"s_xor_b32 m0, s50, 0xc000\n"
"ds_read_b64_tr_b16 a[254:255], v245 offset:26112\n"
"v_mfma_f32_16x16x32_bf16 v[120:123], a[212:215], a[140:143], v[120:123]\n"
"buffer_load_dwordx4 v224, s[28:31], s40 offen lds\n"
"v_mfma_f32_16x16x32_bf16 v[124:127], a[216:219], a[136:139], v[124:127]\n"
"v_mfma_f32_16x16x32_bf16 v[124:127], a[220:223], a[140:143], v[124:127]\n"
"s_memtime s[78:79]\n"
"v_mfma_f32_16x16x32_bf16 v[188:191], a[216:219], a[168:171], v[188:191]\n"
"ds_read_b64_tr_b16 a[232:233], v245 offset:16384\n"
"ds_read_b64_tr_b16 a[238:239], v245 offset:25088\n"
"v_mfma_f32_16x16x32_bf16 v[188:191], a[220:223], a[172:175], v[188:191]\n"
"s_memrealtime s[80:81]\n"
"ds_read_b64_tr_b16 a[226:227], v244 offset:16896\n"
"v_mfma_f32_16x16x32_bf16 v[184:187], a[208:211], a[168:171], v[184:187]\n"
"ds_read_b64_tr_b16 a[252:253], v245 offset:25600\n"
"v_mfma_f32_16x16x32_bf16 v[184:187], a[212:215], a[172:175], v[184:187]\n"
"buffer_load_dwordx4 v225, s[28:31], s40 offen offset:1024 lds\n"
"v_mfma_f32_16x16x32_bf16 v[180:183], a[200:203], a[168:171], v[180:183]\n"
"v_bfe_u32 v241, v242, 4, 6\n"
"v_mfma_f32_16x16x32_bf16 v[180:183], a[204:207], a[172:175], v[180:183]\n"
"v_mfma_f32_16x16x32_bf16 v[176:179], a[192:195], a[168:171], v[176:179]\n"
"ds_read_b64_tr_b16 a[236:237], v245 offset:24576\n"
"v_mfma_f32_16x16x32_bf16 v[176:179], a[196:199], a[172:175], v[176:179]\n"
"ds_read_b64_tr_b16 a[234:235], v245 offset:16896\n"
"v_mul_u32_u24 v241, 24, v241\n"
"v_mfma_f32_16x16x32_bf16 v[192:195], a[192:195], a[176:179], v[192:195]\n"
"ds_read_b64_tr_b16 a[230:231], v244 offset:25088\n"
"ds_read_b64_tr_b16 a[246:247], v244 offset:26112\n"
"v_mfma_f32_16x16x32_bf16 v[192:195], a[196:199], a[180:183], v[192:195]\n"
"buffer_load_dwordx4 v226, s[28:31], s40 offen offset:2048 lds\n"
"v_mfma_f32_16x16x32_bf16 v[196:199], a[200:203], a[176:179], v[196:199]\n"
"v_mfma_f32_16x16x32_bf16 v[196:199], a[204:207], a[180:183], v[196:199]\n"
"v_mfma_f32_16x16x32_bf16 v[200:203], a[208:211], a[176:179], v[200:203]\n"
"ds_read_b64_tr_b16 a[248:249], v245 offset:17408\n"
"v_mfma_f32_16x16x32_bf16 v[200:203], a[212:215], a[180:183], v[200:203]\n"
"ds_read_b64_tr_b16 a[244:245], v244 offset:25600\n"
"v_mfma_f32_16x16x32_bf16 v[204:207], a[216:219], a[176:179], v[204:207]\n"
"ds_read_b64_tr_b16 a[224:225], v244 offset:16384\n"
"ds_read_b64_tr_b16 a[228:229], v244 offset:24576\n"
"v_mfma_f32_16x16x32_bf16 v[204:207], a[220:223], a[180:183], v[204:207]\n"
"s_waitcnt lgkmcnt(6)\n"
"buffer_load_dwordx4 v227, s[28:31], s40 offen offset:3072 lds\n"
"v_mfma_f32_16x16x32_bf16 v[220:223], a[216:219], a[184:187], v[220:223]\n"
"s_cmp_eq_u32 s47, 8\n"
"v_mfma_f32_16x16x32_bf16 v[220:223], a[220:223], a[188:191], v[220:223]\n"
"s_cselect_b32 s41, s52, s58\n"
"v_mfma_f32_16x16x32_bf16 v[216:219], a[208:211], a[184:187], v[216:219]\n"
"s_add_u32 s48, s48, s41\n"
"ds_read_b64_tr_b16 a[242:243], v244 offset:17920\n"
"ds_read_b64_tr_b16 a[250:251], v245 offset:17920\n"
"v_mfma_f32_16x16x32_bf16 v[216:219], a[212:215], a[188:191], v[216:219]\n"
"s_sub_u32 s41, s48, 5242880\n"
"v_pk_fma_f32 v[176:177], v[176:177], s[70:71], s[70:71] op_sel:[0,0,1] op_sel_hi:[1,0,1]\nv_pk_fma_f32 v[178:179], v[178:179], s[70:71], s[70:71] op_sel:[0,0,1] op_sel_hi:[1,0,1]\nv_pk_fma_f32 v[180:181], v[180:181], s[70:71], s[70:71] op_sel:[0,0,1] op_sel_hi:[1,0,1]\nv_pk_fma_f32 v[182:183], v[182:183], s[70:71], s[70:71] op_sel:[0,0,1] op_sel_hi:[1,0,1]\nv_pk_fma_f32 v[184:185], v[184:185], s[70:71], s[70:71] op_sel:[0,0,1] op_sel_hi:[1,0,1]\nv_pk_fma_f32 v[186:187], v[186:187], s[70:71], s[70:71] op_sel:[0,0,1] op_sel_hi:[1,0,1]\nv_pk_fma_f32 v[188:189], v[188:189], s[70:71], s[70:71] op_sel:[0,0,1] op_sel_hi:[1,0,1]\nv_pk_fma_f32 v[190:191], v[190:191], s[70:71], s[70:71] op_sel:[0,0,1] op_sel_hi:[1,0,1]\nv_pk_fma_f32 v[192:193], v[192:193], s[70:71], s[70:71] op_sel:[0,0,1] op_sel_hi:[1,0,1]\nv_pk_fma_f32 v[194:195], v[194:195], s[70:71], s[70:71] op_sel:[0,0,1] op_sel_hi:[1,0,1]\nv_pk_fma_f32 v[196:197], v[196:197], s[70:71], s[70:71] op_sel:[0,0,1] op_sel_hi:[1,0,1]\nv_pk_fma_f32 v[198:199], v[198:199], s[70:71], s[70:71] op_sel:[0,0,1] op_sel_hi:[1,0,1]\nv_pk_fma_f32 v[200:201], v[200:201], s[70:71], s[70:71] op_sel:[0,0,1] op_sel_hi:[1,0,1]\nv_pk_fma_f32 v[202:203], v[202:203], s[70:71], s[70:71] op_sel:[0,0,1] op_sel_hi:[1,0,1]\nv_pk_fma_f32 v[204:205], v[204:205], s[70:71], s[70:71] op_sel:[0,0,1] op_sel_hi:[1,0,1]\nv_pk_fma_f32 v[206:207], v[206:207], s[70:71], s[70:71] op_sel:[0,0,1] op_sel_hi:[1,0,1]\n"
"v_mfma_f32_16x16x32_bf16 v[212:215], a[200:203], a[184:187], v[212:215]\n"
"s_min_u32 s48, s48, s41\n"
"v_cvt_pk_bf16_f32 v176, v176, v177\n"
"v_cvt_pk_bf16_f32 v177, v178, v179\n"
"v_mfma_f32_16x16x32_bf16 v[212:215], a[204:207], a[188:191], v[212:215]\n"
"v_cvt_pk_bf16_f32 v178, v180, v181\n"
"v_cvt_pk_bf16_f32 v192, v192, v193\n"
"v_mfma_f32_16x16x32_bf16 v[208:211], a[192:195], a[184:187], v[208:211]\n"
"s_mov_b32 m0, s50\n"
"v_cvt_pk_bf16_f32 v179, v182, v183\n"
"v_and_b32 v182, 0xf0, v242\n"
"v_mfma_f32_16x16x32_bf16 v[208:211], a[196:199], a[188:191], v[208:211]\n"
"buffer_load_dwordx4 v224, s[28:31], s48 offen lds\n"
"v_mfma_f32_16x16x32_bf16 v[160:163], a[192:195], a[160:163], v[160:163]\n"
"v_bfe_u32 v180, v242, 9, 1\n"
"v_bfe_u32 v181, v242, 9, 1\n"
"v_mfma_f32_16x16x32_bf16 v[160:163], a[196:199], a[164:167], v[160:163]\n"
"v_lshl_add_u32 v180, v180, 8, v182\n"
"v_bfe_u32 v182, v242, 8, 1\n"
"v_mfma_f32_16x16x32_bf16 v[164:167], a[200:203], a[160:163], v[164:167]\n"
"ds_read_b64_tr_b16 a[240:241], v244 offset:17408\n"
"v_cvt_pk_bf16_f32 v193, v194, v195\n"
"v_mfma_f32_16x16x32_bf16 v[164:167], a[204:207], a[164:167], v[164:167]\n"
"buffer_load_dwordx4 v225, s[28:31], s48 offen offset:1024 lds\n"
"v_mfma_f32_16x16x32_bf16 v[168:171], a[208:211], a[160:163], v[168:171]\n"
"v_lshl_add_u32 v180, v182, 3, v180\n"
"v_xor_b32_e64 v182, s42, 1\n"
"v_mfma_f32_16x16x32_bf16 v[168:171], a[212:215], a[164:167], v[168:171]\n"
"v_cvt_pk_bf16_f32 v184, v184, v185\n"
"v_cvt_pk_bf16_f32 v194, v196, v197\n"
"v_mfma_f32_16x16x32_bf16 v[172:175], a[216:219], a[160:163], v[172:175]\n"
"v_lshl_add_u32 v180, v182, 10, v180\n"
"v_xor_b32 v246, s74, v246\n"
"v_mfma_f32_16x16x32_bf16 v[172:175], a[220:223], a[164:167], v[172:175]\n"
"v_cvt_pk_bf16_f32 v200, v200, v201\n"
"v_xor_b32 v250, s74, v250\n"
"v_mfma_f32_16x16x32_bf16 v[140:143], a[216:219], a[144:147], v[140:143]\n"
"v_cvt_pk_bf16_f32 v185, v186, v187\n"
"v_cvt_pk_bf16_f32 v201, v202, v203\n"
"v_mfma_f32_16x16x32_bf16 v[140:143], a[220:223], a[148:151], v[140:143]\n"
"buffer_load_dwordx4 v226, s[28:31], s48 offen offset:2048 lds\n"
"v_mfma_f32_16x16x32_bf16 v[136:139], a[208:211], a[144:147], v[136:139]\n"
"v_add_u32 v180, 94208, v180\n"
"v_cvt_pk_bf16_f32 v195, v198, v199\n"
"v_mfma_f32_16x16x32_bf16 v[136:139], a[212:215], a[148:151], v[136:139]\n"
"v_pk_fma_f32 v[208:209], v[208:209], s[70:71], s[70:71] op_sel:[0,0,1] op_sel_hi:[1,0,1]\nv_pk_fma_f32 v[210:211], v[210:211], s[70:71], s[70:71] op_sel:[0,0,1] op_sel_hi:[1,0,1]\nv_pk_fma_f32 v[212:213], v[212:213], s[70:71], s[70:71] op_sel:[0,0,1] op_sel_hi:[1,0,1]\nv_pk_fma_f32 v[214:215], v[214:215], s[70:71], s[70:71] op_sel:[0,0,1] op_sel_hi:[1,0,1]\nv_pk_fma_f32 v[216:217], v[216:217], s[70:71], s[70:71] op_sel:[0,0,1] op_sel_hi:[1,0,1]\nv_pk_fma_f32 v[218:219], v[218:219], s[70:71], s[70:71] op_sel:[0,0,1] op_sel_hi:[1,0,1]\nv_pk_fma_f32 v[220:221], v[220:221], s[70:71], s[70:71] op_sel:[0,0,1] op_sel_hi:[1,0,1]\nv_pk_fma_f32 v[222:223], v[222:223], s[70:71], s[70:71] op_sel:[0,0,1] op_sel_hi:[1,0,1]\nv_pk_fma_f32 v[160:161], v[160:161], s[70:71], s[70:71] op_sel:[0,0,1] op_sel_hi:[1,0,1]\nv_pk_fma_f32 v[162:163], v[162:163], s[70:71], s[70:71] op_sel:[0,0,1] op_sel_hi:[1,0,1]\nv_pk_fma_f32 v[164:165], v[164:165], s[70:71], s[70:71] op_sel:[0,0,1] op_sel_hi:[1,0,1]\nv_pk_fma_f32 v[166:167], v[166:167], s[70:71], s[70:71] op_sel:[0,0,1] op_sel_hi:[1,0,1]\nv_pk_fma_f32 v[168:169], v[168:169], s[70:71], s[70:71] op_sel:[0,0,1] op_sel_hi:[1,0,1]\nv_pk_fma_f32 v[170:171], v[170:171], s[70:71], s[70:71] op_sel:[0,0,1] op_sel_hi:[1,0,1]\nv_pk_fma_f32 v[172:173], v[172:173], s[70:71], s[70:71] op_sel:[0,0,1] op_sel_hi:[1,0,1]\nv_pk_fma_f32 v[174:175], v[174:175], s[70:71], s[70:71] op_sel:[0,0,1] op_sel_hi:[1,0,1]\n"
"v_mfma_f32_16x16x32_bf16 v[132:135], a[200:203], a[144:147], v[132:135]\n"
"v_cvt_pk_bf16_f32 v208, v208, v209\n"
"v_xor_b32 v244, s74, v244\n"
"v_mfma_f32_16x16x32_bf16 v[132:135], a[204:207], a[148:151], v[132:135]\n"
"v_cvt_pk_bf16_f32 v168, v168, v169\n"
"v_and_b32 v182, 0xf0, v242\n"
"v_mfma_f32_16x16x32_bf16 v[128:131], a[192:195], a[144:147], v[128:131]\n"
"v_cvt_pk_bf16_f32 v216, v216, v217\n"
"v_cvt_pk_bf16_f32 v217, v218, v219\n"
"v_mfma_f32_16x16x32_bf16 v[128:131], a[196:199], a[148:151], v[128:131]\n"
"buffer_load_dwordx4 v227, s[28:31], s48 offen offset:3072 lds\n"
"v_mfma_f32_16x16x32_bf16 v[144:147], a[192:195], a[152:155], v[144:147]\n"
"s_xor_b32 s50, s50, s74\n"
"v_cvt_pk_bf16_f32 v209, v210, v211\n"
"v_cvt_pk_bf16_f32 v169, v170, v171\n"
"v_mfma_f32_16x16x32_bf16 v[144:147], a[196:199], a[156:159], v[144:147]\n"
"v_cvt_pk_bf16_f32 v202, v204, v205\n"
"v_lshl_add_u32 v181, v181, 8, v182\n"
"v_mfma_f32_16x16x32_bf16 v[148:151], a[200:203], a[152:155], v[148:151]\n"
"v_cvt_pk_bf16_f32 v187, v190, v191\n"
"v_cvt_pk_bf16_f32 v203, v206, v207\n"
"v_mfma_f32_16x16x32_bf16 v[148:151], a[204:207], a[156:159], v[148:151]\n"
"ds_write2st64_b64 v180, v[192:193], v[194:195] offset0:88 offset1:89\n"
"v_pk_fma_f32 v[112:113], v[112:113], s[70:71], s[70:71] op_sel:[0,0,1] op_sel_hi:[1,0,1]\nv_pk_fma_f32 v[114:115], v[114:115], s[70:71], s[70:71] op_sel:[0,0,1] op_sel_hi:[1,0,1]\nv_pk_fma_f32 v[116:117], v[116:117], s[70:71], s[70:71] op_sel:[0,0,1] op_sel_hi:[1,0,1]\nv_pk_fma_f32 v[118:119], v[118:119], s[70:71], s[70:71] op_sel:[0,0,1] op_sel_hi:[1,0,1]\nv_pk_fma_f32 v[120:121], v[120:121], s[70:71], s[70:71] op_sel:[0,0,1] op_sel_hi:[1,0,1]\nv_pk_fma_f32 v[122:123], v[122:123], s[70:71], s[70:71] op_sel:[0,0,1] op_sel_hi:[1,0,1]\nv_pk_fma_f32 v[124:125], v[124:125], s[70:71], s[70:71] op_sel:[0,0,1] op_sel_hi:[1,0,1]\nv_pk_fma_f32 v[126:127], v[126:127], s[70:71], s[70:71] op_sel:[0,0,1] op_sel_hi:[1,0,1]\nv_pk_fma_f32 v[128:129], v[128:129], s[70:71], s[70:71] op_sel:[0,0,1] op_sel_hi:[1,0,1]\nv_pk_fma_f32 v[130:131], v[130:131], s[70:71], s[70:71] op_sel:[0,0,1] op_sel_hi:[1,0,1]\nv_pk_fma_f32 v[132:133], v[132:133], s[70:71], s[70:71] op_sel:[0,0,1] op_sel_hi:[1,0,1]\nv_pk_fma_f32 v[134:135], v[134:135], s[70:71], s[70:71] op_sel:[0,0,1] op_sel_hi:[1,0,1]\nv_pk_fma_f32 v[136:137], v[136:137], s[70:71], s[70:71] op_sel:[0,0,1] op_sel_hi:[1,0,1]\nv_pk_fma_f32 v[138:139], v[138:139], s[70:71], s[70:71] op_sel:[0,0,1] op_sel_hi:[1,0,1]\nv_pk_fma_f32 v[140:141], v[140:141], s[70:71], s[70:71] op_sel:[0,0,1] op_sel_hi:[1,0,1]\nv_pk_fma_f32 v[142:143], v[142:143], s[70:71], s[70:71] op_sel:[0,0,1] op_sel_hi:[1,0,1]\n"
"v_mfma_f32_16x16x32_bf16 v[152:155], a[208:211], a[152:155], v[152:155]\n"
"v_cvt_pk_bf16_f32 v218, v220, v221\n"
"v_bfe_u32 v182, v242, 8, 1\n"
"v_mfma_f32_16x16x32_bf16 v[152:155], a[212:215], a[156:159], v[152:155]\n"
"v_cvt_pk_bf16_f32 v120, v120, v121\n"
"v_cvt_pk_bf16_f32 v128, v128, v129\n"
"v_mfma_f32_16x16x32_bf16 v[156:159], a[216:219], a[152:155], v[156:159]\n"
"v_cvt_pk_bf16_f32 v121, v122, v123\n"
"v_cvt_pk_bf16_f32 v136, v136, v137\n"
"v_mfma_f32_16x16x32_bf16 v[156:159], a[220:223], a[156:159], v[156:159]\n"
"s_waitcnt lgkmcnt(1)\n"
"ds_write2st64_b64 v180, v[176:177], v[178:179] offset0:0 offset1:1\n"
"v_cvt_pk_bf16_f32 v160, v160, v161\n"
"v_cvt_pk_bf16_f32 v122, v124, v125\n"
"v_cvt_pk_bf16_f32 v123, v126, v127\n"
"v_xor_b32 v245, s74, v245\n"
"v_lshl_add_u32 v181, v182, 9, v181\n"
"v_mfma_f32_16x16x32_bf16 v[96:99], a[224:227], a[184:187], v[96:99]\n"
"v_cvt_pk_bf16_f32 v210, v212, v213\n"
"v_xor_b32 v253, s74, v253\n"
"v_mfma_f32_16x16x32_bf16 v[96:99], a[228:231], a[188:191], v[96:99]\n"
"ds_write2st64_b64 v180, v[200:201], v[202:203] offset0:96 offset1:97\n"
"v_cvt_pk_bf16_f32 v186, v188, v189\n"
"v_cvt_pk_bf16_f32 v161, v162, v163\n"
"v_cvt_pk_bf16_f32 v112, v112, v113\n"
"v_cvt_pk_bf16_f32 v113, v114, v115\n"
"v_cvt_pk_bf16_f32 v129, v130, v131\n"
"v_mfma_f32_16x16x32_bf16 v[100:103], a[232:235], a[184:187], v[100:103]\n"
"s_waitcnt vmcnt(10)\n"
"v_cvt_pk_bf16_f32 v211, v214, v215\n"
"v_cvt_pk_bf16_f32 v171, v174, v175\n"
"v_mfma_f32_16x16x32_bf16 v[100:103], a[236:239], a[188:191], v[100:103]\n"
"s_barrier\n"
"ds_write2st64_b64 v180, v[208:209], v[210:211] offset0:120 offset1:121\n"
"v_pk_fma_f32 v[144:145], v[144:145], s[70:71], s[70:71] op_sel:[0,0,1] op_sel_hi:[1,0,1]\nv_pk_fma_f32 v[146:147], v[146:147], s[70:71], s[70:71] op_sel:[0,0,1] op_sel_hi:[1,0,1]\nv_pk_fma_f32 v[148:149], v[148:149], s[70:71], s[70:71] op_sel:[0,0,1] op_sel_hi:[1,0,1]\nv_pk_fma_f32 v[150:151], v[150:151], s[70:71], s[70:71] op_sel:[0,0,1] op_sel_hi:[1,0,1]\nv_pk_fma_f32 v[152:153], v[152:153], s[70:71], s[70:71] op_sel:[0,0,1] op_sel_hi:[1,0,1]\nv_pk_fma_f32 v[154:155], v[154:155], s[70:71], s[70:71] op_sel:[0,0,1] op_sel_hi:[1,0,1]\nv_pk_fma_f32 v[156:157], v[156:157], s[70:71], s[70:71] op_sel:[0,0,1] op_sel_hi:[1,0,1]\nv_pk_fma_f32 v[158:159], v[158:159], s[70:71], s[70:71] op_sel:[0,0,1] op_sel_hi:[1,0,1]\n"
"v_mfma_f32_16x16x32_bf16 v[104:107], a[240:243], a[184:187], v[104:107]\n"
"s_mov_b32 m0, s51\n"
"v_cvt_pk_bf16_f32 v219, v222, v223\n"
"v_cvt_pk_bf16_f32 v137, v138, v139\n"
"v_mfma_f32_16x16x32_bf16 v[104:107], a[244:247], a[188:191], v[104:107]\n"
"buffer_load_dwordx4 v248, s[24:27], s49 offen lds\n"
"v_mfma_f32_16x16x32_bf16 v[108:111], a[248:251], a[184:187], v[108:111]\n"
"v_cvt_pk_bf16_f32 v115, v118, v119\n"
"v_cvt_pk_bf16_f32 v144, v144, v145\n"
"v_mfma_f32_16x16x32_bf16 v[108:111], a[252:255], a[188:191], v[108:111]\n"
"v_cvt_pk_bf16_f32 v131, v134, v135\n"
"v_cvt_pk_bf16_f32 v152, v152, v153\n"
"v_mfma_f32_16x16x32_bf16 v[92:95], a[248:251], a[176:179], v[92:95]\n"
"ds_read_b64_tr_b16 a[212:213], v244 offset:9216\n"
"v_cvt_pk_bf16_f32 v162, v164, v165\n"
"v_mfma_f32_16x16x32_bf16 v[92:95], a[252:255], a[180:183], v[92:95]\n"
"ds_write2st64_b64 v180, v[184:185], v[186:187] offset0:64 offset1:65\n"
"v_permlane16_swap_b32 v160, v162\n"
"v_permlane16_swap_b32 v113, v115\n"
"v_cvt_pk_bf16_f32 v130, v132, v133\n"
"v_cvt_pk_bf16_f32 v153, v154, v155\n"
"v_mfma_f32_16x16x32_bf16 v[88:91], a[240:243], a[176:179], v[88:91]\n"
"v_xor_b32_e64 v182, s42, 0\n"
"v_lshl_add_u32 v181, v182, 10, v181\n"
"v_mfma_f32_16x16x32_bf16 v[88:91], a[244:247], a[180:183], v[88:91]\n"
"buffer_load_dwordx4 v249, s[24:27], s49 offen offset:1024 lds\n"
"v_mfma_f32_16x16x32_bf16 v[84:87], a[232:235], a[176:179], v[84:87]\n"
"v_cvt_pk_bf16_f32 v114, v116, v117\n"
"v_xor_b32 v251, s74, v251\n"
"v_mfma_f32_16x16x32_bf16 v[84:87], a[236:239], a[180:183], v[84:87]\n"
"v_permlane16_swap_b32 v121, v123\n"
"v_mfma_f32_16x16x32_bf16 v[80:83], a[224:227], a[176:179], v[80:83]\n"
"ds_read_b64_tr_b16 a[218:219], v245 offset:1536\n"
"ds_read_b128 a[184:187], v250 offset:4096\n"
"v_mfma_f32_16x16x32_bf16 v[80:83], a[228:231], a[180:183], v[80:83]\n"
"ds_write2st64_b64 v180, v[216:217], v[218:219] offset0:128 offset1:129\n"
"v_permlane16_swap_b32 v169, v171\n"
"v_permlane16_swap_b32 v112, v114\n"
"v_permlane16_swap_b32 v129, v131\n"
"v_cvt_pk_bf16_f32 v139, v142, v143\n"
"v_cvt_pk_bf16_f32 v155, v158, v159\n"
"v_mfma_f32_16x16x32_bf16 v[64:67], a[224:227], a[168:171], v[64:67]\n"
"ds_read_b64_tr_b16 a[214:215], v244 offset:9728\n"
"v_add_u32 v181, 94208, v181\n"
"v_mfma_f32_16x16x32_bf16 v[64:67], a[228:231], a[172:175], v[64:67]\n"
"buffer_load_dwordx4 v254, s[24:27], s49 offen offset:2048 lds\n"
"v_mfma_f32_16x16x32_bf16 v[68:71], a[232:235], a[168:171], v[68:71]\n"
"v_cvt_pk_bf16_f32 v145, v146, v147\n"
"v_cvt_pk_bf16_f32 v147, v150, v151\n"
"v_mfma_f32_16x16x32_bf16 v[68:71], a[236:239], a[172:175], v[68:71]\n"
"v_cvt_pk_bf16_f32 v170, v172, v173\n"
"v_cvt_pk_bf16_f32 v154, v156, v157\n"
"v_pk_fma_f32 v[96:97], v[96:97], s[70:71], s[70:71] op_sel:[0,0,1] op_sel_hi:[1,0,1]\nv_pk_fma_f32 v[98:99], v[98:99], s[70:71], s[70:71] op_sel:[0,0,1] op_sel_hi:[1,0,1]\nv_pk_fma_f32 v[100:101], v[100:101], s[70:71], s[70:71] op_sel:[0,0,1] op_sel_hi:[1,0,1]\nv_pk_fma_f32 v[102:103], v[102:103], s[70:71], s[70:71] op_sel:[0,0,1] op_sel_hi:[1,0,1]\nv_pk_fma_f32 v[104:105], v[104:105], s[70:71], s[70:71] op_sel:[0,0,1] op_sel_hi:[1,0,1]\nv_pk_fma_f32 v[106:107], v[106:107], s[70:71], s[70:71] op_sel:[0,0,1] op_sel_hi:[1,0,1]\nv_pk_fma_f32 v[108:109], v[108:109], s[70:71], s[70:71] op_sel:[0,0,1] op_sel_hi:[1,0,1]\nv_pk_fma_f32 v[110:111], v[110:111], s[70:71], s[70:71] op_sel:[0,0,1] op_sel_hi:[1,0,1]\nv_pk_fma_f32 v[80:81], v[80:81], s[70:71], s[70:71] op_sel:[0,0,1] op_sel_hi:[1,0,1]\nv_pk_fma_f32 v[82:83], v[82:83], s[70:71], s[70:71] op_sel:[0,0,1] op_sel_hi:[1,0,1]\nv_pk_fma_f32 v[84:85], v[84:85], s[70:71], s[70:71] op_sel:[0,0,1] op_sel_hi:[1,0,1]\nv_pk_fma_f32 v[86:87], v[86:87], s[70:71], s[70:71] op_sel:[0,0,1] op_sel_hi:[1,0,1]\nv_pk_fma_f32 v[88:89], v[88:89], s[70:71], s[70:71] op_sel:[0,0,1] op_sel_hi:[1,0,1]\nv_pk_fma_f32 v[90:91], v[90:91], s[70:71], s[70:71] op_sel:[0,0,1] op_sel_hi:[1,0,1]\nv_pk_fma_f32 v[92:93], v[92:93], s[70:71], s[70:71] op_sel:[0,0,1] op_sel_hi:[1,0,1]\nv_pk_fma_f32 v[94:95], v[94:95], s[70:71], s[70:71] op_sel:[0,0,1] op_sel_hi:[1,0,1]\n"
"v_cvt_pk_bf16_f32 v88, v88, v89\n"
"v_mfma_f32_16x16x32_bf16 v[72:75], a[240:243], a[168:171], v[72:75]\n"
"ds_read_b64_tr_b16 a[222:223], v245 offset:9728\n"
"v_cvt_pk_bf16_f32 v96, v96, v97\n"
"v_mfma_f32_16x16x32_bf16 v[72:75], a[244:247], a[172:175], v[72:75]\n"
"ds_read_b64_tr_b16 a[196:197], v244 offset:8192\n"
"v_accvgpr_write_b32 a1, v113\n"
"v_mfma_f32_16x16x32_bf16 v[76:79], a[248:251], a[168:171], v[76:79]\n"
"ds_read_b64_tr_b16 a[216:217], v245 offset:1024\n"
"v_xor_b32 v252, s74, v252\n"
"v_mfma_f32_16x16x32_bf16 v[76:79], a[252:255], a[172:175], v[76:79]\n"
"buffer_load_dwordx4 v255, s[24:27], s49 offen offset:3072 lds\n"
"v_mfma_f32_16x16x32_bf16 v[60:63], a[248:251], a[160:163], v[60:63]\n"
"v_permlane16_swap_b32 v128, v130\n"
"v_mfma_f32_16x16x32_bf16 v[60:63], a[252:255], a[164:167], v[60:63]\n"
"v_permlane16_swap_b32 v137, v139\n"
"v_mfma_f32_16x16x32_bf16 v[56:59], a[240:243], a[160:163], v[56:59]\n"
"ds_read_b64_tr_b16 a[194:195], v244 offset:512\n"
"ds_read_b64_tr_b16 a[210:211], v244 offset:1536\n"
"v_mfma_f32_16x16x32_bf16 v[56:59], a[244:247], a[164:167], v[56:59]\n"
"s_add_u32 s40, s49, 40960\n"
"ds_read_b64_tr_b16 a[208:209], v244 offset:1024\n"
"v_cvt_pk_bf16_f32 v138, v140, v141\n"
"v_mfma_f32_16x16x32_bf16 v[52:55], a[232:235], a[160:163], v[52:55]\n"
"s_add_u32 m0, s51, 4096\n"
"ds_read_b128 a[168:171], v250 offset:0\n"
"v_cvt_pk_bf16_f32 v146, v148, v149\n"
"v_mfma_f32_16x16x32_bf16 v[52:55], a[236:239], a[164:167], v[52:55]\n"
"buffer_load_dwordx4 v248, s[24:27], s40 offen lds\n"
"v_mfma_f32_16x16x32_bf16 v[48:51], a[224:227], a[160:163], v[48:51]\n"
"s_waitcnt lgkmcnt(0)\n"
"v_accvgpr_write_b32 a9, v121\n"
"v_cvt_pk_bf16_f32 v80, v80, v81\n"
"v_mfma_f32_16x16x32_bf16 v[48:51], a[228:231], a[164:167], v[48:51]\n"
"s_barrier\n"
"v_mfma_f32_16x16x32_bf16 v[0:3], a[224:227], a[136:139], v[0:3]\n"
"ds_read_b128 a[28:31], v181 offset:49152\n"
"v_add_u32 v182, s75, v181\n"
"v_mfma_f32_16x16x32_bf16 v[0:3], a[228:231], a[140:143], v[0:3]\n"
"ds_read_b128 a[36:39], v181 offset:61440\n"
"v_accvgpr_write_b32 a3, v115\n"
"v_mfma_f32_16x16x32_bf16 v[4:7], a[232:235], a[136:139], v[4:7]\n"
"ds_read_b128 a[4:7], v181 offset:0\n"
"v_accvgpr_write_b32 a11, v123\n"
"v_mfma_f32_16x16x32_bf16 v[4:7], a[236:239], a[140:143], v[4:7]\n"
"buffer_load_dwordx4 v249, s[24:27], s40 offen offset:1024 lds\n"
"v_mfma_f32_16x16x32_bf16 v[8:11], a[240:243], a[136:139], v[8:11]\n"
"v_accvgpr_write_b32 a2, v114\n"
"v_xor_b32 v247, s74, v247\n"
"v_mfma_f32_16x16x32_bf16 v[8:11], a[244:247], a[140:143], v[8:11]\n"
"v_permlane16_swap_b32 v120, v122\n"
"v_mfma_f32_16x16x32_bf16 v[12:15], a[248:251], a[136:139], v[12:15]\n"
"ds_read_b128 a[44:47], v182 offset:0\n"
"v_cvt_pk_bf16_f32 v97, v98, v99\n"
"v_mfma_f32_16x16x32_bf16 v[12:15], a[252:255], a[140:143], v[12:15]\n"
"ds_read_b128 a[12:15], v181 offset:32768\n"
"v_accvgpr_write_b32 a0, v112\n"
"v_mfma_f32_16x16x32_bf16 v[28:31], a[248:251], a[144:147], v[28:31]\n"
"ds_read_b64_tr_b16 a[200:201], v245 offset:0\n"
"ds_read_b64_tr_b16 a[220:221], v245 offset:9216\n"
"v_mfma_f32_16x16x32_bf16 v[28:31], a[252:255], a[148:151], v[28:31]\n"
"buffer_load_dwordx4 v254, s[24:27], s40 offen offset:2048 lds\n"
"v_accvgpr_write_b32 a10, v122\n"
"v_accvgpr_write_b32 a16, v128\n"
"v_permlane16_swap_b32 v144, v146\n"
"v_permlane16_swap_b32 v145, v147\n"
"v_accvgpr_write_b32 a34, v146\n"
"v_permlane16_swap_b32 v152, v154\n"
"v_cvt_pk_bf16_f32 v98, v100, v101\n"
"v_cvt_pk_bf16_f32 v104, v104, v105\n"
"v_pk_fma_f32 v[64:65], v[64:65], s[70:71], s[70:71] op_sel:[0,0,1] op_sel_hi:[1,0,1]\nv_pk_fma_f32 v[66:67], v[66:67], s[70:71], s[70:71] op_sel:[0,0,1] op_sel_hi:[1,0,1]\nv_pk_fma_f32 v[68:69], v[68:69], s[70:71], s[70:71] op_sel:[0,0,1] op_sel_hi:[1,0,1]\nv_pk_fma_f32 v[70:71], v[70:71], s[70:71], s[70:71] op_sel:[0,0,1] op_sel_hi:[1,0,1]\nv_pk_fma_f32 v[72:73], v[72:73], s[70:71], s[70:71] op_sel:[0,0,1] op_sel_hi:[1,0,1]\nv_pk_fma_f32 v[74:75], v[74:75], s[70:71], s[70:71] op_sel:[0,0,1] op_sel_hi:[1,0,1]\nv_pk_fma_f32 v[76:77], v[76:77], s[70:71], s[70:71] op_sel:[0,0,1] op_sel_hi:[1,0,1]\nv_pk_fma_f32 v[78:79], v[78:79], s[70:71], s[70:71] op_sel:[0,0,1] op_sel_hi:[1,0,1]\nv_pk_fma_f32 v[48:49], v[48:49], s[70:71], s[70:71] op_sel:[0,0,1] op_sel_hi:[1,0,1]\nv_pk_fma_f32 v[50:51], v[50:51], s[70:71], s[70:71] op_sel:[0,0,1] op_sel_hi:[1,0,1]\nv_pk_fma_f32 v[52:53], v[52:53], s[70:71], s[70:71] op_sel:[0,0,1] op_sel_hi:[1,0,1]\nv_pk_fma_f32 v[54:55], v[54:55], s[70:71], s[70:71] op_sel:[0,0,1] op_sel_hi:[1,0,1]\nv_pk_fma_f32 v[56:57], v[56:57], s[70:71], s[70:71] op_sel:[0,0,1] op_sel_hi:[1,0,1]\nv_pk_fma_f32 v[58:59], v[58:59], s[70:71], s[70:71] op_sel:[0,0,1] op_sel_hi:[1,0,1]\nv_pk_fma_f32 v[60:61], v[60:61], s[70:71], s[70:71] op_sel:[0,0,1] op_sel_hi:[1,0,1]\nv_pk_fma_f32 v[62:63], v[62:63], s[70:71], s[70:71] op_sel:[0,0,1] op_sel_hi:[1,0,1]\n"
"v_cvt_pk_bf16_f32 v64, v64, v65\n"
"v_mfma_f32_16x16x32_bf16 v[24:27], a[240:243], a[144:147], v[24:27]\n"
"s_add_u32 s49, s49, 128\n"
"v_permlane16_swap_b32 v136, v138\n"
"v_mfma_f32_16x16x32_bf16 v[24:27], a[244:247], a[148:151], v[24:27]\n"
"s_sub_u32 s41, s49, 1280\n"
"v_permlane16_swap_b32 v153, v155\n"
"v_mfma_f32_16x16x32_bf16 v[20:23], a[232:235], a[144:147], v[20:23]\n"
"s_min_u32 s49, s49, s41\n"
"ds_read_b64_tr_b16 a[204:205], v245 offset:8192\n"
"ds_read_b128 a[172:175], v251 offset:0\n"
"v_mfma_f32_16x16x32_bf16 v[20:23], a[236:239], a[148:151], v[20:23]\n"
"s_xor_b32 s51, s51, s74\n"
"ds_read_b128 a[140:143], v247 offset:0\n"
"v_cvt_pk_bf16_f32 v65, v66, v67\n"
"v_mfma_f32_16x16x32_bf16 v[16:19], a[224:227], a[144:147], v[16:19]\n"
"ds_read_b128 a[136:139], v246 offset:0\n"
"v_accvgpr_write_b32 a41, v153\n"
"v_mfma_f32_16x16x32_bf16 v[16:19], a[228:231], a[148:151], v[16:19]\n"
"ds_read_b64_tr_b16 a[192:193], v244 offset:0\n"
"v_accvgpr_write_b32 a40, v152\n"
"v_mfma_f32_16x16x32_bf16 v[32:35], a[224:227], a[152:155], v[32:35]\n"
"ds_read_b64_tr_b16 a[198:199], v244 offset:8704\n"
"v_accvgpr_write_b32 a24, v136\n"
"v_mfma_f32_16x16x32_bf16 v[32:35], a[228:231], a[156:159], v[32:35]\n"
"ds_read_b64_tr_b16 a[202:203], v245 offset:512\n"
"v_accvgpr_write_b32 a17, v129\n"
"v_mfma_f32_16x16x32_bf16 v[36:39], a[232:235], a[152:155], v[36:39]\n"
"ds_read_b64_tr_b16 a[206:207], v245 offset:8704\n"
"v_cvt_pk_bf16_f32 v89, v90, v91\n"
"v_mfma_f32_16x16x32_bf16 v[36:39], a[236:239], a[156:159], v[36:39]\n"
"ds_read_b128 a[20:23], v181 offset:45056\n"
"v_permlane16_swap_b32 v168, v170\n"
"v_accvgpr_write_b32 a8, v120\n"
"v_accvgpr_write_b32 a18, v130\n"
"v_accvgpr_write_b32 a19, v131\n"
"v_bfe_u32 v100, v242, 9, 1\n"
"v_pk_fma_f32 v[0:1], v[0:1], s[70:71], s[70:71] op_sel:[0,0,1] op_sel_hi:[1,0,1]\nv_pk_fma_f32 v[2:3], v[2:3], s[70:71], s[70:71] op_sel:[0,0,1] op_sel_hi:[1,0,1]\nv_pk_fma_f32 v[4:5], v[4:5], s[70:71], s[70:71] op_sel:[0,0,1] op_sel_hi:[1,0,1]\nv_pk_fma_f32 v[6:7], v[6:7], s[70:71], s[70:71] op_sel:[0,0,1] op_sel_hi:[1,0,1]\nv_pk_fma_f32 v[8:9], v[8:9], s[70:71], s[70:71] op_sel:[0,0,1] op_sel_hi:[1,0,1]\nv_pk_fma_f32 v[10:11], v[10:11], s[70:71], s[70:71] op_sel:[0,0,1] op_sel_hi:[1,0,1]\nv_pk_fma_f32 v[12:13], v[12:13], s[70:71], s[70:71] op_sel:[0,0,1] op_sel_hi:[1,0,1]\nv_pk_fma_f32 v[14:15], v[14:15], s[70:71], s[70:71] op_sel:[0,0,1] op_sel_hi:[1,0,1]\nv_pk_fma_f32 v[16:17], v[16:17], s[70:71], s[70:71] op_sel:[0,0,1] op_sel_hi:[1,0,1]\nv_pk_fma_f32 v[18:19], v[18:19], s[70:71], s[70:71] op_sel:[0,0,1] op_sel_hi:[1,0,1]\nv_pk_fma_f32 v[20:21], v[20:21], s[70:71], s[70:71] op_sel:[0,0,1] op_sel_hi:[1,0,1]\nv_pk_fma_f32 v[22:23], v[22:23], s[70:71], s[70:71] op_sel:[0,0,1] op_sel_hi:[1,0,1]\nv_pk_fma_f32 v[24:25], v[24:25], s[70:71], s[70:71] op_sel:[0,0,1] op_sel_hi:[1,0,1]\nv_pk_fma_f32 v[26:27], v[26:27], s[70:71], s[70:71] op_sel:[0,0,1] op_sel_hi:[1,0,1]\nv_pk_fma_f32 v[28:29], v[28:29], s[70:71], s[70:71] op_sel:[0,0,1] op_sel_hi:[1,0,1]\nv_pk_fma_f32 v[30:31], v[30:31], s[70:71], s[70:71] op_sel:[0,0,1] op_sel_hi:[1,0,1]\n"
"v_cvt_pk_bf16_f32 v16, v16, v17\n"
"v_mfma_f32_16x16x32_bf16 v[40:43], a[240:243], a[152:155], v[40:43]\n"
"v_accvgpr_write_b32 a43, v155\n"
"v_cvt_pk_bf16_f32 v0, v0, v1\n"
"v_mfma_f32_16x16x32_bf16 v[40:43], a[244:247], a[156:159], v[40:43]\n"
"ds_read_b128 a[144:147], v246 offset:2048\n"
"v_accvgpr_write_b32 a35, v147\n"
"v_mfma_f32_16x16x32_bf16 v[44:47], a[248:251], a[152:155], v[44:47]\n"
"s_add_u32 s47, s47, 1\n"
"v_cvt_pk_bf16_f32 v56, v56, v57\n"
"v_cvt_pk_bf16_f32 v17, v18, v19\n"
"v_mfma_f32_16x16x32_bf16 v[44:47], a[252:255], a[156:159], v[44:47]\n"
"s_waitcnt lgkmcnt(5)\n"
"ds_read_b128 a[152:155], v246 offset:4096\n"
"v_cvt_pk_bf16_f32 v57, v58, v59\n"
"v_mfma_f32_16x16x32_bf16 v[112:115], a[192:195], a[136:139], v[228:231]\n"
"s_waitcnt lgkmcnt(5)\n"
"v_cvt_pk_bf16_f32 v163, v166, v167\n"
"v_cvt_pk_bf16_f32 v8, v8, v9\n"
"v_mfma_f32_16x16x32_bf16 v[112:115], a[196:199], a[140:143], v[112:115]\n"
"s_waitcnt lgkmcnt(4)\n"
"ds_read_b128 a[148:151], v247 offset:2048\n"
"v_cvt_pk_bf16_f32 v24, v24, v25\n"
"v_mfma_f32_16x16x32_bf16 v[116:119], a[200:203], a[136:139], v[228:231]\n"
"s_waitcnt vmcnt(11) lgkmcnt(3)\n"
"ds_read_b128 a[188:191], v251 offset:4096\n"
"v_cvt_pk_bf16_f32 v66, v68, v69\n"
"v_mfma_f32_16x16x32_bf16 v[116:119], a[204:207], a[140:143], v[116:119]\n"
"s_barrier\n"
"ds_read_b128 a[176:179], v250 offset:2048\n"
"v_permlane16_swap_b32 v161, v163\n"
"v_accvgpr_write_b32 a26, v138\n"
"v_accvgpr_write_b32 a27, v139\n"
"v_pk_fma_f32 v[32:33], v[32:33], s[70:71], s[70:71] op_sel:[0,0,1] op_sel_hi:[1,0,1]\nv_pk_fma_f32 v[34:35], v[34:35], s[70:71], s[70:71] op_sel:[0,0,1] op_sel_hi:[1,0,1]\nv_pk_fma_f32 v[36:37], v[36:37], s[70:71], s[70:71] op_sel:[0,0,1] op_sel_hi:[1,0,1]\nv_pk_fma_f32 v[38:39], v[38:39], s[70:71], s[70:71] op_sel:[0,0,1] op_sel_hi:[1,0,1]\nv_pk_fma_f32 v[40:41], v[40:41], s[70:71], s[70:71] op_sel:[0,0,1] op_sel_hi:[1,0,1]\nv_pk_fma_f32 v[42:43], v[42:43], s[70:71], s[70:71] op_sel:[0,0,1] op_sel_hi:[1,0,1]\nv_pk_fma_f32 v[44:45], v[44:45], s[70:71], s[70:71] op_sel:[0,0,1] op_sel_hi:[1,0,1]\nv_pk_fma_f32 v[46:47], v[46:47], s[70:71], s[70:71] op_sel:[0,0,1] op_sel_hi:[1,0,1]\n"
"v_mfma_f32_16x16x32_bf16 v[120:123], a[208:211], a[136:139], v[228:231]\n"
"s_add_u32 s40, s48, 256\n"
"s_xor_b32 m0, s50, 0xc000\n"
"ds_read_b64_tr_b16 a[228:229], v244 offset:24576\n"
"v_mfma_f32_16x16x32_bf16 v[120:123], a[212:215], a[140:143], v[120:123]\n"
"buffer_load_dwordx4 v224, s[28:31], s40 offen lds\n"
"v_mfma_f32_16x16x32_bf16 v[124:127], a[216:219], a[136:139], v[228:231]\n"
"ds_read_b128 a[160:163], v252 offset:0\n"
"v_cvt_pk_bf16_f32 v32, v32, v33\n"
"v_mfma_f32_16x16x32_bf16 v[124:127], a[220:223], a[140:143], v[124:127]\n"
"s_waitcnt lgkmcnt(5)\n"
"v_accvgpr_write_b32 a25, v137\n"
"v_cvt_pk_bf16_f32 v81, v82, v83\n"
"v_mfma_f32_16x16x32_bf16 v[140:143], a[216:219], a[144:147], v[228:231]\n"
"s_waitcnt lgkmcnt(4)\n"
"ds_read_b128 a[156:159], v247 offset:4096\n"
"v_cvt_pk_bf16_f32 v48, v48, v49\n"
"v_mfma_f32_16x16x32_bf16 v[140:143], a[220:223], a[148:151], v[140:143]\n"
"ds_read_b128 a[180:183], v251 offset:2048\n"
"v_cvt_pk_bf16_f32 v25, v26, v27\n"
"v_mfma_f32_16x16x32_bf16 v[136:139], a[208:211], a[144:147], v[228:231]\n"
"v_cvt_pk_bf16_f32 v33, v34, v35\n"
"v_cvt_pk_bf16_f32 v40, v40, v41\n"
"v_mfma_f32_16x16x32_bf16 v[136:139], a[212:215], a[148:151], v[136:139]\n"
"buffer_load_dwordx4 v225, s[28:31], s40 offen offset:1024 lds\n"
"v_mfma_f32_16x16x32_bf16 v[132:135], a[200:203], a[144:147], v[228:231]\n"
"v_cvt_pk_bf16_f32 v91, v94, v95\n"
"v_cvt_pk_bf16_f32 v9, v10, v11\n"
"v_mfma_f32_16x16x32_bf16 v[132:135], a[204:207], a[148:151], v[132:135]\n"
"v_accvgpr_write_b32 a33, v145\n"
"v_cvt_pk_bf16_f32 v99, v102, v103\n"
"v_mfma_f32_16x16x32_bf16 v[128:131], a[192:195], a[144:147], v[228:231]\n"
"ds_read_b128 a[164:167], v253 offset:0\n"
"ds_read_b64_tr_b16 a[244:245], v244 offset:25600\n"
"v_mfma_f32_16x16x32_bf16 v[128:131], a[196:199], a[148:151], v[128:131]\n"
"ds_write_b128 v243, v[168:171] offset:2048\n"
"v_accvgpr_write_b32 a32, v144\n"
"v_accvgpr_write_b32 a42, v154\n"
"v_cvt_pk_bf16_f32 v72, v72, v73\n"
"v_cvt_pk_bf16_f32 v73, v74, v75\n"
"v_cvt_pk_bf16_f32 v1, v2, v3\n"
"v_mfma_f32_16x16x32_bf16 v[144:147], a[192:195], a[152:155], v[228:231]\n"
"s_waitcnt lgkmcnt(4)\n"
"v_cvt_pk_bf16_f32 v10, v12, v13\n"
"v_cvt_pk_bf16_f32 v18, v20, v21\n"
"v_mfma_f32_16x16x32_bf16 v[144:147], a[196:199], a[156:159], v[144:147]\n"
"buffer_load_dwordx4 v226, s[28:31], s40 offen offset:2048 lds\n"
"v_mfma_f32_16x16x32_bf16 v[148:151], a[200:203], a[152:155], v[228:231]\n"
"v_cvt_pk_bf16_f32 v3, v6, v7\n"
"v_cvt_pk_bf16_f32 v35, v38, v39\n"
"v_mfma_f32_16x16x32_bf16 v[148:151], a[204:207], a[156:159], v[148:151]\n"
"v_cvt_pk_bf16_f32 v2, v4, v5\n"
"v_cvt_pk_bf16_f32 v11, v14, v15\n"
"v_mfma_f32_16x16x32_bf16 v[152:155], a[208:211], a[152:155], v[228:231]\n"
"ds_read_b64_tr_b16 a[248:249], v245 offset:17408\n"
"v_cvt_pk_bf16_f32 v34, v36, v37\n"
"v_mfma_f32_16x16x32_bf16 v[152:155], a[212:215], a[156:159], v[152:155]\n"
"ds_write_b128 v243, v[160:163] offset:0\n"
"v_and_b32 v102, 0xf0, v242\n"
"v_cvt_pk_bf16_f32 v105, v106, v107\n"
"v_permlane16_swap_b32 v1, v3\n"
"v_permlane16_swap_b32 v9, v11\n"
"v_mfma_f32_16x16x32_bf16 v[156:159], a[216:219], a[152:155], v[228:231]\n"
"ds_read_b64_tr_b16 a[236:237], v245 offset:24576\n"
"v_cvt_pk_bf16_f32 v19, v22, v23\n"
"v_mfma_f32_16x16x32_bf16 v[156:159], a[220:223], a[156:159], v[156:159]\n"
"buffer_load_dwordx4 v227, s[28:31], s40 offen offset:3072 lds\n"
"v_mfma_f32_16x16x32_bf16 v[172:175], a[216:219], a[160:163], v[228:231]\n"
"s_waitcnt lgkmcnt(3)\n"
"s_cmp_eq_u32 s47, 8\n"
"v_cvt_pk_bf16_f32 v26, v28, v29\n"
"v_cvt_pk_bf16_f32 v27, v30, v31\n"
"v_mfma_f32_16x16x32_bf16 v[172:175], a[220:223], a[164:167], v[172:175]\n"
"s_cselect_b32 s41, s52, s58\n"
"v_permlane16_swap_b32 v16, v18\n"
"v_mfma_f32_16x16x32_bf16 v[168:171], a[208:211], a[160:163], v[228:231]\n"
"s_add_u32 s48, s48, s41\n"
"ds_read_b64_tr_b16 a[254:255], v245 offset:26112\n"
"v_accvgpr_write_b32 a59, v11\n"
"v_mfma_f32_16x16x32_bf16 v[168:171], a[212:215], a[164:167], v[168:171]\n"
"s_sub_u32 s41, s48, 5242880\n"
"ds_read_b64_tr_b16 a[226:227], v244 offset:16896\n"
"v_lshl_add_u32 v100, v100, 8, v102\n"
"v_mfma_f32_16x16x32_bf16 v[164:167], a[200:203], a[160:163], v[228:231]\n"
"s_min_u32 s48, s48, s41\n"
"ds_read_b64_tr_b16 a[240:241], v244 offset:17408\n"
"v_cvt_pk_bf16_f32 v67, v70, v71\n"
"v_mfma_f32_16x16x32_bf16 v[164:167], a[204:207], a[164:167], v[164:167]\n"
"ds_read_b64_tr_b16 a[234:235], v245 offset:16896\n"
"ds_read_b64_tr_b16 a[238:239], v245 offset:25088\n"
"v_cvt_pk_bf16_f32 v83, v86, v87\n"
"v_permlane16_swap_b32 v8, v10\n"
"v_permlane16_swap_b32 v17, v19\n"
"v_permlane16_swap_b32 v25, v27\n"
"v_cvt_pk_bf16_f32 v41, v42, v43\n"
"v_mfma_f32_16x16x32_bf16 v[160:163], a[192:195], a[160:163], v[228:231]\n"
"s_mov_b32 m0, s50\n"
"v_permlane16_swap_b32 v24, v26\n"
"v_mfma_f32_16x16x32_bf16 v[160:163], a[196:199], a[164:167], v[160:163]\n"
"buffer_load_dwordx4 v224, s[28:31], s48 offen lds\n"
"v_mfma_f32_16x16x32_bf16 v[176:179], a[192:195], a[168:171], v[228:231]\n"
"v_accvgpr_write_b32 a74, v26\n"
"v_cvt_pk_bf16_f32 v43, v46, v47\n"
"v_mfma_f32_16x16x32_bf16 v[176:179], a[196:199], a[172:175], v[176:179]\n"
"v_bfe_u32 v102, v242, 8, 1\n"
"v_cvt_pk_bf16_f32 v49, v50, v51\n"
"v_mfma_f32_16x16x32_bf16 v[180:183], a[200:203], a[168:171], v[228:231]\n"
"ds_read_b64_tr_b16 a[232:233], v245 offset:16384\n"
"v_cvt_pk_bf16_f32 v75, v78, v79\n"
"v_mfma_f32_16x16x32_bf16 v[180:183], a[204:207], a[172:175], v[180:183]\n"
"buffer_load_dwordx4 v225, s[28:31], s48 offen offset:1024 lds\n"
"v_mfma_f32_16x16x32_bf16 v[184:187], a[208:211], a[168:171], v[228:231]\n"
"v_permlane16_swap_b32 v33, v35\n"
"v_mfma_f32_16x16x32_bf16 v[184:187], a[212:215], a[172:175], v[184:187]\n"
"v_permlane16_swap_b32 v0, v2\n"
"v_mfma_f32_16x16x32_bf16 v[188:191], a[216:219], a[168:171], v[228:231]\n"
"ds_read_b64_tr_b16 a[242:243], v244 offset:17920\n"
"v_cvt_pk_bf16_f32 v90, v92, v93\n"
"v_mfma_f32_16x16x32_bf16 v[188:191], a[220:223], a[172:175], v[188:191]\n"
"v_lshl_add_u32 v100, v102, 3, v100\n"
"v_xor_b32_e64 v102, s42, 1\n"
"v_mfma_f32_16x16x32_bf16 v[204:207], a[216:219], a[176:179], v[228:231]\n"
"v_cvt_pk_bf16_f32 v107, v110, v111\n"
"v_bfe_u32 v101, v242, 9, 1\n"
"v_mfma_f32_16x16x32_bf16 v[204:207], a[220:223], a[180:183], v[204:207]\n"
"buffer_load_dwordx4 v226, s[28:31], s48 offen offset:2048 lds\n"
"v_mfma_f32_16x16x32_bf16 v[200:203], a[208:211], a[176:179], v[228:231]\n"
"v_lshl_add_u32 v100, v102, 10, v100\n"
"v_cvt_pk_bf16_f32 v82, v84, v85\n"
"v_mfma_f32_16x16x32_bf16 v[200:203], a[212:215], a[180:183], v[200:203]\n"
"ds_read_b64_tr_b16 a[250:251], v245 offset:17920\n"
"ds_read_b64_tr_b16 a[252:253], v245 offset:25600\n"
"v_mfma_f32_16x16x32_bf16 v[196:199], a[200:203], a[176:179], v[228:231]\n"
"ds_read_b64_tr_b16 a[246:247], v244 offset:26112\n"
"v_cvt_pk_bf16_f32 v58, v60, v61\n"
"v_mfma_f32_16x16x32_bf16 v[196:199], a[204:207], a[180:183], v[196:199]\n"
"v_cvt_pk_bf16_f32 v74, v76, v77\n"
"v_accvgpr_write_b32 a56, v8\n"
"v_mfma_f32_16x16x32_bf16 v[192:195], a[192:195], a[176:179], v[228:231]\n"
"ds_read_b64_tr_b16 a[224:225], v244 offset:16384\n"
"ds_read_b64_tr_b16 a[230:231], v244 offset:25088\n"
"v_mfma_f32_16x16x32_bf16 v[192:195], a[196:199], a[180:183], v[192:195]\n"
"buffer_load_dwordx4 v227, s[28:31], s48 offen offset:3072 lds\n"
"v_mfma_f32_16x16x32_bf16 v[208:211], a[192:195], a[184:187], v[228:231]\n"
"s_xor_b32 s50, s50, s74\n"
"v_cvt_pk_bf16_f32 v51, v54, v55\n"
"v_accvgpr_write_b32 a48, v0\n"
"v_mfma_f32_16x16x32_bf16 v[208:211], a[196:199], a[188:191], v[208:211]\n"
"v_add_u32 v100, 94208, v100\n"
"v_accvgpr_write_b32 a50, v2\n"
"v_mfma_f32_16x16x32_bf16 v[212:215], a[200:203], a[184:187], v[228:231]\n"
"v_permlane16_swap_b32 v32, v34\n"
"v_mfma_f32_16x16x32_bf16 v[212:215], a[204:207], a[188:191], v[212:215]\n"
"ds_write2st64_b64 v100, v[72:73], v[74:75] offset0:64 offset1:65\n"
"v_cvt_pk_bf16_f32 v106, v108, v109\n"
"v_cvt_pk_bf16_f32 v50, v52, v53\n"
"v_permlane16_swap_b32 v41, v43\n"
"v_mfma_f32_16x16x32_bf16 v[216:219], a[208:211], a[184:187], v[228:231]\n"
"v_accvgpr_write_b32 a51, v3\n"
"v_and_b32 v102, 0xf0, v242\n"
"v_mfma_f32_16x16x32_bf16 v[216:219], a[212:215], a[188:191], v[216:219]\n"
"ds_write2st64_b64 v100, v[104:105], v[106:107] offset0:128 offset1:129\n"
"v_accvgpr_write_b32 a49, v1\n"
"v_accvgpr_write_b32 a57, v9\n"
"v_mfma_f32_16x16x32_bf16 v[220:223], a[216:219], a[184:187], v[228:231]\n"
"v_accvgpr_write_b32 a73, v25\n"
"v_accvgpr_write_b32 a83, v35\n"
"v_mfma_f32_16x16x32_bf16 v[220:223], a[220:223], a[188:191], v[220:223]\n"
"s_waitcnt lgkmcnt(2)\n"
"ds_write2st64_b64 v100, v[80:81], v[82:83] offset0:88 offset1:89\n"
"v_permlane16_swap_b32 v49, v51\n"
"v_accvgpr_write_b32 a58, v10\n"
"v_mfma_f32_16x16x32_bf16 v[0:3], a[224:227], a[136:139], v[228:231]\n"
"v_permlane16_swap_b32 v48, v50\n"
"v_mfma_f32_16x16x32_bf16 v[0:3], a[228:231], a[140:143], v[0:3]\n"
"ds_write_b128 v243, v[48:51] offset:4096\n"
"v_accvgpr_write_b32 a65, v17\n"
"v_mfma_f32_16x16x32_bf16 v[4:7], a[232:235], a[136:139], v[228:231]\n"
"s_waitcnt vmcnt(8)\n"
"v_accvgpr_write_b32 a72, v24\n"
"v_accvgpr_write_b32 a89, v41\n"
"v_mfma_f32_16x16x32_bf16 v[4:7], a[236:239], a[140:143], v[4:7]\n"
"s_barrier\n"
"ds_write2st64_b64 v100, v[88:89], v[90:91] offset0:96 offset1:97\n"
"v_mfma_f32_16x16x32_bf16 v[8:11], a[240:243], a[136:139], v[228:231]\n"
"s_mov_b32 m0, s51\n"
"v_accvgpr_write_b32 a75, v27\n"
"v_accvgpr_write_b32 a82, v34\n"
"v_mfma_f32_16x16x32_bf16 v[8:11], a[244:247], a[140:143], v[8:11]\n"
"buffer_load_dwordx4 v248, s[24:27], s49 offen lds\n"
"v_mfma_f32_16x16x32_bf16 v[12:15], a[248:251], a[136:139], v[228:231]\n"
"v_accvgpr_write_b32 a67, v19\n"
"v_xor_b32 v246, s74, v246\n"
"v_mfma_f32_16x16x32_bf16 v[12:15], a[252:255], a[140:143], v[12:15]\n"
"ds_write2st64_b64 v100, v[96:97], v[98:99] offset0:120 offset1:121\n"
"v_cvt_pk_bf16_f32 v59, v62, v63\n"
"v_mfma_f32_16x16x32_bf16 v[28:31], a[248:251], a[144:147], v[228:231]\n"
"v_accvgpr_write_b32 a64, v16\n"
"v_cvt_pk_bf16_f32 v42, v44, v45\n"
"v_mfma_f32_16x16x32_bf16 v[28:31], a[252:255], a[148:151], v[28:31]\n"
"ds_write2st64_b64 v100, v[64:65], v[66:67] offset0:0 offset1:1\n"
"v_permlane16_swap_b32 v56, v58\n"
"v_permlane16_swap_b32 v40, v42\n"
"v_mfma_f32_16x16x32_bf16 v[24:27], a[240:243], a[144:147], v[228:231]\n"
"ds_read_b128 a[136:139], v246 offset:0\n"
"v_accvgpr_write_b32 a66, v18\n"
"v_mfma_f32_16x16x32_bf16 v[24:27], a[244:247], a[148:151], v[24:27]\n"
"buffer_load_dwordx4 v249, s[24:27], s49 offen offset:1024 lds\n"
"v_mfma_f32_16x16x32_bf16 v[20:23], a[232:235], a[144:147], v[228:231]\n"
"v_permlane16_swap_b32 v57, v59\n"
"v_mfma_f32_16x16x32_bf16 v[20:23], a[236:239], a[148:151], v[20:23]\n"
"ds_write_b128 v243, v[56:59] offset:6144\n"
"v_mfma_f32_16x16x32_bf16 v[16:19], a[224:227], a[144:147], v[228:231]\n"
"v_accvgpr_write_b32 a80, v32\n"
"v_xor_b32 v244, s74, v244\n"
"v_mfma_f32_16x16x32_bf16 v[16:19], a[228:231], a[148:151], v[16:19]\n"
"v_accvgpr_write_b32 a81, v33\n"
"v_accvgpr_write_b32 a90, v42\n"
"v_mfma_f32_16x16x32_bf16 v[32:35], a[224:227], a[152:155], v[228:231]\n"
"ds_read_b64_tr_b16 a[212:213], v244 offset:9216\n"
"v_accvgpr_write_b32 a88, v40\n"
"v_mfma_f32_16x16x32_bf16 v[32:35], a[228:231], a[156:159], v[32:35]\n"
"buffer_load_dwordx4 v254, s[24:27], s49 offen offset:2048 lds\n"
"v_mfma_f32_16x16x32_bf16 v[36:39], a[232:235], a[152:155], v[228:231]\n"
"s_waitcnt vmcnt(63) lgkmcnt(2)\n"
"v_accvgpr_write_b32 a91, v43\n"
"v_xor_b32 v247, s74, v247\n"
"v_mfma_f32_16x16x32_bf16 v[36:39], a[236:239], a[156:159], v[36:39]\n"
"s_barrier\n"
"v_mfma_f32_16x16x32_bf16 v[40:43], a[240:243], a[152:155], v[228:231]\n"
"ds_read_b64_tr_b16 a[192:193], v244 offset:0\n"
"ds_read_b128 a[144:147], v246 offset:2048\n"
"v_mfma_f32_16x16x32_bf16 v[40:43], a[244:247], a[156:159], v[40:43]\n"
"ds_read_b128 a[148:151], v247 offset:2048\n"
"v_xor_b32 v251, s74, v251\n"
"v_mfma_f32_16x16x32_bf16 v[44:47], a[248:251], a[152:155], v[228:231]\n"
"ds_read_b128 a[152:155], v246 offset:4096\n"
"v_lshl_add_u32 v101, v101, 8, v102\n"
"v_mfma_f32_16x16x32_bf16 v[44:47], a[252:255], a[156:159], v[44:47]\n"
"buffer_load_dwordx4 v255, s[24:27], s49 offen offset:3072 lds\n"
"v_mfma_f32_16x16x32_bf16 v[60:63], a[248:251], a[160:163], v[228:231]\n"
"v_bfe_u32 v102, v242, 8, 1\n"
"v_lshl_add_u32 v101, v102, 9, v101\n"
"v_mfma_f32_16x16x32_bf16 v[60:63], a[252:255], a[164:167], v[60:63]\n"
"v_xor_b32_e64 v102, s42, 0\n"
"v_lshl_add_u32 v101, v102, 10, v101\n"
"v_mfma_f32_16x16x32_bf16 v[56:59], a[240:243], a[160:163], v[228:231]\n"
"ds_read_b128 a[156:159], v247 offset:4096\n"
"v_xor_b32 v245, s74, v245\n"
"v_mfma_f32_16x16x32_bf16 v[56:59], a[244:247], a[164:167], v[56:59]\n"
"s_add_u32 s40, s49, 40960\n"
"ds_read_b64_tr_b16 a[210:211], v244 offset:1536\n"
"ds_read_b64_tr_b16 a[216:217], v245 offset:1024\n"
"v_mfma_f32_16x16x32_bf16 v[52:55], a[232:235], a[160:163], v[228:231]\n"
"s_add_u32 m0, s51, 4096\n"
"ds_read_b64_tr_b16 a[196:197], v244 offset:8192\n"
"ds_read_b64_tr_b16 a[214:215], v244 offset:9728\n"
"v_mfma_f32_16x16x32_bf16 v[52:55], a[236:239], a[164:167], v[52:55]\n"
"buffer_load_dwordx4 v248, s[24:27], s40 offen lds\n"
"v_mfma_f32_16x16x32_bf16 v[48:51], a[224:227], a[160:163], v[228:231]\n"
"v_add_u32 v101, 94208, v101\n"
"v_add_u32 v102, s75, v101\n"
"v_mfma_f32_16x16x32_bf16 v[48:51], a[228:231], a[164:167], v[48:51]\n"
"ds_read_b128 a[52:55], v101 offset:0\n"
"ds_read_b128 a[92:95], v102 offset:0\n"
"v_mfma_f32_16x16x32_bf16 v[64:67], a[224:227], a[168:171], v[228:231]\n"
"ds_read_b128 a[68:71], v101 offset:45056\n"
"v_mfma_f32_16x16x32_bf16 v[64:67], a[228:231], a[172:175], v[64:67]\n"
"ds_read_b128 a[60:63], v101 offset:32768\n"
"v_xor_b32 v252, s74, v252\n"
"v_mfma_f32_16x16x32_bf16 v[68:71], a[232:235], a[168:171], v[228:231]\n"
"ds_read_b64_tr_b16 a[218:219], v245 offset:1536\n"
"ds_read_b128 a[76:79], v101 offset:49152\n"
"v_mfma_f32_16x16x32_bf16 v[68:71], a[236:239], a[172:175], v[68:71]\n"
"buffer_load_dwordx4 v249, s[24:27], s40 offen offset:1024 lds\n"
"v_mfma_f32_16x16x32_bf16 v[72:75], a[240:243], a[168:171], v[228:231]\n"
"v_xor_b32 v250, s74, v250\n"
"v_xor_b32 v253, s74, v253\n"
"v_mfma_f32_16x16x32_bf16 v[72:75], a[244:247], a[172:175], v[72:75]\n"
"ds_read_b64_tr_b16 a[206:207], v245 offset:8704\n"
"ds_read_b128 a[84:87], v101 offset:61440\n"
"v_mfma_f32_16x16x32_bf16 v[76:79], a[248:251], a[168:171], v[228:231]\n"
"ds_read_b64_tr_b16 a[204:205], v245 offset:8192\n"
"ds_read_b64_tr_b16 a[220:221], v245 offset:9216\n"
"v_mfma_f32_16x16x32_bf16 v[76:79], a[252:255], a[172:175], v[76:79]\n"
"ds_read_b64_tr_b16 a[194:195], v244 offset:512\n"
"ds_read_b64_tr_b16 a[198:199], v244 offset:8704\n"
"v_mfma_f32_16x16x32_bf16 v[92:95], a[248:251], a[176:179], v[228:231]\n"
"ds_read_b64_tr_b16 a[222:223], v245 offset:9728\n"
"ds_read_b128 a[160:163], v252 offset:0\n"
"v_mfma_f32_16x16x32_bf16 v[92:95], a[252:255], a[180:183], v[92:95]\n"
"buffer_load_dwordx4 v254, s[24:27], s40 offen offset:2048 lds\n"
"v_mfma_f32_16x16x32_bf16 v[88:91], a[240:243], a[176:179], v[228:231]\n"
"s_add_u32 s49, s49, 128\n"
"ds_read_b64_tr_b16 a[202:203], v245 offset:512\n"
"ds_read_b128 a[140:143], v247 offset:0\n"
"v_mfma_f32_16x16x32_bf16 v[88:91], a[244:247], a[180:183], v[88:91]\n"
"s_sub_u32 s41, s49, 1280\n"
"ds_read_b64_tr_b16 a[208:209], v244 offset:1024\n"
"ds_read_b128 a[172:175], v251 offset:0\n"
"v_mfma_f32_16x16x32_bf16 v[84:87], a[232:235], a[176:179], v[228:231]\n"
"s_min_u32 s49, s49, s41\n"
"ds_read_b128 a[164:167], v253 offset:0\n"
"v_mfma_f32_16x16x32_bf16 v[84:87], a[236:239], a[180:183], v[84:87]\n"
"s_xor_b32 s51, s51, s74\n"
"ds_read_b64_tr_b16 a[200:201], v245 offset:0\n"
"ds_read_b128 a[168:171], v250 offset:0\n"
"v_mfma_f32_16x16x32_bf16 v[80:83], a[224:227], a[176:179], v[228:231]\n"
"s_sub_u32 s78, s78, s65\n"
"s_sub_u32 s79, s80, s67\n"
"s_mov_b32 s65, 0\n"
"v_mfma_f32_16x16x32_bf16 v[80:83], a[228:231], a[180:183], v[80:83]\n"
"s_cmp_gt_u32 s78, 21100\n"
"ds_read_b128 a[176:179], v250 offset:2048\n"
"ds_read_b128 a[180:183], v251 offset:2048\n"
"v_mfma_f32_16x16x32_bf16 v[96:99], a[224:227], a[184:187], v[228:231]\n"
"s_cselect_b32 s68, 1504, 0\n"
"s_cselect_b32 s72, 114, s72\n"
"s_cmp_lt_i32 s79, -2800\n"
"v_mfma_f32_16x16x32_bf16 v[96:99], a[228:231], a[188:191], v[96:99]\n"
"s_cselect_b32 s64, 1, s68\n"
"s_cmp_eq_u32 s42, 0\n"
"v_mfma_f32_16x16x32_bf16 v[100:103], a[232:235], a[184:187], v[228:231]\n"
"s_cselect_b32 s64, s64, 0\n"
"s_add_u32 s80, s8, s63\n"
"v_mfma_f32_16x16x32_bf16 v[100:103], a[236:239], a[188:191], v[100:103]\n"
"s_addc_u32 s81, s9, 0\n"
"s_mov_b32 s82, s68\n"
"v_mfma_f32_16x16x32_bf16 v[104:107], a[240:243], a[184:187], v[228:231]\n"
"s_mov_b64 vcc, s[64:65]\n"
"v_mfma_f32_16x16x32_bf16 v[104:107], a[244:247], a[188:191], v[104:107]\n"
"v_mfma_f32_16x16x32_bf16 v[108:111], a[248:251], a[184:187], v[228:231]\n"
"s_add_u32 s47, s47, 1\n"
"ds_read_b128 a[184:187], v250 offset:4096\n"
"v_mfma_f32_16x16x32_bf16 v[108:111], a[252:255], a[188:191], v[108:111]\n"
"s_waitcnt lgkmcnt(1)\n"
"ds_read_b128 a[188:191], v251 offset:4096\n"
"s_mov_b32 s62, 18\n"
"s_cmp_eq_u32 s68, 0\n"
"s_cbranch_scc1 LPL_%=\n"
"s_branch LPN_%=\n"
"LPT2_%=:\n"
"v_mfma_f32_16x16x32_bf16 v[112:115], a[192:195], a[136:139], v[112:115]\n"
"s_add_u32 s78, s32, s96\ns_addc_u32 s79, s33, 0\n"
"s_mov_b64 s[80:81], 0\n"
"s_mov_b32 s82, 0\n"
"v_mfma_f32_16x16x32_bf16 v[112:115], a[196:199], a[140:143], v[112:115]\n"
"v_xor_b32 v248, 64, v240\n"
"v_mfma_f32_16x16x32_bf16 v[116:119], a[200:203], a[136:139], v[116:119]\n"
"s_waitcnt vmcnt(11)\n"
"s_load_dword s80, s[78:79], 0\n"
"v_mfma_f32_16x16x32_bf16 v[116:119], a[204:207], a[140:143], v[116:119]\n"
"s_barrier\n"
"v_mfma_f32_16x16x32_bf16 v[120:123], a[208:211], a[136:139], v[120:123]\n"
"s_add_u32 s40, s48, 256\n"
"s_xor_b32 m0, s50, 0xc000\n"
"ds_read_b64_tr_b16 a[238:239], v245 offset:25088\n"
"v_mfma_f32_16x16x32_bf16 v[120:123], a[212:215], a[140:143], v[120:123]\n"
"buffer_load_dwordx4 v224, s[28:31], s40 offen lds\n"
"v_mfma_f32_16x16x32_bf16 v[124:127], a[216:219], a[136:139], v[124:127]\n"
"v_mfma_f32_16x16x32_bf16 v[124:127], a[220:223], a[140:143], v[124:127]\n"
"v_mfma_f32_16x16x32_bf16 v[140:143], a[216:219], a[144:147], v[140:143]\n"
"ds_read_b64_tr_b16 a[244:245], v244 offset:25600\n"
"v_mfma_f32_16x16x32_bf16 v[140:143], a[220:223], a[148:151], v[140:143]\n"
"v_mfma_f32_16x16x32_bf16 v[136:139], a[208:211], a[144:147], v[136:139]\n"
"ds_read_b64_tr_b16 a[250:251], v245 offset:17920\n"
"v_bfe_i32 v241, v242, 4, 1\n"
"v_mfma_f32_16x16x32_bf16 v[136:139], a[212:215], a[148:151], v[136:139]\n"
"buffer_load_dwordx4 v225, s[28:31], s40 offen offset:1024 lds\n"
"v_mfma_f32_16x16x32_bf16 v[132:135], a[200:203], a[144:147], v[132:135]\n"
"s_add_u32 s82, s82, s80\n"
"v_mfma_f32_16x16x32_bf16 v[132:135], a[204:207], a[148:151], v[132:135]\n"
"s_add_u32 s82, s82, s80\n"
"v_mfma_f32_16x16x32_bf16 v[128:131], a[192:195], a[144:147], v[128:131]\n"
"s_add_u32 s82, s82, s80\n"
"ds_read_b64_tr_b16 a[230:231], v244 offset:25088\n"
"v_mfma_f32_16x16x32_bf16 v[128:131], a[196:199], a[148:151], v[128:131]\n"
"s_add_u32 s82, s82, s80\n"
"v_bfe_u32 v228, v242, 8, 2\n"
"v_mfma_f32_16x16x32_bf16 v[144:147], a[192:195], a[152:155], v[144:147]\n"
"s_add_u32 s82, s82, s80\n"
"ds_read_b64_tr_b16 a[242:243], v244 offset:17920\n"
"v_mfma_f32_16x16x32_bf16 v[144:147], a[196:199], a[156:159], v[144:147]\n"
"s_add_u32 s82, s82, s80\n"
"v_mfma_f32_16x16x32_bf16 v[148:151], a[200:203], a[152:155], v[148:151]\n"
"s_add_u32 s82, s82, s80\n"
"ds_read_b64_tr_b16 a[228:229], v244 offset:24576\n"
"v_mfma_f32_16x16x32_bf16 v[148:151], a[204:207], a[156:159], v[148:151]\n"
"s_add_u32 s82, s82, s80\n"
"buffer_load_dwordx4 v226, s[28:31], s40 offen offset:2048 lds\n"
"v_mfma_f32_16x16x32_bf16 v[152:155], a[208:211], a[152:155], v[152:155]\n"
"s_add_u32 s82, s82, s80\n"
"v_mfma_f32_16x16x32_bf16 v[152:155], a[212:215], a[156:159], v[152:155]\n"
"s_add_u32 s82, s82, s80\n"
"v_and_b32 v229, 0x60, v242\n"
"v_mfma_f32_16x16x32_bf16 v[156:159], a[216:219], a[152:155], v[156:159]\n"
"s_add_u32 s82, s82, s80\n"
"ds_read_b64_tr_b16 a[232:233], v245 offset:16384\n"
"v_mfma_f32_16x16x32_bf16 v[156:159], a[220:223], a[156:159], v[156:159]\n"
"s_add_u32 s82, s82, s80\n"
"v_mfma_f32_16x16x32_bf16 v[172:175], a[216:219], a[160:163], v[172:175]\n"
"s_add_u32 s82, s82, s80\n"
"ds_read_b64_tr_b16 a[234:235], v245 offset:16896\n"
"v_lshlrev_b32 v228, 4, v228\n"
"v_mfma_f32_16x16x32_bf16 v[172:175], a[220:223], a[164:167], v[172:175]\n"
"s_add_u32 s82, s82, s80\n"
"buffer_load_dwordx4 v227, s[28:31], s40 offen offset:3072 lds\n"
"v_mfma_f32_16x16x32_bf16 v[168:171], a[208:211], a[160:163], v[168:171]\n"
"s_add_u32 s82, s82, s80\n"
"s_mov_b32 s58, 122880\n"
"v_mfma_f32_16x16x32_bf16 v[168:171], a[212:215], a[164:167], v[168:171]\n"
"s_add_u32 s82, s82, s80\n"
"s_mov_b32 s47, 0\n"
"v_mfma_f32_16x16x32_bf16 v[164:167], a[200:203], a[160:163], v[164:167]\n"
"s_add_u32 s82, s82, s80\n"
"s_sub_u32 s52, s54, 14336\n"
"ds_read_b64_tr_b16 a[246:247], v244 offset:26112\n"
"v_mfma_f32_16x16x32_bf16 v[164:167], a[204:207], a[164:167], v[164:167]\n"
"s_load_dword s81, s[78:79], 0x2000\n"
"s_mov_b32 s53, 14336\n"
"v_mfma_f32_16x16x32_bf16 v[160:163], a[192:195], a[160:163], v[160:163]\n"
"s_mov_b32 m0, s44\n"
"ds_read_b64_tr_b16 a[240:241], v244 offset:17408\n"
"v_mfma_f32_16x16x32_bf16 v[160:163], a[196:199], a[164:167], v[160:163]\n"
"buffer_load_dwordx4 v240, s[16:19], s56 offen nt lds\n"
"v_mfma_f32_16x16x32_bf16 v[176:179], a[192:195], a[168:171], v[176:179]\n"
"v_mfma_f32_16x16x32_bf16 v[176:179], a[196:199], a[172:175], v[176:179]\n"
"v_mfma_f32_16x16x32_bf16 v[180:183], a[200:203], a[168:171], v[180:183]\n"
"ds_read_b64_tr_b16 a[252:253], v245 offset:25600\n"
"v_mfma_f32_16x16x32_bf16 v[180:183], a[204:207], a[172:175], v[180:183]\n"
"v_mfma_f32_16x16x32_bf16 v[184:187], a[208:211], a[168:171], v[184:187]\n"
"ds_read_b64_tr_b16 a[236:237], v245 offset:24576\n"
"v_xor_b32 v228, v228, v229\n"
"v_mfma_f32_16x16x32_bf16 v[184:187], a[212:215], a[172:175], v[184:187]\n"
"v_mfma_f32_16x16x32_bf16 v[188:191], a[216:219], a[168:171], v[188:191]\n"
"s_add_u32 s40, s56, s52\n"
"ds_read_b64_tr_b16 a[226:227], v244 offset:16896\n"
"v_mfma_f32_16x16x32_bf16 v[188:191], a[220:223], a[172:175], v[188:191]\n"
"s_sub_u32 s82, s82, s81\n"
"buffer_load_dwordx4 v248, s[16:19], s40 offen offset:1024 nt lds\n"
"v_mfma_f32_16x16x32_bf16 v[204:207], a[216:219], a[176:179], v[204:207]\n"
"s_sub_u32 s82, s82, s81\n"
"v_mfma_f32_16x16x32_bf16 v[204:207], a[220:223], a[180:183], v[204:207]\n"
"s_sub_u32 s82, s82, s81\n"
"v_mfma_f32_16x16x32_bf16 v[200:203], a[208:211], a[176:179], v[200:203]\n"
"s_sub_u32 s82, s82, s81\n"
"ds_read_b64_tr_b16 a[248:249], v245 offset:17408\n"
"v_and_b32 v229, 0x30, v242\n"
"v_mfma_f32_16x16x32_bf16 v[200:203], a[212:215], a[180:183], v[200:203]\n"
"s_sub_u32 s82, s82, s81\n"
"v_mfma_f32_16x16x32_bf16 v[196:199], a[200:203], a[176:179], v[196:199]\n"
"s_sub_u32 s82, s82, s81\n"
"s_add_u32 s40, s56, s53\n"
"ds_read_b64_tr_b16 a[224:225], v244 offset:16384\n"
"v_mfma_f32_16x16x32_bf16 v[196:199], a[204:207], a[180:183], v[196:199]\n"
"s_sub_u32 s82, s82, s81\n"
"buffer_load_dwordx4 v240, s[16:19], s40 offen offset:2048 nt lds\n"
"v_mfma_f32_16x16x32_bf16 v[192:195], a[192:195], a[176:179], v[192:195]\n"
"s_sub_u32 s82, s82, s81\n"
"v_lshl_add_u32 v228, v229, 3, v228\n"
"v_mfma_f32_16x16x32_bf16 v[192:195], a[196:199], a[180:183], v[192:195]\n"
"s_waitcnt lgkmcnt(2)\n"
"s_sub_u32 s82, s82, s81\n"
"v_xor_b32 v244, s74, v244\n"
"v_mfma_f32_16x16x32_bf16 v[208:211], a[192:195], a[184:187], v[208:211]\n"
"s_sub_u32 s82, s82, s81\n"
"v_mfma_f32_16x16x32_bf16 v[208:211], a[196:199], a[188:191], v[208:211]\n"
"s_sub_u32 s82, s82, s81\n"
"ds_read_b64_tr_b16 a[254:255], v245 offset:26112\n"
"v_mfma_f32_16x16x32_bf16 v[212:215], a[200:203], a[184:187], v[212:215]\n"
"s_sub_u32 s82, s82, s81\n"
"s_add_u32 s40, s56, s54\n"
"v_mfma_f32_16x16x32_bf16 v[212:215], a[204:207], a[188:191], v[212:215]\n"
"s_sub_u32 s82, s82, s81\n"
"buffer_load_dwordx4 v248, s[16:19], s40 offen offset:3072 nt lds\n"
"v_mfma_f32_16x16x32_bf16 v[216:219], a[208:211], a[184:187], v[216:219]\n"
"s_sub_u32 s82, s82, s81\n"
"v_and_b32 v229, 0xc0, v242\n"
"v_xor_b32 v251, s74, v251\n"
"v_mfma_f32_16x16x32_bf16 v[216:219], a[212:215], a[188:191], v[216:219]\n"
"s_sub_u32 s82, s82, s81\n"
"v_mfma_f32_16x16x32_bf16 v[220:223], a[216:219], a[184:187], v[220:223]\n"
"s_sub_u32 s82, s82, s81\n"
"v_mfma_f32_16x16x32_bf16 v[220:223], a[220:223], a[188:191], v[220:223]\n"
"s_waitcnt lgkmcnt(1)\n"
"s_sub_u32 s82, s82, s81\n"
"v_mfma_f32_16x16x32_bf16 v[0:3], a[224:227], a[136:139], v[0:3]\n"
"v_mfma_f32_16x16x32_bf16 v[0:3], a[228:231], a[140:143], v[0:3]\n"
"v_lshl_add_u32 v228, v229, 4, v228\n"
"v_xor_b32 v245, s74, v245\n"
"v_mfma_f32_16x16x32_bf16 v[4:7], a[232:235], a[136:139], v[4:7]\n"
"s_waitcnt vmcnt(8) lgkmcnt(0)\n"
"v_mfma_f32_16x16x32_bf16 v[4:7], a[236:239], a[140:143], v[4:7]\n"
"s_barrier\n"
"v_mfma_f32_16x16x32_bf16 v[8:11], a[240:243], a[136:139], v[8:11]\n"
"s_add_u32 m0, s44, 65536\n"
"ds_read_b64_tr_b16 a[222:223], v245 offset:9728\n"
"v_mfma_f32_16x16x32_bf16 v[8:11], a[244:247], a[140:143], v[8:11]\n"
"buffer_load_dwordx4 v240, s[20:23], s56 offen nt lds\n"
"v_mfma_f32_16x16x32_bf16 v[12:15], a[248:251], a[136:139], v[12:15]\n"
"s_lshr_b32 s82, s82, 31\n"
"v_mfma_f32_16x16x32_bf16 v[12:15], a[252:255], a[140:143], v[12:15]\n"
"s_add_u32 s82, s82, s60\n"
"v_mfma_f32_16x16x32_bf16 v[28:31], a[248:251], a[144:147], v[28:31]\n"
"s_add_u32 s82, s82, 32\n"
"ds_read_b64_tr_b16 a[216:217], v245 offset:1024\n"
"v_xor_b32 v247, s74, v247\n"
"v_mfma_f32_16x16x32_bf16 v[28:31], a[252:255], a[148:151], v[28:31]\n"
"v_mov_b32 v254, s82\n"
"ds_read_b64_tr_b16 a[196:197], v244 offset:8192\n"
"v_mfma_f32_16x16x32_bf16 v[24:27], a[240:243], a[144:147], v[24:27]\n"
"v_lshlrev_b32_e64 v255, 2, s93\n"
"ds_read_b64_tr_b16 a[198:199], v244 offset:8704\n"
"v_xor_b32 v246, s74, v246\n"
"v_mfma_f32_16x16x32_bf16 v[24:27], a[244:247], a[148:151], v[24:27]\n"
"ds_read_b64_tr_b16 a[214:215], v244 offset:9728\n"
"v_mov_b32 v229, s43\n"
"v_mfma_f32_16x16x32_bf16 v[20:23], a[232:235], a[144:147], v[20:23]\n"
"s_add_u32 s40, s56, s52\n"
"ds_read_b64_tr_b16 a[204:205], v245 offset:8192\n"
"v_mfma_f32_16x16x32_bf16 v[20:23], a[236:239], a[148:151], v[20:23]\n"
"buffer_load_dwordx4 v248, s[20:23], s40 offen offset:1024 nt lds\n"
"v_mfma_f32_16x16x32_bf16 v[16:19], a[224:227], a[144:147], v[16:19]\n"
"v_mfma_f32_16x16x32_bf16 v[16:19], a[228:231], a[148:151], v[16:19]\n"
"s_mov_b64 exec, 1\n"
"global_store_dword v255, v254, s[6:7] offset:256 sc1\n"
"s_mov_b64 exec, -1\n"
"v_mfma_f32_16x16x32_bf16 v[32:35], a[224:227], a[152:155], v[32:35]\n"
"ds_read_b64_tr_b16 a[202:203], v245 offset:512\n"
"v_mfma_f32_16x16x32_bf16 v[32:35], a[228:231], a[156:159], v[32:35]\n"
"v_mfma_f32_16x16x32_bf16 v[36:39], a[232:235], a[152:155], v[36:39]\n"
"s_add_u32 s40, s56, s53\n"
"ds_read_b128 a[140:143], v247 offset:0\n"
"v_mfma_f32_16x16x32_bf16 v[36:39], a[236:239], a[156:159], v[36:39]\n"
"buffer_load_dwordx4 v240, s[20:23], s40 offen offset:2048 nt lds\n"
"v_mfma_f32_16x16x32_bf16 v[40:43], a[240:243], a[152:155], v[40:43]\n"
"v_xor_b32 v250, s74, v250\n"
"v_mfma_f32_16x16x32_bf16 v[40:43], a[244:247], a[156:159], v[40:43]\n"
"v_mfma_f32_16x16x32_bf16 v[44:47], a[248:251], a[152:155], v[44:47]\n"
"ds_read_b64_tr_b16 a[200:201], v245 offset:0\n"
"v_mul_u32_u24 v229, 0x81, v229\n"
"v_mfma_f32_16x16x32_bf16 v[44:47], a[252:255], a[156:159], v[44:47]\n"
"ds_read_b64_tr_b16 a[220:221], v245 offset:9216\n"
"v_mfma_f32_16x16x32_bf16 v[60:63], a[248:251], a[160:163], v[60:63]\n"
"s_add_u32 s40, s56, s54\n"
"ds_read_b64_tr_b16 a[210:211], v244 offset:1536\n"
"v_xor_b32 v253, s74, v253\n"
"v_mfma_f32_16x16x32_bf16 v[60:63], a[252:255], a[164:167], v[60:63]\n"
"buffer_load_dwordx4 v248, s[20:23], s40 offen offset:3072 nt lds\n"
"v_mfma_f32_16x16x32_bf16 v[56:59], a[240:243], a[160:163], v[56:59]\n"
"s_add_u32 s55, s55, s59\n"
"v_mfma_f32_16x16x32_bf16 v[56:59], a[244:247], a[164:167], v[56:59]\n"
"s_or_b32 s55, s55, s58\n"
"v_mfma_f32_16x16x32_bf16 v[52:55], a[232:235], a[160:163], v[52:55]\n"
"s_xor_b32 s56, s55, s57\n"
"ds_read_b128 a[136:139], v246 offset:0\n"
"v_mfma_f32_16x16x32_bf16 v[52:55], a[236:239], a[164:167], v[52:55]\n"
"v_mfma_f32_16x16x32_bf16 v[48:51], a[224:227], a[160:163], v[48:51]\n"
"s_add_u32 m0, s44, 16384\n"
"ds_read_b128 a[156:159], v247 offset:4096\n"
"v_mfma_f32_16x16x32_bf16 v[48:51], a[228:231], a[164:167], v[48:51]\n"
"buffer_load_dwordx4 v240, s[16:19], s56 offen nt lds\n"
"v_mfma_f32_16x16x32_bf16 v[64:67], a[224:227], a[168:171], v[64:67]\n"
"v_mfma_f32_16x16x32_bf16 v[64:67], a[228:231], a[172:175], v[64:67]\n"
"v_mfma_f32_16x16x32_bf16 v[68:71], a[232:235], a[168:171], v[68:71]\n"
"ds_read_b128 a[164:167], v253 offset:0\n"
"v_mfma_f32_16x16x32_bf16 v[68:71], a[236:239], a[172:175], v[68:71]\n"
"v_mfma_f32_16x16x32_bf16 v[72:75], a[240:243], a[168:171], v[72:75]\n"
"ds_read_b64_tr_b16 a[218:219], v245 offset:1536\n"
"v_mfma_f32_16x16x32_bf16 v[72:75], a[244:247], a[172:175], v[72:75]\n"
"ds_read_b64_tr_b16 a[194:195], v244 offset:512\n"
"v_mfma_f32_16x16x32_bf16 v[76:79], a[248:251], a[168:171], v[76:79]\n"
"s_add_u32 s40, s56, s52\n"
"ds_read_b64_tr_b16 a[206:207], v245 offset:8704\n"
"v_mfma_f32_16x16x32_bf16 v[76:79], a[252:255], a[172:175], v[76:79]\n"
"buffer_load_dwordx4 v248, s[16:19], s40 offen offset:1024 nt lds\n"
"v_mfma_f32_16x16x32_bf16 v[92:95], a[248:251], a[176:179], v[92:95]\n"
"v_mfma_f32_16x16x32_bf16 v[92:95], a[252:255], a[180:183], v[92:95]\n"
"v_mfma_f32_16x16x32_bf16 v[88:91], a[240:243], a[176:179], v[88:91]\n"
"ds_read_b64_tr_b16 a[208:209], v244 offset:1024\n"
"v_mfma_f32_16x16x32_bf16 v[88:91], a[244:247], a[180:183], v[88:91]\n"
"ds_read_b64_tr_b16 a[212:213], v244 offset:9216\n"
"v_mfma_f32_16x16x32_bf16 v[84:87], a[232:235], a[176:179], v[84:87]\n"
"s_add_u32 s40, s56, s53\n"
"ds_read_b64_tr_b16 a[192:193], v244 offset:0\n"
"v_mfma_f32_16x16x32_bf16 v[84:87], a[236:239], a[180:183], v[84:87]\n"
"buffer_load_dwordx4 v240, s[16:19], s40 offen offset:2048 nt lds\n"
"v_mfma_f32_16x16x32_bf16 v[80:83], a[224:227], a[176:179], v[80:83]\n"
"v_mfma_f32_16x16x32_bf16 v[80:83], a[228:231], a[180:183], v[80:83]\n"
"v_mfma_f32_16x16x32_bf16 v[96:99], a[224:227], a[184:187], v[96:99]\n"
"ds_read_b128 a[172:175], v251 offset:0\n"
"v_mfma_f32_16x16x32_bf16 v[96:99], a[228:231], a[188:191], v[96:99]\n"
"v_mfma_f32_16x16x32_bf16 v[100:103], a[232:235], a[184:187], v[100:103]\n"
"s_add_u32 s40, s56, s54\n"
"ds_read_b128 a[148:151], v247 offset:2048\n"
"v_mfma_f32_16x16x32_bf16 v[100:103], a[236:239], a[188:191], v[100:103]\n"
"buffer_load_dwordx4 v248, s[16:19], s40 offen offset:3072 nt lds\n"
"v_mfma_f32_16x16x32_bf16 v[104:107], a[240:243], a[184:187], v[104:107]\n"
"v_mfma_f32_16x16x32_bf16 v[104:107], a[244:247], a[188:191], v[104:107]\n"
"v_and_b32 v254, 0x20, v242\n"
"v_and_b32 v241, 0xaaaaaaaa, v241\n"
"v_mfma_f32_16x16x32_bf16 v[108:111], a[248:251], a[184:187], v[108:111]\n"
"ds_read_b128 a[168:171], v250 offset:0\n"
"v_mfma_f32_16x16x32_bf16 v[108:111], a[252:255], a[188:191], v[108:111]\n"
"s_waitcnt lgkmcnt(3)\n"
"v_mfma_f32_16x16x32_bf16 v[112:115], a[192:195], a[136:139], v[112:115]\n"
"ds_read_b128 a[184:187], v250 offset:4096\n"
"v_mfma_f32_16x16x32_bf16 v[112:115], a[196:199], a[140:143], v[112:115]\n"
"v_mfma_f32_16x16x32_bf16 v[116:119], a[200:203], a[136:139], v[116:119]\n"
"s_waitcnt vmcnt(13)\n"
"ds_read_b128 a[180:183], v251 offset:2048\n"
"v_and_b32 v249, 0xf0, v242\n"
"v_mfma_f32_16x16x32_bf16 v[116:119], a[204:207], a[140:143], v[116:119]\n"
"s_barrier\n"
"v_mfma_f32_16x16x32_bf16 v[120:123], a[208:211], a[136:139], v[120:123]\n"
"s_add_u32 m0, s44, 81920\n"
"ds_read_b128 a[176:179], v250 offset:2048\n"
"v_mfma_f32_16x16x32_bf16 v[120:123], a[212:215], a[140:143], v[120:123]\n"
"buffer_load_dwordx4 v240, s[20:23], s56 offen nt lds\n"
"v_mfma_f32_16x16x32_bf16 v[124:127], a[216:219], a[136:139], v[124:127]\n"
"v_bfe_u32 v255, v242, 9, 1\n"
"v_lshl_add_u32 v255, v255, 8, v249\n"
"v_mfma_f32_16x16x32_bf16 v[124:127], a[220:223], a[140:143], v[124:127]\n"
"s_waitcnt lgkmcnt(3)\n"
"v_xor_b32 v252, s74, v252\n"
"v_mfma_f32_16x16x32_bf16 v[188:191], a[216:219], a[168:171], v[188:191]\n"
"ds_read_b128 a[188:191], v251 offset:4096\n"
"v_xor_b32 v228, v228, v229\n"
"v_mfma_f32_16x16x32_bf16 v[188:191], a[220:223], a[172:175], v[188:191]\n"
"v_mfma_f32_16x16x32_bf16 v[184:187], a[208:211], a[168:171], v[184:187]\n"
"ds_read_b128 a[144:147], v246 offset:2048\n"
"v_add_u32 v254, -1, v254\n"
"v_mfma_f32_16x16x32_bf16 v[184:187], a[212:215], a[172:175], v[184:187]\n"
"v_mfma_f32_16x16x32_bf16 v[180:183], a[200:203], a[168:171], v[180:183]\n"
"ds_read_b64_tr_b16 a[234:235], v245 offset:16896\n"
"v_mfma_f32_16x16x32_bf16 v[180:183], a[204:207], a[172:175], v[180:183]\n"
"v_mfma_f32_16x16x32_bf16 v[176:179], a[192:195], a[168:171], v[176:179]\n"
"ds_read_b128 a[160:163], v252 offset:0\n"
"v_mfma_f32_16x16x32_bf16 v[176:179], a[196:199], a[172:175], v[176:179]\n"
"s_waitcnt lgkmcnt(4)\n"
"v_mfma_f32_16x16x32_bf16 v[192:195], a[192:195], a[176:179], v[192:195]\n"
"ds_read_b64_tr_b16 a[230:231], v244 offset:25088\n"
"v_or_b32 v241, 0x44444444, v241\n"
"v_mfma_f32_16x16x32_bf16 v[192:195], a[196:199], a[180:183], v[192:195]\n"
"v_bfe_u32 v249, v242, 8, 1\n"
"v_mfma_f32_16x16x32_bf16 v[196:199], a[200:203], a[176:179], v[196:199]\n"
"s_add_u32 s40, s56, s52\n"
"ds_read_b64_tr_b16 a[246:247], v244 offset:26112\n"
"v_mfma_f32_16x16x32_bf16 v[196:199], a[204:207], a[180:183], v[196:199]\n"
"buffer_load_dwordx4 v248, s[20:23], s40 offen offset:1024 nt lds\n"
"v_mfma_f32_16x16x32_bf16 v[200:203], a[208:211], a[176:179], v[200:203]\n"
"v_xor_b32 v229, 64, v228\n"
"v_mfma_f32_16x16x32_bf16 v[200:203], a[212:215], a[180:183], v[200:203]\n"
"v_mfma_f32_16x16x32_bf16 v[204:207], a[216:219], a[176:179], v[204:207]\n"
"ds_read_b64_tr_b16 a[240:241], v244 offset:17408\n"
"v_lshl_add_u32 v255, v249, 3, v255\n"
"v_mfma_f32_16x16x32_bf16 v[204:207], a[220:223], a[180:183], v[204:207]\n"
"v_xor_b32 v231, 0x2000, v229\n"
"v_xor_b32_e64 v249, s42, 1\n"
"v_mfma_f32_16x16x32_bf16 v[220:223], a[216:219], a[184:187], v[220:223]\n"
"s_waitcnt lgkmcnt(2)\n"
"ds_read_b64_tr_b16 a[232:233], v245 offset:16384\n"
"v_mfma_f32_16x16x32_bf16 v[220:223], a[220:223], a[188:191], v[220:223]\n"
"ds_read_b64_tr_b16 a[226:227], v244 offset:16896\n"
"v_mfma_f32_16x16x32_bf16 v[216:219], a[208:211], a[184:187], v[216:219]\n"
"ds_read_b64_tr_b16 a[224:225], v244 offset:16384\n"
"v_xor_b32 v230, 0x2000, v228\n"
"v_mfma_f32_16x16x32_bf16 v[216:219], a[212:215], a[188:191], v[216:219]\n"
"v_pk_fma_f32 v[176:177], v[176:177], s[70:71], s[70:71] op_sel:[0,0,1] op_sel_hi:[1,0,1]\nv_pk_fma_f32 v[178:179], v[178:179], s[70:71], s[70:71] op_sel:[0,0,1] op_sel_hi:[1,0,1]\nv_pk_fma_f32 v[180:181], v[180:181], s[70:71], s[70:71] op_sel:[0,0,1] op_sel_hi:[1,0,1]\nv_pk_fma_f32 v[182:183], v[182:183], s[70:71], s[70:71] op_sel:[0,0,1] op_sel_hi:[1,0,1]\nv_pk_fma_f32 v[184:185], v[184:185], s[70:71], s[70:71] op_sel:[0,0,1] op_sel_hi:[1,0,1]\nv_pk_fma_f32 v[186:187], v[186:187], s[70:71], s[70:71] op_sel:[0,0,1] op_sel_hi:[1,0,1]\nv_pk_fma_f32 v[188:189], v[188:189], s[70:71], s[70:71] op_sel:[0,0,1] op_sel_hi:[1,0,1]\nv_pk_fma_f32 v[190:191], v[190:191], s[70:71], s[70:71] op_sel:[0,0,1] op_sel_hi:[1,0,1]\nv_pk_fma_f32 v[192:193], v[192:193], s[70:71], s[70:71] op_sel:[0,0,1] op_sel_hi:[1,0,1]\nv_pk_fma_f32 v[194:195], v[194:195], s[70:71], s[70:71] op_sel:[0,0,1] op_sel_hi:[1,0,1]\nv_pk_fma_f32 v[196:197], v[196:197], s[70:71], s[70:71] op_sel:[0,0,1] op_sel_hi:[1,0,1]\nv_pk_fma_f32 v[198:199], v[198:199], s[70:71], s[70:71] op_sel:[0,0,1] op_sel_hi:[1,0,1]\nv_pk_fma_f32 v[200:201], v[200:201], s[70:71], s[70:71] op_sel:[0,0,1] op_sel_hi:[1,0,1]\nv_pk_fma_f32 v[202:203], v[202:203], s[70:71], s[70:71] op_sel:[0,0,1] op_sel_hi:[1,0,1]\nv_pk_fma_f32 v[204:205], v[204:205], s[70:71], s[70:71] op_sel:[0,0,1] op_sel_hi:[1,0,1]\nv_pk_fma_f32 v[206:207], v[206:207], s[70:71], s[70:71] op_sel:[0,0,1] op_sel_hi:[1,0,1]\n"
"v_mfma_f32_16x16x32_bf16 v[212:215], a[200:203], a[184:187], v[212:215]\n"
"ds_read_b64_tr_b16 a[252:253], v245 offset:25600\n"
"v_cvt_pk_bf16_f32 v192, v192, v193\n"
"v_mfma_f32_16x16x32_bf16 v[212:215], a[204:207], a[188:191], v[212:215]\n"
"v_cvt_pk_bf16_f32 v176, v176, v177\n"
"v_cvt_pk_bf16_f32 v193, v194, v195\n"
"v_mfma_f32_16x16x32_bf16 v[208:211], a[192:195], a[184:187], v[208:211]\n"
"s_add_u32 s40, s56, s53\n"
"ds_read_b64_tr_b16 a[248:249], v245 offset:17408\n"
"v_mfma_f32_16x16x32_bf16 v[208:211], a[196:199], a[188:191], v[208:211]\n"
"buffer_load_dwordx4 v240, s[20:23], s40 offen offset:2048 nt lds\n"
"v_mfma_f32_16x16x32_bf16 v[160:163], a[192:195], a[160:163], v[160:163]\n"
"v_cvt_pk_bf16_f32 v177, v178, v179\n"
"v_cvt_pk_bf16_f32 v184, v184, v185\n"
"v_mfma_f32_16x16x32_bf16 v[160:163], a[196:199], a[164:167], v[160:163]\n"
"v_cvt_pk_bf16_f32 v179, v182, v183\n"
"v_cvt_pk_bf16_f32 v185, v186, v187\n"
"v_mfma_f32_16x16x32_bf16 v[164:167], a[200:203], a[160:163], v[164:167]\n"
"ds_read_b64_tr_b16 a[244:245], v244 offset:25600\n"
"v_cvt_pk_bf16_f32 v200, v200, v201\n"
"v_mfma_f32_16x16x32_bf16 v[164:167], a[204:207], a[164:167], v[164:167]\n"
"v_cvt_pk_bf16_f32 v201, v202, v203\n"
"v_ashrrev_i32 v254, 31, v254\n"
"v_mfma_f32_16x16x32_bf16 v[168:171], a[208:211], a[160:163], v[168:171]\n"
"ds_read_b64_tr_b16 a[254:255], v245 offset:26112\n"
"v_cvt_pk_bf16_f32 v194, v196, v197\n"
"v_mfma_f32_16x16x32_bf16 v[168:171], a[212:215], a[164:167], v[168:171]\n"
"v_cvt_pk_bf16_f32 v202, v204, v205\n"
"v_mfma_f32_16x16x32_bf16 v[172:175], a[216:219], a[160:163], v[172:175]\n"
"ds_read_b64_tr_b16 a[242:243], v244 offset:17920\n"
"v_and_b32 v254, 0x3f803f80, v254\n"
"v_mfma_f32_16x16x32_bf16 v[172:175], a[220:223], a[164:167], v[172:175]\n"
"v_bfe_u32 v196, v242, 8, 2\n"
"v_lshl_add_u32 v196, v196, 4, s43\n"
"v_mfma_f32_16x16x32_bf16 v[140:143], a[216:219], a[144:147], v[140:143]\n"
"ds_read_b64_tr_b16 a[236:237], v245 offset:24576\n"
"v_lshl_add_u32 v255, v249, 12, v255\n"
"v_mfma_f32_16x16x32_bf16 v[140:143], a[220:223], a[148:151], v[140:143]\n"
"v_cvt_pk_bf16_f32 v203, v206, v207\n"
"v_mfma_f32_16x16x32_bf16 v[136:139], a[208:211], a[144:147], v[136:139]\n"
"ds_read_b128 a[152:155], v246 offset:4096\n"
"v_cvt_pk_bf16_f32 v178, v180, v181\n"
"v_mfma_f32_16x16x32_bf16 v[136:139], a[212:215], a[148:151], v[136:139]\n"
"v_pk_fma_f32 v[208:209], v[208:209], s[70:71], s[70:71] op_sel:[0,0,1] op_sel_hi:[1,0,1]\nv_pk_fma_f32 v[210:211], v[210:211], s[70:71], s[70:71] op_sel:[0,0,1] op_sel_hi:[1,0,1]\nv_pk_fma_f32 v[212:213], v[212:213], s[70:71], s[70:71] op_sel:[0,0,1] op_sel_hi:[1,0,1]\nv_pk_fma_f32 v[214:215], v[214:215], s[70:71], s[70:71] op_sel:[0,0,1] op_sel_hi:[1,0,1]\nv_pk_fma_f32 v[216:217], v[216:217], s[70:71], s[70:71] op_sel:[0,0,1] op_sel_hi:[1,0,1]\nv_pk_fma_f32 v[218:219], v[218:219], s[70:71], s[70:71] op_sel:[0,0,1] op_sel_hi:[1,0,1]\nv_pk_fma_f32 v[220:221], v[220:221], s[70:71], s[70:71] op_sel:[0,0,1] op_sel_hi:[1,0,1]\nv_pk_fma_f32 v[222:223], v[222:223], s[70:71], s[70:71] op_sel:[0,0,1] op_sel_hi:[1,0,1]\nv_pk_fma_f32 v[160:161], v[160:161], s[70:71], s[70:71] op_sel:[0,0,1] op_sel_hi:[1,0,1]\nv_pk_fma_f32 v[162:163], v[162:163], s[70:71], s[70:71] op_sel:[0,0,1] op_sel_hi:[1,0,1]\nv_pk_fma_f32 v[164:165], v[164:165], s[70:71], s[70:71] op_sel:[0,0,1] op_sel_hi:[1,0,1]\nv_pk_fma_f32 v[166:167], v[166:167], s[70:71], s[70:71] op_sel:[0,0,1] op_sel_hi:[1,0,1]\nv_pk_fma_f32 v[168:169], v[168:169], s[70:71], s[70:71] op_sel:[0,0,1] op_sel_hi:[1,0,1]\nv_pk_fma_f32 v[170:171], v[170:171], s[70:71], s[70:71] op_sel:[0,0,1] op_sel_hi:[1,0,1]\nv_pk_fma_f32 v[172:173], v[172:173], s[70:71], s[70:71] op_sel:[0,0,1] op_sel_hi:[1,0,1]\nv_pk_fma_f32 v[174:175], v[174:175], s[70:71], s[70:71] op_sel:[0,0,1] op_sel_hi:[1,0,1]\n"
"v_mfma_f32_16x16x32_bf16 v[132:135], a[200:203], a[144:147], v[132:135]\n"
"s_add_u32 s40, s56, s54\n"
"ds_read_b64_tr_b16 a[250:251], v245 offset:17920\n"
"v_cvt_pk_bf16_f32 v160, v160, v161\n"
"v_mfma_f32_16x16x32_bf16 v[132:135], a[204:207], a[148:151], v[132:135]\n"
"buffer_load_dwordx4 v248, s[20:23], s40 offen offset:3072 nt lds\n"
"v_mfma_f32_16x16x32_bf16 v[128:131], a[192:195], a[144:147], v[128:131]\n"
"s_add_u32 s55, s55, s59\n"
"v_cvt_pk_bf16_f32 v186, v188, v189\n"
"v_cvt_pk_bf16_f32 v216, v216, v217\n"
"v_mfma_f32_16x16x32_bf16 v[128:131], a[196:199], a[148:151], v[128:131]\n"
"s_waitcnt lgkmcnt(1)\n"
"s_or_b32 s55, s55, s58\n"
"v_cvt_pk_bf16_f32 v208, v208, v209\n"
"v_cvt_pk_bf16_f32 v161, v162, v163\n"
"v_mfma_f32_16x16x32_bf16 v[144:147], a[192:195], a[152:155], v[144:147]\n"
"s_xor_b32 s56, s55, s57\n"
"ds_read_b64_tr_b16 a[238:239], v245 offset:25088\n"
"v_cvt_pk_bf16_f32 v187, v190, v191\n"
"v_mfma_f32_16x16x32_bf16 v[144:147], a[196:199], a[156:159], v[144:147]\n"
"v_cvt_pk_bf16_f32 v217, v218, v219\n"
"v_cvt_pk_bf16_f32 v168, v168, v169\n"
"v_mfma_f32_16x16x32_bf16 v[148:151], a[200:203], a[152:155], v[148:151]\n"
"v_cvt_pk_bf16_f32 v195, v198, v199\n"
"v_xor_b32 v198, 0x400, v243\n"
"v_mfma_f32_16x16x32_bf16 v[148:151], a[204:207], a[156:159], v[148:151]\n"
"ds_read_b64_tr_b16 a[228:229], v244 offset:24576\n"
"v_cvt_pk_bf16_f32 v169, v170, v171\n"
"v_mfma_f32_16x16x32_bf16 v[152:155], a[208:211], a[152:155], v[152:155]\n"
"v_cvt_pk_bf16_f32 v163, v166, v167\n"
"v_cvt_pk_bf16_f32 v171, v174, v175\n"
"v_mfma_f32_16x16x32_bf16 v[152:155], a[212:215], a[156:159], v[152:155]\n"
"v_pk_fma_f32 v[112:113], v[112:113], s[70:71], s[70:71] op_sel:[0,0,1] op_sel_hi:[1,0,1]\nv_pk_fma_f32 v[114:115], v[114:115], s[70:71], s[70:71] op_sel:[0,0,1] op_sel_hi:[1,0,1]\nv_pk_fma_f32 v[116:117], v[116:117], s[70:71], s[70:71] op_sel:[0,0,1] op_sel_hi:[1,0,1]\nv_pk_fma_f32 v[118:119], v[118:119], s[70:71], s[70:71] op_sel:[0,0,1] op_sel_hi:[1,0,1]\nv_pk_fma_f32 v[120:121], v[120:121], s[70:71], s[70:71] op_sel:[0,0,1] op_sel_hi:[1,0,1]\nv_pk_fma_f32 v[122:123], v[122:123], s[70:71], s[70:71] op_sel:[0,0,1] op_sel_hi:[1,0,1]\nv_pk_fma_f32 v[124:125], v[124:125], s[70:71], s[70:71] op_sel:[0,0,1] op_sel_hi:[1,0,1]\nv_pk_fma_f32 v[126:127], v[126:127], s[70:71], s[70:71] op_sel:[0,0,1] op_sel_hi:[1,0,1]\nv_pk_fma_f32 v[128:129], v[128:129], s[70:71], s[70:71] op_sel:[0,0,1] op_sel_hi:[1,0,1]\nv_pk_fma_f32 v[130:131], v[130:131], s[70:71], s[70:71] op_sel:[0,0,1] op_sel_hi:[1,0,1]\nv_pk_fma_f32 v[132:133], v[132:133], s[70:71], s[70:71] op_sel:[0,0,1] op_sel_hi:[1,0,1]\nv_pk_fma_f32 v[134:135], v[134:135], s[70:71], s[70:71] op_sel:[0,0,1] op_sel_hi:[1,0,1]\nv_pk_fma_f32 v[136:137], v[136:137], s[70:71], s[70:71] op_sel:[0,0,1] op_sel_hi:[1,0,1]\nv_pk_fma_f32 v[138:139], v[138:139], s[70:71], s[70:71] op_sel:[0,0,1] op_sel_hi:[1,0,1]\nv_pk_fma_f32 v[140:141], v[140:141], s[70:71], s[70:71] op_sel:[0,0,1] op_sel_hi:[1,0,1]\nv_pk_fma_f32 v[142:143], v[142:143], s[70:71], s[70:71] op_sel:[0,0,1] op_sel_hi:[1,0,1]\n"
"v_mfma_f32_16x16x32_bf16 v[156:159], a[216:219], a[152:155], v[156:159]\n"
"v_cvt_pk_bf16_f32 v136, v136, v137\n"
"v_cvt_pk_bf16_f32 v137, v138, v139\n"
"v_mfma_f32_16x16x32_bf16 v[156:159], a[220:223], a[156:159], v[156:159]\n"
"v_xor_b32 v197, 64, v196\n"
"v_cvt_pk_bf16_f32 v120, v120, v121\n"
"v_mfma_f32_16x16x32_bf16 v[96:99], a[224:227], a[184:187], v[96:99]\n"
"s_waitcnt lgkmcnt(0)\n"
"s_add_u32 m0, s44, 32768\n"
"v_cvt_pk_bf16_f32 v209, v210, v211\n"
"v_cvt_pk_bf16_f32 v210, v212, v213\n"
"v_mfma_f32_16x16x32_bf16 v[96:99], a[228:231], a[188:191], v[96:99]\n"
"buffer_load_dwordx4 v240, s[16:19], s56 offen nt lds\n"
"v_mfma_f32_16x16x32_bf16 v[100:103], a[232:235], a[184:187], v[100:103]\n"
"s_waitcnt vmcnt(14)\n"
"v_cvt_pk_bf16_f32 v128, v128, v129\n"
"v_cvt_pk_bf16_f32 v129, v130, v131\n"
"v_mfma_f32_16x16x32_bf16 v[100:103], a[236:239], a[188:191], v[100:103]\n"
"s_barrier\n"
"v_mfma_f32_16x16x32_bf16 v[104:107], a[240:243], a[184:187], v[104:107]\n"
"ds_read_b128 a[196:199], v198 offset:0\n"
"v_cvt_pk_bf16_f32 v211, v214, v215\n"
"v_mfma_f32_16x16x32_bf16 v[104:107], a[244:247], a[188:191], v[104:107]\n"
"s_mov_b32 s74, 0x41400000\n"
"v_cvt_pk_bf16_f32 v138, v140, v141\n"
"v_cvt_pk_bf16_f32 v139, v142, v143\n"
"v_mfma_f32_16x16x32_bf16 v[108:111], a[248:251], a[184:187], v[108:111]\n"
"ds_read_b128 a[200:203], v243 offset:2048\n"
"v_cvt_pk_bf16_f32 v162, v164, v165\n"
"v_mfma_f32_16x16x32_bf16 v[108:111], a[252:255], a[188:191], v[108:111]\n"
"v_pk_fma_f32 v[144:145], v[144:145], s[70:71], s[70:71] op_sel:[0,0,1] op_sel_hi:[1,0,1]\nv_pk_fma_f32 v[146:147], v[146:147], s[70:71], s[70:71] op_sel:[0,0,1] op_sel_hi:[1,0,1]\nv_pk_fma_f32 v[148:149], v[148:149], s[70:71], s[70:71] op_sel:[0,0,1] op_sel_hi:[1,0,1]\nv_pk_fma_f32 v[150:151], v[150:151], s[70:71], s[70:71] op_sel:[0,0,1] op_sel_hi:[1,0,1]\nv_pk_fma_f32 v[152:153], v[152:153], s[70:71], s[70:71] op_sel:[0,0,1] op_sel_hi:[1,0,1]\nv_pk_fma_f32 v[154:155], v[154:155], s[70:71], s[70:71] op_sel:[0,0,1] op_sel_hi:[1,0,1]\nv_pk_fma_f32 v[156:157], v[156:157], s[70:71], s[70:71] op_sel:[0,0,1] op_sel_hi:[1,0,1]\nv_pk_fma_f32 v[158:159], v[158:159], s[70:71], s[70:71] op_sel:[0,0,1] op_sel_hi:[1,0,1]\n"
"v_mfma_f32_16x16x32_bf16 v[92:95], a[248:251], a[176:179], v[92:95]\n"
"v_permlane16_swap_b32 v161, v163\n"
"v_mfma_f32_16x16x32_bf16 v[92:95], a[252:255], a[180:183], v[92:95]\n"
"ds_write2st64_b64 v255, v[200:201], v[202:203] offset0:102 offset1:103\n"
"v_mfma_f32_16x16x32_bf16 v[88:91], a[240:243], a[176:179], v[88:91]\n"
"v_cvt_pk_bf16_f32 v144, v144, v145\n"
"v_cvt_pk_bf16_f32 v152, v152, v153\n"
"v_mfma_f32_16x16x32_bf16 v[88:91], a[244:247], a[180:183], v[88:91]\n"
"v_cvt_pk_bf16_f32 v145, v146, v147\n"
"v_cvt_pk_bf16_f32 v153, v154, v155\n"
"v_mfma_f32_16x16x32_bf16 v[84:87], a[232:235], a[176:179], v[84:87]\n"
"v_cvt_pk_bf16_f32 v130, v132, v133\n"
"v_cvt_pk_bf16_f32 v131, v134, v135\n"
"v_mfma_f32_16x16x32_bf16 v[84:87], a[236:239], a[180:183], v[84:87]\n"
"ds_write2st64_b64 v255, v[176:177], v[178:179] offset0:96 offset1:97\n"
"v_mfma_f32_16x16x32_bf16 v[80:83], a[224:227], a[176:179], v[80:83]\n"
"s_add_u32 s40, s56, s52\n"
"v_permlane16_swap_b32 v160, v162\n"
"v_mfma_f32_16x16x32_bf16 v[80:83], a[228:231], a[180:183], v[80:83]\n"
"buffer_load_dwordx4 v248, s[16:19], s40 offen offset:1024 nt lds\n"
"v_mfma_f32_16x16x32_bf16 v[64:67], a[224:227], a[168:171], v[64:67]\n"
"v_cvt_pk_bf16_f32 v218, v220, v221\n"
"v_cvt_pk_bf16_f32 v154, v156, v157\n"
"v_mfma_f32_16x16x32_bf16 v[64:67], a[228:231], a[172:175], v[64:67]\n"
"v_permlane16_swap_b32 v128, v130\n"
"v_mfma_f32_16x16x32_bf16 v[68:71], a[232:235], a[168:171], v[68:71]\n"
"ds_read_b128 v[212:215], v197 offset:0\n"
"v_cvt_pk_bf16_f32 v155, v158, v159\n"
"v_mfma_f32_16x16x32_bf16 v[68:71], a[236:239], a[172:175], v[68:71]\n"
"v_pk_fma_f32 v[96:97], v[96:97], s[70:71], s[70:71] op_sel:[0,0,1] op_sel_hi:[1,0,1]\nv_pk_fma_f32 v[98:99], v[98:99], s[70:71], s[70:71] op_sel:[0,0,1] op_sel_hi:[1,0,1]\nv_pk_fma_f32 v[100:101], v[100:101], s[70:71], s[70:71] op_sel:[0,0,1] op_sel_hi:[1,0,1]\nv_pk_fma_f32 v[102:103], v[102:103], s[70:71], s[70:71] op_sel:[0,0,1] op_sel_hi:[1,0,1]\nv_pk_fma_f32 v[104:105], v[104:105], s[70:71], s[70:71] op_sel:[0,0,1] op_sel_hi:[1,0,1]\nv_pk_fma_f32 v[106:107], v[106:107], s[70:71], s[70:71] op_sel:[0,0,1] op_sel_hi:[1,0,1]\nv_pk_fma_f32 v[108:109], v[108:109], s[70:71], s[70:71] op_sel:[0,0,1] op_sel_hi:[1,0,1]\nv_pk_fma_f32 v[110:111], v[110:111], s[70:71], s[70:71] op_sel:[0,0,1] op_sel_hi:[1,0,1]\nv_pk_fma_f32 v[80:81], v[80:81], s[70:71], s[70:71] op_sel:[0,0,1] op_sel_hi:[1,0,1]\nv_pk_fma_f32 v[82:83], v[82:83], s[70:71], s[70:71] op_sel:[0,0,1] op_sel_hi:[1,0,1]\nv_pk_fma_f32 v[84:85], v[84:85], s[70:71], s[70:71] op_sel:[0,0,1] op_sel_hi:[1,0,1]\nv_pk_fma_f32 v[86:87], v[86:87], s[70:71], s[70:71] op_sel:[0,0,1] op_sel_hi:[1,0,1]\nv_pk_fma_f32 v[88:89], v[88:89], s[70:71], s[70:71] op_sel:[0,0,1] op_sel_hi:[1,0,1]\nv_pk_fma_f32 v[90:91], v[90:91], s[70:71], s[70:71] op_sel:[0,0,1] op_sel_hi:[1,0,1]\nv_pk_fma_f32 v[92:93], v[92:93], s[70:71], s[70:71] op_sel:[0,0,1] op_sel_hi:[1,0,1]\nv_pk_fma_f32 v[94:95], v[94:95], s[70:71], s[70:71] op_sel:[0,0,1] op_sel_hi:[1,0,1]\n"
"v_mfma_f32_16x16x32_bf16 v[72:75], a[240:243], a[168:171], v[72:75]\n"
"v_permlane16_swap_b32 v137, v139\n"
"v_mfma_f32_16x16x32_bf16 v[72:75], a[244:247], a[172:175], v[72:75]\n"
"ds_write2st64_b64 v255, v[184:185], v[186:187] offset0:98 offset1:99\n"
"v_mfma_f32_16x16x32_bf16 v[76:79], a[248:251], a[168:171], v[76:79]\n"
"v_cvt_pk_bf16_f32 v121, v122, v123\n"
"v_cvt_pk_bf16_f32 v96, v96, v97\n"
"v_mfma_f32_16x16x32_bf16 v[76:79], a[252:255], a[172:175], v[76:79]\n"
"ds_read_b128 a[192:195], v243 offset:0\n"
"v_mov_b32 v199, 0\n"
"v_mfma_f32_16x16x32_bf16 v[60:63], a[248:251], a[160:163], v[60:63]\n"
"v_cvt_pk_bf16_f32 v104, v104, v105\n"
"v_cvt_pk_bf16_f32 v105, v106, v107\n"
"v_mfma_f32_16x16x32_bf16 v[60:63], a[252:255], a[164:167], v[60:63]\n"
"ds_write2st64_b64 v255, v[208:209], v[210:211] offset0:192 offset1:193\n"
"v_mfma_f32_16x16x32_bf16 v[56:59], a[240:243], a[160:163], v[56:59]\n"
"s_add_u32 s40, s56, s53\n"
"v_accvgpr_write_b32 a121, v137\n"
"v_cvt_pk_bf16_f32 v146, v148, v149\n"
"v_mfma_f32_16x16x32_bf16 v[56:59], a[244:247], a[164:167], v[56:59]\n"
"buffer_load_dwordx4 v240, s[16:19], s40 offen offset:2048 nt lds\n"
"v_mfma_f32_16x16x32_bf16 v[52:55], a[232:235], a[160:163], v[52:55]\n"
"v_cvt_pk_bf16_f32 v97, v98, v99\n"
"v_cvt_pk_bf16_f32 v88, v88, v89\n"
"v_mfma_f32_16x16x32_bf16 v[52:55], a[236:239], a[164:167], v[52:55]\n"
"v_cvt_pk_bf16_f32 v106, v108, v109\n"
"v_mov_b32 v204, 0\n"
"v_mfma_f32_16x16x32_bf16 v[48:51], a[224:227], a[160:163], v[48:51]\n"
"v_cvt_pk_bf16_f32 v219, v222, v223\n"
"v_mov_b32 v205, 0\n"
"v_mfma_f32_16x16x32_bf16 v[48:51], a[228:231], a[164:167], v[48:51]\n"
"ds_write_b128 v243, v[160:163] offset:8192\n"
"v_cvt_pk_bf16_f32 v170, v172, v173\n"
"v_mfma_f32_16x16x32_bf16 v[0:3], a[224:227], a[136:139], v[0:3]\n"
"v_permlane16_swap_b32 v153, v155\n"
"v_mfma_f32_16x16x32_bf16 v[0:3], a[228:231], a[140:143], v[0:3]\n"
"ds_write2st64_b64 v255, v[216:217], v[218:219] offset0:194 offset1:195\n"
"v_mfma_f32_16x16x32_bf16 v[4:7], a[232:235], a[136:139], v[4:7]\n"
"v_permlane16_swap_b32 v169, v171\n"
"v_mfma_f32_16x16x32_bf16 v[4:7], a[236:239], a[140:143], v[4:7]\n"
"v_pk_fma_f32 v[64:65], v[64:65], s[70:71], s[70:71] op_sel:[0,0,1] op_sel_hi:[1,0,1]\nv_pk_fma_f32 v[66:67], v[66:67], s[70:71], s[70:71] op_sel:[0,0,1] op_sel_hi:[1,0,1]\nv_pk_fma_f32 v[68:69], v[68:69], s[70:71], s[70:71] op_sel:[0,0,1] op_sel_hi:[1,0,1]\nv_pk_fma_f32 v[70:71], v[70:71], s[70:71], s[70:71] op_sel:[0,0,1] op_sel_hi:[1,0,1]\nv_pk_fma_f32 v[72:73], v[72:73], s[70:71], s[70:71] op_sel:[0,0,1] op_sel_hi:[1,0,1]\nv_pk_fma_f32 v[74:75], v[74:75], s[70:71], s[70:71] op_sel:[0,0,1] op_sel_hi:[1,0,1]\nv_pk_fma_f32 v[76:77], v[76:77], s[70:71], s[70:71] op_sel:[0,0,1] op_sel_hi:[1,0,1]\nv_pk_fma_f32 v[78:79], v[78:79], s[70:71], s[70:71] op_sel:[0,0,1] op_sel_hi:[1,0,1]\nv_pk_fma_f32 v[48:49], v[48:49], s[70:71], s[70:71] op_sel:[0,0,1] op_sel_hi:[1,0,1]\nv_pk_fma_f32 v[50:51], v[50:51], s[70:71], s[70:71] op_sel:[0,0,1] op_sel_hi:[1,0,1]\nv_pk_fma_f32 v[52:53], v[52:53], s[70:71], s[70:71] op_sel:[0,0,1] op_sel_hi:[1,0,1]\nv_pk_fma_f32 v[54:55], v[54:55], s[70:71], s[70:71] op_sel:[0,0,1] op_sel_hi:[1,0,1]\nv_pk_fma_f32 v[56:57], v[56:57], s[70:71], s[70:71] op_sel:[0,0,1] op_sel_hi:[1,0,1]\nv_pk_fma_f32 v[58:59], v[58:59], s[70:71], s[70:71] op_sel:[0,0,1] op_sel_hi:[1,0,1]\nv_pk_fma_f32 v[60:61], v[60:61], s[70:71], s[70:71] op_sel:[0,0,1] op_sel_hi:[1,0,1]\nv_pk_fma_f32 v[62:63], v[62:63], s[70:71], s[70:71] op_sel:[0,0,1] op_sel_hi:[1,0,1]\n"
"v_mfma_f32_16x16x32_bf16 v[8:11], a[240:243], a[136:139], v[8:11]\n"
"ds_read_b128 a[204:207], v198 offset:2048\n"
"v_cvt_pk_bf16_f32 v56, v56, v57\n"
"v_mfma_f32_16x16x32_bf16 v[8:11], a[244:247], a[140:143], v[8:11]\n"
"v_permlane16_swap_b32 v152, v154\n"
"v_mfma_f32_16x16x32_bf16 v[12:15], a[248:251], a[136:139], v[12:15]\n"
"v_permlane16_swap_b32 v144, v146\n"
"v_mfma_f32_16x16x32_bf16 v[12:15], a[252:255], a[140:143], v[12:15]\n"
"v_accvgpr_write_b32 a137, v153\n"
"v_cvt_pk_bf16_f32 v80, v80, v81\n"
"v_mfma_f32_16x16x32_bf16 v[28:31], a[248:251], a[144:147], v[28:31]\n"
"s_add_u32 s40, s56, s54\n"
"ds_read_b128 v[208:211], v196 offset:0\n"
"v_cvt_pk_bf16_f32 v147, v150, v151\n"
"v_mfma_f32_16x16x32_bf16 v[28:31], a[252:255], a[148:151], v[28:31]\n"
"buffer_load_dwordx4 v248, s[16:19], s40 offen offset:3072 nt lds\n"
"v_mfma_f32_16x16x32_bf16 v[24:27], a[240:243], a[144:147], v[24:27]\n"
"v_cvt_pk_bf16_f32 v107, v110, v111\n"
"v_cvt_pk_bf16_f32 v48, v48, v49\n"
"v_mfma_f32_16x16x32_bf16 v[24:27], a[244:247], a[148:151], v[24:27]\n"
"v_permlane16_swap_b32 v129, v131\n"
"v_mfma_f32_16x16x32_bf16 v[20:23], a[232:235], a[144:147], v[20:23]\n"
"v_permlane16_swap_b32 v136, v138\n"
"v_mfma_f32_16x16x32_bf16 v[20:23], a[236:239], a[148:151], v[20:23]\n"
"ds_read_b128 v[216:219], v196 offset:512\n"
"v_cvt_pk_bf16_f32 v64, v64, v65\n"
"v_mfma_f32_16x16x32_bf16 v[16:19], a[224:227], a[144:147], v[16:19]\n"
"v_cvt_pk_bf16_f32 v122, v124, v125\n"
"v_cvt_pk_bf16_f32 v72, v72, v73\n"
"v_mfma_f32_16x16x32_bf16 v[16:19], a[228:231], a[148:151], v[16:19]\n"
"v_accvgpr_write_b32 a113, v129\n"
"v_accvgpr_write_b32 a122, v138\n"
"v_mfma_f32_16x16x32_bf16 v[32:35], a[224:227], a[152:155], v[32:35]\n"
"v_cvt_pk_bf16_f32 v98, v100, v101\n"
"v_cvt_pk_bf16_f32 v65, v66, v67\n"
"v_mfma_f32_16x16x32_bf16 v[32:35], a[228:231], a[156:159], v[32:35]\n"
"ds_read_b128 v[220:223], v197 offset:512\n"
"v_accvgpr_write_b32 a138, v154\n"
"v_mfma_f32_16x16x32_bf16 v[36:39], a[232:235], a[152:155], v[36:39]\n"
"v_accvgpr_write_b32 a136, v152\n"
"v_cvt_pk_bf16_f32 v89, v90, v91\n"
"v_mfma_f32_16x16x32_bf16 v[36:39], a[236:239], a[156:159], v[36:39]\n"
"v_permlane16_swap_b32 v145, v147\n"
"v_mfma_f32_16x16x32_bf16 v[40:43], a[240:243], a[152:155], v[40:43]\n"
"v_accvgpr_write_b32 a128, v144\n"
"v_cvt_pk_bf16_f32 v99, v102, v103\n"
"v_mfma_f32_16x16x32_bf16 v[40:43], a[244:247], a[156:159], v[40:43]\n"
"ds_write2st64_b64 v255, v[192:193], v[194:195] offset0:100 offset1:101\n"
"v_pk_fma_f32 v[0:1], v[0:1], s[70:71], s[70:71] op_sel:[0,0,1] op_sel_hi:[1,0,1]\nv_pk_fma_f32 v[2:3], v[2:3], s[70:71], s[70:71] op_sel:[0,0,1] op_sel_hi:[1,0,1]\nv_pk_fma_f32 v[4:5], v[4:5], s[70:71], s[70:71] op_sel:[0,0,1] op_sel_hi:[1,0,1]\nv_pk_fma_f32 v[6:7], v[6:7], s[70:71], s[70:71] op_sel:[0,0,1] op_sel_hi:[1,0,1]\nv_pk_fma_f32 v[8:9], v[8:9], s[70:71], s[70:71] op_sel:[0,0,1] op_sel_hi:[1,0,1]\nv_pk_fma_f32 v[10:11], v[10:11], s[70:71], s[70:71] op_sel:[0,0,1] op_sel_hi:[1,0,1]\nv_pk_fma_f32 v[12:13], v[12:13], s[70:71], s[70:71] op_sel:[0,0,1] op_sel_hi:[1,0,1]\nv_pk_fma_f32 v[14:15], v[14:15], s[70:71], s[70:71] op_sel:[0,0,1] op_sel_hi:[1,0,1]\nv_pk_fma_f32 v[16:17], v[16:17], s[70:71], s[70:71] op_sel:[0,0,1] op_sel_hi:[1,0,1]\nv_pk_fma_f32 v[18:19], v[18:19], s[70:71], s[70:71] op_sel:[0,0,1] op_sel_hi:[1,0,1]\nv_pk_fma_f32 v[20:21], v[20:21], s[70:71], s[70:71] op_sel:[0,0,1] op_sel_hi:[1,0,1]\nv_pk_fma_f32 v[22:23], v[22:23], s[70:71], s[70:71] op_sel:[0,0,1] op_sel_hi:[1,0,1]\nv_pk_fma_f32 v[24:25], v[24:25], s[70:71], s[70:71] op_sel:[0,0,1] op_sel_hi:[1,0,1]\nv_pk_fma_f32 v[26:27], v[26:27], s[70:71], s[70:71] op_sel:[0,0,1] op_sel_hi:[1,0,1]\nv_pk_fma_f32 v[28:29], v[28:29], s[70:71], s[70:71] op_sel:[0,0,1] op_sel_hi:[1,0,1]\nv_pk_fma_f32 v[30:31], v[30:31], s[70:71], s[70:71] op_sel:[0,0,1] op_sel_hi:[1,0,1]\n"
"v_mfma_f32_16x16x32_bf16 v[44:47], a[248:251], a[152:155], v[44:47]\n"
"v_accvgpr_write_b32 a115, v131\n"
"v_accvgpr_write_b32 a120, v136\n"
"v_mfma_f32_16x16x32_bf16 v[44:47], a[252:255], a[156:159], v[44:47]\n"
"s_waitcnt lgkmcnt(2)\n"
"ds_write2st64_b64 v255, v[96:97], v[98:99] offset0:228 offset1:229\n"
"v_cvt_pk_bf16_f32 v81, v82, v83\n"
"v_mfma_f32_16x16x32_bf16 v[180:183], v[208:211], a[0:3], 0\n"
"v_accvgpr_write_b32 a112, v128\n"
"v_cvt_pk_bf16_f32 v91, v94, v95\n"
"v_mfma_f32_16x16x32_bf16 v[180:183], v[212:215], a[4:7], v[180:183]\n"
"v_permlane16_swap_b32 v168, v170\n"
"v_mfma_f32_16x16x32_bf16 v[180:183], v[216:219], a[8:11], v[180:183]\n"
"s_waitcnt lgkmcnt(2)\n"
"v_cvt_pk_bf16_f32 v73, v74, v75\n"
"v_cvt_pk_bf16_f32 v8, v8, v9\n"
"v_mfma_f32_16x16x32_bf16 v[180:183], v[220:223], a[12:15], v[180:183]\n"
"ds_write_b128 v243, v[168:171] offset:10240\n"
"v_pk_fma_f32 v[32:33], v[32:33], s[70:71], s[70:71] op_sel:[0,0,1] op_sel_hi:[1,0,1]\nv_pk_fma_f32 v[34:35], v[34:35], s[70:71], s[70:71] op_sel:[0,0,1] op_sel_hi:[1,0,1]\nv_pk_fma_f32 v[36:37], v[36:37], s[70:71], s[70:71] op_sel:[0,0,1] op_sel_hi:[1,0,1]\nv_pk_fma_f32 v[38:39], v[38:39], s[70:71], s[70:71] op_sel:[0,0,1] op_sel_hi:[1,0,1]\nv_pk_fma_f32 v[40:41], v[40:41], s[70:71], s[70:71] op_sel:[0,0,1] op_sel_hi:[1,0,1]\nv_pk_fma_f32 v[42:43], v[42:43], s[70:71], s[70:71] op_sel:[0,0,1] op_sel_hi:[1,0,1]\nv_pk_fma_f32 v[44:45], v[44:45], s[70:71], s[70:71] op_sel:[0,0,1] op_sel_hi:[1,0,1]\nv_pk_fma_f32 v[46:47], v[46:47], s[70:71], s[70:71] op_sel:[0,0,1] op_sel_hi:[1,0,1]\n"
"v_mfma_f32_16x16x32_bf16 v[184:187], v[208:211], a[16:19], 0\n"
"v_cvt_pk_bf16_f32 v49, v50, v51\n"
"v_cvt_pk_bf16_f32 v51, v54, v55\n"
"v_mfma_f32_16x16x32_bf16 v[184:187], v[212:215], a[20:23], v[184:187]\n"
"v_permlane16_swap_b32 v120, v122\n"
"v_mfma_f32_16x16x32_bf16 v[184:187], v[216:219], a[24:27], v[184:187]\n"
"v_cvt_pk_bf16_f32 v90, v92, v93\n"
"v_sub_f32 v183, s74, v183\n"
"v_mfma_f32_16x16x32_bf16 v[184:187], v[220:223], a[28:31], v[184:187]\n"
"ds_write2st64_b64 v255, v[104:105], v[106:107] offset0:230 offset1:231\n"
"v_cvt_pk_bf16_f32 v67, v70, v71\n"
"v_sub_f32 v182, s74, v182\n"
"v_mfma_f32_16x16x32_bf16 v[188:191], v[208:211], a[32:35], 0\n"
"v_cvt_pk_bf16_f32 v40, v40, v41\n"
"v_sub_f32 v180, s74, v180\n"
"v_mfma_f32_16x16x32_bf16 v[188:191], v[212:215], a[36:39], v[188:191]\n"
"v_accvgpr_write_b32 a139, v155\n"
"v_sub_f32 v181, s74, v181\n"
"v_mfma_f32_16x16x32_bf16 v[188:191], v[216:219], a[40:43], v[188:191]\n"
"v_cvt_pk_bf16_f32 v9, v10, v11\n"
"v_sub_f32 v186, s74, v186\n"
"v_mfma_f32_16x16x32_bf16 v[188:191], v[220:223], a[44:47], v[188:191]\n"
"v_cvt_pk_bf16_f32 v83, v86, v87\n"
"v_cvt_pk_bf16_f32 v57, v58, v59\n"
"v_mfma_f32_16x16x32_bf16 v[192:195], v[208:211], a[192:195], 0\n"
"ds_read_b128 v[208:211], v228 offset:0\n"
"v_sub_f32 v185, s74, v185\n"
"v_mfma_f32_16x16x32_bf16 v[192:195], v[212:215], a[196:199], v[192:195]\n"
"ds_read_b128 v[212:215], v229 offset:0\n"
"v_cvt_pk_bf16_f32 v41, v42, v43\n"
"v_mfma_f32_16x16x32_bf16 v[192:195], v[216:219], a[200:203], v[192:195]\n"
"ds_read_b128 v[216:219], v228 offset:512\n"
"v_sub_f32 v189, s74, v189\n"
"v_mfma_f32_16x16x32_bf16 v[192:195], v[220:223], a[204:207], v[192:195]\n"
"s_waitcnt lgkmcnt(2)\n"
"ds_read_b128 v[220:223], v229 offset:512\n"
"v_sub_f32 v184, s74, v184\n"
"v_mfma_f32_16x16x32_bf16 v[152:155], v[208:211], a[0:3], v[180:183]\n"
"s_waitcnt lgkmcnt(2)\n"
"v_cvt_pk_bf16_f32 v42, v44, v45\n"
"v_sub_f32 v187, s74, v187\n"
"v_mfma_f32_16x16x32_bf16 v[152:155], v[212:215], a[4:7], v[152:155]\n"
"s_waitcnt lgkmcnt(1)\n"
"v_accvgpr_write_b32 a129, v145\n"
"v_cvt_pk_bf16_f32 v82, v84, v85\n"
"v_mfma_f32_16x16x32_bf16 v[152:155], v[216:219], a[8:11], v[152:155]\n"
"s_waitcnt lgkmcnt(0)\n"
"v_cvt_pk_bf16_f32 v58, v60, v61\n"
"v_sub_f32 v188, s74, v188\n"
"v_mfma_f32_16x16x32_bf16 v[152:155], v[220:223], a[12:15], v[152:155]\n"
"v_cvt_pk_bf16_f32 v59, v62, v63\n"
"v_sub_f32 v190, s74, v190\n"
"v_mfma_f32_16x16x32_bf16 v[156:159], v[216:219], a[24:27], v[184:187]\n"
"v_cvt_pk_bf16_f32 v11, v14, v15\n"
"v_cvt_pk_bf16_f32 v43, v46, v47\n"
"v_mfma_f32_16x16x32_bf16 v[156:159], v[220:223], a[28:31], v[156:159]\n"
"v_sub_f32 v191, s74, v191\n"
"v_sub_f32 v192, s74, v192\n"
"v_mfma_f32_16x16x32_bf16 v[156:159], v[208:211], a[16:19], v[156:159]\n"
"v_cvt_pk_bf16_f32 v123, v126, v127\n"
"v_cvt_pk_bf16_f32 v10, v12, v13\n"
"v_mfma_f32_16x16x32_bf16 v[156:159], v[212:215], a[20:23], v[156:159]\n"
"ds_write2st64_b64 v255, v[88:89], v[90:91] offset0:226 offset1:227\n"
"v_permlane16_swap_b32 v57, v59\n"
"v_cvt_pk_bf16_f32 v16, v16, v17\n"
"v_accvgpr_write_b32 a241, v254\n"
"v_mov_b32 v196, 0\n"
"v_sub_f32 v193, s74, v193\n"
"v_exp_f32 v153, v153\n"
"v_mfma_f32_16x16x32_bf16 v[160:163], v[208:211], a[32:35], v[188:191]\n"
"v_sub_f32 v194, s74, v194\n"
"v_sub_f32 v195, s74, v195\n"
"v_mfma_f32_16x16x32_bf16 v[160:163], v[212:215], a[36:39], v[160:163]\n"
"v_exp_f32 v152, v152\n"
"v_mfma_f32_16x16x32_bf16 v[160:163], v[216:219], a[40:43], v[160:163]\n"
"v_exp_f32 v158, v158\n"
"v_mfma_f32_16x16x32_bf16 v[160:163], v[220:223], a[44:47], v[160:163]\n"
"v_exp_f32 v156, v156\n"
"v_mfma_f32_16x16x32_bf16 v[164:167], v[216:219], a[200:203], v[192:195]\n"
"ds_read_b128 v[216:219], v228 offset:4608\n"
"v_cvt_pk_bf16_f32 v74, v76, v77\n"
"v_mfma_f32_16x16x32_bf16 v[164:167], v[220:223], a[204:207], v[164:167]\n"
"ds_read_b128 v[220:223], v229 offset:4608\n"
"v_cvt_pk_bf16_f32 v66, v68, v69\n"
"v_mfma_f32_16x16x32_bf16 v[164:167], v[208:211], a[192:195], v[164:167]\n"
"ds_read_b128 v[208:211], v228 offset:4096\n"
"v_cvt_pk_bf16_f32 v24, v24, v25\n"
"v_mfma_f32_16x16x32_bf16 v[164:167], v[212:215], a[196:199], v[164:167]\n"
"s_waitcnt lgkmcnt(2)\n"
"ds_read_b128 v[212:215], v229 offset:4096\n"
"v_accvgpr_write_b32 a114, v130\n"
"v_mfma_f32_16x16x32_bf16 v[168:171], v[216:219], a[8:11], v[180:183]\n"
"s_waitcnt lgkmcnt(2)\n"
"v_exp_f32 v163, v163\n"
"v_mfma_f32_16x16x32_bf16 v[168:171], v[220:223], a[12:15], v[168:171]\n"
"s_waitcnt lgkmcnt(1)\n"
"v_exp_f32 v162, v162\n"
"v_mfma_f32_16x16x32_bf16 v[168:171], v[208:211], a[0:3], v[168:171]\n"
"s_waitcnt lgkmcnt(0)\n"
"v_exp_f32 v160, v160\n"
"v_mfma_f32_16x16x32_bf16 v[168:171], v[212:215], a[4:7], v[168:171]\n"
"ds_write2st64_b64 v255, v[80:81], v[82:83] offset0:224 offset1:225\n"
"v_permlane16_swap_b32 v121, v123\n"
"v_accvgpr_write_b32 a131, v147\n"
"v_exp_f32 v154, v154\n"
"v_exp_f32 v157, v157\n"
"v_exp_f32 v159, v159\n"
"v_exp_f32 v161, v161\n"
"v_exp_f32 v167, v167\n"
"v_mfma_f32_16x16x32_bf16 v[172:175], v[208:211], a[16:19], v[184:187]\n"
"v_exp_f32 v165, v165\n"
"v_mfma_f32_16x16x32_bf16 v[172:175], v[212:215], a[20:23], v[172:175]\n"
"v_exp_f32 v166, v166\n"
"v_mfma_f32_16x16x32_bf16 v[172:175], v[216:219], a[24:27], v[172:175]\n"
"v_exp_f32 v169, v169\n"
"v_mfma_f32_16x16x32_bf16 v[172:175], v[220:223], a[28:31], v[172:175]\n"
"v_exp_f32 v155, v155\n"
"v_mfma_f32_16x16x32_bf16 v[176:179], v[216:219], a[40:43], v[188:191]\n"
"v_cvt_pk_bf16_f32 v128, v152, v153\n"
"v_bfe_u32 v248, v242, 9, 1\n"
"v_mfma_f32_16x16x32_bf16 v[176:179], v[220:223], a[44:47], v[176:179]\n"
"v_exp_f32 v171, v171\n"
"v_mfma_f32_16x16x32_bf16 v[176:179], v[208:211], a[32:35], v[176:179]\n"
"v_cvt_pk_bf16_f32 v129, v154, v155\n"
"v_and_b32 v249, 0xf0, v242\n"
"v_mfma_f32_16x16x32_bf16 v[176:179], v[212:215], a[36:39], v[176:179]\n"
"ds_write2st64_b64 v255, v[64:65], v[66:67] offset0:196 offset1:197\n"
"v_cvt_pk_bf16_f32 v136, v156, v157\n"
"v_cvt_pk_bf16_f32 v137, v158, v159\n"
"v_cvt_pk_bf16_f32 v145, v162, v163\n"
"v_exp_f32 v170, v170\n"
"v_mfma_f32_16x16x32_bf16 v[152:155], v[208:211], a[192:195], v[192:195]\n"
"ds_read_b128 v[208:211], v230 offset:0\n"
"v_cvt_pk_bf16_f32 v125, v166, v167\n"
"v_mfma_f32_16x16x32_bf16 v[152:155], v[212:215], a[196:199], v[152:155]\n"
"ds_read_b128 v[212:215], v231 offset:0\n"
"v_accvgpr_write_b32 a123, v139\n"
"v_mfma_f32_16x16x32_bf16 v[152:155], v[216:219], a[200:203], v[152:155]\n"
"ds_read_b128 v[216:219], v230 offset:512\n"
"v_cvt_pk_bf16_f32 v50, v52, v53\n"
"v_mfma_f32_16x16x32_bf16 v[152:155], v[220:223], a[204:207], v[152:155]\n"
"s_waitcnt lgkmcnt(2)\n"
"ds_read_b128 v[220:223], v231 offset:512\n"
"v_permlane16_swap_b32 v40, v42\n"
"v_cvt_pk_bf16_f32 v144, v160, v161\n"
"v_exp_f32 v164, v164\n"
"v_exp_f32 v168, v168\n"
"v_cvt_pk_bf16_f32 v131, v170, v171\n"
"v_exp_f32 v172, v172\n"
"v_exp_f32 v173, v173\n"
"v_exp_f32 v174, v174\n"
"v_exp_f32 v176, v176\n"
"v_exp_f32 v179, v179\n"
"v_lshl_add_u32 v248, v248, 8, v249\n"
"v_mfma_f32_16x16x32_bf16 v[156:159], v[208:211], a[0:3], v[180:183]\n"
"s_waitcnt lgkmcnt(2)\n"
"v_cvt_pk_bf16_f32 v75, v78, v79\n"
"v_cvt_pk_bf16_f32 v130, v168, v169\n"
"v_mfma_f32_16x16x32_bf16 v[156:159], v[212:215], a[4:7], v[156:159]\n"
"s_waitcnt lgkmcnt(1)\n"
"v_bfe_u32 v249, v242, 8, 1\n"
"v_lshl_add_u32 v248, v249, 9, v248\n"
"v_mfma_f32_16x16x32_bf16 v[156:159], v[216:219], a[8:11], v[156:159]\n"
"s_waitcnt lgkmcnt(0)\n"
"v_exp_f32 v177, v177\n"
"v_mfma_f32_16x16x32_bf16 v[156:159], v[220:223], a[12:15], v[156:159]\n"
"v_mov_b32 v202, 0\n"
"v_cvt_pk_bf16_f32 v124, v164, v165\n"
"v_mfma_f32_16x16x32_bf16 v[160:163], v[216:219], a[24:27], v[184:187]\n"
"v_exp_f32 v155, v155\n"
"v_mfma_f32_16x16x32_bf16 v[160:163], v[220:223], a[28:31], v[160:163]\n"
"v_permlane16_swap_b32 v49, v51\n"
"v_mfma_f32_16x16x32_bf16 v[160:163], v[208:211], a[16:19], v[160:163]\n"
"v_mov_b32 v201, 0\n"
"v_xor_b32_e64 v249, s42, 0\n"
"v_mfma_f32_16x16x32_bf16 v[160:163], v[212:215], a[20:23], v[160:163]\n"
"ds_write2st64_b64 v255, v[72:73], v[74:75] offset0:198 offset1:199\n"
"v_permlane16_swap_b32 v48, v50\n"
"v_permlane16_swap_b32 v56, v58\n"
"v_cvt_pk_bf16_f32 v25, v26, v27\n"
"v_cvt_pk_bf16_f32 v138, v172, v173\n"
"v_exp_f32 v153, v153\n"
"v_exp_f32 v157, v157\n"
"v_mfma_f32_16x16x32_bf16 v[164:167], v[208:211], a[32:35], v[188:191]\n"
"ds_read_b128 v[208:211], v230 offset:4096\n"
"v_mov_b32 v197, 0\n"
"v_mfma_f32_16x16x32_bf16 v[164:167], v[212:215], a[36:39], v[164:167]\n"
"ds_read_b128 v[212:215], v231 offset:4096\n"
"v_accvgpr_write_b32 a243, v254\n"
"v_mfma_f32_16x16x32_bf16 v[164:167], v[216:219], a[40:43], v[164:167]\n"
"ds_read_b128 v[216:219], v230 offset:4608\n"
"v_lshl_add_u32 v248, v249, 12, v248\n"
"v_mfma_f32_16x16x32_bf16 v[164:167], v[220:223], a[44:47], v[164:167]\n"
"s_waitcnt lgkmcnt(2)\n"
"ds_read_b128 v[220:223], v231 offset:4608\n"
"v_accvgpr_write_b32 a104, v120\n"
"v_permlane16_swap_b32 v8, v10\n"
"v_permlane16_swap_b32 v9, v11\n"
"v_accvgpr_write_b32 a240, v254\n"
"v_exp_f32 v175, v175\n"
"v_exp_f32 v178, v178\n"
"v_exp_f32 v159, v159\n"
"v_exp_f32 v160, v160\n"
"v_exp_f32 v163, v163\n"
"v_mfma_f32_16x16x32_bf16 v[168:171], v[208:211], a[0:3], v[180:183]\n"
"s_waitcnt lgkmcnt(2)\n"
"v_exp_f32 v154, v154\n"
"v_mfma_f32_16x16x32_bf16 v[168:171], v[212:215], a[4:7], v[168:171]\n"
"s_waitcnt lgkmcnt(1)\n"
"v_accvgpr_write_b32 a186, v42\n"
"v_add_u32 v249, s75, v248\n"
"v_mfma_f32_16x16x32_bf16 v[168:171], v[216:219], a[8:11], v[168:171]\n"
"s_waitcnt lgkmcnt(0)\n"
"v_exp_f32 v152, v152\n"
"v_mfma_f32_16x16x32_bf16 v[168:171], v[220:223], a[12:15], v[168:171]\n"
"ds_write_b128 v243, v[48:51] offset:12288\n"
"v_accvgpr_write_b32 a130, v146\n"
"v_permlane16_swap_b32 v41, v43\n"
"v_cvt_pk_bf16_f32 v139, v174, v175\n"
"v_exp_f32 v164, v164\n"
"v_mfma_f32_16x16x32_bf16 v[172:175], v[216:219], a[24:27], v[184:187]\n"
"v_exp_f32 v156, v156\n"
"v_mfma_f32_16x16x32_bf16 v[172:175], v[220:223], a[28:31], v[172:175]\n"
"v_exp_f32 v158, v158\n"
"v_mfma_f32_16x16x32_bf16 v[172:175], v[208:211], a[16:19], v[172:175]\n"
"v_exp_f32 v168, v168\n"
"v_mfma_f32_16x16x32_bf16 v[172:175], v[212:215], a[20:23], v[172:175]\n"
"s_cbranch_vccnz LPCE0_%=\nLPKE0_%=:\n"
"ds_write_b128 v243, v[56:59] offset:14336\n"
"v_accvgpr_write_b32 a185, v41\n"
"v_cvt_pk_bf16_f32 v146, v176, v177\n"
"v_cvt_pk_bf16_f32 v147, v178, v179\n"
"v_cvt_pk_bf16_f32 v126, v152, v153\n"
"v_cvt_pk_bf16_f32 v133, v158, v159\n"
"v_exp_f32 v165, v165\n"
"v_mfma_f32_16x16x32_bf16 v[176:179], v[208:211], a[32:35], v[188:191]\n"
"v_cvt_pk_bf16_f32 v127, v154, v155\n"
"v_cvt_pk_bf16_f32 v132, v156, v157\n"
"v_mfma_f32_16x16x32_bf16 v[176:179], v[212:215], a[36:39], v[176:179]\n"
"v_exp_f32 v161, v161\n"
"v_mfma_f32_16x16x32_bf16 v[176:179], v[216:219], a[40:43], v[176:179]\n"
"v_cvt_pk_bf16_f32 v17, v18, v19\n"
"v_accvgpr_write_b32 a187, v43\n"
"v_mfma_f32_16x16x32_bf16 v[176:179], v[220:223], a[44:47], v[176:179]\n"
"v_cvt_pk_bf16_f32 v140, v160, v161\n"
"v_exp_f32 v162, v162\n"
"v_exp_f32 v167, v167\n"
"v_exp_f32 v169, v169\n"
"v_exp_f32 v172, v172\n"
"v_exp_f32 v173, v173\n"
"v_exp_f32 v174, v174\n"
"s_waitcnt vmcnt(8) lgkmcnt(0)\n"
"s_barrier\n"
"ds_read_b128 v[208:211], v228 offset:16384\n"
"ds_read_b128 v[212:215], v229 offset:16384\n"
"ds_read_b128 v[216:219], v228 offset:16896\n"
"ds_read_b128 v[220:223], v229 offset:16896\n"
"s_waitcnt lgkmcnt(3)\n"
"v_mfma_f32_16x16x32_bf16 v[152:155], v[208:211], a[0:3], v[180:183]\n"
"v_accvgpr_write_b32 a153, v9\n"
"v_cvt_pk_bf16_f32 v32, v32, v33\n"
"s_waitcnt lgkmcnt(2)\n"
"v_mfma_f32_16x16x32_bf16 v[152:155], v[212:215], a[4:7], v[152:155]\n"
"ds_read_b64_tr_b16 v[224:225], v239 offset:0\n"
"v_cvt_pk_bf16_f32 v141, v162, v163\n"
"s_waitcnt lgkmcnt(2)\n"
"v_mfma_f32_16x16x32_bf16 v[152:155], v[216:219], a[8:11], v[152:155]\n"
"s_waitcnt lgkmcnt(1)\n"
"ds_read_b128 a[188:191], v249 offset:52224\n"
"v_mov_b32 v203, 0\n"
"v_mfma_f32_16x16x32_bf16 v[152:155], v[220:223], a[12:15], v[152:155]\n"
"ds_read_b128 a[116:119], v248 offset:51200\n"
"v_cvt_pk_bf16_f32 v27, v30, v31\n"
"v_mfma_f32_16x16x32_bf16 v[156:159], v[216:219], a[24:27], v[184:187]\n"
"ds_read_b64_tr_b16 v[226:227], v239 offset:4096\n"
"ds_read_b128 a[180:183], v249 offset:51200\n"
"v_mfma_f32_16x16x32_bf16 v[156:159], v[220:223], a[28:31], v[156:159]\n"
"v_accvgpr_write_b32 a154, v10\n"
"v_accvgpr_write_b32 a184, v40\n"
"v_mfma_f32_16x16x32_bf16 v[156:159], v[208:211], a[16:19], v[156:159]\n"
"ds_read_b64_tr_b16 a[244:245], v234 offset:0\n"
"ds_read_b64_tr_b16 a[246:247], v234 offset:4096\n"
"v_mfma_f32_16x16x32_bf16 v[156:159], v[212:215], a[20:23], v[156:159]\n"
"ds_read_b128 a[100:103], v248 offset:49152\n"
"v_exp_f32 v166, v166\n"
"v_exp_f32 v175, v175\n"
"v_exp_f32 v176, v176\n"
"v_exp_f32 v177, v177\n"
"v_exp_f32 v154, v154\n"
"v_mfma_f32_16x16x32_bf16 v[160:163], v[208:211], a[32:35], v[188:191]\n"
"v_mov_b32 v198, 0\n"
"v_cvt_pk_bf16_f32 v148, v164, v165\n"
"v_mfma_f32_16x16x32_bf16 v[160:163], v[212:215], a[36:39], v[160:163]\n"
"ds_read_b64_tr_b16 a[250:251], v238 offset:4096\n"
"ds_read_b64_tr_b16 a[254:255], v235 offset:4096\n"
"v_mfma_f32_16x16x32_bf16 v[160:163], v[216:219], a[40:43], v[160:163]\n"
"ds_read_b64_tr_b16 a[248:249], v238 offset:0\n"
"ds_read_b128 a[156:159], v249 offset:35840\n"
"v_mfma_f32_16x16x32_bf16 v[160:163], v[220:223], a[44:47], v[160:163]\n"
"ds_read_b128 a[172:175], v249 offset:50176\n"
"v_accvgpr_write_b32 a155, v11\n"
"v_cvt_pk_bf16_f32 v149, v166, v167\n"
"v_cvt_pk_bf16_f32 v134, v168, v169\n"
"v_exp_f32 v170, v170\n"
"v_exp_f32 v171, v171\n"
"v_cvt_pk_bf16_f32 v143, v174, v175\n"
"v_exp_f32 v179, v179\n"
"v_mfma_f32_16x16x32_bf16 v[164:167], v[216:219], a[200:203], v[192:195]\n"
"ds_read_b128 a[164:167], v249 offset:49152\n"
"v_cvt_pk_bf16_f32 v142, v172, v173\n"
"v_mfma_f32_16x16x32_bf16 v[164:167], v[220:223], a[204:207], v[164:167]\n"
"ds_read_b64_tr_b16 a[252:253], v235 offset:0\n"
"v_cvt_pk_bf16_f32 v112, v112, v113\n"
"v_mfma_f32_16x16x32_bf16 v[164:167], v[208:211], a[192:195], v[164:167]\n"
"ds_read_b128 a[140:143], v249 offset:33792\n"
"v_cvt_pk_bf16_f32 v0, v0, v1\n"
"v_mfma_f32_16x16x32_bf16 v[164:167], v[212:215], a[196:199], v[164:167]\n"
"s_waitcnt lgkmcnt(8)\n"
"ds_read_b128 a[148:151], v249 offset:34816\n"
"v_accvgpr_write_b32 a152, v8\n"
"v_cvt_pk_bf16_f32 v33, v34, v35\n"
"v_cvt_pk_bf16_f32 v135, v170, v171\n"
"v_exp_f32 v178, v178\n"
"v_xor_b32 v207, 64, v240\n"
"v_exp_f32 v153, v153\n"
"v_exp_f32 v162, v162\n"
"v_mfma_f32_16x16x32_bf16 a[216:219], v[124:127], a[244:247], 0\n"
"ds_read_b128 a[108:111], v248 offset:50176\n"
"v_cvt_pk_bf16_f32 v1, v2, v3\n"
"v_mfma_f32_16x16x32_bf16 v[8:11], v[128:131], a[244:247], 0\n"
"s_waitcnt lgkmcnt(7)\n"
"ds_read_b128 a[124:127], v248 offset:52224\n"
"v_cvt_pk_bf16_f32 v34, v36, v37\n"
"v_mfma_f32_16x16x32_bf16 v[8:11], v[132:135], a[248:251], v[8:11]\n"
"ds_read_b128 v[216:219], v228 offset:20992\n"
"v_cvt_pk_bf16_f32 v26, v28, v29\n"
"v_mfma_f32_16x16x32_bf16 v[40:43], v[136:139], a[244:247], 0\n"
"ds_read_b128 a[132:135], v249 offset:32768\n"
"v_cvt_pk_bf16_f32 v150, v176, v177\n"
"v_mfma_f32_16x16x32_bf16 v[40:43], v[140:143], a[248:251], v[40:43]\n"
"v_cvt_pk_bf16_f32 v3, v6, v7\n"
"v_cvt_pk_bf16_f32 v151, v178, v179\n"
"v_mfma_f32_16x16x32_bf16 v[72:75], v[144:147], a[244:247], 0\n"
"ds_read_b128 v[208:211], v228 offset:20480\n"
"v_accvgpr_write_b32 a242, v254\n"
"v_mfma_f32_16x16x32_bf16 v[72:75], v[148:151], a[248:251], v[72:75]\n"
"ds_read_b128 v[220:223], v229 offset:20992\n"
"v_cvt_pk_bf16_f32 v113, v114, v115\n"
"v_mfma_f32_16x16x32_bf16 v[76:79], v[148:151], v[224:227], 0\n"
"s_waitcnt lgkmcnt(6)\n"
"v_exp_f32 v159, v159\n"
"v_mfma_f32_16x16x32_bf16 v[76:79], v[144:147], a[252:255], v[76:79]\n"
"v_permlane16_swap_b32 v24, v26\n"
"v_mfma_f32_16x16x32_bf16 v[44:47], v[140:143], v[224:227], 0\n"
"v_permlane16_swap_b32 v32, v34\n"
"v_mfma_f32_16x16x32_bf16 v[44:47], v[136:139], a[252:255], v[44:47]\n"
"ds_read_b128 v[212:215], v229 offset:20480\n"
"v_permlane16_swap_b32 v25, v27\n"
"v_cvt_pk_bf16_f32 v35, v38, v39\n"
"v_exp_f32 v155, v155\n"
"v_exp_f32 v160, v160\n"
"v_exp_f32 v167, v167\n"
"v_mfma_f32_16x16x32_bf16 v[12:15], v[132:135], v[224:227], 0\n"
"v_exp_f32 v164, v164\n"
"v_mfma_f32_16x16x32_bf16 v[12:15], v[128:131], a[252:255], v[12:15]\n"
"v_exp_f32 v166, v166\n"
"v_mfma_f32_16x16x32_bf16 a[220:223], v[124:127], a[252:255], 0\n"
"s_waitcnt lgkmcnt(2)\n"
"ds_read_b64_tr_b16 a[254:255], v235 offset:4608\n"
"v_accvgpr_write_b32 a169, v25\n"
"v_smfmac_f32_16x16x64_bf16 v[196:199], a[240:243], v[128:135], v241\n"
"s_add_u32 m0, s44, 98304\n"
"ds_read_b64_tr_b16 v[224:225], v239 offset:512\n"
"v_cvt_pk_bf16_f32 v97, v154, v155\n"
"v_mfma_f32_16x16x32_bf16 v[202:205], a[240:243], v[124:127], v[202:205]\n"
"buffer_load_dwordx4 v240, s[20:23], s56 offen nt lds\n"
"v_mfma_f32_16x16x32_bf16 v[168:171], v[216:219], a[8:11], v[180:183]\n"
"s_waitcnt lgkmcnt(3)\n"
"v_exp_f32 v152, v152\n"
"v_mfma_f32_16x16x32_bf16 v[168:171], v[220:223], a[12:15], v[168:171]\n"
"ds_read_b64_tr_b16 a[252:253], v235 offset:512\n"
"v_accvgpr_write_b32 a168, v24\n"
"v_mfma_f32_16x16x32_bf16 v[168:171], v[208:211], a[0:3], v[168:171]\n"
"s_waitcnt lgkmcnt(3)\n"
"ds_read_b64_tr_b16 a[244:245], v234 offset:512\n"
"v_cvt_pk_bf16_f32 v96, v152, v153\n"
"v_mfma_f32_16x16x32_bf16 v[168:171], v[212:215], a[4:7], v[168:171]\n"
"ds_read_b64_tr_b16 a[248:249], v238 offset:512\n"
"ds_read_b64_tr_b16 a[250:251], v238 offset:4608\n"
"v_mfma_f32_16x16x32_bf16 v[172:175], v[208:211], a[16:19], v[184:187]\n"
"ds_read_b64_tr_b16 a[246:247], v234 offset:4608\n"
"v_cvt_pk_bf16_f32 v115, v118, v119\n"
"v_mfma_f32_16x16x32_bf16 v[172:175], v[212:215], a[20:23], v[172:175]\n"
"v_exp_f32 v165, v165\n"
"v_mfma_f32_16x16x32_bf16 v[172:175], v[216:219], a[24:27], v[172:175]\n"
"s_add_u32 s40, s56, s52\n"
"v_exp_f32 v158, v158\n"
"v_mfma_f32_16x16x32_bf16 v[172:175], v[220:223], a[28:31], v[172:175]\n"
"buffer_load_dwordx4 v207, s[20:23], s40 offen offset:1024 nt lds\n"
"v_mfma_f32_16x16x32_bf16 v[176:179], v[216:219], a[40:43], v[188:191]\n"
"v_exp_f32 v157, v157\n"
"v_mfma_f32_16x16x32_bf16 v[176:179], v[220:223], a[44:47], v[176:179]\n"
"v_accvgpr_write_b32 a105, v121\n"
"v_accvgpr_write_b32 a170, v26\n"
"v_mfma_f32_16x16x32_bf16 v[176:179], v[208:211], a[32:35], v[176:179]\n"
"v_exp_f32 v156, v156\n"
"v_mfma_f32_16x16x32_bf16 v[176:179], v[212:215], a[36:39], v[176:179]\n"
"v_permlane16_swap_b32 v113, v115\n"
"v_cvt_pk_bf16_f32 v2, v4, v5\n"
"v_exp_f32 v161, v161\n"
"v_exp_f32 v163, v163\n"
"v_exp_f32 v172, v172\n"
"v_mfma_f32_16x16x32_bf16 v[152:155], v[208:211], a[192:195], v[192:195]\n"
"v_accvgpr_write_b32 a171, v27\n"
"v_mov_b32 v200, 0\n"
"v_mfma_f32_16x16x32_bf16 v[152:155], v[212:215], a[196:199], v[152:155]\n"
"ds_read_b64_tr_b16 v[226:227], v239 offset:4608\n"
"v_accvgpr_write_b32 a176, v32\n"
"v_mfma_f32_16x16x32_bf16 v[152:155], v[216:219], a[200:203], v[152:155]\n"
"ds_read_b128 v[212:215], v231 offset:16384\n"
"v_cvt_pk_bf16_f32 v105, v158, v159\n"
"v_mfma_f32_16x16x32_bf16 v[152:155], v[220:223], a[204:207], v[152:155]\n"
"s_waitcnt lgkmcnt(2)\n"
"v_permlane16_swap_b32 v0, v2\n"
"v_mfma_f32_16x16x32_bf16 a[232:235], v[124:127], a[244:247], 0\n"
"ds_read_b128 v[216:219], v230 offset:16896\n"
"v_cvt_pk_bf16_f32 v18, v20, v21\n"
"v_mfma_f32_16x16x32_bf16 v[24:27], v[128:131], a[244:247], 0\n"
"s_add_u32 s40, s56, s53\n"
"ds_read_b128 v[208:211], v230 offset:16384\n"
"v_cvt_pk_bf16_f32 v104, v156, v157\n"
"v_mfma_f32_16x16x32_bf16 v[24:27], v[132:135], a[248:251], v[24:27]\n"
"buffer_load_dwordx4 v240, s[20:23], s40 offen offset:2048 nt lds\n"
"v_mfma_f32_16x16x32_bf16 v[56:59], v[136:139], a[244:247], 0\n"
"v_exp_f32 v174, v174\n"
"v_mfma_f32_16x16x32_bf16 v[56:59], v[140:143], a[248:251], v[56:59]\n"
"v_accvgpr_write_b32 a97, v113\n"
"v_accvgpr_write_b32 a144, v0\n"
"v_mfma_f32_16x16x32_bf16 v[88:91], v[144:147], a[244:247], 0\n"
"v_exp_f32 v171, v171\n"
"v_mfma_f32_16x16x32_bf16 v[88:91], v[148:151], a[248:251], v[88:91]\n"
"s_waitcnt lgkmcnt(2)\n"
"ds_read_b128 v[220:223], v231 offset:16896\n"
"v_cvt_pk_bf16_f32 v114, v116, v117\n"
"v_mfma_f32_16x16x32_bf16 v[92:95], v[148:151], v[224:227], 0\n"
"s_add_u32 s40, s56, s54\n"
"v_permlane16_swap_b32 v33, v35\n"
"v_mfma_f32_16x16x32_bf16 v[92:95], v[144:147], a[252:255], v[92:95]\n"
"buffer_load_dwordx4 v207, s[20:23], s40 offen offset:3072 nt lds\n"
"v_permlane16_swap_b32 v112, v114\n"
"v_accvgpr_write_b32 a99, v115\n"
"v_cvt_pk_bf16_f32 v120, v164, v165\n"
"v_cvt_pk_bf16_f32 v121, v166, v167\n"
"v_exp_f32 v175, v175\n"
"v_exp_f32 v178, v178\n"
"v_mfma_f32_16x16x32_bf16 v[60:63], v[140:143], v[224:227], 0\n"
"s_add_u32 s55, s55, s59\n"
"v_permlane16_swap_b32 v1, v3\n"
"v_mfma_f32_16x16x32_bf16 v[60:63], v[136:139], a[252:255], v[60:63]\n"
"s_or_b32 s55, s55, s58\n"
"v_accvgpr_write_b32 a96, v112\n"
"v_accvgpr_write_b32 a178, v34\n"
"v_mfma_f32_16x16x32_bf16 v[28:31], v[132:135], v[224:227], 0\n"
"s_xor_b32 s56, s55, s57\n"
"ds_read_b64_tr_b16 v[224:225], v237 offset:0\n"
"v_accvgpr_write_b32 a179, v35\n"
"v_mfma_f32_16x16x32_bf16 v[28:31], v[128:131], a[252:255], v[28:31]\n"
"ds_read_b64_tr_b16 a[244:245], v232 offset:0\n"
"v_accvgpr_write_b32 a145, v1\n"
"v_mfma_f32_16x16x32_bf16 a[236:239], v[124:127], a[252:255], 0\n"
"v_exp_f32 v179, v179\n"
"v_smfmac_f32_16x16x64_bf16 v[198:201], a[240:243], v[136:143], v241\n"
"s_waitcnt lgkmcnt(2)\n"
"ds_read_b64_tr_b16 a[252:253], v233 offset:0\n"
"v_accvgpr_write_b32 a177, v33\n"
"v_mfma_f32_16x16x32_bf16 v[156:159], v[208:211], a[0:3], v[180:183]\n"
"ds_read_b64_tr_b16 a[246:247], v232 offset:4096\n"
"v_cvt_pk_bf16_f32 v113, v162, v163\n"
"v_mfma_f32_16x16x32_bf16 v[156:159], v[212:215], a[4:7], v[156:159]\n"
"ds_read_b64_tr_b16 a[250:251], v236 offset:4096\n"
"v_cvt_pk_bf16_f32 v112, v160, v161\n"
"v_mfma_f32_16x16x32_bf16 v[156:159], v[216:219], a[8:11], v[156:159]\n"
"s_add_u32 m0, s44, 49152\n"
"ds_read_b64_tr_b16 a[248:249], v236 offset:0\n"
"ds_read_b64_tr_b16 v[226:227], v237 offset:4096\n"
"v_mfma_f32_16x16x32_bf16 v[156:159], v[220:223], a[12:15], v[156:159]\n"
"buffer_load_dwordx4 v240, s[16:19], s56 offen nt lds\n"
"v_mfma_f32_16x16x32_bf16 v[160:163], v[216:219], a[24:27], v[184:187]\n"
"v_exp_f32 v169, v169\n"
"v_mfma_f32_16x16x32_bf16 v[160:163], v[220:223], a[28:31], v[160:163]\n"
"v_accvgpr_write_b32 a98, v114\n"
"v_accvgpr_write_b32 a147, v3\n"
"v_mfma_f32_16x16x32_bf16 v[160:163], v[208:211], a[16:19], v[160:163]\n"
"v_accvgpr_write_b32 a146, v2\n"
"v_cvt_pk_bf16_f32 v107, v174, v175\n"
"v_mfma_f32_16x16x32_bf16 v[160:163], v[212:215], a[20:23], v[160:163]\n"
"ds_read_b64_tr_b16 a[254:255], v233 offset:4096\n"
"v_cvt_pk_bf16_f32 v115, v178, v179\n"
"v_mfma_f32_16x16x32_bf16 v[164:167], v[208:211], a[32:35], v[188:191]\n"
"v_exp_f32 v158, v158\n"
"v_mfma_f32_16x16x32_bf16 v[164:167], v[212:215], a[36:39], v[164:167]\n"
"v_exp_f32 v168, v168\n"
"v_mfma_f32_16x16x32_bf16 v[164:167], v[216:219], a[40:43], v[164:167]\n"
"v_exp_f32 v170, v170\n"
"v_mfma_f32_16x16x32_bf16 v[164:167], v[220:223], a[44:47], v[164:167]\n"
"s_waitcnt lgkmcnt(1)\n"
"v_exp_f32 v159, v159\n"
"v_mfma_f32_16x16x32_bf16 a[208:211], v[124:127], a[244:247], 0\n"
"ds_read_b128 v[208:211], v230 offset:20480\n"
"v_cvt_pk_bf16_f32 v19, v22, v23\n"
"v_mfma_f32_16x16x32_bf16 v[0:3], v[128:131], a[244:247], 0\n"
"s_add_u32 s40, s56, s52\n"
"ds_read_b128 v[216:219], v230 offset:20992\n"
"v_cvt_pk_bf16_f32 v98, v168, v169\n"
"v_mfma_f32_16x16x32_bf16 v[0:3], v[132:135], a[248:251], v[0:3]\n"
"buffer_load_dwordx4 v207, s[16:19], s40 offen offset:1024 nt lds\n"
"v_mfma_f32_16x16x32_bf16 v[32:35], v[136:139], a[244:247], 0\n"
"v_exp_f32 v165, v165\n"
"v_mfma_f32_16x16x32_bf16 v[32:35], v[140:143], a[248:251], v[32:35]\n"
"v_exp_f32 v176, v176\n"
"v_mfma_f32_16x16x32_bf16 v[64:67], v[144:147], a[244:247], 0\n"
"ds_read_b128 v[212:215], v231 offset:20480\n"
"v_cvt_pk_bf16_f32 v99, v170, v171\n"
"v_mfma_f32_16x16x32_bf16 v[64:67], v[148:151], a[248:251], v[64:67]\n"
"ds_read_b128 v[220:223], v231 offset:20992\n"
"v_accvgpr_write_b32 a107, v123\n"
"v_permlane16_swap_b32 v16, v18\n"
"v_exp_f32 v173, v173\n"
"v_exp_f32 v154, v154\n"
"v_exp_f32 v155, v155\n"
"v_mfma_f32_16x16x32_bf16 v[68:71], v[148:151], v[224:227], 0\n"
"s_waitcnt lgkmcnt(4)\n"
"s_add_u32 s40, s56, s53\n"
"v_exp_f32 v164, v164\n"
"v_mfma_f32_16x16x32_bf16 v[68:71], v[144:147], a[252:255], v[68:71]\n"
"buffer_load_dwordx4 v240, s[16:19], s40 offen offset:2048 nt lds\n"
"v_mfma_f32_16x16x32_bf16 v[36:39], v[140:143], v[224:227], 0\n"
"v_exp_f32 v153, v153\n"
"v_mfma_f32_16x16x32_bf16 v[36:39], v[136:139], a[252:255], v[36:39]\n"
"v_exp_f32 v166, v166\n"
"v_mfma_f32_16x16x32_bf16 v[4:7], v[132:135], v[224:227], 0\n"
"v_permlane16_swap_b32 v17, v19\n"
"v_mfma_f32_16x16x32_bf16 v[4:7], v[128:131], a[252:255], v[4:7]\n"
"ds_read_b64_tr_b16 a[250:251], v236 offset:4608\n"
"v_cvt_pk_bf16_f32 v106, v172, v173\n"
"v_mfma_f32_16x16x32_bf16 a[212:215], v[124:127], a[252:255], 0\n"
"v_exp_f32 v162, v162\n"
"v_smfmac_f32_16x16x64_bf16 v[200:203], a[240:243], v[144:151], v241\n"
"s_waitcnt lgkmcnt(1)\n"
"ds_read_b64_tr_b16 a[246:247], v232 offset:4608\n"
"ds_read_b64_tr_b16 a[248:249], v236 offset:512\n"
"v_mfma_f32_16x16x32_bf16 v[168:171], v[208:211], a[0:3], v[180:183]\n"
"v_exp_f32 v177, v177\n"
"v_mfma_f32_16x16x32_bf16 v[168:171], v[212:215], a[4:7], v[168:171]\n"
"ds_read_b64_tr_b16 a[244:245], v232 offset:512\n"
"v_cvt_pk_bf16_f32 v114, v176, v177\n"
"v_mfma_f32_16x16x32_bf16 v[168:171], v[216:219], a[8:11], v[168:171]\n"
"s_add_u32 s40, s56, s54\n"
"v_exp_f32 v157, v157\n"
"v_mfma_f32_16x16x32_bf16 v[168:171], v[220:223], a[12:15], v[168:171]\n"
"buffer_load_dwordx4 v207, s[16:19], s40 offen offset:3072 nt lds\n"
"v_mfma_f32_16x16x32_bf16 v[172:175], v[216:219], a[24:27], v[184:187]\n"
"v_exp_f32 v167, v167\n"
"v_mfma_f32_16x16x32_bf16 v[172:175], v[220:223], a[28:31], v[172:175]\n"
"v_exp_f32 v160, v160\n"
"v_mfma_f32_16x16x32_bf16 v[172:175], v[208:211], a[16:19], v[172:175]\n"
"ds_read_b64_tr_b16 a[254:255], v233 offset:4608\n"
"v_accvgpr_write_b32 a162, v18\n"
"v_mfma_f32_16x16x32_bf16 v[172:175], v[212:215], a[20:23], v[172:175]\n"
"s_cbranch_vccnz LPCE1_%=\nLPKE1_%=:\n"
"v_bfe_u32 v249, v242, 4, 2\n"
"v_mfma_f32_16x16x32_bf16 v[176:179], v[208:211], a[32:35], v[188:191]\n"
"v_accvgpr_write_b32 a106, v122\n"
"v_bfe_u32 v248, v242, 8, 2\n"
"v_mfma_f32_16x16x32_bf16 v[176:179], v[212:215], a[36:39], v[176:179]\n"
"ds_read_b64_tr_b16 a[252:253], v233 offset:512\n"
"v_accvgpr_write_b32 a161, v17\n"
"v_mfma_f32_16x16x32_bf16 v[176:179], v[216:219], a[40:43], v[176:179]\n"
"ds_read_b64_tr_b16 v[224:225], v237 offset:512\n"
"v_accvgpr_write_b32 a160, v16\n"
"v_mfma_f32_16x16x32_bf16 v[176:179], v[220:223], a[44:47], v[176:179]\n"
"s_waitcnt lgkmcnt(3)\n"
"ds_read_b64_tr_b16 v[226:227], v237 offset:4608\n"
"v_accvgpr_write_b32 a163, v19\n"
"v_lshlrev_b32 v248, 6, v248\n"
"v_exp_f32 v152, v152\n"
"v_exp_f32 v163, v163\n"
"v_exp_f32 v169, v169\n"
"v_mfma_f32_16x16x32_bf16 a[224:227], v[124:127], a[244:247], 0\n"
"v_exp_f32 v156, v156\n"
"v_mfma_f32_16x16x32_bf16 v[16:19], v[128:131], a[244:247], 0\n"
"s_waitcnt vmcnt(8)\n"
"v_xor_b32 v248, v248, v242\n"
"v_bfe_u32 v254, v242, 6, 4\n"
"v_mfma_f32_16x16x32_bf16 v[16:19], v[132:135], a[248:251], v[16:19]\n"
"s_barrier\n"
"ds_read_b128 v[216:219], v228 offset:33280\n"
"v_mul_u32_u24 v249, 20, v249\n"
"v_cvt_pk_bf16_f32 v101, v158, v159\n"
"v_exp_f32 v176, v176\n"
"v_exp_f32 v177, v177\n"
"v_mfma_f32_16x16x32_bf16 v[48:51], v[136:139], a[244:247], 0\n"
"ds_read_b128 v[220:223], v229 offset:33280\n"
"v_cvt_pk_bf16_f32 v109, v162, v163\n"
"v_mfma_f32_16x16x32_bf16 v[48:51], v[140:143], a[248:251], v[48:51]\n"
"ds_read_b128 v[208:211], v228 offset:32768\n"
"ds_read_b128 v[212:215], v229 offset:32768\n"
"v_mfma_f32_16x16x32_bf16 v[80:83], v[144:147], a[244:247], 0\n"
"ds_read_b64_tr_b16 a[246:247], v234 offset:20480\n"
"v_cvt_pk_bf16_f32 v122, v152, v153\n"
"v_mfma_f32_16x16x32_bf16 v[80:83], v[148:151], a[248:251], v[80:83]\n"
"s_waitcnt lgkmcnt(5)\n"
"ds_read_b64_tr_b16 a[250:251], v238 offset:20480\n"
"v_cvt_pk_bf16_f32 v123, v154, v155\n"
"v_mfma_f32_16x16x32_bf16 v[84:87], v[148:151], v[224:227], 0\n"
"v_cvt_pk_bf16_f32 v116, v164, v165\n"
"v_cvt_pk_bf16_f32 v117, v166, v167\n"
"v_mfma_f32_16x16x32_bf16 v[84:87], v[144:147], a[252:255], v[84:87]\n"
"v_xor_b32 v249, v249, v254\n"
"v_cvt_pk_bf16_f32 v118, v176, v177\n"
"v_mfma_f32_16x16x32_bf16 v[52:55], v[140:143], v[224:227], 0\n"
"v_lshl_add_u32 v249, v249, 4, s44\n"
"v_cvt_pk_bf16_f32 v100, v156, v157\n"
"v_mfma_f32_16x16x32_bf16 v[52:55], v[136:139], a[252:255], v[52:55]\n"
"v_exp_f32 v161, v161\n"
"v_mfma_f32_16x16x32_bf16 v[20:23], v[132:135], v[224:227], 0\n"
"v_exp_f32 v174, v174\n"
"v_mfma_f32_16x16x32_bf16 v[20:23], v[128:131], a[252:255], v[20:23]\n"
"v_exp_f32 v168, v168\n"
"v_mfma_f32_16x16x32_bf16 a[228:231], v[124:127], a[252:255], 0\n"
"s_waitcnt lgkmcnt(1)\n"
"ds_read_b64_tr_b16 a[254:255], v235 offset:20480\n"
"s_branch LAY2_%=\n"
"LAL_%=:\n"
"v_mfma_f32_16x16x32_bf16 a[224:227], v[124:127], a[244:247], a[224:227]\n"
"v_exp_f32 v162, v162\n"
"v_mfma_f32_16x16x32_bf16 v[16:19], v[128:131], a[244:247], v[16:19]\n"
"s_waitcnt vmcnt(8)\n"
"v_exp_f32 v168, v168\n"
"v_mfma_f32_16x16x32_bf16 v[16:19], v[132:135], a[248:251], v[16:19]\n"
"s_barrier\n"
"v_mfma_f32_16x16x32_bf16 v[48:51], v[136:139], a[244:247], v[48:51]\n"
"ds_read_b128 v[208:211], v228 offset:0\n"
"v_cvt_pk_bf16_f32 v101, v158, v159\n"
"v_mfma_f32_16x16x32_bf16 v[48:51], v[140:143], a[248:251], v[48:51]\n"
"ds_read_b128 v[216:219], v228 offset:512\n"
"v_cvt_pk_bf16_f32 v109, v162, v163\n"
"v_mfma_f32_16x16x32_bf16 v[80:83], v[144:147], a[244:247], v[80:83]\n"
"ds_read_b128 v[220:223], v229 offset:512\n"
"v_cvt_pk_bf16_f32 v123, v154, v155\n"
"v_mfma_f32_16x16x32_bf16 v[80:83], v[148:151], a[248:251], v[80:83]\n"
"ds_read_b128 v[212:215], v229 offset:0\n"
"v_cvt_pk_bf16_f32 v117, v166, v167\n"
"v_mfma_f32_16x16x32_bf16 v[84:87], v[148:151], v[224:227], v[84:87]\n"
"v_exp_f32 v176, v176\n"
"v_mfma_f32_16x16x32_bf16 v[84:87], v[144:147], a[252:255], v[84:87]\n"
"v_exp_f32 v169, v169\n"
"v_mfma_f32_16x16x32_bf16 v[52:55], v[140:143], v[224:227], v[52:55]\n"
"ds_read_b64_tr_b16 a[246:247], v234 offset:53248\n"
"v_cvt_pk_bf16_f32 v116, v164, v165\n"
"v_mfma_f32_16x16x32_bf16 v[52:55], v[136:139], a[252:255], v[52:55]\n"
"v_exp_f32 v177, v177\n"
"v_mfma_f32_16x16x32_bf16 v[20:23], v[132:135], v[224:227], v[20:23]\n"
"ds_read_b64_tr_b16 a[250:251], v238 offset:53248\n"
"v_cvt_pk_bf16_f32 v118, v176, v177\n"
"v_mfma_f32_16x16x32_bf16 v[20:23], v[128:131], a[252:255], v[20:23]\n"
"v_exp_f32 v174, v174\n"
"v_mfma_f32_16x16x32_bf16 a[228:231], v[124:127], a[252:255], a[228:231]\n"
"s_waitcnt lgkmcnt(3)\n"
"ds_read_b64_tr_b16 a[254:255], v235 offset:53248\n"
"v_cvt_pk_bf16_f32 v100, v156, v157\n"
"v_mfma_f32_16x16x32_bf16 v[152:155], v[208:211], a[0:3], v[180:183]\n"
"s_waitcnt lgkmcnt(3)\n"
"ds_read_b64_tr_b16 a[248:249], v238 offset:49152\n"
"v_cvt_pk_bf16_f32 v102, v168, v169\n"
"v_mfma_f32_16x16x32_bf16 v[152:155], v[212:215], a[4:7], v[152:155]\n"
"v_exp_f32 v172, v172\n"
"v_mfma_f32_16x16x32_bf16 v[152:155], v[216:219], a[8:11], v[152:155]\n"
"ds_read_b64_tr_b16 a[244:245], v234 offset:49152\n"
"v_cvt_pk_bf16_f32 v108, v160, v161\n"
"v_mfma_f32_16x16x32_bf16 v[152:155], v[220:223], a[12:15], v[152:155]\n"
"v_exp_f32 v175, v175\n"
"v_mfma_f32_16x16x32_bf16 v[156:159], v[216:219], a[24:27], v[184:187]\n"
"ds_read_b64_tr_b16 a[252:253], v235 offset:49152\n"
"v_cvt_pk_bf16_f32 v111, v174, v175\n"
"v_mfma_f32_16x16x32_bf16 v[156:159], v[220:223], a[28:31], v[156:159]\n"
"ds_read_b64_tr_b16 v[224:225], v239 offset:49152\n"
"v_mfma_f32_16x16x32_bf16 v[156:159], v[208:211], a[16:19], v[156:159]\n"
"s_add_u32 m0, s44, 81920\n"
"ds_read_b64_tr_b16 v[226:227], v239 offset:53248\n"
"v_mfma_f32_16x16x32_bf16 v[156:159], v[212:215], a[20:23], v[156:159]\n"
"buffer_load_dwordx4 v240, s[20:23], s56 offen nt lds\n"
"v_mfma_f32_16x16x32_bf16 v[160:163], v[208:211], a[32:35], v[188:191]\n"
"v_exp_f32 v178, v178\n"
"v_mfma_f32_16x16x32_bf16 v[160:163], v[212:215], a[36:39], v[160:163]\n"
"v_exp_f32 v171, v171\n"
"v_mfma_f32_16x16x32_bf16 v[160:163], v[216:219], a[40:43], v[160:163]\n"
"s_add_u32 s40, s56, s52\n"
"v_mfma_f32_16x16x32_bf16 v[160:163], v[220:223], a[44:47], v[160:163]\n"
"buffer_load_dwordx4 v207, s[20:23], s40 offen offset:1024 nt lds\n"
"v_mfma_f32_16x16x32_bf16 v[164:167], v[216:219], a[200:203], v[192:195]\n"
"v_exp_f32 v170, v170\n"
"v_mfma_f32_16x16x32_bf16 v[164:167], v[220:223], a[204:207], v[164:167]\n"
"v_exp_f32 v153, v153\n"
"v_mfma_f32_16x16x32_bf16 v[164:167], v[208:211], a[192:195], v[164:167]\n"
"v_exp_f32 v179, v179\n"
"v_mfma_f32_16x16x32_bf16 v[164:167], v[212:215], a[196:199], v[164:167]\n"
"s_waitcnt lgkmcnt(0)\n"
"v_exp_f32 v158, v158\n"
"v_mfma_f32_16x16x32_bf16 a[216:219], v[120:123], a[244:247], a[216:219]\n"
"ds_read_b128 v[216:219], v228 offset:4608\n"
"v_cvt_pk_bf16_f32 v103, v170, v171\n"
"v_mfma_f32_16x16x32_bf16 v[8:11], v[96:99], a[244:247], v[8:11]\n"
"v_exp_f32 v173, v173\n"
"v_mfma_f32_16x16x32_bf16 v[8:11], v[100:103], a[248:251], v[8:11]\n"
"ds_read_b128 v[208:211], v228 offset:4096\n"
"v_cvt_pk_bf16_f32 v110, v172, v173\n"
"v_mfma_f32_16x16x32_bf16 v[40:43], v[104:107], a[244:247], v[40:43]\n"
"v_exp_f32 v162, v162\n"
"v_mfma_f32_16x16x32_bf16 v[40:43], v[108:111], a[248:251], v[40:43]\n"
"ds_read_b128 v[220:223], v229 offset:4608\n"
"v_cvt_pk_bf16_f32 v119, v178, v179\n"
"v_mfma_f32_16x16x32_bf16 v[72:75], v[112:115], a[244:247], v[72:75]\n"
"s_add_u32 s40, s56, s53\n"
"ds_read_b128 v[212:215], v229 offset:4096\n"
"v_mfma_f32_16x16x32_bf16 v[72:75], v[116:119], a[248:251], v[72:75]\n"
"buffer_load_dwordx4 v240, s[20:23], s40 offen offset:2048 nt lds\n"
"v_mfma_f32_16x16x32_bf16 v[76:79], v[116:119], v[224:227], v[76:79]\n"
"v_exp_f32 v163, v163\n"
"v_mfma_f32_16x16x32_bf16 v[76:79], v[112:115], a[252:255], v[76:79]\n"
"v_exp_f32 v156, v156\n"
"v_mfma_f32_16x16x32_bf16 v[44:47], v[108:111], v[224:227], v[44:47]\n"
"v_exp_f32 v160, v160\n"
"v_mfma_f32_16x16x32_bf16 v[44:47], v[104:107], a[252:255], v[44:47]\n"
"v_exp_f32 v155, v155\n"
"v_mfma_f32_16x16x32_bf16 v[12:15], v[100:103], v[224:227], v[12:15]\n"
"ds_read_b64_tr_b16 v[226:227], v239 offset:53760\n"
"v_mfma_f32_16x16x32_bf16 v[12:15], v[96:99], a[252:255], v[12:15]\n"
"ds_read_b64_tr_b16 a[248:249], v238 offset:49664\n"
"v_mfma_f32_16x16x32_bf16 a[220:223], v[120:123], a[252:255], a[220:223]\n"
"v_exp_f32 v154, v154\n"
"v_smfmac_f32_16x16x64_bf16 v[196:199], a[240:243], v[96:103], v241\n"
"ds_read_b64_tr_b16 a[250:251], v238 offset:53760\n"
"v_mfma_f32_16x16x32_bf16 v[202:205], a[240:243], v[120:123], v[202:205]\n"
"s_waitcnt lgkmcnt(3)\n"
"v_exp_f32 v152, v152\n"
"v_mfma_f32_16x16x32_bf16 v[168:171], v[216:219], a[8:11], v[180:183]\n"
"ds_read_b64_tr_b16 a[244:245], v234 offset:49664\n"
"v_cvt_pk_bf16_f32 v128, v152, v153\n"
"v_mfma_f32_16x16x32_bf16 v[168:171], v[220:223], a[12:15], v[168:171]\n"
"v_exp_f32 v165, v165\n"
"v_mfma_f32_16x16x32_bf16 v[168:171], v[208:211], a[0:3], v[168:171]\n"
"ds_read_b64_tr_b16 a[246:247], v234 offset:53760\n"
"v_cvt_pk_bf16_f32 v145, v162, v163\n"
"v_mfma_f32_16x16x32_bf16 v[168:171], v[212:215], a[4:7], v[168:171]\n"
"ds_read_b64_tr_b16 a[254:255], v235 offset:53760\n"
"v_mfma_f32_16x16x32_bf16 v[172:175], v[208:211], a[16:19], v[184:187]\n"
"ds_read_b64_tr_b16 v[224:225], v239 offset:49664\n"
"v_cvt_pk_bf16_f32 v129, v154, v155\n"
"v_mfma_f32_16x16x32_bf16 v[172:175], v[212:215], a[20:23], v[172:175]\n"
"v_mfma_f32_16x16x32_bf16 v[172:175], v[216:219], a[24:27], v[172:175]\n"
"s_add_u32 s40, s56, s54\n"
"ds_read_b64_tr_b16 a[252:253], v235 offset:49664\n"
"v_mfma_f32_16x16x32_bf16 v[172:175], v[220:223], a[28:31], v[172:175]\n"
"buffer_load_dwordx4 v207, s[20:23], s40 offen offset:3072 nt lds\n"
"v_mfma_f32_16x16x32_bf16 v[176:179], v[216:219], a[40:43], v[188:191]\n"
"s_add_u32 s55, s55, s59\n"
"v_exp_f32 v157, v157\n"
"v_mfma_f32_16x16x32_bf16 v[176:179], v[220:223], a[44:47], v[176:179]\n"
"s_or_b32 s55, s55, s58\n"
"v_exp_f32 v171, v171\n"
"v_mfma_f32_16x16x32_bf16 v[176:179], v[208:211], a[32:35], v[176:179]\n"
"s_xor_b32 s56, s55, s57\n"
"v_exp_f32 v159, v159\n"
"v_mfma_f32_16x16x32_bf16 v[176:179], v[212:215], a[36:39], v[176:179]\n"
"v_exp_f32 v167, v167\n"
"v_mfma_f32_16x16x32_bf16 v[152:155], v[208:211], a[192:195], v[192:195]\n"
"v_cvt_pk_bf16_f32 v137, v158, v159\n"
"v_mfma_f32_16x16x32_bf16 v[152:155], v[212:215], a[196:199], v[152:155]\n"
"ds_read_b128 v[208:211], v230 offset:0\n"
"v_cvt_pk_bf16_f32 v136, v156, v157\n"
"v_mfma_f32_16x16x32_bf16 v[152:155], v[216:219], a[200:203], v[152:155]\n"
"v_exp_f32 v166, v166\n"
"v_mfma_f32_16x16x32_bf16 v[152:155], v[220:223], a[204:207], v[152:155]\n"
"s_waitcnt lgkmcnt(1)\n"
"ds_read_b128 v[216:219], v230 offset:512\n"
"v_mfma_f32_16x16x32_bf16 a[232:235], v[120:123], a[244:247], a[232:235]\n"
"v_exp_f32 v169, v169\n"
"v_mfma_f32_16x16x32_bf16 v[24:27], v[96:99], a[244:247], v[24:27]\n"
"ds_read_b128 v[220:223], v231 offset:512\n"
"v_mfma_f32_16x16x32_bf16 v[24:27], v[100:103], a[248:251], v[24:27]\n"
"v_exp_f32 v164, v164\n"
"v_mfma_f32_16x16x32_bf16 v[56:59], v[104:107], a[244:247], v[56:59]\n"
"ds_read_b128 v[212:215], v231 offset:0\n"
"v_mfma_f32_16x16x32_bf16 v[56:59], v[108:111], a[248:251], v[56:59]\n"
"v_exp_f32 v178, v178\n"
"v_mfma_f32_16x16x32_bf16 v[88:91], v[112:115], a[244:247], v[88:91]\n"
"s_add_u32 m0, s44, 32768\n"
"ds_read_b64_tr_b16 a[246:247], v232 offset:53248\n"
"v_mfma_f32_16x16x32_bf16 v[88:91], v[116:119], a[248:251], v[88:91]\n"
"buffer_load_dwordx4 v240, s[16:19], s56 offen nt lds\n"
"v_mfma_f32_16x16x32_bf16 v[92:95], v[116:119], v[224:227], v[92:95]\n"
"v_exp_f32 v175, v175\n"
"v_mfma_f32_16x16x32_bf16 v[92:95], v[112:115], a[252:255], v[92:95]\n"
"v_exp_f32 v168, v168\n"
"v_mfma_f32_16x16x32_bf16 v[60:63], v[108:111], v[224:227], v[60:63]\n"
"v_exp_f32 v161, v161\n"
"v_mfma_f32_16x16x32_bf16 v[60:63], v[104:107], a[252:255], v[60:63]\n"
"v_exp_f32 v177, v177\n"
"v_mfma_f32_16x16x32_bf16 v[28:31], v[100:103], v[224:227], v[28:31]\n"
"ds_read_b64_tr_b16 v[226:227], v237 offset:53248\n"
"v_mfma_f32_16x16x32_bf16 v[28:31], v[96:99], a[252:255], v[28:31]\n"
"v_exp_f32 v172, v172\n"
"v_mfma_f32_16x16x32_bf16 a[236:239], v[120:123], a[252:255], a[236:239]\n"
"ds_read_b64_tr_b16 a[250:251], v236 offset:53248\n"
"v_cvt_pk_bf16_f32 v124, v164, v165\n"
"v_smfmac_f32_16x16x64_bf16 v[198:201], a[240:243], v[104:111], v241\n"
"s_waitcnt lgkmcnt(2)\n"
"ds_read_b64_tr_b16 a[252:253], v233 offset:49152\n"
"v_mfma_f32_16x16x32_bf16 v[156:159], v[208:211], a[0:3], v[180:183]\n"
"ds_read_b64_tr_b16 a[248:249], v236 offset:49152\n"
"v_cvt_pk_bf16_f32 v125, v166, v167\n"
"v_mfma_f32_16x16x32_bf16 v[156:159], v[212:215], a[4:7], v[156:159]\n"
"ds_read_b64_tr_b16 v[224:225], v237 offset:49152\n"
"v_cvt_pk_bf16_f32 v130, v168, v169\n"
"v_mfma_f32_16x16x32_bf16 v[156:159], v[216:219], a[8:11], v[156:159]\n"
"ds_read_b64_tr_b16 a[254:255], v233 offset:53248\n"
"v_cvt_pk_bf16_f32 v144, v160, v161\n"
"v_mfma_f32_16x16x32_bf16 v[156:159], v[220:223], a[12:15], v[156:159]\n"
"ds_read_b64_tr_b16 a[244:245], v232 offset:49152\n"
"v_mfma_f32_16x16x32_bf16 v[160:163], v[216:219], a[24:27], v[184:187]\n"
"v_exp_f32 v176, v176\n"
"v_mfma_f32_16x16x32_bf16 v[160:163], v[220:223], a[28:31], v[160:163]\n"
"v_exp_f32 v152, v152\n"
"v_mfma_f32_16x16x32_bf16 v[160:163], v[208:211], a[16:19], v[160:163]\n"
"s_add_u32 s40, s56, s52\n"
"v_cvt_pk_bf16_f32 v146, v176, v177\n"
"v_mfma_f32_16x16x32_bf16 v[160:163], v[212:215], a[20:23], v[160:163]\n"
"buffer_load_dwordx4 v207, s[16:19], s40 offen offset:1024 nt lds\n"
"v_mfma_f32_16x16x32_bf16 v[164:167], v[208:211], a[32:35], v[188:191]\n"
"v_exp_f32 v170, v170\n"
"v_mfma_f32_16x16x32_bf16 v[164:167], v[212:215], a[36:39], v[164:167]\n"
"v_exp_f32 v173, v173\n"
"v_mfma_f32_16x16x32_bf16 v[164:167], v[216:219], a[40:43], v[164:167]\n"
"v_exp_f32 v153, v153\n"
"v_mfma_f32_16x16x32_bf16 v[164:167], v[220:223], a[44:47], v[164:167]\n"
"s_waitcnt lgkmcnt(0)\n"
"v_exp_f32 v174, v174\n"
"v_mfma_f32_16x16x32_bf16 a[208:211], v[120:123], a[244:247], a[208:211]\n"
"ds_read_b128 v[208:211], v230 offset:4096\n"
"v_cvt_pk_bf16_f32 v139, v174, v175\n"
"v_mfma_f32_16x16x32_bf16 v[0:3], v[96:99], a[244:247], v[0:3]\n"
"v_exp_f32 v156, v156\n"
"v_mfma_f32_16x16x32_bf16 v[0:3], v[100:103], a[248:251], v[0:3]\n"
"ds_read_b128 v[220:223], v231 offset:4608\n"
"v_cvt_pk_bf16_f32 v138, v172, v173\n"
"v_mfma_f32_16x16x32_bf16 v[32:35], v[104:107], a[244:247], v[32:35]\n"
"v_exp_f32 v161, v161\n"
"v_mfma_f32_16x16x32_bf16 v[32:35], v[108:111], a[248:251], v[32:35]\n"
"ds_read_b128 v[216:219], v230 offset:4608\n"
"v_cvt_pk_bf16_f32 v126, v152, v153\n"
"v_mfma_f32_16x16x32_bf16 v[64:67], v[112:115], a[244:247], v[64:67]\n"
"s_add_u32 s40, s56, s53\n"
"ds_read_b128 v[212:215], v231 offset:4096\n"
"v_mfma_f32_16x16x32_bf16 v[64:67], v[116:119], a[248:251], v[64:67]\n"
"buffer_load_dwordx4 v240, s[16:19], s40 offen offset:2048 nt lds\n"
"v_mfma_f32_16x16x32_bf16 v[68:71], v[116:119], v[224:227], v[68:71]\n"
"v_exp_f32 v159, v159\n"
"v_mfma_f32_16x16x32_bf16 v[68:71], v[112:115], a[252:255], v[68:71]\n"
"v_exp_f32 v155, v155\n"
"v_mfma_f32_16x16x32_bf16 v[36:39], v[108:111], v[224:227], v[36:39]\n"
"v_exp_f32 v160, v160\n"
"v_mfma_f32_16x16x32_bf16 v[36:39], v[104:107], a[252:255], v[36:39]\n"
"v_exp_f32 v179, v179\n"
"v_mfma_f32_16x16x32_bf16 v[4:7], v[100:103], v[224:227], v[4:7]\n"
"ds_read_b64_tr_b16 a[244:245], v232 offset:49664\n"
"ds_read_b64_tr_b16 v[224:225], v237 offset:49664\n"
"v_mfma_f32_16x16x32_bf16 v[4:7], v[96:99], a[252:255], v[4:7]\n"
"v_exp_f32 v158, v158\n"
"v_mfma_f32_16x16x32_bf16 a[212:215], v[120:123], a[252:255], a[212:215]\n"
"ds_read_b64_tr_b16 a[246:247], v232 offset:53760\n"
"ds_read_b64_tr_b16 a[250:251], v236 offset:53760\n"
"v_smfmac_f32_16x16x64_bf16 v[200:203], a[240:243], v[112:119], v241\n"
"s_waitcnt lgkmcnt(4)\n"
"ds_read_b64_tr_b16 v[226:227], v237 offset:53760\n"
"v_cvt_pk_bf16_f32 v131, v170, v171\n"
"v_mfma_f32_16x16x32_bf16 v[168:171], v[208:211], a[0:3], v[180:183]\n"
"v_exp_f32 v164, v164\n"
"v_mfma_f32_16x16x32_bf16 v[168:171], v[212:215], a[4:7], v[168:171]\n"
"ds_read_b64_tr_b16 a[248:249], v236 offset:49664\n"
"ds_read_b64_tr_b16 a[254:255], v233 offset:53760\n"
"v_mfma_f32_16x16x32_bf16 v[168:171], v[216:219], a[8:11], v[168:171]\n"
"s_add_u32 s40, s56, s54\n"
"ds_read_b64_tr_b16 a[252:253], v233 offset:49664\n"
"v_mfma_f32_16x16x32_bf16 v[168:171], v[220:223], a[12:15], v[168:171]\n"
"buffer_load_dwordx4 v207, s[16:19], s40 offen offset:3072 nt lds\n"
"v_mfma_f32_16x16x32_bf16 v[172:175], v[216:219], a[24:27], v[184:187]\n"
"v_exp_f32 v154, v154\n"
"v_mfma_f32_16x16x32_bf16 v[172:175], v[220:223], a[28:31], v[172:175]\n"
"v_exp_f32 v167, v167\n"
"v_mfma_f32_16x16x32_bf16 v[172:175], v[208:211], a[16:19], v[172:175]\n"
"v_exp_f32 v163, v163\n"
"v_mfma_f32_16x16x32_bf16 v[172:175], v[212:215], a[20:23], v[172:175]\n"
"s_cbranch_vccnz LPCL0_%=\nLPKL0_%=:\n"
"v_cvt_pk_bf16_f32 v147, v178, v179\n"
"v_mfma_f32_16x16x32_bf16 v[176:179], v[208:211], a[32:35], v[188:191]\n"
"v_exp_f32 v165, v165\n"
"v_mfma_f32_16x16x32_bf16 v[176:179], v[212:215], a[36:39], v[176:179]\n"
"v_exp_f32 v157, v157\n"
"v_mfma_f32_16x16x32_bf16 v[176:179], v[216:219], a[40:43], v[176:179]\n"
"v_exp_f32 v166, v166\n"
"v_mfma_f32_16x16x32_bf16 v[176:179], v[220:223], a[44:47], v[176:179]\n"
"s_waitcnt lgkmcnt(0)\n"
"v_mfma_f32_16x16x32_bf16 a[224:227], v[120:123], a[244:247], a[224:227]\n"
"v_exp_f32 v162, v162\n"
"v_mfma_f32_16x16x32_bf16 v[16:19], v[96:99], a[244:247], v[16:19]\n"
"s_waitcnt vmcnt(8)\n"
"v_exp_f32 v168, v168\n"
"v_mfma_f32_16x16x32_bf16 v[16:19], v[100:103], a[248:251], v[16:19]\n"
"s_barrier\n"
"v_mfma_f32_16x16x32_bf16 v[48:51], v[104:107], a[244:247], v[48:51]\n"
"ds_read_b128 v[208:211], v228 offset:16384\n"
"v_cvt_pk_bf16_f32 v133, v158, v159\n"
"v_mfma_f32_16x16x32_bf16 v[48:51], v[108:111], a[248:251], v[48:51]\n"
"ds_read_b128 v[216:219], v228 offset:16896\n"
"v_cvt_pk_bf16_f32 v141, v162, v163\n"
"v_mfma_f32_16x16x32_bf16 v[80:83], v[112:115], a[244:247], v[80:83]\n"
"ds_read_b128 v[220:223], v229 offset:16896\n"
"v_cvt_pk_bf16_f32 v127, v154, v155\n"
"v_mfma_f32_16x16x32_bf16 v[80:83], v[116:119], a[248:251], v[80:83]\n"
"ds_read_b128 v[212:215], v229 offset:16384\n"
"v_cvt_pk_bf16_f32 v149, v166, v167\n"
"v_mfma_f32_16x16x32_bf16 v[84:87], v[116:119], v[224:227], v[84:87]\n"
"v_exp_f32 v176, v176\n"
"v_mfma_f32_16x16x32_bf16 v[84:87], v[112:115], a[252:255], v[84:87]\n"
"v_exp_f32 v169, v169\n"
"v_mfma_f32_16x16x32_bf16 v[52:55], v[108:111], v[224:227], v[52:55]\n"
"ds_read_b64_tr_b16 a[246:247], v234 offset:4096\n"
"v_cvt_pk_bf16_f32 v148, v164, v165\n"
"v_mfma_f32_16x16x32_bf16 v[52:55], v[104:107], a[252:255], v[52:55]\n"
"v_exp_f32 v177, v177\n"
"v_mfma_f32_16x16x32_bf16 v[20:23], v[100:103], v[224:227], v[20:23]\n"
"ds_read_b64_tr_b16 a[250:251], v238 offset:4096\n"
"v_cvt_pk_bf16_f32 v150, v176, v177\n"
"v_mfma_f32_16x16x32_bf16 v[20:23], v[96:99], a[252:255], v[20:23]\n"
"v_exp_f32 v174, v174\n"
"v_mfma_f32_16x16x32_bf16 a[228:231], v[120:123], a[252:255], a[228:231]\n"
"s_waitcnt lgkmcnt(3)\n"
"ds_read_b64_tr_b16 a[254:255], v235 offset:4096\n"
"v_cvt_pk_bf16_f32 v132, v156, v157\n"
"v_mfma_f32_16x16x32_bf16 v[152:155], v[208:211], a[0:3], v[180:183]\n"
"s_waitcnt lgkmcnt(3)\n"
"ds_read_b64_tr_b16 a[248:249], v238 offset:0\n"
"v_cvt_pk_bf16_f32 v134, v168, v169\n"
"v_mfma_f32_16x16x32_bf16 v[152:155], v[212:215], a[4:7], v[152:155]\n"
"v_exp_f32 v172, v172\n"
"v_mfma_f32_16x16x32_bf16 v[152:155], v[216:219], a[8:11], v[152:155]\n"
"ds_read_b64_tr_b16 a[244:245], v234 offset:0\n"
"v_cvt_pk_bf16_f32 v140, v160, v161\n"
"v_mfma_f32_16x16x32_bf16 v[152:155], v[220:223], a[12:15], v[152:155]\n"
"v_exp_f32 v175, v175\n"
"v_mfma_f32_16x16x32_bf16 v[156:159], v[216:219], a[24:27], v[184:187]\n"
"ds_read_b64_tr_b16 a[252:253], v235 offset:0\n"
"v_cvt_pk_bf16_f32 v143, v174, v175\n"
"v_mfma_f32_16x16x32_bf16 v[156:159], v[220:223], a[28:31], v[156:159]\n"
"ds_read_b64_tr_b16 v[224:225], v239 offset:0\n"
"v_mfma_f32_16x16x32_bf16 v[156:159], v[208:211], a[16:19], v[156:159]\n"
"s_add_u32 m0, s44, 98304\n"
"ds_read_b64_tr_b16 v[226:227], v239 offset:4096\n"
"v_mfma_f32_16x16x32_bf16 v[156:159], v[212:215], a[20:23], v[156:159]\n"
"buffer_load_dwordx4 v240, s[20:23], s56 offen nt lds\n"
"v_mfma_f32_16x16x32_bf16 v[160:163], v[208:211], a[32:35], v[188:191]\n"
"v_exp_f32 v178, v178\n"
"v_mfma_f32_16x16x32_bf16 v[160:163], v[212:215], a[36:39], v[160:163]\n"
"v_exp_f32 v171, v171\n"
"v_mfma_f32_16x16x32_bf16 v[160:163], v[216:219], a[40:43], v[160:163]\n"
"s_add_u32 s40, s56, s52\n"
"v_mfma_f32_16x16x32_bf16 v[160:163], v[220:223], a[44:47], v[160:163]\n"
"buffer_load_dwordx4 v207, s[20:23], s40 offen offset:1024 nt lds\n"
"v_mfma_f32_16x16x32_bf16 v[164:167], v[216:219], a[200:203], v[192:195]\n"
"v_exp_f32 v170, v170\n"
"v_mfma_f32_16x16x32_bf16 v[164:167], v[220:223], a[204:207], v[164:167]\n"
"v_exp_f32 v153, v153\n"
"v_mfma_f32_16x16x32_bf16 v[164:167], v[208:211], a[192:195], v[164:167]\n"
"v_exp_f32 v179, v179\n"
"v_mfma_f32_16x16x32_bf16 v[164:167], v[212:215], a[196:199], v[164:167]\n"
"s_waitcnt lgkmcnt(0)\n"
"v_exp_f32 v158, v158\n"
"v_mfma_f32_16x16x32_bf16 a[216:219], v[124:127], a[244:247], a[216:219]\n"
"ds_read_b128 v[216:219], v228 offset:20992\n"
"v_cvt_pk_bf16_f32 v135, v170, v171\n"
"v_mfma_f32_16x16x32_bf16 v[8:11], v[128:131], a[244:247], v[8:11]\n"
"v_exp_f32 v173, v173\n"
"v_mfma_f32_16x16x32_bf16 v[8:11], v[132:135], a[248:251], v[8:11]\n"
"ds_read_b128 v[208:211], v228 offset:20480\n"
"v_cvt_pk_bf16_f32 v142, v172, v173\n"
"v_mfma_f32_16x16x32_bf16 v[40:43], v[136:139], a[244:247], v[40:43]\n"
"v_exp_f32 v162, v162\n"
"v_mfma_f32_16x16x32_bf16 v[40:43], v[140:143], a[248:251], v[40:43]\n"
"ds_read_b128 v[220:223], v229 offset:20992\n"
"v_cvt_pk_bf16_f32 v151, v178, v179\n"
"v_mfma_f32_16x16x32_bf16 v[72:75], v[144:147], a[244:247], v[72:75]\n"
"s_add_u32 s40, s56, s53\n"
"ds_read_b128 v[212:215], v229 offset:20480\n"
"v_mfma_f32_16x16x32_bf16 v[72:75], v[148:151], a[248:251], v[72:75]\n"
"buffer_load_dwordx4 v240, s[20:23], s40 offen offset:2048 nt lds\n"
"v_mfma_f32_16x16x32_bf16 v[76:79], v[148:151], v[224:227], v[76:79]\n"
"v_exp_f32 v163, v163\n"
"v_mfma_f32_16x16x32_bf16 v[76:79], v[144:147], a[252:255], v[76:79]\n"
"v_exp_f32 v156, v156\n"
"v_mfma_f32_16x16x32_bf16 v[44:47], v[140:143], v[224:227], v[44:47]\n"
"v_exp_f32 v160, v160\n"
"v_mfma_f32_16x16x32_bf16 v[44:47], v[136:139], a[252:255], v[44:47]\n"
"v_exp_f32 v155, v155\n"
"v_mfma_f32_16x16x32_bf16 v[12:15], v[132:135], v[224:227], v[12:15]\n"
"ds_read_b64_tr_b16 v[226:227], v239 offset:4608\n"
"v_mfma_f32_16x16x32_bf16 v[12:15], v[128:131], a[252:255], v[12:15]\n"
"ds_read_b64_tr_b16 a[248:249], v238 offset:512\n"
"v_mfma_f32_16x16x32_bf16 a[220:223], v[124:127], a[252:255], a[220:223]\n"
"v_exp_f32 v154, v154\n"
"v_smfmac_f32_16x16x64_bf16 v[196:199], a[240:243], v[128:135], v241\n"
"ds_read_b64_tr_b16 a[250:251], v238 offset:4608\n"
"v_mfma_f32_16x16x32_bf16 v[202:205], a[240:243], v[124:127], v[202:205]\n"
"s_waitcnt lgkmcnt(3)\n"
"v_exp_f32 v152, v152\n"
"v_mfma_f32_16x16x32_bf16 v[168:171], v[216:219], a[8:11], v[180:183]\n"
"ds_read_b64_tr_b16 a[244:245], v234 offset:512\n"
"v_cvt_pk_bf16_f32 v96, v152, v153\n"
"v_mfma_f32_16x16x32_bf16 v[168:171], v[220:223], a[12:15], v[168:171]\n"
"v_exp_f32 v165, v165\n"
"v_mfma_f32_16x16x32_bf16 v[168:171], v[208:211], a[0:3], v[168:171]\n"
"ds_read_b64_tr_b16 a[246:247], v234 offset:4608\n"
"v_cvt_pk_bf16_f32 v113, v162, v163\n"
"v_mfma_f32_16x16x32_bf16 v[168:171], v[212:215], a[4:7], v[168:171]\n"
"ds_read_b64_tr_b16 a[254:255], v235 offset:4608\n"
"v_mfma_f32_16x16x32_bf16 v[172:175], v[208:211], a[16:19], v[184:187]\n"
"ds_read_b64_tr_b16 v[224:225], v239 offset:512\n"
"v_cvt_pk_bf16_f32 v97, v154, v155\n"
"v_mfma_f32_16x16x32_bf16 v[172:175], v[212:215], a[20:23], v[172:175]\n"
"v_mfma_f32_16x16x32_bf16 v[172:175], v[216:219], a[24:27], v[172:175]\n"
"s_add_u32 s40, s56, s54\n"
"ds_read_b64_tr_b16 a[252:253], v235 offset:512\n"
"v_mfma_f32_16x16x32_bf16 v[172:175], v[220:223], a[28:31], v[172:175]\n"
"buffer_load_dwordx4 v207, s[20:23], s40 offen offset:3072 nt lds\n"
"v_mfma_f32_16x16x32_bf16 v[176:179], v[216:219], a[40:43], v[188:191]\n"
"s_add_u32 s55, s55, s59\n"
"v_exp_f32 v157, v157\n"
"v_mfma_f32_16x16x32_bf16 v[176:179], v[220:223], a[44:47], v[176:179]\n"
"s_or_b32 s55, s55, s58\n"
"v_exp_f32 v171, v171\n"
"v_mfma_f32_16x16x32_bf16 v[176:179], v[208:211], a[32:35], v[176:179]\n"
"s_xor_b32 s56, s55, s57\n"
"v_exp_f32 v159, v159\n"
"v_mfma_f32_16x16x32_bf16 v[176:179], v[212:215], a[36:39], v[176:179]\n"
"v_exp_f32 v167, v167\n"
"v_mfma_f32_16x16x32_bf16 v[152:155], v[208:211], a[192:195], v[192:195]\n"
"v_cvt_pk_bf16_f32 v105, v158, v159\n"
"v_mfma_f32_16x16x32_bf16 v[152:155], v[212:215], a[196:199], v[152:155]\n"
"ds_read_b128 v[208:211], v230 offset:16384\n"
"v_cvt_pk_bf16_f32 v104, v156, v157\n"
"v_mfma_f32_16x16x32_bf16 v[152:155], v[216:219], a[200:203], v[152:155]\n"
"v_exp_f32 v166, v166\n"
"v_mfma_f32_16x16x32_bf16 v[152:155], v[220:223], a[204:207], v[152:155]\n"
"s_waitcnt lgkmcnt(1)\n"
"ds_read_b128 v[216:219], v230 offset:16896\n"
"v_mfma_f32_16x16x32_bf16 a[232:235], v[124:127], a[244:247], a[232:235]\n"
"v_exp_f32 v169, v169\n"
"v_mfma_f32_16x16x32_bf16 v[24:27], v[128:131], a[244:247], v[24:27]\n"
"ds_read_b128 v[220:223], v231 offset:16896\n"
"v_mfma_f32_16x16x32_bf16 v[24:27], v[132:135], a[248:251], v[24:27]\n"
"v_exp_f32 v164, v164\n"
"v_mfma_f32_16x16x32_bf16 v[56:59], v[136:139], a[244:247], v[56:59]\n"
"ds_read_b128 v[212:215], v231 offset:16384\n"
"v_mfma_f32_16x16x32_bf16 v[56:59], v[140:143], a[248:251], v[56:59]\n"
"v_exp_f32 v178, v178\n"
"v_mfma_f32_16x16x32_bf16 v[88:91], v[144:147], a[244:247], v[88:91]\n"
"s_add_u32 m0, s44, 49152\n"
"ds_read_b64_tr_b16 a[246:247], v232 offset:4096\n"
"v_mfma_f32_16x16x32_bf16 v[88:91], v[148:151], a[248:251], v[88:91]\n"
"buffer_load_dwordx4 v240, s[16:19], s56 offen nt lds\n"
"v_mfma_f32_16x16x32_bf16 v[92:95], v[148:151], v[224:227], v[92:95]\n"
"v_exp_f32 v175, v175\n"
"v_mfma_f32_16x16x32_bf16 v[92:95], v[144:147], a[252:255], v[92:95]\n"
"v_exp_f32 v168, v168\n"
"v_mfma_f32_16x16x32_bf16 v[60:63], v[140:143], v[224:227], v[60:63]\n"
"v_exp_f32 v161, v161\n"
"v_mfma_f32_16x16x32_bf16 v[60:63], v[136:139], a[252:255], v[60:63]\n"
"v_exp_f32 v177, v177\n"
"v_mfma_f32_16x16x32_bf16 v[28:31], v[132:135], v[224:227], v[28:31]\n"
"ds_read_b64_tr_b16 v[226:227], v237 offset:4096\n"
"v_mfma_f32_16x16x32_bf16 v[28:31], v[128:131], a[252:255], v[28:31]\n"
"v_exp_f32 v172, v172\n"
"v_mfma_f32_16x16x32_bf16 a[236:239], v[124:127], a[252:255], a[236:239]\n"
"ds_read_b64_tr_b16 a[250:251], v236 offset:4096\n"
"v_cvt_pk_bf16_f32 v120, v164, v165\n"
"v_smfmac_f32_16x16x64_bf16 v[198:201], a[240:243], v[136:143], v241\n"
"s_waitcnt lgkmcnt(2)\n"
"ds_read_b64_tr_b16 a[252:253], v233 offset:0\n"
"v_mfma_f32_16x16x32_bf16 v[156:159], v[208:211], a[0:3], v[180:183]\n"
"ds_read_b64_tr_b16 a[248:249], v236 offset:0\n"
"v_cvt_pk_bf16_f32 v121, v166, v167\n"
"v_mfma_f32_16x16x32_bf16 v[156:159], v[212:215], a[4:7], v[156:159]\n"
"ds_read_b64_tr_b16 v[224:225], v237 offset:0\n"
"v_cvt_pk_bf16_f32 v98, v168, v169\n"
"v_mfma_f32_16x16x32_bf16 v[156:159], v[216:219], a[8:11], v[156:159]\n"
"ds_read_b64_tr_b16 a[254:255], v233 offset:4096\n"
"v_cvt_pk_bf16_f32 v112, v160, v161\n"
"v_mfma_f32_16x16x32_bf16 v[156:159], v[220:223], a[12:15], v[156:159]\n"
"ds_read_b64_tr_b16 a[244:245], v232 offset:0\n"
"v_mfma_f32_16x16x32_bf16 v[160:163], v[216:219], a[24:27], v[184:187]\n"
"v_exp_f32 v176, v176\n"
"v_mfma_f32_16x16x32_bf16 v[160:163], v[220:223], a[28:31], v[160:163]\n"
"v_exp_f32 v152, v152\n"
"v_mfma_f32_16x16x32_bf16 v[160:163], v[208:211], a[16:19], v[160:163]\n"
"s_add_u32 s40, s56, s52\n"
"v_cvt_pk_bf16_f32 v114, v176, v177\n"
"v_mfma_f32_16x16x32_bf16 v[160:163], v[212:215], a[20:23], v[160:163]\n"
"buffer_load_dwordx4 v207, s[16:19], s40 offen offset:1024 nt lds\n"
"v_mfma_f32_16x16x32_bf16 v[164:167], v[208:211], a[32:35], v[188:191]\n"
"v_exp_f32 v170, v170\n"
"v_mfma_f32_16x16x32_bf16 v[164:167], v[212:215], a[36:39], v[164:167]\n"
"v_exp_f32 v173, v173\n"
"v_mfma_f32_16x16x32_bf16 v[164:167], v[216:219], a[40:43], v[164:167]\n"
"v_exp_f32 v153, v153\n"
"v_mfma_f32_16x16x32_bf16 v[164:167], v[220:223], a[44:47], v[164:167]\n"
"s_waitcnt lgkmcnt(0)\n"
"v_exp_f32 v174, v174\n"
"v_mfma_f32_16x16x32_bf16 a[208:211], v[124:127], a[244:247], a[208:211]\n"
"ds_read_b128 v[208:211], v230 offset:20480\n"
"v_cvt_pk_bf16_f32 v107, v174, v175\n"
"v_mfma_f32_16x16x32_bf16 v[0:3], v[128:131], a[244:247], v[0:3]\n"
"v_exp_f32 v156, v156\n"
"v_mfma_f32_16x16x32_bf16 v[0:3], v[132:135], a[248:251], v[0:3]\n"
"ds_read_b128 v[220:223], v231 offset:20992\n"
"v_cvt_pk_bf16_f32 v106, v172, v173\n"
"v_mfma_f32_16x16x32_bf16 v[32:35], v[136:139], a[244:247], v[32:35]\n"
"v_exp_f32 v161, v161\n"
"v_mfma_f32_16x16x32_bf16 v[32:35], v[140:143], a[248:251], v[32:35]\n"
"ds_read_b128 v[216:219], v230 offset:20992\n"
"v_cvt_pk_bf16_f32 v122, v152, v153\n"
"v_mfma_f32_16x16x32_bf16 v[64:67], v[144:147], a[244:247], v[64:67]\n"
"s_add_u32 s40, s56, s53\n"
"ds_read_b128 v[212:215], v231 offset:20480\n"
"v_mfma_f32_16x16x32_bf16 v[64:67], v[148:151], a[248:251], v[64:67]\n"
"buffer_load_dwordx4 v240, s[16:19], s40 offen offset:2048 nt lds\n"
"v_mfma_f32_16x16x32_bf16 v[68:71], v[148:151], v[224:227], v[68:71]\n"
"v_exp_f32 v159, v159\n"
"v_mfma_f32_16x16x32_bf16 v[68:71], v[144:147], a[252:255], v[68:71]\n"
"v_exp_f32 v155, v155\n"
"v_mfma_f32_16x16x32_bf16 v[36:39], v[140:143], v[224:227], v[36:39]\n"
"v_exp_f32 v160, v160\n"
"v_mfma_f32_16x16x32_bf16 v[36:39], v[136:139], a[252:255], v[36:39]\n"
"v_exp_f32 v179, v179\n"
"v_mfma_f32_16x16x32_bf16 v[4:7], v[132:135], v[224:227], v[4:7]\n"
"ds_read_b64_tr_b16 a[244:245], v232 offset:512\n"
"ds_read_b64_tr_b16 v[224:225], v237 offset:512\n"
"v_mfma_f32_16x16x32_bf16 v[4:7], v[128:131], a[252:255], v[4:7]\n"
"v_exp_f32 v158, v158\n"
"v_mfma_f32_16x16x32_bf16 a[212:215], v[124:127], a[252:255], a[212:215]\n"
"ds_read_b64_tr_b16 a[246:247], v232 offset:4608\n"
"ds_read_b64_tr_b16 a[250:251], v236 offset:4608\n"
"v_smfmac_f32_16x16x64_bf16 v[200:203], a[240:243], v[144:151], v241\n"
"s_waitcnt lgkmcnt(4)\n"
"ds_read_b64_tr_b16 v[226:227], v237 offset:4608\n"
"v_cvt_pk_bf16_f32 v99, v170, v171\n"
"v_mfma_f32_16x16x32_bf16 v[168:171], v[208:211], a[0:3], v[180:183]\n"
"v_exp_f32 v164, v164\n"
"v_mfma_f32_16x16x32_bf16 v[168:171], v[212:215], a[4:7], v[168:171]\n"
"ds_read_b64_tr_b16 a[248:249], v236 offset:512\n"
"ds_read_b64_tr_b16 a[254:255], v233 offset:4608\n"
"v_mfma_f32_16x16x32_bf16 v[168:171], v[216:219], a[8:11], v[168:171]\n"
"s_add_u32 s40, s56, s54\n"
"ds_read_b64_tr_b16 a[252:253], v233 offset:512\n"
"v_mfma_f32_16x16x32_bf16 v[168:171], v[220:223], a[12:15], v[168:171]\n"
"buffer_load_dwordx4 v207, s[16:19], s40 offen offset:3072 nt lds\n"
"v_mfma_f32_16x16x32_bf16 v[172:175], v[216:219], a[24:27], v[184:187]\n"
"v_exp_f32 v154, v154\n"
"v_mfma_f32_16x16x32_bf16 v[172:175], v[220:223], a[28:31], v[172:175]\n"
"v_exp_f32 v167, v167\n"
"v_mfma_f32_16x16x32_bf16 v[172:175], v[208:211], a[16:19], v[172:175]\n"
"v_exp_f32 v163, v163\n"
"v_mfma_f32_16x16x32_bf16 v[172:175], v[212:215], a[20:23], v[172:175]\n"
"s_cbranch_vccnz LPCL1_%=\nLPKL1_%=:\n"
"v_cvt_pk_bf16_f32 v115, v178, v179\n"
"v_mfma_f32_16x16x32_bf16 v[176:179], v[208:211], a[32:35], v[188:191]\n"
"v_exp_f32 v165, v165\n"
"v_mfma_f32_16x16x32_bf16 v[176:179], v[212:215], a[36:39], v[176:179]\n"
"v_exp_f32 v157, v157\n"
"v_mfma_f32_16x16x32_bf16 v[176:179], v[216:219], a[40:43], v[176:179]\n"
"v_exp_f32 v166, v166\n"
"v_mfma_f32_16x16x32_bf16 v[176:179], v[220:223], a[44:47], v[176:179]\n"
"s_waitcnt lgkmcnt(0)\n"
"v_mfma_f32_16x16x32_bf16 a[224:227], v[124:127], a[244:247], a[224:227]\n"
"v_exp_f32 v162, v162\n"
"v_mfma_f32_16x16x32_bf16 v[16:19], v[128:131], a[244:247], v[16:19]\n"
"s_waitcnt vmcnt(8)\n"
"v_exp_f32 v168, v168\n"
"v_mfma_f32_16x16x32_bf16 v[16:19], v[132:135], a[248:251], v[16:19]\n"
"s_barrier\n"
"v_mfma_f32_16x16x32_bf16 v[48:51], v[136:139], a[244:247], v[48:51]\n"
"ds_read_b128 v[208:211], v228 offset:32768\n"
"v_cvt_pk_bf16_f32 v101, v158, v159\n"
"v_mfma_f32_16x16x32_bf16 v[48:51], v[140:143], a[248:251], v[48:51]\n"
"ds_read_b128 v[216:219], v228 offset:33280\n"
"v_cvt_pk_bf16_f32 v109, v162, v163\n"
"v_mfma_f32_16x16x32_bf16 v[80:83], v[144:147], a[244:247], v[80:83]\n"
"ds_read_b128 v[220:223], v229 offset:33280\n"
"v_cvt_pk_bf16_f32 v123, v154, v155\n"
"v_mfma_f32_16x16x32_bf16 v[80:83], v[148:151], a[248:251], v[80:83]\n"
"ds_read_b128 v[212:215], v229 offset:32768\n"
"v_cvt_pk_bf16_f32 v117, v166, v167\n"
"v_mfma_f32_16x16x32_bf16 v[84:87], v[148:151], v[224:227], v[84:87]\n"
"v_exp_f32 v176, v176\n"
"v_mfma_f32_16x16x32_bf16 v[84:87], v[144:147], a[252:255], v[84:87]\n"
"v_exp_f32 v169, v169\n"
"v_mfma_f32_16x16x32_bf16 v[52:55], v[140:143], v[224:227], v[52:55]\n"
"ds_read_b64_tr_b16 a[246:247], v234 offset:20480\n"
"v_cvt_pk_bf16_f32 v116, v164, v165\n"
"v_mfma_f32_16x16x32_bf16 v[52:55], v[136:139], a[252:255], v[52:55]\n"
"v_exp_f32 v177, v177\n"
"v_mfma_f32_16x16x32_bf16 v[20:23], v[132:135], v[224:227], v[20:23]\n"
"ds_read_b64_tr_b16 a[250:251], v238 offset:20480\n"
"v_cvt_pk_bf16_f32 v118, v176, v177\n"
"v_mfma_f32_16x16x32_bf16 v[20:23], v[128:131], a[252:255], v[20:23]\n"
"v_exp_f32 v174, v174\n"
"v_mfma_f32_16x16x32_bf16 a[228:231], v[124:127], a[252:255], a[228:231]\n"
"s_waitcnt lgkmcnt(3)\n"
"ds_read_b64_tr_b16 a[254:255], v235 offset:20480\n"
"v_cvt_pk_bf16_f32 v100, v156, v157\n"
"LAY2_%=:\n"
"v_mfma_f32_16x16x32_bf16 v[152:155], v[208:211], a[0:3], v[180:183]\n"
"s_waitcnt lgkmcnt(3)\n"
"ds_read_b64_tr_b16 a[248:249], v238 offset:16384\n"
"v_cvt_pk_bf16_f32 v102, v168, v169\n"
"v_mfma_f32_16x16x32_bf16 v[152:155], v[212:215], a[4:7], v[152:155]\n"
"v_exp_f32 v172, v172\n"
"v_mfma_f32_16x16x32_bf16 v[152:155], v[216:219], a[8:11], v[152:155]\n"
"ds_read_b64_tr_b16 a[244:245], v234 offset:16384\n"
"v_cvt_pk_bf16_f32 v108, v160, v161\n"
"v_mfma_f32_16x16x32_bf16 v[152:155], v[220:223], a[12:15], v[152:155]\n"
"v_exp_f32 v175, v175\n"
"v_mfma_f32_16x16x32_bf16 v[156:159], v[216:219], a[24:27], v[184:187]\n"
"ds_read_b64_tr_b16 a[252:253], v235 offset:16384\n"
"v_cvt_pk_bf16_f32 v111, v174, v175\n"
"v_mfma_f32_16x16x32_bf16 v[156:159], v[220:223], a[28:31], v[156:159]\n"
"ds_read_b64_tr_b16 v[224:225], v239 offset:16384\n"
"v_mfma_f32_16x16x32_bf16 v[156:159], v[208:211], a[16:19], v[156:159]\n"
"s_add_u32 m0, s44, 114688\n"
"ds_read_b64_tr_b16 v[226:227], v239 offset:20480\n"
"v_mfma_f32_16x16x32_bf16 v[156:159], v[212:215], a[20:23], v[156:159]\n"
"buffer_load_dwordx4 v240, s[20:23], s56 offen nt lds\n"
"v_mfma_f32_16x16x32_bf16 v[160:163], v[208:211], a[32:35], v[188:191]\n"
"v_exp_f32 v178, v178\n"
"v_mfma_f32_16x16x32_bf16 v[160:163], v[212:215], a[36:39], v[160:163]\n"
"v_exp_f32 v171, v171\n"
"v_mfma_f32_16x16x32_bf16 v[160:163], v[216:219], a[40:43], v[160:163]\n"
"s_add_u32 s40, s56, s52\n"
"v_mfma_f32_16x16x32_bf16 v[160:163], v[220:223], a[44:47], v[160:163]\n"
"buffer_load_dwordx4 v207, s[20:23], s40 offen offset:1024 nt lds\n"
"v_mfma_f32_16x16x32_bf16 v[164:167], v[216:219], a[200:203], v[192:195]\n"
"v_exp_f32 v170, v170\n"
"v_mfma_f32_16x16x32_bf16 v[164:167], v[220:223], a[204:207], v[164:167]\n"
"v_exp_f32 v153, v153\n"
"v_mfma_f32_16x16x32_bf16 v[164:167], v[208:211], a[192:195], v[164:167]\n"
"v_exp_f32 v179, v179\n"
"v_mfma_f32_16x16x32_bf16 v[164:167], v[212:215], a[196:199], v[164:167]\n"
"s_waitcnt lgkmcnt(0)\n"
"v_exp_f32 v158, v158\n"
"v_mfma_f32_16x16x32_bf16 a[216:219], v[120:123], a[244:247], a[216:219]\n"
"ds_read_b128 v[216:219], v228 offset:37376\n"
"v_cvt_pk_bf16_f32 v103, v170, v171\n"
"v_mfma_f32_16x16x32_bf16 v[8:11], v[96:99], a[244:247], v[8:11]\n"
"v_exp_f32 v173, v173\n"
"v_mfma_f32_16x16x32_bf16 v[8:11], v[100:103], a[248:251], v[8:11]\n"
"ds_read_b128 v[208:211], v228 offset:36864\n"
"v_cvt_pk_bf16_f32 v110, v172, v173\n"
"v_mfma_f32_16x16x32_bf16 v[40:43], v[104:107], a[244:247], v[40:43]\n"
"v_exp_f32 v162, v162\n"
"v_mfma_f32_16x16x32_bf16 v[40:43], v[108:111], a[248:251], v[40:43]\n"
"ds_read_b128 v[220:223], v229 offset:37376\n"
"v_cvt_pk_bf16_f32 v119, v178, v179\n"
"v_mfma_f32_16x16x32_bf16 v[72:75], v[112:115], a[244:247], v[72:75]\n"
"s_add_u32 s40, s56, s53\n"
"ds_read_b128 v[212:215], v229 offset:36864\n"
"v_mfma_f32_16x16x32_bf16 v[72:75], v[116:119], a[248:251], v[72:75]\n"
"buffer_load_dwordx4 v240, s[20:23], s40 offen offset:2048 nt lds\n"
"v_mfma_f32_16x16x32_bf16 v[76:79], v[116:119], v[224:227], v[76:79]\n"
"v_exp_f32 v163, v163\n"
"v_mfma_f32_16x16x32_bf16 v[76:79], v[112:115], a[252:255], v[76:79]\n"
"v_exp_f32 v156, v156\n"
"v_mfma_f32_16x16x32_bf16 v[44:47], v[108:111], v[224:227], v[44:47]\n"
"v_exp_f32 v160, v160\n"
"v_mfma_f32_16x16x32_bf16 v[44:47], v[104:107], a[252:255], v[44:47]\n"
"v_exp_f32 v155, v155\n"
"v_mfma_f32_16x16x32_bf16 v[12:15], v[100:103], v[224:227], v[12:15]\n"
"ds_read_b64_tr_b16 v[226:227], v239 offset:20992\n"
"v_mfma_f32_16x16x32_bf16 v[12:15], v[96:99], a[252:255], v[12:15]\n"
"ds_read_b64_tr_b16 a[248:249], v238 offset:16896\n"
"v_mfma_f32_16x16x32_bf16 a[220:223], v[120:123], a[252:255], a[220:223]\n"
"v_exp_f32 v154, v154\n"
"v_smfmac_f32_16x16x64_bf16 v[196:199], a[240:243], v[96:103], v241\n"
"ds_read_b64_tr_b16 a[250:251], v238 offset:20992\n"
"v_mfma_f32_16x16x32_bf16 v[202:205], a[240:243], v[120:123], v[202:205]\n"
"s_waitcnt lgkmcnt(3)\n"
"v_exp_f32 v152, v152\n"
"v_mfma_f32_16x16x32_bf16 v[168:171], v[216:219], a[8:11], v[180:183]\n"
"ds_read_b64_tr_b16 a[244:245], v234 offset:16896\n"
"v_cvt_pk_bf16_f32 v128, v152, v153\n"
"v_mfma_f32_16x16x32_bf16 v[168:171], v[220:223], a[12:15], v[168:171]\n"
"v_exp_f32 v165, v165\n"
"v_mfma_f32_16x16x32_bf16 v[168:171], v[208:211], a[0:3], v[168:171]\n"
"ds_read_b64_tr_b16 a[246:247], v234 offset:20992\n"
"v_cvt_pk_bf16_f32 v145, v162, v163\n"
"v_mfma_f32_16x16x32_bf16 v[168:171], v[212:215], a[4:7], v[168:171]\n"
"ds_read_b64_tr_b16 a[254:255], v235 offset:20992\n"
"v_mfma_f32_16x16x32_bf16 v[172:175], v[208:211], a[16:19], v[184:187]\n"
"ds_read_b64_tr_b16 v[224:225], v239 offset:16896\n"
"v_cvt_pk_bf16_f32 v129, v154, v155\n"
"v_mfma_f32_16x16x32_bf16 v[172:175], v[212:215], a[20:23], v[172:175]\n"
"v_mfma_f32_16x16x32_bf16 v[172:175], v[216:219], a[24:27], v[172:175]\n"
"s_add_u32 s40, s56, s54\n"
"ds_read_b64_tr_b16 a[252:253], v235 offset:16896\n"
"v_mfma_f32_16x16x32_bf16 v[172:175], v[220:223], a[28:31], v[172:175]\n"
"buffer_load_dwordx4 v207, s[20:23], s40 offen offset:3072 nt lds\n"
"v_mfma_f32_16x16x32_bf16 v[176:179], v[216:219], a[40:43], v[188:191]\n"
"s_add_u32 s55, s55, s59\n"
"v_exp_f32 v157, v157\n"
"v_mfma_f32_16x16x32_bf16 v[176:179], v[220:223], a[44:47], v[176:179]\n"
"s_or_b32 s55, s55, s58\n"
"v_exp_f32 v171, v171\n"
"v_mfma_f32_16x16x32_bf16 v[176:179], v[208:211], a[32:35], v[176:179]\n"
"s_xor_b32 s56, s55, s57\n"
"v_exp_f32 v159, v159\n"
"v_mfma_f32_16x16x32_bf16 v[176:179], v[212:215], a[36:39], v[176:179]\n"
"s_cmp_ge_u32 s47, 15\n"
"v_exp_f32 v167, v167\n"
"v_mfma_f32_16x16x32_bf16 v[152:155], v[208:211], a[192:195], v[192:195]\n"
"s_cbranch_scc1 LSW_%=\nLSWK_%=:\n"
"v_cvt_pk_bf16_f32 v137, v158, v159\n"
"v_mfma_f32_16x16x32_bf16 v[152:155], v[212:215], a[196:199], v[152:155]\n"
"ds_read_b128 v[208:211], v230 offset:32768\n"
"v_cvt_pk_bf16_f32 v136, v156, v157\n"
"v_mfma_f32_16x16x32_bf16 v[152:155], v[216:219], a[200:203], v[152:155]\n"
"v_exp_f32 v166, v166\n"
"v_mfma_f32_16x16x32_bf16 v[152:155], v[220:223], a[204:207], v[152:155]\n"
"s_waitcnt lgkmcnt(1)\n"
"ds_read_b128 v[216:219], v230 offset:33280\n"
"v_mfma_f32_16x16x32_bf16 a[232:235], v[120:123], a[244:247], a[232:235]\n"
"v_exp_f32 v169, v169\n"
"v_mfma_f32_16x16x32_bf16 v[24:27], v[96:99], a[244:247], v[24:27]\n"
"ds_read_b128 v[220:223], v231 offset:33280\n"
"v_mfma_f32_16x16x32_bf16 v[24:27], v[100:103], a[248:251], v[24:27]\n"
"v_exp_f32 v164, v164\n"
"v_mfma_f32_16x16x32_bf16 v[56:59], v[104:107], a[244:247], v[56:59]\n"
"ds_read_b128 v[212:215], v231 offset:32768\n"
"v_mfma_f32_16x16x32_bf16 v[56:59], v[108:111], a[248:251], v[56:59]\n"
"v_exp_f32 v178, v178\n"
"v_mfma_f32_16x16x32_bf16 v[88:91], v[112:115], a[244:247], v[88:91]\n"
"s_mov_b32 m0, s44\n"
"ds_read_b64_tr_b16 a[246:247], v232 offset:20480\n"
"v_mfma_f32_16x16x32_bf16 v[88:91], v[116:119], a[248:251], v[88:91]\n"
"buffer_load_dwordx4 v240, s[16:19], s56 offen nt lds\n"
"v_mfma_f32_16x16x32_bf16 v[92:95], v[116:119], v[224:227], v[92:95]\n"
"v_exp_f32 v175, v175\n"
"v_mfma_f32_16x16x32_bf16 v[92:95], v[112:115], a[252:255], v[92:95]\n"
"v_exp_f32 v168, v168\n"
"v_mfma_f32_16x16x32_bf16 v[60:63], v[108:111], v[224:227], v[60:63]\n"
"v_exp_f32 v161, v161\n"
"v_mfma_f32_16x16x32_bf16 v[60:63], v[104:107], a[252:255], v[60:63]\n"
"v_exp_f32 v177, v177\n"
"v_mfma_f32_16x16x32_bf16 v[28:31], v[100:103], v[224:227], v[28:31]\n"
"ds_read_b64_tr_b16 v[226:227], v237 offset:20480\n"
"v_mfma_f32_16x16x32_bf16 v[28:31], v[96:99], a[252:255], v[28:31]\n"
"v_exp_f32 v172, v172\n"
"v_mfma_f32_16x16x32_bf16 a[236:239], v[120:123], a[252:255], a[236:239]\n"
"ds_read_b64_tr_b16 a[250:251], v236 offset:20480\n"
"v_cvt_pk_bf16_f32 v124, v164, v165\n"
"v_smfmac_f32_16x16x64_bf16 v[198:201], a[240:243], v[104:111], v241\n"
"s_waitcnt lgkmcnt(2)\n"
"ds_read_b64_tr_b16 a[252:253], v233 offset:16384\n"
"v_mfma_f32_16x16x32_bf16 v[156:159], v[208:211], a[0:3], v[180:183]\n"
"ds_read_b64_tr_b16 a[248:249], v236 offset:16384\n"
"v_cvt_pk_bf16_f32 v125, v166, v167\n"
"v_mfma_f32_16x16x32_bf16 v[156:159], v[212:215], a[4:7], v[156:159]\n"
"ds_read_b64_tr_b16 v[224:225], v237 offset:16384\n"
"v_cvt_pk_bf16_f32 v130, v168, v169\n"
"v_mfma_f32_16x16x32_bf16 v[156:159], v[216:219], a[8:11], v[156:159]\n"
"ds_read_b64_tr_b16 a[254:255], v233 offset:20480\n"
"v_cvt_pk_bf16_f32 v144, v160, v161\n"
"v_mfma_f32_16x16x32_bf16 v[156:159], v[220:223], a[12:15], v[156:159]\n"
"ds_read_b64_tr_b16 a[244:245], v232 offset:16384\n"
"v_mfma_f32_16x16x32_bf16 v[160:163], v[216:219], a[24:27], v[184:187]\n"
"v_exp_f32 v176, v176\n"
"v_mfma_f32_16x16x32_bf16 v[160:163], v[220:223], a[28:31], v[160:163]\n"
"v_exp_f32 v152, v152\n"
"v_mfma_f32_16x16x32_bf16 v[160:163], v[208:211], a[16:19], v[160:163]\n"
"s_add_u32 s40, s56, s52\n"
"v_cvt_pk_bf16_f32 v146, v176, v177\n"
"v_mfma_f32_16x16x32_bf16 v[160:163], v[212:215], a[20:23], v[160:163]\n"
"buffer_load_dwordx4 v207, s[16:19], s40 offen offset:1024 nt lds\n"
"v_mfma_f32_16x16x32_bf16 v[164:167], v[208:211], a[32:35], v[188:191]\n"
"v_exp_f32 v170, v170\n"
"v_mfma_f32_16x16x32_bf16 v[164:167], v[212:215], a[36:39], v[164:167]\n"
"v_exp_f32 v173, v173\n"
"v_mfma_f32_16x16x32_bf16 v[164:167], v[216:219], a[40:43], v[164:167]\n"
"v_exp_f32 v153, v153\n"
"v_mfma_f32_16x16x32_bf16 v[164:167], v[220:223], a[44:47], v[164:167]\n"
"s_waitcnt lgkmcnt(0)\n"
"v_exp_f32 v174, v174\n"
"v_mfma_f32_16x16x32_bf16 a[208:211], v[120:123], a[244:247], a[208:211]\n"
"ds_read_b128 v[208:211], v230 offset:36864\n"
"v_cvt_pk_bf16_f32 v139, v174, v175\n"
"v_mfma_f32_16x16x32_bf16 v[0:3], v[96:99], a[244:247], v[0:3]\n"
"v_exp_f32 v156, v156\n"
"v_mfma_f32_16x16x32_bf16 v[0:3], v[100:103], a[248:251], v[0:3]\n"
"ds_read_b128 v[220:223], v231 offset:37376\n"
"v_cvt_pk_bf16_f32 v138, v172, v173\n"
"v_mfma_f32_16x16x32_bf16 v[32:35], v[104:107], a[244:247], v[32:35]\n"
"v_exp_f32 v161, v161\n"
"v_mfma_f32_16x16x32_bf16 v[32:35], v[108:111], a[248:251], v[32:35]\n"
"ds_read_b128 v[216:219], v230 offset:37376\n"
"v_cvt_pk_bf16_f32 v126, v152, v153\n"
"v_mfma_f32_16x16x32_bf16 v[64:67], v[112:115], a[244:247], v[64:67]\n"
"s_add_u32 s40, s56, s53\n"
"ds_read_b128 v[212:215], v231 offset:36864\n"
"v_mfma_f32_16x16x32_bf16 v[64:67], v[116:119], a[248:251], v[64:67]\n"
"buffer_load_dwordx4 v240, s[16:19], s40 offen offset:2048 nt lds\n"
"v_mfma_f32_16x16x32_bf16 v[68:71], v[116:119], v[224:227], v[68:71]\n"
"v_exp_f32 v159, v159\n"
"v_mfma_f32_16x16x32_bf16 v[68:71], v[112:115], a[252:255], v[68:71]\n"
"v_exp_f32 v155, v155\n"
"v_mfma_f32_16x16x32_bf16 v[36:39], v[108:111], v[224:227], v[36:39]\n"
"v_exp_f32 v160, v160\n"
"v_mfma_f32_16x16x32_bf16 v[36:39], v[104:107], a[252:255], v[36:39]\n"
"v_exp_f32 v179, v179\n"
"v_mfma_f32_16x16x32_bf16 v[4:7], v[100:103], v[224:227], v[4:7]\n"
"ds_read_b64_tr_b16 a[244:245], v232 offset:16896\n"
"ds_read_b64_tr_b16 v[224:225], v237 offset:16896\n"
"v_mfma_f32_16x16x32_bf16 v[4:7], v[96:99], a[252:255], v[4:7]\n"
"v_exp_f32 v158, v158\n"
"v_mfma_f32_16x16x32_bf16 a[212:215], v[120:123], a[252:255], a[212:215]\n"
"ds_read_b64_tr_b16 a[246:247], v232 offset:20992\n"
"ds_read_b64_tr_b16 a[250:251], v236 offset:20992\n"
"v_smfmac_f32_16x16x64_bf16 v[200:203], a[240:243], v[112:119], v241\n"
"s_waitcnt lgkmcnt(4)\n"
"ds_read_b64_tr_b16 v[226:227], v237 offset:20992\n"
"v_cvt_pk_bf16_f32 v131, v170, v171\n"
"v_mfma_f32_16x16x32_bf16 v[168:171], v[208:211], a[0:3], v[180:183]\n"
"v_exp_f32 v164, v164\n"
"v_mfma_f32_16x16x32_bf16 v[168:171], v[212:215], a[4:7], v[168:171]\n"
"ds_read_b64_tr_b16 a[248:249], v236 offset:16896\n"
"ds_read_b64_tr_b16 a[254:255], v233 offset:20992\n"
"v_mfma_f32_16x16x32_bf16 v[168:171], v[216:219], a[8:11], v[168:171]\n"
"s_add_u32 s40, s56, s54\n"
"ds_read_b64_tr_b16 a[252:253], v233 offset:16896\n"
"v_mfma_f32_16x16x32_bf16 v[168:171], v[220:223], a[12:15], v[168:171]\n"
"buffer_load_dwordx4 v207, s[16:19], s40 offen offset:3072 nt lds\n"
"v_mfma_f32_16x16x32_bf16 v[172:175], v[216:219], a[24:27], v[184:187]\n"
"v_exp_f32 v154, v154\n"
"v_mfma_f32_16x16x32_bf16 v[172:175], v[220:223], a[28:31], v[172:175]\n"
"v_exp_f32 v167, v167\n"
"v_mfma_f32_16x16x32_bf16 v[172:175], v[208:211], a[16:19], v[172:175]\n"
"v_exp_f32 v163, v163\n"
"v_mfma_f32_16x16x32_bf16 v[172:175], v[212:215], a[20:23], v[172:175]\n"
"s_cbranch_vccnz LPCL2_%=\nLPKL2_%=:\n"
"v_cvt_pk_bf16_f32 v147, v178, v179\n"
"v_mfma_f32_16x16x32_bf16 v[176:179], v[208:211], a[32:35], v[188:191]\n"
"v_exp_f32 v165, v165\n"
"v_mfma_f32_16x16x32_bf16 v[176:179], v[212:215], a[36:39], v[176:179]\n"
"v_exp_f32 v157, v157\n"
"v_mfma_f32_16x16x32_bf16 v[176:179], v[216:219], a[40:43], v[176:179]\n"
"v_exp_f32 v166, v166\n"
"v_mfma_f32_16x16x32_bf16 v[176:179], v[220:223], a[44:47], v[176:179]\n"
"s_waitcnt lgkmcnt(0)\n"
"v_mfma_f32_16x16x32_bf16 a[224:227], v[120:123], a[244:247], a[224:227]\n"
"v_exp_f32 v162, v162\n"
"v_mfma_f32_16x16x32_bf16 v[16:19], v[96:99], a[244:247], v[16:19]\n"
"s_waitcnt vmcnt(8)\n"
"v_exp_f32 v168, v168\n"
"v_mfma_f32_16x16x32_bf16 v[16:19], v[100:103], a[248:251], v[16:19]\n"
"s_barrier\n"
"v_mfma_f32_16x16x32_bf16 v[48:51], v[104:107], a[244:247], v[48:51]\n"
"ds_read_b128 v[208:211], v228 offset:49152\n"
"v_cvt_pk_bf16_f32 v133, v158, v159\n"
"v_mfma_f32_16x16x32_bf16 v[48:51], v[108:111], a[248:251], v[48:51]\n"
"ds_read_b128 v[216:219], v228 offset:49664\n"
"v_cvt_pk_bf16_f32 v141, v162, v163\n"
"v_mfma_f32_16x16x32_bf16 v[80:83], v[112:115], a[244:247], v[80:83]\n"
"ds_read_b128 v[220:223], v229 offset:49664\n"
"v_cvt_pk_bf16_f32 v127, v154, v155\n"
"v_mfma_f32_16x16x32_bf16 v[80:83], v[116:119], a[248:251], v[80:83]\n"
"ds_read_b128 v[212:215], v229 offset:49152\n"
"v_cvt_pk_bf16_f32 v149, v166, v167\n"
"v_mfma_f32_16x16x32_bf16 v[84:87], v[116:119], v[224:227], v[84:87]\n"
"v_exp_f32 v176, v176\n"
"v_mfma_f32_16x16x32_bf16 v[84:87], v[112:115], a[252:255], v[84:87]\n"
"v_exp_f32 v169, v169\n"
"v_mfma_f32_16x16x32_bf16 v[52:55], v[108:111], v[224:227], v[52:55]\n"
"ds_read_b64_tr_b16 a[246:247], v234 offset:36864\n"
"v_cvt_pk_bf16_f32 v148, v164, v165\n"
"v_mfma_f32_16x16x32_bf16 v[52:55], v[104:107], a[252:255], v[52:55]\n"
"v_exp_f32 v177, v177\n"
"v_mfma_f32_16x16x32_bf16 v[20:23], v[100:103], v[224:227], v[20:23]\n"
"ds_read_b64_tr_b16 a[250:251], v238 offset:36864\n"
"v_cvt_pk_bf16_f32 v150, v176, v177\n"
"v_mfma_f32_16x16x32_bf16 v[20:23], v[96:99], a[252:255], v[20:23]\n"
"v_exp_f32 v174, v174\n"
"v_mfma_f32_16x16x32_bf16 a[228:231], v[120:123], a[252:255], a[228:231]\n"
"s_waitcnt lgkmcnt(3)\n"
"ds_read_b64_tr_b16 a[254:255], v235 offset:36864\n"
"v_cvt_pk_bf16_f32 v132, v156, v157\n"
"v_mfma_f32_16x16x32_bf16 v[152:155], v[208:211], a[0:3], v[180:183]\n"
"s_waitcnt lgkmcnt(3)\n"
"ds_read_b64_tr_b16 a[248:249], v238 offset:32768\n"
"v_cvt_pk_bf16_f32 v134, v168, v169\n"
"v_mfma_f32_16x16x32_bf16 v[152:155], v[212:215], a[4:7], v[152:155]\n"
"v_exp_f32 v172, v172\n"
"v_mfma_f32_16x16x32_bf16 v[152:155], v[216:219], a[8:11], v[152:155]\n"
"ds_read_b64_tr_b16 a[244:245], v234 offset:32768\n"
"v_cvt_pk_bf16_f32 v140, v160, v161\n"
"v_mfma_f32_16x16x32_bf16 v[152:155], v[220:223], a[12:15], v[152:155]\n"
"v_exp_f32 v175, v175\n"
"v_mfma_f32_16x16x32_bf16 v[156:159], v[216:219], a[24:27], v[184:187]\n"
"ds_read_b64_tr_b16 a[252:253], v235 offset:32768\n"
"v_cvt_pk_bf16_f32 v143, v174, v175\n"
"v_mfma_f32_16x16x32_bf16 v[156:159], v[220:223], a[28:31], v[156:159]\n"
"ds_read_b64_tr_b16 v[224:225], v239 offset:32768\n"
"v_mfma_f32_16x16x32_bf16 v[156:159], v[208:211], a[16:19], v[156:159]\n"
"s_add_u32 m0, s44, 65536\n"
"ds_read_b64_tr_b16 v[226:227], v239 offset:36864\n"
"v_mfma_f32_16x16x32_bf16 v[156:159], v[212:215], a[20:23], v[156:159]\n"
"buffer_load_dwordx4 v240, s[20:23], s56 offen nt lds\n"
"v_mfma_f32_16x16x32_bf16 v[160:163], v[208:211], a[32:35], v[188:191]\n"
"v_exp_f32 v178, v178\n"
"v_mfma_f32_16x16x32_bf16 v[160:163], v[212:215], a[36:39], v[160:163]\n"
"v_exp_f32 v171, v171\n"
"v_mfma_f32_16x16x32_bf16 v[160:163], v[216:219], a[40:43], v[160:163]\n"
"s_add_u32 s40, s56, s52\n"
"v_mfma_f32_16x16x32_bf16 v[160:163], v[220:223], a[44:47], v[160:163]\n"
"buffer_load_dwordx4 v207, s[20:23], s40 offen offset:1024 nt lds\n"
"v_mfma_f32_16x16x32_bf16 v[164:167], v[216:219], a[200:203], v[192:195]\n"
"v_exp_f32 v170, v170\n"
"v_mfma_f32_16x16x32_bf16 v[164:167], v[220:223], a[204:207], v[164:167]\n"
"v_exp_f32 v153, v153\n"
"v_mfma_f32_16x16x32_bf16 v[164:167], v[208:211], a[192:195], v[164:167]\n"
"v_exp_f32 v179, v179\n"
"v_mfma_f32_16x16x32_bf16 v[164:167], v[212:215], a[196:199], v[164:167]\n"
"s_waitcnt lgkmcnt(0)\n"
"v_exp_f32 v158, v158\n"
"v_mfma_f32_16x16x32_bf16 a[216:219], v[124:127], a[244:247], a[216:219]\n"
"ds_read_b128 v[216:219], v228 offset:53760\n"
"v_cvt_pk_bf16_f32 v135, v170, v171\n"
"v_mfma_f32_16x16x32_bf16 v[8:11], v[128:131], a[244:247], v[8:11]\n"
"v_exp_f32 v173, v173\n"
"v_mfma_f32_16x16x32_bf16 v[8:11], v[132:135], a[248:251], v[8:11]\n"
"ds_read_b128 v[208:211], v228 offset:53248\n"
"v_cvt_pk_bf16_f32 v142, v172, v173\n"
"v_mfma_f32_16x16x32_bf16 v[40:43], v[136:139], a[244:247], v[40:43]\n"
"v_exp_f32 v162, v162\n"
"v_mfma_f32_16x16x32_bf16 v[40:43], v[140:143], a[248:251], v[40:43]\n"
"ds_read_b128 v[220:223], v229 offset:53760\n"
"v_cvt_pk_bf16_f32 v151, v178, v179\n"
"v_mfma_f32_16x16x32_bf16 v[72:75], v[144:147], a[244:247], v[72:75]\n"
"s_add_u32 s40, s56, s53\n"
"ds_read_b128 v[212:215], v229 offset:53248\n"
"v_mfma_f32_16x16x32_bf16 v[72:75], v[148:151], a[248:251], v[72:75]\n"
"buffer_load_dwordx4 v240, s[20:23], s40 offen offset:2048 nt lds\n"
"v_mfma_f32_16x16x32_bf16 v[76:79], v[148:151], v[224:227], v[76:79]\n"
"v_exp_f32 v163, v163\n"
"v_mfma_f32_16x16x32_bf16 v[76:79], v[144:147], a[252:255], v[76:79]\n"
"v_exp_f32 v156, v156\n"
"v_mfma_f32_16x16x32_bf16 v[44:47], v[140:143], v[224:227], v[44:47]\n"
"v_exp_f32 v160, v160\n"
"v_mfma_f32_16x16x32_bf16 v[44:47], v[136:139], a[252:255], v[44:47]\n"
"v_exp_f32 v155, v155\n"
"v_mfma_f32_16x16x32_bf16 v[12:15], v[132:135], v[224:227], v[12:15]\n"
"ds_read_b64_tr_b16 v[226:227], v239 offset:37376\n"
"v_mfma_f32_16x16x32_bf16 v[12:15], v[128:131], a[252:255], v[12:15]\n"
"ds_read_b64_tr_b16 a[248:249], v238 offset:33280\n"
"v_mfma_f32_16x16x32_bf16 a[220:223], v[124:127], a[252:255], a[220:223]\n"
"v_exp_f32 v154, v154\n"
"v_smfmac_f32_16x16x64_bf16 v[196:199], a[240:243], v[128:135], v241\n"
"ds_read_b64_tr_b16 a[250:251], v238 offset:37376\n"
"v_mfma_f32_16x16x32_bf16 v[202:205], a[240:243], v[124:127], v[202:205]\n"
"s_waitcnt lgkmcnt(3)\n"
"v_exp_f32 v152, v152\n"
"v_mfma_f32_16x16x32_bf16 v[168:171], v[216:219], a[8:11], v[180:183]\n"
"ds_read_b64_tr_b16 a[244:245], v234 offset:33280\n"
"v_cvt_pk_bf16_f32 v96, v152, v153\n"
"v_mfma_f32_16x16x32_bf16 v[168:171], v[220:223], a[12:15], v[168:171]\n"
"v_exp_f32 v165, v165\n"
"v_mfma_f32_16x16x32_bf16 v[168:171], v[208:211], a[0:3], v[168:171]\n"
"ds_read_b64_tr_b16 a[246:247], v234 offset:37376\n"
"v_cvt_pk_bf16_f32 v113, v162, v163\n"
"v_mfma_f32_16x16x32_bf16 v[168:171], v[212:215], a[4:7], v[168:171]\n"
"ds_read_b64_tr_b16 a[254:255], v235 offset:37376\n"
"v_mfma_f32_16x16x32_bf16 v[172:175], v[208:211], a[16:19], v[184:187]\n"
"ds_read_b64_tr_b16 v[224:225], v239 offset:33280\n"
"v_cvt_pk_bf16_f32 v97, v154, v155\n"
"v_mfma_f32_16x16x32_bf16 v[172:175], v[212:215], a[20:23], v[172:175]\n"
"v_mfma_f32_16x16x32_bf16 v[172:175], v[216:219], a[24:27], v[172:175]\n"
"s_add_u32 s40, s56, s54\n"
"ds_read_b64_tr_b16 a[252:253], v235 offset:33280\n"
"v_mfma_f32_16x16x32_bf16 v[172:175], v[220:223], a[28:31], v[172:175]\n"
"buffer_load_dwordx4 v207, s[20:23], s40 offen offset:3072 nt lds\n"
"v_mfma_f32_16x16x32_bf16 v[176:179], v[216:219], a[40:43], v[188:191]\n"
"s_add_u32 s55, s55, s59\n"
"v_exp_f32 v157, v157\n"
"v_mfma_f32_16x16x32_bf16 v[176:179], v[220:223], a[44:47], v[176:179]\n"
"s_or_b32 s55, s55, s58\n"
"v_exp_f32 v171, v171\n"
"v_mfma_f32_16x16x32_bf16 v[176:179], v[208:211], a[32:35], v[176:179]\n"
"s_xor_b32 s56, s55, s57\n"
"v_exp_f32 v159, v159\n"
"v_mfma_f32_16x16x32_bf16 v[176:179], v[212:215], a[36:39], v[176:179]\n"
"v_exp_f32 v167, v167\n"
"v_mfma_f32_16x16x32_bf16 v[152:155], v[208:211], a[192:195], v[192:195]\n"
"v_cvt_pk_bf16_f32 v105, v158, v159\n"
"v_mfma_f32_16x16x32_bf16 v[152:155], v[212:215], a[196:199], v[152:155]\n"
"ds_read_b128 v[208:211], v230 offset:49152\n"
"v_cvt_pk_bf16_f32 v104, v156, v157\n"
"v_mfma_f32_16x16x32_bf16 v[152:155], v[216:219], a[200:203], v[152:155]\n"
"v_exp_f32 v166, v166\n"
"v_mfma_f32_16x16x32_bf16 v[152:155], v[220:223], a[204:207], v[152:155]\n"
"s_waitcnt lgkmcnt(1)\n"
"ds_read_b128 v[216:219], v230 offset:49664\n"
"v_mfma_f32_16x16x32_bf16 a[232:235], v[124:127], a[244:247], a[232:235]\n"
"v_exp_f32 v169, v169\n"
"v_mfma_f32_16x16x32_bf16 v[24:27], v[128:131], a[244:247], v[24:27]\n"
"ds_read_b128 v[220:223], v231 offset:49664\n"
"v_mfma_f32_16x16x32_bf16 v[24:27], v[132:135], a[248:251], v[24:27]\n"
"v_exp_f32 v164, v164\n"
"v_mfma_f32_16x16x32_bf16 v[56:59], v[136:139], a[244:247], v[56:59]\n"
"ds_read_b128 v[212:215], v231 offset:49152\n"
"v_mfma_f32_16x16x32_bf16 v[56:59], v[140:143], a[248:251], v[56:59]\n"
"v_exp_f32 v178, v178\n"
"v_mfma_f32_16x16x32_bf16 v[88:91], v[144:147], a[244:247], v[88:91]\n"
"s_add_u32 m0, s44, 16384\n"
"ds_read_b64_tr_b16 a[246:247], v232 offset:36864\n"
"v_mfma_f32_16x16x32_bf16 v[88:91], v[148:151], a[248:251], v[88:91]\n"
"buffer_load_dwordx4 v240, s[16:19], s56 offen nt lds\n"
"v_mfma_f32_16x16x32_bf16 v[92:95], v[148:151], v[224:227], v[92:95]\n"
"v_exp_f32 v175, v175\n"
"v_mfma_f32_16x16x32_bf16 v[92:95], v[144:147], a[252:255], v[92:95]\n"
"v_exp_f32 v168, v168\n"
"v_mfma_f32_16x16x32_bf16 v[60:63], v[140:143], v[224:227], v[60:63]\n"
"v_exp_f32 v161, v161\n"
"v_mfma_f32_16x16x32_bf16 v[60:63], v[136:139], a[252:255], v[60:63]\n"
"v_exp_f32 v177, v177\n"
"v_mfma_f32_16x16x32_bf16 v[28:31], v[132:135], v[224:227], v[28:31]\n"
"ds_read_b64_tr_b16 v[226:227], v237 offset:36864\n"
"v_mfma_f32_16x16x32_bf16 v[28:31], v[128:131], a[252:255], v[28:31]\n"
"v_exp_f32 v172, v172\n"
"v_mfma_f32_16x16x32_bf16 a[236:239], v[124:127], a[252:255], a[236:239]\n"
"ds_read_b64_tr_b16 a[250:251], v236 offset:36864\n"
"v_cvt_pk_bf16_f32 v120, v164, v165\n"
"v_smfmac_f32_16x16x64_bf16 v[198:201], a[240:243], v[136:143], v241\n"
"s_waitcnt lgkmcnt(2)\n"
"ds_read_b64_tr_b16 a[252:253], v233 offset:32768\n"
"v_mfma_f32_16x16x32_bf16 v[156:159], v[208:211], a[0:3], v[180:183]\n"
"ds_read_b64_tr_b16 a[248:249], v236 offset:32768\n"
"v_cvt_pk_bf16_f32 v121, v166, v167\n"
"v_mfma_f32_16x16x32_bf16 v[156:159], v[212:215], a[4:7], v[156:159]\n"
"ds_read_b64_tr_b16 v[224:225], v237 offset:32768\n"
"v_cvt_pk_bf16_f32 v98, v168, v169\n"
"v_mfma_f32_16x16x32_bf16 v[156:159], v[216:219], a[8:11], v[156:159]\n"
"ds_read_b64_tr_b16 a[254:255], v233 offset:36864\n"
"v_cvt_pk_bf16_f32 v112, v160, v161\n"
"v_mfma_f32_16x16x32_bf16 v[156:159], v[220:223], a[12:15], v[156:159]\n"
"ds_read_b64_tr_b16 a[244:245], v232 offset:32768\n"
"v_mfma_f32_16x16x32_bf16 v[160:163], v[216:219], a[24:27], v[184:187]\n"
"v_exp_f32 v176, v176\n"
"v_mfma_f32_16x16x32_bf16 v[160:163], v[220:223], a[28:31], v[160:163]\n"
"v_exp_f32 v152, v152\n"
"v_mfma_f32_16x16x32_bf16 v[160:163], v[208:211], a[16:19], v[160:163]\n"
"s_add_u32 s40, s56, s52\n"
"v_cvt_pk_bf16_f32 v114, v176, v177\n"
"v_mfma_f32_16x16x32_bf16 v[160:163], v[212:215], a[20:23], v[160:163]\n"
"buffer_load_dwordx4 v207, s[16:19], s40 offen offset:1024 nt lds\n"
"v_mfma_f32_16x16x32_bf16 v[164:167], v[208:211], a[32:35], v[188:191]\n"
"v_exp_f32 v170, v170\n"
"v_mfma_f32_16x16x32_bf16 v[164:167], v[212:215], a[36:39], v[164:167]\n"
"v_exp_f32 v173, v173\n"
"v_mfma_f32_16x16x32_bf16 v[164:167], v[216:219], a[40:43], v[164:167]\n"
"v_exp_f32 v153, v153\n"
"v_mfma_f32_16x16x32_bf16 v[164:167], v[220:223], a[44:47], v[164:167]\n"
"s_waitcnt lgkmcnt(0)\n"
"v_exp_f32 v174, v174\n"
"v_mfma_f32_16x16x32_bf16 a[208:211], v[124:127], a[244:247], a[208:211]\n"
"ds_read_b128 v[208:211], v230 offset:53248\n"
"v_cvt_pk_bf16_f32 v107, v174, v175\n"
"v_mfma_f32_16x16x32_bf16 v[0:3], v[128:131], a[244:247], v[0:3]\n"
"v_exp_f32 v156, v156\n"
"v_mfma_f32_16x16x32_bf16 v[0:3], v[132:135], a[248:251], v[0:3]\n"
"ds_read_b128 v[220:223], v231 offset:53760\n"
"v_cvt_pk_bf16_f32 v106, v172, v173\n"
"v_mfma_f32_16x16x32_bf16 v[32:35], v[136:139], a[244:247], v[32:35]\n"
"v_exp_f32 v161, v161\n"
"v_mfma_f32_16x16x32_bf16 v[32:35], v[140:143], a[248:251], v[32:35]\n"
"ds_read_b128 v[216:219], v230 offset:53760\n"
"v_cvt_pk_bf16_f32 v122, v152, v153\n"
"v_mfma_f32_16x16x32_bf16 v[64:67], v[144:147], a[244:247], v[64:67]\n"
"s_add_u32 s40, s56, s53\n"
"ds_read_b128 v[212:215], v231 offset:53248\n"
"v_mfma_f32_16x16x32_bf16 v[64:67], v[148:151], a[248:251], v[64:67]\n"
"buffer_load_dwordx4 v240, s[16:19], s40 offen offset:2048 nt lds\n"
"v_mfma_f32_16x16x32_bf16 v[68:71], v[148:151], v[224:227], v[68:71]\n"
"v_exp_f32 v159, v159\n"
"v_mfma_f32_16x16x32_bf16 v[68:71], v[144:147], a[252:255], v[68:71]\n"
"v_exp_f32 v155, v155\n"
"v_mfma_f32_16x16x32_bf16 v[36:39], v[140:143], v[224:227], v[36:39]\n"
"v_exp_f32 v160, v160\n"
"v_mfma_f32_16x16x32_bf16 v[36:39], v[136:139], a[252:255], v[36:39]\n"
"v_exp_f32 v179, v179\n"
"v_mfma_f32_16x16x32_bf16 v[4:7], v[132:135], v[224:227], v[4:7]\n"
"ds_read_b64_tr_b16 a[244:245], v232 offset:33280\n"
"ds_read_b64_tr_b16 v[224:225], v237 offset:33280\n"
"v_mfma_f32_16x16x32_bf16 v[4:7], v[128:131], a[252:255], v[4:7]\n"
"v_exp_f32 v158, v158\n"
"v_mfma_f32_16x16x32_bf16 a[212:215], v[124:127], a[252:255], a[212:215]\n"
"ds_read_b64_tr_b16 a[246:247], v232 offset:37376\n"
"ds_read_b64_tr_b16 a[250:251], v236 offset:37376\n"
"v_smfmac_f32_16x16x64_bf16 v[200:203], a[240:243], v[144:151], v241\n"
"s_waitcnt lgkmcnt(4)\n"
"ds_read_b64_tr_b16 v[226:227], v237 offset:37376\n"
"v_cvt_pk_bf16_f32 v99, v170, v171\n"
"v_mfma_f32_16x16x32_bf16 v[168:171], v[208:211], a[0:3], v[180:183]\n"
"v_exp_f32 v164, v164\n"
"v_mfma_f32_16x16x32_bf16 v[168:171], v[212:215], a[4:7], v[168:171]\n"
"ds_read_b64_tr_b16 a[248:249], v236 offset:33280\n"
"ds_read_b64_tr_b16 a[254:255], v233 offset:37376\n"
"v_mfma_f32_16x16x32_bf16 v[168:171], v[216:219], a[8:11], v[168:171]\n"
"s_add_u32 s40, s56, s54\n"
"ds_read_b64_tr_b16 a[252:253], v233 offset:33280\n"
"v_mfma_f32_16x16x32_bf16 v[168:171], v[220:223], a[12:15], v[168:171]\n"
"buffer_load_dwordx4 v207, s[16:19], s40 offen offset:3072 nt lds\n"
"v_mfma_f32_16x16x32_bf16 v[172:175], v[216:219], a[24:27], v[184:187]\n"
"v_exp_f32 v154, v154\n"
"v_mfma_f32_16x16x32_bf16 v[172:175], v[220:223], a[28:31], v[172:175]\n"
"v_exp_f32 v167, v167\n"
"v_mfma_f32_16x16x32_bf16 v[172:175], v[208:211], a[16:19], v[172:175]\n"
"v_exp_f32 v163, v163\n"
"v_mfma_f32_16x16x32_bf16 v[172:175], v[212:215], a[20:23], v[172:175]\n"
"s_cbranch_vccnz LPCL3_%=\nLPKL3_%=:\n"
"v_cvt_pk_bf16_f32 v115, v178, v179\n"
"v_mfma_f32_16x16x32_bf16 v[176:179], v[208:211], a[32:35], v[188:191]\n"
"s_add_u32 s47, s47, 1\n"
"v_exp_f32 v165, v165\n"
"v_mfma_f32_16x16x32_bf16 v[176:179], v[212:215], a[36:39], v[176:179]\n"
"v_exp_f32 v157, v157\n"
"v_mfma_f32_16x16x32_bf16 v[176:179], v[216:219], a[40:43], v[176:179]\n"
"s_and_b32 s40, s47, 3\n"
"v_exp_f32 v166, v166\n"
"v_mfma_f32_16x16x32_bf16 v[176:179], v[220:223], a[44:47], v[176:179]\n"
"s_waitcnt lgkmcnt(0)\n"
"s_cbranch_scc1 LAL_%=\n"
"s_cmp_eq_u32 s47, 8\n"
"s_cbranch_scc1 LTH1_%=\n"
"s_cmp_eq_u32 s47, 12\n"
"s_cbranch_scc1 LTH2_%=\n"
"s_cmp_eq_u32 s47, 16\n"
"s_cbranch_scc1 LAX_%=\n"
"LTH0_%=:\n"
"v_mfma_f32_16x16x32_bf16 a[224:227], v[124:127], a[244:247], a[224:227]\n"
"v_exp_f32 v171, v171\n"
"v_mfma_f32_16x16x32_bf16 v[16:19], v[128:131], a[244:247], v[16:19]\n"
"s_waitcnt vmcnt(8)\n"
"v_exp_f32 v172, v172\n"
"v_mfma_f32_16x16x32_bf16 v[16:19], v[132:135], a[248:251], v[16:19]\n"
"s_barrier\n"
"v_mfma_f32_16x16x32_bf16 v[48:51], v[136:139], a[244:247], v[48:51]\n"
"v_exp_f32 v169, v169\n"
"v_mfma_f32_16x16x32_bf16 v[48:51], v[140:143], a[248:251], v[48:51]\n"
"ds_read_b128 v[220:223], v229 offset:512\n"
"v_cvt_pk_bf16_f32 v117, v166, v167\n"
"v_exp_f32 v176, v176\n"
"v_exp_f32 v177, v177\n"
"v_exp_f32 v178, v178\n"
"v_mfma_f32_16x16x32_bf16 v[80:83], v[144:147], a[244:247], v[80:83]\n"
"ds_read_b64_tr_b16 a[244:245], v234 offset:49152\n"
"v_cvt_pk_bf16_f32 v108, v160, v161\n"
"v_mfma_f32_16x16x32_bf16 v[80:83], v[148:151], a[248:251], v[80:83]\n"
"ds_read_b64_tr_b16 a[246:247], v234 offset:53248\n"
"ds_read_b64_tr_b16 a[248:249], v238 offset:49152\n"
"v_exp_f32 v162, v162\n"
"v_exp_f32 v168, v168\n"
"v_exp_f32 v173, v173\n"
"v_exp_f32 v175, v175\n"
"v_mfma_f32_16x16x32_bf16 v[84:87], v[148:151], v[224:227], v[84:87]\n"
"ds_read_b64_tr_b16 a[250:251], v238 offset:53248\n"
"v_cvt_pk_bf16_f32 v116, v164, v165\n"
"v_mfma_f32_16x16x32_bf16 v[84:87], v[144:147], a[252:255], v[84:87]\n"
"v_exp_f32 v170, v170\n"
"v_mfma_f32_16x16x32_bf16 v[52:55], v[140:143], v[224:227], v[52:55]\n"
"v_cvt_pk_bf16_f32 v103, v170, v171\n"
"v_cvt_pk_bf16_f32 v110, v172, v173\n"
"v_mfma_f32_16x16x32_bf16 v[52:55], v[136:139], a[252:255], v[52:55]\n"
"v_cvt_pk_bf16_f32 v123, v154, v155\n"
"v_cvt_pk_bf16_f32 v101, v158, v159\n"
"v_mfma_f32_16x16x32_bf16 v[20:23], v[132:135], v[224:227], v[20:23]\n"
"v_cvt_pk_bf16_f32 v100, v156, v157\n"
"v_cvt_pk_bf16_f32 v102, v168, v169\n"
"v_mfma_f32_16x16x32_bf16 v[20:23], v[128:131], a[252:255], v[20:23]\n"
"v_exp_f32 v179, v179\n"
"v_mfma_f32_16x16x32_bf16 a[228:231], v[124:127], a[252:255], a[228:231]\n"
"s_waitcnt lgkmcnt(2)\n"
"ds_read_b64_tr_b16 a[252:253], v235 offset:49152\n"
"ds_read_b64_tr_b16 v[226:227], v239 offset:53248\n"
"v_mfma_f32_16x16x32_bf16 a[216:219], v[120:123], a[244:247], a[216:219]\n"
"s_add_u32 m0, s44, 81920\n"
"ds_read_b64_tr_b16 v[224:225], v239 offset:49152\n"
"v_cvt_pk_bf16_f32 v119, v178, v179\n"
"v_smfmac_f32_16x16x64_bf16 v[196:199], a[240:243], v[96:103], v241\n"
"buffer_load_dwordx4 v240, s[20:23], s56 offen nt lds\n"
"v_mfma_f32_16x16x32_bf16 v[8:11], v[96:99], a[244:247], v[8:11]\n"
"s_waitcnt lgkmcnt(3)\n"
"ds_read_b64_tr_b16 a[254:255], v235 offset:53248\n"
"v_cvt_pk_bf16_f32 v118, v176, v177\n"
"v_mfma_f32_16x16x32_bf16 v[8:11], v[100:103], a[248:251], v[8:11]\n"
"v_exp_f32 v174, v174\n"
"v_smfmac_f32_16x16x64_bf16 v[200:203], a[240:243], v[112:119], v241\n"
"v_cvt_pk_bf16_f32 v109, v162, v163\n"
"v_cvt_pk_bf16_f32 v111, v174, v175\n"
"v_mfma_f32_16x16x32_bf16 v[40:43], v[104:107], a[244:247], v[40:43]\n"
"ds_read_b128 v[212:215], v229 offset:0\n"
"v_and_b32 v245, 0x3f0, v242\n"
"v_mfma_f32_16x16x32_bf16 v[40:43], v[108:111], a[248:251], v[40:43]\n"
"v_lshlrev_b32_e64 v244, 24, s43\n"
"v_mul_f32 v244, 0x45800000, v244\n"
"v_smfmac_f32_16x16x64_bf16 v[198:201], a[240:243], v[104:111], v241\n"
"ds_read_b128 v[208:211], v228 offset:0\n"
"v_add_f32 v196, v196, v197\n"
"v_mfma_f32_16x16x32_bf16 v[72:75], v[112:115], a[244:247], v[72:75]\n"
"ds_read_b64_tr_b16 a[244:245], v234 offset:49664\n"
"v_lshrrev_b32 v245, 1, v245\n"
"v_mfma_f32_16x16x32_bf16 v[72:75], v[116:119], a[248:251], v[72:75]\n"
"ds_read_b64_tr_b16 a[246:247], v234 offset:53760\n"
"v_sub_u32 v245, v242, v245\n"
"v_mfma_f32_16x16x32_bf16 v[202:205], a[240:243], v[120:123], v[202:205]\n"
"s_waitcnt lgkmcnt(4)\n"
"ds_read_b64_tr_b16 a[248:249], v238 offset:49664\n"
"v_add_f32 v200, v200, v201\n"
"v_mfma_f32_16x16x32_bf16 v[76:79], v[116:119], v[224:227], v[76:79]\n"
"v_add_f32 v198, v198, v199\n"
"v_add_f32 v254, v196, v198\n"
"v_mfma_f32_16x16x32_bf16 v[76:79], v[112:115], a[252:255], v[76:79]\n"
"ds_read_b64_tr_b16 a[250:251], v238 offset:53760\n"
"v_xor_b32 v250, 0x1000, v245\n"
"v_mfma_f32_16x16x32_bf16 v[44:47], v[108:111], v[224:227], v[44:47]\n"
"ds_read_b128 v[216:219], v228 offset:512\n"
"v_add_f32 v254, v254, v200\n"
"v_mfma_f32_16x16x32_bf16 v[44:47], v[104:107], a[252:255], v[44:47]\n"
"v_add_f32 v244, v244, v202\n"
"v_add_f32 v254, v254, v202\n"
"v_mfma_f32_16x16x32_bf16 v[12:15], v[100:103], v[224:227], v[12:15]\n"
"s_add_u32 s40, s56, s52\n"
"ds_read_b64_tr_b16 v[226:227], v239 offset:53760\n"
"v_min3_f32 v244, v244, v196, v198\n"
"v_mfma_f32_16x16x32_bf16 v[12:15], v[96:99], a[252:255], v[12:15]\n"
"buffer_load_dwordx4 v207, s[20:23], s40 offen offset:1024 nt lds\n"
"v_min_f32 v244, v244, v200\n"
"v_rcp_f32 v244, v244\n"
"v_mfma_f32_16x16x32_bf16 a[220:223], v[120:123], a[252:255], a[220:223]\n"
"s_waitcnt lgkmcnt(4)\n"
"v_fmac_f32 v254, 0x68800000, v244\n"
"v_cmp_nge_f32 vcc, s45, v254\n"
"s_cbranch_vccnz LSLOW_%=\nLBACK0_%=:\ns_mov_b64 vcc, s[64:65]\n"
"v_mfma_f32_16x16x32_bf16 a[232:235], v[120:123], a[244:247], a[232:235]\n"
"v_rcp_f32 v246, v196\n"
"v_accvgpr_mov_b32 a2, a50\n"
"v_accvgpr_mov_b32 a7, a55\n"
"v_accvgpr_mov_b32 a10, a58\n"
"v_accvgpr_mov_b32 a15, a63\n"
"v_accvgpr_mov_b32 a22, a70\n"
"v_accvgpr_mov_b32 a30, a78\n"
"v_bfe_u32 v149, v242, 8, 2\n"
"v_lshl_add_u32 v149, v149, 4, s43\n"
"v_mfma_f32_16x16x32_bf16 v[24:27], v[96:99], a[244:247], v[24:27]\n"
"s_waitcnt lgkmcnt(2)\n"
"ds_read_b128 v[124:127], v149 offset:0\n"
"v_accvgpr_mov_b32 a28, a76\n"
"v_mfma_f32_16x16x32_bf16 v[24:27], v[100:103], a[248:251], v[24:27]\n"
"ds_read_b128 v[132:135], v149 offset:512\n"
"v_mov_b32_dpp v246, v246 row_ror:12 row_mask:0x2 bank_mask:0xf\n"
"v_accvgpr_mov_b32 a3, a51\n"
"v_accvgpr_mov_b32 a12, a60\n"
"v_accvgpr_mov_b32 a18, a66\n"
"v_accvgpr_mov_b32 a19, a67\n"
"v_accvgpr_mov_b32 a26, a74\n"
"v_accvgpr_mov_b32 a36, a84\n"
"v_xor_b32 v148, 0x400, v243\n"
"v_xor_b32 v150, 64, v149\n"
"v_mfma_f32_16x16x32_bf16 v[56:59], v[104:107], a[244:247], v[56:59]\n"
"ds_read_b128 v[128:131], v150 offset:0\n"
"v_accvgpr_mov_b32 a39, a87\n"
"v_mfma_f32_16x16x32_bf16 v[56:59], v[108:111], a[248:251], v[56:59]\n"
"ds_read_b128 v[136:139], v150 offset:512\n"
"v_rcp_f32 v247, v198\n"
"v_mov_b32_dpp v246, v246 row_ror:8 row_mask:0x4 bank_mask:0xf\n"
"v_accvgpr_mov_b32 a0, a48\n"
"v_accvgpr_mov_b32 a1, a49\n"
"v_accvgpr_mov_b32 a4, a52\n"
"v_accvgpr_mov_b32 a5, a53\n"
"v_accvgpr_mov_b32 a8, a56\n"
"v_accvgpr_mov_b32 a16, a64\n"
"v_accvgpr_mov_b32 a25, a73\n"
"v_mfma_f32_16x16x32_bf16 v[88:91], v[112:115], a[244:247], v[88:91]\n"
"v_accvgpr_mov_b32 a20, a68\n"
"v_accvgpr_mov_b32 a29, a77\n"
"v_mfma_f32_16x16x32_bf16 v[88:91], v[116:119], a[248:251], v[88:91]\n"
"ds_read_b128 a[192:195], v243 offset:4096\n"
"ds_read_b128 a[200:203], v243 offset:6144\n"
"v_accvgpr_mov_b32 a6, a54\n"
"v_accvgpr_mov_b32 a9, a57\n"
"v_accvgpr_mov_b32 a11, a59\n"
"v_accvgpr_mov_b32 a13, a61\n"
"v_accvgpr_mov_b32 a14, a62\n"
"v_accvgpr_mov_b32 a17, a65\n"
"v_accvgpr_mov_b32 a23, a71\n"
"v_accvgpr_mov_b32 a31, a79\n"
"v_accvgpr_mov_b32 a33, a81\n"
"v_accvgpr_mov_b32 a35, a83\n"
"v_accvgpr_mov_b32 a44, a92\n"
"v_accvgpr_mov_b32 a47, a95\n"
"s_waitcnt lgkmcnt(5)\n"
"v_mfma_f32_16x16x32_bf16 v[180:183], v[124:127], a[0:3], 0\n"
"v_xor_b32 v244, 0x11000, v242\n"
"s_waitcnt lgkmcnt(3)\n"
"v_mfma_f32_16x16x32_bf16 v[180:183], v[128:131], a[4:7], v[180:183]\n"
"v_accvgpr_mov_b32 a34, a82\n"
"v_accvgpr_mov_b32 a43, a91\n"
"v_mfma_f32_16x16x32_bf16 v[180:183], v[132:135], a[8:11], v[180:183]\n"
"s_waitcnt lgkmcnt(2)\n"
"ds_read_b128 a[204:207], v148 offset:6144\n"
"v_accvgpr_mov_b32 a37, a85\n"
"v_mfma_f32_16x16x32_bf16 v[180:183], v[136:139], a[12:15], v[180:183]\n"
"ds_read_b128 a[196:199], v148 offset:4096\n"
"v_accvgpr_mov_b32 a21, a69\n"
"v_accvgpr_mov_b32 a24, a72\n"
"v_accvgpr_mov_b32 a32, a80\n"
"v_accvgpr_mov_b32 a38, a86\n"
"v_accvgpr_mov_b32 a40, a88\n"
"v_accvgpr_mov_b32 a42, a90\n"
"v_accvgpr_mov_b32 a46, a94\n"
"v_mfma_f32_16x16x32_bf16 v[184:187], v[124:127], a[16:19], 0\n"
"v_mov_b32_dpp v246, v246 row_ror:4 row_mask:0x8 bank_mask:0xf\n"
"v_accvgpr_mov_b32 a27, a75\n"
"v_mfma_f32_16x16x32_bf16 v[184:187], v[128:131], a[20:23], v[184:187]\n"
"v_mov_b32_dpp v247, v247 row_ror:12 row_mask:0x2 bank_mask:0xf\n"
"v_mfma_f32_16x16x32_bf16 v[184:187], v[132:135], a[24:27], v[184:187]\n"
"s_add_u32 s40, s56, s53\n"
"ds_read_b64_tr_b16 v[224:225], v239 offset:49664\n"
"v_mfma_f32_16x16x32_bf16 v[184:187], v[136:139], a[28:31], v[184:187]\n"
"buffer_load_dwordx4 v240, s[20:23], s40 offen offset:2048 nt lds\n"
"v_accvgpr_mov_b32 a41, a89\n"
"v_sub_f32 v180, s74, v180\n"
"v_sub_f32 v181, s74, v181\n"
"v_sub_f32 v183, s74, v183\n"
"v_mul_f32_dpp v26, v246, v26 row_newbcast:2 row_mask:0xf bank_mask:0xf\n"
"v_mfma_f32_16x16x32_bf16 v[188:191], v[124:127], a[32:35], 0\n"
"v_mov_b32_dpp v247, v247 row_ror:8 row_mask:0x4 bank_mask:0xf\n"
"v_accvgpr_mov_b32 a45, a93\n"
"v_mfma_f32_16x16x32_bf16 v[188:191], v[128:131], a[36:39], v[188:191]\n"
"v_mov_b32 v252, v192\n"
"v_mfma_f32_16x16x32_bf16 v[188:191], v[132:135], a[40:43], v[188:191]\n"
"v_mfma_f32_16x16x32_bf16 v[188:191], v[136:139], a[44:47], v[188:191]\n"
"v_mul_f32_dpp v11, v246, v11 row_newbcast:3 row_mask:0xf bank_mask:0xf\n"
"s_waitcnt lgkmcnt(1)\n"
"v_mfma_f32_16x16x32_bf16 v[192:195], v[124:127], a[192:195], 0\n"
"ds_read_b64_tr_b16 a[252:253], v235 offset:49664\n"
"v_sub_f32 v185, s74, v185\n"
"v_mfma_f32_16x16x32_bf16 v[192:195], v[128:131], a[196:199], v[192:195]\n"
"ds_read_b64_tr_b16 a[248:249], v236 offset:49152\n"
"v_sub_f32 v182, s74, v182\n"
"v_mfma_f32_16x16x32_bf16 v[192:195], v[132:135], a[200:203], v[192:195]\n"
"v_sub_f32 v184, s74, v184\n"
"v_mfma_f32_16x16x32_bf16 v[192:195], v[136:139], a[204:207], v[192:195]\n"
"ds_read_b64_tr_b16 a[254:255], v235 offset:53760\n"
"ds_read_b64_tr_b16 a[246:247], v232 offset:53248\n"
"v_mov_b32 v253, v202\n"
"v_sub_f32 v186, s74, v186\n"
"v_mfma_f32_16x16x32_bf16 v[152:155], v[208:211], a[0:3], v[180:183]\n"
"v_mfma_f32_16x16x32_bf16 v[152:155], v[212:215], a[4:7], v[152:155]\n"
"v_sub_f32 v187, s74, v187\n"
"v_mfma_f32_16x16x32_bf16 v[152:155], v[216:219], a[8:11], v[152:155]\n"
"s_add_u32 s40, s56, s54\n"
"v_mfma_f32_16x16x32_bf16 v[152:155], v[220:223], a[12:15], v[152:155]\n"
"buffer_load_dwordx4 v207, s[20:23], s40 offen offset:3072 nt lds\n"
"v_mov_b32_dpp v247, v247 row_ror:4 row_mask:0x8 bank_mask:0xf\n"
"v_sub_f32 v189, s74, v189\n"
"v_sub_f32 v190, s74, v190\n"
"v_sub_f32 v191, s74, v191\n"
"v_sub_f32 v192, s74, v192\n"
"v_mfma_f32_16x16x32_bf16 v[156:159], v[216:219], a[24:27], v[184:187]\n"
"s_add_u32 s55, s55, s59\n"
"v_mfma_f32_16x16x32_bf16 v[156:159], v[220:223], a[28:31], v[156:159]\n"
"s_or_b32 s55, s55, s58\n"
"v_sub_f32 v188, s74, v188\n"
"v_mfma_f32_16x16x32_bf16 v[156:159], v[208:211], a[16:19], v[156:159]\n"
"s_xor_b32 s56, s55, s57\n"
"v_exp_f32 v153, v153\n"
"v_mfma_f32_16x16x32_bf16 v[156:159], v[212:215], a[20:23], v[156:159]\n"
"ds_write_b64 v250, v[252:253] offset:52224\n"
"v_sub_f32 v193, s74, v193\n"
"v_sub_f32 v194, s74, v194\n"
"v_exp_f32 v152, v152\n"
"v_mfma_f32_16x16x32_bf16 v[160:163], v[208:211], a[32:35], v[188:191]\n"
"v_mfma_f32_16x16x32_bf16 v[160:163], v[212:215], a[36:39], v[160:163]\n"
"v_rcp_f32 v206, v200\n"
"v_mfma_f32_16x16x32_bf16 v[160:163], v[216:219], a[40:43], v[160:163]\n"
"v_sub_f32 v195, s74, v195\n"
"v_mfma_f32_16x16x32_bf16 v[160:163], v[220:223], a[44:47], v[160:163]\n"
"ds_write_b128 v244, a[216:219] offset:32768\n"
"v_mul_f32_dpp v25, v246, v25 row_newbcast:1 row_mask:0xf bank_mask:0xf\n"
"v_mfma_f32_16x16x32_bf16 v[164:167], v[216:219], a[200:203], v[192:195]\n"
"v_mov_b32_dpp v206, v206 row_ror:12 row_mask:0x2 bank_mask:0xf\n"
"v_mfma_f32_16x16x32_bf16 v[164:167], v[220:223], a[204:207], v[164:167]\n"
"v_mfma_f32_16x16x32_bf16 v[164:167], v[208:211], a[192:195], v[164:167]\n"
"ds_read_b128 v[208:211], v228 offset:4096\n"
"v_mfma_f32_16x16x32_bf16 v[164:167], v[212:215], a[196:199], v[164:167]\n"
"ds_read_b128 v[216:219], v228 offset:4608\n"
"ds_read_b128 v[220:223], v229 offset:4608\n"
"v_mov_b32_dpp v206, v206 row_ror:8 row_mask:0x4 bank_mask:0xf\n"
"v_exp_f32 v155, v155\n"
"v_exp_f32 v160, v160\n"
"s_branch LTC_%=\n"
"LTH1_%=:\n"
"v_mfma_f32_16x16x32_bf16 a[224:227], v[124:127], a[244:247], a[224:227]\n"
"v_exp_f32 v171, v171\n"
"v_mfma_f32_16x16x32_bf16 v[16:19], v[128:131], a[244:247], v[16:19]\n"
"s_waitcnt vmcnt(8)\n"
"v_exp_f32 v172, v172\n"
"v_mfma_f32_16x16x32_bf16 v[16:19], v[132:135], a[248:251], v[16:19]\n"
"s_barrier\n"
"v_mfma_f32_16x16x32_bf16 v[48:51], v[136:139], a[244:247], v[48:51]\n"
"v_exp_f32 v169, v169\n"
"v_mfma_f32_16x16x32_bf16 v[48:51], v[140:143], a[248:251], v[48:51]\n"
"ds_read_b128 v[220:223], v229 offset:512\n"
"v_cvt_pk_bf16_f32 v117, v166, v167\n"
"v_exp_f32 v176, v176\n"
"v_exp_f32 v177, v177\n"
"v_exp_f32 v178, v178\n"
"v_mfma_f32_16x16x32_bf16 v[80:83], v[144:147], a[244:247], v[80:83]\n"
"ds_read_b64_tr_b16 a[244:245], v234 offset:49152\n"
"v_cvt_pk_bf16_f32 v108, v160, v161\n"
"v_mfma_f32_16x16x32_bf16 v[80:83], v[148:151], a[248:251], v[80:83]\n"
"ds_read_b64_tr_b16 a[246:247], v234 offset:53248\n"
"ds_read_b64_tr_b16 a[248:249], v238 offset:49152\n"
"v_exp_f32 v162, v162\n"
"v_exp_f32 v168, v168\n"
"v_exp_f32 v173, v173\n"
"v_exp_f32 v175, v175\n"
"v_mfma_f32_16x16x32_bf16 v[84:87], v[148:151], v[224:227], v[84:87]\n"
"ds_read_b64_tr_b16 a[250:251], v238 offset:53248\n"
"v_cvt_pk_bf16_f32 v116, v164, v165\n"
"v_mfma_f32_16x16x32_bf16 v[84:87], v[144:147], a[252:255], v[84:87]\n"
"v_exp_f32 v170, v170\n"
"v_mfma_f32_16x16x32_bf16 v[52:55], v[140:143], v[224:227], v[52:55]\n"
"v_cvt_pk_bf16_f32 v103, v170, v171\n"
"v_cvt_pk_bf16_f32 v110, v172, v173\n"
"v_mfma_f32_16x16x32_bf16 v[52:55], v[136:139], a[252:255], v[52:55]\n"
"v_cvt_pk_bf16_f32 v123, v154, v155\n"
"v_cvt_pk_bf16_f32 v101, v158, v159\n"
"v_mfma_f32_16x16x32_bf16 v[20:23], v[132:135], v[224:227], v[20:23]\n"
"v_cvt_pk_bf16_f32 v100, v156, v157\n"
"v_cvt_pk_bf16_f32 v102, v168, v169\n"
"v_mfma_f32_16x16x32_bf16 v[20:23], v[128:131], a[252:255], v[20:23]\n"
"v_exp_f32 v179, v179\n"
"v_mfma_f32_16x16x32_bf16 a[228:231], v[124:127], a[252:255], a[228:231]\n"
"s_waitcnt lgkmcnt(2)\n"
"ds_read_b64_tr_b16 a[252:253], v235 offset:49152\n"
"ds_read_b64_tr_b16 v[226:227], v239 offset:53248\n"
"v_mfma_f32_16x16x32_bf16 a[216:219], v[120:123], a[244:247], a[216:219]\n"
"s_add_u32 m0, s44, 81920\n"
"ds_read_b64_tr_b16 v[224:225], v239 offset:49152\n"
"v_cvt_pk_bf16_f32 v119, v178, v179\n"
"v_smfmac_f32_16x16x64_bf16 v[196:199], a[240:243], v[96:103], v241\n"
"buffer_load_dwordx4 v240, s[20:23], s56 offen nt lds\n"
"v_mfma_f32_16x16x32_bf16 v[8:11], v[96:99], a[244:247], v[8:11]\n"
"s_waitcnt lgkmcnt(3)\n"
"ds_read_b64_tr_b16 a[254:255], v235 offset:53248\n"
"v_cvt_pk_bf16_f32 v118, v176, v177\n"
"v_mfma_f32_16x16x32_bf16 v[8:11], v[100:103], a[248:251], v[8:11]\n"
"v_exp_f32 v174, v174\n"
"v_smfmac_f32_16x16x64_bf16 v[200:203], a[240:243], v[112:119], v241\n"
"v_cvt_pk_bf16_f32 v109, v162, v163\n"
"v_cvt_pk_bf16_f32 v111, v174, v175\n"
"v_mfma_f32_16x16x32_bf16 v[40:43], v[104:107], a[244:247], v[40:43]\n"
"ds_read_b128 v[212:215], v229 offset:0\n"
"v_and_b32 v245, 0x3f0, v242\n"
"v_mfma_f32_16x16x32_bf16 v[40:43], v[108:111], a[248:251], v[40:43]\n"
"v_lshlrev_b32_e64 v244, 24, s43\n"
"v_mul_f32 v244, 0x45800000, v244\n"
"v_smfmac_f32_16x16x64_bf16 v[198:201], a[240:243], v[104:111], v241\n"
"ds_read_b128 v[208:211], v228 offset:0\n"
"v_add_f32 v196, v196, v197\n"
"v_mfma_f32_16x16x32_bf16 v[72:75], v[112:115], a[244:247], v[72:75]\n"
"ds_read_b64_tr_b16 a[244:245], v234 offset:49664\n"
"v_lshrrev_b32 v245, 1, v245\n"
"v_mfma_f32_16x16x32_bf16 v[72:75], v[116:119], a[248:251], v[72:75]\n"
"ds_read_b64_tr_b16 a[246:247], v234 offset:53760\n"
"v_sub_u32 v245, v242, v245\n"
"v_mfma_f32_16x16x32_bf16 v[202:205], a[240:243], v[120:123], v[202:205]\n"
"s_waitcnt lgkmcnt(4)\n"
"ds_read_b64_tr_b16 a[248:249], v238 offset:49664\n"
"v_add_f32 v200, v200, v201\n"
"v_mfma_f32_16x16x32_bf16 v[76:79], v[116:119], v[224:227], v[76:79]\n"
"v_add_f32 v198, v198, v199\n"
"v_add_f32 v254, v196, v198\n"
"v_mfma_f32_16x16x32_bf16 v[76:79], v[112:115], a[252:255], v[76:79]\n"
"ds_read_b64_tr_b16 a[250:251], v238 offset:53760\n"
"v_xor_b32 v250, 0x1000, v245\n"
"v_mfma_f32_16x16x32_bf16 v[44:47], v[108:111], v[224:227], v[44:47]\n"
"ds_read_b128 v[216:219], v228 offset:512\n"
"v_add_f32 v254, v254, v200\n"
"v_mfma_f32_16x16x32_bf16 v[44:47], v[104:107], a[252:255], v[44:47]\n"
"v_add_f32 v244, v244, v202\n"
"v_add_f32 v254, v254, v202\n"
"v_mfma_f32_16x16x32_bf16 v[12:15], v[100:103], v[224:227], v[12:15]\n"
"s_add_u32 s40, s56, s52\n"
"ds_read_b64_tr_b16 v[226:227], v239 offset:53760\n"
"v_min3_f32 v244, v244, v196, v198\n"
"v_mfma_f32_16x16x32_bf16 v[12:15], v[96:99], a[252:255], v[12:15]\n"
"buffer_load_dwordx4 v207, s[20:23], s40 offen offset:1024 nt lds\n"
"v_min_f32 v244, v244, v200\n"
"v_rcp_f32 v244, v244\n"
"v_mfma_f32_16x16x32_bf16 a[220:223], v[120:123], a[252:255], a[220:223]\n"
"s_waitcnt lgkmcnt(4)\n"
"v_fmac_f32 v254, 0x68800000, v244\n"
"v_cmp_nge_f32 vcc, s45, v254\n"
"s_cbranch_vccnz LSLOW_%=\nLBACK1_%=:\ns_mov_b64 vcc, s[64:65]\n"
"v_mfma_f32_16x16x32_bf16 a[232:235], v[120:123], a[244:247], a[232:235]\n"
"v_rcp_f32 v246, v196\n"
"v_accvgpr_mov_b32 a2, a98\n"
"v_accvgpr_mov_b32 a7, a103\n"
"v_accvgpr_mov_b32 a10, a106\n"
"v_accvgpr_mov_b32 a15, a111\n"
"v_accvgpr_mov_b32 a22, a118\n"
"v_accvgpr_mov_b32 a30, a126\n"
"v_bfe_u32 v149, v242, 8, 2\n"
"v_lshl_add_u32 v149, v149, 4, s43\n"
"v_mfma_f32_16x16x32_bf16 v[24:27], v[96:99], a[244:247], v[24:27]\n"
"s_waitcnt lgkmcnt(2)\n"
"ds_read_b128 v[124:127], v149 offset:0\n"
"v_accvgpr_mov_b32 a28, a124\n"
"v_mfma_f32_16x16x32_bf16 v[24:27], v[100:103], a[248:251], v[24:27]\n"
"ds_read_b128 v[132:135], v149 offset:512\n"
"v_mov_b32_dpp v246, v246 row_ror:12 row_mask:0x2 bank_mask:0xf\n"
"v_accvgpr_mov_b32 a3, a99\n"
"v_accvgpr_mov_b32 a12, a108\n"
"v_accvgpr_mov_b32 a18, a114\n"
"v_accvgpr_mov_b32 a19, a115\n"
"v_accvgpr_mov_b32 a26, a122\n"
"v_accvgpr_mov_b32 a36, a132\n"
"v_xor_b32 v148, 0x400, v243\n"
"v_xor_b32 v150, 64, v149\n"
"v_accvgpr_mov_b32 a124, a76\n"
"v_mfma_f32_16x16x32_bf16 v[56:59], v[104:107], a[244:247], v[56:59]\n"
"ds_read_b128 v[128:131], v150 offset:0\n"
"v_accvgpr_mov_b32 a39, a135\n"
"v_mfma_f32_16x16x32_bf16 v[56:59], v[108:111], a[248:251], v[56:59]\n"
"ds_read_b128 v[136:139], v150 offset:512\n"
"v_rcp_f32 v247, v198\n"
"v_mov_b32_dpp v246, v246 row_ror:8 row_mask:0x4 bank_mask:0xf\n"
"v_accvgpr_mov_b32 a0, a96\n"
"v_accvgpr_mov_b32 a1, a97\n"
"v_accvgpr_mov_b32 a4, a100\n"
"v_accvgpr_mov_b32 a5, a101\n"
"v_accvgpr_mov_b32 a8, a104\n"
"v_accvgpr_mov_b32 a16, a112\n"
"v_accvgpr_mov_b32 a25, a121\n"
"v_mfma_f32_16x16x32_bf16 v[88:91], v[112:115], a[244:247], v[88:91]\n"
"v_accvgpr_mov_b32 a20, a116\n"
"v_accvgpr_mov_b32 a29, a125\n"
"v_mfma_f32_16x16x32_bf16 v[88:91], v[116:119], a[248:251], v[88:91]\n"
"s_waitcnt lgkmcnt(3)\n"
"ds_read_b128 a[192:195], v243 offset:4096\n"
"ds_read_b128 a[200:203], v243 offset:6144\n"
"v_accvgpr_mov_b32 a6, a102\n"
"v_accvgpr_mov_b32 a9, a105\n"
"v_accvgpr_mov_b32 a11, a107\n"
"v_accvgpr_mov_b32 a13, a109\n"
"v_accvgpr_mov_b32 a14, a110\n"
"v_accvgpr_mov_b32 a17, a113\n"
"v_accvgpr_mov_b32 a23, a119\n"
"v_accvgpr_mov_b32 a31, a127\n"
"v_accvgpr_mov_b32 a33, a129\n"
"v_accvgpr_mov_b32 a35, a131\n"
"v_accvgpr_mov_b32 a44, a140\n"
"v_accvgpr_mov_b32 a47, a143\n"
"v_accvgpr_mov_b32 a107, a59\n"
"v_mfma_f32_16x16x32_bf16 v[180:183], v[124:127], a[0:3], 0\n"
"s_waitcnt lgkmcnt(3)\n"
"v_xor_b32 v244, 0x11000, v242\n"
"v_accvgpr_mov_b32 a132, a84\n"
"v_mfma_f32_16x16x32_bf16 v[180:183], v[128:131], a[4:7], v[180:183]\n"
"v_accvgpr_mov_b32 a34, a130\n"
"v_accvgpr_mov_b32 a43, a139\n"
"v_mfma_f32_16x16x32_bf16 v[180:183], v[132:135], a[8:11], v[180:183]\n"
"s_waitcnt lgkmcnt(2)\n"
"ds_read_b128 a[204:207], v148 offset:6144\n"
"v_accvgpr_mov_b32 a37, a133\n"
"v_mfma_f32_16x16x32_bf16 v[180:183], v[136:139], a[12:15], v[180:183]\n"
"ds_read_b128 a[196:199], v148 offset:4096\n"
"v_accvgpr_mov_b32 a21, a117\n"
"v_accvgpr_mov_b32 a24, a120\n"
"v_accvgpr_mov_b32 a32, a128\n"
"v_accvgpr_mov_b32 a38, a134\n"
"v_accvgpr_mov_b32 a40, a136\n"
"v_accvgpr_mov_b32 a42, a138\n"
"v_accvgpr_mov_b32 a46, a142\n"
"v_accvgpr_mov_b32 a117, a69\n"
"v_accvgpr_mov_b32 a111, a63\n"
"v_mfma_f32_16x16x32_bf16 v[184:187], v[124:127], a[16:19], 0\n"
"v_mov_b32_dpp v246, v246 row_ror:4 row_mask:0x8 bank_mask:0xf\n"
"v_accvgpr_mov_b32 a27, a123\n"
"v_mfma_f32_16x16x32_bf16 v[184:187], v[128:131], a[20:23], v[184:187]\n"
"v_mov_b32_dpp v247, v247 row_ror:12 row_mask:0x2 bank_mask:0xf\n"
"v_accvgpr_mov_b32 a97, a49\n"
"v_mfma_f32_16x16x32_bf16 v[184:187], v[132:135], a[24:27], v[184:187]\n"
"s_add_u32 s40, s56, s53\n"
"ds_read_b64_tr_b16 v[224:225], v239 offset:49664\n"
"v_accvgpr_mov_b32 a112, a64\n"
"v_mfma_f32_16x16x32_bf16 v[184:187], v[136:139], a[28:31], v[184:187]\n"
"buffer_load_dwordx4 v240, s[20:23], s40 offen offset:2048 nt lds\n"
"v_accvgpr_mov_b32 a41, a137\n"
"v_accvgpr_mov_b32 a126, a78\n"
"v_accvgpr_mov_b32 a114, a66\n"
"v_accvgpr_mov_b32 a137, a89\n"
"v_sub_f32 v180, s74, v180\n"
"v_sub_f32 v181, s74, v181\n"
"v_sub_f32 v183, s74, v183\n"
"v_mul_f32_dpp v26, v246, v26 row_newbcast:2 row_mask:0xf bank_mask:0xf\n"
"v_mfma_f32_16x16x32_bf16 v[188:191], v[124:127], a[32:35], 0\n"
"v_mov_b32_dpp v247, v247 row_ror:8 row_mask:0x4 bank_mask:0xf\n"
"v_accvgpr_mov_b32 a45, a141\n"
"v_mfma_f32_16x16x32_bf16 v[188:191], v[128:131], a[36:39], v[188:191]\n"
"v_mov_b32 v252, v192\n"
"v_accvgpr_mov_b32 a133, a85\n"
"v_mfma_f32_16x16x32_bf16 v[188:191], v[132:135], a[40:43], v[188:191]\n"
"v_accvgpr_mov_b32 a118, a70\n"
"v_accvgpr_mov_b32 a105, a57\n"
"v_mfma_f32_16x16x32_bf16 v[188:191], v[136:139], a[44:47], v[188:191]\n"
"s_waitcnt lgkmcnt(1)\n"
"v_accvgpr_mov_b32 a109, a61\n"
"v_mul_f32_dpp v11, v246, v11 row_newbcast:3 row_mask:0xf bank_mask:0xf\n"
"v_mfma_f32_16x16x32_bf16 v[192:195], v[124:127], a[192:195], 0\n"
"ds_read_b64_tr_b16 a[252:253], v235 offset:49664\n"
"v_sub_f32 v185, s74, v185\n"
"v_mfma_f32_16x16x32_bf16 v[192:195], v[128:131], a[196:199], v[192:195]\n"
"ds_read_b64_tr_b16 a[248:249], v236 offset:49152\n"
"v_sub_f32 v182, s74, v182\n"
"v_mfma_f32_16x16x32_bf16 v[192:195], v[132:135], a[200:203], v[192:195]\n"
"v_accvgpr_mov_b32 a138, a90\n"
"v_sub_f32 v184, s74, v184\n"
"v_mfma_f32_16x16x32_bf16 v[192:195], v[136:139], a[204:207], v[192:195]\n"
"ds_read_b64_tr_b16 a[254:255], v235 offset:53760\n"
"ds_read_b64_tr_b16 a[246:247], v232 offset:53248\n"
"v_mov_b32 v253, v202\n"
"v_accvgpr_mov_b32 a116, a68\n"
"v_accvgpr_mov_b32 a125, a77\n"
"v_accvgpr_mov_b32 a113, a65\n"
"v_accvgpr_mov_b32 a128, a80\n"
"v_accvgpr_mov_b32 a130, a82\n"
"v_accvgpr_mov_b32 a136, a88\n"
"v_sub_f32 v186, s74, v186\n"
"v_mfma_f32_16x16x32_bf16 v[152:155], v[208:211], a[0:3], v[180:183]\n"
"v_accvgpr_mov_b32 a103, a55\n"
"v_accvgpr_mov_b32 a120, a72\n"
"v_mfma_f32_16x16x32_bf16 v[152:155], v[212:215], a[4:7], v[152:155]\n"
"v_accvgpr_mov_b32 a141, a93\n"
"v_sub_f32 v187, s74, v187\n"
"v_mfma_f32_16x16x32_bf16 v[152:155], v[216:219], a[8:11], v[152:155]\n"
"s_add_u32 s40, s56, s54\n"
"v_accvgpr_mov_b32 a127, a79\n"
"v_accvgpr_mov_b32 a140, a92\n"
"v_mfma_f32_16x16x32_bf16 v[152:155], v[220:223], a[12:15], v[152:155]\n"
"buffer_load_dwordx4 v207, s[20:23], s40 offen offset:3072 nt lds\n"
"v_mov_b32_dpp v247, v247 row_ror:4 row_mask:0x8 bank_mask:0xf\n"
"v_accvgpr_mov_b32 a121, a73\n"
"v_accvgpr_mov_b32 a122, a74\n"
"v_accvgpr_mov_b32 a123, a75\n"
"v_sub_f32 v189, s74, v189\n"
"v_sub_f32 v190, s74, v190\n"
"v_sub_f32 v191, s74, v191\n"
"v_sub_f32 v192, s74, v192\n"
"v_mfma_f32_16x16x32_bf16 v[156:159], v[216:219], a[24:27], v[184:187]\n"
"s_add_u32 s55, s55, s59\n"
"v_accvgpr_mov_b32 a143, a95\n"
"v_accvgpr_mov_b32 a115, a67\n"
"v_mfma_f32_16x16x32_bf16 v[156:159], v[220:223], a[28:31], v[156:159]\n"
"s_or_b32 s55, s55, s58\n"
"v_accvgpr_mov_b32 a101, a53\n"
"v_sub_f32 v188, s74, v188\n"
"v_mfma_f32_16x16x32_bf16 v[156:159], v[208:211], a[16:19], v[156:159]\n"
"s_xor_b32 s56, s55, s57\n"
"v_exp_f32 v153, v153\n"
"v_mfma_f32_16x16x32_bf16 v[156:159], v[212:215], a[20:23], v[156:159]\n"
"ds_write_b64 v250, v[252:253] offset:52224\n"
"v_accvgpr_mov_b32 a100, a52\n"
"v_accvgpr_mov_b32 a135, a87\n"
"v_accvgpr_mov_b32 a98, a50\n"
"v_sub_f32 v193, s74, v193\n"
"v_sub_f32 v194, s74, v194\n"
"v_exp_f32 v152, v152\n"
"v_mfma_f32_16x16x32_bf16 v[160:163], v[208:211], a[32:35], v[188:191]\n"
"v_accvgpr_mov_b32 a110, a62\n"
"v_accvgpr_mov_b32 a96, a48\n"
"v_mfma_f32_16x16x32_bf16 v[160:163], v[212:215], a[36:39], v[160:163]\n"
"v_rcp_f32 v206, v200\n"
"v_mfma_f32_16x16x32_bf16 v[160:163], v[216:219], a[40:43], v[160:163]\n"
"v_accvgpr_mov_b32 a142, a94\n"
"v_sub_f32 v195, s74, v195\n"
"v_mfma_f32_16x16x32_bf16 v[160:163], v[220:223], a[44:47], v[160:163]\n"
"ds_write_b128 v244, a[216:219] offset:32768\n"
"v_accvgpr_mov_b32 a102, a54\n"
"v_accvgpr_mov_b32 a108, a60\n"
"v_accvgpr_mov_b32 a99, a51\n"
"v_accvgpr_mov_b32 a129, a81\n"
"v_mul_f32_dpp v25, v246, v25 row_newbcast:1 row_mask:0xf bank_mask:0xf\n"
"v_mfma_f32_16x16x32_bf16 v[164:167], v[216:219], a[200:203], v[192:195]\n"
"v_mov_b32_dpp v206, v206 row_ror:12 row_mask:0x2 bank_mask:0xf\n"
"v_accvgpr_mov_b32 a119, a71\n"
"v_mfma_f32_16x16x32_bf16 v[164:167], v[220:223], a[204:207], v[164:167]\n"
"v_accvgpr_mov_b32 a131, a83\n"
"v_accvgpr_mov_b32 a139, a91\n"
"v_mfma_f32_16x16x32_bf16 v[164:167], v[208:211], a[192:195], v[164:167]\n"
"ds_read_b128 v[208:211], v228 offset:4096\n"
"v_accvgpr_mov_b32 a106, a58\n"
"v_mfma_f32_16x16x32_bf16 v[164:167], v[212:215], a[196:199], v[164:167]\n"
"s_waitcnt lgkmcnt(2)\n"
"ds_read_b128 v[216:219], v228 offset:4608\n"
"ds_read_b128 v[220:223], v229 offset:4608\n"
"v_mov_b32_dpp v206, v206 row_ror:8 row_mask:0x4 bank_mask:0xf\n"
"v_accvgpr_mov_b32 a134, a86\n"
"v_accvgpr_mov_b32 a104, a56\n"
"v_exp_f32 v155, v155\n"
"v_exp_f32 v160, v160\n"
"s_branch LTC_%=\n"
"LTH2_%=:\n"
"v_mfma_f32_16x16x32_bf16 a[224:227], v[124:127], a[244:247], a[224:227]\n"
"v_exp_f32 v171, v171\n"
"v_mfma_f32_16x16x32_bf16 v[16:19], v[128:131], a[244:247], v[16:19]\n"
"s_waitcnt vmcnt(8)\n"
"v_exp_f32 v172, v172\n"
"v_mfma_f32_16x16x32_bf16 v[16:19], v[132:135], a[248:251], v[16:19]\n"
"s_barrier\n"
"v_mfma_f32_16x16x32_bf16 v[48:51], v[136:139], a[244:247], v[48:51]\n"
"v_exp_f32 v169, v169\n"
"v_mfma_f32_16x16x32_bf16 v[48:51], v[140:143], a[248:251], v[48:51]\n"
"ds_read_b128 v[220:223], v229 offset:512\n"
"v_cvt_pk_bf16_f32 v117, v166, v167\n"
"v_exp_f32 v176, v176\n"
"v_exp_f32 v177, v177\n"
"v_exp_f32 v178, v178\n"
"v_mfma_f32_16x16x32_bf16 v[80:83], v[144:147], a[244:247], v[80:83]\n"
"ds_read_b64_tr_b16 a[244:245], v234 offset:49152\n"
"v_cvt_pk_bf16_f32 v108, v160, v161\n"
"v_mfma_f32_16x16x32_bf16 v[80:83], v[148:151], a[248:251], v[80:83]\n"
"ds_read_b64_tr_b16 a[246:247], v234 offset:53248\n"
"ds_read_b64_tr_b16 a[248:249], v238 offset:49152\n"
"v_exp_f32 v162, v162\n"
"v_exp_f32 v168, v168\n"
"v_exp_f32 v173, v173\n"
"v_exp_f32 v175, v175\n"
"v_mfma_f32_16x16x32_bf16 v[84:87], v[148:151], v[224:227], v[84:87]\n"
"ds_read_b64_tr_b16 a[250:251], v238 offset:53248\n"
"v_cvt_pk_bf16_f32 v116, v164, v165\n"
"v_mfma_f32_16x16x32_bf16 v[84:87], v[144:147], a[252:255], v[84:87]\n"
"v_exp_f32 v170, v170\n"
"v_mfma_f32_16x16x32_bf16 v[52:55], v[140:143], v[224:227], v[52:55]\n"
"v_cvt_pk_bf16_f32 v103, v170, v171\n"
"v_cvt_pk_bf16_f32 v110, v172, v173\n"
"v_mfma_f32_16x16x32_bf16 v[52:55], v[136:139], a[252:255], v[52:55]\n"
"v_cvt_pk_bf16_f32 v123, v154, v155\n"
"v_cvt_pk_bf16_f32 v101, v158, v159\n"
"v_mfma_f32_16x16x32_bf16 v[20:23], v[132:135], v[224:227], v[20:23]\n"
"v_cvt_pk_bf16_f32 v100, v156, v157\n"
"v_cvt_pk_bf16_f32 v102, v168, v169\n"
"v_mfma_f32_16x16x32_bf16 v[20:23], v[128:131], a[252:255], v[20:23]\n"
"v_exp_f32 v179, v179\n"
"v_mfma_f32_16x16x32_bf16 a[228:231], v[124:127], a[252:255], a[228:231]\n"
"s_waitcnt lgkmcnt(2)\n"
"ds_read_b64_tr_b16 a[252:253], v235 offset:49152\n"
"ds_read_b64_tr_b16 v[226:227], v239 offset:53248\n"
"v_mfma_f32_16x16x32_bf16 a[216:219], v[120:123], a[244:247], a[216:219]\n"
"s_add_u32 m0, s44, 81920\n"
"ds_read_b64_tr_b16 v[224:225], v239 offset:49152\n"
"v_cvt_pk_bf16_f32 v119, v178, v179\n"
"v_smfmac_f32_16x16x64_bf16 v[196:199], a[240:243], v[96:103], v241\n"
"buffer_load_dwordx4 v240, s[20:23], s56 offen nt lds\n"
"v_mfma_f32_16x16x32_bf16 v[8:11], v[96:99], a[244:247], v[8:11]\n"
"s_waitcnt lgkmcnt(3)\n"
"ds_read_b64_tr_b16 a[254:255], v235 offset:53248\n"
"v_cvt_pk_bf16_f32 v118, v176, v177\n"
"v_mfma_f32_16x16x32_bf16 v[8:11], v[100:103], a[248:251], v[8:11]\n"
"v_exp_f32 v174, v174\n"
"v_smfmac_f32_16x16x64_bf16 v[200:203], a[240:243], v[112:119], v241\n"
"v_cvt_pk_bf16_f32 v109, v162, v163\n"
"v_cvt_pk_bf16_f32 v111, v174, v175\n"
"v_mfma_f32_16x16x32_bf16 v[40:43], v[104:107], a[244:247], v[40:43]\n"
"ds_read_b128 v[212:215], v229 offset:0\n"
"v_and_b32 v245, 0x3f0, v242\n"
"v_mfma_f32_16x16x32_bf16 v[40:43], v[108:111], a[248:251], v[40:43]\n"
"v_lshlrev_b32_e64 v244, 24, s43\n"
"v_mul_f32 v244, 0x45800000, v244\n"
"v_smfmac_f32_16x16x64_bf16 v[198:201], a[240:243], v[104:111], v241\n"
"ds_read_b128 v[208:211], v228 offset:0\n"
"v_add_f32 v196, v196, v197\n"
"v_mfma_f32_16x16x32_bf16 v[72:75], v[112:115], a[244:247], v[72:75]\n"
"ds_read_b64_tr_b16 a[244:245], v234 offset:49664\n"
"v_lshrrev_b32 v245, 1, v245\n"
"v_mfma_f32_16x16x32_bf16 v[72:75], v[116:119], a[248:251], v[72:75]\n"
"ds_read_b64_tr_b16 a[246:247], v234 offset:53760\n"
"v_sub_u32 v245, v242, v245\n"
"v_mfma_f32_16x16x32_bf16 v[202:205], a[240:243], v[120:123], v[202:205]\n"
"s_waitcnt lgkmcnt(4)\n"
"ds_read_b64_tr_b16 a[248:249], v238 offset:49664\n"
"v_add_f32 v200, v200, v201\n"
"v_mfma_f32_16x16x32_bf16 v[76:79], v[116:119], v[224:227], v[76:79]\n"
"v_add_f32 v198, v198, v199\n"
"v_add_f32 v254, v196, v198\n"
"v_mfma_f32_16x16x32_bf16 v[76:79], v[112:115], a[252:255], v[76:79]\n"
"ds_read_b64_tr_b16 a[250:251], v238 offset:53760\n"
"v_xor_b32 v250, 0x1000, v245\n"
"v_mfma_f32_16x16x32_bf16 v[44:47], v[108:111], v[224:227], v[44:47]\n"
"ds_read_b128 v[216:219], v228 offset:512\n"
"v_add_f32 v254, v254, v200\n"
"v_mfma_f32_16x16x32_bf16 v[44:47], v[104:107], a[252:255], v[44:47]\n"
"v_add_f32 v244, v244, v202\n"
"v_add_f32 v254, v254, v202\n"
"v_mfma_f32_16x16x32_bf16 v[12:15], v[100:103], v[224:227], v[12:15]\n"
"s_add_u32 s40, s56, s52\n"
"ds_read_b64_tr_b16 v[226:227], v239 offset:53760\n"
"v_min3_f32 v244, v244, v196, v198\n"
"v_mfma_f32_16x16x32_bf16 v[12:15], v[96:99], a[252:255], v[12:15]\n"
"buffer_load_dwordx4 v207, s[20:23], s40 offen offset:1024 nt lds\n"
"v_min_f32 v244, v244, v200\n"
"v_rcp_f32 v244, v244\n"
"v_mfma_f32_16x16x32_bf16 a[220:223], v[120:123], a[252:255], a[220:223]\n"
"s_waitcnt lgkmcnt(4)\n"
"v_fmac_f32 v254, 0x68800000, v244\n"
"v_cmp_nge_f32 vcc, s45, v254\n"
"s_cbranch_vccnz LSLOW_%=\nLBACK2_%=:\ns_mov_b64 vcc, s[64:65]\n"
"v_mfma_f32_16x16x32_bf16 a[232:235], v[120:123], a[244:247], a[232:235]\n"
"v_rcp_f32 v246, v196\n"
"v_accvgpr_mov_b32 a2, a146\n"
"v_accvgpr_mov_b32 a7, a151\n"
"v_accvgpr_mov_b32 a10, a154\n"
"v_accvgpr_mov_b32 a15, a159\n"
"v_accvgpr_mov_b32 a22, a166\n"
"v_accvgpr_mov_b32 a30, a174\n"
"v_bfe_u32 v149, v242, 8, 2\n"
"v_lshl_add_u32 v149, v149, 4, s43\n"
"v_mfma_f32_16x16x32_bf16 v[24:27], v[96:99], a[244:247], v[24:27]\n"
"s_waitcnt lgkmcnt(2)\n"
"ds_read_b128 v[124:127], v149 offset:0\n"
"v_accvgpr_mov_b32 a28, a172\n"
"v_mfma_f32_16x16x32_bf16 v[24:27], v[100:103], a[248:251], v[24:27]\n"
"ds_read_b128 v[132:135], v149 offset:512\n"
"v_mov_b32_dpp v246, v246 row_ror:12 row_mask:0x2 bank_mask:0xf\n"
"v_accvgpr_mov_b32 a3, a147\n"
"v_accvgpr_mov_b32 a12, a156\n"
"v_accvgpr_mov_b32 a18, a162\n"
"v_accvgpr_mov_b32 a19, a163\n"
"v_accvgpr_mov_b32 a26, a170\n"
"v_accvgpr_mov_b32 a36, a180\n"
"v_xor_b32 v148, 0x400, v243\n"
"v_xor_b32 v150, 64, v149\n"
"v_accvgpr_mov_b32 a172, a76\n"
"v_mfma_f32_16x16x32_bf16 v[56:59], v[104:107], a[244:247], v[56:59]\n"
"ds_read_b128 v[128:131], v150 offset:0\n"
"v_accvgpr_mov_b32 a39, a183\n"
"v_mfma_f32_16x16x32_bf16 v[56:59], v[108:111], a[248:251], v[56:59]\n"
"ds_read_b128 v[136:139], v150 offset:512\n"
"v_rcp_f32 v247, v198\n"
"v_mov_b32_dpp v246, v246 row_ror:8 row_mask:0x4 bank_mask:0xf\n"
"v_accvgpr_mov_b32 a0, a144\n"
"v_accvgpr_mov_b32 a1, a145\n"
"v_accvgpr_mov_b32 a4, a148\n"
"v_accvgpr_mov_b32 a5, a149\n"
"v_accvgpr_mov_b32 a8, a152\n"
"v_accvgpr_mov_b32 a16, a160\n"
"v_accvgpr_mov_b32 a25, a169\n"
"v_mfma_f32_16x16x32_bf16 v[88:91], v[112:115], a[244:247], v[88:91]\n"
"v_accvgpr_mov_b32 a20, a164\n"
"v_accvgpr_mov_b32 a29, a173\n"
"v_mfma_f32_16x16x32_bf16 v[88:91], v[116:119], a[248:251], v[88:91]\n"
"s_waitcnt lgkmcnt(3)\n"
"ds_read_b128 a[192:195], v243 offset:4096\n"
"ds_read_b128 a[200:203], v243 offset:6144\n"
"v_accvgpr_mov_b32 a6, a150\n"
"v_accvgpr_mov_b32 a9, a153\n"
"v_accvgpr_mov_b32 a11, a155\n"
"v_accvgpr_mov_b32 a13, a157\n"
"v_accvgpr_mov_b32 a14, a158\n"
"v_accvgpr_mov_b32 a17, a161\n"
"v_accvgpr_mov_b32 a23, a167\n"
"v_accvgpr_mov_b32 a31, a175\n"
"v_accvgpr_mov_b32 a33, a177\n"
"v_accvgpr_mov_b32 a35, a179\n"
"v_accvgpr_mov_b32 a44, a188\n"
"v_accvgpr_mov_b32 a47, a191\n"
"v_accvgpr_mov_b32 a155, a59\n"
"v_mfma_f32_16x16x32_bf16 v[180:183], v[124:127], a[0:3], 0\n"
"s_waitcnt lgkmcnt(3)\n"
"v_xor_b32 v244, 0x11000, v242\n"
"v_accvgpr_mov_b32 a180, a84\n"
"v_mfma_f32_16x16x32_bf16 v[180:183], v[128:131], a[4:7], v[180:183]\n"
"v_accvgpr_mov_b32 a34, a178\n"
"v_accvgpr_mov_b32 a43, a187\n"
"v_mfma_f32_16x16x32_bf16 v[180:183], v[132:135], a[8:11], v[180:183]\n"
"s_waitcnt lgkmcnt(2)\n"
"ds_read_b128 a[204:207], v148 offset:6144\n"
"v_accvgpr_mov_b32 a37, a181\n"
"v_mfma_f32_16x16x32_bf16 v[180:183], v[136:139], a[12:15], v[180:183]\n"
"ds_read_b128 a[196:199], v148 offset:4096\n"
"v_accvgpr_mov_b32 a21, a165\n"
"v_accvgpr_mov_b32 a24, a168\n"
"v_accvgpr_mov_b32 a32, a176\n"
"v_accvgpr_mov_b32 a38, a182\n"
"v_accvgpr_mov_b32 a40, a184\n"
"v_accvgpr_mov_b32 a42, a186\n"
"v_accvgpr_mov_b32 a46, a190\n"
"v_accvgpr_mov_b32 a165, a69\n"
"v_accvgpr_mov_b32 a159, a63\n"
"v_mfma_f32_16x16x32_bf16 v[184:187], v[124:127], a[16:19], 0\n"
"v_mov_b32_dpp v246, v246 row_ror:4 row_mask:0x8 bank_mask:0xf\n"
"v_accvgpr_mov_b32 a27, a171\n"
"v_mfma_f32_16x16x32_bf16 v[184:187], v[128:131], a[20:23], v[184:187]\n"
"v_mov_b32_dpp v247, v247 row_ror:12 row_mask:0x2 bank_mask:0xf\n"
"v_accvgpr_mov_b32 a145, a49\n"
"v_mfma_f32_16x16x32_bf16 v[184:187], v[132:135], a[24:27], v[184:187]\n"
"s_add_u32 s40, s56, s53\n"
"ds_read_b64_tr_b16 v[224:225], v239 offset:49664\n"
"v_accvgpr_mov_b32 a160, a64\n"
"v_mfma_f32_16x16x32_bf16 v[184:187], v[136:139], a[28:31], v[184:187]\n"
"buffer_load_dwordx4 v240, s[20:23], s40 offen offset:2048 nt lds\n"
"v_accvgpr_mov_b32 a41, a185\n"
"v_accvgpr_mov_b32 a174, a78\n"
"v_accvgpr_mov_b32 a162, a66\n"
"v_accvgpr_mov_b32 a185, a89\n"
"v_sub_f32 v180, s74, v180\n"
"v_sub_f32 v181, s74, v181\n"
"v_sub_f32 v183, s74, v183\n"
"v_mul_f32_dpp v26, v246, v26 row_newbcast:2 row_mask:0xf bank_mask:0xf\n"
"v_mfma_f32_16x16x32_bf16 v[188:191], v[124:127], a[32:35], 0\n"
"v_mov_b32_dpp v247, v247 row_ror:8 row_mask:0x4 bank_mask:0xf\n"
"v_accvgpr_mov_b32 a45, a189\n"
"v_mfma_f32_16x16x32_bf16 v[188:191], v[128:131], a[36:39], v[188:191]\n"
"v_mov_b32 v252, v192\n"
"v_accvgpr_mov_b32 a181, a85\n"
"v_mfma_f32_16x16x32_bf16 v[188:191], v[132:135], a[40:43], v[188:191]\n"
"v_accvgpr_mov_b32 a166, a70\n"
"v_accvgpr_mov_b32 a153, a57\n"
"v_mfma_f32_16x16x32_bf16 v[188:191], v[136:139], a[44:47], v[188:191]\n"
"s_waitcnt lgkmcnt(1)\n"
"v_accvgpr_mov_b32 a157, a61\n"
"v_mul_f32_dpp v11, v246, v11 row_newbcast:3 row_mask:0xf bank_mask:0xf\n"
"v_mfma_f32_16x16x32_bf16 v[192:195], v[124:127], a[192:195], 0\n"
"ds_read_b64_tr_b16 a[252:253], v235 offset:49664\n"
"v_sub_f32 v185, s74, v185\n"
"v_mfma_f32_16x16x32_bf16 v[192:195], v[128:131], a[196:199], v[192:195]\n"
"ds_read_b64_tr_b16 a[248:249], v236 offset:49152\n"
"v_sub_f32 v182, s74, v182\n"
"v_mfma_f32_16x16x32_bf16 v[192:195], v[132:135], a[200:203], v[192:195]\n"
"v_accvgpr_mov_b32 a186, a90\n"
"v_sub_f32 v184, s74, v184\n"
"v_mfma_f32_16x16x32_bf16 v[192:195], v[136:139], a[204:207], v[192:195]\n"
"ds_read_b64_tr_b16 a[254:255], v235 offset:53760\n"
"ds_read_b64_tr_b16 a[246:247], v232 offset:53248\n"
"v_mov_b32 v253, v202\n"
"v_accvgpr_mov_b32 a164, a68\n"
"v_accvgpr_mov_b32 a173, a77\n"
"v_accvgpr_mov_b32 a161, a65\n"
"v_accvgpr_mov_b32 a176, a80\n"
"v_accvgpr_mov_b32 a178, a82\n"
"v_accvgpr_mov_b32 a184, a88\n"
"v_sub_f32 v186, s74, v186\n"
"v_mfma_f32_16x16x32_bf16 v[152:155], v[208:211], a[0:3], v[180:183]\n"
"v_accvgpr_mov_b32 a151, a55\n"
"v_accvgpr_mov_b32 a168, a72\n"
"v_mfma_f32_16x16x32_bf16 v[152:155], v[212:215], a[4:7], v[152:155]\n"
"v_accvgpr_mov_b32 a189, a93\n"
"v_sub_f32 v187, s74, v187\n"
"v_mfma_f32_16x16x32_bf16 v[152:155], v[216:219], a[8:11], v[152:155]\n"
"s_add_u32 s40, s56, s54\n"
"v_accvgpr_mov_b32 a175, a79\n"
"v_accvgpr_mov_b32 a188, a92\n"
"v_mfma_f32_16x16x32_bf16 v[152:155], v[220:223], a[12:15], v[152:155]\n"
"buffer_load_dwordx4 v207, s[20:23], s40 offen offset:3072 nt lds\n"
"v_mov_b32_dpp v247, v247 row_ror:4 row_mask:0x8 bank_mask:0xf\n"
"v_accvgpr_mov_b32 a169, a73\n"
"v_accvgpr_mov_b32 a170, a74\n"
"v_accvgpr_mov_b32 a171, a75\n"
"v_sub_f32 v189, s74, v189\n"
"v_sub_f32 v190, s74, v190\n"
"v_sub_f32 v191, s74, v191\n"
"v_sub_f32 v192, s74, v192\n"
"v_mfma_f32_16x16x32_bf16 v[156:159], v[216:219], a[24:27], v[184:187]\n"
"s_add_u32 s55, s55, s59\n"
"v_accvgpr_mov_b32 a191, a95\n"
"v_accvgpr_mov_b32 a163, a67\n"
"v_mfma_f32_16x16x32_bf16 v[156:159], v[220:223], a[28:31], v[156:159]\n"
"s_or_b32 s55, s55, s58\n"
"v_accvgpr_mov_b32 a149, a53\n"
"v_sub_f32 v188, s74, v188\n"
"v_mfma_f32_16x16x32_bf16 v[156:159], v[208:211], a[16:19], v[156:159]\n"
"s_xor_b32 s56, s55, s57\n"
"v_exp_f32 v153, v153\n"
"v_mfma_f32_16x16x32_bf16 v[156:159], v[212:215], a[20:23], v[156:159]\n"
"ds_write_b64 v250, v[252:253] offset:52224\n"
"v_accvgpr_mov_b32 a148, a52\n"
"v_accvgpr_mov_b32 a183, a87\n"
"v_accvgpr_mov_b32 a146, a50\n"
"v_sub_f32 v193, s74, v193\n"
"v_sub_f32 v194, s74, v194\n"
"v_exp_f32 v152, v152\n"
"v_mfma_f32_16x16x32_bf16 v[160:163], v[208:211], a[32:35], v[188:191]\n"
"v_accvgpr_mov_b32 a158, a62\n"
"v_accvgpr_mov_b32 a144, a48\n"
"v_mfma_f32_16x16x32_bf16 v[160:163], v[212:215], a[36:39], v[160:163]\n"
"v_rcp_f32 v206, v200\n"
"v_mfma_f32_16x16x32_bf16 v[160:163], v[216:219], a[40:43], v[160:163]\n"
"v_accvgpr_mov_b32 a190, a94\n"
"v_sub_f32 v195, s74, v195\n"
"v_mfma_f32_16x16x32_bf16 v[160:163], v[220:223], a[44:47], v[160:163]\n"
"ds_write_b128 v244, a[216:219] offset:32768\n"
"v_accvgpr_mov_b32 a150, a54\n"
"v_accvgpr_mov_b32 a156, a60\n"
"v_accvgpr_mov_b32 a147, a51\n"
"v_accvgpr_mov_b32 a177, a81\n"
"v_mul_f32_dpp v25, v246, v25 row_newbcast:1 row_mask:0xf bank_mask:0xf\n"
"v_mfma_f32_16x16x32_bf16 v[164:167], v[216:219], a[200:203], v[192:195]\n"
"v_mov_b32_dpp v206, v206 row_ror:12 row_mask:0x2 bank_mask:0xf\n"
"v_accvgpr_mov_b32 a167, a71\n"
"v_mfma_f32_16x16x32_bf16 v[164:167], v[220:223], a[204:207], v[164:167]\n"
"v_accvgpr_mov_b32 a179, a83\n"
"v_accvgpr_mov_b32 a187, a91\n"
"v_mfma_f32_16x16x32_bf16 v[164:167], v[208:211], a[192:195], v[164:167]\n"
"ds_read_b128 v[208:211], v228 offset:4096\n"
"v_accvgpr_mov_b32 a154, a58\n"
"v_mfma_f32_16x16x32_bf16 v[164:167], v[212:215], a[196:199], v[164:167]\n"
"s_waitcnt lgkmcnt(2)\n"
"ds_read_b128 v[216:219], v228 offset:4608\n"
"ds_read_b128 v[220:223], v229 offset:4608\n"
"v_mov_b32_dpp v206, v206 row_ror:8 row_mask:0x4 bank_mask:0xf\n"
"v_accvgpr_mov_b32 a182, a86\n"
"v_accvgpr_mov_b32 a152, a56\n"
"v_exp_f32 v155, v155\n"
"v_exp_f32 v160, v160\n"
"LTC_%=:\n"
"s_waitcnt lgkmcnt(4)\n"
"v_mfma_f32_16x16x32_bf16 v[92:95], v[116:119], v[224:227], v[92:95]\n"
"v_exp_f32 v154, v154\n"
"v_mfma_f32_16x16x32_bf16 v[92:95], v[112:115], a[252:255], v[92:95]\n"
"ds_read_b128 v[212:215], v229 offset:4096\n"
"v_mul_f32_dpp v24, v246, v24 row_newbcast:0 row_mask:0xf bank_mask:0xf\n"
"v_mfma_f32_16x16x32_bf16 v[60:63], v[108:111], v[224:227], v[60:63]\n"
"s_add_u32 m0, s44, 32768\n"
"v_exp_f32 v163, v163\n"
"v_mfma_f32_16x16x32_bf16 v[60:63], v[104:107], a[252:255], v[60:63]\n"
"buffer_load_dwordx4 v240, s[16:19], s56 offen nt lds\n"
"v_mul_f32_dpp v8, v246, v8 row_newbcast:0 row_mask:0xf bank_mask:0xf\n"
"v_exp_f32 v156, v156\n"
"v_exp_f32 v158, v158\n"
"v_exp_f32 v161, v161\n"
"v_exp_f32 v165, v165\n"
"v_mfma_f32_16x16x32_bf16 v[28:31], v[100:103], v[224:227], v[28:31]\n"
"v_cvt_pk_bf16_f32 v128, v152, v153\n"
"v_cvt_pk_bf16_f32 v129, v154, v155\n"
"v_mfma_f32_16x16x32_bf16 v[28:31], v[96:99], a[252:255], v[28:31]\n"
"v_mov_b32_dpp v206, v206 row_ror:4 row_mask:0x8 bank_mask:0xf\n"
"v_mul_f32_dpp v9, v246, v9 row_newbcast:1 row_mask:0xf bank_mask:0xf\n"
"v_mfma_f32_16x16x32_bf16 a[236:239], v[120:123], a[252:255], a[236:239]\n"
"s_waitcnt lgkmcnt(2)\n"
"v_exp_f32 v162, v162\n"
"v_mfma_f32_16x16x32_bf16 v[168:171], v[216:219], a[8:11], v[180:183]\n"
"s_waitcnt lgkmcnt(1)\n"
"v_exp_f32 v167, v167\n"
"v_mfma_f32_16x16x32_bf16 v[168:171], v[220:223], a[12:15], v[168:171]\n"
"v_mul_f32_dpp v12, v246, v12 row_newbcast:0 row_mask:0xf bank_mask:0xf\n"
"v_mul_f32_dpp v46, v247, v46 row_newbcast:2 row_mask:0xf bank_mask:0xf\n"
"v_mfma_f32_16x16x32_bf16 v[168:171], v[208:211], a[0:3], v[168:171]\n"
"s_waitcnt lgkmcnt(0)\n"
"ds_read_b64_tr_b16 a[244:245], v232 offset:49152\n"
"v_cvt_pk_bf16_f32 v145, v162, v163\n"
"v_mfma_f32_16x16x32_bf16 v[168:171], v[212:215], a[4:7], v[168:171]\n"
"v_exp_f32 v159, v159\n"
"v_mfma_f32_16x16x32_bf16 v[172:175], v[208:211], a[16:19], v[184:187]\n"
"ds_read_b64_tr_b16 a[252:253], v233 offset:49152\n"
"v_mul_f32_dpp v72, v206, v72 row_newbcast:0 row_mask:0xf bank_mask:0xf\n"
"v_mfma_f32_16x16x32_bf16 v[172:175], v[212:215], a[20:23], v[172:175]\n"
"v_exp_f32 v164, v164\n"
"v_mfma_f32_16x16x32_bf16 v[172:175], v[216:219], a[24:27], v[172:175]\n"
"ds_read_b64_tr_b16 a[250:251], v236 offset:53248\n"
"v_mul_f32_dpp v14, v246, v14 row_newbcast:2 row_mask:0xf bank_mask:0xf\n"
"v_mfma_f32_16x16x32_bf16 v[172:175], v[220:223], a[28:31], v[172:175]\n"
"ds_write_b128 v244, a[220:223] offset:33792\n"
"v_mul_f32_dpp v78, v206, v78 row_newbcast:2 row_mask:0xf bank_mask:0xf\n"
"v_exp_f32 v157, v157\n"
"v_mul_f32_dpp v89, v206, v89 row_newbcast:1 row_mask:0xf bank_mask:0xf\n"
"v_exp_f32 v168, v168\n"
"v_mfma_f32_16x16x32_bf16 v[176:179], v[216:219], a[40:43], v[188:191]\n"
"v_mul_f32_dpp v10, v246, v10 row_newbcast:2 row_mask:0xf bank_mask:0xf\n"
"v_mul_f32_dpp v40, v247, v40 row_newbcast:0 row_mask:0xf bank_mask:0xf\n"
"v_mfma_f32_16x16x32_bf16 v[176:179], v[220:223], a[44:47], v[176:179]\n"
"v_cvt_pk_bf16_f32 v137, v158, v159\n"
"v_mul_f32_dpp v28, v246, v28 row_newbcast:0 row_mask:0xf bank_mask:0xf\n"
"v_mfma_f32_16x16x32_bf16 v[176:179], v[208:211], a[32:35], v[176:179]\n"
"s_add_u32 s40, s56, s52\n"
"v_mul_f32_dpp v73, v206, v73 row_newbcast:1 row_mask:0xf bank_mask:0xf\n"
"v_cvt_pk_bf16_f32 v136, v156, v157\n"
"v_mfma_f32_16x16x32_bf16 v[176:179], v[212:215], a[36:39], v[176:179]\n"
"buffer_load_dwordx4 v207, s[16:19], s40 offen offset:1024 nt lds\n"
"v_mfma_f32_16x16x32_bf16 v[152:155], v[208:211], a[192:195], v[192:195]\n"
"v_mul_f32_dpp v76, v206, v76 row_newbcast:0 row_mask:0xf bank_mask:0xf\n"
"v_mul_f32_dpp v88, v206, v88 row_newbcast:0 row_mask:0xf bank_mask:0xf\n"
"v_mfma_f32_16x16x32_bf16 v[152:155], v[212:215], a[196:199], v[152:155]\n"
"ds_read_b128 v[208:211], v230 offset:0\n"
"v_cvt_pk_bf16_f32 v144, v160, v161\n"
"v_mfma_f32_16x16x32_bf16 v[152:155], v[216:219], a[200:203], v[152:155]\n"
"v_mul_f32_dpp v93, v206, v93 row_newbcast:1 row_mask:0xf bank_mask:0xf\n"
"v_cvt_pk_bf16_f32 v88, v88, v89\n"
"v_mfma_f32_16x16x32_bf16 v[152:155], v[220:223], a[204:207], v[152:155]\n"
"s_waitcnt lgkmcnt(1)\n"
"ds_read_b128 v[212:215], v231 offset:0\n"
"v_mul_f32_dpp v13, v246, v13 row_newbcast:1 row_mask:0xf bank_mask:0xf\n"
"v_mfma_f32_16x16x32_bf16 a[208:211], v[120:123], a[244:247], a[208:211]\n"
"v_exp_f32 v170, v170\n"
"v_mfma_f32_16x16x32_bf16 v[0:3], v[96:99], a[244:247], v[0:3]\n"
"ds_read_b128 v[216:219], v230 offset:512\n"
"v_cvt_pk_bf16_f32 v124, v164, v165\n"
"v_mfma_f32_16x16x32_bf16 v[0:3], v[100:103], a[248:251], v[0:3]\n"
"ds_read_b64_tr_b16 v[226:227], v237 offset:53248\n"
"v_mul_f32_dpp v15, v246, v15 row_newbcast:3 row_mask:0xf bank_mask:0xf\n"
"v_mul_f32_dpp v41, v247, v41 row_newbcast:1 row_mask:0xf bank_mask:0xf\n"
"v_mul_f32_dpp v42, v247, v42 row_newbcast:2 row_mask:0xf bank_mask:0xf\n"
"v_exp_f32 v166, v166\n"
"v_mul_f32_dpp v57, v247, v57 row_newbcast:1 row_mask:0xf bank_mask:0xf\n"
"v_mul_f32_dpp v91, v206, v91 row_newbcast:3 row_mask:0xf bank_mask:0xf\n"
"v_exp_f32 v172, v172\n"
"v_exp_f32 v177, v177\n"
"v_mfma_f32_16x16x32_bf16 v[32:35], v[104:107], a[244:247], v[32:35]\n"
"ds_read_b64_tr_b16 v[224:225], v237 offset:49152\n"
"ds_read_b128 v[220:223], v231 offset:512\n"
"v_mfma_f32_16x16x32_bf16 v[32:35], v[108:111], a[248:251], v[32:35]\n"
"ds_write_b128 v244, a[232:235] offset:34816\n"
"v_cvt_pk_bf16_f32 v8, v8, v9\n"
"v_mul_f32_dpp v75, v206, v75 row_newbcast:3 row_mask:0xf bank_mask:0xf\n"
"v_mul_f32_dpp v30, v246, v30 row_newbcast:2 row_mask:0xf bank_mask:0xf\n"
"v_mul_f32_dpp v94, v206, v94 row_newbcast:2 row_mask:0xf bank_mask:0xf\n"
"v_exp_f32 v176, v176\n"
"v_mfma_f32_16x16x32_bf16 v[64:67], v[112:115], a[244:247], v[64:67]\n"
"v_exp_f32 v173, v173\n"
"v_mfma_f32_16x16x32_bf16 v[64:67], v[116:119], a[248:251], v[64:67]\n"
"s_waitcnt lgkmcnt(6)\n"
"ds_write_b128 v244, a[236:239] offset:35840\n"
"v_mul_f32_dpp v77, v206, v77 row_newbcast:1 row_mask:0xf bank_mask:0xf\n"
"v_mul_f32_dpp v62, v247, v62 row_newbcast:2 row_mask:0xf bank_mask:0xf\n"
"v_exp_f32 v169, v169\n"
"v_cvt_pk_bf16_f32 v138, v172, v173\n"
"v_exp_f32 v153, v153\n"
"v_mfma_f32_16x16x32_bf16 v[156:159], v[208:211], a[0:3], v[180:183]\n"
"s_waitcnt lgkmcnt(6)\n"
"ds_read_b64_tr_b16 a[254:255], v233 offset:53248\n"
"v_mul_f32_dpp v63, v247, v63 row_newbcast:3 row_mask:0xf bank_mask:0xf\n"
"v_mfma_f32_16x16x32_bf16 v[156:159], v[212:215], a[4:7], v[156:159]\n"
"s_waitcnt lgkmcnt(6)\n"
"v_exp_f32 v174, v174\n"
"v_mfma_f32_16x16x32_bf16 v[156:159], v[216:219], a[8:11], v[156:159]\n"
"s_waitcnt lgkmcnt(3)\n"
"s_add_u32 s40, s56, s53\n"
"v_cvt_pk_bf16_f32 v40, v40, v41\n"
"v_mul_f32_dpp v0, v246, v0 row_newbcast:0 row_mask:0xf bank_mask:0xf\n"
"v_mfma_f32_16x16x32_bf16 v[156:159], v[220:223], a[12:15], v[156:159]\n"
"buffer_load_dwordx4 v240, s[16:19], s40 offen offset:2048 nt lds\n"
"v_cvt_pk_bf16_f32 v9, v10, v11\n"
"v_mul_f32_dpp v44, v247, v44 row_newbcast:0 row_mask:0xf bank_mask:0xf\n"
"v_cvt_pk_bf16_f32 v125, v166, v167\n"
"v_mul_f32_dpp v31, v246, v31 row_newbcast:3 row_mask:0xf bank_mask:0xf\n"
"v_exp_f32 v152, v152\n"
"v_mul_f32_dpp v34, v247, v34 row_newbcast:2 row_mask:0xf bank_mask:0xf\n"
"v_mov_b64 v[200:201], 0\n"
"v_mfma_f32_16x16x32_bf16 v[160:163], v[216:219], a[24:27], v[184:187]\n"
"v_mul_f32_dpp v60, v247, v60 row_newbcast:0 row_mask:0xf bank_mask:0xf\n"
"v_xor_b32 v244, 0x1000, v244\n"
"v_mfma_f32_16x16x32_bf16 v[160:163], v[220:223], a[28:31], v[160:163]\n"
"v_exp_f32 v178, v178\n"
"v_mfma_f32_16x16x32_bf16 v[160:163], v[208:211], a[16:19], v[160:163]\n"
"ds_read_b64_tr_b16 a[250:251], v236 offset:53760\n"
"v_mul_f32_dpp v33, v247, v33 row_newbcast:1 row_mask:0xf bank_mask:0xf\n"
"v_mfma_f32_16x16x32_bf16 v[160:163], v[212:215], a[20:23], v[160:163]\n"
"ds_read_b64_tr_b16 a[244:245], v232 offset:49664\n"
"v_mul_f32_dpp v43, v247, v43 row_newbcast:3 row_mask:0xf bank_mask:0xf\n"
"v_mfma_f32_16x16x32_bf16 v[164:167], v[208:211], a[32:35], v[188:191]\n"
"v_mul_f32_dpp v45, v247, v45 row_newbcast:1 row_mask:0xf bank_mask:0xf\n"
"v_cvt_pk_bf16_f32 v72, v72, v73\n"
"v_mfma_f32_16x16x32_bf16 v[164:167], v[212:215], a[36:39], v[164:167]\n"
"ds_read_b128 v[208:211], v230 offset:4096\n"
"v_mul_f32_dpp v74, v206, v74 row_newbcast:2 row_mask:0xf bank_mask:0xf\n"
"v_mfma_f32_16x16x32_bf16 v[164:167], v[216:219], a[40:43], v[164:167]\n"
"v_mul_f32_dpp v56, v247, v56 row_newbcast:0 row_mask:0xf bank_mask:0xf\n"
"v_mul_f32_dpp v58, v247, v58 row_newbcast:2 row_mask:0xf bank_mask:0xf\n"
"v_mfma_f32_16x16x32_bf16 v[164:167], v[220:223], a[44:47], v[164:167]\n"
"ds_read_b128 v[212:215], v231 offset:4096\n"
"ds_read_b128 v[220:223], v231 offset:4608\n"
"v_mul_f32_dpp v47, v247, v47 row_newbcast:3 row_mask:0xf bank_mask:0xf\n"
"v_cvt_pk_bf16_f32 v73, v74, v75\n"
"v_mul_f32_dpp v29, v246, v29 row_newbcast:1 row_mask:0xf bank_mask:0xf\n"
"v_exp_f32 v175, v175\n"
"v_exp_f32 v179, v179\n"
"v_mul_f32_dpp v1, v246, v1 row_newbcast:1 row_mask:0xf bank_mask:0xf\n"
"v_mul_f32_dpp v2, v246, v2 row_newbcast:2 row_mask:0xf bank_mask:0xf\n"
"v_mfma_f32_16x16x32_bf16 v[68:71], v[116:119], v[224:227], v[68:71]\n"
"s_waitcnt lgkmcnt(5)\n"
"v_exp_f32 v171, v171\n"
"v_mfma_f32_16x16x32_bf16 v[68:71], v[112:115], a[252:255], v[68:71]\n"
"ds_read_b128 v[216:219], v230 offset:4608\n"
"v_mul_f32_dpp v27, v246, v27 row_newbcast:3 row_mask:0xf bank_mask:0xf\n"
"v_mfma_f32_16x16x32_bf16 v[36:39], v[108:111], v[224:227], v[36:39]\n"
"s_add_u32 s40, s56, s54\n"
"v_cvt_pk_bf16_f32 v24, v24, v25\n"
"v_mul_f32_dpp v59, v247, v59 row_newbcast:3 row_mask:0xf bank_mask:0xf\n"
"v_mfma_f32_16x16x32_bf16 v[36:39], v[104:107], a[252:255], v[36:39]\n"
"buffer_load_dwordx4 v207, s[16:19], s40 offen offset:3072 nt lds\n"
"v_cvt_pk_bf16_f32 v74, v76, v77\n"
"v_cvt_pk_bf16_f32 v25, v26, v27\n"
"v_cvt_pk_bf16_f32 v130, v168, v169\n"
"v_exp_f32 v154, v154\n"
"v_exp_f32 v162, v162\n"
"v_mul_f32_dpp v3, v246, v3 row_newbcast:3 row_mask:0xf bank_mask:0xf\n"
"v_mfma_f32_16x16x32_bf16 v[4:7], v[100:103], v[224:227], v[4:7]\n"
"v_cvt_pk_bf16_f32 v10, v12, v13\n"
"v_cvt_pk_bf16_f32 v0, v0, v1\n"
"v_mfma_f32_16x16x32_bf16 v[4:7], v[96:99], a[252:255], v[4:7]\n"
"v_mul_f32_dpp v90, v206, v90 row_newbcast:2 row_mask:0xf bank_mask:0xf\n"
"v_mul_f32_dpp v92, v206, v92 row_newbcast:0 row_mask:0xf bank_mask:0xf\n"
"v_mfma_f32_16x16x32_bf16 a[212:215], v[120:123], a[252:255], a[212:215]\n"
"s_waitcnt lgkmcnt(3)\n"
"ds_read_b64_tr_b16 a[246:247], v232 offset:53760\n"
"ds_read_b64_tr_b16 v[224:225], v237 offset:49664\n"
"v_cvt_pk_bf16_f32 v11, v14, v15\n"
"v_cvt_pk_bf16_f32 v41, v42, v43\n"
"v_cvt_pk_bf16_f32 v43, v46, v47\n"
"v_cvt_pk_bf16_f32 v56, v56, v57\n"
"v_cvt_pk_bf16_f32 v131, v170, v171\n"
"v_cvt_pk_bf16_f32 v146, v176, v177\n"
"v_exp_f32 v167, v167\n"
"v_mfma_f32_16x16x32_bf16 v[168:171], v[208:211], a[0:3], v[180:183]\n"
"s_waitcnt lgkmcnt(3)\n"
"v_exp_f32 v159, v159\n"
"v_mfma_f32_16x16x32_bf16 v[168:171], v[212:215], a[4:7], v[168:171]\n"
"s_waitcnt lgkmcnt(2)\n"
"ds_read_b64_tr_b16 a[248:249], v236 offset:49664\n"
"ds_read_b64_tr_b16 a[254:255], v233 offset:53760\n"
"v_mfma_f32_16x16x32_bf16 v[168:171], v[216:219], a[8:11], v[168:171]\n"
"v_cvt_pk_bf16_f32 v89, v90, v91\n"
"v_cvt_pk_bf16_f32 v139, v174, v175\n"
"v_mfma_f32_16x16x32_bf16 v[168:171], v[220:223], a[12:15], v[168:171]\n"
"ds_write_b128 v248, v[8:11] offset:49152\n"
"v_mul_f32_dpp v79, v206, v79 row_newbcast:3 row_mask:0xf bank_mask:0xf\n"
"v_cvt_pk_bf16_f32 v147, v178, v179\n"
"v_exp_f32 v165, v165\n"
"v_mul_f32_dpp v6, v246, v6 row_newbcast:2 row_mask:0xf bank_mask:0xf\n"
"v_cvt_pk_bf16_f32 v1, v2, v3\n"
"v_mfma_f32_16x16x32_bf16 v[172:175], v[216:219], a[24:27], v[184:187]\n"
"ds_read_b64_tr_b16 v[226:227], v237 offset:53760\n"
"v_mul_f32_dpp v32, v247, v32 row_newbcast:0 row_mask:0xf bank_mask:0xf\n"
"v_mfma_f32_16x16x32_bf16 v[172:175], v[220:223], a[28:31], v[172:175]\n"
"v_mul_f32_dpp v39, v247, v39 row_newbcast:3 row_mask:0xf bank_mask:0xf\n"
"v_mul_f32_dpp v68, v206, v68 row_newbcast:0 row_mask:0xf bank_mask:0xf\n"
"v_mfma_f32_16x16x32_bf16 v[172:175], v[208:211], a[16:19], v[172:175]\n"
"ds_read_b64_tr_b16 a[252:253], v233 offset:49664\n"
"v_cvt_pk_bf16_f32 v75, v78, v79\n"
"v_mfma_f32_16x16x32_bf16 v[172:175], v[212:215], a[20:23], v[172:175]\n"
"s_cbranch_vccnz LPCT0_%=\nLPKT0_%=:\n"
"ds_read_b64_tr_b16 a[54:55], v249 offset:49160\n"
"v_mfma_f32_16x16x32_bf16 v[176:179], v[208:211], a[32:35], v[188:191]\n"
"v_cvt_pk_bf16_f32 v42, v44, v45\n"
"v_mul_f32_dpp v61, v247, v61 row_newbcast:1 row_mask:0xf bank_mask:0xf\n"
"v_mfma_f32_16x16x32_bf16 v[176:179], v[212:215], a[36:39], v[176:179]\n"
"ds_read_b64_tr_b16 a[52:53], v249 offset:49152\n"
"v_cvt_pk_bf16_f32 v57, v58, v59\n"
"v_mfma_f32_16x16x32_bf16 v[176:179], v[216:219], a[40:43], v[176:179]\n"
"v_exp_f32 v155, v155\n"
"v_mfma_f32_16x16x32_bf16 v[176:179], v[220:223], a[44:47], v[176:179]\n"
"s_waitcnt lgkmcnt(4)\n"
"ds_write_b128 v248, v[40:43] offset:50176\n"
"v_cvt_pk_bf16_f32 v27, v30, v31\n"
"v_cvt_pk_bf16_f32 v90, v92, v93\n"
"v_cvt_pk_bf16_f32 v126, v152, v153\n"
"v_mul_f32_dpp v7, v246, v7 row_newbcast:3 row_mask:0xf bank_mask:0xf\n"
"v_mul_f32_dpp v65, v206, v65 row_newbcast:1 row_mask:0xf bank_mask:0xf\n"
"v_mul_f32_dpp v67, v206, v67 row_newbcast:3 row_mask:0xf bank_mask:0xf\n"
"v_mul_f32_dpp v70, v206, v70 row_newbcast:2 row_mask:0xf bank_mask:0xf\n"
"v_mul_f32_dpp v71, v206, v71 row_newbcast:3 row_mask:0xf bank_mask:0xf\n"
"v_mfma_f32_16x16x32_bf16 a[224:227], v[120:123], a[244:247], a[224:227]\n"
"ds_write_b128 v248, v[72:75] offset:51200\n"
"v_cvt_pk_bf16_f32 v26, v28, v29\n"
"v_exp_f32 v166, v166\n"
"v_cvt_pk_bf16_f32 v3, v6, v7\n"
"v_mul_f32_dpp v66, v206, v66 row_newbcast:2 row_mask:0xf bank_mask:0xf\n"
"v_mfma_f32_16x16x32_bf16 v[16:19], v[96:99], a[244:247], v[16:19]\n"
"s_waitcnt vmcnt(8) lgkmcnt(4)\n"
"ds_read_b64_tr_b16 a[68:69], v249 offset:50176\n"
"v_mul_f32_dpp v69, v206, v69 row_newbcast:1 row_mask:0xf bank_mask:0xf\n"
"v_mfma_f32_16x16x32_bf16 v[16:19], v[100:103], a[248:251], v[16:19]\n"
"s_barrier\n"
"ds_read_b64_tr_b16 a[70:71], v249 offset:50184\n"
"ds_read_b128 v[212:215], v229 offset:16384\n"
"ds_read_b64 v[250:251], v245 offset:52224\n"
"v_mul_f32_dpp v5, v246, v5 row_newbcast:1 row_mask:0xf bank_mask:0xf\n"
"v_mul_f32_dpp v35, v247, v35 row_newbcast:3 row_mask:0xf bank_mask:0xf\n"
"v_exp_f32 v173, v173\n"
"v_exp_f32 v175, v175\n"
"v_mfma_f32_16x16x32_bf16 v[48:51], v[104:107], a[244:247], v[48:51]\n"
"v_cvt_pk_bf16_f32 v59, v62, v63\n"
"v_mul_f32_dpp v38, v247, v38 row_newbcast:2 row_mask:0xf bank_mask:0xf\n"
"v_mfma_f32_16x16x32_bf16 v[48:51], v[108:111], a[248:251], v[48:51]\n"
"ds_read_b128 v[208:211], v228 offset:16384\n"
"ds_read_b128 v[220:223], v229 offset:16896\n"
"v_mul_f32_dpp v95, v206, v95 row_newbcast:3 row_mask:0xf bank_mask:0xf\n"
"v_exp_f32 v161, v161\n"
"v_cvt_pk_bf16_f32 v149, v166, v167\n"
"v_cvt_pk_bf16_f32 v32, v32, v33\n"
"v_exp_f32 v170, v170\n"
"v_exp_f32 v172, v172\n"
"v_mfma_f32_16x16x32_bf16 v[80:83], v[112:115], a[244:247], v[80:83]\n"
"ds_read_b64_tr_b16 a[84:85], v249 offset:51200\n"
"ds_read_b64_tr_b16 a[86:87], v249 offset:51208\n"
"v_mfma_f32_16x16x32_bf16 v[80:83], v[116:119], a[248:251], v[80:83]\n"
"ds_read_b128 v[28:31], v244 offset:33792\n"
"v_mov_b64 v[198:199], 0\n"
"v_mfma_f32_16x16x32_bf16 v[84:87], v[116:119], v[224:227], v[84:87]\n"
"ds_write_b128 v248, v[24:27] offset:49152\n"
"v_cvt_pk_bf16_f32 v91, v94, v95\n"
"v_exp_f32 v156, v156\n"
"v_exp_f32 v157, v157\n"
"v_mul_f32_dpp v37, v247, v37 row_newbcast:1 row_mask:0xf bank_mask:0xf\n"
"v_mfma_f32_16x16x32_bf16 v[84:87], v[112:115], a[252:255], v[84:87]\n"
"ds_read_b64_tr_b16 a[60:61], v249 offset:49152\n"
"ds_read_b128 v[216:219], v228 offset:16896\n"
"v_cvt_pk_bf16_f32 v127, v154, v155\n"
"v_exp_f32 v160, v160\n"
"v_exp_f32 v164, v164\n"
"v_mul_f32_dpp v36, v247, v36 row_newbcast:0 row_mask:0xf bank_mask:0xf\n"
"v_exp_f32 v176, v176\n"
"v_mfma_f32_16x16x32_bf16 v[52:55], v[108:111], v[224:227], v[52:55]\n"
"ds_write_b128 v248, v[88:91] offset:51200\n"
"v_cvt_pk_bf16_f32 v58, v60, v61\n"
"v_exp_f32 v163, v163\n"
"v_cvt_pk_bf16_f32 v33, v34, v35\n"
"v_exp_f32 v169, v169\n"
"v_mfma_f32_16x16x32_bf16 v[52:55], v[104:107], a[252:255], v[52:55]\n"
"s_waitcnt lgkmcnt(9)\n"
"ds_write_b128 v248, v[56:59] offset:50176\n"
"v_mul_f32_dpp v4, v246, v4 row_newbcast:0 row_mask:0xf bank_mask:0xf\n"
"v_exp_f32 v168, v168\n"
"v_min_f32 v254, v252, v250\n"
"v_sub_f32 v252, v254, v252\n"
"v_mfma_f32_16x16x32_bf16 v[20:23], v[100:103], v[224:227], v[20:23]\n"
"ds_read_b64_tr_b16 a[92:93], v249 offset:51200\n"
"ds_read_b64_tr_b16 a[244:245], v234 offset:0\n"
"v_mfma_f32_16x16x32_bf16 v[20:23], v[96:99], a[252:255], v[20:23]\n"
"ds_read_b64_tr_b16 a[62:63], v249 offset:49160\n"
"ds_read_b64_tr_b16 a[78:79], v249 offset:50184\n"
"ds_read_b64_tr_b16 a[94:95], v249 offset:51208\n"
"v_cvt_pk_bf16_f32 v2, v4, v5\n"
"v_mul_f32_dpp v64, v206, v64 row_newbcast:0 row_mask:0xf bank_mask:0xf\n"
"v_cvt_pk_bf16_f32 v64, v64, v65\n"
"v_cvt_pk_bf16_f32 v65, v66, v67\n"
"v_cvt_pk_bf16_f32 v66, v68, v69\n"
"v_cvt_pk_bf16_f32 v67, v70, v71\n"
"v_exp_f32 v177, v177\n"
"v_mfma_f32_16x16x32_bf16 a[228:231], v[120:123], a[252:255], a[228:231]\n"
"s_waitcnt lgkmcnt(11)\n"
"ds_write_b128 v248, v[64:67] offset:51200\n"
"v_cvt_pk_bf16_f32 v132, v156, v157\n"
"v_cvt_pk_bf16_f32 v34, v36, v37\n"
"v_cvt_pk_bf16_f32 v35, v38, v39\n"
"v_exp_f32 v171, v171\n"
"v_exp_f32 v179, v179\n"
"v_sub_f32 v250, v254, v250\n"
"v_mul_f32_dpp v83, v206, v83 row_newbcast:3 row_mask:0xf bank_mask:0xf\n"
"v_mfma_f32_16x16x32_bf16 v[152:155], v[208:211], a[0:3], v[180:183]\n"
"ds_read_b64_tr_b16 a[76:77], v249 offset:50176\n"
"ds_read_b64_tr_b16 a[80:81], v249 offset:51200\n"
"v_mfma_f32_16x16x32_bf16 v[152:155], v[212:215], a[4:7], v[152:155]\n"
"s_waitcnt lgkmcnt(10)\n"
"ds_write_b128 v248, v[0:3] offset:49152\n"
"v_exp_f32 v158, v158\n"
"v_cvt_pk_bf16_f32 v140, v160, v161\n"
"v_cvt_pk_bf16_f32 v142, v172, v173\n"
"v_mul_f32_dpp v18, v246, v18 row_newbcast:2 row_mask:0xf bank_mask:0xf\n"
"v_mfma_f32_16x16x32_bf16 v[152:155], v[216:219], a[8:11], v[152:155]\n"
"ds_write_b128 v248, v[32:35] offset:50176\n"
"v_cvt_pk_bf16_f32 v141, v162, v163\n"
"v_exp_f32 v178, v178\n"
"v_exp_f32 v250, v250\n"
"v_mfma_f32_16x16x32_bf16 v[152:155], v[220:223], a[12:15], v[152:155]\n"
"ds_read_b64_tr_b16 a[50:51], v249 offset:49160\n"
"ds_read_b64_tr_b16 a[64:65], v249 offset:50176\n"
"ds_read_b128 v[24:27], v244 offset:32768\n"
"v_cvt_pk_bf16_f32 v133, v158, v159\n"
"v_cvt_pk_bf16_f32 v148, v164, v165\n"
"v_cvt_pk_bf16_f32 v150, v176, v177\n"
"v_exp_f32 v252, v252\n"
"v_mul_f32_dpp v19, v246, v19 row_newbcast:3 row_mask:0xf bank_mask:0xf\n"
"v_mul_f32_dpp v22, v246, v22 row_newbcast:2 row_mask:0xf bank_mask:0xf\n"
"v_mfma_f32_16x16x32_bf16 v[156:159], v[216:219], a[24:27], v[184:187]\n"
"ds_read_b64_tr_b16 a[66:67], v249 offset:50184\n"
"ds_read_b64_tr_b16 a[248:249], v238 offset:0\n"
"v_mfma_f32_16x16x32_bf16 v[156:159], v[220:223], a[28:31], v[156:159]\n"
"ds_read_b64_tr_b16 a[48:49], v249 offset:49152\n"
"ds_read_b128 v[60:63], v244 offset:35840\n"
"v_mfma_f32_16x16x32_bf16 v[156:159], v[208:211], a[16:19], v[156:159]\n"
"ds_read_b64_tr_b16 a[82:83], v249 offset:51208\n"
"ds_read_b128 v[56:59], v244 offset:34816\n"
"v_mfma_f32_16x16x32_bf16 v[156:159], v[212:215], a[20:23], v[156:159]\n"
"ds_read_b64_tr_b16 a[246:247], v234 offset:4096\n"
"ds_read_b64_tr_b16 v[226:227], v239 offset:4096\n"
"v_mfma_f32_16x16x32_bf16 v[160:163], v[208:211], a[32:35], v[188:191]\n"
"v_exp_f32 v174, v174\n"
"v_mfma_f32_16x16x32_bf16 v[160:163], v[212:215], a[36:39], v[160:163]\n"
"ds_read_b64_tr_b16 a[250:251], v238 offset:4096\n"
"v_mul_f32_dpp v49, v247, v49 row_newbcast:1 row_mask:0xf bank_mask:0xf\n"
"v_mfma_f32_16x16x32_bf16 v[160:163], v[216:219], a[40:43], v[160:163]\n"
"ds_read_b64_tr_b16 v[224:225], v239 offset:0\n"
"v_mov_b64 v[202:203], 0\n"
"v_mfma_f32_16x16x32_bf16 v[160:163], v[220:223], a[44:47], v[160:163]\n"
"v_exp_f32 v154, v154\n"
"v_mfma_f32_16x16x32_bf16 v[164:167], v[216:219], a[200:203], v[192:195]\n"
"v_cvt_pk_bf16_f32 v151, v178, v179\n"
"v_mul_f32 v253, v253, v252\n"
"v_mfma_f32_16x16x32_bf16 v[164:167], v[220:223], a[204:207], v[164:167]\n"
"v_cvt_pk_bf16_f32 v134, v168, v169\n"
"v_fmac_f32 v253, v251, v250\n"
"v_mfma_f32_16x16x32_bf16 v[164:167], v[208:211], a[192:195], v[164:167]\n"
"ds_read_b64_tr_b16 a[252:253], v235 offset:0\n"
"ds_read_b64_tr_b16 a[254:255], v235 offset:4096\n"
"v_mfma_f32_16x16x32_bf16 v[164:167], v[212:215], a[196:199], v[164:167]\n"
"s_waitcnt lgkmcnt(4)\n"
"v_cvt_pk_bf16_f32 v135, v170, v171\n"
"v_mfma_f32_16x16x32_bf16 a[216:219], v[124:127], a[244:247], 0\n"
"ds_read_b128 v[216:219], v228 offset:20992\n"
"v_cvt_pk_bf16_f32 v143, v174, v175\n"
"v_mfma_f32_16x16x32_bf16 v[8:11], v[128:131], a[244:247], 0\n"
"s_waitcnt lgkmcnt(4)\n"
"s_add_u32 m0, s44, 98304\n"
"v_rcp_f32 v253, v253\n"
"v_mfma_f32_16x16x32_bf16 v[8:11], v[132:135], a[248:251], v[8:11]\n"
"buffer_load_dwordx4 v240, s[20:23], s56 offen nt lds\n"
"v_exp_f32 v152, v152\n"
"v_exp_f32 v155, v155\n"
"v_exp_f32 v158, v158\n"
"v_exp_f32 v167, v167\n"
"v_mfma_f32_16x16x32_bf16 v[40:43], v[136:139], a[244:247], 0\n"
"v_accvgpr_read_b32 v254, a208\n"
"v_mul_f32_dpp v85, v206, v85 row_newbcast:1 row_mask:0xf bank_mask:0xf\n"
"v_mfma_f32_16x16x32_bf16 v[40:43], v[140:143], a[248:251], v[40:43]\n"
"v_mul_f32 v250, v250, v253\n"
"v_accvgpr_read_b32 v251, a211\n"
"v_mfma_f32_16x16x32_bf16 v[72:75], v[144:147], a[244:247], 0\n"
"ds_read_b128 v[220:223], v229 offset:20992\n"
"v_mul_f32_dpp v17, v246, v17 row_newbcast:1 row_mask:0xf bank_mask:0xf\n"
"v_mfma_f32_16x16x32_bf16 v[72:75], v[148:151], a[248:251], v[72:75]\n"
"s_waitcnt lgkmcnt(2)\n"
"v_exp_f32 v164, v164\n"
"v_mfma_f32_16x16x32_bf16 v[76:79], v[148:151], v[224:227], 0\n"
"v_exp_f32 v153, v153\n"
"v_mfma_f32_16x16x32_bf16 v[76:79], v[144:147], a[252:255], v[76:79]\n"
"v_mul_f32 v252, v252, v253\n"
"v_mov_b32_dpp v250, v250 row_ror:12 row_mask:0x2 bank_mask:0xf\n"
"v_mfma_f32_16x16x32_bf16 v[44:47], v[140:143], v[224:227], 0\n"
"ds_read_b64_tr_b16 a[244:245], v234 offset:512\n"
"v_mov_b32_dpp v252, v252 row_ror:12 row_mask:0x2 bank_mask:0xf\n"
"v_mfma_f32_16x16x32_bf16 v[44:47], v[136:139], a[252:255], v[44:47]\n"
"ds_read_b128 v[212:215], v229 offset:20480\n"
"v_mov_b32_dpp v250, v250 row_ror:8 row_mask:0x4 bank_mask:0xf\n"
"v_mfma_f32_16x16x32_bf16 v[12:15], v[132:135], v[224:227], 0\n"
"s_add_u32 s40, s56, s52\n"
"ds_read_b128 v[208:211], v228 offset:20480\n"
"v_cvt_pk_bf16_f32 v96, v152, v153\n"
"v_mfma_f32_16x16x32_bf16 v[12:15], v[128:131], a[252:255], v[12:15]\n"
"buffer_load_dwordx4 v207, s[20:23], s40 offen offset:1024 nt lds\n"
"v_mov_b32_dpp v252, v252 row_ror:8 row_mask:0x4 bank_mask:0xf\n"
"v_mov_b32_dpp v250, v250 row_ror:4 row_mask:0x8 bank_mask:0xf\n"
"v_accvgpr_read_b32 v255, a209\n"
"v_accvgpr_read_b32 v253, a210\n"
"v_mov_b64 v[196:197], 0\n"
"v_cvt_pk_bf16_f32 v97, v154, v155\n"
"v_exp_f32 v157, v157\n"
"v_mfma_f32_16x16x32_bf16 a[220:223], v[124:127], a[252:255], 0\n"
"v_mov_b32_dpp v252, v252 row_ror:4 row_mask:0x8 bank_mask:0xf\n"
"v_mul_f32_dpp v24, v250, v24 row_newbcast:0 row_mask:0xf bank_mask:0xf\n"
"v_mul_f32_dpp v25, v250, v25 row_newbcast:1 row_mask:0xf bank_mask:0xf\n"
"v_mul_f32_dpp v26, v250, v26 row_newbcast:2 row_mask:0xf bank_mask:0xf\n"
"v_mul_f32_dpp v29, v250, v29 row_newbcast:1 row_mask:0xf bank_mask:0xf\n"
"v_mul_f32_dpp v54, v247, v54 row_newbcast:2 row_mask:0xf bank_mask:0xf\n"
"v_exp_f32 v162, v162\n"
"v_exp_f32 v163, v163\n"
"v_smfmac_f32_16x16x64_bf16 v[196:199], a[240:243], v[128:135], v241\n"
"v_mul_f32_dpp v27, v250, v27 row_newbcast:3 row_mask:0xf bank_mask:0xf\n"
"v_fmac_f32_dpp v24, v252, v254 row_newbcast:0 row_mask:0xf bank_mask:0xf\n"
"v_mfma_f32_16x16x32_bf16 v[202:205], a[240:243], v[124:127], v[202:205]\n"
"s_waitcnt lgkmcnt(3)\n"
"ds_read_b64_tr_b16 a[252:253], v235 offset:512\n"
"ds_read_b64_tr_b16 a[254:255], v235 offset:4608\n"
"ds_read_b64_tr_b16 v[224:225], v239 offset:512\n"
"v_fmac_f32_dpp v25, v252, v255 row_newbcast:1 row_mask:0xf bank_mask:0xf\n"
"v_fmac_f32_dpp v26, v252, v253 row_newbcast:2 row_mask:0xf bank_mask:0xf\n"
"v_mul_f32_dpp v28, v250, v28 row_newbcast:0 row_mask:0xf bank_mask:0xf\n"
"v_mul_f32_dpp v30, v250, v30 row_newbcast:2 row_mask:0xf bank_mask:0xf\n"
"v_mul_f32_dpp v50, v247, v50 row_newbcast:2 row_mask:0xf bank_mask:0xf\n"
"v_mul_f32_dpp v58, v250, v58 row_newbcast:2 row_mask:0xf bank_mask:0xf\n"
"v_exp_f32 v156, v156\n"
"v_mfma_f32_16x16x32_bf16 v[168:171], v[216:219], a[8:11], v[180:183]\n"
"v_accvgpr_read_b32 v254, a212\n"
"v_accvgpr_read_b32 v253, a214\n"
"v_mfma_f32_16x16x32_bf16 v[168:171], v[220:223], a[12:15], v[168:171]\n"
"s_waitcnt lgkmcnt(3)\n"
"v_mul_f32_dpp v48, v247, v48 row_newbcast:0 row_mask:0xf bank_mask:0xf\n"
"v_mul_f32_dpp v57, v250, v57 row_newbcast:1 row_mask:0xf bank_mask:0xf\n"
"v_mfma_f32_16x16x32_bf16 v[168:171], v[208:211], a[0:3], v[168:171]\n"
"ds_read_b64_tr_b16 a[250:251], v238 offset:4608\n"
"v_mul_f32_dpp v56, v250, v56 row_newbcast:0 row_mask:0xf bank_mask:0xf\n"
"v_mfma_f32_16x16x32_bf16 v[168:171], v[212:215], a[4:7], v[168:171]\n"
"ds_read_b64_tr_b16 a[248:249], v238 offset:512\n"
"ds_read_b64_tr_b16 v[226:227], v239 offset:4608\n"
"v_fmac_f32_dpp v27, v252, v251 row_newbcast:3 row_mask:0xf bank_mask:0xf\n"
"v_mul_f32_dpp v31, v250, v31 row_newbcast:3 row_mask:0xf bank_mask:0xf\n"
"v_fmac_f32_dpp v28, v252, v254 row_newbcast:0 row_mask:0xf bank_mask:0xf\n"
"v_fmac_f32_dpp v30, v252, v253 row_newbcast:2 row_mask:0xf bank_mask:0xf\n"
"v_cvt_pk_bf16_f32 v24, v24, v25\n"
"v_cvt_pk_bf16_f32 v25, v26, v27\n"
"v_mul_f32_dpp v52, v247, v52 row_newbcast:0 row_mask:0xf bank_mask:0xf\n"
"v_accvgpr_read_b32 v254, a224\n"
"v_mul_f32_dpp v59, v250, v59 row_newbcast:3 row_mask:0xf bank_mask:0xf\n"
"v_mul_f32_dpp v60, v250, v60 row_newbcast:0 row_mask:0xf bank_mask:0xf\n"
"v_mul_f32_dpp v62, v250, v62 row_newbcast:2 row_mask:0xf bank_mask:0xf\n"
"v_mul_f32_dpp v63, v250, v63 row_newbcast:3 row_mask:0xf bank_mask:0xf\n"
"v_exp_f32 v159, v159\n"
"v_exp_f32 v161, v161\n"
"v_mfma_f32_16x16x32_bf16 v[172:175], v[208:211], a[16:19], v[184:187]\n"
"v_accvgpr_read_b32 v255, a213\n"
"v_accvgpr_read_b32 v251, a215\n"
"v_mfma_f32_16x16x32_bf16 v[172:175], v[212:215], a[20:23], v[172:175]\n"
"ds_read_b64_tr_b16 a[246:247], v234 offset:4608\n"
"v_fmac_f32_dpp v29, v252, v255 row_newbcast:1 row_mask:0xf bank_mask:0xf\n"
"v_mfma_f32_16x16x32_bf16 v[172:175], v[216:219], a[24:27], v[172:175]\n"
"s_add_u32 s40, s56, s53\n"
"v_xor_b32 v249, s75, v249\n"
"v_accvgpr_read_b32 v255, a225\n"
"v_mfma_f32_16x16x32_bf16 v[172:175], v[220:223], a[28:31], v[172:175]\n"
"buffer_load_dwordx4 v240, s[20:23], s40 offen offset:2048 nt lds\n"
"v_fmac_f32_dpp v31, v252, v251 row_newbcast:3 row_mask:0xf bank_mask:0xf\n"
"v_cvt_pk_bf16_f32 v27, v30, v31\n"
"v_mul_f32_dpp v16, v246, v16 row_newbcast:0 row_mask:0xf bank_mask:0xf\n"
"v_mul_f32_dpp v51, v247, v51 row_newbcast:3 row_mask:0xf bank_mask:0xf\n"
"v_accvgpr_read_b32 v253, a226\n"
"v_accvgpr_read_b32 v251, a227\n"
"v_fmac_f32_dpp v56, v252, v254 row_newbcast:0 row_mask:0xf bank_mask:0xf\n"
"v_accvgpr_read_b32 v254, a228\n"
"v_mfma_f32_16x16x32_bf16 v[176:179], v[216:219], a[40:43], v[188:191]\n"
"v_cvt_pk_bf16_f32 v26, v28, v29\n"
"v_fmac_f32_dpp v57, v252, v255 row_newbcast:1 row_mask:0xf bank_mask:0xf\n"
"v_mfma_f32_16x16x32_bf16 v[176:179], v[220:223], a[44:47], v[176:179]\n"
"v_fmac_f32_dpp v58, v252, v253 row_newbcast:2 row_mask:0xf bank_mask:0xf\n"
"v_accvgpr_read_b32 v255, a229\n"
"v_mfma_f32_16x16x32_bf16 v[176:179], v[208:211], a[32:35], v[176:179]\n"
"v_cvt_pk_bf16_f32 v16, v16, v17\n"
"v_mul_f32_dpp v61, v250, v61 row_newbcast:1 row_mask:0xf bank_mask:0xf\n"
"v_mfma_f32_16x16x32_bf16 v[176:179], v[212:215], a[36:39], v[176:179]\n"
"ds_write_b128 v243, v[24:27] offset:0\n"
"v_cvt_pk_bf16_f32 v48, v48, v49\n"
"v_fmac_f32_dpp v59, v252, v251 row_newbcast:3 row_mask:0xf bank_mask:0xf\n"
"v_accvgpr_read_b32 v253, a230\n"
"v_accvgpr_read_b32 v251, a231\n"
"v_fmac_f32_dpp v60, v252, v254 row_newbcast:0 row_mask:0xf bank_mask:0xf\n"
"v_fmac_f32_dpp v61, v252, v255 row_newbcast:1 row_mask:0xf bank_mask:0xf\n"
"v_cvt_pk_bf16_f32 v56, v56, v57\n"
"v_cvt_pk_bf16_f32 v57, v58, v59\n"
"v_cvt_pk_bf16_f32 v58, v60, v61\n"
"v_mfma_f32_16x16x32_bf16 v[152:155], v[208:211], a[192:195], v[192:195]\n"
"v_mul_f32_dpp v81, v206, v81 row_newbcast:1 row_mask:0xf bank_mask:0xf\n"
"v_mul_f32_dpp v82, v206, v82 row_newbcast:2 row_mask:0xf bank_mask:0xf\n"
"v_mul_f32_dpp v87, v206, v87 row_newbcast:3 row_mask:0xf bank_mask:0xf\n"
"v_fmac_f32_dpp v62, v252, v253 row_newbcast:2 row_mask:0xf bank_mask:0xf\n"
"v_fmac_f32_dpp v63, v252, v251 row_newbcast:3 row_mask:0xf bank_mask:0xf\n"
"v_cvt_pk_bf16_f32 v59, v62, v63\n"
"ds_write_b128 v243, v[56:59] offset:2048\n"
"v_mfma_f32_16x16x32_bf16 v[152:155], v[212:215], a[196:199], v[152:155]\n"
"v_exp_f32 v174, v174\n"
"v_mfma_f32_16x16x32_bf16 v[152:155], v[216:219], a[200:203], v[152:155]\n"
"v_exp_f32 v166, v166\n"
"v_mfma_f32_16x16x32_bf16 v[152:155], v[220:223], a[204:207], v[152:155]\n"
"s_waitcnt lgkmcnt(2)\n"
"ds_read_b128 v[212:215], v231 offset:16384\n"
"v_cvt_pk_bf16_f32 v105, v158, v159\n"
"v_mfma_f32_16x16x32_bf16 a[232:235], v[124:127], a[244:247], 0\n"
"v_mul_f32_dpp v20, v246, v20 row_newbcast:0 row_mask:0xf bank_mask:0xf\n"
"v_mul_f32_dpp v23, v246, v23 row_newbcast:3 row_mask:0xf bank_mask:0xf\n"
"v_mfma_f32_16x16x32_bf16 v[24:27], v[128:131], a[244:247], 0\n"
"s_add_u32 s40, s56, s54\n"
"ds_read_b128 v[220:223], v231 offset:16896\n"
"v_mul_f32_dpp v21, v246, v21 row_newbcast:1 row_mask:0xf bank_mask:0xf\n"
"v_mfma_f32_16x16x32_bf16 v[24:27], v[132:135], a[248:251], v[24:27]\n"
"buffer_load_dwordx4 v207, s[20:23], s40 offen offset:3072 nt lds\n"
"v_mfma_f32_16x16x32_bf16 v[56:59], v[136:139], a[244:247], 0\n"
"s_add_u32 s55, s55, s59\n"
"v_cvt_pk_bf16_f32 v17, v18, v19\n"
"v_cvt_pk_bf16_f32 v104, v156, v157\n"
"v_mfma_f32_16x16x32_bf16 v[56:59], v[140:143], a[248:251], v[56:59]\n"
"s_or_b32 s55, s55, s58\n"
"v_exp_f32 v178, v178\n"
"v_mfma_f32_16x16x32_bf16 v[88:91], v[144:147], a[244:247], 0\n"
"s_xor_b32 s56, s55, s57\n"
"ds_read_b128 v[208:211], v230 offset:16384\n"
"v_cvt_pk_bf16_f32 v49, v50, v51\n"
"v_mfma_f32_16x16x32_bf16 v[88:91], v[148:151], a[248:251], v[88:91]\n"
"ds_read_b128 v[216:219], v230 offset:16896\n"
"v_cvt_pk_bf16_f32 v18, v20, v21\n"
"v_mfma_f32_16x16x32_bf16 v[92:95], v[148:151], v[224:227], 0\n"
"v_exp_f32 v160, v160\n"
"v_mfma_f32_16x16x32_bf16 v[92:95], v[144:147], a[252:255], v[92:95]\n"
"v_exp_f32 v165, v165\n"
"v_mfma_f32_16x16x32_bf16 v[60:63], v[140:143], v[224:227], 0\n"
"s_add_u32 m0, s44, 49152\n"
"ds_read_b64_tr_b16 a[248:249], v236 offset:0\n"
"v_mul_f32_dpp v80, v206, v80 row_newbcast:0 row_mask:0xf bank_mask:0xf\n"
"v_mfma_f32_16x16x32_bf16 v[60:63], v[136:139], a[252:255], v[60:63]\n"
"buffer_load_dwordx4 v240, s[16:19], s56 offen nt lds\n"
"v_mfma_f32_16x16x32_bf16 v[28:31], v[132:135], v[224:227], 0\n"
"v_exp_f32 v177, v177\n"
"v_mfma_f32_16x16x32_bf16 v[28:31], v[128:131], a[252:255], v[28:31]\n"
"v_exp_f32 v169, v169\n"
"v_mfma_f32_16x16x32_bf16 a[236:239], v[124:127], a[252:255], 0\n"
"ds_read_b64_tr_b16 a[250:251], v236 offset:4096\n"
"ds_read_b64_tr_b16 v[224:225], v237 offset:0\n"
"v_smfmac_f32_16x16x64_bf16 v[198:201], a[240:243], v[136:143], v241\n"
"s_waitcnt lgkmcnt(3)\n"
"ds_read_b64_tr_b16 a[246:247], v232 offset:4096\n"
"v_cvt_pk_bf16_f32 v112, v160, v161\n"
"v_mfma_f32_16x16x32_bf16 v[156:159], v[208:211], a[0:3], v[180:183]\n"
"v_cvt_pk_bf16_f32 v80, v80, v81\n"
"v_cvt_pk_bf16_f32 v113, v162, v163\n"
"v_mfma_f32_16x16x32_bf16 v[156:159], v[212:215], a[4:7], v[156:159]\n"
"ds_read_b64_tr_b16 a[254:255], v233 offset:4096\n"
"v_xor_b32 v248, s75, v248\n"
"v_mfma_f32_16x16x32_bf16 v[156:159], v[216:219], a[8:11], v[156:159]\n"
"ds_read_b64_tr_b16 a[244:245], v232 offset:0\n"
"v_cvt_pk_bf16_f32 v120, v164, v165\n"
"v_mfma_f32_16x16x32_bf16 v[156:159], v[220:223], a[12:15], v[156:159]\n"
"v_exp_f32 v170, v170\n"
"v_mfma_f32_16x16x32_bf16 v[160:163], v[216:219], a[24:27], v[184:187]\n"
"v_exp_f32 v179, v179\n"
"v_mfma_f32_16x16x32_bf16 v[160:163], v[220:223], a[28:31], v[160:163]\n"
"ds_read_b64_tr_b16 v[226:227], v237 offset:4096\n"
"v_cvt_pk_bf16_f32 v121, v166, v167\n"
"v_mfma_f32_16x16x32_bf16 v[160:163], v[208:211], a[16:19], v[160:163]\n"
"s_add_u32 s40, s56, s52\n"
"v_exp_f32 v168, v168\n"
"v_mfma_f32_16x16x32_bf16 v[160:163], v[212:215], a[20:23], v[160:163]\n"
"buffer_load_dwordx4 v207, s[16:19], s40 offen offset:1024 nt lds\n"
"v_mfma_f32_16x16x32_bf16 v[164:167], v[208:211], a[32:35], v[188:191]\n"
"v_exp_f32 v176, v176\n"
"v_mfma_f32_16x16x32_bf16 v[164:167], v[212:215], a[36:39], v[164:167]\n"
"v_cvt_pk_bf16_f32 v19, v22, v23\n"
"v_mul_f32_dpp v53, v247, v53 row_newbcast:1 row_mask:0xf bank_mask:0xf\n"
"v_mfma_f32_16x16x32_bf16 v[164:167], v[216:219], a[40:43], v[164:167]\n"
"ds_read_b64_tr_b16 a[252:253], v233 offset:0\n"
"ds_read_b128 v[208:211], v230 offset:20480\n"
"v_mfma_f32_16x16x32_bf16 v[164:167], v[220:223], a[44:47], v[164:167]\n"
"s_waitcnt lgkmcnt(3)\n"
"v_exp_f32 v158, v158\n"
"v_mfma_f32_16x16x32_bf16 a[208:211], v[124:127], a[244:247], 0\n"
"ds_read_b128 v[220:223], v231 offset:20992\n"
"v_cvt_pk_bf16_f32 v81, v82, v83\n"
"v_mfma_f32_16x16x32_bf16 v[0:3], v[128:131], a[244:247], 0\n"
"ds_read_b128 v[212:215], v231 offset:20480\n"
"v_mul_f32_dpp v55, v247, v55 row_newbcast:3 row_mask:0xf bank_mask:0xf\n"
"v_mfma_f32_16x16x32_bf16 v[0:3], v[132:135], a[248:251], v[0:3]\n"
"v_exp_f32 v175, v175\n"
"v_mfma_f32_16x16x32_bf16 v[32:35], v[136:139], a[244:247], 0\n"
"s_add_u32 s40, s56, s53\n"
"ds_read_b128 v[216:219], v230 offset:20992\n"
"v_mul_f32_dpp v84, v206, v84 row_newbcast:0 row_mask:0xf bank_mask:0xf\n"
"v_mfma_f32_16x16x32_bf16 v[32:35], v[140:143], a[248:251], v[32:35]\n"
"buffer_load_dwordx4 v240, s[16:19], s40 offen offset:2048 nt lds\n"
"v_mfma_f32_16x16x32_bf16 v[64:67], v[144:147], a[244:247], 0\n"
"v_cvt_pk_bf16_f32 v50, v52, v53\n"
"v_mul_f32_dpp v86, v206, v86 row_newbcast:2 row_mask:0xf bank_mask:0xf\n"
"v_mfma_f32_16x16x32_bf16 v[64:67], v[148:151], a[248:251], v[64:67]\n"
"s_waitcnt lgkmcnt(3)\n"
"ds_write_b128 v248, v[16:19] offset:49152\n"
"v_cvt_pk_bf16_f32 v82, v84, v85\n"
"v_exp_f32 v172, v172\n"
"v_exp_f32 v152, v152\n"
"v_exp_f32 v159, v159\n"
"v_exp_f32 v161, v161\n"
"v_exp_f32 v164, v164\n"
"v_exp_f32 v167, v167\n"
"v_mfma_f32_16x16x32_bf16 v[68:71], v[148:151], v[224:227], 0\n"
"v_cvt_pk_bf16_f32 v51, v54, v55\n"
"v_cvt_pk_bf16_f32 v98, v168, v169\n"
"v_mfma_f32_16x16x32_bf16 v[68:71], v[144:147], a[252:255], v[68:71]\n"
"ds_write_b128 v248, v[48:51] offset:50176\n"
"v_exp_f32 v171, v171\n"
"v_exp_f32 v173, v173\n"
"v_exp_f32 v156, v156\n"
"v_exp_f32 v163, v163\n"
"v_mfma_f32_16x16x32_bf16 v[36:39], v[140:143], v[224:227], 0\n"
"ds_read_b64_tr_b16 a[246:247], v232 offset:4608\n"
"ds_read_b64_tr_b16 a[250:251], v236 offset:4608\n"
"v_mfma_f32_16x16x32_bf16 v[36:39], v[136:139], a[252:255], v[36:39]\n"
"ds_read_b64_tr_b16 a[74:75], v249 offset:50184\n"
"v_cvt_pk_bf16_f32 v107, v174, v175\n"
"v_mfma_f32_16x16x32_bf16 v[4:7], v[132:135], v[224:227], 0\n"
"ds_read_b64_tr_b16 a[56:57], v249 offset:49152\n"
"v_cvt_pk_bf16_f32 v83, v86, v87\n"
"v_mfma_f32_16x16x32_bf16 v[4:7], v[128:131], a[252:255], v[4:7]\n"
"ds_write_b128 v248, v[80:83] offset:51200\n"
"v_cvt_pk_bf16_f32 v99, v170, v171\n"
"v_exp_f32 v155, v155\n"
"v_exp_f32 v157, v157\n"
"v_mfma_f32_16x16x32_bf16 a[212:215], v[124:127], a[252:255], 0\n"
"s_add_u32 s40, s56, s54\n"
"ds_read_b64_tr_b16 a[58:59], v249 offset:49160\n"
"ds_read_b64_tr_b16 v[224:225], v237 offset:512\n"
"v_smfmac_f32_16x16x64_bf16 v[200:203], a[240:243], v[144:151], v241\n"
"buffer_load_dwordx4 v207, s[16:19], s40 offen offset:3072 nt lds\n"
"v_mfma_f32_16x16x32_bf16 v[168:171], v[208:211], a[0:3], v[180:183]\n"
"s_waitcnt lgkmcnt(8)\n"
"v_exp_f32 v153, v153\n"
"v_mfma_f32_16x16x32_bf16 v[168:171], v[212:215], a[4:7], v[168:171]\n"
"ds_read_b64_tr_b16 a[72:73], v249 offset:50176\n"
"ds_read_b64_tr_b16 a[90:91], v249 offset:51208\n"
"v_mfma_f32_16x16x32_bf16 v[168:171], v[216:219], a[8:11], v[168:171]\n"
"ds_read_b64_tr_b16 a[88:89], v249 offset:51200\n"
"v_cvt_pk_bf16_f32 v115, v178, v179\n"
"v_mfma_f32_16x16x32_bf16 v[168:171], v[220:223], a[12:15], v[168:171]\n"
"ds_read_b64_tr_b16 a[244:245], v232 offset:512\n"
"v_cvt_pk_bf16_f32 v106, v172, v173\n"
"v_mfma_f32_16x16x32_bf16 v[172:175], v[216:219], a[24:27], v[184:187]\n"
"ds_read_b64_tr_b16 a[248:249], v236 offset:512\n"
"v_cvt_pk_bf16_f32 v114, v176, v177\n"
"v_mfma_f32_16x16x32_bf16 v[172:175], v[220:223], a[28:31], v[172:175]\n"
"v_exp_f32 v162, v162\n"
"v_mfma_f32_16x16x32_bf16 v[172:175], v[208:211], a[16:19], v[172:175]\n"
"ds_read_b64_tr_b16 v[226:227], v237 offset:4608\n"
"v_cvt_pk_bf16_f32 v122, v152, v153\n"
"v_mfma_f32_16x16x32_bf16 v[172:175], v[212:215], a[20:23], v[172:175]\n"
"s_cbranch_vccnz LPCT1_%=\nLPKT1_%=:\n"
"ds_read_b64_tr_b16 a[252:253], v233 offset:512\n"
"v_mfma_f32_16x16x32_bf16 v[176:179], v[208:211], a[32:35], v[188:191]\n"
"v_exp_f32 v166, v166\n"
"v_mfma_f32_16x16x32_bf16 v[176:179], v[212:215], a[36:39], v[176:179]\n"
"ds_read_b64_tr_b16 a[254:255], v233 offset:4608\n"
"v_cvt_pk_bf16_f32 v109, v162, v163\n"
"v_mfma_f32_16x16x32_bf16 v[176:179], v[216:219], a[40:43], v[176:179]\n"
"v_exp_f32 v169, v169\n"
"v_mfma_f32_16x16x32_bf16 v[176:179], v[220:223], a[44:47], v[176:179]\n"
"s_waitcnt lgkmcnt(4)\n"
"v_cvt_pk_bf16_f32 v101, v158, v159\n"
"v_mfma_f32_16x16x32_bf16 a[224:227], v[124:127], a[244:247], 0\n"
"v_exp_f32 v154, v154\n"
"v_mfma_f32_16x16x32_bf16 v[16:19], v[128:131], a[244:247], 0\n"
"s_waitcnt vmcnt(8) lgkmcnt(3)\n"
"v_exp_f32 v168, v168\n"
"v_mfma_f32_16x16x32_bf16 v[16:19], v[132:135], a[248:251], v[16:19]\n"
"s_barrier\n"
"ds_read_b128 v[212:215], v229 offset:32768\n"
"ds_read_b128 v[216:219], v228 offset:33280\n"
"v_exp_f32 v165, v165\n"
"v_mfma_f32_16x16x32_bf16 v[48:51], v[136:139], a[244:247], 0\n"
"ds_read_b128 v[220:223], v229 offset:33280\n"
"v_xor_b32 v249, s75, v249\n"
"v_mfma_f32_16x16x32_bf16 v[48:51], v[140:143], a[248:251], v[48:51]\n"
"ds_read_b128 v[208:211], v228 offset:32768\n"
"v_add_u32 v243, 0x1000, v243\n"
"v_mfma_f32_16x16x32_bf16 v[80:83], v[144:147], a[244:247], 0\n"
"ds_read_b64_tr_b16 a[246:247], v234 offset:20480\n"
"v_cvt_pk_bf16_f32 v117, v166, v167\n"
"v_mfma_f32_16x16x32_bf16 v[80:83], v[148:151], a[248:251], v[80:83]\n"
"s_waitcnt lgkmcnt(5)\n"
"ds_read_b64_tr_b16 a[250:251], v238 offset:20480\n"
"v_xor_b32 v248, s75, v248\n"
"v_mfma_f32_16x16x32_bf16 v[84:87], v[148:151], v[224:227], 0\n"
"v_cvt_pk_bf16_f32 v100, v156, v157\n"
"v_cvt_pk_bf16_f32 v116, v164, v165\n"
"v_mfma_f32_16x16x32_bf16 v[84:87], v[144:147], a[252:255], v[84:87]\n"
"v_exp_f32 v160, v160\n"
"v_mfma_f32_16x16x32_bf16 v[52:55], v[140:143], v[224:227], 0\n"
"v_exp_f32 v176, v176\n"
"v_mfma_f32_16x16x32_bf16 v[52:55], v[136:139], a[252:255], v[52:55]\n"
"v_exp_f32 v177, v177\n"
"v_mfma_f32_16x16x32_bf16 v[20:23], v[132:135], v[224:227], 0\n"
"v_cvt_pk_bf16_f32 v123, v154, v155\n"
"v_cvt_pk_bf16_f32 v118, v176, v177\n"
"v_mfma_f32_16x16x32_bf16 v[20:23], v[128:131], a[252:255], v[20:23]\n"
"v_exp_f32 v174, v174\n"
"v_mfma_f32_16x16x32_bf16 a[228:231], v[124:127], a[252:255], 0\n"
"s_waitcnt lgkmcnt(1)\n"
"ds_read_b64_tr_b16 a[254:255], v235 offset:20480\n"
"s_branch LAY2_%=\n"
"LAX_%=:\n"
"v_mfma_f32_16x16x32_bf16 a[224:227], v[124:127], a[244:247], a[224:227]\n"
"v_exp_f32 v162, v162\n"
"v_mfma_f32_16x16x32_bf16 v[16:19], v[128:131], a[244:247], v[16:19]\n"
"s_waitcnt vmcnt(8)\n"
"v_exp_f32 v171, v171\n"
"v_mfma_f32_16x16x32_bf16 v[16:19], v[132:135], a[248:251], v[16:19]\n"
"s_barrier\n"
"v_exp_f32 v169, v169\n"
"v_exp_f32 v172, v172\n"
"v_exp_f32 v173, v173\n"
"v_exp_f32 v174, v174\n"
"v_lshlrev_b32_e64 v244, 24, s43\n"
"v_mfma_f32_16x16x32_bf16 v[48:51], v[136:139], a[244:247], v[48:51]\n"
"v_exp_f32 v170, v170\n"
"v_mfma_f32_16x16x32_bf16 v[48:51], v[140:143], a[248:251], v[48:51]\n"
"v_exp_f32 v179, v179\n"
"v_mfma_f32_16x16x32_bf16 v[80:83], v[144:147], a[244:247], v[80:83]\n"
"ds_read_b64_tr_b16 a[246:247], v234 offset:53248\n"
"v_cvt_pk_bf16_f32 v117, v166, v167\n"
"v_mfma_f32_16x16x32_bf16 v[80:83], v[148:151], a[248:251], v[80:83]\n"
"ds_read_b64_tr_b16 a[244:245], v234 offset:49152\n"
"v_cvt_pk_bf16_f32 v116, v164, v165\n"
"v_mfma_f32_16x16x32_bf16 v[84:87], v[148:151], v[224:227], v[84:87]\n"
"v_cvt_pk_bf16_f32 v100, v156, v157\n"
"v_accvgpr_read_b32 v253, a96\n"
"v_mfma_f32_16x16x32_bf16 v[84:87], v[144:147], a[252:255], v[84:87]\n"
"ds_read_b64_tr_b16 a[248:249], v238 offset:49152\n"
"ds_read_b64_tr_b16 a[250:251], v238 offset:53248\n"
"v_exp_f32 v168, v168\n"
"v_exp_f32 v175, v175\n"
"v_exp_f32 v176, v176\n"
"v_exp_f32 v177, v177\n"
"v_exp_f32 v178, v178\n"
"v_mfma_f32_16x16x32_bf16 v[52:55], v[140:143], v[224:227], v[52:55]\n"
"v_cvt_pk_bf16_f32 v102, v168, v169\n"
"v_cvt_pk_bf16_f32 v110, v172, v173\n"
"v_mfma_f32_16x16x32_bf16 v[52:55], v[136:139], a[252:255], v[52:55]\n"
"v_cvt_pk_bf16_f32 v103, v170, v171\n"
"v_and_b32 v253, 0x7fff0000, v253\n"
"v_mfma_f32_16x16x32_bf16 v[20:23], v[132:135], v[224:227], v[20:23]\n"
"ds_read_b64_tr_b16 v[224:225], v239 offset:49152\n"
"v_cvt_pk_bf16_f32 v101, v158, v159\n"
"v_mfma_f32_16x16x32_bf16 v[20:23], v[128:131], a[252:255], v[20:23]\n"
"v_cvt_pk_bf16_f32 v123, v154, v155\n"
"v_cvt_pk_bf16_f32 v111, v174, v175\n"
"v_mfma_f32_16x16x32_bf16 a[228:231], v[124:127], a[252:255], a[228:231]\n"
"s_waitcnt lgkmcnt(3)\n"
"ds_read_b64_tr_b16 a[254:255], v235 offset:53248\n"
"v_mul_f32 v244, 0x45800000, v244\n"
"v_mfma_f32_16x16x32_bf16 a[216:219], v[120:123], a[244:247], a[216:219]\n"
"s_mov_b64 s[28:29], s[8:9]\n"
"ds_read_b64_tr_b16 v[226:227], v239 offset:53248\n"
"v_cvt_pk_bf16_f32 v118, v176, v177\n"
"v_smfmac_f32_16x16x64_bf16 v[196:199], a[240:243], v[96:103], v241\n"
"s_mov_b32 s30, 655360\n"
"ds_read_b64_tr_b16 a[252:253], v235 offset:49152\n"
"v_cvt_pk_bf16_f32 v108, v160, v161\n"
"v_mfma_f32_16x16x32_bf16 v[8:11], v[96:99], a[244:247], v[8:11]\n"
"s_waitcnt lgkmcnt(4)\n"
"s_mov_b32 s71, s44\n"
"v_cvt_pk_bf16_f32 v119, v178, v179\n"
"v_max_u32_dpp v253, v253, v253 quad_perm:[1,0,3,2] row_mask:0xf bank_mask:0xf\n"
"v_mfma_f32_16x16x32_bf16 v[8:11], v[100:103], a[248:251], v[8:11]\n"
"s_mov_b32 s70, s95\n"
"v_and_b32 v252, 0x7fff0000, v252\n"
"v_and_b32 v245, 0x3f0, v242\n"
"v_smfmac_f32_16x16x64_bf16 v[200:203], a[240:243], v[112:119], v241\n"
"v_cvt_pk_bf16_f32 v109, v162, v163\n"
"v_max_u32_dpp v253, v253, v253 quad_perm:[2,3,0,1] row_mask:0xf bank_mask:0xf\n"
"v_mfma_f32_16x16x32_bf16 v[40:43], v[104:107], a[244:247], v[40:43]\n"
"v_add_f32 v196, v196, v197\n"
"v_lshrrev_b32 v245, 1, v245\n"
"v_mfma_f32_16x16x32_bf16 v[40:43], v[108:111], a[248:251], v[40:43]\n"
"s_add_u32 s41, s70, 327680\n"
"s_add_u32 m0, s71, 32768\n"
"v_max_u32_dpp v252, v252, v252 quad_perm:[1,0,3,2] row_mask:0xf bank_mask:0xf\n"
"v_smfmac_f32_16x16x64_bf16 v[198:201], a[240:243], v[104:111], v241\n"
"buffer_load_dwordx4 v240, s[28:31], s41 offen lds\n"
"v_mfma_f32_16x16x32_bf16 v[72:75], v[112:115], a[244:247], v[72:75]\n"
"v_max_u32_dpp v252, v252, v252 quad_perm:[2,3,0,1] row_mask:0xf bank_mask:0xf\n"
"v_max_u32_dpp v253, v253, v253 row_half_mirror row_mask:0xf bank_mask:0xf\n"
"v_mfma_f32_16x16x32_bf16 v[72:75], v[116:119], a[248:251], v[72:75]\n"
"ds_read_b64_tr_b16 a[244:245], v234 offset:49664\n"
"v_sub_u32 v245, v242, v245\n"
"v_mfma_f32_16x16x32_bf16 v[202:205], a[240:243], v[120:123], v[202:205]\n"
"s_waitcnt lgkmcnt(1)\n"
"ds_read_b64_tr_b16 a[246:247], v234 offset:53760\n"
"v_add_f32 v198, v198, v199\n"
"v_mfma_f32_16x16x32_bf16 v[76:79], v[116:119], v[224:227], v[76:79]\n"
"s_add_u32 s40, s41, s52\n"
"ds_read_b64_tr_b16 a[248:249], v238 offset:49664\n"
"ds_read_b64_tr_b16 a[250:251], v238 offset:53760\n"
"v_mfma_f32_16x16x32_bf16 v[76:79], v[112:115], a[252:255], v[76:79]\n"
"buffer_load_dwordx4 v240, s[28:31], s40 offen offset:1024 lds\n"
"v_mfma_f32_16x16x32_bf16 v[44:47], v[108:111], v[224:227], v[44:47]\n"
"v_add_f32 v200, v200, v201\n"
"v_add_f32 v244, v244, v202\n"
"v_mfma_f32_16x16x32_bf16 v[44:47], v[104:107], a[252:255], v[44:47]\n"
"v_add_f32 v254, v196, v198\n"
"v_min3_f32 v244, v244, v196, v198\n"
"v_min_f32 v244, v244, v200\n"
"v_rcp_f32 v244, v244\n"
"v_mfma_f32_16x16x32_bf16 v[12:15], v[100:103], v[224:227], v[12:15]\n"
"s_add_u32 s40, s41, s53\n"
"ds_read_b64_tr_b16 v[224:225], v239 offset:49664\n"
"ds_read_b64_tr_b16 v[226:227], v239 offset:53760\n"
"v_mfma_f32_16x16x32_bf16 v[12:15], v[96:99], a[252:255], v[12:15]\n"
"buffer_load_dwordx4 v240, s[28:31], s40 offen offset:2048 lds\n"
"v_min_u32_dpp v253, v253, v253 row_mirror row_mask:0xf bank_mask:0xf\n"
"v_add_f32 v254, v254, v200\n"
"v_add_f32 v254, v254, v202\n"
"v_fmac_f32 v254, 0x68800000, v244\n"
"v_cmp_nge_f32 vcc, s45, v254\n"
"v_bfe_u32 v254, v242, 8, 2\n"
"v_mfma_f32_16x16x32_bf16 a[220:223], v[120:123], a[252:255], a[220:223]\n"
"s_waitcnt lgkmcnt(4)\n"
"v_xor_b32 v244, 0x11000, v242\n"
"s_cbranch_vccnz LSLOW_%=\nLBACK3_%=:\ns_mov_b64 vcc, s[64:65]\n"
"v_mfma_f32_16x16x32_bf16 a[232:235], v[120:123], a[244:247], a[232:235]\n"
"ds_read_b64_tr_b16 a[252:253], v235 offset:49664\n"
"ds_read_b64_tr_b16 a[254:255], v235 offset:53760\n"
"v_min_u32_dpp v253, v253, v253 row_bcast:15 row_mask:0xa bank_mask:0xf\n"
"v_rcp_f32 v246, v196\n"
"v_rcp_f32 v247, v198\n"
"v_rcp_f32 v206, v200\n"
"v_xor_b32 v250, 0x1000, v245\n"
"v_mfma_f32_16x16x32_bf16 v[24:27], v[96:99], a[244:247], v[24:27]\n"
"s_waitcnt lgkmcnt(4)\n"
"v_mov_b32_dpp v206, v206 row_ror:12 row_mask:0x2 bank_mask:0xf\n"
"v_lshl_add_u32 v254, v254, 3, s43\n"
"v_mfma_f32_16x16x32_bf16 v[24:27], v[100:103], a[248:251], v[24:27]\n"
"ds_write_b128 v244, a[220:223] offset:33792\n"
"v_max_u32_dpp v252, v252, v252 row_half_mirror row_mask:0xf bank_mask:0xf\n"
"v_mov_b32_dpp v246, v246 row_ror:12 row_mask:0x2 bank_mask:0xf\n"
"v_mov_b32_dpp v206, v206 row_ror:8 row_mask:0x4 bank_mask:0xf\n"
"v_mfma_f32_16x16x32_bf16 v[56:59], v[104:107], a[244:247], v[56:59]\n"
"s_add_u32 s40, s41, s54\n"
"v_min_u32_dpp v252, v252, v252 row_mirror row_mask:0xf bank_mask:0xf\n"
"v_mov_b32_dpp v247, v247 row_ror:12 row_mask:0x2 bank_mask:0xf\n"
"v_mfma_f32_16x16x32_bf16 v[56:59], v[108:111], a[248:251], v[56:59]\n"
"buffer_load_dwordx4 v240, s[28:31], s40 offen offset:3072 lds\n"
"v_mfma_f32_16x16x32_bf16 v[88:91], v[112:115], a[244:247], v[88:91]\n"
"s_add_u32 s71, s44, s75\n"
"ds_read_b64_tr_b16 a[244:245], v232 offset:49152\n"
"ds_read_b64_tr_b16 a[246:247], v232 offset:53248\n"
"v_mfma_f32_16x16x32_bf16 v[88:91], v[116:119], a[248:251], v[88:91]\n"
"s_waitcnt lgkmcnt(5)\n"
"s_mov_b32 s70, s100\n"
"ds_write_b128 v244, a[232:235] offset:34816\n"
"v_mov_b32_dpp v246, v246 row_ror:8 row_mask:0x4 bank_mask:0xf\n"
"v_mov_b32_dpp v247, v247 row_ror:8 row_mask:0x4 bank_mask:0xf\n"
"v_mov_b32_dpp v206, v206 row_ror:4 row_mask:0x8 bank_mask:0xf\n"
"v_lshl_add_u32 v254, v254, 6, s75\n"
"v_sub_u32 v230, v234, v254\n"
"v_mfma_f32_16x16x32_bf16 v[92:95], v[116:119], v[224:227], v[92:95]\n"
"s_waitcnt lgkmcnt(4)\n"
"s_add_u32 s41, s70, 163840\n"
"s_add_u32 m0, s71, 16384\n"
"ds_read_b64_tr_b16 a[248:249], v236 offset:49152\n"
"v_mfma_f32_16x16x32_bf16 v[92:95], v[112:115], a[252:255], v[92:95]\n"
"buffer_load_dwordx4 v240, s[28:31], s41 offen lds\n"
"ds_read_b64_tr_b16 a[250:251], v236 offset:53248\n"
"v_min_u32_dpp v252, v252, v252 row_bcast:15 row_mask:0xa bank_mask:0xf\n"
"v_mov_b32_dpp v246, v246 row_ror:4 row_mask:0x8 bank_mask:0xf\n"
"v_mov_b32_dpp v247, v247 row_ror:4 row_mask:0x8 bank_mask:0xf\n"
"v_mul_f32_dpp v72, v206, v72 row_newbcast:0 row_mask:0xf bank_mask:0xf\n"
"v_mul_f32_dpp v74, v206, v74 row_newbcast:2 row_mask:0xf bank_mask:0xf\n"
"v_mul_f32_dpp v76, v206, v76 row_newbcast:0 row_mask:0xf bank_mask:0xf\n"
"v_mul_f32_dpp v77, v206, v77 row_newbcast:1 row_mask:0xf bank_mask:0xf\n"
"v_mfma_f32_16x16x32_bf16 v[60:63], v[108:111], v[224:227], v[60:63]\n"
"v_min_u32_dpp v252, v252, v252 row_bcast:31 row_mask:0xc bank_mask:0xf\n"
"v_mul_f32_dpp v8, v246, v8 row_newbcast:0 row_mask:0xf bank_mask:0xf\n"
"v_mfma_f32_16x16x32_bf16 v[60:63], v[104:107], a[252:255], v[60:63]\n"
"v_mul_f32_dpp v41, v247, v41 row_newbcast:1 row_mask:0xf bank_mask:0xf\n"
"v_mul_f32_dpp v59, v247, v59 row_newbcast:3 row_mask:0xf bank_mask:0xf\n"
"v_mfma_f32_16x16x32_bf16 v[28:31], v[100:103], v[224:227], v[28:31]\n"
"s_add_u32 s40, s41, s52\n"
"ds_read_b64_tr_b16 v[224:225], v237 offset:49152\n"
"v_min_u32_dpp v253, v253, v253 row_bcast:31 row_mask:0xc bank_mask:0xf\n"
"v_mfma_f32_16x16x32_bf16 v[28:31], v[96:99], a[252:255], v[28:31]\n"
"buffer_load_dwordx4 v240, s[28:31], s40 offen offset:1024 lds\n"
"v_mfma_f32_16x16x32_bf16 a[236:239], v[120:123], a[252:255], a[236:239]\n"
"s_waitcnt lgkmcnt(4)\n"
"v_mul_f32_dpp v25, v246, v25 row_newbcast:1 row_mask:0xf bank_mask:0xf\n"
"v_mul_f32_dpp v94, v206, v94 row_newbcast:2 row_mask:0xf bank_mask:0xf\n"
"v_mfma_f32_16x16x32_bf16 a[208:211], v[120:123], a[244:247], a[208:211]\n"
"ds_read_b64_tr_b16 a[252:253], v233 offset:49152\n"
"ds_read_b64_tr_b16 a[254:255], v233 offset:53248\n"
"v_mul_f32 v252, 0x43340000, v252\n"
"v_mul_f32 v252, v252, v253\n"
"v_mul_f32_dpp v10, v246, v10 row_newbcast:2 row_mask:0xf bank_mask:0xf\n"
"v_mul_f32_dpp v14, v246, v14 row_newbcast:2 row_mask:0xf bank_mask:0xf\n"
"v_mul_f32_dpp v40, v247, v40 row_newbcast:0 row_mask:0xf bank_mask:0xf\n"
"v_mul_f32_dpp v46, v247, v46 row_newbcast:2 row_mask:0xf bank_mask:0xf\n"
"v_mul_f32_dpp v73, v206, v73 row_newbcast:1 row_mask:0xf bank_mask:0xf\n"
"v_mul_f32_dpp v90, v206, v90 row_newbcast:2 row_mask:0xf bank_mask:0xf\n"
"v_mfma_f32_16x16x32_bf16 v[0:3], v[96:99], a[244:247], v[0:3]\n"
"s_waitcnt lgkmcnt(3)\n"
"ds_read_b64_tr_b16 v[226:227], v237 offset:53248\n"
"v_mul_f32_dpp v57, v247, v57 row_newbcast:1 row_mask:0xf bank_mask:0xf\n"
"v_mfma_f32_16x16x32_bf16 v[0:3], v[100:103], a[248:251], v[0:3]\n"
"ds_write_b128 v244, a[216:219] offset:32768\n"
"v_min_f32 v252, 0x71800000, v252\n"
"v_mul_f32_dpp v12, v246, v12 row_newbcast:0 row_mask:0xf bank_mask:0xf\n"
"v_mul_f32_dpp v45, v247, v45 row_newbcast:1 row_mask:0xf bank_mask:0xf\n"
"v_mul_f32_dpp v58, v247, v58 row_newbcast:2 row_mask:0xf bank_mask:0xf\n"
"v_mul_f32_dpp v92, v206, v92 row_newbcast:0 row_mask:0xf bank_mask:0xf\n"
"v_mfma_f32_16x16x32_bf16 v[32:35], v[104:107], a[244:247], v[32:35]\n"
"s_add_u32 s40, s41, s53\n"
"v_readlane_b32 s22, v252, 63\n"
"v_mul_f32_dpp v78, v206, v78 row_newbcast:2 row_mask:0xf bank_mask:0xf\n"
"v_mfma_f32_16x16x32_bf16 v[32:35], v[108:111], a[248:251], v[32:35]\n"
"buffer_load_dwordx4 v240, s[28:31], s40 offen offset:2048 lds\n"
"v_mfma_f32_16x16x32_bf16 v[64:67], v[112:115], a[244:247], v[64:67]\n"
"v_mul_f32_dpp v75, v206, v75 row_newbcast:3 row_mask:0xf bank_mask:0xf\n"
"v_mul_f32_dpp v61, v247, v61 row_newbcast:1 row_mask:0xf bank_mask:0xf\n"
"v_mfma_f32_16x16x32_bf16 v[64:67], v[116:119], a[248:251], v[64:67]\n"
"s_waitcnt lgkmcnt(1)\n"
"ds_read_b64_tr_b16 a[244:245], v232 offset:49664\n"
"ds_read_b64_tr_b16 a[246:247], v232 offset:53760\n"
"v_mov_b32 v253, v202\n"
"v_sub_u32 v228, v232, v254\n"
"v_mul_f32_dpp v9, v246, v9 row_newbcast:1 row_mask:0xf bank_mask:0xf\n"
"v_mul_f32_dpp v11, v246, v11 row_newbcast:3 row_mask:0xf bank_mask:0xf\n"
"v_mul_f32_dpp v15, v246, v15 row_newbcast:3 row_mask:0xf bank_mask:0xf\n"
"v_mul_f32_dpp v47, v247, v47 row_newbcast:3 row_mask:0xf bank_mask:0xf\n"
"v_cvt_pk_bf16_f32 v40, v40, v41\n"
"v_cvt_pk_bf16_f32 v72, v72, v73\n"
"v_mfma_f32_16x16x32_bf16 v[68:71], v[116:119], v[224:227], v[68:71]\n"
"s_add_u32 s40, s41, s54\n"
"ds_read_b64_tr_b16 a[248:249], v236 offset:49664\n"
"ds_read_b64_tr_b16 a[250:251], v236 offset:53760\n"
"v_mfma_f32_16x16x32_bf16 v[68:71], v[112:115], a[252:255], v[68:71]\n"
"buffer_load_dwordx4 v240, s[28:31], s40 offen offset:3072 lds\n"
"v_readlane_b32 s23, v252, 63\n"
"v_mul_f32_dpp v43, v247, v43 row_newbcast:3 row_mask:0xf bank_mask:0xf\n"
"v_mul_f32_dpp v24, v246, v24 row_newbcast:0 row_mask:0xf bank_mask:0xf\n"
"v_mul_f32_dpp v29, v246, v29 row_newbcast:1 row_mask:0xf bank_mask:0xf\n"
"v_mul_f32_dpp v56, v247, v56 row_newbcast:0 row_mask:0xf bank_mask:0xf\n"
"v_mul_f32_dpp v1, v246, v1 row_newbcast:1 row_mask:0xf bank_mask:0xf\n"
"v_accvgpr_read_b32 v255, a209\n"
"v_xor_b32 v232, 0x1000, v228\n"
"v_mfma_f32_16x16x32_bf16 v[36:39], v[108:111], v[224:227], v[36:39]\n"
"v_mov_b32 v252, v192\n"
"v_mul_f32_dpp v44, v247, v44 row_newbcast:0 row_mask:0xf bank_mask:0xf\n"
"v_mfma_f32_16x16x32_bf16 v[36:39], v[104:107], a[252:255], v[36:39]\n"
"ds_write_b128 v244, a[236:239] offset:35840\n"
"v_mul_f32_dpp v13, v246, v13 row_newbcast:1 row_mask:0xf bank_mask:0xf\n"
"v_mul_f32_dpp v42, v247, v42 row_newbcast:2 row_mask:0xf bank_mask:0xf\n"
"v_cvt_pk_bf16_f32 v41, v42, v43\n"
"v_cvt_pk_bf16_f32 v42, v44, v45\n"
"v_cvt_pk_bf16_f32 v56, v56, v57\n"
"v_mfma_f32_16x16x32_bf16 v[4:7], v[100:103], v[224:227], v[4:7]\n"
"ds_read_b64_tr_b16 v[224:225], v237 offset:49664\n"
"ds_read_b64_tr_b16 v[226:227], v237 offset:53760\n"
"v_mfma_f32_16x16x32_bf16 v[4:7], v[96:99], a[252:255], v[4:7]\n"
"ds_write_b64 v250, v[252:253] offset:52224\n"
"v_cvt_pk_bf16_f32 v8, v8, v9\n"
"v_cvt_pk_bf16_f32 v43, v46, v47\n"
"v_cvt_pk_bf16_f32 v73, v74, v75\n"
"v_mul_f32_dpp v28, v246, v28 row_newbcast:0 row_mask:0xf bank_mask:0xf\n"
"v_cvt_pk_bf16_f32 v57, v58, v59\n"
"v_mul_f32_dpp v0, v246, v0 row_newbcast:0 row_mask:0xf bank_mask:0xf\n"
"v_mul_f32_dpp v33, v247, v33 row_newbcast:1 row_mask:0xf bank_mask:0xf\n"
"v_mfma_f32_16x16x32_bf16 a[212:215], v[120:123], a[252:255], a[212:215]\n"
"s_waitcnt lgkmcnt(6)\n"
"ds_read_b64_tr_b16 a[252:253], v233 offset:49664\n"
"ds_read_b64_tr_b16 a[254:255], v233 offset:53760\n"
"v_cvt_pk_bf16_f32 v9, v10, v11\n"
"v_cvt_pk_bf16_f32 v74, v76, v77\n"
"v_mul_f32_dpp v60, v247, v60 row_newbcast:0 row_mask:0xf bank_mask:0xf\n"
"v_mul_f32_dpp v62, v247, v62 row_newbcast:2 row_mask:0xf bank_mask:0xf\n"
"v_mul_f32_dpp v88, v206, v88 row_newbcast:0 row_mask:0xf bank_mask:0xf\n"
"v_mul_f32_dpp v64, v206, v64 row_newbcast:0 row_mask:0xf bank_mask:0xf\n"
"v_mul_f32_dpp v66, v206, v66 row_newbcast:2 row_mask:0xf bank_mask:0xf\n"
"v_mfma_f32_16x16x32_bf16 a[224:227], v[120:123], a[244:247], a[224:227]\n"
"ds_write_b128 v248, v[40:43] offset:50176\n"
"v_sub_u32 v229, v233, v254\n"
"v_cvt_pk_bf16_f32 v11, v14, v15\n"
"v_mul_f32_dpp v27, v246, v27 row_newbcast:3 row_mask:0xf bank_mask:0xf\n"
"v_mul_f32_dpp v3, v246, v3 row_newbcast:3 row_mask:0xf bank_mask:0xf\n"
"v_mul_f32_dpp v68, v206, v68 row_newbcast:0 row_mask:0xf bank_mask:0xf\n"
"v_mfma_f32_16x16x32_bf16 v[16:19], v[96:99], a[244:247], v[16:19]\n"
"s_waitcnt vmcnt(8) lgkmcnt(1)\n"
"v_mul_f32_dpp v89, v206, v89 row_newbcast:1 row_mask:0xf bank_mask:0xf\n"
"v_add_u32 v236, s76, v249\n"
"v_mfma_f32_16x16x32_bf16 v[16:19], v[100:103], a[248:251], v[16:19]\n"
"s_barrier\n"
"ds_read_b64_tr_b16 a[22:23], v249 offset:50184\n"
"ds_read_b64_tr_b16 v[182:183], v228 offset:2048\n"
"ds_read_b64_tr_b16 v[216:217], v228 offset:24576\n"
"ds_read_b64_tr_b16 v[218:219], v228 offset:26624\n"
"ds_read_b64_tr_b16 a[202:203], v236 offset:2056\n"
"v_mul_f32_dpp v63, v247, v63 row_newbcast:3 row_mask:0xf bank_mask:0xf\n"
"v_mul_f32_dpp v91, v206, v91 row_newbcast:3 row_mask:0xf bank_mask:0xf\n"
"v_mul_f32_dpp v2, v246, v2 row_newbcast:2 row_mask:0xf bank_mask:0xf\n"
"v_mfma_f32_16x16x32_bf16 v[48:51], v[104:107], a[244:247], v[48:51]\n"
"ds_read_b64_tr_b16 v[192:193], v232 offset:8192\n"
"ds_read_b64_tr_b16 a[240:241], v236 offset:4096\n"
"v_mfma_f32_16x16x32_bf16 v[48:51], v[108:111], a[248:251], v[48:51]\n"
"ds_read_b64 v[250:251], v245 offset:52224\n"
"ds_read_b64_tr_b16 v[186:187], v232 offset:2048\n"
"ds_read_b64_tr_b16 v[194:195], v232 offset:10240\n"
"ds_read_b64_tr_b16 v[208:209], v228 offset:16384\n"
"v_cvt_pk_bf16_f32 v10, v12, v13\n"
"v_mul_f32_dpp v79, v206, v79 row_newbcast:3 row_mask:0xf bank_mask:0xf\n"
"v_cvt_pk_bf16_f32 v75, v78, v79\n"
"v_cvt_pk_bf16_f32 v88, v88, v89\n"
"v_cvt_pk_bf16_f32 v89, v90, v91\n"
"v_cvt_pk_bf16_f32 v0, v0, v1\n"
"v_mfma_f32_16x16x32_bf16 v[80:83], v[112:115], a[244:247], v[80:83]\n"
"ds_read_b64_tr_b16 v[180:181], v228 offset:0\n"
"ds_read_b64_tr_b16 v[188:189], v228 offset:8192\n"
"v_mfma_f32_16x16x32_bf16 v[80:83], v[116:119], a[248:251], v[80:83]\n"
"ds_write_b128 v248, v[72:75] offset:51200\n"
"v_mul_f32_dpp v30, v246, v30 row_newbcast:2 row_mask:0xf bank_mask:0xf\n"
"v_cvt_pk_bf16_f32 v24, v24, v25\n"
"v_cvt_pk_bf16_f32 v58, v60, v61\n"
"v_mul_f32_dpp v93, v206, v93 row_newbcast:1 row_mask:0xf bank_mask:0xf\n"
"v_mul_f32_dpp v36, v247, v36 row_newbcast:0 row_mask:0xf bank_mask:0xf\n"
"v_mfma_f32_16x16x32_bf16 v[84:87], v[116:119], v[224:227], v[84:87]\n"
"ds_write_b128 v248, v[8:11] offset:49152\n"
"v_mul_f32_dpp v26, v246, v26 row_newbcast:2 row_mask:0xf bank_mask:0xf\n"
"v_mul_f32_dpp v32, v247, v32 row_newbcast:0 row_mask:0xf bank_mask:0xf\n"
"v_mul_f32_dpp v65, v206, v65 row_newbcast:1 row_mask:0xf bank_mask:0xf\n"
"v_mul_f32_dpp v69, v206, v69 row_newbcast:1 row_mask:0xf bank_mask:0xf\n"
"v_xor_b32 v233, 0x1000, v229\n"
"v_mfma_f32_16x16x32_bf16 v[84:87], v[112:115], a[252:255], v[84:87]\n"
"ds_read_b64_tr_b16 a[6:7], v249 offset:49160\n"
"ds_read_b64_tr_b16 a[36:37], v249 offset:51200\n"
"ds_read_b64_tr_b16 v[210:211], v228 offset:18432\n"
"ds_read_b64_tr_b16 v[222:223], v232 offset:26624\n"
"v_mul_f32_dpp v31, v246, v31 row_newbcast:3 row_mask:0xf bank_mask:0xf\n"
"v_cvt_pk_bf16_f32 v25, v26, v27\n"
"v_cvt_pk_bf16_f32 v59, v62, v63\n"
"v_mul_f32_dpp v34, v247, v34 row_newbcast:2 row_mask:0xf bank_mask:0xf\n"
"v_mov_b64 v[196:197], s[22:23]\n"
"v_xor_b32 v237, 0x400, v236\n"
"v_mfma_f32_16x16x32_bf16 v[52:55], v[108:111], v[224:227], v[52:55]\n"
"ds_read_b64_tr_b16 v[184:185], v232 offset:0\n"
"ds_read_b64_tr_b16 v[212:213], v232 offset:16384\n"
"v_mfma_f32_16x16x32_bf16 v[52:55], v[104:107], a[252:255], v[52:55]\n"
"ds_read_b64_tr_b16 a[4:5], v249 offset:49152\n"
"ds_read_b64_tr_b16 a[20:21], v249 offset:50176\n"
"ds_read_b64_tr_b16 a[38:39], v249 offset:51208\n"
"ds_read_b64_tr_b16 v[220:221], v232 offset:24576\n"
"ds_read_b64_tr_b16 a[246:247], v237 offset:4104\n"
"v_cvt_pk_bf16_f32 v26, v28, v29\n"
"v_mul_f32_dpp v95, v206, v95 row_newbcast:3 row_mask:0xf bank_mask:0xf\n"
"v_cvt_pk_bf16_f32 v91, v94, v95\n"
"v_mul_f32_dpp v5, v246, v5 row_newbcast:1 row_mask:0xf bank_mask:0xf\n"
"v_mfma_f32_16x16x32_bf16 v[20:23], v[100:103], v[224:227], v[20:23]\n"
"ds_read_b64_tr_b16 v[190:191], v228 offset:10240\n"
"ds_read_b64_tr_b16 v[214:215], v232 offset:18432\n"
"v_mfma_f32_16x16x32_bf16 v[20:23], v[96:99], a[252:255], v[20:23]\n"
"ds_write_b128 v248, v[56:59] offset:50176\n"
"v_cvt_pk_bf16_f32 v27, v30, v31\n"
"v_mul_f32_dpp v4, v246, v4 row_newbcast:0 row_mask:0xf bank_mask:0xf\n"
"v_mul_f32_dpp v35, v247, v35 row_newbcast:3 row_mask:0xf bank_mask:0xf\n"
"v_cvt_pk_bf16_f32 v64, v64, v65\n"
"v_mov_b64 v[198:199], s[22:23]\n"
"v_mfma_f32_16x16x32_bf16 a[228:231], v[120:123], a[252:255], a[228:231]\n"
"s_waitcnt lgkmcnt(15)\n"
"ds_write_b128 v248, v[24:27] offset:49152\n"
"v_cvt_pk_bf16_f32 v90, v92, v93\n"
"v_mul_f32_dpp v6, v246, v6 row_newbcast:2 row_mask:0xf bank_mask:0xf\n"
"v_cvt_pk_bf16_f32 v1, v2, v3\n"
"v_cvt_pk_bf16_f32 v2, v4, v5\n"
"v_mul_f32_dpp v71, v206, v71 row_newbcast:3 row_mask:0xf bank_mask:0xf\n"
"v_mfma_f32_16x16x32_bf16 v[124:127], v[180:183], a[96:99], v[196:199]\n"
"s_waitcnt lgkmcnt(10)\n"
"ds_read_b64_tr_b16 a[14:15], v249 offset:49160\n"
"ds_read_b64_tr_b16 a[30:31], v249 offset:50184\n"
"v_mfma_f32_16x16x32_bf16 v[124:127], v[184:187], a[100:103], v[124:127]\n"
"s_waitcnt lgkmcnt(5)\n"
"ds_write_b128 v248, v[88:91] offset:51200\n"
"v_mul_f32_dpp v7, v246, v7 row_newbcast:3 row_mask:0xf bank_mask:0xf\n"
"v_cvt_pk_bf16_f32 v3, v6, v7\n"
"v_mul_f32_dpp v37, v247, v37 row_newbcast:1 row_mask:0xf bank_mask:0xf\n"
"v_cvt_pk_bf16_f32 v32, v32, v33\n"
"v_mul_f32_dpp v67, v206, v67 row_newbcast:3 row_mask:0xf bank_mask:0xf\n"
"v_mfma_f32_16x16x32_bf16 v[124:127], v[188:191], a[104:107], v[124:127]\n"
"ds_read_b64_tr_b16 a[12:13], v249 offset:49152\n"
"ds_read_b64_tr_b16 a[44:45], v249 offset:51200\n"
"ds_read_b64_tr_b16 a[46:47], v249 offset:51208\n"
"ds_read_b64_tr_b16 v[100:101], v233 offset:0\n"
"ds_read_b64_tr_b16 v[120:121], v229 offset:24576\n"
"v_mul_f32_dpp v38, v247, v38 row_newbcast:2 row_mask:0xf bank_mask:0xf\n"
"v_mul_f32_dpp v70, v206, v70 row_newbcast:2 row_mask:0xf bank_mask:0xf\n"
"v_cvt_pk_bf16_f32 v65, v66, v67\n"
"v_cvt_pk_bf16_f32 v66, v68, v69\n"
"v_cvt_pk_bf16_f32 v67, v70, v71\n"
"v_mfma_f32_16x16x32_bf16 v[124:127], v[192:195], a[108:111], v[124:127]\n"
"ds_write_b128 v248, v[0:3] offset:49152\n"
"v_mul_f32_dpp v39, v247, v39 row_newbcast:3 row_mask:0xf bank_mask:0xf\n"
"v_cvt_pk_bf16_f32 v33, v34, v35\n"
"v_cvt_pk_bf16_f32 v34, v36, v37\n"
"v_cvt_pk_bf16_f32 v35, v38, v39\n"
"v_xor_b32 v244, 0x1000, v244\n"
"v_mfma_f32_16x16x32_bf16 v[124:127], v[208:211], a[144:147], v[124:127]\n"
"s_waitcnt lgkmcnt(11)\n"
"ds_read_b64_tr_b16 a[28:29], v249 offset:50176\n"
"ds_read_b128 v[24:27], v244 offset:32768\n"
"v_mfma_f32_16x16x32_bf16 v[124:127], v[212:215], a[148:151], v[124:127]\n"
"ds_write_b128 v248, v[32:35] offset:50176\n"
"ds_write_b128 v248, v[64:67] offset:51200\n"
"v_mfma_f32_16x16x32_bf16 v[124:127], v[216:219], a[152:155], v[124:127]\n"
"ds_read_b64_tr_b16 a[0:1], v249 offset:49152\n"
"ds_read_b64_tr_b16 a[32:33], v249 offset:51200\n"
"v_mfma_f32_16x16x32_bf16 v[124:127], v[220:223], a[156:159], v[124:127]\n"
"ds_read_b64_tr_b16 a[16:17], v249 offset:50176\n"
"ds_read_b64_tr_b16 a[18:19], v249 offset:50184\n"
"ds_read_b128 v[56:59], v244 offset:34816\n"
"ds_read_b64_tr_b16 a[196:197], v237 offset:0\n"
"v_sub_u32 v231, v235, v254\n"
"v_mfma_f32_16x16x32_bf16 v[140:143], v[220:223], a[172:175], v[196:199]\n"
"ds_read_b64_tr_b16 a[2:3], v249 offset:49160\n"
"ds_read_b64_tr_b16 a[34:35], v249 offset:51208\n"
"v_mfma_f32_16x16x32_bf16 v[140:143], v[216:219], a[168:171], v[140:143]\n"
"ds_read_b128 v[28:31], v244 offset:33792\n"
"ds_read_b128 v[60:63], v244 offset:35840\n"
"v_mfma_f32_16x16x32_bf16 v[140:143], v[212:215], a[164:167], v[140:143]\n"
"ds_read_b64_tr_b16 a[200:201], v236 offset:2048\n"
"ds_read_b64_tr_b16 a[254:255], v237 offset:6152\n"
"v_mfma_f32_16x16x32_bf16 v[140:143], v[208:211], a[160:163], v[140:143]\n"
"ds_read_b64_tr_b16 a[206:207], v237 offset:2056\n"
"ds_read_b64_tr_b16 a[242:243], v236 offset:4104\n"
"v_mfma_f32_16x16x32_bf16 v[140:143], v[192:195], a[124:127], v[140:143]\n"
"ds_read_b64_tr_b16 a[244:245], v237 offset:4096\n"
"v_xor_b32 v234, 0x1000, v230\n"
"v_mfma_f32_16x16x32_bf16 v[140:143], v[188:191], a[120:123], v[140:143]\n"
"ds_read_b64_tr_b16 v[110:111], v233 offset:10240\n"
"ds_read_b64_tr_b16 a[250:251], v236 offset:6152\n"
"v_mfma_f32_16x16x32_bf16 v[140:143], v[184:187], a[116:119], v[140:143]\n"
"ds_read_b64_tr_b16 a[204:205], v237 offset:2048\n"
"ds_read_b64_tr_b16 v[102:103], v233 offset:2048\n"
"v_mfma_f32_16x16x32_bf16 v[140:143], v[180:183], a[112:115], v[140:143]\n"
"ds_read_b64_tr_b16 a[248:249], v236 offset:6144\n"
"ds_read_b64_tr_b16 a[252:253], v237 offset:6144\n"
"v_mfma_f32_16x16x32_bf16 v[156:159], v[180:183], a[128:131], v[196:199]\n"
"ds_read_b64_tr_b16 v[98:99], v229 offset:2048\n"
"ds_read_b64_tr_b16 v[116:117], v233 offset:16384\n"
"v_mfma_f32_16x16x32_bf16 v[156:159], v[184:187], a[132:135], v[156:159]\n"
"ds_read_b64_tr_b16 v[122:123], v229 offset:26624\n"
"ds_read_b64_tr_b16 v[224:225], v233 offset:24576\n"
"v_mfma_f32_16x16x32_bf16 v[156:159], v[188:191], a[136:139], v[156:159]\n"
"ds_read_b64_tr_b16 a[198:199], v237 offset:8\n"
"ds_read_b64_tr_b16 v[96:97], v229 offset:0\n"
"v_mfma_f32_16x16x32_bf16 v[156:159], v[192:195], a[140:143], v[156:159]\n"
"s_waitcnt lgkmcnt(13)\n"
"ds_read_b64_tr_b16 v[108:109], v233 offset:8192\n"
"v_mul_f32_dpp v23, v246, v23 row_newbcast:3 row_mask:0xf bank_mask:0xf\n"
"v_mfma_f32_16x16x32_bf16 v[156:159], v[208:211], a[176:179], v[156:159]\n"
"ds_read_b64_tr_b16 v[106:107], v229 offset:10240\n"
"ds_read_b64_tr_b16 v[112:113], v229 offset:16384\n"
"v_mfma_f32_16x16x32_bf16 v[156:159], v[212:215], a[180:183], v[156:159]\n"
"s_add_u32 s41, s95, 491520\n"
"ds_read_b64_tr_b16 a[192:193], v236 offset:0\n"
"ds_read_b64_tr_b16 v[118:119], v233 offset:18432\n"
"v_mfma_f32_16x16x32_bf16 v[156:159], v[216:219], a[184:187], v[156:159]\n"
"s_add_u32 m0, s44, 49152\n"
"ds_read_b64_tr_b16 a[194:195], v236 offset:8\n"
"ds_read_b64_tr_b16 v[104:105], v229 offset:8192\n"
"v_mfma_f32_16x16x32_bf16 v[156:159], v[220:223], a[188:191], v[156:159]\n"
"s_waitcnt lgkmcnt(13)\n"
"buffer_load_dwordx4 v240, s[28:31], s41 offen lds\n"
"v_mfma_f32_16x16x32_bf16 v[172:175], v[220:223], a[252:255], v[196:199]\n"
"s_add_u32 s40, s41, s52\n"
"v_min_f32 v254, v252, v250\n"
"v_sub_f32 v250, v254, v250\n"
"v_mfma_f32_16x16x32_bf16 v[172:175], v[216:219], a[248:251], v[172:175]\n"
"buffer_load_dwordx4 v240, s[28:31], s40 offen offset:1024 lds\n"
"v_mfma_f32_16x16x32_bf16 v[172:175], v[212:215], a[244:247], v[172:175]\n"
"s_add_u32 s40, s41, s53\n"
"ds_read_b64_tr_b16 v[114:115], v229 offset:18432\n"
"ds_read_b64_tr_b16 v[218:219], v230 offset:26624\n"
"v_mfma_f32_16x16x32_bf16 v[172:175], v[208:211], a[240:243], v[172:175]\n"
"buffer_load_dwordx4 v240, s[28:31], s40 offen offset:2048 lds\n"
"v_mfma_f32_16x16x32_bf16 v[172:175], v[192:195], a[204:207], v[172:175]\n"
"ds_read_b64_tr_b16 v[226:227], v233 offset:26624\n"
"ds_read_b64_tr_b16 a[232:233], v236 offset:10240\n"
"v_mfma_f32_16x16x32_bf16 v[172:175], v[188:191], a[200:203], v[172:175]\n"
"s_waitcnt lgkmcnt(6)\n"
"v_xor_b32 v235, 0x1000, v231\n"
"v_mul_f32_dpp v17, v246, v17 row_newbcast:1 row_mask:0xf bank_mask:0xf\n"
"v_mfma_f32_16x16x32_bf16 v[172:175], v[184:187], a[196:199], v[172:175]\n"
"s_waitcnt lgkmcnt(4)\n"
"s_add_u32 s40, s41, s54\n"
"ds_read_b64_tr_b16 v[222:223], v234 offset:26624\n"
"v_mul_f32_dpp v18, v246, v18 row_newbcast:2 row_mask:0xf bank_mask:0xf\n"
"v_mfma_f32_16x16x32_bf16 v[172:175], v[180:183], a[192:195], v[172:175]\n"
"buffer_load_dwordx4 v240, s[28:31], s40 offen offset:3072 lds\n"
"v_mfma_f32_16x16x32_bf16 v[128:131], v[96:99], a[96:99], v[196:199]\n"
"v_sub_f32 v252, v254, v252\n"
"v_mul_f32_dpp v16, v246, v16 row_newbcast:0 row_mask:0xf bank_mask:0xf\n"
"v_mfma_f32_16x16x32_bf16 v[128:131], v[100:103], a[100:103], v[128:131]\n"
"ds_read_b64_tr_b16 v[190:191], v230 offset:10240\n"
"v_mul_f32_dpp v86, v206, v86 row_newbcast:2 row_mask:0xf bank_mask:0xf\n"
"v_mfma_f32_16x16x32_bf16 v[128:131], v[104:107], a[104:107], v[128:131]\n"
"v_exp_f32 v252, v252\n"
"v_mfma_f32_16x16x32_bf16 v[128:131], v[108:111], a[108:111], v[128:131]\n"
"s_waitcnt lgkmcnt(4)\n"
"v_exp_f32 v250, v250\n"
"v_mfma_f32_16x16x32_bf16 v[128:131], v[112:115], a[144:147], v[128:131]\n"
"s_add_u32 s21, s60, 32\n"
"ds_read_b64_tr_b16 a[220:221], v237 offset:8192\n"
"v_mul_f32 v253, v253, v252\n"
"v_mfma_f32_16x16x32_bf16 v[128:131], v[116:119], a[148:151], v[128:131]\n"
"s_mov_b32 s69, 1\n"
"ds_read_b64_tr_b16 v[186:187], v234 offset:2048\n"
"v_fmac_f32 v253, v251, v250\n"
"v_mfma_f32_16x16x32_bf16 v[128:131], v[120:123], a[152:155], v[128:131]\n"
"s_waitcnt lgkmcnt(3)\n"
"v_bfe_u32 v201, v242, 2, 7\n"
"v_add_u32 v201, 0x100, v201\n"
"ds_read_b64_tr_b16 v[184:185], v234 offset:0\n"
"v_mfma_f32_16x16x32_bf16 v[128:131], v[224:227], a[156:159], v[128:131]\n"
"global_load_dword v200, v201, s[6:7] sc1\n"
"v_mfma_f32_16x16x32_bf16 v[144:147], v[224:227], a[172:175], v[196:199]\n"
"v_rcp_f32 v253, v253\n"
"v_mfma_f32_16x16x32_bf16 v[144:147], v[120:123], a[168:171], v[144:147]\n"
"v_mul_f32 v252, v252, v253\n"
"v_accvgpr_read_b32 v254, a208\n"
"v_mfma_f32_16x16x32_bf16 v[144:147], v[116:119], a[164:167], v[144:147]\n"
"ds_read_b64_tr_b16 v[188:189], v230 offset:8192\n"
"v_mul_f32 v250, v250, v253\n"
"v_mfma_f32_16x16x32_bf16 v[144:147], v[112:115], a[160:163], v[144:147]\n"
"s_lshl_b32 s20, s95, 9\n"
"ds_read_b64_tr_b16 v[194:195], v234 offset:10240\n"
"v_mov_b32_dpp v250, v250 row_ror:12 row_mask:0x2 bank_mask:0xf\n"
"v_mfma_f32_16x16x32_bf16 v[144:147], v[108:111], a[124:127], v[144:147]\n"
"s_lshl_b32 s19, s61, 7\n"
"v_mov_b32_dpp v250, v250 row_ror:8 row_mask:0x4 bank_mask:0xf\n"
"v_accvgpr_read_b32 v253, a210\n"
"v_mfma_f32_16x16x32_bf16 v[144:147], v[104:107], a[120:123], v[144:147]\n"
"s_lshl_b32 s91, s19, 9\n"
"v_mov_b32_dpp v252, v252 row_ror:12 row_mask:0x2 bank_mask:0xf\n"
"v_mov_b32_dpp v250, v250 row_ror:4 row_mask:0x8 bank_mask:0xf\n"
"v_mfma_f32_16x16x32_bf16 v[144:147], v[100:103], a[116:119], v[144:147]\n"
"ds_read_b64_tr_b16 v[214:215], v234 offset:18432\n"
"v_mul_f32_dpp v63, v250, v63 row_newbcast:3 row_mask:0xf bank_mask:0xf\n"
"v_mfma_f32_16x16x32_bf16 v[144:147], v[96:99], a[112:115], v[144:147]\n"
"ds_read_b64_tr_b16 v[208:209], v230 offset:16384\n"
"v_mul_f32_dpp v83, v206, v83 row_newbcast:3 row_mask:0xf bank_mask:0xf\n"
"v_mfma_f32_16x16x32_bf16 v[160:163], v[96:99], a[128:131], v[196:199]\n"
"ds_read_b64_tr_b16 v[192:193], v234 offset:8192\n"
"ds_read_b64_tr_b16 v[212:213], v234 offset:16384\n"
"v_mfma_f32_16x16x32_bf16 v[160:163], v[100:103], a[132:135], v[160:163]\n"
"v_accvgpr_read_b32 v251, a211\n"
"v_mul_f32_dpp v57, v250, v57 row_newbcast:1 row_mask:0xf bank_mask:0xf\n"
"v_mfma_f32_16x16x32_bf16 v[160:163], v[104:107], a[136:139], v[160:163]\n"
"ds_read_b64_tr_b16 v[182:183], v230 offset:2048\n"
"v_mul_f32_dpp v58, v250, v58 row_newbcast:2 row_mask:0xf bank_mask:0xf\n"
"v_mfma_f32_16x16x32_bf16 v[160:163], v[108:111], a[140:143], v[160:163]\n"
"ds_read_b64_tr_b16 v[180:181], v230 offset:0\n"
"v_mul_f32_dpp v31, v250, v31 row_newbcast:3 row_mask:0xf bank_mask:0xf\n"
"v_mfma_f32_16x16x32_bf16 v[160:163], v[112:115], a[176:179], v[160:163]\n"
"ds_read_b64_tr_b16 v[210:211], v230 offset:18432\n"
"v_mov_b32_dpp v252, v252 row_ror:8 row_mask:0x4 bank_mask:0xf\n"
"v_mfma_f32_16x16x32_bf16 v[160:163], v[116:119], a[180:183], v[160:163]\n"
"ds_read_b64_tr_b16 v[220:221], v234 offset:24576\n"
"v_mul_f32_dpp v26, v250, v26 row_newbcast:2 row_mask:0xf bank_mask:0xf\n"
"v_mfma_f32_16x16x32_bf16 v[160:163], v[120:123], a[184:187], v[160:163]\n"
"ds_read_b64_tr_b16 a[238:239], v237 offset:10248\n"
"v_mul_f32_dpp v30, v250, v30 row_newbcast:2 row_mask:0xf bank_mask:0xf\n"
"v_mfma_f32_16x16x32_bf16 v[160:163], v[224:227], a[188:191], v[160:163]\n"
"ds_read_b64_tr_b16 v[216:217], v230 offset:24576\n"
"v_mul_f32_dpp v28, v250, v28 row_newbcast:0 row_mask:0xf bank_mask:0xf\n"
"v_mfma_f32_16x16x32_bf16 v[176:179], v[224:227], a[252:255], v[196:199]\n"
"v_mov_b32_dpp v252, v252 row_ror:4 row_mask:0x8 bank_mask:0xf\n"
"v_mul_f32_dpp v61, v250, v61 row_newbcast:1 row_mask:0xf bank_mask:0xf\n"
"v_mfma_f32_16x16x32_bf16 v[176:179], v[120:123], a[248:251], v[176:179]\n"
"ds_read_b64_tr_b16 v[226:227], v235 offset:26624\n"
"v_mul_f32_dpp v59, v250, v59 row_newbcast:3 row_mask:0xf bank_mask:0xf\n"
"v_mfma_f32_16x16x32_bf16 v[176:179], v[116:119], a[244:247], v[176:179]\n"
"v_mul_f32_dpp v25, v250, v25 row_newbcast:1 row_mask:0xf bank_mask:0xf\n"
"v_mul_f32_dpp v29, v250, v29 row_newbcast:1 row_mask:0xf bank_mask:0xf\n"
"v_mfma_f32_16x16x32_bf16 v[176:179], v[112:115], a[240:243], v[176:179]\n"
"ds_read_b64_tr_b16 v[114:115], v231 offset:18432\n"
"v_mul_f32_dpp v19, v246, v19 row_newbcast:3 row_mask:0xf bank_mask:0xf\n"
"v_mfma_f32_16x16x32_bf16 v[176:179], v[108:111], a[204:207], v[176:179]\n"
"ds_read_b64_tr_b16 a[236:237], v237 offset:10240\n"
"v_mul_f32_dpp v27, v250, v27 row_newbcast:3 row_mask:0xf bank_mask:0xf\n"
"v_mfma_f32_16x16x32_bf16 v[176:179], v[104:107], a[200:203], v[176:179]\n"
"v_mul_f32_dpp v24, v250, v24 row_newbcast:0 row_mask:0xf bank_mask:0xf\n"
"v_fmac_f32_dpp v25, v252, v255 row_newbcast:1 row_mask:0xf bank_mask:0xf\n"
"v_mfma_f32_16x16x32_bf16 v[176:179], v[100:103], a[196:199], v[176:179]\n"
"ds_read_b64_tr_b16 a[234:235], v236 offset:10248\n"
"v_fmac_f32_dpp v27, v252, v251 row_newbcast:3 row_mask:0xf bank_mask:0xf\n"
"v_mfma_f32_16x16x32_bf16 v[176:179], v[96:99], a[192:195], v[176:179]\n"
"s_waitcnt lgkmcnt(4)\n"
"ds_read_b64_tr_b16 v[104:105], v231 offset:8192\n"
"ds_read_b64_tr_b16 v[106:107], v231 offset:10240\n"
"v_accvgpr_read_b32 v251, a215\n"
"v_mul_f32_dpp v55, v247, v55 row_newbcast:3 row_mask:0xf bank_mask:0xf\n"
"v_mul_f32_dpp v56, v250, v56 row_newbcast:0 row_mask:0xf bank_mask:0xf\n"
"v_mul_f32_dpp v62, v250, v62 row_newbcast:2 row_mask:0xf bank_mask:0xf\n"
"v_mfma_f32_16x16x32_bf16 v[132:135], v[180:183], a[96:99], v[196:199]\n"
"v_accvgpr_read_b32 v255, a213\n"
"v_fmac_f32_dpp v31, v252, v251 row_newbcast:3 row_mask:0xf bank_mask:0xf\n"
"v_mfma_f32_16x16x32_bf16 v[132:135], v[184:187], a[100:103], v[132:135]\n"
"ds_read_b64_tr_b16 v[108:109], v235 offset:8192\n"
"ds_read_b64_tr_b16 v[116:117], v235 offset:16384\n"
"v_mfma_f32_16x16x32_bf16 v[132:135], v[188:191], a[104:107], v[132:135]\n"
"ds_read_b64_tr_b16 v[102:103], v235 offset:2048\n"
"v_fmac_f32_dpp v26, v252, v253 row_newbcast:2 row_mask:0xf bank_mask:0xf\n"
"v_mfma_f32_16x16x32_bf16 v[132:135], v[192:195], a[108:111], v[132:135]\n"
"v_accvgpr_read_b32 v253, a214\n"
"v_mul_f32_dpp v60, v250, v60 row_newbcast:0 row_mask:0xf bank_mask:0xf\n"
"v_mfma_f32_16x16x32_bf16 v[132:135], v[208:211], a[144:147], v[132:135]\n"
"s_waitcnt vmcnt(0)\n"
"ds_read_b64_tr_b16 v[96:97], v231 offset:0\n"
"v_fmac_f32_dpp v24, v252, v254 row_newbcast:0 row_mask:0xf bank_mask:0xf\n"
"v_mfma_f32_16x16x32_bf16 v[132:135], v[212:215], a[148:151], v[132:135]\n"
"v_subrev_u32 v200, s21, v200\n"
"v_accvgpr_read_b32 v254, a212\n"
"v_mfma_f32_16x16x32_bf16 v[132:135], v[216:219], a[152:155], v[132:135]\n"
"v_cmp_lt_u32 vcc, s69, v200\n"
"v_accvgpr_read_b32 v251, a227\n"
"v_mfma_f32_16x16x32_bf16 v[132:135], v[220:223], a[156:159], v[132:135]\n"
"ds_read_b64_tr_b16 a[216:217], v236 offset:8192\n"
"ds_read_b64_tr_b16 v[118:119], v235 offset:18432\n"
"v_fmac_f32_dpp v28, v252, v254 row_newbcast:0 row_mask:0xf bank_mask:0xf\n"
"v_fmac_f32_dpp v29, v252, v255 row_newbcast:1 row_mask:0xf bank_mask:0xf\n"
"v_fmac_f32_dpp v30, v252, v253 row_newbcast:2 row_mask:0xf bank_mask:0xf\n"
"v_cvt_pk_bf16_f32 v24, v24, v25\n"
"v_cvt_pk_bf16_f32 v25, v26, v27\n"
"v_cvt_pk_bf16_f32 v26, v28, v29\n"
"v_cvt_pk_bf16_f32 v27, v30, v31\n"
"v_accvgpr_read_b32 v255, a225\n"
"v_accvgpr_read_b32 v253, a226\n"
"v_fmac_f32_dpp v59, v252, v251 row_newbcast:3 row_mask:0xf bank_mask:0xf\n"
"s_cbranch_vccnz LGV_%=\nLGVK_%=:\n"
"v_mfma_f32_16x16x32_bf16 v[148:151], v[220:223], a[172:175], v[196:199]\n"
"v_accvgpr_read_b32 v254, a224\n"
"v_fmac_f32_dpp v58, v252, v253 row_newbcast:2 row_mask:0xf bank_mask:0xf\n"
"v_mfma_f32_16x16x32_bf16 v[148:151], v[216:219], a[168:171], v[148:151]\n"
"v_add_u32_dpp v200, v200, v200 quad_perm:[1,0,3,2] row_mask:0xf bank_mask:0xf\n"
"v_accvgpr_read_b32 v253, a230\n"
"v_mfma_f32_16x16x32_bf16 v[148:151], v[212:215], a[164:167], v[148:151]\n"
"v_fmac_f32_dpp v56, v252, v254 row_newbcast:0 row_mask:0xf bank_mask:0xf\n"
"v_accvgpr_read_b32 v251, a231\n"
"v_mfma_f32_16x16x32_bf16 v[148:151], v[208:211], a[160:163], v[148:151]\n"
"v_add_u32_dpp v200, v200, v200 quad_perm:[2,3,0,1] row_mask:0xf bank_mask:0xf\n"
"ds_write_b128 v243, v[24:27] offset:0\n"
"v_fmac_f32_dpp v57, v252, v255 row_newbcast:1 row_mask:0xf bank_mask:0xf\n"
"v_accvgpr_read_b32 v254, a228\n"
"s_nop 1\n"
"v_fmac_f32_dpp v60, v252, v254 row_newbcast:0 row_mask:0xf bank_mask:0xf\n"
"v_fmac_f32_dpp v62, v252, v253 row_newbcast:2 row_mask:0xf bank_mask:0xf\n"
"v_fmac_f32_dpp v63, v252, v251 row_newbcast:3 row_mask:0xf bank_mask:0xf\n"
"v_cvt_pk_bf16_f32 v56, v56, v57\n"
"v_cvt_pk_bf16_f32 v57, v58, v59\n"
"v_cvt_pk_bf16_f32 v59, v62, v63\n"
"v_mfma_f32_16x16x32_bf16 v[148:151], v[192:195], a[124:127], v[148:151]\n"
"ds_read_b64_tr_b16 v[100:101], v235 offset:0\n"
"v_accvgpr_read_b32 v255, a229\n"
"v_mfma_f32_16x16x32_bf16 v[148:151], v[188:191], a[120:123], v[148:151]\n"
"v_cmp_lt_u32 vcc, 1, v200\n"
"v_fmac_f32_dpp v61, v252, v255 row_newbcast:1 row_mask:0xf bank_mask:0xf\n"
"v_mfma_f32_16x16x32_bf16 v[148:151], v[184:187], a[116:119], v[148:151]\n"
"ds_read_b64_tr_b16 v[98:99], v231 offset:2048\n"
"v_cvt_pk_bf16_f32 v58, v60, v61\n"
"v_mfma_f32_16x16x32_bf16 v[148:151], v[180:183], a[112:115], v[148:151]\n"
"ds_write_b128 v243, v[56:59] offset:2048\n"
"v_mul_f32_dpp v22, v246, v22 row_newbcast:2 row_mask:0xf bank_mask:0xf\n"
"v_mul_f32_dpp v48, v247, v48 row_newbcast:0 row_mask:0xf bank_mask:0xf\n"
"v_mul_f32_dpp v49, v247, v49 row_newbcast:1 row_mask:0xf bank_mask:0xf\n"
"v_mul_f32_dpp v87, v206, v87 row_newbcast:3 row_mask:0xf bank_mask:0xf\n"
"v_mfma_f32_16x16x32_bf16 v[164:167], v[180:183], a[128:131], v[196:199]\n"
"s_mov_b32 s47, 0\n"
"ds_read_b64_tr_b16 v[110:111], v235 offset:10240\n"
"v_bfe_u32 v254, v242, 4, 4\n"
"v_mfma_f32_16x16x32_bf16 v[164:167], v[184:187], a[132:135], v[164:167]\n"
"s_mov_b32 s89, vcc_lo\n"
"v_cvt_pk_bf16_f32 v16, v16, v17\n"
"v_mul_f32_dpp v81, v206, v81 row_newbcast:1 row_mask:0xf bank_mask:0xf\n"
"v_mfma_f32_16x16x32_bf16 v[164:167], v[188:191], a[136:139], v[164:167]\n"
"s_lshr_b32 s90, s89, s93\n"
"ds_read_b64_tr_b16 v[112:113], v231 offset:16384\n"
"v_xor_b32 v249, s75, v249\n"
"v_mfma_f32_16x16x32_bf16 v[164:167], v[192:195], a[140:143], v[164:167]\n"
"s_and_b32 s90, s90, 1\n"
"ds_read_b64_tr_b16 v[122:123], v231 offset:26624\n"
"v_mul_f32_dpp v51, v247, v51 row_newbcast:3 row_mask:0xf bank_mask:0xf\n"
"v_mfma_f32_16x16x32_bf16 v[164:167], v[208:211], a[176:179], v[164:167]\n"
"s_lshl_b32 s16, s61, 7\n"
"v_cvt_pk_bf16_f32 v17, v18, v19\n"
"v_cvt_pk_bf16_f32 v19, v22, v23\n"
"v_mfma_f32_16x16x32_bf16 v[164:167], v[212:215], a[180:183], v[164:167]\n"
"s_add_u32 s90, s90, s16\n"
"ds_read_b64_tr_b16 v[120:121], v231 offset:24576\n"
"ds_read_b64_tr_b16 v[224:225], v235 offset:24576\n"
"v_mfma_f32_16x16x32_bf16 v[164:167], v[216:219], a[184:187], v[164:167]\n"
"s_waitcnt vmcnt(63) lgkmcnt(5)\n"
"s_lshl_b32 s90, s90, 13\n"
"v_mul_f32_dpp v20, v246, v20 row_newbcast:0 row_mask:0xf bank_mask:0xf\n"
"v_mfma_f32_16x16x32_bf16 v[164:167], v[220:223], a[188:191], v[164:167]\n"
"s_barrier\n"
"s_add_u32 s16, s20, s90\n"
"v_mul_f32_dpp v50, v247, v50 row_newbcast:2 row_mask:0xf bank_mask:0xf\n"
"v_mul_f32_dpp v52, v247, v52 row_newbcast:0 row_mask:0xf bank_mask:0xf\n"
"v_cvt_pk_bf16_f32 v48, v48, v49\n"
"v_cvt_pk_bf16_f32 v49, v50, v51\n"
"v_mul_f32_dpp v80, v206, v80 row_newbcast:0 row_mask:0xf bank_mask:0xf\n"
"v_mfma_f32_16x16x32_bf16 v[136:139], v[96:99], a[96:99], v[196:199]\n"
"s_add_u32 s17, s16, 4096\n"
"ds_read_b64_tr_b16 v[180:181], v228 offset:32768\n"
"v_mul_f32_dpp v84, v206, v84 row_newbcast:0 row_mask:0xf bank_mask:0xf\n"
"v_mfma_f32_16x16x32_bf16 v[136:139], v[100:103], a[100:103], v[136:139]\n"
"s_mov_b32 s72, s61\n"
"ds_read_b64_tr_b16 v[182:183], v228 offset:34816\n"
"v_mul_f32_dpp v21, v246, v21 row_newbcast:1 row_mask:0xf bank_mask:0xf\n"
"v_mfma_f32_16x16x32_bf16 v[136:139], v[104:107], a[104:107], v[136:139]\n"
"s_waitcnt lgkmcnt(5)\n"
"s_mov_b32 s21, s60\n"
"ds_read_b64_tr_b16 a[208:209], v236 offset:12288\n"
"v_mul_f32_dpp v82, v206, v82 row_newbcast:2 row_mask:0xf bank_mask:0xf\n"
"v_mfma_f32_16x16x32_bf16 v[136:139], v[108:111], a[108:111], v[136:139]\n"
"s_mov_b32 s18, s77\n"
"v_cvt_pk_bf16_f32 v80, v80, v81\n"
"v_cvt_pk_bf16_f32 v81, v82, v83\n"
"v_mfma_f32_16x16x32_bf16 v[136:139], v[112:115], a[144:147], v[136:139]\n"
"s_mov_b32 s19, s77\n"
"v_mul_f32_dpp v85, v206, v85 row_newbcast:1 row_mask:0xf bank_mask:0xf\n"
"v_cvt_pk_bf16_f32 v83, v86, v87\n"
"v_mfma_f32_16x16x32_bf16 v[136:139], v[116:119], a[148:151], v[136:139]\n"
"s_waitcnt lgkmcnt(3)\n"
"s_mov_b32 s69, -1\n"
"ds_read_b64_tr_b16 v[218:219], v228 offset:59392\n"
"v_cvt_pk_bf16_f32 v18, v20, v21\n"
"v_mfma_f32_16x16x32_bf16 v[136:139], v[120:123], a[152:155], v[136:139]\n"
"s_mul_i32 s88, s61, 0x10011111\n"
"v_xor_b32 v248, s75, v248\n"
"v_cvt_pk_bf16_f32 v82, v84, v85\n"
"v_mfma_f32_16x16x32_bf16 v[136:139], v[224:227], a[156:159], v[136:139]\n"
"s_add_u32 s88, s88, 484675\n"
"ds_write_b128 v248, v[16:19] offset:49152\n"
"v_mul_f32_dpp v53, v247, v53 row_newbcast:1 row_mask:0xf bank_mask:0xf\n"
"v_mul_f32_dpp v54, v247, v54 row_newbcast:2 row_mask:0xf bank_mask:0xf\n"
"v_cvt_pk_bf16_f32 v50, v52, v53\n"
"v_cvt_pk_bf16_f32 v51, v54, v55\n"
"v_mfma_f32_16x16x32_bf16 v[152:155], v[224:227], a[172:175], v[196:199]\n"
"s_and_b32 s88, s88, 0x70077777\n"
"ds_read_b64_tr_b16 a[8:9], v249 offset:49152\n"
"ds_read_b64_tr_b16 a[10:11], v249 offset:49160\n"
"v_mfma_f32_16x16x32_bf16 v[152:155], v[120:123], a[168:171], v[152:155]\n"
"s_or_b32 s88, s88, 0x9800000\n"
"ds_write_b128 v248, v[48:51] offset:50176\n"
"ds_write_b128 v248, v[80:83] offset:51200\n"
"v_mfma_f32_16x16x32_bf16 v[152:155], v[116:119], a[164:167], v[152:155]\n"
"s_mov_b32 s84, s6\n"
"ds_read_b64_tr_b16 a[24:25], v249 offset:50176\n"
"ds_read_b64_tr_b16 a[26:27], v249 offset:50184\n"
"ds_read_b64_tr_b16 a[40:41], v249 offset:51200\n"
"ds_read_b64_tr_b16 a[42:43], v249 offset:51208\n"
"v_mfma_f32_16x16x32_bf16 v[152:155], v[112:115], a[160:163], v[152:155]\n"
"s_and_b32 s85, s7, 0xffff\n"
"ds_read_b64_tr_b16 v[188:189], v228 offset:40960\n"
"v_xor_b32 v248, s75, v248\n"
"v_mfma_f32_16x16x32_bf16 v[152:155], v[108:111], a[124:127], v[152:155]\n"
"s_mov_b32 s86, 128\n"
"ds_read_b64_tr_b16 v[184:185], v232 offset:32768\n"
"ds_read_b64_tr_b16 v[214:215], v232 offset:51200\n"
"v_mfma_f32_16x16x32_bf16 v[152:155], v[104:107], a[120:123], v[152:155]\n"
"s_mov_b32 s87, 131072\n"
"ds_read_b64_tr_b16 a[214:215], v237 offset:12296\n"
"ds_read_b64_tr_b16 v[190:191], v228 offset:43008\n"
"v_mfma_f32_16x16x32_bf16 v[152:155], v[100:103], a[116:119], v[152:155]\n"
"ds_read_b64_tr_b16 a[224:225], v236 offset:14336\n"
"ds_read_b64_tr_b16 v[212:213], v232 offset:49152\n"
"v_mfma_f32_16x16x32_bf16 v[152:155], v[96:99], a[112:115], v[152:155]\n"
"ds_read_b64_tr_b16 v[186:187], v232 offset:34816\n"
"v_mov_b64 v[84:85], 0\n"
"v_mfma_f32_16x16x32_bf16 v[168:171], v[96:99], a[128:131], v[196:199]\n"
"ds_read_b64_tr_b16 v[192:193], v232 offset:40960\n"
"ds_read_b64_tr_b16 v[222:223], v232 offset:59392\n"
"v_mfma_f32_16x16x32_bf16 v[168:171], v[100:103], a[132:135], v[168:171]\n"
"ds_read_b64_tr_b16 v[194:195], v232 offset:43008\n"
"v_bfe_u32 v255, v242, 8, 1\n"
"v_mfma_f32_16x16x32_bf16 v[168:171], v[104:107], a[136:139], v[168:171]\n"
"ds_read_b64_tr_b16 v[208:209], v228 offset:49152\n"
"ds_read_b64_tr_b16 v[210:211], v228 offset:51200\n"
"v_mfma_f32_16x16x32_bf16 v[168:171], v[108:111], a[140:143], v[168:171]\n"
"s_waitcnt lgkmcnt(12)\n"
"ds_read_b64_tr_b16 a[230:231], v237 offset:14344\n"
"ds_read_b64_tr_b16 v[216:217], v228 offset:57344\n"
"v_mfma_f32_16x16x32_bf16 v[168:171], v[112:115], a[176:179], v[168:171]\n"
"ds_read_b64_tr_b16 v[220:221], v232 offset:57344\n"
"v_xor_b32 v249, s75, v249\n"
"v_mfma_f32_16x16x32_bf16 v[168:171], v[116:119], a[180:183], v[168:171]\n"
"ds_read_b64_tr_b16 a[222:223], v237 offset:8200\n"
"ds_read_b64_tr_b16 a[226:227], v236 offset:14344\n"
"v_mfma_f32_16x16x32_bf16 v[168:171], v[120:123], a[184:187], v[168:171]\n"
"s_add_u32 s41, s70, 327680\n"
"s_add_u32 m0, s71, 32768\n"
"ds_read_b64_tr_b16 v[110:111], v233 offset:43008\n"
"v_mfma_f32_16x16x32_bf16 v[168:171], v[224:227], a[188:191], v[168:171]\n"
"buffer_load_dwordx4 v240, s[28:31], s41 offen lds\n"
"v_mfma_f32_16x16x32_bf16 v[124:127], v[180:183], a[48:51], v[124:127]\n"
"s_waitcnt lgkmcnt(9)\n"
"v_mul_u32_u24 v254, 2560, v254\n"
"v_mfma_f32_16x16x32_bf16 v[124:127], v[184:187], a[52:55], v[124:127]\n"
"v_mov_b64 v[32:33], s[22:23]\n"
"v_mov_b64 v[44:45], s[22:23]\n"
"v_mfma_f32_16x16x32_bf16 v[124:127], v[188:191], a[56:59], v[124:127]\n"
"s_waitcnt lgkmcnt(6)\n"
"s_add_u32 s40, s41, s52\n"
"ds_read_b64_tr_b16 a[212:213], v237 offset:12288\n"
"ds_read_b64_tr_b16 v[102:103], v233 offset:34816\n"
"v_mfma_f32_16x16x32_bf16 v[124:127], v[192:195], a[60:63], v[124:127]\n"
"buffer_load_dwordx4 v240, s[28:31], s40 offen offset:1024 lds\n"
"v_mfma_f32_16x16x32_bf16 v[124:127], v[208:211], a[0:3], v[124:127]\n"
"v_mov_b64 v[38:39], s[22:23]\n"
"v_mfma_f32_16x16x32_bf16 v[124:127], v[212:215], a[4:7], v[124:127]\n"
"s_waitcnt lgkmcnt(3)\n"
"v_mov_b64 v[244:245], 0\n"
"v_mfma_f32_16x16x32_bf16 v[124:127], v[216:219], a[8:11], v[124:127]\n"
"ds_read_b64_tr_b16 v[98:99], v229 offset:34816\n"
"ds_read_b64_tr_b16 v[116:117], v233 offset:49152\n"
"v_mfma_f32_16x16x32_bf16 v[124:127], v[220:223], a[12:15], v[124:127]\n"
"s_add_u32 s40, s41, s53\n"
"ds_read_b64_tr_b16 v[122:123], v229 offset:59392\n"
"ds_read_b64_tr_b16 v[224:225], v233 offset:57344\n"
"v_mfma_f32_16x16x32_bf16 v[140:143], v[220:223], a[28:31], v[140:143]\n"
"buffer_load_dwordx4 v240, s[28:31], s40 offen offset:2048 lds\n"
"v_mfma_f32_16x16x32_bf16 v[140:143], v[216:219], a[24:27], v[140:143]\n"
"v_mov_b64 v[16:17], s[22:23]\n"
"v_mov_b64 v[36:37], s[22:23]\n"
"v_mfma_f32_16x16x32_bf16 v[140:143], v[212:215], a[20:23], v[140:143]\n"
"v_mov_b64 v[246:247], 0\n"
"v_subrev_f32 v124, s22, v124\n"
"v_mfma_f32_16x16x32_bf16 v[140:143], v[208:211], a[16:19], v[140:143]\n"
"ds_read_b64_tr_b16 a[210:211], v236 offset:12296\n"
"ds_read_b64_tr_b16 v[106:107], v229 offset:43008\n"
"v_mfma_f32_16x16x32_bf16 v[140:143], v[192:195], a[76:79], v[140:143]\n"
"ds_read_b64_tr_b16 v[112:113], v229 offset:49152\n"
"v_xor_b32 v228, s75, v228\n"
"v_mfma_f32_16x16x32_bf16 v[140:143], v[188:191], a[72:75], v[140:143]\n"
"ds_read_b64_tr_b16 v[104:105], v229 offset:40960\n"
"v_mov_b64 v[26:27], s[22:23]\n"
"v_mfma_f32_16x16x32_bf16 v[140:143], v[184:187], a[68:71], v[140:143]\n"
"s_add_u32 s40, s41, s54\n"
"ds_read_b64_tr_b16 a[228:229], v237 offset:14336\n"
"ds_read_b64_tr_b16 v[118:119], v233 offset:51200\n"
"v_mfma_f32_16x16x32_bf16 v[140:143], v[180:183], a[64:67], v[140:143]\n"
"buffer_load_dwordx4 v240, s[28:31], s40 offen offset:3072 lds\n"
"v_mfma_f32_16x16x32_bf16 v[156:159], v[180:183], a[80:83], v[156:159]\n"
"s_add_u32 s41, s70, 491520\n"
"v_mov_b64 v[86:87], 0\n"
"v_mfma_f32_16x16x32_bf16 v[156:159], v[184:187], a[84:87], v[156:159]\n"
"s_add_u32 m0, s71, 49152\n"
"ds_read_b64_tr_b16 v[114:115], v229 offset:51200\n"
"ds_read_b64_tr_b16 v[226:227], v233 offset:59392\n"
"v_mfma_f32_16x16x32_bf16 v[156:159], v[188:191], a[88:91], v[156:159]\n"
"buffer_load_dwordx4 v240, s[28:31], s41 offen lds\n"
"v_mfma_f32_16x16x32_bf16 v[156:159], v[192:195], a[92:95], v[156:159]\n"
"v_and_b32 v243, 0x3f0, v242\n"
"v_subrev_f32 v142, s22, v142\n"
"v_mfma_f32_16x16x32_bf16 v[156:159], v[208:211], a[32:35], v[156:159]\n"
"v_mov_b64 v[6:7], s[22:23]\n"
"v_subrev_f32 v126, s22, v126\n"
"v_mfma_f32_16x16x32_bf16 v[156:159], v[212:215], a[36:39], v[156:159]\n"
"ds_read_b64_tr_b16 a[218:219], v236 offset:8200\n"
"ds_read_b64_tr_b16 v[96:97], v229 offset:32768\n"
"v_mfma_f32_16x16x32_bf16 v[156:159], v[216:219], a[40:43], v[156:159]\n"
"s_add_u32 s40, s41, s52\n"
"ds_read_b64_tr_b16 v[100:101], v233 offset:32768\n"
"ds_read_b64_tr_b16 v[108:109], v233 offset:40960\n"
"v_mfma_f32_16x16x32_bf16 v[156:159], v[220:223], a[44:47], v[156:159]\n"
"s_waitcnt lgkmcnt(6)\n"
"buffer_load_dwordx4 v240, s[28:31], s40 offen offset:1024 lds\n"
"v_mfma_f32_16x16x32_bf16 v[172:175], v[220:223], a[228:231], v[172:175]\n"
"v_subrev_f32 v143, s22, v143\n"
"v_xor_b32 v233, s75, v233\n"
"v_mfma_f32_16x16x32_bf16 v[172:175], v[216:219], a[224:227], v[172:175]\n"
"v_lshl_add_u32 v254, v255, 5, v254\n"
"v_subrev_f32 v125, s22, v125\n"
"v_mfma_f32_16x16x32_bf16 v[172:175], v[212:215], a[212:215], v[172:175]\n"
"s_add_u32 s40, s41, s53\n"
"ds_read_b64_tr_b16 v[120:121], v229 offset:57344\n"
"v_mfma_f32_16x16x32_bf16 v[172:175], v[208:211], a[208:211], v[172:175]\n"
"buffer_load_dwordx4 v240, s[28:31], s40 offen offset:2048 lds\n"
"v_mfma_f32_16x16x32_bf16 v[172:175], v[192:195], a[236:239], v[172:175]\n"
"v_add_u32 v236, s94, v243\n"
"v_mfma_f32_16x16x32_bf16 v[172:175], v[188:191], a[232:235], v[172:175]\n"
"v_bfe_u32 v255, v242, 9, 1\n"
"v_subrev_f32 v141, s22, v141\n"
"v_mfma_f32_16x16x32_bf16 v[172:175], v[184:187], a[220:223], v[172:175]\n"
"s_waitcnt lgkmcnt(1)\n"
"ds_read_b64_tr_b16 v[184:185], v234 offset:32768\n"
"v_mov_b64 v[54:55], s[22:23]\n"
"v_mfma_f32_16x16x32_bf16 v[172:175], v[180:183], a[216:219], v[172:175]\n"
"s_add_u32 s40, s41, s54\n"
"ds_read_b64_tr_b16 v[210:211], v230 offset:51200\n"
"v_mfma_f32_16x16x32_bf16 v[128:131], v[96:99], a[48:51], v[128:131]\n"
"buffer_load_dwordx4 v240, s[28:31], s40 offen offset:3072 lds\n"
"v_mfma_f32_16x16x32_bf16 v[128:131], v[100:103], a[52:55], v[128:131]\n"
"s_lshl_b32 s62, s70, 9\n"
"v_mov_b64 v[30:31], s[22:23]\n"
"v_xor_b32 v232, s75, v232\n"
"v_mfma_f32_16x16x32_bf16 v[128:131], v[104:107], a[56:59], v[128:131]\n"
"s_and_b32 s70, s88, 15\n"
"v_cvt_pk_bf16_f32 v124, v124, v125\n"
"v_mfma_f32_16x16x32_bf16 v[128:131], v[108:111], a[60:63], v[128:131]\n"
"s_lshr_b32 s88, s88, 4\n"
"ds_read_b64_tr_b16 v[218:219], v230 offset:59392\n"
"v_subrev_f32 v127, s22, v127\n"
"v_mfma_f32_16x16x32_bf16 v[128:131], v[112:115], a[0:3], v[128:131]\n"
"s_lshl_b32 s70, s70, 7\n"
"ds_read_b64_tr_b16 v[194:195], v234 offset:43008\n"
"v_bfe_u32 v237, v242, 4, 3\n"
"v_mfma_f32_16x16x32_bf16 v[128:131], v[116:119], a[4:7], v[128:131]\n"
"s_waitcnt lgkmcnt(4)\n"
"s_xor_b32 s71, s71, s75\n"
"v_lshl_add_u32 v243, s44, 2, v243\n"
"v_mov_b64 v[12:13], s[22:23]\n"
"v_mfma_f32_16x16x32_bf16 v[128:131], v[120:123], a[8:11], v[128:131]\n"
"v_lshlrev_b32_e64 v201, 2, s72\n"
"ds_read_b64_tr_b16 v[214:215], v234 offset:51200\n"
"v_cvt_pk_bf16_f32 v125, v126, v127\n"
"v_mfma_f32_16x16x32_bf16 v[128:131], v[224:227], a[12:15], v[128:131]\n"
"global_load_dword v200, v201, s[6:7] sc1\n"
"v_mfma_f32_16x16x32_bf16 v[144:147], v[224:227], a[28:31], v[144:147]\n"
"v_subrev_f32 v157, s22, v157\n"
"v_subrev_f32 v174, s22, v174\n"
"v_mfma_f32_16x16x32_bf16 v[144:147], v[120:123], a[24:27], v[144:147]\n"
"v_mov_b64 v[52:53], s[22:23]\n"
"v_subrev_f32 v156, s22, v156\n"
"v_mfma_f32_16x16x32_bf16 v[144:147], v[116:119], a[20:23], v[144:147]\n"
"ds_read_b64_tr_b16 v[188:189], v230 offset:40960\n"
"v_subrev_f32 v172, s22, v172\n"
"v_mfma_f32_16x16x32_bf16 v[144:147], v[112:115], a[16:19], v[144:147]\n"
"v_subrev_f32 v140, s22, v140\n"
"v_mfma_f32_16x16x32_bf16 v[144:147], v[108:111], a[76:79], v[144:147]\n"
"ds_read_b64_tr_b16 v[180:181], v230 offset:32768\n"
"v_subrev_f32 v130, s22, v130\n"
"v_mfma_f32_16x16x32_bf16 v[144:147], v[104:107], a[72:75], v[144:147]\n"
"v_lshl_add_u32 v254, v255, 4, v254\n"
"v_mov_b64 v[22:23], s[22:23]\n"
"v_mfma_f32_16x16x32_bf16 v[144:147], v[100:103], a[68:71], v[144:147]\n"
"ds_read_b64_tr_b16 v[192:193], v234 offset:40960\n"
"v_subrev_f32 v128, s22, v128\n"
"v_mfma_f32_16x16x32_bf16 v[144:147], v[96:99], a[64:67], v[144:147]\n"
"v_subrev_f32 v129, s22, v129\n"
"v_cvt_pk_bf16_f32 v126, v128, v129\n"
"v_mfma_f32_16x16x32_bf16 v[160:163], v[96:99], a[80:83], v[160:163]\n"
"ds_read_b64_tr_b16 v[186:187], v234 offset:34816\n"
"v_cvt_pk_bf16_f32 v156, v156, v157\n"
"v_mfma_f32_16x16x32_bf16 v[160:163], v[100:103], a[84:87], v[160:163]\n"
"ds_read_b64_tr_b16 v[190:191], v230 offset:43008\n"
"v_subrev_f32 v131, s22, v131\n"
"v_mfma_f32_16x16x32_bf16 v[160:163], v[104:107], a[88:91], v[160:163]\n"
"v_cvt_pk_bf16_f32 v127, v130, v131\n"
"v_subrev_f32 v146, s22, v146\n"
"v_mfma_f32_16x16x32_bf16 v[160:163], v[108:111], a[92:95], v[160:163]\n"
"ds_read_b64_tr_b16 v[220:221], v234 offset:57344\n"
"v_cvt_pk_bf16_f32 v140, v140, v141\n"
"v_mfma_f32_16x16x32_bf16 v[160:163], v[112:115], a[32:35], v[160:163]\n"
"v_lshl_add_u32 v248, v242, 8, s93\n"
"v_subrev_f32 v145, s22, v145\n"
"v_mfma_f32_16x16x32_bf16 v[160:163], v[116:119], a[36:39], v[160:163]\n"
"ds_read_b64_tr_b16 v[182:183], v230 offset:34816\n"
"v_cvt_pk_bf16_f32 v141, v142, v143\n"
"v_mfma_f32_16x16x32_bf16 v[160:163], v[120:123], a[40:43], v[160:163]\n"
"ds_read_b64_tr_b16 v[208:209], v230 offset:49152\n"
"v_subrev_f32 v159, s22, v159\n"
"v_mfma_f32_16x16x32_bf16 v[160:163], v[224:227], a[44:47], v[160:163]\n"
"buffer_store_dwordx4 v[124:127], v243, s[32:35], s16 offen sc1\n"
"v_mov_b64 v[24:25], s[22:23]\n"
"v_mfma_f32_16x16x32_bf16 v[176:179], v[224:227], a[228:231], v[176:179]\n"
"ds_read_b64_tr_b16 v[212:213], v234 offset:49152\n"
"v_mov_b64 v[50:51], s[22:23]\n"
"v_mfma_f32_16x16x32_bf16 v[176:179], v[120:123], a[224:227], v[176:179]\n"
"v_subrev_f32 v158, s22, v158\n"
"v_subrev_f32 v173, s22, v173\n"
"v_mfma_f32_16x16x32_bf16 v[176:179], v[116:119], a[212:215], v[176:179]\n"
"ds_read_b64_tr_b16 v[216:217], v230 offset:57344\n"
"v_mov_b64 v[94:95], 0\n"
"v_mfma_f32_16x16x32_bf16 v[176:179], v[112:115], a[208:211], v[176:179]\n"
"v_cvt_pk_bf16_f32 v172, v172, v173\n"
"v_mfma_f32_16x16x32_bf16 v[176:179], v[108:111], a[236:239], v[176:179]\n"
"ds_read_b64_tr_b16 v[112:113], v231 offset:49152\n"
"v_mov_b64 v[4:5], s[22:23]\n"
"v_mfma_f32_16x16x32_bf16 v[176:179], v[104:107], a[232:235], v[176:179]\n"
"ds_read_b64_tr_b16 v[222:223], v234 offset:59392\n"
"v_subrev_f32 v144, s22, v144\n"
"v_mfma_f32_16x16x32_bf16 v[176:179], v[100:103], a[220:223], v[176:179]\n"
"v_mov_b64 v[40:41], s[22:23]\n"
"v_subrev_f32 v147, s22, v147\n"
"v_mfma_f32_16x16x32_bf16 v[176:179], v[96:99], a[216:219], v[176:179]\n"
"s_waitcnt lgkmcnt(4)\n"
"ds_read_b64_tr_b16 v[224:225], v235 offset:57344\n"
"v_subrev_f32 v175, s22, v175\n"
"v_mfma_f32_16x16x32_bf16 v[132:135], v[180:183], a[48:51], v[132:135]\n"
"ds_read_b64_tr_b16 v[102:103], v235 offset:34816\n"
"v_mov_b32 v241, s60\n"
"v_mfma_f32_16x16x32_bf16 v[132:135], v[184:187], a[52:55], v[132:135]\n"
"ds_read_b64_tr_b16 v[96:97], v231 offset:32768\n"
"v_cvt_pk_bf16_f32 v157, v158, v159\n"
"v_mfma_f32_16x16x32_bf16 v[132:135], v[188:191], a[56:59], v[132:135]\n"
"ds_read_b64_tr_b16 v[114:115], v231 offset:51200\n"
"v_subrev_f32 v160, s22, v160\n"
"v_mfma_f32_16x16x32_bf16 v[132:135], v[192:195], a[60:63], v[132:135]\n"
"v_cvt_pk_bf16_f32 v142, v144, v145\n"
"v_subrev_f32 v161, s22, v161\n"
"v_mfma_f32_16x16x32_bf16 v[132:135], v[208:211], a[0:3], v[132:135]\n"
"s_waitcnt lgkmcnt(5)\n"
"ds_read_b64_tr_b16 v[98:99], v231 offset:34816\n"
"v_mfma_f32_16x16x32_bf16 v[132:135], v[212:215], a[4:7], v[132:135]\n"
"v_cvt_pk_bf16_f32 v143, v146, v147\n"
"v_mfma_f32_16x16x32_bf16 v[132:135], v[216:219], a[8:11], v[132:135]\n"
"s_waitcnt lgkmcnt(5)\n"
"ds_read_b64_tr_b16 v[116:117], v235 offset:49152\n"
"v_mov_b64 v[34:35], s[22:23]\n"
"v_mfma_f32_16x16x32_bf16 v[132:135], v[220:223], a[12:15], v[132:135]\n"
"buffer_store_dwordx4 v[140:143], v243, s[32:35], s16 offen offset:2048 sc1\n"
"ds_read_b64_tr_b16 v[106:107], v231 offset:43008\n"
"v_mfma_f32_16x16x32_bf16 v[148:151], v[220:223], a[28:31], v[148:151]\n"
"v_cvt_pk_bf16_f32 v173, v174, v175\n"
"v_subrev_f32 v176, s22, v176\n"
"v_mfma_f32_16x16x32_bf16 v[148:151], v[216:219], a[24:27], v[148:151]\n"
"ds_read_b64_tr_b16 v[100:101], v235 offset:32768\n"
"v_max_u32 v237, 1, v237\n"
"v_mfma_f32_16x16x32_bf16 v[148:151], v[212:215], a[20:23], v[148:151]\n"
"v_mov_b64 v[14:15], s[22:23]\n"
"v_mov_b64 v[46:47], s[22:23]\n"
"v_mfma_f32_16x16x32_bf16 v[148:151], v[208:211], a[16:19], v[148:151]\n"
"ds_read_b64_tr_b16 v[104:105], v231 offset:40960\n"
"v_mov_b64 v[42:43], s[22:23]\n"
"v_mfma_f32_16x16x32_bf16 v[148:151], v[192:195], a[76:79], v[148:151]\n"
"ds_read_b64_tr_b16 v[120:121], v231 offset:57344\n"
"v_mov_b64 v[2:3], s[22:23]\n"
"v_mfma_f32_16x16x32_bf16 v[148:151], v[188:191], a[72:75], v[148:151]\n"
"v_subrev_f32 v162, s22, v162\n"
"v_subrev_f32 v179, s22, v179\n"
"v_mfma_f32_16x16x32_bf16 v[148:151], v[184:187], a[68:71], v[148:151]\n"
"ds_read_b64_tr_b16 v[122:123], v231 offset:59392\n"
"v_xor_b32 v229, s75, v229\n"
"v_mfma_f32_16x16x32_bf16 v[148:151], v[180:183], a[64:67], v[148:151]\n"
"ds_read_b64_tr_b16 v[110:111], v235 offset:43008\n"
"v_add_u32 v238, s97, v254\n"
"v_mfma_f32_16x16x32_bf16 v[164:167], v[180:183], a[80:83], v[164:167]\n"
"v_add_u32 v252, s98, v254\n"
"v_add_u32 v241, 1, v241\n"
"v_mfma_f32_16x16x32_bf16 v[164:167], v[184:187], a[84:87], v[164:167]\n"
"ds_read_b64_tr_b16 v[108:109], v235 offset:40960\n"
"v_mov_b64 v[10:11], s[22:23]\n"
"v_mfma_f32_16x16x32_bf16 v[164:167], v[188:191], a[88:91], v[164:167]\n"
"v_mov_b64 v[28:29], s[22:23]\n"
"v_subrev_f32 v177, s22, v177\n"
"v_mfma_f32_16x16x32_bf16 v[164:167], v[192:195], a[92:95], v[164:167]\n"
"ds_read_b64_tr_b16 v[118:119], v235 offset:51200\n"
"v_subrev_f32 v132, s22, v132\n"
"v_mfma_f32_16x16x32_bf16 v[164:167], v[208:211], a[32:35], v[164:167]\n"
"v_subrev_f32 v163, s22, v163\n"
"v_subrev_f32 v135, s22, v135\n"
"v_mfma_f32_16x16x32_bf16 v[164:167], v[212:215], a[36:39], v[164:167]\n"
"ds_read_b64_tr_b16 v[226:227], v235 offset:59392\n"
"v_cvt_pk_bf16_f32 v159, v162, v163\n"
"v_mfma_f32_16x16x32_bf16 v[164:167], v[216:219], a[40:43], v[164:167]\n"
"v_xor_b32 v237, s61, v237\n"
"v_mov_b64 v[18:19], s[22:23]\n"
"v_mfma_f32_16x16x32_bf16 v[164:167], v[220:223], a[44:47], v[164:167]\n"
"s_barrier\n"
"s_waitcnt lgkmcnt(3)\n"
"v_mfma_f32_16x16x32_bf16 v[136:139], v[96:99], a[48:51], v[136:139]\n"
"ds_read_b64_tr_b16 v[214:215], v232 offset:18432\n"
"v_mfma_f32_16x16x32_bf16 v[136:139], v[100:103], a[52:55], v[136:139]\n"
"ds_read_b64_tr_b16 v[212:213], v232 offset:16384\n"
"v_cvt_pk_bf16_f32 v158, v160, v161\n"
"v_mfma_f32_16x16x32_bf16 v[136:139], v[104:107], a[56:59], v[136:139]\n"
"s_waitcnt lgkmcnt(4)\n"
"ds_read_b64_tr_b16 v[220:221], v232 offset:24576\n"
"v_subrev_f32 v178, s22, v178\n"
"v_mfma_f32_16x16x32_bf16 v[136:139], v[108:111], a[60:63], v[136:139]\n"
"ds_read_b64_tr_b16 v[180:181], v228 offset:0\n"
"v_mfma_f32_16x16x32_bf16 v[136:139], v[112:115], a[0:3], v[136:139]\n"
"s_waitcnt lgkmcnt(5)\n"
"ds_read_b64_tr_b16 v[194:195], v232 offset:10240\n"
"v_mfma_f32_16x16x32_bf16 v[136:139], v[116:119], a[4:7], v[136:139]\n"
"ds_read_b64_tr_b16 v[188:189], v228 offset:8192\n"
"v_add_u32 v253, s99, v254\n"
"v_mfma_f32_16x16x32_bf16 v[136:139], v[120:123], a[8:11], v[136:139]\n"
"s_waitcnt lgkmcnt(6)\n"
"ds_read_b64_tr_b16 v[184:185], v232 offset:0\n"
"v_subrev_f32 v133, s22, v133\n"
"v_mfma_f32_16x16x32_bf16 v[136:139], v[224:227], a[12:15], v[136:139]\n"
"buffer_store_dwordx4 v[156:159], v243, s[32:35], s17 offen sc1\n"
"ds_read_b64_tr_b16 v[222:223], v232 offset:26624\n"
"v_mfma_f32_16x16x32_bf16 v[152:155], v[224:227], a[28:31], v[152:155]\n"
"ds_read_b64_tr_b16 v[208:209], v228 offset:16384\n"
"v_mfma_f32_16x16x32_bf16 v[152:155], v[120:123], a[24:27], v[152:155]\n"
"ds_read_b64_tr_b16 v[190:191], v228 offset:10240\n"
"v_mfma_f32_16x16x32_bf16 v[152:155], v[116:119], a[20:23], v[152:155]\n"
"ds_read_b64_tr_b16 v[186:187], v232 offset:2048\n"
"v_subrev_f32 v134, s22, v134\n"
"v_mfma_f32_16x16x32_bf16 v[152:155], v[112:115], a[16:19], v[152:155]\n"
"ds_read_b64_tr_b16 v[218:219], v228 offset:26624\n"
"v_mfma_f32_16x16x32_bf16 v[152:155], v[108:111], a[76:79], v[152:155]\n"
"ds_read_b64_tr_b16 v[192:193], v232 offset:8192\n"
"v_mfma_f32_16x16x32_bf16 v[152:155], v[104:107], a[72:75], v[152:155]\n"
"ds_read_b64_tr_b16 v[182:183], v228 offset:2048\n"
"v_xor_b32 v239, 64, v238\n"
"v_mfma_f32_16x16x32_bf16 v[152:155], v[100:103], a[68:71], v[152:155]\n"
"ds_read_b64_tr_b16 v[210:211], v228 offset:18432\n"
"ds_read_b64_tr_b16 v[216:217], v228 offset:24576\n"
"v_mfma_f32_16x16x32_bf16 v[152:155], v[96:99], a[64:67], v[152:155]\n"
"v_mfma_f32_16x16x32_bf16 v[168:171], v[96:99], a[80:83], v[168:171]\n"
"ds_read_b64_tr_b16 v[96:97], v229 offset:0\n"
"ds_read_b64_tr_b16 v[98:99], v229 offset:2048\n"
"v_mfma_f32_16x16x32_bf16 v[168:171], v[100:103], a[84:87], v[168:171]\n"
"ds_read_b64_tr_b16 v[100:101], v233 offset:0\n"
"ds_read_b64_tr_b16 v[102:103], v233 offset:2048\n"
"v_mfma_f32_16x16x32_bf16 v[168:171], v[104:107], a[88:91], v[168:171]\n"
"ds_read_b64_tr_b16 v[104:105], v229 offset:8192\n"
"v_cvt_pk_bf16_f32 v175, v178, v179\n"
"v_mfma_f32_16x16x32_bf16 v[168:171], v[108:111], a[92:95], v[168:171]\n"
"v_mov_b64 v[20:21], s[22:23]\n"
"v_mov_b64 v[90:91], 0\n"
"v_mfma_f32_16x16x32_bf16 v[168:171], v[112:115], a[32:35], v[168:171]\n"
"v_mov_b64 v[88:89], 0\n"
"v_mov_b64 v[92:93], 0\n"
"v_mfma_f32_16x16x32_bf16 v[168:171], v[116:119], a[36:39], v[168:171]\n"
"v_mov_b64 v[48:49], s[22:23]\n"
"v_cvt_pk_bf16_f32 v132, v132, v133\n"
"v_mfma_f32_16x16x32_bf16 v[168:171], v[120:123], a[40:43], v[168:171]\n"
"s_add_u32 s47, s47, 1\n"
"v_mov_b64 v[0:1], s[22:23]\n"
"v_mov_b64 v[8:9], s[22:23]\n"
"v_mfma_f32_16x16x32_bf16 v[168:171], v[224:227], a[44:47], v[168:171]\n"
"s_waitcnt lgkmcnt(5)\n"
"v_lshlrev_b32 v237, 2, v237\n"
"v_cvt_pk_bf16_f32 v174, v176, v177\n"
"LGL_%=:\n"
"v_mfma_f32_16x16x32_bf16 v[124:127], v[180:183], a[96:99], v[196:199]\n"
"ds_read_b64_tr_b16 v[106:107], v229 offset:10240\n"
"v_cvt_pk_bf16_f32 v133, v134, v135\n"
"v_mfma_f32_16x16x32_bf16 v[124:127], v[184:187], a[100:103], v[124:127]\n"
"ds_read_b64_tr_b16 v[108:109], v233 offset:8192\n"
"v_subrev_f32 v136, s22, v136\n"
"v_mfma_f32_16x16x32_bf16 v[124:127], v[188:191], a[104:107], v[124:127]\n"
"ds_read_b64_tr_b16 v[110:111], v233 offset:10240\n"
"v_subrev_f32 v137, s22, v137\n"
"v_mfma_f32_16x16x32_bf16 v[124:127], v[192:195], a[108:111], v[124:127]\n"
"ds_read_b64_tr_b16 v[112:113], v229 offset:16384\n"
"v_subrev_f32 v138, s22, v138\n"
"v_mfma_f32_16x16x32_bf16 v[124:127], v[208:211], a[144:147], v[124:127]\n"
"ds_read_b64_tr_b16 v[114:115], v229 offset:18432\n"
"v_subrev_f32 v139, s22, v139\n"
"v_mfma_f32_16x16x32_bf16 v[124:127], v[212:215], a[148:151], v[124:127]\n"
"s_waitcnt lgkmcnt(7)\n"
"ds_read_b64_tr_b16 v[116:117], v233 offset:16384\n"
"v_cvt_pk_bf16_f32 v134, v136, v137\n"
"v_mfma_f32_16x16x32_bf16 v[124:127], v[216:219], a[152:155], v[124:127]\n"
"ds_read_b64_tr_b16 v[118:119], v233 offset:18432\n"
"v_cvt_pk_bf16_f32 v135, v138, v139\n"
"v_mfma_f32_16x16x32_bf16 v[124:127], v[220:223], a[156:159], v[124:127]\n"
"buffer_store_dwordx4 v[172:175], v243, s[32:35], s17 offen offset:2048 sc1\n"
"v_subrev_f32 v148, s22, v148\n"
"v_mfma_f32_16x16x32_bf16 v[140:143], v[220:223], a[172:175], v[196:199]\n"
"ds_read_b64_tr_b16 v[120:121], v229 offset:24576\n"
"v_subrev_f32 v149, s22, v149\n"
"v_mfma_f32_16x16x32_bf16 v[140:143], v[216:219], a[168:171], v[140:143]\n"
"ds_read_b64_tr_b16 v[122:123], v229 offset:26624\n"
"v_subrev_f32 v150, s22, v150\n"
"v_mfma_f32_16x16x32_bf16 v[140:143], v[212:215], a[164:167], v[140:143]\n"
"ds_read_b64_tr_b16 v[224:225], v233 offset:24576\n"
"v_subrev_f32 v151, s22, v151\n"
"v_mfma_f32_16x16x32_bf16 v[140:143], v[208:211], a[160:163], v[140:143]\n"
"ds_read_b64_tr_b16 v[226:227], v233 offset:26624\n"
"v_cvt_pk_bf16_f32 v148, v148, v149\n"
"v_mfma_f32_16x16x32_bf16 v[140:143], v[192:195], a[124:127], v[140:143]\n"
"v_cvt_pk_bf16_f32 v149, v150, v151\n"
"v_subrev_f32 v152, s22, v152\n"
"v_mfma_f32_16x16x32_bf16 v[140:143], v[188:191], a[120:123], v[140:143]\n"
"s_add_u32 s41, s70, 0\n"
"v_subrev_f32 v153, s22, v153\n"
"v_subrev_f32 v154, s22, v154\n"
"v_mfma_f32_16x16x32_bf16 v[140:143], v[184:187], a[116:119], v[140:143]\n"
"s_mov_b32 m0, s71\n"
"v_subrev_f32 v155, s22, v155\n"
"v_cvt_pk_bf16_f32 v150, v152, v153\n"
"v_mfma_f32_16x16x32_bf16 v[140:143], v[180:183], a[112:115], v[140:143]\n"
"s_waitcnt vmcnt(14)\n"
"buffer_load_dwordx4 v240, s[28:31], s41 offen lds\n"
"v_mfma_f32_16x16x32_bf16 v[156:159], v[180:183], a[128:131], v[196:199]\n"
"v_xor_b32 v230, s75, v230\n"
"v_cvt_pk_bf16_f32 v151, v154, v155\n"
"v_mfma_f32_16x16x32_bf16 v[156:159], v[184:187], a[132:135], v[156:159]\n"
"v_xor_b32 v234, s75, v234\n"
"v_subrev_f32 v164, s22, v164\n"
"v_mfma_f32_16x16x32_bf16 v[156:159], v[188:191], a[136:139], v[156:159]\n"
"v_subrev_f32 v165, s22, v165\n"
"v_subrev_f32 v166, s22, v166\n"
"v_mfma_f32_16x16x32_bf16 v[156:159], v[192:195], a[140:143], v[156:159]\n"
"v_subrev_f32 v167, s22, v167\n"
"v_cvt_pk_bf16_f32 v164, v164, v165\n"
"v_mfma_f32_16x16x32_bf16 v[156:159], v[208:211], a[176:179], v[156:159]\n"
"v_cvt_pk_bf16_f32 v165, v166, v167\n"
"v_subrev_f32 v168, s22, v168\n"
"v_mfma_f32_16x16x32_bf16 v[156:159], v[212:215], a[180:183], v[156:159]\n"
"v_subrev_f32 v169, s22, v169\n"
"v_subrev_f32 v170, s22, v170\n"
"v_mfma_f32_16x16x32_bf16 v[156:159], v[216:219], a[184:187], v[156:159]\n"
"v_subrev_f32 v171, s22, v171\n"
"v_cvt_pk_bf16_f32 v166, v168, v169\n"
"v_mfma_f32_16x16x32_bf16 v[156:159], v[220:223], a[188:191], v[156:159]\n"
"buffer_store_dwordx4 v[132:135], v243, s[32:35], s16 offen offset:1024 sc1\n"
"v_cvt_pk_bf16_f32 v167, v170, v171\n"
"v_mfma_f32_16x16x32_bf16 v[172:175], v[220:223], a[252:255], v[196:199]\n"
"ds_read_b64_tr_b16 v[220:221], v234 offset:24576\n"
"v_xor_b32 v231, s75, v231\n"
"v_mfma_f32_16x16x32_bf16 v[172:175], v[216:219], a[248:251], v[172:175]\n"
"ds_read_b64_tr_b16 v[216:217], v230 offset:24576\n"
"v_xor_b32 v235, s75, v235\n"
"v_mfma_f32_16x16x32_bf16 v[172:175], v[212:215], a[244:247], v[172:175]\n"
"ds_read_b64_tr_b16 v[212:213], v234 offset:16384\n"
"v_lshlrev_b32 v202, 16, v84\n"
"v_mfma_f32_16x16x32_bf16 v[172:175], v[208:211], a[240:243], v[172:175]\n"
"ds_read_b64_tr_b16 v[208:209], v230 offset:16384\n"
"v_and_b32 v84, s73, v84\n"
"v_mfma_f32_16x16x32_bf16 v[172:175], v[192:195], a[204:207], v[172:175]\n"
"ds_read_b64_tr_b16 v[192:193], v234 offset:8192\n"
"v_add_f32 v25, v25, v84\n"
"v_mfma_f32_16x16x32_bf16 v[172:175], v[188:191], a[200:203], v[172:175]\n"
"ds_read_b64_tr_b16 v[188:189], v230 offset:8192\n"
"v_lshlrev_b32 v203, 16, v85\n"
"v_mfma_f32_16x16x32_bf16 v[172:175], v[184:187], a[196:199], v[172:175]\n"
"s_add_u32 s40, s41, s52\n"
"ds_read_b64_tr_b16 v[184:185], v234 offset:0\n"
"v_and_b32 v85, s73, v85\n"
"v_mfma_f32_16x16x32_bf16 v[172:175], v[180:183], a[192:195], v[172:175]\n"
"s_waitcnt vmcnt(13) lgkmcnt(7)\n"
"buffer_load_dwordx4 v240, s[28:31], s40 offen offset:1024 lds\n"
"v_mfma_f32_16x16x32_bf16 v[128:131], v[96:99], a[96:99], v[196:199]\n"
"s_waitcnt lgkmcnt(6)\n"
"ds_read_b64_tr_b16 v[180:181], v230 offset:0\n"
"v_add_f32 v27, v27, v85\n"
"v_mfma_f32_16x16x32_bf16 v[128:131], v[100:103], a[100:103], v[128:131]\n"
"ds_read_b64_tr_b16 v[182:183], v230 offset:2048\n"
"v_lshlrev_b32 v204, 16, v86\n"
"v_mfma_f32_16x16x32_bf16 v[128:131], v[104:107], a[104:107], v[128:131]\n"
"ds_read_b64_tr_b16 v[186:187], v234 offset:2048\n"
"v_and_b32 v86, s73, v86\n"
"v_mfma_f32_16x16x32_bf16 v[128:131], v[108:111], a[108:111], v[128:131]\n"
"ds_read_b64_tr_b16 v[190:191], v230 offset:10240\n"
"v_add_f32 v29, v29, v86\n"
"v_mfma_f32_16x16x32_bf16 v[128:131], v[112:115], a[144:147], v[128:131]\n"
"s_waitcnt vmcnt(6)\n"
"ds_read_b64_tr_b16 v[194:195], v234 offset:10240\n"
"v_lshlrev_b32 v205, 16, v87\n"
"v_mfma_f32_16x16x32_bf16 v[128:131], v[116:119], a[148:151], v[128:131]\n"
"v_subrev_u32 v200, s21, v200\n"
"ds_read_b64_tr_b16 v[210:211], v230 offset:18432\n"
"v_mfma_f32_16x16x32_bf16 v[128:131], v[120:123], a[152:155], v[128:131]\n"
"v_cmp_lt_u32 vcc, s69, v200\n"
"ds_read_b64_tr_b16 v[214:215], v234 offset:18432\n"
"v_mfma_f32_16x16x32_bf16 v[128:131], v[224:227], a[156:159], v[128:131]\n"
"ds_read_b64_tr_b16 v[218:219], v230 offset:26624\n"
"s_cbranch_vccnz LGP_%=\nLGPK_%=:\n"
"v_mfma_f32_16x16x32_bf16 v[144:147], v[224:227], a[172:175], v[196:199]\n"
"s_sub_u32 s72, s72, 1\n"
"ds_read_b64_tr_b16 v[222:223], v234 offset:26624\n"
"v_and_b32 v87, s73, v87\n"
"v_mfma_f32_16x16x32_bf16 v[144:147], v[120:123], a[168:171], v[144:147]\n"
"s_and_b32 s72, s72, 7\n"
"v_add_f32 v24, v24, v202\n"
"v_add_f32 v31, v31, v87\n"
"v_mfma_f32_16x16x32_bf16 v[144:147], v[116:119], a[164:167], v[144:147]\n"
"v_add_f32 v26, v26, v203\n"
"v_lshlrev_b32 v206, 16, v88\n"
"v_mfma_f32_16x16x32_bf16 v[144:147], v[112:115], a[160:163], v[144:147]\n"
"buffer_load_dwordx4 v[72:75], v243, s[32:35], s18 offen sc1\n"
"v_mfma_f32_16x16x32_bf16 v[144:147], v[108:111], a[124:127], v[144:147]\n"
"v_and_b32 v88, s73, v88\n"
"v_add_f32 v33, v33, v88\n"
"v_mfma_f32_16x16x32_bf16 v[144:147], v[104:107], a[120:123], v[144:147]\n"
"v_lshlrev_b32 v207, 16, v89\n"
"v_and_b32 v89, s73, v89\n"
"v_mfma_f32_16x16x32_bf16 v[144:147], v[100:103], a[116:119], v[144:147]\n"
"v_add_f32 v35, v35, v89\n"
"v_lshlrev_b32 v202, 16, v90\n"
"v_mfma_f32_16x16x32_bf16 v[144:147], v[96:99], a[112:115], v[144:147]\n"
"buffer_store_dwordx4 v[148:151], v243, s[32:35], s16 offen offset:3072 sc1\n"
"v_and_b32 v90, s73, v90\n"
"v_mfma_f32_16x16x32_bf16 v[160:163], v[96:99], a[128:131], v[196:199]\n"
"v_add_f32 v37, v37, v90\n"
"v_lshlrev_b32 v203, 16, v91\n"
"v_mfma_f32_16x16x32_bf16 v[160:163], v[100:103], a[132:135], v[160:163]\n"
"v_and_b32 v91, s73, v91\n"
"v_add_f32 v39, v39, v91\n"
"v_mfma_f32_16x16x32_bf16 v[160:163], v[104:107], a[136:139], v[160:163]\n"
"v_add_f32 v28, v28, v204\n"
"v_add_f32 v30, v30, v205\n"
"v_mfma_f32_16x16x32_bf16 v[160:163], v[108:111], a[140:143], v[160:163]\n"
"v_add_f32 v32, v32, v206\n"
"v_add_f32 v34, v34, v207\n"
"v_mfma_f32_16x16x32_bf16 v[160:163], v[112:115], a[176:179], v[160:163]\n"
"v_lshlrev_b32 v204, 16, v92\n"
"v_and_b32 v92, s73, v92\n"
"v_mfma_f32_16x16x32_bf16 v[160:163], v[116:119], a[180:183], v[160:163]\n"
"v_add_f32 v41, v41, v92\n"
"v_lshlrev_b32 v205, 16, v93\n"
"v_mfma_f32_16x16x32_bf16 v[160:163], v[120:123], a[184:187], v[160:163]\n"
"s_add_u32 s40, s41, s53\n"
"v_and_b32 v93, s73, v93\n"
"v_add_f32 v43, v43, v93\n"
"v_mfma_f32_16x16x32_bf16 v[160:163], v[224:227], a[188:191], v[160:163]\n"
"buffer_load_dwordx4 v240, s[28:31], s40 offen offset:2048 lds\n"
"v_mfma_f32_16x16x32_bf16 v[176:179], v[224:227], a[252:255], v[196:199]\n"
"ds_read_b64_tr_b16 v[224:225], v235 offset:24576\n"
"v_lshlrev_b32 v206, 16, v94\n"
"v_mfma_f32_16x16x32_bf16 v[176:179], v[120:123], a[248:251], v[176:179]\n"
"ds_read_b64_tr_b16 v[120:121], v231 offset:24576\n"
"v_and_b32 v94, s73, v94\n"
"v_mfma_f32_16x16x32_bf16 v[176:179], v[116:119], a[244:247], v[176:179]\n"
"ds_read_b64_tr_b16 v[116:117], v235 offset:16384\n"
"v_add_f32 v45, v45, v94\n"
"v_mfma_f32_16x16x32_bf16 v[176:179], v[112:115], a[240:243], v[176:179]\n"
"buffer_load_dwordx4 v[76:79], v243, s[32:35], s18 offen offset:1024 sc1\n"
"v_mfma_f32_16x16x32_bf16 v[176:179], v[108:111], a[204:207], v[176:179]\n"
"ds_read_b64_tr_b16 v[108:109], v235 offset:8192\n"
"v_lshlrev_b32 v207, 16, v95\n"
"v_mfma_f32_16x16x32_bf16 v[176:179], v[104:107], a[200:203], v[176:179]\n"
"ds_read_b64_tr_b16 v[104:105], v231 offset:8192\n"
"v_and_b32 v95, s73, v95\n"
"v_mfma_f32_16x16x32_bf16 v[176:179], v[100:103], a[196:199], v[176:179]\n"
"s_add_u32 s40, s41, s54\n"
"ds_read_b64_tr_b16 v[100:101], v235 offset:0\n"
"v_add_f32 v47, v47, v95\n"
"v_mfma_f32_16x16x32_bf16 v[176:179], v[96:99], a[192:195], v[176:179]\n"
"s_waitcnt lgkmcnt(6)\n"
"buffer_load_dwordx4 v240, s[28:31], s40 offen offset:3072 lds\n"
"v_mfma_f32_16x16x32_bf16 v[132:135], v[180:183], a[96:99], v[196:199]\n"
"ds_read_b64_tr_b16 v[96:97], v231 offset:0\n"
"v_add_f32 v36, v36, v202\n"
"v_mfma_f32_16x16x32_bf16 v[132:135], v[184:187], a[100:103], v[132:135]\n"
"ds_read_b64_tr_b16 v[98:99], v231 offset:2048\n"
"v_add_f32 v38, v38, v203\n"
"v_mfma_f32_16x16x32_bf16 v[132:135], v[188:191], a[104:107], v[132:135]\n"
"ds_read_b64_tr_b16 v[102:103], v235 offset:2048\n"
"v_add_f32 v44, v44, v206\n"
"v_mfma_f32_16x16x32_bf16 v[132:135], v[192:195], a[108:111], v[132:135]\n"
"ds_read_b64_tr_b16 v[106:107], v231 offset:10240\n"
"v_add_f32 v46, v46, v207\n"
"v_mfma_f32_16x16x32_bf16 v[132:135], v[208:211], a[144:147], v[132:135]\n"
"ds_read_b64_tr_b16 v[110:111], v235 offset:10240\n"
"v_add_f32 v40, v40, v204\n"
"v_mfma_f32_16x16x32_bf16 v[132:135], v[212:215], a[148:151], v[132:135]\n"
"ds_read_b64_tr_b16 v[112:113], v231 offset:16384\n"
"v_add_f32 v42, v42, v205\n"
"v_mfma_f32_16x16x32_bf16 v[132:135], v[216:219], a[152:155], v[132:135]\n"
"ds_read_b64_tr_b16 v[114:115], v231 offset:18432\n"
"v_mfma_f32_16x16x32_bf16 v[132:135], v[220:223], a[156:159], v[132:135]\n"
"buffer_store_dwordx4 v[164:167], v243, s[32:35], s17 offen offset:1024 sc1\n"
"v_lshlrev_b32 v206, 16, v244\n"
"v_mfma_f32_16x16x32_bf16 v[148:151], v[220:223], a[172:175], v[196:199]\n"
"ds_read_b64_tr_b16 v[118:119], v235 offset:18432\n"
"v_and_b32 v244, s73, v244\n"
"v_mfma_f32_16x16x32_bf16 v[148:151], v[216:219], a[168:171], v[148:151]\n"
"ds_read_b64_tr_b16 v[122:123], v231 offset:26624\n"
"v_add_f32 v49, v49, v244\n"
"v_mfma_f32_16x16x32_bf16 v[148:151], v[212:215], a[164:167], v[148:151]\n"
"ds_read_b64_tr_b16 v[226:227], v235 offset:26624\n"
"v_lshlrev_b32 v207, 16, v245\n"
"v_mfma_f32_16x16x32_bf16 v[148:151], v[208:211], a[160:163], v[148:151]\n"
"buffer_load_dwordx4 v[80:83], v243, s[32:35], s18 offen offset:2048 sc1\n"
"v_mfma_f32_16x16x32_bf16 v[148:151], v[192:195], a[124:127], v[148:151]\n"
"v_and_b32 v245, s73, v245\n"
"v_add_f32 v51, v51, v245\n"
"v_mfma_f32_16x16x32_bf16 v[148:151], v[188:191], a[120:123], v[148:151]\n"
"s_add_u32 s41, s70, 163840\n"
"v_lshlrev_b32 v202, 16, v246\n"
"v_and_b32 v246, s73, v246\n"
"v_mfma_f32_16x16x32_bf16 v[148:151], v[184:187], a[116:119], v[148:151]\n"
"s_add_u32 m0, s71, 16384\n"
"v_add_f32 v53, v53, v246\n"
"v_lshlrev_b32 v203, 16, v247\n"
"v_mfma_f32_16x16x32_bf16 v[148:151], v[180:183], a[112:115], v[148:151]\n"
"buffer_load_dwordx4 v240, s[28:31], s41 offen lds\n"
"v_mfma_f32_16x16x32_bf16 v[164:167], v[180:183], a[128:131], v[196:199]\n"
"v_and_b32 v247, s73, v247\n"
"v_add_f32 v55, v55, v247\n"
"v_mfma_f32_16x16x32_bf16 v[164:167], v[184:187], a[132:135], v[164:167]\n"
"v_add_f32 v48, v48, v206\n"
"v_add_f32 v50, v50, v207\n"
"v_mfma_f32_16x16x32_bf16 v[164:167], v[188:191], a[136:139], v[164:167]\n"
"v_add_f32 v52, v52, v202\n"
"v_add_f32 v54, v54, v203\n"
"v_mfma_f32_16x16x32_bf16 v[164:167], v[192:195], a[140:143], v[164:167]\n"
"v_mfma_f32_16x16x32_bf16 v[164:167], v[208:211], a[176:179], v[164:167]\n"
"v_mfma_f32_16x16x32_bf16 v[164:167], v[212:215], a[180:183], v[164:167]\n"
"v_mfma_f32_16x16x32_bf16 v[164:167], v[216:219], a[184:187], v[164:167]\n"
"s_waitcnt vmcnt(12)\n"
"v_mfma_f32_16x16x32_bf16 v[164:167], v[220:223], a[188:191], v[164:167]\n"
"s_barrier\n"
"s_waitcnt lgkmcnt(0)\n"
"v_mfma_f32_16x16x32_bf16 v[136:139], v[96:99], a[96:99], v[196:199]\n"
"ds_read_b64_tr_b16 v[180:181], v228 offset:32768\n"
"v_mfma_f32_16x16x32_bf16 v[136:139], v[100:103], a[100:103], v[136:139]\n"
"ds_read_b64_tr_b16 v[182:183], v228 offset:34816\n"
"v_mfma_f32_16x16x32_bf16 v[136:139], v[104:107], a[104:107], v[136:139]\n"
"ds_read_b64_tr_b16 v[184:185], v232 offset:32768\n"
"v_mfma_f32_16x16x32_bf16 v[136:139], v[108:111], a[108:111], v[136:139]\n"
"ds_read_b64_tr_b16 v[186:187], v232 offset:34816\n"
"v_mfma_f32_16x16x32_bf16 v[136:139], v[112:115], a[144:147], v[136:139]\n"
"ds_read_b64_tr_b16 v[188:189], v228 offset:40960\n"
"v_mfma_f32_16x16x32_bf16 v[136:139], v[116:119], a[148:151], v[136:139]\n"
"ds_read_b64_tr_b16 v[190:191], v228 offset:43008\n"
"v_mfma_f32_16x16x32_bf16 v[136:139], v[120:123], a[152:155], v[136:139]\n"
"s_add_u32 s40, s41, s52\n"
"ds_read_b64_tr_b16 v[192:193], v232 offset:40960\n"
"v_mfma_f32_16x16x32_bf16 v[136:139], v[224:227], a[156:159], v[136:139]\n"
"buffer_load_dwordx4 v240, s[28:31], s40 offen offset:1024 lds\n"
"v_mfma_f32_16x16x32_bf16 v[152:155], v[224:227], a[172:175], v[196:199]\n"
"ds_read_b64_tr_b16 v[194:195], v232 offset:43008\n"
"v_mfma_f32_16x16x32_bf16 v[152:155], v[120:123], a[168:171], v[152:155]\n"
"ds_read_b64_tr_b16 v[208:209], v228 offset:49152\n"
"v_mfma_f32_16x16x32_bf16 v[152:155], v[116:119], a[164:167], v[152:155]\n"
"ds_read_b64_tr_b16 v[210:211], v228 offset:51200\n"
"v_mfma_f32_16x16x32_bf16 v[152:155], v[112:115], a[160:163], v[152:155]\n"
"ds_read_b64_tr_b16 v[212:213], v232 offset:49152\n"
"v_mfma_f32_16x16x32_bf16 v[152:155], v[108:111], a[124:127], v[152:155]\n"
"ds_read_b64_tr_b16 v[214:215], v232 offset:51200\n"
"v_mfma_f32_16x16x32_bf16 v[152:155], v[104:107], a[120:123], v[152:155]\n"
"ds_read_b64_tr_b16 v[216:217], v228 offset:57344\n"
"v_mfma_f32_16x16x32_bf16 v[152:155], v[100:103], a[116:119], v[152:155]\n"
"s_add_u32 s40, s41, s53\n"
"ds_read_b64_tr_b16 v[218:219], v228 offset:59392\n"
"v_xor_b32 v228, s75, v228\n"
"v_mfma_f32_16x16x32_bf16 v[152:155], v[96:99], a[112:115], v[152:155]\n"
"buffer_load_dwordx4 v240, s[28:31], s40 offen offset:2048 lds\n"
"v_mfma_f32_16x16x32_bf16 v[168:171], v[96:99], a[128:131], v[196:199]\n"
"ds_read_b64_tr_b16 v[220:221], v232 offset:57344\n"
"v_mfma_f32_16x16x32_bf16 v[168:171], v[100:103], a[132:135], v[168:171]\n"
"ds_read_b64_tr_b16 v[222:223], v232 offset:59392\n"
"v_xor_b32 v232, s75, v232\n"
"v_mfma_f32_16x16x32_bf16 v[168:171], v[104:107], a[136:139], v[168:171]\n"
"ds_read_b64_tr_b16 v[96:97], v229 offset:32768\n"
"v_mfma_f32_16x16x32_bf16 v[168:171], v[108:111], a[140:143], v[168:171]\n"
"ds_read_b64_tr_b16 v[98:99], v229 offset:34816\n"
"v_mfma_f32_16x16x32_bf16 v[168:171], v[112:115], a[176:179], v[168:171]\n"
"ds_read_b64_tr_b16 v[100:101], v233 offset:32768\n"
"v_mfma_f32_16x16x32_bf16 v[168:171], v[116:119], a[180:183], v[168:171]\n"
"ds_read_b64_tr_b16 v[102:103], v233 offset:34816\n"
"v_mfma_f32_16x16x32_bf16 v[168:171], v[120:123], a[184:187], v[168:171]\n"
"s_add_u32 s40, s41, s54\n"
"ds_read_b64_tr_b16 v[104:105], v229 offset:40960\n"
"v_mfma_f32_16x16x32_bf16 v[168:171], v[224:227], a[188:191], v[168:171]\n"
"s_waitcnt lgkmcnt(7)\n"
"buffer_load_dwordx4 v240, s[28:31], s40 offen offset:3072 lds\n"
"v_mfma_f32_16x16x32_bf16 v[124:127], v[180:183], a[48:51], v[124:127]\n"
"ds_read_b64_tr_b16 v[106:107], v229 offset:43008\n"
"v_mfma_f32_16x16x32_bf16 v[124:127], v[184:187], a[52:55], v[124:127]\n"
"ds_read_b64_tr_b16 v[108:109], v233 offset:40960\n"
"v_mfma_f32_16x16x32_bf16 v[124:127], v[188:191], a[56:59], v[124:127]\n"
"ds_read_b64_tr_b16 v[110:111], v233 offset:43008\n"
"v_mfma_f32_16x16x32_bf16 v[124:127], v[192:195], a[60:63], v[124:127]\n"
"ds_read_b64_tr_b16 v[112:113], v229 offset:49152\n"
"v_mfma_f32_16x16x32_bf16 v[124:127], v[208:211], a[0:3], v[124:127]\n"
"ds_read_b64_tr_b16 v[114:115], v229 offset:51200\n"
"v_mfma_f32_16x16x32_bf16 v[124:127], v[212:215], a[4:7], v[124:127]\n"
"s_add_u32 s41, s70, 327680\n"
"ds_read_b64_tr_b16 v[116:117], v233 offset:49152\n"
"v_mfma_f32_16x16x32_bf16 v[124:127], v[216:219], a[8:11], v[124:127]\n"
"s_waitcnt lgkmcnt(6)\n"
"s_add_u32 m0, s71, 32768\n"
"ds_read_b64_tr_b16 v[118:119], v233 offset:51200\n"
"v_mfma_f32_16x16x32_bf16 v[124:127], v[220:223], a[12:15], v[124:127]\n"
"buffer_load_dwordx4 v240, s[28:31], s41 offen lds\n"
"v_mfma_f32_16x16x32_bf16 v[140:143], v[220:223], a[28:31], v[140:143]\n"
"ds_read_b64_tr_b16 v[120:121], v229 offset:57344\n"
"v_mfma_f32_16x16x32_bf16 v[140:143], v[216:219], a[24:27], v[140:143]\n"
"ds_read_b64_tr_b16 v[122:123], v229 offset:59392\n"
"v_xor_b32 v229, s75, v229\n"
"v_mfma_f32_16x16x32_bf16 v[140:143], v[212:215], a[20:23], v[140:143]\n"
"ds_read_b64_tr_b16 v[224:225], v233 offset:57344\n"
"v_mfma_f32_16x16x32_bf16 v[140:143], v[208:211], a[16:19], v[140:143]\n"
"ds_read_b64_tr_b16 v[226:227], v233 offset:59392\n"
"v_xor_b32 v233, s75, v233\n"
"v_mfma_f32_16x16x32_bf16 v[140:143], v[192:195], a[76:79], v[140:143]\n"
"v_mfma_f32_16x16x32_bf16 v[140:143], v[188:191], a[72:75], v[140:143]\n"
"v_subrev_f32 v124, s22, v124\n"
"v_subrev_f32 v125, s22, v125\n"
"v_mfma_f32_16x16x32_bf16 v[140:143], v[184:187], a[68:71], v[140:143]\n"
"s_add_u32 s40, s41, s52\n"
"v_subrev_f32 v126, s22, v126\n"
"v_subrev_f32 v127, s22, v127\n"
"v_mfma_f32_16x16x32_bf16 v[140:143], v[180:183], a[64:67], v[140:143]\n"
"buffer_load_dwordx4 v240, s[28:31], s40 offen offset:1024 lds\n"
"v_mfma_f32_16x16x32_bf16 v[156:159], v[180:183], a[80:83], v[156:159]\n"
"v_cvt_pk_bf16_f32 v124, v124, v125\n"
"v_cvt_pk_bf16_f32 v125, v126, v127\n"
"v_mfma_f32_16x16x32_bf16 v[156:159], v[184:187], a[84:87], v[156:159]\n"
"v_mfma_f32_16x16x32_bf16 v[156:159], v[188:191], a[88:91], v[156:159]\n"
"v_mfma_f32_16x16x32_bf16 v[156:159], v[192:195], a[92:95], v[156:159]\n"
"v_mfma_f32_16x16x32_bf16 v[156:159], v[208:211], a[32:35], v[156:159]\n"
"v_mfma_f32_16x16x32_bf16 v[156:159], v[212:215], a[36:39], v[156:159]\n"
"v_subrev_f32 v140, s22, v140\n"
"v_subrev_f32 v141, s22, v141\n"
"v_mfma_f32_16x16x32_bf16 v[156:159], v[216:219], a[40:43], v[156:159]\n"
"s_waitcnt vmcnt(7)\n"
"v_subrev_f32 v142, s22, v142\n"
"v_subrev_f32 v143, s22, v143\n"
"v_mfma_f32_16x16x32_bf16 v[156:159], v[220:223], a[44:47], v[156:159]\n"
"s_barrier\n"
"buffer_store_dword v241, v248, s[84:87], 0 offen sc1\n"
"v_mfma_f32_16x16x32_bf16 v[172:175], v[220:223], a[228:231], v[172:175]\n"
"ds_read_b64_tr_b16 v[220:221], v234 offset:57344\n"
"v_cvt_pk_bf16_f32 v140, v140, v141\n"
"v_mfma_f32_16x16x32_bf16 v[172:175], v[216:219], a[224:227], v[172:175]\n"
"ds_read_b64_tr_b16 v[216:217], v230 offset:57344\n"
"v_cvt_pk_bf16_f32 v141, v142, v143\n"
"v_mfma_f32_16x16x32_bf16 v[172:175], v[212:215], a[212:215], v[172:175]\n"
"ds_read_b64_tr_b16 v[212:213], v234 offset:49152\n"
"v_add_u32 v241, 1, v241\n"
"v_mfma_f32_16x16x32_bf16 v[172:175], v[208:211], a[208:211], v[172:175]\n"
"buffer_load_dwordx4 v[84:87], v243, s[32:35], s18 offen offset:3072 sc1\n"
"v_mfma_f32_16x16x32_bf16 v[172:175], v[192:195], a[236:239], v[172:175]\n"
"ds_read_b64_tr_b16 v[192:193], v234 offset:40960\n"
"v_mfma_f32_16x16x32_bf16 v[172:175], v[188:191], a[232:235], v[172:175]\n"
"ds_read_b64_tr_b16 v[188:189], v230 offset:40960\n"
"v_subrev_f32 v156, s22, v156\n"
"v_mfma_f32_16x16x32_bf16 v[172:175], v[184:187], a[220:223], v[172:175]\n"
"s_add_u32 s40, s41, s53\n"
"ds_read_b64_tr_b16 v[184:185], v234 offset:32768\n"
"v_subrev_f32 v157, s22, v157\n"
"v_mfma_f32_16x16x32_bf16 v[172:175], v[180:183], a[216:219], v[172:175]\n"
"buffer_load_dwordx4 v240, s[28:31], s40 offen offset:2048 lds\n"
"v_mfma_f32_16x16x32_bf16 v[128:131], v[96:99], a[48:51], v[128:131]\n"
"s_add_u32 s16, s90, s62\n"
"ds_read_b64_tr_b16 v[180:181], v230 offset:32768\n"
"v_subrev_f32 v158, s22, v158\n"
"v_mfma_f32_16x16x32_bf16 v[128:131], v[100:103], a[52:55], v[128:131]\n"
"s_waitcnt lgkmcnt(5)\n"
"s_add_u32 s17, s16, 4096\n"
"ds_read_b64_tr_b16 v[182:183], v230 offset:34816\n"
"v_subrev_f32 v159, s22, v159\n"
"v_mfma_f32_16x16x32_bf16 v[128:131], v[104:107], a[56:59], v[128:131]\n"
"s_lshl_b32 s62, s70, 9\n"
"ds_read_b64_tr_b16 v[186:187], v234 offset:34816\n"
"v_cvt_pk_bf16_f32 v156, v156, v157\n"
"v_mfma_f32_16x16x32_bf16 v[128:131], v[108:111], a[60:63], v[128:131]\n"
"ds_read_b64_tr_b16 v[190:191], v230 offset:43008\n"
"v_cvt_pk_bf16_f32 v157, v158, v159\n"
"v_mfma_f32_16x16x32_bf16 v[128:131], v[112:115], a[0:3], v[128:131]\n"
"ds_read_b64_tr_b16 v[194:195], v234 offset:43008\n"
"v_lshlrev_b32 v202, 16, v72\n"
"v_mfma_f32_16x16x32_bf16 v[128:131], v[116:119], a[4:7], v[128:131]\n"
"ds_read_b64_tr_b16 v[208:209], v230 offset:49152\n"
"v_and_b32 v72, s73, v72\n"
"v_mfma_f32_16x16x32_bf16 v[128:131], v[120:123], a[8:11], v[128:131]\n"
"s_add_u32 s40, s41, s54\n"
"ds_read_b64_tr_b16 v[210:211], v230 offset:51200\n"
"v_add_f32 v0, v0, v202\n"
"v_mfma_f32_16x16x32_bf16 v[128:131], v[224:227], a[12:15], v[128:131]\n"
"buffer_load_dwordx4 v240, s[28:31], s40 offen offset:3072 lds\n"
"v_mfma_f32_16x16x32_bf16 v[144:147], v[224:227], a[28:31], v[144:147]\n"
"ds_read_b64_tr_b16 v[214:215], v234 offset:51200\n"
"v_add_f32 v1, v1, v72\n"
"v_mfma_f32_16x16x32_bf16 v[144:147], v[120:123], a[24:27], v[144:147]\n"
"ds_read_b64_tr_b16 v[218:219], v230 offset:59392\n"
"v_lshlrev_b32 v203, 16, v73\n"
"v_mfma_f32_16x16x32_bf16 v[144:147], v[116:119], a[20:23], v[144:147]\n"
"ds_read_b64_tr_b16 v[222:223], v234 offset:59392\n"
"v_and_b32 v73, s73, v73\n"
"v_mfma_f32_16x16x32_bf16 v[144:147], v[112:115], a[16:19], v[144:147]\n"
"buffer_load_dwordx4 v[88:91], v243, s[32:35], s19 offen sc1\n"
"v_mfma_f32_16x16x32_bf16 v[144:147], v[108:111], a[76:79], v[144:147]\n"
"v_add_f32 v2, v2, v203\n"
"v_add_f32 v3, v3, v73\n"
"v_mfma_f32_16x16x32_bf16 v[144:147], v[104:107], a[72:75], v[144:147]\n"
"s_add_u32 s41, s70, 491520\n"
"v_subrev_f32 v128, s22, v128\n"
"v_subrev_f32 v129, s22, v129\n"
"v_mfma_f32_16x16x32_bf16 v[144:147], v[100:103], a[68:71], v[144:147]\n"
"s_add_u32 m0, s71, 49152\n"
"v_subrev_f32 v130, s22, v130\n"
"v_subrev_f32 v131, s22, v131\n"
"v_mfma_f32_16x16x32_bf16 v[144:147], v[96:99], a[64:67], v[144:147]\n"
"buffer_load_dwordx4 v240, s[28:31], s41 offen lds\n"
"v_mfma_f32_16x16x32_bf16 v[160:163], v[96:99], a[80:83], v[160:163]\n"
"s_and_b32 s70, s88, 15\n"
"v_cvt_pk_bf16_f32 v126, v128, v129\n"
"v_cvt_pk_bf16_f32 v127, v130, v131\n"
"v_mfma_f32_16x16x32_bf16 v[160:163], v[100:103], a[84:87], v[160:163]\n"
"s_lshr_b32 s88, s88, 4\n"
"v_lshlrev_b32 v204, 16, v74\n"
"v_and_b32 v74, s73, v74\n"
"v_mfma_f32_16x16x32_bf16 v[160:163], v[104:107], a[88:91], v[160:163]\n"
"s_lshl_b32 s70, s70, 7\n"
"v_add_f32 v4, v4, v204\n"
"v_add_f32 v5, v5, v74\n"
"v_mfma_f32_16x16x32_bf16 v[160:163], v[108:111], a[92:95], v[160:163]\n"
"s_xor_b32 s71, s71, s75\n"
"v_lshlrev_b32 v205, 16, v75\n"
"v_and_b32 v75, s73, v75\n"
"v_mfma_f32_16x16x32_bf16 v[160:163], v[112:115], a[32:35], v[160:163]\n"
"v_add_f32 v6, v6, v205\n"
"v_add_f32 v7, v7, v75\n"
"v_mfma_f32_16x16x32_bf16 v[160:163], v[116:119], a[36:39], v[160:163]\n"
"v_subrev_f32 v144, s22, v144\n"
"v_subrev_f32 v145, s22, v145\n"
"v_mfma_f32_16x16x32_bf16 v[160:163], v[120:123], a[40:43], v[160:163]\n"
"v_subrev_f32 v146, s22, v146\n"
"v_subrev_f32 v147, s22, v147\n"
"v_mfma_f32_16x16x32_bf16 v[160:163], v[224:227], a[44:47], v[160:163]\n"
"buffer_store_dwordx4 v[124:127], v243, s[32:35], s16 offen sc1\n"
"v_cvt_pk_bf16_f32 v142, v144, v145\n"
"v_mfma_f32_16x16x32_bf16 v[176:179], v[224:227], a[228:231], v[176:179]\n"
"ds_read_b64_tr_b16 v[224:225], v235 offset:57344\n"
"v_cvt_pk_bf16_f32 v143, v146, v147\n"
"v_mfma_f32_16x16x32_bf16 v[176:179], v[120:123], a[224:227], v[176:179]\n"
"ds_read_b64_tr_b16 v[120:121], v231 offset:57344\n"
"v_lshlrev_b32 v206, 16, v76\n"
"v_mfma_f32_16x16x32_bf16 v[176:179], v[116:119], a[212:215], v[176:179]\n"
"ds_read_b64_tr_b16 v[116:117], v235 offset:49152\n"
"v_and_b32 v76, s73, v76\n"
"v_mfma_f32_16x16x32_bf16 v[176:179], v[112:115], a[208:211], v[176:179]\n"
"buffer_load_dwordx4 v[92:95], v243, s[32:35], s19 offen offset:1024 sc1\n"
"v_mfma_f32_16x16x32_bf16 v[176:179], v[108:111], a[236:239], v[176:179]\n"
"ds_read_b64_tr_b16 v[108:109], v235 offset:40960\n"
"v_add_f32 v8, v8, v206\n"
"v_mfma_f32_16x16x32_bf16 v[176:179], v[104:107], a[232:235], v[176:179]\n"
"ds_read_b64_tr_b16 v[104:105], v231 offset:40960\n"
"v_subrev_f32 v160, s22, v160\n"
"v_mfma_f32_16x16x32_bf16 v[176:179], v[100:103], a[220:223], v[176:179]\n"
"s_add_u32 s40, s41, s52\n"
"ds_read_b64_tr_b16 v[100:101], v235 offset:32768\n"
"v_subrev_f32 v161, s22, v161\n"
"v_mfma_f32_16x16x32_bf16 v[176:179], v[96:99], a[216:219], v[176:179]\n"
"s_waitcnt vmcnt(14) lgkmcnt(6)\n"
"buffer_load_dwordx4 v240, s[28:31], s40 offen offset:1024 lds\n"
"v_mfma_f32_16x16x32_bf16 v[132:135], v[180:183], a[48:51], v[132:135]\n"
"ds_read_b64_tr_b16 v[96:97], v231 offset:32768\n"
"v_subrev_f32 v162, s22, v162\n"
"v_mfma_f32_16x16x32_bf16 v[132:135], v[184:187], a[52:55], v[132:135]\n"
"ds_read_b64_tr_b16 v[98:99], v231 offset:34816\n"
"v_subrev_f32 v163, s22, v163\n"
"v_mfma_f32_16x16x32_bf16 v[132:135], v[188:191], a[56:59], v[132:135]\n"
"ds_read_b64_tr_b16 v[102:103], v235 offset:34816\n"
"v_cvt_pk_bf16_f32 v158, v160, v161\n"
"v_mfma_f32_16x16x32_bf16 v[132:135], v[192:195], a[60:63], v[132:135]\n"
"ds_read_b64_tr_b16 v[106:107], v231 offset:43008\n"
"v_cvt_pk_bf16_f32 v159, v162, v163\n"
"v_mfma_f32_16x16x32_bf16 v[132:135], v[208:211], a[0:3], v[132:135]\n"
"ds_read_b64_tr_b16 v[110:111], v235 offset:43008\n"
"v_add_f32 v9, v9, v76\n"
"v_mfma_f32_16x16x32_bf16 v[132:135], v[212:215], a[4:7], v[132:135]\n"
"ds_read_b64_tr_b16 v[112:113], v231 offset:49152\n"
"v_lshlrev_b32 v207, 16, v77\n"
"v_mfma_f32_16x16x32_bf16 v[132:135], v[216:219], a[8:11], v[132:135]\n"
"ds_read_b64_tr_b16 v[114:115], v231 offset:51200\n"
"v_and_b32 v77, s73, v77\n"
"v_mfma_f32_16x16x32_bf16 v[132:135], v[220:223], a[12:15], v[132:135]\n"
"buffer_store_dwordx4 v[140:143], v243, s[32:35], s16 offen offset:2048 sc1\n"
"v_add_f32 v10, v10, v207\n"
"v_mfma_f32_16x16x32_bf16 v[148:151], v[220:223], a[28:31], v[148:151]\n"
"ds_read_b64_tr_b16 v[118:119], v235 offset:51200\n"
"v_add_f32 v11, v11, v77\n"
"v_mfma_f32_16x16x32_bf16 v[148:151], v[216:219], a[24:27], v[148:151]\n"
"ds_read_b64_tr_b16 v[122:123], v231 offset:59392\n"
"v_lshlrev_b32 v202, 16, v78\n"
"v_mfma_f32_16x16x32_bf16 v[148:151], v[212:215], a[20:23], v[148:151]\n"
"ds_read_b64_tr_b16 v[226:227], v235 offset:59392\n"
"v_and_b32 v78, s73, v78\n"
"v_mfma_f32_16x16x32_bf16 v[148:151], v[208:211], a[16:19], v[148:151]\n"
"v_add_f32 v12, v12, v202\n"
"v_add_f32 v13, v13, v78\n"
"v_mfma_f32_16x16x32_bf16 v[148:151], v[192:195], a[76:79], v[148:151]\n"
"v_lshlrev_b32 v203, 16, v79\n"
"v_and_b32 v79, s73, v79\n"
"v_mfma_f32_16x16x32_bf16 v[148:151], v[188:191], a[72:75], v[148:151]\n"
"v_add_f32 v14, v14, v203\n"
"v_add_f32 v15, v15, v79\n"
"v_mfma_f32_16x16x32_bf16 v[148:151], v[184:187], a[68:71], v[148:151]\n"
"s_add_u32 s40, s41, s53\n"
"v_lshlrev_b32 v204, 16, v80\n"
"v_and_b32 v80, s73, v80\n"
"v_mfma_f32_16x16x32_bf16 v[148:151], v[180:183], a[64:67], v[148:151]\n"
"buffer_load_dwordx4 v240, s[28:31], s40 offen offset:2048 lds\n"
"v_mfma_f32_16x16x32_bf16 v[164:167], v[180:183], a[80:83], v[164:167]\n"
"v_add_f32 v16, v16, v204\n"
"v_add_f32 v17, v17, v80\n"
"v_mfma_f32_16x16x32_bf16 v[164:167], v[184:187], a[84:87], v[164:167]\n"
"v_lshlrev_b32 v205, 16, v81\n"
"v_and_b32 v81, s73, v81\n"
"v_mfma_f32_16x16x32_bf16 v[164:167], v[188:191], a[88:91], v[164:167]\n"
"v_add_f32 v18, v18, v205\n"
"v_add_f32 v19, v19, v81\n"
"v_mfma_f32_16x16x32_bf16 v[164:167], v[192:195], a[92:95], v[164:167]\n"
"buffer_load_dwordx4 v[244:247], v243, s[32:35], s19 offen offset:2048 sc1\n"
"v_mfma_f32_16x16x32_bf16 v[164:167], v[208:211], a[32:35], v[164:167]\n"
"s_lshl_b32 s18, s72, 4\n"
"v_lshlrev_b32 v206, 16, v82\n"
"v_and_b32 v82, s73, v82\n"
"v_mfma_f32_16x16x32_bf16 v[164:167], v[212:215], a[36:39], v[164:167]\n"
"s_lshl_b32 s19, s72, 2\n"
"v_add_f32 v20, v20, v206\n"
"v_add_f32 v21, v21, v82\n"
"v_mfma_f32_16x16x32_bf16 v[164:167], v[216:219], a[40:43], v[164:167]\n"
"s_waitcnt vmcnt(14)\n"
"s_lshr_b32 s19, s89, s19\n"
"v_lshlrev_b32 v207, 16, v83\n"
"v_mfma_f32_16x16x32_bf16 v[164:167], v[220:223], a[44:47], v[164:167]\n"
"s_barrier\n"
"s_and_b32 s19, s19, 1\n"
"s_waitcnt lgkmcnt(0)\n"
"v_mfma_f32_16x16x32_bf16 v[136:139], v[96:99], a[48:51], v[136:139]\n"
"s_lshl3_add_u32 s18, s18, s19\n"
"ds_read_b64_tr_b16 v[180:181], v228 offset:0\n"
"v_and_b32 v83, s73, v83\n"
"v_mfma_f32_16x16x32_bf16 v[136:139], v[100:103], a[52:55], v[136:139]\n"
"s_lshl_b32 s18, s18, 13\n"
"ds_read_b64_tr_b16 v[182:183], v228 offset:2048\n"
"v_add_f32 v22, v22, v207\n"
"v_mfma_f32_16x16x32_bf16 v[136:139], v[104:107], a[56:59], v[136:139]\n"
"s_add_u32 s18, s18, s91\n"
"v_lshlrev_b32_e64 v201, 2, s72\n"
"ds_read_b64_tr_b16 v[184:185], v232 offset:0\n"
"v_mfma_f32_16x16x32_bf16 v[136:139], v[108:111], a[60:63], v[136:139]\n"
"s_add_u32 s19, s18, 4096\n"
"global_load_dword v200, v201, s[6:7] sc1\n"
"v_mfma_f32_16x16x32_bf16 v[136:139], v[112:115], a[0:3], v[136:139]\n"
"s_mov_b32 s69, 15\n"
"ds_read_b64_tr_b16 v[186:187], v232 offset:2048\n"
"v_add_f32 v23, v23, v83\n"
"v_mfma_f32_16x16x32_bf16 v[136:139], v[116:119], a[4:7], v[136:139]\n"
"s_add_u32 s21, s21, 1\n"
"ds_read_b64_tr_b16 v[188:189], v228 offset:8192\n"
"v_subrev_f32 v172, s22, v172\n"
"v_mfma_f32_16x16x32_bf16 v[136:139], v[120:123], a[8:11], v[136:139]\n"
"ds_read_b64_tr_b16 v[190:191], v228 offset:10240\n"
"v_subrev_f32 v173, s22, v173\n"
"v_mfma_f32_16x16x32_bf16 v[136:139], v[224:227], a[12:15], v[136:139]\n"
"buffer_store_dwordx4 v[156:159], v243, s[32:35], s17 offen sc1\n"
"v_subrev_f32 v174, s22, v174\n"
"v_mfma_f32_16x16x32_bf16 v[152:155], v[224:227], a[28:31], v[152:155]\n"
"ds_read_b64_tr_b16 v[192:193], v232 offset:8192\n"
"v_subrev_f32 v175, s22, v175\n"
"v_mfma_f32_16x16x32_bf16 v[152:155], v[120:123], a[24:27], v[152:155]\n"
"ds_read_b64_tr_b16 v[194:195], v232 offset:10240\n"
"v_cvt_pk_bf16_f32 v172, v172, v173\n"
"v_mfma_f32_16x16x32_bf16 v[152:155], v[116:119], a[20:23], v[152:155]\n"
"ds_read_b64_tr_b16 v[208:209], v228 offset:16384\n"
"v_cvt_pk_bf16_f32 v173, v174, v175\n"
"v_mfma_f32_16x16x32_bf16 v[152:155], v[112:115], a[16:19], v[152:155]\n"
"ds_read_b64_tr_b16 v[210:211], v228 offset:18432\n"
"v_subrev_f32 v176, s22, v176\n"
"v_mfma_f32_16x16x32_bf16 v[152:155], v[108:111], a[76:79], v[152:155]\n"
"ds_read_b64_tr_b16 v[212:213], v232 offset:16384\n"
"v_subrev_f32 v177, s22, v177\n"
"v_mfma_f32_16x16x32_bf16 v[152:155], v[104:107], a[72:75], v[152:155]\n"
"ds_read_b64_tr_b16 v[214:215], v232 offset:18432\n"
"v_subrev_f32 v178, s22, v178\n"
"v_mfma_f32_16x16x32_bf16 v[152:155], v[100:103], a[68:71], v[152:155]\n"
"s_add_u32 s40, s41, s54\n"
"ds_read_b64_tr_b16 v[216:217], v228 offset:24576\n"
"v_subrev_f32 v179, s22, v179\n"
"v_mfma_f32_16x16x32_bf16 v[152:155], v[96:99], a[64:67], v[152:155]\n"
"buffer_load_dwordx4 v240, s[28:31], s40 offen offset:3072 lds\n"
"v_mfma_f32_16x16x32_bf16 v[168:171], v[96:99], a[80:83], v[168:171]\n"
"ds_read_b64_tr_b16 v[218:219], v228 offset:26624\n"
"v_cvt_pk_bf16_f32 v174, v176, v177\n"
"v_mfma_f32_16x16x32_bf16 v[168:171], v[100:103], a[84:87], v[168:171]\n"
"ds_read_b64_tr_b16 v[220:221], v232 offset:24576\n"
"v_cvt_pk_bf16_f32 v175, v178, v179\n"
"v_mfma_f32_16x16x32_bf16 v[168:171], v[104:107], a[88:91], v[168:171]\n"
"ds_read_b64_tr_b16 v[222:223], v232 offset:26624\n"
"v_subrev_f32 v132, s22, v132\n"
"v_mfma_f32_16x16x32_bf16 v[168:171], v[108:111], a[92:95], v[168:171]\n"
"ds_read_b64_tr_b16 v[96:97], v229 offset:0\n"
"v_subrev_f32 v133, s22, v133\n"
"v_mfma_f32_16x16x32_bf16 v[168:171], v[112:115], a[32:35], v[168:171]\n"
"ds_read_b64_tr_b16 v[98:99], v229 offset:2048\n"
"v_subrev_f32 v134, s22, v134\n"
"v_mfma_f32_16x16x32_bf16 v[168:171], v[116:119], a[36:39], v[168:171]\n"
"ds_read_b64_tr_b16 v[100:101], v233 offset:0\n"
"v_subrev_f32 v135, s22, v135\n"
"v_mfma_f32_16x16x32_bf16 v[168:171], v[120:123], a[40:43], v[168:171]\n"
"s_add_u32 s47, s47, 1\n"
"ds_read_b64_tr_b16 v[102:103], v233 offset:2048\n"
"v_cvt_pk_bf16_f32 v132, v132, v133\n"
"v_mfma_f32_16x16x32_bf16 v[168:171], v[224:227], a[44:47], v[168:171]\n"
"s_waitcnt lgkmcnt(7)\n"
"s_cmp_lg_u32 s47, 9\n"
"ds_read_b64_tr_b16 v[104:105], v229 offset:8192\n"
"s_cbranch_scc1 LGL_%=\n"
"v_mfma_f32_16x16x32_bf16 v[0:3], v[180:183], a[96:99], v[0:3]\n"
"v_xor_b32 v235, s75, v235\n"
"v_subrev_f32 v136, s22, v136\n"
"v_mfma_f32_16x16x32_bf16 v[0:3], v[184:187], a[100:103], v[0:3]\n"
"ds_read_b64_tr_b16 v[114:115], v229 offset:18432\n"
"v_cvt_pk_bf16_f32 v133, v134, v135\n"
"v_mfma_f32_16x16x32_bf16 v[0:3], v[188:191], a[104:107], v[0:3]\n"
"v_mov_b32 v201, v237\n"
"v_subrev_f32 v138, s22, v138\n"
"v_subrev_f32 v139, s22, v139\n"
"v_mfma_f32_16x16x32_bf16 v[0:3], v[192:195], a[108:111], v[0:3]\n"
"global_load_dword v200, v201, s[6:7] sc1\n"
"v_mfma_f32_16x16x32_bf16 v[0:3], v[208:211], a[144:147], v[0:3]\n"
"v_subrev_f32 v167, s22, v167\n"
"v_cvt_pk_bf16_f32 v135, v138, v139\n"
"v_mfma_f32_16x16x32_bf16 v[0:3], v[212:215], a[148:151], v[0:3]\n"
"s_waitcnt lgkmcnt(3)\n"
"v_subrev_f32 v148, s22, v148\n"
"v_subrev_f32 v137, s22, v137\n"
"v_mfma_f32_16x16x32_bf16 v[0:3], v[216:219], a[152:155], v[0:3]\n"
"v_subrev_f32 v165, s22, v165\n"
"v_subrev_f32 v168, s22, v168\n"
"v_mfma_f32_16x16x32_bf16 v[0:3], v[220:223], a[156:159], v[0:3]\n"
"buffer_store_dwordx4 v[172:175], v243, s[32:35], s17 offen offset:2048 sc1\n"
"v_subrev_f32 v151, s22, v151\n"
"v_cvt_pk_bf16_f32 v134, v136, v137\n"
"v_mfma_f32_16x16x32_bf16 v[16:19], v[220:223], a[172:175], v[16:19]\n"
"buffer_store_dwordx4 v[132:135], v243, s[32:35], s16 offen offset:1024 sc1\n"
"v_subrev_f32 v149, s22, v149\n"
"v_subrev_f32 v150, s22, v150\n"
"v_subrev_f32 v164, s22, v164\n"
"v_cvt_pk_bf16_f32 v164, v164, v165\n"
"v_subrev_f32 v153, s22, v153\n"
"v_subrev_f32 v155, s22, v155\n"
"v_mfma_f32_16x16x32_bf16 v[16:19], v[216:219], a[168:171], v[16:19]\n"
"v_cvt_pk_bf16_f32 v148, v148, v149\n"
"v_subrev_f32 v154, s22, v154\n"
"v_mfma_f32_16x16x32_bf16 v[16:19], v[212:215], a[164:167], v[16:19]\n"
"v_cvt_pk_bf16_f32 v149, v150, v151\n"
"v_subrev_f32 v170, s22, v170\n"
"v_mfma_f32_16x16x32_bf16 v[16:19], v[208:211], a[160:163], v[16:19]\n"
"v_subrev_f32 v166, s22, v166\n"
"v_subrev_f32 v171, s22, v171\n"
"v_mfma_f32_16x16x32_bf16 v[16:19], v[192:195], a[124:127], v[16:19]\n"
"v_cvt_pk_bf16_f32 v165, v166, v167\n"
"v_cvt_pk_bf16_f32 v167, v170, v171\n"
"v_mfma_f32_16x16x32_bf16 v[16:19], v[188:191], a[120:123], v[16:19]\n"
"v_subrev_f32 v152, s22, v152\n"
"v_cvt_pk_bf16_f32 v151, v154, v155\n"
"v_mfma_f32_16x16x32_bf16 v[16:19], v[184:187], a[116:119], v[16:19]\n"
"v_cvt_pk_bf16_f32 v150, v152, v153\n"
"v_subrev_f32 v169, s22, v169\n"
"v_mfma_f32_16x16x32_bf16 v[16:19], v[180:183], a[112:115], v[16:19]\n"
"s_waitcnt vmcnt(16)\n"
"buffer_store_dwordx4 v[148:151], v243, s[32:35], s16 offen offset:3072 sc1\n"
"v_cvt_pk_bf16_f32 v166, v168, v169\n"
"v_mfma_f32_16x16x32_bf16 v[32:35], v[180:183], a[128:131], v[32:35]\n"
"buffer_store_dwordx4 v[164:167], v243, s[32:35], s17 offen offset:1024 sc1\n"
"ds_read_b64_tr_b16 v[120:121], v229 offset:24576\n"
"v_mfma_f32_16x16x32_bf16 v[32:35], v[184:187], a[132:135], v[32:35]\n"
"ds_read_b64_tr_b16 v[224:225], v233 offset:24576\n"
"v_xor_b32 v234, s75, v234\n"
"v_mfma_f32_16x16x32_bf16 v[32:35], v[188:191], a[136:139], v[32:35]\n"
"ds_read_b64_tr_b16 v[122:123], v229 offset:26624\n"
"v_lshlrev_b32 v203, 16, v85\n"
"v_mfma_f32_16x16x32_bf16 v[32:35], v[192:195], a[140:143], v[32:35]\n"
"ds_read_b64_tr_b16 v[108:109], v233 offset:8192\n"
"v_lshlrev_b32 v202, 16, v84\n"
"v_mfma_f32_16x16x32_bf16 v[32:35], v[208:211], a[176:179], v[32:35]\n"
"ds_read_b64_tr_b16 v[226:227], v233 offset:26624\n"
"v_and_b32 v84, s73, v84\n"
"v_mfma_f32_16x16x32_bf16 v[32:35], v[212:215], a[180:183], v[32:35]\n"
"ds_read_b64_tr_b16 v[110:111], v233 offset:10240\n"
"v_bfe_u32 v161, v242, 8, 2\n"
"v_mfma_f32_16x16x32_bf16 v[32:35], v[216:219], a[184:187], v[32:35]\n"
"ds_read_b64_tr_b16 v[106:107], v229 offset:10240\n"
"v_lshlrev_b32 v205, 16, v87\n"
"v_mfma_f32_16x16x32_bf16 v[32:35], v[220:223], a[188:191], v[32:35]\n"
"v_xor_b32 v230, s75, v230\n"
"v_lshlrev_b32 v204, 16, v86\n"
"v_mfma_f32_16x16x32_bf16 v[48:51], v[220:223], a[252:255], v[48:51]\n"
"ds_read_b64_tr_b16 v[220:221], v234 offset:24576\n"
"v_add_f32 v26, v26, v203\n"
"v_mfma_f32_16x16x32_bf16 v[48:51], v[216:219], a[248:251], v[48:51]\n"
"ds_read_b64_tr_b16 v[112:113], v229 offset:16384\n"
"v_bfe_u32 v160, v242, 6, 2\n"
"v_mfma_f32_16x16x32_bf16 v[48:51], v[212:215], a[244:247], v[48:51]\n"
"ds_read_b64_tr_b16 v[116:117], v233 offset:16384\n"
"v_add_f32 v24, v24, v202\n"
"v_mfma_f32_16x16x32_bf16 v[48:51], v[208:211], a[240:243], v[48:51]\n"
"ds_read_b64_tr_b16 v[118:119], v233 offset:18432\n"
"v_add_f32 v25, v25, v84\n"
"v_mfma_f32_16x16x32_bf16 v[48:51], v[192:195], a[204:207], v[48:51]\n"
"ds_read_b64_tr_b16 v[210:211], v230 offset:18432\n"
"v_add_f32 v28, v28, v204\n"
"v_mfma_f32_16x16x32_bf16 v[48:51], v[188:191], a[200:203], v[48:51]\n"
"ds_read_b64_tr_b16 v[188:189], v230 offset:8192\n"
"v_and_b32 v86, s73, v86\n"
"v_mfma_f32_16x16x32_bf16 v[48:51], v[184:187], a[196:199], v[48:51]\n"
"ds_read_b64_tr_b16 v[194:195], v234 offset:10240\n"
"v_mov_b32 v164, 0\n"
"v_mfma_f32_16x16x32_bf16 v[48:51], v[180:183], a[192:195], v[48:51]\n"
"s_waitcnt vmcnt(15)\n"
"v_xor_b32 v160, v160, v161\n"
"v_lshlrev_b32 v202, 16, v90\n"
"v_mfma_f32_16x16x32_bf16 v[4:7], v[96:99], a[96:99], v[4:7]\n"
"s_waitcnt lgkmcnt(6)\n"
"ds_read_b64_tr_b16 v[222:223], v234 offset:26624\n"
"v_lshlrev_b32 v203, 16, v91\n"
"v_mfma_f32_16x16x32_bf16 v[4:7], v[100:103], a[100:103], v[4:7]\n"
"ds_read_b64_tr_b16 v[184:185], v234 offset:0\n"
"v_lshlrev_b32 v206, 16, v88\n"
"v_mfma_f32_16x16x32_bf16 v[4:7], v[104:107], a[104:107], v[4:7]\n"
"ds_read_b64_tr_b16 v[216:217], v230 offset:24576\n"
"v_mfma_f32_16x16x32_bf16 v[4:7], v[108:111], a[108:111], v[4:7]\n"
"s_waitcnt lgkmcnt(6)\n"
"ds_read_b64_tr_b16 v[218:219], v230 offset:26624\n"
"v_and_b32 v91, s73, v91\n"
"v_mfma_f32_16x16x32_bf16 v[4:7], v[112:115], a[144:147], v[4:7]\n"
"s_waitcnt vmcnt(4)\n"
"ds_read_b64_tr_b16 v[190:191], v230 offset:10240\n"
"v_lshlrev_b32 v207, 16, v89\n"
"v_mfma_f32_16x16x32_bf16 v[4:7], v[116:119], a[148:151], v[4:7]\n"
"v_subrev_u32 v200, s21, v200\n"
"ds_read_b64_tr_b16 v[208:209], v230 offset:16384\n"
"v_mfma_f32_16x16x32_bf16 v[4:7], v[120:123], a[152:155], v[4:7]\n"
"v_cmp_lt_u32 vcc, s69, v200\n"
"ds_read_b64_tr_b16 v[182:183], v230 offset:2048\n"
"v_mfma_f32_16x16x32_bf16 v[4:7], v[224:227], a[156:159], v[4:7]\n"
"ds_read_b64_tr_b16 v[180:181], v230 offset:0\n"
"s_cbranch_vccnz LGQ_%=\nLGQK_%=:\n"
"v_mfma_f32_16x16x32_bf16 v[20:23], v[224:227], a[172:175], v[20:23]\n"
"s_bfe_u32 s20, s89, 65536\ns_lshl_b32 s20, s20, 13\ns_add_u32 s20, s20, 0\n"
"ds_read_b64_tr_b16 v[214:215], v234 offset:18432\n"
"v_and_b32 v85, s73, v85\n"
"v_mfma_f32_16x16x32_bf16 v[20:23], v[120:123], a[168:171], v[20:23]\n"
"buffer_load_dwordx4 v[124:127], v236, s[32:35], s20 offen sc1\n"
"v_mfma_f32_16x16x32_bf16 v[20:23], v[116:119], a[164:167], v[20:23]\n"
"v_and_b32 v90, s73, v90\n"
"v_add_f32 v37, v37, v90\n"
"v_mfma_f32_16x16x32_bf16 v[20:23], v[112:115], a[160:163], v[20:23]\n"
"s_bfe_u32 s20, s89, 65540\ns_lshl_b32 s20, s20, 13\ns_add_u32 s20, s20, 1048576\n"
"v_add_f32 v36, v36, v202\n"
"v_add_f32 v38, v38, v203\n"
"v_mfma_f32_16x16x32_bf16 v[20:23], v[108:111], a[124:127], v[20:23]\n"
"buffer_load_dwordx4 v[128:131], v236, s[32:35], s20 offen sc1\n"
"v_mfma_f32_16x16x32_bf16 v[20:23], v[104:107], a[120:123], v[20:23]\n"
"v_add_u32 v160, -1, v160\n"
"v_add_f32 v39, v39, v91\n"
"v_mfma_f32_16x16x32_bf16 v[20:23], v[100:103], a[116:119], v[20:23]\n"
"s_bfe_u32 s20, s89, 65544\ns_lshl_b32 s20, s20, 13\ns_add_u32 s20, s20, 2097152\n"
"v_add_f32 v32, v32, v206\n"
"v_mfma_f32_16x16x32_bf16 v[20:23], v[96:99], a[112:115], v[20:23]\n"
"buffer_load_dwordx4 v[132:135], v236, s[32:35], s20 offen sc1\n"
"v_mfma_f32_16x16x32_bf16 v[36:39], v[96:99], a[128:131], v[36:39]\n"
"v_lshlrev_b32 v204, 16, v92\n"
"v_add_f32 v40, v40, v204\n"
"v_mfma_f32_16x16x32_bf16 v[36:39], v[100:103], a[132:135], v[36:39]\n"
"s_bfe_u32 s20, s89, 65548\ns_lshl_b32 s20, s20, 13\ns_add_u32 s20, s20, 3145728\n"
"ds_read_b64_tr_b16 v[212:213], v234 offset:16384\n"
"v_add_f32 v30, v30, v205\n"
"v_mfma_f32_16x16x32_bf16 v[36:39], v[104:107], a[136:139], v[36:39]\n"
"buffer_load_dwordx4 v[136:139], v236, s[32:35], s20 offen sc1\n"
"v_mfma_f32_16x16x32_bf16 v[36:39], v[108:111], a[140:143], v[36:39]\n"
"v_and_b32 v92, s73, v92\n"
"v_lshlrev_b32 v206, 16, v94\n"
"v_mfma_f32_16x16x32_bf16 v[36:39], v[112:115], a[176:179], v[36:39]\n"
"s_bfe_u32 s20, s89, 65552\ns_lshl_b32 s20, s20, 13\ns_add_u32 s20, s20, 4194304\n"
"ds_read_b64_tr_b16 v[186:187], v234 offset:2048\n"
"ds_read_b64_tr_b16 v[192:193], v234 offset:8192\n"
"v_mfma_f32_16x16x32_bf16 v[36:39], v[116:119], a[180:183], v[36:39]\n"
"buffer_load_dwordx4 v[140:143], v236, s[32:35], s20 offen sc1\n"
"v_mfma_f32_16x16x32_bf16 v[36:39], v[120:123], a[184:187], v[36:39]\n"
"v_and_b32 v94, s73, v94\n"
"v_mfma_f32_16x16x32_bf16 v[36:39], v[224:227], a[188:191], v[36:39]\n"
"s_bfe_u32 s20, s89, 65556\ns_lshl_b32 s20, s20, 13\ns_add_u32 s20, s20, 5242880\n"
"v_add_f32 v34, v34, v207\n"
"v_lshlrev_b32 v205, 16, v93\n"
"v_mfma_f32_16x16x32_bf16 v[52:55], v[224:227], a[252:255], v[52:55]\n"
"buffer_load_dwordx4 v[144:147], v236, s[32:35], s20 offen sc1\n"
"v_mfma_f32_16x16x32_bf16 v[52:55], v[120:123], a[248:251], v[52:55]\n"
"v_ashrrev_i32 v160, 31, v160\n"
"v_add_f32 v44, v44, v206\n"
"v_mfma_f32_16x16x32_bf16 v[52:55], v[116:119], a[244:247], v[52:55]\n"
"s_bfe_u32 s20, s89, 65560\ns_lshl_b32 s20, s20, 13\ns_add_u32 s20, s20, 6291456\n"
"v_xor_b32 v231, s75, v231\n"
"v_lshlrev_b32 v207, 16, v95\n"
"v_mfma_f32_16x16x32_bf16 v[52:55], v[112:115], a[240:243], v[52:55]\n"
"buffer_load_dwordx4 v[148:151], v236, s[32:35], s20 offen sc1\n"
"v_mfma_f32_16x16x32_bf16 v[52:55], v[108:111], a[204:207], v[52:55]\n"
"s_bfe_u32 s20, s89, 65564\ns_lshl_b32 s20, s20, 13\ns_add_u32 s20, s20, 7340032\n"
"v_and_b32 v87, s73, v87\n"
"v_and_b32 v95, s73, v95\n"
"v_mfma_f32_16x16x32_bf16 v[52:55], v[104:107], a[200:203], v[52:55]\n"
"buffer_load_dwordx4 v[152:155], v236, s[32:35], s20 offen sc1\n"
"v_mfma_f32_16x16x32_bf16 v[52:55], v[100:103], a[196:199], v[52:55]\n"
"s_waitcnt vmcnt(8)\n"
"s_add_u32 s21, s21, 1\n"
"v_and_b32 v88, s73, v88\n"
"v_mfma_f32_16x16x32_bf16 v[52:55], v[96:99], a[192:195], v[52:55]\n"
"s_barrier\n"
"s_mov_b32 s52, 40960\n"
"buffer_store_dword v241, v248, s[84:87], 0 offen sc1\n"
"s_waitcnt lgkmcnt(0)\n"
"v_mfma_f32_16x16x32_bf16 v[8:11], v[180:183], a[96:99], v[8:11]\n"
"s_mov_b32 s53, 81920\n"
"ds_read_b64_tr_b16 v[98:99], v231 offset:2048\n"
"v_add_f32 v42, v42, v205\n"
"v_mfma_f32_16x16x32_bf16 v[8:11], v[184:187], a[100:103], v[8:11]\n"
"ds_read_b64_tr_b16 v[112:113], v231 offset:16384\n"
"v_add_f32 v31, v31, v87\n"
"v_mfma_f32_16x16x32_bf16 v[8:11], v[188:191], a[104:107], v[8:11]\n"
"v_add_f32 v29, v29, v86\n"
"v_and_b32 v161, 16, v242\n"
"v_mfma_f32_16x16x32_bf16 v[8:11], v[192:195], a[108:111], v[8:11]\n"
"ds_read_b64_tr_b16 v[118:119], v235 offset:18432\n"
"v_and_b32 v160, 0x3f80, v160\n"
"v_mfma_f32_16x16x32_bf16 v[8:11], v[208:211], a[144:147], v[8:11]\n"
"ds_read_b64_tr_b16 v[102:103], v235 offset:2048\n"
"v_add_f32 v27, v27, v85\n"
"v_mfma_f32_16x16x32_bf16 v[8:11], v[212:215], a[148:151], v[8:11]\n"
"ds_read_b64_tr_b16 v[106:107], v231 offset:10240\n"
"v_lshlrev_b32 v160, v161, v160\n"
"v_mfma_f32_16x16x32_bf16 v[8:11], v[216:219], a[152:155], v[8:11]\n"
"ds_read_b64_tr_b16 v[96:97], v231 offset:0\n"
"v_and_b32 v93, s73, v93\n"
"v_mfma_f32_16x16x32_bf16 v[8:11], v[220:223], a[156:159], v[8:11]\n"
"ds_read_b64_tr_b16 v[108:109], v235 offset:8192\n"
"v_add_f32 v41, v41, v92\n"
"v_mfma_f32_16x16x32_bf16 v[24:27], v[220:223], a[172:175], v[24:27]\n"
"v_bfe_i32 v161, v242, 5, 1\n"
"v_lshlrev_b32 v206, 16, v244\n"
"v_mfma_f32_16x16x32_bf16 v[24:27], v[216:219], a[168:171], v[24:27]\n"
"ds_read_b64_tr_b16 v[114:115], v231 offset:18432\n"
"v_mfma_f32_16x16x32_bf16 v[24:27], v[212:215], a[164:167], v[24:27]\n"
"ds_read_b64_tr_b16 v[104:105], v231 offset:8192\n"
"v_lshlrev_b32 v203, 16, v247\n"
"v_mfma_f32_16x16x32_bf16 v[24:27], v[208:211], a[160:163], v[24:27]\n"
"ds_read_b64_tr_b16 v[116:117], v235 offset:16384\n"
"v_add_f32 v48, v48, v206\n"
"v_mfma_f32_16x16x32_bf16 v[24:27], v[192:195], a[124:127], v[24:27]\n"
"ds_read_b64_tr_b16 v[110:111], v235 offset:10240\n"
"v_and_b32 v89, s73, v89\n"
"v_mfma_f32_16x16x32_bf16 v[24:27], v[188:191], a[120:123], v[24:27]\n"
"v_add_f32 v45, v45, v94\n"
"v_add_f32 v47, v47, v95\n"
"v_mfma_f32_16x16x32_bf16 v[24:27], v[184:187], a[116:119], v[24:27]\n"
"ds_read_b64_tr_b16 v[100:101], v235 offset:0\n"
"v_add_f32 v43, v43, v93\n"
"v_mfma_f32_16x16x32_bf16 v[24:27], v[180:183], a[112:115], v[24:27]\n"
"ds_read_b64_tr_b16 v[122:123], v231 offset:26624\n"
"v_add_f32 v46, v46, v207\n"
"v_mfma_f32_16x16x32_bf16 v[40:43], v[180:183], a[128:131], v[40:43]\n"
"v_add_f32 v33, v33, v88\n"
"v_and_b32 v244, s73, v244\n"
"v_mfma_f32_16x16x32_bf16 v[40:43], v[184:187], a[132:135], v[40:43]\n"
"ds_read_b64_tr_b16 v[120:121], v231 offset:24576\n"
"v_lshlrev_b32 v207, 16, v245\n"
"v_mfma_f32_16x16x32_bf16 v[40:43], v[188:191], a[136:139], v[40:43]\n"
"v_and_b32 v163, v160, v161\n"
"v_lshlrev_b32 v202, 16, v246\n"
"v_mfma_f32_16x16x32_bf16 v[40:43], v[192:195], a[140:143], v[40:43]\n"
"ds_read_b64_tr_b16 v[224:225], v235 offset:24576\n"
"v_and_b32 v246, s73, v246\n"
"v_mfma_f32_16x16x32_bf16 v[40:43], v[208:211], a[176:179], v[40:43]\n"
"v_add_f32 v52, v52, v202\n"
"v_and_b32 v247, s73, v247\n"
"v_mfma_f32_16x16x32_bf16 v[40:43], v[212:215], a[180:183], v[40:43]\n"
"ds_read_b64_tr_b16 v[226:227], v235 offset:26624\n"
"v_and_b32 v245, s73, v245\n"
"v_mfma_f32_16x16x32_bf16 v[40:43], v[216:219], a[184:187], v[40:43]\n"
"v_mov_b32 v161, 0\n"
"v_add_f32 v54, v54, v203\n"
"v_mfma_f32_16x16x32_bf16 v[40:43], v[220:223], a[188:191], v[40:43]\n"
"s_barrier\n"
"s_waitcnt lgkmcnt(3)\n"
"v_mfma_f32_16x16x32_bf16 v[12:15], v[96:99], a[96:99], v[12:15]\n"
"ds_read_b64_tr_b16 v[184:185], v232 offset:32768\n"
"v_add_f32 v49, v49, v244\n"
"v_mfma_f32_16x16x32_bf16 v[12:15], v[100:103], a[100:103], v[12:15]\n"
"ds_read_b64_tr_b16 v[216:217], v228 offset:57344\n"
"v_mfma_f32_16x16x32_bf16 v[12:15], v[104:107], a[104:107], v[12:15]\n"
"ds_read_b64_tr_b16 v[188:189], v228 offset:40960\n"
"v_add_f32 v35, v35, v89\n"
"v_mfma_f32_16x16x32_bf16 v[12:15], v[108:111], a[108:111], v[12:15]\n"
"ds_read_b64_tr_b16 v[190:191], v228 offset:43008\n"
"v_mfma_f32_16x16x32_bf16 v[12:15], v[112:115], a[144:147], v[12:15]\n"
"ds_read_b64_tr_b16 v[192:193], v232 offset:40960\n"
"v_add_f32 v53, v53, v246\n"
"v_mfma_f32_16x16x32_bf16 v[12:15], v[116:119], a[148:151], v[12:15]\n"
"s_waitcnt lgkmcnt(5)\n"
"ds_read_b64_tr_b16 v[212:213], v232 offset:49152\n"
"v_add_f32 v51, v51, v245\n"
"v_mfma_f32_16x16x32_bf16 v[12:15], v[120:123], a[152:155], v[12:15]\n"
"ds_read_b64_tr_b16 v[182:183], v228 offset:34816\n"
"v_xor_b32 v162, v160, v163\n"
"v_mfma_f32_16x16x32_bf16 v[12:15], v[224:227], a[156:159], v[12:15]\n"
"global_load_dword v200, v201, s[6:7] sc1\n"
"v_mfma_f32_16x16x32_bf16 v[28:31], v[224:227], a[172:175], v[28:31]\n"
"v_add_f32 v50, v50, v207\n"
"v_add_f32 v55, v55, v247\n"
"v_mfma_f32_16x16x32_bf16 v[28:31], v[120:123], a[168:171], v[28:31]\n"
"s_waitcnt vmcnt(9)\n"
"v_lshlrev_b32 v58, 16, v125\n"
"v_lshlrev_b32 v62, 16, v127\n"
"v_mfma_f32_16x16x32_bf16 v[28:31], v[116:119], a[164:167], v[28:31]\n"
"ds_read_b64_tr_b16 v[186:187], v232 offset:34816\n"
"ds_read_b64_tr_b16 v[214:215], v232 offset:51200\n"
"v_mfma_f32_16x16x32_bf16 v[28:31], v[112:115], a[160:163], v[28:31]\n"
"ds_read_b64_tr_b16 v[194:195], v232 offset:43008\n"
"v_and_b32 v61, s73, v126\n"
"v_mfma_f32_16x16x32_bf16 v[28:31], v[108:111], a[124:127], v[28:31]\n"
"s_waitcnt vmcnt(8)\n"
"ds_read_b64_tr_b16 v[210:211], v228 offset:51200\n"
"v_and_b32 v57, s73, v124\n"
"v_mfma_f32_16x16x32_bf16 v[28:31], v[104:107], a[120:123], v[28:31]\n"
"ds_read_b64_tr_b16 v[222:223], v232 offset:59392\n"
"v_lshlrev_b32 v56, 16, v124\n"
"v_mfma_f32_16x16x32_bf16 v[28:31], v[100:103], a[116:119], v[28:31]\n"
"ds_read_b64_tr_b16 v[180:181], v228 offset:32768\n"
"v_lshlrev_b32 v205, 16, v129\n"
"v_mfma_f32_16x16x32_bf16 v[28:31], v[96:99], a[112:115], v[28:31]\n"
"s_waitcnt vmcnt(7)\n"
"v_and_b32 v59, s73, v125\n"
"v_lshlrev_b32 v206, 16, v130\n"
"v_mfma_f32_16x16x32_bf16 v[44:47], v[96:99], a[128:131], v[44:47]\n"
"ds_read_b64_tr_b16 v[220:221], v232 offset:57344\n"
"v_lshlrev_b32 v204, 16, v128\n"
"v_mfma_f32_16x16x32_bf16 v[44:47], v[100:103], a[132:135], v[44:47]\n"
"ds_read_b64_tr_b16 v[208:209], v228 offset:49152\n"
"v_and_b32 v130, s73, v130\n"
"v_mfma_f32_16x16x32_bf16 v[44:47], v[104:107], a[136:139], v[44:47]\n"
"s_waitcnt vmcnt(6)\n"
"ds_read_b64_tr_b16 v[102:103], v233 offset:34816\n"
"v_add_f32 v58, v58, v205\n"
"v_mfma_f32_16x16x32_bf16 v[44:47], v[108:111], a[140:143], v[44:47]\n"
"v_lshlrev_b32 v207, 16, v131\n"
"v_add_f32 v62, v62, v207\n"
"v_mfma_f32_16x16x32_bf16 v[44:47], v[112:115], a[176:179], v[44:47]\n"
"ds_read_b64_tr_b16 v[218:219], v228 offset:59392\n"
"v_and_b32 v131, s73, v131\n"
"v_mfma_f32_16x16x32_bf16 v[44:47], v[116:119], a[180:183], v[44:47]\n"
"s_waitcnt vmcnt(5)\n"
"ds_read_b64_tr_b16 v[96:97], v229 offset:32768\n"
"v_lshlrev_b32 v205, 16, v135\n"
"v_mfma_f32_16x16x32_bf16 v[44:47], v[120:123], a[184:187], v[44:47]\n"
"v_lshlrev_b32 v202, 16, v132\n"
"v_lshlrev_b32 v207, 16, v137\n"
"v_mfma_f32_16x16x32_bf16 v[44:47], v[224:227], a[188:191], v[44:47]\n"
"s_waitcnt lgkmcnt(5)\n"
"ds_read_b64_tr_b16 v[120:121], v229 offset:57344\n"
"v_add_f32 v56, v56, v204\n"
"v_mfma_f32_16x16x32_bf16 v[0:3], v[180:183], a[48:51], v[0:3]\n"
"s_waitcnt vmcnt(4)\n"
"ds_read_b64_tr_b16 v[116:117], v233 offset:49152\n"
"v_add_f32 v61, v61, v130\n"
"v_mfma_f32_16x16x32_bf16 v[0:3], v[184:187], a[52:55], v[0:3]\n"
"v_lshlrev_b32 v60, 16, v126\n"
"v_lshlrev_b32 v204, 16, v134\n"
"v_mfma_f32_16x16x32_bf16 v[0:3], v[188:191], a[56:59], v[0:3]\n"
"ds_read_b64_tr_b16 v[110:111], v233 offset:43008\n"
"v_add_f32 v56, v56, v202\n"
"v_mfma_f32_16x16x32_bf16 v[0:3], v[192:195], a[60:63], v[0:3]\n"
"s_waitcnt lgkmcnt(5)\n"
"s_waitcnt vmcnt(3)\n"
"ds_read_b64_tr_b16 v[112:113], v229 offset:49152\n"
"v_and_b32 v63, s73, v127\n"
"v_mfma_f32_16x16x32_bf16 v[0:3], v[208:211], a[0:3], v[0:3]\n"
"ds_read_b64_tr_b16 v[224:225], v233 offset:57344\n"
"v_and_b32 v134, s73, v134\n"
"v_mfma_f32_16x16x32_bf16 v[0:3], v[212:215], a[4:7], v[0:3]\n"
"s_waitcnt lgkmcnt(5)\n"
"s_waitcnt vmcnt(2)\n"
"ds_read_b64_tr_b16 v[108:109], v233 offset:40960\n"
"v_and_b32 v129, s73, v129\n"
"v_mfma_f32_16x16x32_bf16 v[0:3], v[216:219], a[8:11], v[0:3]\n"
"v_add_f32 v60, v60, v206\n"
"v_add_f32 v60, v60, v204\n"
"v_mfma_f32_16x16x32_bf16 v[0:3], v[220:223], a[12:15], v[0:3]\n"
"ds_read_b64_tr_b16 v[98:99], v229 offset:34816\n"
"ds_read_b64_tr_b16 v[122:123], v229 offset:59392\n"
"v_mfma_f32_16x16x32_bf16 v[16:19], v[220:223], a[28:31], v[16:19]\n"
"v_mov_b32 v165, 0\n"
"v_add_f32 v59, v59, v129\n"
"v_mfma_f32_16x16x32_bf16 v[16:19], v[216:219], a[24:27], v[16:19]\n"
"v_add_f32 v62, v62, v205\n"
"v_lshlrev_b32 v206, 16, v136\n"
"v_mfma_f32_16x16x32_bf16 v[16:19], v[212:215], a[20:23], v[16:19]\n"
"ds_read_b64_tr_b16 v[104:105], v229 offset:40960\n"
"ds_read_b64_tr_b16 v[226:227], v233 offset:59392\n"
"v_mfma_f32_16x16x32_bf16 v[16:19], v[208:211], a[16:19], v[16:19]\n"
"v_add_f32 v56, v56, v206\n"
"v_lshlrev_b32 v202, 16, v138\n"
"v_mfma_f32_16x16x32_bf16 v[16:19], v[192:195], a[76:79], v[16:19]\n"
"s_waitcnt vmcnt(0)\n"
"ds_read_b64_tr_b16 v[118:119], v233 offset:51200\n"
"v_subrev_f32 v2, s22, v2\n"
"v_mfma_f32_16x16x32_bf16 v[16:19], v[188:191], a[72:75], v[16:19]\n"
"v_subrev_u32 v200, s21, v200\n"
"ds_read_b64_tr_b16 v[114:115], v229 offset:51200\n"
"v_mfma_f32_16x16x32_bf16 v[16:19], v[184:187], a[68:71], v[16:19]\n"
"v_cmp_lt_u32 vcc, s69, v200\n"
"ds_read_b64_tr_b16 v[100:101], v233 offset:32768\n"
"v_mfma_f32_16x16x32_bf16 v[16:19], v[180:183], a[64:67], v[16:19]\n"
"v_lshlrev_b32 v206, 16, v142\n"
"s_cbranch_vccnz LGR_%=\nLGRK_%=:\n"
"v_mfma_f32_16x16x32_bf16 v[32:35], v[180:183], a[80:83], v[32:35]\n"
"v_subrev_f32 v3, s22, v3\n"
"s_cmp_eq_u32 s93, 0\ns_cbranch_scc1 LEP_%=\nLEPK_%=:\n"
"v_mfma_f32_16x16x32_bf16 v[32:35], v[184:187], a[84:87], v[32:35]\n"
"s_bfe_u32 s20, s89, 65536\ns_lshl_b32 s20, s20, 13\ns_add_u32 s20, s20, 65536\n"
"ds_read_b64_tr_b16 v[106:107], v229 offset:43008\n"
"v_and_b32 v128, s73, v128\n"
"v_mfma_f32_16x16x32_bf16 v[32:35], v[188:191], a[88:91], v[32:35]\n"
"buffer_load_dwordx4 v[72:75], v236, s[32:35], s20 offen sc1\n"
"v_mfma_f32_16x16x32_bf16 v[32:35], v[192:195], a[92:95], v[32:35]\n"
"v_lshlrev_b32 v203, 16, v133\n"
"v_lshlrev_b32 v205, 16, v141\n"
"v_mfma_f32_16x16x32_bf16 v[32:35], v[208:211], a[32:35], v[32:35]\n"
"s_bfe_u32 s20, s89, 65540\ns_lshl_b32 s20, s20, 13\ns_add_u32 s20, s20, 1114112\n"
"v_lshlrev_b32 v204, 16, v140\n"
"v_add_f32 v56, v56, v204\n"
"v_mfma_f32_16x16x32_bf16 v[32:35], v[212:215], a[36:39], v[32:35]\n"
"buffer_load_dwordx4 v[76:79], v236, s[32:35], s20 offen sc1\n"
"v_mfma_f32_16x16x32_bf16 v[32:35], v[216:219], a[40:43], v[32:35]\n"
"v_and_b32 v136, s73, v136\n"
"v_subrev_f32 v17, s22, v17\n"
"v_mfma_f32_16x16x32_bf16 v[32:35], v[220:223], a[44:47], v[32:35]\n"
"s_bfe_u32 s20, s89, 65544\ns_lshl_b32 s20, s20, 13\ns_add_u32 s20, s20, 2162688\n"
"v_add_f32 v61, v61, v134\n"
"v_and_b32 v141, s73, v141\n"
"v_mfma_f32_16x16x32_bf16 v[48:51], v[220:223], a[228:231], v[48:51]\n"
"buffer_load_dwordx4 v[80:83], v236, s[32:35], s20 offen sc1\n"
"v_mfma_f32_16x16x32_bf16 v[48:51], v[216:219], a[224:227], v[48:51]\n"
"v_add_f32 v58, v58, v203\n"
"v_subrev_f32 v19, s22, v19\n"
"v_mfma_f32_16x16x32_bf16 v[48:51], v[212:215], a[212:215], v[48:51]\n"
"s_bfe_u32 s20, s89, 65548\ns_lshl_b32 s20, s20, 13\ns_add_u32 s20, s20, 3211264\n"
"v_and_b32 v140, s73, v140\n"
"v_lshlrev_b32 v204, 16, v146\n"
"v_mfma_f32_16x16x32_bf16 v[48:51], v[208:211], a[208:211], v[48:51]\n"
"buffer_load_dwordx4 v[84:87], v236, s[32:35], s20 offen sc1\n"
"v_mfma_f32_16x16x32_bf16 v[48:51], v[192:195], a[236:239], v[48:51]\n"
"v_add_f32 v57, v57, v128\n"
"v_subrev_f32 v34, s22, v34\n"
"v_mfma_f32_16x16x32_bf16 v[48:51], v[188:191], a[232:235], v[48:51]\n"
"s_bfe_u32 s20, s89, 65552\ns_lshl_b32 s20, s20, 13\ns_add_u32 s20, s20, 4259840\n"
"ds_read_b64_tr_b16 v[214:215], v234 offset:51200\n"
"v_and_b32 v132, s73, v132\n"
"v_mfma_f32_16x16x32_bf16 v[48:51], v[184:187], a[220:223], v[48:51]\n"
"buffer_load_dwordx4 v[88:91], v236, s[32:35], s20 offen sc1\n"
"v_mfma_f32_16x16x32_bf16 v[48:51], v[180:183], a[216:219], v[48:51]\n"
"s_waitcnt lgkmcnt(1)\n"
"v_add_f32 v60, v60, v202\n"
"v_add_f32 v60, v60, v206\n"
"v_mfma_f32_16x16x32_bf16 v[4:7], v[96:99], a[48:51], v[4:7]\n"
"s_bfe_u32 s20, s89, 65556\ns_lshl_b32 s20, s20, 13\ns_add_u32 s20, s20, 5308416\n"
"v_lshlrev_b32 v206, 16, v148\n"
"v_subrev_f32 v35, s22, v35\n"
"v_mfma_f32_16x16x32_bf16 v[4:7], v[100:103], a[52:55], v[4:7]\n"
"buffer_load_dwordx4 v[92:95], v236, s[32:35], s20 offen sc1\n"
"v_mfma_f32_16x16x32_bf16 v[4:7], v[104:107], a[56:59], v[4:7]\n"
"v_add_f32 v60, v60, v204\n"
"v_subrev_f32 v0, s22, v0\n"
"v_mfma_f32_16x16x32_bf16 v[4:7], v[108:111], a[60:63], v[4:7]\n"
"s_bfe_u32 s20, s89, 65560\ns_lshl_b32 s20, s20, 13\ns_add_u32 s20, s20, 6356992\n"
"v_add_f32 v57, v57, v132\n"
"v_subrev_f32 v33, s22, v33\n"
"v_mfma_f32_16x16x32_bf16 v[4:7], v[112:115], a[0:3], v[4:7]\n"
"buffer_load_dwordx4 v[244:247], v236, s[32:35], s20 offen sc1\n"
"v_mfma_f32_16x16x32_bf16 v[4:7], v[116:119], a[4:7], v[4:7]\n"
"v_and_b32 v138, s73, v138\n"
"v_subrev_f32 v18, s22, v18\n"
"v_mfma_f32_16x16x32_bf16 v[4:7], v[120:123], a[8:11], v[4:7]\n"
"s_bfe_u32 s20, s89, 65564\ns_lshl_b32 s20, s20, 13\ns_add_u32 s20, s20, 7405568\n"
"v_and_b32 v135, s73, v135\n"
"v_subrev_f32 v49, s22, v49\n"
"v_mfma_f32_16x16x32_bf16 v[4:7], v[224:227], a[12:15], v[4:7]\n"
"buffer_load_dwordx4 v[156:159], v236, s[32:35], s20 offen sc1\n"
"v_add_f32 v58, v58, v207\n"
"v_lshlrev_b32 v203, 16, v139\n"
"v_add_f32 v58, v58, v205\n"
"v_and_b32 v142, s73, v142\n"
"v_lshlrev_b32 v202, 16, v144\n"
"v_add_f32 v56, v56, v202\n"
"v_add_f32 v56, v56, v206\n"
"v_subrev_f32 v1, s22, v1\n"
"v_mfma_f32_16x16x32_bf16 v[20:23], v[224:227], a[28:31], v[20:23]\n"
"v_add_f32 v63, v63, v131\n"
"v_lshlrev_b32 v204, 16, v152\n"
"v_mfma_f32_16x16x32_bf16 v[20:23], v[120:123], a[24:27], v[20:23]\n"
"v_and_b32 v133, s73, v133\n"
"v_add_f32 v59, v59, v133\n"
"v_mfma_f32_16x16x32_bf16 v[20:23], v[116:119], a[20:23], v[20:23]\n"
"ds_read_b64_tr_b16 v[218:219], v230 offset:59392\n"
"v_and_b32 v137, s73, v137\n"
"v_mfma_f32_16x16x32_bf16 v[20:23], v[112:115], a[16:19], v[20:23]\n"
"v_and_b32 v148, s73, v148\n"
"v_subrev_f32 v16, s22, v16\n"
"v_mfma_f32_16x16x32_bf16 v[20:23], v[108:111], a[76:79], v[20:23]\n"
"v_add_f32 v62, v62, v203\n"
"v_subrev_f32 v6, s22, v6\n"
"v_mfma_f32_16x16x32_bf16 v[20:23], v[104:107], a[72:75], v[20:23]\n"
"v_add_f32 v57, v57, v136\n"
"v_lshlrev_b32 v205, 16, v147\n"
"v_mfma_f32_16x16x32_bf16 v[20:23], v[100:103], a[68:71], v[20:23]\n"
"ds_read_b64_tr_b16 v[194:195], v234 offset:43008\n"
"v_subrev_f32 v32, s22, v32\n"
"v_mfma_f32_16x16x32_bf16 v[20:23], v[96:99], a[64:67], v[20:23]\n"
"ds_read_b64_tr_b16 v[184:185], v234 offset:32768\n"
"v_add_f32 v59, v59, v137\n"
"v_and_b32 v139, s73, v139\n"
"v_add_f32 v59, v59, v141\n"
"v_lshlrev_b32 v207, 16, v143\n"
"v_lshlrev_b32 v203, 16, v145\n"
"v_and_b32 v145, s73, v145\n"
"v_add_f32 v58, v58, v203\n"
"v_add_f32 v59, v59, v145\n"
"v_and_b32 v147, s73, v147\n"
"v_lshlrev_b32 v203, 16, v151\n"
"v_subrev_f32 v4, s22, v4\n"
"v_subrev_f32 v5, s22, v5\n"
"v_subrev_f32 v7, s22, v7\n"
"v_cvt_pk_bf16_f32 v0, v0, v1\n"
"v_cvt_pk_bf16_f32 v1, v2, v3\n"
"v_cvt_pk_bf16_f32 v3, v6, v7\n"
"v_mfma_f32_16x16x32_bf16 v[36:39], v[96:99], a[80:83], v[36:39]\n"
"v_add_f32 v56, v56, v204\n"
"v_cvt_pk_bf16_f32 v2, v4, v5\n"
"v_mfma_f32_16x16x32_bf16 v[36:39], v[100:103], a[84:87], v[36:39]\n"
"ds_read_b64_tr_b16 v[192:193], v234 offset:40960\n"
"v_add_f32 v61, v61, v138\n"
"v_mfma_f32_16x16x32_bf16 v[36:39], v[104:107], a[88:91], v[36:39]\n"
"v_add_f32 v57, v57, v140\n"
"v_subrev_f32 v23, s22, v23\n"
"v_mfma_f32_16x16x32_bf16 v[36:39], v[108:111], a[92:95], v[36:39]\n"
"ds_read_b64_tr_b16 v[180:181], v230 offset:32768\n"
"v_and_b32 v144, s73, v144\n"
"v_mfma_f32_16x16x32_bf16 v[36:39], v[112:115], a[32:35], v[36:39]\n"
"ds_read_b64_tr_b16 v[210:211], v230 offset:51200\n"
"v_add_f32 v62, v62, v207\n"
"v_add_f32 v62, v62, v205\n"
"v_lshlrev_b32 v207, 16, v149\n"
"v_add_f32 v58, v58, v207\n"
"v_lshlrev_b32 v202, 16, v150\n"
"v_permlane16_swap_b32 v0, v2\n"
"v_permlane16_swap_b32 v1, v3\n"
"v_subrev_f32 v20, s22, v20\n"
"v_cvt_pk_bf16_f32 v16, v16, v17\n"
"v_mfma_f32_16x16x32_bf16 v[36:39], v[116:119], a[36:39], v[36:39]\n"
"ds_read_b64_tr_b16 v[186:187], v234 offset:34816\n"
"v_add_f32 v63, v63, v135\n"
"v_mfma_f32_16x16x32_bf16 v[36:39], v[120:123], a[40:43], v[36:39]\n"
"ds_read_b64_tr_b16 v[188:189], v230 offset:40960\n"
"buffer_store_dwordx4 v[0:3], v238, s[36:39], 0 offen sc0 sc1\n"
"v_and_b32 v143, s73, v143\n"
"v_and_b32 v146, s73, v146\n"
"v_and_b32 v149, s73, v149\n"
"v_lshlrev_b32 v205, 16, v153\n"
"v_and_b32 v153, s73, v153\n"
"v_lshlrev_b32 v206, 16, v154\n"
"v_lshlrev_b32 v207, 16, v155\n"
"v_subrev_f32 v21, s22, v21\n"
"v_subrev_f32 v22, s22, v22\n"
"v_cvt_pk_bf16_f32 v17, v18, v19\n"
"v_cvt_pk_bf16_f32 v18, v20, v21\n"
"v_cvt_pk_bf16_f32 v32, v32, v33\n"
"v_mfma_f32_16x16x32_bf16 v[36:39], v[224:227], a[44:47], v[36:39]\n"
"ds_read_b64_tr_b16 v[182:183], v230 offset:34816\n"
"v_add_f32 v63, v63, v139\n"
"v_add_f32 v61, v61, v142\n"
"v_add_f32 v57, v57, v144\n"
"v_add_f32 v57, v57, v148\n"
"v_add_f32 v59, v59, v149\n"
"v_cvt_pk_bf16_f32 v19, v22, v23\n"
"v_permlane16_swap_b32 v16, v18\n"
"v_cvt_pk_bf16_f32 v33, v34, v35\n"
"v_mfma_f32_16x16x32_bf16 v[52:55], v[224:227], a[228:231], v[52:55]\n"
"ds_read_b64_tr_b16 v[190:191], v230 offset:43008\n"
"v_add_f32 v59, v59, v153\n"
"v_mfma_f32_16x16x32_bf16 v[52:55], v[120:123], a[224:227], v[52:55]\n"
"v_permlane16_swap_b32 v17, v19\n"
"v_mfma_f32_16x16x32_bf16 v[52:55], v[116:119], a[212:215], v[52:55]\n"
"ds_read_b64_tr_b16 v[208:209], v230 offset:49152\n"
"v_subrev_f32 v36, s22, v36\n"
"v_mfma_f32_16x16x32_bf16 v[52:55], v[112:115], a[208:211], v[52:55]\n"
"ds_read_b64_tr_b16 v[222:223], v234 offset:59392\n"
"buffer_store_dwordx4 v[16:19], v238, s[36:39], s52 offen sc0 sc1\n"
"v_add_f32 v63, v63, v143\n"
"v_add_f32 v60, v60, v202\n"
"v_add_f32 v62, v62, v203\n"
"v_and_b32 v152, s73, v152\n"
"v_subrev_f32 v38, s22, v38\n"
"v_subrev_f32 v39, s22, v39\n"
"v_mfma_f32_16x16x32_bf16 v[52:55], v[108:111], a[236:239], v[52:55]\n"
"v_and_b32 v155, s73, v155\n"
"v_cvt_pk_bf16_f32 v35, v38, v39\n"
"v_mfma_f32_16x16x32_bf16 v[52:55], v[104:107], a[232:235], v[52:55]\n"
"ds_read_b64_tr_b16 v[216:217], v230 offset:57344\n"
"v_subrev_f32 v37, s22, v37\n"
"v_mfma_f32_16x16x32_bf16 v[52:55], v[100:103], a[220:223], v[52:55]\n"
"v_and_b32 v150, s73, v150\n"
"v_and_b32 v154, s73, v154\n"
"v_mfma_f32_16x16x32_bf16 v[52:55], v[96:99], a[216:219], v[52:55]\n"
"s_waitcnt lgkmcnt(4)\n"
"ds_read_b64_tr_b16 v[212:213], v234 offset:49152\n"
"ds_read_b64_tr_b16 v[220:221], v234 offset:57344\n"
"v_add_f32 v63, v63, v147\n"
"v_and_b32 v151, s73, v151\n"
"v_add_f32 v63, v63, v151\n"
"v_add_f32 v57, v57, v152\n"
"v_add_f32 v58, v58, v205\n"
"v_add_f32 v60, v60, v206\n"
"v_add_f32 v62, v62, v207\n"
"v_cvt_pk_bf16_f32 v34, v36, v37\n"
"v_permlane16_swap_b32 v33, v35\n"
"v_mfma_f32_16x16x32_bf16 v[8:11], v[180:183], a[48:51], v[8:11]\n"
"v_cvt_pk_bf16_f32 v56, v56, v57\n"
"v_cvt_pk_bf16_f32 v57, v58, v59\n"
"v_mfma_f32_16x16x32_bf16 v[8:11], v[184:187], a[52:55], v[8:11]\n"
"s_waitcnt lgkmcnt(5)\n"
"v_permlane16_swap_b32 v32, v34\n"
"v_mfma_f32_16x16x32_bf16 v[8:11], v[188:191], a[56:59], v[8:11]\n"
"buffer_store_dwordx4 v[32:35], v238, s[36:39], s53 offen sc0 sc1\n"
"ds_read_b64_tr_b16 v[120:121], v231 offset:57344\n"
"v_add_f32 v61, v61, v146\n"
"v_add_f32 v61, v61, v150\n"
"v_add_f32 v61, v61, v154\n"
"v_add_f32 v63, v63, v155\n"
"v_cvt_pk_bf16_f32 v58, v60, v61\n"
"v_cvt_pk_bf16_f32 v59, v62, v63\n"
"v_mfma_f32_16x16x32_bf16 v[8:11], v[192:195], a[60:63], v[8:11]\n"
"s_waitcnt lgkmcnt(4)\n"
"v_permlane16_swap_b32 v57, v59\n"
"v_mfma_f32_16x16x32_bf16 v[8:11], v[208:211], a[0:3], v[8:11]\n"
"s_waitcnt lgkmcnt(2)\n"
"v_permlane16_swap_b32 v56, v58\n"
"v_mfma_f32_16x16x32_bf16 v[8:11], v[212:215], a[4:7], v[8:11]\n"
"buffer_store_dwordx4 v[56:59], v253, s[36:39], 0 offen sc0 sc1\n"
"v_subrev_f32 v48, s22, v48\n"
"v_subrev_f32 v50, s22, v50\n"
"v_subrev_f32 v51, s22, v51\n"
"v_mfma_f32_16x16x32_bf16 v[8:11], v[216:219], a[8:11], v[8:11]\n"
"s_waitcnt lgkmcnt(1)\n"
"ds_read_b64_tr_b16 v[112:113], v231 offset:49152\n"
"v_subrev_f32 v54, s22, v54\n"
"v_mfma_f32_16x16x32_bf16 v[8:11], v[220:223], a[12:15], v[8:11]\n"
"ds_read_b64_tr_b16 v[100:101], v235 offset:32768\n"
"v_subrev_f32 v52, s22, v52\n"
"v_mfma_f32_16x16x32_bf16 v[24:27], v[220:223], a[28:31], v[24:27]\n"
"ds_read_b64_tr_b16 v[102:103], v235 offset:34816\n"
"v_subrev_f32 v53, s22, v53\n"
"v_mfma_f32_16x16x32_bf16 v[24:27], v[216:219], a[24:27], v[24:27]\n"
"v_cvt_pk_bf16_f32 v48, v48, v49\n"
"v_cvt_pk_bf16_f32 v49, v50, v51\n"
"v_mfma_f32_16x16x32_bf16 v[24:27], v[212:215], a[20:23], v[24:27]\n"
"ds_read_b64_tr_b16 v[96:97], v231 offset:32768\n"
"v_cvt_pk_bf16_f32 v50, v52, v53\n"
"v_mfma_f32_16x16x32_bf16 v[24:27], v[208:211], a[16:19], v[24:27]\n"
"s_nop 0\n"
"v_permlane16_swap_b32 v48, v50\n"
"v_mfma_f32_16x16x32_bf16 v[24:27], v[192:195], a[76:79], v[24:27]\n"
"ds_read_b64_tr_b16 v[116:117], v235 offset:49152\n"
"v_subrev_f32 v55, s22, v55\n"
"v_mfma_f32_16x16x32_bf16 v[24:27], v[188:191], a[72:75], v[24:27]\n"
"ds_read_b64_tr_b16 v[98:99], v231 offset:34816\n"
"v_cvt_pk_bf16_f32 v51, v54, v55\n"
"v_mfma_f32_16x16x32_bf16 v[24:27], v[184:187], a[68:71], v[24:27]\n"
"s_nop 0\n"
"v_permlane16_swap_b32 v49, v51\n"
"v_mfma_f32_16x16x32_bf16 v[24:27], v[180:183], a[64:67], v[24:27]\n"
"buffer_store_dwordx4 v[48:51], v252, s[36:39], 0 offen sc0 sc1\n"
"ds_read_b64_tr_b16 v[104:105], v231 offset:40960\n"
"v_mfma_f32_16x16x32_bf16 v[40:43], v[180:183], a[80:83], v[40:43]\n"
"ds_read_b64_tr_b16 v[106:107], v231 offset:43008\n"
"ds_read_b64_tr_b16 v[108:109], v235 offset:40960\n"
"v_mfma_f32_16x16x32_bf16 v[40:43], v[184:187], a[84:87], v[40:43]\n"
"ds_read_b64_tr_b16 v[110:111], v235 offset:43008\n"
"v_mfma_f32_16x16x32_bf16 v[40:43], v[188:191], a[88:91], v[40:43]\n"
"ds_read_b64_tr_b16 v[114:115], v231 offset:51200\n"
"v_subrev_f32 v10, s22, v10\n"
"v_mfma_f32_16x16x32_bf16 v[40:43], v[192:195], a[92:95], v[40:43]\n"
"ds_read_b64_tr_b16 v[118:119], v235 offset:51200\n"
"v_mfma_f32_16x16x32_bf16 v[40:43], v[208:211], a[32:35], v[40:43]\n"
"ds_read_b64_tr_b16 v[122:123], v231 offset:59392\n"
"v_subrev_f32 v24, s22, v24\n"
"v_mfma_f32_16x16x32_bf16 v[40:43], v[212:215], a[36:39], v[40:43]\n"
"ds_read_b64_tr_b16 v[224:225], v235 offset:57344\n"
"ds_read_b64_tr_b16 v[226:227], v235 offset:59392\n"
"v_mfma_f32_16x16x32_bf16 v[40:43], v[216:219], a[40:43], v[40:43]\n"
"v_mfma_f32_16x16x32_bf16 v[40:43], v[220:223], a[44:47], v[40:43]\n"
"s_waitcnt lgkmcnt(8)\n"
"v_mov_b32 v160, 0\n"
"v_subrev_f32 v11, s22, v11\n"
"v_mfma_f32_16x16x32_bf16 v[12:15], v[96:99], a[48:51], v[12:15]\n"
"v_subrev_f32 v8, s22, v8\n"
"v_subrev_f32 v9, s22, v9\n"
"v_mfma_f32_16x16x32_bf16 v[12:15], v[100:103], a[52:55], v[12:15]\n"
"s_waitcnt lgkmcnt(5)\n"
"v_cvt_pk_bf16_f32 v8, v8, v9\n"
"v_mfma_f32_16x16x32_bf16 v[12:15], v[104:107], a[56:59], v[12:15]\n"
"v_subrev_f32 v27, s22, v27\n"
"v_mfma_f32_16x16x32_bf16 v[12:15], v[108:111], a[60:63], v[12:15]\n"
"s_waitcnt lgkmcnt(3)\n"
"v_mfma_f32_16x16x32_bf16 v[12:15], v[112:115], a[0:3], v[12:15]\n"
"v_mfma_f32_16x16x32_bf16 v[12:15], v[116:119], a[4:7], v[12:15]\n"
"s_waitcnt lgkmcnt(0)\n"
"v_mfma_f32_16x16x32_bf16 v[12:15], v[120:123], a[8:11], v[12:15]\n"
"v_subrev_f32 v40, s22, v40\n"
"v_mfma_f32_16x16x32_bf16 v[12:15], v[224:227], a[12:15], v[12:15]\n"
"s_waitcnt vmcnt(5)\n"
"v_mfma_f32_16x16x32_bf16 v[64:67], v[162:165], v[72:75], 0\n"
"v_mfma_f32_16x16x32_bf16 v[64:67], v[162:165], v[76:79], v[64:67]\n"
"v_cvt_pk_bf16_f32 v9, v10, v11\n"
"v_mfma_f32_16x16x32_bf16 v[64:67], v[162:165], v[80:83], v[64:67]\n"
"s_nop 2\n"
"v_subrev_f32 v12, s22, v12\n"
"v_mfma_f32_16x16x32_bf16 v[64:67], v[162:165], v[84:87], v[64:67]\n"
"v_subrev_f32 v25, s22, v25\n"
"v_mfma_f32_16x16x32_bf16 v[64:67], v[162:165], v[88:91], v[64:67]\n"
"v_subrev_f32 v13, s22, v13\n"
"v_mfma_f32_16x16x32_bf16 v[64:67], v[162:165], v[92:95], v[64:67]\n"
"v_subrev_f32 v15, s22, v15\n"
"v_mfma_f32_16x16x32_bf16 v[64:67], v[162:165], v[244:247], v[64:67]\n"
"v_subrev_f32 v14, s22, v14\n"
"v_mfma_f32_16x16x32_bf16 v[64:67], v[162:165], v[156:159], v[64:67]\n"
"v_subrev_f32 v42, s22, v42\n"
"v_mfma_f32_16x16x32_bf16 v[68:71], v[160:163], v[72:75], 0\n"
"v_cvt_pk_bf16_f32 v11, v14, v15\n"
"v_subrev_f32 v41, s22, v41\n"
"v_mfma_f32_16x16x32_bf16 v[68:71], v[160:163], v[76:79], v[68:71]\n"
"v_cvt_pk_bf16_f32 v10, v12, v13\n"
"v_cvt_pk_bf16_f32 v24, v24, v25\n"
"v_mfma_f32_16x16x32_bf16 v[68:71], v[160:163], v[80:83], v[68:71]\n"
"v_mfma_f32_16x16x32_bf16 v[68:71], v[160:163], v[84:87], v[68:71]\n"
"v_permlane16_swap_b32 v9, v11\n"
"v_mfma_f32_16x16x32_bf16 v[68:71], v[160:163], v[88:91], v[68:71]\n"
"v_mfma_f32_16x16x32_bf16 v[68:71], v[160:163], v[92:95], v[68:71]\n"
"v_cvt_pk_bf16_f32 v64, v64, v65\n"
"v_cvt_pk_bf16_f32 v40, v40, v41\n"
"v_mfma_f32_16x16x32_bf16 v[68:71], v[160:163], v[244:247], v[68:71]\n"
"v_permlane16_swap_b32 v8, v10\n"
"v_mfma_f32_16x16x32_bf16 v[68:71], v[160:163], v[156:159], v[68:71]\n"
"buffer_store_dwordx4 v[8:11], v239, s[36:39], 0 offen sc0 sc1\n"
"v_mfma_f32_16x16x32_bf16 v[28:31], v[224:227], a[28:31], v[28:31]\n"
"v_mfma_f32_16x16x32_bf16 v[28:31], v[120:123], a[24:27], v[28:31]\n"
"v_cvt_pk_bf16_f32 v65, v66, v67\n"
"v_subrev_f32 v43, s22, v43\n"
"v_mfma_f32_16x16x32_bf16 v[28:31], v[116:119], a[20:23], v[28:31]\n"
"v_mfma_f32_16x16x32_bf16 v[28:31], v[112:115], a[16:19], v[28:31]\n"
"v_mfma_f32_16x16x32_bf16 v[28:31], v[108:111], a[76:79], v[28:31]\n"
"v_cvt_pk_bf16_f32 v66, v68, v69\n"
"v_cvt_pk_bf16_f32 v67, v70, v71\n"
"v_mfma_f32_16x16x32_bf16 v[28:31], v[104:107], a[72:75], v[28:31]\n"
"v_permlane16_swap_b32 v64, v66\n"
"v_mfma_f32_16x16x32_bf16 v[28:31], v[100:103], a[68:71], v[28:31]\n"
"v_permlane16_swap_b32 v65, v67\n"
"v_mfma_f32_16x16x32_bf16 v[28:31], v[96:99], a[64:67], v[28:31]\n"
"buffer_store_dwordx4 v[64:67], v253, s[36:39], 0 offen offset:128 sc0 sc1\n"
"v_cvt_pk_bf16_f32 v41, v42, v43\n"
"v_mfma_f32_16x16x32_bf16 v[44:47], v[96:99], a[80:83], v[44:47]\n"
"v_subrev_f32 v26, s22, v26\n"
"v_cvt_pk_bf16_f32 v25, v26, v27\n"
"v_mfma_f32_16x16x32_bf16 v[44:47], v[100:103], a[84:87], v[44:47]\n"
"v_mfma_f32_16x16x32_bf16 v[44:47], v[104:107], a[88:91], v[44:47]\n"
"s_nop 0\n"
"v_subrev_f32 v30, s22, v30\n"
"v_subrev_f32 v31, s22, v31\n"
"v_mfma_f32_16x16x32_bf16 v[44:47], v[108:111], a[92:95], v[44:47]\n"
"v_subrev_f32 v28, s22, v28\n"
"v_subrev_f32 v29, s22, v29\n"
"v_mfma_f32_16x16x32_bf16 v[44:47], v[112:115], a[32:35], v[44:47]\n"
"v_cvt_pk_bf16_f32 v26, v28, v29\n"
"v_cvt_pk_bf16_f32 v27, v30, v31\n"
"v_mfma_f32_16x16x32_bf16 v[44:47], v[116:119], a[36:39], v[44:47]\n"
"s_nop 0\n"
"v_permlane16_swap_b32 v25, v27\n"
"v_mfma_f32_16x16x32_bf16 v[44:47], v[120:123], a[40:43], v[44:47]\n"
"v_permlane16_swap_b32 v24, v26\n"
"v_mfma_f32_16x16x32_bf16 v[44:47], v[224:227], a[44:47], v[44:47]\n"
"buffer_store_dwordx4 v[24:27], v239, s[36:39], s52 offen sc0 sc1\n"
"s_nop 6\n"
"v_subrev_f32 v44, s22, v44\n"
"v_subrev_f32 v45, s22, v45\n"
"v_subrev_f32 v46, s22, v46\n"
"v_subrev_f32 v47, s22, v47\n"
"v_cvt_pk_bf16_f32 v42, v44, v45\n"
"v_cvt_pk_bf16_f32 v43, v46, v47\n"
"s_nop 0\n"
"v_permlane16_swap_b32 v40, v42\n"
"v_permlane16_swap_b32 v41, v43\n"
"buffer_store_dwordx4 v[40:43], v239, s[36:39], s53 offen sc0 sc1\n"
"s_branch LFIN_%=\n"
"LSW_%=:\n"
"s_add_u32 s16, s8, s95\n"
"s_addc_u32 s17, s9, 0\n"
"s_sub_u32 s18, 655360, s95\n"
"s_add_u32 s20, s8, s100\n"
"s_addc_u32 s21, s9, 0\n"
"s_sub_u32 s22, 655360, s100\n"
"s_mov_b32 s56, 0\n"
"s_mov_b32 s55, 0\n"
"s_mov_b32 s58, 0\n"
"s_mov_b32 s57, 0\n"
"s_mov_b32 s59, 163840\n"
"s_mov_b32 s52, 9216\n"
"s_mov_b32 s53, 18432\n"
"s_mov_b32 s54, 27648\n"
"v_lshrrev_b32 v206, 7, v242\n"
"v_mul_u32_u24 v206, 1280, v206\n"
"v_lshrrev_b32 v240, 3, v242\n"
"v_and_b32 v240, 0x60, v240\n"
"v_and_b32 v207, 0x70, v242\n"
"v_xor_b32 v240, v240, v207\n"
"v_add_u32 v240, v206, v240\n"
"v_mov_b32 v207, v240\n"
"buffer_load_dword v252, v240, s[16:19], 0 offen\n"
"s_branch LSWK_%=\n"
"LGP_%=:\n"
"s_mov_b32 s80, 0x40000\n"
"LGPa_%=:\n"
"s_sleep 1\n"
"global_load_dword v200, v201, s[6:7] sc1\n"
"s_sub_u32 s80, s80, 1\n"
"s_cmp_eq_u32 s80, 0\n"
"s_waitcnt vmcnt(0)\n"
"s_cbranch_scc1 LGPK_%=\n"
"v_subrev_u32 v200, s21, v200\n"
"v_cmp_lt_u32 vcc, s69, v200\n"
"s_nop 4\n"
"s_cbranch_vccnz LGPa_%=\n"
"s_branch LGPK_%=\n"
"LGQ_%=:\n"
"s_mov_b32 s80, 0x40000\n"
"LGQa_%=:\n"
"s_sleep 1\n"
"global_load_dword v200, v201, s[6:7] sc1\n"
"s_sub_u32 s80, s80, 1\n"
"s_cmp_eq_u32 s80, 0\n"
"s_waitcnt vmcnt(0)\n"
"s_cbranch_scc1 LGQK_%=\n"
"v_subrev_u32 v200, s21, v200\n"
"v_cmp_lt_u32 vcc, s69, v200\n"
"s_nop 4\n"
"s_cbranch_vccnz LGQa_%=\n"
"s_branch LGQK_%=\n"
"LGR_%=:\n"
"s_mov_b32 s80, 0x40000\n"
"LGRa_%=:\n"
"s_sleep 1\n"
"global_load_dword v200, v201, s[6:7] sc1\n"
"s_sub_u32 s80, s80, 1\n"
"s_cmp_eq_u32 s80, 0\n"
"s_waitcnt vmcnt(0)\n"
"s_cbranch_scc1 LGRK_%=\n"
"v_subrev_u32 v200, s21, v200\n"
"v_cmp_lt_u32 vcc, s69, v200\n"
"s_nop 4\n"
"s_cbranch_vccnz LGRa_%=\n"
"s_branch LGRK_%=\n"
"LGV_%=:\n"
"s_mov_b32 s80, 0x40000\n"
"LGVa_%=:\n"
"s_sleep 1\n"
"global_load_dword v200, v201, s[6:7] sc1\n"
"s_sub_u32 s80, s80, 1\n"
"s_cmp_eq_u32 s80, 0\n"
"s_waitcnt vmcnt(0)\n"
"s_cbranch_scc1 LGVK_%=\n"
"v_subrev_u32 v200, s21, v200\n"
"v_cmp_lt_u32 vcc, s69, v200\n"
"s_nop 4\n"
"s_cbranch_vccnz LGVa_%=\n"
"s_branch LGVK_%=\n"
"LEP_%=:\n"
"s_add_u32 s92, s92, 1\n"
"v_mov_b32 v200, s92\n"
"v_mov_b32 v255, 0\n"
"s_mov_b64 exec, 1\n"
"global_store_dword v255, v200, s[6:7] offset:128 sc1\n"
"s_mov_b64 exec, -1\n"
"s_branch LEPK_%=\n"
"LPCE0_%=:\n"
"s_memrealtime s[80:81]\n"
"s_sub_u32 s82, s67, s66\n"
"s_cmp_gt_i32 s82, 0\n"
"s_cselect_b32 s83, s67, s66\n"
"s_waitcnt lgkmcnt(0)\n"
"s_mov_b32 s63, 0\n"
"s_mov_b32 s62, s72\n"
"s_sub_u32 s82, s83, s80\n"
"s_cmp_gt_i32 s82, 3\n"
"s_cbranch_scc0 LPEE0_%=\n"
"LPDE0_%=:\n"
"s_sleep 1\n"
"s_memrealtime s[80:81]\n"
"s_waitcnt lgkmcnt(0)\n"
"s_sub_u32 s82, s83, s80\n"
"s_cmp_gt_i32 s82, 3\n"
"s_cbranch_scc1 LPDE0_%=\n"
"LPEE0_%=:\n"
"s_add_u32 s66, s66, s72\n"
"s_add_u32 s66, s66, 4\n"
"s_add_u32 s67, s80, s72\n"
"s_sub_u32 s72, s72, 4\n"
"s_max_u32 s72, s72, s62\n"
"s_branch LPKE0_%=\n"
"LPCE1_%=:\n"
"s_memrealtime s[80:81]\n"
"s_sub_u32 s82, s67, s66\n"
"s_cmp_gt_i32 s82, 0\n"
"s_cselect_b32 s83, s67, s66\n"
"s_waitcnt lgkmcnt(0)\n"
"s_add_u32 s72, s62, 20\n"
"s_sub_u32 s82, s83, s80\n"
"s_cmp_gt_i32 s82, 3\n"
"s_cbranch_scc0 LPEE1_%=\n"
"LPDE1_%=:\n"
"s_sleep 1\n"
"s_memrealtime s[80:81]\n"
"s_waitcnt lgkmcnt(0)\n"
"s_sub_u32 s82, s83, s80\n"
"s_cmp_gt_i32 s82, 3\n"
"s_cbranch_scc1 LPDE1_%=\n"
"LPEE1_%=:\n"
"s_add_u32 s66, s66, s72\n"
"s_add_u32 s66, s66, 4\n"
"s_add_u32 s67, s80, s72\n"
"s_sub_u32 s72, s72, 4\n"
"s_max_u32 s72, s72, s62\n"
"s_branch LPKE1_%=\n"
"LPCT0_%=:\n"
"s_memrealtime s[80:81]\n"
"s_sub_u32 s82, s67, s66\n"
"s_cmp_gt_i32 s82, 0\n"
"s_cselect_b32 s83, s67, s66\n"
"s_waitcnt lgkmcnt(0)\n"
"s_sub_u32 s82, s83, s80\n"
"s_cmp_gt_i32 s82, 3\n"
"s_cbranch_scc0 LPET0_%=\n"
"LPDT0_%=:\n"
"s_sleep 1\n"
"s_memrealtime s[80:81]\n"
"s_waitcnt lgkmcnt(0)\n"
"s_sub_u32 s82, s83, s80\n"
"s_cmp_gt_i32 s82, 3\n"
"s_cbranch_scc1 LPDT0_%=\n"
"LPET0_%=:\n"
"s_add_u32 s66, s66, s72\n"
"s_add_u32 s66, s66, 4\n"
"s_add_u32 s67, s80, s72\n"
"s_sub_u32 s72, s72, 4\n"
"s_max_u32 s72, s72, s62\n"
"s_branch LPKT0_%=\n"
"LPCT1_%=:\n"
"s_memrealtime s[80:81]\n"
"s_sub_u32 s82, s67, s66\n"
"s_cmp_gt_i32 s82, 0\n"
"s_cselect_b32 s83, s67, s66\n"
"s_waitcnt lgkmcnt(0)\n"
"s_add_u32 s72, s62, 20\n"
"s_sub_u32 s82, s83, s80\n"
"s_cmp_gt_i32 s82, 3\n"
"s_cbranch_scc0 LPET1_%=\n"
"LPDT1_%=:\n"
"s_sleep 1\n"
"s_memrealtime s[80:81]\n"
"s_waitcnt lgkmcnt(0)\n"
"s_sub_u32 s82, s83, s80\n"
"s_cmp_gt_i32 s82, 3\n"
"s_cbranch_scc1 LPDT1_%=\n"
"LPET1_%=:\n"
"s_add_u32 s66, s66, s72\n"
"s_add_u32 s66, s66, 4\n"
"s_add_u32 s67, s80, s72\n"
"s_sub_u32 s72, s72, 4\n"
"s_max_u32 s72, s72, s62\n"
"s_branch LPKT1_%=\n"
"LPCL0_%=:\n"
"s_memrealtime s[80:81]\n"
"s_sub_u32 s82, s67, s66\n"
"s_cmp_gt_i32 s82, 0\n"
"s_cselect_b32 s83, s67, s66\n"
"s_waitcnt lgkmcnt(0)\n"
"s_sub_u32 s82, s83, s80\n"
"s_cmp_gt_i32 s82, 3\n"
"s_cbranch_scc0 LPEL0_%=\n"
"LPDL0_%=:\n"
"s_sleep 1\n"
"s_memrealtime s[80:81]\n"
"s_waitcnt lgkmcnt(0)\n"
"s_sub_u32 s82, s83, s80\n"
"s_cmp_gt_i32 s82, 3\n"
"s_cbranch_scc1 LPDL0_%=\n"
"LPEL0_%=:\n"
"s_add_u32 s66, s66, s72\n"
"s_add_u32 s66, s66, 4\n"
"s_add_u32 s67, s80, s72\n"
"s_sub_u32 s72, s72, 4\n"
"s_max_u32 s72, s72, s62\n"
"s_branch LPKL0_%=\n"
"LPCL1_%=:\n"
"s_memrealtime s[80:81]\n"
"s_sub_u32 s82, s67, s66\n"
"s_cmp_gt_i32 s82, 0\n"
"s_cselect_b32 s83, s67, s66\n"
"s_waitcnt lgkmcnt(0)\n"
"s_sub_u32 s82, s83, s80\n"
"s_cmp_gt_i32 s82, 3\n"
"s_cbranch_scc0 LPEL1_%=\n"
"LPDL1_%=:\n"
"s_sleep 1\n"
"s_memrealtime s[80:81]\n"
"s_waitcnt lgkmcnt(0)\n"
"s_sub_u32 s82, s83, s80\n"
"s_cmp_gt_i32 s82, 3\n"
"s_cbranch_scc1 LPDL1_%=\n"
"LPEL1_%=:\n"
"s_add_u32 s66, s66, s72\n"
"s_add_u32 s66, s66, 4\n"
"s_add_u32 s67, s80, s72\n"
"s_sub_u32 s72, s72, 4\n"
"s_max_u32 s72, s72, s62\n"
"s_branch LPKL1_%=\n"
"LPCL2_%=:\n"
"s_memrealtime s[80:81]\n"
"s_sub_u32 s82, s67, s66\n"
"s_cmp_gt_i32 s82, 0\n"
"s_cselect_b32 s83, s67, s66\n"
"s_waitcnt lgkmcnt(0)\n"
"s_sub_u32 s82, s83, s80\n"
"s_cmp_gt_i32 s82, 3\n"
"s_cbranch_scc0 LPEL2_%=\n"
"LPDL2_%=:\n"
"s_sleep 1\n"
"s_memrealtime s[80:81]\n"
"s_waitcnt lgkmcnt(0)\n"
"s_sub_u32 s82, s83, s80\n"
"s_cmp_gt_i32 s82, 3\n"
"s_cbranch_scc1 LPDL2_%=\n"
"LPEL2_%=:\n"
"s_add_u32 s66, s66, s72\n"
"s_add_u32 s66, s66, 4\n"
"s_add_u32 s67, s80, s72\n"
"s_sub_u32 s72, s72, 4\n"
"s_max_u32 s72, s72, s62\n"
"s_branch LPKL2_%=\n"
"LPCL3_%=:\n"
"s_memrealtime s[80:81]\n"
"s_sub_u32 s82, s67, s66\n"
"s_cmp_gt_i32 s82, 0\n"
"s_cselect_b32 s83, s67, s66\n"
"s_waitcnt lgkmcnt(0)\n"
"s_sub_u32 s82, s83, s80\n"
"s_cmp_gt_i32 s82, 3\n"
"s_cbranch_scc0 LPEL3_%=\n"
"LPDL3_%=:\n"
"s_sleep 1\n"
"s_memrealtime s[80:81]\n"
"s_waitcnt lgkmcnt(0)\n"
"s_sub_u32 s82, s83, s80\n"
"s_cmp_gt_i32 s82, 3\n"
"s_cbranch_scc1 LPDL3_%=\n"
"LPEL3_%=:\n"
"s_add_u32 s66, s66, s72\n"
"s_add_u32 s66, s66, 4\n"
"s_add_u32 s67, s80, s72\n"
"s_sub_u32 s72, s72, 4\n"
"s_max_u32 s72, s72, s62\n"
"s_branch LPKL3_%=\n"
"LPN_%=:\n"
"v_mfma_f32_16x16x32_bf16 v[112:115], a[192:195], a[136:139], v[112:115]\n"
"v_mfma_f32_16x16x32_bf16 v[112:115], a[196:199], a[140:143], v[112:115]\n"
"v_mfma_f32_16x16x32_bf16 v[116:119], a[200:203], a[136:139], v[116:119]\n"
"s_waitcnt vmcnt(11)\n"
"v_mfma_f32_16x16x32_bf16 v[116:119], a[204:207], a[140:143], v[116:119]\n"
"s_barrier\n"
"v_mfma_f32_16x16x32_bf16 v[120:123], a[208:211], a[136:139], v[120:123]\n"
"s_add_u32 s40, s48, 256\n"
"s_xor_b32 m0, s50, 0xc000\n"
"ds_read_b64_tr_b16 a[224:225], v244 offset:16384\n"
"v_mfma_f32_16x16x32_bf16 v[120:123], a[212:215], a[140:143], v[120:123]\n"
"buffer_load_dwordx4 v224, s[28:31], s40 offen nt lds\n"
"v_mfma_f32_16x16x32_bf16 v[124:127], a[216:219], a[136:139], v[124:127]\n"
"ds_read_b64_tr_b16 a[226:227], v244 offset:16896\n"
"v_mfma_f32_16x16x32_bf16 v[124:127], a[220:223], a[140:143], v[124:127]\n"
"ds_read_b64_tr_b16 a[228:229], v244 offset:24576\n"
"v_mfma_f32_16x16x32_bf16 v[140:143], a[216:219], a[144:147], v[140:143]\n"
"ds_read_b64_tr_b16 a[230:231], v244 offset:25088\n"
"v_mfma_f32_16x16x32_bf16 v[140:143], a[220:223], a[148:151], v[140:143]\n"
"ds_read_b64_tr_b16 a[232:233], v245 offset:16384\n"
"v_mfma_f32_16x16x32_bf16 v[136:139], a[208:211], a[144:147], v[136:139]\n"
"ds_read_b64_tr_b16 a[234:235], v245 offset:16896\n"
"v_mfma_f32_16x16x32_bf16 v[136:139], a[212:215], a[148:151], v[136:139]\n"
"buffer_load_dwordx4 v225, s[28:31], s40 offen offset:1024 nt lds\n"
"v_mfma_f32_16x16x32_bf16 v[132:135], a[200:203], a[144:147], v[132:135]\n"
"ds_read_b64_tr_b16 a[236:237], v245 offset:24576\n"
"v_mfma_f32_16x16x32_bf16 v[132:135], a[204:207], a[148:151], v[132:135]\n"
"ds_read_b64_tr_b16 a[238:239], v245 offset:25088\n"
"v_mfma_f32_16x16x32_bf16 v[128:131], a[192:195], a[144:147], v[128:131]\n"
"ds_read_b64_tr_b16 a[240:241], v244 offset:17408\n"
"v_mfma_f32_16x16x32_bf16 v[128:131], a[196:199], a[148:151], v[128:131]\n"
"ds_read_b64_tr_b16 a[242:243], v244 offset:17920\n"
"v_mfma_f32_16x16x32_bf16 v[144:147], a[192:195], a[152:155], v[144:147]\n"
"ds_read_b64_tr_b16 a[244:245], v244 offset:25600\n"
"v_mfma_f32_16x16x32_bf16 v[144:147], a[196:199], a[156:159], v[144:147]\n"
"buffer_load_dwordx4 v226, s[28:31], s40 offen offset:2048 nt lds\n"
"v_mfma_f32_16x16x32_bf16 v[148:151], a[200:203], a[152:155], v[148:151]\n"
"ds_read_b64_tr_b16 a[246:247], v244 offset:26112\n"
"v_mfma_f32_16x16x32_bf16 v[148:151], a[204:207], a[156:159], v[148:151]\n"
"ds_read_b64_tr_b16 a[248:249], v245 offset:17408\n"
"v_mfma_f32_16x16x32_bf16 v[152:155], a[208:211], a[152:155], v[152:155]\n"
"ds_read_b64_tr_b16 a[250:251], v245 offset:17920\n"
"v_mfma_f32_16x16x32_bf16 v[152:155], a[212:215], a[156:159], v[152:155]\n"
"ds_read_b64_tr_b16 a[252:253], v245 offset:25600\n"
"v_mfma_f32_16x16x32_bf16 v[156:159], a[216:219], a[152:155], v[156:159]\n"
"ds_read_b64_tr_b16 a[254:255], v245 offset:26112\n"
"v_mfma_f32_16x16x32_bf16 v[156:159], a[220:223], a[156:159], v[156:159]\n"
"buffer_load_dwordx4 v227, s[28:31], s40 offen offset:3072 nt lds\n"
"v_mfma_f32_16x16x32_bf16 v[172:175], a[216:219], a[160:163], v[172:175]\n"
"s_cmp_eq_u32 s47, 8\n"
"v_mfma_f32_16x16x32_bf16 v[172:175], a[220:223], a[164:167], v[172:175]\n"
"s_cselect_b32 s41, s52, s58\n"
"v_mfma_f32_16x16x32_bf16 v[168:171], a[208:211], a[160:163], v[168:171]\n"
"s_add_u32 s48, s48, s41\n"
"v_mfma_f32_16x16x32_bf16 v[168:171], a[212:215], a[164:167], v[168:171]\n"
"s_sub_u32 s41, s48, 5242880\n"
"v_mfma_f32_16x16x32_bf16 v[164:167], a[200:203], a[160:163], v[164:167]\n"
"s_min_u32 s48, s48, s41\n"
"v_mfma_f32_16x16x32_bf16 v[164:167], a[204:207], a[164:167], v[164:167]\n"
"v_xor_b32 v244, s74, v244\n"
"v_xor_b32 v246, s74, v246\n"
"v_mfma_f32_16x16x32_bf16 v[160:163], a[192:195], a[160:163], v[160:163]\n"
"s_mov_b32 m0, s50\n"
"v_xor_b32 v245, s74, v245\n"
"v_xor_b32 v247, s74, v247\n"
"v_mfma_f32_16x16x32_bf16 v[160:163], a[196:199], a[164:167], v[160:163]\n"
"buffer_load_dwordx4 v224, s[28:31], s48 offen nt lds\n"
"v_mfma_f32_16x16x32_bf16 v[176:179], a[192:195], a[168:171], v[176:179]\n"
"v_xor_b32 v252, s74, v252\n"
"v_xor_b32 v253, s74, v253\n"
"v_mfma_f32_16x16x32_bf16 v[176:179], a[196:199], a[172:175], v[176:179]\n"
"v_xor_b32 v250, s74, v250\n"
"v_xor_b32 v251, s74, v251\n"
"v_mfma_f32_16x16x32_bf16 v[180:183], a[200:203], a[168:171], v[180:183]\n"
"v_mfma_f32_16x16x32_bf16 v[180:183], a[204:207], a[172:175], v[180:183]\n"
"buffer_load_dwordx4 v225, s[28:31], s48 offen offset:1024 nt lds\n"
"v_mfma_f32_16x16x32_bf16 v[184:187], a[208:211], a[168:171], v[184:187]\n"
"v_mfma_f32_16x16x32_bf16 v[184:187], a[212:215], a[172:175], v[184:187]\n"
"v_mfma_f32_16x16x32_bf16 v[188:191], a[216:219], a[168:171], v[188:191]\n"
"v_mfma_f32_16x16x32_bf16 v[188:191], a[220:223], a[172:175], v[188:191]\n"
"v_mfma_f32_16x16x32_bf16 v[204:207], a[216:219], a[176:179], v[204:207]\n"
"v_mfma_f32_16x16x32_bf16 v[204:207], a[220:223], a[180:183], v[204:207]\n"
"buffer_load_dwordx4 v226, s[28:31], s48 offen offset:2048 nt lds\n"
"v_mfma_f32_16x16x32_bf16 v[200:203], a[208:211], a[176:179], v[200:203]\n"
"v_mfma_f32_16x16x32_bf16 v[200:203], a[212:215], a[180:183], v[200:203]\n"
"v_mfma_f32_16x16x32_bf16 v[196:199], a[200:203], a[176:179], v[196:199]\n"
"v_mfma_f32_16x16x32_bf16 v[196:199], a[204:207], a[180:183], v[196:199]\n"
"v_mfma_f32_16x16x32_bf16 v[192:195], a[192:195], a[176:179], v[192:195]\n"
"v_mfma_f32_16x16x32_bf16 v[192:195], a[196:199], a[180:183], v[192:195]\n"
"s_waitcnt lgkmcnt(0)\n"
"buffer_load_dwordx4 v227, s[28:31], s48 offen offset:3072 nt lds\n"
"v_mfma_f32_16x16x32_bf16 v[208:211], a[192:195], a[184:187], v[208:211]\n"
"s_xor_b32 s50, s50, s74\n"
"v_mfma_f32_16x16x32_bf16 v[208:211], a[196:199], a[188:191], v[208:211]\n"
"v_mfma_f32_16x16x32_bf16 v[212:215], a[200:203], a[184:187], v[212:215]\n"
"v_mfma_f32_16x16x32_bf16 v[212:215], a[204:207], a[188:191], v[212:215]\n"
"v_mfma_f32_16x16x32_bf16 v[216:219], a[208:211], a[184:187], v[216:219]\n"
"v_mfma_f32_16x16x32_bf16 v[216:219], a[212:215], a[188:191], v[216:219]\n"
"v_mfma_f32_16x16x32_bf16 v[220:223], a[216:219], a[184:187], v[220:223]\n"
"v_mfma_f32_16x16x32_bf16 v[220:223], a[220:223], a[188:191], v[220:223]\n"
"v_mfma_f32_16x16x32_bf16 v[0:3], a[224:227], a[136:139], v[0:3]\n"
"v_mfma_f32_16x16x32_bf16 v[0:3], a[228:231], a[140:143], v[0:3]\n"
"v_mfma_f32_16x16x32_bf16 v[4:7], a[232:235], a[136:139], v[4:7]\n"
"s_waitcnt vmcnt(8)\n"
"v_mfma_f32_16x16x32_bf16 v[4:7], a[236:239], a[140:143], v[4:7]\n"
"s_barrier\n"
"v_mfma_f32_16x16x32_bf16 v[8:11], a[240:243], a[136:139], v[8:11]\n"
"s_mov_b32 m0, s51\n"
"ds_read_b64_tr_b16 a[192:193], v244 offset:0\n"
"v_mfma_f32_16x16x32_bf16 v[8:11], a[244:247], a[140:143], v[8:11]\n"
"buffer_load_dword v241, s[80:83], 0 offen lds\n"
"s_add_u32 s80, s80, 1456\n"
"s_addc_u32 s81, s81, 0\n"
"buffer_load_dwordx4 v248, s[24:27], s49 offen lds\n"
"v_mfma_f32_16x16x32_bf16 v[12:15], a[248:251], a[136:139], v[12:15]\n"
"ds_read_b64_tr_b16 a[194:195], v244 offset:512\n"
"v_mfma_f32_16x16x32_bf16 v[12:15], a[252:255], a[140:143], v[12:15]\n"
"ds_read_b128 a[136:139], v246 offset:0\n"
"v_mfma_f32_16x16x32_bf16 v[28:31], a[248:251], a[144:147], v[28:31]\n"
"ds_read_b64_tr_b16 a[196:197], v244 offset:8192\n"
"v_mfma_f32_16x16x32_bf16 v[28:31], a[252:255], a[148:151], v[28:31]\n"
"ds_read_b64_tr_b16 a[198:199], v244 offset:8704\n"
"v_mfma_f32_16x16x32_bf16 v[24:27], a[240:243], a[144:147], v[24:27]\n"
"ds_read_b128 a[140:143], v247 offset:0\n"
"v_mfma_f32_16x16x32_bf16 v[24:27], a[244:247], a[148:151], v[24:27]\n"
"buffer_load_dwordx4 v249, s[24:27], s49 offen offset:1024 lds\n"
"v_mfma_f32_16x16x32_bf16 v[20:23], a[232:235], a[144:147], v[20:23]\n"
"ds_read_b64_tr_b16 a[200:201], v245 offset:0\n"
"v_mfma_f32_16x16x32_bf16 v[20:23], a[236:239], a[148:151], v[20:23]\n"
"ds_read_b64_tr_b16 a[202:203], v245 offset:512\n"
"v_mfma_f32_16x16x32_bf16 v[16:19], a[224:227], a[144:147], v[16:19]\n"
"ds_read_b64_tr_b16 a[204:205], v245 offset:8192\n"
"v_mfma_f32_16x16x32_bf16 v[16:19], a[228:231], a[148:151], v[16:19]\n"
"ds_read_b64_tr_b16 a[206:207], v245 offset:8704\n"
"v_mfma_f32_16x16x32_bf16 v[32:35], a[224:227], a[152:155], v[32:35]\n"
"ds_read_b64_tr_b16 a[208:209], v244 offset:1024\n"
"v_mfma_f32_16x16x32_bf16 v[32:35], a[228:231], a[156:159], v[32:35]\n"
"buffer_load_dwordx4 v254, s[24:27], s49 offen offset:2048 lds\n"
"v_mfma_f32_16x16x32_bf16 v[36:39], a[232:235], a[152:155], v[36:39]\n"
"ds_read_b64_tr_b16 a[210:211], v244 offset:1536\n"
"v_mfma_f32_16x16x32_bf16 v[36:39], a[236:239], a[156:159], v[36:39]\n"
"ds_read_b64_tr_b16 a[212:213], v244 offset:9216\n"
"v_mfma_f32_16x16x32_bf16 v[40:43], a[240:243], a[152:155], v[40:43]\n"
"ds_read_b64_tr_b16 a[214:215], v244 offset:9728\n"
"v_mfma_f32_16x16x32_bf16 v[40:43], a[244:247], a[156:159], v[40:43]\n"
"ds_read_b64_tr_b16 a[216:217], v245 offset:1024\n"
"v_mfma_f32_16x16x32_bf16 v[44:47], a[248:251], a[152:155], v[44:47]\n"
"ds_read_b64_tr_b16 a[218:219], v245 offset:1536\n"
"v_mfma_f32_16x16x32_bf16 v[44:47], a[252:255], a[156:159], v[44:47]\n"
"buffer_load_dwordx4 v255, s[24:27], s49 offen offset:3072 lds\n"
"v_mfma_f32_16x16x32_bf16 v[60:63], a[248:251], a[160:163], v[60:63]\n"
"ds_read_b64_tr_b16 a[220:221], v245 offset:9216\n"
"v_mfma_f32_16x16x32_bf16 v[60:63], a[252:255], a[164:167], v[60:63]\n"
"ds_read_b64_tr_b16 a[222:223], v245 offset:9728\n"
"v_mfma_f32_16x16x32_bf16 v[56:59], a[240:243], a[160:163], v[56:59]\n"
"ds_read_b128 a[144:147], v246 offset:2048\n"
"v_mfma_f32_16x16x32_bf16 v[56:59], a[244:247], a[164:167], v[56:59]\n"
"s_add_u32 s40, s49, 40960\n"
"ds_read_b128 a[148:151], v247 offset:2048\n"
"v_mfma_f32_16x16x32_bf16 v[52:55], a[232:235], a[160:163], v[52:55]\n"
"s_add_u32 m0, s51, 4096\n"
"ds_read_b128 a[152:155], v246 offset:4096\n"
"v_mfma_f32_16x16x32_bf16 v[52:55], a[236:239], a[164:167], v[52:55]\n"
"buffer_load_dwordx4 v248, s[24:27], s40 offen lds\n"
"v_mfma_f32_16x16x32_bf16 v[48:51], a[224:227], a[160:163], v[48:51]\n"
"ds_read_b128 a[156:159], v247 offset:4096\n"
"v_mfma_f32_16x16x32_bf16 v[48:51], a[228:231], a[164:167], v[48:51]\n"
"ds_read_b128 a[160:163], v252 offset:0\n"
"v_mfma_f32_16x16x32_bf16 v[64:67], a[224:227], a[168:171], v[64:67]\n"
"ds_read_b128 a[164:167], v253 offset:0\n"
"v_mfma_f32_16x16x32_bf16 v[64:67], a[228:231], a[172:175], v[64:67]\n"
"v_mfma_f32_16x16x32_bf16 v[68:71], a[232:235], a[168:171], v[68:71]\n"
"v_mfma_f32_16x16x32_bf16 v[68:71], a[236:239], a[172:175], v[68:71]\n"
"buffer_load_dwordx4 v249, s[24:27], s40 offen offset:1024 lds\n"
"v_mfma_f32_16x16x32_bf16 v[72:75], a[240:243], a[168:171], v[72:75]\n"
"v_mfma_f32_16x16x32_bf16 v[72:75], a[244:247], a[172:175], v[72:75]\n"
"v_mfma_f32_16x16x32_bf16 v[76:79], a[248:251], a[168:171], v[76:79]\n"
"ds_read_b128 a[168:171], v250 offset:0\n"
"v_mfma_f32_16x16x32_bf16 v[76:79], a[252:255], a[172:175], v[76:79]\n"
"ds_read_b128 a[172:175], v251 offset:0\n"
"v_mfma_f32_16x16x32_bf16 v[92:95], a[248:251], a[176:179], v[92:95]\n"
"v_mfma_f32_16x16x32_bf16 v[92:95], a[252:255], a[180:183], v[92:95]\n"
"buffer_load_dwordx4 v254, s[24:27], s40 offen offset:2048 lds\n"
"v_mfma_f32_16x16x32_bf16 v[88:91], a[240:243], a[176:179], v[88:91]\n"
"s_add_u32 s49, s49, 128\n"
"v_mfma_f32_16x16x32_bf16 v[88:91], a[244:247], a[180:183], v[88:91]\n"
"s_sub_u32 s41, s49, 1280\n"
"v_mfma_f32_16x16x32_bf16 v[84:87], a[232:235], a[176:179], v[84:87]\n"
"s_min_u32 s49, s49, s41\n"
"v_mfma_f32_16x16x32_bf16 v[84:87], a[236:239], a[180:183], v[84:87]\n"
"s_xor_b32 s51, s51, s74\n"
"v_mfma_f32_16x16x32_bf16 v[80:83], a[224:227], a[176:179], v[80:83]\n"
"ds_read_b128 a[176:179], v250 offset:2048\n"
"v_mfma_f32_16x16x32_bf16 v[80:83], a[228:231], a[180:183], v[80:83]\n"
"ds_read_b128 a[180:183], v251 offset:2048\n"
"v_mfma_f32_16x16x32_bf16 v[96:99], a[224:227], a[184:187], v[96:99]\n"
"v_mfma_f32_16x16x32_bf16 v[96:99], a[228:231], a[188:191], v[96:99]\n"
"v_mfma_f32_16x16x32_bf16 v[100:103], a[232:235], a[184:187], v[100:103]\n"
"v_mfma_f32_16x16x32_bf16 v[100:103], a[236:239], a[188:191], v[100:103]\n"
"v_mfma_f32_16x16x32_bf16 v[104:107], a[240:243], a[184:187], v[104:107]\n"
"v_mfma_f32_16x16x32_bf16 v[104:107], a[244:247], a[188:191], v[104:107]\n"
"v_mfma_f32_16x16x32_bf16 v[108:111], a[248:251], a[184:187], v[108:111]\n"
"s_add_u32 s47, s47, 1\n"
"ds_read_b128 a[184:187], v250 offset:4096\n"
"v_mfma_f32_16x16x32_bf16 v[108:111], a[252:255], a[188:191], v[108:111]\n"
"s_waitcnt lgkmcnt(1)\n"
"s_cmp_lg_u32 s47, s62\n"
"ds_read_b128 a[188:191], v251 offset:4096\n"
"s_cbranch_scc1 LPN_%=\n"
"s_branch LPX_%=\n"
"LEND_%=:\n"
"LSLOW_%=:\n"
"s_waitcnt vmcnt(0) lgkmcnt(0)\n"
"s_nop 9\n"
"v_bfe_u32 v148, v242, 4, 4\n"
"v_bfe_u32 v149, v242, 8, 2\n"
"v_lshlrev_b32 v144, 8, v148\n"
"v_lshl_add_u32 v144, v149, 4, v144\n"
"v_add_u32 v144, s43, v144\n"
"v_xor_b32 v145, 64, v144\n"
"v_lshlrev_b32 v146, 1, v148\n"
"v_lshl_add_u32 v146, v149, 10, v146\n"
"v_add_u32 v146, s43, v146\n"
"v_xor_b32 v147, 64, v146\n"
"v_mov_b32 v152, 0xff7fffff\n"
"v_mov_b32 v155, 0\n"
"v_mov_b32 v0, 0\n"
"v_mov_b32 v1, 0\n"
"v_mov_b32 v2, 0\n"
"v_mov_b32 v3, 0\n"
"v_mov_b32 v4, 0\n"
"v_mov_b32 v5, 0\n"
"v_mov_b32 v6, 0\n"
"v_mov_b32 v7, 0\n"
"v_mov_b32 v8, 0\n"
"v_mov_b32 v9, 0\n"
"v_mov_b32 v10, 0\n"
"v_mov_b32 v11, 0\n"
"v_mov_b32 v12, 0\n"
"v_mov_b32 v13, 0\n"
"v_mov_b32 v14, 0\n"
"v_mov_b32 v15, 0\n"
"v_mov_b32 v16, 0\n"
"v_mov_b32 v17, 0\n"
"v_mov_b32 v18, 0\n"
"v_mov_b32 v19, 0\n"
"v_mov_b32 v20, 0\n"
"v_mov_b32 v21, 0\n"
"v_mov_b32 v22, 0\n"
"v_mov_b32 v23, 0\n"
"v_mov_b32 v24, 0\n"
"v_mov_b32 v25, 0\n"
"v_mov_b32 v26, 0\n"
"v_mov_b32 v27, 0\n"
"v_mov_b32 v28, 0\n"
"v_mov_b32 v29, 0\n"
"v_mov_b32 v30, 0\n"
"v_mov_b32 v31, 0\n"
"v_mov_b32 v156, 0xff7fffff\n"
"v_mov_b32 v159, 0\n"
"v_mov_b32 v32, 0\n"
"v_mov_b32 v33, 0\n"
"v_mov_b32 v34, 0\n"
"v_mov_b32 v35, 0\n"
"v_mov_b32 v36, 0\n"
"v_mov_b32 v37, 0\n"
"v_mov_b32 v38, 0\n"
"v_mov_b32 v39, 0\n"
"v_mov_b32 v40, 0\n"
"v_mov_b32 v41, 0\n"
"v_mov_b32 v42, 0\n"
"v_mov_b32 v43, 0\n"
"v_mov_b32 v44, 0\n"
"v_mov_b32 v45, 0\n"
"v_mov_b32 v46, 0\n"
"v_mov_b32 v47, 0\n"
"v_mov_b32 v48, 0\n"
"v_mov_b32 v49, 0\n"
"v_mov_b32 v50, 0\n"
"v_mov_b32 v51, 0\n"
"v_mov_b32 v52, 0\n"
"v_mov_b32 v53, 0\n"
"v_mov_b32 v54, 0\n"
"v_mov_b32 v55, 0\n"
"v_mov_b32 v56, 0\n"
"v_mov_b32 v57, 0\n"
"v_mov_b32 v58, 0\n"
"v_mov_b32 v59, 0\n"
"v_mov_b32 v60, 0\n"
"v_mov_b32 v61, 0\n"
"v_mov_b32 v62, 0\n"
"v_mov_b32 v63, 0\n"
"v_mov_b32 v160, 0xff7fffff\n"
"v_mov_b32 v163, 0\n"
"v_mov_b32 v64, 0\n"
"v_mov_b32 v65, 0\n"
"v_mov_b32 v66, 0\n"
"v_mov_b32 v67, 0\n"
"v_mov_b32 v68, 0\n"
"v_mov_b32 v69, 0\n"
"v_mov_b32 v70, 0\n"
"v_mov_b32 v71, 0\n"
"v_mov_b32 v72, 0\n"
"v_mov_b32 v73, 0\n"
"v_mov_b32 v74, 0\n"
"v_mov_b32 v75, 0\n"
"v_mov_b32 v76, 0\n"
"v_mov_b32 v77, 0\n"
"v_mov_b32 v78, 0\n"
"v_mov_b32 v79, 0\n"
"v_mov_b32 v80, 0\n"
"v_mov_b32 v81, 0\n"
"v_mov_b32 v82, 0\n"
"v_mov_b32 v83, 0\n"
"v_mov_b32 v84, 0\n"
"v_mov_b32 v85, 0\n"
"v_mov_b32 v86, 0\n"
"v_mov_b32 v87, 0\n"
"v_mov_b32 v88, 0\n"
"v_mov_b32 v89, 0\n"
"v_mov_b32 v90, 0\n"
"v_mov_b32 v91, 0\n"
"v_mov_b32 v92, 0\n"
"v_mov_b32 v93, 0\n"
"v_mov_b32 v94, 0\n"
"v_mov_b32 v95, 0\n"
"v_mov_b32 v164, 0xff7fffff\n"
"v_mov_b32 v167, 0\n"
"v_accvgpr_write_b32 a208, 0\n"
"v_accvgpr_write_b32 a209, 0\n"
"v_accvgpr_write_b32 a210, 0\n"
"v_accvgpr_write_b32 a211, 0\n"
"v_accvgpr_write_b32 a212, 0\n"
"v_accvgpr_write_b32 a213, 0\n"
"v_accvgpr_write_b32 a214, 0\n"
"v_accvgpr_write_b32 a215, 0\n"
"v_accvgpr_write_b32 a216, 0\n"
"v_accvgpr_write_b32 a217, 0\n"
"v_accvgpr_write_b32 a218, 0\n"
"v_accvgpr_write_b32 a219, 0\n"
"v_accvgpr_write_b32 a220, 0\n"
"v_accvgpr_write_b32 a221, 0\n"
"v_accvgpr_write_b32 a222, 0\n"
"v_accvgpr_write_b32 a223, 0\n"
"v_accvgpr_write_b32 a224, 0\n"
"v_accvgpr_write_b32 a225, 0\n"
"v_accvgpr_write_b32 a226, 0\n"
"v_accvgpr_write_b32 a227, 0\n"
"v_accvgpr_write_b32 a228, 0\n"
"v_accvgpr_write_b32 a229, 0\n"
"v_accvgpr_write_b32 a230, 0\n"
"v_accvgpr_write_b32 a231, 0\n"
"v_accvgpr_write_b32 a232, 0\n"
"v_accvgpr_write_b32 a233, 0\n"
"v_accvgpr_write_b32 a234, 0\n"
"v_accvgpr_write_b32 a235, 0\n"
"v_accvgpr_write_b32 a236, 0\n"
"v_accvgpr_write_b32 a237, 0\n"
"v_accvgpr_write_b32 a238, 0\n"
"v_accvgpr_write_b32 a239, 0\n"
"s_lshl_b32 s80, s47, 16\n"
"s_add_u32 s81, s80, 4096\n"
"s_mov_b32 s82, 0\n"
"LSLOOP_%=:\n"
"s_lshl_b32 s83, s82, 3\n"
"s_and_b32 s83, s83, 64\n"
"buffer_load_dwordx4 v[128:131], v144, s[84:87], s80 offen offset:0\n"
"buffer_load_dwordx4 v[132:135], v145, s[84:87], s80 offen offset:0\n"
"buffer_load_dwordx4 v[136:139], v144, s[84:87], s80 offen offset:128\n"
"buffer_load_dwordx4 v[140:143], v145, s[84:87], s80 offen offset:128\n"
"s_waitcnt vmcnt(0)\n"
"v_mfma_f32_16x16x32_bf16 v[96:99], v[128:131], a[0:3], 0\n"
"v_mfma_f32_16x16x32_bf16 v[96:99], v[132:135], a[4:7], v[96:99]\n"
"v_mfma_f32_16x16x32_bf16 v[96:99], v[136:139], a[8:11], v[96:99]\n"
"v_mfma_f32_16x16x32_bf16 v[96:99], v[140:143], a[12:15], v[96:99]\n"
"v_mfma_f32_16x16x32_bf16 v[104:107], v[128:131], a[16:19], 0\n"
"v_mfma_f32_16x16x32_bf16 v[104:107], v[132:135], a[20:23], v[104:107]\n"
"v_mfma_f32_16x16x32_bf16 v[104:107], v[136:139], a[24:27], v[104:107]\n"
"v_mfma_f32_16x16x32_bf16 v[104:107], v[140:143], a[28:31], v[104:107]\n"
"v_mfma_f32_16x16x32_bf16 v[112:115], v[128:131], a[32:35], 0\n"
"v_mfma_f32_16x16x32_bf16 v[112:115], v[132:135], a[36:39], v[112:115]\n"
"v_mfma_f32_16x16x32_bf16 v[112:115], v[136:139], a[40:43], v[112:115]\n"
"v_mfma_f32_16x16x32_bf16 v[112:115], v[140:143], a[44:47], v[112:115]\n"
"v_mfma_f32_16x16x32_bf16 v[120:123], v[128:131], a[192:195], 0\n"
"v_mfma_f32_16x16x32_bf16 v[120:123], v[132:135], a[196:199], v[120:123]\n"
"v_mfma_f32_16x16x32_bf16 v[120:123], v[136:139], a[200:203], v[120:123]\n"
"v_mfma_f32_16x16x32_bf16 v[120:123], v[140:143], a[204:207], v[120:123]\n"
"s_nop 7\n"
"buffer_load_dwordx4 v[128:131], v144, s[84:87], s81 offen offset:0\n"
"buffer_load_dwordx4 v[132:135], v145, s[84:87], s81 offen offset:0\n"
"buffer_load_dwordx4 v[136:139], v144, s[84:87], s81 offen offset:128\n"
"buffer_load_dwordx4 v[140:143], v145, s[84:87], s81 offen offset:128\n"
"s_waitcnt vmcnt(0)\n"
"v_mfma_f32_16x16x32_bf16 v[100:103], v[128:131], a[0:3], 0\n"
"v_mfma_f32_16x16x32_bf16 v[100:103], v[132:135], a[4:7], v[100:103]\n"
"v_mfma_f32_16x16x32_bf16 v[100:103], v[136:139], a[8:11], v[100:103]\n"
"v_mfma_f32_16x16x32_bf16 v[100:103], v[140:143], a[12:15], v[100:103]\n"
"v_mfma_f32_16x16x32_bf16 v[108:111], v[128:131], a[16:19], 0\n"
"v_mfma_f32_16x16x32_bf16 v[108:111], v[132:135], a[20:23], v[108:111]\n"
"v_mfma_f32_16x16x32_bf16 v[108:111], v[136:139], a[24:27], v[108:111]\n"
"v_mfma_f32_16x16x32_bf16 v[108:111], v[140:143], a[28:31], v[108:111]\n"
"v_mfma_f32_16x16x32_bf16 v[116:119], v[128:131], a[32:35], 0\n"
"v_mfma_f32_16x16x32_bf16 v[116:119], v[132:135], a[36:39], v[116:119]\n"
"v_mfma_f32_16x16x32_bf16 v[116:119], v[136:139], a[40:43], v[116:119]\n"
"v_mfma_f32_16x16x32_bf16 v[116:119], v[140:143], a[44:47], v[116:119]\n"
"v_mfma_f32_16x16x32_bf16 v[124:127], v[128:131], a[192:195], 0\n"
"v_mfma_f32_16x16x32_bf16 v[124:127], v[132:135], a[196:199], v[124:127]\n"
"v_mfma_f32_16x16x32_bf16 v[124:127], v[136:139], a[200:203], v[124:127]\n"
"v_mfma_f32_16x16x32_bf16 v[124:127], v[140:143], a[204:207], v[124:127]\n"
"s_nop 7\n"
"v_max3_f32 v153, v96, v97, v98\n"
"v_max3_f32 v153, v153, v99, v100\n"
"v_max3_f32 v153, v153, v101, v102\n"
"v_max3_f32 v153, v153, v103, v152\n"
"v_mov_b32 v154, v153\n"
"s_nop 1\n"
"v_permlane32_swap_b32 v153, v154\n"
"v_max_f32 v153, v153, v154\n"
"v_mov_b32 v154, v153\n"
"s_nop 1\n"
"v_permlane16_swap_b32 v153, v154\n"
"v_max_f32 v153, v153, v154\n"
"v_sub_f32 v154, v152, v153\n"
"v_exp_f32 v154, v154\n"
"v_mov_b32 v152, v153\n"
"v_sub_f32 v96, v96, v152\n"
"v_sub_f32 v97, v97, v152\n"
"v_sub_f32 v98, v98, v152\n"
"v_sub_f32 v99, v99, v152\n"
"v_sub_f32 v100, v100, v152\n"
"v_sub_f32 v101, v101, v152\n"
"v_sub_f32 v102, v102, v152\n"
"v_sub_f32 v103, v103, v152\n"
"v_exp_f32 v96, v96\n"
"v_exp_f32 v97, v97\n"
"v_exp_f32 v98, v98\n"
"v_exp_f32 v99, v99\n"
"v_exp_f32 v100, v100\n"
"v_exp_f32 v101, v101\n"
"v_exp_f32 v102, v102\n"
"v_exp_f32 v103, v103\n"
"v_mul_f32 v155, v155, v154\n"
"v_add_f32 v155, v155, v96\n"
"v_add_f32 v155, v155, v97\n"
"v_add_f32 v155, v155, v98\n"
"v_add_f32 v155, v155, v99\n"
"v_add_f32 v155, v155, v100\n"
"v_add_f32 v155, v155, v101\n"
"v_add_f32 v155, v155, v102\n"
"v_add_f32 v155, v155, v103\n"
"v_cvt_pk_bf16_f32 v96, v96, v97\n"
"v_cvt_pk_bf16_f32 v97, v98, v99\n"
"v_cvt_pk_bf16_f32 v98, v100, v101\n"
"v_cvt_pk_bf16_f32 v99, v102, v103\n"
"v_mov_b32_dpp v154, v154 row_ror:12 row_mask:0x2 bank_mask:0xf\n"
"s_nop 1\n"
"v_mov_b32_dpp v154, v154 row_ror:8 row_mask:0x4 bank_mask:0xf\n"
"s_nop 1\n"
"v_mov_b32_dpp v154, v154 row_ror:4 row_mask:0x8 bank_mask:0xf\n"
"s_nop 1\n"
"v_mul_f32_dpp v0, v154, v0 row_newbcast:0 row_mask:0xf bank_mask:0xf\n"
"v_mul_f32_dpp v1, v154, v1 row_newbcast:1 row_mask:0xf bank_mask:0xf\n"
"v_mul_f32_dpp v2, v154, v2 row_newbcast:2 row_mask:0xf bank_mask:0xf\n"
"v_mul_f32_dpp v3, v154, v3 row_newbcast:3 row_mask:0xf bank_mask:0xf\n"
"v_mul_f32_dpp v4, v154, v4 row_newbcast:0 row_mask:0xf bank_mask:0xf\n"
"v_mul_f32_dpp v5, v154, v5 row_newbcast:1 row_mask:0xf bank_mask:0xf\n"
"v_mul_f32_dpp v6, v154, v6 row_newbcast:2 row_mask:0xf bank_mask:0xf\n"
"v_mul_f32_dpp v7, v154, v7 row_newbcast:3 row_mask:0xf bank_mask:0xf\n"
"v_mul_f32_dpp v8, v154, v8 row_newbcast:0 row_mask:0xf bank_mask:0xf\n"
"v_mul_f32_dpp v9, v154, v9 row_newbcast:1 row_mask:0xf bank_mask:0xf\n"
"v_mul_f32_dpp v10, v154, v10 row_newbcast:2 row_mask:0xf bank_mask:0xf\n"
"v_mul_f32_dpp v11, v154, v11 row_newbcast:3 row_mask:0xf bank_mask:0xf\n"
"v_mul_f32_dpp v12, v154, v12 row_newbcast:0 row_mask:0xf bank_mask:0xf\n"
"v_mul_f32_dpp v13, v154, v13 row_newbcast:1 row_mask:0xf bank_mask:0xf\n"
"v_mul_f32_dpp v14, v154, v14 row_newbcast:2 row_mask:0xf bank_mask:0xf\n"
"v_mul_f32_dpp v15, v154, v15 row_newbcast:3 row_mask:0xf bank_mask:0xf\n"
"v_mul_f32_dpp v16, v154, v16 row_newbcast:0 row_mask:0xf bank_mask:0xf\n"
"v_mul_f32_dpp v17, v154, v17 row_newbcast:1 row_mask:0xf bank_mask:0xf\n"
"v_mul_f32_dpp v18, v154, v18 row_newbcast:2 row_mask:0xf bank_mask:0xf\n"
"v_mul_f32_dpp v19, v154, v19 row_newbcast:3 row_mask:0xf bank_mask:0xf\n"
"v_mul_f32_dpp v20, v154, v20 row_newbcast:0 row_mask:0xf bank_mask:0xf\n"
"v_mul_f32_dpp v21, v154, v21 row_newbcast:1 row_mask:0xf bank_mask:0xf\n"
"v_mul_f32_dpp v22, v154, v22 row_newbcast:2 row_mask:0xf bank_mask:0xf\n"
"v_mul_f32_dpp v23, v154, v23 row_newbcast:3 row_mask:0xf bank_mask:0xf\n"
"v_mul_f32_dpp v24, v154, v24 row_newbcast:0 row_mask:0xf bank_mask:0xf\n"
"v_mul_f32_dpp v25, v154, v25 row_newbcast:1 row_mask:0xf bank_mask:0xf\n"
"v_mul_f32_dpp v26, v154, v26 row_newbcast:2 row_mask:0xf bank_mask:0xf\n"
"v_mul_f32_dpp v27, v154, v27 row_newbcast:3 row_mask:0xf bank_mask:0xf\n"
"v_mul_f32_dpp v28, v154, v28 row_newbcast:0 row_mask:0xf bank_mask:0xf\n"
"v_mul_f32_dpp v29, v154, v29 row_newbcast:1 row_mask:0xf bank_mask:0xf\n"
"v_mul_f32_dpp v30, v154, v30 row_newbcast:2 row_mask:0xf bank_mask:0xf\n"
"v_mul_f32_dpp v31, v154, v31 row_newbcast:3 row_mask:0xf bank_mask:0xf\n"
"v_max3_f32 v157, v104, v105, v106\n"
"v_max3_f32 v157, v157, v107, v108\n"
"v_max3_f32 v157, v157, v109, v110\n"
"v_max3_f32 v157, v157, v111, v156\n"
"v_mov_b32 v158, v157\n"
"s_nop 1\n"
"v_permlane32_swap_b32 v157, v158\n"
"v_max_f32 v157, v157, v158\n"
"v_mov_b32 v158, v157\n"
"s_nop 1\n"
"v_permlane16_swap_b32 v157, v158\n"
"v_max_f32 v157, v157, v158\n"
"v_sub_f32 v158, v156, v157\n"
"v_exp_f32 v158, v158\n"
"v_mov_b32 v156, v157\n"
"v_sub_f32 v104, v104, v156\n"
"v_sub_f32 v105, v105, v156\n"
"v_sub_f32 v106, v106, v156\n"
"v_sub_f32 v107, v107, v156\n"
"v_sub_f32 v108, v108, v156\n"
"v_sub_f32 v109, v109, v156\n"
"v_sub_f32 v110, v110, v156\n"
"v_sub_f32 v111, v111, v156\n"
"v_exp_f32 v104, v104\n"
"v_exp_f32 v105, v105\n"
"v_exp_f32 v106, v106\n"
"v_exp_f32 v107, v107\n"
"v_exp_f32 v108, v108\n"
"v_exp_f32 v109, v109\n"
"v_exp_f32 v110, v110\n"
"v_exp_f32 v111, v111\n"
"v_mul_f32 v159, v159, v158\n"
"v_add_f32 v159, v159, v104\n"
"v_add_f32 v159, v159, v105\n"
"v_add_f32 v159, v159, v106\n"
"v_add_f32 v159, v159, v107\n"
"v_add_f32 v159, v159, v108\n"
"v_add_f32 v159, v159, v109\n"
"v_add_f32 v159, v159, v110\n"
"v_add_f32 v159, v159, v111\n"
"v_cvt_pk_bf16_f32 v104, v104, v105\n"
"v_cvt_pk_bf16_f32 v105, v106, v107\n"
"v_cvt_pk_bf16_f32 v106, v108, v109\n"
"v_cvt_pk_bf16_f32 v107, v110, v111\n"
"v_mov_b32_dpp v158, v158 row_ror:12 row_mask:0x2 bank_mask:0xf\n"
"s_nop 1\n"
"v_mov_b32_dpp v158, v158 row_ror:8 row_mask:0x4 bank_mask:0xf\n"
"s_nop 1\n"
"v_mov_b32_dpp v158, v158 row_ror:4 row_mask:0x8 bank_mask:0xf\n"
"s_nop 1\n"
"v_mul_f32_dpp v32, v158, v32 row_newbcast:0 row_mask:0xf bank_mask:0xf\n"
"v_mul_f32_dpp v33, v158, v33 row_newbcast:1 row_mask:0xf bank_mask:0xf\n"
"v_mul_f32_dpp v34, v158, v34 row_newbcast:2 row_mask:0xf bank_mask:0xf\n"
"v_mul_f32_dpp v35, v158, v35 row_newbcast:3 row_mask:0xf bank_mask:0xf\n"
"v_mul_f32_dpp v36, v158, v36 row_newbcast:0 row_mask:0xf bank_mask:0xf\n"
"v_mul_f32_dpp v37, v158, v37 row_newbcast:1 row_mask:0xf bank_mask:0xf\n"
"v_mul_f32_dpp v38, v158, v38 row_newbcast:2 row_mask:0xf bank_mask:0xf\n"
"v_mul_f32_dpp v39, v158, v39 row_newbcast:3 row_mask:0xf bank_mask:0xf\n"
"v_mul_f32_dpp v40, v158, v40 row_newbcast:0 row_mask:0xf bank_mask:0xf\n"
"v_mul_f32_dpp v41, v158, v41 row_newbcast:1 row_mask:0xf bank_mask:0xf\n"
"v_mul_f32_dpp v42, v158, v42 row_newbcast:2 row_mask:0xf bank_mask:0xf\n"
"v_mul_f32_dpp v43, v158, v43 row_newbcast:3 row_mask:0xf bank_mask:0xf\n"
"v_mul_f32_dpp v44, v158, v44 row_newbcast:0 row_mask:0xf bank_mask:0xf\n"
"v_mul_f32_dpp v45, v158, v45 row_newbcast:1 row_mask:0xf bank_mask:0xf\n"
"v_mul_f32_dpp v46, v158, v46 row_newbcast:2 row_mask:0xf bank_mask:0xf\n"
"v_mul_f32_dpp v47, v158, v47 row_newbcast:3 row_mask:0xf bank_mask:0xf\n"
"v_mul_f32_dpp v48, v158, v48 row_newbcast:0 row_mask:0xf bank_mask:0xf\n"
"v_mul_f32_dpp v49, v158, v49 row_newbcast:1 row_mask:0xf bank_mask:0xf\n"
"v_mul_f32_dpp v50, v158, v50 row_newbcast:2 row_mask:0xf bank_mask:0xf\n"
"v_mul_f32_dpp v51, v158, v51 row_newbcast:3 row_mask:0xf bank_mask:0xf\n"
"v_mul_f32_dpp v52, v158, v52 row_newbcast:0 row_mask:0xf bank_mask:0xf\n"
"v_mul_f32_dpp v53, v158, v53 row_newbcast:1 row_mask:0xf bank_mask:0xf\n"
"v_mul_f32_dpp v54, v158, v54 row_newbcast:2 row_mask:0xf bank_mask:0xf\n"
"v_mul_f32_dpp v55, v158, v55 row_newbcast:3 row_mask:0xf bank_mask:0xf\n"
"v_mul_f32_dpp v56, v158, v56 row_newbcast:0 row_mask:0xf bank_mask:0xf\n"
"v_mul_f32_dpp v57, v158, v57 row_newbcast:1 row_mask:0xf bank_mask:0xf\n"
"v_mul_f32_dpp v58, v158, v58 row_newbcast:2 row_mask:0xf bank_mask:0xf\n"
"v_mul_f32_dpp v59, v158, v59 row_newbcast:3 row_mask:0xf bank_mask:0xf\n"
"v_mul_f32_dpp v60, v158, v60 row_newbcast:0 row_mask:0xf bank_mask:0xf\n"
"v_mul_f32_dpp v61, v158, v61 row_newbcast:1 row_mask:0xf bank_mask:0xf\n"
"v_mul_f32_dpp v62, v158, v62 row_newbcast:2 row_mask:0xf bank_mask:0xf\n"
"v_mul_f32_dpp v63, v158, v63 row_newbcast:3 row_mask:0xf bank_mask:0xf\n"
"v_max3_f32 v161, v112, v113, v114\n"
"v_max3_f32 v161, v161, v115, v116\n"
"v_max3_f32 v161, v161, v117, v118\n"
"v_max3_f32 v161, v161, v119, v160\n"
"v_mov_b32 v162, v161\n"
"s_nop 1\n"
"v_permlane32_swap_b32 v161, v162\n"
"v_max_f32 v161, v161, v162\n"
"v_mov_b32 v162, v161\n"
"s_nop 1\n"
"v_permlane16_swap_b32 v161, v162\n"
"v_max_f32 v161, v161, v162\n"
"v_sub_f32 v162, v160, v161\n"
"v_exp_f32 v162, v162\n"
"v_mov_b32 v160, v161\n"
"v_sub_f32 v112, v112, v160\n"
"v_sub_f32 v113, v113, v160\n"
"v_sub_f32 v114, v114, v160\n"
"v_sub_f32 v115, v115, v160\n"
"v_sub_f32 v116, v116, v160\n"
"v_sub_f32 v117, v117, v160\n"
"v_sub_f32 v118, v118, v160\n"
"v_sub_f32 v119, v119, v160\n"
"v_exp_f32 v112, v112\n"
"v_exp_f32 v113, v113\n"
"v_exp_f32 v114, v114\n"
"v_exp_f32 v115, v115\n"
"v_exp_f32 v116, v116\n"
"v_exp_f32 v117, v117\n"
"v_exp_f32 v118, v118\n"
"v_exp_f32 v119, v119\n"
"v_mul_f32 v163, v163, v162\n"
"v_add_f32 v163, v163, v112\n"
"v_add_f32 v163, v163, v113\n"
"v_add_f32 v163, v163, v114\n"
"v_add_f32 v163, v163, v115\n"
"v_add_f32 v163, v163, v116\n"
"v_add_f32 v163, v163, v117\n"
"v_add_f32 v163, v163, v118\n"
"v_add_f32 v163, v163, v119\n"
"v_cvt_pk_bf16_f32 v112, v112, v113\n"
"v_cvt_pk_bf16_f32 v113, v114, v115\n"
"v_cvt_pk_bf16_f32 v114, v116, v117\n"
"v_cvt_pk_bf16_f32 v115, v118, v119\n"
"v_mov_b32_dpp v162, v162 row_ror:12 row_mask:0x2 bank_mask:0xf\n"
"s_nop 1\n"
"v_mov_b32_dpp v162, v162 row_ror:8 row_mask:0x4 bank_mask:0xf\n"
"s_nop 1\n"
"v_mov_b32_dpp v162, v162 row_ror:4 row_mask:0x8 bank_mask:0xf\n"
"s_nop 1\n"
"v_mul_f32_dpp v64, v162, v64 row_newbcast:0 row_mask:0xf bank_mask:0xf\n"
"v_mul_f32_dpp v65, v162, v65 row_newbcast:1 row_mask:0xf bank_mask:0xf\n"
"v_mul_f32_dpp v66, v162, v66 row_newbcast:2 row_mask:0xf bank_mask:0xf\n"
"v_mul_f32_dpp v67, v162, v67 row_newbcast:3 row_mask:0xf bank_mask:0xf\n"
"v_mul_f32_dpp v68, v162, v68 row_newbcast:0 row_mask:0xf bank_mask:0xf\n"
"v_mul_f32_dpp v69, v162, v69 row_newbcast:1 row_mask:0xf bank_mask:0xf\n"
"v_mul_f32_dpp v70, v162, v70 row_newbcast:2 row_mask:0xf bank_mask:0xf\n"
"v_mul_f32_dpp v71, v162, v71 row_newbcast:3 row_mask:0xf bank_mask:0xf\n"
"v_mul_f32_dpp v72, v162, v72 row_newbcast:0 row_mask:0xf bank_mask:0xf\n"
"v_mul_f32_dpp v73, v162, v73 row_newbcast:1 row_mask:0xf bank_mask:0xf\n"
"v_mul_f32_dpp v74, v162, v74 row_newbcast:2 row_mask:0xf bank_mask:0xf\n"
"v_mul_f32_dpp v75, v162, v75 row_newbcast:3 row_mask:0xf bank_mask:0xf\n"
"v_mul_f32_dpp v76, v162, v76 row_newbcast:0 row_mask:0xf bank_mask:0xf\n"
"v_mul_f32_dpp v77, v162, v77 row_newbcast:1 row_mask:0xf bank_mask:0xf\n"
"v_mul_f32_dpp v78, v162, v78 row_newbcast:2 row_mask:0xf bank_mask:0xf\n"
"v_mul_f32_dpp v79, v162, v79 row_newbcast:3 row_mask:0xf bank_mask:0xf\n"
"v_mul_f32_dpp v80, v162, v80 row_newbcast:0 row_mask:0xf bank_mask:0xf\n"
"v_mul_f32_dpp v81, v162, v81 row_newbcast:1 row_mask:0xf bank_mask:0xf\n"
"v_mul_f32_dpp v82, v162, v82 row_newbcast:2 row_mask:0xf bank_mask:0xf\n"
"v_mul_f32_dpp v83, v162, v83 row_newbcast:3 row_mask:0xf bank_mask:0xf\n"
"v_mul_f32_dpp v84, v162, v84 row_newbcast:0 row_mask:0xf bank_mask:0xf\n"
"v_mul_f32_dpp v85, v162, v85 row_newbcast:1 row_mask:0xf bank_mask:0xf\n"
"v_mul_f32_dpp v86, v162, v86 row_newbcast:2 row_mask:0xf bank_mask:0xf\n"
"v_mul_f32_dpp v87, v162, v87 row_newbcast:3 row_mask:0xf bank_mask:0xf\n"
"v_mul_f32_dpp v88, v162, v88 row_newbcast:0 row_mask:0xf bank_mask:0xf\n"
"v_mul_f32_dpp v89, v162, v89 row_newbcast:1 row_mask:0xf bank_mask:0xf\n"
"v_mul_f32_dpp v90, v162, v90 row_newbcast:2 row_mask:0xf bank_mask:0xf\n"
"v_mul_f32_dpp v91, v162, v91 row_newbcast:3 row_mask:0xf bank_mask:0xf\n"
"v_mul_f32_dpp v92, v162, v92 row_newbcast:0 row_mask:0xf bank_mask:0xf\n"
"v_mul_f32_dpp v93, v162, v93 row_newbcast:1 row_mask:0xf bank_mask:0xf\n"
"v_mul_f32_dpp v94, v162, v94 row_newbcast:2 row_mask:0xf bank_mask:0xf\n"
"v_mul_f32_dpp v95, v162, v95 row_newbcast:3 row_mask:0xf bank_mask:0xf\n"
"s_cmp_lg_u32 s83, s43\n"
"s_cbranch_scc1 LSK3A_%=\n"
"v_max3_f32 v165, v120, v121, v122\n"
"v_max3_f32 v165, v165, v123, v124\n"
"v_max3_f32 v165, v165, v125, v126\n"
"v_max3_f32 v165, v165, v127, v164\n"
"v_mov_b32 v166, v165\n"
"s_nop 1\n"
"v_permlane32_swap_b32 v165, v166\n"
"v_max_f32 v165, v165, v166\n"
"v_mov_b32 v166, v165\n"
"s_nop 1\n"
"v_permlane16_swap_b32 v165, v166\n"
"v_max_f32 v165, v165, v166\n"
"v_sub_f32 v166, v164, v165\n"
"v_exp_f32 v166, v166\n"
"v_mov_b32 v164, v165\n"
"v_sub_f32 v120, v120, v164\n"
"v_sub_f32 v121, v121, v164\n"
"v_sub_f32 v122, v122, v164\n"
"v_sub_f32 v123, v123, v164\n"
"v_sub_f32 v124, v124, v164\n"
"v_sub_f32 v125, v125, v164\n"
"v_sub_f32 v126, v126, v164\n"
"v_sub_f32 v127, v127, v164\n"
"v_exp_f32 v120, v120\n"
"v_exp_f32 v121, v121\n"
"v_exp_f32 v122, v122\n"
"v_exp_f32 v123, v123\n"
"v_exp_f32 v124, v124\n"
"v_exp_f32 v125, v125\n"
"v_exp_f32 v126, v126\n"
"v_exp_f32 v127, v127\n"
"v_mul_f32 v167, v167, v166\n"
"v_add_f32 v167, v167, v120\n"
"v_add_f32 v167, v167, v121\n"
"v_add_f32 v167, v167, v122\n"
"v_add_f32 v167, v167, v123\n"
"v_add_f32 v167, v167, v124\n"
"v_add_f32 v167, v167, v125\n"
"v_add_f32 v167, v167, v126\n"
"v_add_f32 v167, v167, v127\n"
"v_cvt_pk_bf16_f32 v120, v120, v121\n"
"v_cvt_pk_bf16_f32 v121, v122, v123\n"
"v_cvt_pk_bf16_f32 v122, v124, v125\n"
"v_cvt_pk_bf16_f32 v123, v126, v127\n"
"v_mov_b32_dpp v166, v166 row_ror:12 row_mask:0x2 bank_mask:0xf\n"
"s_nop 1\n"
"v_mov_b32_dpp v166, v166 row_ror:8 row_mask:0x4 bank_mask:0xf\n"
"s_nop 1\n"
"v_mov_b32_dpp v166, v166 row_ror:4 row_mask:0x8 bank_mask:0xf\n"
"v_accvgpr_read_b32 v124, a208\n"
"s_nop 1\n"
"v_mul_f32_dpp v124, v166, v124 row_newbcast:0 row_mask:0xf bank_mask:0xf\n"
"v_accvgpr_write_b32 a208, v124\n"
"v_accvgpr_read_b32 v125, a209\n"
"s_nop 1\n"
"v_mul_f32_dpp v125, v166, v125 row_newbcast:1 row_mask:0xf bank_mask:0xf\n"
"v_accvgpr_write_b32 a209, v125\n"
"v_accvgpr_read_b32 v126, a210\n"
"s_nop 1\n"
"v_mul_f32_dpp v126, v166, v126 row_newbcast:2 row_mask:0xf bank_mask:0xf\n"
"v_accvgpr_write_b32 a210, v126\n"
"v_accvgpr_read_b32 v127, a211\n"
"s_nop 1\n"
"v_mul_f32_dpp v127, v166, v127 row_newbcast:3 row_mask:0xf bank_mask:0xf\n"
"v_accvgpr_write_b32 a211, v127\n"
"v_accvgpr_read_b32 v124, a212\n"
"s_nop 1\n"
"v_mul_f32_dpp v124, v166, v124 row_newbcast:0 row_mask:0xf bank_mask:0xf\n"
"v_accvgpr_write_b32 a212, v124\n"
"v_accvgpr_read_b32 v125, a213\n"
"s_nop 1\n"
"v_mul_f32_dpp v125, v166, v125 row_newbcast:1 row_mask:0xf bank_mask:0xf\n"
"v_accvgpr_write_b32 a213, v125\n"
"v_accvgpr_read_b32 v126, a214\n"
"s_nop 1\n"
"v_mul_f32_dpp v126, v166, v126 row_newbcast:2 row_mask:0xf bank_mask:0xf\n"
"v_accvgpr_write_b32 a214, v126\n"
"v_accvgpr_read_b32 v127, a215\n"
"s_nop 1\n"
"v_mul_f32_dpp v127, v166, v127 row_newbcast:3 row_mask:0xf bank_mask:0xf\n"
"v_accvgpr_write_b32 a215, v127\n"
"v_accvgpr_read_b32 v124, a216\n"
"s_nop 1\n"
"v_mul_f32_dpp v124, v166, v124 row_newbcast:0 row_mask:0xf bank_mask:0xf\n"
"v_accvgpr_write_b32 a216, v124\n"
"v_accvgpr_read_b32 v125, a217\n"
"s_nop 1\n"
"v_mul_f32_dpp v125, v166, v125 row_newbcast:1 row_mask:0xf bank_mask:0xf\n"
"v_accvgpr_write_b32 a217, v125\n"
"v_accvgpr_read_b32 v126, a218\n"
"s_nop 1\n"
"v_mul_f32_dpp v126, v166, v126 row_newbcast:2 row_mask:0xf bank_mask:0xf\n"
"v_accvgpr_write_b32 a218, v126\n"
"v_accvgpr_read_b32 v127, a219\n"
"s_nop 1\n"
"v_mul_f32_dpp v127, v166, v127 row_newbcast:3 row_mask:0xf bank_mask:0xf\n"
"v_accvgpr_write_b32 a219, v127\n"
"v_accvgpr_read_b32 v124, a220\n"
"s_nop 1\n"
"v_mul_f32_dpp v124, v166, v124 row_newbcast:0 row_mask:0xf bank_mask:0xf\n"
"v_accvgpr_write_b32 a220, v124\n"
"v_accvgpr_read_b32 v125, a221\n"
"s_nop 1\n"
"v_mul_f32_dpp v125, v166, v125 row_newbcast:1 row_mask:0xf bank_mask:0xf\n"
"v_accvgpr_write_b32 a221, v125\n"
"v_accvgpr_read_b32 v126, a222\n"
"s_nop 1\n"
"v_mul_f32_dpp v126, v166, v126 row_newbcast:2 row_mask:0xf bank_mask:0xf\n"
"v_accvgpr_write_b32 a222, v126\n"
"v_accvgpr_read_b32 v127, a223\n"
"s_nop 1\n"
"v_mul_f32_dpp v127, v166, v127 row_newbcast:3 row_mask:0xf bank_mask:0xf\n"
"v_accvgpr_write_b32 a223, v127\n"
"v_accvgpr_read_b32 v124, a224\n"
"s_nop 1\n"
"v_mul_f32_dpp v124, v166, v124 row_newbcast:0 row_mask:0xf bank_mask:0xf\n"
"v_accvgpr_write_b32 a224, v124\n"
"v_accvgpr_read_b32 v125, a225\n"
"s_nop 1\n"
"v_mul_f32_dpp v125, v166, v125 row_newbcast:1 row_mask:0xf bank_mask:0xf\n"
"v_accvgpr_write_b32 a225, v125\n"
"v_accvgpr_read_b32 v126, a226\n"
"s_nop 1\n"
"v_mul_f32_dpp v126, v166, v126 row_newbcast:2 row_mask:0xf bank_mask:0xf\n"
"v_accvgpr_write_b32 a226, v126\n"
"v_accvgpr_read_b32 v127, a227\n"
"s_nop 1\n"
"v_mul_f32_dpp v127, v166, v127 row_newbcast:3 row_mask:0xf bank_mask:0xf\n"
"v_accvgpr_write_b32 a227, v127\n"
"v_accvgpr_read_b32 v124, a228\n"
"s_nop 1\n"
"v_mul_f32_dpp v124, v166, v124 row_newbcast:0 row_mask:0xf bank_mask:0xf\n"
"v_accvgpr_write_b32 a228, v124\n"
"v_accvgpr_read_b32 v125, a229\n"
"s_nop 1\n"
"v_mul_f32_dpp v125, v166, v125 row_newbcast:1 row_mask:0xf bank_mask:0xf\n"
"v_accvgpr_write_b32 a229, v125\n"
"v_accvgpr_read_b32 v126, a230\n"
"s_nop 1\n"
"v_mul_f32_dpp v126, v166, v126 row_newbcast:2 row_mask:0xf bank_mask:0xf\n"
"v_accvgpr_write_b32 a230, v126\n"
"v_accvgpr_read_b32 v127, a231\n"
"s_nop 1\n"
"v_mul_f32_dpp v127, v166, v127 row_newbcast:3 row_mask:0xf bank_mask:0xf\n"
"v_accvgpr_write_b32 a231, v127\n"
"v_accvgpr_read_b32 v124, a232\n"
"s_nop 1\n"
"v_mul_f32_dpp v124, v166, v124 row_newbcast:0 row_mask:0xf bank_mask:0xf\n"
"v_accvgpr_write_b32 a232, v124\n"
"v_accvgpr_read_b32 v125, a233\n"
"s_nop 1\n"
"v_mul_f32_dpp v125, v166, v125 row_newbcast:1 row_mask:0xf bank_mask:0xf\n"
"v_accvgpr_write_b32 a233, v125\n"
"v_accvgpr_read_b32 v126, a234\n"
"s_nop 1\n"
"v_mul_f32_dpp v126, v166, v126 row_newbcast:2 row_mask:0xf bank_mask:0xf\n"
"v_accvgpr_write_b32 a234, v126\n"
"v_accvgpr_read_b32 v127, a235\n"
"s_nop 1\n"
"v_mul_f32_dpp v127, v166, v127 row_newbcast:3 row_mask:0xf bank_mask:0xf\n"
"v_accvgpr_write_b32 a235, v127\n"
"v_accvgpr_read_b32 v124, a236\n"
"s_nop 1\n"
"v_mul_f32_dpp v124, v166, v124 row_newbcast:0 row_mask:0xf bank_mask:0xf\n"
"v_accvgpr_write_b32 a236, v124\n"
"v_accvgpr_read_b32 v125, a237\n"
"s_nop 1\n"
"v_mul_f32_dpp v125, v166, v125 row_newbcast:1 row_mask:0xf bank_mask:0xf\n"
"v_accvgpr_write_b32 a237, v125\n"
"v_accvgpr_read_b32 v126, a238\n"
"s_nop 1\n"
"v_mul_f32_dpp v126, v166, v126 row_newbcast:2 row_mask:0xf bank_mask:0xf\n"
"v_accvgpr_write_b32 a238, v126\n"
"v_accvgpr_read_b32 v127, a239\n"
"s_nop 1\n"
"v_mul_f32_dpp v127, v166, v127 row_newbcast:3 row_mask:0xf bank_mask:0xf\n"
"v_accvgpr_write_b32 a239, v127\n"
"LSK3A_%=:\n"
"s_nop 3\n"
"buffer_load_ushort v128, v146, s[88:91], s80 offen offset:0\n"
"buffer_load_ushort v129, v146, s[88:91], s80 offen offset:256\n"
"buffer_load_ushort v130, v146, s[88:91], s80 offen offset:512\n"
"buffer_load_ushort v131, v146, s[88:91], s80 offen offset:768\n"
"buffer_load_ushort v132, v146, s[88:91], s81 offen offset:0\n"
"buffer_load_ushort v133, v146, s[88:91], s81 offen offset:256\n"
"buffer_load_ushort v134, v146, s[88:91], s81 offen offset:512\n"
"buffer_load_ushort v135, v146, s[88:91], s81 offen offset:768\n"
"buffer_load_ushort v136, v146, s[88:91], s80 offen offset:32\n"
"buffer_load_ushort v137, v146, s[88:91], s80 offen offset:288\n"
"buffer_load_ushort v138, v146, s[88:91], s80 offen offset:544\n"
"buffer_load_ushort v139, v146, s[88:91], s80 offen offset:800\n"
"buffer_load_ushort v140, v146, s[88:91], s81 offen offset:32\n"
"buffer_load_ushort v141, v146, s[88:91], s81 offen offset:288\n"
"buffer_load_ushort v142, v146, s[88:91], s81 offen offset:544\n"
"buffer_load_ushort v143, v146, s[88:91], s81 offen offset:800\n"
"s_waitcnt vmcnt(0)\n"
"v_lshl_or_b32 v128, v129, 16, v128\n"
"v_lshl_or_b32 v129, v131, 16, v130\n"
"v_lshl_or_b32 v130, v133, 16, v132\n"
"v_lshl_or_b32 v131, v135, 16, v134\n"
"v_lshl_or_b32 v136, v137, 16, v136\n"
"v_lshl_or_b32 v137, v139, 16, v138\n"
"v_lshl_or_b32 v138, v141, 16, v140\n"
"v_lshl_or_b32 v139, v143, 16, v142\n"
"v_mfma_f32_16x16x32_bf16 v[0:3], v[96:99], v[128:131], v[0:3]\n"
"s_nop 0\n"
"v_mfma_f32_16x16x32_bf16 v[4:7], v[96:99], v[136:139], v[4:7]\n"
"v_mfma_f32_16x16x32_bf16 v[32:35], v[104:107], v[128:131], v[32:35]\n"
"v_mfma_f32_16x16x32_bf16 v[36:39], v[104:107], v[136:139], v[36:39]\n"
"v_mfma_f32_16x16x32_bf16 v[64:67], v[112:115], v[128:131], v[64:67]\n"
"v_mfma_f32_16x16x32_bf16 v[68:71], v[112:115], v[136:139], v[68:71]\n"
"s_cmp_lg_u32 s83, s43\n"
"s_cbranch_scc1 LSK3B0_%=\n"
"v_mfma_f32_16x16x32_bf16 a[208:211], v[120:123], v[128:131], a[208:211]\n"
"v_mfma_f32_16x16x32_bf16 a[212:215], v[120:123], v[136:139], a[212:215]\n"
"LSK3B0_%=:\n"
"s_nop 3\n"
"buffer_load_ushort v128, v147, s[88:91], s80 offen offset:0\n"
"buffer_load_ushort v129, v147, s[88:91], s80 offen offset:256\n"
"buffer_load_ushort v130, v147, s[88:91], s80 offen offset:512\n"
"buffer_load_ushort v131, v147, s[88:91], s80 offen offset:768\n"
"buffer_load_ushort v132, v147, s[88:91], s81 offen offset:0\n"
"buffer_load_ushort v133, v147, s[88:91], s81 offen offset:256\n"
"buffer_load_ushort v134, v147, s[88:91], s81 offen offset:512\n"
"buffer_load_ushort v135, v147, s[88:91], s81 offen offset:768\n"
"buffer_load_ushort v136, v147, s[88:91], s80 offen offset:32\n"
"buffer_load_ushort v137, v147, s[88:91], s80 offen offset:288\n"
"buffer_load_ushort v138, v147, s[88:91], s80 offen offset:544\n"
"buffer_load_ushort v139, v147, s[88:91], s80 offen offset:800\n"
"buffer_load_ushort v140, v147, s[88:91], s81 offen offset:32\n"
"buffer_load_ushort v141, v147, s[88:91], s81 offen offset:288\n"
"buffer_load_ushort v142, v147, s[88:91], s81 offen offset:544\n"
"buffer_load_ushort v143, v147, s[88:91], s81 offen offset:800\n"
"s_waitcnt vmcnt(0)\n"
"v_lshl_or_b32 v128, v129, 16, v128\n"
"v_lshl_or_b32 v129, v131, 16, v130\n"
"v_lshl_or_b32 v130, v133, 16, v132\n"
"v_lshl_or_b32 v131, v135, 16, v134\n"
"v_lshl_or_b32 v136, v137, 16, v136\n"
"v_lshl_or_b32 v137, v139, 16, v138\n"
"v_lshl_or_b32 v138, v141, 16, v140\n"
"v_lshl_or_b32 v139, v143, 16, v142\n"
"v_mfma_f32_16x16x32_bf16 v[8:11], v[96:99], v[128:131], v[8:11]\n"
"s_nop 0\n"
"v_mfma_f32_16x16x32_bf16 v[12:15], v[96:99], v[136:139], v[12:15]\n"
"v_mfma_f32_16x16x32_bf16 v[40:43], v[104:107], v[128:131], v[40:43]\n"
"v_mfma_f32_16x16x32_bf16 v[44:47], v[104:107], v[136:139], v[44:47]\n"
"v_mfma_f32_16x16x32_bf16 v[72:75], v[112:115], v[128:131], v[72:75]\n"
"v_mfma_f32_16x16x32_bf16 v[76:79], v[112:115], v[136:139], v[76:79]\n"
"s_cmp_lg_u32 s83, s43\n"
"s_cbranch_scc1 LSK3B1_%=\n"
"v_mfma_f32_16x16x32_bf16 a[216:219], v[120:123], v[128:131], a[216:219]\n"
"v_mfma_f32_16x16x32_bf16 a[220:223], v[120:123], v[136:139], a[220:223]\n"
"LSK3B1_%=:\n"
"s_nop 3\n"
"buffer_load_ushort v128, v146, s[88:91], s80 offen offset:128\n"
"buffer_load_ushort v129, v146, s[88:91], s80 offen offset:384\n"
"buffer_load_ushort v130, v146, s[88:91], s80 offen offset:640\n"
"buffer_load_ushort v131, v146, s[88:91], s80 offen offset:896\n"
"buffer_load_ushort v132, v146, s[88:91], s81 offen offset:128\n"
"buffer_load_ushort v133, v146, s[88:91], s81 offen offset:384\n"
"buffer_load_ushort v134, v146, s[88:91], s81 offen offset:640\n"
"buffer_load_ushort v135, v146, s[88:91], s81 offen offset:896\n"
"buffer_load_ushort v136, v146, s[88:91], s80 offen offset:160\n"
"buffer_load_ushort v137, v146, s[88:91], s80 offen offset:416\n"
"buffer_load_ushort v138, v146, s[88:91], s80 offen offset:672\n"
"buffer_load_ushort v139, v146, s[88:91], s80 offen offset:928\n"
"buffer_load_ushort v140, v146, s[88:91], s81 offen offset:160\n"
"buffer_load_ushort v141, v146, s[88:91], s81 offen offset:416\n"
"buffer_load_ushort v142, v146, s[88:91], s81 offen offset:672\n"
"buffer_load_ushort v143, v146, s[88:91], s81 offen offset:928\n"
"s_waitcnt vmcnt(0)\n"
"v_lshl_or_b32 v128, v129, 16, v128\n"
"v_lshl_or_b32 v129, v131, 16, v130\n"
"v_lshl_or_b32 v130, v133, 16, v132\n"
"v_lshl_or_b32 v131, v135, 16, v134\n"
"v_lshl_or_b32 v136, v137, 16, v136\n"
"v_lshl_or_b32 v137, v139, 16, v138\n"
"v_lshl_or_b32 v138, v141, 16, v140\n"
"v_lshl_or_b32 v139, v143, 16, v142\n"
"v_mfma_f32_16x16x32_bf16 v[16:19], v[96:99], v[128:131], v[16:19]\n"
"s_nop 0\n"
"v_mfma_f32_16x16x32_bf16 v[20:23], v[96:99], v[136:139], v[20:23]\n"
"v_mfma_f32_16x16x32_bf16 v[48:51], v[104:107], v[128:131], v[48:51]\n"
"v_mfma_f32_16x16x32_bf16 v[52:55], v[104:107], v[136:139], v[52:55]\n"
"v_mfma_f32_16x16x32_bf16 v[80:83], v[112:115], v[128:131], v[80:83]\n"
"v_mfma_f32_16x16x32_bf16 v[84:87], v[112:115], v[136:139], v[84:87]\n"
"s_cmp_lg_u32 s83, s43\n"
"s_cbranch_scc1 LSK3B2_%=\n"
"v_mfma_f32_16x16x32_bf16 a[224:227], v[120:123], v[128:131], a[224:227]\n"
"v_mfma_f32_16x16x32_bf16 a[228:231], v[120:123], v[136:139], a[228:231]\n"
"LSK3B2_%=:\n"
"s_nop 3\n"
"buffer_load_ushort v128, v147, s[88:91], s80 offen offset:128\n"
"buffer_load_ushort v129, v147, s[88:91], s80 offen offset:384\n"
"buffer_load_ushort v130, v147, s[88:91], s80 offen offset:640\n"
"buffer_load_ushort v131, v147, s[88:91], s80 offen offset:896\n"
"buffer_load_ushort v132, v147, s[88:91], s81 offen offset:128\n"
"buffer_load_ushort v133, v147, s[88:91], s81 offen offset:384\n"
"buffer_load_ushort v134, v147, s[88:91], s81 offen offset:640\n"
"buffer_load_ushort v135, v147, s[88:91], s81 offen offset:896\n"
"buffer_load_ushort v136, v147, s[88:91], s80 offen offset:160\n"
"buffer_load_ushort v137, v147, s[88:91], s80 offen offset:416\n"
"buffer_load_ushort v138, v147, s[88:91], s80 offen offset:672\n"
"buffer_load_ushort v139, v147, s[88:91], s80 offen offset:928\n"
"buffer_load_ushort v140, v147, s[88:91], s81 offen offset:160\n"
"buffer_load_ushort v141, v147, s[88:91], s81 offen offset:416\n"
"buffer_load_ushort v142, v147, s[88:91], s81 offen offset:672\n"
"buffer_load_ushort v143, v147, s[88:91], s81 offen offset:928\n"
"s_waitcnt vmcnt(0)\n"
"v_lshl_or_b32 v128, v129, 16, v128\n"
"v_lshl_or_b32 v129, v131, 16, v130\n"
"v_lshl_or_b32 v130, v133, 16, v132\n"
"v_lshl_or_b32 v131, v135, 16, v134\n"
"v_lshl_or_b32 v136, v137, 16, v136\n"
"v_lshl_or_b32 v137, v139, 16, v138\n"
"v_lshl_or_b32 v138, v141, 16, v140\n"
"v_lshl_or_b32 v139, v143, 16, v142\n"
"v_mfma_f32_16x16x32_bf16 v[24:27], v[96:99], v[128:131], v[24:27]\n"
"s_nop 0\n"
"v_mfma_f32_16x16x32_bf16 v[28:31], v[96:99], v[136:139], v[28:31]\n"
"v_mfma_f32_16x16x32_bf16 v[56:59], v[104:107], v[128:131], v[56:59]\n"
"v_mfma_f32_16x16x32_bf16 v[60:63], v[104:107], v[136:139], v[60:63]\n"
"v_mfma_f32_16x16x32_bf16 v[88:91], v[112:115], v[128:131], v[88:91]\n"
"v_mfma_f32_16x16x32_bf16 v[92:95], v[112:115], v[136:139], v[92:95]\n"
"s_cmp_lg_u32 s83, s43\n"
"s_cbranch_scc1 LSK3B3_%=\n"
"v_mfma_f32_16x16x32_bf16 a[232:235], v[120:123], v[128:131], a[232:235]\n"
"v_mfma_f32_16x16x32_bf16 a[236:239], v[120:123], v[136:139], a[236:239]\n"
"LSK3B3_%=:\n"
"s_nop 3\n"
"s_nop 9\n"
"s_add_u32 s80, s80, 8192\n"
"s_add_u32 s81, s81, 8192\n"
"s_add_u32 s82, s82, 1\n"
"s_cmp_lt_u32 s82, 32\n"
"s_cbranch_scc1 LSLOOP_%=\n"
"v_mov_b32 v154, v155\n"
"s_nop 1\n"
"v_permlane32_swap_b32 v155, v154\n"
"v_add_f32 v155, v155, v154\n"
"v_mov_b32 v154, v155\n"
"s_nop 1\n"
"v_permlane16_swap_b32 v155, v154\n"
"v_add_f32 v155, v155, v154\n"
"v_mov_b32 v196, v155\n"
"v_mov_b32 v158, v159\n"
"s_nop 1\n"
"v_permlane32_swap_b32 v159, v158\n"
"v_add_f32 v159, v159, v158\n"
"v_mov_b32 v158, v159\n"
"s_nop 1\n"
"v_permlane16_swap_b32 v159, v158\n"
"v_add_f32 v159, v159, v158\n"
"v_mov_b32 v198, v159\n"
"v_mov_b32 v162, v163\n"
"s_nop 1\n"
"v_permlane32_swap_b32 v163, v162\n"
"v_add_f32 v163, v163, v162\n"
"v_mov_b32 v162, v163\n"
"s_nop 1\n"
"v_permlane16_swap_b32 v163, v162\n"
"v_add_f32 v163, v163, v162\n"
"v_mov_b32 v200, v163\n"
"v_mov_b32 v166, v167\n"
"s_nop 1\n"
"v_permlane32_swap_b32 v167, v166\n"
"v_add_f32 v167, v167, v166\n"
"v_mov_b32 v166, v167\n"
"s_nop 1\n"
"v_permlane16_swap_b32 v167, v166\n"
"v_add_f32 v167, v167, v166\n"
"v_mov_b32 v202, v167\n"
"v_sub_f32 v192, 0, v164\n"
"v_mov_b32 v96, 0\n"
"v_mov_b32 v97, 0\n"
"v_mov_b32 v98, 0\n"
"v_mov_b32 v99, 0\n"
"v_mov_b32 v100, 0\n"
"v_mov_b32 v101, 0\n"
"v_mov_b32 v102, 0\n"
"v_mov_b32 v103, 0\n"
"v_mov_b32 v104, 0\n"
"v_mov_b32 v105, 0\n"
"v_mov_b32 v106, 0\n"
"v_mov_b32 v107, 0\n"
"v_mov_b32 v108, 0\n"
"v_mov_b32 v109, 0\n"
"v_mov_b32 v110, 0\n"
"v_mov_b32 v111, 0\n"
"v_mov_b32 v112, 0\n"
"v_mov_b32 v113, 0\n"
"v_mov_b32 v114, 0\n"
"v_mov_b32 v115, 0\n"
"v_mov_b32 v116, 0\n"
"v_mov_b32 v117, 0\n"
"v_mov_b32 v118, 0\n"
"v_mov_b32 v119, 0\n"
"v_mov_b32 v120, 0\n"
"v_mov_b32 v121, 0\n"
"v_mov_b32 v122, 0\n"
"v_mov_b32 v123, 0\n"
"v_mov_b32 v204, 0\n"
"v_mov_b32 v205, 0\n"
"s_nop 3\n"
"s_mov_b32 s68, 0\n"
"s_cmp_eq_u32 s47, 4\n"
"s_cbranch_scc1 LBACK0_%=\n"
"s_cmp_eq_u32 s47, 8\n"
"s_cbranch_scc1 LBACK1_%=\n"
"s_cmp_eq_u32 s47, 12\n"
"s_cbranch_scc1 LBACK2_%=\n"
"s_branch LBACK3_%=\n"
"LFIN_%=:\n"
    :: [kalo]"s"((unsigned)ka), [kahi]"s"((unsigned)(ka >> 32)), [wg]"s"(blockIdx.x), [wid]"s"(wid)
    : "v0","v1","v2","v3","v4","v5","v6","v7","v8","v9","v10","v11","v12","v13","v14","v15","v16","v17","v18","v19","v20","v21","v22","v23","v24","v25","v26","v27","v28","v29","v30","v31","v32","v33","v34","v35","v36","v37","v38","v39","v40","v41","v42","v43","v44","v45","v46","v47","v48","v49","v50","v51","v52","v53","v54","v55","v56","v57","v58","v59","v60","v61","v62","v63","v64","v65","v66","v67","v68","v69","v70","v71","v72","v73","v74","v75","v76","v77","v78","v79","v80","v81","v82","v83","v84","v85","v86","v87","v88","v89","v90","v91","v92","v93","v94","v95","v96","v97","v98","v99","v100","v101","v102","v103","v104","v105","v106","v107","v108","v109","v110","v111","v112","v113","v114","v115","v116","v117","v118","v119","v120","v121","v122","v123","v124","v125","v126","v127","v128","v129","v130","v131","v132","v133","v134","v135","v136","v137","v138","v139","v140","v141","v142","v143","v144","v145","v146","v147","v148","v149","v150","v151","v152","v153","v154","v155","v156","v157","v158","v159","v160","v161","v162","v163","v164","v165","v166","v167","v168","v169","v170","v171","v172","v173","v174","v175","v176","v177","v178","v179","v180","v181","v182","v183","v184","v185","v186","v187","v188","v189","v190","v191","v192","v193","v194","v195","v196","v197","v198","v199","v200","v201","v202","v203","v204","v205","v206","v207","v208","v209","v210","v211","v212","v213","v214","v215","v216","v217","v218","v219","v220","v221","v222","v223","v224","v225","v226","v227","v228","v229","v230","v231","v232","v233","v234","v235","v236","v237","v238","v239","v240","v241","v242","v243","v244","v245","v246","v247","v248","v249","v250","v251","v252","v253","v254","v255","a0","a1","a2","a3","a4","a5","a6","a7","a8","a9","a10","a11","a12","a13","a14","a15","a16","a17","a18","a19","a20","a21","a22","a23","a24","a25","a26","a27","a28","a29","a30","a31","a32","a33","a34","a35","a36","a37","a38","a39","a40","a41","a42","a43","a44","a45","a46","a47","a48","a49","a50","a51","a52","a53","a54","a55","a56","a57","a58","a59","a60","a61","a62","a63","a64","a65","a66","a67","a68","a69","a70","a71","a72","a73","a74","a75","a76","a77","a78","a79","a80","a81","a82","a83","a84","a85","a86","a87","a88","a89","a90","a91","a92","a93","a94","a95","a96","a97","a98","a99","a100","a101","a102","a103","a104","a105","a106","a107","a108","a109","a110","a111","a112","a113","a114","a115","a116","a117","a118","a119","a120","a121","a122","a123","a124","a125","a126","a127","a128","a129","a130","a131","a132","a133","a134","a135","a136","a137","a138","a139","a140","a141","a142","a143","a144","a145","a146","a147","a148","a149","a150","a151","a152","a153","a154","a155","a156","a157","a158","a159","a160","a161","a162","a163","a164","a165","a166","a167","a168","a169","a170","a171","a172","a173","a174","a175","a176","a177","a178","a179","a180","a181","a182","a183","a184","a185","a186","a187","a188","a189","a190","a191","a192","a193","a194","a195","a196","a197","a198","a199","a200","a201","a202","a203","a204","a205","a206","a207","a208","a209","a210","a211","a212","a213","a214","a215","a216","a217","a218","a219","a220","a221","a222","a223","a224","a225","a226","a227","a228","a229","a230","a231","a232","a233","a234","a235","a236","a237","a238","a239","a240","a241","a242","a243","a244","a245","a246","a247","a248","a249","a250","a251","a252","a253","a254","a255","s6","s7","s8","s9","s10","s11","s12","s13","s14","s15","s16","s17","s18","s19","s20","s21","s22","s23","s24","s25","s26","s27","s28","s29","s30","s31","s32","s33","s34","s35","s36","s37","s38","s39","s40","s41","s42","s43","s44","s45","s46","s47","s48","s49","s50","s51","s52","s53","s54","s55","s56","s57","s58","s59","s60","s61","s62","s63","s64","s65","s66","s67","s68","s69","s70","s71","s72","s73","s74","s75","s76","s77","s78","s79","s80","s81","s82","s83","s84","s85","s86","s87","s88","s89","s90","s91","s92","s93","s94","s95","s96","s97","s98","s99","s100","vcc","scc","m0","memory");
}
extern "C" void kernel_launch(void* const* d_in, const int* in_sizes, int n_in, void* d_out, int out_size, void* d_ws, size_t ws_size, hipStream_t stream) {
  char* xch = (char*)d_ws + ((ws_size - (size_t)270532608) & ~(size_t)0xfffff);
  (void)hipFuncSetAttribute((const void*)fused_attn, hipFuncAttributeMaxDynamicSharedMemorySize, 163840);
  hipLaunchKernelGGL(fused_attn, dim3(256), dim3(256), 163840, stream,
                     (const char*)d_in[0], (const char*)d_in[1], (const char*)d_in[2], (const char*)d_in[3], (const char*)d_in[4],
                     (const char*)d_in[5], (const char*)d_in[6], (const char*)d_in[7], (const char*)d_in[8], (const char*)d_in[9], xch, (char*)d_out, (char*)d_ws);
}
```

```cpp
#include <hip/hip_runtime.h>
#include <hip/hip_bf16.h>
#include <stdint.h>

__global__ __launch_bounds__(256, 1)
void fused_attn(const char* xA, const char* kA, const char* vA, const char* wA, const char* uA,
                const char* xB, const char* kB, const char* vB, const char* wB, const char* uB, char* xch, char* out, char* flags) {
  extern __shared__ __attribute__((aligned(16))) char lds[];
  const unsigned wid = __builtin_amdgcn_readfirstlane(threadIdx.x >> 6);
  const unsigned long long ka = (unsigned long long)__builtin_amdgcn_kernarg_segment_ptr();
  asm volatile(
"s_memtime s[64:65]\n"
"s_memrealtime s[66:67]\n"
"s_bfe_u32 s10, %[wg], 0x10002\n"
"s_mul_i32 s40, s10, 40\n"
"s_add_u32 s40, s40, %[kalo]\n"
"s_addc_u32 s41, %[kahi], 0\n"
"s_mov_b32 s78, %[kalo]\n"
"s_mov_b32 s79, %[kahi]\n"
"s_load_dwordx2 s[24:25], s[40:41], 0\n"
"s_load_dwordx2 s[28:29], s[40:41], 24\n"
"s_load_dwordx2 s[6:7], s[78:79], 0x60\n"
"s_load_dwordx2 s[16:17], s[40:41], 8\n"
"s_load_dwordx2 s[20:21], s[40:41], 16\n"
"s_load_dwordx2 s[8:9], s[40:41], 32\n"
"s_load_dwordx4 s[12:15], s[78:79], 0x50\n"
"s_mov_b32 s42, %[wid]\n"
"s_and_b32 s11, %[wg], 1\n"
"s_lshl_b32 s11, s11, 3\n"
"s_lshr_b32 s60, %[wg], 5\n"
"s_add_u32 s11, s11, s60\n"
"s_bfe_u32 s61, %[wg], 0x10001\n"
"s_bfe_u32 s60, %[wg], 0x20003\n"
"s_lshl2_add_u32 s61, s61, s60\n"
"s_getpc_b64 s[80:81]\n"
"LCP_%=:\n"
"s_mov_b32 s82, LEND_%=-LCP_%=\n"
"s_mov_b32 s83, 0x20000\n"
"v_mbcnt_lo_u32_b32 v2, -1, 0\n"
"v_lshlrev_b32 v2, 7, v2\n"
"s_lshr_b32 s60, %[wg], 3\n"
"s_lshl2_add_u32 s60, s60, s42\n"
"s_mul_i32 s60, s60, 640\n"
"s_mov_b64 exec, 31\n"
"buffer_load_dword v3, v2, s[80:83], s60 offen\n"
"s_mov_b64 exec, -1\n"
"s_mov_b32 s19, 0x20000\n"
"s_mov_b32 s23, 0x20000\n"
"s_mov_b32 s27, 0x20000\n"
"s_mov_b32 s31, 0x20000\n"
"s_mov_b32 s35, 0x20000\n"
"s_mov_b32 s39, 0x20000\n"
"s_mov_b32 s26, 286720\n"
"s_lshl_b32 s44, s42, 12\n"
"s_mov_b32 s50, s44\n"
"s_mul_i32 s51, s42, 7168\n"
"s_add_u32 s51, s51, 0x10000\n"
"s_mov_b32 s74, 0x8000\n"
"s_mov_b32 s58, 524288\n"
"s_mov_b32 s47, 0\n"
"s_add_u32 s60, s11, s61\n"
"s_and_b32 s60, s60, 0\n"
"s_mul_i32 s60, s60, 3\n"
"s_lshl_b32 s49, s60, 7\n"
"s_lshl_b32 s48, s60, 19\n"
"v_mbcnt_lo_u32_b32 v4, -1, 0\n"
"v_mbcnt_hi_u32_b32 v4, -1, v4\n"
"v_and_b32 v5, 15, v4\n"
"v_lshrrev_b32 v6, 4, v4\n"
"v_lshrrev_b32 v7, 3, v4\n"
"v_and_b32 v8, 7, v4\n"
"v_lshlrev_b32 v9, 1, v6\n"
"v_xor_b32 v9, v9, v8\n"
"v_lshlrev_b32 v9, 4, v9\n"
"s_mul_i32 s60, s42, 71680\n"
"v_mul_u32_u24 v10, 1280, v7\n"
"v_add3_u32 v248, v10, v9, s60\n"
"v_add_u32 v249, 9216, v248\n"
"v_add_u32 v254, 18432, v248\n"
"v_add_u32 v255, 27648, v248\n"
"v_and_b32 v10, 2, v7\n"
"v_xor_b32 v240, v10, v8\n"
"v_lshlrev_b32 v240, 4, v240\n"
"v_bfe_u32 v10, v7, 0, 1\n"
"v_lshl_add_u32 v240, v10, 8, v240\n"
"v_bfe_u32 v10, v7, 1, 1\n"
"v_lshl_add_u32 v240, v10, 9, v240\n"
"v_bfe_u32 v10, v7, 2, 1\n"
"v_lshl_add_u32 v240, v10, 7, v240\n"
"s_and_b32 s60, s42, 1\n"
"v_lshl_add_u32 v240, s60, 15, v240\n"
"s_lshr_b32 s60, s42, 1\n"
"v_lshl_add_u32 v240, s60, 16, v240\n"
"v_and_b32 v10, 1, v6\n"
"v_xor_b32 v10, v10, v8\n"
"v_lshlrev_b32 v10, 4, v10\n"
"v_lshl_add_u32 v10, v7, 13, v10\n"
"s_lshl_b32 s60, s42, 17\n"
"v_add_u32 v10, s60, v10\n"
"v_add_u32 v224, 1024, v10\n"
"v_add_u32 v225, 128, v10\n"
"v_xor_b32 v10, 64, v10\n"
"v_add_u32 v226, 64512, v10\n"
"v_add_u32 v227, 63616, v10\n"
"s_mul_i32 s40, s11, 286720\n"
"s_lshl_b32 s41, s61, 10\n"
"s_sub_u32 s30, 5243904, s41\n"
"s_sub_u32 s41, s41, 1024\n"
"s_ashr_i32 s60, s41, 31\n"
"s_lshl4_add_u32 s92, s10, s11\n"
"s_lshl_b32 s93, s92, 9\n"
"v_mov_b32 v0, 0\n"
"s_waitcnt lgkmcnt(0)\n"
"s_add_u32 s24, s24, s40\n"
"s_addc_u32 s25, s25, 0\n"
"s_and_b32 s25, s25, 0xffff\n"
"s_add_u32 s28, s28, s41\n"
"s_addc_u32 s29, s29, s60\n"
"s_and_b32 s29, s29, 0xffff\n"
"s_add_u32 s6, s6, s93\n"
"s_addc_u32 s7, s7, 0\n"
"buffer_load_dword v12, v248, s[24:27], s49 offen\n"
"buffer_load_dword v13, v224, s[28:31], s48 offen\n"
"global_load_dword v1, v0, s[6:7] offset:128 sc1\n"
"s_mov_b32 m0, s51\n"
"s_nop 0\n"
"buffer_load_dwordx4 v248, s[24:27], s49 offen lds\n"
"buffer_load_dwordx4 v249, s[24:27], s49 offen offset:1024 lds\n"
"buffer_load_dwordx4 v254, s[24:27], s49 offen offset:2048 lds\n"
"buffer_load_dwordx4 v255, s[24:27], s49 offen offset:3072 lds\n"
"s_add_u32 m0, s51, 4096\n"
"s_add_u32 s40, s49, 40960\n"
"buffer_load_dwordx4 v248, s[24:27], s40 offen lds\n"
"buffer_load_dwordx4 v249, s[24:27], s40 offen offset:1024 lds\n"
"buffer_load_dwordx4 v254, s[24:27], s40 offen offset:2048 lds\n"
"s_add_u32 s49, s49, 128\n"
"s_sub_u32 s41, s49, 1280\n"
"s_min_u32 s49, s49, s41\n"
"s_xor_b32 s51, s51, s74\n"
"s_mov_b32 m0, s50\n"
"s_nop 0\n"
"buffer_load_dwordx4 v224, s[28:31], s48 offen lds\n"
"buffer_load_dwordx4 v225, s[28:31], s48 offen offset:1024 lds\n"
"buffer_load_dwordx4 v226, s[28:31], s48 offen offset:2048 lds\n"
"buffer_load_dwordx4 v227, s[28:31], s48 offen offset:3072 lds\n"
"s_xor_b32 s50, s50, s74\n"
"s_lshl2_add_u32 s63, s11, s42\n"
"s_mul_i32 s63, s63, 10240\n"
"s_and_b32 s78, s42, 1\n"
"s_lshl_b32 s43, s78, 6\n"
"s_mov_b32 s77, 0x80000000\n"
"s_mov_b32 s75, 0x10000\n"
"s_mov_b32 s73, 0xffff0000\n"
"s_mov_b32 s46, 0x3e0293ee\n"
"s_mov_b32 s45, 0x63800000\n"
"s_mov_b32 s62, 9\n"
"v_lshl_add_u32 v242, v4, 4, s44\n"
"s_lshr_b32 s79, s42, 1\n"
"s_lshl4_add_u32 s76, s79, s78\n"
"s_lshl_b32 s76, s76, 10\n"
"s_add_u32 s76, s76, 0x20000\n"
"s_sub_u32 s76, s76, s44\n"
"v_lshlrev_b32 v9, 6, v6\n"
"v_xor_b32 v9, v9, v242\n"
"v_add_u32 v243, s76, v9\n"
"v_and_b32 v9, 6, v5\n"
"s_xor_b32 m0, s50, 0xc000\n"
"s_add_u32 s40, s48, 256\n"
"buffer_load_dwordx4 v224, s[28:31], s40 offen lds\n"
"v_xor_b32 v9, v9, v6\n"
"v_lshlrev_b32 v9, 4, v9\n"
"v_lshl_add_u32 v10, v5, 7, v9\n"
"s_mul_i32 s79, s42, 6144\n"
"s_add_u32 s79, s79, 0x10000\n"
"v_add_u32 v246, s79, v10\n"
"v_xor_b32 v247, 64, v246\n"
"s_lshr_b32 s79, s42, 1\n"
"s_lshl_b32 s79, s79, 11\n"
"buffer_load_dwordx4 v225, s[28:31], s40 offen offset:1024 lds\n"
"s_add_u32 s79, s79, 90112\n"
"v_add_u32 v252, s79, v10\n"
"v_xor_b32 v253, 64, v252\n"
"s_xor_b32 s79, s42, 1\n"
"s_mul_i32 s79, s79, 6144\n"
"s_add_u32 s79, s79, 0x10000\n"
"v_add_u32 v250, s79, v10\n"
"v_xor_b32 v251, 64, v250\n"
"s_mul_i32 s79, s78, 8256\n"
"buffer_load_dwordx4 v226, s[28:31], s40 offen offset:2048 lds\n"
"v_lshlrev_b32 v9, 10, v6\n"
"v_bfe_u32 v10, v4, 2, 2\n"
"v_lshl_add_u32 v9, v10, 7, v9\n"
"v_and_b32 v10, 3, v4\n"
"v_lshl_add_u32 v9, v10, 3, v9\n"
"v_bfe_u32 v10, v4, 3, 2\n"
"v_lshl_add_u32 v9, v10, 5, v9\n"
"s_add_u32 s79, s79, 0x10000\n"
"v_xor_b32 v232, s79, v9\n"
"buffer_load_dwordx4 v227, s[28:31], s40 offen offset:3072 lds\n"
"s_cmp_eq_u32 s47, 8\n"
"s_cselect_b32 s41, s52, s58\n"
"s_add_u32 s48, s48, s41\n"
"s_sub_u32 s41, s48, 5242880\n"
"s_min_u32 s48, s48, s41\n"
"v_xor_b32 v233, 32, v232\n"
"v_xor_b32 v234, 64, v232\n"
"v_xor_b32 v235, 96, v232\n"
"v_xor_b32 v236, 0x2000, v232\n"
"v_xor_b32 v237, 0x2000, v233\n"
"v_xor_b32 v238, 0x2000, v234\n"
"v_xor_b32 v239, 0x2000, v235\n"
"v_lshlrev_b32 v9, 11, v6\n"
"v_bfe_u32 v10, v4, 2, 2\n"
"s_mov_b32 m0, s50\n"
"s_nop 0\n"
"buffer_load_dwordx4 v224, s[28:31], s48 offen lds\n"
"v_lshl_add_u32 v9, v10, 7, v9\n"
"v_and_b32 v10, 1, v4\n"
"v_lshl_add_u32 v9, v10, 3, v9\n"
"v_bfe_u32 v10, v4, 1, 1\n"
"v_lshl_add_u32 v9, v10, 5, v9\n"
"v_bfe_u32 v10, v4, 3, 1\n"
"v_lshl_add_u32 v9, v10, 4, v9\n"
"v_bfe_u32 v10, v4, 4, 1\n"
"v_lshl_add_u32 v9, v10, 6, v9\n"
"buffer_load_dwordx4 v225, s[28:31], s48 offen offset:1024 lds\n"
"v_xor_b32 v244, s43, v9\n"
"v_xor_b32 v245, 16, v244\n"
"s_lshl3_add_u32 s79, s11, s61\n"
"s_lshl_b32 s79, s79, 20\n"
"s_sub_u32 s79, s79, 262144\n"
"s_ashr_i32 s60, s79, 31\n"
"s_add_u32 s16, s16, s79\n"
"s_addc_u32 s17, s17, s60\n"
"s_and_b32 s17, s17, 0xffff\n"
"buffer_load_dwordx4 v226, s[28:31], s48 offen offset:2048 lds\n"
"s_mov_b32 s18, 1310720\n"
"s_mov_b64 s[84:85], s[16:17]\n"
"s_mov_b64 s[86:87], s[18:19]\n"
"s_add_u32 s20, s20, s79\n"
"s_addc_u32 s21, s21, s60\n"
"s_and_b32 s21, s21, 0xffff\n"
"s_mov_b32 s22, 1310720\n"
"s_mov_b64 s[88:89], s[20:21]\n"
"s_mov_b64 s[90:91], s[22:23]\n"
"buffer_load_dwordx4 v227, s[28:31], s48 offen offset:3072 lds\n"
"s_xor_b32 s50, s50, s74\n"
"s_mov_b32 s57, 122880\n"
"s_bfe_u32 s55, %[wg], 65536\n"
"s_mul_i32 s55, s55, 4096\n"
"s_xor_b32 s57, s57, s55\n"
"s_bfe_u32 s55, %[wg], 65537\n"
"s_mul_i32 s55, s55, 131072\n"
"s_xor_b32 s57, s57, s55\n"
"s_bfe_u32 s55, %[wg], 65541\n"
"s_mul_i32 s55, s55, 1024\n"
"s_mov_b32 m0, s51\n"
"s_nop 0\n"
"buffer_load_dwordx4 v248, s[24:27], s49 offen lds\n"
"s_xor_b32 s57, s57, s55\n"
"s_bfe_u32 s55, %[wg], 65542\n"
"s_mul_i32 s55, s55, 2048\n"
"s_xor_b32 s57, s57, s55\n"
"s_and_b32 s54, %[wg], 145\n"
"s_bcnt1_i32_b32 s54, s54\n"
"s_and_b32 s54, s54, 1\n"
"s_lshl_b32 s54, s54, 13\n"
"s_xor_b32 s57, s57, s54\n"
"buffer_load_dwordx4 v249, s[24:27], s49 offen offset:1024 lds\n"
"s_lshl_b32 s54, s54, 1\n"
"s_sub_u32 s54, 7168, s54\n"
"s_and_b32 s91, %[wg], 42\n"
"s_bcnt1_i32_b32 s91, s91\n"
"s_and_b32 s91, s91, 1\n"
"s_lshl_b32 s91, s91, 14\n"
"s_xor_b32 s57, s57, s91\n"
"s_lshl_b32 s91, s91, 1\n"
"s_sub_u32 s91, 14336, s91\n"
"buffer_load_dwordx4 v254, s[24:27], s49 offen offset:2048 lds\n"
"s_mov_b32 s55, 385024\n"
"s_xor_b32 s56, s55, s57\n"
"s_mov_b32 s59, 1024\n"
"s_add_u32 s52, s11, s61\n"
"s_and_b32 s52, s52, 0\n"
"s_mul_i32 s52, s52, 3\n"
"s_add_u32 s52, s52, 1\n"
"s_lshl_b32 s53, s52, 7\n"
"s_lshl_b32 s52, s52, 19\n"
"buffer_load_dwordx4 v255, s[24:27], s49 offen offset:3072 lds\n"
"s_add_u32 s52, s52, 512\n"
"s_mul_i32 s79, s61, 655360\n"
"s_add_u32 s8, s8, s79\n"
"s_addc_u32 s9, s9, 0\n"
"s_and_b32 s9, s9, 0xffff\n"
"s_add_u32 s12, s12, 0x1fffff\n"
"s_addc_u32 s13, s13, 0\n"
"s_and_b32 s12, s12, 0xffe00000\n"
"s_mul_i32 s96, s42, 3\n"
"s_add_u32 m0, s51, 4096\n"
"s_add_u32 s40, s49, 40960\n"
"buffer_load_dwordx4 v248, s[24:27], s40 offen lds\n"
"s_and_b32 s96, s96, 5\n"
"s_cmp_eq_u32 s42, 3\n"
"s_cselect_b32 s96, 5, s96\n"
"s_lshl_b32 s79, s61, 8\n"
"s_add_u32 s96, s96, s79\n"
"s_add_u32 s96, s96, 160\n"
"s_lshl_b32 s96, s96, 12\n"
"s_mul_i32 s79, s92, 8388608\n"
"s_add_u32 s32, s12, s79\n"
"s_addc_u32 s33, s13, 0\n"
"buffer_load_dwordx4 v249, s[24:27], s40 offen offset:1024 lds\n"
"s_and_b32 s33, s33, 0xffff\n"
"s_mov_b32 s34, 8388608\n"
"s_mul_i32 s79, s11, 573440\n"
"s_mul_i32 s60, s10, 1280\n"
"s_sub_u32 s38, 573440, s60\n"
"s_add_u32 s79, s79, s60\n"
"s_add_u32 s36, s14, s79\n"
"s_addc_u32 s37, s15, 0\n"
"buffer_load_dwordx4 v254, s[24:27], s40 offen offset:2048 lds\n"
"s_add_u32 s49, s49, 128\n"
"s_sub_u32 s41, s49, 1280\n"
"s_min_u32 s49, s49, s41\n"
"s_xor_b32 s51, s51, s74\n"
"s_and_b32 s37, s37, 0xffff\n"
"s_lshl2_add_u32 s93, s61, s42\n"
"s_add_u32 s95, s61, 1\n"
"s_and_b32 s95, s95, 7\n"
"s_lshl_b32 s95, s95, 7\n"
"s_add_u32 s100, s61, 2\n"
"s_and_b32 s100, s100, 7\n"
"s_lshl_b32 s100, s100, 7\n"
"s_lshl_b32 s60, s61, 7\n"
"s_add_u32 s60, s60, s43\n"
"s_mul_i32 s97, s42, 122880\n"
"s_add_u32 s97, s97, s60\n"
"s_lshr_b32 s98, s42, 1\n"
"s_add_u32 s98, s98, 12\n"
"s_mul_i32 s98, s98, 40960\n"
"s_add_u32 s98, s98, s60\n"
"s_mul_i32 s60, s93, 37\n"
"s_lshr_b32 s60, s60, 8\n"
"s_mul_i32 s79, s60, 7\n"
"s_sub_u32 s79, s93, s79\n"
"s_lshl_b32 s94, s60, 4\n"
"s_add_u32 s94, s94, s79\n"
"s_lshl_b32 s94, s94, 10\n"
"s_add_u32 s94, s94, 524288\n"
"s_mul_i32 s99, s60, 3\n"
"s_lshr_b32 s78, s79, 1\n"
"s_add_u32 s99, s99, s78\n"
"s_lshr_b32 s78, s60, 1\n"
"s_add_u32 s78, s78, 12\n"
"s_cmp_eq_u32 s79, 6\n"
"s_cselect_b32 s99, s78, s99\n"
"s_mul_i32 s99, s99, 40960\n"
"s_xor_b32 s79, s79, s60\n"
"s_and_b32 s79, s79, 1\n"
"s_lshl_b32 s79, s79, 6\n"
"s_add_u32 s99, s99, s79\n"
"s_add_u32 s99, s99, 1024\n"
"s_cmp_eq_u32 s61, 7\n"
"s_cselect_b32 s99, 0x40000000, s99\n"
"s_cselect_b32 s94, 0x40000000, s94\n"
"s_waitcnt vmcnt(27)\n"
"v_and_b32 v12, 0x7fff0000, v12\n"
"v_and_b32 v13, 0x7fff0000, v13\n"
"s_nop 0\n"
"v_max_u32_dpp v12, v12, v12 quad_perm:[1,0,3,2] row_mask:0xf bank_mask:0xf\n"
"v_max_u32_dpp v13, v13, v13 quad_perm:[1,0,3,2] row_mask:0xf bank_mask:0xf\n"
"s_nop 0\n"
"v_max_u32_dpp v12, v12, v12 quad_perm:[2,3,0,1] row_mask:0xf bank_mask:0xf\n"
"v_max_u32_dpp v13, v13, v13 quad_perm:[2,3,0,1] row_mask:0xf bank_mask:0xf\n"
"s_nop 0\n"
"v_max_u32_dpp v12, v12, v12 row_half_mirror row_mask:0xf bank_mask:0xf\n"
"v_max_u32_dpp v13, v13, v13 row_half_mirror row_mask:0xf bank_mask:0xf\n"
"s_nop 0\n"
"v_min_u32_dpp v12, v12, v12 row_mirror row_mask:0xf bank_mask:0xf\n"
"v_min_u32_dpp v13, v13, v13 row_mirror row_mask:0xf bank_mask:0xf\n"
"s_nop 0\n"
"v_min_u32_dpp v12, v12, v12 row_bcast:15 row_mask:0xa bank_mask:0xf\n"
"v_min_u32_dpp v13, v13, v13 row_bcast:15 row_mask:0xa bank_mask:0xf\n"
"s_nop 0\n"
"v_min_u32_dpp v12, v12, v12 row_bcast:31 row_mask:0xc bank_mask:0xf\n"
"v_min_u32_dpp v13, v13, v13 row_bcast:31 row_mask:0xc bank_mask:0xf\n"
"s_nop 0\n"
"v_mul_f32 v12, v12, v13\n"
"v_mul_f32 v12, 0x43c80000, v12\n"
"v_min_f32 v12, 0x71800000, v12\n"
"v_mul_f32 v13, s46, v12\n"
"s_nop 0\n"
"v_readlane_b32 s70, v12, 63\n"
"v_readlane_b32 s71, v12, 63\n"
"v_readlane_b32 s69, v13, 63\n"
"s_nop 1\n"
"v_mov_b64 v[228:229], s[70:71]\n"
"v_mov_b64 v[230:231], s[70:71]\n"
"s_waitcnt vmcnt(26)\n"
"v_readfirstlane_b32 s92, v1\n"
"v_mov_b64 v[0:1], s[70:71]\n"
"v_mov_b64 v[2:3], s[70:71]\n"
"s_mov_b32 s70, s46\n"
"s_xor_b32 s71, s69, s77\n"
"s_mov_b32 s65, s64\n"
"s_add_u32 s66, s66, 2800\n"
"s_mov_b32 s67, s66\n"
"s_mov_b32 s72, 118\n"
"s_mov_b32 s83, 0x20000\n"
"s_nop 2\n"
"s_mul_i32 s60, s92, 0x9e3779b1\n"
"s_add_u32 s60, s60, 0x7f4a7c15\n"
"s_waitcnt vmcnt(15)\n"
"s_barrier\n"
"ds_read_b128 a[136:139], v246 offset:0\n"
"ds_read_b128 a[140:143], v247 offset:0\n"
"ds_read_b64_tr_b16 a[192:193], v244 offset:0\n"
"ds_read_b64_tr_b16 a[194:195], v244 offset:512\n"
"ds_read_b64_tr_b16 a[196:197], v244 offset:8192\n"
"ds_read_b64_tr_b16 a[198:199], v244 offset:8704\n"
"ds_read_b64_tr_b16 a[200:201], v245 offset:0\n"
"ds_read_b64_tr_b16 a[202:203], v245 offset:512\n"
"ds_read_b64_tr_b16 a[204:205], v245 offset:8192\n"
"ds_read_b64_tr_b16 a[206:207], v245 offset:8704\n"
"ds_read_b64_tr_b16 a[208:209], v244 offset:1024\n"
"ds_read_b64_tr_b16 a[210:211], v244 offset:1536\n"
"ds_read_b64_tr_b16 a[212:213], v244 offset:9216\n"
"ds_read_b64_tr_b16 a[214:215], v244 offset:9728\n"
"s_waitcnt lgkmcnt(10)\n"
"v_mfma_f32_16x16x32_bf16 v[112:115], a[192:195], a[136:139], v[228:231]\n"
"ds_read_b64_tr_b16 a[216:217], v245 offset:1024\n"
"ds_read_b64_tr_b16 a[218:219], v245 offset:1536\n"
"s_waitcnt lgkmcnt(10)\n"
"v_mfma_f32_16x16x32_bf16 v[112:115], a[196:199], a[140:143], v[112:115]\n"
"ds_read_b64_tr_b16 a[220:221], v245 offset:9216\n"
"ds_read_b64_tr_b16 a[222:223], v245 offset:9728\n"
"s_waitcnt lgkmcnt(10)\n"
"v_mfma_f32_16x16x32_bf16 v[116:119], a[200:203], a[136:139], v[228:231]\n"
"s_waitcnt lgkmcnt(8)\n"
"ds_read_b128 a[144:147], v246 offset:2048\n"
"v_mfma_f32_16x16x32_bf16 v[116:119], a[204:207], a[140:143], v[116:119]\n"
"s_waitcnt lgkmcnt(7)\n"
"ds_read_b128 a[148:151], v247 offset:2048\n"
"v_mfma_f32_16x16x32_bf16 v[120:123], a[208:211], a[136:139], v[228:231]\n"
"s_waitcnt lgkmcnt(6)\n"
"ds_read_b128 a[152:155], v246 offset:4096\n"
"v_mfma_f32_16x16x32_bf16 v[120:123], a[212:215], a[140:143], v[120:123]\n"
"s_waitcnt lgkmcnt(5)\n"
"ds_read_b128 a[156:159], v247 offset:4096\n"
"v_mfma_f32_16x16x32_bf16 v[124:127], a[216:219], a[136:139], v[228:231]\n"
"s_waitcnt lgkmcnt(4)\n"
"ds_read_b128 a[160:163], v252 offset:0\n"
"v_mfma_f32_16x16x32_bf16 v[124:127], a[220:223], a[140:143], v[124:127]\n"
"s_waitcnt lgkmcnt(4)\n"
"ds_read_b128 a[164:167], v253 offset:0\n"
"v_mfma_f32_16x16x32_bf16 v[140:143], a[216:219], a[144:147], v[228:231]\n"
"s_waitcnt lgkmcnt(4)\n"
"ds_read_b128 a[168:171], v250 offset:0\n"
"v_mfma_f32_16x16x32_bf16 v[140:143], a[220:223], a[148:151], v[140:143]\n"
"ds_read_b128 a[172:175], v251 offset:0\n"
"v_mfma_f32_16x16x32_bf16 v[136:139], a[208:211], a[144:147], v[228:231]\n"
"s_waitcnt vmcnt(11)\n"
"ds_read_b128 a[176:179], v250 offset:2048\n"
"v_mfma_f32_16x16x32_bf16 v[136:139], a[212:215], a[148:151], v[136:139]\n"
"s_barrier\n"
"v_mfma_f32_16x16x32_bf16 v[132:135], a[200:203], a[144:147], v[228:231]\n"
"s_add_u32 s40, s48, 256\n"
"s_xor_b32 m0, s50, 0xc000\n"
"ds_read_b128 a[180:183], v251 offset:2048\n"
"v_mfma_f32_16x16x32_bf16 v[132:135], a[204:207], a[148:151], v[132:135]\n"
"buffer_load_dwordx4 v224, s[28:31], s40 offen lds\n"
"v_mfma_f32_16x16x32_bf16 v[128:131], a[192:195], a[144:147], v[228:231]\n"
"ds_read_b128 a[184:187], v250 offset:4096\n"
"v_mfma_f32_16x16x32_bf16 v[128:131], a[196:199], a[148:151], v[128:131]\n"
"s_waitcnt lgkmcnt(5)\n"
"ds_read_b128 a[188:191], v251 offset:4096\n"
"v_mfma_f32_16x16x32_bf16 v[144:147], a[192:195], a[152:155], v[228:231]\n"
"ds_read_b64_tr_b16 a[224:225], v244 offset:16384\n"
"v_mfma_f32_16x16x32_bf16 v[144:147], a[196:199], a[156:159], v[144:147]\n"
"ds_read_b64_tr_b16 a[226:227], v244 offset:16896\n"
"v_mfma_f32_16x16x32_bf16 v[148:151], a[200:203], a[152:155], v[228:231]\n"
"ds_read_b64_tr_b16 a[228:229], v244 offset:24576\n"
"v_mfma_f32_16x16x32_bf16 v[148:151], a[204:207], a[156:159], v[148:151]\n"
"buffer_load_dwordx4 v225, s[28:31], s40 offen offset:1024 lds\n"
"v_mfma_f32_16x16x32_bf16 v[152:155], a[208:211], a[152:155], v[228:231]\n"
"ds_read_b64_tr_b16 a[230:231], v244 offset:25088\n"
"v_mfma_f32_16x16x32_bf16 v[152:155], a[212:215], a[156:159], v[152:155]\n"
"ds_read_b64_tr_b16 a[232:233], v245 offset:16384\n"
"v_mfma_f32_16x16x32_bf16 v[156:159], a[216:219], a[152:155], v[228:231]\n"
"ds_read_b64_tr_b16 a[234:235], v245 offset:16896\n"
"v_mfma_f32_16x16x32_bf16 v[156:159], a[220:223], a[156:159], v[156:159]\n"
"ds_read_b64_tr_b16 a[236:237], v245 offset:24576\n"
"v_mfma_f32_16x16x32_bf16 v[172:175], a[216:219], a[160:163], v[228:231]\n"
"ds_read_b64_tr_b16 a[238:239], v245 offset:25088\n"
"v_mfma_f32_16x16x32_bf16 v[172:175], a[220:223], a[164:167], v[172:175]\n"
"buffer_load_dwordx4 v226, s[28:31], s40 offen offset:2048 lds\n"
"v_mfma_f32_16x16x32_bf16 v[168:171], a[208:211], a[160:163], v[228:231]\n"
"ds_read_b64_tr_b16 a[240:241], v244 offset:17408\n"
"v_mfma_f32_16x16x32_bf16 v[168:171], a[212:215], a[164:167], v[168:171]\n"
"ds_read_b64_tr_b16 a[242:243], v244 offset:17920\n"
"v_mfma_f32_16x16x32_bf16 v[164:167], a[200:203], a[160:163], v[228:231]\n"
"ds_read_b64_tr_b16 a[244:245], v244 offset:25600\n"
"v_mfma_f32_16x16x32_bf16 v[164:167], a[204:207], a[164:167], v[164:167]\n"
"buffer_load_dwordx4 v227, s[28:31], s40 offen offset:3072 lds\n"
"v_mfma_f32_16x16x32_bf16 v[160:163], a[192:195], a[160:163], v[228:231]\n"
"s_cmp_eq_u32 s47, 8\n"
"ds_read_b64_tr_b16 a[246:247], v244 offset:26112\n"
"v_xor_b32 v244, s74, v244\n"
"v_mfma_f32_16x16x32_bf16 v[160:163], a[196:199], a[164:167], v[160:163]\n"
"s_waitcnt lgkmcnt(5)\n"
"s_cselect_b32 s41, s52, s58\n"
"ds_read_b64_tr_b16 a[248:249], v245 offset:17408\n"
"v_xor_b32 v246, s74, v246\n"
"v_mfma_f32_16x16x32_bf16 v[176:179], a[192:195], a[168:171], v[228:231]\n"
"s_add_u32 s48, s48, s41\n"
"ds_read_b64_tr_b16 a[250:251], v245 offset:17920\n"
"v_xor_b32 v247, s74, v247\n"
"v_mfma_f32_16x16x32_bf16 v[176:179], a[196:199], a[172:175], v[176:179]\n"
"s_sub_u32 s41, s48, 5242880\n"
"ds_read_b64_tr_b16 a[252:253], v245 offset:25600\n"
"v_xor_b32 v252, s74, v252\n"
"v_mfma_f32_16x16x32_bf16 v[180:183], a[200:203], a[168:171], v[228:231]\n"
"s_min_u32 s48, s48, s41\n"
"ds_read_b64_tr_b16 a[254:255], v245 offset:26112\n"
"v_xor_b32 v245, s74, v245\n"
"v_mfma_f32_16x16x32_bf16 v[180:183], a[204:207], a[172:175], v[180:183]\n"
"v_xor_b32 v253, s74, v253\n"
"v_xor_b32 v250, s74, v250\n"
"v_mfma_f32_16x16x32_bf16 v[184:187], a[208:211], a[168:171], v[228:231]\n"
"s_mov_b32 m0, s50\n"
"v_xor_b32 v251, s74, v251\n"
"v_mfma_f32_16x16x32_bf16 v[184:187], a[212:215], a[172:175], v[184:187]\n"
"buffer_load_dwordx4 v224, s[28:31], s48 offen lds\n"
"v_mfma_f32_16x16x32_bf16 v[188:191], a[216:219], a[168:171], v[228:231]\n"
"v_mfma_f32_16x16x32_bf16 v[188:191], a[220:223], a[172:175], v[188:191]\n"
"v_mfma_f32_16x16x32_bf16 v[204:207], a[216:219], a[176:179], v[228:231]\n"
"v_mfma_f32_16x16x32_bf16 v[204:207], a[220:223], a[180:183], v[204:207]\n"
"buffer_load_dwordx4 v225, s[28:31], s48 offen offset:1024 lds\n"
"v_mfma_f32_16x16x32_bf16 v[200:203], a[208:211], a[176:179], v[228:231]\n"
"v_mfma_f32_16x16x32_bf16 v[200:203], a[212:215], a[180:183], v[200:203]\n"
"v_mfma_f32_16x16x32_bf16 v[196:199], a[200:203], a[176:179], v[228:231]\n"
"v_mfma_f32_16x16x32_bf16 v[196:199], a[204:207], a[180:183], v[196:199]\n"
"v_mfma_f32_16x16x32_bf16 v[192:195], a[192:195], a[176:179], v[228:231]\n"
"v_mfma_f32_16x16x32_bf16 v[192:195], a[196:199], a[180:183], v[192:195]\n"
"buffer_load_dwordx4 v226, s[28:31], s48 offen offset:2048 lds\n"
"v_mfma_f32_16x16x32_bf16 v[208:211], a[192:195], a[184:187], v[228:231]\n"
"v_mfma_f32_16x16x32_bf16 v[208:211], a[196:199], a[188:191], v[208:211]\n"
"v_mfma_f32_16x16x32_bf16 v[212:215], a[200:203], a[184:187], v[228:231]\n"
"v_mfma_f32_16x16x32_bf16 v[212:215], a[204:207], a[188:191], v[212:215]\n"
"v_mfma_f32_16x16x32_bf16 v[216:219], a[208:211], a[184:187], v[228:231]\n"
"v_mfma_f32_16x16x32_bf16 v[216:219], a[212:215], a[188:191], v[216:219]\n"
"buffer_load_dwordx4 v227, s[28:31], s48 offen offset:3072 lds\n"
"v_mfma_f32_16x16x32_bf16 v[220:223], a[216:219], a[184:187], v[228:231]\n"
"s_xor_b32 s50, s50, s74\n"
"v_mfma_f32_16x16x32_bf16 v[220:223], a[220:223], a[188:191], v[220:223]\n"
"v_mfma_f32_16x16x32_bf16 v[0:3], a[224:227], a[136:139], v[228:231]\n"
"v_mfma_f32_16x16x32_bf16 v[0:3], a[228:231], a[140:143], v[0:3]\n"
"v_mfma_f32_16x16x32_bf16 v[4:7], a[232:235], a[136:139], v[228:231]\n"
"s_waitcnt vmcnt(8) lgkmcnt(0)\n"
"v_mfma_f32_16x16x32_bf16 v[4:7], a[236:239], a[140:143], v[4:7]\n"
"s_barrier\n"
"v_mfma_f32_16x16x32_bf16 v[8:11], a[240:243], a[136:139], v[228:231]\n"
"s_mov_b32 m0, s51\n"
"ds_read_b64_tr_b16 a[192:193], v244 offset:0\n"
"v_mfma_f32_16x16x32_bf16 v[8:11], a[244:247], a[140:143], v[8:11]\n"
"buffer_load_dwordx4 v248, s[24:27], s49 offen lds\n"
"v_mfma_f32_16x16x32_bf16 v[12:15], a[248:251], a[136:139], v[228:231]\n"
"ds_read_b64_tr_b16 a[194:195], v244 offset:512\n"
"v_mfma_f32_16x16x32_bf16 v[12:15], a[252:255], a[140:143], v[12:15]\n"
"ds_read_b128 a[136:139], v246 offset:0\n"
"v_mfma_f32_16x16x32_bf16 v[28:31], a[248:251], a[144:147], v[228:231]\n"
"ds_read_b64_tr_b16 a[196:197], v244 offset:8192\n"
"v_mfma_f32_16x16x32_bf16 v[28:31], a[252:255], a[148:151], v[28:31]\n"
"ds_read_b64_tr_b16 a[198:199], v244 offset:8704\n"
"v_mfma_f32_16x16x32_bf16 v[24:27], a[240:243], a[144:147], v[228:231]\n"
"ds_read_b128 a[140:143], v247 offset:0\n"
"v_mfma_f32_16x16x32_bf16 v[24:27], a[244:247], a[148:151], v[24:27]\n"
"buffer_load_dwordx4 v249, s[24:27], s49 offen offset:1024 lds\n"
"v_mfma_f32_16x16x32_bf16 v[20:23], a[232:235], a[144:147], v[228:231]\n"
"ds_read_b64_tr_b16 a[200:201], v245 offset:0\n"
"v_mfma_f32_16x16x32_bf16 v[20:23], a[236:239], a[148:151], v[20:23]\n"
"ds_read_b64_tr_b16 a[202:203], v245 offset:512\n"
"v_mfma_f32_16x16x32_bf16 v[16:19], a[224:227], a[144:147], v[228:231]\n"
"ds_read_b64_tr_b16 a[204:205], v245 offset:8192\n"
"v_mfma_f32_16x16x32_bf16 v[16:19], a[228:231], a[148:151], v[16:19]\n"
"ds_read_b64_tr_b16 a[206:207], v245 offset:8704\n"
"v_mfma_f32_16x16x32_bf16 v[32:35], a[224:227], a[152:155], v[228:231]\n"
"ds_read_b64_tr_b16 a[208:209], v244 offset:1024\n"
"v_mfma_f32_16x16x32_bf16 v[32:35], a[228:231], a[156:159], v[32:35]\n"
"buffer_load_dwordx4 v254, s[24:27], s49 offen offset:2048 lds\n"
"v_mfma_f32_16x16x32_bf16 v[36:39], a[232:235], a[152:155], v[228:231]\n"
"ds_read_b64_tr_b16 a[210:211], v244 offset:1536\n"
"v_mfma_f32_16x16x32_bf16 v[36:39], a[236:239], a[156:159], v[36:39]\n"
"ds_read_b64_tr_b16 a[212:213], v244 offset:9216\n"
"v_mfma_f32_16x16x32_bf16 v[40:43], a[240:243], a[152:155], v[228:231]\n"
"ds_read_b64_tr_b16 a[214:215], v244 offset:9728\n"
"v_mfma_f32_16x16x32_bf16 v[40:43], a[244:247], a[156:159], v[40:43]\n"
"ds_read_b64_tr_b16 a[216:217], v245 offset:1024\n"
"v_mfma_f32_16x16x32_bf16 v[44:47], a[248:251], a[152:155], v[228:231]\n"
"ds_read_b64_tr_b16 a[218:219], v245 offset:1536\n"
"v_mfma_f32_16x16x32_bf16 v[44:47], a[252:255], a[156:159], v[44:47]\n"
"buffer_load_dwordx4 v255, s[24:27], s49 offen offset:3072 lds\n"
"v_mfma_f32_16x16x32_bf16 v[60:63], a[248:251], a[160:163], v[228:231]\n"
"ds_read_b64_tr_b16 a[220:221], v245 offset:9216\n"
"v_mfma_f32_16x16x32_bf16 v[60:63], a[252:255], a[164:167], v[60:63]\n"
"ds_read_b64_tr_b16 a[222:223], v245 offset:9728\n"
"v_mfma_f32_16x16x32_bf16 v[56:59], a[240:243], a[160:163], v[228:231]\n"
"ds_read_b128 a[144:147], v246 offset:2048\n"
"v_mfma_f32_16x16x32_bf16 v[56:59], a[244:247], a[164:167], v[56:59]\n"
"s_add_u32 s40, s49, 40960\n"
"ds_read_b128 a[148:151], v247 offset:2048\n"
"v_mfma_f32_16x16x32_bf16 v[52:55], a[232:235], a[160:163], v[228:231]\n"
"s_add_u32 m0, s51, 4096\n"
"ds_read_b128 a[152:155], v246 offset:4096\n"
"v_mfma_f32_16x16x32_bf16 v[52:55], a[236:239], a[164:167], v[52:55]\n"
"buffer_load_dwordx4 v248, s[24:27], s40 offen lds\n"
"v_mfma_f32_16x16x32_bf16 v[48:51], a[224:227], a[160:163], v[228:231]\n"
"ds_read_b128 a[156:159], v247 offset:4096\n"
"v_mfma_f32_16x16x32_bf16 v[48:51], a[228:231], a[164:167], v[48:51]\n"
"ds_read_b128 a[160:163], v252 offset:0\n"
"v_mfma_f32_16x16x32_bf16 v[64:67], a[224:227], a[168:171], v[228:231]\n"
"ds_read_b128 a[164:167], v253 offset:0\n"
"v_mfma_f32_16x16x32_bf16 v[64:67], a[228:231], a[172:175], v[64:67]\n"
"v_mfma_f32_16x16x32_bf16 v[68:71], a[232:235], a[168:171], v[228:231]\n"
"v_mfma_f32_16x16x32_bf16 v[68:71], a[236:239], a[172:175], v[68:71]\n"
"buffer_load_dwordx4 v249, s[24:27], s40 offen offset:1024 lds\n"
"v_mfma_f32_16x16x32_bf16 v[72:75], a[240:243], a[168:171], v[228:231]\n"
"v_mfma_f32_16x16x32_bf16 v[72:75], a[244:247], a[172:175], v[72:75]\n"
"v_mfma_f32_16x16x32_bf16 v[76:79], a[248:251], a[168:171], v[228:231]\n"
"ds_read_b128 a[168:171], v250 offset:0\n"
"v_mfma_f32_16x16x32_bf16 v[76:79], a[252:255], a[172:175], v[76:79]\n"
"ds_read_b128 a[172:175], v251 offset:0\n"
"v_mfma_f32_16x16x32_bf16 v[92:95], a[248:251], a[176:179], v[228:231]\n"
"v_mfma_f32_16x16x32_bf16 v[92:95], a[252:255], a[180:183], v[92:95]\n"
"buffer_load_dwordx4 v254, s[24:27], s40 offen offset:2048 lds\n"
"v_mfma_f32_16x16x32_bf16 v[88:91], a[240:243], a[176:179], v[228:231]\n"
"s_add_u32 s49, s49, 128\n"
"v_mfma_f32_16x16x32_bf16 v[88:91], a[244:247], a[180:183], v[88:91]\n"
"s_sub_u32 s41, s49, 1280\n"
"v_mfma_f32_16x16x32_bf16 v[84:87], a[232:235], a[176:179], v[228:231]\n"
"s_min_u32 s49, s49, s41\n"
"v_mfma_f32_16x16x32_bf16 v[84:87], a[236:239], a[180:183], v[84:87]\n"
"s_xor_b32 s51, s51, s74\n"
"v_mfma_f32_16x16x32_bf16 v[80:83], a[224:227], a[176:179], v[228:231]\n"
"ds_read_b128 a[176:179], v250 offset:2048\n"
"v_mfma_f32_16x16x32_bf16 v[80:83], a[228:231], a[180:183], v[80:83]\n"
"ds_read_b128 a[180:183], v251 offset:2048\n"
"v_mfma_f32_16x16x32_bf16 v[96:99], a[224:227], a[184:187], v[228:231]\n"
"v_mfma_f32_16x16x32_bf16 v[96:99], a[228:231], a[188:191], v[96:99]\n"
"v_mfma_f32_16x16x32_bf16 v[100:103], a[232:235], a[184:187], v[228:231]\n"
"v_mfma_f32_16x16x32_bf16 v[100:103], a[236:239], a[188:191], v[100:103]\n"
"v_mfma_f32_16x16x32_bf16 v[104:107], a[240:243], a[184:187], v[228:231]\n"
"v_mfma_f32_16x16x32_bf16 v[104:107], a[244:247], a[188:191], v[104:107]\n"
"v_mfma_f32_16x16x32_bf16 v[108:111], a[248:251], a[184:187], v[228:231]\n"
"s_add_u32 s47, s47, 1\n"
"ds_read_b128 a[184:187], v250 offset:4096\n"
"v_mfma_f32_16x16x32_bf16 v[108:111], a[252:255], a[188:191], v[108:111]\n"
"s_waitcnt lgkmcnt(1)\n"
"ds_read_b128 a[188:191], v251 offset:4096\n"
"LPL_%=:\n"
"v_mfma_f32_16x16x32_bf16 v[112:115], a[192:195], a[136:139], v[112:115]\n"
"v_mfma_f32_16x16x32_bf16 v[112:115], a[196:199], a[140:143], v[112:115]\n"
"v_mfma_f32_16x16x32_bf16 v[116:119], a[200:203], a[136:139], v[116:119]\n"
"s_waitcnt vmcnt(11)\n"
"v_mfma_f32_16x16x32_bf16 v[116:119], a[204:207], a[140:143], v[116:119]\n"
"s_barrier\n"
"v_mfma_f32_16x16x32_bf16 v[120:123], a[208:211], a[136:139], v[120:123]\n"
"s_add_u32 s40, s48, 256\n"
"s_xor_b32 m0, s50, 0xc000\n"
"ds_read_b64_tr_b16 a[224:225], v244 offset:16384\n"
"v_mfma_f32_16x16x32_bf16 v[120:123], a[212:215], a[140:143], v[120:123]\n"
"buffer_load_dwordx4 v224, s[28:31], s40 offen lds\n"
"v_mfma_f32_16x16x32_bf16 v[124:127], a[216:219], a[136:139], v[124:127]\n"
"ds_read_b64_tr_b16 a[226:227], v244 offset:16896\n"
"v_mfma_f32_16x16x32_bf16 v[124:127], a[220:223], a[140:143], v[124:127]\n"
"ds_read_b64_tr_b16 a[228:229], v244 offset:24576\n"
"v_mfma_f32_16x16x32_bf16 v[140:143], a[216:219], a[144:147], v[140:143]\n"
"ds_read_b64_tr_b16 a[230:231], v244 offset:25088\n"
"v_mfma_f32_16x16x32_bf16 v[140:143], a[220:223], a[148:151], v[140:143]\n"
"ds_read_b64_tr_b16 a[232:233], v245 offset:16384\n"
"v_mfma_f32_16x16x32_bf16 v[136:139], a[208:211], a[144:147], v[136:139]\n"
"ds_read_b64_tr_b16 a[234:235], v245 offset:16896\n"
"v_mfma_f32_16x16x32_bf16 v[136:139], a[212:215], a[148:151], v[136:139]\n"
"buffer_load_dwordx4 v225, s[28:31], s40 offen offset:1024 lds\n"
"v_mfma_f32_16x16x32_bf16 v[132:135], a[200:203], a[144:147], v[132:135]\n"
"ds_read_b64_tr_b16 a[236:237], v245 offset:24576\n"
"v_mfma_f32_16x16x32_bf16 v[132:135], a[204:207], a[148:151], v[132:135]\n"
"ds_read_b64_tr_b16 a[238:239], v245 offset:25088\n"
"v_mfma_f32_16x16x32_bf16 v[128:131], a[192:195], a[144:147], v[128:131]\n"
"ds_read_b64_tr_b16 a[240:241], v244 offset:17408\n"
"v_mfma_f32_16x16x32_bf16 v[128:131], a[196:199], a[148:151], v[128:131]\n"
"ds_read_b64_tr_b16 a[242:243], v244 offset:17920\n"
"v_mfma_f32_16x16x32_bf16 v[144:147], a[192:195], a[152:155], v[144:147]\n"
"ds_read_b64_tr_b16 a[244:245], v244 offset:25600\n"
"v_mfma_f32_16x16x32_bf16 v[144:147], a[196:199], a[156:159], v[144:147]\n"
"buffer_load_dwordx4 v226, s[28:31], s40 offen offset:2048 lds\n"
"v_mfma_f32_16x16x32_bf16 v[148:151], a[200:203], a[152:155], v[148:151]\n"
"ds_read_b64_tr_b16 a[246:247], v244 offset:26112\n"
"v_mfma_f32_16x16x32_bf16 v[148:151], a[204:207], a[156:159], v[148:151]\n"
"ds_read_b64_tr_b16 a[248:249], v245 offset:17408\n"
"v_mfma_f32_16x16x32_bf16 v[152:155], a[208:211], a[152:155], v[152:155]\n"
"ds_read_b64_tr_b16 a[250:251], v245 offset:17920\n"
"v_mfma_f32_16x16x32_bf16 v[152:155], a[212:215], a[156:159], v[152:155]\n"
"ds_read_b64_tr_b16 a[252:253], v245 offset:25600\n"
"v_mfma_f32_16x16x32_bf16 v[156:159], a[216:219], a[152:155], v[156:159]\n"
"ds_read_b64_tr_b16 a[254:255], v245 offset:26112\n"
"v_mfma_f32_16x16x32_bf16 v[156:159], a[220:223], a[156:159], v[156:159]\n"
"buffer_load_dwordx4 v227, s[28:31], s40 offen offset:3072 lds\n"
"v_mfma_f32_16x16x32_bf16 v[172:175], a[216:219], a[160:163], v[172:175]\n"
"s_cmp_eq_u32 s47, 8\n"
"v_mfma_f32_16x16x32_bf16 v[172:175], a[220:223], a[164:167], v[172:175]\n"
"s_cselect_b32 s41, s52, s58\n"
"v_mfma_f32_16x16x32_bf16 v[168:171], a[208:211], a[160:163], v[168:171]\n"
"s_add_u32 s48, s48, s41\n"
"v_mfma_f32_16x16x32_bf16 v[168:171], a[212:215], a[164:167], v[168:171]\n"
"s_sub_u32 s41, s48, 5242880\n"
"v_mfma_f32_16x16x32_bf16 v[164:167], a[200:203], a[160:163], v[164:167]\n"
"s_min_u32 s48, s48, s41\n"
"v_mfma_f32_16x16x32_bf16 v[164:167], a[204:207], a[164:167], v[164:167]\n"
"v_xor_b32 v244, s74, v244\n"
"v_xor_b32 v246, s74, v246\n"
"v_mfma_f32_16x16x32_bf16 v[160:163], a[192:195], a[160:163], v[160:163]\n"
"s_mov_b32 m0, s50\n"
"v_xor_b32 v245, s74, v245\n"
"v_xor_b32 v247, s74, v247\n"
"v_mfma_f32_16x16x32_bf16 v[160:163], a[196:199], a[164:167], v[160:163]\n"
"buffer_load_dwordx4 v224, s[28:31], s48 offen lds\n"
"v_mfma_f32_16x16x32_bf16 v[176:179], a[192:195], a[168:171], v[176:179]\n"
"v_xor_b32 v252, s74, v252\n"
"v_xor_b32 v253, s74, v253\n"
"v_mfma_f32_16x16x32_bf16 v[176:179], a[196:199], a[172:175], v[176:179]\n"
"v_xor_b32 v250, s74, v250\n"
"v_xor_b32 v251, s74, v251\n"
"v_mfma_f32_16x16x32_bf16 v[180:183], a[200:203], a[168:171], v[180:183]\n"
"v_mfma_f32_16x16x32_bf16 v[180:183], a[204:207], a[172:175], v[180:183]\n"
"buffer_load_dwordx4 v225, s[28:31], s48 offen offset:1024 lds\n"
"v_mfma_f32_16x16x32_bf16 v[184:187], a[208:211], a[168:171], v[184:187]\n"
"v_mfma_f32_16x16x32_bf16 v[184:187], a[212:215], a[172:175], v[184:187]\n"
"v_mfma_f32_16x16x32_bf16 v[188:191], a[216:219], a[168:171], v[188:191]\n"
"v_mfma_f32_16x16x32_bf16 v[188:191], a[220:223], a[172:175], v[188:191]\n"
"v_mfma_f32_16x16x32_bf16 v[204:207], a[216:219], a[176:179], v[204:207]\n"
"v_mfma_f32_16x16x32_bf16 v[204:207], a[220:223], a[180:183], v[204:207]\n"
"buffer_load_dwordx4 v226, s[28:31], s48 offen offset:2048 lds\n"
"v_mfma_f32_16x16x32_bf16 v[200:203], a[208:211], a[176:179], v[200:203]\n"
"v_mfma_f32_16x16x32_bf16 v[200:203], a[212:215], a[180:183], v[200:203]\n"
"v_mfma_f32_16x16x32_bf16 v[196:199], a[200:203], a[176:179], v[196:199]\n"
"v_mfma_f32_16x16x32_bf16 v[196:199], a[204:207], a[180:183], v[196:199]\n"
"v_mfma_f32_16x16x32_bf16 v[192:195], a[192:195], a[176:179], v[192:195]\n"
"v_mfma_f32_16x16x32_bf16 v[192:195], a[196:199], a[180:183], v[192:195]\n"
"s_waitcnt lgkmcnt(0)\n"
"buffer_load_dwordx4 v227, s[28:31], s48 offen offset:3072 lds\n"
"v_mfma_f32_16x16x32_bf16 v[208:211], a[192:195], a[184:187], v[208:211]\n"
"s_xor_b32 s50, s50, s74\n"
"v_mfma_f32_16x16x32_bf16 v[208:211], a[196:199], a[188:191], v[208:211]\n"
"v_mfma_f32_16x16x32_bf16 v[212:215], a[200:203], a[184:187], v[212:215]\n"
"v_mfma_f32_16x16x32_bf16 v[212:215], a[204:207], a[188:191], v[212:215]\n"
"v_mfma_f32_16x16x32_bf16 v[216:219], a[208:211], a[184:187], v[216:219]\n"
"v_mfma_f32_16x16x32_bf16 v[216:219], a[212:215], a[188:191], v[216:219]\n"
"v_mfma_f32_16x16x32_bf16 v[220:223], a[216:219], a[184:187], v[220:223]\n"
"v_mfma_f32_16x16x32_bf16 v[220:223], a[220:223], a[188:191], v[220:223]\n"
"v_mfma_f32_16x16x32_bf16 v[0:3], a[224:227], a[136:139], v[0:3]\n"
"v_mfma_f32_16x16x32_bf16 v[0:3], a[228:231], a[140:143], v[0:3]\n"
"v_mfma_f32_16x16x32_bf16 v[4:7], a[232:235], a[136:139], v[4:7]\n"
"s_waitcnt vmcnt(8)\n"
"v_mfma_f32_16x16x32_bf16 v[4:7], a[236:239], a[140:143], v[4:7]\n"
"s_barrier\n"
"v_mfma_f32_16x16x32_bf16 v[8:11], a[240:243], a[136:139], v[8:11]\n"
"s_mov_b32 m0, s51\n"
"ds_read_b64_tr_b16 a[192:193], v244 offset:0\n"
"v_mfma_f32_16x16x32_bf16 v[8:11], a[244:247], a[140:143], v[8:11]\n"
"buffer_load_dwordx4 v248, s[24:27], s49 offen lds\n"
"v_mfma_f32_16x16x32_bf16 v[12:15], a[248:251], a[136:139], v[12:15]\n"
"ds_read_b64_tr_b16 a[194:195], v244 offset:512\n"
"v_mfma_f32_16x16x32_bf16 v[12:15], a[252:255], a[140:143], v[12:15]\n"
"ds_read_b128 a[136:139], v246 offset:0\n"
"v_mfma_f32_16x16x32_bf16 v[28:31], a[248:251], a[144:147], v[28:31]\n"
"ds_read_b64_tr_b16 a[196:197], v244 offset:8192\n"
"v_mfma_f32_16x16x32_bf16 v[28:31], a[252:255], a[148:151], v[28:31]\n"
"ds_read_b64_tr_b16 a[198:199], v244 offset:8704\n"
"v_mfma_f32_16x16x32_bf16 v[24:27], a[240:243], a[144:147], v[24:27]\n"
"ds_read_b128 a[140:143], v247 offset:0\n"
"v_mfma_f32_16x16x32_bf16 v[24:27], a[244:247], a[148:151], v[24:27]\n"
"buffer_load_dwordx4 v249, s[24:27], s49 offen offset:1024 lds\n"
"v_mfma_f32_16x16x32_bf16 v[20:23], a[232:235], a[144:147], v[20:23]\n"
"ds_read_b64_tr_b16 a[200:201], v245 offset:0\n"
"v_mfma_f32_16x16x32_bf16 v[20:23], a[236:239], a[148:151], v[20:23]\n"
"ds_read_b64_tr_b16 a[202:203], v245 offset:512\n"
"v_mfma_f32_16x16x32_bf16 v[16:19], a[224:227], a[144:147], v[16:19]\n"
"ds_read_b64_tr_b16 a[204:205], v245 offset:8192\n"
"v_mfma_f32_16x16x32_bf16 v[16:19], a[228:231], a[148:151], v[16:19]\n"
"ds_read_b64_tr_b16 a[206:207], v245 offset:8704\n"
"v_mfma_f32_16x16x32_bf16 v[32:35], a[224:227], a[152:155], v[32:35]\n"
"ds_read_b64_tr_b16 a[208:209], v244 offset:1024\n"
"v_mfma_f32_16x16x32_bf16 v[32:35], a[228:231], a[156:159], v[32:35]\n"
"buffer_load_dwordx4 v254, s[24:27], s49 offen offset:2048 lds\n"
"v_mfma_f32_16x16x32_bf16 v[36:39], a[232:235], a[152:155], v[36:39]\n"
"ds_read_b64_tr_b16 a[210:211], v244 offset:1536\n"
"v_mfma_f32_16x16x32_bf16 v[36:39], a[236:239], a[156:159], v[36:39]\n"
"ds_read_b64_tr_b16 a[212:213], v244 offset:9216\n"
"v_mfma_f32_16x16x32_bf16 v[40:43], a[240:243], a[152:155], v[40:43]\n"
"ds_read_b64_tr_b16 a[214:215], v244 offset:9728\n"
"v_mfma_f32_16x16x32_bf16 v[40:43], a[244:247], a[156:159], v[40:43]\n"
"ds_read_b64_tr_b16 a[216:217], v245 offset:1024\n"
"v_mfma_f32_16x16x32_bf16 v[44:47], a[248:251], a[152:155], v[44:47]\n"
"ds_read_b64_tr_b16 a[218:219], v245 offset:1536\n"
"v_mfma_f32_16x16x32_bf16 v[44:47], a[252:255], a[156:159], v[44:47]\n"
"buffer_load_dwordx4 v255, s[24:27], s49 offen offset:3072 lds\n"
"v_mfma_f32_16x16x32_bf16 v[60:63], a[248:251], a[160:163], v[60:63]\n"
"ds_read_b64_tr_b16 a[220:221], v245 offset:9216\n"
"v_mfma_f32_16x16x32_bf16 v[60:63], a[252:255], a[164:167], v[60:63]\n"
"ds_read_b64_tr_b16 a[222:223], v245 offset:9728\n"
"v_mfma_f32_16x16x32_bf16 v[56:59], a[240:243], a[160:163], v[56:59]\n"
"ds_read_b128 a[144:147], v246 offset:2048\n"
"v_mfma_f32_16x16x32_bf16 v[56:59], a[244:247], a[164:167], v[56:59]\n"
"s_add_u32 s40, s49, 40960\n"
"ds_read_b128 a[148:151], v247 offset:2048\n"
"v_mfma_f32_16x16x32_bf16 v[52:55], a[232:235], a[160:163], v[52:55]\n"
"s_add_u32 m0, s51, 4096\n"
"ds_read_b128 a[152:155], v246 offset:4096\n"
"v_mfma_f32_16x16x32_bf16 v[52:55], a[236:239], a[164:167], v[52:55]\n"
"buffer_load_dwordx4 v248, s[24:27], s40 offen lds\n"
"v_mfma_f32_16x16x32_bf16 v[48:51], a[224:227], a[160:163], v[48:51]\n"
"ds_read_b128 a[156:159], v247 offset:4096\n"
"v_mfma_f32_16x16x32_bf16 v[48:51], a[228:231], a[164:167], v[48:51]\n"
"ds_read_b128 a[160:163], v252 offset:0\n"
"v_mfma_f32_16x16x32_bf16 v[64:67], a[224:227], a[168:171], v[64:67]\n"
"ds_read_b128 a[164:167], v253 offset:0\n"
"v_mfma_f32_16x16x32_bf16 v[64:67], a[228:231], a[172:175], v[64:67]\n"
"v_mfma_f32_16x16x32_bf16 v[68:71], a[232:235], a[168:171], v[68:71]\n"
"v_mfma_f32_16x16x32_bf16 v[68:71], a[236:239], a[172:175], v[68:71]\n"
"buffer_load_dwordx4 v249, s[24:27], s40 offen offset:1024 lds\n"
"v_mfma_f32_16x16x32_bf16 v[72:75], a[240:243], a[168:171], v[72:75]\n"
"v_mfma_f32_16x16x32_bf16 v[72:75], a[244:247], a[172:175], v[72:75]\n"
"v_mfma_f32_16x16x32_bf16 v[76:79], a[248:251], a[168:171], v[76:79]\n"
"ds_read_b128 a[168:171], v250 offset:0\n"
"v_mfma_f32_16x16x32_bf16 v[76:79], a[252:255], a[172:175], v[76:79]\n"
"ds_read_b128 a[172:175], v251 offset:0\n"
"v_mfma_f32_16x16x32_bf16 v[92:95], a[248:251], a[176:179], v[92:95]\n"
"v_mfma_f32_16x16x32_bf16 v[92:95], a[252:255], a[180:183], v[92:95]\n"
"buffer_load_dwordx4 v254, s[24:27], s40 offen offset:2048 lds\n"
"v_mfma_f32_16x16x32_bf16 v[88:91], a[240:243], a[176:179], v[88:91]\n"
"s_add_u32 s49, s49, 128\n"
"v_mfma_f32_16x16x32_bf16 v[88:91], a[244:247], a[180:183], v[88:91]\n"
"s_sub_u32 s41, s49, 1280\n"
"v_mfma_f32_16x16x32_bf16 v[84:87], a[232:235], a[176:179], v[84:87]\n"
"s_min_u32 s49, s49, s41\n"
"v_mfma_f32_16x16x32_bf16 v[84:87], a[236:239], a[180:183], v[84:87]\n"
"s_xor_b32 s51, s51, s74\n"
"v_mfma_f32_16x16x32_bf16 v[80:83], a[224:227], a[176:179], v[80:83]\n"
"ds_read_b128 a[176:179], v250 offset:2048\n"
"v_mfma_f32_16x16x32_bf16 v[80:83], a[228:231], a[180:183], v[80:83]\n"
"ds_read_b128 a[180:183], v251 offset:2048\n"
"v_mfma_f32_16x16x32_bf16 v[96:99], a[224:227], a[184:187], v[96:99]\n"
"v_mfma_f32_16x16x32_bf16 v[96:99], a[228:231], a[188:191], v[96:99]\n"
"v_mfma_f32_16x16x32_bf16 v[100:103], a[232:235], a[184:187], v[100:103]\n"
"v_mfma_f32_16x16x32_bf16 v[100:103], a[236:239], a[188:191], v[100:103]\n"
"v_mfma_f32_16x16x32_bf16 v[104:107], a[240:243], a[184:187], v[104:107]\n"
"v_mfma_f32_16x16x32_bf16 v[104:107], a[244:247], a[188:191], v[104:107]\n"
"v_mfma_f32_16x16x32_bf16 v[108:111], a[248:251], a[184:187], v[108:111]\n"
"s_add_u32 s47, s47, 1\n"
"ds_read_b128 a[184:187], v250 offset:4096\n"
"v_mfma_f32_16x16x32_bf16 v[108:111], a[252:255], a[188:191], v[108:111]\n"
"s_waitcnt lgkmcnt(1)\n"
"s_cmp_lg_u32 s47, s62\n"
"ds_read_b128 a[188:191], v251 offset:4096\n"
"s_cbranch_scc1 LPL_%=\n"
"LPX_%=:\n"
"s_cmp_eq_u32 s47, 18\n"
"s_cbranch_scc1 LPT2_%=\n"
"v_mfma_f32_16x16x32_bf16 v[112:115], a[192:195], a[136:139], v[112:115]\n"
"s_add_u32 s78, s96, 8192\n"
"v_mov_b32 v241, 1\n"
"v_mfma_f32_16x16x32_bf16 v[112:115], a[196:199], a[140:143], v[112:115]\n"
"s_mov_b64 exec, 1\n"
"buffer_store_dword v241, off, s[32:35], s96 sc0 sc1\n"
"buffer_store_dword v241, off, s[32:35], s78 sc0 sc1\n"
"s_mov_b64 exec, -1\n"
"v_mfma_f32_16x16x32_bf16 v[116:119], a[200:203], a[136:139], v[116:119]\n"
"s_waitcnt vmcnt(13)\n"
"v_mfma_f32_16x16x32_bf16 v[116:119], a[204:207], a[140:143], v[116:119]\n"
"s_barrier\n"
"v_mfma_f32_16x16x32_bf16 v[120:123], a[208:211], a[136:139], v[120:123]\n"
"s_add_u32 s40, s48, 256\n"
"s_xor_b32 m0, s50, 0xc000\n"
"ds_read_b64_tr_b16 a[254:255], v245 offset:26112\n"
"v_mfma_f32_16x16x32_bf16 v[120:123], a[212:215], a[140:143], v[120:123]\n"
"buffer_load_dwordx4 v224, s[28:31], s40 offen lds\n"
"v_mfma_f32_16x16x32_bf16 v[124:127], a[216:219], a[136:139], v[124:127]\n"
"v_mfma_f32_16x16x32_bf16 v[124:127], a[220:223], a[140:143], v[124:127]\n"
"s_memtime s[78:79]\n"
"v_mfma_f32_16x16x32_bf16 v[188:191], a[216:219], a[168:171], v[188:191]\n"
"ds_read_b64_tr_b16 a[232:233], v245 offset:16384\n"
"ds_read_b64_tr_b16 a[238:239], v245 offset:25088\n"
"v_mfma_f32_16x16x32_bf16 v[188:191], a[220:223], a[172:175], v[188:191]\n"
"s_memrealtime s[80:81]\n"
"ds_read_b64_tr_b16 a[226:227], v244 offset:16896\n"
"v_mfma_f32_16x16x32_bf16 v[184:187], a[208:211], a[168:171], v[184:187]\n"
"ds_read_b64_tr_b16 a[252:253], v245 offset:25600\n"
"v_mfma_f32_16x16x32_bf16 v[184:187], a[212:215], a[172:175], v[184:187]\n"
"buffer_load_dwordx4 v225, s[28:31], s40 offen offset:1024 lds\n"
"v_mfma_f32_16x16x32_bf16 v[180:183], a[200:203], a[168:171], v[180:183]\n"
"v_bfe_u32 v241, v242, 4, 6\n"
"v_mfma_f32_16x16x32_bf16 v[180:183], a[204:207], a[172:175], v[180:183]\n"
"v_mfma_f32_16x16x32_bf16 v[176:179], a[192:195], a[168:171], v[176:179]\n"
"ds_read_b64_tr_b16 a[236:237], v245 offset:24576\n"
"v_mfma_f32_16x16x32_bf16 v[176:179], a[196:199], a[172:175], v[176:179]\n"
"ds_read_b64_tr_b16 a[234:235], v245 offset:16896\n"
"v_mul_u32_u24 v241, 24, v241\n"
"v_mfma_f32_16x16x32_bf16 v[192:195], a[192:195], a[176:179], v[192:195]\n"
"ds_read_b64_tr_b16 a[230:231], v244 offset:25088\n"
"ds_read_b64_tr_b16 a[246:247], v244 offset:26112\n"
"v_mfma_f32_16x16x32_bf16 v[192:195], a[196:199], a[180:183], v[192:195]\n"
"buffer_load_dwordx4 v226, s[28:31], s40 offen offset:2048 lds\n"
"v_mfma_f32_16x16x32_bf16 v[196:199], a[200:203], a[176:179], v[196:199]\n"
"v_mfma_f32_16x16x32_bf16 v[196:199], a[204:207], a[180:183], v[196:199]\n"
"v_mfma_f32_16x16x32_bf16 v[200:203], a[208:211], a[176:179], v[200:203]\n"
"ds_read_b64_tr_b16 a[248:249], v245 offset:17408\n"
"v_mfma_f32_16x16x32_bf16 v[200:203], a[212:215], a[180:183], v[200:203]\n"
"ds_read_b64_tr_b16 a[244:245], v244 offset:25600\n"
"v_mfma_f32_16x16x32_bf16 v[204:207], a[216:219], a[176:179], v[204:207]\n"
"ds_read_b64_tr_b16 a[224:225], v244 offset:16384\n"
"ds_read_b64_tr_b16 a[228:229], v244 offset:24576\n"
"v_mfma_f32_16x16x32_bf16 v[204:207], a[220:223], a[180:183], v[204:207]\n"
"s_waitcnt lgkmcnt(6)\n"
"buffer_load_dwordx4 v227, s[28:31], s40 offen offset:3072 lds\n"
"v_mfma_f32_16x16x32_bf16 v[220:223], a[216:219], a[184:187], v[220:223]\n"
"s_cmp_eq_u32 s47, 8\n"
"v_mfma_f32_16x16x32_bf16 v[220:223], a[220:223], a[188:191], v[220:223]\n"
"s_cselect_b32 s41, s52, s58\n"
"v_mfma_f32_16x16x32_bf16 v[216:219], a[208:211], a[184:187], v[216:219]\n"
"s_add_u32 s48, s48, s41\n"
"ds_read_b64_tr_b16 a[242:243], v244 offset:17920\n"
"ds_read_b64_tr_b16 a[250:251], v245 offset:17920\n"
"v_mfma_f32_16x16x32_bf16 v[216:219], a[212:215], a[188:191], v[216:219]\n"
"s_sub_u32 s41, s48, 5242880\n"
"v_pk_fma_f32 v[176:177], v[176:177], s[70:71], s[70:71] op_sel:[0,0,1] op_sel_hi:[1,0,1]\nv_pk_fma_f32 v[178:179], v[178:179], s[70:71], s[70:71] op_sel:[0,0,1] op_sel_hi:[1,0,1]\nv_pk_fma_f32 v[180:181], v[180:181], s[70:71], s[70:71] op_sel:[0,0,1] op_sel_hi:[1,0,1]\nv_pk_fma_f32 v[182:183], v[182:183], s[70:71], s[70:71] op_sel:[0,0,1] op_sel_hi:[1,0,1]\nv_pk_fma_f32 v[184:185], v[184:185], s[70:71], s[70:71] op_sel:[0,0,1] op_sel_hi:[1,0,1]\nv_pk_fma_f32 v[186:187], v[186:187], s[70:71], s[70:71] op_sel:[0,0,1] op_sel_hi:[1,0,1]\nv_pk_fma_f32 v[188:189], v[188:189], s[70:71], s[70:71] op_sel:[0,0,1] op_sel_hi:[1,0,1]\nv_pk_fma_f32 v[190:191], v[190:191], s[70:71], s[70:71] op_sel:[0,0,1] op_sel_hi:[1,0,1]\nv_pk_fma_f32 v[192:193], v[192:193], s[70:71], s[70:71] op_sel:[0,0,1] op_sel_hi:[1,0,1]\nv_pk_fma_f32 v[194:195], v[194:195], s[70:71], s[70:71] op_sel:[0,0,1] op_sel_hi:[1,0,1]\nv_pk_fma_f32 v[196:197], v[196:197], s[70:71], s[70:71] op_sel:[0,0,1] op_sel_hi:[1,0,1]\nv_pk_fma_f32 v[198:199], v[198:199], s[70:71], s[70:71] op_sel:[0,0,1] op_sel_hi:[1,0,1]\nv_pk_fma_f32 v[200:201], v[200:201], s[70:71], s[70:71] op_sel:[0,0,1] op_sel_hi:[1,0,1]\nv_pk_fma_f32 v[202:203], v[202:203], s[70:71], s[70:71] op_sel:[0,0,1] op_sel_hi:[1,0,1]\nv_pk_fma_f32 v[204:205], v[204:205], s[70:71], s[70:71] op_sel:[0,0,1] op_sel_hi:[1,0,1]\nv_pk_fma_f32 v[206:207], v[206:207], s[70:71], s[70:71] op_sel:[0,0,1] op_sel_hi:[1,0,1]\n"
"v_mfma_f32_16x16x32_bf16 v[212:215], a[200:203], a[184:187], v[212:215]\n"
"s_min_u32 s48, s48, s41\n"
"v_cvt_pk_bf16_f32 v176, v176, v177\n"
"v_cvt_pk_bf16_f32 v177, v178, v179\n"
"v_mfma_f32_16x16x32_bf16 v[212:215], a[204:207], a[188:191], v[212:215]\n"
"v_cvt_pk_bf16_f32 v178, v180, v181\n"
"v_cvt_pk_bf16_f32 v192, v192, v193\n"
"v_mfma_f32_16x16x32_bf16 v[208:211], a[192:195], a[184:187], v[208:211]\n"
"s_mov_b32 m0, s50\n"
"v_cvt_pk_bf16_f32 v179, v182, v183\n"
"v_and_b32 v182, 0xf0, v242\n"
"v_mfma_f32_16x16x32_bf16 v[208:211], a[196:199], a[188:191], v[208:211]\n"
"buffer_load_dwordx4 v224, s[28:31], s48 offen lds\n"
"v_mfma_f32_16x16x32_bf16 v[160:163], a[192:195], a[160:163], v[160:163]\n"
"v_bfe_u32 v180, v242, 9, 1\n"
"v_bfe_u32 v181, v242, 9, 1\n"
"v_mfma_f32_16x16x32_bf16 v[160:163], a[196:199], a[164:167], v[160:163]\n"
"v_lshl_add_u32 v180, v180, 8, v182\n"
"v_bfe_u32 v182, v242, 8, 1\n"
"v_mfma_f32_16x16x32_bf16 v[164:167], a[200:203], a[160:163], v[164:167]\n"
"ds_read_b64_tr_b16 a[240:241], v244 offset:17408\n"
"v_cvt_pk_bf16_f32 v193, v194, v195\n"
"v_mfma_f32_16x16x32_bf16 v[164:167], a[204:207], a[164:167], v[164:167]\n"
"buffer_load_dwordx4 v225, s[28:31], s48 offen offset:1024 lds\n"
"v_mfma_f32_16x16x32_bf16 v[168:171], a[208:211], a[160:163], v[168:171]\n"
"v_lshl_add_u32 v180, v182, 3, v180\n"
"v_xor_b32_e64 v182, s42, 1\n"
"v_mfma_f32_16x16x32_bf16 v[168:171], a[212:215], a[164:167], v[168:171]\n"
"v_cvt_pk_bf16_f32 v184, v184, v185\n"
"v_cvt_pk_bf16_f32 v194, v196, v197\n"
"v_mfma_f32_16x16x32_bf16 v[172:175], a[216:219], a[160:163], v[172:175]\n"
"v_lshl_add_u32 v180, v182, 10, v180\n"
"v_xor_b32 v246, s74, v246\n"
"v_mfma_f32_16x16x32_bf16 v[172:175], a[220:223], a[164:167], v[172:175]\n"
"v_cvt_pk_bf16_f32 v200, v200, v201\n"
"v_xor_b32 v250, s74, v250\n"
"v_mfma_f32_16x16x32_bf16 v[140:143], a[216:219], a[144:147], v[140:143]\n"
"v_cvt_pk_bf16_f32 v185, v186, v187\n"
"v_cvt_pk_bf16_f32 v201, v202, v203\n"
"v_mfma_f32_16x16x32_bf16 v[140:143], a[220:223], a[148:151], v[140:143]\n"
"buffer_load_dwordx4 v226, s[28:31], s48 offen offset:2048 lds\n"
"v_mfma_f32_16x16x32_bf16 v[136:139], a[208:211], a[144:147], v[136:139]\n"
"v_add_u32 v180, 94208, v180\n"
"v_cvt_pk_bf16_f32 v195, v198, v199\n"
"v_mfma_f32_16x16x32_bf16 v[136:139], a[212:215], a[148:151], v[136:139]\n"
"v_pk_fma_f32 v[208:209], v[208:209], s[70:71], s[70:71] op_sel:[0,0,1] op_sel_hi:[1,0,1]\nv_pk_fma_f32 v[210:211], v[210:211], s[70:71], s[70:71] op_sel:[0,0,1] op_sel_hi:[1,0,1]\nv_pk_fma_f32 v[212:213], v[212:213], s[70:71], s[70:71] op_sel:[0,0,1] op_sel_hi:[1,0,1]\nv_pk_fma_f32 v[214:215], v[214:215], s[70:71], s[70:71] op_sel:[0,0,1] op_sel_hi:[1,0,1]\nv_pk_fma_f32 v[216:217], v[216:217], s[70:71], s[70:71] op_sel:[0,0,1] op_sel_hi:[1,0,1]\nv_pk_fma_f32 v[218:219], v[218:219], s[70:71], s[70:71] op_sel:[0,0,1] op_sel_hi:[1,0,1]\nv_pk_fma_f32 v[220:221], v[220:221], s[70:71], s[70:71] op_sel:[0,0,1] op_sel_hi:[1,0,1]\nv_pk_fma_f32 v[222:223], v[222:223], s[70:71], s[70:71] op_sel:[0,0,1] op_sel_hi:[1,0,1]\nv_pk_fma_f32 v[160:161], v[160:161], s[70:71], s[70:71] op_sel:[0,0,1] op_sel_hi:[1,0,1]\nv_pk_fma_f32 v[162:163], v[162:163], s[70:71], s[70:71] op_sel:[0,0,1] op_sel_hi:[1,0,1]\nv_pk_fma_f32 v[164:165], v[164:165], s[70:71], s[70:71] op_sel:[0,0,1] op_sel_hi:[1,0,1]\nv_pk_fma_f32 v[166:167], v[166:167], s[70:71], s[70:71] op_sel:[0,0,1] op_sel_hi:[1,0,1]\nv_pk_fma_f32 v[168:169], v[168:169], s[70:71], s[70:71] op_sel:[0,0,1] op_sel_hi:[1,0,1]\nv_pk_fma_f32 v[170:171], v[170:171], s[70:71], s[70:71] op_sel:[0,0,1] op_sel_hi:[1,0,1]\nv_pk_fma_f32 v[172:173], v[172:173], s[70:71], s[70:71] op_sel:[0,0,1] op_sel_hi:[1,0,1]\nv_pk_fma_f32 v[174:175], v[174:175], s[70:71], s[70:71] op_sel:[0,0,1] op_sel_hi:[1,0,1]\n"
"v_mfma_f32_16x16x32_bf16 v[132:135], a[200:203], a[144:147], v[132:135]\n"
"v_cvt_pk_bf16_f32 v208, v208, v209\n"
"v_xor_b32 v244, s74, v244\n"
"v_mfma_f32_16x16x32_bf16 v[132:135], a[204:207], a[148:151], v[132:135]\n"
"v_cvt_pk_bf16_f32 v168, v168, v169\n"
"v_and_b32 v182, 0xf0, v242\n"
"v_mfma_f32_16x16x32_bf16 v[128:131], a[192:195], a[144:147], v[128:131]\n"
"v_cvt_pk_bf16_f32 v216, v216, v217\n"
"v_cvt_pk_bf16_f32 v217, v218, v219\n"
"v_mfma_f32_16x16x32_bf16 v[128:131], a[196:199], a[148:151], v[128:131]\n"
"buffer_load_dwordx4 v227, s[28:31], s48 offen offset:3072 lds\n"
"v_mfma_f32_16x16x32_bf16 v[144:147], a[192:195], a[152:155], v[144:147]\n"
"s_xor_b32 s50, s50, s74\n"
"v_cvt_pk_bf16_f32 v209, v210, v211\n"
"v_cvt_pk_bf16_f32 v169, v170, v171\n"
"v_mfma_f32_16x16x32_bf16 v[144:147], a[196:199], a[156:159], v[144:147]\n"
"v_cvt_pk_bf16_f32 v202, v204, v205\n"
"v_lshl_add_u32 v181, v181, 8, v182\n"
"v_mfma_f32_16x16x32_bf16 v[148:151], a[200:203], a[152:155], v[148:151]\n"
"v_cvt_pk_bf16_f32 v187, v190, v191\n"
"v_cvt_pk_bf16_f32 v203, v206, v207\n"
"v_mfma_f32_16x16x32_bf16 v[148:151], a[204:207], a[156:159], v[148:151]\n"
"ds_write2st64_b64 v180, v[192:193], v[194:195] offset0:88 offset1:89\n"
"v_pk_fma_f32 v[112:113], v[112:113], s[70:71], s[70:71] op_sel:[0,0,1] op_sel_hi:[1,0,1]\nv_pk_fma_f32 v[114:115], v[114:115], s[70:71], s[70:71] op_sel:[0,0,1] op_sel_hi:[1,0,1]\nv_pk_fma_f32 v[116:117], v[116:117], s[70:71], s[70:71] op_sel:[0,0,1] op_sel_hi:[1,0,1]\nv_pk_fma_f32 v[118:119], v[118:119], s[70:71], s[70:71] op_sel:[0,0,1] op_sel_hi:[1,0,1]\nv_pk_fma_f32 v[120:121], v[120:121], s[70:71], s[70:71] op_sel:[0,0,1] op_sel_hi:[1,0,1]\nv_pk_fma_f32 v[122:123], v[122:123], s[70:71], s[70:71] op_sel:[0,0,1] op_sel_hi:[1,0,1]\nv_pk_fma_f32 v[124:125], v[124:125], s[70:71], s[70:71] op_sel:[0,0,1] op_sel_hi:[1,0,1]\nv_pk_fma_f32 v[126:127], v[126:127], s[70:71], s[70:71] op_sel:[0,0,1] op_sel_hi:[1,0,1]\nv_pk_fma_f32 v[128:129], v[128:129], s[70:71], s[70:71] op_sel:[0,0,1] op_sel_hi:[1,0,1]\nv_pk_fma_f32 v[130:131], v[130:131], s[70:71], s[70:71] op_sel:[0,0,1] op_sel_hi:[1,0,1]\nv_pk_fma_f32 v[132:133], v[132:133], s[70:71], s[70:71] op_sel:[0,0,1] op_sel_hi:[1,0,1]\nv_pk_fma_f32 v[134:135], v[134:135], s[70:71], s[70:71] op_sel:[0,0,1] op_sel_hi:[1,0,1]\nv_pk_fma_f32 v[136:137], v[136:137], s[70:71], s[70:71] op_sel:[0,0,1] op_sel_hi:[1,0,1]\nv_pk_fma_f32 v[138:139], v[138:139], s[70:71], s[70:71] op_sel:[0,0,1] op_sel_hi:[1,0,1]\nv_pk_fma_f32 v[140:141], v[140:141], s[70:71], s[70:71] op_sel:[0,0,1] op_sel_hi:[1,0,1]\nv_pk_fma_f32 v[142:143], v[142:143], s[70:71], s[70:71] op_sel:[0,0,1] op_sel_hi:[1,0,1]\n"
"v_mfma_f32_16x16x32_bf16 v[152:155], a[208:211], a[152:155], v[152:155]\n"
"v_cvt_pk_bf16_f32 v218, v220, v221\n"
"v_bfe_u32 v182, v242, 8, 1\n"
"v_mfma_f32_16x16x32_bf16 v[152:155], a[212:215], a[156:159], v[152:155]\n"
"v_cvt_pk_bf16_f32 v120, v120, v121\n"
"v_cvt_pk_bf16_f32 v128, v128, v129\n"
"v_mfma_f32_16x16x32_bf16 v[156:159], a[216:219], a[152:155], v[156:159]\n"
"v_cvt_pk_bf16_f32 v121, v122, v123\n"
"v_cvt_pk_bf16_f32 v136, v136, v137\n"
"v_mfma_f32_16x16x32_bf16 v[156:159], a[220:223], a[156:159], v[156:159]\n"
"s_waitcnt lgkmcnt(1)\n"
"ds_write2st64_b64 v180, v[176:177], v[178:179] offset0:0 offset1:1\n"
"v_cvt_pk_bf16_f32 v160, v160, v161\n"
"v_cvt_pk_bf16_f32 v122, v124, v125\n"
"v_cvt_pk_bf16_f32 v123, v126, v127\n"
"v_xor_b32 v245, s74, v245\n"
"v_lshl_add_u32 v181, v182, 9, v181\n"
"v_mfma_f32_16x16x32_bf16 v[96:99], a[224:227], a[184:187], v[96:99]\n"
"v_cvt_pk_bf16_f32 v210, v212, v213\n"
"v_xor_b32 v253, s74, v253\n"
"v_mfma_f32_16x16x32_bf16 v[96:99], a[228:231], a[188:191], v[96:99]\n"
"ds_write2st64_b64 v180, v[200:201], v[202:203] offset0:96 offset1:97\n"
"v_cvt_pk_bf16_f32 v186, v188, v189\n"
"v_cvt_pk_bf16_f32 v161, v162, v163\n"
"v_cvt_pk_bf16_f32 v112, v112, v113\n"
"v_cvt_pk_bf16_f32 v113, v114, v115\n"
"v_cvt_pk_bf16_f32 v129, v130, v131\n"
"v_mfma_f32_16x16x32_bf16 v[100:103], a[232:235], a[184:187], v[100:103]\n"
"s_waitcnt vmcnt(10)\n"
"v_cvt_pk_bf16_f32 v211, v214, v215\n"
"v_cvt_pk_bf16_f32 v171, v174, v175\n"
"v_mfma_f32_16x16x32_bf16 v[100:103], a[236:239], a[188:191], v[100:103]\n"
"s_barrier\n"
"ds_write2st64_b64 v180, v[208:209], v[210:211] offset0:120 offset1:121\n"
"v_pk_fma_f32 v[144:145], v[144:145], s[70:71], s[70:71] op_sel:[0,0,1] op_sel_hi:[1,0,1]\nv_pk_fma_f32 v[146:147], v[146:147], s[70:71], s[70:71] op_sel:[0,0,1] op_sel_hi:[1,0,1]\nv_pk_fma_f32 v[148:149], v[148:149], s[70:71], s[70:71] op_sel:[0,0,1] op_sel_hi:[1,0,1]\nv_pk_fma_f32 v[150:151], v[150:151], s[70:71], s[70:71] op_sel:[0,0,1] op_sel_hi:[1,0,1]\nv_pk_fma_f32 v[152:153], v[152:153], s[70:71], s[70:71] op_sel:[0,0,1] op_sel_hi:[1,0,1]\nv_pk_fma_f32 v[154:155], v[154:155], s[70:71], s[70:71] op_sel:[0,0,1] op_sel_hi:[1,0,1]\nv_pk_fma_f32 v[156:157], v[156:157], s[70:71], s[70:71] op_sel:[0,0,1] op_sel_hi:[1,0,1]\nv_pk_fma_f32 v[158:159], v[158:159], s[70:71], s[70:71] op_sel:[0,0,1] op_sel_hi:[1,0,1]\n"
"v_mfma_f32_16x16x32_bf16 v[104:107], a[240:243], a[184:187], v[104:107]\n"
"s_mov_b32 m0, s51\n"
"v_cvt_pk_bf16_f32 v219, v222, v223\n"
"v_cvt_pk_bf16_f32 v137, v138, v139\n"
"v_mfma_f32_16x16x32_bf16 v[104:107], a[244:247], a[188:191], v[104:107]\n"
"buffer_load_dwordx4 v248, s[24:27], s49 offen lds\n"
"v_mfma_f32_16x16x32_bf16 v[108:111], a[248:251], a[184:187], v[108:111]\n"
"v_cvt_pk_bf16_f32 v115, v118, v119\n"
"v_cvt_pk_bf16_f32 v144, v144, v145\n"
"v_mfma_f32_16x16x32_bf16 v[108:111], a[252:255], a[188:191], v[108:111]\n"
"v_cvt_pk_bf16_f32 v131, v134, v135\n"
"v_cvt_pk_bf16_f32 v152, v152, v153\n"
"v_mfma_f32_16x16x32_bf16 v[92:95], a[248:251], a[176:179], v[92:95]\n"
"ds_read_b64_tr_b16 a[212:213], v244 offset:9216\n"
"v_cvt_pk_bf16_f32 v162, v164, v165\n"
"v_mfma_f32_16x16x32_bf16 v[92:95], a[252:255], a[180:183], v[92:95]\n"
"ds_write2st64_b64 v180, v[184:185], v[186:187] offset0:64 offset1:65\n"
"v_permlane16_swap_b32 v160, v162\n"
"v_permlane16_swap_b32 v113, v115\n"
"v_cvt_pk_bf16_f32 v130, v132, v133\n"
"v_cvt_pk_bf16_f32 v153, v154, v155\n"
"v_mfma_f32_16x16x32_bf16 v[88:91], a[240:243], a[176:179], v[88:91]\n"
"v_xor_b32_e64 v182, s42, 0\n"
"v_lshl_add_u32 v181, v182, 10, v181\n"
"v_mfma_f32_16x16x32_bf16 v[88:91], a[244:247], a[180:183], v[88:91]\n"
"buffer_load_dwordx4 v249, s[24:27], s49 offen offset:1024 lds\n"
"v_mfma_f32_16x16x32_bf16 v[84:87], a[232:235], a[176:179], v[84:87]\n"
"v_cvt_pk_bf16_f32 v114, v116, v117\n"
"v_xor_b32 v251, s74, v251\n"
"v_mfma_f32_16x16x32_bf16 v[84:87], a[236:239], a[180:183], v[84:87]\n"
"v_permlane16_swap_b32 v121, v123\n"
"v_mfma_f32_16x16x32_bf16 v[80:83], a[224:227], a[176:179], v[80:83]\n"
"ds_read_b64_tr_b16 a[218:219], v245 offset:1536\n"
"ds_read_b128 a[184:187], v250 offset:4096\n"
"v_mfma_f32_16x16x32_bf16 v[80:83], a[228:231], a[180:183], v[80:83]\n"
"ds_write2st64_b64 v180, v[216:217], v[218:219] offset0:128 offset1:129\n"
"v_permlane16_swap_b32 v169, v171\n"
"v_permlane16_swap_b32 v112, v114\n"
"v_permlane16_swap_b32 v129, v131\n"
"v_cvt_pk_bf16_f32 v139, v142, v143\n"
"v_cvt_pk_bf16_f32 v155, v158, v159\n"
"v_mfma_f32_16x16x32_bf16 v[64:67], a[224:227], a[168:171], v[64:67]\n"
"ds_read_b64_tr_b16 a[214:215], v244 offset:9728\n"
"v_add_u32 v181, 94208, v181\n"
"v_mfma_f32_16x16x32_bf16 v[64:67], a[228:231], a[172:175], v[64:67]\n"
"buffer_load_dwordx4 v254, s[24:27], s49 offen offset:2048 lds\n"
"v_mfma_f32_16x16x32_bf16 v[68:71], a[232:235], a[168:171], v[68:71]\n"
"v_cvt_pk_bf16_f32 v145, v146, v147\n"
"v_cvt_pk_bf16_f32 v147, v150, v151\n"
"v_mfma_f32_16x16x32_bf16 v[68:71], a[236:239], a[172:175], v[68:71]\n"
"v_cvt_pk_bf16_f32 v170, v172, v173\n"
"v_cvt_pk_bf16_f32 v154, v156, v157\n"
"v_pk_fma_f32 v[96:97], v[96:97], s[70:71], s[70:71] op_sel:[0,0,1] op_sel_hi:[1,0,1]\nv_pk_fma_f32 v[98:99], v[98:99], s[70:71], s[70:71] op_sel:[0,0,1] op_sel_hi:[1,0,1]\nv_pk_fma_f32 v[100:101], v[100:101], s[70:71], s[70:71] op_sel:[0,0,1] op_sel_hi:[1,0,1]\nv_pk_fma_f32 v[102:103], v[102:103], s[70:71], s[70:71] op_sel:[0,0,1] op_sel_hi:[1,0,1]\nv_pk_fma_f32 v[104:105], v[104:105], s[70:71], s[70:71] op_sel:[0,0,1] op_sel_hi:[1,0,1]\nv_pk_fma_f32 v[106:107], v[106:107], s[70:71], s[70:71] op_sel:[0,0,1] op_sel_hi:[1,0,1]\nv_pk_fma_f32 v[108:109], v[108:109], s[70:71], s[70:71] op_sel:[0,0,1] op_sel_hi:[1,0,1]\nv_pk_fma_f32 v[110:111], v[110:111], s[70:71], s[70:71] op_sel:[0,0,1] op_sel_hi:[1,0,1]\nv_pk_fma_f32 v[80:81], v[80:81], s[70:71], s[70:71] op_sel:[0,0,1] op_sel_hi:[1,0,1]\nv_pk_fma_f32 v[82:83], v[82:83], s[70:71], s[70:71] op_sel:[0,0,1] op_sel_hi:[1,0,1]\nv_pk_fma_f32 v[84:85], v[84:85], s[70:71], s[70:71] op_sel:[0,0,1] op_sel_hi:[1,0,1]\nv_pk_fma_f32 v[86:87], v[86:87], s[70:71], s[70:71] op_sel:[0,0,1] op_sel_hi:[1,0,1]\nv_pk_fma_f32 v[88:89], v[88:89], s[70:71], s[70:71] op_sel:[0,0,1] op_sel_hi:[1,0,1]\nv_pk_fma_f32 v[90:91], v[90:91], s[70:71], s[70:71] op_sel:[0,0,1] op_sel_hi:[1,0,1]\nv_pk_fma_f32 v[92:93], v[92:93], s[70:71], s[70:71] op_sel:[0,0,1] op_sel_hi:[1,0,1]\nv_pk_fma_f32 v[94:95], v[94:95], s[70:71], s[70:71] op_sel:[0,0,1] op_sel_hi:[1,0,1]\n"
"v_cvt_pk_bf16_f32 v88, v88, v89\n"
"v_mfma_f32_16x16x32_bf16 v[72:75], a[240:243], a[168:171], v[72:75]\n"
"ds_read_b64_tr_b16 a[222:223], v245 offset:9728\n"
"v_cvt_pk_bf16_f32 v96, v96, v97\n"
"v_mfma_f32_16x16x32_bf16 v[72:75], a[244:247], a[172:175], v[72:75]\n"
"ds_read_b64_tr_b16 a[196:197], v244 offset:8192\n"
"v_accvgpr_write_b32 a1, v113\n"
"v_mfma_f32_16x16x32_bf16 v[76:79], a[248:251], a[168:171], v[76:79]\n"
"ds_read_b64_tr_b16 a[216:217], v245 offset:1024\n"
"v_xor_b32 v252, s74, v252\n"
"v_mfma_f32_16x16x32_bf16 v[76:79], a[252:255], a[172:175], v[76:79]\n"
"buffer_load_dwordx4 v255, s[24:27], s49 offen offset:3072 lds\n"
"v_mfma_f32_16x16x32_bf16 v[60:63], a[248:251], a[160:163], v[60:63]\n"
"v_permlane16_swap_b32 v128, v130\n"
"v_mfma_f32_16x16x32_bf16 v[60:63], a[252:255], a[164:167], v[60:63]\n"
"v_permlane16_swap_b32 v137, v139\n"
"v_mfma_f32_16x16x32_bf16 v[56:59], a[240:243], a[160:163], v[56:59]\n"
"ds_read_b64_tr_b16 a[194:195], v244 offset:512\n"
"ds_read_b64_tr_b16 a[210:211], v244 offset:1536\n"
"v_mfma_f32_16x16x32_bf16 v[56:59], a[244:247], a[164:167], v[56:59]\n"
"s_add_u32 s40, s49, 40960\n"
"ds_read_b64_tr_b16 a[208:209], v244 offset:1024\n"
"v_cvt_pk_bf16_f32 v138, v140, v141\n"
"v_mfma_f32_16x16x32_bf16 v[52:55], a[232:235], a[160:163], v[52:55]\n"
"s_add_u32 m0, s51, 4096\n"
"ds_read_b128 a[168:171], v250 offset:0\n"
"v_cvt_pk_bf16_f32 v146, v148, v149\n"
"v_mfma_f32_16x16x32_bf16 v[52:55], a[236:239], a[164:167], v[52:55]\n"
"buffer_load_dwordx4 v248, s[24:27], s40 offen lds\n"
"v_mfma_f32_16x16x32_bf16 v[48:51], a[224:227], a[160:163], v[48:51]\n"
"s_waitcnt lgkmcnt(0)\n"
"v_accvgpr_write_b32 a9, v121\n"
"v_cvt_pk_bf16_f32 v80, v80, v81\n"
"v_mfma_f32_16x16x32_bf16 v[48:51], a[228:231], a[164:167], v[48:51]\n"
"s_barrier\n"
"v_mfma_f32_16x16x32_bf16 v[0:3], a[224:227], a[136:139], v[0:3]\n"
"ds_read_b128 a[28:31], v181 offset:49152\n"
"v_add_u32 v182, s75, v181\n"
"v_mfma_f32_16x16x32_bf16 v[0:3], a[228:231], a[140:143], v[0:3]\n"
"ds_read_b128 a[36:39], v181 offset:61440\n"
"v_accvgpr_write_b32 a3, v115\n"
"v_mfma_f32_16x16x32_bf16 v[4:7], a[232:235], a[136:139], v[4:7]\n"
"ds_read_b128 a[4:7], v181 offset:0\n"
"v_accvgpr_write_b32 a11, v123\n"
"v_mfma_f32_16x16x32_bf16 v[4:7], a[236:239], a[140:143], v[4:7]\n"
"buffer_load_dwordx4 v249, s[24:27], s40 offen offset:1024 lds\n"
"v_mfma_f32_16x16x32_bf16 v[8:11], a[240:243], a[136:139], v[8:11]\n"
"v_accvgpr_write_b32 a2, v114\n"
"v_xor_b32 v247, s74, v247\n"
"v_mfma_f32_16x16x32_bf16 v[8:11], a[244:247], a[140:143], v[8:11]\n"
"v_permlane16_swap_b32 v120, v122\n"
"v_mfma_f32_16x16x32_bf16 v[12:15], a[248:251], a[136:139], v[12:15]\n"
"ds_read_b128 a[44:47], v182 offset:0\n"
"v_cvt_pk_bf16_f32 v97, v98, v99\n"
"v_mfma_f32_16x16x32_bf16 v[12:15], a[252:255], a[140:143], v[12:15]\n"
"ds_read_b128 a[12:15], v181 offset:32768\n"
"v_accvgpr_write_b32 a0, v112\n"
"v_mfma_f32_16x16x32_bf16 v[28:31], a[248:251], a[144:147], v[28:31]\n"
"ds_read_b64_tr_b16 a[200:201], v245 offset:0\n"
"ds_read_b64_tr_b16 a[220:221], v245 offset:9216\n"
"v_mfma_f32_16x16x32_bf16 v[28:31], a[252:255], a[148:151], v[28:31]\n"
"buffer_load_dwordx4 v254, s[24:27], s40 offen offset:2048 lds\n"
"v_accvgpr_write_b32 a10, v122\n"
"v_accvgpr_write_b32 a16, v128\n"
"v_permlane16_swap_b32 v144, v146\n"
"v_permlane16_swap_b32 v145, v147\n"
"v_accvgpr_write_b32 a34, v146\n"
"v_permlane16_swap_b32 v152, v154\n"
"v_cvt_pk_bf16_f32 v98, v100, v101\n"
"v_cvt_pk_bf16_f32 v104, v104, v105\n"
"v_pk_fma_f32 v[64:65], v[64:65], s[70:71], s[70:71] op_sel:[0,0,1] op_sel_hi:[1,0,1]\nv_pk_fma_f32 v[66:67], v[66:67], s[70:71], s[70:71] op_sel:[0,0,1] op_sel_hi:[1,0,1]\nv_pk_fma_f32 v[68:69], v[68:69], s[70:71], s[70:71] op_sel:[0,0,1] op_sel_hi:[1,0,1]\nv_pk_fma_f32 v[70:71], v[70:71], s[70:71], s[70:71] op_sel:[0,0,1] op_sel_hi:[1,0,1]\nv_pk_fma_f32 v[72:73], v[72:73], s[70:71], s[70:71] op_sel:[0,0,1] op_sel_hi:[1,0,1]\nv_pk_fma_f32 v[74:75], v[74:75], s[70:71], s[70:71] op_sel:[0,0,1] op_sel_hi:[1,0,1]\nv_pk_fma_f32 v[76:77], v[76:77], s[70:71], s[70:71] op_sel:[0,0,1] op_sel_hi:[1,0,1]\nv_pk_fma_f32 v[78:79], v[78:79], s[70:71], s[70:71] op_sel:[0,0,1] op_sel_hi:[1,0,1]\nv_pk_fma_f32 v[48:49], v[48:49], s[70:71], s[70:71] op_sel:[0,0,1] op_sel_hi:[1,0,1]\nv_pk_fma_f32 v[50:51], v[50:51], s[70:71], s[70:71] op_sel:[0,0,1] op_sel_hi:[1,0,1]\nv_pk_fma_f32 v[52:53], v[52:53], s[70:71], s[70:71] op_sel:[0,0,1] op_sel_hi:[1,0,1]\nv_pk_fma_f32 v[54:55], v[54:55], s[70:71], s[70:71] op_sel:[0,0,1] op_sel_hi:[1,0,1]\nv_pk_fma_f32 v[56:57], v[56:57], s[70:71], s[70:71] op_sel:[0,0,1] op_sel_hi:[1,0,1]\nv_pk_fma_f32 v[58:59], v[58:59], s[70:71], s[70:71] op_sel:[0,0,1] op_sel_hi:[1,0,1]\nv_pk_fma_f32 v[60:61], v[60:61], s[70:71], s[70:71] op_sel:[0,0,1] op_sel_hi:[1,0,1]\nv_pk_fma_f32 v[62:63], v[62:63], s[70:71], s[70:71] op_sel:[0,0,1] op_sel_hi:[1,0,1]\n"
"v_cvt_pk_bf16_f32 v64, v64, v65\n"
"v_mfma_f32_16x16x32_bf16 v[24:27], a[240:243], a[144:147], v[24:27]\n"
"s_add_u32 s49, s49, 128\n"
"v_permlane16_swap_b32 v136, v138\n"
"v_mfma_f32_16x16x32_bf16 v[24:27], a[244:247], a[148:151], v[24:27]\n"
"s_sub_u32 s41, s49, 1280\n"
"v_permlane16_swap_b32 v153, v155\n"
"v_mfma_f32_16x16x32_bf16 v[20:23], a[232:235], a[144:147], v[20:23]\n"
"s_min_u32 s49, s49, s41\n"
"ds_read_b64_tr_b16 a[204:205], v245 offset:8192\n"
"ds_read_b128 a[172:175], v251 offset:0\n"
"v_mfma_f32_16x16x32_bf16 v[20:23], a[236:239], a[148:151], v[20:23]\n"
"s_xor_b32 s51, s51, s74\n"
"ds_read_b128 a[140:143], v247 offset:0\n"
"v_cvt_pk_bf16_f32 v65, v66, v67\n"
"v_mfma_f32_16x16x32_bf16 v[16:19], a[224:227], a[144:147], v[16:19]\n"
"ds_read_b128 a[136:139], v246 offset:0\n"
"v_accvgpr_write_b32 a41, v153\n"
"v_mfma_f32_16x16x32_bf16 v[16:19], a[228:231], a[148:151], v[16:19]\n"
"ds_read_b64_tr_b16 a[192:193], v244 offset:0\n"
"v_accvgpr_write_b32 a40, v152\n"
"v_mfma_f32_16x16x32_bf16 v[32:35], a[224:227], a[152:155], v[32:35]\n"
"ds_read_b64_tr_b16 a[198:199], v244 offset:8704\n"
"v_accvgpr_write_b32 a24, v136\n"
"v_mfma_f32_16x16x32_bf16 v[32:35], a[228:231], a[156:159], v[32:35]\n"
"ds_read_b64_tr_b16 a[202:203], v245 offset:512\n"
"v_accvgpr_write_b32 a17, v129\n"
"v_mfma_f32_16x16x32_bf16 v[36:39], a[232:235], a[152:155], v[36:39]\n"
"ds_read_b64_tr_b16 a[206:207], v245 offset:8704\n"
"v_cvt_pk_bf16_f32 v89, v90, v91\n"
"v_mfma_f32_16x16x32_bf16 v[36:39], a[236:239], a[156:159], v[36:39]\n"
"ds_read_b128 a[20:23], v181 offset:45056\n"
"v_permlane16_swap_b32 v168, v170\n"
"v_accvgpr_write_b32 a8, v120\n"
"v_accvgpr_write_b32 a18, v130\n"
"v_accvgpr_write_b32 a19, v131\n"
"v_bfe_u32 v100, v242, 9, 1\n"
"v_pk_fma_f32 v[0:1], v[0:1], s[70:71], s[70:71] op_sel:[0,0,1] op_sel_hi:[1,0,1]\nv_pk_fma_f32 v[2:3], v[2:3], s[70:71], s[70:71] op_sel:[0,0,1] op_sel_hi:[1,0,1]\nv_pk_fma_f32 v[4:5], v[4:5], s[70:71], s[70:71] op_sel:[0,0,1] op_sel_hi:[1,0,1]\nv_pk_fma_f32 v[6:7], v[6:7], s[70:71], s[70:71] op_sel:[0,0,1] op_sel_hi:[1,0,1]\nv_pk_fma_f32 v[8:9], v[8:9], s[70:71], s[70:71] op_sel:[0,0,1] op_sel_hi:[1,0,1]\nv_pk_fma_f32 v[10:11], v[10:11], s[70:71], s[70:71] op_sel:[0,0,1] op_sel_hi:[1,0,1]\nv_pk_fma_f32 v[12:13], v[12:13], s[70:71], s[70:71] op_sel:[0,0,1] op_sel_hi:[1,0,1]\nv_pk_fma_f32 v[14:15], v[14:15], s[70:71], s[70:71] op_sel:[0,0,1] op_sel_hi:[1,0,1]\nv_pk_fma_f32 v[16:17], v[16:17], s[70:71], s[70:71] op_sel:[0,0,1] op_sel_hi:[1,0,1]\nv_pk_fma_f32 v[18:19], v[18:19], s[70:71], s[70:71] op_sel:[0,0,1] op_sel_hi:[1,0,1]\nv_pk_fma_f32 v[20:21], v[20:21], s[70:71], s[70:71] op_sel:[0,0,1] op_sel_hi:[1,0,1]\nv_pk_fma_f32 v[22:23], v[22:23], s[70:71], s[70:71] op_sel:[0,0,1] op_sel_hi:[1,0,1]\nv_pk_fma_f32 v[24:25], v[24:25], s[70:71], s[70:71] op_sel:[0,0,1] op_sel_hi:[1,0,1]\nv_pk_fma_f32 v[26:27], v[26:27], s[70:71], s[70:71] op_sel:[0,0,1] op_sel_hi:[1,0,1]\nv_pk_fma_f32 v[28:29], v[28:29], s[70:71], s[70:71] op_sel:[0,0,1] op_sel_hi:[1,0,1]\nv_pk_fma_f32 v[30:31], v[30:31], s[70:71], s[70:71] op_sel:[0,0,1] op_sel_hi:[1,0,1]\n"
"v_cvt_pk_bf16_f32 v16, v16, v17\n"
"v_mfma_f32_16x16x32_bf16 v[40:43], a[240:243], a[152:155], v[40:43]\n"
"v_accvgpr_write_b32 a43, v155\n"
"v_cvt_pk_bf16_f32 v0, v0, v1\n"
"v_mfma_f32_16x16x32_bf16 v[40:43], a[244:247], a[156:159], v[40:43]\n"
"ds_read_b128 a[144:147], v246 offset:2048\n"
"v_accvgpr_write_b32 a35, v147\n"
"v_mfma_f32_16x16x32_bf16 v[44:47], a[248:251], a[152:155], v[44:47]\n"
"s_add_u32 s47, s47, 1\n"
"v_cvt_pk_bf16_f32 v56, v56, v57\n"
"v_cvt_pk_bf16_f32 v17, v18, v19\n"
"v_mfma_f32_16x16x32_bf16 v[44:47], a[252:255], a[156:159], v[44:47]\n"
"s_waitcnt lgkmcnt(5)\n"
"ds_read_b128 a[152:155], v246 offset:4096\n"
"v_cvt_pk_bf16_f32 v57, v58, v59\n"
"v_mfma_f32_16x16x32_bf16 v[112:115], a[192:195], a[136:139], v[228:231]\n"
"s_waitcnt lgkmcnt(5)\n"
"v_cvt_pk_bf16_f32 v163, v166, v167\n"
"v_cvt_pk_bf16_f32 v8, v8, v9\n"
"v_mfma_f32_16x16x32_bf16 v[112:115], a[196:199], a[140:143], v[112:115]\n"
"s_waitcnt lgkmcnt(4)\n"
"ds_read_b128 a[148:151], v247 offset:2048\n"
"v_cvt_pk_bf16_f32 v24, v24, v25\n"
"v_mfma_f32_16x16x32_bf16 v[116:119], a[200:203], a[136:139], v[228:231]\n"
"s_waitcnt vmcnt(11) lgkmcnt(3)\n"
"ds_read_b128 a[188:191], v251 offset:4096\n"
"v_cvt_pk_bf16_f32 v66, v68, v69\n"
"v_mfma_f32_16x16x32_bf16 v[116:119], a[204:207], a[140:143], v[116:119]\n"
"s_barrier\n"
"ds_read_b128 a[176:179], v250 offset:2048\n"
"v_permlane16_swap_b32 v161, v163\n"
"v_accvgpr_write_b32 a26, v138\n"
"v_accvgpr_write_b32 a27, v139\n"
"v_pk_fma_f32 v[32:33], v[32:33], s[70:71], s[70:71] op_sel:[0,0,1] op_sel_hi:[1,0,1]\nv_pk_fma_f32 v[34:35], v[34:35], s[70:71], s[70:71] op_sel:[0,0,1] op_sel_hi:[1,0,1]\nv_pk_fma_f32 v[36:37], v[36:37], s[70:71], s[70:71] op_sel:[0,0,1] op_sel_hi:[1,0,1]\nv_pk_fma_f32 v[38:39], v[38:39], s[70:71], s[70:71] op_sel:[0,0,1] op_sel_hi:[1,0,1]\nv_pk_fma_f32 v[40:41], v[40:41], s[70:71], s[70:71] op_sel:[0,0,1] op_sel_hi:[1,0,1]\nv_pk_fma_f32 v[42:43], v[42:43], s[70:71], s[70:71] op_sel:[0,0,1] op_sel_hi:[1,0,1]\nv_pk_fma_f32 v[44:45], v[44:45], s[70:71], s[70:71] op_sel:[0,0,1] op_sel_hi:[1,0,1]\nv_pk_fma_f32 v[46:47], v[46:47], s[70:71], s[70:71] op_sel:[0,0,1] op_sel_hi:[1,0,1]\n"
"v_mfma_f32_16x16x32_bf16 v[120:123], a[208:211], a[136:139], v[228:231]\n"
"s_add_u32 s40, s48, 256\n"
"s_xor_b32 m0, s50, 0xc000\n"
"ds_read_b64_tr_b16 a[228:229], v244 offset:24576\n"
"v_mfma_f32_16x16x32_bf16 v[120:123], a[212:215], a[140:143], v[120:123]\n"
"buffer_load_dwordx4 v224, s[28:31], s40 offen lds\n"
"v_mfma_f32_16x16x32_bf16 v[124:127], a[216:219], a[136:139], v[228:231]\n"
"ds_read_b128 a[160:163], v252 offset:0\n"
"v_cvt_pk_bf16_f32 v32, v32, v33\n"
"v_mfma_f32_16x16x32_bf16 v[124:127], a[220:223], a[140:143], v[124:127]\n"
"s_waitcnt lgkmcnt(5)\n"
"v_accvgpr_write_b32 a25, v137\n"
"v_cvt_pk_bf16_f32 v81, v82, v83\n"
"v_mfma_f32_16x16x32_bf16 v[140:143], a[216:219], a[144:147], v[228:231]\n"
"s_waitcnt lgkmcnt(4)\n"
"ds_read_b128 a[156:159], v247 offset:4096\n"
"v_cvt_pk_bf16_f32 v48, v48, v49\n"
"v_mfma_f32_16x16x32_bf16 v[140:143], a[220:223], a[148:151], v[140:143]\n"
"ds_read_b128 a[180:183], v251 offset:2048\n"
"v_cvt_pk_bf16_f32 v25, v26, v27\n"
"v_mfma_f32_16x16x32_bf16 v[136:139], a[208:211], a[144:147], v[228:231]\n"
"v_cvt_pk_bf16_f32 v33, v34, v35\n"
"v_cvt_pk_bf16_f32 v40, v40, v41\n"
"v_mfma_f32_16x16x32_bf16 v[136:139], a[212:215], a[148:151], v[136:139]\n"
"buffer_load_dwordx4 v225, s[28:31], s40 offen offset:1024 lds\n"
"v_mfma_f32_16x16x32_bf16 v[132:135], a[200:203], a[144:147], v[228:231]\n"
"v_cvt_pk_bf16_f32 v91, v94, v95\n"
"v_cvt_pk_bf16_f32 v9, v10, v11\n"
"v_mfma_f32_16x16x32_bf16 v[132:135], a[204:207], a[148:151], v[132:135]\n"
"v_accvgpr_write_b32 a33, v145\n"
"v_cvt_pk_bf16_f32 v99, v102, v103\n"
"v_mfma_f32_16x16x32_bf16 v[128:131], a[192:195], a[144:147], v[228:231]\n"
"ds_read_b128 a[164:167], v253 offset:0\n"
"ds_read_b64_tr_b16 a[244:245], v244 offset:25600\n"
"v_mfma_f32_16x16x32_bf16 v[128:131], a[196:199], a[148:151], v[128:131]\n"
"ds_write_b128 v243, v[168:171] offset:2048\n"
"v_accvgpr_write_b32 a32, v144\n"
"v_accvgpr_write_b32 a42, v154\n"
"v_cvt_pk_bf16_f32 v72, v72, v73\n"
"v_cvt_pk_bf16_f32 v73, v74, v75\n"
"v_cvt_pk_bf16_f32 v1, v2, v3\n"
"v_mfma_f32_16x16x32_bf16 v[144:147], a[192:195], a[152:155], v[228:231]\n"
"s_waitcnt lgkmcnt(4)\n"
"v_cvt_pk_bf16_f32 v10, v12, v13\n"
"v_cvt_pk_bf16_f32 v18, v20, v21\n"
"v_mfma_f32_16x16x32_bf16 v[144:147], a[196:199], a[156:159], v[144:147]\n"
"buffer_load_dwordx4 v226, s[28:31], s40 offen offset:2048 lds\n"
"v_mfma_f32_16x16x32_bf16 v[148:151], a[200:203], a[152:155], v[228:231]\n"
"v_cvt_pk_bf16_f32 v3, v6, v7\n"
"v_cvt_pk_bf16_f32 v35, v38, v39\n"
"v_mfma_f32_16x16x32_bf16 v[148:151], a[204:207], a[156:159], v[148:151]\n"
"v_cvt_pk_bf16_f32 v2, v4, v5\n"
"v_cvt_pk_bf16_f32 v11, v14, v15\n"
"v_mfma_f32_16x16x32_bf16 v[152:155], a[208:211], a[152:155], v[228:231]\n"
"ds_read_b64_tr_b16 a[248:249], v245 offset:17408\n"
"v_cvt_pk_bf16_f32 v34, v36, v37\n"
"v_mfma_f32_16x16x32_bf16 v[152:155], a[212:215], a[156:159], v[152:155]\n"
"ds_write_b128 v243, v[160:163] offset:0\n"
"v_and_b32 v102, 0xf0, v242\n"
"v_cvt_pk_bf16_f32 v105, v106, v107\n"
"v_permlane16_swap_b32 v1, v3\n"
"v_permlane16_swap_b32 v9, v11\n"
"v_mfma_f32_16x16x32_bf16 v[156:159], a[216:219], a[152:155], v[228:231]\n"
"ds_read_b64_tr_b16 a[236:237], v245 offset:24576\n"
"v_cvt_pk_bf16_f32 v19, v22, v23\n"
"v_mfma_f32_16x16x32_bf16 v[156:159], a[220:223], a[156:159], v[156:159]\n"
"buffer_load_dwordx4 v227, s[28:31], s40 offen offset:3072 lds\n"
"v_mfma_f32_16x16x32_bf16 v[172:175], a[216:219], a[160:163], v[228:231]\n"
"s_waitcnt lgkmcnt(3)\n"
"s_cmp_eq_u32 s47, 8\n"
"v_cvt_pk_bf16_f32 v26, v28, v29\n"
"v_cvt_pk_bf16_f32 v27, v30, v31\n"
"v_mfma_f32_16x16x32_bf16 v[172:175], a[220:223], a[164:167], v[172:175]\n"
"s_cselect_b32 s41, s52, s58\n"
"v_permlane16_swap_b32 v16, v18\n"
"v_mfma_f32_16x16x32_bf16 v[168:171], a[208:211], a[160:163], v[228:231]\n"
"s_add_u32 s48, s48, s41\n"
"ds_read_b64_tr_b16 a[254:255], v245 offset:26112\n"
"v_accvgpr_write_b32 a59, v11\n"
"v_mfma_f32_16x16x32_bf16 v[168:171], a[212:215], a[164:167], v[168:171]\n"
"s_sub_u32 s41, s48, 5242880\n"
"ds_read_b64_tr_b16 a[226:227], v244 offset:16896\n"
"v_lshl_add_u32 v100, v100, 8, v102\n"
"v_mfma_f32_16x16x32_bf16 v[164:167], a[200:203], a[160:163], v[228:231]\n"
"s_min_u32 s48, s48, s41\n"
"ds_read_b64_tr_b16 a[240:241], v244 offset:17408\n"
"v_cvt_pk_bf16_f32 v67, v70, v71\n"
"v_mfma_f32_16x16x32_bf16 v[164:167], a[204:207], a[164:167], v[164:167]\n"
"ds_read_b64_tr_b16 a[234:235], v245 offset:16896\n"
"ds_read_b64_tr_b16 a[238:239], v245 offset:25088\n"
"v_cvt_pk_bf16_f32 v83, v86, v87\n"
"v_permlane16_swap_b32 v8, v10\n"
"v_permlane16_swap_b32 v17, v19\n"
"v_permlane16_swap_b32 v25, v27\n"
"v_cvt_pk_bf16_f32 v41, v42, v43\n"
"v_mfma_f32_16x16x32_bf16 v[160:163], a[192:195], a[160:163], v[228:231]\n"
"s_mov_b32 m0, s50\n"
"v_permlane16_swap_b32 v24, v26\n"
"v_mfma_f32_16x16x32_bf16 v[160:163], a[196:199], a[164:167], v[160:163]\n"
"buffer_load_dwordx4 v224, s[28:31], s48 offen lds\n"
"v_mfma_f32_16x16x32_bf16 v[176:179], a[192:195], a[168:171], v[228:231]\n"
"v_accvgpr_write_b32 a74, v26\n"
"v_cvt_pk_bf16_f32 v43, v46, v47\n"
"v_mfma_f32_16x16x32_bf16 v[176:179], a[196:199], a[172:175], v[176:179]\n"
"v_bfe_u32 v102, v242, 8, 1\n"
"v_cvt_pk_bf16_f32 v49, v50, v51\n"
"v_mfma_f32_16x16x32_bf16 v[180:183], a[200:203], a[168:171], v[228:231]\n"
"ds_read_b64_tr_b16 a[232:233], v245 offset:16384\n"
"v_cvt_pk_bf16_f32 v75, v78, v79\n"
"v_mfma_f32_16x16x32_bf16 v[180:183], a[204:207], a[172:175], v[180:183]\n"
"buffer_load_dwordx4 v225, s[28:31], s48 offen offset:1024 lds\n"
"v_mfma_f32_16x16x32_bf16 v[184:187], a[208:211], a[168:171], v[228:231]\n"
"v_permlane16_swap_b32 v33, v35\n"
"v_mfma_f32_16x16x32_bf16 v[184:187], a[212:215], a[172:175], v[184:187]\n"
"v_permlane16_swap_b32 v0, v2\n"
"v_mfma_f32_16x16x32_bf16 v[188:191], a[216:219], a[168:171], v[228:231]\n"
"ds_read_b64_tr_b16 a[242:243], v244 offset:17920\n"
"v_cvt_pk_bf16_f32 v90, v92, v93\n"
"v_mfma_f32_16x16x32_bf16 v[188:191], a[220:223], a[172:175], v[188:191]\n"
"v_lshl_add_u32 v100, v102, 3, v100\n"
"v_xor_b32_e64 v102, s42, 1\n"
"v_mfma_f32_16x16x32_bf16 v[204:207], a[216:219], a[176:179], v[228:231]\n"
"v_cvt_pk_bf16_f32 v107, v110, v111\n"
"v_bfe_u32 v101, v242, 9, 1\n"
"v_mfma_f32_16x16x32_bf16 v[204:207], a[220:223], a[180:183], v[204:207]\n"
"buffer_load_dwordx4 v226, s[28:31], s48 offen offset:2048 lds\n"
"v_mfma_f32_16x16x32_bf16 v[200:203], a[208:211], a[176:179], v[228:231]\n"
"v_lshl_add_u32 v100, v102, 10, v100\n"
"v_cvt_pk_bf16_f32 v82, v84, v85\n"
"v_mfma_f32_16x16x32_bf16 v[200:203], a[212:215], a[180:183], v[200:203]\n"
"ds_read_b64_tr_b16 a[250:251], v245 offset:17920\n"
"ds_read_b64_tr_b16 a[252:253], v245 offset:25600\n"
"v_mfma_f32_16x16x32_bf16 v[196:199], a[200:203], a[176:179], v[228:231]\n"
"ds_read_b64_tr_b16 a[246:247], v244 offset:26112\n"
"v_cvt_pk_bf16_f32 v58, v60, v61\n"
"v_mfma_f32_16x16x32_bf16 v[196:199], a[204:207], a[180:183], v[196:199]\n"
"v_cvt_pk_bf16_f32 v74, v76, v77\n"
"v_accvgpr_write_b32 a56, v8\n"
"v_mfma_f32_16x16x32_bf16 v[192:195], a[192:195], a[176:179], v[228:231]\n"
"ds_read_b64_tr_b16 a[224:225], v244 offset:16384\n"
"ds_read_b64_tr_b16 a[230:231], v244 offset:25088\n"
"v_mfma_f32_16x16x32_bf16 v[192:195], a[196:199], a[180:183], v[192:195]\n"
"buffer_load_dwordx4 v227, s[28:31], s48 offen offset:3072 lds\n"
"v_mfma_f32_16x16x32_bf16 v[208:211], a[192:195], a[184:187], v[228:231]\n"
"s_xor_b32 s50, s50, s74\n"
"v_cvt_pk_bf16_f32 v51, v54, v55\n"
"v_accvgpr_write_b32 a48, v0\n"
"v_mfma_f32_16x16x32_bf16 v[208:211], a[196:199], a[188:191], v[208:211]\n"
"v_add_u32 v100, 94208, v100\n"
"v_accvgpr_write_b32 a50, v2\n"
"v_mfma_f32_16x16x32_bf16 v[212:215], a[200:203], a[184:187], v[228:231]\n"
"v_permlane16_swap_b32 v32, v34\n"
"v_mfma_f32_16x16x32_bf16 v[212:215], a[204:207], a[188:191], v[212:215]\n"
"ds_write2st64_b64 v100, v[72:73], v[74:75] offset0:64 offset1:65\n"
"v_cvt_pk_bf16_f32 v106, v108, v109\n"
"v_cvt_pk_bf16_f32 v50, v52, v53\n"
"v_permlane16_swap_b32 v41, v43\n"
"v_mfma_f32_16x16x32_bf16 v[216:219], a[208:211], a[184:187], v[228:231]\n"
"v_accvgpr_write_b32 a51, v3\n"
"v_and_b32 v102, 0xf0, v242\n"
"v_mfma_f32_16x16x32_bf16 v[216:219], a[212:215], a[188:191], v[216:219]\n"
"ds_write2st64_b64 v100, v[104:105], v[106:107] offset0:128 offset1:129\n"
"v_accvgpr_write_b32 a49, v1\n"
"v_accvgpr_write_b32 a57, v9\n"
"v_mfma_f32_16x16x32_bf16 v[220:223], a[216:219], a[184:187], v[228:231]\n"
"v_accvgpr_write_b32 a73, v25\n"
"v_accvgpr_write_b32 a83, v35\n"
"v_mfma_f32_16x16x32_bf16 v[220:223], a[220:223], a[188:191], v[220:223]\n"
"s_waitcnt lgkmcnt(2)\n"
"ds_write2st64_b64 v100, v[80:81], v[82:83] offset0:88 offset1:89\n"
"v_permlane16_swap_b32 v49, v51\n"
"v_accvgpr_write_b32 a58, v10\n"
"v_mfma_f32_16x16x32_bf16 v[0:3], a[224:227], a[136:139], v[228:231]\n"
"v_permlane16_swap_b32 v48, v50\n"
"v_mfma_f32_16x16x32_bf16 v[0:3], a[228:231], a[140:143], v[0:3]\n"
"ds_write_b128 v243, v[48:51] offset:4096\n"
"v_accvgpr_write_b32 a65, v17\n"
"v_mfma_f32_16x16x32_bf16 v[4:7], a[232:235], a[136:139], v[228:231]\n"
"s_waitcnt vmcnt(8)\n"
"v_accvgpr_write_b32 a72, v24\n"
"v_accvgpr_write_b32 a89, v41\n"
"v_mfma_f32_16x16x32_bf16 v[4:7], a[236:239], a[140:143], v[4:7]\n"
"s_barrier\n"
"ds_write2st64_b64 v100, v[88:89], v[90:91] offset0:96 offset1:97\n"
"v_mfma_f32_16x16x32_bf16 v[8:11], a[240:243], a[136:139], v[228:231]\n"
"s_mov_b32 m0, s51\n"
"v_accvgpr_write_b32 a75, v27\n"
"v_accvgpr_write_b32 a82, v34\n"
"v_mfma_f32_16x16x32_bf16 v[8:11], a[244:247], a[140:143], v[8:11]\n"
"buffer_load_dwordx4 v248, s[24:27], s49 offen lds\n"
"v_mfma_f32_16x16x32_bf16 v[12:15], a[248:251], a[136:139], v[228:231]\n"
"v_accvgpr_write_b32 a67, v19\n"
"v_xor_b32 v246, s74, v246\n"
"v_mfma_f32_16x16x32_bf16 v[12:15], a[252:255], a[140:143], v[12:15]\n"
"ds_write2st64_b64 v100, v[96:97], v[98:99] offset0:120 offset1:121\n"
"v_cvt_pk_bf16_f32 v59, v62, v63\n"
"v_mfma_f32_16x16x32_bf16 v[28:31], a[248:251], a[144:147], v[228:231]\n"
"v_accvgpr_write_b32 a64, v16\n"
"v_cvt_pk_bf16_f32 v42, v44, v45\n"
"v_mfma_f32_16x16x32_bf16 v[28:31], a[252:255], a[148:151], v[28:31]\n"
"ds_write2st64_b64 v100, v[64:65], v[66:67] offset0:0 offset1:1\n"
"v_permlane16_swap_b32 v56, v58\n"
"v_permlane16_swap_b32 v40, v42\n"
"v_mfma_f32_16x16x32_bf16 v[24:27], a[240:243], a[144:147], v[228:231]\n"
"ds_read_b128 a[136:139], v246 offset:0\n"
"v_accvgpr_write_b32 a66, v18\n"
"v_mfma_f32_16x16x32_bf16 v[24:27], a[244:247], a[148:151], v[24:27]\n"
"buffer_load_dwordx4 v249, s[24:27], s49 offen offset:1024 lds\n"
"v_mfma_f32_16x16x32_bf16 v[20:23], a[232:235], a[144:147], v[228:231]\n"
"v_permlane16_swap_b32 v57, v59\n"
"v_mfma_f32_16x16x32_bf16 v[20:23], a[236:239], a[148:151], v[20:23]\n"
"ds_write_b128 v243, v[56:59] offset:6144\n"
"v_mfma_f32_16x16x32_bf16 v[16:19], a[224:227], a[144:147], v[228:231]\n"
"v_accvgpr_write_b32 a80, v32\n"
"v_xor_b32 v244, s74, v244\n"
"v_mfma_f32_16x16x32_bf16 v[16:19], a[228:231], a[148:151], v[16:19]\n"
"v_accvgpr_write_b32 a81, v33\n"
"v_accvgpr_write_b32 a90, v42\n"
"v_mfma_f32_16x16x32_bf16 v[32:35], a[224:227], a[152:155], v[228:231]\n"
"ds_read_b64_tr_b16 a[212:213], v244 offset:9216\n"
"v_accvgpr_write_b32 a88, v40\n"
"v_mfma_f32_16x16x32_bf16 v[32:35], a[228:231], a[156:159], v[32:35]\n"
"buffer_load_dwordx4 v254, s[24:27], s49 offen offset:2048 lds\n"
"v_mfma_f32_16x16x32_bf16 v[36:39], a[232:235], a[152:155], v[228:231]\n"
"s_waitcnt vmcnt(63) lgkmcnt(2)\n"
"v_accvgpr_write_b32 a91, v43\n"
"v_xor_b32 v247, s74, v247\n"
"v_mfma_f32_16x16x32_bf16 v[36:39], a[236:239], a[156:159], v[36:39]\n"
"s_barrier\n"
"v_mfma_f32_16x16x32_bf16 v[40:43], a[240:243], a[152:155], v[228:231]\n"
"ds_read_b64_tr_b16 a[192:193], v244 offset:0\n"
"ds_read_b128 a[144:147], v246 offset:2048\n"
"v_mfma_f32_16x16x32_bf16 v[40:43], a[244:247], a[156:159], v[40:43]\n"
"ds_read_b128 a[148:151], v247 offset:2048\n"
"v_xor_b32 v251, s74, v251\n"
"v_mfma_f32_16x16x32_bf16 v[44:47], a[248:251], a[152:155], v[228:231]\n"
"ds_read_b128 a[152:155], v246 offset:4096\n"
"v_lshl_add_u32 v101, v101, 8, v102\n"
"v_mfma_f32_16x16x32_bf16 v[44:47], a[252:255], a[156:159], v[44:47]\n"
"buffer_load_dwordx4 v255, s[24:27], s49 offen offset:3072 lds\n"
"v_mfma_f32_16x16x32_bf16 v[60:63], a[248:251], a[160:163], v[228:231]\n"
"v_bfe_u32 v102, v242, 8, 1\n"
"v_lshl_add_u32 v101, v102, 9, v101\n"
"v_mfma_f32_16x16x32_bf16 v[60:63], a[252:255], a[164:167], v[60:63]\n"
"v_xor_b32_e64 v102, s42, 0\n"
"v_lshl_add_u32 v101, v102, 10, v101\n"
"v_mfma_f32_16x16x32_bf16 v[56:59], a[240:243], a[160:163], v[228:231]\n"
"ds_read_b128 a[156:159], v247 offset:4096\n"
"v_xor_b32 v245, s74, v245\n"
"v_mfma_f32_16x16x32_bf16 v[56:59], a[244:247], a[164:167], v[56:59]\n"
"s_add_u32 s40, s49, 40960\n"
"ds_read_b64_tr_b16 a[210:211], v244 offset:1536\n"
"ds_read_b64_tr_b16 a[216:217], v245 offset:1024\n"
"v_mfma_f32_16x16x32_bf16 v[52:55], a[232:235], a[160:163], v[228:231]\n"
"s_add_u32 m0, s51, 4096\n"
"ds_read_b64_tr_b16 a[196:197], v244 offset:8192\n"
"ds_read_b64_tr_b16 a[214:215], v244 offset:9728\n"
"v_mfma_f32_16x16x32_bf16 v[52:55], a[236:239], a[164:167], v[52:55]\n"
"buffer_load_dwordx4 v248, s[24:27], s40 offen lds\n"
"v_mfma_f32_16x16x32_bf16 v[48:51], a[224:227], a[160:163], v[228:231]\n"
"v_add_u32 v101, 94208, v101\n"
"v_add_u32 v102, s75, v101\n"
"v_mfma_f32_16x16x32_bf16 v[48:51], a[228:231], a[164:167], v[48:51]\n"
"ds_read_b128 a[52:55], v101 offset:0\n"
"ds_read_b128 a[92:95], v102 offset:0\n"
"v_mfma_f32_16x16x32_bf16 v[64:67], a[224:227], a[168:171], v[228:231]\n"
"ds_read_b128 a[68:71], v101 offset:45056\n"
"v_mfma_f32_16x16x32_bf16 v[64:67], a[228:231], a[172:175], v[64:67]\n"
"ds_read_b128 a[60:63], v101 offset:32768\n"
"v_xor_b32 v252, s74, v252\n"
"v_mfma_f32_16x16x32_bf16 v[68:71], a[232:235], a[168:171], v[228:231]\n"
"ds_read_b64_tr_b16 a[218:219], v245 offset:1536\n"
"ds_read_b128 a[76:79], v101 offset:49152\n"
"v_mfma_f32_16x16x32_bf16 v[68:71], a[236:239], a[172:175], v[68:71]\n"
"buffer_load_dwordx4 v249, s[24:27], s40 offen offset:1024 lds\n"
"v_mfma_f32_16x16x32_bf16 v[72:75], a[240:243], a[168:171], v[228:231]\n"
"v_xor_b32 v250, s74, v250\n"
"v_xor_b32 v253, s74, v253\n"
"v_mfma_f32_16x16x32_bf16 v[72:75], a[244:247], a[172:175], v[72:75]\n"
"ds_read_b64_tr_b16 a[206:207], v245 offset:8704\n"
"ds_read_b128 a[84:87], v101 offset:61440\n"
"v_mfma_f32_16x16x32_bf16 v[76:79], a[248:251], a[168:171], v[228:231]\n"
"ds_read_b64_tr_b16 a[204:205], v245 offset:8192\n"
"ds_read_b64_tr_b16 a[220:221], v245 offset:9216\n"
"v_mfma_f32_16x16x32_bf16 v[76:79], a[252:255], a[172:175], v[76:79]\n"
"ds_read_b64_tr_b16 a[194:195], v244 offset:512\n"
"ds_read_b64_tr_b16 a[198:199], v244 offset:8704\n"
"v_mfma_f32_16x16x32_bf16 v[92:95], a[248:251], a[176:179], v[228:231]\n"
"ds_read_b64_tr_b16 a[222:223], v245 offset:9728\n"
"ds_read_b128 a[160:163], v252 offset:0\n"
"v_mfma_f32_16x16x32_bf16 v[92:95], a[252:255], a[180:183], v[92:95]\n"
"buffer_load_dwordx4 v254, s[24:27], s40 offen offset:2048 lds\n"
"v_mfma_f32_16x16x32_bf16 v[88:91], a[240:243], a[176:179], v[228:231]\n"
"s_add_u32 s49, s49, 128\n"
"ds_read_b64_tr_b16 a[202:203], v245 offset:512\n"
"ds_read_b128 a[140:143], v247 offset:0\n"
"v_mfma_f32_16x16x32_bf16 v[88:91], a[244:247], a[180:183], v[88:91]\n"
"s_sub_u32 s41, s49, 1280\n"
"ds_read_b64_tr_b16 a[208:209], v244 offset:1024\n"
"ds_read_b128 a[172:175], v251 offset:0\n"
"v_mfma_f32_16x16x32_bf16 v[84:87], a[232:235], a[176:179], v[228:231]\n"
"s_min_u32 s49, s49, s41\n"
"ds_read_b128 a[164:167], v253 offset:0\n"
"v_mfma_f32_16x16x32_bf16 v[84:87], a[236:239], a[180:183], v[84:87]\n"
"s_xor_b32 s51, s51, s74\n"
"ds_read_b64_tr_b16 a[200:201], v245 offset:0\n"
"ds_read_b128 a[168:171], v250 offset:0\n"
"v_mfma_f32_16x16x32_bf16 v[80:83], a[224:227], a[176:179], v[228:231]\n"
"s_sub_u32 s78, s78, s65\n"
"s_sub_u32 s79, s80, s67\n"
"s_mov_b32 s65, 0\n"
"v_mfma_f32_16x16x32_bf16 v[80:83], a[228:231], a[180:183], v[80:83]\n"
"s_cmp_gt_u32 s78, 21100\n"
"ds_read_b128 a[176:179], v250 offset:2048\n"
"ds_read_b128 a[180:183], v251 offset:2048\n"
"v_mfma_f32_16x16x32_bf16 v[96:99], a[224:227], a[184:187], v[228:231]\n"
"s_cselect_b32 s68, 1504, 0\n"
"s_cselect_b32 s72, 114, s72\n"
"s_cmp_lt_i32 s79, -2800\n"
"v_mfma_f32_16x16x32_bf16 v[96:99], a[228:231], a[188:191], v[96:99]\n"
"s_cselect_b32 s64, 1, s68\n"
"s_cmp_eq_u32 s42, 0\n"
"v_mfma_f32_16x16x32_bf16 v[100:103], a[232:235], a[184:187], v[228:231]\n"
"s_cselect_b32 s64, s64, 0\n"
"s_add_u32 s80, s8, s63\n"
"v_mfma_f32_16x16x32_bf16 v[100:103], a[236:239], a[188:191], v[100:103]\n"
"s_addc_u32 s81, s9, 0\n"
"s_mov_b32 s82, s68\n"
"v_mfma_f32_16x16x32_bf16 v[104:107], a[240:243], a[184:187], v[228:231]\n"
"s_mov_b64 vcc, s[64:65]\n"
"v_mfma_f32_16x16x32_bf16 v[104:107], a[244:247], a[188:191], v[104:107]\n"
"v_mfma_f32_16x16x32_bf16 v[108:111], a[248:251], a[184:187], v[228:231]\n"
"s_add_u32 s47, s47, 1\n"
"ds_read_b128 a[184:187], v250 offset:4096\n"
"v_mfma_f32_16x16x32_bf16 v[108:111], a[252:255], a[188:191], v[108:111]\n"
"s_waitcnt lgkmcnt(1)\n"
"ds_read_b128 a[188:191], v251 offset:4096\n"
"s_mov_b32 s62, 18\n"
"s_cmp_eq_u32 s68, 0\n"
"s_cbranch_scc1 LPL_%=\n"
"s_branch LPN_%=\n"
"LPT2_%=:\n"
"v_mfma_f32_16x16x32_bf16 v[112:115], a[192:195], a[136:139], v[112:115]\n"
"s_add_u32 s78, s32, s96\ns_addc_u32 s79, s33, 0\n"
"s_mov_b64 s[80:81], 0\n"
"s_mov_b32 s82, 0\n"
"v_mfma_f32_16x16x32_bf16 v[112:115], a[196:199], a[140:143], v[112:115]\n"
"v_xor_b32 v248, 64, v240\n"
"v_mfma_f32_16x16x32_bf16 v[116:119], a[200:203], a[136:139], v[116:119]\n"
"s_waitcnt vmcnt(11)\n"
"s_load_dword s80, s[78:79], 0\n"
"v_mfma_f32_16x16x32_bf16 v[116:119], a[204:207], a[140:143], v[116:119]\n"
"s_barrier\n"
"v_mfma_f32_16x16x32_bf16 v[120:123], a[208:211], a[136:139], v[120:123]\n"
"s_add_u32 s40, s48, 256\n"
"s_xor_b32 m0, s50, 0xc000\n"
"ds_read_b64_tr_b16 a[242:243], v244 offset:17920\n"
"v_mfma_f32_16x16x32_bf16 v[120:123], a[212:215], a[140:143], v[120:123]\n"
"buffer_load_dwordx4 v224, s[28:31], s40 offen lds\n"
"v_mfma_f32_16x16x32_bf16 v[124:127], a[216:219], a[136:139], v[124:127]\n"
"v_mfma_f32_16x16x32_bf16 v[124:127], a[220:223], a[140:143], v[124:127]\n"
"v_mfma_f32_16x16x32_bf16 v[140:143], a[216:219], a[144:147], v[140:143]\n"
"ds_read_b64_tr_b16 a[250:251], v245 offset:17920\n"
"v_mfma_f32_16x16x32_bf16 v[140:143], a[220:223], a[148:151], v[140:143]\n"
"v_mfma_f32_16x16x32_bf16 v[136:139], a[208:211], a[144:147], v[136:139]\n"
"ds_read_b64_tr_b16 a[248:249], v245 offset:17408\n"
"v_mfma_f32_16x16x32_bf16 v[136:139], a[212:215], a[148:151], v[136:139]\n"
"buffer_load_dwordx4 v225, s[28:31], s40 offen offset:1024 lds\n"
"v_mfma_f32_16x16x32_bf16 v[132:135], a[200:203], a[144:147], v[132:135]\n"
"s_add_u32 s82, s82, s80\n"
"v_mfma_f32_16x16x32_bf16 v[132:135], a[204:207], a[148:151], v[132:135]\n"
"s_add_u32 s82, s82, s80\n"
"v_mfma_f32_16x16x32_bf16 v[128:131], a[192:195], a[144:147], v[128:131]\n"
"s_add_u32 s82, s82, s80\n"
"ds_read_b64_tr_b16 a[252:253], v245 offset:25600\n"
"v_mfma_f32_16x16x32_bf16 v[128:131], a[196:199], a[148:151], v[128:131]\n"
"s_add_u32 s82, s82, s80\n"
"v_mfma_f32_16x16x32_bf16 v[144:147], a[192:195], a[152:155], v[144:147]\n"
"s_add_u32 s82, s82, s80\n"
"ds_read_b64_tr_b16 a[226:227], v244 offset:16896\n"
"v_bfe_u32 v228, v242, 8, 2\n"
"v_mfma_f32_16x16x32_bf16 v[144:147], a[196:199], a[156:159], v[144:147]\n"
"s_add_u32 s82, s82, s80\n"
"v_mfma_f32_16x16x32_bf16 v[148:151], a[200:203], a[152:155], v[148:151]\n"
"s_add_u32 s82, s82, s80\n"
"ds_read_b64_tr_b16 a[232:233], v245 offset:16384\n"
"v_and_b32 v229, 0x60, v242\n"
"v_mfma_f32_16x16x32_bf16 v[148:151], a[204:207], a[156:159], v[148:151]\n"
"s_add_u32 s82, s82, s80\n"
"buffer_load_dwordx4 v226, s[28:31], s40 offen offset:2048 lds\n"
"v_mfma_f32_16x16x32_bf16 v[152:155], a[208:211], a[152:155], v[152:155]\n"
"s_add_u32 s82, s82, s80\n"
"v_mfma_f32_16x16x32_bf16 v[152:155], a[212:215], a[156:159], v[152:155]\n"
"s_add_u32 s82, s82, s80\n"
"v_mfma_f32_16x16x32_bf16 v[156:159], a[216:219], a[152:155], v[156:159]\n"
"s_add_u32 s82, s82, s80\n"
"ds_read_b64_tr_b16 a[240:241], v244 offset:17408\n"
"v_mfma_f32_16x16x32_bf16 v[156:159], a[220:223], a[156:159], v[156:159]\n"
"s_add_u32 s82, s82, s80\n"
"v_lshlrev_b32 v228, 4, v228\n"
"v_mfma_f32_16x16x32_bf16 v[172:175], a[216:219], a[160:163], v[172:175]\n"
"s_add_u32 s82, s82, s80\n"
"ds_read_b64_tr_b16 a[230:231], v244 offset:25088\n"
"v_mfma_f32_16x16x32_bf16 v[172:175], a[220:223], a[164:167], v[172:175]\n"
"s_add_u32 s82, s82, s80\n"
"buffer_load_dwordx4 v227, s[28:31], s40 offen offset:3072 lds\n"
"v_mfma_f32_16x16x32_bf16 v[168:171], a[208:211], a[160:163], v[168:171]\n"
"s_add_u32 s82, s82, s80\n"
"s_mov_b32 s58, 122880\n"
"v_mfma_f32_16x16x32_bf16 v[168:171], a[212:215], a[164:167], v[168:171]\n"
"s_add_u32 s82, s82, s80\n"
"s_mov_b32 s47, 0\n"
"v_mfma_f32_16x16x32_bf16 v[164:167], a[200:203], a[160:163], v[164:167]\n"
"s_add_u32 s82, s82, s80\n"
"s_mov_b32 s52, s54\n"
"ds_read_b64_tr_b16 a[228:229], v244 offset:24576\n"
"v_mfma_f32_16x16x32_bf16 v[164:167], a[204:207], a[164:167], v[164:167]\n"
"s_load_dword s81, s[78:79], 0x2000\n"
"s_mov_b32 s53, s91\n"
"v_mfma_f32_16x16x32_bf16 v[160:163], a[192:195], a[160:163], v[160:163]\n"
"s_add_u32 s54, s54, s91\ns_mov_b32 s91, 0x20000\n"
"ds_read_b64_tr_b16 a[244:245], v244 offset:25600\n"
"v_mfma_f32_16x16x32_bf16 v[160:163], a[196:199], a[164:167], v[160:163]\n"
"v_xor_b32 v228, v228, v229\n"
"v_mfma_f32_16x16x32_bf16 v[176:179], a[192:195], a[168:171], v[176:179]\n"
"s_mov_b32 m0, s44\n"
"ds_read_b64_tr_b16 a[238:239], v245 offset:25088\n"
"v_and_b32 v229, 0x30, v242\n"
"v_mfma_f32_16x16x32_bf16 v[176:179], a[196:199], a[172:175], v[176:179]\n"
"buffer_load_dwordx4 v240, s[16:19], s56 offen nt lds\n"
"v_mfma_f32_16x16x32_bf16 v[180:183], a[200:203], a[168:171], v[180:183]\n"
"v_mfma_f32_16x16x32_bf16 v[180:183], a[204:207], a[172:175], v[180:183]\n"
"v_mfma_f32_16x16x32_bf16 v[184:187], a[208:211], a[168:171], v[184:187]\n"
"ds_read_b64_tr_b16 a[246:247], v244 offset:26112\n"
"v_xor_b32 v251, s74, v251\n"
"v_mfma_f32_16x16x32_bf16 v[184:187], a[212:215], a[172:175], v[184:187]\n"
"v_mfma_f32_16x16x32_bf16 v[188:191], a[216:219], a[168:171], v[188:191]\n"
"s_add_u32 s40, s56, s52\n"
"ds_read_b64_tr_b16 a[224:225], v244 offset:16384\n"
"v_mfma_f32_16x16x32_bf16 v[188:191], a[220:223], a[172:175], v[188:191]\n"
"s_sub_u32 s82, s82, s81\n"
"buffer_load_dwordx4 v248, s[16:19], s40 offen offset:1024 nt lds\n"
"v_mfma_f32_16x16x32_bf16 v[204:207], a[216:219], a[176:179], v[204:207]\n"
"s_sub_u32 s82, s82, s81\n"
"v_mfma_f32_16x16x32_bf16 v[204:207], a[220:223], a[180:183], v[204:207]\n"
"s_sub_u32 s82, s82, s81\n"
"v_mfma_f32_16x16x32_bf16 v[200:203], a[208:211], a[176:179], v[200:203]\n"
"s_sub_u32 s82, s82, s81\n"
"ds_read_b64_tr_b16 a[236:237], v245 offset:24576\n"
"v_mfma_f32_16x16x32_bf16 v[200:203], a[212:215], a[180:183], v[200:203]\n"
"s_sub_u32 s82, s82, s81\n"
"v_bfe_i32 v241, v242, 4, 1\n"
"v_mfma_f32_16x16x32_bf16 v[196:199], a[200:203], a[176:179], v[196:199]\n"
"s_sub_u32 s82, s82, s81\n"
"s_add_u32 s40, s56, s53\n"
"ds_read_b64_tr_b16 a[234:235], v245 offset:16896\n"
"v_mfma_f32_16x16x32_bf16 v[196:199], a[204:207], a[180:183], v[196:199]\n"
"s_sub_u32 s82, s82, s81\n"
"buffer_load_dwordx4 v240, s[16:19], s40 offen offset:2048 nt lds\n"
"v_mfma_f32_16x16x32_bf16 v[192:195], a[192:195], a[176:179], v[192:195]\n"
"s_sub_u32 s82, s82, s81\n"
"v_mfma_f32_16x16x32_bf16 v[192:195], a[196:199], a[180:183], v[192:195]\n"
"s_waitcnt lgkmcnt(2)\n"
"s_sub_u32 s82, s82, s81\n"
"v_mfma_f32_16x16x32_bf16 v[208:211], a[192:195], a[184:187], v[208:211]\n"
"s_sub_u32 s82, s82, s81\n"
"ds_read_b64_tr_b16 a[254:255], v245 offset:26112\n"
"v_xor_b32 v246, s74, v246\n"
"v_mfma_f32_16x16x32_bf16 v[208:211], a[196:199], a[188:191], v[208:211]\n"
"s_sub_u32 s82, s82, s81\n"
"v_mfma_f32_16x16x32_bf16 v[212:215], a[200:203], a[184:187], v[212:215]\n"
"s_sub_u32 s82, s82, s81\n"
"s_add_u32 s40, s56, s54\n"
"v_lshl_add_u32 v228, v229, 3, v228\n"
"v_mfma_f32_16x16x32_bf16 v[212:215], a[204:207], a[188:191], v[212:215]\n"
"s_sub_u32 s82, s82, s81\n"
"buffer_load_dwordx4 v248, s[16:19], s40 offen offset:3072 nt lds\n"
"v_mfma_f32_16x16x32_bf16 v[216:219], a[208:211], a[184:187], v[216:219]\n"
"s_sub_u32 s82, s82, s81\n"
"v_and_b32 v229, 0xc0, v242\n"
"v_mfma_f32_16x16x32_bf16 v[216:219], a[212:215], a[188:191], v[216:219]\n"
"s_sub_u32 s82, s82, s81\n"
"v_mfma_f32_16x16x32_bf16 v[220:223], a[216:219], a[184:187], v[220:223]\n"
"s_sub_u32 s82, s82, s81\n"
"v_mfma_f32_16x16x32_bf16 v[220:223], a[220:223], a[188:191], v[220:223]\n"
"s_sub_u32 s82, s82, s81\n"
"v_mfma_f32_16x16x32_bf16 v[0:3], a[224:227], a[136:139], v[0:3]\n"
"v_mfma_f32_16x16x32_bf16 v[0:3], a[228:231], a[140:143], v[0:3]\n"
"s_waitcnt lgkmcnt(0)\n"
"v_xor_b32 v244, s74, v244\n"
"v_xor_b32 v245, s74, v245\n"
"v_mfma_f32_16x16x32_bf16 v[4:7], a[232:235], a[136:139], v[4:7]\n"
"s_waitcnt vmcnt(8)\n"
"v_mfma_f32_16x16x32_bf16 v[4:7], a[236:239], a[140:143], v[4:7]\n"
"s_barrier\n"
"v_mfma_f32_16x16x32_bf16 v[8:11], a[240:243], a[136:139], v[8:11]\n"
"s_add_u32 m0, s44, 65536\n"
"ds_read_b64_tr_b16 a[218:219], v245 offset:1536\n"
"v_mfma_f32_16x16x32_bf16 v[8:11], a[244:247], a[140:143], v[8:11]\n"
"buffer_load_dwordx4 v240, s[20:23], s56 offen nt lds\n"
"v_mfma_f32_16x16x32_bf16 v[12:15], a[248:251], a[136:139], v[12:15]\n"
"s_lshr_b32 s82, s82, 31\n"
"v_mfma_f32_16x16x32_bf16 v[12:15], a[252:255], a[140:143], v[12:15]\n"
"s_add_u32 s82, s82, s60\n"
"v_xor_b32 v250, s74, v250\n"
"v_mfma_f32_16x16x32_bf16 v[28:31], a[248:251], a[144:147], v[28:31]\n"
"s_add_u32 s82, s82, 32\n"
"ds_read_b64_tr_b16 a[204:205], v245 offset:8192\n"
"v_mfma_f32_16x16x32_bf16 v[28:31], a[252:255], a[148:151], v[28:31]\n"
"v_mov_b32 v254, s82\n"
"ds_read_b64_tr_b16 a[212:213], v244 offset:9216\n"
"v_mfma_f32_16x16x32_bf16 v[24:27], a[240:243], a[144:147], v[24:27]\n"
"v_lshlrev_b32_e64 v255, 2, s93\n"
"ds_read_b64_tr_b16 a[200:201], v245 offset:0\n"
"v_mfma_f32_16x16x32_bf16 v[24:27], a[244:247], a[148:151], v[24:27]\n"
"v_xor_b32 v247, s74, v247\n"
"v_mfma_f32_16x16x32_bf16 v[20:23], a[232:235], a[144:147], v[20:23]\n"
"s_add_u32 s40, s56, s52\n"
"ds_read_b128 a[136:139], v246 offset:0\n"
"v_mfma_f32_16x16x32_bf16 v[20:23], a[236:239], a[148:151], v[20:23]\n"
"buffer_load_dwordx4 v248, s[20:23], s40 offen offset:1024 nt lds\n"
"v_mfma_f32_16x16x32_bf16 v[16:19], a[224:227], a[144:147], v[16:19]\n"
"v_mfma_f32_16x16x32_bf16 v[16:19], a[228:231], a[148:151], v[16:19]\n"
"s_mov_b64 exec, 1\n"
"global_store_dword v255, v254, s[6:7] offset:256 sc1\n"
"s_mov_b64 exec, -1\n"
"v_mfma_f32_16x16x32_bf16 v[32:35], a[224:227], a[152:155], v[32:35]\n"
"ds_read_b64_tr_b16 a[210:211], v244 offset:1536\n"
"v_mfma_f32_16x16x32_bf16 v[32:35], a[228:231], a[156:159], v[32:35]\n"
"v_mfma_f32_16x16x32_bf16 v[36:39], a[232:235], a[152:155], v[36:39]\n"
"s_add_u32 s40, s56, s53\n"
"ds_read_b128 a[140:143], v247 offset:0\n"
"v_mfma_f32_16x16x32_bf16 v[36:39], a[236:239], a[156:159], v[36:39]\n"
"buffer_load_dwordx4 v240, s[20:23], s40 offen offset:2048 nt lds\n"
"v_mfma_f32_16x16x32_bf16 v[40:43], a[240:243], a[152:155], v[40:43]\n"
"v_mfma_f32_16x16x32_bf16 v[40:43], a[244:247], a[156:159], v[40:43]\n"
"v_mfma_f32_16x16x32_bf16 v[44:47], a[248:251], a[152:155], v[44:47]\n"
"ds_read_b64_tr_b16 a[192:193], v244 offset:0\n"
"v_mfma_f32_16x16x32_bf16 v[44:47], a[252:255], a[156:159], v[44:47]\n"
"ds_read_b64_tr_b16 a[196:197], v244 offset:8192\n"
"v_mfma_f32_16x16x32_bf16 v[60:63], a[248:251], a[160:163], v[60:63]\n"
"s_add_u32 s40, s56, s54\n"
"ds_read_b64_tr_b16 a[194:195], v244 offset:512\n"
"v_and_b32 v241, 0xaaaaaaaa, v241\n"
"v_mfma_f32_16x16x32_bf16 v[60:63], a[252:255], a[164:167], v[60:63]\n"
"buffer_load_dwordx4 v248, s[20:23], s40 offen offset:3072 nt lds\n"
"v_mfma_f32_16x16x32_bf16 v[56:59], a[240:243], a[160:163], v[56:59]\n"
"s_add_u32 s55, s55, s59\n"
"v_xor_b32 v253, s74, v253\n"
"v_mfma_f32_16x16x32_bf16 v[56:59], a[244:247], a[164:167], v[56:59]\n"
"s_or_b32 s55, s55, s58\n"
"v_mfma_f32_16x16x32_bf16 v[52:55], a[232:235], a[160:163], v[52:55]\n"
"s_xor_b32 s56, s55, s57\n"
"ds_read_b64_tr_b16 a[222:223], v245 offset:9728\n"
"v_lshl_add_u32 v228, v229, 4, v228\n"
"v_mfma_f32_16x16x32_bf16 v[52:55], a[236:239], a[164:167], v[52:55]\n"
"ds_read_b64_tr_b16 a[208:209], v244 offset:1024\n"
"v_mfma_f32_16x16x32_bf16 v[48:51], a[224:227], a[160:163], v[48:51]\n"
"s_add_u32 m0, s44, 16384\n"
"ds_read_b64_tr_b16 a[198:199], v244 offset:8704\n"
"v_xor_b32 v252, s74, v252\n"
"v_mfma_f32_16x16x32_bf16 v[48:51], a[228:231], a[164:167], v[48:51]\n"
"buffer_load_dwordx4 v240, s[16:19], s56 offen nt lds\n"
"v_mfma_f32_16x16x32_bf16 v[64:67], a[224:227], a[168:171], v[64:67]\n"
"v_mfma_f32_16x16x32_bf16 v[64:67], a[228:231], a[172:175], v[64:67]\n"
"v_mfma_f32_16x16x32_bf16 v[68:71], a[232:235], a[168:171], v[68:71]\n"
"ds_read_b128 a[160:163], v252 offset:0\n"
"v_mfma_f32_16x16x32_bf16 v[68:71], a[236:239], a[172:175], v[68:71]\n"
"v_mfma_f32_16x16x32_bf16 v[72:75], a[240:243], a[168:171], v[72:75]\n"
"ds_read_b64_tr_b16 a[214:215], v244 offset:9728\n"
"v_mfma_f32_16x16x32_bf16 v[72:75], a[244:247], a[172:175], v[72:75]\n"
"v_mfma_f32_16x16x32_bf16 v[76:79], a[248:251], a[168:171], v[76:79]\n"
"s_add_u32 s40, s56, s52\n"
"ds_read_b128 a[148:151], v247 offset:2048\n"
"v_mov_b32 v229, s43\n"
"v_mfma_f32_16x16x32_bf16 v[76:79], a[252:255], a[172:175], v[76:79]\n"
"buffer_load_dwordx4 v248, s[16:19], s40 offen offset:1024 nt lds\n"
"v_mfma_f32_16x16x32_bf16 v[92:95], a[248:251], a[176:179], v[92:95]\n"
"v_mfma_f32_16x16x32_bf16 v[92:95], a[252:255], a[180:183], v[92:95]\n"
"v_mfma_f32_16x16x32_bf16 v[88:91], a[240:243], a[176:179], v[88:91]\n"
"ds_read_b64_tr_b16 a[202:203], v245 offset:512\n"
"v_mfma_f32_16x16x32_bf16 v[88:91], a[244:247], a[180:183], v[88:91]\n"
"ds_read_b64_tr_b16 a[206:207], v245 offset:8704\n"
"v_mfma_f32_16x16x32_bf16 v[84:87], a[232:235], a[176:179], v[84:87]\n"
"s_add_u32 s40, s56, s53\n"
"ds_read_b64_tr_b16 a[216:217], v245 offset:1024\n"
"v_mfma_f32_16x16x32_bf16 v[84:87], a[236:239], a[180:183], v[84:87]\n"
"buffer_load_dwordx4 v240, s[16:19], s40 offen offset:2048 nt lds\n"
"v_mfma_f32_16x16x32_bf16 v[80:83], a[224:227], a[176:179], v[80:83]\n"
"v_mul_u32_u24 v229, 0x81, v229\n"
"v_and_b32 v254, 0x20, v242\n"
"v_mfma_f32_16x16x32_bf16 v[80:83], a[228:231], a[180:183], v[80:83]\n"
"v_mfma_f32_16x16x32_bf16 v[96:99], a[224:227], a[184:187], v[96:99]\n"
"ds_read_b128 a[172:175], v251 offset:0\n"
"v_mfma_f32_16x16x32_bf16 v[96:99], a[228:231], a[188:191], v[96:99]\n"
"ds_read_b64_tr_b16 a[220:221], v245 offset:9216\n"
"v_mfma_f32_16x16x32_bf16 v[100:103], a[232:235], a[184:187], v[100:103]\n"
"s_add_u32 s40, s56, s54\n"
"ds_read_b128 a[168:171], v250 offset:0\n"
"v_mfma_f32_16x16x32_bf16 v[100:103], a[236:239], a[188:191], v[100:103]\n"
"buffer_load_dwordx4 v248, s[16:19], s40 offen offset:3072 nt lds\n"
"v_mfma_f32_16x16x32_bf16 v[104:107], a[240:243], a[184:187], v[104:107]\n"
"v_mfma_f32_16x16x32_bf16 v[104:107], a[244:247], a[188:191], v[104:107]\n"
"v_mfma_f32_16x16x32_bf16 v[108:111], a[248:251], a[184:187], v[108:111]\n"
"ds_read_b128 a[176:179], v250 offset:2048\n"
"v_mfma_f32_16x16x32_bf16 v[108:111], a[252:255], a[188:191], v[108:111]\n"
"s_waitcnt lgkmcnt(4)\n"
"v_and_b32 v249, 0xf0, v242\n"
"v_mfma_f32_16x16x32_bf16 v[112:115], a[192:195], a[136:139], v[112:115]\n"
"ds_read_b128 a[180:183], v251 offset:2048\n"
"v_mfma_f32_16x16x32_bf16 v[112:115], a[196:199], a[140:143], v[112:115]\n"
"v_mfma_f32_16x16x32_bf16 v[116:119], a[200:203], a[136:139], v[116:119]\n"
"s_waitcnt vmcnt(13) lgkmcnt(2)\n"
"ds_read_b128 a[188:191], v251 offset:4096\n"
"v_bfe_u32 v255, v242, 9, 1\n"
"v_mfma_f32_16x16x32_bf16 v[116:119], a[204:207], a[140:143], v[116:119]\n"
"s_barrier\n"
"v_mfma_f32_16x16x32_bf16 v[120:123], a[208:211], a[136:139], v[120:123]\n"
"s_add_u32 m0, s44, 81920\n"
"ds_read_b128 a[164:167], v253 offset:0\n"
"v_lshl_add_u32 v255, v255, 8, v249\n"
"v_mfma_f32_16x16x32_bf16 v[120:123], a[212:215], a[140:143], v[120:123]\n"
"buffer_load_dwordx4 v240, s[20:23], s56 offen nt lds\n"
"v_mfma_f32_16x16x32_bf16 v[124:127], a[216:219], a[136:139], v[124:127]\n"
"v_mfma_f32_16x16x32_bf16 v[124:127], a[220:223], a[140:143], v[124:127]\n"
"v_bfe_u32 v249, v242, 8, 1\n"
"v_mfma_f32_16x16x32_bf16 v[188:191], a[216:219], a[168:171], v[188:191]\n"
"ds_read_b64_tr_b16 a[244:245], v244 offset:25600\n"
"v_mfma_f32_16x16x32_bf16 v[188:191], a[220:223], a[172:175], v[188:191]\n"
"v_mfma_f32_16x16x32_bf16 v[184:187], a[208:211], a[168:171], v[184:187]\n"
"ds_read_b128 a[184:187], v250 offset:4096\n"
"v_mfma_f32_16x16x32_bf16 v[184:187], a[212:215], a[172:175], v[184:187]\n"
"v_mfma_f32_16x16x32_bf16 v[180:183], a[200:203], a[168:171], v[180:183]\n"
"ds_read_b128 a[144:147], v246 offset:2048\n"
"v_mfma_f32_16x16x32_bf16 v[180:183], a[204:207], a[172:175], v[180:183]\n"
"v_mfma_f32_16x16x32_bf16 v[176:179], a[192:195], a[168:171], v[176:179]\n"
"ds_read_b64_tr_b16 a[252:253], v245 offset:25600\n"
"v_xor_b32 v228, v228, v229\n"
"v_mfma_f32_16x16x32_bf16 v[176:179], a[196:199], a[172:175], v[176:179]\n"
"s_waitcnt lgkmcnt(4)\n"
"v_xor_b32 v229, 64, v228\n"
"v_mfma_f32_16x16x32_bf16 v[192:195], a[192:195], a[176:179], v[192:195]\n"
"ds_read_b64_tr_b16 a[248:249], v245 offset:17408\n"
"v_add_u32 v254, -1, v254\n"
"v_mfma_f32_16x16x32_bf16 v[192:195], a[196:199], a[180:183], v[192:195]\n"
"v_mfma_f32_16x16x32_bf16 v[196:199], a[200:203], a[176:179], v[196:199]\n"
"s_add_u32 s40, s56, s52\n"
"ds_read_b64_tr_b16 a[234:235], v245 offset:16896\n"
"v_lshl_add_u32 v255, v249, 3, v255\n"
"v_mfma_f32_16x16x32_bf16 v[196:199], a[204:207], a[180:183], v[196:199]\n"
"buffer_load_dwordx4 v248, s[20:23], s40 offen offset:1024 nt lds\n"
"v_mfma_f32_16x16x32_bf16 v[200:203], a[208:211], a[176:179], v[200:203]\n"
"v_xor_b32_e64 v249, s42, 1\n"
"v_mfma_f32_16x16x32_bf16 v[200:203], a[212:215], a[180:183], v[200:203]\n"
"v_ashrrev_i32 v254, 31, v254\n"
"v_mfma_f32_16x16x32_bf16 v[204:207], a[216:219], a[176:179], v[204:207]\n"
"ds_read_b64_tr_b16 a[254:255], v245 offset:26112\n"
"v_mfma_f32_16x16x32_bf16 v[204:207], a[220:223], a[180:183], v[204:207]\n"
"s_waitcnt lgkmcnt(3)\n"
"ds_read_b64_tr_b16 a[240:241], v244 offset:17408\n"
"v_mfma_f32_16x16x32_bf16 v[220:223], a[216:219], a[184:187], v[220:223]\n"
"v_mfma_f32_16x16x32_bf16 v[220:223], a[220:223], a[188:191], v[220:223]\n"
"ds_read_b64_tr_b16 a[232:233], v245 offset:16384\n"
"v_mfma_f32_16x16x32_bf16 v[216:219], a[208:211], a[184:187], v[216:219]\n"
"ds_read_b64_tr_b16 a[230:231], v244 offset:25088\n"
"v_mfma_f32_16x16x32_bf16 v[216:219], a[212:215], a[188:191], v[216:219]\n"
"v_pk_fma_f32 v[176:177], v[176:177], s[70:71], s[70:71] op_sel:[0,0,1] op_sel_hi:[1,0,1]\nv_pk_fma_f32 v[178:179], v[178:179], s[70:71], s[70:71] op_sel:[0,0,1] op_sel_hi:[1,0,1]\nv_pk_fma_f32 v[180:181], v[180:181], s[70:71], s[70:71] op_sel:[0,0,1] op_sel_hi:[1,0,1]\nv_pk_fma_f32 v[182:183], v[182:183], s[70:71], s[70:71] op_sel:[0,0,1] op_sel_hi:[1,0,1]\nv_pk_fma_f32 v[184:185], v[184:185], s[70:71], s[70:71] op_sel:[0,0,1] op_sel_hi:[1,0,1]\nv_pk_fma_f32 v[186:187], v[186:187], s[70:71], s[70:71] op_sel:[0,0,1] op_sel_hi:[1,0,1]\nv_pk_fma_f32 v[188:189], v[188:189], s[70:71], s[70:71] op_sel:[0,0,1] op_sel_hi:[1,0,1]\nv_pk_fma_f32 v[190:191], v[190:191], s[70:71], s[70:71] op_sel:[0,0,1] op_sel_hi:[1,0,1]\nv_pk_fma_f32 v[192:193], v[192:193], s[70:71], s[70:71] op_sel:[0,0,1] op_sel_hi:[1,0,1]\nv_pk_fma_f32 v[194:195], v[194:195], s[70:71], s[70:71] op_sel:[0,0,1] op_sel_hi:[1,0,1]\nv_pk_fma_f32 v[196:197], v[196:197], s[70:71], s[70:71] op_sel:[0,0,1] op_sel_hi:[1,0,1]\nv_pk_fma_f32 v[198:199], v[198:199], s[70:71], s[70:71] op_sel:[0,0,1] op_sel_hi:[1,0,1]\nv_pk_fma_f32 v[200:201], v[200:201], s[70:71], s[70:71] op_sel:[0,0,1] op_sel_hi:[1,0,1]\nv_pk_fma_f32 v[202:203], v[202:203], s[70:71], s[70:71] op_sel:[0,0,1] op_sel_hi:[1,0,1]\nv_pk_fma_f32 v[204:205], v[204:205], s[70:71], s[70:71] op_sel:[0,0,1] op_sel_hi:[1,0,1]\nv_pk_fma_f32 v[206:207], v[206:207], s[70:71], s[70:71] op_sel:[0,0,1] op_sel_hi:[1,0,1]\n"
"v_mfma_f32_16x16x32_bf16 v[212:215], a[200:203], a[184:187], v[212:215]\n"
"ds_read_b128 a[152:155], v246 offset:4096\n"
"v_cvt_pk_bf16_f32 v176, v176, v177\n"
"v_mfma_f32_16x16x32_bf16 v[212:215], a[204:207], a[188:191], v[212:215]\n"
"v_cvt_pk_bf16_f32 v177, v178, v179\n"
"v_mfma_f32_16x16x32_bf16 v[208:211], a[192:195], a[184:187], v[208:211]\n"
"s_add_u32 s40, s56, s53\n"
"ds_read_b64_tr_b16 a[238:239], v245 offset:25088\n"
"v_cvt_pk_bf16_f32 v200, v200, v201\n"
"v_mfma_f32_16x16x32_bf16 v[208:211], a[196:199], a[188:191], v[208:211]\n"
"buffer_load_dwordx4 v240, s[20:23], s40 offen offset:2048 nt lds\n"
"v_mfma_f32_16x16x32_bf16 v[160:163], a[192:195], a[160:163], v[160:163]\n"
"v_xor_b32 v230, 0x2000, v228\n"
"v_cvt_pk_bf16_f32 v192, v192, v193\n"
"v_mfma_f32_16x16x32_bf16 v[160:163], a[196:199], a[164:167], v[160:163]\n"
"v_cvt_pk_bf16_f32 v193, v194, v195\n"
"v_cvt_pk_bf16_f32 v194, v196, v197\n"
"v_mfma_f32_16x16x32_bf16 v[164:167], a[200:203], a[160:163], v[164:167]\n"
"ds_read_b64_tr_b16 a[228:229], v244 offset:24576\n"
"v_cvt_pk_bf16_f32 v201, v202, v203\n"
"v_mfma_f32_16x16x32_bf16 v[164:167], a[204:207], a[164:167], v[164:167]\n"
"v_cvt_pk_bf16_f32 v195, v198, v199\n"
"v_bfe_u32 v196, v242, 8, 2\n"
"v_mfma_f32_16x16x32_bf16 v[168:171], a[208:211], a[160:163], v[168:171]\n"
"ds_read_b64_tr_b16 a[246:247], v244 offset:26112\n"
"v_cvt_pk_bf16_f32 v184, v184, v185\n"
"v_mfma_f32_16x16x32_bf16 v[168:171], a[212:215], a[164:167], v[168:171]\n"
"v_cvt_pk_bf16_f32 v202, v204, v205\n"
"v_cvt_pk_bf16_f32 v203, v206, v207\n"
"v_mfma_f32_16x16x32_bf16 v[172:175], a[216:219], a[160:163], v[172:175]\n"
"ds_read_b128 a[156:159], v247 offset:4096\n"
"v_xor_b32 v198, 0x400, v243\n"
"v_mfma_f32_16x16x32_bf16 v[172:175], a[220:223], a[164:167], v[172:175]\n"
"v_xor_b32 v231, 0x2000, v229\n"
"v_lshl_add_u32 v196, v196, 4, s43\n"
"v_mfma_f32_16x16x32_bf16 v[140:143], a[216:219], a[144:147], v[140:143]\n"
"ds_read_b64_tr_b16 a[250:251], v245 offset:17920\n"
"v_mfma_f32_16x16x32_bf16 v[140:143], a[220:223], a[148:151], v[140:143]\n"
"v_cvt_pk_bf16_f32 v178, v180, v181\n"
"v_or_b32 v241, 0x44444444, v241\n"
"v_mfma_f32_16x16x32_bf16 v[136:139], a[208:211], a[144:147], v[136:139]\n"
"ds_read_b64_tr_b16 a[236:237], v245 offset:24576\n"
"v_lshl_add_u32 v255, v249, 12, v255\n"
"v_mfma_f32_16x16x32_bf16 v[136:139], a[212:215], a[148:151], v[136:139]\n"
"v_pk_fma_f32 v[208:209], v[208:209], s[70:71], s[70:71] op_sel:[0,0,1] op_sel_hi:[1,0,1]\nv_pk_fma_f32 v[210:211], v[210:211], s[70:71], s[70:71] op_sel:[0,0,1] op_sel_hi:[1,0,1]\nv_pk_fma_f32 v[212:213], v[212:213], s[70:71], s[70:71] op_sel:[0,0,1] op_sel_hi:[1,0,1]\nv_pk_fma_f32 v[214:215], v[214:215], s[70:71], s[70:71] op_sel:[0,0,1] op_sel_hi:[1,0,1]\nv_pk_fma_f32 v[216:217], v[216:217], s[70:71], s[70:71] op_sel:[0,0,1] op_sel_hi:[1,0,1]\nv_pk_fma_f32 v[218:219], v[218:219], s[70:71], s[70:71] op_sel:[0,0,1] op_sel_hi:[1,0,1]\nv_pk_fma_f32 v[220:221], v[220:221], s[70:71], s[70:71] op_sel:[0,0,1] op_sel_hi:[1,0,1]\nv_pk_fma_f32 v[222:223], v[222:223], s[70:71], s[70:71] op_sel:[0,0,1] op_sel_hi:[1,0,1]\nv_pk_fma_f32 v[160:161], v[160:161], s[70:71], s[70:71] op_sel:[0,0,1] op_sel_hi:[1,0,1]\nv_pk_fma_f32 v[162:163], v[162:163], s[70:71], s[70:71] op_sel:[0,0,1] op_sel_hi:[1,0,1]\nv_pk_fma_f32 v[164:165], v[164:165], s[70:71], s[70:71] op_sel:[0,0,1] op_sel_hi:[1,0,1]\nv_pk_fma_f32 v[166:167], v[166:167], s[70:71], s[70:71] op_sel:[0,0,1] op_sel_hi:[1,0,1]\nv_pk_fma_f32 v[168:169], v[168:169], s[70:71], s[70:71] op_sel:[0,0,1] op_sel_hi:[1,0,1]\nv_pk_fma_f32 v[170:171], v[170:171], s[70:71], s[70:71] op_sel:[0,0,1] op_sel_hi:[1,0,1]\nv_pk_fma_f32 v[172:173], v[172:173], s[70:71], s[70:71] op_sel:[0,0,1] op_sel_hi:[1,0,1]\nv_pk_fma_f32 v[174:175], v[174:175], s[70:71], s[70:71] op_sel:[0,0,1] op_sel_hi:[1,0,1]\n"
"v_mfma_f32_16x16x32_bf16 v[132:135], a[200:203], a[144:147], v[132:135]\n"
"s_add_u32 s40, s56, s54\n"
"ds_read_b64_tr_b16 a[224:225], v244 offset:16384\n"
"v_cvt_pk_bf16_f32 v179, v182, v183\n"
"v_mfma_f32_16x16x32_bf16 v[132:135], a[204:207], a[148:151], v[132:135]\n"
"buffer_load_dwordx4 v248, s[20:23], s40 offen offset:3072 nt lds\n"
"v_mfma_f32_16x16x32_bf16 v[128:131], a[192:195], a[144:147], v[128:131]\n"
"s_add_u32 s55, s55, s59\n"
"v_cvt_pk_bf16_f32 v185, v186, v187\n"
"v_cvt_pk_bf16_f32 v216, v216, v217\n"
"v_mfma_f32_16x16x32_bf16 v[128:131], a[196:199], a[148:151], v[128:131]\n"
"s_waitcnt lgkmcnt(3)\n"
"s_or_b32 s55, s55, s58\n"
"v_cvt_pk_bf16_f32 v160, v160, v161\n"
"v_cvt_pk_bf16_f32 v168, v168, v169\n"
"v_mfma_f32_16x16x32_bf16 v[144:147], a[192:195], a[152:155], v[144:147]\n"
"s_xor_b32 s56, s55, s57\n"
"ds_read_b64_tr_b16 a[226:227], v244 offset:16896\n"
"v_cvt_pk_bf16_f32 v161, v162, v163\n"
"v_mfma_f32_16x16x32_bf16 v[144:147], a[196:199], a[156:159], v[144:147]\n"
"v_cvt_pk_bf16_f32 v163, v166, v167\n"
"v_mfma_f32_16x16x32_bf16 v[148:151], a[200:203], a[152:155], v[148:151]\n"
"ds_read_b64_tr_b16 a[242:243], v244 offset:17920\n"
"v_cvt_pk_bf16_f32 v208, v208, v209\n"
"v_mfma_f32_16x16x32_bf16 v[148:151], a[204:207], a[156:159], v[148:151]\n"
"v_pk_fma_f32 v[112:113], v[112:113], s[70:71], s[70:71] op_sel:[0,0,1] op_sel_hi:[1,0,1]\nv_pk_fma_f32 v[114:115], v[114:115], s[70:71], s[70:71] op_sel:[0,0,1] op_sel_hi:[1,0,1]\nv_pk_fma_f32 v[116:117], v[116:117], s[70:71], s[70:71] op_sel:[0,0,1] op_sel_hi:[1,0,1]\nv_pk_fma_f32 v[118:119], v[118:119], s[70:71], s[70:71] op_sel:[0,0,1] op_sel_hi:[1,0,1]\nv_pk_fma_f32 v[120:121], v[120:121], s[70:71], s[70:71] op_sel:[0,0,1] op_sel_hi:[1,0,1]\nv_pk_fma_f32 v[122:123], v[122:123], s[70:71], s[70:71] op_sel:[0,0,1] op_sel_hi:[1,0,1]\nv_pk_fma_f32 v[124:125], v[124:125], s[70:71], s[70:71] op_sel:[0,0,1] op_sel_hi:[1,0,1]\nv_pk_fma_f32 v[126:127], v[126:127], s[70:71], s[70:71] op_sel:[0,0,1] op_sel_hi:[1,0,1]\nv_pk_fma_f32 v[128:129], v[128:129], s[70:71], s[70:71] op_sel:[0,0,1] op_sel_hi:[1,0,1]\nv_pk_fma_f32 v[130:131], v[130:131], s[70:71], s[70:71] op_sel:[0,0,1] op_sel_hi:[1,0,1]\nv_pk_fma_f32 v[132:133], v[132:133], s[70:71], s[70:71] op_sel:[0,0,1] op_sel_hi:[1,0,1]\nv_pk_fma_f32 v[134:135], v[134:135], s[70:71], s[70:71] op_sel:[0,0,1] op_sel_hi:[1,0,1]\nv_pk_fma_f32 v[136:137], v[136:137], s[70:71], s[70:71] op_sel:[0,0,1] op_sel_hi:[1,0,1]\nv_pk_fma_f32 v[138:139], v[138:139], s[70:71], s[70:71] op_sel:[0,0,1] op_sel_hi:[1,0,1]\nv_pk_fma_f32 v[140:141], v[140:141], s[70:71], s[70:71] op_sel:[0,0,1] op_sel_hi:[1,0,1]\nv_pk_fma_f32 v[142:143], v[142:143], s[70:71], s[70:71] op_sel:[0,0,1] op_sel_hi:[1,0,1]\n"
"v_mfma_f32_16x16x32_bf16 v[152:155], a[208:211], a[152:155], v[152:155]\n"
"v_cvt_pk_bf16_f32 v217, v218, v219\n"
"v_cvt_pk_bf16_f32 v136, v136, v137\n"
"v_mfma_f32_16x16x32_bf16 v[152:155], a[212:215], a[156:159], v[152:155]\n"
"v_cvt_pk_bf16_f32 v162, v164, v165\n"
"v_cvt_pk_bf16_f32 v120, v120, v121\n"
"v_mfma_f32_16x16x32_bf16 v[156:159], a[216:219], a[152:155], v[156:159]\n"
"v_mfma_f32_16x16x32_bf16 v[156:159], a[220:223], a[156:159], v[156:159]\n"
"s_waitcnt lgkmcnt(1)\n"
"v_cvt_pk_bf16_f32 v128, v128, v129\n"
"v_cvt_pk_bf16_f32 v129, v130, v131\n"
"v_mfma_f32_16x16x32_bf16 v[96:99], a[224:227], a[184:187], v[96:99]\n"
"s_add_u32 m0, s44, 32768\n"
"v_cvt_pk_bf16_f32 v137, v138, v139\n"
"v_cvt_pk_bf16_f32 v139, v142, v143\n"
"v_mfma_f32_16x16x32_bf16 v[96:99], a[228:231], a[188:191], v[96:99]\n"
"buffer_load_dwordx4 v240, s[16:19], s56 offen nt lds\n"
"v_mfma_f32_16x16x32_bf16 v[100:103], a[232:235], a[184:187], v[100:103]\n"
"s_waitcnt vmcnt(14) lgkmcnt(0)\n"
"v_permlane16_swap_b32 v161, v163\n"
"v_mfma_f32_16x16x32_bf16 v[100:103], a[236:239], a[188:191], v[100:103]\n"
"s_barrier\n"
"v_mfma_f32_16x16x32_bf16 v[104:107], a[240:243], a[184:187], v[104:107]\n"
"v_permlane16_swap_b32 v160, v162\n"
"v_mfma_f32_16x16x32_bf16 v[104:107], a[244:247], a[188:191], v[104:107]\n"
"s_mov_b32 s74, 0x41400000\n"
"ds_write2st64_b64 v255, v[192:193], v[194:195] offset0:100 offset1:101\n"
"v_mfma_f32_16x16x32_bf16 v[108:111], a[248:251], a[184:187], v[108:111]\n"
"v_permlane16_swap_b32 v137, v139\n"
"v_mfma_f32_16x16x32_bf16 v[108:111], a[252:255], a[188:191], v[108:111]\n"
"v_pk_fma_f32 v[144:145], v[144:145], s[70:71], s[70:71] op_sel:[0,0,1] op_sel_hi:[1,0,1]\nv_pk_fma_f32 v[146:147], v[146:147], s[70:71], s[70:71] op_sel:[0,0,1] op_sel_hi:[1,0,1]\nv_pk_fma_f32 v[148:149], v[148:149], s[70:71], s[70:71] op_sel:[0,0,1] op_sel_hi:[1,0,1]\nv_pk_fma_f32 v[150:151], v[150:151], s[70:71], s[70:71] op_sel:[0,0,1] op_sel_hi:[1,0,1]\nv_pk_fma_f32 v[152:153], v[152:153], s[70:71], s[70:71] op_sel:[0,0,1] op_sel_hi:[1,0,1]\nv_pk_fma_f32 v[154:155], v[154:155], s[70:71], s[70:71] op_sel:[0,0,1] op_sel_hi:[1,0,1]\nv_pk_fma_f32 v[156:157], v[156:157], s[70:71], s[70:71] op_sel:[0,0,1] op_sel_hi:[1,0,1]\nv_pk_fma_f32 v[158:159], v[158:159], s[70:71], s[70:71] op_sel:[0,0,1] op_sel_hi:[1,0,1]\n"
"v_mfma_f32_16x16x32_bf16 v[92:95], a[248:251], a[176:179], v[92:95]\n"
"v_cvt_pk_bf16_f32 v218, v220, v221\n"
"v_cvt_pk_bf16_f32 v152, v152, v153\n"
"v_mfma_f32_16x16x32_bf16 v[92:95], a[252:255], a[180:183], v[92:95]\n"
"ds_write_b128 v243, v[160:163] offset:8192\n"
"v_mfma_f32_16x16x32_bf16 v[88:91], a[240:243], a[176:179], v[88:91]\n"
"v_cvt_pk_bf16_f32 v131, v134, v135\n"
"v_cvt_pk_bf16_f32 v144, v144, v145\n"
"v_mfma_f32_16x16x32_bf16 v[88:91], a[244:247], a[180:183], v[88:91]\n"
"v_cvt_pk_bf16_f32 v169, v170, v171\n"
"v_cvt_pk_bf16_f32 v130, v132, v133\n"
"v_mfma_f32_16x16x32_bf16 v[84:87], a[232:235], a[176:179], v[84:87]\n"
"ds_read_b128 a[192:195], v243 offset:0\n"
"v_cvt_pk_bf16_f32 v153, v154, v155\n"
"v_mfma_f32_16x16x32_bf16 v[84:87], a[236:239], a[180:183], v[84:87]\n"
"v_cvt_pk_bf16_f32 v219, v222, v223\n"
"v_mov_b32 v205, 0\n"
"v_mfma_f32_16x16x32_bf16 v[80:83], a[224:227], a[176:179], v[80:83]\n"
"s_add_u32 s40, s56, s52\n"
"ds_read_b128 a[196:199], v198 offset:0\n"
"v_cvt_pk_bf16_f32 v138, v140, v141\n"
"v_mfma_f32_16x16x32_bf16 v[80:83], a[228:231], a[180:183], v[80:83]\n"
"buffer_load_dwordx4 v248, s[16:19], s40 offen offset:1024 nt lds\n"
"v_mfma_f32_16x16x32_bf16 v[64:67], a[224:227], a[168:171], v[64:67]\n"
"v_cvt_pk_bf16_f32 v171, v174, v175\n"
"v_cvt_pk_bf16_f32 v145, v146, v147\n"
"v_mfma_f32_16x16x32_bf16 v[64:67], a[228:231], a[172:175], v[64:67]\n"
"v_cvt_pk_bf16_f32 v209, v210, v211\n"
"v_cvt_pk_bf16_f32 v147, v150, v151\n"
"v_mfma_f32_16x16x32_bf16 v[68:71], a[232:235], a[168:171], v[68:71]\n"
"v_permlane16_swap_b32 v169, v171\n"
"v_mfma_f32_16x16x32_bf16 v[68:71], a[236:239], a[172:175], v[68:71]\n"
"ds_write2st64_b64 v255, v[216:217], v[218:219] offset0:194 offset1:195\n"
"v_pk_fma_f32 v[96:97], v[96:97], s[70:71], s[70:71] op_sel:[0,0,1] op_sel_hi:[1,0,1]\nv_pk_fma_f32 v[98:99], v[98:99], s[70:71], s[70:71] op_sel:[0,0,1] op_sel_hi:[1,0,1]\nv_pk_fma_f32 v[100:101], v[100:101], s[70:71], s[70:71] op_sel:[0,0,1] op_sel_hi:[1,0,1]\nv_pk_fma_f32 v[102:103], v[102:103], s[70:71], s[70:71] op_sel:[0,0,1] op_sel_hi:[1,0,1]\nv_pk_fma_f32 v[104:105], v[104:105], s[70:71], s[70:71] op_sel:[0,0,1] op_sel_hi:[1,0,1]\nv_pk_fma_f32 v[106:107], v[106:107], s[70:71], s[70:71] op_sel:[0,0,1] op_sel_hi:[1,0,1]\nv_pk_fma_f32 v[108:109], v[108:109], s[70:71], s[70:71] op_sel:[0,0,1] op_sel_hi:[1,0,1]\nv_pk_fma_f32 v[110:111], v[110:111], s[70:71], s[70:71] op_sel:[0,0,1] op_sel_hi:[1,0,1]\nv_pk_fma_f32 v[80:81], v[80:81], s[70:71], s[70:71] op_sel:[0,0,1] op_sel_hi:[1,0,1]\nv_pk_fma_f32 v[82:83], v[82:83], s[70:71], s[70:71] op_sel:[0,0,1] op_sel_hi:[1,0,1]\nv_pk_fma_f32 v[84:85], v[84:85], s[70:71], s[70:71] op_sel:[0,0,1] op_sel_hi:[1,0,1]\nv_pk_fma_f32 v[86:87], v[86:87], s[70:71], s[70:71] op_sel:[0,0,1] op_sel_hi:[1,0,1]\nv_pk_fma_f32 v[88:89], v[88:89], s[70:71], s[70:71] op_sel:[0,0,1] op_sel_hi:[1,0,1]\nv_pk_fma_f32 v[90:91], v[90:91], s[70:71], s[70:71] op_sel:[0,0,1] op_sel_hi:[1,0,1]\nv_pk_fma_f32 v[92:93], v[92:93], s[70:71], s[70:71] op_sel:[0,0,1] op_sel_hi:[1,0,1]\nv_pk_fma_f32 v[94:95], v[94:95], s[70:71], s[70:71] op_sel:[0,0,1] op_sel_hi:[1,0,1]\n"
"v_mfma_f32_16x16x32_bf16 v[72:75], a[240:243], a[168:171], v[72:75]\n"
"v_cvt_pk_bf16_f32 v96, v96, v97\n"
"v_mov_b32 v204, 0\n"
"v_mfma_f32_16x16x32_bf16 v[72:75], a[244:247], a[172:175], v[72:75]\n"
"ds_read_b128 a[204:207], v198 offset:2048\n"
"v_cvt_pk_bf16_f32 v80, v80, v81\n"
"v_mfma_f32_16x16x32_bf16 v[76:79], a[248:251], a[168:171], v[76:79]\n"
"v_cvt_pk_bf16_f32 v97, v98, v99\n"
"v_cvt_pk_bf16_f32 v81, v82, v83\n"
"v_mfma_f32_16x16x32_bf16 v[76:79], a[252:255], a[172:175], v[76:79]\n"
"ds_write2st64_b64 v255, v[176:177], v[178:179] offset0:96 offset1:97\n"
"v_mfma_f32_16x16x32_bf16 v[60:63], a[248:251], a[160:163], v[60:63]\n"
"v_cvt_pk_bf16_f32 v210, v212, v213\n"
"v_xor_b32 v197, 64, v196\n"
"v_mfma_f32_16x16x32_bf16 v[60:63], a[252:255], a[164:167], v[60:63]\n"
"v_cvt_pk_bf16_f32 v211, v214, v215\n"
"v_cvt_pk_bf16_f32 v170, v172, v173\n"
"v_mfma_f32_16x16x32_bf16 v[56:59], a[240:243], a[160:163], v[56:59]\n"
"s_add_u32 s40, s56, s53\n"
"v_permlane16_swap_b32 v129, v131\n"
"v_mfma_f32_16x16x32_bf16 v[56:59], a[244:247], a[164:167], v[56:59]\n"
"buffer_load_dwordx4 v240, s[16:19], s40 offen offset:2048 nt lds\n"
"v_mfma_f32_16x16x32_bf16 v[52:55], a[232:235], a[160:163], v[52:55]\n"
"v_cvt_pk_bf16_f32 v104, v104, v105\n"
"v_and_b32 v254, 0x3f803f80, v254\n"
"v_mfma_f32_16x16x32_bf16 v[52:55], a[236:239], a[164:167], v[52:55]\n"
"v_cvt_pk_bf16_f32 v154, v156, v157\n"
"v_cvt_pk_bf16_f32 v82, v84, v85\n"
"v_mfma_f32_16x16x32_bf16 v[48:51], a[224:227], a[160:163], v[48:51]\n"
"v_cvt_pk_bf16_f32 v121, v122, v123\n"
"v_accvgpr_write_b32 a123, v139\n"
"v_mfma_f32_16x16x32_bf16 v[48:51], a[228:231], a[164:167], v[48:51]\n"
"v_permlane16_swap_b32 v145, v147\n"
"v_mfma_f32_16x16x32_bf16 v[0:3], a[224:227], a[136:139], v[0:3]\n"
"v_permlane16_swap_b32 v128, v130\n"
"v_mfma_f32_16x16x32_bf16 v[0:3], a[228:231], a[140:143], v[0:3]\n"
"v_accvgpr_write_b32 a121, v137\n"
"v_cvt_pk_bf16_f32 v155, v158, v159\n"
"v_mfma_f32_16x16x32_bf16 v[4:7], a[232:235], a[136:139], v[4:7]\n"
"v_permlane16_swap_b32 v136, v138\n"
"v_mfma_f32_16x16x32_bf16 v[4:7], a[236:239], a[140:143], v[4:7]\n"
"ds_write2st64_b64 v255, v[208:209], v[210:211] offset0:192 offset1:193\n"
"v_pk_fma_f32 v[64:65], v[64:65], s[70:71], s[70:71] op_sel:[0,0,1] op_sel_hi:[1,0,1]\nv_pk_fma_f32 v[66:67], v[66:67], s[70:71], s[70:71] op_sel:[0,0,1] op_sel_hi:[1,0,1]\nv_pk_fma_f32 v[68:69], v[68:69], s[70:71], s[70:71] op_sel:[0,0,1] op_sel_hi:[1,0,1]\nv_pk_fma_f32 v[70:71], v[70:71], s[70:71], s[70:71] op_sel:[0,0,1] op_sel_hi:[1,0,1]\nv_pk_fma_f32 v[72:73], v[72:73], s[70:71], s[70:71] op_sel:[0,0,1] op_sel_hi:[1,0,1]\nv_pk_fma_f32 v[74:75], v[74:75], s[70:71], s[70:71] op_sel:[0,0,1] op_sel_hi:[1,0,1]\nv_pk_fma_f32 v[76:77], v[76:77], s[70:71], s[70:71] op_sel:[0,0,1] op_sel_hi:[1,0,1]\nv_pk_fma_f32 v[78:79], v[78:79], s[70:71], s[70:71] op_sel:[0,0,1] op_sel_hi:[1,0,1]\nv_pk_fma_f32 v[48:49], v[48:49], s[70:71], s[70:71] op_sel:[0,0,1] op_sel_hi:[1,0,1]\nv_pk_fma_f32 v[50:51], v[50:51], s[70:71], s[70:71] op_sel:[0,0,1] op_sel_hi:[1,0,1]\nv_pk_fma_f32 v[52:53], v[52:53], s[70:71], s[70:71] op_sel:[0,0,1] op_sel_hi:[1,0,1]\nv_pk_fma_f32 v[54:55], v[54:55], s[70:71], s[70:71] op_sel:[0,0,1] op_sel_hi:[1,0,1]\nv_pk_fma_f32 v[56:57], v[56:57], s[70:71], s[70:71] op_sel:[0,0,1] op_sel_hi:[1,0,1]\nv_pk_fma_f32 v[58:59], v[58:59], s[70:71], s[70:71] op_sel:[0,0,1] op_sel_hi:[1,0,1]\nv_pk_fma_f32 v[60:61], v[60:61], s[70:71], s[70:71] op_sel:[0,0,1] op_sel_hi:[1,0,1]\nv_pk_fma_f32 v[62:63], v[62:63], s[70:71], s[70:71] op_sel:[0,0,1] op_sel_hi:[1,0,1]\n"
"v_mfma_f32_16x16x32_bf16 v[8:11], a[240:243], a[136:139], v[8:11]\n"
"v_cvt_pk_bf16_f32 v187, v190, v191\n"
"v_cvt_pk_bf16_f32 v72, v72, v73\n"
"v_mfma_f32_16x16x32_bf16 v[8:11], a[244:247], a[140:143], v[8:11]\n"
"ds_write2st64_b64 v255, v[200:201], v[202:203] offset0:102 offset1:103\n"
"v_mfma_f32_16x16x32_bf16 v[12:15], a[248:251], a[136:139], v[12:15]\n"
"v_permlane16_swap_b32 v153, v155\n"
"v_mfma_f32_16x16x32_bf16 v[12:15], a[252:255], a[140:143], v[12:15]\n"
"v_permlane16_swap_b32 v152, v154\n"
"v_mfma_f32_16x16x32_bf16 v[28:31], a[248:251], a[144:147], v[28:31]\n"
"s_add_u32 s40, s56, s54\n"
"ds_read_b128 v[212:215], v197 offset:0\n"
"v_cvt_pk_bf16_f32 v56, v56, v57\n"
"v_mfma_f32_16x16x32_bf16 v[28:31], a[252:255], a[148:151], v[28:31]\n"
"buffer_load_dwordx4 v248, s[16:19], s40 offen offset:3072 nt lds\n"
"v_mfma_f32_16x16x32_bf16 v[24:27], a[240:243], a[144:147], v[24:27]\n"
"v_cvt_pk_bf16_f32 v98, v100, v101\n"
"v_mov_b32 v199, 0\n"
"v_mfma_f32_16x16x32_bf16 v[24:27], a[244:247], a[148:151], v[24:27]\n"
"v_cvt_pk_bf16_f32 v88, v88, v89\n"
"v_cvt_pk_bf16_f32 v48, v48, v49\n"
"v_mfma_f32_16x16x32_bf16 v[20:23], a[232:235], a[144:147], v[20:23]\n"
"v_permlane16_swap_b32 v168, v170\n"
"v_mfma_f32_16x16x32_bf16 v[20:23], a[236:239], a[148:151], v[20:23]\n"
"v_cvt_pk_bf16_f32 v123, v126, v127\n"
"v_cvt_pk_bf16_f32 v64, v64, v65\n"
"v_mfma_f32_16x16x32_bf16 v[16:19], a[224:227], a[144:147], v[16:19]\n"
"ds_read_b128 v[208:211], v196 offset:0\n"
"v_cvt_pk_bf16_f32 v49, v50, v51\n"
"v_mfma_f32_16x16x32_bf16 v[16:19], a[228:231], a[148:151], v[16:19]\n"
"v_cvt_pk_bf16_f32 v186, v188, v189\n"
"v_cvt_pk_bf16_f32 v146, v148, v149\n"
"v_mfma_f32_16x16x32_bf16 v[32:35], a[224:227], a[152:155], v[32:35]\n"
"v_accvgpr_write_b32 a115, v131\n"
"v_accvgpr_write_b32 a122, v138\n"
"v_mfma_f32_16x16x32_bf16 v[32:35], a[228:231], a[156:159], v[32:35]\n"
"ds_read_b128 v[216:219], v196 offset:512\n"
"v_cvt_pk_bf16_f32 v99, v102, v103\n"
"v_mfma_f32_16x16x32_bf16 v[36:39], a[232:235], a[152:155], v[36:39]\n"
"ds_read_b128 v[220:223], v197 offset:512\n"
"v_cvt_pk_bf16_f32 v73, v74, v75\n"
"v_mfma_f32_16x16x32_bf16 v[36:39], a[236:239], a[156:159], v[36:39]\n"
"ds_write2st64_b64 v255, v[184:185], v[186:187] offset0:98 offset1:99\n"
"v_permlane16_swap_b32 v144, v146\n"
"v_mov_b32 v197, 0\n"
"v_mfma_f32_16x16x32_bf16 v[40:43], a[240:243], a[152:155], v[40:43]\n"
"v_accvgpr_write_b32 a131, v147\n"
"v_cvt_pk_bf16_f32 v74, v76, v77\n"
"v_mfma_f32_16x16x32_bf16 v[40:43], a[244:247], a[156:159], v[40:43]\n"
"ds_write_b128 v243, v[168:171] offset:10240\n"
"v_pk_fma_f32 v[0:1], v[0:1], s[70:71], s[70:71] op_sel:[0,0,1] op_sel_hi:[1,0,1]\nv_pk_fma_f32 v[2:3], v[2:3], s[70:71], s[70:71] op_sel:[0,0,1] op_sel_hi:[1,0,1]\nv_pk_fma_f32 v[4:5], v[4:5], s[70:71], s[70:71] op_sel:[0,0,1] op_sel_hi:[1,0,1]\nv_pk_fma_f32 v[6:7], v[6:7], s[70:71], s[70:71] op_sel:[0,0,1] op_sel_hi:[1,0,1]\nv_pk_fma_f32 v[8:9], v[8:9], s[70:71], s[70:71] op_sel:[0,0,1] op_sel_hi:[1,0,1]\nv_pk_fma_f32 v[10:11], v[10:11], s[70:71], s[70:71] op_sel:[0,0,1] op_sel_hi:[1,0,1]\nv_pk_fma_f32 v[12:13], v[12:13], s[70:71], s[70:71] op_sel:[0,0,1] op_sel_hi:[1,0,1]\nv_pk_fma_f32 v[14:15], v[14:15], s[70:71], s[70:71] op_sel:[0,0,1] op_sel_hi:[1,0,1]\nv_pk_fma_f32 v[16:17], v[16:17], s[70:71], s[70:71] op_sel:[0,0,1] op_sel_hi:[1,0,1]\nv_pk_fma_f32 v[18:19], v[18:19], s[70:71], s[70:71] op_sel:[0,0,1] op_sel_hi:[1,0,1]\nv_pk_fma_f32 v[20:21], v[20:21], s[70:71], s[70:71] op_sel:[0,0,1] op_sel_hi:[1,0,1]\nv_pk_fma_f32 v[22:23], v[22:23], s[70:71], s[70:71] op_sel:[0,0,1] op_sel_hi:[1,0,1]\nv_pk_fma_f32 v[24:25], v[24:25], s[70:71], s[70:71] op_sel:[0,0,1] op_sel_hi:[1,0,1]\nv_pk_fma_f32 v[26:27], v[26:27], s[70:71], s[70:71] op_sel:[0,0,1] op_sel_hi:[1,0,1]\nv_pk_fma_f32 v[28:29], v[28:29], s[70:71], s[70:71] op_sel:[0,0,1] op_sel_hi:[1,0,1]\nv_pk_fma_f32 v[30:31], v[30:31], s[70:71], s[70:71] op_sel:[0,0,1] op_sel_hi:[1,0,1]\n"
"v_mfma_f32_16x16x32_bf16 v[44:47], a[248:251], a[152:155], v[44:47]\n"
"v_accvgpr_write_b32 a113, v129\n"
"v_accvgpr_write_b32 a114, v130\n"
"v_mfma_f32_16x16x32_bf16 v[44:47], a[252:255], a[156:159], v[44:47]\n"
"s_waitcnt lgkmcnt(4)\n"
"ds_write2st64_b64 v255, v[96:97], v[98:99] offset0:228 offset1:229\n"
"v_accvgpr_write_b32 a120, v136\n"
"v_cvt_pk_bf16_f32 v83, v86, v87\n"
"v_mfma_f32_16x16x32_bf16 v[180:183], v[208:211], a[0:3], 0\n"
"v_accvgpr_write_b32 a138, v154\n"
"v_cvt_pk_bf16_f32 v75, v78, v79\n"
"v_mfma_f32_16x16x32_bf16 v[180:183], v[212:215], a[4:7], v[180:183]\n"
"s_waitcnt lgkmcnt(4)\n"
"v_accvgpr_write_b32 a128, v144\n"
"v_accvgpr_write_b32 a139, v155\n"
"v_mfma_f32_16x16x32_bf16 v[180:183], v[216:219], a[8:11], v[180:183]\n"
"s_waitcnt lgkmcnt(3)\n"
"v_accvgpr_write_b32 a137, v153\n"
"v_cvt_pk_bf16_f32 v57, v58, v59\n"
"v_mfma_f32_16x16x32_bf16 v[180:183], v[220:223], a[12:15], v[180:183]\n"
"ds_write2st64_b64 v255, v[80:81], v[82:83] offset0:224 offset1:225\n"
"v_pk_fma_f32 v[32:33], v[32:33], s[70:71], s[70:71] op_sel:[0,0,1] op_sel_hi:[1,0,1]\nv_pk_fma_f32 v[34:35], v[34:35], s[70:71], s[70:71] op_sel:[0,0,1] op_sel_hi:[1,0,1]\nv_pk_fma_f32 v[36:37], v[36:37], s[70:71], s[70:71] op_sel:[0,0,1] op_sel_hi:[1,0,1]\nv_pk_fma_f32 v[38:39], v[38:39], s[70:71], s[70:71] op_sel:[0,0,1] op_sel_hi:[1,0,1]\nv_pk_fma_f32 v[40:41], v[40:41], s[70:71], s[70:71] op_sel:[0,0,1] op_sel_hi:[1,0,1]\nv_pk_fma_f32 v[42:43], v[42:43], s[70:71], s[70:71] op_sel:[0,0,1] op_sel_hi:[1,0,1]\nv_pk_fma_f32 v[44:45], v[44:45], s[70:71], s[70:71] op_sel:[0,0,1] op_sel_hi:[1,0,1]\nv_pk_fma_f32 v[46:47], v[46:47], s[70:71], s[70:71] op_sel:[0,0,1] op_sel_hi:[1,0,1]\n"
"v_mfma_f32_16x16x32_bf16 v[184:187], v[208:211], a[16:19], 0\n"
"v_accvgpr_write_b32 a112, v128\n"
"v_cvt_pk_bf16_f32 v89, v90, v91\n"
"v_mfma_f32_16x16x32_bf16 v[184:187], v[212:215], a[20:23], v[184:187]\n"
"ds_read_b128 a[200:203], v243 offset:2048\n"
"v_accvgpr_write_b32 a129, v145\n"
"v_mfma_f32_16x16x32_bf16 v[184:187], v[216:219], a[24:27], v[184:187]\n"
"v_accvgpr_write_b32 a130, v146\n"
"v_mov_b32 v198, 0\n"
"v_mfma_f32_16x16x32_bf16 v[184:187], v[220:223], a[28:31], v[184:187]\n"
"v_cvt_pk_bf16_f32 v65, v66, v67\n"
"v_cvt_pk_bf16_f32 v59, v62, v63\n"
"v_mfma_f32_16x16x32_bf16 v[188:191], v[208:211], a[32:35], 0\n"
"v_sub_f32 v180, s74, v180\n"
"v_sub_f32 v182, s74, v182\n"
"v_mfma_f32_16x16x32_bf16 v[188:191], v[212:215], a[36:39], v[188:191]\n"
"v_cvt_pk_bf16_f32 v58, v60, v61\n"
"v_cvt_pk_bf16_f32 v8, v8, v9\n"
"v_mfma_f32_16x16x32_bf16 v[188:191], v[216:219], a[40:43], v[188:191]\n"
"v_accvgpr_write_b32 a136, v152\n"
"v_sub_f32 v184, s74, v184\n"
"v_mfma_f32_16x16x32_bf16 v[188:191], v[220:223], a[44:47], v[188:191]\n"
"v_cvt_pk_bf16_f32 v40, v40, v41\n"
"v_sub_f32 v183, s74, v183\n"
"v_mfma_f32_16x16x32_bf16 v[192:195], v[208:211], a[192:195], 0\n"
"ds_read_b128 v[208:211], v228 offset:0\n"
"v_sub_f32 v181, s74, v181\n"
"v_mfma_f32_16x16x32_bf16 v[192:195], v[212:215], a[196:199], v[192:195]\n"
"s_waitcnt lgkmcnt(1)\n"
"ds_read_b128 v[212:215], v229 offset:0\n"
"v_cvt_pk_bf16_f32 v122, v124, v125\n"
"v_mfma_f32_16x16x32_bf16 v[192:195], v[216:219], a[200:203], v[192:195]\n"
"ds_read_b128 v[216:219], v228 offset:512\n"
"v_sub_f32 v187, s74, v187\n"
"v_mfma_f32_16x16x32_bf16 v[192:195], v[220:223], a[204:207], v[192:195]\n"
"s_waitcnt lgkmcnt(2)\n"
"ds_read_b128 v[220:223], v229 offset:512\n"
"v_cvt_pk_bf16_f32 v41, v42, v43\n"
"v_mfma_f32_16x16x32_bf16 v[152:155], v[208:211], a[0:3], v[180:183]\n"
"s_waitcnt lgkmcnt(2)\n"
"v_cvt_pk_bf16_f32 v105, v106, v107\n"
"v_accvgpr_write_b32 a241, v254\n"
"v_mfma_f32_16x16x32_bf16 v[152:155], v[212:215], a[4:7], v[152:155]\n"
"s_waitcnt lgkmcnt(1)\n"
"v_sub_f32 v185, s74, v185\n"
"v_sub_f32 v190, s74, v190\n"
"v_mfma_f32_16x16x32_bf16 v[152:155], v[216:219], a[8:11], v[152:155]\n"
"s_waitcnt lgkmcnt(0)\n"
"v_cvt_pk_bf16_f32 v90, v92, v93\n"
"v_sub_f32 v186, s74, v186\n"
"v_mfma_f32_16x16x32_bf16 v[152:155], v[220:223], a[12:15], v[152:155]\n"
"v_cvt_pk_bf16_f32 v9, v10, v11\n"
"v_sub_f32 v188, s74, v188\n"
"v_mfma_f32_16x16x32_bf16 v[156:159], v[216:219], a[24:27], v[184:187]\n"
"v_sub_f32 v192, s74, v192\n"
"v_sub_f32 v194, s74, v194\n"
"v_mfma_f32_16x16x32_bf16 v[156:159], v[220:223], a[28:31], v[156:159]\n"
"v_cvt_pk_bf16_f32 v50, v52, v53\n"
"v_sub_f32 v191, s74, v191\n"
"v_mfma_f32_16x16x32_bf16 v[156:159], v[208:211], a[16:19], v[156:159]\n"
"v_sub_f32 v189, s74, v189\n"
"v_sub_f32 v193, s74, v193\n"
"v_mfma_f32_16x16x32_bf16 v[156:159], v[212:215], a[20:23], v[156:159]\n"
"ds_write2st64_b64 v255, v[72:73], v[74:75] offset0:198 offset1:199\n"
"v_cvt_pk_bf16_f32 v67, v70, v71\n"
"v_exp_f32 v152, v152\n"
"v_exp_f32 v153, v153\n"
"v_exp_f32 v154, v154\n"
"v_mfma_f32_16x16x32_bf16 v[160:163], v[208:211], a[32:35], v[188:191]\n"
"v_cvt_pk_bf16_f32 v106, v108, v109\n"
"v_sub_f32 v195, s74, v195\n"
"v_mfma_f32_16x16x32_bf16 v[160:163], v[212:215], a[36:39], v[160:163]\n"
"v_permlane16_swap_b32 v56, v58\n"
"v_mfma_f32_16x16x32_bf16 v[160:163], v[216:219], a[40:43], v[160:163]\n"
"v_exp_f32 v158, v158\n"
"v_mfma_f32_16x16x32_bf16 v[160:163], v[220:223], a[44:47], v[160:163]\n"
"v_exp_f32 v155, v155\n"
"v_mfma_f32_16x16x32_bf16 v[164:167], v[216:219], a[200:203], v[192:195]\n"
"ds_read_b128 v[216:219], v228 offset:4608\n"
"v_cvt_pk_bf16_f32 v66, v68, v69\n"
"v_mfma_f32_16x16x32_bf16 v[164:167], v[220:223], a[204:207], v[164:167]\n"
"ds_read_b128 v[220:223], v229 offset:4608\n"
"v_cvt_pk_bf16_f32 v128, v152, v153\n"
"v_mfma_f32_16x16x32_bf16 v[164:167], v[208:211], a[192:195], v[164:167]\n"
"ds_read_b128 v[208:211], v228 offset:4096\n"
"v_cvt_pk_bf16_f32 v91, v94, v95\n"
"v_mfma_f32_16x16x32_bf16 v[164:167], v[212:215], a[196:199], v[164:167]\n"
"s_waitcnt lgkmcnt(2)\n"
"ds_read_b128 v[212:215], v229 offset:4096\n"
"v_cvt_pk_bf16_f32 v24, v24, v25\n"
"v_mfma_f32_16x16x32_bf16 v[168:171], v[216:219], a[8:11], v[180:183]\n"
"s_waitcnt lgkmcnt(2)\n"
"v_exp_f32 v160, v160\n"
"v_mfma_f32_16x16x32_bf16 v[168:171], v[220:223], a[12:15], v[168:171]\n"
"s_waitcnt lgkmcnt(1)\n"
"v_exp_f32 v157, v157\n"
"v_mfma_f32_16x16x32_bf16 v[168:171], v[208:211], a[0:3], v[168:171]\n"
"s_waitcnt lgkmcnt(0)\n"
"v_exp_f32 v163, v163\n"
"v_mfma_f32_16x16x32_bf16 v[168:171], v[212:215], a[4:7], v[168:171]\n"
"ds_write2st64_b64 v255, v[88:89], v[90:91] offset0:226 offset1:227\n"
"v_cvt_pk_bf16_f32 v107, v110, v111\n"
"v_exp_f32 v159, v159\n"
"v_exp_f32 v162, v162\n"
"v_exp_f32 v166, v166\n"
"v_mfma_f32_16x16x32_bf16 v[172:175], v[208:211], a[16:19], v[184:187]\n"
"v_exp_f32 v156, v156\n"
"v_mfma_f32_16x16x32_bf16 v[172:175], v[212:215], a[20:23], v[172:175]\n"
"v_exp_f32 v165, v165\n"
"v_mfma_f32_16x16x32_bf16 v[172:175], v[216:219], a[24:27], v[172:175]\n"
"v_cvt_pk_bf16_f32 v136, v156, v157\n"
"v_cvt_pk_bf16_f32 v145, v162, v163\n"
"v_mfma_f32_16x16x32_bf16 v[172:175], v[220:223], a[28:31], v[172:175]\n"
"ds_write2st64_b64 v255, v[104:105], v[106:107] offset0:230 offset1:231\n"
"v_cvt_pk_bf16_f32 v137, v158, v159\n"
"v_exp_f32 v164, v164\n"
"v_exp_f32 v167, v167\n"
"v_mfma_f32_16x16x32_bf16 v[176:179], v[216:219], a[40:43], v[188:191]\n"
"v_cvt_pk_bf16_f32 v11, v14, v15\n"
"v_and_b32 v249, 0xf0, v242\n"
"v_mfma_f32_16x16x32_bf16 v[176:179], v[220:223], a[44:47], v[176:179]\n"
"v_cvt_pk_bf16_f32 v10, v12, v13\n"
"v_cvt_pk_bf16_f32 v129, v154, v155\n"
"v_mfma_f32_16x16x32_bf16 v[176:179], v[208:211], a[32:35], v[176:179]\n"
"v_permlane16_swap_b32 v57, v59\n"
"v_mfma_f32_16x16x32_bf16 v[176:179], v[212:215], a[36:39], v[176:179]\n"
"v_permlane16_swap_b32 v8, v10\n"
"v_permlane16_swap_b32 v9, v11\n"
"v_exp_f32 v161, v161\n"
"v_exp_f32 v168, v168\n"
"v_exp_f32 v174, v174\n"
"v_bfe_u32 v248, v242, 9, 1\n"
"v_lshl_add_u32 v248, v248, 8, v249\n"
"v_mfma_f32_16x16x32_bf16 v[152:155], v[208:211], a[192:195], v[192:195]\n"
"ds_read_b128 v[208:211], v230 offset:0\n"
"v_cvt_pk_bf16_f32 v144, v160, v161\n"
"v_mfma_f32_16x16x32_bf16 v[152:155], v[212:215], a[196:199], v[152:155]\n"
"ds_read_b128 v[212:215], v231 offset:0\n"
"v_bfe_u32 v249, v242, 8, 1\n"
"v_mfma_f32_16x16x32_bf16 v[152:155], v[216:219], a[200:203], v[152:155]\n"
"ds_read_b128 v[216:219], v230 offset:512\n"
"v_lshl_add_u32 v248, v249, 9, v248\n"
"v_mfma_f32_16x16x32_bf16 v[152:155], v[220:223], a[204:207], v[152:155]\n"
"s_waitcnt lgkmcnt(2)\n"
"ds_read_b128 v[220:223], v231 offset:512\n"
"v_cvt_pk_bf16_f32 v51, v54, v55\n"
"v_cvt_pk_bf16_f32 v125, v166, v167\n"
"v_exp_f32 v170, v170\n"
"v_exp_f32 v171, v171\n"
"v_exp_f32 v172, v172\n"
"v_exp_f32 v175, v175\n"
"v_exp_f32 v176, v176\n"
"v_exp_f32 v177, v177\n"
"v_mfma_f32_16x16x32_bf16 v[156:159], v[208:211], a[0:3], v[180:183]\n"
"s_waitcnt lgkmcnt(2)\n"
"v_cvt_pk_bf16_f32 v42, v44, v45\n"
"v_cvt_pk_bf16_f32 v43, v46, v47\n"
"v_mfma_f32_16x16x32_bf16 v[156:159], v[212:215], a[4:7], v[156:159]\n"
"s_waitcnt lgkmcnt(1)\n"
"v_permlane16_swap_b32 v120, v122\n"
"v_mfma_f32_16x16x32_bf16 v[156:159], v[216:219], a[8:11], v[156:159]\n"
"s_waitcnt lgkmcnt(0)\n"
"v_exp_f32 v173, v173\n"
"v_mfma_f32_16x16x32_bf16 v[156:159], v[220:223], a[12:15], v[156:159]\n"
"ds_write2st64_b64 v255, v[64:65], v[66:67] offset0:196 offset1:197\n"
"v_cvt_pk_bf16_f32 v124, v164, v165\n"
"v_exp_f32 v169, v169\n"
"v_cvt_pk_bf16_f32 v131, v170, v171\n"
"v_exp_f32 v154, v154\n"
"v_mfma_f32_16x16x32_bf16 v[160:163], v[216:219], a[24:27], v[184:187]\n"
"v_permlane16_swap_b32 v121, v123\n"
"v_mfma_f32_16x16x32_bf16 v[160:163], v[220:223], a[28:31], v[160:163]\n"
"v_exp_f32 v153, v153\n"
"v_mfma_f32_16x16x32_bf16 v[160:163], v[208:211], a[16:19], v[160:163]\n"
"v_exp_f32 v178, v178\n"
"v_mfma_f32_16x16x32_bf16 v[160:163], v[212:215], a[20:23], v[160:163]\n"
"v_exp_f32 v157, v157\n"
"v_mfma_f32_16x16x32_bf16 v[164:167], v[208:211], a[32:35], v[188:191]\n"
"ds_read_b128 v[208:211], v230 offset:4096\n"
"v_cvt_pk_bf16_f32 v130, v168, v169\n"
"v_mfma_f32_16x16x32_bf16 v[164:167], v[212:215], a[36:39], v[164:167]\n"
"ds_read_b128 v[212:215], v231 offset:4096\n"
"v_cvt_pk_bf16_f32 v138, v172, v173\n"
"v_mfma_f32_16x16x32_bf16 v[164:167], v[216:219], a[40:43], v[164:167]\n"
"ds_read_b128 v[216:219], v230 offset:4608\n"
"v_xor_b32_e64 v249, s42, 0\n"
"v_mfma_f32_16x16x32_bf16 v[164:167], v[220:223], a[44:47], v[164:167]\n"
"s_waitcnt lgkmcnt(2)\n"
"ds_read_b128 v[220:223], v231 offset:4608\n"
"v_cvt_pk_bf16_f32 v25, v26, v27\n"
"v_permlane16_swap_b32 v41, v43\n"
"v_exp_f32 v152, v152\n"
"v_exp_f32 v155, v155\n"
"v_exp_f32 v158, v158\n"
"v_exp_f32 v159, v159\n"
"v_mfma_f32_16x16x32_bf16 v[168:171], v[208:211], a[0:3], v[180:183]\n"
"s_waitcnt lgkmcnt(2)\n"
"v_permlane16_swap_b32 v49, v51\n"
"v_mfma_f32_16x16x32_bf16 v[168:171], v[212:215], a[4:7], v[168:171]\n"
"s_waitcnt lgkmcnt(1)\n"
"v_permlane16_swap_b32 v48, v50\n"
"v_mfma_f32_16x16x32_bf16 v[168:171], v[216:219], a[8:11], v[168:171]\n"
"s_waitcnt lgkmcnt(0)\n"
"v_exp_f32 v179, v179\n"
"v_mfma_f32_16x16x32_bf16 v[168:171], v[220:223], a[12:15], v[168:171]\n"
"ds_write_b128 v243, v[48:51] offset:12288\n"
"v_cvt_pk_bf16_f32 v112, v112, v113\n"
"v_cvt_pk_bf16_f32 v139, v174, v175\n"
"v_cvt_pk_bf16_f32 v147, v178, v179\n"
"v_exp_f32 v160, v160\n"
"v_exp_f32 v161, v161\n"
"v_exp_f32 v163, v163\n"
"v_mfma_f32_16x16x32_bf16 v[172:175], v[216:219], a[24:27], v[184:187]\n"
"v_cvt_pk_bf16_f32 v26, v28, v29\n"
"v_accvgpr_write_b32 a240, v254\n"
"v_mfma_f32_16x16x32_bf16 v[172:175], v[220:223], a[28:31], v[172:175]\n"
"v_accvgpr_write_b32 a187, v43\n"
"v_accvgpr_write_b32 a243, v254\n"
"v_mfma_f32_16x16x32_bf16 v[172:175], v[208:211], a[16:19], v[172:175]\n"
"v_cvt_pk_bf16_f32 v27, v30, v31\n"
"v_cvt_pk_bf16_f32 v146, v176, v177\n"
"v_mfma_f32_16x16x32_bf16 v[172:175], v[212:215], a[20:23], v[172:175]\n"
"s_cbranch_vccnz LPCE0_%=\nLPKE0_%=:\n"
"ds_write_b128 v243, v[56:59] offset:14336\n"
"v_accvgpr_write_b32 a155, v11\n"
"v_mov_b32 v202, 0\n"
"v_exp_f32 v156, v156\n"
"v_cvt_pk_bf16_f32 v133, v158, v159\n"
"v_cvt_pk_bf16_f32 v140, v160, v161\n"
"v_exp_f32 v167, v167\n"
"v_mfma_f32_16x16x32_bf16 v[176:179], v[208:211], a[32:35], v[188:191]\n"
"v_cvt_pk_bf16_f32 v126, v152, v153\n"
"v_lshl_add_u32 v248, v249, 12, v248\n"
"v_mfma_f32_16x16x32_bf16 v[176:179], v[212:215], a[36:39], v[176:179]\n"
"v_exp_f32 v170, v170\n"
"v_mfma_f32_16x16x32_bf16 v[176:179], v[216:219], a[40:43], v[176:179]\n"
"v_exp_f32 v164, v164\n"
"v_mfma_f32_16x16x32_bf16 v[176:179], v[220:223], a[44:47], v[176:179]\n"
"v_permlane16_swap_b32 v25, v27\n"
"v_permlane16_swap_b32 v40, v42\n"
"v_cvt_pk_bf16_f32 v127, v154, v155\n"
"v_exp_f32 v165, v165\n"
"v_exp_f32 v166, v166\n"
"v_exp_f32 v168, v168\n"
"v_exp_f32 v171, v171\n"
"v_add_u32 v249, s75, v248\n"
"s_waitcnt vmcnt(8) lgkmcnt(0)\n"
"s_barrier\n"
"ds_read_b128 v[208:211], v228 offset:16384\n"
"ds_read_b128 v[212:215], v229 offset:16384\n"
"ds_read_b128 v[216:219], v228 offset:16896\n"
"ds_read_b128 v[220:223], v229 offset:16896\n"
"s_waitcnt lgkmcnt(3)\n"
"v_mfma_f32_16x16x32_bf16 v[152:155], v[208:211], a[0:3], v[180:183]\n"
"v_accvgpr_write_b32 a154, v10\n"
"v_accvgpr_write_b32 a242, v254\n"
"s_waitcnt lgkmcnt(2)\n"
"v_mfma_f32_16x16x32_bf16 v[152:155], v[212:215], a[4:7], v[152:155]\n"
"ds_read_b64_tr_b16 a[246:247], v234 offset:4096\n"
"ds_read_b128 a[108:111], v248 offset:50176\n"
"s_waitcnt lgkmcnt(3)\n"
"v_mfma_f32_16x16x32_bf16 v[152:155], v[216:219], a[8:11], v[152:155]\n"
"s_waitcnt lgkmcnt(2)\n"
"ds_read_b128 a[156:159], v249 offset:35840\n"
"v_cvt_pk_bf16_f32 v149, v166, v167\n"
"v_mfma_f32_16x16x32_bf16 v[152:155], v[220:223], a[12:15], v[152:155]\n"
"ds_read_b128 a[100:103], v248 offset:49152\n"
"v_cvt_pk_bf16_f32 v113, v114, v115\n"
"v_cvt_pk_bf16_f32 v132, v156, v157\n"
"v_exp_f32 v162, v162\n"
"v_exp_f32 v173, v173\n"
"v_exp_f32 v175, v175\n"
"v_exp_f32 v176, v176\n"
"v_mfma_f32_16x16x32_bf16 v[156:159], v[216:219], a[24:27], v[184:187]\n"
"v_cvt_pk_bf16_f32 v0, v0, v1\n"
"v_cvt_pk_bf16_f32 v141, v162, v163\n"
"v_mfma_f32_16x16x32_bf16 v[156:159], v[220:223], a[28:31], v[156:159]\n"
"ds_read_b64_tr_b16 a[244:245], v234 offset:0\n"
"ds_read_b64_tr_b16 a[248:249], v238 offset:0\n"
"v_mfma_f32_16x16x32_bf16 v[156:159], v[208:211], a[16:19], v[156:159]\n"
"v_accvgpr_write_b32 a152, v8\n"
"v_cvt_pk_bf16_f32 v135, v170, v171\n"
"v_mfma_f32_16x16x32_bf16 v[156:159], v[212:215], a[20:23], v[156:159]\n"
"ds_read_b64_tr_b16 a[250:251], v238 offset:4096\n"
"ds_read_b64_tr_b16 v[226:227], v239 offset:4096\n"
"v_exp_f32 v169, v169\n"
"v_exp_f32 v177, v177\n"
"v_exp_f32 v178, v178\n"
"v_exp_f32 v179, v179\n"
"v_mfma_f32_16x16x32_bf16 v[160:163], v[208:211], a[32:35], v[188:191]\n"
"ds_read_b128 a[124:127], v248 offset:52224\n"
"v_cvt_pk_bf16_f32 v16, v16, v17\n"
"v_mfma_f32_16x16x32_bf16 v[160:163], v[212:215], a[36:39], v[160:163]\n"
"v_accvgpr_write_b32 a153, v9\n"
"v_accvgpr_write_b32 a185, v41\n"
"v_mfma_f32_16x16x32_bf16 v[160:163], v[216:219], a[40:43], v[160:163]\n"
"ds_read_b64_tr_b16 a[252:253], v235 offset:0\n"
"ds_read_b128 a[172:175], v249 offset:50176\n"
"v_mfma_f32_16x16x32_bf16 v[160:163], v[220:223], a[44:47], v[160:163]\n"
"ds_read_b64_tr_b16 a[254:255], v235 offset:4096\n"
"v_accvgpr_write_b32 a169, v25\n"
"v_accvgpr_write_b32 a186, v42\n"
"v_cvt_pk_bf16_f32 v148, v164, v165\n"
"v_cvt_pk_bf16_f32 v134, v168, v169\n"
"v_exp_f32 v172, v172\n"
"v_exp_f32 v174, v174\n"
"v_xor_b32 v207, 64, v240\n"
"v_exp_f32 v152, v152\n"
"v_exp_f32 v153, v153\n"
"v_mfma_f32_16x16x32_bf16 v[164:167], v[216:219], a[200:203], v[192:195]\n"
"ds_read_b128 a[188:191], v249 offset:52224\n"
"v_accvgpr_write_b32 a184, v40\n"
"v_mfma_f32_16x16x32_bf16 v[164:167], v[220:223], a[204:207], v[164:167]\n"
"ds_read_b64_tr_b16 v[224:225], v239 offset:0\n"
"v_cvt_pk_bf16_f32 v115, v118, v119\n"
"v_mfma_f32_16x16x32_bf16 v[164:167], v[208:211], a[192:195], v[164:167]\n"
"ds_read_b128 a[116:119], v248 offset:51200\n"
"v_cvt_pk_bf16_f32 v142, v172, v173\n"
"v_mfma_f32_16x16x32_bf16 v[164:167], v[212:215], a[196:199], v[164:167]\n"
"s_waitcnt lgkmcnt(7)\n"
"ds_read_b128 a[164:167], v249 offset:49152\n"
"v_cvt_pk_bf16_f32 v150, v176, v177\n"
"v_mfma_f32_16x16x32_bf16 a[216:219], v[124:127], a[244:247], 0\n"
"ds_read_b128 a[180:183], v249 offset:51200\n"
"v_mov_b32 v203, 0\n"
"v_mfma_f32_16x16x32_bf16 v[8:11], v[128:131], a[244:247], 0\n"
"ds_read_b128 a[140:143], v249 offset:33792\n"
"v_cvt_pk_bf16_f32 v17, v18, v19\n"
"v_mfma_f32_16x16x32_bf16 v[8:11], v[132:135], a[248:251], v[8:11]\n"
"ds_read_b128 a[132:135], v249 offset:32768\n"
"v_cvt_pk_bf16_f32 v143, v174, v175\n"
"v_mfma_f32_16x16x32_bf16 v[40:43], v[136:139], a[244:247], 0\n"
"ds_read_b128 v[216:219], v228 offset:20992\n"
"v_bfe_u32 v248, v242, 8, 2\n"
"v_mfma_f32_16x16x32_bf16 v[40:43], v[140:143], a[248:251], v[40:43]\n"
"ds_read_b128 a[148:151], v249 offset:34816\n"
"v_cvt_pk_bf16_f32 v151, v178, v179\n"
"v_mfma_f32_16x16x32_bf16 v[72:75], v[144:147], a[244:247], 0\n"
"v_permlane16_swap_b32 v113, v115\n"
"v_mfma_f32_16x16x32_bf16 v[72:75], v[148:151], a[248:251], v[72:75]\n"
"s_waitcnt lgkmcnt(6)\n"
"ds_read_b128 v[208:211], v228 offset:20480\n"
"v_mov_b32 v196, 0\n"
"v_mfma_f32_16x16x32_bf16 v[76:79], v[148:151], v[224:227], 0\n"
"ds_read_b128 v[220:223], v229 offset:20992\n"
"v_cvt_pk_bf16_f32 v96, v152, v153\n"
"v_mfma_f32_16x16x32_bf16 v[76:79], v[144:147], a[252:255], v[76:79]\n"
"v_exp_f32 v162, v162\n"
"v_mfma_f32_16x16x32_bf16 v[44:47], v[140:143], v[224:227], 0\n"
"ds_read_b128 v[212:215], v229 offset:20480\n"
"v_cvt_pk_bf16_f32 v18, v20, v21\n"
"v_mfma_f32_16x16x32_bf16 v[44:47], v[136:139], a[252:255], v[44:47]\n"
"ds_read_b64_tr_b16 a[250:251], v238 offset:4608\n"
"v_cvt_pk_bf16_f32 v114, v116, v117\n"
"v_mfma_f32_16x16x32_bf16 v[12:15], v[132:135], v[224:227], 0\n"
"v_permlane16_swap_b32 v24, v26\n"
"v_mfma_f32_16x16x32_bf16 v[12:15], v[128:131], a[252:255], v[12:15]\n"
"ds_read_b64_tr_b16 a[248:249], v238 offset:512\n"
"v_accvgpr_write_b32 a105, v121\n"
"v_permlane16_swap_b32 v16, v18\n"
"v_accvgpr_write_b32 a171, v27\n"
"v_exp_f32 v154, v154\n"
"v_exp_f32 v161, v161\n"
"v_mfma_f32_16x16x32_bf16 a[220:223], v[124:127], a[252:255], 0\n"
"s_waitcnt lgkmcnt(5)\n"
"v_exp_f32 v159, v159\n"
"v_smfmac_f32_16x16x64_bf16 v[196:199], a[240:243], v[128:135], v241\n"
"s_add_u32 m0, s44, 98304\n"
"ds_read_b64_tr_b16 a[254:255], v235 offset:4608\n"
"v_mov_b32 v200, 0\n"
"v_mfma_f32_16x16x32_bf16 v[202:205], a[240:243], v[124:127], v[202:205]\n"
"buffer_load_dwordx4 v240, s[20:23], s56 offen nt lds\n"
"v_mfma_f32_16x16x32_bf16 v[168:171], v[216:219], a[8:11], v[180:183]\n"
"s_waitcnt lgkmcnt(4)\n"
"v_exp_f32 v156, v156\n"
"v_mfma_f32_16x16x32_bf16 v[168:171], v[220:223], a[12:15], v[168:171]\n"
"v_exp_f32 v163, v163\n"
"v_mfma_f32_16x16x32_bf16 v[168:171], v[208:211], a[0:3], v[168:171]\n"
"s_waitcnt lgkmcnt(2)\n"
"ds_read_b64_tr_b16 a[252:253], v235 offset:512\n"
"v_accvgpr_write_b32 a104, v120\n"
"v_mfma_f32_16x16x32_bf16 v[168:171], v[212:215], a[4:7], v[168:171]\n"
"ds_read_b64_tr_b16 a[246:247], v234 offset:4608\n"
"v_cvt_pk_bf16_f32 v32, v32, v33\n"
"v_mfma_f32_16x16x32_bf16 v[172:175], v[208:211], a[16:19], v[184:187]\n"
"ds_read_b64_tr_b16 a[244:245], v234 offset:512\n"
"v_mov_b32 v201, 0\n"
"v_mfma_f32_16x16x32_bf16 v[172:175], v[212:215], a[20:23], v[172:175]\n"
"v_exp_f32 v158, v158\n"
"v_mfma_f32_16x16x32_bf16 v[172:175], v[216:219], a[24:27], v[172:175]\n"
"s_add_u32 s40, s56, s52\n"
"v_exp_f32 v155, v155\n"
"v_mfma_f32_16x16x32_bf16 v[172:175], v[220:223], a[28:31], v[172:175]\n"
"buffer_load_dwordx4 v207, s[20:23], s40 offen offset:1024 nt lds\n"
"v_mfma_f32_16x16x32_bf16 v[176:179], v[216:219], a[40:43], v[188:191]\n"
"v_exp_f32 v171, v171\n"
"v_mfma_f32_16x16x32_bf16 v[176:179], v[220:223], a[44:47], v[176:179]\n"
"v_cvt_pk_bf16_f32 v1, v2, v3\n"
"v_accvgpr_write_b32 a168, v24\n"
"v_mfma_f32_16x16x32_bf16 v[176:179], v[208:211], a[32:35], v[176:179]\n"
"v_accvgpr_write_b32 a97, v113\n"
"v_accvgpr_write_b32 a170, v26\n"
"v_mfma_f32_16x16x32_bf16 v[176:179], v[212:215], a[36:39], v[176:179]\n"
"ds_read_b64_tr_b16 v[224:225], v239 offset:512\n"
"v_cvt_pk_bf16_f32 v97, v154, v155\n"
"v_mfma_f32_16x16x32_bf16 v[152:155], v[208:211], a[192:195], v[192:195]\n"
"v_permlane16_swap_b32 v112, v114\n"
"v_mfma_f32_16x16x32_bf16 v[152:155], v[212:215], a[196:199], v[152:155]\n"
"ds_read_b64_tr_b16 v[226:227], v239 offset:4608\n"
"ds_read_b128 v[212:215], v231 offset:16384\n"
"v_mfma_f32_16x16x32_bf16 v[152:155], v[216:219], a[200:203], v[152:155]\n"
"v_cvt_pk_bf16_f32 v33, v34, v35\n"
"v_cvt_pk_bf16_f32 v34, v36, v37\n"
"v_mfma_f32_16x16x32_bf16 v[152:155], v[220:223], a[204:207], v[152:155]\n"
"s_waitcnt lgkmcnt(3)\n"
"v_exp_f32 v157, v157\n"
"v_mfma_f32_16x16x32_bf16 a[232:235], v[124:127], a[244:247], 0\n"
"ds_read_b128 v[220:223], v231 offset:16896\n"
"v_cvt_pk_bf16_f32 v105, v158, v159\n"
"v_mfma_f32_16x16x32_bf16 v[24:27], v[128:131], a[244:247], 0\n"
"s_add_u32 s40, s56, s53\n"
"ds_read_b128 v[216:219], v230 offset:16896\n"
"v_cvt_pk_bf16_f32 v3, v6, v7\n"
"v_mfma_f32_16x16x32_bf16 v[24:27], v[132:135], a[248:251], v[24:27]\n"
"buffer_load_dwordx4 v240, s[20:23], s40 offen offset:2048 nt lds\n"
"v_mfma_f32_16x16x32_bf16 v[56:59], v[136:139], a[244:247], 0\n"
"v_exp_f32 v169, v169\n"
"v_mfma_f32_16x16x32_bf16 v[56:59], v[140:143], a[248:251], v[56:59]\n"
"v_accvgpr_write_b32 a96, v112\n"
"v_cvt_pk_bf16_f32 v2, v4, v5\n"
"v_mfma_f32_16x16x32_bf16 v[88:91], v[144:147], a[244:247], 0\n"
"ds_read_b128 v[208:211], v230 offset:16384\n"
"v_cvt_pk_bf16_f32 v35, v38, v39\n"
"v_mfma_f32_16x16x32_bf16 v[88:91], v[148:151], a[248:251], v[88:91]\n"
"s_waitcnt lgkmcnt(3)\n"
"v_exp_f32 v172, v172\n"
"v_mfma_f32_16x16x32_bf16 v[92:95], v[148:151], v[224:227], 0\n"
"s_add_u32 s40, s56, s54\n"
"v_exp_f32 v170, v170\n"
"v_mfma_f32_16x16x32_bf16 v[92:95], v[144:147], a[252:255], v[92:95]\n"
"buffer_load_dwordx4 v207, s[20:23], s40 offen offset:3072 nt lds\n"
"v_permlane16_swap_b32 v1, v3\n"
"v_accvgpr_write_b32 a147, v3\n"
"v_permlane16_swap_b32 v32, v34\n"
"v_accvgpr_write_b32 a178, v34\n"
"v_exp_f32 v164, v164\n"
"v_exp_f32 v165, v165\n"
"v_exp_f32 v173, v173\n"
"v_exp_f32 v175, v175\n"
"v_exp_f32 v155, v155\n"
"v_mfma_f32_16x16x32_bf16 v[60:63], v[140:143], v[224:227], 0\n"
"s_add_u32 s55, s55, s59\n"
"v_accvgpr_write_b32 a145, v1\n"
"v_cvt_pk_bf16_f32 v104, v156, v157\n"
"v_mfma_f32_16x16x32_bf16 v[60:63], v[136:139], a[252:255], v[60:63]\n"
"s_or_b32 s55, s55, s58\n"
"v_accvgpr_write_b32 a160, v16\n"
"v_cvt_pk_bf16_f32 v113, v162, v163\n"
"v_mfma_f32_16x16x32_bf16 v[28:31], v[132:135], v[224:227], 0\n"
"s_xor_b32 s56, s55, s57\n"
"v_exp_f32 v167, v167\n"
"v_mfma_f32_16x16x32_bf16 v[28:31], v[128:131], a[252:255], v[28:31]\n"
"v_permlane16_swap_b32 v0, v2\n"
"v_permlane16_swap_b32 v33, v35\n"
"v_exp_f32 v166, v166\n"
"v_exp_f32 v168, v168\n"
"v_exp_f32 v153, v153\n"
"v_mfma_f32_16x16x32_bf16 a[236:239], v[124:127], a[252:255], 0\n"
"ds_read_b64_tr_b16 v[226:227], v237 offset:4096\n"
"v_lshlrev_b32 v248, 6, v248\n"
"v_smfmac_f32_16x16x64_bf16 v[198:201], a[240:243], v[136:143], v241\n"
"s_waitcnt lgkmcnt(1)\n"
"ds_read_b64_tr_b16 a[244:245], v232 offset:0\n"
"v_cvt_pk_bf16_f32 v99, v170, v171\n"
"v_mfma_f32_16x16x32_bf16 v[156:159], v[208:211], a[0:3], v[180:183]\n"
"ds_read_b64_tr_b16 a[246:247], v232 offset:4096\n"
"ds_read_b64_tr_b16 a[250:251], v236 offset:4096\n"
"v_mfma_f32_16x16x32_bf16 v[156:159], v[212:215], a[4:7], v[156:159]\n"
"v_exp_f32 v160, v160\n"
"v_mfma_f32_16x16x32_bf16 v[156:159], v[216:219], a[8:11], v[156:159]\n"
"s_add_u32 m0, s44, 49152\n"
"ds_read_b64_tr_b16 a[248:249], v236 offset:0\n"
"v_cvt_pk_bf16_f32 v112, v160, v161\n"
"v_mfma_f32_16x16x32_bf16 v[156:159], v[220:223], a[12:15], v[156:159]\n"
"buffer_load_dwordx4 v240, s[16:19], s56 offen nt lds\n"
"v_mfma_f32_16x16x32_bf16 v[160:163], v[216:219], a[24:27], v[184:187]\n"
"v_accvgpr_write_b32 a176, v32\n"
"v_cvt_pk_bf16_f32 v120, v164, v165\n"
"v_mfma_f32_16x16x32_bf16 v[160:163], v[220:223], a[28:31], v[160:163]\n"
"v_exp_f32 v174, v174\n"
"v_mfma_f32_16x16x32_bf16 v[160:163], v[208:211], a[16:19], v[160:163]\n"
"ds_read_b64_tr_b16 v[224:225], v237 offset:0\n"
"v_cvt_pk_bf16_f32 v121, v166, v167\n"
"v_mfma_f32_16x16x32_bf16 v[160:163], v[212:215], a[20:23], v[160:163]\n"
"v_exp_f32 v176, v176\n"
"v_mfma_f32_16x16x32_bf16 v[164:167], v[208:211], a[32:35], v[188:191]\n"
"v_accvgpr_write_b32 a99, v115\n"
"v_accvgpr_write_b32 a177, v33\n"
"v_mfma_f32_16x16x32_bf16 v[164:167], v[212:215], a[36:39], v[164:167]\n"
"ds_read_b64_tr_b16 a[252:253], v233 offset:0\n"
"v_accvgpr_write_b32 a106, v122\n"
"v_mfma_f32_16x16x32_bf16 v[164:167], v[216:219], a[40:43], v[164:167]\n"
"v_accvgpr_write_b32 a144, v0\n"
"v_accvgpr_write_b32 a146, v2\n"
"v_mfma_f32_16x16x32_bf16 v[164:167], v[220:223], a[44:47], v[164:167]\n"
"s_waitcnt lgkmcnt(2)\n"
"ds_read_b128 v[220:223], v231 offset:20992\n"
"v_accvgpr_write_b32 a179, v35\n"
"v_mfma_f32_16x16x32_bf16 a[208:211], v[124:127], a[244:247], 0\n"
"ds_read_b64_tr_b16 a[254:255], v233 offset:4096\n"
"v_cvt_pk_bf16_f32 v107, v174, v175\n"
"v_mfma_f32_16x16x32_bf16 v[0:3], v[128:131], a[244:247], 0\n"
"s_add_u32 s40, s56, s52\n"
"ds_read_b128 v[212:215], v231 offset:20480\n"
"v_cvt_pk_bf16_f32 v19, v22, v23\n"
"v_mfma_f32_16x16x32_bf16 v[0:3], v[132:135], a[248:251], v[0:3]\n"
"buffer_load_dwordx4 v207, s[16:19], s40 offen offset:1024 nt lds\n"
"v_mfma_f32_16x16x32_bf16 v[32:35], v[136:139], a[244:247], 0\n"
"v_exp_f32 v166, v166\n"
"v_mfma_f32_16x16x32_bf16 v[32:35], v[140:143], a[248:251], v[32:35]\n"
"ds_read_b128 v[208:211], v230 offset:20480\n"
"v_xor_b32 v248, v248, v242\n"
"v_bfe_u32 v254, v242, 6, 4\n"
"v_cvt_pk_bf16_f32 v98, v168, v169\n"
"v_exp_f32 v177, v177\n"
"v_exp_f32 v178, v178\n"
"v_exp_f32 v158, v158\n"
"v_exp_f32 v159, v159\n"
"v_exp_f32 v162, v162\n"
"v_mfma_f32_16x16x32_bf16 v[64:67], v[144:147], a[244:247], 0\n"
"v_exp_f32 v167, v167\n"
"v_mfma_f32_16x16x32_bf16 v[64:67], v[148:151], a[248:251], v[64:67]\n"
"s_waitcnt lgkmcnt(4)\n"
"ds_read_b128 v[216:219], v230 offset:20992\n"
"v_cvt_pk_bf16_f32 v106, v172, v173\n"
"v_mfma_f32_16x16x32_bf16 v[68:71], v[148:151], v[224:227], 0\n"
"s_waitcnt lgkmcnt(3)\n"
"s_add_u32 s40, s56, s53\n"
"v_permlane16_swap_b32 v17, v19\n"
"v_mfma_f32_16x16x32_bf16 v[68:71], v[144:147], a[252:255], v[68:71]\n"
"buffer_load_dwordx4 v240, s[16:19], s40 offen offset:2048 nt lds\n"
"v_mfma_f32_16x16x32_bf16 v[36:39], v[140:143], v[224:227], 0\n"
"v_exp_f32 v157, v157\n"
"v_mfma_f32_16x16x32_bf16 v[36:39], v[136:139], a[252:255], v[36:39]\n"
"v_exp_f32 v156, v156\n"
"v_mfma_f32_16x16x32_bf16 v[4:7], v[132:135], v[224:227], 0\n"
"ds_read_b64_tr_b16 a[250:251], v236 offset:4608\n"
"v_accvgpr_write_b32 a98, v114\n"
"v_mfma_f32_16x16x32_bf16 v[4:7], v[128:131], a[252:255], v[4:7]\n"
"v_exp_f32 v160, v160\n"
"v_mfma_f32_16x16x32_bf16 a[212:215], v[124:127], a[252:255], 0\n"
"ds_read_b64_tr_b16 a[246:247], v232 offset:4608\n"
"v_cvt_pk_bf16_f32 v100, v156, v157\n"
"v_smfmac_f32_16x16x64_bf16 v[200:203], a[240:243], v[144:151], v241\n"
"s_waitcnt lgkmcnt(2)\n"
"ds_read_b64_tr_b16 a[248:249], v236 offset:512\n"
"v_cvt_pk_bf16_f32 v114, v176, v177\n"
"v_mfma_f32_16x16x32_bf16 v[168:171], v[208:211], a[0:3], v[180:183]\n"
"v_exp_f32 v164, v164\n"
"v_mfma_f32_16x16x32_bf16 v[168:171], v[212:215], a[4:7], v[168:171]\n"
"ds_read_b64_tr_b16 a[244:245], v232 offset:512\n"
"v_accvgpr_write_b32 a161, v17\n"
"v_mfma_f32_16x16x32_bf16 v[168:171], v[216:219], a[8:11], v[168:171]\n"
"s_add_u32 s40, s56, s54\n"
"ds_read_b64_tr_b16 a[252:253], v233 offset:512\n"
"ds_read_b64_tr_b16 a[254:255], v233 offset:4608\n"
"v_mfma_f32_16x16x32_bf16 v[168:171], v[220:223], a[12:15], v[168:171]\n"
"buffer_load_dwordx4 v207, s[16:19], s40 offen offset:3072 nt lds\n"
"v_mfma_f32_16x16x32_bf16 v[172:175], v[216:219], a[24:27], v[184:187]\n"
"v_exp_f32 v154, v154\n"
"v_mfma_f32_16x16x32_bf16 v[172:175], v[220:223], a[28:31], v[172:175]\n"
"v_exp_f32 v179, v179\n"
"v_mfma_f32_16x16x32_bf16 v[172:175], v[208:211], a[16:19], v[172:175]\n"
"ds_read_b64_tr_b16 v[224:225], v237 offset:512\n"
"v_bfe_u32 v249, v242, 4, 2\n"
"v_mfma_f32_16x16x32_bf16 v[172:175], v[212:215], a[20:23], v[172:175]\n"
"s_cbranch_vccnz LPCE1_%=\nLPKE1_%=:\n"
"v_cvt_pk_bf16_f32 v115, v178, v179\n"
"v_mfma_f32_16x16x32_bf16 v[176:179], v[208:211], a[32:35], v[188:191]\n"
"v_accvgpr_write_b32 a163, v19\n"
"v_cvt_pk_bf16_f32 v101, v158, v159\n"
"v_mfma_f32_16x16x32_bf16 v[176:179], v[212:215], a[36:39], v[176:179]\n"
"ds_read_b64_tr_b16 v[226:227], v237 offset:4608\n"
"v_accvgpr_write_b32 a162, v18\n"
"v_mfma_f32_16x16x32_bf16 v[176:179], v[216:219], a[40:43], v[176:179]\n"
"v_exp_f32 v152, v152\n"
"v_mfma_f32_16x16x32_bf16 v[176:179], v[220:223], a[44:47], v[176:179]\n"
"s_waitcnt lgkmcnt(2)\n"
"v_exp_f32 v165, v165\n"
"v_mfma_f32_16x16x32_bf16 a[224:227], v[124:127], a[244:247], 0\n"
"v_exp_f32 v163, v163\n"
"v_mfma_f32_16x16x32_bf16 v[16:19], v[128:131], a[244:247], 0\n"
"s_waitcnt vmcnt(8)\n"
"v_exp_f32 v174, v174\n"
"v_mfma_f32_16x16x32_bf16 v[16:19], v[132:135], a[248:251], v[16:19]\n"
"s_barrier\n"
"ds_read_b128 v[208:211], v228 offset:32768\n"
"v_cvt_pk_bf16_f32 v116, v164, v165\n"
"v_exp_f32 v168, v168\n"
"v_exp_f32 v177, v177\n"
"v_mfma_f32_16x16x32_bf16 v[48:51], v[136:139], a[244:247], 0\n"
"ds_read_b128 v[212:215], v229 offset:32768\n"
"v_mul_u32_u24 v249, 20, v249\n"
"v_mfma_f32_16x16x32_bf16 v[48:51], v[140:143], a[248:251], v[48:51]\n"
"ds_read_b128 v[216:219], v228 offset:33280\n"
"ds_read_b128 v[220:223], v229 offset:33280\n"
"v_mfma_f32_16x16x32_bf16 v[80:83], v[144:147], a[244:247], 0\n"
"ds_read_b64_tr_b16 a[246:247], v234 offset:20480\n"
"v_cvt_pk_bf16_f32 v109, v162, v163\n"
"v_mfma_f32_16x16x32_bf16 v[80:83], v[148:151], a[248:251], v[80:83]\n"
"s_waitcnt lgkmcnt(5)\n"
"ds_read_b64_tr_b16 a[250:251], v238 offset:20480\n"
"v_cvt_pk_bf16_f32 v117, v166, v167\n"
"v_mfma_f32_16x16x32_bf16 v[84:87], v[148:151], v[224:227], 0\n"
"v_exp_f32 v169, v169\n"
"v_mfma_f32_16x16x32_bf16 v[84:87], v[144:147], a[252:255], v[84:87]\n"
"v_exp_f32 v161, v161\n"
"v_mfma_f32_16x16x32_bf16 v[52:55], v[140:143], v[224:227], 0\n"
"v_exp_f32 v176, v176\n"
"v_mfma_f32_16x16x32_bf16 v[52:55], v[136:139], a[252:255], v[52:55]\n"
"v_xor_b32 v249, v249, v254\n"
"v_lshl_add_u32 v249, v249, 4, s44\n"
"v_mfma_f32_16x16x32_bf16 v[20:23], v[132:135], v[224:227], 0\n"
"v_cvt_pk_bf16_f32 v122, v152, v153\n"
"v_cvt_pk_bf16_f32 v118, v176, v177\n"
"v_mfma_f32_16x16x32_bf16 v[20:23], v[128:131], a[252:255], v[20:23]\n"
"v_accvgpr_write_b32 a107, v123\n"
"v_cvt_pk_bf16_f32 v123, v154, v155\n"
"v_mfma_f32_16x16x32_bf16 a[228:231], v[124:127], a[252:255], 0\n"
"s_waitcnt lgkmcnt(1)\n"
"ds_read_b64_tr_b16 a[254:255], v235 offset:20480\n"
"s_branch LAY2_%=\n"
"LAL_%=:\n"
"v_mfma_f32_16x16x32_bf16 a[224:227], v[124:127], a[244:247], a[224:227]\n"
"v_exp_f32 v162, v162\n"
"v_mfma_f32_16x16x32_bf16 v[16:19], v[128:131], a[244:247], v[16:19]\n"
"s_waitcnt vmcnt(8)\n"
"v_exp_f32 v168, v168\n"
"v_mfma_f32_16x16x32_bf16 v[16:19], v[132:135], a[248:251], v[16:19]\n"
"s_barrier\n"
"v_mfma_f32_16x16x32_bf16 v[48:51], v[136:139], a[244:247], v[48:51]\n"
"ds_read_b128 v[208:211], v228 offset:0\n"
"v_cvt_pk_bf16_f32 v101, v158, v159\n"
"v_mfma_f32_16x16x32_bf16 v[48:51], v[140:143], a[248:251], v[48:51]\n"
"ds_read_b128 v[216:219], v228 offset:512\n"
"v_cvt_pk_bf16_f32 v109, v162, v163\n"
"v_mfma_f32_16x16x32_bf16 v[80:83], v[144:147], a[244:247], v[80:83]\n"
"ds_read_b128 v[220:223], v229 offset:512\n"
"v_cvt_pk_bf16_f32 v123, v154, v155\n"
"v_mfma_f32_16x16x32_bf16 v[80:83], v[148:151], a[248:251], v[80:83]\n"
"ds_read_b128 v[212:215], v229 offset:0\n"
"v_cvt_pk_bf16_f32 v117, v166, v167\n"
"v_mfma_f32_16x16x32_bf16 v[84:87], v[148:151], v[224:227], v[84:87]\n"
"v_exp_f32 v176, v176\n"
"v_mfma_f32_16x16x32_bf16 v[84:87], v[144:147], a[252:255], v[84:87]\n"
"v_exp_f32 v169, v169\n"
"v_mfma_f32_16x16x32_bf16 v[52:55], v[140:143], v[224:227], v[52:55]\n"
"ds_read_b64_tr_b16 a[246:247], v234 offset:53248\n"
"v_cvt_pk_bf16_f32 v116, v164, v165\n"
"v_mfma_f32_16x16x32_bf16 v[52:55], v[136:139], a[252:255], v[52:55]\n"
"v_exp_f32 v177, v177\n"
"v_mfma_f32_16x16x32_bf16 v[20:23], v[132:135], v[224:227], v[20:23]\n"
"ds_read_b64_tr_b16 a[250:251], v238 offset:53248\n"
"v_cvt_pk_bf16_f32 v118, v176, v177\n"
"v_mfma_f32_16x16x32_bf16 v[20:23], v[128:131], a[252:255], v[20:23]\n"
"v_exp_f32 v174, v174\n"
"v_mfma_f32_16x16x32_bf16 a[228:231], v[124:127], a[252:255], a[228:231]\n"
"s_waitcnt lgkmcnt(3)\n"
"ds_read_b64_tr_b16 a[254:255], v235 offset:53248\n"
"v_cvt_pk_bf16_f32 v100, v156, v157\n"
"v_mfma_f32_16x16x32_bf16 v[152:155], v[208:211], a[0:3], v[180:183]\n"
"s_waitcnt lgkmcnt(3)\n"
"ds_read_b64_tr_b16 a[248:249], v238 offset:49152\n"
"v_cvt_pk_bf16_f32 v102, v168, v169\n"
"v_mfma_f32_16x16x32_bf16 v[152:155], v[212:215], a[4:7], v[152:155]\n"
"v_exp_f32 v172, v172\n"
"v_mfma_f32_16x16x32_bf16 v[152:155], v[216:219], a[8:11], v[152:155]\n"
"ds_read_b64_tr_b16 a[244:245], v234 offset:49152\n"
"v_cvt_pk_bf16_f32 v108, v160, v161\n"
"v_mfma_f32_16x16x32_bf16 v[152:155], v[220:223], a[12:15], v[152:155]\n"
"v_exp_f32 v175, v175\n"
"v_mfma_f32_16x16x32_bf16 v[156:159], v[216:219], a[24:27], v[184:187]\n"
"ds_read_b64_tr_b16 a[252:253], v235 offset:49152\n"
"v_cvt_pk_bf16_f32 v111, v174, v175\n"
"v_mfma_f32_16x16x32_bf16 v[156:159], v[220:223], a[28:31], v[156:159]\n"
"ds_read_b64_tr_b16 v[224:225], v239 offset:49152\n"
"v_mfma_f32_16x16x32_bf16 v[156:159], v[208:211], a[16:19], v[156:159]\n"
"s_add_u32 m0, s44, 81920\n"
"ds_read_b64_tr_b16 v[226:227], v239 offset:53248\n"
"v_mfma_f32_16x16x32_bf16 v[156:159], v[212:215], a[20:23], v[156:159]\n"
"buffer_load_dwordx4 v240, s[20:23], s56 offen nt lds\n"
"v_mfma_f32_16x16x32_bf16 v[160:163], v[208:211], a[32:35], v[188:191]\n"
"v_exp_f32 v178, v178\n"
"v_mfma_f32_16x16x32_bf16 v[160:163], v[212:215], a[36:39], v[160:163]\n"
"v_exp_f32 v171, v171\n"
"v_mfma_f32_16x16x32_bf16 v[160:163], v[216:219], a[40:43], v[160:163]\n"
"s_add_u32 s40, s56, s52\n"
"v_mfma_f32_16x16x32_bf16 v[160:163], v[220:223], a[44:47], v[160:163]\n"
"buffer_load_dwordx4 v207, s[20:23], s40 offen offset:1024 nt lds\n"
"v_mfma_f32_16x16x32_bf16 v[164:167], v[216:219], a[200:203], v[192:195]\n"
"v_exp_f32 v170, v170\n"
"v_mfma_f32_16x16x32_bf16 v[164:167], v[220:223], a[204:207], v[164:167]\n"
"v_exp_f32 v153, v153\n"
"v_mfma_f32_16x16x32_bf16 v[164:167], v[208:211], a[192:195], v[164:167]\n"
"v_exp_f32 v179, v179\n"
"v_mfma_f32_16x16x32_bf16 v[164:167], v[212:215], a[196:199], v[164:167]\n"
"s_waitcnt lgkmcnt(0)\n"
"v_exp_f32 v158, v158\n"
"v_mfma_f32_16x16x32_bf16 a[216:219], v[120:123], a[244:247], a[216:219]\n"
"ds_read_b128 v[216:219], v228 offset:4608\n"
"v_cvt_pk_bf16_f32 v103, v170, v171\n"
"v_mfma_f32_16x16x32_bf16 v[8:11], v[96:99], a[244:247], v[8:11]\n"
"v_exp_f32 v173, v173\n"
"v_mfma_f32_16x16x32_bf16 v[8:11], v[100:103], a[248:251], v[8:11]\n"
"ds_read_b128 v[208:211], v228 offset:4096\n"
"v_cvt_pk_bf16_f32 v110, v172, v173\n"
"v_mfma_f32_16x16x32_bf16 v[40:43], v[104:107], a[244:247], v[40:43]\n"
"v_exp_f32 v162, v162\n"
"v_mfma_f32_16x16x32_bf16 v[40:43], v[108:111], a[248:251], v[40:43]\n"
"ds_read_b128 v[220:223], v229 offset:4608\n"
"v_cvt_pk_bf16_f32 v119, v178, v179\n"
"v_mfma_f32_16x16x32_bf16 v[72:75], v[112:115], a[244:247], v[72:75]\n"
"s_add_u32 s40, s56, s53\n"
"ds_read_b128 v[212:215], v229 offset:4096\n"
"v_mfma_f32_16x16x32_bf16 v[72:75], v[116:119], a[248:251], v[72:75]\n"
"buffer_load_dwordx4 v240, s[20:23], s40 offen offset:2048 nt lds\n"
"v_mfma_f32_16x16x32_bf16 v[76:79], v[116:119], v[224:227], v[76:79]\n"
"v_exp_f32 v163, v163\n"
"v_mfma_f32_16x16x32_bf16 v[76:79], v[112:115], a[252:255], v[76:79]\n"
"v_exp_f32 v156, v156\n"
"v_mfma_f32_16x16x32_bf16 v[44:47], v[108:111], v[224:227], v[44:47]\n"
"v_exp_f32 v160, v160\n"
"v_mfma_f32_16x16x32_bf16 v[44:47], v[104:107], a[252:255], v[44:47]\n"
"v_exp_f32 v155, v155\n"
"v_mfma_f32_16x16x32_bf16 v[12:15], v[100:103], v[224:227], v[12:15]\n"
"ds_read_b64_tr_b16 v[226:227], v239 offset:53760\n"
"v_mfma_f32_16x16x32_bf16 v[12:15], v[96:99], a[252:255], v[12:15]\n"
"ds_read_b64_tr_b16 a[248:249], v238 offset:49664\n"
"v_mfma_f32_16x16x32_bf16 a[220:223], v[120:123], a[252:255], a[220:223]\n"
"v_exp_f32 v154, v154\n"
"v_smfmac_f32_16x16x64_bf16 v[196:199], a[240:243], v[96:103], v241\n"
"ds_read_b64_tr_b16 a[250:251], v238 offset:53760\n"
"v_mfma_f32_16x16x32_bf16 v[202:205], a[240:243], v[120:123], v[202:205]\n"
"s_waitcnt lgkmcnt(3)\n"
"v_exp_f32 v152, v152\n"
"v_mfma_f32_16x16x32_bf16 v[168:171], v[216:219], a[8:11], v[180:183]\n"
"ds_read_b64_tr_b16 a[244:245], v234 offset:49664\n"
"v_cvt_pk_bf16_f32 v128, v152, v153\n"
"v_mfma_f32_16x16x32_bf16 v[168:171], v[220:223], a[12:15], v[168:171]\n"
"v_exp_f32 v165, v165\n"
"v_mfma_f32_16x16x32_bf16 v[168:171], v[208:211], a[0:3], v[168:171]\n"
"ds_read_b64_tr_b16 a[246:247], v234 offset:53760\n"
"v_cvt_pk_bf16_f32 v145, v162, v163\n"
"v_mfma_f32_16x16x32_bf16 v[168:171], v[212:215], a[4:7], v[168:171]\n"
"ds_read_b64_tr_b16 a[254:255], v235 offset:53760\n"
"v_mfma_f32_16x16x32_bf16 v[172:175], v[208:211], a[16:19], v[184:187]\n"
"ds_read_b64_tr_b16 v[224:225], v239 offset:49664\n"
"v_cvt_pk_bf16_f32 v129, v154, v155\n"
"v_mfma_f32_16x16x32_bf16 v[172:175], v[212:215], a[20:23], v[172:175]\n"
"v_mfma_f32_16x16x32_bf16 v[172:175], v[216:219], a[24:27], v[172:175]\n"
"s_add_u32 s40, s56, s54\n"
"ds_read_b64_tr_b16 a[252:253], v235 offset:49664\n"
"v_mfma_f32_16x16x32_bf16 v[172:175], v[220:223], a[28:31], v[172:175]\n"
"buffer_load_dwordx4 v207, s[20:23], s40 offen offset:3072 nt lds\n"
"v_mfma_f32_16x16x32_bf16 v[176:179], v[216:219], a[40:43], v[188:191]\n"
"s_add_u32 s55, s55, s59\n"
"v_exp_f32 v157, v157\n"
"v_mfma_f32_16x16x32_bf16 v[176:179], v[220:223], a[44:47], v[176:179]\n"
"s_or_b32 s55, s55, s58\n"
"v_exp_f32 v171, v171\n"
"v_mfma_f32_16x16x32_bf16 v[176:179], v[208:211], a[32:35], v[176:179]\n"
"s_xor_b32 s56, s55, s57\n"
"v_exp_f32 v159, v159\n"
"v_mfma_f32_16x16x32_bf16 v[176:179], v[212:215], a[36:39], v[176:179]\n"
"v_exp_f32 v167, v167\n"
"v_mfma_f32_16x16x32_bf16 v[152:155], v[208:211], a[192:195], v[192:195]\n"
"v_cvt_pk_bf16_f32 v137, v158, v159\n"
"v_mfma_f32_16x16x32_bf16 v[152:155], v[212:215], a[196:199], v[152:155]\n"
"ds_read_b128 v[208:211], v230 offset:0\n"
"v_cvt_pk_bf16_f32 v136, v156, v157\n"
"v_mfma_f32_16x16x32_bf16 v[152:155], v[216:219], a[200:203], v[152:155]\n"
"v_exp_f32 v166, v166\n"
"v_mfma_f32_16x16x32_bf16 v[152:155], v[220:223], a[204:207], v[152:155]\n"
"s_waitcnt lgkmcnt(1)\n"
"ds_read_b128 v[216:219], v230 offset:512\n"
"v_mfma_f32_16x16x32_bf16 a[232:235], v[120:123], a[244:247], a[232:235]\n"
"v_exp_f32 v169, v169\n"
"v_mfma_f32_16x16x32_bf16 v[24:27], v[96:99], a[244:247], v[24:27]\n"
"ds_read_b128 v[220:223], v231 offset:512\n"
"v_mfma_f32_16x16x32_bf16 v[24:27], v[100:103], a[248:251], v[24:27]\n"
"v_exp_f32 v164, v164\n"
"v_mfma_f32_16x16x32_bf16 v[56:59], v[104:107], a[244:247], v[56:59]\n"
"ds_read_b128 v[212:215], v231 offset:0\n"
"v_mfma_f32_16x16x32_bf16 v[56:59], v[108:111], a[248:251], v[56:59]\n"
"v_exp_f32 v178, v178\n"
"v_mfma_f32_16x16x32_bf16 v[88:91], v[112:115], a[244:247], v[88:91]\n"
"s_add_u32 m0, s44, 32768\n"
"ds_read_b64_tr_b16 a[246:247], v232 offset:53248\n"
"v_mfma_f32_16x16x32_bf16 v[88:91], v[116:119], a[248:251], v[88:91]\n"
"buffer_load_dwordx4 v240, s[16:19], s56 offen nt lds\n"
"v_mfma_f32_16x16x32_bf16 v[92:95], v[116:119], v[224:227], v[92:95]\n"
"v_exp_f32 v175, v175\n"
"v_mfma_f32_16x16x32_bf16 v[92:95], v[112:115], a[252:255], v[92:95]\n"
"v_exp_f32 v168, v168\n"
"v_mfma_f32_16x16x32_bf16 v[60:63], v[108:111], v[224:227], v[60:63]\n"
"v_exp_f32 v161, v161\n"
"v_mfma_f32_16x16x32_bf16 v[60:63], v[104:107], a[252:255], v[60:63]\n"
"v_exp_f32 v177, v177\n"
"v_mfma_f32_16x16x32_bf16 v[28:31], v[100:103], v[224:227], v[28:31]\n"
"ds_read_b64_tr_b16 v[226:227], v237 offset:53248\n"
"v_mfma_f32_16x16x32_bf16 v[28:31], v[96:99], a[252:255], v[28:31]\n"
"v_exp_f32 v172, v172\n"
"v_mfma_f32_16x16x32_bf16 a[236:239], v[120:123], a[252:255], a[236:239]\n"
"ds_read_b64_tr_b16 a[250:251], v236 offset:53248\n"
"v_cvt_pk_bf16_f32 v124, v164, v165\n"
"v_smfmac_f32_16x16x64_bf16 v[198:201], a[240:243], v[104:111], v241\n"
"s_waitcnt lgkmcnt(2)\n"
"ds_read_b64_tr_b16 a[252:253], v233 offset:49152\n"
"v_mfma_f32_16x16x32_bf16 v[156:159], v[208:211], a[0:3], v[180:183]\n"
"ds_read_b64_tr_b16 a[248:249], v236 offset:49152\n"
"v_cvt_pk_bf16_f32 v125, v166, v167\n"
"v_mfma_f32_16x16x32_bf16 v[156:159], v[212:215], a[4:7], v[156:159]\n"
"ds_read_b64_tr_b16 v[224:225], v237 offset:49152\n"
"v_cvt_pk_bf16_f32 v130, v168, v169\n"
"v_mfma_f32_16x16x32_bf16 v[156:159], v[216:219], a[8:11], v[156:159]\n"
"ds_read_b64_tr_b16 a[254:255], v233 offset:53248\n"
"v_cvt_pk_bf16_f32 v144, v160, v161\n"
"v_mfma_f32_16x16x32_bf16 v[156:159], v[220:223], a[12:15], v[156:159]\n"
"ds_read_b64_tr_b16 a[244:245], v232 offset:49152\n"
"v_mfma_f32_16x16x32_bf16 v[160:163], v[216:219], a[24:27], v[184:187]\n"
"v_exp_f32 v176, v176\n"
"v_mfma_f32_16x16x32_bf16 v[160:163], v[220:223], a[28:31], v[160:163]\n"
"v_exp_f32 v152, v152\n"
"v_mfma_f32_16x16x32_bf16 v[160:163], v[208:211], a[16:19], v[160:163]\n"
"s_add_u32 s40, s56, s52\n"
"v_cvt_pk_bf16_f32 v146, v176, v177\n"
"v_mfma_f32_16x16x32_bf16 v[160:163], v[212:215], a[20:23], v[160:163]\n"
"buffer_load_dwordx4 v207, s[16:19], s40 offen offset:1024 nt lds\n"
"v_mfma_f32_16x16x32_bf16 v[164:167], v[208:211], a[32:35], v[188:191]\n"
"v_exp_f32 v170, v170\n"
"v_mfma_f32_16x16x32_bf16 v[164:167], v[212:215], a[36:39], v[164:167]\n"
"v_exp_f32 v173, v173\n"
"v_mfma_f32_16x16x32_bf16 v[164:167], v[216:219], a[40:43], v[164:167]\n"
"v_exp_f32 v153, v153\n"
"v_mfma_f32_16x16x32_bf16 v[164:167], v[220:223], a[44:47], v[164:167]\n"
"s_waitcnt lgkmcnt(0)\n"
"v_exp_f32 v174, v174\n"
"v_mfma_f32_16x16x32_bf16 a[208:211], v[120:123], a[244:247], a[208:211]\n"
"ds_read_b128 v[208:211], v230 offset:4096\n"
"v_cvt_pk_bf16_f32 v139, v174, v175\n"
"v_mfma_f32_16x16x32_bf16 v[0:3], v[96:99], a[244:247], v[0:3]\n"
"v_exp_f32 v156, v156\n"
"v_mfma_f32_16x16x32_bf16 v[0:3], v[100:103], a[248:251], v[0:3]\n"
"ds_read_b128 v[220:223], v231 offset:4608\n"
"v_cvt_pk_bf16_f32 v138, v172, v173\n"
"v_mfma_f32_16x16x32_bf16 v[32:35], v[104:107], a[244:247], v[32:35]\n"
"v_exp_f32 v161, v161\n"
"v_mfma_f32_16x16x32_bf16 v[32:35], v[108:111], a[248:251], v[32:35]\n"
"ds_read_b128 v[216:219], v230 offset:4608\n"
"v_cvt_pk_bf16_f32 v126, v152, v153\n"
"v_mfma_f32_16x16x32_bf16 v[64:67], v[112:115], a[244:247], v[64:67]\n"
"s_add_u32 s40, s56, s53\n"
"ds_read_b128 v[212:215], v231 offset:4096\n"
"v_mfma_f32_16x16x32_bf16 v[64:67], v[116:119], a[248:251], v[64:67]\n"
"buffer_load_dwordx4 v240, s[16:19], s40 offen offset:2048 nt lds\n"
"v_mfma_f32_16x16x32_bf16 v[68:71], v[116:119], v[224:227], v[68:71]\n"
"v_exp_f32 v159, v159\n"
"v_mfma_f32_16x16x32_bf16 v[68:71], v[112:115], a[252:255], v[68:71]\n"
"v_exp_f32 v155, v155\n"
"v_mfma_f32_16x16x32_bf16 v[36:39], v[108:111], v[224:227], v[36:39]\n"
"v_exp_f32 v160, v160\n"
"v_mfma_f32_16x16x32_bf16 v[36:39], v[104:107], a[252:255], v[36:39]\n"
"v_exp_f32 v179, v179\n"
"v_mfma_f32_16x16x32_bf16 v[4:7], v[100:103], v[224:227], v[4:7]\n"
"ds_read_b64_tr_b16 a[244:245], v232 offset:49664\n"
"ds_read_b64_tr_b16 v[224:225], v237 offset:49664\n"
"v_mfma_f32_16x16x32_bf16 v[4:7], v[96:99], a[252:255], v[4:7]\n"
"v_exp_f32 v158, v158\n"
"v_mfma_f32_16x16x32_bf16 a[212:215], v[120:123], a[252:255], a[212:215]\n"
"ds_read_b64_tr_b16 a[246:247], v232 offset:53760\n"
"ds_read_b64_tr_b16 a[250:251], v236 offset:53760\n"
"v_smfmac_f32_16x16x64_bf16 v[200:203], a[240:243], v[112:119], v241\n"
"s_waitcnt lgkmcnt(4)\n"
"ds_read_b64_tr_b16 v[226:227], v237 offset:53760\n"
"v_cvt_pk_bf16_f32 v131, v170, v171\n"
"v_mfma_f32_16x16x32_bf16 v[168:171], v[208:211], a[0:3], v[180:183]\n"
"v_exp_f32 v164, v164\n"
"v_mfma_f32_16x16x32_bf16 v[168:171], v[212:215], a[4:7], v[168:171]\n"
"ds_read_b64_tr_b16 a[248:249], v236 offset:49664\n"
"ds_read_b64_tr_b16 a[254:255], v233 offset:53760\n"
"v_mfma_f32_16x16x32_bf16 v[168:171], v[216:219], a[8:11], v[168:171]\n"
"s_add_u32 s40, s56, s54\n"
"ds_read_b64_tr_b16 a[252:253], v233 offset:49664\n"
"v_mfma_f32_16x16x32_bf16 v[168:171], v[220:223], a[12:15], v[168:171]\n"
"buffer_load_dwordx4 v207, s[16:19], s40 offen offset:3072 nt lds\n"
"v_mfma_f32_16x16x32_bf16 v[172:175], v[216:219], a[24:27], v[184:187]\n"
"v_exp_f32 v154, v154\n"
"v_mfma_f32_16x16x32_bf16 v[172:175], v[220:223], a[28:31], v[172:175]\n"
"v_exp_f32 v167, v167\n"
"v_mfma_f32_16x16x32_bf16 v[172:175], v[208:211], a[16:19], v[172:175]\n"
"v_exp_f32 v163, v163\n"
"v_mfma_f32_16x16x32_bf16 v[172:175], v[212:215], a[20:23], v[172:175]\n"
"s_cbranch_vccnz LPCL0_%=\nLPKL0_%=:\n"
"v_cvt_pk_bf16_f32 v147, v178, v179\n"
"v_mfma_f32_16x16x32_bf16 v[176:179], v[208:211], a[32:35], v[188:191]\n"
"v_exp_f32 v165, v165\n"
"v_mfma_f32_16x16x32_bf16 v[176:179], v[212:215], a[36:39], v[176:179]\n"
"v_exp_f32 v157, v157\n"
"v_mfma_f32_16x16x32_bf16 v[176:179], v[216:219], a[40:43], v[176:179]\n"
"v_exp_f32 v166, v166\n"
"v_mfma_f32_16x16x32_bf16 v[176:179], v[220:223], a[44:47], v[176:179]\n"
"s_waitcnt lgkmcnt(0)\n"
"v_mfma_f32_16x16x32_bf16 a[224:227], v[120:123], a[244:247], a[224:227]\n"
"v_exp_f32 v162, v162\n"
"v_mfma_f32_16x16x32_bf16 v[16:19], v[96:99], a[244:247], v[16:19]\n"
"s_waitcnt vmcnt(8)\n"
"v_exp_f32 v168, v168\n"
"v_mfma_f32_16x16x32_bf16 v[16:19], v[100:103], a[248:251], v[16:19]\n"
"s_barrier\n"
"v_mfma_f32_16x16x32_bf16 v[48:51], v[104:107], a[244:247], v[48:51]\n"
"ds_read_b128 v[208:211], v228 offset:16384\n"
"v_cvt_pk_bf16_f32 v133, v158, v159\n"
"v_mfma_f32_16x16x32_bf16 v[48:51], v[108:111], a[248:251], v[48:51]\n"
"ds_read_b128 v[216:219], v228 offset:16896\n"
"v_cvt_pk_bf16_f32 v141, v162, v163\n"
"v_mfma_f32_16x16x32_bf16 v[80:83], v[112:115], a[244:247], v[80:83]\n"
"ds_read_b128 v[220:223], v229 offset:16896\n"
"v_cvt_pk_bf16_f32 v127, v154, v155\n"
"v_mfma_f32_16x16x32_bf16 v[80:83], v[116:119], a[248:251], v[80:83]\n"
"ds_read_b128 v[212:215], v229 offset:16384\n"
"v_cvt_pk_bf16_f32 v149, v166, v167\n"
"v_mfma_f32_16x16x32_bf16 v[84:87], v[116:119], v[224:227], v[84:87]\n"
"v_exp_f32 v176, v176\n"
"v_mfma_f32_16x16x32_bf16 v[84:87], v[112:115], a[252:255], v[84:87]\n"
"v_exp_f32 v169, v169\n"
"v_mfma_f32_16x16x32_bf16 v[52:55], v[108:111], v[224:227], v[52:55]\n"
"ds_read_b64_tr_b16 a[246:247], v234 offset:4096\n"
"v_cvt_pk_bf16_f32 v148, v164, v165\n"
"v_mfma_f32_16x16x32_bf16 v[52:55], v[104:107], a[252:255], v[52:55]\n"
"v_exp_f32 v177, v177\n"
"v_mfma_f32_16x16x32_bf16 v[20:23], v[100:103], v[224:227], v[20:23]\n"
"ds_read_b64_tr_b16 a[250:251], v238 offset:4096\n"
"v_cvt_pk_bf16_f32 v150, v176, v177\n"
"v_mfma_f32_16x16x32_bf16 v[20:23], v[96:99], a[252:255], v[20:23]\n"
"v_exp_f32 v174, v174\n"
"v_mfma_f32_16x16x32_bf16 a[228:231], v[120:123], a[252:255], a[228:231]\n"
"s_waitcnt lgkmcnt(3)\n"
"ds_read_b64_tr_b16 a[254:255], v235 offset:4096\n"
"v_cvt_pk_bf16_f32 v132, v156, v157\n"
"v_mfma_f32_16x16x32_bf16 v[152:155], v[208:211], a[0:3], v[180:183]\n"
"s_waitcnt lgkmcnt(3)\n"
"ds_read_b64_tr_b16 a[248:249], v238 offset:0\n"
"v_cvt_pk_bf16_f32 v134, v168, v169\n"
"v_mfma_f32_16x16x32_bf16 v[152:155], v[212:215], a[4:7], v[152:155]\n"
"v_exp_f32 v172, v172\n"
"v_mfma_f32_16x16x32_bf16 v[152:155], v[216:219], a[8:11], v[152:155]\n"
"ds_read_b64_tr_b16 a[244:245], v234 offset:0\n"
"v_cvt_pk_bf16_f32 v140, v160, v161\n"
"v_mfma_f32_16x16x32_bf16 v[152:155], v[220:223], a[12:15], v[152:155]\n"
"v_exp_f32 v175, v175\n"
"v_mfma_f32_16x16x32_bf16 v[156:159], v[216:219], a[24:27], v[184:187]\n"
"ds_read_b64_tr_b16 a[252:253], v235 offset:0\n"
"v_cvt_pk_bf16_f32 v143, v174, v175\n"
"v_mfma_f32_16x16x32_bf16 v[156:159], v[220:223], a[28:31], v[156:159]\n"
"ds_read_b64_tr_b16 v[224:225], v239 offset:0\n"
"v_mfma_f32_16x16x32_bf16 v[156:159], v[208:211], a[16:19], v[156:159]\n"
"s_add_u32 m0, s44, 98304\n"
"ds_read_b64_tr_b16 v[226:227], v239 offset:4096\n"
"v_mfma_f32_16x16x32_bf16 v[156:159], v[212:215], a[20:23], v[156:159]\n"
"buffer_load_dwordx4 v240, s[20:23], s56 offen nt lds\n"
"v_mfma_f32_16x16x32_bf16 v[160:163], v[208:211], a[32:35], v[188:191]\n"
"v_exp_f32 v178, v178\n"
"v_mfma_f32_16x16x32_bf16 v[160:163], v[212:215], a[36:39], v[160:163]\n"
"v_exp_f32 v171, v171\n"
"v_mfma_f32_16x16x32_bf16 v[160:163], v[216:219], a[40:43], v[160:163]\n"
"s_add_u32 s40, s56, s52\n"
"v_mfma_f32_16x16x32_bf16 v[160:163], v[220:223], a[44:47], v[160:163]\n"
"buffer_load_dwordx4 v207, s[20:23], s40 offen offset:1024 nt lds\n"
"v_mfma_f32_16x16x32_bf16 v[164:167], v[216:219], a[200:203], v[192:195]\n"
"v_exp_f32 v170, v170\n"
"v_mfma_f32_16x16x32_bf16 v[164:167], v[220:223], a[204:207], v[164:167]\n"
"v_exp_f32 v153, v153\n"
"v_mfma_f32_16x16x32_bf16 v[164:167], v[208:211], a[192:195], v[164:167]\n"
"v_exp_f32 v179, v179\n"
"v_mfma_f32_16x16x32_bf16 v[164:167], v[212:215], a[196:199], v[164:167]\n"
"s_waitcnt lgkmcnt(0)\n"
"v_exp_f32 v158, v158\n"
"v_mfma_f32_16x16x32_bf16 a[216:219], v[124:127], a[244:247], a[216:219]\n"
"ds_read_b128 v[216:219], v228 offset:20992\n"
"v_cvt_pk_bf16_f32 v135, v170, v171\n"
"v_mfma_f32_16x16x32_bf16 v[8:11], v[128:131], a[244:247], v[8:11]\n"
"v_exp_f32 v173, v173\n"
"v_mfma_f32_16x16x32_bf16 v[8:11], v[132:135], a[248:251], v[8:11]\n"
"ds_read_b128 v[208:211], v228 offset:20480\n"
"v_cvt_pk_bf16_f32 v142, v172, v173\n"
"v_mfma_f32_16x16x32_bf16 v[40:43], v[136:139], a[244:247], v[40:43]\n"
"v_exp_f32 v162, v162\n"
"v_mfma_f32_16x16x32_bf16 v[40:43], v[140:143], a[248:251], v[40:43]\n"
"ds_read_b128 v[220:223], v229 offset:20992\n"
"v_cvt_pk_bf16_f32 v151, v178, v179\n"
"v_mfma_f32_16x16x32_bf16 v[72:75], v[144:147], a[244:247], v[72:75]\n"
"s_add_u32 s40, s56, s53\n"
"ds_read_b128 v[212:215], v229 offset:20480\n"
"v_mfma_f32_16x16x32_bf16 v[72:75], v[148:151], a[248:251], v[72:75]\n"
"buffer_load_dwordx4 v240, s[20:23], s40 offen offset:2048 nt lds\n"
"v_mfma_f32_16x16x32_bf16 v[76:79], v[148:151], v[224:227], v[76:79]\n"
"v_exp_f32 v163, v163\n"
"v_mfma_f32_16x16x32_bf16 v[76:79], v[144:147], a[252:255], v[76:79]\n"
"v_exp_f32 v156, v156\n"
"v_mfma_f32_16x16x32_bf16 v[44:47], v[140:143], v[224:227], v[44:47]\n"
"v_exp_f32 v160, v160\n"
"v_mfma_f32_16x16x32_bf16 v[44:47], v[136:139], a[252:255], v[44:47]\n"
"v_exp_f32 v155, v155\n"
"v_mfma_f32_16x16x32_bf16 v[12:15], v[132:135], v[224:227], v[12:15]\n"
"ds_read_b64_tr_b16 v[226:227], v239 offset:4608\n"
"v_mfma_f32_16x16x32_bf16 v[12:15], v[128:131], a[252:255], v[12:15]\n"
"ds_read_b64_tr_b16 a[248:249], v238 offset:512\n"
"v_mfma_f32_16x16x32_bf16 a[220:223], v[124:127], a[252:255], a[220:223]\n"
"v_exp_f32 v154, v154\n"
"v_smfmac_f32_16x16x64_bf16 v[196:199], a[240:243], v[128:135], v241\n"
"ds_read_b64_tr_b16 a[250:251], v238 offset:4608\n"
"v_mfma_f32_16x16x32_bf16 v[202:205], a[240:243], v[124:127], v[202:205]\n"
"s_waitcnt lgkmcnt(3)\n"
"v_exp_f32 v152, v152\n"
"v_mfma_f32_16x16x32_bf16 v[168:171], v[216:219], a[8:11], v[180:183]\n"
"ds_read_b64_tr_b16 a[244:245], v234 offset:512\n"
"v_cvt_pk_bf16_f32 v96, v152, v153\n"
"v_mfma_f32_16x16x32_bf16 v[168:171], v[220:223], a[12:15], v[168:171]\n"
"v_exp_f32 v165, v165\n"
"v_mfma_f32_16x16x32_bf16 v[168:171], v[208:211], a[0:3], v[168:171]\n"
"ds_read_b64_tr_b16 a[246:247], v234 offset:4608\n"
"v_cvt_pk_bf16_f32 v113, v162, v163\n"
"v_mfma_f32_16x16x32_bf16 v[168:171], v[212:215], a[4:7], v[168:171]\n"
"ds_read_b64_tr_b16 a[254:255], v235 offset:4608\n"
"v_mfma_f32_16x16x32_bf16 v[172:175], v[208:211], a[16:19], v[184:187]\n"
"ds_read_b64_tr_b16 v[224:225], v239 offset:512\n"
"v_cvt_pk_bf16_f32 v97, v154, v155\n"
"v_mfma_f32_16x16x32_bf16 v[172:175], v[212:215], a[20:23], v[172:175]\n"
"v_mfma_f32_16x16x32_bf16 v[172:175], v[216:219], a[24:27], v[172:175]\n"
"s_add_u32 s40, s56, s54\n"
"ds_read_b64_tr_b16 a[252:253], v235 offset:512\n"
"v_mfma_f32_16x16x32_bf16 v[172:175], v[220:223], a[28:31], v[172:175]\n"
"buffer_load_dwordx4 v207, s[20:23], s40 offen offset:3072 nt lds\n"
"v_mfma_f32_16x16x32_bf16 v[176:179], v[216:219], a[40:43], v[188:191]\n"
"s_add_u32 s55, s55, s59\n"
"v_exp_f32 v157, v157\n"
"v_mfma_f32_16x16x32_bf16 v[176:179], v[220:223], a[44:47], v[176:179]\n"
"s_or_b32 s55, s55, s58\n"
"v_exp_f32 v171, v171\n"
"v_mfma_f32_16x16x32_bf16 v[176:179], v[208:211], a[32:35], v[176:179]\n"
"s_xor_b32 s56, s55, s57\n"
"v_exp_f32 v159, v159\n"
"v_mfma_f32_16x16x32_bf16 v[176:179], v[212:215], a[36:39], v[176:179]\n"
"v_exp_f32 v167, v167\n"
"v_mfma_f32_16x16x32_bf16 v[152:155], v[208:211], a[192:195], v[192:195]\n"
"v_cvt_pk_bf16_f32 v105, v158, v159\n"
"v_mfma_f32_16x16x32_bf16 v[152:155], v[212:215], a[196:199], v[152:155]\n"
"ds_read_b128 v[208:211], v230 offset:16384\n"
"v_cvt_pk_bf16_f32 v104, v156, v157\n"
"v_mfma_f32_16x16x32_bf16 v[152:155], v[216:219], a[200:203], v[152:155]\n"
"v_exp_f32 v166, v166\n"
"v_mfma_f32_16x16x32_bf16 v[152:155], v[220:223], a[204:207], v[152:155]\n"
"s_waitcnt lgkmcnt(1)\n"
"ds_read_b128 v[216:219], v230 offset:16896\n"
"v_mfma_f32_16x16x32_bf16 a[232:235], v[124:127], a[244:247], a[232:235]\n"
"v_exp_f32 v169, v169\n"
"v_mfma_f32_16x16x32_bf16 v[24:27], v[128:131], a[244:247], v[24:27]\n"
"ds_read_b128 v[220:223], v231 offset:16896\n"
"v_mfma_f32_16x16x32_bf16 v[24:27], v[132:135], a[248:251], v[24:27]\n"
"v_exp_f32 v164, v164\n"
"v_mfma_f32_16x16x32_bf16 v[56:59], v[136:139], a[244:247], v[56:59]\n"
"ds_read_b128 v[212:215], v231 offset:16384\n"
"v_mfma_f32_16x16x32_bf16 v[56:59], v[140:143], a[248:251], v[56:59]\n"
"v_exp_f32 v178, v178\n"
"v_mfma_f32_16x16x32_bf16 v[88:91], v[144:147], a[244:247], v[88:91]\n"
"s_add_u32 m0, s44, 49152\n"
"ds_read_b64_tr_b16 a[246:247], v232 offset:4096\n"
"v_mfma_f32_16x16x32_bf16 v[88:91], v[148:151], a[248:251], v[88:91]\n"
"buffer_load_dwordx4 v240, s[16:19], s56 offen nt lds\n"
"v_mfma_f32_16x16x32_bf16 v[92:95], v[148:151], v[224:227], v[92:95]\n"
"v_exp_f32 v175, v175\n"
"v_mfma_f32_16x16x32_bf16 v[92:95], v[144:147], a[252:255], v[92:95]\n"
"v_exp_f32 v168, v168\n"
"v_mfma_f32_16x16x32_bf16 v[60:63], v[140:143], v[224:227], v[60:63]\n"
"v_exp_f32 v161, v161\n"
"v_mfma_f32_16x16x32_bf16 v[60:63], v[136:139], a[252:255], v[60:63]\n"
"v_exp_f32 v177, v177\n"
"v_mfma_f32_16x16x32_bf16 v[28:31], v[132:135], v[224:227], v[28:31]\n"
"ds_read_b64_tr_b16 v[226:227], v237 offset:4096\n"
"v_mfma_f32_16x16x32_bf16 v[28:31], v[128:131], a[252:255], v[28:31]\n"
"v_exp_f32 v172, v172\n"
"v_mfma_f32_16x16x32_bf16 a[236:239], v[124:127], a[252:255], a[236:239]\n"
"ds_read_b64_tr_b16 a[250:251], v236 offset:4096\n"
"v_cvt_pk_bf16_f32 v120, v164, v165\n"
"v_smfmac_f32_16x16x64_bf16 v[198:201], a[240:243], v[136:143], v241\n"
"s_waitcnt lgkmcnt(2)\n"
"ds_read_b64_tr_b16 a[252:253], v233 offset:0\n"
"v_mfma_f32_16x16x32_bf16 v[156:159], v[208:211], a[0:3], v[180:183]\n"
"ds_read_b64_tr_b16 a[248:249], v236 offset:0\n"
"v_cvt_pk_bf16_f32 v121, v166, v167\n"
"v_mfma_f32_16x16x32_bf16 v[156:159], v[212:215], a[4:7], v[156:159]\n"
"ds_read_b64_tr_b16 v[224:225], v237 offset:0\n"
"v_cvt_pk_bf16_f32 v98, v168, v169\n"
"v_mfma_f32_16x16x32_bf16 v[156:159], v[216:219], a[8:11], v[156:159]\n"
"ds_read_b64_tr_b16 a[254:255], v233 offset:4096\n"
"v_cvt_pk_bf16_f32 v112, v160, v161\n"
"v_mfma_f32_16x16x32_bf16 v[156:159], v[220:223], a[12:15], v[156:159]\n"
"ds_read_b64_tr_b16 a[244:245], v232 offset:0\n"
"v_mfma_f32_16x16x32_bf16 v[160:163], v[216:219], a[24:27], v[184:187]\n"
"v_exp_f32 v176, v176\n"
"v_mfma_f32_16x16x32_bf16 v[160:163], v[220:223], a[28:31], v[160:163]\n"
"v_exp_f32 v152, v152\n"
"v_mfma_f32_16x16x32_bf16 v[160:163], v[208:211], a[16:19], v[160:163]\n"
"s_add_u32 s40, s56, s52\n"
"v_cvt_pk_bf16_f32 v114, v176, v177\n"
"v_mfma_f32_16x16x32_bf16 v[160:163], v[212:215], a[20:23], v[160:163]\n"
"buffer_load_dwordx4 v207, s[16:19], s40 offen offset:1024 nt lds\n"
"v_mfma_f32_16x16x32_bf16 v[164:167], v[208:211], a[32:35], v[188:191]\n"
"v_exp_f32 v170, v170\n"
"v_mfma_f32_16x16x32_bf16 v[164:167], v[212:215], a[36:39], v[164:167]\n"
"v_exp_f32 v173, v173\n"
"v_mfma_f32_16x16x32_bf16 v[164:167], v[216:219], a[40:43], v[164:167]\n"
"v_exp_f32 v153, v153\n"
"v_mfma_f32_16x16x32_bf16 v[164:167], v[220:223], a[44:47], v[164:167]\n"
"s_waitcnt lgkmcnt(0)\n"
"v_exp_f32 v174, v174\n"
"v_mfma_f32_16x16x32_bf16 a[208:211], v[124:127], a[244:247], a[208:211]\n"
"ds_read_b128 v[208:211], v230 offset:20480\n"
"v_cvt_pk_bf16_f32 v107, v174, v175\n"
"v_mfma_f32_16x16x32_bf16 v[0:3], v[128:131], a[244:247], v[0:3]\n"
"v_exp_f32 v156, v156\n"
"v_mfma_f32_16x16x32_bf16 v[0:3], v[132:135], a[248:251], v[0:3]\n"
"ds_read_b128 v[220:223], v231 offset:20992\n"
"v_cvt_pk_bf16_f32 v106, v172, v173\n"
"v_mfma_f32_16x16x32_bf16 v[32:35], v[136:139], a[244:247], v[32:35]\n"
"v_exp_f32 v161, v161\n"
"v_mfma_f32_16x16x32_bf16 v[32:35], v[140:143], a[248:251], v[32:35]\n"
"ds_read_b128 v[216:219], v230 offset:20992\n"
"v_cvt_pk_bf16_f32 v122, v152, v153\n"
"v_mfma_f32_16x16x32_bf16 v[64:67], v[144:147], a[244:247], v[64:67]\n"
"s_add_u32 s40, s56, s53\n"
"ds_read_b128 v[212:215], v231 offset:20480\n"
"v_mfma_f32_16x16x32_bf16 v[64:67], v[148:151], a[248:251], v[64:67]\n"
"buffer_load_dwordx4 v240, s[16:19], s40 offen offset:2048 nt lds\n"
"v_mfma_f32_16x16x32_bf16 v[68:71], v[148:151], v[224:227], v[68:71]\n"
"v_exp_f32 v159, v159\n"
"v_mfma_f32_16x16x32_bf16 v[68:71], v[144:147], a[252:255], v[68:71]\n"
"v_exp_f32 v155, v155\n"
"v_mfma_f32_16x16x32_bf16 v[36:39], v[140:143], v[224:227], v[36:39]\n"
"v_exp_f32 v160, v160\n"
"v_mfma_f32_16x16x32_bf16 v[36:39], v[136:139], a[252:255], v[36:39]\n"
"v_exp_f32 v179, v179\n"
"v_mfma_f32_16x16x32_bf16 v[4:7], v[132:135], v[224:227], v[4:7]\n"
"ds_read_b64_tr_b16 a[244:245], v232 offset:512\n"
"ds_read_b64_tr_b16 v[224:225], v237 offset:512\n"
"v_mfma_f32_16x16x32_bf16 v[4:7], v[128:131], a[252:255], v[4:7]\n"
"v_exp_f32 v158, v158\n"
"v_mfma_f32_16x16x32_bf16 a[212:215], v[124:127], a[252:255], a[212:215]\n"
"ds_read_b64_tr_b16 a[246:247], v232 offset:4608\n"
"ds_read_b64_tr_b16 a[250:251], v236 offset:4608\n"
"v_smfmac_f32_16x16x64_bf16 v[200:203], a[240:243], v[144:151], v241\n"
"s_waitcnt lgkmcnt(4)\n"
"ds_read_b64_tr_b16 v[226:227], v237 offset:4608\n"
"v_cvt_pk_bf16_f32 v99, v170, v171\n"
"v_mfma_f32_16x16x32_bf16 v[168:171], v[208:211], a[0:3], v[180:183]\n"
"v_exp_f32 v164, v164\n"
"v_mfma_f32_16x16x32_bf16 v[168:171], v[212:215], a[4:7], v[168:171]\n"
"ds_read_b64_tr_b16 a[248:249], v236 offset:512\n"
"ds_read_b64_tr_b16 a[254:255], v233 offset:4608\n"
"v_mfma_f32_16x16x32_bf16 v[168:171], v[216:219], a[8:11], v[168:171]\n"
"s_add_u32 s40, s56, s54\n"
"ds_read_b64_tr_b16 a[252:253], v233 offset:512\n"
"v_mfma_f32_16x16x32_bf16 v[168:171], v[220:223], a[12:15], v[168:171]\n"
"buffer_load_dwordx4 v207, s[16:19], s40 offen offset:3072 nt lds\n"
"v_mfma_f32_16x16x32_bf16 v[172:175], v[216:219], a[24:27], v[184:187]\n"
"v_exp_f32 v154, v154\n"
"v_mfma_f32_16x16x32_bf16 v[172:175], v[220:223], a[28:31], v[172:175]\n"
"v_exp_f32 v167, v167\n"
"v_mfma_f32_16x16x32_bf16 v[172:175], v[208:211], a[16:19], v[172:175]\n"
"v_exp_f32 v163, v163\n"
"v_mfma_f32_16x16x32_bf16 v[172:175], v[212:215], a[20:23], v[172:175]\n"
"s_cbranch_vccnz LPCL1_%=\nLPKL1_%=:\n"
"v_cvt_pk_bf16_f32 v115, v178, v179\n"
"v_mfma_f32_16x16x32_bf16 v[176:179], v[208:211], a[32:35], v[188:191]\n"
"v_exp_f32 v165, v165\n"
"v_mfma_f32_16x16x32_bf16 v[176:179], v[212:215], a[36:39], v[176:179]\n"
"v_exp_f32 v157, v157\n"
"v_mfma_f32_16x16x32_bf16 v[176:179], v[216:219], a[40:43], v[176:179]\n"
"v_exp_f32 v166, v166\n"
"v_mfma_f32_16x16x32_bf16 v[176:179], v[220:223], a[44:47], v[176:179]\n"
"s_waitcnt lgkmcnt(0)\n"
"v_mfma_f32_16x16x32_bf16 a[224:227], v[124:127], a[244:247], a[224:227]\n"
"v_exp_f32 v162, v162\n"
"v_mfma_f32_16x16x32_bf16 v[16:19], v[128:131], a[244:247], v[16:19]\n"
"s_waitcnt vmcnt(8)\n"
"v_exp_f32 v168, v168\n"
"v_mfma_f32_16x16x32_bf16 v[16:19], v[132:135], a[248:251], v[16:19]\n"
"s_barrier\n"
"v_mfma_f32_16x16x32_bf16 v[48:51], v[136:139], a[244:247], v[48:51]\n"
"ds_read_b128 v[208:211], v228 offset:32768\n"
"v_cvt_pk_bf16_f32 v101, v158, v159\n"
"v_mfma_f32_16x16x32_bf16 v[48:51], v[140:143], a[248:251], v[48:51]\n"
"ds_read_b128 v[216:219], v228 offset:33280\n"
"v_cvt_pk_bf16_f32 v109, v162, v163\n"
"v_mfma_f32_16x16x32_bf16 v[80:83], v[144:147], a[244:247], v[80:83]\n"
"ds_read_b128 v[220:223], v229 offset:33280\n"
"v_cvt_pk_bf16_f32 v123, v154, v155\n"
"v_mfma_f32_16x16x32_bf16 v[80:83], v[148:151], a[248:251], v[80:83]\n"
"ds_read_b128 v[212:215], v229 offset:32768\n"
"v_cvt_pk_bf16_f32 v117, v166, v167\n"
"v_mfma_f32_16x16x32_bf16 v[84:87], v[148:151], v[224:227], v[84:87]\n"
"v_exp_f32 v176, v176\n"
"v_mfma_f32_16x16x32_bf16 v[84:87], v[144:147], a[252:255], v[84:87]\n"
"v_exp_f32 v169, v169\n"
"v_mfma_f32_16x16x32_bf16 v[52:55], v[140:143], v[224:227], v[52:55]\n"
"ds_read_b64_tr_b16 a[246:247], v234 offset:20480\n"
"v_cvt_pk_bf16_f32 v116, v164, v165\n"
"v_mfma_f32_16x16x32_bf16 v[52:55], v[136:139], a[252:255], v[52:55]\n"
"v_exp_f32 v177, v177\n"
"v_mfma_f32_16x16x32_bf16 v[20:23], v[132:135], v[224:227], v[20:23]\n"
"ds_read_b64_tr_b16 a[250:251], v238 offset:20480\n"
"v_cvt_pk_bf16_f32 v118, v176, v177\n"
"v_mfma_f32_16x16x32_bf16 v[20:23], v[128:131], a[252:255], v[20:23]\n"
"v_exp_f32 v174, v174\n"
"v_mfma_f32_16x16x32_bf16 a[228:231], v[124:127], a[252:255], a[228:231]\n"
"s_waitcnt lgkmcnt(3)\n"
"ds_read_b64_tr_b16 a[254:255], v235 offset:20480\n"
"v_cvt_pk_bf16_f32 v100, v156, v157\n"
"LAY2_%=:\n"
"v_mfma_f32_16x16x32_bf16 v[152:155], v[208:211], a[0:3], v[180:183]\n"
"s_waitcnt lgkmcnt(3)\n"
"ds_read_b64_tr_b16 a[248:249], v238 offset:16384\n"
"v_cvt_pk_bf16_f32 v102, v168, v169\n"
"v_mfma_f32_16x16x32_bf16 v[152:155], v[212:215], a[4:7], v[152:155]\n"
"v_exp_f32 v172, v172\n"
"v_mfma_f32_16x16x32_bf16 v[152:155], v[216:219], a[8:11], v[152:155]\n"
"ds_read_b64_tr_b16 a[244:245], v234 offset:16384\n"
"v_cvt_pk_bf16_f32 v108, v160, v161\n"
"v_mfma_f32_16x16x32_bf16 v[152:155], v[220:223], a[12:15], v[152:155]\n"
"v_exp_f32 v175, v175\n"
"v_mfma_f32_16x16x32_bf16 v[156:159], v[216:219], a[24:27], v[184:187]\n"
"ds_read_b64_tr_b16 a[252:253], v235 offset:16384\n"
"v_cvt_pk_bf16_f32 v111, v174, v175\n"
"v_mfma_f32_16x16x32_bf16 v[156:159], v[220:223], a[28:31], v[156:159]\n"
"ds_read_b64_tr_b16 v[224:225], v239 offset:16384\n"
"v_mfma_f32_16x16x32_bf16 v[156:159], v[208:211], a[16:19], v[156:159]\n"
"s_add_u32 m0, s44, 114688\n"
"ds_read_b64_tr_b16 v[226:227], v239 offset:20480\n"
"v_mfma_f32_16x16x32_bf16 v[156:159], v[212:215], a[20:23], v[156:159]\n"
"buffer_load_dwordx4 v240, s[20:23], s56 offen nt lds\n"
"v_mfma_f32_16x16x32_bf16 v[160:163], v[208:211], a[32:35], v[188:191]\n"
"v_exp_f32 v178, v178\n"
"v_mfma_f32_16x16x32_bf16 v[160:163], v[212:215], a[36:39], v[160:163]\n"
"v_exp_f32 v171, v171\n"
"v_mfma_f32_16x16x32_bf16 v[160:163], v[216:219], a[40:43], v[160:163]\n"
"s_add_u32 s40, s56, s52\n"
"v_mfma_f32_16x16x32_bf16 v[160:163], v[220:223], a[44:47], v[160:163]\n"
"buffer_load_dwordx4 v207, s[20:23], s40 offen offset:1024 nt lds\n"
"v_mfma_f32_16x16x32_bf16 v[164:167], v[216:219], a[200:203], v[192:195]\n"
"v_exp_f32 v170, v170\n"
"v_mfma_f32_16x16x32_bf16 v[164:167], v[220:223], a[204:207], v[164:167]\n"
"v_exp_f32 v153, v153\n"
"v_mfma_f32_16x16x32_bf16 v[164:167], v[208:211], a[192:195], v[164:167]\n"
"v_exp_f32 v179, v179\n"
"v_mfma_f32_16x16x32_bf16 v[164:167], v[212:215], a[196:199], v[164:167]\n"
"s_waitcnt lgkmcnt(0)\n"
"v_exp_f32 v158, v158\n"
"v_mfma_f32_16x16x32_bf16 a[216:219], v[120:123], a[244:247], a[216:219]\n"
"ds_read_b128 v[216:219], v228 offset:37376\n"
"v_cvt_pk_bf16_f32 v103, v170, v171\n"
"v_mfma_f32_16x16x32_bf16 v[8:11], v[96:99], a[244:247], v[8:11]\n"
"v_exp_f32 v173, v173\n"
"v_mfma_f32_16x16x32_bf16 v[8:11], v[100:103], a[248:251], v[8:11]\n"
"ds_read_b128 v[208:211], v228 offset:36864\n"
"v_cvt_pk_bf16_f32 v110, v172, v173\n"
"v_mfma_f32_16x16x32_bf16 v[40:43], v[104:107], a[244:247], v[40:43]\n"
"v_exp_f32 v162, v162\n"
"v_mfma_f32_16x16x32_bf16 v[40:43], v[108:111], a[248:251], v[40:43]\n"
"ds_read_b128 v[220:223], v229 offset:37376\n"
"v_cvt_pk_bf16_f32 v119, v178, v179\n"
"v_mfma_f32_16x16x32_bf16 v[72:75], v[112:115], a[244:247], v[72:75]\n"
"s_add_u32 s40, s56, s53\n"
"ds_read_b128 v[212:215], v229 offset:36864\n"
"v_mfma_f32_16x16x32_bf16 v[72:75], v[116:119], a[248:251], v[72:75]\n"
"buffer_load_dwordx4 v240, s[20:23], s40 offen offset:2048 nt lds\n"
"v_mfma_f32_16x16x32_bf16 v[76:79], v[116:119], v[224:227], v[76:79]\n"
"v_exp_f32 v163, v163\n"
"v_mfma_f32_16x16x32_bf16 v[76:79], v[112:115], a[252:255], v[76:79]\n"
"v_exp_f32 v156, v156\n"
"v_mfma_f32_16x16x32_bf16 v[44:47], v[108:111], v[224:227], v[44:47]\n"
"v_exp_f32 v160, v160\n"
"v_mfma_f32_16x16x32_bf16 v[44:47], v[104:107], a[252:255], v[44:47]\n"
"v_exp_f32 v155, v155\n"
"v_mfma_f32_16x16x32_bf16 v[12:15], v[100:103], v[224:227], v[12:15]\n"
"ds_read_b64_tr_b16 v[226:227], v239 offset:20992\n"
"v_mfma_f32_16x16x32_bf16 v[12:15], v[96:99], a[252:255], v[12:15]\n"
"ds_read_b64_tr_b16 a[248:249], v238 offset:16896\n"
"v_mfma_f32_16x16x32_bf16 a[220:223], v[120:123], a[252:255], a[220:223]\n"
"v_exp_f32 v154, v154\n"
"v_smfmac_f32_16x16x64_bf16 v[196:199], a[240:243], v[96:103], v241\n"
"ds_read_b64_tr_b16 a[250:251], v238 offset:20992\n"
"v_mfma_f32_16x16x32_bf16 v[202:205], a[240:243], v[120:123], v[202:205]\n"
"s_waitcnt lgkmcnt(3)\n"
"v_exp_f32 v152, v152\n"
"v_mfma_f32_16x16x32_bf16 v[168:171], v[216:219], a[8:11], v[180:183]\n"
"ds_read_b64_tr_b16 a[244:245], v234 offset:16896\n"
"v_cvt_pk_bf16_f32 v128, v152, v153\n"
"v_mfma_f32_16x16x32_bf16 v[168:171], v[220:223], a[12:15], v[168:171]\n"
"v_exp_f32 v165, v165\n"
"v_mfma_f32_16x16x32_bf16 v[168:171], v[208:211], a[0:3], v[168:171]\n"
"ds_read_b64_tr_b16 a[246:247], v234 offset:20992\n"
"v_cvt_pk_bf16_f32 v145, v162, v163\n"
"v_mfma_f32_16x16x32_bf16 v[168:171], v[212:215], a[4:7], v[168:171]\n"
"ds_read_b64_tr_b16 a[254:255], v235 offset:20992\n"
"v_mfma_f32_16x16x32_bf16 v[172:175], v[208:211], a[16:19], v[184:187]\n"
"ds_read_b64_tr_b16 v[224:225], v239 offset:16896\n"
"v_cvt_pk_bf16_f32 v129, v154, v155\n"
"v_mfma_f32_16x16x32_bf16 v[172:175], v[212:215], a[20:23], v[172:175]\n"
"v_mfma_f32_16x16x32_bf16 v[172:175], v[216:219], a[24:27], v[172:175]\n"
"s_add_u32 s40, s56, s54\n"
"ds_read_b64_tr_b16 a[252:253], v235 offset:16896\n"
"v_mfma_f32_16x16x32_bf16 v[172:175], v[220:223], a[28:31], v[172:175]\n"
"buffer_load_dwordx4 v207, s[20:23], s40 offen offset:3072 nt lds\n"
"v_mfma_f32_16x16x32_bf16 v[176:179], v[216:219], a[40:43], v[188:191]\n"
"s_add_u32 s55, s55, s59\n"
"v_exp_f32 v157, v157\n"
"v_mfma_f32_16x16x32_bf16 v[176:179], v[220:223], a[44:47], v[176:179]\n"
"s_or_b32 s55, s55, s58\n"
"v_exp_f32 v171, v171\n"
"v_mfma_f32_16x16x32_bf16 v[176:179], v[208:211], a[32:35], v[176:179]\n"
"s_xor_b32 s56, s55, s57\n"
"v_exp_f32 v159, v159\n"
"v_mfma_f32_16x16x32_bf16 v[176:179], v[212:215], a[36:39], v[176:179]\n"
"s_cmp_ge_u32 s47, 15\n"
"v_exp_f32 v167, v167\n"
"v_mfma_f32_16x16x32_bf16 v[152:155], v[208:211], a[192:195], v[192:195]\n"
"s_cbranch_scc1 LSW_%=\nLSWK_%=:\n"
"v_cvt_pk_bf16_f32 v137, v158, v159\n"
"v_mfma_f32_16x16x32_bf16 v[152:155], v[212:215], a[196:199], v[152:155]\n"
"ds_read_b128 v[208:211], v230 offset:32768\n"
"v_cvt_pk_bf16_f32 v136, v156, v157\n"
"v_mfma_f32_16x16x32_bf16 v[152:155], v[216:219], a[200:203], v[152:155]\n"
"v_exp_f32 v166, v166\n"
"v_mfma_f32_16x16x32_bf16 v[152:155], v[220:223], a[204:207], v[152:155]\n"
"s_waitcnt lgkmcnt(1)\n"
"ds_read_b128 v[216:219], v230 offset:33280\n"
"v_mfma_f32_16x16x32_bf16 a[232:235], v[120:123], a[244:247], a[232:235]\n"
"v_exp_f32 v169, v169\n"
"v_mfma_f32_16x16x32_bf16 v[24:27], v[96:99], a[244:247], v[24:27]\n"
"ds_read_b128 v[220:223], v231 offset:33280\n"
"v_mfma_f32_16x16x32_bf16 v[24:27], v[100:103], a[248:251], v[24:27]\n"
"v_exp_f32 v164, v164\n"
"v_mfma_f32_16x16x32_bf16 v[56:59], v[104:107], a[244:247], v[56:59]\n"
"ds_read_b128 v[212:215], v231 offset:32768\n"
"v_mfma_f32_16x16x32_bf16 v[56:59], v[108:111], a[248:251], v[56:59]\n"
"v_exp_f32 v178, v178\n"
"v_mfma_f32_16x16x32_bf16 v[88:91], v[112:115], a[244:247], v[88:91]\n"
"s_mov_b32 m0, s44\n"
"ds_read_b64_tr_b16 a[246:247], v232 offset:20480\n"
"v_mfma_f32_16x16x32_bf16 v[88:91], v[116:119], a[248:251], v[88:91]\n"
"buffer_load_dwordx4 v240, s[16:19], s56 offen nt lds\n"
"v_mfma_f32_16x16x32_bf16 v[92:95], v[116:119], v[224:227], v[92:95]\n"
"v_exp_f32 v175, v175\n"
"v_mfma_f32_16x16x32_bf16 v[92:95], v[112:115], a[252:255], v[92:95]\n"
"v_exp_f32 v168, v168\n"
"v_mfma_f32_16x16x32_bf16 v[60:63], v[108:111], v[224:227], v[60:63]\n"
"v_exp_f32 v161, v161\n"
"v_mfma_f32_16x16x32_bf16 v[60:63], v[104:107], a[252:255], v[60:63]\n"
"v_exp_f32 v177, v177\n"
"v_mfma_f32_16x16x32_bf16 v[28:31], v[100:103], v[224:227], v[28:31]\n"
"ds_read_b64_tr_b16 v[226:227], v237 offset:20480\n"
"v_mfma_f32_16x16x32_bf16 v[28:31], v[96:99], a[252:255], v[28:31]\n"
"v_exp_f32 v172, v172\n"
"v_mfma_f32_16x16x32_bf16 a[236:239], v[120:123], a[252:255], a[236:239]\n"
"ds_read_b64_tr_b16 a[250:251], v236 offset:20480\n"
"v_cvt_pk_bf16_f32 v124, v164, v165\n"
"v_smfmac_f32_16x16x64_bf16 v[198:201], a[240:243], v[104:111], v241\n"
"s_waitcnt lgkmcnt(2)\n"
"ds_read_b64_tr_b16 a[252:253], v233 offset:16384\n"
"v_mfma_f32_16x16x32_bf16 v[156:159], v[208:211], a[0:3], v[180:183]\n"
"ds_read_b64_tr_b16 a[248:249], v236 offset:16384\n"
"v_cvt_pk_bf16_f32 v125, v166, v167\n"
"v_mfma_f32_16x16x32_bf16 v[156:159], v[212:215], a[4:7], v[156:159]\n"
"ds_read_b64_tr_b16 v[224:225], v237 offset:16384\n"
"v_cvt_pk_bf16_f32 v130, v168, v169\n"
"v_mfma_f32_16x16x32_bf16 v[156:159], v[216:219], a[8:11], v[156:159]\n"
"ds_read_b64_tr_b16 a[254:255], v233 offset:20480\n"
"v_cvt_pk_bf16_f32 v144, v160, v161\n"
"v_mfma_f32_16x16x32_bf16 v[156:159], v[220:223], a[12:15], v[156:159]\n"
"ds_read_b64_tr_b16 a[244:245], v232 offset:16384\n"
"v_mfma_f32_16x16x32_bf16 v[160:163], v[216:219], a[24:27], v[184:187]\n"
"v_exp_f32 v176, v176\n"
"v_mfma_f32_16x16x32_bf16 v[160:163], v[220:223], a[28:31], v[160:163]\n"
"v_exp_f32 v152, v152\n"
"v_mfma_f32_16x16x32_bf16 v[160:163], v[208:211], a[16:19], v[160:163]\n"
"s_add_u32 s40, s56, s52\n"
"v_cvt_pk_bf16_f32 v146, v176, v177\n"
"v_mfma_f32_16x16x32_bf16 v[160:163], v[212:215], a[20:23], v[160:163]\n"
"buffer_load_dwordx4 v207, s[16:19], s40 offen offset:1024 nt lds\n"
"v_mfma_f32_16x16x32_bf16 v[164:167], v[208:211], a[32:35], v[188:191]\n"
"v_exp_f32 v170, v170\n"
"v_mfma_f32_16x16x32_bf16 v[164:167], v[212:215], a[36:39], v[164:167]\n"
"v_exp_f32 v173, v173\n"
"v_mfma_f32_16x16x32_bf16 v[164:167], v[216:219], a[40:43], v[164:167]\n"
"v_exp_f32 v153, v153\n"
"v_mfma_f32_16x16x32_bf16 v[164:167], v[220:223], a[44:47], v[164:167]\n"
"s_waitcnt lgkmcnt(0)\n"
"v_exp_f32 v174, v174\n"
"v_mfma_f32_16x16x32_bf16 a[208:211], v[120:123], a[244:247], a[208:211]\n"
"ds_read_b128 v[208:211], v230 offset:36864\n"
"v_cvt_pk_bf16_f32 v139, v174, v175\n"
"v_mfma_f32_16x16x32_bf16 v[0:3], v[96:99], a[244:247], v[0:3]\n"
"v_exp_f32 v156, v156\n"
"v_mfma_f32_16x16x32_bf16 v[0:3], v[100:103], a[248:251], v[0:3]\n"
"ds_read_b128 v[220:223], v231 offset:37376\n"
"v_cvt_pk_bf16_f32 v138, v172, v173\n"
"v_mfma_f32_16x16x32_bf16 v[32:35], v[104:107], a[244:247], v[32:35]\n"
"v_exp_f32 v161, v161\n"
"v_mfma_f32_16x16x32_bf16 v[32:35], v[108:111], a[248:251], v[32:35]\n"
"ds_read_b128 v[216:219], v230 offset:37376\n"
"v_cvt_pk_bf16_f32 v126, v152, v153\n"
"v_mfma_f32_16x16x32_bf16 v[64:67], v[112:115], a[244:247], v[64:67]\n"
"s_add_u32 s40, s56, s53\n"
"ds_read_b128 v[212:215], v231 offset:36864\n"
"v_mfma_f32_16x16x32_bf16 v[64:67], v[116:119], a[248:251], v[64:67]\n"
"buffer_load_dwordx4 v240, s[16:19], s40 offen offset:2048 nt lds\n"
"v_mfma_f32_16x16x32_bf16 v[68:71], v[116:119], v[224:227], v[68:71]\n"
"v_exp_f32 v159, v159\n"
"v_mfma_f32_16x16x32_bf16 v[68:71], v[112:115], a[252:255], v[68:71]\n"
"v_exp_f32 v155, v155\n"
"v_mfma_f32_16x16x32_bf16 v[36:39], v[108:111], v[224:227], v[36:39]\n"
"v_exp_f32 v160, v160\n"
"v_mfma_f32_16x16x32_bf16 v[36:39], v[104:107], a[252:255], v[36:39]\n"
"v_exp_f32 v179, v179\n"
"v_mfma_f32_16x16x32_bf16 v[4:7], v[100:103], v[224:227], v[4:7]\n"
"ds_read_b64_tr_b16 a[244:245], v232 offset:16896\n"
"ds_read_b64_tr_b16 v[224:225], v237 offset:16896\n"
"v_mfma_f32_16x16x32_bf16 v[4:7], v[96:99], a[252:255], v[4:7]\n"
"v_exp_f32 v158, v158\n"
"v_mfma_f32_16x16x32_bf16 a[212:215], v[120:123], a[252:255], a[212:215]\n"
"ds_read_b64_tr_b16 a[246:247], v232 offset:20992\n"
"ds_read_b64_tr_b16 a[250:251], v236 offset:20992\n"
"v_smfmac_f32_16x16x64_bf16 v[200:203], a[240:243], v[112:119], v241\n"
"s_waitcnt lgkmcnt(4)\n"
"ds_read_b64_tr_b16 v[226:227], v237 offset:20992\n"
"v_cvt_pk_bf16_f32 v131, v170, v171\n"
"v_mfma_f32_16x16x32_bf16 v[168:171], v[208:211], a[0:3], v[180:183]\n"
"v_exp_f32 v164, v164\n"
"v_mfma_f32_16x16x32_bf16 v[168:171], v[212:215], a[4:7], v[168:171]\n"
"ds_read_b64_tr_b16 a[248:249], v236 offset:16896\n"
"ds_read_b64_tr_b16 a[254:255], v233 offset:20992\n"
"v_mfma_f32_16x16x32_bf16 v[168:171], v[216:219], a[8:11], v[168:171]\n"
"s_add_u32 s40, s56, s54\n"
"ds_read_b64_tr_b16 a[252:253], v233 offset:16896\n"
"v_mfma_f32_16x16x32_bf16 v[168:171], v[220:223], a[12:15], v[168:171]\n"
"buffer_load_dwordx4 v207, s[16:19], s40 offen offset:3072 nt lds\n"
"v_mfma_f32_16x16x32_bf16 v[172:175], v[216:219], a[24:27], v[184:187]\n"
"v_exp_f32 v154, v154\n"
"v_mfma_f32_16x16x32_bf16 v[172:175], v[220:223], a[28:31], v[172:175]\n"
"v_exp_f32 v167, v167\n"
"v_mfma_f32_16x16x32_bf16 v[172:175], v[208:211], a[16:19], v[172:175]\n"
"v_exp_f32 v163, v163\n"
"v_mfma_f32_16x16x32_bf16 v[172:175], v[212:215], a[20:23], v[172:175]\n"
"s_cbranch_vccnz LPCL2_%=\nLPKL2_%=:\n"
"v_cvt_pk_bf16_f32 v147, v178, v179\n"
"v_mfma_f32_16x16x32_bf16 v[176:179], v[208:211], a[32:35], v[188:191]\n"
"v_exp_f32 v165, v165\n"
"v_mfma_f32_16x16x32_bf16 v[176:179], v[212:215], a[36:39], v[176:179]\n"
"v_exp_f32 v157, v157\n"
"v_mfma_f32_16x16x32_bf16 v[176:179], v[216:219], a[40:43], v[176:179]\n"
"v_exp_f32 v166, v166\n"
"v_mfma_f32_16x16x32_bf16 v[176:179], v[220:223], a[44:47], v[176:179]\n"
"s_waitcnt lgkmcnt(0)\n"
"v_mfma_f32_16x16x32_bf16 a[224:227], v[120:123], a[244:247], a[224:227]\n"
"v_exp_f32 v162, v162\n"
"v_mfma_f32_16x16x32_bf16 v[16:19], v[96:99], a[244:247], v[16:19]\n"
"s_waitcnt vmcnt(8)\n"
"v_exp_f32 v168, v168\n"
"v_mfma_f32_16x16x32_bf16 v[16:19], v[100:103], a[248:251], v[16:19]\n"
"s_barrier\n"
"v_mfma_f32_16x16x32_bf16 v[48:51], v[104:107], a[244:247], v[48:51]\n"
"ds_read_b128 v[208:211], v228 offset:49152\n"
"v_cvt_pk_bf16_f32 v133, v158, v159\n"
"v_mfma_f32_16x16x32_bf16 v[48:51], v[108:111], a[248:251], v[48:51]\n"
"ds_read_b128 v[216:219], v228 offset:49664\n"
"v_cvt_pk_bf16_f32 v141, v162, v163\n"
"v_mfma_f32_16x16x32_bf16 v[80:83], v[112:115], a[244:247], v[80:83]\n"
"ds_read_b128 v[220:223], v229 offset:49664\n"
"v_cvt_pk_bf16_f32 v127, v154, v155\n"
"v_mfma_f32_16x16x32_bf16 v[80:83], v[116:119], a[248:251], v[80:83]\n"
"ds_read_b128 v[212:215], v229 offset:49152\n"
"v_cvt_pk_bf16_f32 v149, v166, v167\n"
"v_mfma_f32_16x16x32_bf16 v[84:87], v[116:119], v[224:227], v[84:87]\n"
"v_exp_f32 v176, v176\n"
"v_mfma_f32_16x16x32_bf16 v[84:87], v[112:115], a[252:255], v[84:87]\n"
"v_exp_f32 v169, v169\n"
"v_mfma_f32_16x16x32_bf16 v[52:55], v[108:111], v[224:227], v[52:55]\n"
"ds_read_b64_tr_b16 a[246:247], v234 offset:36864\n"
"v_cvt_pk_bf16_f32 v148, v164, v165\n"
"v_mfma_f32_16x16x32_bf16 v[52:55], v[104:107], a[252:255], v[52:55]\n"
"v_exp_f32 v177, v177\n"
"v_mfma_f32_16x16x32_bf16 v[20:23], v[100:103], v[224:227], v[20:23]\n"
"ds_read_b64_tr_b16 a[250:251], v238 offset:36864\n"
"v_cvt_pk_bf16_f32 v150, v176, v177\n"
"v_mfma_f32_16x16x32_bf16 v[20:23], v[96:99], a[252:255], v[20:23]\n"
"v_exp_f32 v174, v174\n"
"v_mfma_f32_16x16x32_bf16 a[228:231], v[120:123], a[252:255], a[228:231]\n"
"s_waitcnt lgkmcnt(3)\n"
"ds_read_b64_tr_b16 a[254:255], v235 offset:36864\n"
"v_cvt_pk_bf16_f32 v132, v156, v157\n"
"v_mfma_f32_16x16x32_bf16 v[152:155], v[208:211], a[0:3], v[180:183]\n"
"s_waitcnt lgkmcnt(3)\n"
"ds_read_b64_tr_b16 a[248:249], v238 offset:32768\n"
"v_cvt_pk_bf16_f32 v134, v168, v169\n"
"v_mfma_f32_16x16x32_bf16 v[152:155], v[212:215], a[4:7], v[152:155]\n"
"v_exp_f32 v172, v172\n"
"v_mfma_f32_16x16x32_bf16 v[152:155], v[216:219], a[8:11], v[152:155]\n"
"ds_read_b64_tr_b16 a[244:245], v234 offset:32768\n"
"v_cvt_pk_bf16_f32 v140, v160, v161\n"
"v_mfma_f32_16x16x32_bf16 v[152:155], v[220:223], a[12:15], v[152:155]\n"
"v_exp_f32 v175, v175\n"
"v_mfma_f32_16x16x32_bf16 v[156:159], v[216:219], a[24:27], v[184:187]\n"
"ds_read_b64_tr_b16 a[252:253], v235 offset:32768\n"
"v_cvt_pk_bf16_f32 v143, v174, v175\n"
"v_mfma_f32_16x16x32_bf16 v[156:159], v[220:223], a[28:31], v[156:159]\n"
"ds_read_b64_tr_b16 v[224:225], v239 offset:32768\n"
"v_mfma_f32_16x16x32_bf16 v[156:159], v[208:211], a[16:19], v[156:159]\n"
"s_add_u32 m0, s44, 65536\n"
"ds_read_b64_tr_b16 v[226:227], v239 offset:36864\n"
"v_mfma_f32_16x16x32_bf16 v[156:159], v[212:215], a[20:23], v[156:159]\n"
"buffer_load_dwordx4 v240, s[20:23], s56 offen nt lds\n"
"v_mfma_f32_16x16x32_bf16 v[160:163], v[208:211], a[32:35], v[188:191]\n"
"v_exp_f32 v178, v178\n"
"v_mfma_f32_16x16x32_bf16 v[160:163], v[212:215], a[36:39], v[160:163]\n"
"v_exp_f32 v171, v171\n"
"v_mfma_f32_16x16x32_bf16 v[160:163], v[216:219], a[40:43], v[160:163]\n"
"s_add_u32 s40, s56, s52\n"
"v_mfma_f32_16x16x32_bf16 v[160:163], v[220:223], a[44:47], v[160:163]\n"
"buffer_load_dwordx4 v207, s[20:23], s40 offen offset:1024 nt lds\n"
"v_mfma_f32_16x16x32_bf16 v[164:167], v[216:219], a[200:203], v[192:195]\n"
"v_exp_f32 v170, v170\n"
"v_mfma_f32_16x16x32_bf16 v[164:167], v[220:223], a[204:207], v[164:167]\n"
"v_exp_f32 v153, v153\n"
"v_mfma_f32_16x16x32_bf16 v[164:167], v[208:211], a[192:195], v[164:167]\n"
"v_exp_f32 v179, v179\n"
"v_mfma_f32_16x16x32_bf16 v[164:167], v[212:215], a[196:199], v[164:167]\n"
"s_waitcnt lgkmcnt(0)\n"
"v_exp_f32 v158, v158\n"
"v_mfma_f32_16x16x32_bf16 a[216:219], v[124:127], a[244:247], a[216:219]\n"
"ds_read_b128 v[216:219], v228 offset:53760\n"
"v_cvt_pk_bf16_f32 v135, v170, v171\n"
"v_mfma_f32_16x16x32_bf16 v[8:11], v[128:131], a[244:247], v[8:11]\n"
"v_exp_f32 v173, v173\n"
"v_mfma_f32_16x16x32_bf16 v[8:11], v[132:135], a[248:251], v[8:11]\n"
"ds_read_b128 v[208:211], v228 offset:53248\n"
"v_cvt_pk_bf16_f32 v142, v172, v173\n"
"v_mfma_f32_16x16x32_bf16 v[40:43], v[136:139], a[244:247], v[40:43]\n"
"v_exp_f32 v162, v162\n"
"v_mfma_f32_16x16x32_bf16 v[40:43], v[140:143], a[248:251], v[40:43]\n"
"ds_read_b128 v[220:223], v229 offset:53760\n"
"v_cvt_pk_bf16_f32 v151, v178, v179\n"
"v_mfma_f32_16x16x32_bf16 v[72:75], v[144:147], a[244:247], v[72:75]\n"
"s_add_u32 s40, s56, s53\n"
"ds_read_b128 v[212:215], v229 offset:53248\n"
"v_mfma_f32_16x16x32_bf16 v[72:75], v[148:151], a[248:251], v[72:75]\n"
"buffer_load_dwordx4 v240, s[20:23], s40 offen offset:2048 nt lds\n"
"v_mfma_f32_16x16x32_bf16 v[76:79], v[148:151], v[224:227], v[76:79]\n"
"v_exp_f32 v163, v163\n"
"v_mfma_f32_16x16x32_bf16 v[76:79], v[144:147], a[252:255], v[76:79]\n"
"v_exp_f32 v156, v156\n"
"v_mfma_f32_16x16x32_bf16 v[44:47], v[140:143], v[224:227], v[44:47]\n"
"v_exp_f32 v160, v160\n"
"v_mfma_f32_16x16x32_bf16 v[44:47], v[136:139], a[252:255], v[44:47]\n"
"v_exp_f32 v155, v155\n"
"v_mfma_f32_16x16x32_bf16 v[12:15], v[132:135], v[224:227], v[12:15]\n"
"ds_read_b64_tr_b16 v[226:227], v239 offset:37376\n"
"v_mfma_f32_16x16x32_bf16 v[12:15], v[128:131], a[252:255], v[12:15]\n"
"ds_read_b64_tr_b16 a[248:249], v238 offset:33280\n"
"v_mfma_f32_16x16x32_bf16 a[220:223], v[124:127], a[252:255], a[220:223]\n"
"v_exp_f32 v154, v154\n"
"v_smfmac_f32_16x16x64_bf16 v[196:199], a[240:243], v[128:135], v241\n"
"ds_read_b64_tr_b16 a[250:251], v238 offset:37376\n"
"v_mfma_f32_16x16x32_bf16 v[202:205], a[240:243], v[124:127], v[202:205]\n"
"s_waitcnt lgkmcnt(3)\n"
"v_exp_f32 v152, v152\n"
"v_mfma_f32_16x16x32_bf16 v[168:171], v[216:219], a[8:11], v[180:183]\n"
"ds_read_b64_tr_b16 a[244:245], v234 offset:33280\n"
"v_cvt_pk_bf16_f32 v96, v152, v153\n"
"v_mfma_f32_16x16x32_bf16 v[168:171], v[220:223], a[12:15], v[168:171]\n"
"v_exp_f32 v165, v165\n"
"v_mfma_f32_16x16x32_bf16 v[168:171], v[208:211], a[0:3], v[168:171]\n"
"ds_read_b64_tr_b16 a[246:247], v234 offset:37376\n"
"v_cvt_pk_bf16_f32 v113, v162, v163\n"
"v_mfma_f32_16x16x32_bf16 v[168:171], v[212:215], a[4:7], v[168:171]\n"
"ds_read_b64_tr_b16 a[254:255], v235 offset:37376\n"
"v_mfma_f32_16x16x32_bf16 v[172:175], v[208:211], a[16:19], v[184:187]\n"
"ds_read_b64_tr_b16 v[224:225], v239 offset:33280\n"
"v_cvt_pk_bf16_f32 v97, v154, v155\n"
"v_mfma_f32_16x16x32_bf16 v[172:175], v[212:215], a[20:23], v[172:175]\n"
"v_mfma_f32_16x16x32_bf16 v[172:175], v[216:219], a[24:27], v[172:175]\n"
"s_add_u32 s40, s56, s54\n"
"ds_read_b64_tr_b16 a[252:253], v235 offset:33280\n"
"v_mfma_f32_16x16x32_bf16 v[172:175], v[220:223], a[28:31], v[172:175]\n"
"buffer_load_dwordx4 v207, s[20:23], s40 offen offset:3072 nt lds\n"
"v_mfma_f32_16x16x32_bf16 v[176:179], v[216:219], a[40:43], v[188:191]\n"
"s_add_u32 s55, s55, s59\n"
"v_exp_f32 v157, v157\n"
"v_mfma_f32_16x16x32_bf16 v[176:179], v[220:223], a[44:47], v[176:179]\n"
"s_or_b32 s55, s55, s58\n"
"v_exp_f32 v171, v171\n"
"v_mfma_f32_16x16x32_bf16 v[176:179], v[208:211], a[32:35], v[176:179]\n"
"s_xor_b32 s56, s55, s57\n"
"v_exp_f32 v159, v159\n"
"v_mfma_f32_16x16x32_bf16 v[176:179], v[212:215], a[36:39], v[176:179]\n"
"v_exp_f32 v167, v167\n"
"v_mfma_f32_16x16x32_bf16 v[152:155], v[208:211], a[192:195], v[192:195]\n"
"v_cvt_pk_bf16_f32 v105, v158, v159\n"
"v_mfma_f32_16x16x32_bf16 v[152:155], v[212:215], a[196:199], v[152:155]\n"
"ds_read_b128 v[208:211], v230 offset:49152\n"
"v_cvt_pk_bf16_f32 v104, v156, v157\n"
"v_mfma_f32_16x16x32_bf16 v[152:155], v[216:219], a[200:203], v[152:155]\n"
"v_exp_f32 v166, v166\n"
"v_mfma_f32_16x16x32_bf16 v[152:155], v[220:223], a[204:207], v[152:155]\n"
"s_waitcnt lgkmcnt(1)\n"
"ds_read_b128 v[216:219], v230 offset:49664\n"
"v_mfma_f32_16x16x32_bf16 a[232:235], v[124:127], a[244:247], a[232:235]\n"
"v_exp_f32 v169, v169\n"
"v_mfma_f32_16x16x32_bf16 v[24:27], v[128:131], a[244:247], v[24:27]\n"
"ds_read_b128 v[220:223], v231 offset:49664\n"
"v_mfma_f32_16x16x32_bf16 v[24:27], v[132:135], a[248:251], v[24:27]\n"
"v_exp_f32 v164, v164\n"
"v_mfma_f32_16x16x32_bf16 v[56:59], v[136:139], a[244:247], v[56:59]\n"
"ds_read_b128 v[212:215], v231 offset:49152\n"
"v_mfma_f32_16x16x32_bf16 v[56:59], v[140:143], a[248:251], v[56:59]\n"
"v_exp_f32 v178, v178\n"
"v_mfma_f32_16x16x32_bf16 v[88:91], v[144:147], a[244:247], v[88:91]\n"
"s_add_u32 m0, s44, 16384\n"
"ds_read_b64_tr_b16 a[246:247], v232 offset:36864\n"
"v_mfma_f32_16x16x32_bf16 v[88:91], v[148:151], a[248:251], v[88:91]\n"
"buffer_load_dwordx4 v240, s[16:19], s56 offen nt lds\n"
"v_mfma_f32_16x16x32_bf16 v[92:95], v[148:151], v[224:227], v[92:95]\n"
"v_exp_f32 v175, v175\n"
"v_mfma_f32_16x16x32_bf16 v[92:95], v[144:147], a[252:255], v[92:95]\n"
"v_exp_f32 v168, v168\n"
"v_mfma_f32_16x16x32_bf16 v[60:63], v[140:143], v[224:227], v[60:63]\n"
"v_exp_f32 v161, v161\n"
"v_mfma_f32_16x16x32_bf16 v[60:63], v[136:139], a[252:255], v[60:63]\n"
"v_exp_f32 v177, v177\n"
"v_mfma_f32_16x16x32_bf16 v[28:31], v[132:135], v[224:227], v[28:31]\n"
"ds_read_b64_tr_b16 v[226:227], v237 offset:36864\n"
"v_mfma_f32_16x16x32_bf16 v[28:31], v[128:131], a[252:255], v[28:31]\n"
"v_exp_f32 v172, v172\n"
"v_mfma_f32_16x16x32_bf16 a[236:239], v[124:127], a[252:255], a[236:239]\n"
"ds_read_b64_tr_b16 a[250:251], v236 offset:36864\n"
"v_cvt_pk_bf16_f32 v120, v164, v165\n"
"v_smfmac_f32_16x16x64_bf16 v[198:201], a[240:243], v[136:143], v241\n"
"s_waitcnt lgkmcnt(2)\n"
"ds_read_b64_tr_b16 a[252:253], v233 offset:32768\n"
"v_mfma_f32_16x16x32_bf16 v[156:159], v[208:211], a[0:3], v[180:183]\n"
"ds_read_b64_tr_b16 a[248:249], v236 offset:32768\n"
"v_cvt_pk_bf16_f32 v121, v166, v167\n"
"v_mfma_f32_16x16x32_bf16 v[156:159], v[212:215], a[4:7], v[156:159]\n"
"ds_read_b64_tr_b16 v[224:225], v237 offset:32768\n"
"v_cvt_pk_bf16_f32 v98, v168, v169\n"
"v_mfma_f32_16x16x32_bf16 v[156:159], v[216:219], a[8:11], v[156:159]\n"
"ds_read_b64_tr_b16 a[254:255], v233 offset:36864\n"
"v_cvt_pk_bf16_f32 v112, v160, v161\n"
"v_mfma_f32_16x16x32_bf16 v[156:159], v[220:223], a[12:15], v[156:159]\n"
"ds_read_b64_tr_b16 a[244:245], v232 offset:32768\n"
"v_mfma_f32_16x16x32_bf16 v[160:163], v[216:219], a[24:27], v[184:187]\n"
"v_exp_f32 v176, v176\n"
"v_mfma_f32_16x16x32_bf16 v[160:163], v[220:223], a[28:31], v[160:163]\n"
"v_exp_f32 v152, v152\n"
"v_mfma_f32_16x16x32_bf16 v[160:163], v[208:211], a[16:19], v[160:163]\n"
"s_add_u32 s40, s56, s52\n"
"v_cvt_pk_bf16_f32 v114, v176, v177\n"
"v_mfma_f32_16x16x32_bf16 v[160:163], v[212:215], a[20:23], v[160:163]\n"
"buffer_load_dwordx4 v207, s[16:19], s40 offen offset:1024 nt lds\n"
"v_mfma_f32_16x16x32_bf16 v[164:167], v[208:211], a[32:35], v[188:191]\n"
"v_exp_f32 v170, v170\n"
"v_mfma_f32_16x16x32_bf16 v[164:167], v[212:215], a[36:39], v[164:167]\n"
"v_exp_f32 v173, v173\n"
"v_mfma_f32_16x16x32_bf16 v[164:167], v[216:219], a[40:43], v[164:167]\n"
"v_exp_f32 v153, v153\n"
"v_mfma_f32_16x16x32_bf16 v[164:167], v[220:223], a[44:47], v[164:167]\n"
"s_waitcnt lgkmcnt(0)\n"
"v_exp_f32 v174, v174\n"
"v_mfma_f32_16x16x32_bf16 a[208:211], v[124:127], a[244:247], a[208:211]\n"
"ds_read_b128 v[208:211], v230 offset:53248\n"
"v_cvt_pk_bf16_f32 v107, v174, v175\n"
"v_mfma_f32_16x16x32_bf16 v[0:3], v[128:131], a[244:247], v[0:3]\n"
"v_exp_f32 v156, v156\n"
"v_mfma_f32_16x16x32_bf16 v[0:3], v[132:135], a[248:251], v[0:3]\n"
"ds_read_b128 v[220:223], v231 offset:53760\n"
"v_cvt_pk_bf16_f32 v106, v172, v173\n"
"v_mfma_f32_16x16x32_bf16 v[32:35], v[136:139], a[244:247], v[32:35]\n"
"v_exp_f32 v161, v161\n"
"v_mfma_f32_16x16x32_bf16 v[32:35], v[140:143], a[248:251], v[32:35]\n"
"ds_read_b128 v[216:219], v230 offset:53760\n"
"v_cvt_pk_bf16_f32 v122, v152, v153\n"
"v_mfma_f32_16x16x32_bf16 v[64:67], v[144:147], a[244:247], v[64:67]\n"
"s_add_u32 s40, s56, s53\n"
"ds_read_b128 v[212:215], v231 offset:53248\n"
"v_mfma_f32_16x16x32_bf16 v[64:67], v[148:151], a[248:251], v[64:67]\n"
"buffer_load_dwordx4 v240, s[16:19], s40 offen offset:2048 nt lds\n"
"v_mfma_f32_16x16x32_bf16 v[68:71], v[148:151], v[224:227], v[68:71]\n"
"v_exp_f32 v159, v159\n"
"v_mfma_f32_16x16x32_bf16 v[68:71], v[144:147], a[252:255], v[68:71]\n"
"v_exp_f32 v155, v155\n"
"v_mfma_f32_16x16x32_bf16 v[36:39], v[140:143], v[224:227], v[36:39]\n"
"v_exp_f32 v160, v160\n"
"v_mfma_f32_16x16x32_bf16 v[36:39], v[136:139], a[252:255], v[36:39]\n"
"v_exp_f32 v179, v179\n"
"v_mfma_f32_16x16x32_bf16 v[4:7], v[132:135], v[224:227], v[4:7]\n"
"ds_read_b64_tr_b16 a[244:245], v232 offset:33280\n"
"ds_read_b64_tr_b16 v[224:225], v237 offset:33280\n"
"v_mfma_f32_16x16x32_bf16 v[4:7], v[128:131], a[252:255], v[4:7]\n"
"v_exp_f32 v158, v158\n"
"v_mfma_f32_16x16x32_bf16 a[212:215], v[124:127], a[252:255], a[212:215]\n"
"ds_read_b64_tr_b16 a[246:247], v232 offset:37376\n"
"ds_read_b64_tr_b16 a[250:251], v236 offset:37376\n"
"v_smfmac_f32_16x16x64_bf16 v[200:203], a[240:243], v[144:151], v241\n"
"s_waitcnt lgkmcnt(4)\n"
"ds_read_b64_tr_b16 v[226:227], v237 offset:37376\n"
"v_cvt_pk_bf16_f32 v99, v170, v171\n"
"v_mfma_f32_16x16x32_bf16 v[168:171], v[208:211], a[0:3], v[180:183]\n"
"v_exp_f32 v164, v164\n"
"v_mfma_f32_16x16x32_bf16 v[168:171], v[212:215], a[4:7], v[168:171]\n"
"ds_read_b64_tr_b16 a[248:249], v236 offset:33280\n"
"ds_read_b64_tr_b16 a[254:255], v233 offset:37376\n"
"v_mfma_f32_16x16x32_bf16 v[168:171], v[216:219], a[8:11], v[168:171]\n"
"s_add_u32 s40, s56, s54\n"
"ds_read_b64_tr_b16 a[252:253], v233 offset:33280\n"
"v_mfma_f32_16x16x32_bf16 v[168:171], v[220:223], a[12:15], v[168:171]\n"
"buffer_load_dwordx4 v207, s[16:19], s40 offen offset:3072 nt lds\n"
"v_mfma_f32_16x16x32_bf16 v[172:175], v[216:219], a[24:27], v[184:187]\n"
"v_exp_f32 v154, v154\n"
"v_mfma_f32_16x16x32_bf16 v[172:175], v[220:223], a[28:31], v[172:175]\n"
"v_exp_f32 v167, v167\n"
"v_mfma_f32_16x16x32_bf16 v[172:175], v[208:211], a[16:19], v[172:175]\n"
"v_exp_f32 v163, v163\n"
"v_mfma_f32_16x16x32_bf16 v[172:175], v[212:215], a[20:23], v[172:175]\n"
"s_cbranch_vccnz LPCL3_%=\nLPKL3_%=:\n"
"v_cvt_pk_bf16_f32 v115, v178, v179\n"
"v_mfma_f32_16x16x32_bf16 v[176:179], v[208:211], a[32:35], v[188:191]\n"
"s_add_u32 s47, s47, 1\n"
"v_exp_f32 v165, v165\n"
"v_mfma_f32_16x16x32_bf16 v[176:179], v[212:215], a[36:39], v[176:179]\n"
"v_exp_f32 v157, v157\n"
"v_mfma_f32_16x16x32_bf16 v[176:179], v[216:219], a[40:43], v[176:179]\n"
"s_and_b32 s40, s47, 3\n"
"v_exp_f32 v166, v166\n"
"v_mfma_f32_16x16x32_bf16 v[176:179], v[220:223], a[44:47], v[176:179]\n"
"s_waitcnt lgkmcnt(0)\n"
"s_cbranch_scc1 LAL_%=\n"
"s_cmp_eq_u32 s47, 8\n"
"s_cbranch_scc1 LTH1_%=\n"
"s_cmp_eq_u32 s47, 12\n"
"s_cbranch_scc1 LTH2_%=\n"
"s_cmp_eq_u32 s47, 16\n"
"s_cbranch_scc1 LAX_%=\n"
"LTH0_%=:\n"
"v_mfma_f32_16x16x32_bf16 a[224:227], v[124:127], a[244:247], a[224:227]\n"
"v_exp_f32 v171, v171\n"
"v_mfma_f32_16x16x32_bf16 v[16:19], v[128:131], a[244:247], v[16:19]\n"
"s_waitcnt vmcnt(8)\n"
"v_exp_f32 v172, v172\n"
"v_mfma_f32_16x16x32_bf16 v[16:19], v[132:135], a[248:251], v[16:19]\n"
"s_barrier\n"
"v_mfma_f32_16x16x32_bf16 v[48:51], v[136:139], a[244:247], v[48:51]\n"
"v_exp_f32 v169, v169\n"
"v_mfma_f32_16x16x32_bf16 v[48:51], v[140:143], a[248:251], v[48:51]\n"
"ds_read_b128 v[220:223], v229 offset:512\n"
"v_cvt_pk_bf16_f32 v117, v166, v167\n"
"v_exp_f32 v176, v176\n"
"v_exp_f32 v177, v177\n"
"v_exp_f32 v178, v178\n"
"v_mfma_f32_16x16x32_bf16 v[80:83], v[144:147], a[244:247], v[80:83]\n"
"ds_read_b64_tr_b16 a[244:245], v234 offset:49152\n"
"v_cvt_pk_bf16_f32 v108, v160, v161\n"
"v_mfma_f32_16x16x32_bf16 v[80:83], v[148:151], a[248:251], v[80:83]\n"
"ds_read_b64_tr_b16 a[246:247], v234 offset:53248\n"
"ds_read_b64_tr_b16 a[248:249], v238 offset:49152\n"
"v_exp_f32 v162, v162\n"
"v_exp_f32 v168, v168\n"
"v_exp_f32 v173, v173\n"
"v_exp_f32 v175, v175\n"
"v_mfma_f32_16x16x32_bf16 v[84:87], v[148:151], v[224:227], v[84:87]\n"
"ds_read_b64_tr_b16 a[250:251], v238 offset:53248\n"
"v_cvt_pk_bf16_f32 v116, v164, v165\n"
"v_mfma_f32_16x16x32_bf16 v[84:87], v[144:147], a[252:255], v[84:87]\n"
"v_exp_f32 v170, v170\n"
"v_mfma_f32_16x16x32_bf16 v[52:55], v[140:143], v[224:227], v[52:55]\n"
"v_cvt_pk_bf16_f32 v103, v170, v171\n"
"v_cvt_pk_bf16_f32 v110, v172, v173\n"
"v_mfma_f32_16x16x32_bf16 v[52:55], v[136:139], a[252:255], v[52:55]\n"
"v_cvt_pk_bf16_f32 v123, v154, v155\n"
"v_cvt_pk_bf16_f32 v101, v158, v159\n"
"v_mfma_f32_16x16x32_bf16 v[20:23], v[132:135], v[224:227], v[20:23]\n"
"v_cvt_pk_bf16_f32 v100, v156, v157\n"
"v_cvt_pk_bf16_f32 v102, v168, v169\n"
"v_mfma_f32_16x16x32_bf16 v[20:23], v[128:131], a[252:255], v[20:23]\n"
"v_exp_f32 v179, v179\n"
"v_mfma_f32_16x16x32_bf16 a[228:231], v[124:127], a[252:255], a[228:231]\n"
"s_waitcnt lgkmcnt(2)\n"
"ds_read_b64_tr_b16 a[252:253], v235 offset:49152\n"
"ds_read_b64_tr_b16 v[226:227], v239 offset:53248\n"
"v_mfma_f32_16x16x32_bf16 a[216:219], v[120:123], a[244:247], a[216:219]\n"
"s_add_u32 m0, s44, 81920\n"
"ds_read_b64_tr_b16 v[224:225], v239 offset:49152\n"
"v_cvt_pk_bf16_f32 v119, v178, v179\n"
"v_smfmac_f32_16x16x64_bf16 v[196:199], a[240:243], v[96:103], v241\n"
"buffer_load_dwordx4 v240, s[20:23], s56 offen nt lds\n"
"v_mfma_f32_16x16x32_bf16 v[8:11], v[96:99], a[244:247], v[8:11]\n"
"s_waitcnt lgkmcnt(3)\n"
"ds_read_b64_tr_b16 a[254:255], v235 offset:53248\n"
"v_cvt_pk_bf16_f32 v118, v176, v177\n"
"v_mfma_f32_16x16x32_bf16 v[8:11], v[100:103], a[248:251], v[8:11]\n"
"v_exp_f32 v174, v174\n"
"v_smfmac_f32_16x16x64_bf16 v[200:203], a[240:243], v[112:119], v241\n"
"v_cvt_pk_bf16_f32 v109, v162, v163\n"
"v_cvt_pk_bf16_f32 v111, v174, v175\n"
"v_mfma_f32_16x16x32_bf16 v[40:43], v[104:107], a[244:247], v[40:43]\n"
"ds_read_b128 v[212:215], v229 offset:0\n"
"v_and_b32 v245, 0x3f0, v242\n"
"v_mfma_f32_16x16x32_bf16 v[40:43], v[108:111], a[248:251], v[40:43]\n"
"v_lshlrev_b32_e64 v244, 24, s43\n"
"v_mul_f32 v244, 0x45800000, v244\n"
"v_smfmac_f32_16x16x64_bf16 v[198:201], a[240:243], v[104:111], v241\n"
"ds_read_b128 v[208:211], v228 offset:0\n"
"v_add_f32 v196, v196, v197\n"
"v_mfma_f32_16x16x32_bf16 v[72:75], v[112:115], a[244:247], v[72:75]\n"
"ds_read_b64_tr_b16 a[244:245], v234 offset:49664\n"
"v_lshrrev_b32 v245, 1, v245\n"
"v_mfma_f32_16x16x32_bf16 v[72:75], v[116:119], a[248:251], v[72:75]\n"
"ds_read_b64_tr_b16 a[246:247], v234 offset:53760\n"
"v_sub_u32 v245, v242, v245\n"
"v_mfma_f32_16x16x32_bf16 v[202:205], a[240:243], v[120:123], v[202:205]\n"
"s_waitcnt lgkmcnt(4)\n"
"ds_read_b64_tr_b16 a[248:249], v238 offset:49664\n"
"v_add_f32 v200, v200, v201\n"
"v_mfma_f32_16x16x32_bf16 v[76:79], v[116:119], v[224:227], v[76:79]\n"
"v_add_f32 v198, v198, v199\n"
"v_add_f32 v254, v196, v198\n"
"v_mfma_f32_16x16x32_bf16 v[76:79], v[112:115], a[252:255], v[76:79]\n"
"ds_read_b64_tr_b16 a[250:251], v238 offset:53760\n"
"v_xor_b32 v250, 0x1000, v245\n"
"v_mfma_f32_16x16x32_bf16 v[44:47], v[108:111], v[224:227], v[44:47]\n"
"ds_read_b128 v[216:219], v228 offset:512\n"
"v_add_f32 v254, v254, v200\n"
"v_mfma_f32_16x16x32_bf16 v[44:47], v[104:107], a[252:255], v[44:47]\n"
"v_add_f32 v244, v244, v202\n"
"v_add_f32 v254, v254, v202\n"
"v_mfma_f32_16x16x32_bf16 v[12:15], v[100:103], v[224:227], v[12:15]\n"
"s_add_u32 s40, s56, s52\n"
"ds_read_b64_tr_b16 v[226:227], v239 offset:53760\n"
"v_min3_f32 v244, v244, v196, v198\n"
"v_mfma_f32_16x16x32_bf16 v[12:15], v[96:99], a[252:255], v[12:15]\n"
"buffer_load_dwordx4 v207, s[20:23], s40 offen offset:1024 nt lds\n"
"v_min_f32 v244, v244, v200\n"
"v_rcp_f32 v244, v244\n"
"v_mfma_f32_16x16x32_bf16 a[220:223], v[120:123], a[252:255], a[220:223]\n"
"s_waitcnt lgkmcnt(4)\n"
"v_fmac_f32 v254, 0x68800000, v244\n"
"v_cmp_nge_f32 vcc, s45, v254\n"
"s_cbranch_vccnz LSLOW_%=\nLBACK0_%=:\ns_mov_b64 vcc, s[64:65]\n"
"v_mfma_f32_16x16x32_bf16 a[232:235], v[120:123], a[244:247], a[232:235]\n"
"v_rcp_f32 v246, v196\n"
"v_accvgpr_mov_b32 a2, a50\n"
"v_accvgpr_mov_b32 a7, a55\n"
"v_accvgpr_mov_b32 a10, a58\n"
"v_accvgpr_mov_b32 a15, a63\n"
"v_accvgpr_mov_b32 a22, a70\n"
"v_accvgpr_mov_b32 a30, a78\n"
"v_bfe_u32 v149, v242, 8, 2\n"
"v_lshl_add_u32 v149, v149, 4, s43\n"
"v_mfma_f32_16x16x32_bf16 v[24:27], v[96:99], a[244:247], v[24:27]\n"
"s_waitcnt lgkmcnt(2)\n"
"ds_read_b128 v[124:127], v149 offset:0\n"
"v_accvgpr_mov_b32 a28, a76\n"
"v_mfma_f32_16x16x32_bf16 v[24:27], v[100:103], a[248:251], v[24:27]\n"
"ds_read_b128 v[132:135], v149 offset:512\n"
"v_mov_b32_dpp v246, v246 row_ror:12 row_mask:0x2 bank_mask:0xf\n"
"v_accvgpr_mov_b32 a3, a51\n"
"v_accvgpr_mov_b32 a12, a60\n"
"v_accvgpr_mov_b32 a18, a66\n"
"v_accvgpr_mov_b32 a19, a67\n"
"v_accvgpr_mov_b32 a26, a74\n"
"v_accvgpr_mov_b32 a36, a84\n"
"v_xor_b32 v148, 0x400, v243\n"
"v_xor_b32 v150, 64, v149\n"
"v_mfma_f32_16x16x32_bf16 v[56:59], v[104:107], a[244:247], v[56:59]\n"
"ds_read_b128 v[128:131], v150 offset:0\n"
"v_accvgpr_mov_b32 a39, a87\n"
"v_mfma_f32_16x16x32_bf16 v[56:59], v[108:111], a[248:251], v[56:59]\n"
"ds_read_b128 v[136:139], v150 offset:512\n"
"v_rcp_f32 v247, v198\n"
"v_mov_b32_dpp v246, v246 row_ror:8 row_mask:0x4 bank_mask:0xf\n"
"v_accvgpr_mov_b32 a0, a48\n"
"v_accvgpr_mov_b32 a1, a49\n"
"v_accvgpr_mov_b32 a4, a52\n"
"v_accvgpr_mov_b32 a5, a53\n"
"v_accvgpr_mov_b32 a8, a56\n"
"v_accvgpr_mov_b32 a16, a64\n"
"v_accvgpr_mov_b32 a25, a73\n"
"v_mfma_f32_16x16x32_bf16 v[88:91], v[112:115], a[244:247], v[88:91]\n"
"v_accvgpr_mov_b32 a20, a68\n"
"v_accvgpr_mov_b32 a29, a77\n"
"v_mfma_f32_16x16x32_bf16 v[88:91], v[116:119], a[248:251], v[88:91]\n"
"ds_read_b128 a[192:195], v243 offset:4096\n"
"ds_read_b128 a[200:203], v243 offset:6144\n"
"v_accvgpr_mov_b32 a6, a54\n"
"v_accvgpr_mov_b32 a9, a57\n"
"v_accvgpr_mov_b32 a11, a59\n"
"v_accvgpr_mov_b32 a13, a61\n"
"v_accvgpr_mov_b32 a14, a62\n"
"v_accvgpr_mov_b32 a17, a65\n"
"v_accvgpr_mov_b32 a23, a71\n"
"v_accvgpr_mov_b32 a31, a79\n"
"v_accvgpr_mov_b32 a33, a81\n"
"v_accvgpr_mov_b32 a35, a83\n"
"v_accvgpr_mov_b32 a44, a92\n"
"v_accvgpr_mov_b32 a47, a95\n"
"s_waitcnt lgkmcnt(5)\n"
"v_mfma_f32_16x16x32_bf16 v[180:183], v[124:127], a[0:3], 0\n"
"v_xor_b32 v244, 0x11000, v242\n"
"s_waitcnt lgkmcnt(3)\n"
"v_mfma_f32_16x16x32_bf16 v[180:183], v[128:131], a[4:7], v[180:183]\n"
"v_accvgpr_mov_b32 a34, a82\n"
"v_accvgpr_mov_b32 a43, a91\n"
"v_mfma_f32_16x16x32_bf16 v[180:183], v[132:135], a[8:11], v[180:183]\n"
"s_waitcnt lgkmcnt(2)\n"
"ds_read_b128 a[204:207], v148 offset:6144\n"
"v_accvgpr_mov_b32 a37, a85\n"
"v_mfma_f32_16x16x32_bf16 v[180:183], v[136:139], a[12:15], v[180:183]\n"
"ds_read_b128 a[196:199], v148 offset:4096\n"
"v_accvgpr_mov_b32 a21, a69\n"
"v_accvgpr_mov_b32 a24, a72\n"
"v_accvgpr_mov_b32 a32, a80\n"
"v_accvgpr_mov_b32 a38, a86\n"
"v_accvgpr_mov_b32 a40, a88\n"
"v_accvgpr_mov_b32 a42, a90\n"
"v_accvgpr_mov_b32 a46, a94\n"
"v_mfma_f32_16x16x32_bf16 v[184:187], v[124:127], a[16:19], 0\n"
"v_mov_b32_dpp v246, v246 row_ror:4 row_mask:0x8 bank_mask:0xf\n"
"v_accvgpr_mov_b32 a27, a75\n"
"v_mfma_f32_16x16x32_bf16 v[184:187], v[128:131], a[20:23], v[184:187]\n"
"v_mov_b32_dpp v247, v247 row_ror:12 row_mask:0x2 bank_mask:0xf\n"
"v_mfma_f32_16x16x32_bf16 v[184:187], v[132:135], a[24:27], v[184:187]\n"
"s_add_u32 s40, s56, s53\n"
"ds_read_b64_tr_b16 v[224:225], v239 offset:49664\n"
"v_mfma_f32_16x16x32_bf16 v[184:187], v[136:139], a[28:31], v[184:187]\n"
"buffer_load_dwordx4 v240, s[20:23], s40 offen offset:2048 nt lds\n"
"v_accvgpr_mov_b32 a41, a89\n"
"v_sub_f32 v180, s74, v180\n"
"v_sub_f32 v181, s74, v181\n"
"v_sub_f32 v183, s74, v183\n"
"v_mul_f32_dpp v26, v246, v26 row_newbcast:2 row_mask:0xf bank_mask:0xf\n"
"v_mfma_f32_16x16x32_bf16 v[188:191], v[124:127], a[32:35], 0\n"
"v_mov_b32_dpp v247, v247 row_ror:8 row_mask:0x4 bank_mask:0xf\n"
"v_accvgpr_mov_b32 a45, a93\n"
"v_mfma_f32_16x16x32_bf16 v[188:191], v[128:131], a[36:39], v[188:191]\n"
"v_mov_b32 v252, v192\n"
"v_mfma_f32_16x16x32_bf16 v[188:191], v[132:135], a[40:43], v[188:191]\n"
"v_mfma_f32_16x16x32_bf16 v[188:191], v[136:139], a[44:47], v[188:191]\n"
"v_mul_f32_dpp v11, v246, v11 row_newbcast:3 row_mask:0xf bank_mask:0xf\n"
"s_waitcnt lgkmcnt(1)\n"
"v_mfma_f32_16x16x32_bf16 v[192:195], v[124:127], a[192:195], 0\n"
"ds_read_b64_tr_b16 a[252:253], v235 offset:49664\n"
"v_sub_f32 v185, s74, v185\n"
"v_mfma_f32_16x16x32_bf16 v[192:195], v[128:131], a[196:199], v[192:195]\n"
"ds_read_b64_tr_b16 a[248:249], v236 offset:49152\n"
"v_sub_f32 v182, s74, v182\n"
"v_mfma_f32_16x16x32_bf16 v[192:195], v[132:135], a[200:203], v[192:195]\n"
"v_sub_f32 v184, s74, v184\n"
"v_mfma_f32_16x16x32_bf16 v[192:195], v[136:139], a[204:207], v[192:195]\n"
"ds_read_b64_tr_b16 a[254:255], v235 offset:53760\n"
"ds_read_b64_tr_b16 a[246:247], v232 offset:53248\n"
"v_mov_b32 v253, v202\n"
"v_sub_f32 v186, s74, v186\n"
"v_mfma_f32_16x16x32_bf16 v[152:155], v[208:211], a[0:3], v[180:183]\n"
"v_mfma_f32_16x16x32_bf16 v[152:155], v[212:215], a[4:7], v[152:155]\n"
"v_sub_f32 v187, s74, v187\n"
"v_mfma_f32_16x16x32_bf16 v[152:155], v[216:219], a[8:11], v[152:155]\n"
"s_add_u32 s40, s56, s54\n"
"v_mfma_f32_16x16x32_bf16 v[152:155], v[220:223], a[12:15], v[152:155]\n"
"buffer_load_dwordx4 v207, s[20:23], s40 offen offset:3072 nt lds\n"
"v_mov_b32_dpp v247, v247 row_ror:4 row_mask:0x8 bank_mask:0xf\n"
"v_sub_f32 v189, s74, v189\n"
"v_sub_f32 v190, s74, v190\n"
"v_sub_f32 v191, s74, v191\n"
"v_sub_f32 v192, s74, v192\n"
"v_mfma_f32_16x16x32_bf16 v[156:159], v[216:219], a[24:27], v[184:187]\n"
"s_add_u32 s55, s55, s59\n"
"v_mfma_f32_16x16x32_bf16 v[156:159], v[220:223], a[28:31], v[156:159]\n"
"s_or_b32 s55, s55, s58\n"
"v_sub_f32 v188, s74, v188\n"
"v_mfma_f32_16x16x32_bf16 v[156:159], v[208:211], a[16:19], v[156:159]\n"
"s_xor_b32 s56, s55, s57\n"
"v_exp_f32 v153, v153\n"
"v_mfma_f32_16x16x32_bf16 v[156:159], v[212:215], a[20:23], v[156:159]\n"
"ds_write_b64 v250, v[252:253] offset:52224\n"
"v_sub_f32 v193, s74, v193\n"
"v_sub_f32 v194, s74, v194\n"
"v_exp_f32 v152, v152\n"
"v_mfma_f32_16x16x32_bf16 v[160:163], v[208:211], a[32:35], v[188:191]\n"
"v_mfma_f32_16x16x32_bf16 v[160:163], v[212:215], a[36:39], v[160:163]\n"
"v_rcp_f32 v206, v200\n"
"v_mfma_f32_16x16x32_bf16 v[160:163], v[216:219], a[40:43], v[160:163]\n"
"v_sub_f32 v195, s74, v195\n"
"v_mfma_f32_16x16x32_bf16 v[160:163], v[220:223], a[44:47], v[160:163]\n"
"ds_write_b128 v244, a[216:219] offset:32768\n"
"v_mul_f32_dpp v25, v246, v25 row_newbcast:1 row_mask:0xf bank_mask:0xf\n"
"v_mfma_f32_16x16x32_bf16 v[164:167], v[216:219], a[200:203], v[192:195]\n"
"v_mov_b32_dpp v206, v206 row_ror:12 row_mask:0x2 bank_mask:0xf\n"
"v_mfma_f32_16x16x32_bf16 v[164:167], v[220:223], a[204:207], v[164:167]\n"
"v_mfma_f32_16x16x32_bf16 v[164:167], v[208:211], a[192:195], v[164:167]\n"
"ds_read_b128 v[208:211], v228 offset:4096\n"
"v_mfma_f32_16x16x32_bf16 v[164:167], v[212:215], a[196:199], v[164:167]\n"
"ds_read_b128 v[216:219], v228 offset:4608\n"
"ds_read_b128 v[220:223], v229 offset:4608\n"
"v_mov_b32_dpp v206, v206 row_ror:8 row_mask:0x4 bank_mask:0xf\n"
"v_exp_f32 v155, v155\n"
"v_exp_f32 v160, v160\n"
"s_branch LTC_%=\n"
"LTH1_%=:\n"
"v_mfma_f32_16x16x32_bf16 a[224:227], v[124:127], a[244:247], a[224:227]\n"
"v_exp_f32 v171, v171\n"
"v_mfma_f32_16x16x32_bf16 v[16:19], v[128:131], a[244:247], v[16:19]\n"
"s_waitcnt vmcnt(8)\n"
"v_exp_f32 v172, v172\n"
"v_mfma_f32_16x16x32_bf16 v[16:19], v[132:135], a[248:251], v[16:19]\n"
"s_barrier\n"
"v_mfma_f32_16x16x32_bf16 v[48:51], v[136:139], a[244:247], v[48:51]\n"
"v_exp_f32 v169, v169\n"
"v_mfma_f32_16x16x32_bf16 v[48:51], v[140:143], a[248:251], v[48:51]\n"
"ds_read_b128 v[220:223], v229 offset:512\n"
"v_cvt_pk_bf16_f32 v117, v166, v167\n"
"v_exp_f32 v176, v176\n"
"v_exp_f32 v177, v177\n"
"v_exp_f32 v178, v178\n"
"v_mfma_f32_16x16x32_bf16 v[80:83], v[144:147], a[244:247], v[80:83]\n"
"ds_read_b64_tr_b16 a[244:245], v234 offset:49152\n"
"v_cvt_pk_bf16_f32 v108, v160, v161\n"
"v_mfma_f32_16x16x32_bf16 v[80:83], v[148:151], a[248:251], v[80:83]\n"
"ds_read_b64_tr_b16 a[246:247], v234 offset:53248\n"
"ds_read_b64_tr_b16 a[248:249], v238 offset:49152\n"
"v_exp_f32 v162, v162\n"
"v_exp_f32 v168, v168\n"
"v_exp_f32 v173, v173\n"
"v_exp_f32 v175, v175\n"
"v_mfma_f32_16x16x32_bf16 v[84:87], v[148:151], v[224:227], v[84:87]\n"
"ds_read_b64_tr_b16 a[250:251], v238 offset:53248\n"
"v_cvt_pk_bf16_f32 v116, v164, v165\n"
"v_mfma_f32_16x16x32_bf16 v[84:87], v[144:147], a[252:255], v[84:87]\n"
"v_exp_f32 v170, v170\n"
"v_mfma_f32_16x16x32_bf16 v[52:55], v[140:143], v[224:227], v[52:55]\n"
"v_cvt_pk_bf16_f32 v103, v170, v171\n"
"v_cvt_pk_bf16_f32 v110, v172, v173\n"
"v_mfma_f32_16x16x32_bf16 v[52:55], v[136:139], a[252:255], v[52:55]\n"
"v_cvt_pk_bf16_f32 v123, v154, v155\n"
"v_cvt_pk_bf16_f32 v101, v158, v159\n"
"v_mfma_f32_16x16x32_bf16 v[20:23], v[132:135], v[224:227], v[20:23]\n"
"v_cvt_pk_bf16_f32 v100, v156, v157\n"
"v_cvt_pk_bf16_f32 v102, v168, v169\n"
"v_mfma_f32_16x16x32_bf16 v[20:23], v[128:131], a[252:255], v[20:23]\n"
"v_exp_f32 v179, v179\n"
"v_mfma_f32_16x16x32_bf16 a[228:231], v[124:127], a[252:255], a[228:231]\n"
"s_waitcnt lgkmcnt(2)\n"
"ds_read_b64_tr_b16 a[252:253], v235 offset:49152\n"
"ds_read_b64_tr_b16 v[226:227], v239 offset:53248\n"
"v_mfma_f32_16x16x32_bf16 a[216:219], v[120:123], a[244:247], a[216:219]\n"
"s_add_u32 m0, s44, 81920\n"
"ds_read_b64_tr_b16 v[224:225], v239 offset:49152\n"
"v_cvt_pk_bf16_f32 v119, v178, v179\n"
"v_smfmac_f32_16x16x64_bf16 v[196:199], a[240:243], v[96:103], v241\n"
"buffer_load_dwordx4 v240, s[20:23], s56 offen nt lds\n"
"v_mfma_f32_16x16x32_bf16 v[8:11], v[96:99], a[244:247], v[8:11]\n"
"s_waitcnt lgkmcnt(3)\n"
"ds_read_b64_tr_b16 a[254:255], v235 offset:53248\n"
"v_cvt_pk_bf16_f32 v118, v176, v177\n"
"v_mfma_f32_16x16x32_bf16 v[8:11], v[100:103], a[248:251], v[8:11]\n"
"v_exp_f32 v174, v174\n"
"v_smfmac_f32_16x16x64_bf16 v[200:203], a[240:243], v[112:119], v241\n"
"v_cvt_pk_bf16_f32 v109, v162, v163\n"
"v_cvt_pk_bf16_f32 v111, v174, v175\n"
"v_mfma_f32_16x16x32_bf16 v[40:43], v[104:107], a[244:247], v[40:43]\n"
"ds_read_b128 v[212:215], v229 offset:0\n"
"v_and_b32 v245, 0x3f0, v242\n"
"v_mfma_f32_16x16x32_bf16 v[40:43], v[108:111], a[248:251], v[40:43]\n"
"v_lshlrev_b32_e64 v244, 24, s43\n"
"v_mul_f32 v244, 0x45800000, v244\n"
"v_smfmac_f32_16x16x64_bf16 v[198:201], a[240:243], v[104:111], v241\n"
"ds_read_b128 v[208:211], v228 offset:0\n"
"v_add_f32 v196, v196, v197\n"
"v_mfma_f32_16x16x32_bf16 v[72:75], v[112:115], a[244:247], v[72:75]\n"
"ds_read_b64_tr_b16 a[244:245], v234 offset:49664\n"
"v_lshrrev_b32 v245, 1, v245\n"
"v_mfma_f32_16x16x32_bf16 v[72:75], v[116:119], a[248:251], v[72:75]\n"
"ds_read_b64_tr_b16 a[246:247], v234 offset:53760\n"
"v_sub_u32 v245, v242, v245\n"
"v_mfma_f32_16x16x32_bf16 v[202:205], a[240:243], v[120:123], v[202:205]\n"
"s_waitcnt lgkmcnt(4)\n"
"ds_read_b64_tr_b16 a[248:249], v238 offset:49664\n"
"v_add_f32 v200, v200, v201\n"
"v_mfma_f32_16x16x32_bf16 v[76:79], v[116:119], v[224:227], v[76:79]\n"
"v_add_f32 v198, v198, v199\n"
"v_add_f32 v254, v196, v198\n"
"v_mfma_f32_16x16x32_bf16 v[76:79], v[112:115], a[252:255], v[76:79]\n"
"ds_read_b64_tr_b16 a[250:251], v238 offset:53760\n"
"v_xor_b32 v250, 0x1000, v245\n"
"v_mfma_f32_16x16x32_bf16 v[44:47], v[108:111], v[224:227], v[44:47]\n"
"ds_read_b128 v[216:219], v228 offset:512\n"
"v_add_f32 v254, v254, v200\n"
"v_mfma_f32_16x16x32_bf16 v[44:47], v[104:107], a[252:255], v[44:47]\n"
"v_add_f32 v244, v244, v202\n"
"v_add_f32 v254, v254, v202\n"
"v_mfma_f32_16x16x32_bf16 v[12:15], v[100:103], v[224:227], v[12:15]\n"
"s_add_u32 s40, s56, s52\n"
"ds_read_b64_tr_b16 v[226:227], v239 offset:53760\n"
"v_min3_f32 v244, v244, v196, v198\n"
"v_mfma_f32_16x16x32_bf16 v[12:15], v[96:99], a[252:255], v[12:15]\n"
"buffer_load_dwordx4 v207, s[20:23], s40 offen offset:1024 nt lds\n"
"v_min_f32 v244, v244, v200\n"
"v_rcp_f32 v244, v244\n"
"v_mfma_f32_16x16x32_bf16 a[220:223], v[120:123], a[252:255], a[220:223]\n"
"s_waitcnt lgkmcnt(4)\n"
"v_fmac_f32 v254, 0x68800000, v244\n"
"v_cmp_nge_f32 vcc, s45, v254\n"
"s_cbranch_vccnz LSLOW_%=\nLBACK1_%=:\ns_mov_b64 vcc, s[64:65]\n"
"v_mfma_f32_16x16x32_bf16 a[232:235], v[120:123], a[244:247], a[232:235]\n"
"v_rcp_f32 v246, v196\n"
"v_accvgpr_mov_b32 a2, a98\n"
"v_accvgpr_mov_b32 a7, a103\n"
"v_accvgpr_mov_b32 a10, a106\n"
"v_accvgpr_mov_b32 a15, a111\n"
"v_accvgpr_mov_b32 a22, a118\n"
"v_accvgpr_mov_b32 a30, a126\n"
"v_bfe_u32 v149, v242, 8, 2\n"
"v_lshl_add_u32 v149, v149, 4, s43\n"
"v_mfma_f32_16x16x32_bf16 v[24:27], v[96:99], a[244:247], v[24:27]\n"
"s_waitcnt lgkmcnt(2)\n"
"ds_read_b128 v[124:127], v149 offset:0\n"
"v_accvgpr_mov_b32 a28, a124\n"
"v_mfma_f32_16x16x32_bf16 v[24:27], v[100:103], a[248:251], v[24:27]\n"
"ds_read_b128 v[132:135], v149 offset:512\n"
"v_mov_b32_dpp v246, v246 row_ror:12 row_mask:0x2 bank_mask:0xf\n"
"v_accvgpr_mov_b32 a3, a99\n"
"v_accvgpr_mov_b32 a12, a108\n"
"v_accvgpr_mov_b32 a18, a114\n"
"v_accvgpr_mov_b32 a19, a115\n"
"v_accvgpr_mov_b32 a26, a122\n"
"v_accvgpr_mov_b32 a36, a132\n"
"v_xor_b32 v148, 0x400, v243\n"
"v_xor_b32 v150, 64, v149\n"
"v_accvgpr_mov_b32 a124, a76\n"
"v_mfma_f32_16x16x32_bf16 v[56:59], v[104:107], a[244:247], v[56:59]\n"
"ds_read_b128 v[128:131], v150 offset:0\n"
"v_accvgpr_mov_b32 a39, a135\n"
"v_mfma_f32_16x16x32_bf16 v[56:59], v[108:111], a[248:251], v[56:59]\n"
"ds_read_b128 v[136:139], v150 offset:512\n"
"v_rcp_f32 v247, v198\n"
"v_mov_b32_dpp v246, v246 row_ror:8 row_mask:0x4 bank_mask:0xf\n"
"v_accvgpr_mov_b32 a0, a96\n"
"v_accvgpr_mov_b32 a1, a97\n"
"v_accvgpr_mov_b32 a4, a100\n"
"v_accvgpr_mov_b32 a5, a101\n"
"v_accvgpr_mov_b32 a8, a104\n"
"v_accvgpr_mov_b32 a16, a112\n"
"v_accvgpr_mov_b32 a25, a121\n"
"v_mfma_f32_16x16x32_bf16 v[88:91], v[112:115], a[244:247], v[88:91]\n"
"v_accvgpr_mov_b32 a20, a116\n"
"v_accvgpr_mov_b32 a29, a125\n"
"v_mfma_f32_16x16x32_bf16 v[88:91], v[116:119], a[248:251], v[88:91]\n"
"s_waitcnt lgkmcnt(3)\n"
"ds_read_b128 a[192:195], v243 offset:4096\n"
"ds_read_b128 a[200:203], v243 offset:6144\n"
"v_accvgpr_mov_b32 a6, a102\n"
"v_accvgpr_mov_b32 a9, a105\n"
"v_accvgpr_mov_b32 a11, a107\n"
"v_accvgpr_mov_b32 a13, a109\n"
"v_accvgpr_mov_b32 a14, a110\n"
"v_accvgpr_mov_b32 a17, a113\n"
"v_accvgpr_mov_b32 a23, a119\n"
"v_accvgpr_mov_b32 a31, a127\n"
"v_accvgpr_mov_b32 a33, a129\n"
"v_accvgpr_mov_b32 a35, a131\n"
"v_accvgpr_mov_b32 a44, a140\n"
"v_accvgpr_mov_b32 a47, a143\n"
"v_accvgpr_mov_b32 a107, a59\n"
"v_mfma_f32_16x16x32_bf16 v[180:183], v[124:127], a[0:3], 0\n"
"s_waitcnt lgkmcnt(3)\n"
"v_xor_b32 v244, 0x11000, v242\n"
"v_accvgpr_mov_b32 a132, a84\n"
"v_mfma_f32_16x16x32_bf16 v[180:183], v[128:131], a[4:7], v[180:183]\n"
"v_accvgpr_mov_b32 a34, a130\n"
"v_accvgpr_mov_b32 a43, a139\n"
"v_mfma_f32_16x16x32_bf16 v[180:183], v[132:135], a[8:11], v[180:183]\n"
"s_waitcnt lgkmcnt(2)\n"
"ds_read_b128 a[204:207], v148 offset:6144\n"
"v_accvgpr_mov_b32 a37, a133\n"
"v_mfma_f32_16x16x32_bf16 v[180:183], v[136:139], a[12:15], v[180:183]\n"
"ds_read_b128 a[196:199], v148 offset:4096\n"
"v_accvgpr_mov_b32 a21, a117\n"
"v_accvgpr_mov_b32 a24, a120\n"
"v_accvgpr_mov_b32 a32, a128\n"
"v_accvgpr_mov_b32 a38, a134\n"
"v_accvgpr_mov_b32 a40, a136\n"
"v_accvgpr_mov_b32 a42, a138\n"
"v_accvgpr_mov_b32 a46, a142\n"
"v_accvgpr_mov_b32 a117, a69\n"
"v_accvgpr_mov_b32 a111, a63\n"
"v_mfma_f32_16x16x32_bf16 v[184:187], v[124:127], a[16:19], 0\n"
"v_mov_b32_dpp v246, v246 row_ror:4 row_mask:0x8 bank_mask:0xf\n"
"v_accvgpr_mov_b32 a27, a123\n"
"v_mfma_f32_16x16x32_bf16 v[184:187], v[128:131], a[20:23], v[184:187]\n"
"v_mov_b32_dpp v247, v247 row_ror:12 row_mask:0x2 bank_mask:0xf\n"
"v_accvgpr_mov_b32 a97, a49\n"
"v_mfma_f32_16x16x32_bf16 v[184:187], v[132:135], a[24:27], v[184:187]\n"
"s_add_u32 s40, s56, s53\n"
"ds_read_b64_tr_b16 v[224:225], v239 offset:49664\n"
"v_accvgpr_mov_b32 a112, a64\n"
"v_mfma_f32_16x16x32_bf16 v[184:187], v[136:139], a[28:31], v[184:187]\n"
"buffer_load_dwordx4 v240, s[20:23], s40 offen offset:2048 nt lds\n"
"v_accvgpr_mov_b32 a41, a137\n"
"v_accvgpr_mov_b32 a126, a78\n"
"v_accvgpr_mov_b32 a114, a66\n"
"v_accvgpr_mov_b32 a137, a89\n"
"v_sub_f32 v180, s74, v180\n"
"v_sub_f32 v181, s74, v181\n"
"v_sub_f32 v183, s74, v183\n"
"v_mul_f32_dpp v26, v246, v26 row_newbcast:2 row_mask:0xf bank_mask:0xf\n"
"v_mfma_f32_16x16x32_bf16 v[188:191], v[124:127], a[32:35], 0\n"
"v_mov_b32_dpp v247, v247 row_ror:8 row_mask:0x4 bank_mask:0xf\n"
"v_accvgpr_mov_b32 a45, a141\n"
"v_mfma_f32_16x16x32_bf16 v[188:191], v[128:131], a[36:39], v[188:191]\n"
"v_mov_b32 v252, v192\n"
"v_accvgpr_mov_b32 a133, a85\n"
"v_mfma_f32_16x16x32_bf16 v[188:191], v[132:135], a[40:43], v[188:191]\n"
"v_accvgpr_mov_b32 a118, a70\n"
"v_accvgpr_mov_b32 a105, a57\n"
"v_mfma_f32_16x16x32_bf16 v[188:191], v[136:139], a[44:47], v[188:191]\n"
"s_waitcnt lgkmcnt(1)\n"
"v_accvgpr_mov_b32 a109, a61\n"
"v_mul_f32_dpp v11, v246, v11 row_newbcast:3 row_mask:0xf bank_mask:0xf\n"
"v_mfma_f32_16x16x32_bf16 v[192:195], v[124:127], a[192:195], 0\n"
"ds_read_b64_tr_b16 a[252:253], v235 offset:49664\n"
"v_sub_f32 v185, s74, v185\n"
"v_mfma_f32_16x16x32_bf16 v[192:195], v[128:131], a[196:199], v[192:195]\n"
"ds_read_b64_tr_b16 a[248:249], v236 offset:49152\n"
"v_sub_f32 v182, s74, v182\n"
"v_mfma_f32_16x16x32_bf16 v[192:195], v[132:135], a[200:203], v[192:195]\n"
"v_accvgpr_mov_b32 a138, a90\n"
"v_sub_f32 v184, s74, v184\n"
"v_mfma_f32_16x16x32_bf16 v[192:195], v[136:139], a[204:207], v[192:195]\n"
"ds_read_b64_tr_b16 a[254:255], v235 offset:53760\n"
"ds_read_b64_tr_b16 a[246:247], v232 offset:53248\n"
"v_mov_b32 v253, v202\n"
"v_accvgpr_mov_b32 a116, a68\n"
"v_accvgpr_mov_b32 a125, a77\n"
"v_accvgpr_mov_b32 a113, a65\n"
"v_accvgpr_mov_b32 a128, a80\n"
"v_accvgpr_mov_b32 a130, a82\n"
"v_accvgpr_mov_b32 a136, a88\n"
"v_sub_f32 v186, s74, v186\n"
"v_mfma_f32_16x16x32_bf16 v[152:155], v[208:211], a[0:3], v[180:183]\n"
"v_accvgpr_mov_b32 a103, a55\n"
"v_accvgpr_mov_b32 a120, a72\n"
"v_mfma_f32_16x16x32_bf16 v[152:155], v[212:215], a[4:7], v[152:155]\n"
"v_accvgpr_mov_b32 a141, a93\n"
"v_sub_f32 v187, s74, v187\n"
"v_mfma_f32_16x16x32_bf16 v[152:155], v[216:219], a[8:11], v[152:155]\n"
"s_add_u32 s40, s56, s54\n"
"v_accvgpr_mov_b32 a127, a79\n"
"v_accvgpr_mov_b32 a140, a92\n"
"v_mfma_f32_16x16x32_bf16 v[152:155], v[220:223], a[12:15], v[152:155]\n"
"buffer_load_dwordx4 v207, s[20:23], s40 offen offset:3072 nt lds\n"
"v_mov_b32_dpp v247, v247 row_ror:4 row_mask:0x8 bank_mask:0xf\n"
"v_accvgpr_mov_b32 a121, a73\n"
"v_accvgpr_mov_b32 a122, a74\n"
"v_accvgpr_mov_b32 a123, a75\n"
"v_sub_f32 v189, s74, v189\n"
"v_sub_f32 v190, s74, v190\n"
"v_sub_f32 v191, s74, v191\n"
"v_sub_f32 v192, s74, v192\n"
"v_mfma_f32_16x16x32_bf16 v[156:159], v[216:219], a[24:27], v[184:187]\n"
"s_add_u32 s55, s55, s59\n"
"v_accvgpr_mov_b32 a143, a95\n"
"v_accvgpr_mov_b32 a115, a67\n"
"v_mfma_f32_16x16x32_bf16 v[156:159], v[220:223], a[28:31], v[156:159]\n"
"s_or_b32 s55, s55, s58\n"
"v_accvgpr_mov_b32 a101, a53\n"
"v_sub_f32 v188, s74, v188\n"
"v_mfma_f32_16x16x32_bf16 v[156:159], v[208:211], a[16:19], v[156:159]\n"
"s_xor_b32 s56, s55, s57\n"
"v_exp_f32 v153, v153\n"
"v_mfma_f32_16x16x32_bf16 v[156:159], v[212:215], a[20:23], v[156:159]\n"
"ds_write_b64 v250, v[252:253] offset:52224\n"
"v_accvgpr_mov_b32 a100, a52\n"
"v_accvgpr_mov_b32 a135, a87\n"
"v_accvgpr_mov_b32 a98, a50\n"
"v_sub_f32 v193, s74, v193\n"
"v_sub_f32 v194, s74, v194\n"
"v_exp_f32 v152, v152\n"
"v_mfma_f32_16x16x32_bf16 v[160:163], v[208:211], a[32:35], v[188:191]\n"
"v_accvgpr_mov_b32 a110, a62\n"
"v_accvgpr_mov_b32 a96, a48\n"
"v_mfma_f32_16x16x32_bf16 v[160:163], v[212:215], a[36:39], v[160:163]\n"
"v_rcp_f32 v206, v200\n"
"v_mfma_f32_16x16x32_bf16 v[160:163], v[216:219], a[40:43], v[160:163]\n"
"v_accvgpr_mov_b32 a142, a94\n"
"v_sub_f32 v195, s74, v195\n"
"v_mfma_f32_16x16x32_bf16 v[160:163], v[220:223], a[44:47], v[160:163]\n"
"ds_write_b128 v244, a[216:219] offset:32768\n"
"v_accvgpr_mov_b32 a102, a54\n"
"v_accvgpr_mov_b32 a108, a60\n"
"v_accvgpr_mov_b32 a99, a51\n"
"v_accvgpr_mov_b32 a129, a81\n"
"v_mul_f32_dpp v25, v246, v25 row_newbcast:1 row_mask:0xf bank_mask:0xf\n"
"v_mfma_f32_16x16x32_bf16 v[164:167], v[216:219], a[200:203], v[192:195]\n"
"v_mov_b32_dpp v206, v206 row_ror:12 row_mask:0x2 bank_mask:0xf\n"
"v_accvgpr_mov_b32 a119, a71\n"
"v_mfma_f32_16x16x32_bf16 v[164:167], v[220:223], a[204:207], v[164:167]\n"
"v_accvgpr_mov_b32 a131, a83\n"
"v_accvgpr_mov_b32 a139, a91\n"
"v_mfma_f32_16x16x32_bf16 v[164:167], v[208:211], a[192:195], v[164:167]\n"
"ds_read_b128 v[208:211], v228 offset:4096\n"
"v_accvgpr_mov_b32 a106, a58\n"
"v_mfma_f32_16x16x32_bf16 v[164:167], v[212:215], a[196:199], v[164:167]\n"
"s_waitcnt lgkmcnt(2)\n"
"ds_read_b128 v[216:219], v228 offset:4608\n"
"ds_read_b128 v[220:223], v229 offset:4608\n"
"v_mov_b32_dpp v206, v206 row_ror:8 row_mask:0x4 bank_mask:0xf\n"
"v_accvgpr_mov_b32 a134, a86\n"
"v_accvgpr_mov_b32 a104, a56\n"
"v_exp_f32 v155, v155\n"
"v_exp_f32 v160, v160\n"
"s_branch LTC_%=\n"
"LTH2_%=:\n"
"v_mfma_f32_16x16x32_bf16 a[224:227], v[124:127], a[244:247], a[224:227]\n"
"v_exp_f32 v171, v171\n"
"v_mfma_f32_16x16x32_bf16 v[16:19], v[128:131], a[244:247], v[16:19]\n"
"s_waitcnt vmcnt(8)\n"
"v_exp_f32 v172, v172\n"
"v_mfma_f32_16x16x32_bf16 v[16:19], v[132:135], a[248:251], v[16:19]\n"
"s_barrier\n"
"v_mfma_f32_16x16x32_bf16 v[48:51], v[136:139], a[244:247], v[48:51]\n"
"v_exp_f32 v169, v169\n"
"v_mfma_f32_16x16x32_bf16 v[48:51], v[140:143], a[248:251], v[48:51]\n"
"ds_read_b128 v[220:223], v229 offset:512\n"
"v_cvt_pk_bf16_f32 v117, v166, v167\n"
"v_exp_f32 v176, v176\n"
"v_exp_f32 v177, v177\n"
"v_exp_f32 v178, v178\n"
"v_mfma_f32_16x16x32_bf16 v[80:83], v[144:147], a[244:247], v[80:83]\n"
"ds_read_b64_tr_b16 a[244:245], v234 offset:49152\n"
"v_cvt_pk_bf16_f32 v108, v160, v161\n"
"v_mfma_f32_16x16x32_bf16 v[80:83], v[148:151], a[248:251], v[80:83]\n"
"ds_read_b64_tr_b16 a[246:247], v234 offset:53248\n"
"ds_read_b64_tr_b16 a[248:249], v238 offset:49152\n"
"v_exp_f32 v162, v162\n"
"v_exp_f32 v168, v168\n"
"v_exp_f32 v173, v173\n"
"v_exp_f32 v175, v175\n"
"v_mfma_f32_16x16x32_bf16 v[84:87], v[148:151], v[224:227], v[84:87]\n"
"ds_read_b64_tr_b16 a[250:251], v238 offset:53248\n"
"v_cvt_pk_bf16_f32 v116, v164, v165\n"
"v_mfma_f32_16x16x32_bf16 v[84:87], v[144:147], a[252:255], v[84:87]\n"
"v_exp_f32 v170, v170\n"
"v_mfma_f32_16x16x32_bf16 v[52:55], v[140:143], v[224:227], v[52:55]\n"
"v_cvt_pk_bf16_f32 v103, v170, v171\n"
"v_cvt_pk_bf16_f32 v110, v172, v173\n"
"v_mfma_f32_16x16x32_bf16 v[52:55], v[136:139], a[252:255], v[52:55]\n"
"v_cvt_pk_bf16_f32 v123, v154, v155\n"
"v_cvt_pk_bf16_f32 v101, v158, v159\n"
"v_mfma_f32_16x16x32_bf16 v[20:23], v[132:135], v[224:227], v[20:23]\n"
"v_cvt_pk_bf16_f32 v100, v156, v157\n"
"v_cvt_pk_bf16_f32 v102, v168, v169\n"
"v_mfma_f32_16x16x32_bf16 v[20:23], v[128:131], a[252:255], v[20:23]\n"
"v_exp_f32 v179, v179\n"
"v_mfma_f32_16x16x32_bf16 a[228:231], v[124:127], a[252:255], a[228:231]\n"
"s_waitcnt lgkmcnt(2)\n"
"ds_read_b64_tr_b16 a[252:253], v235 offset:49152\n"
"ds_read_b64_tr_b16 v[226:227], v239 offset:53248\n"
"v_mfma_f32_16x16x32_bf16 a[216:219], v[120:123], a[244:247], a[216:219]\n"
"s_add_u32 m0, s44, 81920\n"
"ds_read_b64_tr_b16 v[224:225], v239 offset:49152\n"
"v_cvt_pk_bf16_f32 v119, v178, v179\n"
"v_smfmac_f32_16x16x64_bf16 v[196:199], a[240:243], v[96:103], v241\n"
"buffer_load_dwordx4 v240, s[20:23], s56 offen nt lds\n"
"v_mfma_f32_16x16x32_bf16 v[8:11], v[96:99], a[244:247], v[8:11]\n"
"s_waitcnt lgkmcnt(3)\n"
"ds_read_b64_tr_b16 a[254:255], v235 offset:53248\n"
"v_cvt_pk_bf16_f32 v118, v176, v177\n"
"v_mfma_f32_16x16x32_bf16 v[8:11], v[100:103], a[248:251], v[8:11]\n"
"v_exp_f32 v174, v174\n"
"v_smfmac_f32_16x16x64_bf16 v[200:203], a[240:243], v[112:119], v241\n"
"v_cvt_pk_bf16_f32 v109, v162, v163\n"
"v_cvt_pk_bf16_f32 v111, v174, v175\n"
"v_mfma_f32_16x16x32_bf16 v[40:43], v[104:107], a[244:247], v[40:43]\n"
"ds_read_b128 v[212:215], v229 offset:0\n"
"v_and_b32 v245, 0x3f0, v242\n"
"v_mfma_f32_16x16x32_bf16 v[40:43], v[108:111], a[248:251], v[40:43]\n"
"v_lshlrev_b32_e64 v244, 24, s43\n"
"v_mul_f32 v244, 0x45800000, v244\n"
"v_smfmac_f32_16x16x64_bf16 v[198:201], a[240:243], v[104:111], v241\n"
"ds_read_b128 v[208:211], v228 offset:0\n"
"v_add_f32 v196, v196, v197\n"
"v_mfma_f32_16x16x32_bf16 v[72:75], v[112:115], a[244:247], v[72:75]\n"
"ds_read_b64_tr_b16 a[244:245], v234 offset:49664\n"
"v_lshrrev_b32 v245, 1, v245\n"
"v_mfma_f32_16x16x32_bf16 v[72:75], v[116:119], a[248:251], v[72:75]\n"
"ds_read_b64_tr_b16 a[246:247], v234 offset:53760\n"
"v_sub_u32 v245, v242, v245\n"
"v_mfma_f32_16x16x32_bf16 v[202:205], a[240:243], v[120:123], v[202:205]\n"
"s_waitcnt lgkmcnt(4)\n"
"ds_read_b64_tr_b16 a[248:249], v238 offset:49664\n"
"v_add_f32 v200, v200, v201\n"
"v_mfma_f32_16x16x32_bf16 v[76:79], v[116:119], v[224:227], v[76:79]\n"
"v_add_f32 v198, v198, v199\n"
"v_add_f32 v254, v196, v198\n"
"v_mfma_f32_16x16x32_bf16 v[76:79], v[112:115], a[252:255], v[76:79]\n"
"ds_read_b64_tr_b16 a[250:251], v238 offset:53760\n"
"v_xor_b32 v250, 0x1000, v245\n"
"v_mfma_f32_16x16x32_bf16 v[44:47], v[108:111], v[224:227], v[44:47]\n"
"ds_read_b128 v[216:219], v228 offset:512\n"
"v_add_f32 v254, v254, v200\n"
"v_mfma_f32_16x16x32_bf16 v[44:47], v[104:107], a[252:255], v[44:47]\n"
"v_add_f32 v244, v244, v202\n"
"v_add_f32 v254, v254, v202\n"
"v_mfma_f32_16x16x32_bf16 v[12:15], v[100:103], v[224:227], v[12:15]\n"
"s_add_u32 s40, s56, s52\n"
"ds_read_b64_tr_b16 v[226:227], v239 offset:53760\n"
"v_min3_f32 v244, v244, v196, v198\n"
"v_mfma_f32_16x16x32_bf16 v[12:15], v[96:99], a[252:255], v[12:15]\n"
"buffer_load_dwordx4 v207, s[20:23], s40 offen offset:1024 nt lds\n"
"v_min_f32 v244, v244, v200\n"
"v_rcp_f32 v244, v244\n"
"v_mfma_f32_16x16x32_bf16 a[220:223], v[120:123], a[252:255], a[220:223]\n"
"s_waitcnt lgkmcnt(4)\n"
"v_fmac_f32 v254, 0x68800000, v244\n"
"v_cmp_nge_f32 vcc, s45, v254\n"
"s_cbranch_vccnz LSLOW_%=\nLBACK2_%=:\ns_mov_b64 vcc, s[64:65]\n"
"v_mfma_f32_16x16x32_bf16 a[232:235], v[120:123], a[244:247], a[232:235]\n"
"v_rcp_f32 v246, v196\n"
"v_accvgpr_mov_b32 a2, a146\n"
"v_accvgpr_mov_b32 a7, a151\n"
"v_accvgpr_mov_b32 a10, a154\n"
"v_accvgpr_mov_b32 a15, a159\n"
"v_accvgpr_mov_b32 a22, a166\n"
"v_accvgpr_mov_b32 a30, a174\n"
"v_bfe_u32 v149, v242, 8, 2\n"
"v_lshl_add_u32 v149, v149, 4, s43\n"
"v_mfma_f32_16x16x32_bf16 v[24:27], v[96:99], a[244:247], v[24:27]\n"
"s_waitcnt lgkmcnt(2)\n"
"ds_read_b128 v[124:127], v149 offset:0\n"
"v_accvgpr_mov_b32 a28, a172\n"
"v_mfma_f32_16x16x32_bf16 v[24:27], v[100:103], a[248:251], v[24:27]\n"
"ds_read_b128 v[132:135], v149 offset:512\n"
"v_mov_b32_dpp v246, v246 row_ror:12 row_mask:0x2 bank_mask:0xf\n"
"v_accvgpr_mov_b32 a3, a147\n"
"v_accvgpr_mov_b32 a12, a156\n"
"v_accvgpr_mov_b32 a18, a162\n"
"v_accvgpr_mov_b32 a19, a163\n"
"v_accvgpr_mov_b32 a26, a170\n"
"v_accvgpr_mov_b32 a36, a180\n"
"v_xor_b32 v148, 0x400, v243\n"
"v_xor_b32 v150, 64, v149\n"
"v_accvgpr_mov_b32 a172, a76\n"
"v_mfma_f32_16x16x32_bf16 v[56:59], v[104:107], a[244:247], v[56:59]\n"
"ds_read_b128 v[128:131], v150 offset:0\n"
"v_accvgpr_mov_b32 a39, a183\n"
"v_mfma_f32_16x16x32_bf16 v[56:59], v[108:111], a[248:251], v[56:59]\n"
"ds_read_b128 v[136:139], v150 offset:512\n"
"v_rcp_f32 v247, v198\n"
"v_mov_b32_dpp v246, v246 row_ror:8 row_mask:0x4 bank_mask:0xf\n"
"v_accvgpr_mov_b32 a0, a144\n"
"v_accvgpr_mov_b32 a1, a145\n"
"v_accvgpr_mov_b32 a4, a148\n"
"v_accvgpr_mov_b32 a5, a149\n"
"v_accvgpr_mov_b32 a8, a152\n"
"v_accvgpr_mov_b32 a16, a160\n"
"v_accvgpr_mov_b32 a25, a169\n"
"v_mfma_f32_16x16x32_bf16 v[88:91], v[112:115], a[244:247], v[88:91]\n"
"v_accvgpr_mov_b32 a20, a164\n"
"v_accvgpr_mov_b32 a29, a173\n"
"v_mfma_f32_16x16x32_bf16 v[88:91], v[116:119], a[248:251], v[88:91]\n"
"s_waitcnt lgkmcnt(3)\n"
"ds_read_b128 a[192:195], v243 offset:4096\n"
"ds_read_b128 a[200:203], v243 offset:6144\n"
"v_accvgpr_mov_b32 a6, a150\n"
"v_accvgpr_mov_b32 a9, a153\n"
"v_accvgpr_mov_b32 a11, a155\n"
"v_accvgpr_mov_b32 a13, a157\n"
"v_accvgpr_mov_b32 a14, a158\n"
"v_accvgpr_mov_b32 a17, a161\n"
"v_accvgpr_mov_b32 a23, a167\n"
"v_accvgpr_mov_b32 a31, a175\n"
"v_accvgpr_mov_b32 a33, a177\n"
"v_accvgpr_mov_b32 a35, a179\n"
"v_accvgpr_mov_b32 a44, a188\n"
"v_accvgpr_mov_b32 a47, a191\n"
"v_accvgpr_mov_b32 a155, a59\n"
"v_mfma_f32_16x16x32_bf16 v[180:183], v[124:127], a[0:3], 0\n"
"s_waitcnt lgkmcnt(3)\n"
"v_xor_b32 v244, 0x11000, v242\n"
"v_accvgpr_mov_b32 a180, a84\n"
"v_mfma_f32_16x16x32_bf16 v[180:183], v[128:131], a[4:7], v[180:183]\n"
"v_accvgpr_mov_b32 a34, a178\n"
"v_accvgpr_mov_b32 a43, a187\n"
"v_mfma_f32_16x16x32_bf16 v[180:183], v[132:135], a[8:11], v[180:183]\n"
"s_waitcnt lgkmcnt(2)\n"
"ds_read_b128 a[204:207], v148 offset:6144\n"
"v_accvgpr_mov_b32 a37, a181\n"
"v_mfma_f32_16x16x32_bf16 v[180:183], v[136:139], a[12:15], v[180:183]\n"
"ds_read_b128 a[196:199], v148 offset:4096\n"
"v_accvgpr_mov_b32 a21, a165\n"
"v_accvgpr_mov_b32 a24, a168\n"
"v_accvgpr_mov_b32 a32, a176\n"
"v_accvgpr_mov_b32 a38, a182\n"
"v_accvgpr_mov_b32 a40, a184\n"
"v_accvgpr_mov_b32 a42, a186\n"
"v_accvgpr_mov_b32 a46, a190\n"
"v_accvgpr_mov_b32 a165, a69\n"
"v_accvgpr_mov_b32 a159, a63\n"
"v_mfma_f32_16x16x32_bf16 v[184:187], v[124:127], a[16:19], 0\n"
"v_mov_b32_dpp v246, v246 row_ror:4 row_mask:0x8 bank_mask:0xf\n"
"v_accvgpr_mov_b32 a27, a171\n"
"v_mfma_f32_16x16x32_bf16 v[184:187], v[128:131], a[20:23], v[184:187]\n"
"v_mov_b32_dpp v247, v247 row_ror:12 row_mask:0x2 bank_mask:0xf\n"
"v_accvgpr_mov_b32 a145, a49\n"
"v_mfma_f32_16x16x32_bf16 v[184:187], v[132:135], a[24:27], v[184:187]\n"
"s_add_u32 s40, s56, s53\n"
"ds_read_b64_tr_b16 v[224:225], v239 offset:49664\n"
"v_accvgpr_mov_b32 a160, a64\n"
"v_mfma_f32_16x16x32_bf16 v[184:187], v[136:139], a[28:31], v[184:187]\n"
"buffer_load_dwordx4 v240, s[20:23], s40 offen offset:2048 nt lds\n"
"v_accvgpr_mov_b32 a41, a185\n"
"v_accvgpr_mov_b32 a174, a78\n"
"v_accvgpr_mov_b32 a162, a66\n"
"v_accvgpr_mov_b32 a185, a89\n"
"v_sub_f32 v180, s74, v180\n"
"v_sub_f32 v181, s74, v181\n"
"v_sub_f32 v183, s74, v183\n"
"v_mul_f32_dpp v26, v246, v26 row_newbcast:2 row_mask:0xf bank_mask:0xf\n"
"v_mfma_f32_16x16x32_bf16 v[188:191], v[124:127], a[32:35], 0\n"
"v_mov_b32_dpp v247, v247 row_ror:8 row_mask:0x4 bank_mask:0xf\n"
"v_accvgpr_mov_b32 a45, a189\n"
"v_mfma_f32_16x16x32_bf16 v[188:191], v[128:131], a[36:39], v[188:191]\n"
"v_mov_b32 v252, v192\n"
"v_accvgpr_mov_b32 a181, a85\n"
"v_mfma_f32_16x16x32_bf16 v[188:191], v[132:135], a[40:43], v[188:191]\n"
"v_accvgpr_mov_b32 a166, a70\n"
"v_accvgpr_mov_b32 a153, a57\n"
"v_mfma_f32_16x16x32_bf16 v[188:191], v[136:139], a[44:47], v[188:191]\n"
"s_waitcnt lgkmcnt(1)\n"
"v_accvgpr_mov_b32 a157, a61\n"
"v_mul_f32_dpp v11, v246, v11 row_newbcast:3 row_mask:0xf bank_mask:0xf\n"
"v_mfma_f32_16x16x32_bf16 v[192:195], v[124:127], a[192:195], 0\n"
"ds_read_b64_tr_b16 a[252:253], v235 offset:49664\n"
"v_sub_f32 v185, s74, v185\n"
"v_mfma_f32_16x16x32_bf16 v[192:195], v[128:131], a[196:199], v[192:195]\n"
"ds_read_b64_tr_b16 a[248:249], v236 offset:49152\n"
"v_sub_f32 v182, s74, v182\n"
"v_mfma_f32_16x16x32_bf16 v[192:195], v[132:135], a[200:203], v[192:195]\n"
"v_accvgpr_mov_b32 a186, a90\n"
"v_sub_f32 v184, s74, v184\n"
"v_mfma_f32_16x16x32_bf16 v[192:195], v[136:139], a[204:207], v[192:195]\n"
"ds_read_b64_tr_b16 a[254:255], v235 offset:53760\n"
"ds_read_b64_tr_b16 a[246:247], v232 offset:53248\n"
"v_mov_b32 v253, v202\n"
"v_accvgpr_mov_b32 a164, a68\n"
"v_accvgpr_mov_b32 a173, a77\n"
"v_accvgpr_mov_b32 a161, a65\n"
"v_accvgpr_mov_b32 a176, a80\n"
"v_accvgpr_mov_b32 a178, a82\n"
"v_accvgpr_mov_b32 a184, a88\n"
"v_sub_f32 v186, s74, v186\n"
"v_mfma_f32_16x16x32_bf16 v[152:155], v[208:211], a[0:3], v[180:183]\n"
"v_accvgpr_mov_b32 a151, a55\n"
"v_accvgpr_mov_b32 a168, a72\n"
"v_mfma_f32_16x16x32_bf16 v[152:155], v[212:215], a[4:7], v[152:155]\n"
"v_accvgpr_mov_b32 a189, a93\n"
"v_sub_f32 v187, s74, v187\n"
"v_mfma_f32_16x16x32_bf16 v[152:155], v[216:219], a[8:11], v[152:155]\n"
"s_add_u32 s40, s56, s54\n"
"v_accvgpr_mov_b32 a175, a79\n"
"v_accvgpr_mov_b32 a188, a92\n"
"v_mfma_f32_16x16x32_bf16 v[152:155], v[220:223], a[12:15], v[152:155]\n"
"buffer_load_dwordx4 v207, s[20:23], s40 offen offset:3072 nt lds\n"
"v_mov_b32_dpp v247, v247 row_ror:4 row_mask:0x8 bank_mask:0xf\n"
"v_accvgpr_mov_b32 a169, a73\n"
"v_accvgpr_mov_b32 a170, a74\n"
"v_accvgpr_mov_b32 a171, a75\n"
"v_sub_f32 v189, s74, v189\n"
"v_sub_f32 v190, s74, v190\n"
"v_sub_f32 v191, s74, v191\n"
"v_sub_f32 v192, s74, v192\n"
"v_mfma_f32_16x16x32_bf16 v[156:159], v[216:219], a[24:27], v[184:187]\n"
"s_add_u32 s55, s55, s59\n"
"v_accvgpr_mov_b32 a191, a95\n"
"v_accvgpr_mov_b32 a163, a67\n"
"v_mfma_f32_16x16x32_bf16 v[156:159], v[220:223], a[28:31], v[156:159]\n"
"s_or_b32 s55, s55, s58\n"
"v_accvgpr_mov_b32 a149, a53\n"
"v_sub_f32 v188, s74, v188\n"
"v_mfma_f32_16x16x32_bf16 v[156:159], v[208:211], a[16:19], v[156:159]\n"
"s_xor_b32 s56, s55, s57\n"
"v_exp_f32 v153, v153\n"
"v_mfma_f32_16x16x32_bf16 v[156:159], v[212:215], a[20:23], v[156:159]\n"
"ds_write_b64 v250, v[252:253] offset:52224\n"
"v_accvgpr_mov_b32 a148, a52\n"
"v_accvgpr_mov_b32 a183, a87\n"
"v_accvgpr_mov_b32 a146, a50\n"
"v_sub_f32 v193, s74, v193\n"
"v_sub_f32 v194, s74, v194\n"
"v_exp_f32 v152, v152\n"
"v_mfma_f32_16x16x32_bf16 v[160:163], v[208:211], a[32:35], v[188:191]\n"
"v_accvgpr_mov_b32 a158, a62\n"
"v_accvgpr_mov_b32 a144, a48\n"
"v_mfma_f32_16x16x32_bf16 v[160:163], v[212:215], a[36:39], v[160:163]\n"
"v_rcp_f32 v206, v200\n"
"v_mfma_f32_16x16x32_bf16 v[160:163], v[216:219], a[40:43], v[160:163]\n"
"v_accvgpr_mov_b32 a190, a94\n"
"v_sub_f32 v195, s74, v195\n"
"v_mfma_f32_16x16x32_bf16 v[160:163], v[220:223], a[44:47], v[160:163]\n"
"ds_write_b128 v244, a[216:219] offset:32768\n"
"v_accvgpr_mov_b32 a150, a54\n"
"v_accvgpr_mov_b32 a156, a60\n"
"v_accvgpr_mov_b32 a147, a51\n"
"v_accvgpr_mov_b32 a177, a81\n"
"v_mul_f32_dpp v25, v246, v25 row_newbcast:1 row_mask:0xf bank_mask:0xf\n"
"v_mfma_f32_16x16x32_bf16 v[164:167], v[216:219], a[200:203], v[192:195]\n"
"v_mov_b32_dpp v206, v206 row_ror:12 row_mask:0x2 bank_mask:0xf\n"
"v_accvgpr_mov_b32 a167, a71\n"
"v_mfma_f32_16x16x32_bf16 v[164:167], v[220:223], a[204:207], v[164:167]\n"
"v_accvgpr_mov_b32 a179, a83\n"
"v_accvgpr_mov_b32 a187, a91\n"
"v_mfma_f32_16x16x32_bf16 v[164:167], v[208:211], a[192:195], v[164:167]\n"
"ds_read_b128 v[208:211], v228 offset:4096\n"
"v_accvgpr_mov_b32 a154, a58\n"
"v_mfma_f32_16x16x32_bf16 v[164:167], v[212:215], a[196:199], v[164:167]\n"
"s_waitcnt lgkmcnt(2)\n"
"ds_read_b128 v[216:219], v228 offset:4608\n"
"ds_read_b128 v[220:223], v229 offset:4608\n"
"v_mov_b32_dpp v206, v206 row_ror:8 row_mask:0x4 bank_mask:0xf\n"
"v_accvgpr_mov_b32 a182, a86\n"
"v_accvgpr_mov_b32 a152, a56\n"
"v_exp_f32 v155, v155\n"
"v_exp_f32 v160, v160\n"
"LTC_%=:\n"
"s_waitcnt lgkmcnt(4)\n"
"v_mfma_f32_16x16x32_bf16 v[92:95], v[116:119], v[224:227], v[92:95]\n"
"v_exp_f32 v154, v154\n"
"v_mfma_f32_16x16x32_bf16 v[92:95], v[112:115], a[252:255], v[92:95]\n"
"ds_read_b128 v[212:215], v229 offset:4096\n"
"v_mul_f32_dpp v24, v246, v24 row_newbcast:0 row_mask:0xf bank_mask:0xf\n"
"v_mfma_f32_16x16x32_bf16 v[60:63], v[108:111], v[224:227], v[60:63]\n"
"s_add_u32 m0, s44, 32768\n"
"v_exp_f32 v163, v163\n"
"v_mfma_f32_16x16x32_bf16 v[60:63], v[104:107], a[252:255], v[60:63]\n"
"buffer_load_dwordx4 v240, s[16:19], s56 offen nt lds\n"
"v_mul_f32_dpp v8, v246, v8 row_newbcast:0 row_mask:0xf bank_mask:0xf\n"
"v_exp_f32 v156, v156\n"
"v_exp_f32 v158, v158\n"
"v_exp_f32 v161, v161\n"
"v_exp_f32 v165, v165\n"
"v_mfma_f32_16x16x32_bf16 v[28:31], v[100:103], v[224:227], v[28:31]\n"
"v_cvt_pk_bf16_f32 v128, v152, v153\n"
"v_cvt_pk_bf16_f32 v129, v154, v155\n"
"v_mfma_f32_16x16x32_bf16 v[28:31], v[96:99], a[252:255], v[28:31]\n"
"v_mov_b32_dpp v206, v206 row_ror:4 row_mask:0x8 bank_mask:0xf\n"
"v_mul_f32_dpp v9, v246, v9 row_newbcast:1 row_mask:0xf bank_mask:0xf\n"
"v_mfma_f32_16x16x32_bf16 a[236:239], v[120:123], a[252:255], a[236:239]\n"
"s_waitcnt lgkmcnt(2)\n"
"v_exp_f32 v162, v162\n"
"v_mfma_f32_16x16x32_bf16 v[168:171], v[216:219], a[8:11], v[180:183]\n"
"s_waitcnt lgkmcnt(1)\n"
"v_exp_f32 v167, v167\n"
"v_mfma_f32_16x16x32_bf16 v[168:171], v[220:223], a[12:15], v[168:171]\n"
"v_mul_f32_dpp v12, v246, v12 row_newbcast:0 row_mask:0xf bank_mask:0xf\n"
"v_mul_f32_dpp v46, v247, v46 row_newbcast:2 row_mask:0xf bank_mask:0xf\n"
"v_mfma_f32_16x16x32_bf16 v[168:171], v[208:211], a[0:3], v[168:171]\n"
"s_waitcnt lgkmcnt(0)\n"
"ds_read_b64_tr_b16 a[244:245], v232 offset:49152\n"
"v_cvt_pk_bf16_f32 v145, v162, v163\n"
"v_mfma_f32_16x16x32_bf16 v[168:171], v[212:215], a[4:7], v[168:171]\n"
"v_exp_f32 v159, v159\n"
"v_mfma_f32_16x16x32_bf16 v[172:175], v[208:211], a[16:19], v[184:187]\n"
"ds_read_b64_tr_b16 a[252:253], v233 offset:49152\n"
"v_mul_f32_dpp v72, v206, v72 row_newbcast:0 row_mask:0xf bank_mask:0xf\n"
"v_mfma_f32_16x16x32_bf16 v[172:175], v[212:215], a[20:23], v[172:175]\n"
"v_exp_f32 v164, v164\n"
"v_mfma_f32_16x16x32_bf16 v[172:175], v[216:219], a[24:27], v[172:175]\n"
"ds_read_b64_tr_b16 a[250:251], v236 offset:53248\n"
"v_mul_f32_dpp v14, v246, v14 row_newbcast:2 row_mask:0xf bank_mask:0xf\n"
"v_mfma_f32_16x16x32_bf16 v[172:175], v[220:223], a[28:31], v[172:175]\n"
"ds_write_b128 v244, a[220:223] offset:33792\n"
"v_mul_f32_dpp v78, v206, v78 row_newbcast:2 row_mask:0xf bank_mask:0xf\n"
"v_exp_f32 v157, v157\n"
"v_mul_f32_dpp v89, v206, v89 row_newbcast:1 row_mask:0xf bank_mask:0xf\n"
"v_exp_f32 v168, v168\n"
"v_mfma_f32_16x16x32_bf16 v[176:179], v[216:219], a[40:43], v[188:191]\n"
"v_mul_f32_dpp v10, v246, v10 row_newbcast:2 row_mask:0xf bank_mask:0xf\n"
"v_mul_f32_dpp v40, v247, v40 row_newbcast:0 row_mask:0xf bank_mask:0xf\n"
"v_mfma_f32_16x16x32_bf16 v[176:179], v[220:223], a[44:47], v[176:179]\n"
"v_cvt_pk_bf16_f32 v137, v158, v159\n"
"v_mul_f32_dpp v28, v246, v28 row_newbcast:0 row_mask:0xf bank_mask:0xf\n"
"v_mfma_f32_16x16x32_bf16 v[176:179], v[208:211], a[32:35], v[176:179]\n"
"s_add_u32 s40, s56, s52\n"
"v_mul_f32_dpp v73, v206, v73 row_newbcast:1 row_mask:0xf bank_mask:0xf\n"
"v_cvt_pk_bf16_f32 v136, v156, v157\n"
"v_mfma_f32_16x16x32_bf16 v[176:179], v[212:215], a[36:39], v[176:179]\n"
"buffer_load_dwordx4 v207, s[16:19], s40 offen offset:1024 nt lds\n"
"v_mfma_f32_16x16x32_bf16 v[152:155], v[208:211], a[192:195], v[192:195]\n"
"v_mul_f32_dpp v76, v206, v76 row_newbcast:0 row_mask:0xf bank_mask:0xf\n"
"v_mul_f32_dpp v88, v206, v88 row_newbcast:0 row_mask:0xf bank_mask:0xf\n"
"v_mfma_f32_16x16x32_bf16 v[152:155], v[212:215], a[196:199], v[152:155]\n"
"ds_read_b128 v[208:211], v230 offset:0\n"
"v_cvt_pk_bf16_f32 v144, v160, v161\n"
"v_mfma_f32_16x16x32_bf16 v[152:155], v[216:219], a[200:203], v[152:155]\n"
"v_mul_f32_dpp v93, v206, v93 row_newbcast:1 row_mask:0xf bank_mask:0xf\n"
"v_cvt_pk_bf16_f32 v88, v88, v89\n"
"v_mfma_f32_16x16x32_bf16 v[152:155], v[220:223], a[204:207], v[152:155]\n"
"s_waitcnt lgkmcnt(1)\n"
"ds_read_b128 v[212:215], v231 offset:0\n"
"v_mul_f32_dpp v13, v246, v13 row_newbcast:1 row_mask:0xf bank_mask:0xf\n"
"v_mfma_f32_16x16x32_bf16 a[208:211], v[120:123], a[244:247], a[208:211]\n"
"v_exp_f32 v170, v170\n"
"v_mfma_f32_16x16x32_bf16 v[0:3], v[96:99], a[244:247], v[0:3]\n"
"ds_read_b128 v[216:219], v230 offset:512\n"
"v_cvt_pk_bf16_f32 v124, v164, v165\n"
"v_mfma_f32_16x16x32_bf16 v[0:3], v[100:103], a[248:251], v[0:3]\n"
"ds_read_b64_tr_b16 v[226:227], v237 offset:53248\n"
"v_mul_f32_dpp v15, v246, v15 row_newbcast:3 row_mask:0xf bank_mask:0xf\n"
"v_mul_f32_dpp v41, v247, v41 row_newbcast:1 row_mask:0xf bank_mask:0xf\n"
"v_mul_f32_dpp v42, v247, v42 row_newbcast:2 row_mask:0xf bank_mask:0xf\n"
"v_exp_f32 v166, v166\n"
"v_mul_f32_dpp v57, v247, v57 row_newbcast:1 row_mask:0xf bank_mask:0xf\n"
"v_mul_f32_dpp v91, v206, v91 row_newbcast:3 row_mask:0xf bank_mask:0xf\n"
"v_exp_f32 v172, v172\n"
"v_exp_f32 v177, v177\n"
"v_mfma_f32_16x16x32_bf16 v[32:35], v[104:107], a[244:247], v[32:35]\n"
"ds_read_b64_tr_b16 v[224:225], v237 offset:49152\n"
"ds_read_b128 v[220:223], v231 offset:512\n"
"v_mfma_f32_16x16x32_bf16 v[32:35], v[108:111], a[248:251], v[32:35]\n"
"ds_write_b128 v244, a[232:235] offset:34816\n"
"v_cvt_pk_bf16_f32 v8, v8, v9\n"
"v_mul_f32_dpp v75, v206, v75 row_newbcast:3 row_mask:0xf bank_mask:0xf\n"
"v_mul_f32_dpp v30, v246, v30 row_newbcast:2 row_mask:0xf bank_mask:0xf\n"
"v_mul_f32_dpp v94, v206, v94 row_newbcast:2 row_mask:0xf bank_mask:0xf\n"
"v_exp_f32 v176, v176\n"
"v_mfma_f32_16x16x32_bf16 v[64:67], v[112:115], a[244:247], v[64:67]\n"
"v_exp_f32 v173, v173\n"
"v_mfma_f32_16x16x32_bf16 v[64:67], v[116:119], a[248:251], v[64:67]\n"
"s_waitcnt lgkmcnt(6)\n"
"ds_write_b128 v244, a[236:239] offset:35840\n"
"v_mul_f32_dpp v77, v206, v77 row_newbcast:1 row_mask:0xf bank_mask:0xf\n"
"v_mul_f32_dpp v62, v247, v62 row_newbcast:2 row_mask:0xf bank_mask:0xf\n"
"v_exp_f32 v169, v169\n"
"v_cvt_pk_bf16_f32 v138, v172, v173\n"
"v_exp_f32 v153, v153\n"
"v_mfma_f32_16x16x32_bf16 v[156:159], v[208:211], a[0:3], v[180:183]\n"
"s_waitcnt lgkmcnt(6)\n"
"ds_read_b64_tr_b16 a[254:255], v233 offset:53248\n"
"v_mul_f32_dpp v63, v247, v63 row_newbcast:3 row_mask:0xf bank_mask:0xf\n"
"v_mfma_f32_16x16x32_bf16 v[156:159], v[212:215], a[4:7], v[156:159]\n"
"s_waitcnt lgkmcnt(6)\n"
"v_exp_f32 v174, v174\n"
"v_mfma_f32_16x16x32_bf16 v[156:159], v[216:219], a[8:11], v[156:159]\n"
"s_waitcnt lgkmcnt(3)\n"
"s_add_u32 s40, s56, s53\n"
"v_cvt_pk_bf16_f32 v40, v40, v41\n"
"v_mul_f32_dpp v0, v246, v0 row_newbcast:0 row_mask:0xf bank_mask:0xf\n"
"v_mfma_f32_16x16x32_bf16 v[156:159], v[220:223], a[12:15], v[156:159]\n"
"buffer_load_dwordx4 v240, s[16:19], s40 offen offset:2048 nt lds\n"
"v_cvt_pk_bf16_f32 v9, v10, v11\n"
"v_mul_f32_dpp v44, v247, v44 row_newbcast:0 row_mask:0xf bank_mask:0xf\n"
"v_cvt_pk_bf16_f32 v125, v166, v167\n"
"v_mul_f32_dpp v31, v246, v31 row_newbcast:3 row_mask:0xf bank_mask:0xf\n"
"v_exp_f32 v152, v152\n"
"v_mul_f32_dpp v34, v247, v34 row_newbcast:2 row_mask:0xf bank_mask:0xf\n"
"v_mov_b64 v[200:201], 0\n"
"v_mfma_f32_16x16x32_bf16 v[160:163], v[216:219], a[24:27], v[184:187]\n"
"v_mul_f32_dpp v60, v247, v60 row_newbcast:0 row_mask:0xf bank_mask:0xf\n"
"v_xor_b32 v244, 0x1000, v244\n"
"v_mfma_f32_16x16x32_bf16 v[160:163], v[220:223], a[28:31], v[160:163]\n"
"v_exp_f32 v178, v178\n"
"v_mfma_f32_16x16x32_bf16 v[160:163], v[208:211], a[16:19], v[160:163]\n"
"ds_read_b64_tr_b16 a[250:251], v236 offset:53760\n"
"v_mul_f32_dpp v33, v247, v33 row_newbcast:1 row_mask:0xf bank_mask:0xf\n"
"v_mfma_f32_16x16x32_bf16 v[160:163], v[212:215], a[20:23], v[160:163]\n"
"ds_read_b64_tr_b16 a[244:245], v232 offset:49664\n"
"v_mul_f32_dpp v43, v247, v43 row_newbcast:3 row_mask:0xf bank_mask:0xf\n"
"v_mfma_f32_16x16x32_bf16 v[164:167], v[208:211], a[32:35], v[188:191]\n"
"v_mul_f32_dpp v45, v247, v45 row_newbcast:1 row_mask:0xf bank_mask:0xf\n"
"v_cvt_pk_bf16_f32 v72, v72, v73\n"
"v_mfma_f32_16x16x32_bf16 v[164:167], v[212:215], a[36:39], v[164:167]\n"
"ds_read_b128 v[208:211], v230 offset:4096\n"
"v_mul_f32_dpp v74, v206, v74 row_newbcast:2 row_mask:0xf bank_mask:0xf\n"
"v_mfma_f32_16x16x32_bf16 v[164:167], v[216:219], a[40:43], v[164:167]\n"
"v_mul_f32_dpp v56, v247, v56 row_newbcast:0 row_mask:0xf bank_mask:0xf\n"
"v_mul_f32_dpp v58, v247, v58 row_newbcast:2 row_mask:0xf bank_mask:0xf\n"
"v_mfma_f32_16x16x32_bf16 v[164:167], v[220:223], a[44:47], v[164:167]\n"
"ds_read_b128 v[212:215], v231 offset:4096\n"
"ds_read_b128 v[220:223], v231 offset:4608\n"
"v_mul_f32_dpp v47, v247, v47 row_newbcast:3 row_mask:0xf bank_mask:0xf\n"
"v_cvt_pk_bf16_f32 v73, v74, v75\n"
"v_mul_f32_dpp v29, v246, v29 row_newbcast:1 row_mask:0xf bank_mask:0xf\n"
"v_exp_f32 v175, v175\n"
"v_exp_f32 v179, v179\n"
"v_mul_f32_dpp v1, v246, v1 row_newbcast:1 row_mask:0xf bank_mask:0xf\n"
"v_mul_f32_dpp v2, v246, v2 row_newbcast:2 row_mask:0xf bank_mask:0xf\n"
"v_mfma_f32_16x16x32_bf16 v[68:71], v[116:119], v[224:227], v[68:71]\n"
"s_waitcnt lgkmcnt(5)\n"
"v_exp_f32 v171, v171\n"
"v_mfma_f32_16x16x32_bf16 v[68:71], v[112:115], a[252:255], v[68:71]\n"
"ds_read_b128 v[216:219], v230 offset:4608\n"
"v_mul_f32_dpp v27, v246, v27 row_newbcast:3 row_mask:0xf bank_mask:0xf\n"
"v_mfma_f32_16x16x32_bf16 v[36:39], v[108:111], v[224:227], v[36:39]\n"
"s_add_u32 s40, s56, s54\n"
"v_cvt_pk_bf16_f32 v24, v24, v25\n"
"v_mul_f32_dpp v59, v247, v59 row_newbcast:3 row_mask:0xf bank_mask:0xf\n"
"v_mfma_f32_16x16x32_bf16 v[36:39], v[104:107], a[252:255], v[36:39]\n"
"buffer_load_dwordx4 v207, s[16:19], s40 offen offset:3072 nt lds\n"
"v_cvt_pk_bf16_f32 v74, v76, v77\n"
"v_cvt_pk_bf16_f32 v25, v26, v27\n"
"v_cvt_pk_bf16_f32 v130, v168, v169\n"
"v_exp_f32 v154, v154\n"
"v_exp_f32 v162, v162\n"
"v_mul_f32_dpp v3, v246, v3 row_newbcast:3 row_mask:0xf bank_mask:0xf\n"
"v_mfma_f32_16x16x32_bf16 v[4:7], v[100:103], v[224:227], v[4:7]\n"
"v_cvt_pk_bf16_f32 v10, v12, v13\n"
"v_cvt_pk_bf16_f32 v0, v0, v1\n"
"v_mfma_f32_16x16x32_bf16 v[4:7], v[96:99], a[252:255], v[4:7]\n"
"v_mul_f32_dpp v90, v206, v90 row_newbcast:2 row_mask:0xf bank_mask:0xf\n"
"v_mul_f32_dpp v92, v206, v92 row_newbcast:0 row_mask:0xf bank_mask:0xf\n"
"v_mfma_f32_16x16x32_bf16 a[212:215], v[120:123], a[252:255], a[212:215]\n"
"s_waitcnt lgkmcnt(3)\n"
"ds_read_b64_tr_b16 a[246:247], v232 offset:53760\n"
"ds_read_b64_tr_b16 v[224:225], v237 offset:49664\n"
"v_cvt_pk_bf16_f32 v11, v14, v15\n"
"v_cvt_pk_bf16_f32 v41, v42, v43\n"
"v_cvt_pk_bf16_f32 v43, v46, v47\n"
"v_cvt_pk_bf16_f32 v56, v56, v57\n"
"v_cvt_pk_bf16_f32 v131, v170, v171\n"
"v_cvt_pk_bf16_f32 v146, v176, v177\n"
"v_exp_f32 v167, v167\n"
"v_mfma_f32_16x16x32_bf16 v[168:171], v[208:211], a[0:3], v[180:183]\n"
"s_waitcnt lgkmcnt(3)\n"
"v_exp_f32 v159, v159\n"
"v_mfma_f32_16x16x32_bf16 v[168:171], v[212:215], a[4:7], v[168:171]\n"
"s_waitcnt lgkmcnt(2)\n"
"ds_read_b64_tr_b16 a[248:249], v236 offset:49664\n"
"ds_read_b64_tr_b16 a[254:255], v233 offset:53760\n"
"v_mfma_f32_16x16x32_bf16 v[168:171], v[216:219], a[8:11], v[168:171]\n"
"v_cvt_pk_bf16_f32 v89, v90, v91\n"
"v_cvt_pk_bf16_f32 v139, v174, v175\n"
"v_mfma_f32_16x16x32_bf16 v[168:171], v[220:223], a[12:15], v[168:171]\n"
"ds_write_b128 v248, v[8:11] offset:49152\n"
"v_mul_f32_dpp v79, v206, v79 row_newbcast:3 row_mask:0xf bank_mask:0xf\n"
"v_cvt_pk_bf16_f32 v147, v178, v179\n"
"v_exp_f32 v165, v165\n"
"v_mul_f32_dpp v6, v246, v6 row_newbcast:2 row_mask:0xf bank_mask:0xf\n"
"v_cvt_pk_bf16_f32 v1, v2, v3\n"
"v_mfma_f32_16x16x32_bf16 v[172:175], v[216:219], a[24:27], v[184:187]\n"
"ds_read_b64_tr_b16 v[226:227], v237 offset:53760\n"
"v_mul_f32_dpp v32, v247, v32 row_newbcast:0 row_mask:0xf bank_mask:0xf\n"
"v_mfma_f32_16x16x32_bf16 v[172:175], v[220:223], a[28:31], v[172:175]\n"
"v_mul_f32_dpp v39, v247, v39 row_newbcast:3 row_mask:0xf bank_mask:0xf\n"
"v_mul_f32_dpp v68, v206, v68 row_newbcast:0 row_mask:0xf bank_mask:0xf\n"
"v_mfma_f32_16x16x32_bf16 v[172:175], v[208:211], a[16:19], v[172:175]\n"
"ds_read_b64_tr_b16 a[252:253], v233 offset:49664\n"
"v_cvt_pk_bf16_f32 v75, v78, v79\n"
"v_mfma_f32_16x16x32_bf16 v[172:175], v[212:215], a[20:23], v[172:175]\n"
"s_cbranch_vccnz LPCT0_%=\nLPKT0_%=:\n"
"ds_read_b64_tr_b16 a[54:55], v249 offset:49160\n"
"v_mfma_f32_16x16x32_bf16 v[176:179], v[208:211], a[32:35], v[188:191]\n"
"v_cvt_pk_bf16_f32 v42, v44, v45\n"
"v_mul_f32_dpp v61, v247, v61 row_newbcast:1 row_mask:0xf bank_mask:0xf\n"
"v_mfma_f32_16x16x32_bf16 v[176:179], v[212:215], a[36:39], v[176:179]\n"
"ds_read_b64_tr_b16 a[52:53], v249 offset:49152\n"
"v_cvt_pk_bf16_f32 v57, v58, v59\n"
"v_mfma_f32_16x16x32_bf16 v[176:179], v[216:219], a[40:43], v[176:179]\n"
"v_exp_f32 v155, v155\n"
"v_mfma_f32_16x16x32_bf16 v[176:179], v[220:223], a[44:47], v[176:179]\n"
"s_waitcnt lgkmcnt(4)\n"
"ds_write_b128 v248, v[40:43] offset:50176\n"
"v_cvt_pk_bf16_f32 v27, v30, v31\n"
"v_cvt_pk_bf16_f32 v90, v92, v93\n"
"v_cvt_pk_bf16_f32 v126, v152, v153\n"
"v_mul_f32_dpp v7, v246, v7 row_newbcast:3 row_mask:0xf bank_mask:0xf\n"
"v_mul_f32_dpp v65, v206, v65 row_newbcast:1 row_mask:0xf bank_mask:0xf\n"
"v_mul_f32_dpp v67, v206, v67 row_newbcast:3 row_mask:0xf bank_mask:0xf\n"
"v_mul_f32_dpp v70, v206, v70 row_newbcast:2 row_mask:0xf bank_mask:0xf\n"
"v_mul_f32_dpp v71, v206, v71 row_newbcast:3 row_mask:0xf bank_mask:0xf\n"
"v_mfma_f32_16x16x32_bf16 a[224:227], v[120:123], a[244:247], a[224:227]\n"
"ds_write_b128 v248, v[72:75] offset:51200\n"
"v_cvt_pk_bf16_f32 v26, v28, v29\n"
"v_exp_f32 v166, v166\n"
"v_cvt_pk_bf16_f32 v3, v6, v7\n"
"v_mul_f32_dpp v66, v206, v66 row_newbcast:2 row_mask:0xf bank_mask:0xf\n"
"v_mfma_f32_16x16x32_bf16 v[16:19], v[96:99], a[244:247], v[16:19]\n"
"s_waitcnt vmcnt(8) lgkmcnt(4)\n"
"ds_read_b64_tr_b16 a[68:69], v249 offset:50176\n"
"v_mul_f32_dpp v69, v206, v69 row_newbcast:1 row_mask:0xf bank_mask:0xf\n"
"v_mfma_f32_16x16x32_bf16 v[16:19], v[100:103], a[248:251], v[16:19]\n"
"s_barrier\n"
"ds_read_b64_tr_b16 a[70:71], v249 offset:50184\n"
"ds_read_b128 v[212:215], v229 offset:16384\n"
"ds_read_b64 v[250:251], v245 offset:52224\n"
"v_mul_f32_dpp v5, v246, v5 row_newbcast:1 row_mask:0xf bank_mask:0xf\n"
"v_mul_f32_dpp v35, v247, v35 row_newbcast:3 row_mask:0xf bank_mask:0xf\n"
"v_exp_f32 v173, v173\n"
"v_exp_f32 v175, v175\n"
"v_mfma_f32_16x16x32_bf16 v[48:51], v[104:107], a[244:247], v[48:51]\n"
"v_cvt_pk_bf16_f32 v59, v62, v63\n"
"v_mul_f32_dpp v38, v247, v38 row_newbcast:2 row_mask:0xf bank_mask:0xf\n"
"v_mfma_f32_16x16x32_bf16 v[48:51], v[108:111], a[248:251], v[48:51]\n"
"ds_read_b128 v[208:211], v228 offset:16384\n"
"ds_read_b128 v[220:223], v229 offset:16896\n"
"v_mul_f32_dpp v95, v206, v95 row_newbcast:3 row_mask:0xf bank_mask:0xf\n"
"v_exp_f32 v161, v161\n"
"v_cvt_pk_bf16_f32 v149, v166, v167\n"
"v_cvt_pk_bf16_f32 v32, v32, v33\n"
"v_exp_f32 v170, v170\n"
"v_exp_f32 v172, v172\n"
"v_mfma_f32_16x16x32_bf16 v[80:83], v[112:115], a[244:247], v[80:83]\n"
"ds_read_b64_tr_b16 a[84:85], v249 offset:51200\n"
"ds_read_b64_tr_b16 a[86:87], v249 offset:51208\n"
"v_mfma_f32_16x16x32_bf16 v[80:83], v[116:119], a[248:251], v[80:83]\n"
"ds_read_b128 v[28:31], v244 offset:33792\n"
"v_mov_b64 v[198:199], 0\n"
"v_mfma_f32_16x16x32_bf16 v[84:87], v[116:119], v[224:227], v[84:87]\n"
"ds_write_b128 v248, v[24:27] offset:49152\n"
"v_cvt_pk_bf16_f32 v91, v94, v95\n"
"v_exp_f32 v156, v156\n"
"v_exp_f32 v157, v157\n"
"v_mul_f32_dpp v37, v247, v37 row_newbcast:1 row_mask:0xf bank_mask:0xf\n"
"v_mfma_f32_16x16x32_bf16 v[84:87], v[112:115], a[252:255], v[84:87]\n"
"ds_read_b64_tr_b16 a[60:61], v249 offset:49152\n"
"ds_read_b128 v[216:219], v228 offset:16896\n"
"v_cvt_pk_bf16_f32 v127, v154, v155\n"
"v_exp_f32 v160, v160\n"
"v_exp_f32 v164, v164\n"
"v_mul_f32_dpp v36, v247, v36 row_newbcast:0 row_mask:0xf bank_mask:0xf\n"
"v_exp_f32 v176, v176\n"
"v_mfma_f32_16x16x32_bf16 v[52:55], v[108:111], v[224:227], v[52:55]\n"
"ds_write_b128 v248, v[88:91] offset:51200\n"
"v_cvt_pk_bf16_f32 v58, v60, v61\n"
"v_exp_f32 v163, v163\n"
"v_cvt_pk_bf16_f32 v33, v34, v35\n"
"v_exp_f32 v169, v169\n"
"v_mfma_f32_16x16x32_bf16 v[52:55], v[104:107], a[252:255], v[52:55]\n"
"s_waitcnt lgkmcnt(9)\n"
"ds_write_b128 v248, v[56:59] offset:50176\n"
"v_mul_f32_dpp v4, v246, v4 row_newbcast:0 row_mask:0xf bank_mask:0xf\n"
"v_exp_f32 v168, v168\n"
"v_min_f32 v254, v252, v250\n"
"v_sub_f32 v252, v254, v252\n"
"v_mfma_f32_16x16x32_bf16 v[20:23], v[100:103], v[224:227], v[20:23]\n"
"ds_read_b64_tr_b16 a[92:93], v249 offset:51200\n"
"ds_read_b64_tr_b16 a[244:245], v234 offset:0\n"
"v_mfma_f32_16x16x32_bf16 v[20:23], v[96:99], a[252:255], v[20:23]\n"
"ds_read_b64_tr_b16 a[62:63], v249 offset:49160\n"
"ds_read_b64_tr_b16 a[78:79], v249 offset:50184\n"
"ds_read_b64_tr_b16 a[94:95], v249 offset:51208\n"
"v_cvt_pk_bf16_f32 v2, v4, v5\n"
"v_mul_f32_dpp v64, v206, v64 row_newbcast:0 row_mask:0xf bank_mask:0xf\n"
"v_cvt_pk_bf16_f32 v64, v64, v65\n"
"v_cvt_pk_bf16_f32 v65, v66, v67\n"
"v_cvt_pk_bf16_f32 v66, v68, v69\n"
"v_cvt_pk_bf16_f32 v67, v70, v71\n"
"v_exp_f32 v177, v177\n"
"v_mfma_f32_16x16x32_bf16 a[228:231], v[120:123], a[252:255], a[228:231]\n"
"s_waitcnt lgkmcnt(11)\n"
"ds_write_b128 v248, v[64:67] offset:51200\n"
"v_cvt_pk_bf16_f32 v132, v156, v157\n"
"v_cvt_pk_bf16_f32 v34, v36, v37\n"
"v_cvt_pk_bf16_f32 v35, v38, v39\n"
"v_exp_f32 v171, v171\n"
"v_exp_f32 v179, v179\n"
"v_sub_f32 v250, v254, v250\n"
"v_mul_f32_dpp v83, v206, v83 row_newbcast:3 row_mask:0xf bank_mask:0xf\n"
"v_mfma_f32_16x16x32_bf16 v[152:155], v[208:211], a[0:3], v[180:183]\n"
"ds_read_b64_tr_b16 a[76:77], v249 offset:50176\n"
"ds_read_b64_tr_b16 a[80:81], v249 offset:51200\n"
"v_mfma_f32_16x16x32_bf16 v[152:155], v[212:215], a[4:7], v[152:155]\n"
"s_waitcnt lgkmcnt(10)\n"
"ds_write_b128 v248, v[0:3] offset:49152\n"
"v_exp_f32 v158, v158\n"
"v_cvt_pk_bf16_f32 v140, v160, v161\n"
"v_cvt_pk_bf16_f32 v142, v172, v173\n"
"v_mul_f32_dpp v18, v246, v18 row_newbcast:2 row_mask:0xf bank_mask:0xf\n"
"v_mfma_f32_16x16x32_bf16 v[152:155], v[216:219], a[8:11], v[152:155]\n"
"ds_write_b128 v248, v[32:35] offset:50176\n"
"v_cvt_pk_bf16_f32 v141, v162, v163\n"
"v_exp_f32 v178, v178\n"
"v_exp_f32 v250, v250\n"
"v_mfma_f32_16x16x32_bf16 v[152:155], v[220:223], a[12:15], v[152:155]\n"
"ds_read_b64_tr_b16 a[50:51], v249 offset:49160\n"
"ds_read_b64_tr_b16 a[64:65], v249 offset:50176\n"
"ds_read_b128 v[24:27], v244 offset:32768\n"
"v_cvt_pk_bf16_f32 v133, v158, v159\n"
"v_cvt_pk_bf16_f32 v148, v164, v165\n"
"v_cvt_pk_bf16_f32 v150, v176, v177\n"
"v_exp_f32 v252, v252\n"
"v_mul_f32_dpp v19, v246, v19 row_newbcast:3 row_mask:0xf bank_mask:0xf\n"
"v_mul_f32_dpp v22, v246, v22 row_newbcast:2 row_mask:0xf bank_mask:0xf\n"
"v_mfma_f32_16x16x32_bf16 v[156:159], v[216:219], a[24:27], v[184:187]\n"
"ds_read_b64_tr_b16 a[66:67], v249 offset:50184\n"
"ds_read_b64_tr_b16 a[248:249], v238 offset:0\n"
"v_mfma_f32_16x16x32_bf16 v[156:159], v[220:223], a[28:31], v[156:159]\n"
"ds_read_b64_tr_b16 a[48:49], v249 offset:49152\n"
"ds_read_b128 v[60:63], v244 offset:35840\n"
"v_mfma_f32_16x16x32_bf16 v[156:159], v[208:211], a[16:19], v[156:159]\n"
"ds_read_b64_tr_b16 a[82:83], v249 offset:51208\n"
"ds_read_b128 v[56:59], v244 offset:34816\n"
"v_mfma_f32_16x16x32_bf16 v[156:159], v[212:215], a[20:23], v[156:159]\n"
"ds_read_b64_tr_b16 a[246:247], v234 offset:4096\n"
"ds_read_b64_tr_b16 v[226:227], v239 offset:4096\n"
"v_mfma_f32_16x16x32_bf16 v[160:163], v[208:211], a[32:35], v[188:191]\n"
"v_exp_f32 v174, v174\n"
"v_mfma_f32_16x16x32_bf16 v[160:163], v[212:215], a[36:39], v[160:163]\n"
"ds_read_b64_tr_b16 a[250:251], v238 offset:4096\n"
"v_mul_f32_dpp v49, v247, v49 row_newbcast:1 row_mask:0xf bank_mask:0xf\n"
"v_mfma_f32_16x16x32_bf16 v[160:163], v[216:219], a[40:43], v[160:163]\n"
"ds_read_b64_tr_b16 v[224:225], v239 offset:0\n"
"v_mov_b64 v[202:203], 0\n"
"v_mfma_f32_16x16x32_bf16 v[160:163], v[220:223], a[44:47], v[160:163]\n"
"v_exp_f32 v154, v154\n"
"v_mfma_f32_16x16x32_bf16 v[164:167], v[216:219], a[200:203], v[192:195]\n"
"v_cvt_pk_bf16_f32 v151, v178, v179\n"
"v_mul_f32 v253, v253, v252\n"
"v_mfma_f32_16x16x32_bf16 v[164:167], v[220:223], a[204:207], v[164:167]\n"
"v_cvt_pk_bf16_f32 v134, v168, v169\n"
"v_fmac_f32 v253, v251, v250\n"
"v_mfma_f32_16x16x32_bf16 v[164:167], v[208:211], a[192:195], v[164:167]\n"
"ds_read_b64_tr_b16 a[252:253], v235 offset:0\n"
"ds_read_b64_tr_b16 a[254:255], v235 offset:4096\n"
"v_mfma_f32_16x16x32_bf16 v[164:167], v[212:215], a[196:199], v[164:167]\n"
"s_waitcnt lgkmcnt(4)\n"
"v_cvt_pk_bf16_f32 v135, v170, v171\n"
"v_mfma_f32_16x16x32_bf16 a[216:219], v[124:127], a[244:247], 0\n"
"ds_read_b128 v[216:219], v228 offset:20992\n"
"v_cvt_pk_bf16_f32 v143, v174, v175\n"
"v_mfma_f32_16x16x32_bf16 v[8:11], v[128:131], a[244:247], 0\n"
"s_waitcnt lgkmcnt(4)\n"
"s_add_u32 m0, s44, 98304\n"
"v_rcp_f32 v253, v253\n"
"v_mfma_f32_16x16x32_bf16 v[8:11], v[132:135], a[248:251], v[8:11]\n"
"buffer_load_dwordx4 v240, s[20:23], s56 offen nt lds\n"
"v_exp_f32 v152, v152\n"
"v_exp_f32 v155, v155\n"
"v_exp_f32 v158, v158\n"
"v_exp_f32 v167, v167\n"
"v_mfma_f32_16x16x32_bf16 v[40:43], v[136:139], a[244:247], 0\n"
"v_accvgpr_read_b32 v254, a208\n"
"v_mul_f32_dpp v85, v206, v85 row_newbcast:1 row_mask:0xf bank_mask:0xf\n"
"v_mfma_f32_16x16x32_bf16 v[40:43], v[140:143], a[248:251], v[40:43]\n"
"v_mul_f32 v250, v250, v253\n"
"v_accvgpr_read_b32 v251, a211\n"
"v_mfma_f32_16x16x32_bf16 v[72:75], v[144:147], a[244:247], 0\n"
"ds_read_b128 v[220:223], v229 offset:20992\n"
"v_mul_f32_dpp v17, v246, v17 row_newbcast:1 row_mask:0xf bank_mask:0xf\n"
"v_mfma_f32_16x16x32_bf16 v[72:75], v[148:151], a[248:251], v[72:75]\n"
"s_waitcnt lgkmcnt(2)\n"
"v_exp_f32 v164, v164\n"
"v_mfma_f32_16x16x32_bf16 v[76:79], v[148:151], v[224:227], 0\n"
"v_exp_f32 v153, v153\n"
"v_mfma_f32_16x16x32_bf16 v[76:79], v[144:147], a[252:255], v[76:79]\n"
"v_mul_f32 v252, v252, v253\n"
"v_mov_b32_dpp v250, v250 row_ror:12 row_mask:0x2 bank_mask:0xf\n"
"v_mfma_f32_16x16x32_bf16 v[44:47], v[140:143], v[224:227], 0\n"
"ds_read_b64_tr_b16 a[244:245], v234 offset:512\n"
"v_mov_b32_dpp v252, v252 row_ror:12 row_mask:0x2 bank_mask:0xf\n"
"v_mfma_f32_16x16x32_bf16 v[44:47], v[136:139], a[252:255], v[44:47]\n"
"ds_read_b128 v[212:215], v229 offset:20480\n"
"v_mov_b32_dpp v250, v250 row_ror:8 row_mask:0x4 bank_mask:0xf\n"
"v_mfma_f32_16x16x32_bf16 v[12:15], v[132:135], v[224:227], 0\n"
"s_add_u32 s40, s56, s52\n"
"ds_read_b128 v[208:211], v228 offset:20480\n"
"v_cvt_pk_bf16_f32 v96, v152, v153\n"
"v_mfma_f32_16x16x32_bf16 v[12:15], v[128:131], a[252:255], v[12:15]\n"
"buffer_load_dwordx4 v207, s[20:23], s40 offen offset:1024 nt lds\n"
"v_mov_b32_dpp v252, v252 row_ror:8 row_mask:0x4 bank_mask:0xf\n"
"v_mov_b32_dpp v250, v250 row_ror:4 row_mask:0x8 bank_mask:0xf\n"
"v_accvgpr_read_b32 v255, a209\n"
"v_accvgpr_read_b32 v253, a210\n"
"v_mov_b64 v[196:197], 0\n"
"v_cvt_pk_bf16_f32 v97, v154, v155\n"
"v_exp_f32 v157, v157\n"
"v_mfma_f32_16x16x32_bf16 a[220:223], v[124:127], a[252:255], 0\n"
"v_mov_b32_dpp v252, v252 row_ror:4 row_mask:0x8 bank_mask:0xf\n"
"v_mul_f32_dpp v24, v250, v24 row_newbcast:0 row_mask:0xf bank_mask:0xf\n"
"v_mul_f32_dpp v25, v250, v25 row_newbcast:1 row_mask:0xf bank_mask:0xf\n"
"v_mul_f32_dpp v26, v250, v26 row_newbcast:2 row_mask:0xf bank_mask:0xf\n"
"v_mul_f32_dpp v29, v250, v29 row_newbcast:1 row_mask:0xf bank_mask:0xf\n"
"v_mul_f32_dpp v54, v247, v54 row_newbcast:2 row_mask:0xf bank_mask:0xf\n"
"v_exp_f32 v162, v162\n"
"v_exp_f32 v163, v163\n"
"v_smfmac_f32_16x16x64_bf16 v[196:199], a[240:243], v[128:135], v241\n"
"v_mul_f32_dpp v27, v250, v27 row_newbcast:3 row_mask:0xf bank_mask:0xf\n"
"v_fmac_f32_dpp v24, v252, v254 row_newbcast:0 row_mask:0xf bank_mask:0xf\n"
"v_mfma_f32_16x16x32_bf16 v[202:205], a[240:243], v[124:127], v[202:205]\n"
"s_waitcnt lgkmcnt(3)\n"
"ds_read_b64_tr_b16 a[252:253], v235 offset:512\n"
"ds_read_b64_tr_b16 a[254:255], v235 offset:4608\n"
"ds_read_b64_tr_b16 v[224:225], v239 offset:512\n"
"v_fmac_f32_dpp v25, v252, v255 row_newbcast:1 row_mask:0xf bank_mask:0xf\n"
"v_fmac_f32_dpp v26, v252, v253 row_newbcast:2 row_mask:0xf bank_mask:0xf\n"
"v_mul_f32_dpp v28, v250, v28 row_newbcast:0 row_mask:0xf bank_mask:0xf\n"
"v_mul_f32_dpp v30, v250, v30 row_newbcast:2 row_mask:0xf bank_mask:0xf\n"
"v_mul_f32_dpp v50, v247, v50 row_newbcast:2 row_mask:0xf bank_mask:0xf\n"
"v_mul_f32_dpp v58, v250, v58 row_newbcast:2 row_mask:0xf bank_mask:0xf\n"
"v_exp_f32 v156, v156\n"
"v_mfma_f32_16x16x32_bf16 v[168:171], v[216:219], a[8:11], v[180:183]\n"
"v_accvgpr_read_b32 v254, a212\n"
"v_accvgpr_read_b32 v253, a214\n"
"v_mfma_f32_16x16x32_bf16 v[168:171], v[220:223], a[12:15], v[168:171]\n"
"s_waitcnt lgkmcnt(3)\n"
"v_mul_f32_dpp v48, v247, v48 row_newbcast:0 row_mask:0xf bank_mask:0xf\n"
"v_mul_f32_dpp v57, v250, v57 row_newbcast:1 row_mask:0xf bank_mask:0xf\n"
"v_mfma_f32_16x16x32_bf16 v[168:171], v[208:211], a[0:3], v[168:171]\n"
"ds_read_b64_tr_b16 a[250:251], v238 offset:4608\n"
"v_mul_f32_dpp v56, v250, v56 row_newbcast:0 row_mask:0xf bank_mask:0xf\n"
"v_mfma_f32_16x16x32_bf16 v[168:171], v[212:215], a[4:7], v[168:171]\n"
"ds_read_b64_tr_b16 a[248:249], v238 offset:512\n"
"ds_read_b64_tr_b16 v[226:227], v239 offset:4608\n"
"v_fmac_f32_dpp v27, v252, v251 row_newbcast:3 row_mask:0xf bank_mask:0xf\n"
"v_mul_f32_dpp v31, v250, v31 row_newbcast:3 row_mask:0xf bank_mask:0xf\n"
"v_fmac_f32_dpp v28, v252, v254 row_newbcast:0 row_mask:0xf bank_mask:0xf\n"
"v_fmac_f32_dpp v30, v252, v253 row_newbcast:2 row_mask:0xf bank_mask:0xf\n"
"v_cvt_pk_bf16_f32 v24, v24, v25\n"
"v_cvt_pk_bf16_f32 v25, v26, v27\n"
"v_mul_f32_dpp v52, v247, v52 row_newbcast:0 row_mask:0xf bank_mask:0xf\n"
"v_accvgpr_read_b32 v254, a224\n"
"v_mul_f32_dpp v59, v250, v59 row_newbcast:3 row_mask:0xf bank_mask:0xf\n"
"v_mul_f32_dpp v60, v250, v60 row_newbcast:0 row_mask:0xf bank_mask:0xf\n"
"v_mul_f32_dpp v62, v250, v62 row_newbcast:2 row_mask:0xf bank_mask:0xf\n"
"v_mul_f32_dpp v63, v250, v63 row_newbcast:3 row_mask:0xf bank_mask:0xf\n"
"v_exp_f32 v159, v159\n"
"v_exp_f32 v161, v161\n"
"v_mfma_f32_16x16x32_bf16 v[172:175], v[208:211], a[16:19], v[184:187]\n"
"v_accvgpr_read_b32 v255, a213\n"
"v_accvgpr_read_b32 v251, a215\n"
"v_mfma_f32_16x16x32_bf16 v[172:175], v[212:215], a[20:23], v[172:175]\n"
"ds_read_b64_tr_b16 a[246:247], v234 offset:4608\n"
"v_fmac_f32_dpp v29, v252, v255 row_newbcast:1 row_mask:0xf bank_mask:0xf\n"
"v_mfma_f32_16x16x32_bf16 v[172:175], v[216:219], a[24:27], v[172:175]\n"
"s_add_u32 s40, s56, s53\n"
"v_xor_b32 v249, s75, v249\n"
"v_accvgpr_read_b32 v255, a225\n"
"v_mfma_f32_16x16x32_bf16 v[172:175], v[220:223], a[28:31], v[172:175]\n"
"buffer_load_dwordx4 v240, s[20:23], s40 offen offset:2048 nt lds\n"
"v_fmac_f32_dpp v31, v252, v251 row_newbcast:3 row_mask:0xf bank_mask:0xf\n"
"v_cvt_pk_bf16_f32 v27, v30, v31\n"
"v_mul_f32_dpp v16, v246, v16 row_newbcast:0 row_mask:0xf bank_mask:0xf\n"
"v_mul_f32_dpp v51, v247, v51 row_newbcast:3 row_mask:0xf bank_mask:0xf\n"
"v_accvgpr_read_b32 v253, a226\n"
"v_accvgpr_read_b32 v251, a227\n"
"v_fmac_f32_dpp v56, v252, v254 row_newbcast:0 row_mask:0xf bank_mask:0xf\n"
"v_accvgpr_read_b32 v254, a228\n"
"v_mfma_f32_16x16x32_bf16 v[176:179], v[216:219], a[40:43], v[188:191]\n"
"v_cvt_pk_bf16_f32 v26, v28, v29\n"
"v_fmac_f32_dpp v57, v252, v255 row_newbcast:1 row_mask:0xf bank_mask:0xf\n"
"v_mfma_f32_16x16x32_bf16 v[176:179], v[220:223], a[44:47], v[176:179]\n"
"v_fmac_f32_dpp v58, v252, v253 row_newbcast:2 row_mask:0xf bank_mask:0xf\n"
"v_accvgpr_read_b32 v255, a229\n"
"v_mfma_f32_16x16x32_bf16 v[176:179], v[208:211], a[32:35], v[176:179]\n"
"v_cvt_pk_bf16_f32 v16, v16, v17\n"
"v_mul_f32_dpp v61, v250, v61 row_newbcast:1 row_mask:0xf bank_mask:0xf\n"
"v_mfma_f32_16x16x32_bf16 v[176:179], v[212:215], a[36:39], v[176:179]\n"
"ds_write_b128 v243, v[24:27] offset:0\n"
"v_cvt_pk_bf16_f32 v48, v48, v49\n"
"v_fmac_f32_dpp v59, v252, v251 row_newbcast:3 row_mask:0xf bank_mask:0xf\n"
"v_accvgpr_read_b32 v253, a230\n"
"v_accvgpr_read_b32 v251, a231\n"
"v_fmac_f32_dpp v60, v252, v254 row_newbcast:0 row_mask:0xf bank_mask:0xf\n"
"v_fmac_f32_dpp v61, v252, v255 row_newbcast:1 row_mask:0xf bank_mask:0xf\n"
"v_cvt_pk_bf16_f32 v56, v56, v57\n"
"v_cvt_pk_bf16_f32 v57, v58, v59\n"
"v_cvt_pk_bf16_f32 v58, v60, v61\n"
"v_mfma_f32_16x16x32_bf16 v[152:155], v[208:211], a[192:195], v[192:195]\n"
"v_mul_f32_dpp v81, v206, v81 row_newbcast:1 row_mask:0xf bank_mask:0xf\n"
"v_mul_f32_dpp v82, v206, v82 row_newbcast:2 row_mask:0xf bank_mask:0xf\n"
"v_mul_f32_dpp v87, v206, v87 row_newbcast:3 row_mask:0xf bank_mask:0xf\n"
"v_fmac_f32_dpp v62, v252, v253 row_newbcast:2 row_mask:0xf bank_mask:0xf\n"
"v_fmac_f32_dpp v63, v252, v251 row_newbcast:3 row_mask:0xf bank_mask:0xf\n"
"v_cvt_pk_bf16_f32 v59, v62, v63\n"
"ds_write_b128 v243, v[56:59] offset:2048\n"
"v_mfma_f32_16x16x32_bf16 v[152:155], v[212:215], a[196:199], v[152:155]\n"
"v_exp_f32 v174, v174\n"
"v_mfma_f32_16x16x32_bf16 v[152:155], v[216:219], a[200:203], v[152:155]\n"
"v_exp_f32 v166, v166\n"
"v_mfma_f32_16x16x32_bf16 v[152:155], v[220:223], a[204:207], v[152:155]\n"
"s_waitcnt lgkmcnt(2)\n"
"ds_read_b128 v[212:215], v231 offset:16384\n"
"v_cvt_pk_bf16_f32 v105, v158, v159\n"
"v_mfma_f32_16x16x32_bf16 a[232:235], v[124:127], a[244:247], 0\n"
"v_mul_f32_dpp v20, v246, v20 row_newbcast:0 row_mask:0xf bank_mask:0xf\n"
"v_mul_f32_dpp v23, v246, v23 row_newbcast:3 row_mask:0xf bank_mask:0xf\n"
"v_mfma_f32_16x16x32_bf16 v[24:27], v[128:131], a[244:247], 0\n"
"s_add_u32 s40, s56, s54\n"
"ds_read_b128 v[220:223], v231 offset:16896\n"
"v_mul_f32_dpp v21, v246, v21 row_newbcast:1 row_mask:0xf bank_mask:0xf\n"
"v_mfma_f32_16x16x32_bf16 v[24:27], v[132:135], a[248:251], v[24:27]\n"
"buffer_load_dwordx4 v207, s[20:23], s40 offen offset:3072 nt lds\n"
"v_mfma_f32_16x16x32_bf16 v[56:59], v[136:139], a[244:247], 0\n"
"s_add_u32 s55, s55, s59\n"
"v_cvt_pk_bf16_f32 v17, v18, v19\n"
"v_cvt_pk_bf16_f32 v104, v156, v157\n"
"v_mfma_f32_16x16x32_bf16 v[56:59], v[140:143], a[248:251], v[56:59]\n"
"s_or_b32 s55, s55, s58\n"
"v_exp_f32 v178, v178\n"
"v_mfma_f32_16x16x32_bf16 v[88:91], v[144:147], a[244:247], 0\n"
"s_xor_b32 s56, s55, s57\n"
"ds_read_b128 v[208:211], v230 offset:16384\n"
"v_cvt_pk_bf16_f32 v49, v50, v51\n"
"v_mfma_f32_16x16x32_bf16 v[88:91], v[148:151], a[248:251], v[88:91]\n"
"ds_read_b128 v[216:219], v230 offset:16896\n"
"v_cvt_pk_bf16_f32 v18, v20, v21\n"
"v_mfma_f32_16x16x32_bf16 v[92:95], v[148:151], v[224:227], 0\n"
"v_exp_f32 v160, v160\n"
"v_mfma_f32_16x16x32_bf16 v[92:95], v[144:147], a[252:255], v[92:95]\n"
"v_exp_f32 v165, v165\n"
"v_mfma_f32_16x16x32_bf16 v[60:63], v[140:143], v[224:227], 0\n"
"s_add_u32 m0, s44, 49152\n"
"ds_read_b64_tr_b16 a[248:249], v236 offset:0\n"
"v_mul_f32_dpp v80, v206, v80 row_newbcast:0 row_mask:0xf bank_mask:0xf\n"
"v_mfma_f32_16x16x32_bf16 v[60:63], v[136:139], a[252:255], v[60:63]\n"
"buffer_load_dwordx4 v240, s[16:19], s56 offen nt lds\n"
"v_mfma_f32_16x16x32_bf16 v[28:31], v[132:135], v[224:227], 0\n"
"v_exp_f32 v177, v177\n"
"v_mfma_f32_16x16x32_bf16 v[28:31], v[128:131], a[252:255], v[28:31]\n"
"v_exp_f32 v169, v169\n"
"v_mfma_f32_16x16x32_bf16 a[236:239], v[124:127], a[252:255], 0\n"
"ds_read_b64_tr_b16 a[250:251], v236 offset:4096\n"
"ds_read_b64_tr_b16 v[224:225], v237 offset:0\n"
"v_smfmac_f32_16x16x64_bf16 v[198:201], a[240:243], v[136:143], v241\n"
"s_waitcnt lgkmcnt(3)\n"
"ds_read_b64_tr_b16 a[246:247], v232 offset:4096\n"
"v_cvt_pk_bf16_f32 v112, v160, v161\n"
"v_mfma_f32_16x16x32_bf16 v[156:159], v[208:211], a[0:3], v[180:183]\n"
"v_cvt_pk_bf16_f32 v80, v80, v81\n"
"v_cvt_pk_bf16_f32 v113, v162, v163\n"
"v_mfma_f32_16x16x32_bf16 v[156:159], v[212:215], a[4:7], v[156:159]\n"
"ds_read_b64_tr_b16 a[254:255], v233 offset:4096\n"
"v_xor_b32 v248, s75, v248\n"
"v_mfma_f32_16x16x32_bf16 v[156:159], v[216:219], a[8:11], v[156:159]\n"
"ds_read_b64_tr_b16 a[244:245], v232 offset:0\n"
"v_cvt_pk_bf16_f32 v120, v164, v165\n"
"v_mfma_f32_16x16x32_bf16 v[156:159], v[220:223], a[12:15], v[156:159]\n"
"v_exp_f32 v170, v170\n"
"v_mfma_f32_16x16x32_bf16 v[160:163], v[216:219], a[24:27], v[184:187]\n"
"v_exp_f32 v179, v179\n"
"v_mfma_f32_16x16x32_bf16 v[160:163], v[220:223], a[28:31], v[160:163]\n"
"ds_read_b64_tr_b16 v[226:227], v237 offset:4096\n"
"v_cvt_pk_bf16_f32 v121, v166, v167\n"
"v_mfma_f32_16x16x32_bf16 v[160:163], v[208:211], a[16:19], v[160:163]\n"
"s_add_u32 s40, s56, s52\n"
"v_exp_f32 v168, v168\n"
"v_mfma_f32_16x16x32_bf16 v[160:163], v[212:215], a[20:23], v[160:163]\n"
"buffer_load_dwordx4 v207, s[16:19], s40 offen offset:1024 nt lds\n"
"v_mfma_f32_16x16x32_bf16 v[164:167], v[208:211], a[32:35], v[188:191]\n"
"v_exp_f32 v176, v176\n"
"v_mfma_f32_16x16x32_bf16 v[164:167], v[212:215], a[36:39], v[164:167]\n"
"v_cvt_pk_bf16_f32 v19, v22, v23\n"
"v_mul_f32_dpp v53, v247, v53 row_newbcast:1 row_mask:0xf bank_mask:0xf\n"
"v_mfma_f32_16x16x32_bf16 v[164:167], v[216:219], a[40:43], v[164:167]\n"
"ds_read_b64_tr_b16 a[252:253], v233 offset:0\n"
"ds_read_b128 v[208:211], v230 offset:20480\n"
"v_mfma_f32_16x16x32_bf16 v[164:167], v[220:223], a[44:47], v[164:167]\n"
"s_waitcnt lgkmcnt(3)\n"
"v_exp_f32 v158, v158\n"
"v_mfma_f32_16x16x32_bf16 a[208:211], v[124:127], a[244:247], 0\n"
"ds_read_b128 v[220:223], v231 offset:20992\n"
"v_cvt_pk_bf16_f32 v81, v82, v83\n"
"v_mfma_f32_16x16x32_bf16 v[0:3], v[128:131], a[244:247], 0\n"
"ds_read_b128 v[212:215], v231 offset:20480\n"
"v_mul_f32_dpp v55, v247, v55 row_newbcast:3 row_mask:0xf bank_mask:0xf\n"
"v_mfma_f32_16x16x32_bf16 v[0:3], v[132:135], a[248:251], v[0:3]\n"
"v_exp_f32 v175, v175\n"
"v_mfma_f32_16x16x32_bf16 v[32:35], v[136:139], a[244:247], 0\n"
"s_add_u32 s40, s56, s53\n"
"ds_read_b128 v[216:219], v230 offset:20992\n"
"v_mul_f32_dpp v84, v206, v84 row_newbcast:0 row_mask:0xf bank_mask:0xf\n"
"v_mfma_f32_16x16x32_bf16 v[32:35], v[140:143], a[248:251], v[32:35]\n"
"buffer_load_dwordx4 v240, s[16:19], s40 offen offset:2048 nt lds\n"
"v_mfma_f32_16x16x32_bf16 v[64:67], v[144:147], a[244:247], 0\n"
"v_cvt_pk_bf16_f32 v50, v52, v53\n"
"v_mul_f32_dpp v86, v206, v86 row_newbcast:2 row_mask:0xf bank_mask:0xf\n"
"v_mfma_f32_16x16x32_bf16 v[64:67], v[148:151], a[248:251], v[64:67]\n"
"s_waitcnt lgkmcnt(3)\n"
"ds_write_b128 v248, v[16:19] offset:49152\n"
"v_cvt_pk_bf16_f32 v82, v84, v85\n"
"v_exp_f32 v172, v172\n"
"v_exp_f32 v152, v152\n"
"v_exp_f32 v159, v159\n"
"v_exp_f32 v161, v161\n"
"v_exp_f32 v164, v164\n"
"v_exp_f32 v167, v167\n"
"v_mfma_f32_16x16x32_bf16 v[68:71], v[148:151], v[224:227], 0\n"
"v_cvt_pk_bf16_f32 v51, v54, v55\n"
"v_cvt_pk_bf16_f32 v98, v168, v169\n"
"v_mfma_f32_16x16x32_bf16 v[68:71], v[144:147], a[252:255], v[68:71]\n"
"ds_write_b128 v248, v[48:51] offset:50176\n"
"v_exp_f32 v171, v171\n"
"v_exp_f32 v173, v173\n"
"v_exp_f32 v156, v156\n"
"v_exp_f32 v163, v163\n"
"v_mfma_f32_16x16x32_bf16 v[36:39], v[140:143], v[224:227], 0\n"
"ds_read_b64_tr_b16 a[246:247], v232 offset:4608\n"
"ds_read_b64_tr_b16 a[250:251], v236 offset:4608\n"
"v_mfma_f32_16x16x32_bf16 v[36:39], v[136:139], a[252:255], v[36:39]\n"
"ds_read_b64_tr_b16 a[74:75], v249 offset:50184\n"
"v_cvt_pk_bf16_f32 v107, v174, v175\n"
"v_mfma_f32_16x16x32_bf16 v[4:7], v[132:135], v[224:227], 0\n"
"ds_read_b64_tr_b16 a[56:57], v249 offset:49152\n"
"v_cvt_pk_bf16_f32 v83, v86, v87\n"
"v_mfma_f32_16x16x32_bf16 v[4:7], v[128:131], a[252:255], v[4:7]\n"
"ds_write_b128 v248, v[80:83] offset:51200\n"
"v_cvt_pk_bf16_f32 v99, v170, v171\n"
"v_exp_f32 v155, v155\n"
"v_exp_f32 v157, v157\n"
"v_mfma_f32_16x16x32_bf16 a[212:215], v[124:127], a[252:255], 0\n"
"s_add_u32 s40, s56, s54\n"
"ds_read_b64_tr_b16 a[58:59], v249 offset:49160\n"
"ds_read_b64_tr_b16 v[224:225], v237 offset:512\n"
"v_smfmac_f32_16x16x64_bf16 v[200:203], a[240:243], v[144:151], v241\n"
"buffer_load_dwordx4 v207, s[16:19], s40 offen offset:3072 nt lds\n"
"v_mfma_f32_16x16x32_bf16 v[168:171], v[208:211], a[0:3], v[180:183]\n"
"s_waitcnt lgkmcnt(8)\n"
"v_exp_f32 v153, v153\n"
"v_mfma_f32_16x16x32_bf16 v[168:171], v[212:215], a[4:7], v[168:171]\n"
"ds_read_b64_tr_b16 a[72:73], v249 offset:50176\n"
"ds_read_b64_tr_b16 a[90:91], v249 offset:51208\n"
"v_mfma_f32_16x16x32_bf16 v[168:171], v[216:219], a[8:11], v[168:171]\n"
"ds_read_b64_tr_b16 a[88:89], v249 offset:51200\n"
"v_cvt_pk_bf16_f32 v115, v178, v179\n"
"v_mfma_f32_16x16x32_bf16 v[168:171], v[220:223], a[12:15], v[168:171]\n"
"ds_read_b64_tr_b16 a[244:245], v232 offset:512\n"
"v_cvt_pk_bf16_f32 v106, v172, v173\n"
"v_mfma_f32_16x16x32_bf16 v[172:175], v[216:219], a[24:27], v[184:187]\n"
"ds_read_b64_tr_b16 a[248:249], v236 offset:512\n"
"v_cvt_pk_bf16_f32 v114, v176, v177\n"
"v_mfma_f32_16x16x32_bf16 v[172:175], v[220:223], a[28:31], v[172:175]\n"
"v_exp_f32 v162, v162\n"
"v_mfma_f32_16x16x32_bf16 v[172:175], v[208:211], a[16:19], v[172:175]\n"
"ds_read_b64_tr_b16 v[226:227], v237 offset:4608\n"
"v_cvt_pk_bf16_f32 v122, v152, v153\n"
"v_mfma_f32_16x16x32_bf16 v[172:175], v[212:215], a[20:23], v[172:175]\n"
"s_cbranch_vccnz LPCT1_%=\nLPKT1_%=:\n"
"ds_read_b64_tr_b16 a[252:253], v233 offset:512\n"
"v_mfma_f32_16x16x32_bf16 v[176:179], v[208:211], a[32:35], v[188:191]\n"
"v_exp_f32 v166, v166\n"
"v_mfma_f32_16x16x32_bf16 v[176:179], v[212:215], a[36:39], v[176:179]\n"
"ds_read_b64_tr_b16 a[254:255], v233 offset:4608\n"
"v_cvt_pk_bf16_f32 v109, v162, v163\n"
"v_mfma_f32_16x16x32_bf16 v[176:179], v[216:219], a[40:43], v[176:179]\n"
"v_exp_f32 v169, v169\n"
"v_mfma_f32_16x16x32_bf16 v[176:179], v[220:223], a[44:47], v[176:179]\n"
"s_waitcnt lgkmcnt(4)\n"
"v_cvt_pk_bf16_f32 v101, v158, v159\n"
"v_mfma_f32_16x16x32_bf16 a[224:227], v[124:127], a[244:247], 0\n"
"v_exp_f32 v154, v154\n"
"v_mfma_f32_16x16x32_bf16 v[16:19], v[128:131], a[244:247], 0\n"
"s_waitcnt vmcnt(8) lgkmcnt(3)\n"
"v_exp_f32 v168, v168\n"
"v_mfma_f32_16x16x32_bf16 v[16:19], v[132:135], a[248:251], v[16:19]\n"
"s_barrier\n"
"ds_read_b128 v[212:215], v229 offset:32768\n"
"ds_read_b128 v[216:219], v228 offset:33280\n"
"v_exp_f32 v165, v165\n"
"v_mfma_f32_16x16x32_bf16 v[48:51], v[136:139], a[244:247], 0\n"
"ds_read_b128 v[220:223], v229 offset:33280\n"
"v_xor_b32 v249, s75, v249\n"
"v_mfma_f32_16x16x32_bf16 v[48:51], v[140:143], a[248:251], v[48:51]\n"
"ds_read_b128 v[208:211], v228 offset:32768\n"
"v_add_u32 v243, 0x1000, v243\n"
"v_mfma_f32_16x16x32_bf16 v[80:83], v[144:147], a[244:247], 0\n"
"ds_read_b64_tr_b16 a[246:247], v234 offset:20480\n"
"v_cvt_pk_bf16_f32 v117, v166, v167\n"
"v_mfma_f32_16x16x32_bf16 v[80:83], v[148:151], a[248:251], v[80:83]\n"
"s_waitcnt lgkmcnt(5)\n"
"ds_read_b64_tr_b16 a[250:251], v238 offset:20480\n"
"v_xor_b32 v248, s75, v248\n"
"v_mfma_f32_16x16x32_bf16 v[84:87], v[148:151], v[224:227], 0\n"
"v_cvt_pk_bf16_f32 v100, v156, v157\n"
"v_cvt_pk_bf16_f32 v116, v164, v165\n"
"v_mfma_f32_16x16x32_bf16 v[84:87], v[144:147], a[252:255], v[84:87]\n"
"v_exp_f32 v160, v160\n"
"v_mfma_f32_16x16x32_bf16 v[52:55], v[140:143], v[224:227], 0\n"
"v_exp_f32 v176, v176\n"
"v_mfma_f32_16x16x32_bf16 v[52:55], v[136:139], a[252:255], v[52:55]\n"
"v_exp_f32 v177, v177\n"
"v_mfma_f32_16x16x32_bf16 v[20:23], v[132:135], v[224:227], 0\n"
"v_cvt_pk_bf16_f32 v123, v154, v155\n"
"v_cvt_pk_bf16_f32 v118, v176, v177\n"
"v_mfma_f32_16x16x32_bf16 v[20:23], v[128:131], a[252:255], v[20:23]\n"
"v_exp_f32 v174, v174\n"
"v_mfma_f32_16x16x32_bf16 a[228:231], v[124:127], a[252:255], 0\n"
"s_waitcnt lgkmcnt(1)\n"
"ds_read_b64_tr_b16 a[254:255], v235 offset:20480\n"
"s_branch LAY2_%=\n"
"LAX_%=:\n"
"v_mfma_f32_16x16x32_bf16 a[224:227], v[124:127], a[244:247], a[224:227]\n"
"v_exp_f32 v162, v162\n"
"v_mfma_f32_16x16x32_bf16 v[16:19], v[128:131], a[244:247], v[16:19]\n"
"s_waitcnt vmcnt(8)\n"
"v_exp_f32 v171, v171\n"
"v_mfma_f32_16x16x32_bf16 v[16:19], v[132:135], a[248:251], v[16:19]\n"
"s_barrier\n"
"v_exp_f32 v169, v169\n"
"v_exp_f32 v172, v172\n"
"v_exp_f32 v173, v173\n"
"v_exp_f32 v174, v174\n"
"v_lshlrev_b32_e64 v244, 24, s43\n"
"v_mfma_f32_16x16x32_bf16 v[48:51], v[136:139], a[244:247], v[48:51]\n"
"v_exp_f32 v170, v170\n"
"v_mfma_f32_16x16x32_bf16 v[48:51], v[140:143], a[248:251], v[48:51]\n"
"v_exp_f32 v179, v179\n"
"v_mfma_f32_16x16x32_bf16 v[80:83], v[144:147], a[244:247], v[80:83]\n"
"ds_read_b64_tr_b16 a[246:247], v234 offset:53248\n"
"v_cvt_pk_bf16_f32 v117, v166, v167\n"
"v_mfma_f32_16x16x32_bf16 v[80:83], v[148:151], a[248:251], v[80:83]\n"
"ds_read_b64_tr_b16 a[244:245], v234 offset:49152\n"
"v_cvt_pk_bf16_f32 v116, v164, v165\n"
"v_mfma_f32_16x16x32_bf16 v[84:87], v[148:151], v[224:227], v[84:87]\n"
"v_cvt_pk_bf16_f32 v100, v156, v157\n"
"v_accvgpr_read_b32 v253, a96\n"
"v_mfma_f32_16x16x32_bf16 v[84:87], v[144:147], a[252:255], v[84:87]\n"
"ds_read_b64_tr_b16 a[248:249], v238 offset:49152\n"
"ds_read_b64_tr_b16 a[250:251], v238 offset:53248\n"
"v_exp_f32 v168, v168\n"
"v_exp_f32 v175, v175\n"
"v_exp_f32 v176, v176\n"
"v_exp_f32 v177, v177\n"
"v_exp_f32 v178, v178\n"
"v_mfma_f32_16x16x32_bf16 v[52:55], v[140:143], v[224:227], v[52:55]\n"
"v_cvt_pk_bf16_f32 v102, v168, v169\n"
"v_cvt_pk_bf16_f32 v110, v172, v173\n"
"v_mfma_f32_16x16x32_bf16 v[52:55], v[136:139], a[252:255], v[52:55]\n"
"v_cvt_pk_bf16_f32 v103, v170, v171\n"
"v_and_b32 v253, 0x7fff0000, v253\n"
"v_mfma_f32_16x16x32_bf16 v[20:23], v[132:135], v[224:227], v[20:23]\n"
"ds_read_b64_tr_b16 v[224:225], v239 offset:49152\n"
"v_cvt_pk_bf16_f32 v101, v158, v159\n"
"v_mfma_f32_16x16x32_bf16 v[20:23], v[128:131], a[252:255], v[20:23]\n"
"v_cvt_pk_bf16_f32 v123, v154, v155\n"
"v_cvt_pk_bf16_f32 v111, v174, v175\n"
"v_mfma_f32_16x16x32_bf16 a[228:231], v[124:127], a[252:255], a[228:231]\n"
"s_waitcnt lgkmcnt(3)\n"
"ds_read_b64_tr_b16 a[254:255], v235 offset:53248\n"
"v_mul_f32 v244, 0x45800000, v244\n"
"v_mfma_f32_16x16x32_bf16 a[216:219], v[120:123], a[244:247], a[216:219]\n"
"s_mov_b64 s[28:29], s[8:9]\n"
"ds_read_b64_tr_b16 v[226:227], v239 offset:53248\n"
"v_cvt_pk_bf16_f32 v118, v176, v177\n"
"v_smfmac_f32_16x16x64_bf16 v[196:199], a[240:243], v[96:103], v241\n"
"s_mov_b32 s30, 655360\n"
"ds_read_b64_tr_b16 a[252:253], v235 offset:49152\n"
"v_cvt_pk_bf16_f32 v108, v160, v161\n"
"v_mfma_f32_16x16x32_bf16 v[8:11], v[96:99], a[244:247], v[8:11]\n"
"s_waitcnt lgkmcnt(4)\n"
"s_mov_b32 s71, s44\n"
"v_cvt_pk_bf16_f32 v119, v178, v179\n"
"v_max_u32_dpp v253, v253, v253 quad_perm:[1,0,3,2] row_mask:0xf bank_mask:0xf\n"
"v_mfma_f32_16x16x32_bf16 v[8:11], v[100:103], a[248:251], v[8:11]\n"
"s_mov_b32 s70, s95\n"
"v_and_b32 v252, 0x7fff0000, v252\n"
"v_and_b32 v245, 0x3f0, v242\n"
"v_smfmac_f32_16x16x64_bf16 v[200:203], a[240:243], v[112:119], v241\n"
"v_cvt_pk_bf16_f32 v109, v162, v163\n"
"v_max_u32_dpp v253, v253, v253 quad_perm:[2,3,0,1] row_mask:0xf bank_mask:0xf\n"
"v_mfma_f32_16x16x32_bf16 v[40:43], v[104:107], a[244:247], v[40:43]\n"
"v_add_f32 v196, v196, v197\n"
"v_lshrrev_b32 v245, 1, v245\n"
"v_mfma_f32_16x16x32_bf16 v[40:43], v[108:111], a[248:251], v[40:43]\n"
"s_add_u32 s41, s70, 327680\n"
"s_add_u32 m0, s71, 32768\n"
"v_max_u32_dpp v252, v252, v252 quad_perm:[1,0,3,2] row_mask:0xf bank_mask:0xf\n"
"v_smfmac_f32_16x16x64_bf16 v[198:201], a[240:243], v[104:111], v241\n"
"buffer_load_dwordx4 v240, s[28:31], s41 offen lds\n"
"v_mfma_f32_16x16x32_bf16 v[72:75], v[112:115], a[244:247], v[72:75]\n"
"v_max_u32_dpp v252, v252, v252 quad_perm:[2,3,0,1] row_mask:0xf bank_mask:0xf\n"
"v_max_u32_dpp v253, v253, v253 row_half_mirror row_mask:0xf bank_mask:0xf\n"
"v_mfma_f32_16x16x32_bf16 v[72:75], v[116:119], a[248:251], v[72:75]\n"
"ds_read_b64_tr_b16 a[244:245], v234 offset:49664\n"
"v_sub_u32 v245, v242, v245\n"
"v_mfma_f32_16x16x32_bf16 v[202:205], a[240:243], v[120:123], v[202:205]\n"
"s_waitcnt lgkmcnt(1)\n"
"ds_read_b64_tr_b16 a[246:247], v234 offset:53760\n"
"v_add_f32 v198, v198, v199\n"
"v_mfma_f32_16x16x32_bf16 v[76:79], v[116:119], v[224:227], v[76:79]\n"
"s_add_u32 s40, s41, s52\n"
"ds_read_b64_tr_b16 a[248:249], v238 offset:49664\n"
"ds_read_b64_tr_b16 a[250:251], v238 offset:53760\n"
"v_mfma_f32_16x16x32_bf16 v[76:79], v[112:115], a[252:255], v[76:79]\n"
"buffer_load_dwordx4 v240, s[28:31], s40 offen offset:1024 lds\n"
"v_mfma_f32_16x16x32_bf16 v[44:47], v[108:111], v[224:227], v[44:47]\n"
"v_add_f32 v200, v200, v201\n"
"v_add_f32 v244, v244, v202\n"
"v_mfma_f32_16x16x32_bf16 v[44:47], v[104:107], a[252:255], v[44:47]\n"
"v_add_f32 v254, v196, v198\n"
"v_min3_f32 v244, v244, v196, v198\n"
"v_min_f32 v244, v244, v200\n"
"v_rcp_f32 v244, v244\n"
"v_mfma_f32_16x16x32_bf16 v[12:15], v[100:103], v[224:227], v[12:15]\n"
"s_add_u32 s40, s41, s53\n"
"ds_read_b64_tr_b16 v[224:225], v239 offset:49664\n"
"ds_read_b64_tr_b16 v[226:227], v239 offset:53760\n"
"v_mfma_f32_16x16x32_bf16 v[12:15], v[96:99], a[252:255], v[12:15]\n"
"buffer_load_dwordx4 v240, s[28:31], s40 offen offset:2048 lds\n"
"v_min_u32_dpp v253, v253, v253 row_mirror row_mask:0xf bank_mask:0xf\n"
"v_add_f32 v254, v254, v200\n"
"v_add_f32 v254, v254, v202\n"
"v_fmac_f32 v254, 0x68800000, v244\n"
"v_cmp_nge_f32 vcc, s45, v254\n"
"v_bfe_u32 v254, v242, 8, 2\n"
"v_mfma_f32_16x16x32_bf16 a[220:223], v[120:123], a[252:255], a[220:223]\n"
"s_waitcnt lgkmcnt(4)\n"
"v_xor_b32 v244, 0x11000, v242\n"
"s_cbranch_vccnz LSLOW_%=\nLBACK3_%=:\ns_mov_b64 vcc, s[64:65]\n"
"v_mfma_f32_16x16x32_bf16 a[232:235], v[120:123], a[244:247], a[232:235]\n"
"ds_read_b64_tr_b16 a[252:253], v235 offset:49664\n"
"ds_read_b64_tr_b16 a[254:255], v235 offset:53760\n"
"v_min_u32_dpp v253, v253, v253 row_bcast:15 row_mask:0xa bank_mask:0xf\n"
"v_rcp_f32 v246, v196\n"
"v_rcp_f32 v247, v198\n"
"v_rcp_f32 v206, v200\n"
"v_xor_b32 v250, 0x1000, v245\n"
"v_mfma_f32_16x16x32_bf16 v[24:27], v[96:99], a[244:247], v[24:27]\n"
"s_waitcnt lgkmcnt(4)\n"
"v_mov_b32_dpp v206, v206 row_ror:12 row_mask:0x2 bank_mask:0xf\n"
"v_lshl_add_u32 v254, v254, 3, s43\n"
"v_mfma_f32_16x16x32_bf16 v[24:27], v[100:103], a[248:251], v[24:27]\n"
"ds_write_b128 v244, a[220:223] offset:33792\n"
"v_max_u32_dpp v252, v252, v252 row_half_mirror row_mask:0xf bank_mask:0xf\n"
"v_mov_b32_dpp v246, v246 row_ror:12 row_mask:0x2 bank_mask:0xf\n"
"v_mov_b32_dpp v206, v206 row_ror:8 row_mask:0x4 bank_mask:0xf\n"
"v_mfma_f32_16x16x32_bf16 v[56:59], v[104:107], a[244:247], v[56:59]\n"
"s_add_u32 s40, s41, s54\n"
"v_min_u32_dpp v252, v252, v252 row_mirror row_mask:0xf bank_mask:0xf\n"
"v_mov_b32_dpp v247, v247 row_ror:12 row_mask:0x2 bank_mask:0xf\n"
"v_mfma_f32_16x16x32_bf16 v[56:59], v[108:111], a[248:251], v[56:59]\n"
"buffer_load_dwordx4 v240, s[28:31], s40 offen offset:3072 lds\n"
"v_mfma_f32_16x16x32_bf16 v[88:91], v[112:115], a[244:247], v[88:91]\n"
"s_add_u32 s71, s44, s75\n"
"ds_read_b64_tr_b16 a[244:245], v232 offset:49152\n"
"ds_read_b64_tr_b16 a[246:247], v232 offset:53248\n"
"v_mfma_f32_16x16x32_bf16 v[88:91], v[116:119], a[248:251], v[88:91]\n"
"s_waitcnt lgkmcnt(5)\n"
"s_mov_b32 s70, s100\n"
"ds_write_b128 v244, a[232:235] offset:34816\n"
"v_mov_b32_dpp v246, v246 row_ror:8 row_mask:0x4 bank_mask:0xf\n"
"v_mov_b32_dpp v247, v247 row_ror:8 row_mask:0x4 bank_mask:0xf\n"
"v_mov_b32_dpp v206, v206 row_ror:4 row_mask:0x8 bank_mask:0xf\n"
"v_lshl_add_u32 v254, v254, 6, s75\n"
"v_sub_u32 v230, v234, v254\n"
"v_mfma_f32_16x16x32_bf16 v[92:95], v[116:119], v[224:227], v[92:95]\n"
"s_waitcnt lgkmcnt(4)\n"
"s_add_u32 s41, s70, 163840\n"
"s_add_u32 m0, s71, 16384\n"
"ds_read_b64_tr_b16 a[248:249], v236 offset:49152\n"
"v_mfma_f32_16x16x32_bf16 v[92:95], v[112:115], a[252:255], v[92:95]\n"
"buffer_load_dwordx4 v240, s[28:31], s41 offen lds\n"
"ds_read_b64_tr_b16 a[250:251], v236 offset:53248\n"
"v_min_u32_dpp v252, v252, v252 row_bcast:15 row_mask:0xa bank_mask:0xf\n"
"v_mov_b32_dpp v246, v246 row_ror:4 row_mask:0x8 bank_mask:0xf\n"
"v_mov_b32_dpp v247, v247 row_ror:4 row_mask:0x8 bank_mask:0xf\n"
"v_mul_f32_dpp v72, v206, v72 row_newbcast:0 row_mask:0xf bank_mask:0xf\n"
"v_mul_f32_dpp v74, v206, v74 row_newbcast:2 row_mask:0xf bank_mask:0xf\n"
"v_mul_f32_dpp v76, v206, v76 row_newbcast:0 row_mask:0xf bank_mask:0xf\n"
"v_mul_f32_dpp v77, v206, v77 row_newbcast:1 row_mask:0xf bank_mask:0xf\n"
"v_mfma_f32_16x16x32_bf16 v[60:63], v[108:111], v[224:227], v[60:63]\n"
"v_min_u32_dpp v252, v252, v252 row_bcast:31 row_mask:0xc bank_mask:0xf\n"
"v_mul_f32_dpp v8, v246, v8 row_newbcast:0 row_mask:0xf bank_mask:0xf\n"
"v_mfma_f32_16x16x32_bf16 v[60:63], v[104:107], a[252:255], v[60:63]\n"
"v_mul_f32_dpp v41, v247, v41 row_newbcast:1 row_mask:0xf bank_mask:0xf\n"
"v_mul_f32_dpp v59, v247, v59 row_newbcast:3 row_mask:0xf bank_mask:0xf\n"
"v_mfma_f32_16x16x32_bf16 v[28:31], v[100:103], v[224:227], v[28:31]\n"
"s_add_u32 s40, s41, s52\n"
"ds_read_b64_tr_b16 v[224:225], v237 offset:49152\n"
"v_min_u32_dpp v253, v253, v253 row_bcast:31 row_mask:0xc bank_mask:0xf\n"
"v_mfma_f32_16x16x32_bf16 v[28:31], v[96:99], a[252:255], v[28:31]\n"
"buffer_load_dwordx4 v240, s[28:31], s40 offen offset:1024 lds\n"
"v_mfma_f32_16x16x32_bf16 a[236:239], v[120:123], a[252:255], a[236:239]\n"
"s_waitcnt lgkmcnt(4)\n"
"v_mul_f32_dpp v25, v246, v25 row_newbcast:1 row_mask:0xf bank_mask:0xf\n"
"v_mul_f32_dpp v94, v206, v94 row_newbcast:2 row_mask:0xf bank_mask:0xf\n"
"v_mfma_f32_16x16x32_bf16 a[208:211], v[120:123], a[244:247], a[208:211]\n"
"ds_read_b64_tr_b16 a[252:253], v233 offset:49152\n"
"ds_read_b64_tr_b16 a[254:255], v233 offset:53248\n"
"v_mul_f32 v252, 0x43340000, v252\n"
"v_mul_f32 v252, v252, v253\n"
"v_mul_f32_dpp v10, v246, v10 row_newbcast:2 row_mask:0xf bank_mask:0xf\n"
"v_mul_f32_dpp v14, v246, v14 row_newbcast:2 row_mask:0xf bank_mask:0xf\n"
"v_mul_f32_dpp v40, v247, v40 row_newbcast:0 row_mask:0xf bank_mask:0xf\n"
"v_mul_f32_dpp v46, v247, v46 row_newbcast:2 row_mask:0xf bank_mask:0xf\n"
"v_mul_f32_dpp v73, v206, v73 row_newbcast:1 row_mask:0xf bank_mask:0xf\n"
"v_mul_f32_dpp v90, v206, v90 row_newbcast:2 row_mask:0xf bank_mask:0xf\n"
"v_mfma_f32_16x16x32_bf16 v[0:3], v[96:99], a[244:247], v[0:3]\n"
"s_waitcnt lgkmcnt(3)\n"
"ds_read_b64_tr_b16 v[226:227], v237 offset:53248\n"
"v_mul_f32_dpp v57, v247, v57 row_newbcast:1 row_mask:0xf bank_mask:0xf\n"
"v_mfma_f32_16x16x32_bf16 v[0:3], v[100:103], a[248:251], v[0:3]\n"
"ds_write_b128 v244, a[216:219] offset:32768\n"
"v_min_f32 v252, 0x71800000, v252\n"
"v_mul_f32_dpp v12, v246, v12 row_newbcast:0 row_mask:0xf bank_mask:0xf\n"
"v_mul_f32_dpp v45, v247, v45 row_newbcast:1 row_mask:0xf bank_mask:0xf\n"
"v_mul_f32_dpp v58, v247, v58 row_newbcast:2 row_mask:0xf bank_mask:0xf\n"
"v_mul_f32_dpp v92, v206, v92 row_newbcast:0 row_mask:0xf bank_mask:0xf\n"
"v_mfma_f32_16x16x32_bf16 v[32:35], v[104:107], a[244:247], v[32:35]\n"
"s_add_u32 s40, s41, s53\n"
"v_readlane_b32 s22, v252, 63\n"
"v_mul_f32_dpp v78, v206, v78 row_newbcast:2 row_mask:0xf bank_mask:0xf\n"
"v_mfma_f32_16x16x32_bf16 v[32:35], v[108:111], a[248:251], v[32:35]\n"
"buffer_load_dwordx4 v240, s[28:31], s40 offen offset:2048 lds\n"
"v_mfma_f32_16x16x32_bf16 v[64:67], v[112:115], a[244:247], v[64:67]\n"
"v_mul_f32_dpp v75, v206, v75 row_newbcast:3 row_mask:0xf bank_mask:0xf\n"
"v_mul_f32_dpp v61, v247, v61 row_newbcast:1 row_mask:0xf bank_mask:0xf\n"
"v_mfma_f32_16x16x32_bf16 v[64:67], v[116:119], a[248:251], v[64:67]\n"
"s_waitcnt lgkmcnt(1)\n"
"ds_read_b64_tr_b16 a[244:245], v232 offset:49664\n"
"ds_read_b64_tr_b16 a[246:247], v232 offset:53760\n"
"v_mov_b32 v253, v202\n"
"v_sub_u32 v228, v232, v254\n"
"v_mul_f32_dpp v9, v246, v9 row_newbcast:1 row_mask:0xf bank_mask:0xf\n"
"v_mul_f32_dpp v11, v246, v11 row_newbcast:3 row_mask:0xf bank_mask:0xf\n"
"v_mul_f32_dpp v15, v246, v15 row_newbcast:3 row_mask:0xf bank_mask:0xf\n"
"v_mul_f32_dpp v47, v247, v47 row_newbcast:3 row_mask:0xf bank_mask:0xf\n"
"v_cvt_pk_bf16_f32 v40, v40, v41\n"
"v_cvt_pk_bf16_f32 v72, v72, v73\n"
"v_mfma_f32_16x16x32_bf16 v[68:71], v[116:119], v[224:227], v[68:71]\n"
"s_add_u32 s40, s41, s54\n"
"ds_read_b64_tr_b16 a[248:249], v236 offset:49664\n"
"ds_read_b64_tr_b16 a[250:251], v236 offset:53760\n"
"v_mfma_f32_16x16x32_bf16 v[68:71], v[112:115], a[252:255], v[68:71]\n"
"buffer_load_dwordx4 v240, s[28:31], s40 offen offset:3072 lds\n"
"v_readlane_b32 s23, v252, 63\n"
"v_mul_f32_dpp v43, v247, v43 row_newbcast:3 row_mask:0xf bank_mask:0xf\n"
"v_mul_f32_dpp v24, v246, v24 row_newbcast:0 row_mask:0xf bank_mask:0xf\n"
"v_mul_f32_dpp v29, v246, v29 row_newbcast:1 row_mask:0xf bank_mask:0xf\n"
"v_mul_f32_dpp v56, v247, v56 row_newbcast:0 row_mask:0xf bank_mask:0xf\n"
"v_mul_f32_dpp v1, v246, v1 row_newbcast:1 row_mask:0xf bank_mask:0xf\n"
"v_accvgpr_read_b32 v255, a209\n"
"v_xor_b32 v232, 0x1000, v228\n"
"v_mfma_f32_16x16x32_bf16 v[36:39], v[108:111], v[224:227], v[36:39]\n"
"v_mov_b32 v252, v192\n"
"v_mul_f32_dpp v44, v247, v44 row_newbcast:0 row_mask:0xf bank_mask:0xf\n"
"v_mfma_f32_16x16x32_bf16 v[36:39], v[104:107], a[252:255], v[36:39]\n"
"ds_write_b128 v244, a[236:239] offset:35840\n"
"v_mul_f32_dpp v13, v246, v13 row_newbcast:1 row_mask:0xf bank_mask:0xf\n"
"v_mul_f32_dpp v42, v247, v42 row_newbcast:2 row_mask:0xf bank_mask:0xf\n"
"v_cvt_pk_bf16_f32 v41, v42, v43\n"
"v_cvt_pk_bf16_f32 v42, v44, v45\n"
"v_cvt_pk_bf16_f32 v56, v56, v57\n"
"v_mfma_f32_16x16x32_bf16 v[4:7], v[100:103], v[224:227], v[4:7]\n"
"ds_read_b64_tr_b16 v[224:225], v237 offset:49664\n"
"ds_read_b64_tr_b16 v[226:227], v237 offset:53760\n"
"v_mfma_f32_16x16x32_bf16 v[4:7], v[96:99], a[252:255], v[4:7]\n"
"ds_write_b64 v250, v[252:253] offset:52224\n"
"v_cvt_pk_bf16_f32 v8, v8, v9\n"
"v_cvt_pk_bf16_f32 v43, v46, v47\n"
"v_cvt_pk_bf16_f32 v73, v74, v75\n"
"v_mul_f32_dpp v28, v246, v28 row_newbcast:0 row_mask:0xf bank_mask:0xf\n"
"v_cvt_pk_bf16_f32 v57, v58, v59\n"
"v_mul_f32_dpp v0, v246, v0 row_newbcast:0 row_mask:0xf bank_mask:0xf\n"
"v_mul_f32_dpp v33, v247, v33 row_newbcast:1 row_mask:0xf bank_mask:0xf\n"
"v_mfma_f32_16x16x32_bf16 a[212:215], v[120:123], a[252:255], a[212:215]\n"
"s_waitcnt lgkmcnt(6)\n"
"ds_read_b64_tr_b16 a[252:253], v233 offset:49664\n"
"ds_read_b64_tr_b16 a[254:255], v233 offset:53760\n"
"v_cvt_pk_bf16_f32 v9, v10, v11\n"
"v_cvt_pk_bf16_f32 v74, v76, v77\n"
"v_mul_f32_dpp v60, v247, v60 row_newbcast:0 row_mask:0xf bank_mask:0xf\n"
"v_mul_f32_dpp v62, v247, v62 row_newbcast:2 row_mask:0xf bank_mask:0xf\n"
"v_mul_f32_dpp v88, v206, v88 row_newbcast:0 row_mask:0xf bank_mask:0xf\n"
"v_mul_f32_dpp v64, v206, v64 row_newbcast:0 row_mask:0xf bank_mask:0xf\n"
"v_mul_f32_dpp v66, v206, v66 row_newbcast:2 row_mask:0xf bank_mask:0xf\n"
"v_mfma_f32_16x16x32_bf16 a[224:227], v[120:123], a[244:247], a[224:227]\n"
"ds_write_b128 v248, v[40:43] offset:50176\n"
"v_sub_u32 v229, v233, v254\n"
"v_cvt_pk_bf16_f32 v11, v14, v15\n"
"v_mul_f32_dpp v27, v246, v27 row_newbcast:3 row_mask:0xf bank_mask:0xf\n"
"v_mul_f32_dpp v3, v246, v3 row_newbcast:3 row_mask:0xf bank_mask:0xf\n"
"v_mul_f32_dpp v68, v206, v68 row_newbcast:0 row_mask:0xf bank_mask:0xf\n"
"v_mfma_f32_16x16x32_bf16 v[16:19], v[96:99], a[244:247], v[16:19]\n"
"s_waitcnt vmcnt(8) lgkmcnt(1)\n"
"v_mul_f32_dpp v89, v206, v89 row_newbcast:1 row_mask:0xf bank_mask:0xf\n"
"v_add_u32 v236, s76, v249\n"
"v_mfma_f32_16x16x32_bf16 v[16:19], v[100:103], a[248:251], v[16:19]\n"
"s_barrier\n"
"ds_read_b64_tr_b16 a[22:23], v249 offset:50184\n"
"ds_read_b64_tr_b16 v[182:183], v228 offset:2048\n"
"ds_read_b64_tr_b16 v[216:217], v228 offset:24576\n"
"ds_read_b64_tr_b16 v[218:219], v228 offset:26624\n"
"ds_read_b64_tr_b16 a[202:203], v236 offset:2056\n"
"v_mul_f32_dpp v63, v247, v63 row_newbcast:3 row_mask:0xf bank_mask:0xf\n"
"v_mul_f32_dpp v91, v206, v91 row_newbcast:3 row_mask:0xf bank_mask:0xf\n"
"v_mul_f32_dpp v2, v246, v2 row_newbcast:2 row_mask:0xf bank_mask:0xf\n"
"v_mfma_f32_16x16x32_bf16 v[48:51], v[104:107], a[244:247], v[48:51]\n"
"ds_read_b64_tr_b16 v[192:193], v232 offset:8192\n"
"ds_read_b64_tr_b16 a[240:241], v236 offset:4096\n"
"v_mfma_f32_16x16x32_bf16 v[48:51], v[108:111], a[248:251], v[48:51]\n"
"ds_read_b64 v[250:251], v245 offset:52224\n"
"ds_read_b64_tr_b16 v[186:187], v232 offset:2048\n"
"ds_read_b64_tr_b16 v[194:195], v232 offset:10240\n"
"ds_read_b64_tr_b16 v[208:209], v228 offset:16384\n"
"v_cvt_pk_bf16_f32 v10, v12, v13\n"
"v_mul_f32_dpp v79, v206, v79 row_newbcast:3 row_mask:0xf bank_mask:0xf\n"
"v_cvt_pk_bf16_f32 v75, v78, v79\n"
"v_cvt_pk_bf16_f32 v88, v88, v89\n"
"v_cvt_pk_bf16_f32 v89, v90, v91\n"
"v_cvt_pk_bf16_f32 v0, v0, v1\n"
"v_mfma_f32_16x16x32_bf16 v[80:83], v[112:115], a[244:247], v[80:83]\n"
"ds_read_b64_tr_b16 v[180:181], v228 offset:0\n"
"ds_read_b64_tr_b16 v[188:189], v228 offset:8192\n"
"v_mfma_f32_16x16x32_bf16 v[80:83], v[116:119], a[248:251], v[80:83]\n"
"ds_write_b128 v248, v[72:75] offset:51200\n"
"v_mul_f32_dpp v30, v246, v30 row_newbcast:2 row_mask:0xf bank_mask:0xf\n"
"v_cvt_pk_bf16_f32 v24, v24, v25\n"
"v_cvt_pk_bf16_f32 v58, v60, v61\n"
"v_mul_f32_dpp v93, v206, v93 row_newbcast:1 row_mask:0xf bank_mask:0xf\n"
"v_mul_f32_dpp v36, v247, v36 row_newbcast:0 row_mask:0xf bank_mask:0xf\n"
"v_mfma_f32_16x16x32_bf16 v[84:87], v[116:119], v[224:227], v[84:87]\n"
"ds_write_b128 v248, v[8:11] offset:49152\n"
"v_mul_f32_dpp v26, v246, v26 row_newbcast:2 row_mask:0xf bank_mask:0xf\n"
"v_mul_f32_dpp v32, v247, v32 row_newbcast:0 row_mask:0xf bank_mask:0xf\n"
"v_mul_f32_dpp v65, v206, v65 row_newbcast:1 row_mask:0xf bank_mask:0xf\n"
"v_mul_f32_dpp v69, v206, v69 row_newbcast:1 row_mask:0xf bank_mask:0xf\n"
"v_xor_b32 v233, 0x1000, v229\n"
"v_mfma_f32_16x16x32_bf16 v[84:87], v[112:115], a[252:255], v[84:87]\n"
"ds_read_b64_tr_b16 a[6:7], v249 offset:49160\n"
"ds_read_b64_tr_b16 a[36:37], v249 offset:51200\n"
"ds_read_b64_tr_b16 v[210:211], v228 offset:18432\n"
"ds_read_b64_tr_b16 v[222:223], v232 offset:26624\n"
"v_mul_f32_dpp v31, v246, v31 row_newbcast:3 row_mask:0xf bank_mask:0xf\n"
"v_cvt_pk_bf16_f32 v25, v26, v27\n"
"v_cvt_pk_bf16_f32 v59, v62, v63\n"
"v_mul_f32_dpp v34, v247, v34 row_newbcast:2 row_mask:0xf bank_mask:0xf\n"
"v_mov_b64 v[196:197], s[22:23]\n"
"v_xor_b32 v237, 0x400, v236\n"
"v_mfma_f32_16x16x32_bf16 v[52:55], v[108:111], v[224:227], v[52:55]\n"
"ds_read_b64_tr_b16 v[184:185], v232 offset:0\n"
"ds_read_b64_tr_b16 v[212:213], v232 offset:16384\n"
"v_mfma_f32_16x16x32_bf16 v[52:55], v[104:107], a[252:255], v[52:55]\n"
"ds_read_b64_tr_b16 a[4:5], v249 offset:49152\n"
"ds_read_b64_tr_b16 a[20:21], v249 offset:50176\n"
"ds_read_b64_tr_b16 a[38:39], v249 offset:51208\n"
"ds_read_b64_tr_b16 v[220:221], v232 offset:24576\n"
"ds_read_b64_tr_b16 a[246:247], v237 offset:4104\n"
"v_cvt_pk_bf16_f32 v26, v28, v29\n"
"v_mul_f32_dpp v95, v206, v95 row_newbcast:3 row_mask:0xf bank_mask:0xf\n"
"v_cvt_pk_bf16_f32 v91, v94, v95\n"
"v_mul_f32_dpp v5, v246, v5 row_newbcast:1 row_mask:0xf bank_mask:0xf\n"
"v_mfma_f32_16x16x32_bf16 v[20:23], v[100:103], v[224:227], v[20:23]\n"
"ds_read_b64_tr_b16 v[190:191], v228 offset:10240\n"
"ds_read_b64_tr_b16 v[214:215], v232 offset:18432\n"
"v_mfma_f32_16x16x32_bf16 v[20:23], v[96:99], a[252:255], v[20:23]\n"
"ds_write_b128 v248, v[56:59] offset:50176\n"
"v_cvt_pk_bf16_f32 v27, v30, v31\n"
"v_mul_f32_dpp v4, v246, v4 row_newbcast:0 row_mask:0xf bank_mask:0xf\n"
"v_mul_f32_dpp v35, v247, v35 row_newbcast:3 row_mask:0xf bank_mask:0xf\n"
"v_cvt_pk_bf16_f32 v64, v64, v65\n"
"v_mov_b64 v[198:199], s[22:23]\n"
"v_mfma_f32_16x16x32_bf16 a[228:231], v[120:123], a[252:255], a[228:231]\n"
"s_waitcnt lgkmcnt(15)\n"
"ds_write_b128 v248, v[24:27] offset:49152\n"
"v_cvt_pk_bf16_f32 v90, v92, v93\n"
"v_mul_f32_dpp v6, v246, v6 row_newbcast:2 row_mask:0xf bank_mask:0xf\n"
"v_cvt_pk_bf16_f32 v1, v2, v3\n"
"v_cvt_pk_bf16_f32 v2, v4, v5\n"
"v_mul_f32_dpp v71, v206, v71 row_newbcast:3 row_mask:0xf bank_mask:0xf\n"
"v_mfma_f32_16x16x32_bf16 v[124:127], v[180:183], a[96:99], v[196:199]\n"
"s_waitcnt lgkmcnt(10)\n"
"ds_read_b64_tr_b16 a[14:15], v249 offset:49160\n"
"ds_read_b64_tr_b16 a[30:31], v249 offset:50184\n"
"v_mfma_f32_16x16x32_bf16 v[124:127], v[184:187], a[100:103], v[124:127]\n"
"s_waitcnt lgkmcnt(5)\n"
"ds_write_b128 v248, v[88:91] offset:51200\n"
"v_mul_f32_dpp v7, v246, v7 row_newbcast:3 row_mask:0xf bank_mask:0xf\n"
"v_cvt_pk_bf16_f32 v3, v6, v7\n"
"v_mul_f32_dpp v37, v247, v37 row_newbcast:1 row_mask:0xf bank_mask:0xf\n"
"v_cvt_pk_bf16_f32 v32, v32, v33\n"
"v_mul_f32_dpp v67, v206, v67 row_newbcast:3 row_mask:0xf bank_mask:0xf\n"
"v_mfma_f32_16x16x32_bf16 v[124:127], v[188:191], a[104:107], v[124:127]\n"
"ds_read_b64_tr_b16 a[12:13], v249 offset:49152\n"
"ds_read_b64_tr_b16 a[44:45], v249 offset:51200\n"
"ds_read_b64_tr_b16 a[46:47], v249 offset:51208\n"
"ds_read_b64_tr_b16 v[100:101], v233 offset:0\n"
"ds_read_b64_tr_b16 v[120:121], v229 offset:24576\n"
"v_mul_f32_dpp v38, v247, v38 row_newbcast:2 row_mask:0xf bank_mask:0xf\n"
"v_mul_f32_dpp v70, v206, v70 row_newbcast:2 row_mask:0xf bank_mask:0xf\n"
"v_cvt_pk_bf16_f32 v65, v66, v67\n"
"v_cvt_pk_bf16_f32 v66, v68, v69\n"
"v_cvt_pk_bf16_f32 v67, v70, v71\n"
"v_mfma_f32_16x16x32_bf16 v[124:127], v[192:195], a[108:111], v[124:127]\n"
"ds_write_b128 v248, v[0:3] offset:49152\n"
"v_mul_f32_dpp v39, v247, v39 row_newbcast:3 row_mask:0xf bank_mask:0xf\n"
"v_cvt_pk_bf16_f32 v33, v34, v35\n"
"v_cvt_pk_bf16_f32 v34, v36, v37\n"
"v_cvt_pk_bf16_f32 v35, v38, v39\n"
"v_xor_b32 v244, 0x1000, v244\n"
"v_mfma_f32_16x16x32_bf16 v[124:127], v[208:211], a[144:147], v[124:127]\n"
"s_waitcnt lgkmcnt(11)\n"
"ds_read_b64_tr_b16 a[28:29], v249 offset:50176\n"
"ds_read_b128 v[24:27], v244 offset:32768\n"
"v_mfma_f32_16x16x32_bf16 v[124:127], v[212:215], a[148:151], v[124:127]\n"
"ds_write_b128 v248, v[32:35] offset:50176\n"
"ds_write_b128 v248, v[64:67] offset:51200\n"
"v_mfma_f32_16x16x32_bf16 v[124:127], v[216:219], a[152:155], v[124:127]\n"
"ds_read_b64_tr_b16 a[0:1], v249 offset:49152\n"
"ds_read_b64_tr_b16 a[32:33], v249 offset:51200\n"
"v_mfma_f32_16x16x32_bf16 v[124:127], v[220:223], a[156:159], v[124:127]\n"
"ds_read_b64_tr_b16 a[16:17], v249 offset:50176\n"
"ds_read_b64_tr_b16 a[18:19], v249 offset:50184\n"
"ds_read_b128 v[56:59], v244 offset:34816\n"
"ds_read_b64_tr_b16 a[196:197], v237 offset:0\n"
"v_sub_u32 v231, v235, v254\n"
"v_mfma_f32_16x16x32_bf16 v[140:143], v[220:223], a[172:175], v[196:199]\n"
"ds_read_b64_tr_b16 a[2:3], v249 offset:49160\n"
"ds_read_b64_tr_b16 a[34:35], v249 offset:51208\n"
"v_mfma_f32_16x16x32_bf16 v[140:143], v[216:219], a[168:171], v[140:143]\n"
"ds_read_b128 v[28:31], v244 offset:33792\n"
"ds_read_b128 v[60:63], v244 offset:35840\n"
"v_mfma_f32_16x16x32_bf16 v[140:143], v[212:215], a[164:167], v[140:143]\n"
"ds_read_b64_tr_b16 a[200:201], v236 offset:2048\n"
"ds_read_b64_tr_b16 a[254:255], v237 offset:6152\n"
"v_mfma_f32_16x16x32_bf16 v[140:143], v[208:211], a[160:163], v[140:143]\n"
"ds_read_b64_tr_b16 a[206:207], v237 offset:2056\n"
"ds_read_b64_tr_b16 a[242:243], v236 offset:4104\n"
"v_mfma_f32_16x16x32_bf16 v[140:143], v[192:195], a[124:127], v[140:143]\n"
"ds_read_b64_tr_b16 a[244:245], v237 offset:4096\n"
"v_xor_b32 v234, 0x1000, v230\n"
"v_mfma_f32_16x16x32_bf16 v[140:143], v[188:191], a[120:123], v[140:143]\n"
"ds_read_b64_tr_b16 v[110:111], v233 offset:10240\n"
"ds_read_b64_tr_b16 a[250:251], v236 offset:6152\n"
"v_mfma_f32_16x16x32_bf16 v[140:143], v[184:187], a[116:119], v[140:143]\n"
"ds_read_b64_tr_b16 a[204:205], v237 offset:2048\n"
"ds_read_b64_tr_b16 v[102:103], v233 offset:2048\n"
"v_mfma_f32_16x16x32_bf16 v[140:143], v[180:183], a[112:115], v[140:143]\n"
"ds_read_b64_tr_b16 a[248:249], v236 offset:6144\n"
"ds_read_b64_tr_b16 a[252:253], v237 offset:6144\n"
"v_mfma_f32_16x16x32_bf16 v[156:159], v[180:183], a[128:131], v[196:199]\n"
"ds_read_b64_tr_b16 v[98:99], v229 offset:2048\n"
"ds_read_b64_tr_b16 v[116:117], v233 offset:16384\n"
"v_mfma_f32_16x16x32_bf16 v[156:159], v[184:187], a[132:135], v[156:159]\n"
"ds_read_b64_tr_b16 v[122:123], v229 offset:26624\n"
"ds_read_b64_tr_b16 v[224:225], v233 offset:24576\n"
"v_mfma_f32_16x16x32_bf16 v[156:159], v[188:191], a[136:139], v[156:159]\n"
"ds_read_b64_tr_b16 a[198:199], v237 offset:8\n"
"ds_read_b64_tr_b16 v[96:97], v229 offset:0\n"
"v_mfma_f32_16x16x32_bf16 v[156:159], v[192:195], a[140:143], v[156:159]\n"
"s_waitcnt lgkmcnt(13)\n"
"ds_read_b64_tr_b16 v[108:109], v233 offset:8192\n"
"v_mul_f32_dpp v23, v246, v23 row_newbcast:3 row_mask:0xf bank_mask:0xf\n"
"v_mfma_f32_16x16x32_bf16 v[156:159], v[208:211], a[176:179], v[156:159]\n"
"ds_read_b64_tr_b16 v[106:107], v229 offset:10240\n"
"ds_read_b64_tr_b16 v[112:113], v229 offset:16384\n"
"v_mfma_f32_16x16x32_bf16 v[156:159], v[212:215], a[180:183], v[156:159]\n"
"s_add_u32 s41, s95, 491520\n"
"ds_read_b64_tr_b16 a[192:193], v236 offset:0\n"
"ds_read_b64_tr_b16 v[118:119], v233 offset:18432\n"
"v_mfma_f32_16x16x32_bf16 v[156:159], v[216:219], a[184:187], v[156:159]\n"
"s_add_u32 m0, s44, 49152\n"
"ds_read_b64_tr_b16 a[194:195], v236 offset:8\n"
"ds_read_b64_tr_b16 v[104:105], v229 offset:8192\n"
"v_mfma_f32_16x16x32_bf16 v[156:159], v[220:223], a[188:191], v[156:159]\n"
"s_waitcnt lgkmcnt(13)\n"
"buffer_load_dwordx4 v240, s[28:31], s41 offen lds\n"
"v_mfma_f32_16x16x32_bf16 v[172:175], v[220:223], a[252:255], v[196:199]\n"
"s_add_u32 s40, s41, s52\n"
"v_min_f32 v254, v252, v250\n"
"v_sub_f32 v250, v254, v250\n"
"v_mfma_f32_16x16x32_bf16 v[172:175], v[216:219], a[248:251], v[172:175]\n"
"buffer_load_dwordx4 v240, s[28:31], s40 offen offset:1024 lds\n"
"v_mfma_f32_16x16x32_bf16 v[172:175], v[212:215], a[244:247], v[172:175]\n"
"s_add_u32 s40, s41, s53\n"
"ds_read_b64_tr_b16 v[114:115], v229 offset:18432\n"
"ds_read_b64_tr_b16 v[218:219], v230 offset:26624\n"
"v_mfma_f32_16x16x32_bf16 v[172:175], v[208:211], a[240:243], v[172:175]\n"
"buffer_load_dwordx4 v240, s[28:31], s40 offen offset:2048 lds\n"
"v_mfma_f32_16x16x32_bf16 v[172:175], v[192:195], a[204:207], v[172:175]\n"
"ds_read_b64_tr_b16 v[226:227], v233 offset:26624\n"
"ds_read_b64_tr_b16 a[232:233], v236 offset:10240\n"
"v_mfma_f32_16x16x32_bf16 v[172:175], v[188:191], a[200:203], v[172:175]\n"
"s_waitcnt lgkmcnt(6)\n"
"v_xor_b32 v235, 0x1000, v231\n"
"v_mul_f32_dpp v17, v246, v17 row_newbcast:1 row_mask:0xf bank_mask:0xf\n"
"v_mfma_f32_16x16x32_bf16 v[172:175], v[184:187], a[196:199], v[172:175]\n"
"s_waitcnt lgkmcnt(4)\n"
"s_add_u32 s40, s41, s54\n"
"ds_read_b64_tr_b16 v[222:223], v234 offset:26624\n"
"v_mul_f32_dpp v18, v246, v18 row_newbcast:2 row_mask:0xf bank_mask:0xf\n"
"v_mfma_f32_16x16x32_bf16 v[172:175], v[180:183], a[192:195], v[172:175]\n"
"buffer_load_dwordx4 v240, s[28:31], s40 offen offset:3072 lds\n"
"v_mfma_f32_16x16x32_bf16 v[128:131], v[96:99], a[96:99], v[196:199]\n"
"v_sub_f32 v252, v254, v252\n"
"v_mul_f32_dpp v16, v246, v16 row_newbcast:0 row_mask:0xf bank_mask:0xf\n"
"v_mfma_f32_16x16x32_bf16 v[128:131], v[100:103], a[100:103], v[128:131]\n"
"ds_read_b64_tr_b16 v[190:191], v230 offset:10240\n"
"v_mul_f32_dpp v86, v206, v86 row_newbcast:2 row_mask:0xf bank_mask:0xf\n"
"v_mfma_f32_16x16x32_bf16 v[128:131], v[104:107], a[104:107], v[128:131]\n"
"v_exp_f32 v252, v252\n"
"v_mfma_f32_16x16x32_bf16 v[128:131], v[108:111], a[108:111], v[128:131]\n"
"s_waitcnt lgkmcnt(4)\n"
"v_exp_f32 v250, v250\n"
"v_mfma_f32_16x16x32_bf16 v[128:131], v[112:115], a[144:147], v[128:131]\n"
"s_add_u32 s21, s60, 32\n"
"ds_read_b64_tr_b16 a[220:221], v237 offset:8192\n"
"v_mul_f32 v253, v253, v252\n"
"v_mfma_f32_16x16x32_bf16 v[128:131], v[116:119], a[148:151], v[128:131]\n"
"s_mov_b32 s69, 1\n"
"ds_read_b64_tr_b16 v[186:187], v234 offset:2048\n"
"v_fmac_f32 v253, v251, v250\n"
"v_mfma_f32_16x16x32_bf16 v[128:131], v[120:123], a[152:155], v[128:131]\n"
"s_waitcnt lgkmcnt(3)\n"
"v_bfe_u32 v201, v242, 2, 7\n"
"v_add_u32 v201, 0x100, v201\n"
"ds_read_b64_tr_b16 v[184:185], v234 offset:0\n"
"v_mfma_f32_16x16x32_bf16 v[128:131], v[224:227], a[156:159], v[128:131]\n"
"global_load_dword v200, v201, s[6:7] sc1\n"
"v_mfma_f32_16x16x32_bf16 v[144:147], v[224:227], a[172:175], v[196:199]\n"
"v_rcp_f32 v253, v253\n"
"v_mfma_f32_16x16x32_bf16 v[144:147], v[120:123], a[168:171], v[144:147]\n"
"v_mul_f32 v252, v252, v253\n"
"v_accvgpr_read_b32 v254, a208\n"
"v_mfma_f32_16x16x32_bf16 v[144:147], v[116:119], a[164:167], v[144:147]\n"
"ds_read_b64_tr_b16 v[188:189], v230 offset:8192\n"
"v_mul_f32 v250, v250, v253\n"
"v_mfma_f32_16x16x32_bf16 v[144:147], v[112:115], a[160:163], v[144:147]\n"
"s_lshl_b32 s20, s95, 9\n"
"ds_read_b64_tr_b16 v[194:195], v234 offset:10240\n"
"v_mov_b32_dpp v250, v250 row_ror:12 row_mask:0x2 bank_mask:0xf\n"
"v_mfma_f32_16x16x32_bf16 v[144:147], v[108:111], a[124:127], v[144:147]\n"
"s_lshl_b32 s19, s61, 7\n"
"v_mov_b32_dpp v250, v250 row_ror:8 row_mask:0x4 bank_mask:0xf\n"
"v_accvgpr_read_b32 v253, a210\n"
"v_mfma_f32_16x16x32_bf16 v[144:147], v[104:107], a[120:123], v[144:147]\n"
"s_lshl_b32 s91, s19, 9\n"
"v_mov_b32_dpp v252, v252 row_ror:12 row_mask:0x2 bank_mask:0xf\n"
"v_mov_b32_dpp v250, v250 row_ror:4 row_mask:0x8 bank_mask:0xf\n"
"v_mfma_f32_16x16x32_bf16 v[144:147], v[100:103], a[116:119], v[144:147]\n"
"ds_read_b64_tr_b16 v[214:215], v234 offset:18432\n"
"v_mul_f32_dpp v63, v250, v63 row_newbcast:3 row_mask:0xf bank_mask:0xf\n"
"v_mfma_f32_16x16x32_bf16 v[144:147], v[96:99], a[112:115], v[144:147]\n"
"ds_read_b64_tr_b16 v[208:209], v230 offset:16384\n"
"v_mul_f32_dpp v83, v206, v83 row_newbcast:3 row_mask:0xf bank_mask:0xf\n"
"v_mfma_f32_16x16x32_bf16 v[160:163], v[96:99], a[128:131], v[196:199]\n"
"ds_read_b64_tr_b16 v[192:193], v234 offset:8192\n"
"ds_read_b64_tr_b16 v[212:213], v234 offset:16384\n"
"v_mfma_f32_16x16x32_bf16 v[160:163], v[100:103], a[132:135], v[160:163]\n"
"v_accvgpr_read_b32 v251, a211\n"
"v_mul_f32_dpp v57, v250, v57 row_newbcast:1 row_mask:0xf bank_mask:0xf\n"
"v_mfma_f32_16x16x32_bf16 v[160:163], v[104:107], a[136:139], v[160:163]\n"
"ds_read_b64_tr_b16 v[182:183], v230 offset:2048\n"
"v_mul_f32_dpp v58, v250, v58 row_newbcast:2 row_mask:0xf bank_mask:0xf\n"
"v_mfma_f32_16x16x32_bf16 v[160:163], v[108:111], a[140:143], v[160:163]\n"
"ds_read_b64_tr_b16 v[180:181], v230 offset:0\n"
"v_mul_f32_dpp v31, v250, v31 row_newbcast:3 row_mask:0xf bank_mask:0xf\n"
"v_mfma_f32_16x16x32_bf16 v[160:163], v[112:115], a[176:179], v[160:163]\n"
"ds_read_b64_tr_b16 v[210:211], v230 offset:18432\n"
"v_mov_b32_dpp v252, v252 row_ror:8 row_mask:0x4 bank_mask:0xf\n"
"v_mfma_f32_16x16x32_bf16 v[160:163], v[116:119], a[180:183], v[160:163]\n"
"ds_read_b64_tr_b16 v[220:221], v234 offset:24576\n"
"v_mul_f32_dpp v26, v250, v26 row_newbcast:2 row_mask:0xf bank_mask:0xf\n"
"v_mfma_f32_16x16x32_bf16 v[160:163], v[120:123], a[184:187], v[160:163]\n"
"ds_read_b64_tr_b16 a[238:239], v237 offset:10248\n"
"v_mul_f32_dpp v30, v250, v30 row_newbcast:2 row_mask:0xf bank_mask:0xf\n"
"v_mfma_f32_16x16x32_bf16 v[160:163], v[224:227], a[188:191], v[160:163]\n"
"ds_read_b64_tr_b16 v[216:217], v230 offset:24576\n"
"v_mul_f32_dpp v28, v250, v28 row_newbcast:0 row_mask:0xf bank_mask:0xf\n"
"v_mfma_f32_16x16x32_bf16 v[176:179], v[224:227], a[252:255], v[196:199]\n"
"v_mov_b32_dpp v252, v252 row_ror:4 row_mask:0x8 bank_mask:0xf\n"
"v_mul_f32_dpp v61, v250, v61 row_newbcast:1 row_mask:0xf bank_mask:0xf\n"
"v_mfma_f32_16x16x32_bf16 v[176:179], v[120:123], a[248:251], v[176:179]\n"
"ds_read_b64_tr_b16 v[226:227], v235 offset:26624\n"
"v_mul_f32_dpp v59, v250, v59 row_newbcast:3 row_mask:0xf bank_mask:0xf\n"
"v_mfma_f32_16x16x32_bf16 v[176:179], v[116:119], a[244:247], v[176:179]\n"
"v_mul_f32_dpp v25, v250, v25 row_newbcast:1 row_mask:0xf bank_mask:0xf\n"
"v_mul_f32_dpp v29, v250, v29 row_newbcast:1 row_mask:0xf bank_mask:0xf\n"
"v_mfma_f32_16x16x32_bf16 v[176:179], v[112:115], a[240:243], v[176:179]\n"
"ds_read_b64_tr_b16 v[114:115], v231 offset:18432\n"
"v_mul_f32_dpp v19, v246, v19 row_newbcast:3 row_mask:0xf bank_mask:0xf\n"
"v_mfma_f32_16x16x32_bf16 v[176:179], v[108:111], a[204:207], v[176:179]\n"
"ds_read_b64_tr_b16 a[236:237], v237 offset:10240\n"
"v_mul_f32_dpp v27, v250, v27 row_newbcast:3 row_mask:0xf bank_mask:0xf\n"
"v_mfma_f32_16x16x32_bf16 v[176:179], v[104:107], a[200:203], v[176:179]\n"
"v_mul_f32_dpp v24, v250, v24 row_newbcast:0 row_mask:0xf bank_mask:0xf\n"
"v_fmac_f32_dpp v25, v252, v255 row_newbcast:1 row_mask:0xf bank_mask:0xf\n"
"v_mfma_f32_16x16x32_bf16 v[176:179], v[100:103], a[196:199], v[176:179]\n"
"ds_read_b64_tr_b16 a[234:235], v236 offset:10248\n"
"v_fmac_f32_dpp v27, v252, v251 row_newbcast:3 row_mask:0xf bank_mask:0xf\n"
"v_mfma_f32_16x16x32_bf16 v[176:179], v[96:99], a[192:195], v[176:179]\n"
"s_waitcnt lgkmcnt(4)\n"
"ds_read_b64_tr_b16 v[104:105], v231 offset:8192\n"
"ds_read_b64_tr_b16 v[106:107], v231 offset:10240\n"
"v_accvgpr_read_b32 v251, a215\n"
"v_mul_f32_dpp v55, v247, v55 row_newbcast:3 row_mask:0xf bank_mask:0xf\n"
"v_mul_f32_dpp v56, v250, v56 row_newbcast:0 row_mask:0xf bank_mask:0xf\n"
"v_mul_f32_dpp v62, v250, v62 row_newbcast:2 row_mask:0xf bank_mask:0xf\n"
"v_mfma_f32_16x16x32_bf16 v[132:135], v[180:183], a[96:99], v[196:199]\n"
"v_accvgpr_read_b32 v255, a213\n"
"v_fmac_f32_dpp v31, v252, v251 row_newbcast:3 row_mask:0xf bank_mask:0xf\n"
"v_mfma_f32_16x16x32_bf16 v[132:135], v[184:187], a[100:103], v[132:135]\n"
"ds_read_b64_tr_b16 v[108:109], v235 offset:8192\n"
"ds_read_b64_tr_b16 v[116:117], v235 offset:16384\n"
"v_mfma_f32_16x16x32_bf16 v[132:135], v[188:191], a[104:107], v[132:135]\n"
"ds_read_b64_tr_b16 v[102:103], v235 offset:2048\n"
"v_fmac_f32_dpp v26, v252, v253 row_newbcast:2 row_mask:0xf bank_mask:0xf\n"
"v_mfma_f32_16x16x32_bf16 v[132:135], v[192:195], a[108:111], v[132:135]\n"
"v_accvgpr_read_b32 v253, a214\n"
"v_mul_f32_dpp v60, v250, v60 row_newbcast:0 row_mask:0xf bank_mask:0xf\n"
"v_mfma_f32_16x16x32_bf16 v[132:135], v[208:211], a[144:147], v[132:135]\n"
"s_waitcnt vmcnt(0)\n"
"ds_read_b64_tr_b16 v[96:97], v231 offset:0\n"
"v_fmac_f32_dpp v24, v252, v254 row_newbcast:0 row_mask:0xf bank_mask:0xf\n"
"v_mfma_f32_16x16x32_bf16 v[132:135], v[212:215], a[148:151], v[132:135]\n"
"v_subrev_u32 v200, s21, v200\n"
"v_accvgpr_read_b32 v254, a212\n"
"v_mfma_f32_16x16x32_bf16 v[132:135], v[216:219], a[152:155], v[132:135]\n"
"v_cmp_lt_u32 vcc, s69, v200\n"
"v_accvgpr_read_b32 v251, a227\n"
"v_mfma_f32_16x16x32_bf16 v[132:135], v[220:223], a[156:159], v[132:135]\n"
"ds_read_b64_tr_b16 a[216:217], v236 offset:8192\n"
"ds_read_b64_tr_b16 v[118:119], v235 offset:18432\n"
"v_fmac_f32_dpp v28, v252, v254 row_newbcast:0 row_mask:0xf bank_mask:0xf\n"
"v_fmac_f32_dpp v29, v252, v255 row_newbcast:1 row_mask:0xf bank_mask:0xf\n"
"v_fmac_f32_dpp v30, v252, v253 row_newbcast:2 row_mask:0xf bank_mask:0xf\n"
"v_cvt_pk_bf16_f32 v24, v24, v25\n"
"v_cvt_pk_bf16_f32 v25, v26, v27\n"
"v_cvt_pk_bf16_f32 v26, v28, v29\n"
"v_cvt_pk_bf16_f32 v27, v30, v31\n"
"v_accvgpr_read_b32 v255, a225\n"
"v_accvgpr_read_b32 v253, a226\n"
"v_fmac_f32_dpp v59, v252, v251 row_newbcast:3 row_mask:0xf bank_mask:0xf\n"
"s_cbranch_vccnz LGV_%=\nLGVK_%=:\n"
"v_mfma_f32_16x16x32_bf16 v[148:151], v[220:223], a[172:175], v[196:199]\n"
"v_accvgpr_read_b32 v254, a224\n"
"v_fmac_f32_dpp v58, v252, v253 row_newbcast:2 row_mask:0xf bank_mask:0xf\n"
"v_mfma_f32_16x16x32_bf16 v[148:151], v[216:219], a[168:171], v[148:151]\n"
"v_add_u32_dpp v200, v200, v200 quad_perm:[1,0,3,2] row_mask:0xf bank_mask:0xf\n"
"v_accvgpr_read_b32 v253, a230\n"
"v_mfma_f32_16x16x32_bf16 v[148:151], v[212:215], a[164:167], v[148:151]\n"
"v_fmac_f32_dpp v56, v252, v254 row_newbcast:0 row_mask:0xf bank_mask:0xf\n"
"v_accvgpr_read_b32 v251, a231\n"
"v_mfma_f32_16x16x32_bf16 v[148:151], v[208:211], a[160:163], v[148:151]\n"
"v_add_u32_dpp v200, v200, v200 quad_perm:[2,3,0,1] row_mask:0xf bank_mask:0xf\n"
"ds_write_b128 v243, v[24:27] offset:0\n"
"v_fmac_f32_dpp v57, v252, v255 row_newbcast:1 row_mask:0xf bank_mask:0xf\n"
"v_accvgpr_read_b32 v254, a228\n"
"s_nop 1\n"
"v_fmac_f32_dpp v60, v252, v254 row_newbcast:0 row_mask:0xf bank_mask:0xf\n"
"v_fmac_f32_dpp v62, v252, v253 row_newbcast:2 row_mask:0xf bank_mask:0xf\n"
"v_fmac_f32_dpp v63, v252, v251 row_newbcast:3 row_mask:0xf bank_mask:0xf\n"
"v_cvt_pk_bf16_f32 v56, v56, v57\n"
"v_cvt_pk_bf16_f32 v57, v58, v59\n"
"v_cvt_pk_bf16_f32 v59, v62, v63\n"
"v_mfma_f32_16x16x32_bf16 v[148:151], v[192:195], a[124:127], v[148:151]\n"
"ds_read_b64_tr_b16 v[100:101], v235 offset:0\n"
"v_accvgpr_read_b32 v255, a229\n"
"v_mfma_f32_16x16x32_bf16 v[148:151], v[188:191], a[120:123], v[148:151]\n"
"v_cmp_lt_u32 vcc, 1, v200\n"
"v_fmac_f32_dpp v61, v252, v255 row_newbcast:1 row_mask:0xf bank_mask:0xf\n"
"v_mfma_f32_16x16x32_bf16 v[148:151], v[184:187], a[116:119], v[148:151]\n"
"ds_read_b64_tr_b16 v[98:99], v231 offset:2048\n"
"v_cvt_pk_bf16_f32 v58, v60, v61\n"
"v_mfma_f32_16x16x32_bf16 v[148:151], v[180:183], a[112:115], v[148:151]\n"
"ds_write_b128 v243, v[56:59] offset:2048\n"
"v_mul_f32_dpp v22, v246, v22 row_newbcast:2 row_mask:0xf bank_mask:0xf\n"
"v_mul_f32_dpp v48, v247, v48 row_newbcast:0 row_mask:0xf bank_mask:0xf\n"
"v_mul_f32_dpp v49, v247, v49 row_newbcast:1 row_mask:0xf bank_mask:0xf\n"
"v_mul_f32_dpp v87, v206, v87 row_newbcast:3 row_mask:0xf bank_mask:0xf\n"
"v_mfma_f32_16x16x32_bf16 v[164:167], v[180:183], a[128:131], v[196:199]\n"
"s_mov_b32 s47, 0\n"
"ds_read_b64_tr_b16 v[110:111], v235 offset:10240\n"
"v_bfe_u32 v254, v242, 4, 4\n"
"v_mfma_f32_16x16x32_bf16 v[164:167], v[184:187], a[132:135], v[164:167]\n"
"s_mov_b32 s89, vcc_lo\n"
"v_cvt_pk_bf16_f32 v16, v16, v17\n"
"v_mul_f32_dpp v81, v206, v81 row_newbcast:1 row_mask:0xf bank_mask:0xf\n"
"v_mfma_f32_16x16x32_bf16 v[164:167], v[188:191], a[136:139], v[164:167]\n"
"s_lshr_b32 s90, s89, s93\n"
"ds_read_b64_tr_b16 v[112:113], v231 offset:16384\n"
"v_xor_b32 v249, s75, v249\n"
"v_mfma_f32_16x16x32_bf16 v[164:167], v[192:195], a[140:143], v[164:167]\n"
"s_and_b32 s90, s90, 1\n"
"ds_read_b64_tr_b16 v[122:123], v231 offset:26624\n"
"v_mul_f32_dpp v51, v247, v51 row_newbcast:3 row_mask:0xf bank_mask:0xf\n"
"v_mfma_f32_16x16x32_bf16 v[164:167], v[208:211], a[176:179], v[164:167]\n"
"s_lshl_b32 s16, s61, 7\n"
"v_cvt_pk_bf16_f32 v17, v18, v19\n"
"v_cvt_pk_bf16_f32 v19, v22, v23\n"
"v_mfma_f32_16x16x32_bf16 v[164:167], v[212:215], a[180:183], v[164:167]\n"
"s_add_u32 s90, s90, s16\n"
"ds_read_b64_tr_b16 v[120:121], v231 offset:24576\n"
"ds_read_b64_tr_b16 v[224:225], v235 offset:24576\n"
"v_mfma_f32_16x16x32_bf16 v[164:167], v[216:219], a[184:187], v[164:167]\n"
"s_waitcnt vmcnt(63) lgkmcnt(5)\n"
"s_lshl_b32 s90, s90, 13\n"
"v_mul_f32_dpp v20, v246, v20 row_newbcast:0 row_mask:0xf bank_mask:0xf\n"
"v_mfma_f32_16x16x32_bf16 v[164:167], v[220:223], a[188:191], v[164:167]\n"
"s_barrier\n"
"s_add_u32 s16, s20, s90\n"
"v_mul_f32_dpp v50, v247, v50 row_newbcast:2 row_mask:0xf bank_mask:0xf\n"
"v_mul_f32_dpp v52, v247, v52 row_newbcast:0 row_mask:0xf bank_mask:0xf\n"
"v_cvt_pk_bf16_f32 v48, v48, v49\n"
"v_cvt_pk_bf16_f32 v49, v50, v51\n"
"v_mul_f32_dpp v80, v206, v80 row_newbcast:0 row_mask:0xf bank_mask:0xf\n"
"v_mfma_f32_16x16x32_bf16 v[136:139], v[96:99], a[96:99], v[196:199]\n"
"s_add_u32 s17, s16, 4096\n"
"ds_read_b64_tr_b16 v[180:181], v228 offset:32768\n"
"v_mul_f32_dpp v84, v206, v84 row_newbcast:0 row_mask:0xf bank_mask:0xf\n"
"v_mfma_f32_16x16x32_bf16 v[136:139], v[100:103], a[100:103], v[136:139]\n"
"s_mov_b32 s72, s61\n"
"ds_read_b64_tr_b16 v[182:183], v228 offset:34816\n"
"v_mul_f32_dpp v21, v246, v21 row_newbcast:1 row_mask:0xf bank_mask:0xf\n"
"v_mfma_f32_16x16x32_bf16 v[136:139], v[104:107], a[104:107], v[136:139]\n"
"s_waitcnt lgkmcnt(5)\n"
"s_mov_b32 s21, s60\n"
"ds_read_b64_tr_b16 a[208:209], v236 offset:12288\n"
"v_mul_f32_dpp v82, v206, v82 row_newbcast:2 row_mask:0xf bank_mask:0xf\n"
"v_mfma_f32_16x16x32_bf16 v[136:139], v[108:111], a[108:111], v[136:139]\n"
"s_mov_b32 s18, s77\n"
"v_cvt_pk_bf16_f32 v80, v80, v81\n"
"v_cvt_pk_bf16_f32 v81, v82, v83\n"
"v_mfma_f32_16x16x32_bf16 v[136:139], v[112:115], a[144:147], v[136:139]\n"
"s_mov_b32 s19, s77\n"
"v_mul_f32_dpp v85, v206, v85 row_newbcast:1 row_mask:0xf bank_mask:0xf\n"
"v_cvt_pk_bf16_f32 v83, v86, v87\n"
"v_mfma_f32_16x16x32_bf16 v[136:139], v[116:119], a[148:151], v[136:139]\n"
"s_waitcnt lgkmcnt(3)\n"
"s_mov_b32 s69, -1\n"
"ds_read_b64_tr_b16 v[218:219], v228 offset:59392\n"
"v_cvt_pk_bf16_f32 v18, v20, v21\n"
"v_mfma_f32_16x16x32_bf16 v[136:139], v[120:123], a[152:155], v[136:139]\n"
"s_mul_i32 s88, s61, 0x10011111\n"
"v_xor_b32 v248, s75, v248\n"
"v_cvt_pk_bf16_f32 v82, v84, v85\n"
"v_mfma_f32_16x16x32_bf16 v[136:139], v[224:227], a[156:159], v[136:139]\n"
"s_add_u32 s88, s88, 484675\n"
"ds_write_b128 v248, v[16:19] offset:49152\n"
"v_mul_f32_dpp v53, v247, v53 row_newbcast:1 row_mask:0xf bank_mask:0xf\n"
"v_mul_f32_dpp v54, v247, v54 row_newbcast:2 row_mask:0xf bank_mask:0xf\n"
"v_cvt_pk_bf16_f32 v50, v52, v53\n"
"v_cvt_pk_bf16_f32 v51, v54, v55\n"
"v_mfma_f32_16x16x32_bf16 v[152:155], v[224:227], a[172:175], v[196:199]\n"
"s_and_b32 s88, s88, 0x70077777\n"
"ds_read_b64_tr_b16 a[8:9], v249 offset:49152\n"
"ds_read_b64_tr_b16 a[10:11], v249 offset:49160\n"
"v_mfma_f32_16x16x32_bf16 v[152:155], v[120:123], a[168:171], v[152:155]\n"
"s_or_b32 s88, s88, 0x9800000\n"
"ds_write_b128 v248, v[48:51] offset:50176\n"
"ds_write_b128 v248, v[80:83] offset:51200\n"
"v_mfma_f32_16x16x32_bf16 v[152:155], v[116:119], a[164:167], v[152:155]\n"
"s_mov_b32 s84, s6\n"
"ds_read_b64_tr_b16 a[24:25], v249 offset:50176\n"
"ds_read_b64_tr_b16 a[26:27], v249 offset:50184\n"
"ds_read_b64_tr_b16 a[40:41], v249 offset:51200\n"
"ds_read_b64_tr_b16 a[42:43], v249 offset:51208\n"
"v_mfma_f32_16x16x32_bf16 v[152:155], v[112:115], a[160:163], v[152:155]\n"
"s_and_b32 s85, s7, 0xffff\n"
"ds_read_b64_tr_b16 v[188:189], v228 offset:40960\n"
"v_xor_b32 v248, s75, v248\n"
"v_mfma_f32_16x16x32_bf16 v[152:155], v[108:111], a[124:127], v[152:155]\n"
"s_mov_b32 s86, 128\n"
"ds_read_b64_tr_b16 v[184:185], v232 offset:32768\n"
"ds_read_b64_tr_b16 v[214:215], v232 offset:51200\n"
"v_mfma_f32_16x16x32_bf16 v[152:155], v[104:107], a[120:123], v[152:155]\n"
"s_mov_b32 s87, 131072\n"
"ds_read_b64_tr_b16 a[214:215], v237 offset:12296\n"
"ds_read_b64_tr_b16 v[190:191], v228 offset:43008\n"
"v_mfma_f32_16x16x32_bf16 v[152:155], v[100:103], a[116:119], v[152:155]\n"
"ds_read_b64_tr_b16 a[224:225], v236 offset:14336\n"
"ds_read_b64_tr_b16 v[212:213], v232 offset:49152\n"
"v_mfma_f32_16x16x32_bf16 v[152:155], v[96:99], a[112:115], v[152:155]\n"
"ds_read_b64_tr_b16 v[186:187], v232 offset:34816\n"
"v_mov_b64 v[84:85], 0\n"
"v_mfma_f32_16x16x32_bf16 v[168:171], v[96:99], a[128:131], v[196:199]\n"
"ds_read_b64_tr_b16 v[192:193], v232 offset:40960\n"
"ds_read_b64_tr_b16 v[222:223], v232 offset:59392\n"
"v_mfma_f32_16x16x32_bf16 v[168:171], v[100:103], a[132:135], v[168:171]\n"
"ds_read_b64_tr_b16 v[194:195], v232 offset:43008\n"
"v_bfe_u32 v255, v242, 8, 1\n"
"v_mfma_f32_16x16x32_bf16 v[168:171], v[104:107], a[136:139], v[168:171]\n"
"ds_read_b64_tr_b16 v[208:209], v228 offset:49152\n"
"ds_read_b64_tr_b16 v[210:211], v228 offset:51200\n"
"v_mfma_f32_16x16x32_bf16 v[168:171], v[108:111], a[140:143], v[168:171]\n"
"s_waitcnt lgkmcnt(12)\n"
"ds_read_b64_tr_b16 a[230:231], v237 offset:14344\n"
"ds_read_b64_tr_b16 v[216:217], v228 offset:57344\n"
"v_mfma_f32_16x16x32_bf16 v[168:171], v[112:115], a[176:179], v[168:171]\n"
"ds_read_b64_tr_b16 v[220:221], v232 offset:57344\n"
"v_xor_b32 v249, s75, v249\n"
"v_mfma_f32_16x16x32_bf16 v[168:171], v[116:119], a[180:183], v[168:171]\n"
"ds_read_b64_tr_b16 a[222:223], v237 offset:8200\n"
"ds_read_b64_tr_b16 a[226:227], v236 offset:14344\n"
"v_mfma_f32_16x16x32_bf16 v[168:171], v[120:123], a[184:187], v[168:171]\n"
"s_add_u32 s41, s70, 327680\n"
"s_add_u32 m0, s71, 32768\n"
"ds_read_b64_tr_b16 v[110:111], v233 offset:43008\n"
"v_mfma_f32_16x16x32_bf16 v[168:171], v[224:227], a[188:191], v[168:171]\n"
"buffer_load_dwordx4 v240, s[28:31], s41 offen lds\n"
"v_mfma_f32_16x16x32_bf16 v[124:127], v[180:183], a[48:51], v[124:127]\n"
"s_waitcnt lgkmcnt(9)\n"
"v_mul_u32_u24 v254, 2560, v254\n"
"v_mfma_f32_16x16x32_bf16 v[124:127], v[184:187], a[52:55], v[124:127]\n"
"v_mov_b64 v[32:33], s[22:23]\n"
"v_mov_b64 v[44:45], s[22:23]\n"
"v_mfma_f32_16x16x32_bf16 v[124:127], v[188:191], a[56:59], v[124:127]\n"
"s_waitcnt lgkmcnt(6)\n"
"s_add_u32 s40, s41, s52\n"
"ds_read_b64_tr_b16 a[212:213], v237 offset:12288\n"
"ds_read_b64_tr_b16 v[102:103], v233 offset:34816\n"
"v_mfma_f32_16x16x32_bf16 v[124:127], v[192:195], a[60:63], v[124:127]\n"
"buffer_load_dwordx4 v240, s[28:31], s40 offen offset:1024 lds\n"
"v_mfma_f32_16x16x32_bf16 v[124:127], v[208:211], a[0:3], v[124:127]\n"
"v_mov_b64 v[38:39], s[22:23]\n"
"v_mfma_f32_16x16x32_bf16 v[124:127], v[212:215], a[4:7], v[124:127]\n"
"s_waitcnt lgkmcnt(3)\n"
"v_mov_b64 v[244:245], 0\n"
"v_mfma_f32_16x16x32_bf16 v[124:127], v[216:219], a[8:11], v[124:127]\n"
"ds_read_b64_tr_b16 v[98:99], v229 offset:34816\n"
"ds_read_b64_tr_b16 v[116:117], v233 offset:49152\n"
"v_mfma_f32_16x16x32_bf16 v[124:127], v[220:223], a[12:15], v[124:127]\n"
"s_add_u32 s40, s41, s53\n"
"ds_read_b64_tr_b16 v[122:123], v229 offset:59392\n"
"ds_read_b64_tr_b16 v[224:225], v233 offset:57344\n"
"v_mfma_f32_16x16x32_bf16 v[140:143], v[220:223], a[28:31], v[140:143]\n"
"buffer_load_dwordx4 v240, s[28:31], s40 offen offset:2048 lds\n"
"v_mfma_f32_16x16x32_bf16 v[140:143], v[216:219], a[24:27], v[140:143]\n"
"v_mov_b64 v[16:17], s[22:23]\n"
"v_mov_b64 v[36:37], s[22:23]\n"
"v_mfma_f32_16x16x32_bf16 v[140:143], v[212:215], a[20:23], v[140:143]\n"
"v_mov_b64 v[246:247], 0\n"
"v_subrev_f32 v124, s22, v124\n"
"v_mfma_f32_16x16x32_bf16 v[140:143], v[208:211], a[16:19], v[140:143]\n"
"ds_read_b64_tr_b16 a[210:211], v236 offset:12296\n"
"ds_read_b64_tr_b16 v[106:107], v229 offset:43008\n"
"v_mfma_f32_16x16x32_bf16 v[140:143], v[192:195], a[76:79], v[140:143]\n"
"ds_read_b64_tr_b16 v[112:113], v229 offset:49152\n"
"v_xor_b32 v228, s75, v228\n"
"v_mfma_f32_16x16x32_bf16 v[140:143], v[188:191], a[72:75], v[140:143]\n"
"ds_read_b64_tr_b16 v[104:105], v229 offset:40960\n"
"v_mov_b64 v[26:27], s[22:23]\n"
"v_mfma_f32_16x16x32_bf16 v[140:143], v[184:187], a[68:71], v[140:143]\n"
"s_add_u32 s40, s41, s54\n"
"ds_read_b64_tr_b16 a[228:229], v237 offset:14336\n"
"ds_read_b64_tr_b16 v[118:119], v233 offset:51200\n"
"v_mfma_f32_16x16x32_bf16 v[140:143], v[180:183], a[64:67], v[140:143]\n"
"buffer_load_dwordx4 v240, s[28:31], s40 offen offset:3072 lds\n"
"v_mfma_f32_16x16x32_bf16 v[156:159], v[180:183], a[80:83], v[156:159]\n"
"s_add_u32 s41, s70, 491520\n"
"v_mov_b64 v[86:87], 0\n"
"v_mfma_f32_16x16x32_bf16 v[156:159], v[184:187], a[84:87], v[156:159]\n"
"s_add_u32 m0, s71, 49152\n"
"ds_read_b64_tr_b16 v[114:115], v229 offset:51200\n"
"ds_read_b64_tr_b16 v[226:227], v233 offset:59392\n"
"v_mfma_f32_16x16x32_bf16 v[156:159], v[188:191], a[88:91], v[156:159]\n"
"buffer_load_dwordx4 v240, s[28:31], s41 offen lds\n"
"v_mfma_f32_16x16x32_bf16 v[156:159], v[192:195], a[92:95], v[156:159]\n"
"v_and_b32 v243, 0x3f0, v242\n"
"v_subrev_f32 v142, s22, v142\n"
"v_mfma_f32_16x16x32_bf16 v[156:159], v[208:211], a[32:35], v[156:159]\n"
"v_mov_b64 v[6:7], s[22:23]\n"
"v_subrev_f32 v126, s22, v126\n"
"v_mfma_f32_16x16x32_bf16 v[156:159], v[212:215], a[36:39], v[156:159]\n"
"ds_read_b64_tr_b16 a[218:219], v236 offset:8200\n"
"ds_read_b64_tr_b16 v[96:97], v229 offset:32768\n"
"v_mfma_f32_16x16x32_bf16 v[156:159], v[216:219], a[40:43], v[156:159]\n"
"s_add_u32 s40, s41, s52\n"
"ds_read_b64_tr_b16 v[100:101], v233 offset:32768\n"
"ds_read_b64_tr_b16 v[108:109], v233 offset:40960\n"
"v_mfma_f32_16x16x32_bf16 v[156:159], v[220:223], a[44:47], v[156:159]\n"
"s_waitcnt lgkmcnt(6)\n"
"buffer_load_dwordx4 v240, s[28:31], s40 offen offset:1024 lds\n"
"v_mfma_f32_16x16x32_bf16 v[172:175], v[220:223], a[228:231], v[172:175]\n"
"v_subrev_f32 v143, s22, v143\n"
"v_xor_b32 v233, s75, v233\n"
"v_mfma_f32_16x16x32_bf16 v[172:175], v[216:219], a[224:227], v[172:175]\n"
"v_lshl_add_u32 v254, v255, 5, v254\n"
"v_subrev_f32 v125, s22, v125\n"
"v_mfma_f32_16x16x32_bf16 v[172:175], v[212:215], a[212:215], v[172:175]\n"
"s_add_u32 s40, s41, s53\n"
"ds_read_b64_tr_b16 v[120:121], v229 offset:57344\n"
"v_mfma_f32_16x16x32_bf16 v[172:175], v[208:211], a[208:211], v[172:175]\n"
"buffer_load_dwordx4 v240, s[28:31], s40 offen offset:2048 lds\n"
"v_mfma_f32_16x16x32_bf16 v[172:175], v[192:195], a[236:239], v[172:175]\n"
"v_add_u32 v236, s94, v243\n"
"v_mfma_f32_16x16x32_bf16 v[172:175], v[188:191], a[232:235], v[172:175]\n"
"v_bfe_u32 v255, v242, 9, 1\n"
"v_subrev_f32 v141, s22, v141\n"
"v_mfma_f32_16x16x32_bf16 v[172:175], v[184:187], a[220:223], v[172:175]\n"
"s_waitcnt lgkmcnt(1)\n"
"ds_read_b64_tr_b16 v[184:185], v234 offset:32768\n"
"v_mov_b64 v[54:55], s[22:23]\n"
"v_mfma_f32_16x16x32_bf16 v[172:175], v[180:183], a[216:219], v[172:175]\n"
"s_add_u32 s40, s41, s54\n"
"ds_read_b64_tr_b16 v[210:211], v230 offset:51200\n"
"v_mfma_f32_16x16x32_bf16 v[128:131], v[96:99], a[48:51], v[128:131]\n"
"buffer_load_dwordx4 v240, s[28:31], s40 offen offset:3072 lds\n"
"v_mfma_f32_16x16x32_bf16 v[128:131], v[100:103], a[52:55], v[128:131]\n"
"s_lshl_b32 s62, s70, 9\n"
"v_mov_b64 v[30:31], s[22:23]\n"
"v_xor_b32 v232, s75, v232\n"
"v_mfma_f32_16x16x32_bf16 v[128:131], v[104:107], a[56:59], v[128:131]\n"
"s_and_b32 s70, s88, 15\n"
"v_cvt_pk_bf16_f32 v124, v124, v125\n"
"v_mfma_f32_16x16x32_bf16 v[128:131], v[108:111], a[60:63], v[128:131]\n"
"s_lshr_b32 s88, s88, 4\n"
"ds_read_b64_tr_b16 v[218:219], v230 offset:59392\n"
"v_subrev_f32 v127, s22, v127\n"
"v_mfma_f32_16x16x32_bf16 v[128:131], v[112:115], a[0:3], v[128:131]\n"
"s_lshl_b32 s70, s70, 7\n"
"ds_read_b64_tr_b16 v[194:195], v234 offset:43008\n"
"v_bfe_u32 v237, v242, 4, 3\n"
"v_mfma_f32_16x16x32_bf16 v[128:131], v[116:119], a[4:7], v[128:131]\n"
"s_waitcnt lgkmcnt(4)\n"
"s_xor_b32 s71, s71, s75\n"
"v_lshl_add_u32 v243, s44, 2, v243\n"
"v_mov_b64 v[12:13], s[22:23]\n"
"v_mfma_f32_16x16x32_bf16 v[128:131], v[120:123], a[8:11], v[128:131]\n"
"v_lshlrev_b32_e64 v201, 2, s72\n"
"ds_read_b64_tr_b16 v[214:215], v234 offset:51200\n"
"v_cvt_pk_bf16_f32 v125, v126, v127\n"
"v_mfma_f32_16x16x32_bf16 v[128:131], v[224:227], a[12:15], v[128:131]\n"
"global_load_dword v200, v201, s[6:7] sc1\n"
"v_mfma_f32_16x16x32_bf16 v[144:147], v[224:227], a[28:31], v[144:147]\n"
"v_subrev_f32 v157, s22, v157\n"
"v_subrev_f32 v174, s22, v174\n"
"v_mfma_f32_16x16x32_bf16 v[144:147], v[120:123], a[24:27], v[144:147]\n"
"v_mov_b64 v[52:53], s[22:23]\n"
"v_subrev_f32 v156, s22, v156\n"
"v_mfma_f32_16x16x32_bf16 v[144:147], v[116:119], a[20:23], v[144:147]\n"
"ds_read_b64_tr_b16 v[188:189], v230 offset:40960\n"
"v_subrev_f32 v172, s22, v172\n"
"v_mfma_f32_16x16x32_bf16 v[144:147], v[112:115], a[16:19], v[144:147]\n"
"v_subrev_f32 v140, s22, v140\n"
"v_mfma_f32_16x16x32_bf16 v[144:147], v[108:111], a[76:79], v[144:147]\n"
"ds_read_b64_tr_b16 v[180:181], v230 offset:32768\n"
"v_subrev_f32 v130, s22, v130\n"
"v_mfma_f32_16x16x32_bf16 v[144:147], v[104:107], a[72:75], v[144:147]\n"
"v_lshl_add_u32 v254, v255, 4, v254\n"
"v_mov_b64 v[22:23], s[22:23]\n"
"v_mfma_f32_16x16x32_bf16 v[144:147], v[100:103], a[68:71], v[144:147]\n"
"ds_read_b64_tr_b16 v[192:193], v234 offset:40960\n"
"v_subrev_f32 v128, s22, v128\n"
"v_mfma_f32_16x16x32_bf16 v[144:147], v[96:99], a[64:67], v[144:147]\n"
"v_subrev_f32 v129, s22, v129\n"
"v_cvt_pk_bf16_f32 v126, v128, v129\n"
"v_mfma_f32_16x16x32_bf16 v[160:163], v[96:99], a[80:83], v[160:163]\n"
"ds_read_b64_tr_b16 v[186:187], v234 offset:34816\n"
"v_cvt_pk_bf16_f32 v156, v156, v157\n"
"v_mfma_f32_16x16x32_bf16 v[160:163], v[100:103], a[84:87], v[160:163]\n"
"ds_read_b64_tr_b16 v[190:191], v230 offset:43008\n"
"v_subrev_f32 v131, s22, v131\n"
"v_mfma_f32_16x16x32_bf16 v[160:163], v[104:107], a[88:91], v[160:163]\n"
"v_cvt_pk_bf16_f32 v127, v130, v131\n"
"v_subrev_f32 v146, s22, v146\n"
"v_mfma_f32_16x16x32_bf16 v[160:163], v[108:111], a[92:95], v[160:163]\n"
"ds_read_b64_tr_b16 v[220:221], v234 offset:57344\n"
"v_cvt_pk_bf16_f32 v140, v140, v141\n"
"v_mfma_f32_16x16x32_bf16 v[160:163], v[112:115], a[32:35], v[160:163]\n"
"v_lshl_add_u32 v248, v242, 8, s93\n"
"v_subrev_f32 v145, s22, v145\n"
"v_mfma_f32_16x16x32_bf16 v[160:163], v[116:119], a[36:39], v[160:163]\n"
"ds_read_b64_tr_b16 v[182:183], v230 offset:34816\n"
"v_cvt_pk_bf16_f32 v141, v142, v143\n"
"v_mfma_f32_16x16x32_bf16 v[160:163], v[120:123], a[40:43], v[160:163]\n"
"ds_read_b64_tr_b16 v[208:209], v230 offset:49152\n"
"v_subrev_f32 v159, s22, v159\n"
"v_mfma_f32_16x16x32_bf16 v[160:163], v[224:227], a[44:47], v[160:163]\n"
"buffer_store_dwordx4 v[124:127], v243, s[32:35], s16 offen sc1\n"
"v_mov_b64 v[24:25], s[22:23]\n"
"v_mfma_f32_16x16x32_bf16 v[176:179], v[224:227], a[228:231], v[176:179]\n"
"ds_read_b64_tr_b16 v[212:213], v234 offset:49152\n"
"v_mov_b64 v[50:51], s[22:23]\n"
"v_mfma_f32_16x16x32_bf16 v[176:179], v[120:123], a[224:227], v[176:179]\n"
"v_subrev_f32 v158, s22, v158\n"
"v_subrev_f32 v173, s22, v173\n"
"v_mfma_f32_16x16x32_bf16 v[176:179], v[116:119], a[212:215], v[176:179]\n"
"ds_read_b64_tr_b16 v[216:217], v230 offset:57344\n"
"v_mov_b64 v[94:95], 0\n"
"v_mfma_f32_16x16x32_bf16 v[176:179], v[112:115], a[208:211], v[176:179]\n"
"v_cvt_pk_bf16_f32 v172, v172, v173\n"
"v_mfma_f32_16x16x32_bf16 v[176:179], v[108:111], a[236:239], v[176:179]\n"
"ds_read_b64_tr_b16 v[112:113], v231 offset:49152\n"
"v_mov_b64 v[4:5], s[22:23]\n"
"v_mfma_f32_16x16x32_bf16 v[176:179], v[104:107], a[232:235], v[176:179]\n"
"ds_read_b64_tr_b16 v[222:223], v234 offset:59392\n"
"v_subrev_f32 v144, s22, v144\n"
"v_mfma_f32_16x16x32_bf16 v[176:179], v[100:103], a[220:223], v[176:179]\n"
"v_mov_b64 v[40:41], s[22:23]\n"
"v_subrev_f32 v147, s22, v147\n"
"v_mfma_f32_16x16x32_bf16 v[176:179], v[96:99], a[216:219], v[176:179]\n"
"s_waitcnt lgkmcnt(4)\n"
"ds_read_b64_tr_b16 v[224:225], v235 offset:57344\n"
"v_subrev_f32 v175, s22, v175\n"
"v_mfma_f32_16x16x32_bf16 v[132:135], v[180:183], a[48:51], v[132:135]\n"
"ds_read_b64_tr_b16 v[102:103], v235 offset:34816\n"
"v_mov_b32 v241, s60\n"
"v_mfma_f32_16x16x32_bf16 v[132:135], v[184:187], a[52:55], v[132:135]\n"
"ds_read_b64_tr_b16 v[96:97], v231 offset:32768\n"
"v_cvt_pk_bf16_f32 v157, v158, v159\n"
"v_mfma_f32_16x16x32_bf16 v[132:135], v[188:191], a[56:59], v[132:135]\n"
"ds_read_b64_tr_b16 v[114:115], v231 offset:51200\n"
"v_subrev_f32 v160, s22, v160\n"
"v_mfma_f32_16x16x32_bf16 v[132:135], v[192:195], a[60:63], v[132:135]\n"
"v_cvt_pk_bf16_f32 v142, v144, v145\n"
"v_subrev_f32 v161, s22, v161\n"
"v_mfma_f32_16x16x32_bf16 v[132:135], v[208:211], a[0:3], v[132:135]\n"
"s_waitcnt lgkmcnt(5)\n"
"ds_read_b64_tr_b16 v[98:99], v231 offset:34816\n"
"v_mfma_f32_16x16x32_bf16 v[132:135], v[212:215], a[4:7], v[132:135]\n"
"v_cvt_pk_bf16_f32 v143, v146, v147\n"
"v_mfma_f32_16x16x32_bf16 v[132:135], v[216:219], a[8:11], v[132:135]\n"
"s_waitcnt lgkmcnt(5)\n"
"ds_read_b64_tr_b16 v[116:117], v235 offset:49152\n"
"v_mov_b64 v[34:35], s[22:23]\n"
"v_mfma_f32_16x16x32_bf16 v[132:135], v[220:223], a[12:15], v[132:135]\n"
"buffer_store_dwordx4 v[140:143], v243, s[32:35], s16 offen offset:2048 sc1\n"
"ds_read_b64_tr_b16 v[106:107], v231 offset:43008\n"
"v_mfma_f32_16x16x32_bf16 v[148:151], v[220:223], a[28:31], v[148:151]\n"
"v_cvt_pk_bf16_f32 v173, v174, v175\n"
"v_subrev_f32 v176, s22, v176\n"
"v_mfma_f32_16x16x32_bf16 v[148:151], v[216:219], a[24:27], v[148:151]\n"
"ds_read_b64_tr_b16 v[100:101], v235 offset:32768\n"
"v_max_u32 v237, 1, v237\n"
"v_mfma_f32_16x16x32_bf16 v[148:151], v[212:215], a[20:23], v[148:151]\n"
"v_mov_b64 v[14:15], s[22:23]\n"
"v_mov_b64 v[46:47], s[22:23]\n"
"v_mfma_f32_16x16x32_bf16 v[148:151], v[208:211], a[16:19], v[148:151]\n"
"ds_read_b64_tr_b16 v[104:105], v231 offset:40960\n"
"v_mov_b64 v[42:43], s[22:23]\n"
"v_mfma_f32_16x16x32_bf16 v[148:151], v[192:195], a[76:79], v[148:151]\n"
"ds_read_b64_tr_b16 v[120:121], v231 offset:57344\n"
"v_mov_b64 v[2:3], s[22:23]\n"
"v_mfma_f32_16x16x32_bf16 v[148:151], v[188:191], a[72:75], v[148:151]\n"
"v_subrev_f32 v162, s22, v162\n"
"v_subrev_f32 v179, s22, v179\n"
"v_mfma_f32_16x16x32_bf16 v[148:151], v[184:187], a[68:71], v[148:151]\n"
"ds_read_b64_tr_b16 v[122:123], v231 offset:59392\n"
"v_xor_b32 v229, s75, v229\n"
"v_mfma_f32_16x16x32_bf16 v[148:151], v[180:183], a[64:67], v[148:151]\n"
"ds_read_b64_tr_b16 v[110:111], v235 offset:43008\n"
"v_add_u32 v238, s97, v254\n"
"v_mfma_f32_16x16x32_bf16 v[164:167], v[180:183], a[80:83], v[164:167]\n"
"v_add_u32 v252, s98, v254\n"
"v_add_u32 v241, 1, v241\n"
"v_mfma_f32_16x16x32_bf16 v[164:167], v[184:187], a[84:87], v[164:167]\n"
"ds_read_b64_tr_b16 v[108:109], v235 offset:40960\n"
"v_mov_b64 v[10:11], s[22:23]\n"
"v_mfma_f32_16x16x32_bf16 v[164:167], v[188:191], a[88:91], v[164:167]\n"
"v_mov_b64 v[28:29], s[22:23]\n"
"v_subrev_f32 v177, s22, v177\n"
"v_mfma_f32_16x16x32_bf16 v[164:167], v[192:195], a[92:95], v[164:167]\n"
"ds_read_b64_tr_b16 v[118:119], v235 offset:51200\n"
"v_subrev_f32 v132, s22, v132\n"
"v_mfma_f32_16x16x32_bf16 v[164:167], v[208:211], a[32:35], v[164:167]\n"
"v_subrev_f32 v163, s22, v163\n"
"v_subrev_f32 v135, s22, v135\n"
"v_mfma_f32_16x16x32_bf16 v[164:167], v[212:215], a[36:39], v[164:167]\n"
"ds_read_b64_tr_b16 v[226:227], v235 offset:59392\n"
"v_cvt_pk_bf16_f32 v159, v162, v163\n"
"v_mfma_f32_16x16x32_bf16 v[164:167], v[216:219], a[40:43], v[164:167]\n"
"v_xor_b32 v237, s61, v237\n"
"v_mov_b64 v[18:19], s[22:23]\n"
"v_mfma_f32_16x16x32_bf16 v[164:167], v[220:223], a[44:47], v[164:167]\n"
"s_barrier\n"
"s_waitcnt lgkmcnt(3)\n"
"v_mfma_f32_16x16x32_bf16 v[136:139], v[96:99], a[48:51], v[136:139]\n"
"ds_read_b64_tr_b16 v[214:215], v232 offset:18432\n"
"v_mfma_f32_16x16x32_bf16 v[136:139], v[100:103], a[52:55], v[136:139]\n"
"ds_read_b64_tr_b16 v[212:213], v232 offset:16384\n"
"v_cvt_pk_bf16_f32 v158, v160, v161\n"
"v_mfma_f32_16x16x32_bf16 v[136:139], v[104:107], a[56:59], v[136:139]\n"
"s_waitcnt lgkmcnt(4)\n"
"ds_read_b64_tr_b16 v[220:221], v232 offset:24576\n"
"v_subrev_f32 v178, s22, v178\n"
"v_mfma_f32_16x16x32_bf16 v[136:139], v[108:111], a[60:63], v[136:139]\n"
"ds_read_b64_tr_b16 v[180:181], v228 offset:0\n"
"v_mfma_f32_16x16x32_bf16 v[136:139], v[112:115], a[0:3], v[136:139]\n"
"s_waitcnt lgkmcnt(5)\n"
"ds_read_b64_tr_b16 v[194:195], v232 offset:10240\n"
"v_mfma_f32_16x16x32_bf16 v[136:139], v[116:119], a[4:7], v[136:139]\n"
"ds_read_b64_tr_b16 v[188:189], v228 offset:8192\n"
"v_add_u32 v253, s99, v254\n"
"v_mfma_f32_16x16x32_bf16 v[136:139], v[120:123], a[8:11], v[136:139]\n"
"s_waitcnt lgkmcnt(6)\n"
"ds_read_b64_tr_b16 v[184:185], v232 offset:0\n"
"v_subrev_f32 v133, s22, v133\n"
"v_mfma_f32_16x16x32_bf16 v[136:139], v[224:227], a[12:15], v[136:139]\n"
"buffer_store_dwordx4 v[156:159], v243, s[32:35], s17 offen sc1\n"
"ds_read_b64_tr_b16 v[222:223], v232 offset:26624\n"
"v_mfma_f32_16x16x32_bf16 v[152:155], v[224:227], a[28:31], v[152:155]\n"
"ds_read_b64_tr_b16 v[208:209], v228 offset:16384\n"
"v_mfma_f32_16x16x32_bf16 v[152:155], v[120:123], a[24:27], v[152:155]\n"
"ds_read_b64_tr_b16 v[190:191], v228 offset:10240\n"
"v_mfma_f32_16x16x32_bf16 v[152:155], v[116:119], a[20:23], v[152:155]\n"
"ds_read_b64_tr_b16 v[186:187], v232 offset:2048\n"
"v_subrev_f32 v134, s22, v134\n"
"v_mfma_f32_16x16x32_bf16 v[152:155], v[112:115], a[16:19], v[152:155]\n"
"ds_read_b64_tr_b16 v[218:219], v228 offset:26624\n"
"v_mfma_f32_16x16x32_bf16 v[152:155], v[108:111], a[76:79], v[152:155]\n"
"ds_read_b64_tr_b16 v[192:193], v232 offset:8192\n"
"v_mfma_f32_16x16x32_bf16 v[152:155], v[104:107], a[72:75], v[152:155]\n"
"ds_read_b64_tr_b16 v[182:183], v228 offset:2048\n"
"v_xor_b32 v239, 64, v238\n"
"v_mfma_f32_16x16x32_bf16 v[152:155], v[100:103], a[68:71], v[152:155]\n"
"ds_read_b64_tr_b16 v[210:211], v228 offset:18432\n"
"ds_read_b64_tr_b16 v[216:217], v228 offset:24576\n"
"v_mfma_f32_16x16x32_bf16 v[152:155], v[96:99], a[64:67], v[152:155]\n"
"v_mfma_f32_16x16x32_bf16 v[168:171], v[96:99], a[80:83], v[168:171]\n"
"ds_read_b64_tr_b16 v[96:97], v229 offset:0\n"
"ds_read_b64_tr_b16 v[98:99], v229 offset:2048\n"
"v_mfma_f32_16x16x32_bf16 v[168:171], v[100:103], a[84:87], v[168:171]\n"
"ds_read_b64_tr_b16 v[100:101], v233 offset:0\n"
"ds_read_b64_tr_b16 v[102:103], v233 offset:2048\n"
"v_mfma_f32_16x16x32_bf16 v[168:171], v[104:107], a[88:91], v[168:171]\n"
"ds_read_b64_tr_b16 v[104:105], v229 offset:8192\n"
"v_cvt_pk_bf16_f32 v175, v178, v179\n"
"v_mfma_f32_16x16x32_bf16 v[168:171], v[108:111], a[92:95], v[168:171]\n"
"v_mov_b64 v[20:21], s[22:23]\n"
"v_mov_b64 v[90:91], 0\n"
"v_mfma_f32_16x16x32_bf16 v[168:171], v[112:115], a[32:35], v[168:171]\n"
"v_mov_b64 v[88:89], 0\n"
"v_mov_b64 v[92:93], 0\n"
"v_mfma_f32_16x16x32_bf16 v[168:171], v[116:119], a[36:39], v[168:171]\n"
"v_mov_b64 v[48:49], s[22:23]\n"
"v_cvt_pk_bf16_f32 v132, v132, v133\n"
"v_mfma_f32_16x16x32_bf16 v[168:171], v[120:123], a[40:43], v[168:171]\n"
"s_add_u32 s47, s47, 1\n"
"v_mov_b64 v[0:1], s[22:23]\n"
"v_mov_b64 v[8:9], s[22:23]\n"
"v_mfma_f32_16x16x32_bf16 v[168:171], v[224:227], a[44:47], v[168:171]\n"
"s_waitcnt lgkmcnt(5)\n"
"v_lshlrev_b32 v237, 2, v237\n"
"v_cvt_pk_bf16_f32 v174, v176, v177\n"
"LGL_%=:\n"
"v_mfma_f32_16x16x32_bf16 v[124:127], v[180:183], a[96:99], v[196:199]\n"
"ds_read_b64_tr_b16 v[106:107], v229 offset:10240\n"
"v_cvt_pk_bf16_f32 v133, v134, v135\n"
"v_mfma_f32_16x16x32_bf16 v[124:127], v[184:187], a[100:103], v[124:127]\n"
"ds_read_b64_tr_b16 v[108:109], v233 offset:8192\n"
"v_subrev_f32 v136, s22, v136\n"
"v_mfma_f32_16x16x32_bf16 v[124:127], v[188:191], a[104:107], v[124:127]\n"
"ds_read_b64_tr_b16 v[110:111], v233 offset:10240\n"
"v_subrev_f32 v137, s22, v137\n"
"v_mfma_f32_16x16x32_bf16 v[124:127], v[192:195], a[108:111], v[124:127]\n"
"ds_read_b64_tr_b16 v[112:113], v229 offset:16384\n"
"v_subrev_f32 v138, s22, v138\n"
"v_mfma_f32_16x16x32_bf16 v[124:127], v[208:211], a[144:147], v[124:127]\n"
"ds_read_b64_tr_b16 v[114:115], v229 offset:18432\n"
"v_subrev_f32 v139, s22, v139\n"
"v_mfma_f32_16x16x32_bf16 v[124:127], v[212:215], a[148:151], v[124:127]\n"
"s_waitcnt lgkmcnt(7)\n"
"ds_read_b64_tr_b16 v[116:117], v233 offset:16384\n"
"v_cvt_pk_bf16_f32 v134, v136, v137\n"
"v_mfma_f32_16x16x32_bf16 v[124:127], v[216:219], a[152:155], v[124:127]\n"
"ds_read_b64_tr_b16 v[118:119], v233 offset:18432\n"
"v_cvt_pk_bf16_f32 v135, v138, v139\n"
"v_mfma_f32_16x16x32_bf16 v[124:127], v[220:223], a[156:159], v[124:127]\n"
"buffer_store_dwordx4 v[172:175], v243, s[32:35], s17 offen offset:2048 sc1\n"
"v_subrev_f32 v148, s22, v148\n"
"v_mfma_f32_16x16x32_bf16 v[140:143], v[220:223], a[172:175], v[196:199]\n"
"ds_read_b64_tr_b16 v[120:121], v229 offset:24576\n"
"v_subrev_f32 v149, s22, v149\n"
"v_mfma_f32_16x16x32_bf16 v[140:143], v[216:219], a[168:171], v[140:143]\n"
"ds_read_b64_tr_b16 v[122:123], v229 offset:26624\n"
"v_subrev_f32 v150, s22, v150\n"
"v_mfma_f32_16x16x32_bf16 v[140:143], v[212:215], a[164:167], v[140:143]\n"
"ds_read_b64_tr_b16 v[224:225], v233 offset:24576\n"
"v_subrev_f32 v151, s22, v151\n"
"v_mfma_f32_16x16x32_bf16 v[140:143], v[208:211], a[160:163], v[140:143]\n"
"ds_read_b64_tr_b16 v[226:227], v233 offset:26624\n"
"v_cvt_pk_bf16_f32 v148, v148, v149\n"
"v_mfma_f32_16x16x32_bf16 v[140:143], v[192:195], a[124:127], v[140:143]\n"
"v_cvt_pk_bf16_f32 v149, v150, v151\n"
"v_subrev_f32 v152, s22, v152\n"
"v_mfma_f32_16x16x32_bf16 v[140:143], v[188:191], a[120:123], v[140:143]\n"
"s_add_u32 s41, s70, 0\n"
"v_subrev_f32 v153, s22, v153\n"
"v_subrev_f32 v154, s22, v154\n"
"v_mfma_f32_16x16x32_bf16 v[140:143], v[184:187], a[116:119], v[140:143]\n"
"s_mov_b32 m0, s71\n"
"v_subrev_f32 v155, s22, v155\n"
"v_cvt_pk_bf16_f32 v150, v152, v153\n"
"v_mfma_f32_16x16x32_bf16 v[140:143], v[180:183], a[112:115], v[140:143]\n"
"s_waitcnt vmcnt(14)\n"
"buffer_load_dwordx4 v240, s[28:31], s41 offen lds\n"
"v_mfma_f32_16x16x32_bf16 v[156:159], v[180:183], a[128:131], v[196:199]\n"
"v_xor_b32 v230, s75, v230\n"
"v_cvt_pk_bf16_f32 v151, v154, v155\n"
"v_mfma_f32_16x16x32_bf16 v[156:159], v[184:187], a[132:135], v[156:159]\n"
"v_xor_b32 v234, s75, v234\n"
"v_subrev_f32 v164, s22, v164\n"
"v_mfma_f32_16x16x32_bf16 v[156:159], v[188:191], a[136:139], v[156:159]\n"
"v_subrev_f32 v165, s22, v165\n"
"v_subrev_f32 v166, s22, v166\n"
"v_mfma_f32_16x16x32_bf16 v[156:159], v[192:195], a[140:143], v[156:159]\n"
"v_subrev_f32 v167, s22, v167\n"
"v_cvt_pk_bf16_f32 v164, v164, v165\n"
"v_mfma_f32_16x16x32_bf16 v[156:159], v[208:211], a[176:179], v[156:159]\n"
"v_cvt_pk_bf16_f32 v165, v166, v167\n"
"v_subrev_f32 v168, s22, v168\n"
"v_mfma_f32_16x16x32_bf16 v[156:159], v[212:215], a[180:183], v[156:159]\n"
"v_subrev_f32 v169, s22, v169\n"
"v_subrev_f32 v170, s22, v170\n"
"v_mfma_f32_16x16x32_bf16 v[156:159], v[216:219], a[184:187], v[156:159]\n"
"v_subrev_f32 v171, s22, v171\n"
"v_cvt_pk_bf16_f32 v166, v168, v169\n"
"v_mfma_f32_16x16x32_bf16 v[156:159], v[220:223], a[188:191], v[156:159]\n"
"buffer_store_dwordx4 v[132:135], v243, s[32:35], s16 offen offset:1024 sc1\n"
"v_cvt_pk_bf16_f32 v167, v170, v171\n"
"v_mfma_f32_16x16x32_bf16 v[172:175], v[220:223], a[252:255], v[196:199]\n"
"ds_read_b64_tr_b16 v[220:221], v234 offset:24576\n"
"v_xor_b32 v231, s75, v231\n"
"v_mfma_f32_16x16x32_bf16 v[172:175], v[216:219], a[248:251], v[172:175]\n"
"ds_read_b64_tr_b16 v[216:217], v230 offset:24576\n"
"v_xor_b32 v235, s75, v235\n"
"v_mfma_f32_16x16x32_bf16 v[172:175], v[212:215], a[244:247], v[172:175]\n"
"ds_read_b64_tr_b16 v[212:213], v234 offset:16384\n"
"v_lshlrev_b32 v202, 16, v84\n"
"v_mfma_f32_16x16x32_bf16 v[172:175], v[208:211], a[240:243], v[172:175]\n"
"ds_read_b64_tr_b16 v[208:209], v230 offset:16384\n"
"v_and_b32 v84, s73, v84\n"
"v_mfma_f32_16x16x32_bf16 v[172:175], v[192:195], a[204:207], v[172:175]\n"
"ds_read_b64_tr_b16 v[192:193], v234 offset:8192\n"
"v_add_f32 v25, v25, v84\n"
"v_mfma_f32_16x16x32_bf16 v[172:175], v[188:191], a[200:203], v[172:175]\n"
"ds_read_b64_tr_b16 v[188:189], v230 offset:8192\n"
"v_lshlrev_b32 v203, 16, v85\n"
"v_mfma_f32_16x16x32_bf16 v[172:175], v[184:187], a[196:199], v[172:175]\n"
"s_add_u32 s40, s41, s52\n"
"ds_read_b64_tr_b16 v[184:185], v234 offset:0\n"
"v_and_b32 v85, s73, v85\n"
"v_mfma_f32_16x16x32_bf16 v[172:175], v[180:183], a[192:195], v[172:175]\n"
"s_waitcnt vmcnt(13) lgkmcnt(7)\n"
"buffer_load_dwordx4 v240, s[28:31], s40 offen offset:1024 lds\n"
"v_mfma_f32_16x16x32_bf16 v[128:131], v[96:99], a[96:99], v[196:199]\n"
"s_waitcnt lgkmcnt(6)\n"
"ds_read_b64_tr_b16 v[180:181], v230 offset:0\n"
"v_add_f32 v27, v27, v85\n"
"v_mfma_f32_16x16x32_bf16 v[128:131], v[100:103], a[100:103], v[128:131]\n"
"ds_read_b64_tr_b16 v[182:183], v230 offset:2048\n"
"v_lshlrev_b32 v204, 16, v86\n"
"v_mfma_f32_16x16x32_bf16 v[128:131], v[104:107], a[104:107], v[128:131]\n"
"ds_read_b64_tr_b16 v[186:187], v234 offset:2048\n"
"v_and_b32 v86, s73, v86\n"
"v_mfma_f32_16x16x32_bf16 v[128:131], v[108:111], a[108:111], v[128:131]\n"
"ds_read_b64_tr_b16 v[190:191], v230 offset:10240\n"
"v_add_f32 v29, v29, v86\n"
"v_mfma_f32_16x16x32_bf16 v[128:131], v[112:115], a[144:147], v[128:131]\n"
"s_waitcnt vmcnt(6)\n"
"ds_read_b64_tr_b16 v[194:195], v234 offset:10240\n"
"v_lshlrev_b32 v205, 16, v87\n"
"v_mfma_f32_16x16x32_bf16 v[128:131], v[116:119], a[148:151], v[128:131]\n"
"v_subrev_u32 v200, s21, v200\n"
"ds_read_b64_tr_b16 v[210:211], v230 offset:18432\n"
"v_mfma_f32_16x16x32_bf16 v[128:131], v[120:123], a[152:155], v[128:131]\n"
"v_cmp_lt_u32 vcc, s69, v200\n"
"ds_read_b64_tr_b16 v[214:215], v234 offset:18432\n"
"v_mfma_f32_16x16x32_bf16 v[128:131], v[224:227], a[156:159], v[128:131]\n"
"ds_read_b64_tr_b16 v[218:219], v230 offset:26624\n"
"s_cbranch_vccnz LGP_%=\nLGPK_%=:\n"
"v_mfma_f32_16x16x32_bf16 v[144:147], v[224:227], a[172:175], v[196:199]\n"
"s_sub_u32 s72, s72, 1\n"
"ds_read_b64_tr_b16 v[222:223], v234 offset:26624\n"
"v_and_b32 v87, s73, v87\n"
"v_mfma_f32_16x16x32_bf16 v[144:147], v[120:123], a[168:171], v[144:147]\n"
"s_and_b32 s72, s72, 7\n"
"v_add_f32 v24, v24, v202\n"
"v_add_f32 v31, v31, v87\n"
"v_mfma_f32_16x16x32_bf16 v[144:147], v[116:119], a[164:167], v[144:147]\n"
"v_add_f32 v26, v26, v203\n"
"v_lshlrev_b32 v206, 16, v88\n"
"v_mfma_f32_16x16x32_bf16 v[144:147], v[112:115], a[160:163], v[144:147]\n"
"buffer_load_dwordx4 v[72:75], v243, s[32:35], s18 offen sc1\n"
"v_mfma_f32_16x16x32_bf16 v[144:147], v[108:111], a[124:127], v[144:147]\n"
"v_and_b32 v88, s73, v88\n"
"v_add_f32 v33, v33, v88\n"
"v_mfma_f32_16x16x32_bf16 v[144:147], v[104:107], a[120:123], v[144:147]\n"
"v_lshlrev_b32 v207, 16, v89\n"
"v_and_b32 v89, s73, v89\n"
"v_mfma_f32_16x16x32_bf16 v[144:147], v[100:103], a[116:119], v[144:147]\n"
"v_add_f32 v35, v35, v89\n"
"v_lshlrev_b32 v202, 16, v90\n"
"v_mfma_f32_16x16x32_bf16 v[144:147], v[96:99], a[112:115], v[144:147]\n"
"buffer_store_dwordx4 v[148:151], v243, s[32:35], s16 offen offset:3072 sc1\n"
"v_and_b32 v90, s73, v90\n"
"v_mfma_f32_16x16x32_bf16 v[160:163], v[96:99], a[128:131], v[196:199]\n"
"v_add_f32 v37, v37, v90\n"
"v_lshlrev_b32 v203, 16, v91\n"
"v_mfma_f32_16x16x32_bf16 v[160:163], v[100:103], a[132:135], v[160:163]\n"
"v_and_b32 v91, s73, v91\n"
"v_add_f32 v39, v39, v91\n"
"v_mfma_f32_16x16x32_bf16 v[160:163], v[104:107], a[136:139], v[160:163]\n"
"v_add_f32 v28, v28, v204\n"
"v_add_f32 v30, v30, v205\n"
"v_mfma_f32_16x16x32_bf16 v[160:163], v[108:111], a[140:143], v[160:163]\n"
"v_add_f32 v32, v32, v206\n"
"v_add_f32 v34, v34, v207\n"
"v_mfma_f32_16x16x32_bf16 v[160:163], v[112:115], a[176:179], v[160:163]\n"
"v_lshlrev_b32 v204, 16, v92\n"
"v_and_b32 v92, s73, v92\n"
"v_mfma_f32_16x16x32_bf16 v[160:163], v[116:119], a[180:183], v[160:163]\n"
"v_add_f32 v41, v41, v92\n"
"v_lshlrev_b32 v205, 16, v93\n"
"v_mfma_f32_16x16x32_bf16 v[160:163], v[120:123], a[184:187], v[160:163]\n"
"s_add_u32 s40, s41, s53\n"
"v_and_b32 v93, s73, v93\n"
"v_add_f32 v43, v43, v93\n"
"v_mfma_f32_16x16x32_bf16 v[160:163], v[224:227], a[188:191], v[160:163]\n"
"buffer_load_dwordx4 v240, s[28:31], s40 offen offset:2048 lds\n"
"v_mfma_f32_16x16x32_bf16 v[176:179], v[224:227], a[252:255], v[196:199]\n"
"ds_read_b64_tr_b16 v[224:225], v235 offset:24576\n"
"v_lshlrev_b32 v206, 16, v94\n"
"v_mfma_f32_16x16x32_bf16 v[176:179], v[120:123], a[248:251], v[176:179]\n"
"ds_read_b64_tr_b16 v[120:121], v231 offset:24576\n"
"v_and_b32 v94, s73, v94\n"
"v_mfma_f32_16x16x32_bf16 v[176:179], v[116:119], a[244:247], v[176:179]\n"
"ds_read_b64_tr_b16 v[116:117], v235 offset:16384\n"
"v_add_f32 v45, v45, v94\n"
"v_mfma_f32_16x16x32_bf16 v[176:179], v[112:115], a[240:243], v[176:179]\n"
"buffer_load_dwordx4 v[76:79], v243, s[32:35], s18 offen offset:1024 sc1\n"
"v_mfma_f32_16x16x32_bf16 v[176:179], v[108:111], a[204:207], v[176:179]\n"
"ds_read_b64_tr_b16 v[108:109], v235 offset:8192\n"
"v_lshlrev_b32 v207, 16, v95\n"
"v_mfma_f32_16x16x32_bf16 v[176:179], v[104:107], a[200:203], v[176:179]\n"
"ds_read_b64_tr_b16 v[104:105], v231 offset:8192\n"
"v_and_b32 v95, s73, v95\n"
"v_mfma_f32_16x16x32_bf16 v[176:179], v[100:103], a[196:199], v[176:179]\n"
"s_add_u32 s40, s41, s54\n"
"ds_read_b64_tr_b16 v[100:101], v235 offset:0\n"
"v_add_f32 v47, v47, v95\n"
"v_mfma_f32_16x16x32_bf16 v[176:179], v[96:99], a[192:195], v[176:179]\n"
"s_waitcnt lgkmcnt(6)\n"
"buffer_load_dwordx4 v240, s[28:31], s40 offen offset:3072 lds\n"
"v_mfma_f32_16x16x32_bf16 v[132:135], v[180:183], a[96:99], v[196:199]\n"
"ds_read_b64_tr_b16 v[96:97], v231 offset:0\n"
"v_add_f32 v36, v36, v202\n"
"v_mfma_f32_16x16x32_bf16 v[132:135], v[184:187], a[100:103], v[132:135]\n"
"ds_read_b64_tr_b16 v[98:99], v231 offset:2048\n"
"v_add_f32 v38, v38, v203\n"
"v_mfma_f32_16x16x32_bf16 v[132:135], v[188:191], a[104:107], v[132:135]\n"
"ds_read_b64_tr_b16 v[102:103], v235 offset:2048\n"
"v_add_f32 v44, v44, v206\n"
"v_mfma_f32_16x16x32_bf16 v[132:135], v[192:195], a[108:111], v[132:135]\n"
"ds_read_b64_tr_b16 v[106:107], v231 offset:10240\n"
"v_add_f32 v46, v46, v207\n"
"v_mfma_f32_16x16x32_bf16 v[132:135], v[208:211], a[144:147], v[132:135]\n"
"ds_read_b64_tr_b16 v[110:111], v235 offset:10240\n"
"v_add_f32 v40, v40, v204\n"
"v_mfma_f32_16x16x32_bf16 v[132:135], v[212:215], a[148:151], v[132:135]\n"
"ds_read_b64_tr_b16 v[112:113], v231 offset:16384\n"
"v_add_f32 v42, v42, v205\n"
"v_mfma_f32_16x16x32_bf16 v[132:135], v[216:219], a[152:155], v[132:135]\n"
"ds_read_b64_tr_b16 v[114:115], v231 offset:18432\n"
"v_mfma_f32_16x16x32_bf16 v[132:135], v[220:223], a[156:159], v[132:135]\n"
"buffer_store_dwordx4 v[164:167], v243, s[32:35], s17 offen offset:1024 sc1\n"
"v_lshlrev_b32 v206, 16, v244\n"
"v_mfma_f32_16x16x32_bf16 v[148:151], v[220:223], a[172:175], v[196:199]\n"
"ds_read_b64_tr_b16 v[118:119], v235 offset:18432\n"
"v_and_b32 v244, s73, v244\n"
"v_mfma_f32_16x16x32_bf16 v[148:151], v[216:219], a[168:171], v[148:151]\n"
"ds_read_b64_tr_b16 v[122:123], v231 offset:26624\n"
"v_add_f32 v49, v49, v244\n"
"v_mfma_f32_16x16x32_bf16 v[148:151], v[212:215], a[164:167], v[148:151]\n"
"ds_read_b64_tr_b16 v[226:227], v235 offset:26624\n"
"v_lshlrev_b32 v207, 16, v245\n"
"v_mfma_f32_16x16x32_bf16 v[148:151], v[208:211], a[160:163], v[148:151]\n"
"buffer_load_dwordx4 v[80:83], v243, s[32:35], s18 offen offset:2048 sc1\n"
"v_mfma_f32_16x16x32_bf16 v[148:151], v[192:195], a[124:127], v[148:151]\n"
"v_and_b32 v245, s73, v245\n"
"v_add_f32 v51, v51, v245\n"
"v_mfma_f32_16x16x32_bf16 v[148:151], v[188:191], a[120:123], v[148:151]\n"
"s_add_u32 s41, s70, 163840\n"
"v_lshlrev_b32 v202, 16, v246\n"
"v_and_b32 v246, s73, v246\n"
"v_mfma_f32_16x16x32_bf16 v[148:151], v[184:187], a[116:119], v[148:151]\n"
"s_add_u32 m0, s71, 16384\n"
"v_add_f32 v53, v53, v246\n"
"v_lshlrev_b32 v203, 16, v247\n"
"v_mfma_f32_16x16x32_bf16 v[148:151], v[180:183], a[112:115], v[148:151]\n"
"buffer_load_dwordx4 v240, s[28:31], s41 offen lds\n"
"v_mfma_f32_16x16x32_bf16 v[164:167], v[180:183], a[128:131], v[196:199]\n"
"v_and_b32 v247, s73, v247\n"
"v_add_f32 v55, v55, v247\n"
"v_mfma_f32_16x16x32_bf16 v[164:167], v[184:187], a[132:135], v[164:167]\n"
"v_add_f32 v48, v48, v206\n"
"v_add_f32 v50, v50, v207\n"
"v_mfma_f32_16x16x32_bf16 v[164:167], v[188:191], a[136:139], v[164:167]\n"
"v_add_f32 v52, v52, v202\n"
"v_add_f32 v54, v54, v203\n"
"v_mfma_f32_16x16x32_bf16 v[164:167], v[192:195], a[140:143], v[164:167]\n"
"v_mfma_f32_16x16x32_bf16 v[164:167], v[208:211], a[176:179], v[164:167]\n"
"v_mfma_f32_16x16x32_bf16 v[164:167], v[212:215], a[180:183], v[164:167]\n"
"v_mfma_f32_16x16x32_bf16 v[164:167], v[216:219], a[184:187], v[164:167]\n"
"s_waitcnt vmcnt(12)\n"
"v_mfma_f32_16x16x32_bf16 v[164:167], v[220:223], a[188:191], v[164:167]\n"
"s_barrier\n"
"s_waitcnt lgkmcnt(0)\n"
"v_mfma_f32_16x16x32_bf16 v[136:139], v[96:99], a[96:99], v[196:199]\n"
"ds_read_b64_tr_b16 v[180:181], v228 offset:32768\n"
"v_mfma_f32_16x16x32_bf16 v[136:139], v[100:103], a[100:103], v[136:139]\n"
"ds_read_b64_tr_b16 v[182:183], v228 offset:34816\n"
"v_mfma_f32_16x16x32_bf16 v[136:139], v[104:107], a[104:107], v[136:139]\n"
"ds_read_b64_tr_b16 v[184:185], v232 offset:32768\n"
"v_mfma_f32_16x16x32_bf16 v[136:139], v[108:111], a[108:111], v[136:139]\n"
"ds_read_b64_tr_b16 v[186:187], v232 offset:34816\n"
"v_mfma_f32_16x16x32_bf16 v[136:139], v[112:115], a[144:147], v[136:139]\n"
"ds_read_b64_tr_b16 v[188:189], v228 offset:40960\n"
"v_mfma_f32_16x16x32_bf16 v[136:139], v[116:119], a[148:151], v[136:139]\n"
"ds_read_b64_tr_b16 v[190:191], v228 offset:43008\n"
"v_mfma_f32_16x16x32_bf16 v[136:139], v[120:123], a[152:155], v[136:139]\n"
"s_add_u32 s40, s41, s52\n"
"ds_read_b64_tr_b16 v[192:193], v232 offset:40960\n"
"v_mfma_f32_16x16x32_bf16 v[136:139], v[224:227], a[156:159], v[136:139]\n"
"buffer_load_dwordx4 v240, s[28:31], s40 offen offset:1024 lds\n"
"v_mfma_f32_16x16x32_bf16 v[152:155], v[224:227], a[172:175], v[196:199]\n"
"ds_read_b64_tr_b16 v[194:195], v232 offset:43008\n"
"v_mfma_f32_16x16x32_bf16 v[152:155], v[120:123], a[168:171], v[152:155]\n"
"ds_read_b64_tr_b16 v[208:209], v228 offset:49152\n"
"v_mfma_f32_16x16x32_bf16 v[152:155], v[116:119], a[164:167], v[152:155]\n"
"ds_read_b64_tr_b16 v[210:211], v228 offset:51200\n"
"v_mfma_f32_16x16x32_bf16 v[152:155], v[112:115], a[160:163], v[152:155]\n"
"ds_read_b64_tr_b16 v[212:213], v232 offset:49152\n"
"v_mfma_f32_16x16x32_bf16 v[152:155], v[108:111], a[124:127], v[152:155]\n"
"ds_read_b64_tr_b16 v[214:215], v232 offset:51200\n"
"v_mfma_f32_16x16x32_bf16 v[152:155], v[104:107], a[120:123], v[152:155]\n"
"ds_read_b64_tr_b16 v[216:217], v228 offset:57344\n"
"v_mfma_f32_16x16x32_bf16 v[152:155], v[100:103], a[116:119], v[152:155]\n"
"s_add_u32 s40, s41, s53\n"
"ds_read_b64_tr_b16 v[218:219], v228 offset:59392\n"
"v_xor_b32 v228, s75, v228\n"
"v_mfma_f32_16x16x32_bf16 v[152:155], v[96:99], a[112:115], v[152:155]\n"
"buffer_load_dwordx4 v240, s[28:31], s40 offen offset:2048 lds\n"
"v_mfma_f32_16x16x32_bf16 v[168:171], v[96:99], a[128:131], v[196:199]\n"
"ds_read_b64_tr_b16 v[220:221], v232 offset:57344\n"
"v_mfma_f32_16x16x32_bf16 v[168:171], v[100:103], a[132:135], v[168:171]\n"
"ds_read_b64_tr_b16 v[222:223], v232 offset:59392\n"
"v_xor_b32 v232, s75, v232\n"
"v_mfma_f32_16x16x32_bf16 v[168:171], v[104:107], a[136:139], v[168:171]\n"
"ds_read_b64_tr_b16 v[96:97], v229 offset:32768\n"
"v_mfma_f32_16x16x32_bf16 v[168:171], v[108:111], a[140:143], v[168:171]\n"
"ds_read_b64_tr_b16 v[98:99], v229 offset:34816\n"
"v_mfma_f32_16x16x32_bf16 v[168:171], v[112:115], a[176:179], v[168:171]\n"
"ds_read_b64_tr_b16 v[100:101], v233 offset:32768\n"
"v_mfma_f32_16x16x32_bf16 v[168:171], v[116:119], a[180:183], v[168:171]\n"
"ds_read_b64_tr_b16 v[102:103], v233 offset:34816\n"
"v_mfma_f32_16x16x32_bf16 v[168:171], v[120:123], a[184:187], v[168:171]\n"
"s_add_u32 s40, s41, s54\n"
"ds_read_b64_tr_b16 v[104:105], v229 offset:40960\n"
"v_mfma_f32_16x16x32_bf16 v[168:171], v[224:227], a[188:191], v[168:171]\n"
"s_waitcnt lgkmcnt(7)\n"
"buffer_load_dwordx4 v240, s[28:31], s40 offen offset:3072 lds\n"
"v_mfma_f32_16x16x32_bf16 v[124:127], v[180:183], a[48:51], v[124:127]\n"
"ds_read_b64_tr_b16 v[106:107], v229 offset:43008\n"
"v_mfma_f32_16x16x32_bf16 v[124:127], v[184:187], a[52:55], v[124:127]\n"
"ds_read_b64_tr_b16 v[108:109], v233 offset:40960\n"
"v_mfma_f32_16x16x32_bf16 v[124:127], v[188:191], a[56:59], v[124:127]\n"
"ds_read_b64_tr_b16 v[110:111], v233 offset:43008\n"
"v_mfma_f32_16x16x32_bf16 v[124:127], v[192:195], a[60:63], v[124:127]\n"
"ds_read_b64_tr_b16 v[112:113], v229 offset:49152\n"
"v_mfma_f32_16x16x32_bf16 v[124:127], v[208:211], a[0:3], v[124:127]\n"
"ds_read_b64_tr_b16 v[114:115], v229 offset:51200\n"
"v_mfma_f32_16x16x32_bf16 v[124:127], v[212:215], a[4:7], v[124:127]\n"
"s_add_u32 s41, s70, 327680\n"
"ds_read_b64_tr_b16 v[116:117], v233 offset:49152\n"
"v_mfma_f32_16x16x32_bf16 v[124:127], v[216:219], a[8:11], v[124:127]\n"
"s_waitcnt lgkmcnt(6)\n"
"s_add_u32 m0, s71, 32768\n"
"ds_read_b64_tr_b16 v[118:119], v233 offset:51200\n"
"v_mfma_f32_16x16x32_bf16 v[124:127], v[220:223], a[12:15], v[124:127]\n"
"buffer_load_dwordx4 v240, s[28:31], s41 offen lds\n"
"v_mfma_f32_16x16x32_bf16 v[140:143], v[220:223], a[28:31], v[140:143]\n"
"ds_read_b64_tr_b16 v[120:121], v229 offset:57344\n"
"v_mfma_f32_16x16x32_bf16 v[140:143], v[216:219], a[24:27], v[140:143]\n"
"ds_read_b64_tr_b16 v[122:123], v229 offset:59392\n"
"v_xor_b32 v229, s75, v229\n"
"v_mfma_f32_16x16x32_bf16 v[140:143], v[212:215], a[20:23], v[140:143]\n"
"ds_read_b64_tr_b16 v[224:225], v233 offset:57344\n"
"v_mfma_f32_16x16x32_bf16 v[140:143], v[208:211], a[16:19], v[140:143]\n"
"ds_read_b64_tr_b16 v[226:227], v233 offset:59392\n"
"v_xor_b32 v233, s75, v233\n"
"v_mfma_f32_16x16x32_bf16 v[140:143], v[192:195], a[76:79], v[140:143]\n"
"v_mfma_f32_16x16x32_bf16 v[140:143], v[188:191], a[72:75], v[140:143]\n"
"v_subrev_f32 v124, s22, v124\n"
"v_subrev_f32 v125, s22, v125\n"
"v_mfma_f32_16x16x32_bf16 v[140:143], v[184:187], a[68:71], v[140:143]\n"
"s_add_u32 s40, s41, s52\n"
"v_subrev_f32 v126, s22, v126\n"
"v_subrev_f32 v127, s22, v127\n"
"v_mfma_f32_16x16x32_bf16 v[140:143], v[180:183], a[64:67], v[140:143]\n"
"buffer_load_dwordx4 v240, s[28:31], s40 offen offset:1024 lds\n"
"v_mfma_f32_16x16x32_bf16 v[156:159], v[180:183], a[80:83], v[156:159]\n"
"v_cvt_pk_bf16_f32 v124, v124, v125\n"
"v_cvt_pk_bf16_f32 v125, v126, v127\n"
"v_mfma_f32_16x16x32_bf16 v[156:159], v[184:187], a[84:87], v[156:159]\n"
"v_mfma_f32_16x16x32_bf16 v[156:159], v[188:191], a[88:91], v[156:159]\n"
"v_mfma_f32_16x16x32_bf16 v[156:159], v[192:195], a[92:95], v[156:159]\n"
"v_mfma_f32_16x16x32_bf16 v[156:159], v[208:211], a[32:35], v[156:159]\n"
"v_mfma_f32_16x16x32_bf16 v[156:159], v[212:215], a[36:39], v[156:159]\n"
"v_subrev_f32 v140, s22, v140\n"
"v_subrev_f32 v141, s22, v141\n"
"v_mfma_f32_16x16x32_bf16 v[156:159], v[216:219], a[40:43], v[156:159]\n"
"s_waitcnt vmcnt(7)\n"
"v_subrev_f32 v142, s22, v142\n"
"v_subrev_f32 v143, s22, v143\n"
"v_mfma_f32_16x16x32_bf16 v[156:159], v[220:223], a[44:47], v[156:159]\n"
"s_barrier\n"
"buffer_store_dword v241, v248, s[84:87], 0 offen sc1\n"
"v_mfma_f32_16x16x32_bf16 v[172:175], v[220:223], a[228:231], v[172:175]\n"
"ds_read_b64_tr_b16 v[220:221], v234 offset:57344\n"
"v_cvt_pk_bf16_f32 v140, v140, v141\n"
"v_mfma_f32_16x16x32_bf16 v[172:175], v[216:219], a[224:227], v[172:175]\n"
"ds_read_b64_tr_b16 v[216:217], v230 offset:57344\n"
"v_cvt_pk_bf16_f32 v141, v142, v143\n"
"v_mfma_f32_16x16x32_bf16 v[172:175], v[212:215], a[212:215], v[172:175]\n"
"ds_read_b64_tr_b16 v[212:213], v234 offset:49152\n"
"v_add_u32 v241, 1, v241\n"
"v_mfma_f32_16x16x32_bf16 v[172:175], v[208:211], a[208:211], v[172:175]\n"
"buffer_load_dwordx4 v[84:87], v243, s[32:35], s18 offen offset:3072 sc1\n"
"v_mfma_f32_16x16x32_bf16 v[172:175], v[192:195], a[236:239], v[172:175]\n"
"ds_read_b64_tr_b16 v[192:193], v234 offset:40960\n"
"v_mfma_f32_16x16x32_bf16 v[172:175], v[188:191], a[232:235], v[172:175]\n"
"ds_read_b64_tr_b16 v[188:189], v230 offset:40960\n"
"v_subrev_f32 v156, s22, v156\n"
"v_mfma_f32_16x16x32_bf16 v[172:175], v[184:187], a[220:223], v[172:175]\n"
"s_add_u32 s40, s41, s53\n"
"ds_read_b64_tr_b16 v[184:185], v234 offset:32768\n"
"v_subrev_f32 v157, s22, v157\n"
"v_mfma_f32_16x16x32_bf16 v[172:175], v[180:183], a[216:219], v[172:175]\n"
"buffer_load_dwordx4 v240, s[28:31], s40 offen offset:2048 lds\n"
"v_mfma_f32_16x16x32_bf16 v[128:131], v[96:99], a[48:51], v[128:131]\n"
"s_add_u32 s16, s90, s62\n"
"ds_read_b64_tr_b16 v[180:181], v230 offset:32768\n"
"v_subrev_f32 v158, s22, v158\n"
"v_mfma_f32_16x16x32_bf16 v[128:131], v[100:103], a[52:55], v[128:131]\n"
"s_waitcnt lgkmcnt(5)\n"
"s_add_u32 s17, s16, 4096\n"
"ds_read_b64_tr_b16 v[182:183], v230 offset:34816\n"
"v_subrev_f32 v159, s22, v159\n"
"v_mfma_f32_16x16x32_bf16 v[128:131], v[104:107], a[56:59], v[128:131]\n"
"s_lshl_b32 s62, s70, 9\n"
"ds_read_b64_tr_b16 v[186:187], v234 offset:34816\n"
"v_cvt_pk_bf16_f32 v156, v156, v157\n"
"v_mfma_f32_16x16x32_bf16 v[128:131], v[108:111], a[60:63], v[128:131]\n"
"ds_read_b64_tr_b16 v[190:191], v230 offset:43008\n"
"v_cvt_pk_bf16_f32 v157, v158, v159\n"
"v_mfma_f32_16x16x32_bf16 v[128:131], v[112:115], a[0:3], v[128:131]\n"
"ds_read_b64_tr_b16 v[194:195], v234 offset:43008\n"
"v_lshlrev_b32 v202, 16, v72\n"
"v_mfma_f32_16x16x32_bf16 v[128:131], v[116:119], a[4:7], v[128:131]\n"
"ds_read_b64_tr_b16 v[208:209], v230 offset:49152\n"
"v_and_b32 v72, s73, v72\n"
"v_mfma_f32_16x16x32_bf16 v[128:131], v[120:123], a[8:11], v[128:131]\n"
"s_add_u32 s40, s41, s54\n"
"ds_read_b64_tr_b16 v[210:211], v230 offset:51200\n"
"v_add_f32 v0, v0, v202\n"
"v_mfma_f32_16x16x32_bf16 v[128:131], v[224:227], a[12:15], v[128:131]\n"
"buffer_load_dwordx4 v240, s[28:31], s40 offen offset:3072 lds\n"
"v_mfma_f32_16x16x32_bf16 v[144:147], v[224:227], a[28:31], v[144:147]\n"
"ds_read_b64_tr_b16 v[214:215], v234 offset:51200\n"
"v_add_f32 v1, v1, v72\n"
"v_mfma_f32_16x16x32_bf16 v[144:147], v[120:123], a[24:27], v[144:147]\n"
"ds_read_b64_tr_b16 v[218:219], v230 offset:59392\n"
"v_lshlrev_b32 v203, 16, v73\n"
"v_mfma_f32_16x16x32_bf16 v[144:147], v[116:119], a[20:23], v[144:147]\n"
"ds_read_b64_tr_b16 v[222:223], v234 offset:59392\n"
"v_and_b32 v73, s73, v73\n"
"v_mfma_f32_16x16x32_bf16 v[144:147], v[112:115], a[16:19], v[144:147]\n"
"buffer_load_dwordx4 v[88:91], v243, s[32:35], s19 offen sc1\n"
"v_mfma_f32_16x16x32_bf16 v[144:147], v[108:111], a[76:79], v[144:147]\n"
"v_add_f32 v2, v2, v203\n"
"v_add_f32 v3, v3, v73\n"
"v_mfma_f32_16x16x32_bf16 v[144:147], v[104:107], a[72:75], v[144:147]\n"
"s_add_u32 s41, s70, 491520\n"
"v_subrev_f32 v128, s22, v128\n"
"v_subrev_f32 v129, s22, v129\n"
"v_mfma_f32_16x16x32_bf16 v[144:147], v[100:103], a[68:71], v[144:147]\n"
"s_add_u32 m0, s71, 49152\n"
"v_subrev_f32 v130, s22, v130\n"
"v_subrev_f32 v131, s22, v131\n"
"v_mfma_f32_16x16x32_bf16 v[144:147], v[96:99], a[64:67], v[144:147]\n"
"buffer_load_dwordx4 v240, s[28:31], s41 offen lds\n"
"v_mfma_f32_16x16x32_bf16 v[160:163], v[96:99], a[80:83], v[160:163]\n"
"s_and_b32 s70, s88, 15\n"
"v_cvt_pk_bf16_f32 v126, v128, v129\n"
"v_cvt_pk_bf16_f32 v127, v130, v131\n"
"v_mfma_f32_16x16x32_bf16 v[160:163], v[100:103], a[84:87], v[160:163]\n"
"s_lshr_b32 s88, s88, 4\n"
"v_lshlrev_b32 v204, 16, v74\n"
"v_and_b32 v74, s73, v74\n"
"v_mfma_f32_16x16x32_bf16 v[160:163], v[104:107], a[88:91], v[160:163]\n"
"s_lshl_b32 s70, s70, 7\n"
"v_add_f32 v4, v4, v204\n"
"v_add_f32 v5, v5, v74\n"
"v_mfma_f32_16x16x32_bf16 v[160:163], v[108:111], a[92:95], v[160:163]\n"
"s_xor_b32 s71, s71, s75\n"
"v_lshlrev_b32 v205, 16, v75\n"
"v_and_b32 v75, s73, v75\n"
"v_mfma_f32_16x16x32_bf16 v[160:163], v[112:115], a[32:35], v[160:163]\n"
"v_add_f32 v6, v6, v205\n"
"v_add_f32 v7, v7, v75\n"
"v_mfma_f32_16x16x32_bf16 v[160:163], v[116:119], a[36:39], v[160:163]\n"
"v_subrev_f32 v144, s22, v144\n"
"v_subrev_f32 v145, s22, v145\n"
"v_mfma_f32_16x16x32_bf16 v[160:163], v[120:123], a[40:43], v[160:163]\n"
"v_subrev_f32 v146, s22, v146\n"
"v_subrev_f32 v147, s22, v147\n"
"v_mfma_f32_16x16x32_bf16 v[160:163], v[224:227], a[44:47], v[160:163]\n"
"buffer_store_dwordx4 v[124:127], v243, s[32:35], s16 offen sc1\n"
"v_cvt_pk_bf16_f32 v142, v144, v145\n"
"v_mfma_f32_16x16x32_bf16 v[176:179], v[224:227], a[228:231], v[176:179]\n"
"ds_read_b64_tr_b16 v[224:225], v235 offset:57344\n"
"v_cvt_pk_bf16_f32 v143, v146, v147\n"
"v_mfma_f32_16x16x32_bf16 v[176:179], v[120:123], a[224:227], v[176:179]\n"
"ds_read_b64_tr_b16 v[120:121], v231 offset:57344\n"
"v_lshlrev_b32 v206, 16, v76\n"
"v_mfma_f32_16x16x32_bf16 v[176:179], v[116:119], a[212:215], v[176:179]\n"
"ds_read_b64_tr_b16 v[116:117], v235 offset:49152\n"
"v_and_b32 v76, s73, v76\n"
"v_mfma_f32_16x16x32_bf16 v[176:179], v[112:115], a[208:211], v[176:179]\n"
"buffer_load_dwordx4 v[92:95], v243, s[32:35], s19 offen offset:1024 sc1\n"
"v_mfma_f32_16x16x32_bf16 v[176:179], v[108:111], a[236:239], v[176:179]\n"
"ds_read_b64_tr_b16 v[108:109], v235 offset:40960\n"
"v_add_f32 v8, v8, v206\n"
"v_mfma_f32_16x16x32_bf16 v[176:179], v[104:107], a[232:235], v[176:179]\n"
"ds_read_b64_tr_b16 v[104:105], v231 offset:40960\n"
"v_subrev_f32 v160, s22, v160\n"
"v_mfma_f32_16x16x32_bf16 v[176:179], v[100:103], a[220:223], v[176:179]\n"
"s_add_u32 s40, s41, s52\n"
"ds_read_b64_tr_b16 v[100:101], v235 offset:32768\n"
"v_subrev_f32 v161, s22, v161\n"
"v_mfma_f32_16x16x32_bf16 v[176:179], v[96:99], a[216:219], v[176:179]\n"
"s_waitcnt vmcnt(14) lgkmcnt(6)\n"
"buffer_load_dwordx4 v240, s[28:31], s40 offen offset:1024 lds\n"
"v_mfma_f32_16x16x32_bf16 v[132:135], v[180:183], a[48:51], v[132:135]\n"
"ds_read_b64_tr_b16 v[96:97], v231 offset:32768\n"
"v_subrev_f32 v162, s22, v162\n"
"v_mfma_f32_16x16x32_bf16 v[132:135], v[184:187], a[52:55], v[132:135]\n"
"ds_read_b64_tr_b16 v[98:99], v231 offset:34816\n"
"v_subrev_f32 v163, s22, v163\n"
"v_mfma_f32_16x16x32_bf16 v[132:135], v[188:191], a[56:59], v[132:135]\n"
"ds_read_b64_tr_b16 v[102:103], v235 offset:34816\n"
"v_cvt_pk_bf16_f32 v158, v160, v161\n"
"v_mfma_f32_16x16x32_bf16 v[132:135], v[192:195], a[60:63], v[132:135]\n"
"ds_read_b64_tr_b16 v[106:107], v231 offset:43008\n"
"v_cvt_pk_bf16_f32 v159, v162, v163\n"
"v_mfma_f32_16x16x32_bf16 v[132:135], v[208:211], a[0:3], v[132:135]\n"
"ds_read_b64_tr_b16 v[110:111], v235 offset:43008\n"
"v_add_f32 v9, v9, v76\n"
"v_mfma_f32_16x16x32_bf16 v[132:135], v[212:215], a[4:7], v[132:135]\n"
"ds_read_b64_tr_b16 v[112:113], v231 offset:49152\n"
"v_lshlrev_b32 v207, 16, v77\n"
"v_mfma_f32_16x16x32_bf16 v[132:135], v[216:219], a[8:11], v[132:135]\n"
"ds_read_b64_tr_b16 v[114:115], v231 offset:51200\n"
"v_and_b32 v77, s73, v77\n"
"v_mfma_f32_16x16x32_bf16 v[132:135], v[220:223], a[12:15], v[132:135]\n"
"buffer_store_dwordx4 v[140:143], v243, s[32:35], s16 offen offset:2048 sc1\n"
"v_add_f32 v10, v10, v207\n"
"v_mfma_f32_16x16x32_bf16 v[148:151], v[220:223], a[28:31], v[148:151]\n"
"ds_read_b64_tr_b16 v[118:119], v235 offset:51200\n"
"v_add_f32 v11, v11, v77\n"
"v_mfma_f32_16x16x32_bf16 v[148:151], v[216:219], a[24:27], v[148:151]\n"
"ds_read_b64_tr_b16 v[122:123], v231 offset:59392\n"
"v_lshlrev_b32 v202, 16, v78\n"
"v_mfma_f32_16x16x32_bf16 v[148:151], v[212:215], a[20:23], v[148:151]\n"
"ds_read_b64_tr_b16 v[226:227], v235 offset:59392\n"
"v_and_b32 v78, s73, v78\n"
"v_mfma_f32_16x16x32_bf16 v[148:151], v[208:211], a[16:19], v[148:151]\n"
"v_add_f32 v12, v12, v202\n"
"v_add_f32 v13, v13, v78\n"
"v_mfma_f32_16x16x32_bf16 v[148:151], v[192:195], a[76:79], v[148:151]\n"
"v_lshlrev_b32 v203, 16, v79\n"
"v_and_b32 v79, s73, v79\n"
"v_mfma_f32_16x16x32_bf16 v[148:151], v[188:191], a[72:75], v[148:151]\n"
"v_add_f32 v14, v14, v203\n"
"v_add_f32 v15, v15, v79\n"
"v_mfma_f32_16x16x32_bf16 v[148:151], v[184:187], a[68:71], v[148:151]\n"
"s_add_u32 s40, s41, s53\n"
"v_lshlrev_b32 v204, 16, v80\n"
"v_and_b32 v80, s73, v80\n"
"v_mfma_f32_16x16x32_bf16 v[148:151], v[180:183], a[64:67], v[148:151]\n"
"buffer_load_dwordx4 v240, s[28:31], s40 offen offset:2048 lds\n"
"v_mfma_f32_16x16x32_bf16 v[164:167], v[180:183], a[80:83], v[164:167]\n"
"v_add_f32 v16, v16, v204\n"
"v_add_f32 v17, v17, v80\n"
"v_mfma_f32_16x16x32_bf16 v[164:167], v[184:187], a[84:87], v[164:167]\n"
"v_lshlrev_b32 v205, 16, v81\n"
"v_and_b32 v81, s73, v81\n"
"v_mfma_f32_16x16x32_bf16 v[164:167], v[188:191], a[88:91], v[164:167]\n"
"v_add_f32 v18, v18, v205\n"
"v_add_f32 v19, v19, v81\n"
"v_mfma_f32_16x16x32_bf16 v[164:167], v[192:195], a[92:95], v[164:167]\n"
"buffer_load_dwordx4 v[244:247], v243, s[32:35], s19 offen offset:2048 sc1\n"
"v_mfma_f32_16x16x32_bf16 v[164:167], v[208:211], a[32:35], v[164:167]\n"
"s_lshl_b32 s18, s72, 4\n"
"v_lshlrev_b32 v206, 16, v82\n"
"v_and_b32 v82, s73, v82\n"
"v_mfma_f32_16x16x32_bf16 v[164:167], v[212:215], a[36:39], v[164:167]\n"
"s_lshl_b32 s19, s72, 2\n"
"v_add_f32 v20, v20, v206\n"
"v_add_f32 v21, v21, v82\n"
"v_mfma_f32_16x16x32_bf16 v[164:167], v[216:219], a[40:43], v[164:167]\n"
"s_waitcnt vmcnt(14)\n"
"s_lshr_b32 s19, s89, s19\n"
"v_lshlrev_b32 v207, 16, v83\n"
"v_mfma_f32_16x16x32_bf16 v[164:167], v[220:223], a[44:47], v[164:167]\n"
"s_barrier\n"
"s_and_b32 s19, s19, 1\n"
"s_waitcnt lgkmcnt(0)\n"
"v_mfma_f32_16x16x32_bf16 v[136:139], v[96:99], a[48:51], v[136:139]\n"
"s_lshl3_add_u32 s18, s18, s19\n"
"ds_read_b64_tr_b16 v[180:181], v228 offset:0\n"
"v_and_b32 v83, s73, v83\n"
"v_mfma_f32_16x16x32_bf16 v[136:139], v[100:103], a[52:55], v[136:139]\n"
"s_lshl_b32 s18, s18, 13\n"
"ds_read_b64_tr_b16 v[182:183], v228 offset:2048\n"
"v_add_f32 v22, v22, v207\n"
"v_mfma_f32_16x16x32_bf16 v[136:139], v[104:107], a[56:59], v[136:139]\n"
"s_add_u32 s18, s18, s91\n"
"v_lshlrev_b32_e64 v201, 2, s72\n"
"ds_read_b64_tr_b16 v[184:185], v232 offset:0\n"
"v_mfma_f32_16x16x32_bf16 v[136:139], v[108:111], a[60:63], v[136:139]\n"
"s_add_u32 s19, s18, 4096\n"
"global_load_dword v200, v201, s[6:7] sc1\n"
"v_mfma_f32_16x16x32_bf16 v[136:139], v[112:115], a[0:3], v[136:139]\n"
"s_mov_b32 s69, 15\n"
"ds_read_b64_tr_b16 v[186:187], v232 offset:2048\n"
"v_add_f32 v23, v23, v83\n"
"v_mfma_f32_16x16x32_bf16 v[136:139], v[116:119], a[4:7], v[136:139]\n"
"s_add_u32 s21, s21, 1\n"
"ds_read_b64_tr_b16 v[188:189], v228 offset:8192\n"
"v_subrev_f32 v172, s22, v172\n"
"v_mfma_f32_16x16x32_bf16 v[136:139], v[120:123], a[8:11], v[136:139]\n"
"ds_read_b64_tr_b16 v[190:191], v228 offset:10240\n"
"v_subrev_f32 v173, s22, v173\n"
"v_mfma_f32_16x16x32_bf16 v[136:139], v[224:227], a[12:15], v[136:139]\n"
"buffer_store_dwordx4 v[156:159], v243, s[32:35], s17 offen sc1\n"
"v_subrev_f32 v174, s22, v174\n"
"v_mfma_f32_16x16x32_bf16 v[152:155], v[224:227], a[28:31], v[152:155]\n"
"ds_read_b64_tr_b16 v[192:193], v232 offset:8192\n"
"v_subrev_f32 v175, s22, v175\n"
"v_mfma_f32_16x16x32_bf16 v[152:155], v[120:123], a[24:27], v[152:155]\n"
"ds_read_b64_tr_b16 v[194:195], v232 offset:10240\n"
"v_cvt_pk_bf16_f32 v172, v172, v173\n"
"v_mfma_f32_16x16x32_bf16 v[152:155], v[116:119], a[20:23], v[152:155]\n"
"ds_read_b64_tr_b16 v[208:209], v228 offset:16384\n"
"v_cvt_pk_bf16_f32 v173, v174, v175\n"
"v_mfma_f32_16x16x32_bf16 v[152:155], v[112:115], a[16:19], v[152:155]\n"
"ds_read_b64_tr_b16 v[210:211], v228 offset:18432\n"
"v_subrev_f32 v176, s22, v176\n"
"v_mfma_f32_16x16x32_bf16 v[152:155], v[108:111], a[76:79], v[152:155]\n"
"ds_read_b64_tr_b16 v[212:213], v232 offset:16384\n"
"v_subrev_f32 v177, s22, v177\n"
"v_mfma_f32_16x16x32_bf16 v[152:155], v[104:107], a[72:75], v[152:155]\n"
"ds_read_b64_tr_b16 v[214:215], v232 offset:18432\n"
"v_subrev_f32 v178, s22, v178\n"
"v_mfma_f32_16x16x32_bf16 v[152:155], v[100:103], a[68:71], v[152:155]\n"
"s_add_u32 s40, s41, s54\n"
"ds_read_b64_tr_b16 v[216:217], v228 offset:24576\n"
"v_subrev_f32 v179, s22, v179\n"
"v_mfma_f32_16x16x32_bf16 v[152:155], v[96:99], a[64:67], v[152:155]\n"
"buffer_load_dwordx4 v240, s[28:31], s40 offen offset:3072 lds\n"
"v_mfma_f32_16x16x32_bf16 v[168:171], v[96:99], a[80:83], v[168:171]\n"
"ds_read_b64_tr_b16 v[218:219], v228 offset:26624\n"
"v_cvt_pk_bf16_f32 v174, v176, v177\n"
"v_mfma_f32_16x16x32_bf16 v[168:171], v[100:103], a[84:87], v[168:171]\n"
"ds_read_b64_tr_b16 v[220:221], v232 offset:24576\n"
"v_cvt_pk_bf16_f32 v175, v178, v179\n"
"v_mfma_f32_16x16x32_bf16 v[168:171], v[104:107], a[88:91], v[168:171]\n"
"ds_read_b64_tr_b16 v[222:223], v232 offset:26624\n"
"v_subrev_f32 v132, s22, v132\n"
"v_mfma_f32_16x16x32_bf16 v[168:171], v[108:111], a[92:95], v[168:171]\n"
"ds_read_b64_tr_b16 v[96:97], v229 offset:0\n"
"v_subrev_f32 v133, s22, v133\n"
"v_mfma_f32_16x16x32_bf16 v[168:171], v[112:115], a[32:35], v[168:171]\n"
"ds_read_b64_tr_b16 v[98:99], v229 offset:2048\n"
"v_subrev_f32 v134, s22, v134\n"
"v_mfma_f32_16x16x32_bf16 v[168:171], v[116:119], a[36:39], v[168:171]\n"
"ds_read_b64_tr_b16 v[100:101], v233 offset:0\n"
"v_subrev_f32 v135, s22, v135\n"
"v_mfma_f32_16x16x32_bf16 v[168:171], v[120:123], a[40:43], v[168:171]\n"
"s_add_u32 s47, s47, 1\n"
"ds_read_b64_tr_b16 v[102:103], v233 offset:2048\n"
"v_cvt_pk_bf16_f32 v132, v132, v133\n"
"v_mfma_f32_16x16x32_bf16 v[168:171], v[224:227], a[44:47], v[168:171]\n"
"s_waitcnt lgkmcnt(7)\n"
"s_cmp_lg_u32 s47, 9\n"
"ds_read_b64_tr_b16 v[104:105], v229 offset:8192\n"
"s_cbranch_scc1 LGL_%=\n"
"v_mfma_f32_16x16x32_bf16 v[0:3], v[180:183], a[96:99], v[0:3]\n"
"v_xor_b32 v235, s75, v235\n"
"v_subrev_f32 v136, s22, v136\n"
"v_mfma_f32_16x16x32_bf16 v[0:3], v[184:187], a[100:103], v[0:3]\n"
"ds_read_b64_tr_b16 v[114:115], v229 offset:18432\n"
"v_cvt_pk_bf16_f32 v133, v134, v135\n"
"v_mfma_f32_16x16x32_bf16 v[0:3], v[188:191], a[104:107], v[0:3]\n"
"v_mov_b32 v201, v237\n"
"v_subrev_f32 v138, s22, v138\n"
"v_subrev_f32 v139, s22, v139\n"
"v_mfma_f32_16x16x32_bf16 v[0:3], v[192:195], a[108:111], v[0:3]\n"
"global_load_dword v200, v201, s[6:7] sc1\n"
"v_mfma_f32_16x16x32_bf16 v[0:3], v[208:211], a[144:147], v[0:3]\n"
"v_subrev_f32 v167, s22, v167\n"
"v_cvt_pk_bf16_f32 v135, v138, v139\n"
"v_mfma_f32_16x16x32_bf16 v[0:3], v[212:215], a[148:151], v[0:3]\n"
"s_waitcnt lgkmcnt(3)\n"
"v_subrev_f32 v148, s22, v148\n"
"v_subrev_f32 v137, s22, v137\n"
"v_mfma_f32_16x16x32_bf16 v[0:3], v[216:219], a[152:155], v[0:3]\n"
"v_subrev_f32 v165, s22, v165\n"
"v_subrev_f32 v168, s22, v168\n"
"v_mfma_f32_16x16x32_bf16 v[0:3], v[220:223], a[156:159], v[0:3]\n"
"buffer_store_dwordx4 v[172:175], v243, s[32:35], s17 offen offset:2048 sc1\n"
"v_subrev_f32 v151, s22, v151\n"
"v_cvt_pk_bf16_f32 v134, v136, v137\n"
"v_mfma_f32_16x16x32_bf16 v[16:19], v[220:223], a[172:175], v[16:19]\n"
"buffer_store_dwordx4 v[132:135], v243, s[32:35], s16 offen offset:1024 sc1\n"
"v_subrev_f32 v149, s22, v149\n"
"v_subrev_f32 v150, s22, v150\n"
"v_subrev_f32 v164, s22, v164\n"
"v_cvt_pk_bf16_f32 v164, v164, v165\n"
"v_subrev_f32 v153, s22, v153\n"
"v_subrev_f32 v155, s22, v155\n"
"v_mfma_f32_16x16x32_bf16 v[16:19], v[216:219], a[168:171], v[16:19]\n"
"v_cvt_pk_bf16_f32 v148, v148, v149\n"
"v_subrev_f32 v154, s22, v154\n"
"v_mfma_f32_16x16x32_bf16 v[16:19], v[212:215], a[164:167], v[16:19]\n"
"v_cvt_pk_bf16_f32 v149, v150, v151\n"
"v_subrev_f32 v170, s22, v170\n"
"v_mfma_f32_16x16x32_bf16 v[16:19], v[208:211], a[160:163], v[16:19]\n"
"v_subrev_f32 v166, s22, v166\n"
"v_subrev_f32 v171, s22, v171\n"
"v_mfma_f32_16x16x32_bf16 v[16:19], v[192:195], a[124:127], v[16:19]\n"
"v_cvt_pk_bf16_f32 v165, v166, v167\n"
"v_cvt_pk_bf16_f32 v167, v170, v171\n"
"v_mfma_f32_16x16x32_bf16 v[16:19], v[188:191], a[120:123], v[16:19]\n"
"v_subrev_f32 v152, s22, v152\n"
"v_cvt_pk_bf16_f32 v151, v154, v155\n"
"v_mfma_f32_16x16x32_bf16 v[16:19], v[184:187], a[116:119], v[16:19]\n"
"v_cvt_pk_bf16_f32 v150, v152, v153\n"
"v_subrev_f32 v169, s22, v169\n"
"v_mfma_f32_16x16x32_bf16 v[16:19], v[180:183], a[112:115], v[16:19]\n"
"s_waitcnt vmcnt(16)\n"
"buffer_store_dwordx4 v[148:151], v243, s[32:35], s16 offen offset:3072 sc1\n"
"v_cvt_pk_bf16_f32 v166, v168, v169\n"
"v_mfma_f32_16x16x32_bf16 v[32:35], v[180:183], a[128:131], v[32:35]\n"
"buffer_store_dwordx4 v[164:167], v243, s[32:35], s17 offen offset:1024 sc1\n"
"ds_read_b64_tr_b16 v[120:121], v229 offset:24576\n"
"v_mfma_f32_16x16x32_bf16 v[32:35], v[184:187], a[132:135], v[32:35]\n"
"ds_read_b64_tr_b16 v[224:225], v233 offset:24576\n"
"v_xor_b32 v234, s75, v234\n"
"v_mfma_f32_16x16x32_bf16 v[32:35], v[188:191], a[136:139], v[32:35]\n"
"ds_read_b64_tr_b16 v[122:123], v229 offset:26624\n"
"v_lshlrev_b32 v203, 16, v85\n"
"v_mfma_f32_16x16x32_bf16 v[32:35], v[192:195], a[140:143], v[32:35]\n"
"ds_read_b64_tr_b16 v[108:109], v233 offset:8192\n"
"v_lshlrev_b32 v202, 16, v84\n"
"v_mfma_f32_16x16x32_bf16 v[32:35], v[208:211], a[176:179], v[32:35]\n"
"ds_read_b64_tr_b16 v[226:227], v233 offset:26624\n"
"v_and_b32 v84, s73, v84\n"
"v_mfma_f32_16x16x32_bf16 v[32:35], v[212:215], a[180:183], v[32:35]\n"
"ds_read_b64_tr_b16 v[110:111], v233 offset:10240\n"
"v_bfe_u32 v161, v242, 8, 2\n"
"v_mfma_f32_16x16x32_bf16 v[32:35], v[216:219], a[184:187], v[32:35]\n"
"ds_read_b64_tr_b16 v[106:107], v229 offset:10240\n"
"v_lshlrev_b32 v205, 16, v87\n"
"v_mfma_f32_16x16x32_bf16 v[32:35], v[220:223], a[188:191], v[32:35]\n"
"v_xor_b32 v230, s75, v230\n"
"v_lshlrev_b32 v204, 16, v86\n"
"v_mfma_f32_16x16x32_bf16 v[48:51], v[220:223], a[252:255], v[48:51]\n"
"ds_read_b64_tr_b16 v[220:221], v234 offset:24576\n"
"v_add_f32 v26, v26, v203\n"
"v_mfma_f32_16x16x32_bf16 v[48:51], v[216:219], a[248:251], v[48:51]\n"
"ds_read_b64_tr_b16 v[112:113], v229 offset:16384\n"
"v_bfe_u32 v160, v242, 6, 2\n"
"v_mfma_f32_16x16x32_bf16 v[48:51], v[212:215], a[244:247], v[48:51]\n"
"ds_read_b64_tr_b16 v[116:117], v233 offset:16384\n"
"v_add_f32 v24, v24, v202\n"
"v_mfma_f32_16x16x32_bf16 v[48:51], v[208:211], a[240:243], v[48:51]\n"
"ds_read_b64_tr_b16 v[118:119], v233 offset:18432\n"
"v_add_f32 v25, v25, v84\n"
"v_mfma_f32_16x16x32_bf16 v[48:51], v[192:195], a[204:207], v[48:51]\n"
"ds_read_b64_tr_b16 v[210:211], v230 offset:18432\n"
"v_add_f32 v28, v28, v204\n"
"v_mfma_f32_16x16x32_bf16 v[48:51], v[188:191], a[200:203], v[48:51]\n"
"ds_read_b64_tr_b16 v[188:189], v230 offset:8192\n"
"v_and_b32 v86, s73, v86\n"
"v_mfma_f32_16x16x32_bf16 v[48:51], v[184:187], a[196:199], v[48:51]\n"
"ds_read_b64_tr_b16 v[194:195], v234 offset:10240\n"
"v_mov_b32 v164, 0\n"
"v_mfma_f32_16x16x32_bf16 v[48:51], v[180:183], a[192:195], v[48:51]\n"
"s_waitcnt vmcnt(15)\n"
"v_xor_b32 v160, v160, v161\n"
"v_lshlrev_b32 v202, 16, v90\n"
"v_mfma_f32_16x16x32_bf16 v[4:7], v[96:99], a[96:99], v[4:7]\n"
"s_waitcnt lgkmcnt(6)\n"
"ds_read_b64_tr_b16 v[222:223], v234 offset:26624\n"
"v_lshlrev_b32 v203, 16, v91\n"
"v_mfma_f32_16x16x32_bf16 v[4:7], v[100:103], a[100:103], v[4:7]\n"
"ds_read_b64_tr_b16 v[184:185], v234 offset:0\n"
"v_lshlrev_b32 v206, 16, v88\n"
"v_mfma_f32_16x16x32_bf16 v[4:7], v[104:107], a[104:107], v[4:7]\n"
"ds_read_b64_tr_b16 v[216:217], v230 offset:24576\n"
"v_mfma_f32_16x16x32_bf16 v[4:7], v[108:111], a[108:111], v[4:7]\n"
"s_waitcnt lgkmcnt(6)\n"
"ds_read_b64_tr_b16 v[218:219], v230 offset:26624\n"
"v_and_b32 v91, s73, v91\n"
"v_mfma_f32_16x16x32_bf16 v[4:7], v[112:115], a[144:147], v[4:7]\n"
"s_waitcnt vmcnt(4)\n"
"ds_read_b64_tr_b16 v[190:191], v230 offset:10240\n"
"v_lshlrev_b32 v207, 16, v89\n"
"v_mfma_f32_16x16x32_bf16 v[4:7], v[116:119], a[148:151], v[4:7]\n"
"v_subrev_u32 v200, s21, v200\n"
"ds_read_b64_tr_b16 v[208:209], v230 offset:16384\n"
"v_mfma_f32_16x16x32_bf16 v[4:7], v[120:123], a[152:155], v[4:7]\n"
"v_cmp_lt_u32 vcc, s69, v200\n"
"ds_read_b64_tr_b16 v[182:183], v230 offset:2048\n"
"v_mfma_f32_16x16x32_bf16 v[4:7], v[224:227], a[156:159], v[4:7]\n"
"ds_read_b64_tr_b16 v[180:181], v230 offset:0\n"
"s_cbranch_vccnz LGQ_%=\nLGQK_%=:\n"
"v_mfma_f32_16x16x32_bf16 v[20:23], v[224:227], a[172:175], v[20:23]\n"
"s_bfe_u32 s20, s89, 65536\ns_lshl_b32 s20, s20, 13\ns_add_u32 s20, s20, 0\n"
"ds_read_b64_tr_b16 v[214:215], v234 offset:18432\n"
"v_and_b32 v85, s73, v85\n"
"v_mfma_f32_16x16x32_bf16 v[20:23], v[120:123], a[168:171], v[20:23]\n"
"buffer_load_dwordx4 v[124:127], v236, s[32:35], s20 offen sc1\n"
"v_mfma_f32_16x16x32_bf16 v[20:23], v[116:119], a[164:167], v[20:23]\n"
"v_and_b32 v90, s73, v90\n"
"v_add_f32 v37, v37, v90\n"
"v_mfma_f32_16x16x32_bf16 v[20:23], v[112:115], a[160:163], v[20:23]\n"
"s_bfe_u32 s20, s89, 65540\ns_lshl_b32 s20, s20, 13\ns_add_u32 s20, s20, 1048576\n"
"v_add_f32 v36, v36, v202\n"
"v_add_f32 v38, v38, v203\n"
"v_mfma_f32_16x16x32_bf16 v[20:23], v[108:111], a[124:127], v[20:23]\n"
"buffer_load_dwordx4 v[128:131], v236, s[32:35], s20 offen sc1\n"
"v_mfma_f32_16x16x32_bf16 v[20:23], v[104:107], a[120:123], v[20:23]\n"
"v_add_u32 v160, -1, v160\n"
"v_add_f32 v39, v39, v91\n"
"v_mfma_f32_16x16x32_bf16 v[20:23], v[100:103], a[116:119], v[20:23]\n"
"s_bfe_u32 s20, s89, 65544\ns_lshl_b32 s20, s20, 13\ns_add_u32 s20, s20, 2097152\n"
"v_add_f32 v32, v32, v206\n"
"v_mfma_f32_16x16x32_bf16 v[20:23], v[96:99], a[112:115], v[20:23]\n"
"buffer_load_dwordx4 v[132:135], v236, s[32:35], s20 offen sc1\n"
"v_mfma_f32_16x16x32_bf16 v[36:39], v[96:99], a[128:131], v[36:39]\n"
"v_lshlrev_b32 v204, 16, v92\n"
"v_add_f32 v40, v40, v204\n"
"v_mfma_f32_16x16x32_bf16 v[36:39], v[100:103], a[132:135], v[36:39]\n"
"s_bfe_u32 s20, s89, 65548\ns_lshl_b32 s20, s20, 13\ns_add_u32 s20, s20, 3145728\n"
"ds_read_b64_tr_b16 v[212:213], v234 offset:16384\n"
"v_add_f32 v30, v30, v205\n"
"v_mfma_f32_16x16x32_bf16 v[36:39], v[104:107], a[136:139], v[36:39]\n"
"buffer_load_dwordx4 v[136:139], v236, s[32:35], s20 offen sc1\n"
"v_mfma_f32_16x16x32_bf16 v[36:39], v[108:111], a[140:143], v[36:39]\n"
"v_and_b32 v92, s73, v92\n"
"v_lshlrev_b32 v206, 16, v94\n"
"v_mfma_f32_16x16x32_bf16 v[36:39], v[112:115], a[176:179], v[36:39]\n"
"s_bfe_u32 s20, s89, 65552\ns_lshl_b32 s20, s20, 13\ns_add_u32 s20, s20, 4194304\n"
"ds_read_b64_tr_b16 v[186:187], v234 offset:2048\n"
"ds_read_b64_tr_b16 v[192:193], v234 offset:8192\n"
"v_mfma_f32_16x16x32_bf16 v[36:39], v[116:119], a[180:183], v[36:39]\n"
"buffer_load_dwordx4 v[140:143], v236, s[32:35], s20 offen sc1\n"
"v_mfma_f32_16x16x32_bf16 v[36:39], v[120:123], a[184:187], v[36:39]\n"
"v_and_b32 v94, s73, v94\n"
"v_mfma_f32_16x16x32_bf16 v[36:39], v[224:227], a[188:191], v[36:39]\n"
"s_bfe_u32 s20, s89, 65556\ns_lshl_b32 s20, s20, 13\ns_add_u32 s20, s20, 5242880\n"
"v_add_f32 v34, v34, v207\n"
"v_lshlrev_b32 v205, 16, v93\n"
"v_mfma_f32_16x16x32_bf16 v[52:55], v[224:227], a[252:255], v[52:55]\n"
"buffer_load_dwordx4 v[144:147], v236, s[32:35], s20 offen sc1\n"
"v_mfma_f32_16x16x32_bf16 v[52:55], v[120:123], a[248:251], v[52:55]\n"
"v_ashrrev_i32 v160, 31, v160\n"
"v_add_f32 v44, v44, v206\n"
"v_mfma_f32_16x16x32_bf16 v[52:55], v[116:119], a[244:247], v[52:55]\n"
"s_bfe_u32 s20, s89, 65560\ns_lshl_b32 s20, s20, 13\ns_add_u32 s20, s20, 6291456\n"
"v_xor_b32 v231, s75, v231\n"
"v_lshlrev_b32 v207, 16, v95\n"
"v_mfma_f32_16x16x32_bf16 v[52:55], v[112:115], a[240:243], v[52:55]\n"
"buffer_load_dwordx4 v[148:151], v236, s[32:35], s20 offen sc1\n"
"v_mfma_f32_16x16x32_bf16 v[52:55], v[108:111], a[204:207], v[52:55]\n"
"s_bfe_u32 s20, s89, 65564\ns_lshl_b32 s20, s20, 13\ns_add_u32 s20, s20, 7340032\n"
"v_and_b32 v87, s73, v87\n"
"v_and_b32 v95, s73, v95\n"
"v_mfma_f32_16x16x32_bf16 v[52:55], v[104:107], a[200:203], v[52:55]\n"
"buffer_load_dwordx4 v[152:155], v236, s[32:35], s20 offen sc1\n"
"v_mfma_f32_16x16x32_bf16 v[52:55], v[100:103], a[196:199], v[52:55]\n"
"s_waitcnt vmcnt(8)\n"
"s_add_u32 s21, s21, 1\n"
"v_and_b32 v88, s73, v88\n"
"v_mfma_f32_16x16x32_bf16 v[52:55], v[96:99], a[192:195], v[52:55]\n"
"s_barrier\n"
"s_mov_b32 s52, 40960\n"
"buffer_store_dword v241, v248, s[84:87], 0 offen sc1\n"
"s_waitcnt lgkmcnt(0)\n"
"v_mfma_f32_16x16x32_bf16 v[8:11], v[180:183], a[96:99], v[8:11]\n"
"s_mov_b32 s53, 81920\n"
"ds_read_b64_tr_b16 v[98:99], v231 offset:2048\n"
"v_add_f32 v42, v42, v205\n"
"v_mfma_f32_16x16x32_bf16 v[8:11], v[184:187], a[100:103], v[8:11]\n"
"ds_read_b64_tr_b16 v[112:113], v231 offset:16384\n"
"v_add_f32 v31, v31, v87\n"
"v_mfma_f32_16x16x32_bf16 v[8:11], v[188:191], a[104:107], v[8:11]\n"
"v_add_f32 v29, v29, v86\n"
"v_and_b32 v161, 16, v242\n"
"v_mfma_f32_16x16x32_bf16 v[8:11], v[192:195], a[108:111], v[8:11]\n"
"ds_read_b64_tr_b16 v[118:119], v235 offset:18432\n"
"v_and_b32 v160, 0x3f80, v160\n"
"v_mfma_f32_16x16x32_bf16 v[8:11], v[208:211], a[144:147], v[8:11]\n"
"ds_read_b64_tr_b16 v[102:103], v235 offset:2048\n"
"v_add_f32 v27, v27, v85\n"
"v_mfma_f32_16x16x32_bf16 v[8:11], v[212:215], a[148:151], v[8:11]\n"
"ds_read_b64_tr_b16 v[106:107], v231 offset:10240\n"
"v_lshlrev_b32 v160, v161, v160\n"
"v_mfma_f32_16x16x32_bf16 v[8:11], v[216:219], a[152:155], v[8:11]\n"
"ds_read_b64_tr_b16 v[96:97], v231 offset:0\n"
"v_and_b32 v93, s73, v93\n"
"v_mfma_f32_16x16x32_bf16 v[8:11], v[220:223], a[156:159], v[8:11]\n"
"ds_read_b64_tr_b16 v[108:109], v235 offset:8192\n"
"v_add_f32 v41, v41, v92\n"
"v_mfma_f32_16x16x32_bf16 v[24:27], v[220:223], a[172:175], v[24:27]\n"
"v_bfe_i32 v161, v242, 5, 1\n"
"v_lshlrev_b32 v206, 16, v244\n"
"v_mfma_f32_16x16x32_bf16 v[24:27], v[216:219], a[168:171], v[24:27]\n"
"ds_read_b64_tr_b16 v[114:115], v231 offset:18432\n"
"v_mfma_f32_16x16x32_bf16 v[24:27], v[212:215], a[164:167], v[24:27]\n"
"ds_read_b64_tr_b16 v[104:105], v231 offset:8192\n"
"v_lshlrev_b32 v203, 16, v247\n"
"v_mfma_f32_16x16x32_bf16 v[24:27], v[208:211], a[160:163], v[24:27]\n"
"ds_read_b64_tr_b16 v[116:117], v235 offset:16384\n"
"v_add_f32 v48, v48, v206\n"
"v_mfma_f32_16x16x32_bf16 v[24:27], v[192:195], a[124:127], v[24:27]\n"
"ds_read_b64_tr_b16 v[110:111], v235 offset:10240\n"
"v_and_b32 v89, s73, v89\n"
"v_mfma_f32_16x16x32_bf16 v[24:27], v[188:191], a[120:123], v[24:27]\n"
"v_add_f32 v45, v45, v94\n"
"v_add_f32 v47, v47, v95\n"
"v_mfma_f32_16x16x32_bf16 v[24:27], v[184:187], a[116:119], v[24:27]\n"
"ds_read_b64_tr_b16 v[100:101], v235 offset:0\n"
"v_add_f32 v43, v43, v93\n"
"v_mfma_f32_16x16x32_bf16 v[24:27], v[180:183], a[112:115], v[24:27]\n"
"ds_read_b64_tr_b16 v[122:123], v231 offset:26624\n"
"v_add_f32 v46, v46, v207\n"
"v_mfma_f32_16x16x32_bf16 v[40:43], v[180:183], a[128:131], v[40:43]\n"
"v_add_f32 v33, v33, v88\n"
"v_and_b32 v244, s73, v244\n"
"v_mfma_f32_16x16x32_bf16 v[40:43], v[184:187], a[132:135], v[40:43]\n"
"ds_read_b64_tr_b16 v[120:121], v231 offset:24576\n"
"v_lshlrev_b32 v207, 16, v245\n"
"v_mfma_f32_16x16x32_bf16 v[40:43], v[188:191], a[136:139], v[40:43]\n"
"v_and_b32 v163, v160, v161\n"
"v_lshlrev_b32 v202, 16, v246\n"
"v_mfma_f32_16x16x32_bf16 v[40:43], v[192:195], a[140:143], v[40:43]\n"
"ds_read_b64_tr_b16 v[224:225], v235 offset:24576\n"
"v_and_b32 v246, s73, v246\n"
"v_mfma_f32_16x16x32_bf16 v[40:43], v[208:211], a[176:179], v[40:43]\n"
"v_add_f32 v52, v52, v202\n"
"v_and_b32 v247, s73, v247\n"
"v_mfma_f32_16x16x32_bf16 v[40:43], v[212:215], a[180:183], v[40:43]\n"
"ds_read_b64_tr_b16 v[226:227], v235 offset:26624\n"
"v_and_b32 v245, s73, v245\n"
"v_mfma_f32_16x16x32_bf16 v[40:43], v[216:219], a[184:187], v[40:43]\n"
"v_mov_b32 v161, 0\n"
"v_add_f32 v54, v54, v203\n"
"v_mfma_f32_16x16x32_bf16 v[40:43], v[220:223], a[188:191], v[40:43]\n"
"s_barrier\n"
"s_waitcnt lgkmcnt(3)\n"
"v_mfma_f32_16x16x32_bf16 v[12:15], v[96:99], a[96:99], v[12:15]\n"
"ds_read_b64_tr_b16 v[184:185], v232 offset:32768\n"
"v_add_f32 v49, v49, v244\n"
"v_mfma_f32_16x16x32_bf16 v[12:15], v[100:103], a[100:103], v[12:15]\n"
"ds_read_b64_tr_b16 v[216:217], v228 offset:57344\n"
"v_mfma_f32_16x16x32_bf16 v[12:15], v[104:107], a[104:107], v[12:15]\n"
"ds_read_b64_tr_b16 v[188:189], v228 offset:40960\n"
"v_add_f32 v35, v35, v89\n"
"v_mfma_f32_16x16x32_bf16 v[12:15], v[108:111], a[108:111], v[12:15]\n"
"ds_read_b64_tr_b16 v[190:191], v228 offset:43008\n"
"v_mfma_f32_16x16x32_bf16 v[12:15], v[112:115], a[144:147], v[12:15]\n"
"ds_read_b64_tr_b16 v[192:193], v232 offset:40960\n"
"v_add_f32 v53, v53, v246\n"
"v_mfma_f32_16x16x32_bf16 v[12:15], v[116:119], a[148:151], v[12:15]\n"
"s_waitcnt lgkmcnt(5)\n"
"ds_read_b64_tr_b16 v[212:213], v232 offset:49152\n"
"v_add_f32 v51, v51, v245\n"
"v_mfma_f32_16x16x32_bf16 v[12:15], v[120:123], a[152:155], v[12:15]\n"
"ds_read_b64_tr_b16 v[182:183], v228 offset:34816\n"
"v_xor_b32 v162, v160, v163\n"
"v_mfma_f32_16x16x32_bf16 v[12:15], v[224:227], a[156:159], v[12:15]\n"
"global_load_dword v200, v201, s[6:7] sc1\n"
"v_mfma_f32_16x16x32_bf16 v[28:31], v[224:227], a[172:175], v[28:31]\n"
"v_add_f32 v50, v50, v207\n"
"v_add_f32 v55, v55, v247\n"
"v_mfma_f32_16x16x32_bf16 v[28:31], v[120:123], a[168:171], v[28:31]\n"
"s_waitcnt vmcnt(9)\n"
"v_lshlrev_b32 v58, 16, v125\n"
"v_lshlrev_b32 v62, 16, v127\n"
"v_mfma_f32_16x16x32_bf16 v[28:31], v[116:119], a[164:167], v[28:31]\n"
"ds_read_b64_tr_b16 v[186:187], v232 offset:34816\n"
"ds_read_b64_tr_b16 v[214:215], v232 offset:51200\n"
"v_mfma_f32_16x16x32_bf16 v[28:31], v[112:115], a[160:163], v[28:31]\n"
"ds_read_b64_tr_b16 v[194:195], v232 offset:43008\n"
"v_and_b32 v61, s73, v126\n"
"v_mfma_f32_16x16x32_bf16 v[28:31], v[108:111], a[124:127], v[28:31]\n"
"s_waitcnt vmcnt(8)\n"
"ds_read_b64_tr_b16 v[210:211], v228 offset:51200\n"
"v_and_b32 v57, s73, v124\n"
"v_mfma_f32_16x16x32_bf16 v[28:31], v[104:107], a[120:123], v[28:31]\n"
"ds_read_b64_tr_b16 v[222:223], v232 offset:59392\n"
"v_lshlrev_b32 v56, 16, v124\n"
"v_mfma_f32_16x16x32_bf16 v[28:31], v[100:103], a[116:119], v[28:31]\n"
"ds_read_b64_tr_b16 v[180:181], v228 offset:32768\n"
"v_lshlrev_b32 v205, 16, v129\n"
"v_mfma_f32_16x16x32_bf16 v[28:31], v[96:99], a[112:115], v[28:31]\n"
"s_waitcnt vmcnt(7)\n"
"v_and_b32 v59, s73, v125\n"
"v_lshlrev_b32 v206, 16, v130\n"
"v_mfma_f32_16x16x32_bf16 v[44:47], v[96:99], a[128:131], v[44:47]\n"
"ds_read_b64_tr_b16 v[220:221], v232 offset:57344\n"
"v_lshlrev_b32 v204, 16, v128\n"
"v_mfma_f32_16x16x32_bf16 v[44:47], v[100:103], a[132:135], v[44:47]\n"
"ds_read_b64_tr_b16 v[208:209], v228 offset:49152\n"
"v_and_b32 v130, s73, v130\n"
"v_mfma_f32_16x16x32_bf16 v[44:47], v[104:107], a[136:139], v[44:47]\n"
"s_waitcnt vmcnt(6)\n"
"ds_read_b64_tr_b16 v[102:103], v233 offset:34816\n"
"v_add_f32 v58, v58, v205\n"
"v_mfma_f32_16x16x32_bf16 v[44:47], v[108:111], a[140:143], v[44:47]\n"
"v_lshlrev_b32 v207, 16, v131\n"
"v_add_f32 v62, v62, v207\n"
"v_mfma_f32_16x16x32_bf16 v[44:47], v[112:115], a[176:179], v[44:47]\n"
"ds_read_b64_tr_b16 v[218:219], v228 offset:59392\n"
"v_and_b32 v131, s73, v131\n"
"v_mfma_f32_16x16x32_bf16 v[44:47], v[116:119], a[180:183], v[44:47]\n"
"s_waitcnt vmcnt(5)\n"
"ds_read_b64_tr_b16 v[96:97], v229 offset:32768\n"
"v_lshlrev_b32 v205, 16, v135\n"
"v_mfma_f32_16x16x32_bf16 v[44:47], v[120:123], a[184:187], v[44:47]\n"
"v_lshlrev_b32 v202, 16, v132\n"
"v_lshlrev_b32 v207, 16, v137\n"
"v_mfma_f32_16x16x32_bf16 v[44:47], v[224:227], a[188:191], v[44:47]\n"
"s_waitcnt lgkmcnt(5)\n"
"ds_read_b64_tr_b16 v[120:121], v229 offset:57344\n"
"v_add_f32 v56, v56, v204\n"
"v_mfma_f32_16x16x32_bf16 v[0:3], v[180:183], a[48:51], v[0:3]\n"
"s_waitcnt vmcnt(4)\n"
"ds_read_b64_tr_b16 v[116:117], v233 offset:49152\n"
"v_add_f32 v61, v61, v130\n"
"v_mfma_f32_16x16x32_bf16 v[0:3], v[184:187], a[52:55], v[0:3]\n"
"v_lshlrev_b32 v60, 16, v126\n"
"v_lshlrev_b32 v204, 16, v134\n"
"v_mfma_f32_16x16x32_bf16 v[0:3], v[188:191], a[56:59], v[0:3]\n"
"ds_read_b64_tr_b16 v[110:111], v233 offset:43008\n"
"v_add_f32 v56, v56, v202\n"
"v_mfma_f32_16x16x32_bf16 v[0:3], v[192:195], a[60:63], v[0:3]\n"
"s_waitcnt lgkmcnt(5)\n"
"s_waitcnt vmcnt(3)\n"
"ds_read_b64_tr_b16 v[112:113], v229 offset:49152\n"
"v_and_b32 v63, s73, v127\n"
"v_mfma_f32_16x16x32_bf16 v[0:3], v[208:211], a[0:3], v[0:3]\n"
"ds_read_b64_tr_b16 v[224:225], v233 offset:57344\n"
"v_and_b32 v134, s73, v134\n"
"v_mfma_f32_16x16x32_bf16 v[0:3], v[212:215], a[4:7], v[0:3]\n"
"s_waitcnt lgkmcnt(5)\n"
"s_waitcnt vmcnt(2)\n"
"ds_read_b64_tr_b16 v[108:109], v233 offset:40960\n"
"v_and_b32 v129, s73, v129\n"
"v_mfma_f32_16x16x32_bf16 v[0:3], v[216:219], a[8:11], v[0:3]\n"
"v_add_f32 v60, v60, v206\n"
"v_add_f32 v60, v60, v204\n"
"v_mfma_f32_16x16x32_bf16 v[0:3], v[220:223], a[12:15], v[0:3]\n"
"ds_read_b64_tr_b16 v[98:99], v229 offset:34816\n"
"ds_read_b64_tr_b16 v[122:123], v229 offset:59392\n"
"v_mfma_f32_16x16x32_bf16 v[16:19], v[220:223], a[28:31], v[16:19]\n"
"v_mov_b32 v165, 0\n"
"v_add_f32 v59, v59, v129\n"
"v_mfma_f32_16x16x32_bf16 v[16:19], v[216:219], a[24:27], v[16:19]\n"
"v_add_f32 v62, v62, v205\n"
"v_lshlrev_b32 v206, 16, v136\n"
"v_mfma_f32_16x16x32_bf16 v[16:19], v[212:215], a[20:23], v[16:19]\n"
"ds_read_b64_tr_b16 v[104:105], v229 offset:40960\n"
"ds_read_b64_tr_b16 v[226:227], v233 offset:59392\n"
"v_mfma_f32_16x16x32_bf16 v[16:19], v[208:211], a[16:19], v[16:19]\n"
"v_add_f32 v56, v56, v206\n"
"v_lshlrev_b32 v202, 16, v138\n"
"v_mfma_f32_16x16x32_bf16 v[16:19], v[192:195], a[76:79], v[16:19]\n"
"s_waitcnt vmcnt(0)\n"
"ds_read_b64_tr_b16 v[118:119], v233 offset:51200\n"
"v_subrev_f32 v2, s22, v2\n"
"v_mfma_f32_16x16x32_bf16 v[16:19], v[188:191], a[72:75], v[16:19]\n"
"v_subrev_u32 v200, s21, v200\n"
"ds_read_b64_tr_b16 v[114:115], v229 offset:51200\n"
"v_mfma_f32_16x16x32_bf16 v[16:19], v[184:187], a[68:71], v[16:19]\n"
"v_cmp_lt_u32 vcc, s69, v200\n"
"ds_read_b64_tr_b16 v[100:101], v233 offset:32768\n"
"v_mfma_f32_16x16x32_bf16 v[16:19], v[180:183], a[64:67], v[16:19]\n"
"v_lshlrev_b32 v206, 16, v142\n"
"s_cbranch_vccnz LGR_%=\nLGRK_%=:\n"
"v_mfma_f32_16x16x32_bf16 v[32:35], v[180:183], a[80:83], v[32:35]\n"
"v_subrev_f32 v3, s22, v3\n"
"s_cmp_eq_u32 s93, 0\ns_cbranch_scc1 LEP_%=\nLEPK_%=:\n"
"v_mfma_f32_16x16x32_bf16 v[32:35], v[184:187], a[84:87], v[32:35]\n"
"s_bfe_u32 s20, s89, 65536\ns_lshl_b32 s20, s20, 13\ns_add_u32 s20, s20, 65536\n"
"ds_read_b64_tr_b16 v[106:107], v229 offset:43008\n"
"v_and_b32 v128, s73, v128\n"
"v_mfma_f32_16x16x32_bf16 v[32:35], v[188:191], a[88:91], v[32:35]\n"
"buffer_load_dwordx4 v[72:75], v236, s[32:35], s20 offen sc1\n"
"v_mfma_f32_16x16x32_bf16 v[32:35], v[192:195], a[92:95], v[32:35]\n"
"v_lshlrev_b32 v203, 16, v133\n"
"v_lshlrev_b32 v205, 16, v141\n"
"v_mfma_f32_16x16x32_bf16 v[32:35], v[208:211], a[32:35], v[32:35]\n"
"s_bfe_u32 s20, s89, 65540\ns_lshl_b32 s20, s20, 13\ns_add_u32 s20, s20, 1114112\n"
"v_lshlrev_b32 v204, 16, v140\n"
"v_add_f32 v56, v56, v204\n"
"v_mfma_f32_16x16x32_bf16 v[32:35], v[212:215], a[36:39], v[32:35]\n"
"buffer_load_dwordx4 v[76:79], v236, s[32:35], s20 offen sc1\n"
"v_mfma_f32_16x16x32_bf16 v[32:35], v[216:219], a[40:43], v[32:35]\n"
"v_and_b32 v136, s73, v136\n"
"v_subrev_f32 v17, s22, v17\n"
"v_mfma_f32_16x16x32_bf16 v[32:35], v[220:223], a[44:47], v[32:35]\n"
"s_bfe_u32 s20, s89, 65544\ns_lshl_b32 s20, s20, 13\ns_add_u32 s20, s20, 2162688\n"
"v_add_f32 v61, v61, v134\n"
"v_and_b32 v141, s73, v141\n"
"v_mfma_f32_16x16x32_bf16 v[48:51], v[220:223], a[228:231], v[48:51]\n"
"buffer_load_dwordx4 v[80:83], v236, s[32:35], s20 offen sc1\n"
"v_mfma_f32_16x16x32_bf16 v[48:51], v[216:219], a[224:227], v[48:51]\n"
"v_add_f32 v58, v58, v203\n"
"v_subrev_f32 v19, s22, v19\n"
"v_mfma_f32_16x16x32_bf16 v[48:51], v[212:215], a[212:215], v[48:51]\n"
"s_bfe_u32 s20, s89, 65548\ns_lshl_b32 s20, s20, 13\ns_add_u32 s20, s20, 3211264\n"
"v_and_b32 v140, s73, v140\n"
"v_lshlrev_b32 v204, 16, v146\n"
"v_mfma_f32_16x16x32_bf16 v[48:51], v[208:211], a[208:211], v[48:51]\n"
"buffer_load_dwordx4 v[84:87], v236, s[32:35], s20 offen sc1\n"
"v_mfma_f32_16x16x32_bf16 v[48:51], v[192:195], a[236:239], v[48:51]\n"
"v_add_f32 v57, v57, v128\n"
"v_subrev_f32 v34, s22, v34\n"
"v_mfma_f32_16x16x32_bf16 v[48:51], v[188:191], a[232:235], v[48:51]\n"
"s_bfe_u32 s20, s89, 65552\ns_lshl_b32 s20, s20, 13\ns_add_u32 s20, s20, 4259840\n"
"ds_read_b64_tr_b16 v[214:215], v234 offset:51200\n"
"v_and_b32 v132, s73, v132\n"
"v_mfma_f32_16x16x32_bf16 v[48:51], v[184:187], a[220:223], v[48:51]\n"
"buffer_load_dwordx4 v[88:91], v236, s[32:35], s20 offen sc1\n"
"v_mfma_f32_16x16x32_bf16 v[48:51], v[180:183], a[216:219], v[48:51]\n"
"s_waitcnt lgkmcnt(1)\n"
"v_add_f32 v60, v60, v202\n"
"v_add_f32 v60, v60, v206\n"
"v_mfma_f32_16x16x32_bf16 v[4:7], v[96:99], a[48:51], v[4:7]\n"
"s_bfe_u32 s20, s89, 65556\ns_lshl_b32 s20, s20, 13\ns_add_u32 s20, s20, 5308416\n"
"v_lshlrev_b32 v206, 16, v148\n"
"v_subrev_f32 v35, s22, v35\n"
"v_mfma_f32_16x16x32_bf16 v[4:7], v[100:103], a[52:55], v[4:7]\n"
"buffer_load_dwordx4 v[92:95], v236, s[32:35], s20 offen sc1\n"
"v_mfma_f32_16x16x32_bf16 v[4:7], v[104:107], a[56:59], v[4:7]\n"
"v_add_f32 v60, v60, v204\n"
"v_subrev_f32 v0, s22, v0\n"
"v_mfma_f32_16x16x32_bf16 v[4:7], v[108:111], a[60:63], v[4:7]\n"
"s_bfe_u32 s20, s89, 65560\ns_lshl_b32 s20, s20, 13\ns_add_u32 s20, s20, 6356992\n"
"v_add_f32 v57, v57, v132\n"
"v_subrev_f32 v33, s22, v33\n"
"v_mfma_f32_16x16x32_bf16 v[4:7], v[112:115], a[0:3], v[4:7]\n"
"buffer_load_dwordx4 v[244:247], v236, s[32:35], s20 offen sc1\n"
"v_mfma_f32_16x16x32_bf16 v[4:7], v[116:119], a[4:7], v[4:7]\n"
"v_and_b32 v138, s73, v138\n"
"v_subrev_f32 v18, s22, v18\n"
"v_mfma_f32_16x16x32_bf16 v[4:7], v[120:123], a[8:11], v[4:7]\n"
"s_bfe_u32 s20, s89, 65564\ns_lshl_b32 s20, s20, 13\ns_add_u32 s20, s20, 7405568\n"
"v_and_b32 v135, s73, v135\n"
"v_subrev_f32 v49, s22, v49\n"
"v_mfma_f32_16x16x32_bf16 v[4:7], v[224:227], a[12:15], v[4:7]\n"
"buffer_load_dwordx4 v[156:159], v236, s[32:35], s20 offen sc1\n"
"v_add_f32 v58, v58, v207\n"
"v_lshlrev_b32 v203, 16, v139\n"
"v_add_f32 v58, v58, v205\n"
"v_and_b32 v142, s73, v142\n"
"v_lshlrev_b32 v202, 16, v144\n"
"v_add_f32 v56, v56, v202\n"
"v_add_f32 v56, v56, v206\n"
"v_subrev_f32 v1, s22, v1\n"
"v_mfma_f32_16x16x32_bf16 v[20:23], v[224:227], a[28:31], v[20:23]\n"
"v_add_f32 v63, v63, v131\n"
"v_lshlrev_b32 v204, 16, v152\n"
"v_mfma_f32_16x16x32_bf16 v[20:23], v[120:123], a[24:27], v[20:23]\n"
"v_and_b32 v133, s73, v133\n"
"v_add_f32 v59, v59, v133\n"
"v_mfma_f32_16x16x32_bf16 v[20:23], v[116:119], a[20:23], v[20:23]\n"
"ds_read_b64_tr_b16 v[218:219], v230 offset:59392\n"
"v_and_b32 v137, s73, v137\n"
"v_mfma_f32_16x16x32_bf16 v[20:23], v[112:115], a[16:19], v[20:23]\n"
"v_and_b32 v148, s73, v148\n"
"v_subrev_f32 v16, s22, v16\n"
"v_mfma_f32_16x16x32_bf16 v[20:23], v[108:111], a[76:79], v[20:23]\n"
"v_add_f32 v62, v62, v203\n"
"v_subrev_f32 v6, s22, v6\n"
"v_mfma_f32_16x16x32_bf16 v[20:23], v[104:107], a[72:75], v[20:23]\n"
"v_add_f32 v57, v57, v136\n"
"v_lshlrev_b32 v205, 16, v147\n"
"v_mfma_f32_16x16x32_bf16 v[20:23], v[100:103], a[68:71], v[20:23]\n"
"ds_read_b64_tr_b16 v[194:195], v234 offset:43008\n"
"v_subrev_f32 v32, s22, v32\n"
"v_mfma_f32_16x16x32_bf16 v[20:23], v[96:99], a[64:67], v[20:23]\n"
"ds_read_b64_tr_b16 v[184:185], v234 offset:32768\n"
"v_add_f32 v59, v59, v137\n"
"v_and_b32 v139, s73, v139\n"
"v_add_f32 v59, v59, v141\n"
"v_lshlrev_b32 v207, 16, v143\n"
"v_lshlrev_b32 v203, 16, v145\n"
"v_and_b32 v145, s73, v145\n"
"v_add_f32 v58, v58, v203\n"
"v_add_f32 v59, v59, v145\n"
"v_and_b32 v147, s73, v147\n"
"v_lshlrev_b32 v203, 16, v151\n"
"v_subrev_f32 v4, s22, v4\n"
"v_subrev_f32 v5, s22, v5\n"
"v_subrev_f32 v7, s22, v7\n"
"v_cvt_pk_bf16_f32 v0, v0, v1\n"
"v_cvt_pk_bf16_f32 v1, v2, v3\n"
"v_cvt_pk_bf16_f32 v3, v6, v7\n"
"v_mfma_f32_16x16x32_bf16 v[36:39], v[96:99], a[80:83], v[36:39]\n"
"v_add_f32 v56, v56, v204\n"
"v_cvt_pk_bf16_f32 v2, v4, v5\n"
"v_mfma_f32_16x16x32_bf16 v[36:39], v[100:103], a[84:87], v[36:39]\n"
"ds_read_b64_tr_b16 v[192:193], v234 offset:40960\n"
"v_add_f32 v61, v61, v138\n"
"v_mfma_f32_16x16x32_bf16 v[36:39], v[104:107], a[88:91], v[36:39]\n"
"v_add_f32 v57, v57, v140\n"
"v_subrev_f32 v23, s22, v23\n"
"v_mfma_f32_16x16x32_bf16 v[36:39], v[108:111], a[92:95], v[36:39]\n"
"ds_read_b64_tr_b16 v[180:181], v230 offset:32768\n"
"v_and_b32 v144, s73, v144\n"
"v_mfma_f32_16x16x32_bf16 v[36:39], v[112:115], a[32:35], v[36:39]\n"
"ds_read_b64_tr_b16 v[210:211], v230 offset:51200\n"
"v_add_f32 v62, v62, v207\n"
"v_add_f32 v62, v62, v205\n"
"v_lshlrev_b32 v207, 16, v149\n"
"v_add_f32 v58, v58, v207\n"
"v_lshlrev_b32 v202, 16, v150\n"
"v_permlane16_swap_b32 v0, v2\n"
"v_permlane16_swap_b32 v1, v3\n"
"v_subrev_f32 v20, s22, v20\n"
"v_cvt_pk_bf16_f32 v16, v16, v17\n"
"v_mfma_f32_16x16x32_bf16 v[36:39], v[116:119], a[36:39], v[36:39]\n"
"ds_read_b64_tr_b16 v[186:187], v234 offset:34816\n"
"v_add_f32 v63, v63, v135\n"
"v_mfma_f32_16x16x32_bf16 v[36:39], v[120:123], a[40:43], v[36:39]\n"
"ds_read_b64_tr_b16 v[188:189], v230 offset:40960\n"
"buffer_store_dwordx4 v[0:3], v238, s[36:39], 0 offen sc0 sc1\n"
"v_and_b32 v143, s73, v143\n"
"v_and_b32 v146, s73, v146\n"
"v_and_b32 v149, s73, v149\n"
"v_lshlrev_b32 v205, 16, v153\n"
"v_and_b32 v153, s73, v153\n"
"v_lshlrev_b32 v206, 16, v154\n"
"v_lshlrev_b32 v207, 16, v155\n"
"v_subrev_f32 v21, s22, v21\n"
"v_subrev_f32 v22, s22, v22\n"
"v_cvt_pk_bf16_f32 v17, v18, v19\n"
"v_cvt_pk_bf16_f32 v18, v20, v21\n"
"v_cvt_pk_bf16_f32 v32, v32, v33\n"
"v_mfma_f32_16x16x32_bf16 v[36:39], v[224:227], a[44:47], v[36:39]\n"
"ds_read_b64_tr_b16 v[182:183], v230 offset:34816\n"
"v_add_f32 v63, v63, v139\n"
"v_add_f32 v61, v61, v142\n"
"v_add_f32 v57, v57, v144\n"
"v_add_f32 v57, v57, v148\n"
"v_add_f32 v59, v59, v149\n"
"v_cvt_pk_bf16_f32 v19, v22, v23\n"
"v_permlane16_swap_b32 v16, v18\n"
"v_cvt_pk_bf16_f32 v33, v34, v35\n"
"v_mfma_f32_16x16x32_bf16 v[52:55], v[224:227], a[228:231], v[52:55]\n"
"ds_read_b64_tr_b16 v[190:191], v230 offset:43008\n"
"v_add_f32 v59, v59, v153\n"
"v_mfma_f32_16x16x32_bf16 v[52:55], v[120:123], a[224:227], v[52:55]\n"
"v_permlane16_swap_b32 v17, v19\n"
"v_mfma_f32_16x16x32_bf16 v[52:55], v[116:119], a[212:215], v[52:55]\n"
"ds_read_b64_tr_b16 v[208:209], v230 offset:49152\n"
"v_subrev_f32 v36, s22, v36\n"
"v_mfma_f32_16x16x32_bf16 v[52:55], v[112:115], a[208:211], v[52:55]\n"
"ds_read_b64_tr_b16 v[222:223], v234 offset:59392\n"
"buffer_store_dwordx4 v[16:19], v238, s[36:39], s52 offen sc0 sc1\n"
"v_add_f32 v63, v63, v143\n"
"v_add_f32 v60, v60, v202\n"
"v_add_f32 v62, v62, v203\n"
"v_and_b32 v152, s73, v152\n"
"v_subrev_f32 v38, s22, v38\n"
"v_subrev_f32 v39, s22, v39\n"
"v_mfma_f32_16x16x32_bf16 v[52:55], v[108:111], a[236:239], v[52:55]\n"
"v_and_b32 v155, s73, v155\n"
"v_cvt_pk_bf16_f32 v35, v38, v39\n"
"v_mfma_f32_16x16x32_bf16 v[52:55], v[104:107], a[232:235], v[52:55]\n"
"ds_read_b64_tr_b16 v[216:217], v230 offset:57344\n"
"v_subrev_f32 v37, s22, v37\n"
"v_mfma_f32_16x16x32_bf16 v[52:55], v[100:103], a[220:223], v[52:55]\n"
"v_and_b32 v150, s73, v150\n"
"v_and_b32 v154, s73, v154\n"
"v_mfma_f32_16x16x32_bf16 v[52:55], v[96:99], a[216:219], v[52:55]\n"
"s_waitcnt lgkmcnt(4)\n"
"ds_read_b64_tr_b16 v[212:213], v234 offset:49152\n"
"ds_read_b64_tr_b16 v[220:221], v234 offset:57344\n"
"v_add_f32 v63, v63, v147\n"
"v_and_b32 v151, s73, v151\n"
"v_add_f32 v63, v63, v151\n"
"v_add_f32 v57, v57, v152\n"
"v_add_f32 v58, v58, v205\n"
"v_add_f32 v60, v60, v206\n"
"v_add_f32 v62, v62, v207\n"
"v_cvt_pk_bf16_f32 v34, v36, v37\n"
"v_permlane16_swap_b32 v33, v35\n"
"v_mfma_f32_16x16x32_bf16 v[8:11], v[180:183], a[48:51], v[8:11]\n"
"v_cvt_pk_bf16_f32 v56, v56, v57\n"
"v_cvt_pk_bf16_f32 v57, v58, v59\n"
"v_mfma_f32_16x16x32_bf16 v[8:11], v[184:187], a[52:55], v[8:11]\n"
"s_waitcnt lgkmcnt(5)\n"
"v_permlane16_swap_b32 v32, v34\n"
"v_mfma_f32_16x16x32_bf16 v[8:11], v[188:191], a[56:59], v[8:11]\n"
"buffer_store_dwordx4 v[32:35], v238, s[36:39], s53 offen sc0 sc1\n"
"ds_read_b64_tr_b16 v[120:121], v231 offset:57344\n"
"v_add_f32 v61, v61, v146\n"
"v_add_f32 v61, v61, v150\n"
"v_add_f32 v61, v61, v154\n"
"v_add_f32 v63, v63, v155\n"
"v_cvt_pk_bf16_f32 v58, v60, v61\n"
"v_cvt_pk_bf16_f32 v59, v62, v63\n"
"v_mfma_f32_16x16x32_bf16 v[8:11], v[192:195], a[60:63], v[8:11]\n"
"s_waitcnt lgkmcnt(4)\n"
"v_permlane16_swap_b32 v57, v59\n"
"v_mfma_f32_16x16x32_bf16 v[8:11], v[208:211], a[0:3], v[8:11]\n"
"s_waitcnt lgkmcnt(2)\n"
"v_permlane16_swap_b32 v56, v58\n"
"v_mfma_f32_16x16x32_bf16 v[8:11], v[212:215], a[4:7], v[8:11]\n"
"buffer_store_dwordx4 v[56:59], v253, s[36:39], 0 offen sc0 sc1\n"
"v_subrev_f32 v48, s22, v48\n"
"v_subrev_f32 v50, s22, v50\n"
"v_subrev_f32 v51, s22, v51\n"
"v_mfma_f32_16x16x32_bf16 v[8:11], v[216:219], a[8:11], v[8:11]\n"
"s_waitcnt lgkmcnt(1)\n"
"ds_read_b64_tr_b16 v[112:113], v231 offset:49152\n"
"v_subrev_f32 v54, s22, v54\n"
"v_mfma_f32_16x16x32_bf16 v[8:11], v[220:223], a[12:15], v[8:11]\n"
"ds_read_b64_tr_b16 v[100:101], v235 offset:32768\n"
"v_subrev_f32 v52, s22, v52\n"
"v_mfma_f32_16x16x32_bf16 v[24:27], v[220:223], a[28:31], v[24:27]\n"
"ds_read_b64_tr_b16 v[102:103], v235 offset:34816\n"
"v_subrev_f32 v53, s22, v53\n"
"v_mfma_f32_16x16x32_bf16 v[24:27], v[216:219], a[24:27], v[24:27]\n"
"v_cvt_pk_bf16_f32 v48, v48, v49\n"
"v_cvt_pk_bf16_f32 v49, v50, v51\n"
"v_mfma_f32_16x16x32_bf16 v[24:27], v[212:215], a[20:23], v[24:27]\n"
"ds_read_b64_tr_b16 v[96:97], v231 offset:32768\n"
"v_cvt_pk_bf16_f32 v50, v52, v53\n"
"v_mfma_f32_16x16x32_bf16 v[24:27], v[208:211], a[16:19], v[24:27]\n"
"s_nop 0\n"
"v_permlane16_swap_b32 v48, v50\n"
"v_mfma_f32_16x16x32_bf16 v[24:27], v[192:195], a[76:79], v[24:27]\n"
"ds_read_b64_tr_b16 v[116:117], v235 offset:49152\n"
"v_subrev_f32 v55, s22, v55\n"
"v_mfma_f32_16x16x32_bf16 v[24:27], v[188:191], a[72:75], v[24:27]\n"
"ds_read_b64_tr_b16 v[98:99], v231 offset:34816\n"
"v_cvt_pk_bf16_f32 v51, v54, v55\n"
"v_mfma_f32_16x16x32_bf16 v[24:27], v[184:187], a[68:71], v[24:27]\n"
"s_nop 0\n"
"v_permlane16_swap_b32 v49, v51\n"
"v_mfma_f32_16x16x32_bf16 v[24:27], v[180:183], a[64:67], v[24:27]\n"
"buffer_store_dwordx4 v[48:51], v252, s[36:39], 0 offen sc0 sc1\n"
"ds_read_b64_tr_b16 v[104:105], v231 offset:40960\n"
"v_mfma_f32_16x16x32_bf16 v[40:43], v[180:183], a[80:83], v[40:43]\n"
"ds_read_b64_tr_b16 v[106:107], v231 offset:43008\n"
"ds_read_b64_tr_b16 v[108:109], v235 offset:40960\n"
"v_mfma_f32_16x16x32_bf16 v[40:43], v[184:187], a[84:87], v[40:43]\n"
"ds_read_b64_tr_b16 v[110:111], v235 offset:43008\n"
"v_mfma_f32_16x16x32_bf16 v[40:43], v[188:191], a[88:91], v[40:43]\n"
"ds_read_b64_tr_b16 v[114:115], v231 offset:51200\n"
"v_subrev_f32 v10, s22, v10\n"
"v_mfma_f32_16x16x32_bf16 v[40:43], v[192:195], a[92:95], v[40:43]\n"
"ds_read_b64_tr_b16 v[118:119], v235 offset:51200\n"
"v_mfma_f32_16x16x32_bf16 v[40:43], v[208:211], a[32:35], v[40:43]\n"
"ds_read_b64_tr_b16 v[122:123], v231 offset:59392\n"
"v_subrev_f32 v24, s22, v24\n"
"v_mfma_f32_16x16x32_bf16 v[40:43], v[212:215], a[36:39], v[40:43]\n"
"ds_read_b64_tr_b16 v[224:225], v235 offset:57344\n"
"ds_read_b64_tr_b16 v[226:227], v235 offset:59392\n"
"v_mfma_f32_16x16x32_bf16 v[40:43], v[216:219], a[40:43], v[40:43]\n"
"v_mfma_f32_16x16x32_bf16 v[40:43], v[220:223], a[44:47], v[40:43]\n"
"s_waitcnt lgkmcnt(8)\n"
"v_mov_b32 v160, 0\n"
"v_subrev_f32 v11, s22, v11\n"
"v_mfma_f32_16x16x32_bf16 v[12:15], v[96:99], a[48:51], v[12:15]\n"
"v_subrev_f32 v8, s22, v8\n"
"v_subrev_f32 v9, s22, v9\n"
"v_mfma_f32_16x16x32_bf16 v[12:15], v[100:103], a[52:55], v[12:15]\n"
"s_waitcnt lgkmcnt(5)\n"
"v_cvt_pk_bf16_f32 v8, v8, v9\n"
"v_mfma_f32_16x16x32_bf16 v[12:15], v[104:107], a[56:59], v[12:15]\n"
"v_subrev_f32 v27, s22, v27\n"
"v_mfma_f32_16x16x32_bf16 v[12:15], v[108:111], a[60:63], v[12:15]\n"
"s_waitcnt lgkmcnt(3)\n"
"v_mfma_f32_16x16x32_bf16 v[12:15], v[112:115], a[0:3], v[12:15]\n"
"v_mfma_f32_16x16x32_bf16 v[12:15], v[116:119], a[4:7], v[12:15]\n"
"s_waitcnt lgkmcnt(0)\n"
"v_mfma_f32_16x16x32_bf16 v[12:15], v[120:123], a[8:11], v[12:15]\n"
"v_subrev_f32 v40, s22, v40\n"
"v_mfma_f32_16x16x32_bf16 v[12:15], v[224:227], a[12:15], v[12:15]\n"
"s_waitcnt vmcnt(5)\n"
"v_mfma_f32_16x16x32_bf16 v[64:67], v[162:165], v[72:75], 0\n"
"v_mfma_f32_16x16x32_bf16 v[64:67], v[162:165], v[76:79], v[64:67]\n"
"v_cvt_pk_bf16_f32 v9, v10, v11\n"
"v_mfma_f32_16x16x32_bf16 v[64:67], v[162:165], v[80:83], v[64:67]\n"
"s_nop 2\n"
"v_subrev_f32 v12, s22, v12\n"
"v_mfma_f32_16x16x32_bf16 v[64:67], v[162:165], v[84:87], v[64:67]\n"
"v_subrev_f32 v25, s22, v25\n"
"v_mfma_f32_16x16x32_bf16 v[64:67], v[162:165], v[88:91], v[64:67]\n"
"v_subrev_f32 v13, s22, v13\n"
"v_mfma_f32_16x16x32_bf16 v[64:67], v[162:165], v[92:95], v[64:67]\n"
"v_subrev_f32 v15, s22, v15\n"
"v_mfma_f32_16x16x32_bf16 v[64:67], v[162:165], v[244:247], v[64:67]\n"
"v_subrev_f32 v14, s22, v14\n"
"v_mfma_f32_16x16x32_bf16 v[64:67], v[162:165], v[156:159], v[64:67]\n"
"v_subrev_f32 v42, s22, v42\n"
"v_mfma_f32_16x16x32_bf16 v[68:71], v[160:163], v[72:75], 0\n"
"v_cvt_pk_bf16_f32 v11, v14, v15\n"
"v_subrev_f32 v41, s22, v41\n"
"v_mfma_f32_16x16x32_bf16 v[68:71], v[160:163], v[76:79], v[68:71]\n"
"v_cvt_pk_bf16_f32 v10, v12, v13\n"
"v_cvt_pk_bf16_f32 v24, v24, v25\n"
"v_mfma_f32_16x16x32_bf16 v[68:71], v[160:163], v[80:83], v[68:71]\n"
"v_mfma_f32_16x16x32_bf16 v[68:71], v[160:163], v[84:87], v[68:71]\n"
"v_permlane16_swap_b32 v9, v11\n"
"v_mfma_f32_16x16x32_bf16 v[68:71], v[160:163], v[88:91], v[68:71]\n"
"v_mfma_f32_16x16x32_bf16 v[68:71], v[160:163], v[92:95], v[68:71]\n"
"v_cvt_pk_bf16_f32 v64, v64, v65\n"
"v_cvt_pk_bf16_f32 v40, v40, v41\n"
"v_mfma_f32_16x16x32_bf16 v[68:71], v[160:163], v[244:247], v[68:71]\n"
"v_permlane16_swap_b32 v8, v10\n"
"v_mfma_f32_16x16x32_bf16 v[68:71], v[160:163], v[156:159], v[68:71]\n"
"buffer_store_dwordx4 v[8:11], v239, s[36:39], 0 offen sc0 sc1\n"
"v_mfma_f32_16x16x32_bf16 v[28:31], v[224:227], a[28:31], v[28:31]\n"
"v_mfma_f32_16x16x32_bf16 v[28:31], v[120:123], a[24:27], v[28:31]\n"
"v_cvt_pk_bf16_f32 v65, v66, v67\n"
"v_subrev_f32 v43, s22, v43\n"
"v_mfma_f32_16x16x32_bf16 v[28:31], v[116:119], a[20:23], v[28:31]\n"
"v_mfma_f32_16x16x32_bf16 v[28:31], v[112:115], a[16:19], v[28:31]\n"
"v_mfma_f32_16x16x32_bf16 v[28:31], v[108:111], a[76:79], v[28:31]\n"
"v_cvt_pk_bf16_f32 v66, v68, v69\n"
"v_cvt_pk_bf16_f32 v67, v70, v71\n"
"v_mfma_f32_16x16x32_bf16 v[28:31], v[104:107], a[72:75], v[28:31]\n"
"v_permlane16_swap_b32 v64, v66\n"
"v_mfma_f32_16x16x32_bf16 v[28:31], v[100:103], a[68:71], v[28:31]\n"
"v_permlane16_swap_b32 v65, v67\n"
"v_mfma_f32_16x16x32_bf16 v[28:31], v[96:99], a[64:67], v[28:31]\n"
"buffer_store_dwordx4 v[64:67], v253, s[36:39], 0 offen offset:128 sc0 sc1\n"
"v_cvt_pk_bf16_f32 v41, v42, v43\n"
"v_mfma_f32_16x16x32_bf16 v[44:47], v[96:99], a[80:83], v[44:47]\n"
"v_subrev_f32 v26, s22, v26\n"
"v_cvt_pk_bf16_f32 v25, v26, v27\n"
"v_mfma_f32_16x16x32_bf16 v[44:47], v[100:103], a[84:87], v[44:47]\n"
"v_mfma_f32_16x16x32_bf16 v[44:47], v[104:107], a[88:91], v[44:47]\n"
"s_nop 0\n"
"v_subrev_f32 v30, s22, v30\n"
"v_subrev_f32 v31, s22, v31\n"
"v_mfma_f32_16x16x32_bf16 v[44:47], v[108:111], a[92:95], v[44:47]\n"
"v_subrev_f32 v28, s22, v28\n"
"v_subrev_f32 v29, s22, v29\n"
"v_mfma_f32_16x16x32_bf16 v[44:47], v[112:115], a[32:35], v[44:47]\n"
"v_cvt_pk_bf16_f32 v26, v28, v29\n"
"v_cvt_pk_bf16_f32 v27, v30, v31\n"
"v_mfma_f32_16x16x32_bf16 v[44:47], v[116:119], a[36:39], v[44:47]\n"
"s_nop 0\n"
"v_permlane16_swap_b32 v25, v27\n"
"v_mfma_f32_16x16x32_bf16 v[44:47], v[120:123], a[40:43], v[44:47]\n"
"v_permlane16_swap_b32 v24, v26\n"
"v_mfma_f32_16x16x32_bf16 v[44:47], v[224:227], a[44:47], v[44:47]\n"
"buffer_store_dwordx4 v[24:27], v239, s[36:39], s52 offen sc0 sc1\n"
"s_nop 6\n"
"v_subrev_f32 v44, s22, v44\n"
"v_subrev_f32 v45, s22, v45\n"
"v_subrev_f32 v46, s22, v46\n"
"v_subrev_f32 v47, s22, v47\n"
"v_cvt_pk_bf16_f32 v42, v44, v45\n"
"v_cvt_pk_bf16_f32 v43, v46, v47\n"
"s_nop 0\n"
"v_permlane16_swap_b32 v40, v42\n"
"v_permlane16_swap_b32 v41, v43\n"
"buffer_store_dwordx4 v[40:43], v239, s[36:39], s53 offen sc0 sc1\n"
"s_branch LFIN_%=\n"
"LSW_%=:\n"
"s_add_u32 s16, s8, s95\n"
"s_addc_u32 s17, s9, 0\n"
"s_sub_u32 s18, 655360, s95\n"
"s_add_u32 s20, s8, s100\n"
"s_addc_u32 s21, s9, 0\n"
"s_sub_u32 s22, 655360, s100\n"
"s_mov_b32 s56, 0\n"
"s_mov_b32 s55, 0\n"
"s_mov_b32 s58, 0\n"
"s_mov_b32 s57, 0\n"
"s_mov_b32 s59, 163840\n"
"s_mov_b32 s52, 9216\n"
"s_mov_b32 s53, 18432\n"
"s_mov_b32 s54, 27648\n"
"v_lshrrev_b32 v206, 7, v242\n"
"v_mul_u32_u24 v206, 1280, v206\n"
"v_lshrrev_b32 v240, 3, v242\n"
"v_and_b32 v240, 0x60, v240\n"
"v_and_b32 v207, 0x70, v242\n"
"v_xor_b32 v240, v240, v207\n"
"v_add_u32 v240, v206, v240\n"
"v_mov_b32 v207, v240\n"
"buffer_load_dword v252, v240, s[16:19], 0 offen\n"
"s_branch LSWK_%=\n"
"LGP_%=:\n"
"s_mov_b32 s80, 0x40000\n"
"LGPa_%=:\n"
"s_sleep 1\n"
"global_load_dword v200, v201, s[6:7] sc1\n"
"s_sub_u32 s80, s80, 1\n"
"s_cmp_eq_u32 s80, 0\n"
"s_waitcnt vmcnt(0)\n"
"s_cbranch_scc1 LGPK_%=\n"
"v_subrev_u32 v200, s21, v200\n"
"v_cmp_lt_u32 vcc, s69, v200\n"
"s_nop 4\n"
"s_cbranch_vccnz LGPa_%=\n"
"s_branch LGPK_%=\n"
"LGQ_%=:\n"
"s_mov_b32 s80, 0x40000\n"
"LGQa_%=:\n"
"s_sleep 1\n"
"global_load_dword v200, v201, s[6:7] sc1\n"
"s_sub_u32 s80, s80, 1\n"
"s_cmp_eq_u32 s80, 0\n"
"s_waitcnt vmcnt(0)\n"
"s_cbranch_scc1 LGQK_%=\n"
"v_subrev_u32 v200, s21, v200\n"
"v_cmp_lt_u32 vcc, s69, v200\n"
"s_nop 4\n"
"s_cbranch_vccnz LGQa_%=\n"
"s_branch LGQK_%=\n"
"LGR_%=:\n"
"s_mov_b32 s80, 0x40000\n"
"LGRa_%=:\n"
"s_sleep 1\n"
"global_load_dword v200, v201, s[6:7] sc1\n"
"s_sub_u32 s80, s80, 1\n"
"s_cmp_eq_u32 s80, 0\n"
"s_waitcnt vmcnt(0)\n"
"s_cbranch_scc1 LGRK_%=\n"
"v_subrev_u32 v200, s21, v200\n"
"v_cmp_lt_u32 vcc, s69, v200\n"
"s_nop 4\n"
"s_cbranch_vccnz LGRa_%=\n"
"s_branch LGRK_%=\n"
"LGV_%=:\n"
"s_mov_b32 s80, 0x40000\n"
"LGVa_%=:\n"
"s_sleep 1\n"
"global_load_dword v200, v201, s[6:7] sc1\n"
"s_sub_u32 s80, s80, 1\n"
"s_cmp_eq_u32 s80, 0\n"
"s_waitcnt vmcnt(0)\n"
"s_cbranch_scc1 LGVK_%=\n"
"v_subrev_u32 v200, s21, v200\n"
"v_cmp_lt_u32 vcc, s69, v200\n"
"s_nop 4\n"
"s_cbranch_vccnz LGVa_%=\n"
"s_branch LGVK_%=\n"
"LEP_%=:\n"
"s_add_u32 s92, s92, 1\n"
"v_mov_b32 v200, s92\n"
"v_mov_b32 v255, 0\n"
"s_mov_b64 exec, 1\n"
"global_store_dword v255, v200, s[6:7] offset:128 sc1\n"
"s_mov_b64 exec, -1\n"
"s_branch LEPK_%=\n"
"LPCE0_%=:\n"
"s_memrealtime s[80:81]\n"
"s_sub_u32 s82, s67, s66\n"
"s_cmp_gt_i32 s82, 0\n"
"s_cselect_b32 s83, s67, s66\n"
"s_waitcnt lgkmcnt(0)\n"
"s_mov_b32 s63, 0\n"
"s_mov_b32 s62, s72\n"
"s_sub_u32 s82, s83, s80\n"
"s_cmp_gt_i32 s82, 3\n"
"s_cbranch_scc0 LPEE0_%=\n"
"LPDE0_%=:\n"
"s_sleep 1\n"
"s_memrealtime s[80:81]\n"
"s_waitcnt lgkmcnt(0)\n"
"s_sub_u32 s82, s83, s80\n"
"s_cmp_gt_i32 s82, 3\n"
"s_cbranch_scc1 LPDE0_%=\n"
"LPEE0_%=:\n"
"s_add_u32 s66, s66, s72\n"
"s_add_u32 s66, s66, 4\n"
"s_add_u32 s67, s80, s72\n"
"s_sub_u32 s72, s72, 4\n"
"s_max_u32 s72, s72, s62\n"
"s_branch LPKE0_%=\n"
"LPCE1_%=:\n"
"s_memrealtime s[80:81]\n"
"s_sub_u32 s82, s67, s66\n"
"s_cmp_gt_i32 s82, 0\n"
"s_cselect_b32 s83, s67, s66\n"
"s_waitcnt lgkmcnt(0)\n"
"s_add_u32 s72, s62, 20\n"
"s_sub_u32 s82, s83, s80\n"
"s_cmp_gt_i32 s82, 3\n"
"s_cbranch_scc0 LPEE1_%=\n"
"LPDE1_%=:\n"
"s_sleep 1\n"
"s_memrealtime s[80:81]\n"
"s_waitcnt lgkmcnt(0)\n"
"s_sub_u32 s82, s83, s80\n"
"s_cmp_gt_i32 s82, 3\n"
"s_cbranch_scc1 LPDE1_%=\n"
"LPEE1_%=:\n"
"s_add_u32 s66, s66, s72\n"
"s_add_u32 s66, s66, 4\n"
"s_add_u32 s67, s80, s72\n"
"s_sub_u32 s72, s72, 4\n"
"s_max_u32 s72, s72, s62\n"
"s_branch LPKE1_%=\n"
"LPCT0_%=:\n"
"s_memrealtime s[80:81]\n"
"s_sub_u32 s82, s67, s66\n"
"s_cmp_gt_i32 s82, 0\n"
"s_cselect_b32 s83, s67, s66\n"
"s_waitcnt lgkmcnt(0)\n"
"s_sub_u32 s82, s83, s80\n"
"s_cmp_gt_i32 s82, 3\n"
"s_cbranch_scc0 LPET0_%=\n"
"LPDT0_%=:\n"
"s_sleep 1\n"
"s_memrealtime s[80:81]\n"
"s_waitcnt lgkmcnt(0)\n"
"s_sub_u32 s82, s83, s80\n"
"s_cmp_gt_i32 s82, 3\n"
"s_cbranch_scc1 LPDT0_%=\n"
"LPET0_%=:\n"
"s_add_u32 s66, s66, s72\n"
"s_add_u32 s66, s66, 4\n"
"s_add_u32 s67, s80, s72\n"
"s_sub_u32 s72, s72, 4\n"
"s_max_u32 s72, s72, s62\n"
"s_branch LPKT0_%=\n"
"LPCT1_%=:\n"
"s_memrealtime s[80:81]\n"
"s_sub_u32 s82, s67, s66\n"
"s_cmp_gt_i32 s82, 0\n"
"s_cselect_b32 s83, s67, s66\n"
"s_waitcnt lgkmcnt(0)\n"
"s_add_u32 s72, s62, 20\n"
"s_sub_u32 s82, s83, s80\n"
"s_cmp_gt_i32 s82, 3\n"
"s_cbranch_scc0 LPET1_%=\n"
"LPDT1_%=:\n"
"s_sleep 1\n"
"s_memrealtime s[80:81]\n"
"s_waitcnt lgkmcnt(0)\n"
"s_sub_u32 s82, s83, s80\n"
"s_cmp_gt_i32 s82, 3\n"
"s_cbranch_scc1 LPDT1_%=\n"
"LPET1_%=:\n"
"s_add_u32 s66, s66, s72\n"
"s_add_u32 s66, s66, 4\n"
"s_add_u32 s67, s80, s72\n"
"s_sub_u32 s72, s72, 4\n"
"s_max_u32 s72, s72, s62\n"
"s_branch LPKT1_%=\n"
"LPCL0_%=:\n"
"s_memrealtime s[80:81]\n"
"s_sub_u32 s82, s67, s66\n"
"s_cmp_gt_i32 s82, 0\n"
"s_cselect_b32 s83, s67, s66\n"
"s_waitcnt lgkmcnt(0)\n"
"s_sub_u32 s82, s83, s80\n"
"s_cmp_gt_i32 s82, 3\n"
"s_cbranch_scc0 LPEL0_%=\n"
"LPDL0_%=:\n"
"s_sleep 1\n"
"s_memrealtime s[80:81]\n"
"s_waitcnt lgkmcnt(0)\n"
"s_sub_u32 s82, s83, s80\n"
"s_cmp_gt_i32 s82, 3\n"
"s_cbranch_scc1 LPDL0_%=\n"
"LPEL0_%=:\n"
"s_add_u32 s66, s66, s72\n"
"s_add_u32 s66, s66, 4\n"
"s_add_u32 s67, s80, s72\n"
"s_sub_u32 s72, s72, 4\n"
"s_max_u32 s72, s72, s62\n"
"s_branch LPKL0_%=\n"
"LPCL1_%=:\n"
"s_memrealtime s[80:81]\n"
"s_sub_u32 s82, s67, s66\n"
"s_cmp_gt_i32 s82, 0\n"
"s_cselect_b32 s83, s67, s66\n"
"s_waitcnt lgkmcnt(0)\n"
"s_sub_u32 s82, s83, s80\n"
"s_cmp_gt_i32 s82, 3\n"
"s_cbranch_scc0 LPEL1_%=\n"
"LPDL1_%=:\n"
"s_sleep 1\n"
"s_memrealtime s[80:81]\n"
"s_waitcnt lgkmcnt(0)\n"
"s_sub_u32 s82, s83, s80\n"
"s_cmp_gt_i32 s82, 3\n"
"s_cbranch_scc1 LPDL1_%=\n"
"LPEL1_%=:\n"
"s_add_u32 s66, s66, s72\n"
"s_add_u32 s66, s66, 4\n"
"s_add_u32 s67, s80, s72\n"
"s_sub_u32 s72, s72, 4\n"
"s_max_u32 s72, s72, s62\n"
"s_branch LPKL1_%=\n"
"LPCL2_%=:\n"
"s_memrealtime s[80:81]\n"
"s_sub_u32 s82, s67, s66\n"
"s_cmp_gt_i32 s82, 0\n"
"s_cselect_b32 s83, s67, s66\n"
"s_waitcnt lgkmcnt(0)\n"
"s_sub_u32 s82, s83, s80\n"
"s_cmp_gt_i32 s82, 3\n"
"s_cbranch_scc0 LPEL2_%=\n"
"LPDL2_%=:\n"
"s_sleep 1\n"
"s_memrealtime s[80:81]\n"
"s_waitcnt lgkmcnt(0)\n"
"s_sub_u32 s82, s83, s80\n"
"s_cmp_gt_i32 s82, 3\n"
"s_cbranch_scc1 LPDL2_%=\n"
"LPEL2_%=:\n"
"s_add_u32 s66, s66, s72\n"
"s_add_u32 s66, s66, 4\n"
"s_add_u32 s67, s80, s72\n"
"s_sub_u32 s72, s72, 4\n"
"s_max_u32 s72, s72, s62\n"
"s_branch LPKL2_%=\n"
"LPCL3_%=:\n"
"s_memrealtime s[80:81]\n"
"s_sub_u32 s82, s67, s66\n"
"s_cmp_gt_i32 s82, 0\n"
"s_cselect_b32 s83, s67, s66\n"
"s_waitcnt lgkmcnt(0)\n"
"s_sub_u32 s82, s83, s80\n"
"s_cmp_gt_i32 s82, 3\n"
"s_cbranch_scc0 LPEL3_%=\n"
"LPDL3_%=:\n"
"s_sleep 1\n"
"s_memrealtime s[80:81]\n"
"s_waitcnt lgkmcnt(0)\n"
"s_sub_u32 s82, s83, s80\n"
"s_cmp_gt_i32 s82, 3\n"
"s_cbranch_scc1 LPDL3_%=\n"
"LPEL3_%=:\n"
"s_add_u32 s66, s66, s72\n"
"s_add_u32 s66, s66, 4\n"
"s_add_u32 s67, s80, s72\n"
"s_sub_u32 s72, s72, 4\n"
"s_max_u32 s72, s72, s62\n"
"s_branch LPKL3_%=\n"
"LPN_%=:\n"
"v_mfma_f32_16x16x32_bf16 v[112:115], a[192:195], a[136:139], v[112:115]\n"
"v_mfma_f32_16x16x32_bf16 v[112:115], a[196:199], a[140:143], v[112:115]\n"
"v_mfma_f32_16x16x32_bf16 v[116:119], a[200:203], a[136:139], v[116:119]\n"
"s_waitcnt vmcnt(11)\n"
"v_mfma_f32_16x16x32_bf16 v[116:119], a[204:207], a[140:143], v[116:119]\n"
"s_barrier\n"
"v_mfma_f32_16x16x32_bf16 v[120:123], a[208:211], a[136:139], v[120:123]\n"
"s_add_u32 s40, s48, 256\n"
"s_xor_b32 m0, s50, 0xc000\n"
"ds_read_b64_tr_b16 a[224:225], v244 offset:16384\n"
"v_mfma_f32_16x16x32_bf16 v[120:123], a[212:215], a[140:143], v[120:123]\n"
"buffer_load_dwordx4 v224, s[28:31], s40 offen nt lds\n"
"v_mfma_f32_16x16x32_bf16 v[124:127], a[216:219], a[136:139], v[124:127]\n"
"ds_read_b64_tr_b16 a[226:227], v244 offset:16896\n"
"v_mfma_f32_16x16x32_bf16 v[124:127], a[220:223], a[140:143], v[124:127]\n"
"ds_read_b64_tr_b16 a[228:229], v244 offset:24576\n"
"v_mfma_f32_16x16x32_bf16 v[140:143], a[216:219], a[144:147], v[140:143]\n"
"ds_read_b64_tr_b16 a[230:231], v244 offset:25088\n"
"v_mfma_f32_16x16x32_bf16 v[140:143], a[220:223], a[148:151], v[140:143]\n"
"ds_read_b64_tr_b16 a[232:233], v245 offset:16384\n"
"v_mfma_f32_16x16x32_bf16 v[136:139], a[208:211], a[144:147], v[136:139]\n"
"ds_read_b64_tr_b16 a[234:235], v245 offset:16896\n"
"v_mfma_f32_16x16x32_bf16 v[136:139], a[212:215], a[148:151], v[136:139]\n"
"buffer_load_dwordx4 v225, s[28:31], s40 offen offset:1024 nt lds\n"
"v_mfma_f32_16x16x32_bf16 v[132:135], a[200:203], a[144:147], v[132:135]\n"
"ds_read_b64_tr_b16 a[236:237], v245 offset:24576\n"
"v_mfma_f32_16x16x32_bf16 v[132:135], a[204:207], a[148:151], v[132:135]\n"
"ds_read_b64_tr_b16 a[238:239], v245 offset:25088\n"
"v_mfma_f32_16x16x32_bf16 v[128:131], a[192:195], a[144:147], v[128:131]\n"
"ds_read_b64_tr_b16 a[240:241], v244 offset:17408\n"
"v_mfma_f32_16x16x32_bf16 v[128:131], a[196:199], a[148:151], v[128:131]\n"
"ds_read_b64_tr_b16 a[242:243], v244 offset:17920\n"
"v_mfma_f32_16x16x32_bf16 v[144:147], a[192:195], a[152:155], v[144:147]\n"
"ds_read_b64_tr_b16 a[244:245], v244 offset:25600\n"
"v_mfma_f32_16x16x32_bf16 v[144:147], a[196:199], a[156:159], v[144:147]\n"
"buffer_load_dwordx4 v226, s[28:31], s40 offen offset:2048 nt lds\n"
"v_mfma_f32_16x16x32_bf16 v[148:151], a[200:203], a[152:155], v[148:151]\n"
"ds_read_b64_tr_b16 a[246:247], v244 offset:26112\n"
"v_mfma_f32_16x16x32_bf16 v[148:151], a[204:207], a[156:159], v[148:151]\n"
"ds_read_b64_tr_b16 a[248:249], v245 offset:17408\n"
"v_mfma_f32_16x16x32_bf16 v[152:155], a[208:211], a[152:155], v[152:155]\n"
"ds_read_b64_tr_b16 a[250:251], v245 offset:17920\n"
"v_mfma_f32_16x16x32_bf16 v[152:155], a[212:215], a[156:159], v[152:155]\n"
"ds_read_b64_tr_b16 a[252:253], v245 offset:25600\n"
"v_mfma_f32_16x16x32_bf16 v[156:159], a[216:219], a[152:155], v[156:159]\n"
"ds_read_b64_tr_b16 a[254:255], v245 offset:26112\n"
"v_mfma_f32_16x16x32_bf16 v[156:159], a[220:223], a[156:159], v[156:159]\n"
"buffer_load_dwordx4 v227, s[28:31], s40 offen offset:3072 nt lds\n"
"v_mfma_f32_16x16x32_bf16 v[172:175], a[216:219], a[160:163], v[172:175]\n"
"s_cmp_eq_u32 s47, 8\n"
"v_mfma_f32_16x16x32_bf16 v[172:175], a[220:223], a[164:167], v[172:175]\n"
"s_cselect_b32 s41, s52, s58\n"
"v_mfma_f32_16x16x32_bf16 v[168:171], a[208:211], a[160:163], v[168:171]\n"
"s_add_u32 s48, s48, s41\n"
"v_mfma_f32_16x16x32_bf16 v[168:171], a[212:215], a[164:167], v[168:171]\n"
"s_sub_u32 s41, s48, 5242880\n"
"v_mfma_f32_16x16x32_bf16 v[164:167], a[200:203], a[160:163], v[164:167]\n"
"s_min_u32 s48, s48, s41\n"
"v_mfma_f32_16x16x32_bf16 v[164:167], a[204:207], a[164:167], v[164:167]\n"
"v_xor_b32 v244, s74, v244\n"
"v_xor_b32 v246, s74, v246\n"
"v_mfma_f32_16x16x32_bf16 v[160:163], a[192:195], a[160:163], v[160:163]\n"
"s_mov_b32 m0, s50\n"
"v_xor_b32 v245, s74, v245\n"
"v_xor_b32 v247, s74, v247\n"
"v_mfma_f32_16x16x32_bf16 v[160:163], a[196:199], a[164:167], v[160:163]\n"
"buffer_load_dwordx4 v224, s[28:31], s48 offen nt lds\n"
"v_mfma_f32_16x16x32_bf16 v[176:179], a[192:195], a[168:171], v[176:179]\n"
"v_xor_b32 v252, s74, v252\n"
"v_xor_b32 v253, s74, v253\n"
"v_mfma_f32_16x16x32_bf16 v[176:179], a[196:199], a[172:175], v[176:179]\n"
"v_xor_b32 v250, s74, v250\n"
"v_xor_b32 v251, s74, v251\n"
"v_mfma_f32_16x16x32_bf16 v[180:183], a[200:203], a[168:171], v[180:183]\n"
"v_mfma_f32_16x16x32_bf16 v[180:183], a[204:207], a[172:175], v[180:183]\n"
"buffer_load_dwordx4 v225, s[28:31], s48 offen offset:1024 nt lds\n"
"v_mfma_f32_16x16x32_bf16 v[184:187], a[208:211], a[168:171], v[184:187]\n"
"v_mfma_f32_16x16x32_bf16 v[184:187], a[212:215], a[172:175], v[184:187]\n"
"v_mfma_f32_16x16x32_bf16 v[188:191], a[216:219], a[168:171], v[188:191]\n"
"v_mfma_f32_16x16x32_bf16 v[188:191], a[220:223], a[172:175], v[188:191]\n"
"v_mfma_f32_16x16x32_bf16 v[204:207], a[216:219], a[176:179], v[204:207]\n"
"v_mfma_f32_16x16x32_bf16 v[204:207], a[220:223], a[180:183], v[204:207]\n"
"buffer_load_dwordx4 v226, s[28:31], s48 offen offset:2048 nt lds\n"
"v_mfma_f32_16x16x32_bf16 v[200:203], a[208:211], a[176:179], v[200:203]\n"
"v_mfma_f32_16x16x32_bf16 v[200:203], a[212:215], a[180:183], v[200:203]\n"
"v_mfma_f32_16x16x32_bf16 v[196:199], a[200:203], a[176:179], v[196:199]\n"
"v_mfma_f32_16x16x32_bf16 v[196:199], a[204:207], a[180:183], v[196:199]\n"
"v_mfma_f32_16x16x32_bf16 v[192:195], a[192:195], a[176:179], v[192:195]\n"
"v_mfma_f32_16x16x32_bf16 v[192:195], a[196:199], a[180:183], v[192:195]\n"
"s_waitcnt lgkmcnt(0)\n"
"buffer_load_dwordx4 v227, s[28:31], s48 offen offset:3072 nt lds\n"
"v_mfma_f32_16x16x32_bf16 v[208:211], a[192:195], a[184:187], v[208:211]\n"
"s_xor_b32 s50, s50, s74\n"
"v_mfma_f32_16x16x32_bf16 v[208:211], a[196:199], a[188:191], v[208:211]\n"
"v_mfma_f32_16x16x32_bf16 v[212:215], a[200:203], a[184:187], v[212:215]\n"
"v_mfma_f32_16x16x32_bf16 v[212:215], a[204:207], a[188:191], v[212:215]\n"
"v_mfma_f32_16x16x32_bf16 v[216:219], a[208:211], a[184:187], v[216:219]\n"
"v_mfma_f32_16x16x32_bf16 v[216:219], a[212:215], a[188:191], v[216:219]\n"
"v_mfma_f32_16x16x32_bf16 v[220:223], a[216:219], a[184:187], v[220:223]\n"
"v_mfma_f32_16x16x32_bf16 v[220:223], a[220:223], a[188:191], v[220:223]\n"
"v_mfma_f32_16x16x32_bf16 v[0:3], a[224:227], a[136:139], v[0:3]\n"
"v_mfma_f32_16x16x32_bf16 v[0:3], a[228:231], a[140:143], v[0:3]\n"
"v_mfma_f32_16x16x32_bf16 v[4:7], a[232:235], a[136:139], v[4:7]\n"
"s_waitcnt vmcnt(8)\n"
"v_mfma_f32_16x16x32_bf16 v[4:7], a[236:239], a[140:143], v[4:7]\n"
"s_barrier\n"
"v_mfma_f32_16x16x32_bf16 v[8:11], a[240:243], a[136:139], v[8:11]\n"
"s_mov_b32 m0, s51\n"
"ds_read_b64_tr_b16 a[192:193], v244 offset:0\n"
"v_mfma_f32_16x16x32_bf16 v[8:11], a[244:247], a[140:143], v[8:11]\n"
"buffer_load_dword v241, s[80:83], 0 offen lds\n"
"s_add_u32 s80, s80, 1456\n"
"s_addc_u32 s81, s81, 0\n"
"buffer_load_dwordx4 v248, s[24:27], s49 offen lds\n"
"v_mfma_f32_16x16x32_bf16 v[12:15], a[248:251], a[136:139], v[12:15]\n"
"ds_read_b64_tr_b16 a[194:195], v244 offset:512\n"
"v_mfma_f32_16x16x32_bf16 v[12:15], a[252:255], a[140:143], v[12:15]\n"
"ds_read_b128 a[136:139], v246 offset:0\n"
"v_mfma_f32_16x16x32_bf16 v[28:31], a[248:251], a[144:147], v[28:31]\n"
"ds_read_b64_tr_b16 a[196:197], v244 offset:8192\n"
"v_mfma_f32_16x16x32_bf16 v[28:31], a[252:255], a[148:151], v[28:31]\n"
"ds_read_b64_tr_b16 a[198:199], v244 offset:8704\n"
"v_mfma_f32_16x16x32_bf16 v[24:27], a[240:243], a[144:147], v[24:27]\n"
"ds_read_b128 a[140:143], v247 offset:0\n"
"v_mfma_f32_16x16x32_bf16 v[24:27], a[244:247], a[148:151], v[24:27]\n"
"buffer_load_dwordx4 v249, s[24:27], s49 offen offset:1024 lds\n"
"v_mfma_f32_16x16x32_bf16 v[20:23], a[232:235], a[144:147], v[20:23]\n"
"ds_read_b64_tr_b16 a[200:201], v245 offset:0\n"
"v_mfma_f32_16x16x32_bf16 v[20:23], a[236:239], a[148:151], v[20:23]\n"
"ds_read_b64_tr_b16 a[202:203], v245 offset:512\n"
"v_mfma_f32_16x16x32_bf16 v[16:19], a[224:227], a[144:147], v[16:19]\n"
"ds_read_b64_tr_b16 a[204:205], v245 offset:8192\n"
"v_mfma_f32_16x16x32_bf16 v[16:19], a[228:231], a[148:151], v[16:19]\n"
"ds_read_b64_tr_b16 a[206:207], v245 offset:8704\n"
"v_mfma_f32_16x16x32_bf16 v[32:35], a[224:227], a[152:155], v[32:35]\n"
"ds_read_b64_tr_b16 a[208:209], v244 offset:1024\n"
"v_mfma_f32_16x16x32_bf16 v[32:35], a[228:231], a[156:159], v[32:35]\n"
"buffer_load_dwordx4 v254, s[24:27], s49 offen offset:2048 lds\n"
"v_mfma_f32_16x16x32_bf16 v[36:39], a[232:235], a[152:155], v[36:39]\n"
"ds_read_b64_tr_b16 a[210:211], v244 offset:1536\n"
"v_mfma_f32_16x16x32_bf16 v[36:39], a[236:239], a[156:159], v[36:39]\n"
"ds_read_b64_tr_b16 a[212:213], v244 offset:9216\n"
"v_mfma_f32_16x16x32_bf16 v[40:43], a[240:243], a[152:155], v[40:43]\n"
"ds_read_b64_tr_b16 a[214:215], v244 offset:9728\n"
"v_mfma_f32_16x16x32_bf16 v[40:43], a[244:247], a[156:159], v[40:43]\n"
"ds_read_b64_tr_b16 a[216:217], v245 offset:1024\n"
"v_mfma_f32_16x16x32_bf16 v[44:47], a[248:251], a[152:155], v[44:47]\n"
"ds_read_b64_tr_b16 a[218:219], v245 offset:1536\n"
"v_mfma_f32_16x16x32_bf16 v[44:47], a[252:255], a[156:159], v[44:47]\n"
"buffer_load_dwordx4 v255, s[24:27], s49 offen offset:3072 lds\n"
"v_mfma_f32_16x16x32_bf16 v[60:63], a[248:251], a[160:163], v[60:63]\n"
"ds_read_b64_tr_b16 a[220:221], v245 offset:9216\n"
"v_mfma_f32_16x16x32_bf16 v[60:63], a[252:255], a[164:167], v[60:63]\n"
"ds_read_b64_tr_b16 a[222:223], v245 offset:9728\n"
"v_mfma_f32_16x16x32_bf16 v[56:59], a[240:243], a[160:163], v[56:59]\n"
"ds_read_b128 a[144:147], v246 offset:2048\n"
"v_mfma_f32_16x16x32_bf16 v[56:59], a[244:247], a[164:167], v[56:59]\n"
"s_add_u32 s40, s49, 40960\n"
"ds_read_b128 a[148:151], v247 offset:2048\n"
"v_mfma_f32_16x16x32_bf16 v[52:55], a[232:235], a[160:163], v[52:55]\n"
"s_add_u32 m0, s51, 4096\n"
"ds_read_b128 a[152:155], v246 offset:4096\n"
"v_mfma_f32_16x16x32_bf16 v[52:55], a[236:239], a[164:167], v[52:55]\n"
"buffer_load_dwordx4 v248, s[24:27], s40 offen lds\n"
"v_mfma_f32_16x16x32_bf16 v[48:51], a[224:227], a[160:163], v[48:51]\n"
"ds_read_b128 a[156:159], v247 offset:4096\n"
"v_mfma_f32_16x16x32_bf16 v[48:51], a[228:231], a[164:167], v[48:51]\n"
"ds_read_b128 a[160:163], v252 offset:0\n"
"v_mfma_f32_16x16x32_bf16 v[64:67], a[224:227], a[168:171], v[64:67]\n"
"ds_read_b128 a[164:167], v253 offset:0\n"
"v_mfma_f32_16x16x32_bf16 v[64:67], a[228:231], a[172:175], v[64:67]\n"
"v_mfma_f32_16x16x32_bf16 v[68:71], a[232:235], a[168:171], v[68:71]\n"
"v_mfma_f32_16x16x32_bf16 v[68:71], a[236:239], a[172:175], v[68:71]\n"
"buffer_load_dwordx4 v249, s[24:27], s40 offen offset:1024 lds\n"
"v_mfma_f32_16x16x32_bf16 v[72:75], a[240:243], a[168:171], v[72:75]\n"
"v_mfma_f32_16x16x32_bf16 v[72:75], a[244:247], a[172:175], v[72:75]\n"
"v_mfma_f32_16x16x32_bf16 v[76:79], a[248:251], a[168:171], v[76:79]\n"
"ds_read_b128 a[168:171], v250 offset:0\n"
"v_mfma_f32_16x16x32_bf16 v[76:79], a[252:255], a[172:175], v[76:79]\n"
"ds_read_b128 a[172:175], v251 offset:0\n"
"v_mfma_f32_16x16x32_bf16 v[92:95], a[248:251], a[176:179], v[92:95]\n"
"v_mfma_f32_16x16x32_bf16 v[92:95], a[252:255], a[180:183], v[92:95]\n"
"buffer_load_dwordx4 v254, s[24:27], s40 offen offset:2048 lds\n"
"v_mfma_f32_16x16x32_bf16 v[88:91], a[240:243], a[176:179], v[88:91]\n"
"s_add_u32 s49, s49, 128\n"
"v_mfma_f32_16x16x32_bf16 v[88:91], a[244:247], a[180:183], v[88:91]\n"
"s_sub_u32 s41, s49, 1280\n"
"v_mfma_f32_16x16x32_bf16 v[84:87], a[232:235], a[176:179], v[84:87]\n"
"s_min_u32 s49, s49, s41\n"
"v_mfma_f32_16x16x32_bf16 v[84:87], a[236:239], a[180:183], v[84:87]\n"
"s_xor_b32 s51, s51, s74\n"
"v_mfma_f32_16x16x32_bf16 v[80:83], a[224:227], a[176:179], v[80:83]\n"
"ds_read_b128 a[176:179], v250 offset:2048\n"
"v_mfma_f32_16x16x32_bf16 v[80:83], a[228:231], a[180:183], v[80:83]\n"
"ds_read_b128 a[180:183], v251 offset:2048\n"
"v_mfma_f32_16x16x32_bf16 v[96:99], a[224:227], a[184:187], v[96:99]\n"
"v_mfma_f32_16x16x32_bf16 v[96:99], a[228:231], a[188:191], v[96:99]\n"
"v_mfma_f32_16x16x32_bf16 v[100:103], a[232:235], a[184:187], v[100:103]\n"
"v_mfma_f32_16x16x32_bf16 v[100:103], a[236:239], a[188:191], v[100:103]\n"
"v_mfma_f32_16x16x32_bf16 v[104:107], a[240:243], a[184:187], v[104:107]\n"
"v_mfma_f32_16x16x32_bf16 v[104:107], a[244:247], a[188:191], v[104:107]\n"
"v_mfma_f32_16x16x32_bf16 v[108:111], a[248:251], a[184:187], v[108:111]\n"
"s_add_u32 s47, s47, 1\n"
"ds_read_b128 a[184:187], v250 offset:4096\n"
"v_mfma_f32_16x16x32_bf16 v[108:111], a[252:255], a[188:191], v[108:111]\n"
"s_waitcnt lgkmcnt(1)\n"
"s_cmp_lg_u32 s47, s62\n"
"ds_read_b128 a[188:191], v251 offset:4096\n"
"s_cbranch_scc1 LPN_%=\n"
"s_branch LPX_%=\n"
"LEND_%=:\n"
"LSLOW_%=:\n"
"s_waitcnt vmcnt(0) lgkmcnt(0)\n"
"s_nop 9\n"
"v_bfe_u32 v148, v242, 4, 4\n"
"v_bfe_u32 v149, v242, 8, 2\n"
"v_lshlrev_b32 v144, 8, v148\n"
"v_lshl_add_u32 v144, v149, 4, v144\n"
"v_add_u32 v144, s43, v144\n"
"v_xor_b32 v145, 64, v144\n"
"v_lshlrev_b32 v146, 1, v148\n"
"v_lshl_add_u32 v146, v149, 10, v146\n"
"v_add_u32 v146, s43, v146\n"
"v_xor_b32 v147, 64, v146\n"
"v_mov_b32 v152, 0xff7fffff\n"
"v_mov_b32 v155, 0\n"
"v_mov_b32 v0, 0\n"
"v_mov_b32 v1, 0\n"
"v_mov_b32 v2, 0\n"
"v_mov_b32 v3, 0\n"
"v_mov_b32 v4, 0\n"
"v_mov_b32 v5, 0\n"
"v_mov_b32 v6, 0\n"
"v_mov_b32 v7, 0\n"
"v_mov_b32 v8, 0\n"
"v_mov_b32 v9, 0\n"
"v_mov_b32 v10, 0\n"
"v_mov_b32 v11, 0\n"
"v_mov_b32 v12, 0\n"
"v_mov_b32 v13, 0\n"
"v_mov_b32 v14, 0\n"
"v_mov_b32 v15, 0\n"
"v_mov_b32 v16, 0\n"
"v_mov_b32 v17, 0\n"
"v_mov_b32 v18, 0\n"
"v_mov_b32 v19, 0\n"
"v_mov_b32 v20, 0\n"
"v_mov_b32 v21, 0\n"
"v_mov_b32 v22, 0\n"
"v_mov_b32 v23, 0\n"
"v_mov_b32 v24, 0\n"
"v_mov_b32 v25, 0\n"
"v_mov_b32 v26, 0\n"
"v_mov_b32 v27, 0\n"
"v_mov_b32 v28, 0\n"
"v_mov_b32 v29, 0\n"
"v_mov_b32 v30, 0\n"
"v_mov_b32 v31, 0\n"
"v_mov_b32 v156, 0xff7fffff\n"
"v_mov_b32 v159, 0\n"
"v_mov_b32 v32, 0\n"
"v_mov_b32 v33, 0\n"
"v_mov_b32 v34, 0\n"
"v_mov_b32 v35, 0\n"
"v_mov_b32 v36, 0\n"
"v_mov_b32 v37, 0\n"
"v_mov_b32 v38, 0\n"
"v_mov_b32 v39, 0\n"
"v_mov_b32 v40, 0\n"
"v_mov_b32 v41, 0\n"
"v_mov_b32 v42, 0\n"
"v_mov_b32 v43, 0\n"
"v_mov_b32 v44, 0\n"
"v_mov_b32 v45, 0\n"
"v_mov_b32 v46, 0\n"
"v_mov_b32 v47, 0\n"
"v_mov_b32 v48, 0\n"
"v_mov_b32 v49, 0\n"
"v_mov_b32 v50, 0\n"
"v_mov_b32 v51, 0\n"
"v_mov_b32 v52, 0\n"
"v_mov_b32 v53, 0\n"
"v_mov_b32 v54, 0\n"
"v_mov_b32 v55, 0\n"
"v_mov_b32 v56, 0\n"
"v_mov_b32 v57, 0\n"
"v_mov_b32 v58, 0\n"
"v_mov_b32 v59, 0\n"
"v_mov_b32 v60, 0\n"
"v_mov_b32 v61, 0\n"
"v_mov_b32 v62, 0\n"
"v_mov_b32 v63, 0\n"
"v_mov_b32 v160, 0xff7fffff\n"
"v_mov_b32 v163, 0\n"
"v_mov_b32 v64, 0\n"
"v_mov_b32 v65, 0\n"
"v_mov_b32 v66, 0\n"
"v_mov_b32 v67, 0\n"
"v_mov_b32 v68, 0\n"
"v_mov_b32 v69, 0\n"
"v_mov_b32 v70, 0\n"
"v_mov_b32 v71, 0\n"
"v_mov_b32 v72, 0\n"
"v_mov_b32 v73, 0\n"
"v_mov_b32 v74, 0\n"
"v_mov_b32 v75, 0\n"
"v_mov_b32 v76, 0\n"
"v_mov_b32 v77, 0\n"
"v_mov_b32 v78, 0\n"
"v_mov_b32 v79, 0\n"
"v_mov_b32 v80, 0\n"
"v_mov_b32 v81, 0\n"
"v_mov_b32 v82, 0\n"
"v_mov_b32 v83, 0\n"
"v_mov_b32 v84, 0\n"
"v_mov_b32 v85, 0\n"
"v_mov_b32 v86, 0\n"
"v_mov_b32 v87, 0\n"
"v_mov_b32 v88, 0\n"
"v_mov_b32 v89, 0\n"
"v_mov_b32 v90, 0\n"
"v_mov_b32 v91, 0\n"
"v_mov_b32 v92, 0\n"
"v_mov_b32 v93, 0\n"
"v_mov_b32 v94, 0\n"
"v_mov_b32 v95, 0\n"
"v_mov_b32 v164, 0xff7fffff\n"
"v_mov_b32 v167, 0\n"
"v_accvgpr_write_b32 a208, 0\n"
"v_accvgpr_write_b32 a209, 0\n"
"v_accvgpr_write_b32 a210, 0\n"
"v_accvgpr_write_b32 a211, 0\n"
"v_accvgpr_write_b32 a212, 0\n"
"v_accvgpr_write_b32 a213, 0\n"
"v_accvgpr_write_b32 a214, 0\n"
"v_accvgpr_write_b32 a215, 0\n"
"v_accvgpr_write_b32 a216, 0\n"
"v_accvgpr_write_b32 a217, 0\n"
"v_accvgpr_write_b32 a218, 0\n"
"v_accvgpr_write_b32 a219, 0\n"
"v_accvgpr_write_b32 a220, 0\n"
"v_accvgpr_write_b32 a221, 0\n"
"v_accvgpr_write_b32 a222, 0\n"
"v_accvgpr_write_b32 a223, 0\n"
"v_accvgpr_write_b32 a224, 0\n"
"v_accvgpr_write_b32 a225, 0\n"
"v_accvgpr_write_b32 a226, 0\n"
"v_accvgpr_write_b32 a227, 0\n"
"v_accvgpr_write_b32 a228, 0\n"
"v_accvgpr_write_b32 a229, 0\n"
"v_accvgpr_write_b32 a230, 0\n"
"v_accvgpr_write_b32 a231, 0\n"
"v_accvgpr_write_b32 a232, 0\n"
"v_accvgpr_write_b32 a233, 0\n"
"v_accvgpr_write_b32 a234, 0\n"
"v_accvgpr_write_b32 a235, 0\n"
"v_accvgpr_write_b32 a236, 0\n"
"v_accvgpr_write_b32 a237, 0\n"
"v_accvgpr_write_b32 a238, 0\n"
"v_accvgpr_write_b32 a239, 0\n"
"s_lshl_b32 s80, s47, 16\n"
"s_add_u32 s81, s80, 4096\n"
"s_mov_b32 s82, 0\n"
"LSLOOP_%=:\n"
"s_lshl_b32 s83, s82, 3\n"
"s_and_b32 s83, s83, 64\n"
"buffer_load_dwordx4 v[128:131], v144, s[84:87], s80 offen offset:0\n"
"buffer_load_dwordx4 v[132:135], v145, s[84:87], s80 offen offset:0\n"
"buffer_load_dwordx4 v[136:139], v144, s[84:87], s80 offen offset:128\n"
"buffer_load_dwordx4 v[140:143], v145, s[84:87], s80 offen offset:128\n"
"s_waitcnt vmcnt(0)\n"
"v_mfma_f32_16x16x32_bf16 v[96:99], v[128:131], a[0:3], 0\n"
"v_mfma_f32_16x16x32_bf16 v[96:99], v[132:135], a[4:7], v[96:99]\n"
"v_mfma_f32_16x16x32_bf16 v[96:99], v[136:139], a[8:11], v[96:99]\n"
"v_mfma_f32_16x16x32_bf16 v[96:99], v[140:143], a[12:15], v[96:99]\n"
"v_mfma_f32_16x16x32_bf16 v[104:107], v[128:131], a[16:19], 0\n"
"v_mfma_f32_16x16x32_bf16 v[104:107], v[132:135], a[20:23], v[104:107]\n"
"v_mfma_f32_16x16x32_bf16 v[104:107], v[136:139], a[24:27], v[104:107]\n"
"v_mfma_f32_16x16x32_bf16 v[104:107], v[140:143], a[28:31], v[104:107]\n"
"v_mfma_f32_16x16x32_bf16 v[112:115], v[128:131], a[32:35], 0\n"
"v_mfma_f32_16x16x32_bf16 v[112:115], v[132:135], a[36:39], v[112:115]\n"
"v_mfma_f32_16x16x32_bf16 v[112:115], v[136:139], a[40:43], v[112:115]\n"
"v_mfma_f32_16x16x32_bf16 v[112:115], v[140:143], a[44:47], v[112:115]\n"
"v_mfma_f32_16x16x32_bf16 v[120:123], v[128:131], a[192:195], 0\n"
"v_mfma_f32_16x16x32_bf16 v[120:123], v[132:135], a[196:199], v[120:123]\n"
"v_mfma_f32_16x16x32_bf16 v[120:123], v[136:139], a[200:203], v[120:123]\n"
"v_mfma_f32_16x16x32_bf16 v[120:123], v[140:143], a[204:207], v[120:123]\n"
"s_nop 7\n"
"buffer_load_dwordx4 v[128:131], v144, s[84:87], s81 offen offset:0\n"
"buffer_load_dwordx4 v[132:135], v145, s[84:87], s81 offen offset:0\n"
"buffer_load_dwordx4 v[136:139], v144, s[84:87], s81 offen offset:128\n"
"buffer_load_dwordx4 v[140:143], v145, s[84:87], s81 offen offset:128\n"
"s_waitcnt vmcnt(0)\n"
"v_mfma_f32_16x16x32_bf16 v[100:103], v[128:131], a[0:3], 0\n"
"v_mfma_f32_16x16x32_bf16 v[100:103], v[132:135], a[4:7], v[100:103]\n"
"v_mfma_f32_16x16x32_bf16 v[100:103], v[136:139], a[8:11], v[100:103]\n"
"v_mfma_f32_16x16x32_bf16 v[100:103], v[140:143], a[12:15], v[100:103]\n"
"v_mfma_f32_16x16x32_bf16 v[108:111], v[128:131], a[16:19], 0\n"
"v_mfma_f32_16x16x32_bf16 v[108:111], v[132:135], a[20:23], v[108:111]\n"
"v_mfma_f32_16x16x32_bf16 v[108:111], v[136:139], a[24:27], v[108:111]\n"
"v_mfma_f32_16x16x32_bf16 v[108:111], v[140:143], a[28:31], v[108:111]\n"
"v_mfma_f32_16x16x32_bf16 v[116:119], v[128:131], a[32:35], 0\n"
"v_mfma_f32_16x16x32_bf16 v[116:119], v[132:135], a[36:39], v[116:119]\n"
"v_mfma_f32_16x16x32_bf16 v[116:119], v[136:139], a[40:43], v[116:119]\n"
"v_mfma_f32_16x16x32_bf16 v[116:119], v[140:143], a[44:47], v[116:119]\n"
"v_mfma_f32_16x16x32_bf16 v[124:127], v[128:131], a[192:195], 0\n"
"v_mfma_f32_16x16x32_bf16 v[124:127], v[132:135], a[196:199], v[124:127]\n"
"v_mfma_f32_16x16x32_bf16 v[124:127], v[136:139], a[200:203], v[124:127]\n"
"v_mfma_f32_16x16x32_bf16 v[124:127], v[140:143], a[204:207], v[124:127]\n"
"s_nop 7\n"
"v_max3_f32 v153, v96, v97, v98\n"
"v_max3_f32 v153, v153, v99, v100\n"
"v_max3_f32 v153, v153, v101, v102\n"
"v_max3_f32 v153, v153, v103, v152\n"
"v_mov_b32 v154, v153\n"
"s_nop 1\n"
"v_permlane32_swap_b32 v153, v154\n"
"v_max_f32 v153, v153, v154\n"
"v_mov_b32 v154, v153\n"
"s_nop 1\n"
"v_permlane16_swap_b32 v153, v154\n"
"v_max_f32 v153, v153, v154\n"
"v_sub_f32 v154, v152, v153\n"
"v_exp_f32 v154, v154\n"
"v_mov_b32 v152, v153\n"
"v_sub_f32 v96, v96, v152\n"
"v_sub_f32 v97, v97, v152\n"
"v_sub_f32 v98, v98, v152\n"
"v_sub_f32 v99, v99, v152\n"
"v_sub_f32 v100, v100, v152\n"
"v_sub_f32 v101, v101, v152\n"
"v_sub_f32 v102, v102, v152\n"
"v_sub_f32 v103, v103, v152\n"
"v_exp_f32 v96, v96\n"
"v_exp_f32 v97, v97\n"
"v_exp_f32 v98, v98\n"
"v_exp_f32 v99, v99\n"
"v_exp_f32 v100, v100\n"
"v_exp_f32 v101, v101\n"
"v_exp_f32 v102, v102\n"
"v_exp_f32 v103, v103\n"
"v_mul_f32 v155, v155, v154\n"
"v_add_f32 v155, v155, v96\n"
"v_add_f32 v155, v155, v97\n"
"v_add_f32 v155, v155, v98\n"
"v_add_f32 v155, v155, v99\n"
"v_add_f32 v155, v155, v100\n"
"v_add_f32 v155, v155, v101\n"
"v_add_f32 v155, v155, v102\n"
"v_add_f32 v155, v155, v103\n"
"v_cvt_pk_bf16_f32 v96, v96, v97\n"
"v_cvt_pk_bf16_f32 v97, v98, v99\n"
"v_cvt_pk_bf16_f32 v98, v100, v101\n"
"v_cvt_pk_bf16_f32 v99, v102, v103\n"
"v_mov_b32_dpp v154, v154 row_ror:12 row_mask:0x2 bank_mask:0xf\n"
"s_nop 1\n"
"v_mov_b32_dpp v154, v154 row_ror:8 row_mask:0x4 bank_mask:0xf\n"
"s_nop 1\n"
"v_mov_b32_dpp v154, v154 row_ror:4 row_mask:0x8 bank_mask:0xf\n"
"s_nop 1\n"
"v_mul_f32_dpp v0, v154, v0 row_newbcast:0 row_mask:0xf bank_mask:0xf\n"
"v_mul_f32_dpp v1, v154, v1 row_newbcast:1 row_mask:0xf bank_mask:0xf\n"
"v_mul_f32_dpp v2, v154, v2 row_newbcast:2 row_mask:0xf bank_mask:0xf\n"
"v_mul_f32_dpp v3, v154, v3 row_newbcast:3 row_mask:0xf bank_mask:0xf\n"
"v_mul_f32_dpp v4, v154, v4 row_newbcast:0 row_mask:0xf bank_mask:0xf\n"
"v_mul_f32_dpp v5, v154, v5 row_newbcast:1 row_mask:0xf bank_mask:0xf\n"
"v_mul_f32_dpp v6, v154, v6 row_newbcast:2 row_mask:0xf bank_mask:0xf\n"
"v_mul_f32_dpp v7, v154, v7 row_newbcast:3 row_mask:0xf bank_mask:0xf\n"
"v_mul_f32_dpp v8, v154, v8 row_newbcast:0 row_mask:0xf bank_mask:0xf\n"
"v_mul_f32_dpp v9, v154, v9 row_newbcast:1 row_mask:0xf bank_mask:0xf\n"
"v_mul_f32_dpp v10, v154, v10 row_newbcast:2 row_mask:0xf bank_mask:0xf\n"
"v_mul_f32_dpp v11, v154, v11 row_newbcast:3 row_mask:0xf bank_mask:0xf\n"
"v_mul_f32_dpp v12, v154, v12 row_newbcast:0 row_mask:0xf bank_mask:0xf\n"
"v_mul_f32_dpp v13, v154, v13 row_newbcast:1 row_mask:0xf bank_mask:0xf\n"
"v_mul_f32_dpp v14, v154, v14 row_newbcast:2 row_mask:0xf bank_mask:0xf\n"
"v_mul_f32_dpp v15, v154, v15 row_newbcast:3 row_mask:0xf bank_mask:0xf\n"
"v_mul_f32_dpp v16, v154, v16 row_newbcast:0 row_mask:0xf bank_mask:0xf\n"
"v_mul_f32_dpp v17, v154, v17 row_newbcast:1 row_mask:0xf bank_mask:0xf\n"
"v_mul_f32_dpp v18, v154, v18 row_newbcast:2 row_mask:0xf bank_mask:0xf\n"
"v_mul_f32_dpp v19, v154, v19 row_newbcast:3 row_mask:0xf bank_mask:0xf\n"
"v_mul_f32_dpp v20, v154, v20 row_newbcast:0 row_mask:0xf bank_mask:0xf\n"
"v_mul_f32_dpp v21, v154, v21 row_newbcast:1 row_mask:0xf bank_mask:0xf\n"
"v_mul_f32_dpp v22, v154, v22 row_newbcast:2 row_mask:0xf bank_mask:0xf\n"
"v_mul_f32_dpp v23, v154, v23 row_newbcast:3 row_mask:0xf bank_mask:0xf\n"
"v_mul_f32_dpp v24, v154, v24 row_newbcast:0 row_mask:0xf bank_mask:0xf\n"
"v_mul_f32_dpp v25, v154, v25 row_newbcast:1 row_mask:0xf bank_mask:0xf\n"
"v_mul_f32_dpp v26, v154, v26 row_newbcast:2 row_mask:0xf bank_mask:0xf\n"
"v_mul_f32_dpp v27, v154, v27 row_newbcast:3 row_mask:0xf bank_mask:0xf\n"
"v_mul_f32_dpp v28, v154, v28 row_newbcast:0 row_mask:0xf bank_mask:0xf\n"
"v_mul_f32_dpp v29, v154, v29 row_newbcast:1 row_mask:0xf bank_mask:0xf\n"
"v_mul_f32_dpp v30, v154, v30 row_newbcast:2 row_mask:0xf bank_mask:0xf\n"
"v_mul_f32_dpp v31, v154, v31 row_newbcast:3 row_mask:0xf bank_mask:0xf\n"
"v_max3_f32 v157, v104, v105, v106\n"
"v_max3_f32 v157, v157, v107, v108\n"
"v_max3_f32 v157, v157, v109, v110\n"
"v_max3_f32 v157, v157, v111, v156\n"
"v_mov_b32 v158, v157\n"
"s_nop 1\n"
"v_permlane32_swap_b32 v157, v158\n"
"v_max_f32 v157, v157, v158\n"
"v_mov_b32 v158, v157\n"
"s_nop 1\n"
"v_permlane16_swap_b32 v157, v158\n"
"v_max_f32 v157, v157, v158\n"
"v_sub_f32 v158, v156, v157\n"
"v_exp_f32 v158, v158\n"
"v_mov_b32 v156, v157\n"
"v_sub_f32 v104, v104, v156\n"
"v_sub_f32 v105, v105, v156\n"
"v_sub_f32 v106, v106, v156\n"
"v_sub_f32 v107, v107, v156\n"
"v_sub_f32 v108, v108, v156\n"
"v_sub_f32 v109, v109, v156\n"
"v_sub_f32 v110, v110, v156\n"
"v_sub_f32 v111, v111, v156\n"
"v_exp_f32 v104, v104\n"
"v_exp_f32 v105, v105\n"
"v_exp_f32 v106, v106\n"
"v_exp_f32 v107, v107\n"
"v_exp_f32 v108, v108\n"
"v_exp_f32 v109, v109\n"
"v_exp_f32 v110, v110\n"
"v_exp_f32 v111, v111\n"
"v_mul_f32 v159, v159, v158\n"
"v_add_f32 v159, v159, v104\n"
"v_add_f32 v159, v159, v105\n"
"v_add_f32 v159, v159, v106\n"
"v_add_f32 v159, v159, v107\n"
"v_add_f32 v159, v159, v108\n"
"v_add_f32 v159, v159, v109\n"
"v_add_f32 v159, v159, v110\n"
"v_add_f32 v159, v159, v111\n"
"v_cvt_pk_bf16_f32 v104, v104, v105\n"
"v_cvt_pk_bf16_f32 v105, v106, v107\n"
"v_cvt_pk_bf16_f32 v106, v108, v109\n"
"v_cvt_pk_bf16_f32 v107, v110, v111\n"
"v_mov_b32_dpp v158, v158 row_ror:12 row_mask:0x2 bank_mask:0xf\n"
"s_nop 1\n"
"v_mov_b32_dpp v158, v158 row_ror:8 row_mask:0x4 bank_mask:0xf\n"
"s_nop 1\n"
"v_mov_b32_dpp v158, v158 row_ror:4 row_mask:0x8 bank_mask:0xf\n"
"s_nop 1\n"
"v_mul_f32_dpp v32, v158, v32 row_newbcast:0 row_mask:0xf bank_mask:0xf\n"
"v_mul_f32_dpp v33, v158, v33 row_newbcast:1 row_mask:0xf bank_mask:0xf\n"
"v_mul_f32_dpp v34, v158, v34 row_newbcast:2 row_mask:0xf bank_mask:0xf\n"
"v_mul_f32_dpp v35, v158, v35 row_newbcast:3 row_mask:0xf bank_mask:0xf\n"
"v_mul_f32_dpp v36, v158, v36 row_newbcast:0 row_mask:0xf bank_mask:0xf\n"
"v_mul_f32_dpp v37, v158, v37 row_newbcast:1 row_mask:0xf bank_mask:0xf\n"
"v_mul_f32_dpp v38, v158, v38 row_newbcast:2 row_mask:0xf bank_mask:0xf\n"
"v_mul_f32_dpp v39, v158, v39 row_newbcast:3 row_mask:0xf bank_mask:0xf\n"
"v_mul_f32_dpp v40, v158, v40 row_newbcast:0 row_mask:0xf bank_mask:0xf\n"
"v_mul_f32_dpp v41, v158, v41 row_newbcast:1 row_mask:0xf bank_mask:0xf\n"
"v_mul_f32_dpp v42, v158, v42 row_newbcast:2 row_mask:0xf bank_mask:0xf\n"
"v_mul_f32_dpp v43, v158, v43 row_newbcast:3 row_mask:0xf bank_mask:0xf\n"
"v_mul_f32_dpp v44, v158, v44 row_newbcast:0 row_mask:0xf bank_mask:0xf\n"
"v_mul_f32_dpp v45, v158, v45 row_newbcast:1 row_mask:0xf bank_mask:0xf\n"
"v_mul_f32_dpp v46, v158, v46 row_newbcast:2 row_mask:0xf bank_mask:0xf\n"
"v_mul_f32_dpp v47, v158, v47 row_newbcast:3 row_mask:0xf bank_mask:0xf\n"
"v_mul_f32_dpp v48, v158, v48 row_newbcast:0 row_mask:0xf bank_mask:0xf\n"
"v_mul_f32_dpp v49, v158, v49 row_newbcast:1 row_mask:0xf bank_mask:0xf\n"
"v_mul_f32_dpp v50, v158, v50 row_newbcast:2 row_mask:0xf bank_mask:0xf\n"
"v_mul_f32_dpp v51, v158, v51 row_newbcast:3 row_mask:0xf bank_mask:0xf\n"
"v_mul_f32_dpp v52, v158, v52 row_newbcast:0 row_mask:0xf bank_mask:0xf\n"
"v_mul_f32_dpp v53, v158, v53 row_newbcast:1 row_mask:0xf bank_mask:0xf\n"
"v_mul_f32_dpp v54, v158, v54 row_newbcast:2 row_mask:0xf bank_mask:0xf\n"
"v_mul_f32_dpp v55, v158, v55 row_newbcast:3 row_mask:0xf bank_mask:0xf\n"
"v_mul_f32_dpp v56, v158, v56 row_newbcast:0 row_mask:0xf bank_mask:0xf\n"
"v_mul_f32_dpp v57, v158, v57 row_newbcast:1 row_mask:0xf bank_mask:0xf\n"
"v_mul_f32_dpp v58, v158, v58 row_newbcast:2 row_mask:0xf bank_mask:0xf\n"
"v_mul_f32_dpp v59, v158, v59 row_newbcast:3 row_mask:0xf bank_mask:0xf\n"
"v_mul_f32_dpp v60, v158, v60 row_newbcast:0 row_mask:0xf bank_mask:0xf\n"
"v_mul_f32_dpp v61, v158, v61 row_newbcast:1 row_mask:0xf bank_mask:0xf\n"
"v_mul_f32_dpp v62, v158, v62 row_newbcast:2 row_mask:0xf bank_mask:0xf\n"
"v_mul_f32_dpp v63, v158, v63 row_newbcast:3 row_mask:0xf bank_mask:0xf\n"
"v_max3_f32 v161, v112, v113, v114\n"
"v_max3_f32 v161, v161, v115, v116\n"
"v_max3_f32 v161, v161, v117, v118\n"
"v_max3_f32 v161, v161, v119, v160\n"
"v_mov_b32 v162, v161\n"
"s_nop 1\n"
"v_permlane32_swap_b32 v161, v162\n"
"v_max_f32 v161, v161, v162\n"
"v_mov_b32 v162, v161\n"
"s_nop 1\n"
"v_permlane16_swap_b32 v161, v162\n"
"v_max_f32 v161, v161, v162\n"
"v_sub_f32 v162, v160, v161\n"
"v_exp_f32 v162, v162\n"
"v_mov_b32 v160, v161\n"
"v_sub_f32 v112, v112, v160\n"
"v_sub_f32 v113, v113, v160\n"
"v_sub_f32 v114, v114, v160\n"
"v_sub_f32 v115, v115, v160\n"
"v_sub_f32 v116, v116, v160\n"
"v_sub_f32 v117, v117, v160\n"
"v_sub_f32 v118, v118, v160\n"
"v_sub_f32 v119, v119, v160\n"
"v_exp_f32 v112, v112\n"
"v_exp_f32 v113, v113\n"
"v_exp_f32 v114, v114\n"
"v_exp_f32 v115, v115\n"
"v_exp_f32 v116, v116\n"
"v_exp_f32 v117, v117\n"
"v_exp_f32 v118, v118\n"
"v_exp_f32 v119, v119\n"
"v_mul_f32 v163, v163, v162\n"
"v_add_f32 v163, v163, v112\n"
"v_add_f32 v163, v163, v113\n"
"v_add_f32 v163, v163, v114\n"
"v_add_f32 v163, v163, v115\n"
"v_add_f32 v163, v163, v116\n"
"v_add_f32 v163, v163, v117\n"
"v_add_f32 v163, v163, v118\n"
"v_add_f32 v163, v163, v119\n"
"v_cvt_pk_bf16_f32 v112, v112, v113\n"
"v_cvt_pk_bf16_f32 v113, v114, v115\n"
"v_cvt_pk_bf16_f32 v114, v116, v117\n"
"v_cvt_pk_bf16_f32 v115, v118, v119\n"
"v_mov_b32_dpp v162, v162 row_ror:12 row_mask:0x2 bank_mask:0xf\n"
"s_nop 1\n"
"v_mov_b32_dpp v162, v162 row_ror:8 row_mask:0x4 bank_mask:0xf\n"
"s_nop 1\n"
"v_mov_b32_dpp v162, v162 row_ror:4 row_mask:0x8 bank_mask:0xf\n"
"s_nop 1\n"
"v_mul_f32_dpp v64, v162, v64 row_newbcast:0 row_mask:0xf bank_mask:0xf\n"
"v_mul_f32_dpp v65, v162, v65 row_newbcast:1 row_mask:0xf bank_mask:0xf\n"
"v_mul_f32_dpp v66, v162, v66 row_newbcast:2 row_mask:0xf bank_mask:0xf\n"
"v_mul_f32_dpp v67, v162, v67 row_newbcast:3 row_mask:0xf bank_mask:0xf\n"
"v_mul_f32_dpp v68, v162, v68 row_newbcast:0 row_mask:0xf bank_mask:0xf\n"
"v_mul_f32_dpp v69, v162, v69 row_newbcast:1 row_mask:0xf bank_mask:0xf\n"
"v_mul_f32_dpp v70, v162, v70 row_newbcast:2 row_mask:0xf bank_mask:0xf\n"
"v_mul_f32_dpp v71, v162, v71 row_newbcast:3 row_mask:0xf bank_mask:0xf\n"
"v_mul_f32_dpp v72, v162, v72 row_newbcast:0 row_mask:0xf bank_mask:0xf\n"
"v_mul_f32_dpp v73, v162, v73 row_newbcast:1 row_mask:0xf bank_mask:0xf\n"
"v_mul_f32_dpp v74, v162, v74 row_newbcast:2 row_mask:0xf bank_mask:0xf\n"
"v_mul_f32_dpp v75, v162, v75 row_newbcast:3 row_mask:0xf bank_mask:0xf\n"
"v_mul_f32_dpp v76, v162, v76 row_newbcast:0 row_mask:0xf bank_mask:0xf\n"
"v_mul_f32_dpp v77, v162, v77 row_newbcast:1 row_mask:0xf bank_mask:0xf\n"
"v_mul_f32_dpp v78, v162, v78 row_newbcast:2 row_mask:0xf bank_mask:0xf\n"
"v_mul_f32_dpp v79, v162, v79 row_newbcast:3 row_mask:0xf bank_mask:0xf\n"
"v_mul_f32_dpp v80, v162, v80 row_newbcast:0 row_mask:0xf bank_mask:0xf\n"
"v_mul_f32_dpp v81, v162, v81 row_newbcast:1 row_mask:0xf bank_mask:0xf\n"
"v_mul_f32_dpp v82, v162, v82 row_newbcast:2 row_mask:0xf bank_mask:0xf\n"
"v_mul_f32_dpp v83, v162, v83 row_newbcast:3 row_mask:0xf bank_mask:0xf\n"
"v_mul_f32_dpp v84, v162, v84 row_newbcast:0 row_mask:0xf bank_mask:0xf\n"
"v_mul_f32_dpp v85, v162, v85 row_newbcast:1 row_mask:0xf bank_mask:0xf\n"
"v_mul_f32_dpp v86, v162, v86 row_newbcast:2 row_mask:0xf bank_mask:0xf\n"
"v_mul_f32_dpp v87, v162, v87 row_newbcast:3 row_mask:0xf bank_mask:0xf\n"
"v_mul_f32_dpp v88, v162, v88 row_newbcast:0 row_mask:0xf bank_mask:0xf\n"
"v_mul_f32_dpp v89, v162, v89 row_newbcast:1 row_mask:0xf bank_mask:0xf\n"
"v_mul_f32_dpp v90, v162, v90 row_newbcast:2 row_mask:0xf bank_mask:0xf\n"
"v_mul_f32_dpp v91, v162, v91 row_newbcast:3 row_mask:0xf bank_mask:0xf\n"
"v_mul_f32_dpp v92, v162, v92 row_newbcast:0 row_mask:0xf bank_mask:0xf\n"
"v_mul_f32_dpp v93, v162, v93 row_newbcast:1 row_mask:0xf bank_mask:0xf\n"
"v_mul_f32_dpp v94, v162, v94 row_newbcast:2 row_mask:0xf bank_mask:0xf\n"
"v_mul_f32_dpp v95, v162, v95 row_newbcast:3 row_mask:0xf bank_mask:0xf\n"
"s_cmp_lg_u32 s83, s43\n"
"s_cbranch_scc1 LSK3A_%=\n"
"v_max3_f32 v165, v120, v121, v122\n"
"v_max3_f32 v165, v165, v123, v124\n"
"v_max3_f32 v165, v165, v125, v126\n"
"v_max3_f32 v165, v165, v127, v164\n"
"v_mov_b32 v166, v165\n"
"s_nop 1\n"
"v_permlane32_swap_b32 v165, v166\n"
"v_max_f32 v165, v165, v166\n"
"v_mov_b32 v166, v165\n"
"s_nop 1\n"
"v_permlane16_swap_b32 v165, v166\n"
"v_max_f32 v165, v165, v166\n"
"v_sub_f32 v166, v164, v165\n"
"v_exp_f32 v166, v166\n"
"v_mov_b32 v164, v165\n"
"v_sub_f32 v120, v120, v164\n"
"v_sub_f32 v121, v121, v164\n"
"v_sub_f32 v122, v122, v164\n"
"v_sub_f32 v123, v123, v164\n"
"v_sub_f32 v124, v124, v164\n"
"v_sub_f32 v125, v125, v164\n"
"v_sub_f32 v126, v126, v164\n"
"v_sub_f32 v127, v127, v164\n"
"v_exp_f32 v120, v120\n"
"v_exp_f32 v121, v121\n"
"v_exp_f32 v122, v122\n"
"v_exp_f32 v123, v123\n"
"v_exp_f32 v124, v124\n"
"v_exp_f32 v125, v125\n"
"v_exp_f32 v126, v126\n"
"v_exp_f32 v127, v127\n"
"v_mul_f32 v167, v167, v166\n"
"v_add_f32 v167, v167, v120\n"
"v_add_f32 v167, v167, v121\n"
"v_add_f32 v167, v167, v122\n"
"v_add_f32 v167, v167, v123\n"
"v_add_f32 v167, v167, v124\n"
"v_add_f32 v167, v167, v125\n"
"v_add_f32 v167, v167, v126\n"
"v_add_f32 v167, v167, v127\n"
"v_cvt_pk_bf16_f32 v120, v120, v121\n"
"v_cvt_pk_bf16_f32 v121, v122, v123\n"
"v_cvt_pk_bf16_f32 v122, v124, v125\n"
"v_cvt_pk_bf16_f32 v123, v126, v127\n"
"v_mov_b32_dpp v166, v166 row_ror:12 row_mask:0x2 bank_mask:0xf\n"
"s_nop 1\n"
"v_mov_b32_dpp v166, v166 row_ror:8 row_mask:0x4 bank_mask:0xf\n"
"s_nop 1\n"
"v_mov_b32_dpp v166, v166 row_ror:4 row_mask:0x8 bank_mask:0xf\n"
"v_accvgpr_read_b32 v124, a208\n"
"s_nop 1\n"
"v_mul_f32_dpp v124, v166, v124 row_newbcast:0 row_mask:0xf bank_mask:0xf\n"
"v_accvgpr_write_b32 a208, v124\n"
"v_accvgpr_read_b32 v125, a209\n"
"s_nop 1\n"
"v_mul_f32_dpp v125, v166, v125 row_newbcast:1 row_mask:0xf bank_mask:0xf\n"
"v_accvgpr_write_b32 a209, v125\n"
"v_accvgpr_read_b32 v126, a210\n"
"s_nop 1\n"
"v_mul_f32_dpp v126, v166, v126 row_newbcast:2 row_mask:0xf bank_mask:0xf\n"
"v_accvgpr_write_b32 a210, v126\n"
"v_accvgpr_read_b32 v127, a211\n"
"s_nop 1\n"
"v_mul_f32_dpp v127, v166, v127 row_newbcast:3 row_mask:0xf bank_mask:0xf\n"
"v_accvgpr_write_b32 a211, v127\n"
"v_accvgpr_read_b32 v124, a212\n"
"s_nop 1\n"
"v_mul_f32_dpp v124, v166, v124 row_newbcast:0 row_mask:0xf bank_mask:0xf\n"
"v_accvgpr_write_b32 a212, v124\n"
"v_accvgpr_read_b32 v125, a213\n"
"s_nop 1\n"
"v_mul_f32_dpp v125, v166, v125 row_newbcast:1 row_mask:0xf bank_mask:0xf\n"
"v_accvgpr_write_b32 a213, v125\n"
"v_accvgpr_read_b32 v126, a214\n"
"s_nop 1\n"
"v_mul_f32_dpp v126, v166, v126 row_newbcast:2 row_mask:0xf bank_mask:0xf\n"
"v_accvgpr_write_b32 a214, v126\n"
"v_accvgpr_read_b32 v127, a215\n"
"s_nop 1\n"
"v_mul_f32_dpp v127, v166, v127 row_newbcast:3 row_mask:0xf bank_mask:0xf\n"
"v_accvgpr_write_b32 a215, v127\n"
"v_accvgpr_read_b32 v124, a216\n"
"s_nop 1\n"
"v_mul_f32_dpp v124, v166, v124 row_newbcast:0 row_mask:0xf bank_mask:0xf\n"
"v_accvgpr_write_b32 a216, v124\n"
"v_accvgpr_read_b32 v125, a217\n"
"s_nop 1\n"
"v_mul_f32_dpp v125, v166, v125 row_newbcast:1 row_mask:0xf bank_mask:0xf\n"
"v_accvgpr_write_b32 a217, v125\n"
"v_accvgpr_read_b32 v126, a218\n"
"s_nop 1\n"
"v_mul_f32_dpp v126, v166, v126 row_newbcast:2 row_mask:0xf bank_mask:0xf\n"
"v_accvgpr_write_b32 a218, v126\n"
"v_accvgpr_read_b32 v127, a219\n"
"s_nop 1\n"
"v_mul_f32_dpp v127, v166, v127 row_newbcast:3 row_mask:0xf bank_mask:0xf\n"
"v_accvgpr_write_b32 a219, v127\n"
"v_accvgpr_read_b32 v124, a220\n"
"s_nop 1\n"
"v_mul_f32_dpp v124, v166, v124 row_newbcast:0 row_mask:0xf bank_mask:0xf\n"
"v_accvgpr_write_b32 a220, v124\n"
"v_accvgpr_read_b32 v125, a221\n"
"s_nop 1\n"
"v_mul_f32_dpp v125, v166, v125 row_newbcast:1 row_mask:0xf bank_mask:0xf\n"
"v_accvgpr_write_b32 a221, v125\n"
"v_accvgpr_read_b32 v126, a222\n"
"s_nop 1\n"
"v_mul_f32_dpp v126, v166, v126 row_newbcast:2 row_mask:0xf bank_mask:0xf\n"
"v_accvgpr_write_b32 a222, v126\n"
"v_accvgpr_read_b32 v127, a223\n"
"s_nop 1\n"
"v_mul_f32_dpp v127, v166, v127 row_newbcast:3 row_mask:0xf bank_mask:0xf\n"
"v_accvgpr_write_b32 a223, v127\n"
"v_accvgpr_read_b32 v124, a224\n"
"s_nop 1\n"
"v_mul_f32_dpp v124, v166, v124 row_newbcast:0 row_mask:0xf bank_mask:0xf\n"
"v_accvgpr_write_b32 a224, v124\n"
"v_accvgpr_read_b32 v125, a225\n"
"s_nop 1\n"
"v_mul_f32_dpp v125, v166, v125 row_newbcast:1 row_mask:0xf bank_mask:0xf\n"
"v_accvgpr_write_b32 a225, v125\n"
"v_accvgpr_read_b32 v126, a226\n"
"s_nop 1\n"
"v_mul_f32_dpp v126, v166, v126 row_newbcast:2 row_mask:0xf bank_mask:0xf\n"
"v_accvgpr_write_b32 a226, v126\n"
"v_accvgpr_read_b32 v127, a227\n"
"s_nop 1\n"
"v_mul_f32_dpp v127, v166, v127 row_newbcast:3 row_mask:0xf bank_mask:0xf\n"
"v_accvgpr_write_b32 a227, v127\n"
"v_accvgpr_read_b32 v124, a228\n"
"s_nop 1\n"
"v_mul_f32_dpp v124, v166, v124 row_newbcast:0 row_mask:0xf bank_mask:0xf\n"
"v_accvgpr_write_b32 a228, v124\n"
"v_accvgpr_read_b32 v125, a229\n"
"s_nop 1\n"
"v_mul_f32_dpp v125, v166, v125 row_newbcast:1 row_mask:0xf bank_mask:0xf\n"
"v_accvgpr_write_b32 a229, v125\n"
"v_accvgpr_read_b32 v126, a230\n"
"s_nop 1\n"
"v_mul_f32_dpp v126, v166, v126 row_newbcast:2 row_mask:0xf bank_mask:0xf\n"
"v_accvgpr_write_b32 a230, v126\n"
"v_accvgpr_read_b32 v127, a231\n"
"s_nop 1\n"
"v_mul_f32_dpp v127, v166, v127 row_newbcast:3 row_mask:0xf bank_mask:0xf\n"
"v_accvgpr_write_b32 a231, v127\n"
"v_accvgpr_read_b32 v124, a232\n"
"s_nop 1\n"
"v_mul_f32_dpp v124, v166, v124 row_newbcast:0 row_mask:0xf bank_mask:0xf\n"
"v_accvgpr_write_b32 a232, v124\n"
"v_accvgpr_read_b32 v125, a233\n"
"s_nop 1\n"
"v_mul_f32_dpp v125, v166, v125 row_newbcast:1 row_mask:0xf bank_mask:0xf\n"
"v_accvgpr_write_b32 a233, v125\n"
"v_accvgpr_read_b32 v126, a234\n"
"s_nop 1\n"
"v_mul_f32_dpp v126, v166, v126 row_newbcast:2 row_mask:0xf bank_mask:0xf\n"
"v_accvgpr_write_b32 a234, v126\n"
"v_accvgpr_read_b32 v127, a235\n"
"s_nop 1\n"
"v_mul_f32_dpp v127, v166, v127 row_newbcast:3 row_mask:0xf bank_mask:0xf\n"
"v_accvgpr_write_b32 a235, v127\n"
"v_accvgpr_read_b32 v124, a236\n"
"s_nop 1\n"
"v_mul_f32_dpp v124, v166, v124 row_newbcast:0 row_mask:0xf bank_mask:0xf\n"
"v_accvgpr_write_b32 a236, v124\n"
"v_accvgpr_read_b32 v125, a237\n"
"s_nop 1\n"
"v_mul_f32_dpp v125, v166, v125 row_newbcast:1 row_mask:0xf bank_mask:0xf\n"
"v_accvgpr_write_b32 a237, v125\n"
"v_accvgpr_read_b32 v126, a238\n"
"s_nop 1\n"
"v_mul_f32_dpp v126, v166, v126 row_newbcast:2 row_mask:0xf bank_mask:0xf\n"
"v_accvgpr_write_b32 a238, v126\n"
"v_accvgpr_read_b32 v127, a239\n"
"s_nop 1\n"
"v_mul_f32_dpp v127, v166, v127 row_newbcast:3 row_mask:0xf bank_mask:0xf\n"
"v_accvgpr_write_b32 a239, v127\n"
"LSK3A_%=:\n"
"s_nop 3\n"
"buffer_load_ushort v128, v146, s[88:91], s80 offen offset:0\n"
"buffer_load_ushort v129, v146, s[88:91], s80 offen offset:256\n"
"buffer_load_ushort v130, v146, s[88:91], s80 offen offset:512\n"
"buffer_load_ushort v131, v146, s[88:91], s80 offen offset:768\n"
"buffer_load_ushort v132, v146, s[88:91], s81 offen offset:0\n"
"buffer_load_ushort v133, v146, s[88:91], s81 offen offset:256\n"
"buffer_load_ushort v134, v146, s[88:91], s81 offen offset:512\n"
"buffer_load_ushort v135, v146, s[88:91], s81 offen offset:768\n"
"buffer_load_ushort v136, v146, s[88:91], s80 offen offset:32\n"
"buffer_load_ushort v137, v146, s[88:91], s80 offen offset:288\n"
"buffer_load_ushort v138, v146, s[88:91], s80 offen offset:544\n"
"buffer_load_ushort v139, v146, s[88:91], s80 offen offset:800\n"
"buffer_load_ushort v140, v146, s[88:91], s81 offen offset:32\n"
"buffer_load_ushort v141, v146, s[88:91], s81 offen offset:288\n"
"buffer_load_ushort v142, v146, s[88:91], s81 offen offset:544\n"
"buffer_load_ushort v143, v146, s[88:91], s81 offen offset:800\n"
"s_waitcnt vmcnt(0)\n"
"v_lshl_or_b32 v128, v129, 16, v128\n"
"v_lshl_or_b32 v129, v131, 16, v130\n"
"v_lshl_or_b32 v130, v133, 16, v132\n"
"v_lshl_or_b32 v131, v135, 16, v134\n"
"v_lshl_or_b32 v136, v137, 16, v136\n"
"v_lshl_or_b32 v137, v139, 16, v138\n"
"v_lshl_or_b32 v138, v141, 16, v140\n"
"v_lshl_or_b32 v139, v143, 16, v142\n"
"v_mfma_f32_16x16x32_bf16 v[0:3], v[96:99], v[128:131], v[0:3]\n"
"s_nop 0\n"
"v_mfma_f32_16x16x32_bf16 v[4:7], v[96:99], v[136:139], v[4:7]\n"
"v_mfma_f32_16x16x32_bf16 v[32:35], v[104:107], v[128:131], v[32:35]\n"
"v_mfma_f32_16x16x32_bf16 v[36:39], v[104:107], v[136:139], v[36:39]\n"
"v_mfma_f32_16x16x32_bf16 v[64:67], v[112:115], v[128:131], v[64:67]\n"
"v_mfma_f32_16x16x32_bf16 v[68:71], v[112:115], v[136:139], v[68:71]\n"
"s_cmp_lg_u32 s83, s43\n"
"s_cbranch_scc1 LSK3B0_%=\n"
"v_mfma_f32_16x16x32_bf16 a[208:211], v[120:123], v[128:131], a[208:211]\n"
"v_mfma_f32_16x16x32_bf16 a[212:215], v[120:123], v[136:139], a[212:215]\n"
"LSK3B0_%=:\n"
"s_nop 3\n"
"buffer_load_ushort v128, v147, s[88:91], s80 offen offset:0\n"
"buffer_load_ushort v129, v147, s[88:91], s80 offen offset:256\n"
"buffer_load_ushort v130, v147, s[88:91], s80 offen offset:512\n"
"buffer_load_ushort v131, v147, s[88:91], s80 offen offset:768\n"
"buffer_load_ushort v132, v147, s[88:91], s81 offen offset:0\n"
"buffer_load_ushort v133, v147, s[88:91], s81 offen offset:256\n"
"buffer_load_ushort v134, v147, s[88:91], s81 offen offset:512\n"
"buffer_load_ushort v135, v147, s[88:91], s81 offen offset:768\n"
"buffer_load_ushort v136, v147, s[88:91], s80 offen offset:32\n"
"buffer_load_ushort v137, v147, s[88:91], s80 offen offset:288\n"
"buffer_load_ushort v138, v147, s[88:91], s80 offen offset:544\n"
"buffer_load_ushort v139, v147, s[88:91], s80 offen offset:800\n"
"buffer_load_ushort v140, v147, s[88:91], s81 offen offset:32\n"
"buffer_load_ushort v141, v147, s[88:91], s81 offen offset:288\n"
"buffer_load_ushort v142, v147, s[88:91], s81 offen offset:544\n"
"buffer_load_ushort v143, v147, s[88:91], s81 offen offset:800\n"
"s_waitcnt vmcnt(0)\n"
"v_lshl_or_b32 v128, v129, 16, v128\n"
"v_lshl_or_b32 v129, v131, 16, v130\n"
"v_lshl_or_b32 v130, v133, 16, v132\n"
"v_lshl_or_b32 v131, v135, 16, v134\n"
"v_lshl_or_b32 v136, v137, 16, v136\n"
"v_lshl_or_b32 v137, v139, 16, v138\n"
"v_lshl_or_b32 v138, v141, 16, v140\n"
"v_lshl_or_b32 v139, v143, 16, v142\n"
"v_mfma_f32_16x16x32_bf16 v[8:11], v[96:99], v[128:131], v[8:11]\n"
"s_nop 0\n"
"v_mfma_f32_16x16x32_bf16 v[12:15], v[96:99], v[136:139], v[12:15]\n"
"v_mfma_f32_16x16x32_bf16 v[40:43], v[104:107], v[128:131], v[40:43]\n"
"v_mfma_f32_16x16x32_bf16 v[44:47], v[104:107], v[136:139], v[44:47]\n"
"v_mfma_f32_16x16x32_bf16 v[72:75], v[112:115], v[128:131], v[72:75]\n"
"v_mfma_f32_16x16x32_bf16 v[76:79], v[112:115], v[136:139], v[76:79]\n"
"s_cmp_lg_u32 s83, s43\n"
"s_cbranch_scc1 LSK3B1_%=\n"
"v_mfma_f32_16x16x32_bf16 a[216:219], v[120:123], v[128:131], a[216:219]\n"
"v_mfma_f32_16x16x32_bf16 a[220:223], v[120:123], v[136:139], a[220:223]\n"
"LSK3B1_%=:\n"
"s_nop 3\n"
"buffer_load_ushort v128, v146, s[88:91], s80 offen offset:128\n"
"buffer_load_ushort v129, v146, s[88:91], s80 offen offset:384\n"
"buffer_load_ushort v130, v146, s[88:91], s80 offen offset:640\n"
"buffer_load_ushort v131, v146, s[88:91], s80 offen offset:896\n"
"buffer_load_ushort v132, v146, s[88:91], s81 offen offset:128\n"
"buffer_load_ushort v133, v146, s[88:91], s81 offen offset:384\n"
"buffer_load_ushort v134, v146, s[88:91], s81 offen offset:640\n"
"buffer_load_ushort v135, v146, s[88:91], s81 offen offset:896\n"
"buffer_load_ushort v136, v146, s[88:91], s80 offen offset:160\n"
"buffer_load_ushort v137, v146, s[88:91], s80 offen offset:416\n"
"buffer_load_ushort v138, v146, s[88:91], s80 offen offset:672\n"
"buffer_load_ushort v139, v146, s[88:91], s80 offen offset:928\n"
"buffer_load_ushort v140, v146, s[88:91], s81 offen offset:160\n"
"buffer_load_ushort v141, v146, s[88:91], s81 offen offset:416\n"
"buffer_load_ushort v142, v146, s[88:91], s81 offen offset:672\n"
"buffer_load_ushort v143, v146, s[88:91], s81 offen offset:928\n"
"s_waitcnt vmcnt(0)\n"
"v_lshl_or_b32 v128, v129, 16, v128\n"
"v_lshl_or_b32 v129, v131, 16, v130\n"
"v_lshl_or_b32 v130, v133, 16, v132\n"
"v_lshl_or_b32 v131, v135, 16, v134\n"
"v_lshl_or_b32 v136, v137, 16, v136\n"
"v_lshl_or_b32 v137, v139, 16, v138\n"
"v_lshl_or_b32 v138, v141, 16, v140\n"
"v_lshl_or_b32 v139, v143, 16, v142\n"
"v_mfma_f32_16x16x32_bf16 v[16:19], v[96:99], v[128:131], v[16:19]\n"
"s_nop 0\n"
"v_mfma_f32_16x16x32_bf16 v[20:23], v[96:99], v[136:139], v[20:23]\n"
"v_mfma_f32_16x16x32_bf16 v[48:51], v[104:107], v[128:131], v[48:51]\n"
"v_mfma_f32_16x16x32_bf16 v[52:55], v[104:107], v[136:139], v[52:55]\n"
"v_mfma_f32_16x16x32_bf16 v[80:83], v[112:115], v[128:131], v[80:83]\n"
"v_mfma_f32_16x16x32_bf16 v[84:87], v[112:115], v[136:139], v[84:87]\n"
"s_cmp_lg_u32 s83, s43\n"
"s_cbranch_scc1 LSK3B2_%=\n"
"v_mfma_f32_16x16x32_bf16 a[224:227], v[120:123], v[128:131], a[224:227]\n"
"v_mfma_f32_16x16x32_bf16 a[228:231], v[120:123], v[136:139], a[228:231]\n"
"LSK3B2_%=:\n"
"s_nop 3\n"
"buffer_load_ushort v128, v147, s[88:91], s80 offen offset:128\n"
"buffer_load_ushort v129, v147, s[88:91], s80 offen offset:384\n"
"buffer_load_ushort v130, v147, s[88:91], s80 offen offset:640\n"
"buffer_load_ushort v131, v147, s[88:91], s80 offen offset:896\n"
"buffer_load_ushort v132, v147, s[88:91], s81 offen offset:128\n"
"buffer_load_ushort v133, v147, s[88:91], s81 offen offset:384\n"
"buffer_load_ushort v134, v147, s[88:91], s81 offen offset:640\n"
"buffer_load_ushort v135, v147, s[88:91], s81 offen offset:896\n"
"buffer_load_ushort v136, v147, s[88:91], s80 offen offset:160\n"
"buffer_load_ushort v137, v147, s[88:91], s80 offen offset:416\n"
"buffer_load_ushort v138, v147, s[88:91], s80 offen offset:672\n"
"buffer_load_ushort v139, v147, s[88:91], s80 offen offset:928\n"
"buffer_load_ushort v140, v147, s[88:91], s81 offen offset:160\n"
"buffer_load_ushort v141, v147, s[88:91], s81 offen offset:416\n"
"buffer_load_ushort v142, v147, s[88:91], s81 offen offset:672\n"
"buffer_load_ushort v143, v147, s[88:91], s81 offen offset:928\n"
"s_waitcnt vmcnt(0)\n"
"v_lshl_or_b32 v128, v129, 16, v128\n"
"v_lshl_or_b32 v129, v131, 16, v130\n"
"v_lshl_or_b32 v130, v133, 16, v132\n"
"v_lshl_or_b32 v131, v135, 16, v134\n"
"v_lshl_or_b32 v136, v137, 16, v136\n"
"v_lshl_or_b32 v137, v139, 16, v138\n"
"v_lshl_or_b32 v138, v141, 16, v140\n"
"v_lshl_or_b32 v139, v143, 16, v142\n"
"v_mfma_f32_16x16x32_bf16 v[24:27], v[96:99], v[128:131], v[24:27]\n"
"s_nop 0\n"
"v_mfma_f32_16x16x32_bf16 v[28:31], v[96:99], v[136:139], v[28:31]\n"
"v_mfma_f32_16x16x32_bf16 v[56:59], v[104:107], v[128:131], v[56:59]\n"
"v_mfma_f32_16x16x32_bf16 v[60:63], v[104:107], v[136:139], v[60:63]\n"
"v_mfma_f32_16x16x32_bf16 v[88:91], v[112:115], v[128:131], v[88:91]\n"
"v_mfma_f32_16x16x32_bf16 v[92:95], v[112:115], v[136:139], v[92:95]\n"
"s_cmp_lg_u32 s83, s43\n"
"s_cbranch_scc1 LSK3B3_%=\n"
"v_mfma_f32_16x16x32_bf16 a[232:235], v[120:123], v[128:131], a[232:235]\n"
"v_mfma_f32_16x16x32_bf16 a[236:239], v[120:123], v[136:139], a[236:239]\n"
"LSK3B3_%=:\n"
"s_nop 3\n"
"s_nop 9\n"
"s_add_u32 s80, s80, 8192\n"
"s_add_u32 s81, s81, 8192\n"
"s_add_u32 s82, s82, 1\n"
"s_cmp_lt_u32 s82, 32\n"
"s_cbranch_scc1 LSLOOP_%=\n"
"v_mov_b32 v154, v155\n"
"s_nop 1\n"
"v_permlane32_swap_b32 v155, v154\n"
"v_add_f32 v155, v155, v154\n"
"v_mov_b32 v154, v155\n"
"s_nop 1\n"
"v_permlane16_swap_b32 v155, v154\n"
"v_add_f32 v155, v155, v154\n"
"v_mov_b32 v196, v155\n"
"v_mov_b32 v158, v159\n"
"s_nop 1\n"
"v_permlane32_swap_b32 v159, v158\n"
"v_add_f32 v159, v159, v158\n"
"v_mov_b32 v158, v159\n"
"s_nop 1\n"
"v_permlane16_swap_b32 v159, v158\n"
"v_add_f32 v159, v159, v158\n"
"v_mov_b32 v198, v159\n"
"v_mov_b32 v162, v163\n"
"s_nop 1\n"
"v_permlane32_swap_b32 v163, v162\n"
"v_add_f32 v163, v163, v162\n"
"v_mov_b32 v162, v163\n"
"s_nop 1\n"
"v_permlane16_swap_b32 v163, v162\n"
"v_add_f32 v163, v163, v162\n"
"v_mov_b32 v200, v163\n"
"v_mov_b32 v166, v167\n"
"s_nop 1\n"
"v_permlane32_swap_b32 v167, v166\n"
"v_add_f32 v167, v167, v166\n"
"v_mov_b32 v166, v167\n"
"s_nop 1\n"
"v_permlane16_swap_b32 v167, v166\n"
"v_add_f32 v167, v167, v166\n"
"v_mov_b32 v202, v167\n"
"v_sub_f32 v192, 0, v164\n"
"v_mov_b32 v96, 0\n"
"v_mov_b32 v97, 0\n"
"v_mov_b32 v98, 0\n"
"v_mov_b32 v99, 0\n"
"v_mov_b32 v100, 0\n"
"v_mov_b32 v101, 0\n"
"v_mov_b32 v102, 0\n"
"v_mov_b32 v103, 0\n"
"v_mov_b32 v104, 0\n"
"v_mov_b32 v105, 0\n"
"v_mov_b32 v106, 0\n"
"v_mov_b32 v107, 0\n"
"v_mov_b32 v108, 0\n"
"v_mov_b32 v109, 0\n"
"v_mov_b32 v110, 0\n"
"v_mov_b32 v111, 0\n"
"v_mov_b32 v112, 0\n"
"v_mov_b32 v113, 0\n"
"v_mov_b32 v114, 0\n"
"v_mov_b32 v115, 0\n"
"v_mov_b32 v116, 0\n"
"v_mov_b32 v117, 0\n"
"v_mov_b32 v118, 0\n"
"v_mov_b32 v119, 0\n"
"v_mov_b32 v120, 0\n"
"v_mov_b32 v121, 0\n"
"v_mov_b32 v122, 0\n"
"v_mov_b32 v123, 0\n"
"v_mov_b32 v204, 0\n"
"v_mov_b32 v205, 0\n"
"s_nop 3\n"
"s_mov_b32 s68, 0\n"
"s_cmp_eq_u32 s47, 4\n"
"s_cbranch_scc1 LBACK0_%=\n"
"s_cmp_eq_u32 s47, 8\n"
"s_cbranch_scc1 LBACK1_%=\n"
"s_cmp_eq_u32 s47, 12\n"
"s_cbranch_scc1 LBACK2_%=\n"
"s_branch LBACK3_%=\n"
"LFIN_%=:\n"
    :: [kalo]"s"((unsigned)ka), [kahi]"s"((unsigned)(ka >> 32)), [wg]"s"(blockIdx.x), [wid]"s"(wid)
    : "v0","v1","v2","v3","v4","v5","v6","v7","v8","v9","v10","v11","v12","v13","v14","v15","v16","v17","v18","v19","v20","v21","v22","v23","v24","v25","v26","v27","v28","v29","v30","v31","v32","v33","v34","v35","v36","v37","v38","v39","v40","v41","v42","v43","v44","v45","v46","v47","v48","v49","v50","v51","v52","v53","v54","v55","v56","v57","v58","v59","v60","v61","v62","v63","v64","v65","v66","v67","v68","v69","v70","v71","v72","v73","v74","v75","v76","v77","v78","v79","v80","v81","v82","v83","v84","v85","v86","v87","v88","v89","v90","v91","v92","v93","v94","v95","v96","v97","v98","v99","v100","v101","v102","v103","v104","v105","v106","v107","v108","v109","v110","v111","v112","v113","v114","v115","v116","v117","v118","v119","v120","v121","v122","v123","v124","v125","v126","v127","v128","v129","v130","v131","v132","v133","v134","v135","v136","v137","v138","v139","v140","v141","v142","v143","v144","v145","v146","v147","v148","v149","v150","v151","v152","v153","v154","v155","v156","v157","v158","v159","v160","v161","v162","v163","v164","v165","v166","v167","v168","v169","v170","v171","v172","v173","v174","v175","v176","v177","v178","v179","v180","v181","v182","v183","v184","v185","v186","v187","v188","v189","v190","v191","v192","v193","v194","v195","v196","v197","v198","v199","v200","v201","v202","v203","v204","v205","v206","v207","v208","v209","v210","v211","v212","v213","v214","v215","v216","v217","v218","v219","v220","v221","v222","v223","v224","v225","v226","v227","v228","v229","v230","v231","v232","v233","v234","v235","v236","v237","v238","v239","v240","v241","v242","v243","v244","v245","v246","v247","v248","v249","v250","v251","v252","v253","v254","v255","a0","a1","a2","a3","a4","a5","a6","a7","a8","a9","a10","a11","a12","a13","a14","a15","a16","a17","a18","a19","a20","a21","a22","a23","a24","a25","a26","a27","a28","a29","a30","a31","a32","a33","a34","a35","a36","a37","a38","a39","a40","a41","a42","a43","a44","a45","a46","a47","a48","a49","a50","a51","a52","a53","a54","a55","a56","a57","a58","a59","a60","a61","a62","a63","a64","a65","a66","a67","a68","a69","a70","a71","a72","a73","a74","a75","a76","a77","a78","a79","a80","a81","a82","a83","a84","a85","a86","a87","a88","a89","a90","a91","a92","a93","a94","a95","a96","a97","a98","a99","a100","a101","a102","a103","a104","a105","a106","a107","a108","a109","a110","a111","a112","a113","a114","a115","a116","a117","a118","a119","a120","a121","a122","a123","a124","a125","a126","a127","a128","a129","a130","a131","a132","a133","a134","a135","a136","a137","a138","a139","a140","a141","a142","a143","a144","a145","a146","a147","a148","a149","a150","a151","a152","a153","a154","a155","a156","a157","a158","a159","a160","a161","a162","a163","a164","a165","a166","a167","a168","a169","a170","a171","a172","a173","a174","a175","a176","a177","a178","a179","a180","a181","a182","a183","a184","a185","a186","a187","a188","a189","a190","a191","a192","a193","a194","a195","a196","a197","a198","a199","a200","a201","a202","a203","a204","a205","a206","a207","a208","a209","a210","a211","a212","a213","a214","a215","a216","a217","a218","a219","a220","a221","a222","a223","a224","a225","a226","a227","a228","a229","a230","a231","a232","a233","a234","a235","a236","a237","a238","a239","a240","a241","a242","a243","a244","a245","a246","a247","a248","a249","a250","a251","a252","a253","a254","a255","s6","s7","s8","s9","s10","s11","s12","s13","s14","s15","s16","s17","s18","s19","s20","s21","s22","s23","s24","s25","s26","s27","s28","s29","s30","s31","s32","s33","s34","s35","s36","s37","s38","s39","s40","s41","s42","s43","s44","s45","s46","s47","s48","s49","s50","s51","s52","s53","s54","s55","s56","s57","s58","s59","s60","s61","s62","s63","s64","s65","s66","s67","s68","s69","s70","s71","s72","s73","s74","s75","s76","s77","s78","s79","s80","s81","s82","s83","s84","s85","s86","s87","s88","s89","s90","s91","s92","s93","s94","s95","s96","s97","s98","s99","s100","vcc","scc","m0","memory");
}
extern "C" void kernel_launch(void* const* d_in, const int* in_sizes, int n_in, void* d_out, int out_size, void* d_ws, size_t ws_size, hipStream_t stream) {
  char* xch = (char*)d_ws + ((ws_size - (size_t)270532608) & ~(size_t)0xfffff);
  (void)hipFuncSetAttribute((const void*)fused_attn, hipFuncAttributeMaxDynamicSharedMemorySize, 163840);
  hipLaunchKernelGGL(fused_attn, dim3(256), dim3(256), 163840, stream,
                     (const char*)d_in[0], (const char*)d_in[1], (const char*)d_in[2], (const char*)d_in[3], (const char*)d_in[4],
                     (const char*)d_in[5], (const char*)d_in[6], (const char*)d_in[7], (const char*)d_in[8], (const char*)d_in[9], xch, (char*)d_out, (char*)d_ws);
}
```
